```python
import torch

def _softmax(x, axis=-1):
    return torch.softmax(x, dim=axis)
def _gelu(x):
    return torch.nn.functional.gelu(x, approximate="tanh")
def _silu(x):
    return torch.nn.functional.silu(x)
def _sigmoid(x):
    return torch.sigmoid(x)
def _layernorm(x, axis=-1, eps=1e-5):
    m = x.mean(dim=axis, keepdim=True)
    v = x.var(dim=axis, keepdim=True, unbiased=False)
    return (x - m) / torch.sqrt(v + eps)
def _scatter_add(tgt, idx, src, axis=0):
    return tgt.clone().index_add_(axis, idx, src)
def _segment_sum(src, seg, n):
    out = torch.zeros((n,) + src.shape[1:], dtype=src.dtype)
    return out.index_add_(0, seg, src)
def _pad(x, pw):
    flat = tuple(v for pair in reversed(pw) for v in pair)
    return torch.nn.functional.pad(x, flat)

kernel_name = 'concat_heads__gqa_block_cached__mha_block_cached__near_ridge'

def setup_inputs():

    _w = (lambda a: (lambda t: t.to(torch.bfloat16) if t.is_floating_point() else t)(torch.as_tensor(a)))
    def _A_setup():
        B, Sq, D = 16, 160, 640
        Hq, Hkv, Sk, Dh = 32, 32, 512, 192
        assert Hq % Hkv == 0, (Hq, Hkv)
        return {
            "x": _w((lambda *s: torch.randn(*s, dtype=torch.bfloat16) * 0.8)(B, Sq, D)),
            "k_cache": _w((lambda *s: torch.randn(*s, dtype=torch.bfloat16) * 0.8)(B, Hkv, Sk, Dh)),
            "v_cache": _w((lambda *s: torch.randn(*s, dtype=torch.bfloat16) * 0.05)(B, Hkv, Sk, Dh)),
            "wq": _w((lambda *s: torch.randn(*s, dtype=torch.bfloat16) * 0.8)(D, Hq * Dh) * (D ** -0.5)),
            "wo": _w((lambda *s: torch.randn(*s, dtype=torch.bfloat16) * 0.05)(Hq * Dh, D) * ((Hq * Dh) ** -0.5)),
        }
    _A_in = _A_setup()

    def _B_setup():
        B, Sq, D = 16, 160, 640
        H, Sk, Dh = 32, 512, 192
        return {
            "x": _w((lambda *s: torch.randn(*s, dtype=torch.bfloat16) * 0.8)(B, Sq, D)),
            "k_cache": _w((lambda *s: torch.randn(*s, dtype=torch.bfloat16) * 0.8)(B, H, Sk, Dh)),
            "v_cache": _w((lambda *s: torch.randn(*s, dtype=torch.bfloat16) * 0.05)(B, H, Sk, Dh)),
            "wq": _w((lambda *s: torch.randn(*s, dtype=torch.bfloat16) * 0.8)(D, H * Dh) * (D ** -0.5)),
            "wo": _w((lambda *s: torch.randn(*s, dtype=torch.bfloat16) * 0.05)(H * Dh, D) * ((H * Dh) ** -0.5)),
        }
    _B_in = _B_setup()

    return {
        "A_x": _A_in["x"],
        "A_k_cache": _A_in["k_cache"],
        "A_v_cache": _A_in["v_cache"],
        "A_wq": _A_in["wq"],
        "A_wo": _A_in["wo"],
        "B_x": _B_in["x"],
        "B_k_cache": _B_in["k_cache"],
        "B_v_cache": _B_in["v_cache"],
        "B_wq": _B_in["wq"],
        "B_wo": _B_in["wo"],
    }

def reference(A_x, A_k_cache, A_v_cache, A_wq, A_wo, B_x, B_k_cache, B_v_cache, B_wq, B_wo):
    def _A_ref(x, k_cache, v_cache, wq, wo):
        B, Sq, D = x.shape
        Hq, Dh = 32, 192
        Hkv = k_cache.shape[1]
        rep = Hq // Hkv
        q = (lambda x, *d: x.permute(*d))((x @ wq).reshape(B, Sq, Hq, Dh), 0, 2, 1, 3)
        k = (lambda x, n, axis: torch.repeat_interleave(x, n, dim=axis))(k_cache, rep, axis=1)
        v = (lambda x, n, axis: torch.repeat_interleave(x, n, dim=axis))(v_cache, rep, axis=1)
        scores = torch.einsum("bhqd,bhkd->bhqk", q, k) * (Dh ** -0.5)
        p = _softmax(scores, axis=-1)
        o = torch.einsum("bhqk,bhkd->bhqd", p, v)
        return (lambda x, *d: x.permute(*d))(o, 0, 2, 1, 3).reshape(B, Sq, Hq * Dh) @ wo

    def _B_ref(x, k_cache, v_cache, wq, wo):
        B, Sq, D = x.shape
        H, Dh = 32, 192
        q = (lambda x, *d: x.permute(*d))((x @ wq).reshape(B, Sq, H, Dh), 0, 2, 1, 3)
        scores = torch.einsum("bhqd,bhkd->bhqk", q, k_cache) * (Dh ** -0.5)
        p = _softmax(scores, axis=-1)
        o = torch.einsum("bhqk,bhkd->bhqd", p, v_cache)
        return (lambda x, *d: x.permute(*d))(o, 0, 2, 1, 3).reshape(B, Sq, H * Dh) @ wo

    a = _A_ref(A_x, A_k_cache, A_v_cache, A_wq, A_wo)
    b = _B_ref(B_x, B_k_cache, B_v_cache, B_wq, B_wo)
    return torch.concatenate([a, b], axis=-1)
```

```cpp
#include <hip/hip_runtime.h>
#include <hip/hip_bf16.h>

#define MF "v_mfma_f32_16x16x32_bf16 "
#define TR "ds_read_b64_tr_b16 "
#define AR "v_accvgpr_read_b32 "
#define AW "v_accvgpr_write_b32 "
#define CV "v_cvt_pk_bf16_f32 "
#define LD "buffer_load_dwordx4 "
constexpr unsigned LDS1 = 160 * 1024;

struct Ptrs { const void* x[2]; const void* k[2]; const void* v[2]; const void* wq[2]; const void* wo[2]; };

__global__ __launch_bounds__(256, 1) void fused_kernel(Ptrs P, char* __restrict__ ws, char* __restrict__ out) {
  unsigned tid = threadIdx.x, bid = blockIdx.x;
  unsigned long long ka = (unsigned long long)__builtin_amdgcn_kernarg_segment_ptr();
  asm volatile(
".p2align 5\n"
".Lcode0:\n"
"s_and_b32 s58,s23,4\n"
"s_lshl_b32 s58,s58,1\n"
"s_load_dwordx2 s[28:29],s[20:21],s58 offset:48\n"
"s_load_dwordx2 s[24:25],s[20:21],s58\n"
"s_load_dwordx2 s[32:33],s[20:21],s58 offset:16\n"
"s_load_dwordx2 s[36:37],s[20:21],s58 offset:32\n"
"s_load_dwordx2 s[48:49],s[20:21],s58 offset:64\n"
"s_memtime s[100:101]\n"
"s_load_dwordx2 s[40:41],s[20:21],0x50\n"
"s_load_dwordx2 s[44:45],s[20:21],0x58\n"
"v_readfirstlane_b32 s22,v0\n"
"v_and_b32 v9,63,v0\n"
"v_bfe_u32 v10,v0,3,3\n"
"v_and_b32 v11,7,v0\n"
"s_lshr_b32 s22,s22,6\n"
"s_or_b32 s22,s22,s58\n"
"s_and_b32 s58,s23,1\n"
"s_lshl_b32 s58,s58,3\n"
"s_lshr_b32 s59,s23,5\n"
"s_add_u32 s59,s59,s58\n"
"s_mul_i32 s59,s59,4112\n"
"s_or_b32 s22,s22,s59\n"
"s_and_b32 s59,s23,2\n"
"s_lshl_b32 s59,s59,1\n"
"s_bfe_u32 s60,s23,0x20003\n"
"s_or_b32 s59,s59,s60\n"
"s_lshl_b32 s60,s59,20\n"
"s_or_b32 s22,s22,s60\n"
"s_or_b32 s59,s59,s58\n"
"s_mul_i32 s59,s59,65792\n"
"s_xor_b32 s59,s59,524288\n"
"s_or_b32 s22,s22,s59\n"
"v_lshrrev_b32 v6,2,v0\n"
"v_and_b32 v6,4,v6\n"
"v_xor_b32 v6,v6,v11\n"
"v_lshlrev_b32 v6,4,v6\n"
"s_mov_b32 s58,12288\n"
"v_mad_u32_u24 v6,v10,s58,v6\n"
"v_bfe_u32 v4,v0,4,3\n"
"v_xor_b32 v4,v4,v11\n"
"v_lshlrev_b32 v4,4,v4\n"
"s_mov_b32 s58,1280\n"
"v_mad_u32_u24 v4,v10,s58,v4\n"
"v_lshlrev_b32 v9,4,v9\n"
"s_mov_b32 s26,3276800\n"
"s_mov_b32 s27,0x00020000\n"
"s_mov_b32 s30,7864320\n"
"s_mov_b32 s31,0x00020000\n"
"s_mov_b32 s34,0\n"
"s_mov_b32 s35,0x00020000\n"
"s_mov_b32 s38,0\n"
"s_mov_b32 s39,0x00020000\n"
"s_mov_b32 s42,268435456\n"
"s_mov_b32 s43,0x00020000\n"
"s_mov_b32 s46,6553600\n"
"s_mov_b32 s47,0x00020000\n"
"s_mov_b32 s50,7864320\n"
"s_mov_b32 s51,0x00020000\n"
"s_mov_b32 s98,20000\n"
"s_bfe_u32 s56,s22,0x20000\n"
"s_bfe_u32 s72,s22,0x10003\n"
"s_lshr_b32 s73,s56,1\n"
"s_and_b32 s74,s56,1\n"
"s_mul_i32 s62,s56,4096\n"
"s_mul_i32 s63,s56,5120\n"
"s_add_u32 s63,s63,98304\n"
"s_bfe_u32 s58,s22,0x40004\n"
"s_bfe_u32 s59,s22,0x40008\n"
"s_mul_i32 s68,s58,204800\n"
"s_mul_i32 s60,s56,51200\n"
"s_add_u32 s68,s68,s60\n"
"s_lshl_b32 s59,s59,1\n"
"s_add_u32 s59,s59,s73\n"
"s_mul_i32 s66,s59,384\n"
"s_mul_i32 s60,s74,393216\n"
"s_add_u32 s66,s66,s60\n"
"v_xor_b32_e64 v19,64,v4\n"
"v_add_u32 v16,97280,v6\n"
"v_add_u32 v17,194560,v6\n"
"v_add_u32 v18,291840,v6\n"
"s_waitcnt lgkmcnt(0)\n"
"s_mov_b32 s80,s100\n"
"s_mov_b32 s87,2\n"
"s_add_u32 m0,s62,0\n"
"s_add_u32 s82,s66,0\n"
LD "v6,s[28:31],s82 offen lds\n"
LD "v16,s[28:31],s82 offen offset:1024 lds\n"
LD "v17,s[28:31],s82 offen offset:2048 lds\n"
LD "v18,s[28:31],s82 offen offset:3072 lds\n"
"s_add_u32 s84,s68,0\n"
"s_add_u32 m0,s63,0\n"
"s_nop 0\n"
LD "v4,s[24:27],s84 offen lds\n"
"s_add_u32 s84,s68,9216\n"
LD "v19,s[24:27],s84 offen offset:1024 lds\n"
"s_add_u32 s84,s68,18432\n"
LD "v4,s[24:27],s84 offen offset:2048 lds\n"
"s_add_u32 s84,s68,27648\n"
LD "v19,s[24:27],s84 offen offset:3072 lds\n"
"s_add_u32 s84,s68,40960\n"
"s_add_u32 m0,s63,4096\n"
"s_nop 0\n"
LD "v4,s[24:27],s84 offen lds\n"
"s_lshr_b32 s58,s23,3\n"
"s_mul_i32 s59,s58,7\n"
"s_and_b32 s60,s23,7\n"
"s_mul_i32 s60,s60,3\n"
"s_add_u32 s59,s59,s60\n"
"s_and_b32 s59,s59,15\n"
"s_lshl_b32 s59,s59,8\n"
"s_or_b32 s58,s58,s59\n"
"s_and_b32 s59,s23,3\n"
"s_lshl_b32 s59,s59,5\n"
"s_or_b32 s58,s58,s59\n"
"s_bfe_u32 s59,s23,0x10001\n"
"s_lshl_b32 s59,s59,12\n"
"s_or_b32 s58,s58,s59\n"
"s_lshl_b32 s23,s23,16\n"
"s_or_b32 s23,s23,s58\n"
"v_bfe_u32 v20,v0,4,2\n"
"v_mul_u32_u24 v21,384,v10\n"
"v_xor_b32_e64 v22,v11,v20\n"
"s_add_u32 m0,s62,16384\n"
"s_add_u32 s82,s66,128\n"
LD "v6,s[28:31],s82 offen lds\n"
"v_lshl_add_u32 v7,v22,4,v21\n"
"s_lshl_b32 s58,s74,6\n"
"v_xor_b32 v7,s58,v7\n"
"v_lshlrev_b32 v22,1,v20\n"
"v_xor_b32 v22,v11,v22\n"
"v_lshl_add_u32 v8,v22,4,v21\n"
"v_bfe_u32 v21,v0,1,3\n"
"v_xor_b32 v21,v21,v20\n"
"v_lshlrev_b32 v21,4,v21\n"
"v_lshl_add_u32 v21,v11,7,v21\n"
"v_bfe_u32 v22,v0,3,1\n"
"s_mul_i32 s58,s73,12288\n"
"v_mul_u32_u24 v23,3072,v22\n"
"v_add3_u32 v1,v23,v21,s58\n"
"s_mul_i32 s59,s74,10240\n"
"s_add_u32 s59,s59,98304\n"
"v_lshl_add_u32 v2,v22,10,v21\n"
"v_add_u32_e64 v2,s59,v2\n"
"v_bfe_u32 v21,v0,2,3\n"
LD "v16,s[28:31],s82 offen offset:1024 lds\n"
"v_lshlrev_b32 v21,7,v21\n"
"v_bfe_u32 v23,v0,3,2\n"
"v_lshl_add_u32 v21,v23,5,v21\n"
"v_and_b32 v23,3,v0\n"
"v_lshl_add_u32 v21,v23,3,v21\n"
"v_bfe_u32 v23,v0,5,1\n"
"v_mul_u32_u24 v3,3072,v23\n"
"v_add3_u32 v3,v3,v21,s58\n"
"s_bfe_u32 s58,s22,0x40004\n"
"s_mul_i32 s58,s58,409600\n"
"s_mul_i32 s59,s72,1280\n"
"s_add_u32 s58,s58,s59\n"
"v_and_b32 v21,15,v0\n"
"v_mul_u32_u24 v21,2560,v21\n"
"v_lshl_add_u32 v21,v23,4,v21\n"
"v_bfe_u32 v23,v0,4,1\n"
"v_lshl_add_u32 v21,v23,5,v21\n"
"v_add_u32 v12,s58,v21\n"
"v_bfe_u32 v21,v0,2,2\n"
LD "v17,s[28:31],s82 offen offset:2048 lds\n"
"v_lshlrev_b32 v21,7,v21\n"
"v_lshl_add_u32 v21,v20,10,v21\n"
"v_lshl_add_u32 v21,v22,6,v21\n"
"v_and_b32 v23,3,v0\n"
"v_lshl_add_u32 v21,v23,4,v21\n"
"s_lshl_b32 s58,s73,13\n"
"v_add_u32 v0,s58,v21\n"
"s_getpc_b64 s[52:53]\n"
".Lcts:\n"
"s_sub_u32 s52,s52,.Lcts-.Lcode0\n"
"s_subb_u32 s53,s53,0\n"
"s_bfe_u32 s58,s23,0x20000\n"
"s_lshl_b32 s58,s58,2\n"
"s_add_u32 s58,s58,s56\n"
"s_lshl_b32 s58,s58,13\n"
"v_lshlrev_b32 v15,3,v9\n"
"v_add_u32 v15,s58,v15\n"
"v_min_u32 v15,.Lcode1-.Lcode0,v15\n"
LD "v18,s[28:31],s82 offen offset:3072 lds\n"
"s_bfe_u32 s58,s22,0x4000c\n"
"s_bfe_u32 s59,s22,0x40010\n"
"s_mul_i32 s69,s58,204800\n"
"s_mul_i32 s60,s56,51200\n"
"s_add_u32 s69,s69,s60\n"
"s_lshl_b32 s59,s59,1\n"
"s_add_u32 s59,s59,s73\n"
"s_mul_i32 s67,s59,384\n"
"s_mul_i32 s60,s74,393216\n"
"s_add_u32 s67,s67,s60\n"
"s_bfe_u32 s58,s22,0x40004\n"
"s_bfe_u32 s59,s22,0x40008\n"
"s_lshl_b32 s59,s59,1\n"
"s_add_u32 s88,s59,s74\n"
"s_mul_i32 s88,s88,245760\n"
"s_add_u32 s59,s59,s73\n"
"s_lshl_b32 s70,s58,5\n"
"s_add_u32 s70,s70,s59\n"
"s_mul_i32 s70,s70,196608\n"
"s_add_u32 m0,s62,32768\n"
"s_add_u32 s82,s66,256\n"
LD "v6,s[28:31],s82 offen lds\n"
"s_bfe_u32 s58,s22,0x4000c\n"
"s_bfe_u32 s59,s22,0x40010\n"
"s_lshl_b32 s59,s59,1\n"
"s_add_u32 s89,s59,s74\n"
"s_mul_i32 s89,s89,245760\n"
"s_add_u32 s59,s59,s73\n"
"s_lshl_b32 s71,s58,5\n"
"s_add_u32 s71,s71,s59\n"
"s_mul_i32 s71,s71,196608\n"
"s_bitcmp1_b32 s23,12\n"
"s_cselect_b32 s58,s71,s70\n"
"s_cselect_b32 s71,s70,s71\n"
"s_mov_b32 s70,s58\n"
"s_cselect_b32 s58,s89,s88\n"
"s_cselect_b32 s89,s88,s89\n"
"s_mov_b32 s88,s58\n"
"s_cselect_b32 s69,98,1\n"
"s_mov_b32 s54,1037384597\n"
"s_mov_b32 s55,1037384597\n"
LD "v16,s[28:31],s82 offen offset:1024 lds\n"
"s_bfe_u32 s58,s22,0x40004\n"
"s_lshl_b32 s59,s72,4\n"
"s_add_u32 s59,s59,s58\n"
"s_lshl_b32 s86,s59,12\n"
"s_add_u32 s86,s86,100663296\n"
"s_mul_i32 s93,s59,1638400\n"
"s_add_u32 s93,s93,33554432\n"
"s_bfe_u32 s58,s22,0x30014\n"
"s_lshl_b32 s78,s58,1\n"
"s_add_u32 s78,s78,s74\n"
"s_lshl_b32 s94,s78,2\n"
"s_add_u32 s94,s94,s86\n"
"s_lshl_b32 s92,s58,2\n"
"s_lshr_b32 s92,0x31752064,s92\n"
"s_and_b32 s92,s92,7\n"
"s_lshl_b32 s92,s92,2\n"
"s_add_u32 s92,s92,s56\n"
"s_mul_i32 s91,s58,204800\n"
"s_add_u32 s91,s91,s93\n"
LD "v17,s[28:31],s82 offen offset:2048 lds\n"
"s_mul_i32 s75,s56,30720\n"
"s_add_u32 s75,s75,s91\n"
"s_add_u32 s75,s75,0\n"
"s_mul_i32 s58,s74,5120\n"
"s_add_u32 s91,s91,s58\n"
"s_mov_b32 s90,0xf0000000\n"
"s_mov_b32 s77,0xf0000000\n"
"s_mov_b32 s85,0xf0000000\n"
AW "a240,v0\n"
AW "a244,v1\n"
AW "a241,v2\n"
AW "a245,v3\n"
AW "a242,v4\n"
AW "a243,v6\n"
AW "a252,v12\n"
"s_mov_b32 s57,0\n"
"s_mov_b32 s83,0\n"
LD "v18,s[28:31],s82 offen offset:3072 lds\n"
"global_load_dword a249,v15,s[52:53]\n"
AR "v0,a240\n"
AR "v2,a241\n"
AR "v4,a242\n"
AR "v6,a243\n"
"v_xor_b32 v1,64,v0\n"
"v_xor_b32 v3,64,v2\n"
"v_xor_b32_e64 v5,64,v4\n"
"v_add_u32 v11,97280,v6\n"
"v_add_u32 v12,194560,v6\n"
"v_add_u32 v13,291840,v6\n"
"s_mov_b64 vcc,0\n"
"s_waitcnt vmcnt(9) lgkmcnt(0)\n"
"s_barrier\n"
"s_nop 0\n"
TR "a[200:201],v0\n"
TR "a[202:203],v0 offset:512\n"
TR "a[204:205],v0 offset:4096\n"
TR "a[206:207],v0 offset:4608\n"
"ds_read_b128 a[60:63],v2\n"
"ds_read_b128 a[100:103],v3\n"
"ds_read_b128 a[64:67],v2 offset:2048\n"
"ds_read_b128 a[104:107],v3 offset:2048\n"
"ds_read_b128 a[68:71],v2 offset:4096\n"
"ds_read_b128 a[108:111],v3 offset:4096\n"
"ds_read_b128 a[72:75],v2 offset:6144\n"
"ds_read_b128 a[112:115],v3 offset:6144\n"
"ds_read_b128 a[76:79],v2 offset:8192\n"
"ds_read_b128 a[116:119],v3 offset:8192\n"
TR "a[208:209],v0 offset:8\n"
TR "a[210:211],v0 offset:520\n"
TR "a[212:213],v0 offset:4104\n"
TR "a[214:215],v0 offset:4616\n"
"s_waitcnt lgkmcnt(0)\n"
MF "v[16:19],a[200:203],a[60:63],0\n"
"s_add_u32 s84,s68,128\n"
TR "a[216:217],v1\n"
MF "v[16:19],a[204:207],a[100:103],v[16:19]\n"
"s_add_u32 m0,s63,20480\n"
TR "a[218:219],v1 offset:512\n"
MF "v[24:27],a[204:207],a[104:107],0\n"
LD "v4,s[24:27],s84 offen lds\n"
MF "v[24:27],a[200:203],a[64:67],v[24:27]\n"
TR "a[220:221],v1 offset:4096\n"
"v_lshrrev_b32 v15,5,v9\n"
MF "v[32:35],a[200:203],a[68:71],0\n"
TR "a[222:223],v1 offset:4608\n"
"v_min_u32 v15,11,v15\n"
MF "v[32:35],a[204:207],a[108:111],v[32:35]\n"
"v_max3_f32 v220,|v16|,|v17|,|v18|\n"
"v_max_f32_e64 v220,v220,|v19|\n"
MF "v[40:43],a[204:207],a[112:115],0\n"
"s_add_u32 s84,s68,9344\n"
"v_readlane_b32 s12,v220,3\n"
"v_readlane_b32 s6,v220,22\n"
MF "v[40:43],a[200:203],a[72:75],v[40:43]\n"
"s_min_u32 s12,s12,s6\n"
"v_readlane_b32 s7,v220,41\n"
"v_readlane_b32 s8,v220,60\n"
MF "v[48:51],a[200:203],a[76:79],0\n"
"s_min_u32 s7,s7,s8\n"
"v_mul_u32_u24 v15,160,v15\n"
"v_and_or_b32 v15,v9,16,v15\n"
MF "v[48:51],a[204:207],a[116:119],v[48:51]\n"
LD "v5,s[24:27],s84 offen offset:1024 lds\n"
MF "v[52:55],a[212:215],a[116:119],0\n"
"s_min_u32 s12,s12,s7\n"
TR "a[200:201],v1 offset:8\n"
"v_lshlrev_b32 v15,3,v15\n"
MF "v[52:55],a[208:211],a[76:79],v[52:55]\n"
"s_and_b32 s12,s12,0x7f800000\n"
TR "a[202:203],v1 offset:520\n"
MF "v[44:47],a[208:211],a[72:75],0\n"
"s_sub_u32 s12,s12,0x14000000\n"
MF "v[44:47],a[212:215],a[112:115],v[44:47]\n"
"s_cmp_lt_u32 s12,0x50000000\n"
"s_cselect_b32 s12,s12,0xe8400000\n"
TR "a[204:205],v1 offset:4104\n"
MF "v[36:39],a[212:215],a[108:111],0\n"
"s_add_u32 s12,s12,0x17c00000\n"
TR "a[206:207],v1 offset:4616\n"
MF "v[36:39],a[208:211],a[68:71],v[36:39]\n"
"s_bfe_u32 s60,s23,0x70000\n"
"v_mov_b32 v220,s12\n"
"v_mov_b32 v221,s12\n"
MF "v[28:31],a[208:211],a[64:67],0\n"
"s_add_u32 s84,s68,18560\n"
"v_mov_b32 v222,s12\n"
"v_mov_b32 v223,s12\n"
MF "v[28:31],a[212:215],a[104:107],v[28:31]\n"
LD "v4,s[24:27],s84 offen offset:2048 lds\n"
MF "v[20:23],a[212:215],a[100:103],0\n"
"s_lshl_b32 s60,s60,2\n"
MF "v[20:23],a[208:211],a[60:63],v[20:23]\n"
"s_waitcnt vmcnt(8) lgkmcnt(0)\n"
"s_barrier\n"
MF "v[56:59],a[216:219],a[60:63],v[220:223]\n"
"s_add_u32 s60,s60,s56\n"
TR "a[208:209],v0 offset:16384\n"
MF "v[56:59],a[220:223],a[100:103],v[56:59]\n"
"s_add_u32 s84,s68,27776\n"
TR "a[210:211],v0 offset:16896\n"
MF "v[64:67],a[220:223],a[104:107],v[220:223]\n"
LD "v5,s[24:27],s84 offen offset:3072 lds\n"
MF "v[64:67],a[216:219],a[64:67],v[64:67]\n"
"s_mul_i32 s60,s60,15360\n"
TR "a[212:213],v0 offset:20480\n"
MF "v[72:75],a[216:219],a[68:71],v[220:223]\n"
TR "a[214:215],v0 offset:20992\n"
MF "v[72:75],a[220:223],a[108:111],v[72:75]\n"
"s_add_u32 s84,s68,41088\n"
MF "v[80:83],a[220:223],a[112:115],v[220:223]\n"
"s_add_u32 m0,s63,24576\n"
MF "v[80:83],a[216:219],a[72:75],v[80:83]\n"
LD "v4,s[24:27],s84 offen lds\n"
MF "v[88:91],a[216:219],a[76:79],v[220:223]\n"
MF "v[88:91],a[220:223],a[116:119],v[88:91]\n"
MF "v[92:95],a[204:207],a[116:119],v[220:223]\n"
"s_add_u32 m0,s62,49152\n"
TR "a[216:217],v0 offset:16392\n"
MF "v[92:95],a[200:203],a[76:79],v[92:95]\n"
"s_add_u32 s82,s66,786432\n"
TR "a[218:219],v0 offset:16904\n"
MF "v[84:87],a[200:203],a[72:75],v[220:223]\n"
LD "v6,s[28:31],s82 offen lds\n"
MF "v[84:87],a[204:207],a[112:115],v[84:87]\n"
TR "a[220:221],v0 offset:20488\n"
MF "v[76:79],a[204:207],a[108:111],v[220:223]\n"
TR "a[222:223],v0 offset:21000\n"
MF "v[76:79],a[200:203],a[68:71],v[76:79]\n"
MF "v[68:71],a[200:203],a[64:67],v[220:223]\n"
MF "v[68:71],a[204:207],a[104:107],v[68:71]\n"
LD "v11,s[28:31],s82 offen offset:1024 lds\n"
MF "v[60:63],a[204:207],a[100:103],v[220:223]\n"
MF "v[60:63],a[200:203],a[60:63],v[60:63]\n"
"s_waitcnt lgkmcnt(4)\n"
MF "v[96:99],a[208:211],a[60:63],v[220:223]\n"
TR "a[200:201],v1 offset:16384\n"
MF "v[96:99],a[212:215],a[100:103],v[96:99]\n"
TR "a[202:203],v1 offset:16896\n"
MF "v[104:107],a[212:215],a[104:107],v[220:223]\n"
LD "v12,s[28:31],s82 offen offset:2048 lds\n"
MF "v[104:107],a[208:211],a[64:67],v[104:107]\n"
TR "a[204:205],v1 offset:20480\n"
MF "v[112:115],a[208:211],a[68:71],v[220:223]\n"
TR "a[206:207],v1 offset:20992\n"
MF "v[112:115],a[212:215],a[108:111],v[112:115]\n"
MF "v[120:123],a[212:215],a[112:115],v[220:223]\n"
MF "v[120:123],a[208:211],a[72:75],v[120:123]\n"
LD "v13,s[28:31],s82 offen offset:3072 lds\n"
MF "v[128:131],a[208:211],a[76:79],v[220:223]\n"
MF "v[128:131],a[212:215],a[116:119],v[128:131]\n"
"s_waitcnt lgkmcnt(4)\n"
MF "v[132:135],a[220:223],a[116:119],v[220:223]\n"
"s_add_u32 m0,s62,65536\n"
TR "a[208:209],v1 offset:16392\n"
MF "v[132:135],a[216:219],a[76:79],v[132:135]\n"
"s_add_u32 s82,s66,786560\n"
TR "a[210:211],v1 offset:16904\n"
MF "v[124:127],a[216:219],a[72:75],v[220:223]\n"
LD "v6,s[28:31],s82 offen lds\n"
MF "v[124:127],a[220:223],a[112:115],v[124:127]\n"
TR "a[212:213],v1 offset:20488\n"
MF "v[116:119],a[220:223],a[108:111],v[220:223]\n"
TR "a[214:215],v1 offset:21000\n"
MF "v[116:119],a[216:219],a[68:71],v[116:119]\n"
MF "v[108:111],a[216:219],a[64:67],v[220:223]\n"
MF "v[108:111],a[220:223],a[104:107],v[108:111]\n"
LD "v11,s[28:31],s82 offen offset:1024 lds\n"
MF "v[100:103],a[220:223],a[100:103],v[220:223]\n"
MF "v[100:103],a[216:219],a[60:63],v[100:103]\n"
"s_waitcnt vmcnt(6) lgkmcnt(0)\n"
"s_barrier\n"
MF "v[136:139],a[200:203],a[60:63],v[220:223]\n"
TR "a[216:217],v0 offset:32768\n"
MF "v[136:139],a[204:207],a[100:103],v[136:139]\n"
TR "a[218:219],v0 offset:33280\n"
MF "v[144:147],a[204:207],a[104:107],v[220:223]\n"
"ds_read_b128 a[140:143],v2 offset:20480\n"
MF "v[144:147],a[200:203],a[64:67],v[144:147]\n"
TR "a[220:221],v0 offset:36864\n"
MF "v[152:155],a[200:203],a[68:71],v[220:223]\n"
TR "a[222:223],v0 offset:37376\n"
MF "v[152:155],a[204:207],a[108:111],v[152:155]\n"
LD "v12,s[28:31],s82 offen offset:2048 lds\n"
MF "v[160:163],a[204:207],a[112:115],v[220:223]\n"
"ds_read_b128 a[180:183],v3 offset:20480\n"
MF "v[160:163],a[200:203],a[72:75],v[160:163]\n"
MF "v[168:171],a[200:203],a[76:79],v[220:223]\n"
MF "v[168:171],a[204:207],a[116:119],v[168:171]\n"
LD "v13,s[28:31],s82 offen offset:3072 lds\n"
MF "v[172:175],a[212:215],a[116:119],v[220:223]\n"
TR "a[200:201],v0 offset:32776\n"
MF "v[172:175],a[208:211],a[76:79],v[172:175]\n"
TR "a[202:203],v0 offset:33288\n"
MF "v[164:167],a[208:211],a[72:75],v[220:223]\n"
"ds_read_b128 a[144:147],v2 offset:22528\n"
MF "v[164:167],a[212:215],a[112:115],v[164:167]\n"
"s_add_u32 m0,s62,81920\n"
TR "a[204:205],v0 offset:36872\n"
MF "v[156:159],a[212:215],a[108:111],v[220:223]\n"
"s_add_u32 s82,s66,786688\n"
TR "a[206:207],v0 offset:37384\n"
MF "v[156:159],a[208:211],a[68:71],v[156:159]\n"
LD "v6,s[28:31],s82 offen lds\n"
MF "v[148:151],a[208:211],a[64:67],v[220:223]\n"
"ds_read_b128 a[184:187],v3 offset:22528\n"
MF "v[148:151],a[212:215],a[104:107],v[148:151]\n"
MF "v[140:143],a[212:215],a[100:103],v[220:223]\n"
MF "v[140:143],a[208:211],a[60:63],v[140:143]\n"
"s_waitcnt lgkmcnt(7)\n"
LD "v11,s[28:31],s82 offen offset:1024 lds\n"
MF "v[176:179],a[216:219],a[60:63],v[220:223]\n"
TR "a[208:209],v1 offset:32768\n"
MF "v[176:179],a[220:223],a[100:103],v[176:179]\n"
TR "a[210:211],v1 offset:33280\n"
MF "v[184:187],a[220:223],a[104:107],v[220:223]\n"
"ds_read_b128 a[148:151],v2 offset:24576\n"
MF "v[184:187],a[216:219],a[64:67],v[184:187]\n"
TR "a[212:213],v1 offset:36864\n"
MF "v[192:195],a[216:219],a[68:71],v[220:223]\n"
TR "a[214:215],v1 offset:37376\n"
MF "v[192:195],a[220:223],a[108:111],v[192:195]\n"
LD "v12,s[28:31],s82 offen offset:2048 lds\n"
MF "v[200:203],a[220:223],a[112:115],v[220:223]\n"
"ds_read_b128 a[188:191],v3 offset:24576\n"
MF "v[200:203],a[216:219],a[72:75],v[200:203]\n"
MF "v[208:211],a[216:219],a[76:79],v[220:223]\n"
MF "v[208:211],a[220:223],a[116:119],v[208:211]\n"
"s_waitcnt lgkmcnt(7)\n"
LD "v13,s[28:31],s82 offen offset:3072 lds\n"
MF "v[212:215],a[204:207],a[116:119],v[220:223]\n"
TR "a[216:217],v1 offset:32776\n"
MF "v[212:215],a[200:203],a[76:79],v[212:215]\n"
TR "a[218:219],v1 offset:33288\n"
MF "v[204:207],a[200:203],a[72:75],v[220:223]\n"
"ds_read_b128 a[152:155],v2 offset:26624\n"
MF "v[204:207],a[204:207],a[112:115],v[204:207]\n"
"s_add_u32 s84,s68,256\n"
TR "a[220:221],v1 offset:36872\n"
MF "v[196:199],a[204:207],a[108:111],v[220:223]\n"
"s_add_u32 m0,s63,40960\n"
TR "a[222:223],v1 offset:37384\n"
MF "v[196:199],a[200:203],a[68:71],v[196:199]\n"
LD "v4,s[24:27],s84 offen lds\n"
MF "v[188:191],a[200:203],a[64:67],v[220:223]\n"
"v_add_u32 v0,0xc000,v0\n"
MF "v[188:191],a[204:207],a[104:107],v[188:191]\n"
"s_add_u32 s84,s68,9472\n"
"v_add_u32 v1,0xc000,v1\n"
MF "v[180:183],a[204:207],a[100:103],v[220:223]\n"
LD "v5,s[24:27],s84 offen offset:1024 lds\n"
MF "v[180:183],a[200:203],a[60:63],v[180:183]\n"
"s_waitcnt vmcnt(10) lgkmcnt(0)\n"
"s_barrier\n"
MF "v[216:219],a[208:211],a[60:63],v[220:223]\n"
TR "a[200:201],v0\n"
MF "v[216:219],a[212:215],a[100:103],v[216:219]\n"
TR "a[202:203],v0 offset:512\n"
MF "v[224:227],a[212:215],a[104:107],v[220:223]\n"
"ds_read_b128 a[192:195],v3 offset:26624\n"
MF "v[224:227],a[208:211],a[64:67],v[224:227]\n"
TR "a[204:205],v0 offset:4096\n"
MF "v[232:235],a[208:211],a[68:71],v[220:223]\n"
"s_add_u32 s84,s68,18688\n"
TR "a[206:207],v0 offset:4608\n"
MF "v[232:235],a[212:215],a[108:111],v[232:235]\n"
LD "v4,s[24:27],s84 offen offset:2048 lds\n"
MF "v[240:243],a[212:215],a[112:115],v[220:223]\n"
"ds_read_b128 a[156:159],v2 offset:28672\n"
MF "v[240:243],a[208:211],a[72:75],v[240:243]\n"
"ds_read_b128 a[196:199],v3 offset:28672\n"
MF "v[248:251],a[208:211],a[76:79],v[220:223]\n"
"s_add_u32 s84,s68,27904\n"
MF "v[248:251],a[212:215],a[116:119],v[248:251]\n"
LD "v5,s[24:27],s84 offen offset:3072 lds\n"
MF "v[252:255],a[220:223],a[116:119],v[220:223]\n"
TR "a[208:209],v0 offset:8\n"
MF "v[252:255],a[216:219],a[76:79],v[252:255]\n"
TR "a[210:211],v0 offset:520\n"
MF "v[244:247],a[216:219],a[72:75],v[220:223]\n"
MF "v[244:247],a[220:223],a[112:115],v[244:247]\n"
"s_add_u32 s84,s68,41216\n"
TR "a[212:213],v0 offset:4104\n"
MF "v[236:239],a[220:223],a[108:111],v[220:223]\n"
"s_add_u32 m0,s63,45056\n"
TR "a[214:215],v0 offset:4616\n"
MF "v[236:239],a[216:219],a[68:71],v[236:239]\n"
LD "v4,s[24:27],s84 offen lds\n"
MF "v[228:231],a[216:219],a[64:67],v[220:223]\n"
MF "v[228:231],a[220:223],a[104:107],v[228:231]\n"
"s_add_u32 m0,s62,0\n"
MF "v[220:223],a[220:223],a[100:103],v[220:223]\n"
"s_add_u32 s82,s66,1572864\n"
MF "v[220:223],a[216:219],a[60:63],v[220:223]\n"
"s_waitcnt lgkmcnt(4)\n"
LD "v6,s[28:31],s82 offen lds\n"
MF "v[16:19],a[200:203],a[140:143],v[16:19]\n"
TR "a[216:217],v1\n"
MF "v[16:19],a[204:207],a[180:183],v[16:19]\n"
TR "a[218:219],v1 offset:512\n"
MF "v[24:27],a[204:207],a[184:187],v[24:27]\n"
MF "v[24:27],a[200:203],a[144:147],v[24:27]\n"
TR "a[220:221],v1 offset:4096\n"
MF "v[32:35],a[200:203],a[148:151],v[32:35]\n"
TR "a[222:223],v1 offset:4608\n"
MF "v[32:35],a[204:207],a[188:191],v[32:35]\n"
LD "v11,s[28:31],s82 offen offset:1024 lds\n"
MF "v[40:43],a[204:207],a[192:195],v[40:43]\n"
MF "v[40:43],a[200:203],a[152:155],v[40:43]\n"
MF "v[48:51],a[200:203],a[156:159],v[48:51]\n"
MF "v[48:51],a[204:207],a[196:199],v[48:51]\n"
"s_waitcnt lgkmcnt(4)\n"
LD "v12,s[28:31],s82 offen offset:2048 lds\n"
MF "v[52:55],a[212:215],a[196:199],v[52:55]\n"
TR "a[200:201],v1 offset:8\n"
MF "v[52:55],a[208:211],a[156:159],v[52:55]\n"
TR "a[202:203],v1 offset:520\n"
MF "v[44:47],a[208:211],a[152:155],v[44:47]\n"
MF "v[44:47],a[212:215],a[192:195],v[44:47]\n"
TR "a[204:205],v1 offset:4104\n"
MF "v[36:39],a[212:215],a[188:191],v[36:39]\n"
TR "a[206:207],v1 offset:4616\n"
MF "v[36:39],a[208:211],a[148:151],v[36:39]\n"
LD "v13,s[28:31],s82 offen offset:3072 lds\n"
MF "v[28:31],a[208:211],a[144:147],v[28:31]\n"
"s_add_u32 s84,s68,384\n"
MF "v[28:31],a[212:215],a[184:187],v[28:31]\n"
"s_add_u32 m0,s63,0\n"
MF "v[20:23],a[212:215],a[180:183],v[20:23]\n"
LD "v4,s[24:27],s84 offen lds\n"
MF "v[20:23],a[208:211],a[140:143],v[20:23]\n"
"s_waitcnt vmcnt(14) lgkmcnt(0)\n"
"s_barrier\n"
"v_bfe_u32 v14,v9,4,4\n"
"v_lshlrev_b32 v14,2,v14\n"
"buffer_load_dword v10,v14,s[40:43],s86 offen sc1\n"
".Lhs2:\n"
MF "v[56:59],a[216:219],a[140:143],v[56:59]\n"
TR "a[208:209],v0 offset:16384\n"
MF "v[56:59],a[220:223],a[180:183],v[56:59]\n"
"s_add_u32 s84,s68,9600\n"
TR "a[210:211],v0 offset:16896\n"
MF "v[64:67],a[220:223],a[184:187],v[64:67]\n"
LD "v5,s[24:27],s84 offen offset:1024 lds\n"
MF "v[64:67],a[216:219],a[144:147],v[64:67]\n"
TR "a[212:213],v0 offset:20480\n"
MF "v[72:75],a[216:219],a[148:151],v[72:75]\n"
TR "a[214:215],v0 offset:20992\n"
MF "v[72:75],a[220:223],a[188:191],v[72:75]\n"
"s_add_u32 m0,s62,16384\n"
"buffer_load_dword a249,v15,s[48:51],s60 offen offset:1024\n"
MF "v[80:83],a[220:223],a[192:195],v[80:83]\n"
"s_add_u32 s82,s66,1572992\n"
MF "v[80:83],a[216:219],a[152:155],v[80:83]\n"
LD "v6,s[28:31],s82 offen lds\n"
MF "v[88:91],a[216:219],a[156:159],v[88:91]\n"
"s_bfe_u32 s59,s23,0x30003\n"
MF "v[88:91],a[220:223],a[196:199],v[88:91]\n"
"s_add_u32 s59,s59,1\n"
MF "v[92:95],a[204:207],a[196:199],v[92:95]\n"
"s_lshl_b32 s59,s59,7\n"
TR "a[216:217],v0 offset:16392\n"
MF "v[92:95],a[200:203],a[156:159],v[92:95]\n"
"s_and_b32 s59,s59,768\n"
TR "a[218:219],v0 offset:16904\n"
MF "v[84:87],a[200:203],a[152:155],v[84:87]\n"
LD "v11,s[28:31],s82 offen offset:1024 lds\n"
MF "v[84:87],a[204:207],a[192:195],v[84:87]\n"
"s_add_u32 s59,s59,s60\n"
TR "a[220:221],v0 offset:20488\n"
MF "v[76:79],a[204:207],a[188:191],v[76:79]\n"
TR "a[222:223],v0 offset:21000\n"
MF "v[76:79],a[200:203],a[148:151],v[76:79]\n"
MF "v[68:71],a[200:203],a[144:147],v[68:71]\n"
MF "v[68:71],a[204:207],a[184:187],v[68:71]\n"
LD "v12,s[28:31],s82 offen offset:2048 lds\n"
MF "v[60:63],a[204:207],a[180:183],v[60:63]\n"
MF "v[60:63],a[200:203],a[140:143],v[60:63]\n"
"s_waitcnt lgkmcnt(4)\n"
MF "v[96:99],a[208:211],a[140:143],v[96:99]\n"
TR "a[200:201],v1 offset:16384\n"
MF "v[96:99],a[212:215],a[180:183],v[96:99]\n"
TR "a[202:203],v1 offset:16896\n"
MF "v[104:107],a[212:215],a[184:187],v[104:107]\n"
LD "v13,s[28:31],s82 offen offset:3072 lds\n"
MF "v[104:107],a[208:211],a[144:147],v[104:107]\n"
TR "a[204:205],v1 offset:20480\n"
MF "v[112:115],a[208:211],a[148:151],v[112:115]\n"
TR "a[206:207],v1 offset:20992\n"
MF "v[112:115],a[212:215],a[188:191],v[112:115]\n"
"s_add_u32 s84,s68,20864\n"
MF "v[120:123],a[212:215],a[192:195],v[120:123]\n"
"s_add_u32 m0,s63,2048\n"
MF "v[120:123],a[208:211],a[152:155],v[120:123]\n"
LD "v4,s[24:27],s84 offen lds\n"
MF "v[128:131],a[208:211],a[156:159],v[128:131]\n"
MF "v[128:131],a[212:215],a[196:199],v[128:131]\n"
"s_waitcnt lgkmcnt(4)\n"
MF "v[132:135],a[220:223],a[196:199],v[132:135]\n"
TR "a[208:209],v1 offset:16392\n"
MF "v[132:135],a[216:219],a[156:159],v[132:135]\n"
"s_add_u32 s84,s68,30080\n"
TR "a[210:211],v1 offset:16904\n"
MF "v[124:127],a[216:219],a[152:155],v[124:127]\n"
LD "v5,s[24:27],s84 offen offset:1024 lds\n"
MF "v[124:127],a[220:223],a[192:195],v[124:127]\n"
TR "a[212:213],v1 offset:20488\n"
MF "v[116:119],a[220:223],a[188:191],v[116:119]\n"
TR "a[214:215],v1 offset:21000\n"
MF "v[116:119],a[216:219],a[148:151],v[116:119]\n"
"s_add_u32 m0,s62,32768\n"
MF "v[108:111],a[216:219],a[144:147],v[108:111]\n"
"s_add_u32 s82,s66,1573120\n"
MF "v[108:111],a[220:223],a[184:187],v[108:111]\n"
LD "v6,s[28:31],s82 offen lds\n"
MF "v[100:103],a[220:223],a[180:183],v[100:103]\n"
MF "v[100:103],a[216:219],a[140:143],v[100:103]\n"
"s_waitcnt vmcnt(14) lgkmcnt(0)\n"
"s_barrier\n"
MF "v[136:139],a[200:203],a[140:143],v[136:139]\n"
TR "a[216:217],v0 offset:32768\n"
MF "v[136:139],a[204:207],a[180:183],v[136:139]\n"
TR "a[218:219],v0 offset:33280\n"
MF "v[144:147],a[204:207],a[184:187],v[144:147]\n"
"ds_read_b128 a[60:63],v2 offset:40960\n"
MF "v[144:147],a[200:203],a[144:147],v[144:147]\n"
TR "a[220:221],v0 offset:36864\n"
MF "v[152:155],a[200:203],a[148:151],v[152:155]\n"
TR "a[222:223],v0 offset:37376\n"
MF "v[152:155],a[204:207],a[188:191],v[152:155]\n"
LD "v11,s[28:31],s82 offen offset:1024 lds\n"
MF "v[160:163],a[204:207],a[192:195],v[160:163]\n"
"ds_read_b128 a[100:103],v3 offset:40960\n"
MF "v[160:163],a[200:203],a[152:155],v[160:163]\n"
MF "v[168:171],a[200:203],a[156:159],v[168:171]\n"
MF "v[168:171],a[204:207],a[196:199],v[168:171]\n"
LD "v12,s[28:31],s82 offen offset:2048 lds\n"
MF "v[172:175],a[212:215],a[196:199],v[172:175]\n"
TR "a[200:201],v0 offset:32776\n"
MF "v[172:175],a[208:211],a[156:159],v[172:175]\n"
TR "a[202:203],v0 offset:33288\n"
MF "v[164:167],a[208:211],a[152:155],v[164:167]\n"
"ds_read_b128 a[64:67],v2 offset:43008\n"
MF "v[164:167],a[212:215],a[192:195],v[164:167]\n"
TR "a[204:205],v0 offset:36872\n"
MF "v[156:159],a[212:215],a[188:191],v[156:159]\n"
TR "a[206:207],v0 offset:37384\n"
MF "v[156:159],a[208:211],a[148:151],v[156:159]\n"
LD "v13,s[28:31],s82 offen offset:3072 lds\n"
MF "v[148:151],a[208:211],a[144:147],v[148:151]\n"
"ds_read_b128 a[104:107],v3 offset:43008\n"
MF "v[148:151],a[212:215],a[184:187],v[148:151]\n"
"s_add_u32 s84,s68,41344\n"
MF "v[140:143],a[212:215],a[180:183],v[140:143]\n"
"s_add_u32 m0,s63,4096\n"
MF "v[140:143],a[208:211],a[140:143],v[140:143]\n"
"s_waitcnt lgkmcnt(7)\n"
LD "v4,s[24:27],s84 offen lds\n"
MF "v[176:179],a[216:219],a[140:143],v[176:179]\n"
TR "a[208:209],v1 offset:32768\n"
MF "v[176:179],a[220:223],a[180:183],v[176:179]\n"
TR "a[210:211],v1 offset:33280\n"
MF "v[184:187],a[220:223],a[184:187],v[184:187]\n"
"s_sub_u32 s66,s66,s83\n"
"ds_read_b128 a[68:71],v2 offset:45056\n"
MF "v[184:187],a[216:219],a[144:147],v[184:187]\n"
"s_add_u32 m0,s62,49152\n"
TR "a[212:213],v1 offset:36864\n"
MF "v[192:195],a[216:219],a[148:151],v[192:195]\n"
"s_add_u32 s82,s66,2359296\n"
TR "a[214:215],v1 offset:37376\n"
MF "v[192:195],a[220:223],a[188:191],v[192:195]\n"
LD "v6,s[28:31],s82 offen lds\n"
MF "v[200:203],a[220:223],a[192:195],v[200:203]\n"
"ds_read_b128 a[108:111],v3 offset:45056\n"
MF "v[200:203],a[216:219],a[152:155],v[200:203]\n"
MF "v[208:211],a[216:219],a[156:159],v[208:211]\n"
MF "v[208:211],a[220:223],a[196:199],v[208:211]\n"
"s_waitcnt lgkmcnt(7)\n"
LD "v11,s[28:31],s82 offen offset:1024 lds\n"
MF "v[212:215],a[204:207],a[196:199],v[212:215]\n"
TR "a[216:217],v1 offset:32776\n"
MF "v[212:215],a[200:203],a[156:159],v[212:215]\n"
TR "a[218:219],v1 offset:33288\n"
MF "v[204:207],a[200:203],a[152:155],v[204:207]\n"
"ds_read_b128 a[72:75],v2 offset:47104\n"
MF "v[204:207],a[204:207],a[192:195],v[204:207]\n"
TR "a[220:221],v1 offset:36872\n"
MF "v[196:199],a[204:207],a[188:191],v[196:199]\n"
TR "a[222:223],v1 offset:37384\n"
MF "v[196:199],a[200:203],a[148:151],v[196:199]\n"
LD "v12,s[28:31],s82 offen offset:2048 lds\n"
MF "v[188:191],a[200:203],a[144:147],v[188:191]\n"
"v_add_u32 v0,0xffff4000,v0\n"
MF "v[188:191],a[204:207],a[184:187],v[188:191]\n"
"v_add_u32 v1,0xffff4000,v1\n"
MF "v[180:183],a[204:207],a[180:183],v[180:183]\n"
LD "v13,s[28:31],s82 offen offset:3072 lds\n"
MF "v[180:183],a[200:203],a[140:143],v[180:183]\n"
"s_waitcnt vmcnt(18) lgkmcnt(0)\n"
"s_barrier\n"
MF "v[216:219],a[208:211],a[140:143],v[216:219]\n"
TR "a[200:201],v0\n"
MF "v[216:219],a[212:215],a[180:183],v[216:219]\n"
TR "a[202:203],v0 offset:512\n"
MF "v[224:227],a[212:215],a[184:187],v[224:227]\n"
"ds_read_b128 a[112:115],v3 offset:47104\n"
MF "v[224:227],a[208:211],a[144:147],v[224:227]\n"
"s_add_u32 s84,s68,512\n"
TR "a[204:205],v0 offset:4096\n"
MF "v[232:235],a[208:211],a[148:151],v[232:235]\n"
"s_add_u32 m0,s63,20480\n"
TR "a[206:207],v0 offset:4608\n"
MF "v[232:235],a[212:215],a[188:191],v[232:235]\n"
LD "v4,s[24:27],s84 offen lds\n"
MF "v[240:243],a[212:215],a[192:195],v[240:243]\n"
"ds_read_b128 a[76:79],v2 offset:49152\n"
MF "v[240:243],a[208:211],a[152:155],v[240:243]\n"
"ds_read_b128 a[116:119],v3 offset:49152\n"
MF "v[248:251],a[208:211],a[156:159],v[248:251]\n"
"s_add_u32 s84,s68,9728\n"
MF "v[248:251],a[212:215],a[196:199],v[248:251]\n"
LD "v5,s[24:27],s84 offen offset:1024 lds\n"
MF "v[252:255],a[220:223],a[196:199],v[252:255]\n"
TR "a[208:209],v0 offset:8\n"
MF "v[252:255],a[216:219],a[156:159],v[252:255]\n"
TR "a[210:211],v0 offset:520\n"
MF "v[244:247],a[216:219],a[152:155],v[244:247]\n"
MF "v[244:247],a[220:223],a[192:195],v[244:247]\n"
"s_add_u32 m0,s62,65536\n"
TR "a[212:213],v0 offset:4104\n"
MF "v[236:239],a[220:223],a[188:191],v[236:239]\n"
"s_add_u32 s82,s66,2359424\n"
TR "a[214:215],v0 offset:4616\n"
MF "v[236:239],a[216:219],a[148:151],v[236:239]\n"
LD "v6,s[28:31],s82 offen lds\n"
MF "v[228:231],a[216:219],a[144:147],v[228:231]\n"
MF "v[228:231],a[220:223],a[184:187],v[228:231]\n"
MF "v[220:223],a[220:223],a[180:183],v[220:223]\n"
MF "v[220:223],a[216:219],a[140:143],v[220:223]\n"
"s_waitcnt lgkmcnt(4)\n"
LD "v11,s[28:31],s82 offen offset:1024 lds\n"
MF "v[16:19],a[200:203],a[60:63],v[16:19]\n"
TR "a[216:217],v1\n"
MF "v[16:19],a[204:207],a[100:103],v[16:19]\n"
TR "a[218:219],v1 offset:512\n"
MF "v[24:27],a[204:207],a[104:107],v[24:27]\n"
MF "v[24:27],a[200:203],a[64:67],v[24:27]\n"
TR "a[220:221],v1 offset:4096\n"
MF "v[32:35],a[200:203],a[68:71],v[32:35]\n"
TR "a[222:223],v1 offset:4608\n"
MF "v[32:35],a[204:207],a[108:111],v[32:35]\n"
LD "v12,s[28:31],s82 offen offset:2048 lds\n"
MF "v[40:43],a[204:207],a[112:115],v[40:43]\n"
MF "v[40:43],a[200:203],a[72:75],v[40:43]\n"
MF "v[48:51],a[200:203],a[76:79],v[48:51]\n"
MF "v[48:51],a[204:207],a[116:119],v[48:51]\n"
"s_waitcnt lgkmcnt(4)\n"
LD "v13,s[28:31],s82 offen offset:3072 lds\n"
MF "v[52:55],a[212:215],a[116:119],v[52:55]\n"
TR "a[200:201],v1 offset:8\n"
MF "v[52:55],a[208:211],a[76:79],v[52:55]\n"
TR "a[202:203],v1 offset:520\n"
MF "v[44:47],a[208:211],a[72:75],v[44:47]\n"
MF "v[44:47],a[212:215],a[112:115],v[44:47]\n"
"s_add_u32 s84,s68,20992\n"
TR "a[204:205],v1 offset:4104\n"
MF "v[36:39],a[212:215],a[108:111],v[36:39]\n"
"s_add_u32 m0,s63,22528\n"
TR "a[206:207],v1 offset:4616\n"
MF "v[36:39],a[208:211],a[68:71],v[36:39]\n"
LD "v4,s[24:27],s84 offen lds\n"
MF "v[28:31],a[208:211],a[64:67],v[28:31]\n"
MF "v[28:31],a[212:215],a[104:107],v[28:31]\n"
"s_add_u32 s84,s68,30208\n"
MF "v[20:23],a[212:215],a[100:103],v[20:23]\n"
LD "v5,s[24:27],s84 offen offset:1024 lds\n"
MF "v[20:23],a[208:211],a[60:63],v[20:23]\n"
"s_waitcnt vmcnt(19) lgkmcnt(0)\n"
"s_barrier\n"
MF "v[56:59],a[216:219],a[60:63],v[56:59]\n"
"s_add_u32 m0,s62,81920\n"
TR "a[208:209],v0 offset:16384\n"
MF "v[56:59],a[220:223],a[100:103],v[56:59]\n"
"s_add_u32 s82,s66,2359552\n"
TR "a[210:211],v0 offset:16896\n"
MF "v[64:67],a[220:223],a[104:107],v[64:67]\n"
LD "v6,s[28:31],s82 offen lds\n"
MF "v[64:67],a[216:219],a[64:67],v[64:67]\n"
TR "a[212:213],v0 offset:20480\n"
MF "v[72:75],a[216:219],a[68:71],v[72:75]\n"
TR "a[214:215],v0 offset:20992\n"
MF "v[72:75],a[220:223],a[108:111],v[72:75]\n"
"buffer_load_dword a249,v15,s[48:51],s59 offen\n"
MF "v[80:83],a[220:223],a[112:115],v[80:83]\n"
"s_bfe_u32 s59,s23,0x30003\n"
MF "v[80:83],a[216:219],a[72:75],v[80:83]\n"
LD "v11,s[28:31],s82 offen offset:1024 lds\n"
MF "v[88:91],a[216:219],a[76:79],v[88:91]\n"
"s_add_u32 s59,s59,3\n"
MF "v[88:91],a[220:223],a[116:119],v[88:91]\n"
"s_lshl_b32 s59,s59,7\n"
MF "v[92:95],a[204:207],a[116:119],v[92:95]\n"
"s_and_b32 s59,s59,768\n"
TR "a[216:217],v0 offset:16392\n"
MF "v[92:95],a[200:203],a[76:79],v[92:95]\n"
"s_add_u32 s59,s59,s60\n"
TR "a[218:219],v0 offset:16904\n"
MF "v[84:87],a[200:203],a[72:75],v[84:87]\n"
LD "v12,s[28:31],s82 offen offset:2048 lds\n"
MF "v[84:87],a[204:207],a[112:115],v[84:87]\n"
TR "a[220:221],v0 offset:20488\n"
MF "v[76:79],a[204:207],a[108:111],v[76:79]\n"
TR "a[222:223],v0 offset:21000\n"
MF "v[76:79],a[200:203],a[68:71],v[76:79]\n"
MF "v[68:71],a[200:203],a[64:67],v[68:71]\n"
MF "v[68:71],a[204:207],a[104:107],v[68:71]\n"
LD "v13,s[28:31],s82 offen offset:3072 lds\n"
MF "v[60:63],a[204:207],a[100:103],v[60:63]\n"
MF "v[60:63],a[200:203],a[60:63],v[60:63]\n"
"s_waitcnt lgkmcnt(4)\n"
MF "v[96:99],a[208:211],a[60:63],v[96:99]\n"
"s_add_u32 s84,s68,41472\n"
TR "a[200:201],v1 offset:16384\n"
MF "v[96:99],a[212:215],a[100:103],v[96:99]\n"
"s_add_u32 m0,s63,24576\n"
TR "a[202:203],v1 offset:16896\n"
MF "v[104:107],a[212:215],a[104:107],v[104:107]\n"
LD "v4,s[24:27],s84 offen lds\n"
MF "v[104:107],a[208:211],a[64:67],v[104:107]\n"
TR "a[204:205],v1 offset:20480\n"
MF "v[112:115],a[208:211],a[68:71],v[112:115]\n"
TR "a[206:207],v1 offset:20992\n"
MF "v[112:115],a[212:215],a[108:111],v[112:115]\n"
"s_add_u32 m0,s62,0\n"
MF "v[120:123],a[212:215],a[112:115],v[120:123]\n"
"s_add_u32 s82,s66,3145728\n"
MF "v[120:123],a[208:211],a[72:75],v[120:123]\n"
LD "v6,s[28:31],s82 offen lds\n"
MF "v[128:131],a[208:211],a[76:79],v[128:131]\n"
MF "v[128:131],a[212:215],a[116:119],v[128:131]\n"
"s_waitcnt lgkmcnt(4)\n"
MF "v[132:135],a[220:223],a[116:119],v[132:135]\n"
TR "a[208:209],v1 offset:16392\n"
MF "v[132:135],a[216:219],a[76:79],v[132:135]\n"
TR "a[210:211],v1 offset:16904\n"
MF "v[124:127],a[216:219],a[72:75],v[124:127]\n"
LD "v11,s[28:31],s82 offen offset:1024 lds\n"
MF "v[124:127],a[220:223],a[112:115],v[124:127]\n"
TR "a[212:213],v1 offset:20488\n"
MF "v[116:119],a[220:223],a[108:111],v[116:119]\n"
TR "a[214:215],v1 offset:21000\n"
MF "v[116:119],a[216:219],a[68:71],v[116:119]\n"
MF "v[108:111],a[216:219],a[64:67],v[108:111]\n"
MF "v[108:111],a[220:223],a[104:107],v[108:111]\n"
LD "v12,s[28:31],s82 offen offset:2048 lds\n"
MF "v[100:103],a[220:223],a[100:103],v[100:103]\n"
MF "v[100:103],a[216:219],a[60:63],v[100:103]\n"
"s_waitcnt vmcnt(21) lgkmcnt(0)\n"
"s_barrier\n"
MF "v[136:139],a[200:203],a[60:63],v[136:139]\n"
TR "a[216:217],v0 offset:32768\n"
MF "v[136:139],a[204:207],a[100:103],v[136:139]\n"
TR "a[218:219],v0 offset:33280\n"
MF "v[144:147],a[204:207],a[104:107],v[144:147]\n"
"ds_read_b128 a[140:143],v2\n"
MF "v[144:147],a[200:203],a[64:67],v[144:147]\n"
TR "a[220:221],v0 offset:36864\n"
MF "v[152:155],a[200:203],a[68:71],v[152:155]\n"
TR "a[222:223],v0 offset:37376\n"
MF "v[152:155],a[204:207],a[108:111],v[152:155]\n"
LD "v13,s[28:31],s82 offen offset:3072 lds\n"
MF "v[160:163],a[204:207],a[112:115],v[160:163]\n"
"ds_read_b128 a[180:183],v3\n"
MF "v[160:163],a[200:203],a[72:75],v[160:163]\n"
"s_add_u32 s84,s68,640\n"
MF "v[168:171],a[200:203],a[76:79],v[168:171]\n"
"s_add_u32 m0,s63,40960\n"
MF "v[168:171],a[204:207],a[116:119],v[168:171]\n"
LD "v4,s[24:27],s84 offen lds\n"
MF "v[172:175],a[212:215],a[116:119],v[172:175]\n"
TR "a[200:201],v0 offset:32776\n"
MF "v[172:175],a[208:211],a[76:79],v[172:175]\n"
TR "a[202:203],v0 offset:33288\n"
MF "v[164:167],a[208:211],a[72:75],v[164:167]\n"
"ds_read_b128 a[144:147],v2 offset:2048\n"
MF "v[164:167],a[212:215],a[112:115],v[164:167]\n"
TR "a[204:205],v0 offset:36872\n"
MF "v[156:159],a[212:215],a[108:111],v[156:159]\n"
"s_add_u32 s84,s68,9856\n"
TR "a[206:207],v0 offset:37384\n"
MF "v[156:159],a[208:211],a[68:71],v[156:159]\n"
LD "v5,s[24:27],s84 offen offset:1024 lds\n"
MF "v[148:151],a[208:211],a[64:67],v[148:151]\n"
"ds_read_b128 a[184:187],v3 offset:2048\n"
MF "v[148:151],a[212:215],a[104:107],v[148:151]\n"
"s_add_u32 m0,s62,16384\n"
MF "v[140:143],a[212:215],a[100:103],v[140:143]\n"
"s_add_u32 s82,s66,3145856\n"
MF "v[140:143],a[208:211],a[60:63],v[140:143]\n"
"s_waitcnt lgkmcnt(7)\n"
LD "v6,s[28:31],s82 offen lds\n"
MF "v[176:179],a[216:219],a[60:63],v[176:179]\n"
TR "a[208:209],v1 offset:32768\n"
MF "v[176:179],a[220:223],a[100:103],v[176:179]\n"
TR "a[210:211],v1 offset:33280\n"
MF "v[184:187],a[220:223],a[104:107],v[184:187]\n"
"ds_read_b128 a[148:151],v2 offset:4096\n"
MF "v[184:187],a[216:219],a[64:67],v[184:187]\n"
TR "a[212:213],v1 offset:36864\n"
MF "v[192:195],a[216:219],a[68:71],v[192:195]\n"
TR "a[214:215],v1 offset:37376\n"
MF "v[192:195],a[220:223],a[108:111],v[192:195]\n"
LD "v11,s[28:31],s82 offen offset:1024 lds\n"
MF "v[200:203],a[220:223],a[112:115],v[200:203]\n"
"ds_read_b128 a[188:191],v3 offset:4096\n"
MF "v[200:203],a[216:219],a[72:75],v[200:203]\n"
MF "v[208:211],a[216:219],a[76:79],v[208:211]\n"
MF "v[208:211],a[220:223],a[116:119],v[208:211]\n"
"s_waitcnt lgkmcnt(7)\n"
LD "v12,s[28:31],s82 offen offset:2048 lds\n"
MF "v[212:215],a[204:207],a[116:119],v[212:215]\n"
TR "a[216:217],v1 offset:32776\n"
MF "v[212:215],a[200:203],a[76:79],v[212:215]\n"
TR "a[218:219],v1 offset:33288\n"
MF "v[204:207],a[200:203],a[72:75],v[204:207]\n"
"ds_read_b128 a[152:155],v2 offset:6144\n"
MF "v[204:207],a[204:207],a[112:115],v[204:207]\n"
TR "a[220:221],v1 offset:36872\n"
MF "v[196:199],a[204:207],a[108:111],v[196:199]\n"
TR "a[222:223],v1 offset:37384\n"
MF "v[196:199],a[200:203],a[68:71],v[196:199]\n"
LD "v13,s[28:31],s82 offen offset:3072 lds\n"
MF "v[188:191],a[200:203],a[64:67],v[188:191]\n"
"s_add_u32 s84,s68,21120\n"
"v_add_u32 v0,0xc000,v0\n"
MF "v[188:191],a[204:207],a[104:107],v[188:191]\n"
"s_add_u32 m0,s63,43008\n"
"v_add_u32 v1,0xc000,v1\n"
MF "v[180:183],a[204:207],a[100:103],v[180:183]\n"
LD "v4,s[24:27],s84 offen lds\n"
MF "v[180:183],a[200:203],a[60:63],v[180:183]\n"
"s_waitcnt vmcnt(25) lgkmcnt(0)\n"
"s_barrier\n"
MF "v[216:219],a[208:211],a[60:63],v[216:219]\n"
TR "a[200:201],v0\n"
MF "v[216:219],a[212:215],a[100:103],v[216:219]\n"
TR "a[202:203],v0 offset:512\n"
MF "v[224:227],a[212:215],a[104:107],v[224:227]\n"
"ds_read_b128 a[192:195],v3 offset:6144\n"
MF "v[224:227],a[208:211],a[64:67],v[224:227]\n"
TR "a[204:205],v0 offset:4096\n"
MF "v[232:235],a[208:211],a[68:71],v[232:235]\n"
"s_add_u32 s84,s68,30336\n"
TR "a[206:207],v0 offset:4608\n"
MF "v[232:235],a[212:215],a[108:111],v[232:235]\n"
LD "v5,s[24:27],s84 offen offset:1024 lds\n"
MF "v[240:243],a[212:215],a[112:115],v[240:243]\n"
"ds_read_b128 a[156:159],v2 offset:8192\n"
MF "v[240:243],a[208:211],a[72:75],v[240:243]\n"
"s_add_u32 m0,s62,32768\n"
"ds_read_b128 a[196:199],v3 offset:8192\n"
MF "v[248:251],a[208:211],a[76:79],v[248:251]\n"
"s_add_u32 s82,s66,3145984\n"
MF "v[248:251],a[212:215],a[116:119],v[248:251]\n"
LD "v6,s[28:31],s82 offen lds\n"
MF "v[252:255],a[220:223],a[116:119],v[252:255]\n"
TR "a[208:209],v0 offset:8\n"
MF "v[252:255],a[216:219],a[76:79],v[252:255]\n"
TR "a[210:211],v0 offset:520\n"
MF "v[244:247],a[216:219],a[72:75],v[244:247]\n"
MF "v[244:247],a[220:223],a[112:115],v[244:247]\n"
TR "a[212:213],v0 offset:4104\n"
MF "v[236:239],a[220:223],a[108:111],v[236:239]\n"
TR "a[214:215],v0 offset:4616\n"
MF "v[236:239],a[216:219],a[68:71],v[236:239]\n"
LD "v11,s[28:31],s82 offen offset:1024 lds\n"
MF "v[228:231],a[216:219],a[64:67],v[228:231]\n"
MF "v[228:231],a[220:223],a[104:107],v[228:231]\n"
MF "v[220:223],a[220:223],a[100:103],v[220:223]\n"
MF "v[220:223],a[216:219],a[60:63],v[220:223]\n"
"s_waitcnt lgkmcnt(4)\n"
LD "v12,s[28:31],s82 offen offset:2048 lds\n"
MF "v[16:19],a[200:203],a[140:143],v[16:19]\n"
TR "a[216:217],v1\n"
MF "v[16:19],a[204:207],a[180:183],v[16:19]\n"
TR "a[218:219],v1 offset:512\n"
MF "v[24:27],a[204:207],a[184:187],v[24:27]\n"
MF "v[24:27],a[200:203],a[144:147],v[24:27]\n"
TR "a[220:221],v1 offset:4096\n"
MF "v[32:35],a[200:203],a[148:151],v[32:35]\n"
TR "a[222:223],v1 offset:4608\n"
MF "v[32:35],a[204:207],a[188:191],v[32:35]\n"
LD "v13,s[28:31],s82 offen offset:3072 lds\n"
MF "v[40:43],a[204:207],a[192:195],v[40:43]\n"
MF "v[40:43],a[200:203],a[152:155],v[40:43]\n"
"s_add_u32 s84,s68,41600\n"
MF "v[48:51],a[200:203],a[156:159],v[48:51]\n"
"s_add_u32 m0,s63,45056\n"
MF "v[48:51],a[204:207],a[196:199],v[48:51]\n"
"s_waitcnt lgkmcnt(4)\n"
LD "v4,s[24:27],s84 offen lds\n"
MF "v[52:55],a[212:215],a[196:199],v[52:55]\n"
TR "a[200:201],v1 offset:8\n"
MF "v[52:55],a[208:211],a[156:159],v[52:55]\n"
TR "a[202:203],v1 offset:520\n"
MF "v[44:47],a[208:211],a[152:155],v[44:47]\n"
MF "v[44:47],a[212:215],a[192:195],v[44:47]\n"
TR "a[204:205],v1 offset:4104\n"
MF "v[36:39],a[212:215],a[188:191],v[36:39]\n"
TR "a[206:207],v1 offset:4616\n"
MF "v[36:39],a[208:211],a[148:151],v[36:39]\n"
MF "v[28:31],a[208:211],a[144:147],v[28:31]\n"
MF "v[28:31],a[212:215],a[184:187],v[28:31]\n"
MF "v[20:23],a[212:215],a[180:183],v[20:23]\n"
MF "v[20:23],a[208:211],a[140:143],v[20:23]\n"
"s_waitcnt vmcnt(25) lgkmcnt(0)\n"
"s_barrier\n"
AW "a253,v10\n"
"v_add_u32 v10,1,v10\n"
"s_bfe_u32 s58,s22,0x30014\n"
"s_lshl_b32 s58,s58,2\n"
"s_add_u32 s58,s58,s56\n"
"s_lshl_b32 s58,s58,6\n"
"s_add_u32 s58,s58,s86\n"
"v_readlane_b32 s76,v10,s78\n"
"buffer_store_dword v10,v14,s[40:43],s58 offen offset:128 sc1\n"
".Lhs5:\n"
MF "v[56:59],a[216:219],a[140:143],v[56:59]\n"
"s_add_u32 m0,s62,49152\n"
TR "a[208:209],v0 offset:16384\n"
MF "v[56:59],a[220:223],a[180:183],v[56:59]\n"
"s_add_u32 s82,s66,3932160\n"
TR "a[210:211],v0 offset:16896\n"
MF "v[64:67],a[220:223],a[184:187],v[64:67]\n"
LD "v6,s[28:31],s82 offen lds\n"
MF "v[64:67],a[216:219],a[144:147],v[64:67]\n"
TR "a[212:213],v0 offset:20480\n"
MF "v[72:75],a[216:219],a[148:151],v[72:75]\n"
TR "a[214:215],v0 offset:20992\n"
MF "v[72:75],a[220:223],a[188:191],v[72:75]\n"
"buffer_load_dword a249,v15,s[48:51],s59 offen\n"
MF "v[80:83],a[220:223],a[192:195],v[80:83]\n"
"s_bfe_u32 s59,s23,0x30003\n"
MF "v[80:83],a[216:219],a[152:155],v[80:83]\n"
LD "v11,s[28:31],s82 offen offset:1024 lds\n"
MF "v[88:91],a[216:219],a[156:159],v[88:91]\n"
"s_add_u32 s59,s59,5\n"
MF "v[88:91],a[220:223],a[196:199],v[88:91]\n"
"s_lshl_b32 s59,s59,7\n"
MF "v[92:95],a[204:207],a[196:199],v[92:95]\n"
"s_and_b32 s59,s59,768\n"
TR "a[216:217],v0 offset:16392\n"
MF "v[92:95],a[200:203],a[156:159],v[92:95]\n"
"s_add_u32 s59,s59,s60\n"
TR "a[218:219],v0 offset:16904\n"
MF "v[84:87],a[200:203],a[152:155],v[84:87]\n"
LD "v12,s[28:31],s82 offen offset:2048 lds\n"
MF "v[84:87],a[204:207],a[192:195],v[84:87]\n"
TR "a[220:221],v0 offset:20488\n"
MF "v[76:79],a[204:207],a[188:191],v[76:79]\n"
TR "a[222:223],v0 offset:21000\n"
MF "v[76:79],a[200:203],a[148:151],v[76:79]\n"
MF "v[68:71],a[200:203],a[144:147],v[68:71]\n"
MF "v[68:71],a[204:207],a[184:187],v[68:71]\n"
LD "v13,s[28:31],s82 offen offset:3072 lds\n"
MF "v[60:63],a[204:207],a[180:183],v[60:63]\n"
MF "v[60:63],a[200:203],a[140:143],v[60:63]\n"
"s_waitcnt lgkmcnt(4)\n"
MF "v[96:99],a[208:211],a[140:143],v[96:99]\n"
"s_add_u32 s84,s68,768\n"
TR "a[200:201],v1 offset:16384\n"
MF "v[96:99],a[212:215],a[180:183],v[96:99]\n"
"s_add_u32 m0,s63,0\n"
TR "a[202:203],v1 offset:16896\n"
MF "v[104:107],a[212:215],a[184:187],v[104:107]\n"
LD "v4,s[24:27],s84 offen lds\n"
MF "v[104:107],a[208:211],a[144:147],v[104:107]\n"
TR "a[204:205],v1 offset:20480\n"
MF "v[112:115],a[208:211],a[148:151],v[112:115]\n"
TR "a[206:207],v1 offset:20992\n"
MF "v[112:115],a[212:215],a[188:191],v[112:115]\n"
MF "v[120:123],a[212:215],a[192:195],v[120:123]\n"
"s_add_u32 s84,s68,9984\n"
MF "v[120:123],a[208:211],a[152:155],v[120:123]\n"
LD "v5,s[24:27],s84 offen offset:1024 lds\n"
MF "v[128:131],a[208:211],a[156:159],v[128:131]\n"
MF "v[128:131],a[212:215],a[196:199],v[128:131]\n"
"s_waitcnt lgkmcnt(4)\n"
MF "v[132:135],a[220:223],a[196:199],v[132:135]\n"
TR "a[208:209],v1 offset:16392\n"
MF "v[132:135],a[216:219],a[156:159],v[132:135]\n"
TR "a[210:211],v1 offset:16904\n"
MF "v[124:127],a[216:219],a[152:155],v[124:127]\n"
MF "v[124:127],a[220:223],a[192:195],v[124:127]\n"
TR "a[212:213],v1 offset:20488\n"
MF "v[116:119],a[220:223],a[188:191],v[116:119]\n"
TR "a[214:215],v1 offset:21000\n"
MF "v[116:119],a[216:219],a[148:151],v[116:119]\n"
MF "v[108:111],a[216:219],a[144:147],v[108:111]\n"
MF "v[108:111],a[220:223],a[184:187],v[108:111]\n"
MF "v[100:103],a[220:223],a[180:183],v[100:103]\n"
MF "v[100:103],a[216:219],a[140:143],v[100:103]\n"
"s_waitcnt vmcnt(24) lgkmcnt(0)\n"
"s_barrier\n"
MF "v[136:139],a[200:203],a[140:143],v[136:139]\n"
TR "a[216:217],v0 offset:32768\n"
MF "v[136:139],a[204:207],a[180:183],v[136:139]\n"
TR "a[218:219],v0 offset:33280\n"
MF "v[144:147],a[204:207],a[184:187],v[144:147]\n"
"ds_read_b128 a[60:63],v2 offset:20480\n"
MF "v[144:147],a[200:203],a[144:147],v[144:147]\n"
"s_add_u32 m0,s62,65536\n"
TR "a[220:221],v0 offset:36864\n"
MF "v[152:155],a[200:203],a[148:151],v[152:155]\n"
"s_add_u32 s82,s66,3932288\n"
TR "a[222:223],v0 offset:37376\n"
MF "v[152:155],a[204:207],a[188:191],v[152:155]\n"
LD "v6,s[28:31],s82 offen lds\n"
MF "v[160:163],a[204:207],a[192:195],v[160:163]\n"
"ds_read_b128 a[100:103],v3 offset:20480\n"
MF "v[160:163],a[200:203],a[152:155],v[160:163]\n"
MF "v[168:171],a[200:203],a[156:159],v[168:171]\n"
MF "v[168:171],a[204:207],a[196:199],v[168:171]\n"
LD "v11,s[28:31],s82 offen offset:1024 lds\n"
MF "v[172:175],a[212:215],a[196:199],v[172:175]\n"
TR "a[200:201],v0 offset:32776\n"
MF "v[172:175],a[208:211],a[156:159],v[172:175]\n"
TR "a[202:203],v0 offset:33288\n"
MF "v[164:167],a[208:211],a[152:155],v[164:167]\n"
"ds_read_b128 a[64:67],v2 offset:22528\n"
MF "v[164:167],a[212:215],a[192:195],v[164:167]\n"
TR "a[204:205],v0 offset:36872\n"
MF "v[156:159],a[212:215],a[188:191],v[156:159]\n"
TR "a[206:207],v0 offset:37384\n"
MF "v[156:159],a[208:211],a[148:151],v[156:159]\n"
LD "v12,s[28:31],s82 offen offset:2048 lds\n"
MF "v[148:151],a[208:211],a[144:147],v[148:151]\n"
"ds_read_b128 a[104:107],v3 offset:22528\n"
MF "v[148:151],a[212:215],a[184:187],v[148:151]\n"
MF "v[140:143],a[212:215],a[180:183],v[140:143]\n"
MF "v[140:143],a[208:211],a[140:143],v[140:143]\n"
"s_waitcnt lgkmcnt(7)\n"
LD "v13,s[28:31],s82 offen offset:3072 lds\n"
MF "v[176:179],a[216:219],a[140:143],v[176:179]\n"
TR "a[208:209],v1 offset:32768\n"
MF "v[176:179],a[220:223],a[180:183],v[176:179]\n"
TR "a[210:211],v1 offset:33280\n"
MF "v[184:187],a[220:223],a[184:187],v[184:187]\n"
"ds_read_b128 a[68:71],v2 offset:24576\n"
MF "v[184:187],a[216:219],a[144:147],v[184:187]\n"
"s_add_u32 s84,s68,21248\n"
TR "a[212:213],v1 offset:36864\n"
MF "v[192:195],a[216:219],a[148:151],v[192:195]\n"
"s_add_u32 m0,s63,2048\n"
TR "a[214:215],v1 offset:37376\n"
MF "v[192:195],a[220:223],a[188:191],v[192:195]\n"
LD "v4,s[24:27],s84 offen lds\n"
MF "v[200:203],a[220:223],a[192:195],v[200:203]\n"
"ds_read_b128 a[108:111],v3 offset:24576\n"
MF "v[200:203],a[216:219],a[152:155],v[200:203]\n"
MF "v[208:211],a[216:219],a[156:159],v[208:211]\n"
"s_add_u32 s84,s68,30464\n"
MF "v[208:211],a[220:223],a[196:199],v[208:211]\n"
"s_waitcnt lgkmcnt(7)\n"
LD "v5,s[24:27],s84 offen offset:1024 lds\n"
MF "v[212:215],a[204:207],a[196:199],v[212:215]\n"
TR "a[216:217],v1 offset:32776\n"
MF "v[212:215],a[200:203],a[156:159],v[212:215]\n"
TR "a[218:219],v1 offset:33288\n"
MF "v[204:207],a[200:203],a[152:155],v[204:207]\n"
"ds_read_b128 a[72:75],v2 offset:26624\n"
MF "v[204:207],a[204:207],a[192:195],v[204:207]\n"
TR "a[220:221],v1 offset:36872\n"
MF "v[196:199],a[204:207],a[188:191],v[196:199]\n"
TR "a[222:223],v1 offset:37384\n"
MF "v[196:199],a[200:203],a[148:151],v[196:199]\n"
MF "v[188:191],a[200:203],a[144:147],v[188:191]\n"
"v_add_u32 v0,0xffff4000,v0\n"
MF "v[188:191],a[204:207],a[184:187],v[188:191]\n"
"v_add_u32 v1,0xffff4000,v1\n"
MF "v[180:183],a[204:207],a[180:183],v[180:183]\n"
MF "v[180:183],a[200:203],a[140:143],v[180:183]\n"
"s_waitcnt vmcnt(26) lgkmcnt(0)\n"
"s_barrier\n"
MF "v[216:219],a[208:211],a[140:143],v[216:219]\n"
TR "a[200:201],v0\n"
MF "v[216:219],a[212:215],a[180:183],v[216:219]\n"
TR "a[202:203],v0 offset:512\n"
MF "v[224:227],a[212:215],a[184:187],v[224:227]\n"
"ds_read_b128 a[112:115],v3 offset:26624\n"
MF "v[224:227],a[208:211],a[144:147],v[224:227]\n"
"s_add_u32 m0,s62,81920\n"
TR "a[204:205],v0 offset:4096\n"
MF "v[232:235],a[208:211],a[148:151],v[232:235]\n"
"s_add_u32 s82,s66,3932416\n"
TR "a[206:207],v0 offset:4608\n"
MF "v[232:235],a[212:215],a[188:191],v[232:235]\n"
LD "v6,s[28:31],s82 offen lds\n"
MF "v[240:243],a[212:215],a[192:195],v[240:243]\n"
"ds_read_b128 a[76:79],v2 offset:28672\n"
MF "v[240:243],a[208:211],a[152:155],v[240:243]\n"
"ds_read_b128 a[116:119],v3 offset:28672\n"
MF "v[248:251],a[208:211],a[156:159],v[248:251]\n"
MF "v[248:251],a[212:215],a[196:199],v[248:251]\n"
LD "v11,s[28:31],s82 offen offset:1024 lds\n"
MF "v[252:255],a[220:223],a[196:199],v[252:255]\n"
TR "a[208:209],v0 offset:8\n"
MF "v[252:255],a[216:219],a[156:159],v[252:255]\n"
TR "a[210:211],v0 offset:520\n"
MF "v[244:247],a[216:219],a[152:155],v[244:247]\n"
MF "v[244:247],a[220:223],a[192:195],v[244:247]\n"
TR "a[212:213],v0 offset:4104\n"
MF "v[236:239],a[220:223],a[188:191],v[236:239]\n"
TR "a[214:215],v0 offset:4616\n"
MF "v[236:239],a[216:219],a[148:151],v[236:239]\n"
LD "v12,s[28:31],s82 offen offset:2048 lds\n"
MF "v[228:231],a[216:219],a[144:147],v[228:231]\n"
MF "v[228:231],a[220:223],a[184:187],v[228:231]\n"
MF "v[220:223],a[220:223],a[180:183],v[220:223]\n"
MF "v[220:223],a[216:219],a[140:143],v[220:223]\n"
"s_waitcnt lgkmcnt(4)\n"
LD "v13,s[28:31],s82 offen offset:3072 lds\n"
MF "v[16:19],a[200:203],a[60:63],v[16:19]\n"
TR "a[216:217],v1\n"
MF "v[16:19],a[204:207],a[100:103],v[16:19]\n"
TR "a[218:219],v1 offset:512\n"
MF "v[24:27],a[204:207],a[104:107],v[24:27]\n"
MF "v[24:27],a[200:203],a[64:67],v[24:27]\n"
"s_add_u32 s84,s68,41728\n"
TR "a[220:221],v1 offset:4096\n"
MF "v[32:35],a[200:203],a[68:71],v[32:35]\n"
"s_add_u32 m0,s63,4096\n"
TR "a[222:223],v1 offset:4608\n"
MF "v[32:35],a[204:207],a[108:111],v[32:35]\n"
LD "v4,s[24:27],s84 offen lds\n"
MF "v[40:43],a[204:207],a[112:115],v[40:43]\n"
MF "v[40:43],a[200:203],a[72:75],v[40:43]\n"
MF "v[48:51],a[200:203],a[76:79],v[48:51]\n"
MF "v[48:51],a[204:207],a[116:119],v[48:51]\n"
"s_waitcnt lgkmcnt(4)\n"
MF "v[52:55],a[212:215],a[116:119],v[52:55]\n"
TR "a[200:201],v1 offset:8\n"
MF "v[52:55],a[208:211],a[76:79],v[52:55]\n"
TR "a[202:203],v1 offset:520\n"
MF "v[44:47],a[208:211],a[72:75],v[44:47]\n"
MF "v[44:47],a[212:215],a[112:115],v[44:47]\n"
TR "a[204:205],v1 offset:4104\n"
MF "v[36:39],a[212:215],a[108:111],v[36:39]\n"
TR "a[206:207],v1 offset:4616\n"
MF "v[36:39],a[208:211],a[68:71],v[36:39]\n"
MF "v[28:31],a[208:211],a[64:67],v[28:31]\n"
MF "v[28:31],a[212:215],a[104:107],v[28:31]\n"
MF "v[20:23],a[212:215],a[100:103],v[20:23]\n"
MF "v[20:23],a[208:211],a[60:63],v[20:23]\n"
"s_waitcnt vmcnt(25) lgkmcnt(0)\n"
"s_barrier\n"
MF "v[56:59],a[216:219],a[60:63],v[56:59]\n"
"s_add_u32 m0,s62,0\n"
TR "a[208:209],v0 offset:16384\n"
MF "v[56:59],a[220:223],a[100:103],v[56:59]\n"
"s_add_u32 s82,s66,4718592\n"
TR "a[210:211],v0 offset:16896\n"
MF "v[64:67],a[220:223],a[104:107],v[64:67]\n"
LD "v6,s[28:31],s82 offen lds\n"
MF "v[64:67],a[216:219],a[64:67],v[64:67]\n"
TR "a[212:213],v0 offset:20480\n"
MF "v[72:75],a[216:219],a[68:71],v[72:75]\n"
TR "a[214:215],v0 offset:20992\n"
MF "v[72:75],a[220:223],a[108:111],v[72:75]\n"
"buffer_load_dword a249,v15,s[48:51],s59 offen\n"
MF "v[80:83],a[220:223],a[112:115],v[80:83]\n"
MF "v[80:83],a[216:219],a[72:75],v[80:83]\n"
LD "v11,s[28:31],s82 offen offset:1024 lds\n"
MF "v[88:91],a[216:219],a[76:79],v[88:91]\n"
MF "v[88:91],a[220:223],a[116:119],v[88:91]\n"
MF "v[92:95],a[204:207],a[116:119],v[92:95]\n"
TR "a[216:217],v0 offset:16392\n"
MF "v[92:95],a[200:203],a[76:79],v[92:95]\n"
TR "a[218:219],v0 offset:16904\n"
MF "v[84:87],a[200:203],a[72:75],v[84:87]\n"
LD "v12,s[28:31],s82 offen offset:2048 lds\n"
MF "v[84:87],a[204:207],a[112:115],v[84:87]\n"
TR "a[220:221],v0 offset:20488\n"
MF "v[76:79],a[204:207],a[108:111],v[76:79]\n"
TR "a[222:223],v0 offset:21000\n"
MF "v[76:79],a[200:203],a[68:71],v[76:79]\n"
MF "v[68:71],a[200:203],a[64:67],v[68:71]\n"
MF "v[68:71],a[204:207],a[104:107],v[68:71]\n"
LD "v13,s[28:31],s82 offen offset:3072 lds\n"
MF "v[60:63],a[204:207],a[100:103],v[60:63]\n"
MF "v[60:63],a[200:203],a[60:63],v[60:63]\n"
"s_waitcnt lgkmcnt(4)\n"
MF "v[96:99],a[208:211],a[60:63],v[96:99]\n"
"s_add_u32 s84,s68,896\n"
TR "a[200:201],v1 offset:16384\n"
MF "v[96:99],a[212:215],a[100:103],v[96:99]\n"
"s_add_u32 m0,s63,20480\n"
TR "a[202:203],v1 offset:16896\n"
MF "v[104:107],a[212:215],a[104:107],v[104:107]\n"
LD "v4,s[24:27],s84 offen lds\n"
MF "v[104:107],a[208:211],a[64:67],v[104:107]\n"
TR "a[204:205],v1 offset:20480\n"
MF "v[112:115],a[208:211],a[68:71],v[112:115]\n"
TR "a[206:207],v1 offset:20992\n"
MF "v[112:115],a[212:215],a[108:111],v[112:115]\n"
MF "v[120:123],a[212:215],a[112:115],v[120:123]\n"
"s_add_u32 s84,s68,10112\n"
MF "v[120:123],a[208:211],a[72:75],v[120:123]\n"
LD "v5,s[24:27],s84 offen offset:1024 lds\n"
MF "v[128:131],a[208:211],a[76:79],v[128:131]\n"
MF "v[128:131],a[212:215],a[116:119],v[128:131]\n"
"s_waitcnt lgkmcnt(4)\n"
MF "v[132:135],a[220:223],a[116:119],v[132:135]\n"
TR "a[208:209],v1 offset:16392\n"
MF "v[132:135],a[216:219],a[76:79],v[132:135]\n"
TR "a[210:211],v1 offset:16904\n"
MF "v[124:127],a[216:219],a[72:75],v[124:127]\n"
MF "v[124:127],a[220:223],a[112:115],v[124:127]\n"
TR "a[212:213],v1 offset:20488\n"
MF "v[116:119],a[220:223],a[108:111],v[116:119]\n"
TR "a[214:215],v1 offset:21000\n"
MF "v[116:119],a[216:219],a[68:71],v[116:119]\n"
MF "v[108:111],a[216:219],a[64:67],v[108:111]\n"
MF "v[108:111],a[220:223],a[104:107],v[108:111]\n"
MF "v[100:103],a[220:223],a[100:103],v[100:103]\n"
"s_branch .Lqj\n"
".Lq0:\n"
MF "v[16:19],a[200:203],a[60:63],0\n"
"s_add_u32 m0,s62,49152\n"
TR "a[216:217],v1\n"
MF "v[16:19],a[204:207],a[100:103],v[16:19]\n"
"s_add_u32 s82,s66,786432\n"
TR "a[218:219],v1 offset:512\n"
MF "v[24:27],a[204:207],a[104:107],0\n"
LD "v6,s[28:31],s82 offen lds\n"
MF "v[24:27],a[200:203],a[64:67],v[24:27]\n"
TR "a[220:221],v1 offset:4096\n"
MF "v[32:35],a[200:203],a[68:71],0\n"
TR "a[222:223],v1 offset:4608\n"
MF "v[32:35],a[204:207],a[108:111],v[32:35]\n"
"v_max3_f32 v220,|v16|,|v17|,|v18|\n"
"v_max_f32_e64 v220,v220,|v19|\n"
MF "v[40:43],a[204:207],a[112:115],0\n"
"v_readlane_b32 s12,v220,3\n"
"v_readlane_b32 s6,v220,22\n"
MF "v[40:43],a[200:203],a[72:75],v[40:43]\n"
"s_min_u32 s12,s12,s6\n"
"v_readlane_b32 s7,v220,41\n"
"v_readlane_b32 s8,v220,60\n"
MF "v[48:51],a[200:203],a[76:79],0\n"
"s_min_u32 s7,s7,s8\n"
MF "v[48:51],a[204:207],a[116:119],v[48:51]\n"
LD "v11,s[28:31],s82 offen offset:1024 lds\n"
MF "v[52:55],a[212:215],a[116:119],0\n"
"s_min_u32 s12,s12,s7\n"
TR "a[200:201],v1 offset:8\n"
MF "v[52:55],a[208:211],a[76:79],v[52:55]\n"
"s_and_b32 s12,s12,0x7f800000\n"
TR "a[202:203],v1 offset:520\n"
MF "v[44:47],a[208:211],a[72:75],0\n"
"s_sub_u32 s12,s12,0x14000000\n"
MF "v[44:47],a[212:215],a[112:115],v[44:47]\n"
"s_cmp_lt_u32 s12,0x50000000\n"
"s_cselect_b32 s12,s12,0xe8400000\n"
TR "a[204:205],v1 offset:4104\n"
MF "v[36:39],a[212:215],a[108:111],0\n"
"s_add_u32 s12,s12,0x17c00000\n"
TR "a[206:207],v1 offset:4616\n"
MF "v[36:39],a[208:211],a[68:71],v[36:39]\n"
"v_mov_b32 v220,s12\n"
"v_mov_b32 v221,s12\n"
MF "v[28:31],a[208:211],a[64:67],0\n"
"v_mov_b32 v222,s12\n"
"v_mov_b32 v223,s12\n"
MF "v[28:31],a[212:215],a[104:107],v[28:31]\n"
LD "v12,s[28:31],s82 offen offset:2048 lds\n"
MF "v[20:23],a[212:215],a[100:103],0\n"
MF "v[20:23],a[208:211],a[60:63],v[20:23]\n"
"s_waitcnt vmcnt(7) lgkmcnt(0)\n"
"s_barrier\n"
MF "v[56:59],a[216:219],a[60:63],v[220:223]\n"
TR "a[208:209],v0 offset:16384\n"
MF "v[56:59],a[220:223],a[100:103],v[56:59]\n"
TR "a[210:211],v0 offset:16896\n"
MF "v[64:67],a[220:223],a[104:107],v[220:223]\n"
LD "v13,s[28:31],s82 offen offset:3072 lds\n"
MF "v[64:67],a[216:219],a[64:67],v[64:67]\n"
TR "a[212:213],v0 offset:20480\n"
MF "v[72:75],a[216:219],a[68:71],v[220:223]\n"
TR "a[214:215],v0 offset:20992\n"
MF "v[72:75],a[220:223],a[108:111],v[72:75]\n"
"s_add_u32 m0,s62,65536\n"
MF "v[80:83],a[220:223],a[112:115],v[220:223]\n"
"s_add_u32 s82,s66,786560\n"
MF "v[80:83],a[216:219],a[72:75],v[80:83]\n"
LD "v6,s[28:31],s82 offen lds\n"
MF "v[88:91],a[216:219],a[76:79],v[220:223]\n"
MF "v[88:91],a[220:223],a[116:119],v[88:91]\n"
MF "v[92:95],a[204:207],a[116:119],v[220:223]\n"
TR "a[216:217],v0 offset:16392\n"
MF "v[92:95],a[200:203],a[76:79],v[92:95]\n"
TR "a[218:219],v0 offset:16904\n"
MF "v[84:87],a[200:203],a[72:75],v[220:223]\n"
LD "v11,s[28:31],s82 offen offset:1024 lds\n"
MF "v[84:87],a[204:207],a[112:115],v[84:87]\n"
TR "a[220:221],v0 offset:20488\n"
MF "v[76:79],a[204:207],a[108:111],v[220:223]\n"
TR "a[222:223],v0 offset:21000\n"
MF "v[76:79],a[200:203],a[68:71],v[76:79]\n"
MF "v[68:71],a[200:203],a[64:67],v[220:223]\n"
MF "v[68:71],a[204:207],a[104:107],v[68:71]\n"
LD "v12,s[28:31],s82 offen offset:2048 lds\n"
MF "v[60:63],a[204:207],a[100:103],v[220:223]\n"
MF "v[60:63],a[200:203],a[60:63],v[60:63]\n"
"s_waitcnt lgkmcnt(4)\n"
MF "v[96:99],a[208:211],a[60:63],v[220:223]\n"
TR "a[200:201],v1 offset:16384\n"
MF "v[96:99],a[212:215],a[100:103],v[96:99]\n"
TR "a[202:203],v1 offset:16896\n"
MF "v[104:107],a[212:215],a[104:107],v[220:223]\n"
LD "v13,s[28:31],s82 offen offset:3072 lds\n"
MF "v[104:107],a[208:211],a[64:67],v[104:107]\n"
TR "a[204:205],v1 offset:20480\n"
MF "v[112:115],a[208:211],a[68:71],v[220:223]\n"
TR "a[206:207],v1 offset:20992\n"
MF "v[112:115],a[212:215],a[108:111],v[112:115]\n"
"s_add_u32 m0,s62,81920\n"
MF "v[120:123],a[212:215],a[112:115],v[220:223]\n"
"s_add_u32 s82,s66,786688\n"
MF "v[120:123],a[208:211],a[72:75],v[120:123]\n"
LD "v6,s[28:31],s82 offen lds\n"
MF "v[128:131],a[208:211],a[76:79],v[220:223]\n"
MF "v[128:131],a[212:215],a[116:119],v[128:131]\n"
"s_waitcnt lgkmcnt(4)\n"
MF "v[132:135],a[220:223],a[116:119],v[220:223]\n"
TR "a[208:209],v1 offset:16392\n"
MF "v[132:135],a[216:219],a[76:79],v[132:135]\n"
TR "a[210:211],v1 offset:16904\n"
MF "v[124:127],a[216:219],a[72:75],v[220:223]\n"
LD "v11,s[28:31],s82 offen offset:1024 lds\n"
MF "v[124:127],a[220:223],a[112:115],v[124:127]\n"
TR "a[212:213],v1 offset:20488\n"
MF "v[116:119],a[220:223],a[108:111],v[220:223]\n"
TR "a[214:215],v1 offset:21000\n"
MF "v[116:119],a[216:219],a[68:71],v[116:119]\n"
MF "v[108:111],a[216:219],a[64:67],v[220:223]\n"
MF "v[108:111],a[220:223],a[104:107],v[108:111]\n"
LD "v12,s[28:31],s82 offen offset:2048 lds\n"
MF "v[100:103],a[220:223],a[100:103],v[220:223]\n"
MF "v[100:103],a[216:219],a[60:63],v[100:103]\n"
"s_waitcnt vmcnt(11) lgkmcnt(0)\n"
"s_barrier\n"
MF "v[136:139],a[200:203],a[60:63],v[220:223]\n"
TR "a[216:217],v0 offset:32768\n"
MF "v[136:139],a[204:207],a[100:103],v[136:139]\n"
TR "a[218:219],v0 offset:33280\n"
MF "v[144:147],a[204:207],a[104:107],v[220:223]\n"
"ds_read_b128 a[140:143],v2 offset:20480\n"
MF "v[144:147],a[200:203],a[64:67],v[144:147]\n"
TR "a[220:221],v0 offset:36864\n"
MF "v[152:155],a[200:203],a[68:71],v[220:223]\n"
TR "a[222:223],v0 offset:37376\n"
MF "v[152:155],a[204:207],a[108:111],v[152:155]\n"
LD "v13,s[28:31],s82 offen offset:3072 lds\n"
MF "v[160:163],a[204:207],a[112:115],v[220:223]\n"
"ds_read_b128 a[180:183],v3 offset:20480\n"
MF "v[160:163],a[200:203],a[72:75],v[160:163]\n"
"s_add_u32 m0,s62,0\n"
MF "v[168:171],a[200:203],a[76:79],v[220:223]\n"
"s_add_u32 s82,s66,1572864\n"
MF "v[168:171],a[204:207],a[116:119],v[168:171]\n"
LD "v6,s[28:31],s82 offen lds\n"
MF "v[172:175],a[212:215],a[116:119],v[220:223]\n"
TR "a[200:201],v0 offset:32776\n"
MF "v[172:175],a[208:211],a[76:79],v[172:175]\n"
TR "a[202:203],v0 offset:33288\n"
MF "v[164:167],a[208:211],a[72:75],v[220:223]\n"
"ds_read_b128 a[144:147],v2 offset:22528\n"
MF "v[164:167],a[212:215],a[112:115],v[164:167]\n"
TR "a[204:205],v0 offset:36872\n"
MF "v[156:159],a[212:215],a[108:111],v[220:223]\n"
TR "a[206:207],v0 offset:37384\n"
MF "v[156:159],a[208:211],a[68:71],v[156:159]\n"
LD "v11,s[28:31],s82 offen offset:1024 lds\n"
MF "v[148:151],a[208:211],a[64:67],v[220:223]\n"
"ds_read_b128 a[184:187],v3 offset:22528\n"
MF "v[148:151],a[212:215],a[104:107],v[148:151]\n"
MF "v[140:143],a[212:215],a[100:103],v[220:223]\n"
MF "v[140:143],a[208:211],a[60:63],v[140:143]\n"
"s_waitcnt lgkmcnt(7)\n"
LD "v12,s[28:31],s82 offen offset:2048 lds\n"
MF "v[176:179],a[216:219],a[60:63],v[220:223]\n"
TR "a[208:209],v1 offset:32768\n"
MF "v[176:179],a[220:223],a[100:103],v[176:179]\n"
TR "a[210:211],v1 offset:33280\n"
MF "v[184:187],a[220:223],a[104:107],v[220:223]\n"
"ds_read_b128 a[148:151],v2 offset:24576\n"
MF "v[184:187],a[216:219],a[64:67],v[184:187]\n"
TR "a[212:213],v1 offset:36864\n"
MF "v[192:195],a[216:219],a[68:71],v[220:223]\n"
TR "a[214:215],v1 offset:37376\n"
MF "v[192:195],a[220:223],a[108:111],v[192:195]\n"
LD "v13,s[28:31],s82 offen offset:3072 lds\n"
MF "v[200:203],a[220:223],a[112:115],v[220:223]\n"
"ds_read_b128 a[188:191],v3 offset:24576\n"
MF "v[200:203],a[216:219],a[72:75],v[200:203]\n"
"s_add_u32 s84,s68,384\n"
MF "v[208:211],a[216:219],a[76:79],v[220:223]\n"
"s_add_u32 m0,s63,0\n"
MF "v[208:211],a[220:223],a[116:119],v[208:211]\n"
"s_waitcnt lgkmcnt(7)\n"
LD "v4,s[24:27],s84 offen lds\n"
MF "v[212:215],a[204:207],a[116:119],v[220:223]\n"
TR "a[216:217],v1 offset:32776\n"
MF "v[212:215],a[200:203],a[76:79],v[212:215]\n"
TR "a[218:219],v1 offset:33288\n"
MF "v[204:207],a[200:203],a[72:75],v[220:223]\n"
"ds_read_b128 a[152:155],v2 offset:26624\n"
MF "v[204:207],a[204:207],a[112:115],v[204:207]\n"
TR "a[220:221],v1 offset:36872\n"
MF "v[196:199],a[204:207],a[108:111],v[220:223]\n"
"s_add_u32 s84,s68,9600\n"
TR "a[222:223],v1 offset:37384\n"
MF "v[196:199],a[200:203],a[68:71],v[196:199]\n"
LD "v5,s[24:27],s84 offen offset:1024 lds\n"
MF "v[188:191],a[200:203],a[64:67],v[220:223]\n"
"s_add_u32 m0,s62,16384\n"
"v_add_u32 v0,0xc000,v0\n"
MF "v[188:191],a[204:207],a[104:107],v[188:191]\n"
"s_add_u32 s82,s66,1572992\n"
"v_add_u32 v1,0xc000,v1\n"
MF "v[180:183],a[204:207],a[100:103],v[220:223]\n"
LD "v6,s[28:31],s82 offen lds\n"
MF "v[180:183],a[200:203],a[60:63],v[180:183]\n"
"s_waitcnt vmcnt(15) lgkmcnt(0)\n"
"s_barrier\n"
MF "v[216:219],a[208:211],a[60:63],v[220:223]\n"
TR "a[200:201],v0\n"
MF "v[216:219],a[212:215],a[100:103],v[216:219]\n"
TR "a[202:203],v0 offset:512\n"
MF "v[224:227],a[212:215],a[104:107],v[220:223]\n"
"ds_read_b128 a[192:195],v3 offset:26624\n"
MF "v[224:227],a[208:211],a[64:67],v[224:227]\n"
TR "a[204:205],v0 offset:4096\n"
MF "v[232:235],a[208:211],a[68:71],v[220:223]\n"
TR "a[206:207],v0 offset:4608\n"
MF "v[232:235],a[212:215],a[108:111],v[232:235]\n"
LD "v11,s[28:31],s82 offen offset:1024 lds\n"
MF "v[240:243],a[212:215],a[112:115],v[220:223]\n"
"ds_read_b128 a[156:159],v2 offset:28672\n"
MF "v[240:243],a[208:211],a[72:75],v[240:243]\n"
"ds_read_b128 a[196:199],v3 offset:28672\n"
MF "v[248:251],a[208:211],a[76:79],v[220:223]\n"
MF "v[248:251],a[212:215],a[116:119],v[248:251]\n"
LD "v12,s[28:31],s82 offen offset:2048 lds\n"
MF "v[252:255],a[220:223],a[116:119],v[220:223]\n"
TR "a[208:209],v0 offset:8\n"
MF "v[252:255],a[216:219],a[76:79],v[252:255]\n"
TR "a[210:211],v0 offset:520\n"
MF "v[244:247],a[216:219],a[72:75],v[220:223]\n"
MF "v[244:247],a[220:223],a[112:115],v[244:247]\n"
TR "a[212:213],v0 offset:4104\n"
MF "v[236:239],a[220:223],a[108:111],v[220:223]\n"
TR "a[214:215],v0 offset:4616\n"
MF "v[236:239],a[216:219],a[68:71],v[236:239]\n"
LD "v13,s[28:31],s82 offen offset:3072 lds\n"
MF "v[228:231],a[216:219],a[64:67],v[220:223]\n"
MF "v[228:231],a[220:223],a[104:107],v[228:231]\n"
"s_add_u32 s84,s68,20864\n"
MF "v[220:223],a[220:223],a[100:103],v[220:223]\n"
"s_add_u32 m0,s63,2048\n"
MF "v[220:223],a[216:219],a[60:63],v[220:223]\n"
"s_waitcnt lgkmcnt(4)\n"
LD "v4,s[24:27],s84 offen lds\n"
MF "v[16:19],a[200:203],a[140:143],v[16:19]\n"
TR "a[216:217],v1\n"
MF "v[16:19],a[204:207],a[180:183],v[16:19]\n"
TR "a[218:219],v1 offset:512\n"
MF "v[24:27],a[204:207],a[184:187],v[24:27]\n"
MF "v[24:27],a[200:203],a[144:147],v[24:27]\n"
TR "a[220:221],v1 offset:4096\n"
MF "v[32:35],a[200:203],a[148:151],v[32:35]\n"
"s_add_u32 s84,s68,30080\n"
TR "a[222:223],v1 offset:4608\n"
MF "v[32:35],a[204:207],a[188:191],v[32:35]\n"
LD "v5,s[24:27],s84 offen offset:1024 lds\n"
MF "v[40:43],a[204:207],a[192:195],v[40:43]\n"
MF "v[40:43],a[200:203],a[152:155],v[40:43]\n"
"s_add_u32 m0,s62,32768\n"
MF "v[48:51],a[200:203],a[156:159],v[48:51]\n"
"s_add_u32 s82,s66,1573120\n"
MF "v[48:51],a[204:207],a[196:199],v[48:51]\n"
"s_waitcnt lgkmcnt(4)\n"
LD "v6,s[28:31],s82 offen lds\n"
MF "v[52:55],a[212:215],a[196:199],v[52:55]\n"
TR "a[200:201],v1 offset:8\n"
MF "v[52:55],a[208:211],a[156:159],v[52:55]\n"
TR "a[202:203],v1 offset:520\n"
MF "v[44:47],a[208:211],a[152:155],v[44:47]\n"
MF "v[44:47],a[212:215],a[192:195],v[44:47]\n"
TR "a[204:205],v1 offset:4104\n"
MF "v[36:39],a[212:215],a[188:191],v[36:39]\n"
TR "a[206:207],v1 offset:4616\n"
MF "v[36:39],a[208:211],a[148:151],v[36:39]\n"
LD "v11,s[28:31],s82 offen offset:1024 lds\n"
MF "v[28:31],a[208:211],a[144:147],v[28:31]\n"
MF "v[28:31],a[212:215],a[184:187],v[28:31]\n"
MF "v[20:23],a[212:215],a[180:183],v[20:23]\n"
LD "v12,s[28:31],s82 offen offset:2048 lds\n"
MF "v[20:23],a[208:211],a[140:143],v[20:23]\n"
"s_waitcnt vmcnt(19) lgkmcnt(0)\n"
"s_barrier\n"
MF "v[56:59],a[216:219],a[140:143],v[56:59]\n"
TR "a[208:209],v0 offset:16384\n"
MF "v[56:59],a[220:223],a[180:183],v[56:59]\n"
TR "a[210:211],v0 offset:16896\n"
MF "v[64:67],a[220:223],a[184:187],v[64:67]\n"
LD "v13,s[28:31],s82 offen offset:3072 lds\n"
MF "v[64:67],a[216:219],a[144:147],v[64:67]\n"
TR "a[212:213],v0 offset:20480\n"
MF "v[72:75],a[216:219],a[148:151],v[72:75]\n"
TR "a[214:215],v0 offset:20992\n"
MF "v[72:75],a[220:223],a[188:191],v[72:75]\n"
"s_add_u32 s84,s68,41344\n"
MF "v[80:83],a[220:223],a[192:195],v[80:83]\n"
"s_add_u32 m0,s63,4096\n"
MF "v[80:83],a[216:219],a[152:155],v[80:83]\n"
LD "v4,s[24:27],s84 offen lds\n"
MF "v[88:91],a[216:219],a[156:159],v[88:91]\n"
MF "v[88:91],a[220:223],a[196:199],v[88:91]\n"
"s_sub_u32 s66,s66,s83\n"
MF "v[92:95],a[204:207],a[196:199],v[92:95]\n"
"s_add_u32 m0,s62,49152\n"
TR "a[216:217],v0 offset:16392\n"
MF "v[92:95],a[200:203],a[156:159],v[92:95]\n"
"s_add_u32 s82,s66,2359296\n"
TR "a[218:219],v0 offset:16904\n"
MF "v[84:87],a[200:203],a[152:155],v[84:87]\n"
LD "v6,s[28:31],s82 offen lds\n"
MF "v[84:87],a[204:207],a[192:195],v[84:87]\n"
TR "a[220:221],v0 offset:20488\n"
MF "v[76:79],a[204:207],a[188:191],v[76:79]\n"
TR "a[222:223],v0 offset:21000\n"
MF "v[76:79],a[200:203],a[148:151],v[76:79]\n"
MF "v[68:71],a[200:203],a[144:147],v[68:71]\n"
MF "v[68:71],a[204:207],a[184:187],v[68:71]\n"
LD "v11,s[28:31],s82 offen offset:1024 lds\n"
MF "v[60:63],a[204:207],a[180:183],v[60:63]\n"
MF "v[60:63],a[200:203],a[140:143],v[60:63]\n"
"s_waitcnt lgkmcnt(4)\n"
MF "v[96:99],a[208:211],a[140:143],v[96:99]\n"
TR "a[200:201],v1 offset:16384\n"
MF "v[96:99],a[212:215],a[180:183],v[96:99]\n"
TR "a[202:203],v1 offset:16896\n"
MF "v[104:107],a[212:215],a[184:187],v[104:107]\n"
LD "v12,s[28:31],s82 offen offset:2048 lds\n"
MF "v[104:107],a[208:211],a[144:147],v[104:107]\n"
TR "a[204:205],v1 offset:20480\n"
MF "v[112:115],a[208:211],a[148:151],v[112:115]\n"
TR "a[206:207],v1 offset:20992\n"
MF "v[112:115],a[212:215],a[188:191],v[112:115]\n"
MF "v[120:123],a[212:215],a[192:195],v[120:123]\n"
MF "v[120:123],a[208:211],a[152:155],v[120:123]\n"
LD "v13,s[28:31],s82 offen offset:3072 lds\n"
MF "v[128:131],a[208:211],a[156:159],v[128:131]\n"
MF "v[128:131],a[212:215],a[196:199],v[128:131]\n"
"s_waitcnt lgkmcnt(4)\n"
MF "v[132:135],a[220:223],a[196:199],v[132:135]\n"
"s_add_u32 s84,s68,512\n"
TR "a[208:209],v1 offset:16392\n"
MF "v[132:135],a[216:219],a[156:159],v[132:135]\n"
"s_add_u32 m0,s63,20480\n"
TR "a[210:211],v1 offset:16904\n"
MF "v[124:127],a[216:219],a[152:155],v[124:127]\n"
LD "v4,s[24:27],s84 offen lds\n"
MF "v[124:127],a[220:223],a[192:195],v[124:127]\n"
TR "a[212:213],v1 offset:20488\n"
MF "v[116:119],a[220:223],a[188:191],v[116:119]\n"
TR "a[214:215],v1 offset:21000\n"
MF "v[116:119],a[216:219],a[148:151],v[116:119]\n"
MF "v[108:111],a[216:219],a[144:147],v[108:111]\n"
"s_add_u32 s84,s68,9728\n"
MF "v[108:111],a[220:223],a[184:187],v[108:111]\n"
LD "v5,s[24:27],s84 offen offset:1024 lds\n"
MF "v[100:103],a[220:223],a[180:183],v[100:103]\n"
MF "v[100:103],a[216:219],a[140:143],v[100:103]\n"
"s_waitcnt vmcnt(23) lgkmcnt(0)\n"
"s_barrier\n"
MF "v[136:139],a[200:203],a[140:143],v[136:139]\n"
TR "a[216:217],v0 offset:32768\n"
MF "v[136:139],a[204:207],a[180:183],v[136:139]\n"
TR "a[218:219],v0 offset:33280\n"
MF "v[144:147],a[204:207],a[184:187],v[144:147]\n"
"ds_read_b128 a[60:63],v2 offset:40960\n"
MF "v[144:147],a[200:203],a[144:147],v[144:147]\n"
"s_add_u32 m0,s62,65536\n"
TR "a[220:221],v0 offset:36864\n"
MF "v[152:155],a[200:203],a[148:151],v[152:155]\n"
"s_add_u32 s82,s66,2359424\n"
TR "a[222:223],v0 offset:37376\n"
MF "v[152:155],a[204:207],a[188:191],v[152:155]\n"
LD "v6,s[28:31],s82 offen lds\n"
MF "v[160:163],a[204:207],a[192:195],v[160:163]\n"
"ds_read_b128 a[100:103],v3 offset:40960\n"
MF "v[160:163],a[200:203],a[152:155],v[160:163]\n"
LD "v11,s[28:31],s82 offen offset:1024 lds\n"
MF "v[168:171],a[200:203],a[156:159],v[168:171]\n"
MF "v[168:171],a[204:207],a[196:199],v[168:171]\n"
LD "v12,s[28:31],s82 offen offset:2048 lds\n"
MF "v[172:175],a[212:215],a[196:199],v[172:175]\n"
TR "a[200:201],v0 offset:32776\n"
MF "v[172:175],a[208:211],a[156:159],v[172:175]\n"
TR "a[202:203],v0 offset:33288\n"
MF "v[164:167],a[208:211],a[152:155],v[164:167]\n"
"ds_read_b128 a[64:67],v2 offset:43008\n"
MF "v[164:167],a[212:215],a[192:195],v[164:167]\n"
TR "a[204:205],v0 offset:36872\n"
MF "v[156:159],a[212:215],a[188:191],v[156:159]\n"
TR "a[206:207],v0 offset:37384\n"
MF "v[156:159],a[208:211],a[148:151],v[156:159]\n"
MF "v[148:151],a[208:211],a[144:147],v[148:151]\n"
"ds_read_b128 a[104:107],v3 offset:43008\n"
MF "v[148:151],a[212:215],a[184:187],v[148:151]\n"
MF "v[140:143],a[212:215],a[180:183],v[140:143]\n"
LD "v13,s[28:31],s82 offen offset:3072 lds\n"
MF "v[140:143],a[208:211],a[140:143],v[140:143]\n"
"s_waitcnt lgkmcnt(7)\n"
MF "v[176:179],a[216:219],a[140:143],v[176:179]\n"
TR "a[208:209],v1 offset:32768\n"
MF "v[176:179],a[220:223],a[180:183],v[176:179]\n"
TR "a[210:211],v1 offset:33280\n"
MF "v[184:187],a[220:223],a[184:187],v[184:187]\n"
"ds_read_b128 a[68:71],v2 offset:45056\n"
MF "v[184:187],a[216:219],a[144:147],v[184:187]\n"
"s_add_u32 s84,s68,20992\n"
TR "a[212:213],v1 offset:36864\n"
MF "v[192:195],a[216:219],a[148:151],v[192:195]\n"
"s_add_u32 m0,s63,22528\n"
TR "a[214:215],v1 offset:37376\n"
MF "v[192:195],a[220:223],a[188:191],v[192:195]\n"
LD "v4,s[24:27],s84 offen lds\n"
MF "v[200:203],a[220:223],a[192:195],v[200:203]\n"
"ds_read_b128 a[108:111],v3 offset:45056\n"
MF "v[200:203],a[216:219],a[152:155],v[200:203]\n"
MF "v[208:211],a[216:219],a[156:159],v[208:211]\n"
"s_add_u32 s84,s68,30208\n"
MF "v[208:211],a[220:223],a[196:199],v[208:211]\n"
"s_waitcnt lgkmcnt(7)\n"
LD "v5,s[24:27],s84 offen offset:1024 lds\n"
MF "v[212:215],a[204:207],a[196:199],v[212:215]\n"
TR "a[216:217],v1 offset:32776\n"
MF "v[212:215],a[200:203],a[156:159],v[212:215]\n"
TR "a[218:219],v1 offset:33288\n"
MF "v[204:207],a[200:203],a[152:155],v[204:207]\n"
"ds_read_b128 a[72:75],v2 offset:47104\n"
MF "v[204:207],a[204:207],a[192:195],v[204:207]\n"
TR "a[220:221],v1 offset:36872\n"
MF "v[196:199],a[204:207],a[188:191],v[196:199]\n"
TR "a[222:223],v1 offset:37384\n"
MF "v[196:199],a[200:203],a[148:151],v[196:199]\n"
MF "v[188:191],a[200:203],a[144:147],v[188:191]\n"
"v_add_u32 v0,0xffff4000,v0\n"
MF "v[188:191],a[204:207],a[184:187],v[188:191]\n"
"v_add_u32 v1,0xffff4000,v1\n"
MF "v[180:183],a[204:207],a[180:183],v[180:183]\n"
MF "v[180:183],a[200:203],a[140:143],v[180:183]\n"
"s_waitcnt vmcnt(25) lgkmcnt(0)\n"
"s_barrier\n"
MF "v[216:219],a[208:211],a[140:143],v[216:219]\n"
TR "a[200:201],v0\n"
MF "v[216:219],a[212:215],a[180:183],v[216:219]\n"
TR "a[202:203],v0 offset:512\n"
MF "v[224:227],a[212:215],a[184:187],v[224:227]\n"
"ds_read_b128 a[112:115],v3 offset:47104\n"
MF "v[224:227],a[208:211],a[144:147],v[224:227]\n"
"s_add_u32 m0,s62,81920\n"
TR "a[204:205],v0 offset:4096\n"
MF "v[232:235],a[208:211],a[148:151],v[232:235]\n"
"s_add_u32 s82,s66,2359552\n"
TR "a[206:207],v0 offset:4608\n"
MF "v[232:235],a[212:215],a[188:191],v[232:235]\n"
LD "v6,s[28:31],s82 offen lds\n"
MF "v[240:243],a[212:215],a[192:195],v[240:243]\n"
"ds_read_b128 a[76:79],v2 offset:49152\n"
MF "v[240:243],a[208:211],a[152:155],v[240:243]\n"
"ds_read_b128 a[116:119],v3 offset:49152\n"
MF "v[248:251],a[208:211],a[156:159],v[248:251]\n"
LD "v11,s[28:31],s82 offen offset:1024 lds\n"
MF "v[248:251],a[212:215],a[196:199],v[248:251]\n"
MF "v[252:255],a[220:223],a[196:199],v[252:255]\n"
TR "a[208:209],v0 offset:8\n"
MF "v[252:255],a[216:219],a[156:159],v[252:255]\n"
TR "a[210:211],v0 offset:520\n"
MF "v[244:247],a[216:219],a[152:155],v[244:247]\n"
MF "v[244:247],a[220:223],a[192:195],v[244:247]\n"
TR "a[212:213],v0 offset:4104\n"
MF "v[236:239],a[220:223],a[188:191],v[236:239]\n"
TR "a[214:215],v0 offset:4616\n"
MF "v[236:239],a[216:219],a[148:151],v[236:239]\n"
LD "v12,s[28:31],s82 offen offset:2048 lds\n"
MF "v[228:231],a[216:219],a[144:147],v[228:231]\n"
MF "v[228:231],a[220:223],a[184:187],v[228:231]\n"
MF "v[220:223],a[220:223],a[180:183],v[220:223]\n"
MF "v[220:223],a[216:219],a[140:143],v[220:223]\n"
"s_waitcnt lgkmcnt(4)\n"
MF "v[16:19],a[200:203],a[60:63],v[16:19]\n"
TR "a[216:217],v1\n"
MF "v[16:19],a[204:207],a[100:103],v[16:19]\n"
TR "a[218:219],v1 offset:512\n"
MF "v[24:27],a[204:207],a[104:107],v[24:27]\n"
LD "v13,s[28:31],s82 offen offset:3072 lds\n"
MF "v[24:27],a[200:203],a[64:67],v[24:27]\n"
TR "a[220:221],v1 offset:4096\n"
MF "v[32:35],a[200:203],a[68:71],v[32:35]\n"
TR "a[222:223],v1 offset:4608\n"
MF "v[32:35],a[204:207],a[108:111],v[32:35]\n"
MF "v[40:43],a[204:207],a[112:115],v[40:43]\n"
MF "v[40:43],a[200:203],a[72:75],v[40:43]\n"
"s_add_u32 s84,s68,41472\n"
MF "v[48:51],a[200:203],a[76:79],v[48:51]\n"
"s_add_u32 m0,s63,24576\n"
MF "v[48:51],a[204:207],a[116:119],v[48:51]\n"
"s_waitcnt lgkmcnt(4)\n"
LD "v4,s[24:27],s84 offen lds\n"
MF "v[52:55],a[212:215],a[116:119],v[52:55]\n"
TR "a[200:201],v1 offset:8\n"
MF "v[52:55],a[208:211],a[76:79],v[52:55]\n"
TR "a[202:203],v1 offset:520\n"
MF "v[44:47],a[208:211],a[72:75],v[44:47]\n"
MF "v[44:47],a[212:215],a[112:115],v[44:47]\n"
TR "a[204:205],v1 offset:4104\n"
MF "v[36:39],a[212:215],a[108:111],v[36:39]\n"
TR "a[206:207],v1 offset:4616\n"
MF "v[36:39],a[208:211],a[68:71],v[36:39]\n"
MF "v[28:31],a[208:211],a[64:67],v[28:31]\n"
MF "v[28:31],a[212:215],a[104:107],v[28:31]\n"
MF "v[20:23],a[212:215],a[100:103],v[20:23]\n"
MF "v[20:23],a[208:211],a[60:63],v[20:23]\n"
"s_waitcnt vmcnt(24) lgkmcnt(0)\n"
"s_barrier\n"
MF "v[56:59],a[216:219],a[60:63],v[56:59]\n"
"s_add_u32 m0,s62,0\n"
TR "a[208:209],v0 offset:16384\n"
MF "v[56:59],a[220:223],a[100:103],v[56:59]\n"
"s_add_u32 s82,s66,3145728\n"
TR "a[210:211],v0 offset:16896\n"
MF "v[64:67],a[220:223],a[104:107],v[64:67]\n"
LD "v6,s[28:31],s82 offen lds\n"
MF "v[64:67],a[216:219],a[64:67],v[64:67]\n"
TR "a[212:213],v0 offset:20480\n"
MF "v[72:75],a[216:219],a[68:71],v[72:75]\n"
TR "a[214:215],v0 offset:20992\n"
MF "v[72:75],a[220:223],a[108:111],v[72:75]\n"
MF "v[80:83],a[220:223],a[112:115],v[80:83]\n"
LD "v11,s[28:31],s82 offen offset:1024 lds\n"
MF "v[80:83],a[216:219],a[72:75],v[80:83]\n"
MF "v[88:91],a[216:219],a[76:79],v[88:91]\n"
MF "v[88:91],a[220:223],a[116:119],v[88:91]\n"
MF "v[92:95],a[204:207],a[116:119],v[92:95]\n"
TR "a[216:217],v0 offset:16392\n"
MF "v[92:95],a[200:203],a[76:79],v[92:95]\n"
TR "a[218:219],v0 offset:16904\n"
MF "v[84:87],a[200:203],a[72:75],v[84:87]\n"
LD "v12,s[28:31],s82 offen offset:2048 lds\n"
MF "v[84:87],a[204:207],a[112:115],v[84:87]\n"
TR "a[220:221],v0 offset:20488\n"
MF "v[76:79],a[204:207],a[108:111],v[76:79]\n"
TR "a[222:223],v0 offset:21000\n"
MF "v[76:79],a[200:203],a[68:71],v[76:79]\n"
MF "v[68:71],a[200:203],a[64:67],v[68:71]\n"
MF "v[68:71],a[204:207],a[104:107],v[68:71]\n"
MF "v[60:63],a[204:207],a[100:103],v[60:63]\n"
LD "v13,s[28:31],s82 offen offset:3072 lds\n"
MF "v[60:63],a[200:203],a[60:63],v[60:63]\n"
"s_waitcnt lgkmcnt(4)\n"
MF "v[96:99],a[208:211],a[60:63],v[96:99]\n"
TR "a[200:201],v1 offset:16384\n"
MF "v[96:99],a[212:215],a[100:103],v[96:99]\n"
TR "a[202:203],v1 offset:16896\n"
MF "v[104:107],a[212:215],a[104:107],v[104:107]\n"
MF "v[104:107],a[208:211],a[64:67],v[104:107]\n"
"s_add_u32 s84,s68,640\n"
TR "a[204:205],v1 offset:20480\n"
MF "v[112:115],a[208:211],a[68:71],v[112:115]\n"
"s_add_u32 m0,s63,40960\n"
TR "a[206:207],v1 offset:20992\n"
MF "v[112:115],a[212:215],a[108:111],v[112:115]\n"
LD "v4,s[24:27],s84 offen lds\n"
MF "v[120:123],a[212:215],a[112:115],v[120:123]\n"
MF "v[120:123],a[208:211],a[72:75],v[120:123]\n"
MF "v[128:131],a[208:211],a[76:79],v[128:131]\n"
"s_add_u32 s84,s68,9856\n"
MF "v[128:131],a[212:215],a[116:119],v[128:131]\n"
"s_waitcnt lgkmcnt(4)\n"
LD "v5,s[24:27],s84 offen offset:1024 lds\n"
MF "v[132:135],a[220:223],a[116:119],v[132:135]\n"
TR "a[208:209],v1 offset:16392\n"
MF "v[132:135],a[216:219],a[76:79],v[132:135]\n"
TR "a[210:211],v1 offset:16904\n"
MF "v[124:127],a[216:219],a[72:75],v[124:127]\n"
MF "v[124:127],a[220:223],a[112:115],v[124:127]\n"
TR "a[212:213],v1 offset:20488\n"
MF "v[116:119],a[220:223],a[108:111],v[116:119]\n"
TR "a[214:215],v1 offset:21000\n"
MF "v[116:119],a[216:219],a[68:71],v[116:119]\n"
MF "v[108:111],a[216:219],a[64:67],v[108:111]\n"
MF "v[108:111],a[220:223],a[104:107],v[108:111]\n"
MF "v[100:103],a[220:223],a[100:103],v[100:103]\n"
MF "v[100:103],a[216:219],a[60:63],v[100:103]\n"
"s_waitcnt vmcnt(23) lgkmcnt(0)\n"
"s_barrier\n"
MF "v[136:139],a[200:203],a[60:63],v[136:139]\n"
TR "a[216:217],v0 offset:32768\n"
MF "v[136:139],a[204:207],a[100:103],v[136:139]\n"
TR "a[218:219],v0 offset:33280\n"
MF "v[144:147],a[204:207],a[104:107],v[144:147]\n"
"ds_read_b128 a[140:143],v2\n"
MF "v[144:147],a[200:203],a[64:67],v[144:147]\n"
"s_add_u32 m0,s62,16384\n"
TR "a[220:221],v0 offset:36864\n"
MF "v[152:155],a[200:203],a[68:71],v[152:155]\n"
"s_add_u32 s82,s66,3145856\n"
TR "a[222:223],v0 offset:37376\n"
MF "v[152:155],a[204:207],a[108:111],v[152:155]\n"
LD "v6,s[28:31],s82 offen lds\n"
MF "v[160:163],a[204:207],a[112:115],v[160:163]\n"
"ds_read_b128 a[180:183],v3\n"
MF "v[160:163],a[200:203],a[72:75],v[160:163]\n"
LD "v11,s[28:31],s82 offen offset:1024 lds\n"
MF "v[168:171],a[200:203],a[76:79],v[168:171]\n"
MF "v[168:171],a[204:207],a[116:119],v[168:171]\n"
LD "v12,s[28:31],s82 offen offset:2048 lds\n"
MF "v[172:175],a[212:215],a[116:119],v[172:175]\n"
TR "a[200:201],v0 offset:32776\n"
MF "v[172:175],a[208:211],a[76:79],v[172:175]\n"
TR "a[202:203],v0 offset:33288\n"
MF "v[164:167],a[208:211],a[72:75],v[164:167]\n"
"ds_read_b128 a[144:147],v2 offset:2048\n"
MF "v[164:167],a[212:215],a[112:115],v[164:167]\n"
TR "a[204:205],v0 offset:36872\n"
MF "v[156:159],a[212:215],a[108:111],v[156:159]\n"
TR "a[206:207],v0 offset:37384\n"
MF "v[156:159],a[208:211],a[68:71],v[156:159]\n"
MF "v[148:151],a[208:211],a[64:67],v[148:151]\n"
"ds_read_b128 a[184:187],v3 offset:2048\n"
MF "v[148:151],a[212:215],a[104:107],v[148:151]\n"
MF "v[140:143],a[212:215],a[100:103],v[140:143]\n"
LD "v13,s[28:31],s82 offen offset:3072 lds\n"
MF "v[140:143],a[208:211],a[60:63],v[140:143]\n"
"s_waitcnt lgkmcnt(7)\n"
MF "v[176:179],a[216:219],a[60:63],v[176:179]\n"
TR "a[208:209],v1 offset:32768\n"
MF "v[176:179],a[220:223],a[100:103],v[176:179]\n"
TR "a[210:211],v1 offset:33280\n"
MF "v[184:187],a[220:223],a[104:107],v[184:187]\n"
"ds_read_b128 a[148:151],v2 offset:4096\n"
MF "v[184:187],a[216:219],a[64:67],v[184:187]\n"
"s_add_u32 s84,s68,21120\n"
TR "a[212:213],v1 offset:36864\n"
MF "v[192:195],a[216:219],a[68:71],v[192:195]\n"
"s_add_u32 m0,s63,43008\n"
TR "a[214:215],v1 offset:37376\n"
MF "v[192:195],a[220:223],a[108:111],v[192:195]\n"
LD "v4,s[24:27],s84 offen lds\n"
MF "v[200:203],a[220:223],a[112:115],v[200:203]\n"
"ds_read_b128 a[188:191],v3 offset:4096\n"
MF "v[200:203],a[216:219],a[72:75],v[200:203]\n"
MF "v[208:211],a[216:219],a[76:79],v[208:211]\n"
"s_add_u32 s84,s68,30336\n"
MF "v[208:211],a[220:223],a[116:119],v[208:211]\n"
"s_waitcnt lgkmcnt(7)\n"
LD "v5,s[24:27],s84 offen offset:1024 lds\n"
MF "v[212:215],a[204:207],a[116:119],v[212:215]\n"
TR "a[216:217],v1 offset:32776\n"
MF "v[212:215],a[200:203],a[76:79],v[212:215]\n"
TR "a[218:219],v1 offset:33288\n"
MF "v[204:207],a[200:203],a[72:75],v[204:207]\n"
"ds_read_b128 a[152:155],v2 offset:6144\n"
MF "v[204:207],a[204:207],a[112:115],v[204:207]\n"
TR "a[220:221],v1 offset:36872\n"
MF "v[196:199],a[204:207],a[108:111],v[196:199]\n"
TR "a[222:223],v1 offset:37384\n"
MF "v[196:199],a[200:203],a[68:71],v[196:199]\n"
MF "v[188:191],a[200:203],a[64:67],v[188:191]\n"
"v_add_u32 v0,0xc000,v0\n"
MF "v[188:191],a[204:207],a[104:107],v[188:191]\n"
"v_add_u32 v1,0xc000,v1\n"
MF "v[180:183],a[204:207],a[100:103],v[180:183]\n"
MF "v[180:183],a[200:203],a[60:63],v[180:183]\n"
"s_waitcnt vmcnt(25) lgkmcnt(0)\n"
"s_barrier\n"
MF "v[216:219],a[208:211],a[60:63],v[216:219]\n"
TR "a[200:201],v0\n"
MF "v[216:219],a[212:215],a[100:103],v[216:219]\n"
TR "a[202:203],v0 offset:512\n"
MF "v[224:227],a[212:215],a[104:107],v[224:227]\n"
"ds_read_b128 a[192:195],v3 offset:6144\n"
MF "v[224:227],a[208:211],a[64:67],v[224:227]\n"
"s_add_u32 m0,s62,32768\n"
TR "a[204:205],v0 offset:4096\n"
MF "v[232:235],a[208:211],a[68:71],v[232:235]\n"
"s_add_u32 s82,s66,3145984\n"
TR "a[206:207],v0 offset:4608\n"
MF "v[232:235],a[212:215],a[108:111],v[232:235]\n"
LD "v6,s[28:31],s82 offen lds\n"
MF "v[240:243],a[212:215],a[112:115],v[240:243]\n"
"ds_read_b128 a[156:159],v2 offset:8192\n"
MF "v[240:243],a[208:211],a[72:75],v[240:243]\n"
"ds_read_b128 a[196:199],v3 offset:8192\n"
MF "v[248:251],a[208:211],a[76:79],v[248:251]\n"
LD "v11,s[28:31],s82 offen offset:1024 lds\n"
MF "v[248:251],a[212:215],a[116:119],v[248:251]\n"
MF "v[252:255],a[220:223],a[116:119],v[252:255]\n"
TR "a[208:209],v0 offset:8\n"
MF "v[252:255],a[216:219],a[76:79],v[252:255]\n"
TR "a[210:211],v0 offset:520\n"
MF "v[244:247],a[216:219],a[72:75],v[244:247]\n"
MF "v[244:247],a[220:223],a[112:115],v[244:247]\n"
TR "a[212:213],v0 offset:4104\n"
MF "v[236:239],a[220:223],a[108:111],v[236:239]\n"
TR "a[214:215],v0 offset:4616\n"
MF "v[236:239],a[216:219],a[68:71],v[236:239]\n"
LD "v12,s[28:31],s82 offen offset:2048 lds\n"
MF "v[228:231],a[216:219],a[64:67],v[228:231]\n"
MF "v[228:231],a[220:223],a[104:107],v[228:231]\n"
MF "v[220:223],a[220:223],a[100:103],v[220:223]\n"
MF "v[220:223],a[216:219],a[60:63],v[220:223]\n"
"s_waitcnt lgkmcnt(4)\n"
MF "v[16:19],a[200:203],a[140:143],v[16:19]\n"
TR "a[216:217],v1\n"
MF "v[16:19],a[204:207],a[180:183],v[16:19]\n"
TR "a[218:219],v1 offset:512\n"
MF "v[24:27],a[204:207],a[184:187],v[24:27]\n"
LD "v13,s[28:31],s82 offen offset:3072 lds\n"
MF "v[24:27],a[200:203],a[144:147],v[24:27]\n"
TR "a[220:221],v1 offset:4096\n"
MF "v[32:35],a[200:203],a[148:151],v[32:35]\n"
TR "a[222:223],v1 offset:4608\n"
MF "v[32:35],a[204:207],a[188:191],v[32:35]\n"
MF "v[40:43],a[204:207],a[192:195],v[40:43]\n"
MF "v[40:43],a[200:203],a[152:155],v[40:43]\n"
"s_add_u32 s84,s68,41600\n"
MF "v[48:51],a[200:203],a[156:159],v[48:51]\n"
"s_add_u32 m0,s63,45056\n"
MF "v[48:51],a[204:207],a[196:199],v[48:51]\n"
"s_waitcnt lgkmcnt(4)\n"
LD "v4,s[24:27],s84 offen lds\n"
MF "v[52:55],a[212:215],a[196:199],v[52:55]\n"
TR "a[200:201],v1 offset:8\n"
MF "v[52:55],a[208:211],a[156:159],v[52:55]\n"
TR "a[202:203],v1 offset:520\n"
MF "v[44:47],a[208:211],a[152:155],v[44:47]\n"
MF "v[44:47],a[212:215],a[192:195],v[44:47]\n"
TR "a[204:205],v1 offset:4104\n"
MF "v[36:39],a[212:215],a[188:191],v[36:39]\n"
TR "a[206:207],v1 offset:4616\n"
MF "v[36:39],a[208:211],a[148:151],v[36:39]\n"
MF "v[28:31],a[208:211],a[144:147],v[28:31]\n"
MF "v[28:31],a[212:215],a[184:187],v[28:31]\n"
MF "v[20:23],a[212:215],a[180:183],v[20:23]\n"
MF "v[20:23],a[208:211],a[140:143],v[20:23]\n"
"s_waitcnt vmcnt(24) lgkmcnt(0)\n"
"s_barrier\n"
MF "v[56:59],a[216:219],a[140:143],v[56:59]\n"
"s_add_u32 m0,s62,49152\n"
TR "a[208:209],v0 offset:16384\n"
MF "v[56:59],a[220:223],a[180:183],v[56:59]\n"
"s_add_u32 s82,s66,3932160\n"
TR "a[210:211],v0 offset:16896\n"
MF "v[64:67],a[220:223],a[184:187],v[64:67]\n"
LD "v6,s[28:31],s82 offen lds\n"
MF "v[64:67],a[216:219],a[144:147],v[64:67]\n"
TR "a[212:213],v0 offset:20480\n"
MF "v[72:75],a[216:219],a[148:151],v[72:75]\n"
TR "a[214:215],v0 offset:20992\n"
MF "v[72:75],a[220:223],a[188:191],v[72:75]\n"
MF "v[80:83],a[220:223],a[192:195],v[80:83]\n"
LD "v11,s[28:31],s82 offen offset:1024 lds\n"
MF "v[80:83],a[216:219],a[152:155],v[80:83]\n"
MF "v[88:91],a[216:219],a[156:159],v[88:91]\n"
MF "v[88:91],a[220:223],a[196:199],v[88:91]\n"
MF "v[92:95],a[204:207],a[196:199],v[92:95]\n"
TR "a[216:217],v0 offset:16392\n"
MF "v[92:95],a[200:203],a[156:159],v[92:95]\n"
TR "a[218:219],v0 offset:16904\n"
MF "v[84:87],a[200:203],a[152:155],v[84:87]\n"
LD "v12,s[28:31],s82 offen offset:2048 lds\n"
MF "v[84:87],a[204:207],a[192:195],v[84:87]\n"
TR "a[220:221],v0 offset:20488\n"
MF "v[76:79],a[204:207],a[188:191],v[76:79]\n"
TR "a[222:223],v0 offset:21000\n"
MF "v[76:79],a[200:203],a[148:151],v[76:79]\n"
MF "v[68:71],a[200:203],a[144:147],v[68:71]\n"
MF "v[68:71],a[204:207],a[184:187],v[68:71]\n"
MF "v[60:63],a[204:207],a[180:183],v[60:63]\n"
LD "v13,s[28:31],s82 offen offset:3072 lds\n"
MF "v[60:63],a[200:203],a[140:143],v[60:63]\n"
"s_waitcnt lgkmcnt(4)\n"
MF "v[96:99],a[208:211],a[140:143],v[96:99]\n"
TR "a[200:201],v1 offset:16384\n"
MF "v[96:99],a[212:215],a[180:183],v[96:99]\n"
TR "a[202:203],v1 offset:16896\n"
MF "v[104:107],a[212:215],a[184:187],v[104:107]\n"
MF "v[104:107],a[208:211],a[144:147],v[104:107]\n"
"s_add_u32 s84,s68,768\n"
TR "a[204:205],v1 offset:20480\n"
MF "v[112:115],a[208:211],a[148:151],v[112:115]\n"
"s_add_u32 m0,s63,0\n"
TR "a[206:207],v1 offset:20992\n"
MF "v[112:115],a[212:215],a[188:191],v[112:115]\n"
LD "v4,s[24:27],s84 offen lds\n"
MF "v[120:123],a[212:215],a[192:195],v[120:123]\n"
MF "v[120:123],a[208:211],a[152:155],v[120:123]\n"
MF "v[128:131],a[208:211],a[156:159],v[128:131]\n"
"s_add_u32 s84,s68,9984\n"
MF "v[128:131],a[212:215],a[196:199],v[128:131]\n"
"s_waitcnt lgkmcnt(4)\n"
LD "v5,s[24:27],s84 offen offset:1024 lds\n"
MF "v[132:135],a[220:223],a[196:199],v[132:135]\n"
TR "a[208:209],v1 offset:16392\n"
MF "v[132:135],a[216:219],a[156:159],v[132:135]\n"
TR "a[210:211],v1 offset:16904\n"
MF "v[124:127],a[216:219],a[152:155],v[124:127]\n"
MF "v[124:127],a[220:223],a[192:195],v[124:127]\n"
TR "a[212:213],v1 offset:20488\n"
MF "v[116:119],a[220:223],a[188:191],v[116:119]\n"
TR "a[214:215],v1 offset:21000\n"
MF "v[116:119],a[216:219],a[148:151],v[116:119]\n"
MF "v[108:111],a[216:219],a[144:147],v[108:111]\n"
MF "v[108:111],a[220:223],a[184:187],v[108:111]\n"
MF "v[100:103],a[220:223],a[180:183],v[100:103]\n"
MF "v[100:103],a[216:219],a[140:143],v[100:103]\n"
"s_waitcnt vmcnt(23) lgkmcnt(0)\n"
"s_barrier\n"
MF "v[136:139],a[200:203],a[140:143],v[136:139]\n"
TR "a[216:217],v0 offset:32768\n"
MF "v[136:139],a[204:207],a[180:183],v[136:139]\n"
TR "a[218:219],v0 offset:33280\n"
MF "v[144:147],a[204:207],a[184:187],v[144:147]\n"
"ds_read_b128 a[60:63],v2 offset:20480\n"
MF "v[144:147],a[200:203],a[144:147],v[144:147]\n"
"s_add_u32 m0,s62,65536\n"
TR "a[220:221],v0 offset:36864\n"
MF "v[152:155],a[200:203],a[148:151],v[152:155]\n"
"s_add_u32 s82,s66,3932288\n"
TR "a[222:223],v0 offset:37376\n"
MF "v[152:155],a[204:207],a[188:191],v[152:155]\n"
LD "v6,s[28:31],s82 offen lds\n"
MF "v[160:163],a[204:207],a[192:195],v[160:163]\n"
"ds_read_b128 a[100:103],v3 offset:20480\n"
MF "v[160:163],a[200:203],a[152:155],v[160:163]\n"
LD "v11,s[28:31],s82 offen offset:1024 lds\n"
MF "v[168:171],a[200:203],a[156:159],v[168:171]\n"
MF "v[168:171],a[204:207],a[196:199],v[168:171]\n"
LD "v12,s[28:31],s82 offen offset:2048 lds\n"
MF "v[172:175],a[212:215],a[196:199],v[172:175]\n"
TR "a[200:201],v0 offset:32776\n"
MF "v[172:175],a[208:211],a[156:159],v[172:175]\n"
TR "a[202:203],v0 offset:33288\n"
MF "v[164:167],a[208:211],a[152:155],v[164:167]\n"
"ds_read_b128 a[64:67],v2 offset:22528\n"
MF "v[164:167],a[212:215],a[192:195],v[164:167]\n"
TR "a[204:205],v0 offset:36872\n"
MF "v[156:159],a[212:215],a[188:191],v[156:159]\n"
TR "a[206:207],v0 offset:37384\n"
MF "v[156:159],a[208:211],a[148:151],v[156:159]\n"
MF "v[148:151],a[208:211],a[144:147],v[148:151]\n"
"ds_read_b128 a[104:107],v3 offset:22528\n"
MF "v[148:151],a[212:215],a[184:187],v[148:151]\n"
MF "v[140:143],a[212:215],a[180:183],v[140:143]\n"
LD "v13,s[28:31],s82 offen offset:3072 lds\n"
MF "v[140:143],a[208:211],a[140:143],v[140:143]\n"
"s_waitcnt lgkmcnt(7)\n"
MF "v[176:179],a[216:219],a[140:143],v[176:179]\n"
TR "a[208:209],v1 offset:32768\n"
MF "v[176:179],a[220:223],a[180:183],v[176:179]\n"
TR "a[210:211],v1 offset:33280\n"
MF "v[184:187],a[220:223],a[184:187],v[184:187]\n"
"ds_read_b128 a[68:71],v2 offset:24576\n"
MF "v[184:187],a[216:219],a[144:147],v[184:187]\n"
"s_add_u32 s84,s68,21248\n"
TR "a[212:213],v1 offset:36864\n"
MF "v[192:195],a[216:219],a[148:151],v[192:195]\n"
"s_add_u32 m0,s63,2048\n"
TR "a[214:215],v1 offset:37376\n"
MF "v[192:195],a[220:223],a[188:191],v[192:195]\n"
LD "v4,s[24:27],s84 offen lds\n"
MF "v[200:203],a[220:223],a[192:195],v[200:203]\n"
"ds_read_b128 a[108:111],v3 offset:24576\n"
MF "v[200:203],a[216:219],a[152:155],v[200:203]\n"
MF "v[208:211],a[216:219],a[156:159],v[208:211]\n"
"s_add_u32 s84,s68,30464\n"
MF "v[208:211],a[220:223],a[196:199],v[208:211]\n"
"s_waitcnt lgkmcnt(7)\n"
LD "v5,s[24:27],s84 offen offset:1024 lds\n"
MF "v[212:215],a[204:207],a[196:199],v[212:215]\n"
TR "a[216:217],v1 offset:32776\n"
MF "v[212:215],a[200:203],a[156:159],v[212:215]\n"
TR "a[218:219],v1 offset:33288\n"
MF "v[204:207],a[200:203],a[152:155],v[204:207]\n"
"ds_read_b128 a[72:75],v2 offset:26624\n"
MF "v[204:207],a[204:207],a[192:195],v[204:207]\n"
TR "a[220:221],v1 offset:36872\n"
MF "v[196:199],a[204:207],a[188:191],v[196:199]\n"
TR "a[222:223],v1 offset:37384\n"
MF "v[196:199],a[200:203],a[148:151],v[196:199]\n"
MF "v[188:191],a[200:203],a[144:147],v[188:191]\n"
"v_add_u32 v0,0xffff4000,v0\n"
MF "v[188:191],a[204:207],a[184:187],v[188:191]\n"
"v_add_u32 v1,0xffff4000,v1\n"
MF "v[180:183],a[204:207],a[180:183],v[180:183]\n"
MF "v[180:183],a[200:203],a[140:143],v[180:183]\n"
"s_waitcnt vmcnt(25) lgkmcnt(0)\n"
"s_barrier\n"
MF "v[216:219],a[208:211],a[140:143],v[216:219]\n"
TR "a[200:201],v0\n"
MF "v[216:219],a[212:215],a[180:183],v[216:219]\n"
TR "a[202:203],v0 offset:512\n"
MF "v[224:227],a[212:215],a[184:187],v[224:227]\n"
"ds_read_b128 a[112:115],v3 offset:26624\n"
MF "v[224:227],a[208:211],a[144:147],v[224:227]\n"
"s_add_u32 m0,s62,81920\n"
TR "a[204:205],v0 offset:4096\n"
MF "v[232:235],a[208:211],a[148:151],v[232:235]\n"
"s_add_u32 s82,s66,3932416\n"
TR "a[206:207],v0 offset:4608\n"
MF "v[232:235],a[212:215],a[188:191],v[232:235]\n"
LD "v6,s[28:31],s82 offen lds\n"
MF "v[240:243],a[212:215],a[192:195],v[240:243]\n"
"ds_read_b128 a[76:79],v2 offset:28672\n"
MF "v[240:243],a[208:211],a[152:155],v[240:243]\n"
"ds_read_b128 a[116:119],v3 offset:28672\n"
MF "v[248:251],a[208:211],a[156:159],v[248:251]\n"
LD "v11,s[28:31],s82 offen offset:1024 lds\n"
MF "v[248:251],a[212:215],a[196:199],v[248:251]\n"
MF "v[252:255],a[220:223],a[196:199],v[252:255]\n"
TR "a[208:209],v0 offset:8\n"
MF "v[252:255],a[216:219],a[156:159],v[252:255]\n"
TR "a[210:211],v0 offset:520\n"
MF "v[244:247],a[216:219],a[152:155],v[244:247]\n"
MF "v[244:247],a[220:223],a[192:195],v[244:247]\n"
TR "a[212:213],v0 offset:4104\n"
MF "v[236:239],a[220:223],a[188:191],v[236:239]\n"
TR "a[214:215],v0 offset:4616\n"
MF "v[236:239],a[216:219],a[148:151],v[236:239]\n"
LD "v12,s[28:31],s82 offen offset:2048 lds\n"
MF "v[228:231],a[216:219],a[144:147],v[228:231]\n"
MF "v[228:231],a[220:223],a[184:187],v[228:231]\n"
MF "v[220:223],a[220:223],a[180:183],v[220:223]\n"
MF "v[220:223],a[216:219],a[140:143],v[220:223]\n"
"s_waitcnt lgkmcnt(4)\n"
MF "v[16:19],a[200:203],a[60:63],v[16:19]\n"
TR "a[216:217],v1\n"
MF "v[16:19],a[204:207],a[100:103],v[16:19]\n"
TR "a[218:219],v1 offset:512\n"
MF "v[24:27],a[204:207],a[104:107],v[24:27]\n"
LD "v13,s[28:31],s82 offen offset:3072 lds\n"
MF "v[24:27],a[200:203],a[64:67],v[24:27]\n"
TR "a[220:221],v1 offset:4096\n"
MF "v[32:35],a[200:203],a[68:71],v[32:35]\n"
TR "a[222:223],v1 offset:4608\n"
MF "v[32:35],a[204:207],a[108:111],v[32:35]\n"
MF "v[40:43],a[204:207],a[112:115],v[40:43]\n"
MF "v[40:43],a[200:203],a[72:75],v[40:43]\n"
"s_add_u32 s84,s68,41728\n"
MF "v[48:51],a[200:203],a[76:79],v[48:51]\n"
"s_add_u32 m0,s63,4096\n"
MF "v[48:51],a[204:207],a[116:119],v[48:51]\n"
"s_waitcnt lgkmcnt(4)\n"
LD "v4,s[24:27],s84 offen lds\n"
MF "v[52:55],a[212:215],a[116:119],v[52:55]\n"
TR "a[200:201],v1 offset:8\n"
MF "v[52:55],a[208:211],a[76:79],v[52:55]\n"
TR "a[202:203],v1 offset:520\n"
MF "v[44:47],a[208:211],a[72:75],v[44:47]\n"
MF "v[44:47],a[212:215],a[112:115],v[44:47]\n"
TR "a[204:205],v1 offset:4104\n"
MF "v[36:39],a[212:215],a[108:111],v[36:39]\n"
TR "a[206:207],v1 offset:4616\n"
MF "v[36:39],a[208:211],a[68:71],v[36:39]\n"
MF "v[28:31],a[208:211],a[64:67],v[28:31]\n"
MF "v[28:31],a[212:215],a[104:107],v[28:31]\n"
MF "v[20:23],a[212:215],a[100:103],v[20:23]\n"
MF "v[20:23],a[208:211],a[60:63],v[20:23]\n"
"s_waitcnt vmcnt(24) lgkmcnt(0)\n"
"s_barrier\n"
MF "v[56:59],a[216:219],a[60:63],v[56:59]\n"
"s_add_u32 m0,s62,0\n"
TR "a[208:209],v0 offset:16384\n"
MF "v[56:59],a[220:223],a[100:103],v[56:59]\n"
"s_add_u32 s82,s66,4718592\n"
TR "a[210:211],v0 offset:16896\n"
MF "v[64:67],a[220:223],a[104:107],v[64:67]\n"
LD "v6,s[28:31],s82 offen lds\n"
MF "v[64:67],a[216:219],a[64:67],v[64:67]\n"
TR "a[212:213],v0 offset:20480\n"
MF "v[72:75],a[216:219],a[68:71],v[72:75]\n"
TR "a[214:215],v0 offset:20992\n"
MF "v[72:75],a[220:223],a[108:111],v[72:75]\n"
MF "v[80:83],a[220:223],a[112:115],v[80:83]\n"
LD "v11,s[28:31],s82 offen offset:1024 lds\n"
MF "v[80:83],a[216:219],a[72:75],v[80:83]\n"
MF "v[88:91],a[216:219],a[76:79],v[88:91]\n"
MF "v[88:91],a[220:223],a[116:119],v[88:91]\n"
MF "v[92:95],a[204:207],a[116:119],v[92:95]\n"
TR "a[216:217],v0 offset:16392\n"
MF "v[92:95],a[200:203],a[76:79],v[92:95]\n"
TR "a[218:219],v0 offset:16904\n"
MF "v[84:87],a[200:203],a[72:75],v[84:87]\n"
LD "v12,s[28:31],s82 offen offset:2048 lds\n"
MF "v[84:87],a[204:207],a[112:115],v[84:87]\n"
TR "a[220:221],v0 offset:20488\n"
MF "v[76:79],a[204:207],a[108:111],v[76:79]\n"
TR "a[222:223],v0 offset:21000\n"
MF "v[76:79],a[200:203],a[68:71],v[76:79]\n"
MF "v[68:71],a[200:203],a[64:67],v[68:71]\n"
MF "v[68:71],a[204:207],a[104:107],v[68:71]\n"
MF "v[60:63],a[204:207],a[100:103],v[60:63]\n"
LD "v13,s[28:31],s82 offen offset:3072 lds\n"
MF "v[60:63],a[200:203],a[60:63],v[60:63]\n"
"s_waitcnt lgkmcnt(4)\n"
MF "v[96:99],a[208:211],a[60:63],v[96:99]\n"
TR "a[200:201],v1 offset:16384\n"
MF "v[96:99],a[212:215],a[100:103],v[96:99]\n"
TR "a[202:203],v1 offset:16896\n"
MF "v[104:107],a[212:215],a[104:107],v[104:107]\n"
MF "v[104:107],a[208:211],a[64:67],v[104:107]\n"
"s_add_u32 s84,s68,896\n"
TR "a[204:205],v1 offset:20480\n"
MF "v[112:115],a[208:211],a[68:71],v[112:115]\n"
"s_add_u32 m0,s63,20480\n"
TR "a[206:207],v1 offset:20992\n"
MF "v[112:115],a[212:215],a[108:111],v[112:115]\n"
LD "v4,s[24:27],s84 offen lds\n"
MF "v[120:123],a[212:215],a[112:115],v[120:123]\n"
MF "v[120:123],a[208:211],a[72:75],v[120:123]\n"
MF "v[128:131],a[208:211],a[76:79],v[128:131]\n"
"s_add_u32 s84,s68,10112\n"
MF "v[128:131],a[212:215],a[116:119],v[128:131]\n"
"s_waitcnt lgkmcnt(4)\n"
LD "v5,s[24:27],s84 offen offset:1024 lds\n"
MF "v[132:135],a[220:223],a[116:119],v[132:135]\n"
TR "a[208:209],v1 offset:16392\n"
MF "v[132:135],a[216:219],a[76:79],v[132:135]\n"
TR "a[210:211],v1 offset:16904\n"
MF "v[124:127],a[216:219],a[72:75],v[124:127]\n"
MF "v[124:127],a[220:223],a[112:115],v[124:127]\n"
TR "a[212:213],v1 offset:20488\n"
MF "v[116:119],a[220:223],a[108:111],v[116:119]\n"
TR "a[214:215],v1 offset:21000\n"
MF "v[116:119],a[216:219],a[68:71],v[116:119]\n"
MF "v[108:111],a[216:219],a[64:67],v[108:111]\n"
MF "v[108:111],a[220:223],a[104:107],v[108:111]\n"
MF "v[100:103],a[220:223],a[100:103],v[100:103]\n"
".Lqj:\n"
MF "v[100:103],a[216:219],a[60:63],v[100:103]\n"
"s_waitcnt vmcnt(23) lgkmcnt(0)\n"
"s_barrier\n"
MF "v[136:139],a[200:203],a[60:63],v[136:139]\n"
TR "a[216:217],v0 offset:32768\n"
MF "v[136:139],a[204:207],a[100:103],v[136:139]\n"
TR "a[218:219],v0 offset:33280\n"
MF "v[144:147],a[204:207],a[104:107],v[144:147]\n"
"ds_read_b128 a[140:143],v2 offset:40960\n"
MF "v[144:147],a[200:203],a[64:67],v[144:147]\n"
"s_add_u32 m0,s62,16384\n"
TR "a[220:221],v0 offset:36864\n"
MF "v[152:155],a[200:203],a[68:71],v[152:155]\n"
"s_add_u32 s82,s66,4718720\n"
TR "a[222:223],v0 offset:37376\n"
MF "v[152:155],a[204:207],a[108:111],v[152:155]\n"
LD "v6,s[28:31],s82 offen lds\n"
MF "v[160:163],a[204:207],a[112:115],v[160:163]\n"
"ds_read_b128 a[180:183],v3 offset:40960\n"
MF "v[160:163],a[200:203],a[72:75],v[160:163]\n"
LD "v11,s[28:31],s82 offen offset:1024 lds\n"
MF "v[168:171],a[200:203],a[76:79],v[168:171]\n"
MF "v[168:171],a[204:207],a[116:119],v[168:171]\n"
LD "v12,s[28:31],s82 offen offset:2048 lds\n"
MF "v[172:175],a[212:215],a[116:119],v[172:175]\n"
TR "a[200:201],v0 offset:32776\n"
MF "v[172:175],a[208:211],a[76:79],v[172:175]\n"
TR "a[202:203],v0 offset:33288\n"
MF "v[164:167],a[208:211],a[72:75],v[164:167]\n"
"ds_read_b128 a[144:147],v2 offset:43008\n"
MF "v[164:167],a[212:215],a[112:115],v[164:167]\n"
TR "a[204:205],v0 offset:36872\n"
MF "v[156:159],a[212:215],a[108:111],v[156:159]\n"
TR "a[206:207],v0 offset:37384\n"
MF "v[156:159],a[208:211],a[68:71],v[156:159]\n"
MF "v[148:151],a[208:211],a[64:67],v[148:151]\n"
"ds_read_b128 a[184:187],v3 offset:43008\n"
MF "v[148:151],a[212:215],a[104:107],v[148:151]\n"
MF "v[140:143],a[212:215],a[100:103],v[140:143]\n"
LD "v13,s[28:31],s82 offen offset:3072 lds\n"
MF "v[140:143],a[208:211],a[60:63],v[140:143]\n"
"s_waitcnt lgkmcnt(7)\n"
MF "v[176:179],a[216:219],a[60:63],v[176:179]\n"
TR "a[208:209],v1 offset:32768\n"
MF "v[176:179],a[220:223],a[100:103],v[176:179]\n"
TR "a[210:211],v1 offset:33280\n"
MF "v[184:187],a[220:223],a[104:107],v[184:187]\n"
"ds_read_b128 a[148:151],v2 offset:45056\n"
MF "v[184:187],a[216:219],a[64:67],v[184:187]\n"
"s_add_u32 s84,s68,21376\n"
TR "a[212:213],v1 offset:36864\n"
MF "v[192:195],a[216:219],a[68:71],v[192:195]\n"
"s_add_u32 m0,s63,22528\n"
TR "a[214:215],v1 offset:37376\n"
MF "v[192:195],a[220:223],a[108:111],v[192:195]\n"
LD "v4,s[24:27],s84 offen lds\n"
MF "v[200:203],a[220:223],a[112:115],v[200:203]\n"
"ds_read_b128 a[188:191],v3 offset:45056\n"
MF "v[200:203],a[216:219],a[72:75],v[200:203]\n"
MF "v[208:211],a[216:219],a[76:79],v[208:211]\n"
"s_add_u32 s84,s68,30592\n"
MF "v[208:211],a[220:223],a[116:119],v[208:211]\n"
"s_waitcnt lgkmcnt(7)\n"
LD "v5,s[24:27],s84 offen offset:1024 lds\n"
MF "v[212:215],a[204:207],a[116:119],v[212:215]\n"
TR "a[216:217],v1 offset:32776\n"
MF "v[212:215],a[200:203],a[76:79],v[212:215]\n"
TR "a[218:219],v1 offset:33288\n"
MF "v[204:207],a[200:203],a[72:75],v[204:207]\n"
"ds_read_b128 a[152:155],v2 offset:47104\n"
MF "v[204:207],a[204:207],a[112:115],v[204:207]\n"
TR "a[220:221],v1 offset:36872\n"
MF "v[196:199],a[204:207],a[108:111],v[196:199]\n"
TR "a[222:223],v1 offset:37384\n"
MF "v[196:199],a[200:203],a[68:71],v[196:199]\n"
MF "v[188:191],a[200:203],a[64:67],v[188:191]\n"
"v_add_u32 v0,0xc000,v0\n"
MF "v[188:191],a[204:207],a[104:107],v[188:191]\n"
"v_add_u32 v1,0xc000,v1\n"
MF "v[180:183],a[204:207],a[100:103],v[180:183]\n"
MF "v[180:183],a[200:203],a[60:63],v[180:183]\n"
"s_waitcnt vmcnt(25) lgkmcnt(0)\n"
"s_barrier\n"
MF "v[216:219],a[208:211],a[60:63],v[216:219]\n"
TR "a[200:201],v0\n"
MF "v[216:219],a[212:215],a[100:103],v[216:219]\n"
TR "a[202:203],v0 offset:512\n"
MF "v[224:227],a[212:215],a[104:107],v[224:227]\n"
"ds_read_b128 a[192:195],v3 offset:47104\n"
MF "v[224:227],a[208:211],a[64:67],v[224:227]\n"
"s_add_u32 m0,s62,32768\n"
TR "a[204:205],v0 offset:4096\n"
MF "v[232:235],a[208:211],a[68:71],v[232:235]\n"
"s_add_u32 s82,s66,4718848\n"
TR "a[206:207],v0 offset:4608\n"
MF "v[232:235],a[212:215],a[108:111],v[232:235]\n"
LD "v6,s[28:31],s82 offen lds\n"
MF "v[240:243],a[212:215],a[112:115],v[240:243]\n"
"ds_read_b128 a[156:159],v2 offset:49152\n"
MF "v[240:243],a[208:211],a[72:75],v[240:243]\n"
"ds_read_b128 a[196:199],v3 offset:49152\n"
MF "v[248:251],a[208:211],a[76:79],v[248:251]\n"
LD "v11,s[28:31],s82 offen offset:1024 lds\n"
MF "v[248:251],a[212:215],a[116:119],v[248:251]\n"
MF "v[252:255],a[220:223],a[116:119],v[252:255]\n"
TR "a[208:209],v0 offset:8\n"
MF "v[252:255],a[216:219],a[76:79],v[252:255]\n"
TR "a[210:211],v0 offset:520\n"
MF "v[244:247],a[216:219],a[72:75],v[244:247]\n"
MF "v[244:247],a[220:223],a[112:115],v[244:247]\n"
TR "a[212:213],v0 offset:4104\n"
MF "v[236:239],a[220:223],a[108:111],v[236:239]\n"
TR "a[214:215],v0 offset:4616\n"
MF "v[236:239],a[216:219],a[68:71],v[236:239]\n"
LD "v12,s[28:31],s82 offen offset:2048 lds\n"
MF "v[228:231],a[216:219],a[64:67],v[228:231]\n"
MF "v[228:231],a[220:223],a[104:107],v[228:231]\n"
MF "v[220:223],a[220:223],a[100:103],v[220:223]\n"
MF "v[220:223],a[216:219],a[60:63],v[220:223]\n"
"s_waitcnt lgkmcnt(4)\n"
MF "v[16:19],a[200:203],a[140:143],v[16:19]\n"
TR "a[216:217],v1\n"
MF "v[16:19],a[204:207],a[180:183],v[16:19]\n"
TR "a[218:219],v1 offset:512\n"
MF "v[24:27],a[204:207],a[184:187],v[24:27]\n"
LD "v13,s[28:31],s82 offen offset:3072 lds\n"
MF "v[24:27],a[200:203],a[144:147],v[24:27]\n"
TR "a[220:221],v1 offset:4096\n"
MF "v[32:35],a[200:203],a[148:151],v[32:35]\n"
TR "a[222:223],v1 offset:4608\n"
MF "v[32:35],a[204:207],a[188:191],v[32:35]\n"
MF "v[40:43],a[204:207],a[192:195],v[40:43]\n"
MF "v[40:43],a[200:203],a[152:155],v[40:43]\n"
"s_add_u32 s84,s68,41856\n"
MF "v[48:51],a[200:203],a[156:159],v[48:51]\n"
"s_add_u32 m0,s63,24576\n"
MF "v[48:51],a[204:207],a[196:199],v[48:51]\n"
"s_waitcnt lgkmcnt(4)\n"
LD "v4,s[24:27],s84 offen lds\n"
MF "v[52:55],a[212:215],a[196:199],v[52:55]\n"
TR "a[200:201],v1 offset:8\n"
MF "v[52:55],a[208:211],a[156:159],v[52:55]\n"
TR "a[202:203],v1 offset:520\n"
MF "v[44:47],a[208:211],a[152:155],v[44:47]\n"
MF "v[44:47],a[212:215],a[192:195],v[44:47]\n"
TR "a[204:205],v1 offset:4104\n"
MF "v[36:39],a[212:215],a[188:191],v[36:39]\n"
TR "a[206:207],v1 offset:4616\n"
MF "v[36:39],a[208:211],a[148:151],v[36:39]\n"
MF "v[28:31],a[208:211],a[144:147],v[28:31]\n"
MF "v[28:31],a[212:215],a[184:187],v[28:31]\n"
MF "v[20:23],a[212:215],a[180:183],v[20:23]\n"
MF "v[20:23],a[208:211],a[140:143],v[20:23]\n"
"s_waitcnt vmcnt(24) lgkmcnt(0)\n"
"s_barrier\n"
MF "v[56:59],a[216:219],a[140:143],v[56:59]\n"
"s_add_u32 m0,s62,49152\n"
TR "a[208:209],v0 offset:16384\n"
MF "v[56:59],a[220:223],a[180:183],v[56:59]\n"
"s_add_u32 s82,s66,5505024\n"
TR "a[210:211],v0 offset:16896\n"
MF "v[64:67],a[220:223],a[184:187],v[64:67]\n"
LD "v6,s[28:31],s82 offen lds\n"
MF "v[64:67],a[216:219],a[144:147],v[64:67]\n"
TR "a[212:213],v0 offset:20480\n"
MF "v[72:75],a[216:219],a[148:151],v[72:75]\n"
TR "a[214:215],v0 offset:20992\n"
MF "v[72:75],a[220:223],a[188:191],v[72:75]\n"
MF "v[80:83],a[220:223],a[192:195],v[80:83]\n"
LD "v11,s[28:31],s82 offen offset:1024 lds\n"
MF "v[80:83],a[216:219],a[152:155],v[80:83]\n"
MF "v[88:91],a[216:219],a[156:159],v[88:91]\n"
MF "v[88:91],a[220:223],a[196:199],v[88:91]\n"
MF "v[92:95],a[204:207],a[196:199],v[92:95]\n"
TR "a[216:217],v0 offset:16392\n"
MF "v[92:95],a[200:203],a[156:159],v[92:95]\n"
TR "a[218:219],v0 offset:16904\n"
MF "v[84:87],a[200:203],a[152:155],v[84:87]\n"
LD "v12,s[28:31],s82 offen offset:2048 lds\n"
MF "v[84:87],a[204:207],a[192:195],v[84:87]\n"
TR "a[220:221],v0 offset:20488\n"
MF "v[76:79],a[204:207],a[188:191],v[76:79]\n"
TR "a[222:223],v0 offset:21000\n"
MF "v[76:79],a[200:203],a[148:151],v[76:79]\n"
MF "v[68:71],a[200:203],a[144:147],v[68:71]\n"
MF "v[68:71],a[204:207],a[184:187],v[68:71]\n"
MF "v[60:63],a[204:207],a[180:183],v[60:63]\n"
LD "v13,s[28:31],s82 offen offset:3072 lds\n"
MF "v[60:63],a[200:203],a[140:143],v[60:63]\n"
"s_waitcnt lgkmcnt(4)\n"
MF "v[96:99],a[208:211],a[140:143],v[96:99]\n"
TR "a[200:201],v1 offset:16384\n"
MF "v[96:99],a[212:215],a[180:183],v[96:99]\n"
TR "a[202:203],v1 offset:16896\n"
MF "v[104:107],a[212:215],a[184:187],v[104:107]\n"
MF "v[104:107],a[208:211],a[144:147],v[104:107]\n"
"s_add_u32 s84,s68,1024\n"
TR "a[204:205],v1 offset:20480\n"
MF "v[112:115],a[208:211],a[148:151],v[112:115]\n"
"s_add_u32 m0,s63,40960\n"
TR "a[206:207],v1 offset:20992\n"
MF "v[112:115],a[212:215],a[188:191],v[112:115]\n"
LD "v4,s[24:27],s84 offen lds\n"
MF "v[120:123],a[212:215],a[192:195],v[120:123]\n"
MF "v[120:123],a[208:211],a[152:155],v[120:123]\n"
MF "v[128:131],a[208:211],a[156:159],v[128:131]\n"
"s_add_u32 s84,s68,10240\n"
MF "v[128:131],a[212:215],a[196:199],v[128:131]\n"
"s_waitcnt lgkmcnt(4)\n"
LD "v5,s[24:27],s84 offen offset:1024 lds\n"
MF "v[132:135],a[220:223],a[196:199],v[132:135]\n"
TR "a[208:209],v1 offset:16392\n"
MF "v[132:135],a[216:219],a[156:159],v[132:135]\n"
TR "a[210:211],v1 offset:16904\n"
MF "v[124:127],a[216:219],a[152:155],v[124:127]\n"
MF "v[124:127],a[220:223],a[192:195],v[124:127]\n"
TR "a[212:213],v1 offset:20488\n"
MF "v[116:119],a[220:223],a[188:191],v[116:119]\n"
TR "a[214:215],v1 offset:21000\n"
MF "v[116:119],a[216:219],a[148:151],v[116:119]\n"
MF "v[108:111],a[216:219],a[144:147],v[108:111]\n"
MF "v[108:111],a[220:223],a[184:187],v[108:111]\n"
MF "v[100:103],a[220:223],a[180:183],v[100:103]\n"
MF "v[100:103],a[216:219],a[140:143],v[100:103]\n"
"s_waitcnt vmcnt(23) lgkmcnt(0)\n"
"s_barrier\n"
"s_cmp_eq_u32 s57,1\n"
"s_cbranch_scc0 .Lhs12\n"
"v_bfe_u32 v14,v9,4,5\n"
"v_lshlrev_b32 v14,6,v14\n"
"s_lshl_b32 s58,s78,2\n"
"s_add_u32 s58,s58,s86\n"
"buffer_load_dword v10,v14,s[40:43],s58 offen offset:128 sc1\n"
".Lhs12:\n"
MF "v[136:139],a[200:203],a[140:143],v[136:139]\n"
TR "a[216:217],v0 offset:32768\n"
MF "v[136:139],a[204:207],a[180:183],v[136:139]\n"
TR "a[218:219],v0 offset:33280\n"
MF "v[144:147],a[204:207],a[184:187],v[144:147]\n"
"ds_read_b128 a[60:63],v2\n"
MF "v[144:147],a[200:203],a[144:147],v[144:147]\n"
"s_add_u32 m0,s62,65536\n"
TR "a[220:221],v0 offset:36864\n"
MF "v[152:155],a[200:203],a[148:151],v[152:155]\n"
"s_add_u32 s82,s66,5505152\n"
TR "a[222:223],v0 offset:37376\n"
MF "v[152:155],a[204:207],a[188:191],v[152:155]\n"
LD "v6,s[28:31],s82 offen lds\n"
MF "v[160:163],a[204:207],a[192:195],v[160:163]\n"
"ds_read_b128 a[100:103],v3\n"
MF "v[160:163],a[200:203],a[152:155],v[160:163]\n"
LD "v11,s[28:31],s82 offen offset:1024 lds\n"
MF "v[168:171],a[200:203],a[156:159],v[168:171]\n"
MF "v[168:171],a[204:207],a[196:199],v[168:171]\n"
LD "v12,s[28:31],s82 offen offset:2048 lds\n"
MF "v[172:175],a[212:215],a[196:199],v[172:175]\n"
TR "a[200:201],v0 offset:32776\n"
MF "v[172:175],a[208:211],a[156:159],v[172:175]\n"
TR "a[202:203],v0 offset:33288\n"
MF "v[164:167],a[208:211],a[152:155],v[164:167]\n"
"ds_read_b128 a[64:67],v2 offset:2048\n"
MF "v[164:167],a[212:215],a[192:195],v[164:167]\n"
TR "a[204:205],v0 offset:36872\n"
MF "v[156:159],a[212:215],a[188:191],v[156:159]\n"
TR "a[206:207],v0 offset:37384\n"
MF "v[156:159],a[208:211],a[148:151],v[156:159]\n"
MF "v[148:151],a[208:211],a[144:147],v[148:151]\n"
"ds_read_b128 a[104:107],v3 offset:2048\n"
MF "v[148:151],a[212:215],a[184:187],v[148:151]\n"
MF "v[140:143],a[212:215],a[180:183],v[140:143]\n"
LD "v13,s[28:31],s82 offen offset:3072 lds\n"
MF "v[140:143],a[208:211],a[140:143],v[140:143]\n"
"s_waitcnt lgkmcnt(7)\n"
MF "v[176:179],a[216:219],a[140:143],v[176:179]\n"
TR "a[208:209],v1 offset:32768\n"
MF "v[176:179],a[220:223],a[180:183],v[176:179]\n"
TR "a[210:211],v1 offset:33280\n"
MF "v[184:187],a[220:223],a[184:187],v[184:187]\n"
"ds_read_b128 a[68:71],v2 offset:4096\n"
MF "v[184:187],a[216:219],a[144:147],v[184:187]\n"
"s_add_u32 s84,s68,21504\n"
TR "a[212:213],v1 offset:36864\n"
MF "v[192:195],a[216:219],a[148:151],v[192:195]\n"
"s_add_u32 m0,s63,43008\n"
TR "a[214:215],v1 offset:37376\n"
MF "v[192:195],a[220:223],a[188:191],v[192:195]\n"
LD "v4,s[24:27],s84 offen lds\n"
MF "v[200:203],a[220:223],a[192:195],v[200:203]\n"
"ds_read_b128 a[108:111],v3 offset:4096\n"
MF "v[200:203],a[216:219],a[152:155],v[200:203]\n"
MF "v[208:211],a[216:219],a[156:159],v[208:211]\n"
"s_add_u32 s84,s68,30720\n"
MF "v[208:211],a[220:223],a[196:199],v[208:211]\n"
"s_waitcnt lgkmcnt(7)\n"
LD "v5,s[24:27],s84 offen offset:1024 lds\n"
MF "v[212:215],a[204:207],a[196:199],v[212:215]\n"
TR "a[216:217],v1 offset:32776\n"
MF "v[212:215],a[200:203],a[156:159],v[212:215]\n"
TR "a[218:219],v1 offset:33288\n"
MF "v[204:207],a[200:203],a[152:155],v[204:207]\n"
"ds_read_b128 a[72:75],v2 offset:6144\n"
MF "v[204:207],a[204:207],a[192:195],v[204:207]\n"
TR "a[220:221],v1 offset:36872\n"
MF "v[196:199],a[204:207],a[188:191],v[196:199]\n"
TR "a[222:223],v1 offset:37384\n"
MF "v[196:199],a[200:203],a[148:151],v[196:199]\n"
MF "v[188:191],a[200:203],a[144:147],v[188:191]\n"
"v_add_u32 v0,0xffff4000,v0\n"
MF "v[188:191],a[204:207],a[184:187],v[188:191]\n"
"v_add_u32 v1,0xffff4000,v1\n"
MF "v[180:183],a[204:207],a[180:183],v[180:183]\n"
MF "v[180:183],a[200:203],a[140:143],v[180:183]\n"
"s_waitcnt vmcnt(25) lgkmcnt(0)\n"
"s_barrier\n"
MF "v[216:219],a[208:211],a[140:143],v[216:219]\n"
TR "a[200:201],v0\n"
MF "v[216:219],a[212:215],a[180:183],v[216:219]\n"
TR "a[202:203],v0 offset:512\n"
MF "v[224:227],a[212:215],a[184:187],v[224:227]\n"
"ds_read_b128 a[112:115],v3 offset:6144\n"
MF "v[224:227],a[208:211],a[144:147],v[224:227]\n"
"s_add_u32 m0,s62,81920\n"
TR "a[204:205],v0 offset:4096\n"
MF "v[232:235],a[208:211],a[148:151],v[232:235]\n"
"s_add_u32 s82,s66,5505280\n"
TR "a[206:207],v0 offset:4608\n"
MF "v[232:235],a[212:215],a[188:191],v[232:235]\n"
LD "v6,s[28:31],s82 offen lds\n"
MF "v[240:243],a[212:215],a[192:195],v[240:243]\n"
"ds_read_b128 a[76:79],v2 offset:8192\n"
MF "v[240:243],a[208:211],a[152:155],v[240:243]\n"
"ds_read_b128 a[116:119],v3 offset:8192\n"
MF "v[248:251],a[208:211],a[156:159],v[248:251]\n"
LD "v11,s[28:31],s82 offen offset:1024 lds\n"
MF "v[248:251],a[212:215],a[196:199],v[248:251]\n"
MF "v[252:255],a[220:223],a[196:199],v[252:255]\n"
TR "a[208:209],v0 offset:8\n"
MF "v[252:255],a[216:219],a[156:159],v[252:255]\n"
TR "a[210:211],v0 offset:520\n"
MF "v[244:247],a[216:219],a[152:155],v[244:247]\n"
MF "v[244:247],a[220:223],a[192:195],v[244:247]\n"
TR "a[212:213],v0 offset:4104\n"
MF "v[236:239],a[220:223],a[188:191],v[236:239]\n"
TR "a[214:215],v0 offset:4616\n"
MF "v[236:239],a[216:219],a[148:151],v[236:239]\n"
LD "v12,s[28:31],s82 offen offset:2048 lds\n"
MF "v[228:231],a[216:219],a[144:147],v[228:231]\n"
MF "v[228:231],a[220:223],a[184:187],v[228:231]\n"
MF "v[220:223],a[220:223],a[180:183],v[220:223]\n"
MF "v[220:223],a[216:219],a[140:143],v[220:223]\n"
"s_waitcnt lgkmcnt(4)\n"
MF "v[16:19],a[200:203],a[60:63],v[16:19]\n"
TR "a[216:217],v1\n"
MF "v[16:19],a[204:207],a[100:103],v[16:19]\n"
TR "a[218:219],v1 offset:512\n"
MF "v[24:27],a[204:207],a[104:107],v[24:27]\n"
LD "v13,s[28:31],s82 offen offset:3072 lds\n"
MF "v[24:27],a[200:203],a[64:67],v[24:27]\n"
TR "a[220:221],v1 offset:4096\n"
MF "v[32:35],a[200:203],a[68:71],v[32:35]\n"
TR "a[222:223],v1 offset:4608\n"
MF "v[32:35],a[204:207],a[108:111],v[32:35]\n"
MF "v[40:43],a[204:207],a[112:115],v[40:43]\n"
MF "v[40:43],a[200:203],a[72:75],v[40:43]\n"
"s_add_u32 s84,s68,41984\n"
MF "v[48:51],a[200:203],a[76:79],v[48:51]\n"
"s_add_u32 m0,s63,45056\n"
MF "v[48:51],a[204:207],a[116:119],v[48:51]\n"
"s_waitcnt lgkmcnt(4)\n"
LD "v4,s[24:27],s84 offen lds\n"
MF "v[52:55],a[212:215],a[116:119],v[52:55]\n"
TR "a[200:201],v1 offset:8\n"
MF "v[52:55],a[208:211],a[76:79],v[52:55]\n"
TR "a[202:203],v1 offset:520\n"
MF "v[44:47],a[208:211],a[72:75],v[44:47]\n"
MF "v[44:47],a[212:215],a[112:115],v[44:47]\n"
TR "a[204:205],v1 offset:4104\n"
MF "v[36:39],a[212:215],a[108:111],v[36:39]\n"
TR "a[206:207],v1 offset:4616\n"
MF "v[36:39],a[208:211],a[68:71],v[36:39]\n"
MF "v[28:31],a[208:211],a[64:67],v[28:31]\n"
MF "v[28:31],a[212:215],a[104:107],v[28:31]\n"
MF "v[20:23],a[212:215],a[100:103],v[20:23]\n"
MF "v[20:23],a[208:211],a[60:63],v[20:23]\n"
"s_waitcnt vmcnt(24) lgkmcnt(0)\n"
"s_barrier\n"
MF "v[56:59],a[216:219],a[60:63],v[56:59]\n"
"s_add_u32 m0,s62,0\n"
TR "a[208:209],v0 offset:16384\n"
MF "v[56:59],a[220:223],a[100:103],v[56:59]\n"
"s_add_u32 s82,s66,6291712\n"
TR "a[210:211],v0 offset:16896\n"
MF "v[64:67],a[220:223],a[104:107],v[64:67]\n"
LD "v6,s[28:31],s82 offen lds\n"
MF "v[64:67],a[216:219],a[64:67],v[64:67]\n"
TR "a[212:213],v0 offset:20480\n"
MF "v[72:75],a[216:219],a[68:71],v[72:75]\n"
TR "a[214:215],v0 offset:20992\n"
MF "v[72:75],a[220:223],a[108:111],v[72:75]\n"
MF "v[80:83],a[220:223],a[112:115],v[80:83]\n"
LD "v11,s[28:31],s82 offen offset:1024 lds\n"
MF "v[80:83],a[216:219],a[72:75],v[80:83]\n"
MF "v[88:91],a[216:219],a[76:79],v[88:91]\n"
MF "v[88:91],a[220:223],a[116:119],v[88:91]\n"
MF "v[92:95],a[204:207],a[116:119],v[92:95]\n"
TR "a[216:217],v0 offset:16392\n"
MF "v[92:95],a[200:203],a[76:79],v[92:95]\n"
TR "a[218:219],v0 offset:16904\n"
MF "v[84:87],a[200:203],a[72:75],v[84:87]\n"
LD "v12,s[28:31],s82 offen offset:2048 lds\n"
MF "v[84:87],a[204:207],a[112:115],v[84:87]\n"
TR "a[220:221],v0 offset:20488\n"
MF "v[76:79],a[204:207],a[108:111],v[76:79]\n"
TR "a[222:223],v0 offset:21000\n"
MF "v[76:79],a[200:203],a[68:71],v[76:79]\n"
MF "v[68:71],a[200:203],a[64:67],v[68:71]\n"
LD "v13,s[28:31],s82 offen offset:3072 lds\n"
MF "v[68:71],a[204:207],a[104:107],v[68:71]\n"
MF "v[60:63],a[204:207],a[100:103],v[60:63]\n"
MF "v[60:63],a[200:203],a[60:63],v[60:63]\n"
"s_waitcnt lgkmcnt(4)\n"
MF "v[96:99],a[208:211],a[60:63],v[96:99]\n"
"s_add_u32 s84,s68,1152\n"
TR "a[200:201],v1 offset:16384\n"
MF "v[96:99],a[212:215],a[100:103],v[96:99]\n"
"s_add_u32 m0,s63,0\n"
TR "a[202:203],v1 offset:16896\n"
MF "v[104:107],a[212:215],a[104:107],v[104:107]\n"
LD "v4,s[24:27],s84 offen lds\n"
MF "v[104:107],a[208:211],a[64:67],v[104:107]\n"
TR "a[204:205],v1 offset:20480\n"
MF "v[112:115],a[208:211],a[68:71],v[112:115]\n"
TR "a[206:207],v1 offset:20992\n"
MF "v[112:115],a[212:215],a[108:111],v[112:115]\n"
"s_add_u32 s84,s68,10368\n"
MF "v[120:123],a[212:215],a[112:115],v[120:123]\n"
LD "v5,s[24:27],s84 offen offset:1024 lds\n"
MF "v[120:123],a[208:211],a[72:75],v[120:123]\n"
MF "v[128:131],a[208:211],a[76:79],v[128:131]\n"
MF "v[128:131],a[212:215],a[116:119],v[128:131]\n"
"s_waitcnt lgkmcnt(4)\n"
MF "v[132:135],a[220:223],a[116:119],v[132:135]\n"
TR "a[208:209],v1 offset:16392\n"
MF "v[132:135],a[216:219],a[76:79],v[132:135]\n"
"s_add_u32 s84,s68,19584\n"
TR "a[210:211],v1 offset:16904\n"
MF "v[124:127],a[216:219],a[72:75],v[124:127]\n"
LD "v4,s[24:27],s84 offen offset:2048 lds\n"
MF "v[124:127],a[220:223],a[112:115],v[124:127]\n"
TR "a[212:213],v1 offset:20488\n"
MF "v[116:119],a[220:223],a[108:111],v[116:119]\n"
TR "a[214:215],v1 offset:21000\n"
MF "v[116:119],a[216:219],a[68:71],v[116:119]\n"
MF "v[108:111],a[216:219],a[64:67],v[108:111]\n"
MF "v[108:111],a[220:223],a[104:107],v[108:111]\n"
MF "v[100:103],a[220:223],a[100:103],v[100:103]\n"
MF "v[100:103],a[216:219],a[60:63],v[100:103]\n"
"s_waitcnt vmcnt(24) lgkmcnt(0)\n"
"s_barrier\n"
MF "v[136:139],a[200:203],a[60:63],v[136:139]\n"
TR "a[216:217],v0 offset:32768\n"
MF "v[136:139],a[204:207],a[100:103],v[136:139]\n"
TR "a[218:219],v0 offset:33280\n"
MF "v[144:147],a[204:207],a[104:107],v[144:147]\n"
"ds_read_b128 a[140:143],v2 offset:20480\n"
MF "v[144:147],a[200:203],a[64:67],v[144:147]\n"
"s_add_u32 m0,s62,16384\n"
TR "a[220:221],v0 offset:36864\n"
MF "v[152:155],a[200:203],a[68:71],v[152:155]\n"
"s_add_u32 s82,s66,7078144\n"
TR "a[222:223],v0 offset:37376\n"
MF "v[152:155],a[204:207],a[108:111],v[152:155]\n"
LD "v6,s[28:31],s82 offen lds\n"
MF "v[160:163],a[204:207],a[112:115],v[160:163]\n"
"ds_read_b128 a[180:183],v3 offset:20480\n"
MF "v[160:163],a[200:203],a[72:75],v[160:163]\n"
LD "v11,s[28:31],s82 offen offset:1024 lds\n"
MF "v[168:171],a[200:203],a[76:79],v[168:171]\n"
MF "v[168:171],a[204:207],a[116:119],v[168:171]\n"
LD "v12,s[28:31],s82 offen offset:2048 lds\n"
MF "v[172:175],a[212:215],a[116:119],v[172:175]\n"
TR "a[200:201],v0 offset:32776\n"
MF "v[172:175],a[208:211],a[76:79],v[172:175]\n"
TR "a[202:203],v0 offset:33288\n"
MF "v[164:167],a[208:211],a[72:75],v[164:167]\n"
"ds_read_b128 a[144:147],v2 offset:22528\n"
MF "v[164:167],a[212:215],a[112:115],v[164:167]\n"
TR "a[204:205],v0 offset:36872\n"
MF "v[156:159],a[212:215],a[108:111],v[156:159]\n"
TR "a[206:207],v0 offset:37384\n"
MF "v[156:159],a[208:211],a[68:71],v[156:159]\n"
MF "v[148:151],a[208:211],a[64:67],v[148:151]\n"
"ds_read_b128 a[184:187],v3 offset:22528\n"
MF "v[148:151],a[212:215],a[104:107],v[148:151]\n"
MF "v[140:143],a[212:215],a[100:103],v[140:143]\n"
LD "v13,s[28:31],s82 offen offset:3072 lds\n"
MF "v[140:143],a[208:211],a[60:63],v[140:143]\n"
"s_waitcnt lgkmcnt(7)\n"
MF "v[176:179],a[216:219],a[60:63],v[176:179]\n"
TR "a[208:209],v1 offset:32768\n"
MF "v[176:179],a[220:223],a[100:103],v[176:179]\n"
TR "a[210:211],v1 offset:33280\n"
MF "v[184:187],a[220:223],a[104:107],v[184:187]\n"
"ds_read_b128 a[148:151],v2 offset:24576\n"
MF "v[184:187],a[216:219],a[64:67],v[184:187]\n"
"s_add_u32 s84,s68,31872\n"
TR "a[212:213],v1 offset:36864\n"
MF "v[192:195],a[216:219],a[68:71],v[192:195]\n"
"s_add_u32 m0,s63,3072\n"
TR "a[214:215],v1 offset:37376\n"
MF "v[192:195],a[220:223],a[108:111],v[192:195]\n"
LD "v5,s[24:27],s84 offen lds\n"
MF "v[200:203],a[220:223],a[112:115],v[200:203]\n"
"ds_read_b128 a[188:191],v3 offset:24576\n"
MF "v[200:203],a[216:219],a[72:75],v[200:203]\n"
MF "v[208:211],a[216:219],a[76:79],v[208:211]\n"
"s_add_u32 s84,s68,41088\n"
MF "v[208:211],a[220:223],a[116:119],v[208:211]\n"
"s_waitcnt lgkmcnt(7)\n"
LD "v4,s[24:27],s84 offen offset:1024 lds\n"
MF "v[212:215],a[204:207],a[116:119],v[212:215]\n"
TR "a[216:217],v1 offset:32776\n"
MF "v[212:215],a[200:203],a[76:79],v[212:215]\n"
TR "a[218:219],v1 offset:33288\n"
MF "v[204:207],a[200:203],a[72:75],v[204:207]\n"
"ds_read_b128 a[152:155],v2 offset:26624\n"
MF "v[204:207],a[204:207],a[112:115],v[204:207]\n"
TR "a[220:221],v1 offset:36872\n"
MF "v[196:199],a[204:207],a[108:111],v[196:199]\n"
TR "a[222:223],v1 offset:37384\n"
MF "v[196:199],a[200:203],a[68:71],v[196:199]\n"
MF "v[188:191],a[200:203],a[64:67],v[188:191]\n"
"v_add_u32 v0,0xc000,v0\n"
MF "v[188:191],a[204:207],a[104:107],v[188:191]\n"
"v_add_u32 v1,0xc000,v1\n"
MF "v[180:183],a[204:207],a[100:103],v[180:183]\n"
MF "v[180:183],a[200:203],a[60:63],v[180:183]\n"
"s_waitcnt vmcnt(26) lgkmcnt(0)\n"
"s_barrier\n"
"s_getpc_b64 s[52:53]\n"
".Lctq:\n"
"s_sub_u32 s52,s52,.Lctq-.Lcode0\n"
"s_subb_u32 s53,s53,0\n"
"s_bfe_u32 s58,s23,0x20000\n"
"s_lshl_b32 s58,s58,2\n"
"s_add_u32 s58,s58,s56\n"
"s_lshl_b32 s58,s58,13\n"
"v_lshlrev_b32 v15,3,v9\n"
"v_add_u32 v15,s58,v15\n"
"v_min_u32 v15,.Lcode1-.Lcode0,v15\n"
"global_load_dword a249,v15,s[52:53]\n"
MF "v[216:219],a[208:211],a[60:63],v[216:219]\n"
TR "a[200:201],v0\n"
MF "v[216:219],a[212:215],a[100:103],v[216:219]\n"
TR "a[202:203],v0 offset:512\n"
MF "v[224:227],a[212:215],a[104:107],v[224:227]\n"
"ds_read_b128 a[192:195],v3 offset:26624\n"
MF "v[224:227],a[208:211],a[64:67],v[224:227]\n"
"s_add_u32 m0,s62,32768\n"
TR "a[204:205],v0 offset:4096\n"
MF "v[232:235],a[208:211],a[68:71],v[232:235]\n"
"s_add_u32 s82,s66,6291584\n"
TR "a[206:207],v0 offset:4608\n"
MF "v[232:235],a[212:215],a[108:111],v[232:235]\n"
LD "v6,s[28:31],s82 offen lds\n"
MF "v[240:243],a[212:215],a[112:115],v[240:243]\n"
"ds_read_b128 a[156:159],v2 offset:28672\n"
MF "v[240:243],a[208:211],a[72:75],v[240:243]\n"
"ds_read_b128 a[196:199],v3 offset:28672\n"
MF "v[248:251],a[208:211],a[76:79],v[248:251]\n"
MF "v[248:251],a[212:215],a[116:119],v[248:251]\n"
LD "v11,s[28:31],s82 offen offset:1024 lds\n"
MF "v[252:255],a[220:223],a[116:119],v[252:255]\n"
TR "a[208:209],v0 offset:8\n"
MF "v[252:255],a[216:219],a[76:79],v[252:255]\n"
TR "a[210:211],v0 offset:520\n"
MF "v[244:247],a[216:219],a[72:75],v[244:247]\n"
MF "v[244:247],a[220:223],a[112:115],v[244:247]\n"
TR "a[212:213],v0 offset:4104\n"
MF "v[236:239],a[220:223],a[108:111],v[236:239]\n"
TR "a[214:215],v0 offset:4616\n"
MF "v[236:239],a[216:219],a[68:71],v[236:239]\n"
MF "v[228:231],a[216:219],a[64:67],v[228:231]\n"
MF "v[228:231],a[220:223],a[104:107],v[228:231]\n"
MF "v[220:223],a[220:223],a[100:103],v[220:223]\n"
LD "v12,s[28:31],s82 offen offset:2048 lds\n"
MF "v[220:223],a[216:219],a[60:63],v[220:223]\n"
"s_waitcnt lgkmcnt(4)\n"
MF "v[16:19],a[200:203],a[140:143],v[16:19]\n"
TR "a[216:217],v1\n"
MF "v[16:19],a[204:207],a[180:183],v[16:19]\n"
TR "a[218:219],v1 offset:512\n"
MF "v[24:27],a[204:207],a[184:187],v[24:27]\n"
MF "v[24:27],a[200:203],a[144:147],v[24:27]\n"
TR "a[220:221],v1 offset:4096\n"
MF "v[32:35],a[200:203],a[148:151],v[32:35]\n"
TR "a[222:223],v1 offset:4608\n"
MF "v[32:35],a[204:207],a[188:191],v[32:35]\n"
MF "v[40:43],a[204:207],a[192:195],v[40:43]\n"
MF "v[40:43],a[200:203],a[152:155],v[40:43]\n"
LD "v13,s[28:31],s82 offen offset:3072 lds\n"
MF "v[48:51],a[200:203],a[156:159],v[48:51]\n"
MF "v[48:51],a[204:207],a[196:199],v[48:51]\n"
"s_waitcnt lgkmcnt(4)\n"
MF "v[52:55],a[212:215],a[196:199],v[52:55]\n"
TR "a[200:201],v1 offset:8\n"
MF "v[52:55],a[208:211],a[156:159],v[52:55]\n"
TR "a[202:203],v1 offset:520\n"
MF "v[44:47],a[208:211],a[152:155],v[44:47]\n"
MF "v[44:47],a[212:215],a[192:195],v[44:47]\n"
TR "a[204:205],v1 offset:4104\n"
MF "v[36:39],a[212:215],a[188:191],v[36:39]\n"
TR "a[206:207],v1 offset:4616\n"
MF "v[36:39],a[208:211],a[148:151],v[36:39]\n"
MF "v[28:31],a[208:211],a[144:147],v[28:31]\n"
MF "v[28:31],a[212:215],a[184:187],v[28:31]\n"
MF "v[20:23],a[212:215],a[180:183],v[20:23]\n"
MF "v[20:23],a[208:211],a[140:143],v[20:23]\n"
"s_waitcnt vmcnt(25) lgkmcnt(0)\n"
"s_barrier\n"
MF "v[56:59],a[216:219],a[140:143],v[56:59]\n"
"s_add_u32 m0,s62,49152\n"
TR "a[208:209],v0 offset:16384\n"
MF "v[56:59],a[220:223],a[180:183],v[56:59]\n"
"s_add_u32 s82,s66,7078016\n"
TR "a[210:211],v0 offset:16896\n"
"v_lshrrev_b32 v15,8,v9\n"
MF "v[64:67],a[220:223],a[184:187],v[64:67]\n"
LD "v6,s[28:31],s82 offen lds\n"
MF "v[64:67],a[216:219],a[144:147],v[64:67]\n"
TR "a[212:213],v0 offset:20480\n"
"v_mul_u32_u24 v15,48960,v15\n"
MF "v[72:75],a[216:219],a[148:151],v[72:75]\n"
"s_lshr_b32 s7,s56,1\n"
TR "a[214:215],v0 offset:20992\n"
"v_add_u32 v15,v15,v9\n"
MF "v[72:75],a[220:223],a[188:191],v[72:75]\n"
"s_and_b32 s8,s56,1\n"
MF "v[80:83],a[220:223],a[192:195],v[80:83]\n"
"s_sub_u32 s7,s7,s8\n"
MF "v[80:83],a[216:219],a[152:155],v[80:83]\n"
"s_mul_i32 s7,s7,245760\n"
MF "v[88:91],a[216:219],a[156:159],v[88:91]\n"
"s_add_u32 s7,s7,s89\n"
MF "v[88:91],a[220:223],a[196:199],v[88:91]\n"
LD "v11,s[28:31],s82 offen offset:1024 lds\n"
MF "v[92:95],a[204:207],a[196:199],v[92:95]\n"
TR "a[216:217],v0 offset:16392\n"
MF "v[92:95],a[200:203],a[156:159],v[92:95]\n"
TR "a[218:219],v0 offset:16904\n"
MF "v[84:87],a[200:203],a[152:155],v[84:87]\n"
MF "v[84:87],a[204:207],a[192:195],v[84:87]\n"
TR "a[220:221],v0 offset:20488\n"
MF "v[76:79],a[204:207],a[188:191],v[76:79]\n"
TR "a[222:223],v0 offset:21000\n"
MF "v[76:79],a[200:203],a[148:151],v[76:79]\n"
MF "v[68:71],a[200:203],a[144:147],v[68:71]\n"
MF "v[68:71],a[204:207],a[184:187],v[68:71]\n"
MF "v[60:63],a[204:207],a[180:183],v[60:63]\n"
LD "v12,s[28:31],s82 offen offset:2048 lds\n"
MF "v[60:63],a[200:203],a[140:143],v[60:63]\n"
"s_waitcnt lgkmcnt(4)\n"
MF "v[96:99],a[208:211],a[140:143],v[96:99]\n"
TR "a[200:201],v1 offset:16384\n"
MF "v[96:99],a[212:215],a[180:183],v[96:99]\n"
TR "a[202:203],v1 offset:16896\n"
MF "v[104:107],a[212:215],a[184:187],v[104:107]\n"
MF "v[104:107],a[208:211],a[144:147],v[104:107]\n"
TR "a[204:205],v1 offset:20480\n"
MF "v[112:115],a[208:211],a[148:151],v[112:115]\n"
TR "a[206:207],v1 offset:20992\n"
MF "v[112:115],a[212:215],a[188:191],v[112:115]\n"
MF "v[120:123],a[212:215],a[192:195],v[120:123]\n"
MF "v[120:123],a[208:211],a[152:155],v[120:123]\n"
LD "v13,s[28:31],s82 offen offset:3072 lds\n"
MF "v[128:131],a[208:211],a[156:159],v[128:131]\n"
MF "v[128:131],a[212:215],a[196:199],v[128:131]\n"
"s_waitcnt lgkmcnt(4)\n"
MF "v[132:135],a[220:223],a[196:199],v[132:135]\n"
TR "a[208:209],v1 offset:16392\n"
MF "v[132:135],a[216:219],a[156:159],v[132:135]\n"
TR "a[210:211],v1 offset:16904\n"
MF "v[124:127],a[216:219],a[152:155],v[124:127]\n"
MF "v[124:127],a[220:223],a[192:195],v[124:127]\n"
TR "a[212:213],v1 offset:20488\n"
MF "v[116:119],a[220:223],a[188:191],v[116:119]\n"
TR "a[214:215],v1 offset:21000\n"
MF "v[116:119],a[216:219],a[148:151],v[116:119]\n"
MF "v[108:111],a[216:219],a[144:147],v[108:111]\n"
MF "v[108:111],a[220:223],a[184:187],v[108:111]\n"
MF "v[100:103],a[220:223],a[180:183],v[100:103]\n"
MF "v[100:103],a[216:219],a[140:143],v[100:103]\n"
"s_waitcnt vmcnt(22) lgkmcnt(0)\n"
"s_barrier\n"
"s_cmp_eq_u32 s57,1\n"
"s_cbranch_scc0 .Lhs15\n"
".Lack_q:\n"
"v_cmp_ne_u32 vcc,s76,v10\n"
"s_cbranch_vccnz .Lackspin_q\n"
".Lackok_q:\n"
".Lhs15:\n"
MF "v[136:139],a[200:203],a[140:143],v[136:139]\n"
"s_and_b32 s58,s56,1\n"
TR "a[216:217],v0 offset:32768\n"
MF "v[136:139],a[204:207],a[180:183],v[136:139]\n"
"s_mul_i32 s59,s58,3072\n"
TR "a[218:219],v0 offset:33280\n"
MF "v[144:147],a[204:207],a[184:187],v[144:147]\n"
"s_add_u32 s16,s70,s59\n"
"ds_read_b128 a[60:63],v2 offset:40960\n"
MF "v[144:147],a[200:203],a[144:147],v[144:147]\n"
"s_add_u32 m0,s62,65536\n"
TR "a[220:221],v0 offset:36864\n"
MF "v[152:155],a[200:203],a[148:151],v[152:155]\n"
"s_add_u32 s82,s66,6291456\n"
TR "a[222:223],v0 offset:37376\n"
MF "v[152:155],a[204:207],a[188:191],v[152:155]\n"
LD "v6,s[28:31],s82 offen lds\n"
MF "v[160:163],a[204:207],a[192:195],v[160:163]\n"
"s_mul_i32 s59,s58,6144\n"
"ds_read_b128 a[100:103],v3 offset:40960\n"
MF "v[160:163],a[200:203],a[152:155],v[160:163]\n"
"s_add_u32 s18,s70,s59\n"
MF "v[168:171],a[200:203],a[156:159],v[168:171]\n"
"s_add_u32 s17,s16,196608\n"
MF "v[168:171],a[204:207],a[196:199],v[168:171]\n"
LD "v11,s[28:31],s82 offen offset:1024 lds\n"
MF "v[172:175],a[212:215],a[196:199],v[172:175]\n"
"s_add_u32 s19,s18,196608\n"
TR "a[200:201],v0 offset:32776\n"
MF "v[172:175],a[208:211],a[156:159],v[172:175]\n"
"s_add_u32 s20,s70,196608\n"
TR "a[202:203],v0 offset:33288\n"
MF "v[164:167],a[208:211],a[152:155],v[164:167]\n"
"s_bfe_u32 s58,s23,0x40008\n"
"ds_read_b128 a[64:67],v2 offset:43008\n"
MF "v[164:167],a[212:215],a[192:195],v[164:167]\n"
"s_lshr_b32 s59,s56,1\n"
TR "a[204:205],v0 offset:36872\n"
MF "v[156:159],a[212:215],a[188:191],v[156:159]\n"
"s_mul_i32 s59,s59,5\n"
TR "a[206:207],v0 offset:37384\n"
MF "v[156:159],a[208:211],a[148:151],v[156:159]\n"
"s_add_u32 s58,s58,s59\n"
MF "v[148:151],a[208:211],a[144:147],v[148:151]\n"
"s_and_b32 s58,s58,15\n"
"ds_read_b128 a[104:107],v3 offset:43008\n"
MF "v[148:151],a[212:215],a[184:187],v[148:151]\n"
"s_mul_i32 s58,s58,12288\n"
MF "v[140:143],a[212:215],a[180:183],v[140:143]\n"
LD "v12,s[28:31],s82 offen offset:2048 lds\n"
MF "v[140:143],a[208:211],a[140:143],v[140:143]\n"
"s_waitcnt lgkmcnt(7)\n"
MF "v[176:179],a[216:219],a[140:143],v[176:179]\n"
"s_add_u32 s72,s16,s58\n"
TR "a[208:209],v1 offset:32768\n"
MF "v[176:179],a[220:223],a[180:183],v[176:179]\n"
"s_add_u32 s73,s18,s58\n"
TR "a[210:211],v1 offset:33280\n"
MF "v[184:187],a[220:223],a[184:187],v[184:187]\n"
"s_add_u32 s74,s70,s58\n"
"ds_read_b128 a[68:71],v2 offset:45056\n"
MF "v[184:187],a[216:219],a[144:147],v[184:187]\n"
"s_add_u32 s74,s74,12288\n"
TR "a[212:213],v1 offset:36864\n"
MF "v[192:195],a[216:219],a[148:151],v[192:195]\n"
"s_cmp_eq_u32 s74,s20\n"
"s_cselect_b32 s74,s70,s74\n"
TR "a[214:215],v1 offset:37376\n"
MF "v[192:195],a[220:223],a[188:191],v[192:195]\n"
"s_lshr_b32 s58,s56,1\n"
MF "v[200:203],a[220:223],a[192:195],v[200:203]\n"
"s_mul_i32 s58,s58,6144\n"
"ds_read_b128 a[108:111],v3 offset:45056\n"
MF "v[200:203],a[216:219],a[152:155],v[200:203]\n"
LD "v13,s[28:31],s82 offen offset:3072 lds\n"
MF "v[208:211],a[216:219],a[156:159],v[208:211]\n"
"s_mul_i32 s64,s56,3072\n"
MF "v[208:211],a[220:223],a[196:199],v[208:211]\n"
"s_waitcnt lgkmcnt(7)\n"
MF "v[212:215],a[204:207],a[196:199],v[212:215]\n"
"s_add_u32 s64,s64,s58\n"
TR "a[216:217],v1 offset:32776\n"
MF "v[212:215],a[200:203],a[156:159],v[212:215]\n"
"s_mul_i32 s65,s56,6144\n"
TR "a[218:219],v1 offset:33288\n"
MF "v[204:207],a[200:203],a[152:155],v[204:207]\n"
"s_sub_u32 s58,s71,s70\n"
"ds_read_b128 a[72:75],v2 offset:47104\n"
MF "v[204:207],a[204:207],a[192:195],v[204:207]\n"
"s_add_u32 s96,s74,s58\n"
TR "a[220:221],v1 offset:36872\n"
MF "v[196:199],a[204:207],a[188:191],v[196:199]\n"
"s_sub_u32 s58,s16,s70\n"
TR "a[222:223],v1 offset:37384\n"
MF "v[196:199],a[200:203],a[148:151],v[196:199]\n"
"s_add_u32 s96,s96,s58\n"
MF "v[188:191],a[200:203],a[144:147],v[188:191]\n"
"s_max_u32 s60,s70,s71\n"
"v_add_u32 v0,0xffff4000,v0\n"
MF "v[188:191],a[204:207],a[184:187],v[188:191]\n"
"s_add_u32 s34,s60,196608\n"
"v_add_u32 v1,0xffff4000,v1\n"
MF "v[180:183],a[204:207],a[180:183],v[180:183]\n"
"s_add_u32 s38,s60,196608\n"
MF "v[180:183],a[200:203],a[140:143],v[180:183]\n"
"s_waitcnt vmcnt(13) lgkmcnt(0)\n"
"s_barrier\n"
MF "v[216:219],a[208:211],a[140:143],v[216:219]\n"
TR "a[200:201],v1 offset:8\n"
MF "v[216:219],a[212:215],a[180:183],v[216:219]\n"
TR "a[202:203],v1 offset:520\n"
"v_mov_b32 v14,s12\n"
MF "v[224:227],a[212:215],a[184:187],v[224:227]\n"
"ds_read_b128 a[112:115],v3 offset:47104\n"
"v_mul_f32 v14,0xbdd53b95,v14\n"
MF "v[224:227],a[208:211],a[144:147],v[224:227]\n"
"s_add_u32 m0,s62,81920\n"
TR "a[204:205],v1 offset:4104\n"
MF "v[232:235],a[208:211],a[148:151],v[232:235]\n"
"s_add_u32 s82,s66,7077888\n"
TR "a[206:207],v1 offset:4616\n"
MF "v[232:235],a[212:215],a[188:191],v[232:235]\n"
LD "v6,s[28:31],s82 offen lds\n"
MF "v[240:243],a[212:215],a[192:195],v[240:243]\n"
"ds_read_b128 a[76:79],v2 offset:49152\n"
MF "v[240:243],a[208:211],a[152:155],v[240:243]\n"
"ds_read_b128 a[116:119],v3 offset:49152\n"
MF "v[248:251],a[208:211],a[156:159],v[248:251]\n"
MF "v[248:251],a[212:215],a[196:199],v[248:251]\n"
LD "v11,s[28:31],s82 offen offset:1024 lds\n"
MF "v[220:223],a[216:219],a[140:143],v[220:223]\n"
TR "a[208:209],v1 offset:16392\n"
MF "v[220:223],a[220:223],a[180:183],v[220:223]\n"
TR "a[210:211],v1 offset:16904\n"
MF "v[228:231],a[220:223],a[184:187],v[228:231]\n"
"ds_read_b128 a[140:143],v2\n"
MF "v[228:231],a[216:219],a[144:147],v[228:231]\n"
TR "a[212:213],v1 offset:20488\n"
MF "v[236:239],a[216:219],a[148:151],v[236:239]\n"
TR "a[214:215],v1 offset:21000\n"
MF "v[236:239],a[220:223],a[188:191],v[236:239]\n"
"ds_read_b128 a[180:183],v3\n"
MF "v[244:247],a[220:223],a[192:195],v[244:247]\n"
"ds_read_b128 a[144:147],v2 offset:2048\n"
MF "v[244:247],a[216:219],a[152:155],v[244:247]\n"
"ds_read_b128 a[184:187],v3 offset:2048\n"
MF "v[252:255],a[216:219],a[156:159],v[252:255]\n"
"ds_read_b128 a[148:151],v2 offset:4096\n"
MF "v[252:255],a[220:223],a[196:199],v[252:255]\n"
"s_waitcnt lgkmcnt(9)\n"
"ds_read_b128 a[188:191],v3 offset:4096\n"
MF "v[220:223],a[200:203],a[60:63],v[220:223]\n"
TR "a[216:217],v1\n"
MF "v[220:223],a[204:207],a[100:103],v[220:223]\n"
TR "a[218:219],v1 offset:512\n"
MF "v[228:231],a[204:207],a[104:107],v[228:231]\n"
"ds_read_b128 a[152:155],v2 offset:6144\n"
MF "v[228:231],a[200:203],a[64:67],v[228:231]\n"
TR "a[220:221],v1 offset:4096\n"
MF "v[236:239],a[200:203],a[68:71],v[236:239]\n"
TR "a[222:223],v1 offset:4608\n"
MF "v[236:239],a[204:207],a[108:111],v[236:239]\n"
"ds_read_b128 a[192:195],v3 offset:6144\n"
MF "v[244:247],a[204:207],a[112:115],v[244:247]\n"
"ds_read_b128 a[156:159],v2 offset:8192\n"
MF "v[244:247],a[200:203],a[72:75],v[244:247]\n"
"ds_read_b128 a[196:199],v3 offset:8192\n"
MF "v[252:255],a[200:203],a[76:79],v[252:255]\n"
LD "v12,s[28:31],s82 offen offset:2048 lds\n"
MF "v[252:255],a[204:207],a[116:119],v[252:255]\n"
"s_waitcnt lgkmcnt(8)\n"
LD "v13,s[28:31],s82 offen offset:3072 lds\n"
MF "v[220:223],a[208:211],a[140:143],v[220:223]\n"
TR "a[200:201],v1 offset:16384\n"
MF "v[220:223],a[212:215],a[180:183],v[220:223]\n"
TR "a[202:203],v1 offset:16896\n"
MF "v[228:231],a[212:215],a[184:187],v[228:231]\n"
MF "v[228:231],a[208:211],a[144:147],v[228:231]\n"
TR "a[204:205],v1 offset:20480\n"
MF "v[236:239],a[208:211],a[148:151],v[236:239]\n"
TR "a[206:207],v1 offset:20992\n"
MF "v[236:239],a[212:215],a[188:191],v[236:239]\n"
"s_waitcnt lgkmcnt(4)\n"
MF "v[244:247],a[212:215],a[192:195],v[244:247]\n"
MF "v[244:247],a[208:211],a[152:155],v[244:247]\n"
MF "v[252:255],a[208:211],a[156:159],v[252:255]\n"
MF "v[252:255],a[212:215],a[196:199],v[252:255]\n"
"s_waitcnt lgkmcnt(0)\n"
"s_cmp_eq_u32 s69,98\n"
"s_cbranch_scc1 .Lqy\n"
MF "v[216:219],a[216:219],a[60:63],v[216:219]\n"
TR "a[208:209],v0 offset:8\n"
MF "v[216:219],a[220:223],a[100:103],v[216:219]\n"
TR "a[210:211],v0 offset:520\n"
MF "v[224:227],a[220:223],a[104:107],v[224:227]\n"
"buffer_load_dword a224,v15,s[36:39],s70 offen offset:2688 nt\n"
MF "v[224:227],a[216:219],a[64:67],v[224:227]\n"
TR "a[212:213],v0 offset:4104\n"
MF "v[232:235],a[216:219],a[68:71],v[232:235]\n"
TR "a[214:215],v0 offset:4616\n"
MF "v[232:235],a[220:223],a[108:111],v[232:235]\n"
"buffer_load_dword a225,v15,s[36:39],s71 offen offset:2688 nt\n"
MF "v[240:243],a[220:223],a[112:115],v[240:243]\n"
MF "v[240:243],a[216:219],a[72:75],v[240:243]\n"
"buffer_load_dword a226,v15,s[48:51],s7 offen offset:2688\n"
MF "v[248:251],a[216:219],a[76:79],v[248:251]\n"
MF "v[248:251],a[220:223],a[116:119],v[248:251]\n"
MF "v[248:251],a[200:203],a[156:159],v[248:251]\n"
TR "a[216:217],v0 offset:16392\n"
MF "v[248:251],a[204:207],a[196:199],v[248:251]\n"
TR "a[218:219],v0 offset:16904\n"
MF "v[240:243],a[204:207],a[192:195],v[240:243]\n"
MF "v[240:243],a[200:203],a[152:155],v[240:243]\n"
TR "a[220:221],v0 offset:20488\n"
MF "v[232:235],a[200:203],a[148:151],v[232:235]\n"
TR "a[222:223],v0 offset:21000\n"
MF "v[232:235],a[204:207],a[188:191],v[232:235]\n"
MF "v[224:227],a[204:207],a[184:187],v[224:227]\n"
MF "v[224:227],a[200:203],a[144:147],v[224:227]\n"
"v_fma_f32 v248,v248,s54,v14\n"
"v_fma_f32 v249,v249,s54,v14\n"
MF "v[216:219],a[200:203],a[140:143],v[216:219]\n"
"v_fma_f32 v250,v250,s54,v14\n"
"v_fma_f32 v251,v251,s54,v14\n"
MF "v[216:219],a[204:207],a[180:183],v[216:219]\n"
"s_waitcnt lgkmcnt(4)\n"
"v_fma_f32 v252,v252,s54,v14\n"
"v_fma_f32 v253,v253,s54,v14\n"
MF "v[180:183],a[208:211],a[60:63],v[180:183]\n"
TR "a[200:201],v0\n"
"v_fma_f32 v254,v254,s54,v14\n"
MF "v[180:183],a[212:215],a[100:103],v[180:183]\n"
TR "a[202:203],v0 offset:512\n"
"v_fma_f32 v255,v255,s54,v14\n"
MF "v[188:191],a[212:215],a[104:107],v[188:191]\n"
CV "v255,v254,v255\n"
CV "v254,v252,v253\n"
MF "v[188:191],a[208:211],a[64:67],v[188:191]\n"
TR "a[204:205],v0 offset:4096\n"
CV "v253,v250,v251\n"
MF "v[196:199],a[208:211],a[68:71],v[196:199]\n"
TR "a[206:207],v0 offset:4608\n"
CV "v252,v248,v249\n"
MF "v[196:199],a[212:215],a[108:111],v[196:199]\n"
"v_fma_f32 v240,v240,s54,v14\n"
"v_fma_f32 v241,v241,s54,v14\n"
MF "v[204:207],a[212:215],a[112:115],v[204:207]\n"
"v_fma_f32 v242,v242,s54,v14\n"
"v_fma_f32 v243,v243,s54,v14\n"
MF "v[204:207],a[208:211],a[72:75],v[204:207]\n"
"v_fma_f32 v244,v244,s54,v14\n"
"v_fma_f32 v245,v245,s54,v14\n"
MF "v[212:215],a[208:211],a[76:79],v[212:215]\n"
"v_fma_f32 v246,v246,s54,v14\n"
"v_fma_f32 v247,v247,s54,v14\n"
MF "v[212:215],a[212:215],a[116:119],v[212:215]\n"
"s_waitcnt lgkmcnt(4)\n"
CV "v251,v246,v247\n"
CV "v250,v244,v245\n"
MF "v[180:183],a[216:219],a[140:143],v[180:183]\n"
TR "a[208:209],v0 offset:16384\n"
CV "v249,v242,v243\n"
MF "v[180:183],a[220:223],a[180:183],v[180:183]\n"
TR "a[210:211],v0 offset:16896\n"
CV "v248,v240,v241\n"
MF "v[188:191],a[220:223],a[184:187],v[188:191]\n"
"v_fma_f32 v232,v232,s54,v14\n"
"v_fma_f32 v233,v233,s54,v14\n"
MF "v[188:191],a[216:219],a[144:147],v[188:191]\n"
TR "a[212:213],v0 offset:20480\n"
"v_fma_f32 v234,v234,s54,v14\n"
MF "v[196:199],a[216:219],a[148:151],v[196:199]\n"
TR "a[214:215],v0 offset:20992\n"
"v_fma_f32 v235,v235,s54,v14\n"
MF "v[196:199],a[220:223],a[188:191],v[196:199]\n"
"v_fma_f32 v236,v236,s54,v14\n"
"v_fma_f32 v237,v237,s54,v14\n"
MF "v[204:207],a[220:223],a[192:195],v[204:207]\n"
"v_fma_f32 v238,v238,s54,v14\n"
"v_fma_f32 v239,v239,s54,v14\n"
MF "v[204:207],a[216:219],a[152:155],v[204:207]\n"
CV "v247,v238,v239\n"
CV "v246,v236,v237\n"
MF "v[212:215],a[216:219],a[156:159],v[212:215]\n"
CV "v245,v234,v235\n"
CV "v244,v232,v233\n"
MF "v[212:215],a[220:223],a[196:199],v[212:215]\n"
"s_waitcnt vmcnt(11) lgkmcnt(0)\n"
"s_barrier\n"
MF "v[176:179],a[200:203],a[60:63],v[176:179]\n"
TR "a[216:217],v1 offset:32776\n"
"v_fma_f32 v224,v224,s54,v14\n"
MF "v[176:179],a[204:207],a[100:103],v[176:179]\n"
"s_add_u32 m0,s64,0\n"
TR "a[218:219],v1 offset:33288\n"
"v_fma_f32 v225,v225,s54,v14\n"
MF "v[184:187],a[204:207],a[104:107],v[184:187]\n"
LD "v7,s[32:35],s72 offen nt lds\n"
MF "v[184:187],a[200:203],a[64:67],v[184:187]\n"
TR "a[220:221],v1 offset:36872\n"
"v_fma_f32 v226,v226,s54,v14\n"
MF "v[192:195],a[200:203],a[68:71],v[192:195]\n"
TR "a[222:223],v1 offset:37384\n"
"v_fma_f32 v227,v227,s54,v14\n"
MF "v[192:195],a[204:207],a[108:111],v[192:195]\n"
"s_add_u32 m0,s64,896\n"
"v_fma_f32 v228,v228,s54,v14\n"
"v_fma_f32 v229,v229,s54,v14\n"
MF "v[200:203],a[204:207],a[112:115],v[200:203]\n"
LD "v7,s[32:35],s72 offen offset:128 nt lds\n"
MF "v[200:203],a[200:203],a[72:75],v[200:203]\n"
"v_fma_f32 v230,v230,s54,v14\n"
"v_fma_f32 v231,v231,s54,v14\n"
MF "v[208:211],a[200:203],a[76:79],v[208:211]\n"
CV "v243,v230,v231\n"
CV "v242,v228,v229\n"
MF "v[208:211],a[204:207],a[116:119],v[208:211]\n"
CV "v241,v226,v227\n"
CV "v240,v224,v225\n"
MF "v[208:211],a[208:211],a[156:159],v[208:211]\n"
TR "a[200:201],v1 offset:49160\n"
"v_fma_f32 v216,v216,s54,v14\n"
MF "v[208:211],a[212:215],a[196:199],v[208:211]\n"
"s_add_u32 m0,s64,1792\n"
TR "a[202:203],v1 offset:49672\n"
"v_fma_f32 v217,v217,s54,v14\n"
MF "v[200:203],a[212:215],a[192:195],v[200:203]\n"
LD "v7,s[32:35],s72 offen offset:256 nt lds\n"
MF "v[200:203],a[208:211],a[152:155],v[200:203]\n"
TR "a[204:205],v1 offset:53256\n"
"v_fma_f32 v218,v218,s54,v14\n"
MF "v[192:195],a[208:211],a[148:151],v[192:195]\n"
TR "a[206:207],v1 offset:53768\n"
"v_fma_f32 v219,v219,s54,v14\n"
MF "v[192:195],a[212:215],a[188:191],v[192:195]\n"
"v_fma_f32 v220,v220,s54,v14\n"
"v_fma_f32 v221,v221,s54,v14\n"
MF "v[184:187],a[212:215],a[184:187],v[184:187]\n"
"s_add_u32 s72,s72,6144\n"
"v_fma_f32 v222,v222,s54,v14\n"
"v_fma_f32 v223,v223,s54,v14\n"
MF "v[184:187],a[208:211],a[144:147],v[184:187]\n"
"s_add_u32 m0,s64,6144\n"
CV "v239,v222,v223\n"
CV "v238,v220,v221\n"
MF "v[176:179],a[208:211],a[140:143],v[176:179]\n"
LD "v7,s[32:35],s72 offen nt lds\n"
MF "v[176:179],a[212:215],a[180:183],v[176:179]\n"
"s_waitcnt lgkmcnt(4)\n"
CV "v237,v218,v219\n"
CV "v236,v216,v217\n"
MF "v[140:143],a[216:219],a[60:63],v[140:143]\n"
TR "a[208:209],v1 offset:32768\n"
"v_fma_f32 v208,v208,s54,v14\n"
MF "v[140:143],a[220:223],a[100:103],v[140:143]\n"
TR "a[210:211],v1 offset:33280\n"
"v_fma_f32 v209,v209,s54,v14\n"
MF "v[148:151],a[220:223],a[104:107],v[148:151]\n"
"v_fma_f32 v210,v210,s54,v14\n"
"v_fma_f32 v211,v211,s54,v14\n"
MF "v[148:151],a[216:219],a[64:67],v[148:151]\n"
TR "a[212:213],v1 offset:36864\n"
"v_fma_f32 v212,v212,s54,v14\n"
MF "v[156:159],a[216:219],a[68:71],v[156:159]\n"
"s_add_u32 m0,s64,7040\n"
TR "a[214:215],v1 offset:37376\n"
"v_fma_f32 v213,v213,s54,v14\n"
MF "v[156:159],a[220:223],a[108:111],v[156:159]\n"
LD "v7,s[32:35],s72 offen offset:128 nt lds\n"
MF "v[164:167],a[220:223],a[112:115],v[164:167]\n"
"v_fma_f32 v214,v214,s54,v14\n"
"v_fma_f32 v215,v215,s54,v14\n"
MF "v[164:167],a[216:219],a[72:75],v[164:167]\n"
CV "v235,v214,v215\n"
CV "v234,v212,v213\n"
MF "v[172:175],a[216:219],a[76:79],v[172:175]\n"
"s_add_u32 m0,s64,7936\n"
CV "v233,v210,v211\n"
CV "v232,v208,v209\n"
MF "v[172:175],a[220:223],a[116:119],v[172:175]\n"
"s_waitcnt lgkmcnt(4)\n"
LD "v7,s[32:35],s72 offen offset:256 nt lds\n"
MF "v[140:143],a[200:203],a[140:143],v[140:143]\n"
TR "a[216:217],v1 offset:49152\n"
"v_fma_f32 v200,v200,s54,v14\n"
MF "v[140:143],a[204:207],a[180:183],v[140:143]\n"
TR "a[218:219],v1 offset:49664\n"
"v_fma_f32 v201,v201,s54,v14\n"
MF "v[148:151],a[204:207],a[184:187],v[148:151]\n"
"v_fma_f32 v202,v202,s54,v14\n"
"v_fma_f32 v203,v203,s54,v14\n"
MF "v[148:151],a[200:203],a[144:147],v[148:151]\n"
TR "a[220:221],v1 offset:53248\n"
"v_fma_f32 v204,v204,s54,v14\n"
MF "v[156:159],a[200:203],a[148:151],v[156:159]\n"
TR "a[222:223],v1 offset:53760\n"
"v_fma_f32 v205,v205,s54,v14\n"
MF "v[156:159],a[204:207],a[188:191],v[156:159]\n"
"v_fma_f32 v206,v206,s54,v14\n"
"v_fma_f32 v207,v207,s54,v14\n"
MF "v[164:167],a[204:207],a[192:195],v[164:167]\n"
CV "v231,v206,v207\n"
CV "v230,v204,v205\n"
MF "v[164:167],a[200:203],a[152:155],v[164:167]\n"
CV "v229,v202,v203\n"
CV "v228,v200,v201\n"
MF "v[172:175],a[200:203],a[156:159],v[172:175]\n"
"v_fma_f32 v192,v192,s54,v14\n"
"v_fma_f32 v193,v193,s54,v14\n"
MF "v[172:175],a[204:207],a[196:199],v[172:175]\n"
"s_waitcnt lgkmcnt(0)\n"
"v_fma_f32 v194,v194,s54,v14\n"
"v_fma_f32 v195,v195,s54,v14\n"
MF "v[136:139],a[208:211],a[60:63],v[136:139]\n"
TR "a[200:201],v0 offset:32776\n"
"v_fma_f32 v196,v196,s54,v14\n"
MF "v[136:139],a[212:215],a[100:103],v[136:139]\n"
TR "a[202:203],v0 offset:33288\n"
"v_fma_f32 v197,v197,s54,v14\n"
MF "v[144:147],a[212:215],a[104:107],v[144:147]\n"
"v_fma_f32 v198,v198,s54,v14\n"
"v_fma_f32 v199,v199,s54,v14\n"
MF "v[144:147],a[208:211],a[64:67],v[144:147]\n"
TR "a[204:205],v0 offset:36872\n"
CV "v227,v198,v199\n"
MF "v[152:155],a[208:211],a[68:71],v[152:155]\n"
TR "a[206:207],v0 offset:37384\n"
CV "v226,v196,v197\n"
MF "v[152:155],a[212:215],a[108:111],v[152:155]\n"
CV "v225,v194,v195\n"
CV "v224,v192,v193\n"
MF "v[160:163],a[212:215],a[112:115],v[160:163]\n"
"v_fma_f32 v184,v184,s54,v14\n"
"v_fma_f32 v185,v185,s54,v14\n"
MF "v[160:163],a[208:211],a[72:75],v[160:163]\n"
"v_fma_f32 v186,v186,s54,v14\n"
"v_fma_f32 v187,v187,s54,v14\n"
MF "v[168:171],a[208:211],a[76:79],v[168:171]\n"
"v_fma_f32 v188,v188,s54,v14\n"
"v_fma_f32 v189,v189,s54,v14\n"
MF "v[168:171],a[212:215],a[116:119],v[168:171]\n"
"v_fma_f32 v190,v190,s54,v14\n"
"v_fma_f32 v191,v191,s54,v14\n"
MF "v[168:171],a[216:219],a[156:159],v[168:171]\n"
TR "a[208:209],v0 offset:49160\n"
CV "v223,v190,v191\n"
MF "v[168:171],a[220:223],a[196:199],v[168:171]\n"
TR "a[210:211],v0 offset:49672\n"
CV "v222,v188,v189\n"
MF "v[160:163],a[220:223],a[192:195],v[160:163]\n"
CV "v221,v186,v187\n"
CV "v220,v184,v185\n"
MF "v[160:163],a[216:219],a[152:155],v[160:163]\n"
TR "a[212:213],v0 offset:53256\n"
"v_fma_f32 v176,v176,s54,v14\n"
MF "v[152:155],a[216:219],a[148:151],v[152:155]\n"
TR "a[214:215],v0 offset:53768\n"
"v_fma_f32 v177,v177,s54,v14\n"
MF "v[152:155],a[220:223],a[188:191],v[152:155]\n"
"v_fma_f32 v178,v178,s54,v14\n"
"v_fma_f32 v179,v179,s54,v14\n"
MF "v[144:147],a[220:223],a[184:187],v[144:147]\n"
"v_fma_f32 v180,v180,s54,v14\n"
"v_fma_f32 v181,v181,s54,v14\n"
MF "v[144:147],a[216:219],a[144:147],v[144:147]\n"
"v_fma_f32 v182,v182,s54,v14\n"
"v_fma_f32 v183,v183,s54,v14\n"
MF "v[136:139],a[216:219],a[140:143],v[136:139]\n"
CV "v219,v182,v183\n"
CV "v218,v180,v181\n"
MF "v[136:139],a[220:223],a[180:183],v[136:139]\n"
"s_waitcnt lgkmcnt(4)\n"
CV "v217,v178,v179\n"
CV "v216,v176,v177\n"
MF "v[100:103],a[200:203],a[60:63],v[100:103]\n"
TR "a[216:217],v0 offset:32768\n"
"v_fma_f32 v168,v168,s54,v14\n"
MF "v[100:103],a[204:207],a[100:103],v[100:103]\n"
TR "a[218:219],v0 offset:33280\n"
"v_fma_f32 v169,v169,s54,v14\n"
MF "v[108:111],a[204:207],a[104:107],v[108:111]\n"
"v_fma_f32 v170,v170,s54,v14\n"
"v_fma_f32 v171,v171,s54,v14\n"
MF "v[108:111],a[200:203],a[64:67],v[108:111]\n"
TR "a[220:221],v0 offset:36864\n"
"v_fma_f32 v172,v172,s54,v14\n"
MF "v[116:119],a[200:203],a[68:71],v[116:119]\n"
TR "a[222:223],v0 offset:37376\n"
"v_fma_f32 v173,v173,s54,v14\n"
MF "v[116:119],a[204:207],a[108:111],v[116:119]\n"
"v_fma_f32 v174,v174,s54,v14\n"
"v_fma_f32 v175,v175,s54,v14\n"
MF "v[124:127],a[204:207],a[112:115],v[124:127]\n"
CV "v215,v174,v175\n"
CV "v214,v172,v173\n"
MF "v[124:127],a[200:203],a[72:75],v[124:127]\n"
CV "v213,v170,v171\n"
CV "v212,v168,v169\n"
MF "v[132:135],a[200:203],a[76:79],v[132:135]\n"
"v_fma_f32 v160,v160,s54,v14\n"
"v_fma_f32 v161,v161,s54,v14\n"
MF "v[132:135],a[204:207],a[116:119],v[132:135]\n"
"s_waitcnt lgkmcnt(4)\n"
"v_fma_f32 v162,v162,s54,v14\n"
"v_fma_f32 v163,v163,s54,v14\n"
MF "v[100:103],a[208:211],a[140:143],v[100:103]\n"
TR "a[200:201],v0 offset:49152\n"
"v_fma_f32 v164,v164,s54,v14\n"
MF "v[100:103],a[212:215],a[180:183],v[100:103]\n"
TR "a[202:203],v0 offset:49664\n"
"v_fma_f32 v165,v165,s54,v14\n"
MF "v[108:111],a[212:215],a[184:187],v[108:111]\n"
"v_fma_f32 v166,v166,s54,v14\n"
"v_fma_f32 v167,v167,s54,v14\n"
MF "v[108:111],a[208:211],a[144:147],v[108:111]\n"
TR "a[204:205],v0 offset:53248\n"
CV "v211,v166,v167\n"
MF "v[116:119],a[208:211],a[148:151],v[116:119]\n"
TR "a[206:207],v0 offset:53760\n"
CV "v210,v164,v165\n"
MF "v[116:119],a[212:215],a[188:191],v[116:119]\n"
CV "v209,v162,v163\n"
CV "v208,v160,v161\n"
MF "v[124:127],a[212:215],a[192:195],v[124:127]\n"
"v_add_u32 v0,0xc000,v0\n"
"v_fma_f32 v152,v152,s54,v14\n"
MF "v[124:127],a[208:211],a[152:155],v[124:127]\n"
"v_add_u32 v1,0xc000,v1\n"
"v_fma_f32 v153,v153,s54,v14\n"
MF "v[132:135],a[208:211],a[156:159],v[132:135]\n"
"v_fma_f32 v154,v154,s54,v14\n"
"v_fma_f32 v155,v155,s54,v14\n"
MF "v[132:135],a[212:215],a[196:199],v[132:135]\n"
"s_waitcnt vmcnt(9) lgkmcnt(0)\n"
"s_barrier\n"
MF "v[96:99],a[216:219],a[60:63],v[96:99]\n"
TR "a[208:209],v1 offset:16392\n"
"v_fma_f32 v156,v156,s54,v14\n"
MF "v[96:99],a[220:223],a[100:103],v[96:99]\n"
TR "a[210:211],v1 offset:16904\n"
"v_fma_f32 v157,v157,s54,v14\n"
MF "v[104:107],a[220:223],a[104:107],v[104:107]\n"
"s_add_u32 s72,s72,6144\n"
"v_fma_f32 v158,v158,s54,v14\n"
"v_fma_f32 v159,v159,s54,v14\n"
MF "v[104:107],a[216:219],a[64:67],v[104:107]\n"
"s_cmp_eq_u32 s72,s17\n"
"s_cselect_b32 s72,s16,s72\n"
TR "a[212:213],v1 offset:20488\n"
MF "v[112:115],a[216:219],a[68:71],v[112:115]\n"
"s_add_u32 m0,s64,24576\n"
TR "a[214:215],v1 offset:21000\n"
CV "v207,v158,v159\n"
MF "v[112:115],a[220:223],a[108:111],v[112:115]\n"
LD "v7,s[32:35],s72 offen nt lds\n"
MF "v[120:123],a[220:223],a[112:115],v[120:123]\n"
"s_add_u32 m0,s64,25472\n"
CV "v206,v156,v157\n"
CV "v205,v154,v155\n"
MF "v[120:123],a[216:219],a[72:75],v[120:123]\n"
LD "v7,s[32:35],s72 offen offset:128 nt lds\n"
MF "v[128:131],a[216:219],a[76:79],v[128:131]\n"
CV "v204,v152,v153\n"
"v_fma_f32 v144,v144,s54,v14\n"
MF "v[128:131],a[220:223],a[116:119],v[128:131]\n"
"v_fma_f32 v145,v145,s54,v14\n"
"v_fma_f32 v146,v146,s54,v14\n"
MF "v[128:131],a[200:203],a[156:159],v[128:131]\n"
TR "a[216:217],v1 offset:32776\n"
"v_fma_f32 v147,v147,s54,v14\n"
MF "v[128:131],a[204:207],a[196:199],v[128:131]\n"
"s_add_u32 m0,s64,26368\n"
TR "a[218:219],v1 offset:33288\n"
"v_fma_f32 v148,v148,s54,v14\n"
MF "v[120:123],a[204:207],a[192:195],v[120:123]\n"
LD "v7,s[32:35],s72 offen offset:256 nt lds\n"
MF "v[120:123],a[200:203],a[152:155],v[120:123]\n"
TR "a[220:221],v1 offset:36872\n"
"v_fma_f32 v149,v149,s54,v14\n"
MF "v[112:115],a[200:203],a[148:151],v[112:115]\n"
TR "a[222:223],v1 offset:37384\n"
"v_fma_f32 v150,v150,s54,v14\n"
MF "v[112:115],a[204:207],a[188:191],v[112:115]\n"
"v_fma_f32 v151,v151,s54,v14\n"
CV "v203,v150,v151\n"
MF "v[104:107],a[204:207],a[184:187],v[104:107]\n"
"s_add_u32 s72,s72,6144\n"
CV "v202,v148,v149\n"
CV "v201,v146,v147\n"
MF "v[104:107],a[200:203],a[144:147],v[104:107]\n"
"s_add_u32 m0,s64,30720\n"
CV "v200,v144,v145\n"
"v_fma_f32 v136,v136,s54,v14\n"
MF "v[96:99],a[200:203],a[140:143],v[96:99]\n"
LD "v7,s[32:35],s72 offen nt lds\n"
MF "v[96:99],a[204:207],a[180:183],v[96:99]\n"
"s_waitcnt lgkmcnt(4)\n"
"v_fma_f32 v137,v137,s54,v14\n"
"v_fma_f32 v138,v138,s54,v14\n"
MF "v[60:63],a[208:211],a[60:63],v[60:63]\n"
TR "a[200:201],v1 offset:16384\n"
"v_fma_f32 v139,v139,s54,v14\n"
MF "v[60:63],a[212:215],a[100:103],v[60:63]\n"
TR "a[202:203],v1 offset:16896\n"
"v_fma_f32 v140,v140,s54,v14\n"
MF "v[68:71],a[212:215],a[104:107],v[68:71]\n"
"v_fma_f32 v141,v141,s54,v14\n"
"v_fma_f32 v142,v142,s54,v14\n"
MF "v[68:71],a[208:211],a[64:67],v[68:71]\n"
TR "a[204:205],v1 offset:20480\n"
"v_fma_f32 v143,v143,s54,v14\n"
MF "v[76:79],a[208:211],a[68:71],v[76:79]\n"
"s_add_u32 m0,s64,31616\n"
TR "a[206:207],v1 offset:20992\n"
CV "v199,v142,v143\n"
MF "v[76:79],a[212:215],a[108:111],v[76:79]\n"
LD "v7,s[32:35],s72 offen offset:128 nt lds\n"
MF "v[84:87],a[212:215],a[112:115],v[84:87]\n"
CV "v198,v140,v141\n"
CV "v197,v138,v139\n"
MF "v[84:87],a[208:211],a[72:75],v[84:87]\n"
CV "v196,v136,v137\n"
"v_fma_f32 v128,v128,s54,v14\n"
MF "v[92:95],a[208:211],a[76:79],v[92:95]\n"
"s_add_u32 m0,s64,32512\n"
"v_fma_f32 v129,v129,s54,v14\n"
"v_fma_f32 v130,v130,s54,v14\n"
MF "v[92:95],a[212:215],a[116:119],v[92:95]\n"
"s_waitcnt lgkmcnt(4)\n"
LD "v7,s[32:35],s72 offen offset:256 nt lds\n"
MF "v[60:63],a[216:219],a[140:143],v[60:63]\n"
TR "a[208:209],v1 offset:32768\n"
"v_fma_f32 v131,v131,s54,v14\n"
MF "v[60:63],a[220:223],a[180:183],v[60:63]\n"
TR "a[210:211],v1 offset:33280\n"
"v_fma_f32 v132,v132,s54,v14\n"
MF "v[68:71],a[220:223],a[184:187],v[68:71]\n"
"v_fma_f32 v133,v133,s54,v14\n"
"v_fma_f32 v134,v134,s54,v14\n"
MF "v[68:71],a[216:219],a[144:147],v[68:71]\n"
TR "a[212:213],v1 offset:36864\n"
"v_fma_f32 v135,v135,s54,v14\n"
MF "v[76:79],a[216:219],a[148:151],v[76:79]\n"
TR "a[214:215],v1 offset:37376\n"
CV "v195,v134,v135\n"
MF "v[76:79],a[220:223],a[188:191],v[76:79]\n"
CV "v194,v132,v133\n"
CV "v193,v130,v131\n"
MF "v[84:87],a[220:223],a[192:195],v[84:87]\n"
CV "v192,v128,v129\n"
"v_fma_f32 v120,v120,s54,v14\n"
MF "v[84:87],a[216:219],a[152:155],v[84:87]\n"
"v_fma_f32 v121,v121,s54,v14\n"
"v_fma_f32 v122,v122,s54,v14\n"
MF "v[92:95],a[216:219],a[156:159],v[92:95]\n"
"v_fma_f32 v123,v123,s54,v14\n"
"v_fma_f32 v124,v124,s54,v14\n"
MF "v[92:95],a[220:223],a[196:199],v[92:95]\n"
"s_waitcnt lgkmcnt(0)\n"
"v_fma_f32 v125,v125,s54,v14\n"
"v_fma_f32 v126,v126,s54,v14\n"
MF "v[56:59],a[200:203],a[60:63],v[56:59]\n"
TR "a[216:217],v0 offset:16392\n"
"v_fma_f32 v127,v127,s54,v14\n"
MF "v[56:59],a[204:207],a[100:103],v[56:59]\n"
TR "a[218:219],v0 offset:16904\n"
CV "v191,v126,v127\n"
MF "v[64:67],a[204:207],a[104:107],v[64:67]\n"
CV "v190,v124,v125\n"
CV "v189,v122,v123\n"
MF "v[64:67],a[200:203],a[64:67],v[64:67]\n"
TR "a[220:221],v0 offset:20488\n"
CV "v188,v120,v121\n"
MF "v[72:75],a[200:203],a[68:71],v[72:75]\n"
TR "a[222:223],v0 offset:21000\n"
"v_fma_f32 v112,v112,s54,v14\n"
MF "v[72:75],a[204:207],a[108:111],v[72:75]\n"
"v_fma_f32 v113,v113,s54,v14\n"
"v_fma_f32 v114,v114,s54,v14\n"
MF "v[80:83],a[204:207],a[112:115],v[80:83]\n"
"v_fma_f32 v115,v115,s54,v14\n"
"v_fma_f32 v116,v116,s54,v14\n"
MF "v[80:83],a[200:203],a[72:75],v[80:83]\n"
"v_fma_f32 v117,v117,s54,v14\n"
"v_fma_f32 v118,v118,s54,v14\n"
MF "v[88:91],a[200:203],a[76:79],v[88:91]\n"
"v_fma_f32 v119,v119,s54,v14\n"
CV "v187,v118,v119\n"
MF "v[88:91],a[204:207],a[116:119],v[88:91]\n"
CV "v186,v116,v117\n"
CV "v185,v114,v115\n"
MF "v[88:91],a[208:211],a[156:159],v[88:91]\n"
TR "a[200:201],v0 offset:32776\n"
CV "v184,v112,v113\n"
MF "v[88:91],a[212:215],a[196:199],v[88:91]\n"
TR "a[202:203],v0 offset:33288\n"
"v_fma_f32 v104,v104,s54,v14\n"
MF "v[80:83],a[212:215],a[192:195],v[80:83]\n"
"v_fma_f32 v105,v105,s54,v14\n"
"v_fma_f32 v106,v106,s54,v14\n"
MF "v[80:83],a[208:211],a[152:155],v[80:83]\n"
TR "a[204:205],v0 offset:36872\n"
"v_fma_f32 v107,v107,s54,v14\n"
MF "v[72:75],a[208:211],a[148:151],v[72:75]\n"
TR "a[206:207],v0 offset:37384\n"
"v_fma_f32 v108,v108,s54,v14\n"
MF "v[72:75],a[212:215],a[188:191],v[72:75]\n"
"v_fma_f32 v109,v109,s54,v14\n"
"v_fma_f32 v110,v110,s54,v14\n"
MF "v[64:67],a[212:215],a[184:187],v[64:67]\n"
"v_fma_f32 v111,v111,s54,v14\n"
CV "v183,v110,v111\n"
MF "v[64:67],a[208:211],a[144:147],v[64:67]\n"
CV "v182,v108,v109\n"
CV "v181,v106,v107\n"
MF "v[56:59],a[208:211],a[140:143],v[56:59]\n"
CV "v180,v104,v105\n"
"v_fma_f32 v96,v96,s54,v14\n"
MF "v[56:59],a[212:215],a[180:183],v[56:59]\n"
"s_waitcnt lgkmcnt(4)\n"
"v_fma_f32 v97,v97,s54,v14\n"
"v_fma_f32 v98,v98,s54,v14\n"
MF "v[20:23],a[216:219],a[60:63],v[20:23]\n"
TR "a[208:209],v0 offset:16384\n"
"v_fma_f32 v99,v99,s54,v14\n"
MF "v[20:23],a[220:223],a[100:103],v[20:23]\n"
TR "a[210:211],v0 offset:16896\n"
"v_fma_f32 v100,v100,s54,v14\n"
MF "v[28:31],a[220:223],a[104:107],v[28:31]\n"
"v_fma_f32 v101,v101,s54,v14\n"
"v_fma_f32 v102,v102,s54,v14\n"
MF "v[28:31],a[216:219],a[64:67],v[28:31]\n"
TR "a[212:213],v0 offset:20480\n"
"v_fma_f32 v103,v103,s54,v14\n"
MF "v[36:39],a[216:219],a[68:71],v[36:39]\n"
TR "a[214:215],v0 offset:20992\n"
CV "v179,v102,v103\n"
MF "v[36:39],a[220:223],a[108:111],v[36:39]\n"
CV "v178,v100,v101\n"
CV "v177,v98,v99\n"
MF "v[44:47],a[220:223],a[112:115],v[44:47]\n"
CV "v176,v96,v97\n"
"v_fma_f32 v88,v88,s54,v14\n"
MF "v[44:47],a[216:219],a[72:75],v[44:47]\n"
"v_fma_f32 v89,v89,s54,v14\n"
"v_fma_f32 v90,v90,s54,v14\n"
MF "v[52:55],a[216:219],a[76:79],v[52:55]\n"
"v_fma_f32 v91,v91,s54,v14\n"
"v_fma_f32 v92,v92,s54,v14\n"
MF "v[52:55],a[220:223],a[116:119],v[52:55]\n"
"s_waitcnt lgkmcnt(4)\n"
"v_fma_f32 v93,v93,s54,v14\n"
"v_fma_f32 v94,v94,s54,v14\n"
MF "v[20:23],a[200:203],a[140:143],v[20:23]\n"
TR "a[216:217],v0 offset:32768\n"
"v_fma_f32 v95,v95,s54,v14\n"
MF "v[20:23],a[204:207],a[180:183],v[20:23]\n"
TR "a[218:219],v0 offset:33280\n"
CV "v175,v94,v95\n"
MF "v[28:31],a[204:207],a[184:187],v[28:31]\n"
CV "v174,v92,v93\n"
CV "v173,v90,v91\n"
MF "v[28:31],a[200:203],a[144:147],v[28:31]\n"
TR "a[220:221],v0 offset:36864\n"
CV "v172,v88,v89\n"
MF "v[36:39],a[200:203],a[148:151],v[36:39]\n"
TR "a[222:223],v0 offset:37376\n"
"v_fma_f32 v80,v80,s54,v14\n"
MF "v[36:39],a[204:207],a[188:191],v[36:39]\n"
"v_fma_f32 v81,v81,s54,v14\n"
"v_fma_f32 v82,v82,s54,v14\n"
MF "v[44:47],a[204:207],a[192:195],v[44:47]\n"
"v_fma_f32 v83,v83,s54,v14\n"
"v_fma_f32 v84,v84,s54,v14\n"
MF "v[44:47],a[200:203],a[152:155],v[44:47]\n"
"v_fma_f32 v85,v85,s54,v14\n"
"v_fma_f32 v86,v86,s54,v14\n"
MF "v[52:55],a[200:203],a[156:159],v[52:55]\n"
"v_fma_f32 v87,v87,s54,v14\n"
CV "v171,v86,v87\n"
MF "v[52:55],a[204:207],a[196:199],v[52:55]\n"
"s_waitcnt lgkmcnt(0)\n"
"s_barrier\n"
MF "v[16:19],a[208:211],a[60:63],v[16:19]\n"
"s_add_u32 m0,s65,49152\n"
CV "v170,v84,v85\n"
CV "v169,v82,v83\n"
MF "v[16:19],a[212:215],a[100:103],v[16:19]\n"
LD "v8,s[36:39],s73 offen nt lds\n"
MF "v[24:27],a[212:215],a[104:107],v[24:27]\n"
"s_add_u32 m0,s65,50048\n"
CV "v168,v80,v81\n"
"v_fma_f32 v72,v72,s54,v14\n"
MF "v[24:27],a[208:211],a[64:67],v[24:27]\n"
LD "v8,s[36:39],s73 offen offset:128 nt lds\n"
MF "v[32:35],a[208:211],a[68:71],v[32:35]\n"
"v_fma_f32 v73,v73,s54,v14\n"
"v_fma_f32 v74,v74,s54,v14\n"
MF "v[32:35],a[212:215],a[108:111],v[32:35]\n"
"s_add_u32 m0,s65,50944\n"
"v_fma_f32 v75,v75,s54,v14\n"
"v_fma_f32 v76,v76,s54,v14\n"
MF "v[40:43],a[212:215],a[112:115],v[40:43]\n"
LD "v8,s[36:39],s73 offen offset:256 nt lds\n"
MF "v[40:43],a[208:211],a[72:75],v[40:43]\n"
"v_fma_f32 v77,v77,s54,v14\n"
"v_fma_f32 v78,v78,s54,v14\n"
MF "v[48:51],a[208:211],a[76:79],v[48:51]\n"
"s_add_u32 m0,s65,49152\n"
"v_fma_f32 v79,v79,s54,v14\n"
CV "v167,v78,v79\n"
MF "v[48:51],a[212:215],a[116:119],v[48:51]\n"
LD "v8,s[36:39],s73 offen offset:3072 nt lds\n"
MF "v[48:51],a[216:219],a[156:159],v[48:51]\n"
"s_add_u32 m0,s65,50048\n"
CV "v166,v76,v77\n"
CV "v165,v74,v75\n"
MF "v[48:51],a[220:223],a[196:199],v[48:51]\n"
LD "v8,s[36:39],s73 offen offset:3200 nt lds\n"
MF "v[40:43],a[220:223],a[192:195],v[40:43]\n"
CV "v164,v72,v73\n"
"v_fma_f32 v64,v64,s54,v14\n"
MF "v[40:43],a[216:219],a[152:155],v[40:43]\n"
"s_add_u32 m0,s65,50944\n"
"v_fma_f32 v65,v65,s54,v14\n"
"v_fma_f32 v66,v66,s54,v14\n"
MF "v[32:35],a[216:219],a[148:151],v[32:35]\n"
LD "v8,s[36:39],s73 offen offset:3328 nt lds\n"
MF "v[32:35],a[220:223],a[188:191],v[32:35]\n"
"v_fma_f32 v67,v67,s54,v14\n"
"v_fma_f32 v68,v68,s54,v14\n"
MF "v[24:27],a[220:223],a[184:187],v[24:27]\n"
"v_fma_f32 v69,v69,s54,v14\n"
"v_fma_f32 v70,v70,s54,v14\n"
MF "v[24:27],a[216:219],a[144:147],v[24:27]\n"
"v_fma_f32 v71,v71,s54,v14\n"
CV "v163,v70,v71\n"
MF "v[16:19],a[216:219],a[140:143],v[16:19]\n"
CV "v162,v68,v69\n"
CV "v161,v66,v67\n"
MF "v[16:19],a[220:223],a[180:183],v[16:19]\n"
CV "v160,v64,v65\n"
"v_pk_fma_f32 v[56:57],v[56:57],s[54:55],v[14:15] op_sel_hi:[1,1,0]\n"
"v_pk_fma_f32 v[58:59],v[58:59],s[54:55],v[14:15] op_sel_hi:[1,1,0]\n"
"v_pk_fma_f32 v[60:61],v[60:61],s[54:55],v[14:15] op_sel_hi:[1,1,0]\n"
"v_pk_fma_f32 v[62:63],v[62:63],s[54:55],v[14:15] op_sel_hi:[1,1,0]\n"
CV "v159,v62,v63\n"
CV "v158,v60,v61\n"
CV "v157,v58,v59\n"
CV "v156,v56,v57\n"
"v_pk_mul_f32 v[48:49],v[48:49],s[54:55]\n"
"v_pk_mul_f32 v[50:51],v[50:51],s[54:55]\n"
"v_pk_mul_f32 v[52:53],v[52:53],s[54:55]\n"
"v_pk_mul_f32 v[54:55],v[54:55],s[54:55]\n"
CV "v155,v54,v55\n"
CV "v154,v52,v53\n"
CV "v153,v50,v51\n"
CV "v152,v48,v49\n"
"v_pk_mul_f32 v[40:41],v[40:41],s[54:55]\n"
"v_pk_mul_f32 v[42:43],v[42:43],s[54:55]\n"
"v_pk_mul_f32 v[44:45],v[44:45],s[54:55]\n"
"v_pk_mul_f32 v[46:47],v[46:47],s[54:55]\n"
CV "v151,v46,v47\n"
CV "v150,v44,v45\n"
CV "v149,v42,v43\n"
CV "v148,v40,v41\n"
"v_pk_mul_f32 v[32:33],v[32:33],s[54:55]\n"
"v_pk_mul_f32 v[34:35],v[34:35],s[54:55]\n"
"v_pk_mul_f32 v[36:37],v[36:37],s[54:55]\n"
"v_pk_mul_f32 v[38:39],v[38:39],s[54:55]\n"
CV "v147,v38,v39\n"
CV "v146,v36,v37\n"
CV "v145,v34,v35\n"
CV "v144,v32,v33\n"
"v_pk_mul_f32 v[24:25],v[24:25],s[54:55]\n"
"v_pk_mul_f32 v[26:27],v[26:27],s[54:55]\n"
"v_pk_mul_f32 v[28:29],v[28:29],s[54:55]\n"
"v_pk_mul_f32 v[30:31],v[30:31],s[54:55]\n"
CV "v143,v30,v31\n"
CV "v142,v28,v29\n"
CV "v141,v26,v27\n"
CV "v140,v24,v25\n"
"v_pk_mul_f32 v[16:17],v[16:17],s[54:55]\n"
"v_pk_mul_f32 v[18:19],v[18:19],s[54:55]\n"
"v_pk_mul_f32 v[20:21],v[20:21],s[54:55]\n"
"v_pk_mul_f32 v[22:23],v[22:23],s[54:55]\n"
CV "v139,v22,v23\n"
CV "v138,v20,v21\n"
CV "v137,v18,v19\n"
CV "v136,v16,v17\n"
"s_cmp_eq_u32 s57,s69\n"
"s_cbranch_scc1 .LattB\n"
AR "v0,a244\n"
AR "v2,a245\n"
"v_xor_b32_e64 v1,64,v0\n"
"v_add_u32 v2,49152,v2\n"
"v_xor_b32 v3,32,v2\n"
"v_xor_b32 v4,64,v2\n"
"v_xor_b32 v5,96,v2\n"
"s_mov_b32 s61,7\n"
"s_memtime s[52:53]\n"
"s_memrealtime s[100:101]\n"
"s_waitcnt lgkmcnt(0)\n"
"s_sub_u32 s53,s52,s80\n"
"s_cmp_gt_u32 s53,27000\n"
"s_cselect_b32 s101,1,0\n"
"s_cmp_eq_u32 s87,2\n"
"s_cselect_b32 s87,s101,s87\n"
"s_cmp_eq_u32 s87,1\n"
"s_cmp_eq_u32 s57,0\n"
"s_mov_b32 s101,0\n"
"s_cselect_b32 s101,130,s101\n"
"s_cmp_eq_u32 s87,1\n"
"s_cselect_b32 s97,s101,0\n"
"s_cselect_b32 s21,0,0\n"
"s_add_u32 s21,s21,s100\n"
".Latt2x:\n"
"s_cmp_eq_u32 s97,0\n"
"s_cselect_b32 s95,0x47800000,0\n"
"s_mov_b32 s77,s95\n"
"s_waitcnt vmcnt(12) lgkmcnt(0)\n"
"s_barrier\n"
"ds_read_b128 v[16:19],v0\n"
"ds_read_b128 v[20:23],v1\n"
"ds_read_b128 v[24:27],v0 offset:1024\n"
"s_cmp_eq_u32 s57,1\n"
"s_cbranch_scc1 .Lt0kx\n"
"s_waitcnt lgkmcnt(2)\n"
MF "v[72:75],v[16:19],v[136:139],0\n"
"s_add_u32 s73,s73,12288\n"
"ds_read_b128 v[28:31],v1 offset:1024\n"
AR "v10,a224\n"
MF "v[76:79],v[16:19],v[140:143],0\n"
"s_cmp_eq_u32 s73,s19\n"
"s_cselect_b32 s73,s18,s73\n"
"s_add_u32 m0,s65,73728\n"
MF "v[80:83],v[16:19],v[144:147],0\n"
LD "v8,s[36:39],s73 offen nt lds\n"
MF "v[84:87],v[16:19],v[148:151],0\n"
"v_max_f32_dpp v10,|v10|,|v10| quad_perm:[1,0,3,2] row_mask:0xf bank_mask:0xf\n"
AR "v11,a225\n"
MF "v[88:91],v[16:19],v[152:155],0\n"
"s_waitcnt lgkmcnt(2)\n"
"ds_read_b128 v[16:19],v0 offset:2048\n"
AR "v12,a226\n"
MF "v[72:75],v[20:23],v[156:159],v[72:75]\n"
"s_add_u32 m0,s65,74624\n"
"v_max_f32_dpp v10,v10,v10 quad_perm:[2,3,0,1] row_mask:0xf bank_mask:0xf\n"
"v_max_f32_dpp v11,|v11|,|v11| quad_perm:[1,0,3,2] row_mask:0xf bank_mask:0xf\n"
MF "v[76:79],v[20:23],v[160:163],v[76:79]\n"
LD "v8,s[36:39],s73 offen offset:128 nt lds\n"
MF "v[80:83],v[20:23],v[164:167],v[80:83]\n"
"v_max_f32_dpp v10,v10,v10 row_half_mirror row_mask:0xf bank_mask:0xf\n"
"v_max_f32_dpp v11,v11,v11 quad_perm:[2,3,0,1] row_mask:0xf bank_mask:0xf\n"
MF "v[84:87],v[20:23],v[168:171],v[84:87]\n"
"v_readlane_b32 s13,v10,0\n"
"v_readlane_b32 s0,v10,24\n"
MF "v[88:91],v[20:23],v[172:175],v[88:91]\n"
"s_waitcnt lgkmcnt(2)\n"
"ds_read_b128 v[20:23],v1 offset:2048\n"
"v_readlane_b32 s1,v10,32\n"
MF "v[72:75],v[24:27],v[176:179],v[72:75]\n"
"s_add_u32 m0,s65,75520\n"
"v_readlane_b32 s2,v10,56\n"
"v_max_f32_dpp v11,v11,v11 row_half_mirror row_mask:0xf bank_mask:0xf\n"
MF "v[76:79],v[24:27],v[180:183],v[76:79]\n"
LD "v8,s[36:39],s73 offen offset:256 nt lds\n"
MF "v[80:83],v[24:27],v[184:187],v[80:83]\n"
"s_min_u32 s13,s13,s0\n"
"v_readlane_b32 s11,v11,0\n"
"v_readlane_b32 s3,v11,24\n"
MF "v[84:87],v[24:27],v[188:191],v[84:87]\n"
"s_min_u32 s1,s1,s2\n"
"v_readlane_b32 s4,v11,32\n"
"v_readlane_b32 s5,v11,56\n"
MF "v[88:91],v[24:27],v[192:195],v[88:91]\n"
"s_waitcnt lgkmcnt(2)\n"
"ds_read_b128 v[24:27],v0 offset:6144\n"
"v_max_f32_dpp v12,|v12|,|v12| quad_perm:[1,0,3,2] row_mask:0xf bank_mask:0xf\n"
MF "v[72:75],v[28:31],v[196:199],v[72:75]\n"
"s_add_u32 m0,s65,73728\n"
MF "v[76:79],v[28:31],v[200:203],v[76:79]\n"
LD "v8,s[36:39],s73 offen offset:3072 nt lds\n"
MF "v[80:83],v[28:31],v[204:207],v[80:83]\n"
"s_min_u32 s13,s13,s1\n"
"v_max_f32_dpp v12,v12,v12 quad_perm:[2,3,0,1] row_mask:0xf bank_mask:0xf\n"
MF "v[84:87],v[28:31],v[208:211],v[84:87]\n"
"s_and_b32 s13,s13,0x7f800000\n"
MF "v[88:91],v[28:31],v[212:215],v[88:91]\n"
"s_waitcnt lgkmcnt(2)\n"
"ds_read_b128 v[28:31],v1 offset:6144\n"
"v_max_f32_dpp v12,v12,v12 row_half_mirror row_mask:0xf bank_mask:0xf\n"
MF "v[72:75],v[16:19],v[216:219],v[72:75]\n"
"s_add_u32 m0,s65,74624\n"
"v_readlane_b32 s10,v12,0\n"
"v_readlane_b32 s6,v12,24\n"
MF "v[76:79],v[16:19],v[220:223],v[76:79]\n"
LD "v8,s[36:39],s73 offen offset:3200 nt lds\n"
MF "v[80:83],v[16:19],v[224:227],v[80:83]\n"
"s_sub_u32 s13,s13,0x14000000\n"
"v_readlane_b32 s7,v12,32\n"
"v_readlane_b32 s8,v12,56\n"
MF "v[84:87],v[16:19],v[228:231],v[84:87]\n"
"s_cmp_lt_u32 s13,0x50000000\n"
"s_cselect_b32 s13,s13,0xed400000\n"
MF "v[88:91],v[16:19],v[232:235],v[88:91]\n"
"s_waitcnt lgkmcnt(2)\n"
"ds_read_b128 v[16:19],v0 offset:7168\n"
MF "v[72:75],v[20:23],v[236:239],v[72:75]\n"
"s_add_u32 m0,s65,75520\n"
MF "v[76:79],v[20:23],v[240:243],v[76:79]\n"
LD "v8,s[36:39],s73 offen offset:3328 nt lds\n"
MF "v[80:83],v[20:23],v[244:247],v[80:83]\n"
"s_add_u32 s13,s13,0x12c00000\n"
MF "v[84:87],v[20:23],v[248:251],v[84:87]\n"
"s_min_u32 s11,s11,s3\n"
MF "v[88:91],v[20:23],v[252:255],v[88:91]\n"
"s_waitcnt lgkmcnt(2)\n"
"ds_read_b128 v[20:23],v1 offset:7168\n"
MF "v[112:115],v[24:27],v[136:139],0\n"
"s_min_u32 s4,s4,s5\n"
MF "v[116:119],v[24:27],v[140:143],0\n"
"s_min_u32 s11,s11,s4\n"
MF "v[120:123],v[24:27],v[144:147],0\n"
"s_and_b32 s11,s11,0x7f800000\n"
MF "v[124:127],v[24:27],v[148:151],0\n"
"s_sub_u32 s11,s11,0x14000000\n"
MF "v[128:131],v[24:27],v[152:155],0\n"
"s_waitcnt lgkmcnt(2)\n"
"ds_read_b128 v[24:27],v0 offset:8192\n"
MF "v[112:115],v[28:31],v[156:159],v[112:115]\n"
"s_min_u32 s10,s10,s6\n"
"v_max3_f32 v52,v72,v73,v74\n"
"v_max_f32 v52,v52,v75\n"
MF "v[116:119],v[28:31],v[160:163],v[116:119]\n"
"s_min_u32 s7,s7,s8\n"
"v_max3_f32 v53,v76,v77,v78\n"
"v_max_f32 v53,v53,v79\n"
MF "v[120:123],v[28:31],v[164:167],v[120:123]\n"
"s_min_u32 s10,s10,s7\n"
"v_max3_f32 v54,v80,v81,v82\n"
"v_max_f32 v54,v54,v83\n"
MF "v[124:127],v[28:31],v[168:171],v[124:127]\n"
"v_max3_f32 v55,v84,v85,v86\n"
"v_max_f32 v55,v55,v87\n"
MF "v[128:131],v[28:31],v[172:175],v[128:131]\n"
"s_waitcnt lgkmcnt(2)\n"
"ds_read_b128 v[28:31],v1 offset:8192\n"
"v_max3_f32 v56,v88,v89,v90\n"
MF "v[112:115],v[16:19],v[176:179],v[112:115]\n"
"s_cmp_lt_u32 s11,0x50000000\n"
"s_cselect_b32 s11,s11,0xed400000\n"
"v_max_f32 v56,v56,v91\n"
MF "v[116:119],v[16:19],v[180:183],v[116:119]\n"
"s_add_u32 s11,s11,0x12c00000\n"
MF "v[120:123],v[16:19],v[184:187],v[120:123]\n"
MF "v[124:127],v[16:19],v[188:191],v[124:127]\n"
MF "v[128:131],v[16:19],v[192:195],v[128:131]\n"
"s_waitcnt lgkmcnt(2)\n"
MF "v[112:115],v[20:23],v[196:199],v[112:115]\n"
MF "v[116:119],v[20:23],v[200:203],v[116:119]\n"
"s_waitcnt lgkmcnt(0)\n"
"s_barrier\n"
MF "v[120:123],v[20:23],v[204:207],v[120:123]\n"
"s_add_u32 s72,s72,6144\n"
MF "v[124:127],v[20:23],v[208:211],v[124:127]\n"
"s_cmp_eq_u32 s72,s17\n"
"s_cselect_b32 s72,s16,s72\n"
"s_add_u32 m0,s64,0\n"
MF "v[128:131],v[20:23],v[212:215],v[128:131]\n"
LD "v7,s[32:35],s72 offen nt lds\n"
MF "v[112:115],v[24:27],v[216:219],v[112:115]\n"
"s_add_u32 m0,s64,896\n"
MF "v[116:119],v[24:27],v[220:223],v[116:119]\n"
LD "v7,s[32:35],s72 offen offset:128 nt lds\n"
MF "v[120:123],v[24:27],v[224:227],v[120:123]\n"
"s_add_u32 m0,s64,1792\n"
MF "v[124:127],v[24:27],v[228:231],v[124:127]\n"
LD "v7,s[32:35],s72 offen offset:256 nt lds\n"
MF "v[128:131],v[24:27],v[232:235],v[128:131]\n"
"s_add_u32 s72,s72,6144\n"
"s_add_u32 m0,s64,6144\n"
MF "v[112:115],v[28:31],v[236:239],v[112:115]\n"
LD "v7,s[32:35],s72 offen nt lds\n"
MF "v[116:119],v[28:31],v[240:243],v[116:119]\n"
"s_add_u32 m0,s64,7040\n"
MF "v[120:123],v[28:31],v[244:247],v[120:123]\n"
LD "v7,s[32:35],s72 offen offset:128 nt lds\n"
MF "v[124:127],v[28:31],v[248:251],v[124:127]\n"
"s_add_u32 m0,s64,7936\n"
MF "v[128:131],v[28:31],v[252:255],v[128:131]\n"
LD "v7,s[32:35],s72 offen offset:256 nt lds\n"
"s_branch .Lt0ex\n"
".Lt0kx:\n"
"s_lshl_b32 s58,s56,10\n"
"s_add_u32 s58,s58,73728\n"
"v_add_u32 v32,s58,v9\n"
"v_add_u32 v33,65536,v32\n"
"s_waitcnt lgkmcnt(2)\n"
MF "v[72:75],v[16:19],v[136:139],0\n"
"s_add_u32 s73,s73,12288\n"
"ds_read_b128 v[28:31],v1 offset:1024\n"
AR "v10,a224\n"
MF "v[76:79],v[16:19],v[140:143],0\n"
"s_cmp_eq_u32 s73,s19\n"
"s_cselect_b32 s73,s18,s73\n"
"s_add_u32 m0,s65,73728\n"
MF "v[80:83],v[16:19],v[144:147],0\n"
LD "v8,s[36:39],s73 offen nt lds\n"
MF "v[84:87],v[16:19],v[148:151],0\n"
"v_max_f32_dpp v10,|v10|,|v10| quad_perm:[1,0,3,2] row_mask:0xf bank_mask:0xf\n"
AR "v11,a225\n"
MF "v[88:91],v[16:19],v[152:155],0\n"
"s_waitcnt lgkmcnt(2)\n"
"ds_read_b128 v[16:19],v0 offset:2048\n"
AR "v12,a226\n"
MF "v[72:75],v[20:23],v[156:159],v[72:75]\n"
"s_add_u32 m0,s65,74624\n"
"v_max_f32_dpp v10,v10,v10 quad_perm:[2,3,0,1] row_mask:0xf bank_mask:0xf\n"
"v_max_f32_dpp v11,|v11|,|v11| quad_perm:[1,0,3,2] row_mask:0xf bank_mask:0xf\n"
MF "v[76:79],v[20:23],v[160:163],v[76:79]\n"
LD "v8,s[36:39],s73 offen offset:128 nt lds\n"
MF "v[80:83],v[20:23],v[164:167],v[80:83]\n"
"v_max_f32_dpp v10,v10,v10 row_half_mirror row_mask:0xf bank_mask:0xf\n"
"v_max_f32_dpp v11,v11,v11 quad_perm:[2,3,0,1] row_mask:0xf bank_mask:0xf\n"
MF "v[84:87],v[20:23],v[168:171],v[84:87]\n"
"v_readlane_b32 s13,v10,0\n"
"v_readlane_b32 s0,v10,24\n"
MF "v[88:91],v[20:23],v[172:175],v[88:91]\n"
"s_waitcnt lgkmcnt(2)\n"
"ds_read_b128 v[20:23],v1 offset:2048\n"
"v_readlane_b32 s1,v10,32\n"
MF "v[72:75],v[24:27],v[176:179],v[72:75]\n"
"s_add_u32 m0,s65,75520\n"
"v_readlane_b32 s2,v10,56\n"
"v_max_f32_dpp v11,v11,v11 row_half_mirror row_mask:0xf bank_mask:0xf\n"
MF "v[76:79],v[24:27],v[180:183],v[76:79]\n"
LD "v8,s[36:39],s73 offen offset:256 nt lds\n"
MF "v[80:83],v[24:27],v[184:187],v[80:83]\n"
"s_min_u32 s13,s13,s0\n"
"v_readlane_b32 s11,v11,0\n"
"v_readlane_b32 s3,v11,24\n"
MF "v[84:87],v[24:27],v[188:191],v[84:87]\n"
"s_min_u32 s1,s1,s2\n"
"v_readlane_b32 s4,v11,32\n"
"v_readlane_b32 s5,v11,56\n"
MF "v[88:91],v[24:27],v[192:195],v[88:91]\n"
"s_waitcnt lgkmcnt(2)\n"
"ds_read_b128 v[24:27],v0 offset:6144\n"
"v_max_f32_dpp v12,|v12|,|v12| quad_perm:[1,0,3,2] row_mask:0xf bank_mask:0xf\n"
MF "v[72:75],v[28:31],v[196:199],v[72:75]\n"
"s_add_u32 m0,s65,73728\n"
"ds_write_b128 v32,a[0:3] offset:24576\n"
MF "v[76:79],v[28:31],v[200:203],v[76:79]\n"
LD "v8,s[36:39],s73 offen offset:3072 nt lds\n"
MF "v[80:83],v[28:31],v[204:207],v[80:83]\n"
"s_min_u32 s13,s13,s1\n"
"v_max_f32_dpp v12,v12,v12 quad_perm:[2,3,0,1] row_mask:0xf bank_mask:0xf\n"
AR "v62,a20\n"
MF "v[84:87],v[28:31],v[208:211],v[84:87]\n"
"s_and_b32 s13,s13,0x7f800000\n"
"ds_write_b128 v32,a[4:7] offset:28672\n"
MF "v[88:91],v[28:31],v[212:215],v[88:91]\n"
"s_waitcnt lgkmcnt(4)\n"
"ds_read_b128 v[28:31],v1 offset:6144\n"
"v_max_f32_dpp v12,v12,v12 row_half_mirror row_mask:0xf bank_mask:0xf\n"
MF "v[72:75],v[16:19],v[216:219],v[72:75]\n"
"s_add_u32 m0,s65,74624\n"
"v_readlane_b32 s10,v12,0\n"
"v_readlane_b32 s6,v12,24\n"
MF "v[76:79],v[16:19],v[220:223],v[76:79]\n"
LD "v8,s[36:39],s73 offen offset:3200 nt lds\n"
MF "v[80:83],v[16:19],v[224:227],v[80:83]\n"
"s_sub_u32 s13,s13,0x14000000\n"
"v_readlane_b32 s7,v12,32\n"
"v_readlane_b32 s8,v12,56\n"
MF "v[84:87],v[16:19],v[228:231],v[84:87]\n"
"s_cmp_lt_u32 s13,0x50000000\n"
"s_cselect_b32 s13,s13,0xed400000\n"
AR "v63,a21\n"
MF "v[88:91],v[16:19],v[232:235],v[88:91]\n"
"s_waitcnt lgkmcnt(4)\n"
"ds_read_b128 v[16:19],v0 offset:7168\n"
AR "v64,a22\n"
MF "v[72:75],v[20:23],v[236:239],v[72:75]\n"
"s_add_u32 m0,s65,75520\n"
"ds_write_b128 v32,a[8:11] offset:32768\n"
MF "v[76:79],v[20:23],v[240:243],v[76:79]\n"
LD "v8,s[36:39],s73 offen offset:3328 nt lds\n"
MF "v[80:83],v[20:23],v[244:247],v[80:83]\n"
"s_add_u32 s13,s13,0x12c00000\n"
"ds_write_b128 v32,a[12:15] offset:36864\n"
MF "v[84:87],v[20:23],v[248:251],v[84:87]\n"
"s_min_u32 s11,s11,s3\n"
"ds_write_b128 v32,a[16:19] offset:40960\n"
MF "v[88:91],v[20:23],v[252:255],v[88:91]\n"
"s_waitcnt lgkmcnt(7)\n"
"ds_read_b128 v[20:23],v1 offset:7168\n"
AR "v65,a23\n"
MF "v[112:115],v[24:27],v[136:139],0\n"
"s_min_u32 s4,s4,s5\n"
"ds_write_b128 v32,a[40:43] offset:45056\n"
MF "v[116:119],v[24:27],v[140:143],0\n"
"s_min_u32 s11,s11,s4\n"
"ds_write_b128 v32,a[44:47] offset:49152\n"
MF "v[120:123],v[24:27],v[144:147],0\n"
"s_and_b32 s11,s11,0x7f800000\n"
"ds_write_b128 v32,a[48:51] offset:53248\n"
MF "v[124:127],v[24:27],v[148:151],0\n"
"s_sub_u32 s11,s11,0x14000000\n"
"ds_write_b128 v32,a[52:55] offset:57344\n"
MF "v[128:131],v[24:27],v[152:155],0\n"
"s_waitcnt lgkmcnt(9)\n"
"ds_read_b128 v[24:27],v0 offset:8192\n"
AR "v66,a24\n"
MF "v[112:115],v[28:31],v[156:159],v[112:115]\n"
"s_min_u32 s10,s10,s6\n"
"v_max3_f32 v52,v72,v73,v74\n"
"v_max_f32 v52,v52,v75\n"
MF "v[116:119],v[28:31],v[160:163],v[116:119]\n"
"s_min_u32 s7,s7,s8\n"
"v_max3_f32 v53,v76,v77,v78\n"
"v_max_f32 v53,v53,v79\n"
MF "v[120:123],v[28:31],v[164:167],v[120:123]\n"
"s_min_u32 s10,s10,s7\n"
"v_max3_f32 v54,v80,v81,v82\n"
"v_max_f32 v54,v54,v83\n"
MF "v[124:127],v[28:31],v[168:171],v[124:127]\n"
"v_max3_f32 v55,v84,v85,v86\n"
"v_max_f32 v55,v55,v87\n"
MF "v[128:131],v[28:31],v[172:175],v[128:131]\n"
"s_waitcnt lgkmcnt(9)\n"
"ds_read_b128 v[28:31],v1 offset:8192\n"
"v_max3_f32 v56,v88,v89,v90\n"
MF "v[112:115],v[16:19],v[176:179],v[112:115]\n"
"s_cmp_lt_u32 s11,0x50000000\n"
"s_cselect_b32 s11,s11,0xed400000\n"
"v_max_f32 v56,v56,v91\n"
MF "v[116:119],v[16:19],v[180:183],v[116:119]\n"
"s_add_u32 s11,s11,0x12c00000\n"
"ds_write_b128 v32,a[56:59] offset:61440\n"
MF "v[120:123],v[16:19],v[184:187],v[120:123]\n"
"ds_write_b128 v33,a[80:83]\n"
MF "v[124:127],v[16:19],v[188:191],v[124:127]\n"
"ds_write_b128 v33,a[84:87] offset:4096\n"
MF "v[128:131],v[16:19],v[192:195],v[128:131]\n"
"s_waitcnt lgkmcnt(9)\n"
"ds_write_b128 v33,a[88:91] offset:8192\n"
MF "v[112:115],v[20:23],v[196:199],v[112:115]\n"
AR "v67,a25\n"
AR "v68,a26\n"
MF "v[116:119],v[20:23],v[200:203],v[116:119]\n"
"s_waitcnt lgkmcnt(0)\n"
"s_barrier\n"
MF "v[120:123],v[20:23],v[204:207],v[120:123]\n"
"s_add_u32 s72,s72,6144\n"
"ds_write_b128 v33,a[92:95] offset:12288\n"
MF "v[124:127],v[20:23],v[208:211],v[124:127]\n"
"s_cmp_eq_u32 s72,s17\n"
"s_cselect_b32 s72,s16,s72\n"
"s_add_u32 m0,s64,0\n"
MF "v[128:131],v[20:23],v[212:215],v[128:131]\n"
LD "v7,s[32:35],s72 offen nt lds\n"
MF "v[112:115],v[24:27],v[216:219],v[112:115]\n"
"s_add_u32 m0,s64,896\n"
"ds_write_b128 v33,a[96:99] offset:16384\n"
MF "v[116:119],v[24:27],v[220:223],v[116:119]\n"
LD "v7,s[32:35],s72 offen offset:128 nt lds\n"
MF "v[120:123],v[24:27],v[224:227],v[120:123]\n"
"s_add_u32 m0,s64,1792\n"
AR "v69,a27\n"
AR "v70,a28\n"
MF "v[124:127],v[24:27],v[228:231],v[124:127]\n"
LD "v7,s[32:35],s72 offen offset:256 nt lds\n"
MF "v[128:131],v[24:27],v[232:235],v[128:131]\n"
"s_add_u32 s72,s72,6144\n"
"s_add_u32 m0,s64,6144\n"
AR "v71,a29\n"
MF "v[112:115],v[28:31],v[236:239],v[112:115]\n"
LD "v7,s[32:35],s72 offen nt lds\n"
MF "v[116:119],v[28:31],v[240:243],v[116:119]\n"
"s_add_u32 m0,s64,7040\n"
"ds_write_b128 v33,a[120:123] offset:20480\n"
MF "v[120:123],v[28:31],v[244:247],v[120:123]\n"
LD "v7,s[32:35],s72 offen offset:128 nt lds\n"
MF "v[124:127],v[28:31],v[248:251],v[124:127]\n"
"s_add_u32 m0,s64,7936\n"
AR "v132,a30\n"
AR "v133,a31\n"
MF "v[128:131],v[28:31],v[252:255],v[128:131]\n"
LD "v7,s[32:35],s72 offen offset:256 nt lds\n"
AR "v134,a32\n"
AR "v135,a33\n"
AR "v6,a34\n"
AR "v13,a35\n"
AR "v14,a36\n"
AR "v15,a37\n"
"v_accvgpr_mov_b32 a254,a38\n"
"v_accvgpr_mov_b32 a255,a39\n"
"v_accvgpr_mov_b32 a240,a168\n"
"v_accvgpr_mov_b32 a241,a169\n"
"v_accvgpr_mov_b32 a242,a170\n"
"v_accvgpr_mov_b32 a243,a171\n"
"v_accvgpr_mov_b32 a244,a172\n"
"v_accvgpr_mov_b32 a245,a173\n"
"v_accvgpr_mov_b32 a246,a174\n"
"v_accvgpr_mov_b32 a247,a175\n"
"v_accvgpr_mov_b32 a248,a176\n"
"v_accvgpr_mov_b32 a249,a177\n"
"v_accvgpr_mov_b32 a250,a178\n"
"v_accvgpr_mov_b32 a251,a179\n"
".Lt0ex:\n"
"s_nop 1\n"
"s_nop 0\n"
"v_max3_f32 v52,v52,v112,v113\n"
"v_max3_f32 v53,v53,v116,v117\n"
"v_max3_f32 v54,v54,v120,v121\n"
"v_max3_f32 v55,v55,v124,v125\n"
"v_max3_f32 v56,v56,v128,v129\n"
"v_max3_f32 v52,v52,v114,v115\n"
"v_max3_f32 v53,v53,v118,v119\n"
"v_max3_f32 v54,v54,v122,v123\n"
"v_max3_f32 v55,v55,v126,v127\n"
"v_max3_f32 v56,v56,v130,v131\n"
"v_mov_b32 v57,v52\n"
"v_mov_b32 v58,v53\n"
"v_mov_b32 v59,v54\n"
"v_mov_b32 v60,v55\n"
"v_mov_b32 v61,v56\n"
"v_permlane16_swap_b32 v52,v57\n"
"v_permlane16_swap_b32 v53,v58\n"
"v_permlane16_swap_b32 v54,v59\n"
"v_permlane16_swap_b32 v55,v60\n"
"v_permlane16_swap_b32 v56,v61\n"
"v_max_f32 v52,v52,v57\n"
"v_max_f32 v53,v53,v58\n"
"v_max_f32 v54,v54,v59\n"
"v_max_f32 v55,v55,v60\n"
"v_max_f32 v56,v56,v61\n"
"v_mov_b32 v57,v52\n"
"v_mov_b32 v58,v53\n"
"v_mov_b32 v59,v54\n"
"v_mov_b32 v60,v55\n"
"v_mov_b32 v61,v56\n"
"v_permlane32_swap_b32 v52,v57\n"
"v_permlane32_swap_b32 v53,v58\n"
"v_permlane32_swap_b32 v54,v59\n"
"v_permlane32_swap_b32 v55,v60\n"
"v_permlane32_swap_b32 v56,v61\n"
"v_max_f32 v52,v52,v57\n"
"v_max_f32 v53,v53,v58\n"
"v_max_f32 v54,v54,v59\n"
"v_max_f32 v55,v55,v60\n"
"v_max_f32 v56,v56,v61\n"
"v_sub_f32 v32,0xc1300000,v52\n"
"v_sub_f32 v36,0xc1300000,v53\n"
"v_sub_f32 v40,0xc1300000,v54\n"
"v_sub_f32 v44,0xc1300000,v55\n"
"v_sub_f32 v48,0xc1300000,v56\n"
"v_mov_b32 v33,v32\n"
"v_mov_b32 v34,v32\n"
"v_mov_b32 v35,v32\n"
"v_mov_b32 v37,v36\n"
"v_mov_b32 v38,v36\n"
"v_mov_b32 v39,v36\n"
"v_mov_b32 v41,v40\n"
"v_mov_b32 v42,v40\n"
"v_mov_b32 v43,v40\n"
"v_mov_b32 v45,v44\n"
"v_mov_b32 v46,v44\n"
"v_mov_b32 v47,v44\n"
"v_mov_b32 v49,v48\n"
"v_mov_b32 v50,v48\n"
"v_mov_b32 v51,v48\n"
"v_add_f32 v72,v72,v32\n"
"v_add_f32 v73,v73,v32\n"
"v_add_f32 v74,v74,v32\n"
"v_add_f32 v75,v75,v32\n"
"v_add_f32 v112,v112,v32\n"
"v_add_f32 v113,v113,v32\n"
"v_add_f32 v114,v114,v32\n"
"v_add_f32 v115,v115,v32\n"
"v_exp_f32 v72,v72\n"
"v_exp_f32 v73,v73\n"
"v_exp_f32 v74,v74\n"
"v_exp_f32 v75,v75\n"
"v_exp_f32 v112,v112\n"
"v_exp_f32 v113,v113\n"
"v_exp_f32 v114,v114\n"
"v_exp_f32 v115,v115\n"
"v_add_f32 v52,v72,v73\n"
"v_add_f32 v52,v52,v74\n"
"v_add_f32 v52,v52,v75\n"
"v_add_f32 v52,v52,v112\n"
"v_add_f32 v52,v52,v113\n"
"v_add_f32 v52,v52,v114\n"
"v_add_f32 v52,v52,v115\n"
CV "v72,v72,v73\n"
CV "v73,v74,v75\n"
CV "v74,v112,v113\n"
CV "v75,v114,v115\n"
"v_add_f32 v76,v76,v36\n"
"v_add_f32 v77,v77,v36\n"
"v_add_f32 v78,v78,v36\n"
"v_add_f32 v79,v79,v36\n"
"v_add_f32 v116,v116,v36\n"
"v_add_f32 v117,v117,v36\n"
"v_add_f32 v118,v118,v36\n"
"v_add_f32 v119,v119,v36\n"
"v_exp_f32 v76,v76\n"
"v_exp_f32 v77,v77\n"
"v_exp_f32 v78,v78\n"
"v_exp_f32 v79,v79\n"
"v_exp_f32 v116,v116\n"
"v_exp_f32 v117,v117\n"
"v_exp_f32 v118,v118\n"
"v_exp_f32 v119,v119\n"
"v_add_f32 v53,v76,v77\n"
"v_add_f32 v53,v53,v78\n"
"v_add_f32 v53,v53,v79\n"
"v_add_f32 v53,v53,v116\n"
"v_add_f32 v53,v53,v117\n"
"v_add_f32 v53,v53,v118\n"
"v_add_f32_e64 v53,v53,v119\n"
CV "v76,v76,v77\n"
CV "v77,v78,v79\n"
CV "v78,v116,v117\n"
CV "v79,v118,v119\n"
"v_add_f32 v80,v80,v40\n"
"v_add_f32 v81,v81,v40\n"
"v_add_f32 v82,v82,v40\n"
"v_add_f32 v83,v83,v40\n"
"v_add_f32 v120,v120,v40\n"
"v_add_f32 v121,v121,v40\n"
"v_add_f32 v122,v122,v40\n"
"v_add_f32 v123,v123,v40\n"
"v_exp_f32 v80,v80\n"
"v_exp_f32 v81,v81\n"
"v_exp_f32 v82,v82\n"
"v_exp_f32 v83,v83\n"
"v_exp_f32 v120,v120\n"
"v_exp_f32 v121,v121\n"
"v_exp_f32 v122,v122\n"
"v_exp_f32 v123,v123\n"
"v_add_f32 v54,v80,v81\n"
"v_add_f32 v54,v54,v82\n"
"v_add_f32 v54,v54,v83\n"
"v_add_f32 v54,v54,v120\n"
"v_add_f32 v54,v54,v121\n"
"v_add_f32 v54,v54,v122\n"
"v_add_f32_e64 v54,v54,v123\n"
CV "v80,v80,v81\n"
CV "v81,v82,v83\n"
CV "v82,v120,v121\n"
CV "v83,v122,v123\n"
"v_add_f32 v84,v84,v44\n"
"v_add_f32 v85,v85,v44\n"
"v_add_f32 v86,v86,v44\n"
"v_add_f32 v87,v87,v44\n"
"v_add_f32 v124,v124,v44\n"
"v_add_f32 v125,v125,v44\n"
"v_add_f32 v126,v126,v44\n"
"v_add_f32 v127,v127,v44\n"
"v_mov_b32 v55,0\n"
"v_add_f32 v88,v88,v48\n"
"v_add_f32 v89,v89,v48\n"
"v_add_f32 v90,v90,v48\n"
"v_add_f32 v91,v91,v48\n"
"v_add_f32 v128,v128,v48\n"
"v_add_f32 v129,v129,v48\n"
"v_add_f32 v130,v130,v48\n"
"v_add_f32 v131,v131,v48\n"
"v_mov_b32 v56,0\n"
"s_waitcnt vmcnt(18) lgkmcnt(0)\n"
"s_barrier\n"
"ds_read_b128 v[16:19],v0 offset:24576\n"
"ds_read_b128 v[20:23],v1 offset:24576\n"
"ds_read_b128 v[24:27],v0 offset:25600\n"
AW "a220,s13\n"
AW "a221,s13\n"
AW "a222,s13\n"
AW "a223,s13\n"
"s_waitcnt lgkmcnt(2)\n"
MF "v[92:95],v[16:19],v[136:139],v[32:35]\n"
"ds_read_b128 v[28:31],v1 offset:25600\n"
"s_waitcnt lgkmcnt(2)\n"
MF "v[92:95],v[20:23],v[156:159],v[92:95]\n"
"v_exp_f32 v84,v84\n"
MF "v[96:99],v[20:23],v[160:163],v[36:39]\n"
"v_exp_f32 v85,v85\n"
MF "v[96:99],v[16:19],v[140:143],v[96:99]\n"
"v_exp_f32 v86,v86\n"
MF "v[100:103],v[16:19],v[144:147],v[40:43]\n"
"v_exp_f32 v87,v87\n"
MF "v[100:103],v[20:23],v[164:167],v[100:103]\n"
"v_add_f32 v55,v55,v84\n"
"v_add_f32 v55,v55,v85\n"
MF "v[104:107],v[20:23],v[168:171],v[44:47]\n"
CV "v84,v84,v85\n"
"v_add_f32 v55,v55,v86\n"
MF "v[104:107],v[16:19],v[148:151],v[104:107]\n"
"v_add_f32 v55,v55,v87\n"
CV "v85,v86,v87\n"
MF "v[108:111],v[16:19],v[152:155],v[48:51]\n"
"v_exp_f32 v124,v124\n"
MF "v[108:111],v[20:23],v[172:175],v[108:111]\n"
"s_waitcnt lgkmcnt(0)\n"
"ds_read_b128 v[16:19],v0 offset:26624\n"
"ds_read_b128 v[20:23],v1 offset:26624\n"
MF "v[108:111],v[24:27],v[192:195],v[108:111]\n"
"v_exp_f32 v125,v125\n"
MF "v[108:111],v[28:31],v[212:215],v[108:111]\n"
"v_exp_f32 v126,v126\n"
MF "v[104:107],v[28:31],v[208:211],v[104:107]\n"
"v_exp_f32 v127,v127\n"
MF "v[104:107],v[24:27],v[188:191],v[104:107]\n"
"v_add_f32 v55,v55,v124\n"
"v_add_f32 v55,v55,v125\n"
MF "v[100:103],v[24:27],v[184:187],v[100:103]\n"
CV "v86,v124,v125\n"
"v_add_f32 v55,v55,v126\n"
MF "v[100:103],v[28:31],v[204:207],v[100:103]\n"
"v_add_f32 v55,v55,v127\n"
CV "v87,v126,v127\n"
MF "v[96:99],v[28:31],v[200:203],v[96:99]\n"
"v_exp_f32 v88,v88\n"
MF "v[96:99],v[24:27],v[180:183],v[96:99]\n"
"v_exp_f32 v89,v89\n"
MF "v[92:95],v[24:27],v[176:179],v[92:95]\n"
"v_exp_f32 v90,v90\n"
MF "v[92:95],v[28:31],v[196:199],v[92:95]\n"
"s_waitcnt lgkmcnt(0)\n"
"ds_read_b128 v[24:27],v0 offset:30720\n"
"ds_read_b128 v[28:31],v1 offset:30720\n"
MF "v[92:95],v[16:19],v[216:219],v[92:95]\n"
"v_exp_f32 v91,v91\n"
MF "v[92:95],v[20:23],v[236:239],v[92:95]\n"
"v_add_f32 v56,v56,v88\n"
"v_add_f32 v56,v56,v89\n"
MF "v[96:99],v[20:23],v[240:243],v[96:99]\n"
CV "v88,v88,v89\n"
"v_add_f32 v56,v56,v90\n"
MF "v[96:99],v[16:19],v[220:223],v[96:99]\n"
"v_add_f32 v56,v56,v91\n"
CV "v89,v90,v91\n"
MF "v[100:103],v[16:19],v[224:227],v[100:103]\n"
"v_exp_f32 v128,v128\n"
MF "v[100:103],v[20:23],v[244:247],v[100:103]\n"
"v_exp_f32 v129,v129\n"
MF "v[104:107],v[20:23],v[248:251],v[104:107]\n"
"v_exp_f32 v130,v130\n"
MF "v[104:107],v[16:19],v[228:231],v[104:107]\n"
"v_exp_f32 v131,v131\n"
MF "v[108:111],v[16:19],v[232:235],v[108:111]\n"
"v_add_f32 v56,v56,v128\n"
"v_add_f32 v56,v56,v129\n"
MF "v[108:111],v[20:23],v[252:255],v[108:111]\n"
"s_waitcnt lgkmcnt(0)\n"
"ds_read_b128 v[16:19],v0 offset:31744\n"
"ds_read_b128 v[20:23],v1 offset:31744\n"
MF "v[112:115],v[24:27],v[136:139],v[32:35]\n"
CV "v90,v128,v129\n"
"v_add_f32 v56,v56,v130\n"
MF "v[112:115],v[28:31],v[156:159],v[112:115]\n"
"v_add_f32 v56,v56,v131\n"
CV "v91,v130,v131\n"
MF "v[116:119],v[28:31],v[160:163],v[36:39]\n"
"v_exp_f32 v92,v92\n"
MF "v[116:119],v[24:27],v[140:143],v[116:119]\n"
"v_exp_f32 v93,v93\n"
MF "v[120:123],v[24:27],v[144:147],v[40:43]\n"
"v_exp_f32 v94,v94\n"
MF "v[120:123],v[28:31],v[164:167],v[120:123]\n"
"v_exp_f32 v95,v95\n"
MF "v[124:127],v[28:31],v[168:171],v[44:47]\n"
"v_add_f32 v57,v52,v92\n"
"v_add_f32 v57,v57,v93\n"
MF "v[124:127],v[24:27],v[148:151],v[124:127]\n"
CV "v92,v92,v93\n"
"v_add_f32 v57,v57,v94\n"
MF "v[128:131],v[24:27],v[152:155],v[48:51]\n"
"v_add_f32 v57,v57,v95\n"
CV "v93,v94,v95\n"
MF "v[128:131],v[28:31],v[172:175],v[128:131]\n"
"s_waitcnt lgkmcnt(0)\n"
"ds_read_b128 v[24:27],v0 offset:32768\n"
"ds_read_b128 v[28:31],v1 offset:32768\n"
MF "v[128:131],v[16:19],v[192:195],v[128:131]\n"
"v_exp_f32 v96,v96\n"
MF "v[128:131],v[20:23],v[212:215],v[128:131]\n"
"v_exp_f32 v97,v97\n"
MF "v[124:127],v[20:23],v[208:211],v[124:127]\n"
"v_exp_f32 v98,v98\n"
MF "v[124:127],v[16:19],v[188:191],v[124:127]\n"
"v_exp_f32 v99,v99\n"
MF "v[120:123],v[16:19],v[184:187],v[120:123]\n"
"v_add_f32 v58,v53,v96\n"
"v_add_f32 v58,v58,v97\n"
MF "v[120:123],v[20:23],v[204:207],v[120:123]\n"
CV "v96,v96,v97\n"
"v_add_f32 v58,v58,v98\n"
MF "v[116:119],v[20:23],v[200:203],v[116:119]\n"
"v_add_f32 v58,v58,v99\n"
CV "v97,v98,v99\n"
MF "v[116:119],v[16:19],v[180:183],v[116:119]\n"
"s_waitcnt vmcnt(12) lgkmcnt(0)\n"
"s_barrier\n"
MF "v[112:115],v[16:19],v[176:179],v[112:115]\n"
"v_exp_f32 v100,v100\n"
MF "v[112:115],v[20:23],v[196:199],v[112:115]\n"
"s_nop 0\n"
TR "v[16:17],v2\n"
TR "v[18:19],v2 offset:6144\n"
MF "v[112:115],v[24:27],v[216:219],v[112:115]\n"
"s_add_u32 s72,s72,6144\n"
TR "v[20:21],v3\n"
TR "v[22:23],v3 offset:6144\n"
MF "v[112:115],v[28:31],v[236:239],v[112:115]\n"
"s_cmp_eq_u32 s72,s17\n"
"s_cselect_b32 s72,s16,s72\n"
"s_add_u32 m0,s64,24576\n"
MF "v[116:119],v[28:31],v[240:243],v[116:119]\n"
LD "v7,s[32:35],s72 offen nt lds\n"
MF "v[116:119],v[24:27],v[220:223],v[116:119]\n"
"s_add_u32 m0,s64,25472\n"
"v_exp_f32 v101,v101\n"
MF "v[120:123],v[24:27],v[224:227],v[120:123]\n"
LD "v7,s[32:35],s72 offen offset:128 nt lds\n"
MF "v[120:123],v[28:31],v[244:247],v[120:123]\n"
"v_exp_f32 v102,v102\n"
MF "v[124:127],v[28:31],v[248:251],v[124:127]\n"
"s_add_u32 m0,s64,26368\n"
"v_exp_f32 v103,v103\n"
MF "v[124:127],v[24:27],v[228:231],v[124:127]\n"
LD "v7,s[32:35],s72 offen offset:256 nt lds\n"
MF "v[128:131],v[24:27],v[232:235],v[128:131]\n"
"s_add_u32 s72,s72,6144\n"
"v_add_f32 v59,v54,v100\n"
"v_add_f32 v59,v59,v101\n"
MF "v[128:131],v[28:31],v[252:255],v[128:131]\n"
"s_waitcnt lgkmcnt(2)\n"
TR "v[24:25],v4\n"
TR "v[26:27],v4 offset:6144\n"
MF "a[0:3],v[16:19],v[72:75],a[220:223]\n"
"s_add_u32 m0,s64,30720\n"
TR "v[28:29],v5\n"
TR "v[30:31],v5 offset:6144\n"
MF "a[4:7],v[16:19],v[76:79],a[220:223]\n"
LD "v7,s[32:35],s72 offen nt lds\n"
MF "a[8:11],v[16:19],v[80:83],a[220:223]\n"
"s_add_u32 m0,s64,31616\n"
CV "v100,v100,v101\n"
"v_add_f32 v59,v59,v102\n"
MF "a[12:15],v[16:19],v[84:87],a[220:223]\n"
LD "v7,s[32:35],s72 offen offset:128 nt lds\n"
MF "a[16:19],v[16:19],v[88:91],a[220:223]\n"
"s_waitcnt lgkmcnt(4)\n"
TR "v[16:17],v2 offset:1024\n"
TR "v[18:19],v2 offset:7168\n"
MF "a[36:39],v[20:23],v[88:91],a[220:223]\n"
"v_add_f32 v59,v59,v103\n"
CV "v101,v102,v103\n"
MF "a[32:35],v[20:23],v[84:87],a[220:223]\n"
"s_add_u32 m0,s64,32512\n"
"v_exp_f32 v104,v104\n"
MF "a[28:31],v[20:23],v[80:83],a[220:223]\n"
LD "v7,s[32:35],s72 offen offset:256 nt lds\n"
MF "a[24:27],v[20:23],v[76:79],a[220:223]\n"
"v_exp_f32 v105,v105\n"
MF "a[20:23],v[20:23],v[72:75],a[220:223]\n"
"s_waitcnt lgkmcnt(4)\n"
TR "v[20:21],v3 offset:1024\n"
TR "v[22:23],v3 offset:7168\n"
MF "a[40:43],v[24:27],v[72:75],a[220:223]\n"
"v_exp_f32 v106,v106\n"
MF "a[44:47],v[24:27],v[76:79],a[220:223]\n"
"v_exp_f32 v107,v107\n"
MF "a[48:51],v[24:27],v[80:83],a[220:223]\n"
"v_add_f32 v60,v55,v104\n"
"v_add_f32 v60,v60,v105\n"
MF "a[52:55],v[24:27],v[84:87],a[220:223]\n"
CV "v104,v104,v105\n"
"v_add_f32 v60,v60,v106\n"
MF "a[56:59],v[24:27],v[88:91],a[220:223]\n"
"s_waitcnt lgkmcnt(4)\n"
TR "v[24:25],v4 offset:1024\n"
TR "v[26:27],v4 offset:7168\n"
MF "a[76:79],v[28:31],v[88:91],a[220:223]\n"
"v_add_f32 v60,v60,v107\n"
CV "v105,v106,v107\n"
MF "a[72:75],v[28:31],v[84:87],a[220:223]\n"
"v_exp_f32 v108,v108\n"
MF "a[68:71],v[28:31],v[80:83],a[220:223]\n"
"v_exp_f32 v109,v109\n"
MF "a[64:67],v[28:31],v[76:79],a[220:223]\n"
"v_exp_f32 v110,v110\n"
MF "a[60:63],v[28:31],v[72:75],a[220:223]\n"
"s_waitcnt lgkmcnt(4)\n"
TR "v[28:29],v5 offset:1024\n"
TR "v[30:31],v5 offset:7168\n"
MF "a[80:83],v[16:19],v[72:75],a[220:223]\n"
"v_exp_f32 v111,v111\n"
MF "a[84:87],v[16:19],v[76:79],a[220:223]\n"
"v_add_f32 v61,v56,v108\n"
"v_add_f32 v61,v61,v109\n"
MF "a[88:91],v[16:19],v[80:83],a[220:223]\n"
CV "v108,v108,v109\n"
"v_add_f32 v61,v61,v110\n"
MF "a[92:95],v[16:19],v[84:87],a[220:223]\n"
"v_add_f32 v61,v61,v111\n"
CV "v109,v110,v111\n"
MF "a[96:99],v[16:19],v[88:91],a[220:223]\n"
"s_waitcnt lgkmcnt(4)\n"
TR "v[16:17],v2 offset:2048\n"
TR "v[18:19],v2 offset:8192\n"
MF "a[116:119],v[20:23],v[88:91],a[220:223]\n"
"v_exp_f32 v112,v112\n"
MF "a[112:115],v[20:23],v[84:87],a[220:223]\n"
"v_exp_f32 v113,v113\n"
MF "a[108:111],v[20:23],v[80:83],a[220:223]\n"
"v_exp_f32 v114,v114\n"
MF "a[104:107],v[20:23],v[76:79],a[220:223]\n"
"v_exp_f32 v115,v115\n"
MF "a[100:103],v[20:23],v[72:75],a[220:223]\n"
"s_waitcnt lgkmcnt(4)\n"
TR "v[20:21],v3 offset:2048\n"
TR "v[22:23],v3 offset:8192\n"
MF "a[120:123],v[24:27],v[72:75],a[220:223]\n"
"v_add_f32 v57,v57,v112\n"
"v_add_f32 v57,v57,v113\n"
MF "a[124:127],v[24:27],v[76:79],a[220:223]\n"
CV "v94,v112,v113\n"
"v_add_f32 v57,v57,v114\n"
MF "a[128:131],v[24:27],v[80:83],a[220:223]\n"
"v_add_f32 v57,v57,v115\n"
CV "v95,v114,v115\n"
MF "a[132:135],v[24:27],v[84:87],a[220:223]\n"
"v_exp_f32 v116,v116\n"
MF "a[136:139],v[24:27],v[88:91],a[220:223]\n"
"s_waitcnt lgkmcnt(4)\n"
TR "v[24:25],v4 offset:2048\n"
TR "v[26:27],v4 offset:8192\n"
MF "a[156:159],v[28:31],v[88:91],a[220:223]\n"
"v_exp_f32 v117,v117\n"
MF "a[152:155],v[28:31],v[84:87],a[220:223]\n"
"v_exp_f32 v118,v118\n"
MF "a[148:151],v[28:31],v[80:83],a[220:223]\n"
"v_exp_f32 v119,v119\n"
MF "a[144:147],v[28:31],v[76:79],a[220:223]\n"
"v_add_f32 v58,v58,v116\n"
"v_add_f32 v58,v58,v117\n"
MF "a[140:143],v[28:31],v[72:75],a[220:223]\n"
"s_waitcnt lgkmcnt(4)\n"
TR "v[28:29],v5 offset:2048\n"
TR "v[30:31],v5 offset:8192\n"
MF "a[160:163],v[16:19],v[72:75],a[220:223]\n"
CV "v98,v116,v117\n"
"v_add_f32 v58,v58,v118\n"
MF "a[164:167],v[16:19],v[76:79],a[220:223]\n"
"v_add_f32 v58,v58,v119\n"
CV "v99,v118,v119\n"
MF "a[168:171],v[16:19],v[80:83],a[220:223]\n"
"v_exp_f32 v120,v120\n"
MF "a[172:175],v[16:19],v[84:87],a[220:223]\n"
"v_exp_f32 v121,v121\n"
MF "a[176:179],v[16:19],v[88:91],a[220:223]\n"
"s_waitcnt lgkmcnt(4)\n"
"v_exp_f32 v122,v122\n"
MF "a[196:199],v[20:23],v[88:91],a[220:223]\n"
"v_exp_f32 v123,v123\n"
MF "a[192:195],v[20:23],v[84:87],a[220:223]\n"
"v_add_f32 v59,v59,v120\n"
"v_add_f32 v59,v59,v121\n"
MF "a[188:191],v[20:23],v[80:83],a[220:223]\n"
CV "v102,v120,v121\n"
"v_add_f32 v59,v59,v122\n"
MF "a[184:187],v[20:23],v[76:79],a[220:223]\n"
"s_waitcnt vmcnt(6) lgkmcnt(0)\n"
"s_barrier\n"
MF "a[180:183],v[20:23],v[72:75],a[220:223]\n"
"s_add_u32 s73,s73,12288\n"
"ds_read_b128 v[16:19],v0\n"
"ds_read_b128 v[20:23],v1\n"
MF "a[200:203],v[24:27],v[72:75],a[220:223]\n"
"s_cmp_eq_u32 s73,s19\n"
"s_cselect_b32 s73,s18,s73\n"
"s_add_u32 m0,s65,49152\n"
MF "a[204:207],v[24:27],v[76:79],a[220:223]\n"
LD "v8,s[36:39],s73 offen nt lds\n"
MF "a[208:211],v[24:27],v[80:83],a[220:223]\n"
"s_add_u32 m0,s65,50048\n"
"v_add_f32 v59,v59,v123\n"
CV "v103,v122,v123\n"
MF "a[212:215],v[24:27],v[84:87],a[220:223]\n"
LD "v8,s[36:39],s73 offen offset:128 nt lds\n"
MF "a[216:219],v[24:27],v[88:91],a[220:223]\n"
"s_add_u32 m0,s65,50944\n"
"ds_read_b128 v[24:27],v0 offset:1024\n"
MF "a[236:239],v[28:31],v[88:91],a[220:223]\n"
LD "v8,s[36:39],s73 offen offset:256 nt lds\n"
MF "a[232:235],v[28:31],v[84:87],a[220:223]\n"
MF "a[228:231],v[28:31],v[80:83],a[220:223]\n"
MF "a[224:227],v[28:31],v[76:79],a[220:223]\n"
MF "a[220:223],v[28:31],v[72:75],a[220:223]\n"
"s_waitcnt lgkmcnt(1)\n"
".Laloopx:\n"
MF "v[72:75],v[16:19],v[136:139],v[32:35]\n"
"ds_read_b128 v[28:31],v1 offset:1024\n"
MF "v[72:75],v[20:23],v[156:159],v[72:75]\n"
MF "v[76:79],v[20:23],v[160:163],v[36:39]\n"
MF "v[76:79],v[16:19],v[140:143],v[76:79]\n"
MF "v[80:83],v[16:19],v[144:147],v[40:43]\n"
MF "v[80:83],v[20:23],v[164:167],v[80:83]\n"
MF "v[84:87],v[20:23],v[168:171],v[44:47]\n"
MF "v[84:87],v[16:19],v[148:151],v[84:87]\n"
"s_add_u32 m0,s65,49152\n"
MF "v[88:91],v[16:19],v[152:155],v[48:51]\n"
LD "v8,s[36:39],s73 offen offset:3072 nt lds\n"
MF "v[88:91],v[20:23],v[172:175],v[88:91]\n"
"s_waitcnt lgkmcnt(0)\n"
"ds_read_b128 v[16:19],v0 offset:2048\n"
"ds_read_b128 v[20:23],v1 offset:2048\n"
MF "v[88:91],v[24:27],v[192:195],v[88:91]\n"
"v_exp_f32 v124,v124\n"
MF "v[88:91],v[28:31],v[212:215],v[88:91]\n"
"s_add_u32 m0,s65,50048\n"
"v_exp_f32 v125,v125\n"
MF "v[84:87],v[28:31],v[208:211],v[84:87]\n"
LD "v8,s[36:39],s73 offen offset:3200 nt lds\n"
MF "v[84:87],v[24:27],v[188:191],v[84:87]\n"
"v_exp_f32 v126,v126\n"
MF "v[80:83],v[24:27],v[184:187],v[80:83]\n"
"v_exp_f32 v127,v127\n"
MF "v[80:83],v[28:31],v[204:207],v[80:83]\n"
"s_add_u32 m0,s65,50944\n"
"v_add_f32 v60,v60,v124\n"
"v_add_f32 v60,v60,v125\n"
MF "v[76:79],v[28:31],v[200:203],v[76:79]\n"
LD "v8,s[36:39],s73 offen offset:3328 nt lds\n"
MF "v[76:79],v[24:27],v[180:183],v[76:79]\n"
CV "v106,v124,v125\n"
"v_add_f32 v60,v60,v126\n"
MF "v[72:75],v[24:27],v[176:179],v[72:75]\n"
"v_add_f32 v60,v60,v127\n"
CV "v107,v126,v127\n"
MF "v[72:75],v[28:31],v[196:199],v[72:75]\n"
"s_waitcnt lgkmcnt(0)\n"
"ds_read_b128 v[24:27],v0 offset:6144\n"
"ds_read_b128 v[28:31],v1 offset:6144\n"
MF "v[72:75],v[16:19],v[216:219],v[72:75]\n"
"v_exp_f32 v128,v128\n"
MF "v[72:75],v[20:23],v[236:239],v[72:75]\n"
"v_exp_f32 v129,v129\n"
MF "v[76:79],v[20:23],v[240:243],v[76:79]\n"
"v_exp_f32 v130,v130\n"
MF "v[76:79],v[16:19],v[220:223],v[76:79]\n"
"v_exp_f32 v131,v131\n"
MF "v[80:83],v[16:19],v[224:227],v[80:83]\n"
"v_add_f32 v61,v61,v128\n"
"v_add_f32 v61,v61,v129\n"
MF "v[80:83],v[20:23],v[244:247],v[80:83]\n"
CV "v110,v128,v129\n"
"v_add_f32 v61,v61,v130\n"
MF "v[84:87],v[20:23],v[248:251],v[84:87]\n"
"v_add_f32 v61,v61,v131\n"
CV "v111,v130,v131\n"
MF "v[84:87],v[16:19],v[228:231],v[84:87]\n"
"v_max3_f32 v10,v57,v58,v59\n"
"v_max3_f32 v10,v10,v60,v61\n"
MF "v[88:91],v[16:19],v[232:235],v[88:91]\n"
"v_cmp_ngt_f32 vcc,s95,v10\n"
MF "v[88:91],v[20:23],v[252:255],v[88:91]\n"
"s_waitcnt lgkmcnt(0)\n"
"s_cbranch_vccnz .Lstub_6\n"
".Lback_6:\n"
MF "v[112:115],v[24:27],v[136:139],v[32:35]\n"
"s_add_u32 s74,s74,12288\n"
"ds_read_b128 v[16:19],v0 offset:7168\n"
"ds_read_b128 v[20:23],v1 offset:7168\n"
MF "v[112:115],v[28:31],v[156:159],v[112:115]\n"
"s_cmp_eq_u32 s74,s20\n"
"s_cselect_b32 s74,s70,s74\n"
MF "v[116:119],v[28:31],v[160:163],v[36:39]\n"
"v_exp_f32 v72,v72\n"
MF "v[116:119],v[24:27],v[140:143],v[116:119]\n"
"v_exp_f32 v73,v73\n"
MF "v[120:123],v[24:27],v[144:147],v[40:43]\n"
"v_exp_f32 v74,v74\n"
MF "v[120:123],v[28:31],v[164:167],v[120:123]\n"
"v_exp_f32 v75,v75\n"
MF "v[124:127],v[28:31],v[168:171],v[44:47]\n"
"v_add_f32 v52,v57,v72\n"
"v_add_f32 v52,v52,v73\n"
MF "v[124:127],v[24:27],v[148:151],v[124:127]\n"
CV "v72,v72,v73\n"
"v_add_f32 v52,v52,v74\n"
MF "v[128:131],v[24:27],v[152:155],v[48:51]\n"
"v_add_f32 v52,v52,v75\n"
CV "v73,v74,v75\n"
MF "v[128:131],v[28:31],v[172:175],v[128:131]\n"
"s_waitcnt lgkmcnt(0)\n"
"ds_read_b128 v[24:27],v0 offset:8192\n"
"ds_read_b128 v[28:31],v1 offset:8192\n"
MF "v[128:131],v[16:19],v[192:195],v[128:131]\n"
"v_exp_f32 v76,v76\n"
MF "v[128:131],v[20:23],v[212:215],v[128:131]\n"
"v_exp_f32 v77,v77\n"
MF "v[124:127],v[20:23],v[208:211],v[124:127]\n"
"v_exp_f32 v78,v78\n"
MF "v[124:127],v[16:19],v[188:191],v[124:127]\n"
"v_exp_f32 v79,v79\n"
MF "v[120:123],v[16:19],v[184:187],v[120:123]\n"
"v_add_f32 v53,v58,v76\n"
"v_add_f32 v53,v53,v77\n"
MF "v[120:123],v[20:23],v[204:207],v[120:123]\n"
CV "v76,v76,v77\n"
"v_add_f32 v53,v53,v78\n"
MF "v[116:119],v[20:23],v[200:203],v[116:119]\n"
"v_add_f32 v53,v53,v79\n"
CV "v77,v78,v79\n"
MF "v[116:119],v[16:19],v[180:183],v[116:119]\n"
"s_waitcnt vmcnt(12) lgkmcnt(0)\n"
"s_barrier\n"
MF "v[112:115],v[16:19],v[176:179],v[112:115]\n"
"v_exp_f32 v80,v80\n"
MF "v[112:115],v[20:23],v[196:199],v[112:115]\n"
"s_nop 0\n"
TR "v[16:17],v2 offset:24576\n"
TR "v[18:19],v2 offset:30720\n"
MF "v[112:115],v[24:27],v[216:219],v[112:115]\n"
"s_add_u32 s72,s72,6144\n"
TR "v[20:21],v3 offset:24576\n"
TR "v[22:23],v3 offset:30720\n"
MF "v[112:115],v[28:31],v[236:239],v[112:115]\n"
"s_cmp_eq_u32 s72,s17\n"
"s_cselect_b32 s72,s16,s72\n"
"s_add_u32 m0,s64,0\n"
MF "v[116:119],v[28:31],v[240:243],v[116:119]\n"
LD "v7,s[32:35],s72 offen nt lds\n"
MF "v[116:119],v[24:27],v[220:223],v[116:119]\n"
"s_add_u32 m0,s64,896\n"
"v_exp_f32 v81,v81\n"
MF "v[120:123],v[24:27],v[224:227],v[120:123]\n"
LD "v7,s[32:35],s72 offen offset:128 nt lds\n"
MF "v[120:123],v[28:31],v[244:247],v[120:123]\n"
"v_exp_f32 v82,v82\n"
MF "v[124:127],v[28:31],v[248:251],v[124:127]\n"
"s_add_u32 m0,s64,1792\n"
"v_exp_f32 v83,v83\n"
MF "v[124:127],v[24:27],v[228:231],v[124:127]\n"
LD "v7,s[32:35],s72 offen offset:256 nt lds\n"
MF "v[128:131],v[24:27],v[232:235],v[128:131]\n"
"s_add_u32 s72,s72,6144\n"
"v_add_f32 v54,v59,v80\n"
"v_add_f32 v54,v54,v81\n"
MF "v[128:131],v[28:31],v[252:255],v[128:131]\n"
"s_waitcnt lgkmcnt(2)\n"
TR "v[24:25],v4 offset:24576\n"
TR "v[26:27],v4 offset:30720\n"
MF "a[0:3],v[16:19],v[92:95],a[0:3]\n"
"s_add_u32 m0,s64,6144\n"
TR "v[28:29],v5 offset:24576\n"
TR "v[30:31],v5 offset:30720\n"
MF "a[4:7],v[16:19],v[96:99],a[4:7]\n"
LD "v7,s[32:35],s72 offen nt lds\n"
MF "a[8:11],v[16:19],v[100:103],a[8:11]\n"
"s_add_u32 m0,s64,7040\n"
CV "v80,v80,v81\n"
"v_add_f32 v54,v54,v82\n"
MF "a[12:15],v[16:19],v[104:107],a[12:15]\n"
LD "v7,s[32:35],s72 offen offset:128 nt lds\n"
MF "a[16:19],v[16:19],v[108:111],a[16:19]\n"
"s_waitcnt lgkmcnt(4)\n"
TR "v[16:17],v2 offset:25600\n"
TR "v[18:19],v2 offset:31744\n"
MF "a[36:39],v[20:23],v[108:111],a[36:39]\n"
"v_add_f32 v54,v54,v83\n"
CV "v81,v82,v83\n"
MF "a[32:35],v[20:23],v[104:107],a[32:35]\n"
"s_add_u32 m0,s64,7936\n"
"v_exp_f32 v84,v84\n"
MF "a[28:31],v[20:23],v[100:103],a[28:31]\n"
LD "v7,s[32:35],s72 offen offset:256 nt lds\n"
MF "a[24:27],v[20:23],v[96:99],a[24:27]\n"
"v_exp_f32 v85,v85\n"
MF "a[20:23],v[20:23],v[92:95],a[20:23]\n"
"s_waitcnt lgkmcnt(4)\n"
TR "v[20:21],v3 offset:25600\n"
TR "v[22:23],v3 offset:31744\n"
MF "a[40:43],v[24:27],v[92:95],a[40:43]\n"
"v_exp_f32 v86,v86\n"
MF "a[44:47],v[24:27],v[96:99],a[44:47]\n"
"v_exp_f32 v87,v87\n"
MF "a[48:51],v[24:27],v[100:103],a[48:51]\n"
"v_add_f32 v55,v60,v84\n"
"v_add_f32 v55,v55,v85\n"
MF "a[52:55],v[24:27],v[104:107],a[52:55]\n"
CV "v84,v84,v85\n"
"v_add_f32 v55,v55,v86\n"
MF "a[56:59],v[24:27],v[108:111],a[56:59]\n"
"s_waitcnt lgkmcnt(4)\n"
TR "v[24:25],v4 offset:25600\n"
TR "v[26:27],v4 offset:31744\n"
MF "a[76:79],v[28:31],v[108:111],a[76:79]\n"
"v_add_f32 v55,v55,v87\n"
CV "v85,v86,v87\n"
MF "a[72:75],v[28:31],v[104:107],a[72:75]\n"
"v_exp_f32 v88,v88\n"
MF "a[68:71],v[28:31],v[100:103],a[68:71]\n"
"v_exp_f32 v89,v89\n"
MF "a[64:67],v[28:31],v[96:99],a[64:67]\n"
"v_exp_f32 v90,v90\n"
MF "a[60:63],v[28:31],v[92:95],a[60:63]\n"
"s_waitcnt lgkmcnt(4)\n"
TR "v[28:29],v5 offset:25600\n"
TR "v[30:31],v5 offset:31744\n"
MF "a[80:83],v[16:19],v[92:95],a[80:83]\n"
"v_exp_f32 v91,v91\n"
MF "a[84:87],v[16:19],v[96:99],a[84:87]\n"
"v_add_f32 v56,v61,v88\n"
"v_add_f32 v56,v56,v89\n"
MF "a[88:91],v[16:19],v[100:103],a[88:91]\n"
CV "v88,v88,v89\n"
"v_add_f32 v56,v56,v90\n"
MF "a[92:95],v[16:19],v[104:107],a[92:95]\n"
"v_add_f32 v56,v56,v91\n"
CV "v89,v90,v91\n"
MF "a[96:99],v[16:19],v[108:111],a[96:99]\n"
"s_waitcnt lgkmcnt(4)\n"
TR "v[16:17],v2 offset:26624\n"
TR "v[18:19],v2 offset:32768\n"
MF "a[116:119],v[20:23],v[108:111],a[116:119]\n"
"v_exp_f32 v112,v112\n"
MF "a[112:115],v[20:23],v[104:107],a[112:115]\n"
"v_exp_f32 v113,v113\n"
MF "a[108:111],v[20:23],v[100:103],a[108:111]\n"
"v_exp_f32 v114,v114\n"
MF "a[104:107],v[20:23],v[96:99],a[104:107]\n"
"v_exp_f32 v115,v115\n"
MF "a[100:103],v[20:23],v[92:95],a[100:103]\n"
"s_waitcnt lgkmcnt(4)\n"
TR "v[20:21],v3 offset:26624\n"
TR "v[22:23],v3 offset:32768\n"
MF "a[120:123],v[24:27],v[92:95],a[120:123]\n"
"v_add_f32 v52,v52,v112\n"
"v_add_f32 v52,v52,v113\n"
MF "a[124:127],v[24:27],v[96:99],a[124:127]\n"
CV "v74,v112,v113\n"
"v_add_f32 v52,v52,v114\n"
MF "a[128:131],v[24:27],v[100:103],a[128:131]\n"
"v_add_f32 v52,v52,v115\n"
CV "v75,v114,v115\n"
MF "a[132:135],v[24:27],v[104:107],a[132:135]\n"
"v_exp_f32 v116,v116\n"
MF "a[136:139],v[24:27],v[108:111],a[136:139]\n"
"s_waitcnt lgkmcnt(4)\n"
TR "v[24:25],v4 offset:26624\n"
TR "v[26:27],v4 offset:32768\n"
MF "a[156:159],v[28:31],v[108:111],a[156:159]\n"
"v_exp_f32 v117,v117\n"
MF "a[152:155],v[28:31],v[104:107],a[152:155]\n"
"v_exp_f32 v118,v118\n"
MF "a[148:151],v[28:31],v[100:103],a[148:151]\n"
"v_exp_f32 v119,v119\n"
MF "a[144:147],v[28:31],v[96:99],a[144:147]\n"
"v_add_f32 v53,v53,v116\n"
"v_add_f32 v53,v53,v117\n"
MF "a[140:143],v[28:31],v[92:95],a[140:143]\n"
"s_waitcnt lgkmcnt(4)\n"
TR "v[28:29],v5 offset:26624\n"
TR "v[30:31],v5 offset:32768\n"
MF "a[160:163],v[16:19],v[92:95],a[160:163]\n"
CV "v78,v116,v117\n"
"v_add_f32 v53,v53,v118\n"
MF "a[164:167],v[16:19],v[96:99],a[164:167]\n"
"v_add_f32 v53,v53,v119\n"
CV "v79,v118,v119\n"
MF "a[168:171],v[16:19],v[100:103],a[168:171]\n"
"v_exp_f32 v120,v120\n"
MF "a[172:175],v[16:19],v[104:107],a[172:175]\n"
"v_exp_f32 v121,v121\n"
MF "a[176:179],v[16:19],v[108:111],a[176:179]\n"
"s_waitcnt lgkmcnt(4)\n"
"v_exp_f32 v122,v122\n"
MF "a[196:199],v[20:23],v[108:111],a[196:199]\n"
"v_exp_f32 v123,v123\n"
MF "a[192:195],v[20:23],v[104:107],a[192:195]\n"
"v_add_f32 v54,v54,v120\n"
"v_add_f32 v54,v54,v121\n"
MF "a[188:191],v[20:23],v[100:103],a[188:191]\n"
CV "v82,v120,v121\n"
"v_add_f32 v54,v54,v122\n"
MF "a[184:187],v[20:23],v[96:99],a[184:187]\n"
"s_waitcnt vmcnt(12) lgkmcnt(0)\n"
"s_barrier\n"
MF "a[180:183],v[20:23],v[92:95],a[180:183]\n"
"s_add_u32 s73,s73,12288\n"
"ds_read_b128 v[16:19],v0 offset:24576\n"
"ds_read_b128 v[20:23],v1 offset:24576\n"
MF "a[200:203],v[24:27],v[92:95],a[200:203]\n"
"s_cmp_eq_u32 s73,s19\n"
"s_cselect_b32 s73,s18,s73\n"
"s_add_u32 m0,s65,73728\n"
MF "a[204:207],v[24:27],v[96:99],a[204:207]\n"
LD "v8,s[36:39],s73 offen nt lds\n"
MF "a[208:211],v[24:27],v[100:103],a[208:211]\n"
"s_add_u32 m0,s65,74624\n"
"v_add_f32 v54,v54,v123\n"
CV "v83,v122,v123\n"
MF "a[212:215],v[24:27],v[104:107],a[212:215]\n"
LD "v8,s[36:39],s73 offen offset:128 nt lds\n"
MF "a[216:219],v[24:27],v[108:111],a[216:219]\n"
"s_add_u32 m0,s65,75520\n"
"ds_read_b128 v[24:27],v0 offset:25600\n"
MF "a[236:239],v[28:31],v[108:111],a[236:239]\n"
LD "v8,s[36:39],s73 offen offset:256 nt lds\n"
MF "a[232:235],v[28:31],v[104:107],a[232:235]\n"
MF "a[228:231],v[28:31],v[100:103],a[228:231]\n"
MF "a[224:227],v[28:31],v[96:99],a[224:227]\n"
MF "a[220:223],v[28:31],v[92:95],a[220:223]\n"
"s_waitcnt lgkmcnt(1)\n"
MF "v[92:95],v[16:19],v[136:139],v[32:35]\n"
"s_add_u32 m0,s65,73728\n"
"ds_read_b128 v[28:31],v1 offset:25600\n"
MF "v[92:95],v[20:23],v[156:159],v[92:95]\n"
LD "v8,s[36:39],s73 offen offset:3072 nt lds\n"
MF "v[96:99],v[20:23],v[160:163],v[36:39]\n"
"v_exp_f32 v124,v124\n"
MF "v[96:99],v[16:19],v[140:143],v[96:99]\n"
"v_exp_f32 v125,v125\n"
MF "v[100:103],v[16:19],v[144:147],v[40:43]\n"
"s_add_u32 m0,s65,74624\n"
"v_exp_f32 v126,v126\n"
MF "v[100:103],v[20:23],v[164:167],v[100:103]\n"
LD "v8,s[36:39],s73 offen offset:3200 nt lds\n"
MF "v[104:107],v[20:23],v[168:171],v[44:47]\n"
"v_exp_f32 v127,v127\n"
MF "v[104:107],v[16:19],v[148:151],v[104:107]\n"
"s_add_u32 m0,s65,75520\n"
"v_add_f32 v55,v55,v124\n"
"v_add_f32 v55,v55,v125\n"
MF "v[108:111],v[16:19],v[152:155],v[48:51]\n"
LD "v8,s[36:39],s73 offen offset:3328 nt lds\n"
MF "v[108:111],v[20:23],v[172:175],v[108:111]\n"
"s_waitcnt lgkmcnt(0)\n"
"ds_read_b128 v[16:19],v0 offset:26624\n"
"ds_read_b128 v[20:23],v1 offset:26624\n"
MF "v[108:111],v[24:27],v[192:195],v[108:111]\n"
CV "v86,v124,v125\n"
"v_add_f32 v55,v55,v126\n"
MF "v[108:111],v[28:31],v[212:215],v[108:111]\n"
"v_add_f32 v55,v55,v127\n"
CV "v87,v126,v127\n"
MF "v[104:107],v[28:31],v[208:211],v[104:107]\n"
"v_exp_f32 v128,v128\n"
MF "v[104:107],v[24:27],v[188:191],v[104:107]\n"
"v_exp_f32 v129,v129\n"
MF "v[100:103],v[24:27],v[184:187],v[100:103]\n"
"v_exp_f32 v130,v130\n"
MF "v[100:103],v[28:31],v[204:207],v[100:103]\n"
"v_exp_f32 v131,v131\n"
MF "v[96:99],v[28:31],v[200:203],v[96:99]\n"
"v_add_f32 v56,v56,v128\n"
"v_add_f32 v56,v56,v129\n"
MF "v[96:99],v[24:27],v[180:183],v[96:99]\n"
CV "v90,v128,v129\n"
"v_add_f32 v56,v56,v130\n"
MF "v[92:95],v[24:27],v[176:179],v[92:95]\n"
"v_add_f32 v56,v56,v131\n"
CV "v91,v130,v131\n"
MF "v[92:95],v[28:31],v[196:199],v[92:95]\n"
"s_waitcnt lgkmcnt(0)\n"
"ds_read_b128 v[24:27],v0 offset:30720\n"
"ds_read_b128 v[28:31],v1 offset:30720\n"
MF "v[92:95],v[16:19],v[216:219],v[92:95]\n"
MF "v[92:95],v[20:23],v[236:239],v[92:95]\n"
MF "v[96:99],v[20:23],v[240:243],v[96:99]\n"
MF "v[96:99],v[16:19],v[220:223],v[96:99]\n"
MF "v[100:103],v[16:19],v[224:227],v[100:103]\n"
MF "v[100:103],v[20:23],v[244:247],v[100:103]\n"
MF "v[104:107],v[20:23],v[248:251],v[104:107]\n"
MF "v[104:107],v[16:19],v[228:231],v[104:107]\n"
"v_max3_f32 v10,v52,v53,v54\n"
"v_max3_f32 v10,v10,v55,v56\n"
MF "v[108:111],v[16:19],v[232:235],v[108:111]\n"
"v_cmp_ngt_f32 vcc,s95,v10\n"
MF "v[108:111],v[20:23],v[252:255],v[108:111]\n"
"s_waitcnt lgkmcnt(0)\n"
"s_cbranch_vccnz .Lstub_7\n"
".Lback_7:\n"
MF "v[112:115],v[24:27],v[136:139],v[32:35]\n"
"s_add_u32 s74,s74,12288\n"
"ds_read_b128 v[16:19],v0 offset:31744\n"
"ds_read_b128 v[20:23],v1 offset:31744\n"
MF "v[112:115],v[28:31],v[156:159],v[112:115]\n"
"s_cmp_eq_u32 s74,s20\n"
"s_cselect_b32 s74,s70,s74\n"
MF "v[116:119],v[28:31],v[160:163],v[36:39]\n"
"v_exp_f32 v92,v92\n"
MF "v[116:119],v[24:27],v[140:143],v[116:119]\n"
"v_exp_f32 v93,v93\n"
MF "v[120:123],v[24:27],v[144:147],v[40:43]\n"
"v_exp_f32 v94,v94\n"
MF "v[120:123],v[28:31],v[164:167],v[120:123]\n"
"v_exp_f32 v95,v95\n"
MF "v[124:127],v[28:31],v[168:171],v[44:47]\n"
"v_add_f32 v57,v52,v92\n"
"v_add_f32 v57,v57,v93\n"
MF "v[124:127],v[24:27],v[148:151],v[124:127]\n"
CV "v92,v92,v93\n"
"v_add_f32 v57,v57,v94\n"
MF "v[128:131],v[24:27],v[152:155],v[48:51]\n"
"v_add_f32 v57,v57,v95\n"
CV "v93,v94,v95\n"
MF "v[128:131],v[28:31],v[172:175],v[128:131]\n"
"s_waitcnt lgkmcnt(0)\n"
"ds_read_b128 v[24:27],v0 offset:32768\n"
"ds_read_b128 v[28:31],v1 offset:32768\n"
MF "v[128:131],v[16:19],v[192:195],v[128:131]\n"
"s_sub_u32 s61,s61,1\n"
"v_exp_f32 v96,v96\n"
MF "v[128:131],v[20:23],v[212:215],v[128:131]\n"
"s_cmp_eq_u32 s61,1\n"
"s_cselect_b32 s95,0,s95\n"
"v_add_f32 v58,v53,v96\n"
MF "v[124:127],v[20:23],v[208:211],v[124:127]\n"
"v_exp_f32 v97,v97\n"
MF "v[124:127],v[16:19],v[188:191],v[124:127]\n"
"v_exp_f32 v98,v98\n"
MF "v[120:123],v[16:19],v[184:187],v[120:123]\n"
"v_exp_f32 v99,v99\n"
MF "v[120:123],v[20:23],v[204:207],v[120:123]\n"
"v_add_f32 v58,v58,v97\n"
CV "v96,v96,v97\n"
MF "v[116:119],v[20:23],v[200:203],v[116:119]\n"
"v_add_f32 v58,v58,v98\n"
"v_add_f32 v58,v58,v99\n"
MF "v[116:119],v[16:19],v[180:183],v[116:119]\n"
"s_waitcnt vmcnt(12) lgkmcnt(0)\n"
"s_barrier\n"
MF "v[112:115],v[16:19],v[176:179],v[112:115]\n"
CV "v97,v98,v99\n"
MF "v[112:115],v[20:23],v[196:199],v[112:115]\n"
"s_nop 0\n"
TR "v[16:17],v2\n"
TR "v[18:19],v2 offset:6144\n"
MF "v[112:115],v[24:27],v[216:219],v[112:115]\n"
"s_add_u32 s72,s72,6144\n"
TR "v[20:21],v3\n"
TR "v[22:23],v3 offset:6144\n"
MF "v[112:115],v[28:31],v[236:239],v[112:115]\n"
"s_cmp_eq_u32 s72,s17\n"
"s_cselect_b32 s72,s16,s72\n"
"s_add_u32 m0,s64,24576\n"
MF "v[116:119],v[28:31],v[240:243],v[116:119]\n"
LD "v7,s[32:35],s72 offen nt lds\n"
MF "v[116:119],v[24:27],v[220:223],v[116:119]\n"
"s_add_u32 m0,s64,25472\n"
"v_exp_f32 v100,v100\n"
MF "v[120:123],v[24:27],v[224:227],v[120:123]\n"
LD "v7,s[32:35],s72 offen offset:128 nt lds\n"
MF "v[120:123],v[28:31],v[244:247],v[120:123]\n"
"v_exp_f32 v101,v101\n"
MF "v[124:127],v[28:31],v[248:251],v[124:127]\n"
"s_add_u32 m0,s64,26368\n"
"v_exp_f32 v102,v102\n"
MF "v[124:127],v[24:27],v[228:231],v[124:127]\n"
LD "v7,s[32:35],s72 offen offset:256 nt lds\n"
MF "v[128:131],v[24:27],v[232:235],v[128:131]\n"
"s_add_u32 s72,s72,6144\n"
"v_exp_f32 v103,v103\n"
MF "v[128:131],v[28:31],v[252:255],v[128:131]\n"
"s_waitcnt lgkmcnt(2)\n"
TR "v[24:25],v4\n"
TR "v[26:27],v4 offset:6144\n"
MF "a[0:3],v[16:19],v[72:75],a[0:3]\n"
"s_add_u32 m0,s64,30720\n"
TR "v[28:29],v5\n"
TR "v[30:31],v5 offset:6144\n"
MF "a[4:7],v[16:19],v[76:79],a[4:7]\n"
LD "v7,s[32:35],s72 offen nt lds\n"
MF "a[8:11],v[16:19],v[80:83],a[8:11]\n"
"s_add_u32 m0,s64,31616\n"
"v_add_f32 v59,v54,v100\n"
"v_add_f32 v59,v59,v101\n"
MF "a[12:15],v[16:19],v[84:87],a[12:15]\n"
LD "v7,s[32:35],s72 offen offset:128 nt lds\n"
MF "a[16:19],v[16:19],v[88:91],a[16:19]\n"
"s_waitcnt lgkmcnt(4)\n"
TR "v[16:17],v2 offset:1024\n"
TR "v[18:19],v2 offset:7168\n"
MF "a[36:39],v[20:23],v[88:91],a[36:39]\n"
CV "v100,v100,v101\n"
"v_add_f32 v59,v59,v102\n"
MF "a[32:35],v[20:23],v[84:87],a[32:35]\n"
"s_add_u32 m0,s64,32512\n"
"v_add_f32 v59,v59,v103\n"
CV "v101,v102,v103\n"
MF "a[28:31],v[20:23],v[80:83],a[28:31]\n"
LD "v7,s[32:35],s72 offen offset:256 nt lds\n"
MF "a[24:27],v[20:23],v[76:79],a[24:27]\n"
"v_exp_f32 v104,v104\n"
MF "a[20:23],v[20:23],v[72:75],a[20:23]\n"
"s_waitcnt lgkmcnt(4)\n"
TR "v[20:21],v3 offset:1024\n"
TR "v[22:23],v3 offset:7168\n"
MF "a[40:43],v[24:27],v[72:75],a[40:43]\n"
"v_exp_f32 v105,v105\n"
MF "a[44:47],v[24:27],v[76:79],a[44:47]\n"
"v_exp_f32 v106,v106\n"
MF "a[48:51],v[24:27],v[80:83],a[48:51]\n"
"v_exp_f32 v107,v107\n"
MF "a[52:55],v[24:27],v[84:87],a[52:55]\n"
"v_add_f32 v60,v55,v104\n"
"v_add_f32 v60,v60,v105\n"
MF "a[56:59],v[24:27],v[88:91],a[56:59]\n"
"s_waitcnt lgkmcnt(4)\n"
TR "v[24:25],v4 offset:1024\n"
TR "v[26:27],v4 offset:7168\n"
MF "a[76:79],v[28:31],v[88:91],a[76:79]\n"
CV "v104,v104,v105\n"
"v_add_f32 v60,v60,v106\n"
MF "a[72:75],v[28:31],v[84:87],a[72:75]\n"
"v_add_f32 v60,v60,v107\n"
CV "v105,v106,v107\n"
MF "a[68:71],v[28:31],v[80:83],a[68:71]\n"
"v_exp_f32 v108,v108\n"
MF "a[64:67],v[28:31],v[76:79],a[64:67]\n"
"v_exp_f32 v109,v109\n"
MF "a[60:63],v[28:31],v[72:75],a[60:63]\n"
"s_waitcnt lgkmcnt(4)\n"
TR "v[28:29],v5 offset:1024\n"
TR "v[30:31],v5 offset:7168\n"
MF "a[80:83],v[16:19],v[72:75],a[80:83]\n"
"v_exp_f32 v110,v110\n"
MF "a[84:87],v[16:19],v[76:79],a[84:87]\n"
"v_exp_f32 v111,v111\n"
MF "a[88:91],v[16:19],v[80:83],a[88:91]\n"
"v_add_f32 v61,v56,v108\n"
"v_add_f32 v61,v61,v109\n"
MF "a[92:95],v[16:19],v[84:87],a[92:95]\n"
CV "v108,v108,v109\n"
"v_add_f32 v61,v61,v110\n"
MF "a[96:99],v[16:19],v[88:91],a[96:99]\n"
"s_waitcnt lgkmcnt(4)\n"
TR "v[16:17],v2 offset:2048\n"
TR "v[18:19],v2 offset:8192\n"
MF "a[116:119],v[20:23],v[88:91],a[116:119]\n"
"v_add_f32 v61,v61,v111\n"
CV "v109,v110,v111\n"
MF "a[112:115],v[20:23],v[84:87],a[112:115]\n"
"v_exp_f32 v112,v112\n"
MF "a[108:111],v[20:23],v[80:83],a[108:111]\n"
"v_exp_f32 v113,v113\n"
MF "a[104:107],v[20:23],v[76:79],a[104:107]\n"
"v_exp_f32 v114,v114\n"
MF "a[100:103],v[20:23],v[72:75],a[100:103]\n"
"s_waitcnt lgkmcnt(4)\n"
TR "v[20:21],v3 offset:2048\n"
TR "v[22:23],v3 offset:8192\n"
MF "a[120:123],v[24:27],v[72:75],a[120:123]\n"
"v_exp_f32 v115,v115\n"
MF "a[124:127],v[24:27],v[76:79],a[124:127]\n"
"v_add_f32 v57,v57,v112\n"
"v_add_f32 v57,v57,v113\n"
MF "a[128:131],v[24:27],v[80:83],a[128:131]\n"
CV "v94,v112,v113\n"
"v_add_f32 v57,v57,v114\n"
MF "a[132:135],v[24:27],v[84:87],a[132:135]\n"
"v_add_f32 v57,v57,v115\n"
CV "v95,v114,v115\n"
MF "a[136:139],v[24:27],v[88:91],a[136:139]\n"
"s_waitcnt lgkmcnt(4)\n"
TR "v[24:25],v4 offset:2048\n"
TR "v[26:27],v4 offset:8192\n"
MF "a[156:159],v[28:31],v[88:91],a[156:159]\n"
"v_exp_f32 v116,v116\n"
MF "a[152:155],v[28:31],v[84:87],a[152:155]\n"
"v_exp_f32 v117,v117\n"
MF "a[148:151],v[28:31],v[80:83],a[148:151]\n"
"v_exp_f32 v118,v118\n"
MF "a[144:147],v[28:31],v[76:79],a[144:147]\n"
"v_exp_f32 v119,v119\n"
MF "a[140:143],v[28:31],v[72:75],a[140:143]\n"
"s_waitcnt lgkmcnt(4)\n"
TR "v[28:29],v5 offset:2048\n"
TR "v[30:31],v5 offset:8192\n"
MF "a[160:163],v[16:19],v[72:75],a[160:163]\n"
"v_add_f32 v58,v58,v116\n"
"v_add_f32 v58,v58,v117\n"
MF "a[164:167],v[16:19],v[76:79],a[164:167]\n"
CV "v98,v116,v117\n"
"v_add_f32 v58,v58,v118\n"
MF "a[168:171],v[16:19],v[80:83],a[168:171]\n"
"v_add_f32 v58,v58,v119\n"
CV "v99,v118,v119\n"
MF "a[172:175],v[16:19],v[84:87],a[172:175]\n"
"v_exp_f32 v120,v120\n"
MF "a[176:179],v[16:19],v[88:91],a[176:179]\n"
"s_waitcnt lgkmcnt(4)\n"
"v_exp_f32 v121,v121\n"
MF "a[196:199],v[20:23],v[88:91],a[196:199]\n"
"v_exp_f32 v122,v122\n"
MF "a[192:195],v[20:23],v[84:87],a[192:195]\n"
"v_exp_f32 v123,v123\n"
MF "a[188:191],v[20:23],v[80:83],a[188:191]\n"
"v_add_f32 v59,v59,v120\n"
"v_add_f32 v59,v59,v121\n"
MF "a[184:187],v[20:23],v[76:79],a[184:187]\n"
"s_waitcnt vmcnt(12) lgkmcnt(0)\n"
"s_barrier\n"
MF "a[180:183],v[20:23],v[72:75],a[180:183]\n"
"s_add_u32 s73,s73,12288\n"
"ds_read_b128 v[16:19],v0\n"
"ds_read_b128 v[20:23],v1\n"
MF "a[200:203],v[24:27],v[72:75],a[200:203]\n"
"s_cmp_eq_u32 s73,s19\n"
"s_cselect_b32 s73,s18,s73\n"
"s_add_u32 m0,s65,49152\n"
MF "a[204:207],v[24:27],v[76:79],a[204:207]\n"
LD "v8,s[36:39],s73 offen nt lds\n"
MF "a[208:211],v[24:27],v[80:83],a[208:211]\n"
"s_add_u32 m0,s65,50048\n"
CV "v102,v120,v121\n"
"v_add_f32 v59,v59,v122\n"
MF "a[212:215],v[24:27],v[84:87],a[212:215]\n"
LD "v8,s[36:39],s73 offen offset:128 nt lds\n"
MF "a[216:219],v[24:27],v[88:91],a[216:219]\n"
"s_add_u32 m0,s65,50944\n"
"ds_read_b128 v[24:27],v0 offset:1024\n"
"v_add_f32 v59,v59,v123\n"
MF "a[236:239],v[28:31],v[88:91],a[236:239]\n"
LD "v8,s[36:39],s73 offen offset:256 nt lds\n"
MF "a[232:235],v[28:31],v[84:87],a[232:235]\n"
CV "v103,v122,v123\n"
MF "a[228:231],v[28:31],v[80:83],a[228:231]\n"
"s_waitcnt lgkmcnt(1)\n"
MF "a[224:227],v[28:31],v[76:79],a[224:227]\n"
"s_cmp_lg_u32 s61,0\n"
MF "a[220:223],v[28:31],v[72:75],a[220:223]\n"
"s_cbranch_scc1 .Laloopx\n"
"v_exp_f32 v124,v124\n"
"v_exp_f32 v125,v125\n"
"v_exp_f32 v126,v126\n"
"v_exp_f32 v127,v127\n"
"v_exp_f32 v128,v128\n"
"v_exp_f32 v129,v129\n"
"v_exp_f32 v130,v130\n"
"v_exp_f32 v131,v131\n"
"v_add_f32 v60,v60,v124\n"
"v_add_f32 v61,v61,v128\n"
"v_add_f32 v60,v60,v125\n"
"v_add_f32 v61,v61,v129\n"
"v_add_f32 v60,v60,v126\n"
"v_add_f32 v61,v61,v130\n"
"v_add_f32 v60,v60,v127\n"
"v_add_f32 v61,v61,v131\n"
CV "v106,v124,v125\n"
CV "v107,v126,v127\n"
CV "v110,v128,v129\n"
CV "v111,v130,v131\n"
"v_max3_f32 v10,v57,v58,v59\n"
"v_max3_f32 v10,v10,v60,v61\n"
"v_cmp_le_f32 vcc,0x47800000,v10\n"
"s_cbranch_vccnz .Lstub_8\n"
".Lback_8:\n"
"s_waitcnt vmcnt(9) lgkmcnt(0)\n"
"s_barrier\n"
"s_cmp_eq_u32 s57,0\n"
"s_cbranch_scc0 .Lep\n"
"s_nop 0\n"
"s_add_u32 s66,s67,4718592\n"
"s_sub_u32 s68,s68,384\n"
"s_mov_b32 s83,7077888\n"
AR "v32,a243\n"
"v_add_u32 v33,97280,v32\n"
"v_add_u32 v34,194560,v32\n"
"v_add_u32 v35,291840,v32\n"
TR "v[136:137],v2 offset:24576\n"
TR "v[138:139],v2 offset:30720\n"
TR "v[140:141],v3 offset:24576\n"
TR "v[142:143],v3 offset:30720\n"
TR "v[144:145],v4 offset:24576\n"
TR "v[146:147],v4 offset:30720\n"
TR "v[148:149],v5 offset:24576\n"
TR "v[150:151],v5 offset:30720\n"
TR "v[152:153],v2 offset:25600\n"
TR "v[154:155],v2 offset:31744\n"
TR "v[156:157],v3 offset:25600\n"
TR "v[158:159],v3 offset:31744\n"
TR "v[160:161],v4 offset:25600\n"
TR "v[162:163],v4 offset:31744\n"
TR "v[164:165],v5 offset:25600\n"
TR "v[166:167],v5 offset:31744\n"
TR "v[168:169],v2 offset:26624\n"
TR "v[170:171],v2 offset:32768\n"
TR "v[172:173],v3 offset:26624\n"
TR "v[174:175],v3 offset:32768\n"
TR "v[176:177],v4 offset:26624\n"
TR "v[178:179],v4 offset:32768\n"
TR "v[180:181],v5 offset:26624\n"
TR "v[182:183],v5 offset:32768\n"
"v_mov_b32 v52,v57\n"
"v_mov_b32 v53,v58\n"
"v_mov_b32 v54,v59\n"
"v_mov_b32 v55,v60\n"
"v_mov_b32 v56,v61\n"
"s_add_u32 m0,s62,0\n"
"s_add_u32 s82,s66,2359296\n"
LD "v32,s[28:31],s82 offen lds\n"
LD "v33,s[28:31],s82 offen offset:1024 lds\n"
"v_permlane16_swap_b32 v57,v52\n"
"v_permlane16_swap_b32 v58,v53\n"
"v_permlane16_swap_b32 v59,v54\n"
"v_permlane16_swap_b32 v60,v55\n"
"v_permlane16_swap_b32 v61,v56\n"
"v_add_f32 v57,v57,v52\n"
"v_add_f32 v58,v58,v53\n"
"v_add_f32 v59,v59,v54\n"
"v_add_f32 v60,v60,v55\n"
"v_add_f32 v61,v61,v56\n"
"v_mov_b32 v52,v57\n"
"v_mov_b32 v53,v58\n"
"v_mov_b32 v54,v59\n"
"v_mov_b32 v55,v60\n"
"v_mov_b32 v56,v61\n"
LD "v34,s[28:31],s82 offen offset:2048 lds\n"
LD "v35,s[28:31],s82 offen offset:3072 lds\n"
"v_permlane32_swap_b32 v57,v52\n"
"v_permlane32_swap_b32 v58,v53\n"
"v_permlane32_swap_b32 v59,v54\n"
"v_permlane32_swap_b32 v60,v55\n"
"v_permlane32_swap_b32 v61,v56\n"
"v_add_f32 v57,v57,v52\n"
"v_add_f32 v58,v58,v53\n"
"v_add_f32 v59,v59,v54\n"
"v_add_f32 v60,v60,v55\n"
"v_add_f32 v61,v61,v56\n"
"s_waitcnt lgkmcnt(15)\n"
MF "a[0:3],v[136:139],v[92:95],a[0:3]\n"
MF "a[20:23],v[140:143],v[92:95],a[20:23]\n"
"v_rcp_f32 v57,v57\n"
"v_rcp_f32 v58,v58\n"
"v_rcp_f32 v59,v59\n"
"v_rcp_f32 v60,v60\n"
"v_rcp_f32 v61,v61\n"
MF "a[4:7],v[136:139],v[96:99],a[4:7]\n"
"v_mul_f32_e64 v52,-s13,v57\n"
"v_mul_f32_e64 v53,-s13,v58\n"
"v_mul_f32_e64 v54,-s13,v59\n"
"v_mul_f32_e64 v55,-s13,v60\n"
"v_mul_f32_e64 v56,-s13,v61\n"
"s_mov_b32 s13,s11\n"
AR "v72,a0\n"
AR "v73,a1\n"
AR "v74,a2\n"
MF "a[24:27],v[140:143],v[96:99],a[24:27]\n"
AR "v75,a3\n"
AR "v76,a20\n"
AR "v77,a21\n"
AR "v78,a22\n"
AR "v79,a23\n"
"v_pk_fma_f32 v[72:73],v[72:73],v[56:57],v[52:53] op_sel:[0,1,0] op_sel_hi:[1,1,0]\n"
"v_pk_fma_f32 v[74:75],v[74:75],v[56:57],v[52:53] op_sel:[0,1,0] op_sel_hi:[1,1,0]\n"
"v_pk_fma_f32 v[76:77],v[76:77],v[56:57],v[52:53] op_sel:[0,1,0] op_sel_hi:[1,1,0]\n"
"v_pk_fma_f32 v[78:79],v[78:79],v[56:57],v[52:53] op_sel:[0,1,0] op_sel_hi:[1,1,0]\n"
CV "v112,v72,v73\n"
CV "v113,v74,v75\n"
CV "v114,v76,v77\n"
CV "v115,v78,v79\n"
MF "a[8:11],v[136:139],v[100:103],a[8:11]\n"
AW "a0,v112\n"
AW "a1,v113\n"
AW "a2,v114\n"
AW "a3,v115\n"
"s_add_u32 m0,s62,16384\n"
"s_add_u32 s82,s66,2359424\n"
LD "v32,s[28:31],s82 offen lds\n"
AR "v80,a4\n"
AR "v81,a5\n"
AR "v82,a6\n"
MF "a[28:31],v[140:143],v[100:103],a[28:31]\n"
AR "v83,a7\n"
AR "v84,a24\n"
AR "v85,a25\n"
AR "v86,a26\n"
AR "v87,a27\n"
"v_pk_fma_f32 v[80:81],v[80:81],v[58:59],v[52:53] op_sel:[0,0,1] op_sel_hi:[1,0,1]\n"
"v_pk_fma_f32 v[82:83],v[82:83],v[58:59],v[52:53] op_sel:[0,0,1] op_sel_hi:[1,0,1]\n"
"v_pk_fma_f32 v[84:85],v[84:85],v[58:59],v[52:53] op_sel:[0,0,1] op_sel_hi:[1,0,1]\n"
"v_pk_fma_f32 v[86:87],v[86:87],v[58:59],v[52:53] op_sel:[0,0,1] op_sel_hi:[1,0,1]\n"
CV "v116,v80,v81\n"
CV "v117,v82,v83\n"
CV "v118,v84,v85\n"
CV "v119,v86,v87\n"
MF "a[12:15],v[136:139],v[104:107],a[12:15]\n"
AW "a4,v116\n"
AW "a5,v117\n"
AW "a6,v118\n"
AW "a7,v119\n"
LD "v33,s[28:31],s82 offen offset:1024 lds\n"
AR "v72,a8\n"
AR "v73,a9\n"
AR "v74,a10\n"
MF "a[32:35],v[140:143],v[104:107],a[32:35]\n"
AR "v75,a11\n"
AR "v76,a28\n"
AR "v77,a29\n"
AR "v78,a30\n"
AR "v79,a31\n"
"v_pk_fma_f32 v[72:73],v[72:73],v[58:59],v[54:55] op_sel:[0,1,0] op_sel_hi:[1,1,0]\n"
"v_pk_fma_f32 v[74:75],v[74:75],v[58:59],v[54:55] op_sel:[0,1,0] op_sel_hi:[1,1,0]\n"
"v_pk_fma_f32 v[76:77],v[76:77],v[58:59],v[54:55] op_sel:[0,1,0] op_sel_hi:[1,1,0]\n"
"v_pk_fma_f32 v[78:79],v[78:79],v[58:59],v[54:55] op_sel:[0,1,0] op_sel_hi:[1,1,0]\n"
CV "v112,v72,v73\n"
CV "v113,v74,v75\n"
CV "v114,v76,v77\n"
CV "v115,v78,v79\n"
MF "a[16:19],v[136:139],v[108:111],a[16:19]\n"
AW "a8,v112\n"
AW "a9,v113\n"
AW "a10,v114\n"
AW "a11,v115\n"
"s_waitcnt lgkmcnt(0)\n"
"s_barrier\n"
AR "v80,a12\n"
AR "v81,a13\n"
AR "v82,a14\n"
MF "a[36:39],v[140:143],v[108:111],a[36:39]\n"
AR "v83,a15\n"
AR "v84,a32\n"
AR "v85,a33\n"
AR "v86,a34\n"
AR "v87,a35\n"
"v_pk_fma_f32 v[80:81],v[80:81],v[60:61],v[54:55] op_sel:[0,0,1] op_sel_hi:[1,0,1]\n"
"v_pk_fma_f32 v[82:83],v[82:83],v[60:61],v[54:55] op_sel:[0,0,1] op_sel_hi:[1,0,1]\n"
"v_pk_fma_f32 v[84:85],v[84:85],v[60:61],v[54:55] op_sel:[0,0,1] op_sel_hi:[1,0,1]\n"
"v_pk_fma_f32 v[86:87],v[86:87],v[60:61],v[54:55] op_sel:[0,0,1] op_sel_hi:[1,0,1]\n"
CV "v116,v80,v81\n"
CV "v117,v82,v83\n"
CV "v118,v84,v85\n"
CV "v119,v86,v87\n"
MF "a[40:43],v[144:147],v[92:95],a[40:43]\n"
AW "a12,v116\n"
AW "a13,v117\n"
AW "a14,v118\n"
AW "a15,v119\n"
LD "v34,s[28:31],s82 offen offset:2048 lds\n"
AR "v72,a16\n"
AR "v73,a17\n"
AR "v74,a18\n"
MF "a[60:63],v[148:151],v[92:95],a[60:63]\n"
AR "v75,a19\n"
AR "v76,a36\n"
AR "v77,a37\n"
AR "v78,a38\n"
AR "v79,a39\n"
"v_pk_fma_f32 v[72:73],v[72:73],v[60:61],v[56:57] op_sel:[0,1,0] op_sel_hi:[1,1,0]\n"
"v_pk_fma_f32 v[74:75],v[74:75],v[60:61],v[56:57] op_sel:[0,1,0] op_sel_hi:[1,1,0]\n"
"v_pk_fma_f32 v[76:77],v[76:77],v[60:61],v[56:57] op_sel:[0,1,0] op_sel_hi:[1,1,0]\n"
"v_pk_fma_f32 v[78:79],v[78:79],v[60:61],v[56:57] op_sel:[0,1,0] op_sel_hi:[1,1,0]\n"
CV "v112,v72,v73\n"
CV "v113,v74,v75\n"
CV "v114,v76,v77\n"
CV "v115,v78,v79\n"
MF "a[44:47],v[144:147],v[96:99],a[44:47]\n"
AW "a16,v112\n"
AW "a17,v113\n"
AW "a18,v114\n"
AW "a19,v115\n"
LD "v35,s[28:31],s82 offen offset:3072 lds\n"
AR "v80,a40\n"
AR "v81,a41\n"
AR "v82,a42\n"
MF "a[64:67],v[148:151],v[96:99],a[64:67]\n"
AR "v83,a43\n"
AR "v84,a60\n"
AR "v85,a61\n"
AR "v86,a62\n"
AR "v87,a63\n"
"v_pk_fma_f32 v[80:81],v[80:81],v[56:57],v[52:53] op_sel:[0,1,0] op_sel_hi:[1,1,0]\n"
"v_pk_fma_f32 v[82:83],v[82:83],v[56:57],v[52:53] op_sel:[0,1,0] op_sel_hi:[1,1,0]\n"
"v_pk_fma_f32 v[84:85],v[84:85],v[56:57],v[52:53] op_sel:[0,1,0] op_sel_hi:[1,1,0]\n"
"v_pk_fma_f32 v[86:87],v[86:87],v[56:57],v[52:53] op_sel:[0,1,0] op_sel_hi:[1,1,0]\n"
CV "v116,v80,v81\n"
CV "v117,v82,v83\n"
CV "v118,v84,v85\n"
CV "v119,v86,v87\n"
MF "a[48:51],v[144:147],v[100:103],a[48:51]\n"
AW "a40,v116\n"
AW "a41,v117\n"
AW "a42,v118\n"
AW "a43,v119\n"
AR "v72,a44\n"
AR "v73,a45\n"
AR "v74,a46\n"
MF "a[68:71],v[148:151],v[100:103],a[68:71]\n"
AR "v75,a47\n"
AR "v76,a64\n"
AR "v77,a65\n"
AR "v78,a66\n"
AR "v79,a67\n"
"v_pk_fma_f32 v[72:73],v[72:73],v[58:59],v[52:53] op_sel:[0,0,1] op_sel_hi:[1,0,1]\n"
"v_pk_fma_f32 v[74:75],v[74:75],v[58:59],v[52:53] op_sel:[0,0,1] op_sel_hi:[1,0,1]\n"
"v_pk_fma_f32 v[76:77],v[76:77],v[58:59],v[52:53] op_sel:[0,0,1] op_sel_hi:[1,0,1]\n"
"v_pk_fma_f32 v[78:79],v[78:79],v[58:59],v[52:53] op_sel:[0,0,1] op_sel_hi:[1,0,1]\n"
CV "v112,v72,v73\n"
CV "v113,v74,v75\n"
CV "v114,v76,v77\n"
CV "v115,v78,v79\n"
MF "a[52:55],v[144:147],v[104:107],a[52:55]\n"
AW "a44,v112\n"
AW "a45,v113\n"
AW "a46,v114\n"
AW "a47,v115\n"
"s_add_u32 m0,s62,32768\n"
"s_add_u32 s82,s66,2359552\n"
LD "v32,s[28:31],s82 offen lds\n"
AR "v80,a48\n"
AR "v81,a49\n"
AR "v82,a50\n"
MF "a[72:75],v[148:151],v[104:107],a[72:75]\n"
AR "v83,a51\n"
AR "v84,a68\n"
AR "v85,a69\n"
AR "v86,a70\n"
AR "v87,a71\n"
"v_pk_fma_f32 v[80:81],v[80:81],v[58:59],v[54:55] op_sel:[0,1,0] op_sel_hi:[1,1,0]\n"
"v_pk_fma_f32 v[82:83],v[82:83],v[58:59],v[54:55] op_sel:[0,1,0] op_sel_hi:[1,1,0]\n"
"v_pk_fma_f32 v[84:85],v[84:85],v[58:59],v[54:55] op_sel:[0,1,0] op_sel_hi:[1,1,0]\n"
"v_pk_fma_f32 v[86:87],v[86:87],v[58:59],v[54:55] op_sel:[0,1,0] op_sel_hi:[1,1,0]\n"
CV "v116,v80,v81\n"
CV "v117,v82,v83\n"
CV "v118,v84,v85\n"
CV "v119,v86,v87\n"
MF "a[56:59],v[144:147],v[108:111],a[56:59]\n"
AW "a48,v116\n"
AW "a49,v117\n"
AW "a50,v118\n"
AW "a51,v119\n"
LD "v33,s[28:31],s82 offen offset:1024 lds\n"
AR "v72,a52\n"
AR "v73,a53\n"
AR "v74,a54\n"
MF "a[76:79],v[148:151],v[108:111],a[76:79]\n"
AR "v75,a55\n"
AR "v76,a72\n"
AR "v77,a73\n"
AR "v78,a74\n"
AR "v79,a75\n"
"v_pk_fma_f32 v[72:73],v[72:73],v[60:61],v[54:55] op_sel:[0,0,1] op_sel_hi:[1,0,1]\n"
"v_pk_fma_f32 v[74:75],v[74:75],v[60:61],v[54:55] op_sel:[0,0,1] op_sel_hi:[1,0,1]\n"
"v_pk_fma_f32 v[76:77],v[76:77],v[60:61],v[54:55] op_sel:[0,0,1] op_sel_hi:[1,0,1]\n"
"v_pk_fma_f32 v[78:79],v[78:79],v[60:61],v[54:55] op_sel:[0,0,1] op_sel_hi:[1,0,1]\n"
CV "v112,v72,v73\n"
CV "v113,v74,v75\n"
CV "v114,v76,v77\n"
CV "v115,v78,v79\n"
MF "a[80:83],v[152:155],v[92:95],a[80:83]\n"
AW "a52,v112\n"
AW "a53,v113\n"
AW "a54,v114\n"
AW "a55,v115\n"
AR "v80,a56\n"
AR "v81,a57\n"
AR "v82,a58\n"
MF "a[100:103],v[156:159],v[92:95],a[100:103]\n"
AR "v83,a59\n"
AR "v84,a76\n"
AR "v85,a77\n"
AR "v86,a78\n"
AR "v87,a79\n"
"v_pk_fma_f32 v[80:81],v[80:81],v[60:61],v[56:57] op_sel:[0,1,0] op_sel_hi:[1,1,0]\n"
"v_pk_fma_f32 v[82:83],v[82:83],v[60:61],v[56:57] op_sel:[0,1,0] op_sel_hi:[1,1,0]\n"
"v_pk_fma_f32 v[84:85],v[84:85],v[60:61],v[56:57] op_sel:[0,1,0] op_sel_hi:[1,1,0]\n"
"v_pk_fma_f32 v[86:87],v[86:87],v[60:61],v[56:57] op_sel:[0,1,0] op_sel_hi:[1,1,0]\n"
CV "v116,v80,v81\n"
CV "v117,v82,v83\n"
CV "v118,v84,v85\n"
CV "v119,v86,v87\n"
MF "a[84:87],v[152:155],v[96:99],a[84:87]\n"
AW "a56,v116\n"
AW "a57,v117\n"
AW "a58,v118\n"
AW "a59,v119\n"
LD "v34,s[28:31],s82 offen offset:2048 lds\n"
AR "v72,a80\n"
AR "v73,a81\n"
AR "v74,a82\n"
MF "a[104:107],v[156:159],v[96:99],a[104:107]\n"
AR "v75,a83\n"
AR "v76,a100\n"
AR "v77,a101\n"
AR "v78,a102\n"
AR "v79,a103\n"
"v_pk_fma_f32 v[72:73],v[72:73],v[56:57],v[52:53] op_sel:[0,1,0] op_sel_hi:[1,1,0]\n"
"v_pk_fma_f32 v[74:75],v[74:75],v[56:57],v[52:53] op_sel:[0,1,0] op_sel_hi:[1,1,0]\n"
"v_pk_fma_f32 v[76:77],v[76:77],v[56:57],v[52:53] op_sel:[0,1,0] op_sel_hi:[1,1,0]\n"
"v_pk_fma_f32 v[78:79],v[78:79],v[56:57],v[52:53] op_sel:[0,1,0] op_sel_hi:[1,1,0]\n"
CV "v112,v72,v73\n"
CV "v113,v74,v75\n"
CV "v114,v76,v77\n"
CV "v115,v78,v79\n"
MF "a[88:91],v[152:155],v[100:103],a[88:91]\n"
AW "a80,v112\n"
AW "a81,v113\n"
AW "a82,v114\n"
AW "a83,v115\n"
LD "v35,s[28:31],s82 offen offset:3072 lds\n"
AR "v80,a84\n"
AR "v81,a85\n"
AR "v82,a86\n"
MF "a[108:111],v[156:159],v[100:103],a[108:111]\n"
AR "v83,a87\n"
AR "v84,a104\n"
AR "v85,a105\n"
AR "v86,a106\n"
AR "v87,a107\n"
"v_pk_fma_f32 v[80:81],v[80:81],v[58:59],v[52:53] op_sel:[0,0,1] op_sel_hi:[1,0,1]\n"
"v_pk_fma_f32 v[82:83],v[82:83],v[58:59],v[52:53] op_sel:[0,0,1] op_sel_hi:[1,0,1]\n"
"v_pk_fma_f32 v[84:85],v[84:85],v[58:59],v[52:53] op_sel:[0,0,1] op_sel_hi:[1,0,1]\n"
"v_pk_fma_f32 v[86:87],v[86:87],v[58:59],v[52:53] op_sel:[0,0,1] op_sel_hi:[1,0,1]\n"
CV "v116,v80,v81\n"
CV "v117,v82,v83\n"
CV "v118,v84,v85\n"
CV "v119,v86,v87\n"
MF "a[92:95],v[152:155],v[104:107],a[92:95]\n"
AW "a84,v116\n"
AW "a85,v117\n"
AW "a86,v118\n"
AW "a87,v119\n"
AR "v72,a88\n"
AR "v73,a89\n"
AR "v74,a90\n"
MF "a[112:115],v[156:159],v[104:107],a[112:115]\n"
AR "v75,a91\n"
AR "v76,a108\n"
AR "v77,a109\n"
AR "v78,a110\n"
AR "v79,a111\n"
"v_pk_fma_f32 v[72:73],v[72:73],v[58:59],v[54:55] op_sel:[0,1,0] op_sel_hi:[1,1,0]\n"
"v_pk_fma_f32 v[74:75],v[74:75],v[58:59],v[54:55] op_sel:[0,1,0] op_sel_hi:[1,1,0]\n"
"v_pk_fma_f32 v[76:77],v[76:77],v[58:59],v[54:55] op_sel:[0,1,0] op_sel_hi:[1,1,0]\n"
"v_pk_fma_f32 v[78:79],v[78:79],v[58:59],v[54:55] op_sel:[0,1,0] op_sel_hi:[1,1,0]\n"
CV "v112,v72,v73\n"
CV "v113,v74,v75\n"
CV "v114,v76,v77\n"
CV "v115,v78,v79\n"
MF "a[96:99],v[152:155],v[108:111],a[96:99]\n"
AW "a88,v112\n"
AW "a89,v113\n"
AW "a90,v114\n"
AW "a91,v115\n"
AR "v80,a92\n"
AR "v81,a93\n"
AR "v82,a94\n"
MF "a[116:119],v[156:159],v[108:111],a[116:119]\n"
AR "v83,a95\n"
AR "v84,a112\n"
AR "v85,a113\n"
AR "v86,a114\n"
AR "v87,a115\n"
"v_pk_fma_f32 v[80:81],v[80:81],v[60:61],v[54:55] op_sel:[0,0,1] op_sel_hi:[1,0,1]\n"
"v_pk_fma_f32 v[82:83],v[82:83],v[60:61],v[54:55] op_sel:[0,0,1] op_sel_hi:[1,0,1]\n"
"v_pk_fma_f32 v[84:85],v[84:85],v[60:61],v[54:55] op_sel:[0,0,1] op_sel_hi:[1,0,1]\n"
"v_pk_fma_f32 v[86:87],v[86:87],v[60:61],v[54:55] op_sel:[0,0,1] op_sel_hi:[1,0,1]\n"
CV "v116,v80,v81\n"
CV "v117,v82,v83\n"
CV "v118,v84,v85\n"
CV "v119,v86,v87\n"
MF "a[120:123],v[160:163],v[92:95],a[120:123]\n"
AW "a92,v116\n"
AW "a93,v117\n"
AW "a94,v118\n"
AW "a95,v119\n"
AR "v72,a96\n"
AR "v73,a97\n"
AR "v74,a98\n"
MF "a[140:143],v[164:167],v[92:95],a[140:143]\n"
AR "v75,a99\n"
AR "v76,a116\n"
AR "v77,a117\n"
AR "v78,a118\n"
AR "v79,a119\n"
"v_pk_fma_f32 v[72:73],v[72:73],v[60:61],v[56:57] op_sel:[0,1,0] op_sel_hi:[1,1,0]\n"
"v_pk_fma_f32 v[74:75],v[74:75],v[60:61],v[56:57] op_sel:[0,1,0] op_sel_hi:[1,1,0]\n"
"v_pk_fma_f32 v[76:77],v[76:77],v[60:61],v[56:57] op_sel:[0,1,0] op_sel_hi:[1,1,0]\n"
"v_pk_fma_f32 v[78:79],v[78:79],v[60:61],v[56:57] op_sel:[0,1,0] op_sel_hi:[1,1,0]\n"
CV "v112,v72,v73\n"
CV "v113,v74,v75\n"
CV "v114,v76,v77\n"
CV "v115,v78,v79\n"
MF "a[124:127],v[160:163],v[96:99],a[124:127]\n"
AW "a96,v112\n"
AW "a97,v113\n"
AW "a98,v114\n"
AW "a99,v115\n"
AR "v80,a120\n"
AR "v81,a121\n"
AR "v82,a122\n"
MF "a[144:147],v[164:167],v[96:99],a[144:147]\n"
AR "v83,a123\n"
AR "v84,a140\n"
AR "v85,a141\n"
AR "v86,a142\n"
AR "v87,a143\n"
"v_pk_fma_f32 v[80:81],v[80:81],v[56:57],v[52:53] op_sel:[0,1,0] op_sel_hi:[1,1,0]\n"
"v_pk_fma_f32 v[82:83],v[82:83],v[56:57],v[52:53] op_sel:[0,1,0] op_sel_hi:[1,1,0]\n"
"v_pk_fma_f32 v[84:85],v[84:85],v[56:57],v[52:53] op_sel:[0,1,0] op_sel_hi:[1,1,0]\n"
"v_pk_fma_f32 v[86:87],v[86:87],v[56:57],v[52:53] op_sel:[0,1,0] op_sel_hi:[1,1,0]\n"
CV "v116,v80,v81\n"
CV "v117,v82,v83\n"
CV "v118,v84,v85\n"
CV "v119,v86,v87\n"
MF "a[128:131],v[160:163],v[100:103],a[128:131]\n"
AW "a120,v116\n"
AW "a121,v117\n"
AW "a122,v118\n"
AW "a123,v119\n"
AR "v72,a124\n"
AR "v73,a125\n"
AR "v74,a126\n"
MF "a[148:151],v[164:167],v[100:103],a[148:151]\n"
AR "v75,a127\n"
AR "v76,a144\n"
AR "v77,a145\n"
AR "v78,a146\n"
AR "v79,a147\n"
"v_pk_fma_f32 v[72:73],v[72:73],v[58:59],v[52:53] op_sel:[0,0,1] op_sel_hi:[1,0,1]\n"
"v_pk_fma_f32 v[74:75],v[74:75],v[58:59],v[52:53] op_sel:[0,0,1] op_sel_hi:[1,0,1]\n"
"v_pk_fma_f32 v[76:77],v[76:77],v[58:59],v[52:53] op_sel:[0,0,1] op_sel_hi:[1,0,1]\n"
"v_pk_fma_f32 v[78:79],v[78:79],v[58:59],v[52:53] op_sel:[0,0,1] op_sel_hi:[1,0,1]\n"
CV "v112,v72,v73\n"
CV "v113,v74,v75\n"
CV "v114,v76,v77\n"
CV "v115,v78,v79\n"
MF "a[132:135],v[160:163],v[104:107],a[132:135]\n"
AW "a124,v112\n"
AW "a125,v113\n"
AW "a126,v114\n"
AW "a127,v115\n"
AR "v80,a128\n"
AR "v81,a129\n"
AR "v82,a130\n"
MF "a[152:155],v[164:167],v[104:107],a[152:155]\n"
AR "v83,a131\n"
AR "v84,a148\n"
AR "v85,a149\n"
AR "v86,a150\n"
AR "v87,a151\n"
"v_pk_fma_f32 v[80:81],v[80:81],v[58:59],v[54:55] op_sel:[0,1,0] op_sel_hi:[1,1,0]\n"
"v_pk_fma_f32 v[82:83],v[82:83],v[58:59],v[54:55] op_sel:[0,1,0] op_sel_hi:[1,1,0]\n"
"v_pk_fma_f32 v[84:85],v[84:85],v[58:59],v[54:55] op_sel:[0,1,0] op_sel_hi:[1,1,0]\n"
"v_pk_fma_f32 v[86:87],v[86:87],v[58:59],v[54:55] op_sel:[0,1,0] op_sel_hi:[1,1,0]\n"
CV "v116,v80,v81\n"
CV "v117,v82,v83\n"
CV "v118,v84,v85\n"
CV "v119,v86,v87\n"
MF "a[136:139],v[160:163],v[108:111],a[136:139]\n"
AW "a128,v116\n"
AW "a129,v117\n"
AW "a130,v118\n"
AW "a131,v119\n"
AR "v72,a132\n"
AR "v73,a133\n"
AR "v74,a134\n"
MF "a[156:159],v[164:167],v[108:111],a[156:159]\n"
AR "v75,a135\n"
AR "v76,a152\n"
AR "v77,a153\n"
AR "v78,a154\n"
AR "v79,a155\n"
"v_pk_fma_f32 v[72:73],v[72:73],v[60:61],v[54:55] op_sel:[0,0,1] op_sel_hi:[1,0,1]\n"
"v_pk_fma_f32 v[74:75],v[74:75],v[60:61],v[54:55] op_sel:[0,0,1] op_sel_hi:[1,0,1]\n"
"v_pk_fma_f32 v[76:77],v[76:77],v[60:61],v[54:55] op_sel:[0,0,1] op_sel_hi:[1,0,1]\n"
"v_pk_fma_f32 v[78:79],v[78:79],v[60:61],v[54:55] op_sel:[0,0,1] op_sel_hi:[1,0,1]\n"
CV "v112,v72,v73\n"
CV "v113,v74,v75\n"
CV "v114,v76,v77\n"
CV "v115,v78,v79\n"
MF "a[160:163],v[168:171],v[92:95],a[160:163]\n"
AW "a132,v112\n"
AW "a133,v113\n"
AW "a134,v114\n"
AW "a135,v115\n"
AR "v80,a136\n"
AR "v81,a137\n"
AR "v82,a138\n"
MF "a[180:183],v[172:175],v[92:95],a[180:183]\n"
AR "v83,a139\n"
AR "v84,a156\n"
AR "v85,a157\n"
AR "v86,a158\n"
AR "v87,a159\n"
"v_pk_fma_f32 v[80:81],v[80:81],v[60:61],v[56:57] op_sel:[0,1,0] op_sel_hi:[1,1,0]\n"
"v_pk_fma_f32 v[82:83],v[82:83],v[60:61],v[56:57] op_sel:[0,1,0] op_sel_hi:[1,1,0]\n"
"v_pk_fma_f32 v[84:85],v[84:85],v[60:61],v[56:57] op_sel:[0,1,0] op_sel_hi:[1,1,0]\n"
"v_pk_fma_f32 v[86:87],v[86:87],v[60:61],v[56:57] op_sel:[0,1,0] op_sel_hi:[1,1,0]\n"
CV "v116,v80,v81\n"
CV "v117,v82,v83\n"
CV "v118,v84,v85\n"
CV "v119,v86,v87\n"
MF "a[164:167],v[168:171],v[96:99],a[164:167]\n"
AW "a136,v116\n"
AW "a137,v117\n"
AW "a138,v118\n"
AW "a139,v119\n"
AR "v72,a160\n"
AR "v73,a161\n"
AR "v74,a162\n"
MF "a[184:187],v[172:175],v[96:99],a[184:187]\n"
AR "v75,a163\n"
AR "v76,a180\n"
AR "v77,a181\n"
AR "v78,a182\n"
AR "v79,a183\n"
"v_pk_fma_f32 v[72:73],v[72:73],v[56:57],v[52:53] op_sel:[0,1,0] op_sel_hi:[1,1,0]\n"
"v_pk_fma_f32 v[74:75],v[74:75],v[56:57],v[52:53] op_sel:[0,1,0] op_sel_hi:[1,1,0]\n"
"v_pk_fma_f32 v[76:77],v[76:77],v[56:57],v[52:53] op_sel:[0,1,0] op_sel_hi:[1,1,0]\n"
"v_pk_fma_f32 v[78:79],v[78:79],v[56:57],v[52:53] op_sel:[0,1,0] op_sel_hi:[1,1,0]\n"
CV "v112,v72,v73\n"
CV "v113,v74,v75\n"
CV "v114,v76,v77\n"
CV "v115,v78,v79\n"
MF "a[168:171],v[168:171],v[100:103],a[168:171]\n"
AW "a160,v112\n"
AW "a161,v113\n"
AW "a162,v114\n"
AW "a163,v115\n"
AR "v80,a164\n"
AR "v81,a165\n"
AR "v82,a166\n"
MF "a[188:191],v[172:175],v[100:103],a[188:191]\n"
AR "v83,a167\n"
AR "v84,a184\n"
AR "v85,a185\n"
AR "v86,a186\n"
AR "v87,a187\n"
"v_pk_fma_f32 v[80:81],v[80:81],v[58:59],v[52:53] op_sel:[0,0,1] op_sel_hi:[1,0,1]\n"
"v_pk_fma_f32 v[82:83],v[82:83],v[58:59],v[52:53] op_sel:[0,0,1] op_sel_hi:[1,0,1]\n"
"v_pk_fma_f32 v[84:85],v[84:85],v[58:59],v[52:53] op_sel:[0,0,1] op_sel_hi:[1,0,1]\n"
"v_pk_fma_f32 v[86:87],v[86:87],v[58:59],v[52:53] op_sel:[0,0,1] op_sel_hi:[1,0,1]\n"
CV "v116,v80,v81\n"
CV "v117,v82,v83\n"
CV "v118,v84,v85\n"
CV "v119,v86,v87\n"
MF "a[172:175],v[168:171],v[104:107],a[172:175]\n"
AW "a164,v116\n"
AW "a165,v117\n"
AW "a166,v118\n"
AW "a167,v119\n"
AR "v72,a168\n"
AR "v73,a169\n"
AR "v74,a170\n"
MF "a[192:195],v[172:175],v[104:107],a[192:195]\n"
AR "v75,a171\n"
AR "v76,a188\n"
AR "v77,a189\n"
AR "v78,a190\n"
AR "v79,a191\n"
"v_pk_fma_f32 v[72:73],v[72:73],v[58:59],v[54:55] op_sel:[0,1,0] op_sel_hi:[1,1,0]\n"
"v_pk_fma_f32 v[74:75],v[74:75],v[58:59],v[54:55] op_sel:[0,1,0] op_sel_hi:[1,1,0]\n"
"v_pk_fma_f32 v[76:77],v[76:77],v[58:59],v[54:55] op_sel:[0,1,0] op_sel_hi:[1,1,0]\n"
"v_pk_fma_f32 v[78:79],v[78:79],v[58:59],v[54:55] op_sel:[0,1,0] op_sel_hi:[1,1,0]\n"
CV "v112,v72,v73\n"
CV "v113,v74,v75\n"
CV "v114,v76,v77\n"
CV "v115,v78,v79\n"
MF "a[176:179],v[168:171],v[108:111],a[176:179]\n"
AW "a168,v112\n"
AW "a169,v113\n"
AW "a170,v114\n"
AW "a171,v115\n"
AR "v80,a172\n"
AR "v81,a173\n"
AR "v82,a174\n"
MF "a[196:199],v[172:175],v[108:111],a[196:199]\n"
AR "v83,a175\n"
AR "v84,a192\n"
AR "v85,a193\n"
AR "v86,a194\n"
AR "v87,a195\n"
"v_pk_fma_f32 v[80:81],v[80:81],v[60:61],v[54:55] op_sel:[0,0,1] op_sel_hi:[1,0,1]\n"
"v_pk_fma_f32 v[82:83],v[82:83],v[60:61],v[54:55] op_sel:[0,0,1] op_sel_hi:[1,0,1]\n"
"v_pk_fma_f32 v[84:85],v[84:85],v[60:61],v[54:55] op_sel:[0,0,1] op_sel_hi:[1,0,1]\n"
"v_pk_fma_f32 v[86:87],v[86:87],v[60:61],v[54:55] op_sel:[0,0,1] op_sel_hi:[1,0,1]\n"
CV "v116,v80,v81\n"
CV "v117,v82,v83\n"
CV "v118,v84,v85\n"
CV "v119,v86,v87\n"
MF "a[200:203],v[176:179],v[92:95],a[200:203]\n"
AW "a172,v116\n"
AW "a173,v117\n"
AW "a174,v118\n"
AW "a175,v119\n"
AR "v72,a176\n"
AR "v73,a177\n"
AR "v74,a178\n"
MF "a[220:223],v[180:183],v[92:95],a[220:223]\n"
AR "v75,a179\n"
AR "v76,a196\n"
AR "v77,a197\n"
AR "v78,a198\n"
AR "v79,a199\n"
"v_pk_fma_f32 v[72:73],v[72:73],v[60:61],v[56:57] op_sel:[0,1,0] op_sel_hi:[1,1,0]\n"
"v_pk_fma_f32 v[74:75],v[74:75],v[60:61],v[56:57] op_sel:[0,1,0] op_sel_hi:[1,1,0]\n"
"v_pk_fma_f32 v[76:77],v[76:77],v[60:61],v[56:57] op_sel:[0,1,0] op_sel_hi:[1,1,0]\n"
"v_pk_fma_f32 v[78:79],v[78:79],v[60:61],v[56:57] op_sel:[0,1,0] op_sel_hi:[1,1,0]\n"
CV "v112,v72,v73\n"
CV "v113,v74,v75\n"
CV "v114,v76,v77\n"
CV "v115,v78,v79\n"
MF "a[204:207],v[176:179],v[96:99],a[204:207]\n"
AW "a176,v112\n"
AW "a177,v113\n"
AW "a178,v114\n"
AW "a179,v115\n"
AR "v80,a200\n"
AR "v81,a201\n"
AR "v82,a202\n"
MF "a[224:227],v[180:183],v[96:99],a[224:227]\n"
AR "v83,a203\n"
AR "v84,a220\n"
AR "v85,a221\n"
AR "v86,a222\n"
AR "v87,a223\n"
"v_pk_fma_f32 v[80:81],v[80:81],v[56:57],v[52:53] op_sel:[0,1,0] op_sel_hi:[1,1,0]\n"
"v_pk_fma_f32 v[82:83],v[82:83],v[56:57],v[52:53] op_sel:[0,1,0] op_sel_hi:[1,1,0]\n"
"v_pk_fma_f32 v[84:85],v[84:85],v[56:57],v[52:53] op_sel:[0,1,0] op_sel_hi:[1,1,0]\n"
"v_pk_fma_f32 v[86:87],v[86:87],v[56:57],v[52:53] op_sel:[0,1,0] op_sel_hi:[1,1,0]\n"
CV "v116,v80,v81\n"
CV "v117,v82,v83\n"
CV "v118,v84,v85\n"
CV "v119,v86,v87\n"
MF "a[208:211],v[176:179],v[100:103],a[208:211]\n"
AW "a20,v116\n"
AW "a21,v117\n"
AW "a22,v118\n"
AW "a23,v119\n"
AR "v72,a204\n"
AR "v73,a205\n"
AR "v74,a206\n"
MF "a[228:231],v[180:183],v[100:103],a[228:231]\n"
AR "v75,a207\n"
AR "v76,a224\n"
AR "v77,a225\n"
AR "v78,a226\n"
AR "v79,a227\n"
"v_pk_fma_f32 v[72:73],v[72:73],v[58:59],v[52:53] op_sel:[0,0,1] op_sel_hi:[1,0,1]\n"
"v_pk_fma_f32 v[74:75],v[74:75],v[58:59],v[52:53] op_sel:[0,0,1] op_sel_hi:[1,0,1]\n"
"v_pk_fma_f32 v[76:77],v[76:77],v[58:59],v[52:53] op_sel:[0,0,1] op_sel_hi:[1,0,1]\n"
"v_pk_fma_f32 v[78:79],v[78:79],v[58:59],v[52:53] op_sel:[0,0,1] op_sel_hi:[1,0,1]\n"
CV "v112,v72,v73\n"
CV "v113,v74,v75\n"
CV "v114,v76,v77\n"
CV "v115,v78,v79\n"
MF "a[212:215],v[176:179],v[104:107],a[212:215]\n"
AW "a24,v112\n"
AW "a25,v113\n"
AW "a26,v114\n"
AW "a27,v115\n"
AR "v80,a208\n"
AR "v81,a209\n"
AR "v82,a210\n"
MF "a[232:235],v[180:183],v[104:107],a[232:235]\n"
AR "v83,a211\n"
AR "v84,a228\n"
AR "v85,a229\n"
AR "v86,a230\n"
AR "v87,a231\n"
"v_pk_fma_f32 v[80:81],v[80:81],v[58:59],v[54:55] op_sel:[0,1,0] op_sel_hi:[1,1,0]\n"
"v_pk_fma_f32 v[82:83],v[82:83],v[58:59],v[54:55] op_sel:[0,1,0] op_sel_hi:[1,1,0]\n"
"v_pk_fma_f32 v[84:85],v[84:85],v[58:59],v[54:55] op_sel:[0,1,0] op_sel_hi:[1,1,0]\n"
"v_pk_fma_f32 v[86:87],v[86:87],v[58:59],v[54:55] op_sel:[0,1,0] op_sel_hi:[1,1,0]\n"
CV "v116,v80,v81\n"
CV "v117,v82,v83\n"
CV "v118,v84,v85\n"
CV "v119,v86,v87\n"
MF "a[216:219],v[176:179],v[108:111],a[216:219]\n"
AW "a28,v116\n"
AW "a29,v117\n"
AW "a30,v118\n"
AW "a31,v119\n"
AR "v72,a212\n"
AR "v73,a213\n"
AR "v74,a214\n"
MF "a[236:239],v[180:183],v[108:111],a[236:239]\n"
AR "v75,a215\n"
AR "v76,a232\n"
AR "v77,a233\n"
AR "v78,a234\n"
AR "v79,a235\n"
"v_pk_fma_f32 v[72:73],v[72:73],v[60:61],v[54:55] op_sel:[0,0,1] op_sel_hi:[1,0,1]\n"
"v_pk_fma_f32 v[74:75],v[74:75],v[60:61],v[54:55] op_sel:[0,0,1] op_sel_hi:[1,0,1]\n"
"v_pk_fma_f32 v[76:77],v[76:77],v[60:61],v[54:55] op_sel:[0,0,1] op_sel_hi:[1,0,1]\n"
"v_pk_fma_f32 v[78:79],v[78:79],v[60:61],v[54:55] op_sel:[0,0,1] op_sel_hi:[1,0,1]\n"
CV "v112,v72,v73\n"
CV "v113,v74,v75\n"
CV "v114,v76,v77\n"
CV "v115,v78,v79\n"
AW "a32,v112\n"
AW "a33,v113\n"
AW "a34,v114\n"
AW "a35,v115\n"
AR "v80,a216\n"
AR "v81,a217\n"
AR "v82,a218\n"
AR "v83,a219\n"
AR "v84,a236\n"
AR "v85,a237\n"
AR "v86,a238\n"
AR "v87,a239\n"
"v_pk_fma_f32 v[80:81],v[80:81],v[60:61],v[56:57] op_sel:[0,1,0] op_sel_hi:[1,1,0]\n"
"v_pk_fma_f32 v[82:83],v[82:83],v[60:61],v[56:57] op_sel:[0,1,0] op_sel_hi:[1,1,0]\n"
"v_pk_fma_f32 v[84:85],v[84:85],v[60:61],v[56:57] op_sel:[0,1,0] op_sel_hi:[1,1,0]\n"
"v_pk_fma_f32 v[86:87],v[86:87],v[60:61],v[56:57] op_sel:[0,1,0] op_sel_hi:[1,1,0]\n"
CV "v116,v80,v81\n"
CV "v117,v82,v83\n"
CV "v118,v84,v85\n"
CV "v119,v86,v87\n"
AW "a36,v116\n"
AW "a37,v117\n"
AW "a38,v118\n"
AW "a39,v119\n"
"s_mov_b32 s70,s71\n"
"s_mov_b32 s57,1\n"
AR "v0,a240\n"
AR "v2,a241\n"
AR "v4,a242\n"
AR "v6,a243\n"
"v_xor_b32 v1,64,v0\n"
"v_xor_b32 v3,64,v2\n"
"v_xor_b32 v5,64,v4\n"
"v_add_u32 v11,97280,v6\n"
"v_add_u32 v12,194560,v6\n"
"v_add_u32 v13,291840,v6\n"
"s_mov_b64 vcc,-1\n"
"s_waitcnt vmcnt(8) lgkmcnt(0)\n"
"s_barrier\n"
TR "a[200:201],v0\n"
TR "a[202:203],v0 offset:512\n"
TR "a[204:205],v0 offset:4096\n"
TR "a[206:207],v0 offset:4608\n"
"ds_read_b128 a[60:63],v2\n"
"ds_read_b128 a[100:103],v3\n"
"ds_read_b128 a[64:67],v2 offset:2048\n"
"ds_read_b128 a[104:107],v3 offset:2048\n"
"ds_read_b128 a[68:71],v2 offset:4096\n"
"ds_read_b128 a[108:111],v3 offset:4096\n"
"ds_read_b128 a[72:75],v2 offset:6144\n"
"ds_read_b128 a[112:115],v3 offset:6144\n"
"ds_read_b128 a[76:79],v2 offset:8192\n"
"ds_read_b128 a[116:119],v3 offset:8192\n"
TR "a[208:209],v0 offset:8\n"
TR "a[210:211],v0 offset:520\n"
TR "a[212:213],v0 offset:4104\n"
TR "a[214:215],v0 offset:4616\n"
"s_waitcnt lgkmcnt(0)\n"
"s_branch .Lq0\n"
".Lep:\n"
"s_lshl_b32 s58,s56,10\n"
"s_add_u32 s58,s58,73728\n"
"v_add_u32 v120,s58,v9\n"
"v_add_u32 v121,65536,v120\n"
"s_sub_u32 s59,s71,s70\n"
"s_nop 0\n"
TR "v[136:137],v2 offset:24576\n"
TR "v[138:139],v2 offset:30720\n"
TR "v[140:141],v3 offset:24576\n"
TR "v[142:143],v3 offset:30720\n"
TR "v[144:145],v4 offset:24576\n"
TR "v[146:147],v4 offset:30720\n"
TR "v[148:149],v5 offset:24576\n"
TR "v[150:151],v5 offset:30720\n"
TR "v[152:153],v2 offset:25600\n"
TR "v[154:155],v2 offset:31744\n"
TR "v[156:157],v3 offset:25600\n"
TR "v[158:159],v3 offset:31744\n"
TR "v[160:161],v4 offset:25600\n"
TR "v[162:163],v4 offset:31744\n"
TR "v[164:165],v5 offset:25600\n"
TR "v[166:167],v5 offset:31744\n"
TR "v[168:169],v2 offset:26624\n"
TR "v[170:171],v2 offset:32768\n"
TR "v[172:173],v3 offset:26624\n"
TR "v[174:175],v3 offset:32768\n"
TR "v[176:177],v4 offset:26624\n"
TR "v[178:179],v4 offset:32768\n"
TR "v[180:181],v5 offset:26624\n"
TR "v[182:183],v5 offset:32768\n"
"v_mov_b32 v52,v57\n"
"v_mov_b32 v53,v58\n"
"v_mov_b32 v54,v59\n"
"v_mov_b32 v55,v60\n"
"v_mov_b32 v56,v61\n"
"s_add_u32 m0,s65,49152\n"
"s_nop 0\n"
LD "v8,s[36:39],s73 offen offset:3072 nt lds\n"
"s_add_u32 m0,s65,50048\n"
"s_nop 0\n"
LD "v8,s[36:39],s73 offen offset:3200 nt lds\n"
"v_permlane16_swap_b32 v57,v52\n"
"v_permlane16_swap_b32 v58,v53\n"
"v_permlane16_swap_b32 v59,v54\n"
"v_permlane16_swap_b32 v60,v55\n"
"v_permlane16_swap_b32 v61,v56\n"
"v_add_f32 v57,v57,v52\n"
"v_add_f32 v58,v58,v53\n"
"v_add_f32 v59,v59,v54\n"
"v_add_f32 v60,v60,v55\n"
"v_add_f32 v61,v61,v56\n"
"v_mov_b32 v52,v57\n"
"v_mov_b32 v53,v58\n"
"v_mov_b32 v54,v59\n"
"v_mov_b32 v55,v60\n"
"v_mov_b32 v56,v61\n"
"s_add_u32 m0,s65,50944\n"
"s_nop 0\n"
LD "v8,s[36:39],s73 offen offset:3328 nt lds\n"
"s_sub_u32 s74,s96,s16\n"
"s_add_u32 s74,s74,s71\n"
"s_add_u32 s20,s20,s59\n"
"s_mov_b32 s70,s71\n"
LD "v[232:235],v9,s[40:43],s75 offen sc1\n"
"v_permlane32_swap_b32 v57,v52\n"
"v_permlane32_swap_b32 v58,v53\n"
"v_permlane32_swap_b32 v59,v54\n"
"v_permlane32_swap_b32 v60,v55\n"
"v_permlane32_swap_b32 v61,v56\n"
"v_add_f32 v57,v57,v52\n"
"v_add_f32 v58,v58,v53\n"
"v_add_f32 v59,v59,v54\n"
"v_add_f32 v60,v60,v55\n"
"v_add_f32 v61,v61,v56\n"
"s_waitcnt lgkmcnt(15)\n"
MF "a[0:3],v[136:139],v[92:95],a[0:3]\n"
MF "a[20:23],v[140:143],v[92:95],a[20:23]\n"
"v_rcp_f32 v57,v57\n"
"v_rcp_f32 v58,v58\n"
"v_rcp_f32 v59,v59\n"
"v_rcp_f32 v60,v60\n"
"v_rcp_f32_e64 v61,v61\n"
MF "a[4:7],v[136:139],v[96:99],a[4:7]\n"
"v_mul_f32_e64 v52,-s13,v57\n"
"v_mul_f32_e64 v53,-s13,v58\n"
"v_mul_f32_e64 v54,-s13,v59\n"
"v_mul_f32_e64 v55,-s13,v60\n"
"v_mul_f32_e64 v56,-s13,v61\n"
"s_mov_b32 s13,s11\n"
AR "v72,a0\n"
AR "v73,a1\n"
AR "v74,a2\n"
MF "a[24:27],v[140:143],v[96:99],a[24:27]\n"
AR "v75,a3\n"
AR "v76,a20\n"
AR "v77,a21\n"
"s_nop 0\n"
AR "v78,a22\n"
AR "v79,a23\n"
"v_pk_fma_f32 v[72:73],v[72:73],v[56:57],v[52:53] op_sel:[0,1,0] op_sel_hi:[1,1,0]\n"
"v_pk_fma_f32 v[74:75],v[74:75],v[56:57],v[52:53] op_sel:[0,1,0] op_sel_hi:[1,1,0]\n"
"v_pk_fma_f32 v[76:77],v[76:77],v[56:57],v[52:53] op_sel:[0,1,0] op_sel_hi:[1,1,0]\n"
"v_pk_fma_f32 v[78:79],v[78:79],v[56:57],v[52:53] op_sel:[0,1,0] op_sel_hi:[1,1,0]\n"
CV "v112,v72,v73\n"
CV "v113,v74,v75\n"
CV "v114,v76,v77\n"
CV "v115,v78,v79\n"
MF "a[8:11],v[136:139],v[100:103],a[8:11]\n"
"ds_read_b128 v[184:187],v120 offset:24576\n"
"ds_write_b128 v120,v[112:115] offset:24576\n"
LD "v[236:239],v9,s[40:43],s75 offen offset:1024 sc1\n"
AR "v80,a4\n"
AR "v81,a5\n"
AR "v82,a6\n"
MF "a[28:31],v[140:143],v[100:103],a[28:31]\n"
AR "v83,a7\n"
AR "v84,a24\n"
AR "v85,a25\n"
AR "v86,a26\n"
AR "v87,a27\n"
"v_pk_fma_f32 v[80:81],v[80:81],v[58:59],v[52:53] op_sel:[0,0,1] op_sel_hi:[1,0,1]\n"
"v_pk_fma_f32 v[82:83],v[82:83],v[58:59],v[52:53] op_sel:[0,0,1] op_sel_hi:[1,0,1]\n"
"v_pk_fma_f32 v[84:85],v[84:85],v[58:59],v[52:53] op_sel:[0,0,1] op_sel_hi:[1,0,1]\n"
"v_pk_fma_f32 v[86:87],v[86:87],v[58:59],v[52:53] op_sel:[0,0,1] op_sel_hi:[1,0,1]\n"
CV "v116,v80,v81\n"
CV "v117,v82,v83\n"
CV "v118,v84,v85\n"
CV "v119,v86,v87\n"
MF "a[12:15],v[136:139],v[104:107],a[12:15]\n"
"ds_read_b128 v[188:191],v120 offset:28672\n"
"ds_write_b128 v120,v[116:119] offset:28672\n"
AR "v72,a8\n"
AR "v73,a9\n"
AR "v74,a10\n"
MF "a[32:35],v[140:143],v[104:107],a[32:35]\n"
AR "v75,a11\n"
AR "v76,a28\n"
AR "v77,a29\n"
AR "v78,a30\n"
AR "v79,a31\n"
"v_pk_fma_f32 v[72:73],v[72:73],v[58:59],v[54:55] op_sel:[0,1,0] op_sel_hi:[1,1,0]\n"
"v_pk_fma_f32 v[74:75],v[74:75],v[58:59],v[54:55] op_sel:[0,1,0] op_sel_hi:[1,1,0]\n"
"v_pk_fma_f32 v[76:77],v[76:77],v[58:59],v[54:55] op_sel:[0,1,0] op_sel_hi:[1,1,0]\n"
"v_pk_fma_f32 v[78:79],v[78:79],v[58:59],v[54:55] op_sel:[0,1,0] op_sel_hi:[1,1,0]\n"
CV "v112,v72,v73\n"
CV "v113,v74,v75\n"
CV "v114,v76,v77\n"
CV "v115,v78,v79\n"
MF "a[16:19],v[136:139],v[108:111],a[16:19]\n"
"ds_read_b128 v[192:195],v120 offset:32768\n"
"ds_write_b128 v120,v[112:115] offset:32768\n"
"s_waitcnt lgkmcnt(0)\n"
"s_barrier\n"
LD "v[240:243],v9,s[40:43],s75 offen offset:2048 sc1\n"
AR "v80,a12\n"
AR "v81,a13\n"
AR "v82,a14\n"
MF "a[36:39],v[140:143],v[108:111],a[36:39]\n"
AR "v83,a15\n"
AR "v84,a32\n"
AR "v85,a33\n"
AR "v86,a34\n"
AR "v87,a35\n"
"v_pk_fma_f32 v[80:81],v[80:81],v[60:61],v[54:55] op_sel:[0,0,1] op_sel_hi:[1,0,1]\n"
"v_pk_fma_f32 v[82:83],v[82:83],v[60:61],v[54:55] op_sel:[0,0,1] op_sel_hi:[1,0,1]\n"
"v_pk_fma_f32 v[84:85],v[84:85],v[60:61],v[54:55] op_sel:[0,0,1] op_sel_hi:[1,0,1]\n"
"v_pk_fma_f32 v[86:87],v[86:87],v[60:61],v[54:55] op_sel:[0,0,1] op_sel_hi:[1,0,1]\n"
CV "v116,v80,v81\n"
CV "v117,v82,v83\n"
CV "v118,v84,v85\n"
CV "v119,v86,v87\n"
MF "a[40:43],v[144:147],v[92:95],a[40:43]\n"
"ds_read_b128 v[196:199],v120 offset:36864\n"
"ds_write_b128 v120,v[116:119] offset:36864\n"
LD "v[244:247],v9,s[40:43],s75 offen offset:3072 sc1\n"
AR "v72,a16\n"
AR "v73,a17\n"
AR "v74,a18\n"
MF "a[60:63],v[148:151],v[92:95],a[60:63]\n"
AR "v75,a19\n"
AR "v76,a36\n"
AR "v77,a37\n"
AR "v78,a38\n"
AR "v79,a39\n"
"v_pk_fma_f32 v[72:73],v[72:73],v[60:61],v[56:57] op_sel:[0,1,0] op_sel_hi:[1,1,0]\n"
"v_pk_fma_f32 v[74:75],v[74:75],v[60:61],v[56:57] op_sel:[0,1,0] op_sel_hi:[1,1,0]\n"
"v_pk_fma_f32 v[76:77],v[76:77],v[60:61],v[56:57] op_sel:[0,1,0] op_sel_hi:[1,1,0]\n"
"v_pk_fma_f32 v[78:79],v[78:79],v[60:61],v[56:57] op_sel:[0,1,0] op_sel_hi:[1,1,0]\n"
CV "v112,v72,v73\n"
CV "v113,v74,v75\n"
CV "v114,v76,v77\n"
CV "v115,v78,v79\n"
MF "a[44:47],v[144:147],v[96:99],a[44:47]\n"
"ds_read_b128 v[200:203],v120 offset:40960\n"
"ds_write_b128 v120,v[112:115] offset:40960\n"
AR "v80,a40\n"
AR "v81,a41\n"
AR "v82,a42\n"
MF "a[64:67],v[148:151],v[96:99],a[64:67]\n"
AR "v83,a43\n"
AR "v84,a60\n"
AR "v85,a61\n"
AR "v86,a62\n"
AR "v87,a63\n"
"v_pk_fma_f32 v[80:81],v[80:81],v[56:57],v[52:53] op_sel:[0,1,0] op_sel_hi:[1,1,0]\n"
"v_pk_fma_f32 v[82:83],v[82:83],v[56:57],v[52:53] op_sel:[0,1,0] op_sel_hi:[1,1,0]\n"
"v_pk_fma_f32 v[84:85],v[84:85],v[56:57],v[52:53] op_sel:[0,1,0] op_sel_hi:[1,1,0]\n"
"v_pk_fma_f32 v[86:87],v[86:87],v[56:57],v[52:53] op_sel:[0,1,0] op_sel_hi:[1,1,0]\n"
CV "v116,v80,v81\n"
CV "v117,v82,v83\n"
CV "v118,v84,v85\n"
CV "v119,v86,v87\n"
MF "a[48:51],v[144:147],v[100:103],a[48:51]\n"
"ds_read_b128 v[204:207],v120 offset:45056\n"
"ds_write_b128 v120,v[116:119] offset:45056\n"
"s_add_u32 s58,s75,4096\n"
LD "v[248:251],v9,s[40:43],s58 offen sc1\n"
AR "v72,a44\n"
AR "v73,a45\n"
AR "v74,a46\n"
MF "a[68:71],v[148:151],v[100:103],a[68:71]\n"
AR "v75,a47\n"
AR "v76,a64\n"
AR "v77,a65\n"
AR "v78,a66\n"
AR "v79,a67\n"
"v_pk_fma_f32 v[72:73],v[72:73],v[58:59],v[52:53] op_sel:[0,0,1] op_sel_hi:[1,0,1]\n"
"v_pk_fma_f32 v[74:75],v[74:75],v[58:59],v[52:53] op_sel:[0,0,1] op_sel_hi:[1,0,1]\n"
"v_pk_fma_f32 v[76:77],v[76:77],v[58:59],v[52:53] op_sel:[0,0,1] op_sel_hi:[1,0,1]\n"
"v_pk_fma_f32 v[78:79],v[78:79],v[58:59],v[52:53] op_sel:[0,0,1] op_sel_hi:[1,0,1]\n"
CV "v112,v72,v73\n"
CV "v113,v74,v75\n"
CV "v114,v76,v77\n"
CV "v115,v78,v79\n"
MF "a[52:55],v[144:147],v[104:107],a[52:55]\n"
"ds_read_b128 v[208:211],v120 offset:49152\n"
"ds_write_b128 v120,v[112:115] offset:49152\n"
LD "v[252:255],v9,s[40:43],s58 offen offset:1024 sc1\n"
AR "v80,a48\n"
AR "v81,a49\n"
AR "v82,a50\n"
MF "a[72:75],v[148:151],v[104:107],a[72:75]\n"
AR "v83,a51\n"
AR "v84,a68\n"
AR "v85,a69\n"
AR "v86,a70\n"
AR "v87,a71\n"
"v_pk_fma_f32 v[80:81],v[80:81],v[58:59],v[54:55] op_sel:[0,1,0] op_sel_hi:[1,1,0]\n"
"v_pk_fma_f32 v[82:83],v[82:83],v[58:59],v[54:55] op_sel:[0,1,0] op_sel_hi:[1,1,0]\n"
"v_pk_fma_f32 v[84:85],v[84:85],v[58:59],v[54:55] op_sel:[0,1,0] op_sel_hi:[1,1,0]\n"
"v_pk_fma_f32 v[86:87],v[86:87],v[58:59],v[54:55] op_sel:[0,1,0] op_sel_hi:[1,1,0]\n"
CV "v116,v80,v81\n"
CV "v117,v82,v83\n"
CV "v118,v84,v85\n"
CV "v119,v86,v87\n"
MF "a[56:59],v[144:147],v[108:111],a[56:59]\n"
"ds_read_b128 v[212:215],v120 offset:53248\n"
"ds_write_b128 v120,v[116:119] offset:53248\n"
AR "v72,a52\n"
AR "v73,a53\n"
AR "v74,a54\n"
MF "a[76:79],v[148:151],v[108:111],a[76:79]\n"
AR "v75,a55\n"
AR "v76,a72\n"
AR "v77,a73\n"
AR "v78,a74\n"
AR "v79,a75\n"
"v_pk_fma_f32 v[72:73],v[72:73],v[60:61],v[54:55] op_sel:[0,0,1] op_sel_hi:[1,0,1]\n"
"v_pk_fma_f32 v[74:75],v[74:75],v[60:61],v[54:55] op_sel:[0,0,1] op_sel_hi:[1,0,1]\n"
"v_pk_fma_f32 v[76:77],v[76:77],v[60:61],v[54:55] op_sel:[0,0,1] op_sel_hi:[1,0,1]\n"
"v_pk_fma_f32 v[78:79],v[78:79],v[60:61],v[54:55] op_sel:[0,0,1] op_sel_hi:[1,0,1]\n"
CV "v112,v72,v73\n"
CV "v113,v74,v75\n"
CV "v114,v76,v77\n"
CV "v115,v78,v79\n"
MF "a[80:83],v[152:155],v[92:95],a[80:83]\n"
"ds_read_b128 v[216:219],v120 offset:57344\n"
"ds_write_b128 v120,v[112:115] offset:57344\n"
AR "v80,a56\n"
AR "v81,a57\n"
AR "v82,a58\n"
MF "a[100:103],v[156:159],v[92:95],a[100:103]\n"
AR "v83,a59\n"
AR "v84,a76\n"
AR "v85,a77\n"
AR "v86,a78\n"
AR "v87,a79\n"
"v_pk_fma_f32 v[80:81],v[80:81],v[60:61],v[56:57] op_sel:[0,1,0] op_sel_hi:[1,1,0]\n"
"v_pk_fma_f32 v[82:83],v[82:83],v[60:61],v[56:57] op_sel:[0,1,0] op_sel_hi:[1,1,0]\n"
"v_pk_fma_f32 v[84:85],v[84:85],v[60:61],v[56:57] op_sel:[0,1,0] op_sel_hi:[1,1,0]\n"
"v_pk_fma_f32 v[86:87],v[86:87],v[60:61],v[56:57] op_sel:[0,1,0] op_sel_hi:[1,1,0]\n"
CV "v116,v80,v81\n"
CV "v117,v82,v83\n"
CV "v118,v84,v85\n"
CV "v119,v86,v87\n"
MF "a[84:87],v[152:155],v[96:99],a[84:87]\n"
"ds_read_b128 v[220:223],v120 offset:61440\n"
"ds_write_b128 v120,v[116:119] offset:61440\n"
AR "v72,a80\n"
AR "v73,a81\n"
AR "v74,a82\n"
MF "a[104:107],v[156:159],v[96:99],a[104:107]\n"
AR "v75,a83\n"
AR "v76,a100\n"
AR "v77,a101\n"
AR "v78,a102\n"
AR "v79,a103\n"
"v_pk_fma_f32 v[72:73],v[72:73],v[56:57],v[52:53] op_sel:[0,1,0] op_sel_hi:[1,1,0]\n"
"v_pk_fma_f32 v[74:75],v[74:75],v[56:57],v[52:53] op_sel:[0,1,0] op_sel_hi:[1,1,0]\n"
"v_pk_fma_f32 v[76:77],v[76:77],v[56:57],v[52:53] op_sel:[0,1,0] op_sel_hi:[1,1,0]\n"
"v_pk_fma_f32 v[78:79],v[78:79],v[56:57],v[52:53] op_sel:[0,1,0] op_sel_hi:[1,1,0]\n"
CV "v112,v72,v73\n"
CV "v113,v74,v75\n"
CV "v114,v76,v77\n"
CV "v115,v78,v79\n"
MF "a[88:91],v[152:155],v[100:103],a[88:91]\n"
"ds_read_b128 v[224:227],v121\n"
"ds_write_b128 v121,v[112:115]\n"
AR "v80,a84\n"
AR "v81,a85\n"
AR "v82,a86\n"
MF "a[108:111],v[156:159],v[100:103],a[108:111]\n"
AR "v83,a87\n"
AR "v84,a104\n"
AR "v85,a105\n"
AR "v86,a106\n"
AR "v87,a107\n"
"v_pk_fma_f32 v[80:81],v[80:81],v[58:59],v[52:53] op_sel:[0,0,1] op_sel_hi:[1,0,1]\n"
"v_pk_fma_f32 v[82:83],v[82:83],v[58:59],v[52:53] op_sel:[0,0,1] op_sel_hi:[1,0,1]\n"
"v_pk_fma_f32 v[84:85],v[84:85],v[58:59],v[52:53] op_sel:[0,0,1] op_sel_hi:[1,0,1]\n"
"v_pk_fma_f32 v[86:87],v[86:87],v[58:59],v[52:53] op_sel:[0,0,1] op_sel_hi:[1,0,1]\n"
CV "v116,v80,v81\n"
CV "v117,v82,v83\n"
CV "v118,v84,v85\n"
CV "v119,v86,v87\n"
MF "a[92:95],v[152:155],v[104:107],a[92:95]\n"
"ds_read_b128 v[228:231],v121 offset:4096\n"
"ds_write_b128 v121,v[116:119] offset:4096\n"
AR "v72,a88\n"
AR "v73,a89\n"
AR "v74,a90\n"
MF "a[112:115],v[156:159],v[104:107],a[112:115]\n"
AR "v75,a91\n"
AR "v76,a108\n"
AR "v77,a109\n"
AR "v78,a110\n"
AR "v79,a111\n"
"v_pk_fma_f32 v[72:73],v[72:73],v[58:59],v[54:55] op_sel:[0,1,0] op_sel_hi:[1,1,0]\n"
"v_pk_fma_f32 v[74:75],v[74:75],v[58:59],v[54:55] op_sel:[0,1,0] op_sel_hi:[1,1,0]\n"
"v_pk_fma_f32 v[76:77],v[76:77],v[58:59],v[54:55] op_sel:[0,1,0] op_sel_hi:[1,1,0]\n"
"v_pk_fma_f32 v[78:79],v[78:79],v[58:59],v[54:55] op_sel:[0,1,0] op_sel_hi:[1,1,0]\n"
CV "v112,v72,v73\n"
CV "v113,v74,v75\n"
CV "v114,v76,v77\n"
CV "v115,v78,v79\n"
MF "a[96:99],v[152:155],v[108:111],a[96:99]\n"
"ds_read_b128 v[136:139],v121 offset:8192\n"
"ds_write_b128 v121,v[112:115] offset:8192\n"
AR "v80,a92\n"
AR "v81,a93\n"
AR "v82,a94\n"
MF "a[116:119],v[156:159],v[108:111],a[116:119]\n"
AR "v83,a95\n"
AR "v84,a112\n"
AR "v85,a113\n"
AR "v86,a114\n"
AR "v87,a115\n"
"v_pk_fma_f32 v[80:81],v[80:81],v[60:61],v[54:55] op_sel:[0,0,1] op_sel_hi:[1,0,1]\n"
"v_pk_fma_f32 v[82:83],v[82:83],v[60:61],v[54:55] op_sel:[0,0,1] op_sel_hi:[1,0,1]\n"
"v_pk_fma_f32 v[84:85],v[84:85],v[60:61],v[54:55] op_sel:[0,0,1] op_sel_hi:[1,0,1]\n"
"v_pk_fma_f32 v[86:87],v[86:87],v[60:61],v[54:55] op_sel:[0,0,1] op_sel_hi:[1,0,1]\n"
CV "v116,v80,v81\n"
CV "v117,v82,v83\n"
CV "v118,v84,v85\n"
CV "v119,v86,v87\n"
MF "a[120:123],v[160:163],v[92:95],a[120:123]\n"
"ds_read_b128 v[140:143],v121 offset:12288\n"
"ds_write_b128 v121,v[116:119] offset:12288\n"
AR "v72,a96\n"
AR "v73,a97\n"
AR "v74,a98\n"
MF "a[140:143],v[164:167],v[92:95],a[140:143]\n"
AR "v75,a99\n"
AR "v76,a116\n"
AR "v77,a117\n"
AR "v78,a118\n"
AR "v79,a119\n"
"v_pk_fma_f32 v[72:73],v[72:73],v[60:61],v[56:57] op_sel:[0,1,0] op_sel_hi:[1,1,0]\n"
"v_pk_fma_f32 v[74:75],v[74:75],v[60:61],v[56:57] op_sel:[0,1,0] op_sel_hi:[1,1,0]\n"
"v_pk_fma_f32 v[76:77],v[76:77],v[60:61],v[56:57] op_sel:[0,1,0] op_sel_hi:[1,1,0]\n"
"v_pk_fma_f32 v[78:79],v[78:79],v[60:61],v[56:57] op_sel:[0,1,0] op_sel_hi:[1,1,0]\n"
CV "v112,v72,v73\n"
CV "v113,v74,v75\n"
CV "v114,v76,v77\n"
CV "v115,v78,v79\n"
MF "a[124:127],v[160:163],v[96:99],a[124:127]\n"
"ds_read_b128 v[144:147],v121 offset:16384\n"
"ds_write_b128 v121,v[112:115] offset:16384\n"
AR "v80,a120\n"
AR "v81,a121\n"
AR "v82,a122\n"
MF "a[144:147],v[164:167],v[96:99],a[144:147]\n"
AR "v83,a123\n"
AR "v84,a140\n"
AR "v85,a141\n"
AR "v86,a142\n"
AR "v87,a143\n"
"v_pk_fma_f32 v[80:81],v[80:81],v[56:57],v[52:53] op_sel:[0,1,0] op_sel_hi:[1,1,0]\n"
"v_pk_fma_f32 v[82:83],v[82:83],v[56:57],v[52:53] op_sel:[0,1,0] op_sel_hi:[1,1,0]\n"
"v_pk_fma_f32 v[84:85],v[84:85],v[56:57],v[52:53] op_sel:[0,1,0] op_sel_hi:[1,1,0]\n"
"v_pk_fma_f32 v[86:87],v[86:87],v[56:57],v[52:53] op_sel:[0,1,0] op_sel_hi:[1,1,0]\n"
CV "v116,v80,v81\n"
CV "v117,v82,v83\n"
CV "v118,v84,v85\n"
CV "v119,v86,v87\n"
MF "a[128:131],v[160:163],v[100:103],a[128:131]\n"
"ds_read_b128 v[148:151],v121 offset:20480\n"
"ds_write_b128 v121,v[116:119] offset:20480\n"
AR "v72,a124\n"
AR "v73,a125\n"
AR "v74,a126\n"
MF "a[148:151],v[164:167],v[100:103],a[148:151]\n"
AR "v75,a127\n"
AR "v76,a144\n"
AR "v77,a145\n"
AR "v78,a146\n"
AR "v79,a147\n"
"v_pk_fma_f32 v[72:73],v[72:73],v[58:59],v[52:53] op_sel:[0,0,1] op_sel_hi:[1,0,1]\n"
"v_pk_fma_f32 v[74:75],v[74:75],v[58:59],v[52:53] op_sel:[0,0,1] op_sel_hi:[1,0,1]\n"
"v_pk_fma_f32 v[76:77],v[76:77],v[58:59],v[52:53] op_sel:[0,0,1] op_sel_hi:[1,0,1]\n"
"v_pk_fma_f32 v[78:79],v[78:79],v[58:59],v[52:53] op_sel:[0,0,1] op_sel_hi:[1,0,1]\n"
CV "v112,v72,v73\n"
CV "v113,v74,v75\n"
CV "v114,v76,v77\n"
CV "v115,v78,v79\n"
MF "a[132:135],v[160:163],v[104:107],a[132:135]\n"
"ds_write_b128 v120,v[112:115]\n"
AR "v80,a128\n"
AR "v81,a129\n"
AR "v82,a130\n"
MF "a[152:155],v[164:167],v[104:107],a[152:155]\n"
AR "v83,a131\n"
AR "v84,a148\n"
AR "v85,a149\n"
AR "v86,a150\n"
AR "v87,a151\n"
"v_pk_fma_f32 v[80:81],v[80:81],v[58:59],v[54:55] op_sel:[0,1,0] op_sel_hi:[1,1,0]\n"
"v_pk_fma_f32 v[82:83],v[82:83],v[58:59],v[54:55] op_sel:[0,1,0] op_sel_hi:[1,1,0]\n"
"v_pk_fma_f32 v[84:85],v[84:85],v[58:59],v[54:55] op_sel:[0,1,0] op_sel_hi:[1,1,0]\n"
"v_pk_fma_f32 v[86:87],v[86:87],v[58:59],v[54:55] op_sel:[0,1,0] op_sel_hi:[1,1,0]\n"
CV "v116,v80,v81\n"
CV "v117,v82,v83\n"
CV "v118,v84,v85\n"
CV "v119,v86,v87\n"
MF "a[136:139],v[160:163],v[108:111],a[136:139]\n"
"ds_write_b128 v120,v[116:119] offset:4096\n"
AR "v72,a132\n"
AR "v73,a133\n"
AR "v74,a134\n"
MF "a[156:159],v[164:167],v[108:111],a[156:159]\n"
AR "v75,a135\n"
AR "v76,a152\n"
AR "v77,a153\n"
AR "v78,a154\n"
AR "v79,a155\n"
"v_pk_fma_f32 v[72:73],v[72:73],v[60:61],v[54:55] op_sel:[0,0,1] op_sel_hi:[1,0,1]\n"
"v_pk_fma_f32 v[74:75],v[74:75],v[60:61],v[54:55] op_sel:[0,0,1] op_sel_hi:[1,0,1]\n"
"v_pk_fma_f32 v[76:77],v[76:77],v[60:61],v[54:55] op_sel:[0,0,1] op_sel_hi:[1,0,1]\n"
"v_pk_fma_f32 v[78:79],v[78:79],v[60:61],v[54:55] op_sel:[0,0,1] op_sel_hi:[1,0,1]\n"
CV "v112,v72,v73\n"
CV "v113,v74,v75\n"
CV "v114,v76,v77\n"
CV "v115,v78,v79\n"
MF "a[160:163],v[168:171],v[92:95],a[160:163]\n"
"ds_write_b128 v120,v[112:115] offset:8192\n"
AR "v80,a136\n"
AR "v81,a137\n"
AR "v82,a138\n"
MF "a[180:183],v[172:175],v[92:95],a[180:183]\n"
AR "v83,a139\n"
AR "v84,a156\n"
AR "v85,a157\n"
AR "v86,a158\n"
AR "v87,a159\n"
"v_pk_fma_f32 v[80:81],v[80:81],v[60:61],v[56:57] op_sel:[0,1,0] op_sel_hi:[1,1,0]\n"
"v_pk_fma_f32 v[82:83],v[82:83],v[60:61],v[56:57] op_sel:[0,1,0] op_sel_hi:[1,1,0]\n"
"v_pk_fma_f32 v[84:85],v[84:85],v[60:61],v[56:57] op_sel:[0,1,0] op_sel_hi:[1,1,0]\n"
"v_pk_fma_f32 v[86:87],v[86:87],v[60:61],v[56:57] op_sel:[0,1,0] op_sel_hi:[1,1,0]\n"
CV "v116,v80,v81\n"
CV "v117,v82,v83\n"
CV "v118,v84,v85\n"
CV "v119,v86,v87\n"
MF "a[164:167],v[168:171],v[96:99],a[164:167]\n"
"ds_write_b128 v120,v[116:119] offset:12288\n"
AR "v72,a160\n"
AR "v73,a161\n"
AR "v74,a162\n"
MF "a[184:187],v[172:175],v[96:99],a[184:187]\n"
AR "v75,a163\n"
AR "v76,a180\n"
AR "v77,a181\n"
AR "v78,a182\n"
AR "v79,a183\n"
"v_pk_fma_f32 v[72:73],v[72:73],v[56:57],v[52:53] op_sel:[0,1,0] op_sel_hi:[1,1,0]\n"
"v_pk_fma_f32 v[74:75],v[74:75],v[56:57],v[52:53] op_sel:[0,1,0] op_sel_hi:[1,1,0]\n"
"v_pk_fma_f32 v[76:77],v[76:77],v[56:57],v[52:53] op_sel:[0,1,0] op_sel_hi:[1,1,0]\n"
"v_pk_fma_f32 v[78:79],v[78:79],v[56:57],v[52:53] op_sel:[0,1,0] op_sel_hi:[1,1,0]\n"
CV "v112,v72,v73\n"
CV "v113,v74,v75\n"
CV "v114,v76,v77\n"
CV "v115,v78,v79\n"
MF "a[168:171],v[168:171],v[100:103],a[168:171]\n"
"ds_write_b128 v120,v[112:115] offset:16384\n"
AR "v80,a164\n"
AR "v81,a165\n"
AR "v82,a166\n"
MF "a[188:191],v[172:175],v[100:103],a[188:191]\n"
AR "v83,a167\n"
AR "v84,a184\n"
AR "v85,a185\n"
AR "v86,a186\n"
AR "v87,a187\n"
"v_pk_fma_f32 v[80:81],v[80:81],v[58:59],v[52:53] op_sel:[0,0,1] op_sel_hi:[1,0,1]\n"
"v_pk_fma_f32 v[82:83],v[82:83],v[58:59],v[52:53] op_sel:[0,0,1] op_sel_hi:[1,0,1]\n"
"v_pk_fma_f32 v[84:85],v[84:85],v[58:59],v[52:53] op_sel:[0,0,1] op_sel_hi:[1,0,1]\n"
"v_pk_fma_f32 v[86:87],v[86:87],v[58:59],v[52:53] op_sel:[0,0,1] op_sel_hi:[1,0,1]\n"
CV "v116,v80,v81\n"
CV "v117,v82,v83\n"
CV "v118,v84,v85\n"
CV "v119,v86,v87\n"
MF "a[172:175],v[168:171],v[104:107],a[172:175]\n"
"ds_write_b128 v120,v[116:119] offset:20480\n"
AR "v72,a168\n"
AR "v73,a169\n"
AR "v74,a170\n"
MF "a[192:195],v[172:175],v[104:107],a[192:195]\n"
AR "v75,a171\n"
AR "v76,a188\n"
AR "v77,a189\n"
AR "v78,a190\n"
AR "v79,a191\n"
"v_pk_fma_f32 v[72:73],v[72:73],v[58:59],v[54:55] op_sel:[0,1,0] op_sel_hi:[1,1,0]\n"
"v_pk_fma_f32 v[74:75],v[74:75],v[58:59],v[54:55] op_sel:[0,1,0] op_sel_hi:[1,1,0]\n"
"v_pk_fma_f32 v[76:77],v[76:77],v[58:59],v[54:55] op_sel:[0,1,0] op_sel_hi:[1,1,0]\n"
"v_pk_fma_f32 v[78:79],v[78:79],v[58:59],v[54:55] op_sel:[0,1,0] op_sel_hi:[1,1,0]\n"
CV "v112,v72,v73\n"
CV "v113,v74,v75\n"
CV "v114,v76,v77\n"
CV "v115,v78,v79\n"
MF "a[176:179],v[168:171],v[108:111],a[176:179]\n"
AR "v152,a240\n"
AW "a240,v112\n"
AR "v153,a241\n"
AW "a241,v113\n"
AR "v154,a242\n"
AW "a242,v114\n"
AR "v155,a243\n"
AW "a243,v115\n"
AR "v80,a172\n"
AR "v81,a173\n"
AR "v82,a174\n"
MF "a[196:199],v[172:175],v[108:111],a[196:199]\n"
AR "v83,a175\n"
AR "v84,a192\n"
AR "v85,a193\n"
AR "v86,a194\n"
AR "v87,a195\n"
"v_pk_fma_f32 v[80:81],v[80:81],v[60:61],v[54:55] op_sel:[0,0,1] op_sel_hi:[1,0,1]\n"
"v_pk_fma_f32 v[82:83],v[82:83],v[60:61],v[54:55] op_sel:[0,0,1] op_sel_hi:[1,0,1]\n"
"v_pk_fma_f32 v[84:85],v[84:85],v[60:61],v[54:55] op_sel:[0,0,1] op_sel_hi:[1,0,1]\n"
"v_pk_fma_f32 v[86:87],v[86:87],v[60:61],v[54:55] op_sel:[0,0,1] op_sel_hi:[1,0,1]\n"
CV "v116,v80,v81\n"
CV "v117,v82,v83\n"
CV "v118,v84,v85\n"
CV "v119,v86,v87\n"
MF "a[200:203],v[176:179],v[92:95],a[200:203]\n"
AR "v156,a244\n"
AW "a244,v116\n"
AR "v157,a245\n"
AW "a245,v117\n"
AR "v158,a246\n"
AW "a246,v118\n"
AR "v159,a247\n"
AW "a247,v119\n"
AR "v72,a176\n"
AR "v73,a177\n"
AR "v74,a178\n"
MF "a[220:223],v[180:183],v[92:95],a[220:223]\n"
AR "v75,a179\n"
AR "v76,a196\n"
AR "v77,a197\n"
AR "v78,a198\n"
AR "v79,a199\n"
"v_pk_fma_f32 v[72:73],v[72:73],v[60:61],v[56:57] op_sel:[0,1,0] op_sel_hi:[1,1,0]\n"
"v_pk_fma_f32 v[74:75],v[74:75],v[60:61],v[56:57] op_sel:[0,1,0] op_sel_hi:[1,1,0]\n"
"v_pk_fma_f32 v[76:77],v[76:77],v[60:61],v[56:57] op_sel:[0,1,0] op_sel_hi:[1,1,0]\n"
"v_pk_fma_f32 v[78:79],v[78:79],v[60:61],v[56:57] op_sel:[0,1,0] op_sel_hi:[1,1,0]\n"
CV "v112,v72,v73\n"
CV "v113,v74,v75\n"
CV "v114,v76,v77\n"
CV "v115,v78,v79\n"
MF "a[204:207],v[176:179],v[96:99],a[204:207]\n"
AR "v160,a248\n"
AW "a248,v112\n"
AR "v161,a249\n"
AW "a249,v113\n"
AR "v162,a250\n"
AW "a250,v114\n"
AR "v163,a251\n"
AW "a251,v115\n"
AR "v80,a200\n"
AR "v81,a201\n"
AR "v82,a202\n"
MF "a[224:227],v[180:183],v[96:99],a[224:227]\n"
AR "v83,a203\n"
AR "v84,a220\n"
AR "v85,a221\n"
AR "v86,a222\n"
AR "v87,a223\n"
"v_pk_fma_f32 v[80:81],v[80:81],v[56:57],v[52:53] op_sel:[0,1,0] op_sel_hi:[1,1,0]\n"
"v_pk_fma_f32 v[82:83],v[82:83],v[56:57],v[52:53] op_sel:[0,1,0] op_sel_hi:[1,1,0]\n"
"v_pk_fma_f32 v[84:85],v[84:85],v[56:57],v[52:53] op_sel:[0,1,0] op_sel_hi:[1,1,0]\n"
"v_pk_fma_f32 v[86:87],v[86:87],v[56:57],v[52:53] op_sel:[0,1,0] op_sel_hi:[1,1,0]\n"
CV "v116,v80,v81\n"
CV "v117,v82,v83\n"
CV "v118,v84,v85\n"
CV "v119,v86,v87\n"
MF "a[208:211],v[176:179],v[100:103],a[208:211]\n"
"v_mov_b32 v164,v62\n"
"v_mov_b32 v62,v116\n"
"v_mov_b32 v165,v63\n"
"v_mov_b32 v63,v117\n"
"v_mov_b32 v166,v64\n"
"v_mov_b32 v64,v118\n"
"v_mov_b32 v167,v65\n"
"v_mov_b32 v65,v119\n"
AR "v72,a204\n"
AR "v73,a205\n"
AR "v74,a206\n"
MF "a[228:231],v[180:183],v[100:103],a[228:231]\n"
AR "v75,a207\n"
AR "v76,a224\n"
AR "v77,a225\n"
AR "v78,a226\n"
AR "v79,a227\n"
"v_pk_fma_f32 v[72:73],v[72:73],v[58:59],v[52:53] op_sel:[0,0,1] op_sel_hi:[1,0,1]\n"
"v_pk_fma_f32 v[74:75],v[74:75],v[58:59],v[52:53] op_sel:[0,0,1] op_sel_hi:[1,0,1]\n"
"v_pk_fma_f32 v[76:77],v[76:77],v[58:59],v[52:53] op_sel:[0,0,1] op_sel_hi:[1,0,1]\n"
"v_pk_fma_f32 v[78:79],v[78:79],v[58:59],v[52:53] op_sel:[0,0,1] op_sel_hi:[1,0,1]\n"
CV "v112,v72,v73\n"
CV "v113,v74,v75\n"
CV "v114,v76,v77\n"
CV "v115,v78,v79\n"
MF "a[212:215],v[176:179],v[104:107],a[212:215]\n"
"v_mov_b32 v168,v66\n"
"v_mov_b32 v66,v112\n"
"v_mov_b32 v169,v67\n"
"v_mov_b32 v67,v113\n"
"v_mov_b32 v170,v68\n"
"v_mov_b32 v68,v114\n"
"v_mov_b32 v171,v69\n"
"v_mov_b32 v69,v115\n"
AR "v80,a208\n"
AR "v81,a209\n"
AR "v82,a210\n"
MF "a[232:235],v[180:183],v[104:107],a[232:235]\n"
AR "v83,a211\n"
AR "v84,a228\n"
AR "v85,a229\n"
AR "v86,a230\n"
AR "v87,a231\n"
"v_pk_fma_f32 v[80:81],v[80:81],v[58:59],v[54:55] op_sel:[0,1,0] op_sel_hi:[1,1,0]\n"
"v_pk_fma_f32 v[82:83],v[82:83],v[58:59],v[54:55] op_sel:[0,1,0] op_sel_hi:[1,1,0]\n"
"v_pk_fma_f32 v[84:85],v[84:85],v[58:59],v[54:55] op_sel:[0,1,0] op_sel_hi:[1,1,0]\n"
"v_pk_fma_f32 v[86:87],v[86:87],v[58:59],v[54:55] op_sel:[0,1,0] op_sel_hi:[1,1,0]\n"
CV "v116,v80,v81\n"
CV "v117,v82,v83\n"
CV "v118,v84,v85\n"
CV "v119,v86,v87\n"
MF "a[216:219],v[176:179],v[108:111],a[216:219]\n"
"v_mov_b32 v172,v70\n"
"v_mov_b32 v70,v116\n"
"v_mov_b32 v173,v71\n"
"v_mov_b32 v71,v117\n"
"v_mov_b32 v174,v132\n"
"v_mov_b32 v132,v118\n"
"v_mov_b32 v175,v133\n"
"v_mov_b32 v133,v119\n"
AR "v72,a212\n"
AR "v73,a213\n"
AR "v74,a214\n"
MF "a[236:239],v[180:183],v[108:111],a[236:239]\n"
AR "v75,a215\n"
AR "v76,a232\n"
AR "v77,a233\n"
AR "v78,a234\n"
AR "v79,a235\n"
"v_pk_fma_f32 v[72:73],v[72:73],v[60:61],v[54:55] op_sel:[0,0,1] op_sel_hi:[1,0,1]\n"
"v_pk_fma_f32 v[74:75],v[74:75],v[60:61],v[54:55] op_sel:[0,0,1] op_sel_hi:[1,0,1]\n"
"v_pk_fma_f32 v[76:77],v[76:77],v[60:61],v[54:55] op_sel:[0,0,1] op_sel_hi:[1,0,1]\n"
"v_pk_fma_f32 v[78:79],v[78:79],v[60:61],v[54:55] op_sel:[0,0,1] op_sel_hi:[1,0,1]\n"
CV "v112,v72,v73\n"
CV "v113,v74,v75\n"
CV "v114,v76,v77\n"
CV "v115,v78,v79\n"
"v_mov_b32 v176,v134\n"
"v_mov_b32 v134,v112\n"
"v_mov_b32 v177,v135\n"
"v_mov_b32 v135,v113\n"
"v_mov_b32 v178,v6\n"
"v_mov_b32 v6,v114\n"
"v_mov_b32 v179,v13\n"
"v_mov_b32 v13,v115\n"
AR "v80,a216\n"
AR "v81,a217\n"
AR "v82,a218\n"
AR "v83,a219\n"
AR "v84,a236\n"
AR "v85,a237\n"
AR "v86,a238\n"
AR "v87,a239\n"
"v_pk_fma_f32 v[80:81],v[80:81],v[60:61],v[56:57] op_sel:[0,1,0] op_sel_hi:[1,1,0]\n"
"v_pk_fma_f32 v[82:83],v[82:83],v[60:61],v[56:57] op_sel:[0,1,0] op_sel_hi:[1,1,0]\n"
"v_pk_fma_f32 v[84:85],v[84:85],v[60:61],v[56:57] op_sel:[0,1,0] op_sel_hi:[1,1,0]\n"
"v_pk_fma_f32 v[86:87],v[86:87],v[60:61],v[56:57] op_sel:[0,1,0] op_sel_hi:[1,1,0]\n"
CV "v116,v80,v81\n"
CV "v117,v82,v83\n"
CV "v118,v84,v85\n"
CV "v119,v86,v87\n"
"v_mov_b32 v180,v14\n"
"v_mov_b32 v14,v116\n"
"v_mov_b32 v181,v15\n"
"v_mov_b32 v15,v117\n"
AR "v182,a254\n"
AW "a254,v118\n"
AR "v183,a255\n"
AW "a255,v119\n"
"v_subrev_u32 v2,49152,v2\n"
"v_subrev_u32 v3,49152,v3\n"
"v_subrev_u32 v4,49152,v4\n"
"v_subrev_u32 v5,49152,v5\n"
"s_mov_b32 s57,2\n"
"s_mov_b32 s69,1\n"
"s_mov_b32 s61,3\n"
"s_memrealtime s[100:101]\n"
"s_waitcnt lgkmcnt(0)\n"
"s_cmp_eq_u32 s87,1\n"
"s_cselect_b32 s97,0,0\n"
"s_cselect_b32 s21,215,0\n"
"s_add_u32 s21,s21,s100\n"
"s_waitcnt vmcnt(0)\n"
"s_branch .Latt2\n"
".LattB:\n"
AR "v0,a244\n"
AR "v2,a245\n"
"v_xor_b32 v1,64,v0\n"
"v_xor_b32 v3,32,v2\n"
"v_xor_b32 v4,64,v2\n"
"v_xor_b32 v5,96,v2\n"
"s_mov_b32 s61,3\n"
"s_memtime s[52:53]\n"
"s_memrealtime s[100:101]\n"
"s_waitcnt lgkmcnt(0)\n"
"s_sub_u32 s53,s52,s80\n"
"s_cmp_gt_u32 s53,27000\n"
"s_cselect_b32 s101,1,0\n"
"s_cmp_eq_u32 s87,2\n"
"s_cselect_b32 s87,s101,s87\n"
"s_cmp_eq_u32 s87,1\n"
"s_cselect_b32 s97,0,0\n"
"s_cselect_b32 s21,215,0\n"
"s_add_u32 s21,s21,s100\n"
".Latt2:\n"
"s_cmp_eq_u32 s97,0\n"
"s_cselect_b32 s95,0x47800000,0\n"
"s_mov_b32 s77,s95\n"
"s_waitcnt vmcnt(12) lgkmcnt(0)\n"
"s_barrier\n"
"ds_read_b128 v[16:19],v0\n"
"ds_read_b128 v[20:23],v1\n"
"ds_read_b128 v[24:27],v0 offset:1024\n"
"s_cmp_eq_u32 s57,1\n"
"s_cbranch_scc1 .Lt0k\n"
"s_waitcnt lgkmcnt(2)\n"
MF "v[72:75],v[16:19],v[136:139],0\n"
"ds_read_b128 v[28:31],v1 offset:1024\n"
MF "v[76:79],v[16:19],v[140:143],0\n"
MF "v[80:83],v[16:19],v[144:147],0\n"
MF "v[84:87],v[16:19],v[148:151],0\n"
MF "v[88:91],v[16:19],v[152:155],0\n"
"s_waitcnt lgkmcnt(2)\n"
"ds_read_b128 v[16:19],v0 offset:2048\n"
MF "v[72:75],v[20:23],v[156:159],v[72:75]\n"
MF "v[76:79],v[20:23],v[160:163],v[76:79]\n"
MF "v[80:83],v[20:23],v[164:167],v[80:83]\n"
MF "v[84:87],v[20:23],v[168:171],v[84:87]\n"
MF "v[88:91],v[20:23],v[172:175],v[88:91]\n"
"s_waitcnt lgkmcnt(2)\n"
"ds_read_b128 v[20:23],v1 offset:2048\n"
MF "v[72:75],v[24:27],v[176:179],v[72:75]\n"
MF "v[76:79],v[24:27],v[180:183],v[76:79]\n"
MF "v[80:83],v[24:27],v[184:187],v[80:83]\n"
MF "v[84:87],v[24:27],v[188:191],v[84:87]\n"
MF "v[88:91],v[24:27],v[192:195],v[88:91]\n"
"s_waitcnt lgkmcnt(2)\n"
"ds_read_b128 v[24:27],v0 offset:6144\n"
MF "v[72:75],v[28:31],v[196:199],v[72:75]\n"
MF "v[76:79],v[28:31],v[200:203],v[76:79]\n"
MF "v[80:83],v[28:31],v[204:207],v[80:83]\n"
MF "v[84:87],v[28:31],v[208:211],v[84:87]\n"
MF "v[88:91],v[28:31],v[212:215],v[88:91]\n"
"s_waitcnt lgkmcnt(2)\n"
"ds_read_b128 v[28:31],v1 offset:6144\n"
MF "v[72:75],v[16:19],v[216:219],v[72:75]\n"
MF "v[76:79],v[16:19],v[220:223],v[76:79]\n"
MF "v[80:83],v[16:19],v[224:227],v[80:83]\n"
MF "v[84:87],v[16:19],v[228:231],v[84:87]\n"
MF "v[88:91],v[16:19],v[232:235],v[88:91]\n"
"s_waitcnt lgkmcnt(2)\n"
"ds_read_b128 v[16:19],v0 offset:7168\n"
MF "v[72:75],v[20:23],v[236:239],v[72:75]\n"
MF "v[76:79],v[20:23],v[240:243],v[76:79]\n"
MF "v[80:83],v[20:23],v[244:247],v[80:83]\n"
MF "v[84:87],v[20:23],v[248:251],v[84:87]\n"
MF "v[88:91],v[20:23],v[252:255],v[88:91]\n"
"s_waitcnt lgkmcnt(2)\n"
"ds_read_b128 v[20:23],v1 offset:7168\n"
MF "v[112:115],v[24:27],v[136:139],0\n"
MF "v[116:119],v[24:27],v[140:143],0\n"
MF "v[120:123],v[24:27],v[144:147],0\n"
MF "v[124:127],v[24:27],v[148:151],0\n"
MF "v[128:131],v[24:27],v[152:155],0\n"
"s_waitcnt lgkmcnt(2)\n"
"ds_read_b128 v[24:27],v0 offset:8192\n"
MF "v[112:115],v[28:31],v[156:159],v[112:115]\n"
"v_max3_f32 v52,v72,v73,v74\n"
"v_max_f32 v52,v52,v75\n"
MF "v[116:119],v[28:31],v[160:163],v[116:119]\n"
"v_max3_f32 v53,v76,v77,v78\n"
"v_max_f32 v53,v53,v79\n"
MF "v[120:123],v[28:31],v[164:167],v[120:123]\n"
"v_max3_f32 v54,v80,v81,v82\n"
"v_max_f32 v54,v54,v83\n"
MF "v[124:127],v[28:31],v[168:171],v[124:127]\n"
"v_max3_f32 v55,v84,v85,v86\n"
"v_max_f32 v55,v55,v87\n"
MF "v[128:131],v[28:31],v[172:175],v[128:131]\n"
"s_waitcnt lgkmcnt(2)\n"
"ds_read_b128 v[28:31],v1 offset:8192\n"
"v_max3_f32 v56,v88,v89,v90\n"
MF "v[112:115],v[16:19],v[176:179],v[112:115]\n"
"v_max_f32 v56,v56,v91\n"
MF "v[116:119],v[16:19],v[180:183],v[116:119]\n"
MF "v[120:123],v[16:19],v[184:187],v[120:123]\n"
MF "v[124:127],v[16:19],v[188:191],v[124:127]\n"
MF "v[128:131],v[16:19],v[192:195],v[128:131]\n"
"s_waitcnt lgkmcnt(2)\n"
MF "v[112:115],v[20:23],v[196:199],v[112:115]\n"
MF "v[116:119],v[20:23],v[200:203],v[116:119]\n"
"s_waitcnt lgkmcnt(0)\n"
"s_barrier\n"
MF "v[120:123],v[20:23],v[204:207],v[120:123]\n"
"s_add_u32 s72,s72,6144\n"
MF "v[124:127],v[20:23],v[208:211],v[124:127]\n"
"s_cmp_eq_u32 s72,s17\n"
"s_cselect_b32 s72,s16,s72\n"
"s_add_u32 m0,s64,0\n"
MF "v[128:131],v[20:23],v[212:215],v[128:131]\n"
LD "v7,s[32:35],s72 offen nt lds\n"
MF "v[112:115],v[24:27],v[216:219],v[112:115]\n"
"s_add_u32 m0,s64,896\n"
MF "v[116:119],v[24:27],v[220:223],v[116:119]\n"
LD "v7,s[32:35],s72 offen offset:128 nt lds\n"
MF "v[120:123],v[24:27],v[224:227],v[120:123]\n"
"s_add_u32 m0,s64,1792\n"
MF "v[124:127],v[24:27],v[228:231],v[124:127]\n"
LD "v7,s[32:35],s72 offen offset:256 nt lds\n"
MF "v[128:131],v[24:27],v[232:235],v[128:131]\n"
"s_add_u32 s72,s72,6144\n"
"s_add_u32 m0,s64,6144\n"
MF "v[112:115],v[28:31],v[236:239],v[112:115]\n"
LD "v7,s[32:35],s72 offen nt lds\n"
MF "v[116:119],v[28:31],v[240:243],v[116:119]\n"
"s_add_u32 m0,s64,7040\n"
MF "v[120:123],v[28:31],v[244:247],v[120:123]\n"
LD "v7,s[32:35],s72 offen offset:128 nt lds\n"
MF "v[124:127],v[28:31],v[248:251],v[124:127]\n"
"s_add_u32 m0,s64,7936\n"
MF "v[128:131],v[28:31],v[252:255],v[128:131]\n"
LD "v7,s[32:35],s72 offen offset:256 nt lds\n"
"s_branch .Lt0e\n"
".Lt0k:\n"
"s_lshl_b32 s58,s56,10\n"
"s_add_u32 s58,s58,73728\n"
"v_add_u32 v32,s58,v9\n"
"v_add_u32 v33,65536,v32\n"
"s_waitcnt lgkmcnt(2)\n"
MF "v[72:75],v[16:19],v[136:139],0\n"
"ds_read_b128 v[28:31],v1 offset:1024\n"
AR "v62,a20\n"
MF "v[76:79],v[16:19],v[140:143],0\n"
"ds_write_b128 v32,a[0:3] offset:24576\n"
MF "v[80:83],v[16:19],v[144:147],0\n"
"ds_write_b128 v32,a[4:7] offset:28672\n"
MF "v[84:87],v[16:19],v[148:151],0\n"
AR "v63,a21\n"
AR "v64,a22\n"
MF "v[88:91],v[16:19],v[152:155],0\n"
"s_waitcnt lgkmcnt(4)\n"
"ds_read_b128 v[16:19],v0 offset:2048\n"
AR "v65,a23\n"
MF "v[72:75],v[20:23],v[156:159],v[72:75]\n"
"ds_write_b128 v32,a[8:11] offset:32768\n"
MF "v[76:79],v[20:23],v[160:163],v[76:79]\n"
AR "v66,a24\n"
AR "v67,a25\n"
MF "v[80:83],v[20:23],v[164:167],v[80:83]\n"
"ds_write_b128 v32,a[12:15] offset:36864\n"
MF "v[84:87],v[20:23],v[168:171],v[84:87]\n"
AR "v68,a26\n"
AR "v69,a27\n"
MF "v[88:91],v[20:23],v[172:175],v[88:91]\n"
"s_waitcnt lgkmcnt(6)\n"
"ds_read_b128 v[20:23],v1 offset:2048\n"
AR "v70,a28\n"
MF "v[72:75],v[24:27],v[176:179],v[72:75]\n"
"ds_write_b128 v32,a[16:19] offset:40960\n"
MF "v[76:79],v[24:27],v[180:183],v[76:79]\n"
AR "v71,a29\n"
AR "v132,a30\n"
MF "v[80:83],v[24:27],v[184:187],v[80:83]\n"
AR "v133,a31\n"
AR "v134,a32\n"
MF "v[84:87],v[24:27],v[188:191],v[84:87]\n"
"ds_write_b128 v32,a[40:43] offset:45056\n"
MF "v[88:91],v[24:27],v[192:195],v[88:91]\n"
"s_waitcnt lgkmcnt(8)\n"
"ds_read_b128 v[24:27],v0 offset:6144\n"
AR "v135,a33\n"
MF "v[72:75],v[28:31],v[196:199],v[72:75]\n"
"ds_write_b128 v32,a[44:47] offset:49152\n"
MF "v[76:79],v[28:31],v[200:203],v[76:79]\n"
AR "v6,a34\n"
AR "v13,a35\n"
MF "v[80:83],v[28:31],v[204:207],v[80:83]\n"
AR "v14,a36\n"
AR "v15,a37\n"
MF "v[84:87],v[28:31],v[208:211],v[84:87]\n"
"ds_write_b128 v32,a[48:51] offset:53248\n"
MF "v[88:91],v[28:31],v[212:215],v[88:91]\n"
"s_waitcnt lgkmcnt(8)\n"
"ds_read_b128 v[28:31],v1 offset:6144\n"
"v_accvgpr_mov_b32 a254,a38\n"
MF "v[72:75],v[16:19],v[216:219],v[72:75]\n"
"v_accvgpr_mov_b32 a255,a39\n"
"v_accvgpr_mov_b32 a240,a168\n"
MF "v[76:79],v[16:19],v[220:223],v[76:79]\n"
"ds_write_b128 v32,a[52:55] offset:57344\n"
MF "v[80:83],v[16:19],v[224:227],v[80:83]\n"
"v_accvgpr_mov_b32 a241,a169\n"
"v_accvgpr_mov_b32 a242,a170\n"
MF "v[84:87],v[16:19],v[228:231],v[84:87]\n"
"ds_write_b128 v32,a[56:59] offset:61440\n"
MF "v[88:91],v[16:19],v[232:235],v[88:91]\n"
"s_waitcnt lgkmcnt(8)\n"
"ds_read_b128 v[16:19],v0 offset:7168\n"
"v_accvgpr_mov_b32 a243,a171\n"
MF "v[72:75],v[20:23],v[236:239],v[72:75]\n"
"v_accvgpr_mov_b32 a244,a172\n"
"v_accvgpr_mov_b32 a245,a173\n"
MF "v[76:79],v[20:23],v[240:243],v[76:79]\n"
"ds_write_b128 v33,a[80:83]\n"
MF "v[80:83],v[20:23],v[244:247],v[80:83]\n"
"v_accvgpr_mov_b32 a246,a174\n"
"v_accvgpr_mov_b32 a247,a175\n"
MF "v[84:87],v[20:23],v[248:251],v[84:87]\n"
"v_accvgpr_mov_b32 a248,a176\n"
"v_accvgpr_mov_b32 a249,a177\n"
MF "v[88:91],v[20:23],v[252:255],v[88:91]\n"
"s_waitcnt lgkmcnt(7)\n"
"ds_read_b128 v[20:23],v1 offset:7168\n"
"v_accvgpr_mov_b32 a250,a178\n"
MF "v[112:115],v[24:27],v[136:139],0\n"
"ds_write_b128 v33,a[84:87] offset:4096\n"
MF "v[116:119],v[24:27],v[140:143],0\n"
"ds_write_b128 v33,a[88:91] offset:8192\n"
MF "v[120:123],v[24:27],v[144:147],0\n"
"v_accvgpr_mov_b32 a251,a179\n"
MF "v[124:127],v[24:27],v[148:151],0\n"
MF "v[128:131],v[24:27],v[152:155],0\n"
"s_waitcnt lgkmcnt(7)\n"
"ds_read_b128 v[24:27],v0 offset:8192\n"
MF "v[112:115],v[28:31],v[156:159],v[112:115]\n"
"v_max3_f32 v52,v72,v73,v74\n"
"v_max_f32 v52,v52,v75\n"
MF "v[116:119],v[28:31],v[160:163],v[116:119]\n"
"v_max3_f32 v53,v76,v77,v78\n"
"v_max_f32 v53,v53,v79\n"
MF "v[120:123],v[28:31],v[164:167],v[120:123]\n"
"v_max3_f32 v54,v80,v81,v82\n"
"v_max_f32 v54,v54,v83\n"
MF "v[124:127],v[28:31],v[168:171],v[124:127]\n"
"v_max3_f32 v55,v84,v85,v86\n"
"v_max_f32 v55,v55,v87\n"
MF "v[128:131],v[28:31],v[172:175],v[128:131]\n"
"s_waitcnt lgkmcnt(5)\n"
"ds_read_b128 v[28:31],v1 offset:8192\n"
"v_max3_f32 v56,v88,v89,v90\n"
MF "v[112:115],v[16:19],v[176:179],v[112:115]\n"
"v_max_f32 v56,v56,v91\n"
MF "v[116:119],v[16:19],v[180:183],v[116:119]\n"
"ds_write_b128 v33,a[92:95] offset:12288\n"
MF "v[120:123],v[16:19],v[184:187],v[120:123]\n"
"ds_write_b128 v33,a[96:99] offset:16384\n"
MF "v[124:127],v[16:19],v[188:191],v[124:127]\n"
"ds_write_b128 v33,a[120:123] offset:20480\n"
MF "v[128:131],v[16:19],v[192:195],v[128:131]\n"
"s_waitcnt lgkmcnt(7)\n"
"ds_write_b128 v32,a[124:127]\n"
MF "v[112:115],v[20:23],v[196:199],v[112:115]\n"
"ds_write_b128 v32,a[128:131] offset:4096\n"
MF "v[116:119],v[20:23],v[200:203],v[116:119]\n"
"s_waitcnt lgkmcnt(0)\n"
"s_barrier\n"
MF "v[120:123],v[20:23],v[204:207],v[120:123]\n"
"s_add_u32 s72,s72,6144\n"
"ds_write_b128 v32,a[132:135] offset:8192\n"
MF "v[124:127],v[20:23],v[208:211],v[124:127]\n"
"s_cmp_eq_u32 s72,s17\n"
"s_cselect_b32 s72,s16,s72\n"
"s_add_u32 m0,s64,0\n"
MF "v[128:131],v[20:23],v[212:215],v[128:131]\n"
LD "v7,s[32:35],s72 offen nt lds\n"
MF "v[112:115],v[24:27],v[216:219],v[112:115]\n"
"s_add_u32 m0,s64,896\n"
"ds_write_b128 v32,a[136:139] offset:12288\n"
MF "v[116:119],v[24:27],v[220:223],v[116:119]\n"
LD "v7,s[32:35],s72 offen offset:128 nt lds\n"
MF "v[120:123],v[24:27],v[224:227],v[120:123]\n"
"s_add_u32 m0,s64,1792\n"
"ds_write_b128 v32,a[160:163] offset:16384\n"
MF "v[124:127],v[24:27],v[228:231],v[124:127]\n"
LD "v7,s[32:35],s72 offen offset:256 nt lds\n"
MF "v[128:131],v[24:27],v[232:235],v[128:131]\n"
"s_add_u32 s72,s72,6144\n"
"s_add_u32 m0,s64,6144\n"
MF "v[112:115],v[28:31],v[236:239],v[112:115]\n"
LD "v7,s[32:35],s72 offen nt lds\n"
MF "v[116:119],v[28:31],v[240:243],v[116:119]\n"
"s_add_u32 m0,s64,7040\n"
"ds_write_b128 v32,a[164:167] offset:20480\n"
MF "v[120:123],v[28:31],v[244:247],v[120:123]\n"
LD "v7,s[32:35],s72 offen offset:128 nt lds\n"
MF "v[124:127],v[28:31],v[248:251],v[124:127]\n"
"s_add_u32 m0,s64,7936\n"
MF "v[128:131],v[28:31],v[252:255],v[128:131]\n"
LD "v7,s[32:35],s72 offen offset:256 nt lds\n"
".Lt0e:\n"
"s_nop 1\n"
"v_max3_f32 v52,v52,v112,v113\n"
"v_max3_f32 v53,v53,v116,v117\n"
"v_max3_f32 v54,v54,v120,v121\n"
"s_nop 0\n"
"v_max3_f32 v55,v55,v124,v125\n"
"s_nop 0\n"
"v_max3_f32 v56,v56,v128,v129\n"
"v_max3_f32 v52,v52,v114,v115\n"
"v_max3_f32 v53,v53,v118,v119\n"
"v_max3_f32 v54,v54,v122,v123\n"
"v_max3_f32 v55,v55,v126,v127\n"
"v_max3_f32 v56,v56,v130,v131\n"
"v_mov_b32 v57,v52\n"
"v_mov_b32 v58,v53\n"
"v_mov_b32 v59,v54\n"
"v_mov_b32 v60,v55\n"
"v_mov_b32 v61,v56\n"
"v_permlane16_swap_b32 v52,v57\n"
"v_permlane16_swap_b32 v53,v58\n"
"v_permlane16_swap_b32 v54,v59\n"
"v_permlane16_swap_b32 v55,v60\n"
"v_permlane16_swap_b32 v56,v61\n"
"v_max_f32 v52,v52,v57\n"
"v_max_f32 v53,v53,v58\n"
"v_max_f32 v54,v54,v59\n"
"v_max_f32 v55,v55,v60\n"
"v_max_f32 v56,v56,v61\n"
"v_mov_b32 v57,v52\n"
"v_mov_b32 v58,v53\n"
"v_mov_b32 v59,v54\n"
"v_mov_b32 v60,v55\n"
"v_mov_b32 v61,v56\n"
"v_permlane32_swap_b32 v52,v57\n"
"v_permlane32_swap_b32 v53,v58\n"
"v_permlane32_swap_b32 v54,v59\n"
"v_permlane32_swap_b32 v55,v60\n"
"v_permlane32_swap_b32 v56,v61\n"
"v_max_f32 v52,v52,v57\n"
"v_max_f32 v53,v53,v58\n"
"v_max_f32 v54,v54,v59\n"
"v_max_f32 v55,v55,v60\n"
"v_max_f32_e64 v56,v56,v61\n"
"v_sub_f32 v32,0xc1300000,v52\n"
"v_sub_f32 v36,0xc1300000,v53\n"
"v_sub_f32 v40,0xc1300000,v54\n"
"v_sub_f32 v44,0xc1300000,v55\n"
"v_sub_f32 v48,0xc1300000,v56\n"
"v_mov_b32 v33,v32\n"
"v_mov_b32 v34,v32\n"
"v_mov_b32 v35,v32\n"
"v_mov_b32 v37,v36\n"
"v_mov_b32 v38,v36\n"
"v_mov_b32 v39,v36\n"
"v_mov_b32 v41,v40\n"
"v_mov_b32 v42,v40\n"
"v_mov_b32 v43,v40\n"
"v_mov_b32 v45,v44\n"
"v_mov_b32 v46,v44\n"
"v_mov_b32 v47,v44\n"
"v_mov_b32 v49,v48\n"
"v_mov_b32 v50,v48\n"
"v_mov_b32 v51,v48\n"
"v_add_f32 v72,v72,v32\n"
"v_add_f32 v73,v73,v32\n"
"v_add_f32 v74,v74,v32\n"
"v_add_f32 v75,v75,v32\n"
"v_add_f32 v112,v112,v32\n"
"v_add_f32 v113,v113,v32\n"
"v_add_f32 v114,v114,v32\n"
"v_add_f32 v115,v115,v32\n"
"v_exp_f32 v72,v72\n"
"v_exp_f32 v73,v73\n"
"v_exp_f32 v74,v74\n"
"v_exp_f32 v75,v75\n"
"v_exp_f32 v112,v112\n"
"v_exp_f32 v113,v113\n"
"v_exp_f32 v114,v114\n"
"v_exp_f32 v115,v115\n"
"v_add_f32 v52,v72,v73\n"
"v_add_f32 v52,v52,v74\n"
"v_add_f32 v52,v52,v75\n"
"v_add_f32 v52,v52,v112\n"
"v_add_f32 v52,v52,v113\n"
"v_add_f32 v52,v52,v114\n"
"v_add_f32 v52,v52,v115\n"
CV "v72,v72,v73\n"
CV "v73,v74,v75\n"
CV "v74,v112,v113\n"
CV "v75,v114,v115\n"
"v_add_f32 v76,v76,v36\n"
"v_add_f32 v77,v77,v36\n"
"v_add_f32 v78,v78,v36\n"
"v_add_f32 v79,v79,v36\n"
"v_add_f32 v116,v116,v36\n"
"v_add_f32 v117,v117,v36\n"
"v_add_f32 v118,v118,v36\n"
"v_add_f32 v119,v119,v36\n"
"v_exp_f32 v76,v76\n"
"v_exp_f32 v77,v77\n"
"v_exp_f32 v78,v78\n"
"v_exp_f32 v79,v79\n"
"v_exp_f32 v116,v116\n"
"v_exp_f32 v117,v117\n"
"v_exp_f32 v118,v118\n"
"v_exp_f32 v119,v119\n"
"v_add_f32 v53,v76,v77\n"
"v_add_f32 v53,v53,v78\n"
"v_add_f32 v53,v53,v79\n"
"v_add_f32 v53,v53,v116\n"
"v_add_f32 v53,v53,v117\n"
"v_add_f32 v53,v53,v118\n"
"v_add_f32_e64 v53,v53,v119\n"
CV "v76,v76,v77\n"
CV "v77,v78,v79\n"
CV "v78,v116,v117\n"
CV "v79,v118,v119\n"
"v_add_f32 v80,v80,v40\n"
"v_add_f32 v81,v81,v40\n"
"v_add_f32 v82,v82,v40\n"
"v_add_f32 v83,v83,v40\n"
"v_add_f32 v120,v120,v40\n"
"v_add_f32 v121,v121,v40\n"
"v_add_f32 v122,v122,v40\n"
"v_add_f32 v123,v123,v40\n"
"v_exp_f32 v80,v80\n"
"v_exp_f32 v81,v81\n"
"v_exp_f32 v82,v82\n"
"v_exp_f32 v83,v83\n"
"v_exp_f32 v120,v120\n"
"v_exp_f32 v121,v121\n"
"v_exp_f32 v122,v122\n"
"v_exp_f32 v123,v123\n"
"v_add_f32 v54,v80,v81\n"
"v_add_f32 v54,v54,v82\n"
"v_add_f32 v54,v54,v83\n"
"v_add_f32 v54,v54,v120\n"
"v_add_f32 v54,v54,v121\n"
"v_add_f32 v54,v54,v122\n"
"v_add_f32_e64 v54,v54,v123\n"
CV "v80,v80,v81\n"
CV "v81,v82,v83\n"
CV "v82,v120,v121\n"
CV "v83,v122,v123\n"
"v_add_f32 v84,v84,v44\n"
"v_add_f32 v85,v85,v44\n"
"v_add_f32 v86,v86,v44\n"
"v_add_f32 v87,v87,v44\n"
"v_add_f32 v124,v124,v44\n"
"v_add_f32 v125,v125,v44\n"
"v_add_f32 v126,v126,v44\n"
"v_add_f32 v127,v127,v44\n"
"v_mov_b32 v55,0\n"
"v_add_f32 v88,v88,v48\n"
"v_add_f32 v89,v89,v48\n"
"v_add_f32 v90,v90,v48\n"
"v_add_f32 v91,v91,v48\n"
"v_add_f32 v128,v128,v48\n"
"v_add_f32 v129,v129,v48\n"
"v_add_f32 v130,v130,v48\n"
"v_add_f32 v131,v131,v48\n"
"v_mov_b32 v56,0\n"
"s_waitcnt vmcnt(12) lgkmcnt(0)\n"
"s_barrier\n"
"ds_read_b128 v[16:19],v0 offset:24576\n"
"ds_read_b128 v[20:23],v1 offset:24576\n"
"ds_read_b128 v[24:27],v0 offset:25600\n"
AW "a220,s13\n"
AW "a221,s13\n"
AW "a222,s13\n"
AW "a223,s13\n"
"s_waitcnt lgkmcnt(2)\n"
MF "v[92:95],v[16:19],v[136:139],v[32:35]\n"
"ds_read_b128 v[28:31],v1 offset:25600\n"
"s_waitcnt lgkmcnt(2)\n"
MF "v[92:95],v[20:23],v[156:159],v[92:95]\n"
"v_exp_f32 v84,v84\n"
MF "v[96:99],v[20:23],v[160:163],v[36:39]\n"
"v_exp_f32 v85,v85\n"
MF "v[96:99],v[16:19],v[140:143],v[96:99]\n"
"v_exp_f32 v86,v86\n"
MF "v[100:103],v[16:19],v[144:147],v[40:43]\n"
"v_exp_f32 v87,v87\n"
MF "v[100:103],v[20:23],v[164:167],v[100:103]\n"
"v_add_f32 v55,v55,v84\n"
"v_add_f32 v55,v55,v85\n"
MF "v[104:107],v[20:23],v[168:171],v[44:47]\n"
CV "v84,v84,v85\n"
"v_add_f32 v55,v55,v86\n"
MF "v[104:107],v[16:19],v[148:151],v[104:107]\n"
"v_add_f32 v55,v55,v87\n"
CV "v85,v86,v87\n"
MF "v[108:111],v[16:19],v[152:155],v[48:51]\n"
"v_exp_f32 v124,v124\n"
MF "v[108:111],v[20:23],v[172:175],v[108:111]\n"
"s_waitcnt lgkmcnt(0)\n"
"ds_read_b128 v[16:19],v0 offset:26624\n"
"ds_read_b128 v[20:23],v1 offset:26624\n"
MF "v[108:111],v[24:27],v[192:195],v[108:111]\n"
"v_exp_f32 v125,v125\n"
MF "v[108:111],v[28:31],v[212:215],v[108:111]\n"
"v_exp_f32 v126,v126\n"
MF "v[104:107],v[28:31],v[208:211],v[104:107]\n"
"v_exp_f32 v127,v127\n"
MF "v[104:107],v[24:27],v[188:191],v[104:107]\n"
"v_add_f32 v55,v55,v124\n"
"v_add_f32 v55,v55,v125\n"
MF "v[100:103],v[24:27],v[184:187],v[100:103]\n"
CV "v86,v124,v125\n"
"v_add_f32 v55,v55,v126\n"
MF "v[100:103],v[28:31],v[204:207],v[100:103]\n"
"v_add_f32 v55,v55,v127\n"
CV "v87,v126,v127\n"
MF "v[96:99],v[28:31],v[200:203],v[96:99]\n"
"v_exp_f32 v88,v88\n"
MF "v[96:99],v[24:27],v[180:183],v[96:99]\n"
"v_exp_f32 v89,v89\n"
MF "v[92:95],v[24:27],v[176:179],v[92:95]\n"
"v_exp_f32 v90,v90\n"
MF "v[92:95],v[28:31],v[196:199],v[92:95]\n"
"s_waitcnt lgkmcnt(0)\n"
"ds_read_b128 v[24:27],v0 offset:30720\n"
"ds_read_b128 v[28:31],v1 offset:30720\n"
MF "v[92:95],v[16:19],v[216:219],v[92:95]\n"
"v_exp_f32 v91,v91\n"
MF "v[92:95],v[20:23],v[236:239],v[92:95]\n"
"v_add_f32 v56,v56,v88\n"
"v_add_f32 v56,v56,v89\n"
MF "v[96:99],v[20:23],v[240:243],v[96:99]\n"
CV "v88,v88,v89\n"
"v_add_f32 v56,v56,v90\n"
MF "v[96:99],v[16:19],v[220:223],v[96:99]\n"
"v_add_f32 v56,v56,v91\n"
CV "v89,v90,v91\n"
MF "v[100:103],v[16:19],v[224:227],v[100:103]\n"
"v_exp_f32 v128,v128\n"
MF "v[100:103],v[20:23],v[244:247],v[100:103]\n"
"v_exp_f32 v129,v129\n"
MF "v[104:107],v[20:23],v[248:251],v[104:107]\n"
"v_exp_f32 v130,v130\n"
MF "v[104:107],v[16:19],v[228:231],v[104:107]\n"
"v_exp_f32 v131,v131\n"
MF "v[108:111],v[16:19],v[232:235],v[108:111]\n"
"v_add_f32 v56,v56,v128\n"
"v_add_f32 v56,v56,v129\n"
MF "v[108:111],v[20:23],v[252:255],v[108:111]\n"
"s_waitcnt lgkmcnt(0)\n"
"ds_read_b128 v[16:19],v0 offset:31744\n"
"ds_read_b128 v[20:23],v1 offset:31744\n"
MF "v[112:115],v[24:27],v[136:139],v[32:35]\n"
CV "v90,v128,v129\n"
"v_add_f32 v56,v56,v130\n"
MF "v[112:115],v[28:31],v[156:159],v[112:115]\n"
"v_add_f32 v56,v56,v131\n"
CV "v91,v130,v131\n"
MF "v[116:119],v[28:31],v[160:163],v[36:39]\n"
"v_exp_f32 v92,v92\n"
MF "v[116:119],v[24:27],v[140:143],v[116:119]\n"
"v_exp_f32 v93,v93\n"
MF "v[120:123],v[24:27],v[144:147],v[40:43]\n"
"v_exp_f32 v94,v94\n"
MF "v[120:123],v[28:31],v[164:167],v[120:123]\n"
"v_exp_f32 v95,v95\n"
MF "v[124:127],v[28:31],v[168:171],v[44:47]\n"
"v_add_f32 v57,v52,v92\n"
"v_add_f32 v57,v57,v93\n"
MF "v[124:127],v[24:27],v[148:151],v[124:127]\n"
CV "v92,v92,v93\n"
"v_add_f32 v57,v57,v94\n"
MF "v[128:131],v[24:27],v[152:155],v[48:51]\n"
"v_add_f32 v57,v57,v95\n"
CV "v93,v94,v95\n"
MF "v[128:131],v[28:31],v[172:175],v[128:131]\n"
"s_waitcnt lgkmcnt(0)\n"
"ds_read_b128 v[24:27],v0 offset:32768\n"
"ds_read_b128 v[28:31],v1 offset:32768\n"
MF "v[128:131],v[16:19],v[192:195],v[128:131]\n"
"v_exp_f32 v96,v96\n"
MF "v[128:131],v[20:23],v[212:215],v[128:131]\n"
"v_exp_f32 v97,v97\n"
MF "v[124:127],v[20:23],v[208:211],v[124:127]\n"
"v_exp_f32 v98,v98\n"
MF "v[124:127],v[16:19],v[188:191],v[124:127]\n"
"v_exp_f32 v99,v99\n"
MF "v[120:123],v[16:19],v[184:187],v[120:123]\n"
"v_add_f32 v58,v53,v96\n"
"v_add_f32 v58,v58,v97\n"
MF "v[120:123],v[20:23],v[204:207],v[120:123]\n"
CV "v96,v96,v97\n"
"v_add_f32 v58,v58,v98\n"
MF "v[116:119],v[20:23],v[200:203],v[116:119]\n"
"v_add_f32 v58,v58,v99\n"
CV "v97,v98,v99\n"
MF "v[116:119],v[16:19],v[180:183],v[116:119]\n"
"s_waitcnt vmcnt(6) lgkmcnt(0)\n"
"s_barrier\n"
MF "v[112:115],v[16:19],v[176:179],v[112:115]\n"
"v_exp_f32 v100,v100\n"
MF "v[112:115],v[20:23],v[196:199],v[112:115]\n"
"s_nop 0\n"
TR "v[16:17],v2 offset:49152\n"
TR "v[18:19],v2 offset:55296\n"
MF "v[112:115],v[24:27],v[216:219],v[112:115]\n"
"s_add_u32 s73,s73,12288\n"
TR "v[20:21],v3 offset:49152\n"
TR "v[22:23],v3 offset:55296\n"
MF "v[112:115],v[28:31],v[236:239],v[112:115]\n"
"s_cmp_eq_u32 s73,s19\n"
"s_cselect_b32 s73,s18,s73\n"
"s_add_u32 m0,s65,24576\n"
MF "v[116:119],v[28:31],v[240:243],v[116:119]\n"
LD "v8,s[36:39],s73 offen nt lds\n"
MF "v[116:119],v[24:27],v[220:223],v[116:119]\n"
"s_add_u32 m0,s65,25472\n"
"v_exp_f32 v101,v101\n"
MF "v[120:123],v[24:27],v[224:227],v[120:123]\n"
LD "v8,s[36:39],s73 offen offset:128 nt lds\n"
MF "v[120:123],v[28:31],v[244:247],v[120:123]\n"
"v_exp_f32 v102,v102\n"
MF "v[124:127],v[28:31],v[248:251],v[124:127]\n"
"s_add_u32 m0,s65,26368\n"
"v_exp_f32 v103,v103\n"
MF "v[124:127],v[24:27],v[228:231],v[124:127]\n"
LD "v8,s[36:39],s73 offen offset:256 nt lds\n"
MF "v[128:131],v[24:27],v[232:235],v[128:131]\n"
"v_add_f32 v59,v54,v100\n"
"v_add_f32 v59,v59,v101\n"
MF "v[128:131],v[28:31],v[252:255],v[128:131]\n"
"s_waitcnt lgkmcnt(2)\n"
TR "v[24:25],v4 offset:49152\n"
TR "v[26:27],v4 offset:55296\n"
MF "a[0:3],v[16:19],v[72:75],a[220:223]\n"
"s_add_u32 m0,s65,24576\n"
TR "v[28:29],v5 offset:49152\n"
TR "v[30:31],v5 offset:55296\n"
MF "a[4:7],v[16:19],v[76:79],a[220:223]\n"
LD "v8,s[36:39],s73 offen offset:3072 nt lds\n"
MF "a[8:11],v[16:19],v[80:83],a[220:223]\n"
"s_add_u32 m0,s65,25472\n"
CV "v100,v100,v101\n"
"v_add_f32 v59,v59,v102\n"
MF "a[12:15],v[16:19],v[84:87],a[220:223]\n"
LD "v8,s[36:39],s73 offen offset:3200 nt lds\n"
MF "a[16:19],v[16:19],v[88:91],a[220:223]\n"
"s_waitcnt lgkmcnt(4)\n"
TR "v[16:17],v2 offset:50176\n"
TR "v[18:19],v2 offset:56320\n"
MF "a[36:39],v[20:23],v[88:91],a[220:223]\n"
"v_add_f32 v59,v59,v103\n"
CV "v101,v102,v103\n"
MF "a[32:35],v[20:23],v[84:87],a[220:223]\n"
"s_add_u32 m0,s65,26368\n"
"v_exp_f32 v104,v104\n"
MF "a[28:31],v[20:23],v[80:83],a[220:223]\n"
LD "v8,s[36:39],s73 offen offset:3328 nt lds\n"
MF "a[24:27],v[20:23],v[76:79],a[220:223]\n"
"v_exp_f32 v105,v105\n"
MF "a[20:23],v[20:23],v[72:75],a[220:223]\n"
"s_waitcnt lgkmcnt(4)\n"
TR "v[20:21],v3 offset:50176\n"
TR "v[22:23],v3 offset:56320\n"
MF "a[40:43],v[24:27],v[72:75],a[220:223]\n"
"v_exp_f32 v106,v106\n"
MF "a[44:47],v[24:27],v[76:79],a[220:223]\n"
"v_exp_f32 v107,v107\n"
MF "a[48:51],v[24:27],v[80:83],a[220:223]\n"
"v_add_f32 v60,v55,v104\n"
"v_add_f32 v60,v60,v105\n"
MF "a[52:55],v[24:27],v[84:87],a[220:223]\n"
CV "v104,v104,v105\n"
"v_add_f32 v60,v60,v106\n"
MF "a[56:59],v[24:27],v[88:91],a[220:223]\n"
"s_waitcnt lgkmcnt(4)\n"
TR "v[24:25],v4 offset:50176\n"
TR "v[26:27],v4 offset:56320\n"
MF "a[76:79],v[28:31],v[88:91],a[220:223]\n"
"v_add_f32 v60,v60,v107\n"
CV "v105,v106,v107\n"
MF "a[72:75],v[28:31],v[84:87],a[220:223]\n"
"v_exp_f32 v108,v108\n"
MF "a[68:71],v[28:31],v[80:83],a[220:223]\n"
"v_exp_f32 v109,v109\n"
MF "a[64:67],v[28:31],v[76:79],a[220:223]\n"
"v_exp_f32 v110,v110\n"
MF "a[60:63],v[28:31],v[72:75],a[220:223]\n"
"s_waitcnt lgkmcnt(4)\n"
TR "v[28:29],v5 offset:50176\n"
TR "v[30:31],v5 offset:56320\n"
MF "a[80:83],v[16:19],v[72:75],a[220:223]\n"
"v_exp_f32 v111,v111\n"
MF "a[84:87],v[16:19],v[76:79],a[220:223]\n"
"v_add_f32 v61,v56,v108\n"
"v_add_f32 v61,v61,v109\n"
MF "a[88:91],v[16:19],v[80:83],a[220:223]\n"
CV "v108,v108,v109\n"
"v_add_f32 v61,v61,v110\n"
MF "a[92:95],v[16:19],v[84:87],a[220:223]\n"
"v_add_f32 v61,v61,v111\n"
CV "v109,v110,v111\n"
MF "a[96:99],v[16:19],v[88:91],a[220:223]\n"
"s_waitcnt lgkmcnt(4)\n"
TR "v[16:17],v2 offset:51200\n"
TR "v[18:19],v2 offset:57344\n"
MF "a[116:119],v[20:23],v[88:91],a[220:223]\n"
"v_exp_f32 v112,v112\n"
MF "a[112:115],v[20:23],v[84:87],a[220:223]\n"
"v_exp_f32 v113,v113\n"
MF "a[108:111],v[20:23],v[80:83],a[220:223]\n"
"v_exp_f32 v114,v114\n"
MF "a[104:107],v[20:23],v[76:79],a[220:223]\n"
"v_exp_f32 v115,v115\n"
MF "a[100:103],v[20:23],v[72:75],a[220:223]\n"
"s_waitcnt lgkmcnt(4)\n"
TR "v[20:21],v3 offset:51200\n"
TR "v[22:23],v3 offset:57344\n"
MF "a[120:123],v[24:27],v[72:75],a[220:223]\n"
"v_add_f32 v57,v57,v112\n"
"v_add_f32 v57,v57,v113\n"
MF "a[124:127],v[24:27],v[76:79],a[220:223]\n"
CV "v94,v112,v113\n"
"v_add_f32 v57,v57,v114\n"
MF "a[128:131],v[24:27],v[80:83],a[220:223]\n"
"v_add_f32 v57,v57,v115\n"
CV "v95,v114,v115\n"
MF "a[132:135],v[24:27],v[84:87],a[220:223]\n"
"v_exp_f32 v116,v116\n"
MF "a[136:139],v[24:27],v[88:91],a[220:223]\n"
"s_waitcnt lgkmcnt(4)\n"
TR "v[24:25],v4 offset:51200\n"
TR "v[26:27],v4 offset:57344\n"
MF "a[156:159],v[28:31],v[88:91],a[220:223]\n"
"v_exp_f32 v117,v117\n"
MF "a[152:155],v[28:31],v[84:87],a[220:223]\n"
"v_exp_f32 v118,v118\n"
MF "a[148:151],v[28:31],v[80:83],a[220:223]\n"
"v_exp_f32 v119,v119\n"
MF "a[144:147],v[28:31],v[76:79],a[220:223]\n"
"v_add_f32 v58,v58,v116\n"
"v_add_f32 v58,v58,v117\n"
MF "a[140:143],v[28:31],v[72:75],a[220:223]\n"
"s_waitcnt lgkmcnt(4)\n"
TR "v[28:29],v5 offset:51200\n"
TR "v[30:31],v5 offset:57344\n"
MF "a[160:163],v[16:19],v[72:75],a[220:223]\n"
CV "v98,v116,v117\n"
"v_add_f32 v58,v58,v118\n"
MF "a[164:167],v[16:19],v[76:79],a[220:223]\n"
"v_add_f32 v58,v58,v119\n"
CV "v99,v118,v119\n"
MF "a[168:171],v[16:19],v[80:83],a[220:223]\n"
"v_exp_f32 v120,v120\n"
MF "a[172:175],v[16:19],v[84:87],a[220:223]\n"
"v_exp_f32 v121,v121\n"
MF "a[176:179],v[16:19],v[88:91],a[220:223]\n"
"s_waitcnt lgkmcnt(4)\n"
"v_exp_f32 v122,v122\n"
MF "a[196:199],v[20:23],v[88:91],a[220:223]\n"
"v_exp_f32 v123,v123\n"
MF "a[192:195],v[20:23],v[84:87],a[220:223]\n"
"v_add_f32 v59,v59,v120\n"
"v_add_f32 v59,v59,v121\n"
MF "a[188:191],v[20:23],v[80:83],a[220:223]\n"
CV "v102,v120,v121\n"
"v_add_f32 v59,v59,v122\n"
MF "a[184:187],v[20:23],v[76:79],a[220:223]\n"
"s_waitcnt vmcnt(6) lgkmcnt(0)\n"
"s_barrier\n"
MF "a[180:183],v[20:23],v[72:75],a[220:223]\n"
"s_add_u32 s72,s72,6144\n"
"ds_read_b128 v[16:19],v0\n"
"ds_read_b128 v[20:23],v1\n"
MF "a[200:203],v[24:27],v[72:75],a[220:223]\n"
"s_cmp_eq_u32 s72,s17\n"
"s_cselect_b32 s72,s16,s72\n"
"s_add_u32 m0,s64,49152\n"
MF "a[204:207],v[24:27],v[76:79],a[220:223]\n"
LD "v7,s[32:35],s72 offen nt lds\n"
MF "a[208:211],v[24:27],v[80:83],a[220:223]\n"
"s_add_u32 m0,s64,50048\n"
"v_add_f32 v59,v59,v123\n"
CV "v103,v122,v123\n"
MF "a[212:215],v[24:27],v[84:87],a[220:223]\n"
LD "v7,s[32:35],s72 offen offset:128 nt lds\n"
MF "a[216:219],v[24:27],v[88:91],a[220:223]\n"
"s_add_u32 m0,s64,50944\n"
"ds_read_b128 v[24:27],v0 offset:1024\n"
MF "a[236:239],v[28:31],v[88:91],a[220:223]\n"
LD "v7,s[32:35],s72 offen offset:256 nt lds\n"
MF "a[232:235],v[28:31],v[84:87],a[220:223]\n"
MF "a[228:231],v[28:31],v[80:83],a[220:223]\n"
MF "a[224:227],v[28:31],v[76:79],a[220:223]\n"
MF "a[220:223],v[28:31],v[72:75],a[220:223]\n"
"s_waitcnt lgkmcnt(1)\n"
"s_branch .Lmid\n"
".Laloop:\n"
MF "v[72:75],v[16:19],v[136:139],v[32:35]\n"
"ds_read_b128 v[28:31],v1 offset:25600\n"
MF "v[72:75],v[20:23],v[156:159],v[72:75]\n"
MF "v[76:79],v[20:23],v[160:163],v[36:39]\n"
MF "v[76:79],v[16:19],v[140:143],v[76:79]\n"
MF "v[80:83],v[16:19],v[144:147],v[40:43]\n"
MF "v[80:83],v[20:23],v[164:167],v[80:83]\n"
MF "v[84:87],v[20:23],v[168:171],v[44:47]\n"
MF "v[84:87],v[16:19],v[148:151],v[84:87]\n"
"s_add_u32 s72,s72,6144\n"
"s_add_u32 m0,s64,6144\n"
MF "v[88:91],v[16:19],v[152:155],v[48:51]\n"
LD "v7,s[32:35],s72 offen nt lds\n"
MF "v[88:91],v[20:23],v[172:175],v[88:91]\n"
"s_waitcnt lgkmcnt(0)\n"
"ds_read_b128 v[16:19],v0 offset:26624\n"
"ds_read_b128 v[20:23],v1 offset:26624\n"
MF "v[88:91],v[24:27],v[192:195],v[88:91]\n"
"v_exp_f32 v124,v124\n"
MF "v[88:91],v[28:31],v[212:215],v[88:91]\n"
"s_add_u32 m0,s64,7040\n"
"v_exp_f32 v125,v125\n"
MF "v[84:87],v[28:31],v[208:211],v[84:87]\n"
LD "v7,s[32:35],s72 offen offset:128 nt lds\n"
MF "v[84:87],v[24:27],v[188:191],v[84:87]\n"
"v_exp_f32 v126,v126\n"
MF "v[80:83],v[24:27],v[184:187],v[80:83]\n"
"v_exp_f32 v127,v127\n"
MF "v[80:83],v[28:31],v[204:207],v[80:83]\n"
"s_add_u32 m0,s64,7936\n"
"v_add_f32 v60,v60,v124\n"
"v_add_f32 v60,v60,v125\n"
MF "v[76:79],v[28:31],v[200:203],v[76:79]\n"
LD "v7,s[32:35],s72 offen offset:256 nt lds\n"
MF "v[76:79],v[24:27],v[180:183],v[76:79]\n"
CV "v106,v124,v125\n"
"v_add_f32 v60,v60,v126\n"
MF "v[72:75],v[24:27],v[176:179],v[72:75]\n"
"v_add_f32 v60,v60,v127\n"
CV "v107,v126,v127\n"
MF "v[72:75],v[28:31],v[196:199],v[72:75]\n"
"s_waitcnt lgkmcnt(0)\n"
"ds_read_b128 v[24:27],v0 offset:30720\n"
"ds_read_b128 v[28:31],v1 offset:30720\n"
MF "v[72:75],v[16:19],v[216:219],v[72:75]\n"
"v_exp_f32 v128,v128\n"
MF "v[72:75],v[20:23],v[236:239],v[72:75]\n"
"v_exp_f32 v129,v129\n"
MF "v[76:79],v[20:23],v[240:243],v[76:79]\n"
"v_exp_f32 v130,v130\n"
MF "v[76:79],v[16:19],v[220:223],v[76:79]\n"
"v_exp_f32 v131,v131\n"
MF "v[80:83],v[16:19],v[224:227],v[80:83]\n"
"v_add_f32 v61,v61,v128\n"
"v_add_f32 v61,v61,v129\n"
MF "v[80:83],v[20:23],v[244:247],v[80:83]\n"
CV "v110,v128,v129\n"
"v_add_f32 v61,v61,v130\n"
MF "v[84:87],v[20:23],v[248:251],v[84:87]\n"
"v_add_f32 v61,v61,v131\n"
CV "v111,v130,v131\n"
MF "v[84:87],v[16:19],v[228:231],v[84:87]\n"
"v_max3_f32 v10,v57,v58,v59\n"
"v_max3_f32 v10,v10,v60,v61\n"
MF "v[88:91],v[16:19],v[232:235],v[88:91]\n"
"v_cmp_ngt_f32 vcc,s77,v10\n"
MF "v[88:91],v[20:23],v[252:255],v[88:91]\n"
"s_waitcnt lgkmcnt(0)\n"
"s_cbranch_vccnz .Lstub_11\n"
".Lback_11:\n"
MF "v[112:115],v[24:27],v[136:139],v[32:35]\n"
"s_add_u32 s74,s74,12288\n"
"ds_read_b128 v[16:19],v0 offset:31744\n"
"ds_read_b128 v[20:23],v1 offset:31744\n"
MF "v[112:115],v[28:31],v[156:159],v[112:115]\n"
"s_cmp_eq_u32 s74,s20\n"
"s_cselect_b32 s74,s70,s74\n"
MF "v[116:119],v[28:31],v[160:163],v[36:39]\n"
"v_exp_f32 v72,v72\n"
MF "v[116:119],v[24:27],v[140:143],v[116:119]\n"
"v_exp_f32 v73,v73\n"
MF "v[120:123],v[24:27],v[144:147],v[40:43]\n"
"v_exp_f32 v74,v74\n"
MF "v[120:123],v[28:31],v[164:167],v[120:123]\n"
"v_exp_f32 v75,v75\n"
MF "v[124:127],v[28:31],v[168:171],v[44:47]\n"
"v_add_f32 v52,v57,v72\n"
"v_add_f32 v52,v52,v73\n"
MF "v[124:127],v[24:27],v[148:151],v[124:127]\n"
CV "v72,v72,v73\n"
"v_add_f32 v52,v52,v74\n"
MF "v[128:131],v[24:27],v[152:155],v[48:51]\n"
"v_add_f32 v52,v52,v75\n"
CV "v73,v74,v75\n"
MF "v[128:131],v[28:31],v[172:175],v[128:131]\n"
"s_waitcnt lgkmcnt(0)\n"
"ds_read_b128 v[24:27],v0 offset:32768\n"
"ds_read_b128 v[28:31],v1 offset:32768\n"
MF "v[128:131],v[16:19],v[192:195],v[128:131]\n"
"v_exp_f32 v76,v76\n"
MF "v[128:131],v[20:23],v[212:215],v[128:131]\n"
"v_exp_f32 v77,v77\n"
MF "v[124:127],v[20:23],v[208:211],v[124:127]\n"
"v_exp_f32 v78,v78\n"
MF "v[124:127],v[16:19],v[188:191],v[124:127]\n"
"v_exp_f32 v79,v79\n"
MF "v[120:123],v[16:19],v[184:187],v[120:123]\n"
"v_add_f32 v53,v58,v76\n"
"v_add_f32 v53,v53,v77\n"
MF "v[120:123],v[20:23],v[204:207],v[120:123]\n"
CV "v76,v76,v77\n"
"v_add_f32 v53,v53,v78\n"
MF "v[116:119],v[20:23],v[200:203],v[116:119]\n"
"v_add_f32 v53,v53,v79\n"
CV "v77,v78,v79\n"
MF "v[116:119],v[16:19],v[180:183],v[116:119]\n"
"s_waitcnt vmcnt(6) lgkmcnt(0)\n"
"s_barrier\n"
MF "v[112:115],v[16:19],v[176:179],v[112:115]\n"
"v_exp_f32 v80,v80\n"
MF "v[112:115],v[20:23],v[196:199],v[112:115]\n"
"s_nop 0\n"
TR "v[16:17],v2 offset:49152\n"
TR "v[18:19],v2 offset:55296\n"
MF "v[112:115],v[24:27],v[216:219],v[112:115]\n"
"s_add_u32 s73,s73,12288\n"
TR "v[20:21],v3 offset:49152\n"
TR "v[22:23],v3 offset:55296\n"
MF "v[112:115],v[28:31],v[236:239],v[112:115]\n"
"s_cmp_eq_u32 s73,s19\n"
"s_cselect_b32 s73,s18,s73\n"
"s_add_u32 m0,s65,24576\n"
MF "v[116:119],v[28:31],v[240:243],v[116:119]\n"
LD "v8,s[36:39],s73 offen nt lds\n"
MF "v[116:119],v[24:27],v[220:223],v[116:119]\n"
"s_add_u32 m0,s65,25472\n"
"v_exp_f32 v81,v81\n"
MF "v[120:123],v[24:27],v[224:227],v[120:123]\n"
LD "v8,s[36:39],s73 offen offset:128 nt lds\n"
MF "v[120:123],v[28:31],v[244:247],v[120:123]\n"
"v_exp_f32 v82,v82\n"
MF "v[124:127],v[28:31],v[248:251],v[124:127]\n"
"s_add_u32 m0,s65,26368\n"
"v_exp_f32 v83,v83\n"
MF "v[124:127],v[24:27],v[228:231],v[124:127]\n"
LD "v8,s[36:39],s73 offen offset:256 nt lds\n"
MF "v[128:131],v[24:27],v[232:235],v[128:131]\n"
"v_add_f32 v54,v59,v80\n"
"v_add_f32 v54,v54,v81\n"
MF "v[128:131],v[28:31],v[252:255],v[128:131]\n"
"s_waitcnt lgkmcnt(2)\n"
TR "v[24:25],v4 offset:49152\n"
TR "v[26:27],v4 offset:55296\n"
MF "a[0:3],v[16:19],v[92:95],a[0:3]\n"
"s_add_u32 m0,s65,24576\n"
TR "v[28:29],v5 offset:49152\n"
TR "v[30:31],v5 offset:55296\n"
MF "a[4:7],v[16:19],v[96:99],a[4:7]\n"
LD "v8,s[36:39],s73 offen offset:3072 nt lds\n"
MF "a[8:11],v[16:19],v[100:103],a[8:11]\n"
"s_add_u32 m0,s65,25472\n"
CV "v80,v80,v81\n"
"v_add_f32 v54,v54,v82\n"
MF "a[12:15],v[16:19],v[104:107],a[12:15]\n"
LD "v8,s[36:39],s73 offen offset:3200 nt lds\n"
MF "a[16:19],v[16:19],v[108:111],a[16:19]\n"
"s_waitcnt lgkmcnt(4)\n"
TR "v[16:17],v2 offset:50176\n"
TR "v[18:19],v2 offset:56320\n"
MF "a[36:39],v[20:23],v[108:111],a[36:39]\n"
"v_add_f32 v54,v54,v83\n"
CV "v81,v82,v83\n"
MF "a[32:35],v[20:23],v[104:107],a[32:35]\n"
"s_add_u32 m0,s65,26368\n"
"v_exp_f32 v84,v84\n"
MF "a[28:31],v[20:23],v[100:103],a[28:31]\n"
LD "v8,s[36:39],s73 offen offset:3328 nt lds\n"
MF "a[24:27],v[20:23],v[96:99],a[24:27]\n"
"v_exp_f32 v85,v85\n"
MF "a[20:23],v[20:23],v[92:95],a[20:23]\n"
"s_waitcnt lgkmcnt(4)\n"
TR "v[20:21],v3 offset:50176\n"
TR "v[22:23],v3 offset:56320\n"
MF "a[40:43],v[24:27],v[92:95],a[40:43]\n"
"v_exp_f32 v86,v86\n"
MF "a[44:47],v[24:27],v[96:99],a[44:47]\n"
"v_exp_f32 v87,v87\n"
MF "a[48:51],v[24:27],v[100:103],a[48:51]\n"
"v_add_f32 v55,v60,v84\n"
"v_add_f32 v55,v55,v85\n"
MF "a[52:55],v[24:27],v[104:107],a[52:55]\n"
CV "v84,v84,v85\n"
"v_add_f32 v55,v55,v86\n"
MF "a[56:59],v[24:27],v[108:111],a[56:59]\n"
"s_waitcnt lgkmcnt(4)\n"
TR "v[24:25],v4 offset:50176\n"
TR "v[26:27],v4 offset:56320\n"
MF "a[76:79],v[28:31],v[108:111],a[76:79]\n"
"v_add_f32 v55,v55,v87\n"
CV "v85,v86,v87\n"
MF "a[72:75],v[28:31],v[104:107],a[72:75]\n"
"v_exp_f32 v88,v88\n"
MF "a[68:71],v[28:31],v[100:103],a[68:71]\n"
"v_exp_f32 v89,v89\n"
MF "a[64:67],v[28:31],v[96:99],a[64:67]\n"
"v_exp_f32 v90,v90\n"
MF "a[60:63],v[28:31],v[92:95],a[60:63]\n"
"s_waitcnt lgkmcnt(4)\n"
TR "v[28:29],v5 offset:50176\n"
TR "v[30:31],v5 offset:56320\n"
MF "a[80:83],v[16:19],v[92:95],a[80:83]\n"
"v_exp_f32 v91,v91\n"
MF "a[84:87],v[16:19],v[96:99],a[84:87]\n"
"v_add_f32 v56,v61,v88\n"
"v_add_f32 v56,v56,v89\n"
MF "a[88:91],v[16:19],v[100:103],a[88:91]\n"
CV "v88,v88,v89\n"
"v_add_f32 v56,v56,v90\n"
MF "a[92:95],v[16:19],v[104:107],a[92:95]\n"
"v_add_f32 v56,v56,v91\n"
CV "v89,v90,v91\n"
MF "a[96:99],v[16:19],v[108:111],a[96:99]\n"
"s_waitcnt lgkmcnt(4)\n"
TR "v[16:17],v2 offset:51200\n"
TR "v[18:19],v2 offset:57344\n"
MF "a[116:119],v[20:23],v[108:111],a[116:119]\n"
"v_exp_f32 v112,v112\n"
MF "a[112:115],v[20:23],v[104:107],a[112:115]\n"
"v_exp_f32 v113,v113\n"
MF "a[108:111],v[20:23],v[100:103],a[108:111]\n"
"v_exp_f32 v114,v114\n"
MF "a[104:107],v[20:23],v[96:99],a[104:107]\n"
"v_exp_f32 v115,v115\n"
MF "a[100:103],v[20:23],v[92:95],a[100:103]\n"
"s_waitcnt lgkmcnt(4)\n"
TR "v[20:21],v3 offset:51200\n"
TR "v[22:23],v3 offset:57344\n"
MF "a[120:123],v[24:27],v[92:95],a[120:123]\n"
"v_add_f32 v52,v52,v112\n"
"v_add_f32 v52,v52,v113\n"
MF "a[124:127],v[24:27],v[96:99],a[124:127]\n"
CV "v74,v112,v113\n"
"v_add_f32 v52,v52,v114\n"
MF "a[128:131],v[24:27],v[100:103],a[128:131]\n"
"v_add_f32 v52,v52,v115\n"
CV "v75,v114,v115\n"
MF "a[132:135],v[24:27],v[104:107],a[132:135]\n"
"v_exp_f32 v116,v116\n"
MF "a[136:139],v[24:27],v[108:111],a[136:139]\n"
"s_waitcnt lgkmcnt(4)\n"
TR "v[24:25],v4 offset:51200\n"
TR "v[26:27],v4 offset:57344\n"
MF "a[156:159],v[28:31],v[108:111],a[156:159]\n"
"v_exp_f32 v117,v117\n"
MF "a[152:155],v[28:31],v[104:107],a[152:155]\n"
"v_exp_f32 v118,v118\n"
MF "a[148:151],v[28:31],v[100:103],a[148:151]\n"
"v_exp_f32 v119,v119\n"
MF "a[144:147],v[28:31],v[96:99],a[144:147]\n"
"v_add_f32 v53,v53,v116\n"
"v_add_f32 v53,v53,v117\n"
MF "a[140:143],v[28:31],v[92:95],a[140:143]\n"
"s_waitcnt lgkmcnt(4)\n"
TR "v[28:29],v5 offset:51200\n"
TR "v[30:31],v5 offset:57344\n"
MF "a[160:163],v[16:19],v[92:95],a[160:163]\n"
CV "v78,v116,v117\n"
"v_add_f32 v53,v53,v118\n"
MF "a[164:167],v[16:19],v[96:99],a[164:167]\n"
"v_add_f32 v53,v53,v119\n"
CV "v79,v118,v119\n"
MF "a[168:171],v[16:19],v[100:103],a[168:171]\n"
"v_exp_f32 v120,v120\n"
MF "a[172:175],v[16:19],v[104:107],a[172:175]\n"
"v_exp_f32 v121,v121\n"
MF "a[176:179],v[16:19],v[108:111],a[176:179]\n"
"s_waitcnt lgkmcnt(4)\n"
"v_exp_f32 v122,v122\n"
MF "a[196:199],v[20:23],v[108:111],a[196:199]\n"
"v_exp_f32 v123,v123\n"
MF "a[192:195],v[20:23],v[104:107],a[192:195]\n"
"v_add_f32 v54,v54,v120\n"
"v_add_f32 v54,v54,v121\n"
MF "a[188:191],v[20:23],v[100:103],a[188:191]\n"
CV "v82,v120,v121\n"
"v_add_f32 v54,v54,v122\n"
MF "a[184:187],v[20:23],v[96:99],a[184:187]\n"
"s_waitcnt vmcnt(6) lgkmcnt(0)\n"
"s_barrier\n"
MF "a[180:183],v[20:23],v[92:95],a[180:183]\n"
"s_add_u32 s72,s72,6144\n"
"ds_read_b128 v[16:19],v0\n"
"ds_read_b128 v[20:23],v1\n"
MF "a[200:203],v[24:27],v[92:95],a[200:203]\n"
"s_cmp_eq_u32 s72,s17\n"
"s_cselect_b32 s72,s16,s72\n"
"s_add_u32 m0,s64,49152\n"
MF "a[204:207],v[24:27],v[96:99],a[204:207]\n"
LD "v7,s[32:35],s72 offen nt lds\n"
MF "a[208:211],v[24:27],v[100:103],a[208:211]\n"
"s_add_u32 m0,s64,50048\n"
"v_add_f32 v54,v54,v123\n"
CV "v83,v122,v123\n"
MF "a[212:215],v[24:27],v[104:107],a[212:215]\n"
LD "v7,s[32:35],s72 offen offset:128 nt lds\n"
MF "a[216:219],v[24:27],v[108:111],a[216:219]\n"
"s_add_u32 m0,s64,50944\n"
"ds_read_b128 v[24:27],v0 offset:1024\n"
MF "a[236:239],v[28:31],v[108:111],a[236:239]\n"
LD "v7,s[32:35],s72 offen offset:256 nt lds\n"
MF "a[232:235],v[28:31],v[104:107],a[232:235]\n"
MF "a[228:231],v[28:31],v[100:103],a[228:231]\n"
MF "a[224:227],v[28:31],v[96:99],a[224:227]\n"
MF "a[220:223],v[28:31],v[92:95],a[220:223]\n"
"s_waitcnt lgkmcnt(1)\n"
MF "v[92:95],v[16:19],v[136:139],v[32:35]\n"
"s_add_u32 s72,s72,6144\n"
"s_add_u32 m0,s64,55296\n"
"ds_read_b128 v[28:31],v1 offset:1024\n"
MF "v[92:95],v[20:23],v[156:159],v[92:95]\n"
LD "v7,s[32:35],s72 offen nt lds\n"
MF "v[96:99],v[20:23],v[160:163],v[36:39]\n"
"v_exp_f32 v124,v124\n"
MF "v[96:99],v[16:19],v[140:143],v[96:99]\n"
"v_exp_f32 v125,v125\n"
MF "v[100:103],v[16:19],v[144:147],v[40:43]\n"
"s_add_u32 m0,s64,56192\n"
"v_exp_f32 v126,v126\n"
MF "v[100:103],v[20:23],v[164:167],v[100:103]\n"
LD "v7,s[32:35],s72 offen offset:128 nt lds\n"
MF "v[104:107],v[20:23],v[168:171],v[44:47]\n"
"v_exp_f32 v127,v127\n"
MF "v[104:107],v[16:19],v[148:151],v[104:107]\n"
"s_add_u32 m0,s64,57088\n"
"v_add_f32 v55,v55,v124\n"
"v_add_f32 v55,v55,v125\n"
MF "v[108:111],v[16:19],v[152:155],v[48:51]\n"
LD "v7,s[32:35],s72 offen offset:256 nt lds\n"
MF "v[108:111],v[20:23],v[172:175],v[108:111]\n"
"s_waitcnt lgkmcnt(0)\n"
"ds_read_b128 v[16:19],v0 offset:2048\n"
"ds_read_b128 v[20:23],v1 offset:2048\n"
MF "v[108:111],v[24:27],v[192:195],v[108:111]\n"
CV "v86,v124,v125\n"
"v_add_f32 v55,v55,v126\n"
MF "v[108:111],v[28:31],v[212:215],v[108:111]\n"
"v_add_f32 v55,v55,v127\n"
CV "v87,v126,v127\n"
MF "v[104:107],v[28:31],v[208:211],v[104:107]\n"
"v_exp_f32 v128,v128\n"
MF "v[104:107],v[24:27],v[188:191],v[104:107]\n"
"v_exp_f32 v129,v129\n"
MF "v[100:103],v[24:27],v[184:187],v[100:103]\n"
"v_exp_f32 v130,v130\n"
MF "v[100:103],v[28:31],v[204:207],v[100:103]\n"
"v_exp_f32 v131,v131\n"
MF "v[96:99],v[28:31],v[200:203],v[96:99]\n"
"v_add_f32 v56,v56,v128\n"
"v_add_f32 v56,v56,v129\n"
MF "v[96:99],v[24:27],v[180:183],v[96:99]\n"
CV "v90,v128,v129\n"
"v_add_f32 v56,v56,v130\n"
MF "v[92:95],v[24:27],v[176:179],v[92:95]\n"
"v_add_f32 v56,v56,v131\n"
CV "v91,v130,v131\n"
MF "v[92:95],v[28:31],v[196:199],v[92:95]\n"
"s_waitcnt lgkmcnt(0)\n"
"ds_read_b128 v[24:27],v0 offset:6144\n"
"ds_read_b128 v[28:31],v1 offset:6144\n"
MF "v[92:95],v[16:19],v[216:219],v[92:95]\n"
MF "v[92:95],v[20:23],v[236:239],v[92:95]\n"
MF "v[96:99],v[20:23],v[240:243],v[96:99]\n"
MF "v[96:99],v[16:19],v[220:223],v[96:99]\n"
MF "v[100:103],v[16:19],v[224:227],v[100:103]\n"
MF "v[100:103],v[20:23],v[244:247],v[100:103]\n"
MF "v[104:107],v[20:23],v[248:251],v[104:107]\n"
MF "v[104:107],v[16:19],v[228:231],v[104:107]\n"
"v_max3_f32 v10,v52,v53,v54\n"
"v_max3_f32 v10,v10,v55,v56\n"
MF "v[108:111],v[16:19],v[232:235],v[108:111]\n"
"v_cmp_ngt_f32 vcc,s77,v10\n"
MF "v[108:111],v[20:23],v[252:255],v[108:111]\n"
"s_waitcnt lgkmcnt(0)\n"
"s_cbranch_vccnz .Lstub_12\n"
".Lback_12:\n"
MF "v[112:115],v[24:27],v[136:139],v[32:35]\n"
"s_add_u32 s74,s74,12288\n"
"ds_read_b128 v[16:19],v0 offset:7168\n"
"ds_read_b128 v[20:23],v1 offset:7168\n"
MF "v[112:115],v[28:31],v[156:159],v[112:115]\n"
"s_cmp_eq_u32 s74,s20\n"
"s_cselect_b32 s74,s70,s74\n"
MF "v[116:119],v[28:31],v[160:163],v[36:39]\n"
"v_exp_f32 v92,v92\n"
MF "v[116:119],v[24:27],v[140:143],v[116:119]\n"
"v_exp_f32 v93,v93\n"
MF "v[120:123],v[24:27],v[144:147],v[40:43]\n"
"v_exp_f32 v94,v94\n"
MF "v[120:123],v[28:31],v[164:167],v[120:123]\n"
"v_exp_f32 v95,v95\n"
MF "v[124:127],v[28:31],v[168:171],v[44:47]\n"
"v_add_f32 v57,v52,v92\n"
"v_add_f32 v57,v57,v93\n"
MF "v[124:127],v[24:27],v[148:151],v[124:127]\n"
CV "v92,v92,v93\n"
"v_add_f32 v57,v57,v94\n"
MF "v[128:131],v[24:27],v[152:155],v[48:51]\n"
"v_add_f32 v57,v57,v95\n"
CV "v93,v94,v95\n"
MF "v[128:131],v[28:31],v[172:175],v[128:131]\n"
"s_waitcnt lgkmcnt(0)\n"
"ds_read_b128 v[24:27],v0 offset:8192\n"
"ds_read_b128 v[28:31],v1 offset:8192\n"
MF "v[128:131],v[16:19],v[192:195],v[128:131]\n"
"v_exp_f32 v96,v96\n"
MF "v[128:131],v[20:23],v[212:215],v[128:131]\n"
"v_exp_f32 v97,v97\n"
MF "v[124:127],v[20:23],v[208:211],v[124:127]\n"
"v_exp_f32 v98,v98\n"
MF "v[124:127],v[16:19],v[188:191],v[124:127]\n"
"v_exp_f32 v99,v99\n"
MF "v[120:123],v[16:19],v[184:187],v[120:123]\n"
"v_add_f32 v58,v53,v96\n"
"v_add_f32 v58,v58,v97\n"
MF "v[120:123],v[20:23],v[204:207],v[120:123]\n"
CV "v96,v96,v97\n"
"v_add_f32 v58,v58,v98\n"
MF "v[116:119],v[20:23],v[200:203],v[116:119]\n"
"v_add_f32 v58,v58,v99\n"
CV "v97,v98,v99\n"
MF "v[116:119],v[16:19],v[180:183],v[116:119]\n"
"s_waitcnt vmcnt(6) lgkmcnt(0)\n"
"s_barrier\n"
MF "v[112:115],v[16:19],v[176:179],v[112:115]\n"
"v_exp_f32 v100,v100\n"
MF "v[112:115],v[20:23],v[196:199],v[112:115]\n"
"s_nop 0\n"
TR "v[16:17],v2 offset:24576\n"
TR "v[18:19],v2 offset:30720\n"
MF "v[112:115],v[24:27],v[216:219],v[112:115]\n"
"s_add_u32 s73,s73,12288\n"
TR "v[20:21],v3 offset:24576\n"
TR "v[22:23],v3 offset:30720\n"
MF "v[112:115],v[28:31],v[236:239],v[112:115]\n"
"s_cmp_eq_u32 s73,s19\n"
"s_cselect_b32 s73,s18,s73\n"
"s_add_u32 m0,s65,0\n"
MF "v[116:119],v[28:31],v[240:243],v[116:119]\n"
LD "v8,s[36:39],s73 offen nt lds\n"
MF "v[116:119],v[24:27],v[220:223],v[116:119]\n"
"s_add_u32 m0,s65,896\n"
"v_exp_f32 v101,v101\n"
MF "v[120:123],v[24:27],v[224:227],v[120:123]\n"
LD "v8,s[36:39],s73 offen offset:128 nt lds\n"
MF "v[120:123],v[28:31],v[244:247],v[120:123]\n"
"v_exp_f32 v102,v102\n"
MF "v[124:127],v[28:31],v[248:251],v[124:127]\n"
"s_add_u32 m0,s65,1792\n"
"v_exp_f32 v103,v103\n"
MF "v[124:127],v[24:27],v[228:231],v[124:127]\n"
LD "v8,s[36:39],s73 offen offset:256 nt lds\n"
MF "v[128:131],v[24:27],v[232:235],v[128:131]\n"
"v_add_f32 v59,v54,v100\n"
"v_add_f32 v59,v59,v101\n"
MF "v[128:131],v[28:31],v[252:255],v[128:131]\n"
"s_waitcnt lgkmcnt(2)\n"
TR "v[24:25],v4 offset:24576\n"
TR "v[26:27],v4 offset:30720\n"
MF "a[0:3],v[16:19],v[72:75],a[0:3]\n"
"s_add_u32 m0,s65,0\n"
TR "v[28:29],v5 offset:24576\n"
TR "v[30:31],v5 offset:30720\n"
MF "a[4:7],v[16:19],v[76:79],a[4:7]\n"
LD "v8,s[36:39],s73 offen offset:3072 nt lds\n"
MF "a[8:11],v[16:19],v[80:83],a[8:11]\n"
"s_add_u32 m0,s65,896\n"
CV "v100,v100,v101\n"
"v_add_f32 v59,v59,v102\n"
MF "a[12:15],v[16:19],v[84:87],a[12:15]\n"
LD "v8,s[36:39],s73 offen offset:3200 nt lds\n"
MF "a[16:19],v[16:19],v[88:91],a[16:19]\n"
"s_waitcnt lgkmcnt(4)\n"
TR "v[16:17],v2 offset:25600\n"
TR "v[18:19],v2 offset:31744\n"
MF "a[36:39],v[20:23],v[88:91],a[36:39]\n"
"v_add_f32 v59,v59,v103\n"
CV "v101,v102,v103\n"
MF "a[32:35],v[20:23],v[84:87],a[32:35]\n"
"s_add_u32 m0,s65,1792\n"
"v_exp_f32 v104,v104\n"
MF "a[28:31],v[20:23],v[80:83],a[28:31]\n"
LD "v8,s[36:39],s73 offen offset:3328 nt lds\n"
MF "a[24:27],v[20:23],v[76:79],a[24:27]\n"
"v_exp_f32 v105,v105\n"
MF "a[20:23],v[20:23],v[72:75],a[20:23]\n"
"s_waitcnt lgkmcnt(4)\n"
TR "v[20:21],v3 offset:25600\n"
TR "v[22:23],v3 offset:31744\n"
MF "a[40:43],v[24:27],v[72:75],a[40:43]\n"
"v_exp_f32 v106,v106\n"
MF "a[44:47],v[24:27],v[76:79],a[44:47]\n"
"v_exp_f32 v107,v107\n"
MF "a[48:51],v[24:27],v[80:83],a[48:51]\n"
"v_add_f32 v60,v55,v104\n"
"v_add_f32 v60,v60,v105\n"
MF "a[52:55],v[24:27],v[84:87],a[52:55]\n"
CV "v104,v104,v105\n"
"v_add_f32 v60,v60,v106\n"
MF "a[56:59],v[24:27],v[88:91],a[56:59]\n"
"s_waitcnt lgkmcnt(4)\n"
TR "v[24:25],v4 offset:25600\n"
TR "v[26:27],v4 offset:31744\n"
MF "a[76:79],v[28:31],v[88:91],a[76:79]\n"
"v_add_f32 v60,v60,v107\n"
CV "v105,v106,v107\n"
MF "a[72:75],v[28:31],v[84:87],a[72:75]\n"
"v_exp_f32 v108,v108\n"
MF "a[68:71],v[28:31],v[80:83],a[68:71]\n"
"v_exp_f32 v109,v109\n"
MF "a[64:67],v[28:31],v[76:79],a[64:67]\n"
"v_exp_f32 v110,v110\n"
MF "a[60:63],v[28:31],v[72:75],a[60:63]\n"
"s_waitcnt lgkmcnt(4)\n"
TR "v[28:29],v5 offset:25600\n"
TR "v[30:31],v5 offset:31744\n"
MF "a[80:83],v[16:19],v[72:75],a[80:83]\n"
"v_exp_f32 v111,v111\n"
MF "a[84:87],v[16:19],v[76:79],a[84:87]\n"
"v_add_f32 v61,v56,v108\n"
"v_add_f32 v61,v61,v109\n"
MF "a[88:91],v[16:19],v[80:83],a[88:91]\n"
CV "v108,v108,v109\n"
"v_add_f32 v61,v61,v110\n"
MF "a[92:95],v[16:19],v[84:87],a[92:95]\n"
"v_add_f32 v61,v61,v111\n"
CV "v109,v110,v111\n"
MF "a[96:99],v[16:19],v[88:91],a[96:99]\n"
"s_waitcnt lgkmcnt(4)\n"
TR "v[16:17],v2 offset:26624\n"
TR "v[18:19],v2 offset:32768\n"
MF "a[116:119],v[20:23],v[88:91],a[116:119]\n"
"v_exp_f32 v112,v112\n"
MF "a[112:115],v[20:23],v[84:87],a[112:115]\n"
"v_exp_f32 v113,v113\n"
MF "a[108:111],v[20:23],v[80:83],a[108:111]\n"
"v_exp_f32 v114,v114\n"
MF "a[104:107],v[20:23],v[76:79],a[104:107]\n"
"v_exp_f32 v115,v115\n"
MF "a[100:103],v[20:23],v[72:75],a[100:103]\n"
"s_waitcnt lgkmcnt(4)\n"
TR "v[20:21],v3 offset:26624\n"
TR "v[22:23],v3 offset:32768\n"
MF "a[120:123],v[24:27],v[72:75],a[120:123]\n"
"v_add_f32 v57,v57,v112\n"
"v_add_f32 v57,v57,v113\n"
MF "a[124:127],v[24:27],v[76:79],a[124:127]\n"
CV "v94,v112,v113\n"
"v_add_f32 v57,v57,v114\n"
MF "a[128:131],v[24:27],v[80:83],a[128:131]\n"
"v_add_f32 v57,v57,v115\n"
CV "v95,v114,v115\n"
MF "a[132:135],v[24:27],v[84:87],a[132:135]\n"
"v_exp_f32 v116,v116\n"
MF "a[136:139],v[24:27],v[88:91],a[136:139]\n"
"s_waitcnt lgkmcnt(4)\n"
TR "v[24:25],v4 offset:26624\n"
TR "v[26:27],v4 offset:32768\n"
MF "a[156:159],v[28:31],v[88:91],a[156:159]\n"
"v_exp_f32 v117,v117\n"
MF "a[152:155],v[28:31],v[84:87],a[152:155]\n"
"v_exp_f32 v118,v118\n"
MF "a[148:151],v[28:31],v[80:83],a[148:151]\n"
"v_exp_f32 v119,v119\n"
MF "a[144:147],v[28:31],v[76:79],a[144:147]\n"
"v_add_f32 v58,v58,v116\n"
"v_add_f32 v58,v58,v117\n"
MF "a[140:143],v[28:31],v[72:75],a[140:143]\n"
"s_waitcnt lgkmcnt(4)\n"
TR "v[28:29],v5 offset:26624\n"
TR "v[30:31],v5 offset:32768\n"
MF "a[160:163],v[16:19],v[72:75],a[160:163]\n"
CV "v98,v116,v117\n"
"v_add_f32 v58,v58,v118\n"
MF "a[164:167],v[16:19],v[76:79],a[164:167]\n"
"v_add_f32 v58,v58,v119\n"
CV "v99,v118,v119\n"
MF "a[168:171],v[16:19],v[80:83],a[168:171]\n"
"v_exp_f32 v120,v120\n"
MF "a[172:175],v[16:19],v[84:87],a[172:175]\n"
"v_exp_f32 v121,v121\n"
MF "a[176:179],v[16:19],v[88:91],a[176:179]\n"
"s_waitcnt lgkmcnt(4)\n"
"v_exp_f32 v122,v122\n"
MF "a[196:199],v[20:23],v[88:91],a[196:199]\n"
"v_exp_f32 v123,v123\n"
MF "a[192:195],v[20:23],v[84:87],a[192:195]\n"
"v_add_f32 v59,v59,v120\n"
"v_add_f32 v59,v59,v121\n"
MF "a[188:191],v[20:23],v[80:83],a[188:191]\n"
CV "v102,v120,v121\n"
"v_add_f32 v59,v59,v122\n"
MF "a[184:187],v[20:23],v[76:79],a[184:187]\n"
"s_waitcnt vmcnt(6) lgkmcnt(0)\n"
"s_barrier\n"
MF "a[180:183],v[20:23],v[72:75],a[180:183]\n"
"s_add_u32 s72,s72,6144\n"
"ds_read_b128 v[16:19],v0 offset:49152\n"
"ds_read_b128 v[20:23],v1 offset:49152\n"
MF "a[200:203],v[24:27],v[72:75],a[200:203]\n"
"s_cmp_eq_u32 s72,s17\n"
"s_cselect_b32 s72,s16,s72\n"
"s_add_u32 m0,s64,24576\n"
MF "a[204:207],v[24:27],v[76:79],a[204:207]\n"
LD "v7,s[32:35],s72 offen nt lds\n"
MF "a[208:211],v[24:27],v[80:83],a[208:211]\n"
"s_add_u32 m0,s64,25472\n"
"v_add_f32 v59,v59,v123\n"
CV "v103,v122,v123\n"
MF "a[212:215],v[24:27],v[84:87],a[212:215]\n"
LD "v7,s[32:35],s72 offen offset:128 nt lds\n"
MF "a[216:219],v[24:27],v[88:91],a[216:219]\n"
"s_add_u32 m0,s64,26368\n"
"ds_read_b128 v[24:27],v0 offset:50176\n"
MF "a[236:239],v[28:31],v[88:91],a[236:239]\n"
LD "v7,s[32:35],s72 offen offset:256 nt lds\n"
MF "a[232:235],v[28:31],v[84:87],a[232:235]\n"
MF "a[228:231],v[28:31],v[80:83],a[228:231]\n"
MF "a[224:227],v[28:31],v[76:79],a[224:227]\n"
MF "a[220:223],v[28:31],v[72:75],a[220:223]\n"
"s_waitcnt lgkmcnt(1)\n"
MF "v[72:75],v[16:19],v[136:139],v[32:35]\n"
"s_add_u32 s72,s72,6144\n"
"s_add_u32 m0,s64,30720\n"
"ds_read_b128 v[28:31],v1 offset:50176\n"
MF "v[72:75],v[20:23],v[156:159],v[72:75]\n"
LD "v7,s[32:35],s72 offen nt lds\n"
MF "v[76:79],v[20:23],v[160:163],v[36:39]\n"
"v_exp_f32 v124,v124\n"
MF "v[76:79],v[16:19],v[140:143],v[76:79]\n"
"v_exp_f32 v125,v125\n"
MF "v[80:83],v[16:19],v[144:147],v[40:43]\n"
"s_add_u32 m0,s64,31616\n"
"v_exp_f32 v126,v126\n"
MF "v[80:83],v[20:23],v[164:167],v[80:83]\n"
LD "v7,s[32:35],s72 offen offset:128 nt lds\n"
MF "v[84:87],v[20:23],v[168:171],v[44:47]\n"
"v_exp_f32 v127,v127\n"
MF "v[84:87],v[16:19],v[148:151],v[84:87]\n"
"s_add_u32 m0,s64,32512\n"
"v_add_f32 v60,v60,v124\n"
"v_add_f32 v60,v60,v125\n"
MF "v[88:91],v[16:19],v[152:155],v[48:51]\n"
LD "v7,s[32:35],s72 offen offset:256 nt lds\n"
MF "v[88:91],v[20:23],v[172:175],v[88:91]\n"
"s_waitcnt lgkmcnt(0)\n"
"ds_read_b128 v[16:19],v0 offset:51200\n"
"ds_read_b128 v[20:23],v1 offset:51200\n"
MF "v[88:91],v[24:27],v[192:195],v[88:91]\n"
CV "v106,v124,v125\n"
"v_add_f32 v60,v60,v126\n"
MF "v[88:91],v[28:31],v[212:215],v[88:91]\n"
"v_add_f32 v60,v60,v127\n"
CV "v107,v126,v127\n"
MF "v[84:87],v[28:31],v[208:211],v[84:87]\n"
"v_exp_f32 v128,v128\n"
MF "v[84:87],v[24:27],v[188:191],v[84:87]\n"
"v_exp_f32 v129,v129\n"
MF "v[80:83],v[24:27],v[184:187],v[80:83]\n"
"v_exp_f32 v130,v130\n"
MF "v[80:83],v[28:31],v[204:207],v[80:83]\n"
"v_exp_f32 v131,v131\n"
MF "v[76:79],v[28:31],v[200:203],v[76:79]\n"
"v_add_f32 v61,v61,v128\n"
"v_add_f32 v61,v61,v129\n"
MF "v[76:79],v[24:27],v[180:183],v[76:79]\n"
CV "v110,v128,v129\n"
"v_add_f32 v61,v61,v130\n"
MF "v[72:75],v[24:27],v[176:179],v[72:75]\n"
"v_add_f32 v61,v61,v131\n"
CV "v111,v130,v131\n"
MF "v[72:75],v[28:31],v[196:199],v[72:75]\n"
"s_waitcnt lgkmcnt(0)\n"
"ds_read_b128 v[24:27],v0 offset:55296\n"
"ds_read_b128 v[28:31],v1 offset:55296\n"
MF "v[72:75],v[16:19],v[216:219],v[72:75]\n"
MF "v[72:75],v[20:23],v[236:239],v[72:75]\n"
MF "v[76:79],v[20:23],v[240:243],v[76:79]\n"
MF "v[76:79],v[16:19],v[220:223],v[76:79]\n"
MF "v[80:83],v[16:19],v[224:227],v[80:83]\n"
MF "v[80:83],v[20:23],v[244:247],v[80:83]\n"
MF "v[84:87],v[20:23],v[248:251],v[84:87]\n"
MF "v[84:87],v[16:19],v[228:231],v[84:87]\n"
"v_max3_f32 v10,v57,v58,v59\n"
"v_max3_f32 v10,v10,v60,v61\n"
MF "v[88:91],v[16:19],v[232:235],v[88:91]\n"
"v_cmp_ngt_f32 vcc,s95,v10\n"
MF "v[88:91],v[20:23],v[252:255],v[88:91]\n"
"s_waitcnt lgkmcnt(0)\n"
"s_cbranch_vccnz .Lstub_13\n"
".Lback_13:\n"
MF "v[112:115],v[24:27],v[136:139],v[32:35]\n"
"s_add_u32 s74,s74,12288\n"
"ds_read_b128 v[16:19],v0 offset:56320\n"
"ds_read_b128 v[20:23],v1 offset:56320\n"
MF "v[112:115],v[28:31],v[156:159],v[112:115]\n"
"s_cmp_eq_u32 s74,s20\n"
"s_cselect_b32 s74,s70,s74\n"
MF "v[116:119],v[28:31],v[160:163],v[36:39]\n"
"v_exp_f32 v72,v72\n"
MF "v[116:119],v[24:27],v[140:143],v[116:119]\n"
"v_exp_f32 v73,v73\n"
MF "v[120:123],v[24:27],v[144:147],v[40:43]\n"
"v_exp_f32 v74,v74\n"
MF "v[120:123],v[28:31],v[164:167],v[120:123]\n"
"v_exp_f32 v75,v75\n"
MF "v[124:127],v[28:31],v[168:171],v[44:47]\n"
"v_add_f32 v52,v57,v72\n"
"v_add_f32 v52,v52,v73\n"
MF "v[124:127],v[24:27],v[148:151],v[124:127]\n"
CV "v72,v72,v73\n"
"v_add_f32 v52,v52,v74\n"
MF "v[128:131],v[24:27],v[152:155],v[48:51]\n"
"v_add_f32 v52,v52,v75\n"
CV "v73,v74,v75\n"
MF "v[128:131],v[28:31],v[172:175],v[128:131]\n"
"s_waitcnt lgkmcnt(0)\n"
"ds_read_b128 v[24:27],v0 offset:57344\n"
"ds_read_b128 v[28:31],v1 offset:57344\n"
MF "v[128:131],v[16:19],v[192:195],v[128:131]\n"
"v_exp_f32 v76,v76\n"
MF "v[128:131],v[20:23],v[212:215],v[128:131]\n"
"v_exp_f32 v77,v77\n"
MF "v[124:127],v[20:23],v[208:211],v[124:127]\n"
"v_exp_f32 v78,v78\n"
MF "v[124:127],v[16:19],v[188:191],v[124:127]\n"
"v_exp_f32 v79,v79\n"
MF "v[120:123],v[16:19],v[184:187],v[120:123]\n"
"v_add_f32 v53,v58,v76\n"
"v_add_f32 v53,v53,v77\n"
MF "v[120:123],v[20:23],v[204:207],v[120:123]\n"
CV "v76,v76,v77\n"
"v_add_f32 v53,v53,v78\n"
MF "v[116:119],v[20:23],v[200:203],v[116:119]\n"
"v_add_f32 v53,v53,v79\n"
CV "v77,v78,v79\n"
MF "v[116:119],v[16:19],v[180:183],v[116:119]\n"
"s_waitcnt vmcnt(6) lgkmcnt(0)\n"
"s_barrier\n"
MF "v[112:115],v[16:19],v[176:179],v[112:115]\n"
"v_exp_f32 v80,v80\n"
MF "v[112:115],v[20:23],v[196:199],v[112:115]\n"
"s_nop 0\n"
TR "v[16:17],v2\n"
TR "v[18:19],v2 offset:6144\n"
MF "v[112:115],v[24:27],v[216:219],v[112:115]\n"
"s_add_u32 s73,s73,12288\n"
TR "v[20:21],v3\n"
TR "v[22:23],v3 offset:6144\n"
MF "v[112:115],v[28:31],v[236:239],v[112:115]\n"
"s_cmp_eq_u32 s73,s19\n"
"s_cselect_b32 s73,s18,s73\n"
"s_add_u32 m0,s65,49152\n"
MF "v[116:119],v[28:31],v[240:243],v[116:119]\n"
LD "v8,s[36:39],s73 offen nt lds\n"
MF "v[116:119],v[24:27],v[220:223],v[116:119]\n"
"s_add_u32 m0,s65,50048\n"
"v_exp_f32 v81,v81\n"
MF "v[120:123],v[24:27],v[224:227],v[120:123]\n"
LD "v8,s[36:39],s73 offen offset:128 nt lds\n"
MF "v[120:123],v[28:31],v[244:247],v[120:123]\n"
"v_exp_f32 v82,v82\n"
MF "v[124:127],v[28:31],v[248:251],v[124:127]\n"
"s_add_u32 m0,s65,50944\n"
"v_exp_f32 v83,v83\n"
MF "v[124:127],v[24:27],v[228:231],v[124:127]\n"
LD "v8,s[36:39],s73 offen offset:256 nt lds\n"
MF "v[128:131],v[24:27],v[232:235],v[128:131]\n"
"v_add_f32 v54,v59,v80\n"
"v_add_f32 v54,v54,v81\n"
MF "v[128:131],v[28:31],v[252:255],v[128:131]\n"
"s_waitcnt lgkmcnt(2)\n"
TR "v[24:25],v4\n"
TR "v[26:27],v4 offset:6144\n"
MF "a[0:3],v[16:19],v[92:95],a[0:3]\n"
"s_add_u32 m0,s65,49152\n"
TR "v[28:29],v5\n"
TR "v[30:31],v5 offset:6144\n"
MF "a[4:7],v[16:19],v[96:99],a[4:7]\n"
LD "v8,s[36:39],s73 offen offset:3072 nt lds\n"
MF "a[8:11],v[16:19],v[100:103],a[8:11]\n"
"s_add_u32 m0,s65,50048\n"
CV "v80,v80,v81\n"
"v_add_f32 v54,v54,v82\n"
MF "a[12:15],v[16:19],v[104:107],a[12:15]\n"
LD "v8,s[36:39],s73 offen offset:3200 nt lds\n"
MF "a[16:19],v[16:19],v[108:111],a[16:19]\n"
"s_waitcnt lgkmcnt(4)\n"
TR "v[16:17],v2 offset:1024\n"
TR "v[18:19],v2 offset:7168\n"
MF "a[36:39],v[20:23],v[108:111],a[36:39]\n"
"v_add_f32 v54,v54,v83\n"
CV "v81,v82,v83\n"
MF "a[32:35],v[20:23],v[104:107],a[32:35]\n"
"s_add_u32 m0,s65,50944\n"
"v_exp_f32 v84,v84\n"
MF "a[28:31],v[20:23],v[100:103],a[28:31]\n"
LD "v8,s[36:39],s73 offen offset:3328 nt lds\n"
MF "a[24:27],v[20:23],v[96:99],a[24:27]\n"
"v_exp_f32 v85,v85\n"
MF "a[20:23],v[20:23],v[92:95],a[20:23]\n"
"s_waitcnt lgkmcnt(4)\n"
TR "v[20:21],v3 offset:1024\n"
TR "v[22:23],v3 offset:7168\n"
MF "a[40:43],v[24:27],v[92:95],a[40:43]\n"
"v_exp_f32 v86,v86\n"
MF "a[44:47],v[24:27],v[96:99],a[44:47]\n"
"v_exp_f32 v87,v87\n"
MF "a[48:51],v[24:27],v[100:103],a[48:51]\n"
"v_add_f32 v55,v60,v84\n"
"v_add_f32 v55,v55,v85\n"
MF "a[52:55],v[24:27],v[104:107],a[52:55]\n"
CV "v84,v84,v85\n"
"v_add_f32 v55,v55,v86\n"
MF "a[56:59],v[24:27],v[108:111],a[56:59]\n"
"s_waitcnt lgkmcnt(4)\n"
TR "v[24:25],v4 offset:1024\n"
TR "v[26:27],v4 offset:7168\n"
MF "a[76:79],v[28:31],v[108:111],a[76:79]\n"
"v_add_f32 v55,v55,v87\n"
CV "v85,v86,v87\n"
MF "a[72:75],v[28:31],v[104:107],a[72:75]\n"
"v_exp_f32 v88,v88\n"
MF "a[68:71],v[28:31],v[100:103],a[68:71]\n"
"v_exp_f32 v89,v89\n"
MF "a[64:67],v[28:31],v[96:99],a[64:67]\n"
"v_exp_f32 v90,v90\n"
MF "a[60:63],v[28:31],v[92:95],a[60:63]\n"
"s_waitcnt lgkmcnt(4)\n"
TR "v[28:29],v5 offset:1024\n"
TR "v[30:31],v5 offset:7168\n"
MF "a[80:83],v[16:19],v[92:95],a[80:83]\n"
"v_exp_f32 v91,v91\n"
MF "a[84:87],v[16:19],v[96:99],a[84:87]\n"
"v_add_f32 v56,v61,v88\n"
"v_add_f32 v56,v56,v89\n"
MF "a[88:91],v[16:19],v[100:103],a[88:91]\n"
CV "v88,v88,v89\n"
"v_add_f32 v56,v56,v90\n"
MF "a[92:95],v[16:19],v[104:107],a[92:95]\n"
"v_add_f32 v56,v56,v91\n"
CV "v89,v90,v91\n"
MF "a[96:99],v[16:19],v[108:111],a[96:99]\n"
"s_waitcnt lgkmcnt(4)\n"
TR "v[16:17],v2 offset:2048\n"
TR "v[18:19],v2 offset:8192\n"
MF "a[116:119],v[20:23],v[108:111],a[116:119]\n"
"v_exp_f32 v112,v112\n"
MF "a[112:115],v[20:23],v[104:107],a[112:115]\n"
"v_exp_f32 v113,v113\n"
MF "a[108:111],v[20:23],v[100:103],a[108:111]\n"
"v_exp_f32 v114,v114\n"
MF "a[104:107],v[20:23],v[96:99],a[104:107]\n"
"v_exp_f32 v115,v115\n"
MF "a[100:103],v[20:23],v[92:95],a[100:103]\n"
"s_waitcnt lgkmcnt(4)\n"
TR "v[20:21],v3 offset:2048\n"
TR "v[22:23],v3 offset:8192\n"
MF "a[120:123],v[24:27],v[92:95],a[120:123]\n"
"v_add_f32 v52,v52,v112\n"
"v_add_f32 v52,v52,v113\n"
MF "a[124:127],v[24:27],v[96:99],a[124:127]\n"
CV "v74,v112,v113\n"
"v_add_f32 v52,v52,v114\n"
MF "a[128:131],v[24:27],v[100:103],a[128:131]\n"
"v_add_f32 v52,v52,v115\n"
CV "v75,v114,v115\n"
MF "a[132:135],v[24:27],v[104:107],a[132:135]\n"
"v_exp_f32 v116,v116\n"
MF "a[136:139],v[24:27],v[108:111],a[136:139]\n"
"s_waitcnt lgkmcnt(4)\n"
TR "v[24:25],v4 offset:2048\n"
TR "v[26:27],v4 offset:8192\n"
MF "a[156:159],v[28:31],v[108:111],a[156:159]\n"
"v_exp_f32 v117,v117\n"
MF "a[152:155],v[28:31],v[104:107],a[152:155]\n"
"v_exp_f32 v118,v118\n"
MF "a[148:151],v[28:31],v[100:103],a[148:151]\n"
"v_exp_f32 v119,v119\n"
MF "a[144:147],v[28:31],v[96:99],a[144:147]\n"
"v_add_f32 v53,v53,v116\n"
"v_add_f32 v53,v53,v117\n"
MF "a[140:143],v[28:31],v[92:95],a[140:143]\n"
"s_waitcnt lgkmcnt(4)\n"
TR "v[28:29],v5 offset:2048\n"
TR "v[30:31],v5 offset:8192\n"
MF "a[160:163],v[16:19],v[92:95],a[160:163]\n"
CV "v78,v116,v117\n"
"v_add_f32 v53,v53,v118\n"
MF "a[164:167],v[16:19],v[96:99],a[164:167]\n"
"v_add_f32 v53,v53,v119\n"
CV "v79,v118,v119\n"
MF "a[168:171],v[16:19],v[100:103],a[168:171]\n"
"v_exp_f32 v120,v120\n"
MF "a[172:175],v[16:19],v[104:107],a[172:175]\n"
"v_exp_f32 v121,v121\n"
MF "a[176:179],v[16:19],v[108:111],a[176:179]\n"
"s_waitcnt lgkmcnt(4)\n"
"v_exp_f32 v122,v122\n"
MF "a[196:199],v[20:23],v[108:111],a[196:199]\n"
"v_exp_f32 v123,v123\n"
MF "a[192:195],v[20:23],v[104:107],a[192:195]\n"
"v_add_f32 v54,v54,v120\n"
"v_add_f32 v54,v54,v121\n"
MF "a[188:191],v[20:23],v[100:103],a[188:191]\n"
CV "v82,v120,v121\n"
"v_add_f32 v54,v54,v122\n"
MF "a[184:187],v[20:23],v[96:99],a[184:187]\n"
"s_waitcnt vmcnt(6) lgkmcnt(0)\n"
"s_barrier\n"
MF "a[180:183],v[20:23],v[92:95],a[180:183]\n"
"s_add_u32 s72,s72,6144\n"
"ds_read_b128 v[16:19],v0 offset:24576\n"
"ds_read_b128 v[20:23],v1 offset:24576\n"
MF "a[200:203],v[24:27],v[92:95],a[200:203]\n"
"s_cmp_eq_u32 s72,s17\n"
"s_cselect_b32 s72,s16,s72\n"
"s_add_u32 m0,s64,0\n"
MF "a[204:207],v[24:27],v[96:99],a[204:207]\n"
LD "v7,s[32:35],s72 offen nt lds\n"
MF "a[208:211],v[24:27],v[100:103],a[208:211]\n"
"s_add_u32 m0,s64,896\n"
"v_add_f32 v54,v54,v123\n"
CV "v83,v122,v123\n"
MF "a[212:215],v[24:27],v[104:107],a[212:215]\n"
LD "v7,s[32:35],s72 offen offset:128 nt lds\n"
MF "a[216:219],v[24:27],v[108:111],a[216:219]\n"
"s_add_u32 m0,s64,1792\n"
"ds_read_b128 v[24:27],v0 offset:25600\n"
MF "a[236:239],v[28:31],v[108:111],a[236:239]\n"
LD "v7,s[32:35],s72 offen offset:256 nt lds\n"
MF "a[232:235],v[28:31],v[104:107],a[232:235]\n"
MF "a[228:231],v[28:31],v[100:103],a[228:231]\n"
MF "a[224:227],v[28:31],v[96:99],a[224:227]\n"
MF "a[220:223],v[28:31],v[92:95],a[220:223]\n"
"s_waitcnt lgkmcnt(1)\n"
MF "v[92:95],v[16:19],v[136:139],v[32:35]\n"
"s_add_u32 s72,s72,6144\n"
"s_add_u32 m0,s64,6144\n"
"ds_read_b128 v[28:31],v1 offset:25600\n"
MF "v[92:95],v[20:23],v[156:159],v[92:95]\n"
LD "v7,s[32:35],s72 offen nt lds\n"
MF "v[96:99],v[20:23],v[160:163],v[36:39]\n"
"v_exp_f32 v124,v124\n"
MF "v[96:99],v[16:19],v[140:143],v[96:99]\n"
"v_exp_f32 v125,v125\n"
MF "v[100:103],v[16:19],v[144:147],v[40:43]\n"
"s_add_u32 m0,s64,7040\n"
"v_exp_f32 v126,v126\n"
MF "v[100:103],v[20:23],v[164:167],v[100:103]\n"
LD "v7,s[32:35],s72 offen offset:128 nt lds\n"
MF "v[104:107],v[20:23],v[168:171],v[44:47]\n"
"v_exp_f32 v127,v127\n"
MF "v[104:107],v[16:19],v[148:151],v[104:107]\n"
"s_add_u32 m0,s64,7936\n"
"v_add_f32 v55,v55,v124\n"
"v_add_f32 v55,v55,v125\n"
MF "v[108:111],v[16:19],v[152:155],v[48:51]\n"
LD "v7,s[32:35],s72 offen offset:256 nt lds\n"
MF "v[108:111],v[20:23],v[172:175],v[108:111]\n"
"s_waitcnt lgkmcnt(0)\n"
"ds_read_b128 v[16:19],v0 offset:26624\n"
"ds_read_b128 v[20:23],v1 offset:26624\n"
MF "v[108:111],v[24:27],v[192:195],v[108:111]\n"
CV "v86,v124,v125\n"
"v_add_f32 v55,v55,v126\n"
MF "v[108:111],v[28:31],v[212:215],v[108:111]\n"
"v_add_f32 v55,v55,v127\n"
CV "v87,v126,v127\n"
MF "v[104:107],v[28:31],v[208:211],v[104:107]\n"
"v_exp_f32 v128,v128\n"
MF "v[104:107],v[24:27],v[188:191],v[104:107]\n"
"v_exp_f32 v129,v129\n"
MF "v[100:103],v[24:27],v[184:187],v[100:103]\n"
"v_exp_f32 v130,v130\n"
MF "v[100:103],v[28:31],v[204:207],v[100:103]\n"
"v_exp_f32 v131,v131\n"
MF "v[96:99],v[28:31],v[200:203],v[96:99]\n"
"v_add_f32 v56,v56,v128\n"
"v_add_f32 v56,v56,v129\n"
MF "v[96:99],v[24:27],v[180:183],v[96:99]\n"
CV "v90,v128,v129\n"
"v_add_f32 v56,v56,v130\n"
MF "v[92:95],v[24:27],v[176:179],v[92:95]\n"
"v_add_f32 v56,v56,v131\n"
CV "v91,v130,v131\n"
MF "v[92:95],v[28:31],v[196:199],v[92:95]\n"
"s_waitcnt lgkmcnt(0)\n"
"ds_read_b128 v[24:27],v0 offset:30720\n"
"ds_read_b128 v[28:31],v1 offset:30720\n"
MF "v[92:95],v[16:19],v[216:219],v[92:95]\n"
MF "v[92:95],v[20:23],v[236:239],v[92:95]\n"
MF "v[96:99],v[20:23],v[240:243],v[96:99]\n"
MF "v[96:99],v[16:19],v[220:223],v[96:99]\n"
MF "v[100:103],v[16:19],v[224:227],v[100:103]\n"
MF "v[100:103],v[20:23],v[244:247],v[100:103]\n"
MF "v[104:107],v[20:23],v[248:251],v[104:107]\n"
MF "v[104:107],v[16:19],v[228:231],v[104:107]\n"
"v_max3_f32 v10,v52,v53,v54\n"
"v_max3_f32 v10,v10,v55,v56\n"
MF "v[108:111],v[16:19],v[232:235],v[108:111]\n"
"v_cmp_ngt_f32 vcc,s77,v10\n"
MF "v[108:111],v[20:23],v[252:255],v[108:111]\n"
"s_waitcnt lgkmcnt(0)\n"
"s_cbranch_vccnz .Lstub_14\n"
".Lback_14:\n"
MF "v[112:115],v[24:27],v[136:139],v[32:35]\n"
"s_add_u32 s74,s74,12288\n"
"ds_read_b128 v[16:19],v0 offset:31744\n"
"ds_read_b128 v[20:23],v1 offset:31744\n"
MF "v[112:115],v[28:31],v[156:159],v[112:115]\n"
"s_cmp_eq_u32 s74,s20\n"
"s_cselect_b32 s74,s70,s74\n"
MF "v[116:119],v[28:31],v[160:163],v[36:39]\n"
"v_exp_f32 v92,v92\n"
MF "v[116:119],v[24:27],v[140:143],v[116:119]\n"
"v_exp_f32 v93,v93\n"
MF "v[120:123],v[24:27],v[144:147],v[40:43]\n"
"v_exp_f32 v94,v94\n"
MF "v[120:123],v[28:31],v[164:167],v[120:123]\n"
"v_exp_f32 v95,v95\n"
MF "v[124:127],v[28:31],v[168:171],v[44:47]\n"
"v_add_f32 v57,v52,v92\n"
"v_add_f32 v57,v57,v93\n"
MF "v[124:127],v[24:27],v[148:151],v[124:127]\n"
CV "v92,v92,v93\n"
"v_add_f32 v57,v57,v94\n"
MF "v[128:131],v[24:27],v[152:155],v[48:51]\n"
"v_add_f32 v57,v57,v95\n"
CV "v93,v94,v95\n"
MF "v[128:131],v[28:31],v[172:175],v[128:131]\n"
"s_waitcnt lgkmcnt(0)\n"
"ds_read_b128 v[24:27],v0 offset:32768\n"
"ds_read_b128 v[28:31],v1 offset:32768\n"
MF "v[128:131],v[16:19],v[192:195],v[128:131]\n"
"v_exp_f32 v96,v96\n"
MF "v[128:131],v[20:23],v[212:215],v[128:131]\n"
"v_exp_f32 v97,v97\n"
MF "v[124:127],v[20:23],v[208:211],v[124:127]\n"
"v_exp_f32 v98,v98\n"
MF "v[124:127],v[16:19],v[188:191],v[124:127]\n"
"v_exp_f32 v99,v99\n"
MF "v[120:123],v[16:19],v[184:187],v[120:123]\n"
"v_add_f32 v58,v53,v96\n"
"v_add_f32 v58,v58,v97\n"
MF "v[120:123],v[20:23],v[204:207],v[120:123]\n"
CV "v96,v96,v97\n"
"v_add_f32 v58,v58,v98\n"
MF "v[116:119],v[20:23],v[200:203],v[116:119]\n"
"v_add_f32 v58,v58,v99\n"
CV "v97,v98,v99\n"
MF "v[116:119],v[16:19],v[180:183],v[116:119]\n"
"s_waitcnt vmcnt(6) lgkmcnt(0)\n"
"s_barrier\n"
MF "v[112:115],v[16:19],v[176:179],v[112:115]\n"
"v_exp_f32 v100,v100\n"
MF "v[112:115],v[20:23],v[196:199],v[112:115]\n"
"s_nop 0\n"
TR "v[16:17],v2 offset:49152\n"
TR "v[18:19],v2 offset:55296\n"
MF "v[112:115],v[24:27],v[216:219],v[112:115]\n"
"s_add_u32 s73,s73,12288\n"
TR "v[20:21],v3 offset:49152\n"
TR "v[22:23],v3 offset:55296\n"
MF "v[112:115],v[28:31],v[236:239],v[112:115]\n"
"s_cmp_eq_u32 s73,s19\n"
"s_cselect_b32 s73,s18,s73\n"
"s_add_u32 m0,s65,24576\n"
MF "v[116:119],v[28:31],v[240:243],v[116:119]\n"
LD "v8,s[36:39],s73 offen nt lds\n"
MF "v[116:119],v[24:27],v[220:223],v[116:119]\n"
"s_add_u32 m0,s65,25472\n"
"v_exp_f32 v101,v101\n"
MF "v[120:123],v[24:27],v[224:227],v[120:123]\n"
LD "v8,s[36:39],s73 offen offset:128 nt lds\n"
MF "v[120:123],v[28:31],v[244:247],v[120:123]\n"
"v_exp_f32 v102,v102\n"
MF "v[124:127],v[28:31],v[248:251],v[124:127]\n"
"s_add_u32 m0,s65,26368\n"
"v_exp_f32 v103,v103\n"
MF "v[124:127],v[24:27],v[228:231],v[124:127]\n"
LD "v8,s[36:39],s73 offen offset:256 nt lds\n"
MF "v[128:131],v[24:27],v[232:235],v[128:131]\n"
"v_add_f32 v59,v54,v100\n"
"v_add_f32 v59,v59,v101\n"
MF "v[128:131],v[28:31],v[252:255],v[128:131]\n"
"s_waitcnt lgkmcnt(2)\n"
TR "v[24:25],v4 offset:49152\n"
TR "v[26:27],v4 offset:55296\n"
MF "a[0:3],v[16:19],v[72:75],a[0:3]\n"
"s_add_u32 m0,s65,24576\n"
TR "v[28:29],v5 offset:49152\n"
TR "v[30:31],v5 offset:55296\n"
MF "a[4:7],v[16:19],v[76:79],a[4:7]\n"
LD "v8,s[36:39],s73 offen offset:3072 nt lds\n"
MF "a[8:11],v[16:19],v[80:83],a[8:11]\n"
"s_add_u32 m0,s65,25472\n"
CV "v100,v100,v101\n"
"v_add_f32 v59,v59,v102\n"
MF "a[12:15],v[16:19],v[84:87],a[12:15]\n"
LD "v8,s[36:39],s73 offen offset:3200 nt lds\n"
MF "a[16:19],v[16:19],v[88:91],a[16:19]\n"
"s_waitcnt lgkmcnt(4)\n"
TR "v[16:17],v2 offset:50176\n"
TR "v[18:19],v2 offset:56320\n"
MF "a[36:39],v[20:23],v[88:91],a[36:39]\n"
"v_add_f32 v59,v59,v103\n"
CV "v101,v102,v103\n"
MF "a[32:35],v[20:23],v[84:87],a[32:35]\n"
"s_add_u32 m0,s65,26368\n"
"v_exp_f32 v104,v104\n"
MF "a[28:31],v[20:23],v[80:83],a[28:31]\n"
LD "v8,s[36:39],s73 offen offset:3328 nt lds\n"
MF "a[24:27],v[20:23],v[76:79],a[24:27]\n"
"v_exp_f32 v105,v105\n"
MF "a[20:23],v[20:23],v[72:75],a[20:23]\n"
"s_waitcnt lgkmcnt(4)\n"
TR "v[20:21],v3 offset:50176\n"
TR "v[22:23],v3 offset:56320\n"
MF "a[40:43],v[24:27],v[72:75],a[40:43]\n"
"v_exp_f32 v106,v106\n"
MF "a[44:47],v[24:27],v[76:79],a[44:47]\n"
"v_exp_f32 v107,v107\n"
MF "a[48:51],v[24:27],v[80:83],a[48:51]\n"
"v_add_f32 v60,v55,v104\n"
"v_add_f32 v60,v60,v105\n"
MF "a[52:55],v[24:27],v[84:87],a[52:55]\n"
CV "v104,v104,v105\n"
"v_add_f32 v60,v60,v106\n"
MF "a[56:59],v[24:27],v[88:91],a[56:59]\n"
"s_waitcnt lgkmcnt(4)\n"
TR "v[24:25],v4 offset:50176\n"
TR "v[26:27],v4 offset:56320\n"
MF "a[76:79],v[28:31],v[88:91],a[76:79]\n"
"v_add_f32 v60,v60,v107\n"
CV "v105,v106,v107\n"
MF "a[72:75],v[28:31],v[84:87],a[72:75]\n"
"v_exp_f32 v108,v108\n"
MF "a[68:71],v[28:31],v[80:83],a[68:71]\n"
"v_exp_f32 v109,v109\n"
MF "a[64:67],v[28:31],v[76:79],a[64:67]\n"
"v_exp_f32 v110,v110\n"
MF "a[60:63],v[28:31],v[72:75],a[60:63]\n"
"s_waitcnt lgkmcnt(4)\n"
TR "v[28:29],v5 offset:50176\n"
TR "v[30:31],v5 offset:56320\n"
MF "a[80:83],v[16:19],v[72:75],a[80:83]\n"
"v_exp_f32 v111,v111\n"
MF "a[84:87],v[16:19],v[76:79],a[84:87]\n"
"v_add_f32 v61,v56,v108\n"
"v_add_f32 v61,v61,v109\n"
MF "a[88:91],v[16:19],v[80:83],a[88:91]\n"
CV "v108,v108,v109\n"
"v_add_f32 v61,v61,v110\n"
MF "a[92:95],v[16:19],v[84:87],a[92:95]\n"
"v_add_f32 v61,v61,v111\n"
CV "v109,v110,v111\n"
MF "a[96:99],v[16:19],v[88:91],a[96:99]\n"
"s_waitcnt lgkmcnt(4)\n"
TR "v[16:17],v2 offset:51200\n"
TR "v[18:19],v2 offset:57344\n"
MF "a[116:119],v[20:23],v[88:91],a[116:119]\n"
"v_exp_f32 v112,v112\n"
MF "a[112:115],v[20:23],v[84:87],a[112:115]\n"
"v_exp_f32 v113,v113\n"
MF "a[108:111],v[20:23],v[80:83],a[108:111]\n"
"v_exp_f32 v114,v114\n"
MF "a[104:107],v[20:23],v[76:79],a[104:107]\n"
"v_exp_f32 v115,v115\n"
MF "a[100:103],v[20:23],v[72:75],a[100:103]\n"
"s_waitcnt lgkmcnt(4)\n"
TR "v[20:21],v3 offset:51200\n"
TR "v[22:23],v3 offset:57344\n"
MF "a[120:123],v[24:27],v[72:75],a[120:123]\n"
"v_add_f32 v57,v57,v112\n"
"v_add_f32 v57,v57,v113\n"
MF "a[124:127],v[24:27],v[76:79],a[124:127]\n"
CV "v94,v112,v113\n"
"v_add_f32 v57,v57,v114\n"
MF "a[128:131],v[24:27],v[80:83],a[128:131]\n"
"v_add_f32 v57,v57,v115\n"
CV "v95,v114,v115\n"
MF "a[132:135],v[24:27],v[84:87],a[132:135]\n"
"v_exp_f32 v116,v116\n"
MF "a[136:139],v[24:27],v[88:91],a[136:139]\n"
"s_waitcnt lgkmcnt(4)\n"
TR "v[24:25],v4 offset:51200\n"
TR "v[26:27],v4 offset:57344\n"
MF "a[156:159],v[28:31],v[88:91],a[156:159]\n"
"v_exp_f32 v117,v117\n"
MF "a[152:155],v[28:31],v[84:87],a[152:155]\n"
"v_exp_f32 v118,v118\n"
MF "a[148:151],v[28:31],v[80:83],a[148:151]\n"
"v_exp_f32 v119,v119\n"
MF "a[144:147],v[28:31],v[76:79],a[144:147]\n"
"v_add_f32 v58,v58,v116\n"
"v_add_f32 v58,v58,v117\n"
MF "a[140:143],v[28:31],v[72:75],a[140:143]\n"
"s_waitcnt lgkmcnt(4)\n"
TR "v[28:29],v5 offset:51200\n"
TR "v[30:31],v5 offset:57344\n"
MF "a[160:163],v[16:19],v[72:75],a[160:163]\n"
CV "v98,v116,v117\n"
"v_add_f32 v58,v58,v118\n"
MF "a[164:167],v[16:19],v[76:79],a[164:167]\n"
"v_add_f32 v58,v58,v119\n"
CV "v99,v118,v119\n"
MF "a[168:171],v[16:19],v[80:83],a[168:171]\n"
"v_exp_f32 v120,v120\n"
MF "a[172:175],v[16:19],v[84:87],a[172:175]\n"
"v_exp_f32 v121,v121\n"
MF "a[176:179],v[16:19],v[88:91],a[176:179]\n"
"s_waitcnt lgkmcnt(4)\n"
"v_exp_f32 v122,v122\n"
MF "a[196:199],v[20:23],v[88:91],a[196:199]\n"
"v_exp_f32 v123,v123\n"
MF "a[192:195],v[20:23],v[84:87],a[192:195]\n"
"v_add_f32 v59,v59,v120\n"
"v_add_f32 v59,v59,v121\n"
MF "a[188:191],v[20:23],v[80:83],a[188:191]\n"
CV "v102,v120,v121\n"
"v_add_f32 v59,v59,v122\n"
MF "a[184:187],v[20:23],v[76:79],a[184:187]\n"
"s_waitcnt vmcnt(6) lgkmcnt(0)\n"
"s_barrier\n"
MF "a[180:183],v[20:23],v[72:75],a[180:183]\n"
"s_add_u32 s72,s72,6144\n"
"ds_read_b128 v[16:19],v0\n"
"ds_read_b128 v[20:23],v1\n"
MF "a[200:203],v[24:27],v[72:75],a[200:203]\n"
"s_cmp_eq_u32 s72,s17\n"
"s_cselect_b32 s72,s16,s72\n"
"s_add_u32 m0,s64,49152\n"
MF "a[204:207],v[24:27],v[76:79],a[204:207]\n"
LD "v7,s[32:35],s72 offen nt lds\n"
MF "a[208:211],v[24:27],v[80:83],a[208:211]\n"
"s_add_u32 m0,s64,50048\n"
"v_add_f32 v59,v59,v123\n"
CV "v103,v122,v123\n"
MF "a[212:215],v[24:27],v[84:87],a[212:215]\n"
LD "v7,s[32:35],s72 offen offset:128 nt lds\n"
MF "a[216:219],v[24:27],v[88:91],a[216:219]\n"
"s_add_u32 m0,s64,50944\n"
"ds_read_b128 v[24:27],v0 offset:1024\n"
MF "a[236:239],v[28:31],v[88:91],a[236:239]\n"
LD "v7,s[32:35],s72 offen offset:256 nt lds\n"
MF "a[232:235],v[28:31],v[84:87],a[232:235]\n"
MF "a[228:231],v[28:31],v[80:83],a[228:231]\n"
MF "a[224:227],v[28:31],v[76:79],a[224:227]\n"
MF "a[220:223],v[28:31],v[72:75],a[220:223]\n"
"s_waitcnt lgkmcnt(1)\n"
".Lmid:\n"
MF "v[72:75],v[16:19],v[136:139],v[32:35]\n"
"s_add_u32 s72,s72,6144\n"
"s_add_u32 m0,s64,55296\n"
"ds_read_b128 v[28:31],v1 offset:1024\n"
MF "v[72:75],v[20:23],v[156:159],v[72:75]\n"
LD "v7,s[32:35],s72 offen nt lds\n"
MF "v[76:79],v[20:23],v[160:163],v[36:39]\n"
"v_exp_f32 v124,v124\n"
MF "v[76:79],v[16:19],v[140:143],v[76:79]\n"
"v_exp_f32 v125,v125\n"
MF "v[80:83],v[16:19],v[144:147],v[40:43]\n"
"s_add_u32 m0,s64,56192\n"
"v_exp_f32 v126,v126\n"
MF "v[80:83],v[20:23],v[164:167],v[80:83]\n"
LD "v7,s[32:35],s72 offen offset:128 nt lds\n"
MF "v[84:87],v[20:23],v[168:171],v[44:47]\n"
"v_exp_f32 v127,v127\n"
MF "v[84:87],v[16:19],v[148:151],v[84:87]\n"
"s_add_u32 m0,s64,57088\n"
"v_add_f32 v60,v60,v124\n"
"v_add_f32 v60,v60,v125\n"
MF "v[88:91],v[16:19],v[152:155],v[48:51]\n"
LD "v7,s[32:35],s72 offen offset:256 nt lds\n"
MF "v[88:91],v[20:23],v[172:175],v[88:91]\n"
"s_waitcnt lgkmcnt(0)\n"
"ds_read_b128 v[16:19],v0 offset:2048\n"
"ds_read_b128 v[20:23],v1 offset:2048\n"
MF "v[88:91],v[24:27],v[192:195],v[88:91]\n"
CV "v106,v124,v125\n"
"v_add_f32 v60,v60,v126\n"
MF "v[88:91],v[28:31],v[212:215],v[88:91]\n"
"v_add_f32 v60,v60,v127\n"
CV "v107,v126,v127\n"
MF "v[84:87],v[28:31],v[208:211],v[84:87]\n"
"v_exp_f32 v128,v128\n"
MF "v[84:87],v[24:27],v[188:191],v[84:87]\n"
"v_exp_f32 v129,v129\n"
MF "v[80:83],v[24:27],v[184:187],v[80:83]\n"
"v_exp_f32 v130,v130\n"
MF "v[80:83],v[28:31],v[204:207],v[80:83]\n"
"v_exp_f32 v131,v131\n"
MF "v[76:79],v[28:31],v[200:203],v[76:79]\n"
"v_add_f32 v61,v61,v128\n"
"v_add_f32 v61,v61,v129\n"
MF "v[76:79],v[24:27],v[180:183],v[76:79]\n"
CV "v110,v128,v129\n"
"v_add_f32 v61,v61,v130\n"
MF "v[72:75],v[24:27],v[176:179],v[72:75]\n"
"v_add_f32 v61,v61,v131\n"
CV "v111,v130,v131\n"
MF "v[72:75],v[28:31],v[196:199],v[72:75]\n"
"s_waitcnt lgkmcnt(0)\n"
"ds_read_b128 v[24:27],v0 offset:6144\n"
"ds_read_b128 v[28:31],v1 offset:6144\n"
MF "v[72:75],v[16:19],v[216:219],v[72:75]\n"
MF "v[72:75],v[20:23],v[236:239],v[72:75]\n"
MF "v[76:79],v[20:23],v[240:243],v[76:79]\n"
MF "v[76:79],v[16:19],v[220:223],v[76:79]\n"
MF "v[80:83],v[16:19],v[224:227],v[80:83]\n"
MF "v[80:83],v[20:23],v[244:247],v[80:83]\n"
MF "v[84:87],v[20:23],v[248:251],v[84:87]\n"
MF "v[84:87],v[16:19],v[228:231],v[84:87]\n"
"v_max3_f32 v10,v57,v58,v59\n"
"v_max3_f32 v10,v10,v60,v61\n"
MF "v[88:91],v[16:19],v[232:235],v[88:91]\n"
"v_cmp_ngt_f32 vcc,s95,v10\n"
MF "v[88:91],v[20:23],v[252:255],v[88:91]\n"
"s_waitcnt lgkmcnt(0)\n"
"s_cbranch_vccnz .Lstub_15\n"
".Lback_15:\n"
MF "v[112:115],v[24:27],v[136:139],v[32:35]\n"
"s_add_u32 s74,s74,12288\n"
"ds_read_b128 v[16:19],v0 offset:7168\n"
"ds_read_b128 v[20:23],v1 offset:7168\n"
MF "v[112:115],v[28:31],v[156:159],v[112:115]\n"
"s_cmp_eq_u32 s74,s20\n"
"s_cselect_b32 s74,s70,s74\n"
MF "v[116:119],v[28:31],v[160:163],v[36:39]\n"
"v_exp_f32 v72,v72\n"
MF "v[116:119],v[24:27],v[140:143],v[116:119]\n"
"v_exp_f32 v73,v73\n"
MF "v[120:123],v[24:27],v[144:147],v[40:43]\n"
"v_exp_f32 v74,v74\n"
MF "v[120:123],v[28:31],v[164:167],v[120:123]\n"
"v_exp_f32 v75,v75\n"
MF "v[124:127],v[28:31],v[168:171],v[44:47]\n"
"v_add_f32 v52,v57,v72\n"
"v_add_f32 v52,v52,v73\n"
MF "v[124:127],v[24:27],v[148:151],v[124:127]\n"
CV "v72,v72,v73\n"
"v_add_f32 v52,v52,v74\n"
MF "v[128:131],v[24:27],v[152:155],v[48:51]\n"
"v_add_f32 v52,v52,v75\n"
CV "v73,v74,v75\n"
MF "v[128:131],v[28:31],v[172:175],v[128:131]\n"
"s_waitcnt lgkmcnt(0)\n"
"ds_read_b128 v[24:27],v0 offset:8192\n"
"ds_read_b128 v[28:31],v1 offset:8192\n"
MF "v[128:131],v[16:19],v[192:195],v[128:131]\n"
"v_exp_f32 v76,v76\n"
MF "v[128:131],v[20:23],v[212:215],v[128:131]\n"
"v_exp_f32 v77,v77\n"
MF "v[124:127],v[20:23],v[208:211],v[124:127]\n"
"v_exp_f32 v78,v78\n"
MF "v[124:127],v[16:19],v[188:191],v[124:127]\n"
"v_exp_f32 v79,v79\n"
MF "v[120:123],v[16:19],v[184:187],v[120:123]\n"
"v_add_f32 v53,v58,v76\n"
"v_add_f32 v53,v53,v77\n"
MF "v[120:123],v[20:23],v[204:207],v[120:123]\n"
CV "v76,v76,v77\n"
"v_add_f32 v53,v53,v78\n"
MF "v[116:119],v[20:23],v[200:203],v[116:119]\n"
"v_add_f32 v53,v53,v79\n"
CV "v77,v78,v79\n"
MF "v[116:119],v[16:19],v[180:183],v[116:119]\n"
"s_waitcnt vmcnt(6) lgkmcnt(0)\n"
"s_barrier\n"
MF "v[112:115],v[16:19],v[176:179],v[112:115]\n"
"v_exp_f32 v80,v80\n"
MF "v[112:115],v[20:23],v[196:199],v[112:115]\n"
"s_nop 0\n"
TR "v[16:17],v2 offset:24576\n"
TR "v[18:19],v2 offset:30720\n"
MF "v[112:115],v[24:27],v[216:219],v[112:115]\n"
"s_add_u32 s73,s73,12288\n"
TR "v[20:21],v3 offset:24576\n"
TR "v[22:23],v3 offset:30720\n"
MF "v[112:115],v[28:31],v[236:239],v[112:115]\n"
"s_cmp_eq_u32 s73,s19\n"
"s_cselect_b32 s73,s18,s73\n"
"s_add_u32 m0,s65,0\n"
MF "v[116:119],v[28:31],v[240:243],v[116:119]\n"
LD "v8,s[36:39],s73 offen nt lds\n"
MF "v[116:119],v[24:27],v[220:223],v[116:119]\n"
"s_add_u32 m0,s65,896\n"
"v_exp_f32 v81,v81\n"
MF "v[120:123],v[24:27],v[224:227],v[120:123]\n"
LD "v8,s[36:39],s73 offen offset:128 nt lds\n"
MF "v[120:123],v[28:31],v[244:247],v[120:123]\n"
"v_exp_f32 v82,v82\n"
MF "v[124:127],v[28:31],v[248:251],v[124:127]\n"
"s_add_u32 m0,s65,1792\n"
"v_exp_f32 v83,v83\n"
MF "v[124:127],v[24:27],v[228:231],v[124:127]\n"
LD "v8,s[36:39],s73 offen offset:256 nt lds\n"
MF "v[128:131],v[24:27],v[232:235],v[128:131]\n"
"v_add_f32 v54,v59,v80\n"
"v_add_f32 v54,v54,v81\n"
MF "v[128:131],v[28:31],v[252:255],v[128:131]\n"
"s_waitcnt lgkmcnt(2)\n"
TR "v[24:25],v4 offset:24576\n"
TR "v[26:27],v4 offset:30720\n"
MF "a[0:3],v[16:19],v[92:95],a[0:3]\n"
"s_add_u32 m0,s65,0\n"
TR "v[28:29],v5 offset:24576\n"
TR "v[30:31],v5 offset:30720\n"
MF "a[4:7],v[16:19],v[96:99],a[4:7]\n"
LD "v8,s[36:39],s73 offen offset:3072 nt lds\n"
MF "a[8:11],v[16:19],v[100:103],a[8:11]\n"
"s_add_u32 m0,s65,896\n"
CV "v80,v80,v81\n"
"v_add_f32 v54,v54,v82\n"
MF "a[12:15],v[16:19],v[104:107],a[12:15]\n"
LD "v8,s[36:39],s73 offen offset:3200 nt lds\n"
MF "a[16:19],v[16:19],v[108:111],a[16:19]\n"
"s_waitcnt lgkmcnt(4)\n"
TR "v[16:17],v2 offset:25600\n"
TR "v[18:19],v2 offset:31744\n"
MF "a[36:39],v[20:23],v[108:111],a[36:39]\n"
"v_add_f32 v54,v54,v83\n"
CV "v81,v82,v83\n"
MF "a[32:35],v[20:23],v[104:107],a[32:35]\n"
"s_add_u32 m0,s65,1792\n"
"v_exp_f32 v84,v84\n"
MF "a[28:31],v[20:23],v[100:103],a[28:31]\n"
LD "v8,s[36:39],s73 offen offset:3328 nt lds\n"
MF "a[24:27],v[20:23],v[96:99],a[24:27]\n"
"v_exp_f32 v85,v85\n"
MF "a[20:23],v[20:23],v[92:95],a[20:23]\n"
"s_waitcnt lgkmcnt(4)\n"
TR "v[20:21],v3 offset:25600\n"
TR "v[22:23],v3 offset:31744\n"
MF "a[40:43],v[24:27],v[92:95],a[40:43]\n"
"v_exp_f32 v86,v86\n"
MF "a[44:47],v[24:27],v[96:99],a[44:47]\n"
"v_exp_f32 v87,v87\n"
MF "a[48:51],v[24:27],v[100:103],a[48:51]\n"
"v_add_f32 v55,v60,v84\n"
"v_add_f32 v55,v55,v85\n"
MF "a[52:55],v[24:27],v[104:107],a[52:55]\n"
CV "v84,v84,v85\n"
"v_add_f32 v55,v55,v86\n"
MF "a[56:59],v[24:27],v[108:111],a[56:59]\n"
"s_waitcnt lgkmcnt(4)\n"
TR "v[24:25],v4 offset:25600\n"
TR "v[26:27],v4 offset:31744\n"
MF "a[76:79],v[28:31],v[108:111],a[76:79]\n"
"v_add_f32 v55,v55,v87\n"
CV "v85,v86,v87\n"
MF "a[72:75],v[28:31],v[104:107],a[72:75]\n"
"v_exp_f32 v88,v88\n"
MF "a[68:71],v[28:31],v[100:103],a[68:71]\n"
"v_exp_f32 v89,v89\n"
MF "a[64:67],v[28:31],v[96:99],a[64:67]\n"
"v_exp_f32 v90,v90\n"
MF "a[60:63],v[28:31],v[92:95],a[60:63]\n"
"s_waitcnt lgkmcnt(4)\n"
TR "v[28:29],v5 offset:25600\n"
TR "v[30:31],v5 offset:31744\n"
MF "a[80:83],v[16:19],v[92:95],a[80:83]\n"
"v_exp_f32 v91,v91\n"
MF "a[84:87],v[16:19],v[96:99],a[84:87]\n"
"v_add_f32 v56,v61,v88\n"
"v_add_f32 v56,v56,v89\n"
MF "a[88:91],v[16:19],v[100:103],a[88:91]\n"
CV "v88,v88,v89\n"
"v_add_f32 v56,v56,v90\n"
MF "a[92:95],v[16:19],v[104:107],a[92:95]\n"
"v_add_f32 v56,v56,v91\n"
CV "v89,v90,v91\n"
MF "a[96:99],v[16:19],v[108:111],a[96:99]\n"
"s_waitcnt lgkmcnt(4)\n"
TR "v[16:17],v2 offset:26624\n"
TR "v[18:19],v2 offset:32768\n"
MF "a[116:119],v[20:23],v[108:111],a[116:119]\n"
"v_exp_f32 v112,v112\n"
MF "a[112:115],v[20:23],v[104:107],a[112:115]\n"
"v_exp_f32 v113,v113\n"
MF "a[108:111],v[20:23],v[100:103],a[108:111]\n"
"v_exp_f32 v114,v114\n"
MF "a[104:107],v[20:23],v[96:99],a[104:107]\n"
"v_exp_f32 v115,v115\n"
MF "a[100:103],v[20:23],v[92:95],a[100:103]\n"
"s_waitcnt lgkmcnt(4)\n"
TR "v[20:21],v3 offset:26624\n"
TR "v[22:23],v3 offset:32768\n"
MF "a[120:123],v[24:27],v[92:95],a[120:123]\n"
"v_add_f32 v52,v52,v112\n"
"v_add_f32 v52,v52,v113\n"
MF "a[124:127],v[24:27],v[96:99],a[124:127]\n"
CV "v74,v112,v113\n"
"v_add_f32 v52,v52,v114\n"
MF "a[128:131],v[24:27],v[100:103],a[128:131]\n"
"v_add_f32 v52,v52,v115\n"
CV "v75,v114,v115\n"
MF "a[132:135],v[24:27],v[104:107],a[132:135]\n"
"v_exp_f32 v116,v116\n"
MF "a[136:139],v[24:27],v[108:111],a[136:139]\n"
"s_waitcnt lgkmcnt(4)\n"
TR "v[24:25],v4 offset:26624\n"
TR "v[26:27],v4 offset:32768\n"
MF "a[156:159],v[28:31],v[108:111],a[156:159]\n"
"v_exp_f32 v117,v117\n"
MF "a[152:155],v[28:31],v[104:107],a[152:155]\n"
"v_exp_f32 v118,v118\n"
MF "a[148:151],v[28:31],v[100:103],a[148:151]\n"
"v_exp_f32 v119,v119\n"
MF "a[144:147],v[28:31],v[96:99],a[144:147]\n"
"v_add_f32 v53,v53,v116\n"
"v_add_f32 v53,v53,v117\n"
MF "a[140:143],v[28:31],v[92:95],a[140:143]\n"
"s_waitcnt lgkmcnt(4)\n"
TR "v[28:29],v5 offset:26624\n"
TR "v[30:31],v5 offset:32768\n"
MF "a[160:163],v[16:19],v[92:95],a[160:163]\n"
CV "v78,v116,v117\n"
"v_add_f32 v53,v53,v118\n"
MF "a[164:167],v[16:19],v[96:99],a[164:167]\n"
"v_add_f32 v53,v53,v119\n"
CV "v79,v118,v119\n"
MF "a[168:171],v[16:19],v[100:103],a[168:171]\n"
"v_exp_f32 v120,v120\n"
MF "a[172:175],v[16:19],v[104:107],a[172:175]\n"
"v_exp_f32 v121,v121\n"
MF "a[176:179],v[16:19],v[108:111],a[176:179]\n"
"s_waitcnt lgkmcnt(4)\n"
"v_exp_f32 v122,v122\n"
MF "a[196:199],v[20:23],v[108:111],a[196:199]\n"
"v_exp_f32 v123,v123\n"
MF "a[192:195],v[20:23],v[104:107],a[192:195]\n"
"v_add_f32 v54,v54,v120\n"
"v_add_f32 v54,v54,v121\n"
MF "a[188:191],v[20:23],v[100:103],a[188:191]\n"
CV "v82,v120,v121\n"
"v_add_f32 v54,v54,v122\n"
MF "a[184:187],v[20:23],v[96:99],a[184:187]\n"
"s_waitcnt vmcnt(6) lgkmcnt(0)\n"
"s_barrier\n"
MF "a[180:183],v[20:23],v[92:95],a[180:183]\n"
"s_add_u32 s72,s72,6144\n"
"ds_read_b128 v[16:19],v0 offset:49152\n"
"ds_read_b128 v[20:23],v1 offset:49152\n"
MF "a[200:203],v[24:27],v[92:95],a[200:203]\n"
"s_cmp_eq_u32 s72,s17\n"
"s_cselect_b32 s72,s16,s72\n"
"s_add_u32 m0,s64,24576\n"
MF "a[204:207],v[24:27],v[96:99],a[204:207]\n"
LD "v7,s[32:35],s72 offen nt lds\n"
MF "a[208:211],v[24:27],v[100:103],a[208:211]\n"
"s_add_u32 m0,s64,25472\n"
"v_add_f32 v54,v54,v123\n"
CV "v83,v122,v123\n"
MF "a[212:215],v[24:27],v[104:107],a[212:215]\n"
LD "v7,s[32:35],s72 offen offset:128 nt lds\n"
MF "a[216:219],v[24:27],v[108:111],a[216:219]\n"
"s_add_u32 m0,s64,26368\n"
"ds_read_b128 v[24:27],v0 offset:50176\n"
MF "a[236:239],v[28:31],v[108:111],a[236:239]\n"
LD "v7,s[32:35],s72 offen offset:256 nt lds\n"
MF "a[232:235],v[28:31],v[104:107],a[232:235]\n"
MF "a[228:231],v[28:31],v[100:103],a[228:231]\n"
MF "a[224:227],v[28:31],v[96:99],a[224:227]\n"
MF "a[220:223],v[28:31],v[92:95],a[220:223]\n"
"s_waitcnt lgkmcnt(1)\n"
MF "v[92:95],v[16:19],v[136:139],v[32:35]\n"
"s_add_u32 s72,s72,6144\n"
"s_add_u32 m0,s64,30720\n"
"ds_read_b128 v[28:31],v1 offset:50176\n"
MF "v[92:95],v[20:23],v[156:159],v[92:95]\n"
LD "v7,s[32:35],s72 offen nt lds\n"
MF "v[96:99],v[20:23],v[160:163],v[36:39]\n"
"v_exp_f32 v124,v124\n"
MF "v[96:99],v[16:19],v[140:143],v[96:99]\n"
"v_exp_f32 v125,v125\n"
MF "v[100:103],v[16:19],v[144:147],v[40:43]\n"
"s_add_u32 m0,s64,31616\n"
"v_exp_f32 v126,v126\n"
MF "v[100:103],v[20:23],v[164:167],v[100:103]\n"
LD "v7,s[32:35],s72 offen offset:128 nt lds\n"
MF "v[104:107],v[20:23],v[168:171],v[44:47]\n"
"v_exp_f32 v127,v127\n"
MF "v[104:107],v[16:19],v[148:151],v[104:107]\n"
"s_add_u32 m0,s64,32512\n"
"v_add_f32 v55,v55,v124\n"
"v_add_f32 v55,v55,v125\n"
MF "v[108:111],v[16:19],v[152:155],v[48:51]\n"
LD "v7,s[32:35],s72 offen offset:256 nt lds\n"
MF "v[108:111],v[20:23],v[172:175],v[108:111]\n"
"s_waitcnt lgkmcnt(0)\n"
"ds_read_b128 v[16:19],v0 offset:51200\n"
"ds_read_b128 v[20:23],v1 offset:51200\n"
MF "v[108:111],v[24:27],v[192:195],v[108:111]\n"
CV "v86,v124,v125\n"
"v_add_f32 v55,v55,v126\n"
MF "v[108:111],v[28:31],v[212:215],v[108:111]\n"
"v_add_f32 v55,v55,v127\n"
CV "v87,v126,v127\n"
MF "v[104:107],v[28:31],v[208:211],v[104:107]\n"
"v_exp_f32 v128,v128\n"
MF "v[104:107],v[24:27],v[188:191],v[104:107]\n"
"v_exp_f32 v129,v129\n"
MF "v[100:103],v[24:27],v[184:187],v[100:103]\n"
"v_exp_f32 v130,v130\n"
MF "v[100:103],v[28:31],v[204:207],v[100:103]\n"
"v_exp_f32 v131,v131\n"
MF "v[96:99],v[28:31],v[200:203],v[96:99]\n"
"v_add_f32 v56,v56,v128\n"
"v_add_f32 v56,v56,v129\n"
MF "v[96:99],v[24:27],v[180:183],v[96:99]\n"
CV "v90,v128,v129\n"
"v_add_f32 v56,v56,v130\n"
MF "v[92:95],v[24:27],v[176:179],v[92:95]\n"
"v_add_f32 v56,v56,v131\n"
CV "v91,v130,v131\n"
MF "v[92:95],v[28:31],v[196:199],v[92:95]\n"
"s_waitcnt lgkmcnt(0)\n"
"ds_read_b128 v[24:27],v0 offset:55296\n"
"ds_read_b128 v[28:31],v1 offset:55296\n"
MF "v[92:95],v[16:19],v[216:219],v[92:95]\n"
MF "v[92:95],v[20:23],v[236:239],v[92:95]\n"
MF "v[96:99],v[20:23],v[240:243],v[96:99]\n"
MF "v[96:99],v[16:19],v[220:223],v[96:99]\n"
MF "v[100:103],v[16:19],v[224:227],v[100:103]\n"
MF "v[100:103],v[20:23],v[244:247],v[100:103]\n"
MF "v[104:107],v[20:23],v[248:251],v[104:107]\n"
MF "v[104:107],v[16:19],v[228:231],v[104:107]\n"
"v_max3_f32 v10,v52,v53,v54\n"
"v_max3_f32 v10,v10,v55,v56\n"
MF "v[108:111],v[16:19],v[232:235],v[108:111]\n"
"v_cmp_ngt_f32 vcc,s77,v10\n"
MF "v[108:111],v[20:23],v[252:255],v[108:111]\n"
"s_waitcnt lgkmcnt(0)\n"
"s_cbranch_vccnz .Lstub_16\n"
".Lback_16:\n"
MF "v[112:115],v[24:27],v[136:139],v[32:35]\n"
"s_add_u32 s74,s74,12288\n"
"ds_read_b128 v[16:19],v0 offset:56320\n"
"ds_read_b128 v[20:23],v1 offset:56320\n"
MF "v[112:115],v[28:31],v[156:159],v[112:115]\n"
"s_cmp_eq_u32 s74,s20\n"
"s_cselect_b32 s74,s70,s74\n"
MF "v[116:119],v[28:31],v[160:163],v[36:39]\n"
"v_exp_f32 v92,v92\n"
MF "v[116:119],v[24:27],v[140:143],v[116:119]\n"
"v_exp_f32 v93,v93\n"
MF "v[120:123],v[24:27],v[144:147],v[40:43]\n"
"v_exp_f32 v94,v94\n"
MF "v[120:123],v[28:31],v[164:167],v[120:123]\n"
"v_exp_f32 v95,v95\n"
MF "v[124:127],v[28:31],v[168:171],v[44:47]\n"
"v_add_f32 v57,v52,v92\n"
"v_add_f32 v57,v57,v93\n"
MF "v[124:127],v[24:27],v[148:151],v[124:127]\n"
CV "v92,v92,v93\n"
"v_add_f32 v57,v57,v94\n"
MF "v[128:131],v[24:27],v[152:155],v[48:51]\n"
"v_add_f32 v57,v57,v95\n"
CV "v93,v94,v95\n"
MF "v[128:131],v[28:31],v[172:175],v[128:131]\n"
"s_waitcnt lgkmcnt(0)\n"
"ds_read_b128 v[24:27],v0 offset:57344\n"
"ds_read_b128 v[28:31],v1 offset:57344\n"
MF "v[128:131],v[16:19],v[192:195],v[128:131]\n"
"s_sub_u32 s61,s61,1\n"
"v_exp_f32 v96,v96\n"
MF "v[128:131],v[20:23],v[212:215],v[128:131]\n"
"s_cmp_eq_u32 s61,1\n"
"s_cselect_b32 s95,0,s95\n"
"v_add_f32 v58,v53,v96\n"
MF "v[124:127],v[20:23],v[208:211],v[124:127]\n"
"v_exp_f32 v97,v97\n"
MF "v[124:127],v[16:19],v[188:191],v[124:127]\n"
"v_exp_f32 v98,v98\n"
MF "v[120:123],v[16:19],v[184:187],v[120:123]\n"
"v_exp_f32 v99,v99\n"
MF "v[120:123],v[20:23],v[204:207],v[120:123]\n"
"v_add_f32 v58,v58,v97\n"
CV "v96,v96,v97\n"
MF "v[116:119],v[20:23],v[200:203],v[116:119]\n"
"v_add_f32 v58,v58,v98\n"
"v_add_f32 v58,v58,v99\n"
MF "v[116:119],v[16:19],v[180:183],v[116:119]\n"
"s_waitcnt vmcnt(6) lgkmcnt(0)\n"
"s_barrier\n"
MF "v[112:115],v[16:19],v[176:179],v[112:115]\n"
CV "v97,v98,v99\n"
MF "v[112:115],v[20:23],v[196:199],v[112:115]\n"
"s_nop 0\n"
TR "v[16:17],v2\n"
TR "v[18:19],v2 offset:6144\n"
MF "v[112:115],v[24:27],v[216:219],v[112:115]\n"
"s_add_u32 s73,s73,12288\n"
TR "v[20:21],v3\n"
TR "v[22:23],v3 offset:6144\n"
MF "v[112:115],v[28:31],v[236:239],v[112:115]\n"
"s_cmp_eq_u32 s73,s19\n"
"s_cselect_b32 s73,s18,s73\n"
"s_add_u32 m0,s65,49152\n"
MF "v[116:119],v[28:31],v[240:243],v[116:119]\n"
LD "v8,s[36:39],s73 offen nt lds\n"
MF "v[116:119],v[24:27],v[220:223],v[116:119]\n"
"s_add_u32 m0,s65,50048\n"
"v_exp_f32 v100,v100\n"
MF "v[120:123],v[24:27],v[224:227],v[120:123]\n"
LD "v8,s[36:39],s73 offen offset:128 nt lds\n"
MF "v[120:123],v[28:31],v[244:247],v[120:123]\n"
"v_exp_f32 v101,v101\n"
MF "v[124:127],v[28:31],v[248:251],v[124:127]\n"
"s_add_u32 m0,s65,50944\n"
"v_exp_f32 v102,v102\n"
MF "v[124:127],v[24:27],v[228:231],v[124:127]\n"
LD "v8,s[36:39],s73 offen offset:256 nt lds\n"
MF "v[128:131],v[24:27],v[232:235],v[128:131]\n"
"v_exp_f32 v103,v103\n"
MF "v[128:131],v[28:31],v[252:255],v[128:131]\n"
"s_waitcnt lgkmcnt(2)\n"
TR "v[24:25],v4\n"
TR "v[26:27],v4 offset:6144\n"
MF "a[0:3],v[16:19],v[72:75],a[0:3]\n"
"s_add_u32 m0,s65,49152\n"
TR "v[28:29],v5\n"
TR "v[30:31],v5 offset:6144\n"
MF "a[4:7],v[16:19],v[76:79],a[4:7]\n"
LD "v8,s[36:39],s73 offen offset:3072 nt lds\n"
MF "a[8:11],v[16:19],v[80:83],a[8:11]\n"
"s_add_u32 m0,s65,50048\n"
"v_add_f32 v59,v54,v100\n"
"v_add_f32 v59,v59,v101\n"
MF "a[12:15],v[16:19],v[84:87],a[12:15]\n"
LD "v8,s[36:39],s73 offen offset:3200 nt lds\n"
MF "a[16:19],v[16:19],v[88:91],a[16:19]\n"
"s_waitcnt lgkmcnt(4)\n"
TR "v[16:17],v2 offset:1024\n"
TR "v[18:19],v2 offset:7168\n"
MF "a[36:39],v[20:23],v[88:91],a[36:39]\n"
CV "v100,v100,v101\n"
"v_add_f32 v59,v59,v102\n"
MF "a[32:35],v[20:23],v[84:87],a[32:35]\n"
"s_add_u32 m0,s65,50944\n"
"v_add_f32 v59,v59,v103\n"
CV "v101,v102,v103\n"
MF "a[28:31],v[20:23],v[80:83],a[28:31]\n"
LD "v8,s[36:39],s73 offen offset:3328 nt lds\n"
MF "a[24:27],v[20:23],v[76:79],a[24:27]\n"
"v_exp_f32 v104,v104\n"
MF "a[20:23],v[20:23],v[72:75],a[20:23]\n"
"s_waitcnt lgkmcnt(4)\n"
TR "v[20:21],v3 offset:1024\n"
TR "v[22:23],v3 offset:7168\n"
MF "a[40:43],v[24:27],v[72:75],a[40:43]\n"
"v_exp_f32 v105,v105\n"
MF "a[44:47],v[24:27],v[76:79],a[44:47]\n"
"v_exp_f32 v106,v106\n"
MF "a[48:51],v[24:27],v[80:83],a[48:51]\n"
"v_exp_f32 v107,v107\n"
MF "a[52:55],v[24:27],v[84:87],a[52:55]\n"
"v_add_f32 v60,v55,v104\n"
"v_add_f32 v60,v60,v105\n"
MF "a[56:59],v[24:27],v[88:91],a[56:59]\n"
"s_waitcnt lgkmcnt(4)\n"
TR "v[24:25],v4 offset:1024\n"
TR "v[26:27],v4 offset:7168\n"
MF "a[76:79],v[28:31],v[88:91],a[76:79]\n"
CV "v104,v104,v105\n"
"v_add_f32 v60,v60,v106\n"
MF "a[72:75],v[28:31],v[84:87],a[72:75]\n"
"v_add_f32 v60,v60,v107\n"
CV "v105,v106,v107\n"
MF "a[68:71],v[28:31],v[80:83],a[68:71]\n"
"v_exp_f32 v108,v108\n"
MF "a[64:67],v[28:31],v[76:79],a[64:67]\n"
"v_exp_f32 v109,v109\n"
MF "a[60:63],v[28:31],v[72:75],a[60:63]\n"
"s_waitcnt lgkmcnt(4)\n"
TR "v[28:29],v5 offset:1024\n"
TR "v[30:31],v5 offset:7168\n"
MF "a[80:83],v[16:19],v[72:75],a[80:83]\n"
"v_exp_f32 v110,v110\n"
MF "a[84:87],v[16:19],v[76:79],a[84:87]\n"
"v_exp_f32 v111,v111\n"
MF "a[88:91],v[16:19],v[80:83],a[88:91]\n"
"v_add_f32 v61,v56,v108\n"
"v_add_f32 v61,v61,v109\n"
MF "a[92:95],v[16:19],v[84:87],a[92:95]\n"
CV "v108,v108,v109\n"
"v_add_f32 v61,v61,v110\n"
MF "a[96:99],v[16:19],v[88:91],a[96:99]\n"
"s_waitcnt lgkmcnt(4)\n"
TR "v[16:17],v2 offset:2048\n"
TR "v[18:19],v2 offset:8192\n"
MF "a[116:119],v[20:23],v[88:91],a[116:119]\n"
"v_add_f32 v61,v61,v111\n"
CV "v109,v110,v111\n"
MF "a[112:115],v[20:23],v[84:87],a[112:115]\n"
"v_exp_f32 v112,v112\n"
MF "a[108:111],v[20:23],v[80:83],a[108:111]\n"
"v_exp_f32 v113,v113\n"
MF "a[104:107],v[20:23],v[76:79],a[104:107]\n"
"v_exp_f32 v114,v114\n"
MF "a[100:103],v[20:23],v[72:75],a[100:103]\n"
"s_waitcnt lgkmcnt(4)\n"
TR "v[20:21],v3 offset:2048\n"
TR "v[22:23],v3 offset:8192\n"
MF "a[120:123],v[24:27],v[72:75],a[120:123]\n"
"v_exp_f32 v115,v115\n"
MF "a[124:127],v[24:27],v[76:79],a[124:127]\n"
"v_add_f32 v57,v57,v112\n"
"v_add_f32 v57,v57,v113\n"
MF "a[128:131],v[24:27],v[80:83],a[128:131]\n"
CV "v94,v112,v113\n"
"v_add_f32 v57,v57,v114\n"
MF "a[132:135],v[24:27],v[84:87],a[132:135]\n"
"v_add_f32 v57,v57,v115\n"
CV "v95,v114,v115\n"
MF "a[136:139],v[24:27],v[88:91],a[136:139]\n"
"s_waitcnt lgkmcnt(4)\n"
TR "v[24:25],v4 offset:2048\n"
TR "v[26:27],v4 offset:8192\n"
MF "a[156:159],v[28:31],v[88:91],a[156:159]\n"
"v_exp_f32 v116,v116\n"
MF "a[152:155],v[28:31],v[84:87],a[152:155]\n"
"v_exp_f32 v117,v117\n"
MF "a[148:151],v[28:31],v[80:83],a[148:151]\n"
"v_exp_f32 v118,v118\n"
MF "a[144:147],v[28:31],v[76:79],a[144:147]\n"
"v_exp_f32 v119,v119\n"
MF "a[140:143],v[28:31],v[72:75],a[140:143]\n"
"s_waitcnt lgkmcnt(4)\n"
TR "v[28:29],v5 offset:2048\n"
TR "v[30:31],v5 offset:8192\n"
MF "a[160:163],v[16:19],v[72:75],a[160:163]\n"
"v_add_f32 v58,v58,v116\n"
"v_add_f32 v58,v58,v117\n"
MF "a[164:167],v[16:19],v[76:79],a[164:167]\n"
CV "v98,v116,v117\n"
"v_add_f32 v58,v58,v118\n"
MF "a[168:171],v[16:19],v[80:83],a[168:171]\n"
"v_add_f32 v58,v58,v119\n"
CV "v99,v118,v119\n"
MF "a[172:175],v[16:19],v[84:87],a[172:175]\n"
"v_exp_f32 v120,v120\n"
MF "a[176:179],v[16:19],v[88:91],a[176:179]\n"
"s_waitcnt lgkmcnt(4)\n"
"v_exp_f32 v121,v121\n"
MF "a[196:199],v[20:23],v[88:91],a[196:199]\n"
"v_exp_f32 v122,v122\n"
MF "a[192:195],v[20:23],v[84:87],a[192:195]\n"
"v_exp_f32 v123,v123\n"
MF "a[188:191],v[20:23],v[80:83],a[188:191]\n"
"v_add_f32 v59,v59,v120\n"
"v_add_f32 v59,v59,v121\n"
MF "a[184:187],v[20:23],v[76:79],a[184:187]\n"
"s_waitcnt vmcnt(6) lgkmcnt(0)\n"
"s_barrier\n"
MF "a[180:183],v[20:23],v[72:75],a[180:183]\n"
"s_add_u32 s72,s72,6144\n"
"ds_read_b128 v[16:19],v0 offset:24576\n"
"ds_read_b128 v[20:23],v1 offset:24576\n"
MF "a[200:203],v[24:27],v[72:75],a[200:203]\n"
"s_cmp_eq_u32 s72,s17\n"
"s_cselect_b32 s72,s16,s72\n"
"s_add_u32 m0,s64,0\n"
MF "a[204:207],v[24:27],v[76:79],a[204:207]\n"
LD "v7,s[32:35],s72 offen nt lds\n"
MF "a[208:211],v[24:27],v[80:83],a[208:211]\n"
"s_add_u32 m0,s64,896\n"
CV "v102,v120,v121\n"
"v_add_f32 v59,v59,v122\n"
MF "a[212:215],v[24:27],v[84:87],a[212:215]\n"
LD "v7,s[32:35],s72 offen offset:128 nt lds\n"
MF "a[216:219],v[24:27],v[88:91],a[216:219]\n"
"s_add_u32 m0,s64,1792\n"
"ds_read_b128 v[24:27],v0 offset:25600\n"
"v_add_f32 v59,v59,v123\n"
MF "a[236:239],v[28:31],v[88:91],a[236:239]\n"
LD "v7,s[32:35],s72 offen offset:256 nt lds\n"
MF "a[232:235],v[28:31],v[84:87],a[232:235]\n"
CV "v103,v122,v123\n"
MF "a[228:231],v[28:31],v[80:83],a[228:231]\n"
"s_waitcnt lgkmcnt(1)\n"
MF "a[224:227],v[28:31],v[76:79],a[224:227]\n"
"s_cmp_lg_u32 s61,0\n"
MF "a[220:223],v[28:31],v[72:75],a[220:223]\n"
"s_cbranch_scc1 .Laloop\n"
"v_exp_f32 v124,v124\n"
"v_exp_f32 v125,v125\n"
"v_exp_f32 v126,v126\n"
"v_exp_f32 v127,v127\n"
"v_exp_f32 v128,v128\n"
"v_exp_f32 v129,v129\n"
"v_exp_f32 v130,v130\n"
"v_exp_f32 v131,v131\n"
"v_add_f32 v60,v60,v124\n"
"v_add_f32 v61,v61,v128\n"
"v_add_f32 v60,v60,v125\n"
"v_add_f32 v61,v61,v129\n"
"v_add_f32 v60,v60,v126\n"
"v_add_f32 v61,v61,v130\n"
"v_add_f32 v60,v60,v127\n"
"v_add_f32 v61,v61,v131\n"
CV "v106,v124,v125\n"
CV "v107,v126,v127\n"
CV "v110,v128,v129\n"
CV "v111,v130,v131\n"
"v_max3_f32 v10,v57,v58,v59\n"
"v_max3_f32 v10,v10,v60,v61\n"
"v_cmp_le_f32 vcc,0x47800000,v10\n"
"s_cbranch_vccnz .Lstub_17\n"
".Lback_17:\n"
"s_waitcnt vmcnt(3) lgkmcnt(0)\n"
"s_barrier\n"
"s_and_b32 s58,s56,1\n"
"s_mul_i32 s79,s58,12288\n"
"s_lshr_b32 s58,s56,1\n"
"s_mul_i32 s72,s58,61440\n"
"s_mul_i32 s73,s58,6144\n"
"s_add_u32 s73,s73,s79\n"
"v_lshrrev_b32_e64 v192,7,v9\n"
"v_mul_u32_u24 v192,896,v192\n"
"v_add_u32 v192,v192,v8\n"
"v_lshrrev_b32 v242,5,v9\n"
"v_min_u32_e64 v242,23,v242\n"
"v_mul_u32_u24 v242,160,v242\n"
"v_and_or_b32 v242,v9,16,v242\n"
"v_lshlrev_b32 v242,3,v242\n"
"s_bfe_u32 s74,s23,0x70000\n"
"s_lshl_b32 s74,s74,1\n"
"s_add_u32 s74,s74,s56\n"
"s_sub_u32 s74,s74,2\n"
"s_mul_i32 s74,s74,30720\n"
"s_bfe_u32 s68,s23,0x30003\n"
"s_add_u32 s68,s68,7\n"
"s_lshl_b32 s68,s68,7\n"
"s_and_b32 s68,s68,768\n"
"s_cmp_ge_u32 s56,2\n"
"s_cselect_b32 s74,s74,0xf0000000\n"
"s_add_u32 s88,s88,1024\n"
"s_add_u32 s89,s89,1024\n"
"s_lshl_b32 s58,s56,10\n"
"s_add_u32 s58,s58,73728\n"
"v_add_u32_e64 v120,s58,v9\n"
"v_add_u32 v121,65536,v120\n"
TR "v[136:137],v2 offset:49152\n"
TR "v[138:139],v2 offset:55296\n"
TR "v[140:141],v3 offset:49152\n"
TR "v[142:143],v3 offset:55296\n"
TR "v[144:145],v4 offset:49152\n"
TR "v[146:147],v4 offset:55296\n"
TR "v[148:149],v5 offset:49152\n"
TR "v[150:151],v5 offset:55296\n"
TR "v[152:153],v2 offset:50176\n"
TR "v[154:155],v2 offset:56320\n"
TR "v[156:157],v3 offset:50176\n"
TR "v[158:159],v3 offset:56320\n"
TR "v[160:161],v4 offset:50176\n"
TR "v[162:163],v4 offset:56320\n"
TR "v[164:165],v5 offset:50176\n"
TR "v[166:167],v5 offset:56320\n"
TR "v[168:169],v2 offset:51200\n"
TR "v[170:171],v2 offset:57344\n"
TR "v[172:173],v3 offset:51200\n"
TR "v[174:175],v3 offset:57344\n"
TR "v[176:177],v4 offset:51200\n"
TR "v[178:179],v4 offset:57344\n"
TR "v[180:181],v5 offset:51200\n"
TR "v[182:183],v5 offset:57344\n"
"v_mov_b32 v52,v57\n"
"v_mov_b32 v53,v58\n"
"v_mov_b32 v54,v59\n"
"v_mov_b32 v55,v60\n"
"v_mov_b32 v56,v61\n"
"s_add_u32 m0,s73,24576\n"
"s_add_u32 s80,s89,s72\n"
LD "v192,s[48:51],s80 offen lds\n"
"v_add_u32 v193,9216,v192\n"
LD "v193,s[48:51],s80 offen offset:1024 lds\n"
"v_permlane16_swap_b32 v57,v52\n"
"v_permlane16_swap_b32 v58,v53\n"
"v_permlane16_swap_b32 v59,v54\n"
"v_permlane16_swap_b32 v60,v55\n"
"v_permlane16_swap_b32 v61,v56\n"
"v_add_f32 v57,v57,v52\n"
"v_add_f32 v58,v58,v53\n"
"v_add_f32 v59,v59,v54\n"
"v_add_f32 v60,v60,v55\n"
"v_add_f32 v61,v61,v56\n"
"v_mov_b32 v52,v57\n"
"v_mov_b32 v53,v58\n"
"v_mov_b32 v54,v59\n"
"v_mov_b32 v55,v60\n"
"v_mov_b32_e64 v56,v61\n"
"v_add_u32 v194,18432,v192\n"
LD "v194,s[48:51],s80 offen offset:2048 lds\n"
"v_add_u32 v195,27648,v192\n"
LD "v195,s[48:51],s80 offen offset:3072 lds\n"
"v_permlane32_swap_b32 v57,v52\n"
"v_permlane32_swap_b32 v58,v53\n"
"v_permlane32_swap_b32 v59,v54\n"
"v_permlane32_swap_b32 v60,v55\n"
"v_permlane32_swap_b32 v61,v56\n"
"v_add_f32 v57,v57,v52\n"
"v_add_f32 v58,v58,v53\n"
"v_add_f32 v59,v59,v54\n"
"v_add_f32 v60,v60,v55\n"
"v_add_f32 v61,v61,v56\n"
"s_waitcnt lgkmcnt(15)\n"
MF "a[0:3],v[136:139],v[92:95],a[0:3]\n"
MF "a[20:23],v[140:143],v[92:95],a[20:23]\n"
"v_rcp_f32 v57,v57\n"
"v_rcp_f32 v58,v58\n"
"v_rcp_f32 v59,v59\n"
"v_rcp_f32 v60,v60\n"
"v_rcp_f32 v61,v61\n"
MF "a[4:7],v[136:139],v[96:99],a[4:7]\n"
"v_mul_f32_e64 v52,-s13,v57\n"
"v_mul_f32_e64 v53,-s13,v58\n"
"v_mul_f32_e64 v54,-s13,v59\n"
"v_mul_f32_e64 v55,-s13,v60\n"
"v_mul_f32_e64 v56,-s13,v61\n"
"s_mov_b32 s13,s11\n"
AR "v72,a0\n"
AR "v73,a1\n"
AR "v74,a2\n"
MF "a[24:27],v[140:143],v[96:99],a[24:27]\n"
AR "v75,a3\n"
AR "v76,a20\n"
AR "v77,a21\n"
"s_nop 0\n"
AR "v78,a22\n"
AR "v79,a23\n"
"v_pk_fma_f32 v[72:73],v[72:73],v[56:57],v[52:53] op_sel:[0,1,0] op_sel_hi:[1,1,0]\n"
"v_pk_fma_f32 v[74:75],v[74:75],v[56:57],v[52:53] op_sel:[0,1,0] op_sel_hi:[1,1,0]\n"
"v_pk_fma_f32 v[76:77],v[76:77],v[56:57],v[52:53] op_sel:[0,1,0] op_sel_hi:[1,1,0]\n"
"v_pk_fma_f32 v[78:79],v[78:79],v[56:57],v[52:53] op_sel:[0,1,0] op_sel_hi:[1,1,0]\n"
CV "v112,v72,v73\n"
CV "v113,v74,v75\n"
CV "v114,v76,v77\n"
CV "v115,v78,v79\n"
MF "a[8:11],v[136:139],v[100:103],a[8:11]\n"
"ds_read_b128 a[20:23],v120 offset:24576\n"
AW "a0,v112\n"
AW "a1,v113\n"
AW "a2,v114\n"
AW "a3,v115\n"
"s_add_u32 m0,s73,28672\n"
"v_add_u32 v196,40960,v192\n"
LD "v196,s[48:51],s80 offen lds\n"
AR "v80,a4\n"
AR "v81,a5\n"
AR "v82,a6\n"
MF "a[28:31],v[140:143],v[100:103],a[28:31]\n"
AR "v83,a7\n"
AR "v84,a24\n"
AR "v85,a25\n"
AR "v86,a26\n"
AR "v87,a27\n"
"v_pk_fma_f32 v[80:81],v[80:81],v[58:59],v[52:53] op_sel:[0,0,1] op_sel_hi:[1,0,1]\n"
"v_pk_fma_f32 v[82:83],v[82:83],v[58:59],v[52:53] op_sel:[0,0,1] op_sel_hi:[1,0,1]\n"
"v_pk_fma_f32 v[84:85],v[84:85],v[58:59],v[52:53] op_sel:[0,0,1] op_sel_hi:[1,0,1]\n"
"v_pk_fma_f32 v[86:87],v[86:87],v[58:59],v[52:53] op_sel:[0,0,1] op_sel_hi:[1,0,1]\n"
CV "v116,v80,v81\n"
CV "v117,v82,v83\n"
CV "v118,v84,v85\n"
CV "v119,v86,v87\n"
MF "a[12:15],v[136:139],v[104:107],a[12:15]\n"
"ds_read_b128 a[24:27],v120 offset:28672\n"
AW "a4,v116\n"
AW "a5,v117\n"
AW "a6,v118\n"
AW "a7,v119\n"
"v_add_u32 v197,50176,v192\n"
LD "v197,s[48:51],s80 offen offset:1024 lds\n"
AR "v72,a8\n"
AR "v73,a9\n"
AR "v74,a10\n"
MF "a[32:35],v[140:143],v[104:107],a[32:35]\n"
AR "v75,a11\n"
AR "v76,a28\n"
AR "v77,a29\n"
AR "v78,a30\n"
AR "v79,a31\n"
"v_pk_fma_f32 v[72:73],v[72:73],v[58:59],v[54:55] op_sel:[0,1,0] op_sel_hi:[1,1,0]\n"
"v_pk_fma_f32 v[74:75],v[74:75],v[58:59],v[54:55] op_sel:[0,1,0] op_sel_hi:[1,1,0]\n"
"v_pk_fma_f32 v[76:77],v[76:77],v[58:59],v[54:55] op_sel:[0,1,0] op_sel_hi:[1,1,0]\n"
"v_pk_fma_f32 v[78:79],v[78:79],v[58:59],v[54:55] op_sel:[0,1,0] op_sel_hi:[1,1,0]\n"
CV "v112,v72,v73\n"
CV "v113,v74,v75\n"
CV "v114,v76,v77\n"
CV "v115,v78,v79\n"
MF "a[16:19],v[136:139],v[108:111],a[16:19]\n"
"ds_read_b128 a[28:31],v120 offset:32768\n"
AW "a8,v112\n"
AW "a9,v113\n"
AW "a10,v114\n"
AW "a11,v115\n"
"s_waitcnt lgkmcnt(0)\n"
"s_barrier\n"
AR "v80,a12\n"
AR "v81,a13\n"
AR "v82,a14\n"
MF "a[36:39],v[140:143],v[108:111],a[36:39]\n"
AR "v83,a15\n"
AR "v84,a32\n"
AR "v85,a33\n"
AR "v86,a34\n"
AR "v87,a35\n"
"v_pk_fma_f32 v[80:81],v[80:81],v[60:61],v[54:55] op_sel:[0,0,1] op_sel_hi:[1,0,1]\n"
"v_pk_fma_f32 v[82:83],v[82:83],v[60:61],v[54:55] op_sel:[0,0,1] op_sel_hi:[1,0,1]\n"
"v_pk_fma_f32 v[84:85],v[84:85],v[60:61],v[54:55] op_sel:[0,0,1] op_sel_hi:[1,0,1]\n"
"v_pk_fma_f32 v[86:87],v[86:87],v[60:61],v[54:55] op_sel:[0,0,1] op_sel_hi:[1,0,1]\n"
CV "v116,v80,v81\n"
CV "v117,v82,v83\n"
CV "v118,v84,v85\n"
CV "v119,v86,v87\n"
MF "a[40:43],v[144:147],v[92:95],a[40:43]\n"
"ds_read_b128 a[32:35],v120 offset:36864\n"
AW "a12,v116\n"
AW "a13,v117\n"
AW "a14,v118\n"
AW "a15,v119\n"
"s_add_u32 m0,s73,0\n"
"s_nop 0\n"
"s_add_u32 s80,s80,122880\n"
LD "v192,s[48:51],s80 offen lds\n"
AR "v72,a16\n"
AR "v73,a17\n"
AR "v74,a18\n"
MF "a[60:63],v[148:151],v[92:95],a[60:63]\n"
AR "v75,a19\n"
AR "v76,a36\n"
AR "v77,a37\n"
AR "v78,a38\n"
AR "v79,a39\n"
"v_pk_fma_f32 v[72:73],v[72:73],v[60:61],v[56:57] op_sel:[0,1,0] op_sel_hi:[1,1,0]\n"
"v_pk_fma_f32 v[74:75],v[74:75],v[60:61],v[56:57] op_sel:[0,1,0] op_sel_hi:[1,1,0]\n"
"v_pk_fma_f32 v[76:77],v[76:77],v[60:61],v[56:57] op_sel:[0,1,0] op_sel_hi:[1,1,0]\n"
"v_pk_fma_f32 v[78:79],v[78:79],v[60:61],v[56:57] op_sel:[0,1,0] op_sel_hi:[1,1,0]\n"
CV "v112,v72,v73\n"
CV "v113,v74,v75\n"
CV "v114,v76,v77\n"
CV "v115,v78,v79\n"
MF "a[44:47],v[144:147],v[96:99],a[44:47]\n"
"ds_read_b128 a[36:39],v120 offset:40960\n"
AW "a16,v112\n"
AW "a17,v113\n"
AW "a18,v114\n"
AW "a19,v115\n"
LD "v193,s[48:51],s80 offen offset:1024 lds\n"
AR "v80,a40\n"
AR "v81,a41\n"
AR "v82,a42\n"
MF "a[64:67],v[148:151],v[96:99],a[64:67]\n"
AR "v83,a43\n"
AR "v84,a60\n"
AR "v85,a61\n"
AR "v86,a62\n"
AR "v87,a63\n"
"v_pk_fma_f32 v[80:81],v[80:81],v[56:57],v[52:53] op_sel:[0,1,0] op_sel_hi:[1,1,0]\n"
"v_pk_fma_f32 v[82:83],v[82:83],v[56:57],v[52:53] op_sel:[0,1,0] op_sel_hi:[1,1,0]\n"
"v_pk_fma_f32 v[84:85],v[84:85],v[56:57],v[52:53] op_sel:[0,1,0] op_sel_hi:[1,1,0]\n"
"v_pk_fma_f32 v[86:87],v[86:87],v[56:57],v[52:53] op_sel:[0,1,0] op_sel_hi:[1,1,0]\n"
CV "v116,v80,v81\n"
CV "v117,v82,v83\n"
CV "v118,v84,v85\n"
CV "v119,v86,v87\n"
MF "a[48:51],v[144:147],v[100:103],a[48:51]\n"
"ds_read_b128 a[60:63],v120 offset:45056\n"
AW "a40,v116\n"
AW "a41,v117\n"
AW "a42,v118\n"
AW "a43,v119\n"
AR "v72,a44\n"
AR "v73,a45\n"
AR "v74,a46\n"
MF "a[68:71],v[148:151],v[100:103],a[68:71]\n"
AR "v75,a47\n"
AR "v76,a64\n"
AR "v77,a65\n"
AR "v78,a66\n"
AR "v79,a67\n"
"v_pk_fma_f32 v[72:73],v[72:73],v[58:59],v[52:53] op_sel:[0,0,1] op_sel_hi:[1,0,1]\n"
"v_pk_fma_f32 v[74:75],v[74:75],v[58:59],v[52:53] op_sel:[0,0,1] op_sel_hi:[1,0,1]\n"
"v_pk_fma_f32 v[76:77],v[76:77],v[58:59],v[52:53] op_sel:[0,0,1] op_sel_hi:[1,0,1]\n"
"v_pk_fma_f32 v[78:79],v[78:79],v[58:59],v[52:53] op_sel:[0,0,1] op_sel_hi:[1,0,1]\n"
CV "v112,v72,v73\n"
CV "v113,v74,v75\n"
CV "v114,v76,v77\n"
CV "v115,v78,v79\n"
MF "a[52:55],v[144:147],v[104:107],a[52:55]\n"
"ds_read_b128 a[64:67],v120 offset:49152\n"
AW "a44,v112\n"
AW "a45,v113\n"
AW "a46,v114\n"
AW "a47,v115\n"
LD "v194,s[48:51],s80 offen offset:2048 lds\n"
AR "v80,a48\n"
AR "v81,a49\n"
AR "v82,a50\n"
MF "a[72:75],v[148:151],v[104:107],a[72:75]\n"
AR "v83,a51\n"
AR "v84,a68\n"
AR "v85,a69\n"
AR "v86,a70\n"
AR "v87,a71\n"
"v_pk_fma_f32 v[80:81],v[80:81],v[58:59],v[54:55] op_sel:[0,1,0] op_sel_hi:[1,1,0]\n"
"v_pk_fma_f32 v[82:83],v[82:83],v[58:59],v[54:55] op_sel:[0,1,0] op_sel_hi:[1,1,0]\n"
"v_pk_fma_f32 v[84:85],v[84:85],v[58:59],v[54:55] op_sel:[0,1,0] op_sel_hi:[1,1,0]\n"
"v_pk_fma_f32 v[86:87],v[86:87],v[58:59],v[54:55] op_sel:[0,1,0] op_sel_hi:[1,1,0]\n"
CV "v116,v80,v81\n"
CV "v117,v82,v83\n"
CV "v118,v84,v85\n"
CV "v119,v86,v87\n"
MF "a[56:59],v[144:147],v[108:111],a[56:59]\n"
"ds_read_b128 a[68:71],v120 offset:53248\n"
AW "a48,v116\n"
AW "a49,v117\n"
AW "a50,v118\n"
AW "a51,v119\n"
LD "v195,s[48:51],s80 offen offset:3072 lds\n"
AR "v72,a52\n"
AR "v73,a53\n"
AR "v74,a54\n"
MF "a[76:79],v[148:151],v[108:111],a[76:79]\n"
AR "v75,a55\n"
AR "v76,a72\n"
AR "v77,a73\n"
AR "v78,a74\n"
AR "v79,a75\n"
"v_pk_fma_f32 v[72:73],v[72:73],v[60:61],v[54:55] op_sel:[0,0,1] op_sel_hi:[1,0,1]\n"
"v_pk_fma_f32 v[74:75],v[74:75],v[60:61],v[54:55] op_sel:[0,0,1] op_sel_hi:[1,0,1]\n"
"v_pk_fma_f32 v[76:77],v[76:77],v[60:61],v[54:55] op_sel:[0,0,1] op_sel_hi:[1,0,1]\n"
"v_pk_fma_f32 v[78:79],v[78:79],v[60:61],v[54:55] op_sel:[0,0,1] op_sel_hi:[1,0,1]\n"
CV "v112,v72,v73\n"
CV "v113,v74,v75\n"
CV "v114,v76,v77\n"
CV "v115,v78,v79\n"
MF "a[80:83],v[152:155],v[92:95],a[80:83]\n"
"ds_read_b128 a[72:75],v120 offset:57344\n"
AW "a52,v112\n"
AW "a53,v113\n"
AW "a54,v114\n"
AW "a55,v115\n"
"s_add_u32 m0,s73,4096\n"
"v_add_u32 v198,59392,v192\n"
LD "v196,s[48:51],s80 offen lds\n"
AR "v80,a56\n"
AR "v81,a57\n"
AR "v82,a58\n"
MF "a[100:103],v[156:159],v[92:95],a[100:103]\n"
AR "v83,a59\n"
AR "v84,a76\n"
AR "v85,a77\n"
AR "v86,a78\n"
AR "v87,a79\n"
"v_pk_fma_f32 v[80:81],v[80:81],v[60:61],v[56:57] op_sel:[0,1,0] op_sel_hi:[1,1,0]\n"
"v_pk_fma_f32 v[82:83],v[82:83],v[60:61],v[56:57] op_sel:[0,1,0] op_sel_hi:[1,1,0]\n"
"v_pk_fma_f32 v[84:85],v[84:85],v[60:61],v[56:57] op_sel:[0,1,0] op_sel_hi:[1,1,0]\n"
"v_pk_fma_f32 v[86:87],v[86:87],v[60:61],v[56:57] op_sel:[0,1,0] op_sel_hi:[1,1,0]\n"
CV "v116,v80,v81\n"
CV "v117,v82,v83\n"
CV "v118,v84,v85\n"
CV "v119,v86,v87\n"
MF "a[84:87],v[152:155],v[96:99],a[84:87]\n"
"ds_read_b128 a[76:79],v120 offset:61440\n"
AW "a56,v116\n"
AW "a57,v117\n"
AW "a58,v118\n"
AW "a59,v119\n"
AR "v72,a80\n"
AR "v73,a81\n"
AR "v74,a82\n"
MF "a[104:107],v[156:159],v[96:99],a[104:107]\n"
AR "v75,a83\n"
AR "v76,a100\n"
AR "v77,a101\n"
AR "v78,a102\n"
AR "v79,a103\n"
"v_pk_fma_f32 v[72:73],v[72:73],v[56:57],v[52:53] op_sel:[0,1,0] op_sel_hi:[1,1,0]\n"
"v_pk_fma_f32 v[74:75],v[74:75],v[56:57],v[52:53] op_sel:[0,1,0] op_sel_hi:[1,1,0]\n"
"v_pk_fma_f32 v[76:77],v[76:77],v[56:57],v[52:53] op_sel:[0,1,0] op_sel_hi:[1,1,0]\n"
"v_pk_fma_f32 v[78:79],v[78:79],v[56:57],v[52:53] op_sel:[0,1,0] op_sel_hi:[1,1,0]\n"
CV "v112,v72,v73\n"
CV "v113,v74,v75\n"
CV "v114,v76,v77\n"
CV "v115,v78,v79\n"
MF "a[88:91],v[152:155],v[100:103],a[88:91]\n"
"ds_read_b128 a[100:103],v121\n"
AW "a80,v112\n"
AW "a81,v113\n"
AW "a82,v114\n"
AW "a83,v115\n"
LD "v197,s[48:51],s80 offen offset:1024 lds\n"
AR "v80,a84\n"
AR "v81,a85\n"
AR "v82,a86\n"
MF "a[108:111],v[156:159],v[100:103],a[108:111]\n"
AR "v83,a87\n"
AR "v84,a104\n"
AR "v85,a105\n"
AR "v86,a106\n"
AR "v87,a107\n"
"v_pk_fma_f32 v[80:81],v[80:81],v[58:59],v[52:53] op_sel:[0,0,1] op_sel_hi:[1,0,1]\n"
"v_pk_fma_f32 v[82:83],v[82:83],v[58:59],v[52:53] op_sel:[0,0,1] op_sel_hi:[1,0,1]\n"
"v_pk_fma_f32 v[84:85],v[84:85],v[58:59],v[52:53] op_sel:[0,0,1] op_sel_hi:[1,0,1]\n"
"v_pk_fma_f32 v[86:87],v[86:87],v[58:59],v[52:53] op_sel:[0,0,1] op_sel_hi:[1,0,1]\n"
CV "v116,v80,v81\n"
CV "v117,v82,v83\n"
CV "v118,v84,v85\n"
CV "v119,v86,v87\n"
MF "a[92:95],v[152:155],v[104:107],a[92:95]\n"
"ds_read_b128 a[104:107],v121 offset:4096\n"
AW "a84,v116\n"
AW "a85,v117\n"
AW "a86,v118\n"
AW "a87,v119\n"
"s_add_u32 m0,s73,49152\n"
"s_add_u32 s80,s88,s72\n"
"s_nop 0\n"
LD "v192,s[48:51],s80 offen lds\n"
AR "v72,a88\n"
AR "v73,a89\n"
AR "v74,a90\n"
MF "a[112:115],v[156:159],v[104:107],a[112:115]\n"
AR "v75,a91\n"
AR "v76,a108\n"
AR "v77,a109\n"
AR "v78,a110\n"
AR "v79,a111\n"
"v_pk_fma_f32 v[72:73],v[72:73],v[58:59],v[54:55] op_sel:[0,1,0] op_sel_hi:[1,1,0]\n"
"v_pk_fma_f32 v[74:75],v[74:75],v[58:59],v[54:55] op_sel:[0,1,0] op_sel_hi:[1,1,0]\n"
"v_pk_fma_f32 v[76:77],v[76:77],v[58:59],v[54:55] op_sel:[0,1,0] op_sel_hi:[1,1,0]\n"
"v_pk_fma_f32 v[78:79],v[78:79],v[58:59],v[54:55] op_sel:[0,1,0] op_sel_hi:[1,1,0]\n"
CV "v112,v72,v73\n"
CV "v113,v74,v75\n"
CV "v114,v76,v77\n"
CV "v115,v78,v79\n"
MF "a[96:99],v[152:155],v[108:111],a[96:99]\n"
"ds_read_b128 a[108:111],v121 offset:8192\n"
AW "a88,v112\n"
AW "a89,v113\n"
AW "a90,v114\n"
AW "a91,v115\n"
AR "v80,a92\n"
AR "v81,a93\n"
AR "v82,a94\n"
MF "a[116:119],v[156:159],v[108:111],a[116:119]\n"
AR "v83,a95\n"
AR "v84,a112\n"
AR "v85,a113\n"
AR "v86,a114\n"
AR "v87,a115\n"
"v_pk_fma_f32 v[80:81],v[80:81],v[60:61],v[54:55] op_sel:[0,0,1] op_sel_hi:[1,0,1]\n"
"v_pk_fma_f32 v[82:83],v[82:83],v[60:61],v[54:55] op_sel:[0,0,1] op_sel_hi:[1,0,1]\n"
"v_pk_fma_f32 v[84:85],v[84:85],v[60:61],v[54:55] op_sel:[0,0,1] op_sel_hi:[1,0,1]\n"
"v_pk_fma_f32 v[86:87],v[86:87],v[60:61],v[54:55] op_sel:[0,0,1] op_sel_hi:[1,0,1]\n"
CV "v116,v80,v81\n"
CV "v117,v82,v83\n"
CV "v118,v84,v85\n"
CV "v119,v86,v87\n"
MF "a[120:123],v[160:163],v[92:95],a[120:123]\n"
"ds_read_b128 a[112:115],v121 offset:12288\n"
AW "a92,v116\n"
AW "a93,v117\n"
AW "a94,v118\n"
AW "a95,v119\n"
LD "v193,s[48:51],s80 offen offset:1024 lds\n"
AR "v72,a96\n"
AR "v73,a97\n"
AR "v74,a98\n"
MF "a[140:143],v[164:167],v[92:95],a[140:143]\n"
AR "v75,a99\n"
AR "v76,a116\n"
AR "v77,a117\n"
AR "v78,a118\n"
AR "v79,a119\n"
"v_pk_fma_f32 v[72:73],v[72:73],v[60:61],v[56:57] op_sel:[0,1,0] op_sel_hi:[1,1,0]\n"
"v_pk_fma_f32 v[74:75],v[74:75],v[60:61],v[56:57] op_sel:[0,1,0] op_sel_hi:[1,1,0]\n"
"v_pk_fma_f32 v[76:77],v[76:77],v[60:61],v[56:57] op_sel:[0,1,0] op_sel_hi:[1,1,0]\n"
"v_pk_fma_f32 v[78:79],v[78:79],v[60:61],v[56:57] op_sel:[0,1,0] op_sel_hi:[1,1,0]\n"
CV "v112,v72,v73\n"
CV "v113,v74,v75\n"
CV "v114,v76,v77\n"
CV "v115,v78,v79\n"
MF "a[124:127],v[160:163],v[96:99],a[124:127]\n"
"ds_read_b128 a[116:119],v121 offset:16384\n"
AW "a96,v112\n"
AW "a97,v113\n"
AW "a98,v114\n"
AW "a99,v115\n"
LD "v194,s[48:51],s80 offen offset:2048 lds\n"
AR "v80,a120\n"
AR "v81,a121\n"
AR "v82,a122\n"
MF "a[144:147],v[164:167],v[96:99],a[144:147]\n"
AR "v83,a123\n"
AR "v84,a140\n"
AR "v85,a141\n"
AR "v86,a142\n"
AR "v87,a143\n"
"v_pk_fma_f32 v[80:81],v[80:81],v[56:57],v[52:53] op_sel:[0,1,0] op_sel_hi:[1,1,0]\n"
"v_pk_fma_f32 v[82:83],v[82:83],v[56:57],v[52:53] op_sel:[0,1,0] op_sel_hi:[1,1,0]\n"
"v_pk_fma_f32 v[84:85],v[84:85],v[56:57],v[52:53] op_sel:[0,1,0] op_sel_hi:[1,1,0]\n"
"v_pk_fma_f32 v[86:87],v[86:87],v[56:57],v[52:53] op_sel:[0,1,0] op_sel_hi:[1,1,0]\n"
CV "v116,v80,v81\n"
CV "v117,v82,v83\n"
CV "v118,v84,v85\n"
CV "v119,v86,v87\n"
MF "a[128:131],v[160:163],v[100:103],a[128:131]\n"
"ds_read_b128 a[140:143],v121 offset:20480\n"
AW "a120,v116\n"
AW "a121,v117\n"
AW "a122,v118\n"
AW "a123,v119\n"
AR "v72,a124\n"
AR "v73,a125\n"
AR "v74,a126\n"
MF "a[148:151],v[164:167],v[100:103],a[148:151]\n"
AR "v75,a127\n"
AR "v76,a144\n"
AR "v77,a145\n"
AR "v78,a146\n"
AR "v79,a147\n"
"v_pk_fma_f32 v[72:73],v[72:73],v[58:59],v[52:53] op_sel:[0,0,1] op_sel_hi:[1,0,1]\n"
"v_pk_fma_f32 v[74:75],v[74:75],v[58:59],v[52:53] op_sel:[0,0,1] op_sel_hi:[1,0,1]\n"
"v_pk_fma_f32 v[76:77],v[76:77],v[58:59],v[52:53] op_sel:[0,0,1] op_sel_hi:[1,0,1]\n"
"v_pk_fma_f32 v[78:79],v[78:79],v[58:59],v[52:53] op_sel:[0,0,1] op_sel_hi:[1,0,1]\n"
CV "v112,v72,v73\n"
CV "v113,v74,v75\n"
CV "v114,v76,v77\n"
CV "v115,v78,v79\n"
MF "a[132:135],v[160:163],v[104:107],a[132:135]\n"
"ds_read_b128 a[144:147],v120\n"
AW "a124,v112\n"
AW "a125,v113\n"
AW "a126,v114\n"
AW "a127,v115\n"
LD "v195,s[48:51],s80 offen offset:3072 lds\n"
AR "v80,a128\n"
AR "v81,a129\n"
AR "v82,a130\n"
MF "a[152:155],v[164:167],v[104:107],a[152:155]\n"
AR "v83,a131\n"
AR "v84,a148\n"
AR "v85,a149\n"
AR "v86,a150\n"
AR "v87,a151\n"
"v_pk_fma_f32 v[80:81],v[80:81],v[58:59],v[54:55] op_sel:[0,1,0] op_sel_hi:[1,1,0]\n"
"v_pk_fma_f32 v[82:83],v[82:83],v[58:59],v[54:55] op_sel:[0,1,0] op_sel_hi:[1,1,0]\n"
"v_pk_fma_f32 v[84:85],v[84:85],v[58:59],v[54:55] op_sel:[0,1,0] op_sel_hi:[1,1,0]\n"
"v_pk_fma_f32 v[86:87],v[86:87],v[58:59],v[54:55] op_sel:[0,1,0] op_sel_hi:[1,1,0]\n"
CV "v116,v80,v81\n"
CV "v117,v82,v83\n"
CV "v118,v84,v85\n"
CV "v119,v86,v87\n"
MF "a[136:139],v[160:163],v[108:111],a[136:139]\n"
"ds_read_b128 a[148:151],v120 offset:4096\n"
AW "a128,v116\n"
AW "a129,v117\n"
AW "a130,v118\n"
AW "a131,v119\n"
"v_max3_f32 v10,|v80|,|v81|,|v82|\n"
"s_lshl_b32 s58,s56,10\n"
"s_add_u32 s58,s58,147456\n"
"v_max3_f32 v10,v10,|v83|,|v84|\n"
"v_max3_f32 v10,v10,|v85|,|v86|\n"
"v_max3_f32 v10,v10,|v86|,|v87|\n"
"v_mul_f32 v10,s10,v10\n"
"v_lshrrev_b32_e64 v11,2,v9\n"
"v_readlane_b32 s12,v10,3\n"
"v_readlane_b32 s6,v10,22\n"
"v_readlane_b32 s7,v10,41\n"
"v_readlane_b32 s8,v10,60\n"
"v_add_u32 v11,s58,v11\n"
"s_min_u32 s12,s12,s6\n"
"s_min_u32 s7,s7,s8\n"
"s_min_u32 s12,s12,s7\n"
"s_and_b32 s12,s12,0x7f800000\n"
"s_sub_u32 s12,s12,0x14000000\n"
"s_cmp_lt_u32 s12,0x50000000\n"
"s_cselect_b32 s12,s12,0xe7400000\n"
"s_add_u32 s12,s12,0x18c00000\n"
"v_mov_b32_e64 v10,s12\n"
"v_or_b32 v12,0x80000000,v10\n"
"ds_write2st64_b32 v11,v10,v12 offset1:1\n"
"s_add_u32 m0,s73,53248\n"
"v_add_u32 v199,68608,v192\n"
LD "v196,s[48:51],s80 offen lds\n"
AR "v72,a132\n"
AR "v73,a133\n"
AR "v74,a134\n"
MF "a[156:159],v[164:167],v[108:111],a[156:159]\n"
AR "v75,a135\n"
AR "v76,a152\n"
AR "v77,a153\n"
AR "v78,a154\n"
AR "v79,a155\n"
"v_pk_fma_f32 v[72:73],v[72:73],v[60:61],v[54:55] op_sel:[0,0,1] op_sel_hi:[1,0,1]\n"
"v_pk_fma_f32 v[74:75],v[74:75],v[60:61],v[54:55] op_sel:[0,0,1] op_sel_hi:[1,0,1]\n"
"v_pk_fma_f32 v[76:77],v[76:77],v[60:61],v[54:55] op_sel:[0,0,1] op_sel_hi:[1,0,1]\n"
"v_pk_fma_f32 v[78:79],v[78:79],v[60:61],v[54:55] op_sel:[0,0,1] op_sel_hi:[1,0,1]\n"
CV "v112,v72,v73\n"
CV "v113,v74,v75\n"
CV "v114,v76,v77\n"
CV "v115,v78,v79\n"
MF "a[160:163],v[168:171],v[92:95],a[160:163]\n"
"ds_read_b128 a[152:155],v120 offset:8192\n"
AW "a132,v112\n"
AW "a133,v113\n"
AW "a134,v114\n"
AW "a135,v115\n"
LD "v197,s[48:51],s80 offen offset:1024 lds\n"
AR "v80,a136\n"
AR "v81,a137\n"
AR "v82,a138\n"
MF "a[180:183],v[172:175],v[92:95],a[180:183]\n"
AR "v83,a139\n"
AR "v84,a156\n"
AR "v85,a157\n"
AR "v86,a158\n"
AR "v87,a159\n"
"v_pk_fma_f32 v[80:81],v[80:81],v[60:61],v[56:57] op_sel:[0,1,0] op_sel_hi:[1,1,0]\n"
"v_pk_fma_f32 v[82:83],v[82:83],v[60:61],v[56:57] op_sel:[0,1,0] op_sel_hi:[1,1,0]\n"
"v_pk_fma_f32 v[84:85],v[84:85],v[60:61],v[56:57] op_sel:[0,1,0] op_sel_hi:[1,1,0]\n"
"v_pk_fma_f32 v[86:87],v[86:87],v[60:61],v[56:57] op_sel:[0,1,0] op_sel_hi:[1,1,0]\n"
CV "v116,v80,v81\n"
CV "v117,v82,v83\n"
CV "v118,v84,v85\n"
CV "v119,v86,v87\n"
MF "a[164:167],v[168:171],v[96:99],a[164:167]\n"
"ds_read_b128 a[156:159],v120 offset:12288\n"
AW "a136,v116\n"
AW "a137,v117\n"
AW "a138,v118\n"
AW "a139,v119\n"
AR "v72,a160\n"
AR "v73,a161\n"
AR "v74,a162\n"
MF "a[184:187],v[172:175],v[96:99],a[184:187]\n"
AR "v75,a163\n"
AR "v76,a180\n"
AR "v77,a181\n"
AR "v78,a182\n"
AR "v79,a183\n"
"v_pk_fma_f32 v[72:73],v[72:73],v[56:57],v[52:53] op_sel:[0,1,0] op_sel_hi:[1,1,0]\n"
"v_pk_fma_f32 v[74:75],v[74:75],v[56:57],v[52:53] op_sel:[0,1,0] op_sel_hi:[1,1,0]\n"
"v_pk_fma_f32 v[76:77],v[76:77],v[56:57],v[52:53] op_sel:[0,1,0] op_sel_hi:[1,1,0]\n"
"v_pk_fma_f32 v[78:79],v[78:79],v[56:57],v[52:53] op_sel:[0,1,0] op_sel_hi:[1,1,0]\n"
CV "v112,v72,v73\n"
CV "v113,v74,v75\n"
CV "v114,v76,v77\n"
CV "v115,v78,v79\n"
MF "a[168:171],v[168:171],v[100:103],a[168:171]\n"
"ds_read_b128 a[180:183],v120 offset:16384\n"
AW "a160,v112\n"
AW "a161,v113\n"
AW "a162,v114\n"
AW "a163,v115\n"
AR "v80,a164\n"
AR "v81,a165\n"
AR "v82,a166\n"
MF "a[188:191],v[172:175],v[100:103],a[188:191]\n"
AR "v83,a167\n"
AR "v84,a184\n"
AR "v85,a185\n"
AR "v86,a186\n"
AR "v87,a187\n"
"v_pk_fma_f32 v[80:81],v[80:81],v[58:59],v[52:53] op_sel:[0,0,1] op_sel_hi:[1,0,1]\n"
"v_pk_fma_f32 v[82:83],v[82:83],v[58:59],v[52:53] op_sel:[0,0,1] op_sel_hi:[1,0,1]\n"
"v_pk_fma_f32 v[84:85],v[84:85],v[58:59],v[52:53] op_sel:[0,0,1] op_sel_hi:[1,0,1]\n"
"v_pk_fma_f32 v[86:87],v[86:87],v[58:59],v[52:53] op_sel:[0,0,1] op_sel_hi:[1,0,1]\n"
CV "v116,v80,v81\n"
CV "v117,v82,v83\n"
CV "v118,v84,v85\n"
CV "v119,v86,v87\n"
MF "a[172:175],v[168:171],v[104:107],a[172:175]\n"
"ds_read_b128 a[184:187],v120 offset:20480\n"
AW "a164,v116\n"
AW "a165,v117\n"
AW "a166,v118\n"
AW "a167,v119\n"
AR "v72,a168\n"
AR "v73,a169\n"
AR "v74,a170\n"
MF "a[192:195],v[172:175],v[104:107],a[192:195]\n"
AR "v75,a171\n"
AR "v76,a188\n"
AR "v77,a189\n"
AR "v78,a190\n"
AR "v79,a191\n"
"v_pk_fma_f32 v[72:73],v[72:73],v[58:59],v[54:55] op_sel:[0,1,0] op_sel_hi:[1,1,0]\n"
"v_pk_fma_f32 v[74:75],v[74:75],v[58:59],v[54:55] op_sel:[0,1,0] op_sel_hi:[1,1,0]\n"
"v_pk_fma_f32 v[76:77],v[76:77],v[58:59],v[54:55] op_sel:[0,1,0] op_sel_hi:[1,1,0]\n"
"v_pk_fma_f32 v[78:79],v[78:79],v[58:59],v[54:55] op_sel:[0,1,0] op_sel_hi:[1,1,0]\n"
CV "v112,v72,v73\n"
CV "v113,v74,v75\n"
CV "v114,v76,v77\n"
CV "v115,v78,v79\n"
MF "a[176:179],v[168:171],v[108:111],a[176:179]\n"
"v_accvgpr_mov_b32 a188,a240\n"
"v_accvgpr_mov_b32 a189,a241\n"
"v_accvgpr_mov_b32 a190,a242\n"
"v_accvgpr_mov_b32 a191,a243\n"
AW "a168,v112\n"
AW "a169,v113\n"
AW "a170,v114\n"
AW "a171,v115\n"
AR "v80,a172\n"
AR "v81,a173\n"
AR "v82,a174\n"
MF "a[196:199],v[172:175],v[108:111],a[196:199]\n"
AR "v83,a175\n"
AR "v84,a192\n"
AR "v85,a193\n"
AR "v86,a194\n"
AR "v87,a195\n"
"v_pk_fma_f32 v[80:81],v[80:81],v[60:61],v[54:55] op_sel:[0,0,1] op_sel_hi:[1,0,1]\n"
"v_pk_fma_f32 v[82:83],v[82:83],v[60:61],v[54:55] op_sel:[0,0,1] op_sel_hi:[1,0,1]\n"
"v_pk_fma_f32 v[84:85],v[84:85],v[60:61],v[54:55] op_sel:[0,0,1] op_sel_hi:[1,0,1]\n"
"v_pk_fma_f32 v[86:87],v[86:87],v[60:61],v[54:55] op_sel:[0,0,1] op_sel_hi:[1,0,1]\n"
CV "v116,v80,v81\n"
CV "v117,v82,v83\n"
CV "v118,v84,v85\n"
CV "v119,v86,v87\n"
MF "a[200:203],v[176:179],v[92:95],a[200:203]\n"
"v_accvgpr_mov_b32 a192,a244\n"
"v_accvgpr_mov_b32 a193,a245\n"
"v_accvgpr_mov_b32 a194,a246\n"
"v_accvgpr_mov_b32 a195,a247\n"
AW "a172,v116\n"
AW "a173,v117\n"
AW "a174,v118\n"
AW "a175,v119\n"
AR "v72,a176\n"
AR "v73,a177\n"
AR "v74,a178\n"
MF "a[220:223],v[180:183],v[92:95],a[220:223]\n"
AR "v75,a179\n"
AR "v76,a196\n"
AR "v77,a197\n"
AR "v78,a198\n"
AR "v79,a199\n"
"v_pk_fma_f32 v[72:73],v[72:73],v[60:61],v[56:57] op_sel:[0,1,0] op_sel_hi:[1,1,0]\n"
"v_pk_fma_f32 v[74:75],v[74:75],v[60:61],v[56:57] op_sel:[0,1,0] op_sel_hi:[1,1,0]\n"
"v_pk_fma_f32 v[76:77],v[76:77],v[60:61],v[56:57] op_sel:[0,1,0] op_sel_hi:[1,1,0]\n"
"v_pk_fma_f32 v[78:79],v[78:79],v[60:61],v[56:57] op_sel:[0,1,0] op_sel_hi:[1,1,0]\n"
CV "v112,v72,v73\n"
CV "v113,v74,v75\n"
CV "v114,v76,v77\n"
CV "v115,v78,v79\n"
MF "a[204:207],v[176:179],v[96:99],a[204:207]\n"
"v_accvgpr_mov_b32 a196,a248\n"
"v_accvgpr_mov_b32 a197,a249\n"
"v_accvgpr_mov_b32 a198,a250\n"
"v_accvgpr_mov_b32 a199,a251\n"
AW "a176,v112\n"
AW "a177,v113\n"
AW "a178,v114\n"
AW "a179,v115\n"
AR "v80,a200\n"
AR "v81,a201\n"
AR "v82,a202\n"
MF "a[224:227],v[180:183],v[96:99],a[224:227]\n"
AR "v83,a203\n"
AR "v84,a220\n"
AR "v85,a221\n"
AR "v86,a222\n"
AR "v87,a223\n"
"v_pk_fma_f32 v[80:81],v[80:81],v[56:57],v[52:53] op_sel:[0,1,0] op_sel_hi:[1,1,0]\n"
"v_pk_fma_f32 v[82:83],v[82:83],v[56:57],v[52:53] op_sel:[0,1,0] op_sel_hi:[1,1,0]\n"
"v_pk_fma_f32 v[84:85],v[84:85],v[56:57],v[52:53] op_sel:[0,1,0] op_sel_hi:[1,1,0]\n"
"v_pk_fma_f32 v[86:87],v[86:87],v[56:57],v[52:53] op_sel:[0,1,0] op_sel_hi:[1,1,0]\n"
CV "v116,v80,v81\n"
CV "v117,v82,v83\n"
CV "v118,v84,v85\n"
CV "v119,v86,v87\n"
MF "a[208:211],v[176:179],v[100:103],a[208:211]\n"
AW "a220,v62\n"
AW "a221,v63\n"
AW "a222,v64\n"
AW "a223,v65\n"
AW "a200,v116\n"
AW "a201,v117\n"
AW "a202,v118\n"
AW "a203,v119\n"
AR "v72,a204\n"
AR "v73,a205\n"
AR "v74,a206\n"
MF "a[228:231],v[180:183],v[100:103],a[228:231]\n"
AR "v75,a207\n"
AR "v76,a224\n"
AR "v77,a225\n"
AR "v78,a226\n"
AR "v79,a227\n"
"v_pk_fma_f32 v[72:73],v[72:73],v[58:59],v[52:53] op_sel:[0,0,1] op_sel_hi:[1,0,1]\n"
"v_pk_fma_f32 v[74:75],v[74:75],v[58:59],v[52:53] op_sel:[0,0,1] op_sel_hi:[1,0,1]\n"
"v_pk_fma_f32 v[76:77],v[76:77],v[58:59],v[52:53] op_sel:[0,0,1] op_sel_hi:[1,0,1]\n"
"v_pk_fma_f32 v[78:79],v[78:79],v[58:59],v[52:53] op_sel:[0,0,1] op_sel_hi:[1,0,1]\n"
CV "v112,v72,v73\n"
CV "v113,v74,v75\n"
CV "v114,v76,v77\n"
CV "v115,v78,v79\n"
MF "a[212:215],v[176:179],v[104:107],a[212:215]\n"
AW "a224,v66\n"
AW "a225,v67\n"
AW "a226,v68\n"
AW "a227,v69\n"
AW "a204,v112\n"
AW "a205,v113\n"
AW "a206,v114\n"
AW "a207,v115\n"
AR "v80,a208\n"
AR "v81,a209\n"
AR "v82,a210\n"
MF "a[232:235],v[180:183],v[104:107],a[232:235]\n"
AR "v83,a211\n"
AR "v84,a228\n"
AR "v85,a229\n"
AR "v86,a230\n"
AR "v87,a231\n"
"v_pk_fma_f32 v[80:81],v[80:81],v[58:59],v[54:55] op_sel:[0,1,0] op_sel_hi:[1,1,0]\n"
"v_pk_fma_f32 v[82:83],v[82:83],v[58:59],v[54:55] op_sel:[0,1,0] op_sel_hi:[1,1,0]\n"
"v_pk_fma_f32 v[84:85],v[84:85],v[58:59],v[54:55] op_sel:[0,1,0] op_sel_hi:[1,1,0]\n"
"v_pk_fma_f32 v[86:87],v[86:87],v[58:59],v[54:55] op_sel:[0,1,0] op_sel_hi:[1,1,0]\n"
CV "v116,v80,v81\n"
CV "v117,v82,v83\n"
CV "v118,v84,v85\n"
CV "v119,v86,v87\n"
MF "a[216:219],v[176:179],v[108:111],a[216:219]\n"
AW "a228,v70\n"
AW "a229,v71\n"
AW "a230,v132\n"
AW "a231,v133\n"
AW "a208,v116\n"
AW "a209,v117\n"
AW "a210,v118\n"
AW "a211,v119\n"
AR "v72,a212\n"
AR "v73,a213\n"
AR "v74,a214\n"
MF "a[236:239],v[180:183],v[108:111],a[236:239]\n"
AR "v75,a215\n"
AR "v76,a232\n"
AR "v77,a233\n"
AR "v78,a234\n"
AR "v79,a235\n"
"v_pk_fma_f32 v[72:73],v[72:73],v[60:61],v[54:55] op_sel:[0,0,1] op_sel_hi:[1,0,1]\n"
"v_pk_fma_f32 v[74:75],v[74:75],v[60:61],v[54:55] op_sel:[0,0,1] op_sel_hi:[1,0,1]\n"
"v_pk_fma_f32 v[76:77],v[76:77],v[60:61],v[54:55] op_sel:[0,0,1] op_sel_hi:[1,0,1]\n"
"v_pk_fma_f32 v[78:79],v[78:79],v[60:61],v[54:55] op_sel:[0,0,1] op_sel_hi:[1,0,1]\n"
CV "v112,v72,v73\n"
CV "v113,v74,v75\n"
CV "v114,v76,v77\n"
CV "v115,v78,v79\n"
AW "a232,v134\n"
AW "a233,v135\n"
AW "a234,v6\n"
AW "a235,v13\n"
AW "a212,v112\n"
AW "a213,v113\n"
AW "a214,v114\n"
AW "a215,v115\n"
AR "v80,a216\n"
AR "v81,a217\n"
AR "v82,a218\n"
AR "v83,a219\n"
AR "v84,a236\n"
AR "v85,a237\n"
AR "v86,a238\n"
AR "v87,a239\n"
"v_pk_fma_f32 v[80:81],v[80:81],v[60:61],v[56:57] op_sel:[0,1,0] op_sel_hi:[1,1,0]\n"
"v_pk_fma_f32 v[82:83],v[82:83],v[60:61],v[56:57] op_sel:[0,1,0] op_sel_hi:[1,1,0]\n"
"v_pk_fma_f32 v[84:85],v[84:85],v[60:61],v[56:57] op_sel:[0,1,0] op_sel_hi:[1,1,0]\n"
"v_pk_fma_f32 v[86:87],v[86:87],v[60:61],v[56:57] op_sel:[0,1,0] op_sel_hi:[1,1,0]\n"
CV "v116,v80,v81\n"
CV "v117,v82,v83\n"
CV "v118,v84,v85\n"
CV "v119,v86,v87\n"
AW "a236,v14\n"
AW "a237,v15\n"
"v_accvgpr_mov_b32 a238,a254\n"
"v_accvgpr_mov_b32 a239,a255\n"
AW "a216,v116\n"
AW "a217,v117\n"
AW "a218,v118\n"
AW "a219,v119\n"
"v_add_u32 v200,81920,v192\n"
"v_add_u32 v201,91136,v192\n"
"v_add_u32 v202,100352,v192\n"
"v_add_u32 v203,109568,v192\n"
"v_and_b32 v0,512,v9\n"
"v_lshlrev_b32 v0,2,v0\n"
"v_sub_u32 v0,v2,v0\n"
"s_bfe_u32 s58,s22,0x10014\n"
"s_lshl_b32 s58,s58,5\n"
"v_xor_b32 v0,s58,v0\n"
AR "v8,a252\n"
AR "v11,a253\n"
"v_xor_b32 v1,32,v0\n"
"v_xor_b32 v2,64,v0\n"
"v_xor_b32 v3,96,v0\n"
"v_add_u32 v4,98304,v0\n"
"v_add_u32 v5,98304,v1\n"
"v_add_u32 v6,98304,v2\n"
"v_add_u32 v7,98304,v3\n"
"v_bfe_u32 v14,v9,5,3\n"
"v_bfe_u32 v15,v9,8,2\n"
"v_lshlrev_b32 v15,1,v15\n"
"v_and_b32 v10,16,v9\n"
"v_mov_b32 v12,0x3f80\n"
"v_lshlrev_b32 v12,v10,v12\n"
"v_cmp_eq_u32 vcc,v14,v15\n"
"s_nop 1\n"
"v_cndmask_b32 v252,0,v12,vcc\n"
"v_mov_b32 v254,v252\n"
"v_add_u32 v15,1,v15\n"
"v_cmp_eq_u32 vcc,v14,v15\n"
"s_nop 1\n"
"v_cndmask_b32 v253,0,v12,vcc\n"
"v_mov_b32 v255,v253\n"
"v_add_u32_e64 v15,1,v15\n"
AW "a248,0\n"
AW "a249,0\n"
AW "a250,v252\n"
AW "a251,v253\n"
AW "a252,v252\n"
AW "a253,v253\n"
AW "a254,0\n"
AW "a255,0\n"
"s_and_b32 s58,s56,1\n"
"s_lshl_b32 s58,s58,10\n"
"s_lshr_b32 s59,s56,1\n"
"s_lshl_b32 s59,s59,8\n"
"s_add_u32 s58,s58,s59\n"
"s_add_u32 s58,s58,147456\n"
"v_mov_b32 v252,s58\n"
"s_bfe_u32 s58,s22,0x30014\n"
"s_add_u32 s61,s58,1\n"
"s_and_b32 s61,s61,7\n"
"s_mov_b32 s64,8\n"
"s_xor_b32 s67,s78,8\n"
"s_mov_b32 s71,204800\n"
"s_mov_b32 s66,204800\n"
"s_and_b32 s73,s56,1\n"
"s_mul_i32 s73,s73,5120\n"
"s_add_u32 s73,s73,s93\n"
"s_xor_b32 s58,s58,4\n"
"s_mul_i32 s70,s58,204800\n"
"s_add_u32 s70,s70,s73\n"
"s_lshr_b32 s58,s56,1\n"
"s_mul_i32 s58,s58,10240\n"
"s_add_u32 s70,s70,s58\n"
"s_sub_u32 s70,s70,61440\n"
"v_bfe_u32 v12,v9,4,4\n"
"v_lshlrev_b32 v12,2,v12\n"
"v_mov_b32 v15,0\n"
"s_add_u32 s58,s79,73728\n"
"v_lshrrev_b32 v13,2,v9\n"
"v_add_u32 v13,s58,v13\n"
"s_mov_b32 s85,0\n"
"s_mov_b32 s83,0xf0000000\n"
"s_mov_b32 s82,0xf0000000\n"
"s_waitcnt vmcnt(0) lgkmcnt(0)\n"
"s_barrier\n"
"ds_read_b128 v[252:255],v252\n"
TR "v[176:177],v0 offset:24576\n"
TR "v[178:179],v0 offset:26624\n"
TR "v[180:181],v0 offset:28672\n"
TR "v[182:183],v0 offset:30720\n"
TR "v[184:185],v0 offset:32768\n"
TR "v[186:187],v0 offset:34816\n"
"s_waitcnt lgkmcnt(0)\n"
"s_cmp_ge_u32 s56,2\n"
"s_cbranch_scc1 .Lrole1\n"
"s_add_u32 s80,s88,122880\n"
"s_add_u32 m0,s79,98304\n"
"s_nop 0\n"
LD "v192,s[48:51],s80 offen lds\n"
LD "v193,s[48:51],s80 offen offset:1024 lds\n"
LD "v194,s[48:51],s80 offen offset:2048 lds\n"
"s_nop 7\n"
".Lpl0:\n"
MF "v[16:19],a[0:3],v[176:179],v[252:255]\n"
"s_nop 0\n"
TR "v[188:189],v1 offset:24576\n"
"ds_write_addtid_b32 v108 offset:27648\n"
MF "v[16:19],a[40:43],v[180:183],v[16:19]\n"
"s_nop 0\n"
TR "v[190:191],v1 offset:26624\n"
"ds_write_addtid_b32 v109 offset:27904\n"
MF "v[16:19],a[80:83],v[184:187],v[16:19]\n"
LD "v195,s[48:51],s80 offen offset:3072 lds\n"
MF "v[24:27],a[84:87],v[184:187],v[252:255]\n"
"s_add_u32 s60,s64,1\n"
TR "a[240:241],v1 offset:28672\n"
"ds_write_addtid_b32 v110 offset:28160\n"
MF "v[24:27],a[44:47],v[180:183],v[24:27]\n"
"s_add_u32 m0,s79,102400\n"
TR "a[242:243],v1 offset:30720\n"
"ds_write_addtid_b32 v111 offset:24320\n"
MF "v[24:27],a[4:7],v[176:179],v[24:27]\n"
LD "v196,s[48:51],s80 offen lds\n"
MF "v[32:35],a[8:11],v[176:179],v[252:255]\n"
"s_cmp_eq_u32 s60,8\n"
"s_cselect_b32 s60,0,s60\n"
TR "a[244:245],v1 offset:32768\n"
MF "v[32:35],a[48:51],v[180:183],v[32:35]\n"
"s_cmp_eq_u32 s60,10\n"
"s_cselect_b32 s60,s61,s60\n"
TR "a[246:247],v1 offset:34816\n"
MF "v[32:35],a[88:91],v[184:187],v[32:35]\n"
"s_sub_u32 s65,s60,s64\n"
"ds_write_addtid_b32 v112 offset:24576\n"
"ds_write_addtid_b32 v113 offset:24832\n"
MF "v[40:43],a[92:95],v[184:187],v[252:255]\n"
"s_lshl_b32 s65,s65,7\n"
"ds_write_addtid_b32 v114 offset:25088\n"
"ds_write_addtid_b32 v115 offset:25344\n"
MF "v[40:43],a[52:55],v[180:183],v[40:43]\n"
LD "v197,s[48:51],s80 offen offset:1024 lds\n"
MF "v[40:43],a[12:15],v[176:179],v[40:43]\n"
"s_mov_b32 s64,s60\n"
"ds_write_addtid_b32 v116 offset:25600\n"
"ds_write_addtid_b32 v117 offset:25856\n"
MF "v[48:51],a[16:19],v[176:179],v[252:255]\n"
"s_nop 0\n"
"ds_write_addtid_b32 v118 offset:26112\n"
"ds_write_addtid_b32 v119 offset:26368\n"
MF "v[48:51],a[56:59],v[180:183],v[48:51]\n"
"s_nop 0\n"
"ds_write_addtid_b32 v120 offset:26624\n"
"ds_write_addtid_b32 v121 offset:26880\n"
MF "v[48:51],a[96:99],v[184:187],v[48:51]\n"
"s_waitcnt lgkmcnt(10)\n"
LD "v198,s[48:51],s80 offen offset:2048 lds\n"
MF "v[52:55],a[96:99],a[244:247],v[252:255]\n"
"s_nop 0\n"
TR "v[176:177],v2 offset:24576\n"
"ds_write_addtid_b32 v122 offset:27136\n"
MF "v[52:55],a[56:59],a[240:243],v[52:55]\n"
"s_nop 0\n"
TR "v[178:179],v2 offset:26624\n"
"ds_write_addtid_b32 v123 offset:27392\n"
MF "v[52:55],a[16:19],v[188:191],v[52:55]\n"
"s_nop 0\n"
"ds_write_addtid_b32 v124 offset:27648\n"
"ds_write_addtid_b32 v125 offset:27904\n"
MF "v[44:47],a[12:15],v[188:191],v[252:255]\n"
"s_nop 0\n"
TR "v[180:181],v2 offset:28672\n"
"ds_write_addtid_b32 v126 offset:28160\n"
MF "v[44:47],a[52:55],a[240:243],v[44:47]\n"
"s_nop 0\n"
TR "v[182:183],v2 offset:30720\n"
"ds_write_addtid_b32 v127 offset:28416\n"
MF "v[44:47],a[92:95],a[244:247],v[44:47]\n"
LD "v199,s[48:51],s80 offen offset:3072 lds\n"
MF "v[36:39],a[88:91],a[244:247],v[252:255]\n"
"s_nop 0\n"
TR "v[184:185],v2 offset:32768\n"
"ds_write_addtid_b32 v128 offset:28672\n"
MF "v[36:39],a[48:51],a[240:243],v[36:39]\n"
"s_add_u32 m0,s79,106496\n"
TR "v[186:187],v2 offset:34816\n"
"ds_write_addtid_b32 v129 offset:24832\n"
MF "v[36:39],a[8:11],v[188:191],v[36:39]\n"
LD "v200,s[48:51],s80 offen lds\n"
MF "v[28:31],a[4:7],v[188:191],v[252:255]\n"
"s_nop 0\n"
"ds_write_addtid_b32 v130 offset:25088\n"
"ds_write_addtid_b32 v131 offset:25344\n"
MF "v[28:31],a[44:47],a[240:243],v[28:31]\n"
"s_nop 0\n"
"ds_write_addtid_b32 v132 offset:25600\n"
"ds_write_addtid_b32 v133 offset:25856\n"
MF "v[28:31],a[84:87],a[244:247],v[28:31]\n"
"s_nop 0\n"
"ds_write_addtid_b32 v134 offset:26112\n"
"ds_write_addtid_b32 v135 offset:26368\n"
MF "v[20:23],a[80:83],a[244:247],v[252:255]\n"
MF "v[20:23],a[40:43],a[240:243],v[20:23]\n"
LD "v201,s[48:51],s80 offen offset:1024 lds\n"
MF "v[20:23],a[0:3],v[188:191],v[20:23]\n"
"s_waitcnt lgkmcnt(7)\n"
MF "v[56:59],a[0:3],v[176:179],v[252:255]\n"
TR "v[188:189],v3 offset:24576\n"
MF "v[56:59],a[40:43],v[180:183],v[56:59]\n"
TR "v[190:191],v3 offset:26624\n"
MF "v[56:59],a[80:83],v[184:187],v[56:59]\n"
LD "v202,s[48:51],s80 offen offset:2048 lds\n"
MF "v[64:67],a[84:87],v[184:187],v[252:255]\n"
TR "a[240:241],v3 offset:28672\n"
MF "v[64:67],a[44:47],v[180:183],v[64:67]\n"
TR "a[242:243],v3 offset:30720\n"
MF "v[64:67],a[4:7],v[176:179],v[64:67]\n"
LD "v203,s[48:51],s80 offen offset:3072 lds\n"
MF "v[72:75],a[8:11],v[176:179],v[252:255]\n"
TR "a[244:245],v3 offset:32768\n"
MF "v[72:75],a[48:51],v[180:183],v[72:75]\n"
TR "a[246:247],v3 offset:34816\n"
MF "v[72:75],a[88:91],v[184:187],v[72:75]\n"
MF "v[80:83],a[92:95],v[184:187],v[252:255]\n"
MF "v[80:83],a[52:55],v[180:183],v[80:83]\n"
MF "v[80:83],a[12:15],v[176:179],v[80:83]\n"
MF "v[88:91],a[16:19],v[176:179],v[252:255]\n"
MF "v[88:91],a[56:59],v[180:183],v[88:91]\n"
MF "v[88:91],a[96:99],v[184:187],v[88:91]\n"
"s_waitcnt vmcnt(18) lgkmcnt(0)\n"
"s_barrier\n"
MF "v[92:95],a[96:99],a[244:247],v[252:255]\n"
"s_add_u32 s89,s89,s65\n"
TR "v[176:177],v0\n"
MF "v[92:95],a[56:59],a[240:243],v[92:95]\n"
"s_add_u32 m0,s79,24576\n"
TR "v[178:179],v0 offset:2048\n"
MF "v[92:95],a[16:19],v[188:191],v[92:95]\n"
LD "v192,s[48:51],s89 offen lds\n"
MF "v[84:87],a[12:15],v[188:191],v[252:255]\n"
TR "v[180:181],v0 offset:4096\n"
MF "v[84:87],a[52:55],a[240:243],v[84:87]\n"
TR "v[182:183],v0 offset:6144\n"
MF "v[84:87],a[92:95],a[244:247],v[84:87]\n"
LD "v193,s[48:51],s89 offen offset:1024 lds\n"
MF "v[76:79],a[88:91],a[244:247],v[252:255]\n"
TR "v[184:185],v0 offset:8192\n"
MF "v[76:79],a[48:51],a[240:243],v[76:79]\n"
"s_nop 0\n"
TR "v[186:187],v0 offset:10240\n"
"ds_write_addtid_b32 v136 offset:49152\n"
MF "v[76:79],a[8:11],v[188:191],v[76:79]\n"
"s_nop 0\n"
"ds_write_addtid_b32 v137 offset:49408\n"
"ds_write_addtid_b32 v138 offset:49664\n"
MF "v[68:71],a[4:7],v[188:191],v[252:255]\n"
LD "v194,s[48:51],s89 offen offset:2048 lds\n"
MF "v[68:71],a[44:47],a[240:243],v[68:71]\n"
"buffer_load_dword v10,v12,s[40:43],s86 offen sc1\n"
MF "v[68:71],a[84:87],a[244:247],v[68:71]\n"
"s_nop 0\n"
"ds_write_addtid_b32 v139 offset:49920\n"
"ds_write_addtid_b32 v140 offset:50176\n"
MF "v[60:63],a[80:83],a[244:247],v[252:255]\n"
"s_nop 0\n"
"ds_write_addtid_b32 v141 offset:50432\n"
"ds_write_addtid_b32 v142 offset:50688\n"
MF "v[60:63],a[40:43],a[240:243],v[60:63]\n"
"s_nop 0\n"
"ds_write_addtid_b32 v143 offset:50944\n"
"ds_write_addtid_b32 v144 offset:51200\n"
MF "v[60:63],a[0:3],v[188:191],v[60:63]\n"
"s_waitcnt lgkmcnt(9)\n"
LD "v195,s[48:51],s89 offen offset:3072 lds\n"
MF "v[16:19],a[120:123],v[176:179],v[16:19]\n"
"s_nop 0\n"
TR "v[188:189],v1\n"
"ds_write_addtid_b32 v145 offset:51456\n"
MF "v[16:19],a[160:163],v[180:183],v[16:19]\n"
"s_nop 0\n"
TR "v[190:191],v1 offset:2048\n"
"ds_write_addtid_b32 v146 offset:51712\n"
MF "v[16:19],a[200:203],v[184:187],v[16:19]\n"
"s_nop 0\n"
"ds_write_addtid_b32 v147 offset:51968\n"
"ds_write_addtid_b32 v148 offset:52224\n"
MF "v[24:27],a[204:207],v[184:187],v[24:27]\n"
"s_nop 0\n"
TR "a[240:241],v1 offset:4096\n"
"ds_write_addtid_b32 v149 offset:52480\n"
MF "v[24:27],a[164:167],v[180:183],v[24:27]\n"
"s_add_u32 m0,s79,28672\n"
TR "a[242:243],v1 offset:6144\n"
"ds_write_addtid_b32 v150 offset:48640\n"
MF "v[24:27],a[124:127],v[176:179],v[24:27]\n"
LD "v196,s[48:51],s89 offen lds\n"
MF "v[32:35],a[128:131],v[176:179],v[32:35]\n"
"s_nop 0\n"
TR "a[244:245],v1 offset:8192\n"
"ds_write_addtid_b32 v151 offset:48896\n"
MF "v[32:35],a[168:171],v[180:183],v[32:35]\n"
"s_nop 0\n"
TR "a[246:247],v1 offset:10240\n"
"ds_write_addtid_b32 v152 offset:49152\n"
MF "v[32:35],a[208:211],v[184:187],v[32:35]\n"
LD "v197,s[48:51],s89 offen offset:1024 lds\n"
MF "v[40:43],a[212:215],v[184:187],v[40:43]\n"
"s_nop 0\n"
"ds_write_addtid_b32 v153 offset:49408\n"
"ds_write_addtid_b32 v154 offset:49664\n"
MF "v[40:43],a[172:175],v[180:183],v[40:43]\n"
"s_nop 0\n"
"ds_write_addtid_b32 v155 offset:49920\n"
"ds_write_addtid_b32 v156 offset:50176\n"
MF "v[40:43],a[132:135],v[176:179],v[40:43]\n"
"s_nop 0\n"
"ds_write_addtid_b32 v157 offset:50432\n"
"ds_write_addtid_b32 v158 offset:50688\n"
MF "v[48:51],a[136:139],v[176:179],v[48:51]\n"
"s_nop 0\n"
"ds_write_addtid_b32 v159 offset:50944\n"
"ds_write_addtid_b32 v160 offset:51200\n"
MF "v[48:51],a[176:179],v[180:183],v[48:51]\n"
LD "v198,s[48:51],s89 offen offset:2048 lds\n"
MF "v[48:51],a[216:219],v[184:187],v[48:51]\n"
"s_waitcnt lgkmcnt(9)\n"
"ds_write_addtid_b32 v161 offset:51456\n"
"ds_write_addtid_b32 v162 offset:51712\n"
MF "v[52:55],a[216:219],a[244:247],v[52:55]\n"
"s_nop 0\n"
TR "v[176:177],v2\n"
"ds_write_addtid_b32 v163 offset:51968\n"
MF "v[52:55],a[176:179],a[240:243],v[52:55]\n"
"s_nop 0\n"
TR "v[178:179],v2 offset:2048\n"
"ds_write_addtid_b32 v164 offset:52224\n"
MF "v[52:55],a[136:139],v[188:191],v[52:55]\n"
LD "v199,s[48:51],s89 offen offset:3072 lds\n"
MF "v[44:47],a[132:135],v[188:191],v[44:47]\n"
"s_nop 0\n"
TR "v[180:181],v2 offset:4096\n"
"ds_write_addtid_b32 v165 offset:52480\n"
MF "v[44:47],a[172:175],a[240:243],v[44:47]\n"
"s_nop 0\n"
TR "v[182:183],v2 offset:6144\n"
"ds_write_addtid_b32 v166 offset:52736\n"
MF "v[44:47],a[212:215],a[244:247],v[44:47]\n"
"s_nop 0\n"
"ds_write_addtid_b32 v167 offset:52992\n"
"ds_write_addtid_b32 v168 offset:53248\n"
MF "v[36:39],a[208:211],a[244:247],v[36:39]\n"
"s_nop 0\n"
TR "v[184:185],v2 offset:8192\n"
"ds_write_addtid_b32 v169 offset:53504\n"
MF "v[36:39],a[168:171],a[240:243],v[36:39]\n"
"s_add_u32 m0,s79,32768\n"
TR "v[186:187],v2 offset:10240\n"
"ds_write_addtid_b32 v170 offset:49664\n"
MF "v[36:39],a[128:131],v[188:191],v[36:39]\n"
LD "v200,s[48:51],s89 offen lds\n"
MF "v[28:31],a[124:127],v[188:191],v[28:31]\n"
"s_nop 0\n"
"ds_write_addtid_b32 v171 offset:49920\n"
"ds_write_addtid_b32 v172 offset:50176\n"
MF "v[28:31],a[164:167],a[240:243],v[28:31]\n"
"s_nop 0\n"
"ds_write_addtid_b32 v173 offset:50432\n"
"ds_write_addtid_b32 v174 offset:50688\n"
MF "v[28:31],a[204:207],a[244:247],v[28:31]\n"
"ds_write_addtid_b32 v175 offset:50944\n"
MF "v[20:23],a[200:203],a[244:247],v[20:23]\n"
LD "v201,s[48:51],s89 offen offset:1024 lds\n"
MF "v[20:23],a[160:163],a[240:243],v[20:23]\n"
MF "v[20:23],a[120:123],v[188:191],v[20:23]\n"
"s_waitcnt lgkmcnt(6)\n"
MF "v[56:59],a[120:123],v[176:179],v[56:59]\n"
TR "v[188:189],v3\n"
MF "v[56:59],a[160:163],v[180:183],v[56:59]\n"
TR "v[190:191],v3 offset:2048\n"
MF "v[56:59],a[200:203],v[184:187],v[56:59]\n"
LD "v202,s[48:51],s89 offen offset:2048 lds\n"
MF "v[64:67],a[204:207],v[184:187],v[64:67]\n"
TR "a[240:241],v3 offset:4096\n"
MF "v[64:67],a[164:167],v[180:183],v[64:67]\n"
TR "a[242:243],v3 offset:6144\n"
MF "v[64:67],a[124:127],v[176:179],v[64:67]\n"
LD "v203,s[48:51],s89 offen offset:3072 lds\n"
MF "v[72:75],a[128:131],v[176:179],v[72:75]\n"
TR "a[244:245],v3 offset:8192\n"
MF "v[72:75],a[168:171],v[180:183],v[72:75]\n"
TR "a[246:247],v3 offset:10240\n"
MF "v[72:75],a[208:211],v[184:187],v[72:75]\n"
MF "v[80:83],a[212:215],v[184:187],v[80:83]\n"
MF "v[80:83],a[172:175],v[180:183],v[80:83]\n"
MF "v[80:83],a[132:135],v[176:179],v[80:83]\n"
MF "v[88:91],a[136:139],v[176:179],v[88:91]\n"
MF "v[88:91],a[176:179],v[180:183],v[88:91]\n"
MF "v[88:91],a[216:219],v[184:187],v[88:91]\n"
"s_waitcnt vmcnt(25) lgkmcnt(0)\n"
"s_barrier\n"
MF "v[92:95],a[216:219],a[244:247],v[92:95]\n"
"s_add_u32 s80,s89,122880\n"
TR "v[176:177],v0 offset:49152\n"
MF "v[92:95],a[176:179],a[240:243],v[92:95]\n"
"s_add_u32 m0,s79,0\n"
"s_mul_i32 s95,s92,3277\n"
TR "v[178:179],v0 offset:51200\n"
MF "v[92:95],a[136:139],v[188:191],v[92:95]\n"
LD "v192,s[48:51],s80 offen lds\n"
MF "v[84:87],a[132:135],v[188:191],v[84:87]\n"
"s_lshr_b32 s95,s95,16\n"
"s_mul_i32 s59,s95,20\n"
TR "v[180:181],v0 offset:53248\n"
MF "v[84:87],a[172:175],a[240:243],v[84:87]\n"
"s_sub_u32 s59,s92,s59\n"
TR "v[182:183],v0 offset:55296\n"
MF "v[84:87],a[212:215],a[244:247],v[84:87]\n"
LD "v193,s[48:51],s80 offen offset:1024 lds\n"
MF "v[76:79],a[208:211],a[244:247],v[76:79]\n"
"s_cmp_ge_u32 s59,10\n"
"s_cselect_b32 s96,4294557760,0\n"
TR "v[184:185],v0 offset:57344\n"
MF "v[76:79],a[168:171],a[240:243],v[76:79]\n"
"s_add_u32 s96,s96,1024\n"
"s_mul_i32 s59,s59,40960\n"
TR "v[186:187],v0 offset:59392\n"
MF "v[76:79],a[128:131],v[188:191],v[76:79]\n"
"s_add_u32 s96,s96,s59\n"
"s_lshl_b32 s59,s95,7\n"
"s_add_u32 s96,s96,s59\n"
MF "v[68:71],a[124:127],v[188:191],v[68:71]\n"
"s_add_u32 s95,s95,3\n"
"s_cmp_ge_u32 s92,40\n"
"s_cselect_b32 s95,99,s95\n"
MF "v[68:71],a[164:167],a[240:243],v[68:71]\n"
LD "v194,s[48:51],s80 offen offset:2048 lds\n"
MF "v[68:71],a[204:207],a[244:247],v[68:71]\n"
"s_cmp_ge_u32 s85,5\n"
"s_cselect_b32 s95,99,s95\n"
"s_cselect_b32 s72,s85,0\n"
MF "v[60:63],a[200:203],a[244:247],v[60:63]\n"
MF "v[60:63],a[160:163],a[240:243],v[60:63]\n"
MF "v[60:63],a[120:123],v[188:191],v[60:63]\n"
"s_waitcnt lgkmcnt(0)\n"
LD "v195,s[48:51],s80 offen offset:3072 lds\n"
MF "v[16:19],a[20:23],v[176:179],v[16:19]\n"
TR "v[188:189],v1 offset:49152\n"
MF "v[16:19],a[60:63],v[180:183],v[16:19]\n"
TR "v[190:191],v1 offset:51200\n"
MF "v[16:19],a[100:103],v[184:187],v[16:19]\n"
MF "v[24:27],a[104:107],v[184:187],v[24:27]\n"
TR "a[240:241],v1 offset:53248\n"
MF "v[24:27],a[64:67],v[180:183],v[24:27]\n"
"s_add_u32 m0,s79,4096\n"
TR "a[242:243],v1 offset:55296\n"
MF "v[24:27],a[24:27],v[176:179],v[24:27]\n"
LD "v196,s[48:51],s80 offen lds\n"
MF "v[32:35],a[28:31],v[176:179],v[32:35]\n"
TR "a[244:245],v1 offset:57344\n"
MF "v[32:35],a[68:71],v[180:183],v[32:35]\n"
TR "a[246:247],v1 offset:59392\n"
MF "v[32:35],a[108:111],v[184:187],v[32:35]\n"
LD "v197,s[48:51],s80 offen offset:1024 lds\n"
MF "v[40:43],a[112:115],v[184:187],v[40:43]\n"
MF "v[40:43],a[72:75],v[180:183],v[40:43]\n"
MF "v[40:43],a[32:35],v[176:179],v[40:43]\n"
MF "v[48:51],a[36:39],v[176:179],v[48:51]\n"
MF "v[48:51],a[76:79],v[180:183],v[48:51]\n"
LD "v198,s[48:51],s80 offen offset:2048 lds\n"
MF "v[48:51],a[116:119],v[184:187],v[48:51]\n"
"s_waitcnt lgkmcnt(0)\n"
MF "v[52:55],a[116:119],a[244:247],v[52:55]\n"
TR "v[176:177],v2 offset:49152\n"
MF "v[52:55],a[76:79],a[240:243],v[52:55]\n"
TR "v[178:179],v2 offset:51200\n"
MF "v[52:55],a[36:39],v[188:191],v[52:55]\n"
LD "v199,s[48:51],s80 offen offset:3072 lds\n"
MF "v[44:47],a[32:35],v[188:191],v[44:47]\n"
TR "v[180:181],v2 offset:53248\n"
MF "v[44:47],a[72:75],a[240:243],v[44:47]\n"
TR "v[182:183],v2 offset:55296\n"
MF "v[44:47],a[112:115],a[244:247],v[44:47]\n"
MF "v[36:39],a[108:111],a[244:247],v[36:39]\n"
TR "v[184:185],v2 offset:57344\n"
MF "v[36:39],a[68:71],a[240:243],v[36:39]\n"
"s_add_u32 m0,s79,8192\n"
TR "v[186:187],v2 offset:59392\n"
MF "v[36:39],a[28:31],v[188:191],v[36:39]\n"
LD "v200,s[48:51],s80 offen lds\n"
MF "v[28:31],a[24:27],v[188:191],v[28:31]\n"
MF "v[28:31],a[64:67],a[240:243],v[28:31]\n"
MF "v[28:31],a[104:107],a[244:247],v[28:31]\n"
MF "v[20:23],a[100:103],a[244:247],v[20:23]\n"
LD "v201,s[48:51],s80 offen offset:1024 lds\n"
MF "v[20:23],a[60:63],a[240:243],v[20:23]\n"
"s_waitcnt vmcnt(19)\n"
"s_cmp_eq_u32 s83,0xf0000000\n"
MF "v[20:23],a[20:23],v[188:191],v[20:23]\n"
"s_cbranch_scc1 .Ljs5\n"
"s_cmp_eq_u32 s83,0xf0000010\n"
"s_cbranch_scc1 .Ljc5\n"
MF "v[248:251],v[210:213],a[250:253],0\n"
"v_mov_b32 v240,v208\n"
"v_mov_b32 v241,v209\n"
MF "v[244:247],v[214:217],a[250:253],0\n"
MF "v[248:251],v[218:221],a[250:253],v[248:251]\n"
MF "v[244:247],v[222:225],a[250:253],v[244:247]\n"
MF "v[248:251],v[226:229],a[250:253],v[248:251]\n"
MF "v[244:247],v[230:233],a[250:253],v[244:247]\n"
MF "v[248:251],v[234:237],a[250:253],v[248:251]\n"
MF "v[244:247],v[238:241],a[250:253],v[244:247]\n"
"s_branch .Ljs5\n"
".Ljc5:\n"
MF "v[16:19],a[252:255],v[208:211],v[16:19]\n"
MF "v[24:27],a[252:255],v[212:215],v[24:27]\n"
MF "v[32:35],a[252:255],v[216:219],v[32:35]\n"
MF "v[40:43],a[252:255],v[220:223],v[40:43]\n"
MF "v[48:51],a[252:255],v[224:227],v[48:51]\n"
MF "v[20:23],a[248:251],v[208:211],v[20:23]\n"
MF "v[28:31],a[248:251],v[212:215],v[28:31]\n"
MF "v[36:39],a[248:251],v[216:219],v[36:39]\n"
MF "v[44:47],a[248:251],v[220:223],v[44:47]\n"
MF "v[52:55],a[248:251],v[224:227],v[52:55]\n"
".Ljs5:\n"
"s_waitcnt lgkmcnt(0)\n"
MF "v[56:59],a[20:23],v[176:179],v[56:59]\n"
TR "v[188:189],v3 offset:49152\n"
"v_sub_u32 v10,v10,v11\n"
MF "v[56:59],a[60:63],v[180:183],v[56:59]\n"
"s_lshl_b32 s82,s92,10\n"
TR "v[190:191],v3 offset:51200\n"
"v_cmp_gt_u32 vcc,s95,v10\n"
MF "v[56:59],a[100:103],v[184:187],v[56:59]\n"
LD "v202,s[48:51],s80 offen offset:2048 lds\n"
MF "v[64:67],a[104:107],v[184:187],v[64:67]\n"
"s_add_u32 s82,s82,s93\n"
TR "a[240:241],v3 offset:53248\n"
CV "v244,v244,v245\n"
MF "v[64:67],a[64:67],v[180:183],v[64:67]\n"
TR "a[242:243],v3 offset:55296\n"
CV "v245,v246,v247\n"
MF "v[64:67],a[24:27],v[176:179],v[64:67]\n"
LD "v203,s[48:51],s80 offen offset:3072 lds\n"
MF "v[72:75],a[28:31],v[176:179],v[72:75]\n"
TR "a[244:245],v3 offset:57344\n"
CV "v246,v248,v249\n"
MF "v[72:75],a[68:71],v[180:183],v[72:75]\n"
TR "a[246:247],v3 offset:59392\n"
CV "v247,v250,v251\n"
MF "v[72:75],a[108:111],v[184:187],v[72:75]\n"
"s_cmp_eq_u64 vcc,0\n"
"s_cselect_b32 s82,s82,0xf0000000\n"
"s_cselect_b32 s84,s96,0xf0000000\n"
"s_cselect_b32 s59,32,0\n"
MF "v[80:83],a[112:115],v[184:187],v[80:83]\n"
"s_add_u32 s92,s92,s59\n"
".Lnf0:\n"
"v_readlane_b32 s58,v10,s67\n"
"s_cmp_lt_u32 s58,s72\n"
"s_cbranch_scc1 .Lsp0\n"
".Lnb0:\n"
MF "v[80:83],a[72:75],v[180:183],v[80:83]\n"
"s_cmp_ge_u32 s85,5\n"
"s_cselect_b32 s82,s70,s82\n"
"s_cselect_b32 s84,0xf0000010,s84\n"
MF "v[80:83],a[32:35],v[176:179],v[80:83]\n"
"s_cmp_ge_u32 s85,5\n"
"s_cselect_b32 s71,1024,s71\n"
"s_cselect_b32 s66,0x80000000,s66\n"
MF "v[88:91],a[36:39],v[176:179],v[88:91]\n"
"s_add_u32 s70,s70,20480\n"
"v_permlane16_swap_b32 v244,v246\n"
"v_permlane16_swap_b32 v245,v247\n"
MF "v[88:91],a[76:79],v[180:183],v[88:91]\n"
"buffer_store_dwordx4 v[244:247],v8,s[44:47],s83 offen sc1\n"
MF "v[88:91],a[116:119],v[184:187],v[88:91]\n"
"s_waitcnt vmcnt(26) lgkmcnt(0)\n"
"s_barrier\n"
MF "v[92:95],a[116:119],a[244:247],v[92:95]\n"
"s_add_u32 s88,s88,s65\n"
"s_mov_b32 s83,s84\n"
TR "v[176:177],v4\n"
MF "v[92:95],a[76:79],a[240:243],v[92:95]\n"
"s_add_u32 m0,s79,49152\n"
TR "v[178:179],v4 offset:2048\n"
MF "v[92:95],a[36:39],v[188:191],v[92:95]\n"
LD "v192,s[48:51],s88 offen lds\n"
MF "v[84:87],a[32:35],v[188:191],v[84:87]\n"
TR "v[180:181],v4 offset:4096\n"
MF "v[84:87],a[72:75],a[240:243],v[84:87]\n"
TR "v[182:183],v4 offset:6144\n"
MF "v[84:87],a[112:115],a[244:247],v[84:87]\n"
LD "v193,s[48:51],s88 offen offset:1024 lds\n"
MF "v[76:79],a[108:111],a[244:247],v[76:79]\n"
TR "v[184:185],v4 offset:8192\n"
MF "v[76:79],a[68:71],a[240:243],v[76:79]\n"
TR "v[186:187],v4 offset:10240\n"
MF "v[76:79],a[28:31],v[188:191],v[76:79]\n"
LD "v[208:211],v9,s[40:43],s82 offen sc1\n"
MF "v[68:71],a[24:27],v[188:191],v[68:71]\n"
MF "v[68:71],a[64:67],a[240:243],v[68:71]\n"
LD "v194,s[48:51],s88 offen offset:2048 lds\n"
MF "v[68:71],a[104:107],a[244:247],v[68:71]\n"
MF "v[60:63],a[100:103],a[244:247],v[60:63]\n"
MF "v[60:63],a[60:63],a[240:243],v[60:63]\n"
MF "v[60:63],a[20:23],v[188:191],v[60:63]\n"
"s_waitcnt lgkmcnt(0)\n"
LD "v195,s[48:51],s88 offen offset:3072 lds\n"
MF "v[16:19],a[140:143],v[176:179],v[16:19]\n"
TR "v[188:189],v5\n"
MF "v[16:19],a[180:183],v[180:183],v[16:19]\n"
"s_add_u32 s82,s82,s71\n"
TR "v[190:191],v5 offset:2048\n"
MF "v[16:19],a[220:223],v[184:187],v[16:19]\n"
LD "v[212:215],v9,s[40:43],s82 offen sc1\n"
MF "v[24:27],a[224:227],v[184:187],v[24:27]\n"
TR "a[240:241],v5 offset:4096\n"
MF "v[24:27],a[184:187],v[180:183],v[24:27]\n"
"s_add_u32 m0,s79,53248\n"
TR "a[242:243],v5 offset:6144\n"
MF "v[24:27],a[144:147],v[176:179],v[24:27]\n"
LD "v196,s[48:51],s88 offen lds\n"
MF "v[32:35],a[148:151],v[176:179],v[32:35]\n"
TR "a[244:245],v5 offset:8192\n"
MF "v[32:35],a[188:191],v[180:183],v[32:35]\n"
TR "a[246:247],v5 offset:10240\n"
MF "v[32:35],a[228:231],v[184:187],v[32:35]\n"
LD "v197,s[48:51],s88 offen offset:1024 lds\n"
MF "v[40:43],a[232:235],v[184:187],v[40:43]\n"
"s_add_u32 s82,s82,s71\n"
MF "v[40:43],a[192:195],v[180:183],v[40:43]\n"
LD "v[216:219],v9,s[40:43],s82 offen sc1\n"
MF "v[40:43],a[152:155],v[176:179],v[40:43]\n"
MF "v[48:51],a[156:159],v[176:179],v[48:51]\n"
MF "v[48:51],a[196:199],v[180:183],v[48:51]\n"
LD "v198,s[48:51],s88 offen offset:2048 lds\n"
MF "v[48:51],a[236:239],v[184:187],v[48:51]\n"
"s_waitcnt lgkmcnt(0)\n"
MF "v[52:55],a[236:239],a[244:247],v[52:55]\n"
TR "v[176:177],v6\n"
MF "v[52:55],a[196:199],a[240:243],v[52:55]\n"
TR "v[178:179],v6 offset:2048\n"
MF "v[52:55],a[156:159],v[188:191],v[52:55]\n"
LD "v199,s[48:51],s88 offen offset:3072 lds\n"
MF "v[44:47],a[152:155],v[188:191],v[44:47]\n"
TR "v[180:181],v6 offset:4096\n"
MF "v[44:47],a[192:195],a[240:243],v[44:47]\n"
"s_add_u32 s82,s82,s71\n"
TR "v[182:183],v6 offset:6144\n"
MF "v[44:47],a[232:235],a[244:247],v[44:47]\n"
LD "v[220:223],v9,s[40:43],s82 offen sc1\n"
MF "v[36:39],a[228:231],a[244:247],v[36:39]\n"
TR "v[184:185],v6 offset:8192\n"
MF "v[36:39],a[188:191],a[240:243],v[36:39]\n"
"s_add_u32 m0,s79,57344\n"
TR "v[186:187],v6 offset:10240\n"
MF "v[36:39],a[148:151],v[188:191],v[36:39]\n"
LD "v200,s[48:51],s88 offen lds\n"
MF "v[28:31],a[144:147],v[188:191],v[28:31]\n"
MF "v[28:31],a[184:187],a[240:243],v[28:31]\n"
"s_add_u32 s82,s82,s71\n"
MF "v[28:31],a[224:227],a[244:247],v[28:31]\n"
LD "v[224:227],v9,s[40:43],s82 offen sc1\n"
MF "v[20:23],a[220:223],a[244:247],v[20:23]\n"
LD "v201,s[48:51],s88 offen offset:1024 lds\n"
MF "v[20:23],a[180:183],a[240:243],v[20:23]\n"
MF "v[20:23],a[140:143],v[188:191],v[20:23]\n"
"s_waitcnt lgkmcnt(0)\n"
MF "v[56:59],a[140:143],v[176:179],v[56:59]\n"
TR "v[188:189],v7\n"
MF "v[56:59],a[180:183],v[180:183],v[56:59]\n"
TR "v[190:191],v7 offset:2048\n"
MF "v[56:59],a[220:223],v[184:187],v[56:59]\n"
LD "v202,s[48:51],s88 offen offset:2048 lds\n"
MF "v[64:67],a[224:227],v[184:187],v[64:67]\n"
TR "a[240:241],v7 offset:4096\n"
MF "v[64:67],a[184:187],v[180:183],v[64:67]\n"
TR "a[242:243],v7 offset:6144\n"
MF "v[64:67],a[144:147],v[176:179],v[64:67]\n"
LD "v203,s[48:51],s88 offen offset:3072 lds\n"
MF "v[72:75],a[148:151],v[176:179],v[72:75]\n"
TR "a[244:245],v7 offset:8192\n"
MF "v[72:75],a[188:191],v[180:183],v[72:75]\n"
"s_add_u32 s82,s82,s66\n"
TR "a[246:247],v7 offset:10240\n"
MF "v[72:75],a[228:231],v[184:187],v[72:75]\n"
LD "v[228:231],v9,s[40:43],s82 offen sc1\n"
MF "v[80:83],a[232:235],v[184:187],v[80:83]\n"
MF "v[80:83],a[192:195],v[180:183],v[80:83]\n"
MF "v[80:83],a[152:155],v[176:179],v[80:83]\n"
MF "v[88:91],a[156:159],v[176:179],v[88:91]\n"
"s_add_u32 s82,s82,s71\n"
MF "v[88:91],a[196:199],v[180:183],v[88:91]\n"
LD "v[232:235],v9,s[40:43],s82 offen sc1\n"
MF "v[88:91],a[236:239],v[184:187],v[88:91]\n"
"s_waitcnt vmcnt(32) lgkmcnt(0)\n"
"s_barrier\n"
MF "v[92:95],a[236:239],a[244:247],v[92:95]\n"
"s_add_u32 s80,s88,122880\n"
TR "v[176:177],v0 offset:24576\n"
MF "v[92:95],a[196:199],a[240:243],v[92:95]\n"
"s_add_u32 m0,s79,98304\n"
"s_add_u32 s85,s85,1\n"
TR "v[178:179],v0 offset:26624\n"
MF "v[92:95],a[156:159],v[188:191],v[92:95]\n"
LD "v192,s[48:51],s80 offen lds\n"
MF "v[84:87],a[152:155],v[188:191],v[84:87]\n"
"s_nop 0\n"
TR "v[180:181],v0 offset:28672\n"
"ds_write_addtid_b32 v16 offset:24576\n"
MF "v[84:87],a[192:195],a[240:243],v[84:87]\n"
"s_nop 0\n"
TR "v[182:183],v0 offset:30720\n"
"ds_write_addtid_b32 v17 offset:24832\n"
MF "v[84:87],a[232:235],a[244:247],v[84:87]\n"
LD "v193,s[48:51],s80 offen offset:1024 lds\n"
MF "v[76:79],a[228:231],a[244:247],v[76:79]\n"
"s_nop 0\n"
TR "v[184:185],v0 offset:32768\n"
"ds_write_addtid_b32 v18 offset:25088\n"
MF "v[76:79],a[188:191],a[240:243],v[76:79]\n"
"s_add_u32 s82,s82,s71\n"
TR "v[186:187],v0 offset:34816\n"
"ds_write_addtid_b32 v19 offset:25344\n"
MF "v[76:79],a[148:151],v[188:191],v[76:79]\n"
LD "v[236:239],v9,s[40:43],s82 offen sc1\n"
MF "v[68:71],a[144:147],v[188:191],v[68:71]\n"
LD "v194,s[48:51],s80 offen offset:2048 lds\n"
MF "v[68:71],a[184:187],a[240:243],v[68:71]\n"
"s_nop 0\n"
"ds_write_addtid_b32 v20 offset:25600\n"
"ds_write_addtid_b32 v21 offset:25856\n"
MF "v[68:71],a[224:227],a[244:247],v[68:71]\n"
"s_nop 0\n"
"ds_write_addtid_b32 v22 offset:26112\n"
"ds_write_addtid_b32 v23 offset:26368\n"
MF "v[60:63],a[220:223],a[244:247],v[60:63]\n"
"s_nop 0\n"
"ds_write_addtid_b32 v24 offset:26624\n"
"ds_write_addtid_b32 v25 offset:26880\n"
MF "v[60:63],a[180:183],a[240:243],v[60:63]\n"
"s_nop 0\n"
"ds_write_addtid_b32 v26 offset:27136\n"
"ds_write_addtid_b32 v27 offset:27392\n"
MF "v[60:63],a[140:143],v[188:191],v[60:63]\n"
"s_waitcnt lgkmcnt(9)\n"
"s_cmp_eq_u32 s85,9\n"
"s_cbranch_scc1 .Lplast0\n"
MF "v[96:99],a[0:3],v[176:179],v[252:255]\n"
"s_nop 0\n"
TR "v[188:189],v1 offset:24576\n"
"ds_write_addtid_b32 v28 offset:27648\n"
MF "v[96:99],a[40:43],v[180:183],v[96:99]\n"
"s_nop 0\n"
TR "v[190:191],v1 offset:26624\n"
"ds_write_addtid_b32 v29 offset:27904\n"
MF "v[96:99],a[80:83],v[184:187],v[96:99]\n"
LD "v195,s[48:51],s80 offen offset:3072 lds\n"
MF "v[104:107],a[84:87],v[184:187],v[252:255]\n"
"s_add_u32 s60,s64,1\n"
TR "a[240:241],v1 offset:28672\n"
"ds_write_addtid_b32 v30 offset:28160\n"
MF "v[104:107],a[44:47],v[180:183],v[104:107]\n"
"s_add_u32 m0,s79,102400\n"
TR "a[242:243],v1 offset:30720\n"
"ds_write_addtid_b32 v31 offset:24320\n"
MF "v[104:107],a[4:7],v[176:179],v[104:107]\n"
LD "v196,s[48:51],s80 offen lds\n"
MF "v[112:115],a[8:11],v[176:179],v[252:255]\n"
"s_cmp_eq_u32 s60,8\n"
"s_cselect_b32 s60,0,s60\n"
TR "a[244:245],v1 offset:32768\n"
MF "v[112:115],a[48:51],v[180:183],v[112:115]\n"
"s_cmp_eq_u32 s60,10\n"
"s_cselect_b32 s60,s61,s60\n"
TR "a[246:247],v1 offset:34816\n"
MF "v[112:115],a[88:91],v[184:187],v[112:115]\n"
"s_sub_u32 s65,s60,s64\n"
"ds_write_addtid_b32 v32 offset:24576\n"
"ds_write_addtid_b32 v33 offset:24832\n"
MF "v[120:123],a[92:95],v[184:187],v[252:255]\n"
"s_lshl_b32 s65,s65,7\n"
"ds_write_addtid_b32 v34 offset:25088\n"
"ds_write_addtid_b32 v35 offset:25344\n"
MF "v[120:123],a[52:55],v[180:183],v[120:123]\n"
LD "v197,s[48:51],s80 offen offset:1024 lds\n"
MF "v[120:123],a[12:15],v[176:179],v[120:123]\n"
"s_mov_b32 s64,s60\n"
"ds_write_addtid_b32 v36 offset:25600\n"
"ds_write_addtid_b32 v37 offset:25856\n"
MF "v[128:131],a[16:19],v[176:179],v[252:255]\n"
"s_nop 0\n"
"ds_write_addtid_b32 v38 offset:26112\n"
"ds_write_addtid_b32 v39 offset:26368\n"
MF "v[128:131],a[56:59],v[180:183],v[128:131]\n"
"s_nop 0\n"
"ds_write_addtid_b32 v40 offset:26624\n"
"ds_write_addtid_b32 v41 offset:26880\n"
MF "v[128:131],a[96:99],v[184:187],v[128:131]\n"
"s_waitcnt lgkmcnt(10)\n"
LD "v198,s[48:51],s80 offen offset:2048 lds\n"
MF "v[132:135],a[96:99],a[244:247],v[252:255]\n"
"s_nop 0\n"
TR "v[176:177],v2 offset:24576\n"
"ds_write_addtid_b32 v42 offset:27136\n"
MF "v[132:135],a[56:59],a[240:243],v[132:135]\n"
"s_nop 0\n"
TR "v[178:179],v2 offset:26624\n"
"ds_write_addtid_b32 v43 offset:27392\n"
MF "v[132:135],a[16:19],v[188:191],v[132:135]\n"
"s_nop 0\n"
"ds_write_addtid_b32 v44 offset:27648\n"
"ds_write_addtid_b32 v45 offset:27904\n"
MF "v[124:127],a[12:15],v[188:191],v[252:255]\n"
"s_nop 0\n"
TR "v[180:181],v2 offset:28672\n"
"ds_write_addtid_b32 v46 offset:28160\n"
MF "v[124:127],a[52:55],a[240:243],v[124:127]\n"
"s_nop 0\n"
TR "v[182:183],v2 offset:30720\n"
"ds_write_addtid_b32 v47 offset:28416\n"
MF "v[124:127],a[92:95],a[244:247],v[124:127]\n"
LD "v199,s[48:51],s80 offen offset:3072 lds\n"
MF "v[116:119],a[88:91],a[244:247],v[252:255]\n"
"s_nop 0\n"
TR "v[184:185],v2 offset:32768\n"
"ds_write_addtid_b32 v48 offset:28672\n"
MF "v[116:119],a[48:51],a[240:243],v[116:119]\n"
"s_add_u32 m0,s79,106496\n"
TR "v[186:187],v2 offset:34816\n"
"ds_write_addtid_b32 v49 offset:24832\n"
MF "v[116:119],a[8:11],v[188:191],v[116:119]\n"
LD "v200,s[48:51],s80 offen lds\n"
MF "v[108:111],a[4:7],v[188:191],v[252:255]\n"
"s_nop 0\n"
"ds_write_addtid_b32 v50 offset:25088\n"
"ds_write_addtid_b32 v51 offset:25344\n"
MF "v[108:111],a[44:47],a[240:243],v[108:111]\n"
"s_nop 0\n"
"ds_write_addtid_b32 v52 offset:25600\n"
"ds_write_addtid_b32 v53 offset:25856\n"
MF "v[108:111],a[84:87],a[244:247],v[108:111]\n"
"s_nop 0\n"
"ds_write_addtid_b32 v54 offset:26112\n"
"ds_write_addtid_b32 v55 offset:26368\n"
MF "v[100:103],a[80:83],a[244:247],v[252:255]\n"
MF "v[100:103],a[40:43],a[240:243],v[100:103]\n"
LD "v201,s[48:51],s80 offen offset:1024 lds\n"
MF "v[100:103],a[0:3],v[188:191],v[100:103]\n"
"s_waitcnt lgkmcnt(7)\n"
MF "v[136:139],a[0:3],v[176:179],v[252:255]\n"
TR "v[188:189],v3 offset:24576\n"
MF "v[136:139],a[40:43],v[180:183],v[136:139]\n"
TR "v[190:191],v3 offset:26624\n"
MF "v[136:139],a[80:83],v[184:187],v[136:139]\n"
LD "v202,s[48:51],s80 offen offset:2048 lds\n"
MF "v[144:147],a[84:87],v[184:187],v[252:255]\n"
TR "a[240:241],v3 offset:28672\n"
MF "v[144:147],a[44:47],v[180:183],v[144:147]\n"
TR "a[242:243],v3 offset:30720\n"
MF "v[144:147],a[4:7],v[176:179],v[144:147]\n"
LD "v203,s[48:51],s80 offen offset:3072 lds\n"
MF "v[152:155],a[8:11],v[176:179],v[252:255]\n"
TR "a[244:245],v3 offset:32768\n"
MF "v[152:155],a[48:51],v[180:183],v[152:155]\n"
TR "a[246:247],v3 offset:34816\n"
MF "v[152:155],a[88:91],v[184:187],v[152:155]\n"
MF "v[160:163],a[92:95],v[184:187],v[252:255]\n"
MF "v[160:163],a[52:55],v[180:183],v[160:163]\n"
MF "v[160:163],a[12:15],v[176:179],v[160:163]\n"
MF "v[168:171],a[16:19],v[176:179],v[252:255]\n"
MF "v[168:171],a[56:59],v[180:183],v[168:171]\n"
MF "v[168:171],a[96:99],v[184:187],v[168:171]\n"
"s_waitcnt vmcnt(33) lgkmcnt(0)\n"
"s_barrier\n"
MF "v[172:175],a[96:99],a[244:247],v[252:255]\n"
"s_add_u32 s89,s89,s65\n"
TR "v[176:177],v0\n"
MF "v[172:175],a[56:59],a[240:243],v[172:175]\n"
"s_add_u32 m0,s79,24576\n"
TR "v[178:179],v0 offset:2048\n"
MF "v[172:175],a[16:19],v[188:191],v[172:175]\n"
LD "v192,s[48:51],s89 offen lds\n"
MF "v[164:167],a[12:15],v[188:191],v[252:255]\n"
TR "v[180:181],v0 offset:4096\n"
MF "v[164:167],a[52:55],a[240:243],v[164:167]\n"
TR "v[182:183],v0 offset:6144\n"
MF "v[164:167],a[92:95],a[244:247],v[164:167]\n"
LD "v193,s[48:51],s89 offen offset:1024 lds\n"
MF "v[156:159],a[88:91],a[244:247],v[252:255]\n"
TR "v[184:185],v0 offset:8192\n"
MF "v[156:159],a[48:51],a[240:243],v[156:159]\n"
"s_nop 0\n"
TR "v[186:187],v0 offset:10240\n"
"ds_write_addtid_b32 v56 offset:49152\n"
MF "v[156:159],a[8:11],v[188:191],v[156:159]\n"
"s_nop 0\n"
"ds_write_addtid_b32 v57 offset:49408\n"
"ds_write_addtid_b32 v58 offset:49664\n"
MF "v[148:151],a[4:7],v[188:191],v[252:255]\n"
LD "v194,s[48:51],s89 offen offset:2048 lds\n"
MF "v[148:151],a[44:47],a[240:243],v[148:151]\n"
"buffer_load_dword v10,v12,s[40:43],s86 offen sc1\n"
MF "v[148:151],a[84:87],a[244:247],v[148:151]\n"
"s_nop 0\n"
"ds_write_addtid_b32 v59 offset:49920\n"
"ds_write_addtid_b32 v60 offset:50176\n"
MF "v[140:143],a[80:83],a[244:247],v[252:255]\n"
"s_nop 0\n"
"ds_write_addtid_b32 v61 offset:50432\n"
"ds_write_addtid_b32 v62 offset:50688\n"
MF "v[140:143],a[40:43],a[240:243],v[140:143]\n"
"s_nop 0\n"
"ds_write_addtid_b32 v63 offset:50944\n"
"ds_write_addtid_b32 v64 offset:51200\n"
MF "v[140:143],a[0:3],v[188:191],v[140:143]\n"
"s_waitcnt lgkmcnt(9)\n"
LD "v195,s[48:51],s89 offen offset:3072 lds\n"
MF "v[96:99],a[120:123],v[176:179],v[96:99]\n"
"s_nop 0\n"
TR "v[188:189],v1\n"
"ds_write_addtid_b32 v65 offset:51456\n"
MF "v[96:99],a[160:163],v[180:183],v[96:99]\n"
"s_nop 0\n"
TR "v[190:191],v1 offset:2048\n"
"ds_write_addtid_b32 v66 offset:51712\n"
MF "v[96:99],a[200:203],v[184:187],v[96:99]\n"
"s_nop 0\n"
"ds_write_addtid_b32 v67 offset:51968\n"
"ds_write_addtid_b32 v68 offset:52224\n"
MF "v[104:107],a[204:207],v[184:187],v[104:107]\n"
"s_nop 0\n"
TR "a[240:241],v1 offset:4096\n"
"ds_write_addtid_b32 v69 offset:52480\n"
MF "v[104:107],a[164:167],v[180:183],v[104:107]\n"
"s_add_u32 m0,s79,28672\n"
TR "a[242:243],v1 offset:6144\n"
"ds_write_addtid_b32 v70 offset:48640\n"
MF "v[104:107],a[124:127],v[176:179],v[104:107]\n"
LD "v196,s[48:51],s89 offen lds\n"
MF "v[112:115],a[128:131],v[176:179],v[112:115]\n"
"s_nop 0\n"
TR "a[244:245],v1 offset:8192\n"
"ds_write_addtid_b32 v71 offset:48896\n"
MF "v[112:115],a[168:171],v[180:183],v[112:115]\n"
"s_nop 0\n"
TR "a[246:247],v1 offset:10240\n"
"ds_write_addtid_b32 v72 offset:49152\n"
MF "v[112:115],a[208:211],v[184:187],v[112:115]\n"
LD "v197,s[48:51],s89 offen offset:1024 lds\n"
MF "v[120:123],a[212:215],v[184:187],v[120:123]\n"
"s_nop 0\n"
"ds_write_addtid_b32 v73 offset:49408\n"
"ds_write_addtid_b32 v74 offset:49664\n"
MF "v[120:123],a[172:175],v[180:183],v[120:123]\n"
"s_nop 0\n"
"ds_write_addtid_b32 v75 offset:49920\n"
"ds_write_addtid_b32 v76 offset:50176\n"
MF "v[120:123],a[132:135],v[176:179],v[120:123]\n"
"s_nop 0\n"
"ds_write_addtid_b32 v77 offset:50432\n"
"ds_write_addtid_b32 v78 offset:50688\n"
MF "v[128:131],a[136:139],v[176:179],v[128:131]\n"
"s_nop 0\n"
"ds_write_addtid_b32 v79 offset:50944\n"
"ds_write_addtid_b32 v80 offset:51200\n"
MF "v[128:131],a[176:179],v[180:183],v[128:131]\n"
LD "v198,s[48:51],s89 offen offset:2048 lds\n"
MF "v[128:131],a[216:219],v[184:187],v[128:131]\n"
"s_waitcnt lgkmcnt(9)\n"
"ds_write_addtid_b32 v81 offset:51456\n"
"ds_write_addtid_b32 v82 offset:51712\n"
MF "v[132:135],a[216:219],a[244:247],v[132:135]\n"
"s_nop 0\n"
TR "v[176:177],v2\n"
"ds_write_addtid_b32 v83 offset:51968\n"
MF "v[132:135],a[176:179],a[240:243],v[132:135]\n"
"s_nop 0\n"
TR "v[178:179],v2 offset:2048\n"
"ds_write_addtid_b32 v84 offset:52224\n"
MF "v[132:135],a[136:139],v[188:191],v[132:135]\n"
LD "v199,s[48:51],s89 offen offset:3072 lds\n"
MF "v[124:127],a[132:135],v[188:191],v[124:127]\n"
"s_nop 0\n"
TR "v[180:181],v2 offset:4096\n"
"ds_write_addtid_b32 v85 offset:52480\n"
MF "v[124:127],a[172:175],a[240:243],v[124:127]\n"
"s_nop 0\n"
TR "v[182:183],v2 offset:6144\n"
"ds_write_addtid_b32 v86 offset:52736\n"
MF "v[124:127],a[212:215],a[244:247],v[124:127]\n"
"s_nop 0\n"
"ds_write_addtid_b32 v87 offset:52992\n"
"ds_write_addtid_b32 v88 offset:53248\n"
MF "v[116:119],a[208:211],a[244:247],v[116:119]\n"
"s_nop 0\n"
TR "v[184:185],v2 offset:8192\n"
"ds_write_addtid_b32 v89 offset:53504\n"
MF "v[116:119],a[168:171],a[240:243],v[116:119]\n"
"s_add_u32 m0,s79,32768\n"
TR "v[186:187],v2 offset:10240\n"
"ds_write_addtid_b32 v90 offset:49664\n"
MF "v[116:119],a[128:131],v[188:191],v[116:119]\n"
LD "v200,s[48:51],s89 offen lds\n"
MF "v[108:111],a[124:127],v[188:191],v[108:111]\n"
"s_nop 0\n"
"ds_write_addtid_b32 v91 offset:49920\n"
"ds_write_addtid_b32 v92 offset:50176\n"
MF "v[108:111],a[164:167],a[240:243],v[108:111]\n"
"s_nop 0\n"
"ds_write_addtid_b32 v93 offset:50432\n"
"ds_write_addtid_b32 v94 offset:50688\n"
MF "v[108:111],a[204:207],a[244:247],v[108:111]\n"
"ds_write_addtid_b32 v95 offset:50944\n"
MF "v[100:103],a[200:203],a[244:247],v[100:103]\n"
LD "v201,s[48:51],s89 offen offset:1024 lds\n"
MF "v[100:103],a[160:163],a[240:243],v[100:103]\n"
MF "v[100:103],a[120:123],v[188:191],v[100:103]\n"
"s_waitcnt lgkmcnt(6)\n"
MF "v[136:139],a[120:123],v[176:179],v[136:139]\n"
TR "v[188:189],v3\n"
MF "v[136:139],a[160:163],v[180:183],v[136:139]\n"
TR "v[190:191],v3 offset:2048\n"
MF "v[136:139],a[200:203],v[184:187],v[136:139]\n"
LD "v202,s[48:51],s89 offen offset:2048 lds\n"
MF "v[144:147],a[204:207],v[184:187],v[144:147]\n"
TR "a[240:241],v3 offset:4096\n"
MF "v[144:147],a[164:167],v[180:183],v[144:147]\n"
TR "a[242:243],v3 offset:6144\n"
MF "v[144:147],a[124:127],v[176:179],v[144:147]\n"
LD "v203,s[48:51],s89 offen offset:3072 lds\n"
MF "v[152:155],a[128:131],v[176:179],v[152:155]\n"
TR "a[244:245],v3 offset:8192\n"
MF "v[152:155],a[168:171],v[180:183],v[152:155]\n"
TR "a[246:247],v3 offset:10240\n"
MF "v[152:155],a[208:211],v[184:187],v[152:155]\n"
MF "v[160:163],a[212:215],v[184:187],v[160:163]\n"
MF "v[160:163],a[172:175],v[180:183],v[160:163]\n"
MF "v[160:163],a[132:135],v[176:179],v[160:163]\n"
MF "v[168:171],a[136:139],v[176:179],v[168:171]\n"
MF "v[168:171],a[176:179],v[180:183],v[168:171]\n"
MF "v[168:171],a[216:219],v[184:187],v[168:171]\n"
"s_waitcnt vmcnt(28) lgkmcnt(0)\n"
"s_barrier\n"
MF "v[172:175],a[216:219],a[244:247],v[172:175]\n"
"s_add_u32 s80,s89,122880\n"
TR "v[176:177],v0 offset:49152\n"
MF "v[172:175],a[176:179],a[240:243],v[172:175]\n"
"s_add_u32 m0,s79,0\n"
"s_mul_i32 s95,s92,3277\n"
TR "v[178:179],v0 offset:51200\n"
MF "v[172:175],a[136:139],v[188:191],v[172:175]\n"
LD "v192,s[48:51],s80 offen lds\n"
MF "v[164:167],a[132:135],v[188:191],v[164:167]\n"
"s_lshr_b32 s95,s95,16\n"
"s_mul_i32 s59,s95,20\n"
TR "v[180:181],v0 offset:53248\n"
MF "v[164:167],a[172:175],a[240:243],v[164:167]\n"
"s_sub_u32 s59,s92,s59\n"
TR "v[182:183],v0 offset:55296\n"
MF "v[164:167],a[212:215],a[244:247],v[164:167]\n"
LD "v193,s[48:51],s80 offen offset:1024 lds\n"
MF "v[156:159],a[208:211],a[244:247],v[156:159]\n"
"s_cmp_ge_u32 s59,10\n"
"s_cselect_b32 s96,4294557760,0\n"
TR "v[184:185],v0 offset:57344\n"
MF "v[156:159],a[168:171],a[240:243],v[156:159]\n"
"s_add_u32 s96,s96,1024\n"
"s_mul_i32 s59,s59,40960\n"
TR "v[186:187],v0 offset:59392\n"
MF "v[156:159],a[128:131],v[188:191],v[156:159]\n"
"s_add_u32 s96,s96,s59\n"
"s_lshl_b32 s59,s95,7\n"
"s_add_u32 s96,s96,s59\n"
MF "v[148:151],a[124:127],v[188:191],v[148:151]\n"
"s_add_u32 s95,s95,3\n"
"s_cmp_ge_u32 s92,40\n"
"s_cselect_b32 s95,99,s95\n"
MF "v[148:151],a[164:167],a[240:243],v[148:151]\n"
LD "v194,s[48:51],s80 offen offset:2048 lds\n"
MF "v[148:151],a[204:207],a[244:247],v[148:151]\n"
"s_cmp_ge_u32 s85,5\n"
"s_cselect_b32 s95,99,s95\n"
"s_cselect_b32 s72,s85,0\n"
MF "v[140:143],a[200:203],a[244:247],v[140:143]\n"
MF "v[140:143],a[160:163],a[240:243],v[140:143]\n"
MF "v[140:143],a[120:123],v[188:191],v[140:143]\n"
"s_waitcnt lgkmcnt(0)\n"
LD "v195,s[48:51],s80 offen offset:3072 lds\n"
MF "v[96:99],a[20:23],v[176:179],v[96:99]\n"
TR "v[188:189],v1 offset:49152\n"
MF "v[96:99],a[60:63],v[180:183],v[96:99]\n"
TR "v[190:191],v1 offset:51200\n"
MF "v[96:99],a[100:103],v[184:187],v[96:99]\n"
MF "v[104:107],a[104:107],v[184:187],v[104:107]\n"
TR "a[240:241],v1 offset:53248\n"
MF "v[104:107],a[64:67],v[180:183],v[104:107]\n"
"s_add_u32 m0,s79,4096\n"
TR "a[242:243],v1 offset:55296\n"
MF "v[104:107],a[24:27],v[176:179],v[104:107]\n"
LD "v196,s[48:51],s80 offen lds\n"
MF "v[112:115],a[28:31],v[176:179],v[112:115]\n"
TR "a[244:245],v1 offset:57344\n"
MF "v[112:115],a[68:71],v[180:183],v[112:115]\n"
TR "a[246:247],v1 offset:59392\n"
MF "v[112:115],a[108:111],v[184:187],v[112:115]\n"
LD "v197,s[48:51],s80 offen offset:1024 lds\n"
MF "v[120:123],a[112:115],v[184:187],v[120:123]\n"
MF "v[120:123],a[72:75],v[180:183],v[120:123]\n"
MF "v[120:123],a[32:35],v[176:179],v[120:123]\n"
MF "v[128:131],a[36:39],v[176:179],v[128:131]\n"
MF "v[128:131],a[76:79],v[180:183],v[128:131]\n"
LD "v198,s[48:51],s80 offen offset:2048 lds\n"
MF "v[128:131],a[116:119],v[184:187],v[128:131]\n"
"s_waitcnt lgkmcnt(0)\n"
MF "v[132:135],a[116:119],a[244:247],v[132:135]\n"
TR "v[176:177],v2 offset:49152\n"
MF "v[132:135],a[76:79],a[240:243],v[132:135]\n"
TR "v[178:179],v2 offset:51200\n"
MF "v[132:135],a[36:39],v[188:191],v[132:135]\n"
LD "v199,s[48:51],s80 offen offset:3072 lds\n"
MF "v[124:127],a[32:35],v[188:191],v[124:127]\n"
TR "v[180:181],v2 offset:53248\n"
MF "v[124:127],a[72:75],a[240:243],v[124:127]\n"
TR "v[182:183],v2 offset:55296\n"
MF "v[124:127],a[112:115],a[244:247],v[124:127]\n"
MF "v[116:119],a[108:111],a[244:247],v[116:119]\n"
TR "v[184:185],v2 offset:57344\n"
MF "v[116:119],a[68:71],a[240:243],v[116:119]\n"
"s_add_u32 m0,s79,8192\n"
TR "v[186:187],v2 offset:59392\n"
MF "v[116:119],a[28:31],v[188:191],v[116:119]\n"
LD "v200,s[48:51],s80 offen lds\n"
MF "v[108:111],a[24:27],v[188:191],v[108:111]\n"
MF "v[108:111],a[64:67],a[240:243],v[108:111]\n"
MF "v[108:111],a[104:107],a[244:247],v[108:111]\n"
MF "v[100:103],a[100:103],a[244:247],v[100:103]\n"
LD "v201,s[48:51],s80 offen offset:1024 lds\n"
MF "v[100:103],a[60:63],a[240:243],v[100:103]\n"
"s_waitcnt vmcnt(19)\n"
"s_cmp_eq_u32 s83,0xf0000000\n"
MF "v[100:103],a[20:23],v[188:191],v[100:103]\n"
"s_cbranch_scc1 .Ljs7\n"
"s_cmp_eq_u32 s83,0xf0000010\n"
"s_cbranch_scc1 .Ljc7\n"
MF "v[248:251],v[210:213],a[250:253],0\n"
"v_mov_b32 v240,v208\n"
"v_mov_b32 v241,v209\n"
MF "v[244:247],v[214:217],a[250:253],0\n"
MF "v[248:251],v[218:221],a[250:253],v[248:251]\n"
MF "v[244:247],v[222:225],a[250:253],v[244:247]\n"
MF "v[248:251],v[226:229],a[250:253],v[248:251]\n"
MF "v[244:247],v[230:233],a[250:253],v[244:247]\n"
MF "v[248:251],v[234:237],a[250:253],v[248:251]\n"
MF "v[244:247],v[238:241],a[250:253],v[244:247]\n"
"s_branch .Ljs7\n"
".Ljc7:\n"
MF "v[96:99],a[252:255],v[208:211],v[96:99]\n"
MF "v[104:107],a[252:255],v[212:215],v[104:107]\n"
MF "v[112:115],a[252:255],v[216:219],v[112:115]\n"
MF "v[120:123],a[252:255],v[220:223],v[120:123]\n"
MF "v[128:131],a[252:255],v[224:227],v[128:131]\n"
MF "v[100:103],a[248:251],v[208:211],v[100:103]\n"
MF "v[108:111],a[248:251],v[212:215],v[108:111]\n"
MF "v[116:119],a[248:251],v[216:219],v[116:119]\n"
MF "v[124:127],a[248:251],v[220:223],v[124:127]\n"
MF "v[132:135],a[248:251],v[224:227],v[132:135]\n"
".Ljs7:\n"
"s_waitcnt lgkmcnt(0)\n"
MF "v[136:139],a[20:23],v[176:179],v[136:139]\n"
TR "v[188:189],v3 offset:49152\n"
"v_sub_u32 v10,v10,v11\n"
MF "v[136:139],a[60:63],v[180:183],v[136:139]\n"
"s_lshl_b32 s82,s92,10\n"
TR "v[190:191],v3 offset:51200\n"
"v_cmp_gt_u32 vcc,s95,v10\n"
MF "v[136:139],a[100:103],v[184:187],v[136:139]\n"
LD "v202,s[48:51],s80 offen offset:2048 lds\n"
MF "v[144:147],a[104:107],v[184:187],v[144:147]\n"
"s_add_u32 s82,s82,s93\n"
TR "a[240:241],v3 offset:53248\n"
CV "v244,v244,v245\n"
MF "v[144:147],a[64:67],v[180:183],v[144:147]\n"
TR "a[242:243],v3 offset:55296\n"
CV "v245,v246,v247\n"
MF "v[144:147],a[24:27],v[176:179],v[144:147]\n"
LD "v203,s[48:51],s80 offen offset:3072 lds\n"
MF "v[152:155],a[28:31],v[176:179],v[152:155]\n"
TR "a[244:245],v3 offset:57344\n"
CV "v246,v248,v249\n"
MF "v[152:155],a[68:71],v[180:183],v[152:155]\n"
TR "a[246:247],v3 offset:59392\n"
CV "v247,v250,v251\n"
MF "v[152:155],a[108:111],v[184:187],v[152:155]\n"
"s_cmp_eq_u64 vcc,0\n"
"s_cselect_b32 s82,s82,0xf0000000\n"
"s_cselect_b32 s84,s96,0xf0000000\n"
"s_cselect_b32 s59,32,0\n"
MF "v[160:163],a[112:115],v[184:187],v[160:163]\n"
"s_add_u32 s92,s92,s59\n"
".Lnf1:\n"
"v_readlane_b32 s58,v10,s67\n"
"s_cmp_lt_u32 s58,s72\n"
"s_cbranch_scc1 .Lsp1\n"
".Lnb1:\n"
MF "v[160:163],a[72:75],v[180:183],v[160:163]\n"
"s_cmp_ge_u32 s85,5\n"
"s_cselect_b32 s82,s70,s82\n"
"s_cselect_b32 s84,0xf0000010,s84\n"
MF "v[160:163],a[32:35],v[176:179],v[160:163]\n"
"s_cmp_ge_u32 s85,5\n"
"s_cselect_b32 s71,1024,s71\n"
"s_cselect_b32 s66,0x80000000,s66\n"
MF "v[168:171],a[36:39],v[176:179],v[168:171]\n"
"s_add_u32 s70,s70,20480\n"
"v_permlane16_swap_b32 v244,v246\n"
"v_permlane16_swap_b32 v245,v247\n"
MF "v[168:171],a[76:79],v[180:183],v[168:171]\n"
"buffer_store_dwordx4 v[244:247],v8,s[44:47],s83 offen sc1\n"
MF "v[168:171],a[116:119],v[184:187],v[168:171]\n"
"s_waitcnt vmcnt(26) lgkmcnt(0)\n"
"s_barrier\n"
MF "v[172:175],a[116:119],a[244:247],v[172:175]\n"
"s_add_u32 s88,s88,s65\n"
"s_mov_b32 s83,s84\n"
TR "v[176:177],v4\n"
MF "v[172:175],a[76:79],a[240:243],v[172:175]\n"
"s_add_u32 m0,s79,49152\n"
TR "v[178:179],v4 offset:2048\n"
MF "v[172:175],a[36:39],v[188:191],v[172:175]\n"
LD "v192,s[48:51],s88 offen lds\n"
MF "v[164:167],a[32:35],v[188:191],v[164:167]\n"
TR "v[180:181],v4 offset:4096\n"
MF "v[164:167],a[72:75],a[240:243],v[164:167]\n"
TR "v[182:183],v4 offset:6144\n"
MF "v[164:167],a[112:115],a[244:247],v[164:167]\n"
LD "v193,s[48:51],s88 offen offset:1024 lds\n"
MF "v[156:159],a[108:111],a[244:247],v[156:159]\n"
TR "v[184:185],v4 offset:8192\n"
MF "v[156:159],a[68:71],a[240:243],v[156:159]\n"
TR "v[186:187],v4 offset:10240\n"
MF "v[156:159],a[28:31],v[188:191],v[156:159]\n"
LD "v[208:211],v9,s[40:43],s82 offen sc1\n"
MF "v[148:151],a[24:27],v[188:191],v[148:151]\n"
MF "v[148:151],a[64:67],a[240:243],v[148:151]\n"
LD "v194,s[48:51],s88 offen offset:2048 lds\n"
MF "v[148:151],a[104:107],a[244:247],v[148:151]\n"
MF "v[140:143],a[100:103],a[244:247],v[140:143]\n"
MF "v[140:143],a[60:63],a[240:243],v[140:143]\n"
MF "v[140:143],a[20:23],v[188:191],v[140:143]\n"
"s_waitcnt lgkmcnt(0)\n"
LD "v195,s[48:51],s88 offen offset:3072 lds\n"
MF "v[96:99],a[140:143],v[176:179],v[96:99]\n"
TR "v[188:189],v5\n"
MF "v[96:99],a[180:183],v[180:183],v[96:99]\n"
"s_add_u32 s82,s82,s71\n"
TR "v[190:191],v5 offset:2048\n"
MF "v[96:99],a[220:223],v[184:187],v[96:99]\n"
LD "v[212:215],v9,s[40:43],s82 offen sc1\n"
MF "v[104:107],a[224:227],v[184:187],v[104:107]\n"
TR "a[240:241],v5 offset:4096\n"
MF "v[104:107],a[184:187],v[180:183],v[104:107]\n"
"s_add_u32 m0,s79,53248\n"
TR "a[242:243],v5 offset:6144\n"
MF "v[104:107],a[144:147],v[176:179],v[104:107]\n"
LD "v196,s[48:51],s88 offen lds\n"
MF "v[112:115],a[148:151],v[176:179],v[112:115]\n"
TR "a[244:245],v5 offset:8192\n"
MF "v[112:115],a[188:191],v[180:183],v[112:115]\n"
TR "a[246:247],v5 offset:10240\n"
MF "v[112:115],a[228:231],v[184:187],v[112:115]\n"
LD "v197,s[48:51],s88 offen offset:1024 lds\n"
MF "v[120:123],a[232:235],v[184:187],v[120:123]\n"
"s_add_u32 s82,s82,s71\n"
MF "v[120:123],a[192:195],v[180:183],v[120:123]\n"
LD "v[216:219],v9,s[40:43],s82 offen sc1\n"
MF "v[120:123],a[152:155],v[176:179],v[120:123]\n"
MF "v[128:131],a[156:159],v[176:179],v[128:131]\n"
MF "v[128:131],a[196:199],v[180:183],v[128:131]\n"
LD "v198,s[48:51],s88 offen offset:2048 lds\n"
MF "v[128:131],a[236:239],v[184:187],v[128:131]\n"
"s_waitcnt lgkmcnt(0)\n"
MF "v[132:135],a[236:239],a[244:247],v[132:135]\n"
TR "v[176:177],v6\n"
MF "v[132:135],a[196:199],a[240:243],v[132:135]\n"
TR "v[178:179],v6 offset:2048\n"
MF "v[132:135],a[156:159],v[188:191],v[132:135]\n"
LD "v199,s[48:51],s88 offen offset:3072 lds\n"
MF "v[124:127],a[152:155],v[188:191],v[124:127]\n"
TR "v[180:181],v6 offset:4096\n"
MF "v[124:127],a[192:195],a[240:243],v[124:127]\n"
"s_add_u32 s82,s82,s71\n"
TR "v[182:183],v6 offset:6144\n"
MF "v[124:127],a[232:235],a[244:247],v[124:127]\n"
LD "v[220:223],v9,s[40:43],s82 offen sc1\n"
MF "v[116:119],a[228:231],a[244:247],v[116:119]\n"
TR "v[184:185],v6 offset:8192\n"
MF "v[116:119],a[188:191],a[240:243],v[116:119]\n"
"s_add_u32 m0,s79,57344\n"
TR "v[186:187],v6 offset:10240\n"
MF "v[116:119],a[148:151],v[188:191],v[116:119]\n"
LD "v200,s[48:51],s88 offen lds\n"
MF "v[108:111],a[144:147],v[188:191],v[108:111]\n"
MF "v[108:111],a[184:187],a[240:243],v[108:111]\n"
"s_add_u32 s82,s82,s71\n"
MF "v[108:111],a[224:227],a[244:247],v[108:111]\n"
LD "v[224:227],v9,s[40:43],s82 offen sc1\n"
MF "v[100:103],a[220:223],a[244:247],v[100:103]\n"
LD "v201,s[48:51],s88 offen offset:1024 lds\n"
MF "v[100:103],a[180:183],a[240:243],v[100:103]\n"
MF "v[100:103],a[140:143],v[188:191],v[100:103]\n"
"s_waitcnt lgkmcnt(0)\n"
MF "v[136:139],a[140:143],v[176:179],v[136:139]\n"
TR "v[188:189],v7\n"
MF "v[136:139],a[180:183],v[180:183],v[136:139]\n"
TR "v[190:191],v7 offset:2048\n"
MF "v[136:139],a[220:223],v[184:187],v[136:139]\n"
LD "v202,s[48:51],s88 offen offset:2048 lds\n"
MF "v[144:147],a[224:227],v[184:187],v[144:147]\n"
TR "a[240:241],v7 offset:4096\n"
MF "v[144:147],a[184:187],v[180:183],v[144:147]\n"
TR "a[242:243],v7 offset:6144\n"
MF "v[144:147],a[144:147],v[176:179],v[144:147]\n"
LD "v203,s[48:51],s88 offen offset:3072 lds\n"
MF "v[152:155],a[148:151],v[176:179],v[152:155]\n"
TR "a[244:245],v7 offset:8192\n"
MF "v[152:155],a[188:191],v[180:183],v[152:155]\n"
"s_add_u32 s82,s82,s66\n"
TR "a[246:247],v7 offset:10240\n"
MF "v[152:155],a[228:231],v[184:187],v[152:155]\n"
LD "v[228:231],v9,s[40:43],s82 offen sc1\n"
MF "v[160:163],a[232:235],v[184:187],v[160:163]\n"
MF "v[160:163],a[192:195],v[180:183],v[160:163]\n"
MF "v[160:163],a[152:155],v[176:179],v[160:163]\n"
MF "v[168:171],a[156:159],v[176:179],v[168:171]\n"
"s_add_u32 s82,s82,s71\n"
MF "v[168:171],a[196:199],v[180:183],v[168:171]\n"
LD "v[232:235],v9,s[40:43],s82 offen sc1\n"
MF "v[168:171],a[236:239],v[184:187],v[168:171]\n"
"s_waitcnt vmcnt(32) lgkmcnt(0)\n"
"s_barrier\n"
MF "v[172:175],a[236:239],a[244:247],v[172:175]\n"
"s_add_u32 s80,s88,122880\n"
TR "v[176:177],v0 offset:24576\n"
MF "v[172:175],a[196:199],a[240:243],v[172:175]\n"
"s_add_u32 m0,s79,98304\n"
"s_add_u32 s85,s85,1\n"
TR "v[178:179],v0 offset:26624\n"
MF "v[172:175],a[156:159],v[188:191],v[172:175]\n"
LD "v192,s[48:51],s80 offen lds\n"
MF "v[164:167],a[152:155],v[188:191],v[164:167]\n"
"s_nop 0\n"
TR "v[180:181],v0 offset:28672\n"
"ds_write_addtid_b32 v96 offset:24576\n"
MF "v[164:167],a[192:195],a[240:243],v[164:167]\n"
"s_nop 0\n"
TR "v[182:183],v0 offset:30720\n"
"ds_write_addtid_b32 v97 offset:24832\n"
MF "v[164:167],a[232:235],a[244:247],v[164:167]\n"
LD "v193,s[48:51],s80 offen offset:1024 lds\n"
MF "v[156:159],a[228:231],a[244:247],v[156:159]\n"
"s_nop 0\n"
TR "v[184:185],v0 offset:32768\n"
"ds_write_addtid_b32 v98 offset:25088\n"
MF "v[156:159],a[188:191],a[240:243],v[156:159]\n"
"s_add_u32 s82,s82,s71\n"
TR "v[186:187],v0 offset:34816\n"
"ds_write_addtid_b32 v99 offset:25344\n"
MF "v[156:159],a[148:151],v[188:191],v[156:159]\n"
LD "v[236:239],v9,s[40:43],s82 offen sc1\n"
MF "v[148:151],a[144:147],v[188:191],v[148:151]\n"
LD "v194,s[48:51],s80 offen offset:2048 lds\n"
MF "v[148:151],a[184:187],a[240:243],v[148:151]\n"
"s_nop 0\n"
"ds_write_addtid_b32 v100 offset:25600\n"
"ds_write_addtid_b32 v101 offset:25856\n"
MF "v[148:151],a[224:227],a[244:247],v[148:151]\n"
"s_nop 0\n"
"ds_write_addtid_b32 v102 offset:26112\n"
"ds_write_addtid_b32 v103 offset:26368\n"
MF "v[140:143],a[220:223],a[244:247],v[140:143]\n"
"s_nop 0\n"
"ds_write_addtid_b32 v104 offset:26624\n"
"ds_write_addtid_b32 v105 offset:26880\n"
MF "v[140:143],a[180:183],a[240:243],v[140:143]\n"
"s_nop 0\n"
"ds_write_addtid_b32 v106 offset:27136\n"
"ds_write_addtid_b32 v107 offset:27392\n"
MF "v[140:143],a[140:143],v[188:191],v[140:143]\n"
"s_waitcnt lgkmcnt(9)\n"
"s_branch .Lpl0\n"
".Lplast0:\n"
MF "v[96:99],v[176:179],a[0:3],v[252:255]\n"
"s_nop 0\n"
TR "v[188:189],v1 offset:24576\n"
TR "v[190:191],v1 offset:26624\n"
MF "v[96:99],v[180:183],a[40:43],v[96:99]\n"
"s_nop 0\n"
TR "a[240:241],v1 offset:28672\n"
TR "a[242:243],v1 offset:30720\n"
MF "v[96:99],v[184:187],a[80:83],v[96:99]\n"
"s_add_u32 s90,s91,163840\n"
TR "a[244:245],v1 offset:32768\n"
TR "a[246:247],v1 offset:34816\n"
MF "v[104:107],v[184:187],a[84:87],v[252:255]\n"
LD "v195,s[48:51],s80 offen offset:3072 lds\n"
MF "v[104:107],v[180:183],a[44:47],v[104:107]\n"
"s_nop 0\n"
"ds_write_addtid_b32 v28 offset:27648\n"
"ds_write_addtid_b32 v29 offset:27904\n"
MF "v[104:107],v[176:179],a[4:7],v[104:107]\n"
"s_nop 0\n"
"ds_write_addtid_b32 v30 offset:28160\n"
"ds_write_addtid_b32 v31 offset:28416\n"
MF "v[112:115],v[176:179],a[8:11],v[252:255]\n"
"s_nop 0\n"
"ds_write_addtid_b32 v32 offset:28672\n"
"ds_write_addtid_b32 v33 offset:28928\n"
MF "v[112:115],v[180:183],a[48:51],v[112:115]\n"
"s_add_u32 m0,s79,102400\n"
"s_nop 0\n"
"ds_write_addtid_b32 v34 offset:25088\n"
"ds_write_addtid_b32 v35 offset:25344\n"
MF "v[112:115],v[184:187],a[88:91],v[112:115]\n"
LD "v196,s[48:51],s80 offen lds\n"
MF "v[120:123],v[184:187],a[92:95],v[252:255]\n"
"s_nop 0\n"
"ds_write_addtid_b32 v36 offset:25600\n"
"ds_write_addtid_b32 v37 offset:25856\n"
MF "v[120:123],v[180:183],a[52:55],v[120:123]\n"
"ds_write_addtid_b32 v38 offset:26112\n"
MF "v[120:123],v[176:179],a[12:15],v[120:123]\n"
"ds_write_addtid_b32 v39 offset:26368\n"
MF "v[128:131],v[176:179],a[16:19],v[252:255]\n"
"ds_write_addtid_b32 v40 offset:26624\n"
MF "v[128:131],v[180:183],a[56:59],v[128:131]\n"
"ds_write_addtid_b32 v41 offset:26880\n"
MF "v[128:131],v[184:187],a[96:99],v[128:131]\n"
"s_waitcnt lgkmcnt(14)\n"
LD "v197,s[48:51],s80 offen offset:1024 lds\n"
MF "v[132:135],a[244:247],a[96:99],v[252:255]\n"
"s_nop 0\n"
TR "v[176:177],v2 offset:24576\n"
TR "v[178:179],v2 offset:26624\n"
MF "v[132:135],a[240:243],a[56:59],v[132:135]\n"
"s_nop 0\n"
TR "v[180:181],v2 offset:28672\n"
TR "v[182:183],v2 offset:30720\n"
MF "v[132:135],v[188:191],a[16:19],v[132:135]\n"
"s_nop 0\n"
TR "v[184:185],v2 offset:32768\n"
TR "v[186:187],v2 offset:34816\n"
MF "v[124:127],v[188:191],a[12:15],v[252:255]\n"
"s_nop 0\n"
"ds_write_addtid_b32 v42 offset:27136\n"
"ds_write_addtid_b32 v43 offset:27392\n"
MF "v[124:127],a[240:243],a[52:55],v[124:127]\n"
"s_nop 0\n"
"ds_write_addtid_b32 v44 offset:27648\n"
"ds_write_addtid_b32 v45 offset:27904\n"
MF "v[124:127],a[244:247],a[92:95],v[124:127]\n"
"s_nop 0\n"
"ds_write_addtid_b32 v46 offset:28160\n"
"ds_write_addtid_b32 v47 offset:28416\n"
MF "v[116:119],a[244:247],a[88:91],v[252:255]\n"
LD "v198,s[48:51],s80 offen offset:2048 lds\n"
MF "v[116:119],a[240:243],a[48:51],v[116:119]\n"
"s_nop 0\n"
"ds_write_addtid_b32 v48 offset:28672\n"
"ds_write_addtid_b32 v49 offset:28928\n"
MF "v[116:119],v[188:191],a[8:11],v[116:119]\n"
"s_nop 0\n"
"ds_write_addtid_b32 v50 offset:29184\n"
"ds_write_addtid_b32 v51 offset:29440\n"
MF "v[108:111],v[188:191],a[4:7],v[252:255]\n"
"ds_write_addtid_b32 v52 offset:29696\n"
MF "v[108:111],a[240:243],a[44:47],v[108:111]\n"
"ds_write_addtid_b32 v53 offset:29952\n"
MF "v[108:111],a[244:247],a[84:87],v[108:111]\n"
"ds_write_addtid_b32 v54 offset:30208\n"
MF "v[100:103],a[244:247],a[80:83],v[252:255]\n"
"ds_write_addtid_b32 v55 offset:30464\n"
MF "v[100:103],a[240:243],a[40:43],v[100:103]\n"
LD "v199,s[48:51],s80 offen offset:3072 lds\n"
MF "v[100:103],v[188:191],a[0:3],v[100:103]\n"
"s_waitcnt lgkmcnt(14)\n"
MF "v[136:139],v[176:179],a[0:3],v[252:255]\n"
"s_nop 0\n"
TR "v[188:189],v3 offset:24576\n"
TR "v[190:191],v3 offset:26624\n"
MF "v[136:139],v[180:183],a[40:43],v[136:139]\n"
"s_nop 0\n"
TR "a[240:241],v3 offset:28672\n"
TR "a[242:243],v3 offset:30720\n"
MF "v[136:139],v[184:187],a[80:83],v[136:139]\n"
"s_add_u32 m0,s79,106496\n"
TR "a[244:245],v3 offset:32768\n"
TR "a[246:247],v3 offset:34816\n"
MF "v[144:147],v[184:187],a[84:87],v[252:255]\n"
"s_waitcnt lgkmcnt(0)\n"
"s_barrier\n"
MF "v[144:147],v[180:183],a[44:47],v[144:147]\n"
LD "v200,s[48:51],s80 offen lds\n"
MF "v[144:147],v[176:179],a[4:7],v[144:147]\n"
"ds_read2st64_b32 v[16:17],v13 offset0:0 offset1:1\n"
MF "v[152:155],v[176:179],a[8:11],v[252:255]\n"
"ds_read2st64_b32 v[18:19],v13 offset0:2 offset1:3\n"
MF "v[152:155],v[180:183],a[48:51],v[152:155]\n"
"ds_read2st64_b32 v[20:21],v13 offset0:4 offset1:5\n"
MF "v[152:155],v[184:187],a[88:91],v[152:155]\n"
"ds_read2st64_b32 v[22:23],v13 offset0:6 offset1:7\n"
MF "v[160:163],v[184:187],a[92:95],v[252:255]\n"
"ds_read2st64_b32 v[24:25],v13 offset0:8 offset1:9\n"
MF "v[160:163],v[180:183],a[52:55],v[160:163]\n"
"ds_read2st64_b32 v[26:27],v13 offset0:10 offset1:11\n"
MF "v[160:163],v[176:179],a[12:15],v[160:163]\n"
LD "v201,s[48:51],s80 offen offset:1024 lds\n"
MF "v[168:171],v[176:179],a[16:19],v[252:255]\n"
"ds_read2st64_b32 v[28:29],v13 offset0:12 offset1:13\n"
MF "v[168:171],v[180:183],a[56:59],v[168:171]\n"
"s_waitcnt lgkmcnt(3)\n"
"ds_read2st64_b32 v[30:31],v13 offset0:14 offset1:15\n"
"v_add_f32 v56,v56,v16\n"
MF "v[168:171],v[184:187],a[96:99],v[168:171]\n"
"s_waitcnt vmcnt(31) lgkmcnt(0)\n"
"s_barrier\n"
MF "v[172:175],a[244:247],a[96:99],v[252:255]\n"
"s_nop 0\n"
TR "v[176:177],v0\n"
TR "v[178:179],v0 offset:2048\n"
MF "v[172:175],a[240:243],a[56:59],v[172:175]\n"
"s_nop 0\n"
TR "v[180:181],v0 offset:4096\n"
TR "v[182:183],v0 offset:6144\n"
MF "v[172:175],v[188:191],a[16:19],v[172:175]\n"
"s_nop 0\n"
TR "v[184:185],v0 offset:8192\n"
TR "v[186:187],v0 offset:10240\n"
MF "v[164:167],v[188:191],a[12:15],v[252:255]\n"
LD "v202,s[48:51],s80 offen offset:2048 lds\n"
MF "v[164:167],a[240:243],a[52:55],v[164:167]\n"
"ds_read2st64_b32 v[32:33],v13 offset0:16 offset1:17\n"
"v_add_f32 v57,v57,v17\n"
MF "v[164:167],a[244:247],a[92:95],v[164:167]\n"
"ds_read2st64_b32 v[34:35],v13 offset0:18 offset1:19\n"
"v_add_f32 v58,v58,v18\n"
MF "v[156:159],a[244:247],a[88:91],v[252:255]\n"
"ds_read2st64_b32 v[36:37],v13 offset0:20 offset1:21\n"
"v_add_f32 v59,v59,v19\n"
MF "v[156:159],a[240:243],a[48:51],v[156:159]\n"
"ds_read2st64_b32 v[38:39],v13 offset0:22 offset1:23\n"
"v_add_f32 v60,v60,v20\n"
MF "v[156:159],v[188:191],a[8:11],v[156:159]\n"
"ds_read2st64_b32 v[40:41],v13 offset0:24 offset1:25\n"
"v_add_f32 v61,v61,v21\n"
MF "v[148:151],v[188:191],a[4:7],v[252:255]\n"
LD "v203,s[48:51],s80 offen offset:3072 lds\n"
MF "v[148:151],a[240:243],a[44:47],v[148:151]\n"
"ds_read2st64_b32 v[42:43],v13 offset0:26 offset1:27\n"
"v_add_f32 v62,v62,v22\n"
MF "v[148:151],a[244:247],a[84:87],v[148:151]\n"
"ds_read2st64_b32 v[44:45],v13 offset0:28 offset1:29\n"
"v_add_f32 v63,v63,v23\n"
MF "v[140:143],a[244:247],a[80:83],v[252:255]\n"
"ds_read2st64_b32 v[46:47],v13 offset0:30 offset1:31\n"
CV "v56,v56,v57\n"
MF "v[140:143],a[240:243],a[40:43],v[140:143]\n"
"ds_read2st64_b32 v[48:49],v13 offset0:32 offset1:33\n"
CV "v57,v58,v59\n"
MF "v[140:143],v[188:191],a[0:3],v[140:143]\n"
"s_waitcnt lgkmcnt(9)\n"
"ds_read2st64_b32 v[50:51],v13 offset0:34 offset1:35\n"
CV "v58,v60,v61\n"
MF "v[96:99],v[176:179],a[120:123],v[96:99]\n"
"s_nop 0\n"
TR "v[188:189],v1\n"
TR "v[190:191],v1 offset:2048\n"
MF "v[96:99],v[180:183],a[160:163],v[96:99]\n"
"s_nop 0\n"
TR "a[240:241],v1 offset:4096\n"
TR "a[242:243],v1 offset:6144\n"
MF "v[96:99],v[184:187],a[200:203],v[96:99]\n"
"s_nop 0\n"
TR "a[244:245],v1 offset:8192\n"
TR "a[246:247],v1 offset:10240\n"
MF "v[104:107],v[184:187],a[204:207],v[104:107]\n"
"ds_read2st64_b32 v[52:53],v13 offset0:36 offset1:37\n"
CV "v59,v62,v63\n"
MF "v[104:107],v[180:183],a[164:167],v[104:107]\n"
"s_add_u32 s87,s90,10240\n"
"ds_read2st64_b32 v[54:55],v13 offset0:38 offset1:39\n"
"v_add_f32 v64,v64,v24\n"
MF "v[104:107],v[176:179],a[124:127],v[104:107]\n"
"buffer_store_dwordx4 v[56:59],v9,s[40:43],s87 offen sc1\n"
MF "v[112:115],v[176:179],a[128:131],v[112:115]\n"
"v_add_f32 v65,v65,v25\n"
"v_add_f32 v66,v66,v26\n"
MF "v[112:115],v[180:183],a[168:171],v[112:115]\n"
"v_add_f32 v67,v67,v27\n"
"v_add_f32 v68,v68,v28\n"
MF "v[112:115],v[184:187],a[208:211],v[112:115]\n"
"v_add_f32 v69,v69,v29\n"
"v_add_f32 v70,v70,v30\n"
MF "v[120:123],v[184:187],a[212:215],v[120:123]\n"
"v_add_f32 v71,v71,v31\n"
CV "v64,v64,v65\n"
MF "v[120:123],v[180:183],a[172:175],v[120:123]\n"
CV "v65,v66,v67\n"
CV "v66,v68,v69\n"
MF "v[120:123],v[176:179],a[132:135],v[120:123]\n"
"s_add_u32 s87,s90,11264\n"
"s_waitcnt lgkmcnt(14)\n"
CV "v67,v70,v71\n"
MF "v[128:131],v[176:179],a[136:139],v[128:131]\n"
"buffer_store_dwordx4 v[64:67],v9,s[40:43],s87 offen sc1\n"
MF "v[128:131],v[180:183],a[176:179],v[128:131]\n"
"v_add_f32 v72,v72,v32\n"
"v_add_f32 v73,v73,v33\n"
MF "v[128:131],v[184:187],a[216:219],v[128:131]\n"
"s_waitcnt lgkmcnt(2)\n"
"v_add_f32 v74,v74,v34\n"
"v_add_f32 v75,v75,v35\n"
MF "v[132:135],a[244:247],a[216:219],v[132:135]\n"
"s_nop 0\n"
TR "v[176:177],v2\n"
TR "v[178:179],v2 offset:2048\n"
MF "v[132:135],a[240:243],a[176:179],v[132:135]\n"
"s_nop 0\n"
TR "v[180:181],v2 offset:4096\n"
TR "v[182:183],v2 offset:6144\n"
MF "v[132:135],v[188:191],a[136:139],v[132:135]\n"
"s_nop 0\n"
TR "v[184:185],v2 offset:8192\n"
TR "v[186:187],v2 offset:10240\n"
MF "v[124:127],v[188:191],a[132:135],v[124:127]\n"
"v_add_f32 v76,v76,v36\n"
"v_add_f32 v77,v77,v37\n"
MF "v[124:127],a[240:243],a[172:175],v[124:127]\n"
"v_add_f32 v78,v78,v38\n"
"v_add_f32 v79,v79,v39\n"
MF "v[124:127],a[244:247],a[212:215],v[124:127]\n"
CV "v72,v72,v73\n"
CV "v73,v74,v75\n"
MF "v[116:119],a[244:247],a[208:211],v[116:119]\n"
"s_add_u32 s87,s90,12288\n"
CV "v74,v76,v77\n"
CV "v75,v78,v79\n"
MF "v[116:119],a[240:243],a[168:171],v[116:119]\n"
"buffer_store_dwordx4 v[72:75],v9,s[40:43],s87 offen sc1\n"
MF "v[116:119],v[188:191],a[128:131],v[116:119]\n"
"v_add_f32 v80,v80,v40\n"
"v_add_f32 v81,v81,v41\n"
MF "v[108:111],v[188:191],a[124:127],v[108:111]\n"
"v_add_f32 v82,v82,v42\n"
"v_add_f32 v83,v83,v43\n"
MF "v[108:111],a[240:243],a[164:167],v[108:111]\n"
"v_add_f32 v84,v84,v44\n"
"v_add_f32 v85,v85,v45\n"
MF "v[108:111],a[244:247],a[204:207],v[108:111]\n"
"v_add_f32 v86,v86,v46\n"
"v_add_f32 v87,v87,v47\n"
MF "v[100:103],a[244:247],a[200:203],v[100:103]\n"
CV "v80,v80,v81\n"
CV "v81,v82,v83\n"
MF "v[100:103],a[240:243],a[160:163],v[100:103]\n"
"s_add_u32 s87,s90,13312\n"
CV "v82,v84,v85\n"
CV "v83,v86,v87\n"
MF "v[100:103],v[188:191],a[120:123],v[100:103]\n"
"s_waitcnt lgkmcnt(0)\n"
"buffer_store_dwordx4 v[80:83],v9,s[40:43],s87 offen sc1\n"
MF "v[136:139],v[176:179],a[120:123],v[136:139]\n"
"s_nop 0\n"
TR "v[188:189],v3\n"
TR "v[190:191],v3 offset:2048\n"
MF "v[136:139],v[180:183],a[160:163],v[136:139]\n"
"s_nop 0\n"
TR "a[240:241],v3 offset:4096\n"
TR "a[242:243],v3 offset:6144\n"
MF "v[136:139],v[184:187],a[200:203],v[136:139]\n"
"s_nop 0\n"
TR "a[244:245],v3 offset:8192\n"
TR "a[246:247],v3 offset:10240\n"
MF "v[144:147],v[184:187],a[204:207],v[144:147]\n"
"v_add_f32 v88,v88,v48\n"
"v_add_f32 v89,v89,v49\n"
MF "v[144:147],v[180:183],a[164:167],v[144:147]\n"
"v_add_f32 v90,v90,v50\n"
"v_add_f32 v91,v91,v51\n"
MF "v[144:147],v[176:179],a[124:127],v[144:147]\n"
"v_add_f32 v92,v92,v52\n"
"v_add_f32 v93,v93,v53\n"
MF "v[152:155],v[176:179],a[128:131],v[152:155]\n"
"v_add_f32 v94,v94,v54\n"
"v_add_f32 v95,v95,v55\n"
MF "v[152:155],v[180:183],a[168:171],v[152:155]\n"
CV "v88,v88,v89\n"
CV "v89,v90,v91\n"
MF "v[152:155],v[184:187],a[208:211],v[152:155]\n"
"s_add_u32 s87,s90,14336\n"
CV "v90,v92,v93\n"
CV "v91,v94,v95\n"
MF "v[160:163],v[184:187],a[212:215],v[160:163]\n"
"buffer_store_dwordx4 v[88:91],v9,s[40:43],s87 offen sc1\n"
MF "v[160:163],v[180:183],a[172:175],v[160:163]\n"
MF "v[160:163],v[176:179],a[132:135],v[160:163]\n"
MF "v[168:171],v[176:179],a[136:139],v[168:171]\n"
MF "v[168:171],v[180:183],a[176:179],v[168:171]\n"
MF "v[168:171],v[184:187],a[216:219],v[168:171]\n"
"s_waitcnt vmcnt(20) lgkmcnt(0)\n"
"s_barrier\n"
MF "v[172:175],a[244:247],a[216:219],v[172:175]\n"
"s_nop 0\n"
TR "v[176:177],v0 offset:49152\n"
TR "v[178:179],v0 offset:51200\n"
MF "v[172:175],a[240:243],a[176:179],v[172:175]\n"
"s_nop 0\n"
TR "v[180:181],v0 offset:53248\n"
TR "v[182:183],v0 offset:55296\n"
MF "v[172:175],v[188:191],a[136:139],v[172:175]\n"
"s_nop 0\n"
TR "v[184:185],v0 offset:57344\n"
TR "v[186:187],v0 offset:59392\n"
MF "v[164:167],v[188:191],a[132:135],v[164:167]\n"
"buffer_load_dword v10,v12,s[40:43],s86 offen sc1\n"
MF "v[164:167],a[240:243],a[172:175],v[164:167]\n"
MF "v[164:167],a[244:247],a[212:215],v[164:167]\n"
MF "v[156:159],a[244:247],a[208:211],v[156:159]\n"
MF "v[156:159],a[240:243],a[168:171],v[156:159]\n"
MF "v[156:159],v[188:191],a[128:131],v[156:159]\n"
MF "v[148:151],v[188:191],a[124:127],v[148:151]\n"
MF "v[148:151],a[240:243],a[164:167],v[148:151]\n"
MF "v[148:151],a[244:247],a[204:207],v[148:151]\n"
MF "v[140:143],a[244:247],a[200:203],v[140:143]\n"
MF "v[140:143],a[240:243],a[160:163],v[140:143]\n"
MF "v[140:143],v[188:191],a[120:123],v[140:143]\n"
"s_waitcnt lgkmcnt(0)\n"
MF "v[96:99],v[176:179],a[20:23],v[96:99]\n"
"s_nop 0\n"
TR "v[188:189],v1 offset:49152\n"
TR "v[190:191],v1 offset:51200\n"
MF "v[96:99],v[180:183],a[60:63],v[96:99]\n"
"s_nop 0\n"
TR "a[240:241],v1 offset:53248\n"
TR "a[242:243],v1 offset:55296\n"
MF "v[96:99],v[184:187],a[100:103],v[96:99]\n"
"s_nop 0\n"
TR "a[244:245],v1 offset:57344\n"
TR "a[246:247],v1 offset:59392\n"
MF "v[104:107],v[184:187],a[104:107],v[104:107]\n"
MF "v[104:107],v[180:183],a[64:67],v[104:107]\n"
MF "v[104:107],v[176:179],a[24:27],v[104:107]\n"
"s_waitcnt vmcnt(16)\n"
MF "v[96:99],v[208:211],a[252:255],v[96:99]\n"
MF "v[104:107],v[212:215],a[252:255],v[104:107]\n"
MF "v[112:115],v[216:219],a[252:255],v[112:115]\n"
MF "v[120:123],v[220:223],a[252:255],v[120:123]\n"
MF "v[128:131],v[224:227],a[252:255],v[128:131]\n"
MF "v[100:103],v[208:211],a[248:251],v[100:103]\n"
MF "v[108:111],v[212:215],a[248:251],v[108:111]\n"
MF "v[116:119],v[216:219],a[248:251],v[116:119]\n"
MF "v[124:127],v[220:223],a[248:251],v[124:127]\n"
MF "v[132:135],v[224:227],a[248:251],v[132:135]\n"
MF "v[112:115],v[176:179],a[28:31],v[112:115]\n"
MF "v[112:115],v[180:183],a[68:71],v[112:115]\n"
MF "v[112:115],v[184:187],a[108:111],v[112:115]\n"
MF "v[120:123],v[184:187],a[112:115],v[120:123]\n"
MF "v[120:123],v[180:183],a[72:75],v[120:123]\n"
"s_bfe_u32 s87,s22,0x30014\n"
MF "v[120:123],v[176:179],a[32:35],v[120:123]\n"
"s_lshl_b32 s87,s87,7\n"
MF "v[128:131],v[176:179],a[36:39],v[128:131]\n"
"s_and_b32 s60,s56,1\n"
MF "v[128:131],v[180:183],a[76:79],v[128:131]\n"
"s_mul_i32 s60,s60,204800\n"
MF "v[128:131],v[184:187],a[116:119],v[128:131]\n"
"s_waitcnt lgkmcnt(0)\n"
MF "v[132:135],a[244:247],a[116:119],v[132:135]\n"
"s_add_u32 s87,s87,s60\n"
TR "v[176:177],v2 offset:49152\n"
TR "v[178:179],v2 offset:51200\n"
MF "v[132:135],a[240:243],a[76:79],v[132:135]\n"
"s_mov_b32 s71,204800\n"
TR "v[180:181],v2 offset:53248\n"
TR "v[182:183],v2 offset:55296\n"
MF "v[132:135],v[188:191],a[36:39],v[132:135]\n"
"s_mov_b32 s66,204800\n"
TR "v[184:185],v2 offset:57344\n"
TR "v[186:187],v2 offset:59392\n"
MF "v[124:127],v[188:191],a[32:35],v[124:127]\n"
"s_add_u32 s72,s78,2\n"
MF "v[124:127],a[240:243],a[72:75],v[124:127]\n"
"s_and_b32 s72,s72,15\n"
MF "v[124:127],a[244:247],a[112:115],v[124:127]\n"
"s_bfe_u32 s60,s22,0x10014\n"
MF "v[116:119],a[244:247],a[108:111],v[116:119]\n"
"s_lshl_b32 s60,s60,5\n"
MF "v[116:119],a[240:243],a[68:71],v[116:119]\n"
"v_xor_b32 v242,s60,v8\n"
MF "v[116:119],v[188:191],a[28:31],v[116:119]\n"
MF "v[108:111],v[188:191],a[24:27],v[108:111]\n"
"s_add_u32 s16,s78,6\n"
MF "v[108:111],a[240:243],a[64:67],v[108:111]\n"
"s_and_b32 s16,s16,15\n"
MF "v[108:111],a[244:247],a[104:107],v[108:111]\n"
"s_add_u32 s17,s78,4\n"
MF "v[100:103],a[244:247],a[100:103],v[100:103]\n"
"s_and_b32 s17,s17,15\n"
MF "v[100:103],a[240:243],a[60:63],v[100:103]\n"
MF "v[100:103],v[188:191],a[20:23],v[100:103]\n"
"s_waitcnt lgkmcnt(0)\n"
MF "v[136:139],v[176:179],a[20:23],v[136:139]\n"
"s_nop 0\n"
TR "v[188:189],v3 offset:49152\n"
TR "v[190:191],v3 offset:51200\n"
MF "v[136:139],v[180:183],a[60:63],v[136:139]\n"
"s_nop 0\n"
TR "a[240:241],v3 offset:53248\n"
TR "a[242:243],v3 offset:55296\n"
MF "v[136:139],v[184:187],a[100:103],v[136:139]\n"
"s_nop 0\n"
TR "a[244:245],v3 offset:57344\n"
TR "a[246:247],v3 offset:59392\n"
MF "v[144:147],v[184:187],a[104:107],v[144:147]\n"
MF "v[144:147],v[180:183],a[64:67],v[144:147]\n"
MF "v[144:147],v[176:179],a[24:27],v[144:147]\n"
MF "v[152:155],v[176:179],a[28:31],v[152:155]\n"
MF "v[152:155],v[180:183],a[68:71],v[152:155]\n"
MF "v[152:155],v[184:187],a[108:111],v[152:155]\n"
MF "v[160:163],v[184:187],a[112:115],v[160:163]\n"
MF "v[160:163],v[180:183],a[72:75],v[160:163]\n"
MF "v[160:163],v[176:179],a[32:35],v[160:163]\n"
MF "v[168:171],v[176:179],a[36:39],v[168:171]\n"
MF "v[168:171],v[180:183],a[76:79],v[168:171]\n"
MF "v[168:171],v[184:187],a[116:119],v[168:171]\n"
"s_waitcnt vmcnt(0) lgkmcnt(0)\n"
"s_barrier\n"
MF "v[172:175],a[244:247],a[116:119],v[172:175]\n"
"v_sub_u32 v10,v10,v11\n"
MF "v[172:175],a[240:243],a[76:79],v[172:175]\n"
"s_nop 0\n"
TR "v[176:177],v4\n"
TR "v[178:179],v4 offset:2048\n"
MF "v[172:175],v[188:191],a[36:39],v[172:175]\n"
"s_nop 0\n"
TR "v[180:181],v4 offset:4096\n"
TR "v[182:183],v4 offset:6144\n"
MF "v[164:167],v[188:191],a[32:35],v[164:167]\n"
"s_nop 0\n"
TR "v[184:185],v4 offset:8192\n"
TR "v[186:187],v4 offset:10240\n"
MF "v[164:167],a[240:243],a[72:75],v[164:167]\n"
"v_readlane_b32 s16,v10,s16\n"
MF "v[164:167],a[244:247],a[112:115],v[164:167]\n"
"s_cmp_lt_u32 s16,9\n"
"s_cselect_b32 s16,0xf0000000,0\n"
MF "v[156:159],a[244:247],a[108:111],v[156:159]\n"
"v_readlane_b32 s17,v10,s17\n"
MF "v[156:159],a[240:243],a[68:71],v[156:159]\n"
"s_cmp_lt_u32 s17,10\n"
"s_cselect_b32 s17,0xf0000000,0\n"
MF "v[156:159],v[188:191],a[28:31],v[156:159]\n"
"s_bfe_u32 s62,s22,0x30014\n"
MF "v[148:151],v[188:191],a[24:27],v[148:151]\n"
"s_add_u32 s62,s62,3\n"
"s_and_b32 s62,s62,7\n"
"s_mul_i32 s62,s62,204800\n"
MF "v[148:151],a[240:243],a[64:67],v[148:151]\n"
"s_add_u32 s62,s62,s73\n"
"s_add_u32 s62,s62,133120\n"
"s_or_b32 s62,s62,s16\n"
MF "v[148:151],a[244:247],a[104:107],v[148:151]\n"
LD "v[16:19],v9,s[40:43],s62 offen sc1\n"
MF "v[140:143],a[244:247],a[100:103],v[140:143]\n"
LD "v[24:27],v9,s[40:43],s62 offen offset:1024 sc1\n"
MF "v[140:143],a[240:243],a[60:63],v[140:143]\n"
MF "v[140:143],v[188:191],a[20:23],v[140:143]\n"
"s_waitcnt lgkmcnt(0)\n"
LD "v[32:35],v9,s[40:43],s62 offen offset:2048 sc1\n"
MF "v[96:99],v[176:179],a[140:143],v[96:99]\n"
"s_nop 0\n"
TR "v[188:189],v5\n"
TR "v[190:191],v5 offset:2048\n"
MF "v[96:99],v[180:183],a[180:183],v[96:99]\n"
"s_nop 0\n"
TR "a[240:241],v5 offset:4096\n"
TR "a[242:243],v5 offset:6144\n"
MF "v[96:99],v[184:187],a[220:223],v[96:99]\n"
"s_nop 0\n"
TR "a[244:245],v5 offset:8192\n"
TR "a[246:247],v5 offset:10240\n"
MF "v[104:107],v[184:187],a[224:227],v[104:107]\n"
LD "v[40:43],v9,s[40:43],s62 offen offset:3072 sc1\n"
MF "v[104:107],v[180:183],a[184:187],v[104:107]\n"
MF "v[104:107],v[176:179],a[144:147],v[104:107]\n"
"s_add_u32 s62,s62,4096\n"
MF "v[112:115],v[176:179],a[148:151],v[112:115]\n"
LD "v[48:51],v9,s[40:43],s62 offen sc1\n"
MF "v[112:115],v[180:183],a[188:191],v[112:115]\n"
"s_bfe_u32 s62,s22,0x30014\n"
"ds_write_addtid_b32 v96 offset:16384\n"
"ds_write_addtid_b32 v97 offset:16640\n"
MF "v[112:115],v[184:187],a[228:231],v[112:115]\n"
"s_add_u32 s62,s62,2\n"
"s_and_b32 s62,s62,7\n"
"s_mul_i32 s62,s62,204800\n"
MF "v[120:123],v[184:187],a[232:235],v[120:123]\n"
"s_add_u32 s62,s62,s73\n"
"s_add_u32 s62,s62,153600\n"
"s_or_b32 s62,s62,s17\n"
MF "v[120:123],v[180:183],a[192:195],v[120:123]\n"
LD "v[20:23],v9,s[40:43],s62 offen sc1\n"
MF "v[120:123],v[176:179],a[152:155],v[120:123]\n"
"s_nop 0\n"
"ds_write_addtid_b32 v98 offset:16896\n"
"ds_write_addtid_b32 v99 offset:17152\n"
MF "v[128:131],v[176:179],a[156:159],v[128:131]\n"
LD "v[28:31],v9,s[40:43],s62 offen offset:1024 sc1\n"
MF "v[128:131],v[180:183],a[196:199],v[128:131]\n"
"s_nop 0\n"
"ds_write_addtid_b32 v104 offset:18432\n"
"ds_write_addtid_b32 v105 offset:18688\n"
MF "v[128:131],v[184:187],a[236:239],v[128:131]\n"
"s_waitcnt lgkmcnt(6)\n"
LD "v[36:39],v9,s[40:43],s62 offen offset:2048 sc1\n"
MF "v[100:103],v[188:191],a[140:143],v[100:103]\n"
"s_nop 0\n"
TR "v[176:177],v6\n"
TR "v[178:179],v6 offset:2048\n"
MF "v[100:103],a[240:243],a[180:183],v[100:103]\n"
"s_nop 0\n"
TR "v[180:181],v6 offset:4096\n"
TR "v[182:183],v6 offset:6144\n"
MF "v[100:103],a[244:247],a[220:223],v[100:103]\n"
"s_nop 0\n"
TR "v[184:185],v6 offset:8192\n"
TR "v[186:187],v6 offset:10240\n"
MF "v[108:111],a[244:247],a[224:227],v[108:111]\n"
LD "v[44:47],v9,s[40:43],s62 offen offset:3072 sc1\n"
MF "v[108:111],a[240:243],a[184:187],v[108:111]\n"
"s_nop 0\n"
"ds_write_addtid_b32 v106 offset:18944\n"
"ds_write_addtid_b32 v107 offset:19200\n"
MF "v[108:111],v[188:191],a[144:147],v[108:111]\n"
"s_add_u32 s62,s62,4096\n"
"ds_write_addtid_b32 v112 offset:20480\n"
"ds_write_addtid_b32 v113 offset:20736\n"
MF "v[116:119],v[188:191],a[148:151],v[116:119]\n"
LD "v[52:55],v9,s[40:43],s62 offen sc1\n"
MF "v[116:119],a[240:243],a[188:191],v[116:119]\n"
"s_nop 0\n"
"ds_write_addtid_b32 v114 offset:20992\n"
"ds_write_addtid_b32 v115 offset:21248\n"
MF "v[116:119],a[244:247],a[228:231],v[116:119]\n"
"s_nop 0\n"
"ds_write_addtid_b32 v120 offset:22528\n"
"ds_write_addtid_b32 v121 offset:22784\n"
MF "v[124:127],a[244:247],a[232:235],v[124:127]\n"
"s_nop 0\n"
"ds_write_addtid_b32 v122 offset:23040\n"
"ds_write_addtid_b32 v123 offset:23296\n"
MF "v[124:127],a[240:243],a[192:195],v[124:127]\n"
"s_nop 0\n"
"ds_write_addtid_b32 v128 offset:24576\n"
"ds_write_addtid_b32 v129 offset:24832\n"
MF "v[124:127],v[188:191],a[152:155],v[124:127]\n"
"buffer_load_dword v10,v12,s[40:43],s86 offen sc1\n"
MF "v[132:135],v[188:191],a[156:159],v[132:135]\n"
"s_nop 0\n"
"ds_write_addtid_b32 v130 offset:25088\n"
"ds_write_addtid_b32 v131 offset:25344\n"
MF "v[132:135],a[240:243],a[196:199],v[132:135]\n"
"s_nop 0\n"
"ds_write_addtid_b32 v100 offset:17408\n"
"ds_write_addtid_b32 v101 offset:17664\n"
MF "v[132:135],a[244:247],a[236:239],v[132:135]\n"
"s_waitcnt lgkmcnt(15)\n"
"ds_write_addtid_b32 v102 offset:17920\n"
"ds_write_addtid_b32 v103 offset:18176\n"
MF "v[136:139],v[176:179],a[140:143],v[136:139]\n"
"s_nop 0\n"
TR "v[188:189],v7\n"
TR "v[190:191],v7 offset:2048\n"
MF "v[136:139],v[180:183],a[180:183],v[136:139]\n"
"s_nop 0\n"
TR "a[240:241],v7 offset:4096\n"
TR "a[242:243],v7 offset:6144\n"
MF "v[136:139],v[184:187],a[220:223],v[136:139]\n"
"s_nop 0\n"
TR "a[244:245],v7 offset:8192\n"
TR "a[246:247],v7 offset:10240\n"
MF "v[144:147],v[184:187],a[224:227],v[144:147]\n"
"s_nop 0\n"
"ds_write_addtid_b32 v108 offset:19456\n"
"ds_write_addtid_b32 v109 offset:19712\n"
MF "v[144:147],v[180:183],a[184:187],v[144:147]\n"
"s_nop 0\n"
"ds_write_addtid_b32 v110 offset:19968\n"
"ds_write_addtid_b32 v111 offset:20224\n"
MF "v[144:147],v[176:179],a[144:147],v[144:147]\n"
"s_nop 0\n"
"ds_write_addtid_b32 v116 offset:21504\n"
"ds_write_addtid_b32 v117 offset:21760\n"
MF "v[152:155],v[176:179],a[148:151],v[152:155]\n"
"s_nop 0\n"
"ds_write_addtid_b32 v118 offset:22016\n"
"ds_write_addtid_b32 v119 offset:22272\n"
MF "v[152:155],v[180:183],a[188:191],v[152:155]\n"
"ds_write_addtid_b32 v124 offset:23552\n"
MF "v[152:155],v[184:187],a[228:231],v[152:155]\n"
"ds_write_addtid_b32 v125 offset:23808\n"
MF "v[160:163],v[184:187],a[232:235],v[160:163]\n"
"ds_write_addtid_b32 v126 offset:24064\n"
MF "v[160:163],v[180:183],a[192:195],v[160:163]\n"
"ds_write_addtid_b32 v127 offset:24320\n"
MF "v[160:163],v[176:179],a[152:155],v[160:163]\n"
"ds_write_addtid_b32 v132 offset:25600\n"
MF "v[168:171],v[176:179],a[156:159],v[168:171]\n"
"ds_write_addtid_b32 v133 offset:25856\n"
MF "v[168:171],v[180:183],a[196:199],v[168:171]\n"
"ds_write_addtid_b32 v134 offset:26112\n"
MF "v[168:171],v[184:187],a[236:239],v[168:171]\n"
"s_waitcnt lgkmcnt(15)\n"
"ds_write_addtid_b32 v135 offset:26368\n"
MF "v[140:143],v[188:191],a[140:143],v[140:143]\n"
MF "v[140:143],a[240:243],a[180:183],v[140:143]\n"
MF "v[140:143],a[244:247],a[220:223],v[140:143]\n"
MF "v[148:151],a[244:247],a[224:227],v[148:151]\n"
MF "v[148:151],a[240:243],a[184:187],v[148:151]\n"
"s_waitcnt lgkmcnt(0)\n"
"s_barrier\n"
MF "v[148:151],v[188:191],a[144:147],v[148:151]\n"
"ds_read2st64_b32 v[56:57],v13 offset0:0 offset1:1\n"
MF "v[156:159],v[188:191],a[148:151],v[156:159]\n"
"ds_read2st64_b32 v[58:59],v13 offset0:2 offset1:3\n"
MF "v[156:159],a[240:243],a[188:191],v[156:159]\n"
"ds_read2st64_b32 v[60:61],v13 offset0:4 offset1:5\n"
MF "v[156:159],a[244:247],a[228:231],v[156:159]\n"
"ds_read2st64_b32 v[62:63],v13 offset0:6 offset1:7\n"
MF "v[164:167],a[244:247],a[232:235],v[164:167]\n"
"ds_read2st64_b32 v[64:65],v13 offset0:8 offset1:9\n"
MF "v[164:167],a[240:243],a[192:195],v[164:167]\n"
"ds_read2st64_b32 v[66:67],v13 offset0:10 offset1:11\n"
MF "v[164:167],v[188:191],a[152:155],v[164:167]\n"
"ds_read2st64_b32 v[68:69],v13 offset0:12 offset1:13\n"
MF "v[172:175],v[188:191],a[156:159],v[172:175]\n"
"ds_read2st64_b32 v[70:71],v13 offset0:14 offset1:15\n"
MF "v[172:175],a[240:243],a[196:199],v[172:175]\n"
"ds_read2st64_b32 v[72:73],v13 offset0:16 offset1:17\n"
MF "v[172:175],a[244:247],a[236:239],v[172:175]\n"
"ds_read2st64_b32 v[74:75],v13 offset0:18 offset1:19\n"
"ds_read2st64_b32 v[76:77],v13 offset0:20 offset1:21\n"
"ds_read2st64_b32 v[78:79],v13 offset0:22 offset1:23\n"
"s_nop 0\n"
"ds_read2st64_b32 v[80:81],v13 offset0:24 offset1:25\n"
"ds_read2st64_b32 v[82:83],v13 offset0:26 offset1:27\n"
"ds_read2st64_b32 v[84:85],v13 offset0:28 offset1:29\n"
"ds_read2st64_b32 v[86:87],v13 offset0:30 offset1:31\n"
"ds_read2st64_b32 v[88:89],v13 offset0:32 offset1:33\n"
"ds_read2st64_b32 v[90:91],v13 offset0:34 offset1:35\n"
"ds_read2st64_b32 v[92:93],v13 offset0:36 offset1:37\n"
"ds_read2st64_b32 v[94:95],v13 offset0:38 offset1:39\n"
"s_waitcnt vmcnt(0)\n"
"v_sub_u32 v10,v10,v11\n"
"s_cmp_eq_u32 s16,0\n"
"s_cbranch_scc1 .Lhave03\n"
"s_add_u32 s59,s78,6\n"
"s_and_b32 s59,s59,15\n"
"s_nop 3\n"
".Lnf2:\n"
"v_readlane_b32 s58,v10,s59\n"
"s_cmp_lt_u32 s58,9\n"
"s_cbranch_scc1 .Lsp2\n"
".Lnb2:\n"
"s_bfe_u32 s62,s22,0x30014\n"
"s_add_u32 s62,s62,3\n"
"s_and_b32 s62,s62,7\n"
"s_mul_i32 s62,s62,204800\n"
"s_add_u32 s62,s62,s73\n"
"s_add_u32 s62,s62,133120\n"
LD "v[16:19],v9,s[40:43],s62 offen sc1\n"
LD "v[24:27],v9,s[40:43],s62 offen offset:1024 sc1\n"
LD "v[32:35],v9,s[40:43],s62 offen offset:2048 sc1\n"
LD "v[40:43],v9,s[40:43],s62 offen offset:3072 sc1\n"
"s_add_u32 s62,s62,4096\n"
LD "v[48:51],v9,s[40:43],s62 offen sc1\n"
".Lhave03:\n"
"s_cmp_eq_u32 s17,0\n"
"s_cbranch_scc1 .Lhave02\n"
"s_add_u32 s59,s78,4\n"
"s_and_b32 s59,s59,15\n"
"s_nop 3\n"
".Lnf3:\n"
"v_readlane_b32 s58,v10,s59\n"
"s_cmp_lt_u32 s58,10\n"
"s_cbranch_scc1 .Lsp3\n"
".Lnb3:\n"
"s_bfe_u32 s62,s22,0x30014\n"
"s_add_u32 s62,s62,2\n"
"s_and_b32 s62,s62,7\n"
"s_mul_i32 s62,s62,204800\n"
"s_add_u32 s62,s62,s73\n"
"s_add_u32 s62,s62,153600\n"
LD "v[20:23],v9,s[40:43],s62 offen sc1\n"
LD "v[28:31],v9,s[40:43],s62 offen offset:1024 sc1\n"
LD "v[36:39],v9,s[40:43],s62 offen offset:2048 sc1\n"
LD "v[44:47],v9,s[40:43],s62 offen offset:3072 sc1\n"
"s_add_u32 s62,s62,4096\n"
LD "v[52:55],v9,s[40:43],s62 offen sc1\n"
".Lhave02:\n"
".Lnf4:\n"
"v_readlane_b32 s58,v10,s72\n"
"s_cmp_lt_u32 s58,11\n"
"s_cbranch_scc1 .Lsp4\n"
".Lnb4:\n"
"s_bfe_u32 s62,s22,0x30014\n"
"s_add_u32 s62,s62,1\n"
"s_and_b32 s62,s62,7\n"
"s_mul_i32 s62,s62,204800\n"
"s_add_u32 s62,s62,s73\n"
"s_add_u32 s62,s62,174080\n"
LD "v[208:211],v9,s[40:43],s62 offen sc1\n"
LD "v[212:215],v9,s[40:43],s62 offen offset:1024 sc1\n"
LD "v[216:219],v9,s[40:43],s62 offen offset:2048 sc1\n"
LD "v[220:223],v9,s[40:43],s62 offen offset:3072 sc1\n"
"s_add_u32 s62,s62,4096\n"
LD "v[224:227],v9,s[40:43],s62 offen sc1\n"
"s_waitcnt lgkmcnt(15)\n"
"v_pk_add_f32 v[136:137],v[136:137],v[56:57]\n"
"v_pk_add_f32 v[138:139],v[138:139],v[58:59]\n"
"v_pk_add_f32 v[140:141],v[140:141],v[60:61]\n"
"v_pk_add_f32 v[142:143],v[142:143],v[62:63]\n"
"v_pk_add_f32 v[144:145],v[144:145],v[64:65]\n"
"s_waitcnt lgkmcnt(14)\n"
"v_pk_add_f32 v[146:147],v[146:147],v[66:67]\n"
"s_waitcnt lgkmcnt(13)\n"
"v_pk_add_f32 v[148:149],v[148:149],v[68:69]\n"
"s_waitcnt lgkmcnt(12)\n"
"v_pk_add_f32 v[150:151],v[150:151],v[70:71]\n"
"s_waitcnt lgkmcnt(11)\n"
"v_pk_add_f32 v[152:153],v[152:153],v[72:73]\n"
"s_waitcnt lgkmcnt(10)\n"
"v_pk_add_f32 v[154:155],v[154:155],v[74:75]\n"
"s_waitcnt lgkmcnt(9)\n"
"v_pk_add_f32 v[156:157],v[156:157],v[76:77]\n"
"s_waitcnt lgkmcnt(8)\n"
"v_pk_add_f32 v[158:159],v[158:159],v[78:79]\n"
"s_waitcnt lgkmcnt(7)\n"
"v_pk_add_f32 v[160:161],v[160:161],v[80:81]\n"
"s_waitcnt lgkmcnt(6)\n"
"v_pk_add_f32 v[162:163],v[162:163],v[82:83]\n"
"s_waitcnt lgkmcnt(5)\n"
"v_pk_add_f32 v[164:165],v[164:165],v[84:85]\n"
"s_waitcnt lgkmcnt(4)\n"
"v_pk_add_f32 v[166:167],v[166:167],v[86:87]\n"
"s_waitcnt lgkmcnt(3)\n"
"v_pk_add_f32 v[168:169],v[168:169],v[88:89]\n"
"s_waitcnt lgkmcnt(2)\n"
"v_pk_add_f32 v[170:171],v[170:171],v[90:91]\n"
"s_waitcnt lgkmcnt(1)\n"
"v_pk_add_f32 v[172:173],v[172:173],v[92:93]\n"
"s_waitcnt lgkmcnt(0)\n"
"v_pk_add_f32 v[174:175],v[174:175],v[94:95]\n"
"s_waitcnt vmcnt(5)\n"
"s_nop 0\n"
MF "v[140:143],v[16:19],a[252:255],v[140:143]\n"
MF "v[148:151],v[24:27],a[252:255],v[148:151]\n"
MF "v[156:159],v[32:35],a[252:255],v[156:159]\n"
MF "v[164:167],v[40:43],a[252:255],v[164:167]\n"
MF "v[172:175],v[48:51],a[252:255],v[172:175]\n"
MF "v[136:139],v[16:19],a[248:251],v[136:139]\n"
MF "v[144:147],v[24:27],a[248:251],v[144:147]\n"
MF "v[152:155],v[32:35],a[248:251],v[152:155]\n"
MF "v[160:163],v[40:43],a[248:251],v[160:163]\n"
MF "v[168:171],v[48:51],a[248:251],v[168:171]\n"
MF "v[136:139],v[20:23],a[252:255],v[136:139]\n"
MF "v[144:147],v[28:31],a[252:255],v[144:147]\n"
MF "v[152:155],v[36:39],a[252:255],v[152:155]\n"
MF "v[160:163],v[44:47],a[252:255],v[160:163]\n"
MF "v[168:171],v[52:55],a[252:255],v[168:171]\n"
MF "v[140:143],v[20:23],a[248:251],v[140:143]\n"
MF "v[148:151],v[28:31],a[248:251],v[148:151]\n"
MF "v[156:159],v[36:39],a[248:251],v[156:159]\n"
MF "v[164:167],v[44:47],a[248:251],v[164:167]\n"
MF "v[172:175],v[52:55],a[248:251],v[172:175]\n"
"s_waitcnt vmcnt(0)\n"
MF "v[140:143],v[208:211],a[252:255],v[140:143]\n"
MF "v[136:139],v[208:211],a[248:251],v[136:139]\n"
MF "v[148:151],v[212:215],a[252:255],v[148:151]\n"
MF "v[144:147],v[212:215],a[248:251],v[144:147]\n"
MF "v[156:159],v[216:219],a[252:255],v[156:159]\n"
"s_nop 4\n"
CV "v136,v136,v137\n"
CV "v137,v138,v139\n"
MF "v[152:155],v[216:219],a[248:251],v[152:155]\n"
CV "v138,v140,v141\n"
CV "v139,v142,v143\n"
MF "v[164:167],v[220:223],a[252:255],v[164:167]\n"
CV "v144,v144,v145\n"
CV "v145,v146,v147\n"
MF "v[160:163],v[220:223],a[248:251],v[160:163]\n"
CV "v146,v148,v149\n"
CV "v147,v150,v151\n"
"v_permlane16_swap_b32 v136,v138\n"
"v_permlane16_swap_b32 v137,v139\n"
"buffer_store_dwordx4 v[136:139],v242,s[44:47],s87 offen offset:64 sc1\n"
"s_add_u32 s87,s87,40960\n"
MF "v[172:175],v[224:227],a[252:255],v[172:175]\n"
CV "v152,v152,v153\n"
CV "v153,v154,v155\n"
MF "v[168:171],v[224:227],a[248:251],v[168:171]\n"
CV "v154,v156,v157\n"
CV "v155,v158,v159\n"
"v_permlane16_swap_b32 v144,v146\n"
"v_permlane16_swap_b32 v145,v147\n"
"buffer_store_dwordx4 v[144:147],v242,s[44:47],s87 offen offset:64 sc1\n"
"s_add_u32 s87,s87,40960\n"
CV "v160,v160,v161\n"
CV "v161,v162,v163\n"
CV "v162,v164,v165\n"
CV "v163,v166,v167\n"
"v_permlane16_swap_b32 v152,v154\n"
"v_permlane16_swap_b32 v153,v155\n"
"buffer_store_dwordx4 v[152:155],v242,s[44:47],s87 offen offset:64 sc1\n"
"s_add_u32 s87,s87,40960\n"
CV "v168,v168,v169\n"
CV "v169,v170,v171\n"
CV "v170,v172,v173\n"
CV "v171,v174,v175\n"
"v_permlane16_swap_b32 v160,v162\n"
"v_permlane16_swap_b32 v161,v163\n"
"buffer_store_dwordx4 v[160:163],v242,s[44:47],s87 offen offset:64 sc1\n"
"s_add_u32 s87,s87,40960\n"
"v_permlane16_swap_b32 v168,v170\n"
"v_permlane16_swap_b32 v169,v171\n"
"buffer_store_dwordx4 v[168:171],v242,s[44:47],s87 offen offset:64 sc1\n"
"s_add_u32 s87,s87,40960\n"
"s_branch .Ljobs\n"
".Lrole1:\n"
"s_nop 7\n"
".Lpl1:\n"
MF "v[16:19],a[0:3],v[176:179],v[252:255]\n"
TR "v[188:189],v1 offset:24576\n"
MF "v[16:19],a[40:43],v[180:183],v[16:19]\n"
"s_add_u32 s82,s82,s71\n"
TR "v[190:191],v1 offset:26624\n"
MF "v[16:19],a[80:83],v[184:187],v[16:19]\n"
LD "v[224:227],v9,s[40:43],s82 offen sc1\n"
MF "v[24:27],a[84:87],v[184:187],v[252:255]\n"
TR "a[240:241],v1 offset:28672\n"
MF "v[24:27],a[44:47],v[180:183],v[24:27]\n"
TR "a[242:243],v1 offset:30720\n"
MF "v[24:27],a[4:7],v[176:179],v[24:27]\n"
MF "v[32:35],a[8:11],v[176:179],v[252:255]\n"
TR "a[244:245],v1 offset:32768\n"
MF "v[32:35],a[48:51],v[180:183],v[32:35]\n"
TR "a[246:247],v1 offset:34816\n"
MF "v[32:35],a[88:91],v[184:187],v[32:35]\n"
MF "v[40:43],a[92:95],v[184:187],v[252:255]\n"
MF "v[40:43],a[52:55],v[180:183],v[40:43]\n"
"s_mul_i32 s95,s92,3277\n"
"s_lshr_b32 s95,s95,16\n"
"s_mul_i32 s59,s95,20\n"
MF "v[40:43],a[12:15],v[176:179],v[40:43]\n"
"s_sub_u32 s59,s92,s59\n"
"s_cmp_ge_u32 s59,10\n"
"s_cselect_b32 s96,4294557760,0\n"
MF "v[48:51],a[16:19],v[176:179],v[252:255]\n"
"s_add_u32 s82,s82,s66\n"
"s_add_u32 s96,s96,1024\n"
"s_mul_i32 s59,s59,40960\n"
MF "v[48:51],a[56:59],v[180:183],v[48:51]\n"
LD "v[228:231],v9,s[40:43],s82 offen sc1\n"
MF "v[48:51],a[96:99],v[184:187],v[48:51]\n"
"s_add_u32 s96,s96,s59\n"
"s_lshl_b32 s59,s95,7\n"
"s_waitcnt lgkmcnt(0)\n"
MF "v[52:55],a[96:99],a[244:247],v[252:255]\n"
"s_add_u32 s96,s96,s59\n"
"s_add_u32 s95,s95,3\n"
TR "v[176:177],v2 offset:24576\n"
MF "v[52:55],a[56:59],a[240:243],v[52:55]\n"
"s_cmp_ge_u32 s92,40\n"
"s_cselect_b32 s95,99,s95\n"
TR "v[178:179],v2 offset:26624\n"
MF "v[52:55],a[16:19],v[188:191],v[52:55]\n"
"s_cmp_ge_u32 s85,5\n"
"s_cselect_b32 s95,99,s95\n"
"s_cselect_b32 s72,s85,0\n"
MF "v[44:47],a[12:15],v[188:191],v[252:255]\n"
TR "v[180:181],v2 offset:28672\n"
MF "v[44:47],a[52:55],a[240:243],v[44:47]\n"
TR "v[182:183],v2 offset:30720\n"
MF "v[44:47],a[92:95],a[244:247],v[44:47]\n"
"s_cmp_ge_u32 s85,2\n"
"s_cselect_b32 s74,0xf0000000,s74\n"
MF "v[36:39],a[88:91],a[244:247],v[252:255]\n"
TR "v[184:185],v2 offset:32768\n"
MF "v[36:39],a[48:51],a[240:243],v[36:39]\n"
"s_add_u32 s82,s82,s71\n"
TR "v[186:187],v2 offset:34816\n"
MF "v[36:39],a[8:11],v[188:191],v[36:39]\n"
LD "v[232:235],v9,s[40:43],s82 offen sc1\n"
MF "v[28:31],a[4:7],v[188:191],v[252:255]\n"
MF "v[28:31],a[44:47],a[240:243],v[28:31]\n"
MF "v[28:31],a[84:87],a[244:247],v[28:31]\n"
MF "v[20:23],a[80:83],a[244:247],v[252:255]\n"
MF "v[20:23],a[40:43],a[240:243],v[20:23]\n"
MF "v[20:23],a[0:3],v[188:191],v[20:23]\n"
"s_waitcnt lgkmcnt(0)\n"
MF "v[56:59],a[0:3],v[176:179],v[252:255]\n"
TR "v[188:189],v3 offset:24576\n"
MF "v[56:59],a[40:43],v[180:183],v[56:59]\n"
"s_add_u32 s82,s82,s71\n"
TR "v[190:191],v3 offset:26624\n"
MF "v[56:59],a[80:83],v[184:187],v[56:59]\n"
LD "v[236:239],v9,s[40:43],s82 offen sc1\n"
MF "v[64:67],a[84:87],v[184:187],v[252:255]\n"
TR "a[240:241],v3 offset:28672\n"
MF "v[64:67],a[44:47],v[180:183],v[64:67]\n"
TR "a[242:243],v3 offset:30720\n"
MF "v[64:67],a[4:7],v[176:179],v[64:67]\n"
MF "v[72:75],a[8:11],v[176:179],v[252:255]\n"
TR "a[244:245],v3 offset:32768\n"
MF "v[72:75],a[48:51],v[180:183],v[72:75]\n"
TR "a[246:247],v3 offset:34816\n"
MF "v[72:75],a[88:91],v[184:187],v[72:75]\n"
MF "v[80:83],a[92:95],v[184:187],v[252:255]\n"
MF "v[80:83],a[52:55],v[180:183],v[80:83]\n"
MF "v[80:83],a[12:15],v[176:179],v[80:83]\n"
MF "v[88:91],a[16:19],v[176:179],v[252:255]\n"
MF "v[88:91],a[56:59],v[180:183],v[88:91]\n"
MF "v[88:91],a[96:99],v[184:187],v[88:91]\n"
"s_waitcnt lgkmcnt(0)\n"
"s_barrier\n"
MF "v[92:95],a[96:99],a[244:247],v[252:255]\n"
TR "v[176:177],v0\n"
MF "v[92:95],a[56:59],a[240:243],v[92:95]\n"
TR "v[178:179],v0 offset:2048\n"
MF "v[92:95],a[16:19],v[188:191],v[92:95]\n"
"ds_read2st64_b32 v[192:193],v13 offset0:192 offset1:193\n"
MF "v[84:87],a[12:15],v[188:191],v[252:255]\n"
TR "v[180:181],v0 offset:4096\n"
MF "v[84:87],a[52:55],a[240:243],v[84:87]\n"
TR "v[182:183],v0 offset:6144\n"
MF "v[84:87],a[92:95],a[244:247],v[84:87]\n"
"ds_read2st64_b32 v[194:195],v13 offset0:194 offset1:195\n"
MF "v[76:79],a[88:91],a[244:247],v[252:255]\n"
TR "v[184:185],v0 offset:8192\n"
MF "v[76:79],a[48:51],a[240:243],v[76:79]\n"
TR "v[186:187],v0 offset:10240\n"
MF "v[76:79],a[8:11],v[188:191],v[76:79]\n"
"ds_read2st64_b32 v[196:197],v13 offset0:196 offset1:197\n"
MF "v[68:71],a[4:7],v[188:191],v[252:255]\n"
"ds_read2st64_b32 v[198:199],v13 offset0:198 offset1:199\n"
MF "v[68:71],a[44:47],a[240:243],v[68:71]\n"
"buffer_load_dword v10,v12,s[40:43],s86 offen sc1\n"
MF "v[68:71],a[84:87],a[244:247],v[68:71]\n"
MF "v[60:63],a[80:83],a[244:247],v[252:255]\n"
MF "v[60:63],a[40:43],a[240:243],v[60:63]\n"
MF "v[60:63],a[0:3],v[188:191],v[60:63]\n"
"s_waitcnt lgkmcnt(2)\n"
MF "v[16:19],a[120:123],v[176:179],v[16:19]\n"
"s_waitcnt lgkmcnt(0)\n"
TR "v[188:189],v1\n"
"v_add_f32 v96,v96,v192\n"
MF "v[16:19],a[160:163],v[180:183],v[16:19]\n"
TR "v[190:191],v1 offset:2048\n"
"v_add_f32 v97,v97,v193\n"
MF "v[16:19],a[200:203],v[184:187],v[16:19]\n"
"ds_read2st64_b32 v[200:201],v13 offset0:200 offset1:201\n"
"v_add_f32 v98,v98,v194\n"
"s_waitcnt vmcnt(9)\n"
"v_mov_b32 v14,s76\n"
"s_add_u32 s76,s76,1\n"
MF "v[24:27],a[204:207],v[184:187],v[24:27]\n"
TR "a[240:241],v1 offset:4096\n"
"v_add_f32 v99,v99,v195\n"
"buffer_store_dword v14,v15,s[40:43],s94 offen sc1\n"
MF "v[24:27],a[164:167],v[180:183],v[24:27]\n"
TR "a[242:243],v1 offset:6144\n"
"v_add_f32 v100,v100,v196\n"
MF "v[24:27],a[124:127],v[176:179],v[24:27]\n"
"ds_read2st64_b32 v[202:203],v13 offset0:202 offset1:203\n"
"v_add_f32 v101,v101,v197\n"
MF "v[32:35],a[128:131],v[176:179],v[32:35]\n"
TR "a[244:245],v1 offset:8192\n"
"v_add_f32 v102,v102,v198\n"
MF "v[32:35],a[168:171],v[180:183],v[32:35]\n"
TR "a[246:247],v1 offset:10240\n"
"v_add_f32 v103,v103,v199\n"
MF "v[32:35],a[208:211],v[184:187],v[32:35]\n"
"ds_read2st64_b32 v[204:205],v13 offset0:204 offset1:205\n"
CV "v96,v96,v97\n"
MF "v[40:43],a[212:215],v[184:187],v[40:43]\n"
"ds_read2st64_b32 v[206:207],v13 offset0:206 offset1:207\n"
CV "v97,v98,v99\n"
MF "v[40:43],a[172:175],v[180:183],v[40:43]\n"
"s_add_u32 s87,s90,0\n"
CV "v98,v100,v101\n"
CV "v99,v102,v103\n"
MF "v[40:43],a[132:135],v[176:179],v[40:43]\n"
MF "v[48:51],a[136:139],v[176:179],v[48:51]\n"
"buffer_store_dwordx4 v[96:99],v9,s[40:43],s87 offen sc1\n"
MF "v[48:51],a[176:179],v[180:183],v[48:51]\n"
MF "v[48:51],a[216:219],v[184:187],v[48:51]\n"
"s_waitcnt lgkmcnt(2)\n"
MF "v[52:55],a[216:219],a[244:247],v[52:55]\n"
"s_waitcnt lgkmcnt(0)\n"
TR "v[176:177],v2\n"
"v_add_f32 v104,v104,v200\n"
MF "v[52:55],a[176:179],a[240:243],v[52:55]\n"
TR "v[178:179],v2 offset:2048\n"
"v_add_f32 v105,v105,v201\n"
MF "v[52:55],a[136:139],v[188:191],v[52:55]\n"
"ds_read2st64_b32 v[192:193],v13 offset0:208 offset1:209\n"
"v_add_f32 v106,v106,v202\n"
MF "v[44:47],a[132:135],v[188:191],v[44:47]\n"
TR "v[180:181],v2 offset:4096\n"
"v_add_f32 v107,v107,v203\n"
MF "v[44:47],a[172:175],a[240:243],v[44:47]\n"
TR "v[182:183],v2 offset:6144\n"
"v_add_f32 v108,v108,v204\n"
MF "v[44:47],a[212:215],a[244:247],v[44:47]\n"
"ds_read2st64_b32 v[194:195],v13 offset0:210 offset1:211\n"
"v_add_f32 v109,v109,v205\n"
MF "v[36:39],a[208:211],a[244:247],v[36:39]\n"
TR "v[184:185],v2 offset:8192\n"
"v_add_f32 v110,v110,v206\n"
MF "v[36:39],a[168:171],a[240:243],v[36:39]\n"
TR "v[186:187],v2 offset:10240\n"
"v_add_f32 v111,v111,v207\n"
MF "v[36:39],a[128:131],v[188:191],v[36:39]\n"
"ds_read2st64_b32 v[196:197],v13 offset0:212 offset1:213\n"
CV "v104,v104,v105\n"
MF "v[28:31],a[124:127],v[188:191],v[28:31]\n"
"ds_read2st64_b32 v[198:199],v13 offset0:214 offset1:215\n"
CV "v105,v106,v107\n"
MF "v[28:31],a[164:167],a[240:243],v[28:31]\n"
"s_add_u32 s87,s90,1024\n"
CV "v106,v108,v109\n"
CV "v107,v110,v111\n"
MF "v[28:31],a[204:207],a[244:247],v[28:31]\n"
MF "v[20:23],a[200:203],a[244:247],v[20:23]\n"
"buffer_store_dwordx4 v[104:107],v9,s[40:43],s87 offen sc1\n"
MF "v[20:23],a[160:163],a[240:243],v[20:23]\n"
MF "v[20:23],a[120:123],v[188:191],v[20:23]\n"
"s_waitcnt lgkmcnt(2)\n"
MF "v[56:59],a[120:123],v[176:179],v[56:59]\n"
"s_waitcnt lgkmcnt(0)\n"
TR "v[188:189],v3\n"
"v_add_f32 v112,v112,v192\n"
MF "v[56:59],a[160:163],v[180:183],v[56:59]\n"
TR "v[190:191],v3 offset:2048\n"
"v_add_f32 v113,v113,v193\n"
MF "v[56:59],a[200:203],v[184:187],v[56:59]\n"
"ds_read2st64_b32 v[200:201],v13 offset0:216 offset1:217\n"
"v_add_f32 v114,v114,v194\n"
MF "v[64:67],a[204:207],v[184:187],v[64:67]\n"
TR "a[240:241],v3 offset:4096\n"
"v_add_f32 v115,v115,v195\n"
MF "v[64:67],a[164:167],v[180:183],v[64:67]\n"
TR "a[242:243],v3 offset:6144\n"
"v_add_f32 v116,v116,v196\n"
MF "v[64:67],a[124:127],v[176:179],v[64:67]\n"
"ds_read2st64_b32 v[202:203],v13 offset0:218 offset1:219\n"
"v_add_f32 v117,v117,v197\n"
MF "v[72:75],a[128:131],v[176:179],v[72:75]\n"
TR "a[244:245],v3 offset:8192\n"
"v_add_f32 v118,v118,v198\n"
MF "v[72:75],a[168:171],v[180:183],v[72:75]\n"
TR "a[246:247],v3 offset:10240\n"
"v_add_f32 v119,v119,v199\n"
MF "v[72:75],a[208:211],v[184:187],v[72:75]\n"
"ds_read2st64_b32 v[204:205],v13 offset0:220 offset1:221\n"
CV "v112,v112,v113\n"
MF "v[80:83],a[212:215],v[184:187],v[80:83]\n"
"ds_read2st64_b32 v[206:207],v13 offset0:222 offset1:223\n"
CV "v113,v114,v115\n"
MF "v[80:83],a[172:175],v[180:183],v[80:83]\n"
"s_add_u32 s87,s90,2048\n"
CV "v114,v116,v117\n"
CV "v115,v118,v119\n"
MF "v[80:83],a[132:135],v[176:179],v[80:83]\n"
MF "v[88:91],a[136:139],v[176:179],v[88:91]\n"
"buffer_store_dwordx4 v[112:115],v9,s[40:43],s87 offen sc1\n"
MF "v[88:91],a[176:179],v[180:183],v[88:91]\n"
MF "v[88:91],a[216:219],v[184:187],v[88:91]\n"
"s_waitcnt lgkmcnt(0)\n"
"s_barrier\n"
MF "v[92:95],a[216:219],a[244:247],v[92:95]\n"
TR "v[176:177],v0 offset:49152\n"
"v_add_f32 v120,v120,v200\n"
MF "v[92:95],a[176:179],a[240:243],v[92:95]\n"
TR "v[178:179],v0 offset:51200\n"
"v_add_f32 v121,v121,v201\n"
MF "v[92:95],a[136:139],v[188:191],v[92:95]\n"
"ds_read2st64_b32 v[192:193],v13 offset0:224 offset1:225\n"
"v_add_f32 v122,v122,v202\n"
MF "v[84:87],a[132:135],v[188:191],v[84:87]\n"
TR "v[180:181],v0 offset:53248\n"
"v_add_f32 v123,v123,v203\n"
MF "v[84:87],a[172:175],a[240:243],v[84:87]\n"
TR "v[182:183],v0 offset:55296\n"
"v_add_f32 v124,v124,v204\n"
MF "v[84:87],a[212:215],a[244:247],v[84:87]\n"
"ds_read2st64_b32 v[194:195],v13 offset0:226 offset1:227\n"
"v_add_f32 v125,v125,v205\n"
MF "v[76:79],a[208:211],a[244:247],v[76:79]\n"
TR "v[184:185],v0 offset:57344\n"
"v_add_f32 v126,v126,v206\n"
MF "v[76:79],a[168:171],a[240:243],v[76:79]\n"
TR "v[186:187],v0 offset:59392\n"
"v_add_f32 v127,v127,v207\n"
MF "v[76:79],a[128:131],v[188:191],v[76:79]\n"
"ds_read2st64_b32 v[196:197],v13 offset0:228 offset1:229\n"
CV "v120,v120,v121\n"
MF "v[68:71],a[124:127],v[188:191],v[68:71]\n"
"ds_read2st64_b32 v[198:199],v13 offset0:230 offset1:231\n"
CV "v121,v122,v123\n"
MF "v[68:71],a[164:167],a[240:243],v[68:71]\n"
"s_add_u32 s87,s90,3072\n"
CV "v122,v124,v125\n"
CV "v123,v126,v127\n"
MF "v[68:71],a[204:207],a[244:247],v[68:71]\n"
MF "v[60:63],a[200:203],a[244:247],v[60:63]\n"
"buffer_store_dwordx4 v[120:123],v9,s[40:43],s87 offen sc1\n"
MF "v[60:63],a[160:163],a[240:243],v[60:63]\n"
MF "v[60:63],a[120:123],v[188:191],v[60:63]\n"
"s_waitcnt lgkmcnt(2)\n"
MF "v[16:19],a[20:23],v[176:179],v[16:19]\n"
"s_waitcnt lgkmcnt(0)\n"
TR "v[188:189],v1 offset:49152\n"
"v_add_f32 v128,v128,v192\n"
MF "v[16:19],a[60:63],v[180:183],v[16:19]\n"
TR "v[190:191],v1 offset:51200\n"
"v_add_f32 v129,v129,v193\n"
MF "v[16:19],a[100:103],v[184:187],v[16:19]\n"
"ds_read2st64_b32 v[200:201],v13 offset0:0 offset1:1\n"
"v_add_f32 v130,v130,v194\n"
MF "v[24:27],a[104:107],v[184:187],v[24:27]\n"
TR "a[240:241],v1 offset:53248\n"
"v_add_f32 v131,v131,v195\n"
MF "v[24:27],a[64:67],v[180:183],v[24:27]\n"
TR "a[242:243],v1 offset:55296\n"
"v_add_f32 v132,v132,v196\n"
MF "v[24:27],a[24:27],v[176:179],v[24:27]\n"
"ds_read2st64_b32 v[202:203],v13 offset0:2 offset1:3\n"
"v_add_f32 v133,v133,v197\n"
MF "v[32:35],a[28:31],v[176:179],v[32:35]\n"
TR "a[244:245],v1 offset:57344\n"
"v_add_f32 v134,v134,v198\n"
MF "v[32:35],a[68:71],v[180:183],v[32:35]\n"
TR "a[246:247],v1 offset:59392\n"
"v_add_f32 v135,v135,v199\n"
MF "v[32:35],a[108:111],v[184:187],v[32:35]\n"
"ds_read2st64_b32 v[204:205],v13 offset0:4 offset1:5\n"
CV "v128,v128,v129\n"
MF "v[40:43],a[112:115],v[184:187],v[40:43]\n"
"ds_read2st64_b32 v[206:207],v13 offset0:6 offset1:7\n"
CV "v129,v130,v131\n"
MF "v[40:43],a[72:75],v[180:183],v[40:43]\n"
"s_add_u32 s87,s90,4096\n"
CV "v130,v132,v133\n"
CV "v131,v134,v135\n"
MF "v[40:43],a[32:35],v[176:179],v[40:43]\n"
MF "v[48:51],a[36:39],v[176:179],v[48:51]\n"
"buffer_store_dwordx4 v[128:131],v9,s[40:43],s87 offen sc1\n"
MF "v[48:51],a[76:79],v[180:183],v[48:51]\n"
MF "v[48:51],a[116:119],v[184:187],v[48:51]\n"
"s_waitcnt lgkmcnt(2)\n"
MF "v[52:55],a[116:119],a[244:247],v[52:55]\n"
"s_waitcnt lgkmcnt(0)\n"
TR "v[176:177],v2 offset:49152\n"
"v_add_f32 v136,v136,v200\n"
MF "v[52:55],a[76:79],a[240:243],v[52:55]\n"
TR "v[178:179],v2 offset:51200\n"
"v_add_f32 v137,v137,v201\n"
MF "v[52:55],a[36:39],v[188:191],v[52:55]\n"
"ds_read2st64_b32 v[192:193],v13 offset0:8 offset1:9\n"
"v_add_f32 v138,v138,v202\n"
MF "v[44:47],a[32:35],v[188:191],v[44:47]\n"
TR "v[180:181],v2 offset:53248\n"
"v_add_f32 v139,v139,v203\n"
MF "v[44:47],a[72:75],a[240:243],v[44:47]\n"
TR "v[182:183],v2 offset:55296\n"
"v_add_f32 v140,v140,v204\n"
MF "v[44:47],a[112:115],a[244:247],v[44:47]\n"
"ds_read2st64_b32 v[194:195],v13 offset0:10 offset1:11\n"
"v_add_f32 v141,v141,v205\n"
MF "v[36:39],a[108:111],a[244:247],v[36:39]\n"
TR "v[184:185],v2 offset:57344\n"
"v_add_f32 v142,v142,v206\n"
MF "v[36:39],a[68:71],a[240:243],v[36:39]\n"
TR "v[186:187],v2 offset:59392\n"
"v_add_f32 v143,v143,v207\n"
MF "v[36:39],a[28:31],v[188:191],v[36:39]\n"
"ds_read2st64_b32 v[196:197],v13 offset0:12 offset1:13\n"
CV "v136,v136,v137\n"
MF "v[28:31],a[24:27],v[188:191],v[28:31]\n"
"ds_read2st64_b32 v[198:199],v13 offset0:14 offset1:15\n"
CV "v137,v138,v139\n"
MF "v[28:31],a[64:67],a[240:243],v[28:31]\n"
"s_add_u32 s87,s90,10240\n"
CV "v138,v140,v141\n"
CV "v139,v142,v143\n"
MF "v[28:31],a[104:107],a[244:247],v[28:31]\n"
MF "v[20:23],a[100:103],a[244:247],v[20:23]\n"
"buffer_store_dwordx4 v[136:139],v9,s[40:43],s87 offen sc1\n"
MF "v[20:23],a[60:63],a[240:243],v[20:23]\n"
"s_waitcnt vmcnt(7)\n"
"s_cmp_eq_u32 s83,0xf0000000\n"
MF "v[20:23],a[20:23],v[188:191],v[20:23]\n"
"s_cbranch_scc1 .Ljs13\n"
"s_cmp_eq_u32 s83,0xf0000010\n"
"s_cbranch_scc1 .Ljc13\n"
MF "v[248:251],v[210:213],a[250:253],0\n"
"v_mov_b32 v240,v208\n"
"v_mov_b32 v241,v209\n"
MF "v[244:247],v[214:217],a[250:253],0\n"
MF "v[248:251],v[218:221],a[250:253],v[248:251]\n"
MF "v[244:247],v[222:225],a[250:253],v[244:247]\n"
MF "v[248:251],v[226:229],a[250:253],v[248:251]\n"
MF "v[244:247],v[230:233],a[250:253],v[244:247]\n"
MF "v[248:251],v[234:237],a[250:253],v[248:251]\n"
MF "v[244:247],v[238:241],a[250:253],v[244:247]\n"
"s_branch .Ljs13\n"
".Ljc13:\n"
MF "v[56:59],a[252:255],v[208:211],v[56:59]\n"
MF "v[64:67],a[252:255],v[212:215],v[64:67]\n"
MF "v[72:75],a[252:255],v[216:219],v[72:75]\n"
MF "v[80:83],a[252:255],v[220:223],v[80:83]\n"
MF "v[88:91],a[252:255],v[224:227],v[88:91]\n"
MF "v[60:63],a[248:251],v[208:211],v[60:63]\n"
MF "v[68:71],a[248:251],v[212:215],v[68:71]\n"
MF "v[76:79],a[248:251],v[216:219],v[76:79]\n"
MF "v[84:87],a[248:251],v[220:223],v[84:87]\n"
MF "v[92:95],a[248:251],v[224:227],v[92:95]\n"
".Ljs13:\n"
"s_waitcnt lgkmcnt(2)\n"
MF "v[56:59],a[20:23],v[176:179],v[56:59]\n"
"s_waitcnt lgkmcnt(0)\n"
TR "v[188:189],v3 offset:49152\n"
"v_add_f32 v144,v144,v192\n"
MF "v[56:59],a[60:63],v[180:183],v[56:59]\n"
TR "v[190:191],v3 offset:51200\n"
"v_add_f32 v145,v145,v193\n"
MF "v[56:59],a[100:103],v[184:187],v[56:59]\n"
"ds_read2st64_b32 v[200:201],v13 offset0:16 offset1:17\n"
"v_add_f32 v146,v146,v194\n"
MF "v[64:67],a[104:107],v[184:187],v[64:67]\n"
TR "a[240:241],v3 offset:53248\n"
"v_add_f32 v147,v147,v195\n"
MF "v[64:67],a[64:67],v[180:183],v[64:67]\n"
TR "a[242:243],v3 offset:55296\n"
"v_add_f32 v148,v148,v196\n"
MF "v[64:67],a[24:27],v[176:179],v[64:67]\n"
"ds_read2st64_b32 v[202:203],v13 offset0:18 offset1:19\n"
"v_add_f32 v149,v149,v197\n"
MF "v[72:75],a[28:31],v[176:179],v[72:75]\n"
TR "a[244:245],v3 offset:57344\n"
"v_add_f32 v150,v150,v198\n"
MF "v[72:75],a[68:71],v[180:183],v[72:75]\n"
TR "a[246:247],v3 offset:59392\n"
"v_add_f32 v151,v151,v199\n"
MF "v[72:75],a[108:111],v[184:187],v[72:75]\n"
"ds_read2st64_b32 v[204:205],v13 offset0:20 offset1:21\n"
CV "v144,v144,v145\n"
MF "v[80:83],a[112:115],v[184:187],v[80:83]\n"
"ds_read2st64_b32 v[206:207],v13 offset0:22 offset1:23\n"
CV "v145,v146,v147\n"
MF "v[80:83],a[72:75],v[180:183],v[80:83]\n"
"s_add_u32 s87,s90,11264\n"
CV "v146,v148,v149\n"
CV "v147,v150,v151\n"
MF "v[80:83],a[32:35],v[176:179],v[80:83]\n"
"s_lshl_b32 s82,s92,10\n"
"v_sub_u32 v10,v10,v11\n"
"v_cmp_gt_u32 vcc,s95,v10\n"
MF "v[88:91],a[36:39],v[176:179],v[88:91]\n"
"buffer_store_dwordx4 v[144:147],v9,s[40:43],s87 offen sc1\n"
MF "v[88:91],a[76:79],v[180:183],v[88:91]\n"
"s_add_u32 s82,s82,s93\n"
CV "v244,v244,v245\n"
CV "v245,v246,v247\n"
MF "v[88:91],a[116:119],v[184:187],v[88:91]\n"
"s_waitcnt lgkmcnt(0)\n"
"s_barrier\n"
MF "v[92:95],a[116:119],a[244:247],v[92:95]\n"
TR "v[176:177],v4\n"
"v_add_f32 v152,v152,v200\n"
MF "v[92:95],a[76:79],a[240:243],v[92:95]\n"
TR "v[178:179],v4 offset:2048\n"
"v_add_f32 v153,v153,v201\n"
MF "v[92:95],a[36:39],v[188:191],v[92:95]\n"
"ds_read2st64_b32 v[192:193],v13 offset0:24 offset1:25\n"
"v_add_f32 v154,v154,v202\n"
MF "v[84:87],a[32:35],v[188:191],v[84:87]\n"
TR "v[180:181],v4 offset:4096\n"
"v_add_f32 v155,v155,v203\n"
MF "v[84:87],a[72:75],a[240:243],v[84:87]\n"
TR "v[182:183],v4 offset:6144\n"
"v_add_f32 v156,v156,v204\n"
MF "v[84:87],a[112:115],a[244:247],v[84:87]\n"
"ds_read2st64_b32 v[194:195],v13 offset0:26 offset1:27\n"
"v_add_f32 v157,v157,v205\n"
MF "v[76:79],a[108:111],a[244:247],v[76:79]\n"
TR "v[184:185],v4 offset:8192\n"
"v_add_f32 v158,v158,v206\n"
MF "v[76:79],a[68:71],a[240:243],v[76:79]\n"
TR "v[186:187],v4 offset:10240\n"
"v_add_f32 v159,v159,v207\n"
MF "v[76:79],a[28:31],v[188:191],v[76:79]\n"
"ds_read2st64_b32 v[196:197],v13 offset0:28 offset1:29\n"
CV "v152,v152,v153\n"
MF "v[68:71],a[24:27],v[188:191],v[68:71]\n"
"ds_read2st64_b32 v[198:199],v13 offset0:30 offset1:31\n"
CV "v153,v154,v155\n"
MF "v[68:71],a[64:67],a[240:243],v[68:71]\n"
"s_add_u32 s87,s90,12288\n"
CV "v154,v156,v157\n"
CV "v155,v158,v159\n"
MF "v[68:71],a[104:107],a[244:247],v[68:71]\n"
"s_cmp_eq_u64 vcc,0\n"
"s_cselect_b32 s82,s82,0xf0000000\n"
"s_cselect_b32 s84,s96,0xf0000000\n"
"s_cselect_b32 s59,32,0\n"
MF "v[60:63],a[100:103],a[244:247],v[60:63]\n"
"buffer_store_dwordx4 v[152:155],v9,s[40:43],s87 offen sc1\n"
MF "v[60:63],a[60:63],a[240:243],v[60:63]\n"
"s_add_u32 s92,s92,s59\n"
".Lnf5:\n"
"v_readlane_b32 s58,v10,s67\n"
"s_cmp_lt_u32 s58,s72\n"
"s_cbranch_scc1 .Lsp5\n"
".Lnb5:\n"
MF "v[60:63],a[20:23],v[188:191],v[60:63]\n"
"s_waitcnt lgkmcnt(2)\n"
CV "v246,v248,v249\n"
CV "v247,v250,v251\n"
MF "v[16:19],a[140:143],v[176:179],v[16:19]\n"
"s_waitcnt lgkmcnt(0)\n"
TR "v[188:189],v5\n"
"v_add_f32 v160,v160,v192\n"
MF "v[16:19],a[180:183],v[180:183],v[16:19]\n"
TR "v[190:191],v5 offset:2048\n"
"v_add_f32 v161,v161,v193\n"
MF "v[16:19],a[220:223],v[184:187],v[16:19]\n"
"ds_read2st64_b32 v[200:201],v13 offset0:32 offset1:33\n"
"v_add_f32 v162,v162,v194\n"
MF "v[24:27],a[224:227],v[184:187],v[24:27]\n"
TR "a[240:241],v5 offset:4096\n"
"v_add_f32 v163,v163,v195\n"
MF "v[24:27],a[184:187],v[180:183],v[24:27]\n"
TR "a[242:243],v5 offset:6144\n"
"v_add_f32 v164,v164,v196\n"
MF "v[24:27],a[144:147],v[176:179],v[24:27]\n"
"ds_read2st64_b32 v[202:203],v13 offset0:34 offset1:35\n"
"v_add_f32 v165,v165,v197\n"
MF "v[32:35],a[148:151],v[176:179],v[32:35]\n"
TR "a[244:245],v5 offset:8192\n"
"v_add_f32 v166,v166,v198\n"
MF "v[32:35],a[188:191],v[180:183],v[32:35]\n"
TR "a[246:247],v5 offset:10240\n"
"v_add_f32 v167,v167,v199\n"
MF "v[32:35],a[228:231],v[184:187],v[32:35]\n"
"ds_read2st64_b32 v[204:205],v13 offset0:36 offset1:37\n"
CV "v160,v160,v161\n"
MF "v[40:43],a[232:235],v[184:187],v[40:43]\n"
"ds_read2st64_b32 v[206:207],v13 offset0:38 offset1:39\n"
CV "v161,v162,v163\n"
MF "v[40:43],a[192:195],v[180:183],v[40:43]\n"
"s_add_u32 s87,s90,13312\n"
CV "v162,v164,v165\n"
CV "v163,v166,v167\n"
MF "v[40:43],a[152:155],v[176:179],v[40:43]\n"
"s_cmp_ge_u32 s85,5\n"
"s_cselect_b32 s82,s70,s82\n"
"s_cselect_b32 s84,0xf0000010,s84\n"
MF "v[48:51],a[156:159],v[176:179],v[48:51]\n"
"buffer_store_dwordx4 v[160:163],v9,s[40:43],s87 offen sc1\n"
MF "v[48:51],a[196:199],v[180:183],v[48:51]\n"
"s_cmp_ge_u32 s85,5\n"
"s_cselect_b32 s71,1024,s71\n"
"s_cselect_b32 s66,0x80000000,s66\n"
MF "v[48:51],a[236:239],v[184:187],v[48:51]\n"
"s_add_u32 s70,s70,20480\n"
"s_waitcnt lgkmcnt(2)\n"
"v_permlane16_swap_b32 v244,v246\n"
MF "v[52:55],a[236:239],a[244:247],v[52:55]\n"
"s_waitcnt lgkmcnt(0)\n"
TR "v[176:177],v6\n"
"v_add_f32 v168,v168,v200\n"
MF "v[52:55],a[196:199],a[240:243],v[52:55]\n"
TR "v[178:179],v6 offset:2048\n"
"v_add_f32 v169,v169,v201\n"
MF "v[52:55],a[156:159],v[188:191],v[52:55]\n"
"v_add_f32 v170,v170,v202\n"
"v_add_f32 v171,v171,v203\n"
MF "v[44:47],a[152:155],v[188:191],v[44:47]\n"
TR "v[180:181],v6 offset:4096\n"
"v_add_f32 v172,v172,v204\n"
MF "v[44:47],a[192:195],a[240:243],v[44:47]\n"
TR "v[182:183],v6 offset:6144\n"
"v_add_f32 v173,v173,v205\n"
MF "v[44:47],a[232:235],a[244:247],v[44:47]\n"
"v_add_f32 v174,v174,v206\n"
"v_add_f32 v175,v175,v207\n"
MF "v[36:39],a[228:231],a[244:247],v[36:39]\n"
TR "v[184:185],v6 offset:8192\n"
CV "v168,v168,v169\n"
MF "v[36:39],a[188:191],a[240:243],v[36:39]\n"
TR "v[186:187],v6 offset:10240\n"
CV "v169,v170,v171\n"
MF "v[36:39],a[148:151],v[188:191],v[36:39]\n"
"s_add_u32 s87,s90,14336\n"
CV "v170,v172,v173\n"
CV "v171,v174,v175\n"
MF "v[28:31],a[144:147],v[188:191],v[28:31]\n"
"v_permlane16_swap_b32 v245,v247\n"
MF "v[28:31],a[184:187],a[240:243],v[28:31]\n"
"buffer_store_dwordx4 v[244:247],v8,s[44:47],s83 offen sc1\n"
MF "v[28:31],a[224:227],a[244:247],v[28:31]\n"
"s_mov_b32 s83,s84\n"
MF "v[20:23],a[220:223],a[244:247],v[20:23]\n"
"buffer_store_dwordx4 v[168:171],v9,s[40:43],s87 offen sc1\n"
MF "v[20:23],a[180:183],a[240:243],v[20:23]\n"
"s_mov_b32 s90,s91\n"
"s_add_u32 s91,s91,20480\n"
MF "v[20:23],a[140:143],v[188:191],v[20:23]\n"
"s_waitcnt lgkmcnt(0)\n"
LD "v[208:211],v9,s[40:43],s82 offen sc1\n"
MF "v[56:59],a[140:143],v[176:179],v[56:59]\n"
TR "v[188:189],v7\n"
MF "v[56:59],a[180:183],v[180:183],v[56:59]\n"
TR "v[190:191],v7 offset:2048\n"
MF "v[56:59],a[220:223],v[184:187],v[56:59]\n"
MF "v[64:67],a[224:227],v[184:187],v[64:67]\n"
TR "a[240:241],v7 offset:4096\n"
MF "v[64:67],a[184:187],v[180:183],v[64:67]\n"
"s_add_u32 s82,s82,s71\n"
TR "a[242:243],v7 offset:6144\n"
MF "v[64:67],a[144:147],v[176:179],v[64:67]\n"
LD "v[212:215],v9,s[40:43],s82 offen sc1\n"
MF "v[72:75],a[148:151],v[176:179],v[72:75]\n"
TR "a[244:245],v7 offset:8192\n"
MF "v[72:75],a[188:191],v[180:183],v[72:75]\n"
"s_add_u32 s62,s74,s68\n"
TR "a[246:247],v7 offset:10240\n"
MF "v[72:75],a[228:231],v[184:187],v[72:75]\n"
"s_add_u32 s68,s68,256\n"
MF "v[80:83],a[232:235],v[184:187],v[80:83]\n"
"s_and_b32 s68,s68,768\n"
MF "v[80:83],a[192:195],v[180:183],v[80:83]\n"
"buffer_load_dword v243,v242,s[48:51],s62 offen\n"
MF "v[80:83],a[152:155],v[176:179],v[80:83]\n"
"s_add_u32 s82,s82,s71\n"
MF "v[88:91],a[156:159],v[176:179],v[88:91]\n"
LD "v[216:219],v9,s[40:43],s82 offen sc1\n"
MF "v[88:91],a[196:199],v[180:183],v[88:91]\n"
MF "v[88:91],a[236:239],v[184:187],v[88:91]\n"
"s_waitcnt lgkmcnt(0)\n"
"s_barrier\n"
MF "v[92:95],a[236:239],a[244:247],v[92:95]\n"
TR "v[176:177],v0 offset:24576\n"
MF "v[92:95],a[196:199],a[240:243],v[92:95]\n"
"s_add_u32 s85,s85,1\n"
TR "v[178:179],v0 offset:26624\n"
MF "v[92:95],a[156:159],v[188:191],v[92:95]\n"
MF "v[84:87],a[152:155],v[188:191],v[84:87]\n"
TR "v[180:181],v0 offset:28672\n"
MF "v[84:87],a[192:195],a[240:243],v[84:87]\n"
"s_add_u32 s82,s82,s71\n"
TR "v[182:183],v0 offset:30720\n"
MF "v[84:87],a[232:235],a[244:247],v[84:87]\n"
LD "v[220:223],v9,s[40:43],s82 offen sc1\n"
MF "v[76:79],a[228:231],a[244:247],v[76:79]\n"
TR "v[184:185],v0 offset:32768\n"
MF "v[76:79],a[188:191],a[240:243],v[76:79]\n"
TR "v[186:187],v0 offset:34816\n"
MF "v[76:79],a[148:151],v[188:191],v[76:79]\n"
MF "v[68:71],a[144:147],v[188:191],v[68:71]\n"
MF "v[68:71],a[184:187],a[240:243],v[68:71]\n"
MF "v[68:71],a[224:227],a[244:247],v[68:71]\n"
MF "v[60:63],a[220:223],a[244:247],v[60:63]\n"
MF "v[60:63],a[180:183],a[240:243],v[60:63]\n"
MF "v[60:63],a[140:143],v[188:191],v[60:63]\n"
"s_waitcnt lgkmcnt(0)\n"
"s_cmp_eq_u32 s85,9\n"
"s_cbranch_scc1 .Lplast1\n"
MF "v[96:99],a[0:3],v[176:179],v[252:255]\n"
TR "v[188:189],v1 offset:24576\n"
MF "v[96:99],a[40:43],v[180:183],v[96:99]\n"
"s_add_u32 s82,s82,s71\n"
TR "v[190:191],v1 offset:26624\n"
MF "v[96:99],a[80:83],v[184:187],v[96:99]\n"
LD "v[224:227],v9,s[40:43],s82 offen sc1\n"
MF "v[104:107],a[84:87],v[184:187],v[252:255]\n"
TR "a[240:241],v1 offset:28672\n"
MF "v[104:107],a[44:47],v[180:183],v[104:107]\n"
TR "a[242:243],v1 offset:30720\n"
MF "v[104:107],a[4:7],v[176:179],v[104:107]\n"
MF "v[112:115],a[8:11],v[176:179],v[252:255]\n"
TR "a[244:245],v1 offset:32768\n"
MF "v[112:115],a[48:51],v[180:183],v[112:115]\n"
TR "a[246:247],v1 offset:34816\n"
MF "v[112:115],a[88:91],v[184:187],v[112:115]\n"
MF "v[120:123],a[92:95],v[184:187],v[252:255]\n"
MF "v[120:123],a[52:55],v[180:183],v[120:123]\n"
"s_mul_i32 s95,s92,3277\n"
"s_lshr_b32 s95,s95,16\n"
"s_mul_i32 s59,s95,20\n"
MF "v[120:123],a[12:15],v[176:179],v[120:123]\n"
"s_sub_u32 s59,s92,s59\n"
"s_cmp_ge_u32 s59,10\n"
"s_cselect_b32 s96,4294557760,0\n"
MF "v[128:131],a[16:19],v[176:179],v[252:255]\n"
"s_add_u32 s82,s82,s66\n"
"s_add_u32 s96,s96,1024\n"
"s_mul_i32 s59,s59,40960\n"
MF "v[128:131],a[56:59],v[180:183],v[128:131]\n"
LD "v[228:231],v9,s[40:43],s82 offen sc1\n"
MF "v[128:131],a[96:99],v[184:187],v[128:131]\n"
"s_add_u32 s96,s96,s59\n"
"s_lshl_b32 s59,s95,7\n"
"s_waitcnt lgkmcnt(0)\n"
MF "v[132:135],a[96:99],a[244:247],v[252:255]\n"
"s_add_u32 s96,s96,s59\n"
"s_add_u32 s95,s95,3\n"
TR "v[176:177],v2 offset:24576\n"
MF "v[132:135],a[56:59],a[240:243],v[132:135]\n"
"s_cmp_ge_u32 s92,40\n"
"s_cselect_b32 s95,99,s95\n"
TR "v[178:179],v2 offset:26624\n"
MF "v[132:135],a[16:19],v[188:191],v[132:135]\n"
"s_cmp_ge_u32 s85,5\n"
"s_cselect_b32 s95,99,s95\n"
"s_cselect_b32 s72,s85,0\n"
MF "v[124:127],a[12:15],v[188:191],v[252:255]\n"
TR "v[180:181],v2 offset:28672\n"
MF "v[124:127],a[52:55],a[240:243],v[124:127]\n"
TR "v[182:183],v2 offset:30720\n"
MF "v[124:127],a[92:95],a[244:247],v[124:127]\n"
MF "v[116:119],a[88:91],a[244:247],v[252:255]\n"
TR "v[184:185],v2 offset:32768\n"
MF "v[116:119],a[48:51],a[240:243],v[116:119]\n"
"s_add_u32 s82,s82,s71\n"
TR "v[186:187],v2 offset:34816\n"
MF "v[116:119],a[8:11],v[188:191],v[116:119]\n"
LD "v[232:235],v9,s[40:43],s82 offen sc1\n"
MF "v[108:111],a[4:7],v[188:191],v[252:255]\n"
MF "v[108:111],a[44:47],a[240:243],v[108:111]\n"
MF "v[108:111],a[84:87],a[244:247],v[108:111]\n"
MF "v[100:103],a[80:83],a[244:247],v[252:255]\n"
MF "v[100:103],a[40:43],a[240:243],v[100:103]\n"
MF "v[100:103],a[0:3],v[188:191],v[100:103]\n"
"s_waitcnt lgkmcnt(0)\n"
MF "v[136:139],a[0:3],v[176:179],v[252:255]\n"
TR "v[188:189],v3 offset:24576\n"
MF "v[136:139],a[40:43],v[180:183],v[136:139]\n"
"s_add_u32 s82,s82,s71\n"
TR "v[190:191],v3 offset:26624\n"
MF "v[136:139],a[80:83],v[184:187],v[136:139]\n"
LD "v[236:239],v9,s[40:43],s82 offen sc1\n"
MF "v[144:147],a[84:87],v[184:187],v[252:255]\n"
TR "a[240:241],v3 offset:28672\n"
MF "v[144:147],a[44:47],v[180:183],v[144:147]\n"
TR "a[242:243],v3 offset:30720\n"
MF "v[144:147],a[4:7],v[176:179],v[144:147]\n"
MF "v[152:155],a[8:11],v[176:179],v[252:255]\n"
TR "a[244:245],v3 offset:32768\n"
MF "v[152:155],a[48:51],v[180:183],v[152:155]\n"
TR "a[246:247],v3 offset:34816\n"
MF "v[152:155],a[88:91],v[184:187],v[152:155]\n"
MF "v[160:163],a[92:95],v[184:187],v[252:255]\n"
MF "v[160:163],a[52:55],v[180:183],v[160:163]\n"
MF "v[160:163],a[12:15],v[176:179],v[160:163]\n"
MF "v[168:171],a[16:19],v[176:179],v[252:255]\n"
MF "v[168:171],a[56:59],v[180:183],v[168:171]\n"
MF "v[168:171],a[96:99],v[184:187],v[168:171]\n"
"s_waitcnt lgkmcnt(0)\n"
"s_barrier\n"
MF "v[172:175],a[96:99],a[244:247],v[252:255]\n"
TR "v[176:177],v0\n"
MF "v[172:175],a[56:59],a[240:243],v[172:175]\n"
TR "v[178:179],v0 offset:2048\n"
MF "v[172:175],a[16:19],v[188:191],v[172:175]\n"
"ds_read2st64_b32 v[192:193],v13 offset0:192 offset1:193\n"
MF "v[164:167],a[12:15],v[188:191],v[252:255]\n"
TR "v[180:181],v0 offset:4096\n"
MF "v[164:167],a[52:55],a[240:243],v[164:167]\n"
TR "v[182:183],v0 offset:6144\n"
MF "v[164:167],a[92:95],a[244:247],v[164:167]\n"
"ds_read2st64_b32 v[194:195],v13 offset0:194 offset1:195\n"
MF "v[156:159],a[88:91],a[244:247],v[252:255]\n"
TR "v[184:185],v0 offset:8192\n"
MF "v[156:159],a[48:51],a[240:243],v[156:159]\n"
TR "v[186:187],v0 offset:10240\n"
MF "v[156:159],a[8:11],v[188:191],v[156:159]\n"
"ds_read2st64_b32 v[196:197],v13 offset0:196 offset1:197\n"
MF "v[148:151],a[4:7],v[188:191],v[252:255]\n"
"ds_read2st64_b32 v[198:199],v13 offset0:198 offset1:199\n"
MF "v[148:151],a[44:47],a[240:243],v[148:151]\n"
"buffer_load_dword v10,v12,s[40:43],s86 offen sc1\n"
MF "v[148:151],a[84:87],a[244:247],v[148:151]\n"
MF "v[140:143],a[80:83],a[244:247],v[252:255]\n"
MF "v[140:143],a[40:43],a[240:243],v[140:143]\n"
MF "v[140:143],a[0:3],v[188:191],v[140:143]\n"
"s_waitcnt lgkmcnt(2)\n"
MF "v[96:99],a[120:123],v[176:179],v[96:99]\n"
"s_waitcnt lgkmcnt(0)\n"
TR "v[188:189],v1\n"
"v_add_f32 v16,v16,v192\n"
MF "v[96:99],a[160:163],v[180:183],v[96:99]\n"
TR "v[190:191],v1 offset:2048\n"
"v_add_f32 v17,v17,v193\n"
MF "v[96:99],a[200:203],v[184:187],v[96:99]\n"
"ds_read2st64_b32 v[200:201],v13 offset0:200 offset1:201\n"
"v_add_f32 v18,v18,v194\n"
"s_waitcnt vmcnt(10)\n"
"v_mov_b32 v14,s76\n"
"s_add_u32 s76,s76,1\n"
MF "v[104:107],a[204:207],v[184:187],v[104:107]\n"
TR "a[240:241],v1 offset:4096\n"
"v_add_f32 v19,v19,v195\n"
"buffer_store_dword v14,v15,s[40:43],s94 offen sc1\n"
MF "v[104:107],a[164:167],v[180:183],v[104:107]\n"
TR "a[242:243],v1 offset:6144\n"
"v_add_f32 v20,v20,v196\n"
MF "v[104:107],a[124:127],v[176:179],v[104:107]\n"
"ds_read2st64_b32 v[202:203],v13 offset0:202 offset1:203\n"
"v_add_f32 v21,v21,v197\n"
MF "v[112:115],a[128:131],v[176:179],v[112:115]\n"
TR "a[244:245],v1 offset:8192\n"
"v_add_f32 v22,v22,v198\n"
MF "v[112:115],a[168:171],v[180:183],v[112:115]\n"
TR "a[246:247],v1 offset:10240\n"
"v_add_f32 v23,v23,v199\n"
MF "v[112:115],a[208:211],v[184:187],v[112:115]\n"
"ds_read2st64_b32 v[204:205],v13 offset0:204 offset1:205\n"
CV "v16,v16,v17\n"
MF "v[120:123],a[212:215],v[184:187],v[120:123]\n"
"ds_read2st64_b32 v[206:207],v13 offset0:206 offset1:207\n"
CV "v17,v18,v19\n"
MF "v[120:123],a[172:175],v[180:183],v[120:123]\n"
"s_add_u32 s87,s90,0\n"
CV "v18,v20,v21\n"
CV "v19,v22,v23\n"
MF "v[120:123],a[132:135],v[176:179],v[120:123]\n"
MF "v[128:131],a[136:139],v[176:179],v[128:131]\n"
"buffer_store_dwordx4 v[16:19],v9,s[40:43],s87 offen sc1\n"
MF "v[128:131],a[176:179],v[180:183],v[128:131]\n"
MF "v[128:131],a[216:219],v[184:187],v[128:131]\n"
"s_waitcnt lgkmcnt(2)\n"
MF "v[132:135],a[216:219],a[244:247],v[132:135]\n"
"s_waitcnt lgkmcnt(0)\n"
TR "v[176:177],v2\n"
"v_add_f32 v24,v24,v200\n"
MF "v[132:135],a[176:179],a[240:243],v[132:135]\n"
TR "v[178:179],v2 offset:2048\n"
"v_add_f32 v25,v25,v201\n"
MF "v[132:135],a[136:139],v[188:191],v[132:135]\n"
"ds_read2st64_b32 v[192:193],v13 offset0:208 offset1:209\n"
"v_add_f32 v26,v26,v202\n"
MF "v[124:127],a[132:135],v[188:191],v[124:127]\n"
TR "v[180:181],v2 offset:4096\n"
"v_add_f32 v27,v27,v203\n"
MF "v[124:127],a[172:175],a[240:243],v[124:127]\n"
TR "v[182:183],v2 offset:6144\n"
"v_add_f32 v28,v28,v204\n"
MF "v[124:127],a[212:215],a[244:247],v[124:127]\n"
"ds_read2st64_b32 v[194:195],v13 offset0:210 offset1:211\n"
"v_add_f32 v29,v29,v205\n"
MF "v[116:119],a[208:211],a[244:247],v[116:119]\n"
TR "v[184:185],v2 offset:8192\n"
"v_add_f32 v30,v30,v206\n"
MF "v[116:119],a[168:171],a[240:243],v[116:119]\n"
TR "v[186:187],v2 offset:10240\n"
"v_add_f32 v31,v31,v207\n"
MF "v[116:119],a[128:131],v[188:191],v[116:119]\n"
"ds_read2st64_b32 v[196:197],v13 offset0:212 offset1:213\n"
CV "v24,v24,v25\n"
MF "v[108:111],a[124:127],v[188:191],v[108:111]\n"
"ds_read2st64_b32 v[198:199],v13 offset0:214 offset1:215\n"
CV "v25,v26,v27\n"
MF "v[108:111],a[164:167],a[240:243],v[108:111]\n"
"s_add_u32 s87,s90,1024\n"
CV "v26,v28,v29\n"
CV "v27,v30,v31\n"
MF "v[108:111],a[204:207],a[244:247],v[108:111]\n"
MF "v[100:103],a[200:203],a[244:247],v[100:103]\n"
"buffer_store_dwordx4 v[24:27],v9,s[40:43],s87 offen sc1\n"
MF "v[100:103],a[160:163],a[240:243],v[100:103]\n"
MF "v[100:103],a[120:123],v[188:191],v[100:103]\n"
"s_waitcnt lgkmcnt(2)\n"
MF "v[136:139],a[120:123],v[176:179],v[136:139]\n"
"s_waitcnt lgkmcnt(0)\n"
TR "v[188:189],v3\n"
"v_add_f32 v32,v32,v192\n"
MF "v[136:139],a[160:163],v[180:183],v[136:139]\n"
TR "v[190:191],v3 offset:2048\n"
"v_add_f32 v33,v33,v193\n"
MF "v[136:139],a[200:203],v[184:187],v[136:139]\n"
"ds_read2st64_b32 v[200:201],v13 offset0:216 offset1:217\n"
"v_add_f32 v34,v34,v194\n"
MF "v[144:147],a[204:207],v[184:187],v[144:147]\n"
TR "a[240:241],v3 offset:4096\n"
"v_add_f32 v35,v35,v195\n"
MF "v[144:147],a[164:167],v[180:183],v[144:147]\n"
TR "a[242:243],v3 offset:6144\n"
"v_add_f32 v36,v36,v196\n"
MF "v[144:147],a[124:127],v[176:179],v[144:147]\n"
"ds_read2st64_b32 v[202:203],v13 offset0:218 offset1:219\n"
"v_add_f32 v37,v37,v197\n"
MF "v[152:155],a[128:131],v[176:179],v[152:155]\n"
TR "a[244:245],v3 offset:8192\n"
"v_add_f32 v38,v38,v198\n"
MF "v[152:155],a[168:171],v[180:183],v[152:155]\n"
TR "a[246:247],v3 offset:10240\n"
"v_add_f32 v39,v39,v199\n"
MF "v[152:155],a[208:211],v[184:187],v[152:155]\n"
"ds_read2st64_b32 v[204:205],v13 offset0:220 offset1:221\n"
CV "v32,v32,v33\n"
MF "v[160:163],a[212:215],v[184:187],v[160:163]\n"
"ds_read2st64_b32 v[206:207],v13 offset0:222 offset1:223\n"
CV "v33,v34,v35\n"
MF "v[160:163],a[172:175],v[180:183],v[160:163]\n"
"s_add_u32 s87,s90,2048\n"
CV "v34,v36,v37\n"
CV "v35,v38,v39\n"
MF "v[160:163],a[132:135],v[176:179],v[160:163]\n"
MF "v[168:171],a[136:139],v[176:179],v[168:171]\n"
"buffer_store_dwordx4 v[32:35],v9,s[40:43],s87 offen sc1\n"
MF "v[168:171],a[176:179],v[180:183],v[168:171]\n"
MF "v[168:171],a[216:219],v[184:187],v[168:171]\n"
"s_waitcnt lgkmcnt(0)\n"
"s_barrier\n"
MF "v[172:175],a[216:219],a[244:247],v[172:175]\n"
TR "v[176:177],v0 offset:49152\n"
"v_add_f32 v40,v40,v200\n"
MF "v[172:175],a[176:179],a[240:243],v[172:175]\n"
TR "v[178:179],v0 offset:51200\n"
"v_add_f32 v41,v41,v201\n"
MF "v[172:175],a[136:139],v[188:191],v[172:175]\n"
"ds_read2st64_b32 v[192:193],v13 offset0:224 offset1:225\n"
"v_add_f32 v42,v42,v202\n"
MF "v[164:167],a[132:135],v[188:191],v[164:167]\n"
TR "v[180:181],v0 offset:53248\n"
"v_add_f32 v43,v43,v203\n"
MF "v[164:167],a[172:175],a[240:243],v[164:167]\n"
TR "v[182:183],v0 offset:55296\n"
"v_add_f32 v44,v44,v204\n"
MF "v[164:167],a[212:215],a[244:247],v[164:167]\n"
"ds_read2st64_b32 v[194:195],v13 offset0:226 offset1:227\n"
"v_add_f32 v45,v45,v205\n"
MF "v[156:159],a[208:211],a[244:247],v[156:159]\n"
TR "v[184:185],v0 offset:57344\n"
"v_add_f32 v46,v46,v206\n"
MF "v[156:159],a[168:171],a[240:243],v[156:159]\n"
TR "v[186:187],v0 offset:59392\n"
"v_add_f32 v47,v47,v207\n"
MF "v[156:159],a[128:131],v[188:191],v[156:159]\n"
"ds_read2st64_b32 v[196:197],v13 offset0:228 offset1:229\n"
CV "v40,v40,v41\n"
MF "v[148:151],a[124:127],v[188:191],v[148:151]\n"
"ds_read2st64_b32 v[198:199],v13 offset0:230 offset1:231\n"
CV "v41,v42,v43\n"
MF "v[148:151],a[164:167],a[240:243],v[148:151]\n"
"s_add_u32 s87,s90,3072\n"
CV "v42,v44,v45\n"
CV "v43,v46,v47\n"
MF "v[148:151],a[204:207],a[244:247],v[148:151]\n"
MF "v[140:143],a[200:203],a[244:247],v[140:143]\n"
"buffer_store_dwordx4 v[40:43],v9,s[40:43],s87 offen sc1\n"
MF "v[140:143],a[160:163],a[240:243],v[140:143]\n"
MF "v[140:143],a[120:123],v[188:191],v[140:143]\n"
"s_waitcnt lgkmcnt(2)\n"
MF "v[96:99],a[20:23],v[176:179],v[96:99]\n"
"s_waitcnt lgkmcnt(0)\n"
TR "v[188:189],v1 offset:49152\n"
"v_add_f32 v48,v48,v192\n"
MF "v[96:99],a[60:63],v[180:183],v[96:99]\n"
TR "v[190:191],v1 offset:51200\n"
"v_add_f32 v49,v49,v193\n"
MF "v[96:99],a[100:103],v[184:187],v[96:99]\n"
"ds_read2st64_b32 v[200:201],v13 offset0:0 offset1:1\n"
"v_add_f32 v50,v50,v194\n"
MF "v[104:107],a[104:107],v[184:187],v[104:107]\n"
TR "a[240:241],v1 offset:53248\n"
"v_add_f32 v51,v51,v195\n"
MF "v[104:107],a[64:67],v[180:183],v[104:107]\n"
TR "a[242:243],v1 offset:55296\n"
"v_add_f32 v52,v52,v196\n"
MF "v[104:107],a[24:27],v[176:179],v[104:107]\n"
"ds_read2st64_b32 v[202:203],v13 offset0:2 offset1:3\n"
"v_add_f32 v53,v53,v197\n"
MF "v[112:115],a[28:31],v[176:179],v[112:115]\n"
TR "a[244:245],v1 offset:57344\n"
"v_add_f32 v54,v54,v198\n"
MF "v[112:115],a[68:71],v[180:183],v[112:115]\n"
TR "a[246:247],v1 offset:59392\n"
"v_add_f32 v55,v55,v199\n"
MF "v[112:115],a[108:111],v[184:187],v[112:115]\n"
"ds_read2st64_b32 v[204:205],v13 offset0:4 offset1:5\n"
CV "v48,v48,v49\n"
MF "v[120:123],a[112:115],v[184:187],v[120:123]\n"
"ds_read2st64_b32 v[206:207],v13 offset0:6 offset1:7\n"
CV "v49,v50,v51\n"
MF "v[120:123],a[72:75],v[180:183],v[120:123]\n"
"s_add_u32 s87,s90,4096\n"
CV "v50,v52,v53\n"
CV "v51,v54,v55\n"
MF "v[120:123],a[32:35],v[176:179],v[120:123]\n"
MF "v[128:131],a[36:39],v[176:179],v[128:131]\n"
"buffer_store_dwordx4 v[48:51],v9,s[40:43],s87 offen sc1\n"
MF "v[128:131],a[76:79],v[180:183],v[128:131]\n"
MF "v[128:131],a[116:119],v[184:187],v[128:131]\n"
"s_waitcnt lgkmcnt(2)\n"
MF "v[132:135],a[116:119],a[244:247],v[132:135]\n"
"s_waitcnt lgkmcnt(0)\n"
TR "v[176:177],v2 offset:49152\n"
"v_add_f32 v56,v56,v200\n"
MF "v[132:135],a[76:79],a[240:243],v[132:135]\n"
TR "v[178:179],v2 offset:51200\n"
"v_add_f32 v57,v57,v201\n"
MF "v[132:135],a[36:39],v[188:191],v[132:135]\n"
"ds_read2st64_b32 v[192:193],v13 offset0:8 offset1:9\n"
"v_add_f32 v58,v58,v202\n"
MF "v[124:127],a[32:35],v[188:191],v[124:127]\n"
TR "v[180:181],v2 offset:53248\n"
"v_add_f32 v59,v59,v203\n"
MF "v[124:127],a[72:75],a[240:243],v[124:127]\n"
TR "v[182:183],v2 offset:55296\n"
"v_add_f32 v60,v60,v204\n"
MF "v[124:127],a[112:115],a[244:247],v[124:127]\n"
"ds_read2st64_b32 v[194:195],v13 offset0:10 offset1:11\n"
"v_add_f32 v61,v61,v205\n"
MF "v[116:119],a[108:111],a[244:247],v[116:119]\n"
TR "v[184:185],v2 offset:57344\n"
"v_add_f32 v62,v62,v206\n"
MF "v[116:119],a[68:71],a[240:243],v[116:119]\n"
TR "v[186:187],v2 offset:59392\n"
"v_add_f32 v63,v63,v207\n"
MF "v[116:119],a[28:31],v[188:191],v[116:119]\n"
"ds_read2st64_b32 v[196:197],v13 offset0:12 offset1:13\n"
CV "v56,v56,v57\n"
MF "v[108:111],a[24:27],v[188:191],v[108:111]\n"
"ds_read2st64_b32 v[198:199],v13 offset0:14 offset1:15\n"
CV "v57,v58,v59\n"
MF "v[108:111],a[64:67],a[240:243],v[108:111]\n"
"s_add_u32 s87,s90,10240\n"
CV "v58,v60,v61\n"
CV "v59,v62,v63\n"
MF "v[108:111],a[104:107],a[244:247],v[108:111]\n"
MF "v[100:103],a[100:103],a[244:247],v[100:103]\n"
"buffer_store_dwordx4 v[56:59],v9,s[40:43],s87 offen sc1\n"
MF "v[100:103],a[60:63],a[240:243],v[100:103]\n"
"s_waitcnt vmcnt(7)\n"
"s_cmp_eq_u32 s83,0xf0000000\n"
MF "v[100:103],a[20:23],v[188:191],v[100:103]\n"
"s_cbranch_scc1 .Ljs15\n"
"s_cmp_eq_u32 s83,0xf0000010\n"
"s_cbranch_scc1 .Ljc15\n"
MF "v[248:251],v[210:213],a[250:253],0\n"
"v_mov_b32 v240,v208\n"
"v_mov_b32 v241,v209\n"
MF "v[244:247],v[214:217],a[250:253],0\n"
MF "v[248:251],v[218:221],a[250:253],v[248:251]\n"
MF "v[244:247],v[222:225],a[250:253],v[244:247]\n"
MF "v[248:251],v[226:229],a[250:253],v[248:251]\n"
MF "v[244:247],v[230:233],a[250:253],v[244:247]\n"
MF "v[248:251],v[234:237],a[250:253],v[248:251]\n"
MF "v[244:247],v[238:241],a[250:253],v[244:247]\n"
"s_branch .Ljs15\n"
".Ljc15:\n"
MF "v[136:139],a[252:255],v[208:211],v[136:139]\n"
MF "v[144:147],a[252:255],v[212:215],v[144:147]\n"
MF "v[152:155],a[252:255],v[216:219],v[152:155]\n"
MF "v[160:163],a[252:255],v[220:223],v[160:163]\n"
MF "v[168:171],a[252:255],v[224:227],v[168:171]\n"
MF "v[140:143],a[248:251],v[208:211],v[140:143]\n"
MF "v[148:151],a[248:251],v[212:215],v[148:151]\n"
MF "v[156:159],a[248:251],v[216:219],v[156:159]\n"
MF "v[164:167],a[248:251],v[220:223],v[164:167]\n"
MF "v[172:175],a[248:251],v[224:227],v[172:175]\n"
".Ljs15:\n"
"s_waitcnt lgkmcnt(2)\n"
MF "v[136:139],a[20:23],v[176:179],v[136:139]\n"
"s_waitcnt lgkmcnt(0)\n"
TR "v[188:189],v3 offset:49152\n"
"v_add_f32 v64,v64,v192\n"
MF "v[136:139],a[60:63],v[180:183],v[136:139]\n"
TR "v[190:191],v3 offset:51200\n"
"v_add_f32 v65,v65,v193\n"
MF "v[136:139],a[100:103],v[184:187],v[136:139]\n"
"ds_read2st64_b32 v[200:201],v13 offset0:16 offset1:17\n"
"v_add_f32 v66,v66,v194\n"
MF "v[144:147],a[104:107],v[184:187],v[144:147]\n"
TR "a[240:241],v3 offset:53248\n"
"v_add_f32 v67,v67,v195\n"
MF "v[144:147],a[64:67],v[180:183],v[144:147]\n"
TR "a[242:243],v3 offset:55296\n"
"v_add_f32 v68,v68,v196\n"
MF "v[144:147],a[24:27],v[176:179],v[144:147]\n"
"ds_read2st64_b32 v[202:203],v13 offset0:18 offset1:19\n"
"v_add_f32 v69,v69,v197\n"
MF "v[152:155],a[28:31],v[176:179],v[152:155]\n"
TR "a[244:245],v3 offset:57344\n"
"v_add_f32 v70,v70,v198\n"
MF "v[152:155],a[68:71],v[180:183],v[152:155]\n"
TR "a[246:247],v3 offset:59392\n"
"v_add_f32 v71,v71,v199\n"
MF "v[152:155],a[108:111],v[184:187],v[152:155]\n"
"ds_read2st64_b32 v[204:205],v13 offset0:20 offset1:21\n"
CV "v64,v64,v65\n"
MF "v[160:163],a[112:115],v[184:187],v[160:163]\n"
"ds_read2st64_b32 v[206:207],v13 offset0:22 offset1:23\n"
CV "v65,v66,v67\n"
MF "v[160:163],a[72:75],v[180:183],v[160:163]\n"
"s_add_u32 s87,s90,11264\n"
CV "v66,v68,v69\n"
CV "v67,v70,v71\n"
MF "v[160:163],a[32:35],v[176:179],v[160:163]\n"
"s_lshl_b32 s82,s92,10\n"
"v_sub_u32 v10,v10,v11\n"
"v_cmp_gt_u32 vcc,s95,v10\n"
MF "v[168:171],a[36:39],v[176:179],v[168:171]\n"
"buffer_store_dwordx4 v[64:67],v9,s[40:43],s87 offen sc1\n"
MF "v[168:171],a[76:79],v[180:183],v[168:171]\n"
"s_add_u32 s82,s82,s93\n"
CV "v244,v244,v245\n"
CV "v245,v246,v247\n"
MF "v[168:171],a[116:119],v[184:187],v[168:171]\n"
"s_waitcnt lgkmcnt(0)\n"
"s_barrier\n"
MF "v[172:175],a[116:119],a[244:247],v[172:175]\n"
TR "v[176:177],v4\n"
"v_add_f32 v72,v72,v200\n"
MF "v[172:175],a[76:79],a[240:243],v[172:175]\n"
TR "v[178:179],v4 offset:2048\n"
"v_add_f32 v73,v73,v201\n"
MF "v[172:175],a[36:39],v[188:191],v[172:175]\n"
"ds_read2st64_b32 v[192:193],v13 offset0:24 offset1:25\n"
"v_add_f32 v74,v74,v202\n"
MF "v[164:167],a[32:35],v[188:191],v[164:167]\n"
TR "v[180:181],v4 offset:4096\n"
"v_add_f32 v75,v75,v203\n"
MF "v[164:167],a[72:75],a[240:243],v[164:167]\n"
TR "v[182:183],v4 offset:6144\n"
"v_add_f32 v76,v76,v204\n"
MF "v[164:167],a[112:115],a[244:247],v[164:167]\n"
"ds_read2st64_b32 v[194:195],v13 offset0:26 offset1:27\n"
"v_add_f32 v77,v77,v205\n"
MF "v[156:159],a[108:111],a[244:247],v[156:159]\n"
TR "v[184:185],v4 offset:8192\n"
"v_add_f32 v78,v78,v206\n"
MF "v[156:159],a[68:71],a[240:243],v[156:159]\n"
TR "v[186:187],v4 offset:10240\n"
"v_add_f32 v79,v79,v207\n"
MF "v[156:159],a[28:31],v[188:191],v[156:159]\n"
"ds_read2st64_b32 v[196:197],v13 offset0:28 offset1:29\n"
CV "v72,v72,v73\n"
MF "v[148:151],a[24:27],v[188:191],v[148:151]\n"
"ds_read2st64_b32 v[198:199],v13 offset0:30 offset1:31\n"
CV "v73,v74,v75\n"
MF "v[148:151],a[64:67],a[240:243],v[148:151]\n"
"s_add_u32 s87,s90,12288\n"
CV "v74,v76,v77\n"
CV "v75,v78,v79\n"
MF "v[148:151],a[104:107],a[244:247],v[148:151]\n"
"s_cmp_eq_u64 vcc,0\n"
"s_cselect_b32 s82,s82,0xf0000000\n"
"s_cselect_b32 s84,s96,0xf0000000\n"
"s_cselect_b32 s59,32,0\n"
MF "v[140:143],a[100:103],a[244:247],v[140:143]\n"
"buffer_store_dwordx4 v[72:75],v9,s[40:43],s87 offen sc1\n"
MF "v[140:143],a[60:63],a[240:243],v[140:143]\n"
"s_add_u32 s92,s92,s59\n"
".Lnf6:\n"
"v_readlane_b32 s58,v10,s67\n"
"s_cmp_lt_u32 s58,s72\n"
"s_cbranch_scc1 .Lsp6\n"
".Lnb6:\n"
MF "v[140:143],a[20:23],v[188:191],v[140:143]\n"
"s_waitcnt lgkmcnt(2)\n"
CV "v246,v248,v249\n"
CV "v247,v250,v251\n"
MF "v[96:99],a[140:143],v[176:179],v[96:99]\n"
"s_waitcnt lgkmcnt(0)\n"
TR "v[188:189],v5\n"
"v_add_f32 v80,v80,v192\n"
MF "v[96:99],a[180:183],v[180:183],v[96:99]\n"
TR "v[190:191],v5 offset:2048\n"
"v_add_f32 v81,v81,v193\n"
MF "v[96:99],a[220:223],v[184:187],v[96:99]\n"
"ds_read2st64_b32 v[200:201],v13 offset0:32 offset1:33\n"
"v_add_f32 v82,v82,v194\n"
MF "v[104:107],a[224:227],v[184:187],v[104:107]\n"
TR "a[240:241],v5 offset:4096\n"
"v_add_f32 v83,v83,v195\n"
MF "v[104:107],a[184:187],v[180:183],v[104:107]\n"
TR "a[242:243],v5 offset:6144\n"
"v_add_f32 v84,v84,v196\n"
MF "v[104:107],a[144:147],v[176:179],v[104:107]\n"
"ds_read2st64_b32 v[202:203],v13 offset0:34 offset1:35\n"
"v_add_f32 v85,v85,v197\n"
MF "v[112:115],a[148:151],v[176:179],v[112:115]\n"
TR "a[244:245],v5 offset:8192\n"
"v_add_f32 v86,v86,v198\n"
MF "v[112:115],a[188:191],v[180:183],v[112:115]\n"
TR "a[246:247],v5 offset:10240\n"
"v_add_f32 v87,v87,v199\n"
MF "v[112:115],a[228:231],v[184:187],v[112:115]\n"
"ds_read2st64_b32 v[204:205],v13 offset0:36 offset1:37\n"
CV "v80,v80,v81\n"
MF "v[120:123],a[232:235],v[184:187],v[120:123]\n"
"ds_read2st64_b32 v[206:207],v13 offset0:38 offset1:39\n"
CV "v81,v82,v83\n"
MF "v[120:123],a[192:195],v[180:183],v[120:123]\n"
"s_add_u32 s87,s90,13312\n"
CV "v82,v84,v85\n"
CV "v83,v86,v87\n"
MF "v[120:123],a[152:155],v[176:179],v[120:123]\n"
"s_cmp_ge_u32 s85,5\n"
"s_cselect_b32 s82,s70,s82\n"
"s_cselect_b32 s84,0xf0000010,s84\n"
MF "v[128:131],a[156:159],v[176:179],v[128:131]\n"
"buffer_store_dwordx4 v[80:83],v9,s[40:43],s87 offen sc1\n"
MF "v[128:131],a[196:199],v[180:183],v[128:131]\n"
"s_cmp_ge_u32 s85,5\n"
"s_cselect_b32 s71,1024,s71\n"
"s_cselect_b32 s66,0x80000000,s66\n"
MF "v[128:131],a[236:239],v[184:187],v[128:131]\n"
"s_add_u32 s70,s70,20480\n"
"s_waitcnt lgkmcnt(2)\n"
"v_permlane16_swap_b32 v244,v246\n"
MF "v[132:135],a[236:239],a[244:247],v[132:135]\n"
"s_waitcnt lgkmcnt(0)\n"
TR "v[176:177],v6\n"
"v_add_f32 v88,v88,v200\n"
MF "v[132:135],a[196:199],a[240:243],v[132:135]\n"
TR "v[178:179],v6 offset:2048\n"
"v_add_f32 v89,v89,v201\n"
MF "v[132:135],a[156:159],v[188:191],v[132:135]\n"
"v_add_f32 v90,v90,v202\n"
"v_add_f32 v91,v91,v203\n"
MF "v[124:127],a[152:155],v[188:191],v[124:127]\n"
TR "v[180:181],v6 offset:4096\n"
"v_add_f32 v92,v92,v204\n"
MF "v[124:127],a[192:195],a[240:243],v[124:127]\n"
TR "v[182:183],v6 offset:6144\n"
"v_add_f32 v93,v93,v205\n"
MF "v[124:127],a[232:235],a[244:247],v[124:127]\n"
"v_add_f32 v94,v94,v206\n"
"v_add_f32 v95,v95,v207\n"
MF "v[116:119],a[228:231],a[244:247],v[116:119]\n"
TR "v[184:185],v6 offset:8192\n"
CV "v88,v88,v89\n"
MF "v[116:119],a[188:191],a[240:243],v[116:119]\n"
TR "v[186:187],v6 offset:10240\n"
CV "v89,v90,v91\n"
MF "v[116:119],a[148:151],v[188:191],v[116:119]\n"
"s_add_u32 s87,s90,14336\n"
CV "v90,v92,v93\n"
CV "v91,v94,v95\n"
MF "v[108:111],a[144:147],v[188:191],v[108:111]\n"
"v_permlane16_swap_b32 v245,v247\n"
MF "v[108:111],a[184:187],a[240:243],v[108:111]\n"
"buffer_store_dwordx4 v[244:247],v8,s[44:47],s83 offen sc1\n"
MF "v[108:111],a[224:227],a[244:247],v[108:111]\n"
"s_mov_b32 s83,s84\n"
MF "v[100:103],a[220:223],a[244:247],v[100:103]\n"
"buffer_store_dwordx4 v[88:91],v9,s[40:43],s87 offen sc1\n"
MF "v[100:103],a[180:183],a[240:243],v[100:103]\n"
"s_mov_b32 s90,s91\n"
"s_add_u32 s91,s91,20480\n"
MF "v[100:103],a[140:143],v[188:191],v[100:103]\n"
"s_waitcnt lgkmcnt(0)\n"
LD "v[208:211],v9,s[40:43],s82 offen sc1\n"
MF "v[136:139],a[140:143],v[176:179],v[136:139]\n"
TR "v[188:189],v7\n"
MF "v[136:139],a[180:183],v[180:183],v[136:139]\n"
TR "v[190:191],v7 offset:2048\n"
MF "v[136:139],a[220:223],v[184:187],v[136:139]\n"
MF "v[144:147],a[224:227],v[184:187],v[144:147]\n"
TR "a[240:241],v7 offset:4096\n"
MF "v[144:147],a[184:187],v[180:183],v[144:147]\n"
"s_add_u32 s82,s82,s71\n"
TR "a[242:243],v7 offset:6144\n"
MF "v[144:147],a[144:147],v[176:179],v[144:147]\n"
LD "v[212:215],v9,s[40:43],s82 offen sc1\n"
MF "v[152:155],a[148:151],v[176:179],v[152:155]\n"
TR "a[244:245],v7 offset:8192\n"
MF "v[152:155],a[188:191],v[180:183],v[152:155]\n"
TR "a[246:247],v7 offset:10240\n"
MF "v[152:155],a[228:231],v[184:187],v[152:155]\n"
MF "v[160:163],a[232:235],v[184:187],v[160:163]\n"
MF "v[160:163],a[192:195],v[180:183],v[160:163]\n"
MF "v[160:163],a[152:155],v[176:179],v[160:163]\n"
"s_add_u32 s82,s82,s71\n"
MF "v[168:171],a[156:159],v[176:179],v[168:171]\n"
LD "v[216:219],v9,s[40:43],s82 offen sc1\n"
MF "v[168:171],a[196:199],v[180:183],v[168:171]\n"
MF "v[168:171],a[236:239],v[184:187],v[168:171]\n"
"s_waitcnt lgkmcnt(0)\n"
"s_barrier\n"
MF "v[172:175],a[236:239],a[244:247],v[172:175]\n"
TR "v[176:177],v0 offset:24576\n"
MF "v[172:175],a[196:199],a[240:243],v[172:175]\n"
"s_add_u32 s85,s85,1\n"
TR "v[178:179],v0 offset:26624\n"
MF "v[172:175],a[156:159],v[188:191],v[172:175]\n"
MF "v[164:167],a[152:155],v[188:191],v[164:167]\n"
TR "v[180:181],v0 offset:28672\n"
MF "v[164:167],a[192:195],a[240:243],v[164:167]\n"
"s_add_u32 s82,s82,s71\n"
TR "v[182:183],v0 offset:30720\n"
MF "v[164:167],a[232:235],a[244:247],v[164:167]\n"
LD "v[220:223],v9,s[40:43],s82 offen sc1\n"
MF "v[156:159],a[228:231],a[244:247],v[156:159]\n"
TR "v[184:185],v0 offset:32768\n"
MF "v[156:159],a[188:191],a[240:243],v[156:159]\n"
TR "v[186:187],v0 offset:34816\n"
MF "v[156:159],a[148:151],v[188:191],v[156:159]\n"
MF "v[148:151],a[144:147],v[188:191],v[148:151]\n"
MF "v[148:151],a[184:187],a[240:243],v[148:151]\n"
MF "v[148:151],a[224:227],a[244:247],v[148:151]\n"
MF "v[140:143],a[220:223],a[244:247],v[140:143]\n"
MF "v[140:143],a[180:183],a[240:243],v[140:143]\n"
MF "v[140:143],a[140:143],v[188:191],v[140:143]\n"
"s_waitcnt lgkmcnt(0)\n"
"s_branch .Lpl1\n"
".Lplast1:\n"
MF "v[96:99],v[176:179],a[0:3],v[252:255]\n"
"s_add_u32 m0,s79,73728\n"
TR "v[188:189],v1 offset:24576\n"
TR "v[190:191],v1 offset:26624\n"
MF "v[96:99],v[180:183],a[40:43],v[96:99]\n"
"s_nop 0\n"
TR "a[240:241],v1 offset:28672\n"
TR "a[242:243],v1 offset:30720\n"
MF "v[96:99],v[184:187],a[80:83],v[96:99]\n"
"s_add_u32 s82,s82,s71\n"
TR "a[244:245],v1 offset:32768\n"
TR "a[246:247],v1 offset:34816\n"
MF "v[104:107],v[184:187],a[84:87],v[252:255]\n"
LD "v[224:227],v9,s[40:43],s82 offen sc1\n"
MF "v[104:107],v[180:183],a[44:47],v[104:107]\n"
"s_nop 0\n"
"ds_write_addtid_b32 v56\n"
"ds_write_addtid_b32 v57 offset:256\n"
MF "v[104:107],v[176:179],a[4:7],v[104:107]\n"
"s_nop 0\n"
"ds_write_addtid_b32 v58 offset:512\n"
"ds_write_addtid_b32 v59 offset:768\n"
MF "v[112:115],v[176:179],a[8:11],v[252:255]\n"
"s_nop 0\n"
"ds_write_addtid_b32 v60 offset:1024\n"
"ds_write_addtid_b32 v61 offset:1280\n"
MF "v[112:115],v[180:183],a[48:51],v[112:115]\n"
"s_nop 0\n"
"ds_write_addtid_b32 v62 offset:1536\n"
"ds_write_addtid_b32 v63 offset:1792\n"
MF "v[112:115],v[184:187],a[88:91],v[112:115]\n"
"s_nop 0\n"
"ds_write_addtid_b32 v64 offset:2048\n"
"ds_write_addtid_b32 v65 offset:2304\n"
MF "v[120:123],v[184:187],a[92:95],v[252:255]\n"
"s_nop 0\n"
"ds_write_addtid_b32 v66 offset:2560\n"
"ds_write_addtid_b32 v67 offset:2816\n"
MF "v[120:123],v[180:183],a[52:55],v[120:123]\n"
"s_nop 0\n"
"ds_write_addtid_b32 v68 offset:3072\n"
"ds_write_addtid_b32 v69 offset:3328\n"
MF "v[120:123],v[176:179],a[12:15],v[120:123]\n"
"s_nop 0\n"
"ds_write_addtid_b32 v70 offset:3584\n"
"ds_write_addtid_b32 v71 offset:3840\n"
MF "v[128:131],v[176:179],a[16:19],v[252:255]\n"
"s_nop 0\n"
"ds_write_addtid_b32 v72 offset:4096\n"
"ds_write_addtid_b32 v73 offset:4352\n"
MF "v[128:131],v[180:183],a[56:59],v[128:131]\n"
"s_nop 0\n"
"ds_write_addtid_b32 v74 offset:4608\n"
"ds_write_addtid_b32 v75 offset:4864\n"
MF "v[128:131],v[184:187],a[96:99],v[128:131]\n"
"s_waitcnt lgkmcnt(15)\n"
"ds_write_addtid_b32 v76 offset:5120\n"
MF "v[132:135],a[244:247],a[96:99],v[252:255]\n"
"s_nop 0\n"
TR "v[176:177],v2 offset:24576\n"
TR "v[178:179],v2 offset:26624\n"
MF "v[132:135],a[240:243],a[56:59],v[132:135]\n"
"s_nop 0\n"
TR "v[180:181],v2 offset:28672\n"
TR "v[182:183],v2 offset:30720\n"
MF "v[132:135],v[188:191],a[16:19],v[132:135]\n"
"s_nop 0\n"
TR "v[184:185],v2 offset:32768\n"
TR "v[186:187],v2 offset:34816\n"
MF "v[124:127],v[188:191],a[12:15],v[252:255]\n"
"s_nop 0\n"
"ds_write_addtid_b32 v77 offset:5376\n"
"ds_write_addtid_b32 v78 offset:5632\n"
MF "v[124:127],a[240:243],a[52:55],v[124:127]\n"
"s_nop 0\n"
"ds_write_addtid_b32 v79 offset:5888\n"
"ds_write_addtid_b32 v80 offset:6144\n"
MF "v[124:127],a[244:247],a[92:95],v[124:127]\n"
"s_nop 0\n"
"ds_write_addtid_b32 v81 offset:6400\n"
"ds_write_addtid_b32 v82 offset:6656\n"
MF "v[116:119],a[244:247],a[88:91],v[252:255]\n"
"s_nop 0\n"
"ds_write_addtid_b32 v83 offset:6912\n"
"ds_write_addtid_b32 v84 offset:7168\n"
MF "v[116:119],a[240:243],a[48:51],v[116:119]\n"
"s_nop 0\n"
"ds_write_addtid_b32 v85 offset:7424\n"
"ds_write_addtid_b32 v86 offset:7680\n"
MF "v[116:119],v[188:191],a[8:11],v[116:119]\n"
"s_nop 0\n"
"ds_write_addtid_b32 v87 offset:7936\n"
"ds_write_addtid_b32 v88 offset:8192\n"
MF "v[108:111],v[188:191],a[4:7],v[252:255]\n"
"s_nop 0\n"
"ds_write_addtid_b32 v89 offset:8448\n"
"ds_write_addtid_b32 v90 offset:8704\n"
MF "v[108:111],a[240:243],a[44:47],v[108:111]\n"
"s_nop 0\n"
"ds_write_addtid_b32 v91 offset:8960\n"
"ds_write_addtid_b32 v92 offset:9216\n"
MF "v[108:111],a[244:247],a[84:87],v[108:111]\n"
"s_nop 0\n"
"ds_write_addtid_b32 v93 offset:9472\n"
"ds_write_addtid_b32 v94 offset:9728\n"
MF "v[100:103],a[244:247],a[80:83],v[252:255]\n"
"ds_write_addtid_b32 v95 offset:9984\n"
MF "v[100:103],a[240:243],a[40:43],v[100:103]\n"
MF "v[100:103],v[188:191],a[0:3],v[100:103]\n"
"s_waitcnt lgkmcnt(15)\n"
MF "v[136:139],v[176:179],a[0:3],v[252:255]\n"
"s_nop 0\n"
TR "v[188:189],v3 offset:24576\n"
TR "v[190:191],v3 offset:26624\n"
MF "v[136:139],v[180:183],a[40:43],v[136:139]\n"
"s_nop 0\n"
TR "a[240:241],v3 offset:28672\n"
TR "a[242:243],v3 offset:30720\n"
MF "v[136:139],v[184:187],a[80:83],v[136:139]\n"
"s_nop 0\n"
TR "a[244:245],v3 offset:32768\n"
TR "a[246:247],v3 offset:34816\n"
MF "v[144:147],v[184:187],a[84:87],v[252:255]\n"
"s_waitcnt lgkmcnt(0)\n"
"s_barrier\n"
MF "v[144:147],v[180:183],a[44:47],v[144:147]\n"
"ds_read2st64_b32 v[56:57],v13 offset0:192 offset1:193\n"
MF "v[144:147],v[176:179],a[4:7],v[144:147]\n"
"ds_read2st64_b32 v[58:59],v13 offset0:194 offset1:195\n"
MF "v[152:155],v[176:179],a[8:11],v[252:255]\n"
"ds_read2st64_b32 v[60:61],v13 offset0:196 offset1:197\n"
MF "v[152:155],v[180:183],a[48:51],v[152:155]\n"
"ds_read2st64_b32 v[62:63],v13 offset0:198 offset1:199\n"
MF "v[152:155],v[184:187],a[88:91],v[152:155]\n"
"ds_read2st64_b32 v[64:65],v13 offset0:200 offset1:201\n"
MF "v[160:163],v[184:187],a[92:95],v[252:255]\n"
"ds_read2st64_b32 v[66:67],v13 offset0:202 offset1:203\n"
MF "v[160:163],v[180:183],a[52:55],v[160:163]\n"
"ds_read2st64_b32 v[68:69],v13 offset0:204 offset1:205\n"
MF "v[160:163],v[176:179],a[12:15],v[160:163]\n"
"ds_read2st64_b32 v[70:71],v13 offset0:206 offset1:207\n"
MF "v[168:171],v[176:179],a[16:19],v[252:255]\n"
"s_waitcnt lgkmcnt(4)\n"
"ds_read2st64_b32 v[72:73],v13 offset0:208 offset1:209\n"
"v_add_f32 v16,v16,v56\n"
MF "v[168:171],v[180:183],a[56:59],v[168:171]\n"
"ds_read2st64_b32 v[74:75],v13 offset0:210 offset1:211\n"
"v_add_f32 v17,v17,v57\n"
MF "v[168:171],v[184:187],a[96:99],v[168:171]\n"
"s_waitcnt lgkmcnt(0)\n"
"s_barrier\n"
MF "v[172:175],a[244:247],a[96:99],v[252:255]\n"
"s_nop 0\n"
TR "v[176:177],v0\n"
TR "v[178:179],v0 offset:2048\n"
MF "v[172:175],a[240:243],a[56:59],v[172:175]\n"
"s_nop 0\n"
TR "v[180:181],v0 offset:4096\n"
TR "v[182:183],v0 offset:6144\n"
MF "v[172:175],v[188:191],a[16:19],v[172:175]\n"
"s_nop 0\n"
TR "v[184:185],v0 offset:8192\n"
TR "v[186:187],v0 offset:10240\n"
MF "v[164:167],v[188:191],a[12:15],v[252:255]\n"
"ds_read2st64_b32 v[76:77],v13 offset0:212 offset1:213\n"
"v_add_f32 v18,v18,v58\n"
MF "v[164:167],a[240:243],a[52:55],v[164:167]\n"
"ds_read2st64_b32 v[78:79],v13 offset0:214 offset1:215\n"
"v_add_f32 v19,v19,v59\n"
MF "v[164:167],a[244:247],a[92:95],v[164:167]\n"
"ds_read2st64_b32 v[80:81],v13 offset0:216 offset1:217\n"
"v_add_f32 v20,v20,v60\n"
MF "v[156:159],a[244:247],a[88:91],v[252:255]\n"
"ds_read2st64_b32 v[82:83],v13 offset0:218 offset1:219\n"
"v_add_f32 v21,v21,v61\n"
MF "v[156:159],a[240:243],a[48:51],v[156:159]\n"
"ds_read2st64_b32 v[84:85],v13 offset0:220 offset1:221\n"
"v_add_f32 v22,v22,v62\n"
MF "v[156:159],v[188:191],a[8:11],v[156:159]\n"
"ds_read2st64_b32 v[86:87],v13 offset0:222 offset1:223\n"
"v_add_f32 v23,v23,v63\n"
MF "v[148:151],v[188:191],a[4:7],v[252:255]\n"
"s_waitcnt vmcnt(6)\n"
"v_mov_b32 v14,s76\n"
"s_add_u32 s76,s76,1\n"
MF "v[148:151],a[240:243],a[44:47],v[148:151]\n"
"buffer_store_dword v14,v15,s[40:43],s94 offen sc1\n"
MF "v[148:151],a[244:247],a[84:87],v[148:151]\n"
"ds_read2st64_b32 v[88:89],v13 offset0:224 offset1:225\n"
CV "v16,v16,v17\n"
MF "v[140:143],a[244:247],a[80:83],v[252:255]\n"
"ds_read2st64_b32 v[90:91],v13 offset0:226 offset1:227\n"
CV "v17,v18,v19\n"
MF "v[140:143],a[240:243],a[40:43],v[140:143]\n"
"ds_read2st64_b32 v[92:93],v13 offset0:228 offset1:229\n"
CV "v18,v20,v21\n"
MF "v[140:143],v[188:191],a[0:3],v[140:143]\n"
"s_waitcnt lgkmcnt(9)\n"
"ds_read2st64_b32 v[94:95],v13 offset0:230 offset1:231\n"
CV "v19,v22,v23\n"
MF "v[96:99],v[176:179],a[120:123],v[96:99]\n"
"s_nop 0\n"
TR "v[188:189],v1\n"
TR "v[190:191],v1 offset:2048\n"
MF "v[96:99],v[180:183],a[160:163],v[96:99]\n"
"s_nop 0\n"
TR "a[240:241],v1 offset:4096\n"
TR "a[242:243],v1 offset:6144\n"
MF "v[96:99],v[184:187],a[200:203],v[96:99]\n"
"s_add_u32 s87,s90,0\n"
TR "a[244:245],v1 offset:8192\n"
TR "a[246:247],v1 offset:10240\n"
MF "v[104:107],v[184:187],a[204:207],v[104:107]\n"
"buffer_store_dwordx4 v[16:19],v9,s[40:43],s87 offen sc1\n"
MF "v[104:107],v[180:183],a[164:167],v[104:107]\n"
"v_add_f32 v24,v24,v64\n"
"v_add_f32 v25,v25,v65\n"
MF "v[104:107],v[176:179],a[124:127],v[104:107]\n"
"v_add_f32 v26,v26,v66\n"
"v_add_f32 v27,v27,v67\n"
MF "v[112:115],v[176:179],a[128:131],v[112:115]\n"
"v_add_f32 v28,v28,v68\n"
"v_add_f32 v29,v29,v69\n"
MF "v[112:115],v[180:183],a[168:171],v[112:115]\n"
"v_add_f32 v30,v30,v70\n"
"v_add_f32 v31,v31,v71\n"
MF "v[112:115],v[184:187],a[208:211],v[112:115]\n"
CV "v24,v24,v25\n"
CV "v25,v26,v27\n"
MF "v[120:123],v[184:187],a[212:215],v[120:123]\n"
"s_add_u32 s87,s90,1024\n"
CV "v26,v28,v29\n"
CV "v27,v30,v31\n"
MF "v[120:123],v[180:183],a[172:175],v[120:123]\n"
"s_waitcnt lgkmcnt(14)\n"
"buffer_store_dwordx4 v[24:27],v9,s[40:43],s87 offen sc1\n"
MF "v[120:123],v[176:179],a[132:135],v[120:123]\n"
"v_add_f32 v32,v32,v72\n"
"v_add_f32 v33,v33,v73\n"
MF "v[128:131],v[176:179],a[136:139],v[128:131]\n"
"v_add_f32 v34,v34,v74\n"
"v_add_f32 v35,v35,v75\n"
MF "v[128:131],v[180:183],a[176:179],v[128:131]\n"
"v_add_f32 v36,v36,v76\n"
"v_add_f32 v37,v37,v77\n"
MF "v[128:131],v[184:187],a[216:219],v[128:131]\n"
"s_waitcnt lgkmcnt(0)\n"
"v_add_f32 v38,v38,v78\n"
"v_add_f32 v39,v39,v79\n"
MF "v[132:135],a[244:247],a[216:219],v[132:135]\n"
"s_nop 0\n"
TR "v[176:177],v2\n"
TR "v[178:179],v2 offset:2048\n"
MF "v[132:135],a[240:243],a[176:179],v[132:135]\n"
"s_nop 0\n"
TR "v[180:181],v2 offset:4096\n"
TR "v[182:183],v2 offset:6144\n"
MF "v[132:135],v[188:191],a[136:139],v[132:135]\n"
"s_nop 0\n"
TR "v[184:185],v2 offset:8192\n"
TR "v[186:187],v2 offset:10240\n"
MF "v[124:127],v[188:191],a[132:135],v[124:127]\n"
CV "v32,v32,v33\n"
CV "v33,v34,v35\n"
MF "v[124:127],a[240:243],a[172:175],v[124:127]\n"
"s_add_u32 s87,s90,2048\n"
CV "v34,v36,v37\n"
CV "v35,v38,v39\n"
MF "v[124:127],a[244:247],a[212:215],v[124:127]\n"
"buffer_store_dwordx4 v[32:35],v9,s[40:43],s87 offen sc1\n"
MF "v[116:119],a[244:247],a[208:211],v[116:119]\n"
"v_add_f32 v40,v40,v80\n"
"v_add_f32 v41,v41,v81\n"
MF "v[116:119],a[240:243],a[168:171],v[116:119]\n"
"v_add_f32 v42,v42,v82\n"
"v_add_f32 v43,v43,v83\n"
MF "v[116:119],v[188:191],a[128:131],v[116:119]\n"
"v_add_f32 v44,v44,v84\n"
"v_add_f32 v45,v45,v85\n"
MF "v[108:111],v[188:191],a[124:127],v[108:111]\n"
"v_add_f32 v46,v46,v86\n"
"v_add_f32 v47,v47,v87\n"
MF "v[108:111],a[240:243],a[164:167],v[108:111]\n"
CV "v40,v40,v41\n"
CV "v41,v42,v43\n"
MF "v[108:111],a[244:247],a[204:207],v[108:111]\n"
"s_add_u32 s87,s90,3072\n"
CV "v42,v44,v45\n"
CV "v43,v46,v47\n"
MF "v[100:103],a[244:247],a[200:203],v[100:103]\n"
"buffer_store_dwordx4 v[40:43],v9,s[40:43],s87 offen sc1\n"
MF "v[100:103],a[240:243],a[160:163],v[100:103]\n"
"v_add_f32 v48,v48,v88\n"
"v_add_f32 v49,v49,v89\n"
MF "v[100:103],v[188:191],a[120:123],v[100:103]\n"
"s_waitcnt lgkmcnt(0)\n"
"v_add_f32 v50,v50,v90\n"
"v_add_f32 v51,v51,v91\n"
MF "v[136:139],v[176:179],a[120:123],v[136:139]\n"
"s_nop 0\n"
TR "v[188:189],v3\n"
TR "v[190:191],v3 offset:2048\n"
MF "v[136:139],v[180:183],a[160:163],v[136:139]\n"
"s_nop 0\n"
TR "a[240:241],v3 offset:4096\n"
TR "a[242:243],v3 offset:6144\n"
MF "v[136:139],v[184:187],a[200:203],v[136:139]\n"
"s_nop 0\n"
TR "a[244:245],v3 offset:8192\n"
TR "a[246:247],v3 offset:10240\n"
MF "v[144:147],v[184:187],a[204:207],v[144:147]\n"
"v_add_f32 v52,v52,v92\n"
"v_add_f32 v53,v53,v93\n"
MF "v[144:147],v[180:183],a[164:167],v[144:147]\n"
"v_add_f32 v54,v54,v94\n"
"v_add_f32 v55,v55,v95\n"
MF "v[144:147],v[176:179],a[124:127],v[144:147]\n"
CV "v48,v48,v49\n"
CV "v49,v50,v51\n"
MF "v[152:155],v[176:179],a[128:131],v[152:155]\n"
"s_add_u32 s87,s90,4096\n"
CV "v50,v52,v53\n"
CV "v51,v54,v55\n"
MF "v[152:155],v[180:183],a[168:171],v[152:155]\n"
"buffer_store_dwordx4 v[48:51],v9,s[40:43],s87 offen sc1\n"
MF "v[152:155],v[184:187],a[208:211],v[152:155]\n"
MF "v[160:163],v[184:187],a[212:215],v[160:163]\n"
MF "v[160:163],v[180:183],a[172:175],v[160:163]\n"
MF "v[160:163],v[176:179],a[132:135],v[160:163]\n"
MF "v[168:171],v[176:179],a[136:139],v[168:171]\n"
MF "v[168:171],v[180:183],a[176:179],v[168:171]\n"
MF "v[168:171],v[184:187],a[216:219],v[168:171]\n"
"s_waitcnt lgkmcnt(0)\n"
"s_barrier\n"
MF "v[172:175],a[244:247],a[216:219],v[172:175]\n"
"s_nop 0\n"
TR "v[176:177],v0 offset:49152\n"
TR "v[178:179],v0 offset:51200\n"
MF "v[172:175],a[240:243],a[176:179],v[172:175]\n"
"s_nop 0\n"
TR "v[180:181],v0 offset:53248\n"
TR "v[182:183],v0 offset:55296\n"
MF "v[172:175],v[188:191],a[136:139],v[172:175]\n"
"s_nop 0\n"
TR "v[184:185],v0 offset:57344\n"
TR "v[186:187],v0 offset:59392\n"
MF "v[164:167],v[188:191],a[132:135],v[164:167]\n"
"buffer_load_dword v10,v12,s[40:43],s86 offen sc1\n"
MF "v[164:167],a[240:243],a[172:175],v[164:167]\n"
MF "v[164:167],a[244:247],a[212:215],v[164:167]\n"
MF "v[156:159],a[244:247],a[208:211],v[156:159]\n"
MF "v[156:159],a[240:243],a[168:171],v[156:159]\n"
MF "v[156:159],v[188:191],a[128:131],v[156:159]\n"
MF "v[148:151],v[188:191],a[124:127],v[148:151]\n"
MF "v[148:151],a[240:243],a[164:167],v[148:151]\n"
MF "v[148:151],a[244:247],a[204:207],v[148:151]\n"
MF "v[140:143],a[244:247],a[200:203],v[140:143]\n"
MF "v[140:143],a[240:243],a[160:163],v[140:143]\n"
MF "v[140:143],v[188:191],a[120:123],v[140:143]\n"
"s_waitcnt lgkmcnt(0)\n"
MF "v[96:99],v[176:179],a[20:23],v[96:99]\n"
"s_nop 0\n"
TR "v[188:189],v1 offset:49152\n"
TR "v[190:191],v1 offset:51200\n"
MF "v[96:99],v[180:183],a[60:63],v[96:99]\n"
"s_nop 0\n"
TR "a[240:241],v1 offset:53248\n"
TR "a[242:243],v1 offset:55296\n"
MF "v[96:99],v[184:187],a[100:103],v[96:99]\n"
"s_nop 0\n"
TR "a[244:245],v1 offset:57344\n"
TR "a[246:247],v1 offset:59392\n"
MF "v[104:107],v[184:187],a[104:107],v[104:107]\n"
MF "v[104:107],v[180:183],a[64:67],v[104:107]\n"
MF "v[104:107],v[176:179],a[24:27],v[104:107]\n"
"s_waitcnt vmcnt(7)\n"
MF "v[136:139],v[208:211],a[252:255],v[136:139]\n"
MF "v[144:147],v[212:215],a[252:255],v[144:147]\n"
MF "v[152:155],v[216:219],a[252:255],v[152:155]\n"
MF "v[160:163],v[220:223],a[252:255],v[160:163]\n"
MF "v[168:171],v[224:227],a[252:255],v[168:171]\n"
MF "v[140:143],v[208:211],a[248:251],v[140:143]\n"
MF "v[148:151],v[212:215],a[248:251],v[148:151]\n"
MF "v[156:159],v[216:219],a[248:251],v[156:159]\n"
MF "v[164:167],v[220:223],a[248:251],v[164:167]\n"
MF "v[172:175],v[224:227],a[248:251],v[172:175]\n"
MF "v[112:115],v[176:179],a[28:31],v[112:115]\n"
MF "v[112:115],v[180:183],a[68:71],v[112:115]\n"
MF "v[112:115],v[184:187],a[108:111],v[112:115]\n"
MF "v[120:123],v[184:187],a[112:115],v[120:123]\n"
MF "v[120:123],v[180:183],a[72:75],v[120:123]\n"
"s_bfe_u32 s87,s22,0x30014\n"
MF "v[120:123],v[176:179],a[32:35],v[120:123]\n"
"s_lshl_b32 s87,s87,7\n"
MF "v[128:131],v[176:179],a[36:39],v[128:131]\n"
"s_and_b32 s60,s56,1\n"
MF "v[128:131],v[180:183],a[76:79],v[128:131]\n"
"s_mul_i32 s60,s60,204800\n"
MF "v[128:131],v[184:187],a[116:119],v[128:131]\n"
"s_waitcnt lgkmcnt(0)\n"
MF "v[132:135],a[244:247],a[116:119],v[132:135]\n"
"s_add_u32 s87,s87,s60\n"
TR "v[176:177],v2 offset:49152\n"
TR "v[178:179],v2 offset:51200\n"
MF "v[132:135],a[240:243],a[76:79],v[132:135]\n"
"s_mov_b32 s71,204800\n"
TR "v[180:181],v2 offset:53248\n"
TR "v[182:183],v2 offset:55296\n"
MF "v[132:135],v[188:191],a[36:39],v[132:135]\n"
"s_mov_b32 s66,204800\n"
TR "v[184:185],v2 offset:57344\n"
TR "v[186:187],v2 offset:59392\n"
MF "v[124:127],v[188:191],a[32:35],v[124:127]\n"
"s_add_u32 s72,s78,2\n"
MF "v[124:127],a[240:243],a[72:75],v[124:127]\n"
"s_and_b32 s72,s72,15\n"
MF "v[124:127],a[244:247],a[112:115],v[124:127]\n"
"s_bfe_u32 s60,s22,0x10014\n"
MF "v[116:119],a[244:247],a[108:111],v[116:119]\n"
"s_lshl_b32 s60,s60,5\n"
MF "v[116:119],a[240:243],a[68:71],v[116:119]\n"
"v_xor_b32 v242,s60,v8\n"
MF "v[116:119],v[188:191],a[28:31],v[116:119]\n"
MF "v[108:111],v[188:191],a[24:27],v[108:111]\n"
"s_add_u32 s16,s78,6\n"
MF "v[108:111],a[240:243],a[64:67],v[108:111]\n"
"s_and_b32 s16,s16,15\n"
MF "v[108:111],a[244:247],a[104:107],v[108:111]\n"
"s_add_u32 s17,s78,4\n"
MF "v[100:103],a[244:247],a[100:103],v[100:103]\n"
"s_and_b32 s17,s17,15\n"
MF "v[100:103],a[240:243],a[60:63],v[100:103]\n"
MF "v[100:103],v[188:191],a[20:23],v[100:103]\n"
"s_waitcnt lgkmcnt(0)\n"
MF "v[136:139],v[176:179],a[20:23],v[136:139]\n"
"s_nop 0\n"
TR "v[188:189],v3 offset:49152\n"
TR "v[190:191],v3 offset:51200\n"
MF "v[136:139],v[180:183],a[60:63],v[136:139]\n"
"s_nop 0\n"
TR "a[240:241],v3 offset:53248\n"
TR "a[242:243],v3 offset:55296\n"
MF "v[136:139],v[184:187],a[100:103],v[136:139]\n"
"s_nop 0\n"
TR "a[244:245],v3 offset:57344\n"
TR "a[246:247],v3 offset:59392\n"
MF "v[144:147],v[184:187],a[104:107],v[144:147]\n"
MF "v[144:147],v[180:183],a[64:67],v[144:147]\n"
"v_mov_b32 v14,s76\n"
"s_add_u32 s76,s76,1\n"
MF "v[144:147],v[176:179],a[24:27],v[144:147]\n"
MF "v[152:155],v[176:179],a[28:31],v[152:155]\n"
MF "v[152:155],v[180:183],a[68:71],v[152:155]\n"
MF "v[152:155],v[184:187],a[108:111],v[152:155]\n"
MF "v[160:163],v[184:187],a[112:115],v[160:163]\n"
MF "v[160:163],v[180:183],a[72:75],v[160:163]\n"
MF "v[160:163],v[176:179],a[32:35],v[160:163]\n"
MF "v[168:171],v[176:179],a[36:39],v[168:171]\n"
MF "v[168:171],v[180:183],a[76:79],v[168:171]\n"
MF "v[168:171],v[184:187],a[116:119],v[168:171]\n"
"s_waitcnt vmcnt(0) lgkmcnt(0)\n"
"s_barrier\n"
MF "v[172:175],a[244:247],a[116:119],v[172:175]\n"
"buffer_store_dword v14,v15,s[40:43],s94 offen sc1\n"
MF "v[172:175],a[240:243],a[76:79],v[172:175]\n"
"s_nop 0\n"
TR "v[176:177],v6\n"
TR "v[178:179],v6 offset:2048\n"
MF "v[172:175],v[188:191],a[36:39],v[172:175]\n"
"s_nop 0\n"
TR "v[180:181],v6 offset:4096\n"
TR "v[182:183],v6 offset:6144\n"
MF "v[164:167],v[188:191],a[32:35],v[164:167]\n"
"s_nop 0\n"
TR "v[184:185],v6 offset:8192\n"
TR "v[186:187],v6 offset:10240\n"
MF "v[164:167],a[240:243],a[72:75],v[164:167]\n"
"v_sub_u32 v10,v10,v11\n"
MF "v[164:167],a[244:247],a[112:115],v[164:167]\n"
"v_readlane_b32 s16,v10,s16\n"
MF "v[156:159],a[244:247],a[108:111],v[156:159]\n"
"s_cmp_lt_u32 s16,9\n"
"s_cselect_b32 s16,0xf0000000,0\n"
MF "v[156:159],a[240:243],a[68:71],v[156:159]\n"
"v_readlane_b32 s17,v10,s17\n"
MF "v[156:159],v[188:191],a[28:31],v[156:159]\n"
"s_cmp_lt_u32 s17,10\n"
"s_cselect_b32 s17,0xf0000000,0\n"
MF "v[148:151],v[188:191],a[24:27],v[148:151]\n"
"s_bfe_u32 s62,s22,0x30014\n"
MF "v[148:151],a[240:243],a[64:67],v[148:151]\n"
"s_add_u32 s62,s62,3\n"
"s_and_b32 s62,s62,7\n"
"s_mul_i32 s62,s62,204800\n"
MF "v[148:151],a[244:247],a[104:107],v[148:151]\n"
"s_add_u32 s62,s62,s73\n"
"s_add_u32 s62,s62,122880\n"
"s_or_b32 s62,s62,s16\n"
MF "v[140:143],a[244:247],a[100:103],v[140:143]\n"
LD "v[56:59],v9,s[40:43],s62 offen sc1\n"
MF "v[140:143],a[240:243],a[60:63],v[140:143]\n"
LD "v[64:67],v9,s[40:43],s62 offen offset:1024 sc1\n"
MF "v[140:143],v[188:191],a[20:23],v[140:143]\n"
"s_waitcnt lgkmcnt(0)\n"
LD "v[72:75],v9,s[40:43],s62 offen offset:2048 sc1\n"
MF "v[136:139],v[176:179],a[140:143],v[136:139]\n"
"s_nop 0\n"
TR "v[188:189],v7\n"
TR "v[190:191],v7 offset:2048\n"
MF "v[136:139],v[180:183],a[180:183],v[136:139]\n"
"s_nop 0\n"
TR "a[240:241],v7 offset:4096\n"
TR "a[242:243],v7 offset:6144\n"
MF "v[136:139],v[184:187],a[220:223],v[136:139]\n"
"s_nop 0\n"
TR "a[244:245],v7 offset:8192\n"
TR "a[246:247],v7 offset:10240\n"
MF "v[144:147],v[184:187],a[224:227],v[144:147]\n"
LD "v[80:83],v9,s[40:43],s62 offen offset:3072 sc1\n"
MF "v[144:147],v[180:183],a[184:187],v[144:147]\n"
MF "v[144:147],v[176:179],a[144:147],v[144:147]\n"
"s_add_u32 s62,s62,4096\n"
MF "v[152:155],v[176:179],a[148:151],v[152:155]\n"
LD "v[88:91],v9,s[40:43],s62 offen sc1\n"
MF "v[152:155],v[180:183],a[188:191],v[152:155]\n"
"s_bfe_u32 s62,s22,0x30014\n"
"ds_write_addtid_b32 v136\n"
"ds_write_addtid_b32 v137 offset:256\n"
MF "v[152:155],v[184:187],a[228:231],v[152:155]\n"
"s_add_u32 s62,s62,2\n"
"s_and_b32 s62,s62,7\n"
"s_mul_i32 s62,s62,204800\n"
MF "v[160:163],v[184:187],a[232:235],v[160:163]\n"
"s_add_u32 s62,s62,s73\n"
"s_add_u32 s62,s62,143360\n"
"s_or_b32 s62,s62,s17\n"
MF "v[160:163],v[180:183],a[192:195],v[160:163]\n"
LD "v[60:63],v9,s[40:43],s62 offen sc1\n"
MF "v[160:163],v[176:179],a[152:155],v[160:163]\n"
"s_nop 0\n"
"ds_write_addtid_b32 v138 offset:512\n"
"ds_write_addtid_b32 v139 offset:768\n"
MF "v[168:171],v[176:179],a[156:159],v[168:171]\n"
LD "v[68:71],v9,s[40:43],s62 offen offset:1024 sc1\n"
MF "v[168:171],v[180:183],a[196:199],v[168:171]\n"
"s_nop 0\n"
"ds_write_addtid_b32 v144 offset:2048\n"
"ds_write_addtid_b32 v145 offset:2304\n"
MF "v[168:171],v[184:187],a[236:239],v[168:171]\n"
"s_waitcnt lgkmcnt(6)\n"
LD "v[76:79],v9,s[40:43],s62 offen offset:2048 sc1\n"
MF "v[140:143],v[188:191],a[140:143],v[140:143]\n"
"s_nop 0\n"
TR "v[176:177],v4\n"
TR "v[178:179],v4 offset:2048\n"
MF "v[140:143],a[240:243],a[180:183],v[140:143]\n"
"s_nop 0\n"
TR "v[180:181],v4 offset:4096\n"
TR "v[182:183],v4 offset:6144\n"
MF "v[140:143],a[244:247],a[220:223],v[140:143]\n"
"s_nop 0\n"
TR "v[184:185],v4 offset:8192\n"
TR "v[186:187],v4 offset:10240\n"
MF "v[148:151],a[244:247],a[224:227],v[148:151]\n"
LD "v[84:87],v9,s[40:43],s62 offen offset:3072 sc1\n"
MF "v[148:151],a[240:243],a[184:187],v[148:151]\n"
"s_nop 0\n"
"ds_write_addtid_b32 v146 offset:2560\n"
"ds_write_addtid_b32 v147 offset:2816\n"
MF "v[148:151],v[188:191],a[144:147],v[148:151]\n"
"s_add_u32 s62,s62,4096\n"
"ds_write_addtid_b32 v152 offset:4096\n"
"ds_write_addtid_b32 v153 offset:4352\n"
MF "v[156:159],v[188:191],a[148:151],v[156:159]\n"
LD "v[92:95],v9,s[40:43],s62 offen sc1\n"
MF "v[156:159],a[240:243],a[188:191],v[156:159]\n"
"s_nop 0\n"
"ds_write_addtid_b32 v154 offset:4608\n"
"ds_write_addtid_b32 v155 offset:4864\n"
MF "v[156:159],a[244:247],a[228:231],v[156:159]\n"
"s_nop 0\n"
"ds_write_addtid_b32 v160 offset:6144\n"
"ds_write_addtid_b32 v161 offset:6400\n"
MF "v[164:167],a[244:247],a[232:235],v[164:167]\n"
"s_nop 0\n"
"ds_write_addtid_b32 v162 offset:6656\n"
"ds_write_addtid_b32 v163 offset:6912\n"
MF "v[164:167],a[240:243],a[192:195],v[164:167]\n"
"s_nop 0\n"
"ds_write_addtid_b32 v168 offset:8192\n"
"ds_write_addtid_b32 v169 offset:8448\n"
MF "v[164:167],v[188:191],a[152:155],v[164:167]\n"
"buffer_load_dword v10,v12,s[40:43],s86 offen sc1\n"
MF "v[172:175],v[188:191],a[156:159],v[172:175]\n"
"s_nop 0\n"
"ds_write_addtid_b32 v170 offset:8704\n"
"ds_write_addtid_b32 v171 offset:8960\n"
MF "v[172:175],a[240:243],a[196:199],v[172:175]\n"
"s_nop 0\n"
"ds_write_addtid_b32 v140 offset:1024\n"
"ds_write_addtid_b32 v141 offset:1280\n"
MF "v[172:175],a[244:247],a[236:239],v[172:175]\n"
"s_waitcnt lgkmcnt(15)\n"
"ds_write_addtid_b32 v142 offset:1536\n"
"ds_write_addtid_b32 v143 offset:1792\n"
MF "v[96:99],v[176:179],a[140:143],v[96:99]\n"
"s_nop 0\n"
TR "v[188:189],v5\n"
TR "v[190:191],v5 offset:2048\n"
MF "v[96:99],v[180:183],a[180:183],v[96:99]\n"
"s_nop 0\n"
TR "a[240:241],v5 offset:4096\n"
TR "a[242:243],v5 offset:6144\n"
MF "v[96:99],v[184:187],a[220:223],v[96:99]\n"
"s_nop 0\n"
TR "a[244:245],v5 offset:8192\n"
TR "a[246:247],v5 offset:10240\n"
MF "v[104:107],v[184:187],a[224:227],v[104:107]\n"
"s_nop 0\n"
"ds_write_addtid_b32 v148 offset:3072\n"
"ds_write_addtid_b32 v149 offset:3328\n"
MF "v[104:107],v[180:183],a[184:187],v[104:107]\n"
"s_nop 0\n"
"ds_write_addtid_b32 v150 offset:3584\n"
"ds_write_addtid_b32 v151 offset:3840\n"
MF "v[104:107],v[176:179],a[144:147],v[104:107]\n"
"s_nop 0\n"
"ds_write_addtid_b32 v156 offset:5120\n"
"ds_write_addtid_b32 v157 offset:5376\n"
MF "v[112:115],v[176:179],a[148:151],v[112:115]\n"
"s_nop 0\n"
"ds_write_addtid_b32 v158 offset:5632\n"
"ds_write_addtid_b32 v159 offset:5888\n"
MF "v[112:115],v[180:183],a[188:191],v[112:115]\n"
"ds_write_addtid_b32 v164 offset:7168\n"
MF "v[112:115],v[184:187],a[228:231],v[112:115]\n"
"ds_write_addtid_b32 v165 offset:7424\n"
MF "v[120:123],v[184:187],a[232:235],v[120:123]\n"
"ds_write_addtid_b32 v166 offset:7680\n"
MF "v[120:123],v[180:183],a[192:195],v[120:123]\n"
"ds_write_addtid_b32 v167 offset:7936\n"
MF "v[120:123],v[176:179],a[152:155],v[120:123]\n"
"ds_write_addtid_b32 v172 offset:9216\n"
MF "v[128:131],v[176:179],a[156:159],v[128:131]\n"
"ds_write_addtid_b32 v173 offset:9472\n"
MF "v[128:131],v[180:183],a[196:199],v[128:131]\n"
"ds_write_addtid_b32 v174 offset:9728\n"
MF "v[128:131],v[184:187],a[236:239],v[128:131]\n"
"s_waitcnt lgkmcnt(15)\n"
"ds_write_addtid_b32 v175 offset:9984\n"
MF "v[100:103],v[188:191],a[140:143],v[100:103]\n"
MF "v[100:103],a[240:243],a[180:183],v[100:103]\n"
MF "v[100:103],a[244:247],a[220:223],v[100:103]\n"
MF "v[108:111],a[244:247],a[224:227],v[108:111]\n"
MF "v[108:111],a[240:243],a[184:187],v[108:111]\n"
"s_waitcnt lgkmcnt(0)\n"
"s_barrier\n"
MF "v[108:111],v[188:191],a[144:147],v[108:111]\n"
"ds_read2st64_b32 v[16:17],v13 offset0:192 offset1:193\n"
MF "v[116:119],v[188:191],a[148:151],v[116:119]\n"
"ds_read2st64_b32 v[18:19],v13 offset0:194 offset1:195\n"
MF "v[116:119],a[240:243],a[188:191],v[116:119]\n"
"ds_read2st64_b32 v[20:21],v13 offset0:196 offset1:197\n"
MF "v[116:119],a[244:247],a[228:231],v[116:119]\n"
"ds_read2st64_b32 v[22:23],v13 offset0:198 offset1:199\n"
MF "v[124:127],a[244:247],a[232:235],v[124:127]\n"
"ds_read2st64_b32 v[24:25],v13 offset0:200 offset1:201\n"
MF "v[124:127],a[240:243],a[192:195],v[124:127]\n"
"ds_read2st64_b32 v[26:27],v13 offset0:202 offset1:203\n"
MF "v[124:127],v[188:191],a[152:155],v[124:127]\n"
"ds_read2st64_b32 v[28:29],v13 offset0:204 offset1:205\n"
MF "v[132:135],v[188:191],a[156:159],v[132:135]\n"
"ds_read2st64_b32 v[30:31],v13 offset0:206 offset1:207\n"
MF "v[132:135],a[240:243],a[196:199],v[132:135]\n"
"ds_read2st64_b32 v[32:33],v13 offset0:208 offset1:209\n"
MF "v[132:135],a[244:247],a[236:239],v[132:135]\n"
"ds_read2st64_b32 v[34:35],v13 offset0:210 offset1:211\n"
"ds_read2st64_b32 v[36:37],v13 offset0:212 offset1:213\n"
"ds_read2st64_b32 v[38:39],v13 offset0:214 offset1:215\n"
"s_nop 0\n"
"ds_read2st64_b32 v[40:41],v13 offset0:216 offset1:217\n"
"ds_read2st64_b32 v[42:43],v13 offset0:218 offset1:219\n"
"ds_read2st64_b32 v[44:45],v13 offset0:220 offset1:221\n"
"ds_read2st64_b32 v[46:47],v13 offset0:222 offset1:223\n"
"ds_read2st64_b32 v[48:49],v13 offset0:224 offset1:225\n"
"ds_read2st64_b32 v[50:51],v13 offset0:226 offset1:227\n"
"ds_read2st64_b32 v[52:53],v13 offset0:228 offset1:229\n"
"ds_read2st64_b32 v[54:55],v13 offset0:230 offset1:231\n"
"s_waitcnt vmcnt(0)\n"
"v_sub_u32 v10,v10,v11\n"
"s_cmp_eq_u32 s16,0\n"
"s_cbranch_scc1 .Lhave13\n"
"s_add_u32 s59,s78,6\n"
"s_and_b32 s59,s59,15\n"
"s_nop 3\n"
".Lnf7:\n"
"v_readlane_b32 s58,v10,s59\n"
"s_cmp_lt_u32 s58,9\n"
"s_cbranch_scc1 .Lsp7\n"
".Lnb7:\n"
"s_bfe_u32 s62,s22,0x30014\n"
"s_add_u32 s62,s62,3\n"
"s_and_b32 s62,s62,7\n"
"s_mul_i32 s62,s62,204800\n"
"s_add_u32 s62,s62,s73\n"
"s_add_u32 s62,s62,122880\n"
LD "v[56:59],v9,s[40:43],s62 offen sc1\n"
LD "v[64:67],v9,s[40:43],s62 offen offset:1024 sc1\n"
LD "v[72:75],v9,s[40:43],s62 offen offset:2048 sc1\n"
LD "v[80:83],v9,s[40:43],s62 offen offset:3072 sc1\n"
"s_add_u32 s62,s62,4096\n"
LD "v[88:91],v9,s[40:43],s62 offen sc1\n"
".Lhave13:\n"
"s_cmp_eq_u32 s17,0\n"
"s_cbranch_scc1 .Lhave12\n"
"s_add_u32 s59,s78,4\n"
"s_and_b32 s59,s59,15\n"
"s_nop 3\n"
".Lnf8:\n"
"v_readlane_b32 s58,v10,s59\n"
"s_cmp_lt_u32 s58,10\n"
"s_cbranch_scc1 .Lsp8\n"
".Lnb8:\n"
"s_bfe_u32 s62,s22,0x30014\n"
"s_add_u32 s62,s62,2\n"
"s_and_b32 s62,s62,7\n"
"s_mul_i32 s62,s62,204800\n"
"s_add_u32 s62,s62,s73\n"
"s_add_u32 s62,s62,143360\n"
LD "v[60:63],v9,s[40:43],s62 offen sc1\n"
LD "v[68:71],v9,s[40:43],s62 offen offset:1024 sc1\n"
LD "v[76:79],v9,s[40:43],s62 offen offset:2048 sc1\n"
LD "v[84:87],v9,s[40:43],s62 offen offset:3072 sc1\n"
"s_add_u32 s62,s62,4096\n"
LD "v[92:95],v9,s[40:43],s62 offen sc1\n"
".Lhave12:\n"
".Lnf9:\n"
"v_readlane_b32 s58,v10,s72\n"
"s_cmp_lt_u32 s58,11\n"
"s_cbranch_scc1 .Lsp9\n"
".Lnb9:\n"
"s_bfe_u32 s62,s22,0x30014\n"
"s_add_u32 s62,s62,1\n"
"s_and_b32 s62,s62,7\n"
"s_mul_i32 s62,s62,204800\n"
"s_add_u32 s62,s62,s73\n"
"s_add_u32 s62,s62,163840\n"
LD "v[208:211],v9,s[40:43],s62 offen sc1\n"
LD "v[212:215],v9,s[40:43],s62 offen offset:1024 sc1\n"
LD "v[216:219],v9,s[40:43],s62 offen offset:2048 sc1\n"
LD "v[220:223],v9,s[40:43],s62 offen offset:3072 sc1\n"
"s_add_u32 s62,s62,4096\n"
LD "v[224:227],v9,s[40:43],s62 offen sc1\n"
"s_waitcnt lgkmcnt(15)\n"
"v_pk_add_f32 v[96:97],v[96:97],v[16:17]\n"
"v_pk_add_f32 v[98:99],v[98:99],v[18:19]\n"
"v_pk_add_f32 v[100:101],v[100:101],v[20:21]\n"
"v_pk_add_f32 v[102:103],v[102:103],v[22:23]\n"
"v_pk_add_f32 v[104:105],v[104:105],v[24:25]\n"
"s_waitcnt lgkmcnt(14)\n"
"v_pk_add_f32 v[106:107],v[106:107],v[26:27]\n"
"s_waitcnt lgkmcnt(13)\n"
"v_pk_add_f32 v[108:109],v[108:109],v[28:29]\n"
"s_waitcnt lgkmcnt(12)\n"
"v_pk_add_f32 v[110:111],v[110:111],v[30:31]\n"
"s_waitcnt lgkmcnt(11)\n"
"v_pk_add_f32 v[112:113],v[112:113],v[32:33]\n"
"s_waitcnt lgkmcnt(10)\n"
"v_pk_add_f32 v[114:115],v[114:115],v[34:35]\n"
"s_waitcnt lgkmcnt(9)\n"
"v_pk_add_f32 v[116:117],v[116:117],v[36:37]\n"
"s_waitcnt lgkmcnt(8)\n"
"v_pk_add_f32 v[118:119],v[118:119],v[38:39]\n"
"s_waitcnt lgkmcnt(7)\n"
"v_pk_add_f32 v[120:121],v[120:121],v[40:41]\n"
"s_waitcnt lgkmcnt(6)\n"
"v_pk_add_f32 v[122:123],v[122:123],v[42:43]\n"
"s_waitcnt lgkmcnt(5)\n"
"v_pk_add_f32 v[124:125],v[124:125],v[44:45]\n"
"s_waitcnt lgkmcnt(4)\n"
"v_pk_add_f32 v[126:127],v[126:127],v[46:47]\n"
"s_waitcnt lgkmcnt(3)\n"
"v_pk_add_f32 v[128:129],v[128:129],v[48:49]\n"
"s_waitcnt lgkmcnt(2)\n"
"v_pk_add_f32 v[130:131],v[130:131],v[50:51]\n"
"s_waitcnt lgkmcnt(1)\n"
"v_pk_add_f32 v[132:133],v[132:133],v[52:53]\n"
"s_waitcnt lgkmcnt(0)\n"
"v_pk_add_f32 v[134:135],v[134:135],v[54:55]\n"
"s_waitcnt vmcnt(5)\n"
"s_nop 0\n"
MF "v[100:103],v[56:59],a[252:255],v[100:103]\n"
MF "v[108:111],v[64:67],a[252:255],v[108:111]\n"
MF "v[116:119],v[72:75],a[252:255],v[116:119]\n"
MF "v[124:127],v[80:83],a[252:255],v[124:127]\n"
MF "v[132:135],v[88:91],a[252:255],v[132:135]\n"
MF "v[96:99],v[56:59],a[248:251],v[96:99]\n"
MF "v[104:107],v[64:67],a[248:251],v[104:107]\n"
MF "v[112:115],v[72:75],a[248:251],v[112:115]\n"
MF "v[120:123],v[80:83],a[248:251],v[120:123]\n"
MF "v[128:131],v[88:91],a[248:251],v[128:131]\n"
MF "v[96:99],v[60:63],a[252:255],v[96:99]\n"
MF "v[104:107],v[68:71],a[252:255],v[104:107]\n"
MF "v[112:115],v[76:79],a[252:255],v[112:115]\n"
MF "v[120:123],v[84:87],a[252:255],v[120:123]\n"
MF "v[128:131],v[92:95],a[252:255],v[128:131]\n"
MF "v[100:103],v[60:63],a[248:251],v[100:103]\n"
MF "v[108:111],v[68:71],a[248:251],v[108:111]\n"
MF "v[116:119],v[76:79],a[248:251],v[116:119]\n"
MF "v[124:127],v[84:87],a[248:251],v[124:127]\n"
MF "v[132:135],v[92:95],a[248:251],v[132:135]\n"
"s_waitcnt vmcnt(0)\n"
MF "v[100:103],v[208:211],a[252:255],v[100:103]\n"
MF "v[96:99],v[208:211],a[248:251],v[96:99]\n"
MF "v[108:111],v[212:215],a[252:255],v[108:111]\n"
MF "v[104:107],v[212:215],a[248:251],v[104:107]\n"
MF "v[116:119],v[216:219],a[252:255],v[116:119]\n"
"s_nop 4\n"
CV "v96,v96,v97\n"
CV "v97,v98,v99\n"
MF "v[112:115],v[216:219],a[248:251],v[112:115]\n"
CV "v98,v100,v101\n"
CV "v99,v102,v103\n"
MF "v[124:127],v[220:223],a[252:255],v[124:127]\n"
CV "v104,v104,v105\n"
CV "v105,v106,v107\n"
MF "v[120:123],v[220:223],a[248:251],v[120:123]\n"
CV "v106,v108,v109\n"
CV "v107,v110,v111\n"
"v_permlane16_swap_b32 v96,v98\n"
"v_permlane16_swap_b32 v97,v99\n"
"buffer_store_dwordx4 v[96:99],v242,s[44:47],s87 offen sc1\n"
"s_add_u32 s87,s87,40960\n"
MF "v[132:135],v[224:227],a[252:255],v[132:135]\n"
CV "v112,v112,v113\n"
CV "v113,v114,v115\n"
MF "v[128:131],v[224:227],a[248:251],v[128:131]\n"
CV "v114,v116,v117\n"
CV "v115,v118,v119\n"
"v_permlane16_swap_b32 v104,v106\n"
"v_permlane16_swap_b32 v105,v107\n"
"buffer_store_dwordx4 v[104:107],v242,s[44:47],s87 offen sc1\n"
"s_add_u32 s87,s87,40960\n"
CV "v120,v120,v121\n"
CV "v121,v122,v123\n"
CV "v122,v124,v125\n"
CV "v123,v126,v127\n"
"v_permlane16_swap_b32 v112,v114\n"
"v_permlane16_swap_b32 v113,v115\n"
"buffer_store_dwordx4 v[112:115],v242,s[44:47],s87 offen sc1\n"
"s_add_u32 s87,s87,40960\n"
CV "v128,v128,v129\n"
CV "v129,v130,v131\n"
CV "v130,v132,v133\n"
CV "v131,v134,v135\n"
"v_permlane16_swap_b32 v120,v122\n"
"v_permlane16_swap_b32 v121,v123\n"
"buffer_store_dwordx4 v[120:123],v242,s[44:47],s87 offen sc1\n"
"s_add_u32 s87,s87,40960\n"
"v_permlane16_swap_b32 v128,v130\n"
"v_permlane16_swap_b32 v129,v131\n"
"buffer_store_dwordx4 v[128:131],v242,s[44:47],s87 offen sc1\n"
"s_add_u32 s87,s87,40960\n"
"s_branch .Ljobs\n"
".Ljobs:\n"
"s_cmp_ge_u32 s92,40\n"
"s_cbranch_scc1 .Ldone\n"
"buffer_load_dword v10,v12,s[40:43],s86 offen sc1\n"
"s_mul_i32 s95,s92,3277\n"
"s_lshr_b32 s95,s95,16\n"
"s_mul_i32 s59,s95,20\n"
"s_sub_u32 s59,s92,s59\n"
"s_cmp_ge_u32 s59,10\n"
"s_cselect_b32 s96,4294557760,0\n"
"s_add_u32 s96,s96,1024\n"
"s_mul_i32 s59,s59,40960\n"
"s_add_u32 s96,s96,s59\n"
"s_lshl_b32 s59,s95,7\n"
"s_add_u32 s96,s96,s59\n"
"s_add_u32 s95,s95,3\n"
"s_waitcnt vmcnt(0)\n"
".Ljtry:\n"
"s_cmp_eq_u32 s98,0\n"
"s_cselect_b32 s95,0,s95\n"
"v_sub_u32 v10,v10,v11\n"
"v_cmp_gt_u32 vcc,s95,v10\n"
"s_lshl_b32 s82,s92,10\n"
"s_add_u32 s82,s82,s93\n"
"s_cmp_eq_u64 vcc,0\n"
"s_cselect_b32 s82,s82,0xf0000000\n"
"s_cselect_b32 s84,s96,0xf0000000\n"
"s_cselect_b32 s59,32,0\n"
"s_add_u32 s92,s92,s59\n"
"s_cmp_eq_u32 s59,0\n"
"s_cbranch_scc0 .Ljgo\n"
"s_sub_u32 s98,s98,1\n"
"s_sleep 2\n"
"s_branch .Ljobs\n"
".Ljgo:\n"
"s_mov_b32 s83,s84\n"
LD "v[208:211],v9,s[40:43],s82 offen sc1\n"
"s_add_u32 s82,s82,s71\n"
LD "v[212:215],v9,s[40:43],s82 offen sc1\n"
"s_add_u32 s82,s82,s71\n"
LD "v[216:219],v9,s[40:43],s82 offen sc1\n"
"s_add_u32 s82,s82,s71\n"
LD "v[220:223],v9,s[40:43],s82 offen sc1\n"
"s_add_u32 s82,s82,s71\n"
LD "v[224:227],v9,s[40:43],s82 offen sc1\n"
"s_add_u32 s82,s82,s66\n"
LD "v[228:231],v9,s[40:43],s82 offen sc1\n"
"s_add_u32 s82,s82,s71\n"
LD "v[232:235],v9,s[40:43],s82 offen sc1\n"
"s_add_u32 s82,s82,s71\n"
LD "v[236:239],v9,s[40:43],s82 offen sc1\n"
"buffer_load_dword v10,v12,s[40:43],s86 offen sc1\n"
"s_waitcnt vmcnt(0)\n"
".Ljfin:\n"
"s_nop 1\n"
"v_mov_b32 v240,v208\n"
"v_mov_b32 v241,v209\n"
MF "v[248:251],v[210:213],a[250:253],0\n"
MF "v[244:247],v[214:217],a[250:253],0\n"
MF "v[248:251],v[218:221],a[250:253],v[248:251]\n"
MF "v[244:247],v[222:225],a[250:253],v[244:247]\n"
MF "v[248:251],v[226:229],a[250:253],v[248:251]\n"
MF "v[244:247],v[230:233],a[250:253],v[244:247]\n"
MF "v[248:251],v[234:237],a[250:253],v[248:251]\n"
MF "v[244:247],v[238:241],a[250:253],v[244:247]\n"
"s_nop 7\n"
CV "v244,v244,v245\n"
CV "v245,v246,v247\n"
CV "v246,v248,v249\n"
CV "v247,v250,v251\n"
"s_nop 0\n"
"v_permlane16_swap_b32 v244,v246\n"
"v_permlane16_swap_b32 v245,v247\n"
"buffer_store_dwordx4 v[244:247],v8,s[44:47],s83 offen sc1\n"
"s_cmp_ge_u32 s92,40\n"
"s_cbranch_scc1 .Ldone\n"
"s_mul_i32 s95,s92,3277\n"
"s_lshr_b32 s95,s95,16\n"
"s_mul_i32 s59,s95,20\n"
"s_sub_u32 s59,s92,s59\n"
"s_cmp_ge_u32 s59,10\n"
"s_cselect_b32 s96,4294557760,0\n"
"s_add_u32 s96,s96,1024\n"
"s_mul_i32 s59,s59,40960\n"
"s_add_u32 s96,s96,s59\n"
"s_lshl_b32 s59,s95,7\n"
"s_add_u32 s96,s96,s59\n"
"s_add_u32 s95,s95,3\n"
"s_branch .Ljtry\n"
".Lsp0:\n"
"s_cmp_eq_u32 s98,0\n"
"s_cbranch_scc1 .Lnb0\n"
"s_sub_u32 s98,s98,1\n"
"s_sleep 2\n"
"buffer_load_dword v10,v12,s[40:43],s86 offen sc1\n"
"s_waitcnt vmcnt(0)\n"
"v_sub_u32 v10,v10,v11\n"
"s_nop 1\n"
"s_branch .Lnf0\n"
".Lsp1:\n"
"s_cmp_eq_u32 s98,0\n"
"s_cbranch_scc1 .Lnb1\n"
"s_sub_u32 s98,s98,1\n"
"s_sleep 2\n"
"buffer_load_dword v10,v12,s[40:43],s86 offen sc1\n"
"s_waitcnt vmcnt(0)\n"
"v_sub_u32 v10,v10,v11\n"
"s_nop 1\n"
"s_branch .Lnf1\n"
".Lsp2:\n"
"s_cmp_eq_u32 s98,0\n"
"s_cbranch_scc1 .Lnb2\n"
"s_sub_u32 s98,s98,1\n"
"s_sleep 2\n"
"buffer_load_dword v10,v12,s[40:43],s86 offen sc1\n"
"s_waitcnt vmcnt(0)\n"
"v_sub_u32 v10,v10,v11\n"
"s_nop 1\n"
"s_branch .Lnf2\n"
".Lsp3:\n"
"s_cmp_eq_u32 s98,0\n"
"s_cbranch_scc1 .Lnb3\n"
"s_sub_u32 s98,s98,1\n"
"s_sleep 2\n"
"buffer_load_dword v10,v12,s[40:43],s86 offen sc1\n"
"s_waitcnt vmcnt(0)\n"
"v_sub_u32 v10,v10,v11\n"
"s_nop 1\n"
"s_branch .Lnf3\n"
".Lsp4:\n"
"s_cmp_eq_u32 s98,0\n"
"s_cbranch_scc1 .Lnb4\n"
"s_sub_u32 s98,s98,1\n"
"s_sleep 2\n"
"buffer_load_dword v10,v12,s[40:43],s86 offen sc1\n"
"s_waitcnt vmcnt(0)\n"
"v_sub_u32 v10,v10,v11\n"
"s_nop 1\n"
"s_branch .Lnf4\n"
".Lsp5:\n"
"s_cmp_eq_u32 s98,0\n"
"s_cbranch_scc1 .Lnb5\n"
"s_sub_u32 s98,s98,1\n"
"s_sleep 2\n"
"buffer_load_dword v10,v12,s[40:43],s86 offen sc1\n"
"s_waitcnt vmcnt(0)\n"
"v_sub_u32 v10,v10,v11\n"
"s_nop 1\n"
"s_branch .Lnf5\n"
".Lsp6:\n"
"s_cmp_eq_u32 s98,0\n"
"s_cbranch_scc1 .Lnb6\n"
"s_sub_u32 s98,s98,1\n"
"s_sleep 2\n"
"buffer_load_dword v10,v12,s[40:43],s86 offen sc1\n"
"s_waitcnt vmcnt(0)\n"
"v_sub_u32 v10,v10,v11\n"
"s_nop 1\n"
"s_branch .Lnf6\n"
".Lsp7:\n"
"s_cmp_eq_u32 s98,0\n"
"s_cbranch_scc1 .Lnb7\n"
"s_sub_u32 s98,s98,1\n"
"s_sleep 2\n"
"buffer_load_dword v10,v12,s[40:43],s86 offen sc1\n"
"s_waitcnt vmcnt(0)\n"
"v_sub_u32 v10,v10,v11\n"
"s_nop 1\n"
"s_branch .Lnf7\n"
".Lsp8:\n"
"s_cmp_eq_u32 s98,0\n"
"s_cbranch_scc1 .Lnb8\n"
"s_sub_u32 s98,s98,1\n"
"s_sleep 2\n"
"buffer_load_dword v10,v12,s[40:43],s86 offen sc1\n"
"s_waitcnt vmcnt(0)\n"
"v_sub_u32 v10,v10,v11\n"
"s_nop 1\n"
"s_branch .Lnf8\n"
".Lsp9:\n"
"s_cmp_eq_u32 s98,0\n"
"s_cbranch_scc1 .Lnb9\n"
"s_sub_u32 s98,s98,1\n"
"s_sleep 2\n"
"buffer_load_dword v10,v12,s[40:43],s86 offen sc1\n"
"s_waitcnt vmcnt(0)\n"
"v_sub_u32 v10,v10,v11\n"
"s_nop 1\n"
"s_branch .Lnf9\n"
".Lqy:\n"
MF "v[216:219],a[216:219],a[60:63],v[216:219]\n"
TR "a[208:209],v0 offset:8\n"
MF "v[216:219],a[220:223],a[100:103],v[216:219]\n"
TR "a[210:211],v0 offset:520\n"
MF "v[224:227],a[220:223],a[104:107],v[224:227]\n"
MF "v[224:227],a[216:219],a[64:67],v[224:227]\n"
TR "a[212:213],v0 offset:4104\n"
MF "v[232:235],a[216:219],a[68:71],v[232:235]\n"
TR "a[214:215],v0 offset:4616\n"
MF "v[232:235],a[220:223],a[108:111],v[232:235]\n"
MF "v[240:243],a[220:223],a[112:115],v[240:243]\n"
MF "v[240:243],a[216:219],a[72:75],v[240:243]\n"
MF "v[248:251],a[216:219],a[76:79],v[248:251]\n"
MF "v[248:251],a[220:223],a[116:119],v[248:251]\n"
MF "v[248:251],a[200:203],a[156:159],v[248:251]\n"
TR "a[216:217],v0 offset:16392\n"
MF "v[248:251],a[204:207],a[196:199],v[248:251]\n"
TR "a[218:219],v0 offset:16904\n"
MF "v[240:243],a[204:207],a[192:195],v[240:243]\n"
MF "v[240:243],a[200:203],a[152:155],v[240:243]\n"
TR "a[220:221],v0 offset:20488\n"
MF "v[232:235],a[200:203],a[148:151],v[232:235]\n"
TR "a[222:223],v0 offset:21000\n"
MF "v[232:235],a[204:207],a[188:191],v[232:235]\n"
MF "v[224:227],a[204:207],a[184:187],v[224:227]\n"
MF "v[224:227],a[200:203],a[144:147],v[224:227]\n"
"v_fma_f32 v248,v248,s54,v14\n"
"v_fma_f32 v249,v249,s54,v14\n"
MF "v[216:219],a[200:203],a[140:143],v[216:219]\n"
"v_fma_f32 v250,v250,s54,v14\n"
"v_fma_f32 v251,v251,s54,v14\n"
MF "v[216:219],a[204:207],a[180:183],v[216:219]\n"
"s_waitcnt lgkmcnt(4)\n"
"v_fma_f32 v252,v252,s54,v14\n"
"v_fma_f32 v253,v253,s54,v14\n"
MF "v[180:183],a[208:211],a[60:63],v[180:183]\n"
TR "a[200:201],v0\n"
"v_fma_f32 v254,v254,s54,v14\n"
MF "v[180:183],a[212:215],a[100:103],v[180:183]\n"
TR "a[202:203],v0 offset:512\n"
"v_fma_f32 v255,v255,s54,v14\n"
MF "v[188:191],a[212:215],a[104:107],v[188:191]\n"
CV "v255,v254,v255\n"
CV "v254,v252,v253\n"
MF "v[188:191],a[208:211],a[64:67],v[188:191]\n"
TR "a[204:205],v0 offset:4096\n"
CV "v253,v250,v251\n"
MF "v[196:199],a[208:211],a[68:71],v[196:199]\n"
TR "a[206:207],v0 offset:4608\n"
CV "v252,v248,v249\n"
MF "v[196:199],a[212:215],a[108:111],v[196:199]\n"
"s_add_u32 s58,s75,4096\n"
"v_fma_f32 v240,v240,s54,v14\n"
"v_fma_f32 v241,v241,s54,v14\n"
MF "v[204:207],a[212:215],a[112:115],v[204:207]\n"
"buffer_store_dwordx4 v[252:255],v9,s[40:43],s58 offen offset:1024 sc1\n"
MF "v[204:207],a[208:211],a[72:75],v[204:207]\n"
"v_fma_f32 v242,v242,s54,v14\n"
"v_fma_f32 v243,v243,s54,v14\n"
MF "v[212:215],a[208:211],a[76:79],v[212:215]\n"
"v_fma_f32 v244,v244,s54,v14\n"
"v_fma_f32 v245,v245,s54,v14\n"
MF "v[212:215],a[212:215],a[116:119],v[212:215]\n"
"s_waitcnt lgkmcnt(4)\n"
"v_fma_f32 v246,v246,s54,v14\n"
"v_fma_f32 v247,v247,s54,v14\n"
MF "v[180:183],a[216:219],a[140:143],v[180:183]\n"
TR "a[208:209],v0 offset:16384\n"
CV "v251,v246,v247\n"
MF "v[180:183],a[220:223],a[180:183],v[180:183]\n"
TR "a[210:211],v0 offset:16896\n"
CV "v250,v244,v245\n"
MF "v[188:191],a[220:223],a[184:187],v[188:191]\n"
CV "v249,v242,v243\n"
CV "v248,v240,v241\n"
MF "v[188:191],a[216:219],a[144:147],v[188:191]\n"
TR "a[212:213],v0 offset:20480\n"
"v_fma_f32 v232,v232,s54,v14\n"
MF "v[196:199],a[216:219],a[148:151],v[196:199]\n"
TR "a[214:215],v0 offset:20992\n"
"v_fma_f32 v233,v233,s54,v14\n"
MF "v[196:199],a[220:223],a[188:191],v[196:199]\n"
"buffer_store_dwordx4 v[248:251],v9,s[40:43],s58 offen sc1\n"
MF "v[204:207],a[220:223],a[192:195],v[204:207]\n"
"v_fma_f32 v234,v234,s54,v14\n"
"v_fma_f32 v235,v235,s54,v14\n"
MF "v[204:207],a[216:219],a[152:155],v[204:207]\n"
"v_fma_f32 v236,v236,s54,v14\n"
"v_fma_f32 v237,v237,s54,v14\n"
MF "v[212:215],a[216:219],a[156:159],v[212:215]\n"
"v_fma_f32 v238,v238,s54,v14\n"
"v_fma_f32 v239,v239,s54,v14\n"
MF "v[212:215],a[220:223],a[196:199],v[212:215]\n"
"s_waitcnt vmcnt(10) lgkmcnt(0)\n"
"s_barrier\n"
MF "v[176:179],a[200:203],a[60:63],v[176:179]\n"
"s_add_u32 s66,s67,4718592\n"
TR "a[216:217],v1 offset:32776\n"
CV "v247,v238,v239\n"
MF "v[176:179],a[204:207],a[100:103],v[176:179]\n"
"s_sub_u32 s68,s68,384\n"
TR "a[218:219],v1 offset:33288\n"
CV "v246,v236,v237\n"
MF "v[184:187],a[204:207],a[104:107],v[184:187]\n"
"s_mov_b32 s83,7077888\n"
CV "v245,v234,v235\n"
CV "v244,v232,v233\n"
MF "v[184:187],a[200:203],a[64:67],v[184:187]\n"
"s_add_u32 m0,s62,0\n"
TR "a[220:221],v1 offset:36872\n"
"v_fma_f32 v224,v224,s54,v14\n"
MF "v[192:195],a[200:203],a[68:71],v[192:195]\n"
"s_add_u32 s82,s66,2359296\n"
TR "a[222:223],v1 offset:37384\n"
"v_fma_f32 v225,v225,s54,v14\n"
MF "v[192:195],a[204:207],a[108:111],v[192:195]\n"
LD "v6,s[28:31],s82 offen lds\n"
MF "v[200:203],a[204:207],a[112:115],v[200:203]\n"
"buffer_store_dwordx4 v[244:247],v9,s[40:43],s75 offen offset:3072 sc1\n"
MF "v[200:203],a[200:203],a[72:75],v[200:203]\n"
"v_fma_f32 v226,v226,s54,v14\n"
"v_fma_f32 v227,v227,s54,v14\n"
MF "v[208:211],a[200:203],a[76:79],v[208:211]\n"
"v_fma_f32 v228,v228,s54,v14\n"
"v_fma_f32 v229,v229,s54,v14\n"
MF "v[208:211],a[204:207],a[116:119],v[208:211]\n"
LD "v11,s[28:31],s82 offen offset:1024 lds\n"
MF "v[208:211],a[208:211],a[156:159],v[208:211]\n"
TR "a[200:201],v1 offset:49160\n"
"v_fma_f32 v230,v230,s54,v14\n"
MF "v[208:211],a[212:215],a[196:199],v[208:211]\n"
TR "a[202:203],v1 offset:49672\n"
"v_fma_f32 v231,v231,s54,v14\n"
MF "v[200:203],a[212:215],a[192:195],v[200:203]\n"
CV "v243,v230,v231\n"
CV "v242,v228,v229\n"
MF "v[200:203],a[208:211],a[152:155],v[200:203]\n"
TR "a[204:205],v1 offset:53256\n"
CV "v241,v226,v227\n"
MF "v[192:195],a[208:211],a[148:151],v[192:195]\n"
TR "a[206:207],v1 offset:53768\n"
CV "v240,v224,v225\n"
MF "v[192:195],a[212:215],a[188:191],v[192:195]\n"
"v_fma_f32 v216,v216,s54,v14\n"
"v_fma_f32 v217,v217,s54,v14\n"
MF "v[184:187],a[212:215],a[184:187],v[184:187]\n"
"buffer_store_dwordx4 v[240:243],v9,s[40:43],s75 offen offset:2048 sc1\n"
MF "v[184:187],a[208:211],a[144:147],v[184:187]\n"
"v_fma_f32 v218,v218,s54,v14\n"
"v_fma_f32 v219,v219,s54,v14\n"
MF "v[176:179],a[208:211],a[140:143],v[176:179]\n"
LD "v12,s[28:31],s82 offen offset:2048 lds\n"
MF "v[176:179],a[212:215],a[180:183],v[176:179]\n"
"s_waitcnt lgkmcnt(4)\n"
"v_fma_f32 v220,v220,s54,v14\n"
"v_fma_f32 v221,v221,s54,v14\n"
MF "v[140:143],a[216:219],a[60:63],v[140:143]\n"
TR "a[208:209],v1 offset:32768\n"
"v_fma_f32 v222,v222,s54,v14\n"
MF "v[140:143],a[220:223],a[100:103],v[140:143]\n"
TR "a[210:211],v1 offset:33280\n"
"v_fma_f32 v223,v223,s54,v14\n"
MF "v[148:151],a[220:223],a[104:107],v[148:151]\n"
CV "v239,v222,v223\n"
CV "v238,v220,v221\n"
MF "v[148:151],a[216:219],a[64:67],v[148:151]\n"
TR "a[212:213],v1 offset:36864\n"
CV "v237,v218,v219\n"
MF "v[156:159],a[216:219],a[68:71],v[156:159]\n"
TR "a[214:215],v1 offset:37376\n"
CV "v236,v216,v217\n"
MF "v[156:159],a[220:223],a[108:111],v[156:159]\n"
"v_fma_f32 v208,v208,s54,v14\n"
"v_fma_f32 v209,v209,s54,v14\n"
MF "v[164:167],a[220:223],a[112:115],v[164:167]\n"
"buffer_store_dwordx4 v[236:239],v9,s[40:43],s75 offen offset:1024 sc1\n"
MF "v[164:167],a[216:219],a[72:75],v[164:167]\n"
LD "v13,s[28:31],s82 offen offset:3072 lds\n"
MF "v[172:175],a[216:219],a[76:79],v[172:175]\n"
"v_fma_f32 v210,v210,s54,v14\n"
"v_fma_f32 v211,v211,s54,v14\n"
MF "v[172:175],a[220:223],a[116:119],v[172:175]\n"
"s_waitcnt lgkmcnt(4)\n"
"v_fma_f32 v212,v212,s54,v14\n"
"v_fma_f32 v213,v213,s54,v14\n"
MF "v[140:143],a[200:203],a[140:143],v[140:143]\n"
TR "a[216:217],v1 offset:49152\n"
"v_fma_f32 v214,v214,s54,v14\n"
MF "v[140:143],a[204:207],a[180:183],v[140:143]\n"
TR "a[218:219],v1 offset:49664\n"
"v_fma_f32 v215,v215,s54,v14\n"
MF "v[148:151],a[204:207],a[184:187],v[148:151]\n"
CV "v235,v214,v215\n"
CV "v234,v212,v213\n"
MF "v[148:151],a[200:203],a[144:147],v[148:151]\n"
TR "a[220:221],v1 offset:53248\n"
CV "v233,v210,v211\n"
MF "v[156:159],a[200:203],a[148:151],v[156:159]\n"
TR "a[222:223],v1 offset:53760\n"
CV "v232,v208,v209\n"
MF "v[156:159],a[204:207],a[188:191],v[156:159]\n"
"v_fma_f32 v200,v200,s54,v14\n"
"v_fma_f32 v201,v201,s54,v14\n"
MF "v[164:167],a[204:207],a[192:195],v[164:167]\n"
"buffer_store_dwordx4 v[232:235],v9,s[40:43],s75 offen sc1\n"
MF "v[164:167],a[200:203],a[152:155],v[164:167]\n"
"v_fma_f32 v202,v202,s54,v14\n"
"v_fma_f32 v203,v203,s54,v14\n"
MF "v[172:175],a[200:203],a[156:159],v[172:175]\n"
"v_fma_f32 v204,v204,s54,v14\n"
"v_fma_f32 v205,v205,s54,v14\n"
MF "v[172:175],a[204:207],a[196:199],v[172:175]\n"
"s_waitcnt lgkmcnt(0)\n"
"v_fma_f32 v206,v206,s54,v14\n"
"v_fma_f32 v207,v207,s54,v14\n"
MF "v[136:139],a[208:211],a[60:63],v[136:139]\n"
"s_add_u32 m0,s62,16384\n"
TR "a[200:201],v0 offset:32776\n"
CV "v231,v206,v207\n"
MF "v[136:139],a[212:215],a[100:103],v[136:139]\n"
"s_add_u32 s82,s66,2359424\n"
TR "a[202:203],v0 offset:33288\n"
CV "v230,v204,v205\n"
MF "v[144:147],a[212:215],a[104:107],v[144:147]\n"
LD "v6,s[28:31],s82 offen lds\n"
MF "v[144:147],a[208:211],a[64:67],v[144:147]\n"
TR "a[204:205],v0 offset:36872\n"
CV "v229,v202,v203\n"
MF "v[152:155],a[208:211],a[68:71],v[152:155]\n"
TR "a[206:207],v0 offset:37384\n"
CV "v228,v200,v201\n"
MF "v[152:155],a[212:215],a[108:111],v[152:155]\n"
AW "a84,v228\n"
AW "a85,v229\n"
MF "v[160:163],a[212:215],a[112:115],v[160:163]\n"
AW "a86,v230\n"
AW "a87,v231\n"
MF "v[160:163],a[208:211],a[72:75],v[160:163]\n"
"v_fma_f32 v192,v192,s54,v14\n"
"v_fma_f32 v193,v193,s54,v14\n"
MF "v[168:171],a[208:211],a[76:79],v[168:171]\n"
"v_fma_f32 v194,v194,s54,v14\n"
"v_fma_f32 v195,v195,s54,v14\n"
MF "v[168:171],a[212:215],a[116:119],v[168:171]\n"
LD "v11,s[28:31],s82 offen offset:1024 lds\n"
MF "v[168:171],a[216:219],a[156:159],v[168:171]\n"
TR "a[208:209],v0 offset:49160\n"
"v_fma_f32 v196,v196,s54,v14\n"
MF "v[168:171],a[220:223],a[196:199],v[168:171]\n"
TR "a[210:211],v0 offset:49672\n"
"v_fma_f32 v197,v197,s54,v14\n"
MF "v[160:163],a[220:223],a[192:195],v[160:163]\n"
"v_fma_f32 v198,v198,s54,v14\n"
"v_fma_f32 v199,v199,s54,v14\n"
MF "v[160:163],a[216:219],a[152:155],v[160:163]\n"
TR "a[212:213],v0 offset:53256\n"
CV "v227,v198,v199\n"
MF "v[152:155],a[216:219],a[148:151],v[152:155]\n"
TR "a[214:215],v0 offset:53768\n"
CV "v226,v196,v197\n"
MF "v[152:155],a[220:223],a[188:191],v[152:155]\n"
CV "v225,v194,v195\n"
CV "v224,v192,v193\n"
MF "v[144:147],a[220:223],a[184:187],v[144:147]\n"
AW "a80,v224\n"
AW "a81,v225\n"
MF "v[144:147],a[216:219],a[144:147],v[144:147]\n"
AW "a82,v226\n"
AW "a83,v227\n"
MF "v[136:139],a[216:219],a[140:143],v[136:139]\n"
LD "v12,s[28:31],s82 offen offset:2048 lds\n"
MF "v[136:139],a[220:223],a[180:183],v[136:139]\n"
"s_waitcnt lgkmcnt(4)\n"
"v_fma_f32 v184,v184,s54,v14\n"
"v_fma_f32 v185,v185,s54,v14\n"
MF "v[100:103],a[200:203],a[60:63],v[100:103]\n"
TR "a[216:217],v0 offset:32768\n"
"v_fma_f32 v186,v186,s54,v14\n"
MF "v[100:103],a[204:207],a[100:103],v[100:103]\n"
TR "a[218:219],v0 offset:33280\n"
"v_fma_f32 v187,v187,s54,v14\n"
MF "v[108:111],a[204:207],a[104:107],v[108:111]\n"
"v_fma_f32 v188,v188,s54,v14\n"
"v_fma_f32 v189,v189,s54,v14\n"
MF "v[108:111],a[200:203],a[64:67],v[108:111]\n"
TR "a[220:221],v0 offset:36864\n"
"v_fma_f32 v190,v190,s54,v14\n"
MF "v[116:119],a[200:203],a[68:71],v[116:119]\n"
TR "a[222:223],v0 offset:37376\n"
"v_fma_f32 v191,v191,s54,v14\n"
MF "v[116:119],a[204:207],a[108:111],v[116:119]\n"
CV "v223,v190,v191\n"
CV "v222,v188,v189\n"
MF "v[124:127],a[204:207],a[112:115],v[124:127]\n"
CV "v221,v186,v187\n"
CV "v220,v184,v185\n"
MF "v[124:127],a[200:203],a[72:75],v[124:127]\n"
LD "v13,s[28:31],s82 offen offset:3072 lds\n"
MF "v[132:135],a[200:203],a[76:79],v[132:135]\n"
AW "a56,v220\n"
AW "a57,v221\n"
MF "v[132:135],a[204:207],a[116:119],v[132:135]\n"
"s_waitcnt lgkmcnt(4)\n"
AW "a58,v222\n"
AW "a59,v223\n"
MF "v[100:103],a[208:211],a[140:143],v[100:103]\n"
TR "a[200:201],v0 offset:49152\n"
"v_fma_f32 v176,v176,s54,v14\n"
MF "v[100:103],a[212:215],a[180:183],v[100:103]\n"
TR "a[202:203],v0 offset:49664\n"
"v_fma_f32 v177,v177,s54,v14\n"
MF "v[108:111],a[212:215],a[184:187],v[108:111]\n"
"v_fma_f32 v178,v178,s54,v14\n"
"v_fma_f32 v179,v179,s54,v14\n"
MF "v[108:111],a[208:211],a[144:147],v[108:111]\n"
TR "a[204:205],v0 offset:53248\n"
"v_fma_f32 v180,v180,s54,v14\n"
MF "v[116:119],a[208:211],a[148:151],v[116:119]\n"
TR "a[206:207],v0 offset:53760\n"
"v_fma_f32 v181,v181,s54,v14\n"
MF "v[116:119],a[212:215],a[188:191],v[116:119]\n"
"v_fma_f32 v182,v182,s54,v14\n"
"v_fma_f32 v183,v183,s54,v14\n"
MF "v[124:127],a[212:215],a[192:195],v[124:127]\n"
"v_add_u32 v0,0xc000,v0\n"
CV "v219,v182,v183\n"
MF "v[124:127],a[208:211],a[152:155],v[124:127]\n"
"v_add_u32 v1,0xc000,v1\n"
CV "v218,v180,v181\n"
MF "v[132:135],a[208:211],a[156:159],v[132:135]\n"
CV "v217,v178,v179\n"
CV "v216,v176,v177\n"
MF "v[132:135],a[212:215],a[196:199],v[132:135]\n"
"s_waitcnt vmcnt(14) lgkmcnt(0)\n"
"s_barrier\n"
MF "v[96:99],a[216:219],a[60:63],v[96:99]\n"
"s_add_u32 m0,s62,32768\n"
TR "a[208:209],v1 offset:16392\n"
AW "a52,v216\n"
MF "v[96:99],a[220:223],a[100:103],v[96:99]\n"
"s_add_u32 s82,s66,2359552\n"
TR "a[210:211],v1 offset:16904\n"
AW "a53,v217\n"
MF "v[104:107],a[220:223],a[104:107],v[104:107]\n"
LD "v6,s[28:31],s82 offen lds\n"
MF "v[104:107],a[216:219],a[64:67],v[104:107]\n"
TR "a[212:213],v1 offset:20488\n"
AW "a54,v218\n"
MF "v[112:115],a[216:219],a[68:71],v[112:115]\n"
TR "a[214:215],v1 offset:21000\n"
AW "a55,v219\n"
MF "v[112:115],a[220:223],a[108:111],v[112:115]\n"
"v_fma_f32 v168,v168,s54,v14\n"
"v_fma_f32 v169,v169,s54,v14\n"
MF "v[120:123],a[220:223],a[112:115],v[120:123]\n"
"v_fma_f32 v170,v170,s54,v14\n"
"v_fma_f32 v171,v171,s54,v14\n"
MF "v[120:123],a[216:219],a[72:75],v[120:123]\n"
"v_fma_f32 v172,v172,s54,v14\n"
"v_fma_f32 v173,v173,s54,v14\n"
MF "v[128:131],a[216:219],a[76:79],v[128:131]\n"
"v_fma_f32 v174,v174,s54,v14\n"
"v_fma_f32 v175,v175,s54,v14\n"
MF "v[128:131],a[220:223],a[116:119],v[128:131]\n"
LD "v11,s[28:31],s82 offen offset:1024 lds\n"
MF "v[128:131],a[200:203],a[156:159],v[128:131]\n"
TR "a[216:217],v1 offset:32776\n"
CV "v215,v174,v175\n"
MF "v[128:131],a[204:207],a[196:199],v[128:131]\n"
TR "a[218:219],v1 offset:33288\n"
CV "v214,v172,v173\n"
MF "v[120:123],a[204:207],a[192:195],v[120:123]\n"
CV "v213,v170,v171\n"
CV "v212,v168,v169\n"
MF "v[120:123],a[200:203],a[152:155],v[120:123]\n"
TR "a[220:221],v1 offset:36872\n"
AW "a48,v212\n"
MF "v[112:115],a[200:203],a[148:151],v[112:115]\n"
TR "a[222:223],v1 offset:37384\n"
AW "a49,v213\n"
MF "v[112:115],a[204:207],a[188:191],v[112:115]\n"
AW "a50,v214\n"
AW "a51,v215\n"
MF "v[104:107],a[204:207],a[184:187],v[104:107]\n"
"v_fma_f32 v160,v160,s54,v14\n"
"v_fma_f32 v161,v161,s54,v14\n"
MF "v[104:107],a[200:203],a[144:147],v[104:107]\n"
"v_fma_f32 v162,v162,s54,v14\n"
"v_fma_f32 v163,v163,s54,v14\n"
MF "v[96:99],a[200:203],a[140:143],v[96:99]\n"
LD "v12,s[28:31],s82 offen offset:2048 lds\n"
MF "v[96:99],a[204:207],a[180:183],v[96:99]\n"
"s_waitcnt lgkmcnt(4)\n"
"v_fma_f32 v164,v164,s54,v14\n"
"v_fma_f32 v165,v165,s54,v14\n"
MF "v[60:63],a[208:211],a[60:63],v[60:63]\n"
TR "a[200:201],v1 offset:16384\n"
"v_fma_f32 v166,v166,s54,v14\n"
MF "v[60:63],a[212:215],a[100:103],v[60:63]\n"
TR "a[202:203],v1 offset:16896\n"
"v_fma_f32 v167,v167,s54,v14\n"
MF "v[68:71],a[212:215],a[104:107],v[68:71]\n"
CV "v211,v166,v167\n"
CV "v210,v164,v165\n"
MF "v[68:71],a[208:211],a[64:67],v[68:71]\n"
TR "a[204:205],v1 offset:20480\n"
CV "v209,v162,v163\n"
MF "v[76:79],a[208:211],a[68:71],v[76:79]\n"
TR "a[206:207],v1 offset:20992\n"
CV "v208,v160,v161\n"
MF "v[76:79],a[212:215],a[108:111],v[76:79]\n"
AW "a44,v208\n"
AW "a45,v209\n"
MF "v[84:87],a[212:215],a[112:115],v[84:87]\n"
AW "a46,v210\n"
AW "a47,v211\n"
MF "v[84:87],a[208:211],a[72:75],v[84:87]\n"
LD "v13,s[28:31],s82 offen offset:3072 lds\n"
MF "v[92:95],a[208:211],a[76:79],v[92:95]\n"
"v_fma_f32 v152,v152,s54,v14\n"
"v_fma_f32 v153,v153,s54,v14\n"
MF "v[92:95],a[212:215],a[116:119],v[92:95]\n"
"s_waitcnt lgkmcnt(4)\n"
"v_fma_f32 v154,v154,s54,v14\n"
"v_fma_f32 v155,v155,s54,v14\n"
MF "v[60:63],a[216:219],a[140:143],v[60:63]\n"
TR "a[208:209],v1 offset:32768\n"
"v_fma_f32 v156,v156,s54,v14\n"
MF "v[60:63],a[220:223],a[180:183],v[60:63]\n"
TR "a[210:211],v1 offset:33280\n"
"v_fma_f32 v157,v157,s54,v14\n"
MF "v[68:71],a[220:223],a[184:187],v[68:71]\n"
"v_fma_f32 v158,v158,s54,v14\n"
"v_fma_f32 v159,v159,s54,v14\n"
MF "v[68:71],a[216:219],a[144:147],v[68:71]\n"
TR "a[212:213],v1 offset:36864\n"
CV "v207,v158,v159\n"
MF "v[76:79],a[216:219],a[148:151],v[76:79]\n"
TR "a[214:215],v1 offset:37376\n"
CV "v206,v156,v157\n"
MF "v[76:79],a[220:223],a[188:191],v[76:79]\n"
CV "v205,v154,v155\n"
CV "v204,v152,v153\n"
MF "v[84:87],a[220:223],a[192:195],v[84:87]\n"
AW "a40,v204\n"
AW "a41,v205\n"
MF "v[84:87],a[216:219],a[152:155],v[84:87]\n"
AW "a42,v206\n"
AW "a43,v207\n"
MF "v[92:95],a[216:219],a[156:159],v[92:95]\n"
"v_fma_f32 v144,v144,s54,v14\n"
"v_fma_f32 v145,v145,s54,v14\n"
MF "v[92:95],a[220:223],a[196:199],v[92:95]\n"
"s_waitcnt lgkmcnt(0)\n"
"v_fma_f32 v146,v146,s54,v14\n"
"v_fma_f32 v147,v147,s54,v14\n"
MF "v[56:59],a[200:203],a[60:63],v[56:59]\n"
TR "a[216:217],v0 offset:16392\n"
"v_fma_f32 v148,v148,s54,v14\n"
MF "v[56:59],a[204:207],a[100:103],v[56:59]\n"
TR "a[218:219],v0 offset:16904\n"
"v_fma_f32 v149,v149,s54,v14\n"
MF "v[64:67],a[204:207],a[104:107],v[64:67]\n"
"v_fma_f32 v150,v150,s54,v14\n"
"v_fma_f32 v151,v151,s54,v14\n"
MF "v[64:67],a[200:203],a[64:67],v[64:67]\n"
TR "a[220:221],v0 offset:20488\n"
CV "v203,v150,v151\n"
MF "v[72:75],a[200:203],a[68:71],v[72:75]\n"
TR "a[222:223],v0 offset:21000\n"
CV "v202,v148,v149\n"
MF "v[72:75],a[204:207],a[108:111],v[72:75]\n"
CV "v201,v146,v147\n"
CV "v200,v144,v145\n"
MF "v[80:83],a[204:207],a[112:115],v[80:83]\n"
AW "a16,v200\n"
AW "a17,v201\n"
MF "v[80:83],a[200:203],a[72:75],v[80:83]\n"
AW "a18,v202\n"
AW "a19,v203\n"
MF "v[88:91],a[200:203],a[76:79],v[88:91]\n"
"v_fma_f32 v136,v136,s54,v14\n"
"v_fma_f32 v137,v137,s54,v14\n"
MF "v[88:91],a[204:207],a[116:119],v[88:91]\n"
"v_fma_f32 v138,v138,s54,v14\n"
"v_fma_f32 v139,v139,s54,v14\n"
MF "v[88:91],a[208:211],a[156:159],v[88:91]\n"
TR "a[200:201],v0 offset:32776\n"
"v_fma_f32 v140,v140,s54,v14\n"
MF "v[88:91],a[212:215],a[196:199],v[88:91]\n"
TR "a[202:203],v0 offset:33288\n"
"v_fma_f32 v141,v141,s54,v14\n"
MF "v[80:83],a[212:215],a[192:195],v[80:83]\n"
"v_fma_f32 v142,v142,s54,v14\n"
"v_fma_f32 v143,v143,s54,v14\n"
MF "v[80:83],a[208:211],a[152:155],v[80:83]\n"
TR "a[204:205],v0 offset:36872\n"
CV "v199,v142,v143\n"
MF "v[72:75],a[208:211],a[148:151],v[72:75]\n"
TR "a[206:207],v0 offset:37384\n"
CV "v198,v140,v141\n"
MF "v[72:75],a[212:215],a[188:191],v[72:75]\n"
CV "v197,v138,v139\n"
CV "v196,v136,v137\n"
MF "v[64:67],a[212:215],a[184:187],v[64:67]\n"
AW "a12,v196\n"
AW "a13,v197\n"
MF "v[64:67],a[208:211],a[144:147],v[64:67]\n"
AW "a14,v198\n"
AW "a15,v199\n"
MF "v[56:59],a[208:211],a[140:143],v[56:59]\n"
"v_fma_f32 v128,v128,s54,v14\n"
"v_fma_f32 v129,v129,s54,v14\n"
MF "v[56:59],a[212:215],a[180:183],v[56:59]\n"
"s_waitcnt lgkmcnt(4)\n"
"v_fma_f32 v130,v130,s54,v14\n"
"v_fma_f32 v131,v131,s54,v14\n"
MF "v[20:23],a[216:219],a[60:63],v[20:23]\n"
TR "a[208:209],v0 offset:16384\n"
"v_fma_f32 v132,v132,s54,v14\n"
MF "v[20:23],a[220:223],a[100:103],v[20:23]\n"
TR "a[210:211],v0 offset:16896\n"
"v_fma_f32 v133,v133,s54,v14\n"
MF "v[28:31],a[220:223],a[104:107],v[28:31]\n"
"v_fma_f32 v134,v134,s54,v14\n"
"v_fma_f32 v135,v135,s54,v14\n"
MF "v[28:31],a[216:219],a[64:67],v[28:31]\n"
TR "a[212:213],v0 offset:20480\n"
CV "v195,v134,v135\n"
MF "v[36:39],a[216:219],a[68:71],v[36:39]\n"
TR "a[214:215],v0 offset:20992\n"
CV "v194,v132,v133\n"
MF "v[36:39],a[220:223],a[108:111],v[36:39]\n"
CV "v193,v130,v131\n"
CV "v192,v128,v129\n"
MF "v[44:47],a[220:223],a[112:115],v[44:47]\n"
AW "a8,v192\n"
AW "a9,v193\n"
MF "v[44:47],a[216:219],a[72:75],v[44:47]\n"
AW "a10,v194\n"
AW "a11,v195\n"
MF "v[52:55],a[216:219],a[76:79],v[52:55]\n"
"v_fma_f32 v120,v120,s54,v14\n"
"v_fma_f32 v121,v121,s54,v14\n"
MF "v[52:55],a[220:223],a[116:119],v[52:55]\n"
"s_waitcnt lgkmcnt(4)\n"
"v_fma_f32 v122,v122,s54,v14\n"
"v_fma_f32 v123,v123,s54,v14\n"
MF "v[20:23],a[200:203],a[140:143],v[20:23]\n"
TR "a[216:217],v0 offset:32768\n"
"v_fma_f32 v124,v124,s54,v14\n"
MF "v[20:23],a[204:207],a[180:183],v[20:23]\n"
TR "a[218:219],v0 offset:33280\n"
"v_fma_f32 v125,v125,s54,v14\n"
MF "v[28:31],a[204:207],a[184:187],v[28:31]\n"
"v_fma_f32 v126,v126,s54,v14\n"
"v_fma_f32 v127,v127,s54,v14\n"
MF "v[28:31],a[200:203],a[144:147],v[28:31]\n"
TR "a[220:221],v0 offset:36864\n"
CV "v191,v126,v127\n"
MF "v[36:39],a[200:203],a[148:151],v[36:39]\n"
TR "a[222:223],v0 offset:37376\n"
CV "v190,v124,v125\n"
MF "v[36:39],a[204:207],a[188:191],v[36:39]\n"
CV "v189,v122,v123\n"
CV "v188,v120,v121\n"
MF "v[44:47],a[204:207],a[192:195],v[44:47]\n"
AW "a4,v188\n"
AW "a5,v189\n"
MF "v[44:47],a[200:203],a[152:155],v[44:47]\n"
AW "a6,v190\n"
AW "a7,v191\n"
MF "v[52:55],a[200:203],a[156:159],v[52:55]\n"
"v_fma_f32 v112,v112,s54,v14\n"
"v_fma_f32 v113,v113,s54,v14\n"
MF "v[52:55],a[204:207],a[196:199],v[52:55]\n"
"s_waitcnt lgkmcnt(0)\n"
"s_barrier\n"
MF "v[16:19],a[208:211],a[60:63],v[16:19]\n"
"v_fma_f32 v114,v114,s54,v14\n"
"v_fma_f32 v115,v115,s54,v14\n"
MF "v[16:19],a[212:215],a[100:103],v[16:19]\n"
"v_fma_f32 v116,v116,s54,v14\n"
"v_fma_f32 v117,v117,s54,v14\n"
MF "v[24:27],a[212:215],a[104:107],v[24:27]\n"
"v_fma_f32 v118,v118,s54,v14\n"
"v_fma_f32 v119,v119,s54,v14\n"
MF "v[24:27],a[208:211],a[64:67],v[24:27]\n"
CV "v187,v118,v119\n"
CV "v186,v116,v117\n"
MF "v[32:35],a[208:211],a[68:71],v[32:35]\n"
CV "v185,v114,v115\n"
CV "v184,v112,v113\n"
MF "v[32:35],a[212:215],a[108:111],v[32:35]\n"
AW "a0,v184\n"
AW "a1,v185\n"
MF "v[40:43],a[212:215],a[112:115],v[40:43]\n"
AW "a2,v186\n"
AW "a3,v187\n"
MF "v[40:43],a[208:211],a[72:75],v[40:43]\n"
"v_fma_f32 v104,v104,s54,v14\n"
"v_fma_f32 v105,v105,s54,v14\n"
MF "v[48:51],a[208:211],a[76:79],v[48:51]\n"
"v_fma_f32 v106,v106,s54,v14\n"
"v_fma_f32 v107,v107,s54,v14\n"
MF "v[48:51],a[212:215],a[116:119],v[48:51]\n"
"v_fma_f32 v108,v108,s54,v14\n"
"v_fma_f32 v109,v109,s54,v14\n"
MF "v[48:51],a[216:219],a[156:159],v[48:51]\n"
"v_fma_f32 v110,v110,s54,v14\n"
"v_fma_f32 v111,v111,s54,v14\n"
MF "v[48:51],a[220:223],a[196:199],v[48:51]\n"
CV "v183,v110,v111\n"
CV "v182,v108,v109\n"
MF "v[40:43],a[220:223],a[192:195],v[40:43]\n"
CV "v181,v106,v107\n"
CV "v180,v104,v105\n"
MF "v[40:43],a[216:219],a[152:155],v[40:43]\n"
AW "a36,v180\n"
AW "a37,v181\n"
MF "v[32:35],a[216:219],a[148:151],v[32:35]\n"
AW "a38,v182\n"
AW "a39,v183\n"
MF "v[32:35],a[220:223],a[188:191],v[32:35]\n"
MF "v[24:27],a[220:223],a[184:187],v[24:27]\n"
MF "v[24:27],a[216:219],a[144:147],v[24:27]\n"
MF "v[16:19],a[216:219],a[140:143],v[16:19]\n"
MF "v[16:19],a[220:223],a[180:183],v[16:19]\n"
"s_mov_b32 s57,1\n"
"s_mov_b32 s69,99\n"
AR "v0,a240\n"
AR "v2,a241\n"
AR "v4,a242\n"
AR "v6,a243\n"
"v_xor_b32 v1,64,v0\n"
"v_xor_b32 v3,64,v2\n"
"v_xor_b32_e64 v5,64,v4\n"
"v_add_u32 v11,97280,v6\n"
"v_add_u32 v12,194560,v6\n"
"v_add_u32 v13,291840,v6\n"
"s_memtime s[52:53]\n"
"s_memrealtime s[100:101]\n"
"s_waitcnt lgkmcnt(0)\n"
"s_sub_u32 s53,s52,s80\n"
"s_cmp_gt_u32 s53,27000\n"
"s_cselect_b32 s101,1,0\n"
"s_cmp_eq_u32 s87,2\n"
"s_cselect_b32 s87,s101,s87\n"
"s_cmp_eq_u32 s87,1\n"
"s_mov_b64 vcc,-1\n"
"s_waitcnt vmcnt(9) lgkmcnt(0)\n"
"s_barrier\n"
"s_nop 0\n"
TR "a[200:201],v0\n"
TR "a[202:203],v0 offset:512\n"
TR "a[204:205],v0 offset:4096\n"
TR "a[206:207],v0 offset:4608\n"
"ds_read_b128 a[60:63],v2\n"
"ds_read_b128 a[100:103],v3\n"
"ds_read_b128 a[64:67],v2 offset:2048\n"
"ds_read_b128 a[104:107],v3 offset:2048\n"
"ds_read_b128 a[68:71],v2 offset:4096\n"
"ds_read_b128 a[108:111],v3 offset:4096\n"
"ds_read_b128 a[72:75],v2 offset:6144\n"
"ds_read_b128 a[112:115],v3 offset:6144\n"
"ds_read_b128 a[76:79],v2 offset:8192\n"
"ds_read_b128 a[116:119],v3 offset:8192\n"
TR "a[208:209],v0 offset:8\n"
TR "a[210:211],v0 offset:520\n"
TR "a[212:213],v0 offset:4104\n"
TR "a[214:215],v0 offset:4616\n"
"v_pk_fma_f32 v[96:97],v[96:97],s[54:55],v[14:15] op_sel_hi:[1,1,0]\n"
"v_pk_fma_f32 v[98:99],v[98:99],s[54:55],v[14:15] op_sel_hi:[1,1,0]\n"
"v_pk_fma_f32 v[100:101],v[100:101],s[54:55],v[14:15] op_sel_hi:[1,1,0]\n"
"v_pk_fma_f32 v[102:103],v[102:103],s[54:55],v[14:15] op_sel_hi:[1,1,0]\n"
CV "v179,v102,v103\n"
CV "v178,v100,v101\n"
CV "v177,v98,v99\n"
CV "v176,v96,v97\n"
AW "a32,v176\n"
AW "a33,v177\n"
AW "a34,v178\n"
AW "a35,v179\n"
"v_pk_fma_f32 v[88:89],v[88:89],s[54:55],v[14:15] op_sel_hi:[1,1,0]\n"
"v_pk_fma_f32 v[90:91],v[90:91],s[54:55],v[14:15] op_sel_hi:[1,1,0]\n"
"v_pk_fma_f32 v[92:93],v[92:93],s[54:55],v[14:15] op_sel_hi:[1,1,0]\n"
"v_pk_fma_f32 v[94:95],v[94:95],s[54:55],v[14:15] op_sel_hi:[1,1,0]\n"
CV "v175,v94,v95\n"
CV "v174,v92,v93\n"
CV "v173,v90,v91\n"
CV "v172,v88,v89\n"
AW "a28,v172\n"
AW "a29,v173\n"
AW "a30,v174\n"
AW "a31,v175\n"
"v_pk_fma_f32 v[80:81],v[80:81],s[54:55],v[14:15] op_sel_hi:[1,1,0]\n"
"v_pk_fma_f32 v[82:83],v[82:83],s[54:55],v[14:15] op_sel_hi:[1,1,0]\n"
"v_pk_fma_f32 v[84:85],v[84:85],s[54:55],v[14:15] op_sel_hi:[1,1,0]\n"
"v_pk_fma_f32 v[86:87],v[86:87],s[54:55],v[14:15] op_sel_hi:[1,1,0]\n"
CV "v171,v86,v87\n"
CV "v170,v84,v85\n"
CV "v169,v82,v83\n"
CV "v168,v80,v81\n"
AW "a24,v168\n"
AW "a25,v169\n"
AW "a26,v170\n"
AW "a27,v171\n"
"v_pk_fma_f32 v[72:73],v[72:73],s[54:55],v[14:15] op_sel_hi:[1,1,0]\n"
"v_pk_fma_f32 v[74:75],v[74:75],s[54:55],v[14:15] op_sel_hi:[1,1,0]\n"
"v_pk_fma_f32 v[76:77],v[76:77],s[54:55],v[14:15] op_sel_hi:[1,1,0]\n"
"v_pk_fma_f32 v[78:79],v[78:79],s[54:55],v[14:15] op_sel_hi:[1,1,0]\n"
CV "v167,v78,v79\n"
CV "v166,v76,v77\n"
CV "v165,v74,v75\n"
CV "v164,v72,v73\n"
AW "a20,v164\n"
AW "a21,v165\n"
AW "a22,v166\n"
AW "a23,v167\n"
"v_pk_fma_f32 v[64:65],v[64:65],s[54:55],v[14:15] op_sel_hi:[1,1,0]\n"
"v_pk_fma_f32 v[66:67],v[66:67],s[54:55],v[14:15] op_sel_hi:[1,1,0]\n"
"v_pk_fma_f32 v[68:69],v[68:69],s[54:55],v[14:15] op_sel_hi:[1,1,0]\n"
"v_pk_fma_f32 v[70:71],v[70:71],s[54:55],v[14:15] op_sel_hi:[1,1,0]\n"
CV "v163,v70,v71\n"
CV "v162,v68,v69\n"
CV "v161,v66,v67\n"
CV "v160,v64,v65\n"
AW "a176,v160\n"
AW "a177,v161\n"
AW "a178,v162\n"
AW "a179,v163\n"
"v_pk_fma_f32 v[56:57],v[56:57],s[54:55],v[14:15] op_sel_hi:[1,1,0]\n"
"v_pk_fma_f32 v[58:59],v[58:59],s[54:55],v[14:15] op_sel_hi:[1,1,0]\n"
"v_pk_fma_f32 v[60:61],v[60:61],s[54:55],v[14:15] op_sel_hi:[1,1,0]\n"
"v_pk_fma_f32 v[62:63],v[62:63],s[54:55],v[14:15] op_sel_hi:[1,1,0]\n"
CV "v159,v62,v63\n"
CV "v158,v60,v61\n"
CV "v157,v58,v59\n"
CV "v156,v56,v57\n"
AW "a172,v156\n"
AW "a173,v157\n"
AW "a174,v158\n"
AW "a175,v159\n"
"v_pk_mul_f32 v[48:49],v[48:49],s[54:55]\n"
"v_pk_mul_f32 v[50:51],v[50:51],s[54:55]\n"
"v_pk_mul_f32 v[52:53],v[52:53],s[54:55]\n"
"v_pk_mul_f32 v[54:55],v[54:55],s[54:55]\n"
CV "v155,v54,v55\n"
CV "v154,v52,v53\n"
CV "v153,v50,v51\n"
CV "v152,v48,v49\n"
AW "a168,v152\n"
AW "a169,v153\n"
AW "a170,v154\n"
AW "a171,v155\n"
"v_pk_mul_f32 v[40:41],v[40:41],s[54:55]\n"
"v_pk_mul_f32 v[42:43],v[42:43],s[54:55]\n"
"v_pk_mul_f32 v[44:45],v[44:45],s[54:55]\n"
"v_pk_mul_f32 v[46:47],v[46:47],s[54:55]\n"
CV "v151,v46,v47\n"
CV "v150,v44,v45\n"
CV "v149,v42,v43\n"
CV "v148,v40,v41\n"
AW "a120,v148\n"
AW "a121,v149\n"
AW "a122,v150\n"
AW "a123,v151\n"
"v_pk_mul_f32 v[32:33],v[32:33],s[54:55]\n"
"v_pk_mul_f32 v[34:35],v[34:35],s[54:55]\n"
"v_pk_mul_f32 v[36:37],v[36:37],s[54:55]\n"
"v_pk_mul_f32 v[38:39],v[38:39],s[54:55]\n"
CV "v147,v38,v39\n"
CV "v146,v36,v37\n"
CV "v145,v34,v35\n"
CV "v144,v32,v33\n"
AW "a96,v144\n"
AW "a97,v145\n"
AW "a98,v146\n"
AW "a99,v147\n"
"v_pk_mul_f32 v[24:25],v[24:25],s[54:55]\n"
"v_pk_mul_f32 v[26:27],v[26:27],s[54:55]\n"
"v_pk_mul_f32 v[28:29],v[28:29],s[54:55]\n"
"v_pk_mul_f32 v[30:31],v[30:31],s[54:55]\n"
CV "v143,v30,v31\n"
CV "v142,v28,v29\n"
CV "v141,v26,v27\n"
CV "v140,v24,v25\n"
AW "a92,v140\n"
AW "a93,v141\n"
AW "a94,v142\n"
AW "a95,v143\n"
"v_pk_mul_f32 v[16:17],v[16:17],s[54:55]\n"
"v_pk_mul_f32 v[18:19],v[18:19],s[54:55]\n"
"v_pk_mul_f32 v[20:21],v[20:21],s[54:55]\n"
"v_pk_mul_f32 v[22:23],v[22:23],s[54:55]\n"
CV "v139,v22,v23\n"
CV "v138,v20,v21\n"
CV "v137,v18,v19\n"
CV "v136,v16,v17\n"
AW "a88,v136\n"
AW "a89,v137\n"
AW "a90,v138\n"
AW "a91,v139\n"
"s_waitcnt lgkmcnt(0)\n"
"s_branch .Lq0\n"
".Lpsl:\n"
"s_sleep 1\n"
".Lpsub:\n"
"s_memrealtime s[100:101]\n"
"s_waitcnt lgkmcnt(0)\n"
"s_sub_u32 s101,s21,s100\n"
"s_cmp_gt_i32 s101,0\n"
"s_cbranch_scc1 .Lpsl\n"
"s_add_u32 s21,s100,s97\n"
"s_setpc_b64 s[52:53]\n"
".Lstub_6:\n"
"v_cmp_le_f32 vcc,0x47800000,v10\n"
"s_cmp_eq_u32 s61,1\n"
"s_cbranch_scc0 .Lhook_6\n"
"s_sub_u32 s58,s71,s70\n"
"s_add_u32 s72,s72,s58\n"
"s_add_u32 s16,s16,s58\n"
"s_add_u32 s17,s17,s58\n"
"s_cmp_eq_u32 s57,0\n"
"s_cselect_b32 s34,0,s34\n"
"s_cselect_b32 s64,163840,s64\n"
".Lhook_6:\n"
"s_cmp_eq_u32 s77,0\n"
"s_cbranch_scc0 .Lnop_6\n"
"s_call_b64 s[52:53],.Lpsub\n"
".Lnop_6:\n"
"s_cbranch_vccz .Lback_6\n"
"s_call_b64 s[52:53],.Lslow_1\n"
"s_branch .Lback_6\n"
".Lstub_7:\n"
"v_cmp_le_f32 vcc,0x47800000,v10\n"
"s_cmp_eq_u32 s61,1\n"
"s_cbranch_scc0 .Lhook_7\n"
"s_sub_u32 s58,s71,s70\n"
"s_add_u32 s73,s73,s58\n"
"s_add_u32 s18,s18,s58\n"
"s_add_u32 s19,s19,s58\n"
"s_cmp_eq_u32 s57,0\n"
"s_cselect_b32 s38,0,s38\n"
"s_cselect_b32 s65,163840,s65\n"
".Lhook_7:\n"
"s_cmp_eq_u32 s77,0\n"
"s_cbranch_scc0 .Lnop_7\n"
"s_call_b64 s[52:53],.Lpsub\n"
"s_cmp_eq_u32 s61,4\n"
"s_cselect_b32 s77,0x47800000,s77\n"
"s_cselect_b32 s95,0x47800000,s95\n"
".Lnop_7:\n"
"s_cbranch_vccz .Lback_7\n"
"s_call_b64 s[52:53],.Lslow_0\n"
"s_branch .Lback_7\n"
".Lstub_8:\n"
"s_call_b64 s[52:53],.Lslow_1\n"
"s_branch .Lback_8\n"
".Lstub_11:\n"
"v_cmp_le_f32 vcc,0x47800000,v10\n"
"s_cmp_eq_u32 s77,0\n"
"s_cbranch_scc0 .Lnop_11\n"
"s_call_b64 s[52:53],.Lpsub\n"
".Lnop_11:\n"
"s_cbranch_vccz .Lback_11\n"
"s_call_b64 s[52:53],.Lslow_1\n"
"s_branch .Lback_11\n"
".Lstub_12:\n"
"v_cmp_le_f32 vcc,0x47800000,v10\n"
"s_cmp_eq_u32 s77,0\n"
"s_cbranch_scc0 .Lnop_12\n"
"s_call_b64 s[52:53],.Lpsub\n"
".Lnop_12:\n"
"s_cbranch_vccz .Lback_12\n"
"s_call_b64 s[52:53],.Lslow_0\n"
"s_branch .Lback_12\n"
".Lstub_13:\n"
"v_cmp_le_f32 vcc,0x47800000,v10\n"
"s_cmp_eq_u32 s61,1\n"
"s_cbranch_scc0 .Lhook_13\n"
"s_mov_b32 s97,0\n"
".Lhook_13:\n"
"s_cmp_eq_u32 s77,0\n"
"s_cbranch_scc0 .Lnop_13\n"
"s_call_b64 s[52:53],.Lpsub\n"
".Lnop_13:\n"
"s_cbranch_vccz .Lback_13\n"
"s_call_b64 s[52:53],.Lslow_1\n"
"s_branch .Lback_13\n"
".Lstub_14:\n"
"v_cmp_le_f32 vcc,0x47800000,v10\n"
"s_cmp_eq_u32 s77,0\n"
"s_cbranch_scc0 .Lnop_14\n"
"s_call_b64 s[52:53],.Lpsub\n"
".Lnop_14:\n"
"s_cbranch_vccz .Lback_14\n"
"s_call_b64 s[52:53],.Lslow_0\n"
"s_branch .Lback_14\n"
".Lstub_15:\n"
"v_cmp_le_f32 vcc,0x47800000,v10\n"
"s_cmp_eq_u32 s61,1\n"
"s_cbranch_scc0 .Lhook_15\n"
"s_mov_b32 s34,0\n"
"s_mov_b32 s64,163840\n"
".Lhook_15:\n"
"s_cmp_eq_u32 s77,0\n"
"s_cbranch_scc0 .Lnop_15\n"
"s_call_b64 s[52:53],.Lpsub\n"
".Lnop_15:\n"
"s_cbranch_vccz .Lback_15\n"
"s_call_b64 s[52:53],.Lslow_1\n"
"s_branch .Lback_15\n"
".Lstub_16:\n"
"v_cmp_le_f32 vcc,0x47800000,v10\n"
"s_cmp_eq_u32 s77,0\n"
"s_cbranch_scc0 .Lnop_16\n"
"s_call_b64 s[52:53],.Lpsub\n"
".Lnop_16:\n"
"s_cbranch_vccz .Lback_16\n"
"s_call_b64 s[52:53],.Lslow_0\n"
"s_branch .Lback_16\n"
".Lstub_17:\n"
"s_call_b64 s[52:53],.Lslow_1\n"
"s_branch .Lback_17\n"
".Lslow_0:\n"
"s_nop 7\n"
"s_nop 7\n"
"v_and_b32 v10,240,v9\n"
"v_mul_u32_u24 v10,24,v10\n"
"v_lshrrev_b32 v11,4,v9\n"
"v_and_or_b32 v10,v11,48,v10\n"
"s_mov_b32 s59,s34\n"
"s_mov_b32 s34,s20\n"
"s_add_u32 s58,s74,0\n"
"s_nop 3\n"
LD "v[52:55],v10,s[32:35],s58 offen\n"
"s_waitcnt vmcnt(0)\n"
MF "v[72:75],v[52:55],v[136:139],v[32:35]\n"
MF "v[76:79],v[52:55],v[140:143],v[36:39]\n"
MF "v[80:83],v[52:55],v[144:147],v[40:43]\n"
MF "v[84:87],v[52:55],v[148:151],v[44:47]\n"
MF "v[88:91],v[52:55],v[152:155],v[48:51]\n"
"s_add_u32 s58,s74,0\n"
"s_nop 3\n"
LD "v[52:55],v10,s[32:35],s58 offen offset:64\n"
"s_waitcnt vmcnt(0)\n"
MF "v[72:75],v[52:55],v[156:159],v[72:75]\n"
MF "v[76:79],v[52:55],v[160:163],v[76:79]\n"
MF "v[80:83],v[52:55],v[164:167],v[80:83]\n"
MF "v[84:87],v[52:55],v[168:171],v[84:87]\n"
MF "v[88:91],v[52:55],v[172:175],v[88:91]\n"
"s_add_u32 s58,s74,0\n"
"s_nop 3\n"
LD "v[52:55],v10,s[32:35],s58 offen offset:128\n"
"s_waitcnt vmcnt(0)\n"
MF "v[72:75],v[52:55],v[176:179],v[72:75]\n"
MF "v[76:79],v[52:55],v[180:183],v[76:79]\n"
MF "v[80:83],v[52:55],v[184:187],v[80:83]\n"
MF "v[84:87],v[52:55],v[188:191],v[84:87]\n"
MF "v[88:91],v[52:55],v[192:195],v[88:91]\n"
"s_add_u32 s58,s74,0\n"
"s_nop 3\n"
LD "v[52:55],v10,s[32:35],s58 offen offset:192\n"
"s_waitcnt vmcnt(0)\n"
MF "v[72:75],v[52:55],v[196:199],v[72:75]\n"
MF "v[76:79],v[52:55],v[200:203],v[76:79]\n"
MF "v[80:83],v[52:55],v[204:207],v[80:83]\n"
MF "v[84:87],v[52:55],v[208:211],v[84:87]\n"
MF "v[88:91],v[52:55],v[212:215],v[88:91]\n"
"s_add_u32 s58,s74,0\n"
"s_nop 3\n"
LD "v[52:55],v10,s[32:35],s58 offen offset:256\n"
"s_waitcnt vmcnt(0)\n"
MF "v[72:75],v[52:55],v[216:219],v[72:75]\n"
MF "v[76:79],v[52:55],v[220:223],v[76:79]\n"
MF "v[80:83],v[52:55],v[224:227],v[80:83]\n"
MF "v[84:87],v[52:55],v[228:231],v[84:87]\n"
MF "v[88:91],v[52:55],v[232:235],v[88:91]\n"
"s_add_u32 s58,s74,0\n"
"s_nop 3\n"
LD "v[52:55],v10,s[32:35],s58 offen offset:320\n"
"s_waitcnt vmcnt(0)\n"
MF "v[72:75],v[52:55],v[236:239],v[72:75]\n"
MF "v[76:79],v[52:55],v[240:243],v[76:79]\n"
MF "v[80:83],v[52:55],v[244:247],v[80:83]\n"
MF "v[84:87],v[52:55],v[248:251],v[84:87]\n"
MF "v[88:91],v[52:55],v[252:255],v[88:91]\n"
"s_add_u32 s58,s74,6144\n"
"s_nop 3\n"
LD "v[52:55],v10,s[32:35],s58 offen\n"
"s_waitcnt vmcnt(0)\n"
MF "v[112:115],v[52:55],v[136:139],v[32:35]\n"
MF "v[116:119],v[52:55],v[140:143],v[36:39]\n"
MF "v[120:123],v[52:55],v[144:147],v[40:43]\n"
MF "v[124:127],v[52:55],v[148:151],v[44:47]\n"
MF "v[128:131],v[52:55],v[152:155],v[48:51]\n"
"s_add_u32 s58,s74,6144\n"
"s_nop 3\n"
LD "v[52:55],v10,s[32:35],s58 offen offset:64\n"
"s_waitcnt vmcnt(0)\n"
MF "v[112:115],v[52:55],v[156:159],v[112:115]\n"
MF "v[116:119],v[52:55],v[160:163],v[116:119]\n"
MF "v[120:123],v[52:55],v[164:167],v[120:123]\n"
MF "v[124:127],v[52:55],v[168:171],v[124:127]\n"
MF "v[128:131],v[52:55],v[172:175],v[128:131]\n"
"s_add_u32 s58,s74,6144\n"
"s_nop 3\n"
LD "v[52:55],v10,s[32:35],s58 offen offset:128\n"
"s_waitcnt vmcnt(0)\n"
MF "v[112:115],v[52:55],v[176:179],v[112:115]\n"
MF "v[116:119],v[52:55],v[180:183],v[116:119]\n"
MF "v[120:123],v[52:55],v[184:187],v[120:123]\n"
MF "v[124:127],v[52:55],v[188:191],v[124:127]\n"
MF "v[128:131],v[52:55],v[192:195],v[128:131]\n"
"s_add_u32 s58,s74,6144\n"
"s_nop 3\n"
LD "v[52:55],v10,s[32:35],s58 offen offset:192\n"
"s_waitcnt vmcnt(0)\n"
MF "v[112:115],v[52:55],v[196:199],v[112:115]\n"
MF "v[116:119],v[52:55],v[200:203],v[116:119]\n"
MF "v[120:123],v[52:55],v[204:207],v[120:123]\n"
MF "v[124:127],v[52:55],v[208:211],v[124:127]\n"
MF "v[128:131],v[52:55],v[212:215],v[128:131]\n"
"s_add_u32 s58,s74,6144\n"
"s_nop 3\n"
LD "v[52:55],v10,s[32:35],s58 offen offset:256\n"
"s_waitcnt vmcnt(0)\n"
MF "v[112:115],v[52:55],v[216:219],v[112:115]\n"
MF "v[116:119],v[52:55],v[220:223],v[116:119]\n"
MF "v[120:123],v[52:55],v[224:227],v[120:123]\n"
MF "v[124:127],v[52:55],v[228:231],v[124:127]\n"
MF "v[128:131],v[52:55],v[232:235],v[128:131]\n"
"s_add_u32 s58,s74,6144\n"
"s_nop 3\n"
LD "v[52:55],v10,s[32:35],s58 offen offset:320\n"
"s_waitcnt vmcnt(0)\n"
MF "v[112:115],v[52:55],v[236:239],v[112:115]\n"
MF "v[116:119],v[52:55],v[240:243],v[116:119]\n"
MF "v[120:123],v[52:55],v[244:247],v[120:123]\n"
MF "v[124:127],v[52:55],v[248:251],v[124:127]\n"
MF "v[128:131],v[52:55],v[252:255],v[128:131]\n"
"s_nop 7\n"
"s_mov_b32 s34,s59\n"
"v_max3_f32 v10,v72,v73,v74\n"
"s_nop 0\n"
"v_max3_f32 v11,v75,v112,v113\n"
"v_max3_f32 v10,v10,v114,v115\n"
"v_max_f32 v10,v10,v11\n"
"v_mov_b32 v11,v10\n"
"s_nop 1\n"
"v_permlane16_swap_b32 v10,v11\n"
"v_max_f32 v10,v10,v11\n"
"v_mov_b32 v11,v10\n"
"s_nop 1\n"
"v_permlane32_swap_b32 v10,v11\n"
"v_max_f32 v10,v10,v11\n"
"v_add_f32 v10,0x41300000,v10\n"
"v_max_f32_e64 v10,0,v10\n"
"v_exp_f32 v11,-v10\n"
"s_nop 0\n"
"v_mul_f32 v52,v57,v11\n"
AR "v12,a0\n"
"v_subrev_f32 v12,s13,v12\n"
"v_fma_f32 v12,v12,v11,s13\n"
AW "a0,v12\n"
AR "v12,a1\n"
"v_subrev_f32 v12,s13,v12\n"
"v_fma_f32 v12,v12,v11,s13\n"
AW "a1,v12\n"
AR "v12,a2\n"
"v_subrev_f32 v12,s13,v12\n"
"v_fma_f32 v12,v12,v11,s13\n"
AW "a2,v12\n"
AR "v12,a3\n"
"v_subrev_f32 v12,s13,v12\n"
"v_fma_f32 v12,v12,v11,s13\n"
AW "a3,v12\n"
AR "v12,a20\n"
"v_subrev_f32 v12,s13,v12\n"
"v_fma_f32 v12,v12,v11,s13\n"
AW "a20,v12\n"
AR "v12,a21\n"
"v_subrev_f32 v12,s13,v12\n"
"v_fma_f32 v12,v12,v11,s13\n"
AW "a21,v12\n"
AR "v12,a22\n"
"v_subrev_f32 v12,s13,v12\n"
"v_fma_f32 v12,v12,v11,s13\n"
AW "a22,v12\n"
AR "v12,a23\n"
"v_subrev_f32 v12,s13,v12\n"
"v_fma_f32 v12,v12,v11,s13\n"
AW "a23,v12\n"
AR "v12,a40\n"
"v_subrev_f32 v12,s13,v12\n"
"v_fma_f32 v12,v12,v11,s13\n"
AW "a40,v12\n"
AR "v12,a41\n"
"v_subrev_f32 v12,s13,v12\n"
"v_fma_f32 v12,v12,v11,s13\n"
AW "a41,v12\n"
AR "v12,a42\n"
"v_subrev_f32 v12,s13,v12\n"
"v_fma_f32 v12,v12,v11,s13\n"
AW "a42,v12\n"
AR "v12,a43\n"
"v_subrev_f32 v12,s13,v12\n"
"v_fma_f32 v12,v12,v11,s13\n"
AW "a43,v12\n"
AR "v12,a60\n"
"v_subrev_f32 v12,s13,v12\n"
"v_fma_f32 v12,v12,v11,s13\n"
AW "a60,v12\n"
AR "v12,a61\n"
"v_subrev_f32 v12,s13,v12\n"
"v_fma_f32 v12,v12,v11,s13\n"
AW "a61,v12\n"
AR "v12,a62\n"
"v_subrev_f32 v12,s13,v12\n"
"v_fma_f32 v12,v12,v11,s13\n"
AW "a62,v12\n"
AR "v12,a63\n"
"v_subrev_f32 v12,s13,v12\n"
"v_fma_f32 v12,v12,v11,s13\n"
AW "a63,v12\n"
AR "v12,a80\n"
"v_subrev_f32 v12,s13,v12\n"
"v_fma_f32 v12,v12,v11,s13\n"
AW "a80,v12\n"
AR "v12,a81\n"
"v_subrev_f32 v12,s13,v12\n"
"v_fma_f32 v12,v12,v11,s13\n"
AW "a81,v12\n"
AR "v12,a82\n"
"v_subrev_f32 v12,s13,v12\n"
"v_fma_f32 v12,v12,v11,s13\n"
AW "a82,v12\n"
AR "v12,a83\n"
"v_subrev_f32 v12,s13,v12\n"
"v_fma_f32 v12,v12,v11,s13\n"
AW "a83,v12\n"
AR "v12,a100\n"
"v_subrev_f32 v12,s13,v12\n"
"v_fma_f32 v12,v12,v11,s13\n"
AW "a100,v12\n"
AR "v12,a101\n"
"v_subrev_f32 v12,s13,v12\n"
"v_fma_f32 v12,v12,v11,s13\n"
AW "a101,v12\n"
AR "v12,a102\n"
"v_subrev_f32 v12,s13,v12\n"
"v_fma_f32 v12,v12,v11,s13\n"
AW "a102,v12\n"
AR "v12,a103\n"
"v_subrev_f32 v12,s13,v12\n"
"v_fma_f32 v12,v12,v11,s13\n"
AW "a103,v12\n"
AR "v12,a120\n"
"v_subrev_f32 v12,s13,v12\n"
"v_fma_f32 v12,v12,v11,s13\n"
AW "a120,v12\n"
AR "v12,a121\n"
"v_subrev_f32 v12,s13,v12\n"
"v_fma_f32 v12,v12,v11,s13\n"
AW "a121,v12\n"
AR "v12,a122\n"
"v_subrev_f32 v12,s13,v12\n"
"v_fma_f32 v12,v12,v11,s13\n"
AW "a122,v12\n"
AR "v12,a123\n"
"v_subrev_f32 v12,s13,v12\n"
"v_fma_f32 v12,v12,v11,s13\n"
AW "a123,v12\n"
AR "v12,a140\n"
"v_subrev_f32 v12,s13,v12\n"
"v_fma_f32 v12,v12,v11,s13\n"
AW "a140,v12\n"
AR "v12,a141\n"
"v_subrev_f32 v12,s13,v12\n"
"v_fma_f32 v12,v12,v11,s13\n"
AW "a141,v12\n"
AR "v12,a142\n"
"v_subrev_f32 v12,s13,v12\n"
"v_fma_f32 v12,v12,v11,s13\n"
AW "a142,v12\n"
AR "v12,a143\n"
"v_subrev_f32 v12,s13,v12\n"
"v_fma_f32 v12,v12,v11,s13\n"
AW "a143,v12\n"
AR "v12,a160\n"
"v_subrev_f32 v12,s13,v12\n"
"v_fma_f32 v12,v12,v11,s13\n"
AW "a160,v12\n"
AR "v12,a161\n"
"v_subrev_f32 v12,s13,v12\n"
"v_fma_f32 v12,v12,v11,s13\n"
AW "a161,v12\n"
AR "v12,a162\n"
"v_subrev_f32 v12,s13,v12\n"
"v_fma_f32 v12,v12,v11,s13\n"
AW "a162,v12\n"
AR "v12,a163\n"
"v_subrev_f32 v12,s13,v12\n"
"v_fma_f32 v12,v12,v11,s13\n"
AW "a163,v12\n"
AR "v12,a180\n"
"v_subrev_f32 v12,s13,v12\n"
"v_fma_f32 v12,v12,v11,s13\n"
AW "a180,v12\n"
AR "v12,a181\n"
"v_subrev_f32 v12,s13,v12\n"
"v_fma_f32 v12,v12,v11,s13\n"
AW "a181,v12\n"
AR "v12,a182\n"
"v_subrev_f32 v12,s13,v12\n"
"v_fma_f32 v12,v12,v11,s13\n"
AW "a182,v12\n"
AR "v12,a183\n"
"v_subrev_f32 v12,s13,v12\n"
"v_fma_f32 v12,v12,v11,s13\n"
AW "a183,v12\n"
AR "v12,a200\n"
"v_subrev_f32 v12,s13,v12\n"
"v_fma_f32 v12,v12,v11,s13\n"
AW "a200,v12\n"
AR "v12,a201\n"
"v_subrev_f32 v12,s13,v12\n"
"v_fma_f32 v12,v12,v11,s13\n"
AW "a201,v12\n"
AR "v12,a202\n"
"v_subrev_f32 v12,s13,v12\n"
"v_fma_f32 v12,v12,v11,s13\n"
AW "a202,v12\n"
AR "v12,a203\n"
"v_subrev_f32 v12,s13,v12\n"
"v_fma_f32 v12,v12,v11,s13\n"
AW "a203,v12\n"
AR "v12,a220\n"
"v_subrev_f32 v12,s13,v12\n"
"v_fma_f32 v12,v12,v11,s13\n"
AW "a220,v12\n"
AR "v12,a221\n"
"v_subrev_f32 v12,s13,v12\n"
"v_fma_f32 v12,v12,v11,s13\n"
AW "a221,v12\n"
AR "v12,a222\n"
"v_subrev_f32 v12,s13,v12\n"
"v_fma_f32 v12,v12,v11,s13\n"
AW "a222,v12\n"
AR "v12,a223\n"
"v_subrev_f32 v12,s13,v12\n"
"v_fma_f32 v12,v12,v11,s13\n"
AW "a223,v12\n"
"v_sub_f32 v32,v32,v10\n"
"v_sub_f32 v33,v33,v10\n"
"v_sub_f32 v34,v34,v10\n"
"v_sub_f32 v35,v35,v10\n"
"v_sub_f32 v92,v92,v10\n"
"v_sub_f32 v93,v93,v10\n"
"v_sub_f32 v94,v94,v10\n"
"v_sub_f32 v95,v95,v10\n"
"v_sub_f32 v72,v72,v10\n"
"v_sub_f32 v73,v73,v10\n"
"v_sub_f32 v74,v74,v10\n"
"v_sub_f32 v75,v75,v10\n"
"v_sub_f32 v112,v112,v10\n"
"v_sub_f32 v113,v113,v10\n"
"v_sub_f32 v114,v114,v10\n"
"v_sub_f32 v115,v115,v10\n"
"v_exp_f32 v72,v72\n"
"v_exp_f32 v73,v73\n"
"v_exp_f32 v74,v74\n"
"v_exp_f32 v75,v75\n"
"v_exp_f32 v112,v112\n"
"v_exp_f32 v113,v113\n"
"v_exp_f32 v114,v114\n"
"v_exp_f32 v115,v115\n"
"v_add_f32 v52,v52,v72\n"
"v_add_f32 v52,v52,v73\n"
"v_add_f32 v52,v52,v74\n"
"v_add_f32 v52,v52,v75\n"
"v_add_f32 v52,v52,v112\n"
"v_add_f32 v52,v52,v113\n"
"v_add_f32 v52,v52,v114\n"
"v_add_f32 v52,v52,v115\n"
CV "v72,v72,v73\n"
CV "v73,v74,v75\n"
CV "v74,v112,v113\n"
CV "v75,v114,v115\n"
"v_max3_f32 v10,v76,v77,v78\n"
"v_max3_f32 v11,v79,v116,v117\n"
"v_max3_f32 v10,v10,v118,v119\n"
"v_max_f32 v10,v10,v11\n"
"v_mov_b32 v11,v10\n"
"s_nop 1\n"
"v_permlane16_swap_b32 v10,v11\n"
"v_max_f32 v10,v10,v11\n"
"v_mov_b32 v11,v10\n"
"s_nop 1\n"
"v_permlane32_swap_b32 v10,v11\n"
"v_max_f32 v10,v10,v11\n"
"v_add_f32 v10,0x41300000,v10\n"
"v_max_f32 v10,0,v10\n"
"v_exp_f32 v11,-v10\n"
"s_nop 0\n"
"v_mul_f32 v53,v58,v11\n"
AR "v12,a4\n"
"v_subrev_f32 v12,s13,v12\n"
"v_fma_f32 v12,v12,v11,s13\n"
AW "a4,v12\n"
AR "v12,a5\n"
"v_subrev_f32 v12,s13,v12\n"
"v_fma_f32 v12,v12,v11,s13\n"
AW "a5,v12\n"
AR "v12,a6\n"
"v_subrev_f32 v12,s13,v12\n"
"v_fma_f32 v12,v12,v11,s13\n"
AW "a6,v12\n"
AR "v12,a7\n"
"v_subrev_f32 v12,s13,v12\n"
"v_fma_f32 v12,v12,v11,s13\n"
AW "a7,v12\n"
AR "v12,a24\n"
"v_subrev_f32 v12,s13,v12\n"
"v_fma_f32 v12,v12,v11,s13\n"
AW "a24,v12\n"
AR "v12,a25\n"
"v_subrev_f32 v12,s13,v12\n"
"v_fma_f32 v12,v12,v11,s13\n"
AW "a25,v12\n"
AR "v12,a26\n"
"v_subrev_f32 v12,s13,v12\n"
"v_fma_f32 v12,v12,v11,s13\n"
AW "a26,v12\n"
AR "v12,a27\n"
"v_subrev_f32 v12,s13,v12\n"
"v_fma_f32 v12,v12,v11,s13\n"
AW "a27,v12\n"
AR "v12,a44\n"
"v_subrev_f32 v12,s13,v12\n"
"v_fma_f32 v12,v12,v11,s13\n"
AW "a44,v12\n"
AR "v12,a45\n"
"v_subrev_f32 v12,s13,v12\n"
"v_fma_f32 v12,v12,v11,s13\n"
AW "a45,v12\n"
AR "v12,a46\n"
"v_subrev_f32 v12,s13,v12\n"
"v_fma_f32 v12,v12,v11,s13\n"
AW "a46,v12\n"
AR "v12,a47\n"
"v_subrev_f32 v12,s13,v12\n"
"v_fma_f32 v12,v12,v11,s13\n"
AW "a47,v12\n"
AR "v12,a64\n"
"v_subrev_f32 v12,s13,v12\n"
"v_fma_f32 v12,v12,v11,s13\n"
AW "a64,v12\n"
AR "v12,a65\n"
"v_subrev_f32 v12,s13,v12\n"
"v_fma_f32 v12,v12,v11,s13\n"
AW "a65,v12\n"
AR "v12,a66\n"
"v_subrev_f32 v12,s13,v12\n"
"v_fma_f32 v12,v12,v11,s13\n"
AW "a66,v12\n"
AR "v12,a67\n"
"v_subrev_f32 v12,s13,v12\n"
"v_fma_f32 v12,v12,v11,s13\n"
AW "a67,v12\n"
AR "v12,a84\n"
"v_subrev_f32 v12,s13,v12\n"
"v_fma_f32 v12,v12,v11,s13\n"
AW "a84,v12\n"
AR "v12,a85\n"
"v_subrev_f32 v12,s13,v12\n"
"v_fma_f32 v12,v12,v11,s13\n"
AW "a85,v12\n"
AR "v12,a86\n"
"v_subrev_f32 v12,s13,v12\n"
"v_fma_f32 v12,v12,v11,s13\n"
AW "a86,v12\n"
AR "v12,a87\n"
"v_subrev_f32 v12,s13,v12\n"
"v_fma_f32 v12,v12,v11,s13\n"
AW "a87,v12\n"
AR "v12,a104\n"
"v_subrev_f32 v12,s13,v12\n"
"v_fma_f32 v12,v12,v11,s13\n"
AW "a104,v12\n"
AR "v12,a105\n"
"v_subrev_f32 v12,s13,v12\n"
"v_fma_f32 v12,v12,v11,s13\n"
AW "a105,v12\n"
AR "v12,a106\n"
"v_subrev_f32 v12,s13,v12\n"
"v_fma_f32 v12,v12,v11,s13\n"
AW "a106,v12\n"
AR "v12,a107\n"
"v_subrev_f32 v12,s13,v12\n"
"v_fma_f32 v12,v12,v11,s13\n"
AW "a107,v12\n"
AR "v12,a124\n"
"v_subrev_f32 v12,s13,v12\n"
"v_fma_f32 v12,v12,v11,s13\n"
AW "a124,v12\n"
AR "v12,a125\n"
"v_subrev_f32 v12,s13,v12\n"
"v_fma_f32 v12,v12,v11,s13\n"
AW "a125,v12\n"
AR "v12,a126\n"
"v_subrev_f32 v12,s13,v12\n"
"v_fma_f32 v12,v12,v11,s13\n"
AW "a126,v12\n"
AR "v12,a127\n"
"v_subrev_f32 v12,s13,v12\n"
"v_fma_f32 v12,v12,v11,s13\n"
AW "a127,v12\n"
AR "v12,a144\n"
"v_subrev_f32 v12,s13,v12\n"
"v_fma_f32 v12,v12,v11,s13\n"
AW "a144,v12\n"
AR "v12,a145\n"
"v_subrev_f32 v12,s13,v12\n"
"v_fma_f32 v12,v12,v11,s13\n"
AW "a145,v12\n"
AR "v12,a146\n"
"v_subrev_f32 v12,s13,v12\n"
"v_fma_f32 v12,v12,v11,s13\n"
AW "a146,v12\n"
AR "v12,a147\n"
"v_subrev_f32 v12,s13,v12\n"
"v_fma_f32 v12,v12,v11,s13\n"
AW "a147,v12\n"
AR "v12,a164\n"
"v_subrev_f32 v12,s13,v12\n"
"v_fma_f32 v12,v12,v11,s13\n"
AW "a164,v12\n"
AR "v12,a165\n"
"v_subrev_f32 v12,s13,v12\n"
"v_fma_f32 v12,v12,v11,s13\n"
AW "a165,v12\n"
AR "v12,a166\n"
"v_subrev_f32 v12,s13,v12\n"
"v_fma_f32 v12,v12,v11,s13\n"
AW "a166,v12\n"
AR "v12,a167\n"
"v_subrev_f32 v12,s13,v12\n"
"v_fma_f32 v12,v12,v11,s13\n"
AW "a167,v12\n"
AR "v12,a184\n"
"v_subrev_f32 v12,s13,v12\n"
"v_fma_f32 v12,v12,v11,s13\n"
AW "a184,v12\n"
AR "v12,a185\n"
"v_subrev_f32 v12,s13,v12\n"
"v_fma_f32 v12,v12,v11,s13\n"
AW "a185,v12\n"
AR "v12,a186\n"
"v_subrev_f32 v12,s13,v12\n"
"v_fma_f32 v12,v12,v11,s13\n"
AW "a186,v12\n"
AR "v12,a187\n"
"v_subrev_f32 v12,s13,v12\n"
"v_fma_f32 v12,v12,v11,s13\n"
AW "a187,v12\n"
AR "v12,a204\n"
"v_subrev_f32 v12,s13,v12\n"
"v_fma_f32 v12,v12,v11,s13\n"
AW "a204,v12\n"
AR "v12,a205\n"
"v_subrev_f32 v12,s13,v12\n"
"v_fma_f32 v12,v12,v11,s13\n"
AW "a205,v12\n"
AR "v12,a206\n"
"v_subrev_f32 v12,s13,v12\n"
"v_fma_f32 v12,v12,v11,s13\n"
AW "a206,v12\n"
AR "v12,a207\n"
"v_subrev_f32 v12,s13,v12\n"
"v_fma_f32 v12,v12,v11,s13\n"
AW "a207,v12\n"
AR "v12,a224\n"
"v_subrev_f32 v12,s13,v12\n"
"v_fma_f32 v12,v12,v11,s13\n"
AW "a224,v12\n"
AR "v12,a225\n"
"v_subrev_f32 v12,s13,v12\n"
"v_fma_f32 v12,v12,v11,s13\n"
AW "a225,v12\n"
AR "v12,a226\n"
"v_subrev_f32 v12,s13,v12\n"
"v_fma_f32 v12,v12,v11,s13\n"
AW "a226,v12\n"
AR "v12,a227\n"
"v_subrev_f32 v12,s13,v12\n"
"v_fma_f32 v12,v12,v11,s13\n"
AW "a227,v12\n"
"v_sub_f32 v36,v36,v10\n"
"v_sub_f32 v37,v37,v10\n"
"v_sub_f32 v38,v38,v10\n"
"v_sub_f32 v39,v39,v10\n"
"v_sub_f32 v96,v96,v10\n"
"v_sub_f32 v97,v97,v10\n"
"v_sub_f32 v98,v98,v10\n"
"v_sub_f32 v99,v99,v10\n"
"v_sub_f32 v76,v76,v10\n"
"v_sub_f32 v77,v77,v10\n"
"v_sub_f32 v78,v78,v10\n"
"v_sub_f32 v79,v79,v10\n"
"v_sub_f32 v116,v116,v10\n"
"v_sub_f32 v117,v117,v10\n"
"v_sub_f32 v118,v118,v10\n"
"v_sub_f32 v119,v119,v10\n"
"v_exp_f32 v76,v76\n"
"v_exp_f32 v77,v77\n"
"v_exp_f32 v78,v78\n"
"v_exp_f32 v79,v79\n"
"v_exp_f32 v116,v116\n"
"v_exp_f32 v117,v117\n"
"v_exp_f32 v118,v118\n"
"v_exp_f32 v119,v119\n"
"v_add_f32 v53,v53,v76\n"
"v_add_f32 v53,v53,v77\n"
"v_add_f32 v53,v53,v78\n"
"v_add_f32 v53,v53,v79\n"
"v_add_f32 v53,v53,v116\n"
"v_add_f32 v53,v53,v117\n"
"v_add_f32 v53,v53,v118\n"
"v_add_f32 v53,v53,v119\n"
CV "v76,v76,v77\n"
CV "v77,v78,v79\n"
CV "v78,v116,v117\n"
CV "v79,v118,v119\n"
"v_max3_f32 v10,v80,v81,v82\n"
"v_max3_f32 v11,v83,v120,v121\n"
"v_max3_f32 v10,v10,v122,v123\n"
"v_max_f32 v10,v10,v11\n"
"v_mov_b32 v11,v10\n"
"s_nop 1\n"
"v_permlane16_swap_b32 v10,v11\n"
"v_max_f32 v10,v10,v11\n"
"v_mov_b32 v11,v10\n"
"s_nop 1\n"
"v_permlane32_swap_b32 v10,v11\n"
"v_max_f32 v10,v10,v11\n"
"v_add_f32 v10,0x41300000,v10\n"
"v_max_f32 v10,0,v10\n"
"v_exp_f32 v11,-v10\n"
"s_nop 0\n"
"v_mul_f32 v54,v59,v11\n"
AR "v12,a8\n"
"v_subrev_f32 v12,s13,v12\n"
"v_fma_f32 v12,v12,v11,s13\n"
AW "a8,v12\n"
AR "v12,a9\n"
"v_subrev_f32 v12,s13,v12\n"
"v_fma_f32 v12,v12,v11,s13\n"
AW "a9,v12\n"
AR "v12,a10\n"
"v_subrev_f32 v12,s13,v12\n"
"v_fma_f32 v12,v12,v11,s13\n"
AW "a10,v12\n"
AR "v12,a11\n"
"v_subrev_f32 v12,s13,v12\n"
"v_fma_f32 v12,v12,v11,s13\n"
AW "a11,v12\n"
AR "v12,a28\n"
"v_subrev_f32 v12,s13,v12\n"
"v_fma_f32 v12,v12,v11,s13\n"
AW "a28,v12\n"
AR "v12,a29\n"
"v_subrev_f32 v12,s13,v12\n"
"v_fma_f32 v12,v12,v11,s13\n"
AW "a29,v12\n"
AR "v12,a30\n"
"v_subrev_f32 v12,s13,v12\n"
"v_fma_f32 v12,v12,v11,s13\n"
AW "a30,v12\n"
AR "v12,a31\n"
"v_subrev_f32 v12,s13,v12\n"
"v_fma_f32 v12,v12,v11,s13\n"
AW "a31,v12\n"
AR "v12,a48\n"
"v_subrev_f32 v12,s13,v12\n"
"v_fma_f32 v12,v12,v11,s13\n"
AW "a48,v12\n"
AR "v12,a49\n"
"v_subrev_f32 v12,s13,v12\n"
"v_fma_f32 v12,v12,v11,s13\n"
AW "a49,v12\n"
AR "v12,a50\n"
"v_subrev_f32 v12,s13,v12\n"
"v_fma_f32 v12,v12,v11,s13\n"
AW "a50,v12\n"
AR "v12,a51\n"
"v_subrev_f32 v12,s13,v12\n"
"v_fma_f32 v12,v12,v11,s13\n"
AW "a51,v12\n"
AR "v12,a68\n"
"v_subrev_f32 v12,s13,v12\n"
"v_fma_f32 v12,v12,v11,s13\n"
AW "a68,v12\n"
AR "v12,a69\n"
"v_subrev_f32 v12,s13,v12\n"
"v_fma_f32 v12,v12,v11,s13\n"
AW "a69,v12\n"
AR "v12,a70\n"
"v_subrev_f32 v12,s13,v12\n"
"v_fma_f32 v12,v12,v11,s13\n"
AW "a70,v12\n"
AR "v12,a71\n"
"v_subrev_f32 v12,s13,v12\n"
"v_fma_f32 v12,v12,v11,s13\n"
AW "a71,v12\n"
AR "v12,a88\n"
"v_subrev_f32 v12,s13,v12\n"
"v_fma_f32 v12,v12,v11,s13\n"
AW "a88,v12\n"
AR "v12,a89\n"
"v_subrev_f32 v12,s13,v12\n"
"v_fma_f32 v12,v12,v11,s13\n"
AW "a89,v12\n"
AR "v12,a90\n"
"v_subrev_f32 v12,s13,v12\n"
"v_fma_f32 v12,v12,v11,s13\n"
AW "a90,v12\n"
AR "v12,a91\n"
"v_subrev_f32 v12,s13,v12\n"
"v_fma_f32 v12,v12,v11,s13\n"
AW "a91,v12\n"
AR "v12,a108\n"
"v_subrev_f32 v12,s13,v12\n"
"v_fma_f32 v12,v12,v11,s13\n"
AW "a108,v12\n"
AR "v12,a109\n"
"v_subrev_f32 v12,s13,v12\n"
"v_fma_f32 v12,v12,v11,s13\n"
AW "a109,v12\n"
AR "v12,a110\n"
"v_subrev_f32 v12,s13,v12\n"
"v_fma_f32 v12,v12,v11,s13\n"
AW "a110,v12\n"
AR "v12,a111\n"
"v_subrev_f32 v12,s13,v12\n"
"v_fma_f32 v12,v12,v11,s13\n"
AW "a111,v12\n"
AR "v12,a128\n"
"v_subrev_f32 v12,s13,v12\n"
"v_fma_f32 v12,v12,v11,s13\n"
AW "a128,v12\n"
AR "v12,a129\n"
"v_subrev_f32 v12,s13,v12\n"
"v_fma_f32 v12,v12,v11,s13\n"
AW "a129,v12\n"
AR "v12,a130\n"
"v_subrev_f32 v12,s13,v12\n"
"v_fma_f32 v12,v12,v11,s13\n"
AW "a130,v12\n"
AR "v12,a131\n"
"v_subrev_f32 v12,s13,v12\n"
"v_fma_f32 v12,v12,v11,s13\n"
AW "a131,v12\n"
AR "v12,a148\n"
"v_subrev_f32 v12,s13,v12\n"
"v_fma_f32 v12,v12,v11,s13\n"
AW "a148,v12\n"
AR "v12,a149\n"
"v_subrev_f32 v12,s13,v12\n"
"v_fma_f32 v12,v12,v11,s13\n"
AW "a149,v12\n"
AR "v12,a150\n"
"v_subrev_f32 v12,s13,v12\n"
"v_fma_f32 v12,v12,v11,s13\n"
AW "a150,v12\n"
AR "v12,a151\n"
"v_subrev_f32 v12,s13,v12\n"
"v_fma_f32 v12,v12,v11,s13\n"
AW "a151,v12\n"
AR "v12,a168\n"
"v_subrev_f32 v12,s13,v12\n"
"v_fma_f32 v12,v12,v11,s13\n"
AW "a168,v12\n"
AR "v12,a169\n"
"v_subrev_f32 v12,s13,v12\n"
"v_fma_f32 v12,v12,v11,s13\n"
AW "a169,v12\n"
AR "v12,a170\n"
"v_subrev_f32 v12,s13,v12\n"
"v_fma_f32 v12,v12,v11,s13\n"
AW "a170,v12\n"
AR "v12,a171\n"
"v_subrev_f32 v12,s13,v12\n"
"v_fma_f32 v12,v12,v11,s13\n"
AW "a171,v12\n"
AR "v12,a188\n"
"v_subrev_f32 v12,s13,v12\n"
"v_fma_f32 v12,v12,v11,s13\n"
AW "a188,v12\n"
AR "v12,a189\n"
"v_subrev_f32 v12,s13,v12\n"
"v_fma_f32 v12,v12,v11,s13\n"
AW "a189,v12\n"
AR "v12,a190\n"
"v_subrev_f32 v12,s13,v12\n"
"v_fma_f32 v12,v12,v11,s13\n"
AW "a190,v12\n"
AR "v12,a191\n"
"v_subrev_f32 v12,s13,v12\n"
"v_fma_f32 v12,v12,v11,s13\n"
AW "a191,v12\n"
AR "v12,a208\n"
"v_subrev_f32 v12,s13,v12\n"
"v_fma_f32 v12,v12,v11,s13\n"
AW "a208,v12\n"
AR "v12,a209\n"
"v_subrev_f32 v12,s13,v12\n"
"v_fma_f32 v12,v12,v11,s13\n"
AW "a209,v12\n"
AR "v12,a210\n"
"v_subrev_f32 v12,s13,v12\n"
"v_fma_f32 v12,v12,v11,s13\n"
AW "a210,v12\n"
AR "v12,a211\n"
"v_subrev_f32 v12,s13,v12\n"
"v_fma_f32 v12,v12,v11,s13\n"
AW "a211,v12\n"
AR "v12,a228\n"
"v_subrev_f32 v12,s13,v12\n"
"v_fma_f32 v12,v12,v11,s13\n"
AW "a228,v12\n"
AR "v12,a229\n"
"v_subrev_f32 v12,s13,v12\n"
"v_fma_f32 v12,v12,v11,s13\n"
AW "a229,v12\n"
AR "v12,a230\n"
"v_subrev_f32 v12,s13,v12\n"
"v_fma_f32 v12,v12,v11,s13\n"
AW "a230,v12\n"
AR "v12,a231\n"
"v_subrev_f32 v12,s13,v12\n"
"v_fma_f32 v12,v12,v11,s13\n"
AW "a231,v12\n"
"v_sub_f32 v40,v40,v10\n"
"v_sub_f32 v41,v41,v10\n"
"v_sub_f32 v42,v42,v10\n"
"v_sub_f32 v43,v43,v10\n"
"v_sub_f32 v100,v100,v10\n"
"v_sub_f32 v101,v101,v10\n"
"v_sub_f32 v102,v102,v10\n"
"v_sub_f32 v103,v103,v10\n"
"v_sub_f32 v80,v80,v10\n"
"v_sub_f32 v81,v81,v10\n"
"v_sub_f32 v82,v82,v10\n"
"v_sub_f32 v83,v83,v10\n"
"v_sub_f32 v120,v120,v10\n"
"v_sub_f32 v121,v121,v10\n"
"v_sub_f32 v122,v122,v10\n"
"v_sub_f32 v123,v123,v10\n"
"v_exp_f32 v80,v80\n"
"v_exp_f32 v81,v81\n"
"v_exp_f32 v82,v82\n"
"v_exp_f32 v83,v83\n"
"v_exp_f32 v120,v120\n"
"v_exp_f32 v121,v121\n"
"v_exp_f32 v122,v122\n"
"v_exp_f32 v123,v123\n"
"v_add_f32 v54,v54,v80\n"
"v_add_f32 v54,v54,v81\n"
"v_add_f32 v54,v54,v82\n"
"v_add_f32 v54,v54,v83\n"
"v_add_f32 v54,v54,v120\n"
"v_add_f32 v54,v54,v121\n"
"v_add_f32 v54,v54,v122\n"
"v_add_f32 v54,v54,v123\n"
CV "v80,v80,v81\n"
CV "v81,v82,v83\n"
CV "v82,v120,v121\n"
CV "v83,v122,v123\n"
"v_max3_f32 v10,v84,v85,v86\n"
"v_max3_f32 v11,v87,v124,v125\n"
"v_max3_f32 v10,v10,v126,v127\n"
"v_max_f32 v10,v10,v11\n"
"v_mov_b32 v11,v10\n"
"s_nop 1\n"
"v_permlane16_swap_b32 v10,v11\n"
"v_max_f32 v10,v10,v11\n"
"v_mov_b32 v11,v10\n"
"s_nop 1\n"
"v_permlane32_swap_b32 v10,v11\n"
"v_max_f32_e64 v10,v10,v11\n"
"v_add_f32 v10,0x41300000,v10\n"
"v_max_f32 v10,0,v10\n"
"v_exp_f32 v11,-v10\n"
"s_nop 0\n"
"v_mul_f32_e64 v55,v60,v11\n"
AR "v12,a12\n"
"v_subrev_f32 v12,s13,v12\n"
"v_fma_f32 v12,v12,v11,s13\n"
AW "a12,v12\n"
AR "v12,a13\n"
"v_subrev_f32 v12,s13,v12\n"
"v_fma_f32 v12,v12,v11,s13\n"
AW "a13,v12\n"
AR "v12,a14\n"
"v_subrev_f32 v12,s13,v12\n"
"v_fma_f32 v12,v12,v11,s13\n"
AW "a14,v12\n"
AR "v12,a15\n"
"v_subrev_f32 v12,s13,v12\n"
"v_fma_f32 v12,v12,v11,s13\n"
AW "a15,v12\n"
AR "v12,a32\n"
"v_subrev_f32 v12,s13,v12\n"
"v_fma_f32 v12,v12,v11,s13\n"
AW "a32,v12\n"
AR "v12,a33\n"
"v_subrev_f32 v12,s13,v12\n"
"v_fma_f32 v12,v12,v11,s13\n"
AW "a33,v12\n"
AR "v12,a34\n"
"v_subrev_f32 v12,s13,v12\n"
"v_fma_f32 v12,v12,v11,s13\n"
AW "a34,v12\n"
AR "v12,a35\n"
"v_subrev_f32 v12,s13,v12\n"
"v_fma_f32 v12,v12,v11,s13\n"
AW "a35,v12\n"
AR "v12,a52\n"
"v_subrev_f32 v12,s13,v12\n"
"v_fma_f32 v12,v12,v11,s13\n"
AW "a52,v12\n"
AR "v12,a53\n"
"v_subrev_f32 v12,s13,v12\n"
"v_fma_f32 v12,v12,v11,s13\n"
AW "a53,v12\n"
AR "v12,a54\n"
"v_subrev_f32 v12,s13,v12\n"
"v_fma_f32 v12,v12,v11,s13\n"
AW "a54,v12\n"
AR "v12,a55\n"
"v_subrev_f32 v12,s13,v12\n"
"v_fma_f32 v12,v12,v11,s13\n"
AW "a55,v12\n"
AR "v12,a72\n"
"v_subrev_f32 v12,s13,v12\n"
"v_fma_f32 v12,v12,v11,s13\n"
AW "a72,v12\n"
AR "v12,a73\n"
"v_subrev_f32 v12,s13,v12\n"
"v_fma_f32 v12,v12,v11,s13\n"
AW "a73,v12\n"
AR "v12,a74\n"
"v_subrev_f32 v12,s13,v12\n"
"v_fma_f32 v12,v12,v11,s13\n"
AW "a74,v12\n"
AR "v12,a75\n"
"v_subrev_f32 v12,s13,v12\n"
"v_fma_f32 v12,v12,v11,s13\n"
AW "a75,v12\n"
AR "v12,a92\n"
"v_subrev_f32 v12,s13,v12\n"
"v_fma_f32 v12,v12,v11,s13\n"
AW "a92,v12\n"
AR "v12,a93\n"
"v_subrev_f32 v12,s13,v12\n"
"v_fma_f32 v12,v12,v11,s13\n"
AW "a93,v12\n"
AR "v12,a94\n"
"v_subrev_f32 v12,s13,v12\n"
"v_fma_f32 v12,v12,v11,s13\n"
AW "a94,v12\n"
AR "v12,a95\n"
"v_subrev_f32 v12,s13,v12\n"
"v_fma_f32 v12,v12,v11,s13\n"
AW "a95,v12\n"
AR "v12,a112\n"
"v_subrev_f32 v12,s13,v12\n"
"v_fma_f32 v12,v12,v11,s13\n"
AW "a112,v12\n"
AR "v12,a113\n"
"v_subrev_f32 v12,s13,v12\n"
"v_fma_f32 v12,v12,v11,s13\n"
AW "a113,v12\n"
AR "v12,a114\n"
"v_subrev_f32 v12,s13,v12\n"
"v_fma_f32 v12,v12,v11,s13\n"
AW "a114,v12\n"
AR "v12,a115\n"
"v_subrev_f32 v12,s13,v12\n"
"v_fma_f32 v12,v12,v11,s13\n"
AW "a115,v12\n"
AR "v12,a132\n"
"v_subrev_f32 v12,s13,v12\n"
"v_fma_f32 v12,v12,v11,s13\n"
AW "a132,v12\n"
AR "v12,a133\n"
"v_subrev_f32 v12,s13,v12\n"
"v_fma_f32 v12,v12,v11,s13\n"
AW "a133,v12\n"
AR "v12,a134\n"
"v_subrev_f32 v12,s13,v12\n"
"v_fma_f32 v12,v12,v11,s13\n"
AW "a134,v12\n"
AR "v12,a135\n"
"v_subrev_f32 v12,s13,v12\n"
"v_fma_f32 v12,v12,v11,s13\n"
AW "a135,v12\n"
AR "v12,a152\n"
"v_subrev_f32 v12,s13,v12\n"
"v_fma_f32 v12,v12,v11,s13\n"
AW "a152,v12\n"
AR "v12,a153\n"
"v_subrev_f32 v12,s13,v12\n"
"v_fma_f32 v12,v12,v11,s13\n"
AW "a153,v12\n"
AR "v12,a154\n"
"v_subrev_f32 v12,s13,v12\n"
"v_fma_f32 v12,v12,v11,s13\n"
AW "a154,v12\n"
AR "v12,a155\n"
"v_subrev_f32 v12,s13,v12\n"
"v_fma_f32 v12,v12,v11,s13\n"
AW "a155,v12\n"
AR "v12,a172\n"
"v_subrev_f32 v12,s13,v12\n"
"v_fma_f32 v12,v12,v11,s13\n"
AW "a172,v12\n"
AR "v12,a173\n"
"v_subrev_f32 v12,s13,v12\n"
"v_fma_f32 v12,v12,v11,s13\n"
AW "a173,v12\n"
AR "v12,a174\n"
"v_subrev_f32 v12,s13,v12\n"
"v_fma_f32 v12,v12,v11,s13\n"
AW "a174,v12\n"
AR "v12,a175\n"
"v_subrev_f32 v12,s13,v12\n"
"v_fma_f32 v12,v12,v11,s13\n"
AW "a175,v12\n"
AR "v12,a192\n"
"v_subrev_f32 v12,s13,v12\n"
"v_fma_f32 v12,v12,v11,s13\n"
AW "a192,v12\n"
AR "v12,a193\n"
"v_subrev_f32 v12,s13,v12\n"
"v_fma_f32 v12,v12,v11,s13\n"
AW "a193,v12\n"
AR "v12,a194\n"
"v_subrev_f32 v12,s13,v12\n"
"v_fma_f32 v12,v12,v11,s13\n"
AW "a194,v12\n"
AR "v12,a195\n"
"v_subrev_f32 v12,s13,v12\n"
"v_fma_f32 v12,v12,v11,s13\n"
AW "a195,v12\n"
AR "v12,a212\n"
"v_subrev_f32 v12,s13,v12\n"
"v_fma_f32 v12,v12,v11,s13\n"
AW "a212,v12\n"
AR "v12,a213\n"
"v_subrev_f32 v12,s13,v12\n"
"v_fma_f32 v12,v12,v11,s13\n"
AW "a213,v12\n"
AR "v12,a214\n"
"v_subrev_f32 v12,s13,v12\n"
"v_fma_f32 v12,v12,v11,s13\n"
AW "a214,v12\n"
AR "v12,a215\n"
"v_subrev_f32 v12,s13,v12\n"
"v_fma_f32 v12,v12,v11,s13\n"
AW "a215,v12\n"
AR "v12,a232\n"
"v_subrev_f32 v12,s13,v12\n"
"v_fma_f32 v12,v12,v11,s13\n"
AW "a232,v12\n"
AR "v12,a233\n"
"v_subrev_f32 v12,s13,v12\n"
"v_fma_f32 v12,v12,v11,s13\n"
AW "a233,v12\n"
AR "v12,a234\n"
"v_subrev_f32 v12,s13,v12\n"
"v_fma_f32 v12,v12,v11,s13\n"
AW "a234,v12\n"
AR "v12,a235\n"
"v_subrev_f32 v12,s13,v12\n"
"v_fma_f32 v12,v12,v11,s13\n"
AW "a235,v12\n"
"v_sub_f32 v44,v44,v10\n"
"v_sub_f32 v45,v45,v10\n"
"v_sub_f32 v46,v46,v10\n"
"v_sub_f32 v47,v47,v10\n"
"v_sub_f32 v104,v104,v10\n"
"v_sub_f32 v105,v105,v10\n"
"v_sub_f32 v106,v106,v10\n"
"v_sub_f32 v107,v107,v10\n"
"v_sub_f32 v84,v84,v10\n"
"v_sub_f32 v85,v85,v10\n"
"v_sub_f32 v86,v86,v10\n"
"v_sub_f32 v87,v87,v10\n"
"v_sub_f32 v124,v124,v10\n"
"v_sub_f32 v125,v125,v10\n"
"v_sub_f32 v126,v126,v10\n"
"v_sub_f32 v127,v127,v10\n"
"v_exp_f32 v84,v84\n"
"v_exp_f32 v85,v85\n"
"v_exp_f32 v86,v86\n"
"v_exp_f32 v87,v87\n"
"v_exp_f32 v124,v124\n"
"v_exp_f32 v125,v125\n"
"v_exp_f32 v126,v126\n"
"v_exp_f32 v127,v127\n"
"v_add_f32 v55,v55,v84\n"
"v_add_f32 v55,v55,v85\n"
"v_add_f32 v55,v55,v86\n"
"v_add_f32 v55,v55,v87\n"
"v_add_f32 v55,v55,v124\n"
"v_add_f32 v55,v55,v125\n"
"v_add_f32 v55,v55,v126\n"
"v_add_f32 v55,v55,v127\n"
CV "v84,v84,v85\n"
CV "v85,v86,v87\n"
CV "v86,v124,v125\n"
CV "v87,v126,v127\n"
"v_max3_f32 v10,v88,v89,v90\n"
"v_max3_f32 v11,v91,v128,v129\n"
"v_max3_f32 v10,v10,v130,v131\n"
"v_max_f32 v10,v10,v11\n"
"v_mov_b32 v11,v10\n"
"s_nop 1\n"
"v_permlane16_swap_b32 v10,v11\n"
"v_max_f32 v10,v10,v11\n"
"v_mov_b32 v11,v10\n"
"s_nop 1\n"
"v_permlane32_swap_b32 v10,v11\n"
"v_max_f32_e64 v10,v10,v11\n"
"v_add_f32 v10,0x41300000,v10\n"
"v_max_f32 v10,0,v10\n"
"v_exp_f32 v11,-v10\n"
"s_nop 0\n"
"v_mul_f32_e64 v56,v61,v11\n"
AR "v12,a16\n"
"v_subrev_f32 v12,s13,v12\n"
"v_fma_f32 v12,v12,v11,s13\n"
AW "a16,v12\n"
AR "v12,a17\n"
"v_subrev_f32 v12,s13,v12\n"
"v_fma_f32 v12,v12,v11,s13\n"
AW "a17,v12\n"
AR "v12,a18\n"
"v_subrev_f32 v12,s13,v12\n"
"v_fma_f32 v12,v12,v11,s13\n"
AW "a18,v12\n"
AR "v12,a19\n"
"v_subrev_f32 v12,s13,v12\n"
"v_fma_f32 v12,v12,v11,s13\n"
AW "a19,v12\n"
AR "v12,a36\n"
"v_subrev_f32 v12,s13,v12\n"
"v_fma_f32 v12,v12,v11,s13\n"
AW "a36,v12\n"
AR "v12,a37\n"
"v_subrev_f32 v12,s13,v12\n"
"v_fma_f32 v12,v12,v11,s13\n"
AW "a37,v12\n"
AR "v12,a38\n"
"v_subrev_f32 v12,s13,v12\n"
"v_fma_f32 v12,v12,v11,s13\n"
AW "a38,v12\n"
AR "v12,a39\n"
"v_subrev_f32 v12,s13,v12\n"
"v_fma_f32 v12,v12,v11,s13\n"
AW "a39,v12\n"
AR "v12,a56\n"
"v_subrev_f32 v12,s13,v12\n"
"v_fma_f32 v12,v12,v11,s13\n"
AW "a56,v12\n"
AR "v12,a57\n"
"v_subrev_f32 v12,s13,v12\n"
"v_fma_f32 v12,v12,v11,s13\n"
AW "a57,v12\n"
AR "v12,a58\n"
"v_subrev_f32 v12,s13,v12\n"
"v_fma_f32 v12,v12,v11,s13\n"
AW "a58,v12\n"
AR "v12,a59\n"
"v_subrev_f32 v12,s13,v12\n"
"v_fma_f32 v12,v12,v11,s13\n"
AW "a59,v12\n"
AR "v12,a76\n"
"v_subrev_f32 v12,s13,v12\n"
"v_fma_f32 v12,v12,v11,s13\n"
AW "a76,v12\n"
AR "v12,a77\n"
"v_subrev_f32 v12,s13,v12\n"
"v_fma_f32 v12,v12,v11,s13\n"
AW "a77,v12\n"
AR "v12,a78\n"
"v_subrev_f32 v12,s13,v12\n"
"v_fma_f32 v12,v12,v11,s13\n"
AW "a78,v12\n"
AR "v12,a79\n"
"v_subrev_f32 v12,s13,v12\n"
"v_fma_f32 v12,v12,v11,s13\n"
AW "a79,v12\n"
AR "v12,a96\n"
"v_subrev_f32 v12,s13,v12\n"
"v_fma_f32 v12,v12,v11,s13\n"
AW "a96,v12\n"
AR "v12,a97\n"
"v_subrev_f32 v12,s13,v12\n"
"v_fma_f32 v12,v12,v11,s13\n"
AW "a97,v12\n"
AR "v12,a98\n"
"v_subrev_f32 v12,s13,v12\n"
"v_fma_f32 v12,v12,v11,s13\n"
AW "a98,v12\n"
AR "v12,a99\n"
"v_subrev_f32 v12,s13,v12\n"
"v_fma_f32 v12,v12,v11,s13\n"
AW "a99,v12\n"
AR "v12,a116\n"
"v_subrev_f32 v12,s13,v12\n"
"v_fma_f32 v12,v12,v11,s13\n"
AW "a116,v12\n"
AR "v12,a117\n"
"v_subrev_f32 v12,s13,v12\n"
"v_fma_f32 v12,v12,v11,s13\n"
AW "a117,v12\n"
AR "v12,a118\n"
"v_subrev_f32 v12,s13,v12\n"
"v_fma_f32 v12,v12,v11,s13\n"
AW "a118,v12\n"
AR "v12,a119\n"
"v_subrev_f32 v12,s13,v12\n"
"v_fma_f32 v12,v12,v11,s13\n"
AW "a119,v12\n"
AR "v12,a136\n"
"v_subrev_f32 v12,s13,v12\n"
"v_fma_f32 v12,v12,v11,s13\n"
AW "a136,v12\n"
AR "v12,a137\n"
"v_subrev_f32 v12,s13,v12\n"
"v_fma_f32 v12,v12,v11,s13\n"
AW "a137,v12\n"
AR "v12,a138\n"
"v_subrev_f32 v12,s13,v12\n"
"v_fma_f32 v12,v12,v11,s13\n"
AW "a138,v12\n"
AR "v12,a139\n"
"v_subrev_f32 v12,s13,v12\n"
"v_fma_f32 v12,v12,v11,s13\n"
AW "a139,v12\n"
AR "v12,a156\n"
"v_subrev_f32 v12,s13,v12\n"
"v_fma_f32 v12,v12,v11,s13\n"
AW "a156,v12\n"
AR "v12,a157\n"
"v_subrev_f32 v12,s13,v12\n"
"v_fma_f32 v12,v12,v11,s13\n"
AW "a157,v12\n"
AR "v12,a158\n"
"v_subrev_f32 v12,s13,v12\n"
"v_fma_f32 v12,v12,v11,s13\n"
AW "a158,v12\n"
AR "v12,a159\n"
"v_subrev_f32 v12,s13,v12\n"
"v_fma_f32 v12,v12,v11,s13\n"
AW "a159,v12\n"
AR "v12,a176\n"
"v_subrev_f32 v12,s13,v12\n"
"v_fma_f32 v12,v12,v11,s13\n"
AW "a176,v12\n"
AR "v12,a177\n"
"v_subrev_f32 v12,s13,v12\n"
"v_fma_f32 v12,v12,v11,s13\n"
AW "a177,v12\n"
AR "v12,a178\n"
"v_subrev_f32 v12,s13,v12\n"
"v_fma_f32 v12,v12,v11,s13\n"
AW "a178,v12\n"
AR "v12,a179\n"
"v_subrev_f32 v12,s13,v12\n"
"v_fma_f32 v12,v12,v11,s13\n"
AW "a179,v12\n"
AR "v12,a196\n"
"v_subrev_f32 v12,s13,v12\n"
"v_fma_f32 v12,v12,v11,s13\n"
AW "a196,v12\n"
AR "v12,a197\n"
"v_subrev_f32 v12,s13,v12\n"
"v_fma_f32 v12,v12,v11,s13\n"
AW "a197,v12\n"
AR "v12,a198\n"
"v_subrev_f32 v12,s13,v12\n"
"v_fma_f32 v12,v12,v11,s13\n"
AW "a198,v12\n"
AR "v12,a199\n"
"v_subrev_f32 v12,s13,v12\n"
"v_fma_f32 v12,v12,v11,s13\n"
AW "a199,v12\n"
AR "v12,a216\n"
"v_subrev_f32 v12,s13,v12\n"
"v_fma_f32 v12,v12,v11,s13\n"
AW "a216,v12\n"
AR "v12,a217\n"
"v_subrev_f32 v12,s13,v12\n"
"v_fma_f32 v12,v12,v11,s13\n"
AW "a217,v12\n"
AR "v12,a218\n"
"v_subrev_f32 v12,s13,v12\n"
"v_fma_f32 v12,v12,v11,s13\n"
AW "a218,v12\n"
AR "v12,a219\n"
"v_subrev_f32 v12,s13,v12\n"
"v_fma_f32 v12,v12,v11,s13\n"
AW "a219,v12\n"
AR "v12,a236\n"
"v_subrev_f32 v12,s13,v12\n"
"v_fma_f32 v12,v12,v11,s13\n"
AW "a236,v12\n"
AR "v12,a237\n"
"v_subrev_f32 v12,s13,v12\n"
"v_fma_f32 v12,v12,v11,s13\n"
AW "a237,v12\n"
AR "v12,a238\n"
"v_subrev_f32 v12,s13,v12\n"
"v_fma_f32 v12,v12,v11,s13\n"
AW "a238,v12\n"
AR "v12,a239\n"
"v_subrev_f32 v12,s13,v12\n"
"v_fma_f32 v12,v12,v11,s13\n"
AW "a239,v12\n"
"v_sub_f32 v48,v48,v10\n"
"v_sub_f32 v49,v49,v10\n"
"v_sub_f32 v50,v50,v10\n"
"v_sub_f32 v51,v51,v10\n"
"v_sub_f32 v108,v108,v10\n"
"v_sub_f32 v109,v109,v10\n"
"v_sub_f32 v110,v110,v10\n"
"v_sub_f32 v111,v111,v10\n"
"v_sub_f32 v88,v88,v10\n"
"v_sub_f32 v89,v89,v10\n"
"v_sub_f32 v90,v90,v10\n"
"v_sub_f32 v91,v91,v10\n"
"v_sub_f32 v128,v128,v10\n"
"v_sub_f32 v129,v129,v10\n"
"v_sub_f32 v130,v130,v10\n"
"v_sub_f32 v131,v131,v10\n"
"v_exp_f32 v88,v88\n"
"v_exp_f32 v89,v89\n"
"v_exp_f32 v90,v90\n"
"v_exp_f32 v91,v91\n"
"v_exp_f32 v128,v128\n"
"v_exp_f32 v129,v129\n"
"v_exp_f32 v130,v130\n"
"v_exp_f32 v131,v131\n"
"v_add_f32 v56,v56,v88\n"
"v_add_f32 v56,v56,v89\n"
"v_add_f32 v56,v56,v90\n"
"v_add_f32 v56,v56,v91\n"
"v_add_f32 v56,v56,v128\n"
"v_add_f32 v56,v56,v129\n"
"v_add_f32 v56,v56,v130\n"
"v_add_f32 v56,v56,v131\n"
CV "v88,v88,v89\n"
CV "v89,v90,v91\n"
CV "v90,v128,v129\n"
CV "v91,v130,v131\n"
"s_nop 7\n"
"s_setpc_b64 s[52:53]\n"
".Lslow_1:\n"
"s_nop 7\n"
"s_nop 7\n"
"v_and_b32 v10,240,v9\n"
"v_mul_u32_u24 v10,24,v10\n"
"v_lshrrev_b32 v11,4,v9\n"
"v_and_or_b32 v10,v11,48,v10\n"
"s_mov_b32 s59,s34\n"
"s_mov_b32 s34,s20\n"
"s_add_u32 s58,s74,0\n"
"s_nop 3\n"
LD "v[58:61],v10,s[32:35],s58 offen\n"
"s_waitcnt vmcnt(0)\n"
MF "v[92:95],v[58:61],v[136:139],v[32:35]\n"
MF "v[96:99],v[58:61],v[140:143],v[36:39]\n"
MF "v[100:103],v[58:61],v[144:147],v[40:43]\n"
MF "v[104:107],v[58:61],v[148:151],v[44:47]\n"
MF "v[108:111],v[58:61],v[152:155],v[48:51]\n"
"s_add_u32 s58,s74,0\n"
"s_nop 3\n"
LD "v[58:61],v10,s[32:35],s58 offen offset:64\n"
"s_waitcnt vmcnt(0)\n"
MF "v[92:95],v[58:61],v[156:159],v[92:95]\n"
MF "v[96:99],v[58:61],v[160:163],v[96:99]\n"
MF "v[100:103],v[58:61],v[164:167],v[100:103]\n"
MF "v[104:107],v[58:61],v[168:171],v[104:107]\n"
MF "v[108:111],v[58:61],v[172:175],v[108:111]\n"
"s_add_u32 s58,s74,0\n"
"s_nop 3\n"
LD "v[58:61],v10,s[32:35],s58 offen offset:128\n"
"s_waitcnt vmcnt(0)\n"
MF "v[92:95],v[58:61],v[176:179],v[92:95]\n"
MF "v[96:99],v[58:61],v[180:183],v[96:99]\n"
MF "v[100:103],v[58:61],v[184:187],v[100:103]\n"
MF "v[104:107],v[58:61],v[188:191],v[104:107]\n"
MF "v[108:111],v[58:61],v[192:195],v[108:111]\n"
"s_add_u32 s58,s74,0\n"
"s_nop 3\n"
LD "v[58:61],v10,s[32:35],s58 offen offset:192\n"
"s_waitcnt vmcnt(0)\n"
MF "v[92:95],v[58:61],v[196:199],v[92:95]\n"
MF "v[96:99],v[58:61],v[200:203],v[96:99]\n"
MF "v[100:103],v[58:61],v[204:207],v[100:103]\n"
MF "v[104:107],v[58:61],v[208:211],v[104:107]\n"
MF "v[108:111],v[58:61],v[212:215],v[108:111]\n"
"s_add_u32 s58,s74,0\n"
"s_nop 3\n"
LD "v[58:61],v10,s[32:35],s58 offen offset:256\n"
"s_waitcnt vmcnt(0)\n"
MF "v[92:95],v[58:61],v[216:219],v[92:95]\n"
MF "v[96:99],v[58:61],v[220:223],v[96:99]\n"
MF "v[100:103],v[58:61],v[224:227],v[100:103]\n"
MF "v[104:107],v[58:61],v[228:231],v[104:107]\n"
MF "v[108:111],v[58:61],v[232:235],v[108:111]\n"
"s_add_u32 s58,s74,0\n"
"s_nop 3\n"
LD "v[58:61],v10,s[32:35],s58 offen offset:320\n"
"s_waitcnt vmcnt(0)\n"
MF "v[92:95],v[58:61],v[236:239],v[92:95]\n"
MF "v[96:99],v[58:61],v[240:243],v[96:99]\n"
MF "v[100:103],v[58:61],v[244:247],v[100:103]\n"
MF "v[104:107],v[58:61],v[248:251],v[104:107]\n"
MF "v[108:111],v[58:61],v[252:255],v[108:111]\n"
"s_add_u32 s58,s74,6144\n"
"s_nop 3\n"
LD "v[58:61],v10,s[32:35],s58 offen\n"
"s_waitcnt vmcnt(0)\n"
MF "v[112:115],v[58:61],v[136:139],v[32:35]\n"
MF "v[116:119],v[58:61],v[140:143],v[36:39]\n"
MF "v[120:123],v[58:61],v[144:147],v[40:43]\n"
MF "v[124:127],v[58:61],v[148:151],v[44:47]\n"
MF "v[128:131],v[58:61],v[152:155],v[48:51]\n"
"s_add_u32 s58,s74,6144\n"
"s_nop 3\n"
LD "v[58:61],v10,s[32:35],s58 offen offset:64\n"
"s_waitcnt vmcnt(0)\n"
MF "v[112:115],v[58:61],v[156:159],v[112:115]\n"
MF "v[116:119],v[58:61],v[160:163],v[116:119]\n"
MF "v[120:123],v[58:61],v[164:167],v[120:123]\n"
MF "v[124:127],v[58:61],v[168:171],v[124:127]\n"
MF "v[128:131],v[58:61],v[172:175],v[128:131]\n"
"s_add_u32 s58,s74,6144\n"
"s_nop 3\n"
LD "v[58:61],v10,s[32:35],s58 offen offset:128\n"
"s_waitcnt vmcnt(0)\n"
MF "v[112:115],v[58:61],v[176:179],v[112:115]\n"
MF "v[116:119],v[58:61],v[180:183],v[116:119]\n"
MF "v[120:123],v[58:61],v[184:187],v[120:123]\n"
MF "v[124:127],v[58:61],v[188:191],v[124:127]\n"
MF "v[128:131],v[58:61],v[192:195],v[128:131]\n"
"s_add_u32 s58,s74,6144\n"
"s_nop 3\n"
LD "v[58:61],v10,s[32:35],s58 offen offset:192\n"
"s_waitcnt vmcnt(0)\n"
MF "v[112:115],v[58:61],v[196:199],v[112:115]\n"
MF "v[116:119],v[58:61],v[200:203],v[116:119]\n"
MF "v[120:123],v[58:61],v[204:207],v[120:123]\n"
MF "v[124:127],v[58:61],v[208:211],v[124:127]\n"
MF "v[128:131],v[58:61],v[212:215],v[128:131]\n"
"s_add_u32 s58,s74,6144\n"
"s_nop 3\n"
LD "v[58:61],v10,s[32:35],s58 offen offset:256\n"
"s_waitcnt vmcnt(0)\n"
MF "v[112:115],v[58:61],v[216:219],v[112:115]\n"
MF "v[116:119],v[58:61],v[220:223],v[116:119]\n"
MF "v[120:123],v[58:61],v[224:227],v[120:123]\n"
MF "v[124:127],v[58:61],v[228:231],v[124:127]\n"
MF "v[128:131],v[58:61],v[232:235],v[128:131]\n"
"s_add_u32 s58,s74,6144\n"
"s_nop 3\n"
LD "v[58:61],v10,s[32:35],s58 offen offset:320\n"
"s_waitcnt vmcnt(0)\n"
MF "v[112:115],v[58:61],v[236:239],v[112:115]\n"
MF "v[116:119],v[58:61],v[240:243],v[116:119]\n"
MF "v[120:123],v[58:61],v[244:247],v[120:123]\n"
MF "v[124:127],v[58:61],v[248:251],v[124:127]\n"
MF "v[128:131],v[58:61],v[252:255],v[128:131]\n"
"s_nop 7\n"
"s_mov_b32 s34,s59\n"
"v_max3_f32 v10,v92,v93,v94\n"
"s_nop 0\n"
"v_max3_f32 v11,v95,v112,v113\n"
"v_max3_f32 v10,v10,v114,v115\n"
"v_max_f32 v10,v10,v11\n"
"v_mov_b32 v11,v10\n"
"s_nop 1\n"
"v_permlane16_swap_b32 v10,v11\n"
"v_max_f32 v10,v10,v11\n"
"v_mov_b32 v11,v10\n"
"s_nop 1\n"
"v_permlane32_swap_b32 v10,v11\n"
"v_max_f32 v10,v10,v11\n"
"v_add_f32 v10,0x41300000,v10\n"
"v_max_f32 v10,0,v10\n"
"v_exp_f32 v11,-v10\n"
"s_nop 0\n"
"v_mul_f32_e64 v57,v52,v11\n"
AR "v12,a0\n"
"v_subrev_f32 v12,s13,v12\n"
"v_fma_f32 v12,v12,v11,s13\n"
AW "a0,v12\n"
AR "v12,a1\n"
"v_subrev_f32 v12,s13,v12\n"
"v_fma_f32 v12,v12,v11,s13\n"
AW "a1,v12\n"
AR "v12,a2\n"
"v_subrev_f32 v12,s13,v12\n"
"v_fma_f32 v12,v12,v11,s13\n"
AW "a2,v12\n"
AR "v12,a3\n"
"v_subrev_f32 v12,s13,v12\n"
"v_fma_f32 v12,v12,v11,s13\n"
AW "a3,v12\n"
AR "v12,a20\n"
"v_subrev_f32 v12,s13,v12\n"
"v_fma_f32 v12,v12,v11,s13\n"
AW "a20,v12\n"
AR "v12,a21\n"
"v_subrev_f32 v12,s13,v12\n"
"v_fma_f32 v12,v12,v11,s13\n"
AW "a21,v12\n"
AR "v12,a22\n"
"v_subrev_f32 v12,s13,v12\n"
"v_fma_f32 v12,v12,v11,s13\n"
AW "a22,v12\n"
AR "v12,a23\n"
"v_subrev_f32 v12,s13,v12\n"
"v_fma_f32 v12,v12,v11,s13\n"
AW "a23,v12\n"
AR "v12,a40\n"
"v_subrev_f32 v12,s13,v12\n"
"v_fma_f32 v12,v12,v11,s13\n"
AW "a40,v12\n"
AR "v12,a41\n"
"v_subrev_f32 v12,s13,v12\n"
"v_fma_f32 v12,v12,v11,s13\n"
AW "a41,v12\n"
AR "v12,a42\n"
"v_subrev_f32 v12,s13,v12\n"
"v_fma_f32 v12,v12,v11,s13\n"
AW "a42,v12\n"
AR "v12,a43\n"
"v_subrev_f32 v12,s13,v12\n"
"v_fma_f32 v12,v12,v11,s13\n"
AW "a43,v12\n"
AR "v12,a60\n"
"v_subrev_f32 v12,s13,v12\n"
"v_fma_f32 v12,v12,v11,s13\n"
AW "a60,v12\n"
AR "v12,a61\n"
"v_subrev_f32 v12,s13,v12\n"
"v_fma_f32 v12,v12,v11,s13\n"
AW "a61,v12\n"
AR "v12,a62\n"
"v_subrev_f32 v12,s13,v12\n"
"v_fma_f32 v12,v12,v11,s13\n"
AW "a62,v12\n"
AR "v12,a63\n"
"v_subrev_f32 v12,s13,v12\n"
"v_fma_f32 v12,v12,v11,s13\n"
AW "a63,v12\n"
AR "v12,a80\n"
"v_subrev_f32 v12,s13,v12\n"
"v_fma_f32 v12,v12,v11,s13\n"
AW "a80,v12\n"
AR "v12,a81\n"
"v_subrev_f32 v12,s13,v12\n"
"v_fma_f32 v12,v12,v11,s13\n"
AW "a81,v12\n"
AR "v12,a82\n"
"v_subrev_f32 v12,s13,v12\n"
"v_fma_f32 v12,v12,v11,s13\n"
AW "a82,v12\n"
AR "v12,a83\n"
"v_subrev_f32 v12,s13,v12\n"
"v_fma_f32 v12,v12,v11,s13\n"
AW "a83,v12\n"
AR "v12,a100\n"
"v_subrev_f32 v12,s13,v12\n"
"v_fma_f32 v12,v12,v11,s13\n"
AW "a100,v12\n"
AR "v12,a101\n"
"v_subrev_f32 v12,s13,v12\n"
"v_fma_f32 v12,v12,v11,s13\n"
AW "a101,v12\n"
AR "v12,a102\n"
"v_subrev_f32 v12,s13,v12\n"
"v_fma_f32 v12,v12,v11,s13\n"
AW "a102,v12\n"
AR "v12,a103\n"
"v_subrev_f32 v12,s13,v12\n"
"v_fma_f32 v12,v12,v11,s13\n"
AW "a103,v12\n"
AR "v12,a120\n"
"v_subrev_f32 v12,s13,v12\n"
"v_fma_f32 v12,v12,v11,s13\n"
AW "a120,v12\n"
AR "v12,a121\n"
"v_subrev_f32 v12,s13,v12\n"
"v_fma_f32 v12,v12,v11,s13\n"
AW "a121,v12\n"
AR "v12,a122\n"
"v_subrev_f32 v12,s13,v12\n"
"v_fma_f32 v12,v12,v11,s13\n"
AW "a122,v12\n"
AR "v12,a123\n"
"v_subrev_f32 v12,s13,v12\n"
"v_fma_f32 v12,v12,v11,s13\n"
AW "a123,v12\n"
AR "v12,a140\n"
"v_subrev_f32 v12,s13,v12\n"
"v_fma_f32 v12,v12,v11,s13\n"
AW "a140,v12\n"
AR "v12,a141\n"
"v_subrev_f32 v12,s13,v12\n"
"v_fma_f32 v12,v12,v11,s13\n"
AW "a141,v12\n"
AR "v12,a142\n"
"v_subrev_f32 v12,s13,v12\n"
"v_fma_f32 v12,v12,v11,s13\n"
AW "a142,v12\n"
AR "v12,a143\n"
"v_subrev_f32 v12,s13,v12\n"
"v_fma_f32 v12,v12,v11,s13\n"
AW "a143,v12\n"
AR "v12,a160\n"
"v_subrev_f32 v12,s13,v12\n"
"v_fma_f32 v12,v12,v11,s13\n"
AW "a160,v12\n"
AR "v12,a161\n"
"v_subrev_f32 v12,s13,v12\n"
"v_fma_f32 v12,v12,v11,s13\n"
AW "a161,v12\n"
AR "v12,a162\n"
"v_subrev_f32 v12,s13,v12\n"
"v_fma_f32 v12,v12,v11,s13\n"
AW "a162,v12\n"
AR "v12,a163\n"
"v_subrev_f32 v12,s13,v12\n"
"v_fma_f32 v12,v12,v11,s13\n"
AW "a163,v12\n"
AR "v12,a180\n"
"v_subrev_f32 v12,s13,v12\n"
"v_fma_f32 v12,v12,v11,s13\n"
AW "a180,v12\n"
AR "v12,a181\n"
"v_subrev_f32 v12,s13,v12\n"
"v_fma_f32 v12,v12,v11,s13\n"
AW "a181,v12\n"
AR "v12,a182\n"
"v_subrev_f32 v12,s13,v12\n"
"v_fma_f32 v12,v12,v11,s13\n"
AW "a182,v12\n"
AR "v12,a183\n"
"v_subrev_f32 v12,s13,v12\n"
"v_fma_f32 v12,v12,v11,s13\n"
AW "a183,v12\n"
AR "v12,a200\n"
"v_subrev_f32 v12,s13,v12\n"
"v_fma_f32 v12,v12,v11,s13\n"
AW "a200,v12\n"
AR "v12,a201\n"
"v_subrev_f32 v12,s13,v12\n"
"v_fma_f32 v12,v12,v11,s13\n"
AW "a201,v12\n"
AR "v12,a202\n"
"v_subrev_f32 v12,s13,v12\n"
"v_fma_f32 v12,v12,v11,s13\n"
AW "a202,v12\n"
AR "v12,a203\n"
"v_subrev_f32 v12,s13,v12\n"
"v_fma_f32 v12,v12,v11,s13\n"
AW "a203,v12\n"
AR "v12,a220\n"
"v_subrev_f32 v12,s13,v12\n"
"v_fma_f32 v12,v12,v11,s13\n"
AW "a220,v12\n"
AR "v12,a221\n"
"v_subrev_f32 v12,s13,v12\n"
"v_fma_f32 v12,v12,v11,s13\n"
AW "a221,v12\n"
AR "v12,a222\n"
"v_subrev_f32 v12,s13,v12\n"
"v_fma_f32 v12,v12,v11,s13\n"
AW "a222,v12\n"
AR "v12,a223\n"
"v_subrev_f32 v12,s13,v12\n"
"v_fma_f32 v12,v12,v11,s13\n"
AW "a223,v12\n"
"v_sub_f32 v32,v32,v10\n"
"v_sub_f32 v33,v33,v10\n"
"v_sub_f32 v34,v34,v10\n"
"v_sub_f32 v35,v35,v10\n"
"v_sub_f32 v72,v72,v10\n"
"v_sub_f32 v73,v73,v10\n"
"v_sub_f32 v74,v74,v10\n"
"v_sub_f32 v75,v75,v10\n"
"v_sub_f32 v92,v92,v10\n"
"v_sub_f32 v93,v93,v10\n"
"v_sub_f32 v94,v94,v10\n"
"v_sub_f32 v95,v95,v10\n"
"v_sub_f32 v112,v112,v10\n"
"v_sub_f32 v113,v113,v10\n"
"v_sub_f32 v114,v114,v10\n"
"v_sub_f32 v115,v115,v10\n"
"v_exp_f32 v92,v92\n"
"v_exp_f32 v93,v93\n"
"v_exp_f32 v94,v94\n"
"v_exp_f32 v95,v95\n"
"v_exp_f32 v112,v112\n"
"v_exp_f32 v113,v113\n"
"v_exp_f32 v114,v114\n"
"v_exp_f32 v115,v115\n"
"v_add_f32 v57,v57,v92\n"
"v_add_f32 v57,v57,v93\n"
"v_add_f32 v57,v57,v94\n"
"v_add_f32 v57,v57,v95\n"
"v_add_f32 v57,v57,v112\n"
"v_add_f32 v57,v57,v113\n"
"v_add_f32 v57,v57,v114\n"
"v_add_f32 v57,v57,v115\n"
CV "v92,v92,v93\n"
CV "v93,v94,v95\n"
CV "v94,v112,v113\n"
CV "v95,v114,v115\n"
"v_max3_f32 v10,v96,v97,v98\n"
"v_max3_f32 v11,v99,v116,v117\n"
"v_max3_f32 v10,v10,v118,v119\n"
"v_max_f32 v10,v10,v11\n"
"v_mov_b32 v11,v10\n"
"s_nop 1\n"
"v_permlane16_swap_b32 v10,v11\n"
"v_max_f32 v10,v10,v11\n"
"v_mov_b32 v11,v10\n"
"s_nop 1\n"
"v_permlane32_swap_b32 v10,v11\n"
"v_max_f32_e64 v10,v10,v11\n"
"v_add_f32 v10,0x41300000,v10\n"
"v_max_f32 v10,0,v10\n"
"v_exp_f32 v11,-v10\n"
"s_nop 0\n"
"v_mul_f32_e64 v58,v53,v11\n"
AR "v12,a4\n"
"v_subrev_f32 v12,s13,v12\n"
"v_fma_f32 v12,v12,v11,s13\n"
AW "a4,v12\n"
AR "v12,a5\n"
"v_subrev_f32 v12,s13,v12\n"
"v_fma_f32 v12,v12,v11,s13\n"
AW "a5,v12\n"
AR "v12,a6\n"
"v_subrev_f32 v12,s13,v12\n"
"v_fma_f32 v12,v12,v11,s13\n"
AW "a6,v12\n"
AR "v12,a7\n"
"v_subrev_f32 v12,s13,v12\n"
"v_fma_f32 v12,v12,v11,s13\n"
AW "a7,v12\n"
AR "v12,a24\n"
"v_subrev_f32 v12,s13,v12\n"
"v_fma_f32 v12,v12,v11,s13\n"
AW "a24,v12\n"
AR "v12,a25\n"
"v_subrev_f32 v12,s13,v12\n"
"v_fma_f32 v12,v12,v11,s13\n"
AW "a25,v12\n"
AR "v12,a26\n"
"v_subrev_f32 v12,s13,v12\n"
"v_fma_f32 v12,v12,v11,s13\n"
AW "a26,v12\n"
AR "v12,a27\n"
"v_subrev_f32 v12,s13,v12\n"
"v_fma_f32 v12,v12,v11,s13\n"
AW "a27,v12\n"
AR "v12,a44\n"
"v_subrev_f32 v12,s13,v12\n"
"v_fma_f32 v12,v12,v11,s13\n"
AW "a44,v12\n"
AR "v12,a45\n"
"v_subrev_f32 v12,s13,v12\n"
"v_fma_f32 v12,v12,v11,s13\n"
AW "a45,v12\n"
AR "v12,a46\n"
"v_subrev_f32 v12,s13,v12\n"
"v_fma_f32 v12,v12,v11,s13\n"
AW "a46,v12\n"
AR "v12,a47\n"
"v_subrev_f32 v12,s13,v12\n"
"v_fma_f32 v12,v12,v11,s13\n"
AW "a47,v12\n"
AR "v12,a64\n"
"v_subrev_f32 v12,s13,v12\n"
"v_fma_f32 v12,v12,v11,s13\n"
AW "a64,v12\n"
AR "v12,a65\n"
"v_subrev_f32 v12,s13,v12\n"
"v_fma_f32 v12,v12,v11,s13\n"
AW "a65,v12\n"
AR "v12,a66\n"
"v_subrev_f32 v12,s13,v12\n"
"v_fma_f32 v12,v12,v11,s13\n"
AW "a66,v12\n"
AR "v12,a67\n"
"v_subrev_f32 v12,s13,v12\n"
"v_fma_f32 v12,v12,v11,s13\n"
AW "a67,v12\n"
AR "v12,a84\n"
"v_subrev_f32 v12,s13,v12\n"
"v_fma_f32 v12,v12,v11,s13\n"
AW "a84,v12\n"
AR "v12,a85\n"
"v_subrev_f32 v12,s13,v12\n"
"v_fma_f32 v12,v12,v11,s13\n"
AW "a85,v12\n"
AR "v12,a86\n"
"v_subrev_f32 v12,s13,v12\n"
"v_fma_f32 v12,v12,v11,s13\n"
AW "a86,v12\n"
AR "v12,a87\n"
"v_subrev_f32 v12,s13,v12\n"
"v_fma_f32 v12,v12,v11,s13\n"
AW "a87,v12\n"
AR "v12,a104\n"
"v_subrev_f32 v12,s13,v12\n"
"v_fma_f32 v12,v12,v11,s13\n"
AW "a104,v12\n"
AR "v12,a105\n"
"v_subrev_f32 v12,s13,v12\n"
"v_fma_f32 v12,v12,v11,s13\n"
AW "a105,v12\n"
AR "v12,a106\n"
"v_subrev_f32 v12,s13,v12\n"
"v_fma_f32 v12,v12,v11,s13\n"
AW "a106,v12\n"
AR "v12,a107\n"
"v_subrev_f32 v12,s13,v12\n"
"v_fma_f32 v12,v12,v11,s13\n"
AW "a107,v12\n"
AR "v12,a124\n"
"v_subrev_f32 v12,s13,v12\n"
"v_fma_f32 v12,v12,v11,s13\n"
AW "a124,v12\n"
AR "v12,a125\n"
"v_subrev_f32 v12,s13,v12\n"
"v_fma_f32 v12,v12,v11,s13\n"
AW "a125,v12\n"
AR "v12,a126\n"
"v_subrev_f32 v12,s13,v12\n"
"v_fma_f32 v12,v12,v11,s13\n"
AW "a126,v12\n"
AR "v12,a127\n"
"v_subrev_f32 v12,s13,v12\n"
"v_fma_f32 v12,v12,v11,s13\n"
AW "a127,v12\n"
AR "v12,a144\n"
"v_subrev_f32 v12,s13,v12\n"
"v_fma_f32 v12,v12,v11,s13\n"
AW "a144,v12\n"
AR "v12,a145\n"
"v_subrev_f32 v12,s13,v12\n"
"v_fma_f32 v12,v12,v11,s13\n"
AW "a145,v12\n"
AR "v12,a146\n"
"v_subrev_f32 v12,s13,v12\n"
"v_fma_f32 v12,v12,v11,s13\n"
AW "a146,v12\n"
AR "v12,a147\n"
"v_subrev_f32 v12,s13,v12\n"
"v_fma_f32 v12,v12,v11,s13\n"
AW "a147,v12\n"
AR "v12,a164\n"
"v_subrev_f32 v12,s13,v12\n"
"v_fma_f32 v12,v12,v11,s13\n"
AW "a164,v12\n"
AR "v12,a165\n"
"v_subrev_f32 v12,s13,v12\n"
"v_fma_f32 v12,v12,v11,s13\n"
AW "a165,v12\n"
AR "v12,a166\n"
"v_subrev_f32 v12,s13,v12\n"
"v_fma_f32 v12,v12,v11,s13\n"
AW "a166,v12\n"
AR "v12,a167\n"
"v_subrev_f32 v12,s13,v12\n"
"v_fma_f32 v12,v12,v11,s13\n"
AW "a167,v12\n"
AR "v12,a184\n"
"v_subrev_f32 v12,s13,v12\n"
"v_fma_f32 v12,v12,v11,s13\n"
AW "a184,v12\n"
AR "v12,a185\n"
"v_subrev_f32 v12,s13,v12\n"
"v_fma_f32 v12,v12,v11,s13\n"
AW "a185,v12\n"
AR "v12,a186\n"
"v_subrev_f32 v12,s13,v12\n"
"v_fma_f32 v12,v12,v11,s13\n"
AW "a186,v12\n"
AR "v12,a187\n"
"v_subrev_f32 v12,s13,v12\n"
"v_fma_f32 v12,v12,v11,s13\n"
AW "a187,v12\n"
AR "v12,a204\n"
"v_subrev_f32 v12,s13,v12\n"
"v_fma_f32 v12,v12,v11,s13\n"
AW "a204,v12\n"
AR "v12,a205\n"
"v_subrev_f32 v12,s13,v12\n"
"v_fma_f32 v12,v12,v11,s13\n"
AW "a205,v12\n"
AR "v12,a206\n"
"v_subrev_f32 v12,s13,v12\n"
"v_fma_f32 v12,v12,v11,s13\n"
AW "a206,v12\n"
AR "v12,a207\n"
"v_subrev_f32 v12,s13,v12\n"
"v_fma_f32 v12,v12,v11,s13\n"
AW "a207,v12\n"
AR "v12,a224\n"
"v_subrev_f32 v12,s13,v12\n"
"v_fma_f32 v12,v12,v11,s13\n"
AW "a224,v12\n"
AR "v12,a225\n"
"v_subrev_f32 v12,s13,v12\n"
"v_fma_f32 v12,v12,v11,s13\n"
AW "a225,v12\n"
AR "v12,a226\n"
"v_subrev_f32 v12,s13,v12\n"
"v_fma_f32 v12,v12,v11,s13\n"
AW "a226,v12\n"
AR "v12,a227\n"
"v_subrev_f32 v12,s13,v12\n"
"v_fma_f32 v12,v12,v11,s13\n"
AW "a227,v12\n"
"v_sub_f32 v36,v36,v10\n"
"v_sub_f32 v37,v37,v10\n"
"v_sub_f32 v38,v38,v10\n"
"v_sub_f32 v39,v39,v10\n"
"v_sub_f32 v76,v76,v10\n"
"v_sub_f32 v77,v77,v10\n"
"v_sub_f32 v78,v78,v10\n"
"v_sub_f32 v79,v79,v10\n"
"v_sub_f32 v96,v96,v10\n"
"v_sub_f32 v97,v97,v10\n"
"v_sub_f32 v98,v98,v10\n"
"v_sub_f32 v99,v99,v10\n"
"v_sub_f32 v116,v116,v10\n"
"v_sub_f32 v117,v117,v10\n"
"v_sub_f32 v118,v118,v10\n"
"v_sub_f32 v119,v119,v10\n"
"v_exp_f32 v96,v96\n"
"v_exp_f32 v97,v97\n"
"v_exp_f32 v98,v98\n"
"v_exp_f32 v99,v99\n"
"v_exp_f32 v116,v116\n"
"v_exp_f32 v117,v117\n"
"v_exp_f32 v118,v118\n"
"v_exp_f32 v119,v119\n"
"v_add_f32 v58,v58,v96\n"
"v_add_f32 v58,v58,v97\n"
"v_add_f32 v58,v58,v98\n"
"v_add_f32 v58,v58,v99\n"
"v_add_f32 v58,v58,v116\n"
"v_add_f32 v58,v58,v117\n"
"v_add_f32 v58,v58,v118\n"
"v_add_f32 v58,v58,v119\n"
CV "v96,v96,v97\n"
CV "v97,v98,v99\n"
CV "v98,v116,v117\n"
CV "v99,v118,v119\n"
"v_max3_f32 v10,v100,v101,v102\n"
"v_max3_f32 v11,v103,v120,v121\n"
"v_max3_f32 v10,v10,v122,v123\n"
"v_max_f32 v10,v10,v11\n"
"v_mov_b32 v11,v10\n"
"s_nop 1\n"
"v_permlane16_swap_b32 v10,v11\n"
"v_max_f32 v10,v10,v11\n"
"v_mov_b32 v11,v10\n"
"s_nop 1\n"
"v_permlane32_swap_b32 v10,v11\n"
"v_max_f32_e64 v10,v10,v11\n"
"v_add_f32 v10,0x41300000,v10\n"
"v_max_f32 v10,0,v10\n"
"v_exp_f32 v11,-v10\n"
"s_nop 0\n"
"v_mul_f32_e64 v59,v54,v11\n"
AR "v12,a8\n"
"v_subrev_f32 v12,s13,v12\n"
"v_fma_f32 v12,v12,v11,s13\n"
AW "a8,v12\n"
AR "v12,a9\n"
"v_subrev_f32 v12,s13,v12\n"
"v_fma_f32 v12,v12,v11,s13\n"
AW "a9,v12\n"
AR "v12,a10\n"
"v_subrev_f32 v12,s13,v12\n"
"v_fma_f32 v12,v12,v11,s13\n"
AW "a10,v12\n"
AR "v12,a11\n"
"v_subrev_f32 v12,s13,v12\n"
"v_fma_f32 v12,v12,v11,s13\n"
AW "a11,v12\n"
AR "v12,a28\n"
"v_subrev_f32 v12,s13,v12\n"
"v_fma_f32 v12,v12,v11,s13\n"
AW "a28,v12\n"
AR "v12,a29\n"
"v_subrev_f32 v12,s13,v12\n"
"v_fma_f32 v12,v12,v11,s13\n"
AW "a29,v12\n"
AR "v12,a30\n"
"v_subrev_f32 v12,s13,v12\n"
"v_fma_f32 v12,v12,v11,s13\n"
AW "a30,v12\n"
AR "v12,a31\n"
"v_subrev_f32 v12,s13,v12\n"
"v_fma_f32 v12,v12,v11,s13\n"
AW "a31,v12\n"
AR "v12,a48\n"
"v_subrev_f32 v12,s13,v12\n"
"v_fma_f32 v12,v12,v11,s13\n"
AW "a48,v12\n"
AR "v12,a49\n"
"v_subrev_f32 v12,s13,v12\n"
"v_fma_f32 v12,v12,v11,s13\n"
AW "a49,v12\n"
AR "v12,a50\n"
"v_subrev_f32 v12,s13,v12\n"
"v_fma_f32 v12,v12,v11,s13\n"
AW "a50,v12\n"
AR "v12,a51\n"
"v_subrev_f32 v12,s13,v12\n"
"v_fma_f32 v12,v12,v11,s13\n"
AW "a51,v12\n"
AR "v12,a68\n"
"v_subrev_f32 v12,s13,v12\n"
"v_fma_f32 v12,v12,v11,s13\n"
AW "a68,v12\n"
AR "v12,a69\n"
"v_subrev_f32 v12,s13,v12\n"
"v_fma_f32 v12,v12,v11,s13\n"
AW "a69,v12\n"
AR "v12,a70\n"
"v_subrev_f32 v12,s13,v12\n"
"v_fma_f32 v12,v12,v11,s13\n"
AW "a70,v12\n"
AR "v12,a71\n"
"v_subrev_f32 v12,s13,v12\n"
"v_fma_f32 v12,v12,v11,s13\n"
AW "a71,v12\n"
AR "v12,a88\n"
"v_subrev_f32 v12,s13,v12\n"
"v_fma_f32 v12,v12,v11,s13\n"
AW "a88,v12\n"
AR "v12,a89\n"
"v_subrev_f32 v12,s13,v12\n"
"v_fma_f32 v12,v12,v11,s13\n"
AW "a89,v12\n"
AR "v12,a90\n"
"v_subrev_f32 v12,s13,v12\n"
"v_fma_f32 v12,v12,v11,s13\n"
AW "a90,v12\n"
AR "v12,a91\n"
"v_subrev_f32 v12,s13,v12\n"
"v_fma_f32 v12,v12,v11,s13\n"
AW "a91,v12\n"
AR "v12,a108\n"
"v_subrev_f32 v12,s13,v12\n"
"v_fma_f32 v12,v12,v11,s13\n"
AW "a108,v12\n"
AR "v12,a109\n"
"v_subrev_f32 v12,s13,v12\n"
"v_fma_f32 v12,v12,v11,s13\n"
AW "a109,v12\n"
AR "v12,a110\n"
"v_subrev_f32 v12,s13,v12\n"
"v_fma_f32 v12,v12,v11,s13\n"
AW "a110,v12\n"
AR "v12,a111\n"
"v_subrev_f32 v12,s13,v12\n"
"v_fma_f32 v12,v12,v11,s13\n"
AW "a111,v12\n"
AR "v12,a128\n"
"v_subrev_f32 v12,s13,v12\n"
"v_fma_f32 v12,v12,v11,s13\n"
AW "a128,v12\n"
AR "v12,a129\n"
"v_subrev_f32 v12,s13,v12\n"
"v_fma_f32 v12,v12,v11,s13\n"
AW "a129,v12\n"
AR "v12,a130\n"
"v_subrev_f32 v12,s13,v12\n"
"v_fma_f32 v12,v12,v11,s13\n"
AW "a130,v12\n"
AR "v12,a131\n"
"v_subrev_f32 v12,s13,v12\n"
"v_fma_f32 v12,v12,v11,s13\n"
AW "a131,v12\n"
AR "v12,a148\n"
"v_subrev_f32 v12,s13,v12\n"
"v_fma_f32 v12,v12,v11,s13\n"
AW "a148,v12\n"
AR "v12,a149\n"
"v_subrev_f32 v12,s13,v12\n"
"v_fma_f32 v12,v12,v11,s13\n"
AW "a149,v12\n"
AR "v12,a150\n"
"v_subrev_f32 v12,s13,v12\n"
"v_fma_f32 v12,v12,v11,s13\n"
AW "a150,v12\n"
AR "v12,a151\n"
"v_subrev_f32 v12,s13,v12\n"
"v_fma_f32 v12,v12,v11,s13\n"
AW "a151,v12\n"
AR "v12,a168\n"
"v_subrev_f32 v12,s13,v12\n"
"v_fma_f32 v12,v12,v11,s13\n"
AW "a168,v12\n"
AR "v12,a169\n"
"v_subrev_f32 v12,s13,v12\n"
"v_fma_f32 v12,v12,v11,s13\n"
AW "a169,v12\n"
AR "v12,a170\n"
"v_subrev_f32 v12,s13,v12\n"
"v_fma_f32 v12,v12,v11,s13\n"
AW "a170,v12\n"
AR "v12,a171\n"
"v_subrev_f32 v12,s13,v12\n"
"v_fma_f32 v12,v12,v11,s13\n"
AW "a171,v12\n"
AR "v12,a188\n"
"v_subrev_f32 v12,s13,v12\n"
"v_fma_f32 v12,v12,v11,s13\n"
AW "a188,v12\n"
AR "v12,a189\n"
"v_subrev_f32 v12,s13,v12\n"
"v_fma_f32 v12,v12,v11,s13\n"
AW "a189,v12\n"
AR "v12,a190\n"
"v_subrev_f32 v12,s13,v12\n"
"v_fma_f32 v12,v12,v11,s13\n"
AW "a190,v12\n"
AR "v12,a191\n"
"v_subrev_f32 v12,s13,v12\n"
"v_fma_f32 v12,v12,v11,s13\n"
AW "a191,v12\n"
AR "v12,a208\n"
"v_subrev_f32 v12,s13,v12\n"
"v_fma_f32 v12,v12,v11,s13\n"
AW "a208,v12\n"
AR "v12,a209\n"
"v_subrev_f32 v12,s13,v12\n"
"v_fma_f32 v12,v12,v11,s13\n"
AW "a209,v12\n"
AR "v12,a210\n"
"v_subrev_f32 v12,s13,v12\n"
"v_fma_f32 v12,v12,v11,s13\n"
AW "a210,v12\n"
AR "v12,a211\n"
"v_subrev_f32 v12,s13,v12\n"
"v_fma_f32 v12,v12,v11,s13\n"
AW "a211,v12\n"
AR "v12,a228\n"
"v_subrev_f32 v12,s13,v12\n"
"v_fma_f32 v12,v12,v11,s13\n"
AW "a228,v12\n"
AR "v12,a229\n"
"v_subrev_f32 v12,s13,v12\n"
"v_fma_f32 v12,v12,v11,s13\n"
AW "a229,v12\n"
AR "v12,a230\n"
"v_subrev_f32 v12,s13,v12\n"
"v_fma_f32 v12,v12,v11,s13\n"
AW "a230,v12\n"
AR "v12,a231\n"
"v_subrev_f32 v12,s13,v12\n"
"v_fma_f32 v12,v12,v11,s13\n"
AW "a231,v12\n"
"v_sub_f32 v40,v40,v10\n"
"v_sub_f32 v41,v41,v10\n"
"v_sub_f32 v42,v42,v10\n"
"v_sub_f32 v43,v43,v10\n"
"v_sub_f32 v80,v80,v10\n"
"v_sub_f32 v81,v81,v10\n"
"v_sub_f32 v82,v82,v10\n"
"v_sub_f32 v83,v83,v10\n"
"v_sub_f32 v100,v100,v10\n"
"v_sub_f32 v101,v101,v10\n"
"v_sub_f32 v102,v102,v10\n"
"v_sub_f32 v103,v103,v10\n"
"v_sub_f32 v120,v120,v10\n"
"v_sub_f32 v121,v121,v10\n"
"v_sub_f32 v122,v122,v10\n"
"v_sub_f32 v123,v123,v10\n"
"v_exp_f32 v100,v100\n"
"v_exp_f32 v101,v101\n"
"v_exp_f32 v102,v102\n"
"v_exp_f32 v103,v103\n"
"v_exp_f32 v120,v120\n"
"v_exp_f32 v121,v121\n"
"v_exp_f32 v122,v122\n"
"v_exp_f32 v123,v123\n"
"v_add_f32 v59,v59,v100\n"
"v_add_f32 v59,v59,v101\n"
"v_add_f32 v59,v59,v102\n"
"v_add_f32 v59,v59,v103\n"
"v_add_f32 v59,v59,v120\n"
"v_add_f32 v59,v59,v121\n"
"v_add_f32 v59,v59,v122\n"
"v_add_f32 v59,v59,v123\n"
CV "v100,v100,v101\n"
CV "v101,v102,v103\n"
CV "v102,v120,v121\n"
CV "v103,v122,v123\n"
"v_max3_f32 v10,v104,v105,v106\n"
"v_max3_f32 v11,v107,v124,v125\n"
"v_max3_f32 v10,v10,v126,v127\n"
"v_max_f32 v10,v10,v11\n"
"v_mov_b32 v11,v10\n"
"s_nop 1\n"
"v_permlane16_swap_b32 v10,v11\n"
"v_max_f32 v10,v10,v11\n"
"v_mov_b32 v11,v10\n"
"s_nop 1\n"
"v_permlane32_swap_b32 v10,v11\n"
"v_max_f32_e64 v10,v10,v11\n"
"v_add_f32 v10,0x41300000,v10\n"
"v_max_f32 v10,0,v10\n"
"v_exp_f32 v11,-v10\n"
"s_nop 0\n"
"v_mul_f32_e64 v60,v55,v11\n"
AR "v12,a12\n"
"v_subrev_f32 v12,s13,v12\n"
"v_fma_f32 v12,v12,v11,s13\n"
AW "a12,v12\n"
AR "v12,a13\n"
"v_subrev_f32 v12,s13,v12\n"
"v_fma_f32 v12,v12,v11,s13\n"
AW "a13,v12\n"
AR "v12,a14\n"
"v_subrev_f32 v12,s13,v12\n"
"v_fma_f32 v12,v12,v11,s13\n"
AW "a14,v12\n"
AR "v12,a15\n"
"v_subrev_f32 v12,s13,v12\n"
"v_fma_f32 v12,v12,v11,s13\n"
AW "a15,v12\n"
AR "v12,a32\n"
"v_subrev_f32 v12,s13,v12\n"
"v_fma_f32 v12,v12,v11,s13\n"
AW "a32,v12\n"
AR "v12,a33\n"
"v_subrev_f32 v12,s13,v12\n"
"v_fma_f32 v12,v12,v11,s13\n"
AW "a33,v12\n"
AR "v12,a34\n"
"v_subrev_f32 v12,s13,v12\n"
"v_fma_f32 v12,v12,v11,s13\n"
AW "a34,v12\n"
AR "v12,a35\n"
"v_subrev_f32 v12,s13,v12\n"
"v_fma_f32 v12,v12,v11,s13\n"
AW "a35,v12\n"
AR "v12,a52\n"
"v_subrev_f32 v12,s13,v12\n"
"v_fma_f32 v12,v12,v11,s13\n"
AW "a52,v12\n"
AR "v12,a53\n"
"v_subrev_f32 v12,s13,v12\n"
"v_fma_f32 v12,v12,v11,s13\n"
AW "a53,v12\n"
AR "v12,a54\n"
"v_subrev_f32 v12,s13,v12\n"
"v_fma_f32 v12,v12,v11,s13\n"
AW "a54,v12\n"
AR "v12,a55\n"
"v_subrev_f32 v12,s13,v12\n"
"v_fma_f32 v12,v12,v11,s13\n"
AW "a55,v12\n"
AR "v12,a72\n"
"v_subrev_f32 v12,s13,v12\n"
"v_fma_f32 v12,v12,v11,s13\n"
AW "a72,v12\n"
AR "v12,a73\n"
"v_subrev_f32 v12,s13,v12\n"
"v_fma_f32 v12,v12,v11,s13\n"
AW "a73,v12\n"
AR "v12,a74\n"
"v_subrev_f32 v12,s13,v12\n"
"v_fma_f32 v12,v12,v11,s13\n"
AW "a74,v12\n"
AR "v12,a75\n"
"v_subrev_f32 v12,s13,v12\n"
"v_fma_f32 v12,v12,v11,s13\n"
AW "a75,v12\n"
AR "v12,a92\n"
"v_subrev_f32 v12,s13,v12\n"
"v_fma_f32 v12,v12,v11,s13\n"
AW "a92,v12\n"
AR "v12,a93\n"
"v_subrev_f32 v12,s13,v12\n"
"v_fma_f32 v12,v12,v11,s13\n"
AW "a93,v12\n"
AR "v12,a94\n"
"v_subrev_f32 v12,s13,v12\n"
"v_fma_f32 v12,v12,v11,s13\n"
AW "a94,v12\n"
AR "v12,a95\n"
"v_subrev_f32 v12,s13,v12\n"
"v_fma_f32 v12,v12,v11,s13\n"
AW "a95,v12\n"
AR "v12,a112\n"
"v_subrev_f32 v12,s13,v12\n"
"v_fma_f32 v12,v12,v11,s13\n"
AW "a112,v12\n"
AR "v12,a113\n"
"v_subrev_f32 v12,s13,v12\n"
"v_fma_f32 v12,v12,v11,s13\n"
AW "a113,v12\n"
AR "v12,a114\n"
"v_subrev_f32 v12,s13,v12\n"
"v_fma_f32 v12,v12,v11,s13\n"
AW "a114,v12\n"
AR "v12,a115\n"
"v_subrev_f32 v12,s13,v12\n"
"v_fma_f32 v12,v12,v11,s13\n"
AW "a115,v12\n"
AR "v12,a132\n"
"v_subrev_f32 v12,s13,v12\n"
"v_fma_f32 v12,v12,v11,s13\n"
AW "a132,v12\n"
AR "v12,a133\n"
"v_subrev_f32 v12,s13,v12\n"
"v_fma_f32 v12,v12,v11,s13\n"
AW "a133,v12\n"
AR "v12,a134\n"
"v_subrev_f32 v12,s13,v12\n"
"v_fma_f32 v12,v12,v11,s13\n"
AW "a134,v12\n"
AR "v12,a135\n"
"v_subrev_f32 v12,s13,v12\n"
"v_fma_f32 v12,v12,v11,s13\n"
AW "a135,v12\n"
AR "v12,a152\n"
"v_subrev_f32 v12,s13,v12\n"
"v_fma_f32 v12,v12,v11,s13\n"
AW "a152,v12\n"
AR "v12,a153\n"
"v_subrev_f32 v12,s13,v12\n"
"v_fma_f32 v12,v12,v11,s13\n"
AW "a153,v12\n"
AR "v12,a154\n"
"v_subrev_f32 v12,s13,v12\n"
"v_fma_f32 v12,v12,v11,s13\n"
AW "a154,v12\n"
AR "v12,a155\n"
"v_subrev_f32 v12,s13,v12\n"
"v_fma_f32 v12,v12,v11,s13\n"
AW "a155,v12\n"
AR "v12,a172\n"
"v_subrev_f32 v12,s13,v12\n"
"v_fma_f32 v12,v12,v11,s13\n"
AW "a172,v12\n"
AR "v12,a173\n"
"v_subrev_f32 v12,s13,v12\n"
"v_fma_f32 v12,v12,v11,s13\n"
AW "a173,v12\n"
AR "v12,a174\n"
"v_subrev_f32 v12,s13,v12\n"
"v_fma_f32 v12,v12,v11,s13\n"
AW "a174,v12\n"
AR "v12,a175\n"
"v_subrev_f32 v12,s13,v12\n"
"v_fma_f32 v12,v12,v11,s13\n"
AW "a175,v12\n"
AR "v12,a192\n"
"v_subrev_f32 v12,s13,v12\n"
"v_fma_f32 v12,v12,v11,s13\n"
AW "a192,v12\n"
AR "v12,a193\n"
"v_subrev_f32 v12,s13,v12\n"
"v_fma_f32 v12,v12,v11,s13\n"
AW "a193,v12\n"
AR "v12,a194\n"
"v_subrev_f32 v12,s13,v12\n"
"v_fma_f32 v12,v12,v11,s13\n"
AW "a194,v12\n"
AR "v12,a195\n"
"v_subrev_f32 v12,s13,v12\n"
"v_fma_f32 v12,v12,v11,s13\n"
AW "a195,v12\n"
AR "v12,a212\n"
"v_subrev_f32 v12,s13,v12\n"
"v_fma_f32 v12,v12,v11,s13\n"
AW "a212,v12\n"
AR "v12,a213\n"
"v_subrev_f32 v12,s13,v12\n"
"v_fma_f32 v12,v12,v11,s13\n"
AW "a213,v12\n"
AR "v12,a214\n"
"v_subrev_f32 v12,s13,v12\n"
"v_fma_f32 v12,v12,v11,s13\n"
AW "a214,v12\n"
AR "v12,a215\n"
"v_subrev_f32 v12,s13,v12\n"
"v_fma_f32 v12,v12,v11,s13\n"
AW "a215,v12\n"
AR "v12,a232\n"
"v_subrev_f32 v12,s13,v12\n"
"v_fma_f32 v12,v12,v11,s13\n"
AW "a232,v12\n"
AR "v12,a233\n"
"v_subrev_f32 v12,s13,v12\n"
"v_fma_f32 v12,v12,v11,s13\n"
AW "a233,v12\n"
AR "v12,a234\n"
"v_subrev_f32 v12,s13,v12\n"
"v_fma_f32 v12,v12,v11,s13\n"
AW "a234,v12\n"
AR "v12,a235\n"
"v_subrev_f32 v12,s13,v12\n"
"v_fma_f32 v12,v12,v11,s13\n"
AW "a235,v12\n"
"v_sub_f32 v44,v44,v10\n"
"v_sub_f32 v45,v45,v10\n"
"v_sub_f32 v46,v46,v10\n"
"v_sub_f32 v47,v47,v10\n"
"v_sub_f32 v84,v84,v10\n"
"v_sub_f32 v85,v85,v10\n"
"v_sub_f32 v86,v86,v10\n"
"v_sub_f32 v87,v87,v10\n"
"v_sub_f32 v104,v104,v10\n"
"v_sub_f32 v105,v105,v10\n"
"v_sub_f32 v106,v106,v10\n"
"v_sub_f32 v107,v107,v10\n"
"v_sub_f32 v124,v124,v10\n"
"v_sub_f32 v125,v125,v10\n"
"v_sub_f32 v126,v126,v10\n"
"v_sub_f32 v127,v127,v10\n"
"v_exp_f32 v104,v104\n"
"v_exp_f32 v105,v105\n"
"v_exp_f32 v106,v106\n"
"v_exp_f32 v107,v107\n"
"v_exp_f32 v124,v124\n"
"v_exp_f32 v125,v125\n"
"v_exp_f32 v126,v126\n"
"v_exp_f32 v127,v127\n"
"v_add_f32 v60,v60,v104\n"
"v_add_f32 v60,v60,v105\n"
"v_add_f32 v60,v60,v106\n"
"v_add_f32 v60,v60,v107\n"
"v_add_f32 v60,v60,v124\n"
"v_add_f32 v60,v60,v125\n"
"v_add_f32 v60,v60,v126\n"
"v_add_f32 v60,v60,v127\n"
CV "v104,v104,v105\n"
CV "v105,v106,v107\n"
CV "v106,v124,v125\n"
CV "v107,v126,v127\n"
"v_max3_f32 v10,v108,v109,v110\n"
"v_max3_f32 v11,v111,v128,v129\n"
"v_max3_f32 v10,v10,v130,v131\n"
"v_max_f32 v10,v10,v11\n"
"v_mov_b32 v11,v10\n"
"s_nop 1\n"
"v_permlane16_swap_b32 v10,v11\n"
"v_max_f32 v10,v10,v11\n"
"v_mov_b32 v11,v10\n"
"s_nop 1\n"
"v_permlane32_swap_b32 v10,v11\n"
"v_max_f32_e64 v10,v10,v11\n"
"v_add_f32 v10,0x41300000,v10\n"
"v_max_f32 v10,0,v10\n"
"v_exp_f32 v11,-v10\n"
"s_nop 0\n"
"v_mul_f32_e64 v61,v56,v11\n"
AR "v12,a16\n"
"v_subrev_f32 v12,s13,v12\n"
"v_fma_f32 v12,v12,v11,s13\n"
AW "a16,v12\n"
AR "v12,a17\n"
"v_subrev_f32 v12,s13,v12\n"
"v_fma_f32 v12,v12,v11,s13\n"
AW "a17,v12\n"
AR "v12,a18\n"
"v_subrev_f32 v12,s13,v12\n"
"v_fma_f32 v12,v12,v11,s13\n"
AW "a18,v12\n"
AR "v12,a19\n"
"v_subrev_f32 v12,s13,v12\n"
"v_fma_f32 v12,v12,v11,s13\n"
AW "a19,v12\n"
AR "v12,a36\n"
"v_subrev_f32 v12,s13,v12\n"
"v_fma_f32 v12,v12,v11,s13\n"
AW "a36,v12\n"
AR "v12,a37\n"
"v_subrev_f32 v12,s13,v12\n"
"v_fma_f32 v12,v12,v11,s13\n"
AW "a37,v12\n"
AR "v12,a38\n"
"v_subrev_f32 v12,s13,v12\n"
"v_fma_f32 v12,v12,v11,s13\n"
AW "a38,v12\n"
AR "v12,a39\n"
"v_subrev_f32 v12,s13,v12\n"
"v_fma_f32 v12,v12,v11,s13\n"
AW "a39,v12\n"
AR "v12,a56\n"
"v_subrev_f32 v12,s13,v12\n"
"v_fma_f32 v12,v12,v11,s13\n"
AW "a56,v12\n"
AR "v12,a57\n"
"v_subrev_f32 v12,s13,v12\n"
"v_fma_f32 v12,v12,v11,s13\n"
AW "a57,v12\n"
AR "v12,a58\n"
"v_subrev_f32 v12,s13,v12\n"
"v_fma_f32 v12,v12,v11,s13\n"
AW "a58,v12\n"
AR "v12,a59\n"
"v_subrev_f32 v12,s13,v12\n"
"v_fma_f32 v12,v12,v11,s13\n"
AW "a59,v12\n"
AR "v12,a76\n"
"v_subrev_f32 v12,s13,v12\n"
"v_fma_f32 v12,v12,v11,s13\n"
AW "a76,v12\n"
AR "v12,a77\n"
"v_subrev_f32 v12,s13,v12\n"
"v_fma_f32 v12,v12,v11,s13\n"
AW "a77,v12\n"
AR "v12,a78\n"
"v_subrev_f32 v12,s13,v12\n"
"v_fma_f32 v12,v12,v11,s13\n"
AW "a78,v12\n"
AR "v12,a79\n"
"v_subrev_f32 v12,s13,v12\n"
"v_fma_f32 v12,v12,v11,s13\n"
AW "a79,v12\n"
AR "v12,a96\n"
"v_subrev_f32 v12,s13,v12\n"
"v_fma_f32 v12,v12,v11,s13\n"
AW "a96,v12\n"
AR "v12,a97\n"
"v_subrev_f32 v12,s13,v12\n"
"v_fma_f32 v12,v12,v11,s13\n"
AW "a97,v12\n"
AR "v12,a98\n"
"v_subrev_f32 v12,s13,v12\n"
"v_fma_f32 v12,v12,v11,s13\n"
AW "a98,v12\n"
AR "v12,a99\n"
"v_subrev_f32 v12,s13,v12\n"
"v_fma_f32 v12,v12,v11,s13\n"
AW "a99,v12\n"
AR "v12,a116\n"
"v_subrev_f32 v12,s13,v12\n"
"v_fma_f32 v12,v12,v11,s13\n"
AW "a116,v12\n"
AR "v12,a117\n"
"v_subrev_f32 v12,s13,v12\n"
"v_fma_f32 v12,v12,v11,s13\n"
AW "a117,v12\n"
AR "v12,a118\n"
"v_subrev_f32 v12,s13,v12\n"
"v_fma_f32 v12,v12,v11,s13\n"
AW "a118,v12\n"
AR "v12,a119\n"
"v_subrev_f32 v12,s13,v12\n"
"v_fma_f32 v12,v12,v11,s13\n"
AW "a119,v12\n"
AR "v12,a136\n"
"v_subrev_f32 v12,s13,v12\n"
"v_fma_f32 v12,v12,v11,s13\n"
AW "a136,v12\n"
AR "v12,a137\n"
"v_subrev_f32 v12,s13,v12\n"
"v_fma_f32 v12,v12,v11,s13\n"
AW "a137,v12\n"
AR "v12,a138\n"
"v_subrev_f32 v12,s13,v12\n"
"v_fma_f32 v12,v12,v11,s13\n"
AW "a138,v12\n"
AR "v12,a139\n"
"v_subrev_f32 v12,s13,v12\n"
"v_fma_f32 v12,v12,v11,s13\n"
AW "a139,v12\n"
AR "v12,a156\n"
"v_subrev_f32 v12,s13,v12\n"
"v_fma_f32 v12,v12,v11,s13\n"
AW "a156,v12\n"
AR "v12,a157\n"
"v_subrev_f32 v12,s13,v12\n"
"v_fma_f32 v12,v12,v11,s13\n"
AW "a157,v12\n"
AR "v12,a158\n"
"v_subrev_f32 v12,s13,v12\n"
"v_fma_f32 v12,v12,v11,s13\n"
AW "a158,v12\n"
AR "v12,a159\n"
"v_subrev_f32 v12,s13,v12\n"
"v_fma_f32 v12,v12,v11,s13\n"
AW "a159,v12\n"
AR "v12,a176\n"
"v_subrev_f32 v12,s13,v12\n"
"v_fma_f32 v12,v12,v11,s13\n"
AW "a176,v12\n"
AR "v12,a177\n"
"v_subrev_f32 v12,s13,v12\n"
"v_fma_f32 v12,v12,v11,s13\n"
AW "a177,v12\n"
AR "v12,a178\n"
"v_subrev_f32 v12,s13,v12\n"
"v_fma_f32 v12,v12,v11,s13\n"
AW "a178,v12\n"
AR "v12,a179\n"
"v_subrev_f32 v12,s13,v12\n"
"v_fma_f32 v12,v12,v11,s13\n"
AW "a179,v12\n"
AR "v12,a196\n"
"v_subrev_f32 v12,s13,v12\n"
"v_fma_f32 v12,v12,v11,s13\n"
AW "a196,v12\n"
AR "v12,a197\n"
"v_subrev_f32 v12,s13,v12\n"
"v_fma_f32 v12,v12,v11,s13\n"
AW "a197,v12\n"
AR "v12,a198\n"
"v_subrev_f32 v12,s13,v12\n"
"v_fma_f32 v12,v12,v11,s13\n"
AW "a198,v12\n"
AR "v12,a199\n"
"v_subrev_f32 v12,s13,v12\n"
"v_fma_f32 v12,v12,v11,s13\n"
AW "a199,v12\n"
AR "v12,a216\n"
"v_subrev_f32 v12,s13,v12\n"
"v_fma_f32 v12,v12,v11,s13\n"
AW "a216,v12\n"
AR "v12,a217\n"
"v_subrev_f32 v12,s13,v12\n"
"v_fma_f32 v12,v12,v11,s13\n"
AW "a217,v12\n"
AR "v12,a218\n"
"v_subrev_f32 v12,s13,v12\n"
"v_fma_f32 v12,v12,v11,s13\n"
AW "a218,v12\n"
AR "v12,a219\n"
"v_subrev_f32 v12,s13,v12\n"
"v_fma_f32 v12,v12,v11,s13\n"
AW "a219,v12\n"
AR "v12,a236\n"
"v_subrev_f32 v12,s13,v12\n"
"v_fma_f32 v12,v12,v11,s13\n"
AW "a236,v12\n"
AR "v12,a237\n"
"v_subrev_f32 v12,s13,v12\n"
"v_fma_f32 v12,v12,v11,s13\n"
AW "a237,v12\n"
AR "v12,a238\n"
"v_subrev_f32 v12,s13,v12\n"
"v_fma_f32 v12,v12,v11,s13\n"
AW "a238,v12\n"
AR "v12,a239\n"
"v_subrev_f32 v12,s13,v12\n"
"v_fma_f32 v12,v12,v11,s13\n"
AW "a239,v12\n"
"v_sub_f32 v48,v48,v10\n"
"v_sub_f32 v49,v49,v10\n"
"v_sub_f32 v50,v50,v10\n"
"v_sub_f32 v51,v51,v10\n"
"v_sub_f32 v88,v88,v10\n"
"v_sub_f32 v89,v89,v10\n"
"v_sub_f32 v90,v90,v10\n"
"v_sub_f32 v91,v91,v10\n"
"v_sub_f32 v108,v108,v10\n"
"v_sub_f32 v109,v109,v10\n"
"v_sub_f32 v110,v110,v10\n"
"v_sub_f32 v111,v111,v10\n"
"v_sub_f32 v128,v128,v10\n"
"v_sub_f32 v129,v129,v10\n"
"v_sub_f32 v130,v130,v10\n"
"v_sub_f32 v131,v131,v10\n"
"v_exp_f32 v108,v108\n"
"v_exp_f32 v109,v109\n"
"v_exp_f32 v110,v110\n"
"v_exp_f32 v111,v111\n"
"v_exp_f32 v128,v128\n"
"v_exp_f32 v129,v129\n"
"v_exp_f32 v130,v130\n"
"v_exp_f32 v131,v131\n"
"v_add_f32 v61,v61,v108\n"
"v_add_f32 v61,v61,v109\n"
"v_add_f32 v61,v61,v110\n"
"v_add_f32 v61,v61,v111\n"
"v_add_f32 v61,v61,v128\n"
"v_add_f32 v61,v61,v129\n"
"v_add_f32 v61,v61,v130\n"
"v_add_f32 v61,v61,v131\n"
CV "v108,v108,v109\n"
CV "v109,v110,v111\n"
CV "v110,v128,v129\n"
CV "v111,v130,v131\n"
"s_nop 7\n"
"s_setpc_b64 s[52:53]\n"
".Lackspin_q:\n"
"s_cmp_eq_u32 s98,0\n"
"s_cbranch_scc1 .Lackok_q\n"
"s_sub_u32 s98,s98,1\n"
"s_sleep 8\n"
"v_bfe_u32 v14,v9,4,5\n"
"v_lshlrev_b32 v14,6,v14\n"
"s_lshl_b32 s58,s78,2\n"
"s_add_u32 s58,s58,s86\n"
"buffer_load_dword v10,v14,s[40:43],s58 offen offset:128 sc1\n"
"s_waitcnt vmcnt(0)\n"
"s_branch .Lack_q\n"
".Lcode1:\n"
".Ldone:\n"
    : "+{v0}"(tid), "+{s23}"(bid), "+{s[20:21]}"(ka)
    :
    : "v1", "v2", "v3", "v4", "v5", "v6", "v7", "v8", "v9", "v10", "v11", "v12", "v13", "v14", "v15", "v16", "v17", "v18", "v19", "v20", "v21", "v22", "v23", "v24", "v25", "v26", "v27", "v28", "v29", "v30", "v31", "v32", "v33", "v34", "v35", "v36", "v37", "v38", "v39", "v40", "v41", "v42", "v43", "v44", "v45", "v46", "v47", "v48", "v49", "v50", "v51", "v52", "v53", "v54", "v55", "v56", "v57", "v58", "v59", "v60", "v61", "v62", "v63", "v64", "v65", "v66", "v67", "v68", "v69", "v70", "v71", "v72", "v73", "v74", "v75", "v76", "v77", "v78", "v79", "v80", "v81", "v82", "v83", "v84", "v85", "v86", "v87", "v88", "v89", "v90", "v91", "v92", "v93", "v94", "v95", "v96", "v97", "v98", "v99", "v100", "v101", "v102", "v103", "v104", "v105", "v106", "v107", "v108", "v109", "v110", "v111", "v112", "v113", "v114", "v115", "v116", "v117", "v118", "v119", "v120", "v121", "v122", "v123", "v124", "v125", "v126", "v127", "v128", "v129", "v130", "v131", "v132", "v133", "v134", "v135", "v136", "v137", "v138", "v139", "v140", "v141", "v142", "v143", "v144", "v145", "v146", "v147", "v148", "v149", "v150", "v151", "v152", "v153", "v154", "v155", "v156", "v157", "v158", "v159", "v160", "v161", "v162", "v163", "v164", "v165", "v166", "v167", "v168", "v169", "v170", "v171", "v172", "v173", "v174", "v175", "v176", "v177", "v178", "v179", "v180", "v181", "v182", "v183", "v184", "v185", "v186", "v187", "v188", "v189", "v190", "v191", "v192", "v193", "v194", "v195", "v196", "v197", "v198", "v199", "v200", "v201", "v202", "v203", "v204", "v205", "v206", "v207", "v208", "v209", "v210", "v211", "v212", "v213", "v214", "v215", "v216", "v217", "v218", "v219", "v220", "v221", "v222", "v223", "v224", "v225", "v226", "v227", "v228", "v229", "v230", "v231", "v232", "v233", "v234", "v235", "v236", "v237", "v238", "v239", "v240", "v241", "v242", "v243", "v244", "v245", "v246", "v247", "v248", "v249", "v250", "v251", "v252", "v253", "v254", "v255", "a0", "a1", "a2", "a3", "a4", "a5", "a6", "a7", "a8", "a9", "a10", "a11", "a12", "a13", "a14", "a15", "a16", "a17", "a18", "a19", "a20", "a21", "a22", "a23", "a24", "a25", "a26", "a27", "a28", "a29", "a30", "a31", "a32", "a33", "a34", "a35", "a36", "a37", "a38", "a39", "a40", "a41", "a42", "a43", "a44", "a45", "a46", "a47", "a48", "a49", "a50", "a51", "a52", "a53", "a54", "a55", "a56", "a57", "a58", "a59", "a60", "a61", "a62", "a63", "a64", "a65", "a66", "a67", "a68", "a69", "a70", "a71", "a72", "a73", "a74", "a75", "a76", "a77", "a78", "a79", "a80", "a81", "a82", "a83", "a84", "a85", "a86", "a87", "a88", "a89", "a90", "a91", "a92", "a93", "a94", "a95", "a96", "a97", "a98", "a99", "a100", "a101", "a102", "a103", "a104", "a105", "a106", "a107", "a108", "a109", "a110", "a111", "a112", "a113", "a114", "a115", "a116", "a117", "a118", "a119", "a120", "a121", "a122", "a123", "a124", "a125", "a126", "a127", "a128", "a129", "a130", "a131", "a132", "a133", "a134", "a135", "a136", "a137", "a138", "a139", "a140", "a141", "a142", "a143", "a144", "a145", "a146", "a147", "a148", "a149", "a150", "a151", "a152", "a153", "a154", "a155", "a156", "a157", "a158", "a159", "a160", "a161", "a162", "a163", "a164", "a165", "a166", "a167", "a168", "a169", "a170", "a171", "a172", "a173", "a174", "a175", "a176", "a177", "a178", "a179", "a180", "a181", "a182", "a183", "a184", "a185", "a186", "a187", "a188", "a189", "a190", "a191", "a192", "a193", "a194", "a195", "a196", "a197", "a198", "a199", "a200", "a201", "a202", "a203", "a204", "a205", "a206", "a207", "a208", "a209", "a210", "a211", "a212", "a213", "a214", "a215", "a216", "a217", "a218", "a219", "a220", "a221", "a222", "a223", "a224", "a225", "a226", "a227", "a228", "a229", "a230", "a231", "a232", "a233", "a234", "a235", "a236", "a237", "a238", "a239", "a240", "a241", "a242", "a243", "a244", "a245", "a246", "a247", "a248", "a249", "a250", "a251", "a252", "a253", "a254", "a255", "s0", "s1", "s2", "s3", "s4", "s5", "s6", "s7", "s8", "s9", "s10", "s11", "s12", "s13", "s14", "s15", "s16", "s17", "s18", "s19", "s22", "s24", "s25", "s26", "s27", "s28", "s29", "s30", "s31", "s32", "s33", "s34", "s35", "s36", "s37", "s38", "s39", "s40", "s41", "s42", "s43", "s44", "s45", "s46", "s47", "s48", "s49", "s50", "s51", "s52", "s53", "s54", "s55", "s56", "s57", "s58", "s59", "s60", "s61", "s62", "s63", "s64", "s65", "s66", "s67", "s68", "s69", "s70", "s71", "s72", "s73", "s74", "s75", "s76", "s77", "s78", "s79", "s80", "s81", "s82", "s83", "s84", "s85", "s86", "s87", "s88", "s89", "s90", "s91", "s92", "s93", "s94", "s95", "s96", "s97", "s98", "s99", "s100", "s101", "vcc", "scc", "memory");
}

extern "C" void kernel_launch(void* const* d_in, const int* in_sizes, int n_in, void* d_out, int out_size, void* d_ws, size_t ws_size, hipStream_t stream) {
  Ptrs P;
  for (int br = 0; br < 2; ++br) { P.x[br] = d_in[5 * br]; P.k[br] = d_in[5 * br + 1]; P.v[br] = d_in[5 * br + 2]; P.wq[br] = d_in[5 * br + 3]; P.wo[br] = d_in[5 * br + 4]; }
  (void)hipFuncSetAttribute((const void*)fused_kernel, hipFuncAttributeMaxDynamicSharedMemorySize, LDS1);
  fused_kernel<<<256, 256, LDS1, stream>>>(P, (char*)d_ws, (char*)d_out);
}
```

```cpp
#include <hip/hip_runtime.h>
#include <hip/hip_bf16.h>

#define MF "v_mfma_f32_16x16x32_bf16 "
#define TR "ds_read_b64_tr_b16 "
#define AR "v_accvgpr_read_b32 "
#define AW "v_accvgpr_write_b32 "
#define CV "v_cvt_pk_bf16_f32 "
#define LD "buffer_load_dwordx4 "
constexpr unsigned LDS1 = 160 * 1024;

struct Ptrs { const void* x[2]; const void* k[2]; const void* v[2]; const void* wq[2]; const void* wo[2]; };

__global__ __launch_bounds__(256, 1) void fused_kernel(Ptrs P, char* __restrict__ ws, char* __restrict__ out) {
  unsigned tid = threadIdx.x, bid = blockIdx.x;
  unsigned long long ka = (unsigned long long)__builtin_amdgcn_kernarg_segment_ptr();
  asm volatile(
".p2align 5\n"
".Lcode0:\n"
"s_and_b32 s58,s23,4\n"
"s_lshl_b32 s58,s58,1\n"
"s_load_dwordx2 s[28:29],s[20:21],s58 offset:48\n"
"s_load_dwordx2 s[24:25],s[20:21],s58\n"
"s_load_dwordx2 s[32:33],s[20:21],s58 offset:16\n"
"s_load_dwordx2 s[36:37],s[20:21],s58 offset:32\n"
"s_load_dwordx2 s[48:49],s[20:21],s58 offset:64\n"
"s_memtime s[100:101]\n"
"s_load_dwordx2 s[40:41],s[20:21],0x50\n"
"s_load_dwordx2 s[44:45],s[20:21],0x58\n"
"v_readfirstlane_b32 s22,v0\n"
"v_and_b32 v9,63,v0\n"
"v_bfe_u32 v10,v0,3,3\n"
"v_and_b32 v11,7,v0\n"
"s_lshr_b32 s22,s22,6\n"
"s_or_b32 s22,s22,s58\n"
"s_and_b32 s58,s23,1\n"
"s_lshl_b32 s58,s58,3\n"
"s_lshr_b32 s59,s23,5\n"
"s_add_u32 s59,s59,s58\n"
"s_mul_i32 s59,s59,4112\n"
"s_or_b32 s22,s22,s59\n"
"s_and_b32 s59,s23,2\n"
"s_lshl_b32 s59,s59,1\n"
"s_bfe_u32 s60,s23,0x20003\n"
"s_or_b32 s59,s59,s60\n"
"s_lshl_b32 s60,s59,20\n"
"s_or_b32 s22,s22,s60\n"
"s_or_b32 s59,s59,s58\n"
"s_mul_i32 s59,s59,65792\n"
"s_xor_b32 s59,s59,524288\n"
"s_or_b32 s22,s22,s59\n"
"v_lshrrev_b32 v6,2,v0\n"
"v_and_b32 v6,4,v6\n"
"v_xor_b32 v6,v6,v11\n"
"v_lshlrev_b32 v6,4,v6\n"
"s_mov_b32 s58,12288\n"
"v_mad_u32_u24 v6,v10,s58,v6\n"
"v_bfe_u32 v4,v0,4,3\n"
"v_xor_b32 v4,v4,v11\n"
"v_lshlrev_b32 v4,4,v4\n"
"s_mov_b32 s58,1280\n"
"v_mad_u32_u24 v4,v10,s58,v4\n"
"v_lshlrev_b32 v9,4,v9\n"
"s_mov_b32 s26,3276800\n"
"s_mov_b32 s27,0x00020000\n"
"s_mov_b32 s30,7864320\n"
"s_mov_b32 s31,0x00020000\n"
"s_mov_b32 s34,0\n"
"s_mov_b32 s35,0x00020000\n"
"s_mov_b32 s38,0\n"
"s_mov_b32 s39,0x00020000\n"
"s_mov_b32 s42,268435456\n"
"s_mov_b32 s43,0x00020000\n"
"s_mov_b32 s46,6553600\n"
"s_mov_b32 s47,0x00020000\n"
"s_mov_b32 s50,7864320\n"
"s_mov_b32 s51,0x00020000\n"
"s_mov_b32 s98,20000\n"
"s_bfe_u32 s56,s22,0x20000\n"
"s_bfe_u32 s72,s22,0x10003\n"
"s_lshr_b32 s73,s56,1\n"
"s_and_b32 s74,s56,1\n"
"s_mul_i32 s62,s56,4096\n"
"s_mul_i32 s63,s56,5120\n"
"s_add_u32 s63,s63,98304\n"
"s_bfe_u32 s58,s22,0x40004\n"
"s_bfe_u32 s59,s22,0x40008\n"
"s_mul_i32 s68,s58,204800\n"
"s_mul_i32 s60,s56,51200\n"
"s_add_u32 s68,s68,s60\n"
"s_lshl_b32 s59,s59,1\n"
"s_add_u32 s59,s59,s73\n"
"s_mul_i32 s66,s59,384\n"
"s_mul_i32 s60,s74,393216\n"
"s_add_u32 s66,s66,s60\n"
"v_xor_b32_e64 v19,64,v4\n"
"v_add_u32 v16,97280,v6\n"
"v_add_u32 v17,194560,v6\n"
"v_add_u32 v18,291840,v6\n"
"s_waitcnt lgkmcnt(0)\n"
"s_mov_b32 s80,s100\n"
"s_mov_b32 s87,2\n"
"s_add_u32 m0,s62,0\n"
"s_add_u32 s82,s66,0\n"
LD "v6,s[28:31],s82 offen lds\n"
LD "v16,s[28:31],s82 offen offset:1024 lds\n"
LD "v17,s[28:31],s82 offen offset:2048 lds\n"
LD "v18,s[28:31],s82 offen offset:3072 lds\n"
"s_add_u32 s84,s68,0\n"
"s_add_u32 m0,s63,0\n"
"s_nop 0\n"
LD "v4,s[24:27],s84 offen lds\n"
"s_add_u32 s84,s68,9216\n"
LD "v19,s[24:27],s84 offen offset:1024 lds\n"
"s_add_u32 s84,s68,18432\n"
LD "v4,s[24:27],s84 offen offset:2048 lds\n"
"s_add_u32 s84,s68,27648\n"
LD "v19,s[24:27],s84 offen offset:3072 lds\n"
"s_add_u32 s84,s68,40960\n"
"s_add_u32 m0,s63,4096\n"
"s_nop 0\n"
LD "v4,s[24:27],s84 offen lds\n"
"s_lshr_b32 s58,s23,3\n"
"s_mul_i32 s59,s58,7\n"
"s_and_b32 s60,s23,7\n"
"s_mul_i32 s60,s60,3\n"
"s_add_u32 s59,s59,s60\n"
"s_and_b32 s59,s59,15\n"
"s_lshl_b32 s59,s59,8\n"
"s_or_b32 s58,s58,s59\n"
"s_and_b32 s59,s23,3\n"
"s_lshl_b32 s59,s59,5\n"
"s_or_b32 s58,s58,s59\n"
"s_bfe_u32 s59,s23,0x10001\n"
"s_lshl_b32 s59,s59,12\n"
"s_or_b32 s58,s58,s59\n"
"s_lshl_b32 s23,s23,16\n"
"s_or_b32 s23,s23,s58\n"
"v_bfe_u32 v20,v0,4,2\n"
"v_mul_u32_u24 v21,384,v10\n"
"v_xor_b32_e64 v22,v11,v20\n"
"s_add_u32 m0,s62,16384\n"
"s_add_u32 s82,s66,128\n"
LD "v6,s[28:31],s82 offen lds\n"
"v_lshl_add_u32 v7,v22,4,v21\n"
"s_lshl_b32 s58,s74,6\n"
"v_xor_b32 v7,s58,v7\n"
"v_lshlrev_b32 v22,1,v20\n"
"v_xor_b32 v22,v11,v22\n"
"v_lshl_add_u32 v8,v22,4,v21\n"
"v_bfe_u32 v21,v0,1,3\n"
"v_xor_b32 v21,v21,v20\n"
"v_lshlrev_b32 v21,4,v21\n"
"v_lshl_add_u32 v21,v11,7,v21\n"
"v_bfe_u32 v22,v0,3,1\n"
"s_mul_i32 s58,s73,12288\n"
"v_mul_u32_u24 v23,3072,v22\n"
"v_add3_u32 v1,v23,v21,s58\n"
"s_mul_i32 s59,s74,10240\n"
"s_add_u32 s59,s59,98304\n"
"v_lshl_add_u32 v2,v22,10,v21\n"
"v_add_u32_e64 v2,s59,v2\n"
"v_bfe_u32 v21,v0,2,3\n"
LD "v16,s[28:31],s82 offen offset:1024 lds\n"
"v_lshlrev_b32 v21,7,v21\n"
"v_bfe_u32 v23,v0,3,2\n"
"v_lshl_add_u32 v21,v23,5,v21\n"
"v_and_b32 v23,3,v0\n"
"v_lshl_add_u32 v21,v23,3,v21\n"
"v_bfe_u32 v23,v0,5,1\n"
"v_mul_u32_u24 v3,3072,v23\n"
"v_add3_u32 v3,v3,v21,s58\n"
"s_bfe_u32 s58,s22,0x40004\n"
"s_mul_i32 s58,s58,409600\n"
"s_mul_i32 s59,s72,1280\n"
"s_add_u32 s58,s58,s59\n"
"v_and_b32 v21,15,v0\n"
"v_mul_u32_u24 v21,2560,v21\n"
"v_lshl_add_u32 v21,v23,4,v21\n"
"v_bfe_u32 v23,v0,4,1\n"
"v_lshl_add_u32 v21,v23,5,v21\n"
"v_add_u32 v12,s58,v21\n"
"v_bfe_u32 v21,v0,2,2\n"
LD "v17,s[28:31],s82 offen offset:2048 lds\n"
"v_lshlrev_b32 v21,7,v21\n"
"v_lshl_add_u32 v21,v20,10,v21\n"
"v_lshl_add_u32 v21,v22,6,v21\n"
"v_and_b32 v23,3,v0\n"
"v_lshl_add_u32 v21,v23,4,v21\n"
"s_lshl_b32 s58,s73,13\n"
"v_add_u32 v0,s58,v21\n"
"s_getpc_b64 s[52:53]\n"
".Lcts:\n"
"s_sub_u32 s52,s52,.Lcts-.Lcode0\n"
"s_subb_u32 s53,s53,0\n"
"s_bfe_u32 s58,s23,0x20000\n"
"s_lshl_b32 s58,s58,2\n"
"s_add_u32 s58,s58,s56\n"
"s_lshl_b32 s58,s58,13\n"
"v_lshlrev_b32 v15,3,v9\n"
"v_add_u32 v15,s58,v15\n"
"v_min_u32 v15,.Lcode1-.Lcode0,v15\n"
LD "v18,s[28:31],s82 offen offset:3072 lds\n"
"s_bfe_u32 s58,s22,0x4000c\n"
"s_bfe_u32 s59,s22,0x40010\n"
"s_mul_i32 s69,s58,204800\n"
"s_mul_i32 s60,s56,51200\n"
"s_add_u32 s69,s69,s60\n"
"s_lshl_b32 s59,s59,1\n"
"s_add_u32 s59,s59,s73\n"
"s_mul_i32 s67,s59,384\n"
"s_mul_i32 s60,s74,393216\n"
"s_add_u32 s67,s67,s60\n"
"s_bfe_u32 s58,s22,0x40004\n"
"s_bfe_u32 s59,s22,0x40008\n"
"s_lshl_b32 s59,s59,1\n"
"s_add_u32 s88,s59,s74\n"
"s_mul_i32 s88,s88,245760\n"
"s_add_u32 s59,s59,s73\n"
"s_lshl_b32 s70,s58,5\n"
"s_add_u32 s70,s70,s59\n"
"s_mul_i32 s70,s70,196608\n"
"s_add_u32 m0,s62,32768\n"
"s_add_u32 s82,s66,256\n"
LD "v6,s[28:31],s82 offen lds\n"
"s_bfe_u32 s58,s22,0x4000c\n"
"s_bfe_u32 s59,s22,0x40010\n"
"s_lshl_b32 s59,s59,1\n"
"s_add_u32 s89,s59,s74\n"
"s_mul_i32 s89,s89,245760\n"
"s_add_u32 s59,s59,s73\n"
"s_lshl_b32 s71,s58,5\n"
"s_add_u32 s71,s71,s59\n"
"s_mul_i32 s71,s71,196608\n"
"s_bitcmp1_b32 s23,12\n"
"s_cselect_b32 s58,s71,s70\n"
"s_cselect_b32 s71,s70,s71\n"
"s_mov_b32 s70,s58\n"
"s_cselect_b32 s58,s89,s88\n"
"s_cselect_b32 s89,s88,s89\n"
"s_mov_b32 s88,s58\n"
"s_cselect_b32 s69,98,1\n"
"s_mov_b32 s54,1037384597\n"
"s_mov_b32 s55,1037384597\n"
LD "v16,s[28:31],s82 offen offset:1024 lds\n"
"s_bfe_u32 s58,s22,0x40004\n"
"s_lshl_b32 s59,s72,4\n"
"s_add_u32 s59,s59,s58\n"
"s_lshl_b32 s86,s59,12\n"
"s_add_u32 s86,s86,100663296\n"
"s_mul_i32 s93,s59,1638400\n"
"s_add_u32 s93,s93,33554432\n"
"s_bfe_u32 s58,s22,0x30014\n"
"s_lshl_b32 s78,s58,1\n"
"s_add_u32 s78,s78,s74\n"
"s_lshl_b32 s94,s78,2\n"
"s_add_u32 s94,s94,s86\n"
"s_lshl_b32 s92,s58,2\n"
"s_lshr_b32 s92,0x31752064,s92\n"
"s_and_b32 s92,s92,7\n"
"s_lshl_b32 s92,s92,2\n"
"s_add_u32 s92,s92,s56\n"
"s_mul_i32 s91,s58,204800\n"
"s_add_u32 s91,s91,s93\n"
LD "v17,s[28:31],s82 offen offset:2048 lds\n"
"s_mul_i32 s75,s56,30720\n"
"s_add_u32 s75,s75,s91\n"
"s_add_u32 s75,s75,0\n"
"s_mul_i32 s58,s74,5120\n"
"s_add_u32 s91,s91,s58\n"
"s_mov_b32 s90,0xf0000000\n"
"s_mov_b32 s77,0xf0000000\n"
"s_mov_b32 s85,0xf0000000\n"
AW "a240,v0\n"
AW "a244,v1\n"
AW "a241,v2\n"
AW "a245,v3\n"
AW "a242,v4\n"
AW "a243,v6\n"
AW "a252,v12\n"
"s_mov_b32 s57,0\n"
"s_mov_b32 s83,0\n"
LD "v18,s[28:31],s82 offen offset:3072 lds\n"
"global_load_dword a249,v15,s[52:53]\n"
AR "v0,a240\n"
AR "v2,a241\n"
AR "v4,a242\n"
AR "v6,a243\n"
"v_xor_b32 v1,64,v0\n"
"v_xor_b32 v3,64,v2\n"
"v_xor_b32_e64 v5,64,v4\n"
"v_add_u32 v11,97280,v6\n"
"v_add_u32 v12,194560,v6\n"
"v_add_u32 v13,291840,v6\n"
"s_mov_b64 vcc,0\n"
"s_waitcnt vmcnt(9) lgkmcnt(0)\n"
"s_barrier\n"
"s_nop 0\n"
TR "a[200:201],v0\n"
TR "a[202:203],v0 offset:512\n"
TR "a[204:205],v0 offset:4096\n"
TR "a[206:207],v0 offset:4608\n"
"ds_read_b128 a[60:63],v2\n"
"ds_read_b128 a[100:103],v3\n"
"ds_read_b128 a[64:67],v2 offset:2048\n"
"ds_read_b128 a[104:107],v3 offset:2048\n"
"ds_read_b128 a[68:71],v2 offset:4096\n"
"ds_read_b128 a[108:111],v3 offset:4096\n"
"ds_read_b128 a[72:75],v2 offset:6144\n"
"ds_read_b128 a[112:115],v3 offset:6144\n"
"ds_read_b128 a[76:79],v2 offset:8192\n"
"ds_read_b128 a[116:119],v3 offset:8192\n"
TR "a[208:209],v0 offset:8\n"
TR "a[210:211],v0 offset:520\n"
TR "a[212:213],v0 offset:4104\n"
TR "a[214:215],v0 offset:4616\n"
"s_waitcnt lgkmcnt(0)\n"
MF "v[16:19],a[200:203],a[60:63],0\n"
"s_add_u32 s84,s68,128\n"
TR "a[216:217],v1\n"
MF "v[16:19],a[204:207],a[100:103],v[16:19]\n"
"s_add_u32 m0,s63,20480\n"
TR "a[218:219],v1 offset:512\n"
MF "v[24:27],a[204:207],a[104:107],0\n"
LD "v4,s[24:27],s84 offen lds\n"
MF "v[24:27],a[200:203],a[64:67],v[24:27]\n"
TR "a[220:221],v1 offset:4096\n"
"v_lshrrev_b32 v15,5,v9\n"
MF "v[32:35],a[200:203],a[68:71],0\n"
TR "a[222:223],v1 offset:4608\n"
"v_min_u32 v15,11,v15\n"
MF "v[32:35],a[204:207],a[108:111],v[32:35]\n"
"v_max3_f32 v220,|v16|,|v17|,|v18|\n"
"v_max_f32_e64 v220,v220,|v19|\n"
MF "v[40:43],a[204:207],a[112:115],0\n"
"s_add_u32 s84,s68,9344\n"
"v_readlane_b32 s12,v220,3\n"
"v_readlane_b32 s6,v220,22\n"
MF "v[40:43],a[200:203],a[72:75],v[40:43]\n"
"s_min_u32 s12,s12,s6\n"
"v_readlane_b32 s7,v220,41\n"
"v_readlane_b32 s8,v220,60\n"
MF "v[48:51],a[200:203],a[76:79],0\n"
"s_min_u32 s7,s7,s8\n"
"v_mul_u32_u24 v15,160,v15\n"
"v_and_or_b32 v15,v9,16,v15\n"
MF "v[48:51],a[204:207],a[116:119],v[48:51]\n"
LD "v5,s[24:27],s84 offen offset:1024 lds\n"
MF "v[52:55],a[212:215],a[116:119],0\n"
"s_min_u32 s12,s12,s7\n"
TR "a[200:201],v1 offset:8\n"
"v_lshlrev_b32 v15,3,v15\n"
MF "v[52:55],a[208:211],a[76:79],v[52:55]\n"
"s_and_b32 s12,s12,0x7f800000\n"
TR "a[202:203],v1 offset:520\n"
MF "v[44:47],a[208:211],a[72:75],0\n"
"s_sub_u32 s12,s12,0x14000000\n"
MF "v[44:47],a[212:215],a[112:115],v[44:47]\n"
"s_cmp_lt_u32 s12,0x50000000\n"
"s_cselect_b32 s12,s12,0xe8400000\n"
TR "a[204:205],v1 offset:4104\n"
MF "v[36:39],a[212:215],a[108:111],0\n"
"s_add_u32 s12,s12,0x17c00000\n"
TR "a[206:207],v1 offset:4616\n"
MF "v[36:39],a[208:211],a[68:71],v[36:39]\n"
"s_bfe_u32 s60,s23,0x70000\n"
"v_mov_b32 v220,s12\n"
"v_mov_b32 v221,s12\n"
MF "v[28:31],a[208:211],a[64:67],0\n"
"s_add_u32 s84,s68,18560\n"
"v_mov_b32 v222,s12\n"
"v_mov_b32 v223,s12\n"
MF "v[28:31],a[212:215],a[104:107],v[28:31]\n"
LD "v4,s[24:27],s84 offen offset:2048 lds\n"
MF "v[20:23],a[212:215],a[100:103],0\n"
"s_lshl_b32 s60,s60,2\n"
MF "v[20:23],a[208:211],a[60:63],v[20:23]\n"
"s_waitcnt vmcnt(8) lgkmcnt(0)\n"
"s_barrier\n"
MF "v[56:59],a[216:219],a[60:63],v[220:223]\n"
"s_add_u32 s60,s60,s56\n"
TR "a[208:209],v0 offset:16384\n"
MF "v[56:59],a[220:223],a[100:103],v[56:59]\n"
"s_add_u32 s84,s68,27776\n"
TR "a[210:211],v0 offset:16896\n"
MF "v[64:67],a[220:223],a[104:107],v[220:223]\n"
LD "v5,s[24:27],s84 offen offset:3072 lds\n"
MF "v[64:67],a[216:219],a[64:67],v[64:67]\n"
"s_mul_i32 s60,s60,15360\n"
TR "a[212:213],v0 offset:20480\n"
MF "v[72:75],a[216:219],a[68:71],v[220:223]\n"
TR "a[214:215],v0 offset:20992\n"
MF "v[72:75],a[220:223],a[108:111],v[72:75]\n"
"s_add_u32 s84,s68,41088\n"
MF "v[80:83],a[220:223],a[112:115],v[220:223]\n"
"s_add_u32 m0,s63,24576\n"
MF "v[80:83],a[216:219],a[72:75],v[80:83]\n"
LD "v4,s[24:27],s84 offen lds\n"
MF "v[88:91],a[216:219],a[76:79],v[220:223]\n"
MF "v[88:91],a[220:223],a[116:119],v[88:91]\n"
MF "v[92:95],a[204:207],a[116:119],v[220:223]\n"
"s_add_u32 m0,s62,49152\n"
TR "a[216:217],v0 offset:16392\n"
MF "v[92:95],a[200:203],a[76:79],v[92:95]\n"
"s_add_u32 s82,s66,786432\n"
TR "a[218:219],v0 offset:16904\n"
MF "v[84:87],a[200:203],a[72:75],v[220:223]\n"
LD "v6,s[28:31],s82 offen lds\n"
MF "v[84:87],a[204:207],a[112:115],v[84:87]\n"
TR "a[220:221],v0 offset:20488\n"
MF "v[76:79],a[204:207],a[108:111],v[220:223]\n"
TR "a[222:223],v0 offset:21000\n"
MF "v[76:79],a[200:203],a[68:71],v[76:79]\n"
MF "v[68:71],a[200:203],a[64:67],v[220:223]\n"
MF "v[68:71],a[204:207],a[104:107],v[68:71]\n"
LD "v11,s[28:31],s82 offen offset:1024 lds\n"
MF "v[60:63],a[204:207],a[100:103],v[220:223]\n"
MF "v[60:63],a[200:203],a[60:63],v[60:63]\n"
"s_waitcnt lgkmcnt(4)\n"
MF "v[96:99],a[208:211],a[60:63],v[220:223]\n"
TR "a[200:201],v1 offset:16384\n"
MF "v[96:99],a[212:215],a[100:103],v[96:99]\n"
TR "a[202:203],v1 offset:16896\n"
MF "v[104:107],a[212:215],a[104:107],v[220:223]\n"
LD "v12,s[28:31],s82 offen offset:2048 lds\n"
MF "v[104:107],a[208:211],a[64:67],v[104:107]\n"
TR "a[204:205],v1 offset:20480\n"
MF "v[112:115],a[208:211],a[68:71],v[220:223]\n"
TR "a[206:207],v1 offset:20992\n"
MF "v[112:115],a[212:215],a[108:111],v[112:115]\n"
MF "v[120:123],a[212:215],a[112:115],v[220:223]\n"
MF "v[120:123],a[208:211],a[72:75],v[120:123]\n"
LD "v13,s[28:31],s82 offen offset:3072 lds\n"
MF "v[128:131],a[208:211],a[76:79],v[220:223]\n"
MF "v[128:131],a[212:215],a[116:119],v[128:131]\n"
"s_waitcnt lgkmcnt(4)\n"
MF "v[132:135],a[220:223],a[116:119],v[220:223]\n"
"s_add_u32 m0,s62,65536\n"
TR "a[208:209],v1 offset:16392\n"
MF "v[132:135],a[216:219],a[76:79],v[132:135]\n"
"s_add_u32 s82,s66,786560\n"
TR "a[210:211],v1 offset:16904\n"
MF "v[124:127],a[216:219],a[72:75],v[220:223]\n"
LD "v6,s[28:31],s82 offen lds\n"
MF "v[124:127],a[220:223],a[112:115],v[124:127]\n"
TR "a[212:213],v1 offset:20488\n"
MF "v[116:119],a[220:223],a[108:111],v[220:223]\n"
TR "a[214:215],v1 offset:21000\n"
MF "v[116:119],a[216:219],a[68:71],v[116:119]\n"
MF "v[108:111],a[216:219],a[64:67],v[220:223]\n"
MF "v[108:111],a[220:223],a[104:107],v[108:111]\n"
LD "v11,s[28:31],s82 offen offset:1024 lds\n"
MF "v[100:103],a[220:223],a[100:103],v[220:223]\n"
MF "v[100:103],a[216:219],a[60:63],v[100:103]\n"
"s_waitcnt vmcnt(6) lgkmcnt(0)\n"
"s_barrier\n"
MF "v[136:139],a[200:203],a[60:63],v[220:223]\n"
TR "a[216:217],v0 offset:32768\n"
MF "v[136:139],a[204:207],a[100:103],v[136:139]\n"
TR "a[218:219],v0 offset:33280\n"
MF "v[144:147],a[204:207],a[104:107],v[220:223]\n"
"ds_read_b128 a[140:143],v2 offset:20480\n"
MF "v[144:147],a[200:203],a[64:67],v[144:147]\n"
TR "a[220:221],v0 offset:36864\n"
MF "v[152:155],a[200:203],a[68:71],v[220:223]\n"
TR "a[222:223],v0 offset:37376\n"
MF "v[152:155],a[204:207],a[108:111],v[152:155]\n"
LD "v12,s[28:31],s82 offen offset:2048 lds\n"
MF "v[160:163],a[204:207],a[112:115],v[220:223]\n"
"ds_read_b128 a[180:183],v3 offset:20480\n"
MF "v[160:163],a[200:203],a[72:75],v[160:163]\n"
MF "v[168:171],a[200:203],a[76:79],v[220:223]\n"
MF "v[168:171],a[204:207],a[116:119],v[168:171]\n"
LD "v13,s[28:31],s82 offen offset:3072 lds\n"
MF "v[172:175],a[212:215],a[116:119],v[220:223]\n"
TR "a[200:201],v0 offset:32776\n"
MF "v[172:175],a[208:211],a[76:79],v[172:175]\n"
TR "a[202:203],v0 offset:33288\n"
MF "v[164:167],a[208:211],a[72:75],v[220:223]\n"
"ds_read_b128 a[144:147],v2 offset:22528\n"
MF "v[164:167],a[212:215],a[112:115],v[164:167]\n"
"s_add_u32 m0,s62,81920\n"
TR "a[204:205],v0 offset:36872\n"
MF "v[156:159],a[212:215],a[108:111],v[220:223]\n"
"s_add_u32 s82,s66,786688\n"
TR "a[206:207],v0 offset:37384\n"
MF "v[156:159],a[208:211],a[68:71],v[156:159]\n"
LD "v6,s[28:31],s82 offen lds\n"
MF "v[148:151],a[208:211],a[64:67],v[220:223]\n"
"ds_read_b128 a[184:187],v3 offset:22528\n"
MF "v[148:151],a[212:215],a[104:107],v[148:151]\n"
MF "v[140:143],a[212:215],a[100:103],v[220:223]\n"
MF "v[140:143],a[208:211],a[60:63],v[140:143]\n"
"s_waitcnt lgkmcnt(7)\n"
LD "v11,s[28:31],s82 offen offset:1024 lds\n"
MF "v[176:179],a[216:219],a[60:63],v[220:223]\n"
TR "a[208:209],v1 offset:32768\n"
MF "v[176:179],a[220:223],a[100:103],v[176:179]\n"
TR "a[210:211],v1 offset:33280\n"
MF "v[184:187],a[220:223],a[104:107],v[220:223]\n"
"ds_read_b128 a[148:151],v2 offset:24576\n"
MF "v[184:187],a[216:219],a[64:67],v[184:187]\n"
TR "a[212:213],v1 offset:36864\n"
MF "v[192:195],a[216:219],a[68:71],v[220:223]\n"
TR "a[214:215],v1 offset:37376\n"
MF "v[192:195],a[220:223],a[108:111],v[192:195]\n"
LD "v12,s[28:31],s82 offen offset:2048 lds\n"
MF "v[200:203],a[220:223],a[112:115],v[220:223]\n"
"ds_read_b128 a[188:191],v3 offset:24576\n"
MF "v[200:203],a[216:219],a[72:75],v[200:203]\n"
MF "v[208:211],a[216:219],a[76:79],v[220:223]\n"
MF "v[208:211],a[220:223],a[116:119],v[208:211]\n"
"s_waitcnt lgkmcnt(7)\n"
LD "v13,s[28:31],s82 offen offset:3072 lds\n"
MF "v[212:215],a[204:207],a[116:119],v[220:223]\n"
TR "a[216:217],v1 offset:32776\n"
MF "v[212:215],a[200:203],a[76:79],v[212:215]\n"
TR "a[218:219],v1 offset:33288\n"
MF "v[204:207],a[200:203],a[72:75],v[220:223]\n"
"ds_read_b128 a[152:155],v2 offset:26624\n"
MF "v[204:207],a[204:207],a[112:115],v[204:207]\n"
"s_add_u32 s84,s68,256\n"
TR "a[220:221],v1 offset:36872\n"
MF "v[196:199],a[204:207],a[108:111],v[220:223]\n"
"s_add_u32 m0,s63,40960\n"
TR "a[222:223],v1 offset:37384\n"
MF "v[196:199],a[200:203],a[68:71],v[196:199]\n"
LD "v4,s[24:27],s84 offen lds\n"
MF "v[188:191],a[200:203],a[64:67],v[220:223]\n"
"v_add_u32 v0,0xc000,v0\n"
MF "v[188:191],a[204:207],a[104:107],v[188:191]\n"
"s_add_u32 s84,s68,9472\n"
"v_add_u32 v1,0xc000,v1\n"
MF "v[180:183],a[204:207],a[100:103],v[220:223]\n"
LD "v5,s[24:27],s84 offen offset:1024 lds\n"
MF "v[180:183],a[200:203],a[60:63],v[180:183]\n"
"s_waitcnt vmcnt(10) lgkmcnt(0)\n"
"s_barrier\n"
MF "v[216:219],a[208:211],a[60:63],v[220:223]\n"
TR "a[200:201],v0\n"
MF "v[216:219],a[212:215],a[100:103],v[216:219]\n"
TR "a[202:203],v0 offset:512\n"
MF "v[224:227],a[212:215],a[104:107],v[220:223]\n"
"ds_read_b128 a[192:195],v3 offset:26624\n"
MF "v[224:227],a[208:211],a[64:67],v[224:227]\n"
TR "a[204:205],v0 offset:4096\n"
MF "v[232:235],a[208:211],a[68:71],v[220:223]\n"
"s_add_u32 s84,s68,18688\n"
TR "a[206:207],v0 offset:4608\n"
MF "v[232:235],a[212:215],a[108:111],v[232:235]\n"
LD "v4,s[24:27],s84 offen offset:2048 lds\n"
MF "v[240:243],a[212:215],a[112:115],v[220:223]\n"
"ds_read_b128 a[156:159],v2 offset:28672\n"
MF "v[240:243],a[208:211],a[72:75],v[240:243]\n"
"ds_read_b128 a[196:199],v3 offset:28672\n"
MF "v[248:251],a[208:211],a[76:79],v[220:223]\n"
"s_add_u32 s84,s68,27904\n"
MF "v[248:251],a[212:215],a[116:119],v[248:251]\n"
LD "v5,s[24:27],s84 offen offset:3072 lds\n"
MF "v[252:255],a[220:223],a[116:119],v[220:223]\n"
TR "a[208:209],v0 offset:8\n"
MF "v[252:255],a[216:219],a[76:79],v[252:255]\n"
TR "a[210:211],v0 offset:520\n"
MF "v[244:247],a[216:219],a[72:75],v[220:223]\n"
MF "v[244:247],a[220:223],a[112:115],v[244:247]\n"
"s_add_u32 s84,s68,41216\n"
TR "a[212:213],v0 offset:4104\n"
MF "v[236:239],a[220:223],a[108:111],v[220:223]\n"
"s_add_u32 m0,s63,45056\n"
TR "a[214:215],v0 offset:4616\n"
MF "v[236:239],a[216:219],a[68:71],v[236:239]\n"
LD "v4,s[24:27],s84 offen lds\n"
MF "v[228:231],a[216:219],a[64:67],v[220:223]\n"
MF "v[228:231],a[220:223],a[104:107],v[228:231]\n"
"s_add_u32 m0,s62,0\n"
MF "v[220:223],a[220:223],a[100:103],v[220:223]\n"
"s_add_u32 s82,s66,1572864\n"
MF "v[220:223],a[216:219],a[60:63],v[220:223]\n"
"s_waitcnt lgkmcnt(4)\n"
LD "v6,s[28:31],s82 offen lds\n"
MF "v[16:19],a[200:203],a[140:143],v[16:19]\n"
TR "a[216:217],v1\n"
MF "v[16:19],a[204:207],a[180:183],v[16:19]\n"
TR "a[218:219],v1 offset:512\n"
MF "v[24:27],a[204:207],a[184:187],v[24:27]\n"
MF "v[24:27],a[200:203],a[144:147],v[24:27]\n"
TR "a[220:221],v1 offset:4096\n"
MF "v[32:35],a[200:203],a[148:151],v[32:35]\n"
TR "a[222:223],v1 offset:4608\n"
MF "v[32:35],a[204:207],a[188:191],v[32:35]\n"
LD "v11,s[28:31],s82 offen offset:1024 lds\n"
MF "v[40:43],a[204:207],a[192:195],v[40:43]\n"
MF "v[40:43],a[200:203],a[152:155],v[40:43]\n"
MF "v[48:51],a[200:203],a[156:159],v[48:51]\n"
MF "v[48:51],a[204:207],a[196:199],v[48:51]\n"
"s_waitcnt lgkmcnt(4)\n"
LD "v12,s[28:31],s82 offen offset:2048 lds\n"
MF "v[52:55],a[212:215],a[196:199],v[52:55]\n"
TR "a[200:201],v1 offset:8\n"
MF "v[52:55],a[208:211],a[156:159],v[52:55]\n"
TR "a[202:203],v1 offset:520\n"
MF "v[44:47],a[208:211],a[152:155],v[44:47]\n"
MF "v[44:47],a[212:215],a[192:195],v[44:47]\n"
TR "a[204:205],v1 offset:4104\n"
MF "v[36:39],a[212:215],a[188:191],v[36:39]\n"
TR "a[206:207],v1 offset:4616\n"
MF "v[36:39],a[208:211],a[148:151],v[36:39]\n"
LD "v13,s[28:31],s82 offen offset:3072 lds\n"
MF "v[28:31],a[208:211],a[144:147],v[28:31]\n"
"s_add_u32 s84,s68,384\n"
MF "v[28:31],a[212:215],a[184:187],v[28:31]\n"
"s_add_u32 m0,s63,0\n"
MF "v[20:23],a[212:215],a[180:183],v[20:23]\n"
LD "v4,s[24:27],s84 offen lds\n"
MF "v[20:23],a[208:211],a[140:143],v[20:23]\n"
"s_waitcnt vmcnt(14) lgkmcnt(0)\n"
"s_barrier\n"
"v_bfe_u32 v14,v9,4,4\n"
"v_lshlrev_b32 v14,2,v14\n"
"buffer_load_dword v10,v14,s[40:43],s86 offen sc1\n"
".Lhs2:\n"
MF "v[56:59],a[216:219],a[140:143],v[56:59]\n"
TR "a[208:209],v0 offset:16384\n"
MF "v[56:59],a[220:223],a[180:183],v[56:59]\n"
"s_add_u32 s84,s68,9600\n"
TR "a[210:211],v0 offset:16896\n"
MF "v[64:67],a[220:223],a[184:187],v[64:67]\n"
LD "v5,s[24:27],s84 offen offset:1024 lds\n"
MF "v[64:67],a[216:219],a[144:147],v[64:67]\n"
TR "a[212:213],v0 offset:20480\n"
MF "v[72:75],a[216:219],a[148:151],v[72:75]\n"
TR "a[214:215],v0 offset:20992\n"
MF "v[72:75],a[220:223],a[188:191],v[72:75]\n"
"s_add_u32 m0,s62,16384\n"
"buffer_load_dword a249,v15,s[48:51],s60 offen offset:1024\n"
MF "v[80:83],a[220:223],a[192:195],v[80:83]\n"
"s_add_u32 s82,s66,1572992\n"
MF "v[80:83],a[216:219],a[152:155],v[80:83]\n"
LD "v6,s[28:31],s82 offen lds\n"
MF "v[88:91],a[216:219],a[156:159],v[88:91]\n"
"s_bfe_u32 s59,s23,0x30003\n"
MF "v[88:91],a[220:223],a[196:199],v[88:91]\n"
"s_add_u32 s59,s59,1\n"
MF "v[92:95],a[204:207],a[196:199],v[92:95]\n"
"s_lshl_b32 s59,s59,7\n"
TR "a[216:217],v0 offset:16392\n"
MF "v[92:95],a[200:203],a[156:159],v[92:95]\n"
"s_and_b32 s59,s59,768\n"
TR "a[218:219],v0 offset:16904\n"
MF "v[84:87],a[200:203],a[152:155],v[84:87]\n"
LD "v11,s[28:31],s82 offen offset:1024 lds\n"
MF "v[84:87],a[204:207],a[192:195],v[84:87]\n"
"s_add_u32 s59,s59,s60\n"
TR "a[220:221],v0 offset:20488\n"
MF "v[76:79],a[204:207],a[188:191],v[76:79]\n"
TR "a[222:223],v0 offset:21000\n"
MF "v[76:79],a[200:203],a[148:151],v[76:79]\n"
MF "v[68:71],a[200:203],a[144:147],v[68:71]\n"
MF "v[68:71],a[204:207],a[184:187],v[68:71]\n"
LD "v12,s[28:31],s82 offen offset:2048 lds\n"
MF "v[60:63],a[204:207],a[180:183],v[60:63]\n"
MF "v[60:63],a[200:203],a[140:143],v[60:63]\n"
"s_waitcnt lgkmcnt(4)\n"
MF "v[96:99],a[208:211],a[140:143],v[96:99]\n"
TR "a[200:201],v1 offset:16384\n"
MF "v[96:99],a[212:215],a[180:183],v[96:99]\n"
TR "a[202:203],v1 offset:16896\n"
MF "v[104:107],a[212:215],a[184:187],v[104:107]\n"
LD "v13,s[28:31],s82 offen offset:3072 lds\n"
MF "v[104:107],a[208:211],a[144:147],v[104:107]\n"
TR "a[204:205],v1 offset:20480\n"
MF "v[112:115],a[208:211],a[148:151],v[112:115]\n"
TR "a[206:207],v1 offset:20992\n"
MF "v[112:115],a[212:215],a[188:191],v[112:115]\n"
"s_add_u32 s84,s68,20864\n"
MF "v[120:123],a[212:215],a[192:195],v[120:123]\n"
"s_add_u32 m0,s63,2048\n"
MF "v[120:123],a[208:211],a[152:155],v[120:123]\n"
LD "v4,s[24:27],s84 offen lds\n"
MF "v[128:131],a[208:211],a[156:159],v[128:131]\n"
MF "v[128:131],a[212:215],a[196:199],v[128:131]\n"
"s_waitcnt lgkmcnt(4)\n"
MF "v[132:135],a[220:223],a[196:199],v[132:135]\n"
TR "a[208:209],v1 offset:16392\n"
MF "v[132:135],a[216:219],a[156:159],v[132:135]\n"
"s_add_u32 s84,s68,30080\n"
TR "a[210:211],v1 offset:16904\n"
MF "v[124:127],a[216:219],a[152:155],v[124:127]\n"
LD "v5,s[24:27],s84 offen offset:1024 lds\n"
MF "v[124:127],a[220:223],a[192:195],v[124:127]\n"
TR "a[212:213],v1 offset:20488\n"
MF "v[116:119],a[220:223],a[188:191],v[116:119]\n"
TR "a[214:215],v1 offset:21000\n"
MF "v[116:119],a[216:219],a[148:151],v[116:119]\n"
"s_add_u32 m0,s62,32768\n"
MF "v[108:111],a[216:219],a[144:147],v[108:111]\n"
"s_add_u32 s82,s66,1573120\n"
MF "v[108:111],a[220:223],a[184:187],v[108:111]\n"
LD "v6,s[28:31],s82 offen lds\n"
MF "v[100:103],a[220:223],a[180:183],v[100:103]\n"
MF "v[100:103],a[216:219],a[140:143],v[100:103]\n"
"s_waitcnt vmcnt(14) lgkmcnt(0)\n"
"s_barrier\n"
MF "v[136:139],a[200:203],a[140:143],v[136:139]\n"
TR "a[216:217],v0 offset:32768\n"
MF "v[136:139],a[204:207],a[180:183],v[136:139]\n"
TR "a[218:219],v0 offset:33280\n"
MF "v[144:147],a[204:207],a[184:187],v[144:147]\n"
"ds_read_b128 a[60:63],v2 offset:40960\n"
MF "v[144:147],a[200:203],a[144:147],v[144:147]\n"
TR "a[220:221],v0 offset:36864\n"
MF "v[152:155],a[200:203],a[148:151],v[152:155]\n"
TR "a[222:223],v0 offset:37376\n"
MF "v[152:155],a[204:207],a[188:191],v[152:155]\n"
LD "v11,s[28:31],s82 offen offset:1024 lds\n"
MF "v[160:163],a[204:207],a[192:195],v[160:163]\n"
"ds_read_b128 a[100:103],v3 offset:40960\n"
MF "v[160:163],a[200:203],a[152:155],v[160:163]\n"
MF "v[168:171],a[200:203],a[156:159],v[168:171]\n"
MF "v[168:171],a[204:207],a[196:199],v[168:171]\n"
LD "v12,s[28:31],s82 offen offset:2048 lds\n"
MF "v[172:175],a[212:215],a[196:199],v[172:175]\n"
TR "a[200:201],v0 offset:32776\n"
MF "v[172:175],a[208:211],a[156:159],v[172:175]\n"
TR "a[202:203],v0 offset:33288\n"
MF "v[164:167],a[208:211],a[152:155],v[164:167]\n"
"ds_read_b128 a[64:67],v2 offset:43008\n"
MF "v[164:167],a[212:215],a[192:195],v[164:167]\n"
TR "a[204:205],v0 offset:36872\n"
MF "v[156:159],a[212:215],a[188:191],v[156:159]\n"
TR "a[206:207],v0 offset:37384\n"
MF "v[156:159],a[208:211],a[148:151],v[156:159]\n"
LD "v13,s[28:31],s82 offen offset:3072 lds\n"
MF "v[148:151],a[208:211],a[144:147],v[148:151]\n"
"ds_read_b128 a[104:107],v3 offset:43008\n"
MF "v[148:151],a[212:215],a[184:187],v[148:151]\n"
"s_add_u32 s84,s68,41344\n"
MF "v[140:143],a[212:215],a[180:183],v[140:143]\n"
"s_add_u32 m0,s63,4096\n"
MF "v[140:143],a[208:211],a[140:143],v[140:143]\n"
"s_waitcnt lgkmcnt(7)\n"
LD "v4,s[24:27],s84 offen lds\n"
MF "v[176:179],a[216:219],a[140:143],v[176:179]\n"
TR "a[208:209],v1 offset:32768\n"
MF "v[176:179],a[220:223],a[180:183],v[176:179]\n"
TR "a[210:211],v1 offset:33280\n"
MF "v[184:187],a[220:223],a[184:187],v[184:187]\n"
"s_sub_u32 s66,s66,s83\n"
"ds_read_b128 a[68:71],v2 offset:45056\n"
MF "v[184:187],a[216:219],a[144:147],v[184:187]\n"
"s_add_u32 m0,s62,49152\n"
TR "a[212:213],v1 offset:36864\n"
MF "v[192:195],a[216:219],a[148:151],v[192:195]\n"
"s_add_u32 s82,s66,2359296\n"
TR "a[214:215],v1 offset:37376\n"
MF "v[192:195],a[220:223],a[188:191],v[192:195]\n"
LD "v6,s[28:31],s82 offen lds\n"
MF "v[200:203],a[220:223],a[192:195],v[200:203]\n"
"ds_read_b128 a[108:111],v3 offset:45056\n"
MF "v[200:203],a[216:219],a[152:155],v[200:203]\n"
MF "v[208:211],a[216:219],a[156:159],v[208:211]\n"
MF "v[208:211],a[220:223],a[196:199],v[208:211]\n"
"s_waitcnt lgkmcnt(7)\n"
LD "v11,s[28:31],s82 offen offset:1024 lds\n"
MF "v[212:215],a[204:207],a[196:199],v[212:215]\n"
TR "a[216:217],v1 offset:32776\n"
MF "v[212:215],a[200:203],a[156:159],v[212:215]\n"
TR "a[218:219],v1 offset:33288\n"
MF "v[204:207],a[200:203],a[152:155],v[204:207]\n"
"ds_read_b128 a[72:75],v2 offset:47104\n"
MF "v[204:207],a[204:207],a[192:195],v[204:207]\n"
TR "a[220:221],v1 offset:36872\n"
MF "v[196:199],a[204:207],a[188:191],v[196:199]\n"
TR "a[222:223],v1 offset:37384\n"
MF "v[196:199],a[200:203],a[148:151],v[196:199]\n"
LD "v12,s[28:31],s82 offen offset:2048 lds\n"
MF "v[188:191],a[200:203],a[144:147],v[188:191]\n"
"v_add_u32 v0,0xffff4000,v0\n"
MF "v[188:191],a[204:207],a[184:187],v[188:191]\n"
"v_add_u32 v1,0xffff4000,v1\n"
MF "v[180:183],a[204:207],a[180:183],v[180:183]\n"
LD "v13,s[28:31],s82 offen offset:3072 lds\n"
MF "v[180:183],a[200:203],a[140:143],v[180:183]\n"
"s_waitcnt vmcnt(18) lgkmcnt(0)\n"
"s_barrier\n"
MF "v[216:219],a[208:211],a[140:143],v[216:219]\n"
TR "a[200:201],v0\n"
MF "v[216:219],a[212:215],a[180:183],v[216:219]\n"
TR "a[202:203],v0 offset:512\n"
MF "v[224:227],a[212:215],a[184:187],v[224:227]\n"
"ds_read_b128 a[112:115],v3 offset:47104\n"
MF "v[224:227],a[208:211],a[144:147],v[224:227]\n"
"s_add_u32 s84,s68,512\n"
TR "a[204:205],v0 offset:4096\n"
MF "v[232:235],a[208:211],a[148:151],v[232:235]\n"
"s_add_u32 m0,s63,20480\n"
TR "a[206:207],v0 offset:4608\n"
MF "v[232:235],a[212:215],a[188:191],v[232:235]\n"
LD "v4,s[24:27],s84 offen lds\n"
MF "v[240:243],a[212:215],a[192:195],v[240:243]\n"
"ds_read_b128 a[76:79],v2 offset:49152\n"
MF "v[240:243],a[208:211],a[152:155],v[240:243]\n"
"ds_read_b128 a[116:119],v3 offset:49152\n"
MF "v[248:251],a[208:211],a[156:159],v[248:251]\n"
"s_add_u32 s84,s68,9728\n"
MF "v[248:251],a[212:215],a[196:199],v[248:251]\n"
LD "v5,s[24:27],s84 offen offset:1024 lds\n"
MF "v[252:255],a[220:223],a[196:199],v[252:255]\n"
TR "a[208:209],v0 offset:8\n"
MF "v[252:255],a[216:219],a[156:159],v[252:255]\n"
TR "a[210:211],v0 offset:520\n"
MF "v[244:247],a[216:219],a[152:155],v[244:247]\n"
MF "v[244:247],a[220:223],a[192:195],v[244:247]\n"
"s_add_u32 m0,s62,65536\n"
TR "a[212:213],v0 offset:4104\n"
MF "v[236:239],a[220:223],a[188:191],v[236:239]\n"
"s_add_u32 s82,s66,2359424\n"
TR "a[214:215],v0 offset:4616\n"
MF "v[236:239],a[216:219],a[148:151],v[236:239]\n"
LD "v6,s[28:31],s82 offen lds\n"
MF "v[228:231],a[216:219],a[144:147],v[228:231]\n"
MF "v[228:231],a[220:223],a[184:187],v[228:231]\n"
MF "v[220:223],a[220:223],a[180:183],v[220:223]\n"
MF "v[220:223],a[216:219],a[140:143],v[220:223]\n"
"s_waitcnt lgkmcnt(4)\n"
LD "v11,s[28:31],s82 offen offset:1024 lds\n"
MF "v[16:19],a[200:203],a[60:63],v[16:19]\n"
TR "a[216:217],v1\n"
MF "v[16:19],a[204:207],a[100:103],v[16:19]\n"
TR "a[218:219],v1 offset:512\n"
MF "v[24:27],a[204:207],a[104:107],v[24:27]\n"
MF "v[24:27],a[200:203],a[64:67],v[24:27]\n"
TR "a[220:221],v1 offset:4096\n"
MF "v[32:35],a[200:203],a[68:71],v[32:35]\n"
TR "a[222:223],v1 offset:4608\n"
MF "v[32:35],a[204:207],a[108:111],v[32:35]\n"
LD "v12,s[28:31],s82 offen offset:2048 lds\n"
MF "v[40:43],a[204:207],a[112:115],v[40:43]\n"
MF "v[40:43],a[200:203],a[72:75],v[40:43]\n"
MF "v[48:51],a[200:203],a[76:79],v[48:51]\n"
MF "v[48:51],a[204:207],a[116:119],v[48:51]\n"
"s_waitcnt lgkmcnt(4)\n"
LD "v13,s[28:31],s82 offen offset:3072 lds\n"
MF "v[52:55],a[212:215],a[116:119],v[52:55]\n"
TR "a[200:201],v1 offset:8\n"
MF "v[52:55],a[208:211],a[76:79],v[52:55]\n"
TR "a[202:203],v1 offset:520\n"
MF "v[44:47],a[208:211],a[72:75],v[44:47]\n"
MF "v[44:47],a[212:215],a[112:115],v[44:47]\n"
"s_add_u32 s84,s68,20992\n"
TR "a[204:205],v1 offset:4104\n"
MF "v[36:39],a[212:215],a[108:111],v[36:39]\n"
"s_add_u32 m0,s63,22528\n"
TR "a[206:207],v1 offset:4616\n"
MF "v[36:39],a[208:211],a[68:71],v[36:39]\n"
LD "v4,s[24:27],s84 offen lds\n"
MF "v[28:31],a[208:211],a[64:67],v[28:31]\n"
MF "v[28:31],a[212:215],a[104:107],v[28:31]\n"
"s_add_u32 s84,s68,30208\n"
MF "v[20:23],a[212:215],a[100:103],v[20:23]\n"
LD "v5,s[24:27],s84 offen offset:1024 lds\n"
MF "v[20:23],a[208:211],a[60:63],v[20:23]\n"
"s_waitcnt vmcnt(19) lgkmcnt(0)\n"
"s_barrier\n"
MF "v[56:59],a[216:219],a[60:63],v[56:59]\n"
"s_add_u32 m0,s62,81920\n"
TR "a[208:209],v0 offset:16384\n"
MF "v[56:59],a[220:223],a[100:103],v[56:59]\n"
"s_add_u32 s82,s66,2359552\n"
TR "a[210:211],v0 offset:16896\n"
MF "v[64:67],a[220:223],a[104:107],v[64:67]\n"
LD "v6,s[28:31],s82 offen lds\n"
MF "v[64:67],a[216:219],a[64:67],v[64:67]\n"
TR "a[212:213],v0 offset:20480\n"
MF "v[72:75],a[216:219],a[68:71],v[72:75]\n"
TR "a[214:215],v0 offset:20992\n"
MF "v[72:75],a[220:223],a[108:111],v[72:75]\n"
"buffer_load_dword a249,v15,s[48:51],s59 offen\n"
MF "v[80:83],a[220:223],a[112:115],v[80:83]\n"
"s_bfe_u32 s59,s23,0x30003\n"
MF "v[80:83],a[216:219],a[72:75],v[80:83]\n"
LD "v11,s[28:31],s82 offen offset:1024 lds\n"
MF "v[88:91],a[216:219],a[76:79],v[88:91]\n"
"s_add_u32 s59,s59,3\n"
MF "v[88:91],a[220:223],a[116:119],v[88:91]\n"
"s_lshl_b32 s59,s59,7\n"
MF "v[92:95],a[204:207],a[116:119],v[92:95]\n"
"s_and_b32 s59,s59,768\n"
TR "a[216:217],v0 offset:16392\n"
MF "v[92:95],a[200:203],a[76:79],v[92:95]\n"
"s_add_u32 s59,s59,s60\n"
TR "a[218:219],v0 offset:16904\n"
MF "v[84:87],a[200:203],a[72:75],v[84:87]\n"
LD "v12,s[28:31],s82 offen offset:2048 lds\n"
MF "v[84:87],a[204:207],a[112:115],v[84:87]\n"
TR "a[220:221],v0 offset:20488\n"
MF "v[76:79],a[204:207],a[108:111],v[76:79]\n"
TR "a[222:223],v0 offset:21000\n"
MF "v[76:79],a[200:203],a[68:71],v[76:79]\n"
MF "v[68:71],a[200:203],a[64:67],v[68:71]\n"
MF "v[68:71],a[204:207],a[104:107],v[68:71]\n"
LD "v13,s[28:31],s82 offen offset:3072 lds\n"
MF "v[60:63],a[204:207],a[100:103],v[60:63]\n"
MF "v[60:63],a[200:203],a[60:63],v[60:63]\n"
"s_waitcnt lgkmcnt(4)\n"
MF "v[96:99],a[208:211],a[60:63],v[96:99]\n"
"s_add_u32 s84,s68,41472\n"
TR "a[200:201],v1 offset:16384\n"
MF "v[96:99],a[212:215],a[100:103],v[96:99]\n"
"s_add_u32 m0,s63,24576\n"
TR "a[202:203],v1 offset:16896\n"
MF "v[104:107],a[212:215],a[104:107],v[104:107]\n"
LD "v4,s[24:27],s84 offen lds\n"
MF "v[104:107],a[208:211],a[64:67],v[104:107]\n"
TR "a[204:205],v1 offset:20480\n"
MF "v[112:115],a[208:211],a[68:71],v[112:115]\n"
TR "a[206:207],v1 offset:20992\n"
MF "v[112:115],a[212:215],a[108:111],v[112:115]\n"
"s_add_u32 m0,s62,0\n"
MF "v[120:123],a[212:215],a[112:115],v[120:123]\n"
"s_add_u32 s82,s66,3145728\n"
MF "v[120:123],a[208:211],a[72:75],v[120:123]\n"
LD "v6,s[28:31],s82 offen lds\n"
MF "v[128:131],a[208:211],a[76:79],v[128:131]\n"
MF "v[128:131],a[212:215],a[116:119],v[128:131]\n"
"s_waitcnt lgkmcnt(4)\n"
MF "v[132:135],a[220:223],a[116:119],v[132:135]\n"
TR "a[208:209],v1 offset:16392\n"
MF "v[132:135],a[216:219],a[76:79],v[132:135]\n"
TR "a[210:211],v1 offset:16904\n"
MF "v[124:127],a[216:219],a[72:75],v[124:127]\n"
LD "v11,s[28:31],s82 offen offset:1024 lds\n"
MF "v[124:127],a[220:223],a[112:115],v[124:127]\n"
TR "a[212:213],v1 offset:20488\n"
MF "v[116:119],a[220:223],a[108:111],v[116:119]\n"
TR "a[214:215],v1 offset:21000\n"
MF "v[116:119],a[216:219],a[68:71],v[116:119]\n"
MF "v[108:111],a[216:219],a[64:67],v[108:111]\n"
MF "v[108:111],a[220:223],a[104:107],v[108:111]\n"
LD "v12,s[28:31],s82 offen offset:2048 lds\n"
MF "v[100:103],a[220:223],a[100:103],v[100:103]\n"
MF "v[100:103],a[216:219],a[60:63],v[100:103]\n"
"s_waitcnt vmcnt(21) lgkmcnt(0)\n"
"s_barrier\n"
MF "v[136:139],a[200:203],a[60:63],v[136:139]\n"
TR "a[216:217],v0 offset:32768\n"
MF "v[136:139],a[204:207],a[100:103],v[136:139]\n"
TR "a[218:219],v0 offset:33280\n"
MF "v[144:147],a[204:207],a[104:107],v[144:147]\n"
"ds_read_b128 a[140:143],v2\n"
MF "v[144:147],a[200:203],a[64:67],v[144:147]\n"
TR "a[220:221],v0 offset:36864\n"
MF "v[152:155],a[200:203],a[68:71],v[152:155]\n"
TR "a[222:223],v0 offset:37376\n"
MF "v[152:155],a[204:207],a[108:111],v[152:155]\n"
LD "v13,s[28:31],s82 offen offset:3072 lds\n"
MF "v[160:163],a[204:207],a[112:115],v[160:163]\n"
"ds_read_b128 a[180:183],v3\n"
MF "v[160:163],a[200:203],a[72:75],v[160:163]\n"
"s_add_u32 s84,s68,640\n"
MF "v[168:171],a[200:203],a[76:79],v[168:171]\n"
"s_add_u32 m0,s63,40960\n"
MF "v[168:171],a[204:207],a[116:119],v[168:171]\n"
LD "v4,s[24:27],s84 offen lds\n"
MF "v[172:175],a[212:215],a[116:119],v[172:175]\n"
TR "a[200:201],v0 offset:32776\n"
MF "v[172:175],a[208:211],a[76:79],v[172:175]\n"
TR "a[202:203],v0 offset:33288\n"
MF "v[164:167],a[208:211],a[72:75],v[164:167]\n"
"ds_read_b128 a[144:147],v2 offset:2048\n"
MF "v[164:167],a[212:215],a[112:115],v[164:167]\n"
TR "a[204:205],v0 offset:36872\n"
MF "v[156:159],a[212:215],a[108:111],v[156:159]\n"
"s_add_u32 s84,s68,9856\n"
TR "a[206:207],v0 offset:37384\n"
MF "v[156:159],a[208:211],a[68:71],v[156:159]\n"
LD "v5,s[24:27],s84 offen offset:1024 lds\n"
MF "v[148:151],a[208:211],a[64:67],v[148:151]\n"
"ds_read_b128 a[184:187],v3 offset:2048\n"
MF "v[148:151],a[212:215],a[104:107],v[148:151]\n"
"s_add_u32 m0,s62,16384\n"
MF "v[140:143],a[212:215],a[100:103],v[140:143]\n"
"s_add_u32 s82,s66,3145856\n"
MF "v[140:143],a[208:211],a[60:63],v[140:143]\n"
"s_waitcnt lgkmcnt(7)\n"
LD "v6,s[28:31],s82 offen lds\n"
MF "v[176:179],a[216:219],a[60:63],v[176:179]\n"
TR "a[208:209],v1 offset:32768\n"
MF "v[176:179],a[220:223],a[100:103],v[176:179]\n"
TR "a[210:211],v1 offset:33280\n"
MF "v[184:187],a[220:223],a[104:107],v[184:187]\n"
"ds_read_b128 a[148:151],v2 offset:4096\n"
MF "v[184:187],a[216:219],a[64:67],v[184:187]\n"
TR "a[212:213],v1 offset:36864\n"
MF "v[192:195],a[216:219],a[68:71],v[192:195]\n"
TR "a[214:215],v1 offset:37376\n"
MF "v[192:195],a[220:223],a[108:111],v[192:195]\n"
LD "v11,s[28:31],s82 offen offset:1024 lds\n"
MF "v[200:203],a[220:223],a[112:115],v[200:203]\n"
"ds_read_b128 a[188:191],v3 offset:4096\n"
MF "v[200:203],a[216:219],a[72:75],v[200:203]\n"
MF "v[208:211],a[216:219],a[76:79],v[208:211]\n"
MF "v[208:211],a[220:223],a[116:119],v[208:211]\n"
"s_waitcnt lgkmcnt(7)\n"
LD "v12,s[28:31],s82 offen offset:2048 lds\n"
MF "v[212:215],a[204:207],a[116:119],v[212:215]\n"
TR "a[216:217],v1 offset:32776\n"
MF "v[212:215],a[200:203],a[76:79],v[212:215]\n"
TR "a[218:219],v1 offset:33288\n"
MF "v[204:207],a[200:203],a[72:75],v[204:207]\n"
"ds_read_b128 a[152:155],v2 offset:6144\n"
MF "v[204:207],a[204:207],a[112:115],v[204:207]\n"
TR "a[220:221],v1 offset:36872\n"
MF "v[196:199],a[204:207],a[108:111],v[196:199]\n"
TR "a[222:223],v1 offset:37384\n"
MF "v[196:199],a[200:203],a[68:71],v[196:199]\n"
LD "v13,s[28:31],s82 offen offset:3072 lds\n"
MF "v[188:191],a[200:203],a[64:67],v[188:191]\n"
"s_add_u32 s84,s68,21120\n"
"v_add_u32 v0,0xc000,v0\n"
MF "v[188:191],a[204:207],a[104:107],v[188:191]\n"
"s_add_u32 m0,s63,43008\n"
"v_add_u32 v1,0xc000,v1\n"
MF "v[180:183],a[204:207],a[100:103],v[180:183]\n"
LD "v4,s[24:27],s84 offen lds\n"
MF "v[180:183],a[200:203],a[60:63],v[180:183]\n"
"s_waitcnt vmcnt(25) lgkmcnt(0)\n"
"s_barrier\n"
MF "v[216:219],a[208:211],a[60:63],v[216:219]\n"
TR "a[200:201],v0\n"
MF "v[216:219],a[212:215],a[100:103],v[216:219]\n"
TR "a[202:203],v0 offset:512\n"
MF "v[224:227],a[212:215],a[104:107],v[224:227]\n"
"ds_read_b128 a[192:195],v3 offset:6144\n"
MF "v[224:227],a[208:211],a[64:67],v[224:227]\n"
TR "a[204:205],v0 offset:4096\n"
MF "v[232:235],a[208:211],a[68:71],v[232:235]\n"
"s_add_u32 s84,s68,30336\n"
TR "a[206:207],v0 offset:4608\n"
MF "v[232:235],a[212:215],a[108:111],v[232:235]\n"
LD "v5,s[24:27],s84 offen offset:1024 lds\n"
MF "v[240:243],a[212:215],a[112:115],v[240:243]\n"
"ds_read_b128 a[156:159],v2 offset:8192\n"
MF "v[240:243],a[208:211],a[72:75],v[240:243]\n"
"s_add_u32 m0,s62,32768\n"
"ds_read_b128 a[196:199],v3 offset:8192\n"
MF "v[248:251],a[208:211],a[76:79],v[248:251]\n"
"s_add_u32 s82,s66,3145984\n"
MF "v[248:251],a[212:215],a[116:119],v[248:251]\n"
LD "v6,s[28:31],s82 offen lds\n"
MF "v[252:255],a[220:223],a[116:119],v[252:255]\n"
TR "a[208:209],v0 offset:8\n"
MF "v[252:255],a[216:219],a[76:79],v[252:255]\n"
TR "a[210:211],v0 offset:520\n"
MF "v[244:247],a[216:219],a[72:75],v[244:247]\n"
MF "v[244:247],a[220:223],a[112:115],v[244:247]\n"
TR "a[212:213],v0 offset:4104\n"
MF "v[236:239],a[220:223],a[108:111],v[236:239]\n"
TR "a[214:215],v0 offset:4616\n"
MF "v[236:239],a[216:219],a[68:71],v[236:239]\n"
LD "v11,s[28:31],s82 offen offset:1024 lds\n"
MF "v[228:231],a[216:219],a[64:67],v[228:231]\n"
MF "v[228:231],a[220:223],a[104:107],v[228:231]\n"
MF "v[220:223],a[220:223],a[100:103],v[220:223]\n"
MF "v[220:223],a[216:219],a[60:63],v[220:223]\n"
"s_waitcnt lgkmcnt(4)\n"
LD "v12,s[28:31],s82 offen offset:2048 lds\n"
MF "v[16:19],a[200:203],a[140:143],v[16:19]\n"
TR "a[216:217],v1\n"
MF "v[16:19],a[204:207],a[180:183],v[16:19]\n"
TR "a[218:219],v1 offset:512\n"
MF "v[24:27],a[204:207],a[184:187],v[24:27]\n"
MF "v[24:27],a[200:203],a[144:147],v[24:27]\n"
TR "a[220:221],v1 offset:4096\n"
MF "v[32:35],a[200:203],a[148:151],v[32:35]\n"
TR "a[222:223],v1 offset:4608\n"
MF "v[32:35],a[204:207],a[188:191],v[32:35]\n"
LD "v13,s[28:31],s82 offen offset:3072 lds\n"
MF "v[40:43],a[204:207],a[192:195],v[40:43]\n"
MF "v[40:43],a[200:203],a[152:155],v[40:43]\n"
"s_add_u32 s84,s68,41600\n"
MF "v[48:51],a[200:203],a[156:159],v[48:51]\n"
"s_add_u32 m0,s63,45056\n"
MF "v[48:51],a[204:207],a[196:199],v[48:51]\n"
"s_waitcnt lgkmcnt(4)\n"
LD "v4,s[24:27],s84 offen lds\n"
MF "v[52:55],a[212:215],a[196:199],v[52:55]\n"
TR "a[200:201],v1 offset:8\n"
MF "v[52:55],a[208:211],a[156:159],v[52:55]\n"
TR "a[202:203],v1 offset:520\n"
MF "v[44:47],a[208:211],a[152:155],v[44:47]\n"
MF "v[44:47],a[212:215],a[192:195],v[44:47]\n"
TR "a[204:205],v1 offset:4104\n"
MF "v[36:39],a[212:215],a[188:191],v[36:39]\n"
TR "a[206:207],v1 offset:4616\n"
MF "v[36:39],a[208:211],a[148:151],v[36:39]\n"
MF "v[28:31],a[208:211],a[144:147],v[28:31]\n"
MF "v[28:31],a[212:215],a[184:187],v[28:31]\n"
MF "v[20:23],a[212:215],a[180:183],v[20:23]\n"
MF "v[20:23],a[208:211],a[140:143],v[20:23]\n"
"s_waitcnt vmcnt(25) lgkmcnt(0)\n"
"s_barrier\n"
AW "a253,v10\n"
"v_add_u32 v10,1,v10\n"
"s_bfe_u32 s58,s22,0x30014\n"
"s_lshl_b32 s58,s58,2\n"
"s_add_u32 s58,s58,s56\n"
"s_lshl_b32 s58,s58,6\n"
"s_add_u32 s58,s58,s86\n"
"v_readlane_b32 s76,v10,s78\n"
"buffer_store_dword v10,v14,s[40:43],s58 offen offset:128 sc1\n"
".Lhs5:\n"
MF "v[56:59],a[216:219],a[140:143],v[56:59]\n"
"s_add_u32 m0,s62,49152\n"
TR "a[208:209],v0 offset:16384\n"
MF "v[56:59],a[220:223],a[180:183],v[56:59]\n"
"s_add_u32 s82,s66,3932160\n"
TR "a[210:211],v0 offset:16896\n"
MF "v[64:67],a[220:223],a[184:187],v[64:67]\n"
LD "v6,s[28:31],s82 offen lds\n"
MF "v[64:67],a[216:219],a[144:147],v[64:67]\n"
TR "a[212:213],v0 offset:20480\n"
MF "v[72:75],a[216:219],a[148:151],v[72:75]\n"
TR "a[214:215],v0 offset:20992\n"
MF "v[72:75],a[220:223],a[188:191],v[72:75]\n"
"buffer_load_dword a249,v15,s[48:51],s59 offen\n"
MF "v[80:83],a[220:223],a[192:195],v[80:83]\n"
"s_bfe_u32 s59,s23,0x30003\n"
MF "v[80:83],a[216:219],a[152:155],v[80:83]\n"
LD "v11,s[28:31],s82 offen offset:1024 lds\n"
MF "v[88:91],a[216:219],a[156:159],v[88:91]\n"
"s_add_u32 s59,s59,5\n"
MF "v[88:91],a[220:223],a[196:199],v[88:91]\n"
"s_lshl_b32 s59,s59,7\n"
MF "v[92:95],a[204:207],a[196:199],v[92:95]\n"
"s_and_b32 s59,s59,768\n"
TR "a[216:217],v0 offset:16392\n"
MF "v[92:95],a[200:203],a[156:159],v[92:95]\n"
"s_add_u32 s59,s59,s60\n"
TR "a[218:219],v0 offset:16904\n"
MF "v[84:87],a[200:203],a[152:155],v[84:87]\n"
LD "v12,s[28:31],s82 offen offset:2048 lds\n"
MF "v[84:87],a[204:207],a[192:195],v[84:87]\n"
TR "a[220:221],v0 offset:20488\n"
MF "v[76:79],a[204:207],a[188:191],v[76:79]\n"
TR "a[222:223],v0 offset:21000\n"
MF "v[76:79],a[200:203],a[148:151],v[76:79]\n"
MF "v[68:71],a[200:203],a[144:147],v[68:71]\n"
MF "v[68:71],a[204:207],a[184:187],v[68:71]\n"
LD "v13,s[28:31],s82 offen offset:3072 lds\n"
MF "v[60:63],a[204:207],a[180:183],v[60:63]\n"
MF "v[60:63],a[200:203],a[140:143],v[60:63]\n"
"s_waitcnt lgkmcnt(4)\n"
MF "v[96:99],a[208:211],a[140:143],v[96:99]\n"
"s_add_u32 s84,s68,768\n"
TR "a[200:201],v1 offset:16384\n"
MF "v[96:99],a[212:215],a[180:183],v[96:99]\n"
"s_add_u32 m0,s63,0\n"
TR "a[202:203],v1 offset:16896\n"
MF "v[104:107],a[212:215],a[184:187],v[104:107]\n"
LD "v4,s[24:27],s84 offen lds\n"
MF "v[104:107],a[208:211],a[144:147],v[104:107]\n"
TR "a[204:205],v1 offset:20480\n"
MF "v[112:115],a[208:211],a[148:151],v[112:115]\n"
TR "a[206:207],v1 offset:20992\n"
MF "v[112:115],a[212:215],a[188:191],v[112:115]\n"
MF "v[120:123],a[212:215],a[192:195],v[120:123]\n"
"s_add_u32 s84,s68,9984\n"
MF "v[120:123],a[208:211],a[152:155],v[120:123]\n"
LD "v5,s[24:27],s84 offen offset:1024 lds\n"
MF "v[128:131],a[208:211],a[156:159],v[128:131]\n"
MF "v[128:131],a[212:215],a[196:199],v[128:131]\n"
"s_waitcnt lgkmcnt(4)\n"
MF "v[132:135],a[220:223],a[196:199],v[132:135]\n"
TR "a[208:209],v1 offset:16392\n"
MF "v[132:135],a[216:219],a[156:159],v[132:135]\n"
TR "a[210:211],v1 offset:16904\n"
MF "v[124:127],a[216:219],a[152:155],v[124:127]\n"
MF "v[124:127],a[220:223],a[192:195],v[124:127]\n"
TR "a[212:213],v1 offset:20488\n"
MF "v[116:119],a[220:223],a[188:191],v[116:119]\n"
TR "a[214:215],v1 offset:21000\n"
MF "v[116:119],a[216:219],a[148:151],v[116:119]\n"
MF "v[108:111],a[216:219],a[144:147],v[108:111]\n"
MF "v[108:111],a[220:223],a[184:187],v[108:111]\n"
MF "v[100:103],a[220:223],a[180:183],v[100:103]\n"
MF "v[100:103],a[216:219],a[140:143],v[100:103]\n"
"s_waitcnt vmcnt(24) lgkmcnt(0)\n"
"s_barrier\n"
MF "v[136:139],a[200:203],a[140:143],v[136:139]\n"
TR "a[216:217],v0 offset:32768\n"
MF "v[136:139],a[204:207],a[180:183],v[136:139]\n"
TR "a[218:219],v0 offset:33280\n"
MF "v[144:147],a[204:207],a[184:187],v[144:147]\n"
"ds_read_b128 a[60:63],v2 offset:20480\n"
MF "v[144:147],a[200:203],a[144:147],v[144:147]\n"
"s_add_u32 m0,s62,65536\n"
TR "a[220:221],v0 offset:36864\n"
MF "v[152:155],a[200:203],a[148:151],v[152:155]\n"
"s_add_u32 s82,s66,3932288\n"
TR "a[222:223],v0 offset:37376\n"
MF "v[152:155],a[204:207],a[188:191],v[152:155]\n"
LD "v6,s[28:31],s82 offen lds\n"
MF "v[160:163],a[204:207],a[192:195],v[160:163]\n"
"ds_read_b128 a[100:103],v3 offset:20480\n"
MF "v[160:163],a[200:203],a[152:155],v[160:163]\n"
MF "v[168:171],a[200:203],a[156:159],v[168:171]\n"
MF "v[168:171],a[204:207],a[196:199],v[168:171]\n"
LD "v11,s[28:31],s82 offen offset:1024 lds\n"
MF "v[172:175],a[212:215],a[196:199],v[172:175]\n"
TR "a[200:201],v0 offset:32776\n"
MF "v[172:175],a[208:211],a[156:159],v[172:175]\n"
TR "a[202:203],v0 offset:33288\n"
MF "v[164:167],a[208:211],a[152:155],v[164:167]\n"
"ds_read_b128 a[64:67],v2 offset:22528\n"
MF "v[164:167],a[212:215],a[192:195],v[164:167]\n"
TR "a[204:205],v0 offset:36872\n"
MF "v[156:159],a[212:215],a[188:191],v[156:159]\n"
TR "a[206:207],v0 offset:37384\n"
MF "v[156:159],a[208:211],a[148:151],v[156:159]\n"
LD "v12,s[28:31],s82 offen offset:2048 lds\n"
MF "v[148:151],a[208:211],a[144:147],v[148:151]\n"
"ds_read_b128 a[104:107],v3 offset:22528\n"
MF "v[148:151],a[212:215],a[184:187],v[148:151]\n"
MF "v[140:143],a[212:215],a[180:183],v[140:143]\n"
MF "v[140:143],a[208:211],a[140:143],v[140:143]\n"
"s_waitcnt lgkmcnt(7)\n"
LD "v13,s[28:31],s82 offen offset:3072 lds\n"
MF "v[176:179],a[216:219],a[140:143],v[176:179]\n"
TR "a[208:209],v1 offset:32768\n"
MF "v[176:179],a[220:223],a[180:183],v[176:179]\n"
TR "a[210:211],v1 offset:33280\n"
MF "v[184:187],a[220:223],a[184:187],v[184:187]\n"
"ds_read_b128 a[68:71],v2 offset:24576\n"
MF "v[184:187],a[216:219],a[144:147],v[184:187]\n"
"s_add_u32 s84,s68,21248\n"
TR "a[212:213],v1 offset:36864\n"
MF "v[192:195],a[216:219],a[148:151],v[192:195]\n"
"s_add_u32 m0,s63,2048\n"
TR "a[214:215],v1 offset:37376\n"
MF "v[192:195],a[220:223],a[188:191],v[192:195]\n"
LD "v4,s[24:27],s84 offen lds\n"
MF "v[200:203],a[220:223],a[192:195],v[200:203]\n"
"ds_read_b128 a[108:111],v3 offset:24576\n"
MF "v[200:203],a[216:219],a[152:155],v[200:203]\n"
MF "v[208:211],a[216:219],a[156:159],v[208:211]\n"
"s_add_u32 s84,s68,30464\n"
MF "v[208:211],a[220:223],a[196:199],v[208:211]\n"
"s_waitcnt lgkmcnt(7)\n"
LD "v5,s[24:27],s84 offen offset:1024 lds\n"
MF "v[212:215],a[204:207],a[196:199],v[212:215]\n"
TR "a[216:217],v1 offset:32776\n"
MF "v[212:215],a[200:203],a[156:159],v[212:215]\n"
TR "a[218:219],v1 offset:33288\n"
MF "v[204:207],a[200:203],a[152:155],v[204:207]\n"
"ds_read_b128 a[72:75],v2 offset:26624\n"
MF "v[204:207],a[204:207],a[192:195],v[204:207]\n"
TR "a[220:221],v1 offset:36872\n"
MF "v[196:199],a[204:207],a[188:191],v[196:199]\n"
TR "a[222:223],v1 offset:37384\n"
MF "v[196:199],a[200:203],a[148:151],v[196:199]\n"
MF "v[188:191],a[200:203],a[144:147],v[188:191]\n"
"v_add_u32 v0,0xffff4000,v0\n"
MF "v[188:191],a[204:207],a[184:187],v[188:191]\n"
"v_add_u32 v1,0xffff4000,v1\n"
MF "v[180:183],a[204:207],a[180:183],v[180:183]\n"
MF "v[180:183],a[200:203],a[140:143],v[180:183]\n"
"s_waitcnt vmcnt(26) lgkmcnt(0)\n"
"s_barrier\n"
MF "v[216:219],a[208:211],a[140:143],v[216:219]\n"
TR "a[200:201],v0\n"
MF "v[216:219],a[212:215],a[180:183],v[216:219]\n"
TR "a[202:203],v0 offset:512\n"
MF "v[224:227],a[212:215],a[184:187],v[224:227]\n"
"ds_read_b128 a[112:115],v3 offset:26624\n"
MF "v[224:227],a[208:211],a[144:147],v[224:227]\n"
"s_add_u32 m0,s62,81920\n"
TR "a[204:205],v0 offset:4096\n"
MF "v[232:235],a[208:211],a[148:151],v[232:235]\n"
"s_add_u32 s82,s66,3932416\n"
TR "a[206:207],v0 offset:4608\n"
MF "v[232:235],a[212:215],a[188:191],v[232:235]\n"
LD "v6,s[28:31],s82 offen lds\n"
MF "v[240:243],a[212:215],a[192:195],v[240:243]\n"
"ds_read_b128 a[76:79],v2 offset:28672\n"
MF "v[240:243],a[208:211],a[152:155],v[240:243]\n"
"ds_read_b128 a[116:119],v3 offset:28672\n"
MF "v[248:251],a[208:211],a[156:159],v[248:251]\n"
MF "v[248:251],a[212:215],a[196:199],v[248:251]\n"
LD "v11,s[28:31],s82 offen offset:1024 lds\n"
MF "v[252:255],a[220:223],a[196:199],v[252:255]\n"
TR "a[208:209],v0 offset:8\n"
MF "v[252:255],a[216:219],a[156:159],v[252:255]\n"
TR "a[210:211],v0 offset:520\n"
MF "v[244:247],a[216:219],a[152:155],v[244:247]\n"
MF "v[244:247],a[220:223],a[192:195],v[244:247]\n"
TR "a[212:213],v0 offset:4104\n"
MF "v[236:239],a[220:223],a[188:191],v[236:239]\n"
TR "a[214:215],v0 offset:4616\n"
MF "v[236:239],a[216:219],a[148:151],v[236:239]\n"
LD "v12,s[28:31],s82 offen offset:2048 lds\n"
MF "v[228:231],a[216:219],a[144:147],v[228:231]\n"
MF "v[228:231],a[220:223],a[184:187],v[228:231]\n"
MF "v[220:223],a[220:223],a[180:183],v[220:223]\n"
MF "v[220:223],a[216:219],a[140:143],v[220:223]\n"
"s_waitcnt lgkmcnt(4)\n"
LD "v13,s[28:31],s82 offen offset:3072 lds\n"
MF "v[16:19],a[200:203],a[60:63],v[16:19]\n"
TR "a[216:217],v1\n"
MF "v[16:19],a[204:207],a[100:103],v[16:19]\n"
TR "a[218:219],v1 offset:512\n"
MF "v[24:27],a[204:207],a[104:107],v[24:27]\n"
MF "v[24:27],a[200:203],a[64:67],v[24:27]\n"
"s_add_u32 s84,s68,41728\n"
TR "a[220:221],v1 offset:4096\n"
MF "v[32:35],a[200:203],a[68:71],v[32:35]\n"
"s_add_u32 m0,s63,4096\n"
TR "a[222:223],v1 offset:4608\n"
MF "v[32:35],a[204:207],a[108:111],v[32:35]\n"
LD "v4,s[24:27],s84 offen lds\n"
MF "v[40:43],a[204:207],a[112:115],v[40:43]\n"
MF "v[40:43],a[200:203],a[72:75],v[40:43]\n"
MF "v[48:51],a[200:203],a[76:79],v[48:51]\n"
MF "v[48:51],a[204:207],a[116:119],v[48:51]\n"
"s_waitcnt lgkmcnt(4)\n"
MF "v[52:55],a[212:215],a[116:119],v[52:55]\n"
TR "a[200:201],v1 offset:8\n"
MF "v[52:55],a[208:211],a[76:79],v[52:55]\n"
TR "a[202:203],v1 offset:520\n"
MF "v[44:47],a[208:211],a[72:75],v[44:47]\n"
MF "v[44:47],a[212:215],a[112:115],v[44:47]\n"
TR "a[204:205],v1 offset:4104\n"
MF "v[36:39],a[212:215],a[108:111],v[36:39]\n"
TR "a[206:207],v1 offset:4616\n"
MF "v[36:39],a[208:211],a[68:71],v[36:39]\n"
MF "v[28:31],a[208:211],a[64:67],v[28:31]\n"
MF "v[28:31],a[212:215],a[104:107],v[28:31]\n"
MF "v[20:23],a[212:215],a[100:103],v[20:23]\n"
MF "v[20:23],a[208:211],a[60:63],v[20:23]\n"
"s_waitcnt vmcnt(25) lgkmcnt(0)\n"
"s_barrier\n"
MF "v[56:59],a[216:219],a[60:63],v[56:59]\n"
"s_add_u32 m0,s62,0\n"
TR "a[208:209],v0 offset:16384\n"
MF "v[56:59],a[220:223],a[100:103],v[56:59]\n"
"s_add_u32 s82,s66,4718592\n"
TR "a[210:211],v0 offset:16896\n"
MF "v[64:67],a[220:223],a[104:107],v[64:67]\n"
LD "v6,s[28:31],s82 offen lds\n"
MF "v[64:67],a[216:219],a[64:67],v[64:67]\n"
TR "a[212:213],v0 offset:20480\n"
MF "v[72:75],a[216:219],a[68:71],v[72:75]\n"
TR "a[214:215],v0 offset:20992\n"
MF "v[72:75],a[220:223],a[108:111],v[72:75]\n"
"buffer_load_dword a249,v15,s[48:51],s59 offen\n"
MF "v[80:83],a[220:223],a[112:115],v[80:83]\n"
MF "v[80:83],a[216:219],a[72:75],v[80:83]\n"
LD "v11,s[28:31],s82 offen offset:1024 lds\n"
MF "v[88:91],a[216:219],a[76:79],v[88:91]\n"
MF "v[88:91],a[220:223],a[116:119],v[88:91]\n"
MF "v[92:95],a[204:207],a[116:119],v[92:95]\n"
TR "a[216:217],v0 offset:16392\n"
MF "v[92:95],a[200:203],a[76:79],v[92:95]\n"
TR "a[218:219],v0 offset:16904\n"
MF "v[84:87],a[200:203],a[72:75],v[84:87]\n"
LD "v12,s[28:31],s82 offen offset:2048 lds\n"
MF "v[84:87],a[204:207],a[112:115],v[84:87]\n"
TR "a[220:221],v0 offset:20488\n"
MF "v[76:79],a[204:207],a[108:111],v[76:79]\n"
TR "a[222:223],v0 offset:21000\n"
MF "v[76:79],a[200:203],a[68:71],v[76:79]\n"
MF "v[68:71],a[200:203],a[64:67],v[68:71]\n"
MF "v[68:71],a[204:207],a[104:107],v[68:71]\n"
LD "v13,s[28:31],s82 offen offset:3072 lds\n"
MF "v[60:63],a[204:207],a[100:103],v[60:63]\n"
MF "v[60:63],a[200:203],a[60:63],v[60:63]\n"
"s_waitcnt lgkmcnt(4)\n"
MF "v[96:99],a[208:211],a[60:63],v[96:99]\n"
"s_add_u32 s84,s68,896\n"
TR "a[200:201],v1 offset:16384\n"
MF "v[96:99],a[212:215],a[100:103],v[96:99]\n"
"s_add_u32 m0,s63,20480\n"
TR "a[202:203],v1 offset:16896\n"
MF "v[104:107],a[212:215],a[104:107],v[104:107]\n"
LD "v4,s[24:27],s84 offen lds\n"
MF "v[104:107],a[208:211],a[64:67],v[104:107]\n"
TR "a[204:205],v1 offset:20480\n"
MF "v[112:115],a[208:211],a[68:71],v[112:115]\n"
TR "a[206:207],v1 offset:20992\n"
MF "v[112:115],a[212:215],a[108:111],v[112:115]\n"
MF "v[120:123],a[212:215],a[112:115],v[120:123]\n"
"s_add_u32 s84,s68,10112\n"
MF "v[120:123],a[208:211],a[72:75],v[120:123]\n"
LD "v5,s[24:27],s84 offen offset:1024 lds\n"
MF "v[128:131],a[208:211],a[76:79],v[128:131]\n"
MF "v[128:131],a[212:215],a[116:119],v[128:131]\n"
"s_waitcnt lgkmcnt(4)\n"
MF "v[132:135],a[220:223],a[116:119],v[132:135]\n"
TR "a[208:209],v1 offset:16392\n"
MF "v[132:135],a[216:219],a[76:79],v[132:135]\n"
TR "a[210:211],v1 offset:16904\n"
MF "v[124:127],a[216:219],a[72:75],v[124:127]\n"
MF "v[124:127],a[220:223],a[112:115],v[124:127]\n"
TR "a[212:213],v1 offset:20488\n"
MF "v[116:119],a[220:223],a[108:111],v[116:119]\n"
TR "a[214:215],v1 offset:21000\n"
MF "v[116:119],a[216:219],a[68:71],v[116:119]\n"
MF "v[108:111],a[216:219],a[64:67],v[108:111]\n"
MF "v[108:111],a[220:223],a[104:107],v[108:111]\n"
MF "v[100:103],a[220:223],a[100:103],v[100:103]\n"
"s_branch .Lqj\n"
".Lq0:\n"
MF "v[16:19],a[200:203],a[60:63],0\n"
"s_add_u32 m0,s62,49152\n"
TR "a[216:217],v1\n"
MF "v[16:19],a[204:207],a[100:103],v[16:19]\n"
"s_add_u32 s82,s66,786432\n"
TR "a[218:219],v1 offset:512\n"
MF "v[24:27],a[204:207],a[104:107],0\n"
LD "v6,s[28:31],s82 offen lds\n"
MF "v[24:27],a[200:203],a[64:67],v[24:27]\n"
TR "a[220:221],v1 offset:4096\n"
MF "v[32:35],a[200:203],a[68:71],0\n"
TR "a[222:223],v1 offset:4608\n"
MF "v[32:35],a[204:207],a[108:111],v[32:35]\n"
"v_max3_f32 v220,|v16|,|v17|,|v18|\n"
"v_max_f32_e64 v220,v220,|v19|\n"
MF "v[40:43],a[204:207],a[112:115],0\n"
"v_readlane_b32 s12,v220,3\n"
"v_readlane_b32 s6,v220,22\n"
MF "v[40:43],a[200:203],a[72:75],v[40:43]\n"
"s_min_u32 s12,s12,s6\n"
"v_readlane_b32 s7,v220,41\n"
"v_readlane_b32 s8,v220,60\n"
MF "v[48:51],a[200:203],a[76:79],0\n"
"s_min_u32 s7,s7,s8\n"
MF "v[48:51],a[204:207],a[116:119],v[48:51]\n"
LD "v11,s[28:31],s82 offen offset:1024 lds\n"
MF "v[52:55],a[212:215],a[116:119],0\n"
"s_min_u32 s12,s12,s7\n"
TR "a[200:201],v1 offset:8\n"
MF "v[52:55],a[208:211],a[76:79],v[52:55]\n"
"s_and_b32 s12,s12,0x7f800000\n"
TR "a[202:203],v1 offset:520\n"
MF "v[44:47],a[208:211],a[72:75],0\n"
"s_sub_u32 s12,s12,0x14000000\n"
MF "v[44:47],a[212:215],a[112:115],v[44:47]\n"
"s_cmp_lt_u32 s12,0x50000000\n"
"s_cselect_b32 s12,s12,0xe8400000\n"
TR "a[204:205],v1 offset:4104\n"
MF "v[36:39],a[212:215],a[108:111],0\n"
"s_add_u32 s12,s12,0x17c00000\n"
TR "a[206:207],v1 offset:4616\n"
MF "v[36:39],a[208:211],a[68:71],v[36:39]\n"
"v_mov_b32 v220,s12\n"
"v_mov_b32 v221,s12\n"
MF "v[28:31],a[208:211],a[64:67],0\n"
"v_mov_b32 v222,s12\n"
"v_mov_b32 v223,s12\n"
MF "v[28:31],a[212:215],a[104:107],v[28:31]\n"
LD "v12,s[28:31],s82 offen offset:2048 lds\n"
MF "v[20:23],a[212:215],a[100:103],0\n"
MF "v[20:23],a[208:211],a[60:63],v[20:23]\n"
"s_waitcnt vmcnt(7) lgkmcnt(0)\n"
"s_barrier\n"
MF "v[56:59],a[216:219],a[60:63],v[220:223]\n"
TR "a[208:209],v0 offset:16384\n"
MF "v[56:59],a[220:223],a[100:103],v[56:59]\n"
TR "a[210:211],v0 offset:16896\n"
MF "v[64:67],a[220:223],a[104:107],v[220:223]\n"
LD "v13,s[28:31],s82 offen offset:3072 lds\n"
MF "v[64:67],a[216:219],a[64:67],v[64:67]\n"
TR "a[212:213],v0 offset:20480\n"
MF "v[72:75],a[216:219],a[68:71],v[220:223]\n"
TR "a[214:215],v0 offset:20992\n"
MF "v[72:75],a[220:223],a[108:111],v[72:75]\n"
"s_add_u32 m0,s62,65536\n"
MF "v[80:83],a[220:223],a[112:115],v[220:223]\n"
"s_add_u32 s82,s66,786560\n"
MF "v[80:83],a[216:219],a[72:75],v[80:83]\n"
LD "v6,s[28:31],s82 offen lds\n"
MF "v[88:91],a[216:219],a[76:79],v[220:223]\n"
MF "v[88:91],a[220:223],a[116:119],v[88:91]\n"
MF "v[92:95],a[204:207],a[116:119],v[220:223]\n"
TR "a[216:217],v0 offset:16392\n"
MF "v[92:95],a[200:203],a[76:79],v[92:95]\n"
TR "a[218:219],v0 offset:16904\n"
MF "v[84:87],a[200:203],a[72:75],v[220:223]\n"
LD "v11,s[28:31],s82 offen offset:1024 lds\n"
MF "v[84:87],a[204:207],a[112:115],v[84:87]\n"
TR "a[220:221],v0 offset:20488\n"
MF "v[76:79],a[204:207],a[108:111],v[220:223]\n"
TR "a[222:223],v0 offset:21000\n"
MF "v[76:79],a[200:203],a[68:71],v[76:79]\n"
MF "v[68:71],a[200:203],a[64:67],v[220:223]\n"
MF "v[68:71],a[204:207],a[104:107],v[68:71]\n"
LD "v12,s[28:31],s82 offen offset:2048 lds\n"
MF "v[60:63],a[204:207],a[100:103],v[220:223]\n"
MF "v[60:63],a[200:203],a[60:63],v[60:63]\n"
"s_waitcnt lgkmcnt(4)\n"
MF "v[96:99],a[208:211],a[60:63],v[220:223]\n"
TR "a[200:201],v1 offset:16384\n"
MF "v[96:99],a[212:215],a[100:103],v[96:99]\n"
TR "a[202:203],v1 offset:16896\n"
MF "v[104:107],a[212:215],a[104:107],v[220:223]\n"
LD "v13,s[28:31],s82 offen offset:3072 lds\n"
MF "v[104:107],a[208:211],a[64:67],v[104:107]\n"
TR "a[204:205],v1 offset:20480\n"
MF "v[112:115],a[208:211],a[68:71],v[220:223]\n"
TR "a[206:207],v1 offset:20992\n"
MF "v[112:115],a[212:215],a[108:111],v[112:115]\n"
"s_add_u32 m0,s62,81920\n"
MF "v[120:123],a[212:215],a[112:115],v[220:223]\n"
"s_add_u32 s82,s66,786688\n"
MF "v[120:123],a[208:211],a[72:75],v[120:123]\n"
LD "v6,s[28:31],s82 offen lds\n"
MF "v[128:131],a[208:211],a[76:79],v[220:223]\n"
MF "v[128:131],a[212:215],a[116:119],v[128:131]\n"
"s_waitcnt lgkmcnt(4)\n"
MF "v[132:135],a[220:223],a[116:119],v[220:223]\n"
TR "a[208:209],v1 offset:16392\n"
MF "v[132:135],a[216:219],a[76:79],v[132:135]\n"
TR "a[210:211],v1 offset:16904\n"
MF "v[124:127],a[216:219],a[72:75],v[220:223]\n"
LD "v11,s[28:31],s82 offen offset:1024 lds\n"
MF "v[124:127],a[220:223],a[112:115],v[124:127]\n"
TR "a[212:213],v1 offset:20488\n"
MF "v[116:119],a[220:223],a[108:111],v[220:223]\n"
TR "a[214:215],v1 offset:21000\n"
MF "v[116:119],a[216:219],a[68:71],v[116:119]\n"
MF "v[108:111],a[216:219],a[64:67],v[220:223]\n"
MF "v[108:111],a[220:223],a[104:107],v[108:111]\n"
LD "v12,s[28:31],s82 offen offset:2048 lds\n"
MF "v[100:103],a[220:223],a[100:103],v[220:223]\n"
MF "v[100:103],a[216:219],a[60:63],v[100:103]\n"
"s_waitcnt vmcnt(11) lgkmcnt(0)\n"
"s_barrier\n"
MF "v[136:139],a[200:203],a[60:63],v[220:223]\n"
TR "a[216:217],v0 offset:32768\n"
MF "v[136:139],a[204:207],a[100:103],v[136:139]\n"
TR "a[218:219],v0 offset:33280\n"
MF "v[144:147],a[204:207],a[104:107],v[220:223]\n"
"ds_read_b128 a[140:143],v2 offset:20480\n"
MF "v[144:147],a[200:203],a[64:67],v[144:147]\n"
TR "a[220:221],v0 offset:36864\n"
MF "v[152:155],a[200:203],a[68:71],v[220:223]\n"
TR "a[222:223],v0 offset:37376\n"
MF "v[152:155],a[204:207],a[108:111],v[152:155]\n"
LD "v13,s[28:31],s82 offen offset:3072 lds\n"
MF "v[160:163],a[204:207],a[112:115],v[220:223]\n"
"ds_read_b128 a[180:183],v3 offset:20480\n"
MF "v[160:163],a[200:203],a[72:75],v[160:163]\n"
"s_add_u32 m0,s62,0\n"
MF "v[168:171],a[200:203],a[76:79],v[220:223]\n"
"s_add_u32 s82,s66,1572864\n"
MF "v[168:171],a[204:207],a[116:119],v[168:171]\n"
LD "v6,s[28:31],s82 offen lds\n"
MF "v[172:175],a[212:215],a[116:119],v[220:223]\n"
TR "a[200:201],v0 offset:32776\n"
MF "v[172:175],a[208:211],a[76:79],v[172:175]\n"
TR "a[202:203],v0 offset:33288\n"
MF "v[164:167],a[208:211],a[72:75],v[220:223]\n"
"ds_read_b128 a[144:147],v2 offset:22528\n"
MF "v[164:167],a[212:215],a[112:115],v[164:167]\n"
TR "a[204:205],v0 offset:36872\n"
MF "v[156:159],a[212:215],a[108:111],v[220:223]\n"
TR "a[206:207],v0 offset:37384\n"
MF "v[156:159],a[208:211],a[68:71],v[156:159]\n"
LD "v11,s[28:31],s82 offen offset:1024 lds\n"
MF "v[148:151],a[208:211],a[64:67],v[220:223]\n"
"ds_read_b128 a[184:187],v3 offset:22528\n"
MF "v[148:151],a[212:215],a[104:107],v[148:151]\n"
MF "v[140:143],a[212:215],a[100:103],v[220:223]\n"
MF "v[140:143],a[208:211],a[60:63],v[140:143]\n"
"s_waitcnt lgkmcnt(7)\n"
LD "v12,s[28:31],s82 offen offset:2048 lds\n"
MF "v[176:179],a[216:219],a[60:63],v[220:223]\n"
TR "a[208:209],v1 offset:32768\n"
MF "v[176:179],a[220:223],a[100:103],v[176:179]\n"
TR "a[210:211],v1 offset:33280\n"
MF "v[184:187],a[220:223],a[104:107],v[220:223]\n"
"ds_read_b128 a[148:151],v2 offset:24576\n"
MF "v[184:187],a[216:219],a[64:67],v[184:187]\n"
TR "a[212:213],v1 offset:36864\n"
MF "v[192:195],a[216:219],a[68:71],v[220:223]\n"
TR "a[214:215],v1 offset:37376\n"
MF "v[192:195],a[220:223],a[108:111],v[192:195]\n"
LD "v13,s[28:31],s82 offen offset:3072 lds\n"
MF "v[200:203],a[220:223],a[112:115],v[220:223]\n"
"ds_read_b128 a[188:191],v3 offset:24576\n"
MF "v[200:203],a[216:219],a[72:75],v[200:203]\n"
"s_add_u32 s84,s68,384\n"
MF "v[208:211],a[216:219],a[76:79],v[220:223]\n"
"s_add_u32 m0,s63,0\n"
MF "v[208:211],a[220:223],a[116:119],v[208:211]\n"
"s_waitcnt lgkmcnt(7)\n"
LD "v4,s[24:27],s84 offen lds\n"
MF "v[212:215],a[204:207],a[116:119],v[220:223]\n"
TR "a[216:217],v1 offset:32776\n"
MF "v[212:215],a[200:203],a[76:79],v[212:215]\n"
TR "a[218:219],v1 offset:33288\n"
MF "v[204:207],a[200:203],a[72:75],v[220:223]\n"
"ds_read_b128 a[152:155],v2 offset:26624\n"
MF "v[204:207],a[204:207],a[112:115],v[204:207]\n"
TR "a[220:221],v1 offset:36872\n"
MF "v[196:199],a[204:207],a[108:111],v[220:223]\n"
"s_add_u32 s84,s68,9600\n"
TR "a[222:223],v1 offset:37384\n"
MF "v[196:199],a[200:203],a[68:71],v[196:199]\n"
LD "v5,s[24:27],s84 offen offset:1024 lds\n"
MF "v[188:191],a[200:203],a[64:67],v[220:223]\n"
"s_add_u32 m0,s62,16384\n"
"v_add_u32 v0,0xc000,v0\n"
MF "v[188:191],a[204:207],a[104:107],v[188:191]\n"
"s_add_u32 s82,s66,1572992\n"
"v_add_u32 v1,0xc000,v1\n"
MF "v[180:183],a[204:207],a[100:103],v[220:223]\n"
LD "v6,s[28:31],s82 offen lds\n"
MF "v[180:183],a[200:203],a[60:63],v[180:183]\n"
"s_waitcnt vmcnt(15) lgkmcnt(0)\n"
"s_barrier\n"
MF "v[216:219],a[208:211],a[60:63],v[220:223]\n"
TR "a[200:201],v0\n"
MF "v[216:219],a[212:215],a[100:103],v[216:219]\n"
TR "a[202:203],v0 offset:512\n"
MF "v[224:227],a[212:215],a[104:107],v[220:223]\n"
"ds_read_b128 a[192:195],v3 offset:26624\n"
MF "v[224:227],a[208:211],a[64:67],v[224:227]\n"
TR "a[204:205],v0 offset:4096\n"
MF "v[232:235],a[208:211],a[68:71],v[220:223]\n"
TR "a[206:207],v0 offset:4608\n"
MF "v[232:235],a[212:215],a[108:111],v[232:235]\n"
LD "v11,s[28:31],s82 offen offset:1024 lds\n"
MF "v[240:243],a[212:215],a[112:115],v[220:223]\n"
"ds_read_b128 a[156:159],v2 offset:28672\n"
MF "v[240:243],a[208:211],a[72:75],v[240:243]\n"
"ds_read_b128 a[196:199],v3 offset:28672\n"
MF "v[248:251],a[208:211],a[76:79],v[220:223]\n"
MF "v[248:251],a[212:215],a[116:119],v[248:251]\n"
LD "v12,s[28:31],s82 offen offset:2048 lds\n"
MF "v[252:255],a[220:223],a[116:119],v[220:223]\n"
TR "a[208:209],v0 offset:8\n"
MF "v[252:255],a[216:219],a[76:79],v[252:255]\n"
TR "a[210:211],v0 offset:520\n"
MF "v[244:247],a[216:219],a[72:75],v[220:223]\n"
MF "v[244:247],a[220:223],a[112:115],v[244:247]\n"
TR "a[212:213],v0 offset:4104\n"
MF "v[236:239],a[220:223],a[108:111],v[220:223]\n"
TR "a[214:215],v0 offset:4616\n"
MF "v[236:239],a[216:219],a[68:71],v[236:239]\n"
LD "v13,s[28:31],s82 offen offset:3072 lds\n"
MF "v[228:231],a[216:219],a[64:67],v[220:223]\n"
MF "v[228:231],a[220:223],a[104:107],v[228:231]\n"
"s_add_u32 s84,s68,20864\n"
MF "v[220:223],a[220:223],a[100:103],v[220:223]\n"
"s_add_u32 m0,s63,2048\n"
MF "v[220:223],a[216:219],a[60:63],v[220:223]\n"
"s_waitcnt lgkmcnt(4)\n"
LD "v4,s[24:27],s84 offen lds\n"
MF "v[16:19],a[200:203],a[140:143],v[16:19]\n"
TR "a[216:217],v1\n"
MF "v[16:19],a[204:207],a[180:183],v[16:19]\n"
TR "a[218:219],v1 offset:512\n"
MF "v[24:27],a[204:207],a[184:187],v[24:27]\n"
MF "v[24:27],a[200:203],a[144:147],v[24:27]\n"
TR "a[220:221],v1 offset:4096\n"
MF "v[32:35],a[200:203],a[148:151],v[32:35]\n"
"s_add_u32 s84,s68,30080\n"
TR "a[222:223],v1 offset:4608\n"
MF "v[32:35],a[204:207],a[188:191],v[32:35]\n"
LD "v5,s[24:27],s84 offen offset:1024 lds\n"
MF "v[40:43],a[204:207],a[192:195],v[40:43]\n"
MF "v[40:43],a[200:203],a[152:155],v[40:43]\n"
"s_add_u32 m0,s62,32768\n"
MF "v[48:51],a[200:203],a[156:159],v[48:51]\n"
"s_add_u32 s82,s66,1573120\n"
MF "v[48:51],a[204:207],a[196:199],v[48:51]\n"
"s_waitcnt lgkmcnt(4)\n"
LD "v6,s[28:31],s82 offen lds\n"
MF "v[52:55],a[212:215],a[196:199],v[52:55]\n"
TR "a[200:201],v1 offset:8\n"
MF "v[52:55],a[208:211],a[156:159],v[52:55]\n"
TR "a[202:203],v1 offset:520\n"
MF "v[44:47],a[208:211],a[152:155],v[44:47]\n"
MF "v[44:47],a[212:215],a[192:195],v[44:47]\n"
TR "a[204:205],v1 offset:4104\n"
MF "v[36:39],a[212:215],a[188:191],v[36:39]\n"
TR "a[206:207],v1 offset:4616\n"
MF "v[36:39],a[208:211],a[148:151],v[36:39]\n"
LD "v11,s[28:31],s82 offen offset:1024 lds\n"
MF "v[28:31],a[208:211],a[144:147],v[28:31]\n"
MF "v[28:31],a[212:215],a[184:187],v[28:31]\n"
MF "v[20:23],a[212:215],a[180:183],v[20:23]\n"
LD "v12,s[28:31],s82 offen offset:2048 lds\n"
MF "v[20:23],a[208:211],a[140:143],v[20:23]\n"
"s_waitcnt vmcnt(19) lgkmcnt(0)\n"
"s_barrier\n"
MF "v[56:59],a[216:219],a[140:143],v[56:59]\n"
TR "a[208:209],v0 offset:16384\n"
MF "v[56:59],a[220:223],a[180:183],v[56:59]\n"
TR "a[210:211],v0 offset:16896\n"
MF "v[64:67],a[220:223],a[184:187],v[64:67]\n"
LD "v13,s[28:31],s82 offen offset:3072 lds\n"
MF "v[64:67],a[216:219],a[144:147],v[64:67]\n"
TR "a[212:213],v0 offset:20480\n"
MF "v[72:75],a[216:219],a[148:151],v[72:75]\n"
TR "a[214:215],v0 offset:20992\n"
MF "v[72:75],a[220:223],a[188:191],v[72:75]\n"
"s_add_u32 s84,s68,41344\n"
MF "v[80:83],a[220:223],a[192:195],v[80:83]\n"
"s_add_u32 m0,s63,4096\n"
MF "v[80:83],a[216:219],a[152:155],v[80:83]\n"
LD "v4,s[24:27],s84 offen lds\n"
MF "v[88:91],a[216:219],a[156:159],v[88:91]\n"
MF "v[88:91],a[220:223],a[196:199],v[88:91]\n"
"s_sub_u32 s66,s66,s83\n"
MF "v[92:95],a[204:207],a[196:199],v[92:95]\n"
"s_add_u32 m0,s62,49152\n"
TR "a[216:217],v0 offset:16392\n"
MF "v[92:95],a[200:203],a[156:159],v[92:95]\n"
"s_add_u32 s82,s66,2359296\n"
TR "a[218:219],v0 offset:16904\n"
MF "v[84:87],a[200:203],a[152:155],v[84:87]\n"
LD "v6,s[28:31],s82 offen lds\n"
MF "v[84:87],a[204:207],a[192:195],v[84:87]\n"
TR "a[220:221],v0 offset:20488\n"
MF "v[76:79],a[204:207],a[188:191],v[76:79]\n"
TR "a[222:223],v0 offset:21000\n"
MF "v[76:79],a[200:203],a[148:151],v[76:79]\n"
MF "v[68:71],a[200:203],a[144:147],v[68:71]\n"
MF "v[68:71],a[204:207],a[184:187],v[68:71]\n"
LD "v11,s[28:31],s82 offen offset:1024 lds\n"
MF "v[60:63],a[204:207],a[180:183],v[60:63]\n"
MF "v[60:63],a[200:203],a[140:143],v[60:63]\n"
"s_waitcnt lgkmcnt(4)\n"
MF "v[96:99],a[208:211],a[140:143],v[96:99]\n"
TR "a[200:201],v1 offset:16384\n"
MF "v[96:99],a[212:215],a[180:183],v[96:99]\n"
TR "a[202:203],v1 offset:16896\n"
MF "v[104:107],a[212:215],a[184:187],v[104:107]\n"
LD "v12,s[28:31],s82 offen offset:2048 lds\n"
MF "v[104:107],a[208:211],a[144:147],v[104:107]\n"
TR "a[204:205],v1 offset:20480\n"
MF "v[112:115],a[208:211],a[148:151],v[112:115]\n"
TR "a[206:207],v1 offset:20992\n"
MF "v[112:115],a[212:215],a[188:191],v[112:115]\n"
MF "v[120:123],a[212:215],a[192:195],v[120:123]\n"
MF "v[120:123],a[208:211],a[152:155],v[120:123]\n"
LD "v13,s[28:31],s82 offen offset:3072 lds\n"
MF "v[128:131],a[208:211],a[156:159],v[128:131]\n"
MF "v[128:131],a[212:215],a[196:199],v[128:131]\n"
"s_waitcnt lgkmcnt(4)\n"
MF "v[132:135],a[220:223],a[196:199],v[132:135]\n"
"s_add_u32 s84,s68,512\n"
TR "a[208:209],v1 offset:16392\n"
MF "v[132:135],a[216:219],a[156:159],v[132:135]\n"
"s_add_u32 m0,s63,20480\n"
TR "a[210:211],v1 offset:16904\n"
MF "v[124:127],a[216:219],a[152:155],v[124:127]\n"
LD "v4,s[24:27],s84 offen lds\n"
MF "v[124:127],a[220:223],a[192:195],v[124:127]\n"
TR "a[212:213],v1 offset:20488\n"
MF "v[116:119],a[220:223],a[188:191],v[116:119]\n"
TR "a[214:215],v1 offset:21000\n"
MF "v[116:119],a[216:219],a[148:151],v[116:119]\n"
MF "v[108:111],a[216:219],a[144:147],v[108:111]\n"
"s_add_u32 s84,s68,9728\n"
MF "v[108:111],a[220:223],a[184:187],v[108:111]\n"
LD "v5,s[24:27],s84 offen offset:1024 lds\n"
MF "v[100:103],a[220:223],a[180:183],v[100:103]\n"
MF "v[100:103],a[216:219],a[140:143],v[100:103]\n"
"s_waitcnt vmcnt(23) lgkmcnt(0)\n"
"s_barrier\n"
MF "v[136:139],a[200:203],a[140:143],v[136:139]\n"
TR "a[216:217],v0 offset:32768\n"
MF "v[136:139],a[204:207],a[180:183],v[136:139]\n"
TR "a[218:219],v0 offset:33280\n"
MF "v[144:147],a[204:207],a[184:187],v[144:147]\n"
"ds_read_b128 a[60:63],v2 offset:40960\n"
MF "v[144:147],a[200:203],a[144:147],v[144:147]\n"
"s_add_u32 m0,s62,65536\n"
TR "a[220:221],v0 offset:36864\n"
MF "v[152:155],a[200:203],a[148:151],v[152:155]\n"
"s_add_u32 s82,s66,2359424\n"
TR "a[222:223],v0 offset:37376\n"
MF "v[152:155],a[204:207],a[188:191],v[152:155]\n"
LD "v6,s[28:31],s82 offen lds\n"
MF "v[160:163],a[204:207],a[192:195],v[160:163]\n"
"ds_read_b128 a[100:103],v3 offset:40960\n"
MF "v[160:163],a[200:203],a[152:155],v[160:163]\n"
LD "v11,s[28:31],s82 offen offset:1024 lds\n"
MF "v[168:171],a[200:203],a[156:159],v[168:171]\n"
MF "v[168:171],a[204:207],a[196:199],v[168:171]\n"
LD "v12,s[28:31],s82 offen offset:2048 lds\n"
MF "v[172:175],a[212:215],a[196:199],v[172:175]\n"
TR "a[200:201],v0 offset:32776\n"
MF "v[172:175],a[208:211],a[156:159],v[172:175]\n"
TR "a[202:203],v0 offset:33288\n"
MF "v[164:167],a[208:211],a[152:155],v[164:167]\n"
"ds_read_b128 a[64:67],v2 offset:43008\n"
MF "v[164:167],a[212:215],a[192:195],v[164:167]\n"
TR "a[204:205],v0 offset:36872\n"
MF "v[156:159],a[212:215],a[188:191],v[156:159]\n"
TR "a[206:207],v0 offset:37384\n"
MF "v[156:159],a[208:211],a[148:151],v[156:159]\n"
MF "v[148:151],a[208:211],a[144:147],v[148:151]\n"
"ds_read_b128 a[104:107],v3 offset:43008\n"
MF "v[148:151],a[212:215],a[184:187],v[148:151]\n"
MF "v[140:143],a[212:215],a[180:183],v[140:143]\n"
LD "v13,s[28:31],s82 offen offset:3072 lds\n"
MF "v[140:143],a[208:211],a[140:143],v[140:143]\n"
"s_waitcnt lgkmcnt(7)\n"
MF "v[176:179],a[216:219],a[140:143],v[176:179]\n"
TR "a[208:209],v1 offset:32768\n"
MF "v[176:179],a[220:223],a[180:183],v[176:179]\n"
TR "a[210:211],v1 offset:33280\n"
MF "v[184:187],a[220:223],a[184:187],v[184:187]\n"
"ds_read_b128 a[68:71],v2 offset:45056\n"
MF "v[184:187],a[216:219],a[144:147],v[184:187]\n"
"s_add_u32 s84,s68,20992\n"
TR "a[212:213],v1 offset:36864\n"
MF "v[192:195],a[216:219],a[148:151],v[192:195]\n"
"s_add_u32 m0,s63,22528\n"
TR "a[214:215],v1 offset:37376\n"
MF "v[192:195],a[220:223],a[188:191],v[192:195]\n"
LD "v4,s[24:27],s84 offen lds\n"
MF "v[200:203],a[220:223],a[192:195],v[200:203]\n"
"ds_read_b128 a[108:111],v3 offset:45056\n"
MF "v[200:203],a[216:219],a[152:155],v[200:203]\n"
MF "v[208:211],a[216:219],a[156:159],v[208:211]\n"
"s_add_u32 s84,s68,30208\n"
MF "v[208:211],a[220:223],a[196:199],v[208:211]\n"
"s_waitcnt lgkmcnt(7)\n"
LD "v5,s[24:27],s84 offen offset:1024 lds\n"
MF "v[212:215],a[204:207],a[196:199],v[212:215]\n"
TR "a[216:217],v1 offset:32776\n"
MF "v[212:215],a[200:203],a[156:159],v[212:215]\n"
TR "a[218:219],v1 offset:33288\n"
MF "v[204:207],a[200:203],a[152:155],v[204:207]\n"
"ds_read_b128 a[72:75],v2 offset:47104\n"
MF "v[204:207],a[204:207],a[192:195],v[204:207]\n"
TR "a[220:221],v1 offset:36872\n"
MF "v[196:199],a[204:207],a[188:191],v[196:199]\n"
TR "a[222:223],v1 offset:37384\n"
MF "v[196:199],a[200:203],a[148:151],v[196:199]\n"
MF "v[188:191],a[200:203],a[144:147],v[188:191]\n"
"v_add_u32 v0,0xffff4000,v0\n"
MF "v[188:191],a[204:207],a[184:187],v[188:191]\n"
"v_add_u32 v1,0xffff4000,v1\n"
MF "v[180:183],a[204:207],a[180:183],v[180:183]\n"
MF "v[180:183],a[200:203],a[140:143],v[180:183]\n"
"s_waitcnt vmcnt(25) lgkmcnt(0)\n"
"s_barrier\n"
MF "v[216:219],a[208:211],a[140:143],v[216:219]\n"
TR "a[200:201],v0\n"
MF "v[216:219],a[212:215],a[180:183],v[216:219]\n"
TR "a[202:203],v0 offset:512\n"
MF "v[224:227],a[212:215],a[184:187],v[224:227]\n"
"ds_read_b128 a[112:115],v3 offset:47104\n"
MF "v[224:227],a[208:211],a[144:147],v[224:227]\n"
"s_add_u32 m0,s62,81920\n"
TR "a[204:205],v0 offset:4096\n"
MF "v[232:235],a[208:211],a[148:151],v[232:235]\n"
"s_add_u32 s82,s66,2359552\n"
TR "a[206:207],v0 offset:4608\n"
MF "v[232:235],a[212:215],a[188:191],v[232:235]\n"
LD "v6,s[28:31],s82 offen lds\n"
MF "v[240:243],a[212:215],a[192:195],v[240:243]\n"
"ds_read_b128 a[76:79],v2 offset:49152\n"
MF "v[240:243],a[208:211],a[152:155],v[240:243]\n"
"ds_read_b128 a[116:119],v3 offset:49152\n"
MF "v[248:251],a[208:211],a[156:159],v[248:251]\n"
LD "v11,s[28:31],s82 offen offset:1024 lds\n"
MF "v[248:251],a[212:215],a[196:199],v[248:251]\n"
MF "v[252:255],a[220:223],a[196:199],v[252:255]\n"
TR "a[208:209],v0 offset:8\n"
MF "v[252:255],a[216:219],a[156:159],v[252:255]\n"
TR "a[210:211],v0 offset:520\n"
MF "v[244:247],a[216:219],a[152:155],v[244:247]\n"
MF "v[244:247],a[220:223],a[192:195],v[244:247]\n"
TR "a[212:213],v0 offset:4104\n"
MF "v[236:239],a[220:223],a[188:191],v[236:239]\n"
TR "a[214:215],v0 offset:4616\n"
MF "v[236:239],a[216:219],a[148:151],v[236:239]\n"
LD "v12,s[28:31],s82 offen offset:2048 lds\n"
MF "v[228:231],a[216:219],a[144:147],v[228:231]\n"
MF "v[228:231],a[220:223],a[184:187],v[228:231]\n"
MF "v[220:223],a[220:223],a[180:183],v[220:223]\n"
MF "v[220:223],a[216:219],a[140:143],v[220:223]\n"
"s_waitcnt lgkmcnt(4)\n"
MF "v[16:19],a[200:203],a[60:63],v[16:19]\n"
TR "a[216:217],v1\n"
MF "v[16:19],a[204:207],a[100:103],v[16:19]\n"
TR "a[218:219],v1 offset:512\n"
MF "v[24:27],a[204:207],a[104:107],v[24:27]\n"
LD "v13,s[28:31],s82 offen offset:3072 lds\n"
MF "v[24:27],a[200:203],a[64:67],v[24:27]\n"
TR "a[220:221],v1 offset:4096\n"
MF "v[32:35],a[200:203],a[68:71],v[32:35]\n"
TR "a[222:223],v1 offset:4608\n"
MF "v[32:35],a[204:207],a[108:111],v[32:35]\n"
MF "v[40:43],a[204:207],a[112:115],v[40:43]\n"
MF "v[40:43],a[200:203],a[72:75],v[40:43]\n"
"s_add_u32 s84,s68,41472\n"
MF "v[48:51],a[200:203],a[76:79],v[48:51]\n"
"s_add_u32 m0,s63,24576\n"
MF "v[48:51],a[204:207],a[116:119],v[48:51]\n"
"s_waitcnt lgkmcnt(4)\n"
LD "v4,s[24:27],s84 offen lds\n"
MF "v[52:55],a[212:215],a[116:119],v[52:55]\n"
TR "a[200:201],v1 offset:8\n"
MF "v[52:55],a[208:211],a[76:79],v[52:55]\n"
TR "a[202:203],v1 offset:520\n"
MF "v[44:47],a[208:211],a[72:75],v[44:47]\n"
MF "v[44:47],a[212:215],a[112:115],v[44:47]\n"
TR "a[204:205],v1 offset:4104\n"
MF "v[36:39],a[212:215],a[108:111],v[36:39]\n"
TR "a[206:207],v1 offset:4616\n"
MF "v[36:39],a[208:211],a[68:71],v[36:39]\n"
MF "v[28:31],a[208:211],a[64:67],v[28:31]\n"
MF "v[28:31],a[212:215],a[104:107],v[28:31]\n"
MF "v[20:23],a[212:215],a[100:103],v[20:23]\n"
MF "v[20:23],a[208:211],a[60:63],v[20:23]\n"
"s_waitcnt vmcnt(24) lgkmcnt(0)\n"
"s_barrier\n"
MF "v[56:59],a[216:219],a[60:63],v[56:59]\n"
"s_add_u32 m0,s62,0\n"
TR "a[208:209],v0 offset:16384\n"
MF "v[56:59],a[220:223],a[100:103],v[56:59]\n"
"s_add_u32 s82,s66,3145728\n"
TR "a[210:211],v0 offset:16896\n"
MF "v[64:67],a[220:223],a[104:107],v[64:67]\n"
LD "v6,s[28:31],s82 offen lds\n"
MF "v[64:67],a[216:219],a[64:67],v[64:67]\n"
TR "a[212:213],v0 offset:20480\n"
MF "v[72:75],a[216:219],a[68:71],v[72:75]\n"
TR "a[214:215],v0 offset:20992\n"
MF "v[72:75],a[220:223],a[108:111],v[72:75]\n"
MF "v[80:83],a[220:223],a[112:115],v[80:83]\n"
LD "v11,s[28:31],s82 offen offset:1024 lds\n"
MF "v[80:83],a[216:219],a[72:75],v[80:83]\n"
MF "v[88:91],a[216:219],a[76:79],v[88:91]\n"
MF "v[88:91],a[220:223],a[116:119],v[88:91]\n"
MF "v[92:95],a[204:207],a[116:119],v[92:95]\n"
TR "a[216:217],v0 offset:16392\n"
MF "v[92:95],a[200:203],a[76:79],v[92:95]\n"
TR "a[218:219],v0 offset:16904\n"
MF "v[84:87],a[200:203],a[72:75],v[84:87]\n"
LD "v12,s[28:31],s82 offen offset:2048 lds\n"
MF "v[84:87],a[204:207],a[112:115],v[84:87]\n"
TR "a[220:221],v0 offset:20488\n"
MF "v[76:79],a[204:207],a[108:111],v[76:79]\n"
TR "a[222:223],v0 offset:21000\n"
MF "v[76:79],a[200:203],a[68:71],v[76:79]\n"
MF "v[68:71],a[200:203],a[64:67],v[68:71]\n"
MF "v[68:71],a[204:207],a[104:107],v[68:71]\n"
MF "v[60:63],a[204:207],a[100:103],v[60:63]\n"
LD "v13,s[28:31],s82 offen offset:3072 lds\n"
MF "v[60:63],a[200:203],a[60:63],v[60:63]\n"
"s_waitcnt lgkmcnt(4)\n"
MF "v[96:99],a[208:211],a[60:63],v[96:99]\n"
TR "a[200:201],v1 offset:16384\n"
MF "v[96:99],a[212:215],a[100:103],v[96:99]\n"
TR "a[202:203],v1 offset:16896\n"
MF "v[104:107],a[212:215],a[104:107],v[104:107]\n"
MF "v[104:107],a[208:211],a[64:67],v[104:107]\n"
"s_add_u32 s84,s68,640\n"
TR "a[204:205],v1 offset:20480\n"
MF "v[112:115],a[208:211],a[68:71],v[112:115]\n"
"s_add_u32 m0,s63,40960\n"
TR "a[206:207],v1 offset:20992\n"
MF "v[112:115],a[212:215],a[108:111],v[112:115]\n"
LD "v4,s[24:27],s84 offen lds\n"
MF "v[120:123],a[212:215],a[112:115],v[120:123]\n"
MF "v[120:123],a[208:211],a[72:75],v[120:123]\n"
MF "v[128:131],a[208:211],a[76:79],v[128:131]\n"
"s_add_u32 s84,s68,9856\n"
MF "v[128:131],a[212:215],a[116:119],v[128:131]\n"
"s_waitcnt lgkmcnt(4)\n"
LD "v5,s[24:27],s84 offen offset:1024 lds\n"
MF "v[132:135],a[220:223],a[116:119],v[132:135]\n"
TR "a[208:209],v1 offset:16392\n"
MF "v[132:135],a[216:219],a[76:79],v[132:135]\n"
TR "a[210:211],v1 offset:16904\n"
MF "v[124:127],a[216:219],a[72:75],v[124:127]\n"
MF "v[124:127],a[220:223],a[112:115],v[124:127]\n"
TR "a[212:213],v1 offset:20488\n"
MF "v[116:119],a[220:223],a[108:111],v[116:119]\n"
TR "a[214:215],v1 offset:21000\n"
MF "v[116:119],a[216:219],a[68:71],v[116:119]\n"
MF "v[108:111],a[216:219],a[64:67],v[108:111]\n"
MF "v[108:111],a[220:223],a[104:107],v[108:111]\n"
MF "v[100:103],a[220:223],a[100:103],v[100:103]\n"
MF "v[100:103],a[216:219],a[60:63],v[100:103]\n"
"s_waitcnt vmcnt(23) lgkmcnt(0)\n"
"s_barrier\n"
MF "v[136:139],a[200:203],a[60:63],v[136:139]\n"
TR "a[216:217],v0 offset:32768\n"
MF "v[136:139],a[204:207],a[100:103],v[136:139]\n"
TR "a[218:219],v0 offset:33280\n"
MF "v[144:147],a[204:207],a[104:107],v[144:147]\n"
"ds_read_b128 a[140:143],v2\n"
MF "v[144:147],a[200:203],a[64:67],v[144:147]\n"
"s_add_u32 m0,s62,16384\n"
TR "a[220:221],v0 offset:36864\n"
MF "v[152:155],a[200:203],a[68:71],v[152:155]\n"
"s_add_u32 s82,s66,3145856\n"
TR "a[222:223],v0 offset:37376\n"
MF "v[152:155],a[204:207],a[108:111],v[152:155]\n"
LD "v6,s[28:31],s82 offen lds\n"
MF "v[160:163],a[204:207],a[112:115],v[160:163]\n"
"ds_read_b128 a[180:183],v3\n"
MF "v[160:163],a[200:203],a[72:75],v[160:163]\n"
LD "v11,s[28:31],s82 offen offset:1024 lds\n"
MF "v[168:171],a[200:203],a[76:79],v[168:171]\n"
MF "v[168:171],a[204:207],a[116:119],v[168:171]\n"
LD "v12,s[28:31],s82 offen offset:2048 lds\n"
MF "v[172:175],a[212:215],a[116:119],v[172:175]\n"
TR "a[200:201],v0 offset:32776\n"
MF "v[172:175],a[208:211],a[76:79],v[172:175]\n"
TR "a[202:203],v0 offset:33288\n"
MF "v[164:167],a[208:211],a[72:75],v[164:167]\n"
"ds_read_b128 a[144:147],v2 offset:2048\n"
MF "v[164:167],a[212:215],a[112:115],v[164:167]\n"
TR "a[204:205],v0 offset:36872\n"
MF "v[156:159],a[212:215],a[108:111],v[156:159]\n"
TR "a[206:207],v0 offset:37384\n"
MF "v[156:159],a[208:211],a[68:71],v[156:159]\n"
MF "v[148:151],a[208:211],a[64:67],v[148:151]\n"
"ds_read_b128 a[184:187],v3 offset:2048\n"
MF "v[148:151],a[212:215],a[104:107],v[148:151]\n"
MF "v[140:143],a[212:215],a[100:103],v[140:143]\n"
LD "v13,s[28:31],s82 offen offset:3072 lds\n"
MF "v[140:143],a[208:211],a[60:63],v[140:143]\n"
"s_waitcnt lgkmcnt(7)\n"
MF "v[176:179],a[216:219],a[60:63],v[176:179]\n"
TR "a[208:209],v1 offset:32768\n"
MF "v[176:179],a[220:223],a[100:103],v[176:179]\n"
TR "a[210:211],v1 offset:33280\n"
MF "v[184:187],a[220:223],a[104:107],v[184:187]\n"
"ds_read_b128 a[148:151],v2 offset:4096\n"
MF "v[184:187],a[216:219],a[64:67],v[184:187]\n"
"s_add_u32 s84,s68,21120\n"
TR "a[212:213],v1 offset:36864\n"
MF "v[192:195],a[216:219],a[68:71],v[192:195]\n"
"s_add_u32 m0,s63,43008\n"
TR "a[214:215],v1 offset:37376\n"
MF "v[192:195],a[220:223],a[108:111],v[192:195]\n"
LD "v4,s[24:27],s84 offen lds\n"
MF "v[200:203],a[220:223],a[112:115],v[200:203]\n"
"ds_read_b128 a[188:191],v3 offset:4096\n"
MF "v[200:203],a[216:219],a[72:75],v[200:203]\n"
MF "v[208:211],a[216:219],a[76:79],v[208:211]\n"
"s_add_u32 s84,s68,30336\n"
MF "v[208:211],a[220:223],a[116:119],v[208:211]\n"
"s_waitcnt lgkmcnt(7)\n"
LD "v5,s[24:27],s84 offen offset:1024 lds\n"
MF "v[212:215],a[204:207],a[116:119],v[212:215]\n"
TR "a[216:217],v1 offset:32776\n"
MF "v[212:215],a[200:203],a[76:79],v[212:215]\n"
TR "a[218:219],v1 offset:33288\n"
MF "v[204:207],a[200:203],a[72:75],v[204:207]\n"
"ds_read_b128 a[152:155],v2 offset:6144\n"
MF "v[204:207],a[204:207],a[112:115],v[204:207]\n"
TR "a[220:221],v1 offset:36872\n"
MF "v[196:199],a[204:207],a[108:111],v[196:199]\n"
TR "a[222:223],v1 offset:37384\n"
MF "v[196:199],a[200:203],a[68:71],v[196:199]\n"
MF "v[188:191],a[200:203],a[64:67],v[188:191]\n"
"v_add_u32 v0,0xc000,v0\n"
MF "v[188:191],a[204:207],a[104:107],v[188:191]\n"
"v_add_u32 v1,0xc000,v1\n"
MF "v[180:183],a[204:207],a[100:103],v[180:183]\n"
MF "v[180:183],a[200:203],a[60:63],v[180:183]\n"
"s_waitcnt vmcnt(25) lgkmcnt(0)\n"
"s_barrier\n"
MF "v[216:219],a[208:211],a[60:63],v[216:219]\n"
TR "a[200:201],v0\n"
MF "v[216:219],a[212:215],a[100:103],v[216:219]\n"
TR "a[202:203],v0 offset:512\n"
MF "v[224:227],a[212:215],a[104:107],v[224:227]\n"
"ds_read_b128 a[192:195],v3 offset:6144\n"
MF "v[224:227],a[208:211],a[64:67],v[224:227]\n"
"s_add_u32 m0,s62,32768\n"
TR "a[204:205],v0 offset:4096\n"
MF "v[232:235],a[208:211],a[68:71],v[232:235]\n"
"s_add_u32 s82,s66,3145984\n"
TR "a[206:207],v0 offset:4608\n"
MF "v[232:235],a[212:215],a[108:111],v[232:235]\n"
LD "v6,s[28:31],s82 offen lds\n"
MF "v[240:243],a[212:215],a[112:115],v[240:243]\n"
"ds_read_b128 a[156:159],v2 offset:8192\n"
MF "v[240:243],a[208:211],a[72:75],v[240:243]\n"
"ds_read_b128 a[196:199],v3 offset:8192\n"
MF "v[248:251],a[208:211],a[76:79],v[248:251]\n"
LD "v11,s[28:31],s82 offen offset:1024 lds\n"
MF "v[248:251],a[212:215],a[116:119],v[248:251]\n"
MF "v[252:255],a[220:223],a[116:119],v[252:255]\n"
TR "a[208:209],v0 offset:8\n"
MF "v[252:255],a[216:219],a[76:79],v[252:255]\n"
TR "a[210:211],v0 offset:520\n"
MF "v[244:247],a[216:219],a[72:75],v[244:247]\n"
MF "v[244:247],a[220:223],a[112:115],v[244:247]\n"
TR "a[212:213],v0 offset:4104\n"
MF "v[236:239],a[220:223],a[108:111],v[236:239]\n"
TR "a[214:215],v0 offset:4616\n"
MF "v[236:239],a[216:219],a[68:71],v[236:239]\n"
LD "v12,s[28:31],s82 offen offset:2048 lds\n"
MF "v[228:231],a[216:219],a[64:67],v[228:231]\n"
MF "v[228:231],a[220:223],a[104:107],v[228:231]\n"
MF "v[220:223],a[220:223],a[100:103],v[220:223]\n"
MF "v[220:223],a[216:219],a[60:63],v[220:223]\n"
"s_waitcnt lgkmcnt(4)\n"
MF "v[16:19],a[200:203],a[140:143],v[16:19]\n"
TR "a[216:217],v1\n"
MF "v[16:19],a[204:207],a[180:183],v[16:19]\n"
TR "a[218:219],v1 offset:512\n"
MF "v[24:27],a[204:207],a[184:187],v[24:27]\n"
LD "v13,s[28:31],s82 offen offset:3072 lds\n"
MF "v[24:27],a[200:203],a[144:147],v[24:27]\n"
TR "a[220:221],v1 offset:4096\n"
MF "v[32:35],a[200:203],a[148:151],v[32:35]\n"
TR "a[222:223],v1 offset:4608\n"
MF "v[32:35],a[204:207],a[188:191],v[32:35]\n"
MF "v[40:43],a[204:207],a[192:195],v[40:43]\n"
MF "v[40:43],a[200:203],a[152:155],v[40:43]\n"
"s_add_u32 s84,s68,41600\n"
MF "v[48:51],a[200:203],a[156:159],v[48:51]\n"
"s_add_u32 m0,s63,45056\n"
MF "v[48:51],a[204:207],a[196:199],v[48:51]\n"
"s_waitcnt lgkmcnt(4)\n"
LD "v4,s[24:27],s84 offen lds\n"
MF "v[52:55],a[212:215],a[196:199],v[52:55]\n"
TR "a[200:201],v1 offset:8\n"
MF "v[52:55],a[208:211],a[156:159],v[52:55]\n"
TR "a[202:203],v1 offset:520\n"
MF "v[44:47],a[208:211],a[152:155],v[44:47]\n"
MF "v[44:47],a[212:215],a[192:195],v[44:47]\n"
TR "a[204:205],v1 offset:4104\n"
MF "v[36:39],a[212:215],a[188:191],v[36:39]\n"
TR "a[206:207],v1 offset:4616\n"
MF "v[36:39],a[208:211],a[148:151],v[36:39]\n"
MF "v[28:31],a[208:211],a[144:147],v[28:31]\n"
MF "v[28:31],a[212:215],a[184:187],v[28:31]\n"
MF "v[20:23],a[212:215],a[180:183],v[20:23]\n"
MF "v[20:23],a[208:211],a[140:143],v[20:23]\n"
"s_waitcnt vmcnt(24) lgkmcnt(0)\n"
"s_barrier\n"
MF "v[56:59],a[216:219],a[140:143],v[56:59]\n"
"s_add_u32 m0,s62,49152\n"
TR "a[208:209],v0 offset:16384\n"
MF "v[56:59],a[220:223],a[180:183],v[56:59]\n"
"s_add_u32 s82,s66,3932160\n"
TR "a[210:211],v0 offset:16896\n"
MF "v[64:67],a[220:223],a[184:187],v[64:67]\n"
LD "v6,s[28:31],s82 offen lds\n"
MF "v[64:67],a[216:219],a[144:147],v[64:67]\n"
TR "a[212:213],v0 offset:20480\n"
MF "v[72:75],a[216:219],a[148:151],v[72:75]\n"
TR "a[214:215],v0 offset:20992\n"
MF "v[72:75],a[220:223],a[188:191],v[72:75]\n"
MF "v[80:83],a[220:223],a[192:195],v[80:83]\n"
LD "v11,s[28:31],s82 offen offset:1024 lds\n"
MF "v[80:83],a[216:219],a[152:155],v[80:83]\n"
MF "v[88:91],a[216:219],a[156:159],v[88:91]\n"
MF "v[88:91],a[220:223],a[196:199],v[88:91]\n"
MF "v[92:95],a[204:207],a[196:199],v[92:95]\n"
TR "a[216:217],v0 offset:16392\n"
MF "v[92:95],a[200:203],a[156:159],v[92:95]\n"
TR "a[218:219],v0 offset:16904\n"
MF "v[84:87],a[200:203],a[152:155],v[84:87]\n"
LD "v12,s[28:31],s82 offen offset:2048 lds\n"
MF "v[84:87],a[204:207],a[192:195],v[84:87]\n"
TR "a[220:221],v0 offset:20488\n"
MF "v[76:79],a[204:207],a[188:191],v[76:79]\n"
TR "a[222:223],v0 offset:21000\n"
MF "v[76:79],a[200:203],a[148:151],v[76:79]\n"
MF "v[68:71],a[200:203],a[144:147],v[68:71]\n"
MF "v[68:71],a[204:207],a[184:187],v[68:71]\n"
MF "v[60:63],a[204:207],a[180:183],v[60:63]\n"
LD "v13,s[28:31],s82 offen offset:3072 lds\n"
MF "v[60:63],a[200:203],a[140:143],v[60:63]\n"
"s_waitcnt lgkmcnt(4)\n"
MF "v[96:99],a[208:211],a[140:143],v[96:99]\n"
TR "a[200:201],v1 offset:16384\n"
MF "v[96:99],a[212:215],a[180:183],v[96:99]\n"
TR "a[202:203],v1 offset:16896\n"
MF "v[104:107],a[212:215],a[184:187],v[104:107]\n"
MF "v[104:107],a[208:211],a[144:147],v[104:107]\n"
"s_add_u32 s84,s68,768\n"
TR "a[204:205],v1 offset:20480\n"
MF "v[112:115],a[208:211],a[148:151],v[112:115]\n"
"s_add_u32 m0,s63,0\n"
TR "a[206:207],v1 offset:20992\n"
MF "v[112:115],a[212:215],a[188:191],v[112:115]\n"
LD "v4,s[24:27],s84 offen lds\n"
MF "v[120:123],a[212:215],a[192:195],v[120:123]\n"
MF "v[120:123],a[208:211],a[152:155],v[120:123]\n"
MF "v[128:131],a[208:211],a[156:159],v[128:131]\n"
"s_add_u32 s84,s68,9984\n"
MF "v[128:131],a[212:215],a[196:199],v[128:131]\n"
"s_waitcnt lgkmcnt(4)\n"
LD "v5,s[24:27],s84 offen offset:1024 lds\n"
MF "v[132:135],a[220:223],a[196:199],v[132:135]\n"
TR "a[208:209],v1 offset:16392\n"
MF "v[132:135],a[216:219],a[156:159],v[132:135]\n"
TR "a[210:211],v1 offset:16904\n"
MF "v[124:127],a[216:219],a[152:155],v[124:127]\n"
MF "v[124:127],a[220:223],a[192:195],v[124:127]\n"
TR "a[212:213],v1 offset:20488\n"
MF "v[116:119],a[220:223],a[188:191],v[116:119]\n"
TR "a[214:215],v1 offset:21000\n"
MF "v[116:119],a[216:219],a[148:151],v[116:119]\n"
MF "v[108:111],a[216:219],a[144:147],v[108:111]\n"
MF "v[108:111],a[220:223],a[184:187],v[108:111]\n"
MF "v[100:103],a[220:223],a[180:183],v[100:103]\n"
MF "v[100:103],a[216:219],a[140:143],v[100:103]\n"
"s_waitcnt vmcnt(23) lgkmcnt(0)\n"
"s_barrier\n"
MF "v[136:139],a[200:203],a[140:143],v[136:139]\n"
TR "a[216:217],v0 offset:32768\n"
MF "v[136:139],a[204:207],a[180:183],v[136:139]\n"
TR "a[218:219],v0 offset:33280\n"
MF "v[144:147],a[204:207],a[184:187],v[144:147]\n"
"ds_read_b128 a[60:63],v2 offset:20480\n"
MF "v[144:147],a[200:203],a[144:147],v[144:147]\n"
"s_add_u32 m0,s62,65536\n"
TR "a[220:221],v0 offset:36864\n"
MF "v[152:155],a[200:203],a[148:151],v[152:155]\n"
"s_add_u32 s82,s66,3932288\n"
TR "a[222:223],v0 offset:37376\n"
MF "v[152:155],a[204:207],a[188:191],v[152:155]\n"
LD "v6,s[28:31],s82 offen lds\n"
MF "v[160:163],a[204:207],a[192:195],v[160:163]\n"
"ds_read_b128 a[100:103],v3 offset:20480\n"
MF "v[160:163],a[200:203],a[152:155],v[160:163]\n"
LD "v11,s[28:31],s82 offen offset:1024 lds\n"
MF "v[168:171],a[200:203],a[156:159],v[168:171]\n"
MF "v[168:171],a[204:207],a[196:199],v[168:171]\n"
LD "v12,s[28:31],s82 offen offset:2048 lds\n"
MF "v[172:175],a[212:215],a[196:199],v[172:175]\n"
TR "a[200:201],v0 offset:32776\n"
MF "v[172:175],a[208:211],a[156:159],v[172:175]\n"
TR "a[202:203],v0 offset:33288\n"
MF "v[164:167],a[208:211],a[152:155],v[164:167]\n"
"ds_read_b128 a[64:67],v2 offset:22528\n"
MF "v[164:167],a[212:215],a[192:195],v[164:167]\n"
TR "a[204:205],v0 offset:36872\n"
MF "v[156:159],a[212:215],a[188:191],v[156:159]\n"
TR "a[206:207],v0 offset:37384\n"
MF "v[156:159],a[208:211],a[148:151],v[156:159]\n"
MF "v[148:151],a[208:211],a[144:147],v[148:151]\n"
"ds_read_b128 a[104:107],v3 offset:22528\n"
MF "v[148:151],a[212:215],a[184:187],v[148:151]\n"
MF "v[140:143],a[212:215],a[180:183],v[140:143]\n"
LD "v13,s[28:31],s82 offen offset:3072 lds\n"
MF "v[140:143],a[208:211],a[140:143],v[140:143]\n"
"s_waitcnt lgkmcnt(7)\n"
MF "v[176:179],a[216:219],a[140:143],v[176:179]\n"
TR "a[208:209],v1 offset:32768\n"
MF "v[176:179],a[220:223],a[180:183],v[176:179]\n"
TR "a[210:211],v1 offset:33280\n"
MF "v[184:187],a[220:223],a[184:187],v[184:187]\n"
"ds_read_b128 a[68:71],v2 offset:24576\n"
MF "v[184:187],a[216:219],a[144:147],v[184:187]\n"
"s_add_u32 s84,s68,21248\n"
TR "a[212:213],v1 offset:36864\n"
MF "v[192:195],a[216:219],a[148:151],v[192:195]\n"
"s_add_u32 m0,s63,2048\n"
TR "a[214:215],v1 offset:37376\n"
MF "v[192:195],a[220:223],a[188:191],v[192:195]\n"
LD "v4,s[24:27],s84 offen lds\n"
MF "v[200:203],a[220:223],a[192:195],v[200:203]\n"
"ds_read_b128 a[108:111],v3 offset:24576\n"
MF "v[200:203],a[216:219],a[152:155],v[200:203]\n"
MF "v[208:211],a[216:219],a[156:159],v[208:211]\n"
"s_add_u32 s84,s68,30464\n"
MF "v[208:211],a[220:223],a[196:199],v[208:211]\n"
"s_waitcnt lgkmcnt(7)\n"
LD "v5,s[24:27],s84 offen offset:1024 lds\n"
MF "v[212:215],a[204:207],a[196:199],v[212:215]\n"
TR "a[216:217],v1 offset:32776\n"
MF "v[212:215],a[200:203],a[156:159],v[212:215]\n"
TR "a[218:219],v1 offset:33288\n"
MF "v[204:207],a[200:203],a[152:155],v[204:207]\n"
"ds_read_b128 a[72:75],v2 offset:26624\n"
MF "v[204:207],a[204:207],a[192:195],v[204:207]\n"
TR "a[220:221],v1 offset:36872\n"
MF "v[196:199],a[204:207],a[188:191],v[196:199]\n"
TR "a[222:223],v1 offset:37384\n"
MF "v[196:199],a[200:203],a[148:151],v[196:199]\n"
MF "v[188:191],a[200:203],a[144:147],v[188:191]\n"
"v_add_u32 v0,0xffff4000,v0\n"
MF "v[188:191],a[204:207],a[184:187],v[188:191]\n"
"v_add_u32 v1,0xffff4000,v1\n"
MF "v[180:183],a[204:207],a[180:183],v[180:183]\n"
MF "v[180:183],a[200:203],a[140:143],v[180:183]\n"
"s_waitcnt vmcnt(25) lgkmcnt(0)\n"
"s_barrier\n"
MF "v[216:219],a[208:211],a[140:143],v[216:219]\n"
TR "a[200:201],v0\n"
MF "v[216:219],a[212:215],a[180:183],v[216:219]\n"
TR "a[202:203],v0 offset:512\n"
MF "v[224:227],a[212:215],a[184:187],v[224:227]\n"
"ds_read_b128 a[112:115],v3 offset:26624\n"
MF "v[224:227],a[208:211],a[144:147],v[224:227]\n"
"s_add_u32 m0,s62,81920\n"
TR "a[204:205],v0 offset:4096\n"
MF "v[232:235],a[208:211],a[148:151],v[232:235]\n"
"s_add_u32 s82,s66,3932416\n"
TR "a[206:207],v0 offset:4608\n"
MF "v[232:235],a[212:215],a[188:191],v[232:235]\n"
LD "v6,s[28:31],s82 offen lds\n"
MF "v[240:243],a[212:215],a[192:195],v[240:243]\n"
"ds_read_b128 a[76:79],v2 offset:28672\n"
MF "v[240:243],a[208:211],a[152:155],v[240:243]\n"
"ds_read_b128 a[116:119],v3 offset:28672\n"
MF "v[248:251],a[208:211],a[156:159],v[248:251]\n"
LD "v11,s[28:31],s82 offen offset:1024 lds\n"
MF "v[248:251],a[212:215],a[196:199],v[248:251]\n"
MF "v[252:255],a[220:223],a[196:199],v[252:255]\n"
TR "a[208:209],v0 offset:8\n"
MF "v[252:255],a[216:219],a[156:159],v[252:255]\n"
TR "a[210:211],v0 offset:520\n"
MF "v[244:247],a[216:219],a[152:155],v[244:247]\n"
MF "v[244:247],a[220:223],a[192:195],v[244:247]\n"
TR "a[212:213],v0 offset:4104\n"
MF "v[236:239],a[220:223],a[188:191],v[236:239]\n"
TR "a[214:215],v0 offset:4616\n"
MF "v[236:239],a[216:219],a[148:151],v[236:239]\n"
LD "v12,s[28:31],s82 offen offset:2048 lds\n"
MF "v[228:231],a[216:219],a[144:147],v[228:231]\n"
MF "v[228:231],a[220:223],a[184:187],v[228:231]\n"
MF "v[220:223],a[220:223],a[180:183],v[220:223]\n"
MF "v[220:223],a[216:219],a[140:143],v[220:223]\n"
"s_waitcnt lgkmcnt(4)\n"
MF "v[16:19],a[200:203],a[60:63],v[16:19]\n"
TR "a[216:217],v1\n"
MF "v[16:19],a[204:207],a[100:103],v[16:19]\n"
TR "a[218:219],v1 offset:512\n"
MF "v[24:27],a[204:207],a[104:107],v[24:27]\n"
LD "v13,s[28:31],s82 offen offset:3072 lds\n"
MF "v[24:27],a[200:203],a[64:67],v[24:27]\n"
TR "a[220:221],v1 offset:4096\n"
MF "v[32:35],a[200:203],a[68:71],v[32:35]\n"
TR "a[222:223],v1 offset:4608\n"
MF "v[32:35],a[204:207],a[108:111],v[32:35]\n"
MF "v[40:43],a[204:207],a[112:115],v[40:43]\n"
MF "v[40:43],a[200:203],a[72:75],v[40:43]\n"
"s_add_u32 s84,s68,41728\n"
MF "v[48:51],a[200:203],a[76:79],v[48:51]\n"
"s_add_u32 m0,s63,4096\n"
MF "v[48:51],a[204:207],a[116:119],v[48:51]\n"
"s_waitcnt lgkmcnt(4)\n"
LD "v4,s[24:27],s84 offen lds\n"
MF "v[52:55],a[212:215],a[116:119],v[52:55]\n"
TR "a[200:201],v1 offset:8\n"
MF "v[52:55],a[208:211],a[76:79],v[52:55]\n"
TR "a[202:203],v1 offset:520\n"
MF "v[44:47],a[208:211],a[72:75],v[44:47]\n"
MF "v[44:47],a[212:215],a[112:115],v[44:47]\n"
TR "a[204:205],v1 offset:4104\n"
MF "v[36:39],a[212:215],a[108:111],v[36:39]\n"
TR "a[206:207],v1 offset:4616\n"
MF "v[36:39],a[208:211],a[68:71],v[36:39]\n"
MF "v[28:31],a[208:211],a[64:67],v[28:31]\n"
MF "v[28:31],a[212:215],a[104:107],v[28:31]\n"
MF "v[20:23],a[212:215],a[100:103],v[20:23]\n"
MF "v[20:23],a[208:211],a[60:63],v[20:23]\n"
"s_waitcnt vmcnt(24) lgkmcnt(0)\n"
"s_barrier\n"
MF "v[56:59],a[216:219],a[60:63],v[56:59]\n"
"s_add_u32 m0,s62,0\n"
TR "a[208:209],v0 offset:16384\n"
MF "v[56:59],a[220:223],a[100:103],v[56:59]\n"
"s_add_u32 s82,s66,4718592\n"
TR "a[210:211],v0 offset:16896\n"
MF "v[64:67],a[220:223],a[104:107],v[64:67]\n"
LD "v6,s[28:31],s82 offen lds\n"
MF "v[64:67],a[216:219],a[64:67],v[64:67]\n"
TR "a[212:213],v0 offset:20480\n"
MF "v[72:75],a[216:219],a[68:71],v[72:75]\n"
TR "a[214:215],v0 offset:20992\n"
MF "v[72:75],a[220:223],a[108:111],v[72:75]\n"
MF "v[80:83],a[220:223],a[112:115],v[80:83]\n"
LD "v11,s[28:31],s82 offen offset:1024 lds\n"
MF "v[80:83],a[216:219],a[72:75],v[80:83]\n"
MF "v[88:91],a[216:219],a[76:79],v[88:91]\n"
MF "v[88:91],a[220:223],a[116:119],v[88:91]\n"
MF "v[92:95],a[204:207],a[116:119],v[92:95]\n"
TR "a[216:217],v0 offset:16392\n"
MF "v[92:95],a[200:203],a[76:79],v[92:95]\n"
TR "a[218:219],v0 offset:16904\n"
MF "v[84:87],a[200:203],a[72:75],v[84:87]\n"
LD "v12,s[28:31],s82 offen offset:2048 lds\n"
MF "v[84:87],a[204:207],a[112:115],v[84:87]\n"
TR "a[220:221],v0 offset:20488\n"
MF "v[76:79],a[204:207],a[108:111],v[76:79]\n"
TR "a[222:223],v0 offset:21000\n"
MF "v[76:79],a[200:203],a[68:71],v[76:79]\n"
MF "v[68:71],a[200:203],a[64:67],v[68:71]\n"
MF "v[68:71],a[204:207],a[104:107],v[68:71]\n"
MF "v[60:63],a[204:207],a[100:103],v[60:63]\n"
LD "v13,s[28:31],s82 offen offset:3072 lds\n"
MF "v[60:63],a[200:203],a[60:63],v[60:63]\n"
"s_waitcnt lgkmcnt(4)\n"
MF "v[96:99],a[208:211],a[60:63],v[96:99]\n"
TR "a[200:201],v1 offset:16384\n"
MF "v[96:99],a[212:215],a[100:103],v[96:99]\n"
TR "a[202:203],v1 offset:16896\n"
MF "v[104:107],a[212:215],a[104:107],v[104:107]\n"
MF "v[104:107],a[208:211],a[64:67],v[104:107]\n"
"s_add_u32 s84,s68,896\n"
TR "a[204:205],v1 offset:20480\n"
MF "v[112:115],a[208:211],a[68:71],v[112:115]\n"
"s_add_u32 m0,s63,20480\n"
TR "a[206:207],v1 offset:20992\n"
MF "v[112:115],a[212:215],a[108:111],v[112:115]\n"
LD "v4,s[24:27],s84 offen lds\n"
MF "v[120:123],a[212:215],a[112:115],v[120:123]\n"
MF "v[120:123],a[208:211],a[72:75],v[120:123]\n"
MF "v[128:131],a[208:211],a[76:79],v[128:131]\n"
"s_add_u32 s84,s68,10112\n"
MF "v[128:131],a[212:215],a[116:119],v[128:131]\n"
"s_waitcnt lgkmcnt(4)\n"
LD "v5,s[24:27],s84 offen offset:1024 lds\n"
MF "v[132:135],a[220:223],a[116:119],v[132:135]\n"
TR "a[208:209],v1 offset:16392\n"
MF "v[132:135],a[216:219],a[76:79],v[132:135]\n"
TR "a[210:211],v1 offset:16904\n"
MF "v[124:127],a[216:219],a[72:75],v[124:127]\n"
MF "v[124:127],a[220:223],a[112:115],v[124:127]\n"
TR "a[212:213],v1 offset:20488\n"
MF "v[116:119],a[220:223],a[108:111],v[116:119]\n"
TR "a[214:215],v1 offset:21000\n"
MF "v[116:119],a[216:219],a[68:71],v[116:119]\n"
MF "v[108:111],a[216:219],a[64:67],v[108:111]\n"
MF "v[108:111],a[220:223],a[104:107],v[108:111]\n"
MF "v[100:103],a[220:223],a[100:103],v[100:103]\n"
".Lqj:\n"
MF "v[100:103],a[216:219],a[60:63],v[100:103]\n"
"s_waitcnt vmcnt(23) lgkmcnt(0)\n"
"s_barrier\n"
MF "v[136:139],a[200:203],a[60:63],v[136:139]\n"
TR "a[216:217],v0 offset:32768\n"
MF "v[136:139],a[204:207],a[100:103],v[136:139]\n"
TR "a[218:219],v0 offset:33280\n"
MF "v[144:147],a[204:207],a[104:107],v[144:147]\n"
"ds_read_b128 a[140:143],v2 offset:40960\n"
MF "v[144:147],a[200:203],a[64:67],v[144:147]\n"
"s_add_u32 m0,s62,16384\n"
TR "a[220:221],v0 offset:36864\n"
MF "v[152:155],a[200:203],a[68:71],v[152:155]\n"
"s_add_u32 s82,s66,4718720\n"
TR "a[222:223],v0 offset:37376\n"
MF "v[152:155],a[204:207],a[108:111],v[152:155]\n"
LD "v6,s[28:31],s82 offen lds\n"
MF "v[160:163],a[204:207],a[112:115],v[160:163]\n"
"ds_read_b128 a[180:183],v3 offset:40960\n"
MF "v[160:163],a[200:203],a[72:75],v[160:163]\n"
LD "v11,s[28:31],s82 offen offset:1024 lds\n"
MF "v[168:171],a[200:203],a[76:79],v[168:171]\n"
MF "v[168:171],a[204:207],a[116:119],v[168:171]\n"
LD "v12,s[28:31],s82 offen offset:2048 lds\n"
MF "v[172:175],a[212:215],a[116:119],v[172:175]\n"
TR "a[200:201],v0 offset:32776\n"
MF "v[172:175],a[208:211],a[76:79],v[172:175]\n"
TR "a[202:203],v0 offset:33288\n"
MF "v[164:167],a[208:211],a[72:75],v[164:167]\n"
"ds_read_b128 a[144:147],v2 offset:43008\n"
MF "v[164:167],a[212:215],a[112:115],v[164:167]\n"
TR "a[204:205],v0 offset:36872\n"
MF "v[156:159],a[212:215],a[108:111],v[156:159]\n"
TR "a[206:207],v0 offset:37384\n"
MF "v[156:159],a[208:211],a[68:71],v[156:159]\n"
MF "v[148:151],a[208:211],a[64:67],v[148:151]\n"
"ds_read_b128 a[184:187],v3 offset:43008\n"
MF "v[148:151],a[212:215],a[104:107],v[148:151]\n"
MF "v[140:143],a[212:215],a[100:103],v[140:143]\n"
LD "v13,s[28:31],s82 offen offset:3072 lds\n"
MF "v[140:143],a[208:211],a[60:63],v[140:143]\n"
"s_waitcnt lgkmcnt(7)\n"
MF "v[176:179],a[216:219],a[60:63],v[176:179]\n"
TR "a[208:209],v1 offset:32768\n"
MF "v[176:179],a[220:223],a[100:103],v[176:179]\n"
TR "a[210:211],v1 offset:33280\n"
MF "v[184:187],a[220:223],a[104:107],v[184:187]\n"
"ds_read_b128 a[148:151],v2 offset:45056\n"
MF "v[184:187],a[216:219],a[64:67],v[184:187]\n"
"s_add_u32 s84,s68,21376\n"
TR "a[212:213],v1 offset:36864\n"
MF "v[192:195],a[216:219],a[68:71],v[192:195]\n"
"s_add_u32 m0,s63,22528\n"
TR "a[214:215],v1 offset:37376\n"
MF "v[192:195],a[220:223],a[108:111],v[192:195]\n"
LD "v4,s[24:27],s84 offen lds\n"
MF "v[200:203],a[220:223],a[112:115],v[200:203]\n"
"ds_read_b128 a[188:191],v3 offset:45056\n"
MF "v[200:203],a[216:219],a[72:75],v[200:203]\n"
MF "v[208:211],a[216:219],a[76:79],v[208:211]\n"
"s_add_u32 s84,s68,30592\n"
MF "v[208:211],a[220:223],a[116:119],v[208:211]\n"
"s_waitcnt lgkmcnt(7)\n"
LD "v5,s[24:27],s84 offen offset:1024 lds\n"
MF "v[212:215],a[204:207],a[116:119],v[212:215]\n"
TR "a[216:217],v1 offset:32776\n"
MF "v[212:215],a[200:203],a[76:79],v[212:215]\n"
TR "a[218:219],v1 offset:33288\n"
MF "v[204:207],a[200:203],a[72:75],v[204:207]\n"
"ds_read_b128 a[152:155],v2 offset:47104\n"
MF "v[204:207],a[204:207],a[112:115],v[204:207]\n"
TR "a[220:221],v1 offset:36872\n"
MF "v[196:199],a[204:207],a[108:111],v[196:199]\n"
TR "a[222:223],v1 offset:37384\n"
MF "v[196:199],a[200:203],a[68:71],v[196:199]\n"
MF "v[188:191],a[200:203],a[64:67],v[188:191]\n"
"v_add_u32 v0,0xc000,v0\n"
MF "v[188:191],a[204:207],a[104:107],v[188:191]\n"
"v_add_u32 v1,0xc000,v1\n"
MF "v[180:183],a[204:207],a[100:103],v[180:183]\n"
MF "v[180:183],a[200:203],a[60:63],v[180:183]\n"
"s_waitcnt vmcnt(25) lgkmcnt(0)\n"
"s_barrier\n"
MF "v[216:219],a[208:211],a[60:63],v[216:219]\n"
TR "a[200:201],v0\n"
MF "v[216:219],a[212:215],a[100:103],v[216:219]\n"
TR "a[202:203],v0 offset:512\n"
MF "v[224:227],a[212:215],a[104:107],v[224:227]\n"
"ds_read_b128 a[192:195],v3 offset:47104\n"
MF "v[224:227],a[208:211],a[64:67],v[224:227]\n"
"s_add_u32 m0,s62,32768\n"
TR "a[204:205],v0 offset:4096\n"
MF "v[232:235],a[208:211],a[68:71],v[232:235]\n"
"s_add_u32 s82,s66,4718848\n"
TR "a[206:207],v0 offset:4608\n"
MF "v[232:235],a[212:215],a[108:111],v[232:235]\n"
LD "v6,s[28:31],s82 offen lds\n"
MF "v[240:243],a[212:215],a[112:115],v[240:243]\n"
"ds_read_b128 a[156:159],v2 offset:49152\n"
MF "v[240:243],a[208:211],a[72:75],v[240:243]\n"
"ds_read_b128 a[196:199],v3 offset:49152\n"
MF "v[248:251],a[208:211],a[76:79],v[248:251]\n"
LD "v11,s[28:31],s82 offen offset:1024 lds\n"
MF "v[248:251],a[212:215],a[116:119],v[248:251]\n"
MF "v[252:255],a[220:223],a[116:119],v[252:255]\n"
TR "a[208:209],v0 offset:8\n"
MF "v[252:255],a[216:219],a[76:79],v[252:255]\n"
TR "a[210:211],v0 offset:520\n"
MF "v[244:247],a[216:219],a[72:75],v[244:247]\n"
MF "v[244:247],a[220:223],a[112:115],v[244:247]\n"
TR "a[212:213],v0 offset:4104\n"
MF "v[236:239],a[220:223],a[108:111],v[236:239]\n"
TR "a[214:215],v0 offset:4616\n"
MF "v[236:239],a[216:219],a[68:71],v[236:239]\n"
LD "v12,s[28:31],s82 offen offset:2048 lds\n"
MF "v[228:231],a[216:219],a[64:67],v[228:231]\n"
MF "v[228:231],a[220:223],a[104:107],v[228:231]\n"
MF "v[220:223],a[220:223],a[100:103],v[220:223]\n"
MF "v[220:223],a[216:219],a[60:63],v[220:223]\n"
"s_waitcnt lgkmcnt(4)\n"
MF "v[16:19],a[200:203],a[140:143],v[16:19]\n"
TR "a[216:217],v1\n"
MF "v[16:19],a[204:207],a[180:183],v[16:19]\n"
TR "a[218:219],v1 offset:512\n"
MF "v[24:27],a[204:207],a[184:187],v[24:27]\n"
LD "v13,s[28:31],s82 offen offset:3072 lds\n"
MF "v[24:27],a[200:203],a[144:147],v[24:27]\n"
TR "a[220:221],v1 offset:4096\n"
MF "v[32:35],a[200:203],a[148:151],v[32:35]\n"
TR "a[222:223],v1 offset:4608\n"
MF "v[32:35],a[204:207],a[188:191],v[32:35]\n"
MF "v[40:43],a[204:207],a[192:195],v[40:43]\n"
MF "v[40:43],a[200:203],a[152:155],v[40:43]\n"
"s_add_u32 s84,s68,41856\n"
MF "v[48:51],a[200:203],a[156:159],v[48:51]\n"
"s_add_u32 m0,s63,24576\n"
MF "v[48:51],a[204:207],a[196:199],v[48:51]\n"
"s_waitcnt lgkmcnt(4)\n"
LD "v4,s[24:27],s84 offen lds\n"
MF "v[52:55],a[212:215],a[196:199],v[52:55]\n"
TR "a[200:201],v1 offset:8\n"
MF "v[52:55],a[208:211],a[156:159],v[52:55]\n"
TR "a[202:203],v1 offset:520\n"
MF "v[44:47],a[208:211],a[152:155],v[44:47]\n"
MF "v[44:47],a[212:215],a[192:195],v[44:47]\n"
TR "a[204:205],v1 offset:4104\n"
MF "v[36:39],a[212:215],a[188:191],v[36:39]\n"
TR "a[206:207],v1 offset:4616\n"
MF "v[36:39],a[208:211],a[148:151],v[36:39]\n"
MF "v[28:31],a[208:211],a[144:147],v[28:31]\n"
MF "v[28:31],a[212:215],a[184:187],v[28:31]\n"
MF "v[20:23],a[212:215],a[180:183],v[20:23]\n"
MF "v[20:23],a[208:211],a[140:143],v[20:23]\n"
"s_waitcnt vmcnt(24) lgkmcnt(0)\n"
"s_barrier\n"
MF "v[56:59],a[216:219],a[140:143],v[56:59]\n"
"s_add_u32 m0,s62,49152\n"
TR "a[208:209],v0 offset:16384\n"
MF "v[56:59],a[220:223],a[180:183],v[56:59]\n"
"s_add_u32 s82,s66,5505024\n"
TR "a[210:211],v0 offset:16896\n"
MF "v[64:67],a[220:223],a[184:187],v[64:67]\n"
LD "v6,s[28:31],s82 offen lds\n"
MF "v[64:67],a[216:219],a[144:147],v[64:67]\n"
TR "a[212:213],v0 offset:20480\n"
MF "v[72:75],a[216:219],a[148:151],v[72:75]\n"
TR "a[214:215],v0 offset:20992\n"
MF "v[72:75],a[220:223],a[188:191],v[72:75]\n"
MF "v[80:83],a[220:223],a[192:195],v[80:83]\n"
LD "v11,s[28:31],s82 offen offset:1024 lds\n"
MF "v[80:83],a[216:219],a[152:155],v[80:83]\n"
MF "v[88:91],a[216:219],a[156:159],v[88:91]\n"
MF "v[88:91],a[220:223],a[196:199],v[88:91]\n"
MF "v[92:95],a[204:207],a[196:199],v[92:95]\n"
TR "a[216:217],v0 offset:16392\n"
MF "v[92:95],a[200:203],a[156:159],v[92:95]\n"
TR "a[218:219],v0 offset:16904\n"
MF "v[84:87],a[200:203],a[152:155],v[84:87]\n"
LD "v12,s[28:31],s82 offen offset:2048 lds\n"
MF "v[84:87],a[204:207],a[192:195],v[84:87]\n"
TR "a[220:221],v0 offset:20488\n"
MF "v[76:79],a[204:207],a[188:191],v[76:79]\n"
TR "a[222:223],v0 offset:21000\n"
MF "v[76:79],a[200:203],a[148:151],v[76:79]\n"
MF "v[68:71],a[200:203],a[144:147],v[68:71]\n"
MF "v[68:71],a[204:207],a[184:187],v[68:71]\n"
MF "v[60:63],a[204:207],a[180:183],v[60:63]\n"
LD "v13,s[28:31],s82 offen offset:3072 lds\n"
MF "v[60:63],a[200:203],a[140:143],v[60:63]\n"
"s_waitcnt lgkmcnt(4)\n"
MF "v[96:99],a[208:211],a[140:143],v[96:99]\n"
TR "a[200:201],v1 offset:16384\n"
MF "v[96:99],a[212:215],a[180:183],v[96:99]\n"
TR "a[202:203],v1 offset:16896\n"
MF "v[104:107],a[212:215],a[184:187],v[104:107]\n"
MF "v[104:107],a[208:211],a[144:147],v[104:107]\n"
"s_add_u32 s84,s68,1024\n"
TR "a[204:205],v1 offset:20480\n"
MF "v[112:115],a[208:211],a[148:151],v[112:115]\n"
"s_add_u32 m0,s63,40960\n"
TR "a[206:207],v1 offset:20992\n"
MF "v[112:115],a[212:215],a[188:191],v[112:115]\n"
LD "v4,s[24:27],s84 offen lds\n"
MF "v[120:123],a[212:215],a[192:195],v[120:123]\n"
MF "v[120:123],a[208:211],a[152:155],v[120:123]\n"
MF "v[128:131],a[208:211],a[156:159],v[128:131]\n"
"s_add_u32 s84,s68,10240\n"
MF "v[128:131],a[212:215],a[196:199],v[128:131]\n"
"s_waitcnt lgkmcnt(4)\n"
LD "v5,s[24:27],s84 offen offset:1024 lds\n"
MF "v[132:135],a[220:223],a[196:199],v[132:135]\n"
TR "a[208:209],v1 offset:16392\n"
MF "v[132:135],a[216:219],a[156:159],v[132:135]\n"
TR "a[210:211],v1 offset:16904\n"
MF "v[124:127],a[216:219],a[152:155],v[124:127]\n"
MF "v[124:127],a[220:223],a[192:195],v[124:127]\n"
TR "a[212:213],v1 offset:20488\n"
MF "v[116:119],a[220:223],a[188:191],v[116:119]\n"
TR "a[214:215],v1 offset:21000\n"
MF "v[116:119],a[216:219],a[148:151],v[116:119]\n"
MF "v[108:111],a[216:219],a[144:147],v[108:111]\n"
MF "v[108:111],a[220:223],a[184:187],v[108:111]\n"
MF "v[100:103],a[220:223],a[180:183],v[100:103]\n"
MF "v[100:103],a[216:219],a[140:143],v[100:103]\n"
"s_waitcnt vmcnt(23) lgkmcnt(0)\n"
"s_barrier\n"
"s_cmp_eq_u32 s57,1\n"
"s_cbranch_scc0 .Lhs12\n"
"v_bfe_u32 v14,v9,4,5\n"
"v_lshlrev_b32 v14,6,v14\n"
"s_lshl_b32 s58,s78,2\n"
"s_add_u32 s58,s58,s86\n"
"buffer_load_dword v10,v14,s[40:43],s58 offen offset:128 sc1\n"
".Lhs12:\n"
MF "v[136:139],a[200:203],a[140:143],v[136:139]\n"
TR "a[216:217],v0 offset:32768\n"
MF "v[136:139],a[204:207],a[180:183],v[136:139]\n"
TR "a[218:219],v0 offset:33280\n"
MF "v[144:147],a[204:207],a[184:187],v[144:147]\n"
"ds_read_b128 a[60:63],v2\n"
MF "v[144:147],a[200:203],a[144:147],v[144:147]\n"
"s_add_u32 m0,s62,65536\n"
TR "a[220:221],v0 offset:36864\n"
MF "v[152:155],a[200:203],a[148:151],v[152:155]\n"
"s_add_u32 s82,s66,5505152\n"
TR "a[222:223],v0 offset:37376\n"
MF "v[152:155],a[204:207],a[188:191],v[152:155]\n"
LD "v6,s[28:31],s82 offen lds\n"
MF "v[160:163],a[204:207],a[192:195],v[160:163]\n"
"ds_read_b128 a[100:103],v3\n"
MF "v[160:163],a[200:203],a[152:155],v[160:163]\n"
LD "v11,s[28:31],s82 offen offset:1024 lds\n"
MF "v[168:171],a[200:203],a[156:159],v[168:171]\n"
MF "v[168:171],a[204:207],a[196:199],v[168:171]\n"
LD "v12,s[28:31],s82 offen offset:2048 lds\n"
MF "v[172:175],a[212:215],a[196:199],v[172:175]\n"
TR "a[200:201],v0 offset:32776\n"
MF "v[172:175],a[208:211],a[156:159],v[172:175]\n"
TR "a[202:203],v0 offset:33288\n"
MF "v[164:167],a[208:211],a[152:155],v[164:167]\n"
"ds_read_b128 a[64:67],v2 offset:2048\n"
MF "v[164:167],a[212:215],a[192:195],v[164:167]\n"
TR "a[204:205],v0 offset:36872\n"
MF "v[156:159],a[212:215],a[188:191],v[156:159]\n"
TR "a[206:207],v0 offset:37384\n"
MF "v[156:159],a[208:211],a[148:151],v[156:159]\n"
MF "v[148:151],a[208:211],a[144:147],v[148:151]\n"
"ds_read_b128 a[104:107],v3 offset:2048\n"
MF "v[148:151],a[212:215],a[184:187],v[148:151]\n"
MF "v[140:143],a[212:215],a[180:183],v[140:143]\n"
LD "v13,s[28:31],s82 offen offset:3072 lds\n"
MF "v[140:143],a[208:211],a[140:143],v[140:143]\n"
"s_waitcnt lgkmcnt(7)\n"
MF "v[176:179],a[216:219],a[140:143],v[176:179]\n"
TR "a[208:209],v1 offset:32768\n"
MF "v[176:179],a[220:223],a[180:183],v[176:179]\n"
TR "a[210:211],v1 offset:33280\n"
MF "v[184:187],a[220:223],a[184:187],v[184:187]\n"
"ds_read_b128 a[68:71],v2 offset:4096\n"
MF "v[184:187],a[216:219],a[144:147],v[184:187]\n"
"s_add_u32 s84,s68,21504\n"
TR "a[212:213],v1 offset:36864\n"
MF "v[192:195],a[216:219],a[148:151],v[192:195]\n"
"s_add_u32 m0,s63,43008\n"
TR "a[214:215],v1 offset:37376\n"
MF "v[192:195],a[220:223],a[188:191],v[192:195]\n"
LD "v4,s[24:27],s84 offen lds\n"
MF "v[200:203],a[220:223],a[192:195],v[200:203]\n"
"ds_read_b128 a[108:111],v3 offset:4096\n"
MF "v[200:203],a[216:219],a[152:155],v[200:203]\n"
MF "v[208:211],a[216:219],a[156:159],v[208:211]\n"
"s_add_u32 s84,s68,30720\n"
MF "v[208:211],a[220:223],a[196:199],v[208:211]\n"
"s_waitcnt lgkmcnt(7)\n"
LD "v5,s[24:27],s84 offen offset:1024 lds\n"
MF "v[212:215],a[204:207],a[196:199],v[212:215]\n"
TR "a[216:217],v1 offset:32776\n"
MF "v[212:215],a[200:203],a[156:159],v[212:215]\n"
TR "a[218:219],v1 offset:33288\n"
MF "v[204:207],a[200:203],a[152:155],v[204:207]\n"
"ds_read_b128 a[72:75],v2 offset:6144\n"
MF "v[204:207],a[204:207],a[192:195],v[204:207]\n"
TR "a[220:221],v1 offset:36872\n"
MF "v[196:199],a[204:207],a[188:191],v[196:199]\n"
TR "a[222:223],v1 offset:37384\n"
MF "v[196:199],a[200:203],a[148:151],v[196:199]\n"
MF "v[188:191],a[200:203],a[144:147],v[188:191]\n"
"v_add_u32 v0,0xffff4000,v0\n"
MF "v[188:191],a[204:207],a[184:187],v[188:191]\n"
"v_add_u32 v1,0xffff4000,v1\n"
MF "v[180:183],a[204:207],a[180:183],v[180:183]\n"
MF "v[180:183],a[200:203],a[140:143],v[180:183]\n"
"s_waitcnt vmcnt(25) lgkmcnt(0)\n"
"s_barrier\n"
MF "v[216:219],a[208:211],a[140:143],v[216:219]\n"
TR "a[200:201],v0\n"
MF "v[216:219],a[212:215],a[180:183],v[216:219]\n"
TR "a[202:203],v0 offset:512\n"
MF "v[224:227],a[212:215],a[184:187],v[224:227]\n"
"ds_read_b128 a[112:115],v3 offset:6144\n"
MF "v[224:227],a[208:211],a[144:147],v[224:227]\n"
"s_add_u32 m0,s62,81920\n"
TR "a[204:205],v0 offset:4096\n"
MF "v[232:235],a[208:211],a[148:151],v[232:235]\n"
"s_add_u32 s82,s66,5505280\n"
TR "a[206:207],v0 offset:4608\n"
MF "v[232:235],a[212:215],a[188:191],v[232:235]\n"
LD "v6,s[28:31],s82 offen lds\n"
MF "v[240:243],a[212:215],a[192:195],v[240:243]\n"
"ds_read_b128 a[76:79],v2 offset:8192\n"
MF "v[240:243],a[208:211],a[152:155],v[240:243]\n"
"ds_read_b128 a[116:119],v3 offset:8192\n"
MF "v[248:251],a[208:211],a[156:159],v[248:251]\n"
LD "v11,s[28:31],s82 offen offset:1024 lds\n"
MF "v[248:251],a[212:215],a[196:199],v[248:251]\n"
MF "v[252:255],a[220:223],a[196:199],v[252:255]\n"
TR "a[208:209],v0 offset:8\n"
MF "v[252:255],a[216:219],a[156:159],v[252:255]\n"
TR "a[210:211],v0 offset:520\n"
MF "v[244:247],a[216:219],a[152:155],v[244:247]\n"
MF "v[244:247],a[220:223],a[192:195],v[244:247]\n"
TR "a[212:213],v0 offset:4104\n"
MF "v[236:239],a[220:223],a[188:191],v[236:239]\n"
TR "a[214:215],v0 offset:4616\n"
MF "v[236:239],a[216:219],a[148:151],v[236:239]\n"
LD "v12,s[28:31],s82 offen offset:2048 lds\n"
MF "v[228:231],a[216:219],a[144:147],v[228:231]\n"
MF "v[228:231],a[220:223],a[184:187],v[228:231]\n"
MF "v[220:223],a[220:223],a[180:183],v[220:223]\n"
MF "v[220:223],a[216:219],a[140:143],v[220:223]\n"
"s_waitcnt lgkmcnt(4)\n"
MF "v[16:19],a[200:203],a[60:63],v[16:19]\n"
TR "a[216:217],v1\n"
MF "v[16:19],a[204:207],a[100:103],v[16:19]\n"
TR "a[218:219],v1 offset:512\n"
MF "v[24:27],a[204:207],a[104:107],v[24:27]\n"
LD "v13,s[28:31],s82 offen offset:3072 lds\n"
MF "v[24:27],a[200:203],a[64:67],v[24:27]\n"
TR "a[220:221],v1 offset:4096\n"
MF "v[32:35],a[200:203],a[68:71],v[32:35]\n"
TR "a[222:223],v1 offset:4608\n"
MF "v[32:35],a[204:207],a[108:111],v[32:35]\n"
MF "v[40:43],a[204:207],a[112:115],v[40:43]\n"
MF "v[40:43],a[200:203],a[72:75],v[40:43]\n"
"s_add_u32 s84,s68,41984\n"
MF "v[48:51],a[200:203],a[76:79],v[48:51]\n"
"s_add_u32 m0,s63,45056\n"
MF "v[48:51],a[204:207],a[116:119],v[48:51]\n"
"s_waitcnt lgkmcnt(4)\n"
LD "v4,s[24:27],s84 offen lds\n"
MF "v[52:55],a[212:215],a[116:119],v[52:55]\n"
TR "a[200:201],v1 offset:8\n"
MF "v[52:55],a[208:211],a[76:79],v[52:55]\n"
TR "a[202:203],v1 offset:520\n"
MF "v[44:47],a[208:211],a[72:75],v[44:47]\n"
MF "v[44:47],a[212:215],a[112:115],v[44:47]\n"
TR "a[204:205],v1 offset:4104\n"
MF "v[36:39],a[212:215],a[108:111],v[36:39]\n"
TR "a[206:207],v1 offset:4616\n"
MF "v[36:39],a[208:211],a[68:71],v[36:39]\n"
MF "v[28:31],a[208:211],a[64:67],v[28:31]\n"
MF "v[28:31],a[212:215],a[104:107],v[28:31]\n"
MF "v[20:23],a[212:215],a[100:103],v[20:23]\n"
MF "v[20:23],a[208:211],a[60:63],v[20:23]\n"
"s_waitcnt vmcnt(24) lgkmcnt(0)\n"
"s_barrier\n"
MF "v[56:59],a[216:219],a[60:63],v[56:59]\n"
"s_add_u32 m0,s62,0\n"
TR "a[208:209],v0 offset:16384\n"
MF "v[56:59],a[220:223],a[100:103],v[56:59]\n"
"s_add_u32 s82,s66,6291712\n"
TR "a[210:211],v0 offset:16896\n"
MF "v[64:67],a[220:223],a[104:107],v[64:67]\n"
LD "v6,s[28:31],s82 offen lds\n"
MF "v[64:67],a[216:219],a[64:67],v[64:67]\n"
TR "a[212:213],v0 offset:20480\n"
MF "v[72:75],a[216:219],a[68:71],v[72:75]\n"
TR "a[214:215],v0 offset:20992\n"
MF "v[72:75],a[220:223],a[108:111],v[72:75]\n"
MF "v[80:83],a[220:223],a[112:115],v[80:83]\n"
LD "v11,s[28:31],s82 offen offset:1024 lds\n"
MF "v[80:83],a[216:219],a[72:75],v[80:83]\n"
MF "v[88:91],a[216:219],a[76:79],v[88:91]\n"
MF "v[88:91],a[220:223],a[116:119],v[88:91]\n"
MF "v[92:95],a[204:207],a[116:119],v[92:95]\n"
TR "a[216:217],v0 offset:16392\n"
MF "v[92:95],a[200:203],a[76:79],v[92:95]\n"
TR "a[218:219],v0 offset:16904\n"
MF "v[84:87],a[200:203],a[72:75],v[84:87]\n"
LD "v12,s[28:31],s82 offen offset:2048 lds\n"
MF "v[84:87],a[204:207],a[112:115],v[84:87]\n"
TR "a[220:221],v0 offset:20488\n"
MF "v[76:79],a[204:207],a[108:111],v[76:79]\n"
TR "a[222:223],v0 offset:21000\n"
MF "v[76:79],a[200:203],a[68:71],v[76:79]\n"
MF "v[68:71],a[200:203],a[64:67],v[68:71]\n"
LD "v13,s[28:31],s82 offen offset:3072 lds\n"
MF "v[68:71],a[204:207],a[104:107],v[68:71]\n"
MF "v[60:63],a[204:207],a[100:103],v[60:63]\n"
MF "v[60:63],a[200:203],a[60:63],v[60:63]\n"
"s_waitcnt lgkmcnt(4)\n"
MF "v[96:99],a[208:211],a[60:63],v[96:99]\n"
"s_add_u32 s84,s68,1152\n"
TR "a[200:201],v1 offset:16384\n"
MF "v[96:99],a[212:215],a[100:103],v[96:99]\n"
"s_add_u32 m0,s63,0\n"
TR "a[202:203],v1 offset:16896\n"
MF "v[104:107],a[212:215],a[104:107],v[104:107]\n"
LD "v4,s[24:27],s84 offen lds\n"
MF "v[104:107],a[208:211],a[64:67],v[104:107]\n"
TR "a[204:205],v1 offset:20480\n"
MF "v[112:115],a[208:211],a[68:71],v[112:115]\n"
TR "a[206:207],v1 offset:20992\n"
MF "v[112:115],a[212:215],a[108:111],v[112:115]\n"
"s_add_u32 s84,s68,10368\n"
MF "v[120:123],a[212:215],a[112:115],v[120:123]\n"
LD "v5,s[24:27],s84 offen offset:1024 lds\n"
MF "v[120:123],a[208:211],a[72:75],v[120:123]\n"
MF "v[128:131],a[208:211],a[76:79],v[128:131]\n"
MF "v[128:131],a[212:215],a[116:119],v[128:131]\n"
"s_waitcnt lgkmcnt(4)\n"
MF "v[132:135],a[220:223],a[116:119],v[132:135]\n"
TR "a[208:209],v1 offset:16392\n"
MF "v[132:135],a[216:219],a[76:79],v[132:135]\n"
"s_add_u32 s84,s68,19584\n"
TR "a[210:211],v1 offset:16904\n"
MF "v[124:127],a[216:219],a[72:75],v[124:127]\n"
LD "v4,s[24:27],s84 offen offset:2048 lds\n"
MF "v[124:127],a[220:223],a[112:115],v[124:127]\n"
TR "a[212:213],v1 offset:20488\n"
MF "v[116:119],a[220:223],a[108:111],v[116:119]\n"
TR "a[214:215],v1 offset:21000\n"
MF "v[116:119],a[216:219],a[68:71],v[116:119]\n"
MF "v[108:111],a[216:219],a[64:67],v[108:111]\n"
MF "v[108:111],a[220:223],a[104:107],v[108:111]\n"
MF "v[100:103],a[220:223],a[100:103],v[100:103]\n"
MF "v[100:103],a[216:219],a[60:63],v[100:103]\n"
"s_waitcnt vmcnt(24) lgkmcnt(0)\n"
"s_barrier\n"
MF "v[136:139],a[200:203],a[60:63],v[136:139]\n"
TR "a[216:217],v0 offset:32768\n"
MF "v[136:139],a[204:207],a[100:103],v[136:139]\n"
TR "a[218:219],v0 offset:33280\n"
MF "v[144:147],a[204:207],a[104:107],v[144:147]\n"
"ds_read_b128 a[140:143],v2 offset:20480\n"
MF "v[144:147],a[200:203],a[64:67],v[144:147]\n"
"s_add_u32 m0,s62,16384\n"
TR "a[220:221],v0 offset:36864\n"
MF "v[152:155],a[200:203],a[68:71],v[152:155]\n"
"s_add_u32 s82,s66,7078144\n"
TR "a[222:223],v0 offset:37376\n"
MF "v[152:155],a[204:207],a[108:111],v[152:155]\n"
LD "v6,s[28:31],s82 offen lds\n"
MF "v[160:163],a[204:207],a[112:115],v[160:163]\n"
"ds_read_b128 a[180:183],v3 offset:20480\n"
MF "v[160:163],a[200:203],a[72:75],v[160:163]\n"
LD "v11,s[28:31],s82 offen offset:1024 lds\n"
MF "v[168:171],a[200:203],a[76:79],v[168:171]\n"
MF "v[168:171],a[204:207],a[116:119],v[168:171]\n"
LD "v12,s[28:31],s82 offen offset:2048 lds\n"
MF "v[172:175],a[212:215],a[116:119],v[172:175]\n"
TR "a[200:201],v0 offset:32776\n"
MF "v[172:175],a[208:211],a[76:79],v[172:175]\n"
TR "a[202:203],v0 offset:33288\n"
MF "v[164:167],a[208:211],a[72:75],v[164:167]\n"
"ds_read_b128 a[144:147],v2 offset:22528\n"
MF "v[164:167],a[212:215],a[112:115],v[164:167]\n"
TR "a[204:205],v0 offset:36872\n"
MF "v[156:159],a[212:215],a[108:111],v[156:159]\n"
TR "a[206:207],v0 offset:37384\n"
MF "v[156:159],a[208:211],a[68:71],v[156:159]\n"
MF "v[148:151],a[208:211],a[64:67],v[148:151]\n"
"ds_read_b128 a[184:187],v3 offset:22528\n"
MF "v[148:151],a[212:215],a[104:107],v[148:151]\n"
MF "v[140:143],a[212:215],a[100:103],v[140:143]\n"
LD "v13,s[28:31],s82 offen offset:3072 lds\n"
MF "v[140:143],a[208:211],a[60:63],v[140:143]\n"
"s_waitcnt lgkmcnt(7)\n"
MF "v[176:179],a[216:219],a[60:63],v[176:179]\n"
TR "a[208:209],v1 offset:32768\n"
MF "v[176:179],a[220:223],a[100:103],v[176:179]\n"
TR "a[210:211],v1 offset:33280\n"
MF "v[184:187],a[220:223],a[104:107],v[184:187]\n"
"ds_read_b128 a[148:151],v2 offset:24576\n"
MF "v[184:187],a[216:219],a[64:67],v[184:187]\n"
"s_add_u32 s84,s68,31872\n"
TR "a[212:213],v1 offset:36864\n"
MF "v[192:195],a[216:219],a[68:71],v[192:195]\n"
"s_add_u32 m0,s63,3072\n"
TR "a[214:215],v1 offset:37376\n"
MF "v[192:195],a[220:223],a[108:111],v[192:195]\n"
LD "v5,s[24:27],s84 offen lds\n"
MF "v[200:203],a[220:223],a[112:115],v[200:203]\n"
"ds_read_b128 a[188:191],v3 offset:24576\n"
MF "v[200:203],a[216:219],a[72:75],v[200:203]\n"
MF "v[208:211],a[216:219],a[76:79],v[208:211]\n"
"s_add_u32 s84,s68,41088\n"
MF "v[208:211],a[220:223],a[116:119],v[208:211]\n"
"s_waitcnt lgkmcnt(7)\n"
LD "v4,s[24:27],s84 offen offset:1024 lds\n"
MF "v[212:215],a[204:207],a[116:119],v[212:215]\n"
TR "a[216:217],v1 offset:32776\n"
MF "v[212:215],a[200:203],a[76:79],v[212:215]\n"
TR "a[218:219],v1 offset:33288\n"
MF "v[204:207],a[200:203],a[72:75],v[204:207]\n"
"ds_read_b128 a[152:155],v2 offset:26624\n"
MF "v[204:207],a[204:207],a[112:115],v[204:207]\n"
TR "a[220:221],v1 offset:36872\n"
MF "v[196:199],a[204:207],a[108:111],v[196:199]\n"
TR "a[222:223],v1 offset:37384\n"
MF "v[196:199],a[200:203],a[68:71],v[196:199]\n"
MF "v[188:191],a[200:203],a[64:67],v[188:191]\n"
"v_add_u32 v0,0xc000,v0\n"
MF "v[188:191],a[204:207],a[104:107],v[188:191]\n"
"v_add_u32 v1,0xc000,v1\n"
MF "v[180:183],a[204:207],a[100:103],v[180:183]\n"
MF "v[180:183],a[200:203],a[60:63],v[180:183]\n"
"s_waitcnt vmcnt(26) lgkmcnt(0)\n"
"s_barrier\n"
"s_getpc_b64 s[52:53]\n"
".Lctq:\n"
"s_sub_u32 s52,s52,.Lctq-.Lcode0\n"
"s_subb_u32 s53,s53,0\n"
"s_bfe_u32 s58,s23,0x20000\n"
"s_lshl_b32 s58,s58,2\n"
"s_add_u32 s58,s58,s56\n"
"s_lshl_b32 s58,s58,13\n"
"v_lshlrev_b32 v15,3,v9\n"
"v_add_u32 v15,s58,v15\n"
"v_min_u32 v15,.Lcode1-.Lcode0,v15\n"
"global_load_dword a249,v15,s[52:53]\n"
MF "v[216:219],a[208:211],a[60:63],v[216:219]\n"
TR "a[200:201],v0\n"
MF "v[216:219],a[212:215],a[100:103],v[216:219]\n"
TR "a[202:203],v0 offset:512\n"
MF "v[224:227],a[212:215],a[104:107],v[224:227]\n"
"ds_read_b128 a[192:195],v3 offset:26624\n"
MF "v[224:227],a[208:211],a[64:67],v[224:227]\n"
"s_add_u32 m0,s62,32768\n"
TR "a[204:205],v0 offset:4096\n"
MF "v[232:235],a[208:211],a[68:71],v[232:235]\n"
"s_add_u32 s82,s66,6291584\n"
TR "a[206:207],v0 offset:4608\n"
MF "v[232:235],a[212:215],a[108:111],v[232:235]\n"
LD "v6,s[28:31],s82 offen lds\n"
MF "v[240:243],a[212:215],a[112:115],v[240:243]\n"
"ds_read_b128 a[156:159],v2 offset:28672\n"
MF "v[240:243],a[208:211],a[72:75],v[240:243]\n"
"ds_read_b128 a[196:199],v3 offset:28672\n"
MF "v[248:251],a[208:211],a[76:79],v[248:251]\n"
MF "v[248:251],a[212:215],a[116:119],v[248:251]\n"
LD "v11,s[28:31],s82 offen offset:1024 lds\n"
MF "v[252:255],a[220:223],a[116:119],v[252:255]\n"
TR "a[208:209],v0 offset:8\n"
MF "v[252:255],a[216:219],a[76:79],v[252:255]\n"
TR "a[210:211],v0 offset:520\n"
MF "v[244:247],a[216:219],a[72:75],v[244:247]\n"
MF "v[244:247],a[220:223],a[112:115],v[244:247]\n"
TR "a[212:213],v0 offset:4104\n"
MF "v[236:239],a[220:223],a[108:111],v[236:239]\n"
TR "a[214:215],v0 offset:4616\n"
MF "v[236:239],a[216:219],a[68:71],v[236:239]\n"
MF "v[228:231],a[216:219],a[64:67],v[228:231]\n"
MF "v[228:231],a[220:223],a[104:107],v[228:231]\n"
MF "v[220:223],a[220:223],a[100:103],v[220:223]\n"
LD "v12,s[28:31],s82 offen offset:2048 lds\n"
MF "v[220:223],a[216:219],a[60:63],v[220:223]\n"
"s_waitcnt lgkmcnt(4)\n"
MF "v[16:19],a[200:203],a[140:143],v[16:19]\n"
TR "a[216:217],v1\n"
MF "v[16:19],a[204:207],a[180:183],v[16:19]\n"
TR "a[218:219],v1 offset:512\n"
MF "v[24:27],a[204:207],a[184:187],v[24:27]\n"
MF "v[24:27],a[200:203],a[144:147],v[24:27]\n"
TR "a[220:221],v1 offset:4096\n"
MF "v[32:35],a[200:203],a[148:151],v[32:35]\n"
TR "a[222:223],v1 offset:4608\n"
MF "v[32:35],a[204:207],a[188:191],v[32:35]\n"
MF "v[40:43],a[204:207],a[192:195],v[40:43]\n"
MF "v[40:43],a[200:203],a[152:155],v[40:43]\n"
LD "v13,s[28:31],s82 offen offset:3072 lds\n"
MF "v[48:51],a[200:203],a[156:159],v[48:51]\n"
MF "v[48:51],a[204:207],a[196:199],v[48:51]\n"
"s_waitcnt lgkmcnt(4)\n"
MF "v[52:55],a[212:215],a[196:199],v[52:55]\n"
TR "a[200:201],v1 offset:8\n"
MF "v[52:55],a[208:211],a[156:159],v[52:55]\n"
TR "a[202:203],v1 offset:520\n"
MF "v[44:47],a[208:211],a[152:155],v[44:47]\n"
MF "v[44:47],a[212:215],a[192:195],v[44:47]\n"
TR "a[204:205],v1 offset:4104\n"
MF "v[36:39],a[212:215],a[188:191],v[36:39]\n"
TR "a[206:207],v1 offset:4616\n"
MF "v[36:39],a[208:211],a[148:151],v[36:39]\n"
MF "v[28:31],a[208:211],a[144:147],v[28:31]\n"
MF "v[28:31],a[212:215],a[184:187],v[28:31]\n"
MF "v[20:23],a[212:215],a[180:183],v[20:23]\n"
MF "v[20:23],a[208:211],a[140:143],v[20:23]\n"
"s_waitcnt vmcnt(25) lgkmcnt(0)\n"
"s_barrier\n"
MF "v[56:59],a[216:219],a[140:143],v[56:59]\n"
"s_add_u32 m0,s62,49152\n"
TR "a[208:209],v0 offset:16384\n"
MF "v[56:59],a[220:223],a[180:183],v[56:59]\n"
"s_add_u32 s82,s66,7078016\n"
TR "a[210:211],v0 offset:16896\n"
"v_lshrrev_b32 v15,8,v9\n"
MF "v[64:67],a[220:223],a[184:187],v[64:67]\n"
LD "v6,s[28:31],s82 offen lds\n"
MF "v[64:67],a[216:219],a[144:147],v[64:67]\n"
TR "a[212:213],v0 offset:20480\n"
"v_mul_u32_u24 v15,48960,v15\n"
MF "v[72:75],a[216:219],a[148:151],v[72:75]\n"
"s_lshr_b32 s7,s56,1\n"
TR "a[214:215],v0 offset:20992\n"
"v_add_u32 v15,v15,v9\n"
MF "v[72:75],a[220:223],a[188:191],v[72:75]\n"
"s_and_b32 s8,s56,1\n"
MF "v[80:83],a[220:223],a[192:195],v[80:83]\n"
"s_sub_u32 s7,s7,s8\n"
MF "v[80:83],a[216:219],a[152:155],v[80:83]\n"
"s_mul_i32 s7,s7,245760\n"
MF "v[88:91],a[216:219],a[156:159],v[88:91]\n"
"s_add_u32 s7,s7,s89\n"
MF "v[88:91],a[220:223],a[196:199],v[88:91]\n"
LD "v11,s[28:31],s82 offen offset:1024 lds\n"
MF "v[92:95],a[204:207],a[196:199],v[92:95]\n"
TR "a[216:217],v0 offset:16392\n"
MF "v[92:95],a[200:203],a[156:159],v[92:95]\n"
TR "a[218:219],v0 offset:16904\n"
MF "v[84:87],a[200:203],a[152:155],v[84:87]\n"
MF "v[84:87],a[204:207],a[192:195],v[84:87]\n"
TR "a[220:221],v0 offset:20488\n"
MF "v[76:79],a[204:207],a[188:191],v[76:79]\n"
TR "a[222:223],v0 offset:21000\n"
MF "v[76:79],a[200:203],a[148:151],v[76:79]\n"
MF "v[68:71],a[200:203],a[144:147],v[68:71]\n"
MF "v[68:71],a[204:207],a[184:187],v[68:71]\n"
MF "v[60:63],a[204:207],a[180:183],v[60:63]\n"
LD "v12,s[28:31],s82 offen offset:2048 lds\n"
MF "v[60:63],a[200:203],a[140:143],v[60:63]\n"
"s_waitcnt lgkmcnt(4)\n"
MF "v[96:99],a[208:211],a[140:143],v[96:99]\n"
TR "a[200:201],v1 offset:16384\n"
MF "v[96:99],a[212:215],a[180:183],v[96:99]\n"
TR "a[202:203],v1 offset:16896\n"
MF "v[104:107],a[212:215],a[184:187],v[104:107]\n"
MF "v[104:107],a[208:211],a[144:147],v[104:107]\n"
TR "a[204:205],v1 offset:20480\n"
MF "v[112:115],a[208:211],a[148:151],v[112:115]\n"
TR "a[206:207],v1 offset:20992\n"
MF "v[112:115],a[212:215],a[188:191],v[112:115]\n"
MF "v[120:123],a[212:215],a[192:195],v[120:123]\n"
MF "v[120:123],a[208:211],a[152:155],v[120:123]\n"
LD "v13,s[28:31],s82 offen offset:3072 lds\n"
MF "v[128:131],a[208:211],a[156:159],v[128:131]\n"
MF "v[128:131],a[212:215],a[196:199],v[128:131]\n"
"s_waitcnt lgkmcnt(4)\n"
MF "v[132:135],a[220:223],a[196:199],v[132:135]\n"
TR "a[208:209],v1 offset:16392\n"
MF "v[132:135],a[216:219],a[156:159],v[132:135]\n"
TR "a[210:211],v1 offset:16904\n"
MF "v[124:127],a[216:219],a[152:155],v[124:127]\n"
MF "v[124:127],a[220:223],a[192:195],v[124:127]\n"
TR "a[212:213],v1 offset:20488\n"
MF "v[116:119],a[220:223],a[188:191],v[116:119]\n"
TR "a[214:215],v1 offset:21000\n"
MF "v[116:119],a[216:219],a[148:151],v[116:119]\n"
MF "v[108:111],a[216:219],a[144:147],v[108:111]\n"
MF "v[108:111],a[220:223],a[184:187],v[108:111]\n"
MF "v[100:103],a[220:223],a[180:183],v[100:103]\n"
MF "v[100:103],a[216:219],a[140:143],v[100:103]\n"
"s_waitcnt vmcnt(22) lgkmcnt(0)\n"
"s_barrier\n"
"s_cmp_eq_u32 s57,1\n"
"s_cbranch_scc0 .Lhs15\n"
".Lack_q:\n"
"v_cmp_ne_u32 vcc,s76,v10\n"
"s_cbranch_vccnz .Lackspin_q\n"
".Lackok_q:\n"
".Lhs15:\n"
MF "v[136:139],a[200:203],a[140:143],v[136:139]\n"
"s_and_b32 s58,s56,1\n"
TR "a[216:217],v0 offset:32768\n"
MF "v[136:139],a[204:207],a[180:183],v[136:139]\n"
"s_mul_i32 s59,s58,3072\n"
TR "a[218:219],v0 offset:33280\n"
MF "v[144:147],a[204:207],a[184:187],v[144:147]\n"
"s_add_u32 s16,s70,s59\n"
"ds_read_b128 a[60:63],v2 offset:40960\n"
MF "v[144:147],a[200:203],a[144:147],v[144:147]\n"
"s_add_u32 m0,s62,65536\n"
TR "a[220:221],v0 offset:36864\n"
MF "v[152:155],a[200:203],a[148:151],v[152:155]\n"
"s_add_u32 s82,s66,6291456\n"
TR "a[222:223],v0 offset:37376\n"
MF "v[152:155],a[204:207],a[188:191],v[152:155]\n"
LD "v6,s[28:31],s82 offen lds\n"
MF "v[160:163],a[204:207],a[192:195],v[160:163]\n"
"s_mul_i32 s59,s58,6144\n"
"ds_read_b128 a[100:103],v3 offset:40960\n"
MF "v[160:163],a[200:203],a[152:155],v[160:163]\n"
"s_add_u32 s18,s70,s59\n"
MF "v[168:171],a[200:203],a[156:159],v[168:171]\n"
"s_add_u32 s17,s16,196608\n"
MF "v[168:171],a[204:207],a[196:199],v[168:171]\n"
LD "v11,s[28:31],s82 offen offset:1024 lds\n"
MF "v[172:175],a[212:215],a[196:199],v[172:175]\n"
"s_add_u32 s19,s18,196608\n"
TR "a[200:201],v0 offset:32776\n"
MF "v[172:175],a[208:211],a[156:159],v[172:175]\n"
"s_add_u32 s20,s70,196608\n"
TR "a[202:203],v0 offset:33288\n"
MF "v[164:167],a[208:211],a[152:155],v[164:167]\n"
"s_bfe_u32 s58,s23,0x40008\n"
"ds_read_b128 a[64:67],v2 offset:43008\n"
MF "v[164:167],a[212:215],a[192:195],v[164:167]\n"
"s_lshr_b32 s59,s56,1\n"
TR "a[204:205],v0 offset:36872\n"
MF "v[156:159],a[212:215],a[188:191],v[156:159]\n"
"s_mul_i32 s59,s59,5\n"
TR "a[206:207],v0 offset:37384\n"
MF "v[156:159],a[208:211],a[148:151],v[156:159]\n"
"s_add_u32 s58,s58,s59\n"
MF "v[148:151],a[208:211],a[144:147],v[148:151]\n"
"s_and_b32 s58,s58,15\n"
"ds_read_b128 a[104:107],v3 offset:43008\n"
MF "v[148:151],a[212:215],a[184:187],v[148:151]\n"
"s_mul_i32 s58,s58,12288\n"
MF "v[140:143],a[212:215],a[180:183],v[140:143]\n"
LD "v12,s[28:31],s82 offen offset:2048 lds\n"
MF "v[140:143],a[208:211],a[140:143],v[140:143]\n"
"s_waitcnt lgkmcnt(7)\n"
MF "v[176:179],a[216:219],a[140:143],v[176:179]\n"
"s_add_u32 s72,s16,s58\n"
TR "a[208:209],v1 offset:32768\n"
MF "v[176:179],a[220:223],a[180:183],v[176:179]\n"
"s_add_u32 s73,s18,s58\n"
TR "a[210:211],v1 offset:33280\n"
MF "v[184:187],a[220:223],a[184:187],v[184:187]\n"
"s_add_u32 s74,s70,s58\n"
"ds_read_b128 a[68:71],v2 offset:45056\n"
MF "v[184:187],a[216:219],a[144:147],v[184:187]\n"
"s_add_u32 s74,s74,12288\n"
TR "a[212:213],v1 offset:36864\n"
MF "v[192:195],a[216:219],a[148:151],v[192:195]\n"
"s_cmp_eq_u32 s74,s20\n"
"s_cselect_b32 s74,s70,s74\n"
TR "a[214:215],v1 offset:37376\n"
MF "v[192:195],a[220:223],a[188:191],v[192:195]\n"
"s_lshr_b32 s58,s56,1\n"
MF "v[200:203],a[220:223],a[192:195],v[200:203]\n"
"s_mul_i32 s58,s58,6144\n"
"ds_read_b128 a[108:111],v3 offset:45056\n"
MF "v[200:203],a[216:219],a[152:155],v[200:203]\n"
LD "v13,s[28:31],s82 offen offset:3072 lds\n"
MF "v[208:211],a[216:219],a[156:159],v[208:211]\n"
"s_mul_i32 s64,s56,3072\n"
MF "v[208:211],a[220:223],a[196:199],v[208:211]\n"
"s_waitcnt lgkmcnt(7)\n"
MF "v[212:215],a[204:207],a[196:199],v[212:215]\n"
"s_add_u32 s64,s64,s58\n"
TR "a[216:217],v1 offset:32776\n"
MF "v[212:215],a[200:203],a[156:159],v[212:215]\n"
"s_mul_i32 s65,s56,6144\n"
TR "a[218:219],v1 offset:33288\n"
MF "v[204:207],a[200:203],a[152:155],v[204:207]\n"
"s_sub_u32 s58,s71,s70\n"
"ds_read_b128 a[72:75],v2 offset:47104\n"
MF "v[204:207],a[204:207],a[192:195],v[204:207]\n"
"s_add_u32 s96,s74,s58\n"
TR "a[220:221],v1 offset:36872\n"
MF "v[196:199],a[204:207],a[188:191],v[196:199]\n"
"s_sub_u32 s58,s16,s70\n"
TR "a[222:223],v1 offset:37384\n"
MF "v[196:199],a[200:203],a[148:151],v[196:199]\n"
"s_add_u32 s96,s96,s58\n"
MF "v[188:191],a[200:203],a[144:147],v[188:191]\n"
"s_max_u32 s60,s70,s71\n"
"v_add_u32 v0,0xffff4000,v0\n"
MF "v[188:191],a[204:207],a[184:187],v[188:191]\n"
"s_add_u32 s34,s60,196608\n"
"v_add_u32 v1,0xffff4000,v1\n"
MF "v[180:183],a[204:207],a[180:183],v[180:183]\n"
"s_add_u32 s38,s60,196608\n"
MF "v[180:183],a[200:203],a[140:143],v[180:183]\n"
"s_waitcnt vmcnt(13) lgkmcnt(0)\n"
"s_barrier\n"
MF "v[216:219],a[208:211],a[140:143],v[216:219]\n"
TR "a[200:201],v1 offset:8\n"
MF "v[216:219],a[212:215],a[180:183],v[216:219]\n"
TR "a[202:203],v1 offset:520\n"
"v_mov_b32 v14,s12\n"
MF "v[224:227],a[212:215],a[184:187],v[224:227]\n"
"ds_read_b128 a[112:115],v3 offset:47104\n"
"v_mul_f32 v14,0xbdd53b95,v14\n"
MF "v[224:227],a[208:211],a[144:147],v[224:227]\n"
"s_add_u32 m0,s62,81920\n"
TR "a[204:205],v1 offset:4104\n"
MF "v[232:235],a[208:211],a[148:151],v[232:235]\n"
"s_add_u32 s82,s66,7077888\n"
TR "a[206:207],v1 offset:4616\n"
MF "v[232:235],a[212:215],a[188:191],v[232:235]\n"
LD "v6,s[28:31],s82 offen lds\n"
MF "v[240:243],a[212:215],a[192:195],v[240:243]\n"
"ds_read_b128 a[76:79],v2 offset:49152\n"
MF "v[240:243],a[208:211],a[152:155],v[240:243]\n"
"ds_read_b128 a[116:119],v3 offset:49152\n"
MF "v[248:251],a[208:211],a[156:159],v[248:251]\n"
MF "v[248:251],a[212:215],a[196:199],v[248:251]\n"
LD "v11,s[28:31],s82 offen offset:1024 lds\n"
MF "v[220:223],a[216:219],a[140:143],v[220:223]\n"
TR "a[208:209],v1 offset:16392\n"
MF "v[220:223],a[220:223],a[180:183],v[220:223]\n"
TR "a[210:211],v1 offset:16904\n"
MF "v[228:231],a[220:223],a[184:187],v[228:231]\n"
"ds_read_b128 a[140:143],v2\n"
MF "v[228:231],a[216:219],a[144:147],v[228:231]\n"
TR "a[212:213],v1 offset:20488\n"
MF "v[236:239],a[216:219],a[148:151],v[236:239]\n"
TR "a[214:215],v1 offset:21000\n"
MF "v[236:239],a[220:223],a[188:191],v[236:239]\n"
"ds_read_b128 a[180:183],v3\n"
MF "v[244:247],a[220:223],a[192:195],v[244:247]\n"
"ds_read_b128 a[144:147],v2 offset:2048\n"
MF "v[244:247],a[216:219],a[152:155],v[244:247]\n"
"ds_read_b128 a[184:187],v3 offset:2048\n"
MF "v[252:255],a[216:219],a[156:159],v[252:255]\n"
"ds_read_b128 a[148:151],v2 offset:4096\n"
MF "v[252:255],a[220:223],a[196:199],v[252:255]\n"
"s_waitcnt lgkmcnt(9)\n"
"ds_read_b128 a[188:191],v3 offset:4096\n"
MF "v[220:223],a[200:203],a[60:63],v[220:223]\n"
TR "a[216:217],v1\n"
MF "v[220:223],a[204:207],a[100:103],v[220:223]\n"
TR "a[218:219],v1 offset:512\n"
MF "v[228:231],a[204:207],a[104:107],v[228:231]\n"
"ds_read_b128 a[152:155],v2 offset:6144\n"
MF "v[228:231],a[200:203],a[64:67],v[228:231]\n"
TR "a[220:221],v1 offset:4096\n"
MF "v[236:239],a[200:203],a[68:71],v[236:239]\n"
TR "a[222:223],v1 offset:4608\n"
MF "v[236:239],a[204:207],a[108:111],v[236:239]\n"
"ds_read_b128 a[192:195],v3 offset:6144\n"
MF "v[244:247],a[204:207],a[112:115],v[244:247]\n"
"ds_read_b128 a[156:159],v2 offset:8192\n"
MF "v[244:247],a[200:203],a[72:75],v[244:247]\n"
"ds_read_b128 a[196:199],v3 offset:8192\n"
MF "v[252:255],a[200:203],a[76:79],v[252:255]\n"
LD "v12,s[28:31],s82 offen offset:2048 lds\n"
MF "v[252:255],a[204:207],a[116:119],v[252:255]\n"
"s_waitcnt lgkmcnt(8)\n"
LD "v13,s[28:31],s82 offen offset:3072 lds\n"
MF "v[220:223],a[208:211],a[140:143],v[220:223]\n"
TR "a[200:201],v1 offset:16384\n"
MF "v[220:223],a[212:215],a[180:183],v[220:223]\n"
TR "a[202:203],v1 offset:16896\n"
MF "v[228:231],a[212:215],a[184:187],v[228:231]\n"
MF "v[228:231],a[208:211],a[144:147],v[228:231]\n"
TR "a[204:205],v1 offset:20480\n"
MF "v[236:239],a[208:211],a[148:151],v[236:239]\n"
TR "a[206:207],v1 offset:20992\n"
MF "v[236:239],a[212:215],a[188:191],v[236:239]\n"
"s_waitcnt lgkmcnt(4)\n"
MF "v[244:247],a[212:215],a[192:195],v[244:247]\n"
MF "v[244:247],a[208:211],a[152:155],v[244:247]\n"
MF "v[252:255],a[208:211],a[156:159],v[252:255]\n"
MF "v[252:255],a[212:215],a[196:199],v[252:255]\n"
"s_waitcnt lgkmcnt(0)\n"
"s_cmp_eq_u32 s69,98\n"
"s_cbranch_scc1 .Lqy\n"
MF "v[216:219],a[216:219],a[60:63],v[216:219]\n"
TR "a[208:209],v0 offset:8\n"
MF "v[216:219],a[220:223],a[100:103],v[216:219]\n"
TR "a[210:211],v0 offset:520\n"
MF "v[224:227],a[220:223],a[104:107],v[224:227]\n"
"buffer_load_dword a224,v15,s[36:39],s70 offen offset:2688 nt\n"
MF "v[224:227],a[216:219],a[64:67],v[224:227]\n"
TR "a[212:213],v0 offset:4104\n"
MF "v[232:235],a[216:219],a[68:71],v[232:235]\n"
TR "a[214:215],v0 offset:4616\n"
MF "v[232:235],a[220:223],a[108:111],v[232:235]\n"
"buffer_load_dword a225,v15,s[36:39],s71 offen offset:2688 nt\n"
MF "v[240:243],a[220:223],a[112:115],v[240:243]\n"
MF "v[240:243],a[216:219],a[72:75],v[240:243]\n"
"buffer_load_dword a226,v15,s[48:51],s7 offen offset:2688\n"
MF "v[248:251],a[216:219],a[76:79],v[248:251]\n"
MF "v[248:251],a[220:223],a[116:119],v[248:251]\n"
MF "v[248:251],a[200:203],a[156:159],v[248:251]\n"
TR "a[216:217],v0 offset:16392\n"
MF "v[248:251],a[204:207],a[196:199],v[248:251]\n"
TR "a[218:219],v0 offset:16904\n"
MF "v[240:243],a[204:207],a[192:195],v[240:243]\n"
MF "v[240:243],a[200:203],a[152:155],v[240:243]\n"
TR "a[220:221],v0 offset:20488\n"
MF "v[232:235],a[200:203],a[148:151],v[232:235]\n"
TR "a[222:223],v0 offset:21000\n"
MF "v[232:235],a[204:207],a[188:191],v[232:235]\n"
MF "v[224:227],a[204:207],a[184:187],v[224:227]\n"
MF "v[224:227],a[200:203],a[144:147],v[224:227]\n"
"v_fma_f32 v248,v248,s54,v14\n"
"v_fma_f32 v249,v249,s54,v14\n"
MF "v[216:219],a[200:203],a[140:143],v[216:219]\n"
"v_fma_f32 v250,v250,s54,v14\n"
"v_fma_f32 v251,v251,s54,v14\n"
MF "v[216:219],a[204:207],a[180:183],v[216:219]\n"
"s_waitcnt lgkmcnt(4)\n"
"v_fma_f32 v252,v252,s54,v14\n"
"v_fma_f32 v253,v253,s54,v14\n"
MF "v[180:183],a[208:211],a[60:63],v[180:183]\n"
TR "a[200:201],v0\n"
"v_fma_f32 v254,v254,s54,v14\n"
MF "v[180:183],a[212:215],a[100:103],v[180:183]\n"
TR "a[202:203],v0 offset:512\n"
"v_fma_f32 v255,v255,s54,v14\n"
MF "v[188:191],a[212:215],a[104:107],v[188:191]\n"
CV "v255,v254,v255\n"
CV "v254,v252,v253\n"
MF "v[188:191],a[208:211],a[64:67],v[188:191]\n"
TR "a[204:205],v0 offset:4096\n"
CV "v253,v250,v251\n"
MF "v[196:199],a[208:211],a[68:71],v[196:199]\n"
TR "a[206:207],v0 offset:4608\n"
CV "v252,v248,v249\n"
MF "v[196:199],a[212:215],a[108:111],v[196:199]\n"
"v_fma_f32 v240,v240,s54,v14\n"
"v_fma_f32 v241,v241,s54,v14\n"
MF "v[204:207],a[212:215],a[112:115],v[204:207]\n"
"v_fma_f32 v242,v242,s54,v14\n"
"v_fma_f32 v243,v243,s54,v14\n"
MF "v[204:207],a[208:211],a[72:75],v[204:207]\n"
"v_fma_f32 v244,v244,s54,v14\n"
"v_fma_f32 v245,v245,s54,v14\n"
MF "v[212:215],a[208:211],a[76:79],v[212:215]\n"
"v_fma_f32 v246,v246,s54,v14\n"
"v_fma_f32 v247,v247,s54,v14\n"
MF "v[212:215],a[212:215],a[116:119],v[212:215]\n"
"s_waitcnt lgkmcnt(4)\n"
CV "v251,v246,v247\n"
CV "v250,v244,v245\n"
MF "v[180:183],a[216:219],a[140:143],v[180:183]\n"
TR "a[208:209],v0 offset:16384\n"
CV "v249,v242,v243\n"
MF "v[180:183],a[220:223],a[180:183],v[180:183]\n"
TR "a[210:211],v0 offset:16896\n"
CV "v248,v240,v241\n"
MF "v[188:191],a[220:223],a[184:187],v[188:191]\n"
"v_fma_f32 v232,v232,s54,v14\n"
"v_fma_f32 v233,v233,s54,v14\n"
MF "v[188:191],a[216:219],a[144:147],v[188:191]\n"
TR "a[212:213],v0 offset:20480\n"
"v_fma_f32 v234,v234,s54,v14\n"
MF "v[196:199],a[216:219],a[148:151],v[196:199]\n"
TR "a[214:215],v0 offset:20992\n"
"v_fma_f32 v235,v235,s54,v14\n"
MF "v[196:199],a[220:223],a[188:191],v[196:199]\n"
"v_fma_f32 v236,v236,s54,v14\n"
"v_fma_f32 v237,v237,s54,v14\n"
MF "v[204:207],a[220:223],a[192:195],v[204:207]\n"
"v_fma_f32 v238,v238,s54,v14\n"
"v_fma_f32 v239,v239,s54,v14\n"
MF "v[204:207],a[216:219],a[152:155],v[204:207]\n"
CV "v247,v238,v239\n"
CV "v246,v236,v237\n"
MF "v[212:215],a[216:219],a[156:159],v[212:215]\n"
CV "v245,v234,v235\n"
CV "v244,v232,v233\n"
MF "v[212:215],a[220:223],a[196:199],v[212:215]\n"
"s_waitcnt vmcnt(11) lgkmcnt(0)\n"
"s_barrier\n"
MF "v[176:179],a[200:203],a[60:63],v[176:179]\n"
TR "a[216:217],v1 offset:32776\n"
"v_fma_f32 v224,v224,s54,v14\n"
MF "v[176:179],a[204:207],a[100:103],v[176:179]\n"
"s_add_u32 m0,s64,0\n"
TR "a[218:219],v1 offset:33288\n"
"v_fma_f32 v225,v225,s54,v14\n"
MF "v[184:187],a[204:207],a[104:107],v[184:187]\n"
LD "v7,s[32:35],s72 offen nt lds\n"
MF "v[184:187],a[200:203],a[64:67],v[184:187]\n"
TR "a[220:221],v1 offset:36872\n"
"v_fma_f32 v226,v226,s54,v14\n"
MF "v[192:195],a[200:203],a[68:71],v[192:195]\n"
TR "a[222:223],v1 offset:37384\n"
"v_fma_f32 v227,v227,s54,v14\n"
MF "v[192:195],a[204:207],a[108:111],v[192:195]\n"
"s_add_u32 m0,s64,896\n"
"v_fma_f32 v228,v228,s54,v14\n"
"v_fma_f32 v229,v229,s54,v14\n"
MF "v[200:203],a[204:207],a[112:115],v[200:203]\n"
LD "v7,s[32:35],s72 offen offset:128 nt lds\n"
MF "v[200:203],a[200:203],a[72:75],v[200:203]\n"
"v_fma_f32 v230,v230,s54,v14\n"
"v_fma_f32 v231,v231,s54,v14\n"
MF "v[208:211],a[200:203],a[76:79],v[208:211]\n"
CV "v243,v230,v231\n"
CV "v242,v228,v229\n"
MF "v[208:211],a[204:207],a[116:119],v[208:211]\n"
CV "v241,v226,v227\n"
CV "v240,v224,v225\n"
MF "v[208:211],a[208:211],a[156:159],v[208:211]\n"
TR "a[200:201],v1 offset:49160\n"
"v_fma_f32 v216,v216,s54,v14\n"
MF "v[208:211],a[212:215],a[196:199],v[208:211]\n"
"s_add_u32 m0,s64,1792\n"
TR "a[202:203],v1 offset:49672\n"
"v_fma_f32 v217,v217,s54,v14\n"
MF "v[200:203],a[212:215],a[192:195],v[200:203]\n"
LD "v7,s[32:35],s72 offen offset:256 nt lds\n"
MF "v[200:203],a[208:211],a[152:155],v[200:203]\n"
TR "a[204:205],v1 offset:53256\n"
"v_fma_f32 v218,v218,s54,v14\n"
MF "v[192:195],a[208:211],a[148:151],v[192:195]\n"
TR "a[206:207],v1 offset:53768\n"
"v_fma_f32 v219,v219,s54,v14\n"
MF "v[192:195],a[212:215],a[188:191],v[192:195]\n"
"v_fma_f32 v220,v220,s54,v14\n"
"v_fma_f32 v221,v221,s54,v14\n"
MF "v[184:187],a[212:215],a[184:187],v[184:187]\n"
"s_add_u32 s72,s72,6144\n"
"v_fma_f32 v222,v222,s54,v14\n"
"v_fma_f32 v223,v223,s54,v14\n"
MF "v[184:187],a[208:211],a[144:147],v[184:187]\n"
"s_add_u32 m0,s64,6144\n"
CV "v239,v222,v223\n"
CV "v238,v220,v221\n"
MF "v[176:179],a[208:211],a[140:143],v[176:179]\n"
LD "v7,s[32:35],s72 offen nt lds\n"
MF "v[176:179],a[212:215],a[180:183],v[176:179]\n"
"s_waitcnt lgkmcnt(4)\n"
CV "v237,v218,v219\n"
CV "v236,v216,v217\n"
MF "v[140:143],a[216:219],a[60:63],v[140:143]\n"
TR "a[208:209],v1 offset:32768\n"
"v_fma_f32 v208,v208,s54,v14\n"
MF "v[140:143],a[220:223],a[100:103],v[140:143]\n"
TR "a[210:211],v1 offset:33280\n"
"v_fma_f32 v209,v209,s54,v14\n"
MF "v[148:151],a[220:223],a[104:107],v[148:151]\n"
"v_fma_f32 v210,v210,s54,v14\n"
"v_fma_f32 v211,v211,s54,v14\n"
MF "v[148:151],a[216:219],a[64:67],v[148:151]\n"
TR "a[212:213],v1 offset:36864\n"
"v_fma_f32 v212,v212,s54,v14\n"
MF "v[156:159],a[216:219],a[68:71],v[156:159]\n"
"s_add_u32 m0,s64,7040\n"
TR "a[214:215],v1 offset:37376\n"
"v_fma_f32 v213,v213,s54,v14\n"
MF "v[156:159],a[220:223],a[108:111],v[156:159]\n"
LD "v7,s[32:35],s72 offen offset:128 nt lds\n"
MF "v[164:167],a[220:223],a[112:115],v[164:167]\n"
"v_fma_f32 v214,v214,s54,v14\n"
"v_fma_f32 v215,v215,s54,v14\n"
MF "v[164:167],a[216:219],a[72:75],v[164:167]\n"
CV "v235,v214,v215\n"
CV "v234,v212,v213\n"
MF "v[172:175],a[216:219],a[76:79],v[172:175]\n"
"s_add_u32 m0,s64,7936\n"
CV "v233,v210,v211\n"
CV "v232,v208,v209\n"
MF "v[172:175],a[220:223],a[116:119],v[172:175]\n"
"s_waitcnt lgkmcnt(4)\n"
LD "v7,s[32:35],s72 offen offset:256 nt lds\n"
MF "v[140:143],a[200:203],a[140:143],v[140:143]\n"
TR "a[216:217],v1 offset:49152\n"
"v_fma_f32 v200,v200,s54,v14\n"
MF "v[140:143],a[204:207],a[180:183],v[140:143]\n"
TR "a[218:219],v1 offset:49664\n"
"v_fma_f32 v201,v201,s54,v14\n"
MF "v[148:151],a[204:207],a[184:187],v[148:151]\n"
"v_fma_f32 v202,v202,s54,v14\n"
"v_fma_f32 v203,v203,s54,v14\n"
MF "v[148:151],a[200:203],a[144:147],v[148:151]\n"
TR "a[220:221],v1 offset:53248\n"
"v_fma_f32 v204,v204,s54,v14\n"
MF "v[156:159],a[200:203],a[148:151],v[156:159]\n"
TR "a[222:223],v1 offset:53760\n"
"v_fma_f32 v205,v205,s54,v14\n"
MF "v[156:159],a[204:207],a[188:191],v[156:159]\n"
"v_fma_f32 v206,v206,s54,v14\n"
"v_fma_f32 v207,v207,s54,v14\n"
MF "v[164:167],a[204:207],a[192:195],v[164:167]\n"
CV "v231,v206,v207\n"
CV "v230,v204,v205\n"
MF "v[164:167],a[200:203],a[152:155],v[164:167]\n"
CV "v229,v202,v203\n"
CV "v228,v200,v201\n"
MF "v[172:175],a[200:203],a[156:159],v[172:175]\n"
"v_fma_f32 v192,v192,s54,v14\n"
"v_fma_f32 v193,v193,s54,v14\n"
MF "v[172:175],a[204:207],a[196:199],v[172:175]\n"
"s_waitcnt lgkmcnt(0)\n"
"v_fma_f32 v194,v194,s54,v14\n"
"v_fma_f32 v195,v195,s54,v14\n"
MF "v[136:139],a[208:211],a[60:63],v[136:139]\n"
TR "a[200:201],v0 offset:32776\n"
"v_fma_f32 v196,v196,s54,v14\n"
MF "v[136:139],a[212:215],a[100:103],v[136:139]\n"
TR "a[202:203],v0 offset:33288\n"
"v_fma_f32 v197,v197,s54,v14\n"
MF "v[144:147],a[212:215],a[104:107],v[144:147]\n"
"v_fma_f32 v198,v198,s54,v14\n"
"v_fma_f32 v199,v199,s54,v14\n"
MF "v[144:147],a[208:211],a[64:67],v[144:147]\n"
TR "a[204:205],v0 offset:36872\n"
CV "v227,v198,v199\n"
MF "v[152:155],a[208:211],a[68:71],v[152:155]\n"
TR "a[206:207],v0 offset:37384\n"
CV "v226,v196,v197\n"
MF "v[152:155],a[212:215],a[108:111],v[152:155]\n"
CV "v225,v194,v195\n"
CV "v224,v192,v193\n"
MF "v[160:163],a[212:215],a[112:115],v[160:163]\n"
"v_fma_f32 v184,v184,s54,v14\n"
"v_fma_f32 v185,v185,s54,v14\n"
MF "v[160:163],a[208:211],a[72:75],v[160:163]\n"
"v_fma_f32 v186,v186,s54,v14\n"
"v_fma_f32 v187,v187,s54,v14\n"
MF "v[168:171],a[208:211],a[76:79],v[168:171]\n"
"v_fma_f32 v188,v188,s54,v14\n"
"v_fma_f32 v189,v189,s54,v14\n"
MF "v[168:171],a[212:215],a[116:119],v[168:171]\n"
"v_fma_f32 v190,v190,s54,v14\n"
"v_fma_f32 v191,v191,s54,v14\n"
MF "v[168:171],a[216:219],a[156:159],v[168:171]\n"
TR "a[208:209],v0 offset:49160\n"
CV "v223,v190,v191\n"
MF "v[168:171],a[220:223],a[196:199],v[168:171]\n"
TR "a[210:211],v0 offset:49672\n"
CV "v222,v188,v189\n"
MF "v[160:163],a[220:223],a[192:195],v[160:163]\n"
CV "v221,v186,v187\n"
CV "v220,v184,v185\n"
MF "v[160:163],a[216:219],a[152:155],v[160:163]\n"
TR "a[212:213],v0 offset:53256\n"
"v_fma_f32 v176,v176,s54,v14\n"
MF "v[152:155],a[216:219],a[148:151],v[152:155]\n"
TR "a[214:215],v0 offset:53768\n"
"v_fma_f32 v177,v177,s54,v14\n"
MF "v[152:155],a[220:223],a[188:191],v[152:155]\n"
"v_fma_f32 v178,v178,s54,v14\n"
"v_fma_f32 v179,v179,s54,v14\n"
MF "v[144:147],a[220:223],a[184:187],v[144:147]\n"
"v_fma_f32 v180,v180,s54,v14\n"
"v_fma_f32 v181,v181,s54,v14\n"
MF "v[144:147],a[216:219],a[144:147],v[144:147]\n"
"v_fma_f32 v182,v182,s54,v14\n"
"v_fma_f32 v183,v183,s54,v14\n"
MF "v[136:139],a[216:219],a[140:143],v[136:139]\n"
CV "v219,v182,v183\n"
CV "v218,v180,v181\n"
MF "v[136:139],a[220:223],a[180:183],v[136:139]\n"
"s_waitcnt lgkmcnt(4)\n"
CV "v217,v178,v179\n"
CV "v216,v176,v177\n"
MF "v[100:103],a[200:203],a[60:63],v[100:103]\n"
TR "a[216:217],v0 offset:32768\n"
"v_fma_f32 v168,v168,s54,v14\n"
MF "v[100:103],a[204:207],a[100:103],v[100:103]\n"
TR "a[218:219],v0 offset:33280\n"
"v_fma_f32 v169,v169,s54,v14\n"
MF "v[108:111],a[204:207],a[104:107],v[108:111]\n"
"v_fma_f32 v170,v170,s54,v14\n"
"v_fma_f32 v171,v171,s54,v14\n"
MF "v[108:111],a[200:203],a[64:67],v[108:111]\n"
TR "a[220:221],v0 offset:36864\n"
"v_fma_f32 v172,v172,s54,v14\n"
MF "v[116:119],a[200:203],a[68:71],v[116:119]\n"
TR "a[222:223],v0 offset:37376\n"
"v_fma_f32 v173,v173,s54,v14\n"
MF "v[116:119],a[204:207],a[108:111],v[116:119]\n"
"v_fma_f32 v174,v174,s54,v14\n"
"v_fma_f32 v175,v175,s54,v14\n"
MF "v[124:127],a[204:207],a[112:115],v[124:127]\n"
CV "v215,v174,v175\n"
CV "v214,v172,v173\n"
MF "v[124:127],a[200:203],a[72:75],v[124:127]\n"
CV "v213,v170,v171\n"
CV "v212,v168,v169\n"
MF "v[132:135],a[200:203],a[76:79],v[132:135]\n"
"v_fma_f32 v160,v160,s54,v14\n"
"v_fma_f32 v161,v161,s54,v14\n"
MF "v[132:135],a[204:207],a[116:119],v[132:135]\n"
"s_waitcnt lgkmcnt(4)\n"
"v_fma_f32 v162,v162,s54,v14\n"
"v_fma_f32 v163,v163,s54,v14\n"
MF "v[100:103],a[208:211],a[140:143],v[100:103]\n"
TR "a[200:201],v0 offset:49152\n"
"v_fma_f32 v164,v164,s54,v14\n"
MF "v[100:103],a[212:215],a[180:183],v[100:103]\n"
TR "a[202:203],v0 offset:49664\n"
"v_fma_f32 v165,v165,s54,v14\n"
MF "v[108:111],a[212:215],a[184:187],v[108:111]\n"
"v_fma_f32 v166,v166,s54,v14\n"
"v_fma_f32 v167,v167,s54,v14\n"
MF "v[108:111],a[208:211],a[144:147],v[108:111]\n"
TR "a[204:205],v0 offset:53248\n"
CV "v211,v166,v167\n"
MF "v[116:119],a[208:211],a[148:151],v[116:119]\n"
TR "a[206:207],v0 offset:53760\n"
CV "v210,v164,v165\n"
MF "v[116:119],a[212:215],a[188:191],v[116:119]\n"
CV "v209,v162,v163\n"
CV "v208,v160,v161\n"
MF "v[124:127],a[212:215],a[192:195],v[124:127]\n"
"v_add_u32 v0,0xc000,v0\n"
"v_fma_f32 v152,v152,s54,v14\n"
MF "v[124:127],a[208:211],a[152:155],v[124:127]\n"
"v_add_u32 v1,0xc000,v1\n"
"v_fma_f32 v153,v153,s54,v14\n"
MF "v[132:135],a[208:211],a[156:159],v[132:135]\n"
"v_fma_f32 v154,v154,s54,v14\n"
"v_fma_f32 v155,v155,s54,v14\n"
MF "v[132:135],a[212:215],a[196:199],v[132:135]\n"
"s_waitcnt vmcnt(9) lgkmcnt(0)\n"
"s_barrier\n"
MF "v[96:99],a[216:219],a[60:63],v[96:99]\n"
TR "a[208:209],v1 offset:16392\n"
"v_fma_f32 v156,v156,s54,v14\n"
MF "v[96:99],a[220:223],a[100:103],v[96:99]\n"
TR "a[210:211],v1 offset:16904\n"
"v_fma_f32 v157,v157,s54,v14\n"
MF "v[104:107],a[220:223],a[104:107],v[104:107]\n"
"s_add_u32 s72,s72,6144\n"
"v_fma_f32 v158,v158,s54,v14\n"
"v_fma_f32 v159,v159,s54,v14\n"
MF "v[104:107],a[216:219],a[64:67],v[104:107]\n"
"s_cmp_eq_u32 s72,s17\n"
"s_cselect_b32 s72,s16,s72\n"
TR "a[212:213],v1 offset:20488\n"
MF "v[112:115],a[216:219],a[68:71],v[112:115]\n"
"s_add_u32 m0,s64,24576\n"
TR "a[214:215],v1 offset:21000\n"
CV "v207,v158,v159\n"
MF "v[112:115],a[220:223],a[108:111],v[112:115]\n"
LD "v7,s[32:35],s72 offen nt lds\n"
MF "v[120:123],a[220:223],a[112:115],v[120:123]\n"
"s_add_u32 m0,s64,25472\n"
CV "v206,v156,v157\n"
CV "v205,v154,v155\n"
MF "v[120:123],a[216:219],a[72:75],v[120:123]\n"
LD "v7,s[32:35],s72 offen offset:128 nt lds\n"
MF "v[128:131],a[216:219],a[76:79],v[128:131]\n"
CV "v204,v152,v153\n"
"v_fma_f32 v144,v144,s54,v14\n"
MF "v[128:131],a[220:223],a[116:119],v[128:131]\n"
"v_fma_f32 v145,v145,s54,v14\n"
"v_fma_f32 v146,v146,s54,v14\n"
MF "v[128:131],a[200:203],a[156:159],v[128:131]\n"
TR "a[216:217],v1 offset:32776\n"
"v_fma_f32 v147,v147,s54,v14\n"
MF "v[128:131],a[204:207],a[196:199],v[128:131]\n"
"s_add_u32 m0,s64,26368\n"
TR "a[218:219],v1 offset:33288\n"
"v_fma_f32 v148,v148,s54,v14\n"
MF "v[120:123],a[204:207],a[192:195],v[120:123]\n"
LD "v7,s[32:35],s72 offen offset:256 nt lds\n"
MF "v[120:123],a[200:203],a[152:155],v[120:123]\n"
TR "a[220:221],v1 offset:36872\n"
"v_fma_f32 v149,v149,s54,v14\n"
MF "v[112:115],a[200:203],a[148:151],v[112:115]\n"
TR "a[222:223],v1 offset:37384\n"
"v_fma_f32 v150,v150,s54,v14\n"
MF "v[112:115],a[204:207],a[188:191],v[112:115]\n"
"v_fma_f32 v151,v151,s54,v14\n"
CV "v203,v150,v151\n"
MF "v[104:107],a[204:207],a[184:187],v[104:107]\n"
"s_add_u32 s72,s72,6144\n"
CV "v202,v148,v149\n"
CV "v201,v146,v147\n"
MF "v[104:107],a[200:203],a[144:147],v[104:107]\n"
"s_add_u32 m0,s64,30720\n"
CV "v200,v144,v145\n"
"v_fma_f32 v136,v136,s54,v14\n"
MF "v[96:99],a[200:203],a[140:143],v[96:99]\n"
LD "v7,s[32:35],s72 offen nt lds\n"
MF "v[96:99],a[204:207],a[180:183],v[96:99]\n"
"s_waitcnt lgkmcnt(4)\n"
"v_fma_f32 v137,v137,s54,v14\n"
"v_fma_f32 v138,v138,s54,v14\n"
MF "v[60:63],a[208:211],a[60:63],v[60:63]\n"
TR "a[200:201],v1 offset:16384\n"
"v_fma_f32 v139,v139,s54,v14\n"
MF "v[60:63],a[212:215],a[100:103],v[60:63]\n"
TR "a[202:203],v1 offset:16896\n"
"v_fma_f32 v140,v140,s54,v14\n"
MF "v[68:71],a[212:215],a[104:107],v[68:71]\n"
"v_fma_f32 v141,v141,s54,v14\n"
"v_fma_f32 v142,v142,s54,v14\n"
MF "v[68:71],a[208:211],a[64:67],v[68:71]\n"
TR "a[204:205],v1 offset:20480\n"
"v_fma_f32 v143,v143,s54,v14\n"
MF "v[76:79],a[208:211],a[68:71],v[76:79]\n"
"s_add_u32 m0,s64,31616\n"
TR "a[206:207],v1 offset:20992\n"
CV "v199,v142,v143\n"
MF "v[76:79],a[212:215],a[108:111],v[76:79]\n"
LD "v7,s[32:35],s72 offen offset:128 nt lds\n"
MF "v[84:87],a[212:215],a[112:115],v[84:87]\n"
CV "v198,v140,v141\n"
CV "v197,v138,v139\n"
MF "v[84:87],a[208:211],a[72:75],v[84:87]\n"
CV "v196,v136,v137\n"
"v_fma_f32 v128,v128,s54,v14\n"
MF "v[92:95],a[208:211],a[76:79],v[92:95]\n"
"s_add_u32 m0,s64,32512\n"
"v_fma_f32 v129,v129,s54,v14\n"
"v_fma_f32 v130,v130,s54,v14\n"
MF "v[92:95],a[212:215],a[116:119],v[92:95]\n"
"s_waitcnt lgkmcnt(4)\n"
LD "v7,s[32:35],s72 offen offset:256 nt lds\n"
MF "v[60:63],a[216:219],a[140:143],v[60:63]\n"
TR "a[208:209],v1 offset:32768\n"
"v_fma_f32 v131,v131,s54,v14\n"
MF "v[60:63],a[220:223],a[180:183],v[60:63]\n"
TR "a[210:211],v1 offset:33280\n"
"v_fma_f32 v132,v132,s54,v14\n"
MF "v[68:71],a[220:223],a[184:187],v[68:71]\n"
"v_fma_f32 v133,v133,s54,v14\n"
"v_fma_f32 v134,v134,s54,v14\n"
MF "v[68:71],a[216:219],a[144:147],v[68:71]\n"
TR "a[212:213],v1 offset:36864\n"
"v_fma_f32 v135,v135,s54,v14\n"
MF "v[76:79],a[216:219],a[148:151],v[76:79]\n"
TR "a[214:215],v1 offset:37376\n"
CV "v195,v134,v135\n"
MF "v[76:79],a[220:223],a[188:191],v[76:79]\n"
CV "v194,v132,v133\n"
CV "v193,v130,v131\n"
MF "v[84:87],a[220:223],a[192:195],v[84:87]\n"
CV "v192,v128,v129\n"
"v_fma_f32 v120,v120,s54,v14\n"
MF "v[84:87],a[216:219],a[152:155],v[84:87]\n"
"v_fma_f32 v121,v121,s54,v14\n"
"v_fma_f32 v122,v122,s54,v14\n"
MF "v[92:95],a[216:219],a[156:159],v[92:95]\n"
"v_fma_f32 v123,v123,s54,v14\n"
"v_fma_f32 v124,v124,s54,v14\n"
MF "v[92:95],a[220:223],a[196:199],v[92:95]\n"
"s_waitcnt lgkmcnt(0)\n"
"v_fma_f32 v125,v125,s54,v14\n"
"v_fma_f32 v126,v126,s54,v14\n"
MF "v[56:59],a[200:203],a[60:63],v[56:59]\n"
TR "a[216:217],v0 offset:16392\n"
"v_fma_f32 v127,v127,s54,v14\n"
MF "v[56:59],a[204:207],a[100:103],v[56:59]\n"
TR "a[218:219],v0 offset:16904\n"
CV "v191,v126,v127\n"
MF "v[64:67],a[204:207],a[104:107],v[64:67]\n"
CV "v190,v124,v125\n"
CV "v189,v122,v123\n"
MF "v[64:67],a[200:203],a[64:67],v[64:67]\n"
TR "a[220:221],v0 offset:20488\n"
CV "v188,v120,v121\n"
MF "v[72:75],a[200:203],a[68:71],v[72:75]\n"
TR "a[222:223],v0 offset:21000\n"
"v_fma_f32 v112,v112,s54,v14\n"
MF "v[72:75],a[204:207],a[108:111],v[72:75]\n"
"v_fma_f32 v113,v113,s54,v14\n"
"v_fma_f32 v114,v114,s54,v14\n"
MF "v[80:83],a[204:207],a[112:115],v[80:83]\n"
"v_fma_f32 v115,v115,s54,v14\n"
"v_fma_f32 v116,v116,s54,v14\n"
MF "v[80:83],a[200:203],a[72:75],v[80:83]\n"
"v_fma_f32 v117,v117,s54,v14\n"
"v_fma_f32 v118,v118,s54,v14\n"
MF "v[88:91],a[200:203],a[76:79],v[88:91]\n"
"v_fma_f32 v119,v119,s54,v14\n"
CV "v187,v118,v119\n"
MF "v[88:91],a[204:207],a[116:119],v[88:91]\n"
CV "v186,v116,v117\n"
CV "v185,v114,v115\n"
MF "v[88:91],a[208:211],a[156:159],v[88:91]\n"
TR "a[200:201],v0 offset:32776\n"
CV "v184,v112,v113\n"
MF "v[88:91],a[212:215],a[196:199],v[88:91]\n"
TR "a[202:203],v0 offset:33288\n"
"v_fma_f32 v104,v104,s54,v14\n"
MF "v[80:83],a[212:215],a[192:195],v[80:83]\n"
"v_fma_f32 v105,v105,s54,v14\n"
"v_fma_f32 v106,v106,s54,v14\n"
MF "v[80:83],a[208:211],a[152:155],v[80:83]\n"
TR "a[204:205],v0 offset:36872\n"
"v_fma_f32 v107,v107,s54,v14\n"
MF "v[72:75],a[208:211],a[148:151],v[72:75]\n"
TR "a[206:207],v0 offset:37384\n"
"v_fma_f32 v108,v108,s54,v14\n"
MF "v[72:75],a[212:215],a[188:191],v[72:75]\n"
"v_fma_f32 v109,v109,s54,v14\n"
"v_fma_f32 v110,v110,s54,v14\n"
MF "v[64:67],a[212:215],a[184:187],v[64:67]\n"
"v_fma_f32 v111,v111,s54,v14\n"
CV "v183,v110,v111\n"
MF "v[64:67],a[208:211],a[144:147],v[64:67]\n"
CV "v182,v108,v109\n"
CV "v181,v106,v107\n"
MF "v[56:59],a[208:211],a[140:143],v[56:59]\n"
CV "v180,v104,v105\n"
"v_fma_f32 v96,v96,s54,v14\n"
MF "v[56:59],a[212:215],a[180:183],v[56:59]\n"
"s_waitcnt lgkmcnt(4)\n"
"v_fma_f32 v97,v97,s54,v14\n"
"v_fma_f32 v98,v98,s54,v14\n"
MF "v[20:23],a[216:219],a[60:63],v[20:23]\n"
TR "a[208:209],v0 offset:16384\n"
"v_fma_f32 v99,v99,s54,v14\n"
MF "v[20:23],a[220:223],a[100:103],v[20:23]\n"
TR "a[210:211],v0 offset:16896\n"
"v_fma_f32 v100,v100,s54,v14\n"
MF "v[28:31],a[220:223],a[104:107],v[28:31]\n"
"v_fma_f32 v101,v101,s54,v14\n"
"v_fma_f32 v102,v102,s54,v14\n"
MF "v[28:31],a[216:219],a[64:67],v[28:31]\n"
TR "a[212:213],v0 offset:20480\n"
"v_fma_f32 v103,v103,s54,v14\n"
MF "v[36:39],a[216:219],a[68:71],v[36:39]\n"
TR "a[214:215],v0 offset:20992\n"
CV "v179,v102,v103\n"
MF "v[36:39],a[220:223],a[108:111],v[36:39]\n"
CV "v178,v100,v101\n"
CV "v177,v98,v99\n"
MF "v[44:47],a[220:223],a[112:115],v[44:47]\n"
CV "v176,v96,v97\n"
"v_fma_f32 v88,v88,s54,v14\n"
MF "v[44:47],a[216:219],a[72:75],v[44:47]\n"
"v_fma_f32 v89,v89,s54,v14\n"
"v_fma_f32 v90,v90,s54,v14\n"
MF "v[52:55],a[216:219],a[76:79],v[52:55]\n"
"v_fma_f32 v91,v91,s54,v14\n"
"v_fma_f32 v92,v92,s54,v14\n"
MF "v[52:55],a[220:223],a[116:119],v[52:55]\n"
"s_waitcnt lgkmcnt(4)\n"
"v_fma_f32 v93,v93,s54,v14\n"
"v_fma_f32 v94,v94,s54,v14\n"
MF "v[20:23],a[200:203],a[140:143],v[20:23]\n"
TR "a[216:217],v0 offset:32768\n"
"v_fma_f32 v95,v95,s54,v14\n"
MF "v[20:23],a[204:207],a[180:183],v[20:23]\n"
TR "a[218:219],v0 offset:33280\n"
CV "v175,v94,v95\n"
MF "v[28:31],a[204:207],a[184:187],v[28:31]\n"
CV "v174,v92,v93\n"
CV "v173,v90,v91\n"
MF "v[28:31],a[200:203],a[144:147],v[28:31]\n"
TR "a[220:221],v0 offset:36864\n"
CV "v172,v88,v89\n"
MF "v[36:39],a[200:203],a[148:151],v[36:39]\n"
TR "a[222:223],v0 offset:37376\n"
"v_fma_f32 v80,v80,s54,v14\n"
MF "v[36:39],a[204:207],a[188:191],v[36:39]\n"
"v_fma_f32 v81,v81,s54,v14\n"
"v_fma_f32 v82,v82,s54,v14\n"
MF "v[44:47],a[204:207],a[192:195],v[44:47]\n"
"v_fma_f32 v83,v83,s54,v14\n"
"v_fma_f32 v84,v84,s54,v14\n"
MF "v[44:47],a[200:203],a[152:155],v[44:47]\n"
"v_fma_f32 v85,v85,s54,v14\n"
"v_fma_f32 v86,v86,s54,v14\n"
MF "v[52:55],a[200:203],a[156:159],v[52:55]\n"
"v_fma_f32 v87,v87,s54,v14\n"
CV "v171,v86,v87\n"
MF "v[52:55],a[204:207],a[196:199],v[52:55]\n"
"s_waitcnt lgkmcnt(0)\n"
"s_barrier\n"
MF "v[16:19],a[208:211],a[60:63],v[16:19]\n"
"s_add_u32 m0,s65,49152\n"
CV "v170,v84,v85\n"
CV "v169,v82,v83\n"
MF "v[16:19],a[212:215],a[100:103],v[16:19]\n"
LD "v8,s[36:39],s73 offen nt lds\n"
MF "v[24:27],a[212:215],a[104:107],v[24:27]\n"
"s_add_u32 m0,s65,50048\n"
CV "v168,v80,v81\n"
"v_fma_f32 v72,v72,s54,v14\n"
MF "v[24:27],a[208:211],a[64:67],v[24:27]\n"
LD "v8,s[36:39],s73 offen offset:128 nt lds\n"
MF "v[32:35],a[208:211],a[68:71],v[32:35]\n"
"v_fma_f32 v73,v73,s54,v14\n"
"v_fma_f32 v74,v74,s54,v14\n"
MF "v[32:35],a[212:215],a[108:111],v[32:35]\n"
"s_add_u32 m0,s65,50944\n"
"v_fma_f32 v75,v75,s54,v14\n"
"v_fma_f32 v76,v76,s54,v14\n"
MF "v[40:43],a[212:215],a[112:115],v[40:43]\n"
LD "v8,s[36:39],s73 offen offset:256 nt lds\n"
MF "v[40:43],a[208:211],a[72:75],v[40:43]\n"
"v_fma_f32 v77,v77,s54,v14\n"
"v_fma_f32 v78,v78,s54,v14\n"
MF "v[48:51],a[208:211],a[76:79],v[48:51]\n"
"s_add_u32 m0,s65,49152\n"
"v_fma_f32 v79,v79,s54,v14\n"
CV "v167,v78,v79\n"
MF "v[48:51],a[212:215],a[116:119],v[48:51]\n"
LD "v8,s[36:39],s73 offen offset:3072 nt lds\n"
MF "v[48:51],a[216:219],a[156:159],v[48:51]\n"
"s_add_u32 m0,s65,50048\n"
CV "v166,v76,v77\n"
CV "v165,v74,v75\n"
MF "v[48:51],a[220:223],a[196:199],v[48:51]\n"
LD "v8,s[36:39],s73 offen offset:3200 nt lds\n"
MF "v[40:43],a[220:223],a[192:195],v[40:43]\n"
CV "v164,v72,v73\n"
"v_fma_f32 v64,v64,s54,v14\n"
MF "v[40:43],a[216:219],a[152:155],v[40:43]\n"
"s_add_u32 m0,s65,50944\n"
"v_fma_f32 v65,v65,s54,v14\n"
"v_fma_f32 v66,v66,s54,v14\n"
MF "v[32:35],a[216:219],a[148:151],v[32:35]\n"
LD "v8,s[36:39],s73 offen offset:3328 nt lds\n"
MF "v[32:35],a[220:223],a[188:191],v[32:35]\n"
"v_fma_f32 v67,v67,s54,v14\n"
"v_fma_f32 v68,v68,s54,v14\n"
MF "v[24:27],a[220:223],a[184:187],v[24:27]\n"
"v_fma_f32 v69,v69,s54,v14\n"
"v_fma_f32 v70,v70,s54,v14\n"
MF "v[24:27],a[216:219],a[144:147],v[24:27]\n"
"v_fma_f32 v71,v71,s54,v14\n"
CV "v163,v70,v71\n"
MF "v[16:19],a[216:219],a[140:143],v[16:19]\n"
CV "v162,v68,v69\n"
CV "v161,v66,v67\n"
MF "v[16:19],a[220:223],a[180:183],v[16:19]\n"
CV "v160,v64,v65\n"
"v_pk_fma_f32 v[56:57],v[56:57],s[54:55],v[14:15] op_sel_hi:[1,1,0]\n"
"v_pk_fma_f32 v[58:59],v[58:59],s[54:55],v[14:15] op_sel_hi:[1,1,0]\n"
"v_pk_fma_f32 v[60:61],v[60:61],s[54:55],v[14:15] op_sel_hi:[1,1,0]\n"
"v_pk_fma_f32 v[62:63],v[62:63],s[54:55],v[14:15] op_sel_hi:[1,1,0]\n"
CV "v159,v62,v63\n"
CV "v158,v60,v61\n"
CV "v157,v58,v59\n"
CV "v156,v56,v57\n"
"v_pk_mul_f32 v[48:49],v[48:49],s[54:55]\n"
"v_pk_mul_f32 v[50:51],v[50:51],s[54:55]\n"
"v_pk_mul_f32 v[52:53],v[52:53],s[54:55]\n"
"v_pk_mul_f32 v[54:55],v[54:55],s[54:55]\n"
CV "v155,v54,v55\n"
CV "v154,v52,v53\n"
CV "v153,v50,v51\n"
CV "v152,v48,v49\n"
"v_pk_mul_f32 v[40:41],v[40:41],s[54:55]\n"
"v_pk_mul_f32 v[42:43],v[42:43],s[54:55]\n"
"v_pk_mul_f32 v[44:45],v[44:45],s[54:55]\n"
"v_pk_mul_f32 v[46:47],v[46:47],s[54:55]\n"
CV "v151,v46,v47\n"
CV "v150,v44,v45\n"
CV "v149,v42,v43\n"
CV "v148,v40,v41\n"
"v_pk_mul_f32 v[32:33],v[32:33],s[54:55]\n"
"v_pk_mul_f32 v[34:35],v[34:35],s[54:55]\n"
"v_pk_mul_f32 v[36:37],v[36:37],s[54:55]\n"
"v_pk_mul_f32 v[38:39],v[38:39],s[54:55]\n"
CV "v147,v38,v39\n"
CV "v146,v36,v37\n"
CV "v145,v34,v35\n"
CV "v144,v32,v33\n"
"v_pk_mul_f32 v[24:25],v[24:25],s[54:55]\n"
"v_pk_mul_f32 v[26:27],v[26:27],s[54:55]\n"
"v_pk_mul_f32 v[28:29],v[28:29],s[54:55]\n"
"v_pk_mul_f32 v[30:31],v[30:31],s[54:55]\n"
CV "v143,v30,v31\n"
CV "v142,v28,v29\n"
CV "v141,v26,v27\n"
CV "v140,v24,v25\n"
"v_pk_mul_f32 v[16:17],v[16:17],s[54:55]\n"
"v_pk_mul_f32 v[18:19],v[18:19],s[54:55]\n"
"v_pk_mul_f32 v[20:21],v[20:21],s[54:55]\n"
"v_pk_mul_f32 v[22:23],v[22:23],s[54:55]\n"
CV "v139,v22,v23\n"
CV "v138,v20,v21\n"
CV "v137,v18,v19\n"
CV "v136,v16,v17\n"
"s_cmp_eq_u32 s57,s69\n"
"s_cbranch_scc1 .LattB\n"
AR "v0,a244\n"
AR "v2,a245\n"
"v_xor_b32_e64 v1,64,v0\n"
"v_add_u32 v2,49152,v2\n"
"v_xor_b32 v3,32,v2\n"
"v_xor_b32 v4,64,v2\n"
"v_xor_b32 v5,96,v2\n"
"s_mov_b32 s61,7\n"
"s_memtime s[52:53]\n"
"s_memrealtime s[100:101]\n"
"s_waitcnt lgkmcnt(0)\n"
"s_sub_u32 s53,s52,s80\n"
"s_cmp_gt_u32 s53,27000\n"
"s_cselect_b32 s101,1,0\n"
"s_cmp_eq_u32 s87,2\n"
"s_cselect_b32 s87,s101,s87\n"
"s_cmp_eq_u32 s87,1\n"
"s_cmp_eq_u32 s57,0\n"
"s_mov_b32 s101,0\n"
"s_cselect_b32 s101,130,s101\n"
"s_cmp_eq_u32 s87,1\n"
"s_cselect_b32 s97,s101,0\n"
"s_cselect_b32 s21,0,0\n"
"s_add_u32 s21,s21,s100\n"
".Latt2x:\n"
"s_cmp_eq_u32 s97,0\n"
"s_cselect_b32 s95,0x47800000,0\n"
"s_mov_b32 s77,s95\n"
"s_waitcnt vmcnt(12) lgkmcnt(0)\n"
"s_barrier\n"
"ds_read_b128 v[16:19],v0\n"
"ds_read_b128 v[20:23],v1\n"
"ds_read_b128 v[24:27],v0 offset:1024\n"
"s_cmp_eq_u32 s57,1\n"
"s_cbranch_scc1 .Lt0kx\n"
"s_waitcnt lgkmcnt(2)\n"
MF "v[72:75],v[16:19],v[136:139],0\n"
"s_add_u32 s73,s73,12288\n"
"ds_read_b128 v[28:31],v1 offset:1024\n"
AR "v10,a224\n"
MF "v[76:79],v[16:19],v[140:143],0\n"
"s_cmp_eq_u32 s73,s19\n"
"s_cselect_b32 s73,s18,s73\n"
"s_add_u32 m0,s65,73728\n"
MF "v[80:83],v[16:19],v[144:147],0\n"
LD "v8,s[36:39],s73 offen nt lds\n"
MF "v[84:87],v[16:19],v[148:151],0\n"
"v_max_f32_dpp v10,|v10|,|v10| quad_perm:[1,0,3,2] row_mask:0xf bank_mask:0xf\n"
AR "v11,a225\n"
MF "v[88:91],v[16:19],v[152:155],0\n"
"s_waitcnt lgkmcnt(2)\n"
"ds_read_b128 v[16:19],v0 offset:2048\n"
AR "v12,a226\n"
MF "v[72:75],v[20:23],v[156:159],v[72:75]\n"
"s_add_u32 m0,s65,74624\n"
"v_max_f32_dpp v10,v10,v10 quad_perm:[2,3,0,1] row_mask:0xf bank_mask:0xf\n"
"v_max_f32_dpp v11,|v11|,|v11| quad_perm:[1,0,3,2] row_mask:0xf bank_mask:0xf\n"
MF "v[76:79],v[20:23],v[160:163],v[76:79]\n"
LD "v8,s[36:39],s73 offen offset:128 nt lds\n"
MF "v[80:83],v[20:23],v[164:167],v[80:83]\n"
"v_max_f32_dpp v10,v10,v10 row_half_mirror row_mask:0xf bank_mask:0xf\n"
"v_max_f32_dpp v11,v11,v11 quad_perm:[2,3,0,1] row_mask:0xf bank_mask:0xf\n"
MF "v[84:87],v[20:23],v[168:171],v[84:87]\n"
"v_readlane_b32 s13,v10,0\n"
"v_readlane_b32 s0,v10,24\n"
MF "v[88:91],v[20:23],v[172:175],v[88:91]\n"
"s_waitcnt lgkmcnt(2)\n"
"ds_read_b128 v[20:23],v1 offset:2048\n"
"v_readlane_b32 s1,v10,32\n"
MF "v[72:75],v[24:27],v[176:179],v[72:75]\n"
"s_add_u32 m0,s65,75520\n"
"v_readlane_b32 s2,v10,56\n"
"v_max_f32_dpp v11,v11,v11 row_half_mirror row_mask:0xf bank_mask:0xf\n"
MF "v[76:79],v[24:27],v[180:183],v[76:79]\n"
LD "v8,s[36:39],s73 offen offset:256 nt lds\n"
MF "v[80:83],v[24:27],v[184:187],v[80:83]\n"
"s_min_u32 s13,s13,s0\n"
"v_readlane_b32 s11,v11,0\n"
"v_readlane_b32 s3,v11,24\n"
MF "v[84:87],v[24:27],v[188:191],v[84:87]\n"
"s_min_u32 s1,s1,s2\n"
"v_readlane_b32 s4,v11,32\n"
"v_readlane_b32 s5,v11,56\n"
MF "v[88:91],v[24:27],v[192:195],v[88:91]\n"
"s_waitcnt lgkmcnt(2)\n"
"ds_read_b128 v[24:27],v0 offset:6144\n"
"v_max_f32_dpp v12,|v12|,|v12| quad_perm:[1,0,3,2] row_mask:0xf bank_mask:0xf\n"
MF "v[72:75],v[28:31],v[196:199],v[72:75]\n"
"s_add_u32 m0,s65,73728\n"
MF "v[76:79],v[28:31],v[200:203],v[76:79]\n"
LD "v8,s[36:39],s73 offen offset:3072 nt lds\n"
MF "v[80:83],v[28:31],v[204:207],v[80:83]\n"
"s_min_u32 s13,s13,s1\n"
"v_max_f32_dpp v12,v12,v12 quad_perm:[2,3,0,1] row_mask:0xf bank_mask:0xf\n"
MF "v[84:87],v[28:31],v[208:211],v[84:87]\n"
"s_and_b32 s13,s13,0x7f800000\n"
MF "v[88:91],v[28:31],v[212:215],v[88:91]\n"
"s_waitcnt lgkmcnt(2)\n"
"ds_read_b128 v[28:31],v1 offset:6144\n"
"v_max_f32_dpp v12,v12,v12 row_half_mirror row_mask:0xf bank_mask:0xf\n"
MF "v[72:75],v[16:19],v[216:219],v[72:75]\n"
"s_add_u32 m0,s65,74624\n"
"v_readlane_b32 s10,v12,0\n"
"v_readlane_b32 s6,v12,24\n"
MF "v[76:79],v[16:19],v[220:223],v[76:79]\n"
LD "v8,s[36:39],s73 offen offset:3200 nt lds\n"
MF "v[80:83],v[16:19],v[224:227],v[80:83]\n"
"s_sub_u32 s13,s13,0x14000000\n"
"v_readlane_b32 s7,v12,32\n"
"v_readlane_b32 s8,v12,56\n"
MF "v[84:87],v[16:19],v[228:231],v[84:87]\n"
"s_cmp_lt_u32 s13,0x50000000\n"
"s_cselect_b32 s13,s13,0xed400000\n"
MF "v[88:91],v[16:19],v[232:235],v[88:91]\n"
"s_waitcnt lgkmcnt(2)\n"
"ds_read_b128 v[16:19],v0 offset:7168\n"
MF "v[72:75],v[20:23],v[236:239],v[72:75]\n"
"s_add_u32 m0,s65,75520\n"
MF "v[76:79],v[20:23],v[240:243],v[76:79]\n"
LD "v8,s[36:39],s73 offen offset:3328 nt lds\n"
MF "v[80:83],v[20:23],v[244:247],v[80:83]\n"
"s_add_u32 s13,s13,0x12c00000\n"
MF "v[84:87],v[20:23],v[248:251],v[84:87]\n"
"s_min_u32 s11,s11,s3\n"
MF "v[88:91],v[20:23],v[252:255],v[88:91]\n"
"s_waitcnt lgkmcnt(2)\n"
"ds_read_b128 v[20:23],v1 offset:7168\n"
MF "v[112:115],v[24:27],v[136:139],0\n"
"s_min_u32 s4,s4,s5\n"
MF "v[116:119],v[24:27],v[140:143],0\n"
"s_min_u32 s11,s11,s4\n"
MF "v[120:123],v[24:27],v[144:147],0\n"
"s_and_b32 s11,s11,0x7f800000\n"
MF "v[124:127],v[24:27],v[148:151],0\n"
"s_sub_u32 s11,s11,0x14000000\n"
MF "v[128:131],v[24:27],v[152:155],0\n"
"s_waitcnt lgkmcnt(2)\n"
"ds_read_b128 v[24:27],v0 offset:8192\n"
MF "v[112:115],v[28:31],v[156:159],v[112:115]\n"
"s_min_u32 s10,s10,s6\n"
"v_max3_f32 v52,v72,v73,v74\n"
"v_max_f32 v52,v52,v75\n"
MF "v[116:119],v[28:31],v[160:163],v[116:119]\n"
"s_min_u32 s7,s7,s8\n"
"v_max3_f32 v53,v76,v77,v78\n"
"v_max_f32 v53,v53,v79\n"
MF "v[120:123],v[28:31],v[164:167],v[120:123]\n"
"s_min_u32 s10,s10,s7\n"
"v_max3_f32 v54,v80,v81,v82\n"
"v_max_f32 v54,v54,v83\n"
MF "v[124:127],v[28:31],v[168:171],v[124:127]\n"
"v_max3_f32 v55,v84,v85,v86\n"
"v_max_f32 v55,v55,v87\n"
MF "v[128:131],v[28:31],v[172:175],v[128:131]\n"
"s_waitcnt lgkmcnt(2)\n"
"ds_read_b128 v[28:31],v1 offset:8192\n"
"v_max3_f32 v56,v88,v89,v90\n"
MF "v[112:115],v[16:19],v[176:179],v[112:115]\n"
"s_cmp_lt_u32 s11,0x50000000\n"
"s_cselect_b32 s11,s11,0xed400000\n"
"v_max_f32 v56,v56,v91\n"
MF "v[116:119],v[16:19],v[180:183],v[116:119]\n"
"s_add_u32 s11,s11,0x12c00000\n"
MF "v[120:123],v[16:19],v[184:187],v[120:123]\n"
MF "v[124:127],v[16:19],v[188:191],v[124:127]\n"
MF "v[128:131],v[16:19],v[192:195],v[128:131]\n"
"s_waitcnt lgkmcnt(2)\n"
MF "v[112:115],v[20:23],v[196:199],v[112:115]\n"
MF "v[116:119],v[20:23],v[200:203],v[116:119]\n"
"s_waitcnt lgkmcnt(0)\n"
"s_barrier\n"
MF "v[120:123],v[20:23],v[204:207],v[120:123]\n"
"s_add_u32 s72,s72,6144\n"
MF "v[124:127],v[20:23],v[208:211],v[124:127]\n"
"s_cmp_eq_u32 s72,s17\n"
"s_cselect_b32 s72,s16,s72\n"
"s_add_u32 m0,s64,0\n"
MF "v[128:131],v[20:23],v[212:215],v[128:131]\n"
LD "v7,s[32:35],s72 offen nt lds\n"
MF "v[112:115],v[24:27],v[216:219],v[112:115]\n"
"s_add_u32 m0,s64,896\n"
MF "v[116:119],v[24:27],v[220:223],v[116:119]\n"
LD "v7,s[32:35],s72 offen offset:128 nt lds\n"
MF "v[120:123],v[24:27],v[224:227],v[120:123]\n"
"s_add_u32 m0,s64,1792\n"
MF "v[124:127],v[24:27],v[228:231],v[124:127]\n"
LD "v7,s[32:35],s72 offen offset:256 nt lds\n"
MF "v[128:131],v[24:27],v[232:235],v[128:131]\n"
"s_add_u32 s72,s72,6144\n"
"s_add_u32 m0,s64,6144\n"
MF "v[112:115],v[28:31],v[236:239],v[112:115]\n"
LD "v7,s[32:35],s72 offen nt lds\n"
MF "v[116:119],v[28:31],v[240:243],v[116:119]\n"
"s_add_u32 m0,s64,7040\n"
MF "v[120:123],v[28:31],v[244:247],v[120:123]\n"
LD "v7,s[32:35],s72 offen offset:128 nt lds\n"
MF "v[124:127],v[28:31],v[248:251],v[124:127]\n"
"s_add_u32 m0,s64,7936\n"
MF "v[128:131],v[28:31],v[252:255],v[128:131]\n"
LD "v7,s[32:35],s72 offen offset:256 nt lds\n"
"s_branch .Lt0ex\n"
".Lt0kx:\n"
"s_lshl_b32 s58,s56,10\n"
"s_add_u32 s58,s58,73728\n"
"v_add_u32 v32,s58,v9\n"
"v_add_u32 v33,65536,v32\n"
"s_waitcnt lgkmcnt(2)\n"
MF "v[72:75],v[16:19],v[136:139],0\n"
"s_add_u32 s73,s73,12288\n"
"ds_read_b128 v[28:31],v1 offset:1024\n"
AR "v10,a224\n"
MF "v[76:79],v[16:19],v[140:143],0\n"
"s_cmp_eq_u32 s73,s19\n"
"s_cselect_b32 s73,s18,s73\n"
"s_add_u32 m0,s65,73728\n"
MF "v[80:83],v[16:19],v[144:147],0\n"
LD "v8,s[36:39],s73 offen nt lds\n"
MF "v[84:87],v[16:19],v[148:151],0\n"
"v_max_f32_dpp v10,|v10|,|v10| quad_perm:[1,0,3,2] row_mask:0xf bank_mask:0xf\n"
AR "v11,a225\n"
MF "v[88:91],v[16:19],v[152:155],0\n"
"s_waitcnt lgkmcnt(2)\n"
"ds_read_b128 v[16:19],v0 offset:2048\n"
AR "v12,a226\n"
MF "v[72:75],v[20:23],v[156:159],v[72:75]\n"
"s_add_u32 m0,s65,74624\n"
"v_max_f32_dpp v10,v10,v10 quad_perm:[2,3,0,1] row_mask:0xf bank_mask:0xf\n"
"v_max_f32_dpp v11,|v11|,|v11| quad_perm:[1,0,3,2] row_mask:0xf bank_mask:0xf\n"
MF "v[76:79],v[20:23],v[160:163],v[76:79]\n"
LD "v8,s[36:39],s73 offen offset:128 nt lds\n"
MF "v[80:83],v[20:23],v[164:167],v[80:83]\n"
"v_max_f32_dpp v10,v10,v10 row_half_mirror row_mask:0xf bank_mask:0xf\n"
"v_max_f32_dpp v11,v11,v11 quad_perm:[2,3,0,1] row_mask:0xf bank_mask:0xf\n"
MF "v[84:87],v[20:23],v[168:171],v[84:87]\n"
"v_readlane_b32 s13,v10,0\n"
"v_readlane_b32 s0,v10,24\n"
MF "v[88:91],v[20:23],v[172:175],v[88:91]\n"
"s_waitcnt lgkmcnt(2)\n"
"ds_read_b128 v[20:23],v1 offset:2048\n"
"v_readlane_b32 s1,v10,32\n"
MF "v[72:75],v[24:27],v[176:179],v[72:75]\n"
"s_add_u32 m0,s65,75520\n"
"v_readlane_b32 s2,v10,56\n"
"v_max_f32_dpp v11,v11,v11 row_half_mirror row_mask:0xf bank_mask:0xf\n"
MF "v[76:79],v[24:27],v[180:183],v[76:79]\n"
LD "v8,s[36:39],s73 offen offset:256 nt lds\n"
MF "v[80:83],v[24:27],v[184:187],v[80:83]\n"
"s_min_u32 s13,s13,s0\n"
"v_readlane_b32 s11,v11,0\n"
"v_readlane_b32 s3,v11,24\n"
MF "v[84:87],v[24:27],v[188:191],v[84:87]\n"
"s_min_u32 s1,s1,s2\n"
"v_readlane_b32 s4,v11,32\n"
"v_readlane_b32 s5,v11,56\n"
MF "v[88:91],v[24:27],v[192:195],v[88:91]\n"
"s_waitcnt lgkmcnt(2)\n"
"ds_read_b128 v[24:27],v0 offset:6144\n"
"v_max_f32_dpp v12,|v12|,|v12| quad_perm:[1,0,3,2] row_mask:0xf bank_mask:0xf\n"
MF "v[72:75],v[28:31],v[196:199],v[72:75]\n"
"s_add_u32 m0,s65,73728\n"
"ds_write_b128 v32,a[0:3] offset:24576\n"
MF "v[76:79],v[28:31],v[200:203],v[76:79]\n"
LD "v8,s[36:39],s73 offen offset:3072 nt lds\n"
MF "v[80:83],v[28:31],v[204:207],v[80:83]\n"
"s_min_u32 s13,s13,s1\n"
"v_max_f32_dpp v12,v12,v12 quad_perm:[2,3,0,1] row_mask:0xf bank_mask:0xf\n"
AR "v62,a20\n"
MF "v[84:87],v[28:31],v[208:211],v[84:87]\n"
"s_and_b32 s13,s13,0x7f800000\n"
"ds_write_b128 v32,a[4:7] offset:28672\n"
MF "v[88:91],v[28:31],v[212:215],v[88:91]\n"
"s_waitcnt lgkmcnt(4)\n"
"ds_read_b128 v[28:31],v1 offset:6144\n"
"v_max_f32_dpp v12,v12,v12 row_half_mirror row_mask:0xf bank_mask:0xf\n"
MF "v[72:75],v[16:19],v[216:219],v[72:75]\n"
"s_add_u32 m0,s65,74624\n"
"v_readlane_b32 s10,v12,0\n"
"v_readlane_b32 s6,v12,24\n"
MF "v[76:79],v[16:19],v[220:223],v[76:79]\n"
LD "v8,s[36:39],s73 offen offset:3200 nt lds\n"
MF "v[80:83],v[16:19],v[224:227],v[80:83]\n"
"s_sub_u32 s13,s13,0x14000000\n"
"v_readlane_b32 s7,v12,32\n"
"v_readlane_b32 s8,v12,56\n"
MF "v[84:87],v[16:19],v[228:231],v[84:87]\n"
"s_cmp_lt_u32 s13,0x50000000\n"
"s_cselect_b32 s13,s13,0xed400000\n"
AR "v63,a21\n"
MF "v[88:91],v[16:19],v[232:235],v[88:91]\n"
"s_waitcnt lgkmcnt(4)\n"
"ds_read_b128 v[16:19],v0 offset:7168\n"
AR "v64,a22\n"
MF "v[72:75],v[20:23],v[236:239],v[72:75]\n"
"s_add_u32 m0,s65,75520\n"
"ds_write_b128 v32,a[8:11] offset:32768\n"
MF "v[76:79],v[20:23],v[240:243],v[76:79]\n"
LD "v8,s[36:39],s73 offen offset:3328 nt lds\n"
MF "v[80:83],v[20:23],v[244:247],v[80:83]\n"
"s_add_u32 s13,s13,0x12c00000\n"
"ds_write_b128 v32,a[12:15] offset:36864\n"
MF "v[84:87],v[20:23],v[248:251],v[84:87]\n"
"s_min_u32 s11,s11,s3\n"
"ds_write_b128 v32,a[16:19] offset:40960\n"
MF "v[88:91],v[20:23],v[252:255],v[88:91]\n"
"s_waitcnt lgkmcnt(7)\n"
"ds_read_b128 v[20:23],v1 offset:7168\n"
AR "v65,a23\n"
MF "v[112:115],v[24:27],v[136:139],0\n"
"s_min_u32 s4,s4,s5\n"
"ds_write_b128 v32,a[40:43] offset:45056\n"
MF "v[116:119],v[24:27],v[140:143],0\n"
"s_min_u32 s11,s11,s4\n"
"ds_write_b128 v32,a[44:47] offset:49152\n"
MF "v[120:123],v[24:27],v[144:147],0\n"
"s_and_b32 s11,s11,0x7f800000\n"
"ds_write_b128 v32,a[48:51] offset:53248\n"
MF "v[124:127],v[24:27],v[148:151],0\n"
"s_sub_u32 s11,s11,0x14000000\n"
"ds_write_b128 v32,a[52:55] offset:57344\n"
MF "v[128:131],v[24:27],v[152:155],0\n"
"s_waitcnt lgkmcnt(9)\n"
"ds_read_b128 v[24:27],v0 offset:8192\n"
AR "v66,a24\n"
MF "v[112:115],v[28:31],v[156:159],v[112:115]\n"
"s_min_u32 s10,s10,s6\n"
"v_max3_f32 v52,v72,v73,v74\n"
"v_max_f32 v52,v52,v75\n"
MF "v[116:119],v[28:31],v[160:163],v[116:119]\n"
"s_min_u32 s7,s7,s8\n"
"v_max3_f32 v53,v76,v77,v78\n"
"v_max_f32 v53,v53,v79\n"
MF "v[120:123],v[28:31],v[164:167],v[120:123]\n"
"s_min_u32 s10,s10,s7\n"
"v_max3_f32 v54,v80,v81,v82\n"
"v_max_f32 v54,v54,v83\n"
MF "v[124:127],v[28:31],v[168:171],v[124:127]\n"
"v_max3_f32 v55,v84,v85,v86\n"
"v_max_f32 v55,v55,v87\n"
MF "v[128:131],v[28:31],v[172:175],v[128:131]\n"
"s_waitcnt lgkmcnt(9)\n"
"ds_read_b128 v[28:31],v1 offset:8192\n"
"v_max3_f32 v56,v88,v89,v90\n"
MF "v[112:115],v[16:19],v[176:179],v[112:115]\n"
"s_cmp_lt_u32 s11,0x50000000\n"
"s_cselect_b32 s11,s11,0xed400000\n"
"v_max_f32 v56,v56,v91\n"
MF "v[116:119],v[16:19],v[180:183],v[116:119]\n"
"s_add_u32 s11,s11,0x12c00000\n"
"ds_write_b128 v32,a[56:59] offset:61440\n"
MF "v[120:123],v[16:19],v[184:187],v[120:123]\n"
"ds_write_b128 v33,a[80:83]\n"
MF "v[124:127],v[16:19],v[188:191],v[124:127]\n"
"ds_write_b128 v33,a[84:87] offset:4096\n"
MF "v[128:131],v[16:19],v[192:195],v[128:131]\n"
"s_waitcnt lgkmcnt(9)\n"
"ds_write_b128 v33,a[88:91] offset:8192\n"
MF "v[112:115],v[20:23],v[196:199],v[112:115]\n"
AR "v67,a25\n"
AR "v68,a26\n"
MF "v[116:119],v[20:23],v[200:203],v[116:119]\n"
"s_waitcnt lgkmcnt(0)\n"
"s_barrier\n"
MF "v[120:123],v[20:23],v[204:207],v[120:123]\n"
"s_add_u32 s72,s72,6144\n"
"ds_write_b128 v33,a[92:95] offset:12288\n"
MF "v[124:127],v[20:23],v[208:211],v[124:127]\n"
"s_cmp_eq_u32 s72,s17\n"
"s_cselect_b32 s72,s16,s72\n"
"s_add_u32 m0,s64,0\n"
MF "v[128:131],v[20:23],v[212:215],v[128:131]\n"
LD "v7,s[32:35],s72 offen nt lds\n"
MF "v[112:115],v[24:27],v[216:219],v[112:115]\n"
"s_add_u32 m0,s64,896\n"
"ds_write_b128 v33,a[96:99] offset:16384\n"
MF "v[116:119],v[24:27],v[220:223],v[116:119]\n"
LD "v7,s[32:35],s72 offen offset:128 nt lds\n"
MF "v[120:123],v[24:27],v[224:227],v[120:123]\n"
"s_add_u32 m0,s64,1792\n"
AR "v69,a27\n"
AR "v70,a28\n"
MF "v[124:127],v[24:27],v[228:231],v[124:127]\n"
LD "v7,s[32:35],s72 offen offset:256 nt lds\n"
MF "v[128:131],v[24:27],v[232:235],v[128:131]\n"
"s_add_u32 s72,s72,6144\n"
"s_add_u32 m0,s64,6144\n"
AR "v71,a29\n"
MF "v[112:115],v[28:31],v[236:239],v[112:115]\n"
LD "v7,s[32:35],s72 offen nt lds\n"
MF "v[116:119],v[28:31],v[240:243],v[116:119]\n"
"s_add_u32 m0,s64,7040\n"
"ds_write_b128 v33,a[120:123] offset:20480\n"
MF "v[120:123],v[28:31],v[244:247],v[120:123]\n"
LD "v7,s[32:35],s72 offen offset:128 nt lds\n"
MF "v[124:127],v[28:31],v[248:251],v[124:127]\n"
"s_add_u32 m0,s64,7936\n"
AR "v132,a30\n"
AR "v133,a31\n"
MF "v[128:131],v[28:31],v[252:255],v[128:131]\n"
LD "v7,s[32:35],s72 offen offset:256 nt lds\n"
AR "v134,a32\n"
AR "v135,a33\n"
AR "v6,a34\n"
AR "v13,a35\n"
AR "v14,a36\n"
AR "v15,a37\n"
"v_accvgpr_mov_b32 a254,a38\n"
"v_accvgpr_mov_b32 a255,a39\n"
"v_accvgpr_mov_b32 a240,a168\n"
"v_accvgpr_mov_b32 a241,a169\n"
"v_accvgpr_mov_b32 a242,a170\n"
"v_accvgpr_mov_b32 a243,a171\n"
"v_accvgpr_mov_b32 a244,a172\n"
"v_accvgpr_mov_b32 a245,a173\n"
"v_accvgpr_mov_b32 a246,a174\n"
"v_accvgpr_mov_b32 a247,a175\n"
"v_accvgpr_mov_b32 a248,a176\n"
"v_accvgpr_mov_b32 a249,a177\n"
"v_accvgpr_mov_b32 a250,a178\n"
"v_accvgpr_mov_b32 a251,a179\n"
".Lt0ex:\n"
"s_nop 1\n"
"s_nop 0\n"
"v_max3_f32 v52,v52,v112,v113\n"
"v_max3_f32 v53,v53,v116,v117\n"
"v_max3_f32 v54,v54,v120,v121\n"
"v_max3_f32 v55,v55,v124,v125\n"
"v_max3_f32 v56,v56,v128,v129\n"
"v_max3_f32 v52,v52,v114,v115\n"
"v_max3_f32 v53,v53,v118,v119\n"
"v_max3_f32 v54,v54,v122,v123\n"
"v_max3_f32 v55,v55,v126,v127\n"
"v_max3_f32 v56,v56,v130,v131\n"
"v_mov_b32 v57,v52\n"
"v_mov_b32 v58,v53\n"
"v_mov_b32 v59,v54\n"
"v_mov_b32 v60,v55\n"
"v_mov_b32 v61,v56\n"
"v_permlane16_swap_b32 v52,v57\n"
"v_permlane16_swap_b32 v53,v58\n"
"v_permlane16_swap_b32 v54,v59\n"
"v_permlane16_swap_b32 v55,v60\n"
"v_permlane16_swap_b32 v56,v61\n"
"v_max_f32 v52,v52,v57\n"
"v_max_f32 v53,v53,v58\n"
"v_max_f32 v54,v54,v59\n"
"v_max_f32 v55,v55,v60\n"
"v_max_f32 v56,v56,v61\n"
"v_mov_b32 v57,v52\n"
"v_mov_b32 v58,v53\n"
"v_mov_b32 v59,v54\n"
"v_mov_b32 v60,v55\n"
"v_mov_b32 v61,v56\n"
"v_permlane32_swap_b32 v52,v57\n"
"v_permlane32_swap_b32 v53,v58\n"
"v_permlane32_swap_b32 v54,v59\n"
"v_permlane32_swap_b32 v55,v60\n"
"v_permlane32_swap_b32 v56,v61\n"
"v_max_f32 v52,v52,v57\n"
"v_max_f32 v53,v53,v58\n"
"v_max_f32 v54,v54,v59\n"
"v_max_f32 v55,v55,v60\n"
"v_max_f32 v56,v56,v61\n"
"v_sub_f32 v32,0xc1300000,v52\n"
"v_sub_f32 v36,0xc1300000,v53\n"
"v_sub_f32 v40,0xc1300000,v54\n"
"v_sub_f32 v44,0xc1300000,v55\n"
"v_sub_f32 v48,0xc1300000,v56\n"
"v_mov_b32 v33,v32\n"
"v_mov_b32 v34,v32\n"
"v_mov_b32 v35,v32\n"
"v_mov_b32 v37,v36\n"
"v_mov_b32 v38,v36\n"
"v_mov_b32 v39,v36\n"
"v_mov_b32 v41,v40\n"
"v_mov_b32 v42,v40\n"
"v_mov_b32 v43,v40\n"
"v_mov_b32 v45,v44\n"
"v_mov_b32 v46,v44\n"
"v_mov_b32 v47,v44\n"
"v_mov_b32 v49,v48\n"
"v_mov_b32 v50,v48\n"
"v_mov_b32 v51,v48\n"
"v_add_f32 v72,v72,v32\n"
"v_add_f32 v73,v73,v32\n"
"v_add_f32 v74,v74,v32\n"
"v_add_f32 v75,v75,v32\n"
"v_add_f32 v112,v112,v32\n"
"v_add_f32 v113,v113,v32\n"
"v_add_f32 v114,v114,v32\n"
"v_add_f32 v115,v115,v32\n"
"v_exp_f32 v72,v72\n"
"v_exp_f32 v73,v73\n"
"v_exp_f32 v74,v74\n"
"v_exp_f32 v75,v75\n"
"v_exp_f32 v112,v112\n"
"v_exp_f32 v113,v113\n"
"v_exp_f32 v114,v114\n"
"v_exp_f32 v115,v115\n"
"v_add_f32 v52,v72,v73\n"
"v_add_f32 v52,v52,v74\n"
"v_add_f32 v52,v52,v75\n"
"v_add_f32 v52,v52,v112\n"
"v_add_f32 v52,v52,v113\n"
"v_add_f32 v52,v52,v114\n"
"v_add_f32 v52,v52,v115\n"
CV "v72,v72,v73\n"
CV "v73,v74,v75\n"
CV "v74,v112,v113\n"
CV "v75,v114,v115\n"
"v_add_f32 v76,v76,v36\n"
"v_add_f32 v77,v77,v36\n"
"v_add_f32 v78,v78,v36\n"
"v_add_f32 v79,v79,v36\n"
"v_add_f32 v116,v116,v36\n"
"v_add_f32 v117,v117,v36\n"
"v_add_f32 v118,v118,v36\n"
"v_add_f32 v119,v119,v36\n"
"v_exp_f32 v76,v76\n"
"v_exp_f32 v77,v77\n"
"v_exp_f32 v78,v78\n"
"v_exp_f32 v79,v79\n"
"v_exp_f32 v116,v116\n"
"v_exp_f32 v117,v117\n"
"v_exp_f32 v118,v118\n"
"v_exp_f32 v119,v119\n"
"v_add_f32 v53,v76,v77\n"
"v_add_f32 v53,v53,v78\n"
"v_add_f32 v53,v53,v79\n"
"v_add_f32 v53,v53,v116\n"
"v_add_f32 v53,v53,v117\n"
"v_add_f32 v53,v53,v118\n"
"v_add_f32_e64 v53,v53,v119\n"
CV "v76,v76,v77\n"
CV "v77,v78,v79\n"
CV "v78,v116,v117\n"
CV "v79,v118,v119\n"
"v_add_f32 v80,v80,v40\n"
"v_add_f32 v81,v81,v40\n"
"v_add_f32 v82,v82,v40\n"
"v_add_f32 v83,v83,v40\n"
"v_add_f32 v120,v120,v40\n"
"v_add_f32 v121,v121,v40\n"
"v_add_f32 v122,v122,v40\n"
"v_add_f32 v123,v123,v40\n"
"v_exp_f32 v80,v80\n"
"v_exp_f32 v81,v81\n"
"v_exp_f32 v82,v82\n"
"v_exp_f32 v83,v83\n"
"v_exp_f32 v120,v120\n"
"v_exp_f32 v121,v121\n"
"v_exp_f32 v122,v122\n"
"v_exp_f32 v123,v123\n"
"v_add_f32 v54,v80,v81\n"
"v_add_f32 v54,v54,v82\n"
"v_add_f32 v54,v54,v83\n"
"v_add_f32 v54,v54,v120\n"
"v_add_f32 v54,v54,v121\n"
"v_add_f32 v54,v54,v122\n"
"v_add_f32_e64 v54,v54,v123\n"
CV "v80,v80,v81\n"
CV "v81,v82,v83\n"
CV "v82,v120,v121\n"
CV "v83,v122,v123\n"
"v_add_f32 v84,v84,v44\n"
"v_add_f32 v85,v85,v44\n"
"v_add_f32 v86,v86,v44\n"
"v_add_f32 v87,v87,v44\n"
"v_add_f32 v124,v124,v44\n"
"v_add_f32 v125,v125,v44\n"
"v_add_f32 v126,v126,v44\n"
"v_add_f32 v127,v127,v44\n"
"v_mov_b32 v55,0\n"
"v_add_f32 v88,v88,v48\n"
"v_add_f32 v89,v89,v48\n"
"v_add_f32 v90,v90,v48\n"
"v_add_f32 v91,v91,v48\n"
"v_add_f32 v128,v128,v48\n"
"v_add_f32 v129,v129,v48\n"
"v_add_f32 v130,v130,v48\n"
"v_add_f32 v131,v131,v48\n"
"v_mov_b32 v56,0\n"
"s_waitcnt vmcnt(18) lgkmcnt(0)\n"
"s_barrier\n"
"ds_read_b128 v[16:19],v0 offset:24576\n"
"ds_read_b128 v[20:23],v1 offset:24576\n"
"ds_read_b128 v[24:27],v0 offset:25600\n"
AW "a220,s13\n"
AW "a221,s13\n"
AW "a222,s13\n"
AW "a223,s13\n"
"s_waitcnt lgkmcnt(2)\n"
MF "v[92:95],v[16:19],v[136:139],v[32:35]\n"
"ds_read_b128 v[28:31],v1 offset:25600\n"
"s_waitcnt lgkmcnt(2)\n"
MF "v[92:95],v[20:23],v[156:159],v[92:95]\n"
"v_exp_f32 v84,v84\n"
MF "v[96:99],v[20:23],v[160:163],v[36:39]\n"
"v_exp_f32 v85,v85\n"
MF "v[96:99],v[16:19],v[140:143],v[96:99]\n"
"v_exp_f32 v86,v86\n"
MF "v[100:103],v[16:19],v[144:147],v[40:43]\n"
"v_exp_f32 v87,v87\n"
MF "v[100:103],v[20:23],v[164:167],v[100:103]\n"
"v_add_f32 v55,v55,v84\n"
"v_add_f32 v55,v55,v85\n"
MF "v[104:107],v[20:23],v[168:171],v[44:47]\n"
CV "v84,v84,v85\n"
"v_add_f32 v55,v55,v86\n"
MF "v[104:107],v[16:19],v[148:151],v[104:107]\n"
"v_add_f32 v55,v55,v87\n"
CV "v85,v86,v87\n"
MF "v[108:111],v[16:19],v[152:155],v[48:51]\n"
"v_exp_f32 v124,v124\n"
MF "v[108:111],v[20:23],v[172:175],v[108:111]\n"
"s_waitcnt lgkmcnt(0)\n"
"ds_read_b128 v[16:19],v0 offset:26624\n"
"ds_read_b128 v[20:23],v1 offset:26624\n"
MF "v[108:111],v[24:27],v[192:195],v[108:111]\n"
"v_exp_f32 v125,v125\n"
MF "v[108:111],v[28:31],v[212:215],v[108:111]\n"
"v_exp_f32 v126,v126\n"
MF "v[104:107],v[28:31],v[208:211],v[104:107]\n"
"v_exp_f32 v127,v127\n"
MF "v[104:107],v[24:27],v[188:191],v[104:107]\n"
"v_add_f32 v55,v55,v124\n"
"v_add_f32 v55,v55,v125\n"
MF "v[100:103],v[24:27],v[184:187],v[100:103]\n"
CV "v86,v124,v125\n"
"v_add_f32 v55,v55,v126\n"
MF "v[100:103],v[28:31],v[204:207],v[100:103]\n"
"v_add_f32 v55,v55,v127\n"
CV "v87,v126,v127\n"
MF "v[96:99],v[28:31],v[200:203],v[96:99]\n"
"v_exp_f32 v88,v88\n"
MF "v[96:99],v[24:27],v[180:183],v[96:99]\n"
"v_exp_f32 v89,v89\n"
MF "v[92:95],v[24:27],v[176:179],v[92:95]\n"
"v_exp_f32 v90,v90\n"
MF "v[92:95],v[28:31],v[196:199],v[92:95]\n"
"s_waitcnt lgkmcnt(0)\n"
"ds_read_b128 v[24:27],v0 offset:30720\n"
"ds_read_b128 v[28:31],v1 offset:30720\n"
MF "v[92:95],v[16:19],v[216:219],v[92:95]\n"
"v_exp_f32 v91,v91\n"
MF "v[92:95],v[20:23],v[236:239],v[92:95]\n"
"v_add_f32 v56,v56,v88\n"
"v_add_f32 v56,v56,v89\n"
MF "v[96:99],v[20:23],v[240:243],v[96:99]\n"
CV "v88,v88,v89\n"
"v_add_f32 v56,v56,v90\n"
MF "v[96:99],v[16:19],v[220:223],v[96:99]\n"
"v_add_f32 v56,v56,v91\n"
CV "v89,v90,v91\n"
MF "v[100:103],v[16:19],v[224:227],v[100:103]\n"
"v_exp_f32 v128,v128\n"
MF "v[100:103],v[20:23],v[244:247],v[100:103]\n"
"v_exp_f32 v129,v129\n"
MF "v[104:107],v[20:23],v[248:251],v[104:107]\n"
"v_exp_f32 v130,v130\n"
MF "v[104:107],v[16:19],v[228:231],v[104:107]\n"
"v_exp_f32 v131,v131\n"
MF "v[108:111],v[16:19],v[232:235],v[108:111]\n"
"v_add_f32 v56,v56,v128\n"
"v_add_f32 v56,v56,v129\n"
MF "v[108:111],v[20:23],v[252:255],v[108:111]\n"
"s_waitcnt lgkmcnt(0)\n"
"ds_read_b128 v[16:19],v0 offset:31744\n"
"ds_read_b128 v[20:23],v1 offset:31744\n"
MF "v[112:115],v[24:27],v[136:139],v[32:35]\n"
CV "v90,v128,v129\n"
"v_add_f32 v56,v56,v130\n"
MF "v[112:115],v[28:31],v[156:159],v[112:115]\n"
"v_add_f32 v56,v56,v131\n"
CV "v91,v130,v131\n"
MF "v[116:119],v[28:31],v[160:163],v[36:39]\n"
"v_exp_f32 v92,v92\n"
MF "v[116:119],v[24:27],v[140:143],v[116:119]\n"
"v_exp_f32 v93,v93\n"
MF "v[120:123],v[24:27],v[144:147],v[40:43]\n"
"v_exp_f32 v94,v94\n"
MF "v[120:123],v[28:31],v[164:167],v[120:123]\n"
"v_exp_f32 v95,v95\n"
MF "v[124:127],v[28:31],v[168:171],v[44:47]\n"
"v_add_f32 v57,v52,v92\n"
"v_add_f32 v57,v57,v93\n"
MF "v[124:127],v[24:27],v[148:151],v[124:127]\n"
CV "v92,v92,v93\n"
"v_add_f32 v57,v57,v94\n"
MF "v[128:131],v[24:27],v[152:155],v[48:51]\n"
"v_add_f32 v57,v57,v95\n"
CV "v93,v94,v95\n"
MF "v[128:131],v[28:31],v[172:175],v[128:131]\n"
"s_waitcnt lgkmcnt(0)\n"
"ds_read_b128 v[24:27],v0 offset:32768\n"
"ds_read_b128 v[28:31],v1 offset:32768\n"
MF "v[128:131],v[16:19],v[192:195],v[128:131]\n"
"v_exp_f32 v96,v96\n"
MF "v[128:131],v[20:23],v[212:215],v[128:131]\n"
"v_exp_f32 v97,v97\n"
MF "v[124:127],v[20:23],v[208:211],v[124:127]\n"
"v_exp_f32 v98,v98\n"
MF "v[124:127],v[16:19],v[188:191],v[124:127]\n"
"v_exp_f32 v99,v99\n"
MF "v[120:123],v[16:19],v[184:187],v[120:123]\n"
"v_add_f32 v58,v53,v96\n"
"v_add_f32 v58,v58,v97\n"
MF "v[120:123],v[20:23],v[204:207],v[120:123]\n"
CV "v96,v96,v97\n"
"v_add_f32 v58,v58,v98\n"
MF "v[116:119],v[20:23],v[200:203],v[116:119]\n"
"v_add_f32 v58,v58,v99\n"
CV "v97,v98,v99\n"
MF "v[116:119],v[16:19],v[180:183],v[116:119]\n"
"s_waitcnt vmcnt(12) lgkmcnt(0)\n"
"s_barrier\n"
MF "v[112:115],v[16:19],v[176:179],v[112:115]\n"
"v_exp_f32 v100,v100\n"
MF "v[112:115],v[20:23],v[196:199],v[112:115]\n"
"s_nop 0\n"
TR "v[16:17],v2\n"
TR "v[18:19],v2 offset:6144\n"
MF "v[112:115],v[24:27],v[216:219],v[112:115]\n"
"s_add_u32 s72,s72,6144\n"
TR "v[20:21],v3\n"
TR "v[22:23],v3 offset:6144\n"
MF "v[112:115],v[28:31],v[236:239],v[112:115]\n"
"s_cmp_eq_u32 s72,s17\n"
"s_cselect_b32 s72,s16,s72\n"
"s_add_u32 m0,s64,24576\n"
MF "v[116:119],v[28:31],v[240:243],v[116:119]\n"
LD "v7,s[32:35],s72 offen nt lds\n"
MF "v[116:119],v[24:27],v[220:223],v[116:119]\n"
"s_add_u32 m0,s64,25472\n"
"v_exp_f32 v101,v101\n"
MF "v[120:123],v[24:27],v[224:227],v[120:123]\n"
LD "v7,s[32:35],s72 offen offset:128 nt lds\n"
MF "v[120:123],v[28:31],v[244:247],v[120:123]\n"
"v_exp_f32 v102,v102\n"
MF "v[124:127],v[28:31],v[248:251],v[124:127]\n"
"s_add_u32 m0,s64,26368\n"
"v_exp_f32 v103,v103\n"
MF "v[124:127],v[24:27],v[228:231],v[124:127]\n"
LD "v7,s[32:35],s72 offen offset:256 nt lds\n"
MF "v[128:131],v[24:27],v[232:235],v[128:131]\n"
"s_add_u32 s72,s72,6144\n"
"v_add_f32 v59,v54,v100\n"
"v_add_f32 v59,v59,v101\n"
MF "v[128:131],v[28:31],v[252:255],v[128:131]\n"
"s_waitcnt lgkmcnt(2)\n"
TR "v[24:25],v4\n"
TR "v[26:27],v4 offset:6144\n"
MF "a[0:3],v[16:19],v[72:75],a[220:223]\n"
"s_add_u32 m0,s64,30720\n"
TR "v[28:29],v5\n"
TR "v[30:31],v5 offset:6144\n"
MF "a[4:7],v[16:19],v[76:79],a[220:223]\n"
LD "v7,s[32:35],s72 offen nt lds\n"
MF "a[8:11],v[16:19],v[80:83],a[220:223]\n"
"s_add_u32 m0,s64,31616\n"
CV "v100,v100,v101\n"
"v_add_f32 v59,v59,v102\n"
MF "a[12:15],v[16:19],v[84:87],a[220:223]\n"
LD "v7,s[32:35],s72 offen offset:128 nt lds\n"
MF "a[16:19],v[16:19],v[88:91],a[220:223]\n"
"s_waitcnt lgkmcnt(4)\n"
TR "v[16:17],v2 offset:1024\n"
TR "v[18:19],v2 offset:7168\n"
MF "a[36:39],v[20:23],v[88:91],a[220:223]\n"
"v_add_f32 v59,v59,v103\n"
CV "v101,v102,v103\n"
MF "a[32:35],v[20:23],v[84:87],a[220:223]\n"
"s_add_u32 m0,s64,32512\n"
"v_exp_f32 v104,v104\n"
MF "a[28:31],v[20:23],v[80:83],a[220:223]\n"
LD "v7,s[32:35],s72 offen offset:256 nt lds\n"
MF "a[24:27],v[20:23],v[76:79],a[220:223]\n"
"v_exp_f32 v105,v105\n"
MF "a[20:23],v[20:23],v[72:75],a[220:223]\n"
"s_waitcnt lgkmcnt(4)\n"
TR "v[20:21],v3 offset:1024\n"
TR "v[22:23],v3 offset:7168\n"
MF "a[40:43],v[24:27],v[72:75],a[220:223]\n"
"v_exp_f32 v106,v106\n"
MF "a[44:47],v[24:27],v[76:79],a[220:223]\n"
"v_exp_f32 v107,v107\n"
MF "a[48:51],v[24:27],v[80:83],a[220:223]\n"
"v_add_f32 v60,v55,v104\n"
"v_add_f32 v60,v60,v105\n"
MF "a[52:55],v[24:27],v[84:87],a[220:223]\n"
CV "v104,v104,v105\n"
"v_add_f32 v60,v60,v106\n"
MF "a[56:59],v[24:27],v[88:91],a[220:223]\n"
"s_waitcnt lgkmcnt(4)\n"
TR "v[24:25],v4 offset:1024\n"
TR "v[26:27],v4 offset:7168\n"
MF "a[76:79],v[28:31],v[88:91],a[220:223]\n"
"v_add_f32 v60,v60,v107\n"
CV "v105,v106,v107\n"
MF "a[72:75],v[28:31],v[84:87],a[220:223]\n"
"v_exp_f32 v108,v108\n"
MF "a[68:71],v[28:31],v[80:83],a[220:223]\n"
"v_exp_f32 v109,v109\n"
MF "a[64:67],v[28:31],v[76:79],a[220:223]\n"
"v_exp_f32 v110,v110\n"
MF "a[60:63],v[28:31],v[72:75],a[220:223]\n"
"s_waitcnt lgkmcnt(4)\n"
TR "v[28:29],v5 offset:1024\n"
TR "v[30:31],v5 offset:7168\n"
MF "a[80:83],v[16:19],v[72:75],a[220:223]\n"
"v_exp_f32 v111,v111\n"
MF "a[84:87],v[16:19],v[76:79],a[220:223]\n"
"v_add_f32 v61,v56,v108\n"
"v_add_f32 v61,v61,v109\n"
MF "a[88:91],v[16:19],v[80:83],a[220:223]\n"
CV "v108,v108,v109\n"
"v_add_f32 v61,v61,v110\n"
MF "a[92:95],v[16:19],v[84:87],a[220:223]\n"
"v_add_f32 v61,v61,v111\n"
CV "v109,v110,v111\n"
MF "a[96:99],v[16:19],v[88:91],a[220:223]\n"
"s_waitcnt lgkmcnt(4)\n"
TR "v[16:17],v2 offset:2048\n"
TR "v[18:19],v2 offset:8192\n"
MF "a[116:119],v[20:23],v[88:91],a[220:223]\n"
"v_exp_f32 v112,v112\n"
MF "a[112:115],v[20:23],v[84:87],a[220:223]\n"
"v_exp_f32 v113,v113\n"
MF "a[108:111],v[20:23],v[80:83],a[220:223]\n"
"v_exp_f32 v114,v114\n"
MF "a[104:107],v[20:23],v[76:79],a[220:223]\n"
"v_exp_f32 v115,v115\n"
MF "a[100:103],v[20:23],v[72:75],a[220:223]\n"
"s_waitcnt lgkmcnt(4)\n"
TR "v[20:21],v3 offset:2048\n"
TR "v[22:23],v3 offset:8192\n"
MF "a[120:123],v[24:27],v[72:75],a[220:223]\n"
"v_add_f32 v57,v57,v112\n"
"v_add_f32 v57,v57,v113\n"
MF "a[124:127],v[24:27],v[76:79],a[220:223]\n"
CV "v94,v112,v113\n"
"v_add_f32 v57,v57,v114\n"
MF "a[128:131],v[24:27],v[80:83],a[220:223]\n"
"v_add_f32 v57,v57,v115\n"
CV "v95,v114,v115\n"
MF "a[132:135],v[24:27],v[84:87],a[220:223]\n"
"v_exp_f32 v116,v116\n"
MF "a[136:139],v[24:27],v[88:91],a[220:223]\n"
"s_waitcnt lgkmcnt(4)\n"
TR "v[24:25],v4 offset:2048\n"
TR "v[26:27],v4 offset:8192\n"
MF "a[156:159],v[28:31],v[88:91],a[220:223]\n"
"v_exp_f32 v117,v117\n"
MF "a[152:155],v[28:31],v[84:87],a[220:223]\n"
"v_exp_f32 v118,v118\n"
MF "a[148:151],v[28:31],v[80:83],a[220:223]\n"
"v_exp_f32 v119,v119\n"
MF "a[144:147],v[28:31],v[76:79],a[220:223]\n"
"v_add_f32 v58,v58,v116\n"
"v_add_f32 v58,v58,v117\n"
MF "a[140:143],v[28:31],v[72:75],a[220:223]\n"
"s_waitcnt lgkmcnt(4)\n"
TR "v[28:29],v5 offset:2048\n"
TR "v[30:31],v5 offset:8192\n"
MF "a[160:163],v[16:19],v[72:75],a[220:223]\n"
CV "v98,v116,v117\n"
"v_add_f32 v58,v58,v118\n"
MF "a[164:167],v[16:19],v[76:79],a[220:223]\n"
"v_add_f32 v58,v58,v119\n"
CV "v99,v118,v119\n"
MF "a[168:171],v[16:19],v[80:83],a[220:223]\n"
"v_exp_f32 v120,v120\n"
MF "a[172:175],v[16:19],v[84:87],a[220:223]\n"
"v_exp_f32 v121,v121\n"
MF "a[176:179],v[16:19],v[88:91],a[220:223]\n"
"s_waitcnt lgkmcnt(4)\n"
"v_exp_f32 v122,v122\n"
MF "a[196:199],v[20:23],v[88:91],a[220:223]\n"
"v_exp_f32 v123,v123\n"
MF "a[192:195],v[20:23],v[84:87],a[220:223]\n"
"v_add_f32 v59,v59,v120\n"
"v_add_f32 v59,v59,v121\n"
MF "a[188:191],v[20:23],v[80:83],a[220:223]\n"
CV "v102,v120,v121\n"
"v_add_f32 v59,v59,v122\n"
MF "a[184:187],v[20:23],v[76:79],a[220:223]\n"
"s_waitcnt vmcnt(6) lgkmcnt(0)\n"
"s_barrier\n"
MF "a[180:183],v[20:23],v[72:75],a[220:223]\n"
"s_add_u32 s73,s73,12288\n"
"ds_read_b128 v[16:19],v0\n"
"ds_read_b128 v[20:23],v1\n"
MF "a[200:203],v[24:27],v[72:75],a[220:223]\n"
"s_cmp_eq_u32 s73,s19\n"
"s_cselect_b32 s73,s18,s73\n"
"s_add_u32 m0,s65,49152\n"
MF "a[204:207],v[24:27],v[76:79],a[220:223]\n"
LD "v8,s[36:39],s73 offen nt lds\n"
MF "a[208:211],v[24:27],v[80:83],a[220:223]\n"
"s_add_u32 m0,s65,50048\n"
"v_add_f32 v59,v59,v123\n"
CV "v103,v122,v123\n"
MF "a[212:215],v[24:27],v[84:87],a[220:223]\n"
LD "v8,s[36:39],s73 offen offset:128 nt lds\n"
MF "a[216:219],v[24:27],v[88:91],a[220:223]\n"
"s_add_u32 m0,s65,50944\n"
"ds_read_b128 v[24:27],v0 offset:1024\n"
MF "a[236:239],v[28:31],v[88:91],a[220:223]\n"
LD "v8,s[36:39],s73 offen offset:256 nt lds\n"
MF "a[232:235],v[28:31],v[84:87],a[220:223]\n"
MF "a[228:231],v[28:31],v[80:83],a[220:223]\n"
MF "a[224:227],v[28:31],v[76:79],a[220:223]\n"
MF "a[220:223],v[28:31],v[72:75],a[220:223]\n"
"s_waitcnt lgkmcnt(1)\n"
".Laloopx:\n"
MF "v[72:75],v[16:19],v[136:139],v[32:35]\n"
"ds_read_b128 v[28:31],v1 offset:1024\n"
MF "v[72:75],v[20:23],v[156:159],v[72:75]\n"
MF "v[76:79],v[20:23],v[160:163],v[36:39]\n"
MF "v[76:79],v[16:19],v[140:143],v[76:79]\n"
MF "v[80:83],v[16:19],v[144:147],v[40:43]\n"
MF "v[80:83],v[20:23],v[164:167],v[80:83]\n"
MF "v[84:87],v[20:23],v[168:171],v[44:47]\n"
MF "v[84:87],v[16:19],v[148:151],v[84:87]\n"
"s_add_u32 m0,s65,49152\n"
MF "v[88:91],v[16:19],v[152:155],v[48:51]\n"
LD "v8,s[36:39],s73 offen offset:3072 nt lds\n"
MF "v[88:91],v[20:23],v[172:175],v[88:91]\n"
"s_waitcnt lgkmcnt(0)\n"
"ds_read_b128 v[16:19],v0 offset:2048\n"
"ds_read_b128 v[20:23],v1 offset:2048\n"
MF "v[88:91],v[24:27],v[192:195],v[88:91]\n"
"v_exp_f32 v124,v124\n"
MF "v[88:91],v[28:31],v[212:215],v[88:91]\n"
"s_add_u32 m0,s65,50048\n"
"v_exp_f32 v125,v125\n"
MF "v[84:87],v[28:31],v[208:211],v[84:87]\n"
LD "v8,s[36:39],s73 offen offset:3200 nt lds\n"
MF "v[84:87],v[24:27],v[188:191],v[84:87]\n"
"v_exp_f32 v126,v126\n"
MF "v[80:83],v[24:27],v[184:187],v[80:83]\n"
"v_exp_f32 v127,v127\n"
MF "v[80:83],v[28:31],v[204:207],v[80:83]\n"
"s_add_u32 m0,s65,50944\n"
"v_add_f32 v60,v60,v124\n"
"v_add_f32 v60,v60,v125\n"
MF "v[76:79],v[28:31],v[200:203],v[76:79]\n"
LD "v8,s[36:39],s73 offen offset:3328 nt lds\n"
MF "v[76:79],v[24:27],v[180:183],v[76:79]\n"
CV "v106,v124,v125\n"
"v_add_f32 v60,v60,v126\n"
MF "v[72:75],v[24:27],v[176:179],v[72:75]\n"
"v_add_f32 v60,v60,v127\n"
CV "v107,v126,v127\n"
MF "v[72:75],v[28:31],v[196:199],v[72:75]\n"
"s_waitcnt lgkmcnt(0)\n"
"ds_read_b128 v[24:27],v0 offset:6144\n"
"ds_read_b128 v[28:31],v1 offset:6144\n"
MF "v[72:75],v[16:19],v[216:219],v[72:75]\n"
"v_exp_f32 v128,v128\n"
MF "v[72:75],v[20:23],v[236:239],v[72:75]\n"
"v_exp_f32 v129,v129\n"
MF "v[76:79],v[20:23],v[240:243],v[76:79]\n"
"v_exp_f32 v130,v130\n"
MF "v[76:79],v[16:19],v[220:223],v[76:79]\n"
"v_exp_f32 v131,v131\n"
MF "v[80:83],v[16:19],v[224:227],v[80:83]\n"
"v_add_f32 v61,v61,v128\n"
"v_add_f32 v61,v61,v129\n"
MF "v[80:83],v[20:23],v[244:247],v[80:83]\n"
CV "v110,v128,v129\n"
"v_add_f32 v61,v61,v130\n"
MF "v[84:87],v[20:23],v[248:251],v[84:87]\n"
"v_add_f32 v61,v61,v131\n"
CV "v111,v130,v131\n"
MF "v[84:87],v[16:19],v[228:231],v[84:87]\n"
"v_max3_f32 v10,v57,v58,v59\n"
"v_max3_f32 v10,v10,v60,v61\n"
MF "v[88:91],v[16:19],v[232:235],v[88:91]\n"
"v_cmp_ngt_f32 vcc,s95,v10\n"
MF "v[88:91],v[20:23],v[252:255],v[88:91]\n"
"s_waitcnt lgkmcnt(0)\n"
"s_cbranch_vccnz .Lstub_6\n"
".Lback_6:\n"
MF "v[112:115],v[24:27],v[136:139],v[32:35]\n"
"s_add_u32 s74,s74,12288\n"
"ds_read_b128 v[16:19],v0 offset:7168\n"
"ds_read_b128 v[20:23],v1 offset:7168\n"
MF "v[112:115],v[28:31],v[156:159],v[112:115]\n"
"s_cmp_eq_u32 s74,s20\n"
"s_cselect_b32 s74,s70,s74\n"
MF "v[116:119],v[28:31],v[160:163],v[36:39]\n"
"v_exp_f32 v72,v72\n"
MF "v[116:119],v[24:27],v[140:143],v[116:119]\n"
"v_exp_f32 v73,v73\n"
MF "v[120:123],v[24:27],v[144:147],v[40:43]\n"
"v_exp_f32 v74,v74\n"
MF "v[120:123],v[28:31],v[164:167],v[120:123]\n"
"v_exp_f32 v75,v75\n"
MF "v[124:127],v[28:31],v[168:171],v[44:47]\n"
"v_add_f32 v52,v57,v72\n"
"v_add_f32 v52,v52,v73\n"
MF "v[124:127],v[24:27],v[148:151],v[124:127]\n"
CV "v72,v72,v73\n"
"v_add_f32 v52,v52,v74\n"
MF "v[128:131],v[24:27],v[152:155],v[48:51]\n"
"v_add_f32 v52,v52,v75\n"
CV "v73,v74,v75\n"
MF "v[128:131],v[28:31],v[172:175],v[128:131]\n"
"s_waitcnt lgkmcnt(0)\n"
"ds_read_b128 v[24:27],v0 offset:8192\n"
"ds_read_b128 v[28:31],v1 offset:8192\n"
MF "v[128:131],v[16:19],v[192:195],v[128:131]\n"
"v_exp_f32 v76,v76\n"
MF "v[128:131],v[20:23],v[212:215],v[128:131]\n"
"v_exp_f32 v77,v77\n"
MF "v[124:127],v[20:23],v[208:211],v[124:127]\n"
"v_exp_f32 v78,v78\n"
MF "v[124:127],v[16:19],v[188:191],v[124:127]\n"
"v_exp_f32 v79,v79\n"
MF "v[120:123],v[16:19],v[184:187],v[120:123]\n"
"v_add_f32 v53,v58,v76\n"
"v_add_f32 v53,v53,v77\n"
MF "v[120:123],v[20:23],v[204:207],v[120:123]\n"
CV "v76,v76,v77\n"
"v_add_f32 v53,v53,v78\n"
MF "v[116:119],v[20:23],v[200:203],v[116:119]\n"
"v_add_f32 v53,v53,v79\n"
CV "v77,v78,v79\n"
MF "v[116:119],v[16:19],v[180:183],v[116:119]\n"
"s_waitcnt vmcnt(12) lgkmcnt(0)\n"
"s_barrier\n"
MF "v[112:115],v[16:19],v[176:179],v[112:115]\n"
"v_exp_f32 v80,v80\n"
MF "v[112:115],v[20:23],v[196:199],v[112:115]\n"
"s_nop 0\n"
TR "v[16:17],v2 offset:24576\n"
TR "v[18:19],v2 offset:30720\n"
MF "v[112:115],v[24:27],v[216:219],v[112:115]\n"
"s_add_u32 s72,s72,6144\n"
TR "v[20:21],v3 offset:24576\n"
TR "v[22:23],v3 offset:30720\n"
MF "v[112:115],v[28:31],v[236:239],v[112:115]\n"
"s_cmp_eq_u32 s72,s17\n"
"s_cselect_b32 s72,s16,s72\n"
"s_add_u32 m0,s64,0\n"
MF "v[116:119],v[28:31],v[240:243],v[116:119]\n"
LD "v7,s[32:35],s72 offen nt lds\n"
MF "v[116:119],v[24:27],v[220:223],v[116:119]\n"
"s_add_u32 m0,s64,896\n"
"v_exp_f32 v81,v81\n"
MF "v[120:123],v[24:27],v[224:227],v[120:123]\n"
LD "v7,s[32:35],s72 offen offset:128 nt lds\n"
MF "v[120:123],v[28:31],v[244:247],v[120:123]\n"
"v_exp_f32 v82,v82\n"
MF "v[124:127],v[28:31],v[248:251],v[124:127]\n"
"s_add_u32 m0,s64,1792\n"
"v_exp_f32 v83,v83\n"
MF "v[124:127],v[24:27],v[228:231],v[124:127]\n"
LD "v7,s[32:35],s72 offen offset:256 nt lds\n"
MF "v[128:131],v[24:27],v[232:235],v[128:131]\n"
"s_add_u32 s72,s72,6144\n"
"v_add_f32 v54,v59,v80\n"
"v_add_f32 v54,v54,v81\n"
MF "v[128:131],v[28:31],v[252:255],v[128:131]\n"
"s_waitcnt lgkmcnt(2)\n"
TR "v[24:25],v4 offset:24576\n"
TR "v[26:27],v4 offset:30720\n"
MF "a[0:3],v[16:19],v[92:95],a[0:3]\n"
"s_add_u32 m0,s64,6144\n"
TR "v[28:29],v5 offset:24576\n"
TR "v[30:31],v5 offset:30720\n"
MF "a[4:7],v[16:19],v[96:99],a[4:7]\n"
LD "v7,s[32:35],s72 offen nt lds\n"
MF "a[8:11],v[16:19],v[100:103],a[8:11]\n"
"s_add_u32 m0,s64,7040\n"
CV "v80,v80,v81\n"
"v_add_f32 v54,v54,v82\n"
MF "a[12:15],v[16:19],v[104:107],a[12:15]\n"
LD "v7,s[32:35],s72 offen offset:128 nt lds\n"
MF "a[16:19],v[16:19],v[108:111],a[16:19]\n"
"s_waitcnt lgkmcnt(4)\n"
TR "v[16:17],v2 offset:25600\n"
TR "v[18:19],v2 offset:31744\n"
MF "a[36:39],v[20:23],v[108:111],a[36:39]\n"
"v_add_f32 v54,v54,v83\n"
CV "v81,v82,v83\n"
MF "a[32:35],v[20:23],v[104:107],a[32:35]\n"
"s_add_u32 m0,s64,7936\n"
"v_exp_f32 v84,v84\n"
MF "a[28:31],v[20:23],v[100:103],a[28:31]\n"
LD "v7,s[32:35],s72 offen offset:256 nt lds\n"
MF "a[24:27],v[20:23],v[96:99],a[24:27]\n"
"v_exp_f32 v85,v85\n"
MF "a[20:23],v[20:23],v[92:95],a[20:23]\n"
"s_waitcnt lgkmcnt(4)\n"
TR "v[20:21],v3 offset:25600\n"
TR "v[22:23],v3 offset:31744\n"
MF "a[40:43],v[24:27],v[92:95],a[40:43]\n"
"v_exp_f32 v86,v86\n"
MF "a[44:47],v[24:27],v[96:99],a[44:47]\n"
"v_exp_f32 v87,v87\n"
MF "a[48:51],v[24:27],v[100:103],a[48:51]\n"
"v_add_f32 v55,v60,v84\n"
"v_add_f32 v55,v55,v85\n"
MF "a[52:55],v[24:27],v[104:107],a[52:55]\n"
CV "v84,v84,v85\n"
"v_add_f32 v55,v55,v86\n"
MF "a[56:59],v[24:27],v[108:111],a[56:59]\n"
"s_waitcnt lgkmcnt(4)\n"
TR "v[24:25],v4 offset:25600\n"
TR "v[26:27],v4 offset:31744\n"
MF "a[76:79],v[28:31],v[108:111],a[76:79]\n"
"v_add_f32 v55,v55,v87\n"
CV "v85,v86,v87\n"
MF "a[72:75],v[28:31],v[104:107],a[72:75]\n"
"v_exp_f32 v88,v88\n"
MF "a[68:71],v[28:31],v[100:103],a[68:71]\n"
"v_exp_f32 v89,v89\n"
MF "a[64:67],v[28:31],v[96:99],a[64:67]\n"
"v_exp_f32 v90,v90\n"
MF "a[60:63],v[28:31],v[92:95],a[60:63]\n"
"s_waitcnt lgkmcnt(4)\n"
TR "v[28:29],v5 offset:25600\n"
TR "v[30:31],v5 offset:31744\n"
MF "a[80:83],v[16:19],v[92:95],a[80:83]\n"
"v_exp_f32 v91,v91\n"
MF "a[84:87],v[16:19],v[96:99],a[84:87]\n"
"v_add_f32 v56,v61,v88\n"
"v_add_f32 v56,v56,v89\n"
MF "a[88:91],v[16:19],v[100:103],a[88:91]\n"
CV "v88,v88,v89\n"
"v_add_f32 v56,v56,v90\n"
MF "a[92:95],v[16:19],v[104:107],a[92:95]\n"
"v_add_f32 v56,v56,v91\n"
CV "v89,v90,v91\n"
MF "a[96:99],v[16:19],v[108:111],a[96:99]\n"
"s_waitcnt lgkmcnt(4)\n"
TR "v[16:17],v2 offset:26624\n"
TR "v[18:19],v2 offset:32768\n"
MF "a[116:119],v[20:23],v[108:111],a[116:119]\n"
"v_exp_f32 v112,v112\n"
MF "a[112:115],v[20:23],v[104:107],a[112:115]\n"
"v_exp_f32 v113,v113\n"
MF "a[108:111],v[20:23],v[100:103],a[108:111]\n"
"v_exp_f32 v114,v114\n"
MF "a[104:107],v[20:23],v[96:99],a[104:107]\n"
"v_exp_f32 v115,v115\n"
MF "a[100:103],v[20:23],v[92:95],a[100:103]\n"
"s_waitcnt lgkmcnt(4)\n"
TR "v[20:21],v3 offset:26624\n"
TR "v[22:23],v3 offset:32768\n"
MF "a[120:123],v[24:27],v[92:95],a[120:123]\n"
"v_add_f32 v52,v52,v112\n"
"v_add_f32 v52,v52,v113\n"
MF "a[124:127],v[24:27],v[96:99],a[124:127]\n"
CV "v74,v112,v113\n"
"v_add_f32 v52,v52,v114\n"
MF "a[128:131],v[24:27],v[100:103],a[128:131]\n"
"v_add_f32 v52,v52,v115\n"
CV "v75,v114,v115\n"
MF "a[132:135],v[24:27],v[104:107],a[132:135]\n"
"v_exp_f32 v116,v116\n"
MF "a[136:139],v[24:27],v[108:111],a[136:139]\n"
"s_waitcnt lgkmcnt(4)\n"
TR "v[24:25],v4 offset:26624\n"
TR "v[26:27],v4 offset:32768\n"
MF "a[156:159],v[28:31],v[108:111],a[156:159]\n"
"v_exp_f32 v117,v117\n"
MF "a[152:155],v[28:31],v[104:107],a[152:155]\n"
"v_exp_f32 v118,v118\n"
MF "a[148:151],v[28:31],v[100:103],a[148:151]\n"
"v_exp_f32 v119,v119\n"
MF "a[144:147],v[28:31],v[96:99],a[144:147]\n"
"v_add_f32 v53,v53,v116\n"
"v_add_f32 v53,v53,v117\n"
MF "a[140:143],v[28:31],v[92:95],a[140:143]\n"
"s_waitcnt lgkmcnt(4)\n"
TR "v[28:29],v5 offset:26624\n"
TR "v[30:31],v5 offset:32768\n"
MF "a[160:163],v[16:19],v[92:95],a[160:163]\n"
CV "v78,v116,v117\n"
"v_add_f32 v53,v53,v118\n"
MF "a[164:167],v[16:19],v[96:99],a[164:167]\n"
"v_add_f32 v53,v53,v119\n"
CV "v79,v118,v119\n"
MF "a[168:171],v[16:19],v[100:103],a[168:171]\n"
"v_exp_f32 v120,v120\n"
MF "a[172:175],v[16:19],v[104:107],a[172:175]\n"
"v_exp_f32 v121,v121\n"
MF "a[176:179],v[16:19],v[108:111],a[176:179]\n"
"s_waitcnt lgkmcnt(4)\n"
"v_exp_f32 v122,v122\n"
MF "a[196:199],v[20:23],v[108:111],a[196:199]\n"
"v_exp_f32 v123,v123\n"
MF "a[192:195],v[20:23],v[104:107],a[192:195]\n"
"v_add_f32 v54,v54,v120\n"
"v_add_f32 v54,v54,v121\n"
MF "a[188:191],v[20:23],v[100:103],a[188:191]\n"
CV "v82,v120,v121\n"
"v_add_f32 v54,v54,v122\n"
MF "a[184:187],v[20:23],v[96:99],a[184:187]\n"
"s_waitcnt vmcnt(12) lgkmcnt(0)\n"
"s_barrier\n"
MF "a[180:183],v[20:23],v[92:95],a[180:183]\n"
"s_add_u32 s73,s73,12288\n"
"ds_read_b128 v[16:19],v0 offset:24576\n"
"ds_read_b128 v[20:23],v1 offset:24576\n"
MF "a[200:203],v[24:27],v[92:95],a[200:203]\n"
"s_cmp_eq_u32 s73,s19\n"
"s_cselect_b32 s73,s18,s73\n"
"s_add_u32 m0,s65,73728\n"
MF "a[204:207],v[24:27],v[96:99],a[204:207]\n"
LD "v8,s[36:39],s73 offen nt lds\n"
MF "a[208:211],v[24:27],v[100:103],a[208:211]\n"
"s_add_u32 m0,s65,74624\n"
"v_add_f32 v54,v54,v123\n"
CV "v83,v122,v123\n"
MF "a[212:215],v[24:27],v[104:107],a[212:215]\n"
LD "v8,s[36:39],s73 offen offset:128 nt lds\n"
MF "a[216:219],v[24:27],v[108:111],a[216:219]\n"
"s_add_u32 m0,s65,75520\n"
"ds_read_b128 v[24:27],v0 offset:25600\n"
MF "a[236:239],v[28:31],v[108:111],a[236:239]\n"
LD "v8,s[36:39],s73 offen offset:256 nt lds\n"
MF "a[232:235],v[28:31],v[104:107],a[232:235]\n"
MF "a[228:231],v[28:31],v[100:103],a[228:231]\n"
MF "a[224:227],v[28:31],v[96:99],a[224:227]\n"
MF "a[220:223],v[28:31],v[92:95],a[220:223]\n"
"s_waitcnt lgkmcnt(1)\n"
MF "v[92:95],v[16:19],v[136:139],v[32:35]\n"
"s_add_u32 m0,s65,73728\n"
"ds_read_b128 v[28:31],v1 offset:25600\n"
MF "v[92:95],v[20:23],v[156:159],v[92:95]\n"
LD "v8,s[36:39],s73 offen offset:3072 nt lds\n"
MF "v[96:99],v[20:23],v[160:163],v[36:39]\n"
"v_exp_f32 v124,v124\n"
MF "v[96:99],v[16:19],v[140:143],v[96:99]\n"
"v_exp_f32 v125,v125\n"
MF "v[100:103],v[16:19],v[144:147],v[40:43]\n"
"s_add_u32 m0,s65,74624\n"
"v_exp_f32 v126,v126\n"
MF "v[100:103],v[20:23],v[164:167],v[100:103]\n"
LD "v8,s[36:39],s73 offen offset:3200 nt lds\n"
MF "v[104:107],v[20:23],v[168:171],v[44:47]\n"
"v_exp_f32 v127,v127\n"
MF "v[104:107],v[16:19],v[148:151],v[104:107]\n"
"s_add_u32 m0,s65,75520\n"
"v_add_f32 v55,v55,v124\n"
"v_add_f32 v55,v55,v125\n"
MF "v[108:111],v[16:19],v[152:155],v[48:51]\n"
LD "v8,s[36:39],s73 offen offset:3328 nt lds\n"
MF "v[108:111],v[20:23],v[172:175],v[108:111]\n"
"s_waitcnt lgkmcnt(0)\n"
"ds_read_b128 v[16:19],v0 offset:26624\n"
"ds_read_b128 v[20:23],v1 offset:26624\n"
MF "v[108:111],v[24:27],v[192:195],v[108:111]\n"
CV "v86,v124,v125\n"
"v_add_f32 v55,v55,v126\n"
MF "v[108:111],v[28:31],v[212:215],v[108:111]\n"
"v_add_f32 v55,v55,v127\n"
CV "v87,v126,v127\n"
MF "v[104:107],v[28:31],v[208:211],v[104:107]\n"
"v_exp_f32 v128,v128\n"
MF "v[104:107],v[24:27],v[188:191],v[104:107]\n"
"v_exp_f32 v129,v129\n"
MF "v[100:103],v[24:27],v[184:187],v[100:103]\n"
"v_exp_f32 v130,v130\n"
MF "v[100:103],v[28:31],v[204:207],v[100:103]\n"
"v_exp_f32 v131,v131\n"
MF "v[96:99],v[28:31],v[200:203],v[96:99]\n"
"v_add_f32 v56,v56,v128\n"
"v_add_f32 v56,v56,v129\n"
MF "v[96:99],v[24:27],v[180:183],v[96:99]\n"
CV "v90,v128,v129\n"
"v_add_f32 v56,v56,v130\n"
MF "v[92:95],v[24:27],v[176:179],v[92:95]\n"
"v_add_f32 v56,v56,v131\n"
CV "v91,v130,v131\n"
MF "v[92:95],v[28:31],v[196:199],v[92:95]\n"
"s_waitcnt lgkmcnt(0)\n"
"ds_read_b128 v[24:27],v0 offset:30720\n"
"ds_read_b128 v[28:31],v1 offset:30720\n"
MF "v[92:95],v[16:19],v[216:219],v[92:95]\n"
MF "v[92:95],v[20:23],v[236:239],v[92:95]\n"
MF "v[96:99],v[20:23],v[240:243],v[96:99]\n"
MF "v[96:99],v[16:19],v[220:223],v[96:99]\n"
MF "v[100:103],v[16:19],v[224:227],v[100:103]\n"
MF "v[100:103],v[20:23],v[244:247],v[100:103]\n"
MF "v[104:107],v[20:23],v[248:251],v[104:107]\n"
MF "v[104:107],v[16:19],v[228:231],v[104:107]\n"
"v_max3_f32 v10,v52,v53,v54\n"
"v_max3_f32 v10,v10,v55,v56\n"
MF "v[108:111],v[16:19],v[232:235],v[108:111]\n"
"v_cmp_ngt_f32 vcc,s95,v10\n"
MF "v[108:111],v[20:23],v[252:255],v[108:111]\n"
"s_waitcnt lgkmcnt(0)\n"
"s_cbranch_vccnz .Lstub_7\n"
".Lback_7:\n"
MF "v[112:115],v[24:27],v[136:139],v[32:35]\n"
"s_add_u32 s74,s74,12288\n"
"ds_read_b128 v[16:19],v0 offset:31744\n"
"ds_read_b128 v[20:23],v1 offset:31744\n"
MF "v[112:115],v[28:31],v[156:159],v[112:115]\n"
"s_cmp_eq_u32 s74,s20\n"
"s_cselect_b32 s74,s70,s74\n"
MF "v[116:119],v[28:31],v[160:163],v[36:39]\n"
"v_exp_f32 v92,v92\n"
MF "v[116:119],v[24:27],v[140:143],v[116:119]\n"
"v_exp_f32 v93,v93\n"
MF "v[120:123],v[24:27],v[144:147],v[40:43]\n"
"v_exp_f32 v94,v94\n"
MF "v[120:123],v[28:31],v[164:167],v[120:123]\n"
"v_exp_f32 v95,v95\n"
MF "v[124:127],v[28:31],v[168:171],v[44:47]\n"
"v_add_f32 v57,v52,v92\n"
"v_add_f32 v57,v57,v93\n"
MF "v[124:127],v[24:27],v[148:151],v[124:127]\n"
CV "v92,v92,v93\n"
"v_add_f32 v57,v57,v94\n"
MF "v[128:131],v[24:27],v[152:155],v[48:51]\n"
"v_add_f32 v57,v57,v95\n"
CV "v93,v94,v95\n"
MF "v[128:131],v[28:31],v[172:175],v[128:131]\n"
"s_waitcnt lgkmcnt(0)\n"
"ds_read_b128 v[24:27],v0 offset:32768\n"
"ds_read_b128 v[28:31],v1 offset:32768\n"
MF "v[128:131],v[16:19],v[192:195],v[128:131]\n"
"s_sub_u32 s61,s61,1\n"
"v_exp_f32 v96,v96\n"
MF "v[128:131],v[20:23],v[212:215],v[128:131]\n"
"s_cmp_eq_u32 s61,1\n"
"s_cselect_b32 s95,0,s95\n"
"v_add_f32 v58,v53,v96\n"
MF "v[124:127],v[20:23],v[208:211],v[124:127]\n"
"v_exp_f32 v97,v97\n"
MF "v[124:127],v[16:19],v[188:191],v[124:127]\n"
"v_exp_f32 v98,v98\n"
MF "v[120:123],v[16:19],v[184:187],v[120:123]\n"
"v_exp_f32 v99,v99\n"
MF "v[120:123],v[20:23],v[204:207],v[120:123]\n"
"v_add_f32 v58,v58,v97\n"
CV "v96,v96,v97\n"
MF "v[116:119],v[20:23],v[200:203],v[116:119]\n"
"v_add_f32 v58,v58,v98\n"
"v_add_f32 v58,v58,v99\n"
MF "v[116:119],v[16:19],v[180:183],v[116:119]\n"
"s_waitcnt vmcnt(12) lgkmcnt(0)\n"
"s_barrier\n"
MF "v[112:115],v[16:19],v[176:179],v[112:115]\n"
CV "v97,v98,v99\n"
MF "v[112:115],v[20:23],v[196:199],v[112:115]\n"
"s_nop 0\n"
TR "v[16:17],v2\n"
TR "v[18:19],v2 offset:6144\n"
MF "v[112:115],v[24:27],v[216:219],v[112:115]\n"
"s_add_u32 s72,s72,6144\n"
TR "v[20:21],v3\n"
TR "v[22:23],v3 offset:6144\n"
MF "v[112:115],v[28:31],v[236:239],v[112:115]\n"
"s_cmp_eq_u32 s72,s17\n"
"s_cselect_b32 s72,s16,s72\n"
"s_add_u32 m0,s64,24576\n"
MF "v[116:119],v[28:31],v[240:243],v[116:119]\n"
LD "v7,s[32:35],s72 offen nt lds\n"
MF "v[116:119],v[24:27],v[220:223],v[116:119]\n"
"s_add_u32 m0,s64,25472\n"
"v_exp_f32 v100,v100\n"
MF "v[120:123],v[24:27],v[224:227],v[120:123]\n"
LD "v7,s[32:35],s72 offen offset:128 nt lds\n"
MF "v[120:123],v[28:31],v[244:247],v[120:123]\n"
"v_exp_f32 v101,v101\n"
MF "v[124:127],v[28:31],v[248:251],v[124:127]\n"
"s_add_u32 m0,s64,26368\n"
"v_exp_f32 v102,v102\n"
MF "v[124:127],v[24:27],v[228:231],v[124:127]\n"
LD "v7,s[32:35],s72 offen offset:256 nt lds\n"
MF "v[128:131],v[24:27],v[232:235],v[128:131]\n"
"s_add_u32 s72,s72,6144\n"
"v_exp_f32 v103,v103\n"
MF "v[128:131],v[28:31],v[252:255],v[128:131]\n"
"s_waitcnt lgkmcnt(2)\n"
TR "v[24:25],v4\n"
TR "v[26:27],v4 offset:6144\n"
MF "a[0:3],v[16:19],v[72:75],a[0:3]\n"
"s_add_u32 m0,s64,30720\n"
TR "v[28:29],v5\n"
TR "v[30:31],v5 offset:6144\n"
MF "a[4:7],v[16:19],v[76:79],a[4:7]\n"
LD "v7,s[32:35],s72 offen nt lds\n"
MF "a[8:11],v[16:19],v[80:83],a[8:11]\n"
"s_add_u32 m0,s64,31616\n"
"v_add_f32 v59,v54,v100\n"
"v_add_f32 v59,v59,v101\n"
MF "a[12:15],v[16:19],v[84:87],a[12:15]\n"
LD "v7,s[32:35],s72 offen offset:128 nt lds\n"
MF "a[16:19],v[16:19],v[88:91],a[16:19]\n"
"s_waitcnt lgkmcnt(4)\n"
TR "v[16:17],v2 offset:1024\n"
TR "v[18:19],v2 offset:7168\n"
MF "a[36:39],v[20:23],v[88:91],a[36:39]\n"
CV "v100,v100,v101\n"
"v_add_f32 v59,v59,v102\n"
MF "a[32:35],v[20:23],v[84:87],a[32:35]\n"
"s_add_u32 m0,s64,32512\n"
"v_add_f32 v59,v59,v103\n"
CV "v101,v102,v103\n"
MF "a[28:31],v[20:23],v[80:83],a[28:31]\n"
LD "v7,s[32:35],s72 offen offset:256 nt lds\n"
MF "a[24:27],v[20:23],v[76:79],a[24:27]\n"
"v_exp_f32 v104,v104\n"
MF "a[20:23],v[20:23],v[72:75],a[20:23]\n"
"s_waitcnt lgkmcnt(4)\n"
TR "v[20:21],v3 offset:1024\n"
TR "v[22:23],v3 offset:7168\n"
MF "a[40:43],v[24:27],v[72:75],a[40:43]\n"
"v_exp_f32 v105,v105\n"
MF "a[44:47],v[24:27],v[76:79],a[44:47]\n"
"v_exp_f32 v106,v106\n"
MF "a[48:51],v[24:27],v[80:83],a[48:51]\n"
"v_exp_f32 v107,v107\n"
MF "a[52:55],v[24:27],v[84:87],a[52:55]\n"
"v_add_f32 v60,v55,v104\n"
"v_add_f32 v60,v60,v105\n"
MF "a[56:59],v[24:27],v[88:91],a[56:59]\n"
"s_waitcnt lgkmcnt(4)\n"
TR "v[24:25],v4 offset:1024\n"
TR "v[26:27],v4 offset:7168\n"
MF "a[76:79],v[28:31],v[88:91],a[76:79]\n"
CV "v104,v104,v105\n"
"v_add_f32 v60,v60,v106\n"
MF "a[72:75],v[28:31],v[84:87],a[72:75]\n"
"v_add_f32 v60,v60,v107\n"
CV "v105,v106,v107\n"
MF "a[68:71],v[28:31],v[80:83],a[68:71]\n"
"v_exp_f32 v108,v108\n"
MF "a[64:67],v[28:31],v[76:79],a[64:67]\n"
"v_exp_f32 v109,v109\n"
MF "a[60:63],v[28:31],v[72:75],a[60:63]\n"
"s_waitcnt lgkmcnt(4)\n"
TR "v[28:29],v5 offset:1024\n"
TR "v[30:31],v5 offset:7168\n"
MF "a[80:83],v[16:19],v[72:75],a[80:83]\n"
"v_exp_f32 v110,v110\n"
MF "a[84:87],v[16:19],v[76:79],a[84:87]\n"
"v_exp_f32 v111,v111\n"
MF "a[88:91],v[16:19],v[80:83],a[88:91]\n"
"v_add_f32 v61,v56,v108\n"
"v_add_f32 v61,v61,v109\n"
MF "a[92:95],v[16:19],v[84:87],a[92:95]\n"
CV "v108,v108,v109\n"
"v_add_f32 v61,v61,v110\n"
MF "a[96:99],v[16:19],v[88:91],a[96:99]\n"
"s_waitcnt lgkmcnt(4)\n"
TR "v[16:17],v2 offset:2048\n"
TR "v[18:19],v2 offset:8192\n"
MF "a[116:119],v[20:23],v[88:91],a[116:119]\n"
"v_add_f32 v61,v61,v111\n"
CV "v109,v110,v111\n"
MF "a[112:115],v[20:23],v[84:87],a[112:115]\n"
"v_exp_f32 v112,v112\n"
MF "a[108:111],v[20:23],v[80:83],a[108:111]\n"
"v_exp_f32 v113,v113\n"
MF "a[104:107],v[20:23],v[76:79],a[104:107]\n"
"v_exp_f32 v114,v114\n"
MF "a[100:103],v[20:23],v[72:75],a[100:103]\n"
"s_waitcnt lgkmcnt(4)\n"
TR "v[20:21],v3 offset:2048\n"
TR "v[22:23],v3 offset:8192\n"
MF "a[120:123],v[24:27],v[72:75],a[120:123]\n"
"v_exp_f32 v115,v115\n"
MF "a[124:127],v[24:27],v[76:79],a[124:127]\n"
"v_add_f32 v57,v57,v112\n"
"v_add_f32 v57,v57,v113\n"
MF "a[128:131],v[24:27],v[80:83],a[128:131]\n"
CV "v94,v112,v113\n"
"v_add_f32 v57,v57,v114\n"
MF "a[132:135],v[24:27],v[84:87],a[132:135]\n"
"v_add_f32 v57,v57,v115\n"
CV "v95,v114,v115\n"
MF "a[136:139],v[24:27],v[88:91],a[136:139]\n"
"s_waitcnt lgkmcnt(4)\n"
TR "v[24:25],v4 offset:2048\n"
TR "v[26:27],v4 offset:8192\n"
MF "a[156:159],v[28:31],v[88:91],a[156:159]\n"
"v_exp_f32 v116,v116\n"
MF "a[152:155],v[28:31],v[84:87],a[152:155]\n"
"v_exp_f32 v117,v117\n"
MF "a[148:151],v[28:31],v[80:83],a[148:151]\n"
"v_exp_f32 v118,v118\n"
MF "a[144:147],v[28:31],v[76:79],a[144:147]\n"
"v_exp_f32 v119,v119\n"
MF "a[140:143],v[28:31],v[72:75],a[140:143]\n"
"s_waitcnt lgkmcnt(4)\n"
TR "v[28:29],v5 offset:2048\n"
TR "v[30:31],v5 offset:8192\n"
MF "a[160:163],v[16:19],v[72:75],a[160:163]\n"
"v_add_f32 v58,v58,v116\n"
"v_add_f32 v58,v58,v117\n"
MF "a[164:167],v[16:19],v[76:79],a[164:167]\n"
CV "v98,v116,v117\n"
"v_add_f32 v58,v58,v118\n"
MF "a[168:171],v[16:19],v[80:83],a[168:171]\n"
"v_add_f32 v58,v58,v119\n"
CV "v99,v118,v119\n"
MF "a[172:175],v[16:19],v[84:87],a[172:175]\n"
"v_exp_f32 v120,v120\n"
MF "a[176:179],v[16:19],v[88:91],a[176:179]\n"
"s_waitcnt lgkmcnt(4)\n"
"v_exp_f32 v121,v121\n"
MF "a[196:199],v[20:23],v[88:91],a[196:199]\n"
"v_exp_f32 v122,v122\n"
MF "a[192:195],v[20:23],v[84:87],a[192:195]\n"
"v_exp_f32 v123,v123\n"
MF "a[188:191],v[20:23],v[80:83],a[188:191]\n"
"v_add_f32 v59,v59,v120\n"
"v_add_f32 v59,v59,v121\n"
MF "a[184:187],v[20:23],v[76:79],a[184:187]\n"
"s_waitcnt vmcnt(12) lgkmcnt(0)\n"
"s_barrier\n"
MF "a[180:183],v[20:23],v[72:75],a[180:183]\n"
"s_add_u32 s73,s73,12288\n"
"ds_read_b128 v[16:19],v0\n"
"ds_read_b128 v[20:23],v1\n"
MF "a[200:203],v[24:27],v[72:75],a[200:203]\n"
"s_cmp_eq_u32 s73,s19\n"
"s_cselect_b32 s73,s18,s73\n"
"s_add_u32 m0,s65,49152\n"
MF "a[204:207],v[24:27],v[76:79],a[204:207]\n"
LD "v8,s[36:39],s73 offen nt lds\n"
MF "a[208:211],v[24:27],v[80:83],a[208:211]\n"
"s_add_u32 m0,s65,50048\n"
CV "v102,v120,v121\n"
"v_add_f32 v59,v59,v122\n"
MF "a[212:215],v[24:27],v[84:87],a[212:215]\n"
LD "v8,s[36:39],s73 offen offset:128 nt lds\n"
MF "a[216:219],v[24:27],v[88:91],a[216:219]\n"
"s_add_u32 m0,s65,50944\n"
"ds_read_b128 v[24:27],v0 offset:1024\n"
"v_add_f32 v59,v59,v123\n"
MF "a[236:239],v[28:31],v[88:91],a[236:239]\n"
LD "v8,s[36:39],s73 offen offset:256 nt lds\n"
MF "a[232:235],v[28:31],v[84:87],a[232:235]\n"
CV "v103,v122,v123\n"
MF "a[228:231],v[28:31],v[80:83],a[228:231]\n"
"s_waitcnt lgkmcnt(1)\n"
MF "a[224:227],v[28:31],v[76:79],a[224:227]\n"
"s_cmp_lg_u32 s61,0\n"
MF "a[220:223],v[28:31],v[72:75],a[220:223]\n"
"s_cbranch_scc1 .Laloopx\n"
"v_exp_f32 v124,v124\n"
"v_exp_f32 v125,v125\n"
"v_exp_f32 v126,v126\n"
"v_exp_f32 v127,v127\n"
"v_exp_f32 v128,v128\n"
"v_exp_f32 v129,v129\n"
"v_exp_f32 v130,v130\n"
"v_exp_f32 v131,v131\n"
"v_add_f32 v60,v60,v124\n"
"v_add_f32 v61,v61,v128\n"
"v_add_f32 v60,v60,v125\n"
"v_add_f32 v61,v61,v129\n"
"v_add_f32 v60,v60,v126\n"
"v_add_f32 v61,v61,v130\n"
"v_add_f32 v60,v60,v127\n"
"v_add_f32 v61,v61,v131\n"
CV "v106,v124,v125\n"
CV "v107,v126,v127\n"
CV "v110,v128,v129\n"
CV "v111,v130,v131\n"
"v_max3_f32 v10,v57,v58,v59\n"
"v_max3_f32 v10,v10,v60,v61\n"
"v_cmp_le_f32 vcc,0x47800000,v10\n"
"s_cbranch_vccnz .Lstub_8\n"
".Lback_8:\n"
"s_waitcnt vmcnt(9) lgkmcnt(0)\n"
"s_barrier\n"
"s_cmp_eq_u32 s57,0\n"
"s_cbranch_scc0 .Lep\n"
"s_nop 0\n"
"s_add_u32 s66,s67,4718592\n"
"s_sub_u32 s68,s68,384\n"
"s_mov_b32 s83,7077888\n"
AR "v122,a243\n"
"v_add_u32 v123,97280,v122\n"
"v_add_u32 v124,194560,v122\n"
"v_add_u32 v125,291840,v122\n"
TR "v[136:137],v2 offset:24576\n"
TR "v[138:139],v2 offset:30720\n"
TR "v[140:141],v3 offset:24576\n"
TR "v[142:143],v3 offset:30720\n"
TR "v[144:145],v4 offset:24576\n"
TR "v[146:147],v4 offset:30720\n"
TR "v[148:149],v5 offset:24576\n"
TR "v[150:151],v5 offset:30720\n"
TR "v[152:153],v2 offset:25600\n"
TR "v[154:155],v2 offset:31744\n"
TR "v[156:157],v3 offset:25600\n"
TR "v[158:159],v3 offset:31744\n"
TR "v[160:161],v4 offset:25600\n"
TR "v[162:163],v4 offset:31744\n"
TR "v[164:165],v5 offset:25600\n"
TR "v[166:167],v5 offset:31744\n"
TR "v[168:169],v2 offset:26624\n"
TR "v[170:171],v2 offset:32768\n"
TR "v[172:173],v3 offset:26624\n"
TR "v[174:175],v3 offset:32768\n"
TR "v[176:177],v4 offset:26624\n"
TR "v[178:179],v4 offset:32768\n"
TR "v[180:181],v5 offset:26624\n"
TR "v[182:183],v5 offset:32768\n"
"v_mov_b32 v52,v57\n"
"v_mov_b32 v53,v58\n"
"v_mov_b32 v54,v59\n"
"v_mov_b32 v55,v60\n"
"v_mov_b32 v56,v61\n"
"s_add_u32 m0,s62,0\n"
"s_add_u32 s82,s66,2359296\n"
LD "v122,s[28:31],s82 offen lds\n"
LD "v123,s[28:31],s82 offen offset:1024 lds\n"
"v_permlane16_swap_b32 v57,v52\n"
"v_permlane16_swap_b32 v58,v53\n"
"v_permlane16_swap_b32 v59,v54\n"
"v_permlane16_swap_b32 v60,v55\n"
"v_permlane16_swap_b32 v61,v56\n"
"v_add_f32 v57,v57,v52\n"
"v_add_f32 v58,v58,v53\n"
"v_add_f32 v59,v59,v54\n"
"v_add_f32 v60,v60,v55\n"
"v_add_f32 v61,v61,v56\n"
"v_mov_b32 v52,v57\n"
"v_mov_b32 v53,v58\n"
"v_mov_b32 v54,v59\n"
"v_mov_b32 v55,v60\n"
"v_mov_b32 v56,v61\n"
LD "v124,s[28:31],s82 offen offset:2048 lds\n"
LD "v125,s[28:31],s82 offen offset:3072 lds\n"
"v_permlane32_swap_b32 v57,v52\n"
"v_permlane32_swap_b32 v58,v53\n"
"v_permlane32_swap_b32 v59,v54\n"
"v_permlane32_swap_b32 v60,v55\n"
"v_permlane32_swap_b32 v61,v56\n"
"v_add_f32 v57,v57,v52\n"
"v_add_f32 v58,v58,v53\n"
"v_add_f32 v59,v59,v54\n"
"v_add_f32 v60,v60,v55\n"
"v_add_f32 v61,v61,v56\n"
"s_waitcnt lgkmcnt(15)\n"
MF "a[0:3],v[136:139],v[92:95],a[0:3]\n"
MF "a[20:23],v[140:143],v[92:95],a[20:23]\n"
"v_rcp_f32 v57,v57\n"
"v_rcp_f32 v58,v58\n"
"v_rcp_f32 v59,v59\n"
"v_rcp_f32 v60,v60\n"
"v_rcp_f32_e64 v61,v61\n"
MF "a[4:7],v[136:139],v[96:99],a[4:7]\n"
"v_mul_f32_e64 v52,-s13,v57\n"
"v_mul_f32_e64 v53,-s13,v58\n"
"v_mul_f32_e64 v54,-s13,v59\n"
"v_mul_f32_e64 v55,-s13,v60\n"
"v_mul_f32_e64 v56,-s13,v61\n"
AR "v72,a0\n"
AR "v73,a1\n"
AR "v74,a2\n"
MF "a[24:27],v[140:143],v[96:99],a[24:27]\n"
AR "v75,a3\n"
AR "v76,a20\n"
AR "v77,a21\n"
AR "v78,a22\n"
AR "v79,a23\n"
"v_pk_fma_f32 v[72:73],v[72:73],v[56:57],v[52:53] op_sel:[0,1,0] op_sel_hi:[1,1,0]\n"
"v_pk_fma_f32 v[74:75],v[74:75],v[56:57],v[52:53] op_sel:[0,1,0] op_sel_hi:[1,1,0]\n"
"v_pk_fma_f32 v[76:77],v[76:77],v[56:57],v[52:53] op_sel:[0,1,0] op_sel_hi:[1,1,0]\n"
"v_pk_fma_f32 v[78:79],v[78:79],v[56:57],v[52:53] op_sel:[0,1,0] op_sel_hi:[1,1,0]\n"
CV "v112,v72,v73\n"
CV "v113,v74,v75\n"
CV "v114,v76,v77\n"
CV "v115,v78,v79\n"
MF "a[8:11],v[136:139],v[100:103],a[8:11]\n"
AW "a0,v112\n"
AW "a1,v113\n"
AW "a2,v114\n"
AW "a3,v115\n"
"s_add_u32 m0,s62,16384\n"
"s_add_u32 s82,s66,2359424\n"
LD "v122,s[28:31],s82 offen lds\n"
AR "v80,a4\n"
AR "v81,a5\n"
AR "v82,a6\n"
MF "a[28:31],v[140:143],v[100:103],a[28:31]\n"
AR "v83,a7\n"
AR "v84,a24\n"
AR "v85,a25\n"
AR "v86,a26\n"
AR "v87,a27\n"
"v_pk_fma_f32 v[80:81],v[80:81],v[58:59],v[52:53] op_sel:[0,0,1] op_sel_hi:[1,0,1]\n"
"v_pk_fma_f32 v[82:83],v[82:83],v[58:59],v[52:53] op_sel:[0,0,1] op_sel_hi:[1,0,1]\n"
"v_pk_fma_f32 v[84:85],v[84:85],v[58:59],v[52:53] op_sel:[0,0,1] op_sel_hi:[1,0,1]\n"
"v_pk_fma_f32 v[86:87],v[86:87],v[58:59],v[52:53] op_sel:[0,0,1] op_sel_hi:[1,0,1]\n"
CV "v116,v80,v81\n"
CV "v117,v82,v83\n"
CV "v118,v84,v85\n"
CV "v119,v86,v87\n"
MF "a[12:15],v[136:139],v[104:107],a[12:15]\n"
AW "a4,v116\n"
AW "a5,v117\n"
AW "a6,v118\n"
AW "a7,v119\n"
LD "v123,s[28:31],s82 offen offset:1024 lds\n"
AR "v72,a8\n"
AR "v73,a9\n"
AR "v74,a10\n"
MF "a[32:35],v[140:143],v[104:107],a[32:35]\n"
AR "v75,a11\n"
AR "v76,a28\n"
AR "v77,a29\n"
AR "v78,a30\n"
AR "v79,a31\n"
"v_pk_fma_f32 v[72:73],v[72:73],v[58:59],v[54:55] op_sel:[0,1,0] op_sel_hi:[1,1,0]\n"
"v_pk_fma_f32 v[74:75],v[74:75],v[58:59],v[54:55] op_sel:[0,1,0] op_sel_hi:[1,1,0]\n"
"v_pk_fma_f32 v[76:77],v[76:77],v[58:59],v[54:55] op_sel:[0,1,0] op_sel_hi:[1,1,0]\n"
"v_pk_fma_f32 v[78:79],v[78:79],v[58:59],v[54:55] op_sel:[0,1,0] op_sel_hi:[1,1,0]\n"
CV "v112,v72,v73\n"
CV "v113,v74,v75\n"
CV "v114,v76,v77\n"
CV "v115,v78,v79\n"
MF "a[16:19],v[136:139],v[108:111],a[16:19]\n"
AW "a8,v112\n"
AW "a9,v113\n"
AW "a10,v114\n"
AW "a11,v115\n"
"s_waitcnt lgkmcnt(0)\n"
"s_barrier\n"
AR "v80,a12\n"
AR "v81,a13\n"
AR "v82,a14\n"
MF "a[36:39],v[140:143],v[108:111],a[36:39]\n"
AR "v83,a15\n"
AR "v84,a32\n"
AR "v85,a33\n"
AR "v86,a34\n"
AR "v87,a35\n"
"v_pk_fma_f32 v[80:81],v[80:81],v[60:61],v[54:55] op_sel:[0,0,1] op_sel_hi:[1,0,1]\n"
"v_pk_fma_f32 v[82:83],v[82:83],v[60:61],v[54:55] op_sel:[0,0,1] op_sel_hi:[1,0,1]\n"
"v_pk_fma_f32 v[84:85],v[84:85],v[60:61],v[54:55] op_sel:[0,0,1] op_sel_hi:[1,0,1]\n"
"v_pk_fma_f32 v[86:87],v[86:87],v[60:61],v[54:55] op_sel:[0,0,1] op_sel_hi:[1,0,1]\n"
CV "v116,v80,v81\n"
CV "v117,v82,v83\n"
CV "v118,v84,v85\n"
CV "v119,v86,v87\n"
MF "a[40:43],v[144:147],v[92:95],a[40:43]\n"
AW "a12,v116\n"
AW "a13,v117\n"
AW "a14,v118\n"
AW "a15,v119\n"
LD "v124,s[28:31],s82 offen offset:2048 lds\n"
AR "v72,a16\n"
AR "v73,a17\n"
AR "v74,a18\n"
MF "a[60:63],v[148:151],v[92:95],a[60:63]\n"
AR "v75,a19\n"
AR "v76,a36\n"
AR "v77,a37\n"
AR "v78,a38\n"
AR "v79,a39\n"
"v_pk_fma_f32 v[72:73],v[72:73],v[60:61],v[56:57] op_sel:[0,1,0] op_sel_hi:[1,1,0]\n"
"v_pk_fma_f32 v[74:75],v[74:75],v[60:61],v[56:57] op_sel:[0,1,0] op_sel_hi:[1,1,0]\n"
"v_pk_fma_f32 v[76:77],v[76:77],v[60:61],v[56:57] op_sel:[0,1,0] op_sel_hi:[1,1,0]\n"
"v_pk_fma_f32 v[78:79],v[78:79],v[60:61],v[56:57] op_sel:[0,1,0] op_sel_hi:[1,1,0]\n"
CV "v112,v72,v73\n"
CV "v113,v74,v75\n"
CV "v114,v76,v77\n"
CV "v115,v78,v79\n"
MF "a[44:47],v[144:147],v[96:99],a[44:47]\n"
AW "a16,v112\n"
AW "a17,v113\n"
AW "a18,v114\n"
AW "a19,v115\n"
LD "v125,s[28:31],s82 offen offset:3072 lds\n"
AR "v80,a40\n"
AR "v81,a41\n"
AR "v82,a42\n"
MF "a[64:67],v[148:151],v[96:99],a[64:67]\n"
AR "v83,a43\n"
AR "v84,a60\n"
AR "v85,a61\n"
AR "v86,a62\n"
AR "v87,a63\n"
"v_pk_fma_f32 v[80:81],v[80:81],v[56:57],v[52:53] op_sel:[0,1,0] op_sel_hi:[1,1,0]\n"
"v_pk_fma_f32 v[82:83],v[82:83],v[56:57],v[52:53] op_sel:[0,1,0] op_sel_hi:[1,1,0]\n"
"v_pk_fma_f32 v[84:85],v[84:85],v[56:57],v[52:53] op_sel:[0,1,0] op_sel_hi:[1,1,0]\n"
"v_pk_fma_f32 v[86:87],v[86:87],v[56:57],v[52:53] op_sel:[0,1,0] op_sel_hi:[1,1,0]\n"
CV "v116,v80,v81\n"
CV "v117,v82,v83\n"
CV "v118,v84,v85\n"
CV "v119,v86,v87\n"
MF "a[48:51],v[144:147],v[100:103],a[48:51]\n"
AW "a40,v116\n"
AW "a41,v117\n"
AW "a42,v118\n"
AW "a43,v119\n"
AR "v72,a44\n"
AR "v73,a45\n"
AR "v74,a46\n"
MF "a[68:71],v[148:151],v[100:103],a[68:71]\n"
AR "v75,a47\n"
AR "v76,a64\n"
AR "v77,a65\n"
AR "v78,a66\n"
AR "v79,a67\n"
"v_pk_fma_f32 v[72:73],v[72:73],v[58:59],v[52:53] op_sel:[0,0,1] op_sel_hi:[1,0,1]\n"
"v_pk_fma_f32 v[74:75],v[74:75],v[58:59],v[52:53] op_sel:[0,0,1] op_sel_hi:[1,0,1]\n"
"v_pk_fma_f32 v[76:77],v[76:77],v[58:59],v[52:53] op_sel:[0,0,1] op_sel_hi:[1,0,1]\n"
"v_pk_fma_f32 v[78:79],v[78:79],v[58:59],v[52:53] op_sel:[0,0,1] op_sel_hi:[1,0,1]\n"
CV "v112,v72,v73\n"
CV "v113,v74,v75\n"
CV "v114,v76,v77\n"
CV "v115,v78,v79\n"
MF "a[52:55],v[144:147],v[104:107],a[52:55]\n"
AW "a44,v112\n"
AW "a45,v113\n"
AW "a46,v114\n"
AW "a47,v115\n"
"s_add_u32 m0,s62,32768\n"
"s_add_u32 s82,s66,2359552\n"
LD "v122,s[28:31],s82 offen lds\n"
AR "v80,a48\n"
AR "v81,a49\n"
AR "v82,a50\n"
MF "a[72:75],v[148:151],v[104:107],a[72:75]\n"
AR "v83,a51\n"
AR "v84,a68\n"
AR "v85,a69\n"
AR "v86,a70\n"
AR "v87,a71\n"
"v_pk_fma_f32 v[80:81],v[80:81],v[58:59],v[54:55] op_sel:[0,1,0] op_sel_hi:[1,1,0]\n"
"v_pk_fma_f32 v[82:83],v[82:83],v[58:59],v[54:55] op_sel:[0,1,0] op_sel_hi:[1,1,0]\n"
"v_pk_fma_f32 v[84:85],v[84:85],v[58:59],v[54:55] op_sel:[0,1,0] op_sel_hi:[1,1,0]\n"
"v_pk_fma_f32 v[86:87],v[86:87],v[58:59],v[54:55] op_sel:[0,1,0] op_sel_hi:[1,1,0]\n"
CV "v116,v80,v81\n"
CV "v117,v82,v83\n"
CV "v118,v84,v85\n"
CV "v119,v86,v87\n"
MF "a[56:59],v[144:147],v[108:111],a[56:59]\n"
AW "a48,v116\n"
AW "a49,v117\n"
AW "a50,v118\n"
AW "a51,v119\n"
LD "v123,s[28:31],s82 offen offset:1024 lds\n"
AR "v72,a52\n"
AR "v73,a53\n"
AR "v74,a54\n"
MF "a[76:79],v[148:151],v[108:111],a[76:79]\n"
AR "v75,a55\n"
AR "v76,a72\n"
AR "v77,a73\n"
AR "v78,a74\n"
AR "v79,a75\n"
"v_pk_fma_f32 v[72:73],v[72:73],v[60:61],v[54:55] op_sel:[0,0,1] op_sel_hi:[1,0,1]\n"
"v_pk_fma_f32 v[74:75],v[74:75],v[60:61],v[54:55] op_sel:[0,0,1] op_sel_hi:[1,0,1]\n"
"v_pk_fma_f32 v[76:77],v[76:77],v[60:61],v[54:55] op_sel:[0,0,1] op_sel_hi:[1,0,1]\n"
"v_pk_fma_f32 v[78:79],v[78:79],v[60:61],v[54:55] op_sel:[0,0,1] op_sel_hi:[1,0,1]\n"
CV "v112,v72,v73\n"
CV "v113,v74,v75\n"
CV "v114,v76,v77\n"
CV "v115,v78,v79\n"
MF "a[80:83],v[152:155],v[92:95],a[80:83]\n"
AW "a52,v112\n"
AW "a53,v113\n"
AW "a54,v114\n"
AW "a55,v115\n"
AR "v80,a56\n"
AR "v81,a57\n"
AR "v82,a58\n"
MF "a[100:103],v[156:159],v[92:95],a[100:103]\n"
AR "v83,a59\n"
AR "v84,a76\n"
AR "v85,a77\n"
AR "v86,a78\n"
AR "v87,a79\n"
"v_pk_fma_f32 v[80:81],v[80:81],v[60:61],v[56:57] op_sel:[0,1,0] op_sel_hi:[1,1,0]\n"
"v_pk_fma_f32 v[82:83],v[82:83],v[60:61],v[56:57] op_sel:[0,1,0] op_sel_hi:[1,1,0]\n"
"v_pk_fma_f32 v[84:85],v[84:85],v[60:61],v[56:57] op_sel:[0,1,0] op_sel_hi:[1,1,0]\n"
"v_pk_fma_f32 v[86:87],v[86:87],v[60:61],v[56:57] op_sel:[0,1,0] op_sel_hi:[1,1,0]\n"
CV "v116,v80,v81\n"
CV "v117,v82,v83\n"
CV "v118,v84,v85\n"
CV "v119,v86,v87\n"
MF "a[84:87],v[152:155],v[96:99],a[84:87]\n"
AW "a56,v116\n"
AW "a57,v117\n"
AW "a58,v118\n"
AW "a59,v119\n"
LD "v124,s[28:31],s82 offen offset:2048 lds\n"
AR "v72,a80\n"
AR "v73,a81\n"
AR "v74,a82\n"
MF "a[104:107],v[156:159],v[96:99],a[104:107]\n"
AR "v75,a83\n"
AR "v76,a100\n"
AR "v77,a101\n"
AR "v78,a102\n"
AR "v79,a103\n"
"v_pk_fma_f32 v[72:73],v[72:73],v[56:57],v[52:53] op_sel:[0,1,0] op_sel_hi:[1,1,0]\n"
"v_pk_fma_f32 v[74:75],v[74:75],v[56:57],v[52:53] op_sel:[0,1,0] op_sel_hi:[1,1,0]\n"
"v_pk_fma_f32 v[76:77],v[76:77],v[56:57],v[52:53] op_sel:[0,1,0] op_sel_hi:[1,1,0]\n"
"v_pk_fma_f32 v[78:79],v[78:79],v[56:57],v[52:53] op_sel:[0,1,0] op_sel_hi:[1,1,0]\n"
CV "v112,v72,v73\n"
CV "v113,v74,v75\n"
CV "v114,v76,v77\n"
CV "v115,v78,v79\n"
MF "a[88:91],v[152:155],v[100:103],a[88:91]\n"
AW "a80,v112\n"
AW "a81,v113\n"
AW "a82,v114\n"
AW "a83,v115\n"
LD "v125,s[28:31],s82 offen offset:3072 lds\n"
AR "v80,a84\n"
AR "v81,a85\n"
AR "v82,a86\n"
MF "a[108:111],v[156:159],v[100:103],a[108:111]\n"
AR "v83,a87\n"
AR "v84,a104\n"
AR "v85,a105\n"
AR "v86,a106\n"
AR "v87,a107\n"
"v_pk_fma_f32 v[80:81],v[80:81],v[58:59],v[52:53] op_sel:[0,0,1] op_sel_hi:[1,0,1]\n"
"v_pk_fma_f32 v[82:83],v[82:83],v[58:59],v[52:53] op_sel:[0,0,1] op_sel_hi:[1,0,1]\n"
"v_pk_fma_f32 v[84:85],v[84:85],v[58:59],v[52:53] op_sel:[0,0,1] op_sel_hi:[1,0,1]\n"
"v_pk_fma_f32 v[86:87],v[86:87],v[58:59],v[52:53] op_sel:[0,0,1] op_sel_hi:[1,0,1]\n"
CV "v116,v80,v81\n"
CV "v117,v82,v83\n"
CV "v118,v84,v85\n"
CV "v119,v86,v87\n"
MF "a[92:95],v[152:155],v[104:107],a[92:95]\n"
AW "a84,v116\n"
AW "a85,v117\n"
AW "a86,v118\n"
AW "a87,v119\n"
AR "v72,a88\n"
AR "v73,a89\n"
AR "v74,a90\n"
MF "a[112:115],v[156:159],v[104:107],a[112:115]\n"
AR "v75,a91\n"
AR "v76,a108\n"
AR "v77,a109\n"
AR "v78,a110\n"
AR "v79,a111\n"
"v_pk_fma_f32 v[72:73],v[72:73],v[58:59],v[54:55] op_sel:[0,1,0] op_sel_hi:[1,1,0]\n"
"v_pk_fma_f32 v[74:75],v[74:75],v[58:59],v[54:55] op_sel:[0,1,0] op_sel_hi:[1,1,0]\n"
"v_pk_fma_f32 v[76:77],v[76:77],v[58:59],v[54:55] op_sel:[0,1,0] op_sel_hi:[1,1,0]\n"
"v_pk_fma_f32 v[78:79],v[78:79],v[58:59],v[54:55] op_sel:[0,1,0] op_sel_hi:[1,1,0]\n"
CV "v112,v72,v73\n"
CV "v113,v74,v75\n"
CV "v114,v76,v77\n"
CV "v115,v78,v79\n"
MF "a[96:99],v[152:155],v[108:111],a[96:99]\n"
AW "a88,v112\n"
AW "a89,v113\n"
AW "a90,v114\n"
AW "a91,v115\n"
AR "v80,a92\n"
AR "v81,a93\n"
AR "v82,a94\n"
MF "a[116:119],v[156:159],v[108:111],a[116:119]\n"
AR "v83,a95\n"
AR "v84,a112\n"
AR "v85,a113\n"
AR "v86,a114\n"
AR "v87,a115\n"
"v_pk_fma_f32 v[80:81],v[80:81],v[60:61],v[54:55] op_sel:[0,0,1] op_sel_hi:[1,0,1]\n"
"v_pk_fma_f32 v[82:83],v[82:83],v[60:61],v[54:55] op_sel:[0,0,1] op_sel_hi:[1,0,1]\n"
"v_pk_fma_f32 v[84:85],v[84:85],v[60:61],v[54:55] op_sel:[0,0,1] op_sel_hi:[1,0,1]\n"
"v_pk_fma_f32 v[86:87],v[86:87],v[60:61],v[54:55] op_sel:[0,0,1] op_sel_hi:[1,0,1]\n"
CV "v116,v80,v81\n"
CV "v117,v82,v83\n"
CV "v118,v84,v85\n"
CV "v119,v86,v87\n"
MF "a[120:123],v[160:163],v[92:95],a[120:123]\n"
AW "a92,v116\n"
AW "a93,v117\n"
AW "a94,v118\n"
AW "a95,v119\n"
AR "v72,a96\n"
AR "v73,a97\n"
AR "v74,a98\n"
MF "a[140:143],v[164:167],v[92:95],a[140:143]\n"
AR "v75,a99\n"
AR "v76,a116\n"
AR "v77,a117\n"
AR "v78,a118\n"
AR "v79,a119\n"
"v_pk_fma_f32 v[72:73],v[72:73],v[60:61],v[56:57] op_sel:[0,1,0] op_sel_hi:[1,1,0]\n"
"v_pk_fma_f32 v[74:75],v[74:75],v[60:61],v[56:57] op_sel:[0,1,0] op_sel_hi:[1,1,0]\n"
"v_pk_fma_f32 v[76:77],v[76:77],v[60:61],v[56:57] op_sel:[0,1,0] op_sel_hi:[1,1,0]\n"
"v_pk_fma_f32 v[78:79],v[78:79],v[60:61],v[56:57] op_sel:[0,1,0] op_sel_hi:[1,1,0]\n"
CV "v112,v72,v73\n"
CV "v113,v74,v75\n"
CV "v114,v76,v77\n"
CV "v115,v78,v79\n"
MF "a[124:127],v[160:163],v[96:99],a[124:127]\n"
AW "a96,v112\n"
AW "a97,v113\n"
AW "a98,v114\n"
AW "a99,v115\n"
AR "v80,a120\n"
AR "v81,a121\n"
AR "v82,a122\n"
MF "a[144:147],v[164:167],v[96:99],a[144:147]\n"
AR "v83,a123\n"
AR "v84,a140\n"
AR "v85,a141\n"
AR "v86,a142\n"
AR "v87,a143\n"
"v_pk_fma_f32 v[80:81],v[80:81],v[56:57],v[52:53] op_sel:[0,1,0] op_sel_hi:[1,1,0]\n"
"v_pk_fma_f32 v[82:83],v[82:83],v[56:57],v[52:53] op_sel:[0,1,0] op_sel_hi:[1,1,0]\n"
"v_pk_fma_f32 v[84:85],v[84:85],v[56:57],v[52:53] op_sel:[0,1,0] op_sel_hi:[1,1,0]\n"
"v_pk_fma_f32 v[86:87],v[86:87],v[56:57],v[52:53] op_sel:[0,1,0] op_sel_hi:[1,1,0]\n"
CV "v116,v80,v81\n"
CV "v117,v82,v83\n"
CV "v118,v84,v85\n"
CV "v119,v86,v87\n"
MF "a[128:131],v[160:163],v[100:103],a[128:131]\n"
AW "a120,v116\n"
AW "a121,v117\n"
AW "a122,v118\n"
AW "a123,v119\n"
AR "v72,a124\n"
AR "v73,a125\n"
AR "v74,a126\n"
MF "a[148:151],v[164:167],v[100:103],a[148:151]\n"
AR "v75,a127\n"
AR "v76,a144\n"
AR "v77,a145\n"
AR "v78,a146\n"
AR "v79,a147\n"
"v_pk_fma_f32 v[72:73],v[72:73],v[58:59],v[52:53] op_sel:[0,0,1] op_sel_hi:[1,0,1]\n"
"v_pk_fma_f32 v[74:75],v[74:75],v[58:59],v[52:53] op_sel:[0,0,1] op_sel_hi:[1,0,1]\n"
"v_pk_fma_f32 v[76:77],v[76:77],v[58:59],v[52:53] op_sel:[0,0,1] op_sel_hi:[1,0,1]\n"
"v_pk_fma_f32 v[78:79],v[78:79],v[58:59],v[52:53] op_sel:[0,0,1] op_sel_hi:[1,0,1]\n"
CV "v112,v72,v73\n"
CV "v113,v74,v75\n"
CV "v114,v76,v77\n"
CV "v115,v78,v79\n"
MF "a[132:135],v[160:163],v[104:107],a[132:135]\n"
AW "a124,v112\n"
AW "a125,v113\n"
AW "a126,v114\n"
AW "a127,v115\n"
AR "v80,a128\n"
AR "v81,a129\n"
AR "v82,a130\n"
MF "a[152:155],v[164:167],v[104:107],a[152:155]\n"
AR "v83,a131\n"
AR "v84,a148\n"
AR "v85,a149\n"
AR "v86,a150\n"
AR "v87,a151\n"
"v_pk_fma_f32 v[80:81],v[80:81],v[58:59],v[54:55] op_sel:[0,1,0] op_sel_hi:[1,1,0]\n"
"v_pk_fma_f32 v[82:83],v[82:83],v[58:59],v[54:55] op_sel:[0,1,0] op_sel_hi:[1,1,0]\n"
"v_pk_fma_f32 v[84:85],v[84:85],v[58:59],v[54:55] op_sel:[0,1,0] op_sel_hi:[1,1,0]\n"
"v_pk_fma_f32 v[86:87],v[86:87],v[58:59],v[54:55] op_sel:[0,1,0] op_sel_hi:[1,1,0]\n"
CV "v116,v80,v81\n"
CV "v117,v82,v83\n"
CV "v118,v84,v85\n"
CV "v119,v86,v87\n"
MF "a[136:139],v[160:163],v[108:111],a[136:139]\n"
AW "a128,v116\n"
AW "a129,v117\n"
AW "a130,v118\n"
AW "a131,v119\n"
AR "v72,a132\n"
AR "v73,a133\n"
AR "v74,a134\n"
MF "a[156:159],v[164:167],v[108:111],a[156:159]\n"
AR "v75,a135\n"
AR "v76,a152\n"
AR "v77,a153\n"
AR "v78,a154\n"
AR "v79,a155\n"
"v_pk_fma_f32 v[72:73],v[72:73],v[60:61],v[54:55] op_sel:[0,0,1] op_sel_hi:[1,0,1]\n"
"v_pk_fma_f32 v[74:75],v[74:75],v[60:61],v[54:55] op_sel:[0,0,1] op_sel_hi:[1,0,1]\n"
"v_pk_fma_f32 v[76:77],v[76:77],v[60:61],v[54:55] op_sel:[0,0,1] op_sel_hi:[1,0,1]\n"
"v_pk_fma_f32 v[78:79],v[78:79],v[60:61],v[54:55] op_sel:[0,0,1] op_sel_hi:[1,0,1]\n"
CV "v112,v72,v73\n"
CV "v113,v74,v75\n"
CV "v114,v76,v77\n"
CV "v115,v78,v79\n"
MF "a[160:163],v[168:171],v[92:95],a[160:163]\n"
AW "a132,v112\n"
AW "a133,v113\n"
AW "a134,v114\n"
AW "a135,v115\n"
AR "v80,a136\n"
AR "v81,a137\n"
AR "v82,a138\n"
MF "a[180:183],v[172:175],v[92:95],a[180:183]\n"
AR "v83,a139\n"
AR "v84,a156\n"
AR "v85,a157\n"
AR "v86,a158\n"
AR "v87,a159\n"
"v_pk_fma_f32 v[80:81],v[80:81],v[60:61],v[56:57] op_sel:[0,1,0] op_sel_hi:[1,1,0]\n"
"v_pk_fma_f32 v[82:83],v[82:83],v[60:61],v[56:57] op_sel:[0,1,0] op_sel_hi:[1,1,0]\n"
"v_pk_fma_f32 v[84:85],v[84:85],v[60:61],v[56:57] op_sel:[0,1,0] op_sel_hi:[1,1,0]\n"
"v_pk_fma_f32 v[86:87],v[86:87],v[60:61],v[56:57] op_sel:[0,1,0] op_sel_hi:[1,1,0]\n"
CV "v116,v80,v81\n"
CV "v117,v82,v83\n"
CV "v118,v84,v85\n"
CV "v119,v86,v87\n"
MF "a[164:167],v[168:171],v[96:99],a[164:167]\n"
AW "a136,v116\n"
AW "a137,v117\n"
AW "a138,v118\n"
AW "a139,v119\n"
AR "v72,a160\n"
AR "v73,a161\n"
AR "v74,a162\n"
MF "a[184:187],v[172:175],v[96:99],a[184:187]\n"
AR "v75,a163\n"
AR "v76,a180\n"
AR "v77,a181\n"
AR "v78,a182\n"
AR "v79,a183\n"
"v_pk_fma_f32 v[72:73],v[72:73],v[56:57],v[52:53] op_sel:[0,1,0] op_sel_hi:[1,1,0]\n"
"v_pk_fma_f32 v[74:75],v[74:75],v[56:57],v[52:53] op_sel:[0,1,0] op_sel_hi:[1,1,0]\n"
"v_pk_fma_f32 v[76:77],v[76:77],v[56:57],v[52:53] op_sel:[0,1,0] op_sel_hi:[1,1,0]\n"
"v_pk_fma_f32 v[78:79],v[78:79],v[56:57],v[52:53] op_sel:[0,1,0] op_sel_hi:[1,1,0]\n"
CV "v112,v72,v73\n"
CV "v113,v74,v75\n"
CV "v114,v76,v77\n"
CV "v115,v78,v79\n"
MF "a[168:171],v[168:171],v[100:103],a[168:171]\n"
AW "a160,v112\n"
AW "a161,v113\n"
AW "a162,v114\n"
AW "a163,v115\n"
AR "v80,a164\n"
AR "v81,a165\n"
AR "v82,a166\n"
MF "a[188:191],v[172:175],v[100:103],a[188:191]\n"
AR "v83,a167\n"
AR "v84,a184\n"
AR "v85,a185\n"
AR "v86,a186\n"
AR "v87,a187\n"
"v_pk_fma_f32 v[80:81],v[80:81],v[58:59],v[52:53] op_sel:[0,0,1] op_sel_hi:[1,0,1]\n"
"v_pk_fma_f32 v[82:83],v[82:83],v[58:59],v[52:53] op_sel:[0,0,1] op_sel_hi:[1,0,1]\n"
"v_pk_fma_f32 v[84:85],v[84:85],v[58:59],v[52:53] op_sel:[0,0,1] op_sel_hi:[1,0,1]\n"
"v_pk_fma_f32 v[86:87],v[86:87],v[58:59],v[52:53] op_sel:[0,0,1] op_sel_hi:[1,0,1]\n"
CV "v116,v80,v81\n"
CV "v117,v82,v83\n"
CV "v118,v84,v85\n"
CV "v119,v86,v87\n"
MF "a[172:175],v[168:171],v[104:107],a[172:175]\n"
AW "a164,v116\n"
AW "a165,v117\n"
AW "a166,v118\n"
AW "a167,v119\n"
AR "v72,a168\n"
AR "v73,a169\n"
AR "v74,a170\n"
MF "a[192:195],v[172:175],v[104:107],a[192:195]\n"
AR "v75,a171\n"
AR "v76,a188\n"
AR "v77,a189\n"
AR "v78,a190\n"
AR "v79,a191\n"
"v_pk_fma_f32 v[72:73],v[72:73],v[58:59],v[54:55] op_sel:[0,1,0] op_sel_hi:[1,1,0]\n"
"v_pk_fma_f32 v[74:75],v[74:75],v[58:59],v[54:55] op_sel:[0,1,0] op_sel_hi:[1,1,0]\n"
"v_pk_fma_f32 v[76:77],v[76:77],v[58:59],v[54:55] op_sel:[0,1,0] op_sel_hi:[1,1,0]\n"
"v_pk_fma_f32 v[78:79],v[78:79],v[58:59],v[54:55] op_sel:[0,1,0] op_sel_hi:[1,1,0]\n"
CV "v112,v72,v73\n"
CV "v113,v74,v75\n"
CV "v114,v76,v77\n"
CV "v115,v78,v79\n"
MF "a[176:179],v[168:171],v[108:111],a[176:179]\n"
AW "a168,v112\n"
AW "a169,v113\n"
AW "a170,v114\n"
AW "a171,v115\n"
AR "v80,a172\n"
AR "v81,a173\n"
AR "v82,a174\n"
MF "a[196:199],v[172:175],v[108:111],a[196:199]\n"
AR "v83,a175\n"
AR "v84,a192\n"
AR "v85,a193\n"
AR "v86,a194\n"
AR "v87,a195\n"
"v_pk_fma_f32 v[80:81],v[80:81],v[60:61],v[54:55] op_sel:[0,0,1] op_sel_hi:[1,0,1]\n"
"v_pk_fma_f32 v[82:83],v[82:83],v[60:61],v[54:55] op_sel:[0,0,1] op_sel_hi:[1,0,1]\n"
"v_pk_fma_f32 v[84:85],v[84:85],v[60:61],v[54:55] op_sel:[0,0,1] op_sel_hi:[1,0,1]\n"
"v_pk_fma_f32 v[86:87],v[86:87],v[60:61],v[54:55] op_sel:[0,0,1] op_sel_hi:[1,0,1]\n"
CV "v116,v80,v81\n"
CV "v117,v82,v83\n"
CV "v118,v84,v85\n"
CV "v119,v86,v87\n"
MF "a[200:203],v[176:179],v[92:95],a[200:203]\n"
AW "a172,v116\n"
AW "a173,v117\n"
AW "a174,v118\n"
AW "a175,v119\n"
AR "v72,a176\n"
AR "v73,a177\n"
AR "v74,a178\n"
MF "a[220:223],v[180:183],v[92:95],a[220:223]\n"
AR "v75,a179\n"
AR "v76,a196\n"
AR "v77,a197\n"
AR "v78,a198\n"
AR "v79,a199\n"
"v_pk_fma_f32 v[72:73],v[72:73],v[60:61],v[56:57] op_sel:[0,1,0] op_sel_hi:[1,1,0]\n"
"v_pk_fma_f32 v[74:75],v[74:75],v[60:61],v[56:57] op_sel:[0,1,0] op_sel_hi:[1,1,0]\n"
"v_pk_fma_f32 v[76:77],v[76:77],v[60:61],v[56:57] op_sel:[0,1,0] op_sel_hi:[1,1,0]\n"
"v_pk_fma_f32 v[78:79],v[78:79],v[60:61],v[56:57] op_sel:[0,1,0] op_sel_hi:[1,1,0]\n"
CV "v112,v72,v73\n"
CV "v113,v74,v75\n"
CV "v114,v76,v77\n"
CV "v115,v78,v79\n"
MF "a[204:207],v[176:179],v[96:99],a[204:207]\n"
AW "a176,v112\n"
AW "a177,v113\n"
AW "a178,v114\n"
AW "a179,v115\n"
AR "v80,a200\n"
AR "v81,a201\n"
AR "v82,a202\n"
MF "a[224:227],v[180:183],v[96:99],a[224:227]\n"
AR "v83,a203\n"
AR "v84,a220\n"
AR "v85,a221\n"
AR "v86,a222\n"
AR "v87,a223\n"
"v_pk_fma_f32 v[80:81],v[80:81],v[56:57],v[52:53] op_sel:[0,1,0] op_sel_hi:[1,1,0]\n"
"v_pk_fma_f32 v[82:83],v[82:83],v[56:57],v[52:53] op_sel:[0,1,0] op_sel_hi:[1,1,0]\n"
"v_pk_fma_f32 v[84:85],v[84:85],v[56:57],v[52:53] op_sel:[0,1,0] op_sel_hi:[1,1,0]\n"
"v_pk_fma_f32 v[86:87],v[86:87],v[56:57],v[52:53] op_sel:[0,1,0] op_sel_hi:[1,1,0]\n"
CV "v116,v80,v81\n"
CV "v117,v82,v83\n"
CV "v118,v84,v85\n"
CV "v119,v86,v87\n"
MF "a[208:211],v[176:179],v[100:103],a[208:211]\n"
AW "a20,v116\n"
AW "a21,v117\n"
AW "a22,v118\n"
AW "a23,v119\n"
AR "v72,a204\n"
AR "v73,a205\n"
AR "v74,a206\n"
MF "a[228:231],v[180:183],v[100:103],a[228:231]\n"
AR "v75,a207\n"
AR "v76,a224\n"
AR "v77,a225\n"
AR "v78,a226\n"
AR "v79,a227\n"
"v_pk_fma_f32 v[72:73],v[72:73],v[58:59],v[52:53] op_sel:[0,0,1] op_sel_hi:[1,0,1]\n"
"v_pk_fma_f32 v[74:75],v[74:75],v[58:59],v[52:53] op_sel:[0,0,1] op_sel_hi:[1,0,1]\n"
"v_pk_fma_f32 v[76:77],v[76:77],v[58:59],v[52:53] op_sel:[0,0,1] op_sel_hi:[1,0,1]\n"
"v_pk_fma_f32 v[78:79],v[78:79],v[58:59],v[52:53] op_sel:[0,0,1] op_sel_hi:[1,0,1]\n"
CV "v112,v72,v73\n"
CV "v113,v74,v75\n"
CV "v114,v76,v77\n"
CV "v115,v78,v79\n"
MF "a[212:215],v[176:179],v[104:107],a[212:215]\n"
AW "a24,v112\n"
AW "a25,v113\n"
AW "a26,v114\n"
AW "a27,v115\n"
AR "v80,a208\n"
AR "v81,a209\n"
AR "v82,a210\n"
MF "a[232:235],v[180:183],v[104:107],a[232:235]\n"
AR "v83,a211\n"
AR "v84,a228\n"
AR "v85,a229\n"
AR "v86,a230\n"
AR "v87,a231\n"
"v_pk_fma_f32 v[80:81],v[80:81],v[58:59],v[54:55] op_sel:[0,1,0] op_sel_hi:[1,1,0]\n"
"v_pk_fma_f32 v[82:83],v[82:83],v[58:59],v[54:55] op_sel:[0,1,0] op_sel_hi:[1,1,0]\n"
"v_pk_fma_f32 v[84:85],v[84:85],v[58:59],v[54:55] op_sel:[0,1,0] op_sel_hi:[1,1,0]\n"
"v_pk_fma_f32 v[86:87],v[86:87],v[58:59],v[54:55] op_sel:[0,1,0] op_sel_hi:[1,1,0]\n"
CV "v116,v80,v81\n"
CV "v117,v82,v83\n"
CV "v118,v84,v85\n"
CV "v119,v86,v87\n"
MF "a[216:219],v[176:179],v[108:111],a[216:219]\n"
AW "a28,v116\n"
AW "a29,v117\n"
AW "a30,v118\n"
AW "a31,v119\n"
AR "v72,a212\n"
AR "v73,a213\n"
AR "v74,a214\n"
MF "a[236:239],v[180:183],v[108:111],a[236:239]\n"
AR "v75,a215\n"
AR "v76,a232\n"
AR "v77,a233\n"
AR "v78,a234\n"
AR "v79,a235\n"
"v_pk_fma_f32 v[72:73],v[72:73],v[60:61],v[54:55] op_sel:[0,0,1] op_sel_hi:[1,0,1]\n"
"v_pk_fma_f32 v[74:75],v[74:75],v[60:61],v[54:55] op_sel:[0,0,1] op_sel_hi:[1,0,1]\n"
"v_pk_fma_f32 v[76:77],v[76:77],v[60:61],v[54:55] op_sel:[0,0,1] op_sel_hi:[1,0,1]\n"
"v_pk_fma_f32 v[78:79],v[78:79],v[60:61],v[54:55] op_sel:[0,0,1] op_sel_hi:[1,0,1]\n"
CV "v112,v72,v73\n"
CV "v113,v74,v75\n"
CV "v114,v76,v77\n"
CV "v115,v78,v79\n"
AW "a32,v112\n"
AW "a33,v113\n"
AW "a34,v114\n"
AW "a35,v115\n"
AR "v80,a216\n"
AR "v81,a217\n"
AR "v82,a218\n"
AR "v83,a219\n"
AR "v84,a236\n"
AR "v85,a237\n"
AR "v86,a238\n"
AR "v87,a239\n"
"v_pk_fma_f32 v[80:81],v[80:81],v[60:61],v[56:57] op_sel:[0,1,0] op_sel_hi:[1,1,0]\n"
"v_pk_fma_f32 v[82:83],v[82:83],v[60:61],v[56:57] op_sel:[0,1,0] op_sel_hi:[1,1,0]\n"
"v_pk_fma_f32 v[84:85],v[84:85],v[60:61],v[56:57] op_sel:[0,1,0] op_sel_hi:[1,1,0]\n"
"v_pk_fma_f32 v[86:87],v[86:87],v[60:61],v[56:57] op_sel:[0,1,0] op_sel_hi:[1,1,0]\n"
CV "v116,v80,v81\n"
CV "v117,v82,v83\n"
CV "v118,v84,v85\n"
CV "v119,v86,v87\n"
AW "a36,v116\n"
AW "a37,v117\n"
AW "a38,v118\n"
AW "a39,v119\n"
"s_mov_b32 s13,s11\n"
"s_mov_b32 s70,s71\n"
"s_mov_b32 s57,1\n"
AR "v0,a240\n"
AR "v2,a241\n"
AR "v4,a242\n"
AR "v6,a243\n"
"v_xor_b32 v1,64,v0\n"
"v_xor_b32 v3,64,v2\n"
"v_xor_b32 v5,64,v4\n"
"v_add_u32 v11,97280,v6\n"
"v_add_u32 v12,194560,v6\n"
"v_add_u32 v13,291840,v6\n"
"s_mov_b64 vcc,-1\n"
"s_waitcnt vmcnt(8) lgkmcnt(0)\n"
"s_barrier\n"
"s_nop 0\n"
TR "a[200:201],v0\n"
TR "a[202:203],v0 offset:512\n"
TR "a[204:205],v0 offset:4096\n"
TR "a[206:207],v0 offset:4608\n"
"ds_read_b128 a[60:63],v2\n"
"ds_read_b128 a[100:103],v3\n"
"ds_read_b128 a[64:67],v2 offset:2048\n"
"ds_read_b128 a[104:107],v3 offset:2048\n"
"ds_read_b128 a[68:71],v2 offset:4096\n"
"ds_read_b128 a[108:111],v3 offset:4096\n"
"ds_read_b128 a[72:75],v2 offset:6144\n"
"ds_read_b128 a[112:115],v3 offset:6144\n"
"ds_read_b128 a[76:79],v2 offset:8192\n"
"ds_read_b128 a[116:119],v3 offset:8192\n"
TR "a[208:209],v0 offset:8\n"
TR "a[210:211],v0 offset:520\n"
TR "a[212:213],v0 offset:4104\n"
TR "a[214:215],v0 offset:4616\n"
"s_waitcnt lgkmcnt(0)\n"
"s_branch .Lq0\n"
".Lep:\n"
"s_lshl_b32 s58,s56,10\n"
"s_add_u32 s58,s58,73728\n"
"v_add_u32 v120,s58,v9\n"
"v_add_u32 v121,65536,v120\n"
"s_sub_u32 s59,s71,s70\n"
"s_nop 0\n"
TR "v[136:137],v2 offset:24576\n"
TR "v[138:139],v2 offset:30720\n"
TR "v[140:141],v3 offset:24576\n"
TR "v[142:143],v3 offset:30720\n"
TR "v[144:145],v4 offset:24576\n"
TR "v[146:147],v4 offset:30720\n"
TR "v[148:149],v5 offset:24576\n"
TR "v[150:151],v5 offset:30720\n"
TR "v[152:153],v2 offset:25600\n"
TR "v[154:155],v2 offset:31744\n"
TR "v[156:157],v3 offset:25600\n"
TR "v[158:159],v3 offset:31744\n"
TR "v[160:161],v4 offset:25600\n"
TR "v[162:163],v4 offset:31744\n"
TR "v[164:165],v5 offset:25600\n"
TR "v[166:167],v5 offset:31744\n"
TR "v[168:169],v2 offset:26624\n"
TR "v[170:171],v2 offset:32768\n"
TR "v[172:173],v3 offset:26624\n"
TR "v[174:175],v3 offset:32768\n"
TR "v[176:177],v4 offset:26624\n"
TR "v[178:179],v4 offset:32768\n"
TR "v[180:181],v5 offset:26624\n"
TR "v[182:183],v5 offset:32768\n"
"v_mov_b32 v52,v57\n"
"v_mov_b32 v53,v58\n"
"v_mov_b32 v54,v59\n"
"v_mov_b32 v55,v60\n"
"v_mov_b32_e64 v56,v61\n"
"s_add_u32 m0,s65,49152\n"
"s_nop 0\n"
LD "v8,s[36:39],s73 offen offset:3072 nt lds\n"
"s_add_u32 m0,s65,50048\n"
"s_nop 0\n"
LD "v8,s[36:39],s73 offen offset:3200 nt lds\n"
"v_permlane16_swap_b32 v57,v52\n"
"v_permlane16_swap_b32 v58,v53\n"
"v_permlane16_swap_b32 v59,v54\n"
"v_permlane16_swap_b32 v60,v55\n"
"v_permlane16_swap_b32 v61,v56\n"
"v_add_f32 v57,v57,v52\n"
"v_add_f32 v58,v58,v53\n"
"v_add_f32 v59,v59,v54\n"
"v_add_f32 v60,v60,v55\n"
"v_add_f32 v61,v61,v56\n"
"v_mov_b32 v52,v57\n"
"v_mov_b32 v53,v58\n"
"v_mov_b32 v54,v59\n"
"v_mov_b32 v55,v60\n"
"v_mov_b32 v56,v61\n"
"s_add_u32 m0,s65,50944\n"
"s_nop 0\n"
LD "v8,s[36:39],s73 offen offset:3328 nt lds\n"
"s_sub_u32 s74,s96,s16\n"
"s_add_u32 s74,s74,s71\n"
"s_add_u32 s20,s20,s59\n"
"s_mov_b32 s70,s71\n"
LD "v[232:235],v9,s[40:43],s75 offen sc1\n"
"v_permlane32_swap_b32 v57,v52\n"
"v_permlane32_swap_b32 v58,v53\n"
"v_permlane32_swap_b32 v59,v54\n"
"v_permlane32_swap_b32 v60,v55\n"
"v_permlane32_swap_b32 v61,v56\n"
"v_add_f32 v57,v57,v52\n"
"v_add_f32 v58,v58,v53\n"
"v_add_f32 v59,v59,v54\n"
"v_add_f32 v60,v60,v55\n"
"v_add_f32 v61,v61,v56\n"
"s_waitcnt lgkmcnt(15)\n"
MF "a[0:3],v[136:139],v[92:95],a[0:3]\n"
MF "a[20:23],v[140:143],v[92:95],a[20:23]\n"
"v_rcp_f32 v57,v57\n"
"v_rcp_f32 v58,v58\n"
"v_rcp_f32 v59,v59\n"
"v_rcp_f32 v60,v60\n"
"v_rcp_f32 v61,v61\n"
MF "a[4:7],v[136:139],v[96:99],a[4:7]\n"
"v_mul_f32_e64 v52,-s13,v57\n"
"v_mul_f32_e64 v53,-s13,v58\n"
"v_mul_f32_e64 v54,-s13,v59\n"
"v_mul_f32_e64 v55,-s13,v60\n"
"v_mul_f32_e64 v56,-s13,v61\n"
AR "v72,a0\n"
AR "v73,a1\n"
AR "v74,a2\n"
MF "a[24:27],v[140:143],v[96:99],a[24:27]\n"
AR "v75,a3\n"
AR "v76,a20\n"
AR "v77,a21\n"
AR "v78,a22\n"
AR "v79,a23\n"
"v_pk_fma_f32 v[72:73],v[72:73],v[56:57],v[52:53] op_sel:[0,1,0] op_sel_hi:[1,1,0]\n"
"v_pk_fma_f32 v[74:75],v[74:75],v[56:57],v[52:53] op_sel:[0,1,0] op_sel_hi:[1,1,0]\n"
"v_pk_fma_f32 v[76:77],v[76:77],v[56:57],v[52:53] op_sel:[0,1,0] op_sel_hi:[1,1,0]\n"
"v_pk_fma_f32 v[78:79],v[78:79],v[56:57],v[52:53] op_sel:[0,1,0] op_sel_hi:[1,1,0]\n"
CV "v112,v72,v73\n"
CV "v113,v74,v75\n"
CV "v114,v76,v77\n"
CV "v115,v78,v79\n"
MF "a[8:11],v[136:139],v[100:103],a[8:11]\n"
"ds_read_b128 v[184:187],v120 offset:24576\n"
"ds_write_b128 v120,v[112:115] offset:24576\n"
LD "v[236:239],v9,s[40:43],s75 offen offset:1024 sc1\n"
AR "v80,a4\n"
AR "v81,a5\n"
AR "v82,a6\n"
MF "a[28:31],v[140:143],v[100:103],a[28:31]\n"
AR "v83,a7\n"
AR "v84,a24\n"
AR "v85,a25\n"
AR "v86,a26\n"
AR "v87,a27\n"
"v_pk_fma_f32 v[80:81],v[80:81],v[58:59],v[52:53] op_sel:[0,0,1] op_sel_hi:[1,0,1]\n"
"v_pk_fma_f32 v[82:83],v[82:83],v[58:59],v[52:53] op_sel:[0,0,1] op_sel_hi:[1,0,1]\n"
"v_pk_fma_f32 v[84:85],v[84:85],v[58:59],v[52:53] op_sel:[0,0,1] op_sel_hi:[1,0,1]\n"
"v_pk_fma_f32 v[86:87],v[86:87],v[58:59],v[52:53] op_sel:[0,0,1] op_sel_hi:[1,0,1]\n"
CV "v116,v80,v81\n"
CV "v117,v82,v83\n"
CV "v118,v84,v85\n"
CV "v119,v86,v87\n"
MF "a[12:15],v[136:139],v[104:107],a[12:15]\n"
"ds_read_b128 v[188:191],v120 offset:28672\n"
"ds_write_b128 v120,v[116:119] offset:28672\n"
AR "v72,a8\n"
AR "v73,a9\n"
AR "v74,a10\n"
MF "a[32:35],v[140:143],v[104:107],a[32:35]\n"
AR "v75,a11\n"
AR "v76,a28\n"
AR "v77,a29\n"
AR "v78,a30\n"
AR "v79,a31\n"
"v_pk_fma_f32 v[72:73],v[72:73],v[58:59],v[54:55] op_sel:[0,1,0] op_sel_hi:[1,1,0]\n"
"v_pk_fma_f32 v[74:75],v[74:75],v[58:59],v[54:55] op_sel:[0,1,0] op_sel_hi:[1,1,0]\n"
"v_pk_fma_f32 v[76:77],v[76:77],v[58:59],v[54:55] op_sel:[0,1,0] op_sel_hi:[1,1,0]\n"
"v_pk_fma_f32 v[78:79],v[78:79],v[58:59],v[54:55] op_sel:[0,1,0] op_sel_hi:[1,1,0]\n"
CV "v112,v72,v73\n"
CV "v113,v74,v75\n"
CV "v114,v76,v77\n"
CV "v115,v78,v79\n"
MF "a[16:19],v[136:139],v[108:111],a[16:19]\n"
"ds_read_b128 v[192:195],v120 offset:32768\n"
"ds_write_b128 v120,v[112:115] offset:32768\n"
"s_waitcnt lgkmcnt(0)\n"
"s_barrier\n"
LD "v[240:243],v9,s[40:43],s75 offen offset:2048 sc1\n"
AR "v80,a12\n"
AR "v81,a13\n"
AR "v82,a14\n"
MF "a[36:39],v[140:143],v[108:111],a[36:39]\n"
AR "v83,a15\n"
AR "v84,a32\n"
AR "v85,a33\n"
AR "v86,a34\n"
AR "v87,a35\n"
"v_pk_fma_f32 v[80:81],v[80:81],v[60:61],v[54:55] op_sel:[0,0,1] op_sel_hi:[1,0,1]\n"
"v_pk_fma_f32 v[82:83],v[82:83],v[60:61],v[54:55] op_sel:[0,0,1] op_sel_hi:[1,0,1]\n"
"v_pk_fma_f32 v[84:85],v[84:85],v[60:61],v[54:55] op_sel:[0,0,1] op_sel_hi:[1,0,1]\n"
"v_pk_fma_f32 v[86:87],v[86:87],v[60:61],v[54:55] op_sel:[0,0,1] op_sel_hi:[1,0,1]\n"
CV "v116,v80,v81\n"
CV "v117,v82,v83\n"
CV "v118,v84,v85\n"
CV "v119,v86,v87\n"
MF "a[40:43],v[144:147],v[92:95],a[40:43]\n"
"ds_read_b128 v[196:199],v120 offset:36864\n"
"ds_write_b128 v120,v[116:119] offset:36864\n"
LD "v[244:247],v9,s[40:43],s75 offen offset:3072 sc1\n"
AR "v72,a16\n"
AR "v73,a17\n"
AR "v74,a18\n"
MF "a[60:63],v[148:151],v[92:95],a[60:63]\n"
AR "v75,a19\n"
AR "v76,a36\n"
AR "v77,a37\n"
AR "v78,a38\n"
AR "v79,a39\n"
"v_pk_fma_f32 v[72:73],v[72:73],v[60:61],v[56:57] op_sel:[0,1,0] op_sel_hi:[1,1,0]\n"
"v_pk_fma_f32 v[74:75],v[74:75],v[60:61],v[56:57] op_sel:[0,1,0] op_sel_hi:[1,1,0]\n"
"v_pk_fma_f32 v[76:77],v[76:77],v[60:61],v[56:57] op_sel:[0,1,0] op_sel_hi:[1,1,0]\n"
"v_pk_fma_f32 v[78:79],v[78:79],v[60:61],v[56:57] op_sel:[0,1,0] op_sel_hi:[1,1,0]\n"
CV "v112,v72,v73\n"
CV "v113,v74,v75\n"
CV "v114,v76,v77\n"
CV "v115,v78,v79\n"
MF "a[44:47],v[144:147],v[96:99],a[44:47]\n"
"ds_read_b128 v[200:203],v120 offset:40960\n"
"ds_write_b128 v120,v[112:115] offset:40960\n"
AR "v80,a40\n"
AR "v81,a41\n"
AR "v82,a42\n"
MF "a[64:67],v[148:151],v[96:99],a[64:67]\n"
AR "v83,a43\n"
AR "v84,a60\n"
AR "v85,a61\n"
AR "v86,a62\n"
AR "v87,a63\n"
"v_pk_fma_f32 v[80:81],v[80:81],v[56:57],v[52:53] op_sel:[0,1,0] op_sel_hi:[1,1,0]\n"
"v_pk_fma_f32 v[82:83],v[82:83],v[56:57],v[52:53] op_sel:[0,1,0] op_sel_hi:[1,1,0]\n"
"v_pk_fma_f32 v[84:85],v[84:85],v[56:57],v[52:53] op_sel:[0,1,0] op_sel_hi:[1,1,0]\n"
"v_pk_fma_f32 v[86:87],v[86:87],v[56:57],v[52:53] op_sel:[0,1,0] op_sel_hi:[1,1,0]\n"
CV "v116,v80,v81\n"
CV "v117,v82,v83\n"
CV "v118,v84,v85\n"
CV "v119,v86,v87\n"
MF "a[48:51],v[144:147],v[100:103],a[48:51]\n"
"ds_read_b128 v[204:207],v120 offset:45056\n"
"ds_write_b128 v120,v[116:119] offset:45056\n"
"s_add_u32 s58,s75,4096\n"
LD "v[248:251],v9,s[40:43],s58 offen sc1\n"
AR "v72,a44\n"
AR "v73,a45\n"
AR "v74,a46\n"
MF "a[68:71],v[148:151],v[100:103],a[68:71]\n"
AR "v75,a47\n"
AR "v76,a64\n"
AR "v77,a65\n"
AR "v78,a66\n"
AR "v79,a67\n"
"v_pk_fma_f32 v[72:73],v[72:73],v[58:59],v[52:53] op_sel:[0,0,1] op_sel_hi:[1,0,1]\n"
"v_pk_fma_f32 v[74:75],v[74:75],v[58:59],v[52:53] op_sel:[0,0,1] op_sel_hi:[1,0,1]\n"
"v_pk_fma_f32 v[76:77],v[76:77],v[58:59],v[52:53] op_sel:[0,0,1] op_sel_hi:[1,0,1]\n"
"v_pk_fma_f32 v[78:79],v[78:79],v[58:59],v[52:53] op_sel:[0,0,1] op_sel_hi:[1,0,1]\n"
CV "v112,v72,v73\n"
CV "v113,v74,v75\n"
CV "v114,v76,v77\n"
CV "v115,v78,v79\n"
MF "a[52:55],v[144:147],v[104:107],a[52:55]\n"
"ds_read_b128 v[208:211],v120 offset:49152\n"
"ds_write_b128 v120,v[112:115] offset:49152\n"
LD "v[252:255],v9,s[40:43],s58 offen offset:1024 sc1\n"
AR "v80,a48\n"
AR "v81,a49\n"
AR "v82,a50\n"
MF "a[72:75],v[148:151],v[104:107],a[72:75]\n"
AR "v83,a51\n"
AR "v84,a68\n"
AR "v85,a69\n"
AR "v86,a70\n"
AR "v87,a71\n"
"v_pk_fma_f32 v[80:81],v[80:81],v[58:59],v[54:55] op_sel:[0,1,0] op_sel_hi:[1,1,0]\n"
"v_pk_fma_f32 v[82:83],v[82:83],v[58:59],v[54:55] op_sel:[0,1,0] op_sel_hi:[1,1,0]\n"
"v_pk_fma_f32 v[84:85],v[84:85],v[58:59],v[54:55] op_sel:[0,1,0] op_sel_hi:[1,1,0]\n"
"v_pk_fma_f32 v[86:87],v[86:87],v[58:59],v[54:55] op_sel:[0,1,0] op_sel_hi:[1,1,0]\n"
CV "v116,v80,v81\n"
CV "v117,v82,v83\n"
CV "v118,v84,v85\n"
CV "v119,v86,v87\n"
MF "a[56:59],v[144:147],v[108:111],a[56:59]\n"
"ds_read_b128 v[212:215],v120 offset:53248\n"
"ds_write_b128 v120,v[116:119] offset:53248\n"
AR "v72,a52\n"
AR "v73,a53\n"
AR "v74,a54\n"
MF "a[76:79],v[148:151],v[108:111],a[76:79]\n"
AR "v75,a55\n"
AR "v76,a72\n"
AR "v77,a73\n"
AR "v78,a74\n"
AR "v79,a75\n"
"v_pk_fma_f32 v[72:73],v[72:73],v[60:61],v[54:55] op_sel:[0,0,1] op_sel_hi:[1,0,1]\n"
"v_pk_fma_f32 v[74:75],v[74:75],v[60:61],v[54:55] op_sel:[0,0,1] op_sel_hi:[1,0,1]\n"
"v_pk_fma_f32 v[76:77],v[76:77],v[60:61],v[54:55] op_sel:[0,0,1] op_sel_hi:[1,0,1]\n"
"v_pk_fma_f32 v[78:79],v[78:79],v[60:61],v[54:55] op_sel:[0,0,1] op_sel_hi:[1,0,1]\n"
CV "v112,v72,v73\n"
CV "v113,v74,v75\n"
CV "v114,v76,v77\n"
CV "v115,v78,v79\n"
MF "a[80:83],v[152:155],v[92:95],a[80:83]\n"
"ds_read_b128 v[216:219],v120 offset:57344\n"
"ds_write_b128 v120,v[112:115] offset:57344\n"
AR "v80,a56\n"
AR "v81,a57\n"
AR "v82,a58\n"
MF "a[100:103],v[156:159],v[92:95],a[100:103]\n"
AR "v83,a59\n"
AR "v84,a76\n"
AR "v85,a77\n"
AR "v86,a78\n"
AR "v87,a79\n"
"v_pk_fma_f32 v[80:81],v[80:81],v[60:61],v[56:57] op_sel:[0,1,0] op_sel_hi:[1,1,0]\n"
"v_pk_fma_f32 v[82:83],v[82:83],v[60:61],v[56:57] op_sel:[0,1,0] op_sel_hi:[1,1,0]\n"
"v_pk_fma_f32 v[84:85],v[84:85],v[60:61],v[56:57] op_sel:[0,1,0] op_sel_hi:[1,1,0]\n"
"v_pk_fma_f32 v[86:87],v[86:87],v[60:61],v[56:57] op_sel:[0,1,0] op_sel_hi:[1,1,0]\n"
CV "v116,v80,v81\n"
CV "v117,v82,v83\n"
CV "v118,v84,v85\n"
CV "v119,v86,v87\n"
MF "a[84:87],v[152:155],v[96:99],a[84:87]\n"
"ds_read_b128 v[220:223],v120 offset:61440\n"
"ds_write_b128 v120,v[116:119] offset:61440\n"
AR "v72,a80\n"
AR "v73,a81\n"
AR "v74,a82\n"
MF "a[104:107],v[156:159],v[96:99],a[104:107]\n"
AR "v75,a83\n"
AR "v76,a100\n"
AR "v77,a101\n"
AR "v78,a102\n"
AR "v79,a103\n"
"v_pk_fma_f32 v[72:73],v[72:73],v[56:57],v[52:53] op_sel:[0,1,0] op_sel_hi:[1,1,0]\n"
"v_pk_fma_f32 v[74:75],v[74:75],v[56:57],v[52:53] op_sel:[0,1,0] op_sel_hi:[1,1,0]\n"
"v_pk_fma_f32 v[76:77],v[76:77],v[56:57],v[52:53] op_sel:[0,1,0] op_sel_hi:[1,1,0]\n"
"v_pk_fma_f32 v[78:79],v[78:79],v[56:57],v[52:53] op_sel:[0,1,0] op_sel_hi:[1,1,0]\n"
CV "v112,v72,v73\n"
CV "v113,v74,v75\n"
CV "v114,v76,v77\n"
CV "v115,v78,v79\n"
MF "a[88:91],v[152:155],v[100:103],a[88:91]\n"
"ds_read_b128 v[224:227],v121\n"
"ds_write_b128 v121,v[112:115]\n"
AR "v80,a84\n"
AR "v81,a85\n"
AR "v82,a86\n"
MF "a[108:111],v[156:159],v[100:103],a[108:111]\n"
AR "v83,a87\n"
AR "v84,a104\n"
AR "v85,a105\n"
AR "v86,a106\n"
AR "v87,a107\n"
"v_pk_fma_f32 v[80:81],v[80:81],v[58:59],v[52:53] op_sel:[0,0,1] op_sel_hi:[1,0,1]\n"
"v_pk_fma_f32 v[82:83],v[82:83],v[58:59],v[52:53] op_sel:[0,0,1] op_sel_hi:[1,0,1]\n"
"v_pk_fma_f32 v[84:85],v[84:85],v[58:59],v[52:53] op_sel:[0,0,1] op_sel_hi:[1,0,1]\n"
"v_pk_fma_f32 v[86:87],v[86:87],v[58:59],v[52:53] op_sel:[0,0,1] op_sel_hi:[1,0,1]\n"
CV "v116,v80,v81\n"
CV "v117,v82,v83\n"
CV "v118,v84,v85\n"
CV "v119,v86,v87\n"
MF "a[92:95],v[152:155],v[104:107],a[92:95]\n"
"ds_read_b128 v[228:231],v121 offset:4096\n"
"ds_write_b128 v121,v[116:119] offset:4096\n"
AR "v72,a88\n"
AR "v73,a89\n"
AR "v74,a90\n"
MF "a[112:115],v[156:159],v[104:107],a[112:115]\n"
AR "v75,a91\n"
AR "v76,a108\n"
AR "v77,a109\n"
AR "v78,a110\n"
AR "v79,a111\n"
"v_pk_fma_f32 v[72:73],v[72:73],v[58:59],v[54:55] op_sel:[0,1,0] op_sel_hi:[1,1,0]\n"
"v_pk_fma_f32 v[74:75],v[74:75],v[58:59],v[54:55] op_sel:[0,1,0] op_sel_hi:[1,1,0]\n"
"v_pk_fma_f32 v[76:77],v[76:77],v[58:59],v[54:55] op_sel:[0,1,0] op_sel_hi:[1,1,0]\n"
"v_pk_fma_f32 v[78:79],v[78:79],v[58:59],v[54:55] op_sel:[0,1,0] op_sel_hi:[1,1,0]\n"
CV "v112,v72,v73\n"
CV "v113,v74,v75\n"
CV "v114,v76,v77\n"
CV "v115,v78,v79\n"
MF "a[96:99],v[152:155],v[108:111],a[96:99]\n"
"ds_read_b128 v[136:139],v121 offset:8192\n"
"ds_write_b128 v121,v[112:115] offset:8192\n"
AR "v80,a92\n"
AR "v81,a93\n"
AR "v82,a94\n"
MF "a[116:119],v[156:159],v[108:111],a[116:119]\n"
AR "v83,a95\n"
AR "v84,a112\n"
AR "v85,a113\n"
AR "v86,a114\n"
AR "v87,a115\n"
"v_pk_fma_f32 v[80:81],v[80:81],v[60:61],v[54:55] op_sel:[0,0,1] op_sel_hi:[1,0,1]\n"
"v_pk_fma_f32 v[82:83],v[82:83],v[60:61],v[54:55] op_sel:[0,0,1] op_sel_hi:[1,0,1]\n"
"v_pk_fma_f32 v[84:85],v[84:85],v[60:61],v[54:55] op_sel:[0,0,1] op_sel_hi:[1,0,1]\n"
"v_pk_fma_f32 v[86:87],v[86:87],v[60:61],v[54:55] op_sel:[0,0,1] op_sel_hi:[1,0,1]\n"
CV "v116,v80,v81\n"
CV "v117,v82,v83\n"
CV "v118,v84,v85\n"
CV "v119,v86,v87\n"
MF "a[120:123],v[160:163],v[92:95],a[120:123]\n"
"ds_read_b128 v[140:143],v121 offset:12288\n"
"ds_write_b128 v121,v[116:119] offset:12288\n"
AR "v72,a96\n"
AR "v73,a97\n"
AR "v74,a98\n"
MF "a[140:143],v[164:167],v[92:95],a[140:143]\n"
AR "v75,a99\n"
AR "v76,a116\n"
AR "v77,a117\n"
AR "v78,a118\n"
AR "v79,a119\n"
"v_pk_fma_f32 v[72:73],v[72:73],v[60:61],v[56:57] op_sel:[0,1,0] op_sel_hi:[1,1,0]\n"
"v_pk_fma_f32 v[74:75],v[74:75],v[60:61],v[56:57] op_sel:[0,1,0] op_sel_hi:[1,1,0]\n"
"v_pk_fma_f32 v[76:77],v[76:77],v[60:61],v[56:57] op_sel:[0,1,0] op_sel_hi:[1,1,0]\n"
"v_pk_fma_f32 v[78:79],v[78:79],v[60:61],v[56:57] op_sel:[0,1,0] op_sel_hi:[1,1,0]\n"
CV "v112,v72,v73\n"
CV "v113,v74,v75\n"
CV "v114,v76,v77\n"
CV "v115,v78,v79\n"
MF "a[124:127],v[160:163],v[96:99],a[124:127]\n"
"ds_read_b128 v[144:147],v121 offset:16384\n"
"ds_write_b128 v121,v[112:115] offset:16384\n"
AR "v80,a120\n"
AR "v81,a121\n"
AR "v82,a122\n"
MF "a[144:147],v[164:167],v[96:99],a[144:147]\n"
AR "v83,a123\n"
AR "v84,a140\n"
AR "v85,a141\n"
AR "v86,a142\n"
AR "v87,a143\n"
"v_pk_fma_f32 v[80:81],v[80:81],v[56:57],v[52:53] op_sel:[0,1,0] op_sel_hi:[1,1,0]\n"
"v_pk_fma_f32 v[82:83],v[82:83],v[56:57],v[52:53] op_sel:[0,1,0] op_sel_hi:[1,1,0]\n"
"v_pk_fma_f32 v[84:85],v[84:85],v[56:57],v[52:53] op_sel:[0,1,0] op_sel_hi:[1,1,0]\n"
"v_pk_fma_f32 v[86:87],v[86:87],v[56:57],v[52:53] op_sel:[0,1,0] op_sel_hi:[1,1,0]\n"
CV "v116,v80,v81\n"
CV "v117,v82,v83\n"
CV "v118,v84,v85\n"
CV "v119,v86,v87\n"
MF "a[128:131],v[160:163],v[100:103],a[128:131]\n"
"ds_read_b128 v[148:151],v121 offset:20480\n"
"ds_write_b128 v121,v[116:119] offset:20480\n"
AR "v72,a124\n"
AR "v73,a125\n"
AR "v74,a126\n"
MF "a[148:151],v[164:167],v[100:103],a[148:151]\n"
AR "v75,a127\n"
AR "v76,a144\n"
AR "v77,a145\n"
AR "v78,a146\n"
AR "v79,a147\n"
"v_pk_fma_f32 v[72:73],v[72:73],v[58:59],v[52:53] op_sel:[0,0,1] op_sel_hi:[1,0,1]\n"
"v_pk_fma_f32 v[74:75],v[74:75],v[58:59],v[52:53] op_sel:[0,0,1] op_sel_hi:[1,0,1]\n"
"v_pk_fma_f32 v[76:77],v[76:77],v[58:59],v[52:53] op_sel:[0,0,1] op_sel_hi:[1,0,1]\n"
"v_pk_fma_f32 v[78:79],v[78:79],v[58:59],v[52:53] op_sel:[0,0,1] op_sel_hi:[1,0,1]\n"
CV "v112,v72,v73\n"
CV "v113,v74,v75\n"
CV "v114,v76,v77\n"
CV "v115,v78,v79\n"
MF "a[132:135],v[160:163],v[104:107],a[132:135]\n"
"ds_write_b128 v120,v[112:115]\n"
AR "v80,a128\n"
AR "v81,a129\n"
AR "v82,a130\n"
MF "a[152:155],v[164:167],v[104:107],a[152:155]\n"
AR "v83,a131\n"
AR "v84,a148\n"
AR "v85,a149\n"
AR "v86,a150\n"
AR "v87,a151\n"
"v_pk_fma_f32 v[80:81],v[80:81],v[58:59],v[54:55] op_sel:[0,1,0] op_sel_hi:[1,1,0]\n"
"v_pk_fma_f32 v[82:83],v[82:83],v[58:59],v[54:55] op_sel:[0,1,0] op_sel_hi:[1,1,0]\n"
"v_pk_fma_f32 v[84:85],v[84:85],v[58:59],v[54:55] op_sel:[0,1,0] op_sel_hi:[1,1,0]\n"
"v_pk_fma_f32 v[86:87],v[86:87],v[58:59],v[54:55] op_sel:[0,1,0] op_sel_hi:[1,1,0]\n"
CV "v116,v80,v81\n"
CV "v117,v82,v83\n"
CV "v118,v84,v85\n"
CV "v119,v86,v87\n"
MF "a[136:139],v[160:163],v[108:111],a[136:139]\n"
"ds_write_b128 v120,v[116:119] offset:4096\n"
AR "v72,a132\n"
AR "v73,a133\n"
AR "v74,a134\n"
MF "a[156:159],v[164:167],v[108:111],a[156:159]\n"
AR "v75,a135\n"
AR "v76,a152\n"
AR "v77,a153\n"
AR "v78,a154\n"
AR "v79,a155\n"
"v_pk_fma_f32 v[72:73],v[72:73],v[60:61],v[54:55] op_sel:[0,0,1] op_sel_hi:[1,0,1]\n"
"v_pk_fma_f32 v[74:75],v[74:75],v[60:61],v[54:55] op_sel:[0,0,1] op_sel_hi:[1,0,1]\n"
"v_pk_fma_f32 v[76:77],v[76:77],v[60:61],v[54:55] op_sel:[0,0,1] op_sel_hi:[1,0,1]\n"
"v_pk_fma_f32 v[78:79],v[78:79],v[60:61],v[54:55] op_sel:[0,0,1] op_sel_hi:[1,0,1]\n"
CV "v112,v72,v73\n"
CV "v113,v74,v75\n"
CV "v114,v76,v77\n"
CV "v115,v78,v79\n"
MF "a[160:163],v[168:171],v[92:95],a[160:163]\n"
"ds_write_b128 v120,v[112:115] offset:8192\n"
AR "v80,a136\n"
AR "v81,a137\n"
AR "v82,a138\n"
MF "a[180:183],v[172:175],v[92:95],a[180:183]\n"
AR "v83,a139\n"
AR "v84,a156\n"
AR "v85,a157\n"
AR "v86,a158\n"
AR "v87,a159\n"
"v_pk_fma_f32 v[80:81],v[80:81],v[60:61],v[56:57] op_sel:[0,1,0] op_sel_hi:[1,1,0]\n"
"v_pk_fma_f32 v[82:83],v[82:83],v[60:61],v[56:57] op_sel:[0,1,0] op_sel_hi:[1,1,0]\n"
"v_pk_fma_f32 v[84:85],v[84:85],v[60:61],v[56:57] op_sel:[0,1,0] op_sel_hi:[1,1,0]\n"
"v_pk_fma_f32 v[86:87],v[86:87],v[60:61],v[56:57] op_sel:[0,1,0] op_sel_hi:[1,1,0]\n"
CV "v116,v80,v81\n"
CV "v117,v82,v83\n"
CV "v118,v84,v85\n"
CV "v119,v86,v87\n"
MF "a[164:167],v[168:171],v[96:99],a[164:167]\n"
"ds_write_b128 v120,v[116:119] offset:12288\n"
AR "v72,a160\n"
AR "v73,a161\n"
AR "v74,a162\n"
MF "a[184:187],v[172:175],v[96:99],a[184:187]\n"
AR "v75,a163\n"
AR "v76,a180\n"
AR "v77,a181\n"
AR "v78,a182\n"
AR "v79,a183\n"
"v_pk_fma_f32 v[72:73],v[72:73],v[56:57],v[52:53] op_sel:[0,1,0] op_sel_hi:[1,1,0]\n"
"v_pk_fma_f32 v[74:75],v[74:75],v[56:57],v[52:53] op_sel:[0,1,0] op_sel_hi:[1,1,0]\n"
"v_pk_fma_f32 v[76:77],v[76:77],v[56:57],v[52:53] op_sel:[0,1,0] op_sel_hi:[1,1,0]\n"
"v_pk_fma_f32 v[78:79],v[78:79],v[56:57],v[52:53] op_sel:[0,1,0] op_sel_hi:[1,1,0]\n"
CV "v112,v72,v73\n"
CV "v113,v74,v75\n"
CV "v114,v76,v77\n"
CV "v115,v78,v79\n"
MF "a[168:171],v[168:171],v[100:103],a[168:171]\n"
"ds_write_b128 v120,v[112:115] offset:16384\n"
AR "v80,a164\n"
AR "v81,a165\n"
AR "v82,a166\n"
MF "a[188:191],v[172:175],v[100:103],a[188:191]\n"
AR "v83,a167\n"
AR "v84,a184\n"
AR "v85,a185\n"
AR "v86,a186\n"
AR "v87,a187\n"
"v_pk_fma_f32 v[80:81],v[80:81],v[58:59],v[52:53] op_sel:[0,0,1] op_sel_hi:[1,0,1]\n"
"v_pk_fma_f32 v[82:83],v[82:83],v[58:59],v[52:53] op_sel:[0,0,1] op_sel_hi:[1,0,1]\n"
"v_pk_fma_f32 v[84:85],v[84:85],v[58:59],v[52:53] op_sel:[0,0,1] op_sel_hi:[1,0,1]\n"
"v_pk_fma_f32 v[86:87],v[86:87],v[58:59],v[52:53] op_sel:[0,0,1] op_sel_hi:[1,0,1]\n"
CV "v116,v80,v81\n"
CV "v117,v82,v83\n"
CV "v118,v84,v85\n"
CV "v119,v86,v87\n"
MF "a[172:175],v[168:171],v[104:107],a[172:175]\n"
"ds_write_b128 v120,v[116:119] offset:20480\n"
AR "v72,a168\n"
AR "v73,a169\n"
AR "v74,a170\n"
MF "a[192:195],v[172:175],v[104:107],a[192:195]\n"
AR "v75,a171\n"
AR "v76,a188\n"
AR "v77,a189\n"
AR "v78,a190\n"
AR "v79,a191\n"
"v_pk_fma_f32 v[72:73],v[72:73],v[58:59],v[54:55] op_sel:[0,1,0] op_sel_hi:[1,1,0]\n"
"v_pk_fma_f32 v[74:75],v[74:75],v[58:59],v[54:55] op_sel:[0,1,0] op_sel_hi:[1,1,0]\n"
"v_pk_fma_f32 v[76:77],v[76:77],v[58:59],v[54:55] op_sel:[0,1,0] op_sel_hi:[1,1,0]\n"
"v_pk_fma_f32 v[78:79],v[78:79],v[58:59],v[54:55] op_sel:[0,1,0] op_sel_hi:[1,1,0]\n"
CV "v112,v72,v73\n"
CV "v113,v74,v75\n"
CV "v114,v76,v77\n"
CV "v115,v78,v79\n"
MF "a[176:179],v[168:171],v[108:111],a[176:179]\n"
AR "v152,a240\n"
AW "a240,v112\n"
AR "v153,a241\n"
AW "a241,v113\n"
AR "v154,a242\n"
AW "a242,v114\n"
AR "v155,a243\n"
AW "a243,v115\n"
AR "v80,a172\n"
AR "v81,a173\n"
AR "v82,a174\n"
MF "a[196:199],v[172:175],v[108:111],a[196:199]\n"
AR "v83,a175\n"
AR "v84,a192\n"
AR "v85,a193\n"
AR "v86,a194\n"
AR "v87,a195\n"
"v_pk_fma_f32 v[80:81],v[80:81],v[60:61],v[54:55] op_sel:[0,0,1] op_sel_hi:[1,0,1]\n"
"v_pk_fma_f32 v[82:83],v[82:83],v[60:61],v[54:55] op_sel:[0,0,1] op_sel_hi:[1,0,1]\n"
"v_pk_fma_f32 v[84:85],v[84:85],v[60:61],v[54:55] op_sel:[0,0,1] op_sel_hi:[1,0,1]\n"
"v_pk_fma_f32 v[86:87],v[86:87],v[60:61],v[54:55] op_sel:[0,0,1] op_sel_hi:[1,0,1]\n"
CV "v116,v80,v81\n"
CV "v117,v82,v83\n"
CV "v118,v84,v85\n"
CV "v119,v86,v87\n"
MF "a[200:203],v[176:179],v[92:95],a[200:203]\n"
AR "v156,a244\n"
AW "a244,v116\n"
AR "v157,a245\n"
AW "a245,v117\n"
AR "v158,a246\n"
AW "a246,v118\n"
AR "v159,a247\n"
AW "a247,v119\n"
AR "v72,a176\n"
AR "v73,a177\n"
AR "v74,a178\n"
MF "a[220:223],v[180:183],v[92:95],a[220:223]\n"
AR "v75,a179\n"
AR "v76,a196\n"
AR "v77,a197\n"
AR "v78,a198\n"
AR "v79,a199\n"
"v_pk_fma_f32 v[72:73],v[72:73],v[60:61],v[56:57] op_sel:[0,1,0] op_sel_hi:[1,1,0]\n"
"v_pk_fma_f32 v[74:75],v[74:75],v[60:61],v[56:57] op_sel:[0,1,0] op_sel_hi:[1,1,0]\n"
"v_pk_fma_f32 v[76:77],v[76:77],v[60:61],v[56:57] op_sel:[0,1,0] op_sel_hi:[1,1,0]\n"
"v_pk_fma_f32 v[78:79],v[78:79],v[60:61],v[56:57] op_sel:[0,1,0] op_sel_hi:[1,1,0]\n"
CV "v112,v72,v73\n"
CV "v113,v74,v75\n"
CV "v114,v76,v77\n"
CV "v115,v78,v79\n"
MF "a[204:207],v[176:179],v[96:99],a[204:207]\n"
AR "v160,a248\n"
AW "a248,v112\n"
AR "v161,a249\n"
AW "a249,v113\n"
AR "v162,a250\n"
AW "a250,v114\n"
AR "v163,a251\n"
AW "a251,v115\n"
AR "v80,a200\n"
AR "v81,a201\n"
AR "v82,a202\n"
MF "a[224:227],v[180:183],v[96:99],a[224:227]\n"
AR "v83,a203\n"
AR "v84,a220\n"
AR "v85,a221\n"
AR "v86,a222\n"
AR "v87,a223\n"
"v_pk_fma_f32 v[80:81],v[80:81],v[56:57],v[52:53] op_sel:[0,1,0] op_sel_hi:[1,1,0]\n"
"v_pk_fma_f32 v[82:83],v[82:83],v[56:57],v[52:53] op_sel:[0,1,0] op_sel_hi:[1,1,0]\n"
"v_pk_fma_f32 v[84:85],v[84:85],v[56:57],v[52:53] op_sel:[0,1,0] op_sel_hi:[1,1,0]\n"
"v_pk_fma_f32 v[86:87],v[86:87],v[56:57],v[52:53] op_sel:[0,1,0] op_sel_hi:[1,1,0]\n"
CV "v116,v80,v81\n"
CV "v117,v82,v83\n"
CV "v118,v84,v85\n"
CV "v119,v86,v87\n"
MF "a[208:211],v[176:179],v[100:103],a[208:211]\n"
"v_mov_b32 v164,v62\n"
"v_mov_b32 v62,v116\n"
"v_mov_b32 v165,v63\n"
"v_mov_b32 v63,v117\n"
"v_mov_b32 v166,v64\n"
"v_mov_b32 v64,v118\n"
"v_mov_b32 v167,v65\n"
"v_mov_b32 v65,v119\n"
AR "v72,a204\n"
AR "v73,a205\n"
AR "v74,a206\n"
MF "a[228:231],v[180:183],v[100:103],a[228:231]\n"
AR "v75,a207\n"
AR "v76,a224\n"
AR "v77,a225\n"
AR "v78,a226\n"
AR "v79,a227\n"
"v_pk_fma_f32 v[72:73],v[72:73],v[58:59],v[52:53] op_sel:[0,0,1] op_sel_hi:[1,0,1]\n"
"v_pk_fma_f32 v[74:75],v[74:75],v[58:59],v[52:53] op_sel:[0,0,1] op_sel_hi:[1,0,1]\n"
"v_pk_fma_f32 v[76:77],v[76:77],v[58:59],v[52:53] op_sel:[0,0,1] op_sel_hi:[1,0,1]\n"
"v_pk_fma_f32 v[78:79],v[78:79],v[58:59],v[52:53] op_sel:[0,0,1] op_sel_hi:[1,0,1]\n"
CV "v112,v72,v73\n"
CV "v113,v74,v75\n"
CV "v114,v76,v77\n"
CV "v115,v78,v79\n"
MF "a[212:215],v[176:179],v[104:107],a[212:215]\n"
"v_mov_b32 v168,v66\n"
"v_mov_b32 v66,v112\n"
"v_mov_b32 v169,v67\n"
"v_mov_b32 v67,v113\n"
"v_mov_b32 v170,v68\n"
"v_mov_b32 v68,v114\n"
"v_mov_b32 v171,v69\n"
"v_mov_b32 v69,v115\n"
AR "v80,a208\n"
AR "v81,a209\n"
AR "v82,a210\n"
MF "a[232:235],v[180:183],v[104:107],a[232:235]\n"
AR "v83,a211\n"
AR "v84,a228\n"
AR "v85,a229\n"
AR "v86,a230\n"
AR "v87,a231\n"
"v_pk_fma_f32 v[80:81],v[80:81],v[58:59],v[54:55] op_sel:[0,1,0] op_sel_hi:[1,1,0]\n"
"v_pk_fma_f32 v[82:83],v[82:83],v[58:59],v[54:55] op_sel:[0,1,0] op_sel_hi:[1,1,0]\n"
"v_pk_fma_f32 v[84:85],v[84:85],v[58:59],v[54:55] op_sel:[0,1,0] op_sel_hi:[1,1,0]\n"
"v_pk_fma_f32 v[86:87],v[86:87],v[58:59],v[54:55] op_sel:[0,1,0] op_sel_hi:[1,1,0]\n"
CV "v116,v80,v81\n"
CV "v117,v82,v83\n"
CV "v118,v84,v85\n"
CV "v119,v86,v87\n"
MF "a[216:219],v[176:179],v[108:111],a[216:219]\n"
"v_mov_b32 v172,v70\n"
"v_mov_b32 v70,v116\n"
"v_mov_b32 v173,v71\n"
"v_mov_b32 v71,v117\n"
"v_mov_b32 v174,v132\n"
"v_mov_b32 v132,v118\n"
"v_mov_b32 v175,v133\n"
"v_mov_b32 v133,v119\n"
AR "v72,a212\n"
AR "v73,a213\n"
AR "v74,a214\n"
MF "a[236:239],v[180:183],v[108:111],a[236:239]\n"
AR "v75,a215\n"
AR "v76,a232\n"
AR "v77,a233\n"
AR "v78,a234\n"
AR "v79,a235\n"
"v_pk_fma_f32 v[72:73],v[72:73],v[60:61],v[54:55] op_sel:[0,0,1] op_sel_hi:[1,0,1]\n"
"v_pk_fma_f32 v[74:75],v[74:75],v[60:61],v[54:55] op_sel:[0,0,1] op_sel_hi:[1,0,1]\n"
"v_pk_fma_f32 v[76:77],v[76:77],v[60:61],v[54:55] op_sel:[0,0,1] op_sel_hi:[1,0,1]\n"
"v_pk_fma_f32 v[78:79],v[78:79],v[60:61],v[54:55] op_sel:[0,0,1] op_sel_hi:[1,0,1]\n"
CV "v112,v72,v73\n"
CV "v113,v74,v75\n"
CV "v114,v76,v77\n"
CV "v115,v78,v79\n"
"v_mov_b32 v176,v134\n"
"v_mov_b32 v134,v112\n"
"v_mov_b32 v177,v135\n"
"v_mov_b32 v135,v113\n"
"v_mov_b32 v178,v6\n"
"v_mov_b32 v6,v114\n"
"v_mov_b32 v179,v13\n"
"v_mov_b32 v13,v115\n"
AR "v80,a216\n"
AR "v81,a217\n"
AR "v82,a218\n"
AR "v83,a219\n"
AR "v84,a236\n"
AR "v85,a237\n"
AR "v86,a238\n"
AR "v87,a239\n"
"v_pk_fma_f32 v[80:81],v[80:81],v[60:61],v[56:57] op_sel:[0,1,0] op_sel_hi:[1,1,0]\n"
"v_pk_fma_f32 v[82:83],v[82:83],v[60:61],v[56:57] op_sel:[0,1,0] op_sel_hi:[1,1,0]\n"
"v_pk_fma_f32 v[84:85],v[84:85],v[60:61],v[56:57] op_sel:[0,1,0] op_sel_hi:[1,1,0]\n"
"v_pk_fma_f32 v[86:87],v[86:87],v[60:61],v[56:57] op_sel:[0,1,0] op_sel_hi:[1,1,0]\n"
CV "v116,v80,v81\n"
CV "v117,v82,v83\n"
CV "v118,v84,v85\n"
CV "v119,v86,v87\n"
"v_mov_b32 v180,v14\n"
"v_mov_b32 v14,v116\n"
"v_mov_b32 v181,v15\n"
"v_mov_b32 v15,v117\n"
AR "v182,a254\n"
AW "a254,v118\n"
AR "v183,a255\n"
AW "a255,v119\n"
"s_mov_b32 s13,s11\n"
"v_subrev_u32 v2,49152,v2\n"
"v_subrev_u32 v3,49152,v3\n"
"v_subrev_u32 v4,49152,v4\n"
"v_subrev_u32 v5,49152,v5\n"
"s_mov_b32 s57,2\n"
"s_mov_b32 s69,1\n"
"s_mov_b32 s61,3\n"
"s_memrealtime s[100:101]\n"
"s_waitcnt lgkmcnt(0)\n"
"s_cmp_eq_u32 s87,1\n"
"s_cselect_b32 s97,0,0\n"
"s_cselect_b32 s21,215,0\n"
"s_add_u32 s21,s21,s100\n"
"s_waitcnt vmcnt(0)\n"
"s_branch .Latt2\n"
".LattB:\n"
AR "v0,a244\n"
AR "v2,a245\n"
"v_xor_b32 v1,64,v0\n"
"v_xor_b32 v3,32,v2\n"
"v_xor_b32 v4,64,v2\n"
"v_xor_b32 v5,96,v2\n"
"s_mov_b32 s61,3\n"
"s_memtime s[52:53]\n"
"s_memrealtime s[100:101]\n"
"s_waitcnt lgkmcnt(0)\n"
"s_sub_u32 s53,s52,s80\n"
"s_cmp_gt_u32 s53,27000\n"
"s_cselect_b32 s101,1,0\n"
"s_cmp_eq_u32 s87,2\n"
"s_cselect_b32 s87,s101,s87\n"
"s_cmp_eq_u32 s87,1\n"
"s_cselect_b32 s97,0,0\n"
"s_cselect_b32 s21,215,0\n"
"s_add_u32 s21,s21,s100\n"
".Latt2:\n"
"s_cmp_eq_u32 s97,0\n"
"s_cselect_b32 s95,0x47800000,0\n"
"s_mov_b32 s77,s95\n"
"s_waitcnt vmcnt(12) lgkmcnt(0)\n"
"s_barrier\n"
"ds_read_b128 v[16:19],v0\n"
"ds_read_b128 v[20:23],v1\n"
"ds_read_b128 v[24:27],v0 offset:1024\n"
"s_cmp_eq_u32 s57,1\n"
"s_cbranch_scc1 .Lt0k\n"
"s_waitcnt lgkmcnt(2)\n"
MF "v[72:75],v[16:19],v[136:139],0\n"
"ds_read_b128 v[28:31],v1 offset:1024\n"
MF "v[76:79],v[16:19],v[140:143],0\n"
MF "v[80:83],v[16:19],v[144:147],0\n"
MF "v[84:87],v[16:19],v[148:151],0\n"
MF "v[88:91],v[16:19],v[152:155],0\n"
"s_waitcnt lgkmcnt(2)\n"
"ds_read_b128 v[16:19],v0 offset:2048\n"
MF "v[72:75],v[20:23],v[156:159],v[72:75]\n"
MF "v[76:79],v[20:23],v[160:163],v[76:79]\n"
MF "v[80:83],v[20:23],v[164:167],v[80:83]\n"
MF "v[84:87],v[20:23],v[168:171],v[84:87]\n"
MF "v[88:91],v[20:23],v[172:175],v[88:91]\n"
"s_waitcnt lgkmcnt(2)\n"
"ds_read_b128 v[20:23],v1 offset:2048\n"
MF "v[72:75],v[24:27],v[176:179],v[72:75]\n"
MF "v[76:79],v[24:27],v[180:183],v[76:79]\n"
MF "v[80:83],v[24:27],v[184:187],v[80:83]\n"
MF "v[84:87],v[24:27],v[188:191],v[84:87]\n"
MF "v[88:91],v[24:27],v[192:195],v[88:91]\n"
"s_waitcnt lgkmcnt(2)\n"
"ds_read_b128 v[24:27],v0 offset:6144\n"
MF "v[72:75],v[28:31],v[196:199],v[72:75]\n"
MF "v[76:79],v[28:31],v[200:203],v[76:79]\n"
MF "v[80:83],v[28:31],v[204:207],v[80:83]\n"
MF "v[84:87],v[28:31],v[208:211],v[84:87]\n"
MF "v[88:91],v[28:31],v[212:215],v[88:91]\n"
"s_waitcnt lgkmcnt(2)\n"
"ds_read_b128 v[28:31],v1 offset:6144\n"
MF "v[72:75],v[16:19],v[216:219],v[72:75]\n"
MF "v[76:79],v[16:19],v[220:223],v[76:79]\n"
MF "v[80:83],v[16:19],v[224:227],v[80:83]\n"
MF "v[84:87],v[16:19],v[228:231],v[84:87]\n"
MF "v[88:91],v[16:19],v[232:235],v[88:91]\n"
"s_waitcnt lgkmcnt(2)\n"
"ds_read_b128 v[16:19],v0 offset:7168\n"
MF "v[72:75],v[20:23],v[236:239],v[72:75]\n"
MF "v[76:79],v[20:23],v[240:243],v[76:79]\n"
MF "v[80:83],v[20:23],v[244:247],v[80:83]\n"
MF "v[84:87],v[20:23],v[248:251],v[84:87]\n"
MF "v[88:91],v[20:23],v[252:255],v[88:91]\n"
"s_waitcnt lgkmcnt(2)\n"
"ds_read_b128 v[20:23],v1 offset:7168\n"
MF "v[112:115],v[24:27],v[136:139],0\n"
MF "v[116:119],v[24:27],v[140:143],0\n"
MF "v[120:123],v[24:27],v[144:147],0\n"
MF "v[124:127],v[24:27],v[148:151],0\n"
MF "v[128:131],v[24:27],v[152:155],0\n"
"s_waitcnt lgkmcnt(2)\n"
"ds_read_b128 v[24:27],v0 offset:8192\n"
MF "v[112:115],v[28:31],v[156:159],v[112:115]\n"
"v_max3_f32 v52,v72,v73,v74\n"
"v_max_f32 v52,v52,v75\n"
MF "v[116:119],v[28:31],v[160:163],v[116:119]\n"
"v_max3_f32 v53,v76,v77,v78\n"
"v_max_f32 v53,v53,v79\n"
MF "v[120:123],v[28:31],v[164:167],v[120:123]\n"
"v_max3_f32 v54,v80,v81,v82\n"
"v_max_f32 v54,v54,v83\n"
MF "v[124:127],v[28:31],v[168:171],v[124:127]\n"
"v_max3_f32 v55,v84,v85,v86\n"
"v_max_f32 v55,v55,v87\n"
MF "v[128:131],v[28:31],v[172:175],v[128:131]\n"
"s_waitcnt lgkmcnt(2)\n"
"ds_read_b128 v[28:31],v1 offset:8192\n"
"v_max3_f32 v56,v88,v89,v90\n"
MF "v[112:115],v[16:19],v[176:179],v[112:115]\n"
"v_max_f32 v56,v56,v91\n"
MF "v[116:119],v[16:19],v[180:183],v[116:119]\n"
MF "v[120:123],v[16:19],v[184:187],v[120:123]\n"
MF "v[124:127],v[16:19],v[188:191],v[124:127]\n"
MF "v[128:131],v[16:19],v[192:195],v[128:131]\n"
"s_waitcnt lgkmcnt(2)\n"
MF "v[112:115],v[20:23],v[196:199],v[112:115]\n"
MF "v[116:119],v[20:23],v[200:203],v[116:119]\n"
"s_waitcnt lgkmcnt(0)\n"
"s_barrier\n"
MF "v[120:123],v[20:23],v[204:207],v[120:123]\n"
"s_add_u32 s72,s72,6144\n"
MF "v[124:127],v[20:23],v[208:211],v[124:127]\n"
"s_cmp_eq_u32 s72,s17\n"
"s_cselect_b32 s72,s16,s72\n"
"s_add_u32 m0,s64,0\n"
MF "v[128:131],v[20:23],v[212:215],v[128:131]\n"
LD "v7,s[32:35],s72 offen nt lds\n"
MF "v[112:115],v[24:27],v[216:219],v[112:115]\n"
"s_add_u32 m0,s64,896\n"
MF "v[116:119],v[24:27],v[220:223],v[116:119]\n"
LD "v7,s[32:35],s72 offen offset:128 nt lds\n"
MF "v[120:123],v[24:27],v[224:227],v[120:123]\n"
"s_add_u32 m0,s64,1792\n"
MF "v[124:127],v[24:27],v[228:231],v[124:127]\n"
LD "v7,s[32:35],s72 offen offset:256 nt lds\n"
MF "v[128:131],v[24:27],v[232:235],v[128:131]\n"
"s_add_u32 s72,s72,6144\n"
"s_add_u32 m0,s64,6144\n"
MF "v[112:115],v[28:31],v[236:239],v[112:115]\n"
LD "v7,s[32:35],s72 offen nt lds\n"
MF "v[116:119],v[28:31],v[240:243],v[116:119]\n"
"s_add_u32 m0,s64,7040\n"
MF "v[120:123],v[28:31],v[244:247],v[120:123]\n"
LD "v7,s[32:35],s72 offen offset:128 nt lds\n"
MF "v[124:127],v[28:31],v[248:251],v[124:127]\n"
"s_add_u32 m0,s64,7936\n"
MF "v[128:131],v[28:31],v[252:255],v[128:131]\n"
LD "v7,s[32:35],s72 offen offset:256 nt lds\n"
"s_branch .Lt0e\n"
".Lt0k:\n"
"s_lshl_b32 s58,s56,10\n"
"s_add_u32 s58,s58,73728\n"
"v_add_u32 v32,s58,v9\n"
"v_add_u32 v33,65536,v32\n"
"s_waitcnt lgkmcnt(2)\n"
MF "v[72:75],v[16:19],v[136:139],0\n"
"ds_read_b128 v[28:31],v1 offset:1024\n"
AR "v62,a20\n"
MF "v[76:79],v[16:19],v[140:143],0\n"
"ds_write_b128 v32,a[0:3] offset:24576\n"
MF "v[80:83],v[16:19],v[144:147],0\n"
"ds_write_b128 v32,a[4:7] offset:28672\n"
MF "v[84:87],v[16:19],v[148:151],0\n"
AR "v63,a21\n"
AR "v64,a22\n"
MF "v[88:91],v[16:19],v[152:155],0\n"
"s_waitcnt lgkmcnt(4)\n"
"ds_read_b128 v[16:19],v0 offset:2048\n"
AR "v65,a23\n"
MF "v[72:75],v[20:23],v[156:159],v[72:75]\n"
"ds_write_b128 v32,a[8:11] offset:32768\n"
MF "v[76:79],v[20:23],v[160:163],v[76:79]\n"
AR "v66,a24\n"
AR "v67,a25\n"
MF "v[80:83],v[20:23],v[164:167],v[80:83]\n"
"ds_write_b128 v32,a[12:15] offset:36864\n"
MF "v[84:87],v[20:23],v[168:171],v[84:87]\n"
AR "v68,a26\n"
AR "v69,a27\n"
MF "v[88:91],v[20:23],v[172:175],v[88:91]\n"
"s_waitcnt lgkmcnt(6)\n"
"ds_read_b128 v[20:23],v1 offset:2048\n"
AR "v70,a28\n"
MF "v[72:75],v[24:27],v[176:179],v[72:75]\n"
"ds_write_b128 v32,a[16:19] offset:40960\n"
MF "v[76:79],v[24:27],v[180:183],v[76:79]\n"
AR "v71,a29\n"
AR "v132,a30\n"
MF "v[80:83],v[24:27],v[184:187],v[80:83]\n"
AR "v133,a31\n"
AR "v134,a32\n"
MF "v[84:87],v[24:27],v[188:191],v[84:87]\n"
"ds_write_b128 v32,a[40:43] offset:45056\n"
MF "v[88:91],v[24:27],v[192:195],v[88:91]\n"
"s_waitcnt lgkmcnt(8)\n"
"ds_read_b128 v[24:27],v0 offset:6144\n"
AR "v135,a33\n"
MF "v[72:75],v[28:31],v[196:199],v[72:75]\n"
"ds_write_b128 v32,a[44:47] offset:49152\n"
MF "v[76:79],v[28:31],v[200:203],v[76:79]\n"
AR "v6,a34\n"
AR "v13,a35\n"
MF "v[80:83],v[28:31],v[204:207],v[80:83]\n"
AR "v14,a36\n"
AR "v15,a37\n"
MF "v[84:87],v[28:31],v[208:211],v[84:87]\n"
"ds_write_b128 v32,a[48:51] offset:53248\n"
MF "v[88:91],v[28:31],v[212:215],v[88:91]\n"
"s_waitcnt lgkmcnt(8)\n"
"ds_read_b128 v[28:31],v1 offset:6144\n"
"v_accvgpr_mov_b32 a254,a38\n"
MF "v[72:75],v[16:19],v[216:219],v[72:75]\n"
"v_accvgpr_mov_b32 a255,a39\n"
"v_accvgpr_mov_b32 a240,a168\n"
MF "v[76:79],v[16:19],v[220:223],v[76:79]\n"
"ds_write_b128 v32,a[52:55] offset:57344\n"
MF "v[80:83],v[16:19],v[224:227],v[80:83]\n"
"v_accvgpr_mov_b32 a241,a169\n"
"v_accvgpr_mov_b32 a242,a170\n"
MF "v[84:87],v[16:19],v[228:231],v[84:87]\n"
"ds_write_b128 v32,a[56:59] offset:61440\n"
MF "v[88:91],v[16:19],v[232:235],v[88:91]\n"
"s_waitcnt lgkmcnt(8)\n"
"ds_read_b128 v[16:19],v0 offset:7168\n"
"v_accvgpr_mov_b32 a243,a171\n"
MF "v[72:75],v[20:23],v[236:239],v[72:75]\n"
"v_accvgpr_mov_b32 a244,a172\n"
"v_accvgpr_mov_b32 a245,a173\n"
MF "v[76:79],v[20:23],v[240:243],v[76:79]\n"
"ds_write_b128 v33,a[80:83]\n"
MF "v[80:83],v[20:23],v[244:247],v[80:83]\n"
"v_accvgpr_mov_b32 a246,a174\n"
"v_accvgpr_mov_b32 a247,a175\n"
MF "v[84:87],v[20:23],v[248:251],v[84:87]\n"
"v_accvgpr_mov_b32 a248,a176\n"
"v_accvgpr_mov_b32 a249,a177\n"
MF "v[88:91],v[20:23],v[252:255],v[88:91]\n"
"s_waitcnt lgkmcnt(7)\n"
"ds_read_b128 v[20:23],v1 offset:7168\n"
"v_accvgpr_mov_b32 a250,a178\n"
MF "v[112:115],v[24:27],v[136:139],0\n"
"ds_write_b128 v33,a[84:87] offset:4096\n"
MF "v[116:119],v[24:27],v[140:143],0\n"
"ds_write_b128 v33,a[88:91] offset:8192\n"
MF "v[120:123],v[24:27],v[144:147],0\n"
"v_accvgpr_mov_b32 a251,a179\n"
MF "v[124:127],v[24:27],v[148:151],0\n"
MF "v[128:131],v[24:27],v[152:155],0\n"
"s_waitcnt lgkmcnt(7)\n"
"ds_read_b128 v[24:27],v0 offset:8192\n"
MF "v[112:115],v[28:31],v[156:159],v[112:115]\n"
"v_max3_f32 v52,v72,v73,v74\n"
"v_max_f32 v52,v52,v75\n"
MF "v[116:119],v[28:31],v[160:163],v[116:119]\n"
"v_max3_f32 v53,v76,v77,v78\n"
"v_max_f32 v53,v53,v79\n"
MF "v[120:123],v[28:31],v[164:167],v[120:123]\n"
"v_max3_f32 v54,v80,v81,v82\n"
"v_max_f32 v54,v54,v83\n"
MF "v[124:127],v[28:31],v[168:171],v[124:127]\n"
"v_max3_f32 v55,v84,v85,v86\n"
"v_max_f32 v55,v55,v87\n"
MF "v[128:131],v[28:31],v[172:175],v[128:131]\n"
"s_waitcnt lgkmcnt(5)\n"
"ds_read_b128 v[28:31],v1 offset:8192\n"
"v_max3_f32 v56,v88,v89,v90\n"
MF "v[112:115],v[16:19],v[176:179],v[112:115]\n"
"v_max_f32 v56,v56,v91\n"
MF "v[116:119],v[16:19],v[180:183],v[116:119]\n"
"ds_write_b128 v33,a[92:95] offset:12288\n"
MF "v[120:123],v[16:19],v[184:187],v[120:123]\n"
"ds_write_b128 v33,a[96:99] offset:16384\n"
MF "v[124:127],v[16:19],v[188:191],v[124:127]\n"
"ds_write_b128 v33,a[120:123] offset:20480\n"
MF "v[128:131],v[16:19],v[192:195],v[128:131]\n"
"s_waitcnt lgkmcnt(7)\n"
"ds_write_b128 v32,a[124:127]\n"
MF "v[112:115],v[20:23],v[196:199],v[112:115]\n"
"ds_write_b128 v32,a[128:131] offset:4096\n"
MF "v[116:119],v[20:23],v[200:203],v[116:119]\n"
"s_waitcnt lgkmcnt(0)\n"
"s_barrier\n"
MF "v[120:123],v[20:23],v[204:207],v[120:123]\n"
"s_add_u32 s72,s72,6144\n"
"ds_write_b128 v32,a[132:135] offset:8192\n"
MF "v[124:127],v[20:23],v[208:211],v[124:127]\n"
"s_cmp_eq_u32 s72,s17\n"
"s_cselect_b32 s72,s16,s72\n"
"s_add_u32 m0,s64,0\n"
MF "v[128:131],v[20:23],v[212:215],v[128:131]\n"
LD "v7,s[32:35],s72 offen nt lds\n"
MF "v[112:115],v[24:27],v[216:219],v[112:115]\n"
"s_add_u32 m0,s64,896\n"
"ds_write_b128 v32,a[136:139] offset:12288\n"
MF "v[116:119],v[24:27],v[220:223],v[116:119]\n"
LD "v7,s[32:35],s72 offen offset:128 nt lds\n"
MF "v[120:123],v[24:27],v[224:227],v[120:123]\n"
"s_add_u32 m0,s64,1792\n"
"ds_write_b128 v32,a[160:163] offset:16384\n"
MF "v[124:127],v[24:27],v[228:231],v[124:127]\n"
LD "v7,s[32:35],s72 offen offset:256 nt lds\n"
MF "v[128:131],v[24:27],v[232:235],v[128:131]\n"
"s_add_u32 s72,s72,6144\n"
"s_add_u32 m0,s64,6144\n"
MF "v[112:115],v[28:31],v[236:239],v[112:115]\n"
LD "v7,s[32:35],s72 offen nt lds\n"
MF "v[116:119],v[28:31],v[240:243],v[116:119]\n"
"s_add_u32 m0,s64,7040\n"
"ds_write_b128 v32,a[164:167] offset:20480\n"
MF "v[120:123],v[28:31],v[244:247],v[120:123]\n"
LD "v7,s[32:35],s72 offen offset:128 nt lds\n"
MF "v[124:127],v[28:31],v[248:251],v[124:127]\n"
"s_add_u32 m0,s64,7936\n"
MF "v[128:131],v[28:31],v[252:255],v[128:131]\n"
LD "v7,s[32:35],s72 offen offset:256 nt lds\n"
".Lt0e:\n"
"s_nop 1\n"
"v_max3_f32 v52,v52,v112,v113\n"
"v_max3_f32 v53,v53,v116,v117\n"
"v_max3_f32 v54,v54,v120,v121\n"
"s_nop 0\n"
"v_max3_f32 v55,v55,v124,v125\n"
"s_nop 0\n"
"v_max3_f32 v56,v56,v128,v129\n"
"v_max3_f32 v52,v52,v114,v115\n"
"v_max3_f32 v53,v53,v118,v119\n"
"v_max3_f32 v54,v54,v122,v123\n"
"v_max3_f32 v55,v55,v126,v127\n"
"v_max3_f32 v56,v56,v130,v131\n"
"v_mov_b32 v57,v52\n"
"v_mov_b32 v58,v53\n"
"v_mov_b32 v59,v54\n"
"v_mov_b32 v60,v55\n"
"v_mov_b32 v61,v56\n"
"v_permlane16_swap_b32 v52,v57\n"
"v_permlane16_swap_b32 v53,v58\n"
"v_permlane16_swap_b32 v54,v59\n"
"v_permlane16_swap_b32 v55,v60\n"
"v_permlane16_swap_b32 v56,v61\n"
"v_max_f32 v52,v52,v57\n"
"v_max_f32 v53,v53,v58\n"
"v_max_f32 v54,v54,v59\n"
"v_max_f32 v55,v55,v60\n"
"v_max_f32 v56,v56,v61\n"
"v_mov_b32 v57,v52\n"
"v_mov_b32 v58,v53\n"
"v_mov_b32 v59,v54\n"
"v_mov_b32 v60,v55\n"
"v_mov_b32 v61,v56\n"
"v_permlane32_swap_b32 v52,v57\n"
"v_permlane32_swap_b32 v53,v58\n"
"v_permlane32_swap_b32 v54,v59\n"
"v_permlane32_swap_b32 v55,v60\n"
"v_permlane32_swap_b32 v56,v61\n"
"v_max_f32 v52,v52,v57\n"
"v_max_f32 v53,v53,v58\n"
"v_max_f32 v54,v54,v59\n"
"v_max_f32 v55,v55,v60\n"
"v_max_f32 v56,v56,v61\n"
"v_sub_f32 v32,0xc1300000,v52\n"
"v_sub_f32 v36,0xc1300000,v53\n"
"v_sub_f32 v40,0xc1300000,v54\n"
"v_sub_f32 v44,0xc1300000,v55\n"
"v_sub_f32 v48,0xc1300000,v56\n"
"v_mov_b32 v33,v32\n"
"v_mov_b32 v34,v32\n"
"v_mov_b32 v35,v32\n"
"v_mov_b32 v37,v36\n"
"v_mov_b32 v38,v36\n"
"v_mov_b32 v39,v36\n"
"v_mov_b32 v41,v40\n"
"v_mov_b32 v42,v40\n"
"v_mov_b32 v43,v40\n"
"v_mov_b32 v45,v44\n"
"v_mov_b32 v46,v44\n"
"v_mov_b32 v47,v44\n"
"v_mov_b32 v49,v48\n"
"v_mov_b32 v50,v48\n"
"v_mov_b32 v51,v48\n"
"v_add_f32 v72,v72,v32\n"
"v_add_f32 v73,v73,v32\n"
"v_add_f32 v74,v74,v32\n"
"v_add_f32 v75,v75,v32\n"
"v_add_f32 v112,v112,v32\n"
"v_add_f32 v113,v113,v32\n"
"v_add_f32 v114,v114,v32\n"
"v_add_f32 v115,v115,v32\n"
"v_exp_f32 v72,v72\n"
"v_exp_f32 v73,v73\n"
"v_exp_f32 v74,v74\n"
"v_exp_f32 v75,v75\n"
"v_exp_f32 v112,v112\n"
"v_exp_f32 v113,v113\n"
"v_exp_f32 v114,v114\n"
"v_exp_f32 v115,v115\n"
"v_add_f32 v52,v72,v73\n"
"v_add_f32 v52,v52,v74\n"
"v_add_f32 v52,v52,v75\n"
"v_add_f32 v52,v52,v112\n"
"v_add_f32 v52,v52,v113\n"
"v_add_f32 v52,v52,v114\n"
"v_add_f32 v52,v52,v115\n"
CV "v72,v72,v73\n"
CV "v73,v74,v75\n"
CV "v74,v112,v113\n"
CV "v75,v114,v115\n"
"v_add_f32 v76,v76,v36\n"
"v_add_f32 v77,v77,v36\n"
"v_add_f32 v78,v78,v36\n"
"v_add_f32 v79,v79,v36\n"
"v_add_f32 v116,v116,v36\n"
"v_add_f32 v117,v117,v36\n"
"v_add_f32 v118,v118,v36\n"
"v_add_f32 v119,v119,v36\n"
"v_exp_f32 v76,v76\n"
"v_exp_f32 v77,v77\n"
"v_exp_f32 v78,v78\n"
"v_exp_f32 v79,v79\n"
"v_exp_f32 v116,v116\n"
"v_exp_f32 v117,v117\n"
"v_exp_f32 v118,v118\n"
"v_exp_f32 v119,v119\n"
"v_add_f32 v53,v76,v77\n"
"v_add_f32 v53,v53,v78\n"
"v_add_f32 v53,v53,v79\n"
"v_add_f32 v53,v53,v116\n"
"v_add_f32 v53,v53,v117\n"
"v_add_f32 v53,v53,v118\n"
"v_add_f32_e64 v53,v53,v119\n"
CV "v76,v76,v77\n"
CV "v77,v78,v79\n"
CV "v78,v116,v117\n"
CV "v79,v118,v119\n"
"v_add_f32 v80,v80,v40\n"
"v_add_f32 v81,v81,v40\n"
"v_add_f32 v82,v82,v40\n"
"v_add_f32 v83,v83,v40\n"
"v_add_f32 v120,v120,v40\n"
"v_add_f32 v121,v121,v40\n"
"v_add_f32 v122,v122,v40\n"
"v_add_f32 v123,v123,v40\n"
"v_exp_f32 v80,v80\n"
"v_exp_f32 v81,v81\n"
"v_exp_f32 v82,v82\n"
"v_exp_f32 v83,v83\n"
"v_exp_f32 v120,v120\n"
"v_exp_f32 v121,v121\n"
"v_exp_f32 v122,v122\n"
"v_exp_f32 v123,v123\n"
"v_add_f32 v54,v80,v81\n"
"v_add_f32 v54,v54,v82\n"
"v_add_f32 v54,v54,v83\n"
"v_add_f32 v54,v54,v120\n"
"v_add_f32 v54,v54,v121\n"
"v_add_f32 v54,v54,v122\n"
"v_add_f32_e64 v54,v54,v123\n"
CV "v80,v80,v81\n"
CV "v81,v82,v83\n"
CV "v82,v120,v121\n"
CV "v83,v122,v123\n"
"v_add_f32 v84,v84,v44\n"
"v_add_f32 v85,v85,v44\n"
"v_add_f32 v86,v86,v44\n"
"v_add_f32 v87,v87,v44\n"
"v_add_f32 v124,v124,v44\n"
"v_add_f32 v125,v125,v44\n"
"v_add_f32 v126,v126,v44\n"
"v_add_f32 v127,v127,v44\n"
"v_mov_b32 v55,0\n"
"v_add_f32 v88,v88,v48\n"
"v_add_f32 v89,v89,v48\n"
"v_add_f32 v90,v90,v48\n"
"v_add_f32 v91,v91,v48\n"
"v_add_f32 v128,v128,v48\n"
"v_add_f32 v129,v129,v48\n"
"v_add_f32 v130,v130,v48\n"
"v_add_f32 v131,v131,v48\n"
"v_mov_b32 v56,0\n"
"s_waitcnt vmcnt(12) lgkmcnt(0)\n"
"s_barrier\n"
"ds_read_b128 v[16:19],v0 offset:24576\n"
"ds_read_b128 v[20:23],v1 offset:24576\n"
"ds_read_b128 v[24:27],v0 offset:25600\n"
AW "a220,s13\n"
AW "a221,s13\n"
AW "a222,s13\n"
AW "a223,s13\n"
"s_waitcnt lgkmcnt(2)\n"
MF "v[92:95],v[16:19],v[136:139],v[32:35]\n"
"ds_read_b128 v[28:31],v1 offset:25600\n"
"s_waitcnt lgkmcnt(2)\n"
MF "v[92:95],v[20:23],v[156:159],v[92:95]\n"
"v_exp_f32 v84,v84\n"
MF "v[96:99],v[20:23],v[160:163],v[36:39]\n"
"v_exp_f32 v85,v85\n"
MF "v[96:99],v[16:19],v[140:143],v[96:99]\n"
"v_exp_f32 v86,v86\n"
MF "v[100:103],v[16:19],v[144:147],v[40:43]\n"
"v_exp_f32 v87,v87\n"
MF "v[100:103],v[20:23],v[164:167],v[100:103]\n"
"v_add_f32 v55,v55,v84\n"
"v_add_f32 v55,v55,v85\n"
MF "v[104:107],v[20:23],v[168:171],v[44:47]\n"
CV "v84,v84,v85\n"
"v_add_f32 v55,v55,v86\n"
MF "v[104:107],v[16:19],v[148:151],v[104:107]\n"
"v_add_f32 v55,v55,v87\n"
CV "v85,v86,v87\n"
MF "v[108:111],v[16:19],v[152:155],v[48:51]\n"
"v_exp_f32 v124,v124\n"
MF "v[108:111],v[20:23],v[172:175],v[108:111]\n"
"s_waitcnt lgkmcnt(0)\n"
"ds_read_b128 v[16:19],v0 offset:26624\n"
"ds_read_b128 v[20:23],v1 offset:26624\n"
MF "v[108:111],v[24:27],v[192:195],v[108:111]\n"
"v_exp_f32 v125,v125\n"
MF "v[108:111],v[28:31],v[212:215],v[108:111]\n"
"v_exp_f32 v126,v126\n"
MF "v[104:107],v[28:31],v[208:211],v[104:107]\n"
"v_exp_f32 v127,v127\n"
MF "v[104:107],v[24:27],v[188:191],v[104:107]\n"
"v_add_f32 v55,v55,v124\n"
"v_add_f32 v55,v55,v125\n"
MF "v[100:103],v[24:27],v[184:187],v[100:103]\n"
CV "v86,v124,v125\n"
"v_add_f32 v55,v55,v126\n"
MF "v[100:103],v[28:31],v[204:207],v[100:103]\n"
"v_add_f32 v55,v55,v127\n"
CV "v87,v126,v127\n"
MF "v[96:99],v[28:31],v[200:203],v[96:99]\n"
"v_exp_f32 v88,v88\n"
MF "v[96:99],v[24:27],v[180:183],v[96:99]\n"
"v_exp_f32 v89,v89\n"
MF "v[92:95],v[24:27],v[176:179],v[92:95]\n"
"v_exp_f32 v90,v90\n"
MF "v[92:95],v[28:31],v[196:199],v[92:95]\n"
"s_waitcnt lgkmcnt(0)\n"
"ds_read_b128 v[24:27],v0 offset:30720\n"
"ds_read_b128 v[28:31],v1 offset:30720\n"
MF "v[92:95],v[16:19],v[216:219],v[92:95]\n"
"v_exp_f32 v91,v91\n"
MF "v[92:95],v[20:23],v[236:239],v[92:95]\n"
"v_add_f32 v56,v56,v88\n"
"v_add_f32 v56,v56,v89\n"
MF "v[96:99],v[20:23],v[240:243],v[96:99]\n"
CV "v88,v88,v89\n"
"v_add_f32 v56,v56,v90\n"
MF "v[96:99],v[16:19],v[220:223],v[96:99]\n"
"v_add_f32 v56,v56,v91\n"
CV "v89,v90,v91\n"
MF "v[100:103],v[16:19],v[224:227],v[100:103]\n"
"v_exp_f32 v128,v128\n"
MF "v[100:103],v[20:23],v[244:247],v[100:103]\n"
"v_exp_f32 v129,v129\n"
MF "v[104:107],v[20:23],v[248:251],v[104:107]\n"
"v_exp_f32 v130,v130\n"
MF "v[104:107],v[16:19],v[228:231],v[104:107]\n"
"v_exp_f32 v131,v131\n"
MF "v[108:111],v[16:19],v[232:235],v[108:111]\n"
"v_add_f32 v56,v56,v128\n"
"v_add_f32 v56,v56,v129\n"
MF "v[108:111],v[20:23],v[252:255],v[108:111]\n"
"s_waitcnt lgkmcnt(0)\n"
"ds_read_b128 v[16:19],v0 offset:31744\n"
"ds_read_b128 v[20:23],v1 offset:31744\n"
MF "v[112:115],v[24:27],v[136:139],v[32:35]\n"
CV "v90,v128,v129\n"
"v_add_f32 v56,v56,v130\n"
MF "v[112:115],v[28:31],v[156:159],v[112:115]\n"
"v_add_f32 v56,v56,v131\n"
CV "v91,v130,v131\n"
MF "v[116:119],v[28:31],v[160:163],v[36:39]\n"
"v_exp_f32 v92,v92\n"
MF "v[116:119],v[24:27],v[140:143],v[116:119]\n"
"v_exp_f32 v93,v93\n"
MF "v[120:123],v[24:27],v[144:147],v[40:43]\n"
"v_exp_f32 v94,v94\n"
MF "v[120:123],v[28:31],v[164:167],v[120:123]\n"
"v_exp_f32 v95,v95\n"
MF "v[124:127],v[28:31],v[168:171],v[44:47]\n"
"v_add_f32 v57,v52,v92\n"
"v_add_f32 v57,v57,v93\n"
MF "v[124:127],v[24:27],v[148:151],v[124:127]\n"
CV "v92,v92,v93\n"
"v_add_f32 v57,v57,v94\n"
MF "v[128:131],v[24:27],v[152:155],v[48:51]\n"
"v_add_f32 v57,v57,v95\n"
CV "v93,v94,v95\n"
MF "v[128:131],v[28:31],v[172:175],v[128:131]\n"
"s_waitcnt lgkmcnt(0)\n"
"ds_read_b128 v[24:27],v0 offset:32768\n"
"ds_read_b128 v[28:31],v1 offset:32768\n"
MF "v[128:131],v[16:19],v[192:195],v[128:131]\n"
"v_exp_f32 v96,v96\n"
MF "v[128:131],v[20:23],v[212:215],v[128:131]\n"
"v_exp_f32 v97,v97\n"
MF "v[124:127],v[20:23],v[208:211],v[124:127]\n"
"v_exp_f32 v98,v98\n"
MF "v[124:127],v[16:19],v[188:191],v[124:127]\n"
"v_exp_f32 v99,v99\n"
MF "v[120:123],v[16:19],v[184:187],v[120:123]\n"
"v_add_f32 v58,v53,v96\n"
"v_add_f32 v58,v58,v97\n"
MF "v[120:123],v[20:23],v[204:207],v[120:123]\n"
CV "v96,v96,v97\n"
"v_add_f32 v58,v58,v98\n"
MF "v[116:119],v[20:23],v[200:203],v[116:119]\n"
"v_add_f32 v58,v58,v99\n"
CV "v97,v98,v99\n"
MF "v[116:119],v[16:19],v[180:183],v[116:119]\n"
"s_waitcnt vmcnt(6) lgkmcnt(0)\n"
"s_barrier\n"
MF "v[112:115],v[16:19],v[176:179],v[112:115]\n"
"v_exp_f32 v100,v100\n"
MF "v[112:115],v[20:23],v[196:199],v[112:115]\n"
"s_nop 0\n"
TR "v[16:17],v2 offset:49152\n"
TR "v[18:19],v2 offset:55296\n"
MF "v[112:115],v[24:27],v[216:219],v[112:115]\n"
"s_add_u32 s73,s73,12288\n"
TR "v[20:21],v3 offset:49152\n"
TR "v[22:23],v3 offset:55296\n"
MF "v[112:115],v[28:31],v[236:239],v[112:115]\n"
"s_cmp_eq_u32 s73,s19\n"
"s_cselect_b32 s73,s18,s73\n"
"s_add_u32 m0,s65,24576\n"
MF "v[116:119],v[28:31],v[240:243],v[116:119]\n"
LD "v8,s[36:39],s73 offen nt lds\n"
MF "v[116:119],v[24:27],v[220:223],v[116:119]\n"
"s_add_u32 m0,s65,25472\n"
"v_exp_f32 v101,v101\n"
MF "v[120:123],v[24:27],v[224:227],v[120:123]\n"
LD "v8,s[36:39],s73 offen offset:128 nt lds\n"
MF "v[120:123],v[28:31],v[244:247],v[120:123]\n"
"v_exp_f32 v102,v102\n"
MF "v[124:127],v[28:31],v[248:251],v[124:127]\n"
"s_add_u32 m0,s65,26368\n"
"v_exp_f32 v103,v103\n"
MF "v[124:127],v[24:27],v[228:231],v[124:127]\n"
LD "v8,s[36:39],s73 offen offset:256 nt lds\n"
MF "v[128:131],v[24:27],v[232:235],v[128:131]\n"
"v_add_f32 v59,v54,v100\n"
"v_add_f32 v59,v59,v101\n"
MF "v[128:131],v[28:31],v[252:255],v[128:131]\n"
"s_waitcnt lgkmcnt(2)\n"
TR "v[24:25],v4 offset:49152\n"
TR "v[26:27],v4 offset:55296\n"
MF "a[0:3],v[16:19],v[72:75],a[220:223]\n"
"s_add_u32 m0,s65,24576\n"
TR "v[28:29],v5 offset:49152\n"
TR "v[30:31],v5 offset:55296\n"
MF "a[4:7],v[16:19],v[76:79],a[220:223]\n"
LD "v8,s[36:39],s73 offen offset:3072 nt lds\n"
MF "a[8:11],v[16:19],v[80:83],a[220:223]\n"
"s_add_u32 m0,s65,25472\n"
CV "v100,v100,v101\n"
"v_add_f32 v59,v59,v102\n"
MF "a[12:15],v[16:19],v[84:87],a[220:223]\n"
LD "v8,s[36:39],s73 offen offset:3200 nt lds\n"
MF "a[16:19],v[16:19],v[88:91],a[220:223]\n"
"s_waitcnt lgkmcnt(4)\n"
TR "v[16:17],v2 offset:50176\n"
TR "v[18:19],v2 offset:56320\n"
MF "a[36:39],v[20:23],v[88:91],a[220:223]\n"
"v_add_f32 v59,v59,v103\n"
CV "v101,v102,v103\n"
MF "a[32:35],v[20:23],v[84:87],a[220:223]\n"
"s_add_u32 m0,s65,26368\n"
"v_exp_f32 v104,v104\n"
MF "a[28:31],v[20:23],v[80:83],a[220:223]\n"
LD "v8,s[36:39],s73 offen offset:3328 nt lds\n"
MF "a[24:27],v[20:23],v[76:79],a[220:223]\n"
"v_exp_f32 v105,v105\n"
MF "a[20:23],v[20:23],v[72:75],a[220:223]\n"
"s_waitcnt lgkmcnt(4)\n"
TR "v[20:21],v3 offset:50176\n"
TR "v[22:23],v3 offset:56320\n"
MF "a[40:43],v[24:27],v[72:75],a[220:223]\n"
"v_exp_f32 v106,v106\n"
MF "a[44:47],v[24:27],v[76:79],a[220:223]\n"
"v_exp_f32 v107,v107\n"
MF "a[48:51],v[24:27],v[80:83],a[220:223]\n"
"v_add_f32 v60,v55,v104\n"
"v_add_f32 v60,v60,v105\n"
MF "a[52:55],v[24:27],v[84:87],a[220:223]\n"
CV "v104,v104,v105\n"
"v_add_f32 v60,v60,v106\n"
MF "a[56:59],v[24:27],v[88:91],a[220:223]\n"
"s_waitcnt lgkmcnt(4)\n"
TR "v[24:25],v4 offset:50176\n"
TR "v[26:27],v4 offset:56320\n"
MF "a[76:79],v[28:31],v[88:91],a[220:223]\n"
"v_add_f32 v60,v60,v107\n"
CV "v105,v106,v107\n"
MF "a[72:75],v[28:31],v[84:87],a[220:223]\n"
"v_exp_f32 v108,v108\n"
MF "a[68:71],v[28:31],v[80:83],a[220:223]\n"
"v_exp_f32 v109,v109\n"
MF "a[64:67],v[28:31],v[76:79],a[220:223]\n"
"v_exp_f32 v110,v110\n"
MF "a[60:63],v[28:31],v[72:75],a[220:223]\n"
"s_waitcnt lgkmcnt(4)\n"
TR "v[28:29],v5 offset:50176\n"
TR "v[30:31],v5 offset:56320\n"
MF "a[80:83],v[16:19],v[72:75],a[220:223]\n"
"v_exp_f32 v111,v111\n"
MF "a[84:87],v[16:19],v[76:79],a[220:223]\n"
"v_add_f32 v61,v56,v108\n"
"v_add_f32 v61,v61,v109\n"
MF "a[88:91],v[16:19],v[80:83],a[220:223]\n"
CV "v108,v108,v109\n"
"v_add_f32 v61,v61,v110\n"
MF "a[92:95],v[16:19],v[84:87],a[220:223]\n"
"v_add_f32 v61,v61,v111\n"
CV "v109,v110,v111\n"
MF "a[96:99],v[16:19],v[88:91],a[220:223]\n"
"s_waitcnt lgkmcnt(4)\n"
TR "v[16:17],v2 offset:51200\n"
TR "v[18:19],v2 offset:57344\n"
MF "a[116:119],v[20:23],v[88:91],a[220:223]\n"
"v_exp_f32 v112,v112\n"
MF "a[112:115],v[20:23],v[84:87],a[220:223]\n"
"v_exp_f32 v113,v113\n"
MF "a[108:111],v[20:23],v[80:83],a[220:223]\n"
"v_exp_f32 v114,v114\n"
MF "a[104:107],v[20:23],v[76:79],a[220:223]\n"
"v_exp_f32 v115,v115\n"
MF "a[100:103],v[20:23],v[72:75],a[220:223]\n"
"s_waitcnt lgkmcnt(4)\n"
TR "v[20:21],v3 offset:51200\n"
TR "v[22:23],v3 offset:57344\n"
MF "a[120:123],v[24:27],v[72:75],a[220:223]\n"
"v_add_f32 v57,v57,v112\n"
"v_add_f32 v57,v57,v113\n"
MF "a[124:127],v[24:27],v[76:79],a[220:223]\n"
CV "v94,v112,v113\n"
"v_add_f32 v57,v57,v114\n"
MF "a[128:131],v[24:27],v[80:83],a[220:223]\n"
"v_add_f32 v57,v57,v115\n"
CV "v95,v114,v115\n"
MF "a[132:135],v[24:27],v[84:87],a[220:223]\n"
"v_exp_f32 v116,v116\n"
MF "a[136:139],v[24:27],v[88:91],a[220:223]\n"
"s_waitcnt lgkmcnt(4)\n"
TR "v[24:25],v4 offset:51200\n"
TR "v[26:27],v4 offset:57344\n"
MF "a[156:159],v[28:31],v[88:91],a[220:223]\n"
"v_exp_f32 v117,v117\n"
MF "a[152:155],v[28:31],v[84:87],a[220:223]\n"
"v_exp_f32 v118,v118\n"
MF "a[148:151],v[28:31],v[80:83],a[220:223]\n"
"v_exp_f32 v119,v119\n"
MF "a[144:147],v[28:31],v[76:79],a[220:223]\n"
"v_add_f32 v58,v58,v116\n"
"v_add_f32 v58,v58,v117\n"
MF "a[140:143],v[28:31],v[72:75],a[220:223]\n"
"s_waitcnt lgkmcnt(4)\n"
TR "v[28:29],v5 offset:51200\n"
TR "v[30:31],v5 offset:57344\n"
MF "a[160:163],v[16:19],v[72:75],a[220:223]\n"
CV "v98,v116,v117\n"
"v_add_f32 v58,v58,v118\n"
MF "a[164:167],v[16:19],v[76:79],a[220:223]\n"
"v_add_f32 v58,v58,v119\n"
CV "v99,v118,v119\n"
MF "a[168:171],v[16:19],v[80:83],a[220:223]\n"
"v_exp_f32 v120,v120\n"
MF "a[172:175],v[16:19],v[84:87],a[220:223]\n"
"v_exp_f32 v121,v121\n"
MF "a[176:179],v[16:19],v[88:91],a[220:223]\n"
"s_waitcnt lgkmcnt(4)\n"
"v_exp_f32 v122,v122\n"
MF "a[196:199],v[20:23],v[88:91],a[220:223]\n"
"v_exp_f32 v123,v123\n"
MF "a[192:195],v[20:23],v[84:87],a[220:223]\n"
"v_add_f32 v59,v59,v120\n"
"v_add_f32 v59,v59,v121\n"
MF "a[188:191],v[20:23],v[80:83],a[220:223]\n"
CV "v102,v120,v121\n"
"v_add_f32 v59,v59,v122\n"
MF "a[184:187],v[20:23],v[76:79],a[220:223]\n"
"s_waitcnt vmcnt(6) lgkmcnt(0)\n"
"s_barrier\n"
MF "a[180:183],v[20:23],v[72:75],a[220:223]\n"
"s_add_u32 s72,s72,6144\n"
"ds_read_b128 v[16:19],v0\n"
"ds_read_b128 v[20:23],v1\n"
MF "a[200:203],v[24:27],v[72:75],a[220:223]\n"
"s_cmp_eq_u32 s72,s17\n"
"s_cselect_b32 s72,s16,s72\n"
"s_add_u32 m0,s64,49152\n"
MF "a[204:207],v[24:27],v[76:79],a[220:223]\n"
LD "v7,s[32:35],s72 offen nt lds\n"
MF "a[208:211],v[24:27],v[80:83],a[220:223]\n"
"s_add_u32 m0,s64,50048\n"
"v_add_f32 v59,v59,v123\n"
CV "v103,v122,v123\n"
MF "a[212:215],v[24:27],v[84:87],a[220:223]\n"
LD "v7,s[32:35],s72 offen offset:128 nt lds\n"
MF "a[216:219],v[24:27],v[88:91],a[220:223]\n"
"s_add_u32 m0,s64,50944\n"
"ds_read_b128 v[24:27],v0 offset:1024\n"
MF "a[236:239],v[28:31],v[88:91],a[220:223]\n"
LD "v7,s[32:35],s72 offen offset:256 nt lds\n"
MF "a[232:235],v[28:31],v[84:87],a[220:223]\n"
MF "a[228:231],v[28:31],v[80:83],a[220:223]\n"
MF "a[224:227],v[28:31],v[76:79],a[220:223]\n"
MF "a[220:223],v[28:31],v[72:75],a[220:223]\n"
"s_waitcnt lgkmcnt(1)\n"
"s_branch .Lmid\n"
".Laloop:\n"
MF "v[72:75],v[16:19],v[136:139],v[32:35]\n"
"ds_read_b128 v[28:31],v1 offset:25600\n"
MF "v[72:75],v[20:23],v[156:159],v[72:75]\n"
MF "v[76:79],v[20:23],v[160:163],v[36:39]\n"
MF "v[76:79],v[16:19],v[140:143],v[76:79]\n"
MF "v[80:83],v[16:19],v[144:147],v[40:43]\n"
MF "v[80:83],v[20:23],v[164:167],v[80:83]\n"
MF "v[84:87],v[20:23],v[168:171],v[44:47]\n"
MF "v[84:87],v[16:19],v[148:151],v[84:87]\n"
"s_add_u32 s72,s72,6144\n"
"s_add_u32 m0,s64,6144\n"
MF "v[88:91],v[16:19],v[152:155],v[48:51]\n"
LD "v7,s[32:35],s72 offen nt lds\n"
MF "v[88:91],v[20:23],v[172:175],v[88:91]\n"
"s_waitcnt lgkmcnt(0)\n"
"ds_read_b128 v[16:19],v0 offset:26624\n"
"ds_read_b128 v[20:23],v1 offset:26624\n"
MF "v[88:91],v[24:27],v[192:195],v[88:91]\n"
"v_exp_f32 v124,v124\n"
MF "v[88:91],v[28:31],v[212:215],v[88:91]\n"
"s_add_u32 m0,s64,7040\n"
"v_exp_f32 v125,v125\n"
MF "v[84:87],v[28:31],v[208:211],v[84:87]\n"
LD "v7,s[32:35],s72 offen offset:128 nt lds\n"
MF "v[84:87],v[24:27],v[188:191],v[84:87]\n"
"v_exp_f32 v126,v126\n"
MF "v[80:83],v[24:27],v[184:187],v[80:83]\n"
"v_exp_f32 v127,v127\n"
MF "v[80:83],v[28:31],v[204:207],v[80:83]\n"
"s_add_u32 m0,s64,7936\n"
"v_add_f32 v60,v60,v124\n"
"v_add_f32 v60,v60,v125\n"
MF "v[76:79],v[28:31],v[200:203],v[76:79]\n"
LD "v7,s[32:35],s72 offen offset:256 nt lds\n"
MF "v[76:79],v[24:27],v[180:183],v[76:79]\n"
CV "v106,v124,v125\n"
"v_add_f32 v60,v60,v126\n"
MF "v[72:75],v[24:27],v[176:179],v[72:75]\n"
"v_add_f32 v60,v60,v127\n"
CV "v107,v126,v127\n"
MF "v[72:75],v[28:31],v[196:199],v[72:75]\n"
"s_waitcnt lgkmcnt(0)\n"
"ds_read_b128 v[24:27],v0 offset:30720\n"
"ds_read_b128 v[28:31],v1 offset:30720\n"
MF "v[72:75],v[16:19],v[216:219],v[72:75]\n"
"v_exp_f32 v128,v128\n"
MF "v[72:75],v[20:23],v[236:239],v[72:75]\n"
"v_exp_f32 v129,v129\n"
MF "v[76:79],v[20:23],v[240:243],v[76:79]\n"
"v_exp_f32 v130,v130\n"
MF "v[76:79],v[16:19],v[220:223],v[76:79]\n"
"v_exp_f32 v131,v131\n"
MF "v[80:83],v[16:19],v[224:227],v[80:83]\n"
"v_add_f32 v61,v61,v128\n"
"v_add_f32 v61,v61,v129\n"
MF "v[80:83],v[20:23],v[244:247],v[80:83]\n"
CV "v110,v128,v129\n"
"v_add_f32 v61,v61,v130\n"
MF "v[84:87],v[20:23],v[248:251],v[84:87]\n"
"v_add_f32 v61,v61,v131\n"
CV "v111,v130,v131\n"
MF "v[84:87],v[16:19],v[228:231],v[84:87]\n"
"v_max3_f32 v10,v57,v58,v59\n"
"v_max3_f32 v10,v10,v60,v61\n"
MF "v[88:91],v[16:19],v[232:235],v[88:91]\n"
"v_cmp_ngt_f32 vcc,s77,v10\n"
MF "v[88:91],v[20:23],v[252:255],v[88:91]\n"
"s_waitcnt lgkmcnt(0)\n"
"s_cbranch_vccnz .Lstub_11\n"
".Lback_11:\n"
MF "v[112:115],v[24:27],v[136:139],v[32:35]\n"
"s_add_u32 s74,s74,12288\n"
"ds_read_b128 v[16:19],v0 offset:31744\n"
"ds_read_b128 v[20:23],v1 offset:31744\n"
MF "v[112:115],v[28:31],v[156:159],v[112:115]\n"
"s_cmp_eq_u32 s74,s20\n"
"s_cselect_b32 s74,s70,s74\n"
MF "v[116:119],v[28:31],v[160:163],v[36:39]\n"
"v_exp_f32 v72,v72\n"
MF "v[116:119],v[24:27],v[140:143],v[116:119]\n"
"v_exp_f32 v73,v73\n"
MF "v[120:123],v[24:27],v[144:147],v[40:43]\n"
"v_exp_f32 v74,v74\n"
MF "v[120:123],v[28:31],v[164:167],v[120:123]\n"
"v_exp_f32 v75,v75\n"
MF "v[124:127],v[28:31],v[168:171],v[44:47]\n"
"v_add_f32 v52,v57,v72\n"
"v_add_f32 v52,v52,v73\n"
MF "v[124:127],v[24:27],v[148:151],v[124:127]\n"
CV "v72,v72,v73\n"
"v_add_f32 v52,v52,v74\n"
MF "v[128:131],v[24:27],v[152:155],v[48:51]\n"
"v_add_f32 v52,v52,v75\n"
CV "v73,v74,v75\n"
MF "v[128:131],v[28:31],v[172:175],v[128:131]\n"
"s_waitcnt lgkmcnt(0)\n"
"ds_read_b128 v[24:27],v0 offset:32768\n"
"ds_read_b128 v[28:31],v1 offset:32768\n"
MF "v[128:131],v[16:19],v[192:195],v[128:131]\n"
"v_exp_f32 v76,v76\n"
MF "v[128:131],v[20:23],v[212:215],v[128:131]\n"
"v_exp_f32 v77,v77\n"
MF "v[124:127],v[20:23],v[208:211],v[124:127]\n"
"v_exp_f32 v78,v78\n"
MF "v[124:127],v[16:19],v[188:191],v[124:127]\n"
"v_exp_f32 v79,v79\n"
MF "v[120:123],v[16:19],v[184:187],v[120:123]\n"
"v_add_f32 v53,v58,v76\n"
"v_add_f32 v53,v53,v77\n"
MF "v[120:123],v[20:23],v[204:207],v[120:123]\n"
CV "v76,v76,v77\n"
"v_add_f32 v53,v53,v78\n"
MF "v[116:119],v[20:23],v[200:203],v[116:119]\n"
"v_add_f32 v53,v53,v79\n"
CV "v77,v78,v79\n"
MF "v[116:119],v[16:19],v[180:183],v[116:119]\n"
"s_waitcnt vmcnt(6) lgkmcnt(0)\n"
"s_barrier\n"
MF "v[112:115],v[16:19],v[176:179],v[112:115]\n"
"v_exp_f32 v80,v80\n"
MF "v[112:115],v[20:23],v[196:199],v[112:115]\n"
"s_nop 0\n"
TR "v[16:17],v2 offset:49152\n"
TR "v[18:19],v2 offset:55296\n"
MF "v[112:115],v[24:27],v[216:219],v[112:115]\n"
"s_add_u32 s73,s73,12288\n"
TR "v[20:21],v3 offset:49152\n"
TR "v[22:23],v3 offset:55296\n"
MF "v[112:115],v[28:31],v[236:239],v[112:115]\n"
"s_cmp_eq_u32 s73,s19\n"
"s_cselect_b32 s73,s18,s73\n"
"s_add_u32 m0,s65,24576\n"
MF "v[116:119],v[28:31],v[240:243],v[116:119]\n"
LD "v8,s[36:39],s73 offen nt lds\n"
MF "v[116:119],v[24:27],v[220:223],v[116:119]\n"
"s_add_u32 m0,s65,25472\n"
"v_exp_f32 v81,v81\n"
MF "v[120:123],v[24:27],v[224:227],v[120:123]\n"
LD "v8,s[36:39],s73 offen offset:128 nt lds\n"
MF "v[120:123],v[28:31],v[244:247],v[120:123]\n"
"v_exp_f32 v82,v82\n"
MF "v[124:127],v[28:31],v[248:251],v[124:127]\n"
"s_add_u32 m0,s65,26368\n"
"v_exp_f32 v83,v83\n"
MF "v[124:127],v[24:27],v[228:231],v[124:127]\n"
LD "v8,s[36:39],s73 offen offset:256 nt lds\n"
MF "v[128:131],v[24:27],v[232:235],v[128:131]\n"
"v_add_f32 v54,v59,v80\n"
"v_add_f32 v54,v54,v81\n"
MF "v[128:131],v[28:31],v[252:255],v[128:131]\n"
"s_waitcnt lgkmcnt(2)\n"
TR "v[24:25],v4 offset:49152\n"
TR "v[26:27],v4 offset:55296\n"
MF "a[0:3],v[16:19],v[92:95],a[0:3]\n"
"s_add_u32 m0,s65,24576\n"
TR "v[28:29],v5 offset:49152\n"
TR "v[30:31],v5 offset:55296\n"
MF "a[4:7],v[16:19],v[96:99],a[4:7]\n"
LD "v8,s[36:39],s73 offen offset:3072 nt lds\n"
MF "a[8:11],v[16:19],v[100:103],a[8:11]\n"
"s_add_u32 m0,s65,25472\n"
CV "v80,v80,v81\n"
"v_add_f32 v54,v54,v82\n"
MF "a[12:15],v[16:19],v[104:107],a[12:15]\n"
LD "v8,s[36:39],s73 offen offset:3200 nt lds\n"
MF "a[16:19],v[16:19],v[108:111],a[16:19]\n"
"s_waitcnt lgkmcnt(4)\n"
TR "v[16:17],v2 offset:50176\n"
TR "v[18:19],v2 offset:56320\n"
MF "a[36:39],v[20:23],v[108:111],a[36:39]\n"
"v_add_f32 v54,v54,v83\n"
CV "v81,v82,v83\n"
MF "a[32:35],v[20:23],v[104:107],a[32:35]\n"
"s_add_u32 m0,s65,26368\n"
"v_exp_f32 v84,v84\n"
MF "a[28:31],v[20:23],v[100:103],a[28:31]\n"
LD "v8,s[36:39],s73 offen offset:3328 nt lds\n"
MF "a[24:27],v[20:23],v[96:99],a[24:27]\n"
"v_exp_f32 v85,v85\n"
MF "a[20:23],v[20:23],v[92:95],a[20:23]\n"
"s_waitcnt lgkmcnt(4)\n"
TR "v[20:21],v3 offset:50176\n"
TR "v[22:23],v3 offset:56320\n"
MF "a[40:43],v[24:27],v[92:95],a[40:43]\n"
"v_exp_f32 v86,v86\n"
MF "a[44:47],v[24:27],v[96:99],a[44:47]\n"
"v_exp_f32 v87,v87\n"
MF "a[48:51],v[24:27],v[100:103],a[48:51]\n"
"v_add_f32 v55,v60,v84\n"
"v_add_f32 v55,v55,v85\n"
MF "a[52:55],v[24:27],v[104:107],a[52:55]\n"
CV "v84,v84,v85\n"
"v_add_f32 v55,v55,v86\n"
MF "a[56:59],v[24:27],v[108:111],a[56:59]\n"
"s_waitcnt lgkmcnt(4)\n"
TR "v[24:25],v4 offset:50176\n"
TR "v[26:27],v4 offset:56320\n"
MF "a[76:79],v[28:31],v[108:111],a[76:79]\n"
"v_add_f32 v55,v55,v87\n"
CV "v85,v86,v87\n"
MF "a[72:75],v[28:31],v[104:107],a[72:75]\n"
"v_exp_f32 v88,v88\n"
MF "a[68:71],v[28:31],v[100:103],a[68:71]\n"
"v_exp_f32 v89,v89\n"
MF "a[64:67],v[28:31],v[96:99],a[64:67]\n"
"v_exp_f32 v90,v90\n"
MF "a[60:63],v[28:31],v[92:95],a[60:63]\n"
"s_waitcnt lgkmcnt(4)\n"
TR "v[28:29],v5 offset:50176\n"
TR "v[30:31],v5 offset:56320\n"
MF "a[80:83],v[16:19],v[92:95],a[80:83]\n"
"v_exp_f32 v91,v91\n"
MF "a[84:87],v[16:19],v[96:99],a[84:87]\n"
"v_add_f32 v56,v61,v88\n"
"v_add_f32 v56,v56,v89\n"
MF "a[88:91],v[16:19],v[100:103],a[88:91]\n"
CV "v88,v88,v89\n"
"v_add_f32 v56,v56,v90\n"
MF "a[92:95],v[16:19],v[104:107],a[92:95]\n"
"v_add_f32 v56,v56,v91\n"
CV "v89,v90,v91\n"
MF "a[96:99],v[16:19],v[108:111],a[96:99]\n"
"s_waitcnt lgkmcnt(4)\n"
TR "v[16:17],v2 offset:51200\n"
TR "v[18:19],v2 offset:57344\n"
MF "a[116:119],v[20:23],v[108:111],a[116:119]\n"
"v_exp_f32 v112,v112\n"
MF "a[112:115],v[20:23],v[104:107],a[112:115]\n"
"v_exp_f32 v113,v113\n"
MF "a[108:111],v[20:23],v[100:103],a[108:111]\n"
"v_exp_f32 v114,v114\n"
MF "a[104:107],v[20:23],v[96:99],a[104:107]\n"
"v_exp_f32 v115,v115\n"
MF "a[100:103],v[20:23],v[92:95],a[100:103]\n"
"s_waitcnt lgkmcnt(4)\n"
TR "v[20:21],v3 offset:51200\n"
TR "v[22:23],v3 offset:57344\n"
MF "a[120:123],v[24:27],v[92:95],a[120:123]\n"
"v_add_f32 v52,v52,v112\n"
"v_add_f32 v52,v52,v113\n"
MF "a[124:127],v[24:27],v[96:99],a[124:127]\n"
CV "v74,v112,v113\n"
"v_add_f32 v52,v52,v114\n"
MF "a[128:131],v[24:27],v[100:103],a[128:131]\n"
"v_add_f32 v52,v52,v115\n"
CV "v75,v114,v115\n"
MF "a[132:135],v[24:27],v[104:107],a[132:135]\n"
"v_exp_f32 v116,v116\n"
MF "a[136:139],v[24:27],v[108:111],a[136:139]\n"
"s_waitcnt lgkmcnt(4)\n"
TR "v[24:25],v4 offset:51200\n"
TR "v[26:27],v4 offset:57344\n"
MF "a[156:159],v[28:31],v[108:111],a[156:159]\n"
"v_exp_f32 v117,v117\n"
MF "a[152:155],v[28:31],v[104:107],a[152:155]\n"
"v_exp_f32 v118,v118\n"
MF "a[148:151],v[28:31],v[100:103],a[148:151]\n"
"v_exp_f32 v119,v119\n"
MF "a[144:147],v[28:31],v[96:99],a[144:147]\n"
"v_add_f32 v53,v53,v116\n"
"v_add_f32 v53,v53,v117\n"
MF "a[140:143],v[28:31],v[92:95],a[140:143]\n"
"s_waitcnt lgkmcnt(4)\n"
TR "v[28:29],v5 offset:51200\n"
TR "v[30:31],v5 offset:57344\n"
MF "a[160:163],v[16:19],v[92:95],a[160:163]\n"
CV "v78,v116,v117\n"
"v_add_f32 v53,v53,v118\n"
MF "a[164:167],v[16:19],v[96:99],a[164:167]\n"
"v_add_f32 v53,v53,v119\n"
CV "v79,v118,v119\n"
MF "a[168:171],v[16:19],v[100:103],a[168:171]\n"
"v_exp_f32 v120,v120\n"
MF "a[172:175],v[16:19],v[104:107],a[172:175]\n"
"v_exp_f32 v121,v121\n"
MF "a[176:179],v[16:19],v[108:111],a[176:179]\n"
"s_waitcnt lgkmcnt(4)\n"
"v_exp_f32 v122,v122\n"
MF "a[196:199],v[20:23],v[108:111],a[196:199]\n"
"v_exp_f32 v123,v123\n"
MF "a[192:195],v[20:23],v[104:107],a[192:195]\n"
"v_add_f32 v54,v54,v120\n"
"v_add_f32 v54,v54,v121\n"
MF "a[188:191],v[20:23],v[100:103],a[188:191]\n"
CV "v82,v120,v121\n"
"v_add_f32 v54,v54,v122\n"
MF "a[184:187],v[20:23],v[96:99],a[184:187]\n"
"s_waitcnt vmcnt(6) lgkmcnt(0)\n"
"s_barrier\n"
MF "a[180:183],v[20:23],v[92:95],a[180:183]\n"
"s_add_u32 s72,s72,6144\n"
"ds_read_b128 v[16:19],v0\n"
"ds_read_b128 v[20:23],v1\n"
MF "a[200:203],v[24:27],v[92:95],a[200:203]\n"
"s_cmp_eq_u32 s72,s17\n"
"s_cselect_b32 s72,s16,s72\n"
"s_add_u32 m0,s64,49152\n"
MF "a[204:207],v[24:27],v[96:99],a[204:207]\n"
LD "v7,s[32:35],s72 offen nt lds\n"
MF "a[208:211],v[24:27],v[100:103],a[208:211]\n"
"s_add_u32 m0,s64,50048\n"
"v_add_f32 v54,v54,v123\n"
CV "v83,v122,v123\n"
MF "a[212:215],v[24:27],v[104:107],a[212:215]\n"
LD "v7,s[32:35],s72 offen offset:128 nt lds\n"
MF "a[216:219],v[24:27],v[108:111],a[216:219]\n"
"s_add_u32 m0,s64,50944\n"
"ds_read_b128 v[24:27],v0 offset:1024\n"
MF "a[236:239],v[28:31],v[108:111],a[236:239]\n"
LD "v7,s[32:35],s72 offen offset:256 nt lds\n"
MF "a[232:235],v[28:31],v[104:107],a[232:235]\n"
MF "a[228:231],v[28:31],v[100:103],a[228:231]\n"
MF "a[224:227],v[28:31],v[96:99],a[224:227]\n"
MF "a[220:223],v[28:31],v[92:95],a[220:223]\n"
"s_waitcnt lgkmcnt(1)\n"
MF "v[92:95],v[16:19],v[136:139],v[32:35]\n"
"s_add_u32 s72,s72,6144\n"
"s_add_u32 m0,s64,55296\n"
"ds_read_b128 v[28:31],v1 offset:1024\n"
MF "v[92:95],v[20:23],v[156:159],v[92:95]\n"
LD "v7,s[32:35],s72 offen nt lds\n"
MF "v[96:99],v[20:23],v[160:163],v[36:39]\n"
"v_exp_f32 v124,v124\n"
MF "v[96:99],v[16:19],v[140:143],v[96:99]\n"
"v_exp_f32 v125,v125\n"
MF "v[100:103],v[16:19],v[144:147],v[40:43]\n"
"s_add_u32 m0,s64,56192\n"
"v_exp_f32 v126,v126\n"
MF "v[100:103],v[20:23],v[164:167],v[100:103]\n"
LD "v7,s[32:35],s72 offen offset:128 nt lds\n"
MF "v[104:107],v[20:23],v[168:171],v[44:47]\n"
"v_exp_f32 v127,v127\n"
MF "v[104:107],v[16:19],v[148:151],v[104:107]\n"
"s_add_u32 m0,s64,57088\n"
"v_add_f32 v55,v55,v124\n"
"v_add_f32 v55,v55,v125\n"
MF "v[108:111],v[16:19],v[152:155],v[48:51]\n"
LD "v7,s[32:35],s72 offen offset:256 nt lds\n"
MF "v[108:111],v[20:23],v[172:175],v[108:111]\n"
"s_waitcnt lgkmcnt(0)\n"
"ds_read_b128 v[16:19],v0 offset:2048\n"
"ds_read_b128 v[20:23],v1 offset:2048\n"
MF "v[108:111],v[24:27],v[192:195],v[108:111]\n"
CV "v86,v124,v125\n"
"v_add_f32 v55,v55,v126\n"
MF "v[108:111],v[28:31],v[212:215],v[108:111]\n"
"v_add_f32 v55,v55,v127\n"
CV "v87,v126,v127\n"
MF "v[104:107],v[28:31],v[208:211],v[104:107]\n"
"v_exp_f32 v128,v128\n"
MF "v[104:107],v[24:27],v[188:191],v[104:107]\n"
"v_exp_f32 v129,v129\n"
MF "v[100:103],v[24:27],v[184:187],v[100:103]\n"
"v_exp_f32 v130,v130\n"
MF "v[100:103],v[28:31],v[204:207],v[100:103]\n"
"v_exp_f32 v131,v131\n"
MF "v[96:99],v[28:31],v[200:203],v[96:99]\n"
"v_add_f32 v56,v56,v128\n"
"v_add_f32 v56,v56,v129\n"
MF "v[96:99],v[24:27],v[180:183],v[96:99]\n"
CV "v90,v128,v129\n"
"v_add_f32 v56,v56,v130\n"
MF "v[92:95],v[24:27],v[176:179],v[92:95]\n"
"v_add_f32 v56,v56,v131\n"
CV "v91,v130,v131\n"
MF "v[92:95],v[28:31],v[196:199],v[92:95]\n"
"s_waitcnt lgkmcnt(0)\n"
"ds_read_b128 v[24:27],v0 offset:6144\n"
"ds_read_b128 v[28:31],v1 offset:6144\n"
MF "v[92:95],v[16:19],v[216:219],v[92:95]\n"
MF "v[92:95],v[20:23],v[236:239],v[92:95]\n"
MF "v[96:99],v[20:23],v[240:243],v[96:99]\n"
MF "v[96:99],v[16:19],v[220:223],v[96:99]\n"
MF "v[100:103],v[16:19],v[224:227],v[100:103]\n"
MF "v[100:103],v[20:23],v[244:247],v[100:103]\n"
MF "v[104:107],v[20:23],v[248:251],v[104:107]\n"
MF "v[104:107],v[16:19],v[228:231],v[104:107]\n"
"v_max3_f32 v10,v52,v53,v54\n"
"v_max3_f32 v10,v10,v55,v56\n"
MF "v[108:111],v[16:19],v[232:235],v[108:111]\n"
"v_cmp_ngt_f32 vcc,s77,v10\n"
MF "v[108:111],v[20:23],v[252:255],v[108:111]\n"
"s_waitcnt lgkmcnt(0)\n"
"s_cbranch_vccnz .Lstub_12\n"
".Lback_12:\n"
MF "v[112:115],v[24:27],v[136:139],v[32:35]\n"
"s_add_u32 s74,s74,12288\n"
"ds_read_b128 v[16:19],v0 offset:7168\n"
"ds_read_b128 v[20:23],v1 offset:7168\n"
MF "v[112:115],v[28:31],v[156:159],v[112:115]\n"
"s_cmp_eq_u32 s74,s20\n"
"s_cselect_b32 s74,s70,s74\n"
MF "v[116:119],v[28:31],v[160:163],v[36:39]\n"
"v_exp_f32 v92,v92\n"
MF "v[116:119],v[24:27],v[140:143],v[116:119]\n"
"v_exp_f32 v93,v93\n"
MF "v[120:123],v[24:27],v[144:147],v[40:43]\n"
"v_exp_f32 v94,v94\n"
MF "v[120:123],v[28:31],v[164:167],v[120:123]\n"
"v_exp_f32 v95,v95\n"
MF "v[124:127],v[28:31],v[168:171],v[44:47]\n"
"v_add_f32 v57,v52,v92\n"
"v_add_f32 v57,v57,v93\n"
MF "v[124:127],v[24:27],v[148:151],v[124:127]\n"
CV "v92,v92,v93\n"
"v_add_f32 v57,v57,v94\n"
MF "v[128:131],v[24:27],v[152:155],v[48:51]\n"
"v_add_f32 v57,v57,v95\n"
CV "v93,v94,v95\n"
MF "v[128:131],v[28:31],v[172:175],v[128:131]\n"
"s_waitcnt lgkmcnt(0)\n"
"ds_read_b128 v[24:27],v0 offset:8192\n"
"ds_read_b128 v[28:31],v1 offset:8192\n"
MF "v[128:131],v[16:19],v[192:195],v[128:131]\n"
"v_exp_f32 v96,v96\n"
MF "v[128:131],v[20:23],v[212:215],v[128:131]\n"
"v_exp_f32 v97,v97\n"
MF "v[124:127],v[20:23],v[208:211],v[124:127]\n"
"v_exp_f32 v98,v98\n"
MF "v[124:127],v[16:19],v[188:191],v[124:127]\n"
"v_exp_f32 v99,v99\n"
MF "v[120:123],v[16:19],v[184:187],v[120:123]\n"
"v_add_f32 v58,v53,v96\n"
"v_add_f32 v58,v58,v97\n"
MF "v[120:123],v[20:23],v[204:207],v[120:123]\n"
CV "v96,v96,v97\n"
"v_add_f32 v58,v58,v98\n"
MF "v[116:119],v[20:23],v[200:203],v[116:119]\n"
"v_add_f32 v58,v58,v99\n"
CV "v97,v98,v99\n"
MF "v[116:119],v[16:19],v[180:183],v[116:119]\n"
"s_waitcnt vmcnt(6) lgkmcnt(0)\n"
"s_barrier\n"
MF "v[112:115],v[16:19],v[176:179],v[112:115]\n"
"v_exp_f32 v100,v100\n"
MF "v[112:115],v[20:23],v[196:199],v[112:115]\n"
"s_nop 0\n"
TR "v[16:17],v2 offset:24576\n"
TR "v[18:19],v2 offset:30720\n"
MF "v[112:115],v[24:27],v[216:219],v[112:115]\n"
"s_add_u32 s73,s73,12288\n"
TR "v[20:21],v3 offset:24576\n"
TR "v[22:23],v3 offset:30720\n"
MF "v[112:115],v[28:31],v[236:239],v[112:115]\n"
"s_cmp_eq_u32 s73,s19\n"
"s_cselect_b32 s73,s18,s73\n"
"s_add_u32 m0,s65,0\n"
MF "v[116:119],v[28:31],v[240:243],v[116:119]\n"
LD "v8,s[36:39],s73 offen nt lds\n"
MF "v[116:119],v[24:27],v[220:223],v[116:119]\n"
"s_add_u32 m0,s65,896\n"
"v_exp_f32 v101,v101\n"
MF "v[120:123],v[24:27],v[224:227],v[120:123]\n"
LD "v8,s[36:39],s73 offen offset:128 nt lds\n"
MF "v[120:123],v[28:31],v[244:247],v[120:123]\n"
"v_exp_f32 v102,v102\n"
MF "v[124:127],v[28:31],v[248:251],v[124:127]\n"
"s_add_u32 m0,s65,1792\n"
"v_exp_f32 v103,v103\n"
MF "v[124:127],v[24:27],v[228:231],v[124:127]\n"
LD "v8,s[36:39],s73 offen offset:256 nt lds\n"
MF "v[128:131],v[24:27],v[232:235],v[128:131]\n"
"v_add_f32 v59,v54,v100\n"
"v_add_f32 v59,v59,v101\n"
MF "v[128:131],v[28:31],v[252:255],v[128:131]\n"
"s_waitcnt lgkmcnt(2)\n"
TR "v[24:25],v4 offset:24576\n"
TR "v[26:27],v4 offset:30720\n"
MF "a[0:3],v[16:19],v[72:75],a[0:3]\n"
"s_add_u32 m0,s65,0\n"
TR "v[28:29],v5 offset:24576\n"
TR "v[30:31],v5 offset:30720\n"
MF "a[4:7],v[16:19],v[76:79],a[4:7]\n"
LD "v8,s[36:39],s73 offen offset:3072 nt lds\n"
MF "a[8:11],v[16:19],v[80:83],a[8:11]\n"
"s_add_u32 m0,s65,896\n"
CV "v100,v100,v101\n"
"v_add_f32 v59,v59,v102\n"
MF "a[12:15],v[16:19],v[84:87],a[12:15]\n"
LD "v8,s[36:39],s73 offen offset:3200 nt lds\n"
MF "a[16:19],v[16:19],v[88:91],a[16:19]\n"
"s_waitcnt lgkmcnt(4)\n"
TR "v[16:17],v2 offset:25600\n"
TR "v[18:19],v2 offset:31744\n"
MF "a[36:39],v[20:23],v[88:91],a[36:39]\n"
"v_add_f32 v59,v59,v103\n"
CV "v101,v102,v103\n"
MF "a[32:35],v[20:23],v[84:87],a[32:35]\n"
"s_add_u32 m0,s65,1792\n"
"v_exp_f32 v104,v104\n"
MF "a[28:31],v[20:23],v[80:83],a[28:31]\n"
LD "v8,s[36:39],s73 offen offset:3328 nt lds\n"
MF "a[24:27],v[20:23],v[76:79],a[24:27]\n"
"v_exp_f32 v105,v105\n"
MF "a[20:23],v[20:23],v[72:75],a[20:23]\n"
"s_waitcnt lgkmcnt(4)\n"
TR "v[20:21],v3 offset:25600\n"
TR "v[22:23],v3 offset:31744\n"
MF "a[40:43],v[24:27],v[72:75],a[40:43]\n"
"v_exp_f32 v106,v106\n"
MF "a[44:47],v[24:27],v[76:79],a[44:47]\n"
"v_exp_f32 v107,v107\n"
MF "a[48:51],v[24:27],v[80:83],a[48:51]\n"
"v_add_f32 v60,v55,v104\n"
"v_add_f32 v60,v60,v105\n"
MF "a[52:55],v[24:27],v[84:87],a[52:55]\n"
CV "v104,v104,v105\n"
"v_add_f32 v60,v60,v106\n"
MF "a[56:59],v[24:27],v[88:91],a[56:59]\n"
"s_waitcnt lgkmcnt(4)\n"
TR "v[24:25],v4 offset:25600\n"
TR "v[26:27],v4 offset:31744\n"
MF "a[76:79],v[28:31],v[88:91],a[76:79]\n"
"v_add_f32 v60,v60,v107\n"
CV "v105,v106,v107\n"
MF "a[72:75],v[28:31],v[84:87],a[72:75]\n"
"v_exp_f32 v108,v108\n"
MF "a[68:71],v[28:31],v[80:83],a[68:71]\n"
"v_exp_f32 v109,v109\n"
MF "a[64:67],v[28:31],v[76:79],a[64:67]\n"
"v_exp_f32 v110,v110\n"
MF "a[60:63],v[28:31],v[72:75],a[60:63]\n"
"s_waitcnt lgkmcnt(4)\n"
TR "v[28:29],v5 offset:25600\n"
TR "v[30:31],v5 offset:31744\n"
MF "a[80:83],v[16:19],v[72:75],a[80:83]\n"
"v_exp_f32 v111,v111\n"
MF "a[84:87],v[16:19],v[76:79],a[84:87]\n"
"v_add_f32 v61,v56,v108\n"
"v_add_f32 v61,v61,v109\n"
MF "a[88:91],v[16:19],v[80:83],a[88:91]\n"
CV "v108,v108,v109\n"
"v_add_f32 v61,v61,v110\n"
MF "a[92:95],v[16:19],v[84:87],a[92:95]\n"
"v_add_f32 v61,v61,v111\n"
CV "v109,v110,v111\n"
MF "a[96:99],v[16:19],v[88:91],a[96:99]\n"
"s_waitcnt lgkmcnt(4)\n"
TR "v[16:17],v2 offset:26624\n"
TR "v[18:19],v2 offset:32768\n"
MF "a[116:119],v[20:23],v[88:91],a[116:119]\n"
"v_exp_f32 v112,v112\n"
MF "a[112:115],v[20:23],v[84:87],a[112:115]\n"
"v_exp_f32 v113,v113\n"
MF "a[108:111],v[20:23],v[80:83],a[108:111]\n"
"v_exp_f32 v114,v114\n"
MF "a[104:107],v[20:23],v[76:79],a[104:107]\n"
"v_exp_f32 v115,v115\n"
MF "a[100:103],v[20:23],v[72:75],a[100:103]\n"
"s_waitcnt lgkmcnt(4)\n"
TR "v[20:21],v3 offset:26624\n"
TR "v[22:23],v3 offset:32768\n"
MF "a[120:123],v[24:27],v[72:75],a[120:123]\n"
"v_add_f32 v57,v57,v112\n"
"v_add_f32 v57,v57,v113\n"
MF "a[124:127],v[24:27],v[76:79],a[124:127]\n"
CV "v94,v112,v113\n"
"v_add_f32 v57,v57,v114\n"
MF "a[128:131],v[24:27],v[80:83],a[128:131]\n"
"v_add_f32 v57,v57,v115\n"
CV "v95,v114,v115\n"
MF "a[132:135],v[24:27],v[84:87],a[132:135]\n"
"v_exp_f32 v116,v116\n"
MF "a[136:139],v[24:27],v[88:91],a[136:139]\n"
"s_waitcnt lgkmcnt(4)\n"
TR "v[24:25],v4 offset:26624\n"
TR "v[26:27],v4 offset:32768\n"
MF "a[156:159],v[28:31],v[88:91],a[156:159]\n"
"v_exp_f32 v117,v117\n"
MF "a[152:155],v[28:31],v[84:87],a[152:155]\n"
"v_exp_f32 v118,v118\n"
MF "a[148:151],v[28:31],v[80:83],a[148:151]\n"
"v_exp_f32 v119,v119\n"
MF "a[144:147],v[28:31],v[76:79],a[144:147]\n"
"v_add_f32 v58,v58,v116\n"
"v_add_f32 v58,v58,v117\n"
MF "a[140:143],v[28:31],v[72:75],a[140:143]\n"
"s_waitcnt lgkmcnt(4)\n"
TR "v[28:29],v5 offset:26624\n"
TR "v[30:31],v5 offset:32768\n"
MF "a[160:163],v[16:19],v[72:75],a[160:163]\n"
CV "v98,v116,v117\n"
"v_add_f32 v58,v58,v118\n"
MF "a[164:167],v[16:19],v[76:79],a[164:167]\n"
"v_add_f32 v58,v58,v119\n"
CV "v99,v118,v119\n"
MF "a[168:171],v[16:19],v[80:83],a[168:171]\n"
"v_exp_f32 v120,v120\n"
MF "a[172:175],v[16:19],v[84:87],a[172:175]\n"
"v_exp_f32 v121,v121\n"
MF "a[176:179],v[16:19],v[88:91],a[176:179]\n"
"s_waitcnt lgkmcnt(4)\n"
"v_exp_f32 v122,v122\n"
MF "a[196:199],v[20:23],v[88:91],a[196:199]\n"
"v_exp_f32 v123,v123\n"
MF "a[192:195],v[20:23],v[84:87],a[192:195]\n"
"v_add_f32 v59,v59,v120\n"
"v_add_f32 v59,v59,v121\n"
MF "a[188:191],v[20:23],v[80:83],a[188:191]\n"
CV "v102,v120,v121\n"
"v_add_f32 v59,v59,v122\n"
MF "a[184:187],v[20:23],v[76:79],a[184:187]\n"
"s_waitcnt vmcnt(6) lgkmcnt(0)\n"
"s_barrier\n"
MF "a[180:183],v[20:23],v[72:75],a[180:183]\n"
"s_add_u32 s72,s72,6144\n"
"ds_read_b128 v[16:19],v0 offset:49152\n"
"ds_read_b128 v[20:23],v1 offset:49152\n"
MF "a[200:203],v[24:27],v[72:75],a[200:203]\n"
"s_cmp_eq_u32 s72,s17\n"
"s_cselect_b32 s72,s16,s72\n"
"s_add_u32 m0,s64,24576\n"
MF "a[204:207],v[24:27],v[76:79],a[204:207]\n"
LD "v7,s[32:35],s72 offen nt lds\n"
MF "a[208:211],v[24:27],v[80:83],a[208:211]\n"
"s_add_u32 m0,s64,25472\n"
"v_add_f32 v59,v59,v123\n"
CV "v103,v122,v123\n"
MF "a[212:215],v[24:27],v[84:87],a[212:215]\n"
LD "v7,s[32:35],s72 offen offset:128 nt lds\n"
MF "a[216:219],v[24:27],v[88:91],a[216:219]\n"
"s_add_u32 m0,s64,26368\n"
"ds_read_b128 v[24:27],v0 offset:50176\n"
MF "a[236:239],v[28:31],v[88:91],a[236:239]\n"
LD "v7,s[32:35],s72 offen offset:256 nt lds\n"
MF "a[232:235],v[28:31],v[84:87],a[232:235]\n"
MF "a[228:231],v[28:31],v[80:83],a[228:231]\n"
MF "a[224:227],v[28:31],v[76:79],a[224:227]\n"
MF "a[220:223],v[28:31],v[72:75],a[220:223]\n"
"s_waitcnt lgkmcnt(1)\n"
MF "v[72:75],v[16:19],v[136:139],v[32:35]\n"
"s_add_u32 s72,s72,6144\n"
"s_add_u32 m0,s64,30720\n"
"ds_read_b128 v[28:31],v1 offset:50176\n"
MF "v[72:75],v[20:23],v[156:159],v[72:75]\n"
LD "v7,s[32:35],s72 offen nt lds\n"
MF "v[76:79],v[20:23],v[160:163],v[36:39]\n"
"v_exp_f32 v124,v124\n"
MF "v[76:79],v[16:19],v[140:143],v[76:79]\n"
"v_exp_f32 v125,v125\n"
MF "v[80:83],v[16:19],v[144:147],v[40:43]\n"
"s_add_u32 m0,s64,31616\n"
"v_exp_f32 v126,v126\n"
MF "v[80:83],v[20:23],v[164:167],v[80:83]\n"
LD "v7,s[32:35],s72 offen offset:128 nt lds\n"
MF "v[84:87],v[20:23],v[168:171],v[44:47]\n"
"v_exp_f32 v127,v127\n"
MF "v[84:87],v[16:19],v[148:151],v[84:87]\n"
"s_add_u32 m0,s64,32512\n"
"v_add_f32 v60,v60,v124\n"
"v_add_f32 v60,v60,v125\n"
MF "v[88:91],v[16:19],v[152:155],v[48:51]\n"
LD "v7,s[32:35],s72 offen offset:256 nt lds\n"
MF "v[88:91],v[20:23],v[172:175],v[88:91]\n"
"s_waitcnt lgkmcnt(0)\n"
"ds_read_b128 v[16:19],v0 offset:51200\n"
"ds_read_b128 v[20:23],v1 offset:51200\n"
MF "v[88:91],v[24:27],v[192:195],v[88:91]\n"
CV "v106,v124,v125\n"
"v_add_f32 v60,v60,v126\n"
MF "v[88:91],v[28:31],v[212:215],v[88:91]\n"
"v_add_f32 v60,v60,v127\n"
CV "v107,v126,v127\n"
MF "v[84:87],v[28:31],v[208:211],v[84:87]\n"
"v_exp_f32 v128,v128\n"
MF "v[84:87],v[24:27],v[188:191],v[84:87]\n"
"v_exp_f32 v129,v129\n"
MF "v[80:83],v[24:27],v[184:187],v[80:83]\n"
"v_exp_f32 v130,v130\n"
MF "v[80:83],v[28:31],v[204:207],v[80:83]\n"
"v_exp_f32 v131,v131\n"
MF "v[76:79],v[28:31],v[200:203],v[76:79]\n"
"v_add_f32 v61,v61,v128\n"
"v_add_f32 v61,v61,v129\n"
MF "v[76:79],v[24:27],v[180:183],v[76:79]\n"
CV "v110,v128,v129\n"
"v_add_f32 v61,v61,v130\n"
MF "v[72:75],v[24:27],v[176:179],v[72:75]\n"
"v_add_f32 v61,v61,v131\n"
CV "v111,v130,v131\n"
MF "v[72:75],v[28:31],v[196:199],v[72:75]\n"
"s_waitcnt lgkmcnt(0)\n"
"ds_read_b128 v[24:27],v0 offset:55296\n"
"ds_read_b128 v[28:31],v1 offset:55296\n"
MF "v[72:75],v[16:19],v[216:219],v[72:75]\n"
MF "v[72:75],v[20:23],v[236:239],v[72:75]\n"
MF "v[76:79],v[20:23],v[240:243],v[76:79]\n"
MF "v[76:79],v[16:19],v[220:223],v[76:79]\n"
MF "v[80:83],v[16:19],v[224:227],v[80:83]\n"
MF "v[80:83],v[20:23],v[244:247],v[80:83]\n"
MF "v[84:87],v[20:23],v[248:251],v[84:87]\n"
MF "v[84:87],v[16:19],v[228:231],v[84:87]\n"
"v_max3_f32 v10,v57,v58,v59\n"
"v_max3_f32 v10,v10,v60,v61\n"
MF "v[88:91],v[16:19],v[232:235],v[88:91]\n"
"v_cmp_ngt_f32 vcc,s95,v10\n"
MF "v[88:91],v[20:23],v[252:255],v[88:91]\n"
"s_waitcnt lgkmcnt(0)\n"
"s_cbranch_vccnz .Lstub_13\n"
".Lback_13:\n"
MF "v[112:115],v[24:27],v[136:139],v[32:35]\n"
"s_add_u32 s74,s74,12288\n"
"ds_read_b128 v[16:19],v0 offset:56320\n"
"ds_read_b128 v[20:23],v1 offset:56320\n"
MF "v[112:115],v[28:31],v[156:159],v[112:115]\n"
"s_cmp_eq_u32 s74,s20\n"
"s_cselect_b32 s74,s70,s74\n"
MF "v[116:119],v[28:31],v[160:163],v[36:39]\n"
"v_exp_f32 v72,v72\n"
MF "v[116:119],v[24:27],v[140:143],v[116:119]\n"
"v_exp_f32 v73,v73\n"
MF "v[120:123],v[24:27],v[144:147],v[40:43]\n"
"v_exp_f32 v74,v74\n"
MF "v[120:123],v[28:31],v[164:167],v[120:123]\n"
"v_exp_f32 v75,v75\n"
MF "v[124:127],v[28:31],v[168:171],v[44:47]\n"
"v_add_f32 v52,v57,v72\n"
"v_add_f32 v52,v52,v73\n"
MF "v[124:127],v[24:27],v[148:151],v[124:127]\n"
CV "v72,v72,v73\n"
"v_add_f32 v52,v52,v74\n"
MF "v[128:131],v[24:27],v[152:155],v[48:51]\n"
"v_add_f32 v52,v52,v75\n"
CV "v73,v74,v75\n"
MF "v[128:131],v[28:31],v[172:175],v[128:131]\n"
"s_waitcnt lgkmcnt(0)\n"
"ds_read_b128 v[24:27],v0 offset:57344\n"
"ds_read_b128 v[28:31],v1 offset:57344\n"
MF "v[128:131],v[16:19],v[192:195],v[128:131]\n"
"v_exp_f32 v76,v76\n"
MF "v[128:131],v[20:23],v[212:215],v[128:131]\n"
"v_exp_f32 v77,v77\n"
MF "v[124:127],v[20:23],v[208:211],v[124:127]\n"
"v_exp_f32 v78,v78\n"
MF "v[124:127],v[16:19],v[188:191],v[124:127]\n"
"v_exp_f32 v79,v79\n"
MF "v[120:123],v[16:19],v[184:187],v[120:123]\n"
"v_add_f32 v53,v58,v76\n"
"v_add_f32 v53,v53,v77\n"
MF "v[120:123],v[20:23],v[204:207],v[120:123]\n"
CV "v76,v76,v77\n"
"v_add_f32 v53,v53,v78\n"
MF "v[116:119],v[20:23],v[200:203],v[116:119]\n"
"v_add_f32 v53,v53,v79\n"
CV "v77,v78,v79\n"
MF "v[116:119],v[16:19],v[180:183],v[116:119]\n"
"s_waitcnt vmcnt(6) lgkmcnt(0)\n"
"s_barrier\n"
MF "v[112:115],v[16:19],v[176:179],v[112:115]\n"
"v_exp_f32 v80,v80\n"
MF "v[112:115],v[20:23],v[196:199],v[112:115]\n"
"s_nop 0\n"
TR "v[16:17],v2\n"
TR "v[18:19],v2 offset:6144\n"
MF "v[112:115],v[24:27],v[216:219],v[112:115]\n"
"s_add_u32 s73,s73,12288\n"
TR "v[20:21],v3\n"
TR "v[22:23],v3 offset:6144\n"
MF "v[112:115],v[28:31],v[236:239],v[112:115]\n"
"s_cmp_eq_u32 s73,s19\n"
"s_cselect_b32 s73,s18,s73\n"
"s_add_u32 m0,s65,49152\n"
MF "v[116:119],v[28:31],v[240:243],v[116:119]\n"
LD "v8,s[36:39],s73 offen nt lds\n"
MF "v[116:119],v[24:27],v[220:223],v[116:119]\n"
"s_add_u32 m0,s65,50048\n"
"v_exp_f32 v81,v81\n"
MF "v[120:123],v[24:27],v[224:227],v[120:123]\n"
LD "v8,s[36:39],s73 offen offset:128 nt lds\n"
MF "v[120:123],v[28:31],v[244:247],v[120:123]\n"
"v_exp_f32 v82,v82\n"
MF "v[124:127],v[28:31],v[248:251],v[124:127]\n"
"s_add_u32 m0,s65,50944\n"
"v_exp_f32 v83,v83\n"
MF "v[124:127],v[24:27],v[228:231],v[124:127]\n"
LD "v8,s[36:39],s73 offen offset:256 nt lds\n"
MF "v[128:131],v[24:27],v[232:235],v[128:131]\n"
"v_add_f32 v54,v59,v80\n"
"v_add_f32 v54,v54,v81\n"
MF "v[128:131],v[28:31],v[252:255],v[128:131]\n"
"s_waitcnt lgkmcnt(2)\n"
TR "v[24:25],v4\n"
TR "v[26:27],v4 offset:6144\n"
MF "a[0:3],v[16:19],v[92:95],a[0:3]\n"
"s_add_u32 m0,s65,49152\n"
TR "v[28:29],v5\n"
TR "v[30:31],v5 offset:6144\n"
MF "a[4:7],v[16:19],v[96:99],a[4:7]\n"
LD "v8,s[36:39],s73 offen offset:3072 nt lds\n"
MF "a[8:11],v[16:19],v[100:103],a[8:11]\n"
"s_add_u32 m0,s65,50048\n"
CV "v80,v80,v81\n"
"v_add_f32 v54,v54,v82\n"
MF "a[12:15],v[16:19],v[104:107],a[12:15]\n"
LD "v8,s[36:39],s73 offen offset:3200 nt lds\n"
MF "a[16:19],v[16:19],v[108:111],a[16:19]\n"
"s_waitcnt lgkmcnt(4)\n"
TR "v[16:17],v2 offset:1024\n"
TR "v[18:19],v2 offset:7168\n"
MF "a[36:39],v[20:23],v[108:111],a[36:39]\n"
"v_add_f32 v54,v54,v83\n"
CV "v81,v82,v83\n"
MF "a[32:35],v[20:23],v[104:107],a[32:35]\n"
"s_add_u32 m0,s65,50944\n"
"v_exp_f32 v84,v84\n"
MF "a[28:31],v[20:23],v[100:103],a[28:31]\n"
LD "v8,s[36:39],s73 offen offset:3328 nt lds\n"
MF "a[24:27],v[20:23],v[96:99],a[24:27]\n"
"v_exp_f32 v85,v85\n"
MF "a[20:23],v[20:23],v[92:95],a[20:23]\n"
"s_waitcnt lgkmcnt(4)\n"
TR "v[20:21],v3 offset:1024\n"
TR "v[22:23],v3 offset:7168\n"
MF "a[40:43],v[24:27],v[92:95],a[40:43]\n"
"v_exp_f32 v86,v86\n"
MF "a[44:47],v[24:27],v[96:99],a[44:47]\n"
"v_exp_f32 v87,v87\n"
MF "a[48:51],v[24:27],v[100:103],a[48:51]\n"
"v_add_f32 v55,v60,v84\n"
"v_add_f32 v55,v55,v85\n"
MF "a[52:55],v[24:27],v[104:107],a[52:55]\n"
CV "v84,v84,v85\n"
"v_add_f32 v55,v55,v86\n"
MF "a[56:59],v[24:27],v[108:111],a[56:59]\n"
"s_waitcnt lgkmcnt(4)\n"
TR "v[24:25],v4 offset:1024\n"
TR "v[26:27],v4 offset:7168\n"
MF "a[76:79],v[28:31],v[108:111],a[76:79]\n"
"v_add_f32 v55,v55,v87\n"
CV "v85,v86,v87\n"
MF "a[72:75],v[28:31],v[104:107],a[72:75]\n"
"v_exp_f32 v88,v88\n"
MF "a[68:71],v[28:31],v[100:103],a[68:71]\n"
"v_exp_f32 v89,v89\n"
MF "a[64:67],v[28:31],v[96:99],a[64:67]\n"
"v_exp_f32 v90,v90\n"
MF "a[60:63],v[28:31],v[92:95],a[60:63]\n"
"s_waitcnt lgkmcnt(4)\n"
TR "v[28:29],v5 offset:1024\n"
TR "v[30:31],v5 offset:7168\n"
MF "a[80:83],v[16:19],v[92:95],a[80:83]\n"
"v_exp_f32 v91,v91\n"
MF "a[84:87],v[16:19],v[96:99],a[84:87]\n"
"v_add_f32 v56,v61,v88\n"
"v_add_f32 v56,v56,v89\n"
MF "a[88:91],v[16:19],v[100:103],a[88:91]\n"
CV "v88,v88,v89\n"
"v_add_f32 v56,v56,v90\n"
MF "a[92:95],v[16:19],v[104:107],a[92:95]\n"
"v_add_f32 v56,v56,v91\n"
CV "v89,v90,v91\n"
MF "a[96:99],v[16:19],v[108:111],a[96:99]\n"
"s_waitcnt lgkmcnt(4)\n"
TR "v[16:17],v2 offset:2048\n"
TR "v[18:19],v2 offset:8192\n"
MF "a[116:119],v[20:23],v[108:111],a[116:119]\n"
"v_exp_f32 v112,v112\n"
MF "a[112:115],v[20:23],v[104:107],a[112:115]\n"
"v_exp_f32 v113,v113\n"
MF "a[108:111],v[20:23],v[100:103],a[108:111]\n"
"v_exp_f32 v114,v114\n"
MF "a[104:107],v[20:23],v[96:99],a[104:107]\n"
"v_exp_f32 v115,v115\n"
MF "a[100:103],v[20:23],v[92:95],a[100:103]\n"
"s_waitcnt lgkmcnt(4)\n"
TR "v[20:21],v3 offset:2048\n"
TR "v[22:23],v3 offset:8192\n"
MF "a[120:123],v[24:27],v[92:95],a[120:123]\n"
"v_add_f32 v52,v52,v112\n"
"v_add_f32 v52,v52,v113\n"
MF "a[124:127],v[24:27],v[96:99],a[124:127]\n"
CV "v74,v112,v113\n"
"v_add_f32 v52,v52,v114\n"
MF "a[128:131],v[24:27],v[100:103],a[128:131]\n"
"v_add_f32 v52,v52,v115\n"
CV "v75,v114,v115\n"
MF "a[132:135],v[24:27],v[104:107],a[132:135]\n"
"v_exp_f32 v116,v116\n"
MF "a[136:139],v[24:27],v[108:111],a[136:139]\n"
"s_waitcnt lgkmcnt(4)\n"
TR "v[24:25],v4 offset:2048\n"
TR "v[26:27],v4 offset:8192\n"
MF "a[156:159],v[28:31],v[108:111],a[156:159]\n"
"v_exp_f32 v117,v117\n"
MF "a[152:155],v[28:31],v[104:107],a[152:155]\n"
"v_exp_f32 v118,v118\n"
MF "a[148:151],v[28:31],v[100:103],a[148:151]\n"
"v_exp_f32 v119,v119\n"
MF "a[144:147],v[28:31],v[96:99],a[144:147]\n"
"v_add_f32 v53,v53,v116\n"
"v_add_f32 v53,v53,v117\n"
MF "a[140:143],v[28:31],v[92:95],a[140:143]\n"
"s_waitcnt lgkmcnt(4)\n"
TR "v[28:29],v5 offset:2048\n"
TR "v[30:31],v5 offset:8192\n"
MF "a[160:163],v[16:19],v[92:95],a[160:163]\n"
CV "v78,v116,v117\n"
"v_add_f32 v53,v53,v118\n"
MF "a[164:167],v[16:19],v[96:99],a[164:167]\n"
"v_add_f32 v53,v53,v119\n"
CV "v79,v118,v119\n"
MF "a[168:171],v[16:19],v[100:103],a[168:171]\n"
"v_exp_f32 v120,v120\n"
MF "a[172:175],v[16:19],v[104:107],a[172:175]\n"
"v_exp_f32 v121,v121\n"
MF "a[176:179],v[16:19],v[108:111],a[176:179]\n"
"s_waitcnt lgkmcnt(4)\n"
"v_exp_f32 v122,v122\n"
MF "a[196:199],v[20:23],v[108:111],a[196:199]\n"
"v_exp_f32 v123,v123\n"
MF "a[192:195],v[20:23],v[104:107],a[192:195]\n"
"v_add_f32 v54,v54,v120\n"
"v_add_f32 v54,v54,v121\n"
MF "a[188:191],v[20:23],v[100:103],a[188:191]\n"
CV "v82,v120,v121\n"
"v_add_f32 v54,v54,v122\n"
MF "a[184:187],v[20:23],v[96:99],a[184:187]\n"
"s_waitcnt vmcnt(6) lgkmcnt(0)\n"
"s_barrier\n"
MF "a[180:183],v[20:23],v[92:95],a[180:183]\n"
"s_add_u32 s72,s72,6144\n"
"ds_read_b128 v[16:19],v0 offset:24576\n"
"ds_read_b128 v[20:23],v1 offset:24576\n"
MF "a[200:203],v[24:27],v[92:95],a[200:203]\n"
"s_cmp_eq_u32 s72,s17\n"
"s_cselect_b32 s72,s16,s72\n"
"s_add_u32 m0,s64,0\n"
MF "a[204:207],v[24:27],v[96:99],a[204:207]\n"
LD "v7,s[32:35],s72 offen nt lds\n"
MF "a[208:211],v[24:27],v[100:103],a[208:211]\n"
"s_add_u32 m0,s64,896\n"
"v_add_f32 v54,v54,v123\n"
CV "v83,v122,v123\n"
MF "a[212:215],v[24:27],v[104:107],a[212:215]\n"
LD "v7,s[32:35],s72 offen offset:128 nt lds\n"
MF "a[216:219],v[24:27],v[108:111],a[216:219]\n"
"s_add_u32 m0,s64,1792\n"
"ds_read_b128 v[24:27],v0 offset:25600\n"
MF "a[236:239],v[28:31],v[108:111],a[236:239]\n"
LD "v7,s[32:35],s72 offen offset:256 nt lds\n"
MF "a[232:235],v[28:31],v[104:107],a[232:235]\n"
MF "a[228:231],v[28:31],v[100:103],a[228:231]\n"
MF "a[224:227],v[28:31],v[96:99],a[224:227]\n"
MF "a[220:223],v[28:31],v[92:95],a[220:223]\n"
"s_waitcnt lgkmcnt(1)\n"
MF "v[92:95],v[16:19],v[136:139],v[32:35]\n"
"s_add_u32 s72,s72,6144\n"
"s_add_u32 m0,s64,6144\n"
"ds_read_b128 v[28:31],v1 offset:25600\n"
MF "v[92:95],v[20:23],v[156:159],v[92:95]\n"
LD "v7,s[32:35],s72 offen nt lds\n"
MF "v[96:99],v[20:23],v[160:163],v[36:39]\n"
"v_exp_f32 v124,v124\n"
MF "v[96:99],v[16:19],v[140:143],v[96:99]\n"
"v_exp_f32 v125,v125\n"
MF "v[100:103],v[16:19],v[144:147],v[40:43]\n"
"s_add_u32 m0,s64,7040\n"
"v_exp_f32 v126,v126\n"
MF "v[100:103],v[20:23],v[164:167],v[100:103]\n"
LD "v7,s[32:35],s72 offen offset:128 nt lds\n"
MF "v[104:107],v[20:23],v[168:171],v[44:47]\n"
"v_exp_f32 v127,v127\n"
MF "v[104:107],v[16:19],v[148:151],v[104:107]\n"
"s_add_u32 m0,s64,7936\n"
"v_add_f32 v55,v55,v124\n"
"v_add_f32 v55,v55,v125\n"
MF "v[108:111],v[16:19],v[152:155],v[48:51]\n"
LD "v7,s[32:35],s72 offen offset:256 nt lds\n"
MF "v[108:111],v[20:23],v[172:175],v[108:111]\n"
"s_waitcnt lgkmcnt(0)\n"
"ds_read_b128 v[16:19],v0 offset:26624\n"
"ds_read_b128 v[20:23],v1 offset:26624\n"
MF "v[108:111],v[24:27],v[192:195],v[108:111]\n"
CV "v86,v124,v125\n"
"v_add_f32 v55,v55,v126\n"
MF "v[108:111],v[28:31],v[212:215],v[108:111]\n"
"v_add_f32 v55,v55,v127\n"
CV "v87,v126,v127\n"
MF "v[104:107],v[28:31],v[208:211],v[104:107]\n"
"v_exp_f32 v128,v128\n"
MF "v[104:107],v[24:27],v[188:191],v[104:107]\n"
"v_exp_f32 v129,v129\n"
MF "v[100:103],v[24:27],v[184:187],v[100:103]\n"
"v_exp_f32 v130,v130\n"
MF "v[100:103],v[28:31],v[204:207],v[100:103]\n"
"v_exp_f32 v131,v131\n"
MF "v[96:99],v[28:31],v[200:203],v[96:99]\n"
"v_add_f32 v56,v56,v128\n"
"v_add_f32 v56,v56,v129\n"
MF "v[96:99],v[24:27],v[180:183],v[96:99]\n"
CV "v90,v128,v129\n"
"v_add_f32 v56,v56,v130\n"
MF "v[92:95],v[24:27],v[176:179],v[92:95]\n"
"v_add_f32 v56,v56,v131\n"
CV "v91,v130,v131\n"
MF "v[92:95],v[28:31],v[196:199],v[92:95]\n"
"s_waitcnt lgkmcnt(0)\n"
"ds_read_b128 v[24:27],v0 offset:30720\n"
"ds_read_b128 v[28:31],v1 offset:30720\n"
MF "v[92:95],v[16:19],v[216:219],v[92:95]\n"
MF "v[92:95],v[20:23],v[236:239],v[92:95]\n"
MF "v[96:99],v[20:23],v[240:243],v[96:99]\n"
MF "v[96:99],v[16:19],v[220:223],v[96:99]\n"
MF "v[100:103],v[16:19],v[224:227],v[100:103]\n"
MF "v[100:103],v[20:23],v[244:247],v[100:103]\n"
MF "v[104:107],v[20:23],v[248:251],v[104:107]\n"
MF "v[104:107],v[16:19],v[228:231],v[104:107]\n"
"v_max3_f32 v10,v52,v53,v54\n"
"v_max3_f32 v10,v10,v55,v56\n"
MF "v[108:111],v[16:19],v[232:235],v[108:111]\n"
"v_cmp_ngt_f32 vcc,s77,v10\n"
MF "v[108:111],v[20:23],v[252:255],v[108:111]\n"
"s_waitcnt lgkmcnt(0)\n"
"s_cbranch_vccnz .Lstub_14\n"
".Lback_14:\n"
MF "v[112:115],v[24:27],v[136:139],v[32:35]\n"
"s_add_u32 s74,s74,12288\n"
"ds_read_b128 v[16:19],v0 offset:31744\n"
"ds_read_b128 v[20:23],v1 offset:31744\n"
MF "v[112:115],v[28:31],v[156:159],v[112:115]\n"
"s_cmp_eq_u32 s74,s20\n"
"s_cselect_b32 s74,s70,s74\n"
MF "v[116:119],v[28:31],v[160:163],v[36:39]\n"
"v_exp_f32 v92,v92\n"
MF "v[116:119],v[24:27],v[140:143],v[116:119]\n"
"v_exp_f32 v93,v93\n"
MF "v[120:123],v[24:27],v[144:147],v[40:43]\n"
"v_exp_f32 v94,v94\n"
MF "v[120:123],v[28:31],v[164:167],v[120:123]\n"
"v_exp_f32 v95,v95\n"
MF "v[124:127],v[28:31],v[168:171],v[44:47]\n"
"v_add_f32 v57,v52,v92\n"
"v_add_f32 v57,v57,v93\n"
MF "v[124:127],v[24:27],v[148:151],v[124:127]\n"
CV "v92,v92,v93\n"
"v_add_f32 v57,v57,v94\n"
MF "v[128:131],v[24:27],v[152:155],v[48:51]\n"
"v_add_f32 v57,v57,v95\n"
CV "v93,v94,v95\n"
MF "v[128:131],v[28:31],v[172:175],v[128:131]\n"
"s_waitcnt lgkmcnt(0)\n"
"ds_read_b128 v[24:27],v0 offset:32768\n"
"ds_read_b128 v[28:31],v1 offset:32768\n"
MF "v[128:131],v[16:19],v[192:195],v[128:131]\n"
"v_exp_f32 v96,v96\n"
MF "v[128:131],v[20:23],v[212:215],v[128:131]\n"
"v_exp_f32 v97,v97\n"
MF "v[124:127],v[20:23],v[208:211],v[124:127]\n"
"v_exp_f32 v98,v98\n"
MF "v[124:127],v[16:19],v[188:191],v[124:127]\n"
"v_exp_f32 v99,v99\n"
MF "v[120:123],v[16:19],v[184:187],v[120:123]\n"
"v_add_f32 v58,v53,v96\n"
"v_add_f32 v58,v58,v97\n"
MF "v[120:123],v[20:23],v[204:207],v[120:123]\n"
CV "v96,v96,v97\n"
"v_add_f32 v58,v58,v98\n"
MF "v[116:119],v[20:23],v[200:203],v[116:119]\n"
"v_add_f32 v58,v58,v99\n"
CV "v97,v98,v99\n"
MF "v[116:119],v[16:19],v[180:183],v[116:119]\n"
"s_waitcnt vmcnt(6) lgkmcnt(0)\n"
"s_barrier\n"
MF "v[112:115],v[16:19],v[176:179],v[112:115]\n"
"v_exp_f32 v100,v100\n"
MF "v[112:115],v[20:23],v[196:199],v[112:115]\n"
"s_nop 0\n"
TR "v[16:17],v2 offset:49152\n"
TR "v[18:19],v2 offset:55296\n"
MF "v[112:115],v[24:27],v[216:219],v[112:115]\n"
"s_add_u32 s73,s73,12288\n"
TR "v[20:21],v3 offset:49152\n"
TR "v[22:23],v3 offset:55296\n"
MF "v[112:115],v[28:31],v[236:239],v[112:115]\n"
"s_cmp_eq_u32 s73,s19\n"
"s_cselect_b32 s73,s18,s73\n"
"s_add_u32 m0,s65,24576\n"
MF "v[116:119],v[28:31],v[240:243],v[116:119]\n"
LD "v8,s[36:39],s73 offen nt lds\n"
MF "v[116:119],v[24:27],v[220:223],v[116:119]\n"
"s_add_u32 m0,s65,25472\n"
"v_exp_f32 v101,v101\n"
MF "v[120:123],v[24:27],v[224:227],v[120:123]\n"
LD "v8,s[36:39],s73 offen offset:128 nt lds\n"
MF "v[120:123],v[28:31],v[244:247],v[120:123]\n"
"v_exp_f32 v102,v102\n"
MF "v[124:127],v[28:31],v[248:251],v[124:127]\n"
"s_add_u32 m0,s65,26368\n"
"v_exp_f32 v103,v103\n"
MF "v[124:127],v[24:27],v[228:231],v[124:127]\n"
LD "v8,s[36:39],s73 offen offset:256 nt lds\n"
MF "v[128:131],v[24:27],v[232:235],v[128:131]\n"
"v_add_f32 v59,v54,v100\n"
"v_add_f32 v59,v59,v101\n"
MF "v[128:131],v[28:31],v[252:255],v[128:131]\n"
"s_waitcnt lgkmcnt(2)\n"
TR "v[24:25],v4 offset:49152\n"
TR "v[26:27],v4 offset:55296\n"
MF "a[0:3],v[16:19],v[72:75],a[0:3]\n"
"s_add_u32 m0,s65,24576\n"
TR "v[28:29],v5 offset:49152\n"
TR "v[30:31],v5 offset:55296\n"
MF "a[4:7],v[16:19],v[76:79],a[4:7]\n"
LD "v8,s[36:39],s73 offen offset:3072 nt lds\n"
MF "a[8:11],v[16:19],v[80:83],a[8:11]\n"
"s_add_u32 m0,s65,25472\n"
CV "v100,v100,v101\n"
"v_add_f32 v59,v59,v102\n"
MF "a[12:15],v[16:19],v[84:87],a[12:15]\n"
LD "v8,s[36:39],s73 offen offset:3200 nt lds\n"
MF "a[16:19],v[16:19],v[88:91],a[16:19]\n"
"s_waitcnt lgkmcnt(4)\n"
TR "v[16:17],v2 offset:50176\n"
TR "v[18:19],v2 offset:56320\n"
MF "a[36:39],v[20:23],v[88:91],a[36:39]\n"
"v_add_f32 v59,v59,v103\n"
CV "v101,v102,v103\n"
MF "a[32:35],v[20:23],v[84:87],a[32:35]\n"
"s_add_u32 m0,s65,26368\n"
"v_exp_f32 v104,v104\n"
MF "a[28:31],v[20:23],v[80:83],a[28:31]\n"
LD "v8,s[36:39],s73 offen offset:3328 nt lds\n"
MF "a[24:27],v[20:23],v[76:79],a[24:27]\n"
"v_exp_f32 v105,v105\n"
MF "a[20:23],v[20:23],v[72:75],a[20:23]\n"
"s_waitcnt lgkmcnt(4)\n"
TR "v[20:21],v3 offset:50176\n"
TR "v[22:23],v3 offset:56320\n"
MF "a[40:43],v[24:27],v[72:75],a[40:43]\n"
"v_exp_f32 v106,v106\n"
MF "a[44:47],v[24:27],v[76:79],a[44:47]\n"
"v_exp_f32 v107,v107\n"
MF "a[48:51],v[24:27],v[80:83],a[48:51]\n"
"v_add_f32 v60,v55,v104\n"
"v_add_f32 v60,v60,v105\n"
MF "a[52:55],v[24:27],v[84:87],a[52:55]\n"
CV "v104,v104,v105\n"
"v_add_f32 v60,v60,v106\n"
MF "a[56:59],v[24:27],v[88:91],a[56:59]\n"
"s_waitcnt lgkmcnt(4)\n"
TR "v[24:25],v4 offset:50176\n"
TR "v[26:27],v4 offset:56320\n"
MF "a[76:79],v[28:31],v[88:91],a[76:79]\n"
"v_add_f32 v60,v60,v107\n"
CV "v105,v106,v107\n"
MF "a[72:75],v[28:31],v[84:87],a[72:75]\n"
"v_exp_f32 v108,v108\n"
MF "a[68:71],v[28:31],v[80:83],a[68:71]\n"
"v_exp_f32 v109,v109\n"
MF "a[64:67],v[28:31],v[76:79],a[64:67]\n"
"v_exp_f32 v110,v110\n"
MF "a[60:63],v[28:31],v[72:75],a[60:63]\n"
"s_waitcnt lgkmcnt(4)\n"
TR "v[28:29],v5 offset:50176\n"
TR "v[30:31],v5 offset:56320\n"
MF "a[80:83],v[16:19],v[72:75],a[80:83]\n"
"v_exp_f32 v111,v111\n"
MF "a[84:87],v[16:19],v[76:79],a[84:87]\n"
"v_add_f32 v61,v56,v108\n"
"v_add_f32 v61,v61,v109\n"
MF "a[88:91],v[16:19],v[80:83],a[88:91]\n"
CV "v108,v108,v109\n"
"v_add_f32 v61,v61,v110\n"
MF "a[92:95],v[16:19],v[84:87],a[92:95]\n"
"v_add_f32 v61,v61,v111\n"
CV "v109,v110,v111\n"
MF "a[96:99],v[16:19],v[88:91],a[96:99]\n"
"s_waitcnt lgkmcnt(4)\n"
TR "v[16:17],v2 offset:51200\n"
TR "v[18:19],v2 offset:57344\n"
MF "a[116:119],v[20:23],v[88:91],a[116:119]\n"
"v_exp_f32 v112,v112\n"
MF "a[112:115],v[20:23],v[84:87],a[112:115]\n"
"v_exp_f32 v113,v113\n"
MF "a[108:111],v[20:23],v[80:83],a[108:111]\n"
"v_exp_f32 v114,v114\n"
MF "a[104:107],v[20:23],v[76:79],a[104:107]\n"
"v_exp_f32 v115,v115\n"
MF "a[100:103],v[20:23],v[72:75],a[100:103]\n"
"s_waitcnt lgkmcnt(4)\n"
TR "v[20:21],v3 offset:51200\n"
TR "v[22:23],v3 offset:57344\n"
MF "a[120:123],v[24:27],v[72:75],a[120:123]\n"
"v_add_f32 v57,v57,v112\n"
"v_add_f32 v57,v57,v113\n"
MF "a[124:127],v[24:27],v[76:79],a[124:127]\n"
CV "v94,v112,v113\n"
"v_add_f32 v57,v57,v114\n"
MF "a[128:131],v[24:27],v[80:83],a[128:131]\n"
"v_add_f32 v57,v57,v115\n"
CV "v95,v114,v115\n"
MF "a[132:135],v[24:27],v[84:87],a[132:135]\n"
"v_exp_f32 v116,v116\n"
MF "a[136:139],v[24:27],v[88:91],a[136:139]\n"
"s_waitcnt lgkmcnt(4)\n"
TR "v[24:25],v4 offset:51200\n"
TR "v[26:27],v4 offset:57344\n"
MF "a[156:159],v[28:31],v[88:91],a[156:159]\n"
"v_exp_f32 v117,v117\n"
MF "a[152:155],v[28:31],v[84:87],a[152:155]\n"
"v_exp_f32 v118,v118\n"
MF "a[148:151],v[28:31],v[80:83],a[148:151]\n"
"v_exp_f32 v119,v119\n"
MF "a[144:147],v[28:31],v[76:79],a[144:147]\n"
"v_add_f32 v58,v58,v116\n"
"v_add_f32 v58,v58,v117\n"
MF "a[140:143],v[28:31],v[72:75],a[140:143]\n"
"s_waitcnt lgkmcnt(4)\n"
TR "v[28:29],v5 offset:51200\n"
TR "v[30:31],v5 offset:57344\n"
MF "a[160:163],v[16:19],v[72:75],a[160:163]\n"
CV "v98,v116,v117\n"
"v_add_f32 v58,v58,v118\n"
MF "a[164:167],v[16:19],v[76:79],a[164:167]\n"
"v_add_f32 v58,v58,v119\n"
CV "v99,v118,v119\n"
MF "a[168:171],v[16:19],v[80:83],a[168:171]\n"
"v_exp_f32 v120,v120\n"
MF "a[172:175],v[16:19],v[84:87],a[172:175]\n"
"v_exp_f32 v121,v121\n"
MF "a[176:179],v[16:19],v[88:91],a[176:179]\n"
"s_waitcnt lgkmcnt(4)\n"
"v_exp_f32 v122,v122\n"
MF "a[196:199],v[20:23],v[88:91],a[196:199]\n"
"v_exp_f32 v123,v123\n"
MF "a[192:195],v[20:23],v[84:87],a[192:195]\n"
"v_add_f32 v59,v59,v120\n"
"v_add_f32 v59,v59,v121\n"
MF "a[188:191],v[20:23],v[80:83],a[188:191]\n"
CV "v102,v120,v121\n"
"v_add_f32 v59,v59,v122\n"
MF "a[184:187],v[20:23],v[76:79],a[184:187]\n"
"s_waitcnt vmcnt(6) lgkmcnt(0)\n"
"s_barrier\n"
MF "a[180:183],v[20:23],v[72:75],a[180:183]\n"
"s_add_u32 s72,s72,6144\n"
"ds_read_b128 v[16:19],v0\n"
"ds_read_b128 v[20:23],v1\n"
MF "a[200:203],v[24:27],v[72:75],a[200:203]\n"
"s_cmp_eq_u32 s72,s17\n"
"s_cselect_b32 s72,s16,s72\n"
"s_add_u32 m0,s64,49152\n"
MF "a[204:207],v[24:27],v[76:79],a[204:207]\n"
LD "v7,s[32:35],s72 offen nt lds\n"
MF "a[208:211],v[24:27],v[80:83],a[208:211]\n"
"s_add_u32 m0,s64,50048\n"
"v_add_f32 v59,v59,v123\n"
CV "v103,v122,v123\n"
MF "a[212:215],v[24:27],v[84:87],a[212:215]\n"
LD "v7,s[32:35],s72 offen offset:128 nt lds\n"
MF "a[216:219],v[24:27],v[88:91],a[216:219]\n"
"s_add_u32 m0,s64,50944\n"
"ds_read_b128 v[24:27],v0 offset:1024\n"
MF "a[236:239],v[28:31],v[88:91],a[236:239]\n"
LD "v7,s[32:35],s72 offen offset:256 nt lds\n"
MF "a[232:235],v[28:31],v[84:87],a[232:235]\n"
MF "a[228:231],v[28:31],v[80:83],a[228:231]\n"
MF "a[224:227],v[28:31],v[76:79],a[224:227]\n"
MF "a[220:223],v[28:31],v[72:75],a[220:223]\n"
"s_waitcnt lgkmcnt(1)\n"
".Lmid:\n"
MF "v[72:75],v[16:19],v[136:139],v[32:35]\n"
"s_add_u32 s72,s72,6144\n"
"s_add_u32 m0,s64,55296\n"
"ds_read_b128 v[28:31],v1 offset:1024\n"
MF "v[72:75],v[20:23],v[156:159],v[72:75]\n"
LD "v7,s[32:35],s72 offen nt lds\n"
MF "v[76:79],v[20:23],v[160:163],v[36:39]\n"
"v_exp_f32 v124,v124\n"
MF "v[76:79],v[16:19],v[140:143],v[76:79]\n"
"v_exp_f32 v125,v125\n"
MF "v[80:83],v[16:19],v[144:147],v[40:43]\n"
"s_add_u32 m0,s64,56192\n"
"v_exp_f32 v126,v126\n"
MF "v[80:83],v[20:23],v[164:167],v[80:83]\n"
LD "v7,s[32:35],s72 offen offset:128 nt lds\n"
MF "v[84:87],v[20:23],v[168:171],v[44:47]\n"
"v_exp_f32 v127,v127\n"
MF "v[84:87],v[16:19],v[148:151],v[84:87]\n"
"s_add_u32 m0,s64,57088\n"
"v_add_f32 v60,v60,v124\n"
"v_add_f32 v60,v60,v125\n"
MF "v[88:91],v[16:19],v[152:155],v[48:51]\n"
LD "v7,s[32:35],s72 offen offset:256 nt lds\n"
MF "v[88:91],v[20:23],v[172:175],v[88:91]\n"
"s_waitcnt lgkmcnt(0)\n"
"ds_read_b128 v[16:19],v0 offset:2048\n"
"ds_read_b128 v[20:23],v1 offset:2048\n"
MF "v[88:91],v[24:27],v[192:195],v[88:91]\n"
CV "v106,v124,v125\n"
"v_add_f32 v60,v60,v126\n"
MF "v[88:91],v[28:31],v[212:215],v[88:91]\n"
"v_add_f32 v60,v60,v127\n"
CV "v107,v126,v127\n"
MF "v[84:87],v[28:31],v[208:211],v[84:87]\n"
"v_exp_f32 v128,v128\n"
MF "v[84:87],v[24:27],v[188:191],v[84:87]\n"
"v_exp_f32 v129,v129\n"
MF "v[80:83],v[24:27],v[184:187],v[80:83]\n"
"v_exp_f32 v130,v130\n"
MF "v[80:83],v[28:31],v[204:207],v[80:83]\n"
"v_exp_f32 v131,v131\n"
MF "v[76:79],v[28:31],v[200:203],v[76:79]\n"
"v_add_f32 v61,v61,v128\n"
"v_add_f32 v61,v61,v129\n"
MF "v[76:79],v[24:27],v[180:183],v[76:79]\n"
CV "v110,v128,v129\n"
"v_add_f32 v61,v61,v130\n"
MF "v[72:75],v[24:27],v[176:179],v[72:75]\n"
"v_add_f32 v61,v61,v131\n"
CV "v111,v130,v131\n"
MF "v[72:75],v[28:31],v[196:199],v[72:75]\n"
"s_waitcnt lgkmcnt(0)\n"
"ds_read_b128 v[24:27],v0 offset:6144\n"
"ds_read_b128 v[28:31],v1 offset:6144\n"
MF "v[72:75],v[16:19],v[216:219],v[72:75]\n"
MF "v[72:75],v[20:23],v[236:239],v[72:75]\n"
MF "v[76:79],v[20:23],v[240:243],v[76:79]\n"
MF "v[76:79],v[16:19],v[220:223],v[76:79]\n"
MF "v[80:83],v[16:19],v[224:227],v[80:83]\n"
MF "v[80:83],v[20:23],v[244:247],v[80:83]\n"
MF "v[84:87],v[20:23],v[248:251],v[84:87]\n"
MF "v[84:87],v[16:19],v[228:231],v[84:87]\n"
"v_max3_f32 v10,v57,v58,v59\n"
"v_max3_f32 v10,v10,v60,v61\n"
MF "v[88:91],v[16:19],v[232:235],v[88:91]\n"
"v_cmp_ngt_f32 vcc,s95,v10\n"
MF "v[88:91],v[20:23],v[252:255],v[88:91]\n"
"s_waitcnt lgkmcnt(0)\n"
"s_cbranch_vccnz .Lstub_15\n"
".Lback_15:\n"
MF "v[112:115],v[24:27],v[136:139],v[32:35]\n"
"s_add_u32 s74,s74,12288\n"
"ds_read_b128 v[16:19],v0 offset:7168\n"
"ds_read_b128 v[20:23],v1 offset:7168\n"
MF "v[112:115],v[28:31],v[156:159],v[112:115]\n"
"s_cmp_eq_u32 s74,s20\n"
"s_cselect_b32 s74,s70,s74\n"
MF "v[116:119],v[28:31],v[160:163],v[36:39]\n"
"v_exp_f32 v72,v72\n"
MF "v[116:119],v[24:27],v[140:143],v[116:119]\n"
"v_exp_f32 v73,v73\n"
MF "v[120:123],v[24:27],v[144:147],v[40:43]\n"
"v_exp_f32 v74,v74\n"
MF "v[120:123],v[28:31],v[164:167],v[120:123]\n"
"v_exp_f32 v75,v75\n"
MF "v[124:127],v[28:31],v[168:171],v[44:47]\n"
"v_add_f32 v52,v57,v72\n"
"v_add_f32 v52,v52,v73\n"
MF "v[124:127],v[24:27],v[148:151],v[124:127]\n"
CV "v72,v72,v73\n"
"v_add_f32 v52,v52,v74\n"
MF "v[128:131],v[24:27],v[152:155],v[48:51]\n"
"v_add_f32 v52,v52,v75\n"
CV "v73,v74,v75\n"
MF "v[128:131],v[28:31],v[172:175],v[128:131]\n"
"s_waitcnt lgkmcnt(0)\n"
"ds_read_b128 v[24:27],v0 offset:8192\n"
"ds_read_b128 v[28:31],v1 offset:8192\n"
MF "v[128:131],v[16:19],v[192:195],v[128:131]\n"
"v_exp_f32 v76,v76\n"
MF "v[128:131],v[20:23],v[212:215],v[128:131]\n"
"v_exp_f32 v77,v77\n"
MF "v[124:127],v[20:23],v[208:211],v[124:127]\n"
"v_exp_f32 v78,v78\n"
MF "v[124:127],v[16:19],v[188:191],v[124:127]\n"
"v_exp_f32 v79,v79\n"
MF "v[120:123],v[16:19],v[184:187],v[120:123]\n"
"v_add_f32 v53,v58,v76\n"
"v_add_f32 v53,v53,v77\n"
MF "v[120:123],v[20:23],v[204:207],v[120:123]\n"
CV "v76,v76,v77\n"
"v_add_f32 v53,v53,v78\n"
MF "v[116:119],v[20:23],v[200:203],v[116:119]\n"
"v_add_f32 v53,v53,v79\n"
CV "v77,v78,v79\n"
MF "v[116:119],v[16:19],v[180:183],v[116:119]\n"
"s_waitcnt vmcnt(6) lgkmcnt(0)\n"
"s_barrier\n"
MF "v[112:115],v[16:19],v[176:179],v[112:115]\n"
"v_exp_f32 v80,v80\n"
MF "v[112:115],v[20:23],v[196:199],v[112:115]\n"
"s_nop 0\n"
TR "v[16:17],v2 offset:24576\n"
TR "v[18:19],v2 offset:30720\n"
MF "v[112:115],v[24:27],v[216:219],v[112:115]\n"
"s_add_u32 s73,s73,12288\n"
TR "v[20:21],v3 offset:24576\n"
TR "v[22:23],v3 offset:30720\n"
MF "v[112:115],v[28:31],v[236:239],v[112:115]\n"
"s_cmp_eq_u32 s73,s19\n"
"s_cselect_b32 s73,s18,s73\n"
"s_add_u32 m0,s65,0\n"
MF "v[116:119],v[28:31],v[240:243],v[116:119]\n"
LD "v8,s[36:39],s73 offen nt lds\n"
MF "v[116:119],v[24:27],v[220:223],v[116:119]\n"
"s_add_u32 m0,s65,896\n"
"v_exp_f32 v81,v81\n"
MF "v[120:123],v[24:27],v[224:227],v[120:123]\n"
LD "v8,s[36:39],s73 offen offset:128 nt lds\n"
MF "v[120:123],v[28:31],v[244:247],v[120:123]\n"
"v_exp_f32 v82,v82\n"
MF "v[124:127],v[28:31],v[248:251],v[124:127]\n"
"s_add_u32 m0,s65,1792\n"
"v_exp_f32 v83,v83\n"
MF "v[124:127],v[24:27],v[228:231],v[124:127]\n"
LD "v8,s[36:39],s73 offen offset:256 nt lds\n"
MF "v[128:131],v[24:27],v[232:235],v[128:131]\n"
"v_add_f32 v54,v59,v80\n"
"v_add_f32 v54,v54,v81\n"
MF "v[128:131],v[28:31],v[252:255],v[128:131]\n"
"s_waitcnt lgkmcnt(2)\n"
TR "v[24:25],v4 offset:24576\n"
TR "v[26:27],v4 offset:30720\n"
MF "a[0:3],v[16:19],v[92:95],a[0:3]\n"
"s_add_u32 m0,s65,0\n"
TR "v[28:29],v5 offset:24576\n"
TR "v[30:31],v5 offset:30720\n"
MF "a[4:7],v[16:19],v[96:99],a[4:7]\n"
LD "v8,s[36:39],s73 offen offset:3072 nt lds\n"
MF "a[8:11],v[16:19],v[100:103],a[8:11]\n"
"s_add_u32 m0,s65,896\n"
CV "v80,v80,v81\n"
"v_add_f32 v54,v54,v82\n"
MF "a[12:15],v[16:19],v[104:107],a[12:15]\n"
LD "v8,s[36:39],s73 offen offset:3200 nt lds\n"
MF "a[16:19],v[16:19],v[108:111],a[16:19]\n"
"s_waitcnt lgkmcnt(4)\n"
TR "v[16:17],v2 offset:25600\n"
TR "v[18:19],v2 offset:31744\n"
MF "a[36:39],v[20:23],v[108:111],a[36:39]\n"
"v_add_f32 v54,v54,v83\n"
CV "v81,v82,v83\n"
MF "a[32:35],v[20:23],v[104:107],a[32:35]\n"
"s_add_u32 m0,s65,1792\n"
"v_exp_f32 v84,v84\n"
MF "a[28:31],v[20:23],v[100:103],a[28:31]\n"
LD "v8,s[36:39],s73 offen offset:3328 nt lds\n"
MF "a[24:27],v[20:23],v[96:99],a[24:27]\n"
"v_exp_f32 v85,v85\n"
MF "a[20:23],v[20:23],v[92:95],a[20:23]\n"
"s_waitcnt lgkmcnt(4)\n"
TR "v[20:21],v3 offset:25600\n"
TR "v[22:23],v3 offset:31744\n"
MF "a[40:43],v[24:27],v[92:95],a[40:43]\n"
"v_exp_f32 v86,v86\n"
MF "a[44:47],v[24:27],v[96:99],a[44:47]\n"
"v_exp_f32 v87,v87\n"
MF "a[48:51],v[24:27],v[100:103],a[48:51]\n"
"v_add_f32 v55,v60,v84\n"
"v_add_f32 v55,v55,v85\n"
MF "a[52:55],v[24:27],v[104:107],a[52:55]\n"
CV "v84,v84,v85\n"
"v_add_f32 v55,v55,v86\n"
MF "a[56:59],v[24:27],v[108:111],a[56:59]\n"
"s_waitcnt lgkmcnt(4)\n"
TR "v[24:25],v4 offset:25600\n"
TR "v[26:27],v4 offset:31744\n"
MF "a[76:79],v[28:31],v[108:111],a[76:79]\n"
"v_add_f32 v55,v55,v87\n"
CV "v85,v86,v87\n"
MF "a[72:75],v[28:31],v[104:107],a[72:75]\n"
"v_exp_f32 v88,v88\n"
MF "a[68:71],v[28:31],v[100:103],a[68:71]\n"
"v_exp_f32 v89,v89\n"
MF "a[64:67],v[28:31],v[96:99],a[64:67]\n"
"v_exp_f32 v90,v90\n"
MF "a[60:63],v[28:31],v[92:95],a[60:63]\n"
"s_waitcnt lgkmcnt(4)\n"
TR "v[28:29],v5 offset:25600\n"
TR "v[30:31],v5 offset:31744\n"
MF "a[80:83],v[16:19],v[92:95],a[80:83]\n"
"v_exp_f32 v91,v91\n"
MF "a[84:87],v[16:19],v[96:99],a[84:87]\n"
"v_add_f32 v56,v61,v88\n"
"v_add_f32 v56,v56,v89\n"
MF "a[88:91],v[16:19],v[100:103],a[88:91]\n"
CV "v88,v88,v89\n"
"v_add_f32 v56,v56,v90\n"
MF "a[92:95],v[16:19],v[104:107],a[92:95]\n"
"v_add_f32 v56,v56,v91\n"
CV "v89,v90,v91\n"
MF "a[96:99],v[16:19],v[108:111],a[96:99]\n"
"s_waitcnt lgkmcnt(4)\n"
TR "v[16:17],v2 offset:26624\n"
TR "v[18:19],v2 offset:32768\n"
MF "a[116:119],v[20:23],v[108:111],a[116:119]\n"
"v_exp_f32 v112,v112\n"
MF "a[112:115],v[20:23],v[104:107],a[112:115]\n"
"v_exp_f32 v113,v113\n"
MF "a[108:111],v[20:23],v[100:103],a[108:111]\n"
"v_exp_f32 v114,v114\n"
MF "a[104:107],v[20:23],v[96:99],a[104:107]\n"
"v_exp_f32 v115,v115\n"
MF "a[100:103],v[20:23],v[92:95],a[100:103]\n"
"s_waitcnt lgkmcnt(4)\n"
TR "v[20:21],v3 offset:26624\n"
TR "v[22:23],v3 offset:32768\n"
MF "a[120:123],v[24:27],v[92:95],a[120:123]\n"
"v_add_f32 v52,v52,v112\n"
"v_add_f32 v52,v52,v113\n"
MF "a[124:127],v[24:27],v[96:99],a[124:127]\n"
CV "v74,v112,v113\n"
"v_add_f32 v52,v52,v114\n"
MF "a[128:131],v[24:27],v[100:103],a[128:131]\n"
"v_add_f32 v52,v52,v115\n"
CV "v75,v114,v115\n"
MF "a[132:135],v[24:27],v[104:107],a[132:135]\n"
"v_exp_f32 v116,v116\n"
MF "a[136:139],v[24:27],v[108:111],a[136:139]\n"
"s_waitcnt lgkmcnt(4)\n"
TR "v[24:25],v4 offset:26624\n"
TR "v[26:27],v4 offset:32768\n"
MF "a[156:159],v[28:31],v[108:111],a[156:159]\n"
"v_exp_f32 v117,v117\n"
MF "a[152:155],v[28:31],v[104:107],a[152:155]\n"
"v_exp_f32 v118,v118\n"
MF "a[148:151],v[28:31],v[100:103],a[148:151]\n"
"v_exp_f32 v119,v119\n"
MF "a[144:147],v[28:31],v[96:99],a[144:147]\n"
"v_add_f32 v53,v53,v116\n"
"v_add_f32 v53,v53,v117\n"
MF "a[140:143],v[28:31],v[92:95],a[140:143]\n"
"s_waitcnt lgkmcnt(4)\n"
TR "v[28:29],v5 offset:26624\n"
TR "v[30:31],v5 offset:32768\n"
MF "a[160:163],v[16:19],v[92:95],a[160:163]\n"
CV "v78,v116,v117\n"
"v_add_f32 v53,v53,v118\n"
MF "a[164:167],v[16:19],v[96:99],a[164:167]\n"
"v_add_f32 v53,v53,v119\n"
CV "v79,v118,v119\n"
MF "a[168:171],v[16:19],v[100:103],a[168:171]\n"
"v_exp_f32 v120,v120\n"
MF "a[172:175],v[16:19],v[104:107],a[172:175]\n"
"v_exp_f32 v121,v121\n"
MF "a[176:179],v[16:19],v[108:111],a[176:179]\n"
"s_waitcnt lgkmcnt(4)\n"
"v_exp_f32 v122,v122\n"
MF "a[196:199],v[20:23],v[108:111],a[196:199]\n"
"v_exp_f32 v123,v123\n"
MF "a[192:195],v[20:23],v[104:107],a[192:195]\n"
"v_add_f32 v54,v54,v120\n"
"v_add_f32 v54,v54,v121\n"
MF "a[188:191],v[20:23],v[100:103],a[188:191]\n"
CV "v82,v120,v121\n"
"v_add_f32 v54,v54,v122\n"
MF "a[184:187],v[20:23],v[96:99],a[184:187]\n"
"s_waitcnt vmcnt(6) lgkmcnt(0)\n"
"s_barrier\n"
MF "a[180:183],v[20:23],v[92:95],a[180:183]\n"
"s_add_u32 s72,s72,6144\n"
"ds_read_b128 v[16:19],v0 offset:49152\n"
"ds_read_b128 v[20:23],v1 offset:49152\n"
MF "a[200:203],v[24:27],v[92:95],a[200:203]\n"
"s_cmp_eq_u32 s72,s17\n"
"s_cselect_b32 s72,s16,s72\n"
"s_add_u32 m0,s64,24576\n"
MF "a[204:207],v[24:27],v[96:99],a[204:207]\n"
LD "v7,s[32:35],s72 offen nt lds\n"
MF "a[208:211],v[24:27],v[100:103],a[208:211]\n"
"s_add_u32 m0,s64,25472\n"
"v_add_f32 v54,v54,v123\n"
CV "v83,v122,v123\n"
MF "a[212:215],v[24:27],v[104:107],a[212:215]\n"
LD "v7,s[32:35],s72 offen offset:128 nt lds\n"
MF "a[216:219],v[24:27],v[108:111],a[216:219]\n"
"s_add_u32 m0,s64,26368\n"
"ds_read_b128 v[24:27],v0 offset:50176\n"
MF "a[236:239],v[28:31],v[108:111],a[236:239]\n"
LD "v7,s[32:35],s72 offen offset:256 nt lds\n"
MF "a[232:235],v[28:31],v[104:107],a[232:235]\n"
MF "a[228:231],v[28:31],v[100:103],a[228:231]\n"
MF "a[224:227],v[28:31],v[96:99],a[224:227]\n"
MF "a[220:223],v[28:31],v[92:95],a[220:223]\n"
"s_waitcnt lgkmcnt(1)\n"
MF "v[92:95],v[16:19],v[136:139],v[32:35]\n"
"s_add_u32 s72,s72,6144\n"
"s_add_u32 m0,s64,30720\n"
"ds_read_b128 v[28:31],v1 offset:50176\n"
MF "v[92:95],v[20:23],v[156:159],v[92:95]\n"
LD "v7,s[32:35],s72 offen nt lds\n"
MF "v[96:99],v[20:23],v[160:163],v[36:39]\n"
"v_exp_f32 v124,v124\n"
MF "v[96:99],v[16:19],v[140:143],v[96:99]\n"
"v_exp_f32 v125,v125\n"
MF "v[100:103],v[16:19],v[144:147],v[40:43]\n"
"s_add_u32 m0,s64,31616\n"
"v_exp_f32 v126,v126\n"
MF "v[100:103],v[20:23],v[164:167],v[100:103]\n"
LD "v7,s[32:35],s72 offen offset:128 nt lds\n"
MF "v[104:107],v[20:23],v[168:171],v[44:47]\n"
"v_exp_f32 v127,v127\n"
MF "v[104:107],v[16:19],v[148:151],v[104:107]\n"
"s_add_u32 m0,s64,32512\n"
"v_add_f32 v55,v55,v124\n"
"v_add_f32 v55,v55,v125\n"
MF "v[108:111],v[16:19],v[152:155],v[48:51]\n"
LD "v7,s[32:35],s72 offen offset:256 nt lds\n"
MF "v[108:111],v[20:23],v[172:175],v[108:111]\n"
"s_waitcnt lgkmcnt(0)\n"
"ds_read_b128 v[16:19],v0 offset:51200\n"
"ds_read_b128 v[20:23],v1 offset:51200\n"
MF "v[108:111],v[24:27],v[192:195],v[108:111]\n"
CV "v86,v124,v125\n"
"v_add_f32 v55,v55,v126\n"
MF "v[108:111],v[28:31],v[212:215],v[108:111]\n"
"v_add_f32 v55,v55,v127\n"
CV "v87,v126,v127\n"
MF "v[104:107],v[28:31],v[208:211],v[104:107]\n"
"v_exp_f32 v128,v128\n"
MF "v[104:107],v[24:27],v[188:191],v[104:107]\n"
"v_exp_f32 v129,v129\n"
MF "v[100:103],v[24:27],v[184:187],v[100:103]\n"
"v_exp_f32 v130,v130\n"
MF "v[100:103],v[28:31],v[204:207],v[100:103]\n"
"v_exp_f32 v131,v131\n"
MF "v[96:99],v[28:31],v[200:203],v[96:99]\n"
"v_add_f32 v56,v56,v128\n"
"v_add_f32 v56,v56,v129\n"
MF "v[96:99],v[24:27],v[180:183],v[96:99]\n"
CV "v90,v128,v129\n"
"v_add_f32 v56,v56,v130\n"
MF "v[92:95],v[24:27],v[176:179],v[92:95]\n"
"v_add_f32 v56,v56,v131\n"
CV "v91,v130,v131\n"
MF "v[92:95],v[28:31],v[196:199],v[92:95]\n"
"s_waitcnt lgkmcnt(0)\n"
"ds_read_b128 v[24:27],v0 offset:55296\n"
"ds_read_b128 v[28:31],v1 offset:55296\n"
MF "v[92:95],v[16:19],v[216:219],v[92:95]\n"
MF "v[92:95],v[20:23],v[236:239],v[92:95]\n"
MF "v[96:99],v[20:23],v[240:243],v[96:99]\n"
MF "v[96:99],v[16:19],v[220:223],v[96:99]\n"
MF "v[100:103],v[16:19],v[224:227],v[100:103]\n"
MF "v[100:103],v[20:23],v[244:247],v[100:103]\n"
MF "v[104:107],v[20:23],v[248:251],v[104:107]\n"
MF "v[104:107],v[16:19],v[228:231],v[104:107]\n"
"v_max3_f32 v10,v52,v53,v54\n"
"v_max3_f32 v10,v10,v55,v56\n"
MF "v[108:111],v[16:19],v[232:235],v[108:111]\n"
"v_cmp_ngt_f32 vcc,s77,v10\n"
MF "v[108:111],v[20:23],v[252:255],v[108:111]\n"
"s_waitcnt lgkmcnt(0)\n"
"s_cbranch_vccnz .Lstub_16\n"
".Lback_16:\n"
MF "v[112:115],v[24:27],v[136:139],v[32:35]\n"
"s_add_u32 s74,s74,12288\n"
"ds_read_b128 v[16:19],v0 offset:56320\n"
"ds_read_b128 v[20:23],v1 offset:56320\n"
MF "v[112:115],v[28:31],v[156:159],v[112:115]\n"
"s_cmp_eq_u32 s74,s20\n"
"s_cselect_b32 s74,s70,s74\n"
MF "v[116:119],v[28:31],v[160:163],v[36:39]\n"
"v_exp_f32 v92,v92\n"
MF "v[116:119],v[24:27],v[140:143],v[116:119]\n"
"v_exp_f32 v93,v93\n"
MF "v[120:123],v[24:27],v[144:147],v[40:43]\n"
"v_exp_f32 v94,v94\n"
MF "v[120:123],v[28:31],v[164:167],v[120:123]\n"
"v_exp_f32 v95,v95\n"
MF "v[124:127],v[28:31],v[168:171],v[44:47]\n"
"v_add_f32 v57,v52,v92\n"
"v_add_f32 v57,v57,v93\n"
MF "v[124:127],v[24:27],v[148:151],v[124:127]\n"
CV "v92,v92,v93\n"
"v_add_f32 v57,v57,v94\n"
MF "v[128:131],v[24:27],v[152:155],v[48:51]\n"
"v_add_f32 v57,v57,v95\n"
CV "v93,v94,v95\n"
MF "v[128:131],v[28:31],v[172:175],v[128:131]\n"
"s_waitcnt lgkmcnt(0)\n"
"ds_read_b128 v[24:27],v0 offset:57344\n"
"ds_read_b128 v[28:31],v1 offset:57344\n"
MF "v[128:131],v[16:19],v[192:195],v[128:131]\n"
"s_sub_u32 s61,s61,1\n"
"v_exp_f32 v96,v96\n"
MF "v[128:131],v[20:23],v[212:215],v[128:131]\n"
"s_cmp_eq_u32 s61,1\n"
"s_cselect_b32 s95,0,s95\n"
"v_add_f32 v58,v53,v96\n"
MF "v[124:127],v[20:23],v[208:211],v[124:127]\n"
"v_exp_f32 v97,v97\n"
MF "v[124:127],v[16:19],v[188:191],v[124:127]\n"
"v_exp_f32 v98,v98\n"
MF "v[120:123],v[16:19],v[184:187],v[120:123]\n"
"v_exp_f32 v99,v99\n"
MF "v[120:123],v[20:23],v[204:207],v[120:123]\n"
"v_add_f32 v58,v58,v97\n"
CV "v96,v96,v97\n"
MF "v[116:119],v[20:23],v[200:203],v[116:119]\n"
"v_add_f32 v58,v58,v98\n"
"v_add_f32 v58,v58,v99\n"
MF "v[116:119],v[16:19],v[180:183],v[116:119]\n"
"s_waitcnt vmcnt(6) lgkmcnt(0)\n"
"s_barrier\n"
MF "v[112:115],v[16:19],v[176:179],v[112:115]\n"
CV "v97,v98,v99\n"
MF "v[112:115],v[20:23],v[196:199],v[112:115]\n"
"s_nop 0\n"
TR "v[16:17],v2\n"
TR "v[18:19],v2 offset:6144\n"
MF "v[112:115],v[24:27],v[216:219],v[112:115]\n"
"s_add_u32 s73,s73,12288\n"
TR "v[20:21],v3\n"
TR "v[22:23],v3 offset:6144\n"
MF "v[112:115],v[28:31],v[236:239],v[112:115]\n"
"s_cmp_eq_u32 s73,s19\n"
"s_cselect_b32 s73,s18,s73\n"
"s_add_u32 m0,s65,49152\n"
MF "v[116:119],v[28:31],v[240:243],v[116:119]\n"
LD "v8,s[36:39],s73 offen nt lds\n"
MF "v[116:119],v[24:27],v[220:223],v[116:119]\n"
"s_add_u32 m0,s65,50048\n"
"v_exp_f32 v100,v100\n"
MF "v[120:123],v[24:27],v[224:227],v[120:123]\n"
LD "v8,s[36:39],s73 offen offset:128 nt lds\n"
MF "v[120:123],v[28:31],v[244:247],v[120:123]\n"
"v_exp_f32 v101,v101\n"
MF "v[124:127],v[28:31],v[248:251],v[124:127]\n"
"s_add_u32 m0,s65,50944\n"
"v_exp_f32 v102,v102\n"
MF "v[124:127],v[24:27],v[228:231],v[124:127]\n"
LD "v8,s[36:39],s73 offen offset:256 nt lds\n"
MF "v[128:131],v[24:27],v[232:235],v[128:131]\n"
"v_exp_f32 v103,v103\n"
MF "v[128:131],v[28:31],v[252:255],v[128:131]\n"
"s_waitcnt lgkmcnt(2)\n"
TR "v[24:25],v4\n"
TR "v[26:27],v4 offset:6144\n"
MF "a[0:3],v[16:19],v[72:75],a[0:3]\n"
"s_add_u32 m0,s65,49152\n"
TR "v[28:29],v5\n"
TR "v[30:31],v5 offset:6144\n"
MF "a[4:7],v[16:19],v[76:79],a[4:7]\n"
LD "v8,s[36:39],s73 offen offset:3072 nt lds\n"
MF "a[8:11],v[16:19],v[80:83],a[8:11]\n"
"s_add_u32 m0,s65,50048\n"
"v_add_f32 v59,v54,v100\n"
"v_add_f32 v59,v59,v101\n"
MF "a[12:15],v[16:19],v[84:87],a[12:15]\n"
LD "v8,s[36:39],s73 offen offset:3200 nt lds\n"
MF "a[16:19],v[16:19],v[88:91],a[16:19]\n"
"s_waitcnt lgkmcnt(4)\n"
TR "v[16:17],v2 offset:1024\n"
TR "v[18:19],v2 offset:7168\n"
MF "a[36:39],v[20:23],v[88:91],a[36:39]\n"
CV "v100,v100,v101\n"
"v_add_f32 v59,v59,v102\n"
MF "a[32:35],v[20:23],v[84:87],a[32:35]\n"
"s_add_u32 m0,s65,50944\n"
"v_add_f32 v59,v59,v103\n"
CV "v101,v102,v103\n"
MF "a[28:31],v[20:23],v[80:83],a[28:31]\n"
LD "v8,s[36:39],s73 offen offset:3328 nt lds\n"
MF "a[24:27],v[20:23],v[76:79],a[24:27]\n"
"v_exp_f32 v104,v104\n"
MF "a[20:23],v[20:23],v[72:75],a[20:23]\n"
"s_waitcnt lgkmcnt(4)\n"
TR "v[20:21],v3 offset:1024\n"
TR "v[22:23],v3 offset:7168\n"
MF "a[40:43],v[24:27],v[72:75],a[40:43]\n"
"v_exp_f32 v105,v105\n"
MF "a[44:47],v[24:27],v[76:79],a[44:47]\n"
"v_exp_f32 v106,v106\n"
MF "a[48:51],v[24:27],v[80:83],a[48:51]\n"
"v_exp_f32 v107,v107\n"
MF "a[52:55],v[24:27],v[84:87],a[52:55]\n"
"v_add_f32 v60,v55,v104\n"
"v_add_f32 v60,v60,v105\n"
MF "a[56:59],v[24:27],v[88:91],a[56:59]\n"
"s_waitcnt lgkmcnt(4)\n"
TR "v[24:25],v4 offset:1024\n"
TR "v[26:27],v4 offset:7168\n"
MF "a[76:79],v[28:31],v[88:91],a[76:79]\n"
CV "v104,v104,v105\n"
"v_add_f32 v60,v60,v106\n"
MF "a[72:75],v[28:31],v[84:87],a[72:75]\n"
"v_add_f32 v60,v60,v107\n"
CV "v105,v106,v107\n"
MF "a[68:71],v[28:31],v[80:83],a[68:71]\n"
"v_exp_f32 v108,v108\n"
MF "a[64:67],v[28:31],v[76:79],a[64:67]\n"
"v_exp_f32 v109,v109\n"
MF "a[60:63],v[28:31],v[72:75],a[60:63]\n"
"s_waitcnt lgkmcnt(4)\n"
TR "v[28:29],v5 offset:1024\n"
TR "v[30:31],v5 offset:7168\n"
MF "a[80:83],v[16:19],v[72:75],a[80:83]\n"
"v_exp_f32 v110,v110\n"
MF "a[84:87],v[16:19],v[76:79],a[84:87]\n"
"v_exp_f32 v111,v111\n"
MF "a[88:91],v[16:19],v[80:83],a[88:91]\n"
"v_add_f32 v61,v56,v108\n"
"v_add_f32 v61,v61,v109\n"
MF "a[92:95],v[16:19],v[84:87],a[92:95]\n"
CV "v108,v108,v109\n"
"v_add_f32 v61,v61,v110\n"
MF "a[96:99],v[16:19],v[88:91],a[96:99]\n"
"s_waitcnt lgkmcnt(4)\n"
TR "v[16:17],v2 offset:2048\n"
TR "v[18:19],v2 offset:8192\n"
MF "a[116:119],v[20:23],v[88:91],a[116:119]\n"
"v_add_f32 v61,v61,v111\n"
CV "v109,v110,v111\n"
MF "a[112:115],v[20:23],v[84:87],a[112:115]\n"
"v_exp_f32 v112,v112\n"
MF "a[108:111],v[20:23],v[80:83],a[108:111]\n"
"v_exp_f32 v113,v113\n"
MF "a[104:107],v[20:23],v[76:79],a[104:107]\n"
"v_exp_f32 v114,v114\n"
MF "a[100:103],v[20:23],v[72:75],a[100:103]\n"
"s_waitcnt lgkmcnt(4)\n"
TR "v[20:21],v3 offset:2048\n"
TR "v[22:23],v3 offset:8192\n"
MF "a[120:123],v[24:27],v[72:75],a[120:123]\n"
"v_exp_f32 v115,v115\n"
MF "a[124:127],v[24:27],v[76:79],a[124:127]\n"
"v_add_f32 v57,v57,v112\n"
"v_add_f32 v57,v57,v113\n"
MF "a[128:131],v[24:27],v[80:83],a[128:131]\n"
CV "v94,v112,v113\n"
"v_add_f32 v57,v57,v114\n"
MF "a[132:135],v[24:27],v[84:87],a[132:135]\n"
"v_add_f32 v57,v57,v115\n"
CV "v95,v114,v115\n"
MF "a[136:139],v[24:27],v[88:91],a[136:139]\n"
"s_waitcnt lgkmcnt(4)\n"
TR "v[24:25],v4 offset:2048\n"
TR "v[26:27],v4 offset:8192\n"
MF "a[156:159],v[28:31],v[88:91],a[156:159]\n"
"v_exp_f32 v116,v116\n"
MF "a[152:155],v[28:31],v[84:87],a[152:155]\n"
"v_exp_f32 v117,v117\n"
MF "a[148:151],v[28:31],v[80:83],a[148:151]\n"
"v_exp_f32 v118,v118\n"
MF "a[144:147],v[28:31],v[76:79],a[144:147]\n"
"v_exp_f32 v119,v119\n"
MF "a[140:143],v[28:31],v[72:75],a[140:143]\n"
"s_waitcnt lgkmcnt(4)\n"
TR "v[28:29],v5 offset:2048\n"
TR "v[30:31],v5 offset:8192\n"
MF "a[160:163],v[16:19],v[72:75],a[160:163]\n"
"v_add_f32 v58,v58,v116\n"
"v_add_f32 v58,v58,v117\n"
MF "a[164:167],v[16:19],v[76:79],a[164:167]\n"
CV "v98,v116,v117\n"
"v_add_f32 v58,v58,v118\n"
MF "a[168:171],v[16:19],v[80:83],a[168:171]\n"
"v_add_f32 v58,v58,v119\n"
CV "v99,v118,v119\n"
MF "a[172:175],v[16:19],v[84:87],a[172:175]\n"
"v_exp_f32 v120,v120\n"
MF "a[176:179],v[16:19],v[88:91],a[176:179]\n"
"s_waitcnt lgkmcnt(4)\n"
"v_exp_f32 v121,v121\n"
MF "a[196:199],v[20:23],v[88:91],a[196:199]\n"
"v_exp_f32 v122,v122\n"
MF "a[192:195],v[20:23],v[84:87],a[192:195]\n"
"v_exp_f32 v123,v123\n"
MF "a[188:191],v[20:23],v[80:83],a[188:191]\n"
"v_add_f32 v59,v59,v120\n"
"v_add_f32 v59,v59,v121\n"
MF "a[184:187],v[20:23],v[76:79],a[184:187]\n"
"s_waitcnt vmcnt(6) lgkmcnt(0)\n"
"s_barrier\n"
MF "a[180:183],v[20:23],v[72:75],a[180:183]\n"
"s_add_u32 s72,s72,6144\n"
"ds_read_b128 v[16:19],v0 offset:24576\n"
"ds_read_b128 v[20:23],v1 offset:24576\n"
MF "a[200:203],v[24:27],v[72:75],a[200:203]\n"
"s_cmp_eq_u32 s72,s17\n"
"s_cselect_b32 s72,s16,s72\n"
"s_add_u32 m0,s64,0\n"
MF "a[204:207],v[24:27],v[76:79],a[204:207]\n"
LD "v7,s[32:35],s72 offen nt lds\n"
MF "a[208:211],v[24:27],v[80:83],a[208:211]\n"
"s_add_u32 m0,s64,896\n"
CV "v102,v120,v121\n"
"v_add_f32 v59,v59,v122\n"
MF "a[212:215],v[24:27],v[84:87],a[212:215]\n"
LD "v7,s[32:35],s72 offen offset:128 nt lds\n"
MF "a[216:219],v[24:27],v[88:91],a[216:219]\n"
"s_add_u32 m0,s64,1792\n"
"ds_read_b128 v[24:27],v0 offset:25600\n"
"v_add_f32 v59,v59,v123\n"
MF "a[236:239],v[28:31],v[88:91],a[236:239]\n"
LD "v7,s[32:35],s72 offen offset:256 nt lds\n"
MF "a[232:235],v[28:31],v[84:87],a[232:235]\n"
CV "v103,v122,v123\n"
MF "a[228:231],v[28:31],v[80:83],a[228:231]\n"
"s_waitcnt lgkmcnt(1)\n"
MF "a[224:227],v[28:31],v[76:79],a[224:227]\n"
"s_cmp_lg_u32 s61,0\n"
MF "a[220:223],v[28:31],v[72:75],a[220:223]\n"
"s_cbranch_scc1 .Laloop\n"
"v_exp_f32 v124,v124\n"
"v_exp_f32 v125,v125\n"
"v_exp_f32 v126,v126\n"
"v_exp_f32 v127,v127\n"
"v_exp_f32 v128,v128\n"
"v_exp_f32 v129,v129\n"
"v_exp_f32 v130,v130\n"
"v_exp_f32 v131,v131\n"
"v_add_f32 v60,v60,v124\n"
"v_add_f32 v61,v61,v128\n"
"v_add_f32 v60,v60,v125\n"
"v_add_f32 v61,v61,v129\n"
"v_add_f32 v60,v60,v126\n"
"v_add_f32 v61,v61,v130\n"
"v_add_f32 v60,v60,v127\n"
"v_add_f32 v61,v61,v131\n"
CV "v106,v124,v125\n"
CV "v107,v126,v127\n"
CV "v110,v128,v129\n"
CV "v111,v130,v131\n"
"v_max3_f32 v10,v57,v58,v59\n"
"v_max3_f32 v10,v10,v60,v61\n"
"v_cmp_le_f32 vcc,0x47800000,v10\n"
"s_cbranch_vccnz .Lstub_17\n"
".Lback_17:\n"
"s_waitcnt vmcnt(3) lgkmcnt(0)\n"
"s_barrier\n"
"s_and_b32 s58,s56,1\n"
"s_mul_i32 s79,s58,12288\n"
"s_lshr_b32 s58,s56,1\n"
"s_mul_i32 s72,s58,61440\n"
"s_mul_i32 s73,s58,6144\n"
"s_add_u32 s73,s73,s79\n"
"v_lshrrev_b32_e64 v192,7,v9\n"
"v_mul_u32_u24 v192,896,v192\n"
"v_add_u32_e64 v192,v192,v8\n"
"s_add_u32 s88,s88,1024\n"
"s_add_u32 s89,s89,1024\n"
"s_add_u32 s72,s72,122880\n"
"s_lshl_b32 s58,s56,10\n"
"s_add_u32 s58,s58,73728\n"
"v_add_u32 v236,s58,v9\n"
"v_add_u32 v237,65536,v236\n"
TR "v[160:161],v4 offset:50176\n"
TR "v[162:163],v4 offset:56320\n"
TR "v[164:165],v5 offset:50176\n"
TR "v[166:167],v5 offset:56320\n"
TR "v[168:169],v2 offset:51200\n"
TR "v[170:171],v2 offset:57344\n"
TR "v[172:173],v3 offset:51200\n"
TR "v[174:175],v3 offset:57344\n"
TR "v[176:177],v4 offset:51200\n"
TR "v[178:179],v4 offset:57344\n"
TR "v[180:181],v5 offset:51200\n"
TR "v[182:183],v5 offset:57344\n"
TR "v[136:137],v2 offset:49152\n"
TR "v[138:139],v2 offset:55296\n"
TR "v[140:141],v3 offset:49152\n"
TR "v[142:143],v3 offset:55296\n"
TR "v[144:145],v4 offset:49152\n"
TR "v[146:147],v4 offset:55296\n"
TR "v[148:149],v5 offset:49152\n"
TR "v[150:151],v5 offset:55296\n"
TR "v[152:153],v2 offset:50176\n"
TR "v[154:155],v2 offset:56320\n"
TR "v[156:157],v3 offset:50176\n"
TR "v[158:159],v3 offset:56320\n"
"v_mov_b32 v52,v57\n"
"v_mov_b32 v53,v58\n"
"v_mov_b32 v54,v59\n"
"v_mov_b32 v55,v60\n"
"v_mov_b32 v56,v61\n"
"s_add_u32 m0,s73,24576\n"
"s_add_u32 s80,s89,s72\n"
LD "v192,s[48:51],s80 offen lds\n"
"v_add_u32 v193,9216,v192\n"
LD "v193,s[48:51],s80 offen offset:1024 lds\n"
"v_permlane16_swap_b32 v57,v52\n"
"v_permlane16_swap_b32 v58,v53\n"
"v_permlane16_swap_b32 v59,v54\n"
"v_permlane16_swap_b32 v60,v55\n"
"v_permlane16_swap_b32 v61,v56\n"
"v_add_f32 v57,v57,v52\n"
"v_add_f32 v58,v58,v53\n"
"v_add_f32 v59,v59,v54\n"
"v_add_f32 v60,v60,v55\n"
"v_add_f32 v61,v61,v56\n"
"v_mov_b32 v52,v57\n"
"v_mov_b32 v53,v58\n"
"v_mov_b32 v54,v59\n"
"v_mov_b32 v55,v60\n"
"v_mov_b32_e64 v56,v61\n"
"v_add_u32 v194,18432,v192\n"
LD "v194,s[48:51],s80 offen offset:2048 lds\n"
"v_add_u32 v195,27648,v192\n"
LD "v195,s[48:51],s80 offen offset:3072 lds\n"
"v_permlane32_swap_b32 v57,v52\n"
"v_permlane32_swap_b32 v58,v53\n"
"v_permlane32_swap_b32 v59,v54\n"
"v_permlane32_swap_b32 v60,v55\n"
"v_permlane32_swap_b32 v61,v56\n"
"v_add_f32 v57,v57,v52\n"
"v_add_f32 v58,v58,v53\n"
"v_add_f32 v59,v59,v54\n"
"v_add_f32 v60,v60,v55\n"
"v_add_f32 v61,v61,v56\n"
"s_waitcnt lgkmcnt(15)\n"
MF "a[120:123],v[160:163],v[92:95],a[120:123]\n"
MF "a[140:143],v[164:167],v[92:95],a[140:143]\n"
"v_rcp_f32 v208,v57\n"
"v_rcp_f32 v209,v58\n"
"v_rcp_f32 v210,v59\n"
"v_rcp_f32 v211,v60\n"
"v_rcp_f32 v212,v61\n"
MF "a[124:127],v[160:163],v[96:99],a[124:127]\n"
"v_mul_f32_e64 v214,-s13,v208\n"
"v_mul_f32_e64 v215,-s13,v209\n"
"v_mul_f32_e64 v216,-s13,v210\n"
"v_mul_f32_e64 v217,-s13,v211\n"
"v_mul_f32_e64 v218,-s13,v212\n"
"s_add_u32 m0,s73,28672\n"
"v_add_u32 v196,40960,v192\n"
LD "v196,s[48:51],s80 offen lds\n"
AR "v72,a120\n"
AR "v73,a121\n"
AR "v74,a122\n"
MF "a[144:147],v[164:167],v[96:99],a[144:147]\n"
AR "v75,a123\n"
AR "v76,a140\n"
AR "v77,a141\n"
AR "v78,a142\n"
AR "v79,a143\n"
"v_pk_fma_f32 v[72:73],v[72:73],v[208:209],v[214:215] op_sel:[0,0,0] op_sel_hi:[1,0,0]\n"
"v_pk_fma_f32 v[74:75],v[74:75],v[208:209],v[214:215] op_sel:[0,0,0] op_sel_hi:[1,0,0]\n"
"v_pk_fma_f32 v[76:77],v[76:77],v[208:209],v[214:215] op_sel:[0,0,0] op_sel_hi:[1,0,0]\n"
"v_pk_fma_f32 v[78:79],v[78:79],v[208:209],v[214:215] op_sel:[0,0,0] op_sel_hi:[1,0,0]\n"
CV "v112,v72,v73\n"
CV "v113,v74,v75\n"
CV "v114,v76,v77\n"
CV "v115,v78,v79\n"
MF "a[128:131],v[160:163],v[100:103],a[128:131]\n"
"ds_read_b128 a[140:143],v237 offset:20480\n"
AW "a120,v112\n"
AW "a121,v113\n"
AW "a122,v114\n"
AW "a123,v115\n"
"s_waitcnt lgkmcnt(7)\n"
MF "a[44:47],v[144:147],v[96:99],a[44:47]\n"
"v_add_u32 v197,50176,v192\n"
LD "v197,s[48:51],s80 offen offset:1024 lds\n"
AR "v80,a124\n"
AR "v81,a125\n"
AR "v82,a126\n"
MF "a[148:151],v[164:167],v[100:103],a[148:151]\n"
AR "v83,a127\n"
AR "v84,a144\n"
AR "v85,a145\n"
"s_nop 0\n"
AR "v86,a146\n"
AR "v87,a147\n"
"v_pk_fma_f32 v[80:81],v[80:81],v[208:209],v[214:215] op_sel:[0,1,1] op_sel_hi:[1,1,1]\n"
"v_pk_fma_f32 v[82:83],v[82:83],v[208:209],v[214:215] op_sel:[0,1,1] op_sel_hi:[1,1,1]\n"
"v_pk_fma_f32 v[84:85],v[84:85],v[208:209],v[214:215] op_sel:[0,1,1] op_sel_hi:[1,1,1]\n"
"v_pk_fma_f32 v[86:87],v[86:87],v[208:209],v[214:215] op_sel:[0,1,1] op_sel_hi:[1,1,1]\n"
CV "v116,v80,v81\n"
CV "v117,v82,v83\n"
CV "v118,v84,v85\n"
CV "v119,v86,v87\n"
MF "a[132:135],v[160:163],v[104:107],a[132:135]\n"
"ds_read_b128 a[144:147],v236\n"
AW "a124,v116\n"
AW "a125,v117\n"
AW "a126,v118\n"
AW "a127,v119\n"
"s_waitcnt lgkmcnt(6)\n"
MF "a[64:67],v[148:151],v[96:99],a[64:67]\n"
"s_add_u32 m0,s73,0\n"
"s_add_u32 s80,s88,s72\n"
"s_sub_u32 s72,s72,122880\n"
"s_nop 0\n"
LD "v192,s[48:51],s80 offen lds\n"
AR "v72,a128\n"
AR "v73,a129\n"
AR "v74,a130\n"
MF "a[152:155],v[164:167],v[104:107],a[152:155]\n"
AR "v75,a131\n"
AR "v76,a148\n"
AR "v77,a149\n"
AR "v78,a150\n"
AR "v79,a151\n"
"v_pk_fma_f32 v[72:73],v[72:73],v[210:211],v[216:217] op_sel:[0,0,0] op_sel_hi:[1,0,0]\n"
"v_pk_fma_f32 v[74:75],v[74:75],v[210:211],v[216:217] op_sel:[0,0,0] op_sel_hi:[1,0,0]\n"
"v_pk_fma_f32 v[76:77],v[76:77],v[210:211],v[216:217] op_sel:[0,0,0] op_sel_hi:[1,0,0]\n"
"v_pk_fma_f32 v[78:79],v[78:79],v[210:211],v[216:217] op_sel:[0,0,0] op_sel_hi:[1,0,0]\n"
LD "v193,s[48:51],s80 offen offset:1024 lds\n"
CV "v112,v72,v73\n"
CV "v113,v74,v75\n"
CV "v114,v76,v77\n"
CV "v115,v78,v79\n"
MF "a[136:139],v[160:163],v[108:111],a[136:139]\n"
"ds_read_b128 a[148:151],v236 offset:4096\n"
AW "a128,v112\n"
AW "a129,v113\n"
AW "a130,v114\n"
AW "a131,v115\n"
"s_waitcnt lgkmcnt(5)\n"
MF "a[84:87],v[152:155],v[96:99],a[84:87]\n"
LD "v194,s[48:51],s80 offen offset:2048 lds\n"
AR "v80,a132\n"
AR "v81,a133\n"
AR "v82,a134\n"
MF "a[156:159],v[164:167],v[108:111],a[156:159]\n"
AR "v83,a135\n"
AR "v84,a152\n"
AR "v85,a153\n"
"s_nop 0\n"
AR "v86,a154\n"
AR "v87,a155\n"
"v_pk_fma_f32 v[80:81],v[80:81],v[210:211],v[216:217] op_sel:[0,1,1] op_sel_hi:[1,1,1]\n"
"v_pk_fma_f32 v[82:83],v[82:83],v[210:211],v[216:217] op_sel:[0,1,1] op_sel_hi:[1,1,1]\n"
"v_pk_fma_f32 v[84:85],v[84:85],v[210:211],v[216:217] op_sel:[0,1,1] op_sel_hi:[1,1,1]\n"
"v_pk_fma_f32 v[86:87],v[86:87],v[210:211],v[216:217] op_sel:[0,1,1] op_sel_hi:[1,1,1]\n"
CV "v116,v80,v81\n"
CV "v117,v82,v83\n"
CV "v118,v84,v85\n"
CV "v119,v86,v87\n"
MF "a[160:163],v[168:171],v[92:95],a[160:163]\n"
"ds_read_b128 a[152:155],v236 offset:8192\n"
AW "a132,v116\n"
AW "a133,v117\n"
AW "a134,v118\n"
AW "a135,v119\n"
"s_waitcnt lgkmcnt(4)\n"
MF "a[104:107],v[156:159],v[96:99],a[104:107]\n"
LD "v195,s[48:51],s80 offen offset:3072 lds\n"
AR "v72,a136\n"
AR "v73,a137\n"
AR "v74,a138\n"
MF "a[180:183],v[172:175],v[92:95],a[180:183]\n"
AR "v75,a139\n"
AR "v76,a156\n"
AR "v77,a157\n"
"s_nop 0\n"
AR "v78,a158\n"
AR "v79,a159\n"
"v_pk_fma_f32 v[72:73],v[72:73],v[212:213],v[218:219] op_sel:[0,0,0] op_sel_hi:[1,0,0]\n"
"v_pk_fma_f32 v[74:75],v[74:75],v[212:213],v[218:219] op_sel:[0,0,0] op_sel_hi:[1,0,0]\n"
"v_pk_fma_f32 v[76:77],v[76:77],v[212:213],v[218:219] op_sel:[0,0,0] op_sel_hi:[1,0,0]\n"
"v_pk_fma_f32 v[78:79],v[78:79],v[212:213],v[218:219] op_sel:[0,0,0] op_sel_hi:[1,0,0]\n"
CV "v112,v72,v73\n"
CV "v113,v74,v75\n"
CV "v114,v76,v77\n"
CV "v115,v78,v79\n"
MF "a[164:167],v[168:171],v[96:99],a[164:167]\n"
"ds_read_b128 a[156:159],v236 offset:12288\n"
AW "a136,v112\n"
AW "a137,v113\n"
AW "a138,v114\n"
AW "a139,v115\n"
"v_max3_f32 v10,|v72|,|v73|,|v74|\n"
"s_lshl_b32 s58,s56,10\n"
"s_add_u32 s58,s58,159744\n"
"v_max3_f32 v10,v10,|v75|,|v76|\n"
"v_max3_f32 v10,v10,|v77|,|v78|\n"
"v_max3_f32 v10,v10,|v78|,|v79|\n"
"v_mul_f32 v10,s10,v10\n"
"v_lshrrev_b32_e64 v11,2,v9\n"
"v_readlane_b32 s12,v10,3\n"
"v_readlane_b32 s6,v10,22\n"
"v_readlane_b32 s7,v10,41\n"
"v_readlane_b32 s8,v10,60\n"
"v_add_u32 v11,s58,v11\n"
"s_min_u32 s12,s12,s6\n"
"s_min_u32 s7,s7,s8\n"
"s_min_u32 s12,s12,s7\n"
"s_and_b32 s12,s12,0x7f800000\n"
"s_sub_u32 s12,s12,0x14000000\n"
"s_cmp_lt_u32 s12,0x50000000\n"
"s_cselect_b32 s12,s12,0xe7400000\n"
"s_add_u32 s12,s12,0x18c00000\n"
"v_mov_b32 v10,s12\n"
"v_or_b32 v12,0x80000000,v10\n"
"ds_write2st64_b32 v11,v10,v12 offset1:1\n"
MF "a[8:11],v[136:139],v[100:103],a[8:11]\n"
"s_add_u32 m0,s73,4096\n"
"s_nop 0\n"
LD "v196,s[48:51],s80 offen lds\n"
AR "v80,a160\n"
AR "v81,a161\n"
AR "v82,a162\n"
MF "a[184:187],v[172:175],v[96:99],a[184:187]\n"
AR "v83,a163\n"
AR "v84,a180\n"
AR "v85,a181\n"
AR "v86,a182\n"
AR "v87,a183\n"
"v_pk_fma_f32 v[80:81],v[80:81],v[208:209],v[214:215] op_sel:[0,0,0] op_sel_hi:[1,0,0]\n"
"v_pk_fma_f32 v[82:83],v[82:83],v[208:209],v[214:215] op_sel:[0,0,0] op_sel_hi:[1,0,0]\n"
"v_pk_fma_f32 v[84:85],v[84:85],v[208:209],v[214:215] op_sel:[0,0,0] op_sel_hi:[1,0,0]\n"
"v_pk_fma_f32 v[86:87],v[86:87],v[208:209],v[214:215] op_sel:[0,0,0] op_sel_hi:[1,0,0]\n"
LD "v197,s[48:51],s80 offen offset:1024 lds\n"
CV "v116,v80,v81\n"
CV "v117,v82,v83\n"
CV "v118,v84,v85\n"
CV "v119,v86,v87\n"
MF "a[168:171],v[168:171],v[100:103],a[168:171]\n"
"ds_read_b128 a[180:183],v236 offset:16384\n"
AW "a160,v116\n"
AW "a161,v117\n"
AW "a162,v118\n"
AW "a163,v119\n"
MF "a[28:31],v[140:143],v[100:103],a[28:31]\n"
AR "v72,a164\n"
AR "v73,a165\n"
AR "v74,a166\n"
MF "a[188:191],v[172:175],v[100:103],a[188:191]\n"
AR "v75,a167\n"
AR "v76,a184\n"
AR "v77,a185\n"
AR "v78,a186\n"
AR "v79,a187\n"
"v_pk_fma_f32 v[72:73],v[72:73],v[208:209],v[214:215] op_sel:[0,1,1] op_sel_hi:[1,1,1]\n"
"v_pk_fma_f32 v[74:75],v[74:75],v[208:209],v[214:215] op_sel:[0,1,1] op_sel_hi:[1,1,1]\n"
"v_pk_fma_f32 v[76:77],v[76:77],v[208:209],v[214:215] op_sel:[0,1,1] op_sel_hi:[1,1,1]\n"
"v_pk_fma_f32 v[78:79],v[78:79],v[208:209],v[214:215] op_sel:[0,1,1] op_sel_hi:[1,1,1]\n"
CV "v112,v72,v73\n"
CV "v113,v74,v75\n"
CV "v114,v76,v77\n"
CV "v115,v78,v79\n"
MF "a[172:175],v[168:171],v[104:107],a[172:175]\n"
"ds_read_b128 a[184:187],v236 offset:20480\n"
AW "a164,v112\n"
AW "a165,v113\n"
AW "a166,v114\n"
AW "a167,v115\n"
MF "a[48:51],v[144:147],v[100:103],a[48:51]\n"
"s_waitcnt lgkmcnt(0)\n"
"s_barrier\n"
"s_add_u32 m0,s73,49152\n"
"s_add_u32 s80,s89,s72\n"
"s_nop 0\n"
LD "v192,s[48:51],s80 offen lds\n"
AR "v80,a168\n"
AR "v81,a169\n"
AR "v82,a170\n"
MF "a[192:195],v[172:175],v[104:107],a[192:195]\n"
AR "v83,a171\n"
AR "v84,a188\n"
AR "v85,a189\n"
AR "v86,a190\n"
AR "v87,a191\n"
"v_pk_fma_f32 v[80:81],v[80:81],v[210:211],v[216:217] op_sel:[0,0,0] op_sel_hi:[1,0,0]\n"
"v_pk_fma_f32 v[82:83],v[82:83],v[210:211],v[216:217] op_sel:[0,0,0] op_sel_hi:[1,0,0]\n"
"v_pk_fma_f32 v[84:85],v[84:85],v[210:211],v[216:217] op_sel:[0,0,0] op_sel_hi:[1,0,0]\n"
"v_pk_fma_f32 v[86:87],v[86:87],v[210:211],v[216:217] op_sel:[0,0,0] op_sel_hi:[1,0,0]\n"
LD "v193,s[48:51],s80 offen offset:1024 lds\n"
CV "v116,v80,v81\n"
CV "v117,v82,v83\n"
CV "v118,v84,v85\n"
CV "v119,v86,v87\n"
MF "a[176:179],v[168:171],v[108:111],a[176:179]\n"
"v_accvgpr_mov_b32 a188,a240\n"
"v_accvgpr_mov_b32 a189,a241\n"
"v_accvgpr_mov_b32 a190,a242\n"
"v_accvgpr_mov_b32 a191,a243\n"
AW "a168,v116\n"
AW "a169,v117\n"
AW "a170,v118\n"
AW "a171,v119\n"
MF "a[68:71],v[148:151],v[100:103],a[68:71]\n"
LD "v194,s[48:51],s80 offen offset:2048 lds\n"
AR "v72,a172\n"
AR "v73,a173\n"
AR "v74,a174\n"
MF "a[196:199],v[172:175],v[108:111],a[196:199]\n"
AR "v75,a175\n"
AR "v76,a192\n"
AR "v77,a193\n"
AR "v78,a194\n"
AR "v79,a195\n"
"v_pk_fma_f32 v[72:73],v[72:73],v[210:211],v[216:217] op_sel:[0,1,1] op_sel_hi:[1,1,1]\n"
"v_pk_fma_f32 v[74:75],v[74:75],v[210:211],v[216:217] op_sel:[0,1,1] op_sel_hi:[1,1,1]\n"
"v_pk_fma_f32 v[76:77],v[76:77],v[210:211],v[216:217] op_sel:[0,1,1] op_sel_hi:[1,1,1]\n"
"v_pk_fma_f32 v[78:79],v[78:79],v[210:211],v[216:217] op_sel:[0,1,1] op_sel_hi:[1,1,1]\n"
LD "v195,s[48:51],s80 offen offset:3072 lds\n"
CV "v112,v72,v73\n"
CV "v113,v74,v75\n"
CV "v114,v76,v77\n"
CV "v115,v78,v79\n"
MF "a[200:203],v[176:179],v[92:95],a[200:203]\n"
"v_accvgpr_mov_b32 a192,a244\n"
"v_accvgpr_mov_b32 a193,a245\n"
"v_accvgpr_mov_b32 a194,a246\n"
"v_accvgpr_mov_b32 a195,a247\n"
AW "a172,v112\n"
AW "a173,v113\n"
AW "a174,v114\n"
AW "a175,v115\n"
MF "a[88:91],v[152:155],v[100:103],a[88:91]\n"
"s_add_u32 m0,s73,53248\n"
"s_nop 0\n"
LD "v196,s[48:51],s80 offen lds\n"
AR "v80,a176\n"
AR "v81,a177\n"
AR "v82,a178\n"
MF "a[220:223],v[180:183],v[92:95],a[220:223]\n"
AR "v83,a179\n"
AR "v84,a196\n"
AR "v85,a197\n"
"s_nop 0\n"
AR "v86,a198\n"
AR "v87,a199\n"
"v_pk_fma_f32 v[80:81],v[80:81],v[212:213],v[218:219] op_sel:[0,0,0] op_sel_hi:[1,0,0]\n"
"v_pk_fma_f32 v[82:83],v[82:83],v[212:213],v[218:219] op_sel:[0,0,0] op_sel_hi:[1,0,0]\n"
"v_pk_fma_f32 v[84:85],v[84:85],v[212:213],v[218:219] op_sel:[0,0,0] op_sel_hi:[1,0,0]\n"
"v_pk_fma_f32 v[86:87],v[86:87],v[212:213],v[218:219] op_sel:[0,0,0] op_sel_hi:[1,0,0]\n"
CV "v116,v80,v81\n"
CV "v117,v82,v83\n"
CV "v118,v84,v85\n"
CV "v119,v86,v87\n"
MF "a[204:207],v[176:179],v[96:99],a[204:207]\n"
"v_accvgpr_mov_b32 a196,a248\n"
"v_accvgpr_mov_b32 a197,a249\n"
"v_accvgpr_mov_b32 a198,a250\n"
"v_accvgpr_mov_b32 a199,a251\n"
AW "a176,v116\n"
AW "a177,v117\n"
AW "a178,v118\n"
AW "a179,v119\n"
MF "a[108:111],v[156:159],v[100:103],a[108:111]\n"
LD "v197,s[48:51],s80 offen offset:1024 lds\n"
AR "v72,a200\n"
AR "v73,a201\n"
AR "v74,a202\n"
MF "a[224:227],v[180:183],v[96:99],a[224:227]\n"
AR "v75,a203\n"
AR "v76,a220\n"
AR "v77,a221\n"
AR "v78,a222\n"
AR "v79,a223\n"
"v_pk_fma_f32 v[72:73],v[72:73],v[208:209],v[214:215] op_sel:[0,0,0] op_sel_hi:[1,0,0]\n"
"v_pk_fma_f32 v[74:75],v[74:75],v[208:209],v[214:215] op_sel:[0,0,0] op_sel_hi:[1,0,0]\n"
"v_pk_fma_f32 v[76:77],v[76:77],v[208:209],v[214:215] op_sel:[0,0,0] op_sel_hi:[1,0,0]\n"
"v_pk_fma_f32 v[78:79],v[78:79],v[208:209],v[214:215] op_sel:[0,0,0] op_sel_hi:[1,0,0]\n"
CV "v112,v72,v73\n"
CV "v113,v74,v75\n"
CV "v114,v76,v77\n"
CV "v115,v78,v79\n"
MF "a[208:211],v[176:179],v[100:103],a[208:211]\n"
AW "a220,v62\n"
AW "a221,v63\n"
AW "a222,v64\n"
AW "a223,v65\n"
AW "a200,v112\n"
AW "a201,v113\n"
AW "a202,v114\n"
AW "a203,v115\n"
MF "a[12:15],v[136:139],v[104:107],a[12:15]\n"
"s_add_u32 m0,s73,73728\n"
"s_add_u32 s80,s88,s72\n"
LD "v192,s[48:51],s80 offen lds\n"
AR "v80,a204\n"
AR "v81,a205\n"
AR "v82,a206\n"
MF "a[228:231],v[180:183],v[100:103],a[228:231]\n"
AR "v83,a207\n"
AR "v84,a224\n"
AR "v85,a225\n"
"s_nop 0\n"
AR "v86,a226\n"
AR "v87,a227\n"
"v_pk_fma_f32 v[80:81],v[80:81],v[208:209],v[214:215] op_sel:[0,1,1] op_sel_hi:[1,1,1]\n"
"v_pk_fma_f32 v[82:83],v[82:83],v[208:209],v[214:215] op_sel:[0,1,1] op_sel_hi:[1,1,1]\n"
"v_pk_fma_f32 v[84:85],v[84:85],v[208:209],v[214:215] op_sel:[0,1,1] op_sel_hi:[1,1,1]\n"
"v_pk_fma_f32 v[86:87],v[86:87],v[208:209],v[214:215] op_sel:[0,1,1] op_sel_hi:[1,1,1]\n"
LD "v193,s[48:51],s80 offen offset:1024 lds\n"
CV "v116,v80,v81\n"
CV "v117,v82,v83\n"
CV "v118,v84,v85\n"
CV "v119,v86,v87\n"
MF "a[212:215],v[176:179],v[104:107],a[212:215]\n"
AW "a224,v66\n"
AW "a225,v67\n"
AW "a226,v68\n"
AW "a227,v69\n"
AW "a204,v116\n"
AW "a205,v117\n"
AW "a206,v118\n"
AW "a207,v119\n"
MF "a[32:35],v[140:143],v[104:107],a[32:35]\n"
LD "v194,s[48:51],s80 offen offset:2048 lds\n"
AR "v72,a208\n"
AR "v73,a209\n"
AR "v74,a210\n"
MF "a[232:235],v[180:183],v[104:107],a[232:235]\n"
AR "v75,a211\n"
AR "v76,a228\n"
AR "v77,a229\n"
AR "v78,a230\n"
AR "v79,a231\n"
"v_pk_fma_f32 v[72:73],v[72:73],v[210:211],v[216:217] op_sel:[0,0,0] op_sel_hi:[1,0,0]\n"
"v_pk_fma_f32 v[74:75],v[74:75],v[210:211],v[216:217] op_sel:[0,0,0] op_sel_hi:[1,0,0]\n"
"v_pk_fma_f32 v[76:77],v[76:77],v[210:211],v[216:217] op_sel:[0,0,0] op_sel_hi:[1,0,0]\n"
"v_pk_fma_f32 v[78:79],v[78:79],v[210:211],v[216:217] op_sel:[0,0,0] op_sel_hi:[1,0,0]\n"
CV "v112,v72,v73\n"
CV "v113,v74,v75\n"
CV "v114,v76,v77\n"
CV "v115,v78,v79\n"
MF "a[216:219],v[176:179],v[108:111],a[216:219]\n"
AW "a228,v70\n"
AW "a229,v71\n"
AW "a230,v132\n"
AW "a231,v133\n"
AW "a208,v112\n"
AW "a209,v113\n"
AW "a210,v114\n"
AW "a211,v115\n"
MF "a[52:55],v[144:147],v[104:107],a[52:55]\n"
LD "v195,s[48:51],s80 offen offset:3072 lds\n"
AR "v80,a212\n"
AR "v81,a213\n"
AR "v82,a214\n"
MF "a[236:239],v[180:183],v[108:111],a[236:239]\n"
AR "v83,a215\n"
AR "v84,a232\n"
AR "v85,a233\n"
AR "v86,a234\n"
AR "v87,a235\n"
"v_pk_fma_f32 v[80:81],v[80:81],v[210:211],v[216:217] op_sel:[0,1,1] op_sel_hi:[1,1,1]\n"
"v_pk_fma_f32 v[82:83],v[82:83],v[210:211],v[216:217] op_sel:[0,1,1] op_sel_hi:[1,1,1]\n"
"v_pk_fma_f32 v[84:85],v[84:85],v[210:211],v[216:217] op_sel:[0,1,1] op_sel_hi:[1,1,1]\n"
"v_pk_fma_f32 v[86:87],v[86:87],v[210:211],v[216:217] op_sel:[0,1,1] op_sel_hi:[1,1,1]\n"
CV "v116,v80,v81\n"
CV "v117,v82,v83\n"
CV "v118,v84,v85\n"
CV "v119,v86,v87\n"
MF "a[0:3],v[136:139],v[92:95],a[0:3]\n"
AW "a232,v134\n"
AW "a233,v135\n"
AW "a234,v6\n"
AW "a235,v13\n"
AW "a212,v116\n"
AW "a213,v117\n"
AW "a214,v118\n"
AW "a215,v119\n"
MF "a[72:75],v[148:151],v[104:107],a[72:75]\n"
"s_add_u32 m0,s73,77824\n"
"s_nop 0\n"
LD "v196,s[48:51],s80 offen lds\n"
AR "v72,a216\n"
AR "v73,a217\n"
AR "v74,a218\n"
MF "a[20:23],v[140:143],v[92:95],a[20:23]\n"
AR "v75,a219\n"
AR "v76,a236\n"
AR "v77,a237\n"
"s_nop 0\n"
AR "v78,a238\n"
AR "v79,a239\n"
"v_pk_fma_f32 v[72:73],v[72:73],v[212:213],v[218:219] op_sel:[0,0,0] op_sel_hi:[1,0,0]\n"
"v_pk_fma_f32 v[74:75],v[74:75],v[212:213],v[218:219] op_sel:[0,0,0] op_sel_hi:[1,0,0]\n"
"v_pk_fma_f32 v[76:77],v[76:77],v[212:213],v[218:219] op_sel:[0,0,0] op_sel_hi:[1,0,0]\n"
"v_pk_fma_f32 v[78:79],v[78:79],v[212:213],v[218:219] op_sel:[0,0,0] op_sel_hi:[1,0,0]\n"
LD "v197,s[48:51],s80 offen offset:1024 lds\n"
CV "v112,v72,v73\n"
CV "v113,v74,v75\n"
CV "v114,v76,v77\n"
CV "v115,v78,v79\n"
MF "a[40:43],v[144:147],v[92:95],a[40:43]\n"
AW "a236,v14\n"
AW "a237,v15\n"
"v_accvgpr_mov_b32 a238,a254\n"
"v_accvgpr_mov_b32 a239,a255\n"
AW "a216,v112\n"
AW "a217,v113\n"
AW "a218,v114\n"
AW "a219,v115\n"
MF "a[92:95],v[152:155],v[104:107],a[92:95]\n"
AR "v80,a0\n"
AR "v81,a1\n"
AR "v82,a2\n"
MF "a[60:63],v[148:151],v[92:95],a[60:63]\n"
AR "v83,a3\n"
AR "v84,a20\n"
AR "v85,a21\n"
AR "v86,a22\n"
AR "v87,a23\n"
"v_pk_fma_f32 v[80:81],v[80:81],v[208:209],v[214:215] op_sel:[0,0,0] op_sel_hi:[1,0,0]\n"
"v_pk_fma_f32 v[82:83],v[82:83],v[208:209],v[214:215] op_sel:[0,0,0] op_sel_hi:[1,0,0]\n"
"v_pk_fma_f32 v[84:85],v[84:85],v[208:209],v[214:215] op_sel:[0,0,0] op_sel_hi:[1,0,0]\n"
"v_pk_fma_f32 v[86:87],v[86:87],v[208:209],v[214:215] op_sel:[0,0,0] op_sel_hi:[1,0,0]\n"
CV "v116,v80,v81\n"
CV "v117,v82,v83\n"
CV "v118,v84,v85\n"
CV "v119,v86,v87\n"
MF "a[80:83],v[152:155],v[92:95],a[80:83]\n"
"ds_read_b128 a[20:23],v236 offset:24576\n"
AW "a0,v116\n"
AW "a1,v117\n"
AW "a2,v118\n"
AW "a3,v119\n"
MF "a[112:115],v[156:159],v[104:107],a[112:115]\n"
AR "v72,a40\n"
AR "v73,a41\n"
AR "v74,a42\n"
MF "a[100:103],v[156:159],v[92:95],a[100:103]\n"
AR "v75,a43\n"
AR "v76,a60\n"
AR "v77,a61\n"
AR "v78,a62\n"
AR "v79,a63\n"
"v_pk_fma_f32 v[72:73],v[72:73],v[208:209],v[214:215] op_sel:[0,0,0] op_sel_hi:[1,0,0]\n"
"v_pk_fma_f32 v[74:75],v[74:75],v[208:209],v[214:215] op_sel:[0,0,0] op_sel_hi:[1,0,0]\n"
"v_pk_fma_f32 v[76:77],v[76:77],v[208:209],v[214:215] op_sel:[0,0,0] op_sel_hi:[1,0,0]\n"
"v_pk_fma_f32 v[78:79],v[78:79],v[208:209],v[214:215] op_sel:[0,0,0] op_sel_hi:[1,0,0]\n"
CV "v112,v72,v73\n"
CV "v113,v74,v75\n"
CV "v114,v76,v77\n"
CV "v115,v78,v79\n"
MF "a[4:7],v[136:139],v[96:99],a[4:7]\n"
"ds_read_b128 a[60:63],v236 offset:45056\n"
AW "a40,v112\n"
AW "a41,v113\n"
AW "a42,v114\n"
AW "a43,v115\n"
MF "a[16:19],v[136:139],v[108:111],a[16:19]\n"
AR "v80,a80\n"
AR "v81,a81\n"
AR "v82,a82\n"
MF "a[24:27],v[140:143],v[96:99],a[24:27]\n"
AR "v83,a83\n"
AR "v84,a100\n"
AR "v85,a101\n"
AR "v86,a102\n"
AR "v87,a103\n"
"v_pk_fma_f32 v[80:81],v[80:81],v[208:209],v[214:215] op_sel:[0,0,0] op_sel_hi:[1,0,0]\n"
"v_pk_fma_f32 v[82:83],v[82:83],v[208:209],v[214:215] op_sel:[0,0,0] op_sel_hi:[1,0,0]\n"
"v_pk_fma_f32 v[84:85],v[84:85],v[208:209],v[214:215] op_sel:[0,0,0] op_sel_hi:[1,0,0]\n"
"v_pk_fma_f32 v[86:87],v[86:87],v[208:209],v[214:215] op_sel:[0,0,0] op_sel_hi:[1,0,0]\n"
CV "v116,v80,v81\n"
CV "v117,v82,v83\n"
CV "v118,v84,v85\n"
CV "v119,v86,v87\n"
MF "a[36:39],v[140:143],v[108:111],a[36:39]\n"
"ds_read_b128 a[100:103],v237\n"
AW "a80,v116\n"
AW "a81,v117\n"
AW "a82,v118\n"
AW "a83,v119\n"
MF "a[56:59],v[144:147],v[108:111],a[56:59]\n"
AR "v72,a4\n"
AR "v73,a5\n"
AR "v74,a6\n"
MF "a[76:79],v[148:151],v[108:111],a[76:79]\n"
AR "v75,a7\n"
AR "v76,a24\n"
AR "v77,a25\n"
AR "v78,a26\n"
AR "v79,a27\n"
"v_pk_fma_f32 v[72:73],v[72:73],v[208:209],v[214:215] op_sel:[0,1,1] op_sel_hi:[1,1,1]\n"
"v_pk_fma_f32 v[74:75],v[74:75],v[208:209],v[214:215] op_sel:[0,1,1] op_sel_hi:[1,1,1]\n"
"v_pk_fma_f32 v[76:77],v[76:77],v[208:209],v[214:215] op_sel:[0,1,1] op_sel_hi:[1,1,1]\n"
"v_pk_fma_f32 v[78:79],v[78:79],v[208:209],v[214:215] op_sel:[0,1,1] op_sel_hi:[1,1,1]\n"
CV "v112,v72,v73\n"
CV "v113,v74,v75\n"
CV "v114,v76,v77\n"
CV "v115,v78,v79\n"
MF "a[96:99],v[152:155],v[108:111],a[96:99]\n"
"ds_read_b128 a[24:27],v236 offset:28672\n"
AW "a4,v112\n"
AW "a5,v113\n"
AW "a6,v114\n"
AW "a7,v115\n"
MF "a[116:119],v[156:159],v[108:111],a[116:119]\n"
"s_mov_b32 s13,s11\n"
"v_and_b32 v0,512,v9\n"
"v_lshlrev_b32 v0,2,v0\n"
"v_sub_u32 v0,v2,v0\n"
"s_bfe_u32 s58,s22,0x10014\n"
"s_lshl_b32 s58,s58,5\n"
"v_xor_b32 v0,s58,v0\n"
"s_and_b32 s58,s56,1\n"
"s_lshl_b32 s58,s58,10\n"
"s_lshr_b32 s59,s56,1\n"
"s_lshl_b32 s59,s59,8\n"
"s_add_u32 s58,s58,s59\n"
"s_add_u32 s58,s58,159744\n"
"v_mov_b32 v252,s58\n"
"v_xor_b32 v1,32,v0\n"
"v_xor_b32_e64 v2,64,v0\n"
"v_xor_b32 v3,96,v0\n"
"v_add_u32 v4,73728,v0\n"
"v_add_u32 v5,73728,v1\n"
"v_add_u32 v6,73728,v2\n"
"v_add_u32 v7,73728,v3\n"
"s_waitcnt vmcnt(12) lgkmcnt(0)\n"
"s_barrier\n"
"ds_read_b128 v[252:255],v252\n"
TR "v[176:177],v0 offset:24576\n"
TR "v[178:179],v0 offset:26624\n"
TR "v[180:181],v0 offset:28672\n"
TR "v[182:183],v0 offset:30720\n"
TR "v[184:185],v0 offset:32768\n"
TR "v[186:187],v0 offset:34816\n"
"s_waitcnt lgkmcnt(0)\n"
"s_cmp_ge_u32 s56,2\n"
"s_cbranch_scc1 .Lrole1\n"
MF "v[16:19],a[120:123],v[176:179],v[252:255]\n"
"s_mov_b32 s65,128\n"
TR "v[188:189],v1 offset:24576\n"
AR "v220,a44\n"
MF "v[16:19],a[160:163],v[180:183],v[16:19]\n"
"s_mov_b32 s9,0x3f80\n"
TR "v[190:191],v1 offset:26624\n"
AR "v221,a45\n"
MF "v[16:19],a[200:203],v[184:187],v[16:19]\n"
"s_add_u32 s89,s89,s65\n"
AR "v222,a46\n"
AR "v223,a47\n"
MF "v[24:27],a[204:207],v[184:187],v[252:255]\n"
"s_bfe_u32 s58,s22,0x30014\n"
TR "a[240:241],v1 offset:28672\n"
AR "v224,a64\n"
MF "v[24:27],a[164:167],v[180:183],v[24:27]\n"
"s_add_u32 s61,s58,1\n"
TR "a[242:243],v1 offset:30720\n"
AR "v225,a65\n"
MF "v[24:27],a[124:127],v[176:179],v[24:27]\n"
"s_and_b32 s61,s61,7\n"
AR "v226,a66\n"
AR "v227,a67\n"
MF "v[32:35],a[128:131],v[176:179],v[252:255]\n"
"s_mov_b32 s64,9\n"
TR "a[244:245],v1 offset:32768\n"
"ds_read_b128 a[64:67],v236 offset:49152\n"
MF "v[32:35],a[168:171],v[180:183],v[32:35]\n"
"s_xor_b32 s67,s78,8\n"
TR "a[246:247],v1 offset:34816\n"
"v_fma_f32 v220,v220,v209,v215\n"
MF "v[32:35],a[208:211],v[184:187],v[32:35]\n"
"s_mov_b32 s71,204800\n"
"v_fma_f32 v221,v221,v209,v215\n"
"v_fma_f32 v222,v222,v209,v215\n"
MF "v[40:43],a[212:215],v[184:187],v[252:255]\n"
"s_mov_b32 s66,204800\n"
"v_fma_f32 v223,v223,v209,v215\n"
"v_fma_f32 v224,v224,v209,v215\n"
MF "v[40:43],a[172:175],v[180:183],v[40:43]\n"
"s_and_b32 s73,s56,1\n"
"v_fma_f32 v225,v225,v209,v215\n"
"v_fma_f32 v226,v226,v209,v215\n"
MF "v[40:43],a[132:135],v[176:179],v[40:43]\n"
"s_mul_i32 s73,s73,5120\n"
"v_fma_f32 v227,v227,v209,v215\n"
CV "v220,v220,v221\n"
MF "v[48:51],a[136:139],v[176:179],v[252:255]\n"
"s_add_u32 s73,s73,s93\n"
CV "v221,v222,v223\n"
CV "v222,v224,v225\n"
MF "v[48:51],a[176:179],v[180:183],v[48:51]\n"
"s_xor_b32 s58,s58,4\n"
CV "v223,v226,v227\n"
AW "a44,v220\n"
MF "v[48:51],a[216:219],v[184:187],v[48:51]\n"
"s_waitcnt lgkmcnt(0)\n"
AW "a45,v221\n"
AW "a46,v222\n"
MF "v[52:55],a[216:219],a[244:247],v[252:255]\n"
"s_mul_i32 s70,s58,204800\n"
TR "v[176:177],v2 offset:24576\n"
AW "a47,v223\n"
MF "v[52:55],a[176:179],a[240:243],v[52:55]\n"
"s_add_u32 s70,s70,s73\n"
TR "v[178:179],v2 offset:26624\n"
AR "v228,a84\n"
MF "v[52:55],a[136:139],v[188:191],v[52:55]\n"
"s_sub_u32 s70,s70,40960\n"
AR "v229,a85\n"
AR "v230,a86\n"
MF "v[44:47],a[132:135],v[188:191],v[252:255]\n"
"s_mov_b32 s85,1\n"
TR "v[180:181],v2 offset:28672\n"
AR "v231,a87\n"
MF "v[44:47],a[172:175],a[240:243],v[44:47]\n"
"s_mov_b32 s83,0xf0000000\n"
TR "v[182:183],v2 offset:30720\n"
AR "v232,a104\n"
MF "v[44:47],a[212:215],a[244:247],v[44:47]\n"
"s_mov_b32 s82,0xf0000000\n"
AR "v233,a105\n"
AR "v234,a106\n"
MF "v[36:39],a[208:211],a[244:247],v[252:255]\n"
"s_add_u32 s58,s79,73728\n"
TR "v[184:185],v2 offset:32768\n"
AR "v235,a107\n"
MF "v[36:39],a[168:171],a[240:243],v[36:39]\n"
"s_nop 0\n"
TR "v[186:187],v2 offset:34816\n"
"ds_read_b128 a[104:107],v237 offset:4096\n"
MF "v[36:39],a[128:131],v[188:191],v[36:39]\n"
"v_fma_f32 v228,v228,v209,v215\n"
"v_fma_f32 v229,v229,v209,v215\n"
MF "v[28:31],a[124:127],v[188:191],v[252:255]\n"
"v_fma_f32 v230,v230,v209,v215\n"
"v_fma_f32 v231,v231,v209,v215\n"
MF "v[28:31],a[164:167],a[240:243],v[28:31]\n"
"v_fma_f32 v232,v232,v209,v215\n"
"v_fma_f32 v233,v233,v209,v215\n"
MF "v[28:31],a[204:207],a[244:247],v[28:31]\n"
"v_fma_f32 v234,v234,v209,v215\n"
"v_fma_f32 v235,v235,v209,v215\n"
MF "v[20:23],a[200:203],a[244:247],v[252:255]\n"
CV "v228,v228,v229\n"
CV "v229,v230,v231\n"
MF "v[20:23],a[160:163],a[240:243],v[20:23]\n"
CV "v230,v232,v233\n"
CV "v231,v234,v235\n"
MF "v[20:23],a[120:123],v[188:191],v[20:23]\n"
"s_waitcnt lgkmcnt(1)\n"
AW "a84,v228\n"
AW "a85,v229\n"
MF "v[56:59],a[120:123],v[176:179],v[252:255]\n"
TR "v[188:189],v3 offset:24576\n"
AW "a86,v230\n"
MF "v[56:59],a[160:163],v[180:183],v[56:59]\n"
TR "v[190:191],v3 offset:26624\n"
AW "a87,v231\n"
MF "v[56:59],a[200:203],v[184:187],v[56:59]\n"
AR "v220,a8\n"
AR "v221,a9\n"
MF "v[64:67],a[204:207],v[184:187],v[252:255]\n"
TR "a[240:241],v3 offset:28672\n"
AR "v222,a10\n"
MF "v[64:67],a[164:167],v[180:183],v[64:67]\n"
TR "a[242:243],v3 offset:30720\n"
AR "v223,a11\n"
MF "v[64:67],a[124:127],v[176:179],v[64:67]\n"
AR "v224,a28\n"
AR "v225,a29\n"
MF "v[72:75],a[128:131],v[176:179],v[252:255]\n"
TR "a[244:245],v3 offset:32768\n"
AR "v226,a30\n"
MF "v[72:75],a[168:171],v[180:183],v[72:75]\n"
TR "a[246:247],v3 offset:34816\n"
AR "v227,a31\n"
MF "v[72:75],a[208:211],v[184:187],v[72:75]\n"
"ds_read_b128 a[28:31],v236 offset:32768\n"
"v_fma_f32 v220,v220,v210,v216\n"
MF "v[80:83],a[212:215],v[184:187],v[252:255]\n"
"v_fma_f32 v221,v221,v210,v216\n"
"v_fma_f32 v222,v222,v210,v216\n"
MF "v[80:83],a[172:175],v[180:183],v[80:83]\n"
"v_fma_f32 v223,v223,v210,v216\n"
"v_fma_f32 v224,v224,v210,v216\n"
MF "v[80:83],a[132:135],v[176:179],v[80:83]\n"
"v_fma_f32 v225,v225,v210,v216\n"
"v_fma_f32 v226,v226,v210,v216\n"
MF "v[88:91],a[136:139],v[176:179],v[252:255]\n"
"v_fma_f32 v227,v227,v210,v216\n"
CV "v220,v220,v221\n"
MF "v[88:91],a[176:179],v[180:183],v[88:91]\n"
CV "v221,v222,v223\n"
CV "v222,v224,v225\n"
MF "v[88:91],a[216:219],v[184:187],v[88:91]\n"
"s_waitcnt vmcnt(0) lgkmcnt(0)\n"
"s_barrier\n"
MF "v[92:95],a[216:219],a[244:247],v[252:255]\n"
TR "v[176:177],v0\n"
CV "v223,v226,v227\n"
MF "v[92:95],a[176:179],a[240:243],v[92:95]\n"
TR "v[178:179],v0 offset:2048\n"
AW "a8,v220\n"
MF "v[92:95],a[136:139],v[188:191],v[92:95]\n"
TR "v[96:97],v0 offset:49152\n"
AW "a9,v221\n"
MF "v[84:87],a[132:135],v[188:191],v[252:255]\n"
TR "v[180:181],v0 offset:4096\n"
AW "a10,v222\n"
MF "v[84:87],a[172:175],a[240:243],v[84:87]\n"
TR "v[182:183],v0 offset:6144\n"
AW "a11,v223\n"
MF "v[84:87],a[212:215],a[244:247],v[84:87]\n"
TR "v[98:99],v0 offset:51200\n"
AR "v228,a48\n"
MF "v[76:79],a[208:211],a[244:247],v[252:255]\n"
TR "v[184:185],v0 offset:8192\n"
AR "v229,a49\n"
MF "v[76:79],a[168:171],a[240:243],v[76:79]\n"
TR "v[186:187],v0 offset:10240\n"
AR "v230,a50\n"
MF "v[76:79],a[128:131],v[188:191],v[76:79]\n"
TR "v[100:101],v0 offset:53248\n"
AR "v231,a51\n"
MF "v[68:71],a[124:127],v[188:191],v[252:255]\n"
TR "v[102:103],v0 offset:55296\n"
AR "v232,a68\n"
MF "v[68:71],a[164:167],a[240:243],v[68:71]\n"
TR "v[104:105],v0 offset:57344\n"
AR "v233,a69\n"
MF "v[68:71],a[204:207],a[244:247],v[68:71]\n"
TR "v[106:107],v0 offset:59392\n"
AR "v234,a70\n"
MF "v[60:63],a[200:203],a[244:247],v[252:255]\n"
TR "v[108:109],v4\n"
AR "v235,a71\n"
MF "v[60:63],a[160:163],a[240:243],v[60:63]\n"
"s_nop 0\n"
TR "v[110:111],v4 offset:2048\n"
"ds_read_b128 a[68:71],v236 offset:53248\n"
MF "v[60:63],a[120:123],v[188:191],v[60:63]\n"
"s_waitcnt lgkmcnt(7)\n"
TR "v[112:113],v4 offset:4096\n"
"v_fma_f32 v228,v228,v210,v216\n"
MF "v[16:19],a[140:143],v[176:179],v[16:19]\n"
TR "v[188:189],v1\n"
"v_fma_f32 v229,v229,v210,v216\n"
MF "v[16:19],a[180:183],v[180:183],v[16:19]\n"
TR "v[190:191],v1 offset:2048\n"
"v_fma_f32 v230,v230,v210,v216\n"
MF "v[16:19],a[220:223],v[184:187],v[16:19]\n"
TR "v[114:115],v4 offset:6144\n"
"v_fma_f32 v231,v231,v210,v216\n"
MF "v[24:27],a[224:227],v[184:187],v[24:27]\n"
TR "a[240:241],v1 offset:4096\n"
"v_fma_f32 v232,v232,v210,v216\n"
MF "v[24:27],a[184:187],v[180:183],v[24:27]\n"
TR "a[242:243],v1 offset:6144\n"
"v_fma_f32 v233,v233,v210,v216\n"
MF "v[24:27],a[144:147],v[176:179],v[24:27]\n"
TR "v[116:117],v4 offset:8192\n"
"v_fma_f32 v234,v234,v210,v216\n"
MF "v[32:35],a[148:151],v[176:179],v[32:35]\n"
TR "a[244:245],v1 offset:8192\n"
"v_fma_f32 v235,v235,v210,v216\n"
MF "v[32:35],a[188:191],v[180:183],v[32:35]\n"
TR "a[246:247],v1 offset:10240\n"
CV "v228,v228,v229\n"
MF "v[32:35],a[228:231],v[184:187],v[32:35]\n"
TR "v[118:119],v4 offset:10240\n"
CV "v229,v230,v231\n"
MF "v[40:43],a[232:235],v[184:187],v[40:43]\n"
TR "v[120:121],v1 offset:49152\n"
CV "v230,v232,v233\n"
MF "v[40:43],a[192:195],v[180:183],v[40:43]\n"
TR "v[122:123],v1 offset:51200\n"
CV "v231,v234,v235\n"
MF "v[40:43],a[152:155],v[176:179],v[40:43]\n"
TR "v[124:125],v1 offset:53248\n"
AW "a48,v228\n"
MF "v[48:51],a[156:159],v[176:179],v[48:51]\n"
TR "v[126:127],v1 offset:55296\n"
AW "a49,v229\n"
MF "v[48:51],a[196:199],v[180:183],v[48:51]\n"
TR "v[128:129],v1 offset:57344\n"
AW "a50,v230\n"
MF "v[48:51],a[236:239],v[184:187],v[48:51]\n"
"s_waitcnt lgkmcnt(6)\n"
TR "v[130:131],v1 offset:59392\n"
AW "a51,v231\n"
MF "v[52:55],a[236:239],a[244:247],v[52:55]\n"
TR "v[176:177],v2\n"
AR "v220,a88\n"
MF "v[52:55],a[196:199],a[240:243],v[52:55]\n"
TR "v[178:179],v2 offset:2048\n"
AR "v221,a89\n"
MF "v[52:55],a[156:159],v[188:191],v[52:55]\n"
TR "v[132:133],v5\n"
AR "v222,a90\n"
MF "v[44:47],a[152:155],v[188:191],v[44:47]\n"
TR "v[180:181],v2 offset:4096\n"
AR "v223,a91\n"
MF "v[44:47],a[192:195],a[240:243],v[44:47]\n"
TR "v[182:183],v2 offset:6144\n"
AR "v224,a108\n"
MF "v[44:47],a[232:235],a[244:247],v[44:47]\n"
TR "v[134:135],v5 offset:2048\n"
AR "v225,a109\n"
MF "v[36:39],a[228:231],a[244:247],v[36:39]\n"
TR "v[184:185],v2 offset:8192\n"
AR "v226,a110\n"
MF "v[36:39],a[188:191],a[240:243],v[36:39]\n"
TR "v[186:187],v2 offset:10240\n"
AR "v227,a111\n"
MF "v[36:39],a[148:151],v[188:191],v[36:39]\n"
"s_nop 0\n"
TR "v[136:137],v5 offset:4096\n"
"ds_read_b128 a[108:111],v237 offset:8192\n"
MF "v[28:31],a[144:147],v[188:191],v[28:31]\n"
TR "v[138:139],v5 offset:6144\n"
"v_fma_f32 v220,v220,v210,v216\n"
MF "v[28:31],a[184:187],a[240:243],v[28:31]\n"
TR "v[140:141],v5 offset:8192\n"
"v_fma_f32 v221,v221,v210,v216\n"
MF "v[28:31],a[224:227],a[244:247],v[28:31]\n"
TR "v[142:143],v5 offset:10240\n"
"v_fma_f32 v222,v222,v210,v216\n"
MF "v[20:23],a[220:223],a[244:247],v[20:23]\n"
TR "v[144:145],v2 offset:49152\n"
"v_fma_f32 v223,v223,v210,v216\n"
MF "v[20:23],a[180:183],a[240:243],v[20:23]\n"
TR "v[146:147],v2 offset:51200\n"
"v_fma_f32 v224,v224,v210,v216\n"
MF "v[20:23],a[140:143],v[188:191],v[20:23]\n"
"s_waitcnt lgkmcnt(7)\n"
TR "v[148:149],v2 offset:53248\n"
"v_fma_f32 v225,v225,v210,v216\n"
MF "v[56:59],a[140:143],v[176:179],v[56:59]\n"
TR "v[188:189],v3\n"
"v_add_u32 v198,59392,v192\n"
MF "v[56:59],a[180:183],v[180:183],v[56:59]\n"
TR "v[190:191],v3 offset:2048\n"
"v_add_u32 v199,68608,v192\n"
MF "v[56:59],a[220:223],v[184:187],v[56:59]\n"
TR "v[150:151],v2 offset:55296\n"
"v_add_u32 v200,81920,v192\n"
"v_add_u32 v201,91136,v192\n"
MF "v[64:67],a[224:227],v[184:187],v[64:67]\n"
TR "a[240:241],v3 offset:4096\n"
"v_add_u32 v202,100352,v192\n"
MF "v[64:67],a[184:187],v[180:183],v[64:67]\n"
TR "a[242:243],v3 offset:6144\n"
"v_add_u32 v203,109568,v192\n"
MF "v[64:67],a[144:147],v[176:179],v[64:67]\n"
TR "v[152:153],v2 offset:57344\n"
"v_fma_f32 v226,v226,v210,v216\n"
MF "v[72:75],a[148:151],v[176:179],v[72:75]\n"
TR "a[244:245],v3 offset:8192\n"
"v_fma_f32 v227,v227,v210,v216\n"
MF "v[72:75],a[188:191],v[180:183],v[72:75]\n"
TR "a[246:247],v3 offset:10240\n"
CV "v220,v220,v221\n"
MF "v[72:75],a[228:231],v[184:187],v[72:75]\n"
TR "v[154:155],v2 offset:59392\n"
CV "v221,v222,v223\n"
MF "v[80:83],a[232:235],v[184:187],v[80:83]\n"
TR "v[156:157],v6\n"
CV "v222,v224,v225\n"
MF "v[80:83],a[192:195],v[180:183],v[80:83]\n"
TR "v[158:159],v6 offset:2048\n"
CV "v223,v226,v227\n"
MF "v[80:83],a[152:155],v[176:179],v[80:83]\n"
TR "v[160:161],v6 offset:4096\n"
AW "a88,v220\n"
MF "v[88:91],a[156:159],v[176:179],v[88:91]\n"
TR "v[162:163],v6 offset:6144\n"
AW "a89,v221\n"
MF "v[88:91],a[196:199],v[180:183],v[88:91]\n"
TR "v[164:165],v6 offset:8192\n"
AW "a90,v222\n"
MF "v[88:91],a[236:239],v[184:187],v[88:91]\n"
"s_waitcnt lgkmcnt(0)\n"
"s_barrier\n"
MF "v[92:95],a[236:239],a[244:247],v[92:95]\n"
"s_add_u32 s80,s89,122880\n"
"s_add_u32 m0,s79,0\n"
TR "v[166:167],v6 offset:10240\n"
MF "v[92:95],a[196:199],a[240:243],v[92:95]\n"
LD "v192,s[48:51],s80 offen lds\n"
MF "v[92:95],a[156:159],v[188:191],v[92:95]\n"
TR "v[168:169],v3 offset:49152\n"
AW "a91,v223\n"
MF "v[84:87],a[152:155],v[188:191],v[84:87]\n"
TR "v[170:171],v3 offset:51200\n"
AR "v228,a12\n"
MF "v[84:87],a[192:195],a[240:243],v[84:87]\n"
TR "v[172:173],v3 offset:53248\n"
AR "v229,a13\n"
MF "v[84:87],a[232:235],a[244:247],v[84:87]\n"
LD "v193,s[48:51],s80 offen offset:1024 lds\n"
MF "v[76:79],a[228:231],a[244:247],v[76:79]\n"
TR "v[174:175],v3 offset:55296\n"
AR "v230,a14\n"
MF "v[76:79],a[188:191],a[240:243],v[76:79]\n"
TR "v[204:205],v3 offset:57344\n"
AR "v231,a15\n"
MF "v[76:79],a[148:151],v[188:191],v[76:79]\n"
TR "v[206:207],v3 offset:59392\n"
AR "v232,a32\n"
MF "v[68:71],a[144:147],v[188:191],v[68:71]\n"
TR "v[244:245],v7\n"
AR "v233,a33\n"
MF "v[68:71],a[184:187],a[240:243],v[68:71]\n"
LD "v194,s[48:51],s80 offen offset:2048 lds\n"
MF "v[68:71],a[224:227],a[244:247],v[68:71]\n"
TR "v[246:247],v7 offset:2048\n"
AR "v234,a34\n"
MF "v[60:63],a[220:223],a[244:247],v[60:63]\n"
TR "v[248:249],v7 offset:4096\n"
AR "v235,a35\n"
MF "v[60:63],a[180:183],a[240:243],v[60:63]\n"
"s_nop 0\n"
TR "v[250:251],v7 offset:6144\n"
"ds_read_b128 a[32:35],v236 offset:36864\n"
MF "v[60:63],a[140:143],v[188:191],v[60:63]\n"
LD "v195,s[48:51],s80 offen offset:3072 lds\n"
MF "v[16:19],a[0:3],v[96:99],v[16:19]\n"
TR "v[238:239],v7 offset:8192\n"
"v_fma_f32 v228,v228,v211,v217\n"
MF "v[16:19],a[40:43],v[100:103],v[16:19]\n"
TR "v[240:241],v7 offset:10240\n"
"v_fma_f32 v229,v229,v211,v217\n"
MF "v[16:19],a[80:83],v[104:107],v[16:19]\n"
"v_fma_f32 v230,v230,v211,v217\n"
"v_fma_f32 v231,v231,v211,v217\n"
MF "v[16:19],a[20:23],v[108:111],v[16:19]\n"
"s_add_u32 m0,s79,4096\n"
"v_fma_f32 v232,v232,v211,v217\n"
"v_fma_f32 v233,v233,v211,v217\n"
MF "v[16:19],a[60:63],v[112:115],v[16:19]\n"
LD "v196,s[48:51],s80 offen lds\n"
MF "v[16:19],a[100:103],v[116:119],v[16:19]\n"
"v_fma_f32 v234,v234,v211,v217\n"
"v_fma_f32 v235,v235,v211,v217\n"
MF "v[20:23],a[100:103],v[140:143],v[20:23]\n"
CV "v228,v228,v229\n"
CV "v229,v230,v231\n"
MF "v[20:23],a[60:63],v[136:139],v[20:23]\n"
CV "v230,v232,v233\n"
CV "v231,v234,v235\n"
MF "v[20:23],a[20:23],v[132:135],v[20:23]\n"
LD "v197,s[48:51],s80 offen offset:1024 lds\n"
MF "v[20:23],a[80:83],v[128:131],v[20:23]\n"
AW "a12,v228\n"
AW "a13,v229\n"
MF "v[20:23],a[40:43],v[124:127],v[20:23]\n"
AW "a14,v230\n"
AW "a15,v231\n"
MF "v[20:23],a[0:3],v[120:123],v[20:23]\n"
"s_waitcnt lgkmcnt(13)\n"
AR "v220,a52\n"
AR "v221,a53\n"
MF "v[56:59],a[0:3],v[144:147],v[56:59]\n"
AR "v222,a54\n"
AR "v223,a55\n"
MF "v[56:59],a[40:43],v[148:151],v[56:59]\n"
LD "v198,s[48:51],s80 offen offset:2048 lds\n"
MF "v[56:59],a[80:83],v[152:155],v[56:59]\n"
AR "v224,a72\n"
AR "v225,a73\n"
MF "v[56:59],a[20:23],v[156:159],v[56:59]\n"
AR "v226,a74\n"
AR "v227,a75\n"
MF "v[56:59],a[60:63],v[160:163],v[56:59]\n"
"ds_read_b128 a[72:75],v236 offset:57344\n"
"v_fma_f32 v220,v220,v211,v217\n"
MF "v[56:59],a[100:103],v[164:167],v[56:59]\n"
"s_waitcnt lgkmcnt(1)\n"
"v_fma_f32 v221,v221,v211,v217\n"
"v_fma_f32 v222,v222,v211,v217\n"
MF "v[60:63],a[100:103],v[238:241],v[60:63]\n"
LD "v199,s[48:51],s80 offen offset:3072 lds\n"
MF "v[60:63],a[60:63],v[248:251],v[60:63]\n"
"v_fma_f32 v223,v223,v211,v217\n"
"v_fma_f32 v224,v224,v211,v217\n"
MF "v[60:63],a[20:23],v[244:247],v[60:63]\n"
"v_fma_f32 v225,v225,v211,v217\n"
"v_fma_f32 v226,v226,v211,v217\n"
MF "v[60:63],a[80:83],v[204:207],v[60:63]\n"
"s_add_u32 m0,s79,8192\n"
"v_fma_f32 v227,v227,v211,v217\n"
CV "v220,v220,v221\n"
MF "v[60:63],a[40:43],v[172:175],v[60:63]\n"
LD "v200,s[48:51],s80 offen lds\n"
MF "v[60:63],a[0:3],v[168:171],v[60:63]\n"
CV "v221,v222,v223\n"
CV "v222,v224,v225\n"
MF "v[68:71],a[4:7],v[168:171],v[68:71]\n"
CV "v223,v226,v227\n"
AW "a52,v220\n"
MF "v[68:71],a[44:47],v[172:175],v[68:71]\n"
AW "a53,v221\n"
AW "a54,v222\n"
MF "v[68:71],a[84:87],v[204:207],v[68:71]\n"
AW "a55,v223\n"
AR "v228,a92\n"
MF "v[68:71],a[24:27],v[244:247],v[68:71]\n"
LD "v201,s[48:51],s80 offen offset:1024 lds\n"
MF "v[68:71],a[64:67],v[248:251],v[68:71]\n"
AR "v229,a93\n"
AR "v230,a94\n"
MF "v[68:71],a[104:107],v[238:241],v[68:71]\n"
AR "v231,a95\n"
AR "v232,a112\n"
MF "v[64:67],a[104:107],v[164:167],v[64:67]\n"
"s_waitcnt lgkmcnt(0)\n"
"s_barrier\n"
MF "v[64:67],a[64:67],v[160:163],v[64:67]\n"
LD "v202,s[48:51],s80 offen offset:2048 lds\n"
MF "v[64:67],a[24:27],v[156:159],v[64:67]\n"
AR "v233,a113\n"
AR "v234,a114\n"
MF "v[64:67],a[84:87],v[152:155],v[64:67]\n"
AR "v235,a115\n"
"v_fma_f32 v228,v228,v211,v217\n"
MF "v[64:67],a[44:47],v[148:151],v[64:67]\n"
"ds_read_b128 a[112:115],v237 offset:12288\n"
"v_fma_f32 v229,v229,v211,v217\n"
MF "v[64:67],a[4:7],v[144:147],v[64:67]\n"
"v_fma_f32 v230,v230,v211,v217\n"
"v_fma_f32 v231,v231,v211,v217\n"
MF "v[28:31],a[4:7],v[120:123],v[28:31]\n"
LD "v203,s[48:51],s80 offen offset:3072 lds\n"
MF "v[28:31],a[44:47],v[124:127],v[28:31]\n"
"v_fma_f32 v232,v232,v211,v217\n"
"v_fma_f32 v233,v233,v211,v217\n"
MF "v[28:31],a[84:87],v[128:131],v[28:31]\n"
"v_fma_f32 v234,v234,v211,v217\n"
"v_fma_f32 v235,v235,v211,v217\n"
MF "v[28:31],a[24:27],v[132:135],v[28:31]\n"
CV "v228,v228,v229\n"
CV "v229,v230,v231\n"
MF "v[28:31],a[64:67],v[136:139],v[28:31]\n"
CV "v230,v232,v233\n"
CV "v231,v234,v235\n"
MF "v[28:31],a[104:107],v[140:143],v[28:31]\n"
"s_add_u32 s88,s88,s65\n"
"s_add_u32 m0,s79,49152\n"
AW "a92,v228\n"
MF "v[24:27],a[104:107],v[116:119],v[24:27]\n"
LD "v192,s[48:51],s88 offen lds\n"
MF "v[24:27],a[64:67],v[112:115],v[24:27]\n"
AW "a93,v229\n"
AW "a94,v230\n"
MF "v[24:27],a[24:27],v[108:111],v[24:27]\n"
AW "a95,v231\n"
AR "v220,a16\n"
MF "v[24:27],a[84:87],v[104:107],v[24:27]\n"
AR "v221,a17\n"
AR "v222,a18\n"
MF "v[24:27],a[44:47],v[100:103],v[24:27]\n"
LD "v193,s[48:51],s88 offen offset:1024 lds\n"
MF "v[24:27],a[4:7],v[96:99],v[24:27]\n"
AR "v223,a19\n"
AR "v224,a36\n"
MF "v[32:35],a[8:11],v[96:99],v[32:35]\n"
AR "v225,a37\n"
AR "v226,a38\n"
MF "v[32:35],a[48:51],v[100:103],v[32:35]\n"
AR "v227,a39\n"
"v_fma_f32 v220,v220,v212,v218\n"
MF "v[32:35],a[88:91],v[104:107],v[32:35]\n"
"ds_read_b128 a[36:39],v236 offset:40960\n"
"v_fma_f32 v221,v221,v212,v218\n"
MF "v[32:35],a[28:31],v[108:111],v[32:35]\n"
LD "v194,s[48:51],s88 offen offset:2048 lds\n"
MF "v[32:35],a[68:71],v[112:115],v[32:35]\n"
"v_fma_f32 v222,v222,v212,v218\n"
"v_fma_f32 v223,v223,v212,v218\n"
MF "v[32:35],a[108:111],v[116:119],v[32:35]\n"
"v_fma_f32 v224,v224,v212,v218\n"
"v_fma_f32 v225,v225,v212,v218\n"
MF "v[36:39],a[108:111],v[140:143],v[36:39]\n"
"v_fma_f32 v226,v226,v212,v218\n"
"v_fma_f32 v227,v227,v212,v218\n"
MF "v[36:39],a[68:71],v[136:139],v[36:39]\n"
CV "v220,v220,v221\n"
CV "v221,v222,v223\n"
MF "v[36:39],a[28:31],v[132:135],v[36:39]\n"
LD "v195,s[48:51],s88 offen offset:3072 lds\n"
MF "v[36:39],a[88:91],v[128:131],v[36:39]\n"
CV "v222,v224,v225\n"
CV "v223,v226,v227\n"
MF "v[36:39],a[48:51],v[124:127],v[36:39]\n"
AW "a16,v220\n"
AW "a17,v221\n"
MF "v[36:39],a[8:11],v[120:123],v[36:39]\n"
AW "a18,v222\n"
AW "a19,v223\n"
MF "v[72:75],a[8:11],v[144:147],v[72:75]\n"
"s_add_u32 m0,s79,53248\n"
AR "v228,a56\n"
AR "v229,a57\n"
MF "v[72:75],a[48:51],v[148:151],v[72:75]\n"
LD "v196,s[48:51],s88 offen lds\n"
MF "v[72:75],a[88:91],v[152:155],v[72:75]\n"
AR "v230,a58\n"
AR "v231,a59\n"
MF "v[72:75],a[28:31],v[156:159],v[72:75]\n"
AR "v232,a76\n"
AR "v233,a77\n"
MF "v[72:75],a[68:71],v[160:163],v[72:75]\n"
AR "v234,a78\n"
AR "v235,a79\n"
MF "v[72:75],a[108:111],v[164:167],v[72:75]\n"
"ds_read_b128 a[76:79],v236 offset:61440\n"
"v_fma_f32 v228,v228,v212,v218\n"
MF "v[76:79],a[108:111],v[238:241],v[76:79]\n"
LD "v197,s[48:51],s88 offen offset:1024 lds\n"
MF "v[76:79],a[68:71],v[248:251],v[76:79]\n"
"v_fma_f32 v229,v229,v212,v218\n"
"v_fma_f32 v230,v230,v212,v218\n"
MF "v[76:79],a[28:31],v[244:247],v[76:79]\n"
"v_fma_f32 v231,v231,v212,v218\n"
"v_fma_f32 v232,v232,v212,v218\n"
MF "v[76:79],a[88:91],v[204:207],v[76:79]\n"
"v_fma_f32 v233,v233,v212,v218\n"
"v_fma_f32 v234,v234,v212,v218\n"
MF "v[76:79],a[48:51],v[172:175],v[76:79]\n"
"v_fma_f32 v235,v235,v212,v218\n"
CV "v228,v228,v229\n"
MF "v[76:79],a[8:11],v[168:171],v[76:79]\n"
"s_waitcnt lgkmcnt(2)\n"
LD "v198,s[48:51],s88 offen offset:2048 lds\n"
MF "v[84:87],a[12:15],v[168:171],v[84:87]\n"
CV "v229,v230,v231\n"
CV "v230,v232,v233\n"
MF "v[84:87],a[52:55],v[172:175],v[84:87]\n"
CV "v231,v234,v235\n"
AW "a56,v228\n"
MF "v[84:87],a[92:95],v[204:207],v[84:87]\n"
AW "a57,v229\n"
AW "a58,v230\n"
MF "v[84:87],a[32:35],v[244:247],v[84:87]\n"
AW "a59,v231\n"
AR "v220,a96\n"
MF "v[84:87],a[72:75],v[248:251],v[84:87]\n"
LD "v199,s[48:51],s88 offen offset:3072 lds\n"
MF "v[84:87],a[112:115],v[238:241],v[84:87]\n"
AR "v221,a97\n"
AR "v222,a98\n"
MF "v[80:83],a[112:115],v[164:167],v[80:83]\n"
AR "v223,a99\n"
AR "v224,a116\n"
MF "v[80:83],a[72:75],v[160:163],v[80:83]\n"
AR "v225,a117\n"
AR "v226,a118\n"
MF "v[80:83],a[32:35],v[156:159],v[80:83]\n"
"s_add_u32 m0,s79,57344\n"
AR "v227,a119\n"
"v_fma_f32 v220,v220,v212,v218\n"
MF "v[80:83],a[92:95],v[152:155],v[80:83]\n"
LD "v200,s[48:51],s88 offen lds\n"
MF "v[80:83],a[52:55],v[148:151],v[80:83]\n"
"ds_read_b128 a[116:119],v237 offset:16384\n"
"v_fma_f32 v221,v221,v212,v218\n"
MF "v[80:83],a[12:15],v[144:147],v[80:83]\n"
"v_fma_f32 v222,v222,v212,v218\n"
"v_fma_f32 v223,v223,v212,v218\n"
MF "v[44:47],a[12:15],v[120:123],v[44:47]\n"
"v_fma_f32 v224,v224,v212,v218\n"
"v_fma_f32 v225,v225,v212,v218\n"
MF "v[44:47],a[52:55],v[124:127],v[44:47]\n"
"v_fma_f32 v226,v226,v212,v218\n"
"v_fma_f32 v227,v227,v212,v218\n"
MF "v[44:47],a[92:95],v[128:131],v[44:47]\n"
LD "v201,s[48:51],s88 offen offset:1024 lds\n"
MF "v[44:47],a[32:35],v[132:135],v[44:47]\n"
CV "v220,v220,v221\n"
CV "v221,v222,v223\n"
MF "v[44:47],a[72:75],v[136:139],v[44:47]\n"
CV "v222,v224,v225\n"
CV "v223,v226,v227\n"
MF "v[44:47],a[112:115],v[140:143],v[44:47]\n"
AW "a96,v220\n"
AW "a97,v221\n"
MF "v[40:43],a[112:115],v[116:119],v[40:43]\n"
AW "a98,v222\n"
AW "a99,v223\n"
MF "v[40:43],a[72:75],v[112:115],v[40:43]\n"
LD "v202,s[48:51],s88 offen offset:2048 lds\n"
MF "v[40:43],a[32:35],v[108:111],v[40:43]\n"
AR "v8,a252\n"
AR "v11,a253\n"
MF "v[40:43],a[92:95],v[104:107],v[40:43]\n"
"v_bfe_u32 v224,v9,5,3\n"
"v_bfe_u32 v225,v9,8,2\n"
MF "v[40:43],a[52:55],v[100:103],v[40:43]\n"
"v_lshlrev_b32 v225,1,v225\n"
"v_and_b32 v226,16,v9\n"
MF "v[40:43],a[12:15],v[96:99],v[40:43]\n"
"s_waitcnt lgkmcnt(0)\n"
"v_lshlrev_b32_e64 v226,v226,s9\n"
"v_sub_u32 v224,v224,v225\n"
MF "v[48:51],a[16:19],v[96:99],v[48:51]\n"
LD "v203,s[48:51],s88 offen offset:3072 lds\n"
MF "v[48:51],a[56:59],v[100:103],v[48:51]\n"
"v_cmp_eq_u32 vcc,0,v224\n"
MF "v[48:51],a[96:99],v[104:107],v[48:51]\n"
"s_nop 0\n"
"v_cndmask_b32 v220,0,v226,vcc\n"
"v_cmp_eq_u32 vcc,1,v224\n"
MF "v[48:51],a[36:39],v[108:111],v[48:51]\n"
"s_nop 0\n"
"v_cndmask_b32 v221,0,v226,vcc\n"
AW "a248,0\n"
MF "v[48:51],a[76:79],v[112:115],v[48:51]\n"
AW "a249,0\n"
AW "a250,v220\n"
MF "v[48:51],a[116:119],v[116:119],v[48:51]\n"
AW "a251,v221\n"
AW "a252,v220\n"
MF "v[52:55],a[116:119],v[140:143],v[52:55]\n"
AW "a253,v221\n"
AW "a254,0\n"
MF "v[52:55],a[76:79],v[136:139],v[52:55]\n"
AW "a255,0\n"
"v_bfe_u32 v12,v9,4,4\n"
MF "v[52:55],a[36:39],v[132:135],v[52:55]\n"
"s_waitcnt lgkmcnt(0)\n"
"s_barrier\n"
MF "v[52:55],a[96:99],v[128:131],v[52:55]\n"
"s_add_u32 s80,s88,122880\n"
TR "v[176:177],v0 offset:24576\n"
"v_lshlrev_b32 v12,2,v12\n"
MF "v[52:55],a[56:59],v[124:127],v[52:55]\n"
"s_add_u32 m0,s79,98304\n"
TR "v[178:179],v0 offset:26624\n"
"v_lshrrev_b32 v13,2,v9\n"
MF "v[52:55],a[16:19],v[120:123],v[52:55]\n"
LD "v192,s[48:51],s80 offen lds\n"
MF "v[88:91],a[16:19],v[144:147],v[88:91]\n"
"s_nop 0\n"
TR "v[180:181],v0 offset:28672\n"
"ds_write_addtid_b32 v16 offset:24576\n"
MF "v[88:91],a[56:59],v[148:151],v[88:91]\n"
"s_nop 0\n"
TR "v[182:183],v0 offset:30720\n"
"ds_write_addtid_b32 v17 offset:24832\n"
MF "v[88:91],a[96:99],v[152:155],v[88:91]\n"
LD "v193,s[48:51],s80 offen offset:1024 lds\n"
MF "v[88:91],a[36:39],v[156:159],v[88:91]\n"
"s_nop 0\n"
TR "v[184:185],v0 offset:32768\n"
"ds_write_addtid_b32 v18 offset:25088\n"
MF "v[88:91],a[76:79],v[160:163],v[88:91]\n"
"s_nop 0\n"
TR "v[186:187],v0 offset:34816\n"
"ds_write_addtid_b32 v19 offset:25344\n"
MF "v[88:91],a[116:119],v[164:167],v[88:91]\n"
"s_nop 0\n"
"ds_write_addtid_b32 v20 offset:25600\n"
"ds_write_addtid_b32 v21 offset:25856\n"
MF "v[92:95],a[116:119],v[238:241],v[92:95]\n"
LD "v194,s[48:51],s80 offen offset:2048 lds\n"
MF "v[92:95],a[76:79],v[248:251],v[92:95]\n"
"s_nop 0\n"
"ds_write_addtid_b32 v22 offset:26112\n"
"ds_write_addtid_b32 v23 offset:26368\n"
MF "v[92:95],a[36:39],v[244:247],v[92:95]\n"
"s_nop 0\n"
"ds_write_addtid_b32 v24 offset:26624\n"
"ds_write_addtid_b32 v25 offset:26880\n"
MF "v[92:95],a[96:99],v[204:207],v[92:95]\n"
"s_nop 0\n"
"ds_write_addtid_b32 v26 offset:27136\n"
"ds_write_addtid_b32 v27 offset:27392\n"
MF "v[92:95],a[56:59],v[172:175],v[92:95]\n"
"v_add_u32 v13,s58,v13\n"
"v_add_u32 v4,24576,v4\n"
MF "v[92:95],a[16:19],v[168:171],v[92:95]\n"
"s_waitcnt lgkmcnt(9)\n"
"v_add_u32 v5,24576,v5\n"
"v_add_u32 v6,24576,v6\n"
"v_add_u32 v7,24576,v7\n"
".Lpl0:\n"
MF "v[96:99],a[0:3],v[176:179],v[252:255]\n"
"s_nop 0\n"
TR "v[188:189],v1 offset:24576\n"
"ds_write_addtid_b32 v28 offset:27648\n"
MF "v[96:99],a[40:43],v[180:183],v[96:99]\n"
"s_nop 0\n"
TR "v[190:191],v1 offset:26624\n"
"ds_write_addtid_b32 v29 offset:27904\n"
MF "v[96:99],a[80:83],v[184:187],v[96:99]\n"
LD "v195,s[48:51],s80 offen offset:3072 lds\n"
MF "v[104:107],a[84:87],v[184:187],v[252:255]\n"
"s_add_u32 s60,s64,1\n"
TR "a[240:241],v1 offset:28672\n"
"ds_write_addtid_b32 v30 offset:28160\n"
MF "v[104:107],a[44:47],v[180:183],v[104:107]\n"
"s_add_u32 m0,s79,102400\n"
TR "a[242:243],v1 offset:30720\n"
"ds_write_addtid_b32 v31 offset:24320\n"
MF "v[104:107],a[4:7],v[176:179],v[104:107]\n"
LD "v196,s[48:51],s80 offen lds\n"
MF "v[112:115],a[8:11],v[176:179],v[252:255]\n"
"s_cmp_eq_u32 s60,8\n"
"s_cselect_b32 s60,0,s60\n"
TR "a[244:245],v1 offset:32768\n"
MF "v[112:115],a[48:51],v[180:183],v[112:115]\n"
"s_cmp_eq_u32 s60,10\n"
"s_cselect_b32 s60,s61,s60\n"
TR "a[246:247],v1 offset:34816\n"
MF "v[112:115],a[88:91],v[184:187],v[112:115]\n"
"s_sub_u32 s65,s60,s64\n"
"ds_write_addtid_b32 v32 offset:24576\n"
"ds_write_addtid_b32 v33 offset:24832\n"
MF "v[120:123],a[92:95],v[184:187],v[252:255]\n"
"s_lshl_b32 s65,s65,7\n"
"ds_write_addtid_b32 v34 offset:25088\n"
"ds_write_addtid_b32 v35 offset:25344\n"
MF "v[120:123],a[52:55],v[180:183],v[120:123]\n"
LD "v197,s[48:51],s80 offen offset:1024 lds\n"
MF "v[120:123],a[12:15],v[176:179],v[120:123]\n"
"s_mov_b32 s64,s60\n"
"ds_write_addtid_b32 v36 offset:25600\n"
"ds_write_addtid_b32 v37 offset:25856\n"
MF "v[128:131],a[16:19],v[176:179],v[252:255]\n"
"s_nop 0\n"
"ds_write_addtid_b32 v38 offset:26112\n"
"ds_write_addtid_b32 v39 offset:26368\n"
MF "v[128:131],a[56:59],v[180:183],v[128:131]\n"
"s_nop 0\n"
"ds_write_addtid_b32 v40 offset:26624\n"
"ds_write_addtid_b32 v41 offset:26880\n"
MF "v[128:131],a[96:99],v[184:187],v[128:131]\n"
"s_waitcnt lgkmcnt(10)\n"
LD "v198,s[48:51],s80 offen offset:2048 lds\n"
MF "v[132:135],a[96:99],a[244:247],v[252:255]\n"
"s_nop 0\n"
TR "v[176:177],v2 offset:24576\n"
"ds_write_addtid_b32 v42 offset:27136\n"
MF "v[132:135],a[56:59],a[240:243],v[132:135]\n"
"s_nop 0\n"
TR "v[178:179],v2 offset:26624\n"
"ds_write_addtid_b32 v43 offset:27392\n"
MF "v[132:135],a[16:19],v[188:191],v[132:135]\n"
"s_nop 0\n"
"ds_write_addtid_b32 v44 offset:27648\n"
"ds_write_addtid_b32 v45 offset:27904\n"
MF "v[124:127],a[12:15],v[188:191],v[252:255]\n"
"s_nop 0\n"
TR "v[180:181],v2 offset:28672\n"
"ds_write_addtid_b32 v46 offset:28160\n"
MF "v[124:127],a[52:55],a[240:243],v[124:127]\n"
"s_nop 0\n"
TR "v[182:183],v2 offset:30720\n"
"ds_write_addtid_b32 v47 offset:28416\n"
MF "v[124:127],a[92:95],a[244:247],v[124:127]\n"
LD "v199,s[48:51],s80 offen offset:3072 lds\n"
MF "v[116:119],a[88:91],a[244:247],v[252:255]\n"
"s_nop 0\n"
TR "v[184:185],v2 offset:32768\n"
"ds_write_addtid_b32 v48 offset:28672\n"
MF "v[116:119],a[48:51],a[240:243],v[116:119]\n"
"s_add_u32 m0,s79,106496\n"
TR "v[186:187],v2 offset:34816\n"
"ds_write_addtid_b32 v49 offset:24832\n"
MF "v[116:119],a[8:11],v[188:191],v[116:119]\n"
LD "v200,s[48:51],s80 offen lds\n"
MF "v[108:111],a[4:7],v[188:191],v[252:255]\n"
"s_nop 0\n"
"ds_write_addtid_b32 v50 offset:25088\n"
"ds_write_addtid_b32 v51 offset:25344\n"
MF "v[108:111],a[44:47],a[240:243],v[108:111]\n"
"s_nop 0\n"
"ds_write_addtid_b32 v52 offset:25600\n"
"ds_write_addtid_b32 v53 offset:25856\n"
MF "v[108:111],a[84:87],a[244:247],v[108:111]\n"
"s_nop 0\n"
"ds_write_addtid_b32 v54 offset:26112\n"
"ds_write_addtid_b32 v55 offset:26368\n"
MF "v[100:103],a[80:83],a[244:247],v[252:255]\n"
MF "v[100:103],a[40:43],a[240:243],v[100:103]\n"
LD "v201,s[48:51],s80 offen offset:1024 lds\n"
MF "v[100:103],a[0:3],v[188:191],v[100:103]\n"
"s_waitcnt lgkmcnt(7)\n"
MF "v[136:139],a[0:3],v[176:179],v[252:255]\n"
TR "v[188:189],v3 offset:24576\n"
MF "v[136:139],a[40:43],v[180:183],v[136:139]\n"
TR "v[190:191],v3 offset:26624\n"
MF "v[136:139],a[80:83],v[184:187],v[136:139]\n"
LD "v202,s[48:51],s80 offen offset:2048 lds\n"
MF "v[144:147],a[84:87],v[184:187],v[252:255]\n"
TR "a[240:241],v3 offset:28672\n"
MF "v[144:147],a[44:47],v[180:183],v[144:147]\n"
TR "a[242:243],v3 offset:30720\n"
MF "v[144:147],a[4:7],v[176:179],v[144:147]\n"
LD "v203,s[48:51],s80 offen offset:3072 lds\n"
MF "v[152:155],a[8:11],v[176:179],v[252:255]\n"
TR "a[244:245],v3 offset:32768\n"
MF "v[152:155],a[48:51],v[180:183],v[152:155]\n"
TR "a[246:247],v3 offset:34816\n"
MF "v[152:155],a[88:91],v[184:187],v[152:155]\n"
MF "v[160:163],a[92:95],v[184:187],v[252:255]\n"
MF "v[160:163],a[52:55],v[180:183],v[160:163]\n"
MF "v[160:163],a[12:15],v[176:179],v[160:163]\n"
MF "v[168:171],a[16:19],v[176:179],v[252:255]\n"
MF "v[168:171],a[56:59],v[180:183],v[168:171]\n"
MF "v[168:171],a[96:99],v[184:187],v[168:171]\n"
"s_waitcnt vmcnt(24) lgkmcnt(0)\n"
"s_barrier\n"
MF "v[172:175],a[96:99],a[244:247],v[252:255]\n"
"s_add_u32 s89,s89,s65\n"
TR "v[176:177],v0\n"
MF "v[172:175],a[56:59],a[240:243],v[172:175]\n"
"s_add_u32 m0,s79,24576\n"
TR "v[178:179],v0 offset:2048\n"
MF "v[172:175],a[16:19],v[188:191],v[172:175]\n"
LD "v192,s[48:51],s89 offen lds\n"
MF "v[164:167],a[12:15],v[188:191],v[252:255]\n"
TR "v[180:181],v0 offset:4096\n"
MF "v[164:167],a[52:55],a[240:243],v[164:167]\n"
TR "v[182:183],v0 offset:6144\n"
MF "v[164:167],a[92:95],a[244:247],v[164:167]\n"
LD "v193,s[48:51],s89 offen offset:1024 lds\n"
MF "v[156:159],a[88:91],a[244:247],v[252:255]\n"
TR "v[184:185],v0 offset:8192\n"
MF "v[156:159],a[48:51],a[240:243],v[156:159]\n"
"s_nop 0\n"
TR "v[186:187],v0 offset:10240\n"
"ds_write_addtid_b32 v56 offset:49152\n"
MF "v[156:159],a[8:11],v[188:191],v[156:159]\n"
"s_nop 0\n"
"ds_write_addtid_b32 v57 offset:49408\n"
"ds_write_addtid_b32 v58 offset:49664\n"
MF "v[148:151],a[4:7],v[188:191],v[252:255]\n"
LD "v194,s[48:51],s89 offen offset:2048 lds\n"
MF "v[148:151],a[44:47],a[240:243],v[148:151]\n"
"buffer_load_dword v10,v12,s[40:43],s86 offen sc1\n"
MF "v[148:151],a[84:87],a[244:247],v[148:151]\n"
"s_nop 0\n"
"ds_write_addtid_b32 v59 offset:49920\n"
"ds_write_addtid_b32 v60 offset:50176\n"
MF "v[140:143],a[80:83],a[244:247],v[252:255]\n"
"s_nop 0\n"
"ds_write_addtid_b32 v61 offset:50432\n"
"ds_write_addtid_b32 v62 offset:50688\n"
MF "v[140:143],a[40:43],a[240:243],v[140:143]\n"
"s_nop 0\n"
"ds_write_addtid_b32 v63 offset:50944\n"
"ds_write_addtid_b32 v64 offset:51200\n"
MF "v[140:143],a[0:3],v[188:191],v[140:143]\n"
"s_waitcnt lgkmcnt(9)\n"
LD "v195,s[48:51],s89 offen offset:3072 lds\n"
MF "v[96:99],a[120:123],v[176:179],v[96:99]\n"
"s_nop 0\n"
TR "v[188:189],v1\n"
"ds_write_addtid_b32 v65 offset:51456\n"
MF "v[96:99],a[160:163],v[180:183],v[96:99]\n"
"s_nop 0\n"
TR "v[190:191],v1 offset:2048\n"
"ds_write_addtid_b32 v66 offset:51712\n"
MF "v[96:99],a[200:203],v[184:187],v[96:99]\n"
"s_nop 0\n"
"ds_write_addtid_b32 v67 offset:51968\n"
"ds_write_addtid_b32 v68 offset:52224\n"
MF "v[104:107],a[204:207],v[184:187],v[104:107]\n"
"s_nop 0\n"
TR "a[240:241],v1 offset:4096\n"
"ds_write_addtid_b32 v69 offset:52480\n"
MF "v[104:107],a[164:167],v[180:183],v[104:107]\n"
"s_add_u32 m0,s79,28672\n"
TR "a[242:243],v1 offset:6144\n"
"ds_write_addtid_b32 v70 offset:48640\n"
MF "v[104:107],a[124:127],v[176:179],v[104:107]\n"
LD "v196,s[48:51],s89 offen lds\n"
MF "v[112:115],a[128:131],v[176:179],v[112:115]\n"
"s_nop 0\n"
TR "a[244:245],v1 offset:8192\n"
"ds_write_addtid_b32 v71 offset:48896\n"
MF "v[112:115],a[168:171],v[180:183],v[112:115]\n"
"s_nop 0\n"
TR "a[246:247],v1 offset:10240\n"
"ds_write_addtid_b32 v72 offset:49152\n"
MF "v[112:115],a[208:211],v[184:187],v[112:115]\n"
LD "v197,s[48:51],s89 offen offset:1024 lds\n"
MF "v[120:123],a[212:215],v[184:187],v[120:123]\n"
"s_nop 0\n"
"ds_write_addtid_b32 v73 offset:49408\n"
"ds_write_addtid_b32 v74 offset:49664\n"
MF "v[120:123],a[172:175],v[180:183],v[120:123]\n"
"s_nop 0\n"
"ds_write_addtid_b32 v75 offset:49920\n"
"ds_write_addtid_b32 v76 offset:50176\n"
MF "v[120:123],a[132:135],v[176:179],v[120:123]\n"
"s_nop 0\n"
"ds_write_addtid_b32 v77 offset:50432\n"
"ds_write_addtid_b32 v78 offset:50688\n"
MF "v[128:131],a[136:139],v[176:179],v[128:131]\n"
"s_nop 0\n"
"ds_write_addtid_b32 v79 offset:50944\n"
"ds_write_addtid_b32 v80 offset:51200\n"
MF "v[128:131],a[176:179],v[180:183],v[128:131]\n"
LD "v198,s[48:51],s89 offen offset:2048 lds\n"
MF "v[128:131],a[216:219],v[184:187],v[128:131]\n"
"s_waitcnt lgkmcnt(9)\n"
"ds_write_addtid_b32 v81 offset:51456\n"
"ds_write_addtid_b32 v82 offset:51712\n"
MF "v[132:135],a[216:219],a[244:247],v[132:135]\n"
"s_nop 0\n"
TR "v[176:177],v2\n"
"ds_write_addtid_b32 v83 offset:51968\n"
MF "v[132:135],a[176:179],a[240:243],v[132:135]\n"
"s_nop 0\n"
TR "v[178:179],v2 offset:2048\n"
"ds_write_addtid_b32 v84 offset:52224\n"
MF "v[132:135],a[136:139],v[188:191],v[132:135]\n"
LD "v199,s[48:51],s89 offen offset:3072 lds\n"
MF "v[124:127],a[132:135],v[188:191],v[124:127]\n"
"s_nop 0\n"
TR "v[180:181],v2 offset:4096\n"
"ds_write_addtid_b32 v85 offset:52480\n"
MF "v[124:127],a[172:175],a[240:243],v[124:127]\n"
"s_nop 0\n"
TR "v[182:183],v2 offset:6144\n"
"ds_write_addtid_b32 v86 offset:52736\n"
MF "v[124:127],a[212:215],a[244:247],v[124:127]\n"
"s_nop 0\n"
"ds_write_addtid_b32 v87 offset:52992\n"
"ds_write_addtid_b32 v88 offset:53248\n"
MF "v[116:119],a[208:211],a[244:247],v[116:119]\n"
"s_nop 0\n"
TR "v[184:185],v2 offset:8192\n"
"ds_write_addtid_b32 v89 offset:53504\n"
MF "v[116:119],a[168:171],a[240:243],v[116:119]\n"
"s_add_u32 m0,s79,32768\n"
TR "v[186:187],v2 offset:10240\n"
"ds_write_addtid_b32 v90 offset:49664\n"
MF "v[116:119],a[128:131],v[188:191],v[116:119]\n"
LD "v200,s[48:51],s89 offen lds\n"
MF "v[108:111],a[124:127],v[188:191],v[108:111]\n"
"s_nop 0\n"
"ds_write_addtid_b32 v91 offset:49920\n"
"ds_write_addtid_b32 v92 offset:50176\n"
MF "v[108:111],a[164:167],a[240:243],v[108:111]\n"
"s_nop 0\n"
"ds_write_addtid_b32 v93 offset:50432\n"
"ds_write_addtid_b32 v94 offset:50688\n"
MF "v[108:111],a[204:207],a[244:247],v[108:111]\n"
"ds_write_addtid_b32 v95 offset:50944\n"
MF "v[100:103],a[200:203],a[244:247],v[100:103]\n"
LD "v201,s[48:51],s89 offen offset:1024 lds\n"
MF "v[100:103],a[160:163],a[240:243],v[100:103]\n"
MF "v[100:103],a[120:123],v[188:191],v[100:103]\n"
"s_waitcnt lgkmcnt(6)\n"
MF "v[136:139],a[120:123],v[176:179],v[136:139]\n"
TR "v[188:189],v3\n"
MF "v[136:139],a[160:163],v[180:183],v[136:139]\n"
TR "v[190:191],v3 offset:2048\n"
MF "v[136:139],a[200:203],v[184:187],v[136:139]\n"
LD "v202,s[48:51],s89 offen offset:2048 lds\n"
MF "v[144:147],a[204:207],v[184:187],v[144:147]\n"
TR "a[240:241],v3 offset:4096\n"
MF "v[144:147],a[164:167],v[180:183],v[144:147]\n"
TR "a[242:243],v3 offset:6144\n"
MF "v[144:147],a[124:127],v[176:179],v[144:147]\n"
LD "v203,s[48:51],s89 offen offset:3072 lds\n"
MF "v[152:155],a[128:131],v[176:179],v[152:155]\n"
TR "a[244:245],v3 offset:8192\n"
MF "v[152:155],a[168:171],v[180:183],v[152:155]\n"
TR "a[246:247],v3 offset:10240\n"
MF "v[152:155],a[208:211],v[184:187],v[152:155]\n"
MF "v[160:163],a[212:215],v[184:187],v[160:163]\n"
MF "v[160:163],a[172:175],v[180:183],v[160:163]\n"
MF "v[160:163],a[132:135],v[176:179],v[160:163]\n"
MF "v[168:171],a[136:139],v[176:179],v[168:171]\n"
MF "v[168:171],a[176:179],v[180:183],v[168:171]\n"
MF "v[168:171],a[216:219],v[184:187],v[168:171]\n"
"s_waitcnt vmcnt(25) lgkmcnt(0)\n"
"s_barrier\n"
MF "v[172:175],a[216:219],a[244:247],v[172:175]\n"
"s_add_u32 s80,s89,122880\n"
TR "v[176:177],v0 offset:49152\n"
MF "v[172:175],a[176:179],a[240:243],v[172:175]\n"
"s_add_u32 m0,s79,0\n"
"s_mul_i32 s95,s92,3277\n"
TR "v[178:179],v0 offset:51200\n"
MF "v[172:175],a[136:139],v[188:191],v[172:175]\n"
LD "v192,s[48:51],s80 offen lds\n"
MF "v[164:167],a[132:135],v[188:191],v[164:167]\n"
"s_lshr_b32 s95,s95,16\n"
"s_mul_i32 s59,s95,20\n"
TR "v[180:181],v0 offset:53248\n"
MF "v[164:167],a[172:175],a[240:243],v[164:167]\n"
"s_sub_u32 s59,s92,s59\n"
TR "v[182:183],v0 offset:55296\n"
MF "v[164:167],a[212:215],a[244:247],v[164:167]\n"
LD "v193,s[48:51],s80 offen offset:1024 lds\n"
MF "v[156:159],a[208:211],a[244:247],v[156:159]\n"
"s_cmp_ge_u32 s59,10\n"
"s_cselect_b32 s96,4294557760,0\n"
TR "v[184:185],v0 offset:57344\n"
MF "v[156:159],a[168:171],a[240:243],v[156:159]\n"
"s_add_u32 s96,s96,1024\n"
"s_mul_i32 s59,s59,40960\n"
TR "v[186:187],v0 offset:59392\n"
MF "v[156:159],a[128:131],v[188:191],v[156:159]\n"
"s_add_u32 s96,s96,s59\n"
"s_lshl_b32 s59,s95,7\n"
"s_add_u32 s96,s96,s59\n"
MF "v[148:151],a[124:127],v[188:191],v[148:151]\n"
"s_add_u32 s95,s95,3\n"
"s_cmp_ge_u32 s92,40\n"
"s_cselect_b32 s95,99,s95\n"
MF "v[148:151],a[164:167],a[240:243],v[148:151]\n"
LD "v194,s[48:51],s80 offen offset:2048 lds\n"
MF "v[148:151],a[204:207],a[244:247],v[148:151]\n"
"s_cmp_ge_u32 s85,5\n"
"s_cselect_b32 s95,99,s95\n"
"s_cselect_b32 s72,s85,0\n"
MF "v[140:143],a[200:203],a[244:247],v[140:143]\n"
MF "v[140:143],a[160:163],a[240:243],v[140:143]\n"
MF "v[140:143],a[120:123],v[188:191],v[140:143]\n"
"s_waitcnt lgkmcnt(0)\n"
LD "v195,s[48:51],s80 offen offset:3072 lds\n"
MF "v[96:99],a[20:23],v[176:179],v[96:99]\n"
TR "v[188:189],v1 offset:49152\n"
MF "v[96:99],a[60:63],v[180:183],v[96:99]\n"
TR "v[190:191],v1 offset:51200\n"
MF "v[96:99],a[100:103],v[184:187],v[96:99]\n"
MF "v[104:107],a[104:107],v[184:187],v[104:107]\n"
TR "a[240:241],v1 offset:53248\n"
MF "v[104:107],a[64:67],v[180:183],v[104:107]\n"
"s_add_u32 m0,s79,4096\n"
TR "a[242:243],v1 offset:55296\n"
MF "v[104:107],a[24:27],v[176:179],v[104:107]\n"
LD "v196,s[48:51],s80 offen lds\n"
MF "v[112:115],a[28:31],v[176:179],v[112:115]\n"
TR "a[244:245],v1 offset:57344\n"
MF "v[112:115],a[68:71],v[180:183],v[112:115]\n"
TR "a[246:247],v1 offset:59392\n"
MF "v[112:115],a[108:111],v[184:187],v[112:115]\n"
LD "v197,s[48:51],s80 offen offset:1024 lds\n"
MF "v[120:123],a[112:115],v[184:187],v[120:123]\n"
MF "v[120:123],a[72:75],v[180:183],v[120:123]\n"
MF "v[120:123],a[32:35],v[176:179],v[120:123]\n"
MF "v[128:131],a[36:39],v[176:179],v[128:131]\n"
MF "v[128:131],a[76:79],v[180:183],v[128:131]\n"
LD "v198,s[48:51],s80 offen offset:2048 lds\n"
MF "v[128:131],a[116:119],v[184:187],v[128:131]\n"
"s_waitcnt lgkmcnt(0)\n"
MF "v[132:135],a[116:119],a[244:247],v[132:135]\n"
TR "v[176:177],v2 offset:49152\n"
MF "v[132:135],a[76:79],a[240:243],v[132:135]\n"
TR "v[178:179],v2 offset:51200\n"
MF "v[132:135],a[36:39],v[188:191],v[132:135]\n"
LD "v199,s[48:51],s80 offen offset:3072 lds\n"
MF "v[124:127],a[32:35],v[188:191],v[124:127]\n"
TR "v[180:181],v2 offset:53248\n"
MF "v[124:127],a[72:75],a[240:243],v[124:127]\n"
TR "v[182:183],v2 offset:55296\n"
MF "v[124:127],a[112:115],a[244:247],v[124:127]\n"
MF "v[116:119],a[108:111],a[244:247],v[116:119]\n"
TR "v[184:185],v2 offset:57344\n"
MF "v[116:119],a[68:71],a[240:243],v[116:119]\n"
"s_add_u32 m0,s79,8192\n"
TR "v[186:187],v2 offset:59392\n"
MF "v[116:119],a[28:31],v[188:191],v[116:119]\n"
LD "v200,s[48:51],s80 offen lds\n"
MF "v[108:111],a[24:27],v[188:191],v[108:111]\n"
MF "v[108:111],a[64:67],a[240:243],v[108:111]\n"
MF "v[108:111],a[104:107],a[244:247],v[108:111]\n"
MF "v[100:103],a[100:103],a[244:247],v[100:103]\n"
LD "v201,s[48:51],s80 offen offset:1024 lds\n"
MF "v[100:103],a[60:63],a[240:243],v[100:103]\n"
"s_waitcnt vmcnt(19)\n"
"s_cmp_eq_u32 s83,0xf0000000\n"
MF "v[100:103],a[20:23],v[188:191],v[100:103]\n"
"s_cbranch_scc1 .Ljs5\n"
"s_cmp_eq_u32 s83,0xf0000010\n"
"s_cbranch_scc1 .Ljc5\n"
MF "v[248:251],v[210:213],a[250:253],0\n"
"v_mov_b32 v240,v208\n"
"v_mov_b32 v241,v209\n"
MF "v[244:247],v[214:217],a[250:253],0\n"
MF "v[248:251],v[218:221],a[250:253],v[248:251]\n"
MF "v[244:247],v[222:225],a[250:253],v[244:247]\n"
MF "v[248:251],v[226:229],a[250:253],v[248:251]\n"
MF "v[244:247],v[230:233],a[250:253],v[244:247]\n"
MF "v[248:251],v[234:237],a[250:253],v[248:251]\n"
MF "v[244:247],v[238:241],a[250:253],v[244:247]\n"
"s_branch .Ljs5\n"
".Ljc5:\n"
MF "v[96:99],a[252:255],v[208:211],v[96:99]\n"
MF "v[104:107],a[252:255],v[212:215],v[104:107]\n"
MF "v[112:115],a[252:255],v[216:219],v[112:115]\n"
MF "v[120:123],a[252:255],v[220:223],v[120:123]\n"
MF "v[128:131],a[252:255],v[224:227],v[128:131]\n"
MF "v[100:103],a[248:251],v[208:211],v[100:103]\n"
MF "v[108:111],a[248:251],v[212:215],v[108:111]\n"
MF "v[116:119],a[248:251],v[216:219],v[116:119]\n"
MF "v[124:127],a[248:251],v[220:223],v[124:127]\n"
MF "v[132:135],a[248:251],v[224:227],v[132:135]\n"
".Ljs5:\n"
"s_waitcnt lgkmcnt(0)\n"
MF "v[136:139],a[20:23],v[176:179],v[136:139]\n"
TR "v[188:189],v3 offset:49152\n"
"v_sub_u32 v10,v10,v11\n"
MF "v[136:139],a[60:63],v[180:183],v[136:139]\n"
"s_lshl_b32 s82,s92,10\n"
TR "v[190:191],v3 offset:51200\n"
"v_cmp_gt_u32 vcc,s95,v10\n"
MF "v[136:139],a[100:103],v[184:187],v[136:139]\n"
LD "v202,s[48:51],s80 offen offset:2048 lds\n"
MF "v[144:147],a[104:107],v[184:187],v[144:147]\n"
"s_add_u32 s82,s82,s93\n"
TR "a[240:241],v3 offset:53248\n"
CV "v244,v244,v245\n"
MF "v[144:147],a[64:67],v[180:183],v[144:147]\n"
TR "a[242:243],v3 offset:55296\n"
CV "v245,v246,v247\n"
MF "v[144:147],a[24:27],v[176:179],v[144:147]\n"
LD "v203,s[48:51],s80 offen offset:3072 lds\n"
MF "v[152:155],a[28:31],v[176:179],v[152:155]\n"
TR "a[244:245],v3 offset:57344\n"
CV "v246,v248,v249\n"
MF "v[152:155],a[68:71],v[180:183],v[152:155]\n"
TR "a[246:247],v3 offset:59392\n"
CV "v247,v250,v251\n"
MF "v[152:155],a[108:111],v[184:187],v[152:155]\n"
"s_cmp_eq_u64 vcc,0\n"
"s_cselect_b32 s82,s82,0xf0000000\n"
"s_cselect_b32 s84,s96,0xf0000000\n"
"s_cselect_b32 s59,32,0\n"
MF "v[160:163],a[112:115],v[184:187],v[160:163]\n"
"s_add_u32 s92,s92,s59\n"
".Lnf0:\n"
"v_readlane_b32 s58,v10,s67\n"
"s_cmp_lt_u32 s58,s72\n"
"s_cbranch_scc1 .Lsp0\n"
".Lnb0:\n"
MF "v[160:163],a[72:75],v[180:183],v[160:163]\n"
"s_cmp_ge_u32 s85,5\n"
"s_cselect_b32 s82,s70,s82\n"
"s_cselect_b32 s84,0xf0000010,s84\n"
MF "v[160:163],a[32:35],v[176:179],v[160:163]\n"
"s_cmp_ge_u32 s85,5\n"
"s_cselect_b32 s71,1024,s71\n"
"s_cselect_b32 s66,0x80000000,s66\n"
MF "v[168:171],a[36:39],v[176:179],v[168:171]\n"
"s_add_u32 s70,s70,20480\n"
"v_permlane16_swap_b32 v244,v246\n"
"v_permlane16_swap_b32 v245,v247\n"
MF "v[168:171],a[76:79],v[180:183],v[168:171]\n"
"buffer_store_dwordx4 v[244:247],v8,s[44:47],s83 offen sc1\n"
MF "v[168:171],a[116:119],v[184:187],v[168:171]\n"
"s_waitcnt vmcnt(26) lgkmcnt(0)\n"
"s_barrier\n"
MF "v[172:175],a[116:119],a[244:247],v[172:175]\n"
"s_add_u32 s88,s88,s65\n"
"s_mov_b32 s83,s84\n"
TR "v[176:177],v4\n"
MF "v[172:175],a[76:79],a[240:243],v[172:175]\n"
"s_add_u32 m0,s79,49152\n"
TR "v[178:179],v4 offset:2048\n"
MF "v[172:175],a[36:39],v[188:191],v[172:175]\n"
LD "v192,s[48:51],s88 offen lds\n"
MF "v[164:167],a[32:35],v[188:191],v[164:167]\n"
TR "v[180:181],v4 offset:4096\n"
MF "v[164:167],a[72:75],a[240:243],v[164:167]\n"
TR "v[182:183],v4 offset:6144\n"
MF "v[164:167],a[112:115],a[244:247],v[164:167]\n"
LD "v193,s[48:51],s88 offen offset:1024 lds\n"
MF "v[156:159],a[108:111],a[244:247],v[156:159]\n"
TR "v[184:185],v4 offset:8192\n"
MF "v[156:159],a[68:71],a[240:243],v[156:159]\n"
TR "v[186:187],v4 offset:10240\n"
MF "v[156:159],a[28:31],v[188:191],v[156:159]\n"
LD "v[208:211],v9,s[40:43],s82 offen sc1\n"
MF "v[148:151],a[24:27],v[188:191],v[148:151]\n"
MF "v[148:151],a[64:67],a[240:243],v[148:151]\n"
LD "v194,s[48:51],s88 offen offset:2048 lds\n"
MF "v[148:151],a[104:107],a[244:247],v[148:151]\n"
MF "v[140:143],a[100:103],a[244:247],v[140:143]\n"
MF "v[140:143],a[60:63],a[240:243],v[140:143]\n"
MF "v[140:143],a[20:23],v[188:191],v[140:143]\n"
"s_waitcnt lgkmcnt(0)\n"
LD "v195,s[48:51],s88 offen offset:3072 lds\n"
MF "v[96:99],a[140:143],v[176:179],v[96:99]\n"
TR "v[188:189],v5\n"
MF "v[96:99],a[180:183],v[180:183],v[96:99]\n"
"s_add_u32 s82,s82,s71\n"
TR "v[190:191],v5 offset:2048\n"
MF "v[96:99],a[220:223],v[184:187],v[96:99]\n"
LD "v[212:215],v9,s[40:43],s82 offen sc1\n"
MF "v[104:107],a[224:227],v[184:187],v[104:107]\n"
TR "a[240:241],v5 offset:4096\n"
MF "v[104:107],a[184:187],v[180:183],v[104:107]\n"
"s_add_u32 m0,s79,53248\n"
TR "a[242:243],v5 offset:6144\n"
MF "v[104:107],a[144:147],v[176:179],v[104:107]\n"
LD "v196,s[48:51],s88 offen lds\n"
MF "v[112:115],a[148:151],v[176:179],v[112:115]\n"
TR "a[244:245],v5 offset:8192\n"
MF "v[112:115],a[188:191],v[180:183],v[112:115]\n"
TR "a[246:247],v5 offset:10240\n"
MF "v[112:115],a[228:231],v[184:187],v[112:115]\n"
LD "v197,s[48:51],s88 offen offset:1024 lds\n"
MF "v[120:123],a[232:235],v[184:187],v[120:123]\n"
"s_add_u32 s82,s82,s71\n"
MF "v[120:123],a[192:195],v[180:183],v[120:123]\n"
LD "v[216:219],v9,s[40:43],s82 offen sc1\n"
MF "v[120:123],a[152:155],v[176:179],v[120:123]\n"
MF "v[128:131],a[156:159],v[176:179],v[128:131]\n"
MF "v[128:131],a[196:199],v[180:183],v[128:131]\n"
LD "v198,s[48:51],s88 offen offset:2048 lds\n"
MF "v[128:131],a[236:239],v[184:187],v[128:131]\n"
"s_waitcnt lgkmcnt(0)\n"
MF "v[132:135],a[236:239],a[244:247],v[132:135]\n"
TR "v[176:177],v6\n"
MF "v[132:135],a[196:199],a[240:243],v[132:135]\n"
TR "v[178:179],v6 offset:2048\n"
MF "v[132:135],a[156:159],v[188:191],v[132:135]\n"
LD "v199,s[48:51],s88 offen offset:3072 lds\n"
MF "v[124:127],a[152:155],v[188:191],v[124:127]\n"
TR "v[180:181],v6 offset:4096\n"
MF "v[124:127],a[192:195],a[240:243],v[124:127]\n"
"s_add_u32 s82,s82,s71\n"
TR "v[182:183],v6 offset:6144\n"
MF "v[124:127],a[232:235],a[244:247],v[124:127]\n"
LD "v[220:223],v9,s[40:43],s82 offen sc1\n"
MF "v[116:119],a[228:231],a[244:247],v[116:119]\n"
TR "v[184:185],v6 offset:8192\n"
MF "v[116:119],a[188:191],a[240:243],v[116:119]\n"
"s_add_u32 m0,s79,57344\n"
TR "v[186:187],v6 offset:10240\n"
MF "v[116:119],a[148:151],v[188:191],v[116:119]\n"
LD "v200,s[48:51],s88 offen lds\n"
MF "v[108:111],a[144:147],v[188:191],v[108:111]\n"
MF "v[108:111],a[184:187],a[240:243],v[108:111]\n"
"s_add_u32 s82,s82,s71\n"
MF "v[108:111],a[224:227],a[244:247],v[108:111]\n"
LD "v[224:227],v9,s[40:43],s82 offen sc1\n"
MF "v[100:103],a[220:223],a[244:247],v[100:103]\n"
LD "v201,s[48:51],s88 offen offset:1024 lds\n"
MF "v[100:103],a[180:183],a[240:243],v[100:103]\n"
MF "v[100:103],a[140:143],v[188:191],v[100:103]\n"
"s_waitcnt lgkmcnt(0)\n"
MF "v[136:139],a[140:143],v[176:179],v[136:139]\n"
TR "v[188:189],v7\n"
MF "v[136:139],a[180:183],v[180:183],v[136:139]\n"
TR "v[190:191],v7 offset:2048\n"
MF "v[136:139],a[220:223],v[184:187],v[136:139]\n"
LD "v202,s[48:51],s88 offen offset:2048 lds\n"
MF "v[144:147],a[224:227],v[184:187],v[144:147]\n"
TR "a[240:241],v7 offset:4096\n"
MF "v[144:147],a[184:187],v[180:183],v[144:147]\n"
TR "a[242:243],v7 offset:6144\n"
MF "v[144:147],a[144:147],v[176:179],v[144:147]\n"
LD "v203,s[48:51],s88 offen offset:3072 lds\n"
MF "v[152:155],a[148:151],v[176:179],v[152:155]\n"
TR "a[244:245],v7 offset:8192\n"
MF "v[152:155],a[188:191],v[180:183],v[152:155]\n"
"s_add_u32 s82,s82,s66\n"
TR "a[246:247],v7 offset:10240\n"
MF "v[152:155],a[228:231],v[184:187],v[152:155]\n"
LD "v[228:231],v9,s[40:43],s82 offen sc1\n"
MF "v[160:163],a[232:235],v[184:187],v[160:163]\n"
MF "v[160:163],a[192:195],v[180:183],v[160:163]\n"
MF "v[160:163],a[152:155],v[176:179],v[160:163]\n"
MF "v[168:171],a[156:159],v[176:179],v[168:171]\n"
"s_add_u32 s82,s82,s71\n"
MF "v[168:171],a[196:199],v[180:183],v[168:171]\n"
LD "v[232:235],v9,s[40:43],s82 offen sc1\n"
MF "v[168:171],a[236:239],v[184:187],v[168:171]\n"
"s_waitcnt vmcnt(32) lgkmcnt(0)\n"
"s_barrier\n"
MF "v[172:175],a[236:239],a[244:247],v[172:175]\n"
"s_add_u32 s80,s88,122880\n"
TR "v[176:177],v0 offset:24576\n"
MF "v[172:175],a[196:199],a[240:243],v[172:175]\n"
"s_add_u32 m0,s79,98304\n"
"s_add_u32 s85,s85,1\n"
TR "v[178:179],v0 offset:26624\n"
MF "v[172:175],a[156:159],v[188:191],v[172:175]\n"
LD "v192,s[48:51],s80 offen lds\n"
MF "v[164:167],a[152:155],v[188:191],v[164:167]\n"
"s_nop 0\n"
TR "v[180:181],v0 offset:28672\n"
"ds_write_addtid_b32 v96 offset:24576\n"
MF "v[164:167],a[192:195],a[240:243],v[164:167]\n"
"s_nop 0\n"
TR "v[182:183],v0 offset:30720\n"
"ds_write_addtid_b32 v97 offset:24832\n"
MF "v[164:167],a[232:235],a[244:247],v[164:167]\n"
LD "v193,s[48:51],s80 offen offset:1024 lds\n"
MF "v[156:159],a[228:231],a[244:247],v[156:159]\n"
"s_nop 0\n"
TR "v[184:185],v0 offset:32768\n"
"ds_write_addtid_b32 v98 offset:25088\n"
MF "v[156:159],a[188:191],a[240:243],v[156:159]\n"
"s_add_u32 s82,s82,s71\n"
TR "v[186:187],v0 offset:34816\n"
"ds_write_addtid_b32 v99 offset:25344\n"
MF "v[156:159],a[148:151],v[188:191],v[156:159]\n"
LD "v[236:239],v9,s[40:43],s82 offen sc1\n"
MF "v[148:151],a[144:147],v[188:191],v[148:151]\n"
LD "v194,s[48:51],s80 offen offset:2048 lds\n"
MF "v[148:151],a[184:187],a[240:243],v[148:151]\n"
"s_nop 0\n"
"ds_write_addtid_b32 v100 offset:25600\n"
"ds_write_addtid_b32 v101 offset:25856\n"
MF "v[148:151],a[224:227],a[244:247],v[148:151]\n"
"s_nop 0\n"
"ds_write_addtid_b32 v102 offset:26112\n"
"ds_write_addtid_b32 v103 offset:26368\n"
MF "v[140:143],a[220:223],a[244:247],v[140:143]\n"
"s_nop 0\n"
"ds_write_addtid_b32 v104 offset:26624\n"
"ds_write_addtid_b32 v105 offset:26880\n"
MF "v[140:143],a[180:183],a[240:243],v[140:143]\n"
"s_nop 0\n"
"ds_write_addtid_b32 v106 offset:27136\n"
"ds_write_addtid_b32 v107 offset:27392\n"
MF "v[140:143],a[140:143],v[188:191],v[140:143]\n"
"s_waitcnt lgkmcnt(9)\n"
MF "v[16:19],a[0:3],v[176:179],v[252:255]\n"
"s_nop 0\n"
TR "v[188:189],v1 offset:24576\n"
"ds_write_addtid_b32 v108 offset:27648\n"
MF "v[16:19],a[40:43],v[180:183],v[16:19]\n"
"s_nop 0\n"
TR "v[190:191],v1 offset:26624\n"
"ds_write_addtid_b32 v109 offset:27904\n"
MF "v[16:19],a[80:83],v[184:187],v[16:19]\n"
LD "v195,s[48:51],s80 offen offset:3072 lds\n"
MF "v[24:27],a[84:87],v[184:187],v[252:255]\n"
"s_add_u32 s60,s64,1\n"
TR "a[240:241],v1 offset:28672\n"
"ds_write_addtid_b32 v110 offset:28160\n"
MF "v[24:27],a[44:47],v[180:183],v[24:27]\n"
"s_add_u32 m0,s79,102400\n"
TR "a[242:243],v1 offset:30720\n"
"ds_write_addtid_b32 v111 offset:24320\n"
MF "v[24:27],a[4:7],v[176:179],v[24:27]\n"
LD "v196,s[48:51],s80 offen lds\n"
MF "v[32:35],a[8:11],v[176:179],v[252:255]\n"
"s_cmp_eq_u32 s60,8\n"
"s_cselect_b32 s60,0,s60\n"
TR "a[244:245],v1 offset:32768\n"
MF "v[32:35],a[48:51],v[180:183],v[32:35]\n"
"s_cmp_eq_u32 s60,10\n"
"s_cselect_b32 s60,s61,s60\n"
TR "a[246:247],v1 offset:34816\n"
MF "v[32:35],a[88:91],v[184:187],v[32:35]\n"
"s_sub_u32 s65,s60,s64\n"
"ds_write_addtid_b32 v112 offset:24576\n"
"ds_write_addtid_b32 v113 offset:24832\n"
MF "v[40:43],a[92:95],v[184:187],v[252:255]\n"
"s_lshl_b32 s65,s65,7\n"
"ds_write_addtid_b32 v114 offset:25088\n"
"ds_write_addtid_b32 v115 offset:25344\n"
MF "v[40:43],a[52:55],v[180:183],v[40:43]\n"
LD "v197,s[48:51],s80 offen offset:1024 lds\n"
MF "v[40:43],a[12:15],v[176:179],v[40:43]\n"
"s_mov_b32 s64,s60\n"
"ds_write_addtid_b32 v116 offset:25600\n"
"ds_write_addtid_b32 v117 offset:25856\n"
MF "v[48:51],a[16:19],v[176:179],v[252:255]\n"
"s_nop 0\n"
"ds_write_addtid_b32 v118 offset:26112\n"
"ds_write_addtid_b32 v119 offset:26368\n"
MF "v[48:51],a[56:59],v[180:183],v[48:51]\n"
"s_nop 0\n"
"ds_write_addtid_b32 v120 offset:26624\n"
"ds_write_addtid_b32 v121 offset:26880\n"
MF "v[48:51],a[96:99],v[184:187],v[48:51]\n"
"s_waitcnt lgkmcnt(10)\n"
LD "v198,s[48:51],s80 offen offset:2048 lds\n"
MF "v[52:55],a[96:99],a[244:247],v[252:255]\n"
"s_nop 0\n"
TR "v[176:177],v2 offset:24576\n"
"ds_write_addtid_b32 v122 offset:27136\n"
MF "v[52:55],a[56:59],a[240:243],v[52:55]\n"
"s_nop 0\n"
TR "v[178:179],v2 offset:26624\n"
"ds_write_addtid_b32 v123 offset:27392\n"
MF "v[52:55],a[16:19],v[188:191],v[52:55]\n"
"s_nop 0\n"
"ds_write_addtid_b32 v124 offset:27648\n"
"ds_write_addtid_b32 v125 offset:27904\n"
MF "v[44:47],a[12:15],v[188:191],v[252:255]\n"
"s_nop 0\n"
TR "v[180:181],v2 offset:28672\n"
"ds_write_addtid_b32 v126 offset:28160\n"
MF "v[44:47],a[52:55],a[240:243],v[44:47]\n"
"s_nop 0\n"
TR "v[182:183],v2 offset:30720\n"
"ds_write_addtid_b32 v127 offset:28416\n"
MF "v[44:47],a[92:95],a[244:247],v[44:47]\n"
LD "v199,s[48:51],s80 offen offset:3072 lds\n"
MF "v[36:39],a[88:91],a[244:247],v[252:255]\n"
"s_nop 0\n"
TR "v[184:185],v2 offset:32768\n"
"ds_write_addtid_b32 v128 offset:28672\n"
MF "v[36:39],a[48:51],a[240:243],v[36:39]\n"
"s_add_u32 m0,s79,106496\n"
TR "v[186:187],v2 offset:34816\n"
"ds_write_addtid_b32 v129 offset:24832\n"
MF "v[36:39],a[8:11],v[188:191],v[36:39]\n"
LD "v200,s[48:51],s80 offen lds\n"
MF "v[28:31],a[4:7],v[188:191],v[252:255]\n"
"s_nop 0\n"
"ds_write_addtid_b32 v130 offset:25088\n"
"ds_write_addtid_b32 v131 offset:25344\n"
MF "v[28:31],a[44:47],a[240:243],v[28:31]\n"
"s_nop 0\n"
"ds_write_addtid_b32 v132 offset:25600\n"
"ds_write_addtid_b32 v133 offset:25856\n"
MF "v[28:31],a[84:87],a[244:247],v[28:31]\n"
"s_nop 0\n"
"ds_write_addtid_b32 v134 offset:26112\n"
"ds_write_addtid_b32 v135 offset:26368\n"
MF "v[20:23],a[80:83],a[244:247],v[252:255]\n"
MF "v[20:23],a[40:43],a[240:243],v[20:23]\n"
LD "v201,s[48:51],s80 offen offset:1024 lds\n"
MF "v[20:23],a[0:3],v[188:191],v[20:23]\n"
"s_waitcnt lgkmcnt(7)\n"
MF "v[56:59],a[0:3],v[176:179],v[252:255]\n"
TR "v[188:189],v3 offset:24576\n"
MF "v[56:59],a[40:43],v[180:183],v[56:59]\n"
TR "v[190:191],v3 offset:26624\n"
MF "v[56:59],a[80:83],v[184:187],v[56:59]\n"
LD "v202,s[48:51],s80 offen offset:2048 lds\n"
MF "v[64:67],a[84:87],v[184:187],v[252:255]\n"
TR "a[240:241],v3 offset:28672\n"
MF "v[64:67],a[44:47],v[180:183],v[64:67]\n"
TR "a[242:243],v3 offset:30720\n"
MF "v[64:67],a[4:7],v[176:179],v[64:67]\n"
LD "v203,s[48:51],s80 offen offset:3072 lds\n"
MF "v[72:75],a[8:11],v[176:179],v[252:255]\n"
TR "a[244:245],v3 offset:32768\n"
MF "v[72:75],a[48:51],v[180:183],v[72:75]\n"
TR "a[246:247],v3 offset:34816\n"
MF "v[72:75],a[88:91],v[184:187],v[72:75]\n"
MF "v[80:83],a[92:95],v[184:187],v[252:255]\n"
MF "v[80:83],a[52:55],v[180:183],v[80:83]\n"
MF "v[80:83],a[12:15],v[176:179],v[80:83]\n"
MF "v[88:91],a[16:19],v[176:179],v[252:255]\n"
MF "v[88:91],a[56:59],v[180:183],v[88:91]\n"
MF "v[88:91],a[96:99],v[184:187],v[88:91]\n"
"s_waitcnt vmcnt(33) lgkmcnt(0)\n"
"s_barrier\n"
MF "v[92:95],a[96:99],a[244:247],v[252:255]\n"
"s_add_u32 s89,s89,s65\n"
TR "v[176:177],v0\n"
MF "v[92:95],a[56:59],a[240:243],v[92:95]\n"
"s_add_u32 m0,s79,24576\n"
TR "v[178:179],v0 offset:2048\n"
MF "v[92:95],a[16:19],v[188:191],v[92:95]\n"
LD "v192,s[48:51],s89 offen lds\n"
MF "v[84:87],a[12:15],v[188:191],v[252:255]\n"
TR "v[180:181],v0 offset:4096\n"
MF "v[84:87],a[52:55],a[240:243],v[84:87]\n"
TR "v[182:183],v0 offset:6144\n"
MF "v[84:87],a[92:95],a[244:247],v[84:87]\n"
LD "v193,s[48:51],s89 offen offset:1024 lds\n"
MF "v[76:79],a[88:91],a[244:247],v[252:255]\n"
TR "v[184:185],v0 offset:8192\n"
MF "v[76:79],a[48:51],a[240:243],v[76:79]\n"
"s_nop 0\n"
TR "v[186:187],v0 offset:10240\n"
"ds_write_addtid_b32 v136 offset:49152\n"
MF "v[76:79],a[8:11],v[188:191],v[76:79]\n"
"s_nop 0\n"
"ds_write_addtid_b32 v137 offset:49408\n"
"ds_write_addtid_b32 v138 offset:49664\n"
MF "v[68:71],a[4:7],v[188:191],v[252:255]\n"
LD "v194,s[48:51],s89 offen offset:2048 lds\n"
MF "v[68:71],a[44:47],a[240:243],v[68:71]\n"
"buffer_load_dword v10,v12,s[40:43],s86 offen sc1\n"
MF "v[68:71],a[84:87],a[244:247],v[68:71]\n"
"s_nop 0\n"
"ds_write_addtid_b32 v139 offset:49920\n"
"ds_write_addtid_b32 v140 offset:50176\n"
MF "v[60:63],a[80:83],a[244:247],v[252:255]\n"
"s_nop 0\n"
"ds_write_addtid_b32 v141 offset:50432\n"
"ds_write_addtid_b32 v142 offset:50688\n"
MF "v[60:63],a[40:43],a[240:243],v[60:63]\n"
"s_nop 0\n"
"ds_write_addtid_b32 v143 offset:50944\n"
"ds_write_addtid_b32 v144 offset:51200\n"
MF "v[60:63],a[0:3],v[188:191],v[60:63]\n"
"s_waitcnt lgkmcnt(9)\n"
LD "v195,s[48:51],s89 offen offset:3072 lds\n"
MF "v[16:19],a[120:123],v[176:179],v[16:19]\n"
"s_nop 0\n"
TR "v[188:189],v1\n"
"ds_write_addtid_b32 v145 offset:51456\n"
MF "v[16:19],a[160:163],v[180:183],v[16:19]\n"
"s_nop 0\n"
TR "v[190:191],v1 offset:2048\n"
"ds_write_addtid_b32 v146 offset:51712\n"
MF "v[16:19],a[200:203],v[184:187],v[16:19]\n"
"s_nop 0\n"
"ds_write_addtid_b32 v147 offset:51968\n"
"ds_write_addtid_b32 v148 offset:52224\n"
MF "v[24:27],a[204:207],v[184:187],v[24:27]\n"
"s_nop 0\n"
TR "a[240:241],v1 offset:4096\n"
"ds_write_addtid_b32 v149 offset:52480\n"
MF "v[24:27],a[164:167],v[180:183],v[24:27]\n"
"s_add_u32 m0,s79,28672\n"
TR "a[242:243],v1 offset:6144\n"
"ds_write_addtid_b32 v150 offset:48640\n"
MF "v[24:27],a[124:127],v[176:179],v[24:27]\n"
LD "v196,s[48:51],s89 offen lds\n"
MF "v[32:35],a[128:131],v[176:179],v[32:35]\n"
"s_nop 0\n"
TR "a[244:245],v1 offset:8192\n"
"ds_write_addtid_b32 v151 offset:48896\n"
MF "v[32:35],a[168:171],v[180:183],v[32:35]\n"
"s_nop 0\n"
TR "a[246:247],v1 offset:10240\n"
"ds_write_addtid_b32 v152 offset:49152\n"
MF "v[32:35],a[208:211],v[184:187],v[32:35]\n"
LD "v197,s[48:51],s89 offen offset:1024 lds\n"
MF "v[40:43],a[212:215],v[184:187],v[40:43]\n"
"s_nop 0\n"
"ds_write_addtid_b32 v153 offset:49408\n"
"ds_write_addtid_b32 v154 offset:49664\n"
MF "v[40:43],a[172:175],v[180:183],v[40:43]\n"
"s_nop 0\n"
"ds_write_addtid_b32 v155 offset:49920\n"
"ds_write_addtid_b32 v156 offset:50176\n"
MF "v[40:43],a[132:135],v[176:179],v[40:43]\n"
"s_nop 0\n"
"ds_write_addtid_b32 v157 offset:50432\n"
"ds_write_addtid_b32 v158 offset:50688\n"
MF "v[48:51],a[136:139],v[176:179],v[48:51]\n"
"s_nop 0\n"
"ds_write_addtid_b32 v159 offset:50944\n"
"ds_write_addtid_b32 v160 offset:51200\n"
MF "v[48:51],a[176:179],v[180:183],v[48:51]\n"
LD "v198,s[48:51],s89 offen offset:2048 lds\n"
MF "v[48:51],a[216:219],v[184:187],v[48:51]\n"
"s_waitcnt lgkmcnt(9)\n"
"ds_write_addtid_b32 v161 offset:51456\n"
"ds_write_addtid_b32 v162 offset:51712\n"
MF "v[52:55],a[216:219],a[244:247],v[52:55]\n"
"s_nop 0\n"
TR "v[176:177],v2\n"
"ds_write_addtid_b32 v163 offset:51968\n"
MF "v[52:55],a[176:179],a[240:243],v[52:55]\n"
"s_nop 0\n"
TR "v[178:179],v2 offset:2048\n"
"ds_write_addtid_b32 v164 offset:52224\n"
MF "v[52:55],a[136:139],v[188:191],v[52:55]\n"
LD "v199,s[48:51],s89 offen offset:3072 lds\n"
MF "v[44:47],a[132:135],v[188:191],v[44:47]\n"
"s_nop 0\n"
TR "v[180:181],v2 offset:4096\n"
"ds_write_addtid_b32 v165 offset:52480\n"
MF "v[44:47],a[172:175],a[240:243],v[44:47]\n"
"s_nop 0\n"
TR "v[182:183],v2 offset:6144\n"
"ds_write_addtid_b32 v166 offset:52736\n"
MF "v[44:47],a[212:215],a[244:247],v[44:47]\n"
"s_nop 0\n"
"ds_write_addtid_b32 v167 offset:52992\n"
"ds_write_addtid_b32 v168 offset:53248\n"
MF "v[36:39],a[208:211],a[244:247],v[36:39]\n"
"s_nop 0\n"
TR "v[184:185],v2 offset:8192\n"
"ds_write_addtid_b32 v169 offset:53504\n"
MF "v[36:39],a[168:171],a[240:243],v[36:39]\n"
"s_add_u32 m0,s79,32768\n"
TR "v[186:187],v2 offset:10240\n"
"ds_write_addtid_b32 v170 offset:49664\n"
MF "v[36:39],a[128:131],v[188:191],v[36:39]\n"
LD "v200,s[48:51],s89 offen lds\n"
MF "v[28:31],a[124:127],v[188:191],v[28:31]\n"
"s_nop 0\n"
"ds_write_addtid_b32 v171 offset:49920\n"
"ds_write_addtid_b32 v172 offset:50176\n"
MF "v[28:31],a[164:167],a[240:243],v[28:31]\n"
"s_nop 0\n"
"ds_write_addtid_b32 v173 offset:50432\n"
"ds_write_addtid_b32 v174 offset:50688\n"
MF "v[28:31],a[204:207],a[244:247],v[28:31]\n"
"ds_write_addtid_b32 v175 offset:50944\n"
MF "v[20:23],a[200:203],a[244:247],v[20:23]\n"
LD "v201,s[48:51],s89 offen offset:1024 lds\n"
MF "v[20:23],a[160:163],a[240:243],v[20:23]\n"
MF "v[20:23],a[120:123],v[188:191],v[20:23]\n"
"s_waitcnt lgkmcnt(6)\n"
MF "v[56:59],a[120:123],v[176:179],v[56:59]\n"
TR "v[188:189],v3\n"
MF "v[56:59],a[160:163],v[180:183],v[56:59]\n"
TR "v[190:191],v3 offset:2048\n"
MF "v[56:59],a[200:203],v[184:187],v[56:59]\n"
LD "v202,s[48:51],s89 offen offset:2048 lds\n"
MF "v[64:67],a[204:207],v[184:187],v[64:67]\n"
TR "a[240:241],v3 offset:4096\n"
MF "v[64:67],a[164:167],v[180:183],v[64:67]\n"
TR "a[242:243],v3 offset:6144\n"
MF "v[64:67],a[124:127],v[176:179],v[64:67]\n"
LD "v203,s[48:51],s89 offen offset:3072 lds\n"
MF "v[72:75],a[128:131],v[176:179],v[72:75]\n"
TR "a[244:245],v3 offset:8192\n"
MF "v[72:75],a[168:171],v[180:183],v[72:75]\n"
TR "a[246:247],v3 offset:10240\n"
MF "v[72:75],a[208:211],v[184:187],v[72:75]\n"
MF "v[80:83],a[212:215],v[184:187],v[80:83]\n"
MF "v[80:83],a[172:175],v[180:183],v[80:83]\n"
MF "v[80:83],a[132:135],v[176:179],v[80:83]\n"
MF "v[88:91],a[136:139],v[176:179],v[88:91]\n"
MF "v[88:91],a[176:179],v[180:183],v[88:91]\n"
MF "v[88:91],a[216:219],v[184:187],v[88:91]\n"
"s_waitcnt vmcnt(28) lgkmcnt(0)\n"
"s_barrier\n"
MF "v[92:95],a[216:219],a[244:247],v[92:95]\n"
"s_add_u32 s80,s89,122880\n"
TR "v[176:177],v0 offset:49152\n"
MF "v[92:95],a[176:179],a[240:243],v[92:95]\n"
"s_add_u32 m0,s79,0\n"
"s_mul_i32 s95,s92,3277\n"
TR "v[178:179],v0 offset:51200\n"
MF "v[92:95],a[136:139],v[188:191],v[92:95]\n"
LD "v192,s[48:51],s80 offen lds\n"
MF "v[84:87],a[132:135],v[188:191],v[84:87]\n"
"s_lshr_b32 s95,s95,16\n"
"s_mul_i32 s59,s95,20\n"
TR "v[180:181],v0 offset:53248\n"
MF "v[84:87],a[172:175],a[240:243],v[84:87]\n"
"s_sub_u32 s59,s92,s59\n"
TR "v[182:183],v0 offset:55296\n"
MF "v[84:87],a[212:215],a[244:247],v[84:87]\n"
LD "v193,s[48:51],s80 offen offset:1024 lds\n"
MF "v[76:79],a[208:211],a[244:247],v[76:79]\n"
"s_cmp_ge_u32 s59,10\n"
"s_cselect_b32 s96,4294557760,0\n"
TR "v[184:185],v0 offset:57344\n"
MF "v[76:79],a[168:171],a[240:243],v[76:79]\n"
"s_add_u32 s96,s96,1024\n"
"s_mul_i32 s59,s59,40960\n"
TR "v[186:187],v0 offset:59392\n"
MF "v[76:79],a[128:131],v[188:191],v[76:79]\n"
"s_add_u32 s96,s96,s59\n"
"s_lshl_b32 s59,s95,7\n"
"s_add_u32 s96,s96,s59\n"
MF "v[68:71],a[124:127],v[188:191],v[68:71]\n"
"s_add_u32 s95,s95,3\n"
"s_cmp_ge_u32 s92,40\n"
"s_cselect_b32 s95,99,s95\n"
MF "v[68:71],a[164:167],a[240:243],v[68:71]\n"
LD "v194,s[48:51],s80 offen offset:2048 lds\n"
MF "v[68:71],a[204:207],a[244:247],v[68:71]\n"
"s_cmp_ge_u32 s85,5\n"
"s_cselect_b32 s95,99,s95\n"
"s_cselect_b32 s72,s85,0\n"
MF "v[60:63],a[200:203],a[244:247],v[60:63]\n"
MF "v[60:63],a[160:163],a[240:243],v[60:63]\n"
MF "v[60:63],a[120:123],v[188:191],v[60:63]\n"
"s_waitcnt lgkmcnt(0)\n"
LD "v195,s[48:51],s80 offen offset:3072 lds\n"
MF "v[16:19],a[20:23],v[176:179],v[16:19]\n"
TR "v[188:189],v1 offset:49152\n"
MF "v[16:19],a[60:63],v[180:183],v[16:19]\n"
TR "v[190:191],v1 offset:51200\n"
MF "v[16:19],a[100:103],v[184:187],v[16:19]\n"
MF "v[24:27],a[104:107],v[184:187],v[24:27]\n"
TR "a[240:241],v1 offset:53248\n"
MF "v[24:27],a[64:67],v[180:183],v[24:27]\n"
"s_add_u32 m0,s79,4096\n"
TR "a[242:243],v1 offset:55296\n"
MF "v[24:27],a[24:27],v[176:179],v[24:27]\n"
LD "v196,s[48:51],s80 offen lds\n"
MF "v[32:35],a[28:31],v[176:179],v[32:35]\n"
TR "a[244:245],v1 offset:57344\n"
MF "v[32:35],a[68:71],v[180:183],v[32:35]\n"
TR "a[246:247],v1 offset:59392\n"
MF "v[32:35],a[108:111],v[184:187],v[32:35]\n"
LD "v197,s[48:51],s80 offen offset:1024 lds\n"
MF "v[40:43],a[112:115],v[184:187],v[40:43]\n"
MF "v[40:43],a[72:75],v[180:183],v[40:43]\n"
MF "v[40:43],a[32:35],v[176:179],v[40:43]\n"
MF "v[48:51],a[36:39],v[176:179],v[48:51]\n"
MF "v[48:51],a[76:79],v[180:183],v[48:51]\n"
LD "v198,s[48:51],s80 offen offset:2048 lds\n"
MF "v[48:51],a[116:119],v[184:187],v[48:51]\n"
"s_waitcnt lgkmcnt(0)\n"
MF "v[52:55],a[116:119],a[244:247],v[52:55]\n"
TR "v[176:177],v2 offset:49152\n"
MF "v[52:55],a[76:79],a[240:243],v[52:55]\n"
TR "v[178:179],v2 offset:51200\n"
MF "v[52:55],a[36:39],v[188:191],v[52:55]\n"
LD "v199,s[48:51],s80 offen offset:3072 lds\n"
MF "v[44:47],a[32:35],v[188:191],v[44:47]\n"
TR "v[180:181],v2 offset:53248\n"
MF "v[44:47],a[72:75],a[240:243],v[44:47]\n"
TR "v[182:183],v2 offset:55296\n"
MF "v[44:47],a[112:115],a[244:247],v[44:47]\n"
MF "v[36:39],a[108:111],a[244:247],v[36:39]\n"
TR "v[184:185],v2 offset:57344\n"
MF "v[36:39],a[68:71],a[240:243],v[36:39]\n"
"s_add_u32 m0,s79,8192\n"
TR "v[186:187],v2 offset:59392\n"
MF "v[36:39],a[28:31],v[188:191],v[36:39]\n"
LD "v200,s[48:51],s80 offen lds\n"
MF "v[28:31],a[24:27],v[188:191],v[28:31]\n"
MF "v[28:31],a[64:67],a[240:243],v[28:31]\n"
MF "v[28:31],a[104:107],a[244:247],v[28:31]\n"
MF "v[20:23],a[100:103],a[244:247],v[20:23]\n"
LD "v201,s[48:51],s80 offen offset:1024 lds\n"
MF "v[20:23],a[60:63],a[240:243],v[20:23]\n"
"s_waitcnt vmcnt(19)\n"
"s_cmp_eq_u32 s83,0xf0000000\n"
MF "v[20:23],a[20:23],v[188:191],v[20:23]\n"
"s_cbranch_scc1 .Ljs7\n"
"s_cmp_eq_u32 s83,0xf0000010\n"
"s_cbranch_scc1 .Ljc7\n"
MF "v[248:251],v[210:213],a[250:253],0\n"
"v_mov_b32 v240,v208\n"
"v_mov_b32 v241,v209\n"
MF "v[244:247],v[214:217],a[250:253],0\n"
MF "v[248:251],v[218:221],a[250:253],v[248:251]\n"
MF "v[244:247],v[222:225],a[250:253],v[244:247]\n"
MF "v[248:251],v[226:229],a[250:253],v[248:251]\n"
MF "v[244:247],v[230:233],a[250:253],v[244:247]\n"
MF "v[248:251],v[234:237],a[250:253],v[248:251]\n"
MF "v[244:247],v[238:241],a[250:253],v[244:247]\n"
"s_branch .Ljs7\n"
".Ljc7:\n"
MF "v[16:19],a[252:255],v[208:211],v[16:19]\n"
MF "v[24:27],a[252:255],v[212:215],v[24:27]\n"
MF "v[32:35],a[252:255],v[216:219],v[32:35]\n"
MF "v[40:43],a[252:255],v[220:223],v[40:43]\n"
MF "v[48:51],a[252:255],v[224:227],v[48:51]\n"
MF "v[20:23],a[248:251],v[208:211],v[20:23]\n"
MF "v[28:31],a[248:251],v[212:215],v[28:31]\n"
MF "v[36:39],a[248:251],v[216:219],v[36:39]\n"
MF "v[44:47],a[248:251],v[220:223],v[44:47]\n"
MF "v[52:55],a[248:251],v[224:227],v[52:55]\n"
".Ljs7:\n"
"s_waitcnt lgkmcnt(0)\n"
MF "v[56:59],a[20:23],v[176:179],v[56:59]\n"
TR "v[188:189],v3 offset:49152\n"
"v_sub_u32 v10,v10,v11\n"
MF "v[56:59],a[60:63],v[180:183],v[56:59]\n"
"s_lshl_b32 s82,s92,10\n"
TR "v[190:191],v3 offset:51200\n"
"v_cmp_gt_u32 vcc,s95,v10\n"
MF "v[56:59],a[100:103],v[184:187],v[56:59]\n"
LD "v202,s[48:51],s80 offen offset:2048 lds\n"
MF "v[64:67],a[104:107],v[184:187],v[64:67]\n"
"s_add_u32 s82,s82,s93\n"
TR "a[240:241],v3 offset:53248\n"
CV "v244,v244,v245\n"
MF "v[64:67],a[64:67],v[180:183],v[64:67]\n"
TR "a[242:243],v3 offset:55296\n"
CV "v245,v246,v247\n"
MF "v[64:67],a[24:27],v[176:179],v[64:67]\n"
LD "v203,s[48:51],s80 offen offset:3072 lds\n"
MF "v[72:75],a[28:31],v[176:179],v[72:75]\n"
TR "a[244:245],v3 offset:57344\n"
CV "v246,v248,v249\n"
MF "v[72:75],a[68:71],v[180:183],v[72:75]\n"
TR "a[246:247],v3 offset:59392\n"
CV "v247,v250,v251\n"
MF "v[72:75],a[108:111],v[184:187],v[72:75]\n"
"s_cmp_eq_u64 vcc,0\n"
"s_cselect_b32 s82,s82,0xf0000000\n"
"s_cselect_b32 s84,s96,0xf0000000\n"
"s_cselect_b32 s59,32,0\n"
MF "v[80:83],a[112:115],v[184:187],v[80:83]\n"
"s_add_u32 s92,s92,s59\n"
".Lnf1:\n"
"v_readlane_b32 s58,v10,s67\n"
"s_cmp_lt_u32 s58,s72\n"
"s_cbranch_scc1 .Lsp1\n"
".Lnb1:\n"
MF "v[80:83],a[72:75],v[180:183],v[80:83]\n"
"s_cmp_ge_u32 s85,5\n"
"s_cselect_b32 s82,s70,s82\n"
"s_cselect_b32 s84,0xf0000010,s84\n"
MF "v[80:83],a[32:35],v[176:179],v[80:83]\n"
"s_cmp_ge_u32 s85,5\n"
"s_cselect_b32 s71,1024,s71\n"
"s_cselect_b32 s66,0x80000000,s66\n"
MF "v[88:91],a[36:39],v[176:179],v[88:91]\n"
"s_add_u32 s70,s70,20480\n"
"v_permlane16_swap_b32 v244,v246\n"
"v_permlane16_swap_b32 v245,v247\n"
MF "v[88:91],a[76:79],v[180:183],v[88:91]\n"
"buffer_store_dwordx4 v[244:247],v8,s[44:47],s83 offen sc1\n"
MF "v[88:91],a[116:119],v[184:187],v[88:91]\n"
"s_waitcnt vmcnt(26) lgkmcnt(0)\n"
"s_barrier\n"
MF "v[92:95],a[116:119],a[244:247],v[92:95]\n"
"s_add_u32 s88,s88,s65\n"
"s_mov_b32 s83,s84\n"
TR "v[176:177],v4\n"
MF "v[92:95],a[76:79],a[240:243],v[92:95]\n"
"s_add_u32 m0,s79,49152\n"
TR "v[178:179],v4 offset:2048\n"
MF "v[92:95],a[36:39],v[188:191],v[92:95]\n"
LD "v192,s[48:51],s88 offen lds\n"
MF "v[84:87],a[32:35],v[188:191],v[84:87]\n"
TR "v[180:181],v4 offset:4096\n"
MF "v[84:87],a[72:75],a[240:243],v[84:87]\n"
TR "v[182:183],v4 offset:6144\n"
MF "v[84:87],a[112:115],a[244:247],v[84:87]\n"
LD "v193,s[48:51],s88 offen offset:1024 lds\n"
MF "v[76:79],a[108:111],a[244:247],v[76:79]\n"
TR "v[184:185],v4 offset:8192\n"
MF "v[76:79],a[68:71],a[240:243],v[76:79]\n"
TR "v[186:187],v4 offset:10240\n"
MF "v[76:79],a[28:31],v[188:191],v[76:79]\n"
LD "v[208:211],v9,s[40:43],s82 offen sc1\n"
MF "v[68:71],a[24:27],v[188:191],v[68:71]\n"
MF "v[68:71],a[64:67],a[240:243],v[68:71]\n"
LD "v194,s[48:51],s88 offen offset:2048 lds\n"
MF "v[68:71],a[104:107],a[244:247],v[68:71]\n"
MF "v[60:63],a[100:103],a[244:247],v[60:63]\n"
MF "v[60:63],a[60:63],a[240:243],v[60:63]\n"
MF "v[60:63],a[20:23],v[188:191],v[60:63]\n"
"s_waitcnt lgkmcnt(0)\n"
LD "v195,s[48:51],s88 offen offset:3072 lds\n"
MF "v[16:19],a[140:143],v[176:179],v[16:19]\n"
TR "v[188:189],v5\n"
MF "v[16:19],a[180:183],v[180:183],v[16:19]\n"
"s_add_u32 s82,s82,s71\n"
TR "v[190:191],v5 offset:2048\n"
MF "v[16:19],a[220:223],v[184:187],v[16:19]\n"
LD "v[212:215],v9,s[40:43],s82 offen sc1\n"
MF "v[24:27],a[224:227],v[184:187],v[24:27]\n"
TR "a[240:241],v5 offset:4096\n"
MF "v[24:27],a[184:187],v[180:183],v[24:27]\n"
"s_add_u32 m0,s79,53248\n"
TR "a[242:243],v5 offset:6144\n"
MF "v[24:27],a[144:147],v[176:179],v[24:27]\n"
LD "v196,s[48:51],s88 offen lds\n"
MF "v[32:35],a[148:151],v[176:179],v[32:35]\n"
TR "a[244:245],v5 offset:8192\n"
MF "v[32:35],a[188:191],v[180:183],v[32:35]\n"
TR "a[246:247],v5 offset:10240\n"
MF "v[32:35],a[228:231],v[184:187],v[32:35]\n"
LD "v197,s[48:51],s88 offen offset:1024 lds\n"
MF "v[40:43],a[232:235],v[184:187],v[40:43]\n"
"s_add_u32 s82,s82,s71\n"
MF "v[40:43],a[192:195],v[180:183],v[40:43]\n"
LD "v[216:219],v9,s[40:43],s82 offen sc1\n"
MF "v[40:43],a[152:155],v[176:179],v[40:43]\n"
MF "v[48:51],a[156:159],v[176:179],v[48:51]\n"
MF "v[48:51],a[196:199],v[180:183],v[48:51]\n"
LD "v198,s[48:51],s88 offen offset:2048 lds\n"
MF "v[48:51],a[236:239],v[184:187],v[48:51]\n"
"s_waitcnt lgkmcnt(0)\n"
MF "v[52:55],a[236:239],a[244:247],v[52:55]\n"
TR "v[176:177],v6\n"
MF "v[52:55],a[196:199],a[240:243],v[52:55]\n"
TR "v[178:179],v6 offset:2048\n"
MF "v[52:55],a[156:159],v[188:191],v[52:55]\n"
LD "v199,s[48:51],s88 offen offset:3072 lds\n"
MF "v[44:47],a[152:155],v[188:191],v[44:47]\n"
TR "v[180:181],v6 offset:4096\n"
MF "v[44:47],a[192:195],a[240:243],v[44:47]\n"
"s_add_u32 s82,s82,s71\n"
TR "v[182:183],v6 offset:6144\n"
MF "v[44:47],a[232:235],a[244:247],v[44:47]\n"
LD "v[220:223],v9,s[40:43],s82 offen sc1\n"
MF "v[36:39],a[228:231],a[244:247],v[36:39]\n"
TR "v[184:185],v6 offset:8192\n"
MF "v[36:39],a[188:191],a[240:243],v[36:39]\n"
"s_add_u32 m0,s79,57344\n"
TR "v[186:187],v6 offset:10240\n"
MF "v[36:39],a[148:151],v[188:191],v[36:39]\n"
LD "v200,s[48:51],s88 offen lds\n"
MF "v[28:31],a[144:147],v[188:191],v[28:31]\n"
MF "v[28:31],a[184:187],a[240:243],v[28:31]\n"
"s_add_u32 s82,s82,s71\n"
MF "v[28:31],a[224:227],a[244:247],v[28:31]\n"
LD "v[224:227],v9,s[40:43],s82 offen sc1\n"
MF "v[20:23],a[220:223],a[244:247],v[20:23]\n"
LD "v201,s[48:51],s88 offen offset:1024 lds\n"
MF "v[20:23],a[180:183],a[240:243],v[20:23]\n"
MF "v[20:23],a[140:143],v[188:191],v[20:23]\n"
"s_waitcnt lgkmcnt(0)\n"
MF "v[56:59],a[140:143],v[176:179],v[56:59]\n"
TR "v[188:189],v7\n"
MF "v[56:59],a[180:183],v[180:183],v[56:59]\n"
TR "v[190:191],v7 offset:2048\n"
MF "v[56:59],a[220:223],v[184:187],v[56:59]\n"
LD "v202,s[48:51],s88 offen offset:2048 lds\n"
MF "v[64:67],a[224:227],v[184:187],v[64:67]\n"
TR "a[240:241],v7 offset:4096\n"
MF "v[64:67],a[184:187],v[180:183],v[64:67]\n"
TR "a[242:243],v7 offset:6144\n"
MF "v[64:67],a[144:147],v[176:179],v[64:67]\n"
LD "v203,s[48:51],s88 offen offset:3072 lds\n"
MF "v[72:75],a[148:151],v[176:179],v[72:75]\n"
TR "a[244:245],v7 offset:8192\n"
MF "v[72:75],a[188:191],v[180:183],v[72:75]\n"
"s_add_u32 s82,s82,s66\n"
TR "a[246:247],v7 offset:10240\n"
MF "v[72:75],a[228:231],v[184:187],v[72:75]\n"
LD "v[228:231],v9,s[40:43],s82 offen sc1\n"
MF "v[80:83],a[232:235],v[184:187],v[80:83]\n"
MF "v[80:83],a[192:195],v[180:183],v[80:83]\n"
MF "v[80:83],a[152:155],v[176:179],v[80:83]\n"
MF "v[88:91],a[156:159],v[176:179],v[88:91]\n"
"s_add_u32 s82,s82,s71\n"
MF "v[88:91],a[196:199],v[180:183],v[88:91]\n"
LD "v[232:235],v9,s[40:43],s82 offen sc1\n"
MF "v[88:91],a[236:239],v[184:187],v[88:91]\n"
"s_waitcnt vmcnt(32) lgkmcnt(0)\n"
"s_barrier\n"
MF "v[92:95],a[236:239],a[244:247],v[92:95]\n"
"s_add_u32 s80,s88,122880\n"
TR "v[176:177],v0 offset:24576\n"
MF "v[92:95],a[196:199],a[240:243],v[92:95]\n"
"s_add_u32 m0,s79,98304\n"
"s_add_u32 s85,s85,1\n"
TR "v[178:179],v0 offset:26624\n"
MF "v[92:95],a[156:159],v[188:191],v[92:95]\n"
LD "v192,s[48:51],s80 offen lds\n"
MF "v[84:87],a[152:155],v[188:191],v[84:87]\n"
"s_nop 0\n"
TR "v[180:181],v0 offset:28672\n"
"ds_write_addtid_b32 v16 offset:24576\n"
MF "v[84:87],a[192:195],a[240:243],v[84:87]\n"
"s_nop 0\n"
TR "v[182:183],v0 offset:30720\n"
"ds_write_addtid_b32 v17 offset:24832\n"
MF "v[84:87],a[232:235],a[244:247],v[84:87]\n"
LD "v193,s[48:51],s80 offen offset:1024 lds\n"
MF "v[76:79],a[228:231],a[244:247],v[76:79]\n"
"s_nop 0\n"
TR "v[184:185],v0 offset:32768\n"
"ds_write_addtid_b32 v18 offset:25088\n"
MF "v[76:79],a[188:191],a[240:243],v[76:79]\n"
"s_add_u32 s82,s82,s71\n"
TR "v[186:187],v0 offset:34816\n"
"ds_write_addtid_b32 v19 offset:25344\n"
MF "v[76:79],a[148:151],v[188:191],v[76:79]\n"
LD "v[236:239],v9,s[40:43],s82 offen sc1\n"
MF "v[68:71],a[144:147],v[188:191],v[68:71]\n"
LD "v194,s[48:51],s80 offen offset:2048 lds\n"
MF "v[68:71],a[184:187],a[240:243],v[68:71]\n"
"s_nop 0\n"
"ds_write_addtid_b32 v20 offset:25600\n"
"ds_write_addtid_b32 v21 offset:25856\n"
MF "v[68:71],a[224:227],a[244:247],v[68:71]\n"
"s_nop 0\n"
"ds_write_addtid_b32 v22 offset:26112\n"
"ds_write_addtid_b32 v23 offset:26368\n"
MF "v[60:63],a[220:223],a[244:247],v[60:63]\n"
"s_nop 0\n"
"ds_write_addtid_b32 v24 offset:26624\n"
"ds_write_addtid_b32 v25 offset:26880\n"
MF "v[60:63],a[180:183],a[240:243],v[60:63]\n"
"s_nop 0\n"
"ds_write_addtid_b32 v26 offset:27136\n"
"ds_write_addtid_b32 v27 offset:27392\n"
MF "v[60:63],a[140:143],v[188:191],v[60:63]\n"
"s_waitcnt lgkmcnt(9)\n"
"s_cmp_lg_u32 s85,9\n"
"s_cbranch_scc1 .Lpl0\n"
".Lplast0:\n"
MF "v[96:99],v[176:179],a[0:3],v[252:255]\n"
"s_nop 0\n"
TR "v[188:189],v1 offset:24576\n"
TR "v[190:191],v1 offset:26624\n"
MF "v[96:99],v[180:183],a[40:43],v[96:99]\n"
"s_nop 0\n"
TR "a[240:241],v1 offset:28672\n"
TR "a[242:243],v1 offset:30720\n"
MF "v[96:99],v[184:187],a[80:83],v[96:99]\n"
"s_add_u32 s90,s91,163840\n"
TR "a[244:245],v1 offset:32768\n"
TR "a[246:247],v1 offset:34816\n"
MF "v[104:107],v[184:187],a[84:87],v[252:255]\n"
LD "v195,s[48:51],s80 offen offset:3072 lds\n"
MF "v[104:107],v[180:183],a[44:47],v[104:107]\n"
"s_nop 0\n"
"ds_write_addtid_b32 v28 offset:27648\n"
"ds_write_addtid_b32 v29 offset:27904\n"
MF "v[104:107],v[176:179],a[4:7],v[104:107]\n"
"s_nop 0\n"
"ds_write_addtid_b32 v30 offset:28160\n"
"ds_write_addtid_b32 v31 offset:28416\n"
MF "v[112:115],v[176:179],a[8:11],v[252:255]\n"
"s_nop 0\n"
"ds_write_addtid_b32 v32 offset:28672\n"
"ds_write_addtid_b32 v33 offset:28928\n"
MF "v[112:115],v[180:183],a[48:51],v[112:115]\n"
"s_add_u32 m0,s79,102400\n"
"s_nop 0\n"
"ds_write_addtid_b32 v34 offset:25088\n"
"ds_write_addtid_b32 v35 offset:25344\n"
MF "v[112:115],v[184:187],a[88:91],v[112:115]\n"
LD "v196,s[48:51],s80 offen lds\n"
MF "v[120:123],v[184:187],a[92:95],v[252:255]\n"
"s_nop 0\n"
"ds_write_addtid_b32 v36 offset:25600\n"
"ds_write_addtid_b32 v37 offset:25856\n"
MF "v[120:123],v[180:183],a[52:55],v[120:123]\n"
"ds_write_addtid_b32 v38 offset:26112\n"
MF "v[120:123],v[176:179],a[12:15],v[120:123]\n"
"ds_write_addtid_b32 v39 offset:26368\n"
MF "v[128:131],v[176:179],a[16:19],v[252:255]\n"
"ds_write_addtid_b32 v40 offset:26624\n"
MF "v[128:131],v[180:183],a[56:59],v[128:131]\n"
"ds_write_addtid_b32 v41 offset:26880\n"
MF "v[128:131],v[184:187],a[96:99],v[128:131]\n"
"s_waitcnt lgkmcnt(14)\n"
LD "v197,s[48:51],s80 offen offset:1024 lds\n"
MF "v[132:135],a[244:247],a[96:99],v[252:255]\n"
"s_nop 0\n"
TR "v[176:177],v2 offset:24576\n"
TR "v[178:179],v2 offset:26624\n"
MF "v[132:135],a[240:243],a[56:59],v[132:135]\n"
"s_nop 0\n"
TR "v[180:181],v2 offset:28672\n"
TR "v[182:183],v2 offset:30720\n"
MF "v[132:135],v[188:191],a[16:19],v[132:135]\n"
"s_nop 0\n"
TR "v[184:185],v2 offset:32768\n"
TR "v[186:187],v2 offset:34816\n"
MF "v[124:127],v[188:191],a[12:15],v[252:255]\n"
"s_nop 0\n"
"ds_write_addtid_b32 v42 offset:27136\n"
"ds_write_addtid_b32 v43 offset:27392\n"
MF "v[124:127],a[240:243],a[52:55],v[124:127]\n"
"s_nop 0\n"
"ds_write_addtid_b32 v44 offset:27648\n"
"ds_write_addtid_b32 v45 offset:27904\n"
MF "v[124:127],a[244:247],a[92:95],v[124:127]\n"
"s_nop 0\n"
"ds_write_addtid_b32 v46 offset:28160\n"
"ds_write_addtid_b32 v47 offset:28416\n"
MF "v[116:119],a[244:247],a[88:91],v[252:255]\n"
LD "v198,s[48:51],s80 offen offset:2048 lds\n"
MF "v[116:119],a[240:243],a[48:51],v[116:119]\n"
"s_nop 0\n"
"ds_write_addtid_b32 v48 offset:28672\n"
"ds_write_addtid_b32 v49 offset:28928\n"
MF "v[116:119],v[188:191],a[8:11],v[116:119]\n"
"s_nop 0\n"
"ds_write_addtid_b32 v50 offset:29184\n"
"ds_write_addtid_b32 v51 offset:29440\n"
MF "v[108:111],v[188:191],a[4:7],v[252:255]\n"
"ds_write_addtid_b32 v52 offset:29696\n"
MF "v[108:111],a[240:243],a[44:47],v[108:111]\n"
"ds_write_addtid_b32 v53 offset:29952\n"
MF "v[108:111],a[244:247],a[84:87],v[108:111]\n"
"ds_write_addtid_b32 v54 offset:30208\n"
MF "v[100:103],a[244:247],a[80:83],v[252:255]\n"
"ds_write_addtid_b32 v55 offset:30464\n"
MF "v[100:103],a[240:243],a[40:43],v[100:103]\n"
LD "v199,s[48:51],s80 offen offset:3072 lds\n"
MF "v[100:103],v[188:191],a[0:3],v[100:103]\n"
"s_waitcnt lgkmcnt(14)\n"
MF "v[136:139],v[176:179],a[0:3],v[252:255]\n"
"s_nop 0\n"
TR "v[188:189],v3 offset:24576\n"
TR "v[190:191],v3 offset:26624\n"
MF "v[136:139],v[180:183],a[40:43],v[136:139]\n"
"s_nop 0\n"
TR "a[240:241],v3 offset:28672\n"
TR "a[242:243],v3 offset:30720\n"
MF "v[136:139],v[184:187],a[80:83],v[136:139]\n"
"s_add_u32 m0,s79,106496\n"
TR "a[244:245],v3 offset:32768\n"
TR "a[246:247],v3 offset:34816\n"
MF "v[144:147],v[184:187],a[84:87],v[252:255]\n"
"s_waitcnt lgkmcnt(0)\n"
"s_barrier\n"
MF "v[144:147],v[180:183],a[44:47],v[144:147]\n"
LD "v200,s[48:51],s80 offen lds\n"
MF "v[144:147],v[176:179],a[4:7],v[144:147]\n"
"ds_read2st64_b32 v[16:17],v13 offset0:0 offset1:1\n"
MF "v[152:155],v[176:179],a[8:11],v[252:255]\n"
"ds_read2st64_b32 v[18:19],v13 offset0:2 offset1:3\n"
MF "v[152:155],v[180:183],a[48:51],v[152:155]\n"
"ds_read2st64_b32 v[20:21],v13 offset0:4 offset1:5\n"
MF "v[152:155],v[184:187],a[88:91],v[152:155]\n"
"ds_read2st64_b32 v[22:23],v13 offset0:6 offset1:7\n"
MF "v[160:163],v[184:187],a[92:95],v[252:255]\n"
"ds_read2st64_b32 v[24:25],v13 offset0:8 offset1:9\n"
MF "v[160:163],v[180:183],a[52:55],v[160:163]\n"
"ds_read2st64_b32 v[26:27],v13 offset0:10 offset1:11\n"
MF "v[160:163],v[176:179],a[12:15],v[160:163]\n"
LD "v201,s[48:51],s80 offen offset:1024 lds\n"
MF "v[168:171],v[176:179],a[16:19],v[252:255]\n"
"ds_read2st64_b32 v[28:29],v13 offset0:12 offset1:13\n"
MF "v[168:171],v[180:183],a[56:59],v[168:171]\n"
"s_waitcnt lgkmcnt(3)\n"
"ds_read2st64_b32 v[30:31],v13 offset0:14 offset1:15\n"
"v_add_f32 v56,v56,v16\n"
MF "v[168:171],v[184:187],a[96:99],v[168:171]\n"
"s_waitcnt vmcnt(31) lgkmcnt(0)\n"
"s_barrier\n"
MF "v[172:175],a[244:247],a[96:99],v[252:255]\n"
"s_nop 0\n"
TR "v[176:177],v0\n"
TR "v[178:179],v0 offset:2048\n"
MF "v[172:175],a[240:243],a[56:59],v[172:175]\n"
"s_nop 0\n"
TR "v[180:181],v0 offset:4096\n"
TR "v[182:183],v0 offset:6144\n"
MF "v[172:175],v[188:191],a[16:19],v[172:175]\n"
"s_nop 0\n"
TR "v[184:185],v0 offset:8192\n"
TR "v[186:187],v0 offset:10240\n"
MF "v[164:167],v[188:191],a[12:15],v[252:255]\n"
LD "v202,s[48:51],s80 offen offset:2048 lds\n"
MF "v[164:167],a[240:243],a[52:55],v[164:167]\n"
"ds_read2st64_b32 v[32:33],v13 offset0:16 offset1:17\n"
"v_add_f32 v57,v57,v17\n"
MF "v[164:167],a[244:247],a[92:95],v[164:167]\n"
"ds_read2st64_b32 v[34:35],v13 offset0:18 offset1:19\n"
"v_add_f32 v58,v58,v18\n"
MF "v[156:159],a[244:247],a[88:91],v[252:255]\n"
"ds_read2st64_b32 v[36:37],v13 offset0:20 offset1:21\n"
"v_add_f32 v59,v59,v19\n"
MF "v[156:159],a[240:243],a[48:51],v[156:159]\n"
"ds_read2st64_b32 v[38:39],v13 offset0:22 offset1:23\n"
"v_add_f32 v60,v60,v20\n"
MF "v[156:159],v[188:191],a[8:11],v[156:159]\n"
"ds_read2st64_b32 v[40:41],v13 offset0:24 offset1:25\n"
"v_add_f32 v61,v61,v21\n"
MF "v[148:151],v[188:191],a[4:7],v[252:255]\n"
LD "v203,s[48:51],s80 offen offset:3072 lds\n"
MF "v[148:151],a[240:243],a[44:47],v[148:151]\n"
"ds_read2st64_b32 v[42:43],v13 offset0:26 offset1:27\n"
"v_add_f32 v62,v62,v22\n"
MF "v[148:151],a[244:247],a[84:87],v[148:151]\n"
"ds_read2st64_b32 v[44:45],v13 offset0:28 offset1:29\n"
"v_add_f32 v63,v63,v23\n"
MF "v[140:143],a[244:247],a[80:83],v[252:255]\n"
"ds_read2st64_b32 v[46:47],v13 offset0:30 offset1:31\n"
CV "v56,v56,v57\n"
MF "v[140:143],a[240:243],a[40:43],v[140:143]\n"
"ds_read2st64_b32 v[48:49],v13 offset0:32 offset1:33\n"
CV "v57,v58,v59\n"
MF "v[140:143],v[188:191],a[0:3],v[140:143]\n"
"s_waitcnt lgkmcnt(9)\n"
"ds_read2st64_b32 v[50:51],v13 offset0:34 offset1:35\n"
CV "v58,v60,v61\n"
MF "v[96:99],v[176:179],a[120:123],v[96:99]\n"
"s_nop 0\n"
TR "v[188:189],v1\n"
TR "v[190:191],v1 offset:2048\n"
MF "v[96:99],v[180:183],a[160:163],v[96:99]\n"
"s_nop 0\n"
TR "a[240:241],v1 offset:4096\n"
TR "a[242:243],v1 offset:6144\n"
MF "v[96:99],v[184:187],a[200:203],v[96:99]\n"
"s_nop 0\n"
TR "a[244:245],v1 offset:8192\n"
TR "a[246:247],v1 offset:10240\n"
MF "v[104:107],v[184:187],a[204:207],v[104:107]\n"
"ds_read2st64_b32 v[52:53],v13 offset0:36 offset1:37\n"
CV "v59,v62,v63\n"
MF "v[104:107],v[180:183],a[164:167],v[104:107]\n"
"s_add_u32 s87,s90,10240\n"
"ds_read2st64_b32 v[54:55],v13 offset0:38 offset1:39\n"
"v_add_f32 v64,v64,v24\n"
MF "v[104:107],v[176:179],a[124:127],v[104:107]\n"
"buffer_store_dwordx4 v[56:59],v9,s[40:43],s87 offen sc1\n"
MF "v[112:115],v[176:179],a[128:131],v[112:115]\n"
"v_add_f32 v65,v65,v25\n"
"v_add_f32 v66,v66,v26\n"
MF "v[112:115],v[180:183],a[168:171],v[112:115]\n"
"v_add_f32 v67,v67,v27\n"
"v_add_f32 v68,v68,v28\n"
MF "v[112:115],v[184:187],a[208:211],v[112:115]\n"
"v_add_f32 v69,v69,v29\n"
"v_add_f32 v70,v70,v30\n"
MF "v[120:123],v[184:187],a[212:215],v[120:123]\n"
"v_add_f32 v71,v71,v31\n"
CV "v64,v64,v65\n"
MF "v[120:123],v[180:183],a[172:175],v[120:123]\n"
CV "v65,v66,v67\n"
CV "v66,v68,v69\n"
MF "v[120:123],v[176:179],a[132:135],v[120:123]\n"
"s_add_u32 s87,s90,11264\n"
"s_waitcnt lgkmcnt(14)\n"
CV "v67,v70,v71\n"
MF "v[128:131],v[176:179],a[136:139],v[128:131]\n"
"buffer_store_dwordx4 v[64:67],v9,s[40:43],s87 offen sc1\n"
MF "v[128:131],v[180:183],a[176:179],v[128:131]\n"
"v_add_f32 v72,v72,v32\n"
"v_add_f32 v73,v73,v33\n"
MF "v[128:131],v[184:187],a[216:219],v[128:131]\n"
"s_waitcnt lgkmcnt(2)\n"
"v_add_f32 v74,v74,v34\n"
"v_add_f32 v75,v75,v35\n"
MF "v[132:135],a[244:247],a[216:219],v[132:135]\n"
"s_nop 0\n"
TR "v[176:177],v2\n"
TR "v[178:179],v2 offset:2048\n"
MF "v[132:135],a[240:243],a[176:179],v[132:135]\n"
"s_nop 0\n"
TR "v[180:181],v2 offset:4096\n"
TR "v[182:183],v2 offset:6144\n"
MF "v[132:135],v[188:191],a[136:139],v[132:135]\n"
"s_nop 0\n"
TR "v[184:185],v2 offset:8192\n"
TR "v[186:187],v2 offset:10240\n"
MF "v[124:127],v[188:191],a[132:135],v[124:127]\n"
"v_add_f32 v76,v76,v36\n"
"v_add_f32 v77,v77,v37\n"
MF "v[124:127],a[240:243],a[172:175],v[124:127]\n"
"v_add_f32 v78,v78,v38\n"
"v_add_f32 v79,v79,v39\n"
MF "v[124:127],a[244:247],a[212:215],v[124:127]\n"
CV "v72,v72,v73\n"
CV "v73,v74,v75\n"
MF "v[116:119],a[244:247],a[208:211],v[116:119]\n"
"s_add_u32 s87,s90,12288\n"
CV "v74,v76,v77\n"
CV "v75,v78,v79\n"
MF "v[116:119],a[240:243],a[168:171],v[116:119]\n"
"buffer_store_dwordx4 v[72:75],v9,s[40:43],s87 offen sc1\n"
MF "v[116:119],v[188:191],a[128:131],v[116:119]\n"
"v_add_f32 v80,v80,v40\n"
"v_add_f32 v81,v81,v41\n"
MF "v[108:111],v[188:191],a[124:127],v[108:111]\n"
"v_add_f32 v82,v82,v42\n"
"v_add_f32 v83,v83,v43\n"
MF "v[108:111],a[240:243],a[164:167],v[108:111]\n"
"v_add_f32 v84,v84,v44\n"
"v_add_f32 v85,v85,v45\n"
MF "v[108:111],a[244:247],a[204:207],v[108:111]\n"
"v_add_f32 v86,v86,v46\n"
"v_add_f32 v87,v87,v47\n"
MF "v[100:103],a[244:247],a[200:203],v[100:103]\n"
CV "v80,v80,v81\n"
CV "v81,v82,v83\n"
MF "v[100:103],a[240:243],a[160:163],v[100:103]\n"
"s_add_u32 s87,s90,13312\n"
CV "v82,v84,v85\n"
CV "v83,v86,v87\n"
MF "v[100:103],v[188:191],a[120:123],v[100:103]\n"
"s_waitcnt lgkmcnt(0)\n"
"buffer_store_dwordx4 v[80:83],v9,s[40:43],s87 offen sc1\n"
MF "v[136:139],v[176:179],a[120:123],v[136:139]\n"
"s_nop 0\n"
TR "v[188:189],v3\n"
TR "v[190:191],v3 offset:2048\n"
MF "v[136:139],v[180:183],a[160:163],v[136:139]\n"
"s_nop 0\n"
TR "a[240:241],v3 offset:4096\n"
TR "a[242:243],v3 offset:6144\n"
MF "v[136:139],v[184:187],a[200:203],v[136:139]\n"
"s_nop 0\n"
TR "a[244:245],v3 offset:8192\n"
TR "a[246:247],v3 offset:10240\n"
MF "v[144:147],v[184:187],a[204:207],v[144:147]\n"
"v_add_f32 v88,v88,v48\n"
"v_add_f32 v89,v89,v49\n"
MF "v[144:147],v[180:183],a[164:167],v[144:147]\n"
"v_add_f32 v90,v90,v50\n"
"v_add_f32 v91,v91,v51\n"
MF "v[144:147],v[176:179],a[124:127],v[144:147]\n"
"v_add_f32 v92,v92,v52\n"
"v_add_f32 v93,v93,v53\n"
MF "v[152:155],v[176:179],a[128:131],v[152:155]\n"
"v_add_f32 v94,v94,v54\n"
"v_add_f32 v95,v95,v55\n"
MF "v[152:155],v[180:183],a[168:171],v[152:155]\n"
CV "v88,v88,v89\n"
CV "v89,v90,v91\n"
MF "v[152:155],v[184:187],a[208:211],v[152:155]\n"
"s_add_u32 s87,s90,14336\n"
CV "v90,v92,v93\n"
CV "v91,v94,v95\n"
MF "v[160:163],v[184:187],a[212:215],v[160:163]\n"
"buffer_store_dwordx4 v[88:91],v9,s[40:43],s87 offen sc1\n"
MF "v[160:163],v[180:183],a[172:175],v[160:163]\n"
MF "v[160:163],v[176:179],a[132:135],v[160:163]\n"
MF "v[168:171],v[176:179],a[136:139],v[168:171]\n"
MF "v[168:171],v[180:183],a[176:179],v[168:171]\n"
MF "v[168:171],v[184:187],a[216:219],v[168:171]\n"
"s_waitcnt vmcnt(20) lgkmcnt(0)\n"
"s_barrier\n"
MF "v[172:175],a[244:247],a[216:219],v[172:175]\n"
"s_nop 0\n"
TR "v[176:177],v0 offset:49152\n"
TR "v[178:179],v0 offset:51200\n"
MF "v[172:175],a[240:243],a[176:179],v[172:175]\n"
"s_nop 0\n"
TR "v[180:181],v0 offset:53248\n"
TR "v[182:183],v0 offset:55296\n"
MF "v[172:175],v[188:191],a[136:139],v[172:175]\n"
"s_nop 0\n"
TR "v[184:185],v0 offset:57344\n"
TR "v[186:187],v0 offset:59392\n"
MF "v[164:167],v[188:191],a[132:135],v[164:167]\n"
"buffer_load_dword v10,v12,s[40:43],s86 offen sc1\n"
MF "v[164:167],a[240:243],a[172:175],v[164:167]\n"
MF "v[164:167],a[244:247],a[212:215],v[164:167]\n"
MF "v[156:159],a[244:247],a[208:211],v[156:159]\n"
MF "v[156:159],a[240:243],a[168:171],v[156:159]\n"
MF "v[156:159],v[188:191],a[128:131],v[156:159]\n"
MF "v[148:151],v[188:191],a[124:127],v[148:151]\n"
MF "v[148:151],a[240:243],a[164:167],v[148:151]\n"
MF "v[148:151],a[244:247],a[204:207],v[148:151]\n"
MF "v[140:143],a[244:247],a[200:203],v[140:143]\n"
MF "v[140:143],a[240:243],a[160:163],v[140:143]\n"
MF "v[140:143],v[188:191],a[120:123],v[140:143]\n"
"s_waitcnt lgkmcnt(0)\n"
MF "v[96:99],v[176:179],a[20:23],v[96:99]\n"
"s_nop 0\n"
TR "v[188:189],v1 offset:49152\n"
TR "v[190:191],v1 offset:51200\n"
MF "v[96:99],v[180:183],a[60:63],v[96:99]\n"
"s_nop 0\n"
TR "a[240:241],v1 offset:53248\n"
TR "a[242:243],v1 offset:55296\n"
MF "v[96:99],v[184:187],a[100:103],v[96:99]\n"
"s_nop 0\n"
TR "a[244:245],v1 offset:57344\n"
TR "a[246:247],v1 offset:59392\n"
MF "v[104:107],v[184:187],a[104:107],v[104:107]\n"
MF "v[104:107],v[180:183],a[64:67],v[104:107]\n"
MF "v[104:107],v[176:179],a[24:27],v[104:107]\n"
"s_waitcnt vmcnt(16)\n"
MF "v[96:99],v[208:211],a[252:255],v[96:99]\n"
MF "v[104:107],v[212:215],a[252:255],v[104:107]\n"
MF "v[112:115],v[216:219],a[252:255],v[112:115]\n"
MF "v[120:123],v[220:223],a[252:255],v[120:123]\n"
MF "v[128:131],v[224:227],a[252:255],v[128:131]\n"
MF "v[100:103],v[208:211],a[248:251],v[100:103]\n"
MF "v[108:111],v[212:215],a[248:251],v[108:111]\n"
MF "v[116:119],v[216:219],a[248:251],v[116:119]\n"
MF "v[124:127],v[220:223],a[248:251],v[124:127]\n"
MF "v[132:135],v[224:227],a[248:251],v[132:135]\n"
MF "v[112:115],v[176:179],a[28:31],v[112:115]\n"
MF "v[112:115],v[180:183],a[68:71],v[112:115]\n"
MF "v[112:115],v[184:187],a[108:111],v[112:115]\n"
MF "v[120:123],v[184:187],a[112:115],v[120:123]\n"
MF "v[120:123],v[180:183],a[72:75],v[120:123]\n"
"s_bfe_u32 s87,s22,0x30014\n"
MF "v[120:123],v[176:179],a[32:35],v[120:123]\n"
"s_lshl_b32 s87,s87,7\n"
MF "v[128:131],v[176:179],a[36:39],v[128:131]\n"
"s_and_b32 s60,s56,1\n"
MF "v[128:131],v[180:183],a[76:79],v[128:131]\n"
"s_mul_i32 s60,s60,204800\n"
MF "v[128:131],v[184:187],a[116:119],v[128:131]\n"
"s_waitcnt lgkmcnt(0)\n"
MF "v[132:135],a[244:247],a[116:119],v[132:135]\n"
"s_add_u32 s87,s87,s60\n"
TR "v[176:177],v2 offset:49152\n"
TR "v[178:179],v2 offset:51200\n"
MF "v[132:135],a[240:243],a[76:79],v[132:135]\n"
"s_mov_b32 s71,204800\n"
TR "v[180:181],v2 offset:53248\n"
TR "v[182:183],v2 offset:55296\n"
MF "v[132:135],v[188:191],a[36:39],v[132:135]\n"
"s_mov_b32 s66,204800\n"
TR "v[184:185],v2 offset:57344\n"
TR "v[186:187],v2 offset:59392\n"
MF "v[124:127],v[188:191],a[32:35],v[124:127]\n"
"s_add_u32 s72,s78,2\n"
MF "v[124:127],a[240:243],a[72:75],v[124:127]\n"
"s_and_b32 s72,s72,15\n"
MF "v[124:127],a[244:247],a[112:115],v[124:127]\n"
"s_bfe_u32 s60,s22,0x10014\n"
MF "v[116:119],a[244:247],a[108:111],v[116:119]\n"
"s_lshl_b32 s60,s60,5\n"
MF "v[116:119],a[240:243],a[68:71],v[116:119]\n"
"v_xor_b32 v242,s60,v8\n"
MF "v[116:119],v[188:191],a[28:31],v[116:119]\n"
MF "v[108:111],v[188:191],a[24:27],v[108:111]\n"
"s_add_u32 s16,s78,6\n"
MF "v[108:111],a[240:243],a[64:67],v[108:111]\n"
"s_and_b32 s16,s16,15\n"
MF "v[108:111],a[244:247],a[104:107],v[108:111]\n"
"s_add_u32 s17,s78,4\n"
MF "v[100:103],a[244:247],a[100:103],v[100:103]\n"
"s_and_b32 s17,s17,15\n"
MF "v[100:103],a[240:243],a[60:63],v[100:103]\n"
MF "v[100:103],v[188:191],a[20:23],v[100:103]\n"
"s_waitcnt lgkmcnt(0)\n"
MF "v[136:139],v[176:179],a[20:23],v[136:139]\n"
"s_nop 0\n"
TR "v[188:189],v3 offset:49152\n"
TR "v[190:191],v3 offset:51200\n"
MF "v[136:139],v[180:183],a[60:63],v[136:139]\n"
"s_nop 0\n"
TR "a[240:241],v3 offset:53248\n"
TR "a[242:243],v3 offset:55296\n"
MF "v[136:139],v[184:187],a[100:103],v[136:139]\n"
"s_nop 0\n"
TR "a[244:245],v3 offset:57344\n"
TR "a[246:247],v3 offset:59392\n"
MF "v[144:147],v[184:187],a[104:107],v[144:147]\n"
MF "v[144:147],v[180:183],a[64:67],v[144:147]\n"
MF "v[144:147],v[176:179],a[24:27],v[144:147]\n"
MF "v[152:155],v[176:179],a[28:31],v[152:155]\n"
MF "v[152:155],v[180:183],a[68:71],v[152:155]\n"
MF "v[152:155],v[184:187],a[108:111],v[152:155]\n"
MF "v[160:163],v[184:187],a[112:115],v[160:163]\n"
MF "v[160:163],v[180:183],a[72:75],v[160:163]\n"
MF "v[160:163],v[176:179],a[32:35],v[160:163]\n"
MF "v[168:171],v[176:179],a[36:39],v[168:171]\n"
MF "v[168:171],v[180:183],a[76:79],v[168:171]\n"
MF "v[168:171],v[184:187],a[116:119],v[168:171]\n"
"s_waitcnt vmcnt(0) lgkmcnt(0)\n"
"s_barrier\n"
MF "v[172:175],a[244:247],a[116:119],v[172:175]\n"
"v_sub_u32 v10,v10,v11\n"
MF "v[172:175],a[240:243],a[76:79],v[172:175]\n"
"s_nop 0\n"
TR "v[176:177],v4\n"
TR "v[178:179],v4 offset:2048\n"
MF "v[172:175],v[188:191],a[36:39],v[172:175]\n"
"s_nop 0\n"
TR "v[180:181],v4 offset:4096\n"
TR "v[182:183],v4 offset:6144\n"
MF "v[164:167],v[188:191],a[32:35],v[164:167]\n"
"s_nop 0\n"
TR "v[184:185],v4 offset:8192\n"
TR "v[186:187],v4 offset:10240\n"
MF "v[164:167],a[240:243],a[72:75],v[164:167]\n"
"v_readlane_b32 s16,v10,s16\n"
MF "v[164:167],a[244:247],a[112:115],v[164:167]\n"
"s_cmp_lt_u32 s16,9\n"
"s_cselect_b32 s16,0xf0000000,0\n"
MF "v[156:159],a[244:247],a[108:111],v[156:159]\n"
"v_readlane_b32 s17,v10,s17\n"
MF "v[156:159],a[240:243],a[68:71],v[156:159]\n"
"s_cmp_lt_u32 s17,10\n"
"s_cselect_b32 s17,0xf0000000,0\n"
MF "v[156:159],v[188:191],a[28:31],v[156:159]\n"
"s_bfe_u32 s62,s22,0x30014\n"
MF "v[148:151],v[188:191],a[24:27],v[148:151]\n"
"s_add_u32 s62,s62,3\n"
"s_and_b32 s62,s62,7\n"
"s_mul_i32 s62,s62,204800\n"
MF "v[148:151],a[240:243],a[64:67],v[148:151]\n"
"s_add_u32 s62,s62,s73\n"
"s_add_u32 s62,s62,133120\n"
"s_or_b32 s62,s62,s16\n"
MF "v[148:151],a[244:247],a[104:107],v[148:151]\n"
LD "v[16:19],v9,s[40:43],s62 offen sc1\n"
MF "v[140:143],a[244:247],a[100:103],v[140:143]\n"
LD "v[24:27],v9,s[40:43],s62 offen offset:1024 sc1\n"
MF "v[140:143],a[240:243],a[60:63],v[140:143]\n"
MF "v[140:143],v[188:191],a[20:23],v[140:143]\n"
"s_waitcnt lgkmcnt(0)\n"
LD "v[32:35],v9,s[40:43],s62 offen offset:2048 sc1\n"
MF "v[96:99],v[176:179],a[140:143],v[96:99]\n"
"s_nop 0\n"
TR "v[188:189],v5\n"
TR "v[190:191],v5 offset:2048\n"
MF "v[96:99],v[180:183],a[180:183],v[96:99]\n"
"s_nop 0\n"
TR "a[240:241],v5 offset:4096\n"
TR "a[242:243],v5 offset:6144\n"
MF "v[96:99],v[184:187],a[220:223],v[96:99]\n"
"s_nop 0\n"
TR "a[244:245],v5 offset:8192\n"
TR "a[246:247],v5 offset:10240\n"
MF "v[104:107],v[184:187],a[224:227],v[104:107]\n"
LD "v[40:43],v9,s[40:43],s62 offen offset:3072 sc1\n"
MF "v[104:107],v[180:183],a[184:187],v[104:107]\n"
MF "v[104:107],v[176:179],a[144:147],v[104:107]\n"
"s_add_u32 s62,s62,4096\n"
MF "v[112:115],v[176:179],a[148:151],v[112:115]\n"
LD "v[48:51],v9,s[40:43],s62 offen sc1\n"
MF "v[112:115],v[180:183],a[188:191],v[112:115]\n"
"s_bfe_u32 s62,s22,0x30014\n"
"ds_write_addtid_b32 v96 offset:16384\n"
"ds_write_addtid_b32 v97 offset:16640\n"
MF "v[112:115],v[184:187],a[228:231],v[112:115]\n"
"s_add_u32 s62,s62,2\n"
"s_and_b32 s62,s62,7\n"
"s_mul_i32 s62,s62,204800\n"
MF "v[120:123],v[184:187],a[232:235],v[120:123]\n"
"s_add_u32 s62,s62,s73\n"
"s_add_u32 s62,s62,153600\n"
"s_or_b32 s62,s62,s17\n"
MF "v[120:123],v[180:183],a[192:195],v[120:123]\n"
LD "v[20:23],v9,s[40:43],s62 offen sc1\n"
MF "v[120:123],v[176:179],a[152:155],v[120:123]\n"
"s_nop 0\n"
"ds_write_addtid_b32 v98 offset:16896\n"
"ds_write_addtid_b32 v99 offset:17152\n"
MF "v[128:131],v[176:179],a[156:159],v[128:131]\n"
LD "v[28:31],v9,s[40:43],s62 offen offset:1024 sc1\n"
MF "v[128:131],v[180:183],a[196:199],v[128:131]\n"
"s_nop 0\n"
"ds_write_addtid_b32 v104 offset:18432\n"
"ds_write_addtid_b32 v105 offset:18688\n"
MF "v[128:131],v[184:187],a[236:239],v[128:131]\n"
"s_waitcnt lgkmcnt(6)\n"
LD "v[36:39],v9,s[40:43],s62 offen offset:2048 sc1\n"
MF "v[100:103],v[188:191],a[140:143],v[100:103]\n"
"s_nop 0\n"
TR "v[176:177],v6\n"
TR "v[178:179],v6 offset:2048\n"
MF "v[100:103],a[240:243],a[180:183],v[100:103]\n"
"s_nop 0\n"
TR "v[180:181],v6 offset:4096\n"
TR "v[182:183],v6 offset:6144\n"
MF "v[100:103],a[244:247],a[220:223],v[100:103]\n"
"s_nop 0\n"
TR "v[184:185],v6 offset:8192\n"
TR "v[186:187],v6 offset:10240\n"
MF "v[108:111],a[244:247],a[224:227],v[108:111]\n"
LD "v[44:47],v9,s[40:43],s62 offen offset:3072 sc1\n"
MF "v[108:111],a[240:243],a[184:187],v[108:111]\n"
"s_nop 0\n"
"ds_write_addtid_b32 v106 offset:18944\n"
"ds_write_addtid_b32 v107 offset:19200\n"
MF "v[108:111],v[188:191],a[144:147],v[108:111]\n"
"s_add_u32 s62,s62,4096\n"
"ds_write_addtid_b32 v112 offset:20480\n"
"ds_write_addtid_b32 v113 offset:20736\n"
MF "v[116:119],v[188:191],a[148:151],v[116:119]\n"
LD "v[52:55],v9,s[40:43],s62 offen sc1\n"
MF "v[116:119],a[240:243],a[188:191],v[116:119]\n"
"s_nop 0\n"
"ds_write_addtid_b32 v114 offset:20992\n"
"ds_write_addtid_b32 v115 offset:21248\n"
MF "v[116:119],a[244:247],a[228:231],v[116:119]\n"
"s_nop 0\n"
"ds_write_addtid_b32 v120 offset:22528\n"
"ds_write_addtid_b32 v121 offset:22784\n"
MF "v[124:127],a[244:247],a[232:235],v[124:127]\n"
"s_nop 0\n"
"ds_write_addtid_b32 v122 offset:23040\n"
"ds_write_addtid_b32 v123 offset:23296\n"
MF "v[124:127],a[240:243],a[192:195],v[124:127]\n"
"s_nop 0\n"
"ds_write_addtid_b32 v128 offset:24576\n"
"ds_write_addtid_b32 v129 offset:24832\n"
MF "v[124:127],v[188:191],a[152:155],v[124:127]\n"
"buffer_load_dword v10,v12,s[40:43],s86 offen sc1\n"
MF "v[132:135],v[188:191],a[156:159],v[132:135]\n"
"s_nop 0\n"
"ds_write_addtid_b32 v130 offset:25088\n"
"ds_write_addtid_b32 v131 offset:25344\n"
MF "v[132:135],a[240:243],a[196:199],v[132:135]\n"
"s_nop 0\n"
"ds_write_addtid_b32 v100 offset:17408\n"
"ds_write_addtid_b32 v101 offset:17664\n"
MF "v[132:135],a[244:247],a[236:239],v[132:135]\n"
"s_waitcnt lgkmcnt(15)\n"
"ds_write_addtid_b32 v102 offset:17920\n"
"ds_write_addtid_b32 v103 offset:18176\n"
MF "v[136:139],v[176:179],a[140:143],v[136:139]\n"
"s_nop 0\n"
TR "v[188:189],v7\n"
TR "v[190:191],v7 offset:2048\n"
MF "v[136:139],v[180:183],a[180:183],v[136:139]\n"
"s_nop 0\n"
TR "a[240:241],v7 offset:4096\n"
TR "a[242:243],v7 offset:6144\n"
MF "v[136:139],v[184:187],a[220:223],v[136:139]\n"
"s_nop 0\n"
TR "a[244:245],v7 offset:8192\n"
TR "a[246:247],v7 offset:10240\n"
MF "v[144:147],v[184:187],a[224:227],v[144:147]\n"
"s_nop 0\n"
"ds_write_addtid_b32 v108 offset:19456\n"
"ds_write_addtid_b32 v109 offset:19712\n"
MF "v[144:147],v[180:183],a[184:187],v[144:147]\n"
"s_nop 0\n"
"ds_write_addtid_b32 v110 offset:19968\n"
"ds_write_addtid_b32 v111 offset:20224\n"
MF "v[144:147],v[176:179],a[144:147],v[144:147]\n"
"s_nop 0\n"
"ds_write_addtid_b32 v116 offset:21504\n"
"ds_write_addtid_b32 v117 offset:21760\n"
MF "v[152:155],v[176:179],a[148:151],v[152:155]\n"
"s_nop 0\n"
"ds_write_addtid_b32 v118 offset:22016\n"
"ds_write_addtid_b32 v119 offset:22272\n"
MF "v[152:155],v[180:183],a[188:191],v[152:155]\n"
"ds_write_addtid_b32 v124 offset:23552\n"
MF "v[152:155],v[184:187],a[228:231],v[152:155]\n"
"ds_write_addtid_b32 v125 offset:23808\n"
MF "v[160:163],v[184:187],a[232:235],v[160:163]\n"
"ds_write_addtid_b32 v126 offset:24064\n"
MF "v[160:163],v[180:183],a[192:195],v[160:163]\n"
"ds_write_addtid_b32 v127 offset:24320\n"
MF "v[160:163],v[176:179],a[152:155],v[160:163]\n"
"ds_write_addtid_b32 v132 offset:25600\n"
MF "v[168:171],v[176:179],a[156:159],v[168:171]\n"
"ds_write_addtid_b32 v133 offset:25856\n"
MF "v[168:171],v[180:183],a[196:199],v[168:171]\n"
"ds_write_addtid_b32 v134 offset:26112\n"
MF "v[168:171],v[184:187],a[236:239],v[168:171]\n"
"s_waitcnt lgkmcnt(15)\n"
"ds_write_addtid_b32 v135 offset:26368\n"
MF "v[140:143],v[188:191],a[140:143],v[140:143]\n"
MF "v[140:143],a[240:243],a[180:183],v[140:143]\n"
MF "v[140:143],a[244:247],a[220:223],v[140:143]\n"
MF "v[148:151],a[244:247],a[224:227],v[148:151]\n"
MF "v[148:151],a[240:243],a[184:187],v[148:151]\n"
"s_waitcnt lgkmcnt(0)\n"
"s_barrier\n"
MF "v[148:151],v[188:191],a[144:147],v[148:151]\n"
"ds_read2st64_b32 v[56:57],v13 offset0:0 offset1:1\n"
MF "v[156:159],v[188:191],a[148:151],v[156:159]\n"
"ds_read2st64_b32 v[58:59],v13 offset0:2 offset1:3\n"
MF "v[156:159],a[240:243],a[188:191],v[156:159]\n"
"ds_read2st64_b32 v[60:61],v13 offset0:4 offset1:5\n"
MF "v[156:159],a[244:247],a[228:231],v[156:159]\n"
"ds_read2st64_b32 v[62:63],v13 offset0:6 offset1:7\n"
MF "v[164:167],a[244:247],a[232:235],v[164:167]\n"
"ds_read2st64_b32 v[64:65],v13 offset0:8 offset1:9\n"
MF "v[164:167],a[240:243],a[192:195],v[164:167]\n"
"ds_read2st64_b32 v[66:67],v13 offset0:10 offset1:11\n"
MF "v[164:167],v[188:191],a[152:155],v[164:167]\n"
"ds_read2st64_b32 v[68:69],v13 offset0:12 offset1:13\n"
MF "v[172:175],v[188:191],a[156:159],v[172:175]\n"
"ds_read2st64_b32 v[70:71],v13 offset0:14 offset1:15\n"
MF "v[172:175],a[240:243],a[196:199],v[172:175]\n"
"ds_read2st64_b32 v[72:73],v13 offset0:16 offset1:17\n"
MF "v[172:175],a[244:247],a[236:239],v[172:175]\n"
"ds_read2st64_b32 v[74:75],v13 offset0:18 offset1:19\n"
"ds_read2st64_b32 v[76:77],v13 offset0:20 offset1:21\n"
"ds_read2st64_b32 v[78:79],v13 offset0:22 offset1:23\n"
"s_nop 0\n"
"ds_read2st64_b32 v[80:81],v13 offset0:24 offset1:25\n"
"ds_read2st64_b32 v[82:83],v13 offset0:26 offset1:27\n"
"ds_read2st64_b32 v[84:85],v13 offset0:28 offset1:29\n"
"ds_read2st64_b32 v[86:87],v13 offset0:30 offset1:31\n"
"ds_read2st64_b32 v[88:89],v13 offset0:32 offset1:33\n"
"ds_read2st64_b32 v[90:91],v13 offset0:34 offset1:35\n"
"ds_read2st64_b32 v[92:93],v13 offset0:36 offset1:37\n"
"ds_read2st64_b32 v[94:95],v13 offset0:38 offset1:39\n"
"s_waitcnt vmcnt(0)\n"
"v_sub_u32 v10,v10,v11\n"
"s_cmp_eq_u32 s16,0\n"
"s_cbranch_scc1 .Lhave03\n"
"s_add_u32 s59,s78,6\n"
"s_and_b32 s59,s59,15\n"
"s_nop 3\n"
".Lnf2:\n"
"v_readlane_b32 s58,v10,s59\n"
"s_cmp_lt_u32 s58,9\n"
"s_cbranch_scc1 .Lsp2\n"
".Lnb2:\n"
"s_bfe_u32 s62,s22,0x30014\n"
"s_add_u32 s62,s62,3\n"
"s_and_b32 s62,s62,7\n"
"s_mul_i32 s62,s62,204800\n"
"s_add_u32 s62,s62,s73\n"
"s_add_u32 s62,s62,133120\n"
LD "v[16:19],v9,s[40:43],s62 offen sc1\n"
LD "v[24:27],v9,s[40:43],s62 offen offset:1024 sc1\n"
LD "v[32:35],v9,s[40:43],s62 offen offset:2048 sc1\n"
LD "v[40:43],v9,s[40:43],s62 offen offset:3072 sc1\n"
"s_add_u32 s62,s62,4096\n"
LD "v[48:51],v9,s[40:43],s62 offen sc1\n"
".Lhave03:\n"
"s_cmp_eq_u32 s17,0\n"
"s_cbranch_scc1 .Lhave02\n"
"s_add_u32 s59,s78,4\n"
"s_and_b32 s59,s59,15\n"
"s_nop 3\n"
".Lnf3:\n"
"v_readlane_b32 s58,v10,s59\n"
"s_cmp_lt_u32 s58,10\n"
"s_cbranch_scc1 .Lsp3\n"
".Lnb3:\n"
"s_bfe_u32 s62,s22,0x30014\n"
"s_add_u32 s62,s62,2\n"
"s_and_b32 s62,s62,7\n"
"s_mul_i32 s62,s62,204800\n"
"s_add_u32 s62,s62,s73\n"
"s_add_u32 s62,s62,153600\n"
LD "v[20:23],v9,s[40:43],s62 offen sc1\n"
LD "v[28:31],v9,s[40:43],s62 offen offset:1024 sc1\n"
LD "v[36:39],v9,s[40:43],s62 offen offset:2048 sc1\n"
LD "v[44:47],v9,s[40:43],s62 offen offset:3072 sc1\n"
"s_add_u32 s62,s62,4096\n"
LD "v[52:55],v9,s[40:43],s62 offen sc1\n"
".Lhave02:\n"
".Lnf4:\n"
"v_readlane_b32 s58,v10,s72\n"
"s_cmp_lt_u32 s58,11\n"
"s_cbranch_scc1 .Lsp4\n"
".Lnb4:\n"
"s_bfe_u32 s62,s22,0x30014\n"
"s_add_u32 s62,s62,1\n"
"s_and_b32 s62,s62,7\n"
"s_mul_i32 s62,s62,204800\n"
"s_add_u32 s62,s62,s73\n"
"s_add_u32 s62,s62,174080\n"
LD "v[208:211],v9,s[40:43],s62 offen sc1\n"
LD "v[212:215],v9,s[40:43],s62 offen offset:1024 sc1\n"
LD "v[216:219],v9,s[40:43],s62 offen offset:2048 sc1\n"
LD "v[220:223],v9,s[40:43],s62 offen offset:3072 sc1\n"
"s_add_u32 s62,s62,4096\n"
LD "v[224:227],v9,s[40:43],s62 offen sc1\n"
"s_waitcnt lgkmcnt(15)\n"
"v_pk_add_f32 v[136:137],v[136:137],v[56:57]\n"
"v_pk_add_f32 v[138:139],v[138:139],v[58:59]\n"
"v_pk_add_f32 v[140:141],v[140:141],v[60:61]\n"
"v_pk_add_f32 v[142:143],v[142:143],v[62:63]\n"
"v_pk_add_f32 v[144:145],v[144:145],v[64:65]\n"
"s_waitcnt lgkmcnt(14)\n"
"v_pk_add_f32 v[146:147],v[146:147],v[66:67]\n"
"s_waitcnt lgkmcnt(13)\n"
"v_pk_add_f32 v[148:149],v[148:149],v[68:69]\n"
"s_waitcnt lgkmcnt(12)\n"
"v_pk_add_f32 v[150:151],v[150:151],v[70:71]\n"
"s_waitcnt lgkmcnt(11)\n"
"v_pk_add_f32 v[152:153],v[152:153],v[72:73]\n"
"s_waitcnt lgkmcnt(10)\n"
"v_pk_add_f32 v[154:155],v[154:155],v[74:75]\n"
"s_waitcnt lgkmcnt(9)\n"
"v_pk_add_f32 v[156:157],v[156:157],v[76:77]\n"
"s_waitcnt lgkmcnt(8)\n"
"v_pk_add_f32 v[158:159],v[158:159],v[78:79]\n"
"s_waitcnt lgkmcnt(7)\n"
"v_pk_add_f32 v[160:161],v[160:161],v[80:81]\n"
"s_waitcnt lgkmcnt(6)\n"
"v_pk_add_f32 v[162:163],v[162:163],v[82:83]\n"
"s_waitcnt lgkmcnt(5)\n"
"v_pk_add_f32 v[164:165],v[164:165],v[84:85]\n"
"s_waitcnt lgkmcnt(4)\n"
"v_pk_add_f32 v[166:167],v[166:167],v[86:87]\n"
"s_waitcnt lgkmcnt(3)\n"
"v_pk_add_f32 v[168:169],v[168:169],v[88:89]\n"
"s_waitcnt lgkmcnt(2)\n"
"v_pk_add_f32 v[170:171],v[170:171],v[90:91]\n"
"s_waitcnt lgkmcnt(1)\n"
"v_pk_add_f32 v[172:173],v[172:173],v[92:93]\n"
"s_waitcnt lgkmcnt(0)\n"
"v_pk_add_f32 v[174:175],v[174:175],v[94:95]\n"
"s_waitcnt vmcnt(5)\n"
"s_nop 0\n"
MF "v[140:143],v[16:19],a[252:255],v[140:143]\n"
MF "v[148:151],v[24:27],a[252:255],v[148:151]\n"
MF "v[156:159],v[32:35],a[252:255],v[156:159]\n"
MF "v[164:167],v[40:43],a[252:255],v[164:167]\n"
MF "v[172:175],v[48:51],a[252:255],v[172:175]\n"
MF "v[136:139],v[16:19],a[248:251],v[136:139]\n"
MF "v[144:147],v[24:27],a[248:251],v[144:147]\n"
MF "v[152:155],v[32:35],a[248:251],v[152:155]\n"
MF "v[160:163],v[40:43],a[248:251],v[160:163]\n"
MF "v[168:171],v[48:51],a[248:251],v[168:171]\n"
MF "v[136:139],v[20:23],a[252:255],v[136:139]\n"
MF "v[144:147],v[28:31],a[252:255],v[144:147]\n"
MF "v[152:155],v[36:39],a[252:255],v[152:155]\n"
MF "v[160:163],v[44:47],a[252:255],v[160:163]\n"
MF "v[168:171],v[52:55],a[252:255],v[168:171]\n"
MF "v[140:143],v[20:23],a[248:251],v[140:143]\n"
MF "v[148:151],v[28:31],a[248:251],v[148:151]\n"
MF "v[156:159],v[36:39],a[248:251],v[156:159]\n"
MF "v[164:167],v[44:47],a[248:251],v[164:167]\n"
MF "v[172:175],v[52:55],a[248:251],v[172:175]\n"
"s_waitcnt vmcnt(0)\n"
MF "v[140:143],v[208:211],a[252:255],v[140:143]\n"
MF "v[136:139],v[208:211],a[248:251],v[136:139]\n"
MF "v[148:151],v[212:215],a[252:255],v[148:151]\n"
MF "v[144:147],v[212:215],a[248:251],v[144:147]\n"
MF "v[156:159],v[216:219],a[252:255],v[156:159]\n"
"s_nop 4\n"
CV "v136,v136,v137\n"
CV "v137,v138,v139\n"
MF "v[152:155],v[216:219],a[248:251],v[152:155]\n"
CV "v138,v140,v141\n"
CV "v139,v142,v143\n"
MF "v[164:167],v[220:223],a[252:255],v[164:167]\n"
CV "v144,v144,v145\n"
CV "v145,v146,v147\n"
MF "v[160:163],v[220:223],a[248:251],v[160:163]\n"
CV "v146,v148,v149\n"
CV "v147,v150,v151\n"
"v_permlane16_swap_b32 v136,v138\n"
"v_permlane16_swap_b32 v137,v139\n"
"buffer_store_dwordx4 v[136:139],v242,s[44:47],s87 offen offset:64 sc1\n"
"s_add_u32 s87,s87,40960\n"
MF "v[172:175],v[224:227],a[252:255],v[172:175]\n"
CV "v152,v152,v153\n"
CV "v153,v154,v155\n"
MF "v[168:171],v[224:227],a[248:251],v[168:171]\n"
CV "v154,v156,v157\n"
CV "v155,v158,v159\n"
"v_permlane16_swap_b32 v144,v146\n"
"v_permlane16_swap_b32 v145,v147\n"
"buffer_store_dwordx4 v[144:147],v242,s[44:47],s87 offen offset:64 sc1\n"
"s_add_u32 s87,s87,40960\n"
CV "v160,v160,v161\n"
CV "v161,v162,v163\n"
CV "v162,v164,v165\n"
CV "v163,v166,v167\n"
"v_permlane16_swap_b32 v152,v154\n"
"v_permlane16_swap_b32 v153,v155\n"
"buffer_store_dwordx4 v[152:155],v242,s[44:47],s87 offen offset:64 sc1\n"
"s_add_u32 s87,s87,40960\n"
CV "v168,v168,v169\n"
CV "v169,v170,v171\n"
CV "v170,v172,v173\n"
CV "v171,v174,v175\n"
"v_permlane16_swap_b32 v160,v162\n"
"v_permlane16_swap_b32 v161,v163\n"
"buffer_store_dwordx4 v[160:163],v242,s[44:47],s87 offen offset:64 sc1\n"
"s_add_u32 s87,s87,40960\n"
"v_permlane16_swap_b32 v168,v170\n"
"v_permlane16_swap_b32 v169,v171\n"
"buffer_store_dwordx4 v[168:171],v242,s[44:47],s87 offen offset:64 sc1\n"
"s_add_u32 s87,s87,40960\n"
"s_branch .Ljobs\n"
".Lrole1:\n"
MF "v[16:19],a[120:123],v[176:179],v[252:255]\n"
"s_mov_b32 s65,128\n"
TR "v[188:189],v1 offset:24576\n"
AR "v220,a44\n"
MF "v[16:19],a[160:163],v[180:183],v[16:19]\n"
"s_mov_b32 s9,0x3f80\n"
TR "v[190:191],v1 offset:26624\n"
AR "v221,a45\n"
MF "v[16:19],a[200:203],v[184:187],v[16:19]\n"
AR "v222,a46\n"
AR "v223,a47\n"
MF "v[24:27],a[204:207],v[184:187],v[252:255]\n"
"s_bfe_u32 s58,s22,0x30014\n"
TR "a[240:241],v1 offset:28672\n"
AR "v224,a64\n"
MF "v[24:27],a[164:167],v[180:183],v[24:27]\n"
"s_add_u32 s61,s58,1\n"
TR "a[242:243],v1 offset:30720\n"
AR "v225,a65\n"
MF "v[24:27],a[124:127],v[176:179],v[24:27]\n"
"s_and_b32 s61,s61,7\n"
AR "v226,a66\n"
AR "v227,a67\n"
MF "v[32:35],a[128:131],v[176:179],v[252:255]\n"
"s_mov_b32 s64,9\n"
TR "a[244:245],v1 offset:32768\n"
"ds_read_b128 a[64:67],v236 offset:49152\n"
MF "v[32:35],a[168:171],v[180:183],v[32:35]\n"
"s_xor_b32 s67,s78,8\n"
TR "a[246:247],v1 offset:34816\n"
"v_fma_f32 v220,v220,v209,v215\n"
MF "v[32:35],a[208:211],v[184:187],v[32:35]\n"
"s_mov_b32 s71,204800\n"
"v_fma_f32 v221,v221,v209,v215\n"
"v_fma_f32 v222,v222,v209,v215\n"
MF "v[40:43],a[212:215],v[184:187],v[252:255]\n"
"s_mov_b32 s66,204800\n"
"v_fma_f32 v223,v223,v209,v215\n"
"v_fma_f32 v224,v224,v209,v215\n"
MF "v[40:43],a[172:175],v[180:183],v[40:43]\n"
"s_and_b32 s73,s56,1\n"
"v_fma_f32 v225,v225,v209,v215\n"
"v_fma_f32 v226,v226,v209,v215\n"
MF "v[40:43],a[132:135],v[176:179],v[40:43]\n"
"s_mul_i32 s73,s73,5120\n"
"v_fma_f32 v227,v227,v209,v215\n"
CV "v220,v220,v221\n"
MF "v[48:51],a[136:139],v[176:179],v[252:255]\n"
"s_add_u32 s73,s73,s93\n"
CV "v221,v222,v223\n"
CV "v222,v224,v225\n"
MF "v[48:51],a[176:179],v[180:183],v[48:51]\n"
"s_xor_b32 s58,s58,4\n"
CV "v223,v226,v227\n"
AW "a44,v220\n"
MF "v[48:51],a[216:219],v[184:187],v[48:51]\n"
"s_waitcnt lgkmcnt(0)\n"
AW "a45,v221\n"
AW "a46,v222\n"
MF "v[52:55],a[216:219],a[244:247],v[252:255]\n"
"s_mul_i32 s70,s58,204800\n"
TR "v[176:177],v2 offset:24576\n"
AW "a47,v223\n"
MF "v[52:55],a[176:179],a[240:243],v[52:55]\n"
"s_add_u32 s70,s70,s73\n"
TR "v[178:179],v2 offset:26624\n"
AR "v228,a84\n"
MF "v[52:55],a[136:139],v[188:191],v[52:55]\n"
"s_sub_u32 s70,s70,30720\n"
AR "v229,a85\n"
AR "v230,a86\n"
MF "v[44:47],a[132:135],v[188:191],v[252:255]\n"
"s_mov_b32 s85,1\n"
TR "v[180:181],v2 offset:28672\n"
AR "v231,a87\n"
MF "v[44:47],a[172:175],a[240:243],v[44:47]\n"
"s_mov_b32 s83,0xf0000000\n"
TR "v[182:183],v2 offset:30720\n"
AR "v232,a104\n"
MF "v[44:47],a[212:215],a[244:247],v[44:47]\n"
"s_mov_b32 s82,0xf0000000\n"
AR "v233,a105\n"
AR "v234,a106\n"
MF "v[36:39],a[208:211],a[244:247],v[252:255]\n"
"s_add_u32 s58,s79,73728\n"
TR "v[184:185],v2 offset:32768\n"
AR "v235,a107\n"
MF "v[36:39],a[168:171],a[240:243],v[36:39]\n"
"s_mov_b32 s90,s91\n"
TR "v[186:187],v2 offset:34816\n"
"ds_read_b128 a[104:107],v237 offset:4096\n"
MF "v[36:39],a[128:131],v[188:191],v[36:39]\n"
"s_add_u32 s91,s91,20480\n"
"v_fma_f32 v228,v228,v209,v215\n"
"v_fma_f32 v229,v229,v209,v215\n"
MF "v[28:31],a[124:127],v[188:191],v[252:255]\n"
"s_bfe_u32 s74,s23,0x70000\n"
"v_fma_f32 v230,v230,v209,v215\n"
"v_fma_f32 v231,v231,v209,v215\n"
MF "v[28:31],a[164:167],a[240:243],v[28:31]\n"
"s_lshl_b32 s74,s74,1\n"
"v_fma_f32 v232,v232,v209,v215\n"
"v_fma_f32 v233,v233,v209,v215\n"
MF "v[28:31],a[204:207],a[244:247],v[28:31]\n"
"s_add_u32 s74,s74,s56\n"
"v_fma_f32 v234,v234,v209,v215\n"
"v_fma_f32 v235,v235,v209,v215\n"
MF "v[20:23],a[200:203],a[244:247],v[252:255]\n"
"s_sub_u32 s74,s74,2\n"
CV "v228,v228,v229\n"
CV "v229,v230,v231\n"
MF "v[20:23],a[160:163],a[240:243],v[20:23]\n"
"s_mul_i32 s74,s74,30720\n"
CV "v230,v232,v233\n"
CV "v231,v234,v235\n"
MF "v[20:23],a[120:123],v[188:191],v[20:23]\n"
"s_waitcnt lgkmcnt(1)\n"
AW "a84,v228\n"
AW "a85,v229\n"
MF "v[56:59],a[120:123],v[176:179],v[252:255]\n"
"s_bfe_u32 s68,s23,0x30003\n"
TR "v[188:189],v3 offset:24576\n"
"v_add_u32 v198,59392,v192\n"
MF "v[56:59],a[160:163],v[180:183],v[56:59]\n"
"s_add_u32 s68,s68,7\n"
TR "v[190:191],v3 offset:26624\n"
"v_add_u32 v199,68608,v192\n"
MF "v[56:59],a[200:203],v[184:187],v[56:59]\n"
"s_lshl_b32 s68,s68,7\n"
"v_add_u32 v200,81920,v192\n"
"v_add_u32 v201,91136,v192\n"
MF "v[64:67],a[204:207],v[184:187],v[252:255]\n"
"s_and_b32 s68,s68,768\n"
TR "a[240:241],v3 offset:28672\n"
"v_add_u32 v202,100352,v192\n"
MF "v[64:67],a[164:167],v[180:183],v[64:67]\n"
TR "a[242:243],v3 offset:30720\n"
"v_add_u32 v203,109568,v192\n"
MF "v[64:67],a[124:127],v[176:179],v[64:67]\n"
AW "a86,v230\n"
AW "a87,v231\n"
MF "v[72:75],a[128:131],v[176:179],v[252:255]\n"
TR "a[244:245],v3 offset:32768\n"
AR "v220,a8\n"
MF "v[72:75],a[168:171],v[180:183],v[72:75]\n"
TR "a[246:247],v3 offset:34816\n"
AR "v221,a9\n"
MF "v[72:75],a[208:211],v[184:187],v[72:75]\n"
AR "v222,a10\n"
AR "v223,a11\n"
MF "v[80:83],a[212:215],v[184:187],v[252:255]\n"
AR "v224,a28\n"
AR "v225,a29\n"
MF "v[80:83],a[172:175],v[180:183],v[80:83]\n"
AR "v226,a30\n"
AR "v227,a31\n"
MF "v[80:83],a[132:135],v[176:179],v[80:83]\n"
"ds_read_b128 a[28:31],v236 offset:32768\n"
"v_fma_f32 v220,v220,v210,v216\n"
MF "v[88:91],a[136:139],v[176:179],v[252:255]\n"
"v_fma_f32 v221,v221,v210,v216\n"
"v_fma_f32 v222,v222,v210,v216\n"
MF "v[88:91],a[176:179],v[180:183],v[88:91]\n"
"v_fma_f32 v223,v223,v210,v216\n"
"v_fma_f32 v224,v224,v210,v216\n"
MF "v[88:91],a[216:219],v[184:187],v[88:91]\n"
"s_waitcnt vmcnt(0) lgkmcnt(0)\n"
"s_barrier\n"
MF "v[92:95],a[216:219],a[244:247],v[252:255]\n"
"s_add_u32 s89,s89,s65\n"
TR "v[176:177],v0\n"
"v_fma_f32 v225,v225,v210,v216\n"
MF "v[92:95],a[176:179],a[240:243],v[92:95]\n"
"s_add_u32 m0,s79,24576\n"
TR "v[178:179],v0 offset:2048\n"
"v_fma_f32 v226,v226,v210,v216\n"
MF "v[92:95],a[136:139],v[188:191],v[92:95]\n"
LD "v192,s[48:51],s89 offen lds\n"
MF "v[84:87],a[132:135],v[188:191],v[252:255]\n"
TR "v[180:181],v0 offset:4096\n"
"v_fma_f32 v227,v227,v210,v216\n"
MF "v[84:87],a[172:175],a[240:243],v[84:87]\n"
TR "v[182:183],v0 offset:6144\n"
CV "v220,v220,v221\n"
MF "v[84:87],a[212:215],a[244:247],v[84:87]\n"
LD "v193,s[48:51],s89 offen offset:1024 lds\n"
MF "v[76:79],a[208:211],a[244:247],v[252:255]\n"
TR "v[184:185],v0 offset:8192\n"
CV "v221,v222,v223\n"
MF "v[76:79],a[168:171],a[240:243],v[76:79]\n"
TR "v[186:187],v0 offset:10240\n"
CV "v222,v224,v225\n"
MF "v[76:79],a[128:131],v[188:191],v[76:79]\n"
TR "v[96:97],v0 offset:49152\n"
CV "v223,v226,v227\n"
MF "v[68:71],a[124:127],v[188:191],v[252:255]\n"
TR "v[98:99],v0 offset:51200\n"
AW "a8,v220\n"
MF "v[68:71],a[164:167],a[240:243],v[68:71]\n"
LD "v194,s[48:51],s89 offen offset:2048 lds\n"
MF "v[68:71],a[204:207],a[244:247],v[68:71]\n"
TR "v[100:101],v0 offset:53248\n"
AW "a9,v221\n"
MF "v[60:63],a[200:203],a[244:247],v[252:255]\n"
TR "v[102:103],v0 offset:55296\n"
AW "a10,v222\n"
MF "v[60:63],a[160:163],a[240:243],v[60:63]\n"
TR "v[104:105],v0 offset:57344\n"
AW "a11,v223\n"
MF "v[60:63],a[120:123],v[188:191],v[60:63]\n"
"s_waitcnt lgkmcnt(5)\n"
LD "v195,s[48:51],s89 offen offset:3072 lds\n"
MF "v[16:19],a[140:143],v[176:179],v[16:19]\n"
TR "v[188:189],v1\n"
AR "v228,a48\n"
MF "v[16:19],a[180:183],v[180:183],v[16:19]\n"
TR "v[190:191],v1 offset:2048\n"
AR "v229,a49\n"
MF "v[16:19],a[220:223],v[184:187],v[16:19]\n"
TR "v[106:107],v0 offset:59392\n"
AR "v230,a50\n"
MF "v[24:27],a[224:227],v[184:187],v[24:27]\n"
TR "a[240:241],v1 offset:4096\n"
AR "v231,a51\n"
MF "v[24:27],a[184:187],v[180:183],v[24:27]\n"
"s_add_u32 m0,s79,28672\n"
TR "a[242:243],v1 offset:6144\n"
AR "v232,a68\n"
MF "v[24:27],a[144:147],v[176:179],v[24:27]\n"
LD "v196,s[48:51],s89 offen lds\n"
MF "v[32:35],a[148:151],v[176:179],v[32:35]\n"
TR "a[244:245],v1 offset:8192\n"
AR "v233,a69\n"
MF "v[32:35],a[188:191],v[180:183],v[32:35]\n"
TR "a[246:247],v1 offset:10240\n"
AR "v234,a70\n"
MF "v[32:35],a[228:231],v[184:187],v[32:35]\n"
LD "v197,s[48:51],s89 offen offset:1024 lds\n"
MF "v[40:43],a[232:235],v[184:187],v[40:43]\n"
TR "v[108:109],v4\n"
AR "v235,a71\n"
MF "v[40:43],a[192:195],v[180:183],v[40:43]\n"
"s_nop 0\n"
TR "v[110:111],v4 offset:2048\n"
"ds_read_b128 a[68:71],v236 offset:53248\n"
MF "v[40:43],a[152:155],v[176:179],v[40:43]\n"
TR "v[112:113],v4 offset:4096\n"
"v_fma_f32 v228,v228,v210,v216\n"
MF "v[48:51],a[156:159],v[176:179],v[48:51]\n"
TR "v[114:115],v4 offset:6144\n"
"v_fma_f32 v229,v229,v210,v216\n"
MF "v[48:51],a[196:199],v[180:183],v[48:51]\n"
LD "v198,s[48:51],s89 offen offset:2048 lds\n"
MF "v[48:51],a[236:239],v[184:187],v[48:51]\n"
"s_waitcnt lgkmcnt(5)\n"
TR "v[116:117],v4 offset:8192\n"
"v_fma_f32 v230,v230,v210,v216\n"
MF "v[52:55],a[236:239],a[244:247],v[52:55]\n"
TR "v[176:177],v2\n"
"v_fma_f32 v231,v231,v210,v216\n"
MF "v[52:55],a[196:199],a[240:243],v[52:55]\n"
TR "v[178:179],v2 offset:2048\n"
"v_fma_f32 v232,v232,v210,v216\n"
MF "v[52:55],a[156:159],v[188:191],v[52:55]\n"
LD "v199,s[48:51],s89 offen offset:3072 lds\n"
MF "v[44:47],a[152:155],v[188:191],v[44:47]\n"
TR "v[180:181],v2 offset:4096\n"
"v_fma_f32 v233,v233,v210,v216\n"
MF "v[44:47],a[192:195],a[240:243],v[44:47]\n"
TR "v[182:183],v2 offset:6144\n"
"v_fma_f32 v234,v234,v210,v216\n"
MF "v[44:47],a[232:235],a[244:247],v[44:47]\n"
TR "v[118:119],v4 offset:10240\n"
"v_fma_f32 v235,v235,v210,v216\n"
MF "v[36:39],a[228:231],a[244:247],v[36:39]\n"
TR "v[184:185],v2 offset:8192\n"
CV "v228,v228,v229\n"
MF "v[36:39],a[188:191],a[240:243],v[36:39]\n"
"s_add_u32 m0,s79,32768\n"
TR "v[186:187],v2 offset:10240\n"
CV "v229,v230,v231\n"
MF "v[36:39],a[148:151],v[188:191],v[36:39]\n"
LD "v200,s[48:51],s89 offen lds\n"
MF "v[28:31],a[144:147],v[188:191],v[28:31]\n"
TR "v[120:121],v1 offset:49152\n"
CV "v230,v232,v233\n"
MF "v[28:31],a[184:187],a[240:243],v[28:31]\n"
TR "v[122:123],v1 offset:51200\n"
CV "v231,v234,v235\n"
MF "v[28:31],a[224:227],a[244:247],v[28:31]\n"
TR "v[124:125],v1 offset:53248\n"
AW "a48,v228\n"
MF "v[20:23],a[220:223],a[244:247],v[20:23]\n"
LD "v201,s[48:51],s89 offen offset:1024 lds\n"
MF "v[20:23],a[180:183],a[240:243],v[20:23]\n"
TR "v[126:127],v1 offset:55296\n"
AW "a49,v229\n"
MF "v[20:23],a[140:143],v[188:191],v[20:23]\n"
"s_waitcnt lgkmcnt(4)\n"
TR "v[128:129],v1 offset:57344\n"
AW "a50,v230\n"
MF "v[56:59],a[140:143],v[176:179],v[56:59]\n"
TR "v[188:189],v3\n"
AW "a51,v231\n"
MF "v[56:59],a[180:183],v[180:183],v[56:59]\n"
TR "v[190:191],v3 offset:2048\n"
AR "v220,a88\n"
MF "v[56:59],a[220:223],v[184:187],v[56:59]\n"
LD "v202,s[48:51],s89 offen offset:2048 lds\n"
MF "v[64:67],a[224:227],v[184:187],v[64:67]\n"
TR "a[240:241],v3 offset:4096\n"
AR "v221,a89\n"
MF "v[64:67],a[184:187],v[180:183],v[64:67]\n"
TR "a[242:243],v3 offset:6144\n"
AR "v222,a90\n"
MF "v[64:67],a[144:147],v[176:179],v[64:67]\n"
LD "v203,s[48:51],s89 offen offset:3072 lds\n"
MF "v[72:75],a[148:151],v[176:179],v[72:75]\n"
TR "a[244:245],v3 offset:8192\n"
AR "v223,a91\n"
MF "v[72:75],a[188:191],v[180:183],v[72:75]\n"
TR "a[246:247],v3 offset:10240\n"
AR "v224,a108\n"
MF "v[72:75],a[228:231],v[184:187],v[72:75]\n"
TR "v[130:131],v1 offset:59392\n"
AR "v225,a109\n"
MF "v[80:83],a[232:235],v[184:187],v[80:83]\n"
TR "v[132:133],v5\n"
AR "v226,a110\n"
MF "v[80:83],a[192:195],v[180:183],v[80:83]\n"
TR "v[134:135],v5 offset:2048\n"
AR "v227,a111\n"
MF "v[80:83],a[152:155],v[176:179],v[80:83]\n"
"s_nop 0\n"
TR "v[136:137],v5 offset:4096\n"
"ds_read_b128 a[108:111],v237 offset:8192\n"
MF "v[88:91],a[156:159],v[176:179],v[88:91]\n"
TR "v[138:139],v5 offset:6144\n"
"v_fma_f32 v220,v220,v210,v216\n"
MF "v[88:91],a[196:199],v[180:183],v[88:91]\n"
TR "v[140:141],v5 offset:8192\n"
"v_fma_f32 v221,v221,v210,v216\n"
MF "v[88:91],a[236:239],v[184:187],v[88:91]\n"
"s_waitcnt lgkmcnt(0)\n"
"s_barrier\n"
MF "v[92:95],a[236:239],a[244:247],v[92:95]\n"
TR "v[142:143],v5 offset:10240\n"
"v_fma_f32 v222,v222,v210,v216\n"
MF "v[92:95],a[196:199],a[240:243],v[92:95]\n"
TR "v[144:145],v2 offset:49152\n"
"v_fma_f32 v223,v223,v210,v216\n"
MF "v[92:95],a[156:159],v[188:191],v[92:95]\n"
TR "v[146:147],v2 offset:51200\n"
"v_fma_f32 v224,v224,v210,v216\n"
MF "v[84:87],a[152:155],v[188:191],v[84:87]\n"
TR "v[148:149],v2 offset:53248\n"
"v_fma_f32 v225,v225,v210,v216\n"
MF "v[84:87],a[192:195],a[240:243],v[84:87]\n"
TR "v[150:151],v2 offset:55296\n"
"v_fma_f32 v226,v226,v210,v216\n"
MF "v[84:87],a[232:235],a[244:247],v[84:87]\n"
TR "v[152:153],v2 offset:57344\n"
"v_fma_f32 v227,v227,v210,v216\n"
MF "v[76:79],a[228:231],a[244:247],v[76:79]\n"
TR "v[154:155],v2 offset:59392\n"
CV "v220,v220,v221\n"
MF "v[76:79],a[188:191],a[240:243],v[76:79]\n"
TR "v[156:157],v6\n"
CV "v221,v222,v223\n"
MF "v[76:79],a[148:151],v[188:191],v[76:79]\n"
TR "v[158:159],v6 offset:2048\n"
CV "v222,v224,v225\n"
MF "v[68:71],a[144:147],v[188:191],v[68:71]\n"
TR "v[160:161],v6 offset:4096\n"
CV "v223,v226,v227\n"
MF "v[68:71],a[184:187],a[240:243],v[68:71]\n"
TR "v[162:163],v6 offset:6144\n"
AW "a88,v220\n"
MF "v[68:71],a[224:227],a[244:247],v[68:71]\n"
TR "v[164:165],v6 offset:8192\n"
AW "a89,v221\n"
MF "v[60:63],a[220:223],a[244:247],v[60:63]\n"
TR "v[166:167],v6 offset:10240\n"
AW "a90,v222\n"
MF "v[60:63],a[180:183],a[240:243],v[60:63]\n"
TR "v[168:169],v3 offset:49152\n"
AW "a91,v223\n"
MF "v[60:63],a[140:143],v[188:191],v[60:63]\n"
TR "v[170:171],v3 offset:51200\n"
AR "v228,a12\n"
MF "v[16:19],a[0:3],v[96:99],v[16:19]\n"
TR "v[172:173],v3 offset:53248\n"
AR "v229,a13\n"
MF "v[16:19],a[40:43],v[100:103],v[16:19]\n"
TR "v[174:175],v3 offset:55296\n"
AR "v230,a14\n"
MF "v[16:19],a[80:83],v[104:107],v[16:19]\n"
TR "v[204:205],v3 offset:57344\n"
AR "v231,a15\n"
MF "v[16:19],a[20:23],v[108:111],v[16:19]\n"
TR "v[206:207],v3 offset:59392\n"
AR "v232,a32\n"
MF "v[16:19],a[60:63],v[112:115],v[16:19]\n"
TR "v[244:245],v7\n"
AR "v233,a33\n"
MF "v[16:19],a[100:103],v[116:119],v[16:19]\n"
"s_waitcnt lgkmcnt(15)\n"
TR "v[246:247],v7 offset:2048\n"
AR "v234,a34\n"
MF "v[20:23],a[100:103],v[140:143],v[20:23]\n"
TR "v[248:249],v7 offset:4096\n"
AR "v235,a35\n"
MF "v[20:23],a[60:63],v[136:139],v[20:23]\n"
"s_nop 0\n"
TR "v[250:251],v7 offset:6144\n"
"ds_read_b128 a[32:35],v236 offset:36864\n"
MF "v[20:23],a[20:23],v[132:135],v[20:23]\n"
TR "v[238:239],v7 offset:8192\n"
"v_fma_f32 v228,v228,v211,v217\n"
MF "v[20:23],a[80:83],v[128:131],v[20:23]\n"
TR "v[240:241],v7 offset:10240\n"
"v_fma_f32 v229,v229,v211,v217\n"
MF "v[20:23],a[40:43],v[124:127],v[20:23]\n"
"v_fma_f32 v230,v230,v211,v217\n"
"v_fma_f32 v231,v231,v211,v217\n"
MF "v[20:23],a[0:3],v[120:123],v[20:23]\n"
"s_waitcnt lgkmcnt(13)\n"
"v_fma_f32 v232,v232,v211,v217\n"
"v_fma_f32 v233,v233,v211,v217\n"
MF "v[56:59],a[0:3],v[144:147],v[56:59]\n"
"v_fma_f32 v234,v234,v211,v217\n"
"v_fma_f32 v235,v235,v211,v217\n"
MF "v[56:59],a[40:43],v[148:151],v[56:59]\n"
CV "v228,v228,v229\n"
CV "v229,v230,v231\n"
MF "v[56:59],a[80:83],v[152:155],v[56:59]\n"
CV "v230,v232,v233\n"
CV "v231,v234,v235\n"
MF "v[56:59],a[20:23],v[156:159],v[56:59]\n"
AW "a12,v228\n"
AW "a13,v229\n"
MF "v[56:59],a[60:63],v[160:163],v[56:59]\n"
AW "a14,v230\n"
AW "a15,v231\n"
MF "v[56:59],a[100:103],v[164:167],v[56:59]\n"
"s_waitcnt lgkmcnt(0)\n"
AR "v220,a52\n"
AR "v221,a53\n"
MF "v[60:63],a[100:103],v[238:241],v[60:63]\n"
AR "v222,a54\n"
AR "v223,a55\n"
MF "v[60:63],a[60:63],v[248:251],v[60:63]\n"
AR "v224,a72\n"
AR "v225,a73\n"
MF "v[60:63],a[20:23],v[244:247],v[60:63]\n"
AR "v226,a74\n"
AR "v227,a75\n"
MF "v[60:63],a[80:83],v[204:207],v[60:63]\n"
"ds_read_b128 a[72:75],v236 offset:57344\n"
"v_fma_f32 v220,v220,v211,v217\n"
MF "v[60:63],a[40:43],v[172:175],v[60:63]\n"
"v_fma_f32 v221,v221,v211,v217\n"
"v_fma_f32 v222,v222,v211,v217\n"
MF "v[60:63],a[0:3],v[168:171],v[60:63]\n"
"v_fma_f32 v223,v223,v211,v217\n"
"v_fma_f32 v224,v224,v211,v217\n"
MF "v[68:71],a[4:7],v[168:171],v[68:71]\n"
"v_fma_f32 v225,v225,v211,v217\n"
"v_fma_f32 v226,v226,v211,v217\n"
MF "v[68:71],a[44:47],v[172:175],v[68:71]\n"
"v_fma_f32 v227,v227,v211,v217\n"
CV "v220,v220,v221\n"
MF "v[68:71],a[84:87],v[204:207],v[68:71]\n"
CV "v221,v222,v223\n"
CV "v222,v224,v225\n"
MF "v[68:71],a[24:27],v[244:247],v[68:71]\n"
CV "v223,v226,v227\n"
AW "a52,v220\n"
MF "v[68:71],a[64:67],v[248:251],v[68:71]\n"
AW "a53,v221\n"
AW "a54,v222\n"
MF "v[68:71],a[104:107],v[238:241],v[68:71]\n"
AW "a55,v223\n"
AR "v228,a92\n"
MF "v[64:67],a[104:107],v[164:167],v[64:67]\n"
"s_waitcnt lgkmcnt(0)\n"
"s_barrier\n"
MF "v[64:67],a[64:67],v[160:163],v[64:67]\n"
AR "v229,a93\n"
AR "v230,a94\n"
MF "v[64:67],a[24:27],v[156:159],v[64:67]\n"
AR "v231,a95\n"
AR "v232,a112\n"
MF "v[64:67],a[84:87],v[152:155],v[64:67]\n"
AR "v233,a113\n"
AR "v234,a114\n"
MF "v[64:67],a[44:47],v[148:151],v[64:67]\n"
AR "v235,a115\n"
"v_fma_f32 v228,v228,v211,v217\n"
MF "v[64:67],a[4:7],v[144:147],v[64:67]\n"
"ds_read_b128 a[112:115],v237 offset:12288\n"
"v_fma_f32 v229,v229,v211,v217\n"
MF "v[28:31],a[4:7],v[120:123],v[28:31]\n"
"v_fma_f32 v230,v230,v211,v217\n"
"v_fma_f32 v231,v231,v211,v217\n"
MF "v[28:31],a[44:47],v[124:127],v[28:31]\n"
"v_fma_f32 v232,v232,v211,v217\n"
"v_fma_f32 v233,v233,v211,v217\n"
MF "v[28:31],a[84:87],v[128:131],v[28:31]\n"
"v_fma_f32 v234,v234,v211,v217\n"
"v_fma_f32 v235,v235,v211,v217\n"
MF "v[28:31],a[24:27],v[132:135],v[28:31]\n"
CV "v228,v228,v229\n"
CV "v229,v230,v231\n"
MF "v[28:31],a[64:67],v[136:139],v[28:31]\n"
CV "v230,v232,v233\n"
CV "v231,v234,v235\n"
MF "v[28:31],a[104:107],v[140:143],v[28:31]\n"
AW "a92,v228\n"
AW "a93,v229\n"
MF "v[24:27],a[104:107],v[116:119],v[24:27]\n"
AW "a94,v230\n"
AW "a95,v231\n"
MF "v[24:27],a[64:67],v[112:115],v[24:27]\n"
AR "v220,a16\n"
AR "v221,a17\n"
MF "v[24:27],a[24:27],v[108:111],v[24:27]\n"
AR "v222,a18\n"
AR "v223,a19\n"
MF "v[24:27],a[84:87],v[104:107],v[24:27]\n"
AR "v224,a36\n"
AR "v225,a37\n"
MF "v[24:27],a[44:47],v[100:103],v[24:27]\n"
AR "v226,a38\n"
AR "v227,a39\n"
MF "v[24:27],a[4:7],v[96:99],v[24:27]\n"
"ds_read_b128 a[36:39],v236 offset:40960\n"
"v_fma_f32 v220,v220,v212,v218\n"
MF "v[32:35],a[8:11],v[96:99],v[32:35]\n"
"v_fma_f32 v221,v221,v212,v218\n"
"v_fma_f32 v222,v222,v212,v218\n"
MF "v[32:35],a[48:51],v[100:103],v[32:35]\n"
"v_fma_f32 v223,v223,v212,v218\n"
"v_fma_f32 v224,v224,v212,v218\n"
MF "v[32:35],a[88:91],v[104:107],v[32:35]\n"
"v_fma_f32 v225,v225,v212,v218\n"
"v_fma_f32 v226,v226,v212,v218\n"
MF "v[32:35],a[28:31],v[108:111],v[32:35]\n"
"v_fma_f32 v227,v227,v212,v218\n"
CV "v220,v220,v221\n"
MF "v[32:35],a[68:71],v[112:115],v[32:35]\n"
CV "v221,v222,v223\n"
CV "v222,v224,v225\n"
MF "v[32:35],a[108:111],v[116:119],v[32:35]\n"
CV "v223,v226,v227\n"
AW "a16,v220\n"
MF "v[36:39],a[108:111],v[140:143],v[36:39]\n"
AW "a17,v221\n"
AW "a18,v222\n"
MF "v[36:39],a[68:71],v[136:139],v[36:39]\n"
AW "a19,v223\n"
AR "v228,a56\n"
MF "v[36:39],a[28:31],v[132:135],v[36:39]\n"
AR "v229,a57\n"
AR "v230,a58\n"
MF "v[36:39],a[88:91],v[128:131],v[36:39]\n"
AR "v231,a59\n"
AR "v232,a76\n"
MF "v[36:39],a[48:51],v[124:127],v[36:39]\n"
AR "v233,a77\n"
AR "v234,a78\n"
MF "v[36:39],a[8:11],v[120:123],v[36:39]\n"
AR "v235,a79\n"
"v_fma_f32 v228,v228,v212,v218\n"
MF "v[72:75],a[8:11],v[144:147],v[72:75]\n"
"ds_read_b128 a[76:79],v236 offset:61440\n"
"v_fma_f32 v229,v229,v212,v218\n"
MF "v[72:75],a[48:51],v[148:151],v[72:75]\n"
"v_fma_f32 v230,v230,v212,v218\n"
"v_fma_f32 v231,v231,v212,v218\n"
MF "v[72:75],a[88:91],v[152:155],v[72:75]\n"
"v_fma_f32 v232,v232,v212,v218\n"
"v_fma_f32 v233,v233,v212,v218\n"
MF "v[72:75],a[28:31],v[156:159],v[72:75]\n"
"v_fma_f32 v234,v234,v212,v218\n"
"v_fma_f32 v235,v235,v212,v218\n"
MF "v[72:75],a[68:71],v[160:163],v[72:75]\n"
CV "v228,v228,v229\n"
CV "v229,v230,v231\n"
MF "v[72:75],a[108:111],v[164:167],v[72:75]\n"
CV "v230,v232,v233\n"
CV "v231,v234,v235\n"
MF "v[76:79],a[108:111],v[238:241],v[76:79]\n"
AW "a56,v228\n"
AW "a57,v229\n"
MF "v[76:79],a[68:71],v[248:251],v[76:79]\n"
AW "a58,v230\n"
AW "a59,v231\n"
MF "v[76:79],a[28:31],v[244:247],v[76:79]\n"
AR "v220,a96\n"
AR "v221,a97\n"
MF "v[76:79],a[88:91],v[204:207],v[76:79]\n"
AR "v222,a98\n"
AR "v223,a99\n"
MF "v[76:79],a[48:51],v[172:175],v[76:79]\n"
AR "v224,a116\n"
AR "v225,a117\n"
MF "v[76:79],a[8:11],v[168:171],v[76:79]\n"
"s_waitcnt lgkmcnt(2)\n"
AR "v226,a118\n"
AR "v227,a119\n"
MF "v[84:87],a[12:15],v[168:171],v[84:87]\n"
"ds_read_b128 a[116:119],v237 offset:16384\n"
"v_fma_f32 v220,v220,v212,v218\n"
MF "v[84:87],a[52:55],v[172:175],v[84:87]\n"
"v_fma_f32 v221,v221,v212,v218\n"
"v_fma_f32 v222,v222,v212,v218\n"
MF "v[84:87],a[92:95],v[204:207],v[84:87]\n"
"v_fma_f32 v223,v223,v212,v218\n"
"v_fma_f32 v224,v224,v212,v218\n"
MF "v[84:87],a[32:35],v[244:247],v[84:87]\n"
"v_fma_f32 v225,v225,v212,v218\n"
"v_fma_f32 v226,v226,v212,v218\n"
MF "v[84:87],a[72:75],v[248:251],v[84:87]\n"
"v_fma_f32 v227,v227,v212,v218\n"
CV "v220,v220,v221\n"
MF "v[84:87],a[112:115],v[238:241],v[84:87]\n"
CV "v221,v222,v223\n"
CV "v222,v224,v225\n"
MF "v[80:83],a[112:115],v[164:167],v[80:83]\n"
CV "v223,v226,v227\n"
AW "a96,v220\n"
MF "v[80:83],a[72:75],v[160:163],v[80:83]\n"
AW "a97,v221\n"
AW "a98,v222\n"
MF "v[80:83],a[32:35],v[156:159],v[80:83]\n"
AW "a99,v223\n"
"v_mov_b32 v15,0\n"
MF "v[80:83],a[92:95],v[152:155],v[80:83]\n"
"v_lshrrev_b32 v242,5,v9\n"
"v_min_u32 v242,23,v242\n"
MF "v[80:83],a[52:55],v[148:151],v[80:83]\n"
"v_mul_u32_u24 v242,160,v242\n"
"v_and_or_b32 v242,v9,16,v242\n"
MF "v[80:83],a[12:15],v[144:147],v[80:83]\n"
"v_lshlrev_b32 v242,3,v242\n"
AR "v8,a252\n"
MF "v[44:47],a[12:15],v[120:123],v[44:47]\n"
AR "v11,a253\n"
"v_bfe_u32 v224,v9,5,3\n"
MF "v[44:47],a[52:55],v[124:127],v[44:47]\n"
"s_add_u32 s62,s74,s68\n"
"s_add_u32 s68,s68,256\n"
"s_and_b32 s68,s68,768\n"
MF "v[44:47],a[92:95],v[128:131],v[44:47]\n"
"buffer_load_dword v243,v242,s[48:51],s62 offen\n"
MF "v[44:47],a[32:35],v[132:135],v[44:47]\n"
"v_bfe_u32 v225,v9,8,2\n"
"v_lshlrev_b32 v225,1,v225\n"
MF "v[44:47],a[72:75],v[136:139],v[44:47]\n"
"v_mov_b32 v14,s76\n"
"s_add_u32 s76,s76,1\n"
MF "v[44:47],a[112:115],v[140:143],v[44:47]\n"
"buffer_store_dword v14,v15,s[40:43],s94 offen sc1\n"
MF "v[40:43],a[112:115],v[116:119],v[40:43]\n"
"v_and_b32 v226,16,v9\n"
"v_lshlrev_b32_e64 v226,v226,s9\n"
MF "v[40:43],a[72:75],v[112:115],v[40:43]\n"
"v_sub_u32 v224,v224,v225\n"
"v_cmp_eq_u32 vcc,0,v224\n"
MF "v[40:43],a[32:35],v[108:111],v[40:43]\n"
"s_nop 0\n"
"v_cndmask_b32 v220,0,v226,vcc\n"
"v_cmp_eq_u32 vcc,1,v224\n"
MF "v[40:43],a[92:95],v[104:107],v[40:43]\n"
"s_nop 0\n"
"v_cndmask_b32 v221,0,v226,vcc\n"
AW "a248,0\n"
MF "v[40:43],a[52:55],v[100:103],v[40:43]\n"
AW "a249,0\n"
AW "a250,v220\n"
MF "v[40:43],a[12:15],v[96:99],v[40:43]\n"
"s_waitcnt lgkmcnt(0)\n"
AW "a251,v221\n"
AW "a252,v220\n"
MF "v[48:51],a[16:19],v[96:99],v[48:51]\n"
AW "a253,v221\n"
AW "a254,0\n"
MF "v[48:51],a[56:59],v[100:103],v[48:51]\n"
AW "a255,0\n"
"v_bfe_u32 v12,v9,4,4\n"
MF "v[48:51],a[96:99],v[104:107],v[48:51]\n"
"v_lshlrev_b32 v12,2,v12\n"
"v_lshrrev_b32 v13,2,v9\n"
MF "v[48:51],a[36:39],v[108:111],v[48:51]\n"
"v_add_u32 v13,s58,v13\n"
"v_add_u32 v4,24576,v4\n"
MF "v[48:51],a[76:79],v[112:115],v[48:51]\n"
"v_add_u32 v5,24576,v5\n"
"v_add_u32 v6,24576,v6\n"
MF "v[48:51],a[116:119],v[116:119],v[48:51]\n"
"v_add_u32 v7,24576,v7\n"
MF "v[52:55],a[116:119],v[140:143],v[52:55]\n"
MF "v[52:55],a[76:79],v[136:139],v[52:55]\n"
MF "v[52:55],a[36:39],v[132:135],v[52:55]\n"
"s_waitcnt vmcnt(2) lgkmcnt(0)\n"
"s_barrier\n"
MF "v[52:55],a[96:99],v[128:131],v[52:55]\n"
TR "v[176:177],v0 offset:24576\n"
MF "v[52:55],a[56:59],v[124:127],v[52:55]\n"
TR "v[178:179],v0 offset:26624\n"
MF "v[52:55],a[16:19],v[120:123],v[52:55]\n"
MF "v[88:91],a[16:19],v[144:147],v[88:91]\n"
TR "v[180:181],v0 offset:28672\n"
MF "v[88:91],a[56:59],v[148:151],v[88:91]\n"
TR "v[182:183],v0 offset:30720\n"
MF "v[88:91],a[96:99],v[152:155],v[88:91]\n"
MF "v[88:91],a[36:39],v[156:159],v[88:91]\n"
TR "v[184:185],v0 offset:32768\n"
MF "v[88:91],a[76:79],v[160:163],v[88:91]\n"
TR "v[186:187],v0 offset:34816\n"
MF "v[88:91],a[116:119],v[164:167],v[88:91]\n"
MF "v[92:95],a[116:119],v[238:241],v[92:95]\n"
MF "v[92:95],a[76:79],v[248:251],v[92:95]\n"
MF "v[92:95],a[36:39],v[244:247],v[92:95]\n"
MF "v[92:95],a[96:99],v[204:207],v[92:95]\n"
MF "v[92:95],a[56:59],v[172:175],v[92:95]\n"
MF "v[92:95],a[16:19],v[168:171],v[92:95]\n"
"s_waitcnt lgkmcnt(0)\n"
".Lpl1:\n"
MF "v[96:99],a[0:3],v[176:179],v[252:255]\n"
TR "v[188:189],v1 offset:24576\n"
MF "v[96:99],a[40:43],v[180:183],v[96:99]\n"
"s_add_u32 s82,s82,s71\n"
TR "v[190:191],v1 offset:26624\n"
MF "v[96:99],a[80:83],v[184:187],v[96:99]\n"
LD "v[224:227],v9,s[40:43],s82 offen sc1\n"
MF "v[104:107],a[84:87],v[184:187],v[252:255]\n"
TR "a[240:241],v1 offset:28672\n"
MF "v[104:107],a[44:47],v[180:183],v[104:107]\n"
TR "a[242:243],v1 offset:30720\n"
MF "v[104:107],a[4:7],v[176:179],v[104:107]\n"
MF "v[112:115],a[8:11],v[176:179],v[252:255]\n"
TR "a[244:245],v1 offset:32768\n"
MF "v[112:115],a[48:51],v[180:183],v[112:115]\n"
TR "a[246:247],v1 offset:34816\n"
MF "v[112:115],a[88:91],v[184:187],v[112:115]\n"
MF "v[120:123],a[92:95],v[184:187],v[252:255]\n"
MF "v[120:123],a[52:55],v[180:183],v[120:123]\n"
"s_mul_i32 s95,s92,3277\n"
"s_lshr_b32 s95,s95,16\n"
"s_mul_i32 s59,s95,20\n"
MF "v[120:123],a[12:15],v[176:179],v[120:123]\n"
"s_sub_u32 s59,s92,s59\n"
"s_cmp_ge_u32 s59,10\n"
"s_cselect_b32 s96,4294557760,0\n"
MF "v[128:131],a[16:19],v[176:179],v[252:255]\n"
"s_add_u32 s82,s82,s66\n"
"s_add_u32 s96,s96,1024\n"
"s_mul_i32 s59,s59,40960\n"
MF "v[128:131],a[56:59],v[180:183],v[128:131]\n"
LD "v[228:231],v9,s[40:43],s82 offen sc1\n"
MF "v[128:131],a[96:99],v[184:187],v[128:131]\n"
"s_add_u32 s96,s96,s59\n"
"s_lshl_b32 s59,s95,7\n"
"s_waitcnt lgkmcnt(0)\n"
MF "v[132:135],a[96:99],a[244:247],v[252:255]\n"
"s_add_u32 s96,s96,s59\n"
"s_add_u32 s95,s95,3\n"
TR "v[176:177],v2 offset:24576\n"
MF "v[132:135],a[56:59],a[240:243],v[132:135]\n"
"s_cmp_ge_u32 s92,40\n"
"s_cselect_b32 s95,99,s95\n"
TR "v[178:179],v2 offset:26624\n"
MF "v[132:135],a[16:19],v[188:191],v[132:135]\n"
"s_cmp_ge_u32 s85,5\n"
"s_cselect_b32 s95,99,s95\n"
"s_cselect_b32 s72,s85,0\n"
MF "v[124:127],a[12:15],v[188:191],v[252:255]\n"
TR "v[180:181],v2 offset:28672\n"
MF "v[124:127],a[52:55],a[240:243],v[124:127]\n"
TR "v[182:183],v2 offset:30720\n"
MF "v[124:127],a[92:95],a[244:247],v[124:127]\n"
MF "v[116:119],a[88:91],a[244:247],v[252:255]\n"
TR "v[184:185],v2 offset:32768\n"
MF "v[116:119],a[48:51],a[240:243],v[116:119]\n"
"s_add_u32 s82,s82,s71\n"
TR "v[186:187],v2 offset:34816\n"
MF "v[116:119],a[8:11],v[188:191],v[116:119]\n"
LD "v[232:235],v9,s[40:43],s82 offen sc1\n"
MF "v[108:111],a[4:7],v[188:191],v[252:255]\n"
MF "v[108:111],a[44:47],a[240:243],v[108:111]\n"
MF "v[108:111],a[84:87],a[244:247],v[108:111]\n"
MF "v[100:103],a[80:83],a[244:247],v[252:255]\n"
MF "v[100:103],a[40:43],a[240:243],v[100:103]\n"
MF "v[100:103],a[0:3],v[188:191],v[100:103]\n"
"s_waitcnt lgkmcnt(0)\n"
MF "v[136:139],a[0:3],v[176:179],v[252:255]\n"
TR "v[188:189],v3 offset:24576\n"
MF "v[136:139],a[40:43],v[180:183],v[136:139]\n"
"s_add_u32 s82,s82,s71\n"
TR "v[190:191],v3 offset:26624\n"
MF "v[136:139],a[80:83],v[184:187],v[136:139]\n"
LD "v[236:239],v9,s[40:43],s82 offen sc1\n"
MF "v[144:147],a[84:87],v[184:187],v[252:255]\n"
TR "a[240:241],v3 offset:28672\n"
MF "v[144:147],a[44:47],v[180:183],v[144:147]\n"
TR "a[242:243],v3 offset:30720\n"
MF "v[144:147],a[4:7],v[176:179],v[144:147]\n"
MF "v[152:155],a[8:11],v[176:179],v[252:255]\n"
TR "a[244:245],v3 offset:32768\n"
MF "v[152:155],a[48:51],v[180:183],v[152:155]\n"
TR "a[246:247],v3 offset:34816\n"
MF "v[152:155],a[88:91],v[184:187],v[152:155]\n"
MF "v[160:163],a[92:95],v[184:187],v[252:255]\n"
MF "v[160:163],a[52:55],v[180:183],v[160:163]\n"
MF "v[160:163],a[12:15],v[176:179],v[160:163]\n"
MF "v[168:171],a[16:19],v[176:179],v[252:255]\n"
MF "v[168:171],a[56:59],v[180:183],v[168:171]\n"
MF "v[168:171],a[96:99],v[184:187],v[168:171]\n"
"s_waitcnt lgkmcnt(0)\n"
"s_barrier\n"
MF "v[172:175],a[96:99],a[244:247],v[252:255]\n"
TR "v[176:177],v0\n"
MF "v[172:175],a[56:59],a[240:243],v[172:175]\n"
TR "v[178:179],v0 offset:2048\n"
MF "v[172:175],a[16:19],v[188:191],v[172:175]\n"
"ds_read2st64_b32 v[192:193],v13 offset0:192 offset1:193\n"
MF "v[164:167],a[12:15],v[188:191],v[252:255]\n"
TR "v[180:181],v0 offset:4096\n"
MF "v[164:167],a[52:55],a[240:243],v[164:167]\n"
TR "v[182:183],v0 offset:6144\n"
MF "v[164:167],a[92:95],a[244:247],v[164:167]\n"
"ds_read2st64_b32 v[194:195],v13 offset0:194 offset1:195\n"
MF "v[156:159],a[88:91],a[244:247],v[252:255]\n"
TR "v[184:185],v0 offset:8192\n"
MF "v[156:159],a[48:51],a[240:243],v[156:159]\n"
TR "v[186:187],v0 offset:10240\n"
MF "v[156:159],a[8:11],v[188:191],v[156:159]\n"
"ds_read2st64_b32 v[196:197],v13 offset0:196 offset1:197\n"
MF "v[148:151],a[4:7],v[188:191],v[252:255]\n"
"ds_read2st64_b32 v[198:199],v13 offset0:198 offset1:199\n"
MF "v[148:151],a[44:47],a[240:243],v[148:151]\n"
"buffer_load_dword v10,v12,s[40:43],s86 offen sc1\n"
MF "v[148:151],a[84:87],a[244:247],v[148:151]\n"
MF "v[140:143],a[80:83],a[244:247],v[252:255]\n"
MF "v[140:143],a[40:43],a[240:243],v[140:143]\n"
MF "v[140:143],a[0:3],v[188:191],v[140:143]\n"
"s_waitcnt lgkmcnt(2)\n"
MF "v[96:99],a[120:123],v[176:179],v[96:99]\n"
"s_waitcnt lgkmcnt(0)\n"
TR "v[188:189],v1\n"
"v_add_f32 v16,v16,v192\n"
MF "v[96:99],a[160:163],v[180:183],v[96:99]\n"
TR "v[190:191],v1 offset:2048\n"
"v_add_f32 v17,v17,v193\n"
MF "v[96:99],a[200:203],v[184:187],v[96:99]\n"
"ds_read2st64_b32 v[200:201],v13 offset0:200 offset1:201\n"
"v_add_f32 v18,v18,v194\n"
"s_waitcnt vmcnt(9)\n"
"v_mov_b32 v14,s76\n"
"s_add_u32 s76,s76,1\n"
MF "v[104:107],a[204:207],v[184:187],v[104:107]\n"
TR "a[240:241],v1 offset:4096\n"
"v_add_f32 v19,v19,v195\n"
"buffer_store_dword v14,v15,s[40:43],s94 offen sc1\n"
MF "v[104:107],a[164:167],v[180:183],v[104:107]\n"
TR "a[242:243],v1 offset:6144\n"
"v_add_f32 v20,v20,v196\n"
MF "v[104:107],a[124:127],v[176:179],v[104:107]\n"
"ds_read2st64_b32 v[202:203],v13 offset0:202 offset1:203\n"
"v_add_f32 v21,v21,v197\n"
MF "v[112:115],a[128:131],v[176:179],v[112:115]\n"
TR "a[244:245],v1 offset:8192\n"
"v_add_f32 v22,v22,v198\n"
MF "v[112:115],a[168:171],v[180:183],v[112:115]\n"
TR "a[246:247],v1 offset:10240\n"
"v_add_f32 v23,v23,v199\n"
MF "v[112:115],a[208:211],v[184:187],v[112:115]\n"
"ds_read2st64_b32 v[204:205],v13 offset0:204 offset1:205\n"
CV "v16,v16,v17\n"
MF "v[120:123],a[212:215],v[184:187],v[120:123]\n"
"ds_read2st64_b32 v[206:207],v13 offset0:206 offset1:207\n"
CV "v17,v18,v19\n"
MF "v[120:123],a[172:175],v[180:183],v[120:123]\n"
"s_add_u32 s87,s90,0\n"
CV "v18,v20,v21\n"
CV "v19,v22,v23\n"
MF "v[120:123],a[132:135],v[176:179],v[120:123]\n"
MF "v[128:131],a[136:139],v[176:179],v[128:131]\n"
"buffer_store_dwordx4 v[16:19],v9,s[40:43],s87 offen sc1\n"
MF "v[128:131],a[176:179],v[180:183],v[128:131]\n"
MF "v[128:131],a[216:219],v[184:187],v[128:131]\n"
"s_waitcnt lgkmcnt(2)\n"
MF "v[132:135],a[216:219],a[244:247],v[132:135]\n"
"s_waitcnt lgkmcnt(0)\n"
TR "v[176:177],v2\n"
"v_add_f32 v24,v24,v200\n"
MF "v[132:135],a[176:179],a[240:243],v[132:135]\n"
TR "v[178:179],v2 offset:2048\n"
"v_add_f32 v25,v25,v201\n"
MF "v[132:135],a[136:139],v[188:191],v[132:135]\n"
"ds_read2st64_b32 v[192:193],v13 offset0:208 offset1:209\n"
"v_add_f32 v26,v26,v202\n"
MF "v[124:127],a[132:135],v[188:191],v[124:127]\n"
TR "v[180:181],v2 offset:4096\n"
"v_add_f32 v27,v27,v203\n"
MF "v[124:127],a[172:175],a[240:243],v[124:127]\n"
TR "v[182:183],v2 offset:6144\n"
"v_add_f32 v28,v28,v204\n"
MF "v[124:127],a[212:215],a[244:247],v[124:127]\n"
"ds_read2st64_b32 v[194:195],v13 offset0:210 offset1:211\n"
"v_add_f32 v29,v29,v205\n"
MF "v[116:119],a[208:211],a[244:247],v[116:119]\n"
TR "v[184:185],v2 offset:8192\n"
"v_add_f32 v30,v30,v206\n"
MF "v[116:119],a[168:171],a[240:243],v[116:119]\n"
TR "v[186:187],v2 offset:10240\n"
"v_add_f32 v31,v31,v207\n"
MF "v[116:119],a[128:131],v[188:191],v[116:119]\n"
"ds_read2st64_b32 v[196:197],v13 offset0:212 offset1:213\n"
CV "v24,v24,v25\n"
MF "v[108:111],a[124:127],v[188:191],v[108:111]\n"
"ds_read2st64_b32 v[198:199],v13 offset0:214 offset1:215\n"
CV "v25,v26,v27\n"
MF "v[108:111],a[164:167],a[240:243],v[108:111]\n"
"s_add_u32 s87,s90,1024\n"
CV "v26,v28,v29\n"
CV "v27,v30,v31\n"
MF "v[108:111],a[204:207],a[244:247],v[108:111]\n"
MF "v[100:103],a[200:203],a[244:247],v[100:103]\n"
"buffer_store_dwordx4 v[24:27],v9,s[40:43],s87 offen sc1\n"
MF "v[100:103],a[160:163],a[240:243],v[100:103]\n"
MF "v[100:103],a[120:123],v[188:191],v[100:103]\n"
"s_waitcnt lgkmcnt(2)\n"
MF "v[136:139],a[120:123],v[176:179],v[136:139]\n"
"s_waitcnt lgkmcnt(0)\n"
TR "v[188:189],v3\n"
"v_add_f32 v32,v32,v192\n"
MF "v[136:139],a[160:163],v[180:183],v[136:139]\n"
TR "v[190:191],v3 offset:2048\n"
"v_add_f32 v33,v33,v193\n"
MF "v[136:139],a[200:203],v[184:187],v[136:139]\n"
"ds_read2st64_b32 v[200:201],v13 offset0:216 offset1:217\n"
"v_add_f32 v34,v34,v194\n"
MF "v[144:147],a[204:207],v[184:187],v[144:147]\n"
TR "a[240:241],v3 offset:4096\n"
"v_add_f32 v35,v35,v195\n"
MF "v[144:147],a[164:167],v[180:183],v[144:147]\n"
TR "a[242:243],v3 offset:6144\n"
"v_add_f32 v36,v36,v196\n"
MF "v[144:147],a[124:127],v[176:179],v[144:147]\n"
"ds_read2st64_b32 v[202:203],v13 offset0:218 offset1:219\n"
"v_add_f32 v37,v37,v197\n"
MF "v[152:155],a[128:131],v[176:179],v[152:155]\n"
TR "a[244:245],v3 offset:8192\n"
"v_add_f32 v38,v38,v198\n"
MF "v[152:155],a[168:171],v[180:183],v[152:155]\n"
TR "a[246:247],v3 offset:10240\n"
"v_add_f32 v39,v39,v199\n"
MF "v[152:155],a[208:211],v[184:187],v[152:155]\n"
"ds_read2st64_b32 v[204:205],v13 offset0:220 offset1:221\n"
CV "v32,v32,v33\n"
MF "v[160:163],a[212:215],v[184:187],v[160:163]\n"
"ds_read2st64_b32 v[206:207],v13 offset0:222 offset1:223\n"
CV "v33,v34,v35\n"
MF "v[160:163],a[172:175],v[180:183],v[160:163]\n"
"s_add_u32 s87,s90,2048\n"
CV "v34,v36,v37\n"
CV "v35,v38,v39\n"
MF "v[160:163],a[132:135],v[176:179],v[160:163]\n"
MF "v[168:171],a[136:139],v[176:179],v[168:171]\n"
"buffer_store_dwordx4 v[32:35],v9,s[40:43],s87 offen sc1\n"
MF "v[168:171],a[176:179],v[180:183],v[168:171]\n"
MF "v[168:171],a[216:219],v[184:187],v[168:171]\n"
"s_waitcnt lgkmcnt(0)\n"
"s_barrier\n"
MF "v[172:175],a[216:219],a[244:247],v[172:175]\n"
TR "v[176:177],v0 offset:49152\n"
"v_add_f32 v40,v40,v200\n"
MF "v[172:175],a[176:179],a[240:243],v[172:175]\n"
TR "v[178:179],v0 offset:51200\n"
"v_add_f32 v41,v41,v201\n"
MF "v[172:175],a[136:139],v[188:191],v[172:175]\n"
"ds_read2st64_b32 v[192:193],v13 offset0:224 offset1:225\n"
"v_add_f32 v42,v42,v202\n"
MF "v[164:167],a[132:135],v[188:191],v[164:167]\n"
TR "v[180:181],v0 offset:53248\n"
"v_add_f32 v43,v43,v203\n"
MF "v[164:167],a[172:175],a[240:243],v[164:167]\n"
TR "v[182:183],v0 offset:55296\n"
"v_add_f32 v44,v44,v204\n"
MF "v[164:167],a[212:215],a[244:247],v[164:167]\n"
"ds_read2st64_b32 v[194:195],v13 offset0:226 offset1:227\n"
"v_add_f32 v45,v45,v205\n"
MF "v[156:159],a[208:211],a[244:247],v[156:159]\n"
TR "v[184:185],v0 offset:57344\n"
"v_add_f32 v46,v46,v206\n"
MF "v[156:159],a[168:171],a[240:243],v[156:159]\n"
TR "v[186:187],v0 offset:59392\n"
"v_add_f32 v47,v47,v207\n"
MF "v[156:159],a[128:131],v[188:191],v[156:159]\n"
"ds_read2st64_b32 v[196:197],v13 offset0:228 offset1:229\n"
CV "v40,v40,v41\n"
MF "v[148:151],a[124:127],v[188:191],v[148:151]\n"
"ds_read2st64_b32 v[198:199],v13 offset0:230 offset1:231\n"
CV "v41,v42,v43\n"
MF "v[148:151],a[164:167],a[240:243],v[148:151]\n"
"s_add_u32 s87,s90,3072\n"
CV "v42,v44,v45\n"
CV "v43,v46,v47\n"
MF "v[148:151],a[204:207],a[244:247],v[148:151]\n"
MF "v[140:143],a[200:203],a[244:247],v[140:143]\n"
"buffer_store_dwordx4 v[40:43],v9,s[40:43],s87 offen sc1\n"
MF "v[140:143],a[160:163],a[240:243],v[140:143]\n"
MF "v[140:143],a[120:123],v[188:191],v[140:143]\n"
"s_waitcnt lgkmcnt(2)\n"
MF "v[96:99],a[20:23],v[176:179],v[96:99]\n"
"s_waitcnt lgkmcnt(0)\n"
TR "v[188:189],v1 offset:49152\n"
"v_add_f32 v48,v48,v192\n"
MF "v[96:99],a[60:63],v[180:183],v[96:99]\n"
TR "v[190:191],v1 offset:51200\n"
"v_add_f32 v49,v49,v193\n"
MF "v[96:99],a[100:103],v[184:187],v[96:99]\n"
"ds_read2st64_b32 v[200:201],v13 offset0:0 offset1:1\n"
"v_add_f32 v50,v50,v194\n"
MF "v[104:107],a[104:107],v[184:187],v[104:107]\n"
TR "a[240:241],v1 offset:53248\n"
"v_add_f32 v51,v51,v195\n"
MF "v[104:107],a[64:67],v[180:183],v[104:107]\n"
TR "a[242:243],v1 offset:55296\n"
"v_add_f32 v52,v52,v196\n"
MF "v[104:107],a[24:27],v[176:179],v[104:107]\n"
"ds_read2st64_b32 v[202:203],v13 offset0:2 offset1:3\n"
"v_add_f32 v53,v53,v197\n"
MF "v[112:115],a[28:31],v[176:179],v[112:115]\n"
TR "a[244:245],v1 offset:57344\n"
"v_add_f32 v54,v54,v198\n"
MF "v[112:115],a[68:71],v[180:183],v[112:115]\n"
TR "a[246:247],v1 offset:59392\n"
"v_add_f32 v55,v55,v199\n"
MF "v[112:115],a[108:111],v[184:187],v[112:115]\n"
"ds_read2st64_b32 v[204:205],v13 offset0:4 offset1:5\n"
CV "v48,v48,v49\n"
MF "v[120:123],a[112:115],v[184:187],v[120:123]\n"
"ds_read2st64_b32 v[206:207],v13 offset0:6 offset1:7\n"
CV "v49,v50,v51\n"
MF "v[120:123],a[72:75],v[180:183],v[120:123]\n"
"s_add_u32 s87,s90,4096\n"
CV "v50,v52,v53\n"
CV "v51,v54,v55\n"
MF "v[120:123],a[32:35],v[176:179],v[120:123]\n"
MF "v[128:131],a[36:39],v[176:179],v[128:131]\n"
"buffer_store_dwordx4 v[48:51],v9,s[40:43],s87 offen sc1\n"
MF "v[128:131],a[76:79],v[180:183],v[128:131]\n"
MF "v[128:131],a[116:119],v[184:187],v[128:131]\n"
"s_waitcnt lgkmcnt(2)\n"
MF "v[132:135],a[116:119],a[244:247],v[132:135]\n"
"s_waitcnt lgkmcnt(0)\n"
TR "v[176:177],v2 offset:49152\n"
"v_add_f32 v56,v56,v200\n"
MF "v[132:135],a[76:79],a[240:243],v[132:135]\n"
TR "v[178:179],v2 offset:51200\n"
"v_add_f32 v57,v57,v201\n"
MF "v[132:135],a[36:39],v[188:191],v[132:135]\n"
"ds_read2st64_b32 v[192:193],v13 offset0:8 offset1:9\n"
"v_add_f32 v58,v58,v202\n"
MF "v[124:127],a[32:35],v[188:191],v[124:127]\n"
TR "v[180:181],v2 offset:53248\n"
"v_add_f32 v59,v59,v203\n"
MF "v[124:127],a[72:75],a[240:243],v[124:127]\n"
TR "v[182:183],v2 offset:55296\n"
"v_add_f32 v60,v60,v204\n"
MF "v[124:127],a[112:115],a[244:247],v[124:127]\n"
"ds_read2st64_b32 v[194:195],v13 offset0:10 offset1:11\n"
"v_add_f32 v61,v61,v205\n"
MF "v[116:119],a[108:111],a[244:247],v[116:119]\n"
TR "v[184:185],v2 offset:57344\n"
"v_add_f32 v62,v62,v206\n"
MF "v[116:119],a[68:71],a[240:243],v[116:119]\n"
TR "v[186:187],v2 offset:59392\n"
"v_add_f32 v63,v63,v207\n"
MF "v[116:119],a[28:31],v[188:191],v[116:119]\n"
"ds_read2st64_b32 v[196:197],v13 offset0:12 offset1:13\n"
CV "v56,v56,v57\n"
MF "v[108:111],a[24:27],v[188:191],v[108:111]\n"
"ds_read2st64_b32 v[198:199],v13 offset0:14 offset1:15\n"
CV "v57,v58,v59\n"
MF "v[108:111],a[64:67],a[240:243],v[108:111]\n"
"s_add_u32 s87,s90,10240\n"
CV "v58,v60,v61\n"
CV "v59,v62,v63\n"
MF "v[108:111],a[104:107],a[244:247],v[108:111]\n"
MF "v[100:103],a[100:103],a[244:247],v[100:103]\n"
"buffer_store_dwordx4 v[56:59],v9,s[40:43],s87 offen sc1\n"
MF "v[100:103],a[60:63],a[240:243],v[100:103]\n"
"s_waitcnt vmcnt(7)\n"
"s_cmp_eq_u32 s83,0xf0000000\n"
MF "v[100:103],a[20:23],v[188:191],v[100:103]\n"
"s_cbranch_scc1 .Ljs13\n"
"s_cmp_eq_u32 s83,0xf0000010\n"
"s_cbranch_scc1 .Ljc13\n"
MF "v[248:251],v[210:213],a[250:253],0\n"
"v_mov_b32 v240,v208\n"
"v_mov_b32 v241,v209\n"
MF "v[244:247],v[214:217],a[250:253],0\n"
MF "v[248:251],v[218:221],a[250:253],v[248:251]\n"
MF "v[244:247],v[222:225],a[250:253],v[244:247]\n"
MF "v[248:251],v[226:229],a[250:253],v[248:251]\n"
MF "v[244:247],v[230:233],a[250:253],v[244:247]\n"
MF "v[248:251],v[234:237],a[250:253],v[248:251]\n"
MF "v[244:247],v[238:241],a[250:253],v[244:247]\n"
"s_branch .Ljs13\n"
".Ljc13:\n"
MF "v[136:139],a[252:255],v[208:211],v[136:139]\n"
MF "v[144:147],a[252:255],v[212:215],v[144:147]\n"
MF "v[152:155],a[252:255],v[216:219],v[152:155]\n"
MF "v[160:163],a[252:255],v[220:223],v[160:163]\n"
MF "v[168:171],a[252:255],v[224:227],v[168:171]\n"
MF "v[140:143],a[248:251],v[208:211],v[140:143]\n"
MF "v[148:151],a[248:251],v[212:215],v[148:151]\n"
MF "v[156:159],a[248:251],v[216:219],v[156:159]\n"
MF "v[164:167],a[248:251],v[220:223],v[164:167]\n"
MF "v[172:175],a[248:251],v[224:227],v[172:175]\n"
".Ljs13:\n"
"s_waitcnt lgkmcnt(2)\n"
MF "v[136:139],a[20:23],v[176:179],v[136:139]\n"
"s_waitcnt lgkmcnt(0)\n"
TR "v[188:189],v3 offset:49152\n"
"v_add_f32 v64,v64,v192\n"
MF "v[136:139],a[60:63],v[180:183],v[136:139]\n"
TR "v[190:191],v3 offset:51200\n"
"v_add_f32 v65,v65,v193\n"
MF "v[136:139],a[100:103],v[184:187],v[136:139]\n"
"ds_read2st64_b32 v[200:201],v13 offset0:16 offset1:17\n"
"v_add_f32 v66,v66,v194\n"
MF "v[144:147],a[104:107],v[184:187],v[144:147]\n"
TR "a[240:241],v3 offset:53248\n"
"v_add_f32 v67,v67,v195\n"
MF "v[144:147],a[64:67],v[180:183],v[144:147]\n"
TR "a[242:243],v3 offset:55296\n"
"v_add_f32 v68,v68,v196\n"
MF "v[144:147],a[24:27],v[176:179],v[144:147]\n"
"ds_read2st64_b32 v[202:203],v13 offset0:18 offset1:19\n"
"v_add_f32 v69,v69,v197\n"
MF "v[152:155],a[28:31],v[176:179],v[152:155]\n"
TR "a[244:245],v3 offset:57344\n"
"v_add_f32 v70,v70,v198\n"
MF "v[152:155],a[68:71],v[180:183],v[152:155]\n"
TR "a[246:247],v3 offset:59392\n"
"v_add_f32 v71,v71,v199\n"
MF "v[152:155],a[108:111],v[184:187],v[152:155]\n"
"ds_read2st64_b32 v[204:205],v13 offset0:20 offset1:21\n"
CV "v64,v64,v65\n"
MF "v[160:163],a[112:115],v[184:187],v[160:163]\n"
"ds_read2st64_b32 v[206:207],v13 offset0:22 offset1:23\n"
CV "v65,v66,v67\n"
MF "v[160:163],a[72:75],v[180:183],v[160:163]\n"
"s_add_u32 s87,s90,11264\n"
CV "v66,v68,v69\n"
CV "v67,v70,v71\n"
MF "v[160:163],a[32:35],v[176:179],v[160:163]\n"
"s_lshl_b32 s82,s92,10\n"
"v_sub_u32 v10,v10,v11\n"
"v_cmp_gt_u32 vcc,s95,v10\n"
MF "v[168:171],a[36:39],v[176:179],v[168:171]\n"
"buffer_store_dwordx4 v[64:67],v9,s[40:43],s87 offen sc1\n"
MF "v[168:171],a[76:79],v[180:183],v[168:171]\n"
"s_add_u32 s82,s82,s93\n"
CV "v244,v244,v245\n"
CV "v245,v246,v247\n"
MF "v[168:171],a[116:119],v[184:187],v[168:171]\n"
"s_waitcnt lgkmcnt(0)\n"
"s_barrier\n"
MF "v[172:175],a[116:119],a[244:247],v[172:175]\n"
TR "v[176:177],v4\n"
"v_add_f32 v72,v72,v200\n"
MF "v[172:175],a[76:79],a[240:243],v[172:175]\n"
TR "v[178:179],v4 offset:2048\n"
"v_add_f32 v73,v73,v201\n"
MF "v[172:175],a[36:39],v[188:191],v[172:175]\n"
"ds_read2st64_b32 v[192:193],v13 offset0:24 offset1:25\n"
"v_add_f32 v74,v74,v202\n"
MF "v[164:167],a[32:35],v[188:191],v[164:167]\n"
TR "v[180:181],v4 offset:4096\n"
"v_add_f32 v75,v75,v203\n"
MF "v[164:167],a[72:75],a[240:243],v[164:167]\n"
TR "v[182:183],v4 offset:6144\n"
"v_add_f32 v76,v76,v204\n"
MF "v[164:167],a[112:115],a[244:247],v[164:167]\n"
"ds_read2st64_b32 v[194:195],v13 offset0:26 offset1:27\n"
"v_add_f32 v77,v77,v205\n"
MF "v[156:159],a[108:111],a[244:247],v[156:159]\n"
TR "v[184:185],v4 offset:8192\n"
"v_add_f32 v78,v78,v206\n"
MF "v[156:159],a[68:71],a[240:243],v[156:159]\n"
TR "v[186:187],v4 offset:10240\n"
"v_add_f32 v79,v79,v207\n"
MF "v[156:159],a[28:31],v[188:191],v[156:159]\n"
"ds_read2st64_b32 v[196:197],v13 offset0:28 offset1:29\n"
CV "v72,v72,v73\n"
MF "v[148:151],a[24:27],v[188:191],v[148:151]\n"
"ds_read2st64_b32 v[198:199],v13 offset0:30 offset1:31\n"
CV "v73,v74,v75\n"
MF "v[148:151],a[64:67],a[240:243],v[148:151]\n"
"s_add_u32 s87,s90,12288\n"
CV "v74,v76,v77\n"
CV "v75,v78,v79\n"
MF "v[148:151],a[104:107],a[244:247],v[148:151]\n"
"s_cmp_eq_u64 vcc,0\n"
"s_cselect_b32 s82,s82,0xf0000000\n"
"s_cselect_b32 s84,s96,0xf0000000\n"
"s_cselect_b32 s59,32,0\n"
MF "v[140:143],a[100:103],a[244:247],v[140:143]\n"
"buffer_store_dwordx4 v[72:75],v9,s[40:43],s87 offen sc1\n"
MF "v[140:143],a[60:63],a[240:243],v[140:143]\n"
"s_add_u32 s92,s92,s59\n"
".Lnf5:\n"
"v_readlane_b32 s58,v10,s67\n"
"s_cmp_lt_u32 s58,s72\n"
"s_cbranch_scc1 .Lsp5\n"
".Lnb5:\n"
MF "v[140:143],a[20:23],v[188:191],v[140:143]\n"
"s_waitcnt lgkmcnt(2)\n"
CV "v246,v248,v249\n"
CV "v247,v250,v251\n"
MF "v[96:99],a[140:143],v[176:179],v[96:99]\n"
"s_waitcnt lgkmcnt(0)\n"
TR "v[188:189],v5\n"
"v_add_f32 v80,v80,v192\n"
MF "v[96:99],a[180:183],v[180:183],v[96:99]\n"
TR "v[190:191],v5 offset:2048\n"
"v_add_f32 v81,v81,v193\n"
MF "v[96:99],a[220:223],v[184:187],v[96:99]\n"
"ds_read2st64_b32 v[200:201],v13 offset0:32 offset1:33\n"
"v_add_f32 v82,v82,v194\n"
MF "v[104:107],a[224:227],v[184:187],v[104:107]\n"
TR "a[240:241],v5 offset:4096\n"
"v_add_f32 v83,v83,v195\n"
MF "v[104:107],a[184:187],v[180:183],v[104:107]\n"
TR "a[242:243],v5 offset:6144\n"
"v_add_f32 v84,v84,v196\n"
MF "v[104:107],a[144:147],v[176:179],v[104:107]\n"
"ds_read2st64_b32 v[202:203],v13 offset0:34 offset1:35\n"
"v_add_f32 v85,v85,v197\n"
MF "v[112:115],a[148:151],v[176:179],v[112:115]\n"
TR "a[244:245],v5 offset:8192\n"
"v_add_f32 v86,v86,v198\n"
MF "v[112:115],a[188:191],v[180:183],v[112:115]\n"
TR "a[246:247],v5 offset:10240\n"
"v_add_f32 v87,v87,v199\n"
MF "v[112:115],a[228:231],v[184:187],v[112:115]\n"
"ds_read2st64_b32 v[204:205],v13 offset0:36 offset1:37\n"
CV "v80,v80,v81\n"
MF "v[120:123],a[232:235],v[184:187],v[120:123]\n"
"ds_read2st64_b32 v[206:207],v13 offset0:38 offset1:39\n"
CV "v81,v82,v83\n"
MF "v[120:123],a[192:195],v[180:183],v[120:123]\n"
"s_add_u32 s87,s90,13312\n"
CV "v82,v84,v85\n"
CV "v83,v86,v87\n"
MF "v[120:123],a[152:155],v[176:179],v[120:123]\n"
"s_cmp_ge_u32 s85,5\n"
"s_cselect_b32 s82,s70,s82\n"
"s_cselect_b32 s84,0xf0000010,s84\n"
MF "v[128:131],a[156:159],v[176:179],v[128:131]\n"
"buffer_store_dwordx4 v[80:83],v9,s[40:43],s87 offen sc1\n"
MF "v[128:131],a[196:199],v[180:183],v[128:131]\n"
"s_cmp_ge_u32 s85,5\n"
"s_cselect_b32 s71,1024,s71\n"
"s_cselect_b32 s66,0x80000000,s66\n"
MF "v[128:131],a[236:239],v[184:187],v[128:131]\n"
"s_add_u32 s70,s70,20480\n"
"s_waitcnt lgkmcnt(2)\n"
"v_permlane16_swap_b32 v244,v246\n"
MF "v[132:135],a[236:239],a[244:247],v[132:135]\n"
"s_waitcnt lgkmcnt(0)\n"
TR "v[176:177],v6\n"
"v_add_f32 v88,v88,v200\n"
MF "v[132:135],a[196:199],a[240:243],v[132:135]\n"
TR "v[178:179],v6 offset:2048\n"
"v_add_f32 v89,v89,v201\n"
MF "v[132:135],a[156:159],v[188:191],v[132:135]\n"
"v_add_f32 v90,v90,v202\n"
"v_add_f32 v91,v91,v203\n"
MF "v[124:127],a[152:155],v[188:191],v[124:127]\n"
TR "v[180:181],v6 offset:4096\n"
"v_add_f32 v92,v92,v204\n"
MF "v[124:127],a[192:195],a[240:243],v[124:127]\n"
TR "v[182:183],v6 offset:6144\n"
"v_add_f32 v93,v93,v205\n"
MF "v[124:127],a[232:235],a[244:247],v[124:127]\n"
"v_add_f32 v94,v94,v206\n"
"v_add_f32 v95,v95,v207\n"
MF "v[116:119],a[228:231],a[244:247],v[116:119]\n"
TR "v[184:185],v6 offset:8192\n"
CV "v88,v88,v89\n"
MF "v[116:119],a[188:191],a[240:243],v[116:119]\n"
TR "v[186:187],v6 offset:10240\n"
CV "v89,v90,v91\n"
MF "v[116:119],a[148:151],v[188:191],v[116:119]\n"
"s_add_u32 s87,s90,14336\n"
CV "v90,v92,v93\n"
CV "v91,v94,v95\n"
MF "v[108:111],a[144:147],v[188:191],v[108:111]\n"
"v_permlane16_swap_b32 v245,v247\n"
MF "v[108:111],a[184:187],a[240:243],v[108:111]\n"
"buffer_store_dwordx4 v[244:247],v8,s[44:47],s83 offen sc1\n"
MF "v[108:111],a[224:227],a[244:247],v[108:111]\n"
"s_mov_b32 s83,s84\n"
MF "v[100:103],a[220:223],a[244:247],v[100:103]\n"
"buffer_store_dwordx4 v[88:91],v9,s[40:43],s87 offen sc1\n"
MF "v[100:103],a[180:183],a[240:243],v[100:103]\n"
"s_mov_b32 s90,s91\n"
"s_add_u32 s91,s91,20480\n"
MF "v[100:103],a[140:143],v[188:191],v[100:103]\n"
"s_waitcnt lgkmcnt(0)\n"
LD "v[208:211],v9,s[40:43],s82 offen sc1\n"
MF "v[136:139],a[140:143],v[176:179],v[136:139]\n"
TR "v[188:189],v7\n"
MF "v[136:139],a[180:183],v[180:183],v[136:139]\n"
TR "v[190:191],v7 offset:2048\n"
MF "v[136:139],a[220:223],v[184:187],v[136:139]\n"
MF "v[144:147],a[224:227],v[184:187],v[144:147]\n"
TR "a[240:241],v7 offset:4096\n"
MF "v[144:147],a[184:187],v[180:183],v[144:147]\n"
"s_add_u32 s82,s82,s71\n"
TR "a[242:243],v7 offset:6144\n"
MF "v[144:147],a[144:147],v[176:179],v[144:147]\n"
LD "v[212:215],v9,s[40:43],s82 offen sc1\n"
MF "v[152:155],a[148:151],v[176:179],v[152:155]\n"
TR "a[244:245],v7 offset:8192\n"
MF "v[152:155],a[188:191],v[180:183],v[152:155]\n"
TR "a[246:247],v7 offset:10240\n"
MF "v[152:155],a[228:231],v[184:187],v[152:155]\n"
MF "v[160:163],a[232:235],v[184:187],v[160:163]\n"
MF "v[160:163],a[192:195],v[180:183],v[160:163]\n"
MF "v[160:163],a[152:155],v[176:179],v[160:163]\n"
"s_add_u32 s82,s82,s71\n"
MF "v[168:171],a[156:159],v[176:179],v[168:171]\n"
LD "v[216:219],v9,s[40:43],s82 offen sc1\n"
MF "v[168:171],a[196:199],v[180:183],v[168:171]\n"
MF "v[168:171],a[236:239],v[184:187],v[168:171]\n"
"s_waitcnt lgkmcnt(0)\n"
"s_barrier\n"
MF "v[172:175],a[236:239],a[244:247],v[172:175]\n"
TR "v[176:177],v0 offset:24576\n"
MF "v[172:175],a[196:199],a[240:243],v[172:175]\n"
"s_add_u32 s85,s85,1\n"
TR "v[178:179],v0 offset:26624\n"
MF "v[172:175],a[156:159],v[188:191],v[172:175]\n"
MF "v[164:167],a[152:155],v[188:191],v[164:167]\n"
TR "v[180:181],v0 offset:28672\n"
MF "v[164:167],a[192:195],a[240:243],v[164:167]\n"
"s_add_u32 s82,s82,s71\n"
TR "v[182:183],v0 offset:30720\n"
MF "v[164:167],a[232:235],a[244:247],v[164:167]\n"
LD "v[220:223],v9,s[40:43],s82 offen sc1\n"
MF "v[156:159],a[228:231],a[244:247],v[156:159]\n"
TR "v[184:185],v0 offset:32768\n"
MF "v[156:159],a[188:191],a[240:243],v[156:159]\n"
TR "v[186:187],v0 offset:34816\n"
MF "v[156:159],a[148:151],v[188:191],v[156:159]\n"
MF "v[148:151],a[144:147],v[188:191],v[148:151]\n"
MF "v[148:151],a[184:187],a[240:243],v[148:151]\n"
MF "v[148:151],a[224:227],a[244:247],v[148:151]\n"
MF "v[140:143],a[220:223],a[244:247],v[140:143]\n"
MF "v[140:143],a[180:183],a[240:243],v[140:143]\n"
MF "v[140:143],a[140:143],v[188:191],v[140:143]\n"
"s_waitcnt lgkmcnt(0)\n"
MF "v[16:19],a[0:3],v[176:179],v[252:255]\n"
TR "v[188:189],v1 offset:24576\n"
MF "v[16:19],a[40:43],v[180:183],v[16:19]\n"
"s_add_u32 s82,s82,s71\n"
TR "v[190:191],v1 offset:26624\n"
MF "v[16:19],a[80:83],v[184:187],v[16:19]\n"
LD "v[224:227],v9,s[40:43],s82 offen sc1\n"
MF "v[24:27],a[84:87],v[184:187],v[252:255]\n"
TR "a[240:241],v1 offset:28672\n"
MF "v[24:27],a[44:47],v[180:183],v[24:27]\n"
TR "a[242:243],v1 offset:30720\n"
MF "v[24:27],a[4:7],v[176:179],v[24:27]\n"
MF "v[32:35],a[8:11],v[176:179],v[252:255]\n"
TR "a[244:245],v1 offset:32768\n"
MF "v[32:35],a[48:51],v[180:183],v[32:35]\n"
TR "a[246:247],v1 offset:34816\n"
MF "v[32:35],a[88:91],v[184:187],v[32:35]\n"
MF "v[40:43],a[92:95],v[184:187],v[252:255]\n"
MF "v[40:43],a[52:55],v[180:183],v[40:43]\n"
"s_mul_i32 s95,s92,3277\n"
"s_lshr_b32 s95,s95,16\n"
"s_mul_i32 s59,s95,20\n"
MF "v[40:43],a[12:15],v[176:179],v[40:43]\n"
"s_sub_u32 s59,s92,s59\n"
"s_cmp_ge_u32 s59,10\n"
"s_cselect_b32 s96,4294557760,0\n"
MF "v[48:51],a[16:19],v[176:179],v[252:255]\n"
"s_add_u32 s82,s82,s66\n"
"s_add_u32 s96,s96,1024\n"
"s_mul_i32 s59,s59,40960\n"
MF "v[48:51],a[56:59],v[180:183],v[48:51]\n"
LD "v[228:231],v9,s[40:43],s82 offen sc1\n"
MF "v[48:51],a[96:99],v[184:187],v[48:51]\n"
"s_add_u32 s96,s96,s59\n"
"s_lshl_b32 s59,s95,7\n"
"s_waitcnt lgkmcnt(0)\n"
MF "v[52:55],a[96:99],a[244:247],v[252:255]\n"
"s_add_u32 s96,s96,s59\n"
"s_add_u32 s95,s95,3\n"
TR "v[176:177],v2 offset:24576\n"
MF "v[52:55],a[56:59],a[240:243],v[52:55]\n"
"s_cmp_ge_u32 s92,40\n"
"s_cselect_b32 s95,99,s95\n"
TR "v[178:179],v2 offset:26624\n"
MF "v[52:55],a[16:19],v[188:191],v[52:55]\n"
"s_cmp_ge_u32 s85,5\n"
"s_cselect_b32 s95,99,s95\n"
"s_cselect_b32 s72,s85,0\n"
MF "v[44:47],a[12:15],v[188:191],v[252:255]\n"
TR "v[180:181],v2 offset:28672\n"
MF "v[44:47],a[52:55],a[240:243],v[44:47]\n"
TR "v[182:183],v2 offset:30720\n"
MF "v[44:47],a[92:95],a[244:247],v[44:47]\n"
MF "v[36:39],a[88:91],a[244:247],v[252:255]\n"
TR "v[184:185],v2 offset:32768\n"
MF "v[36:39],a[48:51],a[240:243],v[36:39]\n"
"s_add_u32 s82,s82,s71\n"
TR "v[186:187],v2 offset:34816\n"
MF "v[36:39],a[8:11],v[188:191],v[36:39]\n"
LD "v[232:235],v9,s[40:43],s82 offen sc1\n"
MF "v[28:31],a[4:7],v[188:191],v[252:255]\n"
MF "v[28:31],a[44:47],a[240:243],v[28:31]\n"
MF "v[28:31],a[84:87],a[244:247],v[28:31]\n"
MF "v[20:23],a[80:83],a[244:247],v[252:255]\n"
MF "v[20:23],a[40:43],a[240:243],v[20:23]\n"
MF "v[20:23],a[0:3],v[188:191],v[20:23]\n"
"s_waitcnt lgkmcnt(0)\n"
MF "v[56:59],a[0:3],v[176:179],v[252:255]\n"
TR "v[188:189],v3 offset:24576\n"
MF "v[56:59],a[40:43],v[180:183],v[56:59]\n"
"s_add_u32 s82,s82,s71\n"
TR "v[190:191],v3 offset:26624\n"
MF "v[56:59],a[80:83],v[184:187],v[56:59]\n"
LD "v[236:239],v9,s[40:43],s82 offen sc1\n"
MF "v[64:67],a[84:87],v[184:187],v[252:255]\n"
TR "a[240:241],v3 offset:28672\n"
MF "v[64:67],a[44:47],v[180:183],v[64:67]\n"
TR "a[242:243],v3 offset:30720\n"
MF "v[64:67],a[4:7],v[176:179],v[64:67]\n"
MF "v[72:75],a[8:11],v[176:179],v[252:255]\n"
TR "a[244:245],v3 offset:32768\n"
MF "v[72:75],a[48:51],v[180:183],v[72:75]\n"
TR "a[246:247],v3 offset:34816\n"
MF "v[72:75],a[88:91],v[184:187],v[72:75]\n"
MF "v[80:83],a[92:95],v[184:187],v[252:255]\n"
MF "v[80:83],a[52:55],v[180:183],v[80:83]\n"
MF "v[80:83],a[12:15],v[176:179],v[80:83]\n"
MF "v[88:91],a[16:19],v[176:179],v[252:255]\n"
MF "v[88:91],a[56:59],v[180:183],v[88:91]\n"
MF "v[88:91],a[96:99],v[184:187],v[88:91]\n"
"s_waitcnt lgkmcnt(0)\n"
"s_barrier\n"
MF "v[92:95],a[96:99],a[244:247],v[252:255]\n"
TR "v[176:177],v0\n"
MF "v[92:95],a[56:59],a[240:243],v[92:95]\n"
TR "v[178:179],v0 offset:2048\n"
MF "v[92:95],a[16:19],v[188:191],v[92:95]\n"
"ds_read2st64_b32 v[192:193],v13 offset0:192 offset1:193\n"
MF "v[84:87],a[12:15],v[188:191],v[252:255]\n"
TR "v[180:181],v0 offset:4096\n"
MF "v[84:87],a[52:55],a[240:243],v[84:87]\n"
TR "v[182:183],v0 offset:6144\n"
MF "v[84:87],a[92:95],a[244:247],v[84:87]\n"
"ds_read2st64_b32 v[194:195],v13 offset0:194 offset1:195\n"
MF "v[76:79],a[88:91],a[244:247],v[252:255]\n"
TR "v[184:185],v0 offset:8192\n"
MF "v[76:79],a[48:51],a[240:243],v[76:79]\n"
TR "v[186:187],v0 offset:10240\n"
MF "v[76:79],a[8:11],v[188:191],v[76:79]\n"
"ds_read2st64_b32 v[196:197],v13 offset0:196 offset1:197\n"
MF "v[68:71],a[4:7],v[188:191],v[252:255]\n"
"ds_read2st64_b32 v[198:199],v13 offset0:198 offset1:199\n"
MF "v[68:71],a[44:47],a[240:243],v[68:71]\n"
"buffer_load_dword v10,v12,s[40:43],s86 offen sc1\n"
MF "v[68:71],a[84:87],a[244:247],v[68:71]\n"
MF "v[60:63],a[80:83],a[244:247],v[252:255]\n"
MF "v[60:63],a[40:43],a[240:243],v[60:63]\n"
MF "v[60:63],a[0:3],v[188:191],v[60:63]\n"
"s_waitcnt lgkmcnt(2)\n"
MF "v[16:19],a[120:123],v[176:179],v[16:19]\n"
"s_waitcnt lgkmcnt(0)\n"
TR "v[188:189],v1\n"
"v_add_f32 v96,v96,v192\n"
MF "v[16:19],a[160:163],v[180:183],v[16:19]\n"
TR "v[190:191],v1 offset:2048\n"
"v_add_f32 v97,v97,v193\n"
MF "v[16:19],a[200:203],v[184:187],v[16:19]\n"
"ds_read2st64_b32 v[200:201],v13 offset0:200 offset1:201\n"
"v_add_f32 v98,v98,v194\n"
"s_waitcnt vmcnt(9)\n"
"v_mov_b32 v14,s76\n"
"s_add_u32 s76,s76,1\n"
MF "v[24:27],a[204:207],v[184:187],v[24:27]\n"
TR "a[240:241],v1 offset:4096\n"
"v_add_f32 v99,v99,v195\n"
"buffer_store_dword v14,v15,s[40:43],s94 offen sc1\n"
MF "v[24:27],a[164:167],v[180:183],v[24:27]\n"
TR "a[242:243],v1 offset:6144\n"
"v_add_f32 v100,v100,v196\n"
MF "v[24:27],a[124:127],v[176:179],v[24:27]\n"
"ds_read2st64_b32 v[202:203],v13 offset0:202 offset1:203\n"
"v_add_f32 v101,v101,v197\n"
MF "v[32:35],a[128:131],v[176:179],v[32:35]\n"
TR "a[244:245],v1 offset:8192\n"
"v_add_f32 v102,v102,v198\n"
MF "v[32:35],a[168:171],v[180:183],v[32:35]\n"
TR "a[246:247],v1 offset:10240\n"
"v_add_f32 v103,v103,v199\n"
MF "v[32:35],a[208:211],v[184:187],v[32:35]\n"
"ds_read2st64_b32 v[204:205],v13 offset0:204 offset1:205\n"
CV "v96,v96,v97\n"
MF "v[40:43],a[212:215],v[184:187],v[40:43]\n"
"ds_read2st64_b32 v[206:207],v13 offset0:206 offset1:207\n"
CV "v97,v98,v99\n"
MF "v[40:43],a[172:175],v[180:183],v[40:43]\n"
"s_add_u32 s87,s90,0\n"
CV "v98,v100,v101\n"
CV "v99,v102,v103\n"
MF "v[40:43],a[132:135],v[176:179],v[40:43]\n"
MF "v[48:51],a[136:139],v[176:179],v[48:51]\n"
"buffer_store_dwordx4 v[96:99],v9,s[40:43],s87 offen sc1\n"
MF "v[48:51],a[176:179],v[180:183],v[48:51]\n"
MF "v[48:51],a[216:219],v[184:187],v[48:51]\n"
"s_waitcnt lgkmcnt(2)\n"
MF "v[52:55],a[216:219],a[244:247],v[52:55]\n"
"s_waitcnt lgkmcnt(0)\n"
TR "v[176:177],v2\n"
"v_add_f32 v104,v104,v200\n"
MF "v[52:55],a[176:179],a[240:243],v[52:55]\n"
TR "v[178:179],v2 offset:2048\n"
"v_add_f32 v105,v105,v201\n"
MF "v[52:55],a[136:139],v[188:191],v[52:55]\n"
"ds_read2st64_b32 v[192:193],v13 offset0:208 offset1:209\n"
"v_add_f32 v106,v106,v202\n"
MF "v[44:47],a[132:135],v[188:191],v[44:47]\n"
TR "v[180:181],v2 offset:4096\n"
"v_add_f32 v107,v107,v203\n"
MF "v[44:47],a[172:175],a[240:243],v[44:47]\n"
TR "v[182:183],v2 offset:6144\n"
"v_add_f32 v108,v108,v204\n"
MF "v[44:47],a[212:215],a[244:247],v[44:47]\n"
"ds_read2st64_b32 v[194:195],v13 offset0:210 offset1:211\n"
"v_add_f32 v109,v109,v205\n"
MF "v[36:39],a[208:211],a[244:247],v[36:39]\n"
TR "v[184:185],v2 offset:8192\n"
"v_add_f32 v110,v110,v206\n"
MF "v[36:39],a[168:171],a[240:243],v[36:39]\n"
TR "v[186:187],v2 offset:10240\n"
"v_add_f32 v111,v111,v207\n"
MF "v[36:39],a[128:131],v[188:191],v[36:39]\n"
"ds_read2st64_b32 v[196:197],v13 offset0:212 offset1:213\n"
CV "v104,v104,v105\n"
MF "v[28:31],a[124:127],v[188:191],v[28:31]\n"
"ds_read2st64_b32 v[198:199],v13 offset0:214 offset1:215\n"
CV "v105,v106,v107\n"
MF "v[28:31],a[164:167],a[240:243],v[28:31]\n"
"s_add_u32 s87,s90,1024\n"
CV "v106,v108,v109\n"
CV "v107,v110,v111\n"
MF "v[28:31],a[204:207],a[244:247],v[28:31]\n"
MF "v[20:23],a[200:203],a[244:247],v[20:23]\n"
"buffer_store_dwordx4 v[104:107],v9,s[40:43],s87 offen sc1\n"
MF "v[20:23],a[160:163],a[240:243],v[20:23]\n"
MF "v[20:23],a[120:123],v[188:191],v[20:23]\n"
"s_waitcnt lgkmcnt(2)\n"
MF "v[56:59],a[120:123],v[176:179],v[56:59]\n"
"s_waitcnt lgkmcnt(0)\n"
TR "v[188:189],v3\n"
"v_add_f32 v112,v112,v192\n"
MF "v[56:59],a[160:163],v[180:183],v[56:59]\n"
TR "v[190:191],v3 offset:2048\n"
"v_add_f32 v113,v113,v193\n"
MF "v[56:59],a[200:203],v[184:187],v[56:59]\n"
"ds_read2st64_b32 v[200:201],v13 offset0:216 offset1:217\n"
"v_add_f32 v114,v114,v194\n"
MF "v[64:67],a[204:207],v[184:187],v[64:67]\n"
TR "a[240:241],v3 offset:4096\n"
"v_add_f32 v115,v115,v195\n"
MF "v[64:67],a[164:167],v[180:183],v[64:67]\n"
TR "a[242:243],v3 offset:6144\n"
"v_add_f32 v116,v116,v196\n"
MF "v[64:67],a[124:127],v[176:179],v[64:67]\n"
"ds_read2st64_b32 v[202:203],v13 offset0:218 offset1:219\n"
"v_add_f32 v117,v117,v197\n"
MF "v[72:75],a[128:131],v[176:179],v[72:75]\n"
TR "a[244:245],v3 offset:8192\n"
"v_add_f32 v118,v118,v198\n"
MF "v[72:75],a[168:171],v[180:183],v[72:75]\n"
TR "a[246:247],v3 offset:10240\n"
"v_add_f32 v119,v119,v199\n"
MF "v[72:75],a[208:211],v[184:187],v[72:75]\n"
"ds_read2st64_b32 v[204:205],v13 offset0:220 offset1:221\n"
CV "v112,v112,v113\n"
MF "v[80:83],a[212:215],v[184:187],v[80:83]\n"
"ds_read2st64_b32 v[206:207],v13 offset0:222 offset1:223\n"
CV "v113,v114,v115\n"
MF "v[80:83],a[172:175],v[180:183],v[80:83]\n"
"s_add_u32 s87,s90,2048\n"
CV "v114,v116,v117\n"
CV "v115,v118,v119\n"
MF "v[80:83],a[132:135],v[176:179],v[80:83]\n"
MF "v[88:91],a[136:139],v[176:179],v[88:91]\n"
"buffer_store_dwordx4 v[112:115],v9,s[40:43],s87 offen sc1\n"
MF "v[88:91],a[176:179],v[180:183],v[88:91]\n"
MF "v[88:91],a[216:219],v[184:187],v[88:91]\n"
"s_waitcnt lgkmcnt(0)\n"
"s_barrier\n"
MF "v[92:95],a[216:219],a[244:247],v[92:95]\n"
TR "v[176:177],v0 offset:49152\n"
"v_add_f32 v120,v120,v200\n"
MF "v[92:95],a[176:179],a[240:243],v[92:95]\n"
TR "v[178:179],v0 offset:51200\n"
"v_add_f32 v121,v121,v201\n"
MF "v[92:95],a[136:139],v[188:191],v[92:95]\n"
"ds_read2st64_b32 v[192:193],v13 offset0:224 offset1:225\n"
"v_add_f32 v122,v122,v202\n"
MF "v[84:87],a[132:135],v[188:191],v[84:87]\n"
TR "v[180:181],v0 offset:53248\n"
"v_add_f32 v123,v123,v203\n"
MF "v[84:87],a[172:175],a[240:243],v[84:87]\n"
TR "v[182:183],v0 offset:55296\n"
"v_add_f32 v124,v124,v204\n"
MF "v[84:87],a[212:215],a[244:247],v[84:87]\n"
"ds_read2st64_b32 v[194:195],v13 offset0:226 offset1:227\n"
"v_add_f32 v125,v125,v205\n"
MF "v[76:79],a[208:211],a[244:247],v[76:79]\n"
TR "v[184:185],v0 offset:57344\n"
"v_add_f32 v126,v126,v206\n"
MF "v[76:79],a[168:171],a[240:243],v[76:79]\n"
TR "v[186:187],v0 offset:59392\n"
"v_add_f32 v127,v127,v207\n"
MF "v[76:79],a[128:131],v[188:191],v[76:79]\n"
"ds_read2st64_b32 v[196:197],v13 offset0:228 offset1:229\n"
CV "v120,v120,v121\n"
MF "v[68:71],a[124:127],v[188:191],v[68:71]\n"
"ds_read2st64_b32 v[198:199],v13 offset0:230 offset1:231\n"
CV "v121,v122,v123\n"
MF "v[68:71],a[164:167],a[240:243],v[68:71]\n"
"s_add_u32 s87,s90,3072\n"
CV "v122,v124,v125\n"
CV "v123,v126,v127\n"
MF "v[68:71],a[204:207],a[244:247],v[68:71]\n"
MF "v[60:63],a[200:203],a[244:247],v[60:63]\n"
"buffer_store_dwordx4 v[120:123],v9,s[40:43],s87 offen sc1\n"
MF "v[60:63],a[160:163],a[240:243],v[60:63]\n"
MF "v[60:63],a[120:123],v[188:191],v[60:63]\n"
"s_waitcnt lgkmcnt(2)\n"
MF "v[16:19],a[20:23],v[176:179],v[16:19]\n"
"s_waitcnt lgkmcnt(0)\n"
TR "v[188:189],v1 offset:49152\n"
"v_add_f32 v128,v128,v192\n"
MF "v[16:19],a[60:63],v[180:183],v[16:19]\n"
TR "v[190:191],v1 offset:51200\n"
"v_add_f32 v129,v129,v193\n"
MF "v[16:19],a[100:103],v[184:187],v[16:19]\n"
"ds_read2st64_b32 v[200:201],v13 offset0:0 offset1:1\n"
"v_add_f32 v130,v130,v194\n"
MF "v[24:27],a[104:107],v[184:187],v[24:27]\n"
TR "a[240:241],v1 offset:53248\n"
"v_add_f32 v131,v131,v195\n"
MF "v[24:27],a[64:67],v[180:183],v[24:27]\n"
TR "a[242:243],v1 offset:55296\n"
"v_add_f32 v132,v132,v196\n"
MF "v[24:27],a[24:27],v[176:179],v[24:27]\n"
"ds_read2st64_b32 v[202:203],v13 offset0:2 offset1:3\n"
"v_add_f32 v133,v133,v197\n"
MF "v[32:35],a[28:31],v[176:179],v[32:35]\n"
TR "a[244:245],v1 offset:57344\n"
"v_add_f32 v134,v134,v198\n"
MF "v[32:35],a[68:71],v[180:183],v[32:35]\n"
TR "a[246:247],v1 offset:59392\n"
"v_add_f32 v135,v135,v199\n"
MF "v[32:35],a[108:111],v[184:187],v[32:35]\n"
"ds_read2st64_b32 v[204:205],v13 offset0:4 offset1:5\n"
CV "v128,v128,v129\n"
MF "v[40:43],a[112:115],v[184:187],v[40:43]\n"
"ds_read2st64_b32 v[206:207],v13 offset0:6 offset1:7\n"
CV "v129,v130,v131\n"
MF "v[40:43],a[72:75],v[180:183],v[40:43]\n"
"s_add_u32 s87,s90,4096\n"
CV "v130,v132,v133\n"
CV "v131,v134,v135\n"
MF "v[40:43],a[32:35],v[176:179],v[40:43]\n"
MF "v[48:51],a[36:39],v[176:179],v[48:51]\n"
"buffer_store_dwordx4 v[128:131],v9,s[40:43],s87 offen sc1\n"
MF "v[48:51],a[76:79],v[180:183],v[48:51]\n"
MF "v[48:51],a[116:119],v[184:187],v[48:51]\n"
"s_waitcnt lgkmcnt(2)\n"
MF "v[52:55],a[116:119],a[244:247],v[52:55]\n"
"s_waitcnt lgkmcnt(0)\n"
TR "v[176:177],v2 offset:49152\n"
"v_add_f32 v136,v136,v200\n"
MF "v[52:55],a[76:79],a[240:243],v[52:55]\n"
TR "v[178:179],v2 offset:51200\n"
"v_add_f32 v137,v137,v201\n"
MF "v[52:55],a[36:39],v[188:191],v[52:55]\n"
"ds_read2st64_b32 v[192:193],v13 offset0:8 offset1:9\n"
"v_add_f32 v138,v138,v202\n"
MF "v[44:47],a[32:35],v[188:191],v[44:47]\n"
TR "v[180:181],v2 offset:53248\n"
"v_add_f32 v139,v139,v203\n"
MF "v[44:47],a[72:75],a[240:243],v[44:47]\n"
TR "v[182:183],v2 offset:55296\n"
"v_add_f32 v140,v140,v204\n"
MF "v[44:47],a[112:115],a[244:247],v[44:47]\n"
"ds_read2st64_b32 v[194:195],v13 offset0:10 offset1:11\n"
"v_add_f32 v141,v141,v205\n"
MF "v[36:39],a[108:111],a[244:247],v[36:39]\n"
TR "v[184:185],v2 offset:57344\n"
"v_add_f32 v142,v142,v206\n"
MF "v[36:39],a[68:71],a[240:243],v[36:39]\n"
TR "v[186:187],v2 offset:59392\n"
"v_add_f32 v143,v143,v207\n"
MF "v[36:39],a[28:31],v[188:191],v[36:39]\n"
"ds_read2st64_b32 v[196:197],v13 offset0:12 offset1:13\n"
CV "v136,v136,v137\n"
MF "v[28:31],a[24:27],v[188:191],v[28:31]\n"
"ds_read2st64_b32 v[198:199],v13 offset0:14 offset1:15\n"
CV "v137,v138,v139\n"
MF "v[28:31],a[64:67],a[240:243],v[28:31]\n"
"s_add_u32 s87,s90,10240\n"
CV "v138,v140,v141\n"
CV "v139,v142,v143\n"
MF "v[28:31],a[104:107],a[244:247],v[28:31]\n"
MF "v[20:23],a[100:103],a[244:247],v[20:23]\n"
"buffer_store_dwordx4 v[136:139],v9,s[40:43],s87 offen sc1\n"
MF "v[20:23],a[60:63],a[240:243],v[20:23]\n"
"s_waitcnt vmcnt(7)\n"
"s_cmp_eq_u32 s83,0xf0000000\n"
MF "v[20:23],a[20:23],v[188:191],v[20:23]\n"
"s_cbranch_scc1 .Ljs15\n"
"s_cmp_eq_u32 s83,0xf0000010\n"
"s_cbranch_scc1 .Ljc15\n"
MF "v[248:251],v[210:213],a[250:253],0\n"
"v_mov_b32 v240,v208\n"
"v_mov_b32 v241,v209\n"
MF "v[244:247],v[214:217],a[250:253],0\n"
MF "v[248:251],v[218:221],a[250:253],v[248:251]\n"
MF "v[244:247],v[222:225],a[250:253],v[244:247]\n"
MF "v[248:251],v[226:229],a[250:253],v[248:251]\n"
MF "v[244:247],v[230:233],a[250:253],v[244:247]\n"
MF "v[248:251],v[234:237],a[250:253],v[248:251]\n"
MF "v[244:247],v[238:241],a[250:253],v[244:247]\n"
"s_branch .Ljs15\n"
".Ljc15:\n"
MF "v[56:59],a[252:255],v[208:211],v[56:59]\n"
MF "v[64:67],a[252:255],v[212:215],v[64:67]\n"
MF "v[72:75],a[252:255],v[216:219],v[72:75]\n"
MF "v[80:83],a[252:255],v[220:223],v[80:83]\n"
MF "v[88:91],a[252:255],v[224:227],v[88:91]\n"
MF "v[60:63],a[248:251],v[208:211],v[60:63]\n"
MF "v[68:71],a[248:251],v[212:215],v[68:71]\n"
MF "v[76:79],a[248:251],v[216:219],v[76:79]\n"
MF "v[84:87],a[248:251],v[220:223],v[84:87]\n"
MF "v[92:95],a[248:251],v[224:227],v[92:95]\n"
".Ljs15:\n"
"s_waitcnt lgkmcnt(2)\n"
MF "v[56:59],a[20:23],v[176:179],v[56:59]\n"
"s_waitcnt lgkmcnt(0)\n"
TR "v[188:189],v3 offset:49152\n"
"v_add_f32 v144,v144,v192\n"
MF "v[56:59],a[60:63],v[180:183],v[56:59]\n"
TR "v[190:191],v3 offset:51200\n"
"v_add_f32 v145,v145,v193\n"
MF "v[56:59],a[100:103],v[184:187],v[56:59]\n"
"ds_read2st64_b32 v[200:201],v13 offset0:16 offset1:17\n"
"v_add_f32 v146,v146,v194\n"
MF "v[64:67],a[104:107],v[184:187],v[64:67]\n"
TR "a[240:241],v3 offset:53248\n"
"v_add_f32 v147,v147,v195\n"
MF "v[64:67],a[64:67],v[180:183],v[64:67]\n"
TR "a[242:243],v3 offset:55296\n"
"v_add_f32 v148,v148,v196\n"
MF "v[64:67],a[24:27],v[176:179],v[64:67]\n"
"ds_read2st64_b32 v[202:203],v13 offset0:18 offset1:19\n"
"v_add_f32 v149,v149,v197\n"
MF "v[72:75],a[28:31],v[176:179],v[72:75]\n"
TR "a[244:245],v3 offset:57344\n"
"v_add_f32 v150,v150,v198\n"
MF "v[72:75],a[68:71],v[180:183],v[72:75]\n"
TR "a[246:247],v3 offset:59392\n"
"v_add_f32 v151,v151,v199\n"
MF "v[72:75],a[108:111],v[184:187],v[72:75]\n"
"ds_read2st64_b32 v[204:205],v13 offset0:20 offset1:21\n"
CV "v144,v144,v145\n"
MF "v[80:83],a[112:115],v[184:187],v[80:83]\n"
"ds_read2st64_b32 v[206:207],v13 offset0:22 offset1:23\n"
CV "v145,v146,v147\n"
MF "v[80:83],a[72:75],v[180:183],v[80:83]\n"
"s_add_u32 s87,s90,11264\n"
CV "v146,v148,v149\n"
CV "v147,v150,v151\n"
MF "v[80:83],a[32:35],v[176:179],v[80:83]\n"
"s_lshl_b32 s82,s92,10\n"
"v_sub_u32 v10,v10,v11\n"
"v_cmp_gt_u32 vcc,s95,v10\n"
MF "v[88:91],a[36:39],v[176:179],v[88:91]\n"
"buffer_store_dwordx4 v[144:147],v9,s[40:43],s87 offen sc1\n"
MF "v[88:91],a[76:79],v[180:183],v[88:91]\n"
"s_add_u32 s82,s82,s93\n"
CV "v244,v244,v245\n"
CV "v245,v246,v247\n"
MF "v[88:91],a[116:119],v[184:187],v[88:91]\n"
"s_waitcnt lgkmcnt(0)\n"
"s_barrier\n"
MF "v[92:95],a[116:119],a[244:247],v[92:95]\n"
TR "v[176:177],v4\n"
"v_add_f32 v152,v152,v200\n"
MF "v[92:95],a[76:79],a[240:243],v[92:95]\n"
TR "v[178:179],v4 offset:2048\n"
"v_add_f32 v153,v153,v201\n"
MF "v[92:95],a[36:39],v[188:191],v[92:95]\n"
"ds_read2st64_b32 v[192:193],v13 offset0:24 offset1:25\n"
"v_add_f32 v154,v154,v202\n"
MF "v[84:87],a[32:35],v[188:191],v[84:87]\n"
TR "v[180:181],v4 offset:4096\n"
"v_add_f32 v155,v155,v203\n"
MF "v[84:87],a[72:75],a[240:243],v[84:87]\n"
TR "v[182:183],v4 offset:6144\n"
"v_add_f32 v156,v156,v204\n"
MF "v[84:87],a[112:115],a[244:247],v[84:87]\n"
"ds_read2st64_b32 v[194:195],v13 offset0:26 offset1:27\n"
"v_add_f32 v157,v157,v205\n"
MF "v[76:79],a[108:111],a[244:247],v[76:79]\n"
TR "v[184:185],v4 offset:8192\n"
"v_add_f32 v158,v158,v206\n"
MF "v[76:79],a[68:71],a[240:243],v[76:79]\n"
TR "v[186:187],v4 offset:10240\n"
"v_add_f32 v159,v159,v207\n"
MF "v[76:79],a[28:31],v[188:191],v[76:79]\n"
"ds_read2st64_b32 v[196:197],v13 offset0:28 offset1:29\n"
CV "v152,v152,v153\n"
MF "v[68:71],a[24:27],v[188:191],v[68:71]\n"
"ds_read2st64_b32 v[198:199],v13 offset0:30 offset1:31\n"
CV "v153,v154,v155\n"
MF "v[68:71],a[64:67],a[240:243],v[68:71]\n"
"s_add_u32 s87,s90,12288\n"
CV "v154,v156,v157\n"
CV "v155,v158,v159\n"
MF "v[68:71],a[104:107],a[244:247],v[68:71]\n"
"s_cmp_eq_u64 vcc,0\n"
"s_cselect_b32 s82,s82,0xf0000000\n"
"s_cselect_b32 s84,s96,0xf0000000\n"
"s_cselect_b32 s59,32,0\n"
MF "v[60:63],a[100:103],a[244:247],v[60:63]\n"
"buffer_store_dwordx4 v[152:155],v9,s[40:43],s87 offen sc1\n"
MF "v[60:63],a[60:63],a[240:243],v[60:63]\n"
"s_add_u32 s92,s92,s59\n"
".Lnf6:\n"
"v_readlane_b32 s58,v10,s67\n"
"s_cmp_lt_u32 s58,s72\n"
"s_cbranch_scc1 .Lsp6\n"
".Lnb6:\n"
MF "v[60:63],a[20:23],v[188:191],v[60:63]\n"
"s_waitcnt lgkmcnt(2)\n"
CV "v246,v248,v249\n"
CV "v247,v250,v251\n"
MF "v[16:19],a[140:143],v[176:179],v[16:19]\n"
"s_waitcnt lgkmcnt(0)\n"
TR "v[188:189],v5\n"
"v_add_f32 v160,v160,v192\n"
MF "v[16:19],a[180:183],v[180:183],v[16:19]\n"
TR "v[190:191],v5 offset:2048\n"
"v_add_f32 v161,v161,v193\n"
MF "v[16:19],a[220:223],v[184:187],v[16:19]\n"
"ds_read2st64_b32 v[200:201],v13 offset0:32 offset1:33\n"
"v_add_f32 v162,v162,v194\n"
MF "v[24:27],a[224:227],v[184:187],v[24:27]\n"
TR "a[240:241],v5 offset:4096\n"
"v_add_f32 v163,v163,v195\n"
MF "v[24:27],a[184:187],v[180:183],v[24:27]\n"
TR "a[242:243],v5 offset:6144\n"
"v_add_f32 v164,v164,v196\n"
MF "v[24:27],a[144:147],v[176:179],v[24:27]\n"
"ds_read2st64_b32 v[202:203],v13 offset0:34 offset1:35\n"
"v_add_f32 v165,v165,v197\n"
MF "v[32:35],a[148:151],v[176:179],v[32:35]\n"
TR "a[244:245],v5 offset:8192\n"
"v_add_f32 v166,v166,v198\n"
MF "v[32:35],a[188:191],v[180:183],v[32:35]\n"
TR "a[246:247],v5 offset:10240\n"
"v_add_f32 v167,v167,v199\n"
MF "v[32:35],a[228:231],v[184:187],v[32:35]\n"
"ds_read2st64_b32 v[204:205],v13 offset0:36 offset1:37\n"
CV "v160,v160,v161\n"
MF "v[40:43],a[232:235],v[184:187],v[40:43]\n"
"ds_read2st64_b32 v[206:207],v13 offset0:38 offset1:39\n"
CV "v161,v162,v163\n"
MF "v[40:43],a[192:195],v[180:183],v[40:43]\n"
"s_add_u32 s87,s90,13312\n"
CV "v162,v164,v165\n"
CV "v163,v166,v167\n"
MF "v[40:43],a[152:155],v[176:179],v[40:43]\n"
"s_cmp_ge_u32 s85,5\n"
"s_cselect_b32 s82,s70,s82\n"
"s_cselect_b32 s84,0xf0000010,s84\n"
MF "v[48:51],a[156:159],v[176:179],v[48:51]\n"
"buffer_store_dwordx4 v[160:163],v9,s[40:43],s87 offen sc1\n"
MF "v[48:51],a[196:199],v[180:183],v[48:51]\n"
"s_cmp_ge_u32 s85,5\n"
"s_cselect_b32 s71,1024,s71\n"
"s_cselect_b32 s66,0x80000000,s66\n"
MF "v[48:51],a[236:239],v[184:187],v[48:51]\n"
"s_add_u32 s70,s70,20480\n"
"s_waitcnt lgkmcnt(2)\n"
"v_permlane16_swap_b32 v244,v246\n"
MF "v[52:55],a[236:239],a[244:247],v[52:55]\n"
"s_waitcnt lgkmcnt(0)\n"
TR "v[176:177],v6\n"
"v_add_f32 v168,v168,v200\n"
MF "v[52:55],a[196:199],a[240:243],v[52:55]\n"
TR "v[178:179],v6 offset:2048\n"
"v_add_f32 v169,v169,v201\n"
MF "v[52:55],a[156:159],v[188:191],v[52:55]\n"
"v_add_f32 v170,v170,v202\n"
"v_add_f32 v171,v171,v203\n"
MF "v[44:47],a[152:155],v[188:191],v[44:47]\n"
TR "v[180:181],v6 offset:4096\n"
"v_add_f32 v172,v172,v204\n"
MF "v[44:47],a[192:195],a[240:243],v[44:47]\n"
TR "v[182:183],v6 offset:6144\n"
"v_add_f32 v173,v173,v205\n"
MF "v[44:47],a[232:235],a[244:247],v[44:47]\n"
"v_add_f32 v174,v174,v206\n"
"v_add_f32 v175,v175,v207\n"
MF "v[36:39],a[228:231],a[244:247],v[36:39]\n"
TR "v[184:185],v6 offset:8192\n"
CV "v168,v168,v169\n"
MF "v[36:39],a[188:191],a[240:243],v[36:39]\n"
TR "v[186:187],v6 offset:10240\n"
CV "v169,v170,v171\n"
MF "v[36:39],a[148:151],v[188:191],v[36:39]\n"
"s_add_u32 s87,s90,14336\n"
CV "v170,v172,v173\n"
CV "v171,v174,v175\n"
MF "v[28:31],a[144:147],v[188:191],v[28:31]\n"
"v_permlane16_swap_b32 v245,v247\n"
MF "v[28:31],a[184:187],a[240:243],v[28:31]\n"
"buffer_store_dwordx4 v[244:247],v8,s[44:47],s83 offen sc1\n"
MF "v[28:31],a[224:227],a[244:247],v[28:31]\n"
"s_mov_b32 s83,s84\n"
MF "v[20:23],a[220:223],a[244:247],v[20:23]\n"
"buffer_store_dwordx4 v[168:171],v9,s[40:43],s87 offen sc1\n"
MF "v[20:23],a[180:183],a[240:243],v[20:23]\n"
"s_mov_b32 s90,s91\n"
"s_add_u32 s91,s91,20480\n"
MF "v[20:23],a[140:143],v[188:191],v[20:23]\n"
"s_waitcnt lgkmcnt(0)\n"
LD "v[208:211],v9,s[40:43],s82 offen sc1\n"
MF "v[56:59],a[140:143],v[176:179],v[56:59]\n"
TR "v[188:189],v7\n"
MF "v[56:59],a[180:183],v[180:183],v[56:59]\n"
TR "v[190:191],v7 offset:2048\n"
MF "v[56:59],a[220:223],v[184:187],v[56:59]\n"
MF "v[64:67],a[224:227],v[184:187],v[64:67]\n"
TR "a[240:241],v7 offset:4096\n"
MF "v[64:67],a[184:187],v[180:183],v[64:67]\n"
"s_add_u32 s82,s82,s71\n"
TR "a[242:243],v7 offset:6144\n"
MF "v[64:67],a[144:147],v[176:179],v[64:67]\n"
LD "v[212:215],v9,s[40:43],s82 offen sc1\n"
MF "v[72:75],a[148:151],v[176:179],v[72:75]\n"
TR "a[244:245],v7 offset:8192\n"
MF "v[72:75],a[188:191],v[180:183],v[72:75]\n"
TR "a[246:247],v7 offset:10240\n"
MF "v[72:75],a[228:231],v[184:187],v[72:75]\n"
MF "v[80:83],a[232:235],v[184:187],v[80:83]\n"
MF "v[80:83],a[192:195],v[180:183],v[80:83]\n"
MF "v[80:83],a[152:155],v[176:179],v[80:83]\n"
"s_add_u32 s82,s82,s71\n"
MF "v[88:91],a[156:159],v[176:179],v[88:91]\n"
LD "v[216:219],v9,s[40:43],s82 offen sc1\n"
MF "v[88:91],a[196:199],v[180:183],v[88:91]\n"
MF "v[88:91],a[236:239],v[184:187],v[88:91]\n"
"s_waitcnt lgkmcnt(0)\n"
"s_barrier\n"
MF "v[92:95],a[236:239],a[244:247],v[92:95]\n"
TR "v[176:177],v0 offset:24576\n"
MF "v[92:95],a[196:199],a[240:243],v[92:95]\n"
"s_add_u32 s85,s85,1\n"
TR "v[178:179],v0 offset:26624\n"
MF "v[92:95],a[156:159],v[188:191],v[92:95]\n"
MF "v[84:87],a[152:155],v[188:191],v[84:87]\n"
TR "v[180:181],v0 offset:28672\n"
MF "v[84:87],a[192:195],a[240:243],v[84:87]\n"
"s_add_u32 s82,s82,s71\n"
TR "v[182:183],v0 offset:30720\n"
MF "v[84:87],a[232:235],a[244:247],v[84:87]\n"
LD "v[220:223],v9,s[40:43],s82 offen sc1\n"
MF "v[76:79],a[228:231],a[244:247],v[76:79]\n"
TR "v[184:185],v0 offset:32768\n"
MF "v[76:79],a[188:191],a[240:243],v[76:79]\n"
TR "v[186:187],v0 offset:34816\n"
MF "v[76:79],a[148:151],v[188:191],v[76:79]\n"
MF "v[68:71],a[144:147],v[188:191],v[68:71]\n"
MF "v[68:71],a[184:187],a[240:243],v[68:71]\n"
MF "v[68:71],a[224:227],a[244:247],v[68:71]\n"
MF "v[60:63],a[220:223],a[244:247],v[60:63]\n"
MF "v[60:63],a[180:183],a[240:243],v[60:63]\n"
MF "v[60:63],a[140:143],v[188:191],v[60:63]\n"
"s_waitcnt lgkmcnt(0)\n"
"s_cmp_lg_u32 s85,9\n"
"s_cbranch_scc1 .Lpl1\n"
".Lplast1:\n"
MF "v[96:99],v[176:179],a[0:3],v[252:255]\n"
"s_add_u32 m0,s79,73728\n"
TR "v[188:189],v1 offset:24576\n"
TR "v[190:191],v1 offset:26624\n"
MF "v[96:99],v[180:183],a[40:43],v[96:99]\n"
"s_nop 0\n"
TR "a[240:241],v1 offset:28672\n"
TR "a[242:243],v1 offset:30720\n"
MF "v[96:99],v[184:187],a[80:83],v[96:99]\n"
"s_add_u32 s82,s82,s71\n"
TR "a[244:245],v1 offset:32768\n"
TR "a[246:247],v1 offset:34816\n"
MF "v[104:107],v[184:187],a[84:87],v[252:255]\n"
LD "v[224:227],v9,s[40:43],s82 offen sc1\n"
MF "v[104:107],v[180:183],a[44:47],v[104:107]\n"
"s_nop 0\n"
"ds_write_addtid_b32 v56\n"
"ds_write_addtid_b32 v57 offset:256\n"
MF "v[104:107],v[176:179],a[4:7],v[104:107]\n"
"s_nop 0\n"
"ds_write_addtid_b32 v58 offset:512\n"
"ds_write_addtid_b32 v59 offset:768\n"
MF "v[112:115],v[176:179],a[8:11],v[252:255]\n"
"s_nop 0\n"
"ds_write_addtid_b32 v60 offset:1024\n"
"ds_write_addtid_b32 v61 offset:1280\n"
MF "v[112:115],v[180:183],a[48:51],v[112:115]\n"
"s_nop 0\n"
"ds_write_addtid_b32 v62 offset:1536\n"
"ds_write_addtid_b32 v63 offset:1792\n"
MF "v[112:115],v[184:187],a[88:91],v[112:115]\n"
"s_nop 0\n"
"ds_write_addtid_b32 v64 offset:2048\n"
"ds_write_addtid_b32 v65 offset:2304\n"
MF "v[120:123],v[184:187],a[92:95],v[252:255]\n"
"s_nop 0\n"
"ds_write_addtid_b32 v66 offset:2560\n"
"ds_write_addtid_b32 v67 offset:2816\n"
MF "v[120:123],v[180:183],a[52:55],v[120:123]\n"
"s_nop 0\n"
"ds_write_addtid_b32 v68 offset:3072\n"
"ds_write_addtid_b32 v69 offset:3328\n"
MF "v[120:123],v[176:179],a[12:15],v[120:123]\n"
"s_nop 0\n"
"ds_write_addtid_b32 v70 offset:3584\n"
"ds_write_addtid_b32 v71 offset:3840\n"
MF "v[128:131],v[176:179],a[16:19],v[252:255]\n"
"s_nop 0\n"
"ds_write_addtid_b32 v72 offset:4096\n"
"ds_write_addtid_b32 v73 offset:4352\n"
MF "v[128:131],v[180:183],a[56:59],v[128:131]\n"
"s_nop 0\n"
"ds_write_addtid_b32 v74 offset:4608\n"
"ds_write_addtid_b32 v75 offset:4864\n"
MF "v[128:131],v[184:187],a[96:99],v[128:131]\n"
"s_waitcnt lgkmcnt(15)\n"
"ds_write_addtid_b32 v76 offset:5120\n"
MF "v[132:135],a[244:247],a[96:99],v[252:255]\n"
"s_nop 0\n"
TR "v[176:177],v2 offset:24576\n"
TR "v[178:179],v2 offset:26624\n"
MF "v[132:135],a[240:243],a[56:59],v[132:135]\n"
"s_nop 0\n"
TR "v[180:181],v2 offset:28672\n"
TR "v[182:183],v2 offset:30720\n"
MF "v[132:135],v[188:191],a[16:19],v[132:135]\n"
"s_nop 0\n"
TR "v[184:185],v2 offset:32768\n"
TR "v[186:187],v2 offset:34816\n"
MF "v[124:127],v[188:191],a[12:15],v[252:255]\n"
"s_nop 0\n"
"ds_write_addtid_b32 v77 offset:5376\n"
"ds_write_addtid_b32 v78 offset:5632\n"
MF "v[124:127],a[240:243],a[52:55],v[124:127]\n"
"s_nop 0\n"
"ds_write_addtid_b32 v79 offset:5888\n"
"ds_write_addtid_b32 v80 offset:6144\n"
MF "v[124:127],a[244:247],a[92:95],v[124:127]\n"
"s_nop 0\n"
"ds_write_addtid_b32 v81 offset:6400\n"
"ds_write_addtid_b32 v82 offset:6656\n"
MF "v[116:119],a[244:247],a[88:91],v[252:255]\n"
"s_nop 0\n"
"ds_write_addtid_b32 v83 offset:6912\n"
"ds_write_addtid_b32 v84 offset:7168\n"
MF "v[116:119],a[240:243],a[48:51],v[116:119]\n"
"s_nop 0\n"
"ds_write_addtid_b32 v85 offset:7424\n"
"ds_write_addtid_b32 v86 offset:7680\n"
MF "v[116:119],v[188:191],a[8:11],v[116:119]\n"
"s_nop 0\n"
"ds_write_addtid_b32 v87 offset:7936\n"
"ds_write_addtid_b32 v88 offset:8192\n"
MF "v[108:111],v[188:191],a[4:7],v[252:255]\n"
"s_nop 0\n"
"ds_write_addtid_b32 v89 offset:8448\n"
"ds_write_addtid_b32 v90 offset:8704\n"
MF "v[108:111],a[240:243],a[44:47],v[108:111]\n"
"s_nop 0\n"
"ds_write_addtid_b32 v91 offset:8960\n"
"ds_write_addtid_b32 v92 offset:9216\n"
MF "v[108:111],a[244:247],a[84:87],v[108:111]\n"
"s_nop 0\n"
"ds_write_addtid_b32 v93 offset:9472\n"
"ds_write_addtid_b32 v94 offset:9728\n"
MF "v[100:103],a[244:247],a[80:83],v[252:255]\n"
"ds_write_addtid_b32 v95 offset:9984\n"
MF "v[100:103],a[240:243],a[40:43],v[100:103]\n"
MF "v[100:103],v[188:191],a[0:3],v[100:103]\n"
"s_waitcnt lgkmcnt(15)\n"
MF "v[136:139],v[176:179],a[0:3],v[252:255]\n"
"s_nop 0\n"
TR "v[188:189],v3 offset:24576\n"
TR "v[190:191],v3 offset:26624\n"
MF "v[136:139],v[180:183],a[40:43],v[136:139]\n"
"s_nop 0\n"
TR "a[240:241],v3 offset:28672\n"
TR "a[242:243],v3 offset:30720\n"
MF "v[136:139],v[184:187],a[80:83],v[136:139]\n"
"s_nop 0\n"
TR "a[244:245],v3 offset:32768\n"
TR "a[246:247],v3 offset:34816\n"
MF "v[144:147],v[184:187],a[84:87],v[252:255]\n"
"s_waitcnt lgkmcnt(0)\n"
"s_barrier\n"
MF "v[144:147],v[180:183],a[44:47],v[144:147]\n"
"ds_read2st64_b32 v[56:57],v13 offset0:192 offset1:193\n"
MF "v[144:147],v[176:179],a[4:7],v[144:147]\n"
"ds_read2st64_b32 v[58:59],v13 offset0:194 offset1:195\n"
MF "v[152:155],v[176:179],a[8:11],v[252:255]\n"
"ds_read2st64_b32 v[60:61],v13 offset0:196 offset1:197\n"
MF "v[152:155],v[180:183],a[48:51],v[152:155]\n"
"ds_read2st64_b32 v[62:63],v13 offset0:198 offset1:199\n"
MF "v[152:155],v[184:187],a[88:91],v[152:155]\n"
"ds_read2st64_b32 v[64:65],v13 offset0:200 offset1:201\n"
MF "v[160:163],v[184:187],a[92:95],v[252:255]\n"
"ds_read2st64_b32 v[66:67],v13 offset0:202 offset1:203\n"
MF "v[160:163],v[180:183],a[52:55],v[160:163]\n"
"ds_read2st64_b32 v[68:69],v13 offset0:204 offset1:205\n"
MF "v[160:163],v[176:179],a[12:15],v[160:163]\n"
"ds_read2st64_b32 v[70:71],v13 offset0:206 offset1:207\n"
MF "v[168:171],v[176:179],a[16:19],v[252:255]\n"
"s_waitcnt lgkmcnt(4)\n"
"ds_read2st64_b32 v[72:73],v13 offset0:208 offset1:209\n"
"v_add_f32 v16,v16,v56\n"
MF "v[168:171],v[180:183],a[56:59],v[168:171]\n"
"ds_read2st64_b32 v[74:75],v13 offset0:210 offset1:211\n"
"v_add_f32 v17,v17,v57\n"
MF "v[168:171],v[184:187],a[96:99],v[168:171]\n"
"s_waitcnt lgkmcnt(0)\n"
"s_barrier\n"
MF "v[172:175],a[244:247],a[96:99],v[252:255]\n"
"s_nop 0\n"
TR "v[176:177],v0\n"
TR "v[178:179],v0 offset:2048\n"
MF "v[172:175],a[240:243],a[56:59],v[172:175]\n"
"s_nop 0\n"
TR "v[180:181],v0 offset:4096\n"
TR "v[182:183],v0 offset:6144\n"
MF "v[172:175],v[188:191],a[16:19],v[172:175]\n"
"s_nop 0\n"
TR "v[184:185],v0 offset:8192\n"
TR "v[186:187],v0 offset:10240\n"
MF "v[164:167],v[188:191],a[12:15],v[252:255]\n"
"ds_read2st64_b32 v[76:77],v13 offset0:212 offset1:213\n"
"v_add_f32 v18,v18,v58\n"
MF "v[164:167],a[240:243],a[52:55],v[164:167]\n"
"ds_read2st64_b32 v[78:79],v13 offset0:214 offset1:215\n"
"v_add_f32 v19,v19,v59\n"
MF "v[164:167],a[244:247],a[92:95],v[164:167]\n"
"ds_read2st64_b32 v[80:81],v13 offset0:216 offset1:217\n"
"v_add_f32 v20,v20,v60\n"
MF "v[156:159],a[244:247],a[88:91],v[252:255]\n"
"ds_read2st64_b32 v[82:83],v13 offset0:218 offset1:219\n"
"v_add_f32 v21,v21,v61\n"
MF "v[156:159],a[240:243],a[48:51],v[156:159]\n"
"ds_read2st64_b32 v[84:85],v13 offset0:220 offset1:221\n"
"v_add_f32 v22,v22,v62\n"
MF "v[156:159],v[188:191],a[8:11],v[156:159]\n"
"ds_read2st64_b32 v[86:87],v13 offset0:222 offset1:223\n"
"v_add_f32 v23,v23,v63\n"
MF "v[148:151],v[188:191],a[4:7],v[252:255]\n"
"s_waitcnt vmcnt(5)\n"
"v_mov_b32 v14,s76\n"
"s_add_u32 s76,s76,1\n"
MF "v[148:151],a[240:243],a[44:47],v[148:151]\n"
"buffer_store_dword v14,v15,s[40:43],s94 offen sc1\n"
MF "v[148:151],a[244:247],a[84:87],v[148:151]\n"
"ds_read2st64_b32 v[88:89],v13 offset0:224 offset1:225\n"
CV "v16,v16,v17\n"
MF "v[140:143],a[244:247],a[80:83],v[252:255]\n"
"ds_read2st64_b32 v[90:91],v13 offset0:226 offset1:227\n"
CV "v17,v18,v19\n"
MF "v[140:143],a[240:243],a[40:43],v[140:143]\n"
"ds_read2st64_b32 v[92:93],v13 offset0:228 offset1:229\n"
CV "v18,v20,v21\n"
MF "v[140:143],v[188:191],a[0:3],v[140:143]\n"
"s_waitcnt lgkmcnt(9)\n"
"ds_read2st64_b32 v[94:95],v13 offset0:230 offset1:231\n"
CV "v19,v22,v23\n"
MF "v[96:99],v[176:179],a[120:123],v[96:99]\n"
"s_nop 0\n"
TR "v[188:189],v1\n"
TR "v[190:191],v1 offset:2048\n"
MF "v[96:99],v[180:183],a[160:163],v[96:99]\n"
"s_nop 0\n"
TR "a[240:241],v1 offset:4096\n"
TR "a[242:243],v1 offset:6144\n"
MF "v[96:99],v[184:187],a[200:203],v[96:99]\n"
"s_add_u32 s87,s90,0\n"
TR "a[244:245],v1 offset:8192\n"
TR "a[246:247],v1 offset:10240\n"
MF "v[104:107],v[184:187],a[204:207],v[104:107]\n"
"buffer_store_dwordx4 v[16:19],v9,s[40:43],s87 offen sc1\n"
MF "v[104:107],v[180:183],a[164:167],v[104:107]\n"
"v_add_f32 v24,v24,v64\n"
"v_add_f32 v25,v25,v65\n"
MF "v[104:107],v[176:179],a[124:127],v[104:107]\n"
"v_add_f32 v26,v26,v66\n"
"v_add_f32 v27,v27,v67\n"
MF "v[112:115],v[176:179],a[128:131],v[112:115]\n"
"v_add_f32 v28,v28,v68\n"
"v_add_f32 v29,v29,v69\n"
MF "v[112:115],v[180:183],a[168:171],v[112:115]\n"
"v_add_f32 v30,v30,v70\n"
"v_add_f32 v31,v31,v71\n"
MF "v[112:115],v[184:187],a[208:211],v[112:115]\n"
CV "v24,v24,v25\n"
CV "v25,v26,v27\n"
MF "v[120:123],v[184:187],a[212:215],v[120:123]\n"
"s_add_u32 s87,s90,1024\n"
CV "v26,v28,v29\n"
CV "v27,v30,v31\n"
MF "v[120:123],v[180:183],a[172:175],v[120:123]\n"
"s_waitcnt lgkmcnt(14)\n"
"buffer_store_dwordx4 v[24:27],v9,s[40:43],s87 offen sc1\n"
MF "v[120:123],v[176:179],a[132:135],v[120:123]\n"
"v_add_f32 v32,v32,v72\n"
"v_add_f32 v33,v33,v73\n"
MF "v[128:131],v[176:179],a[136:139],v[128:131]\n"
"v_add_f32 v34,v34,v74\n"
"v_add_f32 v35,v35,v75\n"
MF "v[128:131],v[180:183],a[176:179],v[128:131]\n"
"v_add_f32 v36,v36,v76\n"
"v_add_f32 v37,v37,v77\n"
MF "v[128:131],v[184:187],a[216:219],v[128:131]\n"
"s_waitcnt lgkmcnt(0)\n"
"v_add_f32 v38,v38,v78\n"
"v_add_f32 v39,v39,v79\n"
MF "v[132:135],a[244:247],a[216:219],v[132:135]\n"
"s_nop 0\n"
TR "v[176:177],v2\n"
TR "v[178:179],v2 offset:2048\n"
MF "v[132:135],a[240:243],a[176:179],v[132:135]\n"
"s_nop 0\n"
TR "v[180:181],v2 offset:4096\n"
TR "v[182:183],v2 offset:6144\n"
MF "v[132:135],v[188:191],a[136:139],v[132:135]\n"
"s_nop 0\n"
TR "v[184:185],v2 offset:8192\n"
TR "v[186:187],v2 offset:10240\n"
MF "v[124:127],v[188:191],a[132:135],v[124:127]\n"
CV "v32,v32,v33\n"
CV "v33,v34,v35\n"
MF "v[124:127],a[240:243],a[172:175],v[124:127]\n"
"s_add_u32 s87,s90,2048\n"
CV "v34,v36,v37\n"
CV "v35,v38,v39\n"
MF "v[124:127],a[244:247],a[212:215],v[124:127]\n"
"buffer_store_dwordx4 v[32:35],v9,s[40:43],s87 offen sc1\n"
MF "v[116:119],a[244:247],a[208:211],v[116:119]\n"
"v_add_f32 v40,v40,v80\n"
"v_add_f32 v41,v41,v81\n"
MF "v[116:119],a[240:243],a[168:171],v[116:119]\n"
"v_add_f32 v42,v42,v82\n"
"v_add_f32 v43,v43,v83\n"
MF "v[116:119],v[188:191],a[128:131],v[116:119]\n"
"v_add_f32 v44,v44,v84\n"
"v_add_f32 v45,v45,v85\n"
MF "v[108:111],v[188:191],a[124:127],v[108:111]\n"
"v_add_f32 v46,v46,v86\n"
"v_add_f32 v47,v47,v87\n"
MF "v[108:111],a[240:243],a[164:167],v[108:111]\n"
CV "v40,v40,v41\n"
CV "v41,v42,v43\n"
MF "v[108:111],a[244:247],a[204:207],v[108:111]\n"
"s_add_u32 s87,s90,3072\n"
CV "v42,v44,v45\n"
CV "v43,v46,v47\n"
MF "v[100:103],a[244:247],a[200:203],v[100:103]\n"
"buffer_store_dwordx4 v[40:43],v9,s[40:43],s87 offen sc1\n"
MF "v[100:103],a[240:243],a[160:163],v[100:103]\n"
"v_add_f32 v48,v48,v88\n"
"v_add_f32 v49,v49,v89\n"
MF "v[100:103],v[188:191],a[120:123],v[100:103]\n"
"s_waitcnt lgkmcnt(0)\n"
"v_add_f32 v50,v50,v90\n"
"v_add_f32 v51,v51,v91\n"
MF "v[136:139],v[176:179],a[120:123],v[136:139]\n"
"s_nop 0\n"
TR "v[188:189],v3\n"
TR "v[190:191],v3 offset:2048\n"
MF "v[136:139],v[180:183],a[160:163],v[136:139]\n"
"s_nop 0\n"
TR "a[240:241],v3 offset:4096\n"
TR "a[242:243],v3 offset:6144\n"
MF "v[136:139],v[184:187],a[200:203],v[136:139]\n"
"s_nop 0\n"
TR "a[244:245],v3 offset:8192\n"
TR "a[246:247],v3 offset:10240\n"
MF "v[144:147],v[184:187],a[204:207],v[144:147]\n"
"v_add_f32 v52,v52,v92\n"
"v_add_f32 v53,v53,v93\n"
MF "v[144:147],v[180:183],a[164:167],v[144:147]\n"
"v_add_f32 v54,v54,v94\n"
"v_add_f32 v55,v55,v95\n"
MF "v[144:147],v[176:179],a[124:127],v[144:147]\n"
CV "v48,v48,v49\n"
CV "v49,v50,v51\n"
MF "v[152:155],v[176:179],a[128:131],v[152:155]\n"
"s_add_u32 s87,s90,4096\n"
CV "v50,v52,v53\n"
CV "v51,v54,v55\n"
MF "v[152:155],v[180:183],a[168:171],v[152:155]\n"
"buffer_store_dwordx4 v[48:51],v9,s[40:43],s87 offen sc1\n"
MF "v[152:155],v[184:187],a[208:211],v[152:155]\n"
MF "v[160:163],v[184:187],a[212:215],v[160:163]\n"
MF "v[160:163],v[180:183],a[172:175],v[160:163]\n"
MF "v[160:163],v[176:179],a[132:135],v[160:163]\n"
MF "v[168:171],v[176:179],a[136:139],v[168:171]\n"
MF "v[168:171],v[180:183],a[176:179],v[168:171]\n"
MF "v[168:171],v[184:187],a[216:219],v[168:171]\n"
"s_waitcnt lgkmcnt(0)\n"
"s_barrier\n"
MF "v[172:175],a[244:247],a[216:219],v[172:175]\n"
"s_nop 0\n"
TR "v[176:177],v0 offset:49152\n"
TR "v[178:179],v0 offset:51200\n"
MF "v[172:175],a[240:243],a[176:179],v[172:175]\n"
"s_nop 0\n"
TR "v[180:181],v0 offset:53248\n"
TR "v[182:183],v0 offset:55296\n"
MF "v[172:175],v[188:191],a[136:139],v[172:175]\n"
"s_nop 0\n"
TR "v[184:185],v0 offset:57344\n"
TR "v[186:187],v0 offset:59392\n"
MF "v[164:167],v[188:191],a[132:135],v[164:167]\n"
"buffer_load_dword v10,v12,s[40:43],s86 offen sc1\n"
MF "v[164:167],a[240:243],a[172:175],v[164:167]\n"
MF "v[164:167],a[244:247],a[212:215],v[164:167]\n"
MF "v[156:159],a[244:247],a[208:211],v[156:159]\n"
MF "v[156:159],a[240:243],a[168:171],v[156:159]\n"
MF "v[156:159],v[188:191],a[128:131],v[156:159]\n"
MF "v[148:151],v[188:191],a[124:127],v[148:151]\n"
MF "v[148:151],a[240:243],a[164:167],v[148:151]\n"
MF "v[148:151],a[244:247],a[204:207],v[148:151]\n"
MF "v[140:143],a[244:247],a[200:203],v[140:143]\n"
MF "v[140:143],a[240:243],a[160:163],v[140:143]\n"
MF "v[140:143],v[188:191],a[120:123],v[140:143]\n"
"s_waitcnt lgkmcnt(0)\n"
MF "v[96:99],v[176:179],a[20:23],v[96:99]\n"
"s_nop 0\n"
TR "v[188:189],v1 offset:49152\n"
TR "v[190:191],v1 offset:51200\n"
MF "v[96:99],v[180:183],a[60:63],v[96:99]\n"
"s_nop 0\n"
TR "a[240:241],v1 offset:53248\n"
TR "a[242:243],v1 offset:55296\n"
MF "v[96:99],v[184:187],a[100:103],v[96:99]\n"
"s_nop 0\n"
TR "a[244:245],v1 offset:57344\n"
TR "a[246:247],v1 offset:59392\n"
MF "v[104:107],v[184:187],a[104:107],v[104:107]\n"
MF "v[104:107],v[180:183],a[64:67],v[104:107]\n"
MF "v[104:107],v[176:179],a[24:27],v[104:107]\n"
"s_waitcnt vmcnt(7)\n"
MF "v[136:139],v[208:211],a[252:255],v[136:139]\n"
MF "v[144:147],v[212:215],a[252:255],v[144:147]\n"
MF "v[152:155],v[216:219],a[252:255],v[152:155]\n"
MF "v[160:163],v[220:223],a[252:255],v[160:163]\n"
MF "v[168:171],v[224:227],a[252:255],v[168:171]\n"
MF "v[140:143],v[208:211],a[248:251],v[140:143]\n"
MF "v[148:151],v[212:215],a[248:251],v[148:151]\n"
MF "v[156:159],v[216:219],a[248:251],v[156:159]\n"
MF "v[164:167],v[220:223],a[248:251],v[164:167]\n"
MF "v[172:175],v[224:227],a[248:251],v[172:175]\n"
MF "v[112:115],v[176:179],a[28:31],v[112:115]\n"
MF "v[112:115],v[180:183],a[68:71],v[112:115]\n"
MF "v[112:115],v[184:187],a[108:111],v[112:115]\n"
MF "v[120:123],v[184:187],a[112:115],v[120:123]\n"
MF "v[120:123],v[180:183],a[72:75],v[120:123]\n"
"s_bfe_u32 s87,s22,0x30014\n"
MF "v[120:123],v[176:179],a[32:35],v[120:123]\n"
"s_lshl_b32 s87,s87,7\n"
MF "v[128:131],v[176:179],a[36:39],v[128:131]\n"
"s_and_b32 s60,s56,1\n"
MF "v[128:131],v[180:183],a[76:79],v[128:131]\n"
"s_mul_i32 s60,s60,204800\n"
MF "v[128:131],v[184:187],a[116:119],v[128:131]\n"
"s_waitcnt lgkmcnt(0)\n"
MF "v[132:135],a[244:247],a[116:119],v[132:135]\n"
"s_add_u32 s87,s87,s60\n"
TR "v[176:177],v2 offset:49152\n"
TR "v[178:179],v2 offset:51200\n"
MF "v[132:135],a[240:243],a[76:79],v[132:135]\n"
"s_mov_b32 s71,204800\n"
TR "v[180:181],v2 offset:53248\n"
TR "v[182:183],v2 offset:55296\n"
MF "v[132:135],v[188:191],a[36:39],v[132:135]\n"
"s_mov_b32 s66,204800\n"
TR "v[184:185],v2 offset:57344\n"
TR "v[186:187],v2 offset:59392\n"
MF "v[124:127],v[188:191],a[32:35],v[124:127]\n"
"s_add_u32 s72,s78,2\n"
MF "v[124:127],a[240:243],a[72:75],v[124:127]\n"
"s_and_b32 s72,s72,15\n"
MF "v[124:127],a[244:247],a[112:115],v[124:127]\n"
"s_bfe_u32 s60,s22,0x10014\n"
MF "v[116:119],a[244:247],a[108:111],v[116:119]\n"
"s_lshl_b32 s60,s60,5\n"
MF "v[116:119],a[240:243],a[68:71],v[116:119]\n"
"v_xor_b32 v242,s60,v8\n"
MF "v[116:119],v[188:191],a[28:31],v[116:119]\n"
MF "v[108:111],v[188:191],a[24:27],v[108:111]\n"
"s_add_u32 s16,s78,6\n"
MF "v[108:111],a[240:243],a[64:67],v[108:111]\n"
"s_and_b32 s16,s16,15\n"
MF "v[108:111],a[244:247],a[104:107],v[108:111]\n"
"s_add_u32 s17,s78,4\n"
MF "v[100:103],a[244:247],a[100:103],v[100:103]\n"
"s_and_b32 s17,s17,15\n"
MF "v[100:103],a[240:243],a[60:63],v[100:103]\n"
MF "v[100:103],v[188:191],a[20:23],v[100:103]\n"
"s_waitcnt lgkmcnt(0)\n"
MF "v[136:139],v[176:179],a[20:23],v[136:139]\n"
"s_nop 0\n"
TR "v[188:189],v3 offset:49152\n"
TR "v[190:191],v3 offset:51200\n"
MF "v[136:139],v[180:183],a[60:63],v[136:139]\n"
"s_nop 0\n"
TR "a[240:241],v3 offset:53248\n"
TR "a[242:243],v3 offset:55296\n"
MF "v[136:139],v[184:187],a[100:103],v[136:139]\n"
"s_nop 0\n"
TR "a[244:245],v3 offset:57344\n"
TR "a[246:247],v3 offset:59392\n"
MF "v[144:147],v[184:187],a[104:107],v[144:147]\n"
MF "v[144:147],v[180:183],a[64:67],v[144:147]\n"
"v_mov_b32 v14,s76\n"
"s_add_u32 s76,s76,1\n"
MF "v[144:147],v[176:179],a[24:27],v[144:147]\n"
MF "v[152:155],v[176:179],a[28:31],v[152:155]\n"
MF "v[152:155],v[180:183],a[68:71],v[152:155]\n"
MF "v[152:155],v[184:187],a[108:111],v[152:155]\n"
MF "v[160:163],v[184:187],a[112:115],v[160:163]\n"
MF "v[160:163],v[180:183],a[72:75],v[160:163]\n"
MF "v[160:163],v[176:179],a[32:35],v[160:163]\n"
MF "v[168:171],v[176:179],a[36:39],v[168:171]\n"
MF "v[168:171],v[180:183],a[76:79],v[168:171]\n"
MF "v[168:171],v[184:187],a[116:119],v[168:171]\n"
"s_waitcnt vmcnt(0) lgkmcnt(0)\n"
"s_barrier\n"
MF "v[172:175],a[244:247],a[116:119],v[172:175]\n"
"buffer_store_dword v14,v15,s[40:43],s94 offen sc1\n"
MF "v[172:175],a[240:243],a[76:79],v[172:175]\n"
"s_nop 0\n"
TR "v[176:177],v6\n"
TR "v[178:179],v6 offset:2048\n"
MF "v[172:175],v[188:191],a[36:39],v[172:175]\n"
"s_nop 0\n"
TR "v[180:181],v6 offset:4096\n"
TR "v[182:183],v6 offset:6144\n"
MF "v[164:167],v[188:191],a[32:35],v[164:167]\n"
"s_nop 0\n"
TR "v[184:185],v6 offset:8192\n"
TR "v[186:187],v6 offset:10240\n"
MF "v[164:167],a[240:243],a[72:75],v[164:167]\n"
"v_sub_u32 v10,v10,v11\n"
MF "v[164:167],a[244:247],a[112:115],v[164:167]\n"
"v_readlane_b32 s16,v10,s16\n"
MF "v[156:159],a[244:247],a[108:111],v[156:159]\n"
"s_cmp_lt_u32 s16,9\n"
"s_cselect_b32 s16,0xf0000000,0\n"
MF "v[156:159],a[240:243],a[68:71],v[156:159]\n"
"v_readlane_b32 s17,v10,s17\n"
MF "v[156:159],v[188:191],a[28:31],v[156:159]\n"
"s_cmp_lt_u32 s17,10\n"
"s_cselect_b32 s17,0xf0000000,0\n"
MF "v[148:151],v[188:191],a[24:27],v[148:151]\n"
"s_bfe_u32 s62,s22,0x30014\n"
MF "v[148:151],a[240:243],a[64:67],v[148:151]\n"
"s_add_u32 s62,s62,3\n"
"s_and_b32 s62,s62,7\n"
"s_mul_i32 s62,s62,204800\n"
MF "v[148:151],a[244:247],a[104:107],v[148:151]\n"
"s_add_u32 s62,s62,s73\n"
"s_add_u32 s62,s62,122880\n"
"s_or_b32 s62,s62,s16\n"
MF "v[140:143],a[244:247],a[100:103],v[140:143]\n"
LD "v[56:59],v9,s[40:43],s62 offen sc1\n"
MF "v[140:143],a[240:243],a[60:63],v[140:143]\n"
LD "v[64:67],v9,s[40:43],s62 offen offset:1024 sc1\n"
MF "v[140:143],v[188:191],a[20:23],v[140:143]\n"
"s_waitcnt lgkmcnt(0)\n"
LD "v[72:75],v9,s[40:43],s62 offen offset:2048 sc1\n"
MF "v[136:139],v[176:179],a[140:143],v[136:139]\n"
"s_nop 0\n"
TR "v[188:189],v7\n"
TR "v[190:191],v7 offset:2048\n"
MF "v[136:139],v[180:183],a[180:183],v[136:139]\n"
"s_nop 0\n"
TR "a[240:241],v7 offset:4096\n"
TR "a[242:243],v7 offset:6144\n"
MF "v[136:139],v[184:187],a[220:223],v[136:139]\n"
"s_nop 0\n"
TR "a[244:245],v7 offset:8192\n"
TR "a[246:247],v7 offset:10240\n"
MF "v[144:147],v[184:187],a[224:227],v[144:147]\n"
LD "v[80:83],v9,s[40:43],s62 offen offset:3072 sc1\n"
MF "v[144:147],v[180:183],a[184:187],v[144:147]\n"
MF "v[144:147],v[176:179],a[144:147],v[144:147]\n"
"s_add_u32 s62,s62,4096\n"
MF "v[152:155],v[176:179],a[148:151],v[152:155]\n"
LD "v[88:91],v9,s[40:43],s62 offen sc1\n"
MF "v[152:155],v[180:183],a[188:191],v[152:155]\n"
"s_bfe_u32 s62,s22,0x30014\n"
"ds_write_addtid_b32 v136\n"
"ds_write_addtid_b32 v137 offset:256\n"
MF "v[152:155],v[184:187],a[228:231],v[152:155]\n"
"s_add_u32 s62,s62,2\n"
"s_and_b32 s62,s62,7\n"
"s_mul_i32 s62,s62,204800\n"
MF "v[160:163],v[184:187],a[232:235],v[160:163]\n"
"s_add_u32 s62,s62,s73\n"
"s_add_u32 s62,s62,143360\n"
"s_or_b32 s62,s62,s17\n"
MF "v[160:163],v[180:183],a[192:195],v[160:163]\n"
LD "v[60:63],v9,s[40:43],s62 offen sc1\n"
MF "v[160:163],v[176:179],a[152:155],v[160:163]\n"
"s_nop 0\n"
"ds_write_addtid_b32 v138 offset:512\n"
"ds_write_addtid_b32 v139 offset:768\n"
MF "v[168:171],v[176:179],a[156:159],v[168:171]\n"
LD "v[68:71],v9,s[40:43],s62 offen offset:1024 sc1\n"
MF "v[168:171],v[180:183],a[196:199],v[168:171]\n"
"s_nop 0\n"
"ds_write_addtid_b32 v144 offset:2048\n"
"ds_write_addtid_b32 v145 offset:2304\n"
MF "v[168:171],v[184:187],a[236:239],v[168:171]\n"
"s_waitcnt lgkmcnt(6)\n"
LD "v[76:79],v9,s[40:43],s62 offen offset:2048 sc1\n"
MF "v[140:143],v[188:191],a[140:143],v[140:143]\n"
"s_nop 0\n"
TR "v[176:177],v4\n"
TR "v[178:179],v4 offset:2048\n"
MF "v[140:143],a[240:243],a[180:183],v[140:143]\n"
"s_nop 0\n"
TR "v[180:181],v4 offset:4096\n"
TR "v[182:183],v4 offset:6144\n"
MF "v[140:143],a[244:247],a[220:223],v[140:143]\n"
"s_nop 0\n"
TR "v[184:185],v4 offset:8192\n"
TR "v[186:187],v4 offset:10240\n"
MF "v[148:151],a[244:247],a[224:227],v[148:151]\n"
LD "v[84:87],v9,s[40:43],s62 offen offset:3072 sc1\n"
MF "v[148:151],a[240:243],a[184:187],v[148:151]\n"
"s_nop 0\n"
"ds_write_addtid_b32 v146 offset:2560\n"
"ds_write_addtid_b32 v147 offset:2816\n"
MF "v[148:151],v[188:191],a[144:147],v[148:151]\n"
"s_add_u32 s62,s62,4096\n"
"ds_write_addtid_b32 v152 offset:4096\n"
"ds_write_addtid_b32 v153 offset:4352\n"
MF "v[156:159],v[188:191],a[148:151],v[156:159]\n"
LD "v[92:95],v9,s[40:43],s62 offen sc1\n"
MF "v[156:159],a[240:243],a[188:191],v[156:159]\n"
"s_nop 0\n"
"ds_write_addtid_b32 v154 offset:4608\n"
"ds_write_addtid_b32 v155 offset:4864\n"
MF "v[156:159],a[244:247],a[228:231],v[156:159]\n"
"s_nop 0\n"
"ds_write_addtid_b32 v160 offset:6144\n"
"ds_write_addtid_b32 v161 offset:6400\n"
MF "v[164:167],a[244:247],a[232:235],v[164:167]\n"
"s_nop 0\n"
"ds_write_addtid_b32 v162 offset:6656\n"
"ds_write_addtid_b32 v163 offset:6912\n"
MF "v[164:167],a[240:243],a[192:195],v[164:167]\n"
"s_nop 0\n"
"ds_write_addtid_b32 v168 offset:8192\n"
"ds_write_addtid_b32 v169 offset:8448\n"
MF "v[164:167],v[188:191],a[152:155],v[164:167]\n"
"buffer_load_dword v10,v12,s[40:43],s86 offen sc1\n"
MF "v[172:175],v[188:191],a[156:159],v[172:175]\n"
"s_nop 0\n"
"ds_write_addtid_b32 v170 offset:8704\n"
"ds_write_addtid_b32 v171 offset:8960\n"
MF "v[172:175],a[240:243],a[196:199],v[172:175]\n"
"s_nop 0\n"
"ds_write_addtid_b32 v140 offset:1024\n"
"ds_write_addtid_b32 v141 offset:1280\n"
MF "v[172:175],a[244:247],a[236:239],v[172:175]\n"
"s_waitcnt lgkmcnt(15)\n"
"ds_write_addtid_b32 v142 offset:1536\n"
"ds_write_addtid_b32 v143 offset:1792\n"
MF "v[96:99],v[176:179],a[140:143],v[96:99]\n"
"s_nop 0\n"
TR "v[188:189],v5\n"
TR "v[190:191],v5 offset:2048\n"
MF "v[96:99],v[180:183],a[180:183],v[96:99]\n"
"s_nop 0\n"
TR "a[240:241],v5 offset:4096\n"
TR "a[242:243],v5 offset:6144\n"
MF "v[96:99],v[184:187],a[220:223],v[96:99]\n"
"s_nop 0\n"
TR "a[244:245],v5 offset:8192\n"
TR "a[246:247],v5 offset:10240\n"
MF "v[104:107],v[184:187],a[224:227],v[104:107]\n"
"s_nop 0\n"
"ds_write_addtid_b32 v148 offset:3072\n"
"ds_write_addtid_b32 v149 offset:3328\n"
MF "v[104:107],v[180:183],a[184:187],v[104:107]\n"
"s_nop 0\n"
"ds_write_addtid_b32 v150 offset:3584\n"
"ds_write_addtid_b32 v151 offset:3840\n"
MF "v[104:107],v[176:179],a[144:147],v[104:107]\n"
"s_nop 0\n"
"ds_write_addtid_b32 v156 offset:5120\n"
"ds_write_addtid_b32 v157 offset:5376\n"
MF "v[112:115],v[176:179],a[148:151],v[112:115]\n"
"s_nop 0\n"
"ds_write_addtid_b32 v158 offset:5632\n"
"ds_write_addtid_b32 v159 offset:5888\n"
MF "v[112:115],v[180:183],a[188:191],v[112:115]\n"
"ds_write_addtid_b32 v164 offset:7168\n"
MF "v[112:115],v[184:187],a[228:231],v[112:115]\n"
"ds_write_addtid_b32 v165 offset:7424\n"
MF "v[120:123],v[184:187],a[232:235],v[120:123]\n"
"ds_write_addtid_b32 v166 offset:7680\n"
MF "v[120:123],v[180:183],a[192:195],v[120:123]\n"
"ds_write_addtid_b32 v167 offset:7936\n"
MF "v[120:123],v[176:179],a[152:155],v[120:123]\n"
"ds_write_addtid_b32 v172 offset:9216\n"
MF "v[128:131],v[176:179],a[156:159],v[128:131]\n"
"ds_write_addtid_b32 v173 offset:9472\n"
MF "v[128:131],v[180:183],a[196:199],v[128:131]\n"
"ds_write_addtid_b32 v174 offset:9728\n"
MF "v[128:131],v[184:187],a[236:239],v[128:131]\n"
"s_waitcnt lgkmcnt(15)\n"
"ds_write_addtid_b32 v175 offset:9984\n"
MF "v[100:103],v[188:191],a[140:143],v[100:103]\n"
MF "v[100:103],a[240:243],a[180:183],v[100:103]\n"
MF "v[100:103],a[244:247],a[220:223],v[100:103]\n"
MF "v[108:111],a[244:247],a[224:227],v[108:111]\n"
MF "v[108:111],a[240:243],a[184:187],v[108:111]\n"
"s_waitcnt lgkmcnt(0)\n"
"s_barrier\n"
MF "v[108:111],v[188:191],a[144:147],v[108:111]\n"
"ds_read2st64_b32 v[16:17],v13 offset0:192 offset1:193\n"
MF "v[116:119],v[188:191],a[148:151],v[116:119]\n"
"ds_read2st64_b32 v[18:19],v13 offset0:194 offset1:195\n"
MF "v[116:119],a[240:243],a[188:191],v[116:119]\n"
"ds_read2st64_b32 v[20:21],v13 offset0:196 offset1:197\n"
MF "v[116:119],a[244:247],a[228:231],v[116:119]\n"
"ds_read2st64_b32 v[22:23],v13 offset0:198 offset1:199\n"
MF "v[124:127],a[244:247],a[232:235],v[124:127]\n"
"ds_read2st64_b32 v[24:25],v13 offset0:200 offset1:201\n"
MF "v[124:127],a[240:243],a[192:195],v[124:127]\n"
"ds_read2st64_b32 v[26:27],v13 offset0:202 offset1:203\n"
MF "v[124:127],v[188:191],a[152:155],v[124:127]\n"
"ds_read2st64_b32 v[28:29],v13 offset0:204 offset1:205\n"
MF "v[132:135],v[188:191],a[156:159],v[132:135]\n"
"ds_read2st64_b32 v[30:31],v13 offset0:206 offset1:207\n"
MF "v[132:135],a[240:243],a[196:199],v[132:135]\n"
"ds_read2st64_b32 v[32:33],v13 offset0:208 offset1:209\n"
MF "v[132:135],a[244:247],a[236:239],v[132:135]\n"
"ds_read2st64_b32 v[34:35],v13 offset0:210 offset1:211\n"
"ds_read2st64_b32 v[36:37],v13 offset0:212 offset1:213\n"
"ds_read2st64_b32 v[38:39],v13 offset0:214 offset1:215\n"
"ds_read2st64_b32 v[40:41],v13 offset0:216 offset1:217\n"
"ds_read2st64_b32 v[42:43],v13 offset0:218 offset1:219\n"
"ds_read2st64_b32 v[44:45],v13 offset0:220 offset1:221\n"
"ds_read2st64_b32 v[46:47],v13 offset0:222 offset1:223\n"
"ds_read2st64_b32 v[48:49],v13 offset0:224 offset1:225\n"
"ds_read2st64_b32 v[50:51],v13 offset0:226 offset1:227\n"
"ds_read2st64_b32 v[52:53],v13 offset0:228 offset1:229\n"
"ds_read2st64_b32 v[54:55],v13 offset0:230 offset1:231\n"
"s_waitcnt vmcnt(0)\n"
"v_sub_u32 v10,v10,v11\n"
"s_cmp_eq_u32 s16,0\n"
"s_cbranch_scc1 .Lhave13\n"
"s_add_u32 s59,s78,6\n"
"s_and_b32 s59,s59,15\n"
"s_nop 3\n"
".Lnf7:\n"
"v_readlane_b32 s58,v10,s59\n"
"s_cmp_lt_u32 s58,9\n"
"s_cbranch_scc1 .Lsp7\n"
".Lnb7:\n"
"s_bfe_u32 s62,s22,0x30014\n"
"s_add_u32 s62,s62,3\n"
"s_and_b32 s62,s62,7\n"
"s_mul_i32 s62,s62,204800\n"
"s_add_u32 s62,s62,s73\n"
"s_add_u32 s62,s62,122880\n"
LD "v[56:59],v9,s[40:43],s62 offen sc1\n"
LD "v[64:67],v9,s[40:43],s62 offen offset:1024 sc1\n"
LD "v[72:75],v9,s[40:43],s62 offen offset:2048 sc1\n"
LD "v[80:83],v9,s[40:43],s62 offen offset:3072 sc1\n"
"s_add_u32 s62,s62,4096\n"
LD "v[88:91],v9,s[40:43],s62 offen sc1\n"
".Lhave13:\n"
"s_cmp_eq_u32 s17,0\n"
"s_cbranch_scc1 .Lhave12\n"
"s_add_u32 s59,s78,4\n"
"s_and_b32 s59,s59,15\n"
"s_nop 3\n"
".Lnf8:\n"
"v_readlane_b32 s58,v10,s59\n"
"s_cmp_lt_u32 s58,10\n"
"s_cbranch_scc1 .Lsp8\n"
".Lnb8:\n"
"s_bfe_u32 s62,s22,0x30014\n"
"s_add_u32 s62,s62,2\n"
"s_and_b32 s62,s62,7\n"
"s_mul_i32 s62,s62,204800\n"
"s_add_u32 s62,s62,s73\n"
"s_add_u32 s62,s62,143360\n"
LD "v[60:63],v9,s[40:43],s62 offen sc1\n"
LD "v[68:71],v9,s[40:43],s62 offen offset:1024 sc1\n"
LD "v[76:79],v9,s[40:43],s62 offen offset:2048 sc1\n"
LD "v[84:87],v9,s[40:43],s62 offen offset:3072 sc1\n"
"s_add_u32 s62,s62,4096\n"
LD "v[92:95],v9,s[40:43],s62 offen sc1\n"
".Lhave12:\n"
".Lnf9:\n"
"v_readlane_b32 s58,v10,s72\n"
"s_cmp_lt_u32 s58,11\n"
"s_cbranch_scc1 .Lsp9\n"
".Lnb9:\n"
"s_bfe_u32 s62,s22,0x30014\n"
"s_add_u32 s62,s62,1\n"
"s_and_b32 s62,s62,7\n"
"s_mul_i32 s62,s62,204800\n"
"s_add_u32 s62,s62,s73\n"
"s_add_u32 s62,s62,163840\n"
LD "v[208:211],v9,s[40:43],s62 offen sc1\n"
LD "v[212:215],v9,s[40:43],s62 offen offset:1024 sc1\n"
LD "v[216:219],v9,s[40:43],s62 offen offset:2048 sc1\n"
LD "v[220:223],v9,s[40:43],s62 offen offset:3072 sc1\n"
"s_add_u32 s62,s62,4096\n"
LD "v[224:227],v9,s[40:43],s62 offen sc1\n"
"s_waitcnt lgkmcnt(15)\n"
"v_pk_add_f32 v[96:97],v[96:97],v[16:17]\n"
"v_pk_add_f32 v[98:99],v[98:99],v[18:19]\n"
"v_pk_add_f32 v[100:101],v[100:101],v[20:21]\n"
"v_pk_add_f32 v[102:103],v[102:103],v[22:23]\n"
"v_pk_add_f32 v[104:105],v[104:105],v[24:25]\n"
"s_waitcnt lgkmcnt(14)\n"
"v_pk_add_f32 v[106:107],v[106:107],v[26:27]\n"
"s_waitcnt lgkmcnt(13)\n"
"v_pk_add_f32 v[108:109],v[108:109],v[28:29]\n"
"s_waitcnt lgkmcnt(12)\n"
"v_pk_add_f32 v[110:111],v[110:111],v[30:31]\n"
"s_waitcnt lgkmcnt(11)\n"
"v_pk_add_f32 v[112:113],v[112:113],v[32:33]\n"
"s_waitcnt lgkmcnt(10)\n"
"v_pk_add_f32 v[114:115],v[114:115],v[34:35]\n"
"s_waitcnt lgkmcnt(9)\n"
"v_pk_add_f32 v[116:117],v[116:117],v[36:37]\n"
"s_waitcnt lgkmcnt(8)\n"
"v_pk_add_f32 v[118:119],v[118:119],v[38:39]\n"
"s_waitcnt lgkmcnt(7)\n"
"v_pk_add_f32 v[120:121],v[120:121],v[40:41]\n"
"s_waitcnt lgkmcnt(6)\n"
"v_pk_add_f32 v[122:123],v[122:123],v[42:43]\n"
"s_waitcnt lgkmcnt(5)\n"
"v_pk_add_f32 v[124:125],v[124:125],v[44:45]\n"
"s_waitcnt lgkmcnt(4)\n"
"v_pk_add_f32 v[126:127],v[126:127],v[46:47]\n"
"s_waitcnt lgkmcnt(3)\n"
"v_pk_add_f32 v[128:129],v[128:129],v[48:49]\n"
"s_waitcnt lgkmcnt(2)\n"
"v_pk_add_f32 v[130:131],v[130:131],v[50:51]\n"
"s_waitcnt lgkmcnt(1)\n"
"v_pk_add_f32 v[132:133],v[132:133],v[52:53]\n"
"s_waitcnt lgkmcnt(0)\n"
"v_pk_add_f32 v[134:135],v[134:135],v[54:55]\n"
"s_waitcnt vmcnt(5)\n"
"s_nop 0\n"
MF "v[100:103],v[56:59],a[252:255],v[100:103]\n"
MF "v[108:111],v[64:67],a[252:255],v[108:111]\n"
MF "v[116:119],v[72:75],a[252:255],v[116:119]\n"
MF "v[124:127],v[80:83],a[252:255],v[124:127]\n"
MF "v[132:135],v[88:91],a[252:255],v[132:135]\n"
MF "v[96:99],v[56:59],a[248:251],v[96:99]\n"
MF "v[104:107],v[64:67],a[248:251],v[104:107]\n"
MF "v[112:115],v[72:75],a[248:251],v[112:115]\n"
MF "v[120:123],v[80:83],a[248:251],v[120:123]\n"
MF "v[128:131],v[88:91],a[248:251],v[128:131]\n"
MF "v[96:99],v[60:63],a[252:255],v[96:99]\n"
MF "v[104:107],v[68:71],a[252:255],v[104:107]\n"
MF "v[112:115],v[76:79],a[252:255],v[112:115]\n"
MF "v[120:123],v[84:87],a[252:255],v[120:123]\n"
MF "v[128:131],v[92:95],a[252:255],v[128:131]\n"
MF "v[100:103],v[60:63],a[248:251],v[100:103]\n"
MF "v[108:111],v[68:71],a[248:251],v[108:111]\n"
MF "v[116:119],v[76:79],a[248:251],v[116:119]\n"
MF "v[124:127],v[84:87],a[248:251],v[124:127]\n"
MF "v[132:135],v[92:95],a[248:251],v[132:135]\n"
"s_waitcnt vmcnt(0)\n"
MF "v[100:103],v[208:211],a[252:255],v[100:103]\n"
MF "v[96:99],v[208:211],a[248:251],v[96:99]\n"
MF "v[108:111],v[212:215],a[252:255],v[108:111]\n"
MF "v[104:107],v[212:215],a[248:251],v[104:107]\n"
MF "v[116:119],v[216:219],a[252:255],v[116:119]\n"
"s_nop 4\n"
CV "v96,v96,v97\n"
CV "v97,v98,v99\n"
MF "v[112:115],v[216:219],a[248:251],v[112:115]\n"
CV "v98,v100,v101\n"
CV "v99,v102,v103\n"
MF "v[124:127],v[220:223],a[252:255],v[124:127]\n"
CV "v104,v104,v105\n"
CV "v105,v106,v107\n"
MF "v[120:123],v[220:223],a[248:251],v[120:123]\n"
CV "v106,v108,v109\n"
CV "v107,v110,v111\n"
"v_permlane16_swap_b32 v96,v98\n"
"v_permlane16_swap_b32 v97,v99\n"
"buffer_store_dwordx4 v[96:99],v242,s[44:47],s87 offen sc1\n"
"s_add_u32 s87,s87,40960\n"
MF "v[132:135],v[224:227],a[252:255],v[132:135]\n"
CV "v112,v112,v113\n"
CV "v113,v114,v115\n"
MF "v[128:131],v[224:227],a[248:251],v[128:131]\n"
CV "v114,v116,v117\n"
CV "v115,v118,v119\n"
"v_permlane16_swap_b32 v104,v106\n"
"v_permlane16_swap_b32 v105,v107\n"
"buffer_store_dwordx4 v[104:107],v242,s[44:47],s87 offen sc1\n"
"s_add_u32 s87,s87,40960\n"
CV "v120,v120,v121\n"
CV "v121,v122,v123\n"
CV "v122,v124,v125\n"
CV "v123,v126,v127\n"
"v_permlane16_swap_b32 v112,v114\n"
"v_permlane16_swap_b32 v113,v115\n"
"buffer_store_dwordx4 v[112:115],v242,s[44:47],s87 offen sc1\n"
"s_add_u32 s87,s87,40960\n"
CV "v128,v128,v129\n"
CV "v129,v130,v131\n"
CV "v130,v132,v133\n"
CV "v131,v134,v135\n"
"v_permlane16_swap_b32 v120,v122\n"
"v_permlane16_swap_b32 v121,v123\n"
"buffer_store_dwordx4 v[120:123],v242,s[44:47],s87 offen sc1\n"
"s_add_u32 s87,s87,40960\n"
"v_permlane16_swap_b32 v128,v130\n"
"v_permlane16_swap_b32 v129,v131\n"
"buffer_store_dwordx4 v[128:131],v242,s[44:47],s87 offen sc1\n"
"s_add_u32 s87,s87,40960\n"
"s_branch .Ljobs\n"
".Ljobs:\n"
"s_cmp_ge_u32 s92,40\n"
"s_cbranch_scc1 .Ldone\n"
"buffer_load_dword v10,v12,s[40:43],s86 offen sc1\n"
"s_mul_i32 s95,s92,3277\n"
"s_lshr_b32 s95,s95,16\n"
"s_mul_i32 s59,s95,20\n"
"s_sub_u32 s59,s92,s59\n"
"s_cmp_ge_u32 s59,10\n"
"s_cselect_b32 s96,4294557760,0\n"
"s_add_u32 s96,s96,1024\n"
"s_mul_i32 s59,s59,40960\n"
"s_add_u32 s96,s96,s59\n"
"s_lshl_b32 s59,s95,7\n"
"s_add_u32 s96,s96,s59\n"
"s_add_u32 s95,s95,3\n"
"s_waitcnt vmcnt(0)\n"
".Ljtry:\n"
"s_cmp_eq_u32 s98,0\n"
"s_cselect_b32 s95,0,s95\n"
"v_sub_u32 v10,v10,v11\n"
"v_cmp_gt_u32 vcc,s95,v10\n"
"s_lshl_b32 s82,s92,10\n"
"s_add_u32 s82,s82,s93\n"
"s_cmp_eq_u64 vcc,0\n"
"s_cselect_b32 s82,s82,0xf0000000\n"
"s_cselect_b32 s84,s96,0xf0000000\n"
"s_cselect_b32 s59,32,0\n"
"s_add_u32 s92,s92,s59\n"
"s_cmp_eq_u32 s59,0\n"
"s_cbranch_scc0 .Ljgo\n"
"s_sub_u32 s98,s98,1\n"
"s_sleep 2\n"
"s_branch .Ljobs\n"
".Ljgo:\n"
"s_mov_b32 s83,s84\n"
LD "v[208:211],v9,s[40:43],s82 offen sc1\n"
"s_add_u32 s82,s82,s71\n"
LD "v[212:215],v9,s[40:43],s82 offen sc1\n"
"s_add_u32 s82,s82,s71\n"
LD "v[216:219],v9,s[40:43],s82 offen sc1\n"
"s_add_u32 s82,s82,s71\n"
LD "v[220:223],v9,s[40:43],s82 offen sc1\n"
"s_add_u32 s82,s82,s71\n"
LD "v[224:227],v9,s[40:43],s82 offen sc1\n"
"s_add_u32 s82,s82,s66\n"
LD "v[228:231],v9,s[40:43],s82 offen sc1\n"
"s_add_u32 s82,s82,s71\n"
LD "v[232:235],v9,s[40:43],s82 offen sc1\n"
"s_add_u32 s82,s82,s71\n"
LD "v[236:239],v9,s[40:43],s82 offen sc1\n"
"buffer_load_dword v10,v12,s[40:43],s86 offen sc1\n"
"s_waitcnt vmcnt(0)\n"
".Ljfin:\n"
"s_nop 1\n"
"v_mov_b32 v240,v208\n"
"v_mov_b32 v241,v209\n"
MF "v[248:251],v[210:213],a[250:253],0\n"
MF "v[244:247],v[214:217],a[250:253],0\n"
MF "v[248:251],v[218:221],a[250:253],v[248:251]\n"
MF "v[244:247],v[222:225],a[250:253],v[244:247]\n"
MF "v[248:251],v[226:229],a[250:253],v[248:251]\n"
MF "v[244:247],v[230:233],a[250:253],v[244:247]\n"
MF "v[248:251],v[234:237],a[250:253],v[248:251]\n"
MF "v[244:247],v[238:241],a[250:253],v[244:247]\n"
"s_nop 7\n"
CV "v244,v244,v245\n"
CV "v245,v246,v247\n"
CV "v246,v248,v249\n"
CV "v247,v250,v251\n"
"s_nop 0\n"
"v_permlane16_swap_b32 v244,v246\n"
"v_permlane16_swap_b32 v245,v247\n"
"buffer_store_dwordx4 v[244:247],v8,s[44:47],s83 offen sc1\n"
"s_cmp_ge_u32 s92,40\n"
"s_cbranch_scc1 .Ldone\n"
"s_mul_i32 s95,s92,3277\n"
"s_lshr_b32 s95,s95,16\n"
"s_mul_i32 s59,s95,20\n"
"s_sub_u32 s59,s92,s59\n"
"s_cmp_ge_u32 s59,10\n"
"s_cselect_b32 s96,4294557760,0\n"
"s_add_u32 s96,s96,1024\n"
"s_mul_i32 s59,s59,40960\n"
"s_add_u32 s96,s96,s59\n"
"s_lshl_b32 s59,s95,7\n"
"s_add_u32 s96,s96,s59\n"
"s_add_u32 s95,s95,3\n"
"s_branch .Ljtry\n"
".Lsp0:\n"
"s_cmp_eq_u32 s98,0\n"
"s_cbranch_scc1 .Lnb0\n"
"s_sub_u32 s98,s98,1\n"
"s_sleep 2\n"
"buffer_load_dword v10,v12,s[40:43],s86 offen sc1\n"
"s_waitcnt vmcnt(0)\n"
"v_sub_u32 v10,v10,v11\n"
"s_nop 1\n"
"s_branch .Lnf0\n"
".Lsp1:\n"
"s_cmp_eq_u32 s98,0\n"
"s_cbranch_scc1 .Lnb1\n"
"s_sub_u32 s98,s98,1\n"
"s_sleep 2\n"
"buffer_load_dword v10,v12,s[40:43],s86 offen sc1\n"
"s_waitcnt vmcnt(0)\n"
"v_sub_u32 v10,v10,v11\n"
"s_nop 1\n"
"s_branch .Lnf1\n"
".Lsp2:\n"
"s_cmp_eq_u32 s98,0\n"
"s_cbranch_scc1 .Lnb2\n"
"s_sub_u32 s98,s98,1\n"
"s_sleep 2\n"
"buffer_load_dword v10,v12,s[40:43],s86 offen sc1\n"
"s_waitcnt vmcnt(0)\n"
"v_sub_u32 v10,v10,v11\n"
"s_nop 1\n"
"s_branch .Lnf2\n"
".Lsp3:\n"
"s_cmp_eq_u32 s98,0\n"
"s_cbranch_scc1 .Lnb3\n"
"s_sub_u32 s98,s98,1\n"
"s_sleep 2\n"
"buffer_load_dword v10,v12,s[40:43],s86 offen sc1\n"
"s_waitcnt vmcnt(0)\n"
"v_sub_u32 v10,v10,v11\n"
"s_nop 1\n"
"s_branch .Lnf3\n"
".Lsp4:\n"
"s_cmp_eq_u32 s98,0\n"
"s_cbranch_scc1 .Lnb4\n"
"s_sub_u32 s98,s98,1\n"
"s_sleep 2\n"
"buffer_load_dword v10,v12,s[40:43],s86 offen sc1\n"
"s_waitcnt vmcnt(0)\n"
"v_sub_u32 v10,v10,v11\n"
"s_nop 1\n"
"s_branch .Lnf4\n"
".Lsp5:\n"
"s_cmp_eq_u32 s98,0\n"
"s_cbranch_scc1 .Lnb5\n"
"s_sub_u32 s98,s98,1\n"
"s_sleep 2\n"
"buffer_load_dword v10,v12,s[40:43],s86 offen sc1\n"
"s_waitcnt vmcnt(0)\n"
"v_sub_u32 v10,v10,v11\n"
"s_nop 1\n"
"s_branch .Lnf5\n"
".Lsp6:\n"
"s_cmp_eq_u32 s98,0\n"
"s_cbranch_scc1 .Lnb6\n"
"s_sub_u32 s98,s98,1\n"
"s_sleep 2\n"
"buffer_load_dword v10,v12,s[40:43],s86 offen sc1\n"
"s_waitcnt vmcnt(0)\n"
"v_sub_u32 v10,v10,v11\n"
"s_nop 1\n"
"s_branch .Lnf6\n"
".Lsp7:\n"
"s_cmp_eq_u32 s98,0\n"
"s_cbranch_scc1 .Lnb7\n"
"s_sub_u32 s98,s98,1\n"
"s_sleep 2\n"
"buffer_load_dword v10,v12,s[40:43],s86 offen sc1\n"
"s_waitcnt vmcnt(0)\n"
"v_sub_u32 v10,v10,v11\n"
"s_nop 1\n"
"s_branch .Lnf7\n"
".Lsp8:\n"
"s_cmp_eq_u32 s98,0\n"
"s_cbranch_scc1 .Lnb8\n"
"s_sub_u32 s98,s98,1\n"
"s_sleep 2\n"
"buffer_load_dword v10,v12,s[40:43],s86 offen sc1\n"
"s_waitcnt vmcnt(0)\n"
"v_sub_u32 v10,v10,v11\n"
"s_nop 1\n"
"s_branch .Lnf8\n"
".Lsp9:\n"
"s_cmp_eq_u32 s98,0\n"
"s_cbranch_scc1 .Lnb9\n"
"s_sub_u32 s98,s98,1\n"
"s_sleep 2\n"
"buffer_load_dword v10,v12,s[40:43],s86 offen sc1\n"
"s_waitcnt vmcnt(0)\n"
"v_sub_u32 v10,v10,v11\n"
"s_nop 1\n"
"s_branch .Lnf9\n"
".Lqy:\n"
MF "v[216:219],a[216:219],a[60:63],v[216:219]\n"
TR "a[208:209],v0 offset:8\n"
MF "v[216:219],a[220:223],a[100:103],v[216:219]\n"
TR "a[210:211],v0 offset:520\n"
MF "v[224:227],a[220:223],a[104:107],v[224:227]\n"
MF "v[224:227],a[216:219],a[64:67],v[224:227]\n"
TR "a[212:213],v0 offset:4104\n"
MF "v[232:235],a[216:219],a[68:71],v[232:235]\n"
TR "a[214:215],v0 offset:4616\n"
MF "v[232:235],a[220:223],a[108:111],v[232:235]\n"
MF "v[240:243],a[220:223],a[112:115],v[240:243]\n"
MF "v[240:243],a[216:219],a[72:75],v[240:243]\n"
MF "v[248:251],a[216:219],a[76:79],v[248:251]\n"
MF "v[248:251],a[220:223],a[116:119],v[248:251]\n"
MF "v[248:251],a[200:203],a[156:159],v[248:251]\n"
TR "a[216:217],v0 offset:16392\n"
MF "v[248:251],a[204:207],a[196:199],v[248:251]\n"
TR "a[218:219],v0 offset:16904\n"
MF "v[240:243],a[204:207],a[192:195],v[240:243]\n"
MF "v[240:243],a[200:203],a[152:155],v[240:243]\n"
TR "a[220:221],v0 offset:20488\n"
MF "v[232:235],a[200:203],a[148:151],v[232:235]\n"
TR "a[222:223],v0 offset:21000\n"
MF "v[232:235],a[204:207],a[188:191],v[232:235]\n"
MF "v[224:227],a[204:207],a[184:187],v[224:227]\n"
MF "v[224:227],a[200:203],a[144:147],v[224:227]\n"
"v_fma_f32 v248,v248,s54,v14\n"
"v_fma_f32 v249,v249,s54,v14\n"
MF "v[216:219],a[200:203],a[140:143],v[216:219]\n"
"v_fma_f32 v250,v250,s54,v14\n"
"v_fma_f32 v251,v251,s54,v14\n"
MF "v[216:219],a[204:207],a[180:183],v[216:219]\n"
"s_waitcnt lgkmcnt(4)\n"
"v_fma_f32 v252,v252,s54,v14\n"
"v_fma_f32 v253,v253,s54,v14\n"
MF "v[180:183],a[208:211],a[60:63],v[180:183]\n"
TR "a[200:201],v0\n"
"v_fma_f32 v254,v254,s54,v14\n"
MF "v[180:183],a[212:215],a[100:103],v[180:183]\n"
TR "a[202:203],v0 offset:512\n"
"v_fma_f32 v255,v255,s54,v14\n"
MF "v[188:191],a[212:215],a[104:107],v[188:191]\n"
CV "v255,v254,v255\n"
CV "v254,v252,v253\n"
MF "v[188:191],a[208:211],a[64:67],v[188:191]\n"
TR "a[204:205],v0 offset:4096\n"
CV "v253,v250,v251\n"
MF "v[196:199],a[208:211],a[68:71],v[196:199]\n"
TR "a[206:207],v0 offset:4608\n"
CV "v252,v248,v249\n"
MF "v[196:199],a[212:215],a[108:111],v[196:199]\n"
"s_add_u32 s58,s75,4096\n"
"v_fma_f32 v240,v240,s54,v14\n"
"v_fma_f32 v241,v241,s54,v14\n"
MF "v[204:207],a[212:215],a[112:115],v[204:207]\n"
"buffer_store_dwordx4 v[252:255],v9,s[40:43],s58 offen offset:1024 sc1\n"
MF "v[204:207],a[208:211],a[72:75],v[204:207]\n"
"v_fma_f32 v242,v242,s54,v14\n"
"v_fma_f32 v243,v243,s54,v14\n"
MF "v[212:215],a[208:211],a[76:79],v[212:215]\n"
"v_fma_f32 v244,v244,s54,v14\n"
"v_fma_f32 v245,v245,s54,v14\n"
MF "v[212:215],a[212:215],a[116:119],v[212:215]\n"
"s_waitcnt lgkmcnt(4)\n"
"v_fma_f32 v246,v246,s54,v14\n"
"v_fma_f32 v247,v247,s54,v14\n"
MF "v[180:183],a[216:219],a[140:143],v[180:183]\n"
TR "a[208:209],v0 offset:16384\n"
CV "v251,v246,v247\n"
MF "v[180:183],a[220:223],a[180:183],v[180:183]\n"
TR "a[210:211],v0 offset:16896\n"
CV "v250,v244,v245\n"
MF "v[188:191],a[220:223],a[184:187],v[188:191]\n"
CV "v249,v242,v243\n"
CV "v248,v240,v241\n"
MF "v[188:191],a[216:219],a[144:147],v[188:191]\n"
TR "a[212:213],v0 offset:20480\n"
"v_fma_f32 v232,v232,s54,v14\n"
MF "v[196:199],a[216:219],a[148:151],v[196:199]\n"
TR "a[214:215],v0 offset:20992\n"
"v_fma_f32 v233,v233,s54,v14\n"
MF "v[196:199],a[220:223],a[188:191],v[196:199]\n"
"buffer_store_dwordx4 v[248:251],v9,s[40:43],s58 offen sc1\n"
MF "v[204:207],a[220:223],a[192:195],v[204:207]\n"
"v_fma_f32 v234,v234,s54,v14\n"
"v_fma_f32 v235,v235,s54,v14\n"
MF "v[204:207],a[216:219],a[152:155],v[204:207]\n"
"v_fma_f32 v236,v236,s54,v14\n"
"v_fma_f32 v237,v237,s54,v14\n"
MF "v[212:215],a[216:219],a[156:159],v[212:215]\n"
"v_fma_f32 v238,v238,s54,v14\n"
"v_fma_f32 v239,v239,s54,v14\n"
MF "v[212:215],a[220:223],a[196:199],v[212:215]\n"
"s_waitcnt vmcnt(10) lgkmcnt(0)\n"
"s_barrier\n"
MF "v[176:179],a[200:203],a[60:63],v[176:179]\n"
"s_add_u32 s66,s67,4718592\n"
TR "a[216:217],v1 offset:32776\n"
CV "v247,v238,v239\n"
MF "v[176:179],a[204:207],a[100:103],v[176:179]\n"
"s_sub_u32 s68,s68,384\n"
TR "a[218:219],v1 offset:33288\n"
CV "v246,v236,v237\n"
MF "v[184:187],a[204:207],a[104:107],v[184:187]\n"
"s_mov_b32 s83,7077888\n"
CV "v245,v234,v235\n"
CV "v244,v232,v233\n"
MF "v[184:187],a[200:203],a[64:67],v[184:187]\n"
"s_add_u32 m0,s62,0\n"
TR "a[220:221],v1 offset:36872\n"
"v_fma_f32 v224,v224,s54,v14\n"
MF "v[192:195],a[200:203],a[68:71],v[192:195]\n"
"s_add_u32 s82,s66,2359296\n"
TR "a[222:223],v1 offset:37384\n"
"v_fma_f32 v225,v225,s54,v14\n"
MF "v[192:195],a[204:207],a[108:111],v[192:195]\n"
LD "v6,s[28:31],s82 offen lds\n"
MF "v[200:203],a[204:207],a[112:115],v[200:203]\n"
"buffer_store_dwordx4 v[244:247],v9,s[40:43],s75 offen offset:3072 sc1\n"
MF "v[200:203],a[200:203],a[72:75],v[200:203]\n"
"v_fma_f32 v226,v226,s54,v14\n"
"v_fma_f32 v227,v227,s54,v14\n"
MF "v[208:211],a[200:203],a[76:79],v[208:211]\n"
"v_fma_f32 v228,v228,s54,v14\n"
"v_fma_f32 v229,v229,s54,v14\n"
MF "v[208:211],a[204:207],a[116:119],v[208:211]\n"
LD "v11,s[28:31],s82 offen offset:1024 lds\n"
MF "v[208:211],a[208:211],a[156:159],v[208:211]\n"
TR "a[200:201],v1 offset:49160\n"
"v_fma_f32 v230,v230,s54,v14\n"
MF "v[208:211],a[212:215],a[196:199],v[208:211]\n"
TR "a[202:203],v1 offset:49672\n"
"v_fma_f32 v231,v231,s54,v14\n"
MF "v[200:203],a[212:215],a[192:195],v[200:203]\n"
CV "v243,v230,v231\n"
CV "v242,v228,v229\n"
MF "v[200:203],a[208:211],a[152:155],v[200:203]\n"
TR "a[204:205],v1 offset:53256\n"
CV "v241,v226,v227\n"
MF "v[192:195],a[208:211],a[148:151],v[192:195]\n"
TR "a[206:207],v1 offset:53768\n"
CV "v240,v224,v225\n"
MF "v[192:195],a[212:215],a[188:191],v[192:195]\n"
"v_fma_f32 v216,v216,s54,v14\n"
"v_fma_f32 v217,v217,s54,v14\n"
MF "v[184:187],a[212:215],a[184:187],v[184:187]\n"
"buffer_store_dwordx4 v[240:243],v9,s[40:43],s75 offen offset:2048 sc1\n"
MF "v[184:187],a[208:211],a[144:147],v[184:187]\n"
"v_fma_f32 v218,v218,s54,v14\n"
"v_fma_f32 v219,v219,s54,v14\n"
MF "v[176:179],a[208:211],a[140:143],v[176:179]\n"
LD "v12,s[28:31],s82 offen offset:2048 lds\n"
MF "v[176:179],a[212:215],a[180:183],v[176:179]\n"
"s_waitcnt lgkmcnt(4)\n"
"v_fma_f32 v220,v220,s54,v14\n"
"v_fma_f32 v221,v221,s54,v14\n"
MF "v[140:143],a[216:219],a[60:63],v[140:143]\n"
TR "a[208:209],v1 offset:32768\n"
"v_fma_f32 v222,v222,s54,v14\n"
MF "v[140:143],a[220:223],a[100:103],v[140:143]\n"
TR "a[210:211],v1 offset:33280\n"
"v_fma_f32 v223,v223,s54,v14\n"
MF "v[148:151],a[220:223],a[104:107],v[148:151]\n"
CV "v239,v222,v223\n"
CV "v238,v220,v221\n"
MF "v[148:151],a[216:219],a[64:67],v[148:151]\n"
TR "a[212:213],v1 offset:36864\n"
CV "v237,v218,v219\n"
MF "v[156:159],a[216:219],a[68:71],v[156:159]\n"
TR "a[214:215],v1 offset:37376\n"
CV "v236,v216,v217\n"
MF "v[156:159],a[220:223],a[108:111],v[156:159]\n"
"v_fma_f32 v208,v208,s54,v14\n"
"v_fma_f32 v209,v209,s54,v14\n"
MF "v[164:167],a[220:223],a[112:115],v[164:167]\n"
"buffer_store_dwordx4 v[236:239],v9,s[40:43],s75 offen offset:1024 sc1\n"
MF "v[164:167],a[216:219],a[72:75],v[164:167]\n"
LD "v13,s[28:31],s82 offen offset:3072 lds\n"
MF "v[172:175],a[216:219],a[76:79],v[172:175]\n"
"v_fma_f32 v210,v210,s54,v14\n"
"v_fma_f32 v211,v211,s54,v14\n"
MF "v[172:175],a[220:223],a[116:119],v[172:175]\n"
"s_waitcnt lgkmcnt(4)\n"
"v_fma_f32 v212,v212,s54,v14\n"
"v_fma_f32 v213,v213,s54,v14\n"
MF "v[140:143],a[200:203],a[140:143],v[140:143]\n"
TR "a[216:217],v1 offset:49152\n"
"v_fma_f32 v214,v214,s54,v14\n"
MF "v[140:143],a[204:207],a[180:183],v[140:143]\n"
TR "a[218:219],v1 offset:49664\n"
"v_fma_f32 v215,v215,s54,v14\n"
MF "v[148:151],a[204:207],a[184:187],v[148:151]\n"
CV "v235,v214,v215\n"
CV "v234,v212,v213\n"
MF "v[148:151],a[200:203],a[144:147],v[148:151]\n"
TR "a[220:221],v1 offset:53248\n"
CV "v233,v210,v211\n"
MF "v[156:159],a[200:203],a[148:151],v[156:159]\n"
TR "a[222:223],v1 offset:53760\n"
CV "v232,v208,v209\n"
MF "v[156:159],a[204:207],a[188:191],v[156:159]\n"
"v_fma_f32 v200,v200,s54,v14\n"
"v_fma_f32 v201,v201,s54,v14\n"
MF "v[164:167],a[204:207],a[192:195],v[164:167]\n"
"buffer_store_dwordx4 v[232:235],v9,s[40:43],s75 offen sc1\n"
MF "v[164:167],a[200:203],a[152:155],v[164:167]\n"
"v_fma_f32 v202,v202,s54,v14\n"
"v_fma_f32 v203,v203,s54,v14\n"
MF "v[172:175],a[200:203],a[156:159],v[172:175]\n"
"v_fma_f32 v204,v204,s54,v14\n"
"v_fma_f32 v205,v205,s54,v14\n"
MF "v[172:175],a[204:207],a[196:199],v[172:175]\n"
"s_waitcnt lgkmcnt(0)\n"
"v_fma_f32 v206,v206,s54,v14\n"
"v_fma_f32 v207,v207,s54,v14\n"
MF "v[136:139],a[208:211],a[60:63],v[136:139]\n"
"s_add_u32 m0,s62,16384\n"
TR "a[200:201],v0 offset:32776\n"
CV "v231,v206,v207\n"
MF "v[136:139],a[212:215],a[100:103],v[136:139]\n"
"s_add_u32 s82,s66,2359424\n"
TR "a[202:203],v0 offset:33288\n"
CV "v230,v204,v205\n"
MF "v[144:147],a[212:215],a[104:107],v[144:147]\n"
LD "v6,s[28:31],s82 offen lds\n"
MF "v[144:147],a[208:211],a[64:67],v[144:147]\n"
TR "a[204:205],v0 offset:36872\n"
CV "v229,v202,v203\n"
MF "v[152:155],a[208:211],a[68:71],v[152:155]\n"
TR "a[206:207],v0 offset:37384\n"
CV "v228,v200,v201\n"
MF "v[152:155],a[212:215],a[108:111],v[152:155]\n"
AW "a84,v228\n"
AW "a85,v229\n"
MF "v[160:163],a[212:215],a[112:115],v[160:163]\n"
AW "a86,v230\n"
AW "a87,v231\n"
MF "v[160:163],a[208:211],a[72:75],v[160:163]\n"
"v_fma_f32 v192,v192,s54,v14\n"
"v_fma_f32 v193,v193,s54,v14\n"
MF "v[168:171],a[208:211],a[76:79],v[168:171]\n"
"v_fma_f32 v194,v194,s54,v14\n"
"v_fma_f32 v195,v195,s54,v14\n"
MF "v[168:171],a[212:215],a[116:119],v[168:171]\n"
LD "v11,s[28:31],s82 offen offset:1024 lds\n"
MF "v[168:171],a[216:219],a[156:159],v[168:171]\n"
TR "a[208:209],v0 offset:49160\n"
"v_fma_f32 v196,v196,s54,v14\n"
MF "v[168:171],a[220:223],a[196:199],v[168:171]\n"
TR "a[210:211],v0 offset:49672\n"
"v_fma_f32 v197,v197,s54,v14\n"
MF "v[160:163],a[220:223],a[192:195],v[160:163]\n"
"v_fma_f32 v198,v198,s54,v14\n"
"v_fma_f32 v199,v199,s54,v14\n"
MF "v[160:163],a[216:219],a[152:155],v[160:163]\n"
TR "a[212:213],v0 offset:53256\n"
CV "v227,v198,v199\n"
MF "v[152:155],a[216:219],a[148:151],v[152:155]\n"
TR "a[214:215],v0 offset:53768\n"
CV "v226,v196,v197\n"
MF "v[152:155],a[220:223],a[188:191],v[152:155]\n"
CV "v225,v194,v195\n"
CV "v224,v192,v193\n"
MF "v[144:147],a[220:223],a[184:187],v[144:147]\n"
AW "a80,v224\n"
AW "a81,v225\n"
MF "v[144:147],a[216:219],a[144:147],v[144:147]\n"
AW "a82,v226\n"
AW "a83,v227\n"
MF "v[136:139],a[216:219],a[140:143],v[136:139]\n"
LD "v12,s[28:31],s82 offen offset:2048 lds\n"
MF "v[136:139],a[220:223],a[180:183],v[136:139]\n"
"s_waitcnt lgkmcnt(4)\n"
"v_fma_f32 v184,v184,s54,v14\n"
"v_fma_f32 v185,v185,s54,v14\n"
MF "v[100:103],a[200:203],a[60:63],v[100:103]\n"
TR "a[216:217],v0 offset:32768\n"
"v_fma_f32 v186,v186,s54,v14\n"
MF "v[100:103],a[204:207],a[100:103],v[100:103]\n"
TR "a[218:219],v0 offset:33280\n"
"v_fma_f32 v187,v187,s54,v14\n"
MF "v[108:111],a[204:207],a[104:107],v[108:111]\n"
"v_fma_f32 v188,v188,s54,v14\n"
"v_fma_f32 v189,v189,s54,v14\n"
MF "v[108:111],a[200:203],a[64:67],v[108:111]\n"
TR "a[220:221],v0 offset:36864\n"
"v_fma_f32 v190,v190,s54,v14\n"
MF "v[116:119],a[200:203],a[68:71],v[116:119]\n"
TR "a[222:223],v0 offset:37376\n"
"v_fma_f32 v191,v191,s54,v14\n"
MF "v[116:119],a[204:207],a[108:111],v[116:119]\n"
CV "v223,v190,v191\n"
CV "v222,v188,v189\n"
MF "v[124:127],a[204:207],a[112:115],v[124:127]\n"
CV "v221,v186,v187\n"
CV "v220,v184,v185\n"
MF "v[124:127],a[200:203],a[72:75],v[124:127]\n"
LD "v13,s[28:31],s82 offen offset:3072 lds\n"
MF "v[132:135],a[200:203],a[76:79],v[132:135]\n"
AW "a56,v220\n"
AW "a57,v221\n"
MF "v[132:135],a[204:207],a[116:119],v[132:135]\n"
"s_waitcnt lgkmcnt(4)\n"
AW "a58,v222\n"
AW "a59,v223\n"
MF "v[100:103],a[208:211],a[140:143],v[100:103]\n"
TR "a[200:201],v0 offset:49152\n"
"v_fma_f32 v176,v176,s54,v14\n"
MF "v[100:103],a[212:215],a[180:183],v[100:103]\n"
TR "a[202:203],v0 offset:49664\n"
"v_fma_f32 v177,v177,s54,v14\n"
MF "v[108:111],a[212:215],a[184:187],v[108:111]\n"
"v_fma_f32 v178,v178,s54,v14\n"
"v_fma_f32 v179,v179,s54,v14\n"
MF "v[108:111],a[208:211],a[144:147],v[108:111]\n"
TR "a[204:205],v0 offset:53248\n"
"v_fma_f32 v180,v180,s54,v14\n"
MF "v[116:119],a[208:211],a[148:151],v[116:119]\n"
TR "a[206:207],v0 offset:53760\n"
"v_fma_f32 v181,v181,s54,v14\n"
MF "v[116:119],a[212:215],a[188:191],v[116:119]\n"
"v_fma_f32 v182,v182,s54,v14\n"
"v_fma_f32 v183,v183,s54,v14\n"
MF "v[124:127],a[212:215],a[192:195],v[124:127]\n"
"v_add_u32 v0,0xc000,v0\n"
CV "v219,v182,v183\n"
MF "v[124:127],a[208:211],a[152:155],v[124:127]\n"
"v_add_u32 v1,0xc000,v1\n"
CV "v218,v180,v181\n"
MF "v[132:135],a[208:211],a[156:159],v[132:135]\n"
CV "v217,v178,v179\n"
CV "v216,v176,v177\n"
MF "v[132:135],a[212:215],a[196:199],v[132:135]\n"
"s_waitcnt vmcnt(14) lgkmcnt(0)\n"
"s_barrier\n"
MF "v[96:99],a[216:219],a[60:63],v[96:99]\n"
"s_add_u32 m0,s62,32768\n"
TR "a[208:209],v1 offset:16392\n"
AW "a52,v216\n"
MF "v[96:99],a[220:223],a[100:103],v[96:99]\n"
"s_add_u32 s82,s66,2359552\n"
TR "a[210:211],v1 offset:16904\n"
AW "a53,v217\n"
MF "v[104:107],a[220:223],a[104:107],v[104:107]\n"
LD "v6,s[28:31],s82 offen lds\n"
MF "v[104:107],a[216:219],a[64:67],v[104:107]\n"
TR "a[212:213],v1 offset:20488\n"
AW "a54,v218\n"
MF "v[112:115],a[216:219],a[68:71],v[112:115]\n"
TR "a[214:215],v1 offset:21000\n"
AW "a55,v219\n"
MF "v[112:115],a[220:223],a[108:111],v[112:115]\n"
"v_fma_f32 v168,v168,s54,v14\n"
"v_fma_f32 v169,v169,s54,v14\n"
MF "v[120:123],a[220:223],a[112:115],v[120:123]\n"
"v_fma_f32 v170,v170,s54,v14\n"
"v_fma_f32 v171,v171,s54,v14\n"
MF "v[120:123],a[216:219],a[72:75],v[120:123]\n"
"v_fma_f32 v172,v172,s54,v14\n"
"v_fma_f32 v173,v173,s54,v14\n"
MF "v[128:131],a[216:219],a[76:79],v[128:131]\n"
"v_fma_f32 v174,v174,s54,v14\n"
"v_fma_f32 v175,v175,s54,v14\n"
MF "v[128:131],a[220:223],a[116:119],v[128:131]\n"
LD "v11,s[28:31],s82 offen offset:1024 lds\n"
MF "v[128:131],a[200:203],a[156:159],v[128:131]\n"
TR "a[216:217],v1 offset:32776\n"
CV "v215,v174,v175\n"
MF "v[128:131],a[204:207],a[196:199],v[128:131]\n"
TR "a[218:219],v1 offset:33288\n"
CV "v214,v172,v173\n"
MF "v[120:123],a[204:207],a[192:195],v[120:123]\n"
CV "v213,v170,v171\n"
CV "v212,v168,v169\n"
MF "v[120:123],a[200:203],a[152:155],v[120:123]\n"
TR "a[220:221],v1 offset:36872\n"
AW "a48,v212\n"
MF "v[112:115],a[200:203],a[148:151],v[112:115]\n"
TR "a[222:223],v1 offset:37384\n"
AW "a49,v213\n"
MF "v[112:115],a[204:207],a[188:191],v[112:115]\n"
AW "a50,v214\n"
AW "a51,v215\n"
MF "v[104:107],a[204:207],a[184:187],v[104:107]\n"
"v_fma_f32 v160,v160,s54,v14\n"
"v_fma_f32 v161,v161,s54,v14\n"
MF "v[104:107],a[200:203],a[144:147],v[104:107]\n"
"v_fma_f32 v162,v162,s54,v14\n"
"v_fma_f32 v163,v163,s54,v14\n"
MF "v[96:99],a[200:203],a[140:143],v[96:99]\n"
LD "v12,s[28:31],s82 offen offset:2048 lds\n"
MF "v[96:99],a[204:207],a[180:183],v[96:99]\n"
"s_waitcnt lgkmcnt(4)\n"
"v_fma_f32 v164,v164,s54,v14\n"
"v_fma_f32 v165,v165,s54,v14\n"
MF "v[60:63],a[208:211],a[60:63],v[60:63]\n"
TR "a[200:201],v1 offset:16384\n"
"v_fma_f32 v166,v166,s54,v14\n"
MF "v[60:63],a[212:215],a[100:103],v[60:63]\n"
TR "a[202:203],v1 offset:16896\n"
"v_fma_f32 v167,v167,s54,v14\n"
MF "v[68:71],a[212:215],a[104:107],v[68:71]\n"
CV "v211,v166,v167\n"
CV "v210,v164,v165\n"
MF "v[68:71],a[208:211],a[64:67],v[68:71]\n"
TR "a[204:205],v1 offset:20480\n"
CV "v209,v162,v163\n"
MF "v[76:79],a[208:211],a[68:71],v[76:79]\n"
TR "a[206:207],v1 offset:20992\n"
CV "v208,v160,v161\n"
MF "v[76:79],a[212:215],a[108:111],v[76:79]\n"
AW "a44,v208\n"
AW "a45,v209\n"
MF "v[84:87],a[212:215],a[112:115],v[84:87]\n"
AW "a46,v210\n"
AW "a47,v211\n"
MF "v[84:87],a[208:211],a[72:75],v[84:87]\n"
LD "v13,s[28:31],s82 offen offset:3072 lds\n"
MF "v[92:95],a[208:211],a[76:79],v[92:95]\n"
"v_fma_f32 v152,v152,s54,v14\n"
"v_fma_f32 v153,v153,s54,v14\n"
MF "v[92:95],a[212:215],a[116:119],v[92:95]\n"
"s_waitcnt lgkmcnt(4)\n"
"v_fma_f32 v154,v154,s54,v14\n"
"v_fma_f32 v155,v155,s54,v14\n"
MF "v[60:63],a[216:219],a[140:143],v[60:63]\n"
TR "a[208:209],v1 offset:32768\n"
"v_fma_f32 v156,v156,s54,v14\n"
MF "v[60:63],a[220:223],a[180:183],v[60:63]\n"
TR "a[210:211],v1 offset:33280\n"
"v_fma_f32 v157,v157,s54,v14\n"
MF "v[68:71],a[220:223],a[184:187],v[68:71]\n"
"v_fma_f32 v158,v158,s54,v14\n"
"v_fma_f32 v159,v159,s54,v14\n"
MF "v[68:71],a[216:219],a[144:147],v[68:71]\n"
TR "a[212:213],v1 offset:36864\n"
CV "v207,v158,v159\n"
MF "v[76:79],a[216:219],a[148:151],v[76:79]\n"
TR "a[214:215],v1 offset:37376\n"
CV "v206,v156,v157\n"
MF "v[76:79],a[220:223],a[188:191],v[76:79]\n"
CV "v205,v154,v155\n"
CV "v204,v152,v153\n"
MF "v[84:87],a[220:223],a[192:195],v[84:87]\n"
AW "a40,v204\n"
AW "a41,v205\n"
MF "v[84:87],a[216:219],a[152:155],v[84:87]\n"
AW "a42,v206\n"
AW "a43,v207\n"
MF "v[92:95],a[216:219],a[156:159],v[92:95]\n"
"v_fma_f32 v144,v144,s54,v14\n"
"v_fma_f32 v145,v145,s54,v14\n"
MF "v[92:95],a[220:223],a[196:199],v[92:95]\n"
"s_waitcnt lgkmcnt(0)\n"
"v_fma_f32 v146,v146,s54,v14\n"
"v_fma_f32 v147,v147,s54,v14\n"
MF "v[56:59],a[200:203],a[60:63],v[56:59]\n"
TR "a[216:217],v0 offset:16392\n"
"v_fma_f32 v148,v148,s54,v14\n"
MF "v[56:59],a[204:207],a[100:103],v[56:59]\n"
TR "a[218:219],v0 offset:16904\n"
"v_fma_f32 v149,v149,s54,v14\n"
MF "v[64:67],a[204:207],a[104:107],v[64:67]\n"
"v_fma_f32 v150,v150,s54,v14\n"
"v_fma_f32 v151,v151,s54,v14\n"
MF "v[64:67],a[200:203],a[64:67],v[64:67]\n"
TR "a[220:221],v0 offset:20488\n"
CV "v203,v150,v151\n"
MF "v[72:75],a[200:203],a[68:71],v[72:75]\n"
TR "a[222:223],v0 offset:21000\n"
CV "v202,v148,v149\n"
MF "v[72:75],a[204:207],a[108:111],v[72:75]\n"
CV "v201,v146,v147\n"
CV "v200,v144,v145\n"
MF "v[80:83],a[204:207],a[112:115],v[80:83]\n"
AW "a16,v200\n"
AW "a17,v201\n"
MF "v[80:83],a[200:203],a[72:75],v[80:83]\n"
AW "a18,v202\n"
AW "a19,v203\n"
MF "v[88:91],a[200:203],a[76:79],v[88:91]\n"
"v_fma_f32 v136,v136,s54,v14\n"
"v_fma_f32 v137,v137,s54,v14\n"
MF "v[88:91],a[204:207],a[116:119],v[88:91]\n"
"v_fma_f32 v138,v138,s54,v14\n"
"v_fma_f32 v139,v139,s54,v14\n"
MF "v[88:91],a[208:211],a[156:159],v[88:91]\n"
TR "a[200:201],v0 offset:32776\n"
"v_fma_f32 v140,v140,s54,v14\n"
MF "v[88:91],a[212:215],a[196:199],v[88:91]\n"
TR "a[202:203],v0 offset:33288\n"
"v_fma_f32 v141,v141,s54,v14\n"
MF "v[80:83],a[212:215],a[192:195],v[80:83]\n"
"v_fma_f32 v142,v142,s54,v14\n"
"v_fma_f32 v143,v143,s54,v14\n"
MF "v[80:83],a[208:211],a[152:155],v[80:83]\n"
TR "a[204:205],v0 offset:36872\n"
CV "v199,v142,v143\n"
MF "v[72:75],a[208:211],a[148:151],v[72:75]\n"
TR "a[206:207],v0 offset:37384\n"
CV "v198,v140,v141\n"
MF "v[72:75],a[212:215],a[188:191],v[72:75]\n"
CV "v197,v138,v139\n"
CV "v196,v136,v137\n"
MF "v[64:67],a[212:215],a[184:187],v[64:67]\n"
AW "a12,v196\n"
AW "a13,v197\n"
MF "v[64:67],a[208:211],a[144:147],v[64:67]\n"
AW "a14,v198\n"
AW "a15,v199\n"
MF "v[56:59],a[208:211],a[140:143],v[56:59]\n"
"v_fma_f32 v128,v128,s54,v14\n"
"v_fma_f32 v129,v129,s54,v14\n"
MF "v[56:59],a[212:215],a[180:183],v[56:59]\n"
"s_waitcnt lgkmcnt(4)\n"
"v_fma_f32 v130,v130,s54,v14\n"
"v_fma_f32 v131,v131,s54,v14\n"
MF "v[20:23],a[216:219],a[60:63],v[20:23]\n"
TR "a[208:209],v0 offset:16384\n"
"v_fma_f32 v132,v132,s54,v14\n"
MF "v[20:23],a[220:223],a[100:103],v[20:23]\n"
TR "a[210:211],v0 offset:16896\n"
"v_fma_f32 v133,v133,s54,v14\n"
MF "v[28:31],a[220:223],a[104:107],v[28:31]\n"
"v_fma_f32 v134,v134,s54,v14\n"
"v_fma_f32 v135,v135,s54,v14\n"
MF "v[28:31],a[216:219],a[64:67],v[28:31]\n"
TR "a[212:213],v0 offset:20480\n"
CV "v195,v134,v135\n"
MF "v[36:39],a[216:219],a[68:71],v[36:39]\n"
TR "a[214:215],v0 offset:20992\n"
CV "v194,v132,v133\n"
MF "v[36:39],a[220:223],a[108:111],v[36:39]\n"
CV "v193,v130,v131\n"
CV "v192,v128,v129\n"
MF "v[44:47],a[220:223],a[112:115],v[44:47]\n"
AW "a8,v192\n"
AW "a9,v193\n"
MF "v[44:47],a[216:219],a[72:75],v[44:47]\n"
AW "a10,v194\n"
AW "a11,v195\n"
MF "v[52:55],a[216:219],a[76:79],v[52:55]\n"
"v_fma_f32 v120,v120,s54,v14\n"
"v_fma_f32 v121,v121,s54,v14\n"
MF "v[52:55],a[220:223],a[116:119],v[52:55]\n"
"s_waitcnt lgkmcnt(4)\n"
"v_fma_f32 v122,v122,s54,v14\n"
"v_fma_f32 v123,v123,s54,v14\n"
MF "v[20:23],a[200:203],a[140:143],v[20:23]\n"
TR "a[216:217],v0 offset:32768\n"
"v_fma_f32 v124,v124,s54,v14\n"
MF "v[20:23],a[204:207],a[180:183],v[20:23]\n"
TR "a[218:219],v0 offset:33280\n"
"v_fma_f32 v125,v125,s54,v14\n"
MF "v[28:31],a[204:207],a[184:187],v[28:31]\n"
"v_fma_f32 v126,v126,s54,v14\n"
"v_fma_f32 v127,v127,s54,v14\n"
MF "v[28:31],a[200:203],a[144:147],v[28:31]\n"
TR "a[220:221],v0 offset:36864\n"
CV "v191,v126,v127\n"
MF "v[36:39],a[200:203],a[148:151],v[36:39]\n"
TR "a[222:223],v0 offset:37376\n"
CV "v190,v124,v125\n"
MF "v[36:39],a[204:207],a[188:191],v[36:39]\n"
CV "v189,v122,v123\n"
CV "v188,v120,v121\n"
MF "v[44:47],a[204:207],a[192:195],v[44:47]\n"
AW "a4,v188\n"
AW "a5,v189\n"
MF "v[44:47],a[200:203],a[152:155],v[44:47]\n"
AW "a6,v190\n"
AW "a7,v191\n"
MF "v[52:55],a[200:203],a[156:159],v[52:55]\n"
"v_fma_f32 v112,v112,s54,v14\n"
"v_fma_f32 v113,v113,s54,v14\n"
MF "v[52:55],a[204:207],a[196:199],v[52:55]\n"
"s_waitcnt lgkmcnt(0)\n"
"s_barrier\n"
MF "v[16:19],a[208:211],a[60:63],v[16:19]\n"
"v_fma_f32 v114,v114,s54,v14\n"
"v_fma_f32 v115,v115,s54,v14\n"
MF "v[16:19],a[212:215],a[100:103],v[16:19]\n"
"v_fma_f32 v116,v116,s54,v14\n"
"v_fma_f32 v117,v117,s54,v14\n"
MF "v[24:27],a[212:215],a[104:107],v[24:27]\n"
"v_fma_f32 v118,v118,s54,v14\n"
"v_fma_f32 v119,v119,s54,v14\n"
MF "v[24:27],a[208:211],a[64:67],v[24:27]\n"
CV "v187,v118,v119\n"
CV "v186,v116,v117\n"
MF "v[32:35],a[208:211],a[68:71],v[32:35]\n"
CV "v185,v114,v115\n"
CV "v184,v112,v113\n"
MF "v[32:35],a[212:215],a[108:111],v[32:35]\n"
AW "a0,v184\n"
AW "a1,v185\n"
MF "v[40:43],a[212:215],a[112:115],v[40:43]\n"
AW "a2,v186\n"
AW "a3,v187\n"
MF "v[40:43],a[208:211],a[72:75],v[40:43]\n"
"v_fma_f32 v104,v104,s54,v14\n"
"v_fma_f32 v105,v105,s54,v14\n"
MF "v[48:51],a[208:211],a[76:79],v[48:51]\n"
"v_fma_f32 v106,v106,s54,v14\n"
"v_fma_f32 v107,v107,s54,v14\n"
MF "v[48:51],a[212:215],a[116:119],v[48:51]\n"
"v_fma_f32 v108,v108,s54,v14\n"
"v_fma_f32 v109,v109,s54,v14\n"
MF "v[48:51],a[216:219],a[156:159],v[48:51]\n"
"v_fma_f32 v110,v110,s54,v14\n"
"v_fma_f32 v111,v111,s54,v14\n"
MF "v[48:51],a[220:223],a[196:199],v[48:51]\n"
CV "v183,v110,v111\n"
CV "v182,v108,v109\n"
MF "v[40:43],a[220:223],a[192:195],v[40:43]\n"
CV "v181,v106,v107\n"
CV "v180,v104,v105\n"
MF "v[40:43],a[216:219],a[152:155],v[40:43]\n"
AW "a36,v180\n"
AW "a37,v181\n"
MF "v[32:35],a[216:219],a[148:151],v[32:35]\n"
AW "a38,v182\n"
AW "a39,v183\n"
MF "v[32:35],a[220:223],a[188:191],v[32:35]\n"
MF "v[24:27],a[220:223],a[184:187],v[24:27]\n"
MF "v[24:27],a[216:219],a[144:147],v[24:27]\n"
MF "v[16:19],a[216:219],a[140:143],v[16:19]\n"
MF "v[16:19],a[220:223],a[180:183],v[16:19]\n"
"s_mov_b32 s57,1\n"
"s_mov_b32 s69,99\n"
AR "v0,a240\n"
AR "v2,a241\n"
AR "v4,a242\n"
AR "v6,a243\n"
"v_xor_b32 v1,64,v0\n"
"v_xor_b32 v3,64,v2\n"
"v_xor_b32_e64 v5,64,v4\n"
"v_add_u32 v11,97280,v6\n"
"v_add_u32 v12,194560,v6\n"
"v_add_u32 v13,291840,v6\n"
"s_memtime s[52:53]\n"
"s_memrealtime s[100:101]\n"
"s_waitcnt lgkmcnt(0)\n"
"s_sub_u32 s53,s52,s80\n"
"s_cmp_gt_u32 s53,27000\n"
"s_cselect_b32 s101,1,0\n"
"s_cmp_eq_u32 s87,2\n"
"s_cselect_b32 s87,s101,s87\n"
"s_cmp_eq_u32 s87,1\n"
"s_mov_b64 vcc,-1\n"
"s_waitcnt vmcnt(9) lgkmcnt(0)\n"
"s_barrier\n"
"s_nop 0\n"
TR "a[200:201],v0\n"
TR "a[202:203],v0 offset:512\n"
TR "a[204:205],v0 offset:4096\n"
TR "a[206:207],v0 offset:4608\n"
"ds_read_b128 a[60:63],v2\n"
"ds_read_b128 a[100:103],v3\n"
"ds_read_b128 a[64:67],v2 offset:2048\n"
"ds_read_b128 a[104:107],v3 offset:2048\n"
"ds_read_b128 a[68:71],v2 offset:4096\n"
"ds_read_b128 a[108:111],v3 offset:4096\n"
"ds_read_b128 a[72:75],v2 offset:6144\n"
"ds_read_b128 a[112:115],v3 offset:6144\n"
"ds_read_b128 a[76:79],v2 offset:8192\n"
"ds_read_b128 a[116:119],v3 offset:8192\n"
TR "a[208:209],v0 offset:8\n"
TR "a[210:211],v0 offset:520\n"
TR "a[212:213],v0 offset:4104\n"
TR "a[214:215],v0 offset:4616\n"
"v_pk_fma_f32 v[96:97],v[96:97],s[54:55],v[14:15] op_sel_hi:[1,1,0]\n"
"v_pk_fma_f32 v[98:99],v[98:99],s[54:55],v[14:15] op_sel_hi:[1,1,0]\n"
"v_pk_fma_f32 v[100:101],v[100:101],s[54:55],v[14:15] op_sel_hi:[1,1,0]\n"
"v_pk_fma_f32 v[102:103],v[102:103],s[54:55],v[14:15] op_sel_hi:[1,1,0]\n"
CV "v179,v102,v103\n"
CV "v178,v100,v101\n"
CV "v177,v98,v99\n"
CV "v176,v96,v97\n"
AW "a32,v176\n"
AW "a33,v177\n"
AW "a34,v178\n"
AW "a35,v179\n"
"v_pk_fma_f32 v[88:89],v[88:89],s[54:55],v[14:15] op_sel_hi:[1,1,0]\n"
"v_pk_fma_f32 v[90:91],v[90:91],s[54:55],v[14:15] op_sel_hi:[1,1,0]\n"
"v_pk_fma_f32 v[92:93],v[92:93],s[54:55],v[14:15] op_sel_hi:[1,1,0]\n"
"v_pk_fma_f32 v[94:95],v[94:95],s[54:55],v[14:15] op_sel_hi:[1,1,0]\n"
CV "v175,v94,v95\n"
CV "v174,v92,v93\n"
CV "v173,v90,v91\n"
CV "v172,v88,v89\n"
AW "a28,v172\n"
AW "a29,v173\n"
AW "a30,v174\n"
AW "a31,v175\n"
"v_pk_fma_f32 v[80:81],v[80:81],s[54:55],v[14:15] op_sel_hi:[1,1,0]\n"
"v_pk_fma_f32 v[82:83],v[82:83],s[54:55],v[14:15] op_sel_hi:[1,1,0]\n"
"v_pk_fma_f32 v[84:85],v[84:85],s[54:55],v[14:15] op_sel_hi:[1,1,0]\n"
"v_pk_fma_f32 v[86:87],v[86:87],s[54:55],v[14:15] op_sel_hi:[1,1,0]\n"
CV "v171,v86,v87\n"
CV "v170,v84,v85\n"
CV "v169,v82,v83\n"
CV "v168,v80,v81\n"
AW "a24,v168\n"
AW "a25,v169\n"
AW "a26,v170\n"
AW "a27,v171\n"
"v_pk_fma_f32 v[72:73],v[72:73],s[54:55],v[14:15] op_sel_hi:[1,1,0]\n"
"v_pk_fma_f32 v[74:75],v[74:75],s[54:55],v[14:15] op_sel_hi:[1,1,0]\n"
"v_pk_fma_f32 v[76:77],v[76:77],s[54:55],v[14:15] op_sel_hi:[1,1,0]\n"
"v_pk_fma_f32 v[78:79],v[78:79],s[54:55],v[14:15] op_sel_hi:[1,1,0]\n"
CV "v167,v78,v79\n"
CV "v166,v76,v77\n"
CV "v165,v74,v75\n"
CV "v164,v72,v73\n"
AW "a20,v164\n"
AW "a21,v165\n"
AW "a22,v166\n"
AW "a23,v167\n"
"v_pk_fma_f32 v[64:65],v[64:65],s[54:55],v[14:15] op_sel_hi:[1,1,0]\n"
"v_pk_fma_f32 v[66:67],v[66:67],s[54:55],v[14:15] op_sel_hi:[1,1,0]\n"
"v_pk_fma_f32 v[68:69],v[68:69],s[54:55],v[14:15] op_sel_hi:[1,1,0]\n"
"v_pk_fma_f32 v[70:71],v[70:71],s[54:55],v[14:15] op_sel_hi:[1,1,0]\n"
CV "v163,v70,v71\n"
CV "v162,v68,v69\n"
CV "v161,v66,v67\n"
CV "v160,v64,v65\n"
AW "a176,v160\n"
AW "a177,v161\n"
AW "a178,v162\n"
AW "a179,v163\n"
"v_pk_fma_f32 v[56:57],v[56:57],s[54:55],v[14:15] op_sel_hi:[1,1,0]\n"
"v_pk_fma_f32 v[58:59],v[58:59],s[54:55],v[14:15] op_sel_hi:[1,1,0]\n"
"v_pk_fma_f32 v[60:61],v[60:61],s[54:55],v[14:15] op_sel_hi:[1,1,0]\n"
"v_pk_fma_f32 v[62:63],v[62:63],s[54:55],v[14:15] op_sel_hi:[1,1,0]\n"
CV "v159,v62,v63\n"
CV "v158,v60,v61\n"
CV "v157,v58,v59\n"
CV "v156,v56,v57\n"
AW "a172,v156\n"
AW "a173,v157\n"
AW "a174,v158\n"
AW "a175,v159\n"
"v_pk_mul_f32 v[48:49],v[48:49],s[54:55]\n"
"v_pk_mul_f32 v[50:51],v[50:51],s[54:55]\n"
"v_pk_mul_f32 v[52:53],v[52:53],s[54:55]\n"
"v_pk_mul_f32 v[54:55],v[54:55],s[54:55]\n"
CV "v155,v54,v55\n"
CV "v154,v52,v53\n"
CV "v153,v50,v51\n"
CV "v152,v48,v49\n"
AW "a168,v152\n"
AW "a169,v153\n"
AW "a170,v154\n"
AW "a171,v155\n"
"v_pk_mul_f32 v[40:41],v[40:41],s[54:55]\n"
"v_pk_mul_f32 v[42:43],v[42:43],s[54:55]\n"
"v_pk_mul_f32 v[44:45],v[44:45],s[54:55]\n"
"v_pk_mul_f32 v[46:47],v[46:47],s[54:55]\n"
CV "v151,v46,v47\n"
CV "v150,v44,v45\n"
CV "v149,v42,v43\n"
CV "v148,v40,v41\n"
AW "a120,v148\n"
AW "a121,v149\n"
AW "a122,v150\n"
AW "a123,v151\n"
"v_pk_mul_f32 v[32:33],v[32:33],s[54:55]\n"
"v_pk_mul_f32 v[34:35],v[34:35],s[54:55]\n"
"v_pk_mul_f32 v[36:37],v[36:37],s[54:55]\n"
"v_pk_mul_f32 v[38:39],v[38:39],s[54:55]\n"
CV "v147,v38,v39\n"
CV "v146,v36,v37\n"
CV "v145,v34,v35\n"
CV "v144,v32,v33\n"
AW "a96,v144\n"
AW "a97,v145\n"
AW "a98,v146\n"
AW "a99,v147\n"
"v_pk_mul_f32 v[24:25],v[24:25],s[54:55]\n"
"v_pk_mul_f32 v[26:27],v[26:27],s[54:55]\n"
"v_pk_mul_f32 v[28:29],v[28:29],s[54:55]\n"
"v_pk_mul_f32 v[30:31],v[30:31],s[54:55]\n"
CV "v143,v30,v31\n"
CV "v142,v28,v29\n"
CV "v141,v26,v27\n"
CV "v140,v24,v25\n"
AW "a92,v140\n"
AW "a93,v141\n"
AW "a94,v142\n"
AW "a95,v143\n"
"v_pk_mul_f32 v[16:17],v[16:17],s[54:55]\n"
"v_pk_mul_f32 v[18:19],v[18:19],s[54:55]\n"
"v_pk_mul_f32 v[20:21],v[20:21],s[54:55]\n"
"v_pk_mul_f32 v[22:23],v[22:23],s[54:55]\n"
CV "v139,v22,v23\n"
CV "v138,v20,v21\n"
CV "v137,v18,v19\n"
CV "v136,v16,v17\n"
AW "a88,v136\n"
AW "a89,v137\n"
AW "a90,v138\n"
AW "a91,v139\n"
"s_waitcnt lgkmcnt(0)\n"
"s_branch .Lq0\n"
".Lpsl:\n"
"s_sleep 1\n"
".Lpsub:\n"
"s_memrealtime s[100:101]\n"
"s_waitcnt lgkmcnt(0)\n"
"s_sub_u32 s101,s21,s100\n"
"s_cmp_gt_i32 s101,0\n"
"s_cbranch_scc1 .Lpsl\n"
"s_add_u32 s21,s100,s97\n"
"s_setpc_b64 s[52:53]\n"
".Lstub_6:\n"
"v_cmp_le_f32 vcc,0x47800000,v10\n"
"s_cmp_eq_u32 s61,1\n"
"s_cbranch_scc0 .Lhook_6\n"
"s_sub_u32 s58,s71,s70\n"
"s_add_u32 s72,s72,s58\n"
"s_add_u32 s16,s16,s58\n"
"s_add_u32 s17,s17,s58\n"
"s_cmp_eq_u32 s57,0\n"
"s_cselect_b32 s34,0,s34\n"
"s_cselect_b32 s64,163840,s64\n"
".Lhook_6:\n"
"s_cmp_eq_u32 s77,0\n"
"s_cbranch_scc0 .Lnop_6\n"
"s_call_b64 s[52:53],.Lpsub\n"
".Lnop_6:\n"
"s_cbranch_vccz .Lback_6\n"
"s_call_b64 s[52:53],.Lslow_1\n"
"s_branch .Lback_6\n"
".Lstub_7:\n"
"v_cmp_le_f32 vcc,0x47800000,v10\n"
"s_cmp_eq_u32 s61,1\n"
"s_cbranch_scc0 .Lhook_7\n"
"s_sub_u32 s58,s71,s70\n"
"s_add_u32 s73,s73,s58\n"
"s_add_u32 s18,s18,s58\n"
"s_add_u32 s19,s19,s58\n"
"s_cmp_eq_u32 s57,0\n"
"s_cselect_b32 s38,0,s38\n"
"s_cselect_b32 s65,163840,s65\n"
".Lhook_7:\n"
"s_cmp_eq_u32 s77,0\n"
"s_cbranch_scc0 .Lnop_7\n"
"s_call_b64 s[52:53],.Lpsub\n"
"s_cmp_eq_u32 s61,4\n"
"s_cselect_b32 s77,0x47800000,s77\n"
"s_cselect_b32 s95,0x47800000,s95\n"
".Lnop_7:\n"
"s_cbranch_vccz .Lback_7\n"
"s_call_b64 s[52:53],.Lslow_0\n"
"s_branch .Lback_7\n"
".Lstub_8:\n"
"s_call_b64 s[52:53],.Lslow_1\n"
"s_branch .Lback_8\n"
".Lstub_11:\n"
"v_cmp_le_f32 vcc,0x47800000,v10\n"
"s_cmp_eq_u32 s77,0\n"
"s_cbranch_scc0 .Lnop_11\n"
"s_call_b64 s[52:53],.Lpsub\n"
".Lnop_11:\n"
"s_cbranch_vccz .Lback_11\n"
"s_call_b64 s[52:53],.Lslow_1\n"
"s_branch .Lback_11\n"
".Lstub_12:\n"
"v_cmp_le_f32 vcc,0x47800000,v10\n"
"s_cmp_eq_u32 s77,0\n"
"s_cbranch_scc0 .Lnop_12\n"
"s_call_b64 s[52:53],.Lpsub\n"
".Lnop_12:\n"
"s_cbranch_vccz .Lback_12\n"
"s_call_b64 s[52:53],.Lslow_0\n"
"s_branch .Lback_12\n"
".Lstub_13:\n"
"v_cmp_le_f32 vcc,0x47800000,v10\n"
"s_cmp_eq_u32 s61,1\n"
"s_cbranch_scc0 .Lhook_13\n"
"s_mov_b32 s97,0\n"
".Lhook_13:\n"
"s_cmp_eq_u32 s77,0\n"
"s_cbranch_scc0 .Lnop_13\n"
"s_call_b64 s[52:53],.Lpsub\n"
".Lnop_13:\n"
"s_cbranch_vccz .Lback_13\n"
"s_call_b64 s[52:53],.Lslow_1\n"
"s_branch .Lback_13\n"
".Lstub_14:\n"
"v_cmp_le_f32 vcc,0x47800000,v10\n"
"s_cmp_eq_u32 s77,0\n"
"s_cbranch_scc0 .Lnop_14\n"
"s_call_b64 s[52:53],.Lpsub\n"
".Lnop_14:\n"
"s_cbranch_vccz .Lback_14\n"
"s_call_b64 s[52:53],.Lslow_0\n"
"s_branch .Lback_14\n"
".Lstub_15:\n"
"v_cmp_le_f32 vcc,0x47800000,v10\n"
"s_cmp_eq_u32 s61,1\n"
"s_cbranch_scc0 .Lhook_15\n"
"s_mov_b32 s34,0\n"
"s_mov_b32 s64,163840\n"
".Lhook_15:\n"
"s_cmp_eq_u32 s77,0\n"
"s_cbranch_scc0 .Lnop_15\n"
"s_call_b64 s[52:53],.Lpsub\n"
".Lnop_15:\n"
"s_cbranch_vccz .Lback_15\n"
"s_call_b64 s[52:53],.Lslow_1\n"
"s_branch .Lback_15\n"
".Lstub_16:\n"
"v_cmp_le_f32 vcc,0x47800000,v10\n"
"s_cmp_eq_u32 s77,0\n"
"s_cbranch_scc0 .Lnop_16\n"
"s_call_b64 s[52:53],.Lpsub\n"
".Lnop_16:\n"
"s_cbranch_vccz .Lback_16\n"
"s_call_b64 s[52:53],.Lslow_0\n"
"s_branch .Lback_16\n"
".Lstub_17:\n"
"s_call_b64 s[52:53],.Lslow_1\n"
"s_branch .Lback_17\n"
".Lslow_0:\n"
"s_nop 7\n"
"s_nop 7\n"
"v_and_b32 v10,240,v9\n"
"v_mul_u32_u24 v10,24,v10\n"
"v_lshrrev_b32 v11,4,v9\n"
"v_and_or_b32 v10,v11,48,v10\n"
"s_mov_b32 s59,s34\n"
"s_mov_b32 s34,s20\n"
"s_add_u32 s58,s74,0\n"
"s_nop 3\n"
LD "v[52:55],v10,s[32:35],s58 offen\n"
"s_waitcnt vmcnt(0)\n"
MF "v[72:75],v[52:55],v[136:139],v[32:35]\n"
MF "v[76:79],v[52:55],v[140:143],v[36:39]\n"
MF "v[80:83],v[52:55],v[144:147],v[40:43]\n"
MF "v[84:87],v[52:55],v[148:151],v[44:47]\n"
MF "v[88:91],v[52:55],v[152:155],v[48:51]\n"
"s_add_u32 s58,s74,0\n"
"s_nop 3\n"
LD "v[52:55],v10,s[32:35],s58 offen offset:64\n"
"s_waitcnt vmcnt(0)\n"
MF "v[72:75],v[52:55],v[156:159],v[72:75]\n"
MF "v[76:79],v[52:55],v[160:163],v[76:79]\n"
MF "v[80:83],v[52:55],v[164:167],v[80:83]\n"
MF "v[84:87],v[52:55],v[168:171],v[84:87]\n"
MF "v[88:91],v[52:55],v[172:175],v[88:91]\n"
"s_add_u32 s58,s74,0\n"
"s_nop 3\n"
LD "v[52:55],v10,s[32:35],s58 offen offset:128\n"
"s_waitcnt vmcnt(0)\n"
MF "v[72:75],v[52:55],v[176:179],v[72:75]\n"
MF "v[76:79],v[52:55],v[180:183],v[76:79]\n"
MF "v[80:83],v[52:55],v[184:187],v[80:83]\n"
MF "v[84:87],v[52:55],v[188:191],v[84:87]\n"
MF "v[88:91],v[52:55],v[192:195],v[88:91]\n"
"s_add_u32 s58,s74,0\n"
"s_nop 3\n"
LD "v[52:55],v10,s[32:35],s58 offen offset:192\n"
"s_waitcnt vmcnt(0)\n"
MF "v[72:75],v[52:55],v[196:199],v[72:75]\n"
MF "v[76:79],v[52:55],v[200:203],v[76:79]\n"
MF "v[80:83],v[52:55],v[204:207],v[80:83]\n"
MF "v[84:87],v[52:55],v[208:211],v[84:87]\n"
MF "v[88:91],v[52:55],v[212:215],v[88:91]\n"
"s_add_u32 s58,s74,0\n"
"s_nop 3\n"
LD "v[52:55],v10,s[32:35],s58 offen offset:256\n"
"s_waitcnt vmcnt(0)\n"
MF "v[72:75],v[52:55],v[216:219],v[72:75]\n"
MF "v[76:79],v[52:55],v[220:223],v[76:79]\n"
MF "v[80:83],v[52:55],v[224:227],v[80:83]\n"
MF "v[84:87],v[52:55],v[228:231],v[84:87]\n"
MF "v[88:91],v[52:55],v[232:235],v[88:91]\n"
"s_add_u32 s58,s74,0\n"
"s_nop 3\n"
LD "v[52:55],v10,s[32:35],s58 offen offset:320\n"
"s_waitcnt vmcnt(0)\n"
MF "v[72:75],v[52:55],v[236:239],v[72:75]\n"
MF "v[76:79],v[52:55],v[240:243],v[76:79]\n"
MF "v[80:83],v[52:55],v[244:247],v[80:83]\n"
MF "v[84:87],v[52:55],v[248:251],v[84:87]\n"
MF "v[88:91],v[52:55],v[252:255],v[88:91]\n"
"s_add_u32 s58,s74,6144\n"
"s_nop 3\n"
LD "v[52:55],v10,s[32:35],s58 offen\n"
"s_waitcnt vmcnt(0)\n"
MF "v[112:115],v[52:55],v[136:139],v[32:35]\n"
MF "v[116:119],v[52:55],v[140:143],v[36:39]\n"
MF "v[120:123],v[52:55],v[144:147],v[40:43]\n"
MF "v[124:127],v[52:55],v[148:151],v[44:47]\n"
MF "v[128:131],v[52:55],v[152:155],v[48:51]\n"
"s_add_u32 s58,s74,6144\n"
"s_nop 3\n"
LD "v[52:55],v10,s[32:35],s58 offen offset:64\n"
"s_waitcnt vmcnt(0)\n"
MF "v[112:115],v[52:55],v[156:159],v[112:115]\n"
MF "v[116:119],v[52:55],v[160:163],v[116:119]\n"
MF "v[120:123],v[52:55],v[164:167],v[120:123]\n"
MF "v[124:127],v[52:55],v[168:171],v[124:127]\n"
MF "v[128:131],v[52:55],v[172:175],v[128:131]\n"
"s_add_u32 s58,s74,6144\n"
"s_nop 3\n"
LD "v[52:55],v10,s[32:35],s58 offen offset:128\n"
"s_waitcnt vmcnt(0)\n"
MF "v[112:115],v[52:55],v[176:179],v[112:115]\n"
MF "v[116:119],v[52:55],v[180:183],v[116:119]\n"
MF "v[120:123],v[52:55],v[184:187],v[120:123]\n"
MF "v[124:127],v[52:55],v[188:191],v[124:127]\n"
MF "v[128:131],v[52:55],v[192:195],v[128:131]\n"
"s_add_u32 s58,s74,6144\n"
"s_nop 3\n"
LD "v[52:55],v10,s[32:35],s58 offen offset:192\n"
"s_waitcnt vmcnt(0)\n"
MF "v[112:115],v[52:55],v[196:199],v[112:115]\n"
MF "v[116:119],v[52:55],v[200:203],v[116:119]\n"
MF "v[120:123],v[52:55],v[204:207],v[120:123]\n"
MF "v[124:127],v[52:55],v[208:211],v[124:127]\n"
MF "v[128:131],v[52:55],v[212:215],v[128:131]\n"
"s_add_u32 s58,s74,6144\n"
"s_nop 3\n"
LD "v[52:55],v10,s[32:35],s58 offen offset:256\n"
"s_waitcnt vmcnt(0)\n"
MF "v[112:115],v[52:55],v[216:219],v[112:115]\n"
MF "v[116:119],v[52:55],v[220:223],v[116:119]\n"
MF "v[120:123],v[52:55],v[224:227],v[120:123]\n"
MF "v[124:127],v[52:55],v[228:231],v[124:127]\n"
MF "v[128:131],v[52:55],v[232:235],v[128:131]\n"
"s_add_u32 s58,s74,6144\n"
"s_nop 3\n"
LD "v[52:55],v10,s[32:35],s58 offen offset:320\n"
"s_waitcnt vmcnt(0)\n"
MF "v[112:115],v[52:55],v[236:239],v[112:115]\n"
MF "v[116:119],v[52:55],v[240:243],v[116:119]\n"
MF "v[120:123],v[52:55],v[244:247],v[120:123]\n"
MF "v[124:127],v[52:55],v[248:251],v[124:127]\n"
MF "v[128:131],v[52:55],v[252:255],v[128:131]\n"
"s_nop 7\n"
"s_mov_b32 s34,s59\n"
"v_max3_f32 v10,v72,v73,v74\n"
"s_nop 0\n"
"v_max3_f32 v11,v75,v112,v113\n"
"v_max3_f32 v10,v10,v114,v115\n"
"v_max_f32 v10,v10,v11\n"
"v_mov_b32 v11,v10\n"
"s_nop 1\n"
"v_permlane16_swap_b32 v10,v11\n"
"v_max_f32 v10,v10,v11\n"
"v_mov_b32 v11,v10\n"
"s_nop 1\n"
"v_permlane32_swap_b32 v10,v11\n"
"v_max_f32 v10,v10,v11\n"
"v_add_f32 v10,0x41300000,v10\n"
"v_max_f32_e64 v10,0,v10\n"
"v_exp_f32 v11,-v10\n"
AR "v12,a0\n"
"v_subrev_f32 v12,s13,v12\n"
"v_fma_f32 v12,v12,v11,s13\n"
AW "a0,v12\n"
AR "v12,a20\n"
"v_subrev_f32 v12,s13,v12\n"
"v_fma_f32 v12,v12,v11,s13\n"
AW "a20,v12\n"
AR "v12,a40\n"
"v_subrev_f32 v12,s13,v12\n"
"v_fma_f32 v12,v12,v11,s13\n"
AW "a40,v12\n"
AR "v12,a60\n"
"v_subrev_f32 v12,s13,v12\n"
"v_fma_f32 v12,v12,v11,s13\n"
AW "a60,v12\n"
AR "v12,a80\n"
"v_subrev_f32 v12,s13,v12\n"
"v_fma_f32 v12,v12,v11,s13\n"
AW "a80,v12\n"
AR "v12,a100\n"
"v_subrev_f32 v12,s13,v12\n"
"v_fma_f32 v12,v12,v11,s13\n"
AW "a100,v12\n"
AR "v12,a120\n"
"v_subrev_f32 v12,s13,v12\n"
"v_fma_f32 v12,v12,v11,s13\n"
AW "a120,v12\n"
AR "v12,a140\n"
"v_subrev_f32 v12,s13,v12\n"
"v_fma_f32 v12,v12,v11,s13\n"
AW "a140,v12\n"
AR "v12,a160\n"
"v_subrev_f32 v12,s13,v12\n"
"v_fma_f32 v12,v12,v11,s13\n"
AW "a160,v12\n"
AR "v12,a180\n"
"v_subrev_f32 v12,s13,v12\n"
"v_fma_f32 v12,v12,v11,s13\n"
AW "a180,v12\n"
AR "v12,a200\n"
"v_subrev_f32 v12,s13,v12\n"
"v_fma_f32 v12,v12,v11,s13\n"
AW "a200,v12\n"
AR "v12,a220\n"
"v_subrev_f32 v12,s13,v12\n"
"v_fma_f32 v12,v12,v11,s13\n"
AW "a220,v12\n"
AR "v12,a1\n"
"v_subrev_f32 v12,s13,v12\n"
"v_fma_f32 v12,v12,v11,s13\n"
AW "a1,v12\n"
AR "v12,a21\n"
"v_subrev_f32 v12,s13,v12\n"
"v_fma_f32 v12,v12,v11,s13\n"
AW "a21,v12\n"
AR "v12,a41\n"
"v_subrev_f32 v12,s13,v12\n"
"v_fma_f32 v12,v12,v11,s13\n"
AW "a41,v12\n"
AR "v12,a61\n"
"v_subrev_f32 v12,s13,v12\n"
"v_fma_f32 v12,v12,v11,s13\n"
AW "a61,v12\n"
AR "v12,a81\n"
"v_subrev_f32 v12,s13,v12\n"
"v_fma_f32 v12,v12,v11,s13\n"
AW "a81,v12\n"
AR "v12,a101\n"
"v_subrev_f32 v12,s13,v12\n"
"v_fma_f32 v12,v12,v11,s13\n"
AW "a101,v12\n"
AR "v12,a121\n"
"v_subrev_f32 v12,s13,v12\n"
"v_fma_f32 v12,v12,v11,s13\n"
AW "a121,v12\n"
AR "v12,a141\n"
"v_subrev_f32 v12,s13,v12\n"
"v_fma_f32 v12,v12,v11,s13\n"
AW "a141,v12\n"
AR "v12,a161\n"
"v_subrev_f32 v12,s13,v12\n"
"v_fma_f32 v12,v12,v11,s13\n"
AW "a161,v12\n"
AR "v12,a181\n"
"v_subrev_f32 v12,s13,v12\n"
"v_fma_f32 v12,v12,v11,s13\n"
AW "a181,v12\n"
AR "v12,a201\n"
"v_subrev_f32 v12,s13,v12\n"
"v_fma_f32 v12,v12,v11,s13\n"
AW "a201,v12\n"
AR "v12,a221\n"
"v_subrev_f32 v12,s13,v12\n"
"v_fma_f32 v12,v12,v11,s13\n"
AW "a221,v12\n"
AR "v12,a2\n"
"v_subrev_f32 v12,s13,v12\n"
"v_fma_f32 v12,v12,v11,s13\n"
AW "a2,v12\n"
AR "v12,a22\n"
"v_subrev_f32 v12,s13,v12\n"
"v_fma_f32 v12,v12,v11,s13\n"
AW "a22,v12\n"
AR "v12,a42\n"
"v_subrev_f32 v12,s13,v12\n"
"v_fma_f32 v12,v12,v11,s13\n"
AW "a42,v12\n"
AR "v12,a62\n"
"v_subrev_f32 v12,s13,v12\n"
"v_fma_f32 v12,v12,v11,s13\n"
AW "a62,v12\n"
AR "v12,a82\n"
"v_subrev_f32 v12,s13,v12\n"
"v_fma_f32 v12,v12,v11,s13\n"
AW "a82,v12\n"
AR "v12,a102\n"
"v_subrev_f32 v12,s13,v12\n"
"v_fma_f32 v12,v12,v11,s13\n"
AW "a102,v12\n"
AR "v12,a122\n"
"v_subrev_f32 v12,s13,v12\n"
"v_fma_f32 v12,v12,v11,s13\n"
AW "a122,v12\n"
AR "v12,a142\n"
"v_subrev_f32 v12,s13,v12\n"
"v_fma_f32 v12,v12,v11,s13\n"
AW "a142,v12\n"
AR "v12,a162\n"
"v_subrev_f32 v12,s13,v12\n"
"v_fma_f32 v12,v12,v11,s13\n"
AW "a162,v12\n"
AR "v12,a182\n"
"v_subrev_f32 v12,s13,v12\n"
"v_fma_f32 v12,v12,v11,s13\n"
AW "a182,v12\n"
AR "v12,a202\n"
"v_subrev_f32 v12,s13,v12\n"
"v_fma_f32 v12,v12,v11,s13\n"
AW "a202,v12\n"
AR "v12,a222\n"
"v_subrev_f32 v12,s13,v12\n"
"v_fma_f32 v12,v12,v11,s13\n"
AW "a222,v12\n"
AR "v12,a3\n"
"v_subrev_f32 v12,s13,v12\n"
"v_fma_f32 v12,v12,v11,s13\n"
AW "a3,v12\n"
AR "v12,a23\n"
"v_subrev_f32 v12,s13,v12\n"
"v_fma_f32 v12,v12,v11,s13\n"
AW "a23,v12\n"
AR "v12,a43\n"
"v_subrev_f32 v12,s13,v12\n"
"v_fma_f32 v12,v12,v11,s13\n"
AW "a43,v12\n"
AR "v12,a63\n"
"v_subrev_f32 v12,s13,v12\n"
"v_fma_f32 v12,v12,v11,s13\n"
AW "a63,v12\n"
AR "v12,a83\n"
"v_subrev_f32 v12,s13,v12\n"
"v_fma_f32 v12,v12,v11,s13\n"
AW "a83,v12\n"
AR "v12,a103\n"
"v_subrev_f32 v12,s13,v12\n"
"v_fma_f32 v12,v12,v11,s13\n"
AW "a103,v12\n"
AR "v12,a123\n"
"v_subrev_f32 v12,s13,v12\n"
"v_fma_f32 v12,v12,v11,s13\n"
AW "a123,v12\n"
AR "v12,a143\n"
"v_subrev_f32 v12,s13,v12\n"
"v_fma_f32 v12,v12,v11,s13\n"
AW "a143,v12\n"
AR "v12,a163\n"
"v_subrev_f32 v12,s13,v12\n"
"v_fma_f32 v12,v12,v11,s13\n"
AW "a163,v12\n"
AR "v12,a183\n"
"v_subrev_f32 v12,s13,v12\n"
"v_fma_f32 v12,v12,v11,s13\n"
AW "a183,v12\n"
AR "v12,a203\n"
"v_subrev_f32 v12,s13,v12\n"
"v_fma_f32 v12,v12,v11,s13\n"
AW "a203,v12\n"
AR "v12,a223\n"
"v_subrev_f32 v12,s13,v12\n"
"v_fma_f32 v12,v12,v11,s13\n"
AW "a223,v12\n"
"v_mul_f32 v52,v57,v11\n"
"v_sub_f32 v32,v32,v10\n"
"v_sub_f32 v33,v33,v10\n"
"v_sub_f32 v34,v34,v10\n"
"v_sub_f32 v35,v35,v10\n"
"v_sub_f32 v92,v92,v10\n"
"v_sub_f32 v93,v93,v10\n"
"v_sub_f32 v94,v94,v10\n"
"v_sub_f32 v95,v95,v10\n"
"v_sub_f32 v72,v72,v10\n"
"v_sub_f32 v73,v73,v10\n"
"v_sub_f32 v74,v74,v10\n"
"v_sub_f32 v75,v75,v10\n"
"v_sub_f32 v112,v112,v10\n"
"v_sub_f32 v113,v113,v10\n"
"v_sub_f32 v114,v114,v10\n"
"v_sub_f32 v115,v115,v10\n"
"v_exp_f32 v72,v72\n"
"v_exp_f32 v73,v73\n"
"v_exp_f32 v74,v74\n"
"v_exp_f32 v75,v75\n"
"v_exp_f32 v112,v112\n"
"v_exp_f32 v113,v113\n"
"v_exp_f32 v114,v114\n"
"v_exp_f32 v115,v115\n"
"v_add_f32 v52,v52,v72\n"
"v_add_f32 v52,v52,v73\n"
"v_add_f32 v52,v52,v74\n"
"v_add_f32 v52,v52,v75\n"
"v_add_f32 v52,v52,v112\n"
"v_add_f32 v52,v52,v113\n"
"v_add_f32 v52,v52,v114\n"
"v_add_f32_e64 v52,v52,v115\n"
CV "v72,v72,v73\n"
CV "v73,v74,v75\n"
CV "v74,v112,v113\n"
CV "v75,v114,v115\n"
"v_max3_f32 v10,v76,v77,v78\n"
"v_max3_f32 v11,v79,v116,v117\n"
"v_max3_f32 v10,v10,v118,v119\n"
"v_max_f32 v10,v10,v11\n"
"v_mov_b32 v11,v10\n"
"s_nop 1\n"
"v_permlane16_swap_b32 v10,v11\n"
"v_max_f32 v10,v10,v11\n"
"v_mov_b32 v11,v10\n"
"s_nop 1\n"
"v_permlane32_swap_b32 v10,v11\n"
"v_max_f32 v10,v10,v11\n"
"v_add_f32 v10,0x41300000,v10\n"
"v_max_f32 v10,0,v10\n"
"v_exp_f32 v11,-v10\n"
AR "v12,a4\n"
"v_subrev_f32 v12,s13,v12\n"
"v_fma_f32 v12,v12,v11,s13\n"
AW "a4,v12\n"
AR "v12,a24\n"
"v_subrev_f32 v12,s13,v12\n"
"v_fma_f32 v12,v12,v11,s13\n"
AW "a24,v12\n"
AR "v12,a44\n"
"v_subrev_f32 v12,s13,v12\n"
"v_fma_f32 v12,v12,v11,s13\n"
AW "a44,v12\n"
AR "v12,a64\n"
"v_subrev_f32 v12,s13,v12\n"
"v_fma_f32 v12,v12,v11,s13\n"
AW "a64,v12\n"
AR "v12,a84\n"
"v_subrev_f32 v12,s13,v12\n"
"v_fma_f32 v12,v12,v11,s13\n"
AW "a84,v12\n"
AR "v12,a104\n"
"v_subrev_f32 v12,s13,v12\n"
"v_fma_f32 v12,v12,v11,s13\n"
AW "a104,v12\n"
AR "v12,a124\n"
"v_subrev_f32 v12,s13,v12\n"
"v_fma_f32 v12,v12,v11,s13\n"
AW "a124,v12\n"
AR "v12,a144\n"
"v_subrev_f32 v12,s13,v12\n"
"v_fma_f32 v12,v12,v11,s13\n"
AW "a144,v12\n"
AR "v12,a164\n"
"v_subrev_f32 v12,s13,v12\n"
"v_fma_f32 v12,v12,v11,s13\n"
AW "a164,v12\n"
AR "v12,a184\n"
"v_subrev_f32 v12,s13,v12\n"
"v_fma_f32 v12,v12,v11,s13\n"
AW "a184,v12\n"
AR "v12,a204\n"
"v_subrev_f32 v12,s13,v12\n"
"v_fma_f32 v12,v12,v11,s13\n"
AW "a204,v12\n"
AR "v12,a224\n"
"v_subrev_f32 v12,s13,v12\n"
"v_fma_f32 v12,v12,v11,s13\n"
AW "a224,v12\n"
AR "v12,a5\n"
"v_subrev_f32 v12,s13,v12\n"
"v_fma_f32 v12,v12,v11,s13\n"
AW "a5,v12\n"
AR "v12,a25\n"
"v_subrev_f32 v12,s13,v12\n"
"v_fma_f32 v12,v12,v11,s13\n"
AW "a25,v12\n"
AR "v12,a45\n"
"v_subrev_f32 v12,s13,v12\n"
"v_fma_f32 v12,v12,v11,s13\n"
AW "a45,v12\n"
AR "v12,a65\n"
"v_subrev_f32 v12,s13,v12\n"
"v_fma_f32 v12,v12,v11,s13\n"
AW "a65,v12\n"
AR "v12,a85\n"
"v_subrev_f32 v12,s13,v12\n"
"v_fma_f32 v12,v12,v11,s13\n"
AW "a85,v12\n"
AR "v12,a105\n"
"v_subrev_f32 v12,s13,v12\n"
"v_fma_f32 v12,v12,v11,s13\n"
AW "a105,v12\n"
AR "v12,a125\n"
"v_subrev_f32 v12,s13,v12\n"
"v_fma_f32 v12,v12,v11,s13\n"
AW "a125,v12\n"
AR "v12,a145\n"
"v_subrev_f32 v12,s13,v12\n"
"v_fma_f32 v12,v12,v11,s13\n"
AW "a145,v12\n"
AR "v12,a165\n"
"v_subrev_f32 v12,s13,v12\n"
"v_fma_f32 v12,v12,v11,s13\n"
AW "a165,v12\n"
AR "v12,a185\n"
"v_subrev_f32 v12,s13,v12\n"
"v_fma_f32 v12,v12,v11,s13\n"
AW "a185,v12\n"
AR "v12,a205\n"
"v_subrev_f32 v12,s13,v12\n"
"v_fma_f32 v12,v12,v11,s13\n"
AW "a205,v12\n"
AR "v12,a225\n"
"v_subrev_f32 v12,s13,v12\n"
"v_fma_f32 v12,v12,v11,s13\n"
AW "a225,v12\n"
AR "v12,a6\n"
"v_subrev_f32 v12,s13,v12\n"
"v_fma_f32 v12,v12,v11,s13\n"
AW "a6,v12\n"
AR "v12,a26\n"
"v_subrev_f32 v12,s13,v12\n"
"v_fma_f32 v12,v12,v11,s13\n"
AW "a26,v12\n"
AR "v12,a46\n"
"v_subrev_f32 v12,s13,v12\n"
"v_fma_f32 v12,v12,v11,s13\n"
AW "a46,v12\n"
AR "v12,a66\n"
"v_subrev_f32 v12,s13,v12\n"
"v_fma_f32 v12,v12,v11,s13\n"
AW "a66,v12\n"
AR "v12,a86\n"
"v_subrev_f32 v12,s13,v12\n"
"v_fma_f32 v12,v12,v11,s13\n"
AW "a86,v12\n"
AR "v12,a106\n"
"v_subrev_f32 v12,s13,v12\n"
"v_fma_f32 v12,v12,v11,s13\n"
AW "a106,v12\n"
AR "v12,a126\n"
"v_subrev_f32 v12,s13,v12\n"
"v_fma_f32 v12,v12,v11,s13\n"
AW "a126,v12\n"
AR "v12,a146\n"
"v_subrev_f32 v12,s13,v12\n"
"v_fma_f32 v12,v12,v11,s13\n"
AW "a146,v12\n"
AR "v12,a166\n"
"v_subrev_f32 v12,s13,v12\n"
"v_fma_f32 v12,v12,v11,s13\n"
AW "a166,v12\n"
AR "v12,a186\n"
"v_subrev_f32 v12,s13,v12\n"
"v_fma_f32 v12,v12,v11,s13\n"
AW "a186,v12\n"
AR "v12,a206\n"
"v_subrev_f32 v12,s13,v12\n"
"v_fma_f32 v12,v12,v11,s13\n"
AW "a206,v12\n"
AR "v12,a226\n"
"v_subrev_f32 v12,s13,v12\n"
"v_fma_f32 v12,v12,v11,s13\n"
AW "a226,v12\n"
AR "v12,a7\n"
"v_subrev_f32 v12,s13,v12\n"
"v_fma_f32 v12,v12,v11,s13\n"
AW "a7,v12\n"
AR "v12,a27\n"
"v_subrev_f32 v12,s13,v12\n"
"v_fma_f32 v12,v12,v11,s13\n"
AW "a27,v12\n"
AR "v12,a47\n"
"v_subrev_f32 v12,s13,v12\n"
"v_fma_f32 v12,v12,v11,s13\n"
AW "a47,v12\n"
AR "v12,a67\n"
"v_subrev_f32 v12,s13,v12\n"
"v_fma_f32 v12,v12,v11,s13\n"
AW "a67,v12\n"
AR "v12,a87\n"
"v_subrev_f32 v12,s13,v12\n"
"v_fma_f32 v12,v12,v11,s13\n"
AW "a87,v12\n"
AR "v12,a107\n"
"v_subrev_f32 v12,s13,v12\n"
"v_fma_f32 v12,v12,v11,s13\n"
AW "a107,v12\n"
AR "v12,a127\n"
"v_subrev_f32 v12,s13,v12\n"
"v_fma_f32 v12,v12,v11,s13\n"
AW "a127,v12\n"
AR "v12,a147\n"
"v_subrev_f32 v12,s13,v12\n"
"v_fma_f32 v12,v12,v11,s13\n"
AW "a147,v12\n"
AR "v12,a167\n"
"v_subrev_f32 v12,s13,v12\n"
"v_fma_f32 v12,v12,v11,s13\n"
AW "a167,v12\n"
AR "v12,a187\n"
"v_subrev_f32 v12,s13,v12\n"
"v_fma_f32 v12,v12,v11,s13\n"
AW "a187,v12\n"
AR "v12,a207\n"
"v_subrev_f32 v12,s13,v12\n"
"v_fma_f32 v12,v12,v11,s13\n"
AW "a207,v12\n"
AR "v12,a227\n"
"v_subrev_f32 v12,s13,v12\n"
"v_fma_f32 v12,v12,v11,s13\n"
AW "a227,v12\n"
"v_mul_f32 v53,v58,v11\n"
"v_sub_f32 v36,v36,v10\n"
"v_sub_f32 v37,v37,v10\n"
"v_sub_f32 v38,v38,v10\n"
"v_sub_f32 v39,v39,v10\n"
"v_sub_f32 v96,v96,v10\n"
"v_sub_f32 v97,v97,v10\n"
"v_sub_f32 v98,v98,v10\n"
"v_sub_f32 v99,v99,v10\n"
"v_sub_f32 v76,v76,v10\n"
"v_sub_f32 v77,v77,v10\n"
"v_sub_f32 v78,v78,v10\n"
"v_sub_f32 v79,v79,v10\n"
"v_sub_f32 v116,v116,v10\n"
"v_sub_f32 v117,v117,v10\n"
"v_sub_f32 v118,v118,v10\n"
"v_sub_f32 v119,v119,v10\n"
"v_exp_f32 v76,v76\n"
"v_exp_f32 v77,v77\n"
"v_exp_f32 v78,v78\n"
"v_exp_f32 v79,v79\n"
"v_exp_f32 v116,v116\n"
"v_exp_f32 v117,v117\n"
"v_exp_f32 v118,v118\n"
"v_exp_f32 v119,v119\n"
"v_add_f32 v53,v53,v76\n"
"v_add_f32 v53,v53,v77\n"
"v_add_f32 v53,v53,v78\n"
"v_add_f32 v53,v53,v79\n"
"v_add_f32 v53,v53,v116\n"
"v_add_f32 v53,v53,v117\n"
"v_add_f32 v53,v53,v118\n"
"v_add_f32_e64 v53,v53,v119\n"
CV "v76,v76,v77\n"
CV "v77,v78,v79\n"
CV "v78,v116,v117\n"
CV "v79,v118,v119\n"
"v_max3_f32 v10,v80,v81,v82\n"
"v_max3_f32 v11,v83,v120,v121\n"
"v_max3_f32 v10,v10,v122,v123\n"
"v_max_f32 v10,v10,v11\n"
"v_mov_b32 v11,v10\n"
"s_nop 1\n"
"v_permlane16_swap_b32 v10,v11\n"
"v_max_f32 v10,v10,v11\n"
"v_mov_b32 v11,v10\n"
"s_nop 1\n"
"v_permlane32_swap_b32 v10,v11\n"
"v_max_f32 v10,v10,v11\n"
"v_add_f32 v10,0x41300000,v10\n"
"v_max_f32 v10,0,v10\n"
"v_exp_f32 v11,-v10\n"
AR "v12,a8\n"
"v_subrev_f32 v12,s13,v12\n"
"v_fma_f32 v12,v12,v11,s13\n"
AW "a8,v12\n"
AR "v12,a28\n"
"v_subrev_f32 v12,s13,v12\n"
"v_fma_f32 v12,v12,v11,s13\n"
AW "a28,v12\n"
AR "v12,a48\n"
"v_subrev_f32 v12,s13,v12\n"
"v_fma_f32 v12,v12,v11,s13\n"
AW "a48,v12\n"
AR "v12,a68\n"
"v_subrev_f32 v12,s13,v12\n"
"v_fma_f32 v12,v12,v11,s13\n"
AW "a68,v12\n"
AR "v12,a88\n"
"v_subrev_f32 v12,s13,v12\n"
"v_fma_f32 v12,v12,v11,s13\n"
AW "a88,v12\n"
AR "v12,a108\n"
"v_subrev_f32 v12,s13,v12\n"
"v_fma_f32 v12,v12,v11,s13\n"
AW "a108,v12\n"
AR "v12,a128\n"
"v_subrev_f32 v12,s13,v12\n"
"v_fma_f32 v12,v12,v11,s13\n"
AW "a128,v12\n"
AR "v12,a148\n"
"v_subrev_f32 v12,s13,v12\n"
"v_fma_f32 v12,v12,v11,s13\n"
AW "a148,v12\n"
AR "v12,a168\n"
"v_subrev_f32 v12,s13,v12\n"
"v_fma_f32 v12,v12,v11,s13\n"
AW "a168,v12\n"
AR "v12,a188\n"
"v_subrev_f32 v12,s13,v12\n"
"v_fma_f32 v12,v12,v11,s13\n"
AW "a188,v12\n"
AR "v12,a208\n"
"v_subrev_f32 v12,s13,v12\n"
"v_fma_f32 v12,v12,v11,s13\n"
AW "a208,v12\n"
AR "v12,a228\n"
"v_subrev_f32 v12,s13,v12\n"
"v_fma_f32 v12,v12,v11,s13\n"
AW "a228,v12\n"
AR "v12,a9\n"
"v_subrev_f32 v12,s13,v12\n"
"v_fma_f32 v12,v12,v11,s13\n"
AW "a9,v12\n"
AR "v12,a29\n"
"v_subrev_f32 v12,s13,v12\n"
"v_fma_f32 v12,v12,v11,s13\n"
AW "a29,v12\n"
AR "v12,a49\n"
"v_subrev_f32 v12,s13,v12\n"
"v_fma_f32 v12,v12,v11,s13\n"
AW "a49,v12\n"
AR "v12,a69\n"
"v_subrev_f32 v12,s13,v12\n"
"v_fma_f32 v12,v12,v11,s13\n"
AW "a69,v12\n"
AR "v12,a89\n"
"v_subrev_f32 v12,s13,v12\n"
"v_fma_f32 v12,v12,v11,s13\n"
AW "a89,v12\n"
AR "v12,a109\n"
"v_subrev_f32 v12,s13,v12\n"
"v_fma_f32 v12,v12,v11,s13\n"
AW "a109,v12\n"
AR "v12,a129\n"
"v_subrev_f32 v12,s13,v12\n"
"v_fma_f32 v12,v12,v11,s13\n"
AW "a129,v12\n"
AR "v12,a149\n"
"v_subrev_f32 v12,s13,v12\n"
"v_fma_f32 v12,v12,v11,s13\n"
AW "a149,v12\n"
AR "v12,a169\n"
"v_subrev_f32 v12,s13,v12\n"
"v_fma_f32 v12,v12,v11,s13\n"
AW "a169,v12\n"
AR "v12,a189\n"
"v_subrev_f32 v12,s13,v12\n"
"v_fma_f32 v12,v12,v11,s13\n"
AW "a189,v12\n"
AR "v12,a209\n"
"v_subrev_f32 v12,s13,v12\n"
"v_fma_f32 v12,v12,v11,s13\n"
AW "a209,v12\n"
AR "v12,a229\n"
"v_subrev_f32 v12,s13,v12\n"
"v_fma_f32 v12,v12,v11,s13\n"
AW "a229,v12\n"
AR "v12,a10\n"
"v_subrev_f32 v12,s13,v12\n"
"v_fma_f32 v12,v12,v11,s13\n"
AW "a10,v12\n"
AR "v12,a30\n"
"v_subrev_f32 v12,s13,v12\n"
"v_fma_f32 v12,v12,v11,s13\n"
AW "a30,v12\n"
AR "v12,a50\n"
"v_subrev_f32 v12,s13,v12\n"
"v_fma_f32 v12,v12,v11,s13\n"
AW "a50,v12\n"
AR "v12,a70\n"
"v_subrev_f32 v12,s13,v12\n"
"v_fma_f32 v12,v12,v11,s13\n"
AW "a70,v12\n"
AR "v12,a90\n"
"v_subrev_f32 v12,s13,v12\n"
"v_fma_f32 v12,v12,v11,s13\n"
AW "a90,v12\n"
AR "v12,a110\n"
"v_subrev_f32 v12,s13,v12\n"
"v_fma_f32 v12,v12,v11,s13\n"
AW "a110,v12\n"
AR "v12,a130\n"
"v_subrev_f32 v12,s13,v12\n"
"v_fma_f32 v12,v12,v11,s13\n"
AW "a130,v12\n"
AR "v12,a150\n"
"v_subrev_f32 v12,s13,v12\n"
"v_fma_f32 v12,v12,v11,s13\n"
AW "a150,v12\n"
AR "v12,a170\n"
"v_subrev_f32 v12,s13,v12\n"
"v_fma_f32 v12,v12,v11,s13\n"
AW "a170,v12\n"
AR "v12,a190\n"
"v_subrev_f32 v12,s13,v12\n"
"v_fma_f32 v12,v12,v11,s13\n"
AW "a190,v12\n"
AR "v12,a210\n"
"v_subrev_f32 v12,s13,v12\n"
"v_fma_f32 v12,v12,v11,s13\n"
AW "a210,v12\n"
AR "v12,a230\n"
"v_subrev_f32 v12,s13,v12\n"
"v_fma_f32 v12,v12,v11,s13\n"
AW "a230,v12\n"
AR "v12,a11\n"
"v_subrev_f32 v12,s13,v12\n"
"v_fma_f32 v12,v12,v11,s13\n"
AW "a11,v12\n"
AR "v12,a31\n"
"v_subrev_f32 v12,s13,v12\n"
"v_fma_f32 v12,v12,v11,s13\n"
AW "a31,v12\n"
AR "v12,a51\n"
"v_subrev_f32 v12,s13,v12\n"
"v_fma_f32 v12,v12,v11,s13\n"
AW "a51,v12\n"
AR "v12,a71\n"
"v_subrev_f32 v12,s13,v12\n"
"v_fma_f32 v12,v12,v11,s13\n"
AW "a71,v12\n"
AR "v12,a91\n"
"v_subrev_f32 v12,s13,v12\n"
"v_fma_f32 v12,v12,v11,s13\n"
AW "a91,v12\n"
AR "v12,a111\n"
"v_subrev_f32 v12,s13,v12\n"
"v_fma_f32 v12,v12,v11,s13\n"
AW "a111,v12\n"
AR "v12,a131\n"
"v_subrev_f32 v12,s13,v12\n"
"v_fma_f32 v12,v12,v11,s13\n"
AW "a131,v12\n"
AR "v12,a151\n"
"v_subrev_f32 v12,s13,v12\n"
"v_fma_f32 v12,v12,v11,s13\n"
AW "a151,v12\n"
AR "v12,a171\n"
"v_subrev_f32 v12,s13,v12\n"
"v_fma_f32 v12,v12,v11,s13\n"
AW "a171,v12\n"
AR "v12,a191\n"
"v_subrev_f32 v12,s13,v12\n"
"v_fma_f32 v12,v12,v11,s13\n"
AW "a191,v12\n"
AR "v12,a211\n"
"v_subrev_f32 v12,s13,v12\n"
"v_fma_f32 v12,v12,v11,s13\n"
AW "a211,v12\n"
AR "v12,a231\n"
"v_subrev_f32 v12,s13,v12\n"
"v_fma_f32 v12,v12,v11,s13\n"
AW "a231,v12\n"
"v_mul_f32 v54,v59,v11\n"
"v_sub_f32 v40,v40,v10\n"
"v_sub_f32 v41,v41,v10\n"
"v_sub_f32 v42,v42,v10\n"
"v_sub_f32 v43,v43,v10\n"
"v_sub_f32 v100,v100,v10\n"
"v_sub_f32 v101,v101,v10\n"
"v_sub_f32 v102,v102,v10\n"
"v_sub_f32 v103,v103,v10\n"
"v_sub_f32 v80,v80,v10\n"
"v_sub_f32 v81,v81,v10\n"
"v_sub_f32 v82,v82,v10\n"
"v_sub_f32 v83,v83,v10\n"
"v_sub_f32 v120,v120,v10\n"
"v_sub_f32 v121,v121,v10\n"
"v_sub_f32 v122,v122,v10\n"
"v_sub_f32 v123,v123,v10\n"
"v_exp_f32 v80,v80\n"
"v_exp_f32 v81,v81\n"
"v_exp_f32 v82,v82\n"
"v_exp_f32 v83,v83\n"
"v_exp_f32 v120,v120\n"
"v_exp_f32 v121,v121\n"
"v_exp_f32 v122,v122\n"
"v_exp_f32 v123,v123\n"
"v_add_f32 v54,v54,v80\n"
"v_add_f32 v54,v54,v81\n"
"v_add_f32 v54,v54,v82\n"
"v_add_f32 v54,v54,v83\n"
"v_add_f32 v54,v54,v120\n"
"v_add_f32 v54,v54,v121\n"
"v_add_f32 v54,v54,v122\n"
"v_add_f32_e64 v54,v54,v123\n"
CV "v80,v80,v81\n"
CV "v81,v82,v83\n"
CV "v82,v120,v121\n"
CV "v83,v122,v123\n"
"v_max3_f32 v10,v84,v85,v86\n"
"v_max3_f32 v11,v87,v124,v125\n"
"v_max3_f32 v10,v10,v126,v127\n"
"v_max_f32 v10,v10,v11\n"
"v_mov_b32 v11,v10\n"
"s_nop 1\n"
"v_permlane16_swap_b32 v10,v11\n"
"v_max_f32 v10,v10,v11\n"
"v_mov_b32 v11,v10\n"
"s_nop 1\n"
"v_permlane32_swap_b32 v10,v11\n"
"v_max_f32_e64 v10,v10,v11\n"
"v_add_f32 v10,0x41300000,v10\n"
"v_max_f32 v10,0,v10\n"
"v_exp_f32 v11,-v10\n"
AR "v12,a12\n"
"v_subrev_f32 v12,s13,v12\n"
"v_fma_f32 v12,v12,v11,s13\n"
AW "a12,v12\n"
AR "v12,a32\n"
"v_subrev_f32 v12,s13,v12\n"
"v_fma_f32 v12,v12,v11,s13\n"
AW "a32,v12\n"
AR "v12,a52\n"
"v_subrev_f32 v12,s13,v12\n"
"v_fma_f32 v12,v12,v11,s13\n"
AW "a52,v12\n"
AR "v12,a72\n"
"v_subrev_f32 v12,s13,v12\n"
"v_fma_f32 v12,v12,v11,s13\n"
AW "a72,v12\n"
AR "v12,a92\n"
"v_subrev_f32 v12,s13,v12\n"
"v_fma_f32 v12,v12,v11,s13\n"
AW "a92,v12\n"
AR "v12,a112\n"
"v_subrev_f32 v12,s13,v12\n"
"v_fma_f32 v12,v12,v11,s13\n"
AW "a112,v12\n"
AR "v12,a132\n"
"v_subrev_f32 v12,s13,v12\n"
"v_fma_f32 v12,v12,v11,s13\n"
AW "a132,v12\n"
AR "v12,a152\n"
"v_subrev_f32 v12,s13,v12\n"
"v_fma_f32 v12,v12,v11,s13\n"
AW "a152,v12\n"
AR "v12,a172\n"
"v_subrev_f32 v12,s13,v12\n"
"v_fma_f32 v12,v12,v11,s13\n"
AW "a172,v12\n"
AR "v12,a192\n"
"v_subrev_f32 v12,s13,v12\n"
"v_fma_f32 v12,v12,v11,s13\n"
AW "a192,v12\n"
AR "v12,a212\n"
"v_subrev_f32 v12,s13,v12\n"
"v_fma_f32 v12,v12,v11,s13\n"
AW "a212,v12\n"
AR "v12,a232\n"
"v_subrev_f32 v12,s13,v12\n"
"v_fma_f32 v12,v12,v11,s13\n"
AW "a232,v12\n"
AR "v12,a13\n"
"v_subrev_f32 v12,s13,v12\n"
"v_fma_f32 v12,v12,v11,s13\n"
AW "a13,v12\n"
AR "v12,a33\n"
"v_subrev_f32 v12,s13,v12\n"
"v_fma_f32 v12,v12,v11,s13\n"
AW "a33,v12\n"
AR "v12,a53\n"
"v_subrev_f32 v12,s13,v12\n"
"v_fma_f32 v12,v12,v11,s13\n"
AW "a53,v12\n"
AR "v12,a73\n"
"v_subrev_f32 v12,s13,v12\n"
"v_fma_f32 v12,v12,v11,s13\n"
AW "a73,v12\n"
AR "v12,a93\n"
"v_subrev_f32 v12,s13,v12\n"
"v_fma_f32 v12,v12,v11,s13\n"
AW "a93,v12\n"
AR "v12,a113\n"
"v_subrev_f32 v12,s13,v12\n"
"v_fma_f32 v12,v12,v11,s13\n"
AW "a113,v12\n"
AR "v12,a133\n"
"v_subrev_f32 v12,s13,v12\n"
"v_fma_f32 v12,v12,v11,s13\n"
AW "a133,v12\n"
AR "v12,a153\n"
"v_subrev_f32 v12,s13,v12\n"
"v_fma_f32 v12,v12,v11,s13\n"
AW "a153,v12\n"
AR "v12,a173\n"
"v_subrev_f32 v12,s13,v12\n"
"v_fma_f32 v12,v12,v11,s13\n"
AW "a173,v12\n"
AR "v12,a193\n"
"v_subrev_f32 v12,s13,v12\n"
"v_fma_f32 v12,v12,v11,s13\n"
AW "a193,v12\n"
AR "v12,a213\n"
"v_subrev_f32 v12,s13,v12\n"
"v_fma_f32 v12,v12,v11,s13\n"
AW "a213,v12\n"
AR "v12,a233\n"
"v_subrev_f32 v12,s13,v12\n"
"v_fma_f32 v12,v12,v11,s13\n"
AW "a233,v12\n"
AR "v12,a14\n"
"v_subrev_f32 v12,s13,v12\n"
"v_fma_f32 v12,v12,v11,s13\n"
AW "a14,v12\n"
AR "v12,a34\n"
"v_subrev_f32 v12,s13,v12\n"
"v_fma_f32 v12,v12,v11,s13\n"
AW "a34,v12\n"
AR "v12,a54\n"
"v_subrev_f32 v12,s13,v12\n"
"v_fma_f32 v12,v12,v11,s13\n"
AW "a54,v12\n"
AR "v12,a74\n"
"v_subrev_f32 v12,s13,v12\n"
"v_fma_f32 v12,v12,v11,s13\n"
AW "a74,v12\n"
AR "v12,a94\n"
"v_subrev_f32 v12,s13,v12\n"
"v_fma_f32 v12,v12,v11,s13\n"
AW "a94,v12\n"
AR "v12,a114\n"
"v_subrev_f32 v12,s13,v12\n"
"v_fma_f32 v12,v12,v11,s13\n"
AW "a114,v12\n"
AR "v12,a134\n"
"v_subrev_f32 v12,s13,v12\n"
"v_fma_f32 v12,v12,v11,s13\n"
AW "a134,v12\n"
AR "v12,a154\n"
"v_subrev_f32 v12,s13,v12\n"
"v_fma_f32 v12,v12,v11,s13\n"
AW "a154,v12\n"
AR "v12,a174\n"
"v_subrev_f32 v12,s13,v12\n"
"v_fma_f32 v12,v12,v11,s13\n"
AW "a174,v12\n"
AR "v12,a194\n"
"v_subrev_f32 v12,s13,v12\n"
"v_fma_f32 v12,v12,v11,s13\n"
AW "a194,v12\n"
AR "v12,a214\n"
"v_subrev_f32 v12,s13,v12\n"
"v_fma_f32 v12,v12,v11,s13\n"
AW "a214,v12\n"
AR "v12,a234\n"
"v_subrev_f32 v12,s13,v12\n"
"v_fma_f32 v12,v12,v11,s13\n"
AW "a234,v12\n"
AR "v12,a15\n"
"v_subrev_f32 v12,s13,v12\n"
"v_fma_f32 v12,v12,v11,s13\n"
AW "a15,v12\n"
AR "v12,a35\n"
"v_subrev_f32 v12,s13,v12\n"
"v_fma_f32 v12,v12,v11,s13\n"
AW "a35,v12\n"
AR "v12,a55\n"
"v_subrev_f32 v12,s13,v12\n"
"v_fma_f32 v12,v12,v11,s13\n"
AW "a55,v12\n"
AR "v12,a75\n"
"v_subrev_f32 v12,s13,v12\n"
"v_fma_f32 v12,v12,v11,s13\n"
AW "a75,v12\n"
AR "v12,a95\n"
"v_subrev_f32 v12,s13,v12\n"
"v_fma_f32 v12,v12,v11,s13\n"
AW "a95,v12\n"
AR "v12,a115\n"
"v_subrev_f32 v12,s13,v12\n"
"v_fma_f32 v12,v12,v11,s13\n"
AW "a115,v12\n"
AR "v12,a135\n"
"v_subrev_f32 v12,s13,v12\n"
"v_fma_f32 v12,v12,v11,s13\n"
AW "a135,v12\n"
AR "v12,a155\n"
"v_subrev_f32 v12,s13,v12\n"
"v_fma_f32 v12,v12,v11,s13\n"
AW "a155,v12\n"
AR "v12,a175\n"
"v_subrev_f32 v12,s13,v12\n"
"v_fma_f32 v12,v12,v11,s13\n"
AW "a175,v12\n"
AR "v12,a195\n"
"v_subrev_f32 v12,s13,v12\n"
"v_fma_f32 v12,v12,v11,s13\n"
AW "a195,v12\n"
AR "v12,a215\n"
"v_subrev_f32 v12,s13,v12\n"
"v_fma_f32 v12,v12,v11,s13\n"
AW "a215,v12\n"
AR "v12,a235\n"
"v_subrev_f32 v12,s13,v12\n"
"v_fma_f32 v12,v12,v11,s13\n"
AW "a235,v12\n"
"v_mul_f32 v55,v60,v11\n"
"v_sub_f32 v44,v44,v10\n"
"v_sub_f32 v45,v45,v10\n"
"v_sub_f32 v46,v46,v10\n"
"v_sub_f32 v47,v47,v10\n"
"v_sub_f32 v104,v104,v10\n"
"v_sub_f32 v105,v105,v10\n"
"v_sub_f32 v106,v106,v10\n"
"v_sub_f32 v107,v107,v10\n"
"v_sub_f32 v84,v84,v10\n"
"v_sub_f32 v85,v85,v10\n"
"v_sub_f32 v86,v86,v10\n"
"v_sub_f32 v87,v87,v10\n"
"v_sub_f32 v124,v124,v10\n"
"v_sub_f32 v125,v125,v10\n"
"v_sub_f32 v126,v126,v10\n"
"v_sub_f32 v127,v127,v10\n"
"v_exp_f32 v84,v84\n"
"v_exp_f32 v85,v85\n"
"v_exp_f32 v86,v86\n"
"v_exp_f32 v87,v87\n"
"v_exp_f32 v124,v124\n"
"v_exp_f32 v125,v125\n"
"v_exp_f32 v126,v126\n"
"v_exp_f32 v127,v127\n"
"v_add_f32 v55,v55,v84\n"
"v_add_f32 v55,v55,v85\n"
"v_add_f32 v55,v55,v86\n"
"v_add_f32 v55,v55,v87\n"
"v_add_f32 v55,v55,v124\n"
"v_add_f32 v55,v55,v125\n"
"v_add_f32 v55,v55,v126\n"
"v_add_f32 v55,v55,v127\n"
CV "v84,v84,v85\n"
CV "v85,v86,v87\n"
CV "v86,v124,v125\n"
CV "v87,v126,v127\n"
"v_max3_f32 v10,v88,v89,v90\n"
"v_max3_f32 v11,v91,v128,v129\n"
"v_max3_f32 v10,v10,v130,v131\n"
"v_max_f32 v10,v10,v11\n"
"v_mov_b32 v11,v10\n"
"s_nop 1\n"
"v_permlane16_swap_b32 v10,v11\n"
"v_max_f32 v10,v10,v11\n"
"v_mov_b32 v11,v10\n"
"s_nop 1\n"
"v_permlane32_swap_b32 v10,v11\n"
"v_max_f32 v10,v10,v11\n"
"v_add_f32 v10,0x41300000,v10\n"
"v_max_f32 v10,0,v10\n"
"v_exp_f32 v11,-v10\n"
AR "v12,a16\n"
"v_subrev_f32 v12,s13,v12\n"
"v_fma_f32 v12,v12,v11,s13\n"
AW "a16,v12\n"
AR "v12,a36\n"
"v_subrev_f32 v12,s13,v12\n"
"v_fma_f32 v12,v12,v11,s13\n"
AW "a36,v12\n"
AR "v12,a56\n"
"v_subrev_f32 v12,s13,v12\n"
"v_fma_f32 v12,v12,v11,s13\n"
AW "a56,v12\n"
AR "v12,a76\n"
"v_subrev_f32 v12,s13,v12\n"
"v_fma_f32 v12,v12,v11,s13\n"
AW "a76,v12\n"
AR "v12,a96\n"
"v_subrev_f32 v12,s13,v12\n"
"v_fma_f32 v12,v12,v11,s13\n"
AW "a96,v12\n"
AR "v12,a116\n"
"v_subrev_f32 v12,s13,v12\n"
"v_fma_f32 v12,v12,v11,s13\n"
AW "a116,v12\n"
AR "v12,a136\n"
"v_subrev_f32 v12,s13,v12\n"
"v_fma_f32 v12,v12,v11,s13\n"
AW "a136,v12\n"
AR "v12,a156\n"
"v_subrev_f32 v12,s13,v12\n"
"v_fma_f32 v12,v12,v11,s13\n"
AW "a156,v12\n"
AR "v12,a176\n"
"v_subrev_f32 v12,s13,v12\n"
"v_fma_f32 v12,v12,v11,s13\n"
AW "a176,v12\n"
AR "v12,a196\n"
"v_subrev_f32 v12,s13,v12\n"
"v_fma_f32 v12,v12,v11,s13\n"
AW "a196,v12\n"
AR "v12,a216\n"
"v_subrev_f32 v12,s13,v12\n"
"v_fma_f32 v12,v12,v11,s13\n"
AW "a216,v12\n"
AR "v12,a236\n"
"v_subrev_f32 v12,s13,v12\n"
"v_fma_f32 v12,v12,v11,s13\n"
AW "a236,v12\n"
AR "v12,a17\n"
"v_subrev_f32 v12,s13,v12\n"
"v_fma_f32 v12,v12,v11,s13\n"
AW "a17,v12\n"
AR "v12,a37\n"
"v_subrev_f32 v12,s13,v12\n"
"v_fma_f32 v12,v12,v11,s13\n"
AW "a37,v12\n"
AR "v12,a57\n"
"v_subrev_f32 v12,s13,v12\n"
"v_fma_f32 v12,v12,v11,s13\n"
AW "a57,v12\n"
AR "v12,a77\n"
"v_subrev_f32 v12,s13,v12\n"
"v_fma_f32 v12,v12,v11,s13\n"
AW "a77,v12\n"
AR "v12,a97\n"
"v_subrev_f32 v12,s13,v12\n"
"v_fma_f32 v12,v12,v11,s13\n"
AW "a97,v12\n"
AR "v12,a117\n"
"v_subrev_f32 v12,s13,v12\n"
"v_fma_f32 v12,v12,v11,s13\n"
AW "a117,v12\n"
AR "v12,a137\n"
"v_subrev_f32 v12,s13,v12\n"
"v_fma_f32 v12,v12,v11,s13\n"
AW "a137,v12\n"
AR "v12,a157\n"
"v_subrev_f32 v12,s13,v12\n"
"v_fma_f32 v12,v12,v11,s13\n"
AW "a157,v12\n"
AR "v12,a177\n"
"v_subrev_f32 v12,s13,v12\n"
"v_fma_f32 v12,v12,v11,s13\n"
AW "a177,v12\n"
AR "v12,a197\n"
"v_subrev_f32 v12,s13,v12\n"
"v_fma_f32 v12,v12,v11,s13\n"
AW "a197,v12\n"
AR "v12,a217\n"
"v_subrev_f32 v12,s13,v12\n"
"v_fma_f32 v12,v12,v11,s13\n"
AW "a217,v12\n"
AR "v12,a237\n"
"v_subrev_f32 v12,s13,v12\n"
"v_fma_f32 v12,v12,v11,s13\n"
AW "a237,v12\n"
AR "v12,a18\n"
"v_subrev_f32 v12,s13,v12\n"
"v_fma_f32 v12,v12,v11,s13\n"
AW "a18,v12\n"
AR "v12,a38\n"
"v_subrev_f32 v12,s13,v12\n"
"v_fma_f32 v12,v12,v11,s13\n"
AW "a38,v12\n"
AR "v12,a58\n"
"v_subrev_f32 v12,s13,v12\n"
"v_fma_f32 v12,v12,v11,s13\n"
AW "a58,v12\n"
AR "v12,a78\n"
"v_subrev_f32 v12,s13,v12\n"
"v_fma_f32 v12,v12,v11,s13\n"
AW "a78,v12\n"
AR "v12,a98\n"
"v_subrev_f32 v12,s13,v12\n"
"v_fma_f32 v12,v12,v11,s13\n"
AW "a98,v12\n"
AR "v12,a118\n"
"v_subrev_f32 v12,s13,v12\n"
"v_fma_f32 v12,v12,v11,s13\n"
AW "a118,v12\n"
AR "v12,a138\n"
"v_subrev_f32 v12,s13,v12\n"
"v_fma_f32 v12,v12,v11,s13\n"
AW "a138,v12\n"
AR "v12,a158\n"
"v_subrev_f32 v12,s13,v12\n"
"v_fma_f32 v12,v12,v11,s13\n"
AW "a158,v12\n"
AR "v12,a178\n"
"v_subrev_f32 v12,s13,v12\n"
"v_fma_f32 v12,v12,v11,s13\n"
AW "a178,v12\n"
AR "v12,a198\n"
"v_subrev_f32 v12,s13,v12\n"
"v_fma_f32 v12,v12,v11,s13\n"
AW "a198,v12\n"
AR "v12,a218\n"
"v_subrev_f32 v12,s13,v12\n"
"v_fma_f32 v12,v12,v11,s13\n"
AW "a218,v12\n"
AR "v12,a238\n"
"v_subrev_f32 v12,s13,v12\n"
"v_fma_f32 v12,v12,v11,s13\n"
AW "a238,v12\n"
AR "v12,a19\n"
"v_subrev_f32 v12,s13,v12\n"
"v_fma_f32 v12,v12,v11,s13\n"
AW "a19,v12\n"
AR "v12,a39\n"
"v_subrev_f32 v12,s13,v12\n"
"v_fma_f32 v12,v12,v11,s13\n"
AW "a39,v12\n"
AR "v12,a59\n"
"v_subrev_f32 v12,s13,v12\n"
"v_fma_f32 v12,v12,v11,s13\n"
AW "a59,v12\n"
AR "v12,a79\n"
"v_subrev_f32 v12,s13,v12\n"
"v_fma_f32 v12,v12,v11,s13\n"
AW "a79,v12\n"
AR "v12,a99\n"
"v_subrev_f32 v12,s13,v12\n"
"v_fma_f32 v12,v12,v11,s13\n"
AW "a99,v12\n"
AR "v12,a119\n"
"v_subrev_f32 v12,s13,v12\n"
"v_fma_f32 v12,v12,v11,s13\n"
AW "a119,v12\n"
AR "v12,a139\n"
"v_subrev_f32 v12,s13,v12\n"
"v_fma_f32 v12,v12,v11,s13\n"
AW "a139,v12\n"
AR "v12,a159\n"
"v_subrev_f32 v12,s13,v12\n"
"v_fma_f32 v12,v12,v11,s13\n"
AW "a159,v12\n"
AR "v12,a179\n"
"v_subrev_f32 v12,s13,v12\n"
"v_fma_f32 v12,v12,v11,s13\n"
AW "a179,v12\n"
AR "v12,a199\n"
"v_subrev_f32 v12,s13,v12\n"
"v_fma_f32 v12,v12,v11,s13\n"
AW "a199,v12\n"
AR "v12,a219\n"
"v_subrev_f32 v12,s13,v12\n"
"v_fma_f32 v12,v12,v11,s13\n"
AW "a219,v12\n"
AR "v12,a239\n"
"v_subrev_f32 v12,s13,v12\n"
"v_fma_f32 v12,v12,v11,s13\n"
AW "a239,v12\n"
"v_mul_f32 v56,v61,v11\n"
"v_sub_f32 v48,v48,v10\n"
"v_sub_f32 v49,v49,v10\n"
"v_sub_f32 v50,v50,v10\n"
"v_sub_f32 v51,v51,v10\n"
"v_sub_f32 v108,v108,v10\n"
"v_sub_f32 v109,v109,v10\n"
"v_sub_f32 v110,v110,v10\n"
"v_sub_f32 v111,v111,v10\n"
"v_sub_f32 v88,v88,v10\n"
"v_sub_f32 v89,v89,v10\n"
"v_sub_f32 v90,v90,v10\n"
"v_sub_f32 v91,v91,v10\n"
"v_sub_f32 v128,v128,v10\n"
"v_sub_f32 v129,v129,v10\n"
"v_sub_f32 v130,v130,v10\n"
"v_sub_f32 v131,v131,v10\n"
"v_exp_f32 v88,v88\n"
"v_exp_f32 v89,v89\n"
"v_exp_f32 v90,v90\n"
"v_exp_f32 v91,v91\n"
"v_exp_f32 v128,v128\n"
"v_exp_f32 v129,v129\n"
"v_exp_f32 v130,v130\n"
"v_exp_f32 v131,v131\n"
"v_add_f32 v56,v56,v88\n"
"v_add_f32 v56,v56,v89\n"
"v_add_f32 v56,v56,v90\n"
"v_add_f32 v56,v56,v91\n"
"v_add_f32 v56,v56,v128\n"
"v_add_f32 v56,v56,v129\n"
"v_add_f32 v56,v56,v130\n"
"v_add_f32_e64 v56,v56,v131\n"
CV "v88,v88,v89\n"
CV "v89,v90,v91\n"
CV "v90,v128,v129\n"
CV "v91,v130,v131\n"
"s_nop 7\n"
"s_setpc_b64 s[52:53]\n"
".Lslow_1:\n"
"s_nop 7\n"
"s_nop 7\n"
"v_and_b32 v10,240,v9\n"
"v_mul_u32_u24 v10,24,v10\n"
"v_lshrrev_b32 v11,4,v9\n"
"v_and_or_b32 v10,v11,48,v10\n"
"s_mov_b32 s59,s34\n"
"s_mov_b32 s34,s20\n"
"s_add_u32 s58,s74,0\n"
"s_nop 3\n"
LD "v[58:61],v10,s[32:35],s58 offen\n"
"s_waitcnt vmcnt(0)\n"
MF "v[92:95],v[58:61],v[136:139],v[32:35]\n"
MF "v[96:99],v[58:61],v[140:143],v[36:39]\n"
MF "v[100:103],v[58:61],v[144:147],v[40:43]\n"
MF "v[104:107],v[58:61],v[148:151],v[44:47]\n"
MF "v[108:111],v[58:61],v[152:155],v[48:51]\n"
"s_add_u32 s58,s74,0\n"
"s_nop 3\n"
LD "v[58:61],v10,s[32:35],s58 offen offset:64\n"
"s_waitcnt vmcnt(0)\n"
MF "v[92:95],v[58:61],v[156:159],v[92:95]\n"
MF "v[96:99],v[58:61],v[160:163],v[96:99]\n"
MF "v[100:103],v[58:61],v[164:167],v[100:103]\n"
MF "v[104:107],v[58:61],v[168:171],v[104:107]\n"
MF "v[108:111],v[58:61],v[172:175],v[108:111]\n"
"s_add_u32 s58,s74,0\n"
"s_nop 3\n"
LD "v[58:61],v10,s[32:35],s58 offen offset:128\n"
"s_waitcnt vmcnt(0)\n"
MF "v[92:95],v[58:61],v[176:179],v[92:95]\n"
MF "v[96:99],v[58:61],v[180:183],v[96:99]\n"
MF "v[100:103],v[58:61],v[184:187],v[100:103]\n"
MF "v[104:107],v[58:61],v[188:191],v[104:107]\n"
MF "v[108:111],v[58:61],v[192:195],v[108:111]\n"
"s_add_u32 s58,s74,0\n"
"s_nop 3\n"
LD "v[58:61],v10,s[32:35],s58 offen offset:192\n"
"s_waitcnt vmcnt(0)\n"
MF "v[92:95],v[58:61],v[196:199],v[92:95]\n"
MF "v[96:99],v[58:61],v[200:203],v[96:99]\n"
MF "v[100:103],v[58:61],v[204:207],v[100:103]\n"
MF "v[104:107],v[58:61],v[208:211],v[104:107]\n"
MF "v[108:111],v[58:61],v[212:215],v[108:111]\n"
"s_add_u32 s58,s74,0\n"
"s_nop 3\n"
LD "v[58:61],v10,s[32:35],s58 offen offset:256\n"
"s_waitcnt vmcnt(0)\n"
MF "v[92:95],v[58:61],v[216:219],v[92:95]\n"
MF "v[96:99],v[58:61],v[220:223],v[96:99]\n"
MF "v[100:103],v[58:61],v[224:227],v[100:103]\n"
MF "v[104:107],v[58:61],v[228:231],v[104:107]\n"
MF "v[108:111],v[58:61],v[232:235],v[108:111]\n"
"s_add_u32 s58,s74,0\n"
"s_nop 3\n"
LD "v[58:61],v10,s[32:35],s58 offen offset:320\n"
"s_waitcnt vmcnt(0)\n"
MF "v[92:95],v[58:61],v[236:239],v[92:95]\n"
MF "v[96:99],v[58:61],v[240:243],v[96:99]\n"
MF "v[100:103],v[58:61],v[244:247],v[100:103]\n"
MF "v[104:107],v[58:61],v[248:251],v[104:107]\n"
MF "v[108:111],v[58:61],v[252:255],v[108:111]\n"
"s_add_u32 s58,s74,6144\n"
"s_nop 3\n"
LD "v[58:61],v10,s[32:35],s58 offen\n"
"s_waitcnt vmcnt(0)\n"
MF "v[112:115],v[58:61],v[136:139],v[32:35]\n"
MF "v[116:119],v[58:61],v[140:143],v[36:39]\n"
MF "v[120:123],v[58:61],v[144:147],v[40:43]\n"
MF "v[124:127],v[58:61],v[148:151],v[44:47]\n"
MF "v[128:131],v[58:61],v[152:155],v[48:51]\n"
"s_add_u32 s58,s74,6144\n"
"s_nop 3\n"
LD "v[58:61],v10,s[32:35],s58 offen offset:64\n"
"s_waitcnt vmcnt(0)\n"
MF "v[112:115],v[58:61],v[156:159],v[112:115]\n"
MF "v[116:119],v[58:61],v[160:163],v[116:119]\n"
MF "v[120:123],v[58:61],v[164:167],v[120:123]\n"
MF "v[124:127],v[58:61],v[168:171],v[124:127]\n"
MF "v[128:131],v[58:61],v[172:175],v[128:131]\n"
"s_add_u32 s58,s74,6144\n"
"s_nop 3\n"
LD "v[58:61],v10,s[32:35],s58 offen offset:128\n"
"s_waitcnt vmcnt(0)\n"
MF "v[112:115],v[58:61],v[176:179],v[112:115]\n"
MF "v[116:119],v[58:61],v[180:183],v[116:119]\n"
MF "v[120:123],v[58:61],v[184:187],v[120:123]\n"
MF "v[124:127],v[58:61],v[188:191],v[124:127]\n"
MF "v[128:131],v[58:61],v[192:195],v[128:131]\n"
"s_add_u32 s58,s74,6144\n"
"s_nop 3\n"
LD "v[58:61],v10,s[32:35],s58 offen offset:192\n"
"s_waitcnt vmcnt(0)\n"
MF "v[112:115],v[58:61],v[196:199],v[112:115]\n"
MF "v[116:119],v[58:61],v[200:203],v[116:119]\n"
MF "v[120:123],v[58:61],v[204:207],v[120:123]\n"
MF "v[124:127],v[58:61],v[208:211],v[124:127]\n"
MF "v[128:131],v[58:61],v[212:215],v[128:131]\n"
"s_add_u32 s58,s74,6144\n"
"s_nop 3\n"
LD "v[58:61],v10,s[32:35],s58 offen offset:256\n"
"s_waitcnt vmcnt(0)\n"
MF "v[112:115],v[58:61],v[216:219],v[112:115]\n"
MF "v[116:119],v[58:61],v[220:223],v[116:119]\n"
MF "v[120:123],v[58:61],v[224:227],v[120:123]\n"
MF "v[124:127],v[58:61],v[228:231],v[124:127]\n"
MF "v[128:131],v[58:61],v[232:235],v[128:131]\n"
"s_add_u32 s58,s74,6144\n"
"s_nop 3\n"
LD "v[58:61],v10,s[32:35],s58 offen offset:320\n"
"s_waitcnt vmcnt(0)\n"
MF "v[112:115],v[58:61],v[236:239],v[112:115]\n"
MF "v[116:119],v[58:61],v[240:243],v[116:119]\n"
MF "v[120:123],v[58:61],v[244:247],v[120:123]\n"
MF "v[124:127],v[58:61],v[248:251],v[124:127]\n"
MF "v[128:131],v[58:61],v[252:255],v[128:131]\n"
"s_nop 7\n"
"s_mov_b32 s34,s59\n"
"v_max3_f32 v10,v92,v93,v94\n"
"s_nop 0\n"
"v_max3_f32 v11,v95,v112,v113\n"
"v_max3_f32 v10,v10,v114,v115\n"
"v_max_f32 v10,v10,v11\n"
"v_mov_b32 v11,v10\n"
"s_nop 1\n"
"v_permlane16_swap_b32 v10,v11\n"
"v_max_f32 v10,v10,v11\n"
"v_mov_b32 v11,v10\n"
"s_nop 1\n"
"v_permlane32_swap_b32 v10,v11\n"
"v_max_f32 v10,v10,v11\n"
"v_add_f32 v10,0x41300000,v10\n"
"v_max_f32_e64 v10,0,v10\n"
"v_exp_f32 v11,-v10\n"
AR "v12,a0\n"
"v_subrev_f32 v12,s13,v12\n"
"v_fma_f32 v12,v12,v11,s13\n"
AW "a0,v12\n"
AR "v12,a20\n"
"v_subrev_f32 v12,s13,v12\n"
"v_fma_f32 v12,v12,v11,s13\n"
AW "a20,v12\n"
AR "v12,a40\n"
"v_subrev_f32 v12,s13,v12\n"
"v_fma_f32 v12,v12,v11,s13\n"
AW "a40,v12\n"
AR "v12,a60\n"
"v_subrev_f32 v12,s13,v12\n"
"v_fma_f32 v12,v12,v11,s13\n"
AW "a60,v12\n"
AR "v12,a80\n"
"v_subrev_f32 v12,s13,v12\n"
"v_fma_f32 v12,v12,v11,s13\n"
AW "a80,v12\n"
AR "v12,a100\n"
"v_subrev_f32 v12,s13,v12\n"
"v_fma_f32 v12,v12,v11,s13\n"
AW "a100,v12\n"
AR "v12,a120\n"
"v_subrev_f32 v12,s13,v12\n"
"v_fma_f32 v12,v12,v11,s13\n"
AW "a120,v12\n"
AR "v12,a140\n"
"v_subrev_f32 v12,s13,v12\n"
"v_fma_f32 v12,v12,v11,s13\n"
AW "a140,v12\n"
AR "v12,a160\n"
"v_subrev_f32 v12,s13,v12\n"
"v_fma_f32 v12,v12,v11,s13\n"
AW "a160,v12\n"
AR "v12,a180\n"
"v_subrev_f32 v12,s13,v12\n"
"v_fma_f32 v12,v12,v11,s13\n"
AW "a180,v12\n"
AR "v12,a200\n"
"v_subrev_f32 v12,s13,v12\n"
"v_fma_f32 v12,v12,v11,s13\n"
AW "a200,v12\n"
AR "v12,a220\n"
"v_subrev_f32 v12,s13,v12\n"
"v_fma_f32 v12,v12,v11,s13\n"
AW "a220,v12\n"
AR "v12,a1\n"
"v_subrev_f32 v12,s13,v12\n"
"v_fma_f32 v12,v12,v11,s13\n"
AW "a1,v12\n"
AR "v12,a21\n"
"v_subrev_f32 v12,s13,v12\n"
"v_fma_f32 v12,v12,v11,s13\n"
AW "a21,v12\n"
AR "v12,a41\n"
"v_subrev_f32 v12,s13,v12\n"
"v_fma_f32 v12,v12,v11,s13\n"
AW "a41,v12\n"
AR "v12,a61\n"
"v_subrev_f32 v12,s13,v12\n"
"v_fma_f32 v12,v12,v11,s13\n"
AW "a61,v12\n"
AR "v12,a81\n"
"v_subrev_f32 v12,s13,v12\n"
"v_fma_f32 v12,v12,v11,s13\n"
AW "a81,v12\n"
AR "v12,a101\n"
"v_subrev_f32 v12,s13,v12\n"
"v_fma_f32 v12,v12,v11,s13\n"
AW "a101,v12\n"
AR "v12,a121\n"
"v_subrev_f32 v12,s13,v12\n"
"v_fma_f32 v12,v12,v11,s13\n"
AW "a121,v12\n"
AR "v12,a141\n"
"v_subrev_f32 v12,s13,v12\n"
"v_fma_f32 v12,v12,v11,s13\n"
AW "a141,v12\n"
AR "v12,a161\n"
"v_subrev_f32 v12,s13,v12\n"
"v_fma_f32 v12,v12,v11,s13\n"
AW "a161,v12\n"
AR "v12,a181\n"
"v_subrev_f32 v12,s13,v12\n"
"v_fma_f32 v12,v12,v11,s13\n"
AW "a181,v12\n"
AR "v12,a201\n"
"v_subrev_f32 v12,s13,v12\n"
"v_fma_f32 v12,v12,v11,s13\n"
AW "a201,v12\n"
AR "v12,a221\n"
"v_subrev_f32 v12,s13,v12\n"
"v_fma_f32 v12,v12,v11,s13\n"
AW "a221,v12\n"
AR "v12,a2\n"
"v_subrev_f32 v12,s13,v12\n"
"v_fma_f32 v12,v12,v11,s13\n"
AW "a2,v12\n"
AR "v12,a22\n"
"v_subrev_f32 v12,s13,v12\n"
"v_fma_f32 v12,v12,v11,s13\n"
AW "a22,v12\n"
AR "v12,a42\n"
"v_subrev_f32 v12,s13,v12\n"
"v_fma_f32 v12,v12,v11,s13\n"
AW "a42,v12\n"
AR "v12,a62\n"
"v_subrev_f32 v12,s13,v12\n"
"v_fma_f32 v12,v12,v11,s13\n"
AW "a62,v12\n"
AR "v12,a82\n"
"v_subrev_f32 v12,s13,v12\n"
"v_fma_f32 v12,v12,v11,s13\n"
AW "a82,v12\n"
AR "v12,a102\n"
"v_subrev_f32 v12,s13,v12\n"
"v_fma_f32 v12,v12,v11,s13\n"
AW "a102,v12\n"
AR "v12,a122\n"
"v_subrev_f32 v12,s13,v12\n"
"v_fma_f32 v12,v12,v11,s13\n"
AW "a122,v12\n"
AR "v12,a142\n"
"v_subrev_f32 v12,s13,v12\n"
"v_fma_f32 v12,v12,v11,s13\n"
AW "a142,v12\n"
AR "v12,a162\n"
"v_subrev_f32 v12,s13,v12\n"
"v_fma_f32 v12,v12,v11,s13\n"
AW "a162,v12\n"
AR "v12,a182\n"
"v_subrev_f32 v12,s13,v12\n"
"v_fma_f32 v12,v12,v11,s13\n"
AW "a182,v12\n"
AR "v12,a202\n"
"v_subrev_f32 v12,s13,v12\n"
"v_fma_f32 v12,v12,v11,s13\n"
AW "a202,v12\n"
AR "v12,a222\n"
"v_subrev_f32 v12,s13,v12\n"
"v_fma_f32 v12,v12,v11,s13\n"
AW "a222,v12\n"
AR "v12,a3\n"
"v_subrev_f32 v12,s13,v12\n"
"v_fma_f32 v12,v12,v11,s13\n"
AW "a3,v12\n"
AR "v12,a23\n"
"v_subrev_f32 v12,s13,v12\n"
"v_fma_f32 v12,v12,v11,s13\n"
AW "a23,v12\n"
AR "v12,a43\n"
"v_subrev_f32 v12,s13,v12\n"
"v_fma_f32 v12,v12,v11,s13\n"
AW "a43,v12\n"
AR "v12,a63\n"
"v_subrev_f32 v12,s13,v12\n"
"v_fma_f32 v12,v12,v11,s13\n"
AW "a63,v12\n"
AR "v12,a83\n"
"v_subrev_f32 v12,s13,v12\n"
"v_fma_f32 v12,v12,v11,s13\n"
AW "a83,v12\n"
AR "v12,a103\n"
"v_subrev_f32 v12,s13,v12\n"
"v_fma_f32 v12,v12,v11,s13\n"
AW "a103,v12\n"
AR "v12,a123\n"
"v_subrev_f32 v12,s13,v12\n"
"v_fma_f32 v12,v12,v11,s13\n"
AW "a123,v12\n"
AR "v12,a143\n"
"v_subrev_f32 v12,s13,v12\n"
"v_fma_f32 v12,v12,v11,s13\n"
AW "a143,v12\n"
AR "v12,a163\n"
"v_subrev_f32 v12,s13,v12\n"
"v_fma_f32 v12,v12,v11,s13\n"
AW "a163,v12\n"
AR "v12,a183\n"
"v_subrev_f32 v12,s13,v12\n"
"v_fma_f32 v12,v12,v11,s13\n"
AW "a183,v12\n"
AR "v12,a203\n"
"v_subrev_f32 v12,s13,v12\n"
"v_fma_f32 v12,v12,v11,s13\n"
AW "a203,v12\n"
AR "v12,a223\n"
"v_subrev_f32 v12,s13,v12\n"
"v_fma_f32 v12,v12,v11,s13\n"
AW "a223,v12\n"
"v_mul_f32 v57,v52,v11\n"
"v_sub_f32 v32,v32,v10\n"
"v_sub_f32 v33,v33,v10\n"
"v_sub_f32 v34,v34,v10\n"
"v_sub_f32 v35,v35,v10\n"
"v_sub_f32 v72,v72,v10\n"
"v_sub_f32 v73,v73,v10\n"
"v_sub_f32 v74,v74,v10\n"
"v_sub_f32 v75,v75,v10\n"
"v_sub_f32 v92,v92,v10\n"
"v_sub_f32 v93,v93,v10\n"
"v_sub_f32 v94,v94,v10\n"
"v_sub_f32 v95,v95,v10\n"
"v_sub_f32 v112,v112,v10\n"
"v_sub_f32 v113,v113,v10\n"
"v_sub_f32 v114,v114,v10\n"
"v_sub_f32 v115,v115,v10\n"
"v_exp_f32 v92,v92\n"
"v_exp_f32 v93,v93\n"
"v_exp_f32 v94,v94\n"
"v_exp_f32 v95,v95\n"
"v_exp_f32 v112,v112\n"
"v_exp_f32 v113,v113\n"
"v_exp_f32 v114,v114\n"
"v_exp_f32 v115,v115\n"
"v_add_f32 v57,v57,v92\n"
"v_add_f32 v57,v57,v93\n"
"v_add_f32 v57,v57,v94\n"
"v_add_f32 v57,v57,v95\n"
"v_add_f32 v57,v57,v112\n"
"v_add_f32 v57,v57,v113\n"
"v_add_f32 v57,v57,v114\n"
"v_add_f32_e64 v57,v57,v115\n"
CV "v92,v92,v93\n"
CV "v93,v94,v95\n"
CV "v94,v112,v113\n"
CV "v95,v114,v115\n"
"v_max3_f32 v10,v96,v97,v98\n"
"v_max3_f32 v11,v99,v116,v117\n"
"v_max3_f32 v10,v10,v118,v119\n"
"v_max_f32 v10,v10,v11\n"
"v_mov_b32 v11,v10\n"
"s_nop 1\n"
"v_permlane16_swap_b32 v10,v11\n"
"v_max_f32 v10,v10,v11\n"
"v_mov_b32 v11,v10\n"
"s_nop 1\n"
"v_permlane32_swap_b32 v10,v11\n"
"v_max_f32 v10,v10,v11\n"
"v_add_f32 v10,0x41300000,v10\n"
"v_max_f32 v10,0,v10\n"
"v_exp_f32 v11,-v10\n"
AR "v12,a4\n"
"v_subrev_f32 v12,s13,v12\n"
"v_fma_f32 v12,v12,v11,s13\n"
AW "a4,v12\n"
AR "v12,a24\n"
"v_subrev_f32 v12,s13,v12\n"
"v_fma_f32 v12,v12,v11,s13\n"
AW "a24,v12\n"
AR "v12,a44\n"
"v_subrev_f32 v12,s13,v12\n"
"v_fma_f32 v12,v12,v11,s13\n"
AW "a44,v12\n"
AR "v12,a64\n"
"v_subrev_f32 v12,s13,v12\n"
"v_fma_f32 v12,v12,v11,s13\n"
AW "a64,v12\n"
AR "v12,a84\n"
"v_subrev_f32 v12,s13,v12\n"
"v_fma_f32 v12,v12,v11,s13\n"
AW "a84,v12\n"
AR "v12,a104\n"
"v_subrev_f32 v12,s13,v12\n"
"v_fma_f32 v12,v12,v11,s13\n"
AW "a104,v12\n"
AR "v12,a124\n"
"v_subrev_f32 v12,s13,v12\n"
"v_fma_f32 v12,v12,v11,s13\n"
AW "a124,v12\n"
AR "v12,a144\n"
"v_subrev_f32 v12,s13,v12\n"
"v_fma_f32 v12,v12,v11,s13\n"
AW "a144,v12\n"
AR "v12,a164\n"
"v_subrev_f32 v12,s13,v12\n"
"v_fma_f32 v12,v12,v11,s13\n"
AW "a164,v12\n"
AR "v12,a184\n"
"v_subrev_f32 v12,s13,v12\n"
"v_fma_f32 v12,v12,v11,s13\n"
AW "a184,v12\n"
AR "v12,a204\n"
"v_subrev_f32 v12,s13,v12\n"
"v_fma_f32 v12,v12,v11,s13\n"
AW "a204,v12\n"
AR "v12,a224\n"
"v_subrev_f32 v12,s13,v12\n"
"v_fma_f32 v12,v12,v11,s13\n"
AW "a224,v12\n"
AR "v12,a5\n"
"v_subrev_f32 v12,s13,v12\n"
"v_fma_f32 v12,v12,v11,s13\n"
AW "a5,v12\n"
AR "v12,a25\n"
"v_subrev_f32 v12,s13,v12\n"
"v_fma_f32 v12,v12,v11,s13\n"
AW "a25,v12\n"
AR "v12,a45\n"
"v_subrev_f32 v12,s13,v12\n"
"v_fma_f32 v12,v12,v11,s13\n"
AW "a45,v12\n"
AR "v12,a65\n"
"v_subrev_f32 v12,s13,v12\n"
"v_fma_f32 v12,v12,v11,s13\n"
AW "a65,v12\n"
AR "v12,a85\n"
"v_subrev_f32 v12,s13,v12\n"
"v_fma_f32 v12,v12,v11,s13\n"
AW "a85,v12\n"
AR "v12,a105\n"
"v_subrev_f32 v12,s13,v12\n"
"v_fma_f32 v12,v12,v11,s13\n"
AW "a105,v12\n"
AR "v12,a125\n"
"v_subrev_f32 v12,s13,v12\n"
"v_fma_f32 v12,v12,v11,s13\n"
AW "a125,v12\n"
AR "v12,a145\n"
"v_subrev_f32 v12,s13,v12\n"
"v_fma_f32 v12,v12,v11,s13\n"
AW "a145,v12\n"
AR "v12,a165\n"
"v_subrev_f32 v12,s13,v12\n"
"v_fma_f32 v12,v12,v11,s13\n"
AW "a165,v12\n"
AR "v12,a185\n"
"v_subrev_f32 v12,s13,v12\n"
"v_fma_f32 v12,v12,v11,s13\n"
AW "a185,v12\n"
AR "v12,a205\n"
"v_subrev_f32 v12,s13,v12\n"
"v_fma_f32 v12,v12,v11,s13\n"
AW "a205,v12\n"
AR "v12,a225\n"
"v_subrev_f32 v12,s13,v12\n"
"v_fma_f32 v12,v12,v11,s13\n"
AW "a225,v12\n"
AR "v12,a6\n"
"v_subrev_f32 v12,s13,v12\n"
"v_fma_f32 v12,v12,v11,s13\n"
AW "a6,v12\n"
AR "v12,a26\n"
"v_subrev_f32 v12,s13,v12\n"
"v_fma_f32 v12,v12,v11,s13\n"
AW "a26,v12\n"
AR "v12,a46\n"
"v_subrev_f32 v12,s13,v12\n"
"v_fma_f32 v12,v12,v11,s13\n"
AW "a46,v12\n"
AR "v12,a66\n"
"v_subrev_f32 v12,s13,v12\n"
"v_fma_f32 v12,v12,v11,s13\n"
AW "a66,v12\n"
AR "v12,a86\n"
"v_subrev_f32 v12,s13,v12\n"
"v_fma_f32 v12,v12,v11,s13\n"
AW "a86,v12\n"
AR "v12,a106\n"
"v_subrev_f32 v12,s13,v12\n"
"v_fma_f32 v12,v12,v11,s13\n"
AW "a106,v12\n"
AR "v12,a126\n"
"v_subrev_f32 v12,s13,v12\n"
"v_fma_f32 v12,v12,v11,s13\n"
AW "a126,v12\n"
AR "v12,a146\n"
"v_subrev_f32 v12,s13,v12\n"
"v_fma_f32 v12,v12,v11,s13\n"
AW "a146,v12\n"
AR "v12,a166\n"
"v_subrev_f32 v12,s13,v12\n"
"v_fma_f32 v12,v12,v11,s13\n"
AW "a166,v12\n"
AR "v12,a186\n"
"v_subrev_f32 v12,s13,v12\n"
"v_fma_f32 v12,v12,v11,s13\n"
AW "a186,v12\n"
AR "v12,a206\n"
"v_subrev_f32 v12,s13,v12\n"
"v_fma_f32 v12,v12,v11,s13\n"
AW "a206,v12\n"
AR "v12,a226\n"
"v_subrev_f32 v12,s13,v12\n"
"v_fma_f32 v12,v12,v11,s13\n"
AW "a226,v12\n"
AR "v12,a7\n"
"v_subrev_f32 v12,s13,v12\n"
"v_fma_f32 v12,v12,v11,s13\n"
AW "a7,v12\n"
AR "v12,a27\n"
"v_subrev_f32 v12,s13,v12\n"
"v_fma_f32 v12,v12,v11,s13\n"
AW "a27,v12\n"
AR "v12,a47\n"
"v_subrev_f32 v12,s13,v12\n"
"v_fma_f32 v12,v12,v11,s13\n"
AW "a47,v12\n"
AR "v12,a67\n"
"v_subrev_f32 v12,s13,v12\n"
"v_fma_f32 v12,v12,v11,s13\n"
AW "a67,v12\n"
AR "v12,a87\n"
"v_subrev_f32 v12,s13,v12\n"
"v_fma_f32 v12,v12,v11,s13\n"
AW "a87,v12\n"
AR "v12,a107\n"
"v_subrev_f32 v12,s13,v12\n"
"v_fma_f32 v12,v12,v11,s13\n"
AW "a107,v12\n"
AR "v12,a127\n"
"v_subrev_f32 v12,s13,v12\n"
"v_fma_f32 v12,v12,v11,s13\n"
AW "a127,v12\n"
AR "v12,a147\n"
"v_subrev_f32 v12,s13,v12\n"
"v_fma_f32 v12,v12,v11,s13\n"
AW "a147,v12\n"
AR "v12,a167\n"
"v_subrev_f32 v12,s13,v12\n"
"v_fma_f32 v12,v12,v11,s13\n"
AW "a167,v12\n"
AR "v12,a187\n"
"v_subrev_f32 v12,s13,v12\n"
"v_fma_f32 v12,v12,v11,s13\n"
AW "a187,v12\n"
AR "v12,a207\n"
"v_subrev_f32 v12,s13,v12\n"
"v_fma_f32 v12,v12,v11,s13\n"
AW "a207,v12\n"
AR "v12,a227\n"
"v_subrev_f32 v12,s13,v12\n"
"v_fma_f32 v12,v12,v11,s13\n"
AW "a227,v12\n"
"v_mul_f32 v58,v53,v11\n"
"v_sub_f32 v36,v36,v10\n"
"v_sub_f32 v37,v37,v10\n"
"v_sub_f32 v38,v38,v10\n"
"v_sub_f32 v39,v39,v10\n"
"v_sub_f32 v76,v76,v10\n"
"v_sub_f32 v77,v77,v10\n"
"v_sub_f32 v78,v78,v10\n"
"v_sub_f32 v79,v79,v10\n"
"v_sub_f32 v96,v96,v10\n"
"v_sub_f32 v97,v97,v10\n"
"v_sub_f32 v98,v98,v10\n"
"v_sub_f32 v99,v99,v10\n"
"v_sub_f32 v116,v116,v10\n"
"v_sub_f32 v117,v117,v10\n"
"v_sub_f32 v118,v118,v10\n"
"v_sub_f32 v119,v119,v10\n"
"v_exp_f32 v96,v96\n"
"v_exp_f32 v97,v97\n"
"v_exp_f32 v98,v98\n"
"v_exp_f32 v99,v99\n"
"v_exp_f32 v116,v116\n"
"v_exp_f32 v117,v117\n"
"v_exp_f32 v118,v118\n"
"v_exp_f32 v119,v119\n"
"v_add_f32 v58,v58,v96\n"
"v_add_f32 v58,v58,v97\n"
"v_add_f32 v58,v58,v98\n"
"v_add_f32 v58,v58,v99\n"
"v_add_f32 v58,v58,v116\n"
"v_add_f32 v58,v58,v117\n"
"v_add_f32 v58,v58,v118\n"
"v_add_f32_e64 v58,v58,v119\n"
CV "v96,v96,v97\n"
CV "v97,v98,v99\n"
CV "v98,v116,v117\n"
CV "v99,v118,v119\n"
"v_max3_f32 v10,v100,v101,v102\n"
"v_max3_f32 v11,v103,v120,v121\n"
"v_max3_f32 v10,v10,v122,v123\n"
"v_max_f32 v10,v10,v11\n"
"v_mov_b32 v11,v10\n"
"s_nop 1\n"
"v_permlane16_swap_b32 v10,v11\n"
"v_max_f32 v10,v10,v11\n"
"v_mov_b32 v11,v10\n"
"s_nop 1\n"
"v_permlane32_swap_b32 v10,v11\n"
"v_max_f32 v10,v10,v11\n"
"v_add_f32 v10,0x41300000,v10\n"
"v_max_f32 v10,0,v10\n"
"v_exp_f32 v11,-v10\n"
AR "v12,a8\n"
"v_subrev_f32 v12,s13,v12\n"
"v_fma_f32 v12,v12,v11,s13\n"
AW "a8,v12\n"
AR "v12,a28\n"
"v_subrev_f32 v12,s13,v12\n"
"v_fma_f32 v12,v12,v11,s13\n"
AW "a28,v12\n"
AR "v12,a48\n"
"v_subrev_f32 v12,s13,v12\n"
"v_fma_f32 v12,v12,v11,s13\n"
AW "a48,v12\n"
AR "v12,a68\n"
"v_subrev_f32 v12,s13,v12\n"
"v_fma_f32 v12,v12,v11,s13\n"
AW "a68,v12\n"
AR "v12,a88\n"
"v_subrev_f32 v12,s13,v12\n"
"v_fma_f32 v12,v12,v11,s13\n"
AW "a88,v12\n"
AR "v12,a108\n"
"v_subrev_f32 v12,s13,v12\n"
"v_fma_f32 v12,v12,v11,s13\n"
AW "a108,v12\n"
AR "v12,a128\n"
"v_subrev_f32 v12,s13,v12\n"
"v_fma_f32 v12,v12,v11,s13\n"
AW "a128,v12\n"
AR "v12,a148\n"
"v_subrev_f32 v12,s13,v12\n"
"v_fma_f32 v12,v12,v11,s13\n"
AW "a148,v12\n"
AR "v12,a168\n"
"v_subrev_f32 v12,s13,v12\n"
"v_fma_f32 v12,v12,v11,s13\n"
AW "a168,v12\n"
AR "v12,a188\n"
"v_subrev_f32 v12,s13,v12\n"
"v_fma_f32 v12,v12,v11,s13\n"
AW "a188,v12\n"
AR "v12,a208\n"
"v_subrev_f32 v12,s13,v12\n"
"v_fma_f32 v12,v12,v11,s13\n"
AW "a208,v12\n"
AR "v12,a228\n"
"v_subrev_f32 v12,s13,v12\n"
"v_fma_f32 v12,v12,v11,s13\n"
AW "a228,v12\n"
AR "v12,a9\n"
"v_subrev_f32 v12,s13,v12\n"
"v_fma_f32 v12,v12,v11,s13\n"
AW "a9,v12\n"
AR "v12,a29\n"
"v_subrev_f32 v12,s13,v12\n"
"v_fma_f32 v12,v12,v11,s13\n"
AW "a29,v12\n"
AR "v12,a49\n"
"v_subrev_f32 v12,s13,v12\n"
"v_fma_f32 v12,v12,v11,s13\n"
AW "a49,v12\n"
AR "v12,a69\n"
"v_subrev_f32 v12,s13,v12\n"
"v_fma_f32 v12,v12,v11,s13\n"
AW "a69,v12\n"
AR "v12,a89\n"
"v_subrev_f32 v12,s13,v12\n"
"v_fma_f32 v12,v12,v11,s13\n"
AW "a89,v12\n"
AR "v12,a109\n"
"v_subrev_f32 v12,s13,v12\n"
"v_fma_f32 v12,v12,v11,s13\n"
AW "a109,v12\n"
AR "v12,a129\n"
"v_subrev_f32 v12,s13,v12\n"
"v_fma_f32 v12,v12,v11,s13\n"
AW "a129,v12\n"
AR "v12,a149\n"
"v_subrev_f32 v12,s13,v12\n"
"v_fma_f32 v12,v12,v11,s13\n"
AW "a149,v12\n"
AR "v12,a169\n"
"v_subrev_f32 v12,s13,v12\n"
"v_fma_f32 v12,v12,v11,s13\n"
AW "a169,v12\n"
AR "v12,a189\n"
"v_subrev_f32 v12,s13,v12\n"
"v_fma_f32 v12,v12,v11,s13\n"
AW "a189,v12\n"
AR "v12,a209\n"
"v_subrev_f32 v12,s13,v12\n"
"v_fma_f32 v12,v12,v11,s13\n"
AW "a209,v12\n"
AR "v12,a229\n"
"v_subrev_f32 v12,s13,v12\n"
"v_fma_f32 v12,v12,v11,s13\n"
AW "a229,v12\n"
AR "v12,a10\n"
"v_subrev_f32 v12,s13,v12\n"
"v_fma_f32 v12,v12,v11,s13\n"
AW "a10,v12\n"
AR "v12,a30\n"
"v_subrev_f32 v12,s13,v12\n"
"v_fma_f32 v12,v12,v11,s13\n"
AW "a30,v12\n"
AR "v12,a50\n"
"v_subrev_f32 v12,s13,v12\n"
"v_fma_f32 v12,v12,v11,s13\n"
AW "a50,v12\n"
AR "v12,a70\n"
"v_subrev_f32 v12,s13,v12\n"
"v_fma_f32 v12,v12,v11,s13\n"
AW "a70,v12\n"
AR "v12,a90\n"
"v_subrev_f32 v12,s13,v12\n"
"v_fma_f32 v12,v12,v11,s13\n"
AW "a90,v12\n"
AR "v12,a110\n"
"v_subrev_f32 v12,s13,v12\n"
"v_fma_f32 v12,v12,v11,s13\n"
AW "a110,v12\n"
AR "v12,a130\n"
"v_subrev_f32 v12,s13,v12\n"
"v_fma_f32 v12,v12,v11,s13\n"
AW "a130,v12\n"
AR "v12,a150\n"
"v_subrev_f32 v12,s13,v12\n"
"v_fma_f32 v12,v12,v11,s13\n"
AW "a150,v12\n"
AR "v12,a170\n"
"v_subrev_f32 v12,s13,v12\n"
"v_fma_f32 v12,v12,v11,s13\n"
AW "a170,v12\n"
AR "v12,a190\n"
"v_subrev_f32 v12,s13,v12\n"
"v_fma_f32 v12,v12,v11,s13\n"
AW "a190,v12\n"
AR "v12,a210\n"
"v_subrev_f32 v12,s13,v12\n"
"v_fma_f32 v12,v12,v11,s13\n"
AW "a210,v12\n"
AR "v12,a230\n"
"v_subrev_f32 v12,s13,v12\n"
"v_fma_f32 v12,v12,v11,s13\n"
AW "a230,v12\n"
AR "v12,a11\n"
"v_subrev_f32 v12,s13,v12\n"
"v_fma_f32 v12,v12,v11,s13\n"
AW "a11,v12\n"
AR "v12,a31\n"
"v_subrev_f32 v12,s13,v12\n"
"v_fma_f32 v12,v12,v11,s13\n"
AW "a31,v12\n"
AR "v12,a51\n"
"v_subrev_f32 v12,s13,v12\n"
"v_fma_f32 v12,v12,v11,s13\n"
AW "a51,v12\n"
AR "v12,a71\n"
"v_subrev_f32 v12,s13,v12\n"
"v_fma_f32 v12,v12,v11,s13\n"
AW "a71,v12\n"
AR "v12,a91\n"
"v_subrev_f32 v12,s13,v12\n"
"v_fma_f32 v12,v12,v11,s13\n"
AW "a91,v12\n"
AR "v12,a111\n"
"v_subrev_f32 v12,s13,v12\n"
"v_fma_f32 v12,v12,v11,s13\n"
AW "a111,v12\n"
AR "v12,a131\n"
"v_subrev_f32 v12,s13,v12\n"
"v_fma_f32 v12,v12,v11,s13\n"
AW "a131,v12\n"
AR "v12,a151\n"
"v_subrev_f32 v12,s13,v12\n"
"v_fma_f32 v12,v12,v11,s13\n"
AW "a151,v12\n"
AR "v12,a171\n"
"v_subrev_f32 v12,s13,v12\n"
"v_fma_f32 v12,v12,v11,s13\n"
AW "a171,v12\n"
AR "v12,a191\n"
"v_subrev_f32 v12,s13,v12\n"
"v_fma_f32 v12,v12,v11,s13\n"
AW "a191,v12\n"
AR "v12,a211\n"
"v_subrev_f32 v12,s13,v12\n"
"v_fma_f32 v12,v12,v11,s13\n"
AW "a211,v12\n"
AR "v12,a231\n"
"v_subrev_f32 v12,s13,v12\n"
"v_fma_f32 v12,v12,v11,s13\n"
AW "a231,v12\n"
"v_mul_f32 v59,v54,v11\n"
"v_sub_f32 v40,v40,v10\n"
"v_sub_f32 v41,v41,v10\n"
"v_sub_f32 v42,v42,v10\n"
"v_sub_f32 v43,v43,v10\n"
"v_sub_f32 v80,v80,v10\n"
"v_sub_f32 v81,v81,v10\n"
"v_sub_f32 v82,v82,v10\n"
"v_sub_f32 v83,v83,v10\n"
"v_sub_f32 v100,v100,v10\n"
"v_sub_f32 v101,v101,v10\n"
"v_sub_f32 v102,v102,v10\n"
"v_sub_f32 v103,v103,v10\n"
"v_sub_f32 v120,v120,v10\n"
"v_sub_f32 v121,v121,v10\n"
"v_sub_f32 v122,v122,v10\n"
"v_sub_f32 v123,v123,v10\n"
"v_exp_f32 v100,v100\n"
"v_exp_f32 v101,v101\n"
"v_exp_f32 v102,v102\n"
"v_exp_f32 v103,v103\n"
"v_exp_f32 v120,v120\n"
"v_exp_f32 v121,v121\n"
"v_exp_f32 v122,v122\n"
"v_exp_f32 v123,v123\n"
"v_add_f32 v59,v59,v100\n"
"v_add_f32 v59,v59,v101\n"
"v_add_f32 v59,v59,v102\n"
"v_add_f32 v59,v59,v103\n"
"v_add_f32 v59,v59,v120\n"
"v_add_f32 v59,v59,v121\n"
"v_add_f32 v59,v59,v122\n"
"v_add_f32_e64 v59,v59,v123\n"
CV "v100,v100,v101\n"
CV "v101,v102,v103\n"
CV "v102,v120,v121\n"
CV "v103,v122,v123\n"
"v_max3_f32 v10,v104,v105,v106\n"
"v_max3_f32 v11,v107,v124,v125\n"
"v_max3_f32 v10,v10,v126,v127\n"
"v_max_f32 v10,v10,v11\n"
"v_mov_b32 v11,v10\n"
"s_nop 1\n"
"v_permlane16_swap_b32 v10,v11\n"
"v_max_f32 v10,v10,v11\n"
"v_mov_b32 v11,v10\n"
"s_nop 1\n"
"v_permlane32_swap_b32 v10,v11\n"
"v_max_f32_e64 v10,v10,v11\n"
"v_add_f32 v10,0x41300000,v10\n"
"v_max_f32 v10,0,v10\n"
"v_exp_f32 v11,-v10\n"
AR "v12,a12\n"
"v_subrev_f32 v12,s13,v12\n"
"v_fma_f32 v12,v12,v11,s13\n"
AW "a12,v12\n"
AR "v12,a32\n"
"v_subrev_f32 v12,s13,v12\n"
"v_fma_f32 v12,v12,v11,s13\n"
AW "a32,v12\n"
AR "v12,a52\n"
"v_subrev_f32 v12,s13,v12\n"
"v_fma_f32 v12,v12,v11,s13\n"
AW "a52,v12\n"
AR "v12,a72\n"
"v_subrev_f32 v12,s13,v12\n"
"v_fma_f32 v12,v12,v11,s13\n"
AW "a72,v12\n"
AR "v12,a92\n"
"v_subrev_f32 v12,s13,v12\n"
"v_fma_f32 v12,v12,v11,s13\n"
AW "a92,v12\n"
AR "v12,a112\n"
"v_subrev_f32 v12,s13,v12\n"
"v_fma_f32 v12,v12,v11,s13\n"
AW "a112,v12\n"
AR "v12,a132\n"
"v_subrev_f32 v12,s13,v12\n"
"v_fma_f32 v12,v12,v11,s13\n"
AW "a132,v12\n"
AR "v12,a152\n"
"v_subrev_f32 v12,s13,v12\n"
"v_fma_f32 v12,v12,v11,s13\n"
AW "a152,v12\n"
AR "v12,a172\n"
"v_subrev_f32 v12,s13,v12\n"
"v_fma_f32 v12,v12,v11,s13\n"
AW "a172,v12\n"
AR "v12,a192\n"
"v_subrev_f32 v12,s13,v12\n"
"v_fma_f32 v12,v12,v11,s13\n"
AW "a192,v12\n"
AR "v12,a212\n"
"v_subrev_f32 v12,s13,v12\n"
"v_fma_f32 v12,v12,v11,s13\n"
AW "a212,v12\n"
AR "v12,a232\n"
"v_subrev_f32 v12,s13,v12\n"
"v_fma_f32 v12,v12,v11,s13\n"
AW "a232,v12\n"
AR "v12,a13\n"
"v_subrev_f32 v12,s13,v12\n"
"v_fma_f32 v12,v12,v11,s13\n"
AW "a13,v12\n"
AR "v12,a33\n"
"v_subrev_f32 v12,s13,v12\n"
"v_fma_f32 v12,v12,v11,s13\n"
AW "a33,v12\n"
AR "v12,a53\n"
"v_subrev_f32 v12,s13,v12\n"
"v_fma_f32 v12,v12,v11,s13\n"
AW "a53,v12\n"
AR "v12,a73\n"
"v_subrev_f32 v12,s13,v12\n"
"v_fma_f32 v12,v12,v11,s13\n"
AW "a73,v12\n"
AR "v12,a93\n"
"v_subrev_f32 v12,s13,v12\n"
"v_fma_f32 v12,v12,v11,s13\n"
AW "a93,v12\n"
AR "v12,a113\n"
"v_subrev_f32 v12,s13,v12\n"
"v_fma_f32 v12,v12,v11,s13\n"
AW "a113,v12\n"
AR "v12,a133\n"
"v_subrev_f32 v12,s13,v12\n"
"v_fma_f32 v12,v12,v11,s13\n"
AW "a133,v12\n"
AR "v12,a153\n"
"v_subrev_f32 v12,s13,v12\n"
"v_fma_f32 v12,v12,v11,s13\n"
AW "a153,v12\n"
AR "v12,a173\n"
"v_subrev_f32 v12,s13,v12\n"
"v_fma_f32 v12,v12,v11,s13\n"
AW "a173,v12\n"
AR "v12,a193\n"
"v_subrev_f32 v12,s13,v12\n"
"v_fma_f32 v12,v12,v11,s13\n"
AW "a193,v12\n"
AR "v12,a213\n"
"v_subrev_f32 v12,s13,v12\n"
"v_fma_f32 v12,v12,v11,s13\n"
AW "a213,v12\n"
AR "v12,a233\n"
"v_subrev_f32 v12,s13,v12\n"
"v_fma_f32 v12,v12,v11,s13\n"
AW "a233,v12\n"
AR "v12,a14\n"
"v_subrev_f32 v12,s13,v12\n"
"v_fma_f32 v12,v12,v11,s13\n"
AW "a14,v12\n"
AR "v12,a34\n"
"v_subrev_f32 v12,s13,v12\n"
"v_fma_f32 v12,v12,v11,s13\n"
AW "a34,v12\n"
AR "v12,a54\n"
"v_subrev_f32 v12,s13,v12\n"
"v_fma_f32 v12,v12,v11,s13\n"
AW "a54,v12\n"
AR "v12,a74\n"
"v_subrev_f32 v12,s13,v12\n"
"v_fma_f32 v12,v12,v11,s13\n"
AW "a74,v12\n"
AR "v12,a94\n"
"v_subrev_f32 v12,s13,v12\n"
"v_fma_f32 v12,v12,v11,s13\n"
AW "a94,v12\n"
AR "v12,a114\n"
"v_subrev_f32 v12,s13,v12\n"
"v_fma_f32 v12,v12,v11,s13\n"
AW "a114,v12\n"
AR "v12,a134\n"
"v_subrev_f32 v12,s13,v12\n"
"v_fma_f32 v12,v12,v11,s13\n"
AW "a134,v12\n"
AR "v12,a154\n"
"v_subrev_f32 v12,s13,v12\n"
"v_fma_f32 v12,v12,v11,s13\n"
AW "a154,v12\n"
AR "v12,a174\n"
"v_subrev_f32 v12,s13,v12\n"
"v_fma_f32 v12,v12,v11,s13\n"
AW "a174,v12\n"
AR "v12,a194\n"
"v_subrev_f32 v12,s13,v12\n"
"v_fma_f32 v12,v12,v11,s13\n"
AW "a194,v12\n"
AR "v12,a214\n"
"v_subrev_f32 v12,s13,v12\n"
"v_fma_f32 v12,v12,v11,s13\n"
AW "a214,v12\n"
AR "v12,a234\n"
"v_subrev_f32 v12,s13,v12\n"
"v_fma_f32 v12,v12,v11,s13\n"
AW "a234,v12\n"
AR "v12,a15\n"
"v_subrev_f32 v12,s13,v12\n"
"v_fma_f32 v12,v12,v11,s13\n"
AW "a15,v12\n"
AR "v12,a35\n"
"v_subrev_f32 v12,s13,v12\n"
"v_fma_f32 v12,v12,v11,s13\n"
AW "a35,v12\n"
AR "v12,a55\n"
"v_subrev_f32 v12,s13,v12\n"
"v_fma_f32 v12,v12,v11,s13\n"
AW "a55,v12\n"
AR "v12,a75\n"
"v_subrev_f32 v12,s13,v12\n"
"v_fma_f32 v12,v12,v11,s13\n"
AW "a75,v12\n"
AR "v12,a95\n"
"v_subrev_f32 v12,s13,v12\n"
"v_fma_f32 v12,v12,v11,s13\n"
AW "a95,v12\n"
AR "v12,a115\n"
"v_subrev_f32 v12,s13,v12\n"
"v_fma_f32 v12,v12,v11,s13\n"
AW "a115,v12\n"
AR "v12,a135\n"
"v_subrev_f32 v12,s13,v12\n"
"v_fma_f32 v12,v12,v11,s13\n"
AW "a135,v12\n"
AR "v12,a155\n"
"v_subrev_f32 v12,s13,v12\n"
"v_fma_f32 v12,v12,v11,s13\n"
AW "a155,v12\n"
AR "v12,a175\n"
"v_subrev_f32 v12,s13,v12\n"
"v_fma_f32 v12,v12,v11,s13\n"
AW "a175,v12\n"
AR "v12,a195\n"
"v_subrev_f32 v12,s13,v12\n"
"v_fma_f32 v12,v12,v11,s13\n"
AW "a195,v12\n"
AR "v12,a215\n"
"v_subrev_f32 v12,s13,v12\n"
"v_fma_f32 v12,v12,v11,s13\n"
AW "a215,v12\n"
AR "v12,a235\n"
"v_subrev_f32 v12,s13,v12\n"
"v_fma_f32 v12,v12,v11,s13\n"
AW "a235,v12\n"
"v_mul_f32 v60,v55,v11\n"
"v_sub_f32 v44,v44,v10\n"
"v_sub_f32 v45,v45,v10\n"
"v_sub_f32 v46,v46,v10\n"
"v_sub_f32 v47,v47,v10\n"
"v_sub_f32 v84,v84,v10\n"
"v_sub_f32 v85,v85,v10\n"
"v_sub_f32 v86,v86,v10\n"
"v_sub_f32 v87,v87,v10\n"
"v_sub_f32 v104,v104,v10\n"
"v_sub_f32 v105,v105,v10\n"
"v_sub_f32 v106,v106,v10\n"
"v_sub_f32 v107,v107,v10\n"
"v_sub_f32 v124,v124,v10\n"
"v_sub_f32 v125,v125,v10\n"
"v_sub_f32 v126,v126,v10\n"
"v_sub_f32 v127,v127,v10\n"
"v_exp_f32 v104,v104\n"
"v_exp_f32 v105,v105\n"
"v_exp_f32 v106,v106\n"
"v_exp_f32 v107,v107\n"
"v_exp_f32 v124,v124\n"
"v_exp_f32 v125,v125\n"
"v_exp_f32 v126,v126\n"
"v_exp_f32 v127,v127\n"
"v_add_f32 v60,v60,v104\n"
"v_add_f32 v60,v60,v105\n"
"v_add_f32 v60,v60,v106\n"
"v_add_f32 v60,v60,v107\n"
"v_add_f32 v60,v60,v124\n"
"v_add_f32 v60,v60,v125\n"
"v_add_f32 v60,v60,v126\n"
"v_add_f32 v60,v60,v127\n"
CV "v104,v104,v105\n"
CV "v105,v106,v107\n"
CV "v106,v124,v125\n"
CV "v107,v126,v127\n"
"v_max3_f32 v10,v108,v109,v110\n"
"v_max3_f32 v11,v111,v128,v129\n"
"v_max3_f32 v10,v10,v130,v131\n"
"v_max_f32 v10,v10,v11\n"
"v_mov_b32 v11,v10\n"
"s_nop 1\n"
"v_permlane16_swap_b32 v10,v11\n"
"v_max_f32 v10,v10,v11\n"
"v_mov_b32 v11,v10\n"
"s_nop 1\n"
"v_permlane32_swap_b32 v10,v11\n"
"v_max_f32 v10,v10,v11\n"
"v_add_f32 v10,0x41300000,v10\n"
"v_max_f32 v10,0,v10\n"
"v_exp_f32 v11,-v10\n"
AR "v12,a16\n"
"v_subrev_f32 v12,s13,v12\n"
"v_fma_f32 v12,v12,v11,s13\n"
AW "a16,v12\n"
AR "v12,a36\n"
"v_subrev_f32 v12,s13,v12\n"
"v_fma_f32 v12,v12,v11,s13\n"
AW "a36,v12\n"
AR "v12,a56\n"
"v_subrev_f32 v12,s13,v12\n"
"v_fma_f32 v12,v12,v11,s13\n"
AW "a56,v12\n"
AR "v12,a76\n"
"v_subrev_f32 v12,s13,v12\n"
"v_fma_f32 v12,v12,v11,s13\n"
AW "a76,v12\n"
AR "v12,a96\n"
"v_subrev_f32 v12,s13,v12\n"
"v_fma_f32 v12,v12,v11,s13\n"
AW "a96,v12\n"
AR "v12,a116\n"
"v_subrev_f32 v12,s13,v12\n"
"v_fma_f32 v12,v12,v11,s13\n"
AW "a116,v12\n"
AR "v12,a136\n"
"v_subrev_f32 v12,s13,v12\n"
"v_fma_f32 v12,v12,v11,s13\n"
AW "a136,v12\n"
AR "v12,a156\n"
"v_subrev_f32 v12,s13,v12\n"
"v_fma_f32 v12,v12,v11,s13\n"
AW "a156,v12\n"
AR "v12,a176\n"
"v_subrev_f32 v12,s13,v12\n"
"v_fma_f32 v12,v12,v11,s13\n"
AW "a176,v12\n"
AR "v12,a196\n"
"v_subrev_f32 v12,s13,v12\n"
"v_fma_f32 v12,v12,v11,s13\n"
AW "a196,v12\n"
AR "v12,a216\n"
"v_subrev_f32 v12,s13,v12\n"
"v_fma_f32 v12,v12,v11,s13\n"
AW "a216,v12\n"
AR "v12,a236\n"
"v_subrev_f32 v12,s13,v12\n"
"v_fma_f32 v12,v12,v11,s13\n"
AW "a236,v12\n"
AR "v12,a17\n"
"v_subrev_f32 v12,s13,v12\n"
"v_fma_f32 v12,v12,v11,s13\n"
AW "a17,v12\n"
AR "v12,a37\n"
"v_subrev_f32 v12,s13,v12\n"
"v_fma_f32 v12,v12,v11,s13\n"
AW "a37,v12\n"
AR "v12,a57\n"
"v_subrev_f32 v12,s13,v12\n"
"v_fma_f32 v12,v12,v11,s13\n"
AW "a57,v12\n"
AR "v12,a77\n"
"v_subrev_f32 v12,s13,v12\n"
"v_fma_f32 v12,v12,v11,s13\n"
AW "a77,v12\n"
AR "v12,a97\n"
"v_subrev_f32 v12,s13,v12\n"
"v_fma_f32 v12,v12,v11,s13\n"
AW "a97,v12\n"
AR "v12,a117\n"
"v_subrev_f32 v12,s13,v12\n"
"v_fma_f32 v12,v12,v11,s13\n"
AW "a117,v12\n"
AR "v12,a137\n"
"v_subrev_f32 v12,s13,v12\n"
"v_fma_f32 v12,v12,v11,s13\n"
AW "a137,v12\n"
AR "v12,a157\n"
"v_subrev_f32 v12,s13,v12\n"
"v_fma_f32 v12,v12,v11,s13\n"
AW "a157,v12\n"
AR "v12,a177\n"
"v_subrev_f32 v12,s13,v12\n"
"v_fma_f32 v12,v12,v11,s13\n"
AW "a177,v12\n"
AR "v12,a197\n"
"v_subrev_f32 v12,s13,v12\n"
"v_fma_f32 v12,v12,v11,s13\n"
AW "a197,v12\n"
AR "v12,a217\n"
"v_subrev_f32 v12,s13,v12\n"
"v_fma_f32 v12,v12,v11,s13\n"
AW "a217,v12\n"
AR "v12,a237\n"
"v_subrev_f32 v12,s13,v12\n"
"v_fma_f32 v12,v12,v11,s13\n"
AW "a237,v12\n"
AR "v12,a18\n"
"v_subrev_f32 v12,s13,v12\n"
"v_fma_f32 v12,v12,v11,s13\n"
AW "a18,v12\n"
AR "v12,a38\n"
"v_subrev_f32 v12,s13,v12\n"
"v_fma_f32 v12,v12,v11,s13\n"
AW "a38,v12\n"
AR "v12,a58\n"
"v_subrev_f32 v12,s13,v12\n"
"v_fma_f32 v12,v12,v11,s13\n"
AW "a58,v12\n"
AR "v12,a78\n"
"v_subrev_f32 v12,s13,v12\n"
"v_fma_f32 v12,v12,v11,s13\n"
AW "a78,v12\n"
AR "v12,a98\n"
"v_subrev_f32 v12,s13,v12\n"
"v_fma_f32 v12,v12,v11,s13\n"
AW "a98,v12\n"
AR "v12,a118\n"
"v_subrev_f32 v12,s13,v12\n"
"v_fma_f32 v12,v12,v11,s13\n"
AW "a118,v12\n"
AR "v12,a138\n"
"v_subrev_f32 v12,s13,v12\n"
"v_fma_f32 v12,v12,v11,s13\n"
AW "a138,v12\n"
AR "v12,a158\n"
"v_subrev_f32 v12,s13,v12\n"
"v_fma_f32 v12,v12,v11,s13\n"
AW "a158,v12\n"
AR "v12,a178\n"
"v_subrev_f32 v12,s13,v12\n"
"v_fma_f32 v12,v12,v11,s13\n"
AW "a178,v12\n"
AR "v12,a198\n"
"v_subrev_f32 v12,s13,v12\n"
"v_fma_f32 v12,v12,v11,s13\n"
AW "a198,v12\n"
AR "v12,a218\n"
"v_subrev_f32 v12,s13,v12\n"
"v_fma_f32 v12,v12,v11,s13\n"
AW "a218,v12\n"
AR "v12,a238\n"
"v_subrev_f32 v12,s13,v12\n"
"v_fma_f32 v12,v12,v11,s13\n"
AW "a238,v12\n"
AR "v12,a19\n"
"v_subrev_f32 v12,s13,v12\n"
"v_fma_f32 v12,v12,v11,s13\n"
AW "a19,v12\n"
AR "v12,a39\n"
"v_subrev_f32 v12,s13,v12\n"
"v_fma_f32 v12,v12,v11,s13\n"
AW "a39,v12\n"
AR "v12,a59\n"
"v_subrev_f32 v12,s13,v12\n"
"v_fma_f32 v12,v12,v11,s13\n"
AW "a59,v12\n"
AR "v12,a79\n"
"v_subrev_f32 v12,s13,v12\n"
"v_fma_f32 v12,v12,v11,s13\n"
AW "a79,v12\n"
AR "v12,a99\n"
"v_subrev_f32 v12,s13,v12\n"
"v_fma_f32 v12,v12,v11,s13\n"
AW "a99,v12\n"
AR "v12,a119\n"
"v_subrev_f32 v12,s13,v12\n"
"v_fma_f32 v12,v12,v11,s13\n"
AW "a119,v12\n"
AR "v12,a139\n"
"v_subrev_f32 v12,s13,v12\n"
"v_fma_f32 v12,v12,v11,s13\n"
AW "a139,v12\n"
AR "v12,a159\n"
"v_subrev_f32 v12,s13,v12\n"
"v_fma_f32 v12,v12,v11,s13\n"
AW "a159,v12\n"
AR "v12,a179\n"
"v_subrev_f32 v12,s13,v12\n"
"v_fma_f32 v12,v12,v11,s13\n"
AW "a179,v12\n"
AR "v12,a199\n"
"v_subrev_f32 v12,s13,v12\n"
"v_fma_f32 v12,v12,v11,s13\n"
AW "a199,v12\n"
AR "v12,a219\n"
"v_subrev_f32 v12,s13,v12\n"
"v_fma_f32 v12,v12,v11,s13\n"
AW "a219,v12\n"
AR "v12,a239\n"
"v_subrev_f32 v12,s13,v12\n"
"v_fma_f32 v12,v12,v11,s13\n"
AW "a239,v12\n"
"v_mul_f32 v61,v56,v11\n"
"v_sub_f32 v48,v48,v10\n"
"v_sub_f32 v49,v49,v10\n"
"v_sub_f32 v50,v50,v10\n"
"v_sub_f32 v51,v51,v10\n"
"v_sub_f32 v88,v88,v10\n"
"v_sub_f32 v89,v89,v10\n"
"v_sub_f32 v90,v90,v10\n"
"v_sub_f32 v91,v91,v10\n"
"v_sub_f32 v108,v108,v10\n"
"v_sub_f32 v109,v109,v10\n"
"v_sub_f32 v110,v110,v10\n"
"v_sub_f32 v111,v111,v10\n"
"v_sub_f32 v128,v128,v10\n"
"v_sub_f32 v129,v129,v10\n"
"v_sub_f32 v130,v130,v10\n"
"v_sub_f32 v131,v131,v10\n"
"v_exp_f32 v108,v108\n"
"v_exp_f32 v109,v109\n"
"v_exp_f32 v110,v110\n"
"v_exp_f32 v111,v111\n"
"v_exp_f32 v128,v128\n"
"v_exp_f32 v129,v129\n"
"v_exp_f32 v130,v130\n"
"v_exp_f32 v131,v131\n"
"v_add_f32 v61,v61,v108\n"
"v_add_f32 v61,v61,v109\n"
"v_add_f32 v61,v61,v110\n"
"v_add_f32 v61,v61,v111\n"
"v_add_f32 v61,v61,v128\n"
"v_add_f32 v61,v61,v129\n"
"v_add_f32 v61,v61,v130\n"
"v_add_f32_e64 v61,v61,v131\n"
CV "v108,v108,v109\n"
CV "v109,v110,v111\n"
CV "v110,v128,v129\n"
CV "v111,v130,v131\n"
"s_nop 7\n"
"s_setpc_b64 s[52:53]\n"
".Lackspin_q:\n"
"s_cmp_eq_u32 s98,0\n"
"s_cbranch_scc1 .Lackok_q\n"
"s_sub_u32 s98,s98,1\n"
"s_sleep 8\n"
"v_bfe_u32 v14,v9,4,5\n"
"v_lshlrev_b32 v14,6,v14\n"
"s_lshl_b32 s58,s78,2\n"
"s_add_u32 s58,s58,s86\n"
"buffer_load_dword v10,v14,s[40:43],s58 offen offset:128 sc1\n"
"s_waitcnt vmcnt(0)\n"
"s_branch .Lack_q\n"
".Lcode1:\n"
".Ldone:\n"
    : "+{v0}"(tid), "+{s23}"(bid), "+{s[20:21]}"(ka)
    :
    : "v1", "v2", "v3", "v4", "v5", "v6", "v7", "v8", "v9", "v10", "v11", "v12", "v13", "v14", "v15", "v16", "v17", "v18", "v19", "v20", "v21", "v22", "v23", "v24", "v25", "v26", "v27", "v28", "v29", "v30", "v31", "v32", "v33", "v34", "v35", "v36", "v37", "v38", "v39", "v40", "v41", "v42", "v43", "v44", "v45", "v46", "v47", "v48", "v49", "v50", "v51", "v52", "v53", "v54", "v55", "v56", "v57", "v58", "v59", "v60", "v61", "v62", "v63", "v64", "v65", "v66", "v67", "v68", "v69", "v70", "v71", "v72", "v73", "v74", "v75", "v76", "v77", "v78", "v79", "v80", "v81", "v82", "v83", "v84", "v85", "v86", "v87", "v88", "v89", "v90", "v91", "v92", "v93", "v94", "v95", "v96", "v97", "v98", "v99", "v100", "v101", "v102", "v103", "v104", "v105", "v106", "v107", "v108", "v109", "v110", "v111", "v112", "v113", "v114", "v115", "v116", "v117", "v118", "v119", "v120", "v121", "v122", "v123", "v124", "v125", "v126", "v127", "v128", "v129", "v130", "v131", "v132", "v133", "v134", "v135", "v136", "v137", "v138", "v139", "v140", "v141", "v142", "v143", "v144", "v145", "v146", "v147", "v148", "v149", "v150", "v151", "v152", "v153", "v154", "v155", "v156", "v157", "v158", "v159", "v160", "v161", "v162", "v163", "v164", "v165", "v166", "v167", "v168", "v169", "v170", "v171", "v172", "v173", "v174", "v175", "v176", "v177", "v178", "v179", "v180", "v181", "v182", "v183", "v184", "v185", "v186", "v187", "v188", "v189", "v190", "v191", "v192", "v193", "v194", "v195", "v196", "v197", "v198", "v199", "v200", "v201", "v202", "v203", "v204", "v205", "v206", "v207", "v208", "v209", "v210", "v211", "v212", "v213", "v214", "v215", "v216", "v217", "v218", "v219", "v220", "v221", "v222", "v223", "v224", "v225", "v226", "v227", "v228", "v229", "v230", "v231", "v232", "v233", "v234", "v235", "v236", "v237", "v238", "v239", "v240", "v241", "v242", "v243", "v244", "v245", "v246", "v247", "v248", "v249", "v250", "v251", "v252", "v253", "v254", "v255", "a0", "a1", "a2", "a3", "a4", "a5", "a6", "a7", "a8", "a9", "a10", "a11", "a12", "a13", "a14", "a15", "a16", "a17", "a18", "a19", "a20", "a21", "a22", "a23", "a24", "a25", "a26", "a27", "a28", "a29", "a30", "a31", "a32", "a33", "a34", "a35", "a36", "a37", "a38", "a39", "a40", "a41", "a42", "a43", "a44", "a45", "a46", "a47", "a48", "a49", "a50", "a51", "a52", "a53", "a54", "a55", "a56", "a57", "a58", "a59", "a60", "a61", "a62", "a63", "a64", "a65", "a66", "a67", "a68", "a69", "a70", "a71", "a72", "a73", "a74", "a75", "a76", "a77", "a78", "a79", "a80", "a81", "a82", "a83", "a84", "a85", "a86", "a87", "a88", "a89", "a90", "a91", "a92", "a93", "a94", "a95", "a96", "a97", "a98", "a99", "a100", "a101", "a102", "a103", "a104", "a105", "a106", "a107", "a108", "a109", "a110", "a111", "a112", "a113", "a114", "a115", "a116", "a117", "a118", "a119", "a120", "a121", "a122", "a123", "a124", "a125", "a126", "a127", "a128", "a129", "a130", "a131", "a132", "a133", "a134", "a135", "a136", "a137", "a138", "a139", "a140", "a141", "a142", "a143", "a144", "a145", "a146", "a147", "a148", "a149", "a150", "a151", "a152", "a153", "a154", "a155", "a156", "a157", "a158", "a159", "a160", "a161", "a162", "a163", "a164", "a165", "a166", "a167", "a168", "a169", "a170", "a171", "a172", "a173", "a174", "a175", "a176", "a177", "a178", "a179", "a180", "a181", "a182", "a183", "a184", "a185", "a186", "a187", "a188", "a189", "a190", "a191", "a192", "a193", "a194", "a195", "a196", "a197", "a198", "a199", "a200", "a201", "a202", "a203", "a204", "a205", "a206", "a207", "a208", "a209", "a210", "a211", "a212", "a213", "a214", "a215", "a216", "a217", "a218", "a219", "a220", "a221", "a222", "a223", "a224", "a225", "a226", "a227", "a228", "a229", "a230", "a231", "a232", "a233", "a234", "a235", "a236", "a237", "a238", "a239", "a240", "a241", "a242", "a243", "a244", "a245", "a246", "a247", "a248", "a249", "a250", "a251", "a252", "a253", "a254", "a255", "s0", "s1", "s2", "s3", "s4", "s5", "s6", "s7", "s8", "s9", "s10", "s11", "s12", "s13", "s14", "s15", "s16", "s17", "s18", "s19", "s22", "s24", "s25", "s26", "s27", "s28", "s29", "s30", "s31", "s32", "s33", "s34", "s35", "s36", "s37", "s38", "s39", "s40", "s41", "s42", "s43", "s44", "s45", "s46", "s47", "s48", "s49", "s50", "s51", "s52", "s53", "s54", "s55", "s56", "s57", "s58", "s59", "s60", "s61", "s62", "s63", "s64", "s65", "s66", "s67", "s68", "s69", "s70", "s71", "s72", "s73", "s74", "s75", "s76", "s77", "s78", "s79", "s80", "s81", "s82", "s83", "s84", "s85", "s86", "s87", "s88", "s89", "s90", "s91", "s92", "s93", "s94", "s95", "s96", "s97", "s98", "s99", "s100", "s101", "vcc", "scc", "memory");
}

extern "C" void kernel_launch(void* const* d_in, const int* in_sizes, int n_in, void* d_out, int out_size, void* d_ws, size_t ws_size, hipStream_t stream) {
  Ptrs P;
  for (int br = 0; br < 2; ++br) { P.x[br] = d_in[5 * br]; P.k[br] = d_in[5 * br + 1]; P.v[br] = d_in[5 * br + 2]; P.wq[br] = d_in[5 * br + 3]; P.wo[br] = d_in[5 * br + 4]; }
  (void)hipFuncSetAttribute((const void*)fused_kernel, hipFuncAttributeMaxDynamicSharedMemorySize, LDS1);
  fused_kernel<<<256, 256, LDS1, stream>>>(P, (char*)d_ws, (char*)d_out);
}
```

```cpp
#include <hip/hip_runtime.h>
#include <hip/hip_bf16.h>

#define MF "v_mfma_f32_16x16x32_bf16 "
#define TR "ds_read_b64_tr_b16 "
#define AR "v_accvgpr_read_b32 "
#define AW "v_accvgpr_write_b32 "
#define CV "v_cvt_pk_bf16_f32 "
#define LD "buffer_load_dwordx4 "
constexpr unsigned LDS1 = 160 * 1024;

struct Ptrs { const void* x[2]; const void* k[2]; const void* v[2]; const void* wq[2]; const void* wo[2]; };

__global__ __launch_bounds__(256, 1) void fused_kernel(Ptrs P, char* __restrict__ ws, char* __restrict__ out) {
  unsigned tid = threadIdx.x, bid = blockIdx.x;
  unsigned long long ka = (unsigned long long)__builtin_amdgcn_kernarg_segment_ptr();
  asm volatile(
".p2align 5\n"
".Lcode0:\n"
"s_and_b32 s58,s23,4\n"
"s_lshl_b32 s58,s58,1\n"
"s_load_dwordx2 s[28:29],s[20:21],s58 offset:48\n"
"s_load_dwordx2 s[24:25],s[20:21],s58\n"
"s_load_dwordx2 s[32:33],s[20:21],s58 offset:16\n"
"s_load_dwordx2 s[36:37],s[20:21],s58 offset:32\n"
"s_load_dwordx2 s[48:49],s[20:21],s58 offset:64\n"
"s_memtime s[100:101]\n"
"s_load_dwordx2 s[40:41],s[20:21],0x50\n"
"s_load_dwordx2 s[44:45],s[20:21],0x58\n"
"v_readfirstlane_b32 s22,v0\n"
"v_and_b32 v9,63,v0\n"
"v_bfe_u32 v10,v0,3,3\n"
"v_and_b32 v11,7,v0\n"
"s_lshr_b32 s22,s22,6\n"
"s_or_b32 s22,s22,s58\n"
"s_and_b32 s58,s23,1\n"
"s_lshl_b32 s58,s58,3\n"
"s_lshr_b32 s59,s23,5\n"
"s_add_u32 s59,s59,s58\n"
"s_mul_i32 s59,s59,4112\n"
"s_or_b32 s22,s22,s59\n"
"s_and_b32 s59,s23,2\n"
"s_lshl_b32 s59,s59,1\n"
"s_bfe_u32 s60,s23,0x20003\n"
"s_or_b32 s59,s59,s60\n"
"s_lshl_b32 s60,s59,20\n"
"s_or_b32 s22,s22,s60\n"
"s_or_b32 s59,s59,s58\n"
"s_mul_i32 s59,s59,65792\n"
"s_xor_b32 s59,s59,524288\n"
"s_or_b32 s22,s22,s59\n"
"v_lshrrev_b32 v6,2,v0\n"
"v_and_b32 v6,4,v6\n"
"v_xor_b32 v6,v6,v11\n"
"v_lshlrev_b32 v6,4,v6\n"
"s_mov_b32 s58,12288\n"
"v_mad_u32_u24 v6,v10,s58,v6\n"
"v_bfe_u32 v4,v0,4,3\n"
"v_xor_b32 v4,v4,v11\n"
"v_lshlrev_b32 v4,4,v4\n"
"s_mov_b32 s58,1280\n"
"v_mad_u32_u24 v4,v10,s58,v4\n"
"v_lshlrev_b32 v9,4,v9\n"
"s_mov_b32 s26,3276800\n"
"s_mov_b32 s27,0x00020000\n"
"s_mov_b32 s30,7864320\n"
"s_mov_b32 s31,0x00020000\n"
"s_mov_b32 s34,0\n"
"s_mov_b32 s35,0x00020000\n"
"s_mov_b32 s38,0\n"
"s_mov_b32 s39,0x00020000\n"
"s_mov_b32 s42,268435456\n"
"s_mov_b32 s43,0x00020000\n"
"s_mov_b32 s46,6553600\n"
"s_mov_b32 s47,0x00020000\n"
"s_mov_b32 s50,7864320\n"
"s_mov_b32 s51,0x00020000\n"
"s_mov_b32 s98,20000\n"
"s_bfe_u32 s56,s22,0x20000\n"
"s_bfe_u32 s72,s22,0x10003\n"
"s_lshr_b32 s73,s56,1\n"
"s_and_b32 s74,s56,1\n"
"s_mul_i32 s62,s56,4096\n"
"s_mul_i32 s63,s56,5120\n"
"s_add_u32 s63,s63,98304\n"
"s_bfe_u32 s58,s22,0x40004\n"
"s_bfe_u32 s59,s22,0x40008\n"
"s_mul_i32 s68,s58,204800\n"
"s_mul_i32 s60,s56,51200\n"
"s_add_u32 s68,s68,s60\n"
"s_lshl_b32 s59,s59,1\n"
"s_add_u32 s59,s59,s73\n"
"s_mul_i32 s66,s59,384\n"
"s_mul_i32 s60,s74,393216\n"
"s_add_u32 s66,s66,s60\n"
"s_lshr_b32 s58,s22,6\n"
"s_xor_b32 s58,s58,s22\n"
"s_bfe_u32 s58,s58,0x10004\n"
"s_mul_i32 s81,s58,640\n"
"s_mul_i32 s60,s81,6144\n"
"s_add_u32 s66,s66,s60\n"
"v_xor_b32 v19,64,v4\n"
"v_add_u32 v16,97280,v6\n"
"v_add_u32 v17,194560,v6\n"
"v_add_u32 v18,291840,v6\n"
"s_waitcnt lgkmcnt(0)\n"
"s_mov_b32 s80,s100\n"
"s_mov_b32 s87,2\n"
"s_add_u32 m0,s62,0\n"
"s_add_u32 s82,s66,0\n"
"s_sub_u32 s15,s82,7864320\n"
"s_min_u32 s82,s82,s15\n"
LD "v6,s[28:31],s82 offen lds\n"
LD "v16,s[28:31],s82 offen offset:1024 lds\n"
LD "v17,s[28:31],s82 offen offset:2048 lds\n"
LD "v18,s[28:31],s82 offen offset:3072 lds\n"
"s_add_u32 s14,s81,0\n"
"s_sub_u32 s15,s14,1280\n"
"s_min_u32 s14,s14,s15\n"
"s_add_u32 s14,s14,s68\n"
"s_add_u32 s84,s14,0\n"
"s_add_u32 m0,s63,0\n"
"s_nop 0\n"
LD "v4,s[24:27],s84 offen lds\n"
"s_add_u32 s84,s14,9216\n"
LD "v19,s[24:27],s84 offen offset:1024 lds\n"
"s_add_u32 s84,s14,18432\n"
LD "v4,s[24:27],s84 offen offset:2048 lds\n"
"s_add_u32 s84,s14,27648\n"
LD "v19,s[24:27],s84 offen offset:3072 lds\n"
"s_add_u32 s84,s14,40960\n"
"s_add_u32 m0,s63,4096\n"
"s_nop 0\n"
LD "v4,s[24:27],s84 offen lds\n"
"s_lshr_b32 s58,s23,3\n"
"s_mul_i32 s59,s58,7\n"
"s_and_b32 s60,s23,7\n"
"s_mul_i32 s60,s60,3\n"
"s_add_u32 s59,s59,s60\n"
"s_and_b32 s59,s59,15\n"
"s_lshl_b32 s59,s59,8\n"
"s_or_b32 s58,s58,s59\n"
"s_and_b32 s59,s23,3\n"
"s_lshl_b32 s59,s59,5\n"
"s_or_b32 s58,s58,s59\n"
"s_bfe_u32 s59,s23,0x10001\n"
"s_lshl_b32 s59,s59,12\n"
"s_or_b32 s58,s58,s59\n"
"s_lshl_b32 s23,s23,16\n"
"s_or_b32 s23,s23,s58\n"
"v_bfe_u32 v20,v0,4,2\n"
"v_mul_u32_u24 v21,384,v10\n"
"v_xor_b32_e64 v22,v11,v20\n"
"v_lshl_add_u32 v7,v22,4,v21\n"
"s_add_u32 m0,s62,16384\n"
"s_add_u32 s82,s66,128\n"
"s_sub_u32 s15,s82,7864320\n"
"s_min_u32 s82,s82,s15\n"
LD "v6,s[28:31],s82 offen lds\n"
"s_lshl_b32 s58,s74,6\n"
"v_xor_b32 v7,s58,v7\n"
"v_lshlrev_b32 v22,1,v20\n"
"v_xor_b32 v22,v11,v22\n"
"v_lshl_add_u32 v8,v22,4,v21\n"
"v_bfe_u32 v21,v0,1,3\n"
"v_xor_b32 v21,v21,v20\n"
"v_lshlrev_b32_e64 v21,4,v21\n"
"v_lshl_add_u32 v21,v11,7,v21\n"
"v_bfe_u32 v22,v0,3,1\n"
"s_mul_i32 s58,s73,12288\n"
"v_mul_u32_u24 v23,3072,v22\n"
"v_add3_u32 v1,v23,v21,s58\n"
"s_mul_i32 s59,s74,10240\n"
"s_add_u32 s59,s59,98304\n"
"v_lshl_add_u32 v2,v22,10,v21\n"
"v_add_u32 v2,s59,v2\n"
"v_bfe_u32 v21,v0,2,3\n"
"v_lshlrev_b32 v21,7,v21\n"
"v_bfe_u32 v23,v0,3,2\n"
LD "v16,s[28:31],s82 offen offset:1024 lds\n"
"v_lshl_add_u32 v21,v23,5,v21\n"
"v_and_b32_e64 v23,3,v0\n"
"v_lshl_add_u32 v21,v23,3,v21\n"
"v_bfe_u32 v23,v0,5,1\n"
"v_mul_u32_u24 v3,3072,v23\n"
"v_add3_u32 v3,v3,v21,s58\n"
"s_bfe_u32 s58,s22,0x40004\n"
"s_mul_i32 s58,s58,409600\n"
"s_mul_i32 s59,s72,1280\n"
"s_add_u32 s58,s58,s59\n"
"v_and_b32 v21,15,v0\n"
"v_mul_u32_u24 v21,2560,v21\n"
"v_lshl_add_u32 v21,v23,4,v21\n"
"v_bfe_u32 v23,v0,4,1\n"
"v_lshl_add_u32 v21,v23,5,v21\n"
"v_add_u32 v12,s58,v21\n"
"v_bfe_u32 v21,v0,2,2\n"
"v_lshlrev_b32 v21,7,v21\n"
"v_lshl_add_u32 v21,v20,10,v21\n"
"v_lshl_add_u32 v21,v22,6,v21\n"
LD "v17,s[28:31],s82 offen offset:2048 lds\n"
"v_and_b32_e64 v23,3,v0\n"
"v_lshl_add_u32 v21,v23,4,v21\n"
"s_lshl_b32 s58,s73,13\n"
"v_add_u32 v0,s58,v21\n"
"s_getpc_b64 s[52:53]\n"
".Lcts:\n"
"s_sub_u32 s52,s52,.Lcts-.Lcode0\n"
"s_subb_u32 s53,s53,0\n"
"s_bfe_u32 s58,s23,0x20000\n"
"s_lshl_b32 s58,s58,2\n"
"s_add_u32 s58,s58,s56\n"
"s_lshl_b32 s58,s58,13\n"
"v_lshlrev_b32 v15,3,v9\n"
"v_add_u32_e64 v15,s58,v15\n"
"v_min_u32 v15,.Lcode1-.Lcode0,v15\n"
"s_bfe_u32 s58,s22,0x4000c\n"
"s_bfe_u32 s59,s22,0x40010\n"
"s_mul_i32 s69,s58,204800\n"
"s_mul_i32 s60,s56,51200\n"
LD "v18,s[28:31],s82 offen offset:3072 lds\n"
"s_add_u32 s69,s69,s60\n"
"s_lshl_b32 s59,s59,1\n"
"s_add_u32 s59,s59,s73\n"
"s_mul_i32 s67,s59,384\n"
"s_mul_i32 s60,s74,393216\n"
"s_add_u32 s67,s67,s60\n"
"s_mul_i32 s60,s81,6144\n"
"s_add_u32 s67,s67,s60\n"
"s_bfe_u32 s58,s22,0x40004\n"
"s_bfe_u32 s59,s22,0x40008\n"
"s_lshl_b32 s59,s59,1\n"
"s_add_u32 s88,s59,s74\n"
"s_mul_i32 s88,s88,245760\n"
"s_add_u32 s59,s59,s73\n"
"s_lshl_b32 s70,s58,5\n"
"s_add_u32 s70,s70,s59\n"
"s_mul_i32 s70,s70,196608\n"
"s_bfe_u32 s58,s22,0x4000c\n"
"s_bfe_u32 s59,s22,0x40010\n"
"s_lshl_b32 s59,s59,1\n"
"s_add_u32 m0,s62,32768\n"
"s_add_u32 s82,s66,256\n"
"s_sub_u32 s15,s82,7864320\n"
"s_min_u32 s82,s82,s15\n"
LD "v6,s[28:31],s82 offen lds\n"
"s_add_u32 s89,s59,s74\n"
"s_mul_i32 s89,s89,245760\n"
"s_add_u32 s59,s59,s73\n"
"s_lshl_b32 s71,s58,5\n"
"s_add_u32 s71,s71,s59\n"
"s_mul_i32 s71,s71,196608\n"
"s_bitcmp1_b32 s23,12\n"
"s_cselect_b32 s58,s71,s70\n"
"s_cselect_b32 s71,s70,s71\n"
"s_mov_b32 s70,s58\n"
"s_cselect_b32 s58,s89,s88\n"
"s_cselect_b32 s89,s88,s89\n"
"s_mov_b32 s88,s58\n"
"s_cselect_b32 s69,98,1\n"
"s_mov_b32 s54,1037384597\n"
"s_mov_b32 s55,1037384597\n"
"s_bfe_u32 s58,s22,0x40004\n"
"s_lshl_b32 s59,s72,4\n"
"s_add_u32 s59,s59,s58\n"
"s_lshl_b32 s86,s59,12\n"
LD "v16,s[28:31],s82 offen offset:1024 lds\n"
"s_add_u32 s86,s86,100663296\n"
"s_mul_i32 s93,s59,1638400\n"
"s_add_u32 s93,s93,33554432\n"
"s_bfe_u32 s58,s22,0x30014\n"
"s_lshl_b32 s78,s58,1\n"
"s_add_u32 s78,s78,s74\n"
"s_lshl_b32 s94,s78,2\n"
"s_add_u32 s94,s94,s86\n"
"s_lshl_b32 s92,s58,2\n"
"s_lshr_b32 s92,0x31752064,s92\n"
"s_and_b32 s92,s92,7\n"
"s_lshl_b32 s92,s92,2\n"
"s_add_u32 s92,s92,s56\n"
"s_mul_i32 s91,s58,204800\n"
"s_add_u32 s91,s91,s93\n"
"s_mul_i32 s75,s56,30720\n"
"s_add_u32 s75,s75,s91\n"
"s_add_u32 s75,s75,0\n"
"s_mul_i32 s58,s74,5120\n"
"s_add_u32 s91,s91,s58\n"
LD "v17,s[28:31],s82 offen offset:2048 lds\n"
"s_mov_b32 s90,0xf0000000\n"
"s_mov_b32 s77,0xf0000000\n"
"s_mov_b32 s85,0xf0000000\n"
AW "a240,v0\n"
AW "a244,v1\n"
AW "a241,v2\n"
AW "a245,v3\n"
AW "a242,v4\n"
AW "a243,v6\n"
AW "a252,v12\n"
"s_mov_b32 s57,0\n"
"s_mov_b32 s83,0\n"
LD "v18,s[28:31],s82 offen offset:3072 lds\n"
"global_load_dword a249,v15,s[52:53]\n"
AR "v0,a240\n"
AR "v2,a241\n"
AR "v4,a242\n"
AR "v6,a243\n"
"v_xor_b32 v1,64,v0\n"
"v_xor_b32 v3,64,v2\n"
"v_xor_b32 v5,64,v4\n"
"v_add_u32 v11,97280,v6\n"
"v_add_u32 v12,194560,v6\n"
"v_add_u32 v13,291840,v6\n"
"s_mov_b64 vcc,0\n"
"s_waitcnt vmcnt(9) lgkmcnt(0)\n"
"s_barrier\n"
TR "a[200:201],v0\n"
TR "a[202:203],v0 offset:512\n"
TR "a[204:205],v0 offset:4096\n"
TR "a[206:207],v0 offset:4608\n"
"ds_read_b128 a[60:63],v2\n"
"ds_read_b128 a[100:103],v3\n"
"ds_read_b128 a[64:67],v2 offset:2048\n"
"ds_read_b128 a[104:107],v3 offset:2048\n"
"ds_read_b128 a[68:71],v2 offset:4096\n"
"ds_read_b128 a[108:111],v3 offset:4096\n"
"ds_read_b128 a[72:75],v2 offset:6144\n"
"ds_read_b128 a[112:115],v3 offset:6144\n"
"ds_read_b128 a[76:79],v2 offset:8192\n"
"ds_read_b128 a[116:119],v3 offset:8192\n"
TR "a[208:209],v0 offset:8\n"
TR "a[210:211],v0 offset:520\n"
TR "a[212:213],v0 offset:4104\n"
TR "a[214:215],v0 offset:4616\n"
"s_waitcnt lgkmcnt(0)\n"
MF "v[16:19],a[200:203],a[60:63],0\n"
TR "a[216:217],v1\n"
MF "v[16:19],a[204:207],a[100:103],v[16:19]\n"
"s_add_u32 s14,s81,128\n"
TR "a[218:219],v1 offset:512\n"
MF "v[24:27],a[204:207],a[104:107],0\n"
"s_sub_u32 s15,s14,1280\n"
"v_lshrrev_b32 v15,5,v9\n"
"v_min_u32 v15,11,v15\n"
MF "v[24:27],a[200:203],a[64:67],v[24:27]\n"
"s_min_u32 s14,s14,s15\n"
TR "a[220:221],v1 offset:4096\n"
"v_mul_u32_u24 v15,160,v15\n"
MF "v[32:35],a[200:203],a[68:71],0\n"
"s_add_u32 s14,s14,s68\n"
TR "a[222:223],v1 offset:4608\n"
"v_and_or_b32 v15,v9,16,v15\n"
MF "v[32:35],a[204:207],a[108:111],v[32:35]\n"
"s_add_u32 s84,s14,0\n"
"v_max3_f32 v220,|v16|,|v17|,|v18|\n"
"v_max_f32_e64 v220,v220,|v19|\n"
MF "v[40:43],a[204:207],a[112:115],0\n"
"s_add_u32 m0,s63,20480\n"
"v_readlane_b32 s12,v220,3\n"
"v_readlane_b32 s6,v220,22\n"
MF "v[40:43],a[200:203],a[72:75],v[40:43]\n"
"s_min_u32 s12,s12,s6\n"
"v_readlane_b32 s7,v220,41\n"
"v_readlane_b32 s8,v220,60\n"
MF "v[48:51],a[200:203],a[76:79],0\n"
"s_min_u32 s7,s7,s8\n"
"v_lshlrev_b32 v15,3,v15\n"
MF "v[48:51],a[204:207],a[116:119],v[48:51]\n"
LD "v4,s[24:27],s84 offen lds\n"
MF "v[52:55],a[212:215],a[116:119],0\n"
"s_min_u32 s12,s12,s7\n"
TR "a[200:201],v1 offset:8\n"
MF "v[52:55],a[208:211],a[76:79],v[52:55]\n"
"s_and_b32 s12,s12,0x7f800000\n"
TR "a[202:203],v1 offset:520\n"
MF "v[44:47],a[208:211],a[72:75],0\n"
"s_sub_u32 s12,s12,0x14000000\n"
MF "v[44:47],a[212:215],a[112:115],v[44:47]\n"
"s_cmp_lt_u32 s12,0x50000000\n"
"s_cselect_b32 s12,s12,0xe8400000\n"
TR "a[204:205],v1 offset:4104\n"
MF "v[36:39],a[212:215],a[108:111],0\n"
"s_add_u32 s12,s12,0x17c00000\n"
TR "a[206:207],v1 offset:4616\n"
MF "v[36:39],a[208:211],a[68:71],v[36:39]\n"
"s_bfe_u32 s60,s23,0x70000\n"
"v_mov_b32 v220,s12\n"
"v_mov_b32 v221,s12\n"
MF "v[28:31],a[208:211],a[64:67],0\n"
"s_add_u32 s84,s14,9216\n"
"v_mov_b32 v222,s12\n"
"v_mov_b32 v223,s12\n"
MF "v[28:31],a[212:215],a[104:107],v[28:31]\n"
LD "v5,s[24:27],s84 offen offset:1024 lds\n"
MF "v[20:23],a[212:215],a[100:103],0\n"
"s_lshl_b32 s60,s60,2\n"
MF "v[20:23],a[208:211],a[60:63],v[20:23]\n"
"s_waitcnt vmcnt(7) lgkmcnt(0)\n"
"s_barrier\n"
MF "v[56:59],a[216:219],a[60:63],v[220:223]\n"
"s_add_u32 s60,s60,s56\n"
TR "a[208:209],v0 offset:16384\n"
MF "v[56:59],a[220:223],a[100:103],v[56:59]\n"
"s_add_u32 s84,s14,18432\n"
TR "a[210:211],v0 offset:16896\n"
MF "v[64:67],a[220:223],a[104:107],v[220:223]\n"
LD "v4,s[24:27],s84 offen offset:2048 lds\n"
MF "v[64:67],a[216:219],a[64:67],v[64:67]\n"
"s_mul_i32 s60,s60,15360\n"
TR "a[212:213],v0 offset:20480\n"
MF "v[72:75],a[216:219],a[68:71],v[220:223]\n"
TR "a[214:215],v0 offset:20992\n"
MF "v[72:75],a[220:223],a[108:111],v[72:75]\n"
MF "v[80:83],a[220:223],a[112:115],v[220:223]\n"
"s_add_u32 s84,s14,27648\n"
MF "v[80:83],a[216:219],a[72:75],v[80:83]\n"
LD "v5,s[24:27],s84 offen offset:3072 lds\n"
MF "v[88:91],a[216:219],a[76:79],v[220:223]\n"
MF "v[88:91],a[220:223],a[116:119],v[88:91]\n"
MF "v[92:95],a[204:207],a[116:119],v[220:223]\n"
"s_add_u32 s84,s14,40960\n"
TR "a[216:217],v0 offset:16392\n"
MF "v[92:95],a[200:203],a[76:79],v[92:95]\n"
"s_add_u32 m0,s63,24576\n"
TR "a[218:219],v0 offset:16904\n"
MF "v[84:87],a[200:203],a[72:75],v[220:223]\n"
LD "v4,s[24:27],s84 offen lds\n"
MF "v[84:87],a[204:207],a[112:115],v[84:87]\n"
"s_add_u32 m0,s62,49152\n"
TR "a[220:221],v0 offset:20488\n"
MF "v[76:79],a[204:207],a[108:111],v[220:223]\n"
"s_add_u32 s82,s66,786432\n"
TR "a[222:223],v0 offset:21000\n"
MF "v[76:79],a[200:203],a[68:71],v[76:79]\n"
"s_sub_u32 s15,s82,7864320\n"
MF "v[68:71],a[200:203],a[64:67],v[220:223]\n"
"s_min_u32 s82,s82,s15\n"
MF "v[68:71],a[204:207],a[104:107],v[68:71]\n"
LD "v6,s[28:31],s82 offen lds\n"
MF "v[60:63],a[204:207],a[100:103],v[220:223]\n"
MF "v[60:63],a[200:203],a[60:63],v[60:63]\n"
"s_waitcnt lgkmcnt(4)\n"
MF "v[96:99],a[208:211],a[60:63],v[220:223]\n"
TR "a[200:201],v1 offset:16384\n"
MF "v[96:99],a[212:215],a[100:103],v[96:99]\n"
TR "a[202:203],v1 offset:16896\n"
MF "v[104:107],a[212:215],a[104:107],v[220:223]\n"
LD "v11,s[28:31],s82 offen offset:1024 lds\n"
MF "v[104:107],a[208:211],a[64:67],v[104:107]\n"
TR "a[204:205],v1 offset:20480\n"
MF "v[112:115],a[208:211],a[68:71],v[220:223]\n"
TR "a[206:207],v1 offset:20992\n"
MF "v[112:115],a[212:215],a[108:111],v[112:115]\n"
MF "v[120:123],a[212:215],a[112:115],v[220:223]\n"
MF "v[120:123],a[208:211],a[72:75],v[120:123]\n"
LD "v12,s[28:31],s82 offen offset:2048 lds\n"
MF "v[128:131],a[208:211],a[76:79],v[220:223]\n"
MF "v[128:131],a[212:215],a[116:119],v[128:131]\n"
"s_waitcnt lgkmcnt(4)\n"
MF "v[132:135],a[220:223],a[116:119],v[220:223]\n"
TR "a[208:209],v1 offset:16392\n"
MF "v[132:135],a[216:219],a[76:79],v[132:135]\n"
TR "a[210:211],v1 offset:16904\n"
MF "v[124:127],a[216:219],a[72:75],v[220:223]\n"
LD "v13,s[28:31],s82 offen offset:3072 lds\n"
MF "v[124:127],a[220:223],a[112:115],v[124:127]\n"
"s_add_u32 m0,s62,65536\n"
TR "a[212:213],v1 offset:20488\n"
MF "v[116:119],a[220:223],a[108:111],v[220:223]\n"
"s_add_u32 s82,s66,786560\n"
TR "a[214:215],v1 offset:21000\n"
MF "v[116:119],a[216:219],a[68:71],v[116:119]\n"
"s_sub_u32 s15,s82,7864320\n"
MF "v[108:111],a[216:219],a[64:67],v[220:223]\n"
"s_min_u32 s82,s82,s15\n"
MF "v[108:111],a[220:223],a[104:107],v[108:111]\n"
LD "v6,s[28:31],s82 offen lds\n"
MF "v[100:103],a[220:223],a[100:103],v[220:223]\n"
"s_mov_b64 exec,0\n"
"s_cmp_eq_u32 s56,0\n"
"s_cbranch_scc1 .Lqj\n"
"s_cmp_eq_u32 s56,1\n"
"s_cbranch_scc1 .Lqy\n"
"s_cmp_eq_u32 s56,2\n"
"s_cbranch_scc1 .Lt0ex\n"
"s_branch .Lt0kx\n"
".Lipr1:\n"
"s_mov_b64 exec,-1\n"
MF "v[100:103],a[216:219],a[60:63],v[100:103]\n"
"s_waitcnt vmcnt(5) lgkmcnt(0)\n"
"s_barrier\n"
MF "v[136:139],a[200:203],a[60:63],v[220:223]\n"
TR "a[216:217],v0 offset:32768\n"
MF "v[136:139],a[204:207],a[100:103],v[136:139]\n"
TR "a[218:219],v0 offset:33280\n"
MF "v[144:147],a[204:207],a[104:107],v[220:223]\n"
"ds_read_b128 a[140:143],v2 offset:20480\n"
MF "v[144:147],a[200:203],a[64:67],v[144:147]\n"
TR "a[220:221],v0 offset:36864\n"
MF "v[152:155],a[200:203],a[68:71],v[220:223]\n"
TR "a[222:223],v0 offset:37376\n"
MF "v[152:155],a[204:207],a[108:111],v[152:155]\n"
LD "v11,s[28:31],s82 offen offset:1024 lds\n"
MF "v[160:163],a[204:207],a[112:115],v[220:223]\n"
"ds_read_b128 a[180:183],v3 offset:20480\n"
MF "v[160:163],a[200:203],a[72:75],v[160:163]\n"
MF "v[168:171],a[200:203],a[76:79],v[220:223]\n"
MF "v[168:171],a[204:207],a[116:119],v[168:171]\n"
LD "v12,s[28:31],s82 offen offset:2048 lds\n"
MF "v[172:175],a[212:215],a[116:119],v[220:223]\n"
TR "a[200:201],v0 offset:32776\n"
MF "v[172:175],a[208:211],a[76:79],v[172:175]\n"
TR "a[202:203],v0 offset:33288\n"
MF "v[164:167],a[208:211],a[72:75],v[220:223]\n"
"ds_read_b128 a[144:147],v2 offset:22528\n"
MF "v[164:167],a[212:215],a[112:115],v[164:167]\n"
TR "a[204:205],v0 offset:36872\n"
MF "v[156:159],a[212:215],a[108:111],v[220:223]\n"
TR "a[206:207],v0 offset:37384\n"
MF "v[156:159],a[208:211],a[68:71],v[156:159]\n"
LD "v13,s[28:31],s82 offen offset:3072 lds\n"
MF "v[148:151],a[208:211],a[64:67],v[220:223]\n"
"ds_read_b128 a[184:187],v3 offset:22528\n"
MF "v[148:151],a[212:215],a[104:107],v[148:151]\n"
"s_add_u32 m0,s62,81920\n"
MF "v[140:143],a[212:215],a[100:103],v[220:223]\n"
"s_add_u32 s82,s66,786688\n"
"s_sub_u32 s15,s82,7864320\n"
"s_min_u32 s82,s82,s15\n"
MF "v[140:143],a[208:211],a[60:63],v[140:143]\n"
"s_waitcnt lgkmcnt(7)\n"
LD "v6,s[28:31],s82 offen lds\n"
MF "v[176:179],a[216:219],a[60:63],v[220:223]\n"
TR "a[208:209],v1 offset:32768\n"
MF "v[176:179],a[220:223],a[100:103],v[176:179]\n"
TR "a[210:211],v1 offset:33280\n"
MF "v[184:187],a[220:223],a[104:107],v[220:223]\n"
"ds_read_b128 a[148:151],v2 offset:24576\n"
MF "v[184:187],a[216:219],a[64:67],v[184:187]\n"
TR "a[212:213],v1 offset:36864\n"
MF "v[192:195],a[216:219],a[68:71],v[220:223]\n"
TR "a[214:215],v1 offset:37376\n"
MF "v[192:195],a[220:223],a[108:111],v[192:195]\n"
LD "v11,s[28:31],s82 offen offset:1024 lds\n"
MF "v[200:203],a[220:223],a[112:115],v[220:223]\n"
"ds_read_b128 a[188:191],v3 offset:24576\n"
MF "v[200:203],a[216:219],a[72:75],v[200:203]\n"
MF "v[208:211],a[216:219],a[76:79],v[220:223]\n"
MF "v[208:211],a[220:223],a[116:119],v[208:211]\n"
"s_waitcnt lgkmcnt(7)\n"
LD "v12,s[28:31],s82 offen offset:2048 lds\n"
MF "v[212:215],a[204:207],a[116:119],v[220:223]\n"
TR "a[216:217],v1 offset:32776\n"
MF "v[212:215],a[200:203],a[76:79],v[212:215]\n"
TR "a[218:219],v1 offset:33288\n"
MF "v[204:207],a[200:203],a[72:75],v[220:223]\n"
"ds_read_b128 a[152:155],v2 offset:26624\n"
MF "v[204:207],a[204:207],a[112:115],v[204:207]\n"
TR "a[220:221],v1 offset:36872\n"
MF "v[196:199],a[204:207],a[108:111],v[220:223]\n"
TR "a[222:223],v1 offset:37384\n"
MF "v[196:199],a[200:203],a[68:71],v[196:199]\n"
LD "v13,s[28:31],s82 offen offset:3072 lds\n"
MF "v[188:191],a[200:203],a[64:67],v[220:223]\n"
"v_add_u32 v0,0xc000,v0\n"
MF "v[188:191],a[204:207],a[104:107],v[188:191]\n"
"v_add_u32 v1,0xc000,v1\n"
MF "v[180:183],a[204:207],a[100:103],v[220:223]\n"
"s_add_u32 s14,s81,256\n"
MF "v[180:183],a[200:203],a[60:63],v[180:183]\n"
"s_waitcnt vmcnt(8) lgkmcnt(0)\n"
"s_barrier\n"
MF "v[216:219],a[208:211],a[60:63],v[220:223]\n"
"s_sub_u32 s15,s14,1280\n"
TR "a[200:201],v0\n"
MF "v[216:219],a[212:215],a[100:103],v[216:219]\n"
"s_min_u32 s14,s14,s15\n"
TR "a[202:203],v0 offset:512\n"
MF "v[224:227],a[212:215],a[104:107],v[220:223]\n"
"s_add_u32 s14,s14,s68\n"
"ds_read_b128 a[192:195],v3 offset:26624\n"
MF "v[224:227],a[208:211],a[64:67],v[224:227]\n"
"s_add_u32 s84,s14,0\n"
TR "a[204:205],v0 offset:4096\n"
MF "v[232:235],a[208:211],a[68:71],v[220:223]\n"
"s_add_u32 m0,s63,40960\n"
TR "a[206:207],v0 offset:4608\n"
MF "v[232:235],a[212:215],a[108:111],v[232:235]\n"
LD "v4,s[24:27],s84 offen lds\n"
MF "v[240:243],a[212:215],a[112:115],v[220:223]\n"
"ds_read_b128 a[156:159],v2 offset:28672\n"
MF "v[240:243],a[208:211],a[72:75],v[240:243]\n"
"ds_read_b128 a[196:199],v3 offset:28672\n"
MF "v[248:251],a[208:211],a[76:79],v[220:223]\n"
"s_add_u32 s84,s14,9216\n"
MF "v[248:251],a[212:215],a[116:119],v[248:251]\n"
LD "v5,s[24:27],s84 offen offset:1024 lds\n"
MF "v[252:255],a[220:223],a[116:119],v[220:223]\n"
TR "a[208:209],v0 offset:8\n"
MF "v[252:255],a[216:219],a[76:79],v[252:255]\n"
TR "a[210:211],v0 offset:520\n"
MF "v[244:247],a[216:219],a[72:75],v[220:223]\n"
MF "v[244:247],a[220:223],a[112:115],v[244:247]\n"
TR "a[212:213],v0 offset:4104\n"
MF "v[236:239],a[220:223],a[108:111],v[220:223]\n"
"s_add_u32 s84,s14,18432\n"
TR "a[214:215],v0 offset:4616\n"
MF "v[236:239],a[216:219],a[68:71],v[236:239]\n"
LD "v4,s[24:27],s84 offen offset:2048 lds\n"
MF "v[228:231],a[216:219],a[64:67],v[220:223]\n"
MF "v[228:231],a[220:223],a[104:107],v[228:231]\n"
MF "v[220:223],a[220:223],a[100:103],v[220:223]\n"
"s_add_u32 s84,s14,27648\n"
MF "v[220:223],a[216:219],a[60:63],v[220:223]\n"
"s_waitcnt lgkmcnt(4)\n"
LD "v5,s[24:27],s84 offen offset:3072 lds\n"
MF "v[16:19],a[200:203],a[140:143],v[16:19]\n"
TR "a[216:217],v1\n"
MF "v[16:19],a[204:207],a[180:183],v[16:19]\n"
TR "a[218:219],v1 offset:512\n"
MF "v[24:27],a[204:207],a[184:187],v[24:27]\n"
MF "v[24:27],a[200:203],a[144:147],v[24:27]\n"
"s_add_u32 s84,s14,40960\n"
TR "a[220:221],v1 offset:4096\n"
MF "v[32:35],a[200:203],a[148:151],v[32:35]\n"
"s_add_u32 m0,s63,45056\n"
TR "a[222:223],v1 offset:4608\n"
MF "v[32:35],a[204:207],a[188:191],v[32:35]\n"
LD "v4,s[24:27],s84 offen lds\n"
MF "v[40:43],a[204:207],a[192:195],v[40:43]\n"
MF "v[40:43],a[200:203],a[152:155],v[40:43]\n"
"s_add_u32 m0,s62,0\n"
MF "v[48:51],a[200:203],a[156:159],v[48:51]\n"
"s_add_u32 s82,s66,1572864\n"
"s_sub_u32 s15,s82,7864320\n"
"s_min_u32 s82,s82,s15\n"
MF "v[48:51],a[204:207],a[196:199],v[48:51]\n"
"s_waitcnt lgkmcnt(4)\n"
LD "v6,s[28:31],s82 offen lds\n"
MF "v[52:55],a[212:215],a[196:199],v[52:55]\n"
TR "a[200:201],v1 offset:8\n"
MF "v[52:55],a[208:211],a[156:159],v[52:55]\n"
TR "a[202:203],v1 offset:520\n"
MF "v[44:47],a[208:211],a[152:155],v[44:47]\n"
MF "v[44:47],a[212:215],a[192:195],v[44:47]\n"
TR "a[204:205],v1 offset:4104\n"
MF "v[36:39],a[212:215],a[188:191],v[36:39]\n"
TR "a[206:207],v1 offset:4616\n"
MF "v[36:39],a[208:211],a[148:151],v[36:39]\n"
LD "v11,s[28:31],s82 offen offset:1024 lds\n"
MF "v[28:31],a[208:211],a[144:147],v[28:31]\n"
MF "v[28:31],a[212:215],a[184:187],v[28:31]\n"
MF "v[20:23],a[212:215],a[180:183],v[20:23]\n"
LD "v12,s[28:31],s82 offen offset:2048 lds\n"
MF "v[20:23],a[208:211],a[140:143],v[20:23]\n"
"s_waitcnt vmcnt(12) lgkmcnt(0)\n"
"s_barrier\n"
"v_bfe_u32 v14,v9,4,4\n"
"v_lshlrev_b32 v14,2,v14\n"
"buffer_load_dword v10,v14,s[40:43],s86 offen sc1\n"
".Lhs2:\n"
MF "v[56:59],a[216:219],a[140:143],v[56:59]\n"
TR "a[208:209],v0 offset:16384\n"
MF "v[56:59],a[220:223],a[180:183],v[56:59]\n"
TR "a[210:211],v0 offset:16896\n"
MF "v[64:67],a[220:223],a[184:187],v[64:67]\n"
LD "v13,s[28:31],s82 offen offset:3072 lds\n"
MF "v[64:67],a[216:219],a[144:147],v[64:67]\n"
TR "a[212:213],v0 offset:20480\n"
MF "v[72:75],a[216:219],a[148:151],v[72:75]\n"
TR "a[214:215],v0 offset:20992\n"
MF "v[72:75],a[220:223],a[188:191],v[72:75]\n"
"s_add_u32 s14,s81,384\n"
"s_sub_u32 s15,s14,1280\n"
"s_min_u32 s14,s14,s15\n"
MF "v[80:83],a[220:223],a[192:195],v[80:83]\n"
"s_add_u32 s14,s14,s68\n"
"s_add_u32 s84,s14,0\n"
"s_add_u32 m0,s63,0\n"
MF "v[80:83],a[216:219],a[152:155],v[80:83]\n"
LD "v4,s[24:27],s84 offen lds\n"
MF "v[88:91],a[216:219],a[156:159],v[88:91]\n"
MF "v[88:91],a[220:223],a[196:199],v[88:91]\n"
MF "v[92:95],a[204:207],a[196:199],v[92:95]\n"
TR "a[216:217],v0 offset:16392\n"
MF "v[92:95],a[200:203],a[156:159],v[92:95]\n"
"s_add_u32 s84,s14,9216\n"
TR "a[218:219],v0 offset:16904\n"
MF "v[84:87],a[200:203],a[152:155],v[84:87]\n"
LD "v5,s[24:27],s84 offen offset:1024 lds\n"
MF "v[84:87],a[204:207],a[192:195],v[84:87]\n"
"s_add_u32 m0,s62,16384\n"
TR "a[220:221],v0 offset:20488\n"
MF "v[76:79],a[204:207],a[188:191],v[76:79]\n"
"s_add_u32 s82,s66,1572992\n"
TR "a[222:223],v0 offset:21000\n"
MF "v[76:79],a[200:203],a[148:151],v[76:79]\n"
"s_sub_u32 s15,s82,7864320\n"
MF "v[68:71],a[200:203],a[144:147],v[68:71]\n"
"s_min_u32 s82,s82,s15\n"
MF "v[68:71],a[204:207],a[184:187],v[68:71]\n"
LD "v6,s[28:31],s82 offen lds\n"
MF "v[60:63],a[204:207],a[180:183],v[60:63]\n"
MF "v[60:63],a[200:203],a[140:143],v[60:63]\n"
"s_waitcnt lgkmcnt(4)\n"
MF "v[96:99],a[208:211],a[140:143],v[96:99]\n"
TR "a[200:201],v1 offset:16384\n"
MF "v[96:99],a[212:215],a[180:183],v[96:99]\n"
TR "a[202:203],v1 offset:16896\n"
MF "v[104:107],a[212:215],a[184:187],v[104:107]\n"
LD "v11,s[28:31],s82 offen offset:1024 lds\n"
MF "v[104:107],a[208:211],a[144:147],v[104:107]\n"
TR "a[204:205],v1 offset:20480\n"
MF "v[112:115],a[208:211],a[148:151],v[112:115]\n"
TR "a[206:207],v1 offset:20992\n"
MF "v[112:115],a[212:215],a[188:191],v[112:115]\n"
MF "v[120:123],a[212:215],a[192:195],v[120:123]\n"
MF "v[120:123],a[208:211],a[152:155],v[120:123]\n"
LD "v12,s[28:31],s82 offen offset:2048 lds\n"
MF "v[128:131],a[208:211],a[156:159],v[128:131]\n"
MF "v[128:131],a[212:215],a[196:199],v[128:131]\n"
"s_waitcnt lgkmcnt(4)\n"
MF "v[132:135],a[220:223],a[196:199],v[132:135]\n"
TR "a[208:209],v1 offset:16392\n"
MF "v[132:135],a[216:219],a[156:159],v[132:135]\n"
TR "a[210:211],v1 offset:16904\n"
MF "v[124:127],a[216:219],a[152:155],v[124:127]\n"
LD "v13,s[28:31],s82 offen offset:3072 lds\n"
MF "v[124:127],a[220:223],a[192:195],v[124:127]\n"
TR "a[212:213],v1 offset:20488\n"
MF "v[116:119],a[220:223],a[188:191],v[116:119]\n"
TR "a[214:215],v1 offset:21000\n"
MF "v[116:119],a[216:219],a[148:151],v[116:119]\n"
"s_add_u32 s84,s14,20480\n"
MF "v[108:111],a[216:219],a[144:147],v[108:111]\n"
"s_add_u32 m0,s63,2048\n"
MF "v[108:111],a[220:223],a[184:187],v[108:111]\n"
LD "v4,s[24:27],s84 offen lds\n"
MF "v[100:103],a[220:223],a[180:183],v[100:103]\n"
MF "v[100:103],a[216:219],a[140:143],v[100:103]\n"
"s_waitcnt vmcnt(11) lgkmcnt(0)\n"
"s_barrier\n"
MF "v[136:139],a[200:203],a[140:143],v[136:139]\n"
TR "a[216:217],v0 offset:32768\n"
MF "v[136:139],a[204:207],a[180:183],v[136:139]\n"
TR "a[218:219],v0 offset:33280\n"
MF "v[144:147],a[204:207],a[184:187],v[144:147]\n"
"ds_read_b128 a[60:63],v2 offset:40960\n"
MF "v[144:147],a[200:203],a[144:147],v[144:147]\n"
TR "a[220:221],v0 offset:36864\n"
MF "v[152:155],a[200:203],a[148:151],v[152:155]\n"
"s_add_u32 s84,s14,29696\n"
TR "a[222:223],v0 offset:37376\n"
MF "v[152:155],a[204:207],a[188:191],v[152:155]\n"
LD "v5,s[24:27],s84 offen offset:1024 lds\n"
MF "v[160:163],a[204:207],a[192:195],v[160:163]\n"
"ds_read_b128 a[100:103],v3 offset:40960\n"
MF "v[160:163],a[200:203],a[152:155],v[160:163]\n"
"s_add_u32 m0,s62,32768\n"
MF "v[168:171],a[200:203],a[156:159],v[168:171]\n"
"s_add_u32 s82,s66,1573120\n"
"s_sub_u32 s15,s82,7864320\n"
"s_min_u32 s82,s82,s15\n"
MF "v[168:171],a[204:207],a[196:199],v[168:171]\n"
LD "v6,s[28:31],s82 offen lds\n"
MF "v[172:175],a[212:215],a[196:199],v[172:175]\n"
TR "a[200:201],v0 offset:32776\n"
MF "v[172:175],a[208:211],a[156:159],v[172:175]\n"
TR "a[202:203],v0 offset:33288\n"
MF "v[164:167],a[208:211],a[152:155],v[164:167]\n"
"ds_read_b128 a[64:67],v2 offset:43008\n"
MF "v[164:167],a[212:215],a[192:195],v[164:167]\n"
TR "a[204:205],v0 offset:36872\n"
MF "v[156:159],a[212:215],a[188:191],v[156:159]\n"
TR "a[206:207],v0 offset:37384\n"
MF "v[156:159],a[208:211],a[148:151],v[156:159]\n"
LD "v11,s[28:31],s82 offen offset:1024 lds\n"
MF "v[148:151],a[208:211],a[144:147],v[148:151]\n"
"ds_read_b128 a[104:107],v3 offset:43008\n"
MF "v[148:151],a[212:215],a[184:187],v[148:151]\n"
MF "v[140:143],a[212:215],a[180:183],v[140:143]\n"
MF "v[140:143],a[208:211],a[140:143],v[140:143]\n"
"s_waitcnt lgkmcnt(7)\n"
LD "v12,s[28:31],s82 offen offset:2048 lds\n"
MF "v[176:179],a[216:219],a[140:143],v[176:179]\n"
TR "a[208:209],v1 offset:32768\n"
MF "v[176:179],a[220:223],a[180:183],v[176:179]\n"
TR "a[210:211],v1 offset:33280\n"
MF "v[184:187],a[220:223],a[184:187],v[184:187]\n"
"ds_read_b128 a[68:71],v2 offset:45056\n"
MF "v[184:187],a[216:219],a[144:147],v[184:187]\n"
TR "a[212:213],v1 offset:36864\n"
MF "v[192:195],a[216:219],a[148:151],v[192:195]\n"
TR "a[214:215],v1 offset:37376\n"
MF "v[192:195],a[220:223],a[188:191],v[192:195]\n"
LD "v13,s[28:31],s82 offen offset:3072 lds\n"
MF "v[200:203],a[220:223],a[192:195],v[200:203]\n"
"ds_read_b128 a[108:111],v3 offset:45056\n"
MF "v[200:203],a[216:219],a[152:155],v[200:203]\n"
"s_add_u32 s84,s14,40960\n"
MF "v[208:211],a[216:219],a[156:159],v[208:211]\n"
"s_add_u32 m0,s63,4096\n"
MF "v[208:211],a[220:223],a[196:199],v[208:211]\n"
"s_waitcnt lgkmcnt(7)\n"
LD "v4,s[24:27],s84 offen lds\n"
MF "v[212:215],a[204:207],a[196:199],v[212:215]\n"
"s_sub_u32 s66,s66,s83\n"
TR "a[216:217],v1 offset:32776\n"
MF "v[212:215],a[200:203],a[156:159],v[212:215]\n"
"s_add_u32 m0,s62,49152\n"
TR "a[218:219],v1 offset:33288\n"
MF "v[204:207],a[200:203],a[152:155],v[204:207]\n"
"s_add_u32 s82,s66,2359296\n"
"ds_read_b128 a[72:75],v2 offset:47104\n"
MF "v[204:207],a[204:207],a[192:195],v[204:207]\n"
"s_sub_u32 s15,s82,7864320\n"
TR "a[220:221],v1 offset:36872\n"
MF "v[196:199],a[204:207],a[188:191],v[196:199]\n"
"s_min_u32 s82,s82,s15\n"
TR "a[222:223],v1 offset:37384\n"
MF "v[196:199],a[200:203],a[148:151],v[196:199]\n"
LD "v6,s[28:31],s82 offen lds\n"
MF "v[188:191],a[200:203],a[144:147],v[188:191]\n"
"v_add_u32 v0,0xffff4000,v0\n"
MF "v[188:191],a[204:207],a[184:187],v[188:191]\n"
"v_add_u32 v1,0xffff4000,v1\n"
MF "v[180:183],a[204:207],a[180:183],v[180:183]\n"
LD "v11,s[28:31],s82 offen offset:1024 lds\n"
MF "v[180:183],a[200:203],a[140:143],v[180:183]\n"
"s_waitcnt vmcnt(15) lgkmcnt(0)\n"
"s_barrier\n"
MF "v[216:219],a[208:211],a[140:143],v[216:219]\n"
TR "a[200:201],v0\n"
MF "v[216:219],a[212:215],a[180:183],v[216:219]\n"
TR "a[202:203],v0 offset:512\n"
MF "v[224:227],a[212:215],a[184:187],v[224:227]\n"
"ds_read_b128 a[112:115],v3 offset:47104\n"
MF "v[224:227],a[208:211],a[144:147],v[224:227]\n"
TR "a[204:205],v0 offset:4096\n"
MF "v[232:235],a[208:211],a[148:151],v[232:235]\n"
TR "a[206:207],v0 offset:4608\n"
MF "v[232:235],a[212:215],a[188:191],v[232:235]\n"
LD "v12,s[28:31],s82 offen offset:2048 lds\n"
MF "v[240:243],a[212:215],a[192:195],v[240:243]\n"
"ds_read_b128 a[76:79],v2 offset:49152\n"
MF "v[240:243],a[208:211],a[152:155],v[240:243]\n"
"ds_read_b128 a[116:119],v3 offset:49152\n"
MF "v[248:251],a[208:211],a[156:159],v[248:251]\n"
MF "v[248:251],a[212:215],a[196:199],v[248:251]\n"
LD "v13,s[28:31],s82 offen offset:3072 lds\n"
MF "v[252:255],a[220:223],a[196:199],v[252:255]\n"
TR "a[208:209],v0 offset:8\n"
MF "v[252:255],a[216:219],a[156:159],v[252:255]\n"
TR "a[210:211],v0 offset:520\n"
MF "v[244:247],a[216:219],a[152:155],v[244:247]\n"
"s_add_u32 s14,s81,512\n"
"s_sub_u32 s15,s14,1280\n"
MF "v[244:247],a[220:223],a[192:195],v[244:247]\n"
"s_min_u32 s14,s14,s15\n"
"s_add_u32 s14,s14,s68\n"
TR "a[212:213],v0 offset:4104\n"
MF "v[236:239],a[220:223],a[188:191],v[236:239]\n"
"s_add_u32 s84,s14,0\n"
"s_add_u32 m0,s63,20480\n"
TR "a[214:215],v0 offset:4616\n"
MF "v[236:239],a[216:219],a[148:151],v[236:239]\n"
LD "v4,s[24:27],s84 offen lds\n"
MF "v[228:231],a[216:219],a[144:147],v[228:231]\n"
MF "v[228:231],a[220:223],a[184:187],v[228:231]\n"
MF "v[220:223],a[220:223],a[180:183],v[220:223]\n"
"s_add_u32 s84,s14,9216\n"
MF "v[220:223],a[216:219],a[140:143],v[220:223]\n"
"s_waitcnt lgkmcnt(4)\n"
LD "v5,s[24:27],s84 offen offset:1024 lds\n"
MF "v[16:19],a[200:203],a[60:63],v[16:19]\n"
TR "a[216:217],v1\n"
MF "v[16:19],a[204:207],a[100:103],v[16:19]\n"
"s_add_u32 m0,s62,65536\n"
TR "a[218:219],v1 offset:512\n"
MF "v[24:27],a[204:207],a[104:107],v[24:27]\n"
"s_add_u32 s82,s66,2359424\n"
MF "v[24:27],a[200:203],a[64:67],v[24:27]\n"
"s_sub_u32 s15,s82,7864320\n"
TR "a[220:221],v1 offset:4096\n"
MF "v[32:35],a[200:203],a[68:71],v[32:35]\n"
"s_min_u32 s82,s82,s15\n"
TR "a[222:223],v1 offset:4608\n"
MF "v[32:35],a[204:207],a[108:111],v[32:35]\n"
LD "v6,s[28:31],s82 offen lds\n"
MF "v[40:43],a[204:207],a[112:115],v[40:43]\n"
MF "v[40:43],a[200:203],a[72:75],v[40:43]\n"
MF "v[48:51],a[200:203],a[76:79],v[48:51]\n"
MF "v[48:51],a[204:207],a[116:119],v[48:51]\n"
"s_waitcnt lgkmcnt(4)\n"
LD "v11,s[28:31],s82 offen offset:1024 lds\n"
MF "v[52:55],a[212:215],a[116:119],v[52:55]\n"
TR "a[200:201],v1 offset:8\n"
MF "v[52:55],a[208:211],a[76:79],v[52:55]\n"
TR "a[202:203],v1 offset:520\n"
MF "v[44:47],a[208:211],a[72:75],v[44:47]\n"
MF "v[44:47],a[212:215],a[112:115],v[44:47]\n"
TR "a[204:205],v1 offset:4104\n"
MF "v[36:39],a[212:215],a[108:111],v[36:39]\n"
TR "a[206:207],v1 offset:4616\n"
MF "v[36:39],a[208:211],a[68:71],v[36:39]\n"
LD "v12,s[28:31],s82 offen offset:2048 lds\n"
MF "v[28:31],a[208:211],a[64:67],v[28:31]\n"
MF "v[28:31],a[212:215],a[104:107],v[28:31]\n"
MF "v[20:23],a[212:215],a[100:103],v[20:23]\n"
LD "v13,s[28:31],s82 offen offset:3072 lds\n"
MF "v[20:23],a[208:211],a[60:63],v[20:23]\n"
"s_waitcnt vmcnt(17) lgkmcnt(0)\n"
"s_barrier\n"
MF "v[56:59],a[216:219],a[60:63],v[56:59]\n"
"s_add_u32 s84,s14,20480\n"
TR "a[208:209],v0 offset:16384\n"
MF "v[56:59],a[220:223],a[100:103],v[56:59]\n"
"s_add_u32 m0,s63,22528\n"
TR "a[210:211],v0 offset:16896\n"
MF "v[64:67],a[220:223],a[104:107],v[64:67]\n"
LD "v4,s[24:27],s84 offen lds\n"
MF "v[64:67],a[216:219],a[64:67],v[64:67]\n"
TR "a[212:213],v0 offset:20480\n"
MF "v[72:75],a[216:219],a[68:71],v[72:75]\n"
TR "a[214:215],v0 offset:20992\n"
MF "v[72:75],a[220:223],a[108:111],v[72:75]\n"
MF "v[80:83],a[220:223],a[112:115],v[80:83]\n"
"s_add_u32 s84,s14,29696\n"
MF "v[80:83],a[216:219],a[72:75],v[80:83]\n"
LD "v5,s[24:27],s84 offen offset:1024 lds\n"
MF "v[88:91],a[216:219],a[76:79],v[88:91]\n"
"s_add_u32 m0,s62,81920\n"
MF "v[88:91],a[220:223],a[116:119],v[88:91]\n"
"s_add_u32 s82,s66,2359552\n"
MF "v[92:95],a[204:207],a[116:119],v[92:95]\n"
"s_sub_u32 s15,s82,7864320\n"
TR "a[216:217],v0 offset:16392\n"
MF "v[92:95],a[200:203],a[76:79],v[92:95]\n"
"s_min_u32 s82,s82,s15\n"
TR "a[218:219],v0 offset:16904\n"
MF "v[84:87],a[200:203],a[72:75],v[84:87]\n"
LD "v6,s[28:31],s82 offen lds\n"
MF "v[84:87],a[204:207],a[112:115],v[84:87]\n"
TR "a[220:221],v0 offset:20488\n"
MF "v[76:79],a[204:207],a[108:111],v[76:79]\n"
TR "a[222:223],v0 offset:21000\n"
MF "v[76:79],a[200:203],a[68:71],v[76:79]\n"
MF "v[68:71],a[200:203],a[64:67],v[68:71]\n"
MF "v[68:71],a[204:207],a[104:107],v[68:71]\n"
LD "v11,s[28:31],s82 offen offset:1024 lds\n"
MF "v[60:63],a[204:207],a[100:103],v[60:63]\n"
MF "v[60:63],a[200:203],a[60:63],v[60:63]\n"
"s_waitcnt lgkmcnt(4)\n"
MF "v[96:99],a[208:211],a[60:63],v[96:99]\n"
TR "a[200:201],v1 offset:16384\n"
MF "v[96:99],a[212:215],a[100:103],v[96:99]\n"
TR "a[202:203],v1 offset:16896\n"
MF "v[104:107],a[212:215],a[104:107],v[104:107]\n"
LD "v12,s[28:31],s82 offen offset:2048 lds\n"
MF "v[104:107],a[208:211],a[64:67],v[104:107]\n"
TR "a[204:205],v1 offset:20480\n"
MF "v[112:115],a[208:211],a[68:71],v[112:115]\n"
TR "a[206:207],v1 offset:20992\n"
MF "v[112:115],a[212:215],a[108:111],v[112:115]\n"
MF "v[120:123],a[212:215],a[112:115],v[120:123]\n"
MF "v[120:123],a[208:211],a[72:75],v[120:123]\n"
LD "v13,s[28:31],s82 offen offset:3072 lds\n"
MF "v[128:131],a[208:211],a[76:79],v[128:131]\n"
MF "v[128:131],a[212:215],a[116:119],v[128:131]\n"
"s_waitcnt lgkmcnt(4)\n"
MF "v[132:135],a[220:223],a[116:119],v[132:135]\n"
"s_add_u32 s84,s14,40960\n"
TR "a[208:209],v1 offset:16392\n"
MF "v[132:135],a[216:219],a[76:79],v[132:135]\n"
"s_add_u32 m0,s63,24576\n"
TR "a[210:211],v1 offset:16904\n"
MF "v[124:127],a[216:219],a[72:75],v[124:127]\n"
LD "v4,s[24:27],s84 offen lds\n"
MF "v[124:127],a[220:223],a[112:115],v[124:127]\n"
"s_add_u32 m0,s62,0\n"
TR "a[212:213],v1 offset:20488\n"
MF "v[116:119],a[220:223],a[108:111],v[116:119]\n"
"s_add_u32 s82,s66,3145728\n"
TR "a[214:215],v1 offset:21000\n"
MF "v[116:119],a[216:219],a[68:71],v[116:119]\n"
"s_sub_u32 s15,s82,7864320\n"
MF "v[108:111],a[216:219],a[64:67],v[108:111]\n"
"s_min_u32 s82,s82,s15\n"
MF "v[108:111],a[220:223],a[104:107],v[108:111]\n"
LD "v6,s[28:31],s82 offen lds\n"
MF "v[100:103],a[220:223],a[100:103],v[100:103]\n"
MF "v[100:103],a[216:219],a[60:63],v[100:103]\n"
"s_waitcnt vmcnt(18) lgkmcnt(0)\n"
"s_barrier\n"
MF "v[136:139],a[200:203],a[60:63],v[136:139]\n"
TR "a[216:217],v0 offset:32768\n"
MF "v[136:139],a[204:207],a[100:103],v[136:139]\n"
TR "a[218:219],v0 offset:33280\n"
MF "v[144:147],a[204:207],a[104:107],v[144:147]\n"
"ds_read_b128 a[140:143],v2\n"
MF "v[144:147],a[200:203],a[64:67],v[144:147]\n"
TR "a[220:221],v0 offset:36864\n"
MF "v[152:155],a[200:203],a[68:71],v[152:155]\n"
TR "a[222:223],v0 offset:37376\n"
MF "v[152:155],a[204:207],a[108:111],v[152:155]\n"
LD "v11,s[28:31],s82 offen offset:1024 lds\n"
MF "v[160:163],a[204:207],a[112:115],v[160:163]\n"
"ds_read_b128 a[180:183],v3\n"
MF "v[160:163],a[200:203],a[72:75],v[160:163]\n"
MF "v[168:171],a[200:203],a[76:79],v[168:171]\n"
MF "v[168:171],a[204:207],a[116:119],v[168:171]\n"
LD "v12,s[28:31],s82 offen offset:2048 lds\n"
MF "v[172:175],a[212:215],a[116:119],v[172:175]\n"
TR "a[200:201],v0 offset:32776\n"
MF "v[172:175],a[208:211],a[76:79],v[172:175]\n"
TR "a[202:203],v0 offset:33288\n"
MF "v[164:167],a[208:211],a[72:75],v[164:167]\n"
"ds_read_b128 a[144:147],v2 offset:2048\n"
MF "v[164:167],a[212:215],a[112:115],v[164:167]\n"
TR "a[204:205],v0 offset:36872\n"
MF "v[156:159],a[212:215],a[108:111],v[156:159]\n"
TR "a[206:207],v0 offset:37384\n"
MF "v[156:159],a[208:211],a[68:71],v[156:159]\n"
LD "v13,s[28:31],s82 offen offset:3072 lds\n"
MF "v[148:151],a[208:211],a[64:67],v[148:151]\n"
"ds_read_b128 a[184:187],v3 offset:2048\n"
MF "v[148:151],a[212:215],a[104:107],v[148:151]\n"
"s_add_u32 s14,s81,640\n"
"s_sub_u32 s15,s14,1280\n"
"s_min_u32 s14,s14,s15\n"
MF "v[140:143],a[212:215],a[100:103],v[140:143]\n"
"s_add_u32 s14,s14,s68\n"
"s_add_u32 s84,s14,0\n"
"s_add_u32 m0,s63,40960\n"
MF "v[140:143],a[208:211],a[60:63],v[140:143]\n"
"s_waitcnt lgkmcnt(7)\n"
LD "v4,s[24:27],s84 offen lds\n"
MF "v[176:179],a[216:219],a[60:63],v[176:179]\n"
TR "a[208:209],v1 offset:32768\n"
MF "v[176:179],a[220:223],a[100:103],v[176:179]\n"
TR "a[210:211],v1 offset:33280\n"
MF "v[184:187],a[220:223],a[104:107],v[184:187]\n"
"ds_read_b128 a[148:151],v2 offset:4096\n"
MF "v[184:187],a[216:219],a[64:67],v[184:187]\n"
TR "a[212:213],v1 offset:36864\n"
MF "v[192:195],a[216:219],a[68:71],v[192:195]\n"
"s_add_u32 s84,s14,9216\n"
TR "a[214:215],v1 offset:37376\n"
MF "v[192:195],a[220:223],a[108:111],v[192:195]\n"
LD "v5,s[24:27],s84 offen offset:1024 lds\n"
MF "v[200:203],a[220:223],a[112:115],v[200:203]\n"
"ds_read_b128 a[188:191],v3 offset:4096\n"
MF "v[200:203],a[216:219],a[72:75],v[200:203]\n"
"s_add_u32 m0,s62,16384\n"
MF "v[208:211],a[216:219],a[76:79],v[208:211]\n"
"s_add_u32 s82,s66,3145856\n"
"s_sub_u32 s15,s82,7864320\n"
"s_min_u32 s82,s82,s15\n"
MF "v[208:211],a[220:223],a[116:119],v[208:211]\n"
"s_waitcnt lgkmcnt(7)\n"
LD "v6,s[28:31],s82 offen lds\n"
MF "v[212:215],a[204:207],a[116:119],v[212:215]\n"
TR "a[216:217],v1 offset:32776\n"
MF "v[212:215],a[200:203],a[76:79],v[212:215]\n"
TR "a[218:219],v1 offset:33288\n"
MF "v[204:207],a[200:203],a[72:75],v[204:207]\n"
"ds_read_b128 a[152:155],v2 offset:6144\n"
MF "v[204:207],a[204:207],a[112:115],v[204:207]\n"
TR "a[220:221],v1 offset:36872\n"
MF "v[196:199],a[204:207],a[108:111],v[196:199]\n"
TR "a[222:223],v1 offset:37384\n"
MF "v[196:199],a[200:203],a[68:71],v[196:199]\n"
LD "v11,s[28:31],s82 offen offset:1024 lds\n"
MF "v[188:191],a[200:203],a[64:67],v[188:191]\n"
"v_add_u32 v0,0xc000,v0\n"
MF "v[188:191],a[204:207],a[104:107],v[188:191]\n"
"v_add_u32 v1,0xc000,v1\n"
MF "v[180:183],a[204:207],a[100:103],v[180:183]\n"
LD "v12,s[28:31],s82 offen offset:2048 lds\n"
MF "v[180:183],a[200:203],a[60:63],v[180:183]\n"
"s_waitcnt vmcnt(22) lgkmcnt(0)\n"
"s_barrier\n"
MF "v[216:219],a[208:211],a[60:63],v[216:219]\n"
TR "a[200:201],v0\n"
MF "v[216:219],a[212:215],a[100:103],v[216:219]\n"
TR "a[202:203],v0 offset:512\n"
MF "v[224:227],a[212:215],a[104:107],v[224:227]\n"
"ds_read_b128 a[192:195],v3 offset:6144\n"
MF "v[224:227],a[208:211],a[64:67],v[224:227]\n"
TR "a[204:205],v0 offset:4096\n"
MF "v[232:235],a[208:211],a[68:71],v[232:235]\n"
TR "a[206:207],v0 offset:4608\n"
MF "v[232:235],a[212:215],a[108:111],v[232:235]\n"
LD "v13,s[28:31],s82 offen offset:3072 lds\n"
MF "v[240:243],a[212:215],a[112:115],v[240:243]\n"
"ds_read_b128 a[156:159],v2 offset:8192\n"
MF "v[240:243],a[208:211],a[72:75],v[240:243]\n"
"s_add_u32 s84,s14,20480\n"
"ds_read_b128 a[196:199],v3 offset:8192\n"
MF "v[248:251],a[208:211],a[76:79],v[248:251]\n"
"s_add_u32 m0,s63,43008\n"
MF "v[248:251],a[212:215],a[116:119],v[248:251]\n"
LD "v4,s[24:27],s84 offen lds\n"
MF "v[252:255],a[220:223],a[116:119],v[252:255]\n"
TR "a[208:209],v0 offset:8\n"
MF "v[252:255],a[216:219],a[76:79],v[252:255]\n"
TR "a[210:211],v0 offset:520\n"
MF "v[244:247],a[216:219],a[72:75],v[244:247]\n"
MF "v[244:247],a[220:223],a[112:115],v[244:247]\n"
TR "a[212:213],v0 offset:4104\n"
MF "v[236:239],a[220:223],a[108:111],v[236:239]\n"
"s_add_u32 s84,s14,29696\n"
TR "a[214:215],v0 offset:4616\n"
MF "v[236:239],a[216:219],a[68:71],v[236:239]\n"
LD "v5,s[24:27],s84 offen offset:1024 lds\n"
MF "v[228:231],a[216:219],a[64:67],v[228:231]\n"
MF "v[228:231],a[220:223],a[104:107],v[228:231]\n"
"s_add_u32 m0,s62,32768\n"
MF "v[220:223],a[220:223],a[100:103],v[220:223]\n"
"s_add_u32 s82,s66,3145984\n"
"s_sub_u32 s15,s82,7864320\n"
"s_min_u32 s82,s82,s15\n"
MF "v[220:223],a[216:219],a[60:63],v[220:223]\n"
"s_waitcnt lgkmcnt(4)\n"
LD "v6,s[28:31],s82 offen lds\n"
MF "v[16:19],a[200:203],a[140:143],v[16:19]\n"
TR "a[216:217],v1\n"
MF "v[16:19],a[204:207],a[180:183],v[16:19]\n"
TR "a[218:219],v1 offset:512\n"
MF "v[24:27],a[204:207],a[184:187],v[24:27]\n"
MF "v[24:27],a[200:203],a[144:147],v[24:27]\n"
TR "a[220:221],v1 offset:4096\n"
MF "v[32:35],a[200:203],a[148:151],v[32:35]\n"
TR "a[222:223],v1 offset:4608\n"
MF "v[32:35],a[204:207],a[188:191],v[32:35]\n"
LD "v11,s[28:31],s82 offen offset:1024 lds\n"
MF "v[40:43],a[204:207],a[192:195],v[40:43]\n"
MF "v[40:43],a[200:203],a[152:155],v[40:43]\n"
MF "v[48:51],a[200:203],a[156:159],v[48:51]\n"
MF "v[48:51],a[204:207],a[196:199],v[48:51]\n"
"s_waitcnt lgkmcnt(4)\n"
LD "v12,s[28:31],s82 offen offset:2048 lds\n"
MF "v[52:55],a[212:215],a[196:199],v[52:55]\n"
TR "a[200:201],v1 offset:8\n"
MF "v[52:55],a[208:211],a[156:159],v[52:55]\n"
TR "a[202:203],v1 offset:520\n"
MF "v[44:47],a[208:211],a[152:155],v[44:47]\n"
MF "v[44:47],a[212:215],a[192:195],v[44:47]\n"
TR "a[204:205],v1 offset:4104\n"
MF "v[36:39],a[212:215],a[188:191],v[36:39]\n"
TR "a[206:207],v1 offset:4616\n"
MF "v[36:39],a[208:211],a[148:151],v[36:39]\n"
LD "v13,s[28:31],s82 offen offset:3072 lds\n"
MF "v[28:31],a[208:211],a[144:147],v[28:31]\n"
"s_add_u32 s84,s14,40960\n"
MF "v[28:31],a[212:215],a[184:187],v[28:31]\n"
"s_add_u32 m0,s63,45056\n"
MF "v[20:23],a[212:215],a[180:183],v[20:23]\n"
LD "v4,s[24:27],s84 offen lds\n"
MF "v[20:23],a[208:211],a[140:143],v[20:23]\n"
"s_waitcnt vmcnt(24) lgkmcnt(0)\n"
"s_barrier\n"
AW "a253,v10\n"
"v_add_u32 v10,1,v10\n"
"s_bfe_u32 s58,s22,0x30014\n"
"s_lshl_b32 s58,s58,2\n"
"s_add_u32 s58,s58,s56\n"
"s_lshl_b32 s58,s58,6\n"
"s_add_u32 s58,s58,s86\n"
"v_readlane_b32 s76,v10,s78\n"
"buffer_store_dword v10,v14,s[40:43],s58 offen offset:128 sc1\n"
".Lhs5:\n"
MF "v[56:59],a[216:219],a[140:143],v[56:59]\n"
"s_add_u32 m0,s62,49152\n"
"s_add_u32 s82,s66,3932160\n"
TR "a[208:209],v0 offset:16384\n"
MF "v[56:59],a[220:223],a[180:183],v[56:59]\n"
"s_sub_u32 s15,s82,7864320\n"
"s_min_u32 s82,s82,s15\n"
TR "a[210:211],v0 offset:16896\n"
MF "v[64:67],a[220:223],a[184:187],v[64:67]\n"
LD "v6,s[28:31],s82 offen lds\n"
MF "v[64:67],a[216:219],a[144:147],v[64:67]\n"
TR "a[212:213],v0 offset:20480\n"
MF "v[72:75],a[216:219],a[148:151],v[72:75]\n"
TR "a[214:215],v0 offset:20992\n"
MF "v[72:75],a[220:223],a[188:191],v[72:75]\n"
"buffer_load_dword a249,v15,s[48:51],s60 offen offset:1024\n"
MF "v[80:83],a[220:223],a[192:195],v[80:83]\n"
"s_bfe_u32 s59,s23,0x30003\n"
MF "v[80:83],a[216:219],a[152:155],v[80:83]\n"
LD "v11,s[28:31],s82 offen offset:1024 lds\n"
MF "v[88:91],a[216:219],a[156:159],v[88:91]\n"
"s_add_u32 s59,s59,1\n"
MF "v[88:91],a[220:223],a[196:199],v[88:91]\n"
"s_lshl_b32 s59,s59,7\n"
MF "v[92:95],a[204:207],a[196:199],v[92:95]\n"
"s_and_b32 s59,s59,768\n"
TR "a[216:217],v0 offset:16392\n"
MF "v[92:95],a[200:203],a[156:159],v[92:95]\n"
"s_add_u32 s59,s59,s60\n"
TR "a[218:219],v0 offset:16904\n"
MF "v[84:87],a[200:203],a[152:155],v[84:87]\n"
LD "v12,s[28:31],s82 offen offset:2048 lds\n"
MF "v[84:87],a[204:207],a[192:195],v[84:87]\n"
TR "a[220:221],v0 offset:20488\n"
MF "v[76:79],a[204:207],a[188:191],v[76:79]\n"
TR "a[222:223],v0 offset:21000\n"
MF "v[76:79],a[200:203],a[148:151],v[76:79]\n"
MF "v[68:71],a[200:203],a[144:147],v[68:71]\n"
MF "v[68:71],a[204:207],a[184:187],v[68:71]\n"
LD "v13,s[28:31],s82 offen offset:3072 lds\n"
MF "v[60:63],a[204:207],a[180:183],v[60:63]\n"
MF "v[60:63],a[200:203],a[140:143],v[60:63]\n"
"s_add_u32 s14,s81,768\n"
"s_sub_u32 s15,s14,1280\n"
"s_waitcnt lgkmcnt(4)\n"
MF "v[96:99],a[208:211],a[140:143],v[96:99]\n"
"s_min_u32 s14,s14,s15\n"
"s_add_u32 s14,s14,s68\n"
TR "a[200:201],v1 offset:16384\n"
MF "v[96:99],a[212:215],a[180:183],v[96:99]\n"
"s_add_u32 s84,s14,0\n"
"s_add_u32 m0,s63,0\n"
TR "a[202:203],v1 offset:16896\n"
MF "v[104:107],a[212:215],a[184:187],v[104:107]\n"
LD "v4,s[24:27],s84 offen lds\n"
MF "v[104:107],a[208:211],a[144:147],v[104:107]\n"
TR "a[204:205],v1 offset:20480\n"
MF "v[112:115],a[208:211],a[148:151],v[112:115]\n"
TR "a[206:207],v1 offset:20992\n"
MF "v[112:115],a[212:215],a[188:191],v[112:115]\n"
MF "v[120:123],a[212:215],a[192:195],v[120:123]\n"
"s_add_u32 s84,s14,9216\n"
MF "v[120:123],a[208:211],a[152:155],v[120:123]\n"
LD "v5,s[24:27],s84 offen offset:1024 lds\n"
MF "v[128:131],a[208:211],a[156:159],v[128:131]\n"
MF "v[128:131],a[212:215],a[196:199],v[128:131]\n"
"s_waitcnt lgkmcnt(4)\n"
MF "v[132:135],a[220:223],a[196:199],v[132:135]\n"
TR "a[208:209],v1 offset:16392\n"
MF "v[132:135],a[216:219],a[156:159],v[132:135]\n"
TR "a[210:211],v1 offset:16904\n"
MF "v[124:127],a[216:219],a[152:155],v[124:127]\n"
MF "v[124:127],a[220:223],a[192:195],v[124:127]\n"
TR "a[212:213],v1 offset:20488\n"
MF "v[116:119],a[220:223],a[188:191],v[116:119]\n"
TR "a[214:215],v1 offset:21000\n"
MF "v[116:119],a[216:219],a[148:151],v[116:119]\n"
MF "v[108:111],a[216:219],a[144:147],v[108:111]\n"
MF "v[108:111],a[220:223],a[184:187],v[108:111]\n"
MF "v[100:103],a[220:223],a[180:183],v[100:103]\n"
MF "v[100:103],a[216:219],a[140:143],v[100:103]\n"
"s_waitcnt vmcnt(24) lgkmcnt(0)\n"
"s_barrier\n"
MF "v[136:139],a[200:203],a[140:143],v[136:139]\n"
TR "a[216:217],v0 offset:32768\n"
MF "v[136:139],a[204:207],a[180:183],v[136:139]\n"
"s_add_u32 m0,s62,65536\n"
TR "a[218:219],v0 offset:33280\n"
MF "v[144:147],a[204:207],a[184:187],v[144:147]\n"
"s_add_u32 s82,s66,3932288\n"
"ds_read_b128 a[60:63],v2 offset:20480\n"
MF "v[144:147],a[200:203],a[144:147],v[144:147]\n"
"s_sub_u32 s15,s82,7864320\n"
TR "a[220:221],v0 offset:36864\n"
MF "v[152:155],a[200:203],a[148:151],v[152:155]\n"
"s_min_u32 s82,s82,s15\n"
TR "a[222:223],v0 offset:37376\n"
MF "v[152:155],a[204:207],a[188:191],v[152:155]\n"
LD "v6,s[28:31],s82 offen lds\n"
MF "v[160:163],a[204:207],a[192:195],v[160:163]\n"
"ds_read_b128 a[100:103],v3 offset:20480\n"
MF "v[160:163],a[200:203],a[152:155],v[160:163]\n"
MF "v[168:171],a[200:203],a[156:159],v[168:171]\n"
MF "v[168:171],a[204:207],a[196:199],v[168:171]\n"
LD "v11,s[28:31],s82 offen offset:1024 lds\n"
MF "v[172:175],a[212:215],a[196:199],v[172:175]\n"
TR "a[200:201],v0 offset:32776\n"
MF "v[172:175],a[208:211],a[156:159],v[172:175]\n"
TR "a[202:203],v0 offset:33288\n"
MF "v[164:167],a[208:211],a[152:155],v[164:167]\n"
"ds_read_b128 a[64:67],v2 offset:22528\n"
MF "v[164:167],a[212:215],a[192:195],v[164:167]\n"
TR "a[204:205],v0 offset:36872\n"
MF "v[156:159],a[212:215],a[188:191],v[156:159]\n"
TR "a[206:207],v0 offset:37384\n"
MF "v[156:159],a[208:211],a[148:151],v[156:159]\n"
LD "v12,s[28:31],s82 offen offset:2048 lds\n"
MF "v[148:151],a[208:211],a[144:147],v[148:151]\n"
"ds_read_b128 a[104:107],v3 offset:22528\n"
MF "v[148:151],a[212:215],a[184:187],v[148:151]\n"
MF "v[140:143],a[212:215],a[180:183],v[140:143]\n"
MF "v[140:143],a[208:211],a[140:143],v[140:143]\n"
"s_waitcnt lgkmcnt(7)\n"
LD "v13,s[28:31],s82 offen offset:3072 lds\n"
MF "v[176:179],a[216:219],a[140:143],v[176:179]\n"
TR "a[208:209],v1 offset:32768\n"
MF "v[176:179],a[220:223],a[180:183],v[176:179]\n"
TR "a[210:211],v1 offset:33280\n"
MF "v[184:187],a[220:223],a[184:187],v[184:187]\n"
"ds_read_b128 a[68:71],v2 offset:24576\n"
MF "v[184:187],a[216:219],a[144:147],v[184:187]\n"
"s_add_u32 s84,s14,20480\n"
TR "a[212:213],v1 offset:36864\n"
MF "v[192:195],a[216:219],a[148:151],v[192:195]\n"
"s_add_u32 m0,s63,2048\n"
TR "a[214:215],v1 offset:37376\n"
MF "v[192:195],a[220:223],a[188:191],v[192:195]\n"
LD "v4,s[24:27],s84 offen lds\n"
MF "v[200:203],a[220:223],a[192:195],v[200:203]\n"
"ds_read_b128 a[108:111],v3 offset:24576\n"
MF "v[200:203],a[216:219],a[152:155],v[200:203]\n"
MF "v[208:211],a[216:219],a[156:159],v[208:211]\n"
"s_add_u32 s84,s14,29696\n"
MF "v[208:211],a[220:223],a[196:199],v[208:211]\n"
"s_waitcnt lgkmcnt(7)\n"
LD "v5,s[24:27],s84 offen offset:1024 lds\n"
MF "v[212:215],a[204:207],a[196:199],v[212:215]\n"
TR "a[216:217],v1 offset:32776\n"
MF "v[212:215],a[200:203],a[156:159],v[212:215]\n"
TR "a[218:219],v1 offset:33288\n"
MF "v[204:207],a[200:203],a[152:155],v[204:207]\n"
"ds_read_b128 a[72:75],v2 offset:26624\n"
MF "v[204:207],a[204:207],a[192:195],v[204:207]\n"
TR "a[220:221],v1 offset:36872\n"
MF "v[196:199],a[204:207],a[188:191],v[196:199]\n"
TR "a[222:223],v1 offset:37384\n"
MF "v[196:199],a[200:203],a[148:151],v[196:199]\n"
MF "v[188:191],a[200:203],a[144:147],v[188:191]\n"
"v_add_u32 v0,0xffff4000,v0\n"
MF "v[188:191],a[204:207],a[184:187],v[188:191]\n"
"v_add_u32 v1,0xffff4000,v1\n"
MF "v[180:183],a[204:207],a[180:183],v[180:183]\n"
MF "v[180:183],a[200:203],a[140:143],v[180:183]\n"
"s_waitcnt vmcnt(26) lgkmcnt(0)\n"
"s_barrier\n"
MF "v[216:219],a[208:211],a[140:143],v[216:219]\n"
TR "a[200:201],v0\n"
MF "v[216:219],a[212:215],a[180:183],v[216:219]\n"
"s_add_u32 m0,s62,81920\n"
TR "a[202:203],v0 offset:512\n"
MF "v[224:227],a[212:215],a[184:187],v[224:227]\n"
"s_add_u32 s82,s66,3932416\n"
"ds_read_b128 a[112:115],v3 offset:26624\n"
MF "v[224:227],a[208:211],a[144:147],v[224:227]\n"
"s_sub_u32 s15,s82,7864320\n"
TR "a[204:205],v0 offset:4096\n"
MF "v[232:235],a[208:211],a[148:151],v[232:235]\n"
"s_min_u32 s82,s82,s15\n"
TR "a[206:207],v0 offset:4608\n"
MF "v[232:235],a[212:215],a[188:191],v[232:235]\n"
LD "v6,s[28:31],s82 offen lds\n"
MF "v[240:243],a[212:215],a[192:195],v[240:243]\n"
"ds_read_b128 a[76:79],v2 offset:28672\n"
MF "v[240:243],a[208:211],a[152:155],v[240:243]\n"
"ds_read_b128 a[116:119],v3 offset:28672\n"
MF "v[248:251],a[208:211],a[156:159],v[248:251]\n"
MF "v[248:251],a[212:215],a[196:199],v[248:251]\n"
LD "v11,s[28:31],s82 offen offset:1024 lds\n"
MF "v[252:255],a[220:223],a[196:199],v[252:255]\n"
TR "a[208:209],v0 offset:8\n"
MF "v[252:255],a[216:219],a[156:159],v[252:255]\n"
TR "a[210:211],v0 offset:520\n"
MF "v[244:247],a[216:219],a[152:155],v[244:247]\n"
MF "v[244:247],a[220:223],a[192:195],v[244:247]\n"
TR "a[212:213],v0 offset:4104\n"
MF "v[236:239],a[220:223],a[188:191],v[236:239]\n"
TR "a[214:215],v0 offset:4616\n"
MF "v[236:239],a[216:219],a[148:151],v[236:239]\n"
LD "v12,s[28:31],s82 offen offset:2048 lds\n"
MF "v[228:231],a[216:219],a[144:147],v[228:231]\n"
MF "v[228:231],a[220:223],a[184:187],v[228:231]\n"
MF "v[220:223],a[220:223],a[180:183],v[220:223]\n"
MF "v[220:223],a[216:219],a[140:143],v[220:223]\n"
"s_waitcnt lgkmcnt(4)\n"
LD "v13,s[28:31],s82 offen offset:3072 lds\n"
MF "v[16:19],a[200:203],a[60:63],v[16:19]\n"
TR "a[216:217],v1\n"
MF "v[16:19],a[204:207],a[100:103],v[16:19]\n"
TR "a[218:219],v1 offset:512\n"
MF "v[24:27],a[204:207],a[104:107],v[24:27]\n"
MF "v[24:27],a[200:203],a[64:67],v[24:27]\n"
"s_add_u32 s84,s14,40960\n"
TR "a[220:221],v1 offset:4096\n"
MF "v[32:35],a[200:203],a[68:71],v[32:35]\n"
"s_add_u32 m0,s63,4096\n"
TR "a[222:223],v1 offset:4608\n"
MF "v[32:35],a[204:207],a[108:111],v[32:35]\n"
LD "v4,s[24:27],s84 offen lds\n"
MF "v[40:43],a[204:207],a[112:115],v[40:43]\n"
MF "v[40:43],a[200:203],a[72:75],v[40:43]\n"
MF "v[48:51],a[200:203],a[76:79],v[48:51]\n"
MF "v[48:51],a[204:207],a[116:119],v[48:51]\n"
"s_waitcnt lgkmcnt(4)\n"
MF "v[52:55],a[212:215],a[116:119],v[52:55]\n"
TR "a[200:201],v1 offset:8\n"
MF "v[52:55],a[208:211],a[76:79],v[52:55]\n"
TR "a[202:203],v1 offset:520\n"
MF "v[44:47],a[208:211],a[72:75],v[44:47]\n"
MF "v[44:47],a[212:215],a[112:115],v[44:47]\n"
TR "a[204:205],v1 offset:4104\n"
MF "v[36:39],a[212:215],a[108:111],v[36:39]\n"
TR "a[206:207],v1 offset:4616\n"
MF "v[36:39],a[208:211],a[68:71],v[36:39]\n"
MF "v[28:31],a[208:211],a[64:67],v[28:31]\n"
MF "v[28:31],a[212:215],a[104:107],v[28:31]\n"
MF "v[20:23],a[212:215],a[100:103],v[20:23]\n"
MF "v[20:23],a[208:211],a[60:63],v[20:23]\n"
"s_waitcnt vmcnt(25) lgkmcnt(0)\n"
"s_barrier\n"
MF "v[56:59],a[216:219],a[60:63],v[56:59]\n"
"s_add_u32 m0,s62,0\n"
"s_add_u32 s82,s66,4718592\n"
TR "a[208:209],v0 offset:16384\n"
MF "v[56:59],a[220:223],a[100:103],v[56:59]\n"
"s_sub_u32 s15,s82,7864320\n"
"s_min_u32 s82,s82,s15\n"
TR "a[210:211],v0 offset:16896\n"
MF "v[64:67],a[220:223],a[104:107],v[64:67]\n"
LD "v6,s[28:31],s82 offen lds\n"
MF "v[64:67],a[216:219],a[64:67],v[64:67]\n"
TR "a[212:213],v0 offset:20480\n"
MF "v[72:75],a[216:219],a[68:71],v[72:75]\n"
TR "a[214:215],v0 offset:20992\n"
MF "v[72:75],a[220:223],a[108:111],v[72:75]\n"
"buffer_load_dword a249,v15,s[48:51],s59 offen\n"
MF "v[80:83],a[220:223],a[112:115],v[80:83]\n"
"s_bfe_u32 s59,s23,0x30003\n"
MF "v[80:83],a[216:219],a[72:75],v[80:83]\n"
LD "v11,s[28:31],s82 offen offset:1024 lds\n"
MF "v[88:91],a[216:219],a[76:79],v[88:91]\n"
"s_add_u32 s59,s59,3\n"
MF "v[88:91],a[220:223],a[116:119],v[88:91]\n"
"s_lshl_b32 s59,s59,7\n"
MF "v[92:95],a[204:207],a[116:119],v[92:95]\n"
"s_and_b32 s59,s59,768\n"
TR "a[216:217],v0 offset:16392\n"
MF "v[92:95],a[200:203],a[76:79],v[92:95]\n"
"s_add_u32 s59,s59,s60\n"
TR "a[218:219],v0 offset:16904\n"
MF "v[84:87],a[200:203],a[72:75],v[84:87]\n"
LD "v12,s[28:31],s82 offen offset:2048 lds\n"
MF "v[84:87],a[204:207],a[112:115],v[84:87]\n"
TR "a[220:221],v0 offset:20488\n"
MF "v[76:79],a[204:207],a[108:111],v[76:79]\n"
TR "a[222:223],v0 offset:21000\n"
MF "v[76:79],a[200:203],a[68:71],v[76:79]\n"
MF "v[68:71],a[200:203],a[64:67],v[68:71]\n"
MF "v[68:71],a[204:207],a[104:107],v[68:71]\n"
LD "v13,s[28:31],s82 offen offset:3072 lds\n"
MF "v[60:63],a[204:207],a[100:103],v[60:63]\n"
MF "v[60:63],a[200:203],a[60:63],v[60:63]\n"
"s_add_u32 s14,s81,896\n"
"s_sub_u32 s15,s14,1280\n"
"s_waitcnt lgkmcnt(4)\n"
MF "v[96:99],a[208:211],a[60:63],v[96:99]\n"
"s_min_u32 s14,s14,s15\n"
"s_add_u32 s14,s14,s68\n"
TR "a[200:201],v1 offset:16384\n"
MF "v[96:99],a[212:215],a[100:103],v[96:99]\n"
"s_add_u32 s84,s14,0\n"
"s_add_u32 m0,s63,20480\n"
TR "a[202:203],v1 offset:16896\n"
MF "v[104:107],a[212:215],a[104:107],v[104:107]\n"
LD "v4,s[24:27],s84 offen lds\n"
MF "v[104:107],a[208:211],a[64:67],v[104:107]\n"
TR "a[204:205],v1 offset:20480\n"
MF "v[112:115],a[208:211],a[68:71],v[112:115]\n"
TR "a[206:207],v1 offset:20992\n"
MF "v[112:115],a[212:215],a[108:111],v[112:115]\n"
MF "v[120:123],a[212:215],a[112:115],v[120:123]\n"
"s_add_u32 s84,s14,9216\n"
MF "v[120:123],a[208:211],a[72:75],v[120:123]\n"
LD "v5,s[24:27],s84 offen offset:1024 lds\n"
MF "v[128:131],a[208:211],a[76:79],v[128:131]\n"
MF "v[128:131],a[212:215],a[116:119],v[128:131]\n"
"s_waitcnt lgkmcnt(4)\n"
MF "v[132:135],a[220:223],a[116:119],v[132:135]\n"
TR "a[208:209],v1 offset:16392\n"
MF "v[132:135],a[216:219],a[76:79],v[132:135]\n"
TR "a[210:211],v1 offset:16904\n"
MF "v[124:127],a[216:219],a[72:75],v[124:127]\n"
MF "v[124:127],a[220:223],a[112:115],v[124:127]\n"
TR "a[212:213],v1 offset:20488\n"
MF "v[116:119],a[220:223],a[108:111],v[116:119]\n"
TR "a[214:215],v1 offset:21000\n"
MF "v[116:119],a[216:219],a[68:71],v[116:119]\n"
MF "v[108:111],a[216:219],a[64:67],v[108:111]\n"
MF "v[108:111],a[220:223],a[104:107],v[108:111]\n"
MF "v[100:103],a[220:223],a[100:103],v[100:103]\n"
MF "v[100:103],a[216:219],a[60:63],v[100:103]\n"
"s_waitcnt vmcnt(25) lgkmcnt(0)\n"
"s_barrier\n"
MF "v[136:139],a[200:203],a[60:63],v[136:139]\n"
TR "a[216:217],v0 offset:32768\n"
MF "v[136:139],a[204:207],a[100:103],v[136:139]\n"
"s_add_u32 m0,s62,16384\n"
TR "a[218:219],v0 offset:33280\n"
MF "v[144:147],a[204:207],a[104:107],v[144:147]\n"
"s_add_u32 s82,s66,4718720\n"
"ds_read_b128 a[140:143],v2 offset:40960\n"
MF "v[144:147],a[200:203],a[64:67],v[144:147]\n"
"s_sub_u32 s15,s82,7864320\n"
TR "a[220:221],v0 offset:36864\n"
MF "v[152:155],a[200:203],a[68:71],v[152:155]\n"
"s_min_u32 s82,s82,s15\n"
TR "a[222:223],v0 offset:37376\n"
MF "v[152:155],a[204:207],a[108:111],v[152:155]\n"
LD "v6,s[28:31],s82 offen lds\n"
MF "v[160:163],a[204:207],a[112:115],v[160:163]\n"
"ds_read_b128 a[180:183],v3 offset:40960\n"
MF "v[160:163],a[200:203],a[72:75],v[160:163]\n"
MF "v[168:171],a[200:203],a[76:79],v[168:171]\n"
MF "v[168:171],a[204:207],a[116:119],v[168:171]\n"
LD "v11,s[28:31],s82 offen offset:1024 lds\n"
MF "v[172:175],a[212:215],a[116:119],v[172:175]\n"
TR "a[200:201],v0 offset:32776\n"
MF "v[172:175],a[208:211],a[76:79],v[172:175]\n"
TR "a[202:203],v0 offset:33288\n"
MF "v[164:167],a[208:211],a[72:75],v[164:167]\n"
"ds_read_b128 a[144:147],v2 offset:43008\n"
MF "v[164:167],a[212:215],a[112:115],v[164:167]\n"
TR "a[204:205],v0 offset:36872\n"
MF "v[156:159],a[212:215],a[108:111],v[156:159]\n"
TR "a[206:207],v0 offset:37384\n"
MF "v[156:159],a[208:211],a[68:71],v[156:159]\n"
LD "v12,s[28:31],s82 offen offset:2048 lds\n"
MF "v[148:151],a[208:211],a[64:67],v[148:151]\n"
"ds_read_b128 a[184:187],v3 offset:43008\n"
MF "v[148:151],a[212:215],a[104:107],v[148:151]\n"
MF "v[140:143],a[212:215],a[100:103],v[140:143]\n"
MF "v[140:143],a[208:211],a[60:63],v[140:143]\n"
"s_waitcnt lgkmcnt(7)\n"
LD "v13,s[28:31],s82 offen offset:3072 lds\n"
MF "v[176:179],a[216:219],a[60:63],v[176:179]\n"
TR "a[208:209],v1 offset:32768\n"
MF "v[176:179],a[220:223],a[100:103],v[176:179]\n"
TR "a[210:211],v1 offset:33280\n"
MF "v[184:187],a[220:223],a[104:107],v[184:187]\n"
"ds_read_b128 a[148:151],v2 offset:45056\n"
MF "v[184:187],a[216:219],a[64:67],v[184:187]\n"
"s_add_u32 s84,s14,20480\n"
TR "a[212:213],v1 offset:36864\n"
MF "v[192:195],a[216:219],a[68:71],v[192:195]\n"
"s_add_u32 m0,s63,22528\n"
TR "a[214:215],v1 offset:37376\n"
MF "v[192:195],a[220:223],a[108:111],v[192:195]\n"
LD "v4,s[24:27],s84 offen lds\n"
MF "v[200:203],a[220:223],a[112:115],v[200:203]\n"
"ds_read_b128 a[188:191],v3 offset:45056\n"
MF "v[200:203],a[216:219],a[72:75],v[200:203]\n"
MF "v[208:211],a[216:219],a[76:79],v[208:211]\n"
"s_add_u32 s84,s14,29696\n"
MF "v[208:211],a[220:223],a[116:119],v[208:211]\n"
"s_waitcnt lgkmcnt(7)\n"
LD "v5,s[24:27],s84 offen offset:1024 lds\n"
MF "v[212:215],a[204:207],a[116:119],v[212:215]\n"
TR "a[216:217],v1 offset:32776\n"
MF "v[212:215],a[200:203],a[76:79],v[212:215]\n"
TR "a[218:219],v1 offset:33288\n"
MF "v[204:207],a[200:203],a[72:75],v[204:207]\n"
"ds_read_b128 a[152:155],v2 offset:47104\n"
MF "v[204:207],a[204:207],a[112:115],v[204:207]\n"
TR "a[220:221],v1 offset:36872\n"
MF "v[196:199],a[204:207],a[108:111],v[196:199]\n"
TR "a[222:223],v1 offset:37384\n"
MF "v[196:199],a[200:203],a[68:71],v[196:199]\n"
MF "v[188:191],a[200:203],a[64:67],v[188:191]\n"
"v_add_u32 v0,0xc000,v0\n"
MF "v[188:191],a[204:207],a[104:107],v[188:191]\n"
"v_add_u32 v1,0xc000,v1\n"
MF "v[180:183],a[204:207],a[100:103],v[180:183]\n"
MF "v[180:183],a[200:203],a[60:63],v[180:183]\n"
"s_waitcnt vmcnt(26) lgkmcnt(0)\n"
"s_barrier\n"
MF "v[216:219],a[208:211],a[60:63],v[216:219]\n"
TR "a[200:201],v0\n"
MF "v[216:219],a[212:215],a[100:103],v[216:219]\n"
"s_add_u32 m0,s62,32768\n"
TR "a[202:203],v0 offset:512\n"
MF "v[224:227],a[212:215],a[104:107],v[224:227]\n"
"s_add_u32 s82,s66,4718848\n"
"ds_read_b128 a[192:195],v3 offset:47104\n"
MF "v[224:227],a[208:211],a[64:67],v[224:227]\n"
"s_sub_u32 s15,s82,7864320\n"
TR "a[204:205],v0 offset:4096\n"
MF "v[232:235],a[208:211],a[68:71],v[232:235]\n"
"s_min_u32 s82,s82,s15\n"
TR "a[206:207],v0 offset:4608\n"
MF "v[232:235],a[212:215],a[108:111],v[232:235]\n"
LD "v6,s[28:31],s82 offen lds\n"
MF "v[240:243],a[212:215],a[112:115],v[240:243]\n"
"ds_read_b128 a[156:159],v2 offset:49152\n"
MF "v[240:243],a[208:211],a[72:75],v[240:243]\n"
"ds_read_b128 a[196:199],v3 offset:49152\n"
MF "v[248:251],a[208:211],a[76:79],v[248:251]\n"
MF "v[248:251],a[212:215],a[116:119],v[248:251]\n"
LD "v11,s[28:31],s82 offen offset:1024 lds\n"
MF "v[252:255],a[220:223],a[116:119],v[252:255]\n"
TR "a[208:209],v0 offset:8\n"
MF "v[252:255],a[216:219],a[76:79],v[252:255]\n"
TR "a[210:211],v0 offset:520\n"
MF "v[244:247],a[216:219],a[72:75],v[244:247]\n"
MF "v[244:247],a[220:223],a[112:115],v[244:247]\n"
TR "a[212:213],v0 offset:4104\n"
MF "v[236:239],a[220:223],a[108:111],v[236:239]\n"
TR "a[214:215],v0 offset:4616\n"
MF "v[236:239],a[216:219],a[68:71],v[236:239]\n"
LD "v12,s[28:31],s82 offen offset:2048 lds\n"
MF "v[228:231],a[216:219],a[64:67],v[228:231]\n"
MF "v[228:231],a[220:223],a[104:107],v[228:231]\n"
MF "v[220:223],a[220:223],a[100:103],v[220:223]\n"
MF "v[220:223],a[216:219],a[60:63],v[220:223]\n"
"s_waitcnt lgkmcnt(4)\n"
LD "v13,s[28:31],s82 offen offset:3072 lds\n"
MF "v[16:19],a[200:203],a[140:143],v[16:19]\n"
TR "a[216:217],v1\n"
MF "v[16:19],a[204:207],a[180:183],v[16:19]\n"
TR "a[218:219],v1 offset:512\n"
MF "v[24:27],a[204:207],a[184:187],v[24:27]\n"
MF "v[24:27],a[200:203],a[144:147],v[24:27]\n"
"s_add_u32 s84,s14,40960\n"
TR "a[220:221],v1 offset:4096\n"
MF "v[32:35],a[200:203],a[148:151],v[32:35]\n"
"s_add_u32 m0,s63,24576\n"
TR "a[222:223],v1 offset:4608\n"
MF "v[32:35],a[204:207],a[188:191],v[32:35]\n"
LD "v4,s[24:27],s84 offen lds\n"
MF "v[40:43],a[204:207],a[192:195],v[40:43]\n"
MF "v[40:43],a[200:203],a[152:155],v[40:43]\n"
MF "v[48:51],a[200:203],a[156:159],v[48:51]\n"
MF "v[48:51],a[204:207],a[196:199],v[48:51]\n"
"s_waitcnt lgkmcnt(4)\n"
MF "v[52:55],a[212:215],a[196:199],v[52:55]\n"
TR "a[200:201],v1 offset:8\n"
MF "v[52:55],a[208:211],a[156:159],v[52:55]\n"
TR "a[202:203],v1 offset:520\n"
MF "v[44:47],a[208:211],a[152:155],v[44:47]\n"
MF "v[44:47],a[212:215],a[192:195],v[44:47]\n"
TR "a[204:205],v1 offset:4104\n"
MF "v[36:39],a[212:215],a[188:191],v[36:39]\n"
TR "a[206:207],v1 offset:4616\n"
MF "v[36:39],a[208:211],a[148:151],v[36:39]\n"
MF "v[28:31],a[208:211],a[144:147],v[28:31]\n"
MF "v[28:31],a[212:215],a[184:187],v[28:31]\n"
MF "v[20:23],a[212:215],a[180:183],v[20:23]\n"
MF "v[20:23],a[208:211],a[140:143],v[20:23]\n"
"s_waitcnt vmcnt(25) lgkmcnt(0)\n"
"s_barrier\n"
MF "v[56:59],a[216:219],a[140:143],v[56:59]\n"
"s_add_u32 m0,s62,49152\n"
"s_add_u32 s82,s66,5505024\n"
TR "a[208:209],v0 offset:16384\n"
MF "v[56:59],a[220:223],a[180:183],v[56:59]\n"
"s_sub_u32 s15,s82,7864320\n"
"s_min_u32 s82,s82,s15\n"
TR "a[210:211],v0 offset:16896\n"
MF "v[64:67],a[220:223],a[184:187],v[64:67]\n"
LD "v6,s[28:31],s82 offen lds\n"
MF "v[64:67],a[216:219],a[144:147],v[64:67]\n"
TR "a[212:213],v0 offset:20480\n"
MF "v[72:75],a[216:219],a[148:151],v[72:75]\n"
TR "a[214:215],v0 offset:20992\n"
MF "v[72:75],a[220:223],a[188:191],v[72:75]\n"
"buffer_load_dword a249,v15,s[48:51],s59 offen\n"
MF "v[80:83],a[220:223],a[192:195],v[80:83]\n"
"s_bfe_u32 s59,s23,0x30003\n"
MF "v[80:83],a[216:219],a[152:155],v[80:83]\n"
LD "v11,s[28:31],s82 offen offset:1024 lds\n"
MF "v[88:91],a[216:219],a[156:159],v[88:91]\n"
"s_add_u32 s59,s59,5\n"
MF "v[88:91],a[220:223],a[196:199],v[88:91]\n"
"s_lshl_b32 s59,s59,7\n"
MF "v[92:95],a[204:207],a[196:199],v[92:95]\n"
"s_and_b32 s59,s59,768\n"
TR "a[216:217],v0 offset:16392\n"
MF "v[92:95],a[200:203],a[156:159],v[92:95]\n"
"s_add_u32 s59,s59,s60\n"
TR "a[218:219],v0 offset:16904\n"
MF "v[84:87],a[200:203],a[152:155],v[84:87]\n"
LD "v12,s[28:31],s82 offen offset:2048 lds\n"
MF "v[84:87],a[204:207],a[192:195],v[84:87]\n"
TR "a[220:221],v0 offset:20488\n"
MF "v[76:79],a[204:207],a[188:191],v[76:79]\n"
TR "a[222:223],v0 offset:21000\n"
MF "v[76:79],a[200:203],a[148:151],v[76:79]\n"
MF "v[68:71],a[200:203],a[144:147],v[68:71]\n"
MF "v[68:71],a[204:207],a[184:187],v[68:71]\n"
LD "v13,s[28:31],s82 offen offset:3072 lds\n"
MF "v[60:63],a[204:207],a[180:183],v[60:63]\n"
MF "v[60:63],a[200:203],a[140:143],v[60:63]\n"
"s_add_u32 s14,s81,1024\n"
"s_sub_u32 s15,s14,1280\n"
"s_waitcnt lgkmcnt(4)\n"
MF "v[96:99],a[208:211],a[140:143],v[96:99]\n"
"s_min_u32 s14,s14,s15\n"
"s_add_u32 s14,s14,s68\n"
TR "a[200:201],v1 offset:16384\n"
MF "v[96:99],a[212:215],a[180:183],v[96:99]\n"
"s_add_u32 s84,s14,0\n"
"s_add_u32 m0,s63,40960\n"
TR "a[202:203],v1 offset:16896\n"
MF "v[104:107],a[212:215],a[184:187],v[104:107]\n"
LD "v4,s[24:27],s84 offen lds\n"
MF "v[104:107],a[208:211],a[144:147],v[104:107]\n"
TR "a[204:205],v1 offset:20480\n"
MF "v[112:115],a[208:211],a[148:151],v[112:115]\n"
TR "a[206:207],v1 offset:20992\n"
MF "v[112:115],a[212:215],a[188:191],v[112:115]\n"
MF "v[120:123],a[212:215],a[192:195],v[120:123]\n"
"s_add_u32 s84,s14,9216\n"
MF "v[120:123],a[208:211],a[152:155],v[120:123]\n"
LD "v5,s[24:27],s84 offen offset:1024 lds\n"
MF "v[128:131],a[208:211],a[156:159],v[128:131]\n"
MF "v[128:131],a[212:215],a[196:199],v[128:131]\n"
"s_waitcnt lgkmcnt(4)\n"
MF "v[132:135],a[220:223],a[196:199],v[132:135]\n"
TR "a[208:209],v1 offset:16392\n"
MF "v[132:135],a[216:219],a[156:159],v[132:135]\n"
TR "a[210:211],v1 offset:16904\n"
MF "v[124:127],a[216:219],a[152:155],v[124:127]\n"
MF "v[124:127],a[220:223],a[192:195],v[124:127]\n"
TR "a[212:213],v1 offset:20488\n"
MF "v[116:119],a[220:223],a[188:191],v[116:119]\n"
TR "a[214:215],v1 offset:21000\n"
MF "v[116:119],a[216:219],a[148:151],v[116:119]\n"
MF "v[108:111],a[216:219],a[144:147],v[108:111]\n"
MF "v[108:111],a[220:223],a[184:187],v[108:111]\n"
MF "v[100:103],a[220:223],a[180:183],v[100:103]\n"
MF "v[100:103],a[216:219],a[140:143],v[100:103]\n"
"s_waitcnt vmcnt(25) lgkmcnt(0)\n"
"s_barrier\n"
MF "v[136:139],a[200:203],a[140:143],v[136:139]\n"
TR "a[216:217],v0 offset:32768\n"
MF "v[136:139],a[204:207],a[180:183],v[136:139]\n"
"s_add_u32 m0,s62,65536\n"
TR "a[218:219],v0 offset:33280\n"
MF "v[144:147],a[204:207],a[184:187],v[144:147]\n"
"s_add_u32 s82,s66,5505152\n"
"ds_read_b128 a[60:63],v2\n"
MF "v[144:147],a[200:203],a[144:147],v[144:147]\n"
"s_sub_u32 s15,s82,7864320\n"
TR "a[220:221],v0 offset:36864\n"
MF "v[152:155],a[200:203],a[148:151],v[152:155]\n"
"s_min_u32 s82,s82,s15\n"
TR "a[222:223],v0 offset:37376\n"
MF "v[152:155],a[204:207],a[188:191],v[152:155]\n"
LD "v6,s[28:31],s82 offen lds\n"
MF "v[160:163],a[204:207],a[192:195],v[160:163]\n"
"ds_read_b128 a[100:103],v3\n"
MF "v[160:163],a[200:203],a[152:155],v[160:163]\n"
MF "v[168:171],a[200:203],a[156:159],v[168:171]\n"
MF "v[168:171],a[204:207],a[196:199],v[168:171]\n"
LD "v11,s[28:31],s82 offen offset:1024 lds\n"
MF "v[172:175],a[212:215],a[196:199],v[172:175]\n"
TR "a[200:201],v0 offset:32776\n"
MF "v[172:175],a[208:211],a[156:159],v[172:175]\n"
TR "a[202:203],v0 offset:33288\n"
MF "v[164:167],a[208:211],a[152:155],v[164:167]\n"
"ds_read_b128 a[64:67],v2 offset:2048\n"
MF "v[164:167],a[212:215],a[192:195],v[164:167]\n"
TR "a[204:205],v0 offset:36872\n"
MF "v[156:159],a[212:215],a[188:191],v[156:159]\n"
TR "a[206:207],v0 offset:37384\n"
MF "v[156:159],a[208:211],a[148:151],v[156:159]\n"
LD "v12,s[28:31],s82 offen offset:2048 lds\n"
MF "v[148:151],a[208:211],a[144:147],v[148:151]\n"
"ds_read_b128 a[104:107],v3 offset:2048\n"
MF "v[148:151],a[212:215],a[184:187],v[148:151]\n"
MF "v[140:143],a[212:215],a[180:183],v[140:143]\n"
MF "v[140:143],a[208:211],a[140:143],v[140:143]\n"
"s_waitcnt lgkmcnt(7)\n"
LD "v13,s[28:31],s82 offen offset:3072 lds\n"
MF "v[176:179],a[216:219],a[140:143],v[176:179]\n"
TR "a[208:209],v1 offset:32768\n"
MF "v[176:179],a[220:223],a[180:183],v[176:179]\n"
TR "a[210:211],v1 offset:33280\n"
MF "v[184:187],a[220:223],a[184:187],v[184:187]\n"
"ds_read_b128 a[68:71],v2 offset:4096\n"
MF "v[184:187],a[216:219],a[144:147],v[184:187]\n"
"s_add_u32 s84,s14,20480\n"
TR "a[212:213],v1 offset:36864\n"
MF "v[192:195],a[216:219],a[148:151],v[192:195]\n"
"s_add_u32 m0,s63,43008\n"
TR "a[214:215],v1 offset:37376\n"
MF "v[192:195],a[220:223],a[188:191],v[192:195]\n"
LD "v4,s[24:27],s84 offen lds\n"
MF "v[200:203],a[220:223],a[192:195],v[200:203]\n"
"ds_read_b128 a[108:111],v3 offset:4096\n"
MF "v[200:203],a[216:219],a[152:155],v[200:203]\n"
MF "v[208:211],a[216:219],a[156:159],v[208:211]\n"
"s_add_u32 s84,s14,29696\n"
MF "v[208:211],a[220:223],a[196:199],v[208:211]\n"
"s_waitcnt lgkmcnt(7)\n"
LD "v5,s[24:27],s84 offen offset:1024 lds\n"
MF "v[212:215],a[204:207],a[196:199],v[212:215]\n"
TR "a[216:217],v1 offset:32776\n"
MF "v[212:215],a[200:203],a[156:159],v[212:215]\n"
TR "a[218:219],v1 offset:33288\n"
MF "v[204:207],a[200:203],a[152:155],v[204:207]\n"
"ds_read_b128 a[72:75],v2 offset:6144\n"
MF "v[204:207],a[204:207],a[192:195],v[204:207]\n"
TR "a[220:221],v1 offset:36872\n"
MF "v[196:199],a[204:207],a[188:191],v[196:199]\n"
TR "a[222:223],v1 offset:37384\n"
MF "v[196:199],a[200:203],a[148:151],v[196:199]\n"
MF "v[188:191],a[200:203],a[144:147],v[188:191]\n"
"v_add_u32 v0,0xffff4000,v0\n"
MF "v[188:191],a[204:207],a[184:187],v[188:191]\n"
"v_add_u32 v1,0xffff4000,v1\n"
MF "v[180:183],a[204:207],a[180:183],v[180:183]\n"
MF "v[180:183],a[200:203],a[140:143],v[180:183]\n"
"s_waitcnt vmcnt(26) lgkmcnt(0)\n"
"s_barrier\n"
MF "v[216:219],a[208:211],a[140:143],v[216:219]\n"
TR "a[200:201],v0\n"
MF "v[216:219],a[212:215],a[180:183],v[216:219]\n"
"s_add_u32 m0,s62,81920\n"
TR "a[202:203],v0 offset:512\n"
MF "v[224:227],a[212:215],a[184:187],v[224:227]\n"
"s_add_u32 s82,s66,5505280\n"
"ds_read_b128 a[112:115],v3 offset:6144\n"
MF "v[224:227],a[208:211],a[144:147],v[224:227]\n"
"s_sub_u32 s15,s82,7864320\n"
TR "a[204:205],v0 offset:4096\n"
MF "v[232:235],a[208:211],a[148:151],v[232:235]\n"
"s_min_u32 s82,s82,s15\n"
TR "a[206:207],v0 offset:4608\n"
MF "v[232:235],a[212:215],a[188:191],v[232:235]\n"
LD "v6,s[28:31],s82 offen lds\n"
MF "v[240:243],a[212:215],a[192:195],v[240:243]\n"
"ds_read_b128 a[76:79],v2 offset:8192\n"
MF "v[240:243],a[208:211],a[152:155],v[240:243]\n"
"ds_read_b128 a[116:119],v3 offset:8192\n"
MF "v[248:251],a[208:211],a[156:159],v[248:251]\n"
MF "v[248:251],a[212:215],a[196:199],v[248:251]\n"
LD "v11,s[28:31],s82 offen offset:1024 lds\n"
MF "v[252:255],a[220:223],a[196:199],v[252:255]\n"
TR "a[208:209],v0 offset:8\n"
MF "v[252:255],a[216:219],a[156:159],v[252:255]\n"
TR "a[210:211],v0 offset:520\n"
MF "v[244:247],a[216:219],a[152:155],v[244:247]\n"
MF "v[244:247],a[220:223],a[192:195],v[244:247]\n"
TR "a[212:213],v0 offset:4104\n"
MF "v[236:239],a[220:223],a[188:191],v[236:239]\n"
TR "a[214:215],v0 offset:4616\n"
MF "v[236:239],a[216:219],a[148:151],v[236:239]\n"
LD "v12,s[28:31],s82 offen offset:2048 lds\n"
MF "v[228:231],a[216:219],a[144:147],v[228:231]\n"
MF "v[228:231],a[220:223],a[184:187],v[228:231]\n"
MF "v[220:223],a[220:223],a[180:183],v[220:223]\n"
MF "v[220:223],a[216:219],a[140:143],v[220:223]\n"
"s_waitcnt lgkmcnt(4)\n"
LD "v13,s[28:31],s82 offen offset:3072 lds\n"
MF "v[16:19],a[200:203],a[60:63],v[16:19]\n"
TR "a[216:217],v1\n"
MF "v[16:19],a[204:207],a[100:103],v[16:19]\n"
TR "a[218:219],v1 offset:512\n"
MF "v[24:27],a[204:207],a[104:107],v[24:27]\n"
MF "v[24:27],a[200:203],a[64:67],v[24:27]\n"
"s_add_u32 s84,s14,40960\n"
TR "a[220:221],v1 offset:4096\n"
MF "v[32:35],a[200:203],a[68:71],v[32:35]\n"
"s_add_u32 m0,s63,45056\n"
TR "a[222:223],v1 offset:4608\n"
MF "v[32:35],a[204:207],a[108:111],v[32:35]\n"
LD "v4,s[24:27],s84 offen lds\n"
MF "v[40:43],a[204:207],a[112:115],v[40:43]\n"
MF "v[40:43],a[200:203],a[72:75],v[40:43]\n"
MF "v[48:51],a[200:203],a[76:79],v[48:51]\n"
MF "v[48:51],a[204:207],a[116:119],v[48:51]\n"
"s_waitcnt lgkmcnt(4)\n"
MF "v[52:55],a[212:215],a[116:119],v[52:55]\n"
TR "a[200:201],v1 offset:8\n"
MF "v[52:55],a[208:211],a[76:79],v[52:55]\n"
TR "a[202:203],v1 offset:520\n"
MF "v[44:47],a[208:211],a[72:75],v[44:47]\n"
MF "v[44:47],a[212:215],a[112:115],v[44:47]\n"
TR "a[204:205],v1 offset:4104\n"
MF "v[36:39],a[212:215],a[108:111],v[36:39]\n"
TR "a[206:207],v1 offset:4616\n"
MF "v[36:39],a[208:211],a[68:71],v[36:39]\n"
MF "v[28:31],a[208:211],a[64:67],v[28:31]\n"
MF "v[28:31],a[212:215],a[104:107],v[28:31]\n"
MF "v[20:23],a[212:215],a[100:103],v[20:23]\n"
MF "v[20:23],a[208:211],a[60:63],v[20:23]\n"
"s_waitcnt vmcnt(25) lgkmcnt(0)\n"
"s_barrier\n"
MF "v[56:59],a[216:219],a[60:63],v[56:59]\n"
"s_add_u32 m0,s62,0\n"
"s_add_u32 s82,s66,6291712\n"
TR "a[208:209],v0 offset:16384\n"
MF "v[56:59],a[220:223],a[100:103],v[56:59]\n"
"s_sub_u32 s15,s82,7864320\n"
"s_min_u32 s82,s82,s15\n"
TR "a[210:211],v0 offset:16896\n"
MF "v[64:67],a[220:223],a[104:107],v[64:67]\n"
LD "v6,s[28:31],s82 offen lds\n"
MF "v[64:67],a[216:219],a[64:67],v[64:67]\n"
TR "a[212:213],v0 offset:20480\n"
MF "v[72:75],a[216:219],a[68:71],v[72:75]\n"
TR "a[214:215],v0 offset:20992\n"
MF "v[72:75],a[220:223],a[108:111],v[72:75]\n"
"buffer_load_dword a249,v15,s[48:51],s59 offen\n"
MF "v[80:83],a[220:223],a[112:115],v[80:83]\n"
MF "v[80:83],a[216:219],a[72:75],v[80:83]\n"
LD "v11,s[28:31],s82 offen offset:1024 lds\n"
MF "v[88:91],a[216:219],a[76:79],v[88:91]\n"
MF "v[88:91],a[220:223],a[116:119],v[88:91]\n"
MF "v[92:95],a[204:207],a[116:119],v[92:95]\n"
TR "a[216:217],v0 offset:16392\n"
MF "v[92:95],a[200:203],a[76:79],v[92:95]\n"
TR "a[218:219],v0 offset:16904\n"
MF "v[84:87],a[200:203],a[72:75],v[84:87]\n"
LD "v12,s[28:31],s82 offen offset:2048 lds\n"
MF "v[84:87],a[204:207],a[112:115],v[84:87]\n"
TR "a[220:221],v0 offset:20488\n"
MF "v[76:79],a[204:207],a[108:111],v[76:79]\n"
TR "a[222:223],v0 offset:21000\n"
MF "v[76:79],a[200:203],a[68:71],v[76:79]\n"
MF "v[68:71],a[200:203],a[64:67],v[68:71]\n"
MF "v[68:71],a[204:207],a[104:107],v[68:71]\n"
LD "v13,s[28:31],s82 offen offset:3072 lds\n"
MF "v[60:63],a[204:207],a[100:103],v[60:63]\n"
MF "v[60:63],a[200:203],a[60:63],v[60:63]\n"
"s_add_u32 s14,s81,1152\n"
"s_sub_u32 s15,s14,1280\n"
"s_waitcnt lgkmcnt(4)\n"
MF "v[96:99],a[208:211],a[60:63],v[96:99]\n"
"s_min_u32 s14,s14,s15\n"
"s_add_u32 s14,s14,s68\n"
TR "a[200:201],v1 offset:16384\n"
MF "v[96:99],a[212:215],a[100:103],v[96:99]\n"
"s_add_u32 s84,s14,0\n"
"s_add_u32 m0,s63,0\n"
TR "a[202:203],v1 offset:16896\n"
MF "v[104:107],a[212:215],a[104:107],v[104:107]\n"
LD "v4,s[24:27],s84 offen lds\n"
MF "v[104:107],a[208:211],a[64:67],v[104:107]\n"
TR "a[204:205],v1 offset:20480\n"
MF "v[112:115],a[208:211],a[68:71],v[112:115]\n"
TR "a[206:207],v1 offset:20992\n"
MF "v[112:115],a[212:215],a[108:111],v[112:115]\n"
MF "v[120:123],a[212:215],a[112:115],v[120:123]\n"
"s_add_u32 s84,s14,9216\n"
MF "v[120:123],a[208:211],a[72:75],v[120:123]\n"
LD "v5,s[24:27],s84 offen offset:1024 lds\n"
MF "v[128:131],a[208:211],a[76:79],v[128:131]\n"
MF "v[128:131],a[212:215],a[116:119],v[128:131]\n"
"s_waitcnt lgkmcnt(4)\n"
MF "v[132:135],a[220:223],a[116:119],v[132:135]\n"
TR "a[208:209],v1 offset:16392\n"
MF "v[132:135],a[216:219],a[76:79],v[132:135]\n"
"s_add_u32 s84,s14,18432\n"
TR "a[210:211],v1 offset:16904\n"
MF "v[124:127],a[216:219],a[72:75],v[124:127]\n"
LD "v4,s[24:27],s84 offen offset:2048 lds\n"
MF "v[124:127],a[220:223],a[112:115],v[124:127]\n"
TR "a[212:213],v1 offset:20488\n"
MF "v[116:119],a[220:223],a[108:111],v[116:119]\n"
TR "a[214:215],v1 offset:21000\n"
MF "v[116:119],a[216:219],a[68:71],v[116:119]\n"
MF "v[108:111],a[216:219],a[64:67],v[108:111]\n"
MF "v[108:111],a[220:223],a[104:107],v[108:111]\n"
MF "v[100:103],a[220:223],a[100:103],v[100:103]\n"
MF "v[100:103],a[216:219],a[60:63],v[100:103]\n"
"s_waitcnt vmcnt(26) lgkmcnt(0)\n"
"s_barrier\n"
MF "v[136:139],a[200:203],a[60:63],v[136:139]\n"
TR "a[216:217],v0 offset:32768\n"
MF "v[136:139],a[204:207],a[100:103],v[136:139]\n"
"s_add_u32 m0,s62,16384\n"
TR "a[218:219],v0 offset:33280\n"
MF "v[144:147],a[204:207],a[104:107],v[144:147]\n"
"s_add_u32 s82,s66,7078144\n"
"ds_read_b128 a[140:143],v2 offset:20480\n"
MF "v[144:147],a[200:203],a[64:67],v[144:147]\n"
"s_sub_u32 s15,s82,7864320\n"
TR "a[220:221],v0 offset:36864\n"
MF "v[152:155],a[200:203],a[68:71],v[152:155]\n"
"s_min_u32 s82,s82,s15\n"
TR "a[222:223],v0 offset:37376\n"
MF "v[152:155],a[204:207],a[108:111],v[152:155]\n"
LD "v6,s[28:31],s82 offen lds\n"
MF "v[160:163],a[204:207],a[112:115],v[160:163]\n"
"ds_read_b128 a[180:183],v3 offset:20480\n"
MF "v[160:163],a[200:203],a[72:75],v[160:163]\n"
MF "v[168:171],a[200:203],a[76:79],v[168:171]\n"
MF "v[168:171],a[204:207],a[116:119],v[168:171]\n"
LD "v11,s[28:31],s82 offen offset:1024 lds\n"
MF "v[172:175],a[212:215],a[116:119],v[172:175]\n"
TR "a[200:201],v0 offset:32776\n"
MF "v[172:175],a[208:211],a[76:79],v[172:175]\n"
TR "a[202:203],v0 offset:33288\n"
MF "v[164:167],a[208:211],a[72:75],v[164:167]\n"
"ds_read_b128 a[144:147],v2 offset:22528\n"
MF "v[164:167],a[212:215],a[112:115],v[164:167]\n"
TR "a[204:205],v0 offset:36872\n"
MF "v[156:159],a[212:215],a[108:111],v[156:159]\n"
TR "a[206:207],v0 offset:37384\n"
MF "v[156:159],a[208:211],a[68:71],v[156:159]\n"
LD "v12,s[28:31],s82 offen offset:2048 lds\n"
MF "v[148:151],a[208:211],a[64:67],v[148:151]\n"
"ds_read_b128 a[184:187],v3 offset:22528\n"
MF "v[148:151],a[212:215],a[104:107],v[148:151]\n"
MF "v[140:143],a[212:215],a[100:103],v[140:143]\n"
MF "v[140:143],a[208:211],a[60:63],v[140:143]\n"
"s_waitcnt lgkmcnt(7)\n"
LD "v13,s[28:31],s82 offen offset:3072 lds\n"
MF "v[176:179],a[216:219],a[60:63],v[176:179]\n"
TR "a[208:209],v1 offset:32768\n"
MF "v[176:179],a[220:223],a[100:103],v[176:179]\n"
TR "a[210:211],v1 offset:33280\n"
MF "v[184:187],a[220:223],a[104:107],v[184:187]\n"
"ds_read_b128 a[148:151],v2 offset:24576\n"
MF "v[184:187],a[216:219],a[64:67],v[184:187]\n"
"s_add_u32 s84,s14,30720\n"
TR "a[212:213],v1 offset:36864\n"
MF "v[192:195],a[216:219],a[68:71],v[192:195]\n"
"s_add_u32 m0,s63,3072\n"
TR "a[214:215],v1 offset:37376\n"
MF "v[192:195],a[220:223],a[108:111],v[192:195]\n"
LD "v5,s[24:27],s84 offen lds\n"
MF "v[200:203],a[220:223],a[112:115],v[200:203]\n"
"ds_read_b128 a[188:191],v3 offset:24576\n"
MF "v[200:203],a[216:219],a[72:75],v[200:203]\n"
MF "v[208:211],a[216:219],a[76:79],v[208:211]\n"
"s_add_u32 s84,s14,39936\n"
MF "v[208:211],a[220:223],a[116:119],v[208:211]\n"
"s_waitcnt lgkmcnt(7)\n"
LD "v4,s[24:27],s84 offen offset:1024 lds\n"
MF "v[212:215],a[204:207],a[116:119],v[212:215]\n"
TR "a[216:217],v1 offset:32776\n"
MF "v[212:215],a[200:203],a[76:79],v[212:215]\n"
TR "a[218:219],v1 offset:33288\n"
MF "v[204:207],a[200:203],a[72:75],v[204:207]\n"
"ds_read_b128 a[152:155],v2 offset:26624\n"
MF "v[204:207],a[204:207],a[112:115],v[204:207]\n"
TR "a[220:221],v1 offset:36872\n"
MF "v[196:199],a[204:207],a[108:111],v[196:199]\n"
TR "a[222:223],v1 offset:37384\n"
MF "v[196:199],a[200:203],a[68:71],v[196:199]\n"
MF "v[188:191],a[200:203],a[64:67],v[188:191]\n"
"v_add_u32 v0,0xc000,v0\n"
MF "v[188:191],a[204:207],a[104:107],v[188:191]\n"
"v_add_u32 v1,0xc000,v1\n"
MF "v[180:183],a[204:207],a[100:103],v[180:183]\n"
"s_branch .Lqj\n"
".Lq0:\n"
"s_cbranch_execz .Lipr2\n"
MF "v[16:19],a[200:203],a[60:63],0\n"
"s_add_u32 m0,s62,49152\n"
"s_add_u32 s82,s66,786432\n"
TR "a[216:217],v1\n"
MF "v[16:19],a[204:207],a[100:103],v[16:19]\n"
"s_sub_u32 s15,s82,7864320\n"
"s_min_u32 s82,s82,s15\n"
TR "a[218:219],v1 offset:512\n"
MF "v[24:27],a[204:207],a[104:107],0\n"
LD "v6,s[28:31],s82 offen lds\n"
MF "v[24:27],a[200:203],a[64:67],v[24:27]\n"
TR "a[220:221],v1 offset:4096\n"
MF "v[32:35],a[200:203],a[68:71],0\n"
TR "a[222:223],v1 offset:4608\n"
MF "v[32:35],a[204:207],a[108:111],v[32:35]\n"
"v_max3_f32 v220,|v16|,|v17|,|v18|\n"
"v_max_f32_e64 v220,v220,|v19|\n"
MF "v[40:43],a[204:207],a[112:115],0\n"
"v_readlane_b32 s12,v220,3\n"
"v_readlane_b32 s6,v220,22\n"
MF "v[40:43],a[200:203],a[72:75],v[40:43]\n"
"s_min_u32 s12,s12,s6\n"
"v_readlane_b32 s7,v220,41\n"
"v_readlane_b32 s8,v220,60\n"
MF "v[48:51],a[200:203],a[76:79],0\n"
"s_min_u32 s7,s7,s8\n"
MF "v[48:51],a[204:207],a[116:119],v[48:51]\n"
LD "v11,s[28:31],s82 offen offset:1024 lds\n"
MF "v[52:55],a[212:215],a[116:119],0\n"
"s_min_u32 s12,s12,s7\n"
TR "a[200:201],v1 offset:8\n"
MF "v[52:55],a[208:211],a[76:79],v[52:55]\n"
"s_and_b32 s12,s12,0x7f800000\n"
TR "a[202:203],v1 offset:520\n"
MF "v[44:47],a[208:211],a[72:75],0\n"
"s_sub_u32 s12,s12,0x14000000\n"
MF "v[44:47],a[212:215],a[112:115],v[44:47]\n"
"s_cmp_lt_u32 s12,0x50000000\n"
"s_cselect_b32 s12,s12,0xe8400000\n"
TR "a[204:205],v1 offset:4104\n"
MF "v[36:39],a[212:215],a[108:111],0\n"
"s_add_u32 s12,s12,0x17c00000\n"
TR "a[206:207],v1 offset:4616\n"
MF "v[36:39],a[208:211],a[68:71],v[36:39]\n"
"v_mov_b32 v220,s12\n"
"v_mov_b32 v221,s12\n"
MF "v[28:31],a[208:211],a[64:67],0\n"
"v_mov_b32 v222,s12\n"
"v_mov_b32 v223,s12\n"
MF "v[28:31],a[212:215],a[104:107],v[28:31]\n"
LD "v12,s[28:31],s82 offen offset:2048 lds\n"
MF "v[20:23],a[212:215],a[100:103],0\n"
MF "v[20:23],a[208:211],a[60:63],v[20:23]\n"
"s_waitcnt vmcnt(7) lgkmcnt(0)\n"
"s_barrier\n"
MF "v[56:59],a[216:219],a[60:63],v[220:223]\n"
TR "a[208:209],v0 offset:16384\n"
MF "v[56:59],a[220:223],a[100:103],v[56:59]\n"
TR "a[210:211],v0 offset:16896\n"
MF "v[64:67],a[220:223],a[104:107],v[220:223]\n"
LD "v13,s[28:31],s82 offen offset:3072 lds\n"
MF "v[64:67],a[216:219],a[64:67],v[64:67]\n"
"s_add_u32 m0,s62,65536\n"
TR "a[212:213],v0 offset:20480\n"
MF "v[72:75],a[216:219],a[68:71],v[220:223]\n"
"s_add_u32 s82,s66,786560\n"
TR "a[214:215],v0 offset:20992\n"
MF "v[72:75],a[220:223],a[108:111],v[72:75]\n"
"s_sub_u32 s15,s82,7864320\n"
MF "v[80:83],a[220:223],a[112:115],v[220:223]\n"
"s_min_u32 s82,s82,s15\n"
MF "v[80:83],a[216:219],a[72:75],v[80:83]\n"
LD "v6,s[28:31],s82 offen lds\n"
MF "v[88:91],a[216:219],a[76:79],v[220:223]\n"
MF "v[88:91],a[220:223],a[116:119],v[88:91]\n"
MF "v[92:95],a[204:207],a[116:119],v[220:223]\n"
TR "a[216:217],v0 offset:16392\n"
MF "v[92:95],a[200:203],a[76:79],v[92:95]\n"
TR "a[218:219],v0 offset:16904\n"
MF "v[84:87],a[200:203],a[72:75],v[220:223]\n"
LD "v11,s[28:31],s82 offen offset:1024 lds\n"
MF "v[84:87],a[204:207],a[112:115],v[84:87]\n"
TR "a[220:221],v0 offset:20488\n"
MF "v[76:79],a[204:207],a[108:111],v[220:223]\n"
TR "a[222:223],v0 offset:21000\n"
MF "v[76:79],a[200:203],a[68:71],v[76:79]\n"
MF "v[68:71],a[200:203],a[64:67],v[220:223]\n"
MF "v[68:71],a[204:207],a[104:107],v[68:71]\n"
LD "v12,s[28:31],s82 offen offset:2048 lds\n"
MF "v[60:63],a[204:207],a[100:103],v[220:223]\n"
MF "v[60:63],a[200:203],a[60:63],v[60:63]\n"
"s_waitcnt lgkmcnt(4)\n"
MF "v[96:99],a[208:211],a[60:63],v[220:223]\n"
TR "a[200:201],v1 offset:16384\n"
MF "v[96:99],a[212:215],a[100:103],v[96:99]\n"
TR "a[202:203],v1 offset:16896\n"
MF "v[104:107],a[212:215],a[104:107],v[220:223]\n"
LD "v13,s[28:31],s82 offen offset:3072 lds\n"
MF "v[104:107],a[208:211],a[64:67],v[104:107]\n"
"s_add_u32 m0,s62,81920\n"
TR "a[204:205],v1 offset:20480\n"
MF "v[112:115],a[208:211],a[68:71],v[220:223]\n"
"s_add_u32 s82,s66,786688\n"
TR "a[206:207],v1 offset:20992\n"
MF "v[112:115],a[212:215],a[108:111],v[112:115]\n"
"s_sub_u32 s15,s82,7864320\n"
MF "v[120:123],a[212:215],a[112:115],v[220:223]\n"
"s_min_u32 s82,s82,s15\n"
MF "v[120:123],a[208:211],a[72:75],v[120:123]\n"
LD "v6,s[28:31],s82 offen lds\n"
MF "v[128:131],a[208:211],a[76:79],v[220:223]\n"
MF "v[128:131],a[212:215],a[116:119],v[128:131]\n"
"s_waitcnt lgkmcnt(4)\n"
MF "v[132:135],a[220:223],a[116:119],v[220:223]\n"
TR "a[208:209],v1 offset:16392\n"
MF "v[132:135],a[216:219],a[76:79],v[132:135]\n"
TR "a[210:211],v1 offset:16904\n"
MF "v[124:127],a[216:219],a[72:75],v[220:223]\n"
LD "v11,s[28:31],s82 offen offset:1024 lds\n"
MF "v[124:127],a[220:223],a[112:115],v[124:127]\n"
TR "a[212:213],v1 offset:20488\n"
MF "v[116:119],a[220:223],a[108:111],v[220:223]\n"
TR "a[214:215],v1 offset:21000\n"
MF "v[116:119],a[216:219],a[68:71],v[116:119]\n"
MF "v[108:111],a[216:219],a[64:67],v[220:223]\n"
MF "v[108:111],a[220:223],a[104:107],v[108:111]\n"
LD "v12,s[28:31],s82 offen offset:2048 lds\n"
MF "v[100:103],a[220:223],a[100:103],v[220:223]\n"
MF "v[100:103],a[216:219],a[60:63],v[100:103]\n"
"s_waitcnt vmcnt(11) lgkmcnt(0)\n"
"s_barrier\n"
MF "v[136:139],a[200:203],a[60:63],v[220:223]\n"
TR "a[216:217],v0 offset:32768\n"
MF "v[136:139],a[204:207],a[100:103],v[136:139]\n"
TR "a[218:219],v0 offset:33280\n"
MF "v[144:147],a[204:207],a[104:107],v[220:223]\n"
"ds_read_b128 a[140:143],v2 offset:20480\n"
MF "v[144:147],a[200:203],a[64:67],v[144:147]\n"
TR "a[220:221],v0 offset:36864\n"
MF "v[152:155],a[200:203],a[68:71],v[220:223]\n"
TR "a[222:223],v0 offset:37376\n"
MF "v[152:155],a[204:207],a[108:111],v[152:155]\n"
LD "v13,s[28:31],s82 offen offset:3072 lds\n"
MF "v[160:163],a[204:207],a[112:115],v[220:223]\n"
"ds_read_b128 a[180:183],v3 offset:20480\n"
MF "v[160:163],a[200:203],a[72:75],v[160:163]\n"
"s_add_u32 m0,s62,0\n"
MF "v[168:171],a[200:203],a[76:79],v[220:223]\n"
"s_add_u32 s82,s66,1572864\n"
"s_sub_u32 s15,s82,7864320\n"
"s_min_u32 s82,s82,s15\n"
MF "v[168:171],a[204:207],a[116:119],v[168:171]\n"
LD "v6,s[28:31],s82 offen lds\n"
MF "v[172:175],a[212:215],a[116:119],v[220:223]\n"
TR "a[200:201],v0 offset:32776\n"
MF "v[172:175],a[208:211],a[76:79],v[172:175]\n"
TR "a[202:203],v0 offset:33288\n"
MF "v[164:167],a[208:211],a[72:75],v[220:223]\n"
"ds_read_b128 a[144:147],v2 offset:22528\n"
MF "v[164:167],a[212:215],a[112:115],v[164:167]\n"
TR "a[204:205],v0 offset:36872\n"
MF "v[156:159],a[212:215],a[108:111],v[220:223]\n"
TR "a[206:207],v0 offset:37384\n"
MF "v[156:159],a[208:211],a[68:71],v[156:159]\n"
LD "v11,s[28:31],s82 offen offset:1024 lds\n"
MF "v[148:151],a[208:211],a[64:67],v[220:223]\n"
"ds_read_b128 a[184:187],v3 offset:22528\n"
MF "v[148:151],a[212:215],a[104:107],v[148:151]\n"
MF "v[140:143],a[212:215],a[100:103],v[220:223]\n"
MF "v[140:143],a[208:211],a[60:63],v[140:143]\n"
"s_waitcnt lgkmcnt(7)\n"
LD "v12,s[28:31],s82 offen offset:2048 lds\n"
MF "v[176:179],a[216:219],a[60:63],v[220:223]\n"
TR "a[208:209],v1 offset:32768\n"
MF "v[176:179],a[220:223],a[100:103],v[176:179]\n"
TR "a[210:211],v1 offset:33280\n"
MF "v[184:187],a[220:223],a[104:107],v[220:223]\n"
"ds_read_b128 a[148:151],v2 offset:24576\n"
MF "v[184:187],a[216:219],a[64:67],v[184:187]\n"
TR "a[212:213],v1 offset:36864\n"
MF "v[192:195],a[216:219],a[68:71],v[220:223]\n"
TR "a[214:215],v1 offset:37376\n"
MF "v[192:195],a[220:223],a[108:111],v[192:195]\n"
LD "v13,s[28:31],s82 offen offset:3072 lds\n"
MF "v[200:203],a[220:223],a[112:115],v[220:223]\n"
"ds_read_b128 a[188:191],v3 offset:24576\n"
MF "v[200:203],a[216:219],a[72:75],v[200:203]\n"
"s_add_u32 s14,s81,384\n"
"s_sub_u32 s15,s14,1280\n"
"s_min_u32 s14,s14,s15\n"
MF "v[208:211],a[216:219],a[76:79],v[220:223]\n"
"s_add_u32 s14,s14,s68\n"
"s_add_u32 s84,s14,0\n"
"s_add_u32 m0,s63,0\n"
MF "v[208:211],a[220:223],a[116:119],v[208:211]\n"
"s_waitcnt lgkmcnt(7)\n"
LD "v4,s[24:27],s84 offen lds\n"
MF "v[212:215],a[204:207],a[116:119],v[220:223]\n"
TR "a[216:217],v1 offset:32776\n"
MF "v[212:215],a[200:203],a[76:79],v[212:215]\n"
TR "a[218:219],v1 offset:33288\n"
MF "v[204:207],a[200:203],a[72:75],v[220:223]\n"
"ds_read_b128 a[152:155],v2 offset:26624\n"
MF "v[204:207],a[204:207],a[112:115],v[204:207]\n"
TR "a[220:221],v1 offset:36872\n"
MF "v[196:199],a[204:207],a[108:111],v[220:223]\n"
"s_add_u32 s84,s14,9216\n"
TR "a[222:223],v1 offset:37384\n"
MF "v[196:199],a[200:203],a[68:71],v[196:199]\n"
LD "v5,s[24:27],s84 offen offset:1024 lds\n"
MF "v[188:191],a[200:203],a[64:67],v[220:223]\n"
"s_add_u32 m0,s62,16384\n"
"s_add_u32 s82,s66,1572992\n"
"v_add_u32 v0,0xc000,v0\n"
MF "v[188:191],a[204:207],a[104:107],v[188:191]\n"
"s_sub_u32 s15,s82,7864320\n"
"s_min_u32 s82,s82,s15\n"
"v_add_u32 v1,0xc000,v1\n"
MF "v[180:183],a[204:207],a[100:103],v[220:223]\n"
LD "v6,s[28:31],s82 offen lds\n"
MF "v[180:183],a[200:203],a[60:63],v[180:183]\n"
"s_waitcnt vmcnt(15) lgkmcnt(0)\n"
"s_barrier\n"
MF "v[216:219],a[208:211],a[60:63],v[220:223]\n"
TR "a[200:201],v0\n"
MF "v[216:219],a[212:215],a[100:103],v[216:219]\n"
TR "a[202:203],v0 offset:512\n"
MF "v[224:227],a[212:215],a[104:107],v[220:223]\n"
"ds_read_b128 a[192:195],v3 offset:26624\n"
MF "v[224:227],a[208:211],a[64:67],v[224:227]\n"
TR "a[204:205],v0 offset:4096\n"
MF "v[232:235],a[208:211],a[68:71],v[220:223]\n"
TR "a[206:207],v0 offset:4608\n"
MF "v[232:235],a[212:215],a[108:111],v[232:235]\n"
LD "v11,s[28:31],s82 offen offset:1024 lds\n"
MF "v[240:243],a[212:215],a[112:115],v[220:223]\n"
"ds_read_b128 a[156:159],v2 offset:28672\n"
MF "v[240:243],a[208:211],a[72:75],v[240:243]\n"
"ds_read_b128 a[196:199],v3 offset:28672\n"
MF "v[248:251],a[208:211],a[76:79],v[220:223]\n"
MF "v[248:251],a[212:215],a[116:119],v[248:251]\n"
LD "v12,s[28:31],s82 offen offset:2048 lds\n"
MF "v[252:255],a[220:223],a[116:119],v[220:223]\n"
TR "a[208:209],v0 offset:8\n"
MF "v[252:255],a[216:219],a[76:79],v[252:255]\n"
TR "a[210:211],v0 offset:520\n"
MF "v[244:247],a[216:219],a[72:75],v[220:223]\n"
MF "v[244:247],a[220:223],a[112:115],v[244:247]\n"
TR "a[212:213],v0 offset:4104\n"
MF "v[236:239],a[220:223],a[108:111],v[220:223]\n"
TR "a[214:215],v0 offset:4616\n"
MF "v[236:239],a[216:219],a[68:71],v[236:239]\n"
LD "v13,s[28:31],s82 offen offset:3072 lds\n"
MF "v[228:231],a[216:219],a[64:67],v[220:223]\n"
MF "v[228:231],a[220:223],a[104:107],v[228:231]\n"
"s_add_u32 s84,s14,20480\n"
MF "v[220:223],a[220:223],a[100:103],v[220:223]\n"
"s_add_u32 m0,s63,2048\n"
MF "v[220:223],a[216:219],a[60:63],v[220:223]\n"
"s_waitcnt lgkmcnt(4)\n"
LD "v4,s[24:27],s84 offen lds\n"
MF "v[16:19],a[200:203],a[140:143],v[16:19]\n"
TR "a[216:217],v1\n"
MF "v[16:19],a[204:207],a[180:183],v[16:19]\n"
TR "a[218:219],v1 offset:512\n"
MF "v[24:27],a[204:207],a[184:187],v[24:27]\n"
MF "v[24:27],a[200:203],a[144:147],v[24:27]\n"
TR "a[220:221],v1 offset:4096\n"
MF "v[32:35],a[200:203],a[148:151],v[32:35]\n"
"s_add_u32 s84,s14,29696\n"
TR "a[222:223],v1 offset:4608\n"
MF "v[32:35],a[204:207],a[188:191],v[32:35]\n"
LD "v5,s[24:27],s84 offen offset:1024 lds\n"
MF "v[40:43],a[204:207],a[192:195],v[40:43]\n"
MF "v[40:43],a[200:203],a[152:155],v[40:43]\n"
"s_add_u32 m0,s62,32768\n"
MF "v[48:51],a[200:203],a[156:159],v[48:51]\n"
"s_add_u32 s82,s66,1573120\n"
"s_sub_u32 s15,s82,7864320\n"
"s_min_u32 s82,s82,s15\n"
MF "v[48:51],a[204:207],a[196:199],v[48:51]\n"
"s_waitcnt lgkmcnt(4)\n"
LD "v6,s[28:31],s82 offen lds\n"
MF "v[52:55],a[212:215],a[196:199],v[52:55]\n"
TR "a[200:201],v1 offset:8\n"
MF "v[52:55],a[208:211],a[156:159],v[52:55]\n"
TR "a[202:203],v1 offset:520\n"
MF "v[44:47],a[208:211],a[152:155],v[44:47]\n"
MF "v[44:47],a[212:215],a[192:195],v[44:47]\n"
TR "a[204:205],v1 offset:4104\n"
MF "v[36:39],a[212:215],a[188:191],v[36:39]\n"
TR "a[206:207],v1 offset:4616\n"
MF "v[36:39],a[208:211],a[148:151],v[36:39]\n"
LD "v11,s[28:31],s82 offen offset:1024 lds\n"
MF "v[28:31],a[208:211],a[144:147],v[28:31]\n"
MF "v[28:31],a[212:215],a[184:187],v[28:31]\n"
MF "v[20:23],a[212:215],a[180:183],v[20:23]\n"
LD "v12,s[28:31],s82 offen offset:2048 lds\n"
MF "v[20:23],a[208:211],a[140:143],v[20:23]\n"
"s_waitcnt vmcnt(19) lgkmcnt(0)\n"
"s_barrier\n"
MF "v[56:59],a[216:219],a[140:143],v[56:59]\n"
TR "a[208:209],v0 offset:16384\n"
MF "v[56:59],a[220:223],a[180:183],v[56:59]\n"
TR "a[210:211],v0 offset:16896\n"
MF "v[64:67],a[220:223],a[184:187],v[64:67]\n"
LD "v13,s[28:31],s82 offen offset:3072 lds\n"
MF "v[64:67],a[216:219],a[144:147],v[64:67]\n"
TR "a[212:213],v0 offset:20480\n"
MF "v[72:75],a[216:219],a[148:151],v[72:75]\n"
TR "a[214:215],v0 offset:20992\n"
MF "v[72:75],a[220:223],a[188:191],v[72:75]\n"
"s_add_u32 s84,s14,40960\n"
MF "v[80:83],a[220:223],a[192:195],v[80:83]\n"
"s_add_u32 m0,s63,4096\n"
MF "v[80:83],a[216:219],a[152:155],v[80:83]\n"
LD "v4,s[24:27],s84 offen lds\n"
MF "v[88:91],a[216:219],a[156:159],v[88:91]\n"
MF "v[88:91],a[220:223],a[196:199],v[88:91]\n"
"s_sub_u32 s66,s66,s83\n"
MF "v[92:95],a[204:207],a[196:199],v[92:95]\n"
"s_add_u32 m0,s62,49152\n"
"s_add_u32 s82,s66,2359296\n"
TR "a[216:217],v0 offset:16392\n"
MF "v[92:95],a[200:203],a[156:159],v[92:95]\n"
"s_sub_u32 s15,s82,7864320\n"
"s_min_u32 s82,s82,s15\n"
TR "a[218:219],v0 offset:16904\n"
MF "v[84:87],a[200:203],a[152:155],v[84:87]\n"
LD "v6,s[28:31],s82 offen lds\n"
MF "v[84:87],a[204:207],a[192:195],v[84:87]\n"
TR "a[220:221],v0 offset:20488\n"
MF "v[76:79],a[204:207],a[188:191],v[76:79]\n"
TR "a[222:223],v0 offset:21000\n"
MF "v[76:79],a[200:203],a[148:151],v[76:79]\n"
MF "v[68:71],a[200:203],a[144:147],v[68:71]\n"
MF "v[68:71],a[204:207],a[184:187],v[68:71]\n"
LD "v11,s[28:31],s82 offen offset:1024 lds\n"
MF "v[60:63],a[204:207],a[180:183],v[60:63]\n"
MF "v[60:63],a[200:203],a[140:143],v[60:63]\n"
"s_waitcnt lgkmcnt(4)\n"
MF "v[96:99],a[208:211],a[140:143],v[96:99]\n"
TR "a[200:201],v1 offset:16384\n"
MF "v[96:99],a[212:215],a[180:183],v[96:99]\n"
TR "a[202:203],v1 offset:16896\n"
MF "v[104:107],a[212:215],a[184:187],v[104:107]\n"
LD "v12,s[28:31],s82 offen offset:2048 lds\n"
MF "v[104:107],a[208:211],a[144:147],v[104:107]\n"
TR "a[204:205],v1 offset:20480\n"
MF "v[112:115],a[208:211],a[148:151],v[112:115]\n"
TR "a[206:207],v1 offset:20992\n"
MF "v[112:115],a[212:215],a[188:191],v[112:115]\n"
MF "v[120:123],a[212:215],a[192:195],v[120:123]\n"
MF "v[120:123],a[208:211],a[152:155],v[120:123]\n"
LD "v13,s[28:31],s82 offen offset:3072 lds\n"
MF "v[128:131],a[208:211],a[156:159],v[128:131]\n"
MF "v[128:131],a[212:215],a[196:199],v[128:131]\n"
"s_add_u32 s14,s81,512\n"
"s_sub_u32 s15,s14,1280\n"
"s_waitcnt lgkmcnt(4)\n"
MF "v[132:135],a[220:223],a[196:199],v[132:135]\n"
"s_min_u32 s14,s14,s15\n"
"s_add_u32 s14,s14,s68\n"
TR "a[208:209],v1 offset:16392\n"
MF "v[132:135],a[216:219],a[156:159],v[132:135]\n"
"s_add_u32 s84,s14,0\n"
"s_add_u32 m0,s63,20480\n"
TR "a[210:211],v1 offset:16904\n"
MF "v[124:127],a[216:219],a[152:155],v[124:127]\n"
LD "v4,s[24:27],s84 offen lds\n"
MF "v[124:127],a[220:223],a[192:195],v[124:127]\n"
TR "a[212:213],v1 offset:20488\n"
MF "v[116:119],a[220:223],a[188:191],v[116:119]\n"
TR "a[214:215],v1 offset:21000\n"
MF "v[116:119],a[216:219],a[148:151],v[116:119]\n"
MF "v[108:111],a[216:219],a[144:147],v[108:111]\n"
"s_add_u32 s84,s14,9216\n"
MF "v[108:111],a[220:223],a[184:187],v[108:111]\n"
LD "v5,s[24:27],s84 offen offset:1024 lds\n"
MF "v[100:103],a[220:223],a[180:183],v[100:103]\n"
MF "v[100:103],a[216:219],a[140:143],v[100:103]\n"
"s_waitcnt vmcnt(23) lgkmcnt(0)\n"
"s_barrier\n"
MF "v[136:139],a[200:203],a[140:143],v[136:139]\n"
TR "a[216:217],v0 offset:32768\n"
MF "v[136:139],a[204:207],a[180:183],v[136:139]\n"
"s_add_u32 m0,s62,65536\n"
TR "a[218:219],v0 offset:33280\n"
MF "v[144:147],a[204:207],a[184:187],v[144:147]\n"
"s_add_u32 s82,s66,2359424\n"
"ds_read_b128 a[60:63],v2 offset:40960\n"
MF "v[144:147],a[200:203],a[144:147],v[144:147]\n"
"s_sub_u32 s15,s82,7864320\n"
TR "a[220:221],v0 offset:36864\n"
MF "v[152:155],a[200:203],a[148:151],v[152:155]\n"
"s_min_u32 s82,s82,s15\n"
TR "a[222:223],v0 offset:37376\n"
MF "v[152:155],a[204:207],a[188:191],v[152:155]\n"
LD "v6,s[28:31],s82 offen lds\n"
MF "v[160:163],a[204:207],a[192:195],v[160:163]\n"
"ds_read_b128 a[100:103],v3 offset:40960\n"
MF "v[160:163],a[200:203],a[152:155],v[160:163]\n"
LD "v11,s[28:31],s82 offen offset:1024 lds\n"
MF "v[168:171],a[200:203],a[156:159],v[168:171]\n"
MF "v[168:171],a[204:207],a[196:199],v[168:171]\n"
LD "v12,s[28:31],s82 offen offset:2048 lds\n"
MF "v[172:175],a[212:215],a[196:199],v[172:175]\n"
TR "a[200:201],v0 offset:32776\n"
MF "v[172:175],a[208:211],a[156:159],v[172:175]\n"
TR "a[202:203],v0 offset:33288\n"
MF "v[164:167],a[208:211],a[152:155],v[164:167]\n"
"ds_read_b128 a[64:67],v2 offset:43008\n"
MF "v[164:167],a[212:215],a[192:195],v[164:167]\n"
TR "a[204:205],v0 offset:36872\n"
MF "v[156:159],a[212:215],a[188:191],v[156:159]\n"
TR "a[206:207],v0 offset:37384\n"
MF "v[156:159],a[208:211],a[148:151],v[156:159]\n"
MF "v[148:151],a[208:211],a[144:147],v[148:151]\n"
"ds_read_b128 a[104:107],v3 offset:43008\n"
MF "v[148:151],a[212:215],a[184:187],v[148:151]\n"
MF "v[140:143],a[212:215],a[180:183],v[140:143]\n"
LD "v13,s[28:31],s82 offen offset:3072 lds\n"
MF "v[140:143],a[208:211],a[140:143],v[140:143]\n"
"s_waitcnt lgkmcnt(7)\n"
MF "v[176:179],a[216:219],a[140:143],v[176:179]\n"
TR "a[208:209],v1 offset:32768\n"
MF "v[176:179],a[220:223],a[180:183],v[176:179]\n"
TR "a[210:211],v1 offset:33280\n"
MF "v[184:187],a[220:223],a[184:187],v[184:187]\n"
"ds_read_b128 a[68:71],v2 offset:45056\n"
MF "v[184:187],a[216:219],a[144:147],v[184:187]\n"
"s_add_u32 s84,s14,20480\n"
TR "a[212:213],v1 offset:36864\n"
MF "v[192:195],a[216:219],a[148:151],v[192:195]\n"
"s_add_u32 m0,s63,22528\n"
TR "a[214:215],v1 offset:37376\n"
MF "v[192:195],a[220:223],a[188:191],v[192:195]\n"
LD "v4,s[24:27],s84 offen lds\n"
MF "v[200:203],a[220:223],a[192:195],v[200:203]\n"
"ds_read_b128 a[108:111],v3 offset:45056\n"
MF "v[200:203],a[216:219],a[152:155],v[200:203]\n"
MF "v[208:211],a[216:219],a[156:159],v[208:211]\n"
"s_add_u32 s84,s14,29696\n"
MF "v[208:211],a[220:223],a[196:199],v[208:211]\n"
"s_waitcnt lgkmcnt(7)\n"
LD "v5,s[24:27],s84 offen offset:1024 lds\n"
MF "v[212:215],a[204:207],a[196:199],v[212:215]\n"
TR "a[216:217],v1 offset:32776\n"
MF "v[212:215],a[200:203],a[156:159],v[212:215]\n"
TR "a[218:219],v1 offset:33288\n"
MF "v[204:207],a[200:203],a[152:155],v[204:207]\n"
"ds_read_b128 a[72:75],v2 offset:47104\n"
MF "v[204:207],a[204:207],a[192:195],v[204:207]\n"
TR "a[220:221],v1 offset:36872\n"
MF "v[196:199],a[204:207],a[188:191],v[196:199]\n"
TR "a[222:223],v1 offset:37384\n"
MF "v[196:199],a[200:203],a[148:151],v[196:199]\n"
MF "v[188:191],a[200:203],a[144:147],v[188:191]\n"
"v_add_u32 v0,0xffff4000,v0\n"
MF "v[188:191],a[204:207],a[184:187],v[188:191]\n"
"v_add_u32 v1,0xffff4000,v1\n"
MF "v[180:183],a[204:207],a[180:183],v[180:183]\n"
MF "v[180:183],a[200:203],a[140:143],v[180:183]\n"
"s_waitcnt vmcnt(25) lgkmcnt(0)\n"
"s_barrier\n"
MF "v[216:219],a[208:211],a[140:143],v[216:219]\n"
TR "a[200:201],v0\n"
MF "v[216:219],a[212:215],a[180:183],v[216:219]\n"
"s_add_u32 m0,s62,81920\n"
TR "a[202:203],v0 offset:512\n"
MF "v[224:227],a[212:215],a[184:187],v[224:227]\n"
"s_add_u32 s82,s66,2359552\n"
"ds_read_b128 a[112:115],v3 offset:47104\n"
MF "v[224:227],a[208:211],a[144:147],v[224:227]\n"
"s_sub_u32 s15,s82,7864320\n"
TR "a[204:205],v0 offset:4096\n"
MF "v[232:235],a[208:211],a[148:151],v[232:235]\n"
"s_min_u32 s82,s82,s15\n"
TR "a[206:207],v0 offset:4608\n"
MF "v[232:235],a[212:215],a[188:191],v[232:235]\n"
LD "v6,s[28:31],s82 offen lds\n"
MF "v[240:243],a[212:215],a[192:195],v[240:243]\n"
"ds_read_b128 a[76:79],v2 offset:49152\n"
MF "v[240:243],a[208:211],a[152:155],v[240:243]\n"
"ds_read_b128 a[116:119],v3 offset:49152\n"
MF "v[248:251],a[208:211],a[156:159],v[248:251]\n"
LD "v11,s[28:31],s82 offen offset:1024 lds\n"
MF "v[248:251],a[212:215],a[196:199],v[248:251]\n"
MF "v[252:255],a[220:223],a[196:199],v[252:255]\n"
TR "a[208:209],v0 offset:8\n"
MF "v[252:255],a[216:219],a[156:159],v[252:255]\n"
TR "a[210:211],v0 offset:520\n"
MF "v[244:247],a[216:219],a[152:155],v[244:247]\n"
MF "v[244:247],a[220:223],a[192:195],v[244:247]\n"
TR "a[212:213],v0 offset:4104\n"
MF "v[236:239],a[220:223],a[188:191],v[236:239]\n"
TR "a[214:215],v0 offset:4616\n"
MF "v[236:239],a[216:219],a[148:151],v[236:239]\n"
LD "v12,s[28:31],s82 offen offset:2048 lds\n"
MF "v[228:231],a[216:219],a[144:147],v[228:231]\n"
MF "v[228:231],a[220:223],a[184:187],v[228:231]\n"
MF "v[220:223],a[220:223],a[180:183],v[220:223]\n"
MF "v[220:223],a[216:219],a[140:143],v[220:223]\n"
"s_waitcnt lgkmcnt(4)\n"
MF "v[16:19],a[200:203],a[60:63],v[16:19]\n"
TR "a[216:217],v1\n"
MF "v[16:19],a[204:207],a[100:103],v[16:19]\n"
TR "a[218:219],v1 offset:512\n"
MF "v[24:27],a[204:207],a[104:107],v[24:27]\n"
LD "v13,s[28:31],s82 offen offset:3072 lds\n"
MF "v[24:27],a[200:203],a[64:67],v[24:27]\n"
TR "a[220:221],v1 offset:4096\n"
MF "v[32:35],a[200:203],a[68:71],v[32:35]\n"
TR "a[222:223],v1 offset:4608\n"
MF "v[32:35],a[204:207],a[108:111],v[32:35]\n"
MF "v[40:43],a[204:207],a[112:115],v[40:43]\n"
MF "v[40:43],a[200:203],a[72:75],v[40:43]\n"
"s_add_u32 s84,s14,40960\n"
MF "v[48:51],a[200:203],a[76:79],v[48:51]\n"
"s_add_u32 m0,s63,24576\n"
MF "v[48:51],a[204:207],a[116:119],v[48:51]\n"
"s_waitcnt lgkmcnt(4)\n"
LD "v4,s[24:27],s84 offen lds\n"
MF "v[52:55],a[212:215],a[116:119],v[52:55]\n"
TR "a[200:201],v1 offset:8\n"
MF "v[52:55],a[208:211],a[76:79],v[52:55]\n"
TR "a[202:203],v1 offset:520\n"
MF "v[44:47],a[208:211],a[72:75],v[44:47]\n"
MF "v[44:47],a[212:215],a[112:115],v[44:47]\n"
TR "a[204:205],v1 offset:4104\n"
MF "v[36:39],a[212:215],a[108:111],v[36:39]\n"
TR "a[206:207],v1 offset:4616\n"
MF "v[36:39],a[208:211],a[68:71],v[36:39]\n"
MF "v[28:31],a[208:211],a[64:67],v[28:31]\n"
MF "v[28:31],a[212:215],a[104:107],v[28:31]\n"
MF "v[20:23],a[212:215],a[100:103],v[20:23]\n"
MF "v[20:23],a[208:211],a[60:63],v[20:23]\n"
"s_waitcnt vmcnt(24) lgkmcnt(0)\n"
"s_barrier\n"
MF "v[56:59],a[216:219],a[60:63],v[56:59]\n"
"s_add_u32 m0,s62,0\n"
"s_add_u32 s82,s66,3145728\n"
TR "a[208:209],v0 offset:16384\n"
MF "v[56:59],a[220:223],a[100:103],v[56:59]\n"
"s_sub_u32 s15,s82,7864320\n"
"s_min_u32 s82,s82,s15\n"
TR "a[210:211],v0 offset:16896\n"
MF "v[64:67],a[220:223],a[104:107],v[64:67]\n"
LD "v6,s[28:31],s82 offen lds\n"
MF "v[64:67],a[216:219],a[64:67],v[64:67]\n"
TR "a[212:213],v0 offset:20480\n"
MF "v[72:75],a[216:219],a[68:71],v[72:75]\n"
TR "a[214:215],v0 offset:20992\n"
MF "v[72:75],a[220:223],a[108:111],v[72:75]\n"
MF "v[80:83],a[220:223],a[112:115],v[80:83]\n"
LD "v11,s[28:31],s82 offen offset:1024 lds\n"
MF "v[80:83],a[216:219],a[72:75],v[80:83]\n"
MF "v[88:91],a[216:219],a[76:79],v[88:91]\n"
MF "v[88:91],a[220:223],a[116:119],v[88:91]\n"
MF "v[92:95],a[204:207],a[116:119],v[92:95]\n"
TR "a[216:217],v0 offset:16392\n"
MF "v[92:95],a[200:203],a[76:79],v[92:95]\n"
TR "a[218:219],v0 offset:16904\n"
MF "v[84:87],a[200:203],a[72:75],v[84:87]\n"
LD "v12,s[28:31],s82 offen offset:2048 lds\n"
MF "v[84:87],a[204:207],a[112:115],v[84:87]\n"
TR "a[220:221],v0 offset:20488\n"
MF "v[76:79],a[204:207],a[108:111],v[76:79]\n"
TR "a[222:223],v0 offset:21000\n"
MF "v[76:79],a[200:203],a[68:71],v[76:79]\n"
MF "v[68:71],a[200:203],a[64:67],v[68:71]\n"
MF "v[68:71],a[204:207],a[104:107],v[68:71]\n"
MF "v[60:63],a[204:207],a[100:103],v[60:63]\n"
LD "v13,s[28:31],s82 offen offset:3072 lds\n"
MF "v[60:63],a[200:203],a[60:63],v[60:63]\n"
"s_waitcnt lgkmcnt(4)\n"
MF "v[96:99],a[208:211],a[60:63],v[96:99]\n"
TR "a[200:201],v1 offset:16384\n"
MF "v[96:99],a[212:215],a[100:103],v[96:99]\n"
TR "a[202:203],v1 offset:16896\n"
MF "v[104:107],a[212:215],a[104:107],v[104:107]\n"
"s_add_u32 s14,s81,640\n"
"s_sub_u32 s15,s14,1280\n"
MF "v[104:107],a[208:211],a[64:67],v[104:107]\n"
"s_min_u32 s14,s14,s15\n"
"s_add_u32 s14,s14,s68\n"
TR "a[204:205],v1 offset:20480\n"
MF "v[112:115],a[208:211],a[68:71],v[112:115]\n"
"s_add_u32 s84,s14,0\n"
"s_add_u32 m0,s63,40960\n"
TR "a[206:207],v1 offset:20992\n"
MF "v[112:115],a[212:215],a[108:111],v[112:115]\n"
LD "v4,s[24:27],s84 offen lds\n"
MF "v[120:123],a[212:215],a[112:115],v[120:123]\n"
MF "v[120:123],a[208:211],a[72:75],v[120:123]\n"
MF "v[128:131],a[208:211],a[76:79],v[128:131]\n"
"s_add_u32 s84,s14,9216\n"
MF "v[128:131],a[212:215],a[116:119],v[128:131]\n"
"s_waitcnt lgkmcnt(4)\n"
LD "v5,s[24:27],s84 offen offset:1024 lds\n"
MF "v[132:135],a[220:223],a[116:119],v[132:135]\n"
TR "a[208:209],v1 offset:16392\n"
MF "v[132:135],a[216:219],a[76:79],v[132:135]\n"
TR "a[210:211],v1 offset:16904\n"
MF "v[124:127],a[216:219],a[72:75],v[124:127]\n"
MF "v[124:127],a[220:223],a[112:115],v[124:127]\n"
TR "a[212:213],v1 offset:20488\n"
MF "v[116:119],a[220:223],a[108:111],v[116:119]\n"
TR "a[214:215],v1 offset:21000\n"
MF "v[116:119],a[216:219],a[68:71],v[116:119]\n"
MF "v[108:111],a[216:219],a[64:67],v[108:111]\n"
MF "v[108:111],a[220:223],a[104:107],v[108:111]\n"
MF "v[100:103],a[220:223],a[100:103],v[100:103]\n"
MF "v[100:103],a[216:219],a[60:63],v[100:103]\n"
"s_waitcnt vmcnt(23) lgkmcnt(0)\n"
"s_barrier\n"
MF "v[136:139],a[200:203],a[60:63],v[136:139]\n"
TR "a[216:217],v0 offset:32768\n"
MF "v[136:139],a[204:207],a[100:103],v[136:139]\n"
"s_add_u32 m0,s62,16384\n"
TR "a[218:219],v0 offset:33280\n"
MF "v[144:147],a[204:207],a[104:107],v[144:147]\n"
"s_add_u32 s82,s66,3145856\n"
"ds_read_b128 a[140:143],v2\n"
MF "v[144:147],a[200:203],a[64:67],v[144:147]\n"
"s_sub_u32 s15,s82,7864320\n"
TR "a[220:221],v0 offset:36864\n"
MF "v[152:155],a[200:203],a[68:71],v[152:155]\n"
"s_min_u32 s82,s82,s15\n"
TR "a[222:223],v0 offset:37376\n"
MF "v[152:155],a[204:207],a[108:111],v[152:155]\n"
LD "v6,s[28:31],s82 offen lds\n"
MF "v[160:163],a[204:207],a[112:115],v[160:163]\n"
"ds_read_b128 a[180:183],v3\n"
MF "v[160:163],a[200:203],a[72:75],v[160:163]\n"
LD "v11,s[28:31],s82 offen offset:1024 lds\n"
MF "v[168:171],a[200:203],a[76:79],v[168:171]\n"
MF "v[168:171],a[204:207],a[116:119],v[168:171]\n"
LD "v12,s[28:31],s82 offen offset:2048 lds\n"
MF "v[172:175],a[212:215],a[116:119],v[172:175]\n"
TR "a[200:201],v0 offset:32776\n"
MF "v[172:175],a[208:211],a[76:79],v[172:175]\n"
TR "a[202:203],v0 offset:33288\n"
MF "v[164:167],a[208:211],a[72:75],v[164:167]\n"
"ds_read_b128 a[144:147],v2 offset:2048\n"
MF "v[164:167],a[212:215],a[112:115],v[164:167]\n"
TR "a[204:205],v0 offset:36872\n"
MF "v[156:159],a[212:215],a[108:111],v[156:159]\n"
TR "a[206:207],v0 offset:37384\n"
MF "v[156:159],a[208:211],a[68:71],v[156:159]\n"
MF "v[148:151],a[208:211],a[64:67],v[148:151]\n"
"ds_read_b128 a[184:187],v3 offset:2048\n"
MF "v[148:151],a[212:215],a[104:107],v[148:151]\n"
MF "v[140:143],a[212:215],a[100:103],v[140:143]\n"
LD "v13,s[28:31],s82 offen offset:3072 lds\n"
MF "v[140:143],a[208:211],a[60:63],v[140:143]\n"
"s_waitcnt lgkmcnt(7)\n"
MF "v[176:179],a[216:219],a[60:63],v[176:179]\n"
TR "a[208:209],v1 offset:32768\n"
MF "v[176:179],a[220:223],a[100:103],v[176:179]\n"
TR "a[210:211],v1 offset:33280\n"
MF "v[184:187],a[220:223],a[104:107],v[184:187]\n"
"ds_read_b128 a[148:151],v2 offset:4096\n"
MF "v[184:187],a[216:219],a[64:67],v[184:187]\n"
"s_add_u32 s84,s14,20480\n"
TR "a[212:213],v1 offset:36864\n"
MF "v[192:195],a[216:219],a[68:71],v[192:195]\n"
"s_add_u32 m0,s63,43008\n"
TR "a[214:215],v1 offset:37376\n"
MF "v[192:195],a[220:223],a[108:111],v[192:195]\n"
LD "v4,s[24:27],s84 offen lds\n"
MF "v[200:203],a[220:223],a[112:115],v[200:203]\n"
"ds_read_b128 a[188:191],v3 offset:4096\n"
MF "v[200:203],a[216:219],a[72:75],v[200:203]\n"
MF "v[208:211],a[216:219],a[76:79],v[208:211]\n"
"s_add_u32 s84,s14,29696\n"
MF "v[208:211],a[220:223],a[116:119],v[208:211]\n"
"s_waitcnt lgkmcnt(7)\n"
LD "v5,s[24:27],s84 offen offset:1024 lds\n"
MF "v[212:215],a[204:207],a[116:119],v[212:215]\n"
TR "a[216:217],v1 offset:32776\n"
MF "v[212:215],a[200:203],a[76:79],v[212:215]\n"
TR "a[218:219],v1 offset:33288\n"
MF "v[204:207],a[200:203],a[72:75],v[204:207]\n"
"ds_read_b128 a[152:155],v2 offset:6144\n"
MF "v[204:207],a[204:207],a[112:115],v[204:207]\n"
TR "a[220:221],v1 offset:36872\n"
MF "v[196:199],a[204:207],a[108:111],v[196:199]\n"
TR "a[222:223],v1 offset:37384\n"
MF "v[196:199],a[200:203],a[68:71],v[196:199]\n"
MF "v[188:191],a[200:203],a[64:67],v[188:191]\n"
"v_add_u32 v0,0xc000,v0\n"
MF "v[188:191],a[204:207],a[104:107],v[188:191]\n"
"v_add_u32 v1,0xc000,v1\n"
MF "v[180:183],a[204:207],a[100:103],v[180:183]\n"
MF "v[180:183],a[200:203],a[60:63],v[180:183]\n"
"s_waitcnt vmcnt(25) lgkmcnt(0)\n"
"s_barrier\n"
MF "v[216:219],a[208:211],a[60:63],v[216:219]\n"
TR "a[200:201],v0\n"
MF "v[216:219],a[212:215],a[100:103],v[216:219]\n"
"s_add_u32 m0,s62,32768\n"
TR "a[202:203],v0 offset:512\n"
MF "v[224:227],a[212:215],a[104:107],v[224:227]\n"
"s_add_u32 s82,s66,3145984\n"
"ds_read_b128 a[192:195],v3 offset:6144\n"
MF "v[224:227],a[208:211],a[64:67],v[224:227]\n"
"s_sub_u32 s15,s82,7864320\n"
TR "a[204:205],v0 offset:4096\n"
MF "v[232:235],a[208:211],a[68:71],v[232:235]\n"
"s_min_u32 s82,s82,s15\n"
TR "a[206:207],v0 offset:4608\n"
MF "v[232:235],a[212:215],a[108:111],v[232:235]\n"
LD "v6,s[28:31],s82 offen lds\n"
MF "v[240:243],a[212:215],a[112:115],v[240:243]\n"
"ds_read_b128 a[156:159],v2 offset:8192\n"
MF "v[240:243],a[208:211],a[72:75],v[240:243]\n"
"ds_read_b128 a[196:199],v3 offset:8192\n"
MF "v[248:251],a[208:211],a[76:79],v[248:251]\n"
LD "v11,s[28:31],s82 offen offset:1024 lds\n"
MF "v[248:251],a[212:215],a[116:119],v[248:251]\n"
MF "v[252:255],a[220:223],a[116:119],v[252:255]\n"
TR "a[208:209],v0 offset:8\n"
MF "v[252:255],a[216:219],a[76:79],v[252:255]\n"
TR "a[210:211],v0 offset:520\n"
MF "v[244:247],a[216:219],a[72:75],v[244:247]\n"
MF "v[244:247],a[220:223],a[112:115],v[244:247]\n"
TR "a[212:213],v0 offset:4104\n"
MF "v[236:239],a[220:223],a[108:111],v[236:239]\n"
TR "a[214:215],v0 offset:4616\n"
MF "v[236:239],a[216:219],a[68:71],v[236:239]\n"
LD "v12,s[28:31],s82 offen offset:2048 lds\n"
MF "v[228:231],a[216:219],a[64:67],v[228:231]\n"
MF "v[228:231],a[220:223],a[104:107],v[228:231]\n"
MF "v[220:223],a[220:223],a[100:103],v[220:223]\n"
MF "v[220:223],a[216:219],a[60:63],v[220:223]\n"
"s_waitcnt lgkmcnt(4)\n"
MF "v[16:19],a[200:203],a[140:143],v[16:19]\n"
TR "a[216:217],v1\n"
MF "v[16:19],a[204:207],a[180:183],v[16:19]\n"
TR "a[218:219],v1 offset:512\n"
MF "v[24:27],a[204:207],a[184:187],v[24:27]\n"
LD "v13,s[28:31],s82 offen offset:3072 lds\n"
MF "v[24:27],a[200:203],a[144:147],v[24:27]\n"
TR "a[220:221],v1 offset:4096\n"
MF "v[32:35],a[200:203],a[148:151],v[32:35]\n"
TR "a[222:223],v1 offset:4608\n"
MF "v[32:35],a[204:207],a[188:191],v[32:35]\n"
MF "v[40:43],a[204:207],a[192:195],v[40:43]\n"
MF "v[40:43],a[200:203],a[152:155],v[40:43]\n"
"s_add_u32 s84,s14,40960\n"
MF "v[48:51],a[200:203],a[156:159],v[48:51]\n"
"s_add_u32 m0,s63,45056\n"
MF "v[48:51],a[204:207],a[196:199],v[48:51]\n"
"s_waitcnt lgkmcnt(4)\n"
LD "v4,s[24:27],s84 offen lds\n"
MF "v[52:55],a[212:215],a[196:199],v[52:55]\n"
TR "a[200:201],v1 offset:8\n"
MF "v[52:55],a[208:211],a[156:159],v[52:55]\n"
TR "a[202:203],v1 offset:520\n"
MF "v[44:47],a[208:211],a[152:155],v[44:47]\n"
MF "v[44:47],a[212:215],a[192:195],v[44:47]\n"
TR "a[204:205],v1 offset:4104\n"
MF "v[36:39],a[212:215],a[188:191],v[36:39]\n"
TR "a[206:207],v1 offset:4616\n"
MF "v[36:39],a[208:211],a[148:151],v[36:39]\n"
MF "v[28:31],a[208:211],a[144:147],v[28:31]\n"
MF "v[28:31],a[212:215],a[184:187],v[28:31]\n"
MF "v[20:23],a[212:215],a[180:183],v[20:23]\n"
MF "v[20:23],a[208:211],a[140:143],v[20:23]\n"
"s_waitcnt vmcnt(24) lgkmcnt(0)\n"
"s_barrier\n"
MF "v[56:59],a[216:219],a[140:143],v[56:59]\n"
"s_add_u32 m0,s62,49152\n"
"s_add_u32 s82,s66,3932160\n"
TR "a[208:209],v0 offset:16384\n"
MF "v[56:59],a[220:223],a[180:183],v[56:59]\n"
"s_sub_u32 s15,s82,7864320\n"
"s_min_u32 s82,s82,s15\n"
TR "a[210:211],v0 offset:16896\n"
MF "v[64:67],a[220:223],a[184:187],v[64:67]\n"
LD "v6,s[28:31],s82 offen lds\n"
MF "v[64:67],a[216:219],a[144:147],v[64:67]\n"
TR "a[212:213],v0 offset:20480\n"
MF "v[72:75],a[216:219],a[148:151],v[72:75]\n"
TR "a[214:215],v0 offset:20992\n"
MF "v[72:75],a[220:223],a[188:191],v[72:75]\n"
MF "v[80:83],a[220:223],a[192:195],v[80:83]\n"
LD "v11,s[28:31],s82 offen offset:1024 lds\n"
MF "v[80:83],a[216:219],a[152:155],v[80:83]\n"
MF "v[88:91],a[216:219],a[156:159],v[88:91]\n"
MF "v[88:91],a[220:223],a[196:199],v[88:91]\n"
MF "v[92:95],a[204:207],a[196:199],v[92:95]\n"
TR "a[216:217],v0 offset:16392\n"
MF "v[92:95],a[200:203],a[156:159],v[92:95]\n"
TR "a[218:219],v0 offset:16904\n"
MF "v[84:87],a[200:203],a[152:155],v[84:87]\n"
LD "v12,s[28:31],s82 offen offset:2048 lds\n"
MF "v[84:87],a[204:207],a[192:195],v[84:87]\n"
TR "a[220:221],v0 offset:20488\n"
MF "v[76:79],a[204:207],a[188:191],v[76:79]\n"
TR "a[222:223],v0 offset:21000\n"
MF "v[76:79],a[200:203],a[148:151],v[76:79]\n"
MF "v[68:71],a[200:203],a[144:147],v[68:71]\n"
MF "v[68:71],a[204:207],a[184:187],v[68:71]\n"
MF "v[60:63],a[204:207],a[180:183],v[60:63]\n"
LD "v13,s[28:31],s82 offen offset:3072 lds\n"
MF "v[60:63],a[200:203],a[140:143],v[60:63]\n"
"s_waitcnt lgkmcnt(4)\n"
MF "v[96:99],a[208:211],a[140:143],v[96:99]\n"
TR "a[200:201],v1 offset:16384\n"
MF "v[96:99],a[212:215],a[180:183],v[96:99]\n"
TR "a[202:203],v1 offset:16896\n"
MF "v[104:107],a[212:215],a[184:187],v[104:107]\n"
"s_add_u32 s14,s81,768\n"
"s_sub_u32 s15,s14,1280\n"
MF "v[104:107],a[208:211],a[144:147],v[104:107]\n"
"s_min_u32 s14,s14,s15\n"
"s_add_u32 s14,s14,s68\n"
TR "a[204:205],v1 offset:20480\n"
MF "v[112:115],a[208:211],a[148:151],v[112:115]\n"
"s_add_u32 s84,s14,0\n"
"s_add_u32 m0,s63,0\n"
TR "a[206:207],v1 offset:20992\n"
MF "v[112:115],a[212:215],a[188:191],v[112:115]\n"
LD "v4,s[24:27],s84 offen lds\n"
MF "v[120:123],a[212:215],a[192:195],v[120:123]\n"
MF "v[120:123],a[208:211],a[152:155],v[120:123]\n"
MF "v[128:131],a[208:211],a[156:159],v[128:131]\n"
"s_add_u32 s84,s14,9216\n"
MF "v[128:131],a[212:215],a[196:199],v[128:131]\n"
"s_waitcnt lgkmcnt(4)\n"
LD "v5,s[24:27],s84 offen offset:1024 lds\n"
MF "v[132:135],a[220:223],a[196:199],v[132:135]\n"
TR "a[208:209],v1 offset:16392\n"
MF "v[132:135],a[216:219],a[156:159],v[132:135]\n"
TR "a[210:211],v1 offset:16904\n"
MF "v[124:127],a[216:219],a[152:155],v[124:127]\n"
MF "v[124:127],a[220:223],a[192:195],v[124:127]\n"
TR "a[212:213],v1 offset:20488\n"
MF "v[116:119],a[220:223],a[188:191],v[116:119]\n"
TR "a[214:215],v1 offset:21000\n"
MF "v[116:119],a[216:219],a[148:151],v[116:119]\n"
MF "v[108:111],a[216:219],a[144:147],v[108:111]\n"
MF "v[108:111],a[220:223],a[184:187],v[108:111]\n"
MF "v[100:103],a[220:223],a[180:183],v[100:103]\n"
MF "v[100:103],a[216:219],a[140:143],v[100:103]\n"
"s_waitcnt vmcnt(23) lgkmcnt(0)\n"
"s_barrier\n"
MF "v[136:139],a[200:203],a[140:143],v[136:139]\n"
TR "a[216:217],v0 offset:32768\n"
MF "v[136:139],a[204:207],a[180:183],v[136:139]\n"
"s_add_u32 m0,s62,65536\n"
TR "a[218:219],v0 offset:33280\n"
MF "v[144:147],a[204:207],a[184:187],v[144:147]\n"
"s_add_u32 s82,s66,3932288\n"
"ds_read_b128 a[60:63],v2 offset:20480\n"
MF "v[144:147],a[200:203],a[144:147],v[144:147]\n"
"s_sub_u32 s15,s82,7864320\n"
TR "a[220:221],v0 offset:36864\n"
MF "v[152:155],a[200:203],a[148:151],v[152:155]\n"
"s_min_u32 s82,s82,s15\n"
TR "a[222:223],v0 offset:37376\n"
MF "v[152:155],a[204:207],a[188:191],v[152:155]\n"
LD "v6,s[28:31],s82 offen lds\n"
MF "v[160:163],a[204:207],a[192:195],v[160:163]\n"
"ds_read_b128 a[100:103],v3 offset:20480\n"
MF "v[160:163],a[200:203],a[152:155],v[160:163]\n"
LD "v11,s[28:31],s82 offen offset:1024 lds\n"
MF "v[168:171],a[200:203],a[156:159],v[168:171]\n"
MF "v[168:171],a[204:207],a[196:199],v[168:171]\n"
LD "v12,s[28:31],s82 offen offset:2048 lds\n"
MF "v[172:175],a[212:215],a[196:199],v[172:175]\n"
TR "a[200:201],v0 offset:32776\n"
MF "v[172:175],a[208:211],a[156:159],v[172:175]\n"
TR "a[202:203],v0 offset:33288\n"
MF "v[164:167],a[208:211],a[152:155],v[164:167]\n"
"ds_read_b128 a[64:67],v2 offset:22528\n"
MF "v[164:167],a[212:215],a[192:195],v[164:167]\n"
TR "a[204:205],v0 offset:36872\n"
MF "v[156:159],a[212:215],a[188:191],v[156:159]\n"
TR "a[206:207],v0 offset:37384\n"
MF "v[156:159],a[208:211],a[148:151],v[156:159]\n"
MF "v[148:151],a[208:211],a[144:147],v[148:151]\n"
"ds_read_b128 a[104:107],v3 offset:22528\n"
MF "v[148:151],a[212:215],a[184:187],v[148:151]\n"
MF "v[140:143],a[212:215],a[180:183],v[140:143]\n"
LD "v13,s[28:31],s82 offen offset:3072 lds\n"
MF "v[140:143],a[208:211],a[140:143],v[140:143]\n"
"s_waitcnt lgkmcnt(7)\n"
MF "v[176:179],a[216:219],a[140:143],v[176:179]\n"
TR "a[208:209],v1 offset:32768\n"
MF "v[176:179],a[220:223],a[180:183],v[176:179]\n"
TR "a[210:211],v1 offset:33280\n"
MF "v[184:187],a[220:223],a[184:187],v[184:187]\n"
"ds_read_b128 a[68:71],v2 offset:24576\n"
MF "v[184:187],a[216:219],a[144:147],v[184:187]\n"
"s_add_u32 s84,s14,20480\n"
TR "a[212:213],v1 offset:36864\n"
MF "v[192:195],a[216:219],a[148:151],v[192:195]\n"
"s_add_u32 m0,s63,2048\n"
TR "a[214:215],v1 offset:37376\n"
MF "v[192:195],a[220:223],a[188:191],v[192:195]\n"
LD "v4,s[24:27],s84 offen lds\n"
MF "v[200:203],a[220:223],a[192:195],v[200:203]\n"
"ds_read_b128 a[108:111],v3 offset:24576\n"
MF "v[200:203],a[216:219],a[152:155],v[200:203]\n"
MF "v[208:211],a[216:219],a[156:159],v[208:211]\n"
"s_add_u32 s84,s14,29696\n"
MF "v[208:211],a[220:223],a[196:199],v[208:211]\n"
"s_waitcnt lgkmcnt(7)\n"
LD "v5,s[24:27],s84 offen offset:1024 lds\n"
MF "v[212:215],a[204:207],a[196:199],v[212:215]\n"
TR "a[216:217],v1 offset:32776\n"
MF "v[212:215],a[200:203],a[156:159],v[212:215]\n"
TR "a[218:219],v1 offset:33288\n"
MF "v[204:207],a[200:203],a[152:155],v[204:207]\n"
"ds_read_b128 a[72:75],v2 offset:26624\n"
MF "v[204:207],a[204:207],a[192:195],v[204:207]\n"
TR "a[220:221],v1 offset:36872\n"
MF "v[196:199],a[204:207],a[188:191],v[196:199]\n"
TR "a[222:223],v1 offset:37384\n"
MF "v[196:199],a[200:203],a[148:151],v[196:199]\n"
MF "v[188:191],a[200:203],a[144:147],v[188:191]\n"
"v_add_u32 v0,0xffff4000,v0\n"
MF "v[188:191],a[204:207],a[184:187],v[188:191]\n"
"v_add_u32 v1,0xffff4000,v1\n"
MF "v[180:183],a[204:207],a[180:183],v[180:183]\n"
MF "v[180:183],a[200:203],a[140:143],v[180:183]\n"
"s_waitcnt vmcnt(25) lgkmcnt(0)\n"
"s_barrier\n"
MF "v[216:219],a[208:211],a[140:143],v[216:219]\n"
TR "a[200:201],v0\n"
MF "v[216:219],a[212:215],a[180:183],v[216:219]\n"
"s_add_u32 m0,s62,81920\n"
TR "a[202:203],v0 offset:512\n"
MF "v[224:227],a[212:215],a[184:187],v[224:227]\n"
"s_add_u32 s82,s66,3932416\n"
"ds_read_b128 a[112:115],v3 offset:26624\n"
MF "v[224:227],a[208:211],a[144:147],v[224:227]\n"
"s_sub_u32 s15,s82,7864320\n"
TR "a[204:205],v0 offset:4096\n"
MF "v[232:235],a[208:211],a[148:151],v[232:235]\n"
"s_min_u32 s82,s82,s15\n"
TR "a[206:207],v0 offset:4608\n"
MF "v[232:235],a[212:215],a[188:191],v[232:235]\n"
LD "v6,s[28:31],s82 offen lds\n"
MF "v[240:243],a[212:215],a[192:195],v[240:243]\n"
"ds_read_b128 a[76:79],v2 offset:28672\n"
MF "v[240:243],a[208:211],a[152:155],v[240:243]\n"
"ds_read_b128 a[116:119],v3 offset:28672\n"
MF "v[248:251],a[208:211],a[156:159],v[248:251]\n"
LD "v11,s[28:31],s82 offen offset:1024 lds\n"
MF "v[248:251],a[212:215],a[196:199],v[248:251]\n"
MF "v[252:255],a[220:223],a[196:199],v[252:255]\n"
TR "a[208:209],v0 offset:8\n"
MF "v[252:255],a[216:219],a[156:159],v[252:255]\n"
TR "a[210:211],v0 offset:520\n"
MF "v[244:247],a[216:219],a[152:155],v[244:247]\n"
MF "v[244:247],a[220:223],a[192:195],v[244:247]\n"
TR "a[212:213],v0 offset:4104\n"
MF "v[236:239],a[220:223],a[188:191],v[236:239]\n"
TR "a[214:215],v0 offset:4616\n"
MF "v[236:239],a[216:219],a[148:151],v[236:239]\n"
LD "v12,s[28:31],s82 offen offset:2048 lds\n"
MF "v[228:231],a[216:219],a[144:147],v[228:231]\n"
MF "v[228:231],a[220:223],a[184:187],v[228:231]\n"
MF "v[220:223],a[220:223],a[180:183],v[220:223]\n"
MF "v[220:223],a[216:219],a[140:143],v[220:223]\n"
"s_waitcnt lgkmcnt(4)\n"
MF "v[16:19],a[200:203],a[60:63],v[16:19]\n"
TR "a[216:217],v1\n"
MF "v[16:19],a[204:207],a[100:103],v[16:19]\n"
TR "a[218:219],v1 offset:512\n"
MF "v[24:27],a[204:207],a[104:107],v[24:27]\n"
LD "v13,s[28:31],s82 offen offset:3072 lds\n"
MF "v[24:27],a[200:203],a[64:67],v[24:27]\n"
TR "a[220:221],v1 offset:4096\n"
MF "v[32:35],a[200:203],a[68:71],v[32:35]\n"
TR "a[222:223],v1 offset:4608\n"
MF "v[32:35],a[204:207],a[108:111],v[32:35]\n"
MF "v[40:43],a[204:207],a[112:115],v[40:43]\n"
MF "v[40:43],a[200:203],a[72:75],v[40:43]\n"
"s_add_u32 s84,s14,40960\n"
MF "v[48:51],a[200:203],a[76:79],v[48:51]\n"
"s_add_u32 m0,s63,4096\n"
MF "v[48:51],a[204:207],a[116:119],v[48:51]\n"
"s_waitcnt lgkmcnt(4)\n"
LD "v4,s[24:27],s84 offen lds\n"
MF "v[52:55],a[212:215],a[116:119],v[52:55]\n"
TR "a[200:201],v1 offset:8\n"
MF "v[52:55],a[208:211],a[76:79],v[52:55]\n"
TR "a[202:203],v1 offset:520\n"
MF "v[44:47],a[208:211],a[72:75],v[44:47]\n"
MF "v[44:47],a[212:215],a[112:115],v[44:47]\n"
TR "a[204:205],v1 offset:4104\n"
MF "v[36:39],a[212:215],a[108:111],v[36:39]\n"
TR "a[206:207],v1 offset:4616\n"
MF "v[36:39],a[208:211],a[68:71],v[36:39]\n"
MF "v[28:31],a[208:211],a[64:67],v[28:31]\n"
MF "v[28:31],a[212:215],a[104:107],v[28:31]\n"
MF "v[20:23],a[212:215],a[100:103],v[20:23]\n"
MF "v[20:23],a[208:211],a[60:63],v[20:23]\n"
"s_waitcnt vmcnt(24) lgkmcnt(0)\n"
"s_barrier\n"
MF "v[56:59],a[216:219],a[60:63],v[56:59]\n"
"s_add_u32 m0,s62,0\n"
"s_add_u32 s82,s66,4718592\n"
TR "a[208:209],v0 offset:16384\n"
MF "v[56:59],a[220:223],a[100:103],v[56:59]\n"
"s_sub_u32 s15,s82,7864320\n"
"s_min_u32 s82,s82,s15\n"
TR "a[210:211],v0 offset:16896\n"
MF "v[64:67],a[220:223],a[104:107],v[64:67]\n"
LD "v6,s[28:31],s82 offen lds\n"
MF "v[64:67],a[216:219],a[64:67],v[64:67]\n"
TR "a[212:213],v0 offset:20480\n"
MF "v[72:75],a[216:219],a[68:71],v[72:75]\n"
TR "a[214:215],v0 offset:20992\n"
MF "v[72:75],a[220:223],a[108:111],v[72:75]\n"
MF "v[80:83],a[220:223],a[112:115],v[80:83]\n"
LD "v11,s[28:31],s82 offen offset:1024 lds\n"
MF "v[80:83],a[216:219],a[72:75],v[80:83]\n"
MF "v[88:91],a[216:219],a[76:79],v[88:91]\n"
MF "v[88:91],a[220:223],a[116:119],v[88:91]\n"
MF "v[92:95],a[204:207],a[116:119],v[92:95]\n"
TR "a[216:217],v0 offset:16392\n"
MF "v[92:95],a[200:203],a[76:79],v[92:95]\n"
TR "a[218:219],v0 offset:16904\n"
MF "v[84:87],a[200:203],a[72:75],v[84:87]\n"
LD "v12,s[28:31],s82 offen offset:2048 lds\n"
MF "v[84:87],a[204:207],a[112:115],v[84:87]\n"
TR "a[220:221],v0 offset:20488\n"
MF "v[76:79],a[204:207],a[108:111],v[76:79]\n"
TR "a[222:223],v0 offset:21000\n"
MF "v[76:79],a[200:203],a[68:71],v[76:79]\n"
MF "v[68:71],a[200:203],a[64:67],v[68:71]\n"
MF "v[68:71],a[204:207],a[104:107],v[68:71]\n"
MF "v[60:63],a[204:207],a[100:103],v[60:63]\n"
LD "v13,s[28:31],s82 offen offset:3072 lds\n"
MF "v[60:63],a[200:203],a[60:63],v[60:63]\n"
"s_waitcnt lgkmcnt(4)\n"
MF "v[96:99],a[208:211],a[60:63],v[96:99]\n"
TR "a[200:201],v1 offset:16384\n"
MF "v[96:99],a[212:215],a[100:103],v[96:99]\n"
TR "a[202:203],v1 offset:16896\n"
MF "v[104:107],a[212:215],a[104:107],v[104:107]\n"
"s_add_u32 s14,s81,896\n"
"s_sub_u32 s15,s14,1280\n"
MF "v[104:107],a[208:211],a[64:67],v[104:107]\n"
"s_min_u32 s14,s14,s15\n"
"s_add_u32 s14,s14,s68\n"
TR "a[204:205],v1 offset:20480\n"
MF "v[112:115],a[208:211],a[68:71],v[112:115]\n"
"s_add_u32 s84,s14,0\n"
"s_add_u32 m0,s63,20480\n"
TR "a[206:207],v1 offset:20992\n"
MF "v[112:115],a[212:215],a[108:111],v[112:115]\n"
LD "v4,s[24:27],s84 offen lds\n"
MF "v[120:123],a[212:215],a[112:115],v[120:123]\n"
MF "v[120:123],a[208:211],a[72:75],v[120:123]\n"
MF "v[128:131],a[208:211],a[76:79],v[128:131]\n"
"s_add_u32 s84,s14,9216\n"
MF "v[128:131],a[212:215],a[116:119],v[128:131]\n"
"s_waitcnt lgkmcnt(4)\n"
LD "v5,s[24:27],s84 offen offset:1024 lds\n"
MF "v[132:135],a[220:223],a[116:119],v[132:135]\n"
TR "a[208:209],v1 offset:16392\n"
MF "v[132:135],a[216:219],a[76:79],v[132:135]\n"
TR "a[210:211],v1 offset:16904\n"
MF "v[124:127],a[216:219],a[72:75],v[124:127]\n"
MF "v[124:127],a[220:223],a[112:115],v[124:127]\n"
TR "a[212:213],v1 offset:20488\n"
MF "v[116:119],a[220:223],a[108:111],v[116:119]\n"
TR "a[214:215],v1 offset:21000\n"
MF "v[116:119],a[216:219],a[68:71],v[116:119]\n"
MF "v[108:111],a[216:219],a[64:67],v[108:111]\n"
MF "v[108:111],a[220:223],a[104:107],v[108:111]\n"
MF "v[100:103],a[220:223],a[100:103],v[100:103]\n"
MF "v[100:103],a[216:219],a[60:63],v[100:103]\n"
"s_waitcnt vmcnt(23) lgkmcnt(0)\n"
"s_barrier\n"
MF "v[136:139],a[200:203],a[60:63],v[136:139]\n"
TR "a[216:217],v0 offset:32768\n"
MF "v[136:139],a[204:207],a[100:103],v[136:139]\n"
"s_add_u32 m0,s62,16384\n"
TR "a[218:219],v0 offset:33280\n"
MF "v[144:147],a[204:207],a[104:107],v[144:147]\n"
"s_add_u32 s82,s66,4718720\n"
"ds_read_b128 a[140:143],v2 offset:40960\n"
MF "v[144:147],a[200:203],a[64:67],v[144:147]\n"
"s_sub_u32 s15,s82,7864320\n"
TR "a[220:221],v0 offset:36864\n"
MF "v[152:155],a[200:203],a[68:71],v[152:155]\n"
"s_min_u32 s82,s82,s15\n"
TR "a[222:223],v0 offset:37376\n"
MF "v[152:155],a[204:207],a[108:111],v[152:155]\n"
LD "v6,s[28:31],s82 offen lds\n"
MF "v[160:163],a[204:207],a[112:115],v[160:163]\n"
"ds_read_b128 a[180:183],v3 offset:40960\n"
MF "v[160:163],a[200:203],a[72:75],v[160:163]\n"
LD "v11,s[28:31],s82 offen offset:1024 lds\n"
MF "v[168:171],a[200:203],a[76:79],v[168:171]\n"
MF "v[168:171],a[204:207],a[116:119],v[168:171]\n"
LD "v12,s[28:31],s82 offen offset:2048 lds\n"
MF "v[172:175],a[212:215],a[116:119],v[172:175]\n"
TR "a[200:201],v0 offset:32776\n"
MF "v[172:175],a[208:211],a[76:79],v[172:175]\n"
TR "a[202:203],v0 offset:33288\n"
MF "v[164:167],a[208:211],a[72:75],v[164:167]\n"
"ds_read_b128 a[144:147],v2 offset:43008\n"
MF "v[164:167],a[212:215],a[112:115],v[164:167]\n"
TR "a[204:205],v0 offset:36872\n"
MF "v[156:159],a[212:215],a[108:111],v[156:159]\n"
TR "a[206:207],v0 offset:37384\n"
MF "v[156:159],a[208:211],a[68:71],v[156:159]\n"
MF "v[148:151],a[208:211],a[64:67],v[148:151]\n"
"ds_read_b128 a[184:187],v3 offset:43008\n"
MF "v[148:151],a[212:215],a[104:107],v[148:151]\n"
MF "v[140:143],a[212:215],a[100:103],v[140:143]\n"
LD "v13,s[28:31],s82 offen offset:3072 lds\n"
MF "v[140:143],a[208:211],a[60:63],v[140:143]\n"
"s_waitcnt lgkmcnt(7)\n"
MF "v[176:179],a[216:219],a[60:63],v[176:179]\n"
TR "a[208:209],v1 offset:32768\n"
MF "v[176:179],a[220:223],a[100:103],v[176:179]\n"
TR "a[210:211],v1 offset:33280\n"
MF "v[184:187],a[220:223],a[104:107],v[184:187]\n"
"ds_read_b128 a[148:151],v2 offset:45056\n"
MF "v[184:187],a[216:219],a[64:67],v[184:187]\n"
"s_add_u32 s84,s14,20480\n"
TR "a[212:213],v1 offset:36864\n"
MF "v[192:195],a[216:219],a[68:71],v[192:195]\n"
"s_add_u32 m0,s63,22528\n"
TR "a[214:215],v1 offset:37376\n"
MF "v[192:195],a[220:223],a[108:111],v[192:195]\n"
LD "v4,s[24:27],s84 offen lds\n"
MF "v[200:203],a[220:223],a[112:115],v[200:203]\n"
"ds_read_b128 a[188:191],v3 offset:45056\n"
MF "v[200:203],a[216:219],a[72:75],v[200:203]\n"
MF "v[208:211],a[216:219],a[76:79],v[208:211]\n"
"s_add_u32 s84,s14,29696\n"
MF "v[208:211],a[220:223],a[116:119],v[208:211]\n"
"s_waitcnt lgkmcnt(7)\n"
LD "v5,s[24:27],s84 offen offset:1024 lds\n"
MF "v[212:215],a[204:207],a[116:119],v[212:215]\n"
TR "a[216:217],v1 offset:32776\n"
MF "v[212:215],a[200:203],a[76:79],v[212:215]\n"
TR "a[218:219],v1 offset:33288\n"
MF "v[204:207],a[200:203],a[72:75],v[204:207]\n"
"ds_read_b128 a[152:155],v2 offset:47104\n"
MF "v[204:207],a[204:207],a[112:115],v[204:207]\n"
TR "a[220:221],v1 offset:36872\n"
MF "v[196:199],a[204:207],a[108:111],v[196:199]\n"
TR "a[222:223],v1 offset:37384\n"
MF "v[196:199],a[200:203],a[68:71],v[196:199]\n"
MF "v[188:191],a[200:203],a[64:67],v[188:191]\n"
"v_add_u32 v0,0xc000,v0\n"
MF "v[188:191],a[204:207],a[104:107],v[188:191]\n"
"v_add_u32 v1,0xc000,v1\n"
MF "v[180:183],a[204:207],a[100:103],v[180:183]\n"
MF "v[180:183],a[200:203],a[60:63],v[180:183]\n"
"s_waitcnt vmcnt(25) lgkmcnt(0)\n"
"s_barrier\n"
MF "v[216:219],a[208:211],a[60:63],v[216:219]\n"
TR "a[200:201],v0\n"
MF "v[216:219],a[212:215],a[100:103],v[216:219]\n"
"s_add_u32 m0,s62,32768\n"
TR "a[202:203],v0 offset:512\n"
MF "v[224:227],a[212:215],a[104:107],v[224:227]\n"
"s_add_u32 s82,s66,4718848\n"
"ds_read_b128 a[192:195],v3 offset:47104\n"
MF "v[224:227],a[208:211],a[64:67],v[224:227]\n"
"s_sub_u32 s15,s82,7864320\n"
TR "a[204:205],v0 offset:4096\n"
MF "v[232:235],a[208:211],a[68:71],v[232:235]\n"
"s_min_u32 s82,s82,s15\n"
TR "a[206:207],v0 offset:4608\n"
MF "v[232:235],a[212:215],a[108:111],v[232:235]\n"
LD "v6,s[28:31],s82 offen lds\n"
MF "v[240:243],a[212:215],a[112:115],v[240:243]\n"
"ds_read_b128 a[156:159],v2 offset:49152\n"
MF "v[240:243],a[208:211],a[72:75],v[240:243]\n"
"ds_read_b128 a[196:199],v3 offset:49152\n"
MF "v[248:251],a[208:211],a[76:79],v[248:251]\n"
LD "v11,s[28:31],s82 offen offset:1024 lds\n"
MF "v[248:251],a[212:215],a[116:119],v[248:251]\n"
MF "v[252:255],a[220:223],a[116:119],v[252:255]\n"
TR "a[208:209],v0 offset:8\n"
MF "v[252:255],a[216:219],a[76:79],v[252:255]\n"
TR "a[210:211],v0 offset:520\n"
MF "v[244:247],a[216:219],a[72:75],v[244:247]\n"
MF "v[244:247],a[220:223],a[112:115],v[244:247]\n"
TR "a[212:213],v0 offset:4104\n"
MF "v[236:239],a[220:223],a[108:111],v[236:239]\n"
TR "a[214:215],v0 offset:4616\n"
MF "v[236:239],a[216:219],a[68:71],v[236:239]\n"
LD "v12,s[28:31],s82 offen offset:2048 lds\n"
MF "v[228:231],a[216:219],a[64:67],v[228:231]\n"
MF "v[228:231],a[220:223],a[104:107],v[228:231]\n"
MF "v[220:223],a[220:223],a[100:103],v[220:223]\n"
MF "v[220:223],a[216:219],a[60:63],v[220:223]\n"
"s_waitcnt lgkmcnt(4)\n"
MF "v[16:19],a[200:203],a[140:143],v[16:19]\n"
TR "a[216:217],v1\n"
MF "v[16:19],a[204:207],a[180:183],v[16:19]\n"
TR "a[218:219],v1 offset:512\n"
MF "v[24:27],a[204:207],a[184:187],v[24:27]\n"
LD "v13,s[28:31],s82 offen offset:3072 lds\n"
MF "v[24:27],a[200:203],a[144:147],v[24:27]\n"
TR "a[220:221],v1 offset:4096\n"
MF "v[32:35],a[200:203],a[148:151],v[32:35]\n"
TR "a[222:223],v1 offset:4608\n"
MF "v[32:35],a[204:207],a[188:191],v[32:35]\n"
MF "v[40:43],a[204:207],a[192:195],v[40:43]\n"
MF "v[40:43],a[200:203],a[152:155],v[40:43]\n"
"s_add_u32 s84,s14,40960\n"
MF "v[48:51],a[200:203],a[156:159],v[48:51]\n"
"s_add_u32 m0,s63,24576\n"
MF "v[48:51],a[204:207],a[196:199],v[48:51]\n"
"s_waitcnt lgkmcnt(4)\n"
LD "v4,s[24:27],s84 offen lds\n"
MF "v[52:55],a[212:215],a[196:199],v[52:55]\n"
TR "a[200:201],v1 offset:8\n"
MF "v[52:55],a[208:211],a[156:159],v[52:55]\n"
TR "a[202:203],v1 offset:520\n"
MF "v[44:47],a[208:211],a[152:155],v[44:47]\n"
MF "v[44:47],a[212:215],a[192:195],v[44:47]\n"
TR "a[204:205],v1 offset:4104\n"
MF "v[36:39],a[212:215],a[188:191],v[36:39]\n"
TR "a[206:207],v1 offset:4616\n"
MF "v[36:39],a[208:211],a[148:151],v[36:39]\n"
MF "v[28:31],a[208:211],a[144:147],v[28:31]\n"
MF "v[28:31],a[212:215],a[184:187],v[28:31]\n"
MF "v[20:23],a[212:215],a[180:183],v[20:23]\n"
MF "v[20:23],a[208:211],a[140:143],v[20:23]\n"
"s_waitcnt vmcnt(24) lgkmcnt(0)\n"
"s_barrier\n"
MF "v[56:59],a[216:219],a[140:143],v[56:59]\n"
"s_add_u32 m0,s62,49152\n"
"s_add_u32 s82,s66,5505024\n"
TR "a[208:209],v0 offset:16384\n"
MF "v[56:59],a[220:223],a[180:183],v[56:59]\n"
"s_sub_u32 s15,s82,7864320\n"
"s_min_u32 s82,s82,s15\n"
TR "a[210:211],v0 offset:16896\n"
MF "v[64:67],a[220:223],a[184:187],v[64:67]\n"
LD "v6,s[28:31],s82 offen lds\n"
MF "v[64:67],a[216:219],a[144:147],v[64:67]\n"
TR "a[212:213],v0 offset:20480\n"
MF "v[72:75],a[216:219],a[148:151],v[72:75]\n"
TR "a[214:215],v0 offset:20992\n"
MF "v[72:75],a[220:223],a[188:191],v[72:75]\n"
MF "v[80:83],a[220:223],a[192:195],v[80:83]\n"
LD "v11,s[28:31],s82 offen offset:1024 lds\n"
MF "v[80:83],a[216:219],a[152:155],v[80:83]\n"
MF "v[88:91],a[216:219],a[156:159],v[88:91]\n"
MF "v[88:91],a[220:223],a[196:199],v[88:91]\n"
MF "v[92:95],a[204:207],a[196:199],v[92:95]\n"
TR "a[216:217],v0 offset:16392\n"
MF "v[92:95],a[200:203],a[156:159],v[92:95]\n"
TR "a[218:219],v0 offset:16904\n"
MF "v[84:87],a[200:203],a[152:155],v[84:87]\n"
LD "v12,s[28:31],s82 offen offset:2048 lds\n"
MF "v[84:87],a[204:207],a[192:195],v[84:87]\n"
TR "a[220:221],v0 offset:20488\n"
MF "v[76:79],a[204:207],a[188:191],v[76:79]\n"
TR "a[222:223],v0 offset:21000\n"
MF "v[76:79],a[200:203],a[148:151],v[76:79]\n"
MF "v[68:71],a[200:203],a[144:147],v[68:71]\n"
MF "v[68:71],a[204:207],a[184:187],v[68:71]\n"
MF "v[60:63],a[204:207],a[180:183],v[60:63]\n"
LD "v13,s[28:31],s82 offen offset:3072 lds\n"
MF "v[60:63],a[200:203],a[140:143],v[60:63]\n"
"s_waitcnt lgkmcnt(4)\n"
MF "v[96:99],a[208:211],a[140:143],v[96:99]\n"
TR "a[200:201],v1 offset:16384\n"
MF "v[96:99],a[212:215],a[180:183],v[96:99]\n"
TR "a[202:203],v1 offset:16896\n"
MF "v[104:107],a[212:215],a[184:187],v[104:107]\n"
"s_add_u32 s14,s81,1024\n"
"s_sub_u32 s15,s14,1280\n"
MF "v[104:107],a[208:211],a[144:147],v[104:107]\n"
"s_min_u32 s14,s14,s15\n"
"s_add_u32 s14,s14,s68\n"
TR "a[204:205],v1 offset:20480\n"
MF "v[112:115],a[208:211],a[148:151],v[112:115]\n"
"s_add_u32 s84,s14,0\n"
"s_add_u32 m0,s63,40960\n"
TR "a[206:207],v1 offset:20992\n"
MF "v[112:115],a[212:215],a[188:191],v[112:115]\n"
LD "v4,s[24:27],s84 offen lds\n"
MF "v[120:123],a[212:215],a[192:195],v[120:123]\n"
MF "v[120:123],a[208:211],a[152:155],v[120:123]\n"
MF "v[128:131],a[208:211],a[156:159],v[128:131]\n"
"s_add_u32 s84,s14,9216\n"
MF "v[128:131],a[212:215],a[196:199],v[128:131]\n"
"s_waitcnt lgkmcnt(4)\n"
LD "v5,s[24:27],s84 offen offset:1024 lds\n"
MF "v[132:135],a[220:223],a[196:199],v[132:135]\n"
TR "a[208:209],v1 offset:16392\n"
MF "v[132:135],a[216:219],a[156:159],v[132:135]\n"
TR "a[210:211],v1 offset:16904\n"
MF "v[124:127],a[216:219],a[152:155],v[124:127]\n"
MF "v[124:127],a[220:223],a[192:195],v[124:127]\n"
TR "a[212:213],v1 offset:20488\n"
MF "v[116:119],a[220:223],a[188:191],v[116:119]\n"
TR "a[214:215],v1 offset:21000\n"
MF "v[116:119],a[216:219],a[148:151],v[116:119]\n"
MF "v[108:111],a[216:219],a[144:147],v[108:111]\n"
MF "v[108:111],a[220:223],a[184:187],v[108:111]\n"
MF "v[100:103],a[220:223],a[180:183],v[100:103]\n"
MF "v[100:103],a[216:219],a[140:143],v[100:103]\n"
"s_waitcnt vmcnt(23) lgkmcnt(0)\n"
"s_barrier\n"
"s_cmp_eq_u32 s57,1\n"
"s_cbranch_scc0 .Lhs12\n"
"v_bfe_u32 v14,v9,4,5\n"
"v_lshlrev_b32 v14,6,v14\n"
"s_lshl_b32 s58,s78,2\n"
"s_add_u32 s58,s58,s86\n"
"buffer_load_dword v10,v14,s[40:43],s58 offen offset:128 sc1\n"
".Lhs12:\n"
MF "v[136:139],a[200:203],a[140:143],v[136:139]\n"
TR "a[216:217],v0 offset:32768\n"
MF "v[136:139],a[204:207],a[180:183],v[136:139]\n"
"s_add_u32 m0,s62,65536\n"
TR "a[218:219],v0 offset:33280\n"
MF "v[144:147],a[204:207],a[184:187],v[144:147]\n"
"s_add_u32 s82,s66,5505152\n"
"ds_read_b128 a[60:63],v2\n"
MF "v[144:147],a[200:203],a[144:147],v[144:147]\n"
"s_sub_u32 s15,s82,7864320\n"
TR "a[220:221],v0 offset:36864\n"
MF "v[152:155],a[200:203],a[148:151],v[152:155]\n"
"s_min_u32 s82,s82,s15\n"
TR "a[222:223],v0 offset:37376\n"
MF "v[152:155],a[204:207],a[188:191],v[152:155]\n"
LD "v6,s[28:31],s82 offen lds\n"
MF "v[160:163],a[204:207],a[192:195],v[160:163]\n"
"ds_read_b128 a[100:103],v3\n"
MF "v[160:163],a[200:203],a[152:155],v[160:163]\n"
LD "v11,s[28:31],s82 offen offset:1024 lds\n"
MF "v[168:171],a[200:203],a[156:159],v[168:171]\n"
MF "v[168:171],a[204:207],a[196:199],v[168:171]\n"
LD "v12,s[28:31],s82 offen offset:2048 lds\n"
MF "v[172:175],a[212:215],a[196:199],v[172:175]\n"
TR "a[200:201],v0 offset:32776\n"
MF "v[172:175],a[208:211],a[156:159],v[172:175]\n"
TR "a[202:203],v0 offset:33288\n"
MF "v[164:167],a[208:211],a[152:155],v[164:167]\n"
"ds_read_b128 a[64:67],v2 offset:2048\n"
MF "v[164:167],a[212:215],a[192:195],v[164:167]\n"
TR "a[204:205],v0 offset:36872\n"
MF "v[156:159],a[212:215],a[188:191],v[156:159]\n"
TR "a[206:207],v0 offset:37384\n"
MF "v[156:159],a[208:211],a[148:151],v[156:159]\n"
MF "v[148:151],a[208:211],a[144:147],v[148:151]\n"
"ds_read_b128 a[104:107],v3 offset:2048\n"
MF "v[148:151],a[212:215],a[184:187],v[148:151]\n"
MF "v[140:143],a[212:215],a[180:183],v[140:143]\n"
LD "v13,s[28:31],s82 offen offset:3072 lds\n"
MF "v[140:143],a[208:211],a[140:143],v[140:143]\n"
"s_waitcnt lgkmcnt(7)\n"
MF "v[176:179],a[216:219],a[140:143],v[176:179]\n"
TR "a[208:209],v1 offset:32768\n"
MF "v[176:179],a[220:223],a[180:183],v[176:179]\n"
TR "a[210:211],v1 offset:33280\n"
MF "v[184:187],a[220:223],a[184:187],v[184:187]\n"
"ds_read_b128 a[68:71],v2 offset:4096\n"
MF "v[184:187],a[216:219],a[144:147],v[184:187]\n"
"s_add_u32 s84,s14,20480\n"
TR "a[212:213],v1 offset:36864\n"
MF "v[192:195],a[216:219],a[148:151],v[192:195]\n"
"s_add_u32 m0,s63,43008\n"
TR "a[214:215],v1 offset:37376\n"
MF "v[192:195],a[220:223],a[188:191],v[192:195]\n"
LD "v4,s[24:27],s84 offen lds\n"
MF "v[200:203],a[220:223],a[192:195],v[200:203]\n"
"ds_read_b128 a[108:111],v3 offset:4096\n"
MF "v[200:203],a[216:219],a[152:155],v[200:203]\n"
MF "v[208:211],a[216:219],a[156:159],v[208:211]\n"
"s_add_u32 s84,s14,29696\n"
MF "v[208:211],a[220:223],a[196:199],v[208:211]\n"
"s_waitcnt lgkmcnt(7)\n"
LD "v5,s[24:27],s84 offen offset:1024 lds\n"
MF "v[212:215],a[204:207],a[196:199],v[212:215]\n"
TR "a[216:217],v1 offset:32776\n"
MF "v[212:215],a[200:203],a[156:159],v[212:215]\n"
TR "a[218:219],v1 offset:33288\n"
MF "v[204:207],a[200:203],a[152:155],v[204:207]\n"
"ds_read_b128 a[72:75],v2 offset:6144\n"
MF "v[204:207],a[204:207],a[192:195],v[204:207]\n"
TR "a[220:221],v1 offset:36872\n"
MF "v[196:199],a[204:207],a[188:191],v[196:199]\n"
TR "a[222:223],v1 offset:37384\n"
MF "v[196:199],a[200:203],a[148:151],v[196:199]\n"
MF "v[188:191],a[200:203],a[144:147],v[188:191]\n"
"v_add_u32 v0,0xffff4000,v0\n"
MF "v[188:191],a[204:207],a[184:187],v[188:191]\n"
"v_add_u32 v1,0xffff4000,v1\n"
MF "v[180:183],a[204:207],a[180:183],v[180:183]\n"
MF "v[180:183],a[200:203],a[140:143],v[180:183]\n"
"s_waitcnt vmcnt(25) lgkmcnt(0)\n"
"s_barrier\n"
MF "v[216:219],a[208:211],a[140:143],v[216:219]\n"
TR "a[200:201],v0\n"
MF "v[216:219],a[212:215],a[180:183],v[216:219]\n"
"s_add_u32 m0,s62,81920\n"
TR "a[202:203],v0 offset:512\n"
MF "v[224:227],a[212:215],a[184:187],v[224:227]\n"
"s_add_u32 s82,s66,5505280\n"
"ds_read_b128 a[112:115],v3 offset:6144\n"
MF "v[224:227],a[208:211],a[144:147],v[224:227]\n"
"s_sub_u32 s15,s82,7864320\n"
TR "a[204:205],v0 offset:4096\n"
MF "v[232:235],a[208:211],a[148:151],v[232:235]\n"
"s_min_u32 s82,s82,s15\n"
TR "a[206:207],v0 offset:4608\n"
MF "v[232:235],a[212:215],a[188:191],v[232:235]\n"
LD "v6,s[28:31],s82 offen lds\n"
MF "v[240:243],a[212:215],a[192:195],v[240:243]\n"
"ds_read_b128 a[76:79],v2 offset:8192\n"
MF "v[240:243],a[208:211],a[152:155],v[240:243]\n"
"ds_read_b128 a[116:119],v3 offset:8192\n"
MF "v[248:251],a[208:211],a[156:159],v[248:251]\n"
LD "v11,s[28:31],s82 offen offset:1024 lds\n"
MF "v[248:251],a[212:215],a[196:199],v[248:251]\n"
MF "v[252:255],a[220:223],a[196:199],v[252:255]\n"
TR "a[208:209],v0 offset:8\n"
MF "v[252:255],a[216:219],a[156:159],v[252:255]\n"
TR "a[210:211],v0 offset:520\n"
MF "v[244:247],a[216:219],a[152:155],v[244:247]\n"
MF "v[244:247],a[220:223],a[192:195],v[244:247]\n"
TR "a[212:213],v0 offset:4104\n"
MF "v[236:239],a[220:223],a[188:191],v[236:239]\n"
TR "a[214:215],v0 offset:4616\n"
MF "v[236:239],a[216:219],a[148:151],v[236:239]\n"
LD "v12,s[28:31],s82 offen offset:2048 lds\n"
MF "v[228:231],a[216:219],a[144:147],v[228:231]\n"
MF "v[228:231],a[220:223],a[184:187],v[228:231]\n"
MF "v[220:223],a[220:223],a[180:183],v[220:223]\n"
MF "v[220:223],a[216:219],a[140:143],v[220:223]\n"
"s_waitcnt lgkmcnt(4)\n"
MF "v[16:19],a[200:203],a[60:63],v[16:19]\n"
TR "a[216:217],v1\n"
MF "v[16:19],a[204:207],a[100:103],v[16:19]\n"
TR "a[218:219],v1 offset:512\n"
MF "v[24:27],a[204:207],a[104:107],v[24:27]\n"
LD "v13,s[28:31],s82 offen offset:3072 lds\n"
MF "v[24:27],a[200:203],a[64:67],v[24:27]\n"
TR "a[220:221],v1 offset:4096\n"
MF "v[32:35],a[200:203],a[68:71],v[32:35]\n"
TR "a[222:223],v1 offset:4608\n"
MF "v[32:35],a[204:207],a[108:111],v[32:35]\n"
MF "v[40:43],a[204:207],a[112:115],v[40:43]\n"
MF "v[40:43],a[200:203],a[72:75],v[40:43]\n"
"s_add_u32 s84,s14,40960\n"
MF "v[48:51],a[200:203],a[76:79],v[48:51]\n"
"s_add_u32 m0,s63,45056\n"
MF "v[48:51],a[204:207],a[116:119],v[48:51]\n"
"s_waitcnt lgkmcnt(4)\n"
LD "v4,s[24:27],s84 offen lds\n"
MF "v[52:55],a[212:215],a[116:119],v[52:55]\n"
TR "a[200:201],v1 offset:8\n"
MF "v[52:55],a[208:211],a[76:79],v[52:55]\n"
TR "a[202:203],v1 offset:520\n"
MF "v[44:47],a[208:211],a[72:75],v[44:47]\n"
MF "v[44:47],a[212:215],a[112:115],v[44:47]\n"
TR "a[204:205],v1 offset:4104\n"
MF "v[36:39],a[212:215],a[108:111],v[36:39]\n"
TR "a[206:207],v1 offset:4616\n"
MF "v[36:39],a[208:211],a[68:71],v[36:39]\n"
MF "v[28:31],a[208:211],a[64:67],v[28:31]\n"
MF "v[28:31],a[212:215],a[104:107],v[28:31]\n"
MF "v[20:23],a[212:215],a[100:103],v[20:23]\n"
MF "v[20:23],a[208:211],a[60:63],v[20:23]\n"
"s_waitcnt vmcnt(24) lgkmcnt(0)\n"
"s_barrier\n"
MF "v[56:59],a[216:219],a[60:63],v[56:59]\n"
"s_add_u32 m0,s62,0\n"
"s_add_u32 s82,s66,6291712\n"
TR "a[208:209],v0 offset:16384\n"
MF "v[56:59],a[220:223],a[100:103],v[56:59]\n"
"s_sub_u32 s15,s82,7864320\n"
"s_min_u32 s82,s82,s15\n"
TR "a[210:211],v0 offset:16896\n"
MF "v[64:67],a[220:223],a[104:107],v[64:67]\n"
LD "v6,s[28:31],s82 offen lds\n"
MF "v[64:67],a[216:219],a[64:67],v[64:67]\n"
TR "a[212:213],v0 offset:20480\n"
MF "v[72:75],a[216:219],a[68:71],v[72:75]\n"
TR "a[214:215],v0 offset:20992\n"
MF "v[72:75],a[220:223],a[108:111],v[72:75]\n"
MF "v[80:83],a[220:223],a[112:115],v[80:83]\n"
LD "v11,s[28:31],s82 offen offset:1024 lds\n"
MF "v[80:83],a[216:219],a[72:75],v[80:83]\n"
MF "v[88:91],a[216:219],a[76:79],v[88:91]\n"
MF "v[88:91],a[220:223],a[116:119],v[88:91]\n"
MF "v[92:95],a[204:207],a[116:119],v[92:95]\n"
TR "a[216:217],v0 offset:16392\n"
MF "v[92:95],a[200:203],a[76:79],v[92:95]\n"
TR "a[218:219],v0 offset:16904\n"
MF "v[84:87],a[200:203],a[72:75],v[84:87]\n"
LD "v12,s[28:31],s82 offen offset:2048 lds\n"
MF "v[84:87],a[204:207],a[112:115],v[84:87]\n"
TR "a[220:221],v0 offset:20488\n"
MF "v[76:79],a[204:207],a[108:111],v[76:79]\n"
TR "a[222:223],v0 offset:21000\n"
MF "v[76:79],a[200:203],a[68:71],v[76:79]\n"
MF "v[68:71],a[200:203],a[64:67],v[68:71]\n"
LD "v13,s[28:31],s82 offen offset:3072 lds\n"
MF "v[68:71],a[204:207],a[104:107],v[68:71]\n"
MF "v[60:63],a[204:207],a[100:103],v[60:63]\n"
MF "v[60:63],a[200:203],a[60:63],v[60:63]\n"
"s_add_u32 s14,s81,1152\n"
"s_sub_u32 s15,s14,1280\n"
"s_waitcnt lgkmcnt(4)\n"
MF "v[96:99],a[208:211],a[60:63],v[96:99]\n"
"s_min_u32 s14,s14,s15\n"
"s_add_u32 s14,s14,s68\n"
TR "a[200:201],v1 offset:16384\n"
MF "v[96:99],a[212:215],a[100:103],v[96:99]\n"
"s_add_u32 s84,s14,0\n"
"s_add_u32 m0,s63,0\n"
TR "a[202:203],v1 offset:16896\n"
MF "v[104:107],a[212:215],a[104:107],v[104:107]\n"
LD "v4,s[24:27],s84 offen lds\n"
MF "v[104:107],a[208:211],a[64:67],v[104:107]\n"
TR "a[204:205],v1 offset:20480\n"
MF "v[112:115],a[208:211],a[68:71],v[112:115]\n"
TR "a[206:207],v1 offset:20992\n"
MF "v[112:115],a[212:215],a[108:111],v[112:115]\n"
"s_add_u32 s84,s14,9216\n"
MF "v[120:123],a[212:215],a[112:115],v[120:123]\n"
LD "v5,s[24:27],s84 offen offset:1024 lds\n"
MF "v[120:123],a[208:211],a[72:75],v[120:123]\n"
MF "v[128:131],a[208:211],a[76:79],v[128:131]\n"
MF "v[128:131],a[212:215],a[116:119],v[128:131]\n"
"s_waitcnt lgkmcnt(4)\n"
MF "v[132:135],a[220:223],a[116:119],v[132:135]\n"
TR "a[208:209],v1 offset:16392\n"
MF "v[132:135],a[216:219],a[76:79],v[132:135]\n"
"s_add_u32 s84,s14,18432\n"
TR "a[210:211],v1 offset:16904\n"
MF "v[124:127],a[216:219],a[72:75],v[124:127]\n"
LD "v4,s[24:27],s84 offen offset:2048 lds\n"
MF "v[124:127],a[220:223],a[112:115],v[124:127]\n"
TR "a[212:213],v1 offset:20488\n"
MF "v[116:119],a[220:223],a[108:111],v[116:119]\n"
TR "a[214:215],v1 offset:21000\n"
MF "v[116:119],a[216:219],a[68:71],v[116:119]\n"
MF "v[108:111],a[216:219],a[64:67],v[108:111]\n"
MF "v[108:111],a[220:223],a[104:107],v[108:111]\n"
MF "v[100:103],a[220:223],a[100:103],v[100:103]\n"
MF "v[100:103],a[216:219],a[60:63],v[100:103]\n"
"s_waitcnt vmcnt(24) lgkmcnt(0)\n"
"s_barrier\n"
MF "v[136:139],a[200:203],a[60:63],v[136:139]\n"
TR "a[216:217],v0 offset:32768\n"
MF "v[136:139],a[204:207],a[100:103],v[136:139]\n"
"s_add_u32 m0,s62,16384\n"
TR "a[218:219],v0 offset:33280\n"
MF "v[144:147],a[204:207],a[104:107],v[144:147]\n"
"s_add_u32 s82,s66,7078144\n"
"ds_read_b128 a[140:143],v2 offset:20480\n"
MF "v[144:147],a[200:203],a[64:67],v[144:147]\n"
"s_sub_u32 s15,s82,7864320\n"
TR "a[220:221],v0 offset:36864\n"
MF "v[152:155],a[200:203],a[68:71],v[152:155]\n"
"s_min_u32 s82,s82,s15\n"
TR "a[222:223],v0 offset:37376\n"
MF "v[152:155],a[204:207],a[108:111],v[152:155]\n"
LD "v6,s[28:31],s82 offen lds\n"
MF "v[160:163],a[204:207],a[112:115],v[160:163]\n"
"ds_read_b128 a[180:183],v3 offset:20480\n"
MF "v[160:163],a[200:203],a[72:75],v[160:163]\n"
LD "v11,s[28:31],s82 offen offset:1024 lds\n"
MF "v[168:171],a[200:203],a[76:79],v[168:171]\n"
MF "v[168:171],a[204:207],a[116:119],v[168:171]\n"
LD "v12,s[28:31],s82 offen offset:2048 lds\n"
MF "v[172:175],a[212:215],a[116:119],v[172:175]\n"
TR "a[200:201],v0 offset:32776\n"
MF "v[172:175],a[208:211],a[76:79],v[172:175]\n"
TR "a[202:203],v0 offset:33288\n"
MF "v[164:167],a[208:211],a[72:75],v[164:167]\n"
"ds_read_b128 a[144:147],v2 offset:22528\n"
MF "v[164:167],a[212:215],a[112:115],v[164:167]\n"
TR "a[204:205],v0 offset:36872\n"
MF "v[156:159],a[212:215],a[108:111],v[156:159]\n"
TR "a[206:207],v0 offset:37384\n"
MF "v[156:159],a[208:211],a[68:71],v[156:159]\n"
MF "v[148:151],a[208:211],a[64:67],v[148:151]\n"
"ds_read_b128 a[184:187],v3 offset:22528\n"
MF "v[148:151],a[212:215],a[104:107],v[148:151]\n"
MF "v[140:143],a[212:215],a[100:103],v[140:143]\n"
LD "v13,s[28:31],s82 offen offset:3072 lds\n"
MF "v[140:143],a[208:211],a[60:63],v[140:143]\n"
"s_waitcnt lgkmcnt(7)\n"
MF "v[176:179],a[216:219],a[60:63],v[176:179]\n"
TR "a[208:209],v1 offset:32768\n"
MF "v[176:179],a[220:223],a[100:103],v[176:179]\n"
TR "a[210:211],v1 offset:33280\n"
MF "v[184:187],a[220:223],a[104:107],v[184:187]\n"
"ds_read_b128 a[148:151],v2 offset:24576\n"
MF "v[184:187],a[216:219],a[64:67],v[184:187]\n"
"s_add_u32 s84,s14,30720\n"
TR "a[212:213],v1 offset:36864\n"
MF "v[192:195],a[216:219],a[68:71],v[192:195]\n"
"s_add_u32 m0,s63,3072\n"
TR "a[214:215],v1 offset:37376\n"
MF "v[192:195],a[220:223],a[108:111],v[192:195]\n"
LD "v5,s[24:27],s84 offen lds\n"
MF "v[200:203],a[220:223],a[112:115],v[200:203]\n"
"ds_read_b128 a[188:191],v3 offset:24576\n"
MF "v[200:203],a[216:219],a[72:75],v[200:203]\n"
MF "v[208:211],a[216:219],a[76:79],v[208:211]\n"
"s_add_u32 s84,s14,39936\n"
MF "v[208:211],a[220:223],a[116:119],v[208:211]\n"
"s_waitcnt lgkmcnt(7)\n"
LD "v4,s[24:27],s84 offen offset:1024 lds\n"
MF "v[212:215],a[204:207],a[116:119],v[212:215]\n"
TR "a[216:217],v1 offset:32776\n"
MF "v[212:215],a[200:203],a[76:79],v[212:215]\n"
TR "a[218:219],v1 offset:33288\n"
MF "v[204:207],a[200:203],a[72:75],v[204:207]\n"
"ds_read_b128 a[152:155],v2 offset:26624\n"
MF "v[204:207],a[204:207],a[112:115],v[204:207]\n"
TR "a[220:221],v1 offset:36872\n"
MF "v[196:199],a[204:207],a[108:111],v[196:199]\n"
TR "a[222:223],v1 offset:37384\n"
MF "v[196:199],a[200:203],a[68:71],v[196:199]\n"
MF "v[188:191],a[200:203],a[64:67],v[188:191]\n"
"v_add_u32 v0,0xc000,v0\n"
MF "v[188:191],a[204:207],a[104:107],v[188:191]\n"
"v_add_u32 v1,0xc000,v1\n"
MF "v[180:183],a[204:207],a[100:103],v[180:183]\n"
".Lqj:\n"
"s_cbranch_execz .Lipr1\n"
MF "v[180:183],a[200:203],a[60:63],v[180:183]\n"
"s_waitcnt vmcnt(26) lgkmcnt(0)\n"
"s_barrier\n"
"s_getpc_b64 s[52:53]\n"
".Lctq:\n"
"s_sub_u32 s52,s52,.Lctq-.Lcode0\n"
"s_subb_u32 s53,s53,0\n"
"s_bfe_u32 s58,s23,0x20000\n"
"s_lshl_b32 s58,s58,2\n"
"s_add_u32 s58,s58,s56\n"
"s_lshl_b32 s58,s58,13\n"
"v_lshlrev_b32 v15,3,v9\n"
"v_add_u32 v15,s58,v15\n"
"v_min_u32 v15,.Lcode1-.Lcode0,v15\n"
"global_load_dword a249,v15,s[52:53]\n"
MF "v[216:219],a[208:211],a[60:63],v[216:219]\n"
TR "a[200:201],v0\n"
MF "v[216:219],a[212:215],a[100:103],v[216:219]\n"
"s_add_u32 m0,s62,32768\n"
TR "a[202:203],v0 offset:512\n"
MF "v[224:227],a[212:215],a[104:107],v[224:227]\n"
"s_add_u32 s82,s66,6291584\n"
"ds_read_b128 a[192:195],v3 offset:26624\n"
MF "v[224:227],a[208:211],a[64:67],v[224:227]\n"
"s_sub_u32 s15,s82,7864320\n"
TR "a[204:205],v0 offset:4096\n"
MF "v[232:235],a[208:211],a[68:71],v[232:235]\n"
"s_min_u32 s82,s82,s15\n"
TR "a[206:207],v0 offset:4608\n"
MF "v[232:235],a[212:215],a[108:111],v[232:235]\n"
LD "v6,s[28:31],s82 offen lds\n"
MF "v[240:243],a[212:215],a[112:115],v[240:243]\n"
"ds_read_b128 a[156:159],v2 offset:28672\n"
MF "v[240:243],a[208:211],a[72:75],v[240:243]\n"
"ds_read_b128 a[196:199],v3 offset:28672\n"
MF "v[248:251],a[208:211],a[76:79],v[248:251]\n"
MF "v[248:251],a[212:215],a[116:119],v[248:251]\n"
LD "v11,s[28:31],s82 offen offset:1024 lds\n"
MF "v[252:255],a[220:223],a[116:119],v[252:255]\n"
TR "a[208:209],v0 offset:8\n"
MF "v[252:255],a[216:219],a[76:79],v[252:255]\n"
TR "a[210:211],v0 offset:520\n"
MF "v[244:247],a[216:219],a[72:75],v[244:247]\n"
MF "v[244:247],a[220:223],a[112:115],v[244:247]\n"
TR "a[212:213],v0 offset:4104\n"
MF "v[236:239],a[220:223],a[108:111],v[236:239]\n"
TR "a[214:215],v0 offset:4616\n"
MF "v[236:239],a[216:219],a[68:71],v[236:239]\n"
MF "v[228:231],a[216:219],a[64:67],v[228:231]\n"
MF "v[228:231],a[220:223],a[104:107],v[228:231]\n"
MF "v[220:223],a[220:223],a[100:103],v[220:223]\n"
LD "v12,s[28:31],s82 offen offset:2048 lds\n"
MF "v[220:223],a[216:219],a[60:63],v[220:223]\n"
"s_waitcnt lgkmcnt(4)\n"
MF "v[16:19],a[200:203],a[140:143],v[16:19]\n"
TR "a[216:217],v1\n"
MF "v[16:19],a[204:207],a[180:183],v[16:19]\n"
TR "a[218:219],v1 offset:512\n"
MF "v[24:27],a[204:207],a[184:187],v[24:27]\n"
MF "v[24:27],a[200:203],a[144:147],v[24:27]\n"
TR "a[220:221],v1 offset:4096\n"
MF "v[32:35],a[200:203],a[148:151],v[32:35]\n"
TR "a[222:223],v1 offset:4608\n"
MF "v[32:35],a[204:207],a[188:191],v[32:35]\n"
MF "v[40:43],a[204:207],a[192:195],v[40:43]\n"
MF "v[40:43],a[200:203],a[152:155],v[40:43]\n"
LD "v13,s[28:31],s82 offen offset:3072 lds\n"
MF "v[48:51],a[200:203],a[156:159],v[48:51]\n"
MF "v[48:51],a[204:207],a[196:199],v[48:51]\n"
"s_waitcnt lgkmcnt(4)\n"
MF "v[52:55],a[212:215],a[196:199],v[52:55]\n"
TR "a[200:201],v1 offset:8\n"
MF "v[52:55],a[208:211],a[156:159],v[52:55]\n"
TR "a[202:203],v1 offset:520\n"
MF "v[44:47],a[208:211],a[152:155],v[44:47]\n"
MF "v[44:47],a[212:215],a[192:195],v[44:47]\n"
TR "a[204:205],v1 offset:4104\n"
MF "v[36:39],a[212:215],a[188:191],v[36:39]\n"
TR "a[206:207],v1 offset:4616\n"
MF "v[36:39],a[208:211],a[148:151],v[36:39]\n"
MF "v[28:31],a[208:211],a[144:147],v[28:31]\n"
MF "v[28:31],a[212:215],a[184:187],v[28:31]\n"
MF "v[20:23],a[212:215],a[180:183],v[20:23]\n"
MF "v[20:23],a[208:211],a[140:143],v[20:23]\n"
"s_waitcnt vmcnt(25) lgkmcnt(0)\n"
"s_barrier\n"
MF "v[56:59],a[216:219],a[140:143],v[56:59]\n"
"s_add_u32 m0,s62,49152\n"
"s_add_u32 s82,s66,7078016\n"
TR "a[208:209],v0 offset:16384\n"
MF "v[56:59],a[220:223],a[180:183],v[56:59]\n"
"s_sub_u32 s15,s82,7864320\n"
"s_min_u32 s82,s82,s15\n"
TR "a[210:211],v0 offset:16896\n"
MF "v[64:67],a[220:223],a[184:187],v[64:67]\n"
LD "v6,s[28:31],s82 offen lds\n"
MF "v[64:67],a[216:219],a[144:147],v[64:67]\n"
TR "a[212:213],v0 offset:20480\n"
"v_lshrrev_b32 v15,8,v9\n"
MF "v[72:75],a[216:219],a[148:151],v[72:75]\n"
TR "a[214:215],v0 offset:20992\n"
"v_mul_u32_u24 v15,48960,v15\n"
MF "v[72:75],a[220:223],a[188:191],v[72:75]\n"
"s_lshr_b32 s7,s56,1\n"
"v_add_u32 v15,v15,v9\n"
MF "v[80:83],a[220:223],a[192:195],v[80:83]\n"
"s_and_b32 s8,s56,1\n"
MF "v[80:83],a[216:219],a[152:155],v[80:83]\n"
"s_sub_u32 s7,s7,s8\n"
MF "v[88:91],a[216:219],a[156:159],v[88:91]\n"
"s_mul_i32 s7,s7,245760\n"
MF "v[88:91],a[220:223],a[196:199],v[88:91]\n"
LD "v11,s[28:31],s82 offen offset:1024 lds\n"
MF "v[92:95],a[204:207],a[196:199],v[92:95]\n"
"s_add_u32 s7,s7,s89\n"
TR "a[216:217],v0 offset:16392\n"
MF "v[92:95],a[200:203],a[156:159],v[92:95]\n"
TR "a[218:219],v0 offset:16904\n"
MF "v[84:87],a[200:203],a[152:155],v[84:87]\n"
MF "v[84:87],a[204:207],a[192:195],v[84:87]\n"
TR "a[220:221],v0 offset:20488\n"
MF "v[76:79],a[204:207],a[188:191],v[76:79]\n"
TR "a[222:223],v0 offset:21000\n"
MF "v[76:79],a[200:203],a[148:151],v[76:79]\n"
MF "v[68:71],a[200:203],a[144:147],v[68:71]\n"
MF "v[68:71],a[204:207],a[184:187],v[68:71]\n"
MF "v[60:63],a[204:207],a[180:183],v[60:63]\n"
LD "v12,s[28:31],s82 offen offset:2048 lds\n"
MF "v[60:63],a[200:203],a[140:143],v[60:63]\n"
"s_waitcnt lgkmcnt(4)\n"
MF "v[96:99],a[208:211],a[140:143],v[96:99]\n"
TR "a[200:201],v1 offset:16384\n"
MF "v[96:99],a[212:215],a[180:183],v[96:99]\n"
TR "a[202:203],v1 offset:16896\n"
MF "v[104:107],a[212:215],a[184:187],v[104:107]\n"
MF "v[104:107],a[208:211],a[144:147],v[104:107]\n"
TR "a[204:205],v1 offset:20480\n"
MF "v[112:115],a[208:211],a[148:151],v[112:115]\n"
TR "a[206:207],v1 offset:20992\n"
MF "v[112:115],a[212:215],a[188:191],v[112:115]\n"
MF "v[120:123],a[212:215],a[192:195],v[120:123]\n"
MF "v[120:123],a[208:211],a[152:155],v[120:123]\n"
LD "v13,s[28:31],s82 offen offset:3072 lds\n"
MF "v[128:131],a[208:211],a[156:159],v[128:131]\n"
MF "v[128:131],a[212:215],a[196:199],v[128:131]\n"
"s_waitcnt lgkmcnt(4)\n"
MF "v[132:135],a[220:223],a[196:199],v[132:135]\n"
TR "a[208:209],v1 offset:16392\n"
MF "v[132:135],a[216:219],a[156:159],v[132:135]\n"
TR "a[210:211],v1 offset:16904\n"
MF "v[124:127],a[216:219],a[152:155],v[124:127]\n"
MF "v[124:127],a[220:223],a[192:195],v[124:127]\n"
TR "a[212:213],v1 offset:20488\n"
MF "v[116:119],a[220:223],a[188:191],v[116:119]\n"
TR "a[214:215],v1 offset:21000\n"
MF "v[116:119],a[216:219],a[148:151],v[116:119]\n"
MF "v[108:111],a[216:219],a[144:147],v[108:111]\n"
MF "v[108:111],a[220:223],a[184:187],v[108:111]\n"
MF "v[100:103],a[220:223],a[180:183],v[100:103]\n"
MF "v[100:103],a[216:219],a[140:143],v[100:103]\n"
"s_waitcnt vmcnt(22) lgkmcnt(0)\n"
"s_barrier\n"
"s_cmp_eq_u32 s57,1\n"
"s_cbranch_scc0 .Lhs15\n"
".Lack_q:\n"
"v_cmp_ne_u32 vcc,s76,v10\n"
"s_cbranch_vccnz .Lackspin_q\n"
".Lackok_q:\n"
".Lhs15:\n"
MF "v[136:139],a[200:203],a[140:143],v[136:139]\n"
"s_and_b32 s58,s56,1\n"
TR "a[216:217],v0 offset:32768\n"
MF "v[136:139],a[204:207],a[180:183],v[136:139]\n"
"s_add_u32 m0,s62,65536\n"
TR "a[218:219],v0 offset:33280\n"
MF "v[144:147],a[204:207],a[184:187],v[144:147]\n"
"s_add_u32 s82,s66,6291456\n"
"ds_read_b128 a[60:63],v2 offset:40960\n"
MF "v[144:147],a[200:203],a[144:147],v[144:147]\n"
"s_sub_u32 s15,s82,7864320\n"
TR "a[220:221],v0 offset:36864\n"
MF "v[152:155],a[200:203],a[148:151],v[152:155]\n"
"s_min_u32 s82,s82,s15\n"
TR "a[222:223],v0 offset:37376\n"
MF "v[152:155],a[204:207],a[188:191],v[152:155]\n"
LD "v6,s[28:31],s82 offen lds\n"
MF "v[160:163],a[204:207],a[192:195],v[160:163]\n"
"s_mul_i32 s59,s58,3072\n"
"ds_read_b128 a[100:103],v3 offset:40960\n"
MF "v[160:163],a[200:203],a[152:155],v[160:163]\n"
"s_add_u32 s16,s70,s59\n"
MF "v[168:171],a[200:203],a[156:159],v[168:171]\n"
"s_mul_i32 s59,s58,6144\n"
MF "v[168:171],a[204:207],a[196:199],v[168:171]\n"
LD "v11,s[28:31],s82 offen offset:1024 lds\n"
MF "v[172:175],a[212:215],a[196:199],v[172:175]\n"
"s_add_u32 s18,s70,s59\n"
TR "a[200:201],v0 offset:32776\n"
MF "v[172:175],a[208:211],a[156:159],v[172:175]\n"
"s_add_u32 s17,s16,196608\n"
TR "a[202:203],v0 offset:33288\n"
MF "v[164:167],a[208:211],a[152:155],v[164:167]\n"
"s_add_u32 s19,s18,196608\n"
"ds_read_b128 a[64:67],v2 offset:43008\n"
MF "v[164:167],a[212:215],a[192:195],v[164:167]\n"
"s_add_u32 s20,s70,196608\n"
TR "a[204:205],v0 offset:36872\n"
MF "v[156:159],a[212:215],a[188:191],v[156:159]\n"
"s_bfe_u32 s58,s23,0x40008\n"
TR "a[206:207],v0 offset:37384\n"
MF "v[156:159],a[208:211],a[148:151],v[156:159]\n"
"s_lshr_b32 s59,s56,1\n"
MF "v[148:151],a[208:211],a[144:147],v[148:151]\n"
"s_mul_i32 s59,s59,5\n"
"ds_read_b128 a[104:107],v3 offset:43008\n"
MF "v[148:151],a[212:215],a[184:187],v[148:151]\n"
"s_add_u32 s58,s58,s59\n"
MF "v[140:143],a[212:215],a[180:183],v[140:143]\n"
LD "v12,s[28:31],s82 offen offset:2048 lds\n"
MF "v[140:143],a[208:211],a[140:143],v[140:143]\n"
"s_waitcnt lgkmcnt(7)\n"
MF "v[176:179],a[216:219],a[140:143],v[176:179]\n"
"s_and_b32 s58,s58,15\n"
TR "a[208:209],v1 offset:32768\n"
MF "v[176:179],a[220:223],a[180:183],v[176:179]\n"
"s_mul_i32 s58,s58,12288\n"
TR "a[210:211],v1 offset:33280\n"
MF "v[184:187],a[220:223],a[184:187],v[184:187]\n"
"s_add_u32 s72,s16,s58\n"
"ds_read_b128 a[68:71],v2 offset:45056\n"
MF "v[184:187],a[216:219],a[144:147],v[184:187]\n"
"s_add_u32 s73,s18,s58\n"
TR "a[212:213],v1 offset:36864\n"
MF "v[192:195],a[216:219],a[148:151],v[192:195]\n"
"s_add_u32 s74,s70,s58\n"
TR "a[214:215],v1 offset:37376\n"
MF "v[192:195],a[220:223],a[188:191],v[192:195]\n"
"s_add_u32 s74,s74,12288\n"
MF "v[200:203],a[220:223],a[192:195],v[200:203]\n"
"s_cmp_eq_u32 s74,s20\n"
"s_cselect_b32 s74,s70,s74\n"
"ds_read_b128 a[108:111],v3 offset:45056\n"
MF "v[200:203],a[216:219],a[152:155],v[200:203]\n"
LD "v13,s[28:31],s82 offen offset:3072 lds\n"
MF "v[208:211],a[216:219],a[156:159],v[208:211]\n"
"s_lshr_b32 s58,s56,1\n"
MF "v[208:211],a[220:223],a[196:199],v[208:211]\n"
"s_waitcnt lgkmcnt(7)\n"
MF "v[212:215],a[204:207],a[196:199],v[212:215]\n"
"s_mul_i32 s58,s58,6144\n"
TR "a[216:217],v1 offset:32776\n"
MF "v[212:215],a[200:203],a[156:159],v[212:215]\n"
"s_mul_i32 s64,s56,3072\n"
TR "a[218:219],v1 offset:33288\n"
MF "v[204:207],a[200:203],a[152:155],v[204:207]\n"
"s_add_u32 s64,s64,s58\n"
"ds_read_b128 a[72:75],v2 offset:47104\n"
MF "v[204:207],a[204:207],a[192:195],v[204:207]\n"
"s_mul_i32 s65,s56,6144\n"
TR "a[220:221],v1 offset:36872\n"
MF "v[196:199],a[204:207],a[188:191],v[196:199]\n"
"s_sub_u32 s58,s71,s70\n"
TR "a[222:223],v1 offset:37384\n"
MF "v[196:199],a[200:203],a[148:151],v[196:199]\n"
"s_add_u32 s96,s74,s58\n"
MF "v[188:191],a[200:203],a[144:147],v[188:191]\n"
"s_sub_u32 s58,s16,s70\n"
"v_add_u32 v0,0xffff4000,v0\n"
MF "v[188:191],a[204:207],a[184:187],v[188:191]\n"
"s_add_u32 s96,s96,s58\n"
"v_add_u32 v1,0xffff4000,v1\n"
MF "v[180:183],a[204:207],a[180:183],v[180:183]\n"
"s_max_u32 s60,s70,s71\n"
MF "v[180:183],a[200:203],a[140:143],v[180:183]\n"
"s_waitcnt vmcnt(13) lgkmcnt(0)\n"
"s_barrier\n"
MF "v[216:219],a[208:211],a[140:143],v[216:219]\n"
"s_add_u32 s34,s60,196608\n"
TR "a[200:201],v1 offset:8\n"
MF "v[216:219],a[212:215],a[180:183],v[216:219]\n"
"s_add_u32 m0,s62,81920\n"
TR "a[202:203],v1 offset:520\n"
"v_mov_b32 v14,s12\n"
MF "v[224:227],a[212:215],a[184:187],v[224:227]\n"
"s_add_u32 s82,s66,7077888\n"
"ds_read_b128 a[112:115],v3 offset:47104\n"
"v_mul_f32 v14,0xbdd53b95,v14\n"
MF "v[224:227],a[208:211],a[144:147],v[224:227]\n"
"s_sub_u32 s15,s82,7864320\n"
TR "a[204:205],v1 offset:4104\n"
MF "v[232:235],a[208:211],a[148:151],v[232:235]\n"
"s_min_u32 s82,s82,s15\n"
TR "a[206:207],v1 offset:4616\n"
MF "v[232:235],a[212:215],a[188:191],v[232:235]\n"
LD "v6,s[28:31],s82 offen lds\n"
MF "v[240:243],a[212:215],a[192:195],v[240:243]\n"
"s_add_u32 s38,s60,196608\n"
"ds_read_b128 a[76:79],v2 offset:49152\n"
MF "v[240:243],a[208:211],a[152:155],v[240:243]\n"
"ds_read_b128 a[116:119],v3 offset:49152\n"
MF "v[248:251],a[208:211],a[156:159],v[248:251]\n"
MF "v[248:251],a[212:215],a[196:199],v[248:251]\n"
LD "v11,s[28:31],s82 offen offset:1024 lds\n"
MF "v[220:223],a[216:219],a[140:143],v[220:223]\n"
TR "a[208:209],v1 offset:16392\n"
MF "v[220:223],a[220:223],a[180:183],v[220:223]\n"
TR "a[210:211],v1 offset:16904\n"
MF "v[228:231],a[220:223],a[184:187],v[228:231]\n"
"ds_read_b128 a[140:143],v2\n"
MF "v[228:231],a[216:219],a[144:147],v[228:231]\n"
TR "a[212:213],v1 offset:20488\n"
MF "v[236:239],a[216:219],a[148:151],v[236:239]\n"
TR "a[214:215],v1 offset:21000\n"
MF "v[236:239],a[220:223],a[188:191],v[236:239]\n"
"ds_read_b128 a[180:183],v3\n"
MF "v[244:247],a[220:223],a[192:195],v[244:247]\n"
"ds_read_b128 a[144:147],v2 offset:2048\n"
MF "v[244:247],a[216:219],a[152:155],v[244:247]\n"
"ds_read_b128 a[184:187],v3 offset:2048\n"
MF "v[252:255],a[216:219],a[156:159],v[252:255]\n"
"ds_read_b128 a[148:151],v2 offset:4096\n"
MF "v[252:255],a[220:223],a[196:199],v[252:255]\n"
"s_waitcnt lgkmcnt(9)\n"
"ds_read_b128 a[188:191],v3 offset:4096\n"
MF "v[220:223],a[200:203],a[60:63],v[220:223]\n"
TR "a[216:217],v1\n"
MF "v[220:223],a[204:207],a[100:103],v[220:223]\n"
TR "a[218:219],v1 offset:512\n"
MF "v[228:231],a[204:207],a[104:107],v[228:231]\n"
"ds_read_b128 a[152:155],v2 offset:6144\n"
MF "v[228:231],a[200:203],a[64:67],v[228:231]\n"
TR "a[220:221],v1 offset:4096\n"
MF "v[236:239],a[200:203],a[68:71],v[236:239]\n"
TR "a[222:223],v1 offset:4608\n"
MF "v[236:239],a[204:207],a[108:111],v[236:239]\n"
"ds_read_b128 a[192:195],v3 offset:6144\n"
MF "v[244:247],a[204:207],a[112:115],v[244:247]\n"
"ds_read_b128 a[156:159],v2 offset:8192\n"
MF "v[244:247],a[200:203],a[72:75],v[244:247]\n"
"ds_read_b128 a[196:199],v3 offset:8192\n"
MF "v[252:255],a[200:203],a[76:79],v[252:255]\n"
LD "v12,s[28:31],s82 offen offset:2048 lds\n"
MF "v[252:255],a[204:207],a[116:119],v[252:255]\n"
"s_waitcnt lgkmcnt(8)\n"
LD "v13,s[28:31],s82 offen offset:3072 lds\n"
MF "v[220:223],a[208:211],a[140:143],v[220:223]\n"
TR "a[200:201],v1 offset:16384\n"
MF "v[220:223],a[212:215],a[180:183],v[220:223]\n"
TR "a[202:203],v1 offset:16896\n"
MF "v[228:231],a[212:215],a[184:187],v[228:231]\n"
MF "v[228:231],a[208:211],a[144:147],v[228:231]\n"
TR "a[204:205],v1 offset:20480\n"
MF "v[236:239],a[208:211],a[148:151],v[236:239]\n"
TR "a[206:207],v1 offset:20992\n"
MF "v[236:239],a[212:215],a[188:191],v[236:239]\n"
"s_waitcnt lgkmcnt(4)\n"
MF "v[244:247],a[212:215],a[192:195],v[244:247]\n"
MF "v[244:247],a[208:211],a[152:155],v[244:247]\n"
MF "v[252:255],a[208:211],a[156:159],v[252:255]\n"
MF "v[252:255],a[212:215],a[196:199],v[252:255]\n"
"s_waitcnt lgkmcnt(0)\n"
"s_cmp_eq_u32 s69,98\n"
"s_cbranch_scc1 .Lqy\n"
MF "v[216:219],a[216:219],a[60:63],v[216:219]\n"
TR "a[208:209],v0 offset:8\n"
MF "v[216:219],a[220:223],a[100:103],v[216:219]\n"
TR "a[210:211],v0 offset:520\n"
MF "v[224:227],a[220:223],a[104:107],v[224:227]\n"
"buffer_load_dword a224,v15,s[36:39],s70 offen offset:2688 nt\n"
MF "v[224:227],a[216:219],a[64:67],v[224:227]\n"
TR "a[212:213],v0 offset:4104\n"
MF "v[232:235],a[216:219],a[68:71],v[232:235]\n"
TR "a[214:215],v0 offset:4616\n"
MF "v[232:235],a[220:223],a[108:111],v[232:235]\n"
"buffer_load_dword a225,v15,s[36:39],s71 offen offset:2688 nt\n"
MF "v[240:243],a[220:223],a[112:115],v[240:243]\n"
MF "v[240:243],a[216:219],a[72:75],v[240:243]\n"
"buffer_load_dword a226,v15,s[48:51],s7 offen offset:2688\n"
MF "v[248:251],a[216:219],a[76:79],v[248:251]\n"
MF "v[248:251],a[220:223],a[116:119],v[248:251]\n"
MF "v[248:251],a[200:203],a[156:159],v[248:251]\n"
TR "a[216:217],v0 offset:16392\n"
MF "v[248:251],a[204:207],a[196:199],v[248:251]\n"
TR "a[218:219],v0 offset:16904\n"
MF "v[240:243],a[204:207],a[192:195],v[240:243]\n"
MF "v[240:243],a[200:203],a[152:155],v[240:243]\n"
TR "a[220:221],v0 offset:20488\n"
MF "v[232:235],a[200:203],a[148:151],v[232:235]\n"
TR "a[222:223],v0 offset:21000\n"
MF "v[232:235],a[204:207],a[188:191],v[232:235]\n"
MF "v[224:227],a[204:207],a[184:187],v[224:227]\n"
MF "v[224:227],a[200:203],a[144:147],v[224:227]\n"
"v_fma_f32 v248,v248,s54,v14\n"
"v_fma_f32 v249,v249,s54,v14\n"
MF "v[216:219],a[200:203],a[140:143],v[216:219]\n"
"v_fma_f32 v250,v250,s54,v14\n"
"v_fma_f32 v251,v251,s54,v14\n"
MF "v[216:219],a[204:207],a[180:183],v[216:219]\n"
"s_waitcnt lgkmcnt(4)\n"
"v_fma_f32 v252,v252,s54,v14\n"
"v_fma_f32 v253,v253,s54,v14\n"
MF "v[180:183],a[208:211],a[60:63],v[180:183]\n"
TR "a[200:201],v0\n"
"v_fma_f32 v254,v254,s54,v14\n"
MF "v[180:183],a[212:215],a[100:103],v[180:183]\n"
TR "a[202:203],v0 offset:512\n"
"v_fma_f32 v255,v255,s54,v14\n"
MF "v[188:191],a[212:215],a[104:107],v[188:191]\n"
CV "v255,v254,v255\n"
CV "v254,v252,v253\n"
MF "v[188:191],a[208:211],a[64:67],v[188:191]\n"
TR "a[204:205],v0 offset:4096\n"
CV "v253,v250,v251\n"
MF "v[196:199],a[208:211],a[68:71],v[196:199]\n"
TR "a[206:207],v0 offset:4608\n"
CV "v252,v248,v249\n"
MF "v[196:199],a[212:215],a[108:111],v[196:199]\n"
"v_fma_f32 v240,v240,s54,v14\n"
"v_fma_f32 v241,v241,s54,v14\n"
MF "v[204:207],a[212:215],a[112:115],v[204:207]\n"
"v_fma_f32 v242,v242,s54,v14\n"
"v_fma_f32 v243,v243,s54,v14\n"
MF "v[204:207],a[208:211],a[72:75],v[204:207]\n"
"v_fma_f32 v244,v244,s54,v14\n"
"v_fma_f32 v245,v245,s54,v14\n"
MF "v[212:215],a[208:211],a[76:79],v[212:215]\n"
"v_fma_f32 v246,v246,s54,v14\n"
"v_fma_f32 v247,v247,s54,v14\n"
MF "v[212:215],a[212:215],a[116:119],v[212:215]\n"
"s_waitcnt lgkmcnt(4)\n"
CV "v251,v246,v247\n"
CV "v250,v244,v245\n"
MF "v[180:183],a[216:219],a[140:143],v[180:183]\n"
TR "a[208:209],v0 offset:16384\n"
CV "v249,v242,v243\n"
MF "v[180:183],a[220:223],a[180:183],v[180:183]\n"
TR "a[210:211],v0 offset:16896\n"
CV "v248,v240,v241\n"
MF "v[188:191],a[220:223],a[184:187],v[188:191]\n"
"v_fma_f32 v232,v232,s54,v14\n"
"v_fma_f32 v233,v233,s54,v14\n"
MF "v[188:191],a[216:219],a[144:147],v[188:191]\n"
TR "a[212:213],v0 offset:20480\n"
"v_fma_f32 v234,v234,s54,v14\n"
MF "v[196:199],a[216:219],a[148:151],v[196:199]\n"
TR "a[214:215],v0 offset:20992\n"
"v_fma_f32 v235,v235,s54,v14\n"
MF "v[196:199],a[220:223],a[188:191],v[196:199]\n"
"v_fma_f32 v236,v236,s54,v14\n"
"v_fma_f32 v237,v237,s54,v14\n"
MF "v[204:207],a[220:223],a[192:195],v[204:207]\n"
"v_fma_f32 v238,v238,s54,v14\n"
"v_fma_f32 v239,v239,s54,v14\n"
MF "v[204:207],a[216:219],a[152:155],v[204:207]\n"
CV "v247,v238,v239\n"
CV "v246,v236,v237\n"
MF "v[212:215],a[216:219],a[156:159],v[212:215]\n"
CV "v245,v234,v235\n"
CV "v244,v232,v233\n"
MF "v[212:215],a[220:223],a[196:199],v[212:215]\n"
"s_waitcnt vmcnt(11) lgkmcnt(0)\n"
"s_barrier\n"
MF "v[176:179],a[200:203],a[60:63],v[176:179]\n"
TR "a[216:217],v1 offset:32776\n"
"v_fma_f32 v224,v224,s54,v14\n"
MF "v[176:179],a[204:207],a[100:103],v[176:179]\n"
"s_add_u32 m0,s64,0\n"
TR "a[218:219],v1 offset:33288\n"
"v_fma_f32 v225,v225,s54,v14\n"
MF "v[184:187],a[204:207],a[104:107],v[184:187]\n"
LD "v7,s[32:35],s72 offen nt lds\n"
MF "v[184:187],a[200:203],a[64:67],v[184:187]\n"
TR "a[220:221],v1 offset:36872\n"
"v_fma_f32 v226,v226,s54,v14\n"
MF "v[192:195],a[200:203],a[68:71],v[192:195]\n"
TR "a[222:223],v1 offset:37384\n"
"v_fma_f32 v227,v227,s54,v14\n"
MF "v[192:195],a[204:207],a[108:111],v[192:195]\n"
"s_add_u32 m0,s64,896\n"
"v_fma_f32 v228,v228,s54,v14\n"
"v_fma_f32 v229,v229,s54,v14\n"
MF "v[200:203],a[204:207],a[112:115],v[200:203]\n"
LD "v7,s[32:35],s72 offen offset:128 nt lds\n"
MF "v[200:203],a[200:203],a[72:75],v[200:203]\n"
"v_fma_f32 v230,v230,s54,v14\n"
"v_fma_f32 v231,v231,s54,v14\n"
MF "v[208:211],a[200:203],a[76:79],v[208:211]\n"
CV "v243,v230,v231\n"
CV "v242,v228,v229\n"
MF "v[208:211],a[204:207],a[116:119],v[208:211]\n"
CV "v241,v226,v227\n"
CV "v240,v224,v225\n"
MF "v[208:211],a[208:211],a[156:159],v[208:211]\n"
TR "a[200:201],v1 offset:49160\n"
"v_fma_f32 v216,v216,s54,v14\n"
MF "v[208:211],a[212:215],a[196:199],v[208:211]\n"
"s_add_u32 m0,s64,1792\n"
TR "a[202:203],v1 offset:49672\n"
"v_fma_f32 v217,v217,s54,v14\n"
MF "v[200:203],a[212:215],a[192:195],v[200:203]\n"
LD "v7,s[32:35],s72 offen offset:256 nt lds\n"
MF "v[200:203],a[208:211],a[152:155],v[200:203]\n"
TR "a[204:205],v1 offset:53256\n"
"v_fma_f32 v218,v218,s54,v14\n"
MF "v[192:195],a[208:211],a[148:151],v[192:195]\n"
TR "a[206:207],v1 offset:53768\n"
"v_fma_f32 v219,v219,s54,v14\n"
MF "v[192:195],a[212:215],a[188:191],v[192:195]\n"
"v_fma_f32 v220,v220,s54,v14\n"
"v_fma_f32 v221,v221,s54,v14\n"
MF "v[184:187],a[212:215],a[184:187],v[184:187]\n"
"s_add_u32 s72,s72,6144\n"
"v_fma_f32 v222,v222,s54,v14\n"
"v_fma_f32 v223,v223,s54,v14\n"
MF "v[184:187],a[208:211],a[144:147],v[184:187]\n"
"s_add_u32 m0,s64,6144\n"
CV "v239,v222,v223\n"
CV "v238,v220,v221\n"
MF "v[176:179],a[208:211],a[140:143],v[176:179]\n"
LD "v7,s[32:35],s72 offen nt lds\n"
MF "v[176:179],a[212:215],a[180:183],v[176:179]\n"
"s_waitcnt lgkmcnt(4)\n"
CV "v237,v218,v219\n"
CV "v236,v216,v217\n"
MF "v[140:143],a[216:219],a[60:63],v[140:143]\n"
TR "a[208:209],v1 offset:32768\n"
"v_fma_f32 v208,v208,s54,v14\n"
MF "v[140:143],a[220:223],a[100:103],v[140:143]\n"
TR "a[210:211],v1 offset:33280\n"
"v_fma_f32 v209,v209,s54,v14\n"
MF "v[148:151],a[220:223],a[104:107],v[148:151]\n"
"v_fma_f32 v210,v210,s54,v14\n"
"v_fma_f32 v211,v211,s54,v14\n"
MF "v[148:151],a[216:219],a[64:67],v[148:151]\n"
TR "a[212:213],v1 offset:36864\n"
"v_fma_f32 v212,v212,s54,v14\n"
MF "v[156:159],a[216:219],a[68:71],v[156:159]\n"
"s_add_u32 m0,s64,7040\n"
TR "a[214:215],v1 offset:37376\n"
"v_fma_f32 v213,v213,s54,v14\n"
MF "v[156:159],a[220:223],a[108:111],v[156:159]\n"
LD "v7,s[32:35],s72 offen offset:128 nt lds\n"
MF "v[164:167],a[220:223],a[112:115],v[164:167]\n"
"v_fma_f32 v214,v214,s54,v14\n"
"v_fma_f32 v215,v215,s54,v14\n"
MF "v[164:167],a[216:219],a[72:75],v[164:167]\n"
CV "v235,v214,v215\n"
CV "v234,v212,v213\n"
MF "v[172:175],a[216:219],a[76:79],v[172:175]\n"
"s_add_u32 m0,s64,7936\n"
CV "v233,v210,v211\n"
CV "v232,v208,v209\n"
MF "v[172:175],a[220:223],a[116:119],v[172:175]\n"
"s_waitcnt lgkmcnt(4)\n"
LD "v7,s[32:35],s72 offen offset:256 nt lds\n"
MF "v[140:143],a[200:203],a[140:143],v[140:143]\n"
TR "a[216:217],v1 offset:49152\n"
"v_fma_f32 v200,v200,s54,v14\n"
MF "v[140:143],a[204:207],a[180:183],v[140:143]\n"
TR "a[218:219],v1 offset:49664\n"
"v_fma_f32 v201,v201,s54,v14\n"
MF "v[148:151],a[204:207],a[184:187],v[148:151]\n"
"v_fma_f32 v202,v202,s54,v14\n"
"v_fma_f32 v203,v203,s54,v14\n"
MF "v[148:151],a[200:203],a[144:147],v[148:151]\n"
TR "a[220:221],v1 offset:53248\n"
"v_fma_f32 v204,v204,s54,v14\n"
MF "v[156:159],a[200:203],a[148:151],v[156:159]\n"
TR "a[222:223],v1 offset:53760\n"
"v_fma_f32 v205,v205,s54,v14\n"
MF "v[156:159],a[204:207],a[188:191],v[156:159]\n"
"v_fma_f32 v206,v206,s54,v14\n"
"v_fma_f32 v207,v207,s54,v14\n"
MF "v[164:167],a[204:207],a[192:195],v[164:167]\n"
CV "v231,v206,v207\n"
CV "v230,v204,v205\n"
MF "v[164:167],a[200:203],a[152:155],v[164:167]\n"
CV "v229,v202,v203\n"
CV "v228,v200,v201\n"
MF "v[172:175],a[200:203],a[156:159],v[172:175]\n"
"v_fma_f32 v192,v192,s54,v14\n"
"v_fma_f32 v193,v193,s54,v14\n"
MF "v[172:175],a[204:207],a[196:199],v[172:175]\n"
"s_waitcnt lgkmcnt(0)\n"
"v_fma_f32 v194,v194,s54,v14\n"
"v_fma_f32 v195,v195,s54,v14\n"
MF "v[136:139],a[208:211],a[60:63],v[136:139]\n"
TR "a[200:201],v0 offset:32776\n"
"v_fma_f32 v196,v196,s54,v14\n"
MF "v[136:139],a[212:215],a[100:103],v[136:139]\n"
TR "a[202:203],v0 offset:33288\n"
"v_fma_f32 v197,v197,s54,v14\n"
MF "v[144:147],a[212:215],a[104:107],v[144:147]\n"
"v_fma_f32 v198,v198,s54,v14\n"
"v_fma_f32 v199,v199,s54,v14\n"
MF "v[144:147],a[208:211],a[64:67],v[144:147]\n"
TR "a[204:205],v0 offset:36872\n"
CV "v227,v198,v199\n"
MF "v[152:155],a[208:211],a[68:71],v[152:155]\n"
TR "a[206:207],v0 offset:37384\n"
CV "v226,v196,v197\n"
MF "v[152:155],a[212:215],a[108:111],v[152:155]\n"
CV "v225,v194,v195\n"
CV "v224,v192,v193\n"
MF "v[160:163],a[212:215],a[112:115],v[160:163]\n"
"v_fma_f32 v184,v184,s54,v14\n"
"v_fma_f32 v185,v185,s54,v14\n"
MF "v[160:163],a[208:211],a[72:75],v[160:163]\n"
"v_fma_f32 v186,v186,s54,v14\n"
"v_fma_f32 v187,v187,s54,v14\n"
MF "v[168:171],a[208:211],a[76:79],v[168:171]\n"
"v_fma_f32 v188,v188,s54,v14\n"
"v_fma_f32 v189,v189,s54,v14\n"
MF "v[168:171],a[212:215],a[116:119],v[168:171]\n"
"v_fma_f32 v190,v190,s54,v14\n"
"v_fma_f32 v191,v191,s54,v14\n"
MF "v[168:171],a[216:219],a[156:159],v[168:171]\n"
TR "a[208:209],v0 offset:49160\n"
CV "v223,v190,v191\n"
MF "v[168:171],a[220:223],a[196:199],v[168:171]\n"
TR "a[210:211],v0 offset:49672\n"
CV "v222,v188,v189\n"
MF "v[160:163],a[220:223],a[192:195],v[160:163]\n"
CV "v221,v186,v187\n"
CV "v220,v184,v185\n"
MF "v[160:163],a[216:219],a[152:155],v[160:163]\n"
TR "a[212:213],v0 offset:53256\n"
"v_fma_f32 v176,v176,s54,v14\n"
MF "v[152:155],a[216:219],a[148:151],v[152:155]\n"
TR "a[214:215],v0 offset:53768\n"
"v_fma_f32 v177,v177,s54,v14\n"
MF "v[152:155],a[220:223],a[188:191],v[152:155]\n"
"v_fma_f32 v178,v178,s54,v14\n"
"v_fma_f32 v179,v179,s54,v14\n"
MF "v[144:147],a[220:223],a[184:187],v[144:147]\n"
"v_fma_f32 v180,v180,s54,v14\n"
"v_fma_f32 v181,v181,s54,v14\n"
MF "v[144:147],a[216:219],a[144:147],v[144:147]\n"
"v_fma_f32 v182,v182,s54,v14\n"
"v_fma_f32 v183,v183,s54,v14\n"
MF "v[136:139],a[216:219],a[140:143],v[136:139]\n"
CV "v219,v182,v183\n"
CV "v218,v180,v181\n"
MF "v[136:139],a[220:223],a[180:183],v[136:139]\n"
"s_waitcnt lgkmcnt(4)\n"
CV "v217,v178,v179\n"
CV "v216,v176,v177\n"
MF "v[100:103],a[200:203],a[60:63],v[100:103]\n"
TR "a[216:217],v0 offset:32768\n"
"v_fma_f32 v168,v168,s54,v14\n"
MF "v[100:103],a[204:207],a[100:103],v[100:103]\n"
TR "a[218:219],v0 offset:33280\n"
"v_fma_f32 v169,v169,s54,v14\n"
MF "v[108:111],a[204:207],a[104:107],v[108:111]\n"
"v_fma_f32 v170,v170,s54,v14\n"
"v_fma_f32 v171,v171,s54,v14\n"
MF "v[108:111],a[200:203],a[64:67],v[108:111]\n"
TR "a[220:221],v0 offset:36864\n"
"v_fma_f32 v172,v172,s54,v14\n"
MF "v[116:119],a[200:203],a[68:71],v[116:119]\n"
TR "a[222:223],v0 offset:37376\n"
"v_fma_f32 v173,v173,s54,v14\n"
MF "v[116:119],a[204:207],a[108:111],v[116:119]\n"
"v_fma_f32 v174,v174,s54,v14\n"
"v_fma_f32 v175,v175,s54,v14\n"
MF "v[124:127],a[204:207],a[112:115],v[124:127]\n"
CV "v215,v174,v175\n"
CV "v214,v172,v173\n"
MF "v[124:127],a[200:203],a[72:75],v[124:127]\n"
CV "v213,v170,v171\n"
CV "v212,v168,v169\n"
MF "v[132:135],a[200:203],a[76:79],v[132:135]\n"
"v_fma_f32 v160,v160,s54,v14\n"
"v_fma_f32 v161,v161,s54,v14\n"
MF "v[132:135],a[204:207],a[116:119],v[132:135]\n"
"s_waitcnt lgkmcnt(4)\n"
"v_fma_f32 v162,v162,s54,v14\n"
"v_fma_f32 v163,v163,s54,v14\n"
MF "v[100:103],a[208:211],a[140:143],v[100:103]\n"
TR "a[200:201],v0 offset:49152\n"
"v_fma_f32 v164,v164,s54,v14\n"
MF "v[100:103],a[212:215],a[180:183],v[100:103]\n"
TR "a[202:203],v0 offset:49664\n"
"v_fma_f32 v165,v165,s54,v14\n"
MF "v[108:111],a[212:215],a[184:187],v[108:111]\n"
"v_fma_f32 v166,v166,s54,v14\n"
"v_fma_f32 v167,v167,s54,v14\n"
MF "v[108:111],a[208:211],a[144:147],v[108:111]\n"
TR "a[204:205],v0 offset:53248\n"
CV "v211,v166,v167\n"
MF "v[116:119],a[208:211],a[148:151],v[116:119]\n"
TR "a[206:207],v0 offset:53760\n"
CV "v210,v164,v165\n"
MF "v[116:119],a[212:215],a[188:191],v[116:119]\n"
CV "v209,v162,v163\n"
CV "v208,v160,v161\n"
MF "v[124:127],a[212:215],a[192:195],v[124:127]\n"
"v_add_u32 v0,0xc000,v0\n"
"v_fma_f32 v152,v152,s54,v14\n"
MF "v[124:127],a[208:211],a[152:155],v[124:127]\n"
"v_add_u32 v1,0xc000,v1\n"
"v_fma_f32 v153,v153,s54,v14\n"
MF "v[132:135],a[208:211],a[156:159],v[132:135]\n"
"v_fma_f32 v154,v154,s54,v14\n"
"v_fma_f32 v155,v155,s54,v14\n"
MF "v[132:135],a[212:215],a[196:199],v[132:135]\n"
"s_waitcnt vmcnt(9) lgkmcnt(0)\n"
"s_barrier\n"
MF "v[96:99],a[216:219],a[60:63],v[96:99]\n"
TR "a[208:209],v1 offset:16392\n"
"v_fma_f32 v156,v156,s54,v14\n"
MF "v[96:99],a[220:223],a[100:103],v[96:99]\n"
TR "a[210:211],v1 offset:16904\n"
"v_fma_f32 v157,v157,s54,v14\n"
MF "v[104:107],a[220:223],a[104:107],v[104:107]\n"
"s_add_u32 s72,s72,6144\n"
"v_fma_f32 v158,v158,s54,v14\n"
"v_fma_f32 v159,v159,s54,v14\n"
MF "v[104:107],a[216:219],a[64:67],v[104:107]\n"
"s_cmp_eq_u32 s72,s17\n"
"s_cselect_b32 s72,s16,s72\n"
TR "a[212:213],v1 offset:20488\n"
MF "v[112:115],a[216:219],a[68:71],v[112:115]\n"
"s_add_u32 m0,s64,24576\n"
TR "a[214:215],v1 offset:21000\n"
CV "v207,v158,v159\n"
MF "v[112:115],a[220:223],a[108:111],v[112:115]\n"
LD "v7,s[32:35],s72 offen nt lds\n"
MF "v[120:123],a[220:223],a[112:115],v[120:123]\n"
"s_add_u32 m0,s64,25472\n"
CV "v206,v156,v157\n"
CV "v205,v154,v155\n"
MF "v[120:123],a[216:219],a[72:75],v[120:123]\n"
LD "v7,s[32:35],s72 offen offset:128 nt lds\n"
MF "v[128:131],a[216:219],a[76:79],v[128:131]\n"
CV "v204,v152,v153\n"
"v_fma_f32 v144,v144,s54,v14\n"
MF "v[128:131],a[220:223],a[116:119],v[128:131]\n"
"v_fma_f32 v145,v145,s54,v14\n"
"v_fma_f32 v146,v146,s54,v14\n"
MF "v[128:131],a[200:203],a[156:159],v[128:131]\n"
TR "a[216:217],v1 offset:32776\n"
"v_fma_f32 v147,v147,s54,v14\n"
MF "v[128:131],a[204:207],a[196:199],v[128:131]\n"
"s_add_u32 m0,s64,26368\n"
TR "a[218:219],v1 offset:33288\n"
"v_fma_f32 v148,v148,s54,v14\n"
MF "v[120:123],a[204:207],a[192:195],v[120:123]\n"
LD "v7,s[32:35],s72 offen offset:256 nt lds\n"
MF "v[120:123],a[200:203],a[152:155],v[120:123]\n"
TR "a[220:221],v1 offset:36872\n"
"v_fma_f32 v149,v149,s54,v14\n"
MF "v[112:115],a[200:203],a[148:151],v[112:115]\n"
TR "a[222:223],v1 offset:37384\n"
"v_fma_f32 v150,v150,s54,v14\n"
MF "v[112:115],a[204:207],a[188:191],v[112:115]\n"
"v_fma_f32 v151,v151,s54,v14\n"
CV "v203,v150,v151\n"
MF "v[104:107],a[204:207],a[184:187],v[104:107]\n"
"s_add_u32 s72,s72,6144\n"
CV "v202,v148,v149\n"
CV "v201,v146,v147\n"
MF "v[104:107],a[200:203],a[144:147],v[104:107]\n"
"s_add_u32 m0,s64,30720\n"
CV "v200,v144,v145\n"
"v_fma_f32 v136,v136,s54,v14\n"
MF "v[96:99],a[200:203],a[140:143],v[96:99]\n"
LD "v7,s[32:35],s72 offen nt lds\n"
MF "v[96:99],a[204:207],a[180:183],v[96:99]\n"
"s_waitcnt lgkmcnt(4)\n"
"v_fma_f32 v137,v137,s54,v14\n"
"v_fma_f32 v138,v138,s54,v14\n"
MF "v[60:63],a[208:211],a[60:63],v[60:63]\n"
TR "a[200:201],v1 offset:16384\n"
"v_fma_f32 v139,v139,s54,v14\n"
MF "v[60:63],a[212:215],a[100:103],v[60:63]\n"
TR "a[202:203],v1 offset:16896\n"
"v_fma_f32 v140,v140,s54,v14\n"
MF "v[68:71],a[212:215],a[104:107],v[68:71]\n"
"v_fma_f32 v141,v141,s54,v14\n"
"v_fma_f32 v142,v142,s54,v14\n"
MF "v[68:71],a[208:211],a[64:67],v[68:71]\n"
TR "a[204:205],v1 offset:20480\n"
"v_fma_f32 v143,v143,s54,v14\n"
MF "v[76:79],a[208:211],a[68:71],v[76:79]\n"
"s_add_u32 m0,s64,31616\n"
TR "a[206:207],v1 offset:20992\n"
CV "v199,v142,v143\n"
MF "v[76:79],a[212:215],a[108:111],v[76:79]\n"
LD "v7,s[32:35],s72 offen offset:128 nt lds\n"
MF "v[84:87],a[212:215],a[112:115],v[84:87]\n"
CV "v198,v140,v141\n"
CV "v197,v138,v139\n"
MF "v[84:87],a[208:211],a[72:75],v[84:87]\n"
CV "v196,v136,v137\n"
"v_fma_f32 v128,v128,s54,v14\n"
MF "v[92:95],a[208:211],a[76:79],v[92:95]\n"
"s_add_u32 m0,s64,32512\n"
"v_fma_f32 v129,v129,s54,v14\n"
"v_fma_f32 v130,v130,s54,v14\n"
MF "v[92:95],a[212:215],a[116:119],v[92:95]\n"
"s_waitcnt lgkmcnt(4)\n"
LD "v7,s[32:35],s72 offen offset:256 nt lds\n"
MF "v[60:63],a[216:219],a[140:143],v[60:63]\n"
TR "a[208:209],v1 offset:32768\n"
"v_fma_f32 v131,v131,s54,v14\n"
MF "v[60:63],a[220:223],a[180:183],v[60:63]\n"
TR "a[210:211],v1 offset:33280\n"
"v_fma_f32 v132,v132,s54,v14\n"
MF "v[68:71],a[220:223],a[184:187],v[68:71]\n"
"v_fma_f32 v133,v133,s54,v14\n"
"v_fma_f32 v134,v134,s54,v14\n"
MF "v[68:71],a[216:219],a[144:147],v[68:71]\n"
TR "a[212:213],v1 offset:36864\n"
"v_fma_f32 v135,v135,s54,v14\n"
MF "v[76:79],a[216:219],a[148:151],v[76:79]\n"
TR "a[214:215],v1 offset:37376\n"
CV "v195,v134,v135\n"
MF "v[76:79],a[220:223],a[188:191],v[76:79]\n"
CV "v194,v132,v133\n"
CV "v193,v130,v131\n"
MF "v[84:87],a[220:223],a[192:195],v[84:87]\n"
CV "v192,v128,v129\n"
"v_fma_f32 v120,v120,s54,v14\n"
MF "v[84:87],a[216:219],a[152:155],v[84:87]\n"
"v_fma_f32 v121,v121,s54,v14\n"
"v_fma_f32 v122,v122,s54,v14\n"
MF "v[92:95],a[216:219],a[156:159],v[92:95]\n"
"v_fma_f32 v123,v123,s54,v14\n"
"v_fma_f32 v124,v124,s54,v14\n"
MF "v[92:95],a[220:223],a[196:199],v[92:95]\n"
"s_waitcnt lgkmcnt(0)\n"
"v_fma_f32 v125,v125,s54,v14\n"
"v_fma_f32 v126,v126,s54,v14\n"
MF "v[56:59],a[200:203],a[60:63],v[56:59]\n"
TR "a[216:217],v0 offset:16392\n"
"v_fma_f32 v127,v127,s54,v14\n"
MF "v[56:59],a[204:207],a[100:103],v[56:59]\n"
TR "a[218:219],v0 offset:16904\n"
CV "v191,v126,v127\n"
MF "v[64:67],a[204:207],a[104:107],v[64:67]\n"
CV "v190,v124,v125\n"
CV "v189,v122,v123\n"
MF "v[64:67],a[200:203],a[64:67],v[64:67]\n"
TR "a[220:221],v0 offset:20488\n"
CV "v188,v120,v121\n"
MF "v[72:75],a[200:203],a[68:71],v[72:75]\n"
TR "a[222:223],v0 offset:21000\n"
"v_fma_f32 v112,v112,s54,v14\n"
MF "v[72:75],a[204:207],a[108:111],v[72:75]\n"
"v_fma_f32 v113,v113,s54,v14\n"
"v_fma_f32 v114,v114,s54,v14\n"
MF "v[80:83],a[204:207],a[112:115],v[80:83]\n"
"v_fma_f32 v115,v115,s54,v14\n"
"v_fma_f32 v116,v116,s54,v14\n"
MF "v[80:83],a[200:203],a[72:75],v[80:83]\n"
"v_fma_f32 v117,v117,s54,v14\n"
"v_fma_f32 v118,v118,s54,v14\n"
MF "v[88:91],a[200:203],a[76:79],v[88:91]\n"
"v_fma_f32 v119,v119,s54,v14\n"
CV "v187,v118,v119\n"
MF "v[88:91],a[204:207],a[116:119],v[88:91]\n"
CV "v186,v116,v117\n"
CV "v185,v114,v115\n"
MF "v[88:91],a[208:211],a[156:159],v[88:91]\n"
TR "a[200:201],v0 offset:32776\n"
CV "v184,v112,v113\n"
MF "v[88:91],a[212:215],a[196:199],v[88:91]\n"
TR "a[202:203],v0 offset:33288\n"
"v_fma_f32 v104,v104,s54,v14\n"
MF "v[80:83],a[212:215],a[192:195],v[80:83]\n"
"v_fma_f32 v105,v105,s54,v14\n"
"v_fma_f32 v106,v106,s54,v14\n"
MF "v[80:83],a[208:211],a[152:155],v[80:83]\n"
TR "a[204:205],v0 offset:36872\n"
"v_fma_f32 v107,v107,s54,v14\n"
MF "v[72:75],a[208:211],a[148:151],v[72:75]\n"
TR "a[206:207],v0 offset:37384\n"
"v_fma_f32 v108,v108,s54,v14\n"
MF "v[72:75],a[212:215],a[188:191],v[72:75]\n"
"v_fma_f32 v109,v109,s54,v14\n"
"v_fma_f32 v110,v110,s54,v14\n"
MF "v[64:67],a[212:215],a[184:187],v[64:67]\n"
"v_fma_f32 v111,v111,s54,v14\n"
CV "v183,v110,v111\n"
MF "v[64:67],a[208:211],a[144:147],v[64:67]\n"
CV "v182,v108,v109\n"
CV "v181,v106,v107\n"
MF "v[56:59],a[208:211],a[140:143],v[56:59]\n"
CV "v180,v104,v105\n"
"v_fma_f32 v96,v96,s54,v14\n"
MF "v[56:59],a[212:215],a[180:183],v[56:59]\n"
"s_waitcnt lgkmcnt(4)\n"
"v_fma_f32 v97,v97,s54,v14\n"
"v_fma_f32 v98,v98,s54,v14\n"
MF "v[20:23],a[216:219],a[60:63],v[20:23]\n"
TR "a[208:209],v0 offset:16384\n"
"v_fma_f32 v99,v99,s54,v14\n"
MF "v[20:23],a[220:223],a[100:103],v[20:23]\n"
TR "a[210:211],v0 offset:16896\n"
"v_fma_f32 v100,v100,s54,v14\n"
MF "v[28:31],a[220:223],a[104:107],v[28:31]\n"
"v_fma_f32 v101,v101,s54,v14\n"
"v_fma_f32 v102,v102,s54,v14\n"
MF "v[28:31],a[216:219],a[64:67],v[28:31]\n"
TR "a[212:213],v0 offset:20480\n"
"v_fma_f32 v103,v103,s54,v14\n"
MF "v[36:39],a[216:219],a[68:71],v[36:39]\n"
TR "a[214:215],v0 offset:20992\n"
CV "v179,v102,v103\n"
MF "v[36:39],a[220:223],a[108:111],v[36:39]\n"
CV "v178,v100,v101\n"
CV "v177,v98,v99\n"
MF "v[44:47],a[220:223],a[112:115],v[44:47]\n"
CV "v176,v96,v97\n"
"v_fma_f32 v88,v88,s54,v14\n"
MF "v[44:47],a[216:219],a[72:75],v[44:47]\n"
"v_fma_f32 v89,v89,s54,v14\n"
"v_fma_f32 v90,v90,s54,v14\n"
MF "v[52:55],a[216:219],a[76:79],v[52:55]\n"
"v_fma_f32 v91,v91,s54,v14\n"
"v_fma_f32 v92,v92,s54,v14\n"
MF "v[52:55],a[220:223],a[116:119],v[52:55]\n"
"s_waitcnt lgkmcnt(4)\n"
"v_fma_f32 v93,v93,s54,v14\n"
"v_fma_f32 v94,v94,s54,v14\n"
MF "v[20:23],a[200:203],a[140:143],v[20:23]\n"
TR "a[216:217],v0 offset:32768\n"
"v_fma_f32 v95,v95,s54,v14\n"
MF "v[20:23],a[204:207],a[180:183],v[20:23]\n"
TR "a[218:219],v0 offset:33280\n"
CV "v175,v94,v95\n"
MF "v[28:31],a[204:207],a[184:187],v[28:31]\n"
CV "v174,v92,v93\n"
CV "v173,v90,v91\n"
MF "v[28:31],a[200:203],a[144:147],v[28:31]\n"
TR "a[220:221],v0 offset:36864\n"
CV "v172,v88,v89\n"
MF "v[36:39],a[200:203],a[148:151],v[36:39]\n"
TR "a[222:223],v0 offset:37376\n"
"v_fma_f32 v80,v80,s54,v14\n"
MF "v[36:39],a[204:207],a[188:191],v[36:39]\n"
"v_fma_f32 v81,v81,s54,v14\n"
"v_fma_f32 v82,v82,s54,v14\n"
MF "v[44:47],a[204:207],a[192:195],v[44:47]\n"
"v_fma_f32 v83,v83,s54,v14\n"
"v_fma_f32 v84,v84,s54,v14\n"
MF "v[44:47],a[200:203],a[152:155],v[44:47]\n"
"v_fma_f32 v85,v85,s54,v14\n"
"v_fma_f32 v86,v86,s54,v14\n"
MF "v[52:55],a[200:203],a[156:159],v[52:55]\n"
"v_fma_f32 v87,v87,s54,v14\n"
CV "v171,v86,v87\n"
MF "v[52:55],a[204:207],a[196:199],v[52:55]\n"
"s_waitcnt lgkmcnt(0)\n"
"s_barrier\n"
MF "v[16:19],a[208:211],a[60:63],v[16:19]\n"
"s_add_u32 m0,s65,49152\n"
CV "v170,v84,v85\n"
CV "v169,v82,v83\n"
MF "v[16:19],a[212:215],a[100:103],v[16:19]\n"
LD "v8,s[36:39],s73 offen nt lds\n"
MF "v[24:27],a[212:215],a[104:107],v[24:27]\n"
"s_add_u32 m0,s65,50048\n"
CV "v168,v80,v81\n"
"v_fma_f32 v72,v72,s54,v14\n"
MF "v[24:27],a[208:211],a[64:67],v[24:27]\n"
LD "v8,s[36:39],s73 offen offset:128 nt lds\n"
MF "v[32:35],a[208:211],a[68:71],v[32:35]\n"
"v_fma_f32 v73,v73,s54,v14\n"
"v_fma_f32 v74,v74,s54,v14\n"
MF "v[32:35],a[212:215],a[108:111],v[32:35]\n"
"s_add_u32 m0,s65,50944\n"
"v_fma_f32 v75,v75,s54,v14\n"
"v_fma_f32 v76,v76,s54,v14\n"
MF "v[40:43],a[212:215],a[112:115],v[40:43]\n"
LD "v8,s[36:39],s73 offen offset:256 nt lds\n"
MF "v[40:43],a[208:211],a[72:75],v[40:43]\n"
"v_fma_f32 v77,v77,s54,v14\n"
"v_fma_f32 v78,v78,s54,v14\n"
MF "v[48:51],a[208:211],a[76:79],v[48:51]\n"
"s_add_u32 m0,s65,49152\n"
"v_fma_f32 v79,v79,s54,v14\n"
CV "v167,v78,v79\n"
MF "v[48:51],a[212:215],a[116:119],v[48:51]\n"
LD "v8,s[36:39],s73 offen offset:3072 nt lds\n"
MF "v[48:51],a[216:219],a[156:159],v[48:51]\n"
"s_add_u32 m0,s65,50048\n"
CV "v166,v76,v77\n"
CV "v165,v74,v75\n"
MF "v[48:51],a[220:223],a[196:199],v[48:51]\n"
LD "v8,s[36:39],s73 offen offset:3200 nt lds\n"
MF "v[40:43],a[220:223],a[192:195],v[40:43]\n"
CV "v164,v72,v73\n"
"v_fma_f32 v64,v64,s54,v14\n"
MF "v[40:43],a[216:219],a[152:155],v[40:43]\n"
"s_add_u32 m0,s65,50944\n"
"v_fma_f32 v65,v65,s54,v14\n"
"v_fma_f32 v66,v66,s54,v14\n"
MF "v[32:35],a[216:219],a[148:151],v[32:35]\n"
LD "v8,s[36:39],s73 offen offset:3328 nt lds\n"
MF "v[32:35],a[220:223],a[188:191],v[32:35]\n"
"v_fma_f32 v67,v67,s54,v14\n"
"v_fma_f32 v68,v68,s54,v14\n"
MF "v[24:27],a[220:223],a[184:187],v[24:27]\n"
"v_fma_f32 v69,v69,s54,v14\n"
"v_fma_f32 v70,v70,s54,v14\n"
MF "v[24:27],a[216:219],a[144:147],v[24:27]\n"
"v_fma_f32 v71,v71,s54,v14\n"
CV "v163,v70,v71\n"
MF "v[16:19],a[216:219],a[140:143],v[16:19]\n"
CV "v162,v68,v69\n"
CV "v161,v66,v67\n"
MF "v[16:19],a[220:223],a[180:183],v[16:19]\n"
CV "v160,v64,v65\n"
"v_pk_fma_f32 v[56:57],v[56:57],s[54:55],v[14:15] op_sel_hi:[1,1,0]\n"
"v_pk_fma_f32 v[58:59],v[58:59],s[54:55],v[14:15] op_sel_hi:[1,1,0]\n"
"s_nop 0\n"
"v_pk_fma_f32 v[60:61],v[60:61],s[54:55],v[14:15] op_sel_hi:[1,1,0]\n"
"v_pk_fma_f32 v[62:63],v[62:63],s[54:55],v[14:15] op_sel_hi:[1,1,0]\n"
CV "v159,v62,v63\n"
CV "v158,v60,v61\n"
CV "v157,v58,v59\n"
CV "v156,v56,v57\n"
"v_pk_mul_f32 v[48:49],v[48:49],s[54:55]\n"
"v_pk_mul_f32 v[50:51],v[50:51],s[54:55]\n"
"v_pk_mul_f32 v[52:53],v[52:53],s[54:55]\n"
"v_pk_mul_f32 v[54:55],v[54:55],s[54:55]\n"
CV "v155,v54,v55\n"
CV "v154,v52,v53\n"
CV "v153,v50,v51\n"
CV "v152,v48,v49\n"
"v_pk_mul_f32 v[40:41],v[40:41],s[54:55]\n"
"v_pk_mul_f32 v[42:43],v[42:43],s[54:55]\n"
"v_pk_mul_f32 v[44:45],v[44:45],s[54:55]\n"
"v_pk_mul_f32 v[46:47],v[46:47],s[54:55]\n"
CV "v151,v46,v47\n"
CV "v150,v44,v45\n"
CV "v149,v42,v43\n"
CV "v148,v40,v41\n"
"v_pk_mul_f32 v[32:33],v[32:33],s[54:55]\n"
"v_pk_mul_f32 v[34:35],v[34:35],s[54:55]\n"
"v_pk_mul_f32 v[36:37],v[36:37],s[54:55]\n"
"v_pk_mul_f32 v[38:39],v[38:39],s[54:55]\n"
CV "v147,v38,v39\n"
CV "v146,v36,v37\n"
CV "v145,v34,v35\n"
CV "v144,v32,v33\n"
"v_pk_mul_f32 v[24:25],v[24:25],s[54:55]\n"
"v_pk_mul_f32 v[26:27],v[26:27],s[54:55]\n"
"v_pk_mul_f32 v[28:29],v[28:29],s[54:55]\n"
"v_pk_mul_f32 v[30:31],v[30:31],s[54:55]\n"
CV "v143,v30,v31\n"
CV "v142,v28,v29\n"
CV "v141,v26,v27\n"
CV "v140,v24,v25\n"
"v_pk_mul_f32 v[16:17],v[16:17],s[54:55]\n"
"v_pk_mul_f32 v[18:19],v[18:19],s[54:55]\n"
"v_pk_mul_f32 v[20:21],v[20:21],s[54:55]\n"
"v_pk_mul_f32 v[22:23],v[22:23],s[54:55]\n"
CV "v139,v22,v23\n"
CV "v138,v20,v21\n"
CV "v137,v18,v19\n"
CV "v136,v16,v17\n"
"s_cmp_eq_u32 s57,s69\n"
"s_cbranch_scc1 .LattB\n"
AR "v0,a244\n"
AR "v2,a245\n"
"v_xor_b32 v1,64,v0\n"
"v_add_u32 v2,49152,v2\n"
"v_xor_b32 v3,32,v2\n"
"v_xor_b32 v4,64,v2\n"
"v_xor_b32 v5,96,v2\n"
"s_mov_b32 s61,7\n"
"s_memtime s[52:53]\n"
"s_memrealtime s[100:101]\n"
"s_waitcnt lgkmcnt(0)\n"
"s_sub_u32 s53,s52,s80\n"
"s_cmp_gt_u32 s53,30000\n"
"s_cselect_b32 s101,1,0\n"
"s_cmp_eq_u32 s87,2\n"
"s_cselect_b32 s87,s101,s87\n"
"s_cmp_eq_u32 s87,1\n"
"s_cmp_eq_u32 s57,0\n"
"s_mov_b32 s101,0\n"
"s_cselect_b32 s101,130,s101\n"
"s_cmp_eq_u32 s87,1\n"
"s_cselect_b32 s97,s101,0\n"
"s_cselect_b32 s21,0,0\n"
"s_add_u32 s21,s21,s100\n"
".Latt2x:\n"
"s_cmp_eq_u32 s97,0\n"
"s_cselect_b32 s95,0x47800000,0\n"
"s_mov_b32 s77,s95\n"
"s_waitcnt vmcnt(12) lgkmcnt(0)\n"
"s_barrier\n"
"ds_read_b128 v[16:19],v0\n"
"ds_read_b128 v[20:23],v1\n"
"ds_read_b128 v[24:27],v0 offset:1024\n"
"s_cmp_eq_u32 s57,1\n"
"s_cbranch_scc1 .Lt0kx\n"
"s_waitcnt lgkmcnt(2)\n"
MF "v[72:75],v[16:19],v[136:139],0\n"
"s_add_u32 s73,s73,12288\n"
"ds_read_b128 v[28:31],v1 offset:1024\n"
AR "v10,a224\n"
MF "v[76:79],v[16:19],v[140:143],0\n"
"s_cmp_eq_u32 s73,s19\n"
"s_cselect_b32 s73,s18,s73\n"
"s_add_u32 m0,s65,73728\n"
MF "v[80:83],v[16:19],v[144:147],0\n"
LD "v8,s[36:39],s73 offen nt lds\n"
MF "v[84:87],v[16:19],v[148:151],0\n"
"v_max_f32_dpp v10,|v10|,|v10| quad_perm:[1,0,3,2] row_mask:0xf bank_mask:0xf\n"
AR "v11,a225\n"
MF "v[88:91],v[16:19],v[152:155],0\n"
"s_waitcnt lgkmcnt(2)\n"
"ds_read_b128 v[16:19],v0 offset:2048\n"
AR "v12,a226\n"
MF "v[72:75],v[20:23],v[156:159],v[72:75]\n"
"s_add_u32 m0,s65,74624\n"
"v_max_f32_dpp v10,v10,v10 quad_perm:[2,3,0,1] row_mask:0xf bank_mask:0xf\n"
"v_max_f32_dpp v11,|v11|,|v11| quad_perm:[1,0,3,2] row_mask:0xf bank_mask:0xf\n"
MF "v[76:79],v[20:23],v[160:163],v[76:79]\n"
LD "v8,s[36:39],s73 offen offset:128 nt lds\n"
MF "v[80:83],v[20:23],v[164:167],v[80:83]\n"
"v_max_f32_dpp v10,v10,v10 row_half_mirror row_mask:0xf bank_mask:0xf\n"
"v_max_f32_dpp v11,v11,v11 quad_perm:[2,3,0,1] row_mask:0xf bank_mask:0xf\n"
MF "v[84:87],v[20:23],v[168:171],v[84:87]\n"
"v_readlane_b32 s13,v10,0\n"
"v_readlane_b32 s0,v10,24\n"
MF "v[88:91],v[20:23],v[172:175],v[88:91]\n"
"s_waitcnt lgkmcnt(2)\n"
"ds_read_b128 v[20:23],v1 offset:2048\n"
"v_readlane_b32 s1,v10,32\n"
MF "v[72:75],v[24:27],v[176:179],v[72:75]\n"
"s_add_u32 m0,s65,75520\n"
"v_readlane_b32 s2,v10,56\n"
"v_max_f32_dpp v11,v11,v11 row_half_mirror row_mask:0xf bank_mask:0xf\n"
MF "v[76:79],v[24:27],v[180:183],v[76:79]\n"
LD "v8,s[36:39],s73 offen offset:256 nt lds\n"
MF "v[80:83],v[24:27],v[184:187],v[80:83]\n"
"s_min_u32 s13,s13,s0\n"
"v_readlane_b32 s11,v11,0\n"
"v_readlane_b32 s3,v11,24\n"
MF "v[84:87],v[24:27],v[188:191],v[84:87]\n"
"s_min_u32 s1,s1,s2\n"
"v_readlane_b32 s4,v11,32\n"
"v_readlane_b32 s5,v11,56\n"
MF "v[88:91],v[24:27],v[192:195],v[88:91]\n"
"s_waitcnt lgkmcnt(2)\n"
"ds_read_b128 v[24:27],v0 offset:6144\n"
"v_max_f32_dpp v12,|v12|,|v12| quad_perm:[1,0,3,2] row_mask:0xf bank_mask:0xf\n"
MF "v[72:75],v[28:31],v[196:199],v[72:75]\n"
"s_add_u32 m0,s65,73728\n"
MF "v[76:79],v[28:31],v[200:203],v[76:79]\n"
LD "v8,s[36:39],s73 offen offset:3072 nt lds\n"
MF "v[80:83],v[28:31],v[204:207],v[80:83]\n"
"s_min_u32 s13,s13,s1\n"
"v_max_f32_dpp v12,v12,v12 quad_perm:[2,3,0,1] row_mask:0xf bank_mask:0xf\n"
MF "v[84:87],v[28:31],v[208:211],v[84:87]\n"
"s_and_b32 s13,s13,0x7f800000\n"
MF "v[88:91],v[28:31],v[212:215],v[88:91]\n"
"s_waitcnt lgkmcnt(2)\n"
"ds_read_b128 v[28:31],v1 offset:6144\n"
"v_max_f32_dpp v12,v12,v12 row_half_mirror row_mask:0xf bank_mask:0xf\n"
MF "v[72:75],v[16:19],v[216:219],v[72:75]\n"
"s_add_u32 m0,s65,74624\n"
"v_readlane_b32 s10,v12,0\n"
"v_readlane_b32 s6,v12,24\n"
MF "v[76:79],v[16:19],v[220:223],v[76:79]\n"
LD "v8,s[36:39],s73 offen offset:3200 nt lds\n"
MF "v[80:83],v[16:19],v[224:227],v[80:83]\n"
"s_sub_u32 s13,s13,0x14000000\n"
"v_readlane_b32 s7,v12,32\n"
"v_readlane_b32 s8,v12,56\n"
MF "v[84:87],v[16:19],v[228:231],v[84:87]\n"
"s_cmp_lt_u32 s13,0x50000000\n"
"s_cselect_b32 s13,s13,0xed400000\n"
MF "v[88:91],v[16:19],v[232:235],v[88:91]\n"
"s_waitcnt lgkmcnt(2)\n"
"ds_read_b128 v[16:19],v0 offset:7168\n"
MF "v[72:75],v[20:23],v[236:239],v[72:75]\n"
"s_add_u32 m0,s65,75520\n"
MF "v[76:79],v[20:23],v[240:243],v[76:79]\n"
LD "v8,s[36:39],s73 offen offset:3328 nt lds\n"
MF "v[80:83],v[20:23],v[244:247],v[80:83]\n"
"s_add_u32 s13,s13,0x12c00000\n"
MF "v[84:87],v[20:23],v[248:251],v[84:87]\n"
"s_min_u32 s11,s11,s3\n"
MF "v[88:91],v[20:23],v[252:255],v[88:91]\n"
"s_waitcnt lgkmcnt(2)\n"
"ds_read_b128 v[20:23],v1 offset:7168\n"
MF "v[112:115],v[24:27],v[136:139],0\n"
"s_min_u32 s4,s4,s5\n"
MF "v[116:119],v[24:27],v[140:143],0\n"
"s_min_u32 s11,s11,s4\n"
MF "v[120:123],v[24:27],v[144:147],0\n"
"s_and_b32 s11,s11,0x7f800000\n"
MF "v[124:127],v[24:27],v[148:151],0\n"
"s_sub_u32 s11,s11,0x14000000\n"
MF "v[128:131],v[24:27],v[152:155],0\n"
"s_waitcnt lgkmcnt(2)\n"
"ds_read_b128 v[24:27],v0 offset:8192\n"
MF "v[112:115],v[28:31],v[156:159],v[112:115]\n"
"s_min_u32 s10,s10,s6\n"
"v_max3_f32 v52,v72,v73,v74\n"
"v_max_f32 v52,v52,v75\n"
MF "v[116:119],v[28:31],v[160:163],v[116:119]\n"
"s_min_u32 s7,s7,s8\n"
"v_max3_f32 v53,v76,v77,v78\n"
"v_max_f32 v53,v53,v79\n"
MF "v[120:123],v[28:31],v[164:167],v[120:123]\n"
"s_min_u32 s10,s10,s7\n"
"v_max3_f32 v54,v80,v81,v82\n"
"v_max_f32 v54,v54,v83\n"
MF "v[124:127],v[28:31],v[168:171],v[124:127]\n"
"v_max3_f32 v55,v84,v85,v86\n"
"v_max_f32 v55,v55,v87\n"
MF "v[128:131],v[28:31],v[172:175],v[128:131]\n"
"s_waitcnt lgkmcnt(2)\n"
"ds_read_b128 v[28:31],v1 offset:8192\n"
"v_max3_f32 v56,v88,v89,v90\n"
MF "v[112:115],v[16:19],v[176:179],v[112:115]\n"
"s_cmp_lt_u32 s11,0x50000000\n"
"s_cselect_b32 s11,s11,0xed400000\n"
"v_max_f32 v56,v56,v91\n"
MF "v[116:119],v[16:19],v[180:183],v[116:119]\n"
"s_add_u32 s11,s11,0x12c00000\n"
MF "v[120:123],v[16:19],v[184:187],v[120:123]\n"
MF "v[124:127],v[16:19],v[188:191],v[124:127]\n"
MF "v[128:131],v[16:19],v[192:195],v[128:131]\n"
"s_waitcnt lgkmcnt(2)\n"
MF "v[112:115],v[20:23],v[196:199],v[112:115]\n"
MF "v[116:119],v[20:23],v[200:203],v[116:119]\n"
"s_waitcnt lgkmcnt(0)\n"
"s_barrier\n"
MF "v[120:123],v[20:23],v[204:207],v[120:123]\n"
"s_add_u32 s72,s72,6144\n"
MF "v[124:127],v[20:23],v[208:211],v[124:127]\n"
"s_cmp_eq_u32 s72,s17\n"
"s_cselect_b32 s72,s16,s72\n"
"s_add_u32 m0,s64,0\n"
MF "v[128:131],v[20:23],v[212:215],v[128:131]\n"
LD "v7,s[32:35],s72 offen nt lds\n"
MF "v[112:115],v[24:27],v[216:219],v[112:115]\n"
"s_add_u32 m0,s64,896\n"
MF "v[116:119],v[24:27],v[220:223],v[116:119]\n"
LD "v7,s[32:35],s72 offen offset:128 nt lds\n"
MF "v[120:123],v[24:27],v[224:227],v[120:123]\n"
"s_add_u32 m0,s64,1792\n"
MF "v[124:127],v[24:27],v[228:231],v[124:127]\n"
LD "v7,s[32:35],s72 offen offset:256 nt lds\n"
MF "v[128:131],v[24:27],v[232:235],v[128:131]\n"
"s_add_u32 s72,s72,6144\n"
"s_add_u32 m0,s64,6144\n"
MF "v[112:115],v[28:31],v[236:239],v[112:115]\n"
LD "v7,s[32:35],s72 offen nt lds\n"
MF "v[116:119],v[28:31],v[240:243],v[116:119]\n"
"s_add_u32 m0,s64,7040\n"
MF "v[120:123],v[28:31],v[244:247],v[120:123]\n"
LD "v7,s[32:35],s72 offen offset:128 nt lds\n"
MF "v[124:127],v[28:31],v[248:251],v[124:127]\n"
"s_add_u32 m0,s64,7936\n"
MF "v[128:131],v[28:31],v[252:255],v[128:131]\n"
LD "v7,s[32:35],s72 offen offset:256 nt lds\n"
"s_branch .Lt0ex\n"
".Lt0kx:\n"
"s_cbranch_execz .Lipr1\n"
"s_lshl_b32 s58,s56,10\n"
"s_add_u32 s58,s58,73728\n"
"v_add_u32 v32,s58,v9\n"
"v_add_u32 v33,65536,v32\n"
"s_waitcnt lgkmcnt(2)\n"
MF "v[72:75],v[16:19],v[136:139],0\n"
"s_add_u32 s73,s73,12288\n"
"ds_read_b128 v[28:31],v1 offset:1024\n"
AR "v10,a224\n"
MF "v[76:79],v[16:19],v[140:143],0\n"
"s_cmp_eq_u32 s73,s19\n"
"s_cselect_b32 s73,s18,s73\n"
"s_add_u32 m0,s65,73728\n"
MF "v[80:83],v[16:19],v[144:147],0\n"
LD "v8,s[36:39],s73 offen nt lds\n"
MF "v[84:87],v[16:19],v[148:151],0\n"
"v_max_f32_dpp v10,|v10|,|v10| quad_perm:[1,0,3,2] row_mask:0xf bank_mask:0xf\n"
AR "v11,a225\n"
MF "v[88:91],v[16:19],v[152:155],0\n"
"s_waitcnt lgkmcnt(2)\n"
"ds_read_b128 v[16:19],v0 offset:2048\n"
AR "v12,a226\n"
MF "v[72:75],v[20:23],v[156:159],v[72:75]\n"
"s_add_u32 m0,s65,74624\n"
"v_max_f32_dpp v10,v10,v10 quad_perm:[2,3,0,1] row_mask:0xf bank_mask:0xf\n"
"v_max_f32_dpp v11,|v11|,|v11| quad_perm:[1,0,3,2] row_mask:0xf bank_mask:0xf\n"
MF "v[76:79],v[20:23],v[160:163],v[76:79]\n"
LD "v8,s[36:39],s73 offen offset:128 nt lds\n"
MF "v[80:83],v[20:23],v[164:167],v[80:83]\n"
"v_max_f32_dpp v10,v10,v10 row_half_mirror row_mask:0xf bank_mask:0xf\n"
"v_max_f32_dpp v11,v11,v11 quad_perm:[2,3,0,1] row_mask:0xf bank_mask:0xf\n"
MF "v[84:87],v[20:23],v[168:171],v[84:87]\n"
"v_readlane_b32 s13,v10,0\n"
"v_readlane_b32 s0,v10,24\n"
MF "v[88:91],v[20:23],v[172:175],v[88:91]\n"
"s_waitcnt lgkmcnt(2)\n"
"ds_read_b128 v[20:23],v1 offset:2048\n"
"v_readlane_b32 s1,v10,32\n"
MF "v[72:75],v[24:27],v[176:179],v[72:75]\n"
"s_add_u32 m0,s65,75520\n"
"v_readlane_b32 s2,v10,56\n"
"v_max_f32_dpp v11,v11,v11 row_half_mirror row_mask:0xf bank_mask:0xf\n"
MF "v[76:79],v[24:27],v[180:183],v[76:79]\n"
LD "v8,s[36:39],s73 offen offset:256 nt lds\n"
MF "v[80:83],v[24:27],v[184:187],v[80:83]\n"
"s_min_u32 s13,s13,s0\n"
"v_readlane_b32 s11,v11,0\n"
"v_readlane_b32 s3,v11,24\n"
MF "v[84:87],v[24:27],v[188:191],v[84:87]\n"
"s_min_u32 s1,s1,s2\n"
"v_readlane_b32 s4,v11,32\n"
"v_readlane_b32 s5,v11,56\n"
MF "v[88:91],v[24:27],v[192:195],v[88:91]\n"
"s_waitcnt lgkmcnt(2)\n"
"ds_read_b128 v[24:27],v0 offset:6144\n"
"v_max_f32_dpp v12,|v12|,|v12| quad_perm:[1,0,3,2] row_mask:0xf bank_mask:0xf\n"
MF "v[72:75],v[28:31],v[196:199],v[72:75]\n"
"s_add_u32 m0,s65,73728\n"
"ds_write_b128 v32,a[0:3] offset:24576\n"
MF "v[76:79],v[28:31],v[200:203],v[76:79]\n"
LD "v8,s[36:39],s73 offen offset:3072 nt lds\n"
MF "v[80:83],v[28:31],v[204:207],v[80:83]\n"
"s_min_u32 s13,s13,s1\n"
"v_max_f32_dpp v12,v12,v12 quad_perm:[2,3,0,1] row_mask:0xf bank_mask:0xf\n"
AR "v62,a20\n"
MF "v[84:87],v[28:31],v[208:211],v[84:87]\n"
"s_and_b32 s13,s13,0x7f800000\n"
"ds_write_b128 v32,a[4:7] offset:28672\n"
MF "v[88:91],v[28:31],v[212:215],v[88:91]\n"
"s_waitcnt lgkmcnt(4)\n"
"ds_read_b128 v[28:31],v1 offset:6144\n"
"v_max_f32_dpp v12,v12,v12 row_half_mirror row_mask:0xf bank_mask:0xf\n"
MF "v[72:75],v[16:19],v[216:219],v[72:75]\n"
"s_add_u32 m0,s65,74624\n"
"v_readlane_b32 s10,v12,0\n"
"v_readlane_b32 s6,v12,24\n"
MF "v[76:79],v[16:19],v[220:223],v[76:79]\n"
LD "v8,s[36:39],s73 offen offset:3200 nt lds\n"
MF "v[80:83],v[16:19],v[224:227],v[80:83]\n"
"s_sub_u32 s13,s13,0x14000000\n"
"v_readlane_b32 s7,v12,32\n"
"v_readlane_b32 s8,v12,56\n"
MF "v[84:87],v[16:19],v[228:231],v[84:87]\n"
"s_cmp_lt_u32 s13,0x50000000\n"
"s_cselect_b32 s13,s13,0xed400000\n"
AR "v63,a21\n"
MF "v[88:91],v[16:19],v[232:235],v[88:91]\n"
"s_waitcnt lgkmcnt(4)\n"
"ds_read_b128 v[16:19],v0 offset:7168\n"
AR "v64,a22\n"
MF "v[72:75],v[20:23],v[236:239],v[72:75]\n"
"s_add_u32 m0,s65,75520\n"
"ds_write_b128 v32,a[8:11] offset:32768\n"
MF "v[76:79],v[20:23],v[240:243],v[76:79]\n"
LD "v8,s[36:39],s73 offen offset:3328 nt lds\n"
MF "v[80:83],v[20:23],v[244:247],v[80:83]\n"
"s_add_u32 s13,s13,0x12c00000\n"
"ds_write_b128 v32,a[12:15] offset:36864\n"
MF "v[84:87],v[20:23],v[248:251],v[84:87]\n"
"s_min_u32 s11,s11,s3\n"
"ds_write_b128 v32,a[16:19] offset:40960\n"
MF "v[88:91],v[20:23],v[252:255],v[88:91]\n"
"s_waitcnt lgkmcnt(7)\n"
"ds_read_b128 v[20:23],v1 offset:7168\n"
AR "v65,a23\n"
MF "v[112:115],v[24:27],v[136:139],0\n"
"s_min_u32 s4,s4,s5\n"
"ds_write_b128 v32,a[40:43] offset:45056\n"
MF "v[116:119],v[24:27],v[140:143],0\n"
"s_min_u32 s11,s11,s4\n"
"ds_write_b128 v32,a[44:47] offset:49152\n"
MF "v[120:123],v[24:27],v[144:147],0\n"
"s_and_b32 s11,s11,0x7f800000\n"
"ds_write_b128 v32,a[48:51] offset:53248\n"
MF "v[124:127],v[24:27],v[148:151],0\n"
"s_sub_u32 s11,s11,0x14000000\n"
"ds_write_b128 v32,a[52:55] offset:57344\n"
MF "v[128:131],v[24:27],v[152:155],0\n"
"s_waitcnt lgkmcnt(9)\n"
"ds_read_b128 v[24:27],v0 offset:8192\n"
AR "v66,a24\n"
MF "v[112:115],v[28:31],v[156:159],v[112:115]\n"
"s_min_u32 s10,s10,s6\n"
"v_max3_f32 v52,v72,v73,v74\n"
"v_max_f32 v52,v52,v75\n"
MF "v[116:119],v[28:31],v[160:163],v[116:119]\n"
"s_min_u32 s7,s7,s8\n"
"v_max3_f32 v53,v76,v77,v78\n"
"v_max_f32 v53,v53,v79\n"
MF "v[120:123],v[28:31],v[164:167],v[120:123]\n"
"s_min_u32 s10,s10,s7\n"
"v_max3_f32 v54,v80,v81,v82\n"
"v_max_f32 v54,v54,v83\n"
MF "v[124:127],v[28:31],v[168:171],v[124:127]\n"
"v_max3_f32 v55,v84,v85,v86\n"
"v_max_f32 v55,v55,v87\n"
MF "v[128:131],v[28:31],v[172:175],v[128:131]\n"
"s_waitcnt lgkmcnt(9)\n"
"ds_read_b128 v[28:31],v1 offset:8192\n"
"v_max3_f32 v56,v88,v89,v90\n"
MF "v[112:115],v[16:19],v[176:179],v[112:115]\n"
"s_cmp_lt_u32 s11,0x50000000\n"
"s_cselect_b32 s11,s11,0xed400000\n"
"v_max_f32 v56,v56,v91\n"
MF "v[116:119],v[16:19],v[180:183],v[116:119]\n"
"s_add_u32 s11,s11,0x12c00000\n"
"ds_write_b128 v32,a[56:59] offset:61440\n"
MF "v[120:123],v[16:19],v[184:187],v[120:123]\n"
"ds_write_b128 v33,a[80:83]\n"
MF "v[124:127],v[16:19],v[188:191],v[124:127]\n"
"ds_write_b128 v33,a[84:87] offset:4096\n"
MF "v[128:131],v[16:19],v[192:195],v[128:131]\n"
"s_waitcnt lgkmcnt(9)\n"
"ds_write_b128 v33,a[88:91] offset:8192\n"
MF "v[112:115],v[20:23],v[196:199],v[112:115]\n"
AR "v67,a25\n"
AR "v68,a26\n"
MF "v[116:119],v[20:23],v[200:203],v[116:119]\n"
"s_waitcnt lgkmcnt(0)\n"
"s_barrier\n"
MF "v[120:123],v[20:23],v[204:207],v[120:123]\n"
"s_add_u32 s72,s72,6144\n"
"ds_write_b128 v33,a[92:95] offset:12288\n"
MF "v[124:127],v[20:23],v[208:211],v[124:127]\n"
"s_cmp_eq_u32 s72,s17\n"
"s_cselect_b32 s72,s16,s72\n"
"s_add_u32 m0,s64,0\n"
MF "v[128:131],v[20:23],v[212:215],v[128:131]\n"
LD "v7,s[32:35],s72 offen nt lds\n"
MF "v[112:115],v[24:27],v[216:219],v[112:115]\n"
"s_add_u32 m0,s64,896\n"
"ds_write_b128 v33,a[96:99] offset:16384\n"
MF "v[116:119],v[24:27],v[220:223],v[116:119]\n"
LD "v7,s[32:35],s72 offen offset:128 nt lds\n"
MF "v[120:123],v[24:27],v[224:227],v[120:123]\n"
"s_add_u32 m0,s64,1792\n"
AR "v69,a27\n"
AR "v70,a28\n"
MF "v[124:127],v[24:27],v[228:231],v[124:127]\n"
LD "v7,s[32:35],s72 offen offset:256 nt lds\n"
MF "v[128:131],v[24:27],v[232:235],v[128:131]\n"
"s_add_u32 s72,s72,6144\n"
"s_add_u32 m0,s64,6144\n"
AR "v71,a29\n"
MF "v[112:115],v[28:31],v[236:239],v[112:115]\n"
LD "v7,s[32:35],s72 offen nt lds\n"
MF "v[116:119],v[28:31],v[240:243],v[116:119]\n"
"s_add_u32 m0,s64,7040\n"
"ds_write_b128 v33,a[120:123] offset:20480\n"
MF "v[120:123],v[28:31],v[244:247],v[120:123]\n"
LD "v7,s[32:35],s72 offen offset:128 nt lds\n"
MF "v[124:127],v[28:31],v[248:251],v[124:127]\n"
"s_add_u32 m0,s64,7936\n"
AR "v132,a30\n"
AR "v133,a31\n"
MF "v[128:131],v[28:31],v[252:255],v[128:131]\n"
LD "v7,s[32:35],s72 offen offset:256 nt lds\n"
AR "v134,a32\n"
AR "v135,a33\n"
AR "v6,a34\n"
AR "v13,a35\n"
AR "v14,a36\n"
AR "v15,a37\n"
"v_accvgpr_mov_b32 a254,a38\n"
"v_accvgpr_mov_b32 a255,a39\n"
"v_accvgpr_mov_b32 a240,a168\n"
"v_accvgpr_mov_b32 a241,a169\n"
"v_accvgpr_mov_b32 a242,a170\n"
"v_accvgpr_mov_b32 a243,a171\n"
"v_accvgpr_mov_b32 a244,a172\n"
"v_accvgpr_mov_b32 a245,a173\n"
"v_accvgpr_mov_b32 a246,a174\n"
"v_accvgpr_mov_b32 a247,a175\n"
"v_accvgpr_mov_b32 a248,a176\n"
"v_accvgpr_mov_b32 a249,a177\n"
"v_accvgpr_mov_b32 a250,a178\n"
"v_accvgpr_mov_b32 a251,a179\n"
".Lt0ex:\n"
"s_cbranch_execz .Lipr1\n"
"s_nop 1\n"
"v_max3_f32 v52,v52,v112,v113\n"
"v_max3_f32 v53,v53,v116,v117\n"
"v_max3_f32 v54,v54,v120,v121\n"
"v_max3_f32 v55,v55,v124,v125\n"
"v_max3_f32 v56,v56,v128,v129\n"
"v_max3_f32 v52,v52,v114,v115\n"
"v_max3_f32 v53,v53,v118,v119\n"
"v_max3_f32 v54,v54,v122,v123\n"
"v_max3_f32 v55,v55,v126,v127\n"
"v_max3_f32 v56,v56,v130,v131\n"
"v_mov_b32 v57,v52\n"
"v_mov_b32 v58,v53\n"
"v_mov_b32 v59,v54\n"
"v_mov_b32 v60,v55\n"
"v_mov_b32 v61,v56\n"
"v_permlane16_swap_b32 v52,v57\n"
"v_permlane16_swap_b32 v53,v58\n"
"v_permlane16_swap_b32 v54,v59\n"
"v_permlane16_swap_b32 v55,v60\n"
"v_permlane16_swap_b32 v56,v61\n"
"v_max_f32 v52,v52,v57\n"
"v_max_f32 v53,v53,v58\n"
"v_max_f32 v54,v54,v59\n"
"v_max_f32 v55,v55,v60\n"
"v_max_f32 v56,v56,v61\n"
"v_mov_b32 v57,v52\n"
"v_mov_b32 v58,v53\n"
"v_mov_b32 v59,v54\n"
"v_mov_b32 v60,v55\n"
"v_mov_b32 v61,v56\n"
"v_permlane32_swap_b32 v52,v57\n"
"v_permlane32_swap_b32 v53,v58\n"
"v_permlane32_swap_b32 v54,v59\n"
"v_permlane32_swap_b32 v55,v60\n"
"v_permlane32_swap_b32 v56,v61\n"
"v_max_f32 v52,v52,v57\n"
"v_max_f32 v53,v53,v58\n"
"v_max_f32 v54,v54,v59\n"
"v_max_f32 v55,v55,v60\n"
"v_max_f32 v56,v56,v61\n"
"v_sub_f32 v32,0xc1300000,v52\n"
"v_sub_f32 v36,0xc1300000,v53\n"
"v_sub_f32 v40,0xc1300000,v54\n"
"v_sub_f32 v44,0xc1300000,v55\n"
"v_sub_f32 v48,0xc1300000,v56\n"
"v_mov_b32 v33,v32\n"
"v_mov_b32 v34,v32\n"
"v_mov_b32 v35,v32\n"
"v_mov_b32 v37,v36\n"
"v_mov_b32 v38,v36\n"
"v_mov_b32 v39,v36\n"
"v_mov_b32 v41,v40\n"
"v_mov_b32 v42,v40\n"
"v_mov_b32 v43,v40\n"
"v_mov_b32 v45,v44\n"
"v_mov_b32 v46,v44\n"
"v_mov_b32 v47,v44\n"
"v_mov_b32 v49,v48\n"
"v_mov_b32 v50,v48\n"
"v_mov_b32 v51,v48\n"
"v_add_f32 v72,v72,v32\n"
"v_add_f32 v73,v73,v32\n"
"v_add_f32 v74,v74,v32\n"
"v_add_f32 v75,v75,v32\n"
"v_add_f32 v112,v112,v32\n"
"v_add_f32 v113,v113,v32\n"
"v_add_f32 v114,v114,v32\n"
"v_add_f32 v115,v115,v32\n"
"v_exp_f32 v72,v72\n"
"v_exp_f32 v73,v73\n"
"v_exp_f32 v74,v74\n"
"v_exp_f32 v75,v75\n"
"v_exp_f32 v112,v112\n"
"v_exp_f32 v113,v113\n"
"v_exp_f32 v114,v114\n"
"v_exp_f32 v115,v115\n"
"v_add_f32 v52,v72,v73\n"
"v_add_f32 v52,v52,v74\n"
"v_add_f32 v52,v52,v75\n"
"v_add_f32 v52,v52,v112\n"
"v_add_f32 v52,v52,v113\n"
"v_add_f32 v52,v52,v114\n"
"v_add_f32 v52,v52,v115\n"
CV "v72,v72,v73\n"
CV "v73,v74,v75\n"
CV "v74,v112,v113\n"
CV "v75,v114,v115\n"
"v_add_f32 v76,v76,v36\n"
"v_add_f32 v77,v77,v36\n"
"v_add_f32 v78,v78,v36\n"
"v_add_f32 v79,v79,v36\n"
"v_add_f32 v116,v116,v36\n"
"v_add_f32 v117,v117,v36\n"
"v_add_f32 v118,v118,v36\n"
"v_add_f32 v119,v119,v36\n"
"v_exp_f32 v76,v76\n"
"v_exp_f32 v77,v77\n"
"v_exp_f32 v78,v78\n"
"v_exp_f32 v79,v79\n"
"v_exp_f32 v116,v116\n"
"v_exp_f32 v117,v117\n"
"v_exp_f32 v118,v118\n"
"v_exp_f32 v119,v119\n"
"v_add_f32 v53,v76,v77\n"
"v_add_f32 v53,v53,v78\n"
"v_add_f32 v53,v53,v79\n"
"v_add_f32 v53,v53,v116\n"
"v_add_f32 v53,v53,v117\n"
"v_add_f32 v53,v53,v118\n"
"v_add_f32_e64 v53,v53,v119\n"
CV "v76,v76,v77\n"
CV "v77,v78,v79\n"
CV "v78,v116,v117\n"
CV "v79,v118,v119\n"
"v_add_f32 v80,v80,v40\n"
"v_add_f32 v81,v81,v40\n"
"v_add_f32 v82,v82,v40\n"
"v_add_f32 v83,v83,v40\n"
"v_add_f32 v120,v120,v40\n"
"v_add_f32 v121,v121,v40\n"
"v_add_f32 v122,v122,v40\n"
"v_add_f32 v123,v123,v40\n"
"v_exp_f32 v80,v80\n"
"v_exp_f32 v81,v81\n"
"v_exp_f32 v82,v82\n"
"v_exp_f32 v83,v83\n"
"v_exp_f32 v120,v120\n"
"v_exp_f32 v121,v121\n"
"v_exp_f32 v122,v122\n"
"v_exp_f32 v123,v123\n"
"v_add_f32 v54,v80,v81\n"
"v_add_f32 v54,v54,v82\n"
"v_add_f32 v54,v54,v83\n"
"v_add_f32 v54,v54,v120\n"
"v_add_f32 v54,v54,v121\n"
"v_add_f32 v54,v54,v122\n"
"v_add_f32_e64 v54,v54,v123\n"
CV "v80,v80,v81\n"
CV "v81,v82,v83\n"
CV "v82,v120,v121\n"
CV "v83,v122,v123\n"
"v_add_f32 v84,v84,v44\n"
"v_add_f32 v85,v85,v44\n"
"v_add_f32 v86,v86,v44\n"
"v_add_f32 v87,v87,v44\n"
"v_add_f32 v124,v124,v44\n"
"v_add_f32 v125,v125,v44\n"
"v_add_f32 v126,v126,v44\n"
"v_add_f32 v127,v127,v44\n"
"v_mov_b32 v55,0\n"
"v_add_f32 v88,v88,v48\n"
"v_add_f32 v89,v89,v48\n"
"v_add_f32 v90,v90,v48\n"
"v_add_f32 v91,v91,v48\n"
"v_add_f32 v128,v128,v48\n"
"v_add_f32 v129,v129,v48\n"
"v_add_f32 v130,v130,v48\n"
"v_add_f32 v131,v131,v48\n"
"v_mov_b32 v56,0\n"
"s_waitcnt vmcnt(18) lgkmcnt(0)\n"
"s_barrier\n"
"ds_read_b128 v[16:19],v0 offset:24576\n"
"ds_read_b128 v[20:23],v1 offset:24576\n"
"ds_read_b128 v[24:27],v0 offset:25600\n"
AW "a220,s13\n"
AW "a221,s13\n"
AW "a222,s13\n"
AW "a223,s13\n"
"s_waitcnt lgkmcnt(2)\n"
MF "v[92:95],v[16:19],v[136:139],v[32:35]\n"
"ds_read_b128 v[28:31],v1 offset:25600\n"
"s_waitcnt lgkmcnt(2)\n"
MF "v[92:95],v[20:23],v[156:159],v[92:95]\n"
"v_exp_f32 v84,v84\n"
MF "v[96:99],v[20:23],v[160:163],v[36:39]\n"
"v_exp_f32 v85,v85\n"
MF "v[96:99],v[16:19],v[140:143],v[96:99]\n"
"v_exp_f32 v86,v86\n"
MF "v[100:103],v[16:19],v[144:147],v[40:43]\n"
"v_exp_f32 v87,v87\n"
MF "v[100:103],v[20:23],v[164:167],v[100:103]\n"
"v_add_f32 v55,v55,v84\n"
"v_add_f32 v55,v55,v85\n"
MF "v[104:107],v[20:23],v[168:171],v[44:47]\n"
CV "v84,v84,v85\n"
"v_add_f32 v55,v55,v86\n"
MF "v[104:107],v[16:19],v[148:151],v[104:107]\n"
"v_add_f32 v55,v55,v87\n"
CV "v85,v86,v87\n"
MF "v[108:111],v[16:19],v[152:155],v[48:51]\n"
"v_exp_f32 v124,v124\n"
MF "v[108:111],v[20:23],v[172:175],v[108:111]\n"
"s_waitcnt lgkmcnt(0)\n"
"ds_read_b128 v[16:19],v0 offset:26624\n"
"ds_read_b128 v[20:23],v1 offset:26624\n"
MF "v[108:111],v[24:27],v[192:195],v[108:111]\n"
"v_exp_f32 v125,v125\n"
MF "v[108:111],v[28:31],v[212:215],v[108:111]\n"
"v_exp_f32 v126,v126\n"
MF "v[104:107],v[28:31],v[208:211],v[104:107]\n"
"v_exp_f32 v127,v127\n"
MF "v[104:107],v[24:27],v[188:191],v[104:107]\n"
"v_add_f32 v55,v55,v124\n"
"v_add_f32 v55,v55,v125\n"
MF "v[100:103],v[24:27],v[184:187],v[100:103]\n"
CV "v86,v124,v125\n"
"v_add_f32 v55,v55,v126\n"
MF "v[100:103],v[28:31],v[204:207],v[100:103]\n"
"v_add_f32 v55,v55,v127\n"
CV "v87,v126,v127\n"
MF "v[96:99],v[28:31],v[200:203],v[96:99]\n"
"v_exp_f32 v88,v88\n"
MF "v[96:99],v[24:27],v[180:183],v[96:99]\n"
"v_exp_f32 v89,v89\n"
MF "v[92:95],v[24:27],v[176:179],v[92:95]\n"
"v_exp_f32 v90,v90\n"
MF "v[92:95],v[28:31],v[196:199],v[92:95]\n"
"s_waitcnt lgkmcnt(0)\n"
"ds_read_b128 v[24:27],v0 offset:30720\n"
"ds_read_b128 v[28:31],v1 offset:30720\n"
MF "v[92:95],v[16:19],v[216:219],v[92:95]\n"
"v_exp_f32 v91,v91\n"
MF "v[92:95],v[20:23],v[236:239],v[92:95]\n"
"v_add_f32 v56,v56,v88\n"
"v_add_f32 v56,v56,v89\n"
MF "v[96:99],v[20:23],v[240:243],v[96:99]\n"
CV "v88,v88,v89\n"
"v_add_f32 v56,v56,v90\n"
MF "v[96:99],v[16:19],v[220:223],v[96:99]\n"
"v_add_f32 v56,v56,v91\n"
CV "v89,v90,v91\n"
MF "v[100:103],v[16:19],v[224:227],v[100:103]\n"
"v_exp_f32 v128,v128\n"
MF "v[100:103],v[20:23],v[244:247],v[100:103]\n"
"v_exp_f32 v129,v129\n"
MF "v[104:107],v[20:23],v[248:251],v[104:107]\n"
"v_exp_f32 v130,v130\n"
MF "v[104:107],v[16:19],v[228:231],v[104:107]\n"
"v_exp_f32 v131,v131\n"
MF "v[108:111],v[16:19],v[232:235],v[108:111]\n"
"v_add_f32 v56,v56,v128\n"
"v_add_f32 v56,v56,v129\n"
MF "v[108:111],v[20:23],v[252:255],v[108:111]\n"
"s_waitcnt lgkmcnt(0)\n"
"ds_read_b128 v[16:19],v0 offset:31744\n"
"ds_read_b128 v[20:23],v1 offset:31744\n"
MF "v[112:115],v[24:27],v[136:139],v[32:35]\n"
CV "v90,v128,v129\n"
"v_add_f32 v56,v56,v130\n"
MF "v[112:115],v[28:31],v[156:159],v[112:115]\n"
"v_add_f32 v56,v56,v131\n"
CV "v91,v130,v131\n"
MF "v[116:119],v[28:31],v[160:163],v[36:39]\n"
"v_exp_f32 v92,v92\n"
MF "v[116:119],v[24:27],v[140:143],v[116:119]\n"
"v_exp_f32 v93,v93\n"
MF "v[120:123],v[24:27],v[144:147],v[40:43]\n"
"v_exp_f32 v94,v94\n"
MF "v[120:123],v[28:31],v[164:167],v[120:123]\n"
"v_exp_f32 v95,v95\n"
MF "v[124:127],v[28:31],v[168:171],v[44:47]\n"
"v_add_f32 v57,v52,v92\n"
"v_add_f32 v57,v57,v93\n"
MF "v[124:127],v[24:27],v[148:151],v[124:127]\n"
CV "v92,v92,v93\n"
"v_add_f32 v57,v57,v94\n"
MF "v[128:131],v[24:27],v[152:155],v[48:51]\n"
"v_add_f32 v57,v57,v95\n"
CV "v93,v94,v95\n"
MF "v[128:131],v[28:31],v[172:175],v[128:131]\n"
"s_waitcnt lgkmcnt(0)\n"
"ds_read_b128 v[24:27],v0 offset:32768\n"
"ds_read_b128 v[28:31],v1 offset:32768\n"
MF "v[128:131],v[16:19],v[192:195],v[128:131]\n"
"v_exp_f32 v96,v96\n"
MF "v[128:131],v[20:23],v[212:215],v[128:131]\n"
"v_exp_f32 v97,v97\n"
MF "v[124:127],v[20:23],v[208:211],v[124:127]\n"
"v_exp_f32 v98,v98\n"
MF "v[124:127],v[16:19],v[188:191],v[124:127]\n"
"v_exp_f32 v99,v99\n"
MF "v[120:123],v[16:19],v[184:187],v[120:123]\n"
"v_add_f32 v58,v53,v96\n"
"v_add_f32 v58,v58,v97\n"
MF "v[120:123],v[20:23],v[204:207],v[120:123]\n"
CV "v96,v96,v97\n"
"v_add_f32 v58,v58,v98\n"
MF "v[116:119],v[20:23],v[200:203],v[116:119]\n"
"v_add_f32 v58,v58,v99\n"
CV "v97,v98,v99\n"
MF "v[116:119],v[16:19],v[180:183],v[116:119]\n"
"s_waitcnt vmcnt(12) lgkmcnt(0)\n"
"s_barrier\n"
MF "v[112:115],v[16:19],v[176:179],v[112:115]\n"
"v_exp_f32 v100,v100\n"
MF "v[112:115],v[20:23],v[196:199],v[112:115]\n"
"s_nop 0\n"
TR "v[16:17],v2\n"
TR "v[18:19],v2 offset:6144\n"
MF "v[112:115],v[24:27],v[216:219],v[112:115]\n"
"s_add_u32 s72,s72,6144\n"
TR "v[20:21],v3\n"
TR "v[22:23],v3 offset:6144\n"
MF "v[112:115],v[28:31],v[236:239],v[112:115]\n"
"s_cmp_eq_u32 s72,s17\n"
"s_cselect_b32 s72,s16,s72\n"
"s_add_u32 m0,s64,24576\n"
MF "v[116:119],v[28:31],v[240:243],v[116:119]\n"
LD "v7,s[32:35],s72 offen nt lds\n"
MF "v[116:119],v[24:27],v[220:223],v[116:119]\n"
"s_add_u32 m0,s64,25472\n"
"v_exp_f32 v101,v101\n"
MF "v[120:123],v[24:27],v[224:227],v[120:123]\n"
LD "v7,s[32:35],s72 offen offset:128 nt lds\n"
MF "v[120:123],v[28:31],v[244:247],v[120:123]\n"
"v_exp_f32 v102,v102\n"
MF "v[124:127],v[28:31],v[248:251],v[124:127]\n"
"s_add_u32 m0,s64,26368\n"
"v_exp_f32 v103,v103\n"
MF "v[124:127],v[24:27],v[228:231],v[124:127]\n"
LD "v7,s[32:35],s72 offen offset:256 nt lds\n"
MF "v[128:131],v[24:27],v[232:235],v[128:131]\n"
"s_add_u32 s72,s72,6144\n"
"v_add_f32 v59,v54,v100\n"
"v_add_f32 v59,v59,v101\n"
MF "v[128:131],v[28:31],v[252:255],v[128:131]\n"
"s_waitcnt lgkmcnt(2)\n"
TR "v[24:25],v4\n"
TR "v[26:27],v4 offset:6144\n"
MF "a[0:3],v[16:19],v[72:75],a[220:223]\n"
"s_add_u32 m0,s64,30720\n"
TR "v[28:29],v5\n"
TR "v[30:31],v5 offset:6144\n"
MF "a[4:7],v[16:19],v[76:79],a[220:223]\n"
LD "v7,s[32:35],s72 offen nt lds\n"
MF "a[8:11],v[16:19],v[80:83],a[220:223]\n"
"s_add_u32 m0,s64,31616\n"
CV "v100,v100,v101\n"
"v_add_f32 v59,v59,v102\n"
MF "a[12:15],v[16:19],v[84:87],a[220:223]\n"
LD "v7,s[32:35],s72 offen offset:128 nt lds\n"
MF "a[16:19],v[16:19],v[88:91],a[220:223]\n"
"s_waitcnt lgkmcnt(4)\n"
TR "v[16:17],v2 offset:1024\n"
TR "v[18:19],v2 offset:7168\n"
MF "a[36:39],v[20:23],v[88:91],a[220:223]\n"
"v_add_f32 v59,v59,v103\n"
CV "v101,v102,v103\n"
MF "a[32:35],v[20:23],v[84:87],a[220:223]\n"
"s_add_u32 m0,s64,32512\n"
"v_exp_f32 v104,v104\n"
MF "a[28:31],v[20:23],v[80:83],a[220:223]\n"
LD "v7,s[32:35],s72 offen offset:256 nt lds\n"
MF "a[24:27],v[20:23],v[76:79],a[220:223]\n"
"v_exp_f32 v105,v105\n"
MF "a[20:23],v[20:23],v[72:75],a[220:223]\n"
"s_waitcnt lgkmcnt(4)\n"
TR "v[20:21],v3 offset:1024\n"
TR "v[22:23],v3 offset:7168\n"
MF "a[40:43],v[24:27],v[72:75],a[220:223]\n"
"v_exp_f32 v106,v106\n"
MF "a[44:47],v[24:27],v[76:79],a[220:223]\n"
"v_exp_f32 v107,v107\n"
MF "a[48:51],v[24:27],v[80:83],a[220:223]\n"
"v_add_f32 v60,v55,v104\n"
"v_add_f32 v60,v60,v105\n"
MF "a[52:55],v[24:27],v[84:87],a[220:223]\n"
CV "v104,v104,v105\n"
"v_add_f32 v60,v60,v106\n"
MF "a[56:59],v[24:27],v[88:91],a[220:223]\n"
"s_waitcnt lgkmcnt(4)\n"
TR "v[24:25],v4 offset:1024\n"
TR "v[26:27],v4 offset:7168\n"
MF "a[76:79],v[28:31],v[88:91],a[220:223]\n"
"v_add_f32 v60,v60,v107\n"
CV "v105,v106,v107\n"
MF "a[72:75],v[28:31],v[84:87],a[220:223]\n"
"v_exp_f32 v108,v108\n"
MF "a[68:71],v[28:31],v[80:83],a[220:223]\n"
"v_exp_f32 v109,v109\n"
MF "a[64:67],v[28:31],v[76:79],a[220:223]\n"
"v_exp_f32 v110,v110\n"
MF "a[60:63],v[28:31],v[72:75],a[220:223]\n"
"s_waitcnt lgkmcnt(4)\n"
TR "v[28:29],v5 offset:1024\n"
TR "v[30:31],v5 offset:7168\n"
MF "a[80:83],v[16:19],v[72:75],a[220:223]\n"
"v_exp_f32 v111,v111\n"
MF "a[84:87],v[16:19],v[76:79],a[220:223]\n"
"v_add_f32 v61,v56,v108\n"
"v_add_f32 v61,v61,v109\n"
MF "a[88:91],v[16:19],v[80:83],a[220:223]\n"
CV "v108,v108,v109\n"
"v_add_f32 v61,v61,v110\n"
MF "a[92:95],v[16:19],v[84:87],a[220:223]\n"
"v_add_f32 v61,v61,v111\n"
CV "v109,v110,v111\n"
MF "a[96:99],v[16:19],v[88:91],a[220:223]\n"
"s_waitcnt lgkmcnt(4)\n"
TR "v[16:17],v2 offset:2048\n"
TR "v[18:19],v2 offset:8192\n"
MF "a[116:119],v[20:23],v[88:91],a[220:223]\n"
"v_exp_f32 v112,v112\n"
MF "a[112:115],v[20:23],v[84:87],a[220:223]\n"
"v_exp_f32 v113,v113\n"
MF "a[108:111],v[20:23],v[80:83],a[220:223]\n"
"v_exp_f32 v114,v114\n"
MF "a[104:107],v[20:23],v[76:79],a[220:223]\n"
"v_exp_f32 v115,v115\n"
MF "a[100:103],v[20:23],v[72:75],a[220:223]\n"
"s_waitcnt lgkmcnt(4)\n"
TR "v[20:21],v3 offset:2048\n"
TR "v[22:23],v3 offset:8192\n"
MF "a[120:123],v[24:27],v[72:75],a[220:223]\n"
"v_add_f32 v57,v57,v112\n"
"v_add_f32 v57,v57,v113\n"
MF "a[124:127],v[24:27],v[76:79],a[220:223]\n"
CV "v94,v112,v113\n"
"v_add_f32 v57,v57,v114\n"
MF "a[128:131],v[24:27],v[80:83],a[220:223]\n"
"v_add_f32 v57,v57,v115\n"
CV "v95,v114,v115\n"
MF "a[132:135],v[24:27],v[84:87],a[220:223]\n"
"v_exp_f32 v116,v116\n"
MF "a[136:139],v[24:27],v[88:91],a[220:223]\n"
"s_waitcnt lgkmcnt(4)\n"
TR "v[24:25],v4 offset:2048\n"
TR "v[26:27],v4 offset:8192\n"
MF "a[156:159],v[28:31],v[88:91],a[220:223]\n"
"v_exp_f32 v117,v117\n"
MF "a[152:155],v[28:31],v[84:87],a[220:223]\n"
"v_exp_f32 v118,v118\n"
MF "a[148:151],v[28:31],v[80:83],a[220:223]\n"
"v_exp_f32 v119,v119\n"
MF "a[144:147],v[28:31],v[76:79],a[220:223]\n"
"v_add_f32 v58,v58,v116\n"
"v_add_f32 v58,v58,v117\n"
MF "a[140:143],v[28:31],v[72:75],a[220:223]\n"
"s_waitcnt lgkmcnt(4)\n"
TR "v[28:29],v5 offset:2048\n"
TR "v[30:31],v5 offset:8192\n"
MF "a[160:163],v[16:19],v[72:75],a[220:223]\n"
CV "v98,v116,v117\n"
"v_add_f32 v58,v58,v118\n"
MF "a[164:167],v[16:19],v[76:79],a[220:223]\n"
"v_add_f32 v58,v58,v119\n"
CV "v99,v118,v119\n"
MF "a[168:171],v[16:19],v[80:83],a[220:223]\n"
"v_exp_f32 v120,v120\n"
MF "a[172:175],v[16:19],v[84:87],a[220:223]\n"
"v_exp_f32 v121,v121\n"
MF "a[176:179],v[16:19],v[88:91],a[220:223]\n"
"s_waitcnt lgkmcnt(4)\n"
"v_exp_f32 v122,v122\n"
MF "a[196:199],v[20:23],v[88:91],a[220:223]\n"
"v_exp_f32 v123,v123\n"
MF "a[192:195],v[20:23],v[84:87],a[220:223]\n"
"v_add_f32 v59,v59,v120\n"
"v_add_f32 v59,v59,v121\n"
MF "a[188:191],v[20:23],v[80:83],a[220:223]\n"
CV "v102,v120,v121\n"
"v_add_f32 v59,v59,v122\n"
MF "a[184:187],v[20:23],v[76:79],a[220:223]\n"
"s_waitcnt vmcnt(6) lgkmcnt(0)\n"
"s_barrier\n"
MF "a[180:183],v[20:23],v[72:75],a[220:223]\n"
"s_add_u32 s73,s73,12288\n"
"ds_read_b128 v[16:19],v0\n"
"ds_read_b128 v[20:23],v1\n"
MF "a[200:203],v[24:27],v[72:75],a[220:223]\n"
"s_cmp_eq_u32 s73,s19\n"
"s_cselect_b32 s73,s18,s73\n"
"s_add_u32 m0,s65,49152\n"
MF "a[204:207],v[24:27],v[76:79],a[220:223]\n"
LD "v8,s[36:39],s73 offen nt lds\n"
MF "a[208:211],v[24:27],v[80:83],a[220:223]\n"
"s_add_u32 m0,s65,50048\n"
"v_add_f32 v59,v59,v123\n"
CV "v103,v122,v123\n"
MF "a[212:215],v[24:27],v[84:87],a[220:223]\n"
LD "v8,s[36:39],s73 offen offset:128 nt lds\n"
MF "a[216:219],v[24:27],v[88:91],a[220:223]\n"
"s_add_u32 m0,s65,50944\n"
"ds_read_b128 v[24:27],v0 offset:1024\n"
MF "a[236:239],v[28:31],v[88:91],a[220:223]\n"
LD "v8,s[36:39],s73 offen offset:256 nt lds\n"
MF "a[232:235],v[28:31],v[84:87],a[220:223]\n"
MF "a[228:231],v[28:31],v[80:83],a[220:223]\n"
MF "a[224:227],v[28:31],v[76:79],a[220:223]\n"
MF "a[220:223],v[28:31],v[72:75],a[220:223]\n"
"s_waitcnt lgkmcnt(1)\n"
".Laloopx:\n"
MF "v[72:75],v[16:19],v[136:139],v[32:35]\n"
"ds_read_b128 v[28:31],v1 offset:1024\n"
MF "v[72:75],v[20:23],v[156:159],v[72:75]\n"
MF "v[76:79],v[20:23],v[160:163],v[36:39]\n"
MF "v[76:79],v[16:19],v[140:143],v[76:79]\n"
MF "v[80:83],v[16:19],v[144:147],v[40:43]\n"
MF "v[80:83],v[20:23],v[164:167],v[80:83]\n"
MF "v[84:87],v[20:23],v[168:171],v[44:47]\n"
MF "v[84:87],v[16:19],v[148:151],v[84:87]\n"
"s_add_u32 m0,s65,49152\n"
MF "v[88:91],v[16:19],v[152:155],v[48:51]\n"
LD "v8,s[36:39],s73 offen offset:3072 nt lds\n"
MF "v[88:91],v[20:23],v[172:175],v[88:91]\n"
"s_waitcnt lgkmcnt(0)\n"
"ds_read_b128 v[16:19],v0 offset:2048\n"
"ds_read_b128 v[20:23],v1 offset:2048\n"
MF "v[88:91],v[24:27],v[192:195],v[88:91]\n"
"v_exp_f32 v124,v124\n"
MF "v[88:91],v[28:31],v[212:215],v[88:91]\n"
"s_add_u32 m0,s65,50048\n"
"v_exp_f32 v125,v125\n"
MF "v[84:87],v[28:31],v[208:211],v[84:87]\n"
LD "v8,s[36:39],s73 offen offset:3200 nt lds\n"
MF "v[84:87],v[24:27],v[188:191],v[84:87]\n"
"v_exp_f32 v126,v126\n"
MF "v[80:83],v[24:27],v[184:187],v[80:83]\n"
"v_exp_f32 v127,v127\n"
MF "v[80:83],v[28:31],v[204:207],v[80:83]\n"
"s_add_u32 m0,s65,50944\n"
"v_add_f32 v60,v60,v124\n"
"v_add_f32 v60,v60,v125\n"
MF "v[76:79],v[28:31],v[200:203],v[76:79]\n"
LD "v8,s[36:39],s73 offen offset:3328 nt lds\n"
MF "v[76:79],v[24:27],v[180:183],v[76:79]\n"
CV "v106,v124,v125\n"
"v_add_f32 v60,v60,v126\n"
MF "v[72:75],v[24:27],v[176:179],v[72:75]\n"
"v_add_f32 v60,v60,v127\n"
CV "v107,v126,v127\n"
MF "v[72:75],v[28:31],v[196:199],v[72:75]\n"
"s_waitcnt lgkmcnt(0)\n"
"ds_read_b128 v[24:27],v0 offset:6144\n"
"ds_read_b128 v[28:31],v1 offset:6144\n"
MF "v[72:75],v[16:19],v[216:219],v[72:75]\n"
"v_exp_f32 v128,v128\n"
MF "v[72:75],v[20:23],v[236:239],v[72:75]\n"
"v_exp_f32 v129,v129\n"
MF "v[76:79],v[20:23],v[240:243],v[76:79]\n"
"v_exp_f32 v130,v130\n"
MF "v[76:79],v[16:19],v[220:223],v[76:79]\n"
"v_exp_f32 v131,v131\n"
MF "v[80:83],v[16:19],v[224:227],v[80:83]\n"
"v_add_f32 v61,v61,v128\n"
"v_add_f32 v61,v61,v129\n"
MF "v[80:83],v[20:23],v[244:247],v[80:83]\n"
CV "v110,v128,v129\n"
"v_add_f32 v61,v61,v130\n"
MF "v[84:87],v[20:23],v[248:251],v[84:87]\n"
"v_add_f32 v61,v61,v131\n"
CV "v111,v130,v131\n"
MF "v[84:87],v[16:19],v[228:231],v[84:87]\n"
"v_max3_f32 v10,v57,v58,v59\n"
"v_max3_f32 v10,v10,v60,v61\n"
MF "v[88:91],v[16:19],v[232:235],v[88:91]\n"
"v_cmp_ngt_f32 vcc,s95,v10\n"
MF "v[88:91],v[20:23],v[252:255],v[88:91]\n"
"s_waitcnt lgkmcnt(0)\n"
"s_cbranch_vccnz .Lstub_6\n"
".Lback_6:\n"
MF "v[112:115],v[24:27],v[136:139],v[32:35]\n"
"s_add_u32 s74,s74,12288\n"
"ds_read_b128 v[16:19],v0 offset:7168\n"
"ds_read_b128 v[20:23],v1 offset:7168\n"
MF "v[112:115],v[28:31],v[156:159],v[112:115]\n"
"s_cmp_eq_u32 s74,s20\n"
"s_cselect_b32 s74,s70,s74\n"
MF "v[116:119],v[28:31],v[160:163],v[36:39]\n"
"v_exp_f32 v72,v72\n"
MF "v[116:119],v[24:27],v[140:143],v[116:119]\n"
"v_exp_f32 v73,v73\n"
MF "v[120:123],v[24:27],v[144:147],v[40:43]\n"
"v_exp_f32 v74,v74\n"
MF "v[120:123],v[28:31],v[164:167],v[120:123]\n"
"v_exp_f32 v75,v75\n"
MF "v[124:127],v[28:31],v[168:171],v[44:47]\n"
"v_add_f32 v52,v57,v72\n"
"v_add_f32 v52,v52,v73\n"
MF "v[124:127],v[24:27],v[148:151],v[124:127]\n"
CV "v72,v72,v73\n"
"v_add_f32 v52,v52,v74\n"
MF "v[128:131],v[24:27],v[152:155],v[48:51]\n"
"v_add_f32 v52,v52,v75\n"
CV "v73,v74,v75\n"
MF "v[128:131],v[28:31],v[172:175],v[128:131]\n"
"s_waitcnt lgkmcnt(0)\n"
"ds_read_b128 v[24:27],v0 offset:8192\n"
"ds_read_b128 v[28:31],v1 offset:8192\n"
MF "v[128:131],v[16:19],v[192:195],v[128:131]\n"
"v_exp_f32 v76,v76\n"
MF "v[128:131],v[20:23],v[212:215],v[128:131]\n"
"v_exp_f32 v77,v77\n"
MF "v[124:127],v[20:23],v[208:211],v[124:127]\n"
"v_exp_f32 v78,v78\n"
MF "v[124:127],v[16:19],v[188:191],v[124:127]\n"
"v_exp_f32 v79,v79\n"
MF "v[120:123],v[16:19],v[184:187],v[120:123]\n"
"v_add_f32 v53,v58,v76\n"
"v_add_f32 v53,v53,v77\n"
MF "v[120:123],v[20:23],v[204:207],v[120:123]\n"
CV "v76,v76,v77\n"
"v_add_f32 v53,v53,v78\n"
MF "v[116:119],v[20:23],v[200:203],v[116:119]\n"
"v_add_f32 v53,v53,v79\n"
CV "v77,v78,v79\n"
MF "v[116:119],v[16:19],v[180:183],v[116:119]\n"
"s_waitcnt vmcnt(12) lgkmcnt(0)\n"
"s_barrier\n"
MF "v[112:115],v[16:19],v[176:179],v[112:115]\n"
"v_exp_f32 v80,v80\n"
MF "v[112:115],v[20:23],v[196:199],v[112:115]\n"
"s_nop 0\n"
TR "v[16:17],v2 offset:24576\n"
TR "v[18:19],v2 offset:30720\n"
MF "v[112:115],v[24:27],v[216:219],v[112:115]\n"
"s_add_u32 s72,s72,6144\n"
TR "v[20:21],v3 offset:24576\n"
TR "v[22:23],v3 offset:30720\n"
MF "v[112:115],v[28:31],v[236:239],v[112:115]\n"
"s_cmp_eq_u32 s72,s17\n"
"s_cselect_b32 s72,s16,s72\n"
"s_add_u32 m0,s64,0\n"
MF "v[116:119],v[28:31],v[240:243],v[116:119]\n"
LD "v7,s[32:35],s72 offen nt lds\n"
MF "v[116:119],v[24:27],v[220:223],v[116:119]\n"
"s_add_u32 m0,s64,896\n"
"v_exp_f32 v81,v81\n"
MF "v[120:123],v[24:27],v[224:227],v[120:123]\n"
LD "v7,s[32:35],s72 offen offset:128 nt lds\n"
MF "v[120:123],v[28:31],v[244:247],v[120:123]\n"
"v_exp_f32 v82,v82\n"
MF "v[124:127],v[28:31],v[248:251],v[124:127]\n"
"s_add_u32 m0,s64,1792\n"
"v_exp_f32 v83,v83\n"
MF "v[124:127],v[24:27],v[228:231],v[124:127]\n"
LD "v7,s[32:35],s72 offen offset:256 nt lds\n"
MF "v[128:131],v[24:27],v[232:235],v[128:131]\n"
"s_add_u32 s72,s72,6144\n"
"v_add_f32 v54,v59,v80\n"
"v_add_f32 v54,v54,v81\n"
MF "v[128:131],v[28:31],v[252:255],v[128:131]\n"
"s_waitcnt lgkmcnt(2)\n"
TR "v[24:25],v4 offset:24576\n"
TR "v[26:27],v4 offset:30720\n"
MF "a[0:3],v[16:19],v[92:95],a[0:3]\n"
"s_add_u32 m0,s64,6144\n"
TR "v[28:29],v5 offset:24576\n"
TR "v[30:31],v5 offset:30720\n"
MF "a[4:7],v[16:19],v[96:99],a[4:7]\n"
LD "v7,s[32:35],s72 offen nt lds\n"
MF "a[8:11],v[16:19],v[100:103],a[8:11]\n"
"s_add_u32 m0,s64,7040\n"
CV "v80,v80,v81\n"
"v_add_f32 v54,v54,v82\n"
MF "a[12:15],v[16:19],v[104:107],a[12:15]\n"
LD "v7,s[32:35],s72 offen offset:128 nt lds\n"
MF "a[16:19],v[16:19],v[108:111],a[16:19]\n"
"s_waitcnt lgkmcnt(4)\n"
TR "v[16:17],v2 offset:25600\n"
TR "v[18:19],v2 offset:31744\n"
MF "a[36:39],v[20:23],v[108:111],a[36:39]\n"
"v_add_f32 v54,v54,v83\n"
CV "v81,v82,v83\n"
MF "a[32:35],v[20:23],v[104:107],a[32:35]\n"
"s_add_u32 m0,s64,7936\n"
"v_exp_f32 v84,v84\n"
MF "a[28:31],v[20:23],v[100:103],a[28:31]\n"
LD "v7,s[32:35],s72 offen offset:256 nt lds\n"
MF "a[24:27],v[20:23],v[96:99],a[24:27]\n"
"v_exp_f32 v85,v85\n"
MF "a[20:23],v[20:23],v[92:95],a[20:23]\n"
"s_waitcnt lgkmcnt(4)\n"
TR "v[20:21],v3 offset:25600\n"
TR "v[22:23],v3 offset:31744\n"
MF "a[40:43],v[24:27],v[92:95],a[40:43]\n"
"v_exp_f32 v86,v86\n"
MF "a[44:47],v[24:27],v[96:99],a[44:47]\n"
"v_exp_f32 v87,v87\n"
MF "a[48:51],v[24:27],v[100:103],a[48:51]\n"
"v_add_f32 v55,v60,v84\n"
"v_add_f32 v55,v55,v85\n"
MF "a[52:55],v[24:27],v[104:107],a[52:55]\n"
CV "v84,v84,v85\n"
"v_add_f32 v55,v55,v86\n"
MF "a[56:59],v[24:27],v[108:111],a[56:59]\n"
"s_waitcnt lgkmcnt(4)\n"
TR "v[24:25],v4 offset:25600\n"
TR "v[26:27],v4 offset:31744\n"
MF "a[76:79],v[28:31],v[108:111],a[76:79]\n"
"v_add_f32 v55,v55,v87\n"
CV "v85,v86,v87\n"
MF "a[72:75],v[28:31],v[104:107],a[72:75]\n"
"v_exp_f32 v88,v88\n"
MF "a[68:71],v[28:31],v[100:103],a[68:71]\n"
"v_exp_f32 v89,v89\n"
MF "a[64:67],v[28:31],v[96:99],a[64:67]\n"
"v_exp_f32 v90,v90\n"
MF "a[60:63],v[28:31],v[92:95],a[60:63]\n"
"s_waitcnt lgkmcnt(4)\n"
TR "v[28:29],v5 offset:25600\n"
TR "v[30:31],v5 offset:31744\n"
MF "a[80:83],v[16:19],v[92:95],a[80:83]\n"
"v_exp_f32 v91,v91\n"
MF "a[84:87],v[16:19],v[96:99],a[84:87]\n"
"v_add_f32 v56,v61,v88\n"
"v_add_f32 v56,v56,v89\n"
MF "a[88:91],v[16:19],v[100:103],a[88:91]\n"
CV "v88,v88,v89\n"
"v_add_f32 v56,v56,v90\n"
MF "a[92:95],v[16:19],v[104:107],a[92:95]\n"
"v_add_f32 v56,v56,v91\n"
CV "v89,v90,v91\n"
MF "a[96:99],v[16:19],v[108:111],a[96:99]\n"
"s_waitcnt lgkmcnt(4)\n"
TR "v[16:17],v2 offset:26624\n"
TR "v[18:19],v2 offset:32768\n"
MF "a[116:119],v[20:23],v[108:111],a[116:119]\n"
"v_exp_f32 v112,v112\n"
MF "a[112:115],v[20:23],v[104:107],a[112:115]\n"
"v_exp_f32 v113,v113\n"
MF "a[108:111],v[20:23],v[100:103],a[108:111]\n"
"v_exp_f32 v114,v114\n"
MF "a[104:107],v[20:23],v[96:99],a[104:107]\n"
"v_exp_f32 v115,v115\n"
MF "a[100:103],v[20:23],v[92:95],a[100:103]\n"
"s_waitcnt lgkmcnt(4)\n"
TR "v[20:21],v3 offset:26624\n"
TR "v[22:23],v3 offset:32768\n"
MF "a[120:123],v[24:27],v[92:95],a[120:123]\n"
"v_add_f32 v52,v52,v112\n"
"v_add_f32 v52,v52,v113\n"
MF "a[124:127],v[24:27],v[96:99],a[124:127]\n"
CV "v74,v112,v113\n"
"v_add_f32 v52,v52,v114\n"
MF "a[128:131],v[24:27],v[100:103],a[128:131]\n"
"v_add_f32 v52,v52,v115\n"
CV "v75,v114,v115\n"
MF "a[132:135],v[24:27],v[104:107],a[132:135]\n"
"v_exp_f32 v116,v116\n"
MF "a[136:139],v[24:27],v[108:111],a[136:139]\n"
"s_waitcnt lgkmcnt(4)\n"
TR "v[24:25],v4 offset:26624\n"
TR "v[26:27],v4 offset:32768\n"
MF "a[156:159],v[28:31],v[108:111],a[156:159]\n"
"v_exp_f32 v117,v117\n"
MF "a[152:155],v[28:31],v[104:107],a[152:155]\n"
"v_exp_f32 v118,v118\n"
MF "a[148:151],v[28:31],v[100:103],a[148:151]\n"
"v_exp_f32 v119,v119\n"
MF "a[144:147],v[28:31],v[96:99],a[144:147]\n"
"v_add_f32 v53,v53,v116\n"
"v_add_f32 v53,v53,v117\n"
MF "a[140:143],v[28:31],v[92:95],a[140:143]\n"
"s_waitcnt lgkmcnt(4)\n"
TR "v[28:29],v5 offset:26624\n"
TR "v[30:31],v5 offset:32768\n"
MF "a[160:163],v[16:19],v[92:95],a[160:163]\n"
CV "v78,v116,v117\n"
"v_add_f32 v53,v53,v118\n"
MF "a[164:167],v[16:19],v[96:99],a[164:167]\n"
"v_add_f32 v53,v53,v119\n"
CV "v79,v118,v119\n"
MF "a[168:171],v[16:19],v[100:103],a[168:171]\n"
"v_exp_f32 v120,v120\n"
MF "a[172:175],v[16:19],v[104:107],a[172:175]\n"
"v_exp_f32 v121,v121\n"
MF "a[176:179],v[16:19],v[108:111],a[176:179]\n"
"s_waitcnt lgkmcnt(4)\n"
"v_exp_f32 v122,v122\n"
MF "a[196:199],v[20:23],v[108:111],a[196:199]\n"
"v_exp_f32 v123,v123\n"
MF "a[192:195],v[20:23],v[104:107],a[192:195]\n"
"v_add_f32 v54,v54,v120\n"
"v_add_f32 v54,v54,v121\n"
MF "a[188:191],v[20:23],v[100:103],a[188:191]\n"
CV "v82,v120,v121\n"
"v_add_f32 v54,v54,v122\n"
MF "a[184:187],v[20:23],v[96:99],a[184:187]\n"
"s_waitcnt vmcnt(12) lgkmcnt(0)\n"
"s_barrier\n"
MF "a[180:183],v[20:23],v[92:95],a[180:183]\n"
"s_add_u32 s73,s73,12288\n"
"ds_read_b128 v[16:19],v0 offset:24576\n"
"ds_read_b128 v[20:23],v1 offset:24576\n"
MF "a[200:203],v[24:27],v[92:95],a[200:203]\n"
"s_cmp_eq_u32 s73,s19\n"
"s_cselect_b32 s73,s18,s73\n"
"s_add_u32 m0,s65,73728\n"
MF "a[204:207],v[24:27],v[96:99],a[204:207]\n"
LD "v8,s[36:39],s73 offen nt lds\n"
MF "a[208:211],v[24:27],v[100:103],a[208:211]\n"
"s_add_u32 m0,s65,74624\n"
"v_add_f32 v54,v54,v123\n"
CV "v83,v122,v123\n"
MF "a[212:215],v[24:27],v[104:107],a[212:215]\n"
LD "v8,s[36:39],s73 offen offset:128 nt lds\n"
MF "a[216:219],v[24:27],v[108:111],a[216:219]\n"
"s_add_u32 m0,s65,75520\n"
"ds_read_b128 v[24:27],v0 offset:25600\n"
MF "a[236:239],v[28:31],v[108:111],a[236:239]\n"
LD "v8,s[36:39],s73 offen offset:256 nt lds\n"
MF "a[232:235],v[28:31],v[104:107],a[232:235]\n"
MF "a[228:231],v[28:31],v[100:103],a[228:231]\n"
MF "a[224:227],v[28:31],v[96:99],a[224:227]\n"
MF "a[220:223],v[28:31],v[92:95],a[220:223]\n"
"s_waitcnt lgkmcnt(1)\n"
MF "v[92:95],v[16:19],v[136:139],v[32:35]\n"
"s_add_u32 m0,s65,73728\n"
"ds_read_b128 v[28:31],v1 offset:25600\n"
MF "v[92:95],v[20:23],v[156:159],v[92:95]\n"
LD "v8,s[36:39],s73 offen offset:3072 nt lds\n"
MF "v[96:99],v[20:23],v[160:163],v[36:39]\n"
"v_exp_f32 v124,v124\n"
MF "v[96:99],v[16:19],v[140:143],v[96:99]\n"
"v_exp_f32 v125,v125\n"
MF "v[100:103],v[16:19],v[144:147],v[40:43]\n"
"s_add_u32 m0,s65,74624\n"
"v_exp_f32 v126,v126\n"
MF "v[100:103],v[20:23],v[164:167],v[100:103]\n"
LD "v8,s[36:39],s73 offen offset:3200 nt lds\n"
MF "v[104:107],v[20:23],v[168:171],v[44:47]\n"
"v_exp_f32 v127,v127\n"
MF "v[104:107],v[16:19],v[148:151],v[104:107]\n"
"s_add_u32 m0,s65,75520\n"
"v_add_f32 v55,v55,v124\n"
"v_add_f32 v55,v55,v125\n"
MF "v[108:111],v[16:19],v[152:155],v[48:51]\n"
LD "v8,s[36:39],s73 offen offset:3328 nt lds\n"
MF "v[108:111],v[20:23],v[172:175],v[108:111]\n"
"s_waitcnt lgkmcnt(0)\n"
"ds_read_b128 v[16:19],v0 offset:26624\n"
"ds_read_b128 v[20:23],v1 offset:26624\n"
MF "v[108:111],v[24:27],v[192:195],v[108:111]\n"
CV "v86,v124,v125\n"
"v_add_f32 v55,v55,v126\n"
MF "v[108:111],v[28:31],v[212:215],v[108:111]\n"
"v_add_f32 v55,v55,v127\n"
CV "v87,v126,v127\n"
MF "v[104:107],v[28:31],v[208:211],v[104:107]\n"
"v_exp_f32 v128,v128\n"
MF "v[104:107],v[24:27],v[188:191],v[104:107]\n"
"v_exp_f32 v129,v129\n"
MF "v[100:103],v[24:27],v[184:187],v[100:103]\n"
"v_exp_f32 v130,v130\n"
MF "v[100:103],v[28:31],v[204:207],v[100:103]\n"
"v_exp_f32 v131,v131\n"
MF "v[96:99],v[28:31],v[200:203],v[96:99]\n"
"v_add_f32 v56,v56,v128\n"
"v_add_f32 v56,v56,v129\n"
MF "v[96:99],v[24:27],v[180:183],v[96:99]\n"
CV "v90,v128,v129\n"
"v_add_f32 v56,v56,v130\n"
MF "v[92:95],v[24:27],v[176:179],v[92:95]\n"
"v_add_f32 v56,v56,v131\n"
CV "v91,v130,v131\n"
MF "v[92:95],v[28:31],v[196:199],v[92:95]\n"
"s_waitcnt lgkmcnt(0)\n"
"ds_read_b128 v[24:27],v0 offset:30720\n"
"ds_read_b128 v[28:31],v1 offset:30720\n"
MF "v[92:95],v[16:19],v[216:219],v[92:95]\n"
MF "v[92:95],v[20:23],v[236:239],v[92:95]\n"
MF "v[96:99],v[20:23],v[240:243],v[96:99]\n"
MF "v[96:99],v[16:19],v[220:223],v[96:99]\n"
MF "v[100:103],v[16:19],v[224:227],v[100:103]\n"
MF "v[100:103],v[20:23],v[244:247],v[100:103]\n"
MF "v[104:107],v[20:23],v[248:251],v[104:107]\n"
MF "v[104:107],v[16:19],v[228:231],v[104:107]\n"
"v_max3_f32 v10,v52,v53,v54\n"
"v_max3_f32 v10,v10,v55,v56\n"
MF "v[108:111],v[16:19],v[232:235],v[108:111]\n"
"v_cmp_ngt_f32 vcc,s95,v10\n"
MF "v[108:111],v[20:23],v[252:255],v[108:111]\n"
"s_waitcnt lgkmcnt(0)\n"
"s_cbranch_vccnz .Lstub_7\n"
".Lback_7:\n"
MF "v[112:115],v[24:27],v[136:139],v[32:35]\n"
"s_add_u32 s74,s74,12288\n"
"ds_read_b128 v[16:19],v0 offset:31744\n"
"ds_read_b128 v[20:23],v1 offset:31744\n"
MF "v[112:115],v[28:31],v[156:159],v[112:115]\n"
"s_cmp_eq_u32 s74,s20\n"
"s_cselect_b32 s74,s70,s74\n"
MF "v[116:119],v[28:31],v[160:163],v[36:39]\n"
"v_exp_f32 v92,v92\n"
MF "v[116:119],v[24:27],v[140:143],v[116:119]\n"
"v_exp_f32 v93,v93\n"
MF "v[120:123],v[24:27],v[144:147],v[40:43]\n"
"v_exp_f32 v94,v94\n"
MF "v[120:123],v[28:31],v[164:167],v[120:123]\n"
"v_exp_f32 v95,v95\n"
MF "v[124:127],v[28:31],v[168:171],v[44:47]\n"
"v_add_f32 v57,v52,v92\n"
"v_add_f32 v57,v57,v93\n"
MF "v[124:127],v[24:27],v[148:151],v[124:127]\n"
CV "v92,v92,v93\n"
"v_add_f32 v57,v57,v94\n"
MF "v[128:131],v[24:27],v[152:155],v[48:51]\n"
"v_add_f32 v57,v57,v95\n"
CV "v93,v94,v95\n"
MF "v[128:131],v[28:31],v[172:175],v[128:131]\n"
"s_waitcnt lgkmcnt(0)\n"
"ds_read_b128 v[24:27],v0 offset:32768\n"
"ds_read_b128 v[28:31],v1 offset:32768\n"
MF "v[128:131],v[16:19],v[192:195],v[128:131]\n"
"s_sub_u32 s61,s61,1\n"
"v_exp_f32 v96,v96\n"
MF "v[128:131],v[20:23],v[212:215],v[128:131]\n"
"s_cmp_eq_u32 s61,1\n"
"s_cselect_b32 s95,0,s95\n"
"v_add_f32 v58,v53,v96\n"
MF "v[124:127],v[20:23],v[208:211],v[124:127]\n"
"v_exp_f32 v97,v97\n"
MF "v[124:127],v[16:19],v[188:191],v[124:127]\n"
"v_exp_f32 v98,v98\n"
MF "v[120:123],v[16:19],v[184:187],v[120:123]\n"
"v_exp_f32 v99,v99\n"
MF "v[120:123],v[20:23],v[204:207],v[120:123]\n"
"v_add_f32 v58,v58,v97\n"
CV "v96,v96,v97\n"
MF "v[116:119],v[20:23],v[200:203],v[116:119]\n"
"v_add_f32 v58,v58,v98\n"
"v_add_f32 v58,v58,v99\n"
MF "v[116:119],v[16:19],v[180:183],v[116:119]\n"
"s_waitcnt vmcnt(12) lgkmcnt(0)\n"
"s_barrier\n"
MF "v[112:115],v[16:19],v[176:179],v[112:115]\n"
CV "v97,v98,v99\n"
MF "v[112:115],v[20:23],v[196:199],v[112:115]\n"
"s_nop 0\n"
TR "v[16:17],v2\n"
TR "v[18:19],v2 offset:6144\n"
MF "v[112:115],v[24:27],v[216:219],v[112:115]\n"
"s_add_u32 s72,s72,6144\n"
TR "v[20:21],v3\n"
TR "v[22:23],v3 offset:6144\n"
MF "v[112:115],v[28:31],v[236:239],v[112:115]\n"
"s_cmp_eq_u32 s72,s17\n"
"s_cselect_b32 s72,s16,s72\n"
"s_add_u32 m0,s64,24576\n"
MF "v[116:119],v[28:31],v[240:243],v[116:119]\n"
LD "v7,s[32:35],s72 offen nt lds\n"
MF "v[116:119],v[24:27],v[220:223],v[116:119]\n"
"s_add_u32 m0,s64,25472\n"
"v_exp_f32 v100,v100\n"
MF "v[120:123],v[24:27],v[224:227],v[120:123]\n"
LD "v7,s[32:35],s72 offen offset:128 nt lds\n"
MF "v[120:123],v[28:31],v[244:247],v[120:123]\n"
"v_exp_f32 v101,v101\n"
MF "v[124:127],v[28:31],v[248:251],v[124:127]\n"
"s_add_u32 m0,s64,26368\n"
"v_exp_f32 v102,v102\n"
MF "v[124:127],v[24:27],v[228:231],v[124:127]\n"
LD "v7,s[32:35],s72 offen offset:256 nt lds\n"
MF "v[128:131],v[24:27],v[232:235],v[128:131]\n"
"s_add_u32 s72,s72,6144\n"
"v_exp_f32 v103,v103\n"
MF "v[128:131],v[28:31],v[252:255],v[128:131]\n"
"s_waitcnt lgkmcnt(2)\n"
TR "v[24:25],v4\n"
TR "v[26:27],v4 offset:6144\n"
MF "a[0:3],v[16:19],v[72:75],a[0:3]\n"
"s_add_u32 m0,s64,30720\n"
TR "v[28:29],v5\n"
TR "v[30:31],v5 offset:6144\n"
MF "a[4:7],v[16:19],v[76:79],a[4:7]\n"
LD "v7,s[32:35],s72 offen nt lds\n"
MF "a[8:11],v[16:19],v[80:83],a[8:11]\n"
"s_add_u32 m0,s64,31616\n"
"v_add_f32 v59,v54,v100\n"
"v_add_f32 v59,v59,v101\n"
MF "a[12:15],v[16:19],v[84:87],a[12:15]\n"
LD "v7,s[32:35],s72 offen offset:128 nt lds\n"
MF "a[16:19],v[16:19],v[88:91],a[16:19]\n"
"s_waitcnt lgkmcnt(4)\n"
TR "v[16:17],v2 offset:1024\n"
TR "v[18:19],v2 offset:7168\n"
MF "a[36:39],v[20:23],v[88:91],a[36:39]\n"
CV "v100,v100,v101\n"
"v_add_f32 v59,v59,v102\n"
MF "a[32:35],v[20:23],v[84:87],a[32:35]\n"
"s_add_u32 m0,s64,32512\n"
"v_add_f32 v59,v59,v103\n"
CV "v101,v102,v103\n"
MF "a[28:31],v[20:23],v[80:83],a[28:31]\n"
LD "v7,s[32:35],s72 offen offset:256 nt lds\n"
MF "a[24:27],v[20:23],v[76:79],a[24:27]\n"
"v_exp_f32 v104,v104\n"
MF "a[20:23],v[20:23],v[72:75],a[20:23]\n"
"s_waitcnt lgkmcnt(4)\n"
TR "v[20:21],v3 offset:1024\n"
TR "v[22:23],v3 offset:7168\n"
MF "a[40:43],v[24:27],v[72:75],a[40:43]\n"
"v_exp_f32 v105,v105\n"
MF "a[44:47],v[24:27],v[76:79],a[44:47]\n"
"v_exp_f32 v106,v106\n"
MF "a[48:51],v[24:27],v[80:83],a[48:51]\n"
"v_exp_f32 v107,v107\n"
MF "a[52:55],v[24:27],v[84:87],a[52:55]\n"
"v_add_f32 v60,v55,v104\n"
"v_add_f32 v60,v60,v105\n"
MF "a[56:59],v[24:27],v[88:91],a[56:59]\n"
"s_waitcnt lgkmcnt(4)\n"
TR "v[24:25],v4 offset:1024\n"
TR "v[26:27],v4 offset:7168\n"
MF "a[76:79],v[28:31],v[88:91],a[76:79]\n"
CV "v104,v104,v105\n"
"v_add_f32 v60,v60,v106\n"
MF "a[72:75],v[28:31],v[84:87],a[72:75]\n"
"v_add_f32 v60,v60,v107\n"
CV "v105,v106,v107\n"
MF "a[68:71],v[28:31],v[80:83],a[68:71]\n"
"v_exp_f32 v108,v108\n"
MF "a[64:67],v[28:31],v[76:79],a[64:67]\n"
"v_exp_f32 v109,v109\n"
MF "a[60:63],v[28:31],v[72:75],a[60:63]\n"
"s_waitcnt lgkmcnt(4)\n"
TR "v[28:29],v5 offset:1024\n"
TR "v[30:31],v5 offset:7168\n"
MF "a[80:83],v[16:19],v[72:75],a[80:83]\n"
"v_exp_f32 v110,v110\n"
MF "a[84:87],v[16:19],v[76:79],a[84:87]\n"
"v_exp_f32 v111,v111\n"
MF "a[88:91],v[16:19],v[80:83],a[88:91]\n"
"v_add_f32 v61,v56,v108\n"
"v_add_f32 v61,v61,v109\n"
MF "a[92:95],v[16:19],v[84:87],a[92:95]\n"
CV "v108,v108,v109\n"
"v_add_f32 v61,v61,v110\n"
MF "a[96:99],v[16:19],v[88:91],a[96:99]\n"
"s_waitcnt lgkmcnt(4)\n"
TR "v[16:17],v2 offset:2048\n"
TR "v[18:19],v2 offset:8192\n"
MF "a[116:119],v[20:23],v[88:91],a[116:119]\n"
"v_add_f32 v61,v61,v111\n"
CV "v109,v110,v111\n"
MF "a[112:115],v[20:23],v[84:87],a[112:115]\n"
"v_exp_f32 v112,v112\n"
MF "a[108:111],v[20:23],v[80:83],a[108:111]\n"
"v_exp_f32 v113,v113\n"
MF "a[104:107],v[20:23],v[76:79],a[104:107]\n"
"v_exp_f32 v114,v114\n"
MF "a[100:103],v[20:23],v[72:75],a[100:103]\n"
"s_waitcnt lgkmcnt(4)\n"
TR "v[20:21],v3 offset:2048\n"
TR "v[22:23],v3 offset:8192\n"
MF "a[120:123],v[24:27],v[72:75],a[120:123]\n"
"v_exp_f32 v115,v115\n"
MF "a[124:127],v[24:27],v[76:79],a[124:127]\n"
"v_add_f32 v57,v57,v112\n"
"v_add_f32 v57,v57,v113\n"
MF "a[128:131],v[24:27],v[80:83],a[128:131]\n"
CV "v94,v112,v113\n"
"v_add_f32 v57,v57,v114\n"
MF "a[132:135],v[24:27],v[84:87],a[132:135]\n"
"v_add_f32 v57,v57,v115\n"
CV "v95,v114,v115\n"
MF "a[136:139],v[24:27],v[88:91],a[136:139]\n"
"s_waitcnt lgkmcnt(4)\n"
TR "v[24:25],v4 offset:2048\n"
TR "v[26:27],v4 offset:8192\n"
MF "a[156:159],v[28:31],v[88:91],a[156:159]\n"
"v_exp_f32 v116,v116\n"
MF "a[152:155],v[28:31],v[84:87],a[152:155]\n"
"v_exp_f32 v117,v117\n"
MF "a[148:151],v[28:31],v[80:83],a[148:151]\n"
"v_exp_f32 v118,v118\n"
MF "a[144:147],v[28:31],v[76:79],a[144:147]\n"
"v_exp_f32 v119,v119\n"
MF "a[140:143],v[28:31],v[72:75],a[140:143]\n"
"s_waitcnt lgkmcnt(4)\n"
TR "v[28:29],v5 offset:2048\n"
TR "v[30:31],v5 offset:8192\n"
MF "a[160:163],v[16:19],v[72:75],a[160:163]\n"
"v_add_f32 v58,v58,v116\n"
"v_add_f32 v58,v58,v117\n"
MF "a[164:167],v[16:19],v[76:79],a[164:167]\n"
CV "v98,v116,v117\n"
"v_add_f32 v58,v58,v118\n"
MF "a[168:171],v[16:19],v[80:83],a[168:171]\n"
"v_add_f32 v58,v58,v119\n"
CV "v99,v118,v119\n"
MF "a[172:175],v[16:19],v[84:87],a[172:175]\n"
"v_exp_f32 v120,v120\n"
MF "a[176:179],v[16:19],v[88:91],a[176:179]\n"
"s_waitcnt lgkmcnt(4)\n"
"v_exp_f32 v121,v121\n"
MF "a[196:199],v[20:23],v[88:91],a[196:199]\n"
"v_exp_f32 v122,v122\n"
MF "a[192:195],v[20:23],v[84:87],a[192:195]\n"
"v_exp_f32 v123,v123\n"
MF "a[188:191],v[20:23],v[80:83],a[188:191]\n"
"v_add_f32 v59,v59,v120\n"
"v_add_f32 v59,v59,v121\n"
MF "a[184:187],v[20:23],v[76:79],a[184:187]\n"
"s_waitcnt vmcnt(12) lgkmcnt(0)\n"
"s_barrier\n"
MF "a[180:183],v[20:23],v[72:75],a[180:183]\n"
"s_add_u32 s73,s73,12288\n"
"ds_read_b128 v[16:19],v0\n"
"ds_read_b128 v[20:23],v1\n"
MF "a[200:203],v[24:27],v[72:75],a[200:203]\n"
"s_cmp_eq_u32 s73,s19\n"
"s_cselect_b32 s73,s18,s73\n"
"s_add_u32 m0,s65,49152\n"
MF "a[204:207],v[24:27],v[76:79],a[204:207]\n"
LD "v8,s[36:39],s73 offen nt lds\n"
MF "a[208:211],v[24:27],v[80:83],a[208:211]\n"
"s_add_u32 m0,s65,50048\n"
CV "v102,v120,v121\n"
"v_add_f32 v59,v59,v122\n"
MF "a[212:215],v[24:27],v[84:87],a[212:215]\n"
LD "v8,s[36:39],s73 offen offset:128 nt lds\n"
MF "a[216:219],v[24:27],v[88:91],a[216:219]\n"
"s_add_u32 m0,s65,50944\n"
"ds_read_b128 v[24:27],v0 offset:1024\n"
"v_add_f32 v59,v59,v123\n"
MF "a[236:239],v[28:31],v[88:91],a[236:239]\n"
LD "v8,s[36:39],s73 offen offset:256 nt lds\n"
MF "a[232:235],v[28:31],v[84:87],a[232:235]\n"
CV "v103,v122,v123\n"
MF "a[228:231],v[28:31],v[80:83],a[228:231]\n"
"s_waitcnt lgkmcnt(1)\n"
MF "a[224:227],v[28:31],v[76:79],a[224:227]\n"
"s_cmp_lg_u32 s61,0\n"
MF "a[220:223],v[28:31],v[72:75],a[220:223]\n"
"s_cbranch_scc1 .Laloopx\n"
"v_exp_f32 v124,v124\n"
"v_exp_f32 v125,v125\n"
"v_exp_f32 v126,v126\n"
"v_exp_f32 v127,v127\n"
"v_exp_f32 v128,v128\n"
"v_exp_f32 v129,v129\n"
"v_exp_f32 v130,v130\n"
"v_exp_f32 v131,v131\n"
"v_add_f32 v60,v60,v124\n"
"v_add_f32 v61,v61,v128\n"
"v_add_f32 v60,v60,v125\n"
"v_add_f32 v61,v61,v129\n"
"v_add_f32 v60,v60,v126\n"
"v_add_f32 v61,v61,v130\n"
"v_add_f32 v60,v60,v127\n"
"v_add_f32 v61,v61,v131\n"
CV "v106,v124,v125\n"
CV "v107,v126,v127\n"
CV "v110,v128,v129\n"
CV "v111,v130,v131\n"
"v_max3_f32 v10,v57,v58,v59\n"
"v_max3_f32 v10,v10,v60,v61\n"
"v_cmp_le_f32 vcc,0x47800000,v10\n"
"s_cbranch_vccnz .Lstub_8\n"
".Lback_8:\n"
"s_waitcnt vmcnt(9) lgkmcnt(0)\n"
"s_barrier\n"
"s_cmp_eq_u32 s57,0\n"
"s_cbranch_scc0 .Lep\n"
"s_nop 0\n"
"s_add_u32 s66,s67,4718592\n"
"s_sub_u32 s81,s81,384\n"
"s_mov_b32 s83,7077888\n"
AR "v122,a243\n"
"v_add_u32 v123,97280,v122\n"
"v_add_u32 v124,194560,v122\n"
"v_add_u32 v125,291840,v122\n"
TR "v[136:137],v2 offset:24576\n"
TR "v[138:139],v2 offset:30720\n"
TR "v[140:141],v3 offset:24576\n"
TR "v[142:143],v3 offset:30720\n"
TR "v[144:145],v4 offset:24576\n"
TR "v[146:147],v4 offset:30720\n"
TR "v[148:149],v5 offset:24576\n"
TR "v[150:151],v5 offset:30720\n"
TR "v[152:153],v2 offset:25600\n"
TR "v[154:155],v2 offset:31744\n"
TR "v[156:157],v3 offset:25600\n"
TR "v[158:159],v3 offset:31744\n"
TR "v[160:161],v4 offset:25600\n"
TR "v[162:163],v4 offset:31744\n"
TR "v[164:165],v5 offset:25600\n"
TR "v[166:167],v5 offset:31744\n"
TR "v[168:169],v2 offset:26624\n"
TR "v[170:171],v2 offset:32768\n"
TR "v[172:173],v3 offset:26624\n"
TR "v[174:175],v3 offset:32768\n"
TR "v[176:177],v4 offset:26624\n"
TR "v[178:179],v4 offset:32768\n"
TR "v[180:181],v5 offset:26624\n"
TR "v[182:183],v5 offset:32768\n"
"v_mov_b32 v52,v57\n"
"v_mov_b32 v53,v58\n"
"v_mov_b32 v54,v59\n"
"v_mov_b32 v55,v60\n"
"v_mov_b32 v56,v61\n"
"s_add_u32 m0,s62,0\n"
"s_add_u32 s82,s66,2359296\n"
"s_sub_u32 s15,s82,7864320\n"
"s_min_u32 s82,s82,s15\n"
LD "v122,s[28:31],s82 offen lds\n"
LD "v123,s[28:31],s82 offen offset:1024 lds\n"
"v_permlane16_swap_b32 v57,v52\n"
"v_permlane16_swap_b32 v58,v53\n"
"v_permlane16_swap_b32 v59,v54\n"
"v_permlane16_swap_b32 v60,v55\n"
"v_permlane16_swap_b32 v61,v56\n"
"v_add_f32 v57,v57,v52\n"
"v_add_f32 v58,v58,v53\n"
"v_add_f32 v59,v59,v54\n"
"v_add_f32 v60,v60,v55\n"
"v_add_f32 v61,v61,v56\n"
"v_mov_b32 v52,v57\n"
"v_mov_b32 v53,v58\n"
"v_mov_b32 v54,v59\n"
"v_mov_b32 v55,v60\n"
"v_mov_b32 v56,v61\n"
LD "v124,s[28:31],s82 offen offset:2048 lds\n"
LD "v125,s[28:31],s82 offen offset:3072 lds\n"
"v_permlane32_swap_b32 v57,v52\n"
"v_permlane32_swap_b32 v58,v53\n"
"v_permlane32_swap_b32 v59,v54\n"
"v_permlane32_swap_b32 v60,v55\n"
"v_permlane32_swap_b32 v61,v56\n"
"v_add_f32 v57,v57,v52\n"
"v_add_f32 v58,v58,v53\n"
"v_add_f32 v59,v59,v54\n"
"v_add_f32 v60,v60,v55\n"
"v_add_f32 v61,v61,v56\n"
"s_waitcnt lgkmcnt(15)\n"
MF "a[0:3],v[136:139],v[92:95],a[0:3]\n"
MF "a[20:23],v[140:143],v[92:95],a[20:23]\n"
"v_rcp_f32 v57,v57\n"
"v_rcp_f32 v58,v58\n"
"v_rcp_f32 v59,v59\n"
"v_rcp_f32 v60,v60\n"
"v_rcp_f32 v61,v61\n"
MF "a[4:7],v[136:139],v[96:99],a[4:7]\n"
"v_mul_f32_e64 v52,-s13,v57\n"
"v_mul_f32_e64 v53,-s13,v58\n"
"v_mul_f32_e64 v54,-s13,v59\n"
"v_mul_f32_e64 v55,-s13,v60\n"
"v_mul_f32_e64 v56,-s13,v61\n"
AR "v72,a0\n"
AR "v73,a1\n"
AR "v74,a2\n"
MF "a[24:27],v[140:143],v[96:99],a[24:27]\n"
AR "v75,a3\n"
AR "v76,a20\n"
AR "v77,a21\n"
AR "v78,a22\n"
AR "v79,a23\n"
"v_pk_fma_f32 v[72:73],v[72:73],v[56:57],v[52:53] op_sel:[0,1,0] op_sel_hi:[1,1,0]\n"
"v_pk_fma_f32 v[74:75],v[74:75],v[56:57],v[52:53] op_sel:[0,1,0] op_sel_hi:[1,1,0]\n"
"v_pk_fma_f32 v[76:77],v[76:77],v[56:57],v[52:53] op_sel:[0,1,0] op_sel_hi:[1,1,0]\n"
"v_pk_fma_f32 v[78:79],v[78:79],v[56:57],v[52:53] op_sel:[0,1,0] op_sel_hi:[1,1,0]\n"
CV "v112,v72,v73\n"
CV "v113,v74,v75\n"
CV "v114,v76,v77\n"
CV "v115,v78,v79\n"
MF "a[8:11],v[136:139],v[100:103],a[8:11]\n"
AW "a0,v112\n"
AW "a1,v113\n"
AW "a2,v114\n"
AW "a3,v115\n"
"s_add_u32 m0,s62,16384\n"
"s_add_u32 s82,s66,2359424\n"
"s_sub_u32 s15,s82,7864320\n"
"s_min_u32 s82,s82,s15\n"
"s_nop 0\n"
LD "v122,s[28:31],s82 offen lds\n"
AR "v80,a4\n"
AR "v81,a5\n"
AR "v82,a6\n"
MF "a[28:31],v[140:143],v[100:103],a[28:31]\n"
AR "v83,a7\n"
AR "v84,a24\n"
AR "v85,a25\n"
AR "v86,a26\n"
AR "v87,a27\n"
"v_pk_fma_f32 v[80:81],v[80:81],v[58:59],v[52:53] op_sel:[0,0,1] op_sel_hi:[1,0,1]\n"
"v_pk_fma_f32 v[82:83],v[82:83],v[58:59],v[52:53] op_sel:[0,0,1] op_sel_hi:[1,0,1]\n"
"v_pk_fma_f32 v[84:85],v[84:85],v[58:59],v[52:53] op_sel:[0,0,1] op_sel_hi:[1,0,1]\n"
"v_pk_fma_f32 v[86:87],v[86:87],v[58:59],v[52:53] op_sel:[0,0,1] op_sel_hi:[1,0,1]\n"
CV "v116,v80,v81\n"
CV "v117,v82,v83\n"
CV "v118,v84,v85\n"
CV "v119,v86,v87\n"
MF "a[12:15],v[136:139],v[104:107],a[12:15]\n"
AW "a4,v116\n"
AW "a5,v117\n"
AW "a6,v118\n"
AW "a7,v119\n"
LD "v123,s[28:31],s82 offen offset:1024 lds\n"
AR "v72,a8\n"
AR "v73,a9\n"
AR "v74,a10\n"
MF "a[32:35],v[140:143],v[104:107],a[32:35]\n"
AR "v75,a11\n"
AR "v76,a28\n"
AR "v77,a29\n"
AR "v78,a30\n"
AR "v79,a31\n"
"v_pk_fma_f32 v[72:73],v[72:73],v[58:59],v[54:55] op_sel:[0,1,0] op_sel_hi:[1,1,0]\n"
"v_pk_fma_f32 v[74:75],v[74:75],v[58:59],v[54:55] op_sel:[0,1,0] op_sel_hi:[1,1,0]\n"
"v_pk_fma_f32 v[76:77],v[76:77],v[58:59],v[54:55] op_sel:[0,1,0] op_sel_hi:[1,1,0]\n"
"v_pk_fma_f32 v[78:79],v[78:79],v[58:59],v[54:55] op_sel:[0,1,0] op_sel_hi:[1,1,0]\n"
CV "v112,v72,v73\n"
CV "v113,v74,v75\n"
CV "v114,v76,v77\n"
CV "v115,v78,v79\n"
MF "a[16:19],v[136:139],v[108:111],a[16:19]\n"
AW "a8,v112\n"
AW "a9,v113\n"
AW "a10,v114\n"
AW "a11,v115\n"
"s_waitcnt lgkmcnt(0)\n"
"s_barrier\n"
AR "v80,a12\n"
AR "v81,a13\n"
AR "v82,a14\n"
MF "a[36:39],v[140:143],v[108:111],a[36:39]\n"
AR "v83,a15\n"
AR "v84,a32\n"
AR "v85,a33\n"
AR "v86,a34\n"
AR "v87,a35\n"
"v_pk_fma_f32 v[80:81],v[80:81],v[60:61],v[54:55] op_sel:[0,0,1] op_sel_hi:[1,0,1]\n"
"v_pk_fma_f32 v[82:83],v[82:83],v[60:61],v[54:55] op_sel:[0,0,1] op_sel_hi:[1,0,1]\n"
"v_pk_fma_f32 v[84:85],v[84:85],v[60:61],v[54:55] op_sel:[0,0,1] op_sel_hi:[1,0,1]\n"
"v_pk_fma_f32 v[86:87],v[86:87],v[60:61],v[54:55] op_sel:[0,0,1] op_sel_hi:[1,0,1]\n"
CV "v116,v80,v81\n"
CV "v117,v82,v83\n"
CV "v118,v84,v85\n"
CV "v119,v86,v87\n"
MF "a[40:43],v[144:147],v[92:95],a[40:43]\n"
AW "a12,v116\n"
AW "a13,v117\n"
AW "a14,v118\n"
AW "a15,v119\n"
LD "v124,s[28:31],s82 offen offset:2048 lds\n"
AR "v72,a16\n"
AR "v73,a17\n"
AR "v74,a18\n"
MF "a[60:63],v[148:151],v[92:95],a[60:63]\n"
AR "v75,a19\n"
AR "v76,a36\n"
AR "v77,a37\n"
AR "v78,a38\n"
AR "v79,a39\n"
"v_pk_fma_f32 v[72:73],v[72:73],v[60:61],v[56:57] op_sel:[0,1,0] op_sel_hi:[1,1,0]\n"
"v_pk_fma_f32 v[74:75],v[74:75],v[60:61],v[56:57] op_sel:[0,1,0] op_sel_hi:[1,1,0]\n"
"v_pk_fma_f32 v[76:77],v[76:77],v[60:61],v[56:57] op_sel:[0,1,0] op_sel_hi:[1,1,0]\n"
"v_pk_fma_f32 v[78:79],v[78:79],v[60:61],v[56:57] op_sel:[0,1,0] op_sel_hi:[1,1,0]\n"
CV "v112,v72,v73\n"
CV "v113,v74,v75\n"
CV "v114,v76,v77\n"
CV "v115,v78,v79\n"
MF "a[44:47],v[144:147],v[96:99],a[44:47]\n"
AW "a16,v112\n"
AW "a17,v113\n"
AW "a18,v114\n"
AW "a19,v115\n"
LD "v125,s[28:31],s82 offen offset:3072 lds\n"
AR "v80,a40\n"
AR "v81,a41\n"
AR "v82,a42\n"
MF "a[64:67],v[148:151],v[96:99],a[64:67]\n"
AR "v83,a43\n"
AR "v84,a60\n"
AR "v85,a61\n"
AR "v86,a62\n"
AR "v87,a63\n"
"v_pk_fma_f32 v[80:81],v[80:81],v[56:57],v[52:53] op_sel:[0,1,0] op_sel_hi:[1,1,0]\n"
"v_pk_fma_f32 v[82:83],v[82:83],v[56:57],v[52:53] op_sel:[0,1,0] op_sel_hi:[1,1,0]\n"
"v_pk_fma_f32 v[84:85],v[84:85],v[56:57],v[52:53] op_sel:[0,1,0] op_sel_hi:[1,1,0]\n"
"v_pk_fma_f32 v[86:87],v[86:87],v[56:57],v[52:53] op_sel:[0,1,0] op_sel_hi:[1,1,0]\n"
CV "v116,v80,v81\n"
CV "v117,v82,v83\n"
CV "v118,v84,v85\n"
CV "v119,v86,v87\n"
MF "a[48:51],v[144:147],v[100:103],a[48:51]\n"
AW "a40,v116\n"
AW "a41,v117\n"
AW "a42,v118\n"
AW "a43,v119\n"
AR "v72,a44\n"
AR "v73,a45\n"
AR "v74,a46\n"
MF "a[68:71],v[148:151],v[100:103],a[68:71]\n"
AR "v75,a47\n"
AR "v76,a64\n"
AR "v77,a65\n"
AR "v78,a66\n"
AR "v79,a67\n"
"v_pk_fma_f32 v[72:73],v[72:73],v[58:59],v[52:53] op_sel:[0,0,1] op_sel_hi:[1,0,1]\n"
"v_pk_fma_f32 v[74:75],v[74:75],v[58:59],v[52:53] op_sel:[0,0,1] op_sel_hi:[1,0,1]\n"
"v_pk_fma_f32 v[76:77],v[76:77],v[58:59],v[52:53] op_sel:[0,0,1] op_sel_hi:[1,0,1]\n"
"v_pk_fma_f32 v[78:79],v[78:79],v[58:59],v[52:53] op_sel:[0,0,1] op_sel_hi:[1,0,1]\n"
CV "v112,v72,v73\n"
CV "v113,v74,v75\n"
CV "v114,v76,v77\n"
CV "v115,v78,v79\n"
MF "a[52:55],v[144:147],v[104:107],a[52:55]\n"
AW "a44,v112\n"
AW "a45,v113\n"
AW "a46,v114\n"
AW "a47,v115\n"
"s_add_u32 m0,s62,32768\n"
"s_add_u32 s82,s66,2359552\n"
"s_sub_u32 s15,s82,7864320\n"
"s_min_u32 s82,s82,s15\n"
"s_nop 0\n"
LD "v122,s[28:31],s82 offen lds\n"
AR "v80,a48\n"
AR "v81,a49\n"
AR "v82,a50\n"
MF "a[72:75],v[148:151],v[104:107],a[72:75]\n"
AR "v83,a51\n"
AR "v84,a68\n"
AR "v85,a69\n"
AR "v86,a70\n"
AR "v87,a71\n"
"v_pk_fma_f32 v[80:81],v[80:81],v[58:59],v[54:55] op_sel:[0,1,0] op_sel_hi:[1,1,0]\n"
"v_pk_fma_f32 v[82:83],v[82:83],v[58:59],v[54:55] op_sel:[0,1,0] op_sel_hi:[1,1,0]\n"
"v_pk_fma_f32 v[84:85],v[84:85],v[58:59],v[54:55] op_sel:[0,1,0] op_sel_hi:[1,1,0]\n"
"v_pk_fma_f32 v[86:87],v[86:87],v[58:59],v[54:55] op_sel:[0,1,0] op_sel_hi:[1,1,0]\n"
CV "v116,v80,v81\n"
CV "v117,v82,v83\n"
CV "v118,v84,v85\n"
CV "v119,v86,v87\n"
MF "a[56:59],v[144:147],v[108:111],a[56:59]\n"
AW "a48,v116\n"
AW "a49,v117\n"
AW "a50,v118\n"
AW "a51,v119\n"
LD "v123,s[28:31],s82 offen offset:1024 lds\n"
AR "v72,a52\n"
AR "v73,a53\n"
AR "v74,a54\n"
MF "a[76:79],v[148:151],v[108:111],a[76:79]\n"
AR "v75,a55\n"
AR "v76,a72\n"
AR "v77,a73\n"
AR "v78,a74\n"
AR "v79,a75\n"
"v_pk_fma_f32 v[72:73],v[72:73],v[60:61],v[54:55] op_sel:[0,0,1] op_sel_hi:[1,0,1]\n"
"v_pk_fma_f32 v[74:75],v[74:75],v[60:61],v[54:55] op_sel:[0,0,1] op_sel_hi:[1,0,1]\n"
"v_pk_fma_f32 v[76:77],v[76:77],v[60:61],v[54:55] op_sel:[0,0,1] op_sel_hi:[1,0,1]\n"
"v_pk_fma_f32 v[78:79],v[78:79],v[60:61],v[54:55] op_sel:[0,0,1] op_sel_hi:[1,0,1]\n"
CV "v112,v72,v73\n"
CV "v113,v74,v75\n"
CV "v114,v76,v77\n"
CV "v115,v78,v79\n"
MF "a[80:83],v[152:155],v[92:95],a[80:83]\n"
AW "a52,v112\n"
AW "a53,v113\n"
AW "a54,v114\n"
AW "a55,v115\n"
AR "v80,a56\n"
AR "v81,a57\n"
AR "v82,a58\n"
MF "a[100:103],v[156:159],v[92:95],a[100:103]\n"
AR "v83,a59\n"
AR "v84,a76\n"
AR "v85,a77\n"
AR "v86,a78\n"
AR "v87,a79\n"
"v_pk_fma_f32 v[80:81],v[80:81],v[60:61],v[56:57] op_sel:[0,1,0] op_sel_hi:[1,1,0]\n"
"v_pk_fma_f32 v[82:83],v[82:83],v[60:61],v[56:57] op_sel:[0,1,0] op_sel_hi:[1,1,0]\n"
"v_pk_fma_f32 v[84:85],v[84:85],v[60:61],v[56:57] op_sel:[0,1,0] op_sel_hi:[1,1,0]\n"
"v_pk_fma_f32 v[86:87],v[86:87],v[60:61],v[56:57] op_sel:[0,1,0] op_sel_hi:[1,1,0]\n"
CV "v116,v80,v81\n"
CV "v117,v82,v83\n"
CV "v118,v84,v85\n"
CV "v119,v86,v87\n"
MF "a[84:87],v[152:155],v[96:99],a[84:87]\n"
AW "a56,v116\n"
AW "a57,v117\n"
AW "a58,v118\n"
AW "a59,v119\n"
LD "v124,s[28:31],s82 offen offset:2048 lds\n"
AR "v72,a80\n"
AR "v73,a81\n"
AR "v74,a82\n"
MF "a[104:107],v[156:159],v[96:99],a[104:107]\n"
AR "v75,a83\n"
AR "v76,a100\n"
AR "v77,a101\n"
AR "v78,a102\n"
AR "v79,a103\n"
"v_pk_fma_f32 v[72:73],v[72:73],v[56:57],v[52:53] op_sel:[0,1,0] op_sel_hi:[1,1,0]\n"
"v_pk_fma_f32 v[74:75],v[74:75],v[56:57],v[52:53] op_sel:[0,1,0] op_sel_hi:[1,1,0]\n"
"v_pk_fma_f32 v[76:77],v[76:77],v[56:57],v[52:53] op_sel:[0,1,0] op_sel_hi:[1,1,0]\n"
"v_pk_fma_f32 v[78:79],v[78:79],v[56:57],v[52:53] op_sel:[0,1,0] op_sel_hi:[1,1,0]\n"
CV "v112,v72,v73\n"
CV "v113,v74,v75\n"
CV "v114,v76,v77\n"
CV "v115,v78,v79\n"
MF "a[88:91],v[152:155],v[100:103],a[88:91]\n"
AW "a80,v112\n"
AW "a81,v113\n"
AW "a82,v114\n"
AW "a83,v115\n"
LD "v125,s[28:31],s82 offen offset:3072 lds\n"
AR "v80,a84\n"
AR "v81,a85\n"
AR "v82,a86\n"
MF "a[108:111],v[156:159],v[100:103],a[108:111]\n"
AR "v83,a87\n"
AR "v84,a104\n"
AR "v85,a105\n"
AR "v86,a106\n"
AR "v87,a107\n"
"v_pk_fma_f32 v[80:81],v[80:81],v[58:59],v[52:53] op_sel:[0,0,1] op_sel_hi:[1,0,1]\n"
"v_pk_fma_f32 v[82:83],v[82:83],v[58:59],v[52:53] op_sel:[0,0,1] op_sel_hi:[1,0,1]\n"
"v_pk_fma_f32 v[84:85],v[84:85],v[58:59],v[52:53] op_sel:[0,0,1] op_sel_hi:[1,0,1]\n"
"v_pk_fma_f32 v[86:87],v[86:87],v[58:59],v[52:53] op_sel:[0,0,1] op_sel_hi:[1,0,1]\n"
CV "v116,v80,v81\n"
CV "v117,v82,v83\n"
CV "v118,v84,v85\n"
CV "v119,v86,v87\n"
MF "a[92:95],v[152:155],v[104:107],a[92:95]\n"
AW "a84,v116\n"
AW "a85,v117\n"
AW "a86,v118\n"
AW "a87,v119\n"
AR "v72,a88\n"
AR "v73,a89\n"
AR "v74,a90\n"
MF "a[112:115],v[156:159],v[104:107],a[112:115]\n"
AR "v75,a91\n"
AR "v76,a108\n"
AR "v77,a109\n"
AR "v78,a110\n"
AR "v79,a111\n"
"v_pk_fma_f32 v[72:73],v[72:73],v[58:59],v[54:55] op_sel:[0,1,0] op_sel_hi:[1,1,0]\n"
"v_pk_fma_f32 v[74:75],v[74:75],v[58:59],v[54:55] op_sel:[0,1,0] op_sel_hi:[1,1,0]\n"
"v_pk_fma_f32 v[76:77],v[76:77],v[58:59],v[54:55] op_sel:[0,1,0] op_sel_hi:[1,1,0]\n"
"v_pk_fma_f32 v[78:79],v[78:79],v[58:59],v[54:55] op_sel:[0,1,0] op_sel_hi:[1,1,0]\n"
CV "v112,v72,v73\n"
CV "v113,v74,v75\n"
CV "v114,v76,v77\n"
CV "v115,v78,v79\n"
MF "a[96:99],v[152:155],v[108:111],a[96:99]\n"
AW "a88,v112\n"
AW "a89,v113\n"
AW "a90,v114\n"
AW "a91,v115\n"
AR "v80,a92\n"
AR "v81,a93\n"
AR "v82,a94\n"
MF "a[116:119],v[156:159],v[108:111],a[116:119]\n"
AR "v83,a95\n"
AR "v84,a112\n"
AR "v85,a113\n"
AR "v86,a114\n"
AR "v87,a115\n"
"v_pk_fma_f32 v[80:81],v[80:81],v[60:61],v[54:55] op_sel:[0,0,1] op_sel_hi:[1,0,1]\n"
"v_pk_fma_f32 v[82:83],v[82:83],v[60:61],v[54:55] op_sel:[0,0,1] op_sel_hi:[1,0,1]\n"
"v_pk_fma_f32 v[84:85],v[84:85],v[60:61],v[54:55] op_sel:[0,0,1] op_sel_hi:[1,0,1]\n"
"v_pk_fma_f32 v[86:87],v[86:87],v[60:61],v[54:55] op_sel:[0,0,1] op_sel_hi:[1,0,1]\n"
CV "v116,v80,v81\n"
CV "v117,v82,v83\n"
CV "v118,v84,v85\n"
CV "v119,v86,v87\n"
MF "a[120:123],v[160:163],v[92:95],a[120:123]\n"
AW "a92,v116\n"
AW "a93,v117\n"
AW "a94,v118\n"
AW "a95,v119\n"
AR "v72,a96\n"
AR "v73,a97\n"
AR "v74,a98\n"
MF "a[140:143],v[164:167],v[92:95],a[140:143]\n"
AR "v75,a99\n"
AR "v76,a116\n"
AR "v77,a117\n"
AR "v78,a118\n"
AR "v79,a119\n"
"v_pk_fma_f32 v[72:73],v[72:73],v[60:61],v[56:57] op_sel:[0,1,0] op_sel_hi:[1,1,0]\n"
"v_pk_fma_f32 v[74:75],v[74:75],v[60:61],v[56:57] op_sel:[0,1,0] op_sel_hi:[1,1,0]\n"
"v_pk_fma_f32 v[76:77],v[76:77],v[60:61],v[56:57] op_sel:[0,1,0] op_sel_hi:[1,1,0]\n"
"v_pk_fma_f32 v[78:79],v[78:79],v[60:61],v[56:57] op_sel:[0,1,0] op_sel_hi:[1,1,0]\n"
CV "v112,v72,v73\n"
CV "v113,v74,v75\n"
CV "v114,v76,v77\n"
CV "v115,v78,v79\n"
MF "a[124:127],v[160:163],v[96:99],a[124:127]\n"
AW "a96,v112\n"
AW "a97,v113\n"
AW "a98,v114\n"
AW "a99,v115\n"
AR "v80,a120\n"
AR "v81,a121\n"
AR "v82,a122\n"
MF "a[144:147],v[164:167],v[96:99],a[144:147]\n"
AR "v83,a123\n"
AR "v84,a140\n"
AR "v85,a141\n"
AR "v86,a142\n"
AR "v87,a143\n"
"v_pk_fma_f32 v[80:81],v[80:81],v[56:57],v[52:53] op_sel:[0,1,0] op_sel_hi:[1,1,0]\n"
"v_pk_fma_f32 v[82:83],v[82:83],v[56:57],v[52:53] op_sel:[0,1,0] op_sel_hi:[1,1,0]\n"
"v_pk_fma_f32 v[84:85],v[84:85],v[56:57],v[52:53] op_sel:[0,1,0] op_sel_hi:[1,1,0]\n"
"v_pk_fma_f32 v[86:87],v[86:87],v[56:57],v[52:53] op_sel:[0,1,0] op_sel_hi:[1,1,0]\n"
CV "v116,v80,v81\n"
CV "v117,v82,v83\n"
CV "v118,v84,v85\n"
CV "v119,v86,v87\n"
MF "a[128:131],v[160:163],v[100:103],a[128:131]\n"
AW "a120,v116\n"
AW "a121,v117\n"
AW "a122,v118\n"
AW "a123,v119\n"
AR "v72,a124\n"
AR "v73,a125\n"
AR "v74,a126\n"
MF "a[148:151],v[164:167],v[100:103],a[148:151]\n"
AR "v75,a127\n"
AR "v76,a144\n"
AR "v77,a145\n"
AR "v78,a146\n"
AR "v79,a147\n"
"v_pk_fma_f32 v[72:73],v[72:73],v[58:59],v[52:53] op_sel:[0,0,1] op_sel_hi:[1,0,1]\n"
"v_pk_fma_f32 v[74:75],v[74:75],v[58:59],v[52:53] op_sel:[0,0,1] op_sel_hi:[1,0,1]\n"
"v_pk_fma_f32 v[76:77],v[76:77],v[58:59],v[52:53] op_sel:[0,0,1] op_sel_hi:[1,0,1]\n"
"v_pk_fma_f32 v[78:79],v[78:79],v[58:59],v[52:53] op_sel:[0,0,1] op_sel_hi:[1,0,1]\n"
CV "v112,v72,v73\n"
CV "v113,v74,v75\n"
CV "v114,v76,v77\n"
CV "v115,v78,v79\n"
MF "a[132:135],v[160:163],v[104:107],a[132:135]\n"
AW "a124,v112\n"
AW "a125,v113\n"
AW "a126,v114\n"
AW "a127,v115\n"
AR "v80,a128\n"
AR "v81,a129\n"
AR "v82,a130\n"
MF "a[152:155],v[164:167],v[104:107],a[152:155]\n"
AR "v83,a131\n"
AR "v84,a148\n"
AR "v85,a149\n"
AR "v86,a150\n"
AR "v87,a151\n"
"v_pk_fma_f32 v[80:81],v[80:81],v[58:59],v[54:55] op_sel:[0,1,0] op_sel_hi:[1,1,0]\n"
"v_pk_fma_f32 v[82:83],v[82:83],v[58:59],v[54:55] op_sel:[0,1,0] op_sel_hi:[1,1,0]\n"
"v_pk_fma_f32 v[84:85],v[84:85],v[58:59],v[54:55] op_sel:[0,1,0] op_sel_hi:[1,1,0]\n"
"v_pk_fma_f32 v[86:87],v[86:87],v[58:59],v[54:55] op_sel:[0,1,0] op_sel_hi:[1,1,0]\n"
CV "v116,v80,v81\n"
CV "v117,v82,v83\n"
CV "v118,v84,v85\n"
CV "v119,v86,v87\n"
MF "a[136:139],v[160:163],v[108:111],a[136:139]\n"
AW "a128,v116\n"
AW "a129,v117\n"
AW "a130,v118\n"
AW "a131,v119\n"
AR "v72,a132\n"
AR "v73,a133\n"
AR "v74,a134\n"
MF "a[156:159],v[164:167],v[108:111],a[156:159]\n"
AR "v75,a135\n"
AR "v76,a152\n"
AR "v77,a153\n"
AR "v78,a154\n"
AR "v79,a155\n"
"v_pk_fma_f32 v[72:73],v[72:73],v[60:61],v[54:55] op_sel:[0,0,1] op_sel_hi:[1,0,1]\n"
"v_pk_fma_f32 v[74:75],v[74:75],v[60:61],v[54:55] op_sel:[0,0,1] op_sel_hi:[1,0,1]\n"
"v_pk_fma_f32 v[76:77],v[76:77],v[60:61],v[54:55] op_sel:[0,0,1] op_sel_hi:[1,0,1]\n"
"v_pk_fma_f32 v[78:79],v[78:79],v[60:61],v[54:55] op_sel:[0,0,1] op_sel_hi:[1,0,1]\n"
CV "v112,v72,v73\n"
CV "v113,v74,v75\n"
CV "v114,v76,v77\n"
CV "v115,v78,v79\n"
MF "a[160:163],v[168:171],v[92:95],a[160:163]\n"
AW "a132,v112\n"
AW "a133,v113\n"
AW "a134,v114\n"
AW "a135,v115\n"
AR "v80,a136\n"
AR "v81,a137\n"
AR "v82,a138\n"
MF "a[180:183],v[172:175],v[92:95],a[180:183]\n"
AR "v83,a139\n"
AR "v84,a156\n"
AR "v85,a157\n"
AR "v86,a158\n"
AR "v87,a159\n"
"v_pk_fma_f32 v[80:81],v[80:81],v[60:61],v[56:57] op_sel:[0,1,0] op_sel_hi:[1,1,0]\n"
"v_pk_fma_f32 v[82:83],v[82:83],v[60:61],v[56:57] op_sel:[0,1,0] op_sel_hi:[1,1,0]\n"
"v_pk_fma_f32 v[84:85],v[84:85],v[60:61],v[56:57] op_sel:[0,1,0] op_sel_hi:[1,1,0]\n"
"v_pk_fma_f32 v[86:87],v[86:87],v[60:61],v[56:57] op_sel:[0,1,0] op_sel_hi:[1,1,0]\n"
CV "v116,v80,v81\n"
CV "v117,v82,v83\n"
CV "v118,v84,v85\n"
CV "v119,v86,v87\n"
MF "a[164:167],v[168:171],v[96:99],a[164:167]\n"
AW "a136,v116\n"
AW "a137,v117\n"
AW "a138,v118\n"
AW "a139,v119\n"
AR "v72,a160\n"
AR "v73,a161\n"
AR "v74,a162\n"
MF "a[184:187],v[172:175],v[96:99],a[184:187]\n"
AR "v75,a163\n"
AR "v76,a180\n"
AR "v77,a181\n"
AR "v78,a182\n"
AR "v79,a183\n"
"v_pk_fma_f32 v[72:73],v[72:73],v[56:57],v[52:53] op_sel:[0,1,0] op_sel_hi:[1,1,0]\n"
"v_pk_fma_f32 v[74:75],v[74:75],v[56:57],v[52:53] op_sel:[0,1,0] op_sel_hi:[1,1,0]\n"
"v_pk_fma_f32 v[76:77],v[76:77],v[56:57],v[52:53] op_sel:[0,1,0] op_sel_hi:[1,1,0]\n"
"v_pk_fma_f32 v[78:79],v[78:79],v[56:57],v[52:53] op_sel:[0,1,0] op_sel_hi:[1,1,0]\n"
CV "v112,v72,v73\n"
CV "v113,v74,v75\n"
CV "v114,v76,v77\n"
CV "v115,v78,v79\n"
MF "a[168:171],v[168:171],v[100:103],a[168:171]\n"
AW "a160,v112\n"
AW "a161,v113\n"
AW "a162,v114\n"
AW "a163,v115\n"
AR "v80,a164\n"
AR "v81,a165\n"
AR "v82,a166\n"
MF "a[188:191],v[172:175],v[100:103],a[188:191]\n"
AR "v83,a167\n"
AR "v84,a184\n"
AR "v85,a185\n"
AR "v86,a186\n"
AR "v87,a187\n"
"v_pk_fma_f32 v[80:81],v[80:81],v[58:59],v[52:53] op_sel:[0,0,1] op_sel_hi:[1,0,1]\n"
"v_pk_fma_f32 v[82:83],v[82:83],v[58:59],v[52:53] op_sel:[0,0,1] op_sel_hi:[1,0,1]\n"
"v_pk_fma_f32 v[84:85],v[84:85],v[58:59],v[52:53] op_sel:[0,0,1] op_sel_hi:[1,0,1]\n"
"v_pk_fma_f32 v[86:87],v[86:87],v[58:59],v[52:53] op_sel:[0,0,1] op_sel_hi:[1,0,1]\n"
CV "v116,v80,v81\n"
CV "v117,v82,v83\n"
CV "v118,v84,v85\n"
CV "v119,v86,v87\n"
MF "a[172:175],v[168:171],v[104:107],a[172:175]\n"
AW "a164,v116\n"
AW "a165,v117\n"
AW "a166,v118\n"
AW "a167,v119\n"
AR "v72,a168\n"
AR "v73,a169\n"
AR "v74,a170\n"
MF "a[192:195],v[172:175],v[104:107],a[192:195]\n"
AR "v75,a171\n"
AR "v76,a188\n"
AR "v77,a189\n"
AR "v78,a190\n"
AR "v79,a191\n"
"v_pk_fma_f32 v[72:73],v[72:73],v[58:59],v[54:55] op_sel:[0,1,0] op_sel_hi:[1,1,0]\n"
"v_pk_fma_f32 v[74:75],v[74:75],v[58:59],v[54:55] op_sel:[0,1,0] op_sel_hi:[1,1,0]\n"
"v_pk_fma_f32 v[76:77],v[76:77],v[58:59],v[54:55] op_sel:[0,1,0] op_sel_hi:[1,1,0]\n"
"v_pk_fma_f32 v[78:79],v[78:79],v[58:59],v[54:55] op_sel:[0,1,0] op_sel_hi:[1,1,0]\n"
CV "v112,v72,v73\n"
CV "v113,v74,v75\n"
CV "v114,v76,v77\n"
CV "v115,v78,v79\n"
MF "a[176:179],v[168:171],v[108:111],a[176:179]\n"
AW "a168,v112\n"
AW "a169,v113\n"
AW "a170,v114\n"
AW "a171,v115\n"
AR "v80,a172\n"
AR "v81,a173\n"
AR "v82,a174\n"
MF "a[196:199],v[172:175],v[108:111],a[196:199]\n"
AR "v83,a175\n"
AR "v84,a192\n"
AR "v85,a193\n"
AR "v86,a194\n"
AR "v87,a195\n"
"v_pk_fma_f32 v[80:81],v[80:81],v[60:61],v[54:55] op_sel:[0,0,1] op_sel_hi:[1,0,1]\n"
"v_pk_fma_f32 v[82:83],v[82:83],v[60:61],v[54:55] op_sel:[0,0,1] op_sel_hi:[1,0,1]\n"
"v_pk_fma_f32 v[84:85],v[84:85],v[60:61],v[54:55] op_sel:[0,0,1] op_sel_hi:[1,0,1]\n"
"v_pk_fma_f32 v[86:87],v[86:87],v[60:61],v[54:55] op_sel:[0,0,1] op_sel_hi:[1,0,1]\n"
CV "v116,v80,v81\n"
CV "v117,v82,v83\n"
CV "v118,v84,v85\n"
CV "v119,v86,v87\n"
MF "a[200:203],v[176:179],v[92:95],a[200:203]\n"
AW "a172,v116\n"
AW "a173,v117\n"
AW "a174,v118\n"
AW "a175,v119\n"
AR "v72,a176\n"
AR "v73,a177\n"
AR "v74,a178\n"
MF "a[220:223],v[180:183],v[92:95],a[220:223]\n"
AR "v75,a179\n"
AR "v76,a196\n"
AR "v77,a197\n"
AR "v78,a198\n"
AR "v79,a199\n"
"v_pk_fma_f32 v[72:73],v[72:73],v[60:61],v[56:57] op_sel:[0,1,0] op_sel_hi:[1,1,0]\n"
"v_pk_fma_f32 v[74:75],v[74:75],v[60:61],v[56:57] op_sel:[0,1,0] op_sel_hi:[1,1,0]\n"
"v_pk_fma_f32 v[76:77],v[76:77],v[60:61],v[56:57] op_sel:[0,1,0] op_sel_hi:[1,1,0]\n"
"v_pk_fma_f32 v[78:79],v[78:79],v[60:61],v[56:57] op_sel:[0,1,0] op_sel_hi:[1,1,0]\n"
CV "v112,v72,v73\n"
CV "v113,v74,v75\n"
CV "v114,v76,v77\n"
CV "v115,v78,v79\n"
MF "a[204:207],v[176:179],v[96:99],a[204:207]\n"
AW "a176,v112\n"
AW "a177,v113\n"
AW "a178,v114\n"
AW "a179,v115\n"
AR "v80,a200\n"
AR "v81,a201\n"
AR "v82,a202\n"
MF "a[224:227],v[180:183],v[96:99],a[224:227]\n"
AR "v83,a203\n"
AR "v84,a220\n"
AR "v85,a221\n"
AR "v86,a222\n"
AR "v87,a223\n"
"v_pk_fma_f32 v[80:81],v[80:81],v[56:57],v[52:53] op_sel:[0,1,0] op_sel_hi:[1,1,0]\n"
"v_pk_fma_f32 v[82:83],v[82:83],v[56:57],v[52:53] op_sel:[0,1,0] op_sel_hi:[1,1,0]\n"
"v_pk_fma_f32 v[84:85],v[84:85],v[56:57],v[52:53] op_sel:[0,1,0] op_sel_hi:[1,1,0]\n"
"v_pk_fma_f32 v[86:87],v[86:87],v[56:57],v[52:53] op_sel:[0,1,0] op_sel_hi:[1,1,0]\n"
CV "v116,v80,v81\n"
CV "v117,v82,v83\n"
CV "v118,v84,v85\n"
CV "v119,v86,v87\n"
MF "a[208:211],v[176:179],v[100:103],a[208:211]\n"
AW "a20,v116\n"
AW "a21,v117\n"
AW "a22,v118\n"
AW "a23,v119\n"
AR "v72,a204\n"
AR "v73,a205\n"
AR "v74,a206\n"
MF "a[228:231],v[180:183],v[100:103],a[228:231]\n"
AR "v75,a207\n"
AR "v76,a224\n"
AR "v77,a225\n"
AR "v78,a226\n"
AR "v79,a227\n"
"v_pk_fma_f32 v[72:73],v[72:73],v[58:59],v[52:53] op_sel:[0,0,1] op_sel_hi:[1,0,1]\n"
"v_pk_fma_f32 v[74:75],v[74:75],v[58:59],v[52:53] op_sel:[0,0,1] op_sel_hi:[1,0,1]\n"
"v_pk_fma_f32 v[76:77],v[76:77],v[58:59],v[52:53] op_sel:[0,0,1] op_sel_hi:[1,0,1]\n"
"v_pk_fma_f32 v[78:79],v[78:79],v[58:59],v[52:53] op_sel:[0,0,1] op_sel_hi:[1,0,1]\n"
CV "v112,v72,v73\n"
CV "v113,v74,v75\n"
CV "v114,v76,v77\n"
CV "v115,v78,v79\n"
MF "a[212:215],v[176:179],v[104:107],a[212:215]\n"
AW "a24,v112\n"
AW "a25,v113\n"
AW "a26,v114\n"
AW "a27,v115\n"
AR "v80,a208\n"
AR "v81,a209\n"
AR "v82,a210\n"
MF "a[232:235],v[180:183],v[104:107],a[232:235]\n"
AR "v83,a211\n"
AR "v84,a228\n"
AR "v85,a229\n"
AR "v86,a230\n"
AR "v87,a231\n"
"v_pk_fma_f32 v[80:81],v[80:81],v[58:59],v[54:55] op_sel:[0,1,0] op_sel_hi:[1,1,0]\n"
"v_pk_fma_f32 v[82:83],v[82:83],v[58:59],v[54:55] op_sel:[0,1,0] op_sel_hi:[1,1,0]\n"
"v_pk_fma_f32 v[84:85],v[84:85],v[58:59],v[54:55] op_sel:[0,1,0] op_sel_hi:[1,1,0]\n"
"v_pk_fma_f32 v[86:87],v[86:87],v[58:59],v[54:55] op_sel:[0,1,0] op_sel_hi:[1,1,0]\n"
CV "v116,v80,v81\n"
CV "v117,v82,v83\n"
CV "v118,v84,v85\n"
CV "v119,v86,v87\n"
MF "a[216:219],v[176:179],v[108:111],a[216:219]\n"
AW "a28,v116\n"
AW "a29,v117\n"
AW "a30,v118\n"
AW "a31,v119\n"
AR "v72,a212\n"
AR "v73,a213\n"
AR "v74,a214\n"
MF "a[236:239],v[180:183],v[108:111],a[236:239]\n"
AR "v75,a215\n"
AR "v76,a232\n"
AR "v77,a233\n"
AR "v78,a234\n"
AR "v79,a235\n"
"v_pk_fma_f32 v[72:73],v[72:73],v[60:61],v[54:55] op_sel:[0,0,1] op_sel_hi:[1,0,1]\n"
"v_pk_fma_f32 v[74:75],v[74:75],v[60:61],v[54:55] op_sel:[0,0,1] op_sel_hi:[1,0,1]\n"
"v_pk_fma_f32 v[76:77],v[76:77],v[60:61],v[54:55] op_sel:[0,0,1] op_sel_hi:[1,0,1]\n"
"v_pk_fma_f32 v[78:79],v[78:79],v[60:61],v[54:55] op_sel:[0,0,1] op_sel_hi:[1,0,1]\n"
CV "v112,v72,v73\n"
CV "v113,v74,v75\n"
CV "v114,v76,v77\n"
CV "v115,v78,v79\n"
AW "a32,v112\n"
AW "a33,v113\n"
AW "a34,v114\n"
AW "a35,v115\n"
AR "v80,a216\n"
AR "v81,a217\n"
AR "v82,a218\n"
AR "v83,a219\n"
AR "v84,a236\n"
AR "v85,a237\n"
AR "v86,a238\n"
AR "v87,a239\n"
"v_pk_fma_f32 v[80:81],v[80:81],v[60:61],v[56:57] op_sel:[0,1,0] op_sel_hi:[1,1,0]\n"
"v_pk_fma_f32 v[82:83],v[82:83],v[60:61],v[56:57] op_sel:[0,1,0] op_sel_hi:[1,1,0]\n"
"v_pk_fma_f32 v[84:85],v[84:85],v[60:61],v[56:57] op_sel:[0,1,0] op_sel_hi:[1,1,0]\n"
"v_pk_fma_f32 v[86:87],v[86:87],v[60:61],v[56:57] op_sel:[0,1,0] op_sel_hi:[1,1,0]\n"
CV "v116,v80,v81\n"
CV "v117,v82,v83\n"
CV "v118,v84,v85\n"
CV "v119,v86,v87\n"
AW "a36,v116\n"
AW "a37,v117\n"
AW "a38,v118\n"
AW "a39,v119\n"
"s_mov_b32 s13,s11\n"
"s_mov_b32 s70,s71\n"
"s_mov_b32 s57,1\n"
AR "v0,a240\n"
AR "v2,a241\n"
AR "v4,a242\n"
AR "v6,a243\n"
"v_xor_b32 v1,64,v0\n"
"v_xor_b32 v3,64,v2\n"
"v_xor_b32 v5,64,v4\n"
"v_add_u32 v11,97280,v6\n"
"v_add_u32 v12,194560,v6\n"
"v_add_u32 v13,291840,v6\n"
"s_mov_b64 vcc,-1\n"
"s_waitcnt vmcnt(8) lgkmcnt(0)\n"
"s_barrier\n"
"s_nop 0\n"
TR "a[200:201],v0\n"
TR "a[202:203],v0 offset:512\n"
TR "a[204:205],v0 offset:4096\n"
TR "a[206:207],v0 offset:4608\n"
"ds_read_b128 a[60:63],v2\n"
"ds_read_b128 a[100:103],v3\n"
"ds_read_b128 a[64:67],v2 offset:2048\n"
"ds_read_b128 a[104:107],v3 offset:2048\n"
"ds_read_b128 a[68:71],v2 offset:4096\n"
"ds_read_b128 a[108:111],v3 offset:4096\n"
"ds_read_b128 a[72:75],v2 offset:6144\n"
"ds_read_b128 a[112:115],v3 offset:6144\n"
"ds_read_b128 a[76:79],v2 offset:8192\n"
"ds_read_b128 a[116:119],v3 offset:8192\n"
TR "a[208:209],v0 offset:8\n"
TR "a[210:211],v0 offset:520\n"
TR "a[212:213],v0 offset:4104\n"
TR "a[214:215],v0 offset:4616\n"
"s_waitcnt lgkmcnt(0)\n"
"s_branch .Lq0\n"
".Lep:\n"
"s_cbranch_execz .Lipr2\n"
"s_lshl_b32 s58,s56,10\n"
"s_add_u32 s58,s58,73728\n"
"v_add_u32_e64 v120,s58,v9\n"
"v_add_u32 v121,65536,v120\n"
"s_sub_u32 s59,s71,s70\n"
"s_nop 0\n"
TR "v[136:137],v2 offset:24576\n"
TR "v[138:139],v2 offset:30720\n"
TR "v[140:141],v3 offset:24576\n"
TR "v[142:143],v3 offset:30720\n"
TR "v[144:145],v4 offset:24576\n"
TR "v[146:147],v4 offset:30720\n"
TR "v[148:149],v5 offset:24576\n"
TR "v[150:151],v5 offset:30720\n"
TR "v[152:153],v2 offset:25600\n"
TR "v[154:155],v2 offset:31744\n"
TR "v[156:157],v3 offset:25600\n"
TR "v[158:159],v3 offset:31744\n"
TR "v[160:161],v4 offset:25600\n"
TR "v[162:163],v4 offset:31744\n"
TR "v[164:165],v5 offset:25600\n"
TR "v[166:167],v5 offset:31744\n"
TR "v[168:169],v2 offset:26624\n"
TR "v[170:171],v2 offset:32768\n"
TR "v[172:173],v3 offset:26624\n"
TR "v[174:175],v3 offset:32768\n"
TR "v[176:177],v4 offset:26624\n"
TR "v[178:179],v4 offset:32768\n"
TR "v[180:181],v5 offset:26624\n"
TR "v[182:183],v5 offset:32768\n"
"v_mov_b32 v52,v57\n"
"v_mov_b32 v53,v58\n"
"v_mov_b32 v54,v59\n"
"v_mov_b32 v55,v60\n"
"v_mov_b32 v56,v61\n"
"s_add_u32 m0,s65,49152\n"
"s_nop 0\n"
LD "v8,s[36:39],s73 offen offset:3072 nt lds\n"
"s_add_u32 m0,s65,50048\n"
"s_nop 0\n"
LD "v8,s[36:39],s73 offen offset:3200 nt lds\n"
"v_permlane16_swap_b32 v57,v52\n"
"v_permlane16_swap_b32 v58,v53\n"
"v_permlane16_swap_b32 v59,v54\n"
"v_permlane16_swap_b32 v60,v55\n"
"v_permlane16_swap_b32 v61,v56\n"
"v_add_f32 v57,v57,v52\n"
"v_add_f32 v58,v58,v53\n"
"v_add_f32 v59,v59,v54\n"
"v_add_f32 v60,v60,v55\n"
"v_add_f32 v61,v61,v56\n"
"v_mov_b32 v52,v57\n"
"v_mov_b32 v53,v58\n"
"v_mov_b32 v54,v59\n"
"v_mov_b32 v55,v60\n"
"v_mov_b32 v56,v61\n"
"s_add_u32 m0,s65,50944\n"
"s_nop 0\n"
LD "v8,s[36:39],s73 offen offset:3328 nt lds\n"
"s_sub_u32 s74,s96,s16\n"
"s_add_u32 s74,s74,s71\n"
"s_add_u32 s20,s20,s59\n"
"s_mov_b32 s70,s71\n"
LD "v[232:235],v9,s[40:43],s75 offen sc1\n"
"v_permlane32_swap_b32 v57,v52\n"
"v_permlane32_swap_b32 v58,v53\n"
"v_permlane32_swap_b32 v59,v54\n"
"v_permlane32_swap_b32 v60,v55\n"
"v_permlane32_swap_b32 v61,v56\n"
"v_add_f32 v57,v57,v52\n"
"v_add_f32 v58,v58,v53\n"
"v_add_f32 v59,v59,v54\n"
"v_add_f32 v60,v60,v55\n"
"v_add_f32 v61,v61,v56\n"
"s_waitcnt lgkmcnt(15)\n"
MF "a[0:3],v[136:139],v[92:95],a[0:3]\n"
MF "a[20:23],v[140:143],v[92:95],a[20:23]\n"
"v_rcp_f32 v57,v57\n"
"v_rcp_f32 v58,v58\n"
"v_rcp_f32 v59,v59\n"
"v_rcp_f32 v60,v60\n"
"v_rcp_f32_e64 v61,v61\n"
MF "a[4:7],v[136:139],v[96:99],a[4:7]\n"
"v_mul_f32_e64 v52,-s13,v57\n"
"v_mul_f32_e64 v53,-s13,v58\n"
"v_mul_f32_e64 v54,-s13,v59\n"
"v_mul_f32_e64 v55,-s13,v60\n"
"v_mul_f32_e64 v56,-s13,v61\n"
AR "v72,a0\n"
AR "v73,a1\n"
AR "v74,a2\n"
MF "a[24:27],v[140:143],v[96:99],a[24:27]\n"
AR "v75,a3\n"
AR "v76,a20\n"
AR "v77,a21\n"
AR "v78,a22\n"
AR "v79,a23\n"
"v_pk_fma_f32 v[72:73],v[72:73],v[56:57],v[52:53] op_sel:[0,1,0] op_sel_hi:[1,1,0]\n"
"v_pk_fma_f32 v[74:75],v[74:75],v[56:57],v[52:53] op_sel:[0,1,0] op_sel_hi:[1,1,0]\n"
"v_pk_fma_f32 v[76:77],v[76:77],v[56:57],v[52:53] op_sel:[0,1,0] op_sel_hi:[1,1,0]\n"
"v_pk_fma_f32 v[78:79],v[78:79],v[56:57],v[52:53] op_sel:[0,1,0] op_sel_hi:[1,1,0]\n"
CV "v112,v72,v73\n"
CV "v113,v74,v75\n"
CV "v114,v76,v77\n"
CV "v115,v78,v79\n"
MF "a[8:11],v[136:139],v[100:103],a[8:11]\n"
"ds_read_b128 v[184:187],v120 offset:24576\n"
"ds_write_b128 v120,v[112:115] offset:24576\n"
LD "v[236:239],v9,s[40:43],s75 offen offset:1024 sc1\n"
AR "v80,a4\n"
AR "v81,a5\n"
AR "v82,a6\n"
MF "a[28:31],v[140:143],v[100:103],a[28:31]\n"
AR "v83,a7\n"
AR "v84,a24\n"
AR "v85,a25\n"
AR "v86,a26\n"
AR "v87,a27\n"
"v_pk_fma_f32 v[80:81],v[80:81],v[58:59],v[52:53] op_sel:[0,0,1] op_sel_hi:[1,0,1]\n"
"v_pk_fma_f32 v[82:83],v[82:83],v[58:59],v[52:53] op_sel:[0,0,1] op_sel_hi:[1,0,1]\n"
"v_pk_fma_f32 v[84:85],v[84:85],v[58:59],v[52:53] op_sel:[0,0,1] op_sel_hi:[1,0,1]\n"
"v_pk_fma_f32 v[86:87],v[86:87],v[58:59],v[52:53] op_sel:[0,0,1] op_sel_hi:[1,0,1]\n"
CV "v116,v80,v81\n"
CV "v117,v82,v83\n"
CV "v118,v84,v85\n"
CV "v119,v86,v87\n"
MF "a[12:15],v[136:139],v[104:107],a[12:15]\n"
"ds_read_b128 v[188:191],v120 offset:28672\n"
"ds_write_b128 v120,v[116:119] offset:28672\n"
AR "v72,a8\n"
AR "v73,a9\n"
AR "v74,a10\n"
MF "a[32:35],v[140:143],v[104:107],a[32:35]\n"
AR "v75,a11\n"
AR "v76,a28\n"
AR "v77,a29\n"
AR "v78,a30\n"
AR "v79,a31\n"
"v_pk_fma_f32 v[72:73],v[72:73],v[58:59],v[54:55] op_sel:[0,1,0] op_sel_hi:[1,1,0]\n"
"v_pk_fma_f32 v[74:75],v[74:75],v[58:59],v[54:55] op_sel:[0,1,0] op_sel_hi:[1,1,0]\n"
"v_pk_fma_f32 v[76:77],v[76:77],v[58:59],v[54:55] op_sel:[0,1,0] op_sel_hi:[1,1,0]\n"
"v_pk_fma_f32 v[78:79],v[78:79],v[58:59],v[54:55] op_sel:[0,1,0] op_sel_hi:[1,1,0]\n"
CV "v112,v72,v73\n"
CV "v113,v74,v75\n"
CV "v114,v76,v77\n"
CV "v115,v78,v79\n"
MF "a[16:19],v[136:139],v[108:111],a[16:19]\n"
"ds_read_b128 v[192:195],v120 offset:32768\n"
"ds_write_b128 v120,v[112:115] offset:32768\n"
"s_waitcnt lgkmcnt(0)\n"
"s_barrier\n"
LD "v[240:243],v9,s[40:43],s75 offen offset:2048 sc1\n"
AR "v80,a12\n"
AR "v81,a13\n"
AR "v82,a14\n"
MF "a[36:39],v[140:143],v[108:111],a[36:39]\n"
AR "v83,a15\n"
AR "v84,a32\n"
AR "v85,a33\n"
AR "v86,a34\n"
AR "v87,a35\n"
"v_pk_fma_f32 v[80:81],v[80:81],v[60:61],v[54:55] op_sel:[0,0,1] op_sel_hi:[1,0,1]\n"
"v_pk_fma_f32 v[82:83],v[82:83],v[60:61],v[54:55] op_sel:[0,0,1] op_sel_hi:[1,0,1]\n"
"v_pk_fma_f32 v[84:85],v[84:85],v[60:61],v[54:55] op_sel:[0,0,1] op_sel_hi:[1,0,1]\n"
"v_pk_fma_f32 v[86:87],v[86:87],v[60:61],v[54:55] op_sel:[0,0,1] op_sel_hi:[1,0,1]\n"
CV "v116,v80,v81\n"
CV "v117,v82,v83\n"
CV "v118,v84,v85\n"
CV "v119,v86,v87\n"
MF "a[40:43],v[144:147],v[92:95],a[40:43]\n"
"ds_read_b128 v[196:199],v120 offset:36864\n"
"ds_write_b128 v120,v[116:119] offset:36864\n"
LD "v[244:247],v9,s[40:43],s75 offen offset:3072 sc1\n"
AR "v72,a16\n"
AR "v73,a17\n"
AR "v74,a18\n"
MF "a[60:63],v[148:151],v[92:95],a[60:63]\n"
AR "v75,a19\n"
AR "v76,a36\n"
AR "v77,a37\n"
AR "v78,a38\n"
AR "v79,a39\n"
"v_pk_fma_f32 v[72:73],v[72:73],v[60:61],v[56:57] op_sel:[0,1,0] op_sel_hi:[1,1,0]\n"
"v_pk_fma_f32 v[74:75],v[74:75],v[60:61],v[56:57] op_sel:[0,1,0] op_sel_hi:[1,1,0]\n"
"v_pk_fma_f32 v[76:77],v[76:77],v[60:61],v[56:57] op_sel:[0,1,0] op_sel_hi:[1,1,0]\n"
"v_pk_fma_f32 v[78:79],v[78:79],v[60:61],v[56:57] op_sel:[0,1,0] op_sel_hi:[1,1,0]\n"
CV "v112,v72,v73\n"
CV "v113,v74,v75\n"
CV "v114,v76,v77\n"
CV "v115,v78,v79\n"
MF "a[44:47],v[144:147],v[96:99],a[44:47]\n"
"ds_read_b128 v[200:203],v120 offset:40960\n"
"ds_write_b128 v120,v[112:115] offset:40960\n"
AR "v80,a40\n"
AR "v81,a41\n"
AR "v82,a42\n"
MF "a[64:67],v[148:151],v[96:99],a[64:67]\n"
AR "v83,a43\n"
AR "v84,a60\n"
AR "v85,a61\n"
AR "v86,a62\n"
AR "v87,a63\n"
"v_pk_fma_f32 v[80:81],v[80:81],v[56:57],v[52:53] op_sel:[0,1,0] op_sel_hi:[1,1,0]\n"
"v_pk_fma_f32 v[82:83],v[82:83],v[56:57],v[52:53] op_sel:[0,1,0] op_sel_hi:[1,1,0]\n"
"v_pk_fma_f32 v[84:85],v[84:85],v[56:57],v[52:53] op_sel:[0,1,0] op_sel_hi:[1,1,0]\n"
"v_pk_fma_f32 v[86:87],v[86:87],v[56:57],v[52:53] op_sel:[0,1,0] op_sel_hi:[1,1,0]\n"
CV "v116,v80,v81\n"
CV "v117,v82,v83\n"
CV "v118,v84,v85\n"
CV "v119,v86,v87\n"
MF "a[48:51],v[144:147],v[100:103],a[48:51]\n"
"ds_read_b128 v[204:207],v120 offset:45056\n"
"ds_write_b128 v120,v[116:119] offset:45056\n"
"s_add_u32 s58,s75,4096\n"
LD "v[248:251],v9,s[40:43],s58 offen sc1\n"
AR "v72,a44\n"
AR "v73,a45\n"
AR "v74,a46\n"
MF "a[68:71],v[148:151],v[100:103],a[68:71]\n"
AR "v75,a47\n"
AR "v76,a64\n"
AR "v77,a65\n"
AR "v78,a66\n"
AR "v79,a67\n"
"v_pk_fma_f32 v[72:73],v[72:73],v[58:59],v[52:53] op_sel:[0,0,1] op_sel_hi:[1,0,1]\n"
"v_pk_fma_f32 v[74:75],v[74:75],v[58:59],v[52:53] op_sel:[0,0,1] op_sel_hi:[1,0,1]\n"
"v_pk_fma_f32 v[76:77],v[76:77],v[58:59],v[52:53] op_sel:[0,0,1] op_sel_hi:[1,0,1]\n"
"v_pk_fma_f32 v[78:79],v[78:79],v[58:59],v[52:53] op_sel:[0,0,1] op_sel_hi:[1,0,1]\n"
CV "v112,v72,v73\n"
CV "v113,v74,v75\n"
CV "v114,v76,v77\n"
CV "v115,v78,v79\n"
MF "a[52:55],v[144:147],v[104:107],a[52:55]\n"
"ds_read_b128 v[208:211],v120 offset:49152\n"
"ds_write_b128 v120,v[112:115] offset:49152\n"
LD "v[252:255],v9,s[40:43],s58 offen offset:1024 sc1\n"
AR "v80,a48\n"
AR "v81,a49\n"
AR "v82,a50\n"
MF "a[72:75],v[148:151],v[104:107],a[72:75]\n"
AR "v83,a51\n"
AR "v84,a68\n"
AR "v85,a69\n"
AR "v86,a70\n"
AR "v87,a71\n"
"v_pk_fma_f32 v[80:81],v[80:81],v[58:59],v[54:55] op_sel:[0,1,0] op_sel_hi:[1,1,0]\n"
"v_pk_fma_f32 v[82:83],v[82:83],v[58:59],v[54:55] op_sel:[0,1,0] op_sel_hi:[1,1,0]\n"
"v_pk_fma_f32 v[84:85],v[84:85],v[58:59],v[54:55] op_sel:[0,1,0] op_sel_hi:[1,1,0]\n"
"v_pk_fma_f32 v[86:87],v[86:87],v[58:59],v[54:55] op_sel:[0,1,0] op_sel_hi:[1,1,0]\n"
CV "v116,v80,v81\n"
CV "v117,v82,v83\n"
CV "v118,v84,v85\n"
CV "v119,v86,v87\n"
MF "a[56:59],v[144:147],v[108:111],a[56:59]\n"
"ds_read_b128 v[212:215],v120 offset:53248\n"
"ds_write_b128 v120,v[116:119] offset:53248\n"
AR "v72,a52\n"
AR "v73,a53\n"
AR "v74,a54\n"
MF "a[76:79],v[148:151],v[108:111],a[76:79]\n"
AR "v75,a55\n"
AR "v76,a72\n"
AR "v77,a73\n"
AR "v78,a74\n"
AR "v79,a75\n"
"v_pk_fma_f32 v[72:73],v[72:73],v[60:61],v[54:55] op_sel:[0,0,1] op_sel_hi:[1,0,1]\n"
"v_pk_fma_f32 v[74:75],v[74:75],v[60:61],v[54:55] op_sel:[0,0,1] op_sel_hi:[1,0,1]\n"
"v_pk_fma_f32 v[76:77],v[76:77],v[60:61],v[54:55] op_sel:[0,0,1] op_sel_hi:[1,0,1]\n"
"v_pk_fma_f32 v[78:79],v[78:79],v[60:61],v[54:55] op_sel:[0,0,1] op_sel_hi:[1,0,1]\n"
CV "v112,v72,v73\n"
CV "v113,v74,v75\n"
CV "v114,v76,v77\n"
CV "v115,v78,v79\n"
MF "a[80:83],v[152:155],v[92:95],a[80:83]\n"
"ds_read_b128 v[216:219],v120 offset:57344\n"
"ds_write_b128 v120,v[112:115] offset:57344\n"
AR "v80,a56\n"
AR "v81,a57\n"
AR "v82,a58\n"
MF "a[100:103],v[156:159],v[92:95],a[100:103]\n"
AR "v83,a59\n"
AR "v84,a76\n"
AR "v85,a77\n"
AR "v86,a78\n"
AR "v87,a79\n"
"v_pk_fma_f32 v[80:81],v[80:81],v[60:61],v[56:57] op_sel:[0,1,0] op_sel_hi:[1,1,0]\n"
"v_pk_fma_f32 v[82:83],v[82:83],v[60:61],v[56:57] op_sel:[0,1,0] op_sel_hi:[1,1,0]\n"
"v_pk_fma_f32 v[84:85],v[84:85],v[60:61],v[56:57] op_sel:[0,1,0] op_sel_hi:[1,1,0]\n"
"v_pk_fma_f32 v[86:87],v[86:87],v[60:61],v[56:57] op_sel:[0,1,0] op_sel_hi:[1,1,0]\n"
CV "v116,v80,v81\n"
CV "v117,v82,v83\n"
CV "v118,v84,v85\n"
CV "v119,v86,v87\n"
MF "a[84:87],v[152:155],v[96:99],a[84:87]\n"
"ds_read_b128 v[220:223],v120 offset:61440\n"
"ds_write_b128 v120,v[116:119] offset:61440\n"
AR "v72,a80\n"
AR "v73,a81\n"
AR "v74,a82\n"
MF "a[104:107],v[156:159],v[96:99],a[104:107]\n"
AR "v75,a83\n"
AR "v76,a100\n"
AR "v77,a101\n"
AR "v78,a102\n"
AR "v79,a103\n"
"v_pk_fma_f32 v[72:73],v[72:73],v[56:57],v[52:53] op_sel:[0,1,0] op_sel_hi:[1,1,0]\n"
"v_pk_fma_f32 v[74:75],v[74:75],v[56:57],v[52:53] op_sel:[0,1,0] op_sel_hi:[1,1,0]\n"
"v_pk_fma_f32 v[76:77],v[76:77],v[56:57],v[52:53] op_sel:[0,1,0] op_sel_hi:[1,1,0]\n"
"v_pk_fma_f32 v[78:79],v[78:79],v[56:57],v[52:53] op_sel:[0,1,0] op_sel_hi:[1,1,0]\n"
CV "v112,v72,v73\n"
CV "v113,v74,v75\n"
CV "v114,v76,v77\n"
CV "v115,v78,v79\n"
MF "a[88:91],v[152:155],v[100:103],a[88:91]\n"
"ds_read_b128 v[224:227],v121\n"
"ds_write_b128 v121,v[112:115]\n"
AR "v80,a84\n"
AR "v81,a85\n"
AR "v82,a86\n"
MF "a[108:111],v[156:159],v[100:103],a[108:111]\n"
AR "v83,a87\n"
AR "v84,a104\n"
AR "v85,a105\n"
AR "v86,a106\n"
AR "v87,a107\n"
"v_pk_fma_f32 v[80:81],v[80:81],v[58:59],v[52:53] op_sel:[0,0,1] op_sel_hi:[1,0,1]\n"
"v_pk_fma_f32 v[82:83],v[82:83],v[58:59],v[52:53] op_sel:[0,0,1] op_sel_hi:[1,0,1]\n"
"v_pk_fma_f32 v[84:85],v[84:85],v[58:59],v[52:53] op_sel:[0,0,1] op_sel_hi:[1,0,1]\n"
"v_pk_fma_f32 v[86:87],v[86:87],v[58:59],v[52:53] op_sel:[0,0,1] op_sel_hi:[1,0,1]\n"
CV "v116,v80,v81\n"
CV "v117,v82,v83\n"
CV "v118,v84,v85\n"
CV "v119,v86,v87\n"
MF "a[92:95],v[152:155],v[104:107],a[92:95]\n"
"ds_read_b128 v[228:231],v121 offset:4096\n"
"ds_write_b128 v121,v[116:119] offset:4096\n"
AR "v72,a88\n"
AR "v73,a89\n"
AR "v74,a90\n"
MF "a[112:115],v[156:159],v[104:107],a[112:115]\n"
AR "v75,a91\n"
AR "v76,a108\n"
AR "v77,a109\n"
AR "v78,a110\n"
AR "v79,a111\n"
"v_pk_fma_f32 v[72:73],v[72:73],v[58:59],v[54:55] op_sel:[0,1,0] op_sel_hi:[1,1,0]\n"
"v_pk_fma_f32 v[74:75],v[74:75],v[58:59],v[54:55] op_sel:[0,1,0] op_sel_hi:[1,1,0]\n"
"v_pk_fma_f32 v[76:77],v[76:77],v[58:59],v[54:55] op_sel:[0,1,0] op_sel_hi:[1,1,0]\n"
"v_pk_fma_f32 v[78:79],v[78:79],v[58:59],v[54:55] op_sel:[0,1,0] op_sel_hi:[1,1,0]\n"
CV "v112,v72,v73\n"
CV "v113,v74,v75\n"
CV "v114,v76,v77\n"
CV "v115,v78,v79\n"
MF "a[96:99],v[152:155],v[108:111],a[96:99]\n"
"ds_read_b128 v[136:139],v121 offset:8192\n"
"ds_write_b128 v121,v[112:115] offset:8192\n"
AR "v80,a92\n"
AR "v81,a93\n"
AR "v82,a94\n"
MF "a[116:119],v[156:159],v[108:111],a[116:119]\n"
AR "v83,a95\n"
AR "v84,a112\n"
AR "v85,a113\n"
AR "v86,a114\n"
AR "v87,a115\n"
"v_pk_fma_f32 v[80:81],v[80:81],v[60:61],v[54:55] op_sel:[0,0,1] op_sel_hi:[1,0,1]\n"
"v_pk_fma_f32 v[82:83],v[82:83],v[60:61],v[54:55] op_sel:[0,0,1] op_sel_hi:[1,0,1]\n"
"v_pk_fma_f32 v[84:85],v[84:85],v[60:61],v[54:55] op_sel:[0,0,1] op_sel_hi:[1,0,1]\n"
"v_pk_fma_f32 v[86:87],v[86:87],v[60:61],v[54:55] op_sel:[0,0,1] op_sel_hi:[1,0,1]\n"
CV "v116,v80,v81\n"
CV "v117,v82,v83\n"
CV "v118,v84,v85\n"
CV "v119,v86,v87\n"
MF "a[120:123],v[160:163],v[92:95],a[120:123]\n"
"ds_read_b128 v[140:143],v121 offset:12288\n"
"ds_write_b128 v121,v[116:119] offset:12288\n"
AR "v72,a96\n"
AR "v73,a97\n"
AR "v74,a98\n"
MF "a[140:143],v[164:167],v[92:95],a[140:143]\n"
AR "v75,a99\n"
AR "v76,a116\n"
AR "v77,a117\n"
AR "v78,a118\n"
AR "v79,a119\n"
"v_pk_fma_f32 v[72:73],v[72:73],v[60:61],v[56:57] op_sel:[0,1,0] op_sel_hi:[1,1,0]\n"
"v_pk_fma_f32 v[74:75],v[74:75],v[60:61],v[56:57] op_sel:[0,1,0] op_sel_hi:[1,1,0]\n"
"v_pk_fma_f32 v[76:77],v[76:77],v[60:61],v[56:57] op_sel:[0,1,0] op_sel_hi:[1,1,0]\n"
"v_pk_fma_f32 v[78:79],v[78:79],v[60:61],v[56:57] op_sel:[0,1,0] op_sel_hi:[1,1,0]\n"
CV "v112,v72,v73\n"
CV "v113,v74,v75\n"
CV "v114,v76,v77\n"
CV "v115,v78,v79\n"
MF "a[124:127],v[160:163],v[96:99],a[124:127]\n"
"ds_read_b128 v[144:147],v121 offset:16384\n"
"ds_write_b128 v121,v[112:115] offset:16384\n"
AR "v80,a120\n"
AR "v81,a121\n"
AR "v82,a122\n"
MF "a[144:147],v[164:167],v[96:99],a[144:147]\n"
AR "v83,a123\n"
AR "v84,a140\n"
AR "v85,a141\n"
AR "v86,a142\n"
AR "v87,a143\n"
"v_pk_fma_f32 v[80:81],v[80:81],v[56:57],v[52:53] op_sel:[0,1,0] op_sel_hi:[1,1,0]\n"
"v_pk_fma_f32 v[82:83],v[82:83],v[56:57],v[52:53] op_sel:[0,1,0] op_sel_hi:[1,1,0]\n"
"v_pk_fma_f32 v[84:85],v[84:85],v[56:57],v[52:53] op_sel:[0,1,0] op_sel_hi:[1,1,0]\n"
"v_pk_fma_f32 v[86:87],v[86:87],v[56:57],v[52:53] op_sel:[0,1,0] op_sel_hi:[1,1,0]\n"
CV "v116,v80,v81\n"
CV "v117,v82,v83\n"
CV "v118,v84,v85\n"
CV "v119,v86,v87\n"
MF "a[128:131],v[160:163],v[100:103],a[128:131]\n"
"ds_read_b128 v[148:151],v121 offset:20480\n"
"ds_write_b128 v121,v[116:119] offset:20480\n"
AR "v72,a124\n"
AR "v73,a125\n"
AR "v74,a126\n"
MF "a[148:151],v[164:167],v[100:103],a[148:151]\n"
AR "v75,a127\n"
AR "v76,a144\n"
AR "v77,a145\n"
AR "v78,a146\n"
AR "v79,a147\n"
"v_pk_fma_f32 v[72:73],v[72:73],v[58:59],v[52:53] op_sel:[0,0,1] op_sel_hi:[1,0,1]\n"
"v_pk_fma_f32 v[74:75],v[74:75],v[58:59],v[52:53] op_sel:[0,0,1] op_sel_hi:[1,0,1]\n"
"v_pk_fma_f32 v[76:77],v[76:77],v[58:59],v[52:53] op_sel:[0,0,1] op_sel_hi:[1,0,1]\n"
"v_pk_fma_f32 v[78:79],v[78:79],v[58:59],v[52:53] op_sel:[0,0,1] op_sel_hi:[1,0,1]\n"
CV "v112,v72,v73\n"
CV "v113,v74,v75\n"
CV "v114,v76,v77\n"
CV "v115,v78,v79\n"
MF "a[132:135],v[160:163],v[104:107],a[132:135]\n"
"ds_write_b128 v120,v[112:115]\n"
AR "v80,a128\n"
AR "v81,a129\n"
AR "v82,a130\n"
MF "a[152:155],v[164:167],v[104:107],a[152:155]\n"
AR "v83,a131\n"
AR "v84,a148\n"
AR "v85,a149\n"
AR "v86,a150\n"
AR "v87,a151\n"
"v_pk_fma_f32 v[80:81],v[80:81],v[58:59],v[54:55] op_sel:[0,1,0] op_sel_hi:[1,1,0]\n"
"v_pk_fma_f32 v[82:83],v[82:83],v[58:59],v[54:55] op_sel:[0,1,0] op_sel_hi:[1,1,0]\n"
"v_pk_fma_f32 v[84:85],v[84:85],v[58:59],v[54:55] op_sel:[0,1,0] op_sel_hi:[1,1,0]\n"
"v_pk_fma_f32 v[86:87],v[86:87],v[58:59],v[54:55] op_sel:[0,1,0] op_sel_hi:[1,1,0]\n"
CV "v116,v80,v81\n"
CV "v117,v82,v83\n"
CV "v118,v84,v85\n"
CV "v119,v86,v87\n"
MF "a[136:139],v[160:163],v[108:111],a[136:139]\n"
"ds_write_b128 v120,v[116:119] offset:4096\n"
AR "v72,a132\n"
AR "v73,a133\n"
AR "v74,a134\n"
MF "a[156:159],v[164:167],v[108:111],a[156:159]\n"
AR "v75,a135\n"
AR "v76,a152\n"
AR "v77,a153\n"
AR "v78,a154\n"
AR "v79,a155\n"
"v_pk_fma_f32 v[72:73],v[72:73],v[60:61],v[54:55] op_sel:[0,0,1] op_sel_hi:[1,0,1]\n"
"v_pk_fma_f32 v[74:75],v[74:75],v[60:61],v[54:55] op_sel:[0,0,1] op_sel_hi:[1,0,1]\n"
"v_pk_fma_f32 v[76:77],v[76:77],v[60:61],v[54:55] op_sel:[0,0,1] op_sel_hi:[1,0,1]\n"
"v_pk_fma_f32 v[78:79],v[78:79],v[60:61],v[54:55] op_sel:[0,0,1] op_sel_hi:[1,0,1]\n"
CV "v112,v72,v73\n"
CV "v113,v74,v75\n"
CV "v114,v76,v77\n"
CV "v115,v78,v79\n"
MF "a[160:163],v[168:171],v[92:95],a[160:163]\n"
"ds_write_b128 v120,v[112:115] offset:8192\n"
AR "v80,a136\n"
AR "v81,a137\n"
AR "v82,a138\n"
MF "a[180:183],v[172:175],v[92:95],a[180:183]\n"
AR "v83,a139\n"
AR "v84,a156\n"
AR "v85,a157\n"
AR "v86,a158\n"
AR "v87,a159\n"
"v_pk_fma_f32 v[80:81],v[80:81],v[60:61],v[56:57] op_sel:[0,1,0] op_sel_hi:[1,1,0]\n"
"v_pk_fma_f32 v[82:83],v[82:83],v[60:61],v[56:57] op_sel:[0,1,0] op_sel_hi:[1,1,0]\n"
"v_pk_fma_f32 v[84:85],v[84:85],v[60:61],v[56:57] op_sel:[0,1,0] op_sel_hi:[1,1,0]\n"
"v_pk_fma_f32 v[86:87],v[86:87],v[60:61],v[56:57] op_sel:[0,1,0] op_sel_hi:[1,1,0]\n"
CV "v116,v80,v81\n"
CV "v117,v82,v83\n"
CV "v118,v84,v85\n"
CV "v119,v86,v87\n"
MF "a[164:167],v[168:171],v[96:99],a[164:167]\n"
"ds_write_b128 v120,v[116:119] offset:12288\n"
AR "v72,a160\n"
AR "v73,a161\n"
AR "v74,a162\n"
MF "a[184:187],v[172:175],v[96:99],a[184:187]\n"
AR "v75,a163\n"
AR "v76,a180\n"
AR "v77,a181\n"
AR "v78,a182\n"
AR "v79,a183\n"
"v_pk_fma_f32 v[72:73],v[72:73],v[56:57],v[52:53] op_sel:[0,1,0] op_sel_hi:[1,1,0]\n"
"v_pk_fma_f32 v[74:75],v[74:75],v[56:57],v[52:53] op_sel:[0,1,0] op_sel_hi:[1,1,0]\n"
"v_pk_fma_f32 v[76:77],v[76:77],v[56:57],v[52:53] op_sel:[0,1,0] op_sel_hi:[1,1,0]\n"
"v_pk_fma_f32 v[78:79],v[78:79],v[56:57],v[52:53] op_sel:[0,1,0] op_sel_hi:[1,1,0]\n"
CV "v112,v72,v73\n"
CV "v113,v74,v75\n"
CV "v114,v76,v77\n"
CV "v115,v78,v79\n"
MF "a[168:171],v[168:171],v[100:103],a[168:171]\n"
"ds_write_b128 v120,v[112:115] offset:16384\n"
AR "v80,a164\n"
AR "v81,a165\n"
AR "v82,a166\n"
MF "a[188:191],v[172:175],v[100:103],a[188:191]\n"
AR "v83,a167\n"
AR "v84,a184\n"
AR "v85,a185\n"
AR "v86,a186\n"
AR "v87,a187\n"
"v_pk_fma_f32 v[80:81],v[80:81],v[58:59],v[52:53] op_sel:[0,0,1] op_sel_hi:[1,0,1]\n"
"v_pk_fma_f32 v[82:83],v[82:83],v[58:59],v[52:53] op_sel:[0,0,1] op_sel_hi:[1,0,1]\n"
"v_pk_fma_f32 v[84:85],v[84:85],v[58:59],v[52:53] op_sel:[0,0,1] op_sel_hi:[1,0,1]\n"
"v_pk_fma_f32 v[86:87],v[86:87],v[58:59],v[52:53] op_sel:[0,0,1] op_sel_hi:[1,0,1]\n"
CV "v116,v80,v81\n"
CV "v117,v82,v83\n"
CV "v118,v84,v85\n"
CV "v119,v86,v87\n"
MF "a[172:175],v[168:171],v[104:107],a[172:175]\n"
"ds_write_b128 v120,v[116:119] offset:20480\n"
AR "v72,a168\n"
AR "v73,a169\n"
AR "v74,a170\n"
MF "a[192:195],v[172:175],v[104:107],a[192:195]\n"
AR "v75,a171\n"
AR "v76,a188\n"
AR "v77,a189\n"
AR "v78,a190\n"
AR "v79,a191\n"
"v_pk_fma_f32 v[72:73],v[72:73],v[58:59],v[54:55] op_sel:[0,1,0] op_sel_hi:[1,1,0]\n"
"v_pk_fma_f32 v[74:75],v[74:75],v[58:59],v[54:55] op_sel:[0,1,0] op_sel_hi:[1,1,0]\n"
"v_pk_fma_f32 v[76:77],v[76:77],v[58:59],v[54:55] op_sel:[0,1,0] op_sel_hi:[1,1,0]\n"
"v_pk_fma_f32 v[78:79],v[78:79],v[58:59],v[54:55] op_sel:[0,1,0] op_sel_hi:[1,1,0]\n"
CV "v112,v72,v73\n"
CV "v113,v74,v75\n"
CV "v114,v76,v77\n"
CV "v115,v78,v79\n"
MF "a[176:179],v[168:171],v[108:111],a[176:179]\n"
AR "v152,a240\n"
AW "a240,v112\n"
AR "v153,a241\n"
AW "a241,v113\n"
AR "v154,a242\n"
AW "a242,v114\n"
AR "v155,a243\n"
AW "a243,v115\n"
AR "v80,a172\n"
AR "v81,a173\n"
AR "v82,a174\n"
MF "a[196:199],v[172:175],v[108:111],a[196:199]\n"
AR "v83,a175\n"
AR "v84,a192\n"
AR "v85,a193\n"
AR "v86,a194\n"
AR "v87,a195\n"
"v_pk_fma_f32 v[80:81],v[80:81],v[60:61],v[54:55] op_sel:[0,0,1] op_sel_hi:[1,0,1]\n"
"v_pk_fma_f32 v[82:83],v[82:83],v[60:61],v[54:55] op_sel:[0,0,1] op_sel_hi:[1,0,1]\n"
"v_pk_fma_f32 v[84:85],v[84:85],v[60:61],v[54:55] op_sel:[0,0,1] op_sel_hi:[1,0,1]\n"
"v_pk_fma_f32 v[86:87],v[86:87],v[60:61],v[54:55] op_sel:[0,0,1] op_sel_hi:[1,0,1]\n"
CV "v116,v80,v81\n"
CV "v117,v82,v83\n"
CV "v118,v84,v85\n"
CV "v119,v86,v87\n"
MF "a[200:203],v[176:179],v[92:95],a[200:203]\n"
AR "v156,a244\n"
AW "a244,v116\n"
AR "v157,a245\n"
AW "a245,v117\n"
AR "v158,a246\n"
AW "a246,v118\n"
AR "v159,a247\n"
AW "a247,v119\n"
AR "v72,a176\n"
AR "v73,a177\n"
AR "v74,a178\n"
MF "a[220:223],v[180:183],v[92:95],a[220:223]\n"
AR "v75,a179\n"
AR "v76,a196\n"
AR "v77,a197\n"
AR "v78,a198\n"
AR "v79,a199\n"
"v_pk_fma_f32 v[72:73],v[72:73],v[60:61],v[56:57] op_sel:[0,1,0] op_sel_hi:[1,1,0]\n"
"v_pk_fma_f32 v[74:75],v[74:75],v[60:61],v[56:57] op_sel:[0,1,0] op_sel_hi:[1,1,0]\n"
"v_pk_fma_f32 v[76:77],v[76:77],v[60:61],v[56:57] op_sel:[0,1,0] op_sel_hi:[1,1,0]\n"
"v_pk_fma_f32 v[78:79],v[78:79],v[60:61],v[56:57] op_sel:[0,1,0] op_sel_hi:[1,1,0]\n"
CV "v112,v72,v73\n"
CV "v113,v74,v75\n"
CV "v114,v76,v77\n"
CV "v115,v78,v79\n"
MF "a[204:207],v[176:179],v[96:99],a[204:207]\n"
AR "v160,a248\n"
AW "a248,v112\n"
AR "v161,a249\n"
AW "a249,v113\n"
AR "v162,a250\n"
AW "a250,v114\n"
AR "v163,a251\n"
AW "a251,v115\n"
AR "v80,a200\n"
AR "v81,a201\n"
AR "v82,a202\n"
MF "a[224:227],v[180:183],v[96:99],a[224:227]\n"
AR "v83,a203\n"
AR "v84,a220\n"
AR "v85,a221\n"
AR "v86,a222\n"
AR "v87,a223\n"
"v_pk_fma_f32 v[80:81],v[80:81],v[56:57],v[52:53] op_sel:[0,1,0] op_sel_hi:[1,1,0]\n"
"v_pk_fma_f32 v[82:83],v[82:83],v[56:57],v[52:53] op_sel:[0,1,0] op_sel_hi:[1,1,0]\n"
"v_pk_fma_f32 v[84:85],v[84:85],v[56:57],v[52:53] op_sel:[0,1,0] op_sel_hi:[1,1,0]\n"
"v_pk_fma_f32 v[86:87],v[86:87],v[56:57],v[52:53] op_sel:[0,1,0] op_sel_hi:[1,1,0]\n"
CV "v116,v80,v81\n"
CV "v117,v82,v83\n"
CV "v118,v84,v85\n"
CV "v119,v86,v87\n"
MF "a[208:211],v[176:179],v[100:103],a[208:211]\n"
"v_mov_b32 v164,v62\n"
"v_mov_b32 v62,v116\n"
"v_mov_b32 v165,v63\n"
"v_mov_b32 v63,v117\n"
"v_mov_b32 v166,v64\n"
"v_mov_b32 v64,v118\n"
"v_mov_b32 v167,v65\n"
"v_mov_b32 v65,v119\n"
AR "v72,a204\n"
AR "v73,a205\n"
AR "v74,a206\n"
MF "a[228:231],v[180:183],v[100:103],a[228:231]\n"
AR "v75,a207\n"
AR "v76,a224\n"
AR "v77,a225\n"
AR "v78,a226\n"
AR "v79,a227\n"
"v_pk_fma_f32 v[72:73],v[72:73],v[58:59],v[52:53] op_sel:[0,0,1] op_sel_hi:[1,0,1]\n"
"v_pk_fma_f32 v[74:75],v[74:75],v[58:59],v[52:53] op_sel:[0,0,1] op_sel_hi:[1,0,1]\n"
"v_pk_fma_f32 v[76:77],v[76:77],v[58:59],v[52:53] op_sel:[0,0,1] op_sel_hi:[1,0,1]\n"
"v_pk_fma_f32 v[78:79],v[78:79],v[58:59],v[52:53] op_sel:[0,0,1] op_sel_hi:[1,0,1]\n"
CV "v112,v72,v73\n"
CV "v113,v74,v75\n"
CV "v114,v76,v77\n"
CV "v115,v78,v79\n"
MF "a[212:215],v[176:179],v[104:107],a[212:215]\n"
"v_mov_b32 v168,v66\n"
"v_mov_b32 v66,v112\n"
"v_mov_b32 v169,v67\n"
"v_mov_b32 v67,v113\n"
"v_mov_b32 v170,v68\n"
"v_mov_b32 v68,v114\n"
"v_mov_b32 v171,v69\n"
"v_mov_b32 v69,v115\n"
AR "v80,a208\n"
AR "v81,a209\n"
AR "v82,a210\n"
MF "a[232:235],v[180:183],v[104:107],a[232:235]\n"
AR "v83,a211\n"
AR "v84,a228\n"
AR "v85,a229\n"
AR "v86,a230\n"
AR "v87,a231\n"
"v_pk_fma_f32 v[80:81],v[80:81],v[58:59],v[54:55] op_sel:[0,1,0] op_sel_hi:[1,1,0]\n"
"v_pk_fma_f32 v[82:83],v[82:83],v[58:59],v[54:55] op_sel:[0,1,0] op_sel_hi:[1,1,0]\n"
"v_pk_fma_f32 v[84:85],v[84:85],v[58:59],v[54:55] op_sel:[0,1,0] op_sel_hi:[1,1,0]\n"
"v_pk_fma_f32 v[86:87],v[86:87],v[58:59],v[54:55] op_sel:[0,1,0] op_sel_hi:[1,1,0]\n"
CV "v116,v80,v81\n"
CV "v117,v82,v83\n"
CV "v118,v84,v85\n"
CV "v119,v86,v87\n"
MF "a[216:219],v[176:179],v[108:111],a[216:219]\n"
"v_mov_b32 v172,v70\n"
"v_mov_b32 v70,v116\n"
"v_mov_b32 v173,v71\n"
"v_mov_b32 v71,v117\n"
"v_mov_b32 v174,v132\n"
"v_mov_b32 v132,v118\n"
"v_mov_b32 v175,v133\n"
"v_mov_b32 v133,v119\n"
AR "v72,a212\n"
AR "v73,a213\n"
AR "v74,a214\n"
MF "a[236:239],v[180:183],v[108:111],a[236:239]\n"
AR "v75,a215\n"
AR "v76,a232\n"
AR "v77,a233\n"
AR "v78,a234\n"
AR "v79,a235\n"
"v_pk_fma_f32 v[72:73],v[72:73],v[60:61],v[54:55] op_sel:[0,0,1] op_sel_hi:[1,0,1]\n"
"v_pk_fma_f32 v[74:75],v[74:75],v[60:61],v[54:55] op_sel:[0,0,1] op_sel_hi:[1,0,1]\n"
"v_pk_fma_f32 v[76:77],v[76:77],v[60:61],v[54:55] op_sel:[0,0,1] op_sel_hi:[1,0,1]\n"
"v_pk_fma_f32 v[78:79],v[78:79],v[60:61],v[54:55] op_sel:[0,0,1] op_sel_hi:[1,0,1]\n"
CV "v112,v72,v73\n"
CV "v113,v74,v75\n"
CV "v114,v76,v77\n"
CV "v115,v78,v79\n"
"v_mov_b32 v176,v134\n"
"v_mov_b32 v134,v112\n"
"v_mov_b32 v177,v135\n"
"v_mov_b32 v135,v113\n"
"v_mov_b32 v178,v6\n"
"v_mov_b32 v6,v114\n"
"v_mov_b32 v179,v13\n"
"v_mov_b32 v13,v115\n"
AR "v80,a216\n"
AR "v81,a217\n"
AR "v82,a218\n"
AR "v83,a219\n"
AR "v84,a236\n"
AR "v85,a237\n"
AR "v86,a238\n"
AR "v87,a239\n"
"v_pk_fma_f32 v[80:81],v[80:81],v[60:61],v[56:57] op_sel:[0,1,0] op_sel_hi:[1,1,0]\n"
"v_pk_fma_f32 v[82:83],v[82:83],v[60:61],v[56:57] op_sel:[0,1,0] op_sel_hi:[1,1,0]\n"
"v_pk_fma_f32 v[84:85],v[84:85],v[60:61],v[56:57] op_sel:[0,1,0] op_sel_hi:[1,1,0]\n"
"v_pk_fma_f32 v[86:87],v[86:87],v[60:61],v[56:57] op_sel:[0,1,0] op_sel_hi:[1,1,0]\n"
CV "v116,v80,v81\n"
CV "v117,v82,v83\n"
CV "v118,v84,v85\n"
CV "v119,v86,v87\n"
"v_mov_b32 v180,v14\n"
"v_mov_b32 v14,v116\n"
"v_mov_b32 v181,v15\n"
"v_mov_b32 v15,v117\n"
AR "v182,a254\n"
AW "a254,v118\n"
AR "v183,a255\n"
AW "a255,v119\n"
"s_mov_b32 s13,s11\n"
"v_subrev_u32 v2,49152,v2\n"
"v_subrev_u32 v3,49152,v3\n"
"v_subrev_u32 v4,49152,v4\n"
"v_subrev_u32 v5,49152,v5\n"
"s_mov_b32 s57,2\n"
"s_mov_b32 s69,1\n"
"s_mov_b32 s61,3\n"
"s_memrealtime s[100:101]\n"
"s_waitcnt lgkmcnt(0)\n"
"s_cmp_eq_u32 s87,1\n"
"s_cselect_b32 s97,0,0\n"
"s_cselect_b32 s21,215,0\n"
"s_add_u32 s21,s21,s100\n"
"s_waitcnt vmcnt(0)\n"
"s_branch .Latt2\n"
".LattB:\n"
"s_cbranch_execz .Lipr2\n"
AR "v0,a244\n"
AR "v2,a245\n"
"v_xor_b32 v1,64,v0\n"
"v_xor_b32 v3,32,v2\n"
"v_xor_b32 v4,64,v2\n"
"v_xor_b32 v5,96,v2\n"
"s_mov_b32 s61,3\n"
"s_memtime s[52:53]\n"
"s_memrealtime s[100:101]\n"
"s_waitcnt lgkmcnt(0)\n"
"s_sub_u32 s53,s52,s80\n"
"s_cmp_gt_u32 s53,30000\n"
"s_cselect_b32 s101,1,0\n"
"s_cmp_eq_u32 s87,2\n"
"s_cselect_b32 s87,s101,s87\n"
"s_cmp_eq_u32 s87,1\n"
"s_cselect_b32 s97,0,0\n"
"s_cselect_b32 s21,215,0\n"
"s_add_u32 s21,s21,s100\n"
".Latt2:\n"
"s_cmp_eq_u32 s97,0\n"
"s_cselect_b32 s95,0x47800000,0\n"
"s_mov_b32 s77,s95\n"
"s_waitcnt vmcnt(12) lgkmcnt(0)\n"
"s_barrier\n"
"ds_read_b128 v[16:19],v0\n"
"ds_read_b128 v[20:23],v1\n"
"ds_read_b128 v[24:27],v0 offset:1024\n"
"s_cmp_eq_u32 s57,1\n"
"s_cbranch_scc1 .Lt0k\n"
"s_waitcnt lgkmcnt(2)\n"
MF "v[72:75],v[16:19],v[136:139],0\n"
"ds_read_b128 v[28:31],v1 offset:1024\n"
MF "v[76:79],v[16:19],v[140:143],0\n"
MF "v[80:83],v[16:19],v[144:147],0\n"
MF "v[84:87],v[16:19],v[148:151],0\n"
MF "v[88:91],v[16:19],v[152:155],0\n"
"s_waitcnt lgkmcnt(2)\n"
"ds_read_b128 v[16:19],v0 offset:2048\n"
MF "v[72:75],v[20:23],v[156:159],v[72:75]\n"
MF "v[76:79],v[20:23],v[160:163],v[76:79]\n"
MF "v[80:83],v[20:23],v[164:167],v[80:83]\n"
MF "v[84:87],v[20:23],v[168:171],v[84:87]\n"
MF "v[88:91],v[20:23],v[172:175],v[88:91]\n"
"s_waitcnt lgkmcnt(2)\n"
"ds_read_b128 v[20:23],v1 offset:2048\n"
MF "v[72:75],v[24:27],v[176:179],v[72:75]\n"
MF "v[76:79],v[24:27],v[180:183],v[76:79]\n"
MF "v[80:83],v[24:27],v[184:187],v[80:83]\n"
MF "v[84:87],v[24:27],v[188:191],v[84:87]\n"
MF "v[88:91],v[24:27],v[192:195],v[88:91]\n"
"s_waitcnt lgkmcnt(2)\n"
"ds_read_b128 v[24:27],v0 offset:6144\n"
MF "v[72:75],v[28:31],v[196:199],v[72:75]\n"
MF "v[76:79],v[28:31],v[200:203],v[76:79]\n"
MF "v[80:83],v[28:31],v[204:207],v[80:83]\n"
MF "v[84:87],v[28:31],v[208:211],v[84:87]\n"
MF "v[88:91],v[28:31],v[212:215],v[88:91]\n"
"s_waitcnt lgkmcnt(2)\n"
"ds_read_b128 v[28:31],v1 offset:6144\n"
MF "v[72:75],v[16:19],v[216:219],v[72:75]\n"
MF "v[76:79],v[16:19],v[220:223],v[76:79]\n"
MF "v[80:83],v[16:19],v[224:227],v[80:83]\n"
MF "v[84:87],v[16:19],v[228:231],v[84:87]\n"
MF "v[88:91],v[16:19],v[232:235],v[88:91]\n"
"s_waitcnt lgkmcnt(2)\n"
"ds_read_b128 v[16:19],v0 offset:7168\n"
MF "v[72:75],v[20:23],v[236:239],v[72:75]\n"
MF "v[76:79],v[20:23],v[240:243],v[76:79]\n"
MF "v[80:83],v[20:23],v[244:247],v[80:83]\n"
MF "v[84:87],v[20:23],v[248:251],v[84:87]\n"
MF "v[88:91],v[20:23],v[252:255],v[88:91]\n"
"s_waitcnt lgkmcnt(2)\n"
"ds_read_b128 v[20:23],v1 offset:7168\n"
MF "v[112:115],v[24:27],v[136:139],0\n"
MF "v[116:119],v[24:27],v[140:143],0\n"
MF "v[120:123],v[24:27],v[144:147],0\n"
MF "v[124:127],v[24:27],v[148:151],0\n"
MF "v[128:131],v[24:27],v[152:155],0\n"
"s_waitcnt lgkmcnt(2)\n"
"ds_read_b128 v[24:27],v0 offset:8192\n"
MF "v[112:115],v[28:31],v[156:159],v[112:115]\n"
"v_max3_f32 v52,v72,v73,v74\n"
"v_max_f32 v52,v52,v75\n"
MF "v[116:119],v[28:31],v[160:163],v[116:119]\n"
"v_max3_f32 v53,v76,v77,v78\n"
"v_max_f32 v53,v53,v79\n"
MF "v[120:123],v[28:31],v[164:167],v[120:123]\n"
"v_max3_f32 v54,v80,v81,v82\n"
"v_max_f32 v54,v54,v83\n"
MF "v[124:127],v[28:31],v[168:171],v[124:127]\n"
"v_max3_f32 v55,v84,v85,v86\n"
"v_max_f32 v55,v55,v87\n"
MF "v[128:131],v[28:31],v[172:175],v[128:131]\n"
"s_waitcnt lgkmcnt(2)\n"
"ds_read_b128 v[28:31],v1 offset:8192\n"
"v_max3_f32 v56,v88,v89,v90\n"
MF "v[112:115],v[16:19],v[176:179],v[112:115]\n"
"v_max_f32 v56,v56,v91\n"
MF "v[116:119],v[16:19],v[180:183],v[116:119]\n"
MF "v[120:123],v[16:19],v[184:187],v[120:123]\n"
MF "v[124:127],v[16:19],v[188:191],v[124:127]\n"
MF "v[128:131],v[16:19],v[192:195],v[128:131]\n"
"s_waitcnt lgkmcnt(2)\n"
MF "v[112:115],v[20:23],v[196:199],v[112:115]\n"
MF "v[116:119],v[20:23],v[200:203],v[116:119]\n"
"s_waitcnt lgkmcnt(0)\n"
"s_barrier\n"
MF "v[120:123],v[20:23],v[204:207],v[120:123]\n"
"s_add_u32 s72,s72,6144\n"
MF "v[124:127],v[20:23],v[208:211],v[124:127]\n"
"s_cmp_eq_u32 s72,s17\n"
"s_cselect_b32 s72,s16,s72\n"
"s_add_u32 m0,s64,0\n"
MF "v[128:131],v[20:23],v[212:215],v[128:131]\n"
LD "v7,s[32:35],s72 offen nt lds\n"
MF "v[112:115],v[24:27],v[216:219],v[112:115]\n"
"s_add_u32 m0,s64,896\n"
MF "v[116:119],v[24:27],v[220:223],v[116:119]\n"
LD "v7,s[32:35],s72 offen offset:128 nt lds\n"
MF "v[120:123],v[24:27],v[224:227],v[120:123]\n"
"s_add_u32 m0,s64,1792\n"
MF "v[124:127],v[24:27],v[228:231],v[124:127]\n"
LD "v7,s[32:35],s72 offen offset:256 nt lds\n"
MF "v[128:131],v[24:27],v[232:235],v[128:131]\n"
"s_add_u32 s72,s72,6144\n"
"s_add_u32 m0,s64,6144\n"
MF "v[112:115],v[28:31],v[236:239],v[112:115]\n"
LD "v7,s[32:35],s72 offen nt lds\n"
MF "v[116:119],v[28:31],v[240:243],v[116:119]\n"
"s_add_u32 m0,s64,7040\n"
MF "v[120:123],v[28:31],v[244:247],v[120:123]\n"
LD "v7,s[32:35],s72 offen offset:128 nt lds\n"
MF "v[124:127],v[28:31],v[248:251],v[124:127]\n"
"s_add_u32 m0,s64,7936\n"
MF "v[128:131],v[28:31],v[252:255],v[128:131]\n"
LD "v7,s[32:35],s72 offen offset:256 nt lds\n"
"s_branch .Lt0e\n"
".Lt0k:\n"
"s_cbranch_execz .Lipr2\n"
"s_lshl_b32 s58,s56,10\n"
"s_add_u32 s58,s58,73728\n"
"v_add_u32 v32,s58,v9\n"
"v_add_u32 v33,65536,v32\n"
"s_waitcnt lgkmcnt(2)\n"
MF "v[72:75],v[16:19],v[136:139],0\n"
"ds_read_b128 v[28:31],v1 offset:1024\n"
AR "v62,a20\n"
MF "v[76:79],v[16:19],v[140:143],0\n"
"ds_write_b128 v32,a[0:3] offset:24576\n"
MF "v[80:83],v[16:19],v[144:147],0\n"
"ds_write_b128 v32,a[4:7] offset:28672\n"
MF "v[84:87],v[16:19],v[148:151],0\n"
AR "v63,a21\n"
AR "v64,a22\n"
MF "v[88:91],v[16:19],v[152:155],0\n"
"s_waitcnt lgkmcnt(4)\n"
"ds_read_b128 v[16:19],v0 offset:2048\n"
AR "v65,a23\n"
MF "v[72:75],v[20:23],v[156:159],v[72:75]\n"
"ds_write_b128 v32,a[8:11] offset:32768\n"
MF "v[76:79],v[20:23],v[160:163],v[76:79]\n"
AR "v66,a24\n"
AR "v67,a25\n"
MF "v[80:83],v[20:23],v[164:167],v[80:83]\n"
"ds_write_b128 v32,a[12:15] offset:36864\n"
MF "v[84:87],v[20:23],v[168:171],v[84:87]\n"
AR "v68,a26\n"
AR "v69,a27\n"
MF "v[88:91],v[20:23],v[172:175],v[88:91]\n"
"s_waitcnt lgkmcnt(6)\n"
"ds_read_b128 v[20:23],v1 offset:2048\n"
AR "v70,a28\n"
MF "v[72:75],v[24:27],v[176:179],v[72:75]\n"
"ds_write_b128 v32,a[16:19] offset:40960\n"
MF "v[76:79],v[24:27],v[180:183],v[76:79]\n"
AR "v71,a29\n"
AR "v132,a30\n"
MF "v[80:83],v[24:27],v[184:187],v[80:83]\n"
AR "v133,a31\n"
AR "v134,a32\n"
MF "v[84:87],v[24:27],v[188:191],v[84:87]\n"
"ds_write_b128 v32,a[40:43] offset:45056\n"
MF "v[88:91],v[24:27],v[192:195],v[88:91]\n"
"s_waitcnt lgkmcnt(8)\n"
"ds_read_b128 v[24:27],v0 offset:6144\n"
AR "v135,a33\n"
MF "v[72:75],v[28:31],v[196:199],v[72:75]\n"
"ds_write_b128 v32,a[44:47] offset:49152\n"
MF "v[76:79],v[28:31],v[200:203],v[76:79]\n"
AR "v6,a34\n"
AR "v13,a35\n"
MF "v[80:83],v[28:31],v[204:207],v[80:83]\n"
AR "v14,a36\n"
AR "v15,a37\n"
MF "v[84:87],v[28:31],v[208:211],v[84:87]\n"
"ds_write_b128 v32,a[48:51] offset:53248\n"
MF "v[88:91],v[28:31],v[212:215],v[88:91]\n"
"s_waitcnt lgkmcnt(8)\n"
"ds_read_b128 v[28:31],v1 offset:6144\n"
"v_accvgpr_mov_b32 a254,a38\n"
MF "v[72:75],v[16:19],v[216:219],v[72:75]\n"
"v_accvgpr_mov_b32 a255,a39\n"
"v_accvgpr_mov_b32 a240,a168\n"
MF "v[76:79],v[16:19],v[220:223],v[76:79]\n"
"ds_write_b128 v32,a[52:55] offset:57344\n"
MF "v[80:83],v[16:19],v[224:227],v[80:83]\n"
"v_accvgpr_mov_b32 a241,a169\n"
"v_accvgpr_mov_b32 a242,a170\n"
MF "v[84:87],v[16:19],v[228:231],v[84:87]\n"
"ds_write_b128 v32,a[56:59] offset:61440\n"
MF "v[88:91],v[16:19],v[232:235],v[88:91]\n"
"s_waitcnt lgkmcnt(8)\n"
"ds_read_b128 v[16:19],v0 offset:7168\n"
"v_accvgpr_mov_b32 a243,a171\n"
MF "v[72:75],v[20:23],v[236:239],v[72:75]\n"
"v_accvgpr_mov_b32 a244,a172\n"
"v_accvgpr_mov_b32 a245,a173\n"
MF "v[76:79],v[20:23],v[240:243],v[76:79]\n"
"ds_write_b128 v33,a[80:83]\n"
MF "v[80:83],v[20:23],v[244:247],v[80:83]\n"
"v_accvgpr_mov_b32 a246,a174\n"
"v_accvgpr_mov_b32 a247,a175\n"
MF "v[84:87],v[20:23],v[248:251],v[84:87]\n"
"v_accvgpr_mov_b32 a248,a176\n"
"v_accvgpr_mov_b32 a249,a177\n"
MF "v[88:91],v[20:23],v[252:255],v[88:91]\n"
"s_waitcnt lgkmcnt(7)\n"
"ds_read_b128 v[20:23],v1 offset:7168\n"
"v_accvgpr_mov_b32 a250,a178\n"
MF "v[112:115],v[24:27],v[136:139],0\n"
"ds_write_b128 v33,a[84:87] offset:4096\n"
MF "v[116:119],v[24:27],v[140:143],0\n"
"ds_write_b128 v33,a[88:91] offset:8192\n"
MF "v[120:123],v[24:27],v[144:147],0\n"
"v_accvgpr_mov_b32 a251,a179\n"
MF "v[124:127],v[24:27],v[148:151],0\n"
MF "v[128:131],v[24:27],v[152:155],0\n"
"s_waitcnt lgkmcnt(7)\n"
"ds_read_b128 v[24:27],v0 offset:8192\n"
MF "v[112:115],v[28:31],v[156:159],v[112:115]\n"
"v_max3_f32 v52,v72,v73,v74\n"
"v_max_f32 v52,v52,v75\n"
MF "v[116:119],v[28:31],v[160:163],v[116:119]\n"
"v_max3_f32 v53,v76,v77,v78\n"
"v_max_f32 v53,v53,v79\n"
MF "v[120:123],v[28:31],v[164:167],v[120:123]\n"
"v_max3_f32 v54,v80,v81,v82\n"
"v_max_f32 v54,v54,v83\n"
MF "v[124:127],v[28:31],v[168:171],v[124:127]\n"
"v_max3_f32 v55,v84,v85,v86\n"
"v_max_f32 v55,v55,v87\n"
MF "v[128:131],v[28:31],v[172:175],v[128:131]\n"
"s_waitcnt lgkmcnt(5)\n"
"ds_read_b128 v[28:31],v1 offset:8192\n"
"v_max3_f32 v56,v88,v89,v90\n"
MF "v[112:115],v[16:19],v[176:179],v[112:115]\n"
"v_max_f32 v56,v56,v91\n"
MF "v[116:119],v[16:19],v[180:183],v[116:119]\n"
"ds_write_b128 v33,a[92:95] offset:12288\n"
MF "v[120:123],v[16:19],v[184:187],v[120:123]\n"
"ds_write_b128 v33,a[96:99] offset:16384\n"
MF "v[124:127],v[16:19],v[188:191],v[124:127]\n"
"ds_write_b128 v33,a[120:123] offset:20480\n"
MF "v[128:131],v[16:19],v[192:195],v[128:131]\n"
"s_waitcnt lgkmcnt(7)\n"
"ds_write_b128 v32,a[124:127]\n"
MF "v[112:115],v[20:23],v[196:199],v[112:115]\n"
"ds_write_b128 v32,a[128:131] offset:4096\n"
MF "v[116:119],v[20:23],v[200:203],v[116:119]\n"
"s_waitcnt lgkmcnt(0)\n"
"s_barrier\n"
MF "v[120:123],v[20:23],v[204:207],v[120:123]\n"
"s_add_u32 s72,s72,6144\n"
"ds_write_b128 v32,a[132:135] offset:8192\n"
MF "v[124:127],v[20:23],v[208:211],v[124:127]\n"
"s_cmp_eq_u32 s72,s17\n"
"s_cselect_b32 s72,s16,s72\n"
"s_add_u32 m0,s64,0\n"
MF "v[128:131],v[20:23],v[212:215],v[128:131]\n"
LD "v7,s[32:35],s72 offen nt lds\n"
MF "v[112:115],v[24:27],v[216:219],v[112:115]\n"
"s_add_u32 m0,s64,896\n"
"ds_write_b128 v32,a[136:139] offset:12288\n"
MF "v[116:119],v[24:27],v[220:223],v[116:119]\n"
LD "v7,s[32:35],s72 offen offset:128 nt lds\n"
MF "v[120:123],v[24:27],v[224:227],v[120:123]\n"
"s_add_u32 m0,s64,1792\n"
"ds_write_b128 v32,a[160:163] offset:16384\n"
MF "v[124:127],v[24:27],v[228:231],v[124:127]\n"
LD "v7,s[32:35],s72 offen offset:256 nt lds\n"
MF "v[128:131],v[24:27],v[232:235],v[128:131]\n"
"s_add_u32 s72,s72,6144\n"
"s_add_u32 m0,s64,6144\n"
MF "v[112:115],v[28:31],v[236:239],v[112:115]\n"
LD "v7,s[32:35],s72 offen nt lds\n"
MF "v[116:119],v[28:31],v[240:243],v[116:119]\n"
"s_add_u32 m0,s64,7040\n"
"ds_write_b128 v32,a[164:167] offset:20480\n"
MF "v[120:123],v[28:31],v[244:247],v[120:123]\n"
LD "v7,s[32:35],s72 offen offset:128 nt lds\n"
MF "v[124:127],v[28:31],v[248:251],v[124:127]\n"
"s_add_u32 m0,s64,7936\n"
MF "v[128:131],v[28:31],v[252:255],v[128:131]\n"
LD "v7,s[32:35],s72 offen offset:256 nt lds\n"
".Lt0e:\n"
"s_cbranch_execz .Lipr2\n"
"s_nop 1\n"
"v_max3_f32 v52,v52,v112,v113\n"
"v_max3_f32 v53,v53,v116,v117\n"
"v_max3_f32 v54,v54,v120,v121\n"
"s_nop 0\n"
"v_max3_f32 v55,v55,v124,v125\n"
"s_nop 0\n"
"v_max3_f32 v56,v56,v128,v129\n"
"v_max3_f32 v52,v52,v114,v115\n"
"v_max3_f32 v53,v53,v118,v119\n"
"v_max3_f32 v54,v54,v122,v123\n"
"v_max3_f32 v55,v55,v126,v127\n"
"v_max3_f32 v56,v56,v130,v131\n"
"v_mov_b32 v57,v52\n"
"v_mov_b32 v58,v53\n"
"v_mov_b32 v59,v54\n"
"v_mov_b32 v60,v55\n"
"v_mov_b32 v61,v56\n"
"v_permlane16_swap_b32 v52,v57\n"
"v_permlane16_swap_b32 v53,v58\n"
"v_permlane16_swap_b32 v54,v59\n"
"v_permlane16_swap_b32 v55,v60\n"
"v_permlane16_swap_b32 v56,v61\n"
"v_max_f32 v52,v52,v57\n"
"v_max_f32 v53,v53,v58\n"
"v_max_f32 v54,v54,v59\n"
"v_max_f32 v55,v55,v60\n"
"v_max_f32 v56,v56,v61\n"
"v_mov_b32 v57,v52\n"
"v_mov_b32 v58,v53\n"
"v_mov_b32 v59,v54\n"
"v_mov_b32 v60,v55\n"
"v_mov_b32 v61,v56\n"
"v_permlane32_swap_b32 v52,v57\n"
"v_permlane32_swap_b32 v53,v58\n"
"v_permlane32_swap_b32 v54,v59\n"
"v_permlane32_swap_b32 v55,v60\n"
"v_permlane32_swap_b32 v56,v61\n"
"v_max_f32 v52,v52,v57\n"
"v_max_f32 v53,v53,v58\n"
"v_max_f32 v54,v54,v59\n"
"v_max_f32 v55,v55,v60\n"
"v_max_f32_e64 v56,v56,v61\n"
"v_sub_f32 v32,0xc1300000,v52\n"
"v_sub_f32 v36,0xc1300000,v53\n"
"v_sub_f32 v40,0xc1300000,v54\n"
"v_sub_f32 v44,0xc1300000,v55\n"
"v_sub_f32 v48,0xc1300000,v56\n"
"v_mov_b32 v33,v32\n"
"v_mov_b32 v34,v32\n"
"v_mov_b32 v35,v32\n"
"v_mov_b32 v37,v36\n"
"v_mov_b32 v38,v36\n"
"v_mov_b32 v39,v36\n"
"v_mov_b32 v41,v40\n"
"v_mov_b32 v42,v40\n"
"v_mov_b32 v43,v40\n"
"v_mov_b32 v45,v44\n"
"v_mov_b32 v46,v44\n"
"v_mov_b32 v47,v44\n"
"v_mov_b32 v49,v48\n"
"v_mov_b32 v50,v48\n"
"v_mov_b32 v51,v48\n"
"v_add_f32 v72,v72,v32\n"
"v_add_f32 v73,v73,v32\n"
"v_add_f32 v74,v74,v32\n"
"v_add_f32 v75,v75,v32\n"
"v_add_f32 v112,v112,v32\n"
"v_add_f32 v113,v113,v32\n"
"v_add_f32 v114,v114,v32\n"
"v_add_f32 v115,v115,v32\n"
"v_exp_f32 v72,v72\n"
"v_exp_f32 v73,v73\n"
"v_exp_f32 v74,v74\n"
"v_exp_f32 v75,v75\n"
"v_exp_f32 v112,v112\n"
"v_exp_f32 v113,v113\n"
"v_exp_f32 v114,v114\n"
"v_exp_f32 v115,v115\n"
"v_add_f32 v52,v72,v73\n"
"v_add_f32 v52,v52,v74\n"
"v_add_f32 v52,v52,v75\n"
"v_add_f32 v52,v52,v112\n"
"v_add_f32 v52,v52,v113\n"
"v_add_f32 v52,v52,v114\n"
"v_add_f32 v52,v52,v115\n"
CV "v72,v72,v73\n"
CV "v73,v74,v75\n"
CV "v74,v112,v113\n"
CV "v75,v114,v115\n"
"v_add_f32 v76,v76,v36\n"
"v_add_f32 v77,v77,v36\n"
"v_add_f32 v78,v78,v36\n"
"v_add_f32 v79,v79,v36\n"
"v_add_f32 v116,v116,v36\n"
"v_add_f32 v117,v117,v36\n"
"v_add_f32 v118,v118,v36\n"
"v_add_f32 v119,v119,v36\n"
"v_exp_f32 v76,v76\n"
"v_exp_f32 v77,v77\n"
"v_exp_f32 v78,v78\n"
"v_exp_f32 v79,v79\n"
"v_exp_f32 v116,v116\n"
"v_exp_f32 v117,v117\n"
"v_exp_f32 v118,v118\n"
"v_exp_f32 v119,v119\n"
"v_add_f32 v53,v76,v77\n"
"v_add_f32 v53,v53,v78\n"
"v_add_f32 v53,v53,v79\n"
"v_add_f32 v53,v53,v116\n"
"v_add_f32 v53,v53,v117\n"
"v_add_f32 v53,v53,v118\n"
"v_add_f32_e64 v53,v53,v119\n"
CV "v76,v76,v77\n"
CV "v77,v78,v79\n"
CV "v78,v116,v117\n"
CV "v79,v118,v119\n"
"v_add_f32 v80,v80,v40\n"
"v_add_f32 v81,v81,v40\n"
"v_add_f32 v82,v82,v40\n"
"v_add_f32 v83,v83,v40\n"
"v_add_f32 v120,v120,v40\n"
"v_add_f32 v121,v121,v40\n"
"v_add_f32 v122,v122,v40\n"
"v_add_f32 v123,v123,v40\n"
"v_exp_f32 v80,v80\n"
"v_exp_f32 v81,v81\n"
"v_exp_f32 v82,v82\n"
"v_exp_f32 v83,v83\n"
"v_exp_f32 v120,v120\n"
"v_exp_f32 v121,v121\n"
"v_exp_f32 v122,v122\n"
"v_exp_f32 v123,v123\n"
"v_add_f32 v54,v80,v81\n"
"v_add_f32 v54,v54,v82\n"
"v_add_f32 v54,v54,v83\n"
"v_add_f32 v54,v54,v120\n"
"v_add_f32 v54,v54,v121\n"
"v_add_f32 v54,v54,v122\n"
"v_add_f32_e64 v54,v54,v123\n"
CV "v80,v80,v81\n"
CV "v81,v82,v83\n"
CV "v82,v120,v121\n"
CV "v83,v122,v123\n"
"v_add_f32 v84,v84,v44\n"
"v_add_f32 v85,v85,v44\n"
"v_add_f32 v86,v86,v44\n"
"v_add_f32 v87,v87,v44\n"
"v_add_f32 v124,v124,v44\n"
"v_add_f32 v125,v125,v44\n"
"v_add_f32 v126,v126,v44\n"
"v_add_f32 v127,v127,v44\n"
"v_mov_b32 v55,0\n"
"v_add_f32 v88,v88,v48\n"
"v_add_f32 v89,v89,v48\n"
"v_add_f32 v90,v90,v48\n"
"v_add_f32 v91,v91,v48\n"
"v_add_f32 v128,v128,v48\n"
"v_add_f32 v129,v129,v48\n"
"v_add_f32 v130,v130,v48\n"
"v_add_f32 v131,v131,v48\n"
"v_mov_b32 v56,0\n"
"s_waitcnt vmcnt(12) lgkmcnt(0)\n"
"s_barrier\n"
"ds_read_b128 v[16:19],v0 offset:24576\n"
"ds_read_b128 v[20:23],v1 offset:24576\n"
"ds_read_b128 v[24:27],v0 offset:25600\n"
AW "a220,s13\n"
AW "a221,s13\n"
AW "a222,s13\n"
AW "a223,s13\n"
"s_waitcnt lgkmcnt(2)\n"
MF "v[92:95],v[16:19],v[136:139],v[32:35]\n"
"ds_read_b128 v[28:31],v1 offset:25600\n"
"s_waitcnt lgkmcnt(2)\n"
MF "v[92:95],v[20:23],v[156:159],v[92:95]\n"
"v_exp_f32 v84,v84\n"
MF "v[96:99],v[20:23],v[160:163],v[36:39]\n"
"v_exp_f32 v85,v85\n"
MF "v[96:99],v[16:19],v[140:143],v[96:99]\n"
"v_exp_f32 v86,v86\n"
MF "v[100:103],v[16:19],v[144:147],v[40:43]\n"
"v_exp_f32 v87,v87\n"
MF "v[100:103],v[20:23],v[164:167],v[100:103]\n"
"v_add_f32 v55,v55,v84\n"
"v_add_f32 v55,v55,v85\n"
MF "v[104:107],v[20:23],v[168:171],v[44:47]\n"
CV "v84,v84,v85\n"
"v_add_f32 v55,v55,v86\n"
MF "v[104:107],v[16:19],v[148:151],v[104:107]\n"
"v_add_f32 v55,v55,v87\n"
CV "v85,v86,v87\n"
MF "v[108:111],v[16:19],v[152:155],v[48:51]\n"
"v_exp_f32 v124,v124\n"
MF "v[108:111],v[20:23],v[172:175],v[108:111]\n"
"s_waitcnt lgkmcnt(0)\n"
"ds_read_b128 v[16:19],v0 offset:26624\n"
"ds_read_b128 v[20:23],v1 offset:26624\n"
MF "v[108:111],v[24:27],v[192:195],v[108:111]\n"
"v_exp_f32 v125,v125\n"
MF "v[108:111],v[28:31],v[212:215],v[108:111]\n"
"v_exp_f32 v126,v126\n"
MF "v[104:107],v[28:31],v[208:211],v[104:107]\n"
"v_exp_f32 v127,v127\n"
MF "v[104:107],v[24:27],v[188:191],v[104:107]\n"
"v_add_f32 v55,v55,v124\n"
"v_add_f32 v55,v55,v125\n"
MF "v[100:103],v[24:27],v[184:187],v[100:103]\n"
CV "v86,v124,v125\n"
"v_add_f32 v55,v55,v126\n"
MF "v[100:103],v[28:31],v[204:207],v[100:103]\n"
"v_add_f32 v55,v55,v127\n"
CV "v87,v126,v127\n"
MF "v[96:99],v[28:31],v[200:203],v[96:99]\n"
"v_exp_f32 v88,v88\n"
MF "v[96:99],v[24:27],v[180:183],v[96:99]\n"
"v_exp_f32 v89,v89\n"
MF "v[92:95],v[24:27],v[176:179],v[92:95]\n"
"v_exp_f32 v90,v90\n"
MF "v[92:95],v[28:31],v[196:199],v[92:95]\n"
"s_waitcnt lgkmcnt(0)\n"
"ds_read_b128 v[24:27],v0 offset:30720\n"
"ds_read_b128 v[28:31],v1 offset:30720\n"
MF "v[92:95],v[16:19],v[216:219],v[92:95]\n"
"v_exp_f32 v91,v91\n"
MF "v[92:95],v[20:23],v[236:239],v[92:95]\n"
"v_add_f32 v56,v56,v88\n"
"v_add_f32 v56,v56,v89\n"
MF "v[96:99],v[20:23],v[240:243],v[96:99]\n"
CV "v88,v88,v89\n"
"v_add_f32 v56,v56,v90\n"
MF "v[96:99],v[16:19],v[220:223],v[96:99]\n"
"v_add_f32 v56,v56,v91\n"
CV "v89,v90,v91\n"
MF "v[100:103],v[16:19],v[224:227],v[100:103]\n"
"v_exp_f32 v128,v128\n"
MF "v[100:103],v[20:23],v[244:247],v[100:103]\n"
"v_exp_f32 v129,v129\n"
MF "v[104:107],v[20:23],v[248:251],v[104:107]\n"
"v_exp_f32 v130,v130\n"
MF "v[104:107],v[16:19],v[228:231],v[104:107]\n"
"v_exp_f32 v131,v131\n"
MF "v[108:111],v[16:19],v[232:235],v[108:111]\n"
"v_add_f32 v56,v56,v128\n"
"v_add_f32 v56,v56,v129\n"
MF "v[108:111],v[20:23],v[252:255],v[108:111]\n"
"s_waitcnt lgkmcnt(0)\n"
"ds_read_b128 v[16:19],v0 offset:31744\n"
"ds_read_b128 v[20:23],v1 offset:31744\n"
MF "v[112:115],v[24:27],v[136:139],v[32:35]\n"
CV "v90,v128,v129\n"
"v_add_f32 v56,v56,v130\n"
MF "v[112:115],v[28:31],v[156:159],v[112:115]\n"
"v_add_f32 v56,v56,v131\n"
CV "v91,v130,v131\n"
MF "v[116:119],v[28:31],v[160:163],v[36:39]\n"
"v_exp_f32 v92,v92\n"
MF "v[116:119],v[24:27],v[140:143],v[116:119]\n"
"v_exp_f32 v93,v93\n"
MF "v[120:123],v[24:27],v[144:147],v[40:43]\n"
"v_exp_f32 v94,v94\n"
MF "v[120:123],v[28:31],v[164:167],v[120:123]\n"
"v_exp_f32 v95,v95\n"
MF "v[124:127],v[28:31],v[168:171],v[44:47]\n"
"v_add_f32 v57,v52,v92\n"
"v_add_f32 v57,v57,v93\n"
MF "v[124:127],v[24:27],v[148:151],v[124:127]\n"
CV "v92,v92,v93\n"
"v_add_f32 v57,v57,v94\n"
MF "v[128:131],v[24:27],v[152:155],v[48:51]\n"
"v_add_f32 v57,v57,v95\n"
CV "v93,v94,v95\n"
MF "v[128:131],v[28:31],v[172:175],v[128:131]\n"
"s_waitcnt lgkmcnt(0)\n"
"ds_read_b128 v[24:27],v0 offset:32768\n"
"ds_read_b128 v[28:31],v1 offset:32768\n"
MF "v[128:131],v[16:19],v[192:195],v[128:131]\n"
"v_exp_f32 v96,v96\n"
MF "v[128:131],v[20:23],v[212:215],v[128:131]\n"
"v_exp_f32 v97,v97\n"
MF "v[124:127],v[20:23],v[208:211],v[124:127]\n"
"v_exp_f32 v98,v98\n"
MF "v[124:127],v[16:19],v[188:191],v[124:127]\n"
"v_exp_f32 v99,v99\n"
MF "v[120:123],v[16:19],v[184:187],v[120:123]\n"
"v_add_f32 v58,v53,v96\n"
"v_add_f32 v58,v58,v97\n"
MF "v[120:123],v[20:23],v[204:207],v[120:123]\n"
CV "v96,v96,v97\n"
"v_add_f32 v58,v58,v98\n"
MF "v[116:119],v[20:23],v[200:203],v[116:119]\n"
"v_add_f32 v58,v58,v99\n"
CV "v97,v98,v99\n"
MF "v[116:119],v[16:19],v[180:183],v[116:119]\n"
"s_waitcnt vmcnt(6) lgkmcnt(0)\n"
"s_barrier\n"
MF "v[112:115],v[16:19],v[176:179],v[112:115]\n"
"v_exp_f32 v100,v100\n"
MF "v[112:115],v[20:23],v[196:199],v[112:115]\n"
"s_nop 0\n"
TR "v[16:17],v2 offset:49152\n"
TR "v[18:19],v2 offset:55296\n"
MF "v[112:115],v[24:27],v[216:219],v[112:115]\n"
"s_add_u32 s73,s73,12288\n"
TR "v[20:21],v3 offset:49152\n"
TR "v[22:23],v3 offset:55296\n"
MF "v[112:115],v[28:31],v[236:239],v[112:115]\n"
"s_cmp_eq_u32 s73,s19\n"
"s_cselect_b32 s73,s18,s73\n"
"s_add_u32 m0,s65,24576\n"
MF "v[116:119],v[28:31],v[240:243],v[116:119]\n"
LD "v8,s[36:39],s73 offen nt lds\n"
MF "v[116:119],v[24:27],v[220:223],v[116:119]\n"
"s_add_u32 m0,s65,25472\n"
"v_exp_f32 v101,v101\n"
MF "v[120:123],v[24:27],v[224:227],v[120:123]\n"
LD "v8,s[36:39],s73 offen offset:128 nt lds\n"
MF "v[120:123],v[28:31],v[244:247],v[120:123]\n"
"v_exp_f32 v102,v102\n"
MF "v[124:127],v[28:31],v[248:251],v[124:127]\n"
"s_add_u32 m0,s65,26368\n"
"v_exp_f32 v103,v103\n"
MF "v[124:127],v[24:27],v[228:231],v[124:127]\n"
LD "v8,s[36:39],s73 offen offset:256 nt lds\n"
MF "v[128:131],v[24:27],v[232:235],v[128:131]\n"
"v_add_f32 v59,v54,v100\n"
"v_add_f32 v59,v59,v101\n"
MF "v[128:131],v[28:31],v[252:255],v[128:131]\n"
"s_waitcnt lgkmcnt(2)\n"
TR "v[24:25],v4 offset:49152\n"
TR "v[26:27],v4 offset:55296\n"
MF "a[0:3],v[16:19],v[72:75],a[220:223]\n"
"s_add_u32 m0,s65,24576\n"
TR "v[28:29],v5 offset:49152\n"
TR "v[30:31],v5 offset:55296\n"
MF "a[4:7],v[16:19],v[76:79],a[220:223]\n"
LD "v8,s[36:39],s73 offen offset:3072 nt lds\n"
MF "a[8:11],v[16:19],v[80:83],a[220:223]\n"
"s_add_u32 m0,s65,25472\n"
CV "v100,v100,v101\n"
"v_add_f32 v59,v59,v102\n"
MF "a[12:15],v[16:19],v[84:87],a[220:223]\n"
LD "v8,s[36:39],s73 offen offset:3200 nt lds\n"
MF "a[16:19],v[16:19],v[88:91],a[220:223]\n"
"s_waitcnt lgkmcnt(4)\n"
TR "v[16:17],v2 offset:50176\n"
TR "v[18:19],v2 offset:56320\n"
MF "a[36:39],v[20:23],v[88:91],a[220:223]\n"
"v_add_f32 v59,v59,v103\n"
CV "v101,v102,v103\n"
MF "a[32:35],v[20:23],v[84:87],a[220:223]\n"
"s_add_u32 m0,s65,26368\n"
"v_exp_f32 v104,v104\n"
MF "a[28:31],v[20:23],v[80:83],a[220:223]\n"
LD "v8,s[36:39],s73 offen offset:3328 nt lds\n"
MF "a[24:27],v[20:23],v[76:79],a[220:223]\n"
"v_exp_f32 v105,v105\n"
MF "a[20:23],v[20:23],v[72:75],a[220:223]\n"
"s_waitcnt lgkmcnt(4)\n"
TR "v[20:21],v3 offset:50176\n"
TR "v[22:23],v3 offset:56320\n"
MF "a[40:43],v[24:27],v[72:75],a[220:223]\n"
"v_exp_f32 v106,v106\n"
MF "a[44:47],v[24:27],v[76:79],a[220:223]\n"
"v_exp_f32 v107,v107\n"
MF "a[48:51],v[24:27],v[80:83],a[220:223]\n"
"v_add_f32 v60,v55,v104\n"
"v_add_f32 v60,v60,v105\n"
MF "a[52:55],v[24:27],v[84:87],a[220:223]\n"
CV "v104,v104,v105\n"
"v_add_f32 v60,v60,v106\n"
MF "a[56:59],v[24:27],v[88:91],a[220:223]\n"
"s_waitcnt lgkmcnt(4)\n"
TR "v[24:25],v4 offset:50176\n"
TR "v[26:27],v4 offset:56320\n"
MF "a[76:79],v[28:31],v[88:91],a[220:223]\n"
"v_add_f32 v60,v60,v107\n"
CV "v105,v106,v107\n"
MF "a[72:75],v[28:31],v[84:87],a[220:223]\n"
"v_exp_f32 v108,v108\n"
MF "a[68:71],v[28:31],v[80:83],a[220:223]\n"
"v_exp_f32 v109,v109\n"
MF "a[64:67],v[28:31],v[76:79],a[220:223]\n"
"v_exp_f32 v110,v110\n"
MF "a[60:63],v[28:31],v[72:75],a[220:223]\n"
"s_waitcnt lgkmcnt(4)\n"
TR "v[28:29],v5 offset:50176\n"
TR "v[30:31],v5 offset:56320\n"
MF "a[80:83],v[16:19],v[72:75],a[220:223]\n"
"v_exp_f32 v111,v111\n"
MF "a[84:87],v[16:19],v[76:79],a[220:223]\n"
"v_add_f32 v61,v56,v108\n"
"v_add_f32 v61,v61,v109\n"
MF "a[88:91],v[16:19],v[80:83],a[220:223]\n"
CV "v108,v108,v109\n"
"v_add_f32 v61,v61,v110\n"
MF "a[92:95],v[16:19],v[84:87],a[220:223]\n"
"v_add_f32 v61,v61,v111\n"
CV "v109,v110,v111\n"
MF "a[96:99],v[16:19],v[88:91],a[220:223]\n"
"s_waitcnt lgkmcnt(4)\n"
TR "v[16:17],v2 offset:51200\n"
TR "v[18:19],v2 offset:57344\n"
MF "a[116:119],v[20:23],v[88:91],a[220:223]\n"
"v_exp_f32 v112,v112\n"
MF "a[112:115],v[20:23],v[84:87],a[220:223]\n"
"v_exp_f32 v113,v113\n"
MF "a[108:111],v[20:23],v[80:83],a[220:223]\n"
"v_exp_f32 v114,v114\n"
MF "a[104:107],v[20:23],v[76:79],a[220:223]\n"
"v_exp_f32 v115,v115\n"
MF "a[100:103],v[20:23],v[72:75],a[220:223]\n"
"s_waitcnt lgkmcnt(4)\n"
TR "v[20:21],v3 offset:51200\n"
TR "v[22:23],v3 offset:57344\n"
MF "a[120:123],v[24:27],v[72:75],a[220:223]\n"
"v_add_f32 v57,v57,v112\n"
"v_add_f32 v57,v57,v113\n"
MF "a[124:127],v[24:27],v[76:79],a[220:223]\n"
CV "v94,v112,v113\n"
"v_add_f32 v57,v57,v114\n"
MF "a[128:131],v[24:27],v[80:83],a[220:223]\n"
"v_add_f32 v57,v57,v115\n"
CV "v95,v114,v115\n"
MF "a[132:135],v[24:27],v[84:87],a[220:223]\n"
"v_exp_f32 v116,v116\n"
MF "a[136:139],v[24:27],v[88:91],a[220:223]\n"
"s_waitcnt lgkmcnt(4)\n"
TR "v[24:25],v4 offset:51200\n"
TR "v[26:27],v4 offset:57344\n"
MF "a[156:159],v[28:31],v[88:91],a[220:223]\n"
"v_exp_f32 v117,v117\n"
MF "a[152:155],v[28:31],v[84:87],a[220:223]\n"
"v_exp_f32 v118,v118\n"
MF "a[148:151],v[28:31],v[80:83],a[220:223]\n"
"v_exp_f32 v119,v119\n"
MF "a[144:147],v[28:31],v[76:79],a[220:223]\n"
"v_add_f32 v58,v58,v116\n"
"v_add_f32 v58,v58,v117\n"
MF "a[140:143],v[28:31],v[72:75],a[220:223]\n"
"s_waitcnt lgkmcnt(4)\n"
TR "v[28:29],v5 offset:51200\n"
TR "v[30:31],v5 offset:57344\n"
MF "a[160:163],v[16:19],v[72:75],a[220:223]\n"
CV "v98,v116,v117\n"
"v_add_f32 v58,v58,v118\n"
MF "a[164:167],v[16:19],v[76:79],a[220:223]\n"
"v_add_f32 v58,v58,v119\n"
CV "v99,v118,v119\n"
MF "a[168:171],v[16:19],v[80:83],a[220:223]\n"
"v_exp_f32 v120,v120\n"
MF "a[172:175],v[16:19],v[84:87],a[220:223]\n"
"v_exp_f32 v121,v121\n"
MF "a[176:179],v[16:19],v[88:91],a[220:223]\n"
"s_waitcnt lgkmcnt(4)\n"
"v_exp_f32 v122,v122\n"
MF "a[196:199],v[20:23],v[88:91],a[220:223]\n"
"v_exp_f32 v123,v123\n"
MF "a[192:195],v[20:23],v[84:87],a[220:223]\n"
"v_add_f32 v59,v59,v120\n"
"v_add_f32 v59,v59,v121\n"
MF "a[188:191],v[20:23],v[80:83],a[220:223]\n"
CV "v102,v120,v121\n"
"v_add_f32 v59,v59,v122\n"
MF "a[184:187],v[20:23],v[76:79],a[220:223]\n"
"s_waitcnt vmcnt(6) lgkmcnt(0)\n"
"s_barrier\n"
MF "a[180:183],v[20:23],v[72:75],a[220:223]\n"
"s_add_u32 s72,s72,6144\n"
"ds_read_b128 v[16:19],v0\n"
"ds_read_b128 v[20:23],v1\n"
MF "a[200:203],v[24:27],v[72:75],a[220:223]\n"
"s_cmp_eq_u32 s72,s17\n"
"s_cselect_b32 s72,s16,s72\n"
"s_add_u32 m0,s64,49152\n"
MF "a[204:207],v[24:27],v[76:79],a[220:223]\n"
LD "v7,s[32:35],s72 offen nt lds\n"
MF "a[208:211],v[24:27],v[80:83],a[220:223]\n"
"s_add_u32 m0,s64,50048\n"
"v_add_f32 v59,v59,v123\n"
CV "v103,v122,v123\n"
MF "a[212:215],v[24:27],v[84:87],a[220:223]\n"
LD "v7,s[32:35],s72 offen offset:128 nt lds\n"
MF "a[216:219],v[24:27],v[88:91],a[220:223]\n"
"s_add_u32 m0,s64,50944\n"
"ds_read_b128 v[24:27],v0 offset:1024\n"
MF "a[236:239],v[28:31],v[88:91],a[220:223]\n"
LD "v7,s[32:35],s72 offen offset:256 nt lds\n"
MF "a[232:235],v[28:31],v[84:87],a[220:223]\n"
MF "a[228:231],v[28:31],v[80:83],a[220:223]\n"
MF "a[224:227],v[28:31],v[76:79],a[220:223]\n"
MF "a[220:223],v[28:31],v[72:75],a[220:223]\n"
"s_waitcnt lgkmcnt(1)\n"
"s_branch .Lmid\n"
".Laloop:\n"
"s_cbranch_execz .Lipr2\n"
MF "v[72:75],v[16:19],v[136:139],v[32:35]\n"
"ds_read_b128 v[28:31],v1 offset:25600\n"
MF "v[72:75],v[20:23],v[156:159],v[72:75]\n"
MF "v[76:79],v[20:23],v[160:163],v[36:39]\n"
MF "v[76:79],v[16:19],v[140:143],v[76:79]\n"
MF "v[80:83],v[16:19],v[144:147],v[40:43]\n"
MF "v[80:83],v[20:23],v[164:167],v[80:83]\n"
MF "v[84:87],v[20:23],v[168:171],v[44:47]\n"
MF "v[84:87],v[16:19],v[148:151],v[84:87]\n"
"s_add_u32 s72,s72,6144\n"
"s_add_u32 m0,s64,6144\n"
MF "v[88:91],v[16:19],v[152:155],v[48:51]\n"
LD "v7,s[32:35],s72 offen nt lds\n"
MF "v[88:91],v[20:23],v[172:175],v[88:91]\n"
"s_waitcnt lgkmcnt(0)\n"
"ds_read_b128 v[16:19],v0 offset:26624\n"
"ds_read_b128 v[20:23],v1 offset:26624\n"
MF "v[88:91],v[24:27],v[192:195],v[88:91]\n"
"v_exp_f32 v124,v124\n"
MF "v[88:91],v[28:31],v[212:215],v[88:91]\n"
"s_add_u32 m0,s64,7040\n"
"v_exp_f32 v125,v125\n"
MF "v[84:87],v[28:31],v[208:211],v[84:87]\n"
LD "v7,s[32:35],s72 offen offset:128 nt lds\n"
MF "v[84:87],v[24:27],v[188:191],v[84:87]\n"
"v_exp_f32 v126,v126\n"
MF "v[80:83],v[24:27],v[184:187],v[80:83]\n"
"v_exp_f32 v127,v127\n"
MF "v[80:83],v[28:31],v[204:207],v[80:83]\n"
"s_add_u32 m0,s64,7936\n"
"v_add_f32 v60,v60,v124\n"
"v_add_f32 v60,v60,v125\n"
MF "v[76:79],v[28:31],v[200:203],v[76:79]\n"
LD "v7,s[32:35],s72 offen offset:256 nt lds\n"
MF "v[76:79],v[24:27],v[180:183],v[76:79]\n"
CV "v106,v124,v125\n"
"v_add_f32 v60,v60,v126\n"
MF "v[72:75],v[24:27],v[176:179],v[72:75]\n"
"v_add_f32 v60,v60,v127\n"
CV "v107,v126,v127\n"
MF "v[72:75],v[28:31],v[196:199],v[72:75]\n"
"s_waitcnt lgkmcnt(0)\n"
"ds_read_b128 v[24:27],v0 offset:30720\n"
"ds_read_b128 v[28:31],v1 offset:30720\n"
MF "v[72:75],v[16:19],v[216:219],v[72:75]\n"
"v_exp_f32 v128,v128\n"
MF "v[72:75],v[20:23],v[236:239],v[72:75]\n"
"v_exp_f32 v129,v129\n"
MF "v[76:79],v[20:23],v[240:243],v[76:79]\n"
"v_exp_f32 v130,v130\n"
MF "v[76:79],v[16:19],v[220:223],v[76:79]\n"
"v_exp_f32 v131,v131\n"
MF "v[80:83],v[16:19],v[224:227],v[80:83]\n"
"v_add_f32 v61,v61,v128\n"
"v_add_f32 v61,v61,v129\n"
MF "v[80:83],v[20:23],v[244:247],v[80:83]\n"
CV "v110,v128,v129\n"
"v_add_f32 v61,v61,v130\n"
MF "v[84:87],v[20:23],v[248:251],v[84:87]\n"
"v_add_f32 v61,v61,v131\n"
CV "v111,v130,v131\n"
MF "v[84:87],v[16:19],v[228:231],v[84:87]\n"
"v_max3_f32 v10,v57,v58,v59\n"
"v_max3_f32 v10,v10,v60,v61\n"
MF "v[88:91],v[16:19],v[232:235],v[88:91]\n"
"v_cmp_ngt_f32 vcc,s77,v10\n"
MF "v[88:91],v[20:23],v[252:255],v[88:91]\n"
"s_waitcnt lgkmcnt(0)\n"
"s_cbranch_vccnz .Lstub_11\n"
".Lback_11:\n"
MF "v[112:115],v[24:27],v[136:139],v[32:35]\n"
"s_add_u32 s74,s74,12288\n"
"ds_read_b128 v[16:19],v0 offset:31744\n"
"ds_read_b128 v[20:23],v1 offset:31744\n"
MF "v[112:115],v[28:31],v[156:159],v[112:115]\n"
"s_cmp_eq_u32 s74,s20\n"
"s_cselect_b32 s74,s70,s74\n"
MF "v[116:119],v[28:31],v[160:163],v[36:39]\n"
"v_exp_f32 v72,v72\n"
MF "v[116:119],v[24:27],v[140:143],v[116:119]\n"
"v_exp_f32 v73,v73\n"
MF "v[120:123],v[24:27],v[144:147],v[40:43]\n"
"v_exp_f32 v74,v74\n"
MF "v[120:123],v[28:31],v[164:167],v[120:123]\n"
"v_exp_f32 v75,v75\n"
MF "v[124:127],v[28:31],v[168:171],v[44:47]\n"
"v_add_f32 v52,v57,v72\n"
"v_add_f32 v52,v52,v73\n"
MF "v[124:127],v[24:27],v[148:151],v[124:127]\n"
CV "v72,v72,v73\n"
"v_add_f32 v52,v52,v74\n"
MF "v[128:131],v[24:27],v[152:155],v[48:51]\n"
"v_add_f32 v52,v52,v75\n"
CV "v73,v74,v75\n"
MF "v[128:131],v[28:31],v[172:175],v[128:131]\n"
"s_waitcnt lgkmcnt(0)\n"
"ds_read_b128 v[24:27],v0 offset:32768\n"
"ds_read_b128 v[28:31],v1 offset:32768\n"
MF "v[128:131],v[16:19],v[192:195],v[128:131]\n"
"v_exp_f32 v76,v76\n"
MF "v[128:131],v[20:23],v[212:215],v[128:131]\n"
"v_exp_f32 v77,v77\n"
MF "v[124:127],v[20:23],v[208:211],v[124:127]\n"
"v_exp_f32 v78,v78\n"
MF "v[124:127],v[16:19],v[188:191],v[124:127]\n"
"v_exp_f32 v79,v79\n"
MF "v[120:123],v[16:19],v[184:187],v[120:123]\n"
"v_add_f32 v53,v58,v76\n"
"v_add_f32 v53,v53,v77\n"
MF "v[120:123],v[20:23],v[204:207],v[120:123]\n"
CV "v76,v76,v77\n"
"v_add_f32 v53,v53,v78\n"
MF "v[116:119],v[20:23],v[200:203],v[116:119]\n"
"v_add_f32 v53,v53,v79\n"
CV "v77,v78,v79\n"
MF "v[116:119],v[16:19],v[180:183],v[116:119]\n"
"s_waitcnt vmcnt(6) lgkmcnt(0)\n"
"s_barrier\n"
MF "v[112:115],v[16:19],v[176:179],v[112:115]\n"
"v_exp_f32 v80,v80\n"
MF "v[112:115],v[20:23],v[196:199],v[112:115]\n"
"s_nop 0\n"
TR "v[16:17],v2 offset:49152\n"
TR "v[18:19],v2 offset:55296\n"
MF "v[112:115],v[24:27],v[216:219],v[112:115]\n"
"s_add_u32 s73,s73,12288\n"
TR "v[20:21],v3 offset:49152\n"
TR "v[22:23],v3 offset:55296\n"
MF "v[112:115],v[28:31],v[236:239],v[112:115]\n"
"s_cmp_eq_u32 s73,s19\n"
"s_cselect_b32 s73,s18,s73\n"
"s_add_u32 m0,s65,24576\n"
MF "v[116:119],v[28:31],v[240:243],v[116:119]\n"
LD "v8,s[36:39],s73 offen nt lds\n"
MF "v[116:119],v[24:27],v[220:223],v[116:119]\n"
"s_add_u32 m0,s65,25472\n"
"v_exp_f32 v81,v81\n"
MF "v[120:123],v[24:27],v[224:227],v[120:123]\n"
LD "v8,s[36:39],s73 offen offset:128 nt lds\n"
MF "v[120:123],v[28:31],v[244:247],v[120:123]\n"
"v_exp_f32 v82,v82\n"
MF "v[124:127],v[28:31],v[248:251],v[124:127]\n"
"s_add_u32 m0,s65,26368\n"
"v_exp_f32 v83,v83\n"
MF "v[124:127],v[24:27],v[228:231],v[124:127]\n"
LD "v8,s[36:39],s73 offen offset:256 nt lds\n"
MF "v[128:131],v[24:27],v[232:235],v[128:131]\n"
"v_add_f32 v54,v59,v80\n"
"v_add_f32 v54,v54,v81\n"
MF "v[128:131],v[28:31],v[252:255],v[128:131]\n"
"s_waitcnt lgkmcnt(2)\n"
TR "v[24:25],v4 offset:49152\n"
TR "v[26:27],v4 offset:55296\n"
MF "a[0:3],v[16:19],v[92:95],a[0:3]\n"
"s_add_u32 m0,s65,24576\n"
TR "v[28:29],v5 offset:49152\n"
TR "v[30:31],v5 offset:55296\n"
MF "a[4:7],v[16:19],v[96:99],a[4:7]\n"
LD "v8,s[36:39],s73 offen offset:3072 nt lds\n"
MF "a[8:11],v[16:19],v[100:103],a[8:11]\n"
"s_add_u32 m0,s65,25472\n"
CV "v80,v80,v81\n"
"v_add_f32 v54,v54,v82\n"
MF "a[12:15],v[16:19],v[104:107],a[12:15]\n"
LD "v8,s[36:39],s73 offen offset:3200 nt lds\n"
MF "a[16:19],v[16:19],v[108:111],a[16:19]\n"
"s_waitcnt lgkmcnt(4)\n"
TR "v[16:17],v2 offset:50176\n"
TR "v[18:19],v2 offset:56320\n"
MF "a[36:39],v[20:23],v[108:111],a[36:39]\n"
"v_add_f32 v54,v54,v83\n"
CV "v81,v82,v83\n"
MF "a[32:35],v[20:23],v[104:107],a[32:35]\n"
"s_add_u32 m0,s65,26368\n"
"v_exp_f32 v84,v84\n"
MF "a[28:31],v[20:23],v[100:103],a[28:31]\n"
LD "v8,s[36:39],s73 offen offset:3328 nt lds\n"
MF "a[24:27],v[20:23],v[96:99],a[24:27]\n"
"v_exp_f32 v85,v85\n"
MF "a[20:23],v[20:23],v[92:95],a[20:23]\n"
"s_waitcnt lgkmcnt(4)\n"
TR "v[20:21],v3 offset:50176\n"
TR "v[22:23],v3 offset:56320\n"
MF "a[40:43],v[24:27],v[92:95],a[40:43]\n"
"v_exp_f32 v86,v86\n"
MF "a[44:47],v[24:27],v[96:99],a[44:47]\n"
"v_exp_f32 v87,v87\n"
MF "a[48:51],v[24:27],v[100:103],a[48:51]\n"
"v_add_f32 v55,v60,v84\n"
"v_add_f32 v55,v55,v85\n"
MF "a[52:55],v[24:27],v[104:107],a[52:55]\n"
CV "v84,v84,v85\n"
"v_add_f32 v55,v55,v86\n"
MF "a[56:59],v[24:27],v[108:111],a[56:59]\n"
"s_waitcnt lgkmcnt(4)\n"
TR "v[24:25],v4 offset:50176\n"
TR "v[26:27],v4 offset:56320\n"
MF "a[76:79],v[28:31],v[108:111],a[76:79]\n"
"v_add_f32 v55,v55,v87\n"
CV "v85,v86,v87\n"
MF "a[72:75],v[28:31],v[104:107],a[72:75]\n"
"v_exp_f32 v88,v88\n"
MF "a[68:71],v[28:31],v[100:103],a[68:71]\n"
"v_exp_f32 v89,v89\n"
MF "a[64:67],v[28:31],v[96:99],a[64:67]\n"
"v_exp_f32 v90,v90\n"
MF "a[60:63],v[28:31],v[92:95],a[60:63]\n"
"s_waitcnt lgkmcnt(4)\n"
TR "v[28:29],v5 offset:50176\n"
TR "v[30:31],v5 offset:56320\n"
MF "a[80:83],v[16:19],v[92:95],a[80:83]\n"
"v_exp_f32 v91,v91\n"
MF "a[84:87],v[16:19],v[96:99],a[84:87]\n"
"v_add_f32 v56,v61,v88\n"
"v_add_f32 v56,v56,v89\n"
MF "a[88:91],v[16:19],v[100:103],a[88:91]\n"
CV "v88,v88,v89\n"
"v_add_f32 v56,v56,v90\n"
MF "a[92:95],v[16:19],v[104:107],a[92:95]\n"
"v_add_f32 v56,v56,v91\n"
CV "v89,v90,v91\n"
MF "a[96:99],v[16:19],v[108:111],a[96:99]\n"
"s_waitcnt lgkmcnt(4)\n"
TR "v[16:17],v2 offset:51200\n"
TR "v[18:19],v2 offset:57344\n"
MF "a[116:119],v[20:23],v[108:111],a[116:119]\n"
"v_exp_f32 v112,v112\n"
MF "a[112:115],v[20:23],v[104:107],a[112:115]\n"
"v_exp_f32 v113,v113\n"
MF "a[108:111],v[20:23],v[100:103],a[108:111]\n"
"v_exp_f32 v114,v114\n"
MF "a[104:107],v[20:23],v[96:99],a[104:107]\n"
"v_exp_f32 v115,v115\n"
MF "a[100:103],v[20:23],v[92:95],a[100:103]\n"
"s_waitcnt lgkmcnt(4)\n"
TR "v[20:21],v3 offset:51200\n"
TR "v[22:23],v3 offset:57344\n"
MF "a[120:123],v[24:27],v[92:95],a[120:123]\n"
"v_add_f32 v52,v52,v112\n"
"v_add_f32 v52,v52,v113\n"
MF "a[124:127],v[24:27],v[96:99],a[124:127]\n"
CV "v74,v112,v113\n"
"v_add_f32 v52,v52,v114\n"
MF "a[128:131],v[24:27],v[100:103],a[128:131]\n"
"v_add_f32 v52,v52,v115\n"
CV "v75,v114,v115\n"
MF "a[132:135],v[24:27],v[104:107],a[132:135]\n"
"v_exp_f32 v116,v116\n"
MF "a[136:139],v[24:27],v[108:111],a[136:139]\n"
"s_waitcnt lgkmcnt(4)\n"
TR "v[24:25],v4 offset:51200\n"
TR "v[26:27],v4 offset:57344\n"
MF "a[156:159],v[28:31],v[108:111],a[156:159]\n"
"v_exp_f32 v117,v117\n"
MF "a[152:155],v[28:31],v[104:107],a[152:155]\n"
"v_exp_f32 v118,v118\n"
MF "a[148:151],v[28:31],v[100:103],a[148:151]\n"
"v_exp_f32 v119,v119\n"
MF "a[144:147],v[28:31],v[96:99],a[144:147]\n"
"v_add_f32 v53,v53,v116\n"
"v_add_f32 v53,v53,v117\n"
MF "a[140:143],v[28:31],v[92:95],a[140:143]\n"
"s_waitcnt lgkmcnt(4)\n"
TR "v[28:29],v5 offset:51200\n"
TR "v[30:31],v5 offset:57344\n"
MF "a[160:163],v[16:19],v[92:95],a[160:163]\n"
CV "v78,v116,v117\n"
"v_add_f32 v53,v53,v118\n"
MF "a[164:167],v[16:19],v[96:99],a[164:167]\n"
"v_add_f32 v53,v53,v119\n"
CV "v79,v118,v119\n"
MF "a[168:171],v[16:19],v[100:103],a[168:171]\n"
"v_exp_f32 v120,v120\n"
MF "a[172:175],v[16:19],v[104:107],a[172:175]\n"
"v_exp_f32 v121,v121\n"
MF "a[176:179],v[16:19],v[108:111],a[176:179]\n"
"s_waitcnt lgkmcnt(4)\n"
"v_exp_f32 v122,v122\n"
MF "a[196:199],v[20:23],v[108:111],a[196:199]\n"
"v_exp_f32 v123,v123\n"
MF "a[192:195],v[20:23],v[104:107],a[192:195]\n"
"v_add_f32 v54,v54,v120\n"
"v_add_f32 v54,v54,v121\n"
MF "a[188:191],v[20:23],v[100:103],a[188:191]\n"
CV "v82,v120,v121\n"
"v_add_f32 v54,v54,v122\n"
MF "a[184:187],v[20:23],v[96:99],a[184:187]\n"
"s_waitcnt vmcnt(6) lgkmcnt(0)\n"
"s_barrier\n"
MF "a[180:183],v[20:23],v[92:95],a[180:183]\n"
"s_add_u32 s72,s72,6144\n"
"ds_read_b128 v[16:19],v0\n"
"ds_read_b128 v[20:23],v1\n"
MF "a[200:203],v[24:27],v[92:95],a[200:203]\n"
"s_cmp_eq_u32 s72,s17\n"
"s_cselect_b32 s72,s16,s72\n"
"s_add_u32 m0,s64,49152\n"
MF "a[204:207],v[24:27],v[96:99],a[204:207]\n"
LD "v7,s[32:35],s72 offen nt lds\n"
MF "a[208:211],v[24:27],v[100:103],a[208:211]\n"
"s_add_u32 m0,s64,50048\n"
"v_add_f32 v54,v54,v123\n"
CV "v83,v122,v123\n"
MF "a[212:215],v[24:27],v[104:107],a[212:215]\n"
LD "v7,s[32:35],s72 offen offset:128 nt lds\n"
MF "a[216:219],v[24:27],v[108:111],a[216:219]\n"
"s_add_u32 m0,s64,50944\n"
"ds_read_b128 v[24:27],v0 offset:1024\n"
MF "a[236:239],v[28:31],v[108:111],a[236:239]\n"
LD "v7,s[32:35],s72 offen offset:256 nt lds\n"
MF "a[232:235],v[28:31],v[104:107],a[232:235]\n"
MF "a[228:231],v[28:31],v[100:103],a[228:231]\n"
MF "a[224:227],v[28:31],v[96:99],a[224:227]\n"
MF "a[220:223],v[28:31],v[92:95],a[220:223]\n"
"s_waitcnt lgkmcnt(1)\n"
MF "v[92:95],v[16:19],v[136:139],v[32:35]\n"
"s_add_u32 s72,s72,6144\n"
"s_add_u32 m0,s64,55296\n"
"ds_read_b128 v[28:31],v1 offset:1024\n"
MF "v[92:95],v[20:23],v[156:159],v[92:95]\n"
LD "v7,s[32:35],s72 offen nt lds\n"
MF "v[96:99],v[20:23],v[160:163],v[36:39]\n"
"v_exp_f32 v124,v124\n"
MF "v[96:99],v[16:19],v[140:143],v[96:99]\n"
"v_exp_f32 v125,v125\n"
MF "v[100:103],v[16:19],v[144:147],v[40:43]\n"
"s_add_u32 m0,s64,56192\n"
"v_exp_f32 v126,v126\n"
MF "v[100:103],v[20:23],v[164:167],v[100:103]\n"
LD "v7,s[32:35],s72 offen offset:128 nt lds\n"
MF "v[104:107],v[20:23],v[168:171],v[44:47]\n"
"v_exp_f32 v127,v127\n"
MF "v[104:107],v[16:19],v[148:151],v[104:107]\n"
"s_add_u32 m0,s64,57088\n"
"v_add_f32 v55,v55,v124\n"
"v_add_f32 v55,v55,v125\n"
MF "v[108:111],v[16:19],v[152:155],v[48:51]\n"
LD "v7,s[32:35],s72 offen offset:256 nt lds\n"
MF "v[108:111],v[20:23],v[172:175],v[108:111]\n"
"s_waitcnt lgkmcnt(0)\n"
"ds_read_b128 v[16:19],v0 offset:2048\n"
"ds_read_b128 v[20:23],v1 offset:2048\n"
MF "v[108:111],v[24:27],v[192:195],v[108:111]\n"
CV "v86,v124,v125\n"
"v_add_f32 v55,v55,v126\n"
MF "v[108:111],v[28:31],v[212:215],v[108:111]\n"
"v_add_f32 v55,v55,v127\n"
CV "v87,v126,v127\n"
MF "v[104:107],v[28:31],v[208:211],v[104:107]\n"
"v_exp_f32 v128,v128\n"
MF "v[104:107],v[24:27],v[188:191],v[104:107]\n"
"v_exp_f32 v129,v129\n"
MF "v[100:103],v[24:27],v[184:187],v[100:103]\n"
"v_exp_f32 v130,v130\n"
MF "v[100:103],v[28:31],v[204:207],v[100:103]\n"
"v_exp_f32 v131,v131\n"
MF "v[96:99],v[28:31],v[200:203],v[96:99]\n"
"v_add_f32 v56,v56,v128\n"
"v_add_f32 v56,v56,v129\n"
MF "v[96:99],v[24:27],v[180:183],v[96:99]\n"
CV "v90,v128,v129\n"
"v_add_f32 v56,v56,v130\n"
MF "v[92:95],v[24:27],v[176:179],v[92:95]\n"
"v_add_f32 v56,v56,v131\n"
CV "v91,v130,v131\n"
MF "v[92:95],v[28:31],v[196:199],v[92:95]\n"
"s_waitcnt lgkmcnt(0)\n"
"ds_read_b128 v[24:27],v0 offset:6144\n"
"ds_read_b128 v[28:31],v1 offset:6144\n"
MF "v[92:95],v[16:19],v[216:219],v[92:95]\n"
MF "v[92:95],v[20:23],v[236:239],v[92:95]\n"
MF "v[96:99],v[20:23],v[240:243],v[96:99]\n"
MF "v[96:99],v[16:19],v[220:223],v[96:99]\n"
MF "v[100:103],v[16:19],v[224:227],v[100:103]\n"
MF "v[100:103],v[20:23],v[244:247],v[100:103]\n"
MF "v[104:107],v[20:23],v[248:251],v[104:107]\n"
MF "v[104:107],v[16:19],v[228:231],v[104:107]\n"
"v_max3_f32 v10,v52,v53,v54\n"
"v_max3_f32 v10,v10,v55,v56\n"
MF "v[108:111],v[16:19],v[232:235],v[108:111]\n"
"v_cmp_ngt_f32 vcc,s77,v10\n"
MF "v[108:111],v[20:23],v[252:255],v[108:111]\n"
"s_waitcnt lgkmcnt(0)\n"
"s_cbranch_vccnz .Lstub_12\n"
".Lback_12:\n"
MF "v[112:115],v[24:27],v[136:139],v[32:35]\n"
"s_add_u32 s74,s74,12288\n"
"ds_read_b128 v[16:19],v0 offset:7168\n"
"ds_read_b128 v[20:23],v1 offset:7168\n"
MF "v[112:115],v[28:31],v[156:159],v[112:115]\n"
"s_cmp_eq_u32 s74,s20\n"
"s_cselect_b32 s74,s70,s74\n"
MF "v[116:119],v[28:31],v[160:163],v[36:39]\n"
"v_exp_f32 v92,v92\n"
MF "v[116:119],v[24:27],v[140:143],v[116:119]\n"
"v_exp_f32 v93,v93\n"
MF "v[120:123],v[24:27],v[144:147],v[40:43]\n"
"v_exp_f32 v94,v94\n"
MF "v[120:123],v[28:31],v[164:167],v[120:123]\n"
"v_exp_f32 v95,v95\n"
MF "v[124:127],v[28:31],v[168:171],v[44:47]\n"
"v_add_f32 v57,v52,v92\n"
"v_add_f32 v57,v57,v93\n"
MF "v[124:127],v[24:27],v[148:151],v[124:127]\n"
CV "v92,v92,v93\n"
"v_add_f32 v57,v57,v94\n"
MF "v[128:131],v[24:27],v[152:155],v[48:51]\n"
"v_add_f32 v57,v57,v95\n"
CV "v93,v94,v95\n"
MF "v[128:131],v[28:31],v[172:175],v[128:131]\n"
"s_waitcnt lgkmcnt(0)\n"
"ds_read_b128 v[24:27],v0 offset:8192\n"
"ds_read_b128 v[28:31],v1 offset:8192\n"
MF "v[128:131],v[16:19],v[192:195],v[128:131]\n"
"v_exp_f32 v96,v96\n"
MF "v[128:131],v[20:23],v[212:215],v[128:131]\n"
"v_exp_f32 v97,v97\n"
MF "v[124:127],v[20:23],v[208:211],v[124:127]\n"
"v_exp_f32 v98,v98\n"
MF "v[124:127],v[16:19],v[188:191],v[124:127]\n"
"v_exp_f32 v99,v99\n"
MF "v[120:123],v[16:19],v[184:187],v[120:123]\n"
"v_add_f32 v58,v53,v96\n"
"v_add_f32 v58,v58,v97\n"
MF "v[120:123],v[20:23],v[204:207],v[120:123]\n"
CV "v96,v96,v97\n"
"v_add_f32 v58,v58,v98\n"
MF "v[116:119],v[20:23],v[200:203],v[116:119]\n"
"v_add_f32 v58,v58,v99\n"
CV "v97,v98,v99\n"
MF "v[116:119],v[16:19],v[180:183],v[116:119]\n"
"s_waitcnt vmcnt(6) lgkmcnt(0)\n"
"s_barrier\n"
MF "v[112:115],v[16:19],v[176:179],v[112:115]\n"
"v_exp_f32 v100,v100\n"
MF "v[112:115],v[20:23],v[196:199],v[112:115]\n"
"s_nop 0\n"
TR "v[16:17],v2 offset:24576\n"
TR "v[18:19],v2 offset:30720\n"
MF "v[112:115],v[24:27],v[216:219],v[112:115]\n"
"s_add_u32 s73,s73,12288\n"
TR "v[20:21],v3 offset:24576\n"
TR "v[22:23],v3 offset:30720\n"
MF "v[112:115],v[28:31],v[236:239],v[112:115]\n"
"s_cmp_eq_u32 s73,s19\n"
"s_cselect_b32 s73,s18,s73\n"
"s_add_u32 m0,s65,0\n"
MF "v[116:119],v[28:31],v[240:243],v[116:119]\n"
LD "v8,s[36:39],s73 offen nt lds\n"
MF "v[116:119],v[24:27],v[220:223],v[116:119]\n"
"s_add_u32 m0,s65,896\n"
"v_exp_f32 v101,v101\n"
MF "v[120:123],v[24:27],v[224:227],v[120:123]\n"
LD "v8,s[36:39],s73 offen offset:128 nt lds\n"
MF "v[120:123],v[28:31],v[244:247],v[120:123]\n"
"v_exp_f32 v102,v102\n"
MF "v[124:127],v[28:31],v[248:251],v[124:127]\n"
"s_add_u32 m0,s65,1792\n"
"v_exp_f32 v103,v103\n"
MF "v[124:127],v[24:27],v[228:231],v[124:127]\n"
LD "v8,s[36:39],s73 offen offset:256 nt lds\n"
MF "v[128:131],v[24:27],v[232:235],v[128:131]\n"
"v_add_f32 v59,v54,v100\n"
"v_add_f32 v59,v59,v101\n"
MF "v[128:131],v[28:31],v[252:255],v[128:131]\n"
"s_waitcnt lgkmcnt(2)\n"
TR "v[24:25],v4 offset:24576\n"
TR "v[26:27],v4 offset:30720\n"
MF "a[0:3],v[16:19],v[72:75],a[0:3]\n"
"s_add_u32 m0,s65,0\n"
TR "v[28:29],v5 offset:24576\n"
TR "v[30:31],v5 offset:30720\n"
MF "a[4:7],v[16:19],v[76:79],a[4:7]\n"
LD "v8,s[36:39],s73 offen offset:3072 nt lds\n"
MF "a[8:11],v[16:19],v[80:83],a[8:11]\n"
"s_add_u32 m0,s65,896\n"
CV "v100,v100,v101\n"
"v_add_f32 v59,v59,v102\n"
MF "a[12:15],v[16:19],v[84:87],a[12:15]\n"
LD "v8,s[36:39],s73 offen offset:3200 nt lds\n"
MF "a[16:19],v[16:19],v[88:91],a[16:19]\n"
"s_waitcnt lgkmcnt(4)\n"
TR "v[16:17],v2 offset:25600\n"
TR "v[18:19],v2 offset:31744\n"
MF "a[36:39],v[20:23],v[88:91],a[36:39]\n"
"v_add_f32 v59,v59,v103\n"
CV "v101,v102,v103\n"
MF "a[32:35],v[20:23],v[84:87],a[32:35]\n"
"s_add_u32 m0,s65,1792\n"
"v_exp_f32 v104,v104\n"
MF "a[28:31],v[20:23],v[80:83],a[28:31]\n"
LD "v8,s[36:39],s73 offen offset:3328 nt lds\n"
MF "a[24:27],v[20:23],v[76:79],a[24:27]\n"
"v_exp_f32 v105,v105\n"
MF "a[20:23],v[20:23],v[72:75],a[20:23]\n"
"s_waitcnt lgkmcnt(4)\n"
TR "v[20:21],v3 offset:25600\n"
TR "v[22:23],v3 offset:31744\n"
MF "a[40:43],v[24:27],v[72:75],a[40:43]\n"
"v_exp_f32 v106,v106\n"
MF "a[44:47],v[24:27],v[76:79],a[44:47]\n"
"v_exp_f32 v107,v107\n"
MF "a[48:51],v[24:27],v[80:83],a[48:51]\n"
"v_add_f32 v60,v55,v104\n"
"v_add_f32 v60,v60,v105\n"
MF "a[52:55],v[24:27],v[84:87],a[52:55]\n"
CV "v104,v104,v105\n"
"v_add_f32 v60,v60,v106\n"
MF "a[56:59],v[24:27],v[88:91],a[56:59]\n"
"s_waitcnt lgkmcnt(4)\n"
TR "v[24:25],v4 offset:25600\n"
TR "v[26:27],v4 offset:31744\n"
MF "a[76:79],v[28:31],v[88:91],a[76:79]\n"
"v_add_f32 v60,v60,v107\n"
CV "v105,v106,v107\n"
MF "a[72:75],v[28:31],v[84:87],a[72:75]\n"
"v_exp_f32 v108,v108\n"
MF "a[68:71],v[28:31],v[80:83],a[68:71]\n"
"v_exp_f32 v109,v109\n"
MF "a[64:67],v[28:31],v[76:79],a[64:67]\n"
"v_exp_f32 v110,v110\n"
MF "a[60:63],v[28:31],v[72:75],a[60:63]\n"
"s_waitcnt lgkmcnt(4)\n"
TR "v[28:29],v5 offset:25600\n"
TR "v[30:31],v5 offset:31744\n"
MF "a[80:83],v[16:19],v[72:75],a[80:83]\n"
"v_exp_f32 v111,v111\n"
MF "a[84:87],v[16:19],v[76:79],a[84:87]\n"
"v_add_f32 v61,v56,v108\n"
"v_add_f32 v61,v61,v109\n"
MF "a[88:91],v[16:19],v[80:83],a[88:91]\n"
CV "v108,v108,v109\n"
"v_add_f32 v61,v61,v110\n"
MF "a[92:95],v[16:19],v[84:87],a[92:95]\n"
"v_add_f32 v61,v61,v111\n"
CV "v109,v110,v111\n"
MF "a[96:99],v[16:19],v[88:91],a[96:99]\n"
"s_waitcnt lgkmcnt(4)\n"
TR "v[16:17],v2 offset:26624\n"
TR "v[18:19],v2 offset:32768\n"
MF "a[116:119],v[20:23],v[88:91],a[116:119]\n"
"v_exp_f32 v112,v112\n"
MF "a[112:115],v[20:23],v[84:87],a[112:115]\n"
"v_exp_f32 v113,v113\n"
MF "a[108:111],v[20:23],v[80:83],a[108:111]\n"
"v_exp_f32 v114,v114\n"
MF "a[104:107],v[20:23],v[76:79],a[104:107]\n"
"v_exp_f32 v115,v115\n"
MF "a[100:103],v[20:23],v[72:75],a[100:103]\n"
"s_waitcnt lgkmcnt(4)\n"
TR "v[20:21],v3 offset:26624\n"
TR "v[22:23],v3 offset:32768\n"
MF "a[120:123],v[24:27],v[72:75],a[120:123]\n"
"v_add_f32 v57,v57,v112\n"
"v_add_f32 v57,v57,v113\n"
MF "a[124:127],v[24:27],v[76:79],a[124:127]\n"
CV "v94,v112,v113\n"
"v_add_f32 v57,v57,v114\n"
MF "a[128:131],v[24:27],v[80:83],a[128:131]\n"
"v_add_f32 v57,v57,v115\n"
CV "v95,v114,v115\n"
MF "a[132:135],v[24:27],v[84:87],a[132:135]\n"
"v_exp_f32 v116,v116\n"
MF "a[136:139],v[24:27],v[88:91],a[136:139]\n"
"s_waitcnt lgkmcnt(4)\n"
TR "v[24:25],v4 offset:26624\n"
TR "v[26:27],v4 offset:32768\n"
MF "a[156:159],v[28:31],v[88:91],a[156:159]\n"
"v_exp_f32 v117,v117\n"
MF "a[152:155],v[28:31],v[84:87],a[152:155]\n"
"v_exp_f32 v118,v118\n"
MF "a[148:151],v[28:31],v[80:83],a[148:151]\n"
"v_exp_f32 v119,v119\n"
MF "a[144:147],v[28:31],v[76:79],a[144:147]\n"
"v_add_f32 v58,v58,v116\n"
"v_add_f32 v58,v58,v117\n"
MF "a[140:143],v[28:31],v[72:75],a[140:143]\n"
"s_waitcnt lgkmcnt(4)\n"
TR "v[28:29],v5 offset:26624\n"
TR "v[30:31],v5 offset:32768\n"
MF "a[160:163],v[16:19],v[72:75],a[160:163]\n"
CV "v98,v116,v117\n"
"v_add_f32 v58,v58,v118\n"
MF "a[164:167],v[16:19],v[76:79],a[164:167]\n"
"v_add_f32 v58,v58,v119\n"
CV "v99,v118,v119\n"
MF "a[168:171],v[16:19],v[80:83],a[168:171]\n"
"v_exp_f32 v120,v120\n"
MF "a[172:175],v[16:19],v[84:87],a[172:175]\n"
"v_exp_f32 v121,v121\n"
MF "a[176:179],v[16:19],v[88:91],a[176:179]\n"
"s_waitcnt lgkmcnt(4)\n"
"v_exp_f32 v122,v122\n"
MF "a[196:199],v[20:23],v[88:91],a[196:199]\n"
"v_exp_f32 v123,v123\n"
MF "a[192:195],v[20:23],v[84:87],a[192:195]\n"
"v_add_f32 v59,v59,v120\n"
"v_add_f32 v59,v59,v121\n"
MF "a[188:191],v[20:23],v[80:83],a[188:191]\n"
CV "v102,v120,v121\n"
"v_add_f32 v59,v59,v122\n"
MF "a[184:187],v[20:23],v[76:79],a[184:187]\n"
"s_waitcnt vmcnt(6) lgkmcnt(0)\n"
"s_barrier\n"
MF "a[180:183],v[20:23],v[72:75],a[180:183]\n"
"s_add_u32 s72,s72,6144\n"
"ds_read_b128 v[16:19],v0 offset:49152\n"
"ds_read_b128 v[20:23],v1 offset:49152\n"
MF "a[200:203],v[24:27],v[72:75],a[200:203]\n"
"s_cmp_eq_u32 s72,s17\n"
"s_cselect_b32 s72,s16,s72\n"
"s_add_u32 m0,s64,24576\n"
MF "a[204:207],v[24:27],v[76:79],a[204:207]\n"
LD "v7,s[32:35],s72 offen nt lds\n"
MF "a[208:211],v[24:27],v[80:83],a[208:211]\n"
"s_add_u32 m0,s64,25472\n"
"v_add_f32 v59,v59,v123\n"
CV "v103,v122,v123\n"
MF "a[212:215],v[24:27],v[84:87],a[212:215]\n"
LD "v7,s[32:35],s72 offen offset:128 nt lds\n"
MF "a[216:219],v[24:27],v[88:91],a[216:219]\n"
"s_add_u32 m0,s64,26368\n"
"ds_read_b128 v[24:27],v0 offset:50176\n"
MF "a[236:239],v[28:31],v[88:91],a[236:239]\n"
LD "v7,s[32:35],s72 offen offset:256 nt lds\n"
MF "a[232:235],v[28:31],v[84:87],a[232:235]\n"
MF "a[228:231],v[28:31],v[80:83],a[228:231]\n"
MF "a[224:227],v[28:31],v[76:79],a[224:227]\n"
MF "a[220:223],v[28:31],v[72:75],a[220:223]\n"
"s_waitcnt lgkmcnt(1)\n"
MF "v[72:75],v[16:19],v[136:139],v[32:35]\n"
"s_add_u32 s72,s72,6144\n"
"s_add_u32 m0,s64,30720\n"
"ds_read_b128 v[28:31],v1 offset:50176\n"
MF "v[72:75],v[20:23],v[156:159],v[72:75]\n"
LD "v7,s[32:35],s72 offen nt lds\n"
MF "v[76:79],v[20:23],v[160:163],v[36:39]\n"
"v_exp_f32 v124,v124\n"
MF "v[76:79],v[16:19],v[140:143],v[76:79]\n"
"v_exp_f32 v125,v125\n"
MF "v[80:83],v[16:19],v[144:147],v[40:43]\n"
"s_add_u32 m0,s64,31616\n"
"v_exp_f32 v126,v126\n"
MF "v[80:83],v[20:23],v[164:167],v[80:83]\n"
LD "v7,s[32:35],s72 offen offset:128 nt lds\n"
MF "v[84:87],v[20:23],v[168:171],v[44:47]\n"
"v_exp_f32 v127,v127\n"
MF "v[84:87],v[16:19],v[148:151],v[84:87]\n"
"s_add_u32 m0,s64,32512\n"
"v_add_f32 v60,v60,v124\n"
"v_add_f32 v60,v60,v125\n"
MF "v[88:91],v[16:19],v[152:155],v[48:51]\n"
LD "v7,s[32:35],s72 offen offset:256 nt lds\n"
MF "v[88:91],v[20:23],v[172:175],v[88:91]\n"
"s_waitcnt lgkmcnt(0)\n"
"ds_read_b128 v[16:19],v0 offset:51200\n"
"ds_read_b128 v[20:23],v1 offset:51200\n"
MF "v[88:91],v[24:27],v[192:195],v[88:91]\n"
CV "v106,v124,v125\n"
"v_add_f32 v60,v60,v126\n"
MF "v[88:91],v[28:31],v[212:215],v[88:91]\n"
"v_add_f32 v60,v60,v127\n"
CV "v107,v126,v127\n"
MF "v[84:87],v[28:31],v[208:211],v[84:87]\n"
"v_exp_f32 v128,v128\n"
MF "v[84:87],v[24:27],v[188:191],v[84:87]\n"
"v_exp_f32 v129,v129\n"
MF "v[80:83],v[24:27],v[184:187],v[80:83]\n"
"v_exp_f32 v130,v130\n"
MF "v[80:83],v[28:31],v[204:207],v[80:83]\n"
"v_exp_f32 v131,v131\n"
MF "v[76:79],v[28:31],v[200:203],v[76:79]\n"
"v_add_f32 v61,v61,v128\n"
"v_add_f32 v61,v61,v129\n"
MF "v[76:79],v[24:27],v[180:183],v[76:79]\n"
CV "v110,v128,v129\n"
"v_add_f32 v61,v61,v130\n"
MF "v[72:75],v[24:27],v[176:179],v[72:75]\n"
"v_add_f32 v61,v61,v131\n"
CV "v111,v130,v131\n"
MF "v[72:75],v[28:31],v[196:199],v[72:75]\n"
"s_waitcnt lgkmcnt(0)\n"
"ds_read_b128 v[24:27],v0 offset:55296\n"
"ds_read_b128 v[28:31],v1 offset:55296\n"
MF "v[72:75],v[16:19],v[216:219],v[72:75]\n"
MF "v[72:75],v[20:23],v[236:239],v[72:75]\n"
MF "v[76:79],v[20:23],v[240:243],v[76:79]\n"
MF "v[76:79],v[16:19],v[220:223],v[76:79]\n"
MF "v[80:83],v[16:19],v[224:227],v[80:83]\n"
MF "v[80:83],v[20:23],v[244:247],v[80:83]\n"
MF "v[84:87],v[20:23],v[248:251],v[84:87]\n"
MF "v[84:87],v[16:19],v[228:231],v[84:87]\n"
"v_max3_f32 v10,v57,v58,v59\n"
"v_max3_f32 v10,v10,v60,v61\n"
MF "v[88:91],v[16:19],v[232:235],v[88:91]\n"
"v_cmp_ngt_f32 vcc,s95,v10\n"
MF "v[88:91],v[20:23],v[252:255],v[88:91]\n"
"s_waitcnt lgkmcnt(0)\n"
"s_cbranch_vccnz .Lstub_13\n"
".Lback_13:\n"
MF "v[112:115],v[24:27],v[136:139],v[32:35]\n"
"s_add_u32 s74,s74,12288\n"
"ds_read_b128 v[16:19],v0 offset:56320\n"
"ds_read_b128 v[20:23],v1 offset:56320\n"
MF "v[112:115],v[28:31],v[156:159],v[112:115]\n"
"s_cmp_eq_u32 s74,s20\n"
"s_cselect_b32 s74,s70,s74\n"
MF "v[116:119],v[28:31],v[160:163],v[36:39]\n"
"v_exp_f32 v72,v72\n"
MF "v[116:119],v[24:27],v[140:143],v[116:119]\n"
"v_exp_f32 v73,v73\n"
MF "v[120:123],v[24:27],v[144:147],v[40:43]\n"
"v_exp_f32 v74,v74\n"
MF "v[120:123],v[28:31],v[164:167],v[120:123]\n"
"v_exp_f32 v75,v75\n"
MF "v[124:127],v[28:31],v[168:171],v[44:47]\n"
"v_add_f32 v52,v57,v72\n"
"v_add_f32 v52,v52,v73\n"
MF "v[124:127],v[24:27],v[148:151],v[124:127]\n"
CV "v72,v72,v73\n"
"v_add_f32 v52,v52,v74\n"
MF "v[128:131],v[24:27],v[152:155],v[48:51]\n"
"v_add_f32 v52,v52,v75\n"
CV "v73,v74,v75\n"
MF "v[128:131],v[28:31],v[172:175],v[128:131]\n"
"s_waitcnt lgkmcnt(0)\n"
"ds_read_b128 v[24:27],v0 offset:57344\n"
"ds_read_b128 v[28:31],v1 offset:57344\n"
MF "v[128:131],v[16:19],v[192:195],v[128:131]\n"
"v_exp_f32 v76,v76\n"
MF "v[128:131],v[20:23],v[212:215],v[128:131]\n"
"v_exp_f32 v77,v77\n"
MF "v[124:127],v[20:23],v[208:211],v[124:127]\n"
"v_exp_f32 v78,v78\n"
MF "v[124:127],v[16:19],v[188:191],v[124:127]\n"
"v_exp_f32 v79,v79\n"
MF "v[120:123],v[16:19],v[184:187],v[120:123]\n"
"v_add_f32 v53,v58,v76\n"
"v_add_f32 v53,v53,v77\n"
MF "v[120:123],v[20:23],v[204:207],v[120:123]\n"
CV "v76,v76,v77\n"
"v_add_f32 v53,v53,v78\n"
MF "v[116:119],v[20:23],v[200:203],v[116:119]\n"
"v_add_f32 v53,v53,v79\n"
CV "v77,v78,v79\n"
MF "v[116:119],v[16:19],v[180:183],v[116:119]\n"
"s_waitcnt vmcnt(6) lgkmcnt(0)\n"
"s_barrier\n"
MF "v[112:115],v[16:19],v[176:179],v[112:115]\n"
"v_exp_f32 v80,v80\n"
MF "v[112:115],v[20:23],v[196:199],v[112:115]\n"
"s_nop 0\n"
TR "v[16:17],v2\n"
TR "v[18:19],v2 offset:6144\n"
MF "v[112:115],v[24:27],v[216:219],v[112:115]\n"
"s_add_u32 s73,s73,12288\n"
TR "v[20:21],v3\n"
TR "v[22:23],v3 offset:6144\n"
MF "v[112:115],v[28:31],v[236:239],v[112:115]\n"
"s_cmp_eq_u32 s73,s19\n"
"s_cselect_b32 s73,s18,s73\n"
"s_add_u32 m0,s65,49152\n"
MF "v[116:119],v[28:31],v[240:243],v[116:119]\n"
LD "v8,s[36:39],s73 offen nt lds\n"
MF "v[116:119],v[24:27],v[220:223],v[116:119]\n"
"s_add_u32 m0,s65,50048\n"
"v_exp_f32 v81,v81\n"
MF "v[120:123],v[24:27],v[224:227],v[120:123]\n"
LD "v8,s[36:39],s73 offen offset:128 nt lds\n"
MF "v[120:123],v[28:31],v[244:247],v[120:123]\n"
"v_exp_f32 v82,v82\n"
MF "v[124:127],v[28:31],v[248:251],v[124:127]\n"
"s_add_u32 m0,s65,50944\n"
"v_exp_f32 v83,v83\n"
MF "v[124:127],v[24:27],v[228:231],v[124:127]\n"
LD "v8,s[36:39],s73 offen offset:256 nt lds\n"
MF "v[128:131],v[24:27],v[232:235],v[128:131]\n"
"v_add_f32 v54,v59,v80\n"
"v_add_f32 v54,v54,v81\n"
MF "v[128:131],v[28:31],v[252:255],v[128:131]\n"
"s_waitcnt lgkmcnt(2)\n"
TR "v[24:25],v4\n"
TR "v[26:27],v4 offset:6144\n"
MF "a[0:3],v[16:19],v[92:95],a[0:3]\n"
"s_add_u32 m0,s65,49152\n"
TR "v[28:29],v5\n"
TR "v[30:31],v5 offset:6144\n"
MF "a[4:7],v[16:19],v[96:99],a[4:7]\n"
LD "v8,s[36:39],s73 offen offset:3072 nt lds\n"
MF "a[8:11],v[16:19],v[100:103],a[8:11]\n"
"s_add_u32 m0,s65,50048\n"
CV "v80,v80,v81\n"
"v_add_f32 v54,v54,v82\n"
MF "a[12:15],v[16:19],v[104:107],a[12:15]\n"
LD "v8,s[36:39],s73 offen offset:3200 nt lds\n"
MF "a[16:19],v[16:19],v[108:111],a[16:19]\n"
"s_waitcnt lgkmcnt(4)\n"
TR "v[16:17],v2 offset:1024\n"
TR "v[18:19],v2 offset:7168\n"
MF "a[36:39],v[20:23],v[108:111],a[36:39]\n"
"v_add_f32 v54,v54,v83\n"
CV "v81,v82,v83\n"
MF "a[32:35],v[20:23],v[104:107],a[32:35]\n"
"s_add_u32 m0,s65,50944\n"
"v_exp_f32 v84,v84\n"
MF "a[28:31],v[20:23],v[100:103],a[28:31]\n"
LD "v8,s[36:39],s73 offen offset:3328 nt lds\n"
MF "a[24:27],v[20:23],v[96:99],a[24:27]\n"
"v_exp_f32 v85,v85\n"
MF "a[20:23],v[20:23],v[92:95],a[20:23]\n"
"s_waitcnt lgkmcnt(4)\n"
TR "v[20:21],v3 offset:1024\n"
TR "v[22:23],v3 offset:7168\n"
MF "a[40:43],v[24:27],v[92:95],a[40:43]\n"
"v_exp_f32 v86,v86\n"
MF "a[44:47],v[24:27],v[96:99],a[44:47]\n"
"v_exp_f32 v87,v87\n"
MF "a[48:51],v[24:27],v[100:103],a[48:51]\n"
"v_add_f32 v55,v60,v84\n"
"v_add_f32 v55,v55,v85\n"
MF "a[52:55],v[24:27],v[104:107],a[52:55]\n"
CV "v84,v84,v85\n"
"v_add_f32 v55,v55,v86\n"
MF "a[56:59],v[24:27],v[108:111],a[56:59]\n"
"s_waitcnt lgkmcnt(4)\n"
TR "v[24:25],v4 offset:1024\n"
TR "v[26:27],v4 offset:7168\n"
MF "a[76:79],v[28:31],v[108:111],a[76:79]\n"
"v_add_f32 v55,v55,v87\n"
CV "v85,v86,v87\n"
MF "a[72:75],v[28:31],v[104:107],a[72:75]\n"
"v_exp_f32 v88,v88\n"
MF "a[68:71],v[28:31],v[100:103],a[68:71]\n"
"v_exp_f32 v89,v89\n"
MF "a[64:67],v[28:31],v[96:99],a[64:67]\n"
"v_exp_f32 v90,v90\n"
MF "a[60:63],v[28:31],v[92:95],a[60:63]\n"
"s_waitcnt lgkmcnt(4)\n"
TR "v[28:29],v5 offset:1024\n"
TR "v[30:31],v5 offset:7168\n"
MF "a[80:83],v[16:19],v[92:95],a[80:83]\n"
"v_exp_f32 v91,v91\n"
MF "a[84:87],v[16:19],v[96:99],a[84:87]\n"
"v_add_f32 v56,v61,v88\n"
"v_add_f32 v56,v56,v89\n"
MF "a[88:91],v[16:19],v[100:103],a[88:91]\n"
CV "v88,v88,v89\n"
"v_add_f32 v56,v56,v90\n"
MF "a[92:95],v[16:19],v[104:107],a[92:95]\n"
"v_add_f32 v56,v56,v91\n"
CV "v89,v90,v91\n"
MF "a[96:99],v[16:19],v[108:111],a[96:99]\n"
"s_waitcnt lgkmcnt(4)\n"
TR "v[16:17],v2 offset:2048\n"
TR "v[18:19],v2 offset:8192\n"
MF "a[116:119],v[20:23],v[108:111],a[116:119]\n"
"v_exp_f32 v112,v112\n"
MF "a[112:115],v[20:23],v[104:107],a[112:115]\n"
"v_exp_f32 v113,v113\n"
MF "a[108:111],v[20:23],v[100:103],a[108:111]\n"
"v_exp_f32 v114,v114\n"
MF "a[104:107],v[20:23],v[96:99],a[104:107]\n"
"v_exp_f32 v115,v115\n"
MF "a[100:103],v[20:23],v[92:95],a[100:103]\n"
"s_waitcnt lgkmcnt(4)\n"
TR "v[20:21],v3 offset:2048\n"
TR "v[22:23],v3 offset:8192\n"
MF "a[120:123],v[24:27],v[92:95],a[120:123]\n"
"v_add_f32 v52,v52,v112\n"
"v_add_f32 v52,v52,v113\n"
MF "a[124:127],v[24:27],v[96:99],a[124:127]\n"
CV "v74,v112,v113\n"
"v_add_f32 v52,v52,v114\n"
MF "a[128:131],v[24:27],v[100:103],a[128:131]\n"
"v_add_f32 v52,v52,v115\n"
CV "v75,v114,v115\n"
MF "a[132:135],v[24:27],v[104:107],a[132:135]\n"
"v_exp_f32 v116,v116\n"
MF "a[136:139],v[24:27],v[108:111],a[136:139]\n"
"s_waitcnt lgkmcnt(4)\n"
TR "v[24:25],v4 offset:2048\n"
TR "v[26:27],v4 offset:8192\n"
MF "a[156:159],v[28:31],v[108:111],a[156:159]\n"
"v_exp_f32 v117,v117\n"
MF "a[152:155],v[28:31],v[104:107],a[152:155]\n"
"v_exp_f32 v118,v118\n"
MF "a[148:151],v[28:31],v[100:103],a[148:151]\n"
"v_exp_f32 v119,v119\n"
MF "a[144:147],v[28:31],v[96:99],a[144:147]\n"
"v_add_f32 v53,v53,v116\n"
"v_add_f32 v53,v53,v117\n"
MF "a[140:143],v[28:31],v[92:95],a[140:143]\n"
"s_waitcnt lgkmcnt(4)\n"
TR "v[28:29],v5 offset:2048\n"
TR "v[30:31],v5 offset:8192\n"
MF "a[160:163],v[16:19],v[92:95],a[160:163]\n"
CV "v78,v116,v117\n"
"v_add_f32 v53,v53,v118\n"
MF "a[164:167],v[16:19],v[96:99],a[164:167]\n"
"v_add_f32 v53,v53,v119\n"
CV "v79,v118,v119\n"
MF "a[168:171],v[16:19],v[100:103],a[168:171]\n"
"v_exp_f32 v120,v120\n"
MF "a[172:175],v[16:19],v[104:107],a[172:175]\n"
"v_exp_f32 v121,v121\n"
MF "a[176:179],v[16:19],v[108:111],a[176:179]\n"
"s_waitcnt lgkmcnt(4)\n"
"v_exp_f32 v122,v122\n"
MF "a[196:199],v[20:23],v[108:111],a[196:199]\n"
"v_exp_f32 v123,v123\n"
MF "a[192:195],v[20:23],v[104:107],a[192:195]\n"
"v_add_f32 v54,v54,v120\n"
"v_add_f32 v54,v54,v121\n"
MF "a[188:191],v[20:23],v[100:103],a[188:191]\n"
CV "v82,v120,v121\n"
"v_add_f32 v54,v54,v122\n"
MF "a[184:187],v[20:23],v[96:99],a[184:187]\n"
"s_waitcnt vmcnt(6) lgkmcnt(0)\n"
"s_barrier\n"
MF "a[180:183],v[20:23],v[92:95],a[180:183]\n"
"s_add_u32 s72,s72,6144\n"
"ds_read_b128 v[16:19],v0 offset:24576\n"
"ds_read_b128 v[20:23],v1 offset:24576\n"
MF "a[200:203],v[24:27],v[92:95],a[200:203]\n"
"s_cmp_eq_u32 s72,s17\n"
"s_cselect_b32 s72,s16,s72\n"
"s_add_u32 m0,s64,0\n"
MF "a[204:207],v[24:27],v[96:99],a[204:207]\n"
LD "v7,s[32:35],s72 offen nt lds\n"
MF "a[208:211],v[24:27],v[100:103],a[208:211]\n"
"s_add_u32 m0,s64,896\n"
"v_add_f32 v54,v54,v123\n"
CV "v83,v122,v123\n"
MF "a[212:215],v[24:27],v[104:107],a[212:215]\n"
LD "v7,s[32:35],s72 offen offset:128 nt lds\n"
MF "a[216:219],v[24:27],v[108:111],a[216:219]\n"
"s_add_u32 m0,s64,1792\n"
"ds_read_b128 v[24:27],v0 offset:25600\n"
MF "a[236:239],v[28:31],v[108:111],a[236:239]\n"
LD "v7,s[32:35],s72 offen offset:256 nt lds\n"
MF "a[232:235],v[28:31],v[104:107],a[232:235]\n"
MF "a[228:231],v[28:31],v[100:103],a[228:231]\n"
MF "a[224:227],v[28:31],v[96:99],a[224:227]\n"
MF "a[220:223],v[28:31],v[92:95],a[220:223]\n"
"s_waitcnt lgkmcnt(1)\n"
MF "v[92:95],v[16:19],v[136:139],v[32:35]\n"
"s_add_u32 s72,s72,6144\n"
"s_add_u32 m0,s64,6144\n"
"ds_read_b128 v[28:31],v1 offset:25600\n"
MF "v[92:95],v[20:23],v[156:159],v[92:95]\n"
LD "v7,s[32:35],s72 offen nt lds\n"
MF "v[96:99],v[20:23],v[160:163],v[36:39]\n"
"v_exp_f32 v124,v124\n"
MF "v[96:99],v[16:19],v[140:143],v[96:99]\n"
"v_exp_f32 v125,v125\n"
MF "v[100:103],v[16:19],v[144:147],v[40:43]\n"
"s_add_u32 m0,s64,7040\n"
"v_exp_f32 v126,v126\n"
MF "v[100:103],v[20:23],v[164:167],v[100:103]\n"
LD "v7,s[32:35],s72 offen offset:128 nt lds\n"
MF "v[104:107],v[20:23],v[168:171],v[44:47]\n"
"v_exp_f32 v127,v127\n"
MF "v[104:107],v[16:19],v[148:151],v[104:107]\n"
"s_add_u32 m0,s64,7936\n"
"v_add_f32 v55,v55,v124\n"
"v_add_f32 v55,v55,v125\n"
MF "v[108:111],v[16:19],v[152:155],v[48:51]\n"
LD "v7,s[32:35],s72 offen offset:256 nt lds\n"
MF "v[108:111],v[20:23],v[172:175],v[108:111]\n"
"s_waitcnt lgkmcnt(0)\n"
"ds_read_b128 v[16:19],v0 offset:26624\n"
"ds_read_b128 v[20:23],v1 offset:26624\n"
MF "v[108:111],v[24:27],v[192:195],v[108:111]\n"
CV "v86,v124,v125\n"
"v_add_f32 v55,v55,v126\n"
MF "v[108:111],v[28:31],v[212:215],v[108:111]\n"
"v_add_f32 v55,v55,v127\n"
CV "v87,v126,v127\n"
MF "v[104:107],v[28:31],v[208:211],v[104:107]\n"
"v_exp_f32 v128,v128\n"
MF "v[104:107],v[24:27],v[188:191],v[104:107]\n"
"v_exp_f32 v129,v129\n"
MF "v[100:103],v[24:27],v[184:187],v[100:103]\n"
"v_exp_f32 v130,v130\n"
MF "v[100:103],v[28:31],v[204:207],v[100:103]\n"
"v_exp_f32 v131,v131\n"
MF "v[96:99],v[28:31],v[200:203],v[96:99]\n"
"v_add_f32 v56,v56,v128\n"
"v_add_f32 v56,v56,v129\n"
MF "v[96:99],v[24:27],v[180:183],v[96:99]\n"
CV "v90,v128,v129\n"
"v_add_f32 v56,v56,v130\n"
MF "v[92:95],v[24:27],v[176:179],v[92:95]\n"
"v_add_f32 v56,v56,v131\n"
CV "v91,v130,v131\n"
MF "v[92:95],v[28:31],v[196:199],v[92:95]\n"
"s_waitcnt lgkmcnt(0)\n"
"ds_read_b128 v[24:27],v0 offset:30720\n"
"ds_read_b128 v[28:31],v1 offset:30720\n"
MF "v[92:95],v[16:19],v[216:219],v[92:95]\n"
MF "v[92:95],v[20:23],v[236:239],v[92:95]\n"
MF "v[96:99],v[20:23],v[240:243],v[96:99]\n"
MF "v[96:99],v[16:19],v[220:223],v[96:99]\n"
MF "v[100:103],v[16:19],v[224:227],v[100:103]\n"
MF "v[100:103],v[20:23],v[244:247],v[100:103]\n"
MF "v[104:107],v[20:23],v[248:251],v[104:107]\n"
MF "v[104:107],v[16:19],v[228:231],v[104:107]\n"
"v_max3_f32 v10,v52,v53,v54\n"
"v_max3_f32 v10,v10,v55,v56\n"
MF "v[108:111],v[16:19],v[232:235],v[108:111]\n"
"v_cmp_ngt_f32 vcc,s77,v10\n"
MF "v[108:111],v[20:23],v[252:255],v[108:111]\n"
"s_waitcnt lgkmcnt(0)\n"
"s_cbranch_vccnz .Lstub_14\n"
".Lback_14:\n"
MF "v[112:115],v[24:27],v[136:139],v[32:35]\n"
"s_add_u32 s74,s74,12288\n"
"ds_read_b128 v[16:19],v0 offset:31744\n"
"ds_read_b128 v[20:23],v1 offset:31744\n"
MF "v[112:115],v[28:31],v[156:159],v[112:115]\n"
"s_cmp_eq_u32 s74,s20\n"
"s_cselect_b32 s74,s70,s74\n"
MF "v[116:119],v[28:31],v[160:163],v[36:39]\n"
"v_exp_f32 v92,v92\n"
MF "v[116:119],v[24:27],v[140:143],v[116:119]\n"
"v_exp_f32 v93,v93\n"
MF "v[120:123],v[24:27],v[144:147],v[40:43]\n"
"v_exp_f32 v94,v94\n"
MF "v[120:123],v[28:31],v[164:167],v[120:123]\n"
"v_exp_f32 v95,v95\n"
MF "v[124:127],v[28:31],v[168:171],v[44:47]\n"
"v_add_f32 v57,v52,v92\n"
"v_add_f32 v57,v57,v93\n"
MF "v[124:127],v[24:27],v[148:151],v[124:127]\n"
CV "v92,v92,v93\n"
"v_add_f32 v57,v57,v94\n"
MF "v[128:131],v[24:27],v[152:155],v[48:51]\n"
"v_add_f32 v57,v57,v95\n"
CV "v93,v94,v95\n"
MF "v[128:131],v[28:31],v[172:175],v[128:131]\n"
"s_waitcnt lgkmcnt(0)\n"
"ds_read_b128 v[24:27],v0 offset:32768\n"
"ds_read_b128 v[28:31],v1 offset:32768\n"
MF "v[128:131],v[16:19],v[192:195],v[128:131]\n"
"v_exp_f32 v96,v96\n"
MF "v[128:131],v[20:23],v[212:215],v[128:131]\n"
"v_exp_f32 v97,v97\n"
MF "v[124:127],v[20:23],v[208:211],v[124:127]\n"
"v_exp_f32 v98,v98\n"
MF "v[124:127],v[16:19],v[188:191],v[124:127]\n"
"v_exp_f32 v99,v99\n"
MF "v[120:123],v[16:19],v[184:187],v[120:123]\n"
"v_add_f32 v58,v53,v96\n"
"v_add_f32 v58,v58,v97\n"
MF "v[120:123],v[20:23],v[204:207],v[120:123]\n"
CV "v96,v96,v97\n"
"v_add_f32 v58,v58,v98\n"
MF "v[116:119],v[20:23],v[200:203],v[116:119]\n"
"v_add_f32 v58,v58,v99\n"
CV "v97,v98,v99\n"
MF "v[116:119],v[16:19],v[180:183],v[116:119]\n"
"s_waitcnt vmcnt(6) lgkmcnt(0)\n"
"s_barrier\n"
MF "v[112:115],v[16:19],v[176:179],v[112:115]\n"
"v_exp_f32 v100,v100\n"
MF "v[112:115],v[20:23],v[196:199],v[112:115]\n"
"s_nop 0\n"
TR "v[16:17],v2 offset:49152\n"
TR "v[18:19],v2 offset:55296\n"
MF "v[112:115],v[24:27],v[216:219],v[112:115]\n"
"s_add_u32 s73,s73,12288\n"
TR "v[20:21],v3 offset:49152\n"
TR "v[22:23],v3 offset:55296\n"
MF "v[112:115],v[28:31],v[236:239],v[112:115]\n"
"s_cmp_eq_u32 s73,s19\n"
"s_cselect_b32 s73,s18,s73\n"
"s_add_u32 m0,s65,24576\n"
MF "v[116:119],v[28:31],v[240:243],v[116:119]\n"
LD "v8,s[36:39],s73 offen nt lds\n"
MF "v[116:119],v[24:27],v[220:223],v[116:119]\n"
"s_add_u32 m0,s65,25472\n"
"v_exp_f32 v101,v101\n"
MF "v[120:123],v[24:27],v[224:227],v[120:123]\n"
LD "v8,s[36:39],s73 offen offset:128 nt lds\n"
MF "v[120:123],v[28:31],v[244:247],v[120:123]\n"
"v_exp_f32 v102,v102\n"
MF "v[124:127],v[28:31],v[248:251],v[124:127]\n"
"s_add_u32 m0,s65,26368\n"
"v_exp_f32 v103,v103\n"
MF "v[124:127],v[24:27],v[228:231],v[124:127]\n"
LD "v8,s[36:39],s73 offen offset:256 nt lds\n"
MF "v[128:131],v[24:27],v[232:235],v[128:131]\n"
"v_add_f32 v59,v54,v100\n"
"v_add_f32 v59,v59,v101\n"
MF "v[128:131],v[28:31],v[252:255],v[128:131]\n"
"s_waitcnt lgkmcnt(2)\n"
TR "v[24:25],v4 offset:49152\n"
TR "v[26:27],v4 offset:55296\n"
MF "a[0:3],v[16:19],v[72:75],a[0:3]\n"
"s_add_u32 m0,s65,24576\n"
TR "v[28:29],v5 offset:49152\n"
TR "v[30:31],v5 offset:55296\n"
MF "a[4:7],v[16:19],v[76:79],a[4:7]\n"
LD "v8,s[36:39],s73 offen offset:3072 nt lds\n"
MF "a[8:11],v[16:19],v[80:83],a[8:11]\n"
"s_add_u32 m0,s65,25472\n"
CV "v100,v100,v101\n"
"v_add_f32 v59,v59,v102\n"
MF "a[12:15],v[16:19],v[84:87],a[12:15]\n"
LD "v8,s[36:39],s73 offen offset:3200 nt lds\n"
MF "a[16:19],v[16:19],v[88:91],a[16:19]\n"
"s_waitcnt lgkmcnt(4)\n"
TR "v[16:17],v2 offset:50176\n"
TR "v[18:19],v2 offset:56320\n"
MF "a[36:39],v[20:23],v[88:91],a[36:39]\n"
"v_add_f32 v59,v59,v103\n"
CV "v101,v102,v103\n"
MF "a[32:35],v[20:23],v[84:87],a[32:35]\n"
"s_add_u32 m0,s65,26368\n"
"v_exp_f32 v104,v104\n"
MF "a[28:31],v[20:23],v[80:83],a[28:31]\n"
LD "v8,s[36:39],s73 offen offset:3328 nt lds\n"
MF "a[24:27],v[20:23],v[76:79],a[24:27]\n"
"v_exp_f32 v105,v105\n"
MF "a[20:23],v[20:23],v[72:75],a[20:23]\n"
"s_waitcnt lgkmcnt(4)\n"
TR "v[20:21],v3 offset:50176\n"
TR "v[22:23],v3 offset:56320\n"
MF "a[40:43],v[24:27],v[72:75],a[40:43]\n"
"v_exp_f32 v106,v106\n"
MF "a[44:47],v[24:27],v[76:79],a[44:47]\n"
"v_exp_f32 v107,v107\n"
MF "a[48:51],v[24:27],v[80:83],a[48:51]\n"
"v_add_f32 v60,v55,v104\n"
"v_add_f32 v60,v60,v105\n"
MF "a[52:55],v[24:27],v[84:87],a[52:55]\n"
CV "v104,v104,v105\n"
"v_add_f32 v60,v60,v106\n"
MF "a[56:59],v[24:27],v[88:91],a[56:59]\n"
"s_waitcnt lgkmcnt(4)\n"
TR "v[24:25],v4 offset:50176\n"
TR "v[26:27],v4 offset:56320\n"
MF "a[76:79],v[28:31],v[88:91],a[76:79]\n"
"v_add_f32 v60,v60,v107\n"
CV "v105,v106,v107\n"
MF "a[72:75],v[28:31],v[84:87],a[72:75]\n"
"v_exp_f32 v108,v108\n"
MF "a[68:71],v[28:31],v[80:83],a[68:71]\n"
"v_exp_f32 v109,v109\n"
MF "a[64:67],v[28:31],v[76:79],a[64:67]\n"
"v_exp_f32 v110,v110\n"
MF "a[60:63],v[28:31],v[72:75],a[60:63]\n"
"s_waitcnt lgkmcnt(4)\n"
TR "v[28:29],v5 offset:50176\n"
TR "v[30:31],v5 offset:56320\n"
MF "a[80:83],v[16:19],v[72:75],a[80:83]\n"
"v_exp_f32 v111,v111\n"
MF "a[84:87],v[16:19],v[76:79],a[84:87]\n"
"v_add_f32 v61,v56,v108\n"
"v_add_f32 v61,v61,v109\n"
MF "a[88:91],v[16:19],v[80:83],a[88:91]\n"
CV "v108,v108,v109\n"
"v_add_f32 v61,v61,v110\n"
MF "a[92:95],v[16:19],v[84:87],a[92:95]\n"
"v_add_f32 v61,v61,v111\n"
CV "v109,v110,v111\n"
MF "a[96:99],v[16:19],v[88:91],a[96:99]\n"
"s_waitcnt lgkmcnt(4)\n"
TR "v[16:17],v2 offset:51200\n"
TR "v[18:19],v2 offset:57344\n"
MF "a[116:119],v[20:23],v[88:91],a[116:119]\n"
"v_exp_f32 v112,v112\n"
MF "a[112:115],v[20:23],v[84:87],a[112:115]\n"
"v_exp_f32 v113,v113\n"
MF "a[108:111],v[20:23],v[80:83],a[108:111]\n"
"v_exp_f32 v114,v114\n"
MF "a[104:107],v[20:23],v[76:79],a[104:107]\n"
"v_exp_f32 v115,v115\n"
MF "a[100:103],v[20:23],v[72:75],a[100:103]\n"
"s_waitcnt lgkmcnt(4)\n"
TR "v[20:21],v3 offset:51200\n"
TR "v[22:23],v3 offset:57344\n"
MF "a[120:123],v[24:27],v[72:75],a[120:123]\n"
"v_add_f32 v57,v57,v112\n"
"v_add_f32 v57,v57,v113\n"
MF "a[124:127],v[24:27],v[76:79],a[124:127]\n"
CV "v94,v112,v113\n"
"v_add_f32 v57,v57,v114\n"
MF "a[128:131],v[24:27],v[80:83],a[128:131]\n"
"v_add_f32 v57,v57,v115\n"
CV "v95,v114,v115\n"
MF "a[132:135],v[24:27],v[84:87],a[132:135]\n"
"v_exp_f32 v116,v116\n"
MF "a[136:139],v[24:27],v[88:91],a[136:139]\n"
"s_waitcnt lgkmcnt(4)\n"
TR "v[24:25],v4 offset:51200\n"
TR "v[26:27],v4 offset:57344\n"
MF "a[156:159],v[28:31],v[88:91],a[156:159]\n"
"v_exp_f32 v117,v117\n"
MF "a[152:155],v[28:31],v[84:87],a[152:155]\n"
"v_exp_f32 v118,v118\n"
MF "a[148:151],v[28:31],v[80:83],a[148:151]\n"
"v_exp_f32 v119,v119\n"
MF "a[144:147],v[28:31],v[76:79],a[144:147]\n"
"v_add_f32 v58,v58,v116\n"
"v_add_f32 v58,v58,v117\n"
MF "a[140:143],v[28:31],v[72:75],a[140:143]\n"
"s_waitcnt lgkmcnt(4)\n"
TR "v[28:29],v5 offset:51200\n"
TR "v[30:31],v5 offset:57344\n"
MF "a[160:163],v[16:19],v[72:75],a[160:163]\n"
CV "v98,v116,v117\n"
"v_add_f32 v58,v58,v118\n"
MF "a[164:167],v[16:19],v[76:79],a[164:167]\n"
"v_add_f32 v58,v58,v119\n"
CV "v99,v118,v119\n"
MF "a[168:171],v[16:19],v[80:83],a[168:171]\n"
"v_exp_f32 v120,v120\n"
MF "a[172:175],v[16:19],v[84:87],a[172:175]\n"
"v_exp_f32 v121,v121\n"
MF "a[176:179],v[16:19],v[88:91],a[176:179]\n"
"s_waitcnt lgkmcnt(4)\n"
"v_exp_f32 v122,v122\n"
MF "a[196:199],v[20:23],v[88:91],a[196:199]\n"
"v_exp_f32 v123,v123\n"
MF "a[192:195],v[20:23],v[84:87],a[192:195]\n"
"v_add_f32 v59,v59,v120\n"
"v_add_f32 v59,v59,v121\n"
MF "a[188:191],v[20:23],v[80:83],a[188:191]\n"
CV "v102,v120,v121\n"
"v_add_f32 v59,v59,v122\n"
MF "a[184:187],v[20:23],v[76:79],a[184:187]\n"
"s_waitcnt vmcnt(6) lgkmcnt(0)\n"
"s_barrier\n"
MF "a[180:183],v[20:23],v[72:75],a[180:183]\n"
"s_add_u32 s72,s72,6144\n"
"ds_read_b128 v[16:19],v0\n"
"ds_read_b128 v[20:23],v1\n"
MF "a[200:203],v[24:27],v[72:75],a[200:203]\n"
"s_cmp_eq_u32 s72,s17\n"
"s_cselect_b32 s72,s16,s72\n"
"s_add_u32 m0,s64,49152\n"
MF "a[204:207],v[24:27],v[76:79],a[204:207]\n"
LD "v7,s[32:35],s72 offen nt lds\n"
MF "a[208:211],v[24:27],v[80:83],a[208:211]\n"
"s_add_u32 m0,s64,50048\n"
"v_add_f32 v59,v59,v123\n"
CV "v103,v122,v123\n"
MF "a[212:215],v[24:27],v[84:87],a[212:215]\n"
LD "v7,s[32:35],s72 offen offset:128 nt lds\n"
MF "a[216:219],v[24:27],v[88:91],a[216:219]\n"
"s_add_u32 m0,s64,50944\n"
"ds_read_b128 v[24:27],v0 offset:1024\n"
MF "a[236:239],v[28:31],v[88:91],a[236:239]\n"
LD "v7,s[32:35],s72 offen offset:256 nt lds\n"
MF "a[232:235],v[28:31],v[84:87],a[232:235]\n"
MF "a[228:231],v[28:31],v[80:83],a[228:231]\n"
MF "a[224:227],v[28:31],v[76:79],a[224:227]\n"
MF "a[220:223],v[28:31],v[72:75],a[220:223]\n"
"s_waitcnt lgkmcnt(1)\n"
".Lmid:\n"
"s_cbranch_execz .Lipr2\n"
MF "v[72:75],v[16:19],v[136:139],v[32:35]\n"
"s_add_u32 s72,s72,6144\n"
"s_add_u32 m0,s64,55296\n"
"ds_read_b128 v[28:31],v1 offset:1024\n"
MF "v[72:75],v[20:23],v[156:159],v[72:75]\n"
LD "v7,s[32:35],s72 offen nt lds\n"
MF "v[76:79],v[20:23],v[160:163],v[36:39]\n"
"v_exp_f32 v124,v124\n"
MF "v[76:79],v[16:19],v[140:143],v[76:79]\n"
"v_exp_f32 v125,v125\n"
MF "v[80:83],v[16:19],v[144:147],v[40:43]\n"
"s_add_u32 m0,s64,56192\n"
"v_exp_f32 v126,v126\n"
MF "v[80:83],v[20:23],v[164:167],v[80:83]\n"
LD "v7,s[32:35],s72 offen offset:128 nt lds\n"
MF "v[84:87],v[20:23],v[168:171],v[44:47]\n"
"v_exp_f32 v127,v127\n"
MF "v[84:87],v[16:19],v[148:151],v[84:87]\n"
"s_add_u32 m0,s64,57088\n"
"v_add_f32 v60,v60,v124\n"
"v_add_f32 v60,v60,v125\n"
MF "v[88:91],v[16:19],v[152:155],v[48:51]\n"
LD "v7,s[32:35],s72 offen offset:256 nt lds\n"
MF "v[88:91],v[20:23],v[172:175],v[88:91]\n"
"s_waitcnt lgkmcnt(0)\n"
"ds_read_b128 v[16:19],v0 offset:2048\n"
"ds_read_b128 v[20:23],v1 offset:2048\n"
MF "v[88:91],v[24:27],v[192:195],v[88:91]\n"
CV "v106,v124,v125\n"
"v_add_f32 v60,v60,v126\n"
MF "v[88:91],v[28:31],v[212:215],v[88:91]\n"
"v_add_f32 v60,v60,v127\n"
CV "v107,v126,v127\n"
MF "v[84:87],v[28:31],v[208:211],v[84:87]\n"
"v_exp_f32 v128,v128\n"
MF "v[84:87],v[24:27],v[188:191],v[84:87]\n"
"v_exp_f32 v129,v129\n"
MF "v[80:83],v[24:27],v[184:187],v[80:83]\n"
"v_exp_f32 v130,v130\n"
MF "v[80:83],v[28:31],v[204:207],v[80:83]\n"
"v_exp_f32 v131,v131\n"
MF "v[76:79],v[28:31],v[200:203],v[76:79]\n"
"v_add_f32 v61,v61,v128\n"
"v_add_f32 v61,v61,v129\n"
MF "v[76:79],v[24:27],v[180:183],v[76:79]\n"
CV "v110,v128,v129\n"
"v_add_f32 v61,v61,v130\n"
MF "v[72:75],v[24:27],v[176:179],v[72:75]\n"
"v_add_f32 v61,v61,v131\n"
CV "v111,v130,v131\n"
MF "v[72:75],v[28:31],v[196:199],v[72:75]\n"
"s_waitcnt lgkmcnt(0)\n"
"ds_read_b128 v[24:27],v0 offset:6144\n"
"ds_read_b128 v[28:31],v1 offset:6144\n"
MF "v[72:75],v[16:19],v[216:219],v[72:75]\n"
MF "v[72:75],v[20:23],v[236:239],v[72:75]\n"
MF "v[76:79],v[20:23],v[240:243],v[76:79]\n"
MF "v[76:79],v[16:19],v[220:223],v[76:79]\n"
MF "v[80:83],v[16:19],v[224:227],v[80:83]\n"
MF "v[80:83],v[20:23],v[244:247],v[80:83]\n"
MF "v[84:87],v[20:23],v[248:251],v[84:87]\n"
MF "v[84:87],v[16:19],v[228:231],v[84:87]\n"
"v_max3_f32 v10,v57,v58,v59\n"
"v_max3_f32 v10,v10,v60,v61\n"
MF "v[88:91],v[16:19],v[232:235],v[88:91]\n"
"v_cmp_ngt_f32 vcc,s95,v10\n"
MF "v[88:91],v[20:23],v[252:255],v[88:91]\n"
"s_waitcnt lgkmcnt(0)\n"
"s_cbranch_vccnz .Lstub_15\n"
".Lback_15:\n"
MF "v[112:115],v[24:27],v[136:139],v[32:35]\n"
"s_add_u32 s74,s74,12288\n"
"ds_read_b128 v[16:19],v0 offset:7168\n"
"ds_read_b128 v[20:23],v1 offset:7168\n"
MF "v[112:115],v[28:31],v[156:159],v[112:115]\n"
"s_cmp_eq_u32 s74,s20\n"
"s_cselect_b32 s74,s70,s74\n"
MF "v[116:119],v[28:31],v[160:163],v[36:39]\n"
"v_exp_f32 v72,v72\n"
MF "v[116:119],v[24:27],v[140:143],v[116:119]\n"
"v_exp_f32 v73,v73\n"
MF "v[120:123],v[24:27],v[144:147],v[40:43]\n"
"v_exp_f32 v74,v74\n"
MF "v[120:123],v[28:31],v[164:167],v[120:123]\n"
"v_exp_f32 v75,v75\n"
MF "v[124:127],v[28:31],v[168:171],v[44:47]\n"
"v_add_f32 v52,v57,v72\n"
"v_add_f32 v52,v52,v73\n"
MF "v[124:127],v[24:27],v[148:151],v[124:127]\n"
CV "v72,v72,v73\n"
"v_add_f32 v52,v52,v74\n"
MF "v[128:131],v[24:27],v[152:155],v[48:51]\n"
"v_add_f32 v52,v52,v75\n"
CV "v73,v74,v75\n"
MF "v[128:131],v[28:31],v[172:175],v[128:131]\n"
"s_waitcnt lgkmcnt(0)\n"
"ds_read_b128 v[24:27],v0 offset:8192\n"
"ds_read_b128 v[28:31],v1 offset:8192\n"
MF "v[128:131],v[16:19],v[192:195],v[128:131]\n"
"v_exp_f32 v76,v76\n"
MF "v[128:131],v[20:23],v[212:215],v[128:131]\n"
"v_exp_f32 v77,v77\n"
MF "v[124:127],v[20:23],v[208:211],v[124:127]\n"
"v_exp_f32 v78,v78\n"
MF "v[124:127],v[16:19],v[188:191],v[124:127]\n"
"v_exp_f32 v79,v79\n"
MF "v[120:123],v[16:19],v[184:187],v[120:123]\n"
"v_add_f32 v53,v58,v76\n"
"v_add_f32 v53,v53,v77\n"
MF "v[120:123],v[20:23],v[204:207],v[120:123]\n"
CV "v76,v76,v77\n"
"v_add_f32 v53,v53,v78\n"
MF "v[116:119],v[20:23],v[200:203],v[116:119]\n"
"v_add_f32 v53,v53,v79\n"
CV "v77,v78,v79\n"
MF "v[116:119],v[16:19],v[180:183],v[116:119]\n"
"s_waitcnt vmcnt(6) lgkmcnt(0)\n"
"s_barrier\n"
MF "v[112:115],v[16:19],v[176:179],v[112:115]\n"
"v_exp_f32 v80,v80\n"
MF "v[112:115],v[20:23],v[196:199],v[112:115]\n"
"s_nop 0\n"
TR "v[16:17],v2 offset:24576\n"
TR "v[18:19],v2 offset:30720\n"
MF "v[112:115],v[24:27],v[216:219],v[112:115]\n"
"s_add_u32 s73,s73,12288\n"
TR "v[20:21],v3 offset:24576\n"
TR "v[22:23],v3 offset:30720\n"
MF "v[112:115],v[28:31],v[236:239],v[112:115]\n"
"s_cmp_eq_u32 s73,s19\n"
"s_cselect_b32 s73,s18,s73\n"
"s_add_u32 m0,s65,0\n"
MF "v[116:119],v[28:31],v[240:243],v[116:119]\n"
LD "v8,s[36:39],s73 offen nt lds\n"
MF "v[116:119],v[24:27],v[220:223],v[116:119]\n"
"s_add_u32 m0,s65,896\n"
"v_exp_f32 v81,v81\n"
MF "v[120:123],v[24:27],v[224:227],v[120:123]\n"
LD "v8,s[36:39],s73 offen offset:128 nt lds\n"
MF "v[120:123],v[28:31],v[244:247],v[120:123]\n"
"v_exp_f32 v82,v82\n"
MF "v[124:127],v[28:31],v[248:251],v[124:127]\n"
"s_add_u32 m0,s65,1792\n"
"v_exp_f32 v83,v83\n"
MF "v[124:127],v[24:27],v[228:231],v[124:127]\n"
LD "v8,s[36:39],s73 offen offset:256 nt lds\n"
MF "v[128:131],v[24:27],v[232:235],v[128:131]\n"
"v_add_f32 v54,v59,v80\n"
"v_add_f32 v54,v54,v81\n"
MF "v[128:131],v[28:31],v[252:255],v[128:131]\n"
"s_waitcnt lgkmcnt(2)\n"
TR "v[24:25],v4 offset:24576\n"
TR "v[26:27],v4 offset:30720\n"
MF "a[0:3],v[16:19],v[92:95],a[0:3]\n"
"s_add_u32 m0,s65,0\n"
TR "v[28:29],v5 offset:24576\n"
TR "v[30:31],v5 offset:30720\n"
MF "a[4:7],v[16:19],v[96:99],a[4:7]\n"
LD "v8,s[36:39],s73 offen offset:3072 nt lds\n"
MF "a[8:11],v[16:19],v[100:103],a[8:11]\n"
"s_add_u32 m0,s65,896\n"
CV "v80,v80,v81\n"
"v_add_f32 v54,v54,v82\n"
MF "a[12:15],v[16:19],v[104:107],a[12:15]\n"
LD "v8,s[36:39],s73 offen offset:3200 nt lds\n"
MF "a[16:19],v[16:19],v[108:111],a[16:19]\n"
"s_waitcnt lgkmcnt(4)\n"
TR "v[16:17],v2 offset:25600\n"
TR "v[18:19],v2 offset:31744\n"
MF "a[36:39],v[20:23],v[108:111],a[36:39]\n"
"v_add_f32 v54,v54,v83\n"
CV "v81,v82,v83\n"
MF "a[32:35],v[20:23],v[104:107],a[32:35]\n"
"s_add_u32 m0,s65,1792\n"
"v_exp_f32 v84,v84\n"
MF "a[28:31],v[20:23],v[100:103],a[28:31]\n"
LD "v8,s[36:39],s73 offen offset:3328 nt lds\n"
MF "a[24:27],v[20:23],v[96:99],a[24:27]\n"
"v_exp_f32 v85,v85\n"
MF "a[20:23],v[20:23],v[92:95],a[20:23]\n"
"s_waitcnt lgkmcnt(4)\n"
TR "v[20:21],v3 offset:25600\n"
TR "v[22:23],v3 offset:31744\n"
MF "a[40:43],v[24:27],v[92:95],a[40:43]\n"
"v_exp_f32 v86,v86\n"
MF "a[44:47],v[24:27],v[96:99],a[44:47]\n"
"v_exp_f32 v87,v87\n"
MF "a[48:51],v[24:27],v[100:103],a[48:51]\n"
"v_add_f32 v55,v60,v84\n"
"v_add_f32 v55,v55,v85\n"
MF "a[52:55],v[24:27],v[104:107],a[52:55]\n"
CV "v84,v84,v85\n"
"v_add_f32 v55,v55,v86\n"
MF "a[56:59],v[24:27],v[108:111],a[56:59]\n"
"s_waitcnt lgkmcnt(4)\n"
TR "v[24:25],v4 offset:25600\n"
TR "v[26:27],v4 offset:31744\n"
MF "a[76:79],v[28:31],v[108:111],a[76:79]\n"
"v_add_f32 v55,v55,v87\n"
CV "v85,v86,v87\n"
MF "a[72:75],v[28:31],v[104:107],a[72:75]\n"
"v_exp_f32 v88,v88\n"
MF "a[68:71],v[28:31],v[100:103],a[68:71]\n"
"v_exp_f32 v89,v89\n"
MF "a[64:67],v[28:31],v[96:99],a[64:67]\n"
"v_exp_f32 v90,v90\n"
MF "a[60:63],v[28:31],v[92:95],a[60:63]\n"
"s_waitcnt lgkmcnt(4)\n"
TR "v[28:29],v5 offset:25600\n"
TR "v[30:31],v5 offset:31744\n"
MF "a[80:83],v[16:19],v[92:95],a[80:83]\n"
"v_exp_f32 v91,v91\n"
MF "a[84:87],v[16:19],v[96:99],a[84:87]\n"
"v_add_f32 v56,v61,v88\n"
"v_add_f32 v56,v56,v89\n"
MF "a[88:91],v[16:19],v[100:103],a[88:91]\n"
CV "v88,v88,v89\n"
"v_add_f32 v56,v56,v90\n"
MF "a[92:95],v[16:19],v[104:107],a[92:95]\n"
"v_add_f32 v56,v56,v91\n"
CV "v89,v90,v91\n"
MF "a[96:99],v[16:19],v[108:111],a[96:99]\n"
"s_waitcnt lgkmcnt(4)\n"
TR "v[16:17],v2 offset:26624\n"
TR "v[18:19],v2 offset:32768\n"
MF "a[116:119],v[20:23],v[108:111],a[116:119]\n"
"v_exp_f32 v112,v112\n"
MF "a[112:115],v[20:23],v[104:107],a[112:115]\n"
"v_exp_f32 v113,v113\n"
MF "a[108:111],v[20:23],v[100:103],a[108:111]\n"
"v_exp_f32 v114,v114\n"
MF "a[104:107],v[20:23],v[96:99],a[104:107]\n"
"v_exp_f32 v115,v115\n"
MF "a[100:103],v[20:23],v[92:95],a[100:103]\n"
"s_waitcnt lgkmcnt(4)\n"
TR "v[20:21],v3 offset:26624\n"
TR "v[22:23],v3 offset:32768\n"
MF "a[120:123],v[24:27],v[92:95],a[120:123]\n"
"v_add_f32 v52,v52,v112\n"
"v_add_f32 v52,v52,v113\n"
MF "a[124:127],v[24:27],v[96:99],a[124:127]\n"
CV "v74,v112,v113\n"
"v_add_f32 v52,v52,v114\n"
MF "a[128:131],v[24:27],v[100:103],a[128:131]\n"
"v_add_f32 v52,v52,v115\n"
CV "v75,v114,v115\n"
MF "a[132:135],v[24:27],v[104:107],a[132:135]\n"
"v_exp_f32 v116,v116\n"
MF "a[136:139],v[24:27],v[108:111],a[136:139]\n"
"s_waitcnt lgkmcnt(4)\n"
TR "v[24:25],v4 offset:26624\n"
TR "v[26:27],v4 offset:32768\n"
MF "a[156:159],v[28:31],v[108:111],a[156:159]\n"
"v_exp_f32 v117,v117\n"
MF "a[152:155],v[28:31],v[104:107],a[152:155]\n"
"v_exp_f32 v118,v118\n"
MF "a[148:151],v[28:31],v[100:103],a[148:151]\n"
"v_exp_f32 v119,v119\n"
MF "a[144:147],v[28:31],v[96:99],a[144:147]\n"
"v_add_f32 v53,v53,v116\n"
"v_add_f32 v53,v53,v117\n"
MF "a[140:143],v[28:31],v[92:95],a[140:143]\n"
"s_waitcnt lgkmcnt(4)\n"
TR "v[28:29],v5 offset:26624\n"
TR "v[30:31],v5 offset:32768\n"
MF "a[160:163],v[16:19],v[92:95],a[160:163]\n"
CV "v78,v116,v117\n"
"v_add_f32 v53,v53,v118\n"
MF "a[164:167],v[16:19],v[96:99],a[164:167]\n"
"v_add_f32 v53,v53,v119\n"
CV "v79,v118,v119\n"
MF "a[168:171],v[16:19],v[100:103],a[168:171]\n"
"v_exp_f32 v120,v120\n"
MF "a[172:175],v[16:19],v[104:107],a[172:175]\n"
"v_exp_f32 v121,v121\n"
MF "a[176:179],v[16:19],v[108:111],a[176:179]\n"
"s_waitcnt lgkmcnt(4)\n"
"v_exp_f32 v122,v122\n"
MF "a[196:199],v[20:23],v[108:111],a[196:199]\n"
"v_exp_f32 v123,v123\n"
MF "a[192:195],v[20:23],v[104:107],a[192:195]\n"
"v_add_f32 v54,v54,v120\n"
"v_add_f32 v54,v54,v121\n"
MF "a[188:191],v[20:23],v[100:103],a[188:191]\n"
CV "v82,v120,v121\n"
"v_add_f32 v54,v54,v122\n"
MF "a[184:187],v[20:23],v[96:99],a[184:187]\n"
"s_waitcnt vmcnt(6) lgkmcnt(0)\n"
"s_barrier\n"
MF "a[180:183],v[20:23],v[92:95],a[180:183]\n"
"s_add_u32 s72,s72,6144\n"
"ds_read_b128 v[16:19],v0 offset:49152\n"
"ds_read_b128 v[20:23],v1 offset:49152\n"
MF "a[200:203],v[24:27],v[92:95],a[200:203]\n"
"s_cmp_eq_u32 s72,s17\n"
"s_cselect_b32 s72,s16,s72\n"
"s_add_u32 m0,s64,24576\n"
MF "a[204:207],v[24:27],v[96:99],a[204:207]\n"
LD "v7,s[32:35],s72 offen nt lds\n"
MF "a[208:211],v[24:27],v[100:103],a[208:211]\n"
"s_add_u32 m0,s64,25472\n"
"v_add_f32 v54,v54,v123\n"
CV "v83,v122,v123\n"
MF "a[212:215],v[24:27],v[104:107],a[212:215]\n"
LD "v7,s[32:35],s72 offen offset:128 nt lds\n"
MF "a[216:219],v[24:27],v[108:111],a[216:219]\n"
"s_add_u32 m0,s64,26368\n"
"ds_read_b128 v[24:27],v0 offset:50176\n"
MF "a[236:239],v[28:31],v[108:111],a[236:239]\n"
LD "v7,s[32:35],s72 offen offset:256 nt lds\n"
MF "a[232:235],v[28:31],v[104:107],a[232:235]\n"
MF "a[228:231],v[28:31],v[100:103],a[228:231]\n"
MF "a[224:227],v[28:31],v[96:99],a[224:227]\n"
MF "a[220:223],v[28:31],v[92:95],a[220:223]\n"
"s_waitcnt lgkmcnt(1)\n"
MF "v[92:95],v[16:19],v[136:139],v[32:35]\n"
"s_add_u32 s72,s72,6144\n"
"s_add_u32 m0,s64,30720\n"
"ds_read_b128 v[28:31],v1 offset:50176\n"
MF "v[92:95],v[20:23],v[156:159],v[92:95]\n"
LD "v7,s[32:35],s72 offen nt lds\n"
MF "v[96:99],v[20:23],v[160:163],v[36:39]\n"
"v_exp_f32 v124,v124\n"
MF "v[96:99],v[16:19],v[140:143],v[96:99]\n"
"v_exp_f32 v125,v125\n"
MF "v[100:103],v[16:19],v[144:147],v[40:43]\n"
"s_add_u32 m0,s64,31616\n"
"v_exp_f32 v126,v126\n"
MF "v[100:103],v[20:23],v[164:167],v[100:103]\n"
LD "v7,s[32:35],s72 offen offset:128 nt lds\n"
MF "v[104:107],v[20:23],v[168:171],v[44:47]\n"
"v_exp_f32 v127,v127\n"
MF "v[104:107],v[16:19],v[148:151],v[104:107]\n"
"s_add_u32 m0,s64,32512\n"
"v_add_f32 v55,v55,v124\n"
"v_add_f32 v55,v55,v125\n"
MF "v[108:111],v[16:19],v[152:155],v[48:51]\n"
LD "v7,s[32:35],s72 offen offset:256 nt lds\n"
MF "v[108:111],v[20:23],v[172:175],v[108:111]\n"
"s_waitcnt lgkmcnt(0)\n"
"ds_read_b128 v[16:19],v0 offset:51200\n"
"ds_read_b128 v[20:23],v1 offset:51200\n"
MF "v[108:111],v[24:27],v[192:195],v[108:111]\n"
CV "v86,v124,v125\n"
"v_add_f32 v55,v55,v126\n"
MF "v[108:111],v[28:31],v[212:215],v[108:111]\n"
"v_add_f32 v55,v55,v127\n"
CV "v87,v126,v127\n"
MF "v[104:107],v[28:31],v[208:211],v[104:107]\n"
"v_exp_f32 v128,v128\n"
MF "v[104:107],v[24:27],v[188:191],v[104:107]\n"
"v_exp_f32 v129,v129\n"
MF "v[100:103],v[24:27],v[184:187],v[100:103]\n"
"v_exp_f32 v130,v130\n"
MF "v[100:103],v[28:31],v[204:207],v[100:103]\n"
"v_exp_f32 v131,v131\n"
MF "v[96:99],v[28:31],v[200:203],v[96:99]\n"
"v_add_f32 v56,v56,v128\n"
"v_add_f32 v56,v56,v129\n"
MF "v[96:99],v[24:27],v[180:183],v[96:99]\n"
CV "v90,v128,v129\n"
"v_add_f32 v56,v56,v130\n"
MF "v[92:95],v[24:27],v[176:179],v[92:95]\n"
"v_add_f32 v56,v56,v131\n"
CV "v91,v130,v131\n"
MF "v[92:95],v[28:31],v[196:199],v[92:95]\n"
"s_waitcnt lgkmcnt(0)\n"
"ds_read_b128 v[24:27],v0 offset:55296\n"
"ds_read_b128 v[28:31],v1 offset:55296\n"
MF "v[92:95],v[16:19],v[216:219],v[92:95]\n"
MF "v[92:95],v[20:23],v[236:239],v[92:95]\n"
MF "v[96:99],v[20:23],v[240:243],v[96:99]\n"
MF "v[96:99],v[16:19],v[220:223],v[96:99]\n"
MF "v[100:103],v[16:19],v[224:227],v[100:103]\n"
MF "v[100:103],v[20:23],v[244:247],v[100:103]\n"
MF "v[104:107],v[20:23],v[248:251],v[104:107]\n"
MF "v[104:107],v[16:19],v[228:231],v[104:107]\n"
"v_max3_f32 v10,v52,v53,v54\n"
"v_max3_f32 v10,v10,v55,v56\n"
MF "v[108:111],v[16:19],v[232:235],v[108:111]\n"
"v_cmp_ngt_f32 vcc,s77,v10\n"
MF "v[108:111],v[20:23],v[252:255],v[108:111]\n"
"s_waitcnt lgkmcnt(0)\n"
"s_cbranch_vccnz .Lstub_16\n"
".Lback_16:\n"
MF "v[112:115],v[24:27],v[136:139],v[32:35]\n"
"s_add_u32 s74,s74,12288\n"
"ds_read_b128 v[16:19],v0 offset:56320\n"
"ds_read_b128 v[20:23],v1 offset:56320\n"
MF "v[112:115],v[28:31],v[156:159],v[112:115]\n"
"s_cmp_eq_u32 s74,s20\n"
"s_cselect_b32 s74,s70,s74\n"
MF "v[116:119],v[28:31],v[160:163],v[36:39]\n"
"v_exp_f32 v92,v92\n"
MF "v[116:119],v[24:27],v[140:143],v[116:119]\n"
"v_exp_f32 v93,v93\n"
MF "v[120:123],v[24:27],v[144:147],v[40:43]\n"
"v_exp_f32 v94,v94\n"
MF "v[120:123],v[28:31],v[164:167],v[120:123]\n"
"v_exp_f32 v95,v95\n"
MF "v[124:127],v[28:31],v[168:171],v[44:47]\n"
"v_add_f32 v57,v52,v92\n"
"v_add_f32 v57,v57,v93\n"
MF "v[124:127],v[24:27],v[148:151],v[124:127]\n"
CV "v92,v92,v93\n"
"v_add_f32 v57,v57,v94\n"
MF "v[128:131],v[24:27],v[152:155],v[48:51]\n"
"v_add_f32 v57,v57,v95\n"
CV "v93,v94,v95\n"
MF "v[128:131],v[28:31],v[172:175],v[128:131]\n"
"s_waitcnt lgkmcnt(0)\n"
"ds_read_b128 v[24:27],v0 offset:57344\n"
"ds_read_b128 v[28:31],v1 offset:57344\n"
MF "v[128:131],v[16:19],v[192:195],v[128:131]\n"
"s_sub_u32 s61,s61,1\n"
"v_exp_f32 v96,v96\n"
MF "v[128:131],v[20:23],v[212:215],v[128:131]\n"
"s_cmp_eq_u32 s61,1\n"
"s_cselect_b32 s95,0,s95\n"
"v_add_f32 v58,v53,v96\n"
MF "v[124:127],v[20:23],v[208:211],v[124:127]\n"
"v_exp_f32 v97,v97\n"
MF "v[124:127],v[16:19],v[188:191],v[124:127]\n"
"v_exp_f32 v98,v98\n"
MF "v[120:123],v[16:19],v[184:187],v[120:123]\n"
"v_exp_f32 v99,v99\n"
MF "v[120:123],v[20:23],v[204:207],v[120:123]\n"
"v_add_f32 v58,v58,v97\n"
CV "v96,v96,v97\n"
MF "v[116:119],v[20:23],v[200:203],v[116:119]\n"
"v_add_f32 v58,v58,v98\n"
"v_add_f32 v58,v58,v99\n"
MF "v[116:119],v[16:19],v[180:183],v[116:119]\n"
"s_waitcnt vmcnt(6) lgkmcnt(0)\n"
"s_barrier\n"
MF "v[112:115],v[16:19],v[176:179],v[112:115]\n"
CV "v97,v98,v99\n"
MF "v[112:115],v[20:23],v[196:199],v[112:115]\n"
"s_nop 0\n"
TR "v[16:17],v2\n"
TR "v[18:19],v2 offset:6144\n"
MF "v[112:115],v[24:27],v[216:219],v[112:115]\n"
"s_add_u32 s73,s73,12288\n"
TR "v[20:21],v3\n"
TR "v[22:23],v3 offset:6144\n"
MF "v[112:115],v[28:31],v[236:239],v[112:115]\n"
"s_cmp_eq_u32 s73,s19\n"
"s_cselect_b32 s73,s18,s73\n"
"s_add_u32 m0,s65,49152\n"
MF "v[116:119],v[28:31],v[240:243],v[116:119]\n"
LD "v8,s[36:39],s73 offen nt lds\n"
MF "v[116:119],v[24:27],v[220:223],v[116:119]\n"
"s_add_u32 m0,s65,50048\n"
"v_exp_f32 v100,v100\n"
MF "v[120:123],v[24:27],v[224:227],v[120:123]\n"
LD "v8,s[36:39],s73 offen offset:128 nt lds\n"
MF "v[120:123],v[28:31],v[244:247],v[120:123]\n"
"v_exp_f32 v101,v101\n"
MF "v[124:127],v[28:31],v[248:251],v[124:127]\n"
"s_add_u32 m0,s65,50944\n"
"v_exp_f32 v102,v102\n"
MF "v[124:127],v[24:27],v[228:231],v[124:127]\n"
LD "v8,s[36:39],s73 offen offset:256 nt lds\n"
MF "v[128:131],v[24:27],v[232:235],v[128:131]\n"
"v_exp_f32 v103,v103\n"
MF "v[128:131],v[28:31],v[252:255],v[128:131]\n"
"s_waitcnt lgkmcnt(2)\n"
TR "v[24:25],v4\n"
TR "v[26:27],v4 offset:6144\n"
MF "a[0:3],v[16:19],v[72:75],a[0:3]\n"
"s_add_u32 m0,s65,49152\n"
TR "v[28:29],v5\n"
TR "v[30:31],v5 offset:6144\n"
MF "a[4:7],v[16:19],v[76:79],a[4:7]\n"
LD "v8,s[36:39],s73 offen offset:3072 nt lds\n"
MF "a[8:11],v[16:19],v[80:83],a[8:11]\n"
"s_add_u32 m0,s65,50048\n"
"v_add_f32 v59,v54,v100\n"
"v_add_f32 v59,v59,v101\n"
MF "a[12:15],v[16:19],v[84:87],a[12:15]\n"
LD "v8,s[36:39],s73 offen offset:3200 nt lds\n"
MF "a[16:19],v[16:19],v[88:91],a[16:19]\n"
"s_waitcnt lgkmcnt(4)\n"
TR "v[16:17],v2 offset:1024\n"
TR "v[18:19],v2 offset:7168\n"
MF "a[36:39],v[20:23],v[88:91],a[36:39]\n"
CV "v100,v100,v101\n"
"v_add_f32 v59,v59,v102\n"
MF "a[32:35],v[20:23],v[84:87],a[32:35]\n"
"s_add_u32 m0,s65,50944\n"
"v_add_f32 v59,v59,v103\n"
CV "v101,v102,v103\n"
MF "a[28:31],v[20:23],v[80:83],a[28:31]\n"
LD "v8,s[36:39],s73 offen offset:3328 nt lds\n"
MF "a[24:27],v[20:23],v[76:79],a[24:27]\n"
"v_exp_f32 v104,v104\n"
MF "a[20:23],v[20:23],v[72:75],a[20:23]\n"
"s_waitcnt lgkmcnt(4)\n"
TR "v[20:21],v3 offset:1024\n"
TR "v[22:23],v3 offset:7168\n"
MF "a[40:43],v[24:27],v[72:75],a[40:43]\n"
"v_exp_f32 v105,v105\n"
MF "a[44:47],v[24:27],v[76:79],a[44:47]\n"
"v_exp_f32 v106,v106\n"
MF "a[48:51],v[24:27],v[80:83],a[48:51]\n"
"v_exp_f32 v107,v107\n"
MF "a[52:55],v[24:27],v[84:87],a[52:55]\n"
"v_add_f32 v60,v55,v104\n"
"v_add_f32 v60,v60,v105\n"
MF "a[56:59],v[24:27],v[88:91],a[56:59]\n"
"s_waitcnt lgkmcnt(4)\n"
TR "v[24:25],v4 offset:1024\n"
TR "v[26:27],v4 offset:7168\n"
MF "a[76:79],v[28:31],v[88:91],a[76:79]\n"
CV "v104,v104,v105\n"
"v_add_f32 v60,v60,v106\n"
MF "a[72:75],v[28:31],v[84:87],a[72:75]\n"
"v_add_f32 v60,v60,v107\n"
CV "v105,v106,v107\n"
MF "a[68:71],v[28:31],v[80:83],a[68:71]\n"
"v_exp_f32 v108,v108\n"
MF "a[64:67],v[28:31],v[76:79],a[64:67]\n"
"v_exp_f32 v109,v109\n"
MF "a[60:63],v[28:31],v[72:75],a[60:63]\n"
"s_waitcnt lgkmcnt(4)\n"
TR "v[28:29],v5 offset:1024\n"
TR "v[30:31],v5 offset:7168\n"
MF "a[80:83],v[16:19],v[72:75],a[80:83]\n"
"v_exp_f32 v110,v110\n"
MF "a[84:87],v[16:19],v[76:79],a[84:87]\n"
"v_exp_f32 v111,v111\n"
MF "a[88:91],v[16:19],v[80:83],a[88:91]\n"
"v_add_f32 v61,v56,v108\n"
"v_add_f32 v61,v61,v109\n"
MF "a[92:95],v[16:19],v[84:87],a[92:95]\n"
CV "v108,v108,v109\n"
"v_add_f32 v61,v61,v110\n"
MF "a[96:99],v[16:19],v[88:91],a[96:99]\n"
"s_waitcnt lgkmcnt(4)\n"
TR "v[16:17],v2 offset:2048\n"
TR "v[18:19],v2 offset:8192\n"
MF "a[116:119],v[20:23],v[88:91],a[116:119]\n"
"v_add_f32 v61,v61,v111\n"
CV "v109,v110,v111\n"
MF "a[112:115],v[20:23],v[84:87],a[112:115]\n"
"v_exp_f32 v112,v112\n"
MF "a[108:111],v[20:23],v[80:83],a[108:111]\n"
"v_exp_f32 v113,v113\n"
MF "a[104:107],v[20:23],v[76:79],a[104:107]\n"
"v_exp_f32 v114,v114\n"
MF "a[100:103],v[20:23],v[72:75],a[100:103]\n"
"s_waitcnt lgkmcnt(4)\n"
TR "v[20:21],v3 offset:2048\n"
TR "v[22:23],v3 offset:8192\n"
MF "a[120:123],v[24:27],v[72:75],a[120:123]\n"
"v_exp_f32 v115,v115\n"
MF "a[124:127],v[24:27],v[76:79],a[124:127]\n"
"v_add_f32 v57,v57,v112\n"
"v_add_f32 v57,v57,v113\n"
MF "a[128:131],v[24:27],v[80:83],a[128:131]\n"
CV "v94,v112,v113\n"
"v_add_f32 v57,v57,v114\n"
MF "a[132:135],v[24:27],v[84:87],a[132:135]\n"
"v_add_f32 v57,v57,v115\n"
CV "v95,v114,v115\n"
MF "a[136:139],v[24:27],v[88:91],a[136:139]\n"
"s_waitcnt lgkmcnt(4)\n"
TR "v[24:25],v4 offset:2048\n"
TR "v[26:27],v4 offset:8192\n"
MF "a[156:159],v[28:31],v[88:91],a[156:159]\n"
"v_exp_f32 v116,v116\n"
MF "a[152:155],v[28:31],v[84:87],a[152:155]\n"
"v_exp_f32 v117,v117\n"
MF "a[148:151],v[28:31],v[80:83],a[148:151]\n"
"v_exp_f32 v118,v118\n"
MF "a[144:147],v[28:31],v[76:79],a[144:147]\n"
"v_exp_f32 v119,v119\n"
MF "a[140:143],v[28:31],v[72:75],a[140:143]\n"
"s_waitcnt lgkmcnt(4)\n"
TR "v[28:29],v5 offset:2048\n"
TR "v[30:31],v5 offset:8192\n"
MF "a[160:163],v[16:19],v[72:75],a[160:163]\n"
"v_add_f32 v58,v58,v116\n"
"v_add_f32 v58,v58,v117\n"
MF "a[164:167],v[16:19],v[76:79],a[164:167]\n"
CV "v98,v116,v117\n"
"v_add_f32 v58,v58,v118\n"
MF "a[168:171],v[16:19],v[80:83],a[168:171]\n"
"v_add_f32 v58,v58,v119\n"
CV "v99,v118,v119\n"
MF "a[172:175],v[16:19],v[84:87],a[172:175]\n"
"v_exp_f32 v120,v120\n"
MF "a[176:179],v[16:19],v[88:91],a[176:179]\n"
"s_waitcnt lgkmcnt(4)\n"
"v_exp_f32 v121,v121\n"
MF "a[196:199],v[20:23],v[88:91],a[196:199]\n"
"v_exp_f32 v122,v122\n"
MF "a[192:195],v[20:23],v[84:87],a[192:195]\n"
"v_exp_f32 v123,v123\n"
MF "a[188:191],v[20:23],v[80:83],a[188:191]\n"
"v_add_f32 v59,v59,v120\n"
"v_add_f32 v59,v59,v121\n"
MF "a[184:187],v[20:23],v[76:79],a[184:187]\n"
"s_waitcnt vmcnt(6) lgkmcnt(0)\n"
"s_barrier\n"
MF "a[180:183],v[20:23],v[72:75],a[180:183]\n"
"s_add_u32 s72,s72,6144\n"
"ds_read_b128 v[16:19],v0 offset:24576\n"
"ds_read_b128 v[20:23],v1 offset:24576\n"
MF "a[200:203],v[24:27],v[72:75],a[200:203]\n"
"s_cmp_eq_u32 s72,s17\n"
"s_cselect_b32 s72,s16,s72\n"
"s_add_u32 m0,s64,0\n"
MF "a[204:207],v[24:27],v[76:79],a[204:207]\n"
LD "v7,s[32:35],s72 offen nt lds\n"
MF "a[208:211],v[24:27],v[80:83],a[208:211]\n"
"s_add_u32 m0,s64,896\n"
CV "v102,v120,v121\n"
"v_add_f32 v59,v59,v122\n"
MF "a[212:215],v[24:27],v[84:87],a[212:215]\n"
LD "v7,s[32:35],s72 offen offset:128 nt lds\n"
MF "a[216:219],v[24:27],v[88:91],a[216:219]\n"
"s_add_u32 m0,s64,1792\n"
"ds_read_b128 v[24:27],v0 offset:25600\n"
"v_add_f32 v59,v59,v123\n"
MF "a[236:239],v[28:31],v[88:91],a[236:239]\n"
LD "v7,s[32:35],s72 offen offset:256 nt lds\n"
MF "a[232:235],v[28:31],v[84:87],a[232:235]\n"
CV "v103,v122,v123\n"
MF "a[228:231],v[28:31],v[80:83],a[228:231]\n"
"s_waitcnt lgkmcnt(1)\n"
MF "a[224:227],v[28:31],v[76:79],a[224:227]\n"
"s_cmp_lg_u32 s61,0\n"
MF "a[220:223],v[28:31],v[72:75],a[220:223]\n"
"s_cbranch_scc1 .Laloop\n"
"v_exp_f32 v124,v124\n"
"v_exp_f32 v125,v125\n"
"v_exp_f32 v126,v126\n"
"v_exp_f32 v127,v127\n"
"v_exp_f32 v128,v128\n"
"v_exp_f32 v129,v129\n"
"v_exp_f32 v130,v130\n"
"v_exp_f32 v131,v131\n"
"v_add_f32 v60,v60,v124\n"
"v_add_f32 v61,v61,v128\n"
"v_add_f32 v60,v60,v125\n"
"v_add_f32 v61,v61,v129\n"
"v_add_f32 v60,v60,v126\n"
"v_add_f32 v61,v61,v130\n"
"v_add_f32 v60,v60,v127\n"
"v_add_f32 v61,v61,v131\n"
CV "v106,v124,v125\n"
CV "v107,v126,v127\n"
CV "v110,v128,v129\n"
CV "v111,v130,v131\n"
"v_max3_f32 v10,v57,v58,v59\n"
"v_max3_f32 v10,v10,v60,v61\n"
"v_cmp_le_f32 vcc,0x47800000,v10\n"
"s_cbranch_vccnz .Lstub_17\n"
".Lback_17:\n"
"s_waitcnt vmcnt(3) lgkmcnt(0)\n"
"s_barrier\n"
"s_and_b32 s58,s56,1\n"
"s_mul_i32 s79,s58,12288\n"
"s_lshr_b32 s58,s56,1\n"
"s_mul_i32 s72,s58,61440\n"
"s_mul_i32 s73,s58,6144\n"
"s_add_u32 s73,s73,s79\n"
"v_lshrrev_b32_e64 v192,7,v9\n"
"v_mul_u32_u24 v192,896,v192\n"
"v_add_u32_e64 v192,v192,v8\n"
"s_add_u32 s88,s88,1024\n"
"s_add_u32 s89,s89,1024\n"
"s_add_u32 s72,s72,122880\n"
"s_lshl_b32 s58,s56,10\n"
"s_add_u32 s58,s58,73728\n"
"v_add_u32 v236,s58,v9\n"
"v_add_u32 v237,65536,v236\n"
TR "v[160:161],v4 offset:50176\n"
TR "v[162:163],v4 offset:56320\n"
TR "v[164:165],v5 offset:50176\n"
TR "v[166:167],v5 offset:56320\n"
TR "v[168:169],v2 offset:51200\n"
TR "v[170:171],v2 offset:57344\n"
TR "v[172:173],v3 offset:51200\n"
TR "v[174:175],v3 offset:57344\n"
TR "v[176:177],v4 offset:51200\n"
TR "v[178:179],v4 offset:57344\n"
TR "v[180:181],v5 offset:51200\n"
TR "v[182:183],v5 offset:57344\n"
TR "v[136:137],v2 offset:49152\n"
TR "v[138:139],v2 offset:55296\n"
TR "v[140:141],v3 offset:49152\n"
TR "v[142:143],v3 offset:55296\n"
TR "v[144:145],v4 offset:49152\n"
TR "v[146:147],v4 offset:55296\n"
TR "v[148:149],v5 offset:49152\n"
TR "v[150:151],v5 offset:55296\n"
TR "v[152:153],v2 offset:50176\n"
TR "v[154:155],v2 offset:56320\n"
TR "v[156:157],v3 offset:50176\n"
TR "v[158:159],v3 offset:56320\n"
"v_mov_b32 v52,v57\n"
"v_mov_b32 v53,v58\n"
"v_mov_b32 v54,v59\n"
"v_mov_b32 v55,v60\n"
"v_mov_b32 v56,v61\n"
"s_add_u32 m0,s73,24576\n"
"s_add_u32 s80,s89,s72\n"
LD "v192,s[48:51],s80 offen lds\n"
"v_add_u32 v193,9216,v192\n"
LD "v193,s[48:51],s80 offen offset:1024 lds\n"
"v_permlane16_swap_b32 v57,v52\n"
"v_permlane16_swap_b32 v58,v53\n"
"v_permlane16_swap_b32 v59,v54\n"
"v_permlane16_swap_b32 v60,v55\n"
"v_permlane16_swap_b32 v61,v56\n"
"v_add_f32 v57,v57,v52\n"
"v_add_f32 v58,v58,v53\n"
"v_add_f32 v59,v59,v54\n"
"v_add_f32 v60,v60,v55\n"
"v_add_f32 v61,v61,v56\n"
"v_mov_b32 v52,v57\n"
"v_mov_b32 v53,v58\n"
"v_mov_b32 v54,v59\n"
"v_mov_b32 v55,v60\n"
"v_mov_b32_e64 v56,v61\n"
"v_add_u32 v194,18432,v192\n"
LD "v194,s[48:51],s80 offen offset:2048 lds\n"
"v_add_u32 v195,27648,v192\n"
LD "v195,s[48:51],s80 offen offset:3072 lds\n"
"v_permlane32_swap_b32 v57,v52\n"
"v_permlane32_swap_b32 v58,v53\n"
"v_permlane32_swap_b32 v59,v54\n"
"v_permlane32_swap_b32 v60,v55\n"
"v_permlane32_swap_b32 v61,v56\n"
"v_add_f32 v57,v57,v52\n"
"v_add_f32 v58,v58,v53\n"
"v_add_f32 v59,v59,v54\n"
"v_add_f32 v60,v60,v55\n"
"v_add_f32 v61,v61,v56\n"
"s_waitcnt lgkmcnt(15)\n"
MF "a[120:123],v[160:163],v[92:95],a[120:123]\n"
MF "a[140:143],v[164:167],v[92:95],a[140:143]\n"
"v_rcp_f32 v208,v57\n"
"v_rcp_f32 v209,v58\n"
"v_rcp_f32 v210,v59\n"
"v_rcp_f32 v211,v60\n"
"v_rcp_f32 v212,v61\n"
MF "a[124:127],v[160:163],v[96:99],a[124:127]\n"
"v_mul_f32_e64 v214,-s13,v208\n"
"v_mul_f32_e64 v215,-s13,v209\n"
"v_mul_f32_e64 v216,-s13,v210\n"
"v_mul_f32_e64 v217,-s13,v211\n"
"v_mul_f32_e64 v218,-s13,v212\n"
"s_add_u32 m0,s73,28672\n"
"v_add_u32 v196,40960,v192\n"
LD "v196,s[48:51],s80 offen lds\n"
AR "v72,a120\n"
AR "v73,a121\n"
AR "v74,a122\n"
MF "a[144:147],v[164:167],v[96:99],a[144:147]\n"
AR "v75,a123\n"
AR "v76,a140\n"
AR "v77,a141\n"
AR "v78,a142\n"
AR "v79,a143\n"
"v_pk_fma_f32 v[72:73],v[72:73],v[208:209],v[214:215] op_sel:[0,0,0] op_sel_hi:[1,0,0]\n"
"v_pk_fma_f32 v[74:75],v[74:75],v[208:209],v[214:215] op_sel:[0,0,0] op_sel_hi:[1,0,0]\n"
"v_pk_fma_f32 v[76:77],v[76:77],v[208:209],v[214:215] op_sel:[0,0,0] op_sel_hi:[1,0,0]\n"
"v_pk_fma_f32 v[78:79],v[78:79],v[208:209],v[214:215] op_sel:[0,0,0] op_sel_hi:[1,0,0]\n"
CV "v112,v72,v73\n"
CV "v113,v74,v75\n"
CV "v114,v76,v77\n"
CV "v115,v78,v79\n"
MF "a[128:131],v[160:163],v[100:103],a[128:131]\n"
"ds_read_b128 a[140:143],v237 offset:20480\n"
AW "a120,v112\n"
AW "a121,v113\n"
AW "a122,v114\n"
AW "a123,v115\n"
"s_waitcnt lgkmcnt(7)\n"
MF "a[44:47],v[144:147],v[96:99],a[44:47]\n"
"v_add_u32 v197,50176,v192\n"
LD "v197,s[48:51],s80 offen offset:1024 lds\n"
AR "v80,a124\n"
AR "v81,a125\n"
AR "v82,a126\n"
MF "a[148:151],v[164:167],v[100:103],a[148:151]\n"
AR "v83,a127\n"
AR "v84,a144\n"
AR "v85,a145\n"
"s_nop 0\n"
AR "v86,a146\n"
AR "v87,a147\n"
"v_pk_fma_f32 v[80:81],v[80:81],v[208:209],v[214:215] op_sel:[0,1,1] op_sel_hi:[1,1,1]\n"
"v_pk_fma_f32 v[82:83],v[82:83],v[208:209],v[214:215] op_sel:[0,1,1] op_sel_hi:[1,1,1]\n"
"v_pk_fma_f32 v[84:85],v[84:85],v[208:209],v[214:215] op_sel:[0,1,1] op_sel_hi:[1,1,1]\n"
"v_pk_fma_f32 v[86:87],v[86:87],v[208:209],v[214:215] op_sel:[0,1,1] op_sel_hi:[1,1,1]\n"
CV "v116,v80,v81\n"
CV "v117,v82,v83\n"
CV "v118,v84,v85\n"
CV "v119,v86,v87\n"
MF "a[132:135],v[160:163],v[104:107],a[132:135]\n"
"ds_read_b128 a[144:147],v236\n"
AW "a124,v116\n"
AW "a125,v117\n"
AW "a126,v118\n"
AW "a127,v119\n"
"s_waitcnt lgkmcnt(6)\n"
MF "a[64:67],v[148:151],v[96:99],a[64:67]\n"
"s_add_u32 m0,s73,0\n"
"s_add_u32 s80,s88,s72\n"
"s_sub_u32 s72,s72,122880\n"
"s_nop 0\n"
LD "v192,s[48:51],s80 offen lds\n"
AR "v72,a128\n"
AR "v73,a129\n"
AR "v74,a130\n"
MF "a[152:155],v[164:167],v[104:107],a[152:155]\n"
AR "v75,a131\n"
AR "v76,a148\n"
AR "v77,a149\n"
AR "v78,a150\n"
AR "v79,a151\n"
"v_pk_fma_f32 v[72:73],v[72:73],v[210:211],v[216:217] op_sel:[0,0,0] op_sel_hi:[1,0,0]\n"
"v_pk_fma_f32 v[74:75],v[74:75],v[210:211],v[216:217] op_sel:[0,0,0] op_sel_hi:[1,0,0]\n"
"v_pk_fma_f32 v[76:77],v[76:77],v[210:211],v[216:217] op_sel:[0,0,0] op_sel_hi:[1,0,0]\n"
"v_pk_fma_f32 v[78:79],v[78:79],v[210:211],v[216:217] op_sel:[0,0,0] op_sel_hi:[1,0,0]\n"
LD "v193,s[48:51],s80 offen offset:1024 lds\n"
CV "v112,v72,v73\n"
CV "v113,v74,v75\n"
CV "v114,v76,v77\n"
CV "v115,v78,v79\n"
MF "a[136:139],v[160:163],v[108:111],a[136:139]\n"
"ds_read_b128 a[148:151],v236 offset:4096\n"
AW "a128,v112\n"
AW "a129,v113\n"
AW "a130,v114\n"
AW "a131,v115\n"
"s_waitcnt lgkmcnt(5)\n"
MF "a[84:87],v[152:155],v[96:99],a[84:87]\n"
LD "v194,s[48:51],s80 offen offset:2048 lds\n"
AR "v80,a132\n"
AR "v81,a133\n"
AR "v82,a134\n"
MF "a[156:159],v[164:167],v[108:111],a[156:159]\n"
AR "v83,a135\n"
AR "v84,a152\n"
AR "v85,a153\n"
"s_nop 0\n"
AR "v86,a154\n"
AR "v87,a155\n"
"v_pk_fma_f32 v[80:81],v[80:81],v[210:211],v[216:217] op_sel:[0,1,1] op_sel_hi:[1,1,1]\n"
"v_pk_fma_f32 v[82:83],v[82:83],v[210:211],v[216:217] op_sel:[0,1,1] op_sel_hi:[1,1,1]\n"
"v_pk_fma_f32 v[84:85],v[84:85],v[210:211],v[216:217] op_sel:[0,1,1] op_sel_hi:[1,1,1]\n"
"v_pk_fma_f32 v[86:87],v[86:87],v[210:211],v[216:217] op_sel:[0,1,1] op_sel_hi:[1,1,1]\n"
CV "v116,v80,v81\n"
CV "v117,v82,v83\n"
CV "v118,v84,v85\n"
CV "v119,v86,v87\n"
MF "a[160:163],v[168:171],v[92:95],a[160:163]\n"
"ds_read_b128 a[152:155],v236 offset:8192\n"
AW "a132,v116\n"
AW "a133,v117\n"
AW "a134,v118\n"
AW "a135,v119\n"
"s_waitcnt lgkmcnt(4)\n"
MF "a[104:107],v[156:159],v[96:99],a[104:107]\n"
LD "v195,s[48:51],s80 offen offset:3072 lds\n"
AR "v72,a136\n"
AR "v73,a137\n"
AR "v74,a138\n"
MF "a[180:183],v[172:175],v[92:95],a[180:183]\n"
AR "v75,a139\n"
AR "v76,a156\n"
AR "v77,a157\n"
"s_nop 0\n"
AR "v78,a158\n"
AR "v79,a159\n"
"v_pk_fma_f32 v[72:73],v[72:73],v[212:213],v[218:219] op_sel:[0,0,0] op_sel_hi:[1,0,0]\n"
"v_pk_fma_f32 v[74:75],v[74:75],v[212:213],v[218:219] op_sel:[0,0,0] op_sel_hi:[1,0,0]\n"
"v_pk_fma_f32 v[76:77],v[76:77],v[212:213],v[218:219] op_sel:[0,0,0] op_sel_hi:[1,0,0]\n"
"v_pk_fma_f32 v[78:79],v[78:79],v[212:213],v[218:219] op_sel:[0,0,0] op_sel_hi:[1,0,0]\n"
CV "v112,v72,v73\n"
CV "v113,v74,v75\n"
CV "v114,v76,v77\n"
CV "v115,v78,v79\n"
MF "a[164:167],v[168:171],v[96:99],a[164:167]\n"
"ds_read_b128 a[156:159],v236 offset:12288\n"
AW "a136,v112\n"
AW "a137,v113\n"
AW "a138,v114\n"
AW "a139,v115\n"
"v_max3_f32 v10,|v72|,|v73|,|v74|\n"
"s_lshl_b32 s58,s56,10\n"
"s_add_u32 s58,s58,159744\n"
"v_max3_f32 v10,v10,|v75|,|v76|\n"
"v_max3_f32 v10,v10,|v77|,|v78|\n"
"v_max3_f32 v10,v10,|v78|,|v79|\n"
"v_mul_f32 v10,s10,v10\n"
"v_lshrrev_b32_e64 v11,2,v9\n"
"v_readlane_b32 s12,v10,3\n"
"v_readlane_b32 s6,v10,22\n"
"v_readlane_b32 s7,v10,41\n"
"v_readlane_b32 s8,v10,60\n"
"v_add_u32 v11,s58,v11\n"
"s_min_u32 s12,s12,s6\n"
"s_min_u32 s7,s7,s8\n"
"s_min_u32 s12,s12,s7\n"
"s_and_b32 s12,s12,0x7f800000\n"
"s_sub_u32 s12,s12,0x14000000\n"
"s_cmp_lt_u32 s12,0x50000000\n"
"s_cselect_b32 s12,s12,0xe7400000\n"
"s_add_u32 s12,s12,0x18c00000\n"
"v_mov_b32 v10,s12\n"
"v_or_b32 v12,0x80000000,v10\n"
"ds_write2st64_b32 v11,v10,v12 offset1:1\n"
MF "a[8:11],v[136:139],v[100:103],a[8:11]\n"
"s_add_u32 m0,s73,4096\n"
"s_nop 0\n"
LD "v196,s[48:51],s80 offen lds\n"
AR "v80,a160\n"
AR "v81,a161\n"
AR "v82,a162\n"
MF "a[184:187],v[172:175],v[96:99],a[184:187]\n"
AR "v83,a163\n"
AR "v84,a180\n"
AR "v85,a181\n"
AR "v86,a182\n"
AR "v87,a183\n"
"v_pk_fma_f32 v[80:81],v[80:81],v[208:209],v[214:215] op_sel:[0,0,0] op_sel_hi:[1,0,0]\n"
"v_pk_fma_f32 v[82:83],v[82:83],v[208:209],v[214:215] op_sel:[0,0,0] op_sel_hi:[1,0,0]\n"
"v_pk_fma_f32 v[84:85],v[84:85],v[208:209],v[214:215] op_sel:[0,0,0] op_sel_hi:[1,0,0]\n"
"v_pk_fma_f32 v[86:87],v[86:87],v[208:209],v[214:215] op_sel:[0,0,0] op_sel_hi:[1,0,0]\n"
LD "v197,s[48:51],s80 offen offset:1024 lds\n"
CV "v116,v80,v81\n"
CV "v117,v82,v83\n"
CV "v118,v84,v85\n"
CV "v119,v86,v87\n"
MF "a[168:171],v[168:171],v[100:103],a[168:171]\n"
"ds_read_b128 a[180:183],v236 offset:16384\n"
AW "a160,v116\n"
AW "a161,v117\n"
AW "a162,v118\n"
AW "a163,v119\n"
MF "a[28:31],v[140:143],v[100:103],a[28:31]\n"
AR "v72,a164\n"
AR "v73,a165\n"
AR "v74,a166\n"
MF "a[188:191],v[172:175],v[100:103],a[188:191]\n"
AR "v75,a167\n"
AR "v76,a184\n"
AR "v77,a185\n"
AR "v78,a186\n"
AR "v79,a187\n"
"v_pk_fma_f32 v[72:73],v[72:73],v[208:209],v[214:215] op_sel:[0,1,1] op_sel_hi:[1,1,1]\n"
"v_pk_fma_f32 v[74:75],v[74:75],v[208:209],v[214:215] op_sel:[0,1,1] op_sel_hi:[1,1,1]\n"
"v_pk_fma_f32 v[76:77],v[76:77],v[208:209],v[214:215] op_sel:[0,1,1] op_sel_hi:[1,1,1]\n"
"v_pk_fma_f32 v[78:79],v[78:79],v[208:209],v[214:215] op_sel:[0,1,1] op_sel_hi:[1,1,1]\n"
CV "v112,v72,v73\n"
CV "v113,v74,v75\n"
CV "v114,v76,v77\n"
CV "v115,v78,v79\n"
MF "a[172:175],v[168:171],v[104:107],a[172:175]\n"
"ds_read_b128 a[184:187],v236 offset:20480\n"
AW "a164,v112\n"
AW "a165,v113\n"
AW "a166,v114\n"
AW "a167,v115\n"
MF "a[48:51],v[144:147],v[100:103],a[48:51]\n"
"s_waitcnt lgkmcnt(0)\n"
"s_barrier\n"
"s_add_u32 m0,s73,49152\n"
"s_add_u32 s80,s89,s72\n"
"s_nop 0\n"
LD "v192,s[48:51],s80 offen lds\n"
AR "v80,a168\n"
AR "v81,a169\n"
AR "v82,a170\n"
MF "a[192:195],v[172:175],v[104:107],a[192:195]\n"
AR "v83,a171\n"
AR "v84,a188\n"
AR "v85,a189\n"
AR "v86,a190\n"
AR "v87,a191\n"
"v_pk_fma_f32 v[80:81],v[80:81],v[210:211],v[216:217] op_sel:[0,0,0] op_sel_hi:[1,0,0]\n"
"v_pk_fma_f32 v[82:83],v[82:83],v[210:211],v[216:217] op_sel:[0,0,0] op_sel_hi:[1,0,0]\n"
"v_pk_fma_f32 v[84:85],v[84:85],v[210:211],v[216:217] op_sel:[0,0,0] op_sel_hi:[1,0,0]\n"
"v_pk_fma_f32 v[86:87],v[86:87],v[210:211],v[216:217] op_sel:[0,0,0] op_sel_hi:[1,0,0]\n"
LD "v193,s[48:51],s80 offen offset:1024 lds\n"
CV "v116,v80,v81\n"
CV "v117,v82,v83\n"
CV "v118,v84,v85\n"
CV "v119,v86,v87\n"
MF "a[176:179],v[168:171],v[108:111],a[176:179]\n"
"v_accvgpr_mov_b32 a188,a240\n"
"v_accvgpr_mov_b32 a189,a241\n"
"v_accvgpr_mov_b32 a190,a242\n"
"v_accvgpr_mov_b32 a191,a243\n"
AW "a168,v116\n"
AW "a169,v117\n"
AW "a170,v118\n"
AW "a171,v119\n"
MF "a[68:71],v[148:151],v[100:103],a[68:71]\n"
LD "v194,s[48:51],s80 offen offset:2048 lds\n"
AR "v72,a172\n"
AR "v73,a173\n"
AR "v74,a174\n"
MF "a[196:199],v[172:175],v[108:111],a[196:199]\n"
AR "v75,a175\n"
AR "v76,a192\n"
AR "v77,a193\n"
AR "v78,a194\n"
AR "v79,a195\n"
"v_pk_fma_f32 v[72:73],v[72:73],v[210:211],v[216:217] op_sel:[0,1,1] op_sel_hi:[1,1,1]\n"
"v_pk_fma_f32 v[74:75],v[74:75],v[210:211],v[216:217] op_sel:[0,1,1] op_sel_hi:[1,1,1]\n"
"v_pk_fma_f32 v[76:77],v[76:77],v[210:211],v[216:217] op_sel:[0,1,1] op_sel_hi:[1,1,1]\n"
"v_pk_fma_f32 v[78:79],v[78:79],v[210:211],v[216:217] op_sel:[0,1,1] op_sel_hi:[1,1,1]\n"
LD "v195,s[48:51],s80 offen offset:3072 lds\n"
CV "v112,v72,v73\n"
CV "v113,v74,v75\n"
CV "v114,v76,v77\n"
CV "v115,v78,v79\n"
MF "a[200:203],v[176:179],v[92:95],a[200:203]\n"
"v_accvgpr_mov_b32 a192,a244\n"
"v_accvgpr_mov_b32 a193,a245\n"
"v_accvgpr_mov_b32 a194,a246\n"
"v_accvgpr_mov_b32 a195,a247\n"
AW "a172,v112\n"
AW "a173,v113\n"
AW "a174,v114\n"
AW "a175,v115\n"
MF "a[88:91],v[152:155],v[100:103],a[88:91]\n"
"s_add_u32 m0,s73,53248\n"
"s_nop 0\n"
LD "v196,s[48:51],s80 offen lds\n"
AR "v80,a176\n"
AR "v81,a177\n"
AR "v82,a178\n"
MF "a[220:223],v[180:183],v[92:95],a[220:223]\n"
AR "v83,a179\n"
AR "v84,a196\n"
AR "v85,a197\n"
"s_nop 0\n"
AR "v86,a198\n"
AR "v87,a199\n"
"v_pk_fma_f32 v[80:81],v[80:81],v[212:213],v[218:219] op_sel:[0,0,0] op_sel_hi:[1,0,0]\n"
"v_pk_fma_f32 v[82:83],v[82:83],v[212:213],v[218:219] op_sel:[0,0,0] op_sel_hi:[1,0,0]\n"
"v_pk_fma_f32 v[84:85],v[84:85],v[212:213],v[218:219] op_sel:[0,0,0] op_sel_hi:[1,0,0]\n"
"v_pk_fma_f32 v[86:87],v[86:87],v[212:213],v[218:219] op_sel:[0,0,0] op_sel_hi:[1,0,0]\n"
CV "v116,v80,v81\n"
CV "v117,v82,v83\n"
CV "v118,v84,v85\n"
CV "v119,v86,v87\n"
MF "a[204:207],v[176:179],v[96:99],a[204:207]\n"
"v_accvgpr_mov_b32 a196,a248\n"
"v_accvgpr_mov_b32 a197,a249\n"
"v_accvgpr_mov_b32 a198,a250\n"
"v_accvgpr_mov_b32 a199,a251\n"
AW "a176,v116\n"
AW "a177,v117\n"
AW "a178,v118\n"
AW "a179,v119\n"
MF "a[108:111],v[156:159],v[100:103],a[108:111]\n"
LD "v197,s[48:51],s80 offen offset:1024 lds\n"
AR "v72,a200\n"
AR "v73,a201\n"
AR "v74,a202\n"
MF "a[224:227],v[180:183],v[96:99],a[224:227]\n"
AR "v75,a203\n"
AR "v76,a220\n"
AR "v77,a221\n"
AR "v78,a222\n"
AR "v79,a223\n"
"v_pk_fma_f32 v[72:73],v[72:73],v[208:209],v[214:215] op_sel:[0,0,0] op_sel_hi:[1,0,0]\n"
"v_pk_fma_f32 v[74:75],v[74:75],v[208:209],v[214:215] op_sel:[0,0,0] op_sel_hi:[1,0,0]\n"
"v_pk_fma_f32 v[76:77],v[76:77],v[208:209],v[214:215] op_sel:[0,0,0] op_sel_hi:[1,0,0]\n"
"v_pk_fma_f32 v[78:79],v[78:79],v[208:209],v[214:215] op_sel:[0,0,0] op_sel_hi:[1,0,0]\n"
CV "v112,v72,v73\n"
CV "v113,v74,v75\n"
CV "v114,v76,v77\n"
CV "v115,v78,v79\n"
MF "a[208:211],v[176:179],v[100:103],a[208:211]\n"
AW "a220,v62\n"
AW "a221,v63\n"
AW "a222,v64\n"
AW "a223,v65\n"
AW "a200,v112\n"
AW "a201,v113\n"
AW "a202,v114\n"
AW "a203,v115\n"
MF "a[12:15],v[136:139],v[104:107],a[12:15]\n"
"s_add_u32 m0,s73,73728\n"
"s_add_u32 s80,s88,s72\n"
LD "v192,s[48:51],s80 offen lds\n"
AR "v80,a204\n"
AR "v81,a205\n"
AR "v82,a206\n"
MF "a[228:231],v[180:183],v[100:103],a[228:231]\n"
AR "v83,a207\n"
AR "v84,a224\n"
AR "v85,a225\n"
"s_nop 0\n"
AR "v86,a226\n"
AR "v87,a227\n"
"v_pk_fma_f32 v[80:81],v[80:81],v[208:209],v[214:215] op_sel:[0,1,1] op_sel_hi:[1,1,1]\n"
"v_pk_fma_f32 v[82:83],v[82:83],v[208:209],v[214:215] op_sel:[0,1,1] op_sel_hi:[1,1,1]\n"
"v_pk_fma_f32 v[84:85],v[84:85],v[208:209],v[214:215] op_sel:[0,1,1] op_sel_hi:[1,1,1]\n"
"v_pk_fma_f32 v[86:87],v[86:87],v[208:209],v[214:215] op_sel:[0,1,1] op_sel_hi:[1,1,1]\n"
LD "v193,s[48:51],s80 offen offset:1024 lds\n"
CV "v116,v80,v81\n"
CV "v117,v82,v83\n"
CV "v118,v84,v85\n"
CV "v119,v86,v87\n"
MF "a[212:215],v[176:179],v[104:107],a[212:215]\n"
AW "a224,v66\n"
AW "a225,v67\n"
AW "a226,v68\n"
AW "a227,v69\n"
AW "a204,v116\n"
AW "a205,v117\n"
AW "a206,v118\n"
AW "a207,v119\n"
MF "a[32:35],v[140:143],v[104:107],a[32:35]\n"
LD "v194,s[48:51],s80 offen offset:2048 lds\n"
AR "v72,a208\n"
AR "v73,a209\n"
AR "v74,a210\n"
MF "a[232:235],v[180:183],v[104:107],a[232:235]\n"
AR "v75,a211\n"
AR "v76,a228\n"
AR "v77,a229\n"
AR "v78,a230\n"
AR "v79,a231\n"
"v_pk_fma_f32 v[72:73],v[72:73],v[210:211],v[216:217] op_sel:[0,0,0] op_sel_hi:[1,0,0]\n"
"v_pk_fma_f32 v[74:75],v[74:75],v[210:211],v[216:217] op_sel:[0,0,0] op_sel_hi:[1,0,0]\n"
"v_pk_fma_f32 v[76:77],v[76:77],v[210:211],v[216:217] op_sel:[0,0,0] op_sel_hi:[1,0,0]\n"
"v_pk_fma_f32 v[78:79],v[78:79],v[210:211],v[216:217] op_sel:[0,0,0] op_sel_hi:[1,0,0]\n"
CV "v112,v72,v73\n"
CV "v113,v74,v75\n"
CV "v114,v76,v77\n"
CV "v115,v78,v79\n"
MF "a[216:219],v[176:179],v[108:111],a[216:219]\n"
AW "a228,v70\n"
AW "a229,v71\n"
AW "a230,v132\n"
AW "a231,v133\n"
AW "a208,v112\n"
AW "a209,v113\n"
AW "a210,v114\n"
AW "a211,v115\n"
MF "a[52:55],v[144:147],v[104:107],a[52:55]\n"
LD "v195,s[48:51],s80 offen offset:3072 lds\n"
AR "v80,a212\n"
AR "v81,a213\n"
AR "v82,a214\n"
MF "a[236:239],v[180:183],v[108:111],a[236:239]\n"
AR "v83,a215\n"
AR "v84,a232\n"
AR "v85,a233\n"
AR "v86,a234\n"
AR "v87,a235\n"
"v_pk_fma_f32 v[80:81],v[80:81],v[210:211],v[216:217] op_sel:[0,1,1] op_sel_hi:[1,1,1]\n"
"v_pk_fma_f32 v[82:83],v[82:83],v[210:211],v[216:217] op_sel:[0,1,1] op_sel_hi:[1,1,1]\n"
"v_pk_fma_f32 v[84:85],v[84:85],v[210:211],v[216:217] op_sel:[0,1,1] op_sel_hi:[1,1,1]\n"
"v_pk_fma_f32 v[86:87],v[86:87],v[210:211],v[216:217] op_sel:[0,1,1] op_sel_hi:[1,1,1]\n"
CV "v116,v80,v81\n"
CV "v117,v82,v83\n"
CV "v118,v84,v85\n"
CV "v119,v86,v87\n"
MF "a[0:3],v[136:139],v[92:95],a[0:3]\n"
AW "a232,v134\n"
AW "a233,v135\n"
AW "a234,v6\n"
AW "a235,v13\n"
AW "a212,v116\n"
AW "a213,v117\n"
AW "a214,v118\n"
AW "a215,v119\n"
MF "a[72:75],v[148:151],v[104:107],a[72:75]\n"
"s_add_u32 m0,s73,77824\n"
"s_nop 0\n"
LD "v196,s[48:51],s80 offen lds\n"
AR "v72,a216\n"
AR "v73,a217\n"
AR "v74,a218\n"
MF "a[20:23],v[140:143],v[92:95],a[20:23]\n"
AR "v75,a219\n"
AR "v76,a236\n"
AR "v77,a237\n"
"s_nop 0\n"
AR "v78,a238\n"
AR "v79,a239\n"
"v_pk_fma_f32 v[72:73],v[72:73],v[212:213],v[218:219] op_sel:[0,0,0] op_sel_hi:[1,0,0]\n"
"v_pk_fma_f32 v[74:75],v[74:75],v[212:213],v[218:219] op_sel:[0,0,0] op_sel_hi:[1,0,0]\n"
"v_pk_fma_f32 v[76:77],v[76:77],v[212:213],v[218:219] op_sel:[0,0,0] op_sel_hi:[1,0,0]\n"
"v_pk_fma_f32 v[78:79],v[78:79],v[212:213],v[218:219] op_sel:[0,0,0] op_sel_hi:[1,0,0]\n"
LD "v197,s[48:51],s80 offen offset:1024 lds\n"
CV "v112,v72,v73\n"
CV "v113,v74,v75\n"
CV "v114,v76,v77\n"
CV "v115,v78,v79\n"
MF "a[40:43],v[144:147],v[92:95],a[40:43]\n"
AW "a236,v14\n"
AW "a237,v15\n"
"v_accvgpr_mov_b32 a238,a254\n"
"v_accvgpr_mov_b32 a239,a255\n"
AW "a216,v112\n"
AW "a217,v113\n"
AW "a218,v114\n"
AW "a219,v115\n"
MF "a[92:95],v[152:155],v[104:107],a[92:95]\n"
AR "v80,a0\n"
AR "v81,a1\n"
AR "v82,a2\n"
MF "a[60:63],v[148:151],v[92:95],a[60:63]\n"
AR "v83,a3\n"
AR "v84,a20\n"
AR "v85,a21\n"
AR "v86,a22\n"
AR "v87,a23\n"
"v_pk_fma_f32 v[80:81],v[80:81],v[208:209],v[214:215] op_sel:[0,0,0] op_sel_hi:[1,0,0]\n"
"v_pk_fma_f32 v[82:83],v[82:83],v[208:209],v[214:215] op_sel:[0,0,0] op_sel_hi:[1,0,0]\n"
"v_pk_fma_f32 v[84:85],v[84:85],v[208:209],v[214:215] op_sel:[0,0,0] op_sel_hi:[1,0,0]\n"
"v_pk_fma_f32 v[86:87],v[86:87],v[208:209],v[214:215] op_sel:[0,0,0] op_sel_hi:[1,0,0]\n"
CV "v116,v80,v81\n"
CV "v117,v82,v83\n"
CV "v118,v84,v85\n"
CV "v119,v86,v87\n"
MF "a[80:83],v[152:155],v[92:95],a[80:83]\n"
"ds_read_b128 a[20:23],v236 offset:24576\n"
AW "a0,v116\n"
AW "a1,v117\n"
AW "a2,v118\n"
AW "a3,v119\n"
MF "a[112:115],v[156:159],v[104:107],a[112:115]\n"
AR "v72,a40\n"
AR "v73,a41\n"
AR "v74,a42\n"
MF "a[100:103],v[156:159],v[92:95],a[100:103]\n"
AR "v75,a43\n"
AR "v76,a60\n"
AR "v77,a61\n"
AR "v78,a62\n"
AR "v79,a63\n"
"v_pk_fma_f32 v[72:73],v[72:73],v[208:209],v[214:215] op_sel:[0,0,0] op_sel_hi:[1,0,0]\n"
"v_pk_fma_f32 v[74:75],v[74:75],v[208:209],v[214:215] op_sel:[0,0,0] op_sel_hi:[1,0,0]\n"
"v_pk_fma_f32 v[76:77],v[76:77],v[208:209],v[214:215] op_sel:[0,0,0] op_sel_hi:[1,0,0]\n"
"v_pk_fma_f32 v[78:79],v[78:79],v[208:209],v[214:215] op_sel:[0,0,0] op_sel_hi:[1,0,0]\n"
CV "v112,v72,v73\n"
CV "v113,v74,v75\n"
CV "v114,v76,v77\n"
CV "v115,v78,v79\n"
MF "a[4:7],v[136:139],v[96:99],a[4:7]\n"
"ds_read_b128 a[60:63],v236 offset:45056\n"
AW "a40,v112\n"
AW "a41,v113\n"
AW "a42,v114\n"
AW "a43,v115\n"
MF "a[16:19],v[136:139],v[108:111],a[16:19]\n"
AR "v80,a80\n"
AR "v81,a81\n"
AR "v82,a82\n"
MF "a[24:27],v[140:143],v[96:99],a[24:27]\n"
AR "v83,a83\n"
AR "v84,a100\n"
AR "v85,a101\n"
AR "v86,a102\n"
AR "v87,a103\n"
"v_pk_fma_f32 v[80:81],v[80:81],v[208:209],v[214:215] op_sel:[0,0,0] op_sel_hi:[1,0,0]\n"
"v_pk_fma_f32 v[82:83],v[82:83],v[208:209],v[214:215] op_sel:[0,0,0] op_sel_hi:[1,0,0]\n"
"v_pk_fma_f32 v[84:85],v[84:85],v[208:209],v[214:215] op_sel:[0,0,0] op_sel_hi:[1,0,0]\n"
"v_pk_fma_f32 v[86:87],v[86:87],v[208:209],v[214:215] op_sel:[0,0,0] op_sel_hi:[1,0,0]\n"
CV "v116,v80,v81\n"
CV "v117,v82,v83\n"
CV "v118,v84,v85\n"
CV "v119,v86,v87\n"
MF "a[36:39],v[140:143],v[108:111],a[36:39]\n"
"ds_read_b128 a[100:103],v237\n"
AW "a80,v116\n"
AW "a81,v117\n"
AW "a82,v118\n"
AW "a83,v119\n"
MF "a[56:59],v[144:147],v[108:111],a[56:59]\n"
AR "v72,a4\n"
AR "v73,a5\n"
AR "v74,a6\n"
MF "a[76:79],v[148:151],v[108:111],a[76:79]\n"
AR "v75,a7\n"
AR "v76,a24\n"
AR "v77,a25\n"
AR "v78,a26\n"
AR "v79,a27\n"
"v_pk_fma_f32 v[72:73],v[72:73],v[208:209],v[214:215] op_sel:[0,1,1] op_sel_hi:[1,1,1]\n"
"v_pk_fma_f32 v[74:75],v[74:75],v[208:209],v[214:215] op_sel:[0,1,1] op_sel_hi:[1,1,1]\n"
"v_pk_fma_f32 v[76:77],v[76:77],v[208:209],v[214:215] op_sel:[0,1,1] op_sel_hi:[1,1,1]\n"
"v_pk_fma_f32 v[78:79],v[78:79],v[208:209],v[214:215] op_sel:[0,1,1] op_sel_hi:[1,1,1]\n"
CV "v112,v72,v73\n"
CV "v113,v74,v75\n"
CV "v114,v76,v77\n"
CV "v115,v78,v79\n"
MF "a[96:99],v[152:155],v[108:111],a[96:99]\n"
"ds_read_b128 a[24:27],v236 offset:28672\n"
AW "a4,v112\n"
AW "a5,v113\n"
AW "a6,v114\n"
AW "a7,v115\n"
MF "a[116:119],v[156:159],v[108:111],a[116:119]\n"
"s_mov_b32 s13,s11\n"
"v_and_b32 v0,512,v9\n"
"v_lshlrev_b32 v0,2,v0\n"
"v_sub_u32 v0,v2,v0\n"
"s_bfe_u32 s58,s22,0x10014\n"
"s_lshl_b32 s58,s58,5\n"
"v_xor_b32 v0,s58,v0\n"
"s_and_b32 s58,s56,1\n"
"s_lshl_b32 s58,s58,10\n"
"s_lshr_b32 s59,s56,1\n"
"s_lshl_b32 s59,s59,8\n"
"s_add_u32 s58,s58,s59\n"
"s_add_u32 s58,s58,159744\n"
"v_mov_b32 v252,s58\n"
"v_xor_b32 v1,32,v0\n"
"v_xor_b32_e64 v2,64,v0\n"
"v_xor_b32 v3,96,v0\n"
"v_add_u32 v4,73728,v0\n"
"v_add_u32 v5,73728,v1\n"
"v_add_u32 v6,73728,v2\n"
"v_add_u32 v7,73728,v3\n"
"s_waitcnt vmcnt(12) lgkmcnt(0)\n"
"s_barrier\n"
"ds_read_b128 v[252:255],v252\n"
TR "v[176:177],v0 offset:24576\n"
TR "v[178:179],v0 offset:26624\n"
TR "v[180:181],v0 offset:28672\n"
TR "v[182:183],v0 offset:30720\n"
TR "v[184:185],v0 offset:32768\n"
TR "v[186:187],v0 offset:34816\n"
"s_waitcnt lgkmcnt(0)\n"
"s_cmp_ge_u32 s56,2\n"
"s_cbranch_scc1 .Lrole1\n"
MF "v[16:19],a[120:123],v[176:179],v[252:255]\n"
"s_mov_b32 s65,128\n"
TR "v[188:189],v1 offset:24576\n"
AR "v220,a44\n"
MF "v[16:19],a[160:163],v[180:183],v[16:19]\n"
"s_mov_b32 s9,0x3f80\n"
TR "v[190:191],v1 offset:26624\n"
AR "v221,a45\n"
MF "v[16:19],a[200:203],v[184:187],v[16:19]\n"
"s_add_u32 s89,s89,s65\n"
AR "v222,a46\n"
AR "v223,a47\n"
MF "v[24:27],a[204:207],v[184:187],v[252:255]\n"
"s_bfe_u32 s58,s22,0x30014\n"
TR "a[240:241],v1 offset:28672\n"
AR "v224,a64\n"
MF "v[24:27],a[164:167],v[180:183],v[24:27]\n"
"s_add_u32 s61,s58,1\n"
TR "a[242:243],v1 offset:30720\n"
AR "v225,a65\n"
MF "v[24:27],a[124:127],v[176:179],v[24:27]\n"
"s_and_b32 s61,s61,7\n"
AR "v226,a66\n"
AR "v227,a67\n"
MF "v[32:35],a[128:131],v[176:179],v[252:255]\n"
"s_mov_b32 s64,9\n"
TR "a[244:245],v1 offset:32768\n"
"ds_read_b128 a[64:67],v236 offset:49152\n"
MF "v[32:35],a[168:171],v[180:183],v[32:35]\n"
"s_xor_b32 s67,s78,8\n"
TR "a[246:247],v1 offset:34816\n"
"v_fma_f32 v220,v220,v209,v215\n"
MF "v[32:35],a[208:211],v[184:187],v[32:35]\n"
"s_mov_b32 s71,204800\n"
"v_fma_f32 v221,v221,v209,v215\n"
"v_fma_f32 v222,v222,v209,v215\n"
MF "v[40:43],a[212:215],v[184:187],v[252:255]\n"
"s_mov_b32 s66,204800\n"
"v_fma_f32 v223,v223,v209,v215\n"
"v_fma_f32 v224,v224,v209,v215\n"
MF "v[40:43],a[172:175],v[180:183],v[40:43]\n"
"s_and_b32 s73,s56,1\n"
"v_fma_f32 v225,v225,v209,v215\n"
"v_fma_f32 v226,v226,v209,v215\n"
MF "v[40:43],a[132:135],v[176:179],v[40:43]\n"
"s_mul_i32 s73,s73,5120\n"
"v_fma_f32 v227,v227,v209,v215\n"
CV "v220,v220,v221\n"
MF "v[48:51],a[136:139],v[176:179],v[252:255]\n"
"s_add_u32 s73,s73,s93\n"
CV "v221,v222,v223\n"
CV "v222,v224,v225\n"
MF "v[48:51],a[176:179],v[180:183],v[48:51]\n"
"s_xor_b32 s58,s58,4\n"
CV "v223,v226,v227\n"
AW "a44,v220\n"
MF "v[48:51],a[216:219],v[184:187],v[48:51]\n"
"s_waitcnt lgkmcnt(0)\n"
AW "a45,v221\n"
AW "a46,v222\n"
MF "v[52:55],a[216:219],a[244:247],v[252:255]\n"
"s_mul_i32 s70,s58,204800\n"
TR "v[176:177],v2 offset:24576\n"
AW "a47,v223\n"
MF "v[52:55],a[176:179],a[240:243],v[52:55]\n"
"s_add_u32 s70,s70,s73\n"
TR "v[178:179],v2 offset:26624\n"
AR "v228,a84\n"
MF "v[52:55],a[136:139],v[188:191],v[52:55]\n"
"s_sub_u32 s70,s70,40960\n"
AR "v229,a85\n"
AR "v230,a86\n"
MF "v[44:47],a[132:135],v[188:191],v[252:255]\n"
"s_mov_b32 s85,1\n"
TR "v[180:181],v2 offset:28672\n"
AR "v231,a87\n"
MF "v[44:47],a[172:175],a[240:243],v[44:47]\n"
"s_mov_b32 s83,0xf0000000\n"
TR "v[182:183],v2 offset:30720\n"
AR "v232,a104\n"
MF "v[44:47],a[212:215],a[244:247],v[44:47]\n"
"s_mov_b32 s82,0xf0000000\n"
AR "v233,a105\n"
AR "v234,a106\n"
MF "v[36:39],a[208:211],a[244:247],v[252:255]\n"
"s_add_u32 s58,s79,73728\n"
TR "v[184:185],v2 offset:32768\n"
AR "v235,a107\n"
MF "v[36:39],a[168:171],a[240:243],v[36:39]\n"
"s_nop 0\n"
TR "v[186:187],v2 offset:34816\n"
"ds_read_b128 a[104:107],v237 offset:4096\n"
MF "v[36:39],a[128:131],v[188:191],v[36:39]\n"
"v_fma_f32 v228,v228,v209,v215\n"
"v_fma_f32 v229,v229,v209,v215\n"
MF "v[28:31],a[124:127],v[188:191],v[252:255]\n"
"v_fma_f32 v230,v230,v209,v215\n"
"v_fma_f32 v231,v231,v209,v215\n"
MF "v[28:31],a[164:167],a[240:243],v[28:31]\n"
"v_fma_f32 v232,v232,v209,v215\n"
"v_fma_f32 v233,v233,v209,v215\n"
MF "v[28:31],a[204:207],a[244:247],v[28:31]\n"
"v_fma_f32 v234,v234,v209,v215\n"
"v_fma_f32 v235,v235,v209,v215\n"
MF "v[20:23],a[200:203],a[244:247],v[252:255]\n"
CV "v228,v228,v229\n"
CV "v229,v230,v231\n"
MF "v[20:23],a[160:163],a[240:243],v[20:23]\n"
CV "v230,v232,v233\n"
CV "v231,v234,v235\n"
MF "v[20:23],a[120:123],v[188:191],v[20:23]\n"
"s_waitcnt lgkmcnt(1)\n"
AW "a84,v228\n"
AW "a85,v229\n"
MF "v[56:59],a[120:123],v[176:179],v[252:255]\n"
TR "v[188:189],v3 offset:24576\n"
AW "a86,v230\n"
MF "v[56:59],a[160:163],v[180:183],v[56:59]\n"
TR "v[190:191],v3 offset:26624\n"
AW "a87,v231\n"
MF "v[56:59],a[200:203],v[184:187],v[56:59]\n"
AR "v220,a8\n"
AR "v221,a9\n"
MF "v[64:67],a[204:207],v[184:187],v[252:255]\n"
TR "a[240:241],v3 offset:28672\n"
AR "v222,a10\n"
MF "v[64:67],a[164:167],v[180:183],v[64:67]\n"
TR "a[242:243],v3 offset:30720\n"
AR "v223,a11\n"
MF "v[64:67],a[124:127],v[176:179],v[64:67]\n"
AR "v224,a28\n"
AR "v225,a29\n"
MF "v[72:75],a[128:131],v[176:179],v[252:255]\n"
TR "a[244:245],v3 offset:32768\n"
AR "v226,a30\n"
MF "v[72:75],a[168:171],v[180:183],v[72:75]\n"
TR "a[246:247],v3 offset:34816\n"
AR "v227,a31\n"
MF "v[72:75],a[208:211],v[184:187],v[72:75]\n"
"ds_read_b128 a[28:31],v236 offset:32768\n"
"v_fma_f32 v220,v220,v210,v216\n"
MF "v[80:83],a[212:215],v[184:187],v[252:255]\n"
"v_fma_f32 v221,v221,v210,v216\n"
"v_fma_f32 v222,v222,v210,v216\n"
MF "v[80:83],a[172:175],v[180:183],v[80:83]\n"
"v_fma_f32 v223,v223,v210,v216\n"
"v_fma_f32 v224,v224,v210,v216\n"
MF "v[80:83],a[132:135],v[176:179],v[80:83]\n"
"v_fma_f32 v225,v225,v210,v216\n"
"v_fma_f32 v226,v226,v210,v216\n"
MF "v[88:91],a[136:139],v[176:179],v[252:255]\n"
"v_fma_f32 v227,v227,v210,v216\n"
CV "v220,v220,v221\n"
MF "v[88:91],a[176:179],v[180:183],v[88:91]\n"
CV "v221,v222,v223\n"
CV "v222,v224,v225\n"
MF "v[88:91],a[216:219],v[184:187],v[88:91]\n"
"s_waitcnt vmcnt(0) lgkmcnt(0)\n"
"s_barrier\n"
MF "v[92:95],a[216:219],a[244:247],v[252:255]\n"
TR "v[176:177],v0\n"
CV "v223,v226,v227\n"
MF "v[92:95],a[176:179],a[240:243],v[92:95]\n"
TR "v[178:179],v0 offset:2048\n"
AW "a8,v220\n"
MF "v[92:95],a[136:139],v[188:191],v[92:95]\n"
TR "v[96:97],v0 offset:49152\n"
AW "a9,v221\n"
MF "v[84:87],a[132:135],v[188:191],v[252:255]\n"
TR "v[180:181],v0 offset:4096\n"
AW "a10,v222\n"
MF "v[84:87],a[172:175],a[240:243],v[84:87]\n"
TR "v[182:183],v0 offset:6144\n"
AW "a11,v223\n"
MF "v[84:87],a[212:215],a[244:247],v[84:87]\n"
TR "v[98:99],v0 offset:51200\n"
AR "v228,a48\n"
MF "v[76:79],a[208:211],a[244:247],v[252:255]\n"
TR "v[184:185],v0 offset:8192\n"
AR "v229,a49\n"
MF "v[76:79],a[168:171],a[240:243],v[76:79]\n"
TR "v[186:187],v0 offset:10240\n"
AR "v230,a50\n"
MF "v[76:79],a[128:131],v[188:191],v[76:79]\n"
TR "v[100:101],v0 offset:53248\n"
AR "v231,a51\n"
MF "v[68:71],a[124:127],v[188:191],v[252:255]\n"
TR "v[102:103],v0 offset:55296\n"
AR "v232,a68\n"
MF "v[68:71],a[164:167],a[240:243],v[68:71]\n"
TR "v[104:105],v0 offset:57344\n"
AR "v233,a69\n"
MF "v[68:71],a[204:207],a[244:247],v[68:71]\n"
TR "v[106:107],v0 offset:59392\n"
AR "v234,a70\n"
MF "v[60:63],a[200:203],a[244:247],v[252:255]\n"
TR "v[108:109],v4\n"
AR "v235,a71\n"
MF "v[60:63],a[160:163],a[240:243],v[60:63]\n"
"s_nop 0\n"
TR "v[110:111],v4 offset:2048\n"
"ds_read_b128 a[68:71],v236 offset:53248\n"
MF "v[60:63],a[120:123],v[188:191],v[60:63]\n"
"s_waitcnt lgkmcnt(7)\n"
TR "v[112:113],v4 offset:4096\n"
"v_fma_f32 v228,v228,v210,v216\n"
MF "v[16:19],a[140:143],v[176:179],v[16:19]\n"
TR "v[188:189],v1\n"
"v_fma_f32 v229,v229,v210,v216\n"
MF "v[16:19],a[180:183],v[180:183],v[16:19]\n"
TR "v[190:191],v1 offset:2048\n"
"v_fma_f32 v230,v230,v210,v216\n"
MF "v[16:19],a[220:223],v[184:187],v[16:19]\n"
TR "v[114:115],v4 offset:6144\n"
"v_fma_f32 v231,v231,v210,v216\n"
MF "v[24:27],a[224:227],v[184:187],v[24:27]\n"
TR "a[240:241],v1 offset:4096\n"
"v_fma_f32 v232,v232,v210,v216\n"
MF "v[24:27],a[184:187],v[180:183],v[24:27]\n"
TR "a[242:243],v1 offset:6144\n"
"v_fma_f32 v233,v233,v210,v216\n"
MF "v[24:27],a[144:147],v[176:179],v[24:27]\n"
TR "v[116:117],v4 offset:8192\n"
"v_fma_f32 v234,v234,v210,v216\n"
MF "v[32:35],a[148:151],v[176:179],v[32:35]\n"
TR "a[244:245],v1 offset:8192\n"
"v_fma_f32 v235,v235,v210,v216\n"
MF "v[32:35],a[188:191],v[180:183],v[32:35]\n"
TR "a[246:247],v1 offset:10240\n"
CV "v228,v228,v229\n"
MF "v[32:35],a[228:231],v[184:187],v[32:35]\n"
TR "v[118:119],v4 offset:10240\n"
CV "v229,v230,v231\n"
MF "v[40:43],a[232:235],v[184:187],v[40:43]\n"
TR "v[120:121],v1 offset:49152\n"
CV "v230,v232,v233\n"
MF "v[40:43],a[192:195],v[180:183],v[40:43]\n"
TR "v[122:123],v1 offset:51200\n"
CV "v231,v234,v235\n"
MF "v[40:43],a[152:155],v[176:179],v[40:43]\n"
TR "v[124:125],v1 offset:53248\n"
AW "a48,v228\n"
MF "v[48:51],a[156:159],v[176:179],v[48:51]\n"
TR "v[126:127],v1 offset:55296\n"
AW "a49,v229\n"
MF "v[48:51],a[196:199],v[180:183],v[48:51]\n"
TR "v[128:129],v1 offset:57344\n"
AW "a50,v230\n"
MF "v[48:51],a[236:239],v[184:187],v[48:51]\n"
"s_waitcnt lgkmcnt(6)\n"
TR "v[130:131],v1 offset:59392\n"
AW "a51,v231\n"
MF "v[52:55],a[236:239],a[244:247],v[52:55]\n"
TR "v[176:177],v2\n"
AR "v220,a88\n"
MF "v[52:55],a[196:199],a[240:243],v[52:55]\n"
TR "v[178:179],v2 offset:2048\n"
AR "v221,a89\n"
MF "v[52:55],a[156:159],v[188:191],v[52:55]\n"
TR "v[132:133],v5\n"
AR "v222,a90\n"
MF "v[44:47],a[152:155],v[188:191],v[44:47]\n"
TR "v[180:181],v2 offset:4096\n"
AR "v223,a91\n"
MF "v[44:47],a[192:195],a[240:243],v[44:47]\n"
TR "v[182:183],v2 offset:6144\n"
AR "v224,a108\n"
MF "v[44:47],a[232:235],a[244:247],v[44:47]\n"
TR "v[134:135],v5 offset:2048\n"
AR "v225,a109\n"
MF "v[36:39],a[228:231],a[244:247],v[36:39]\n"
TR "v[184:185],v2 offset:8192\n"
AR "v226,a110\n"
MF "v[36:39],a[188:191],a[240:243],v[36:39]\n"
TR "v[186:187],v2 offset:10240\n"
AR "v227,a111\n"
MF "v[36:39],a[148:151],v[188:191],v[36:39]\n"
"s_nop 0\n"
TR "v[136:137],v5 offset:4096\n"
"ds_read_b128 a[108:111],v237 offset:8192\n"
MF "v[28:31],a[144:147],v[188:191],v[28:31]\n"
TR "v[138:139],v5 offset:6144\n"
"v_fma_f32 v220,v220,v210,v216\n"
MF "v[28:31],a[184:187],a[240:243],v[28:31]\n"
TR "v[140:141],v5 offset:8192\n"
"v_fma_f32 v221,v221,v210,v216\n"
MF "v[28:31],a[224:227],a[244:247],v[28:31]\n"
TR "v[142:143],v5 offset:10240\n"
"v_fma_f32 v222,v222,v210,v216\n"
MF "v[20:23],a[220:223],a[244:247],v[20:23]\n"
TR "v[144:145],v2 offset:49152\n"
"v_fma_f32 v223,v223,v210,v216\n"
MF "v[20:23],a[180:183],a[240:243],v[20:23]\n"
TR "v[146:147],v2 offset:51200\n"
"v_fma_f32 v224,v224,v210,v216\n"
MF "v[20:23],a[140:143],v[188:191],v[20:23]\n"
"s_waitcnt lgkmcnt(7)\n"
TR "v[148:149],v2 offset:53248\n"
"v_fma_f32 v225,v225,v210,v216\n"
MF "v[56:59],a[140:143],v[176:179],v[56:59]\n"
TR "v[188:189],v3\n"
"v_add_u32 v198,59392,v192\n"
MF "v[56:59],a[180:183],v[180:183],v[56:59]\n"
TR "v[190:191],v3 offset:2048\n"
"v_add_u32 v199,68608,v192\n"
MF "v[56:59],a[220:223],v[184:187],v[56:59]\n"
TR "v[150:151],v2 offset:55296\n"
"v_add_u32 v200,81920,v192\n"
"v_add_u32 v201,91136,v192\n"
MF "v[64:67],a[224:227],v[184:187],v[64:67]\n"
TR "a[240:241],v3 offset:4096\n"
"v_add_u32 v202,100352,v192\n"
MF "v[64:67],a[184:187],v[180:183],v[64:67]\n"
TR "a[242:243],v3 offset:6144\n"
"v_add_u32 v203,109568,v192\n"
MF "v[64:67],a[144:147],v[176:179],v[64:67]\n"
TR "v[152:153],v2 offset:57344\n"
"v_fma_f32 v226,v226,v210,v216\n"
MF "v[72:75],a[148:151],v[176:179],v[72:75]\n"
TR "a[244:245],v3 offset:8192\n"
"v_fma_f32 v227,v227,v210,v216\n"
MF "v[72:75],a[188:191],v[180:183],v[72:75]\n"
TR "a[246:247],v3 offset:10240\n"
CV "v220,v220,v221\n"
MF "v[72:75],a[228:231],v[184:187],v[72:75]\n"
TR "v[154:155],v2 offset:59392\n"
CV "v221,v222,v223\n"
MF "v[80:83],a[232:235],v[184:187],v[80:83]\n"
TR "v[156:157],v6\n"
CV "v222,v224,v225\n"
MF "v[80:83],a[192:195],v[180:183],v[80:83]\n"
TR "v[158:159],v6 offset:2048\n"
CV "v223,v226,v227\n"
MF "v[80:83],a[152:155],v[176:179],v[80:83]\n"
TR "v[160:161],v6 offset:4096\n"
AW "a88,v220\n"
MF "v[88:91],a[156:159],v[176:179],v[88:91]\n"
TR "v[162:163],v6 offset:6144\n"
AW "a89,v221\n"
MF "v[88:91],a[196:199],v[180:183],v[88:91]\n"
TR "v[164:165],v6 offset:8192\n"
AW "a90,v222\n"
MF "v[88:91],a[236:239],v[184:187],v[88:91]\n"
"s_waitcnt lgkmcnt(0)\n"
"s_barrier\n"
MF "v[92:95],a[236:239],a[244:247],v[92:95]\n"
"s_add_u32 s80,s89,122880\n"
"s_add_u32 m0,s79,0\n"
TR "v[166:167],v6 offset:10240\n"
MF "v[92:95],a[196:199],a[240:243],v[92:95]\n"
LD "v192,s[48:51],s80 offen lds\n"
MF "v[92:95],a[156:159],v[188:191],v[92:95]\n"
TR "v[168:169],v3 offset:49152\n"
AW "a91,v223\n"
MF "v[84:87],a[152:155],v[188:191],v[84:87]\n"
TR "v[170:171],v3 offset:51200\n"
AR "v228,a12\n"
MF "v[84:87],a[192:195],a[240:243],v[84:87]\n"
TR "v[172:173],v3 offset:53248\n"
AR "v229,a13\n"
MF "v[84:87],a[232:235],a[244:247],v[84:87]\n"
LD "v193,s[48:51],s80 offen offset:1024 lds\n"
MF "v[76:79],a[228:231],a[244:247],v[76:79]\n"
TR "v[174:175],v3 offset:55296\n"
AR "v230,a14\n"
MF "v[76:79],a[188:191],a[240:243],v[76:79]\n"
TR "v[204:205],v3 offset:57344\n"
AR "v231,a15\n"
MF "v[76:79],a[148:151],v[188:191],v[76:79]\n"
TR "v[206:207],v3 offset:59392\n"
AR "v232,a32\n"
MF "v[68:71],a[144:147],v[188:191],v[68:71]\n"
TR "v[244:245],v7\n"
AR "v233,a33\n"
MF "v[68:71],a[184:187],a[240:243],v[68:71]\n"
LD "v194,s[48:51],s80 offen offset:2048 lds\n"
MF "v[68:71],a[224:227],a[244:247],v[68:71]\n"
TR "v[246:247],v7 offset:2048\n"
AR "v234,a34\n"
MF "v[60:63],a[220:223],a[244:247],v[60:63]\n"
TR "v[248:249],v7 offset:4096\n"
AR "v235,a35\n"
MF "v[60:63],a[180:183],a[240:243],v[60:63]\n"
"s_nop 0\n"
TR "v[250:251],v7 offset:6144\n"
"ds_read_b128 a[32:35],v236 offset:36864\n"
MF "v[60:63],a[140:143],v[188:191],v[60:63]\n"
LD "v195,s[48:51],s80 offen offset:3072 lds\n"
MF "v[16:19],a[0:3],v[96:99],v[16:19]\n"
TR "v[238:239],v7 offset:8192\n"
"v_fma_f32 v228,v228,v211,v217\n"
MF "v[16:19],a[40:43],v[100:103],v[16:19]\n"
TR "v[240:241],v7 offset:10240\n"
"v_fma_f32 v229,v229,v211,v217\n"
MF "v[16:19],a[80:83],v[104:107],v[16:19]\n"
"v_fma_f32 v230,v230,v211,v217\n"
"v_fma_f32 v231,v231,v211,v217\n"
MF "v[16:19],a[20:23],v[108:111],v[16:19]\n"
"s_add_u32 m0,s79,4096\n"
"v_fma_f32 v232,v232,v211,v217\n"
"v_fma_f32 v233,v233,v211,v217\n"
MF "v[16:19],a[60:63],v[112:115],v[16:19]\n"
LD "v196,s[48:51],s80 offen lds\n"
MF "v[16:19],a[100:103],v[116:119],v[16:19]\n"
"v_fma_f32 v234,v234,v211,v217\n"
"v_fma_f32 v235,v235,v211,v217\n"
MF "v[20:23],a[100:103],v[140:143],v[20:23]\n"
CV "v228,v228,v229\n"
CV "v229,v230,v231\n"
MF "v[20:23],a[60:63],v[136:139],v[20:23]\n"
CV "v230,v232,v233\n"
CV "v231,v234,v235\n"
MF "v[20:23],a[20:23],v[132:135],v[20:23]\n"
LD "v197,s[48:51],s80 offen offset:1024 lds\n"
MF "v[20:23],a[80:83],v[128:131],v[20:23]\n"
AW "a12,v228\n"
AW "a13,v229\n"
MF "v[20:23],a[40:43],v[124:127],v[20:23]\n"
AW "a14,v230\n"
AW "a15,v231\n"
MF "v[20:23],a[0:3],v[120:123],v[20:23]\n"
"s_waitcnt lgkmcnt(13)\n"
AR "v220,a52\n"
AR "v221,a53\n"
MF "v[56:59],a[0:3],v[144:147],v[56:59]\n"
AR "v222,a54\n"
AR "v223,a55\n"
MF "v[56:59],a[40:43],v[148:151],v[56:59]\n"
LD "v198,s[48:51],s80 offen offset:2048 lds\n"
MF "v[56:59],a[80:83],v[152:155],v[56:59]\n"
AR "v224,a72\n"
AR "v225,a73\n"
MF "v[56:59],a[20:23],v[156:159],v[56:59]\n"
AR "v226,a74\n"
AR "v227,a75\n"
MF "v[56:59],a[60:63],v[160:163],v[56:59]\n"
"ds_read_b128 a[72:75],v236 offset:57344\n"
"v_fma_f32 v220,v220,v211,v217\n"
MF "v[56:59],a[100:103],v[164:167],v[56:59]\n"
"s_waitcnt lgkmcnt(1)\n"
"v_fma_f32 v221,v221,v211,v217\n"
"v_fma_f32 v222,v222,v211,v217\n"
MF "v[60:63],a[100:103],v[238:241],v[60:63]\n"
LD "v199,s[48:51],s80 offen offset:3072 lds\n"
MF "v[60:63],a[60:63],v[248:251],v[60:63]\n"
"v_fma_f32 v223,v223,v211,v217\n"
"v_fma_f32 v224,v224,v211,v217\n"
MF "v[60:63],a[20:23],v[244:247],v[60:63]\n"
"v_fma_f32 v225,v225,v211,v217\n"
"v_fma_f32 v226,v226,v211,v217\n"
MF "v[60:63],a[80:83],v[204:207],v[60:63]\n"
"s_add_u32 m0,s79,8192\n"
"v_fma_f32 v227,v227,v211,v217\n"
CV "v220,v220,v221\n"
MF "v[60:63],a[40:43],v[172:175],v[60:63]\n"
LD "v200,s[48:51],s80 offen lds\n"
MF "v[60:63],a[0:3],v[168:171],v[60:63]\n"
CV "v221,v222,v223\n"
CV "v222,v224,v225\n"
MF "v[68:71],a[4:7],v[168:171],v[68:71]\n"
CV "v223,v226,v227\n"
AW "a52,v220\n"
MF "v[68:71],a[44:47],v[172:175],v[68:71]\n"
AW "a53,v221\n"
AW "a54,v222\n"
MF "v[68:71],a[84:87],v[204:207],v[68:71]\n"
AW "a55,v223\n"
AR "v228,a92\n"
MF "v[68:71],a[24:27],v[244:247],v[68:71]\n"
LD "v201,s[48:51],s80 offen offset:1024 lds\n"
MF "v[68:71],a[64:67],v[248:251],v[68:71]\n"
AR "v229,a93\n"
AR "v230,a94\n"
MF "v[68:71],a[104:107],v[238:241],v[68:71]\n"
AR "v231,a95\n"
AR "v232,a112\n"
MF "v[64:67],a[104:107],v[164:167],v[64:67]\n"
"s_waitcnt lgkmcnt(0)\n"
"s_barrier\n"
MF "v[64:67],a[64:67],v[160:163],v[64:67]\n"
LD "v202,s[48:51],s80 offen offset:2048 lds\n"
MF "v[64:67],a[24:27],v[156:159],v[64:67]\n"
AR "v233,a113\n"
AR "v234,a114\n"
MF "v[64:67],a[84:87],v[152:155],v[64:67]\n"
AR "v235,a115\n"
"v_fma_f32 v228,v228,v211,v217\n"
MF "v[64:67],a[44:47],v[148:151],v[64:67]\n"
"ds_read_b128 a[112:115],v237 offset:12288\n"
"v_fma_f32 v229,v229,v211,v217\n"
MF "v[64:67],a[4:7],v[144:147],v[64:67]\n"
"v_fma_f32 v230,v230,v211,v217\n"
"v_fma_f32 v231,v231,v211,v217\n"
MF "v[28:31],a[4:7],v[120:123],v[28:31]\n"
LD "v203,s[48:51],s80 offen offset:3072 lds\n"
MF "v[28:31],a[44:47],v[124:127],v[28:31]\n"
"v_fma_f32 v232,v232,v211,v217\n"
"v_fma_f32 v233,v233,v211,v217\n"
MF "v[28:31],a[84:87],v[128:131],v[28:31]\n"
"v_fma_f32 v234,v234,v211,v217\n"
"v_fma_f32 v235,v235,v211,v217\n"
MF "v[28:31],a[24:27],v[132:135],v[28:31]\n"
CV "v228,v228,v229\n"
CV "v229,v230,v231\n"
MF "v[28:31],a[64:67],v[136:139],v[28:31]\n"
CV "v230,v232,v233\n"
CV "v231,v234,v235\n"
MF "v[28:31],a[104:107],v[140:143],v[28:31]\n"
"s_add_u32 s88,s88,s65\n"
"s_add_u32 m0,s79,49152\n"
AW "a92,v228\n"
MF "v[24:27],a[104:107],v[116:119],v[24:27]\n"
LD "v192,s[48:51],s88 offen lds\n"
MF "v[24:27],a[64:67],v[112:115],v[24:27]\n"
AW "a93,v229\n"
AW "a94,v230\n"
MF "v[24:27],a[24:27],v[108:111],v[24:27]\n"
AW "a95,v231\n"
AR "v220,a16\n"
MF "v[24:27],a[84:87],v[104:107],v[24:27]\n"
AR "v221,a17\n"
AR "v222,a18\n"
MF "v[24:27],a[44:47],v[100:103],v[24:27]\n"
LD "v193,s[48:51],s88 offen offset:1024 lds\n"
MF "v[24:27],a[4:7],v[96:99],v[24:27]\n"
AR "v223,a19\n"
AR "v224,a36\n"
MF "v[32:35],a[8:11],v[96:99],v[32:35]\n"
AR "v225,a37\n"
AR "v226,a38\n"
MF "v[32:35],a[48:51],v[100:103],v[32:35]\n"
AR "v227,a39\n"
"v_fma_f32 v220,v220,v212,v218\n"
MF "v[32:35],a[88:91],v[104:107],v[32:35]\n"
"ds_read_b128 a[36:39],v236 offset:40960\n"
"v_fma_f32 v221,v221,v212,v218\n"
MF "v[32:35],a[28:31],v[108:111],v[32:35]\n"
LD "v194,s[48:51],s88 offen offset:2048 lds\n"
MF "v[32:35],a[68:71],v[112:115],v[32:35]\n"
"v_fma_f32 v222,v222,v212,v218\n"
"v_fma_f32 v223,v223,v212,v218\n"
MF "v[32:35],a[108:111],v[116:119],v[32:35]\n"
"v_fma_f32 v224,v224,v212,v218\n"
"v_fma_f32 v225,v225,v212,v218\n"
MF "v[36:39],a[108:111],v[140:143],v[36:39]\n"
"v_fma_f32 v226,v226,v212,v218\n"
"v_fma_f32 v227,v227,v212,v218\n"
MF "v[36:39],a[68:71],v[136:139],v[36:39]\n"
CV "v220,v220,v221\n"
CV "v221,v222,v223\n"
MF "v[36:39],a[28:31],v[132:135],v[36:39]\n"
LD "v195,s[48:51],s88 offen offset:3072 lds\n"
MF "v[36:39],a[88:91],v[128:131],v[36:39]\n"
CV "v222,v224,v225\n"
CV "v223,v226,v227\n"
MF "v[36:39],a[48:51],v[124:127],v[36:39]\n"
AW "a16,v220\n"
AW "a17,v221\n"
MF "v[36:39],a[8:11],v[120:123],v[36:39]\n"
AW "a18,v222\n"
AW "a19,v223\n"
MF "v[72:75],a[8:11],v[144:147],v[72:75]\n"
"s_add_u32 m0,s79,53248\n"
AR "v228,a56\n"
AR "v229,a57\n"
MF "v[72:75],a[48:51],v[148:151],v[72:75]\n"
LD "v196,s[48:51],s88 offen lds\n"
MF "v[72:75],a[88:91],v[152:155],v[72:75]\n"
AR "v230,a58\n"
AR "v231,a59\n"
MF "v[72:75],a[28:31],v[156:159],v[72:75]\n"
AR "v232,a76\n"
AR "v233,a77\n"
MF "v[72:75],a[68:71],v[160:163],v[72:75]\n"
AR "v234,a78\n"
AR "v235,a79\n"
MF "v[72:75],a[108:111],v[164:167],v[72:75]\n"
"ds_read_b128 a[76:79],v236 offset:61440\n"
"v_fma_f32 v228,v228,v212,v218\n"
MF "v[76:79],a[108:111],v[238:241],v[76:79]\n"
LD "v197,s[48:51],s88 offen offset:1024 lds\n"
MF "v[76:79],a[68:71],v[248:251],v[76:79]\n"
"v_fma_f32 v229,v229,v212,v218\n"
"v_fma_f32 v230,v230,v212,v218\n"
MF "v[76:79],a[28:31],v[244:247],v[76:79]\n"
"v_fma_f32 v231,v231,v212,v218\n"
"v_fma_f32 v232,v232,v212,v218\n"
MF "v[76:79],a[88:91],v[204:207],v[76:79]\n"
"v_fma_f32 v233,v233,v212,v218\n"
"v_fma_f32 v234,v234,v212,v218\n"
MF "v[76:79],a[48:51],v[172:175],v[76:79]\n"
"v_fma_f32 v235,v235,v212,v218\n"
CV "v228,v228,v229\n"
MF "v[76:79],a[8:11],v[168:171],v[76:79]\n"
"s_waitcnt lgkmcnt(2)\n"
LD "v198,s[48:51],s88 offen offset:2048 lds\n"
MF "v[84:87],a[12:15],v[168:171],v[84:87]\n"
CV "v229,v230,v231\n"
CV "v230,v232,v233\n"
MF "v[84:87],a[52:55],v[172:175],v[84:87]\n"
CV "v231,v234,v235\n"
AW "a56,v228\n"
MF "v[84:87],a[92:95],v[204:207],v[84:87]\n"
AW "a57,v229\n"
AW "a58,v230\n"
MF "v[84:87],a[32:35],v[244:247],v[84:87]\n"
AW "a59,v231\n"
AR "v220,a96\n"
MF "v[84:87],a[72:75],v[248:251],v[84:87]\n"
LD "v199,s[48:51],s88 offen offset:3072 lds\n"
MF "v[84:87],a[112:115],v[238:241],v[84:87]\n"
AR "v221,a97\n"
AR "v222,a98\n"
MF "v[80:83],a[112:115],v[164:167],v[80:83]\n"
AR "v223,a99\n"
AR "v224,a116\n"
MF "v[80:83],a[72:75],v[160:163],v[80:83]\n"
AR "v225,a117\n"
AR "v226,a118\n"
MF "v[80:83],a[32:35],v[156:159],v[80:83]\n"
"s_add_u32 m0,s79,57344\n"
AR "v227,a119\n"
"v_fma_f32 v220,v220,v212,v218\n"
MF "v[80:83],a[92:95],v[152:155],v[80:83]\n"
LD "v200,s[48:51],s88 offen lds\n"
MF "v[80:83],a[52:55],v[148:151],v[80:83]\n"
"ds_read_b128 a[116:119],v237 offset:16384\n"
"v_fma_f32 v221,v221,v212,v218\n"
MF "v[80:83],a[12:15],v[144:147],v[80:83]\n"
"v_fma_f32 v222,v222,v212,v218\n"
"v_fma_f32 v223,v223,v212,v218\n"
MF "v[44:47],a[12:15],v[120:123],v[44:47]\n"
"v_fma_f32 v224,v224,v212,v218\n"
"v_fma_f32 v225,v225,v212,v218\n"
MF "v[44:47],a[52:55],v[124:127],v[44:47]\n"
"v_fma_f32 v226,v226,v212,v218\n"
"v_fma_f32 v227,v227,v212,v218\n"
MF "v[44:47],a[92:95],v[128:131],v[44:47]\n"
LD "v201,s[48:51],s88 offen offset:1024 lds\n"
MF "v[44:47],a[32:35],v[132:135],v[44:47]\n"
CV "v220,v220,v221\n"
CV "v221,v222,v223\n"
MF "v[44:47],a[72:75],v[136:139],v[44:47]\n"
CV "v222,v224,v225\n"
CV "v223,v226,v227\n"
MF "v[44:47],a[112:115],v[140:143],v[44:47]\n"
AW "a96,v220\n"
AW "a97,v221\n"
MF "v[40:43],a[112:115],v[116:119],v[40:43]\n"
AW "a98,v222\n"
AW "a99,v223\n"
MF "v[40:43],a[72:75],v[112:115],v[40:43]\n"
LD "v202,s[48:51],s88 offen offset:2048 lds\n"
MF "v[40:43],a[32:35],v[108:111],v[40:43]\n"
AR "v8,a252\n"
AR "v11,a253\n"
MF "v[40:43],a[92:95],v[104:107],v[40:43]\n"
"v_bfe_u32 v224,v9,5,3\n"
"v_bfe_u32 v225,v9,8,2\n"
MF "v[40:43],a[52:55],v[100:103],v[40:43]\n"
"v_lshlrev_b32 v225,1,v225\n"
"v_and_b32 v226,16,v9\n"
MF "v[40:43],a[12:15],v[96:99],v[40:43]\n"
"s_waitcnt lgkmcnt(0)\n"
"v_lshlrev_b32_e64 v226,v226,s9\n"
"v_sub_u32 v224,v224,v225\n"
MF "v[48:51],a[16:19],v[96:99],v[48:51]\n"
LD "v203,s[48:51],s88 offen offset:3072 lds\n"
MF "v[48:51],a[56:59],v[100:103],v[48:51]\n"
"v_cmp_eq_u32 vcc,0,v224\n"
MF "v[48:51],a[96:99],v[104:107],v[48:51]\n"
"s_nop 0\n"
"v_cndmask_b32 v220,0,v226,vcc\n"
"v_cmp_eq_u32 vcc,1,v224\n"
MF "v[48:51],a[36:39],v[108:111],v[48:51]\n"
"s_nop 0\n"
"v_cndmask_b32 v221,0,v226,vcc\n"
AW "a248,0\n"
MF "v[48:51],a[76:79],v[112:115],v[48:51]\n"
AW "a249,0\n"
AW "a250,v220\n"
MF "v[48:51],a[116:119],v[116:119],v[48:51]\n"
AW "a251,v221\n"
AW "a252,v220\n"
MF "v[52:55],a[116:119],v[140:143],v[52:55]\n"
AW "a253,v221\n"
AW "a254,0\n"
MF "v[52:55],a[76:79],v[136:139],v[52:55]\n"
AW "a255,0\n"
"v_bfe_u32 v12,v9,4,4\n"
MF "v[52:55],a[36:39],v[132:135],v[52:55]\n"
"s_waitcnt lgkmcnt(0)\n"
"s_barrier\n"
MF "v[52:55],a[96:99],v[128:131],v[52:55]\n"
"s_add_u32 s80,s88,122880\n"
TR "v[176:177],v0 offset:24576\n"
"v_lshlrev_b32 v12,2,v12\n"
MF "v[52:55],a[56:59],v[124:127],v[52:55]\n"
"s_add_u32 m0,s79,98304\n"
TR "v[178:179],v0 offset:26624\n"
"v_lshrrev_b32 v13,2,v9\n"
MF "v[52:55],a[16:19],v[120:123],v[52:55]\n"
LD "v192,s[48:51],s80 offen lds\n"
MF "v[88:91],a[16:19],v[144:147],v[88:91]\n"
"s_nop 0\n"
TR "v[180:181],v0 offset:28672\n"
"ds_write_addtid_b32 v16 offset:24576\n"
MF "v[88:91],a[56:59],v[148:151],v[88:91]\n"
"s_nop 0\n"
TR "v[182:183],v0 offset:30720\n"
"ds_write_addtid_b32 v17 offset:24832\n"
MF "v[88:91],a[96:99],v[152:155],v[88:91]\n"
LD "v193,s[48:51],s80 offen offset:1024 lds\n"
MF "v[88:91],a[36:39],v[156:159],v[88:91]\n"
"s_nop 0\n"
TR "v[184:185],v0 offset:32768\n"
"ds_write_addtid_b32 v18 offset:25088\n"
MF "v[88:91],a[76:79],v[160:163],v[88:91]\n"
"s_nop 0\n"
TR "v[186:187],v0 offset:34816\n"
"ds_write_addtid_b32 v19 offset:25344\n"
MF "v[88:91],a[116:119],v[164:167],v[88:91]\n"
"s_nop 0\n"
"ds_write_addtid_b32 v20 offset:25600\n"
"ds_write_addtid_b32 v21 offset:25856\n"
MF "v[92:95],a[116:119],v[238:241],v[92:95]\n"
LD "v194,s[48:51],s80 offen offset:2048 lds\n"
MF "v[92:95],a[76:79],v[248:251],v[92:95]\n"
"s_nop 0\n"
"ds_write_addtid_b32 v22 offset:26112\n"
"ds_write_addtid_b32 v23 offset:26368\n"
MF "v[92:95],a[36:39],v[244:247],v[92:95]\n"
"s_nop 0\n"
"ds_write_addtid_b32 v24 offset:26624\n"
"ds_write_addtid_b32 v25 offset:26880\n"
MF "v[92:95],a[96:99],v[204:207],v[92:95]\n"
"s_nop 0\n"
"ds_write_addtid_b32 v26 offset:27136\n"
"ds_write_addtid_b32 v27 offset:27392\n"
MF "v[92:95],a[56:59],v[172:175],v[92:95]\n"
"v_add_u32 v13,s58,v13\n"
"v_add_u32 v4,24576,v4\n"
MF "v[92:95],a[16:19],v[168:171],v[92:95]\n"
"s_waitcnt lgkmcnt(9)\n"
"v_add_u32 v5,24576,v5\n"
"v_add_u32 v6,24576,v6\n"
"v_add_u32 v7,24576,v7\n"
".Lpl0:\n"
MF "v[96:99],a[0:3],v[176:179],v[252:255]\n"
"s_nop 0\n"
TR "v[188:189],v1 offset:24576\n"
"ds_write_addtid_b32 v28 offset:27648\n"
MF "v[96:99],a[40:43],v[180:183],v[96:99]\n"
"s_nop 0\n"
TR "v[190:191],v1 offset:26624\n"
"ds_write_addtid_b32 v29 offset:27904\n"
MF "v[96:99],a[80:83],v[184:187],v[96:99]\n"
LD "v195,s[48:51],s80 offen offset:3072 lds\n"
MF "v[104:107],a[84:87],v[184:187],v[252:255]\n"
"s_add_u32 s60,s64,1\n"
TR "a[240:241],v1 offset:28672\n"
"ds_write_addtid_b32 v30 offset:28160\n"
MF "v[104:107],a[44:47],v[180:183],v[104:107]\n"
"s_add_u32 m0,s79,102400\n"
TR "a[242:243],v1 offset:30720\n"
"ds_write_addtid_b32 v31 offset:24320\n"
MF "v[104:107],a[4:7],v[176:179],v[104:107]\n"
LD "v196,s[48:51],s80 offen lds\n"
MF "v[112:115],a[8:11],v[176:179],v[252:255]\n"
"s_cmp_eq_u32 s60,8\n"
"s_cselect_b32 s60,0,s60\n"
TR "a[244:245],v1 offset:32768\n"
MF "v[112:115],a[48:51],v[180:183],v[112:115]\n"
"s_cmp_eq_u32 s60,10\n"
"s_cselect_b32 s60,s61,s60\n"
TR "a[246:247],v1 offset:34816\n"
MF "v[112:115],a[88:91],v[184:187],v[112:115]\n"
"s_sub_u32 s65,s60,s64\n"
"ds_write_addtid_b32 v32 offset:24576\n"
"ds_write_addtid_b32 v33 offset:24832\n"
MF "v[120:123],a[92:95],v[184:187],v[252:255]\n"
"s_lshl_b32 s65,s65,7\n"
"ds_write_addtid_b32 v34 offset:25088\n"
"ds_write_addtid_b32 v35 offset:25344\n"
MF "v[120:123],a[52:55],v[180:183],v[120:123]\n"
LD "v197,s[48:51],s80 offen offset:1024 lds\n"
MF "v[120:123],a[12:15],v[176:179],v[120:123]\n"
"s_mov_b32 s64,s60\n"
"ds_write_addtid_b32 v36 offset:25600\n"
"ds_write_addtid_b32 v37 offset:25856\n"
MF "v[128:131],a[16:19],v[176:179],v[252:255]\n"
"s_nop 0\n"
"ds_write_addtid_b32 v38 offset:26112\n"
"ds_write_addtid_b32 v39 offset:26368\n"
MF "v[128:131],a[56:59],v[180:183],v[128:131]\n"
"s_nop 0\n"
"ds_write_addtid_b32 v40 offset:26624\n"
"ds_write_addtid_b32 v41 offset:26880\n"
MF "v[128:131],a[96:99],v[184:187],v[128:131]\n"
"s_waitcnt lgkmcnt(10)\n"
LD "v198,s[48:51],s80 offen offset:2048 lds\n"
MF "v[132:135],a[96:99],a[244:247],v[252:255]\n"
"s_nop 0\n"
TR "v[176:177],v2 offset:24576\n"
"ds_write_addtid_b32 v42 offset:27136\n"
MF "v[132:135],a[56:59],a[240:243],v[132:135]\n"
"s_nop 0\n"
TR "v[178:179],v2 offset:26624\n"
"ds_write_addtid_b32 v43 offset:27392\n"
MF "v[132:135],a[16:19],v[188:191],v[132:135]\n"
"s_nop 0\n"
"ds_write_addtid_b32 v44 offset:27648\n"
"ds_write_addtid_b32 v45 offset:27904\n"
MF "v[124:127],a[12:15],v[188:191],v[252:255]\n"
"s_nop 0\n"
TR "v[180:181],v2 offset:28672\n"
"ds_write_addtid_b32 v46 offset:28160\n"
MF "v[124:127],a[52:55],a[240:243],v[124:127]\n"
"s_nop 0\n"
TR "v[182:183],v2 offset:30720\n"
"ds_write_addtid_b32 v47 offset:28416\n"
MF "v[124:127],a[92:95],a[244:247],v[124:127]\n"
LD "v199,s[48:51],s80 offen offset:3072 lds\n"
MF "v[116:119],a[88:91],a[244:247],v[252:255]\n"
"s_nop 0\n"
TR "v[184:185],v2 offset:32768\n"
"ds_write_addtid_b32 v48 offset:28672\n"
MF "v[116:119],a[48:51],a[240:243],v[116:119]\n"
"s_add_u32 m0,s79,106496\n"
TR "v[186:187],v2 offset:34816\n"
"ds_write_addtid_b32 v49 offset:24832\n"
MF "v[116:119],a[8:11],v[188:191],v[116:119]\n"
LD "v200,s[48:51],s80 offen lds\n"
MF "v[108:111],a[4:7],v[188:191],v[252:255]\n"
"s_nop 0\n"
"ds_write_addtid_b32 v50 offset:25088\n"
"ds_write_addtid_b32 v51 offset:25344\n"
MF "v[108:111],a[44:47],a[240:243],v[108:111]\n"
"s_nop 0\n"
"ds_write_addtid_b32 v52 offset:25600\n"
"ds_write_addtid_b32 v53 offset:25856\n"
MF "v[108:111],a[84:87],a[244:247],v[108:111]\n"
"s_nop 0\n"
"ds_write_addtid_b32 v54 offset:26112\n"
"ds_write_addtid_b32 v55 offset:26368\n"
MF "v[100:103],a[80:83],a[244:247],v[252:255]\n"
MF "v[100:103],a[40:43],a[240:243],v[100:103]\n"
LD "v201,s[48:51],s80 offen offset:1024 lds\n"
MF "v[100:103],a[0:3],v[188:191],v[100:103]\n"
"s_waitcnt lgkmcnt(7)\n"
MF "v[136:139],a[0:3],v[176:179],v[252:255]\n"
TR "v[188:189],v3 offset:24576\n"
MF "v[136:139],a[40:43],v[180:183],v[136:139]\n"
TR "v[190:191],v3 offset:26624\n"
MF "v[136:139],a[80:83],v[184:187],v[136:139]\n"
LD "v202,s[48:51],s80 offen offset:2048 lds\n"
MF "v[144:147],a[84:87],v[184:187],v[252:255]\n"
TR "a[240:241],v3 offset:28672\n"
MF "v[144:147],a[44:47],v[180:183],v[144:147]\n"
TR "a[242:243],v3 offset:30720\n"
MF "v[144:147],a[4:7],v[176:179],v[144:147]\n"
LD "v203,s[48:51],s80 offen offset:3072 lds\n"
MF "v[152:155],a[8:11],v[176:179],v[252:255]\n"
TR "a[244:245],v3 offset:32768\n"
MF "v[152:155],a[48:51],v[180:183],v[152:155]\n"
TR "a[246:247],v3 offset:34816\n"
MF "v[152:155],a[88:91],v[184:187],v[152:155]\n"
MF "v[160:163],a[92:95],v[184:187],v[252:255]\n"
MF "v[160:163],a[52:55],v[180:183],v[160:163]\n"
MF "v[160:163],a[12:15],v[176:179],v[160:163]\n"
MF "v[168:171],a[16:19],v[176:179],v[252:255]\n"
MF "v[168:171],a[56:59],v[180:183],v[168:171]\n"
MF "v[168:171],a[96:99],v[184:187],v[168:171]\n"
"s_waitcnt vmcnt(24) lgkmcnt(0)\n"
"s_barrier\n"
MF "v[172:175],a[96:99],a[244:247],v[252:255]\n"
"s_add_u32 s89,s89,s65\n"
TR "v[176:177],v0\n"
MF "v[172:175],a[56:59],a[240:243],v[172:175]\n"
"s_add_u32 m0,s79,24576\n"
TR "v[178:179],v0 offset:2048\n"
MF "v[172:175],a[16:19],v[188:191],v[172:175]\n"
LD "v192,s[48:51],s89 offen lds\n"
MF "v[164:167],a[12:15],v[188:191],v[252:255]\n"
TR "v[180:181],v0 offset:4096\n"
MF "v[164:167],a[52:55],a[240:243],v[164:167]\n"
TR "v[182:183],v0 offset:6144\n"
MF "v[164:167],a[92:95],a[244:247],v[164:167]\n"
LD "v193,s[48:51],s89 offen offset:1024 lds\n"
MF "v[156:159],a[88:91],a[244:247],v[252:255]\n"
TR "v[184:185],v0 offset:8192\n"
MF "v[156:159],a[48:51],a[240:243],v[156:159]\n"
"s_nop 0\n"
TR "v[186:187],v0 offset:10240\n"
"ds_write_addtid_b32 v56 offset:49152\n"
MF "v[156:159],a[8:11],v[188:191],v[156:159]\n"
"s_nop 0\n"
"ds_write_addtid_b32 v57 offset:49408\n"
"ds_write_addtid_b32 v58 offset:49664\n"
MF "v[148:151],a[4:7],v[188:191],v[252:255]\n"
LD "v194,s[48:51],s89 offen offset:2048 lds\n"
MF "v[148:151],a[44:47],a[240:243],v[148:151]\n"
"buffer_load_dword v10,v12,s[40:43],s86 offen sc1\n"
MF "v[148:151],a[84:87],a[244:247],v[148:151]\n"
"s_nop 0\n"
"ds_write_addtid_b32 v59 offset:49920\n"
"ds_write_addtid_b32 v60 offset:50176\n"
MF "v[140:143],a[80:83],a[244:247],v[252:255]\n"
"s_nop 0\n"
"ds_write_addtid_b32 v61 offset:50432\n"
"ds_write_addtid_b32 v62 offset:50688\n"
MF "v[140:143],a[40:43],a[240:243],v[140:143]\n"
"s_nop 0\n"
"ds_write_addtid_b32 v63 offset:50944\n"
"ds_write_addtid_b32 v64 offset:51200\n"
MF "v[140:143],a[0:3],v[188:191],v[140:143]\n"
"s_waitcnt lgkmcnt(9)\n"
LD "v195,s[48:51],s89 offen offset:3072 lds\n"
MF "v[96:99],a[120:123],v[176:179],v[96:99]\n"
"s_nop 0\n"
TR "v[188:189],v1\n"
"ds_write_addtid_b32 v65 offset:51456\n"
MF "v[96:99],a[160:163],v[180:183],v[96:99]\n"
"s_nop 0\n"
TR "v[190:191],v1 offset:2048\n"
"ds_write_addtid_b32 v66 offset:51712\n"
MF "v[96:99],a[200:203],v[184:187],v[96:99]\n"
"s_nop 0\n"
"ds_write_addtid_b32 v67 offset:51968\n"
"ds_write_addtid_b32 v68 offset:52224\n"
MF "v[104:107],a[204:207],v[184:187],v[104:107]\n"
"s_nop 0\n"
TR "a[240:241],v1 offset:4096\n"
"ds_write_addtid_b32 v69 offset:52480\n"
MF "v[104:107],a[164:167],v[180:183],v[104:107]\n"
"s_add_u32 m0,s79,28672\n"
TR "a[242:243],v1 offset:6144\n"
"ds_write_addtid_b32 v70 offset:48640\n"
MF "v[104:107],a[124:127],v[176:179],v[104:107]\n"
LD "v196,s[48:51],s89 offen lds\n"
MF "v[112:115],a[128:131],v[176:179],v[112:115]\n"
"s_nop 0\n"
TR "a[244:245],v1 offset:8192\n"
"ds_write_addtid_b32 v71 offset:48896\n"
MF "v[112:115],a[168:171],v[180:183],v[112:115]\n"
"s_nop 0\n"
TR "a[246:247],v1 offset:10240\n"
"ds_write_addtid_b32 v72 offset:49152\n"
MF "v[112:115],a[208:211],v[184:187],v[112:115]\n"
LD "v197,s[48:51],s89 offen offset:1024 lds\n"
MF "v[120:123],a[212:215],v[184:187],v[120:123]\n"
"s_nop 0\n"
"ds_write_addtid_b32 v73 offset:49408\n"
"ds_write_addtid_b32 v74 offset:49664\n"
MF "v[120:123],a[172:175],v[180:183],v[120:123]\n"
"s_nop 0\n"
"ds_write_addtid_b32 v75 offset:49920\n"
"ds_write_addtid_b32 v76 offset:50176\n"
MF "v[120:123],a[132:135],v[176:179],v[120:123]\n"
"s_nop 0\n"
"ds_write_addtid_b32 v77 offset:50432\n"
"ds_write_addtid_b32 v78 offset:50688\n"
MF "v[128:131],a[136:139],v[176:179],v[128:131]\n"
"s_nop 0\n"
"ds_write_addtid_b32 v79 offset:50944\n"
"ds_write_addtid_b32 v80 offset:51200\n"
MF "v[128:131],a[176:179],v[180:183],v[128:131]\n"
LD "v198,s[48:51],s89 offen offset:2048 lds\n"
MF "v[128:131],a[216:219],v[184:187],v[128:131]\n"
"s_waitcnt lgkmcnt(9)\n"
"ds_write_addtid_b32 v81 offset:51456\n"
"ds_write_addtid_b32 v82 offset:51712\n"
MF "v[132:135],a[216:219],a[244:247],v[132:135]\n"
"s_nop 0\n"
TR "v[176:177],v2\n"
"ds_write_addtid_b32 v83 offset:51968\n"
MF "v[132:135],a[176:179],a[240:243],v[132:135]\n"
"s_nop 0\n"
TR "v[178:179],v2 offset:2048\n"
"ds_write_addtid_b32 v84 offset:52224\n"
MF "v[132:135],a[136:139],v[188:191],v[132:135]\n"
LD "v199,s[48:51],s89 offen offset:3072 lds\n"
MF "v[124:127],a[132:135],v[188:191],v[124:127]\n"
"s_nop 0\n"
TR "v[180:181],v2 offset:4096\n"
"ds_write_addtid_b32 v85 offset:52480\n"
MF "v[124:127],a[172:175],a[240:243],v[124:127]\n"
"s_nop 0\n"
TR "v[182:183],v2 offset:6144\n"
"ds_write_addtid_b32 v86 offset:52736\n"
MF "v[124:127],a[212:215],a[244:247],v[124:127]\n"
"s_nop 0\n"
"ds_write_addtid_b32 v87 offset:52992\n"
"ds_write_addtid_b32 v88 offset:53248\n"
MF "v[116:119],a[208:211],a[244:247],v[116:119]\n"
"s_nop 0\n"
TR "v[184:185],v2 offset:8192\n"
"ds_write_addtid_b32 v89 offset:53504\n"
MF "v[116:119],a[168:171],a[240:243],v[116:119]\n"
"s_add_u32 m0,s79,32768\n"
TR "v[186:187],v2 offset:10240\n"
"ds_write_addtid_b32 v90 offset:49664\n"
MF "v[116:119],a[128:131],v[188:191],v[116:119]\n"
LD "v200,s[48:51],s89 offen lds\n"
MF "v[108:111],a[124:127],v[188:191],v[108:111]\n"
"s_nop 0\n"
"ds_write_addtid_b32 v91 offset:49920\n"
"ds_write_addtid_b32 v92 offset:50176\n"
MF "v[108:111],a[164:167],a[240:243],v[108:111]\n"
"s_nop 0\n"
"ds_write_addtid_b32 v93 offset:50432\n"
"ds_write_addtid_b32 v94 offset:50688\n"
MF "v[108:111],a[204:207],a[244:247],v[108:111]\n"
"ds_write_addtid_b32 v95 offset:50944\n"
MF "v[100:103],a[200:203],a[244:247],v[100:103]\n"
LD "v201,s[48:51],s89 offen offset:1024 lds\n"
MF "v[100:103],a[160:163],a[240:243],v[100:103]\n"
MF "v[100:103],a[120:123],v[188:191],v[100:103]\n"
"s_waitcnt lgkmcnt(6)\n"
MF "v[136:139],a[120:123],v[176:179],v[136:139]\n"
TR "v[188:189],v3\n"
MF "v[136:139],a[160:163],v[180:183],v[136:139]\n"
TR "v[190:191],v3 offset:2048\n"
MF "v[136:139],a[200:203],v[184:187],v[136:139]\n"
LD "v202,s[48:51],s89 offen offset:2048 lds\n"
MF "v[144:147],a[204:207],v[184:187],v[144:147]\n"
TR "a[240:241],v3 offset:4096\n"
MF "v[144:147],a[164:167],v[180:183],v[144:147]\n"
TR "a[242:243],v3 offset:6144\n"
MF "v[144:147],a[124:127],v[176:179],v[144:147]\n"
LD "v203,s[48:51],s89 offen offset:3072 lds\n"
MF "v[152:155],a[128:131],v[176:179],v[152:155]\n"
TR "a[244:245],v3 offset:8192\n"
MF "v[152:155],a[168:171],v[180:183],v[152:155]\n"
TR "a[246:247],v3 offset:10240\n"
MF "v[152:155],a[208:211],v[184:187],v[152:155]\n"
MF "v[160:163],a[212:215],v[184:187],v[160:163]\n"
MF "v[160:163],a[172:175],v[180:183],v[160:163]\n"
MF "v[160:163],a[132:135],v[176:179],v[160:163]\n"
MF "v[168:171],a[136:139],v[176:179],v[168:171]\n"
MF "v[168:171],a[176:179],v[180:183],v[168:171]\n"
MF "v[168:171],a[216:219],v[184:187],v[168:171]\n"
"s_waitcnt vmcnt(25) lgkmcnt(0)\n"
"s_barrier\n"
MF "v[172:175],a[216:219],a[244:247],v[172:175]\n"
"s_add_u32 s80,s89,122880\n"
TR "v[176:177],v0 offset:49152\n"
MF "v[172:175],a[176:179],a[240:243],v[172:175]\n"
"s_add_u32 m0,s79,0\n"
"s_mul_i32 s95,s92,3277\n"
TR "v[178:179],v0 offset:51200\n"
MF "v[172:175],a[136:139],v[188:191],v[172:175]\n"
LD "v192,s[48:51],s80 offen lds\n"
MF "v[164:167],a[132:135],v[188:191],v[164:167]\n"
"s_lshr_b32 s95,s95,16\n"
"s_mul_i32 s59,s95,20\n"
TR "v[180:181],v0 offset:53248\n"
MF "v[164:167],a[172:175],a[240:243],v[164:167]\n"
"s_sub_u32 s59,s92,s59\n"
TR "v[182:183],v0 offset:55296\n"
MF "v[164:167],a[212:215],a[244:247],v[164:167]\n"
LD "v193,s[48:51],s80 offen offset:1024 lds\n"
MF "v[156:159],a[208:211],a[244:247],v[156:159]\n"
"s_cmp_ge_u32 s59,10\n"
"s_cselect_b32 s96,4294557760,0\n"
TR "v[184:185],v0 offset:57344\n"
MF "v[156:159],a[168:171],a[240:243],v[156:159]\n"
"s_add_u32 s96,s96,1024\n"
"s_mul_i32 s59,s59,40960\n"
TR "v[186:187],v0 offset:59392\n"
MF "v[156:159],a[128:131],v[188:191],v[156:159]\n"
"s_add_u32 s96,s96,s59\n"
"s_lshl_b32 s59,s95,7\n"
"s_add_u32 s96,s96,s59\n"
MF "v[148:151],a[124:127],v[188:191],v[148:151]\n"
"s_add_u32 s95,s95,3\n"
"s_cmp_ge_u32 s92,40\n"
"s_cselect_b32 s95,99,s95\n"
MF "v[148:151],a[164:167],a[240:243],v[148:151]\n"
LD "v194,s[48:51],s80 offen offset:2048 lds\n"
MF "v[148:151],a[204:207],a[244:247],v[148:151]\n"
"s_cmp_ge_u32 s85,5\n"
"s_cselect_b32 s95,99,s95\n"
"s_cselect_b32 s72,s85,0\n"
MF "v[140:143],a[200:203],a[244:247],v[140:143]\n"
MF "v[140:143],a[160:163],a[240:243],v[140:143]\n"
MF "v[140:143],a[120:123],v[188:191],v[140:143]\n"
"s_waitcnt lgkmcnt(0)\n"
LD "v195,s[48:51],s80 offen offset:3072 lds\n"
MF "v[96:99],a[20:23],v[176:179],v[96:99]\n"
TR "v[188:189],v1 offset:49152\n"
MF "v[96:99],a[60:63],v[180:183],v[96:99]\n"
TR "v[190:191],v1 offset:51200\n"
MF "v[96:99],a[100:103],v[184:187],v[96:99]\n"
MF "v[104:107],a[104:107],v[184:187],v[104:107]\n"
TR "a[240:241],v1 offset:53248\n"
MF "v[104:107],a[64:67],v[180:183],v[104:107]\n"
"s_add_u32 m0,s79,4096\n"
TR "a[242:243],v1 offset:55296\n"
MF "v[104:107],a[24:27],v[176:179],v[104:107]\n"
LD "v196,s[48:51],s80 offen lds\n"
MF "v[112:115],a[28:31],v[176:179],v[112:115]\n"
TR "a[244:245],v1 offset:57344\n"
MF "v[112:115],a[68:71],v[180:183],v[112:115]\n"
TR "a[246:247],v1 offset:59392\n"
MF "v[112:115],a[108:111],v[184:187],v[112:115]\n"
LD "v197,s[48:51],s80 offen offset:1024 lds\n"
MF "v[120:123],a[112:115],v[184:187],v[120:123]\n"
MF "v[120:123],a[72:75],v[180:183],v[120:123]\n"
MF "v[120:123],a[32:35],v[176:179],v[120:123]\n"
MF "v[128:131],a[36:39],v[176:179],v[128:131]\n"
MF "v[128:131],a[76:79],v[180:183],v[128:131]\n"
LD "v198,s[48:51],s80 offen offset:2048 lds\n"
MF "v[128:131],a[116:119],v[184:187],v[128:131]\n"
"s_waitcnt lgkmcnt(0)\n"
MF "v[132:135],a[116:119],a[244:247],v[132:135]\n"
TR "v[176:177],v2 offset:49152\n"
MF "v[132:135],a[76:79],a[240:243],v[132:135]\n"
TR "v[178:179],v2 offset:51200\n"
MF "v[132:135],a[36:39],v[188:191],v[132:135]\n"
LD "v199,s[48:51],s80 offen offset:3072 lds\n"
MF "v[124:127],a[32:35],v[188:191],v[124:127]\n"
TR "v[180:181],v2 offset:53248\n"
MF "v[124:127],a[72:75],a[240:243],v[124:127]\n"
TR "v[182:183],v2 offset:55296\n"
MF "v[124:127],a[112:115],a[244:247],v[124:127]\n"
MF "v[116:119],a[108:111],a[244:247],v[116:119]\n"
TR "v[184:185],v2 offset:57344\n"
MF "v[116:119],a[68:71],a[240:243],v[116:119]\n"
"s_add_u32 m0,s79,8192\n"
TR "v[186:187],v2 offset:59392\n"
MF "v[116:119],a[28:31],v[188:191],v[116:119]\n"
LD "v200,s[48:51],s80 offen lds\n"
MF "v[108:111],a[24:27],v[188:191],v[108:111]\n"
MF "v[108:111],a[64:67],a[240:243],v[108:111]\n"
MF "v[108:111],a[104:107],a[244:247],v[108:111]\n"
MF "v[100:103],a[100:103],a[244:247],v[100:103]\n"
LD "v201,s[48:51],s80 offen offset:1024 lds\n"
MF "v[100:103],a[60:63],a[240:243],v[100:103]\n"
"s_waitcnt vmcnt(19)\n"
"s_cmp_eq_u32 s83,0xf0000000\n"
MF "v[100:103],a[20:23],v[188:191],v[100:103]\n"
"s_cbranch_scc1 .Ljs5\n"
"s_cmp_eq_u32 s83,0xf0000010\n"
"s_cbranch_scc1 .Ljc5\n"
MF "v[248:251],v[210:213],a[250:253],0\n"
"v_mov_b32 v240,v208\n"
"v_mov_b32 v241,v209\n"
MF "v[244:247],v[214:217],a[250:253],0\n"
MF "v[248:251],v[218:221],a[250:253],v[248:251]\n"
MF "v[244:247],v[222:225],a[250:253],v[244:247]\n"
MF "v[248:251],v[226:229],a[250:253],v[248:251]\n"
MF "v[244:247],v[230:233],a[250:253],v[244:247]\n"
MF "v[248:251],v[234:237],a[250:253],v[248:251]\n"
MF "v[244:247],v[238:241],a[250:253],v[244:247]\n"
"s_branch .Ljs5\n"
".Ljc5:\n"
MF "v[96:99],a[252:255],v[208:211],v[96:99]\n"
MF "v[104:107],a[252:255],v[212:215],v[104:107]\n"
MF "v[112:115],a[252:255],v[216:219],v[112:115]\n"
MF "v[120:123],a[252:255],v[220:223],v[120:123]\n"
MF "v[128:131],a[252:255],v[224:227],v[128:131]\n"
MF "v[100:103],a[248:251],v[208:211],v[100:103]\n"
MF "v[108:111],a[248:251],v[212:215],v[108:111]\n"
MF "v[116:119],a[248:251],v[216:219],v[116:119]\n"
MF "v[124:127],a[248:251],v[220:223],v[124:127]\n"
MF "v[132:135],a[248:251],v[224:227],v[132:135]\n"
".Ljs5:\n"
"s_waitcnt lgkmcnt(0)\n"
MF "v[136:139],a[20:23],v[176:179],v[136:139]\n"
TR "v[188:189],v3 offset:49152\n"
"v_sub_u32 v10,v10,v11\n"
MF "v[136:139],a[60:63],v[180:183],v[136:139]\n"
"s_lshl_b32 s82,s92,10\n"
TR "v[190:191],v3 offset:51200\n"
"v_cmp_gt_u32 vcc,s95,v10\n"
MF "v[136:139],a[100:103],v[184:187],v[136:139]\n"
LD "v202,s[48:51],s80 offen offset:2048 lds\n"
MF "v[144:147],a[104:107],v[184:187],v[144:147]\n"
"s_add_u32 s82,s82,s93\n"
TR "a[240:241],v3 offset:53248\n"
CV "v244,v244,v245\n"
MF "v[144:147],a[64:67],v[180:183],v[144:147]\n"
TR "a[242:243],v3 offset:55296\n"
CV "v245,v246,v247\n"
MF "v[144:147],a[24:27],v[176:179],v[144:147]\n"
LD "v203,s[48:51],s80 offen offset:3072 lds\n"
MF "v[152:155],a[28:31],v[176:179],v[152:155]\n"
TR "a[244:245],v3 offset:57344\n"
CV "v246,v248,v249\n"
MF "v[152:155],a[68:71],v[180:183],v[152:155]\n"
TR "a[246:247],v3 offset:59392\n"
CV "v247,v250,v251\n"
MF "v[152:155],a[108:111],v[184:187],v[152:155]\n"
"s_cmp_eq_u64 vcc,0\n"
"s_cselect_b32 s82,s82,0xf0000000\n"
"s_cselect_b32 s84,s96,0xf0000000\n"
"s_cselect_b32 s59,32,0\n"
MF "v[160:163],a[112:115],v[184:187],v[160:163]\n"
"s_add_u32 s92,s92,s59\n"
".Lnf0:\n"
"v_readlane_b32 s58,v10,s67\n"
"s_cmp_lt_u32 s58,s72\n"
"s_cbranch_scc1 .Lsp0\n"
".Lnb0:\n"
MF "v[160:163],a[72:75],v[180:183],v[160:163]\n"
"s_cmp_ge_u32 s85,5\n"
"s_cselect_b32 s82,s70,s82\n"
"s_cselect_b32 s84,0xf0000010,s84\n"
MF "v[160:163],a[32:35],v[176:179],v[160:163]\n"
"s_cmp_ge_u32 s85,5\n"
"s_cselect_b32 s71,1024,s71\n"
"s_cselect_b32 s66,0x80000000,s66\n"
MF "v[168:171],a[36:39],v[176:179],v[168:171]\n"
"s_add_u32 s70,s70,20480\n"
"v_permlane16_swap_b32 v244,v246\n"
"v_permlane16_swap_b32 v245,v247\n"
MF "v[168:171],a[76:79],v[180:183],v[168:171]\n"
"buffer_store_dwordx4 v[244:247],v8,s[44:47],s83 offen sc1\n"
MF "v[168:171],a[116:119],v[184:187],v[168:171]\n"
"s_waitcnt vmcnt(26) lgkmcnt(0)\n"
"s_barrier\n"
MF "v[172:175],a[116:119],a[244:247],v[172:175]\n"
"s_add_u32 s88,s88,s65\n"
"s_mov_b32 s83,s84\n"
TR "v[176:177],v4\n"
MF "v[172:175],a[76:79],a[240:243],v[172:175]\n"
"s_add_u32 m0,s79,49152\n"
TR "v[178:179],v4 offset:2048\n"
MF "v[172:175],a[36:39],v[188:191],v[172:175]\n"
LD "v192,s[48:51],s88 offen lds\n"
MF "v[164:167],a[32:35],v[188:191],v[164:167]\n"
TR "v[180:181],v4 offset:4096\n"
MF "v[164:167],a[72:75],a[240:243],v[164:167]\n"
TR "v[182:183],v4 offset:6144\n"
MF "v[164:167],a[112:115],a[244:247],v[164:167]\n"
LD "v193,s[48:51],s88 offen offset:1024 lds\n"
MF "v[156:159],a[108:111],a[244:247],v[156:159]\n"
TR "v[184:185],v4 offset:8192\n"
MF "v[156:159],a[68:71],a[240:243],v[156:159]\n"
TR "v[186:187],v4 offset:10240\n"
MF "v[156:159],a[28:31],v[188:191],v[156:159]\n"
LD "v[208:211],v9,s[40:43],s82 offen sc1\n"
MF "v[148:151],a[24:27],v[188:191],v[148:151]\n"
MF "v[148:151],a[64:67],a[240:243],v[148:151]\n"
LD "v194,s[48:51],s88 offen offset:2048 lds\n"
MF "v[148:151],a[104:107],a[244:247],v[148:151]\n"
MF "v[140:143],a[100:103],a[244:247],v[140:143]\n"
MF "v[140:143],a[60:63],a[240:243],v[140:143]\n"
MF "v[140:143],a[20:23],v[188:191],v[140:143]\n"
"s_waitcnt lgkmcnt(0)\n"
LD "v195,s[48:51],s88 offen offset:3072 lds\n"
MF "v[96:99],a[140:143],v[176:179],v[96:99]\n"
TR "v[188:189],v5\n"
MF "v[96:99],a[180:183],v[180:183],v[96:99]\n"
"s_add_u32 s82,s82,s71\n"
TR "v[190:191],v5 offset:2048\n"
MF "v[96:99],a[220:223],v[184:187],v[96:99]\n"
LD "v[212:215],v9,s[40:43],s82 offen sc1\n"
MF "v[104:107],a[224:227],v[184:187],v[104:107]\n"
TR "a[240:241],v5 offset:4096\n"
MF "v[104:107],a[184:187],v[180:183],v[104:107]\n"
"s_add_u32 m0,s79,53248\n"
TR "a[242:243],v5 offset:6144\n"
MF "v[104:107],a[144:147],v[176:179],v[104:107]\n"
LD "v196,s[48:51],s88 offen lds\n"
MF "v[112:115],a[148:151],v[176:179],v[112:115]\n"
TR "a[244:245],v5 offset:8192\n"
MF "v[112:115],a[188:191],v[180:183],v[112:115]\n"
TR "a[246:247],v5 offset:10240\n"
MF "v[112:115],a[228:231],v[184:187],v[112:115]\n"
LD "v197,s[48:51],s88 offen offset:1024 lds\n"
MF "v[120:123],a[232:235],v[184:187],v[120:123]\n"
"s_add_u32 s82,s82,s71\n"
MF "v[120:123],a[192:195],v[180:183],v[120:123]\n"
LD "v[216:219],v9,s[40:43],s82 offen sc1\n"
MF "v[120:123],a[152:155],v[176:179],v[120:123]\n"
MF "v[128:131],a[156:159],v[176:179],v[128:131]\n"
MF "v[128:131],a[196:199],v[180:183],v[128:131]\n"
LD "v198,s[48:51],s88 offen offset:2048 lds\n"
MF "v[128:131],a[236:239],v[184:187],v[128:131]\n"
"s_waitcnt lgkmcnt(0)\n"
MF "v[132:135],a[236:239],a[244:247],v[132:135]\n"
TR "v[176:177],v6\n"
MF "v[132:135],a[196:199],a[240:243],v[132:135]\n"
TR "v[178:179],v6 offset:2048\n"
MF "v[132:135],a[156:159],v[188:191],v[132:135]\n"
LD "v199,s[48:51],s88 offen offset:3072 lds\n"
MF "v[124:127],a[152:155],v[188:191],v[124:127]\n"
TR "v[180:181],v6 offset:4096\n"
MF "v[124:127],a[192:195],a[240:243],v[124:127]\n"
"s_add_u32 s82,s82,s71\n"
TR "v[182:183],v6 offset:6144\n"
MF "v[124:127],a[232:235],a[244:247],v[124:127]\n"
LD "v[220:223],v9,s[40:43],s82 offen sc1\n"
MF "v[116:119],a[228:231],a[244:247],v[116:119]\n"
TR "v[184:185],v6 offset:8192\n"
MF "v[116:119],a[188:191],a[240:243],v[116:119]\n"
"s_add_u32 m0,s79,57344\n"
TR "v[186:187],v6 offset:10240\n"
MF "v[116:119],a[148:151],v[188:191],v[116:119]\n"
LD "v200,s[48:51],s88 offen lds\n"
MF "v[108:111],a[144:147],v[188:191],v[108:111]\n"
MF "v[108:111],a[184:187],a[240:243],v[108:111]\n"
"s_add_u32 s82,s82,s71\n"
MF "v[108:111],a[224:227],a[244:247],v[108:111]\n"
LD "v[224:227],v9,s[40:43],s82 offen sc1\n"
MF "v[100:103],a[220:223],a[244:247],v[100:103]\n"
LD "v201,s[48:51],s88 offen offset:1024 lds\n"
MF "v[100:103],a[180:183],a[240:243],v[100:103]\n"
MF "v[100:103],a[140:143],v[188:191],v[100:103]\n"
"s_waitcnt lgkmcnt(0)\n"
MF "v[136:139],a[140:143],v[176:179],v[136:139]\n"
TR "v[188:189],v7\n"
MF "v[136:139],a[180:183],v[180:183],v[136:139]\n"
TR "v[190:191],v7 offset:2048\n"
MF "v[136:139],a[220:223],v[184:187],v[136:139]\n"
LD "v202,s[48:51],s88 offen offset:2048 lds\n"
MF "v[144:147],a[224:227],v[184:187],v[144:147]\n"
TR "a[240:241],v7 offset:4096\n"
MF "v[144:147],a[184:187],v[180:183],v[144:147]\n"
TR "a[242:243],v7 offset:6144\n"
MF "v[144:147],a[144:147],v[176:179],v[144:147]\n"
LD "v203,s[48:51],s88 offen offset:3072 lds\n"
MF "v[152:155],a[148:151],v[176:179],v[152:155]\n"
TR "a[244:245],v7 offset:8192\n"
MF "v[152:155],a[188:191],v[180:183],v[152:155]\n"
"s_add_u32 s82,s82,s66\n"
TR "a[246:247],v7 offset:10240\n"
MF "v[152:155],a[228:231],v[184:187],v[152:155]\n"
LD "v[228:231],v9,s[40:43],s82 offen sc1\n"
MF "v[160:163],a[232:235],v[184:187],v[160:163]\n"
MF "v[160:163],a[192:195],v[180:183],v[160:163]\n"
MF "v[160:163],a[152:155],v[176:179],v[160:163]\n"
MF "v[168:171],a[156:159],v[176:179],v[168:171]\n"
"s_add_u32 s82,s82,s71\n"
MF "v[168:171],a[196:199],v[180:183],v[168:171]\n"
LD "v[232:235],v9,s[40:43],s82 offen sc1\n"
MF "v[168:171],a[236:239],v[184:187],v[168:171]\n"
"s_waitcnt vmcnt(32) lgkmcnt(0)\n"
"s_barrier\n"
MF "v[172:175],a[236:239],a[244:247],v[172:175]\n"
"s_add_u32 s80,s88,122880\n"
TR "v[176:177],v0 offset:24576\n"
MF "v[172:175],a[196:199],a[240:243],v[172:175]\n"
"s_add_u32 m0,s79,98304\n"
"s_add_u32 s85,s85,1\n"
TR "v[178:179],v0 offset:26624\n"
MF "v[172:175],a[156:159],v[188:191],v[172:175]\n"
LD "v192,s[48:51],s80 offen lds\n"
MF "v[164:167],a[152:155],v[188:191],v[164:167]\n"
"s_nop 0\n"
TR "v[180:181],v0 offset:28672\n"
"ds_write_addtid_b32 v96 offset:24576\n"
MF "v[164:167],a[192:195],a[240:243],v[164:167]\n"
"s_nop 0\n"
TR "v[182:183],v0 offset:30720\n"
"ds_write_addtid_b32 v97 offset:24832\n"
MF "v[164:167],a[232:235],a[244:247],v[164:167]\n"
LD "v193,s[48:51],s80 offen offset:1024 lds\n"
MF "v[156:159],a[228:231],a[244:247],v[156:159]\n"
"s_nop 0\n"
TR "v[184:185],v0 offset:32768\n"
"ds_write_addtid_b32 v98 offset:25088\n"
MF "v[156:159],a[188:191],a[240:243],v[156:159]\n"
"s_add_u32 s82,s82,s71\n"
TR "v[186:187],v0 offset:34816\n"
"ds_write_addtid_b32 v99 offset:25344\n"
MF "v[156:159],a[148:151],v[188:191],v[156:159]\n"
LD "v[236:239],v9,s[40:43],s82 offen sc1\n"
MF "v[148:151],a[144:147],v[188:191],v[148:151]\n"
LD "v194,s[48:51],s80 offen offset:2048 lds\n"
MF "v[148:151],a[184:187],a[240:243],v[148:151]\n"
"s_nop 0\n"
"ds_write_addtid_b32 v100 offset:25600\n"
"ds_write_addtid_b32 v101 offset:25856\n"
MF "v[148:151],a[224:227],a[244:247],v[148:151]\n"
"s_nop 0\n"
"ds_write_addtid_b32 v102 offset:26112\n"
"ds_write_addtid_b32 v103 offset:26368\n"
MF "v[140:143],a[220:223],a[244:247],v[140:143]\n"
"s_nop 0\n"
"ds_write_addtid_b32 v104 offset:26624\n"
"ds_write_addtid_b32 v105 offset:26880\n"
MF "v[140:143],a[180:183],a[240:243],v[140:143]\n"
"s_nop 0\n"
"ds_write_addtid_b32 v106 offset:27136\n"
"ds_write_addtid_b32 v107 offset:27392\n"
MF "v[140:143],a[140:143],v[188:191],v[140:143]\n"
"s_waitcnt lgkmcnt(9)\n"
MF "v[16:19],a[0:3],v[176:179],v[252:255]\n"
"s_nop 0\n"
TR "v[188:189],v1 offset:24576\n"
"ds_write_addtid_b32 v108 offset:27648\n"
MF "v[16:19],a[40:43],v[180:183],v[16:19]\n"
"s_nop 0\n"
TR "v[190:191],v1 offset:26624\n"
"ds_write_addtid_b32 v109 offset:27904\n"
MF "v[16:19],a[80:83],v[184:187],v[16:19]\n"
LD "v195,s[48:51],s80 offen offset:3072 lds\n"
MF "v[24:27],a[84:87],v[184:187],v[252:255]\n"
"s_add_u32 s60,s64,1\n"
TR "a[240:241],v1 offset:28672\n"
"ds_write_addtid_b32 v110 offset:28160\n"
MF "v[24:27],a[44:47],v[180:183],v[24:27]\n"
"s_add_u32 m0,s79,102400\n"
TR "a[242:243],v1 offset:30720\n"
"ds_write_addtid_b32 v111 offset:24320\n"
MF "v[24:27],a[4:7],v[176:179],v[24:27]\n"
LD "v196,s[48:51],s80 offen lds\n"
MF "v[32:35],a[8:11],v[176:179],v[252:255]\n"
"s_cmp_eq_u32 s60,8\n"
"s_cselect_b32 s60,0,s60\n"
TR "a[244:245],v1 offset:32768\n"
MF "v[32:35],a[48:51],v[180:183],v[32:35]\n"
"s_cmp_eq_u32 s60,10\n"
"s_cselect_b32 s60,s61,s60\n"
TR "a[246:247],v1 offset:34816\n"
MF "v[32:35],a[88:91],v[184:187],v[32:35]\n"
"s_sub_u32 s65,s60,s64\n"
"ds_write_addtid_b32 v112 offset:24576\n"
"ds_write_addtid_b32 v113 offset:24832\n"
MF "v[40:43],a[92:95],v[184:187],v[252:255]\n"
"s_lshl_b32 s65,s65,7\n"
"ds_write_addtid_b32 v114 offset:25088\n"
"ds_write_addtid_b32 v115 offset:25344\n"
MF "v[40:43],a[52:55],v[180:183],v[40:43]\n"
LD "v197,s[48:51],s80 offen offset:1024 lds\n"
MF "v[40:43],a[12:15],v[176:179],v[40:43]\n"
"s_mov_b32 s64,s60\n"
"ds_write_addtid_b32 v116 offset:25600\n"
"ds_write_addtid_b32 v117 offset:25856\n"
MF "v[48:51],a[16:19],v[176:179],v[252:255]\n"
"s_nop 0\n"
"ds_write_addtid_b32 v118 offset:26112\n"
"ds_write_addtid_b32 v119 offset:26368\n"
MF "v[48:51],a[56:59],v[180:183],v[48:51]\n"
"s_nop 0\n"
"ds_write_addtid_b32 v120 offset:26624\n"
"ds_write_addtid_b32 v121 offset:26880\n"
MF "v[48:51],a[96:99],v[184:187],v[48:51]\n"
"s_waitcnt lgkmcnt(10)\n"
LD "v198,s[48:51],s80 offen offset:2048 lds\n"
MF "v[52:55],a[96:99],a[244:247],v[252:255]\n"
"s_nop 0\n"
TR "v[176:177],v2 offset:24576\n"
"ds_write_addtid_b32 v122 offset:27136\n"
MF "v[52:55],a[56:59],a[240:243],v[52:55]\n"
"s_nop 0\n"
TR "v[178:179],v2 offset:26624\n"
"ds_write_addtid_b32 v123 offset:27392\n"
MF "v[52:55],a[16:19],v[188:191],v[52:55]\n"
"s_nop 0\n"
"ds_write_addtid_b32 v124 offset:27648\n"
"ds_write_addtid_b32 v125 offset:27904\n"
MF "v[44:47],a[12:15],v[188:191],v[252:255]\n"
"s_nop 0\n"
TR "v[180:181],v2 offset:28672\n"
"ds_write_addtid_b32 v126 offset:28160\n"
MF "v[44:47],a[52:55],a[240:243],v[44:47]\n"
"s_nop 0\n"
TR "v[182:183],v2 offset:30720\n"
"ds_write_addtid_b32 v127 offset:28416\n"
MF "v[44:47],a[92:95],a[244:247],v[44:47]\n"
LD "v199,s[48:51],s80 offen offset:3072 lds\n"
MF "v[36:39],a[88:91],a[244:247],v[252:255]\n"
"s_nop 0\n"
TR "v[184:185],v2 offset:32768\n"
"ds_write_addtid_b32 v128 offset:28672\n"
MF "v[36:39],a[48:51],a[240:243],v[36:39]\n"
"s_add_u32 m0,s79,106496\n"
TR "v[186:187],v2 offset:34816\n"
"ds_write_addtid_b32 v129 offset:24832\n"
MF "v[36:39],a[8:11],v[188:191],v[36:39]\n"
LD "v200,s[48:51],s80 offen lds\n"
MF "v[28:31],a[4:7],v[188:191],v[252:255]\n"
"s_nop 0\n"
"ds_write_addtid_b32 v130 offset:25088\n"
"ds_write_addtid_b32 v131 offset:25344\n"
MF "v[28:31],a[44:47],a[240:243],v[28:31]\n"
"s_nop 0\n"
"ds_write_addtid_b32 v132 offset:25600\n"
"ds_write_addtid_b32 v133 offset:25856\n"
MF "v[28:31],a[84:87],a[244:247],v[28:31]\n"
"s_nop 0\n"
"ds_write_addtid_b32 v134 offset:26112\n"
"ds_write_addtid_b32 v135 offset:26368\n"
MF "v[20:23],a[80:83],a[244:247],v[252:255]\n"
MF "v[20:23],a[40:43],a[240:243],v[20:23]\n"
LD "v201,s[48:51],s80 offen offset:1024 lds\n"
MF "v[20:23],a[0:3],v[188:191],v[20:23]\n"
"s_waitcnt lgkmcnt(7)\n"
MF "v[56:59],a[0:3],v[176:179],v[252:255]\n"
TR "v[188:189],v3 offset:24576\n"
MF "v[56:59],a[40:43],v[180:183],v[56:59]\n"
TR "v[190:191],v3 offset:26624\n"
MF "v[56:59],a[80:83],v[184:187],v[56:59]\n"
LD "v202,s[48:51],s80 offen offset:2048 lds\n"
MF "v[64:67],a[84:87],v[184:187],v[252:255]\n"
TR "a[240:241],v3 offset:28672\n"
MF "v[64:67],a[44:47],v[180:183],v[64:67]\n"
TR "a[242:243],v3 offset:30720\n"
MF "v[64:67],a[4:7],v[176:179],v[64:67]\n"
LD "v203,s[48:51],s80 offen offset:3072 lds\n"
MF "v[72:75],a[8:11],v[176:179],v[252:255]\n"
TR "a[244:245],v3 offset:32768\n"
MF "v[72:75],a[48:51],v[180:183],v[72:75]\n"
TR "a[246:247],v3 offset:34816\n"
MF "v[72:75],a[88:91],v[184:187],v[72:75]\n"
MF "v[80:83],a[92:95],v[184:187],v[252:255]\n"
MF "v[80:83],a[52:55],v[180:183],v[80:83]\n"
MF "v[80:83],a[12:15],v[176:179],v[80:83]\n"
MF "v[88:91],a[16:19],v[176:179],v[252:255]\n"
MF "v[88:91],a[56:59],v[180:183],v[88:91]\n"
MF "v[88:91],a[96:99],v[184:187],v[88:91]\n"
"s_waitcnt vmcnt(33) lgkmcnt(0)\n"
"s_barrier\n"
MF "v[92:95],a[96:99],a[244:247],v[252:255]\n"
"s_add_u32 s89,s89,s65\n"
TR "v[176:177],v0\n"
MF "v[92:95],a[56:59],a[240:243],v[92:95]\n"
"s_add_u32 m0,s79,24576\n"
TR "v[178:179],v0 offset:2048\n"
MF "v[92:95],a[16:19],v[188:191],v[92:95]\n"
LD "v192,s[48:51],s89 offen lds\n"
MF "v[84:87],a[12:15],v[188:191],v[252:255]\n"
TR "v[180:181],v0 offset:4096\n"
MF "v[84:87],a[52:55],a[240:243],v[84:87]\n"
TR "v[182:183],v0 offset:6144\n"
MF "v[84:87],a[92:95],a[244:247],v[84:87]\n"
LD "v193,s[48:51],s89 offen offset:1024 lds\n"
MF "v[76:79],a[88:91],a[244:247],v[252:255]\n"
TR "v[184:185],v0 offset:8192\n"
MF "v[76:79],a[48:51],a[240:243],v[76:79]\n"
"s_nop 0\n"
TR "v[186:187],v0 offset:10240\n"
"ds_write_addtid_b32 v136 offset:49152\n"
MF "v[76:79],a[8:11],v[188:191],v[76:79]\n"
"s_nop 0\n"
"ds_write_addtid_b32 v137 offset:49408\n"
"ds_write_addtid_b32 v138 offset:49664\n"
MF "v[68:71],a[4:7],v[188:191],v[252:255]\n"
LD "v194,s[48:51],s89 offen offset:2048 lds\n"
MF "v[68:71],a[44:47],a[240:243],v[68:71]\n"
"buffer_load_dword v10,v12,s[40:43],s86 offen sc1\n"
MF "v[68:71],a[84:87],a[244:247],v[68:71]\n"
"s_nop 0\n"
"ds_write_addtid_b32 v139 offset:49920\n"
"ds_write_addtid_b32 v140 offset:50176\n"
MF "v[60:63],a[80:83],a[244:247],v[252:255]\n"
"s_nop 0\n"
"ds_write_addtid_b32 v141 offset:50432\n"
"ds_write_addtid_b32 v142 offset:50688\n"
MF "v[60:63],a[40:43],a[240:243],v[60:63]\n"
"s_nop 0\n"
"ds_write_addtid_b32 v143 offset:50944\n"
"ds_write_addtid_b32 v144 offset:51200\n"
MF "v[60:63],a[0:3],v[188:191],v[60:63]\n"
"s_waitcnt lgkmcnt(9)\n"
LD "v195,s[48:51],s89 offen offset:3072 lds\n"
MF "v[16:19],a[120:123],v[176:179],v[16:19]\n"
"s_nop 0\n"
TR "v[188:189],v1\n"
"ds_write_addtid_b32 v145 offset:51456\n"
MF "v[16:19],a[160:163],v[180:183],v[16:19]\n"
"s_nop 0\n"
TR "v[190:191],v1 offset:2048\n"
"ds_write_addtid_b32 v146 offset:51712\n"
MF "v[16:19],a[200:203],v[184:187],v[16:19]\n"
"s_nop 0\n"
"ds_write_addtid_b32 v147 offset:51968\n"
"ds_write_addtid_b32 v148 offset:52224\n"
MF "v[24:27],a[204:207],v[184:187],v[24:27]\n"
"s_nop 0\n"
TR "a[240:241],v1 offset:4096\n"
"ds_write_addtid_b32 v149 offset:52480\n"
MF "v[24:27],a[164:167],v[180:183],v[24:27]\n"
"s_add_u32 m0,s79,28672\n"
TR "a[242:243],v1 offset:6144\n"
"ds_write_addtid_b32 v150 offset:48640\n"
MF "v[24:27],a[124:127],v[176:179],v[24:27]\n"
LD "v196,s[48:51],s89 offen lds\n"
MF "v[32:35],a[128:131],v[176:179],v[32:35]\n"
"s_nop 0\n"
TR "a[244:245],v1 offset:8192\n"
"ds_write_addtid_b32 v151 offset:48896\n"
MF "v[32:35],a[168:171],v[180:183],v[32:35]\n"
"s_nop 0\n"
TR "a[246:247],v1 offset:10240\n"
"ds_write_addtid_b32 v152 offset:49152\n"
MF "v[32:35],a[208:211],v[184:187],v[32:35]\n"
LD "v197,s[48:51],s89 offen offset:1024 lds\n"
MF "v[40:43],a[212:215],v[184:187],v[40:43]\n"
"s_nop 0\n"
"ds_write_addtid_b32 v153 offset:49408\n"
"ds_write_addtid_b32 v154 offset:49664\n"
MF "v[40:43],a[172:175],v[180:183],v[40:43]\n"
"s_nop 0\n"
"ds_write_addtid_b32 v155 offset:49920\n"
"ds_write_addtid_b32 v156 offset:50176\n"
MF "v[40:43],a[132:135],v[176:179],v[40:43]\n"
"s_nop 0\n"
"ds_write_addtid_b32 v157 offset:50432\n"
"ds_write_addtid_b32 v158 offset:50688\n"
MF "v[48:51],a[136:139],v[176:179],v[48:51]\n"
"s_nop 0\n"
"ds_write_addtid_b32 v159 offset:50944\n"
"ds_write_addtid_b32 v160 offset:51200\n"
MF "v[48:51],a[176:179],v[180:183],v[48:51]\n"
LD "v198,s[48:51],s89 offen offset:2048 lds\n"
MF "v[48:51],a[216:219],v[184:187],v[48:51]\n"
"s_waitcnt lgkmcnt(9)\n"
"ds_write_addtid_b32 v161 offset:51456\n"
"ds_write_addtid_b32 v162 offset:51712\n"
MF "v[52:55],a[216:219],a[244:247],v[52:55]\n"
"s_nop 0\n"
TR "v[176:177],v2\n"
"ds_write_addtid_b32 v163 offset:51968\n"
MF "v[52:55],a[176:179],a[240:243],v[52:55]\n"
"s_nop 0\n"
TR "v[178:179],v2 offset:2048\n"
"ds_write_addtid_b32 v164 offset:52224\n"
MF "v[52:55],a[136:139],v[188:191],v[52:55]\n"
LD "v199,s[48:51],s89 offen offset:3072 lds\n"
MF "v[44:47],a[132:135],v[188:191],v[44:47]\n"
"s_nop 0\n"
TR "v[180:181],v2 offset:4096\n"
"ds_write_addtid_b32 v165 offset:52480\n"
MF "v[44:47],a[172:175],a[240:243],v[44:47]\n"
"s_nop 0\n"
TR "v[182:183],v2 offset:6144\n"
"ds_write_addtid_b32 v166 offset:52736\n"
MF "v[44:47],a[212:215],a[244:247],v[44:47]\n"
"s_nop 0\n"
"ds_write_addtid_b32 v167 offset:52992\n"
"ds_write_addtid_b32 v168 offset:53248\n"
MF "v[36:39],a[208:211],a[244:247],v[36:39]\n"
"s_nop 0\n"
TR "v[184:185],v2 offset:8192\n"
"ds_write_addtid_b32 v169 offset:53504\n"
MF "v[36:39],a[168:171],a[240:243],v[36:39]\n"
"s_add_u32 m0,s79,32768\n"
TR "v[186:187],v2 offset:10240\n"
"ds_write_addtid_b32 v170 offset:49664\n"
MF "v[36:39],a[128:131],v[188:191],v[36:39]\n"
LD "v200,s[48:51],s89 offen lds\n"
MF "v[28:31],a[124:127],v[188:191],v[28:31]\n"
"s_nop 0\n"
"ds_write_addtid_b32 v171 offset:49920\n"
"ds_write_addtid_b32 v172 offset:50176\n"
MF "v[28:31],a[164:167],a[240:243],v[28:31]\n"
"s_nop 0\n"
"ds_write_addtid_b32 v173 offset:50432\n"
"ds_write_addtid_b32 v174 offset:50688\n"
MF "v[28:31],a[204:207],a[244:247],v[28:31]\n"
"ds_write_addtid_b32 v175 offset:50944\n"
MF "v[20:23],a[200:203],a[244:247],v[20:23]\n"
LD "v201,s[48:51],s89 offen offset:1024 lds\n"
MF "v[20:23],a[160:163],a[240:243],v[20:23]\n"
MF "v[20:23],a[120:123],v[188:191],v[20:23]\n"
"s_waitcnt lgkmcnt(6)\n"
MF "v[56:59],a[120:123],v[176:179],v[56:59]\n"
TR "v[188:189],v3\n"
MF "v[56:59],a[160:163],v[180:183],v[56:59]\n"
TR "v[190:191],v3 offset:2048\n"
MF "v[56:59],a[200:203],v[184:187],v[56:59]\n"
LD "v202,s[48:51],s89 offen offset:2048 lds\n"
MF "v[64:67],a[204:207],v[184:187],v[64:67]\n"
TR "a[240:241],v3 offset:4096\n"
MF "v[64:67],a[164:167],v[180:183],v[64:67]\n"
TR "a[242:243],v3 offset:6144\n"
MF "v[64:67],a[124:127],v[176:179],v[64:67]\n"
LD "v203,s[48:51],s89 offen offset:3072 lds\n"
MF "v[72:75],a[128:131],v[176:179],v[72:75]\n"
TR "a[244:245],v3 offset:8192\n"
MF "v[72:75],a[168:171],v[180:183],v[72:75]\n"
TR "a[246:247],v3 offset:10240\n"
MF "v[72:75],a[208:211],v[184:187],v[72:75]\n"
MF "v[80:83],a[212:215],v[184:187],v[80:83]\n"
MF "v[80:83],a[172:175],v[180:183],v[80:83]\n"
MF "v[80:83],a[132:135],v[176:179],v[80:83]\n"
MF "v[88:91],a[136:139],v[176:179],v[88:91]\n"
MF "v[88:91],a[176:179],v[180:183],v[88:91]\n"
MF "v[88:91],a[216:219],v[184:187],v[88:91]\n"
"s_waitcnt vmcnt(28) lgkmcnt(0)\n"
"s_barrier\n"
MF "v[92:95],a[216:219],a[244:247],v[92:95]\n"
"s_add_u32 s80,s89,122880\n"
TR "v[176:177],v0 offset:49152\n"
MF "v[92:95],a[176:179],a[240:243],v[92:95]\n"
"s_add_u32 m0,s79,0\n"
"s_mul_i32 s95,s92,3277\n"
TR "v[178:179],v0 offset:51200\n"
MF "v[92:95],a[136:139],v[188:191],v[92:95]\n"
LD "v192,s[48:51],s80 offen lds\n"
MF "v[84:87],a[132:135],v[188:191],v[84:87]\n"
"s_lshr_b32 s95,s95,16\n"
"s_mul_i32 s59,s95,20\n"
TR "v[180:181],v0 offset:53248\n"
MF "v[84:87],a[172:175],a[240:243],v[84:87]\n"
"s_sub_u32 s59,s92,s59\n"
TR "v[182:183],v0 offset:55296\n"
MF "v[84:87],a[212:215],a[244:247],v[84:87]\n"
LD "v193,s[48:51],s80 offen offset:1024 lds\n"
MF "v[76:79],a[208:211],a[244:247],v[76:79]\n"
"s_cmp_ge_u32 s59,10\n"
"s_cselect_b32 s96,4294557760,0\n"
TR "v[184:185],v0 offset:57344\n"
MF "v[76:79],a[168:171],a[240:243],v[76:79]\n"
"s_add_u32 s96,s96,1024\n"
"s_mul_i32 s59,s59,40960\n"
TR "v[186:187],v0 offset:59392\n"
MF "v[76:79],a[128:131],v[188:191],v[76:79]\n"
"s_add_u32 s96,s96,s59\n"
"s_lshl_b32 s59,s95,7\n"
"s_add_u32 s96,s96,s59\n"
MF "v[68:71],a[124:127],v[188:191],v[68:71]\n"
"s_add_u32 s95,s95,3\n"
"s_cmp_ge_u32 s92,40\n"
"s_cselect_b32 s95,99,s95\n"
MF "v[68:71],a[164:167],a[240:243],v[68:71]\n"
LD "v194,s[48:51],s80 offen offset:2048 lds\n"
MF "v[68:71],a[204:207],a[244:247],v[68:71]\n"
"s_cmp_ge_u32 s85,5\n"
"s_cselect_b32 s95,99,s95\n"
"s_cselect_b32 s72,s85,0\n"
MF "v[60:63],a[200:203],a[244:247],v[60:63]\n"
MF "v[60:63],a[160:163],a[240:243],v[60:63]\n"
MF "v[60:63],a[120:123],v[188:191],v[60:63]\n"
"s_waitcnt lgkmcnt(0)\n"
LD "v195,s[48:51],s80 offen offset:3072 lds\n"
MF "v[16:19],a[20:23],v[176:179],v[16:19]\n"
TR "v[188:189],v1 offset:49152\n"
MF "v[16:19],a[60:63],v[180:183],v[16:19]\n"
TR "v[190:191],v1 offset:51200\n"
MF "v[16:19],a[100:103],v[184:187],v[16:19]\n"
MF "v[24:27],a[104:107],v[184:187],v[24:27]\n"
TR "a[240:241],v1 offset:53248\n"
MF "v[24:27],a[64:67],v[180:183],v[24:27]\n"
"s_add_u32 m0,s79,4096\n"
TR "a[242:243],v1 offset:55296\n"
MF "v[24:27],a[24:27],v[176:179],v[24:27]\n"
LD "v196,s[48:51],s80 offen lds\n"
MF "v[32:35],a[28:31],v[176:179],v[32:35]\n"
TR "a[244:245],v1 offset:57344\n"
MF "v[32:35],a[68:71],v[180:183],v[32:35]\n"
TR "a[246:247],v1 offset:59392\n"
MF "v[32:35],a[108:111],v[184:187],v[32:35]\n"
LD "v197,s[48:51],s80 offen offset:1024 lds\n"
MF "v[40:43],a[112:115],v[184:187],v[40:43]\n"
MF "v[40:43],a[72:75],v[180:183],v[40:43]\n"
MF "v[40:43],a[32:35],v[176:179],v[40:43]\n"
MF "v[48:51],a[36:39],v[176:179],v[48:51]\n"
MF "v[48:51],a[76:79],v[180:183],v[48:51]\n"
LD "v198,s[48:51],s80 offen offset:2048 lds\n"
MF "v[48:51],a[116:119],v[184:187],v[48:51]\n"
"s_waitcnt lgkmcnt(0)\n"
MF "v[52:55],a[116:119],a[244:247],v[52:55]\n"
TR "v[176:177],v2 offset:49152\n"
MF "v[52:55],a[76:79],a[240:243],v[52:55]\n"
TR "v[178:179],v2 offset:51200\n"
MF "v[52:55],a[36:39],v[188:191],v[52:55]\n"
LD "v199,s[48:51],s80 offen offset:3072 lds\n"
MF "v[44:47],a[32:35],v[188:191],v[44:47]\n"
TR "v[180:181],v2 offset:53248\n"
MF "v[44:47],a[72:75],a[240:243],v[44:47]\n"
TR "v[182:183],v2 offset:55296\n"
MF "v[44:47],a[112:115],a[244:247],v[44:47]\n"
MF "v[36:39],a[108:111],a[244:247],v[36:39]\n"
TR "v[184:185],v2 offset:57344\n"
MF "v[36:39],a[68:71],a[240:243],v[36:39]\n"
"s_add_u32 m0,s79,8192\n"
TR "v[186:187],v2 offset:59392\n"
MF "v[36:39],a[28:31],v[188:191],v[36:39]\n"
LD "v200,s[48:51],s80 offen lds\n"
MF "v[28:31],a[24:27],v[188:191],v[28:31]\n"
MF "v[28:31],a[64:67],a[240:243],v[28:31]\n"
MF "v[28:31],a[104:107],a[244:247],v[28:31]\n"
MF "v[20:23],a[100:103],a[244:247],v[20:23]\n"
LD "v201,s[48:51],s80 offen offset:1024 lds\n"
MF "v[20:23],a[60:63],a[240:243],v[20:23]\n"
"s_waitcnt vmcnt(19)\n"
"s_cmp_eq_u32 s83,0xf0000000\n"
MF "v[20:23],a[20:23],v[188:191],v[20:23]\n"
"s_cbranch_scc1 .Ljs7\n"
"s_cmp_eq_u32 s83,0xf0000010\n"
"s_cbranch_scc1 .Ljc7\n"
MF "v[248:251],v[210:213],a[250:253],0\n"
"v_mov_b32 v240,v208\n"
"v_mov_b32 v241,v209\n"
MF "v[244:247],v[214:217],a[250:253],0\n"
MF "v[248:251],v[218:221],a[250:253],v[248:251]\n"
MF "v[244:247],v[222:225],a[250:253],v[244:247]\n"
MF "v[248:251],v[226:229],a[250:253],v[248:251]\n"
MF "v[244:247],v[230:233],a[250:253],v[244:247]\n"
MF "v[248:251],v[234:237],a[250:253],v[248:251]\n"
MF "v[244:247],v[238:241],a[250:253],v[244:247]\n"
"s_branch .Ljs7\n"
".Ljc7:\n"
MF "v[16:19],a[252:255],v[208:211],v[16:19]\n"
MF "v[24:27],a[252:255],v[212:215],v[24:27]\n"
MF "v[32:35],a[252:255],v[216:219],v[32:35]\n"
MF "v[40:43],a[252:255],v[220:223],v[40:43]\n"
MF "v[48:51],a[252:255],v[224:227],v[48:51]\n"
MF "v[20:23],a[248:251],v[208:211],v[20:23]\n"
MF "v[28:31],a[248:251],v[212:215],v[28:31]\n"
MF "v[36:39],a[248:251],v[216:219],v[36:39]\n"
MF "v[44:47],a[248:251],v[220:223],v[44:47]\n"
MF "v[52:55],a[248:251],v[224:227],v[52:55]\n"
".Ljs7:\n"
"s_waitcnt lgkmcnt(0)\n"
MF "v[56:59],a[20:23],v[176:179],v[56:59]\n"
TR "v[188:189],v3 offset:49152\n"
"v_sub_u32 v10,v10,v11\n"
MF "v[56:59],a[60:63],v[180:183],v[56:59]\n"
"s_lshl_b32 s82,s92,10\n"
TR "v[190:191],v3 offset:51200\n"
"v_cmp_gt_u32 vcc,s95,v10\n"
MF "v[56:59],a[100:103],v[184:187],v[56:59]\n"
LD "v202,s[48:51],s80 offen offset:2048 lds\n"
MF "v[64:67],a[104:107],v[184:187],v[64:67]\n"
"s_add_u32 s82,s82,s93\n"
TR "a[240:241],v3 offset:53248\n"
CV "v244,v244,v245\n"
MF "v[64:67],a[64:67],v[180:183],v[64:67]\n"
TR "a[242:243],v3 offset:55296\n"
CV "v245,v246,v247\n"
MF "v[64:67],a[24:27],v[176:179],v[64:67]\n"
LD "v203,s[48:51],s80 offen offset:3072 lds\n"
MF "v[72:75],a[28:31],v[176:179],v[72:75]\n"
TR "a[244:245],v3 offset:57344\n"
CV "v246,v248,v249\n"
MF "v[72:75],a[68:71],v[180:183],v[72:75]\n"
TR "a[246:247],v3 offset:59392\n"
CV "v247,v250,v251\n"
MF "v[72:75],a[108:111],v[184:187],v[72:75]\n"
"s_cmp_eq_u64 vcc,0\n"
"s_cselect_b32 s82,s82,0xf0000000\n"
"s_cselect_b32 s84,s96,0xf0000000\n"
"s_cselect_b32 s59,32,0\n"
MF "v[80:83],a[112:115],v[184:187],v[80:83]\n"
"s_add_u32 s92,s92,s59\n"
".Lnf1:\n"
"v_readlane_b32 s58,v10,s67\n"
"s_cmp_lt_u32 s58,s72\n"
"s_cbranch_scc1 .Lsp1\n"
".Lnb1:\n"
MF "v[80:83],a[72:75],v[180:183],v[80:83]\n"
"s_cmp_ge_u32 s85,5\n"
"s_cselect_b32 s82,s70,s82\n"
"s_cselect_b32 s84,0xf0000010,s84\n"
MF "v[80:83],a[32:35],v[176:179],v[80:83]\n"
"s_cmp_ge_u32 s85,5\n"
"s_cselect_b32 s71,1024,s71\n"
"s_cselect_b32 s66,0x80000000,s66\n"
MF "v[88:91],a[36:39],v[176:179],v[88:91]\n"
"s_add_u32 s70,s70,20480\n"
"v_permlane16_swap_b32 v244,v246\n"
"v_permlane16_swap_b32 v245,v247\n"
MF "v[88:91],a[76:79],v[180:183],v[88:91]\n"
"buffer_store_dwordx4 v[244:247],v8,s[44:47],s83 offen sc1\n"
MF "v[88:91],a[116:119],v[184:187],v[88:91]\n"
"s_waitcnt vmcnt(26) lgkmcnt(0)\n"
"s_barrier\n"
MF "v[92:95],a[116:119],a[244:247],v[92:95]\n"
"s_add_u32 s88,s88,s65\n"
"s_mov_b32 s83,s84\n"
TR "v[176:177],v4\n"
MF "v[92:95],a[76:79],a[240:243],v[92:95]\n"
"s_add_u32 m0,s79,49152\n"
TR "v[178:179],v4 offset:2048\n"
MF "v[92:95],a[36:39],v[188:191],v[92:95]\n"
LD "v192,s[48:51],s88 offen lds\n"
MF "v[84:87],a[32:35],v[188:191],v[84:87]\n"
TR "v[180:181],v4 offset:4096\n"
MF "v[84:87],a[72:75],a[240:243],v[84:87]\n"
TR "v[182:183],v4 offset:6144\n"
MF "v[84:87],a[112:115],a[244:247],v[84:87]\n"
LD "v193,s[48:51],s88 offen offset:1024 lds\n"
MF "v[76:79],a[108:111],a[244:247],v[76:79]\n"
TR "v[184:185],v4 offset:8192\n"
MF "v[76:79],a[68:71],a[240:243],v[76:79]\n"
TR "v[186:187],v4 offset:10240\n"
MF "v[76:79],a[28:31],v[188:191],v[76:79]\n"
LD "v[208:211],v9,s[40:43],s82 offen sc1\n"
MF "v[68:71],a[24:27],v[188:191],v[68:71]\n"
MF "v[68:71],a[64:67],a[240:243],v[68:71]\n"
LD "v194,s[48:51],s88 offen offset:2048 lds\n"
MF "v[68:71],a[104:107],a[244:247],v[68:71]\n"
MF "v[60:63],a[100:103],a[244:247],v[60:63]\n"
MF "v[60:63],a[60:63],a[240:243],v[60:63]\n"
MF "v[60:63],a[20:23],v[188:191],v[60:63]\n"
"s_waitcnt lgkmcnt(0)\n"
LD "v195,s[48:51],s88 offen offset:3072 lds\n"
MF "v[16:19],a[140:143],v[176:179],v[16:19]\n"
TR "v[188:189],v5\n"
MF "v[16:19],a[180:183],v[180:183],v[16:19]\n"
"s_add_u32 s82,s82,s71\n"
TR "v[190:191],v5 offset:2048\n"
MF "v[16:19],a[220:223],v[184:187],v[16:19]\n"
LD "v[212:215],v9,s[40:43],s82 offen sc1\n"
MF "v[24:27],a[224:227],v[184:187],v[24:27]\n"
TR "a[240:241],v5 offset:4096\n"
MF "v[24:27],a[184:187],v[180:183],v[24:27]\n"
"s_add_u32 m0,s79,53248\n"
TR "a[242:243],v5 offset:6144\n"
MF "v[24:27],a[144:147],v[176:179],v[24:27]\n"
LD "v196,s[48:51],s88 offen lds\n"
MF "v[32:35],a[148:151],v[176:179],v[32:35]\n"
TR "a[244:245],v5 offset:8192\n"
MF "v[32:35],a[188:191],v[180:183],v[32:35]\n"
TR "a[246:247],v5 offset:10240\n"
MF "v[32:35],a[228:231],v[184:187],v[32:35]\n"
LD "v197,s[48:51],s88 offen offset:1024 lds\n"
MF "v[40:43],a[232:235],v[184:187],v[40:43]\n"
"s_add_u32 s82,s82,s71\n"
MF "v[40:43],a[192:195],v[180:183],v[40:43]\n"
LD "v[216:219],v9,s[40:43],s82 offen sc1\n"
MF "v[40:43],a[152:155],v[176:179],v[40:43]\n"
MF "v[48:51],a[156:159],v[176:179],v[48:51]\n"
MF "v[48:51],a[196:199],v[180:183],v[48:51]\n"
LD "v198,s[48:51],s88 offen offset:2048 lds\n"
MF "v[48:51],a[236:239],v[184:187],v[48:51]\n"
"s_waitcnt lgkmcnt(0)\n"
MF "v[52:55],a[236:239],a[244:247],v[52:55]\n"
TR "v[176:177],v6\n"
MF "v[52:55],a[196:199],a[240:243],v[52:55]\n"
TR "v[178:179],v6 offset:2048\n"
MF "v[52:55],a[156:159],v[188:191],v[52:55]\n"
LD "v199,s[48:51],s88 offen offset:3072 lds\n"
MF "v[44:47],a[152:155],v[188:191],v[44:47]\n"
TR "v[180:181],v6 offset:4096\n"
MF "v[44:47],a[192:195],a[240:243],v[44:47]\n"
"s_add_u32 s82,s82,s71\n"
TR "v[182:183],v6 offset:6144\n"
MF "v[44:47],a[232:235],a[244:247],v[44:47]\n"
LD "v[220:223],v9,s[40:43],s82 offen sc1\n"
MF "v[36:39],a[228:231],a[244:247],v[36:39]\n"
TR "v[184:185],v6 offset:8192\n"
MF "v[36:39],a[188:191],a[240:243],v[36:39]\n"
"s_add_u32 m0,s79,57344\n"
TR "v[186:187],v6 offset:10240\n"
MF "v[36:39],a[148:151],v[188:191],v[36:39]\n"
LD "v200,s[48:51],s88 offen lds\n"
MF "v[28:31],a[144:147],v[188:191],v[28:31]\n"
MF "v[28:31],a[184:187],a[240:243],v[28:31]\n"
"s_add_u32 s82,s82,s71\n"
MF "v[28:31],a[224:227],a[244:247],v[28:31]\n"
LD "v[224:227],v9,s[40:43],s82 offen sc1\n"
MF "v[20:23],a[220:223],a[244:247],v[20:23]\n"
LD "v201,s[48:51],s88 offen offset:1024 lds\n"
MF "v[20:23],a[180:183],a[240:243],v[20:23]\n"
MF "v[20:23],a[140:143],v[188:191],v[20:23]\n"
"s_waitcnt lgkmcnt(0)\n"
MF "v[56:59],a[140:143],v[176:179],v[56:59]\n"
TR "v[188:189],v7\n"
MF "v[56:59],a[180:183],v[180:183],v[56:59]\n"
TR "v[190:191],v7 offset:2048\n"
MF "v[56:59],a[220:223],v[184:187],v[56:59]\n"
LD "v202,s[48:51],s88 offen offset:2048 lds\n"
MF "v[64:67],a[224:227],v[184:187],v[64:67]\n"
TR "a[240:241],v7 offset:4096\n"
MF "v[64:67],a[184:187],v[180:183],v[64:67]\n"
TR "a[242:243],v7 offset:6144\n"
MF "v[64:67],a[144:147],v[176:179],v[64:67]\n"
LD "v203,s[48:51],s88 offen offset:3072 lds\n"
MF "v[72:75],a[148:151],v[176:179],v[72:75]\n"
TR "a[244:245],v7 offset:8192\n"
MF "v[72:75],a[188:191],v[180:183],v[72:75]\n"
"s_add_u32 s82,s82,s66\n"
TR "a[246:247],v7 offset:10240\n"
MF "v[72:75],a[228:231],v[184:187],v[72:75]\n"
LD "v[228:231],v9,s[40:43],s82 offen sc1\n"
MF "v[80:83],a[232:235],v[184:187],v[80:83]\n"
MF "v[80:83],a[192:195],v[180:183],v[80:83]\n"
MF "v[80:83],a[152:155],v[176:179],v[80:83]\n"
MF "v[88:91],a[156:159],v[176:179],v[88:91]\n"
"s_add_u32 s82,s82,s71\n"
MF "v[88:91],a[196:199],v[180:183],v[88:91]\n"
LD "v[232:235],v9,s[40:43],s82 offen sc1\n"
MF "v[88:91],a[236:239],v[184:187],v[88:91]\n"
"s_waitcnt vmcnt(32) lgkmcnt(0)\n"
"s_barrier\n"
MF "v[92:95],a[236:239],a[244:247],v[92:95]\n"
"s_add_u32 s80,s88,122880\n"
TR "v[176:177],v0 offset:24576\n"
MF "v[92:95],a[196:199],a[240:243],v[92:95]\n"
"s_add_u32 m0,s79,98304\n"
"s_add_u32 s85,s85,1\n"
TR "v[178:179],v0 offset:26624\n"
MF "v[92:95],a[156:159],v[188:191],v[92:95]\n"
LD "v192,s[48:51],s80 offen lds\n"
MF "v[84:87],a[152:155],v[188:191],v[84:87]\n"
"s_nop 0\n"
TR "v[180:181],v0 offset:28672\n"
"ds_write_addtid_b32 v16 offset:24576\n"
MF "v[84:87],a[192:195],a[240:243],v[84:87]\n"
"s_nop 0\n"
TR "v[182:183],v0 offset:30720\n"
"ds_write_addtid_b32 v17 offset:24832\n"
MF "v[84:87],a[232:235],a[244:247],v[84:87]\n"
LD "v193,s[48:51],s80 offen offset:1024 lds\n"
MF "v[76:79],a[228:231],a[244:247],v[76:79]\n"
"s_nop 0\n"
TR "v[184:185],v0 offset:32768\n"
"ds_write_addtid_b32 v18 offset:25088\n"
MF "v[76:79],a[188:191],a[240:243],v[76:79]\n"
"s_add_u32 s82,s82,s71\n"
TR "v[186:187],v0 offset:34816\n"
"ds_write_addtid_b32 v19 offset:25344\n"
MF "v[76:79],a[148:151],v[188:191],v[76:79]\n"
LD "v[236:239],v9,s[40:43],s82 offen sc1\n"
MF "v[68:71],a[144:147],v[188:191],v[68:71]\n"
LD "v194,s[48:51],s80 offen offset:2048 lds\n"
MF "v[68:71],a[184:187],a[240:243],v[68:71]\n"
"s_nop 0\n"
"ds_write_addtid_b32 v20 offset:25600\n"
"ds_write_addtid_b32 v21 offset:25856\n"
MF "v[68:71],a[224:227],a[244:247],v[68:71]\n"
"s_nop 0\n"
"ds_write_addtid_b32 v22 offset:26112\n"
"ds_write_addtid_b32 v23 offset:26368\n"
MF "v[60:63],a[220:223],a[244:247],v[60:63]\n"
"s_nop 0\n"
"ds_write_addtid_b32 v24 offset:26624\n"
"ds_write_addtid_b32 v25 offset:26880\n"
MF "v[60:63],a[180:183],a[240:243],v[60:63]\n"
"s_nop 0\n"
"ds_write_addtid_b32 v26 offset:27136\n"
"ds_write_addtid_b32 v27 offset:27392\n"
MF "v[60:63],a[140:143],v[188:191],v[60:63]\n"
"s_waitcnt lgkmcnt(9)\n"
"s_cmp_lg_u32 s85,9\n"
"s_cbranch_scc1 .Lpl0\n"
".Lplast0:\n"
"s_cbranch_execz .Lipr3\n"
MF "v[96:99],v[176:179],a[0:3],v[252:255]\n"
"s_nop 0\n"
TR "v[188:189],v1 offset:24576\n"
TR "v[190:191],v1 offset:26624\n"
MF "v[96:99],v[180:183],a[40:43],v[96:99]\n"
"s_nop 0\n"
TR "a[240:241],v1 offset:28672\n"
TR "a[242:243],v1 offset:30720\n"
MF "v[96:99],v[184:187],a[80:83],v[96:99]\n"
"s_add_u32 s90,s91,163840\n"
TR "a[244:245],v1 offset:32768\n"
TR "a[246:247],v1 offset:34816\n"
MF "v[104:107],v[184:187],a[84:87],v[252:255]\n"
LD "v195,s[48:51],s80 offen offset:3072 lds\n"
MF "v[104:107],v[180:183],a[44:47],v[104:107]\n"
"s_nop 0\n"
"ds_write_addtid_b32 v28 offset:27648\n"
"ds_write_addtid_b32 v29 offset:27904\n"
MF "v[104:107],v[176:179],a[4:7],v[104:107]\n"
"s_nop 0\n"
"ds_write_addtid_b32 v30 offset:28160\n"
"ds_write_addtid_b32 v31 offset:28416\n"
MF "v[112:115],v[176:179],a[8:11],v[252:255]\n"
"s_nop 0\n"
"ds_write_addtid_b32 v32 offset:28672\n"
"ds_write_addtid_b32 v33 offset:28928\n"
MF "v[112:115],v[180:183],a[48:51],v[112:115]\n"
"s_add_u32 m0,s79,102400\n"
"s_nop 0\n"
"ds_write_addtid_b32 v34 offset:25088\n"
"ds_write_addtid_b32 v35 offset:25344\n"
MF "v[112:115],v[184:187],a[88:91],v[112:115]\n"
LD "v196,s[48:51],s80 offen lds\n"
MF "v[120:123],v[184:187],a[92:95],v[252:255]\n"
"s_nop 0\n"
"ds_write_addtid_b32 v36 offset:25600\n"
"ds_write_addtid_b32 v37 offset:25856\n"
MF "v[120:123],v[180:183],a[52:55],v[120:123]\n"
"ds_write_addtid_b32 v38 offset:26112\n"
MF "v[120:123],v[176:179],a[12:15],v[120:123]\n"
"ds_write_addtid_b32 v39 offset:26368\n"
MF "v[128:131],v[176:179],a[16:19],v[252:255]\n"
"ds_write_addtid_b32 v40 offset:26624\n"
MF "v[128:131],v[180:183],a[56:59],v[128:131]\n"
"ds_write_addtid_b32 v41 offset:26880\n"
MF "v[128:131],v[184:187],a[96:99],v[128:131]\n"
"s_waitcnt lgkmcnt(14)\n"
LD "v197,s[48:51],s80 offen offset:1024 lds\n"
MF "v[132:135],a[244:247],a[96:99],v[252:255]\n"
"s_nop 0\n"
TR "v[176:177],v2 offset:24576\n"
TR "v[178:179],v2 offset:26624\n"
MF "v[132:135],a[240:243],a[56:59],v[132:135]\n"
"s_nop 0\n"
TR "v[180:181],v2 offset:28672\n"
TR "v[182:183],v2 offset:30720\n"
MF "v[132:135],v[188:191],a[16:19],v[132:135]\n"
"s_nop 0\n"
TR "v[184:185],v2 offset:32768\n"
TR "v[186:187],v2 offset:34816\n"
MF "v[124:127],v[188:191],a[12:15],v[252:255]\n"
"s_nop 0\n"
"ds_write_addtid_b32 v42 offset:27136\n"
"ds_write_addtid_b32 v43 offset:27392\n"
MF "v[124:127],a[240:243],a[52:55],v[124:127]\n"
"s_nop 0\n"
"ds_write_addtid_b32 v44 offset:27648\n"
"ds_write_addtid_b32 v45 offset:27904\n"
MF "v[124:127],a[244:247],a[92:95],v[124:127]\n"
"s_nop 0\n"
"ds_write_addtid_b32 v46 offset:28160\n"
"ds_write_addtid_b32 v47 offset:28416\n"
MF "v[116:119],a[244:247],a[88:91],v[252:255]\n"
LD "v198,s[48:51],s80 offen offset:2048 lds\n"
MF "v[116:119],a[240:243],a[48:51],v[116:119]\n"
"s_nop 0\n"
"ds_write_addtid_b32 v48 offset:28672\n"
"ds_write_addtid_b32 v49 offset:28928\n"
MF "v[116:119],v[188:191],a[8:11],v[116:119]\n"
"s_nop 0\n"
"ds_write_addtid_b32 v50 offset:29184\n"
"ds_write_addtid_b32 v51 offset:29440\n"
MF "v[108:111],v[188:191],a[4:7],v[252:255]\n"
"ds_write_addtid_b32 v52 offset:29696\n"
MF "v[108:111],a[240:243],a[44:47],v[108:111]\n"
"ds_write_addtid_b32 v53 offset:29952\n"
MF "v[108:111],a[244:247],a[84:87],v[108:111]\n"
"ds_write_addtid_b32 v54 offset:30208\n"
MF "v[100:103],a[244:247],a[80:83],v[252:255]\n"
"ds_write_addtid_b32 v55 offset:30464\n"
MF "v[100:103],a[240:243],a[40:43],v[100:103]\n"
LD "v199,s[48:51],s80 offen offset:3072 lds\n"
MF "v[100:103],v[188:191],a[0:3],v[100:103]\n"
"s_waitcnt lgkmcnt(14)\n"
MF "v[136:139],v[176:179],a[0:3],v[252:255]\n"
"s_nop 0\n"
TR "v[188:189],v3 offset:24576\n"
TR "v[190:191],v3 offset:26624\n"
MF "v[136:139],v[180:183],a[40:43],v[136:139]\n"
"s_nop 0\n"
TR "a[240:241],v3 offset:28672\n"
TR "a[242:243],v3 offset:30720\n"
MF "v[136:139],v[184:187],a[80:83],v[136:139]\n"
"s_add_u32 m0,s79,106496\n"
TR "a[244:245],v3 offset:32768\n"
TR "a[246:247],v3 offset:34816\n"
MF "v[144:147],v[184:187],a[84:87],v[252:255]\n"
"s_waitcnt lgkmcnt(0)\n"
"s_barrier\n"
MF "v[144:147],v[180:183],a[44:47],v[144:147]\n"
LD "v200,s[48:51],s80 offen lds\n"
MF "v[144:147],v[176:179],a[4:7],v[144:147]\n"
"ds_read2st64_b32 v[16:17],v13 offset0:0 offset1:1\n"
MF "v[152:155],v[176:179],a[8:11],v[252:255]\n"
"ds_read2st64_b32 v[18:19],v13 offset0:2 offset1:3\n"
MF "v[152:155],v[180:183],a[48:51],v[152:155]\n"
"ds_read2st64_b32 v[20:21],v13 offset0:4 offset1:5\n"
MF "v[152:155],v[184:187],a[88:91],v[152:155]\n"
"ds_read2st64_b32 v[22:23],v13 offset0:6 offset1:7\n"
MF "v[160:163],v[184:187],a[92:95],v[252:255]\n"
"ds_read2st64_b32 v[24:25],v13 offset0:8 offset1:9\n"
MF "v[160:163],v[180:183],a[52:55],v[160:163]\n"
"ds_read2st64_b32 v[26:27],v13 offset0:10 offset1:11\n"
MF "v[160:163],v[176:179],a[12:15],v[160:163]\n"
LD "v201,s[48:51],s80 offen offset:1024 lds\n"
MF "v[168:171],v[176:179],a[16:19],v[252:255]\n"
"ds_read2st64_b32 v[28:29],v13 offset0:12 offset1:13\n"
MF "v[168:171],v[180:183],a[56:59],v[168:171]\n"
"s_waitcnt lgkmcnt(3)\n"
"ds_read2st64_b32 v[30:31],v13 offset0:14 offset1:15\n"
"v_add_f32 v56,v56,v16\n"
MF "v[168:171],v[184:187],a[96:99],v[168:171]\n"
"s_waitcnt vmcnt(31) lgkmcnt(0)\n"
"s_barrier\n"
MF "v[172:175],a[244:247],a[96:99],v[252:255]\n"
"s_nop 0\n"
TR "v[176:177],v0\n"
TR "v[178:179],v0 offset:2048\n"
MF "v[172:175],a[240:243],a[56:59],v[172:175]\n"
"s_nop 0\n"
TR "v[180:181],v0 offset:4096\n"
TR "v[182:183],v0 offset:6144\n"
MF "v[172:175],v[188:191],a[16:19],v[172:175]\n"
"s_nop 0\n"
TR "v[184:185],v0 offset:8192\n"
TR "v[186:187],v0 offset:10240\n"
MF "v[164:167],v[188:191],a[12:15],v[252:255]\n"
LD "v202,s[48:51],s80 offen offset:2048 lds\n"
MF "v[164:167],a[240:243],a[52:55],v[164:167]\n"
"ds_read2st64_b32 v[32:33],v13 offset0:16 offset1:17\n"
"v_add_f32 v57,v57,v17\n"
MF "v[164:167],a[244:247],a[92:95],v[164:167]\n"
"ds_read2st64_b32 v[34:35],v13 offset0:18 offset1:19\n"
"v_add_f32 v58,v58,v18\n"
MF "v[156:159],a[244:247],a[88:91],v[252:255]\n"
"ds_read2st64_b32 v[36:37],v13 offset0:20 offset1:21\n"
"v_add_f32 v59,v59,v19\n"
MF "v[156:159],a[240:243],a[48:51],v[156:159]\n"
"ds_read2st64_b32 v[38:39],v13 offset0:22 offset1:23\n"
"v_add_f32 v60,v60,v20\n"
MF "v[156:159],v[188:191],a[8:11],v[156:159]\n"
"ds_read2st64_b32 v[40:41],v13 offset0:24 offset1:25\n"
"v_add_f32 v61,v61,v21\n"
MF "v[148:151],v[188:191],a[4:7],v[252:255]\n"
LD "v203,s[48:51],s80 offen offset:3072 lds\n"
MF "v[148:151],a[240:243],a[44:47],v[148:151]\n"
"ds_read2st64_b32 v[42:43],v13 offset0:26 offset1:27\n"
"v_add_f32 v62,v62,v22\n"
MF "v[148:151],a[244:247],a[84:87],v[148:151]\n"
"ds_read2st64_b32 v[44:45],v13 offset0:28 offset1:29\n"
"v_add_f32 v63,v63,v23\n"
MF "v[140:143],a[244:247],a[80:83],v[252:255]\n"
"ds_read2st64_b32 v[46:47],v13 offset0:30 offset1:31\n"
CV "v56,v56,v57\n"
MF "v[140:143],a[240:243],a[40:43],v[140:143]\n"
"ds_read2st64_b32 v[48:49],v13 offset0:32 offset1:33\n"
CV "v57,v58,v59\n"
MF "v[140:143],v[188:191],a[0:3],v[140:143]\n"
"s_waitcnt lgkmcnt(9)\n"
"ds_read2st64_b32 v[50:51],v13 offset0:34 offset1:35\n"
CV "v58,v60,v61\n"
MF "v[96:99],v[176:179],a[120:123],v[96:99]\n"
"s_nop 0\n"
TR "v[188:189],v1\n"
TR "v[190:191],v1 offset:2048\n"
MF "v[96:99],v[180:183],a[160:163],v[96:99]\n"
"s_nop 0\n"
TR "a[240:241],v1 offset:4096\n"
TR "a[242:243],v1 offset:6144\n"
MF "v[96:99],v[184:187],a[200:203],v[96:99]\n"
"s_nop 0\n"
TR "a[244:245],v1 offset:8192\n"
TR "a[246:247],v1 offset:10240\n"
MF "v[104:107],v[184:187],a[204:207],v[104:107]\n"
"ds_read2st64_b32 v[52:53],v13 offset0:36 offset1:37\n"
CV "v59,v62,v63\n"
MF "v[104:107],v[180:183],a[164:167],v[104:107]\n"
"s_add_u32 s87,s90,10240\n"
"ds_read2st64_b32 v[54:55],v13 offset0:38 offset1:39\n"
"v_add_f32 v64,v64,v24\n"
MF "v[104:107],v[176:179],a[124:127],v[104:107]\n"
"buffer_store_dwordx4 v[56:59],v9,s[40:43],s87 offen sc1\n"
MF "v[112:115],v[176:179],a[128:131],v[112:115]\n"
"v_add_f32 v65,v65,v25\n"
"v_add_f32 v66,v66,v26\n"
MF "v[112:115],v[180:183],a[168:171],v[112:115]\n"
"v_add_f32 v67,v67,v27\n"
"v_add_f32 v68,v68,v28\n"
MF "v[112:115],v[184:187],a[208:211],v[112:115]\n"
"v_add_f32 v69,v69,v29\n"
"v_add_f32 v70,v70,v30\n"
MF "v[120:123],v[184:187],a[212:215],v[120:123]\n"
"v_add_f32 v71,v71,v31\n"
CV "v64,v64,v65\n"
MF "v[120:123],v[180:183],a[172:175],v[120:123]\n"
CV "v65,v66,v67\n"
CV "v66,v68,v69\n"
MF "v[120:123],v[176:179],a[132:135],v[120:123]\n"
"s_add_u32 s87,s90,11264\n"
"s_waitcnt lgkmcnt(14)\n"
CV "v67,v70,v71\n"
MF "v[128:131],v[176:179],a[136:139],v[128:131]\n"
"buffer_store_dwordx4 v[64:67],v9,s[40:43],s87 offen sc1\n"
MF "v[128:131],v[180:183],a[176:179],v[128:131]\n"
"v_add_f32 v72,v72,v32\n"
"v_add_f32 v73,v73,v33\n"
MF "v[128:131],v[184:187],a[216:219],v[128:131]\n"
"s_waitcnt lgkmcnt(2)\n"
"v_add_f32 v74,v74,v34\n"
"v_add_f32 v75,v75,v35\n"
MF "v[132:135],a[244:247],a[216:219],v[132:135]\n"
"s_nop 0\n"
TR "v[176:177],v2\n"
TR "v[178:179],v2 offset:2048\n"
MF "v[132:135],a[240:243],a[176:179],v[132:135]\n"
"s_nop 0\n"
TR "v[180:181],v2 offset:4096\n"
TR "v[182:183],v2 offset:6144\n"
MF "v[132:135],v[188:191],a[136:139],v[132:135]\n"
"s_nop 0\n"
TR "v[184:185],v2 offset:8192\n"
TR "v[186:187],v2 offset:10240\n"
MF "v[124:127],v[188:191],a[132:135],v[124:127]\n"
"v_add_f32 v76,v76,v36\n"
"v_add_f32 v77,v77,v37\n"
MF "v[124:127],a[240:243],a[172:175],v[124:127]\n"
"v_add_f32 v78,v78,v38\n"
"v_add_f32 v79,v79,v39\n"
MF "v[124:127],a[244:247],a[212:215],v[124:127]\n"
CV "v72,v72,v73\n"
CV "v73,v74,v75\n"
MF "v[116:119],a[244:247],a[208:211],v[116:119]\n"
"s_add_u32 s87,s90,12288\n"
CV "v74,v76,v77\n"
CV "v75,v78,v79\n"
MF "v[116:119],a[240:243],a[168:171],v[116:119]\n"
"buffer_store_dwordx4 v[72:75],v9,s[40:43],s87 offen sc1\n"
MF "v[116:119],v[188:191],a[128:131],v[116:119]\n"
"v_add_f32 v80,v80,v40\n"
"v_add_f32 v81,v81,v41\n"
MF "v[108:111],v[188:191],a[124:127],v[108:111]\n"
"v_add_f32 v82,v82,v42\n"
"v_add_f32 v83,v83,v43\n"
MF "v[108:111],a[240:243],a[164:167],v[108:111]\n"
"v_add_f32 v84,v84,v44\n"
"v_add_f32 v85,v85,v45\n"
MF "v[108:111],a[244:247],a[204:207],v[108:111]\n"
"v_add_f32 v86,v86,v46\n"
"v_add_f32 v87,v87,v47\n"
MF "v[100:103],a[244:247],a[200:203],v[100:103]\n"
CV "v80,v80,v81\n"
CV "v81,v82,v83\n"
MF "v[100:103],a[240:243],a[160:163],v[100:103]\n"
"s_add_u32 s87,s90,13312\n"
CV "v82,v84,v85\n"
CV "v83,v86,v87\n"
MF "v[100:103],v[188:191],a[120:123],v[100:103]\n"
"s_waitcnt lgkmcnt(0)\n"
"buffer_store_dwordx4 v[80:83],v9,s[40:43],s87 offen sc1\n"
MF "v[136:139],v[176:179],a[120:123],v[136:139]\n"
"s_nop 0\n"
TR "v[188:189],v3\n"
TR "v[190:191],v3 offset:2048\n"
MF "v[136:139],v[180:183],a[160:163],v[136:139]\n"
"s_nop 0\n"
TR "a[240:241],v3 offset:4096\n"
TR "a[242:243],v3 offset:6144\n"
MF "v[136:139],v[184:187],a[200:203],v[136:139]\n"
"s_nop 0\n"
TR "a[244:245],v3 offset:8192\n"
TR "a[246:247],v3 offset:10240\n"
MF "v[144:147],v[184:187],a[204:207],v[144:147]\n"
"v_add_f32 v88,v88,v48\n"
"v_add_f32 v89,v89,v49\n"
MF "v[144:147],v[180:183],a[164:167],v[144:147]\n"
"v_add_f32 v90,v90,v50\n"
"v_add_f32 v91,v91,v51\n"
MF "v[144:147],v[176:179],a[124:127],v[144:147]\n"
"v_add_f32 v92,v92,v52\n"
"v_add_f32 v93,v93,v53\n"
MF "v[152:155],v[176:179],a[128:131],v[152:155]\n"
"v_add_f32 v94,v94,v54\n"
"v_add_f32 v95,v95,v55\n"
MF "v[152:155],v[180:183],a[168:171],v[152:155]\n"
CV "v88,v88,v89\n"
CV "v89,v90,v91\n"
MF "v[152:155],v[184:187],a[208:211],v[152:155]\n"
"s_add_u32 s87,s90,14336\n"
CV "v90,v92,v93\n"
CV "v91,v94,v95\n"
MF "v[160:163],v[184:187],a[212:215],v[160:163]\n"
"buffer_store_dwordx4 v[88:91],v9,s[40:43],s87 offen sc1\n"
MF "v[160:163],v[180:183],a[172:175],v[160:163]\n"
MF "v[160:163],v[176:179],a[132:135],v[160:163]\n"
MF "v[168:171],v[176:179],a[136:139],v[168:171]\n"
MF "v[168:171],v[180:183],a[176:179],v[168:171]\n"
MF "v[168:171],v[184:187],a[216:219],v[168:171]\n"
"s_waitcnt vmcnt(20) lgkmcnt(0)\n"
"s_barrier\n"
MF "v[172:175],a[244:247],a[216:219],v[172:175]\n"
"s_nop 0\n"
TR "v[176:177],v0 offset:49152\n"
TR "v[178:179],v0 offset:51200\n"
MF "v[172:175],a[240:243],a[176:179],v[172:175]\n"
"s_nop 0\n"
TR "v[180:181],v0 offset:53248\n"
TR "v[182:183],v0 offset:55296\n"
MF "v[172:175],v[188:191],a[136:139],v[172:175]\n"
"s_nop 0\n"
TR "v[184:185],v0 offset:57344\n"
TR "v[186:187],v0 offset:59392\n"
MF "v[164:167],v[188:191],a[132:135],v[164:167]\n"
"buffer_load_dword v10,v12,s[40:43],s86 offen sc1\n"
MF "v[164:167],a[240:243],a[172:175],v[164:167]\n"
MF "v[164:167],a[244:247],a[212:215],v[164:167]\n"
MF "v[156:159],a[244:247],a[208:211],v[156:159]\n"
MF "v[156:159],a[240:243],a[168:171],v[156:159]\n"
MF "v[156:159],v[188:191],a[128:131],v[156:159]\n"
MF "v[148:151],v[188:191],a[124:127],v[148:151]\n"
MF "v[148:151],a[240:243],a[164:167],v[148:151]\n"
MF "v[148:151],a[244:247],a[204:207],v[148:151]\n"
MF "v[140:143],a[244:247],a[200:203],v[140:143]\n"
MF "v[140:143],a[240:243],a[160:163],v[140:143]\n"
MF "v[140:143],v[188:191],a[120:123],v[140:143]\n"
"s_waitcnt lgkmcnt(0)\n"
MF "v[96:99],v[176:179],a[20:23],v[96:99]\n"
"s_nop 0\n"
TR "v[188:189],v1 offset:49152\n"
TR "v[190:191],v1 offset:51200\n"
MF "v[96:99],v[180:183],a[60:63],v[96:99]\n"
"s_nop 0\n"
TR "a[240:241],v1 offset:53248\n"
TR "a[242:243],v1 offset:55296\n"
MF "v[96:99],v[184:187],a[100:103],v[96:99]\n"
"s_nop 0\n"
TR "a[244:245],v1 offset:57344\n"
TR "a[246:247],v1 offset:59392\n"
MF "v[104:107],v[184:187],a[104:107],v[104:107]\n"
MF "v[104:107],v[180:183],a[64:67],v[104:107]\n"
MF "v[104:107],v[176:179],a[24:27],v[104:107]\n"
"s_waitcnt vmcnt(16)\n"
MF "v[96:99],v[208:211],a[252:255],v[96:99]\n"
MF "v[104:107],v[212:215],a[252:255],v[104:107]\n"
MF "v[112:115],v[216:219],a[252:255],v[112:115]\n"
MF "v[120:123],v[220:223],a[252:255],v[120:123]\n"
MF "v[128:131],v[224:227],a[252:255],v[128:131]\n"
MF "v[100:103],v[208:211],a[248:251],v[100:103]\n"
MF "v[108:111],v[212:215],a[248:251],v[108:111]\n"
MF "v[116:119],v[216:219],a[248:251],v[116:119]\n"
MF "v[124:127],v[220:223],a[248:251],v[124:127]\n"
MF "v[132:135],v[224:227],a[248:251],v[132:135]\n"
MF "v[112:115],v[176:179],a[28:31],v[112:115]\n"
MF "v[112:115],v[180:183],a[68:71],v[112:115]\n"
MF "v[112:115],v[184:187],a[108:111],v[112:115]\n"
MF "v[120:123],v[184:187],a[112:115],v[120:123]\n"
MF "v[120:123],v[180:183],a[72:75],v[120:123]\n"
"s_bfe_u32 s87,s22,0x30014\n"
MF "v[120:123],v[176:179],a[32:35],v[120:123]\n"
"s_lshl_b32 s87,s87,7\n"
MF "v[128:131],v[176:179],a[36:39],v[128:131]\n"
"s_and_b32 s60,s56,1\n"
MF "v[128:131],v[180:183],a[76:79],v[128:131]\n"
"s_mul_i32 s60,s60,204800\n"
MF "v[128:131],v[184:187],a[116:119],v[128:131]\n"
"s_waitcnt lgkmcnt(0)\n"
MF "v[132:135],a[244:247],a[116:119],v[132:135]\n"
"s_add_u32 s87,s87,s60\n"
TR "v[176:177],v2 offset:49152\n"
TR "v[178:179],v2 offset:51200\n"
MF "v[132:135],a[240:243],a[76:79],v[132:135]\n"
"s_mov_b32 s71,204800\n"
TR "v[180:181],v2 offset:53248\n"
TR "v[182:183],v2 offset:55296\n"
MF "v[132:135],v[188:191],a[36:39],v[132:135]\n"
"s_mov_b32 s66,204800\n"
TR "v[184:185],v2 offset:57344\n"
TR "v[186:187],v2 offset:59392\n"
MF "v[124:127],v[188:191],a[32:35],v[124:127]\n"
"s_add_u32 s72,s78,2\n"
MF "v[124:127],a[240:243],a[72:75],v[124:127]\n"
"s_and_b32 s72,s72,15\n"
MF "v[124:127],a[244:247],a[112:115],v[124:127]\n"
"s_bfe_u32 s60,s22,0x10014\n"
MF "v[116:119],a[244:247],a[108:111],v[116:119]\n"
"s_lshl_b32 s60,s60,5\n"
MF "v[116:119],a[240:243],a[68:71],v[116:119]\n"
"v_xor_b32 v242,s60,v8\n"
MF "v[116:119],v[188:191],a[28:31],v[116:119]\n"
MF "v[108:111],v[188:191],a[24:27],v[108:111]\n"
"s_add_u32 s16,s78,6\n"
MF "v[108:111],a[240:243],a[64:67],v[108:111]\n"
"s_and_b32 s16,s16,15\n"
MF "v[108:111],a[244:247],a[104:107],v[108:111]\n"
"s_add_u32 s17,s78,4\n"
MF "v[100:103],a[244:247],a[100:103],v[100:103]\n"
"s_and_b32 s17,s17,15\n"
MF "v[100:103],a[240:243],a[60:63],v[100:103]\n"
MF "v[100:103],v[188:191],a[20:23],v[100:103]\n"
"s_waitcnt lgkmcnt(0)\n"
MF "v[136:139],v[176:179],a[20:23],v[136:139]\n"
"s_nop 0\n"
TR "v[188:189],v3 offset:49152\n"
TR "v[190:191],v3 offset:51200\n"
MF "v[136:139],v[180:183],a[60:63],v[136:139]\n"
"s_nop 0\n"
TR "a[240:241],v3 offset:53248\n"
TR "a[242:243],v3 offset:55296\n"
MF "v[136:139],v[184:187],a[100:103],v[136:139]\n"
"s_nop 0\n"
TR "a[244:245],v3 offset:57344\n"
TR "a[246:247],v3 offset:59392\n"
MF "v[144:147],v[184:187],a[104:107],v[144:147]\n"
MF "v[144:147],v[180:183],a[64:67],v[144:147]\n"
MF "v[144:147],v[176:179],a[24:27],v[144:147]\n"
MF "v[152:155],v[176:179],a[28:31],v[152:155]\n"
MF "v[152:155],v[180:183],a[68:71],v[152:155]\n"
MF "v[152:155],v[184:187],a[108:111],v[152:155]\n"
MF "v[160:163],v[184:187],a[112:115],v[160:163]\n"
MF "v[160:163],v[180:183],a[72:75],v[160:163]\n"
MF "v[160:163],v[176:179],a[32:35],v[160:163]\n"
MF "v[168:171],v[176:179],a[36:39],v[168:171]\n"
MF "v[168:171],v[180:183],a[76:79],v[168:171]\n"
MF "v[168:171],v[184:187],a[116:119],v[168:171]\n"
"s_waitcnt vmcnt(0) lgkmcnt(0)\n"
"s_barrier\n"
MF "v[172:175],a[244:247],a[116:119],v[172:175]\n"
"v_sub_u32 v10,v10,v11\n"
MF "v[172:175],a[240:243],a[76:79],v[172:175]\n"
"s_nop 0\n"
TR "v[176:177],v4\n"
TR "v[178:179],v4 offset:2048\n"
MF "v[172:175],v[188:191],a[36:39],v[172:175]\n"
"s_nop 0\n"
TR "v[180:181],v4 offset:4096\n"
TR "v[182:183],v4 offset:6144\n"
MF "v[164:167],v[188:191],a[32:35],v[164:167]\n"
"s_nop 0\n"
TR "v[184:185],v4 offset:8192\n"
TR "v[186:187],v4 offset:10240\n"
MF "v[164:167],a[240:243],a[72:75],v[164:167]\n"
"v_readlane_b32 s16,v10,s16\n"
MF "v[164:167],a[244:247],a[112:115],v[164:167]\n"
"s_cmp_lt_u32 s16,9\n"
"s_cselect_b32 s16,0xf0000000,0\n"
MF "v[156:159],a[244:247],a[108:111],v[156:159]\n"
"v_readlane_b32 s17,v10,s17\n"
MF "v[156:159],a[240:243],a[68:71],v[156:159]\n"
"s_cmp_lt_u32 s17,10\n"
"s_cselect_b32 s17,0xf0000000,0\n"
MF "v[156:159],v[188:191],a[28:31],v[156:159]\n"
"s_bfe_u32 s62,s22,0x30014\n"
MF "v[148:151],v[188:191],a[24:27],v[148:151]\n"
"s_add_u32 s62,s62,3\n"
"s_and_b32 s62,s62,7\n"
"s_mul_i32 s62,s62,204800\n"
MF "v[148:151],a[240:243],a[64:67],v[148:151]\n"
"s_add_u32 s62,s62,s73\n"
"s_add_u32 s62,s62,133120\n"
"s_or_b32 s62,s62,s16\n"
MF "v[148:151],a[244:247],a[104:107],v[148:151]\n"
LD "v[16:19],v9,s[40:43],s62 offen sc1\n"
MF "v[140:143],a[244:247],a[100:103],v[140:143]\n"
LD "v[24:27],v9,s[40:43],s62 offen offset:1024 sc1\n"
MF "v[140:143],a[240:243],a[60:63],v[140:143]\n"
MF "v[140:143],v[188:191],a[20:23],v[140:143]\n"
"s_waitcnt lgkmcnt(0)\n"
LD "v[32:35],v9,s[40:43],s62 offen offset:2048 sc1\n"
MF "v[96:99],v[176:179],a[140:143],v[96:99]\n"
"s_nop 0\n"
TR "v[188:189],v5\n"
TR "v[190:191],v5 offset:2048\n"
MF "v[96:99],v[180:183],a[180:183],v[96:99]\n"
"s_nop 0\n"
TR "a[240:241],v5 offset:4096\n"
TR "a[242:243],v5 offset:6144\n"
MF "v[96:99],v[184:187],a[220:223],v[96:99]\n"
"s_nop 0\n"
TR "a[244:245],v5 offset:8192\n"
TR "a[246:247],v5 offset:10240\n"
MF "v[104:107],v[184:187],a[224:227],v[104:107]\n"
LD "v[40:43],v9,s[40:43],s62 offen offset:3072 sc1\n"
MF "v[104:107],v[180:183],a[184:187],v[104:107]\n"
MF "v[104:107],v[176:179],a[144:147],v[104:107]\n"
"s_add_u32 s62,s62,4096\n"
MF "v[112:115],v[176:179],a[148:151],v[112:115]\n"
LD "v[48:51],v9,s[40:43],s62 offen sc1\n"
MF "v[112:115],v[180:183],a[188:191],v[112:115]\n"
"s_bfe_u32 s62,s22,0x30014\n"
"ds_write_addtid_b32 v96 offset:16384\n"
"ds_write_addtid_b32 v97 offset:16640\n"
MF "v[112:115],v[184:187],a[228:231],v[112:115]\n"
"s_add_u32 s62,s62,2\n"
"s_and_b32 s62,s62,7\n"
"s_mul_i32 s62,s62,204800\n"
MF "v[120:123],v[184:187],a[232:235],v[120:123]\n"
"s_add_u32 s62,s62,s73\n"
"s_add_u32 s62,s62,153600\n"
"s_or_b32 s62,s62,s17\n"
MF "v[120:123],v[180:183],a[192:195],v[120:123]\n"
LD "v[20:23],v9,s[40:43],s62 offen sc1\n"
MF "v[120:123],v[176:179],a[152:155],v[120:123]\n"
"s_nop 0\n"
"ds_write_addtid_b32 v98 offset:16896\n"
"ds_write_addtid_b32 v99 offset:17152\n"
MF "v[128:131],v[176:179],a[156:159],v[128:131]\n"
LD "v[28:31],v9,s[40:43],s62 offen offset:1024 sc1\n"
MF "v[128:131],v[180:183],a[196:199],v[128:131]\n"
"s_nop 0\n"
"ds_write_addtid_b32 v104 offset:18432\n"
"ds_write_addtid_b32 v105 offset:18688\n"
MF "v[128:131],v[184:187],a[236:239],v[128:131]\n"
"s_waitcnt lgkmcnt(6)\n"
LD "v[36:39],v9,s[40:43],s62 offen offset:2048 sc1\n"
MF "v[100:103],v[188:191],a[140:143],v[100:103]\n"
"s_nop 0\n"
TR "v[176:177],v6\n"
TR "v[178:179],v6 offset:2048\n"
MF "v[100:103],a[240:243],a[180:183],v[100:103]\n"
"s_nop 0\n"
TR "v[180:181],v6 offset:4096\n"
TR "v[182:183],v6 offset:6144\n"
MF "v[100:103],a[244:247],a[220:223],v[100:103]\n"
"s_nop 0\n"
TR "v[184:185],v6 offset:8192\n"
TR "v[186:187],v6 offset:10240\n"
MF "v[108:111],a[244:247],a[224:227],v[108:111]\n"
LD "v[44:47],v9,s[40:43],s62 offen offset:3072 sc1\n"
MF "v[108:111],a[240:243],a[184:187],v[108:111]\n"
"s_nop 0\n"
"ds_write_addtid_b32 v106 offset:18944\n"
"ds_write_addtid_b32 v107 offset:19200\n"
MF "v[108:111],v[188:191],a[144:147],v[108:111]\n"
"s_add_u32 s62,s62,4096\n"
"ds_write_addtid_b32 v112 offset:20480\n"
"ds_write_addtid_b32 v113 offset:20736\n"
MF "v[116:119],v[188:191],a[148:151],v[116:119]\n"
LD "v[52:55],v9,s[40:43],s62 offen sc1\n"
MF "v[116:119],a[240:243],a[188:191],v[116:119]\n"
"s_nop 0\n"
"ds_write_addtid_b32 v114 offset:20992\n"
"ds_write_addtid_b32 v115 offset:21248\n"
MF "v[116:119],a[244:247],a[228:231],v[116:119]\n"
"s_nop 0\n"
"ds_write_addtid_b32 v120 offset:22528\n"
"ds_write_addtid_b32 v121 offset:22784\n"
MF "v[124:127],a[244:247],a[232:235],v[124:127]\n"
"s_nop 0\n"
"ds_write_addtid_b32 v122 offset:23040\n"
"ds_write_addtid_b32 v123 offset:23296\n"
MF "v[124:127],a[240:243],a[192:195],v[124:127]\n"
"s_nop 0\n"
"ds_write_addtid_b32 v128 offset:24576\n"
"ds_write_addtid_b32 v129 offset:24832\n"
MF "v[124:127],v[188:191],a[152:155],v[124:127]\n"
"buffer_load_dword v10,v12,s[40:43],s86 offen sc1\n"
MF "v[132:135],v[188:191],a[156:159],v[132:135]\n"
"s_nop 0\n"
"ds_write_addtid_b32 v130 offset:25088\n"
"ds_write_addtid_b32 v131 offset:25344\n"
MF "v[132:135],a[240:243],a[196:199],v[132:135]\n"
"s_nop 0\n"
"ds_write_addtid_b32 v100 offset:17408\n"
"ds_write_addtid_b32 v101 offset:17664\n"
MF "v[132:135],a[244:247],a[236:239],v[132:135]\n"
"s_waitcnt lgkmcnt(15)\n"
"ds_write_addtid_b32 v102 offset:17920\n"
"ds_write_addtid_b32 v103 offset:18176\n"
MF "v[136:139],v[176:179],a[140:143],v[136:139]\n"
"s_nop 0\n"
TR "v[188:189],v7\n"
TR "v[190:191],v7 offset:2048\n"
MF "v[136:139],v[180:183],a[180:183],v[136:139]\n"
"s_nop 0\n"
TR "a[240:241],v7 offset:4096\n"
TR "a[242:243],v7 offset:6144\n"
MF "v[136:139],v[184:187],a[220:223],v[136:139]\n"
"s_nop 0\n"
TR "a[244:245],v7 offset:8192\n"
TR "a[246:247],v7 offset:10240\n"
MF "v[144:147],v[184:187],a[224:227],v[144:147]\n"
"s_nop 0\n"
"ds_write_addtid_b32 v108 offset:19456\n"
"ds_write_addtid_b32 v109 offset:19712\n"
MF "v[144:147],v[180:183],a[184:187],v[144:147]\n"
"s_nop 0\n"
"ds_write_addtid_b32 v110 offset:19968\n"
"ds_write_addtid_b32 v111 offset:20224\n"
MF "v[144:147],v[176:179],a[144:147],v[144:147]\n"
"s_nop 0\n"
"ds_write_addtid_b32 v116 offset:21504\n"
"ds_write_addtid_b32 v117 offset:21760\n"
MF "v[152:155],v[176:179],a[148:151],v[152:155]\n"
"s_nop 0\n"
"ds_write_addtid_b32 v118 offset:22016\n"
"ds_write_addtid_b32 v119 offset:22272\n"
MF "v[152:155],v[180:183],a[188:191],v[152:155]\n"
"ds_write_addtid_b32 v124 offset:23552\n"
MF "v[152:155],v[184:187],a[228:231],v[152:155]\n"
"ds_write_addtid_b32 v125 offset:23808\n"
MF "v[160:163],v[184:187],a[232:235],v[160:163]\n"
"ds_write_addtid_b32 v126 offset:24064\n"
MF "v[160:163],v[180:183],a[192:195],v[160:163]\n"
"ds_write_addtid_b32 v127 offset:24320\n"
MF "v[160:163],v[176:179],a[152:155],v[160:163]\n"
"ds_write_addtid_b32 v132 offset:25600\n"
MF "v[168:171],v[176:179],a[156:159],v[168:171]\n"
"ds_write_addtid_b32 v133 offset:25856\n"
MF "v[168:171],v[180:183],a[196:199],v[168:171]\n"
"ds_write_addtid_b32 v134 offset:26112\n"
MF "v[168:171],v[184:187],a[236:239],v[168:171]\n"
"s_waitcnt lgkmcnt(15)\n"
"ds_write_addtid_b32 v135 offset:26368\n"
MF "v[140:143],v[188:191],a[140:143],v[140:143]\n"
MF "v[140:143],a[240:243],a[180:183],v[140:143]\n"
MF "v[140:143],a[244:247],a[220:223],v[140:143]\n"
MF "v[148:151],a[244:247],a[224:227],v[148:151]\n"
MF "v[148:151],a[240:243],a[184:187],v[148:151]\n"
"s_waitcnt lgkmcnt(0)\n"
"s_barrier\n"
MF "v[148:151],v[188:191],a[144:147],v[148:151]\n"
"ds_read2st64_b32 v[56:57],v13 offset0:0 offset1:1\n"
MF "v[156:159],v[188:191],a[148:151],v[156:159]\n"
"ds_read2st64_b32 v[58:59],v13 offset0:2 offset1:3\n"
MF "v[156:159],a[240:243],a[188:191],v[156:159]\n"
"ds_read2st64_b32 v[60:61],v13 offset0:4 offset1:5\n"
MF "v[156:159],a[244:247],a[228:231],v[156:159]\n"
"ds_read2st64_b32 v[62:63],v13 offset0:6 offset1:7\n"
MF "v[164:167],a[244:247],a[232:235],v[164:167]\n"
"ds_read2st64_b32 v[64:65],v13 offset0:8 offset1:9\n"
MF "v[164:167],a[240:243],a[192:195],v[164:167]\n"
"ds_read2st64_b32 v[66:67],v13 offset0:10 offset1:11\n"
MF "v[164:167],v[188:191],a[152:155],v[164:167]\n"
"ds_read2st64_b32 v[68:69],v13 offset0:12 offset1:13\n"
MF "v[172:175],v[188:191],a[156:159],v[172:175]\n"
"ds_read2st64_b32 v[70:71],v13 offset0:14 offset1:15\n"
MF "v[172:175],a[240:243],a[196:199],v[172:175]\n"
"ds_read2st64_b32 v[72:73],v13 offset0:16 offset1:17\n"
MF "v[172:175],a[244:247],a[236:239],v[172:175]\n"
"ds_read2st64_b32 v[74:75],v13 offset0:18 offset1:19\n"
"ds_read2st64_b32 v[76:77],v13 offset0:20 offset1:21\n"
"ds_read2st64_b32 v[78:79],v13 offset0:22 offset1:23\n"
"ds_read2st64_b32 v[80:81],v13 offset0:24 offset1:25\n"
"ds_read2st64_b32 v[82:83],v13 offset0:26 offset1:27\n"
"ds_read2st64_b32 v[84:85],v13 offset0:28 offset1:29\n"
"ds_read2st64_b32 v[86:87],v13 offset0:30 offset1:31\n"
"ds_read2st64_b32 v[88:89],v13 offset0:32 offset1:33\n"
"ds_read2st64_b32 v[90:91],v13 offset0:34 offset1:35\n"
"ds_read2st64_b32 v[92:93],v13 offset0:36 offset1:37\n"
"ds_read2st64_b32 v[94:95],v13 offset0:38 offset1:39\n"
"s_waitcnt vmcnt(0)\n"
"v_sub_u32 v10,v10,v11\n"
"s_cmp_eq_u32 s16,0\n"
"s_cbranch_scc1 .Lhave03\n"
"s_add_u32 s59,s78,6\n"
"s_and_b32 s59,s59,15\n"
"s_nop 3\n"
".Lnf2:\n"
"v_readlane_b32 s58,v10,s59\n"
"s_cmp_lt_u32 s58,9\n"
"s_cbranch_scc1 .Lsp2\n"
".Lnb2:\n"
"s_bfe_u32 s62,s22,0x30014\n"
"s_add_u32 s62,s62,3\n"
"s_and_b32 s62,s62,7\n"
"s_mul_i32 s62,s62,204800\n"
"s_add_u32 s62,s62,s73\n"
"s_add_u32 s62,s62,133120\n"
LD "v[16:19],v9,s[40:43],s62 offen sc1\n"
LD "v[24:27],v9,s[40:43],s62 offen offset:1024 sc1\n"
LD "v[32:35],v9,s[40:43],s62 offen offset:2048 sc1\n"
LD "v[40:43],v9,s[40:43],s62 offen offset:3072 sc1\n"
"s_add_u32 s62,s62,4096\n"
LD "v[48:51],v9,s[40:43],s62 offen sc1\n"
".Lhave03:\n"
"s_cmp_eq_u32 s17,0\n"
"s_cbranch_scc1 .Lhave02\n"
"s_add_u32 s59,s78,4\n"
"s_and_b32 s59,s59,15\n"
"s_nop 3\n"
".Lnf3:\n"
"v_readlane_b32 s58,v10,s59\n"
"s_cmp_lt_u32 s58,10\n"
"s_cbranch_scc1 .Lsp3\n"
".Lnb3:\n"
"s_bfe_u32 s62,s22,0x30014\n"
"s_add_u32 s62,s62,2\n"
"s_and_b32 s62,s62,7\n"
"s_mul_i32 s62,s62,204800\n"
"s_add_u32 s62,s62,s73\n"
"s_add_u32 s62,s62,153600\n"
LD "v[20:23],v9,s[40:43],s62 offen sc1\n"
LD "v[28:31],v9,s[40:43],s62 offen offset:1024 sc1\n"
LD "v[36:39],v9,s[40:43],s62 offen offset:2048 sc1\n"
LD "v[44:47],v9,s[40:43],s62 offen offset:3072 sc1\n"
"s_add_u32 s62,s62,4096\n"
LD "v[52:55],v9,s[40:43],s62 offen sc1\n"
".Lhave02:\n"
".Lnf4:\n"
"v_readlane_b32 s58,v10,s72\n"
"s_cmp_lt_u32 s58,11\n"
"s_cbranch_scc1 .Lsp4\n"
".Lnb4:\n"
"s_bfe_u32 s62,s22,0x30014\n"
"s_add_u32 s62,s62,1\n"
"s_and_b32 s62,s62,7\n"
"s_mul_i32 s62,s62,204800\n"
"s_add_u32 s62,s62,s73\n"
"s_add_u32 s62,s62,174080\n"
LD "v[208:211],v9,s[40:43],s62 offen sc1\n"
LD "v[212:215],v9,s[40:43],s62 offen offset:1024 sc1\n"
LD "v[216:219],v9,s[40:43],s62 offen offset:2048 sc1\n"
LD "v[220:223],v9,s[40:43],s62 offen offset:3072 sc1\n"
"s_add_u32 s62,s62,4096\n"
LD "v[224:227],v9,s[40:43],s62 offen sc1\n"
"s_waitcnt lgkmcnt(15)\n"
"v_pk_add_f32 v[136:137],v[136:137],v[56:57]\n"
"v_pk_add_f32 v[138:139],v[138:139],v[58:59]\n"
"v_pk_add_f32 v[140:141],v[140:141],v[60:61]\n"
"v_pk_add_f32 v[142:143],v[142:143],v[62:63]\n"
"v_pk_add_f32 v[144:145],v[144:145],v[64:65]\n"
"s_waitcnt lgkmcnt(14)\n"
"v_pk_add_f32 v[146:147],v[146:147],v[66:67]\n"
"s_waitcnt lgkmcnt(13)\n"
"v_pk_add_f32 v[148:149],v[148:149],v[68:69]\n"
"s_waitcnt lgkmcnt(12)\n"
"v_pk_add_f32 v[150:151],v[150:151],v[70:71]\n"
"s_waitcnt lgkmcnt(11)\n"
"v_pk_add_f32 v[152:153],v[152:153],v[72:73]\n"
"s_waitcnt lgkmcnt(10)\n"
"v_pk_add_f32 v[154:155],v[154:155],v[74:75]\n"
"s_waitcnt lgkmcnt(9)\n"
"v_pk_add_f32 v[156:157],v[156:157],v[76:77]\n"
"s_waitcnt lgkmcnt(8)\n"
"v_pk_add_f32 v[158:159],v[158:159],v[78:79]\n"
"s_waitcnt lgkmcnt(7)\n"
"v_pk_add_f32 v[160:161],v[160:161],v[80:81]\n"
"s_waitcnt lgkmcnt(6)\n"
"v_pk_add_f32 v[162:163],v[162:163],v[82:83]\n"
"s_waitcnt lgkmcnt(5)\n"
"v_pk_add_f32 v[164:165],v[164:165],v[84:85]\n"
"s_waitcnt lgkmcnt(4)\n"
"v_pk_add_f32 v[166:167],v[166:167],v[86:87]\n"
"s_waitcnt lgkmcnt(3)\n"
"v_pk_add_f32 v[168:169],v[168:169],v[88:89]\n"
"s_waitcnt lgkmcnt(2)\n"
"v_pk_add_f32 v[170:171],v[170:171],v[90:91]\n"
"s_waitcnt lgkmcnt(1)\n"
"v_pk_add_f32 v[172:173],v[172:173],v[92:93]\n"
"s_waitcnt lgkmcnt(0)\n"
"v_pk_add_f32 v[174:175],v[174:175],v[94:95]\n"
"s_waitcnt vmcnt(5)\n"
"s_nop 0\n"
MF "v[140:143],v[16:19],a[252:255],v[140:143]\n"
MF "v[148:151],v[24:27],a[252:255],v[148:151]\n"
MF "v[156:159],v[32:35],a[252:255],v[156:159]\n"
MF "v[164:167],v[40:43],a[252:255],v[164:167]\n"
MF "v[172:175],v[48:51],a[252:255],v[172:175]\n"
MF "v[136:139],v[16:19],a[248:251],v[136:139]\n"
MF "v[144:147],v[24:27],a[248:251],v[144:147]\n"
MF "v[152:155],v[32:35],a[248:251],v[152:155]\n"
MF "v[160:163],v[40:43],a[248:251],v[160:163]\n"
MF "v[168:171],v[48:51],a[248:251],v[168:171]\n"
MF "v[136:139],v[20:23],a[252:255],v[136:139]\n"
MF "v[144:147],v[28:31],a[252:255],v[144:147]\n"
MF "v[152:155],v[36:39],a[252:255],v[152:155]\n"
MF "v[160:163],v[44:47],a[252:255],v[160:163]\n"
MF "v[168:171],v[52:55],a[252:255],v[168:171]\n"
MF "v[140:143],v[20:23],a[248:251],v[140:143]\n"
MF "v[148:151],v[28:31],a[248:251],v[148:151]\n"
MF "v[156:159],v[36:39],a[248:251],v[156:159]\n"
MF "v[164:167],v[44:47],a[248:251],v[164:167]\n"
MF "v[172:175],v[52:55],a[248:251],v[172:175]\n"
"s_waitcnt vmcnt(0)\n"
MF "v[140:143],v[208:211],a[252:255],v[140:143]\n"
MF "v[136:139],v[208:211],a[248:251],v[136:139]\n"
MF "v[148:151],v[212:215],a[252:255],v[148:151]\n"
MF "v[144:147],v[212:215],a[248:251],v[144:147]\n"
MF "v[156:159],v[216:219],a[252:255],v[156:159]\n"
"s_nop 4\n"
CV "v136,v136,v137\n"
CV "v137,v138,v139\n"
MF "v[152:155],v[216:219],a[248:251],v[152:155]\n"
CV "v138,v140,v141\n"
CV "v139,v142,v143\n"
MF "v[164:167],v[220:223],a[252:255],v[164:167]\n"
CV "v144,v144,v145\n"
CV "v145,v146,v147\n"
MF "v[160:163],v[220:223],a[248:251],v[160:163]\n"
CV "v146,v148,v149\n"
CV "v147,v150,v151\n"
"v_permlane16_swap_b32 v136,v138\n"
"v_permlane16_swap_b32 v137,v139\n"
"buffer_store_dwordx4 v[136:139],v242,s[44:47],s87 offen offset:64 sc1\n"
"s_add_u32 s87,s87,40960\n"
MF "v[172:175],v[224:227],a[252:255],v[172:175]\n"
CV "v152,v152,v153\n"
CV "v153,v154,v155\n"
MF "v[168:171],v[224:227],a[248:251],v[168:171]\n"
CV "v154,v156,v157\n"
CV "v155,v158,v159\n"
"v_permlane16_swap_b32 v144,v146\n"
"v_permlane16_swap_b32 v145,v147\n"
"buffer_store_dwordx4 v[144:147],v242,s[44:47],s87 offen offset:64 sc1\n"
"s_add_u32 s87,s87,40960\n"
CV "v160,v160,v161\n"
CV "v161,v162,v163\n"
CV "v162,v164,v165\n"
CV "v163,v166,v167\n"
"v_permlane16_swap_b32 v152,v154\n"
"v_permlane16_swap_b32 v153,v155\n"
"buffer_store_dwordx4 v[152:155],v242,s[44:47],s87 offen offset:64 sc1\n"
"s_add_u32 s87,s87,40960\n"
CV "v168,v168,v169\n"
CV "v169,v170,v171\n"
CV "v170,v172,v173\n"
CV "v171,v174,v175\n"
"v_permlane16_swap_b32 v160,v162\n"
"v_permlane16_swap_b32 v161,v163\n"
"buffer_store_dwordx4 v[160:163],v242,s[44:47],s87 offen offset:64 sc1\n"
"s_add_u32 s87,s87,40960\n"
"v_permlane16_swap_b32 v168,v170\n"
"v_permlane16_swap_b32 v169,v171\n"
"buffer_store_dwordx4 v[168:171],v242,s[44:47],s87 offen offset:64 sc1\n"
"s_add_u32 s87,s87,40960\n"
"s_branch .Ljobs\n"
".Lrole1:\n"
"s_cbranch_execz .Lipr3\n"
MF "v[16:19],a[120:123],v[176:179],v[252:255]\n"
"s_mov_b32 s65,128\n"
TR "v[188:189],v1 offset:24576\n"
AR "v220,a44\n"
MF "v[16:19],a[160:163],v[180:183],v[16:19]\n"
"s_mov_b32 s9,0x3f80\n"
TR "v[190:191],v1 offset:26624\n"
AR "v221,a45\n"
MF "v[16:19],a[200:203],v[184:187],v[16:19]\n"
AR "v222,a46\n"
AR "v223,a47\n"
MF "v[24:27],a[204:207],v[184:187],v[252:255]\n"
"s_bfe_u32 s58,s22,0x30014\n"
TR "a[240:241],v1 offset:28672\n"
AR "v224,a64\n"
MF "v[24:27],a[164:167],v[180:183],v[24:27]\n"
"s_add_u32 s61,s58,1\n"
TR "a[242:243],v1 offset:30720\n"
AR "v225,a65\n"
MF "v[24:27],a[124:127],v[176:179],v[24:27]\n"
"s_and_b32 s61,s61,7\n"
AR "v226,a66\n"
AR "v227,a67\n"
MF "v[32:35],a[128:131],v[176:179],v[252:255]\n"
"s_mov_b32 s64,9\n"
TR "a[244:245],v1 offset:32768\n"
"ds_read_b128 a[64:67],v236 offset:49152\n"
MF "v[32:35],a[168:171],v[180:183],v[32:35]\n"
"s_xor_b32 s67,s78,8\n"
TR "a[246:247],v1 offset:34816\n"
"v_fma_f32 v220,v220,v209,v215\n"
MF "v[32:35],a[208:211],v[184:187],v[32:35]\n"
"s_mov_b32 s71,204800\n"
"v_fma_f32 v221,v221,v209,v215\n"
"v_fma_f32 v222,v222,v209,v215\n"
MF "v[40:43],a[212:215],v[184:187],v[252:255]\n"
"s_mov_b32 s66,204800\n"
"v_fma_f32 v223,v223,v209,v215\n"
"v_fma_f32 v224,v224,v209,v215\n"
MF "v[40:43],a[172:175],v[180:183],v[40:43]\n"
"s_and_b32 s73,s56,1\n"
"v_fma_f32 v225,v225,v209,v215\n"
"v_fma_f32 v226,v226,v209,v215\n"
MF "v[40:43],a[132:135],v[176:179],v[40:43]\n"
"s_mul_i32 s73,s73,5120\n"
"v_fma_f32 v227,v227,v209,v215\n"
CV "v220,v220,v221\n"
MF "v[48:51],a[136:139],v[176:179],v[252:255]\n"
"s_add_u32 s73,s73,s93\n"
CV "v221,v222,v223\n"
CV "v222,v224,v225\n"
MF "v[48:51],a[176:179],v[180:183],v[48:51]\n"
"s_xor_b32 s58,s58,4\n"
CV "v223,v226,v227\n"
AW "a44,v220\n"
MF "v[48:51],a[216:219],v[184:187],v[48:51]\n"
"s_waitcnt lgkmcnt(0)\n"
AW "a45,v221\n"
AW "a46,v222\n"
MF "v[52:55],a[216:219],a[244:247],v[252:255]\n"
"s_mul_i32 s70,s58,204800\n"
TR "v[176:177],v2 offset:24576\n"
AW "a47,v223\n"
MF "v[52:55],a[176:179],a[240:243],v[52:55]\n"
"s_add_u32 s70,s70,s73\n"
TR "v[178:179],v2 offset:26624\n"
AR "v228,a84\n"
MF "v[52:55],a[136:139],v[188:191],v[52:55]\n"
"s_sub_u32 s70,s70,30720\n"
AR "v229,a85\n"
AR "v230,a86\n"
MF "v[44:47],a[132:135],v[188:191],v[252:255]\n"
"s_mov_b32 s85,1\n"
TR "v[180:181],v2 offset:28672\n"
AR "v231,a87\n"
MF "v[44:47],a[172:175],a[240:243],v[44:47]\n"
"s_mov_b32 s83,0xf0000000\n"
TR "v[182:183],v2 offset:30720\n"
AR "v232,a104\n"
MF "v[44:47],a[212:215],a[244:247],v[44:47]\n"
"s_mov_b32 s82,0xf0000000\n"
AR "v233,a105\n"
AR "v234,a106\n"
MF "v[36:39],a[208:211],a[244:247],v[252:255]\n"
"s_add_u32 s58,s79,73728\n"
TR "v[184:185],v2 offset:32768\n"
AR "v235,a107\n"
MF "v[36:39],a[168:171],a[240:243],v[36:39]\n"
"s_mov_b32 s90,s91\n"
TR "v[186:187],v2 offset:34816\n"
"ds_read_b128 a[104:107],v237 offset:4096\n"
MF "v[36:39],a[128:131],v[188:191],v[36:39]\n"
"s_add_u32 s91,s91,20480\n"
"v_fma_f32 v228,v228,v209,v215\n"
"v_fma_f32 v229,v229,v209,v215\n"
MF "v[28:31],a[124:127],v[188:191],v[252:255]\n"
"s_bfe_u32 s74,s23,0x70000\n"
"v_fma_f32 v230,v230,v209,v215\n"
"v_fma_f32 v231,v231,v209,v215\n"
MF "v[28:31],a[164:167],a[240:243],v[28:31]\n"
"s_lshl_b32 s74,s74,1\n"
"v_fma_f32 v232,v232,v209,v215\n"
"v_fma_f32 v233,v233,v209,v215\n"
MF "v[28:31],a[204:207],a[244:247],v[28:31]\n"
"s_add_u32 s74,s74,s56\n"
"v_fma_f32 v234,v234,v209,v215\n"
"v_fma_f32 v235,v235,v209,v215\n"
MF "v[20:23],a[200:203],a[244:247],v[252:255]\n"
"s_sub_u32 s74,s74,2\n"
CV "v228,v228,v229\n"
CV "v229,v230,v231\n"
MF "v[20:23],a[160:163],a[240:243],v[20:23]\n"
"s_mul_i32 s74,s74,30720\n"
CV "v230,v232,v233\n"
CV "v231,v234,v235\n"
MF "v[20:23],a[120:123],v[188:191],v[20:23]\n"
"s_waitcnt lgkmcnt(1)\n"
AW "a84,v228\n"
AW "a85,v229\n"
MF "v[56:59],a[120:123],v[176:179],v[252:255]\n"
"s_bfe_u32 s68,s23,0x30003\n"
TR "v[188:189],v3 offset:24576\n"
"v_add_u32 v198,59392,v192\n"
MF "v[56:59],a[160:163],v[180:183],v[56:59]\n"
"s_add_u32 s68,s68,7\n"
TR "v[190:191],v3 offset:26624\n"
"v_add_u32 v199,68608,v192\n"
MF "v[56:59],a[200:203],v[184:187],v[56:59]\n"
"s_lshl_b32 s68,s68,7\n"
"v_add_u32 v200,81920,v192\n"
"v_add_u32 v201,91136,v192\n"
MF "v[64:67],a[204:207],v[184:187],v[252:255]\n"
"s_and_b32 s68,s68,768\n"
TR "a[240:241],v3 offset:28672\n"
"v_add_u32 v202,100352,v192\n"
MF "v[64:67],a[164:167],v[180:183],v[64:67]\n"
TR "a[242:243],v3 offset:30720\n"
"v_add_u32 v203,109568,v192\n"
MF "v[64:67],a[124:127],v[176:179],v[64:67]\n"
AW "a86,v230\n"
AW "a87,v231\n"
MF "v[72:75],a[128:131],v[176:179],v[252:255]\n"
TR "a[244:245],v3 offset:32768\n"
AR "v220,a8\n"
MF "v[72:75],a[168:171],v[180:183],v[72:75]\n"
TR "a[246:247],v3 offset:34816\n"
AR "v221,a9\n"
MF "v[72:75],a[208:211],v[184:187],v[72:75]\n"
AR "v222,a10\n"
AR "v223,a11\n"
MF "v[80:83],a[212:215],v[184:187],v[252:255]\n"
AR "v224,a28\n"
AR "v225,a29\n"
MF "v[80:83],a[172:175],v[180:183],v[80:83]\n"
AR "v226,a30\n"
AR "v227,a31\n"
MF "v[80:83],a[132:135],v[176:179],v[80:83]\n"
"ds_read_b128 a[28:31],v236 offset:32768\n"
"v_fma_f32 v220,v220,v210,v216\n"
MF "v[88:91],a[136:139],v[176:179],v[252:255]\n"
"v_fma_f32 v221,v221,v210,v216\n"
"v_fma_f32 v222,v222,v210,v216\n"
MF "v[88:91],a[176:179],v[180:183],v[88:91]\n"
"v_fma_f32 v223,v223,v210,v216\n"
"v_fma_f32 v224,v224,v210,v216\n"
MF "v[88:91],a[216:219],v[184:187],v[88:91]\n"
"s_waitcnt vmcnt(0) lgkmcnt(0)\n"
"s_barrier\n"
MF "v[92:95],a[216:219],a[244:247],v[252:255]\n"
"s_add_u32 s89,s89,s65\n"
TR "v[176:177],v0\n"
"v_fma_f32 v225,v225,v210,v216\n"
MF "v[92:95],a[176:179],a[240:243],v[92:95]\n"
"s_add_u32 m0,s79,24576\n"
TR "v[178:179],v0 offset:2048\n"
"v_fma_f32 v226,v226,v210,v216\n"
MF "v[92:95],a[136:139],v[188:191],v[92:95]\n"
LD "v192,s[48:51],s89 offen lds\n"
MF "v[84:87],a[132:135],v[188:191],v[252:255]\n"
TR "v[180:181],v0 offset:4096\n"
"v_fma_f32 v227,v227,v210,v216\n"
MF "v[84:87],a[172:175],a[240:243],v[84:87]\n"
TR "v[182:183],v0 offset:6144\n"
CV "v220,v220,v221\n"
MF "v[84:87],a[212:215],a[244:247],v[84:87]\n"
LD "v193,s[48:51],s89 offen offset:1024 lds\n"
MF "v[76:79],a[208:211],a[244:247],v[252:255]\n"
TR "v[184:185],v0 offset:8192\n"
CV "v221,v222,v223\n"
MF "v[76:79],a[168:171],a[240:243],v[76:79]\n"
TR "v[186:187],v0 offset:10240\n"
CV "v222,v224,v225\n"
MF "v[76:79],a[128:131],v[188:191],v[76:79]\n"
TR "v[96:97],v0 offset:49152\n"
CV "v223,v226,v227\n"
MF "v[68:71],a[124:127],v[188:191],v[252:255]\n"
TR "v[98:99],v0 offset:51200\n"
AW "a8,v220\n"
MF "v[68:71],a[164:167],a[240:243],v[68:71]\n"
LD "v194,s[48:51],s89 offen offset:2048 lds\n"
MF "v[68:71],a[204:207],a[244:247],v[68:71]\n"
TR "v[100:101],v0 offset:53248\n"
AW "a9,v221\n"
MF "v[60:63],a[200:203],a[244:247],v[252:255]\n"
TR "v[102:103],v0 offset:55296\n"
AW "a10,v222\n"
MF "v[60:63],a[160:163],a[240:243],v[60:63]\n"
TR "v[104:105],v0 offset:57344\n"
AW "a11,v223\n"
MF "v[60:63],a[120:123],v[188:191],v[60:63]\n"
"s_waitcnt lgkmcnt(5)\n"
LD "v195,s[48:51],s89 offen offset:3072 lds\n"
MF "v[16:19],a[140:143],v[176:179],v[16:19]\n"
TR "v[188:189],v1\n"
AR "v228,a48\n"
MF "v[16:19],a[180:183],v[180:183],v[16:19]\n"
TR "v[190:191],v1 offset:2048\n"
AR "v229,a49\n"
MF "v[16:19],a[220:223],v[184:187],v[16:19]\n"
TR "v[106:107],v0 offset:59392\n"
AR "v230,a50\n"
MF "v[24:27],a[224:227],v[184:187],v[24:27]\n"
TR "a[240:241],v1 offset:4096\n"
AR "v231,a51\n"
MF "v[24:27],a[184:187],v[180:183],v[24:27]\n"
"s_add_u32 m0,s79,28672\n"
TR "a[242:243],v1 offset:6144\n"
AR "v232,a68\n"
MF "v[24:27],a[144:147],v[176:179],v[24:27]\n"
LD "v196,s[48:51],s89 offen lds\n"
MF "v[32:35],a[148:151],v[176:179],v[32:35]\n"
TR "a[244:245],v1 offset:8192\n"
AR "v233,a69\n"
MF "v[32:35],a[188:191],v[180:183],v[32:35]\n"
TR "a[246:247],v1 offset:10240\n"
AR "v234,a70\n"
MF "v[32:35],a[228:231],v[184:187],v[32:35]\n"
LD "v197,s[48:51],s89 offen offset:1024 lds\n"
MF "v[40:43],a[232:235],v[184:187],v[40:43]\n"
TR "v[108:109],v4\n"
AR "v235,a71\n"
MF "v[40:43],a[192:195],v[180:183],v[40:43]\n"
"s_nop 0\n"
TR "v[110:111],v4 offset:2048\n"
"ds_read_b128 a[68:71],v236 offset:53248\n"
MF "v[40:43],a[152:155],v[176:179],v[40:43]\n"
TR "v[112:113],v4 offset:4096\n"
"v_fma_f32 v228,v228,v210,v216\n"
MF "v[48:51],a[156:159],v[176:179],v[48:51]\n"
TR "v[114:115],v4 offset:6144\n"
"v_fma_f32 v229,v229,v210,v216\n"
MF "v[48:51],a[196:199],v[180:183],v[48:51]\n"
LD "v198,s[48:51],s89 offen offset:2048 lds\n"
MF "v[48:51],a[236:239],v[184:187],v[48:51]\n"
"s_waitcnt lgkmcnt(5)\n"
TR "v[116:117],v4 offset:8192\n"
"v_fma_f32 v230,v230,v210,v216\n"
MF "v[52:55],a[236:239],a[244:247],v[52:55]\n"
TR "v[176:177],v2\n"
"v_fma_f32 v231,v231,v210,v216\n"
MF "v[52:55],a[196:199],a[240:243],v[52:55]\n"
TR "v[178:179],v2 offset:2048\n"
"v_fma_f32 v232,v232,v210,v216\n"
MF "v[52:55],a[156:159],v[188:191],v[52:55]\n"
LD "v199,s[48:51],s89 offen offset:3072 lds\n"
MF "v[44:47],a[152:155],v[188:191],v[44:47]\n"
TR "v[180:181],v2 offset:4096\n"
"v_fma_f32 v233,v233,v210,v216\n"
MF "v[44:47],a[192:195],a[240:243],v[44:47]\n"
TR "v[182:183],v2 offset:6144\n"
"v_fma_f32 v234,v234,v210,v216\n"
MF "v[44:47],a[232:235],a[244:247],v[44:47]\n"
TR "v[118:119],v4 offset:10240\n"
"v_fma_f32 v235,v235,v210,v216\n"
MF "v[36:39],a[228:231],a[244:247],v[36:39]\n"
TR "v[184:185],v2 offset:8192\n"
CV "v228,v228,v229\n"
MF "v[36:39],a[188:191],a[240:243],v[36:39]\n"
"s_add_u32 m0,s79,32768\n"
TR "v[186:187],v2 offset:10240\n"
CV "v229,v230,v231\n"
MF "v[36:39],a[148:151],v[188:191],v[36:39]\n"
LD "v200,s[48:51],s89 offen lds\n"
MF "v[28:31],a[144:147],v[188:191],v[28:31]\n"
TR "v[120:121],v1 offset:49152\n"
CV "v230,v232,v233\n"
MF "v[28:31],a[184:187],a[240:243],v[28:31]\n"
TR "v[122:123],v1 offset:51200\n"
CV "v231,v234,v235\n"
MF "v[28:31],a[224:227],a[244:247],v[28:31]\n"
TR "v[124:125],v1 offset:53248\n"
AW "a48,v228\n"
MF "v[20:23],a[220:223],a[244:247],v[20:23]\n"
LD "v201,s[48:51],s89 offen offset:1024 lds\n"
MF "v[20:23],a[180:183],a[240:243],v[20:23]\n"
TR "v[126:127],v1 offset:55296\n"
AW "a49,v229\n"
MF "v[20:23],a[140:143],v[188:191],v[20:23]\n"
"s_waitcnt lgkmcnt(4)\n"
TR "v[128:129],v1 offset:57344\n"
AW "a50,v230\n"
MF "v[56:59],a[140:143],v[176:179],v[56:59]\n"
TR "v[188:189],v3\n"
AW "a51,v231\n"
MF "v[56:59],a[180:183],v[180:183],v[56:59]\n"
TR "v[190:191],v3 offset:2048\n"
AR "v220,a88\n"
MF "v[56:59],a[220:223],v[184:187],v[56:59]\n"
LD "v202,s[48:51],s89 offen offset:2048 lds\n"
MF "v[64:67],a[224:227],v[184:187],v[64:67]\n"
TR "a[240:241],v3 offset:4096\n"
AR "v221,a89\n"
MF "v[64:67],a[184:187],v[180:183],v[64:67]\n"
TR "a[242:243],v3 offset:6144\n"
AR "v222,a90\n"
MF "v[64:67],a[144:147],v[176:179],v[64:67]\n"
LD "v203,s[48:51],s89 offen offset:3072 lds\n"
MF "v[72:75],a[148:151],v[176:179],v[72:75]\n"
TR "a[244:245],v3 offset:8192\n"
AR "v223,a91\n"
MF "v[72:75],a[188:191],v[180:183],v[72:75]\n"
TR "a[246:247],v3 offset:10240\n"
AR "v224,a108\n"
MF "v[72:75],a[228:231],v[184:187],v[72:75]\n"
TR "v[130:131],v1 offset:59392\n"
AR "v225,a109\n"
MF "v[80:83],a[232:235],v[184:187],v[80:83]\n"
TR "v[132:133],v5\n"
AR "v226,a110\n"
MF "v[80:83],a[192:195],v[180:183],v[80:83]\n"
TR "v[134:135],v5 offset:2048\n"
AR "v227,a111\n"
MF "v[80:83],a[152:155],v[176:179],v[80:83]\n"
"s_nop 0\n"
TR "v[136:137],v5 offset:4096\n"
"ds_read_b128 a[108:111],v237 offset:8192\n"
MF "v[88:91],a[156:159],v[176:179],v[88:91]\n"
TR "v[138:139],v5 offset:6144\n"
"v_fma_f32 v220,v220,v210,v216\n"
MF "v[88:91],a[196:199],v[180:183],v[88:91]\n"
TR "v[140:141],v5 offset:8192\n"
"v_fma_f32 v221,v221,v210,v216\n"
MF "v[88:91],a[236:239],v[184:187],v[88:91]\n"
"s_waitcnt lgkmcnt(0)\n"
"s_barrier\n"
MF "v[92:95],a[236:239],a[244:247],v[92:95]\n"
TR "v[142:143],v5 offset:10240\n"
"v_fma_f32 v222,v222,v210,v216\n"
MF "v[92:95],a[196:199],a[240:243],v[92:95]\n"
TR "v[144:145],v2 offset:49152\n"
"v_fma_f32 v223,v223,v210,v216\n"
MF "v[92:95],a[156:159],v[188:191],v[92:95]\n"
TR "v[146:147],v2 offset:51200\n"
"v_fma_f32 v224,v224,v210,v216\n"
MF "v[84:87],a[152:155],v[188:191],v[84:87]\n"
TR "v[148:149],v2 offset:53248\n"
"v_fma_f32 v225,v225,v210,v216\n"
MF "v[84:87],a[192:195],a[240:243],v[84:87]\n"
TR "v[150:151],v2 offset:55296\n"
"v_fma_f32 v226,v226,v210,v216\n"
MF "v[84:87],a[232:235],a[244:247],v[84:87]\n"
TR "v[152:153],v2 offset:57344\n"
"v_fma_f32 v227,v227,v210,v216\n"
MF "v[76:79],a[228:231],a[244:247],v[76:79]\n"
TR "v[154:155],v2 offset:59392\n"
CV "v220,v220,v221\n"
MF "v[76:79],a[188:191],a[240:243],v[76:79]\n"
TR "v[156:157],v6\n"
CV "v221,v222,v223\n"
MF "v[76:79],a[148:151],v[188:191],v[76:79]\n"
TR "v[158:159],v6 offset:2048\n"
CV "v222,v224,v225\n"
MF "v[68:71],a[144:147],v[188:191],v[68:71]\n"
TR "v[160:161],v6 offset:4096\n"
CV "v223,v226,v227\n"
MF "v[68:71],a[184:187],a[240:243],v[68:71]\n"
TR "v[162:163],v6 offset:6144\n"
AW "a88,v220\n"
MF "v[68:71],a[224:227],a[244:247],v[68:71]\n"
TR "v[164:165],v6 offset:8192\n"
AW "a89,v221\n"
MF "v[60:63],a[220:223],a[244:247],v[60:63]\n"
TR "v[166:167],v6 offset:10240\n"
AW "a90,v222\n"
MF "v[60:63],a[180:183],a[240:243],v[60:63]\n"
TR "v[168:169],v3 offset:49152\n"
AW "a91,v223\n"
MF "v[60:63],a[140:143],v[188:191],v[60:63]\n"
TR "v[170:171],v3 offset:51200\n"
AR "v228,a12\n"
MF "v[16:19],a[0:3],v[96:99],v[16:19]\n"
TR "v[172:173],v3 offset:53248\n"
AR "v229,a13\n"
MF "v[16:19],a[40:43],v[100:103],v[16:19]\n"
TR "v[174:175],v3 offset:55296\n"
AR "v230,a14\n"
MF "v[16:19],a[80:83],v[104:107],v[16:19]\n"
TR "v[204:205],v3 offset:57344\n"
AR "v231,a15\n"
MF "v[16:19],a[20:23],v[108:111],v[16:19]\n"
TR "v[206:207],v3 offset:59392\n"
AR "v232,a32\n"
MF "v[16:19],a[60:63],v[112:115],v[16:19]\n"
TR "v[244:245],v7\n"
AR "v233,a33\n"
MF "v[16:19],a[100:103],v[116:119],v[16:19]\n"
"s_waitcnt lgkmcnt(15)\n"
TR "v[246:247],v7 offset:2048\n"
AR "v234,a34\n"
MF "v[20:23],a[100:103],v[140:143],v[20:23]\n"
TR "v[248:249],v7 offset:4096\n"
AR "v235,a35\n"
MF "v[20:23],a[60:63],v[136:139],v[20:23]\n"
"s_nop 0\n"
TR "v[250:251],v7 offset:6144\n"
"ds_read_b128 a[32:35],v236 offset:36864\n"
MF "v[20:23],a[20:23],v[132:135],v[20:23]\n"
TR "v[238:239],v7 offset:8192\n"
"v_fma_f32 v228,v228,v211,v217\n"
MF "v[20:23],a[80:83],v[128:131],v[20:23]\n"
TR "v[240:241],v7 offset:10240\n"
"v_fma_f32 v229,v229,v211,v217\n"
MF "v[20:23],a[40:43],v[124:127],v[20:23]\n"
"v_fma_f32 v230,v230,v211,v217\n"
"v_fma_f32 v231,v231,v211,v217\n"
MF "v[20:23],a[0:3],v[120:123],v[20:23]\n"
"s_waitcnt lgkmcnt(13)\n"
"v_fma_f32 v232,v232,v211,v217\n"
"v_fma_f32 v233,v233,v211,v217\n"
MF "v[56:59],a[0:3],v[144:147],v[56:59]\n"
"v_fma_f32 v234,v234,v211,v217\n"
"v_fma_f32 v235,v235,v211,v217\n"
MF "v[56:59],a[40:43],v[148:151],v[56:59]\n"
CV "v228,v228,v229\n"
CV "v229,v230,v231\n"
MF "v[56:59],a[80:83],v[152:155],v[56:59]\n"
CV "v230,v232,v233\n"
CV "v231,v234,v235\n"
MF "v[56:59],a[20:23],v[156:159],v[56:59]\n"
AW "a12,v228\n"
AW "a13,v229\n"
MF "v[56:59],a[60:63],v[160:163],v[56:59]\n"
AW "a14,v230\n"
AW "a15,v231\n"
MF "v[56:59],a[100:103],v[164:167],v[56:59]\n"
"s_waitcnt lgkmcnt(0)\n"
AR "v220,a52\n"
AR "v221,a53\n"
MF "v[60:63],a[100:103],v[238:241],v[60:63]\n"
AR "v222,a54\n"
AR "v223,a55\n"
MF "v[60:63],a[60:63],v[248:251],v[60:63]\n"
AR "v224,a72\n"
AR "v225,a73\n"
MF "v[60:63],a[20:23],v[244:247],v[60:63]\n"
AR "v226,a74\n"
AR "v227,a75\n"
MF "v[60:63],a[80:83],v[204:207],v[60:63]\n"
"ds_read_b128 a[72:75],v236 offset:57344\n"
"v_fma_f32 v220,v220,v211,v217\n"
MF "v[60:63],a[40:43],v[172:175],v[60:63]\n"
"v_fma_f32 v221,v221,v211,v217\n"
"v_fma_f32 v222,v222,v211,v217\n"
MF "v[60:63],a[0:3],v[168:171],v[60:63]\n"
"v_fma_f32 v223,v223,v211,v217\n"
"v_fma_f32 v224,v224,v211,v217\n"
MF "v[68:71],a[4:7],v[168:171],v[68:71]\n"
"v_fma_f32 v225,v225,v211,v217\n"
"v_fma_f32 v226,v226,v211,v217\n"
MF "v[68:71],a[44:47],v[172:175],v[68:71]\n"
"v_fma_f32 v227,v227,v211,v217\n"
CV "v220,v220,v221\n"
MF "v[68:71],a[84:87],v[204:207],v[68:71]\n"
CV "v221,v222,v223\n"
CV "v222,v224,v225\n"
MF "v[68:71],a[24:27],v[244:247],v[68:71]\n"
CV "v223,v226,v227\n"
AW "a52,v220\n"
MF "v[68:71],a[64:67],v[248:251],v[68:71]\n"
AW "a53,v221\n"
AW "a54,v222\n"
MF "v[68:71],a[104:107],v[238:241],v[68:71]\n"
AW "a55,v223\n"
AR "v228,a92\n"
MF "v[64:67],a[104:107],v[164:167],v[64:67]\n"
"s_waitcnt lgkmcnt(0)\n"
"s_barrier\n"
MF "v[64:67],a[64:67],v[160:163],v[64:67]\n"
AR "v229,a93\n"
AR "v230,a94\n"
MF "v[64:67],a[24:27],v[156:159],v[64:67]\n"
AR "v231,a95\n"
AR "v232,a112\n"
MF "v[64:67],a[84:87],v[152:155],v[64:67]\n"
AR "v233,a113\n"
AR "v234,a114\n"
MF "v[64:67],a[44:47],v[148:151],v[64:67]\n"
AR "v235,a115\n"
"v_fma_f32 v228,v228,v211,v217\n"
MF "v[64:67],a[4:7],v[144:147],v[64:67]\n"
"ds_read_b128 a[112:115],v237 offset:12288\n"
"v_fma_f32 v229,v229,v211,v217\n"
MF "v[28:31],a[4:7],v[120:123],v[28:31]\n"
"v_fma_f32 v230,v230,v211,v217\n"
"v_fma_f32 v231,v231,v211,v217\n"
MF "v[28:31],a[44:47],v[124:127],v[28:31]\n"
"v_fma_f32 v232,v232,v211,v217\n"
"v_fma_f32 v233,v233,v211,v217\n"
MF "v[28:31],a[84:87],v[128:131],v[28:31]\n"
"v_fma_f32 v234,v234,v211,v217\n"
"v_fma_f32 v235,v235,v211,v217\n"
MF "v[28:31],a[24:27],v[132:135],v[28:31]\n"
CV "v228,v228,v229\n"
CV "v229,v230,v231\n"
MF "v[28:31],a[64:67],v[136:139],v[28:31]\n"
CV "v230,v232,v233\n"
CV "v231,v234,v235\n"
MF "v[28:31],a[104:107],v[140:143],v[28:31]\n"
AW "a92,v228\n"
AW "a93,v229\n"
MF "v[24:27],a[104:107],v[116:119],v[24:27]\n"
AW "a94,v230\n"
AW "a95,v231\n"
MF "v[24:27],a[64:67],v[112:115],v[24:27]\n"
AR "v220,a16\n"
AR "v221,a17\n"
MF "v[24:27],a[24:27],v[108:111],v[24:27]\n"
AR "v222,a18\n"
AR "v223,a19\n"
MF "v[24:27],a[84:87],v[104:107],v[24:27]\n"
AR "v224,a36\n"
AR "v225,a37\n"
MF "v[24:27],a[44:47],v[100:103],v[24:27]\n"
AR "v226,a38\n"
AR "v227,a39\n"
MF "v[24:27],a[4:7],v[96:99],v[24:27]\n"
"ds_read_b128 a[36:39],v236 offset:40960\n"
"v_fma_f32 v220,v220,v212,v218\n"
MF "v[32:35],a[8:11],v[96:99],v[32:35]\n"
"v_fma_f32 v221,v221,v212,v218\n"
"v_fma_f32 v222,v222,v212,v218\n"
MF "v[32:35],a[48:51],v[100:103],v[32:35]\n"
"v_fma_f32 v223,v223,v212,v218\n"
"v_fma_f32 v224,v224,v212,v218\n"
MF "v[32:35],a[88:91],v[104:107],v[32:35]\n"
"v_fma_f32 v225,v225,v212,v218\n"
"v_fma_f32 v226,v226,v212,v218\n"
MF "v[32:35],a[28:31],v[108:111],v[32:35]\n"
"v_fma_f32 v227,v227,v212,v218\n"
CV "v220,v220,v221\n"
MF "v[32:35],a[68:71],v[112:115],v[32:35]\n"
CV "v221,v222,v223\n"
CV "v222,v224,v225\n"
MF "v[32:35],a[108:111],v[116:119],v[32:35]\n"
CV "v223,v226,v227\n"
AW "a16,v220\n"
MF "v[36:39],a[108:111],v[140:143],v[36:39]\n"
AW "a17,v221\n"
AW "a18,v222\n"
MF "v[36:39],a[68:71],v[136:139],v[36:39]\n"
AW "a19,v223\n"
AR "v228,a56\n"
MF "v[36:39],a[28:31],v[132:135],v[36:39]\n"
AR "v229,a57\n"
AR "v230,a58\n"
MF "v[36:39],a[88:91],v[128:131],v[36:39]\n"
AR "v231,a59\n"
AR "v232,a76\n"
MF "v[36:39],a[48:51],v[124:127],v[36:39]\n"
AR "v233,a77\n"
AR "v234,a78\n"
MF "v[36:39],a[8:11],v[120:123],v[36:39]\n"
AR "v235,a79\n"
"v_fma_f32 v228,v228,v212,v218\n"
MF "v[72:75],a[8:11],v[144:147],v[72:75]\n"
"ds_read_b128 a[76:79],v236 offset:61440\n"
"v_fma_f32 v229,v229,v212,v218\n"
MF "v[72:75],a[48:51],v[148:151],v[72:75]\n"
"v_fma_f32 v230,v230,v212,v218\n"
"v_fma_f32 v231,v231,v212,v218\n"
MF "v[72:75],a[88:91],v[152:155],v[72:75]\n"
"v_fma_f32 v232,v232,v212,v218\n"
"v_fma_f32 v233,v233,v212,v218\n"
MF "v[72:75],a[28:31],v[156:159],v[72:75]\n"
"v_fma_f32 v234,v234,v212,v218\n"
"v_fma_f32 v235,v235,v212,v218\n"
MF "v[72:75],a[68:71],v[160:163],v[72:75]\n"
CV "v228,v228,v229\n"
CV "v229,v230,v231\n"
MF "v[72:75],a[108:111],v[164:167],v[72:75]\n"
CV "v230,v232,v233\n"
CV "v231,v234,v235\n"
MF "v[76:79],a[108:111],v[238:241],v[76:79]\n"
AW "a56,v228\n"
AW "a57,v229\n"
MF "v[76:79],a[68:71],v[248:251],v[76:79]\n"
AW "a58,v230\n"
AW "a59,v231\n"
MF "v[76:79],a[28:31],v[244:247],v[76:79]\n"
AR "v220,a96\n"
AR "v221,a97\n"
MF "v[76:79],a[88:91],v[204:207],v[76:79]\n"
AR "v222,a98\n"
AR "v223,a99\n"
MF "v[76:79],a[48:51],v[172:175],v[76:79]\n"
AR "v224,a116\n"
AR "v225,a117\n"
MF "v[76:79],a[8:11],v[168:171],v[76:79]\n"
"s_waitcnt lgkmcnt(2)\n"
AR "v226,a118\n"
AR "v227,a119\n"
MF "v[84:87],a[12:15],v[168:171],v[84:87]\n"
"ds_read_b128 a[116:119],v237 offset:16384\n"
"v_fma_f32 v220,v220,v212,v218\n"
MF "v[84:87],a[52:55],v[172:175],v[84:87]\n"
"v_fma_f32 v221,v221,v212,v218\n"
"v_fma_f32 v222,v222,v212,v218\n"
MF "v[84:87],a[92:95],v[204:207],v[84:87]\n"
"v_fma_f32 v223,v223,v212,v218\n"
"v_fma_f32 v224,v224,v212,v218\n"
MF "v[84:87],a[32:35],v[244:247],v[84:87]\n"
"v_fma_f32 v225,v225,v212,v218\n"
"v_fma_f32 v226,v226,v212,v218\n"
MF "v[84:87],a[72:75],v[248:251],v[84:87]\n"
"v_fma_f32 v227,v227,v212,v218\n"
CV "v220,v220,v221\n"
MF "v[84:87],a[112:115],v[238:241],v[84:87]\n"
CV "v221,v222,v223\n"
CV "v222,v224,v225\n"
MF "v[80:83],a[112:115],v[164:167],v[80:83]\n"
CV "v223,v226,v227\n"
AW "a96,v220\n"
MF "v[80:83],a[72:75],v[160:163],v[80:83]\n"
AW "a97,v221\n"
AW "a98,v222\n"
MF "v[80:83],a[32:35],v[156:159],v[80:83]\n"
AW "a99,v223\n"
"v_mov_b32 v15,0\n"
MF "v[80:83],a[92:95],v[152:155],v[80:83]\n"
"v_lshrrev_b32 v242,5,v9\n"
"v_min_u32 v242,23,v242\n"
MF "v[80:83],a[52:55],v[148:151],v[80:83]\n"
"v_mul_u32_u24 v242,160,v242\n"
"v_and_or_b32 v242,v9,16,v242\n"
MF "v[80:83],a[12:15],v[144:147],v[80:83]\n"
"v_lshlrev_b32 v242,3,v242\n"
AR "v8,a252\n"
MF "v[44:47],a[12:15],v[120:123],v[44:47]\n"
AR "v11,a253\n"
"v_bfe_u32 v224,v9,5,3\n"
MF "v[44:47],a[52:55],v[124:127],v[44:47]\n"
"s_add_u32 s62,s74,s68\n"
"s_add_u32 s68,s68,256\n"
"s_and_b32 s68,s68,768\n"
MF "v[44:47],a[92:95],v[128:131],v[44:47]\n"
"buffer_load_dword v243,v242,s[48:51],s62 offen\n"
MF "v[44:47],a[32:35],v[132:135],v[44:47]\n"
"v_bfe_u32 v225,v9,8,2\n"
"v_lshlrev_b32 v225,1,v225\n"
MF "v[44:47],a[72:75],v[136:139],v[44:47]\n"
"v_mov_b32 v14,s76\n"
"s_add_u32 s76,s76,1\n"
MF "v[44:47],a[112:115],v[140:143],v[44:47]\n"
"buffer_store_dword v14,v15,s[40:43],s94 offen sc1\n"
MF "v[40:43],a[112:115],v[116:119],v[40:43]\n"
"v_and_b32 v226,16,v9\n"
"v_lshlrev_b32_e64 v226,v226,s9\n"
MF "v[40:43],a[72:75],v[112:115],v[40:43]\n"
"v_sub_u32 v224,v224,v225\n"
"v_cmp_eq_u32 vcc,0,v224\n"
MF "v[40:43],a[32:35],v[108:111],v[40:43]\n"
"s_nop 0\n"
"v_cndmask_b32 v220,0,v226,vcc\n"
"v_cmp_eq_u32 vcc,1,v224\n"
MF "v[40:43],a[92:95],v[104:107],v[40:43]\n"
"s_nop 0\n"
"v_cndmask_b32 v221,0,v226,vcc\n"
AW "a248,0\n"
MF "v[40:43],a[52:55],v[100:103],v[40:43]\n"
AW "a249,0\n"
AW "a250,v220\n"
MF "v[40:43],a[12:15],v[96:99],v[40:43]\n"
"s_waitcnt lgkmcnt(0)\n"
AW "a251,v221\n"
AW "a252,v220\n"
MF "v[48:51],a[16:19],v[96:99],v[48:51]\n"
AW "a253,v221\n"
AW "a254,0\n"
MF "v[48:51],a[56:59],v[100:103],v[48:51]\n"
AW "a255,0\n"
"v_bfe_u32 v12,v9,4,4\n"
MF "v[48:51],a[96:99],v[104:107],v[48:51]\n"
"v_lshlrev_b32 v12,2,v12\n"
"v_lshrrev_b32 v13,2,v9\n"
MF "v[48:51],a[36:39],v[108:111],v[48:51]\n"
"v_add_u32 v13,s58,v13\n"
"v_add_u32 v4,24576,v4\n"
MF "v[48:51],a[76:79],v[112:115],v[48:51]\n"
"v_add_u32 v5,24576,v5\n"
"v_add_u32 v6,24576,v6\n"
MF "v[48:51],a[116:119],v[116:119],v[48:51]\n"
"v_add_u32 v7,24576,v7\n"
MF "v[52:55],a[116:119],v[140:143],v[52:55]\n"
MF "v[52:55],a[76:79],v[136:139],v[52:55]\n"
MF "v[52:55],a[36:39],v[132:135],v[52:55]\n"
"s_waitcnt vmcnt(2) lgkmcnt(0)\n"
"s_barrier\n"
MF "v[52:55],a[96:99],v[128:131],v[52:55]\n"
TR "v[176:177],v0 offset:24576\n"
MF "v[52:55],a[56:59],v[124:127],v[52:55]\n"
TR "v[178:179],v0 offset:26624\n"
MF "v[52:55],a[16:19],v[120:123],v[52:55]\n"
MF "v[88:91],a[16:19],v[144:147],v[88:91]\n"
TR "v[180:181],v0 offset:28672\n"
MF "v[88:91],a[56:59],v[148:151],v[88:91]\n"
TR "v[182:183],v0 offset:30720\n"
MF "v[88:91],a[96:99],v[152:155],v[88:91]\n"
MF "v[88:91],a[36:39],v[156:159],v[88:91]\n"
TR "v[184:185],v0 offset:32768\n"
MF "v[88:91],a[76:79],v[160:163],v[88:91]\n"
TR "v[186:187],v0 offset:34816\n"
MF "v[88:91],a[116:119],v[164:167],v[88:91]\n"
MF "v[92:95],a[116:119],v[238:241],v[92:95]\n"
MF "v[92:95],a[76:79],v[248:251],v[92:95]\n"
MF "v[92:95],a[36:39],v[244:247],v[92:95]\n"
MF "v[92:95],a[96:99],v[204:207],v[92:95]\n"
MF "v[92:95],a[56:59],v[172:175],v[92:95]\n"
MF "v[92:95],a[16:19],v[168:171],v[92:95]\n"
"s_waitcnt lgkmcnt(0)\n"
".Lpl1:\n"
MF "v[96:99],a[0:3],v[176:179],v[252:255]\n"
TR "v[188:189],v1 offset:24576\n"
MF "v[96:99],a[40:43],v[180:183],v[96:99]\n"
"s_add_u32 s82,s82,s71\n"
TR "v[190:191],v1 offset:26624\n"
MF "v[96:99],a[80:83],v[184:187],v[96:99]\n"
LD "v[224:227],v9,s[40:43],s82 offen sc1\n"
MF "v[104:107],a[84:87],v[184:187],v[252:255]\n"
TR "a[240:241],v1 offset:28672\n"
MF "v[104:107],a[44:47],v[180:183],v[104:107]\n"
TR "a[242:243],v1 offset:30720\n"
MF "v[104:107],a[4:7],v[176:179],v[104:107]\n"
MF "v[112:115],a[8:11],v[176:179],v[252:255]\n"
TR "a[244:245],v1 offset:32768\n"
MF "v[112:115],a[48:51],v[180:183],v[112:115]\n"
TR "a[246:247],v1 offset:34816\n"
MF "v[112:115],a[88:91],v[184:187],v[112:115]\n"
MF "v[120:123],a[92:95],v[184:187],v[252:255]\n"
MF "v[120:123],a[52:55],v[180:183],v[120:123]\n"
"s_mul_i32 s95,s92,3277\n"
"s_lshr_b32 s95,s95,16\n"
"s_mul_i32 s59,s95,20\n"
MF "v[120:123],a[12:15],v[176:179],v[120:123]\n"
"s_sub_u32 s59,s92,s59\n"
"s_cmp_ge_u32 s59,10\n"
"s_cselect_b32 s96,4294557760,0\n"
MF "v[128:131],a[16:19],v[176:179],v[252:255]\n"
"s_add_u32 s82,s82,s66\n"
"s_add_u32 s96,s96,1024\n"
"s_mul_i32 s59,s59,40960\n"
MF "v[128:131],a[56:59],v[180:183],v[128:131]\n"
LD "v[228:231],v9,s[40:43],s82 offen sc1\n"
MF "v[128:131],a[96:99],v[184:187],v[128:131]\n"
"s_add_u32 s96,s96,s59\n"
"s_lshl_b32 s59,s95,7\n"
"s_waitcnt lgkmcnt(0)\n"
MF "v[132:135],a[96:99],a[244:247],v[252:255]\n"
"s_add_u32 s96,s96,s59\n"
"s_add_u32 s95,s95,3\n"
TR "v[176:177],v2 offset:24576\n"
MF "v[132:135],a[56:59],a[240:243],v[132:135]\n"
"s_cmp_ge_u32 s92,40\n"
"s_cselect_b32 s95,99,s95\n"
TR "v[178:179],v2 offset:26624\n"
MF "v[132:135],a[16:19],v[188:191],v[132:135]\n"
"s_cmp_ge_u32 s85,5\n"
"s_cselect_b32 s95,99,s95\n"
"s_cselect_b32 s72,s85,0\n"
MF "v[124:127],a[12:15],v[188:191],v[252:255]\n"
TR "v[180:181],v2 offset:28672\n"
MF "v[124:127],a[52:55],a[240:243],v[124:127]\n"
TR "v[182:183],v2 offset:30720\n"
MF "v[124:127],a[92:95],a[244:247],v[124:127]\n"
MF "v[116:119],a[88:91],a[244:247],v[252:255]\n"
TR "v[184:185],v2 offset:32768\n"
MF "v[116:119],a[48:51],a[240:243],v[116:119]\n"
"s_add_u32 s82,s82,s71\n"
TR "v[186:187],v2 offset:34816\n"
MF "v[116:119],a[8:11],v[188:191],v[116:119]\n"
LD "v[232:235],v9,s[40:43],s82 offen sc1\n"
MF "v[108:111],a[4:7],v[188:191],v[252:255]\n"
MF "v[108:111],a[44:47],a[240:243],v[108:111]\n"
MF "v[108:111],a[84:87],a[244:247],v[108:111]\n"
MF "v[100:103],a[80:83],a[244:247],v[252:255]\n"
MF "v[100:103],a[40:43],a[240:243],v[100:103]\n"
MF "v[100:103],a[0:3],v[188:191],v[100:103]\n"
"s_waitcnt lgkmcnt(0)\n"
MF "v[136:139],a[0:3],v[176:179],v[252:255]\n"
TR "v[188:189],v3 offset:24576\n"
MF "v[136:139],a[40:43],v[180:183],v[136:139]\n"
"s_add_u32 s82,s82,s71\n"
TR "v[190:191],v3 offset:26624\n"
MF "v[136:139],a[80:83],v[184:187],v[136:139]\n"
LD "v[236:239],v9,s[40:43],s82 offen sc1\n"
MF "v[144:147],a[84:87],v[184:187],v[252:255]\n"
TR "a[240:241],v3 offset:28672\n"
MF "v[144:147],a[44:47],v[180:183],v[144:147]\n"
TR "a[242:243],v3 offset:30720\n"
MF "v[144:147],a[4:7],v[176:179],v[144:147]\n"
MF "v[152:155],a[8:11],v[176:179],v[252:255]\n"
TR "a[244:245],v3 offset:32768\n"
MF "v[152:155],a[48:51],v[180:183],v[152:155]\n"
TR "a[246:247],v3 offset:34816\n"
MF "v[152:155],a[88:91],v[184:187],v[152:155]\n"
MF "v[160:163],a[92:95],v[184:187],v[252:255]\n"
MF "v[160:163],a[52:55],v[180:183],v[160:163]\n"
MF "v[160:163],a[12:15],v[176:179],v[160:163]\n"
MF "v[168:171],a[16:19],v[176:179],v[252:255]\n"
MF "v[168:171],a[56:59],v[180:183],v[168:171]\n"
MF "v[168:171],a[96:99],v[184:187],v[168:171]\n"
"s_waitcnt lgkmcnt(0)\n"
"s_barrier\n"
MF "v[172:175],a[96:99],a[244:247],v[252:255]\n"
TR "v[176:177],v0\n"
MF "v[172:175],a[56:59],a[240:243],v[172:175]\n"
TR "v[178:179],v0 offset:2048\n"
MF "v[172:175],a[16:19],v[188:191],v[172:175]\n"
"ds_read2st64_b32 v[192:193],v13 offset0:192 offset1:193\n"
MF "v[164:167],a[12:15],v[188:191],v[252:255]\n"
TR "v[180:181],v0 offset:4096\n"
MF "v[164:167],a[52:55],a[240:243],v[164:167]\n"
TR "v[182:183],v0 offset:6144\n"
MF "v[164:167],a[92:95],a[244:247],v[164:167]\n"
"ds_read2st64_b32 v[194:195],v13 offset0:194 offset1:195\n"
MF "v[156:159],a[88:91],a[244:247],v[252:255]\n"
TR "v[184:185],v0 offset:8192\n"
MF "v[156:159],a[48:51],a[240:243],v[156:159]\n"
TR "v[186:187],v0 offset:10240\n"
MF "v[156:159],a[8:11],v[188:191],v[156:159]\n"
"ds_read2st64_b32 v[196:197],v13 offset0:196 offset1:197\n"
MF "v[148:151],a[4:7],v[188:191],v[252:255]\n"
"ds_read2st64_b32 v[198:199],v13 offset0:198 offset1:199\n"
MF "v[148:151],a[44:47],a[240:243],v[148:151]\n"
"buffer_load_dword v10,v12,s[40:43],s86 offen sc1\n"
MF "v[148:151],a[84:87],a[244:247],v[148:151]\n"
MF "v[140:143],a[80:83],a[244:247],v[252:255]\n"
MF "v[140:143],a[40:43],a[240:243],v[140:143]\n"
MF "v[140:143],a[0:3],v[188:191],v[140:143]\n"
"s_waitcnt lgkmcnt(2)\n"
MF "v[96:99],a[120:123],v[176:179],v[96:99]\n"
"s_waitcnt lgkmcnt(0)\n"
TR "v[188:189],v1\n"
"v_add_f32 v16,v16,v192\n"
MF "v[96:99],a[160:163],v[180:183],v[96:99]\n"
TR "v[190:191],v1 offset:2048\n"
"v_add_f32 v17,v17,v193\n"
MF "v[96:99],a[200:203],v[184:187],v[96:99]\n"
"ds_read2st64_b32 v[200:201],v13 offset0:200 offset1:201\n"
"v_add_f32 v18,v18,v194\n"
"s_waitcnt vmcnt(9)\n"
"v_mov_b32 v14,s76\n"
"s_add_u32 s76,s76,1\n"
MF "v[104:107],a[204:207],v[184:187],v[104:107]\n"
TR "a[240:241],v1 offset:4096\n"
"v_add_f32 v19,v19,v195\n"
"buffer_store_dword v14,v15,s[40:43],s94 offen sc1\n"
MF "v[104:107],a[164:167],v[180:183],v[104:107]\n"
TR "a[242:243],v1 offset:6144\n"
"v_add_f32 v20,v20,v196\n"
MF "v[104:107],a[124:127],v[176:179],v[104:107]\n"
"ds_read2st64_b32 v[202:203],v13 offset0:202 offset1:203\n"
"v_add_f32 v21,v21,v197\n"
MF "v[112:115],a[128:131],v[176:179],v[112:115]\n"
TR "a[244:245],v1 offset:8192\n"
"v_add_f32 v22,v22,v198\n"
MF "v[112:115],a[168:171],v[180:183],v[112:115]\n"
TR "a[246:247],v1 offset:10240\n"
"v_add_f32 v23,v23,v199\n"
MF "v[112:115],a[208:211],v[184:187],v[112:115]\n"
"ds_read2st64_b32 v[204:205],v13 offset0:204 offset1:205\n"
CV "v16,v16,v17\n"
MF "v[120:123],a[212:215],v[184:187],v[120:123]\n"
"ds_read2st64_b32 v[206:207],v13 offset0:206 offset1:207\n"
CV "v17,v18,v19\n"
MF "v[120:123],a[172:175],v[180:183],v[120:123]\n"
"s_add_u32 s87,s90,0\n"
CV "v18,v20,v21\n"
CV "v19,v22,v23\n"
MF "v[120:123],a[132:135],v[176:179],v[120:123]\n"
MF "v[128:131],a[136:139],v[176:179],v[128:131]\n"
"buffer_store_dwordx4 v[16:19],v9,s[40:43],s87 offen sc1\n"
MF "v[128:131],a[176:179],v[180:183],v[128:131]\n"
MF "v[128:131],a[216:219],v[184:187],v[128:131]\n"
"s_waitcnt lgkmcnt(2)\n"
MF "v[132:135],a[216:219],a[244:247],v[132:135]\n"
"s_waitcnt lgkmcnt(0)\n"
TR "v[176:177],v2\n"
"v_add_f32 v24,v24,v200\n"
MF "v[132:135],a[176:179],a[240:243],v[132:135]\n"
TR "v[178:179],v2 offset:2048\n"
"v_add_f32 v25,v25,v201\n"
MF "v[132:135],a[136:139],v[188:191],v[132:135]\n"
"ds_read2st64_b32 v[192:193],v13 offset0:208 offset1:209\n"
"v_add_f32 v26,v26,v202\n"
MF "v[124:127],a[132:135],v[188:191],v[124:127]\n"
TR "v[180:181],v2 offset:4096\n"
"v_add_f32 v27,v27,v203\n"
MF "v[124:127],a[172:175],a[240:243],v[124:127]\n"
TR "v[182:183],v2 offset:6144\n"
"v_add_f32 v28,v28,v204\n"
MF "v[124:127],a[212:215],a[244:247],v[124:127]\n"
"ds_read2st64_b32 v[194:195],v13 offset0:210 offset1:211\n"
"v_add_f32 v29,v29,v205\n"
MF "v[116:119],a[208:211],a[244:247],v[116:119]\n"
TR "v[184:185],v2 offset:8192\n"
"v_add_f32 v30,v30,v206\n"
MF "v[116:119],a[168:171],a[240:243],v[116:119]\n"
TR "v[186:187],v2 offset:10240\n"
"v_add_f32 v31,v31,v207\n"
MF "v[116:119],a[128:131],v[188:191],v[116:119]\n"
"ds_read2st64_b32 v[196:197],v13 offset0:212 offset1:213\n"
CV "v24,v24,v25\n"
MF "v[108:111],a[124:127],v[188:191],v[108:111]\n"
"ds_read2st64_b32 v[198:199],v13 offset0:214 offset1:215\n"
CV "v25,v26,v27\n"
MF "v[108:111],a[164:167],a[240:243],v[108:111]\n"
"s_add_u32 s87,s90,1024\n"
CV "v26,v28,v29\n"
CV "v27,v30,v31\n"
MF "v[108:111],a[204:207],a[244:247],v[108:111]\n"
MF "v[100:103],a[200:203],a[244:247],v[100:103]\n"
"buffer_store_dwordx4 v[24:27],v9,s[40:43],s87 offen sc1\n"
MF "v[100:103],a[160:163],a[240:243],v[100:103]\n"
MF "v[100:103],a[120:123],v[188:191],v[100:103]\n"
"s_waitcnt lgkmcnt(2)\n"
MF "v[136:139],a[120:123],v[176:179],v[136:139]\n"
"s_waitcnt lgkmcnt(0)\n"
TR "v[188:189],v3\n"
"v_add_f32 v32,v32,v192\n"
MF "v[136:139],a[160:163],v[180:183],v[136:139]\n"
TR "v[190:191],v3 offset:2048\n"
"v_add_f32 v33,v33,v193\n"
MF "v[136:139],a[200:203],v[184:187],v[136:139]\n"
"ds_read2st64_b32 v[200:201],v13 offset0:216 offset1:217\n"
"v_add_f32 v34,v34,v194\n"
MF "v[144:147],a[204:207],v[184:187],v[144:147]\n"
TR "a[240:241],v3 offset:4096\n"
"v_add_f32 v35,v35,v195\n"
MF "v[144:147],a[164:167],v[180:183],v[144:147]\n"
TR "a[242:243],v3 offset:6144\n"
"v_add_f32 v36,v36,v196\n"
MF "v[144:147],a[124:127],v[176:179],v[144:147]\n"
"ds_read2st64_b32 v[202:203],v13 offset0:218 offset1:219\n"
"v_add_f32 v37,v37,v197\n"
MF "v[152:155],a[128:131],v[176:179],v[152:155]\n"
TR "a[244:245],v3 offset:8192\n"
"v_add_f32 v38,v38,v198\n"
MF "v[152:155],a[168:171],v[180:183],v[152:155]\n"
TR "a[246:247],v3 offset:10240\n"
"v_add_f32 v39,v39,v199\n"
MF "v[152:155],a[208:211],v[184:187],v[152:155]\n"
"ds_read2st64_b32 v[204:205],v13 offset0:220 offset1:221\n"
CV "v32,v32,v33\n"
MF "v[160:163],a[212:215],v[184:187],v[160:163]\n"
"ds_read2st64_b32 v[206:207],v13 offset0:222 offset1:223\n"
CV "v33,v34,v35\n"
MF "v[160:163],a[172:175],v[180:183],v[160:163]\n"
"s_add_u32 s87,s90,2048\n"
CV "v34,v36,v37\n"
CV "v35,v38,v39\n"
MF "v[160:163],a[132:135],v[176:179],v[160:163]\n"
MF "v[168:171],a[136:139],v[176:179],v[168:171]\n"
"buffer_store_dwordx4 v[32:35],v9,s[40:43],s87 offen sc1\n"
MF "v[168:171],a[176:179],v[180:183],v[168:171]\n"
MF "v[168:171],a[216:219],v[184:187],v[168:171]\n"
"s_waitcnt lgkmcnt(0)\n"
"s_barrier\n"
MF "v[172:175],a[216:219],a[244:247],v[172:175]\n"
TR "v[176:177],v0 offset:49152\n"
"v_add_f32 v40,v40,v200\n"
MF "v[172:175],a[176:179],a[240:243],v[172:175]\n"
TR "v[178:179],v0 offset:51200\n"
"v_add_f32 v41,v41,v201\n"
MF "v[172:175],a[136:139],v[188:191],v[172:175]\n"
"ds_read2st64_b32 v[192:193],v13 offset0:224 offset1:225\n"
"v_add_f32 v42,v42,v202\n"
MF "v[164:167],a[132:135],v[188:191],v[164:167]\n"
TR "v[180:181],v0 offset:53248\n"
"v_add_f32 v43,v43,v203\n"
MF "v[164:167],a[172:175],a[240:243],v[164:167]\n"
TR "v[182:183],v0 offset:55296\n"
"v_add_f32 v44,v44,v204\n"
MF "v[164:167],a[212:215],a[244:247],v[164:167]\n"
"ds_read2st64_b32 v[194:195],v13 offset0:226 offset1:227\n"
"v_add_f32 v45,v45,v205\n"
MF "v[156:159],a[208:211],a[244:247],v[156:159]\n"
TR "v[184:185],v0 offset:57344\n"
"v_add_f32 v46,v46,v206\n"
MF "v[156:159],a[168:171],a[240:243],v[156:159]\n"
TR "v[186:187],v0 offset:59392\n"
"v_add_f32 v47,v47,v207\n"
MF "v[156:159],a[128:131],v[188:191],v[156:159]\n"
"ds_read2st64_b32 v[196:197],v13 offset0:228 offset1:229\n"
CV "v40,v40,v41\n"
MF "v[148:151],a[124:127],v[188:191],v[148:151]\n"
"ds_read2st64_b32 v[198:199],v13 offset0:230 offset1:231\n"
CV "v41,v42,v43\n"
MF "v[148:151],a[164:167],a[240:243],v[148:151]\n"
"s_add_u32 s87,s90,3072\n"
CV "v42,v44,v45\n"
CV "v43,v46,v47\n"
MF "v[148:151],a[204:207],a[244:247],v[148:151]\n"
MF "v[140:143],a[200:203],a[244:247],v[140:143]\n"
"buffer_store_dwordx4 v[40:43],v9,s[40:43],s87 offen sc1\n"
MF "v[140:143],a[160:163],a[240:243],v[140:143]\n"
MF "v[140:143],a[120:123],v[188:191],v[140:143]\n"
"s_waitcnt lgkmcnt(2)\n"
MF "v[96:99],a[20:23],v[176:179],v[96:99]\n"
"s_waitcnt lgkmcnt(0)\n"
TR "v[188:189],v1 offset:49152\n"
"v_add_f32 v48,v48,v192\n"
MF "v[96:99],a[60:63],v[180:183],v[96:99]\n"
TR "v[190:191],v1 offset:51200\n"
"v_add_f32 v49,v49,v193\n"
MF "v[96:99],a[100:103],v[184:187],v[96:99]\n"
"ds_read2st64_b32 v[200:201],v13 offset0:0 offset1:1\n"
"v_add_f32 v50,v50,v194\n"
MF "v[104:107],a[104:107],v[184:187],v[104:107]\n"
TR "a[240:241],v1 offset:53248\n"
"v_add_f32 v51,v51,v195\n"
MF "v[104:107],a[64:67],v[180:183],v[104:107]\n"
TR "a[242:243],v1 offset:55296\n"
"v_add_f32 v52,v52,v196\n"
MF "v[104:107],a[24:27],v[176:179],v[104:107]\n"
"ds_read2st64_b32 v[202:203],v13 offset0:2 offset1:3\n"
"v_add_f32 v53,v53,v197\n"
MF "v[112:115],a[28:31],v[176:179],v[112:115]\n"
TR "a[244:245],v1 offset:57344\n"
"v_add_f32 v54,v54,v198\n"
MF "v[112:115],a[68:71],v[180:183],v[112:115]\n"
TR "a[246:247],v1 offset:59392\n"
"v_add_f32 v55,v55,v199\n"
MF "v[112:115],a[108:111],v[184:187],v[112:115]\n"
"ds_read2st64_b32 v[204:205],v13 offset0:4 offset1:5\n"
CV "v48,v48,v49\n"
MF "v[120:123],a[112:115],v[184:187],v[120:123]\n"
"ds_read2st64_b32 v[206:207],v13 offset0:6 offset1:7\n"
CV "v49,v50,v51\n"
MF "v[120:123],a[72:75],v[180:183],v[120:123]\n"
"s_add_u32 s87,s90,4096\n"
CV "v50,v52,v53\n"
CV "v51,v54,v55\n"
MF "v[120:123],a[32:35],v[176:179],v[120:123]\n"
MF "v[128:131],a[36:39],v[176:179],v[128:131]\n"
"buffer_store_dwordx4 v[48:51],v9,s[40:43],s87 offen sc1\n"
MF "v[128:131],a[76:79],v[180:183],v[128:131]\n"
MF "v[128:131],a[116:119],v[184:187],v[128:131]\n"
"s_waitcnt lgkmcnt(2)\n"
MF "v[132:135],a[116:119],a[244:247],v[132:135]\n"
"s_waitcnt lgkmcnt(0)\n"
TR "v[176:177],v2 offset:49152\n"
"v_add_f32 v56,v56,v200\n"
MF "v[132:135],a[76:79],a[240:243],v[132:135]\n"
TR "v[178:179],v2 offset:51200\n"
"v_add_f32 v57,v57,v201\n"
MF "v[132:135],a[36:39],v[188:191],v[132:135]\n"
"ds_read2st64_b32 v[192:193],v13 offset0:8 offset1:9\n"
"v_add_f32 v58,v58,v202\n"
MF "v[124:127],a[32:35],v[188:191],v[124:127]\n"
TR "v[180:181],v2 offset:53248\n"
"v_add_f32 v59,v59,v203\n"
MF "v[124:127],a[72:75],a[240:243],v[124:127]\n"
TR "v[182:183],v2 offset:55296\n"
"v_add_f32 v60,v60,v204\n"
MF "v[124:127],a[112:115],a[244:247],v[124:127]\n"
"ds_read2st64_b32 v[194:195],v13 offset0:10 offset1:11\n"
"v_add_f32 v61,v61,v205\n"
MF "v[116:119],a[108:111],a[244:247],v[116:119]\n"
TR "v[184:185],v2 offset:57344\n"
"v_add_f32 v62,v62,v206\n"
MF "v[116:119],a[68:71],a[240:243],v[116:119]\n"
TR "v[186:187],v2 offset:59392\n"
"v_add_f32 v63,v63,v207\n"
MF "v[116:119],a[28:31],v[188:191],v[116:119]\n"
"ds_read2st64_b32 v[196:197],v13 offset0:12 offset1:13\n"
CV "v56,v56,v57\n"
MF "v[108:111],a[24:27],v[188:191],v[108:111]\n"
"ds_read2st64_b32 v[198:199],v13 offset0:14 offset1:15\n"
CV "v57,v58,v59\n"
MF "v[108:111],a[64:67],a[240:243],v[108:111]\n"
"s_add_u32 s87,s90,10240\n"
CV "v58,v60,v61\n"
CV "v59,v62,v63\n"
MF "v[108:111],a[104:107],a[244:247],v[108:111]\n"
MF "v[100:103],a[100:103],a[244:247],v[100:103]\n"
"buffer_store_dwordx4 v[56:59],v9,s[40:43],s87 offen sc1\n"
MF "v[100:103],a[60:63],a[240:243],v[100:103]\n"
"s_waitcnt vmcnt(7)\n"
"s_cmp_eq_u32 s83,0xf0000000\n"
MF "v[100:103],a[20:23],v[188:191],v[100:103]\n"
"s_cbranch_scc1 .Ljs13\n"
"s_cmp_eq_u32 s83,0xf0000010\n"
"s_cbranch_scc1 .Ljc13\n"
MF "v[248:251],v[210:213],a[250:253],0\n"
"v_mov_b32 v240,v208\n"
"v_mov_b32 v241,v209\n"
MF "v[244:247],v[214:217],a[250:253],0\n"
MF "v[248:251],v[218:221],a[250:253],v[248:251]\n"
MF "v[244:247],v[222:225],a[250:253],v[244:247]\n"
MF "v[248:251],v[226:229],a[250:253],v[248:251]\n"
MF "v[244:247],v[230:233],a[250:253],v[244:247]\n"
MF "v[248:251],v[234:237],a[250:253],v[248:251]\n"
MF "v[244:247],v[238:241],a[250:253],v[244:247]\n"
"s_branch .Ljs13\n"
".Ljc13:\n"
MF "v[136:139],a[252:255],v[208:211],v[136:139]\n"
MF "v[144:147],a[252:255],v[212:215],v[144:147]\n"
MF "v[152:155],a[252:255],v[216:219],v[152:155]\n"
MF "v[160:163],a[252:255],v[220:223],v[160:163]\n"
MF "v[168:171],a[252:255],v[224:227],v[168:171]\n"
MF "v[140:143],a[248:251],v[208:211],v[140:143]\n"
MF "v[148:151],a[248:251],v[212:215],v[148:151]\n"
MF "v[156:159],a[248:251],v[216:219],v[156:159]\n"
MF "v[164:167],a[248:251],v[220:223],v[164:167]\n"
MF "v[172:175],a[248:251],v[224:227],v[172:175]\n"
".Ljs13:\n"
"s_waitcnt lgkmcnt(2)\n"
MF "v[136:139],a[20:23],v[176:179],v[136:139]\n"
"s_waitcnt lgkmcnt(0)\n"
TR "v[188:189],v3 offset:49152\n"
"v_add_f32 v64,v64,v192\n"
MF "v[136:139],a[60:63],v[180:183],v[136:139]\n"
TR "v[190:191],v3 offset:51200\n"
"v_add_f32 v65,v65,v193\n"
MF "v[136:139],a[100:103],v[184:187],v[136:139]\n"
"ds_read2st64_b32 v[200:201],v13 offset0:16 offset1:17\n"
"v_add_f32 v66,v66,v194\n"
MF "v[144:147],a[104:107],v[184:187],v[144:147]\n"
TR "a[240:241],v3 offset:53248\n"
"v_add_f32 v67,v67,v195\n"
MF "v[144:147],a[64:67],v[180:183],v[144:147]\n"
TR "a[242:243],v3 offset:55296\n"
"v_add_f32 v68,v68,v196\n"
MF "v[144:147],a[24:27],v[176:179],v[144:147]\n"
"ds_read2st64_b32 v[202:203],v13 offset0:18 offset1:19\n"
"v_add_f32 v69,v69,v197\n"
MF "v[152:155],a[28:31],v[176:179],v[152:155]\n"
TR "a[244:245],v3 offset:57344\n"
"v_add_f32 v70,v70,v198\n"
MF "v[152:155],a[68:71],v[180:183],v[152:155]\n"
TR "a[246:247],v3 offset:59392\n"
"v_add_f32 v71,v71,v199\n"
MF "v[152:155],a[108:111],v[184:187],v[152:155]\n"
"ds_read2st64_b32 v[204:205],v13 offset0:20 offset1:21\n"
CV "v64,v64,v65\n"
MF "v[160:163],a[112:115],v[184:187],v[160:163]\n"
"ds_read2st64_b32 v[206:207],v13 offset0:22 offset1:23\n"
CV "v65,v66,v67\n"
MF "v[160:163],a[72:75],v[180:183],v[160:163]\n"
"s_add_u32 s87,s90,11264\n"
CV "v66,v68,v69\n"
CV "v67,v70,v71\n"
MF "v[160:163],a[32:35],v[176:179],v[160:163]\n"
"s_lshl_b32 s82,s92,10\n"
"v_sub_u32 v10,v10,v11\n"
"v_cmp_gt_u32 vcc,s95,v10\n"
MF "v[168:171],a[36:39],v[176:179],v[168:171]\n"
"buffer_store_dwordx4 v[64:67],v9,s[40:43],s87 offen sc1\n"
MF "v[168:171],a[76:79],v[180:183],v[168:171]\n"
"s_add_u32 s82,s82,s93\n"
CV "v244,v244,v245\n"
CV "v245,v246,v247\n"
MF "v[168:171],a[116:119],v[184:187],v[168:171]\n"
"s_waitcnt lgkmcnt(0)\n"
"s_barrier\n"
MF "v[172:175],a[116:119],a[244:247],v[172:175]\n"
TR "v[176:177],v4\n"
"v_add_f32 v72,v72,v200\n"
MF "v[172:175],a[76:79],a[240:243],v[172:175]\n"
TR "v[178:179],v4 offset:2048\n"
"v_add_f32 v73,v73,v201\n"
MF "v[172:175],a[36:39],v[188:191],v[172:175]\n"
"ds_read2st64_b32 v[192:193],v13 offset0:24 offset1:25\n"
"v_add_f32 v74,v74,v202\n"
MF "v[164:167],a[32:35],v[188:191],v[164:167]\n"
TR "v[180:181],v4 offset:4096\n"
"v_add_f32 v75,v75,v203\n"
MF "v[164:167],a[72:75],a[240:243],v[164:167]\n"
TR "v[182:183],v4 offset:6144\n"
"v_add_f32 v76,v76,v204\n"
MF "v[164:167],a[112:115],a[244:247],v[164:167]\n"
"ds_read2st64_b32 v[194:195],v13 offset0:26 offset1:27\n"
"v_add_f32 v77,v77,v205\n"
MF "v[156:159],a[108:111],a[244:247],v[156:159]\n"
TR "v[184:185],v4 offset:8192\n"
"v_add_f32 v78,v78,v206\n"
MF "v[156:159],a[68:71],a[240:243],v[156:159]\n"
TR "v[186:187],v4 offset:10240\n"
"v_add_f32 v79,v79,v207\n"
MF "v[156:159],a[28:31],v[188:191],v[156:159]\n"
"ds_read2st64_b32 v[196:197],v13 offset0:28 offset1:29\n"
CV "v72,v72,v73\n"
MF "v[148:151],a[24:27],v[188:191],v[148:151]\n"
"ds_read2st64_b32 v[198:199],v13 offset0:30 offset1:31\n"
CV "v73,v74,v75\n"
MF "v[148:151],a[64:67],a[240:243],v[148:151]\n"
"s_add_u32 s87,s90,12288\n"
CV "v74,v76,v77\n"
CV "v75,v78,v79\n"
MF "v[148:151],a[104:107],a[244:247],v[148:151]\n"
"s_cmp_eq_u64 vcc,0\n"
"s_cselect_b32 s82,s82,0xf0000000\n"
"s_cselect_b32 s84,s96,0xf0000000\n"
"s_cselect_b32 s59,32,0\n"
MF "v[140:143],a[100:103],a[244:247],v[140:143]\n"
"buffer_store_dwordx4 v[72:75],v9,s[40:43],s87 offen sc1\n"
MF "v[140:143],a[60:63],a[240:243],v[140:143]\n"
"s_add_u32 s92,s92,s59\n"
".Lnf5:\n"
"v_readlane_b32 s58,v10,s67\n"
"s_cmp_lt_u32 s58,s72\n"
"s_cbranch_scc1 .Lsp5\n"
".Lnb5:\n"
MF "v[140:143],a[20:23],v[188:191],v[140:143]\n"
"s_waitcnt lgkmcnt(2)\n"
CV "v246,v248,v249\n"
CV "v247,v250,v251\n"
MF "v[96:99],a[140:143],v[176:179],v[96:99]\n"
"s_waitcnt lgkmcnt(0)\n"
TR "v[188:189],v5\n"
"v_add_f32 v80,v80,v192\n"
MF "v[96:99],a[180:183],v[180:183],v[96:99]\n"
TR "v[190:191],v5 offset:2048\n"
"v_add_f32 v81,v81,v193\n"
MF "v[96:99],a[220:223],v[184:187],v[96:99]\n"
"ds_read2st64_b32 v[200:201],v13 offset0:32 offset1:33\n"
"v_add_f32 v82,v82,v194\n"
MF "v[104:107],a[224:227],v[184:187],v[104:107]\n"
TR "a[240:241],v5 offset:4096\n"
"v_add_f32 v83,v83,v195\n"
MF "v[104:107],a[184:187],v[180:183],v[104:107]\n"
TR "a[242:243],v5 offset:6144\n"
"v_add_f32 v84,v84,v196\n"
MF "v[104:107],a[144:147],v[176:179],v[104:107]\n"
"ds_read2st64_b32 v[202:203],v13 offset0:34 offset1:35\n"
"v_add_f32 v85,v85,v197\n"
MF "v[112:115],a[148:151],v[176:179],v[112:115]\n"
TR "a[244:245],v5 offset:8192\n"
"v_add_f32 v86,v86,v198\n"
MF "v[112:115],a[188:191],v[180:183],v[112:115]\n"
TR "a[246:247],v5 offset:10240\n"
"v_add_f32 v87,v87,v199\n"
MF "v[112:115],a[228:231],v[184:187],v[112:115]\n"
"ds_read2st64_b32 v[204:205],v13 offset0:36 offset1:37\n"
CV "v80,v80,v81\n"
MF "v[120:123],a[232:235],v[184:187],v[120:123]\n"
"ds_read2st64_b32 v[206:207],v13 offset0:38 offset1:39\n"
CV "v81,v82,v83\n"
MF "v[120:123],a[192:195],v[180:183],v[120:123]\n"
"s_add_u32 s87,s90,13312\n"
CV "v82,v84,v85\n"
CV "v83,v86,v87\n"
MF "v[120:123],a[152:155],v[176:179],v[120:123]\n"
"s_cmp_ge_u32 s85,5\n"
"s_cselect_b32 s82,s70,s82\n"
"s_cselect_b32 s84,0xf0000010,s84\n"
MF "v[128:131],a[156:159],v[176:179],v[128:131]\n"
"buffer_store_dwordx4 v[80:83],v9,s[40:43],s87 offen sc1\n"
MF "v[128:131],a[196:199],v[180:183],v[128:131]\n"
"s_cmp_ge_u32 s85,5\n"
"s_cselect_b32 s71,1024,s71\n"
"s_cselect_b32 s66,0x80000000,s66\n"
MF "v[128:131],a[236:239],v[184:187],v[128:131]\n"
"s_add_u32 s70,s70,20480\n"
"s_waitcnt lgkmcnt(2)\n"
"v_permlane16_swap_b32 v244,v246\n"
MF "v[132:135],a[236:239],a[244:247],v[132:135]\n"
"s_waitcnt lgkmcnt(0)\n"
TR "v[176:177],v6\n"
"v_add_f32 v88,v88,v200\n"
MF "v[132:135],a[196:199],a[240:243],v[132:135]\n"
TR "v[178:179],v6 offset:2048\n"
"v_add_f32 v89,v89,v201\n"
MF "v[132:135],a[156:159],v[188:191],v[132:135]\n"
"v_add_f32 v90,v90,v202\n"
"v_add_f32 v91,v91,v203\n"
MF "v[124:127],a[152:155],v[188:191],v[124:127]\n"
TR "v[180:181],v6 offset:4096\n"
"v_add_f32 v92,v92,v204\n"
MF "v[124:127],a[192:195],a[240:243],v[124:127]\n"
TR "v[182:183],v6 offset:6144\n"
"v_add_f32 v93,v93,v205\n"
MF "v[124:127],a[232:235],a[244:247],v[124:127]\n"
"v_add_f32 v94,v94,v206\n"
"v_add_f32 v95,v95,v207\n"
MF "v[116:119],a[228:231],a[244:247],v[116:119]\n"
TR "v[184:185],v6 offset:8192\n"
CV "v88,v88,v89\n"
MF "v[116:119],a[188:191],a[240:243],v[116:119]\n"
TR "v[186:187],v6 offset:10240\n"
CV "v89,v90,v91\n"
MF "v[116:119],a[148:151],v[188:191],v[116:119]\n"
"s_add_u32 s87,s90,14336\n"
CV "v90,v92,v93\n"
CV "v91,v94,v95\n"
MF "v[108:111],a[144:147],v[188:191],v[108:111]\n"
"v_permlane16_swap_b32 v245,v247\n"
MF "v[108:111],a[184:187],a[240:243],v[108:111]\n"
"buffer_store_dwordx4 v[244:247],v8,s[44:47],s83 offen sc1\n"
MF "v[108:111],a[224:227],a[244:247],v[108:111]\n"
"s_mov_b32 s83,s84\n"
MF "v[100:103],a[220:223],a[244:247],v[100:103]\n"
"buffer_store_dwordx4 v[88:91],v9,s[40:43],s87 offen sc1\n"
MF "v[100:103],a[180:183],a[240:243],v[100:103]\n"
"s_mov_b32 s90,s91\n"
"s_add_u32 s91,s91,20480\n"
MF "v[100:103],a[140:143],v[188:191],v[100:103]\n"
"s_waitcnt lgkmcnt(0)\n"
LD "v[208:211],v9,s[40:43],s82 offen sc1\n"
MF "v[136:139],a[140:143],v[176:179],v[136:139]\n"
TR "v[188:189],v7\n"
MF "v[136:139],a[180:183],v[180:183],v[136:139]\n"
TR "v[190:191],v7 offset:2048\n"
MF "v[136:139],a[220:223],v[184:187],v[136:139]\n"
MF "v[144:147],a[224:227],v[184:187],v[144:147]\n"
TR "a[240:241],v7 offset:4096\n"
MF "v[144:147],a[184:187],v[180:183],v[144:147]\n"
"s_add_u32 s82,s82,s71\n"
TR "a[242:243],v7 offset:6144\n"
MF "v[144:147],a[144:147],v[176:179],v[144:147]\n"
LD "v[212:215],v9,s[40:43],s82 offen sc1\n"
MF "v[152:155],a[148:151],v[176:179],v[152:155]\n"
TR "a[244:245],v7 offset:8192\n"
MF "v[152:155],a[188:191],v[180:183],v[152:155]\n"
TR "a[246:247],v7 offset:10240\n"
MF "v[152:155],a[228:231],v[184:187],v[152:155]\n"
MF "v[160:163],a[232:235],v[184:187],v[160:163]\n"
MF "v[160:163],a[192:195],v[180:183],v[160:163]\n"
MF "v[160:163],a[152:155],v[176:179],v[160:163]\n"
"s_add_u32 s82,s82,s71\n"
MF "v[168:171],a[156:159],v[176:179],v[168:171]\n"
LD "v[216:219],v9,s[40:43],s82 offen sc1\n"
MF "v[168:171],a[196:199],v[180:183],v[168:171]\n"
MF "v[168:171],a[236:239],v[184:187],v[168:171]\n"
"s_waitcnt lgkmcnt(0)\n"
"s_barrier\n"
MF "v[172:175],a[236:239],a[244:247],v[172:175]\n"
TR "v[176:177],v0 offset:24576\n"
MF "v[172:175],a[196:199],a[240:243],v[172:175]\n"
"s_add_u32 s85,s85,1\n"
TR "v[178:179],v0 offset:26624\n"
MF "v[172:175],a[156:159],v[188:191],v[172:175]\n"
MF "v[164:167],a[152:155],v[188:191],v[164:167]\n"
TR "v[180:181],v0 offset:28672\n"
MF "v[164:167],a[192:195],a[240:243],v[164:167]\n"
"s_add_u32 s82,s82,s71\n"
TR "v[182:183],v0 offset:30720\n"
MF "v[164:167],a[232:235],a[244:247],v[164:167]\n"
LD "v[220:223],v9,s[40:43],s82 offen sc1\n"
MF "v[156:159],a[228:231],a[244:247],v[156:159]\n"
TR "v[184:185],v0 offset:32768\n"
MF "v[156:159],a[188:191],a[240:243],v[156:159]\n"
TR "v[186:187],v0 offset:34816\n"
MF "v[156:159],a[148:151],v[188:191],v[156:159]\n"
MF "v[148:151],a[144:147],v[188:191],v[148:151]\n"
MF "v[148:151],a[184:187],a[240:243],v[148:151]\n"
MF "v[148:151],a[224:227],a[244:247],v[148:151]\n"
MF "v[140:143],a[220:223],a[244:247],v[140:143]\n"
MF "v[140:143],a[180:183],a[240:243],v[140:143]\n"
MF "v[140:143],a[140:143],v[188:191],v[140:143]\n"
"s_waitcnt lgkmcnt(0)\n"
MF "v[16:19],a[0:3],v[176:179],v[252:255]\n"
TR "v[188:189],v1 offset:24576\n"
MF "v[16:19],a[40:43],v[180:183],v[16:19]\n"
"s_add_u32 s82,s82,s71\n"
TR "v[190:191],v1 offset:26624\n"
MF "v[16:19],a[80:83],v[184:187],v[16:19]\n"
LD "v[224:227],v9,s[40:43],s82 offen sc1\n"
MF "v[24:27],a[84:87],v[184:187],v[252:255]\n"
TR "a[240:241],v1 offset:28672\n"
MF "v[24:27],a[44:47],v[180:183],v[24:27]\n"
TR "a[242:243],v1 offset:30720\n"
MF "v[24:27],a[4:7],v[176:179],v[24:27]\n"
MF "v[32:35],a[8:11],v[176:179],v[252:255]\n"
TR "a[244:245],v1 offset:32768\n"
MF "v[32:35],a[48:51],v[180:183],v[32:35]\n"
TR "a[246:247],v1 offset:34816\n"
MF "v[32:35],a[88:91],v[184:187],v[32:35]\n"
MF "v[40:43],a[92:95],v[184:187],v[252:255]\n"
MF "v[40:43],a[52:55],v[180:183],v[40:43]\n"
"s_mul_i32 s95,s92,3277\n"
"s_lshr_b32 s95,s95,16\n"
"s_mul_i32 s59,s95,20\n"
MF "v[40:43],a[12:15],v[176:179],v[40:43]\n"
"s_sub_u32 s59,s92,s59\n"
"s_cmp_ge_u32 s59,10\n"
"s_cselect_b32 s96,4294557760,0\n"
MF "v[48:51],a[16:19],v[176:179],v[252:255]\n"
"s_add_u32 s82,s82,s66\n"
"s_add_u32 s96,s96,1024\n"
"s_mul_i32 s59,s59,40960\n"
MF "v[48:51],a[56:59],v[180:183],v[48:51]\n"
LD "v[228:231],v9,s[40:43],s82 offen sc1\n"
MF "v[48:51],a[96:99],v[184:187],v[48:51]\n"
"s_add_u32 s96,s96,s59\n"
"s_lshl_b32 s59,s95,7\n"
"s_waitcnt lgkmcnt(0)\n"
MF "v[52:55],a[96:99],a[244:247],v[252:255]\n"
"s_add_u32 s96,s96,s59\n"
"s_add_u32 s95,s95,3\n"
TR "v[176:177],v2 offset:24576\n"
MF "v[52:55],a[56:59],a[240:243],v[52:55]\n"
"s_cmp_ge_u32 s92,40\n"
"s_cselect_b32 s95,99,s95\n"
TR "v[178:179],v2 offset:26624\n"
MF "v[52:55],a[16:19],v[188:191],v[52:55]\n"
"s_cmp_ge_u32 s85,5\n"
"s_cselect_b32 s95,99,s95\n"
"s_cselect_b32 s72,s85,0\n"
MF "v[44:47],a[12:15],v[188:191],v[252:255]\n"
TR "v[180:181],v2 offset:28672\n"
MF "v[44:47],a[52:55],a[240:243],v[44:47]\n"
TR "v[182:183],v2 offset:30720\n"
MF "v[44:47],a[92:95],a[244:247],v[44:47]\n"
MF "v[36:39],a[88:91],a[244:247],v[252:255]\n"
TR "v[184:185],v2 offset:32768\n"
MF "v[36:39],a[48:51],a[240:243],v[36:39]\n"
"s_add_u32 s82,s82,s71\n"
TR "v[186:187],v2 offset:34816\n"
MF "v[36:39],a[8:11],v[188:191],v[36:39]\n"
LD "v[232:235],v9,s[40:43],s82 offen sc1\n"
MF "v[28:31],a[4:7],v[188:191],v[252:255]\n"
MF "v[28:31],a[44:47],a[240:243],v[28:31]\n"
MF "v[28:31],a[84:87],a[244:247],v[28:31]\n"
MF "v[20:23],a[80:83],a[244:247],v[252:255]\n"
MF "v[20:23],a[40:43],a[240:243],v[20:23]\n"
MF "v[20:23],a[0:3],v[188:191],v[20:23]\n"
"s_waitcnt lgkmcnt(0)\n"
MF "v[56:59],a[0:3],v[176:179],v[252:255]\n"
TR "v[188:189],v3 offset:24576\n"
MF "v[56:59],a[40:43],v[180:183],v[56:59]\n"
"s_add_u32 s82,s82,s71\n"
TR "v[190:191],v3 offset:26624\n"
MF "v[56:59],a[80:83],v[184:187],v[56:59]\n"
LD "v[236:239],v9,s[40:43],s82 offen sc1\n"
MF "v[64:67],a[84:87],v[184:187],v[252:255]\n"
TR "a[240:241],v3 offset:28672\n"
MF "v[64:67],a[44:47],v[180:183],v[64:67]\n"
TR "a[242:243],v3 offset:30720\n"
MF "v[64:67],a[4:7],v[176:179],v[64:67]\n"
MF "v[72:75],a[8:11],v[176:179],v[252:255]\n"
TR "a[244:245],v3 offset:32768\n"
MF "v[72:75],a[48:51],v[180:183],v[72:75]\n"
TR "a[246:247],v3 offset:34816\n"
MF "v[72:75],a[88:91],v[184:187],v[72:75]\n"
MF "v[80:83],a[92:95],v[184:187],v[252:255]\n"
MF "v[80:83],a[52:55],v[180:183],v[80:83]\n"
MF "v[80:83],a[12:15],v[176:179],v[80:83]\n"
MF "v[88:91],a[16:19],v[176:179],v[252:255]\n"
MF "v[88:91],a[56:59],v[180:183],v[88:91]\n"
MF "v[88:91],a[96:99],v[184:187],v[88:91]\n"
"s_waitcnt lgkmcnt(0)\n"
"s_barrier\n"
MF "v[92:95],a[96:99],a[244:247],v[252:255]\n"
TR "v[176:177],v0\n"
MF "v[92:95],a[56:59],a[240:243],v[92:95]\n"
TR "v[178:179],v0 offset:2048\n"
MF "v[92:95],a[16:19],v[188:191],v[92:95]\n"
"ds_read2st64_b32 v[192:193],v13 offset0:192 offset1:193\n"
MF "v[84:87],a[12:15],v[188:191],v[252:255]\n"
TR "v[180:181],v0 offset:4096\n"
MF "v[84:87],a[52:55],a[240:243],v[84:87]\n"
TR "v[182:183],v0 offset:6144\n"
MF "v[84:87],a[92:95],a[244:247],v[84:87]\n"
"ds_read2st64_b32 v[194:195],v13 offset0:194 offset1:195\n"
MF "v[76:79],a[88:91],a[244:247],v[252:255]\n"
TR "v[184:185],v0 offset:8192\n"
MF "v[76:79],a[48:51],a[240:243],v[76:79]\n"
TR "v[186:187],v0 offset:10240\n"
MF "v[76:79],a[8:11],v[188:191],v[76:79]\n"
"ds_read2st64_b32 v[196:197],v13 offset0:196 offset1:197\n"
MF "v[68:71],a[4:7],v[188:191],v[252:255]\n"
"ds_read2st64_b32 v[198:199],v13 offset0:198 offset1:199\n"
MF "v[68:71],a[44:47],a[240:243],v[68:71]\n"
"buffer_load_dword v10,v12,s[40:43],s86 offen sc1\n"
MF "v[68:71],a[84:87],a[244:247],v[68:71]\n"
MF "v[60:63],a[80:83],a[244:247],v[252:255]\n"
MF "v[60:63],a[40:43],a[240:243],v[60:63]\n"
MF "v[60:63],a[0:3],v[188:191],v[60:63]\n"
"s_waitcnt lgkmcnt(2)\n"
MF "v[16:19],a[120:123],v[176:179],v[16:19]\n"
"s_waitcnt lgkmcnt(0)\n"
TR "v[188:189],v1\n"
"v_add_f32 v96,v96,v192\n"
MF "v[16:19],a[160:163],v[180:183],v[16:19]\n"
TR "v[190:191],v1 offset:2048\n"
"v_add_f32 v97,v97,v193\n"
MF "v[16:19],a[200:203],v[184:187],v[16:19]\n"
"ds_read2st64_b32 v[200:201],v13 offset0:200 offset1:201\n"
"v_add_f32 v98,v98,v194\n"
"s_waitcnt vmcnt(9)\n"
"v_mov_b32 v14,s76\n"
"s_add_u32 s76,s76,1\n"
MF "v[24:27],a[204:207],v[184:187],v[24:27]\n"
TR "a[240:241],v1 offset:4096\n"
"v_add_f32 v99,v99,v195\n"
"buffer_store_dword v14,v15,s[40:43],s94 offen sc1\n"
MF "v[24:27],a[164:167],v[180:183],v[24:27]\n"
TR "a[242:243],v1 offset:6144\n"
"v_add_f32 v100,v100,v196\n"
MF "v[24:27],a[124:127],v[176:179],v[24:27]\n"
"ds_read2st64_b32 v[202:203],v13 offset0:202 offset1:203\n"
"v_add_f32 v101,v101,v197\n"
MF "v[32:35],a[128:131],v[176:179],v[32:35]\n"
TR "a[244:245],v1 offset:8192\n"
"v_add_f32 v102,v102,v198\n"
MF "v[32:35],a[168:171],v[180:183],v[32:35]\n"
TR "a[246:247],v1 offset:10240\n"
"v_add_f32 v103,v103,v199\n"
MF "v[32:35],a[208:211],v[184:187],v[32:35]\n"
"ds_read2st64_b32 v[204:205],v13 offset0:204 offset1:205\n"
CV "v96,v96,v97\n"
MF "v[40:43],a[212:215],v[184:187],v[40:43]\n"
"ds_read2st64_b32 v[206:207],v13 offset0:206 offset1:207\n"
CV "v97,v98,v99\n"
MF "v[40:43],a[172:175],v[180:183],v[40:43]\n"
"s_add_u32 s87,s90,0\n"
CV "v98,v100,v101\n"
CV "v99,v102,v103\n"
MF "v[40:43],a[132:135],v[176:179],v[40:43]\n"
MF "v[48:51],a[136:139],v[176:179],v[48:51]\n"
"buffer_store_dwordx4 v[96:99],v9,s[40:43],s87 offen sc1\n"
MF "v[48:51],a[176:179],v[180:183],v[48:51]\n"
MF "v[48:51],a[216:219],v[184:187],v[48:51]\n"
"s_waitcnt lgkmcnt(2)\n"
MF "v[52:55],a[216:219],a[244:247],v[52:55]\n"
"s_waitcnt lgkmcnt(0)\n"
TR "v[176:177],v2\n"
"v_add_f32 v104,v104,v200\n"
MF "v[52:55],a[176:179],a[240:243],v[52:55]\n"
TR "v[178:179],v2 offset:2048\n"
"v_add_f32 v105,v105,v201\n"
MF "v[52:55],a[136:139],v[188:191],v[52:55]\n"
"ds_read2st64_b32 v[192:193],v13 offset0:208 offset1:209\n"
"v_add_f32 v106,v106,v202\n"
MF "v[44:47],a[132:135],v[188:191],v[44:47]\n"
TR "v[180:181],v2 offset:4096\n"
"v_add_f32 v107,v107,v203\n"
MF "v[44:47],a[172:175],a[240:243],v[44:47]\n"
TR "v[182:183],v2 offset:6144\n"
"v_add_f32 v108,v108,v204\n"
MF "v[44:47],a[212:215],a[244:247],v[44:47]\n"
"ds_read2st64_b32 v[194:195],v13 offset0:210 offset1:211\n"
"v_add_f32 v109,v109,v205\n"
MF "v[36:39],a[208:211],a[244:247],v[36:39]\n"
TR "v[184:185],v2 offset:8192\n"
"v_add_f32 v110,v110,v206\n"
MF "v[36:39],a[168:171],a[240:243],v[36:39]\n"
TR "v[186:187],v2 offset:10240\n"
"v_add_f32 v111,v111,v207\n"
MF "v[36:39],a[128:131],v[188:191],v[36:39]\n"
"ds_read2st64_b32 v[196:197],v13 offset0:212 offset1:213\n"
CV "v104,v104,v105\n"
MF "v[28:31],a[124:127],v[188:191],v[28:31]\n"
"ds_read2st64_b32 v[198:199],v13 offset0:214 offset1:215\n"
CV "v105,v106,v107\n"
MF "v[28:31],a[164:167],a[240:243],v[28:31]\n"
"s_add_u32 s87,s90,1024\n"
CV "v106,v108,v109\n"
CV "v107,v110,v111\n"
MF "v[28:31],a[204:207],a[244:247],v[28:31]\n"
MF "v[20:23],a[200:203],a[244:247],v[20:23]\n"
"buffer_store_dwordx4 v[104:107],v9,s[40:43],s87 offen sc1\n"
MF "v[20:23],a[160:163],a[240:243],v[20:23]\n"
MF "v[20:23],a[120:123],v[188:191],v[20:23]\n"
"s_waitcnt lgkmcnt(2)\n"
MF "v[56:59],a[120:123],v[176:179],v[56:59]\n"
"s_waitcnt lgkmcnt(0)\n"
TR "v[188:189],v3\n"
"v_add_f32 v112,v112,v192\n"
MF "v[56:59],a[160:163],v[180:183],v[56:59]\n"
TR "v[190:191],v3 offset:2048\n"
"v_add_f32 v113,v113,v193\n"
MF "v[56:59],a[200:203],v[184:187],v[56:59]\n"
"ds_read2st64_b32 v[200:201],v13 offset0:216 offset1:217\n"
"v_add_f32 v114,v114,v194\n"
MF "v[64:67],a[204:207],v[184:187],v[64:67]\n"
TR "a[240:241],v3 offset:4096\n"
"v_add_f32 v115,v115,v195\n"
MF "v[64:67],a[164:167],v[180:183],v[64:67]\n"
TR "a[242:243],v3 offset:6144\n"
"v_add_f32 v116,v116,v196\n"
MF "v[64:67],a[124:127],v[176:179],v[64:67]\n"
"ds_read2st64_b32 v[202:203],v13 offset0:218 offset1:219\n"
"v_add_f32 v117,v117,v197\n"
MF "v[72:75],a[128:131],v[176:179],v[72:75]\n"
TR "a[244:245],v3 offset:8192\n"
"v_add_f32 v118,v118,v198\n"
MF "v[72:75],a[168:171],v[180:183],v[72:75]\n"
TR "a[246:247],v3 offset:10240\n"
"v_add_f32 v119,v119,v199\n"
MF "v[72:75],a[208:211],v[184:187],v[72:75]\n"
"ds_read2st64_b32 v[204:205],v13 offset0:220 offset1:221\n"
CV "v112,v112,v113\n"
MF "v[80:83],a[212:215],v[184:187],v[80:83]\n"
"ds_read2st64_b32 v[206:207],v13 offset0:222 offset1:223\n"
CV "v113,v114,v115\n"
MF "v[80:83],a[172:175],v[180:183],v[80:83]\n"
"s_add_u32 s87,s90,2048\n"
CV "v114,v116,v117\n"
CV "v115,v118,v119\n"
MF "v[80:83],a[132:135],v[176:179],v[80:83]\n"
MF "v[88:91],a[136:139],v[176:179],v[88:91]\n"
"buffer_store_dwordx4 v[112:115],v9,s[40:43],s87 offen sc1\n"
MF "v[88:91],a[176:179],v[180:183],v[88:91]\n"
MF "v[88:91],a[216:219],v[184:187],v[88:91]\n"
"s_waitcnt lgkmcnt(0)\n"
"s_barrier\n"
MF "v[92:95],a[216:219],a[244:247],v[92:95]\n"
TR "v[176:177],v0 offset:49152\n"
"v_add_f32 v120,v120,v200\n"
MF "v[92:95],a[176:179],a[240:243],v[92:95]\n"
TR "v[178:179],v0 offset:51200\n"
"v_add_f32 v121,v121,v201\n"
MF "v[92:95],a[136:139],v[188:191],v[92:95]\n"
"ds_read2st64_b32 v[192:193],v13 offset0:224 offset1:225\n"
"v_add_f32 v122,v122,v202\n"
MF "v[84:87],a[132:135],v[188:191],v[84:87]\n"
TR "v[180:181],v0 offset:53248\n"
"v_add_f32 v123,v123,v203\n"
MF "v[84:87],a[172:175],a[240:243],v[84:87]\n"
TR "v[182:183],v0 offset:55296\n"
"v_add_f32 v124,v124,v204\n"
MF "v[84:87],a[212:215],a[244:247],v[84:87]\n"
"ds_read2st64_b32 v[194:195],v13 offset0:226 offset1:227\n"
"v_add_f32 v125,v125,v205\n"
MF "v[76:79],a[208:211],a[244:247],v[76:79]\n"
TR "v[184:185],v0 offset:57344\n"
"v_add_f32 v126,v126,v206\n"
MF "v[76:79],a[168:171],a[240:243],v[76:79]\n"
TR "v[186:187],v0 offset:59392\n"
"v_add_f32 v127,v127,v207\n"
MF "v[76:79],a[128:131],v[188:191],v[76:79]\n"
"ds_read2st64_b32 v[196:197],v13 offset0:228 offset1:229\n"
CV "v120,v120,v121\n"
MF "v[68:71],a[124:127],v[188:191],v[68:71]\n"
"ds_read2st64_b32 v[198:199],v13 offset0:230 offset1:231\n"
CV "v121,v122,v123\n"
MF "v[68:71],a[164:167],a[240:243],v[68:71]\n"
"s_add_u32 s87,s90,3072\n"
CV "v122,v124,v125\n"
CV "v123,v126,v127\n"
MF "v[68:71],a[204:207],a[244:247],v[68:71]\n"
MF "v[60:63],a[200:203],a[244:247],v[60:63]\n"
"buffer_store_dwordx4 v[120:123],v9,s[40:43],s87 offen sc1\n"
MF "v[60:63],a[160:163],a[240:243],v[60:63]\n"
MF "v[60:63],a[120:123],v[188:191],v[60:63]\n"
"s_waitcnt lgkmcnt(2)\n"
MF "v[16:19],a[20:23],v[176:179],v[16:19]\n"
"s_waitcnt lgkmcnt(0)\n"
TR "v[188:189],v1 offset:49152\n"
"v_add_f32 v128,v128,v192\n"
MF "v[16:19],a[60:63],v[180:183],v[16:19]\n"
TR "v[190:191],v1 offset:51200\n"
"v_add_f32 v129,v129,v193\n"
MF "v[16:19],a[100:103],v[184:187],v[16:19]\n"
"ds_read2st64_b32 v[200:201],v13 offset0:0 offset1:1\n"
"v_add_f32 v130,v130,v194\n"
MF "v[24:27],a[104:107],v[184:187],v[24:27]\n"
TR "a[240:241],v1 offset:53248\n"
"v_add_f32 v131,v131,v195\n"
MF "v[24:27],a[64:67],v[180:183],v[24:27]\n"
TR "a[242:243],v1 offset:55296\n"
"v_add_f32 v132,v132,v196\n"
MF "v[24:27],a[24:27],v[176:179],v[24:27]\n"
"ds_read2st64_b32 v[202:203],v13 offset0:2 offset1:3\n"
"v_add_f32 v133,v133,v197\n"
MF "v[32:35],a[28:31],v[176:179],v[32:35]\n"
TR "a[244:245],v1 offset:57344\n"
"v_add_f32 v134,v134,v198\n"
MF "v[32:35],a[68:71],v[180:183],v[32:35]\n"
TR "a[246:247],v1 offset:59392\n"
"v_add_f32 v135,v135,v199\n"
MF "v[32:35],a[108:111],v[184:187],v[32:35]\n"
"ds_read2st64_b32 v[204:205],v13 offset0:4 offset1:5\n"
CV "v128,v128,v129\n"
MF "v[40:43],a[112:115],v[184:187],v[40:43]\n"
"ds_read2st64_b32 v[206:207],v13 offset0:6 offset1:7\n"
CV "v129,v130,v131\n"
MF "v[40:43],a[72:75],v[180:183],v[40:43]\n"
"s_add_u32 s87,s90,4096\n"
CV "v130,v132,v133\n"
CV "v131,v134,v135\n"
MF "v[40:43],a[32:35],v[176:179],v[40:43]\n"
MF "v[48:51],a[36:39],v[176:179],v[48:51]\n"
"buffer_store_dwordx4 v[128:131],v9,s[40:43],s87 offen sc1\n"
MF "v[48:51],a[76:79],v[180:183],v[48:51]\n"
MF "v[48:51],a[116:119],v[184:187],v[48:51]\n"
"s_waitcnt lgkmcnt(2)\n"
MF "v[52:55],a[116:119],a[244:247],v[52:55]\n"
"s_waitcnt lgkmcnt(0)\n"
TR "v[176:177],v2 offset:49152\n"
"v_add_f32 v136,v136,v200\n"
MF "v[52:55],a[76:79],a[240:243],v[52:55]\n"
TR "v[178:179],v2 offset:51200\n"
"v_add_f32 v137,v137,v201\n"
MF "v[52:55],a[36:39],v[188:191],v[52:55]\n"
"ds_read2st64_b32 v[192:193],v13 offset0:8 offset1:9\n"
"v_add_f32 v138,v138,v202\n"
MF "v[44:47],a[32:35],v[188:191],v[44:47]\n"
TR "v[180:181],v2 offset:53248\n"
"v_add_f32 v139,v139,v203\n"
MF "v[44:47],a[72:75],a[240:243],v[44:47]\n"
TR "v[182:183],v2 offset:55296\n"
"v_add_f32 v140,v140,v204\n"
MF "v[44:47],a[112:115],a[244:247],v[44:47]\n"
"ds_read2st64_b32 v[194:195],v13 offset0:10 offset1:11\n"
"v_add_f32 v141,v141,v205\n"
MF "v[36:39],a[108:111],a[244:247],v[36:39]\n"
TR "v[184:185],v2 offset:57344\n"
"v_add_f32 v142,v142,v206\n"
MF "v[36:39],a[68:71],a[240:243],v[36:39]\n"
TR "v[186:187],v2 offset:59392\n"
"v_add_f32 v143,v143,v207\n"
MF "v[36:39],a[28:31],v[188:191],v[36:39]\n"
"ds_read2st64_b32 v[196:197],v13 offset0:12 offset1:13\n"
CV "v136,v136,v137\n"
MF "v[28:31],a[24:27],v[188:191],v[28:31]\n"
"ds_read2st64_b32 v[198:199],v13 offset0:14 offset1:15\n"
CV "v137,v138,v139\n"
MF "v[28:31],a[64:67],a[240:243],v[28:31]\n"
"s_add_u32 s87,s90,10240\n"
CV "v138,v140,v141\n"
CV "v139,v142,v143\n"
MF "v[28:31],a[104:107],a[244:247],v[28:31]\n"
MF "v[20:23],a[100:103],a[244:247],v[20:23]\n"
"buffer_store_dwordx4 v[136:139],v9,s[40:43],s87 offen sc1\n"
MF "v[20:23],a[60:63],a[240:243],v[20:23]\n"
"s_waitcnt vmcnt(7)\n"
"s_cmp_eq_u32 s83,0xf0000000\n"
MF "v[20:23],a[20:23],v[188:191],v[20:23]\n"
"s_cbranch_scc1 .Ljs15\n"
"s_cmp_eq_u32 s83,0xf0000010\n"
"s_cbranch_scc1 .Ljc15\n"
MF "v[248:251],v[210:213],a[250:253],0\n"
"v_mov_b32 v240,v208\n"
"v_mov_b32 v241,v209\n"
MF "v[244:247],v[214:217],a[250:253],0\n"
MF "v[248:251],v[218:221],a[250:253],v[248:251]\n"
MF "v[244:247],v[222:225],a[250:253],v[244:247]\n"
MF "v[248:251],v[226:229],a[250:253],v[248:251]\n"
MF "v[244:247],v[230:233],a[250:253],v[244:247]\n"
MF "v[248:251],v[234:237],a[250:253],v[248:251]\n"
MF "v[244:247],v[238:241],a[250:253],v[244:247]\n"
"s_branch .Ljs15\n"
".Ljc15:\n"
MF "v[56:59],a[252:255],v[208:211],v[56:59]\n"
MF "v[64:67],a[252:255],v[212:215],v[64:67]\n"
MF "v[72:75],a[252:255],v[216:219],v[72:75]\n"
MF "v[80:83],a[252:255],v[220:223],v[80:83]\n"
MF "v[88:91],a[252:255],v[224:227],v[88:91]\n"
MF "v[60:63],a[248:251],v[208:211],v[60:63]\n"
MF "v[68:71],a[248:251],v[212:215],v[68:71]\n"
MF "v[76:79],a[248:251],v[216:219],v[76:79]\n"
MF "v[84:87],a[248:251],v[220:223],v[84:87]\n"
MF "v[92:95],a[248:251],v[224:227],v[92:95]\n"
".Ljs15:\n"
"s_waitcnt lgkmcnt(2)\n"
MF "v[56:59],a[20:23],v[176:179],v[56:59]\n"
"s_waitcnt lgkmcnt(0)\n"
TR "v[188:189],v3 offset:49152\n"
"v_add_f32 v144,v144,v192\n"
MF "v[56:59],a[60:63],v[180:183],v[56:59]\n"
TR "v[190:191],v3 offset:51200\n"
"v_add_f32 v145,v145,v193\n"
MF "v[56:59],a[100:103],v[184:187],v[56:59]\n"
"ds_read2st64_b32 v[200:201],v13 offset0:16 offset1:17\n"
"v_add_f32 v146,v146,v194\n"
MF "v[64:67],a[104:107],v[184:187],v[64:67]\n"
TR "a[240:241],v3 offset:53248\n"
"v_add_f32 v147,v147,v195\n"
MF "v[64:67],a[64:67],v[180:183],v[64:67]\n"
TR "a[242:243],v3 offset:55296\n"
"v_add_f32 v148,v148,v196\n"
MF "v[64:67],a[24:27],v[176:179],v[64:67]\n"
"ds_read2st64_b32 v[202:203],v13 offset0:18 offset1:19\n"
"v_add_f32 v149,v149,v197\n"
MF "v[72:75],a[28:31],v[176:179],v[72:75]\n"
TR "a[244:245],v3 offset:57344\n"
"v_add_f32 v150,v150,v198\n"
MF "v[72:75],a[68:71],v[180:183],v[72:75]\n"
TR "a[246:247],v3 offset:59392\n"
"v_add_f32 v151,v151,v199\n"
MF "v[72:75],a[108:111],v[184:187],v[72:75]\n"
"ds_read2st64_b32 v[204:205],v13 offset0:20 offset1:21\n"
CV "v144,v144,v145\n"
MF "v[80:83],a[112:115],v[184:187],v[80:83]\n"
"ds_read2st64_b32 v[206:207],v13 offset0:22 offset1:23\n"
CV "v145,v146,v147\n"
MF "v[80:83],a[72:75],v[180:183],v[80:83]\n"
"s_add_u32 s87,s90,11264\n"
CV "v146,v148,v149\n"
CV "v147,v150,v151\n"
MF "v[80:83],a[32:35],v[176:179],v[80:83]\n"
"s_lshl_b32 s82,s92,10\n"
"v_sub_u32 v10,v10,v11\n"
"v_cmp_gt_u32 vcc,s95,v10\n"
MF "v[88:91],a[36:39],v[176:179],v[88:91]\n"
"buffer_store_dwordx4 v[144:147],v9,s[40:43],s87 offen sc1\n"
MF "v[88:91],a[76:79],v[180:183],v[88:91]\n"
"s_add_u32 s82,s82,s93\n"
CV "v244,v244,v245\n"
CV "v245,v246,v247\n"
MF "v[88:91],a[116:119],v[184:187],v[88:91]\n"
"s_waitcnt lgkmcnt(0)\n"
"s_barrier\n"
MF "v[92:95],a[116:119],a[244:247],v[92:95]\n"
TR "v[176:177],v4\n"
"v_add_f32 v152,v152,v200\n"
MF "v[92:95],a[76:79],a[240:243],v[92:95]\n"
TR "v[178:179],v4 offset:2048\n"
"v_add_f32 v153,v153,v201\n"
MF "v[92:95],a[36:39],v[188:191],v[92:95]\n"
"ds_read2st64_b32 v[192:193],v13 offset0:24 offset1:25\n"
"v_add_f32 v154,v154,v202\n"
MF "v[84:87],a[32:35],v[188:191],v[84:87]\n"
TR "v[180:181],v4 offset:4096\n"
"v_add_f32 v155,v155,v203\n"
MF "v[84:87],a[72:75],a[240:243],v[84:87]\n"
TR "v[182:183],v4 offset:6144\n"
"v_add_f32 v156,v156,v204\n"
MF "v[84:87],a[112:115],a[244:247],v[84:87]\n"
"ds_read2st64_b32 v[194:195],v13 offset0:26 offset1:27\n"
"v_add_f32 v157,v157,v205\n"
MF "v[76:79],a[108:111],a[244:247],v[76:79]\n"
TR "v[184:185],v4 offset:8192\n"
"v_add_f32 v158,v158,v206\n"
MF "v[76:79],a[68:71],a[240:243],v[76:79]\n"
TR "v[186:187],v4 offset:10240\n"
"v_add_f32 v159,v159,v207\n"
MF "v[76:79],a[28:31],v[188:191],v[76:79]\n"
"ds_read2st64_b32 v[196:197],v13 offset0:28 offset1:29\n"
CV "v152,v152,v153\n"
MF "v[68:71],a[24:27],v[188:191],v[68:71]\n"
"ds_read2st64_b32 v[198:199],v13 offset0:30 offset1:31\n"
CV "v153,v154,v155\n"
MF "v[68:71],a[64:67],a[240:243],v[68:71]\n"
"s_add_u32 s87,s90,12288\n"
CV "v154,v156,v157\n"
CV "v155,v158,v159\n"
MF "v[68:71],a[104:107],a[244:247],v[68:71]\n"
"s_cmp_eq_u64 vcc,0\n"
"s_cselect_b32 s82,s82,0xf0000000\n"
"s_cselect_b32 s84,s96,0xf0000000\n"
"s_cselect_b32 s59,32,0\n"
MF "v[60:63],a[100:103],a[244:247],v[60:63]\n"
"buffer_store_dwordx4 v[152:155],v9,s[40:43],s87 offen sc1\n"
MF "v[60:63],a[60:63],a[240:243],v[60:63]\n"
"s_add_u32 s92,s92,s59\n"
".Lnf6:\n"
"v_readlane_b32 s58,v10,s67\n"
"s_cmp_lt_u32 s58,s72\n"
"s_cbranch_scc1 .Lsp6\n"
".Lnb6:\n"
MF "v[60:63],a[20:23],v[188:191],v[60:63]\n"
"s_waitcnt lgkmcnt(2)\n"
CV "v246,v248,v249\n"
CV "v247,v250,v251\n"
MF "v[16:19],a[140:143],v[176:179],v[16:19]\n"
"s_waitcnt lgkmcnt(0)\n"
TR "v[188:189],v5\n"
"v_add_f32 v160,v160,v192\n"
MF "v[16:19],a[180:183],v[180:183],v[16:19]\n"
TR "v[190:191],v5 offset:2048\n"
"v_add_f32 v161,v161,v193\n"
MF "v[16:19],a[220:223],v[184:187],v[16:19]\n"
"ds_read2st64_b32 v[200:201],v13 offset0:32 offset1:33\n"
"v_add_f32 v162,v162,v194\n"
MF "v[24:27],a[224:227],v[184:187],v[24:27]\n"
TR "a[240:241],v5 offset:4096\n"
"v_add_f32 v163,v163,v195\n"
MF "v[24:27],a[184:187],v[180:183],v[24:27]\n"
TR "a[242:243],v5 offset:6144\n"
"v_add_f32 v164,v164,v196\n"
MF "v[24:27],a[144:147],v[176:179],v[24:27]\n"
"ds_read2st64_b32 v[202:203],v13 offset0:34 offset1:35\n"
"v_add_f32 v165,v165,v197\n"
MF "v[32:35],a[148:151],v[176:179],v[32:35]\n"
TR "a[244:245],v5 offset:8192\n"
"v_add_f32 v166,v166,v198\n"
MF "v[32:35],a[188:191],v[180:183],v[32:35]\n"
TR "a[246:247],v5 offset:10240\n"
"v_add_f32 v167,v167,v199\n"
MF "v[32:35],a[228:231],v[184:187],v[32:35]\n"
"ds_read2st64_b32 v[204:205],v13 offset0:36 offset1:37\n"
CV "v160,v160,v161\n"
MF "v[40:43],a[232:235],v[184:187],v[40:43]\n"
"ds_read2st64_b32 v[206:207],v13 offset0:38 offset1:39\n"
CV "v161,v162,v163\n"
MF "v[40:43],a[192:195],v[180:183],v[40:43]\n"
"s_add_u32 s87,s90,13312\n"
CV "v162,v164,v165\n"
CV "v163,v166,v167\n"
MF "v[40:43],a[152:155],v[176:179],v[40:43]\n"
"s_cmp_ge_u32 s85,5\n"
"s_cselect_b32 s82,s70,s82\n"
"s_cselect_b32 s84,0xf0000010,s84\n"
MF "v[48:51],a[156:159],v[176:179],v[48:51]\n"
"buffer_store_dwordx4 v[160:163],v9,s[40:43],s87 offen sc1\n"
MF "v[48:51],a[196:199],v[180:183],v[48:51]\n"
"s_cmp_ge_u32 s85,5\n"
"s_cselect_b32 s71,1024,s71\n"
"s_cselect_b32 s66,0x80000000,s66\n"
MF "v[48:51],a[236:239],v[184:187],v[48:51]\n"
"s_add_u32 s70,s70,20480\n"
"s_waitcnt lgkmcnt(2)\n"
"v_permlane16_swap_b32 v244,v246\n"
MF "v[52:55],a[236:239],a[244:247],v[52:55]\n"
"s_waitcnt lgkmcnt(0)\n"
TR "v[176:177],v6\n"
"v_add_f32 v168,v168,v200\n"
MF "v[52:55],a[196:199],a[240:243],v[52:55]\n"
TR "v[178:179],v6 offset:2048\n"
"v_add_f32 v169,v169,v201\n"
MF "v[52:55],a[156:159],v[188:191],v[52:55]\n"
"v_add_f32 v170,v170,v202\n"
"v_add_f32 v171,v171,v203\n"
MF "v[44:47],a[152:155],v[188:191],v[44:47]\n"
TR "v[180:181],v6 offset:4096\n"
"v_add_f32 v172,v172,v204\n"
MF "v[44:47],a[192:195],a[240:243],v[44:47]\n"
TR "v[182:183],v6 offset:6144\n"
"v_add_f32 v173,v173,v205\n"
MF "v[44:47],a[232:235],a[244:247],v[44:47]\n"
"v_add_f32 v174,v174,v206\n"
"v_add_f32 v175,v175,v207\n"
MF "v[36:39],a[228:231],a[244:247],v[36:39]\n"
TR "v[184:185],v6 offset:8192\n"
CV "v168,v168,v169\n"
MF "v[36:39],a[188:191],a[240:243],v[36:39]\n"
TR "v[186:187],v6 offset:10240\n"
CV "v169,v170,v171\n"
MF "v[36:39],a[148:151],v[188:191],v[36:39]\n"
"s_add_u32 s87,s90,14336\n"
CV "v170,v172,v173\n"
CV "v171,v174,v175\n"
MF "v[28:31],a[144:147],v[188:191],v[28:31]\n"
"v_permlane16_swap_b32 v245,v247\n"
MF "v[28:31],a[184:187],a[240:243],v[28:31]\n"
"buffer_store_dwordx4 v[244:247],v8,s[44:47],s83 offen sc1\n"
MF "v[28:31],a[224:227],a[244:247],v[28:31]\n"
"s_mov_b32 s83,s84\n"
MF "v[20:23],a[220:223],a[244:247],v[20:23]\n"
"buffer_store_dwordx4 v[168:171],v9,s[40:43],s87 offen sc1\n"
MF "v[20:23],a[180:183],a[240:243],v[20:23]\n"
"s_mov_b32 s90,s91\n"
"s_add_u32 s91,s91,20480\n"
MF "v[20:23],a[140:143],v[188:191],v[20:23]\n"
"s_waitcnt lgkmcnt(0)\n"
LD "v[208:211],v9,s[40:43],s82 offen sc1\n"
MF "v[56:59],a[140:143],v[176:179],v[56:59]\n"
TR "v[188:189],v7\n"
MF "v[56:59],a[180:183],v[180:183],v[56:59]\n"
TR "v[190:191],v7 offset:2048\n"
MF "v[56:59],a[220:223],v[184:187],v[56:59]\n"
MF "v[64:67],a[224:227],v[184:187],v[64:67]\n"
TR "a[240:241],v7 offset:4096\n"
MF "v[64:67],a[184:187],v[180:183],v[64:67]\n"
"s_add_u32 s82,s82,s71\n"
TR "a[242:243],v7 offset:6144\n"
MF "v[64:67],a[144:147],v[176:179],v[64:67]\n"
LD "v[212:215],v9,s[40:43],s82 offen sc1\n"
MF "v[72:75],a[148:151],v[176:179],v[72:75]\n"
TR "a[244:245],v7 offset:8192\n"
MF "v[72:75],a[188:191],v[180:183],v[72:75]\n"
TR "a[246:247],v7 offset:10240\n"
MF "v[72:75],a[228:231],v[184:187],v[72:75]\n"
MF "v[80:83],a[232:235],v[184:187],v[80:83]\n"
MF "v[80:83],a[192:195],v[180:183],v[80:83]\n"
MF "v[80:83],a[152:155],v[176:179],v[80:83]\n"
"s_add_u32 s82,s82,s71\n"
MF "v[88:91],a[156:159],v[176:179],v[88:91]\n"
LD "v[216:219],v9,s[40:43],s82 offen sc1\n"
MF "v[88:91],a[196:199],v[180:183],v[88:91]\n"
MF "v[88:91],a[236:239],v[184:187],v[88:91]\n"
"s_waitcnt lgkmcnt(0)\n"
"s_barrier\n"
MF "v[92:95],a[236:239],a[244:247],v[92:95]\n"
TR "v[176:177],v0 offset:24576\n"
MF "v[92:95],a[196:199],a[240:243],v[92:95]\n"
"s_add_u32 s85,s85,1\n"
TR "v[178:179],v0 offset:26624\n"
MF "v[92:95],a[156:159],v[188:191],v[92:95]\n"
MF "v[84:87],a[152:155],v[188:191],v[84:87]\n"
TR "v[180:181],v0 offset:28672\n"
MF "v[84:87],a[192:195],a[240:243],v[84:87]\n"
"s_add_u32 s82,s82,s71\n"
TR "v[182:183],v0 offset:30720\n"
MF "v[84:87],a[232:235],a[244:247],v[84:87]\n"
LD "v[220:223],v9,s[40:43],s82 offen sc1\n"
MF "v[76:79],a[228:231],a[244:247],v[76:79]\n"
TR "v[184:185],v0 offset:32768\n"
MF "v[76:79],a[188:191],a[240:243],v[76:79]\n"
TR "v[186:187],v0 offset:34816\n"
MF "v[76:79],a[148:151],v[188:191],v[76:79]\n"
MF "v[68:71],a[144:147],v[188:191],v[68:71]\n"
MF "v[68:71],a[184:187],a[240:243],v[68:71]\n"
MF "v[68:71],a[224:227],a[244:247],v[68:71]\n"
MF "v[60:63],a[220:223],a[244:247],v[60:63]\n"
MF "v[60:63],a[180:183],a[240:243],v[60:63]\n"
MF "v[60:63],a[140:143],v[188:191],v[60:63]\n"
"s_waitcnt lgkmcnt(0)\n"
"s_cmp_lg_u32 s85,9\n"
"s_cbranch_scc1 .Lpl1\n"
".Lplast1:\n"
"s_cbranch_execz .Lipr3\n"
MF "v[96:99],v[176:179],a[0:3],v[252:255]\n"
"s_add_u32 m0,s79,73728\n"
TR "v[188:189],v1 offset:24576\n"
TR "v[190:191],v1 offset:26624\n"
MF "v[96:99],v[180:183],a[40:43],v[96:99]\n"
"s_nop 0\n"
TR "a[240:241],v1 offset:28672\n"
TR "a[242:243],v1 offset:30720\n"
MF "v[96:99],v[184:187],a[80:83],v[96:99]\n"
"s_add_u32 s82,s82,s71\n"
TR "a[244:245],v1 offset:32768\n"
TR "a[246:247],v1 offset:34816\n"
MF "v[104:107],v[184:187],a[84:87],v[252:255]\n"
LD "v[224:227],v9,s[40:43],s82 offen sc1\n"
MF "v[104:107],v[180:183],a[44:47],v[104:107]\n"
"s_nop 0\n"
"ds_write_addtid_b32 v56\n"
"ds_write_addtid_b32 v57 offset:256\n"
MF "v[104:107],v[176:179],a[4:7],v[104:107]\n"
"s_nop 0\n"
"ds_write_addtid_b32 v58 offset:512\n"
"ds_write_addtid_b32 v59 offset:768\n"
MF "v[112:115],v[176:179],a[8:11],v[252:255]\n"
"s_nop 0\n"
"ds_write_addtid_b32 v60 offset:1024\n"
"ds_write_addtid_b32 v61 offset:1280\n"
MF "v[112:115],v[180:183],a[48:51],v[112:115]\n"
"s_nop 0\n"
"ds_write_addtid_b32 v62 offset:1536\n"
"ds_write_addtid_b32 v63 offset:1792\n"
MF "v[112:115],v[184:187],a[88:91],v[112:115]\n"
"s_nop 0\n"
"ds_write_addtid_b32 v64 offset:2048\n"
"ds_write_addtid_b32 v65 offset:2304\n"
MF "v[120:123],v[184:187],a[92:95],v[252:255]\n"
"s_nop 0\n"
"ds_write_addtid_b32 v66 offset:2560\n"
"ds_write_addtid_b32 v67 offset:2816\n"
MF "v[120:123],v[180:183],a[52:55],v[120:123]\n"
"s_nop 0\n"
"ds_write_addtid_b32 v68 offset:3072\n"
"ds_write_addtid_b32 v69 offset:3328\n"
MF "v[120:123],v[176:179],a[12:15],v[120:123]\n"
"s_nop 0\n"
"ds_write_addtid_b32 v70 offset:3584\n"
"ds_write_addtid_b32 v71 offset:3840\n"
MF "v[128:131],v[176:179],a[16:19],v[252:255]\n"
"s_nop 0\n"
"ds_write_addtid_b32 v72 offset:4096\n"
"ds_write_addtid_b32 v73 offset:4352\n"
MF "v[128:131],v[180:183],a[56:59],v[128:131]\n"
"s_nop 0\n"
"ds_write_addtid_b32 v74 offset:4608\n"
"ds_write_addtid_b32 v75 offset:4864\n"
MF "v[128:131],v[184:187],a[96:99],v[128:131]\n"
"s_waitcnt lgkmcnt(15)\n"
"ds_write_addtid_b32 v76 offset:5120\n"
MF "v[132:135],a[244:247],a[96:99],v[252:255]\n"
"s_nop 0\n"
TR "v[176:177],v2 offset:24576\n"
TR "v[178:179],v2 offset:26624\n"
MF "v[132:135],a[240:243],a[56:59],v[132:135]\n"
"s_nop 0\n"
TR "v[180:181],v2 offset:28672\n"
TR "v[182:183],v2 offset:30720\n"
MF "v[132:135],v[188:191],a[16:19],v[132:135]\n"
"s_nop 0\n"
TR "v[184:185],v2 offset:32768\n"
TR "v[186:187],v2 offset:34816\n"
MF "v[124:127],v[188:191],a[12:15],v[252:255]\n"
"s_nop 0\n"
"ds_write_addtid_b32 v77 offset:5376\n"
"ds_write_addtid_b32 v78 offset:5632\n"
MF "v[124:127],a[240:243],a[52:55],v[124:127]\n"
"s_nop 0\n"
"ds_write_addtid_b32 v79 offset:5888\n"
"ds_write_addtid_b32 v80 offset:6144\n"
MF "v[124:127],a[244:247],a[92:95],v[124:127]\n"
"s_nop 0\n"
"ds_write_addtid_b32 v81 offset:6400\n"
"ds_write_addtid_b32 v82 offset:6656\n"
MF "v[116:119],a[244:247],a[88:91],v[252:255]\n"
"s_nop 0\n"
"ds_write_addtid_b32 v83 offset:6912\n"
"ds_write_addtid_b32 v84 offset:7168\n"
MF "v[116:119],a[240:243],a[48:51],v[116:119]\n"
"s_nop 0\n"
"ds_write_addtid_b32 v85 offset:7424\n"
"ds_write_addtid_b32 v86 offset:7680\n"
MF "v[116:119],v[188:191],a[8:11],v[116:119]\n"
"s_nop 0\n"
"ds_write_addtid_b32 v87 offset:7936\n"
"ds_write_addtid_b32 v88 offset:8192\n"
MF "v[108:111],v[188:191],a[4:7],v[252:255]\n"
"s_nop 0\n"
"ds_write_addtid_b32 v89 offset:8448\n"
"ds_write_addtid_b32 v90 offset:8704\n"
MF "v[108:111],a[240:243],a[44:47],v[108:111]\n"
"s_nop 0\n"
"ds_write_addtid_b32 v91 offset:8960\n"
"ds_write_addtid_b32 v92 offset:9216\n"
MF "v[108:111],a[244:247],a[84:87],v[108:111]\n"
"s_nop 0\n"
"ds_write_addtid_b32 v93 offset:9472\n"
"ds_write_addtid_b32 v94 offset:9728\n"
MF "v[100:103],a[244:247],a[80:83],v[252:255]\n"
"ds_write_addtid_b32 v95 offset:9984\n"
MF "v[100:103],a[240:243],a[40:43],v[100:103]\n"
MF "v[100:103],v[188:191],a[0:3],v[100:103]\n"
"s_waitcnt lgkmcnt(15)\n"
MF "v[136:139],v[176:179],a[0:3],v[252:255]\n"
"s_nop 0\n"
TR "v[188:189],v3 offset:24576\n"
TR "v[190:191],v3 offset:26624\n"
MF "v[136:139],v[180:183],a[40:43],v[136:139]\n"
"s_nop 0\n"
TR "a[240:241],v3 offset:28672\n"
TR "a[242:243],v3 offset:30720\n"
MF "v[136:139],v[184:187],a[80:83],v[136:139]\n"
"s_nop 0\n"
TR "a[244:245],v3 offset:32768\n"
TR "a[246:247],v3 offset:34816\n"
MF "v[144:147],v[184:187],a[84:87],v[252:255]\n"
"s_waitcnt lgkmcnt(0)\n"
"s_barrier\n"
MF "v[144:147],v[180:183],a[44:47],v[144:147]\n"
"ds_read2st64_b32 v[56:57],v13 offset0:192 offset1:193\n"
MF "v[144:147],v[176:179],a[4:7],v[144:147]\n"
"ds_read2st64_b32 v[58:59],v13 offset0:194 offset1:195\n"
MF "v[152:155],v[176:179],a[8:11],v[252:255]\n"
"ds_read2st64_b32 v[60:61],v13 offset0:196 offset1:197\n"
MF "v[152:155],v[180:183],a[48:51],v[152:155]\n"
"ds_read2st64_b32 v[62:63],v13 offset0:198 offset1:199\n"
MF "v[152:155],v[184:187],a[88:91],v[152:155]\n"
"ds_read2st64_b32 v[64:65],v13 offset0:200 offset1:201\n"
MF "v[160:163],v[184:187],a[92:95],v[252:255]\n"
"ds_read2st64_b32 v[66:67],v13 offset0:202 offset1:203\n"
MF "v[160:163],v[180:183],a[52:55],v[160:163]\n"
"ds_read2st64_b32 v[68:69],v13 offset0:204 offset1:205\n"
MF "v[160:163],v[176:179],a[12:15],v[160:163]\n"
"ds_read2st64_b32 v[70:71],v13 offset0:206 offset1:207\n"
MF "v[168:171],v[176:179],a[16:19],v[252:255]\n"
"s_waitcnt lgkmcnt(4)\n"
"ds_read2st64_b32 v[72:73],v13 offset0:208 offset1:209\n"
"v_add_f32 v16,v16,v56\n"
MF "v[168:171],v[180:183],a[56:59],v[168:171]\n"
"ds_read2st64_b32 v[74:75],v13 offset0:210 offset1:211\n"
"v_add_f32 v17,v17,v57\n"
MF "v[168:171],v[184:187],a[96:99],v[168:171]\n"
"s_waitcnt lgkmcnt(0)\n"
"s_barrier\n"
MF "v[172:175],a[244:247],a[96:99],v[252:255]\n"
"s_nop 0\n"
TR "v[176:177],v0\n"
TR "v[178:179],v0 offset:2048\n"
MF "v[172:175],a[240:243],a[56:59],v[172:175]\n"
"s_nop 0\n"
TR "v[180:181],v0 offset:4096\n"
TR "v[182:183],v0 offset:6144\n"
MF "v[172:175],v[188:191],a[16:19],v[172:175]\n"
"s_nop 0\n"
TR "v[184:185],v0 offset:8192\n"
TR "v[186:187],v0 offset:10240\n"
MF "v[164:167],v[188:191],a[12:15],v[252:255]\n"
"ds_read2st64_b32 v[76:77],v13 offset0:212 offset1:213\n"
"v_add_f32 v18,v18,v58\n"
MF "v[164:167],a[240:243],a[52:55],v[164:167]\n"
"ds_read2st64_b32 v[78:79],v13 offset0:214 offset1:215\n"
"v_add_f32 v19,v19,v59\n"
MF "v[164:167],a[244:247],a[92:95],v[164:167]\n"
"ds_read2st64_b32 v[80:81],v13 offset0:216 offset1:217\n"
"v_add_f32 v20,v20,v60\n"
MF "v[156:159],a[244:247],a[88:91],v[252:255]\n"
"ds_read2st64_b32 v[82:83],v13 offset0:218 offset1:219\n"
"v_add_f32 v21,v21,v61\n"
MF "v[156:159],a[240:243],a[48:51],v[156:159]\n"
"ds_read2st64_b32 v[84:85],v13 offset0:220 offset1:221\n"
"v_add_f32 v22,v22,v62\n"
MF "v[156:159],v[188:191],a[8:11],v[156:159]\n"
"ds_read2st64_b32 v[86:87],v13 offset0:222 offset1:223\n"
"v_add_f32 v23,v23,v63\n"
MF "v[148:151],v[188:191],a[4:7],v[252:255]\n"
"s_waitcnt vmcnt(5)\n"
"v_mov_b32 v14,s76\n"
"s_add_u32 s76,s76,1\n"
MF "v[148:151],a[240:243],a[44:47],v[148:151]\n"
"buffer_store_dword v14,v15,s[40:43],s94 offen sc1\n"
MF "v[148:151],a[244:247],a[84:87],v[148:151]\n"
"ds_read2st64_b32 v[88:89],v13 offset0:224 offset1:225\n"
CV "v16,v16,v17\n"
MF "v[140:143],a[244:247],a[80:83],v[252:255]\n"
"ds_read2st64_b32 v[90:91],v13 offset0:226 offset1:227\n"
CV "v17,v18,v19\n"
MF "v[140:143],a[240:243],a[40:43],v[140:143]\n"
"ds_read2st64_b32 v[92:93],v13 offset0:228 offset1:229\n"
CV "v18,v20,v21\n"
MF "v[140:143],v[188:191],a[0:3],v[140:143]\n"
"s_waitcnt lgkmcnt(9)\n"
"ds_read2st64_b32 v[94:95],v13 offset0:230 offset1:231\n"
CV "v19,v22,v23\n"
MF "v[96:99],v[176:179],a[120:123],v[96:99]\n"
"s_nop 0\n"
TR "v[188:189],v1\n"
TR "v[190:191],v1 offset:2048\n"
MF "v[96:99],v[180:183],a[160:163],v[96:99]\n"
"s_nop 0\n"
TR "a[240:241],v1 offset:4096\n"
TR "a[242:243],v1 offset:6144\n"
MF "v[96:99],v[184:187],a[200:203],v[96:99]\n"
"s_add_u32 s87,s90,0\n"
TR "a[244:245],v1 offset:8192\n"
TR "a[246:247],v1 offset:10240\n"
MF "v[104:107],v[184:187],a[204:207],v[104:107]\n"
"buffer_store_dwordx4 v[16:19],v9,s[40:43],s87 offen sc1\n"
MF "v[104:107],v[180:183],a[164:167],v[104:107]\n"
"v_add_f32 v24,v24,v64\n"
"v_add_f32 v25,v25,v65\n"
MF "v[104:107],v[176:179],a[124:127],v[104:107]\n"
"v_add_f32 v26,v26,v66\n"
"v_add_f32 v27,v27,v67\n"
MF "v[112:115],v[176:179],a[128:131],v[112:115]\n"
"v_add_f32 v28,v28,v68\n"
"v_add_f32 v29,v29,v69\n"
MF "v[112:115],v[180:183],a[168:171],v[112:115]\n"
"v_add_f32 v30,v30,v70\n"
"v_add_f32 v31,v31,v71\n"
MF "v[112:115],v[184:187],a[208:211],v[112:115]\n"
CV "v24,v24,v25\n"
CV "v25,v26,v27\n"
MF "v[120:123],v[184:187],a[212:215],v[120:123]\n"
"s_add_u32 s87,s90,1024\n"
CV "v26,v28,v29\n"
CV "v27,v30,v31\n"
MF "v[120:123],v[180:183],a[172:175],v[120:123]\n"
"s_waitcnt lgkmcnt(14)\n"
"buffer_store_dwordx4 v[24:27],v9,s[40:43],s87 offen sc1\n"
MF "v[120:123],v[176:179],a[132:135],v[120:123]\n"
"v_add_f32 v32,v32,v72\n"
"v_add_f32 v33,v33,v73\n"
MF "v[128:131],v[176:179],a[136:139],v[128:131]\n"
"v_add_f32 v34,v34,v74\n"
"v_add_f32 v35,v35,v75\n"
MF "v[128:131],v[180:183],a[176:179],v[128:131]\n"
"v_add_f32 v36,v36,v76\n"
"v_add_f32 v37,v37,v77\n"
MF "v[128:131],v[184:187],a[216:219],v[128:131]\n"
"s_waitcnt lgkmcnt(0)\n"
"v_add_f32 v38,v38,v78\n"
"v_add_f32 v39,v39,v79\n"
MF "v[132:135],a[244:247],a[216:219],v[132:135]\n"
"s_nop 0\n"
TR "v[176:177],v2\n"
TR "v[178:179],v2 offset:2048\n"
MF "v[132:135],a[240:243],a[176:179],v[132:135]\n"
"s_nop 0\n"
TR "v[180:181],v2 offset:4096\n"
TR "v[182:183],v2 offset:6144\n"
MF "v[132:135],v[188:191],a[136:139],v[132:135]\n"
"s_nop 0\n"
TR "v[184:185],v2 offset:8192\n"
TR "v[186:187],v2 offset:10240\n"
MF "v[124:127],v[188:191],a[132:135],v[124:127]\n"
CV "v32,v32,v33\n"
CV "v33,v34,v35\n"
MF "v[124:127],a[240:243],a[172:175],v[124:127]\n"
"s_add_u32 s87,s90,2048\n"
CV "v34,v36,v37\n"
CV "v35,v38,v39\n"
MF "v[124:127],a[244:247],a[212:215],v[124:127]\n"
"buffer_store_dwordx4 v[32:35],v9,s[40:43],s87 offen sc1\n"
MF "v[116:119],a[244:247],a[208:211],v[116:119]\n"
"v_add_f32 v40,v40,v80\n"
"v_add_f32 v41,v41,v81\n"
MF "v[116:119],a[240:243],a[168:171],v[116:119]\n"
"v_add_f32 v42,v42,v82\n"
"v_add_f32 v43,v43,v83\n"
MF "v[116:119],v[188:191],a[128:131],v[116:119]\n"
"v_add_f32 v44,v44,v84\n"
"v_add_f32 v45,v45,v85\n"
MF "v[108:111],v[188:191],a[124:127],v[108:111]\n"
"v_add_f32 v46,v46,v86\n"
"v_add_f32 v47,v47,v87\n"
MF "v[108:111],a[240:243],a[164:167],v[108:111]\n"
CV "v40,v40,v41\n"
CV "v41,v42,v43\n"
MF "v[108:111],a[244:247],a[204:207],v[108:111]\n"
"s_add_u32 s87,s90,3072\n"
CV "v42,v44,v45\n"
CV "v43,v46,v47\n"
MF "v[100:103],a[244:247],a[200:203],v[100:103]\n"
"buffer_store_dwordx4 v[40:43],v9,s[40:43],s87 offen sc1\n"
MF "v[100:103],a[240:243],a[160:163],v[100:103]\n"
"v_add_f32 v48,v48,v88\n"
"v_add_f32 v49,v49,v89\n"
MF "v[100:103],v[188:191],a[120:123],v[100:103]\n"
"s_waitcnt lgkmcnt(0)\n"
"v_add_f32 v50,v50,v90\n"
"v_add_f32 v51,v51,v91\n"
MF "v[136:139],v[176:179],a[120:123],v[136:139]\n"
"s_nop 0\n"
TR "v[188:189],v3\n"
TR "v[190:191],v3 offset:2048\n"
MF "v[136:139],v[180:183],a[160:163],v[136:139]\n"
"s_nop 0\n"
TR "a[240:241],v3 offset:4096\n"
TR "a[242:243],v3 offset:6144\n"
MF "v[136:139],v[184:187],a[200:203],v[136:139]\n"
"s_nop 0\n"
TR "a[244:245],v3 offset:8192\n"
TR "a[246:247],v3 offset:10240\n"
MF "v[144:147],v[184:187],a[204:207],v[144:147]\n"
"v_add_f32 v52,v52,v92\n"
"v_add_f32 v53,v53,v93\n"
MF "v[144:147],v[180:183],a[164:167],v[144:147]\n"
"v_add_f32 v54,v54,v94\n"
"v_add_f32 v55,v55,v95\n"
MF "v[144:147],v[176:179],a[124:127],v[144:147]\n"
CV "v48,v48,v49\n"
CV "v49,v50,v51\n"
MF "v[152:155],v[176:179],a[128:131],v[152:155]\n"
"s_add_u32 s87,s90,4096\n"
CV "v50,v52,v53\n"
CV "v51,v54,v55\n"
MF "v[152:155],v[180:183],a[168:171],v[152:155]\n"
"buffer_store_dwordx4 v[48:51],v9,s[40:43],s87 offen sc1\n"
MF "v[152:155],v[184:187],a[208:211],v[152:155]\n"
MF "v[160:163],v[184:187],a[212:215],v[160:163]\n"
MF "v[160:163],v[180:183],a[172:175],v[160:163]\n"
MF "v[160:163],v[176:179],a[132:135],v[160:163]\n"
MF "v[168:171],v[176:179],a[136:139],v[168:171]\n"
MF "v[168:171],v[180:183],a[176:179],v[168:171]\n"
MF "v[168:171],v[184:187],a[216:219],v[168:171]\n"
"s_waitcnt lgkmcnt(0)\n"
"s_barrier\n"
MF "v[172:175],a[244:247],a[216:219],v[172:175]\n"
"s_nop 0\n"
TR "v[176:177],v0 offset:49152\n"
TR "v[178:179],v0 offset:51200\n"
MF "v[172:175],a[240:243],a[176:179],v[172:175]\n"
"s_nop 0\n"
TR "v[180:181],v0 offset:53248\n"
TR "v[182:183],v0 offset:55296\n"
MF "v[172:175],v[188:191],a[136:139],v[172:175]\n"
"s_nop 0\n"
TR "v[184:185],v0 offset:57344\n"
TR "v[186:187],v0 offset:59392\n"
MF "v[164:167],v[188:191],a[132:135],v[164:167]\n"
"buffer_load_dword v10,v12,s[40:43],s86 offen sc1\n"
MF "v[164:167],a[240:243],a[172:175],v[164:167]\n"
MF "v[164:167],a[244:247],a[212:215],v[164:167]\n"
MF "v[156:159],a[244:247],a[208:211],v[156:159]\n"
MF "v[156:159],a[240:243],a[168:171],v[156:159]\n"
MF "v[156:159],v[188:191],a[128:131],v[156:159]\n"
MF "v[148:151],v[188:191],a[124:127],v[148:151]\n"
MF "v[148:151],a[240:243],a[164:167],v[148:151]\n"
MF "v[148:151],a[244:247],a[204:207],v[148:151]\n"
MF "v[140:143],a[244:247],a[200:203],v[140:143]\n"
MF "v[140:143],a[240:243],a[160:163],v[140:143]\n"
MF "v[140:143],v[188:191],a[120:123],v[140:143]\n"
"s_waitcnt lgkmcnt(0)\n"
MF "v[96:99],v[176:179],a[20:23],v[96:99]\n"
"s_nop 0\n"
TR "v[188:189],v1 offset:49152\n"
TR "v[190:191],v1 offset:51200\n"
MF "v[96:99],v[180:183],a[60:63],v[96:99]\n"
"s_nop 0\n"
TR "a[240:241],v1 offset:53248\n"
TR "a[242:243],v1 offset:55296\n"
MF "v[96:99],v[184:187],a[100:103],v[96:99]\n"
"s_nop 0\n"
TR "a[244:245],v1 offset:57344\n"
TR "a[246:247],v1 offset:59392\n"
MF "v[104:107],v[184:187],a[104:107],v[104:107]\n"
MF "v[104:107],v[180:183],a[64:67],v[104:107]\n"
MF "v[104:107],v[176:179],a[24:27],v[104:107]\n"
"s_waitcnt vmcnt(7)\n"
MF "v[136:139],v[208:211],a[252:255],v[136:139]\n"
MF "v[144:147],v[212:215],a[252:255],v[144:147]\n"
MF "v[152:155],v[216:219],a[252:255],v[152:155]\n"
MF "v[160:163],v[220:223],a[252:255],v[160:163]\n"
MF "v[168:171],v[224:227],a[252:255],v[168:171]\n"
MF "v[140:143],v[208:211],a[248:251],v[140:143]\n"
MF "v[148:151],v[212:215],a[248:251],v[148:151]\n"
MF "v[156:159],v[216:219],a[248:251],v[156:159]\n"
MF "v[164:167],v[220:223],a[248:251],v[164:167]\n"
MF "v[172:175],v[224:227],a[248:251],v[172:175]\n"
MF "v[112:115],v[176:179],a[28:31],v[112:115]\n"
MF "v[112:115],v[180:183],a[68:71],v[112:115]\n"
MF "v[112:115],v[184:187],a[108:111],v[112:115]\n"
MF "v[120:123],v[184:187],a[112:115],v[120:123]\n"
MF "v[120:123],v[180:183],a[72:75],v[120:123]\n"
"s_bfe_u32 s87,s22,0x30014\n"
MF "v[120:123],v[176:179],a[32:35],v[120:123]\n"
"s_lshl_b32 s87,s87,7\n"
MF "v[128:131],v[176:179],a[36:39],v[128:131]\n"
"s_and_b32 s60,s56,1\n"
MF "v[128:131],v[180:183],a[76:79],v[128:131]\n"
"s_mul_i32 s60,s60,204800\n"
MF "v[128:131],v[184:187],a[116:119],v[128:131]\n"
"s_waitcnt lgkmcnt(0)\n"
MF "v[132:135],a[244:247],a[116:119],v[132:135]\n"
"s_add_u32 s87,s87,s60\n"
TR "v[176:177],v2 offset:49152\n"
TR "v[178:179],v2 offset:51200\n"
MF "v[132:135],a[240:243],a[76:79],v[132:135]\n"
"s_mov_b32 s71,204800\n"
TR "v[180:181],v2 offset:53248\n"
TR "v[182:183],v2 offset:55296\n"
MF "v[132:135],v[188:191],a[36:39],v[132:135]\n"
"s_mov_b32 s66,204800\n"
TR "v[184:185],v2 offset:57344\n"
TR "v[186:187],v2 offset:59392\n"
MF "v[124:127],v[188:191],a[32:35],v[124:127]\n"
"s_add_u32 s72,s78,2\n"
MF "v[124:127],a[240:243],a[72:75],v[124:127]\n"
"s_and_b32 s72,s72,15\n"
MF "v[124:127],a[244:247],a[112:115],v[124:127]\n"
"s_bfe_u32 s60,s22,0x10014\n"
MF "v[116:119],a[244:247],a[108:111],v[116:119]\n"
"s_lshl_b32 s60,s60,5\n"
MF "v[116:119],a[240:243],a[68:71],v[116:119]\n"
"v_xor_b32 v242,s60,v8\n"
MF "v[116:119],v[188:191],a[28:31],v[116:119]\n"
MF "v[108:111],v[188:191],a[24:27],v[108:111]\n"
"s_add_u32 s16,s78,6\n"
MF "v[108:111],a[240:243],a[64:67],v[108:111]\n"
"s_and_b32 s16,s16,15\n"
MF "v[108:111],a[244:247],a[104:107],v[108:111]\n"
"s_add_u32 s17,s78,4\n"
MF "v[100:103],a[244:247],a[100:103],v[100:103]\n"
"s_and_b32 s17,s17,15\n"
MF "v[100:103],a[240:243],a[60:63],v[100:103]\n"
MF "v[100:103],v[188:191],a[20:23],v[100:103]\n"
"s_waitcnt lgkmcnt(0)\n"
MF "v[136:139],v[176:179],a[20:23],v[136:139]\n"
"s_nop 0\n"
TR "v[188:189],v3 offset:49152\n"
TR "v[190:191],v3 offset:51200\n"
MF "v[136:139],v[180:183],a[60:63],v[136:139]\n"
"s_nop 0\n"
TR "a[240:241],v3 offset:53248\n"
TR "a[242:243],v3 offset:55296\n"
MF "v[136:139],v[184:187],a[100:103],v[136:139]\n"
"s_nop 0\n"
TR "a[244:245],v3 offset:57344\n"
TR "a[246:247],v3 offset:59392\n"
MF "v[144:147],v[184:187],a[104:107],v[144:147]\n"
MF "v[144:147],v[180:183],a[64:67],v[144:147]\n"
"v_mov_b32 v14,s76\n"
"s_add_u32 s76,s76,1\n"
MF "v[144:147],v[176:179],a[24:27],v[144:147]\n"
MF "v[152:155],v[176:179],a[28:31],v[152:155]\n"
MF "v[152:155],v[180:183],a[68:71],v[152:155]\n"
MF "v[152:155],v[184:187],a[108:111],v[152:155]\n"
MF "v[160:163],v[184:187],a[112:115],v[160:163]\n"
MF "v[160:163],v[180:183],a[72:75],v[160:163]\n"
MF "v[160:163],v[176:179],a[32:35],v[160:163]\n"
MF "v[168:171],v[176:179],a[36:39],v[168:171]\n"
MF "v[168:171],v[180:183],a[76:79],v[168:171]\n"
MF "v[168:171],v[184:187],a[116:119],v[168:171]\n"
"s_waitcnt vmcnt(0) lgkmcnt(0)\n"
"s_barrier\n"
MF "v[172:175],a[244:247],a[116:119],v[172:175]\n"
"buffer_store_dword v14,v15,s[40:43],s94 offen sc1\n"
MF "v[172:175],a[240:243],a[76:79],v[172:175]\n"
"s_nop 0\n"
TR "v[176:177],v6\n"
TR "v[178:179],v6 offset:2048\n"
MF "v[172:175],v[188:191],a[36:39],v[172:175]\n"
"s_nop 0\n"
TR "v[180:181],v6 offset:4096\n"
TR "v[182:183],v6 offset:6144\n"
MF "v[164:167],v[188:191],a[32:35],v[164:167]\n"
"s_nop 0\n"
TR "v[184:185],v6 offset:8192\n"
TR "v[186:187],v6 offset:10240\n"
MF "v[164:167],a[240:243],a[72:75],v[164:167]\n"
"v_sub_u32 v10,v10,v11\n"
MF "v[164:167],a[244:247],a[112:115],v[164:167]\n"
"v_readlane_b32 s16,v10,s16\n"
MF "v[156:159],a[244:247],a[108:111],v[156:159]\n"
"s_cmp_lt_u32 s16,9\n"
"s_cselect_b32 s16,0xf0000000,0\n"
MF "v[156:159],a[240:243],a[68:71],v[156:159]\n"
"v_readlane_b32 s17,v10,s17\n"
MF "v[156:159],v[188:191],a[28:31],v[156:159]\n"
"s_cmp_lt_u32 s17,10\n"
"s_cselect_b32 s17,0xf0000000,0\n"
MF "v[148:151],v[188:191],a[24:27],v[148:151]\n"
"s_bfe_u32 s62,s22,0x30014\n"
MF "v[148:151],a[240:243],a[64:67],v[148:151]\n"
"s_add_u32 s62,s62,3\n"
"s_and_b32 s62,s62,7\n"
"s_mul_i32 s62,s62,204800\n"
MF "v[148:151],a[244:247],a[104:107],v[148:151]\n"
"s_add_u32 s62,s62,s73\n"
"s_add_u32 s62,s62,122880\n"
"s_or_b32 s62,s62,s16\n"
MF "v[140:143],a[244:247],a[100:103],v[140:143]\n"
LD "v[56:59],v9,s[40:43],s62 offen sc1\n"
MF "v[140:143],a[240:243],a[60:63],v[140:143]\n"
LD "v[64:67],v9,s[40:43],s62 offen offset:1024 sc1\n"
MF "v[140:143],v[188:191],a[20:23],v[140:143]\n"
"s_waitcnt lgkmcnt(0)\n"
LD "v[72:75],v9,s[40:43],s62 offen offset:2048 sc1\n"
MF "v[136:139],v[176:179],a[140:143],v[136:139]\n"
"s_nop 0\n"
TR "v[188:189],v7\n"
TR "v[190:191],v7 offset:2048\n"
MF "v[136:139],v[180:183],a[180:183],v[136:139]\n"
"s_nop 0\n"
TR "a[240:241],v7 offset:4096\n"
TR "a[242:243],v7 offset:6144\n"
MF "v[136:139],v[184:187],a[220:223],v[136:139]\n"
"s_nop 0\n"
TR "a[244:245],v7 offset:8192\n"
TR "a[246:247],v7 offset:10240\n"
MF "v[144:147],v[184:187],a[224:227],v[144:147]\n"
LD "v[80:83],v9,s[40:43],s62 offen offset:3072 sc1\n"
MF "v[144:147],v[180:183],a[184:187],v[144:147]\n"
MF "v[144:147],v[176:179],a[144:147],v[144:147]\n"
"s_add_u32 s62,s62,4096\n"
MF "v[152:155],v[176:179],a[148:151],v[152:155]\n"
LD "v[88:91],v9,s[40:43],s62 offen sc1\n"
MF "v[152:155],v[180:183],a[188:191],v[152:155]\n"
"s_bfe_u32 s62,s22,0x30014\n"
"ds_write_addtid_b32 v136\n"
"ds_write_addtid_b32 v137 offset:256\n"
MF "v[152:155],v[184:187],a[228:231],v[152:155]\n"
"s_add_u32 s62,s62,2\n"
"s_and_b32 s62,s62,7\n"
"s_mul_i32 s62,s62,204800\n"
MF "v[160:163],v[184:187],a[232:235],v[160:163]\n"
"s_add_u32 s62,s62,s73\n"
"s_add_u32 s62,s62,143360\n"
"s_or_b32 s62,s62,s17\n"
MF "v[160:163],v[180:183],a[192:195],v[160:163]\n"
LD "v[60:63],v9,s[40:43],s62 offen sc1\n"
MF "v[160:163],v[176:179],a[152:155],v[160:163]\n"
"s_nop 0\n"
"ds_write_addtid_b32 v138 offset:512\n"
"ds_write_addtid_b32 v139 offset:768\n"
MF "v[168:171],v[176:179],a[156:159],v[168:171]\n"
LD "v[68:71],v9,s[40:43],s62 offen offset:1024 sc1\n"
MF "v[168:171],v[180:183],a[196:199],v[168:171]\n"
"s_nop 0\n"
"ds_write_addtid_b32 v144 offset:2048\n"
"ds_write_addtid_b32 v145 offset:2304\n"
MF "v[168:171],v[184:187],a[236:239],v[168:171]\n"
"s_waitcnt lgkmcnt(6)\n"
LD "v[76:79],v9,s[40:43],s62 offen offset:2048 sc1\n"
MF "v[140:143],v[188:191],a[140:143],v[140:143]\n"
"s_nop 0\n"
TR "v[176:177],v4\n"
TR "v[178:179],v4 offset:2048\n"
MF "v[140:143],a[240:243],a[180:183],v[140:143]\n"
"s_nop 0\n"
TR "v[180:181],v4 offset:4096\n"
TR "v[182:183],v4 offset:6144\n"
MF "v[140:143],a[244:247],a[220:223],v[140:143]\n"
"s_nop 0\n"
TR "v[184:185],v4 offset:8192\n"
TR "v[186:187],v4 offset:10240\n"
MF "v[148:151],a[244:247],a[224:227],v[148:151]\n"
LD "v[84:87],v9,s[40:43],s62 offen offset:3072 sc1\n"
MF "v[148:151],a[240:243],a[184:187],v[148:151]\n"
"s_nop 0\n"
"ds_write_addtid_b32 v146 offset:2560\n"
"ds_write_addtid_b32 v147 offset:2816\n"
MF "v[148:151],v[188:191],a[144:147],v[148:151]\n"
"s_add_u32 s62,s62,4096\n"
"ds_write_addtid_b32 v152 offset:4096\n"
"ds_write_addtid_b32 v153 offset:4352\n"
MF "v[156:159],v[188:191],a[148:151],v[156:159]\n"
LD "v[92:95],v9,s[40:43],s62 offen sc1\n"
MF "v[156:159],a[240:243],a[188:191],v[156:159]\n"
"s_nop 0\n"
"ds_write_addtid_b32 v154 offset:4608\n"
"ds_write_addtid_b32 v155 offset:4864\n"
MF "v[156:159],a[244:247],a[228:231],v[156:159]\n"
"s_nop 0\n"
"ds_write_addtid_b32 v160 offset:6144\n"
"ds_write_addtid_b32 v161 offset:6400\n"
MF "v[164:167],a[244:247],a[232:235],v[164:167]\n"
"s_nop 0\n"
"ds_write_addtid_b32 v162 offset:6656\n"
"ds_write_addtid_b32 v163 offset:6912\n"
MF "v[164:167],a[240:243],a[192:195],v[164:167]\n"
"s_nop 0\n"
"ds_write_addtid_b32 v168 offset:8192\n"
"ds_write_addtid_b32 v169 offset:8448\n"
MF "v[164:167],v[188:191],a[152:155],v[164:167]\n"
"buffer_load_dword v10,v12,s[40:43],s86 offen sc1\n"
MF "v[172:175],v[188:191],a[156:159],v[172:175]\n"
"s_nop 0\n"
"ds_write_addtid_b32 v170 offset:8704\n"
"ds_write_addtid_b32 v171 offset:8960\n"
MF "v[172:175],a[240:243],a[196:199],v[172:175]\n"
"s_nop 0\n"
"ds_write_addtid_b32 v140 offset:1024\n"
"ds_write_addtid_b32 v141 offset:1280\n"
MF "v[172:175],a[244:247],a[236:239],v[172:175]\n"
"s_waitcnt lgkmcnt(15)\n"
"ds_write_addtid_b32 v142 offset:1536\n"
"ds_write_addtid_b32 v143 offset:1792\n"
MF "v[96:99],v[176:179],a[140:143],v[96:99]\n"
"s_nop 0\n"
TR "v[188:189],v5\n"
TR "v[190:191],v5 offset:2048\n"
MF "v[96:99],v[180:183],a[180:183],v[96:99]\n"
"s_nop 0\n"
TR "a[240:241],v5 offset:4096\n"
TR "a[242:243],v5 offset:6144\n"
MF "v[96:99],v[184:187],a[220:223],v[96:99]\n"
"s_nop 0\n"
TR "a[244:245],v5 offset:8192\n"
TR "a[246:247],v5 offset:10240\n"
MF "v[104:107],v[184:187],a[224:227],v[104:107]\n"
"s_nop 0\n"
"ds_write_addtid_b32 v148 offset:3072\n"
"ds_write_addtid_b32 v149 offset:3328\n"
MF "v[104:107],v[180:183],a[184:187],v[104:107]\n"
"s_nop 0\n"
"ds_write_addtid_b32 v150 offset:3584\n"
"ds_write_addtid_b32 v151 offset:3840\n"
MF "v[104:107],v[176:179],a[144:147],v[104:107]\n"
"s_nop 0\n"
"ds_write_addtid_b32 v156 offset:5120\n"
"ds_write_addtid_b32 v157 offset:5376\n"
MF "v[112:115],v[176:179],a[148:151],v[112:115]\n"
"s_nop 0\n"
"ds_write_addtid_b32 v158 offset:5632\n"
"ds_write_addtid_b32 v159 offset:5888\n"
MF "v[112:115],v[180:183],a[188:191],v[112:115]\n"
"ds_write_addtid_b32 v164 offset:7168\n"
MF "v[112:115],v[184:187],a[228:231],v[112:115]\n"
"ds_write_addtid_b32 v165 offset:7424\n"
MF "v[120:123],v[184:187],a[232:235],v[120:123]\n"
"ds_write_addtid_b32 v166 offset:7680\n"
MF "v[120:123],v[180:183],a[192:195],v[120:123]\n"
"ds_write_addtid_b32 v167 offset:7936\n"
MF "v[120:123],v[176:179],a[152:155],v[120:123]\n"
"ds_write_addtid_b32 v172 offset:9216\n"
MF "v[128:131],v[176:179],a[156:159],v[128:131]\n"
"ds_write_addtid_b32 v173 offset:9472\n"
MF "v[128:131],v[180:183],a[196:199],v[128:131]\n"
"ds_write_addtid_b32 v174 offset:9728\n"
MF "v[128:131],v[184:187],a[236:239],v[128:131]\n"
"s_waitcnt lgkmcnt(15)\n"
"ds_write_addtid_b32 v175 offset:9984\n"
MF "v[100:103],v[188:191],a[140:143],v[100:103]\n"
MF "v[100:103],a[240:243],a[180:183],v[100:103]\n"
MF "v[100:103],a[244:247],a[220:223],v[100:103]\n"
MF "v[108:111],a[244:247],a[224:227],v[108:111]\n"
MF "v[108:111],a[240:243],a[184:187],v[108:111]\n"
"s_waitcnt lgkmcnt(0)\n"
"s_barrier\n"
MF "v[108:111],v[188:191],a[144:147],v[108:111]\n"
"ds_read2st64_b32 v[16:17],v13 offset0:192 offset1:193\n"
MF "v[116:119],v[188:191],a[148:151],v[116:119]\n"
"ds_read2st64_b32 v[18:19],v13 offset0:194 offset1:195\n"
MF "v[116:119],a[240:243],a[188:191],v[116:119]\n"
"ds_read2st64_b32 v[20:21],v13 offset0:196 offset1:197\n"
MF "v[116:119],a[244:247],a[228:231],v[116:119]\n"
"ds_read2st64_b32 v[22:23],v13 offset0:198 offset1:199\n"
MF "v[124:127],a[244:247],a[232:235],v[124:127]\n"
"ds_read2st64_b32 v[24:25],v13 offset0:200 offset1:201\n"
MF "v[124:127],a[240:243],a[192:195],v[124:127]\n"
"ds_read2st64_b32 v[26:27],v13 offset0:202 offset1:203\n"
MF "v[124:127],v[188:191],a[152:155],v[124:127]\n"
"ds_read2st64_b32 v[28:29],v13 offset0:204 offset1:205\n"
MF "v[132:135],v[188:191],a[156:159],v[132:135]\n"
"ds_read2st64_b32 v[30:31],v13 offset0:206 offset1:207\n"
MF "v[132:135],a[240:243],a[196:199],v[132:135]\n"
"ds_read2st64_b32 v[32:33],v13 offset0:208 offset1:209\n"
MF "v[132:135],a[244:247],a[236:239],v[132:135]\n"
"ds_read2st64_b32 v[34:35],v13 offset0:210 offset1:211\n"
"ds_read2st64_b32 v[36:37],v13 offset0:212 offset1:213\n"
"ds_read2st64_b32 v[38:39],v13 offset0:214 offset1:215\n"
"ds_read2st64_b32 v[40:41],v13 offset0:216 offset1:217\n"
"ds_read2st64_b32 v[42:43],v13 offset0:218 offset1:219\n"
"ds_read2st64_b32 v[44:45],v13 offset0:220 offset1:221\n"
"ds_read2st64_b32 v[46:47],v13 offset0:222 offset1:223\n"
"ds_read2st64_b32 v[48:49],v13 offset0:224 offset1:225\n"
"ds_read2st64_b32 v[50:51],v13 offset0:226 offset1:227\n"
"ds_read2st64_b32 v[52:53],v13 offset0:228 offset1:229\n"
"ds_read2st64_b32 v[54:55],v13 offset0:230 offset1:231\n"
"s_waitcnt vmcnt(0)\n"
"v_sub_u32 v10,v10,v11\n"
"s_cmp_eq_u32 s16,0\n"
"s_cbranch_scc1 .Lhave13\n"
"s_add_u32 s59,s78,6\n"
"s_and_b32 s59,s59,15\n"
"s_nop 3\n"
".Lnf7:\n"
"v_readlane_b32 s58,v10,s59\n"
"s_cmp_lt_u32 s58,9\n"
"s_cbranch_scc1 .Lsp7\n"
".Lnb7:\n"
"s_bfe_u32 s62,s22,0x30014\n"
"s_add_u32 s62,s62,3\n"
"s_and_b32 s62,s62,7\n"
"s_mul_i32 s62,s62,204800\n"
"s_add_u32 s62,s62,s73\n"
"s_add_u32 s62,s62,122880\n"
LD "v[56:59],v9,s[40:43],s62 offen sc1\n"
LD "v[64:67],v9,s[40:43],s62 offen offset:1024 sc1\n"
LD "v[72:75],v9,s[40:43],s62 offen offset:2048 sc1\n"
LD "v[80:83],v9,s[40:43],s62 offen offset:3072 sc1\n"
"s_add_u32 s62,s62,4096\n"
LD "v[88:91],v9,s[40:43],s62 offen sc1\n"
".Lhave13:\n"
"s_cmp_eq_u32 s17,0\n"
"s_cbranch_scc1 .Lhave12\n"
"s_add_u32 s59,s78,4\n"
"s_and_b32 s59,s59,15\n"
"s_nop 3\n"
".Lnf8:\n"
"v_readlane_b32 s58,v10,s59\n"
"s_cmp_lt_u32 s58,10\n"
"s_cbranch_scc1 .Lsp8\n"
".Lnb8:\n"
"s_bfe_u32 s62,s22,0x30014\n"
"s_add_u32 s62,s62,2\n"
"s_and_b32 s62,s62,7\n"
"s_mul_i32 s62,s62,204800\n"
"s_add_u32 s62,s62,s73\n"
"s_add_u32 s62,s62,143360\n"
LD "v[60:63],v9,s[40:43],s62 offen sc1\n"
LD "v[68:71],v9,s[40:43],s62 offen offset:1024 sc1\n"
LD "v[76:79],v9,s[40:43],s62 offen offset:2048 sc1\n"
LD "v[84:87],v9,s[40:43],s62 offen offset:3072 sc1\n"
"s_add_u32 s62,s62,4096\n"
LD "v[92:95],v9,s[40:43],s62 offen sc1\n"
".Lhave12:\n"
".Lnf9:\n"
"v_readlane_b32 s58,v10,s72\n"
"s_cmp_lt_u32 s58,11\n"
"s_cbranch_scc1 .Lsp9\n"
".Lnb9:\n"
"s_bfe_u32 s62,s22,0x30014\n"
"s_add_u32 s62,s62,1\n"
"s_and_b32 s62,s62,7\n"
"s_mul_i32 s62,s62,204800\n"
"s_add_u32 s62,s62,s73\n"
"s_add_u32 s62,s62,163840\n"
LD "v[208:211],v9,s[40:43],s62 offen sc1\n"
LD "v[212:215],v9,s[40:43],s62 offen offset:1024 sc1\n"
LD "v[216:219],v9,s[40:43],s62 offen offset:2048 sc1\n"
LD "v[220:223],v9,s[40:43],s62 offen offset:3072 sc1\n"
"s_add_u32 s62,s62,4096\n"
LD "v[224:227],v9,s[40:43],s62 offen sc1\n"
"s_waitcnt lgkmcnt(15)\n"
"v_pk_add_f32 v[96:97],v[96:97],v[16:17]\n"
"v_pk_add_f32 v[98:99],v[98:99],v[18:19]\n"
"v_pk_add_f32 v[100:101],v[100:101],v[20:21]\n"
"v_pk_add_f32 v[102:103],v[102:103],v[22:23]\n"
"v_pk_add_f32 v[104:105],v[104:105],v[24:25]\n"
"s_waitcnt lgkmcnt(14)\n"
"v_pk_add_f32 v[106:107],v[106:107],v[26:27]\n"
"s_waitcnt lgkmcnt(13)\n"
"v_pk_add_f32 v[108:109],v[108:109],v[28:29]\n"
"s_waitcnt lgkmcnt(12)\n"
"v_pk_add_f32 v[110:111],v[110:111],v[30:31]\n"
"s_waitcnt lgkmcnt(11)\n"
"v_pk_add_f32 v[112:113],v[112:113],v[32:33]\n"
"s_waitcnt lgkmcnt(10)\n"
"v_pk_add_f32 v[114:115],v[114:115],v[34:35]\n"
"s_waitcnt lgkmcnt(9)\n"
"v_pk_add_f32 v[116:117],v[116:117],v[36:37]\n"
"s_waitcnt lgkmcnt(8)\n"
"v_pk_add_f32 v[118:119],v[118:119],v[38:39]\n"
"s_waitcnt lgkmcnt(7)\n"
"v_pk_add_f32 v[120:121],v[120:121],v[40:41]\n"
"s_waitcnt lgkmcnt(6)\n"
"v_pk_add_f32 v[122:123],v[122:123],v[42:43]\n"
"s_waitcnt lgkmcnt(5)\n"
"v_pk_add_f32 v[124:125],v[124:125],v[44:45]\n"
"s_waitcnt lgkmcnt(4)\n"
"v_pk_add_f32 v[126:127],v[126:127],v[46:47]\n"
"s_waitcnt lgkmcnt(3)\n"
"v_pk_add_f32 v[128:129],v[128:129],v[48:49]\n"
"s_waitcnt lgkmcnt(2)\n"
"v_pk_add_f32 v[130:131],v[130:131],v[50:51]\n"
"s_waitcnt lgkmcnt(1)\n"
"v_pk_add_f32 v[132:133],v[132:133],v[52:53]\n"
"s_waitcnt lgkmcnt(0)\n"
"v_pk_add_f32 v[134:135],v[134:135],v[54:55]\n"
"s_waitcnt vmcnt(5)\n"
"s_nop 0\n"
MF "v[100:103],v[56:59],a[252:255],v[100:103]\n"
MF "v[108:111],v[64:67],a[252:255],v[108:111]\n"
MF "v[116:119],v[72:75],a[252:255],v[116:119]\n"
MF "v[124:127],v[80:83],a[252:255],v[124:127]\n"
MF "v[132:135],v[88:91],a[252:255],v[132:135]\n"
MF "v[96:99],v[56:59],a[248:251],v[96:99]\n"
MF "v[104:107],v[64:67],a[248:251],v[104:107]\n"
MF "v[112:115],v[72:75],a[248:251],v[112:115]\n"
MF "v[120:123],v[80:83],a[248:251],v[120:123]\n"
MF "v[128:131],v[88:91],a[248:251],v[128:131]\n"
MF "v[96:99],v[60:63],a[252:255],v[96:99]\n"
MF "v[104:107],v[68:71],a[252:255],v[104:107]\n"
MF "v[112:115],v[76:79],a[252:255],v[112:115]\n"
MF "v[120:123],v[84:87],a[252:255],v[120:123]\n"
MF "v[128:131],v[92:95],a[252:255],v[128:131]\n"
MF "v[100:103],v[60:63],a[248:251],v[100:103]\n"
MF "v[108:111],v[68:71],a[248:251],v[108:111]\n"
MF "v[116:119],v[76:79],a[248:251],v[116:119]\n"
MF "v[124:127],v[84:87],a[248:251],v[124:127]\n"
MF "v[132:135],v[92:95],a[248:251],v[132:135]\n"
"s_waitcnt vmcnt(0)\n"
MF "v[100:103],v[208:211],a[252:255],v[100:103]\n"
MF "v[96:99],v[208:211],a[248:251],v[96:99]\n"
MF "v[108:111],v[212:215],a[252:255],v[108:111]\n"
MF "v[104:107],v[212:215],a[248:251],v[104:107]\n"
MF "v[116:119],v[216:219],a[252:255],v[116:119]\n"
"s_nop 4\n"
CV "v96,v96,v97\n"
CV "v97,v98,v99\n"
MF "v[112:115],v[216:219],a[248:251],v[112:115]\n"
CV "v98,v100,v101\n"
CV "v99,v102,v103\n"
MF "v[124:127],v[220:223],a[252:255],v[124:127]\n"
CV "v104,v104,v105\n"
CV "v105,v106,v107\n"
MF "v[120:123],v[220:223],a[248:251],v[120:123]\n"
CV "v106,v108,v109\n"
CV "v107,v110,v111\n"
"v_permlane16_swap_b32 v96,v98\n"
"v_permlane16_swap_b32 v97,v99\n"
"buffer_store_dwordx4 v[96:99],v242,s[44:47],s87 offen sc1\n"
"s_add_u32 s87,s87,40960\n"
MF "v[132:135],v[224:227],a[252:255],v[132:135]\n"
CV "v112,v112,v113\n"
CV "v113,v114,v115\n"
MF "v[128:131],v[224:227],a[248:251],v[128:131]\n"
CV "v114,v116,v117\n"
CV "v115,v118,v119\n"
"v_permlane16_swap_b32 v104,v106\n"
"v_permlane16_swap_b32 v105,v107\n"
"buffer_store_dwordx4 v[104:107],v242,s[44:47],s87 offen sc1\n"
"s_add_u32 s87,s87,40960\n"
CV "v120,v120,v121\n"
CV "v121,v122,v123\n"
CV "v122,v124,v125\n"
CV "v123,v126,v127\n"
"v_permlane16_swap_b32 v112,v114\n"
"v_permlane16_swap_b32 v113,v115\n"
"buffer_store_dwordx4 v[112:115],v242,s[44:47],s87 offen sc1\n"
"s_add_u32 s87,s87,40960\n"
CV "v128,v128,v129\n"
CV "v129,v130,v131\n"
CV "v130,v132,v133\n"
CV "v131,v134,v135\n"
"v_permlane16_swap_b32 v120,v122\n"
"v_permlane16_swap_b32 v121,v123\n"
"buffer_store_dwordx4 v[120:123],v242,s[44:47],s87 offen sc1\n"
"s_add_u32 s87,s87,40960\n"
"v_permlane16_swap_b32 v128,v130\n"
"v_permlane16_swap_b32 v129,v131\n"
"buffer_store_dwordx4 v[128:131],v242,s[44:47],s87 offen sc1\n"
"s_add_u32 s87,s87,40960\n"
"s_branch .Ljobs\n"
".Ljobs:\n"
"s_cbranch_execz .Lipr3\n"
"s_cmp_ge_u32 s92,40\n"
"s_cbranch_scc1 .Ldone\n"
"buffer_load_dword v10,v12,s[40:43],s86 offen sc1\n"
"s_mul_i32 s95,s92,3277\n"
"s_lshr_b32 s95,s95,16\n"
"s_mul_i32 s59,s95,20\n"
"s_sub_u32 s59,s92,s59\n"
"s_cmp_ge_u32 s59,10\n"
"s_cselect_b32 s96,4294557760,0\n"
"s_add_u32 s96,s96,1024\n"
"s_mul_i32 s59,s59,40960\n"
"s_add_u32 s96,s96,s59\n"
"s_lshl_b32 s59,s95,7\n"
"s_add_u32 s96,s96,s59\n"
"s_add_u32 s95,s95,3\n"
"s_waitcnt vmcnt(0)\n"
".Ljtry:\n"
"s_cmp_eq_u32 s98,0\n"
"s_cselect_b32 s95,0,s95\n"
"v_sub_u32 v10,v10,v11\n"
"v_cmp_gt_u32 vcc,s95,v10\n"
"s_lshl_b32 s82,s92,10\n"
"s_add_u32 s82,s82,s93\n"
"s_cmp_eq_u64 vcc,0\n"
"s_cselect_b32 s82,s82,0xf0000000\n"
"s_cselect_b32 s84,s96,0xf0000000\n"
"s_cselect_b32 s59,32,0\n"
"s_add_u32 s92,s92,s59\n"
"s_cmp_eq_u32 s59,0\n"
"s_cbranch_scc0 .Ljgo\n"
"s_sub_u32 s98,s98,1\n"
"s_sleep 2\n"
"s_branch .Ljobs\n"
".Ljgo:\n"
"s_mov_b32 s83,s84\n"
LD "v[208:211],v9,s[40:43],s82 offen sc1\n"
"s_add_u32 s82,s82,s71\n"
LD "v[212:215],v9,s[40:43],s82 offen sc1\n"
"s_add_u32 s82,s82,s71\n"
LD "v[216:219],v9,s[40:43],s82 offen sc1\n"
"s_add_u32 s82,s82,s71\n"
LD "v[220:223],v9,s[40:43],s82 offen sc1\n"
"s_add_u32 s82,s82,s71\n"
LD "v[224:227],v9,s[40:43],s82 offen sc1\n"
"s_add_u32 s82,s82,s66\n"
LD "v[228:231],v9,s[40:43],s82 offen sc1\n"
"s_add_u32 s82,s82,s71\n"
LD "v[232:235],v9,s[40:43],s82 offen sc1\n"
"s_add_u32 s82,s82,s71\n"
LD "v[236:239],v9,s[40:43],s82 offen sc1\n"
"buffer_load_dword v10,v12,s[40:43],s86 offen sc1\n"
"s_waitcnt vmcnt(0)\n"
".Ljfin:\n"
"s_nop 1\n"
"v_mov_b32 v240,v208\n"
"v_mov_b32 v241,v209\n"
MF "v[248:251],v[210:213],a[250:253],0\n"
MF "v[244:247],v[214:217],a[250:253],0\n"
MF "v[248:251],v[218:221],a[250:253],v[248:251]\n"
MF "v[244:247],v[222:225],a[250:253],v[244:247]\n"
MF "v[248:251],v[226:229],a[250:253],v[248:251]\n"
MF "v[244:247],v[230:233],a[250:253],v[244:247]\n"
MF "v[248:251],v[234:237],a[250:253],v[248:251]\n"
MF "v[244:247],v[238:241],a[250:253],v[244:247]\n"
"s_nop 7\n"
CV "v244,v244,v245\n"
CV "v245,v246,v247\n"
CV "v246,v248,v249\n"
CV "v247,v250,v251\n"
"s_nop 0\n"
"v_permlane16_swap_b32 v244,v246\n"
"v_permlane16_swap_b32 v245,v247\n"
"buffer_store_dwordx4 v[244:247],v8,s[44:47],s83 offen sc1\n"
"s_cmp_ge_u32 s92,40\n"
"s_cbranch_scc1 .Ldone\n"
"s_mul_i32 s95,s92,3277\n"
"s_lshr_b32 s95,s95,16\n"
"s_mul_i32 s59,s95,20\n"
"s_sub_u32 s59,s92,s59\n"
"s_cmp_ge_u32 s59,10\n"
"s_cselect_b32 s96,4294557760,0\n"
"s_add_u32 s96,s96,1024\n"
"s_mul_i32 s59,s59,40960\n"
"s_add_u32 s96,s96,s59\n"
"s_lshl_b32 s59,s95,7\n"
"s_add_u32 s96,s96,s59\n"
"s_add_u32 s95,s95,3\n"
"s_branch .Ljtry\n"
".Lsp0:\n"
"s_cmp_eq_u32 s98,0\n"
"s_cbranch_scc1 .Lnb0\n"
"s_sub_u32 s98,s98,1\n"
"s_sleep 2\n"
"buffer_load_dword v10,v12,s[40:43],s86 offen sc1\n"
"s_waitcnt vmcnt(0)\n"
"v_sub_u32 v10,v10,v11\n"
"s_nop 1\n"
"s_branch .Lnf0\n"
".Lsp1:\n"
"s_cmp_eq_u32 s98,0\n"
"s_cbranch_scc1 .Lnb1\n"
"s_sub_u32 s98,s98,1\n"
"s_sleep 2\n"
"buffer_load_dword v10,v12,s[40:43],s86 offen sc1\n"
"s_waitcnt vmcnt(0)\n"
"v_sub_u32 v10,v10,v11\n"
"s_nop 1\n"
"s_branch .Lnf1\n"
".Lsp2:\n"
"s_cmp_eq_u32 s98,0\n"
"s_cbranch_scc1 .Lnb2\n"
"s_sub_u32 s98,s98,1\n"
"s_sleep 2\n"
"buffer_load_dword v10,v12,s[40:43],s86 offen sc1\n"
"s_waitcnt vmcnt(0)\n"
"v_sub_u32 v10,v10,v11\n"
"s_nop 1\n"
"s_branch .Lnf2\n"
".Lsp3:\n"
"s_cmp_eq_u32 s98,0\n"
"s_cbranch_scc1 .Lnb3\n"
"s_sub_u32 s98,s98,1\n"
"s_sleep 2\n"
"buffer_load_dword v10,v12,s[40:43],s86 offen sc1\n"
"s_waitcnt vmcnt(0)\n"
"v_sub_u32 v10,v10,v11\n"
"s_nop 1\n"
"s_branch .Lnf3\n"
".Lsp4:\n"
"s_cmp_eq_u32 s98,0\n"
"s_cbranch_scc1 .Lnb4\n"
"s_sub_u32 s98,s98,1\n"
"s_sleep 2\n"
"buffer_load_dword v10,v12,s[40:43],s86 offen sc1\n"
"s_waitcnt vmcnt(0)\n"
"v_sub_u32 v10,v10,v11\n"
"s_nop 1\n"
"s_branch .Lnf4\n"
".Lsp5:\n"
"s_cmp_eq_u32 s98,0\n"
"s_cbranch_scc1 .Lnb5\n"
"s_sub_u32 s98,s98,1\n"
"s_sleep 2\n"
"buffer_load_dword v10,v12,s[40:43],s86 offen sc1\n"
"s_waitcnt vmcnt(0)\n"
"v_sub_u32 v10,v10,v11\n"
"s_nop 1\n"
"s_branch .Lnf5\n"
".Lsp6:\n"
"s_cmp_eq_u32 s98,0\n"
"s_cbranch_scc1 .Lnb6\n"
"s_sub_u32 s98,s98,1\n"
"s_sleep 2\n"
"buffer_load_dword v10,v12,s[40:43],s86 offen sc1\n"
"s_waitcnt vmcnt(0)\n"
"v_sub_u32 v10,v10,v11\n"
"s_nop 1\n"
"s_branch .Lnf6\n"
".Lsp7:\n"
"s_cmp_eq_u32 s98,0\n"
"s_cbranch_scc1 .Lnb7\n"
"s_sub_u32 s98,s98,1\n"
"s_sleep 2\n"
"buffer_load_dword v10,v12,s[40:43],s86 offen sc1\n"
"s_waitcnt vmcnt(0)\n"
"v_sub_u32 v10,v10,v11\n"
"s_nop 1\n"
"s_branch .Lnf7\n"
".Lsp8:\n"
"s_cmp_eq_u32 s98,0\n"
"s_cbranch_scc1 .Lnb8\n"
"s_sub_u32 s98,s98,1\n"
"s_sleep 2\n"
"buffer_load_dword v10,v12,s[40:43],s86 offen sc1\n"
"s_waitcnt vmcnt(0)\n"
"v_sub_u32 v10,v10,v11\n"
"s_nop 1\n"
"s_branch .Lnf8\n"
".Lsp9:\n"
"s_cmp_eq_u32 s98,0\n"
"s_cbranch_scc1 .Lnb9\n"
"s_sub_u32 s98,s98,1\n"
"s_sleep 2\n"
"buffer_load_dword v10,v12,s[40:43],s86 offen sc1\n"
"s_waitcnt vmcnt(0)\n"
"v_sub_u32 v10,v10,v11\n"
"s_nop 1\n"
"s_branch .Lnf9\n"
".Lqy:\n"
"s_cbranch_execz .Lipr1\n"
MF "v[216:219],a[216:219],a[60:63],v[216:219]\n"
TR "a[208:209],v0 offset:8\n"
MF "v[216:219],a[220:223],a[100:103],v[216:219]\n"
TR "a[210:211],v0 offset:520\n"
MF "v[224:227],a[220:223],a[104:107],v[224:227]\n"
MF "v[224:227],a[216:219],a[64:67],v[224:227]\n"
TR "a[212:213],v0 offset:4104\n"
MF "v[232:235],a[216:219],a[68:71],v[232:235]\n"
TR "a[214:215],v0 offset:4616\n"
MF "v[232:235],a[220:223],a[108:111],v[232:235]\n"
MF "v[240:243],a[220:223],a[112:115],v[240:243]\n"
MF "v[240:243],a[216:219],a[72:75],v[240:243]\n"
MF "v[248:251],a[216:219],a[76:79],v[248:251]\n"
MF "v[248:251],a[220:223],a[116:119],v[248:251]\n"
MF "v[248:251],a[200:203],a[156:159],v[248:251]\n"
TR "a[216:217],v0 offset:16392\n"
MF "v[248:251],a[204:207],a[196:199],v[248:251]\n"
TR "a[218:219],v0 offset:16904\n"
MF "v[240:243],a[204:207],a[192:195],v[240:243]\n"
MF "v[240:243],a[200:203],a[152:155],v[240:243]\n"
TR "a[220:221],v0 offset:20488\n"
MF "v[232:235],a[200:203],a[148:151],v[232:235]\n"
TR "a[222:223],v0 offset:21000\n"
MF "v[232:235],a[204:207],a[188:191],v[232:235]\n"
MF "v[224:227],a[204:207],a[184:187],v[224:227]\n"
MF "v[224:227],a[200:203],a[144:147],v[224:227]\n"
"v_fma_f32 v248,v248,s54,v14\n"
"v_fma_f32 v249,v249,s54,v14\n"
MF "v[216:219],a[200:203],a[140:143],v[216:219]\n"
"v_fma_f32 v250,v250,s54,v14\n"
"v_fma_f32 v251,v251,s54,v14\n"
MF "v[216:219],a[204:207],a[180:183],v[216:219]\n"
"s_waitcnt lgkmcnt(4)\n"
"v_fma_f32 v252,v252,s54,v14\n"
"v_fma_f32 v253,v253,s54,v14\n"
MF "v[180:183],a[208:211],a[60:63],v[180:183]\n"
TR "a[200:201],v0\n"
"v_fma_f32 v254,v254,s54,v14\n"
MF "v[180:183],a[212:215],a[100:103],v[180:183]\n"
TR "a[202:203],v0 offset:512\n"
"v_fma_f32 v255,v255,s54,v14\n"
MF "v[188:191],a[212:215],a[104:107],v[188:191]\n"
CV "v255,v254,v255\n"
CV "v254,v252,v253\n"
MF "v[188:191],a[208:211],a[64:67],v[188:191]\n"
TR "a[204:205],v0 offset:4096\n"
CV "v253,v250,v251\n"
MF "v[196:199],a[208:211],a[68:71],v[196:199]\n"
TR "a[206:207],v0 offset:4608\n"
CV "v252,v248,v249\n"
MF "v[196:199],a[212:215],a[108:111],v[196:199]\n"
"s_add_u32 s58,s75,4096\n"
"v_fma_f32 v240,v240,s54,v14\n"
"v_fma_f32 v241,v241,s54,v14\n"
MF "v[204:207],a[212:215],a[112:115],v[204:207]\n"
"buffer_store_dwordx4 v[252:255],v9,s[40:43],s58 offen offset:1024 sc1\n"
MF "v[204:207],a[208:211],a[72:75],v[204:207]\n"
"v_fma_f32 v242,v242,s54,v14\n"
"v_fma_f32 v243,v243,s54,v14\n"
MF "v[212:215],a[208:211],a[76:79],v[212:215]\n"
"v_fma_f32 v244,v244,s54,v14\n"
"v_fma_f32 v245,v245,s54,v14\n"
MF "v[212:215],a[212:215],a[116:119],v[212:215]\n"
"s_waitcnt lgkmcnt(4)\n"
"v_fma_f32 v246,v246,s54,v14\n"
"v_fma_f32 v247,v247,s54,v14\n"
MF "v[180:183],a[216:219],a[140:143],v[180:183]\n"
TR "a[208:209],v0 offset:16384\n"
CV "v251,v246,v247\n"
MF "v[180:183],a[220:223],a[180:183],v[180:183]\n"
TR "a[210:211],v0 offset:16896\n"
CV "v250,v244,v245\n"
MF "v[188:191],a[220:223],a[184:187],v[188:191]\n"
CV "v249,v242,v243\n"
CV "v248,v240,v241\n"
MF "v[188:191],a[216:219],a[144:147],v[188:191]\n"
TR "a[212:213],v0 offset:20480\n"
"v_fma_f32 v232,v232,s54,v14\n"
MF "v[196:199],a[216:219],a[148:151],v[196:199]\n"
TR "a[214:215],v0 offset:20992\n"
"v_fma_f32 v233,v233,s54,v14\n"
MF "v[196:199],a[220:223],a[188:191],v[196:199]\n"
"buffer_store_dwordx4 v[248:251],v9,s[40:43],s58 offen sc1\n"
MF "v[204:207],a[220:223],a[192:195],v[204:207]\n"
"v_fma_f32 v234,v234,s54,v14\n"
"v_fma_f32 v235,v235,s54,v14\n"
MF "v[204:207],a[216:219],a[152:155],v[204:207]\n"
"v_fma_f32 v236,v236,s54,v14\n"
"v_fma_f32 v237,v237,s54,v14\n"
MF "v[212:215],a[216:219],a[156:159],v[212:215]\n"
"v_fma_f32 v238,v238,s54,v14\n"
"v_fma_f32 v239,v239,s54,v14\n"
MF "v[212:215],a[220:223],a[196:199],v[212:215]\n"
"s_waitcnt vmcnt(10) lgkmcnt(0)\n"
"s_barrier\n"
MF "v[176:179],a[200:203],a[60:63],v[176:179]\n"
TR "a[216:217],v1 offset:32776\n"
CV "v247,v238,v239\n"
MF "v[176:179],a[204:207],a[100:103],v[176:179]\n"
TR "a[218:219],v1 offset:33288\n"
CV "v246,v236,v237\n"
MF "v[184:187],a[204:207],a[104:107],v[184:187]\n"
"s_add_u32 s66,s67,4718592\n"
"s_sub_u32 s81,s81,384\n"
"s_mov_b32 s83,7077888\n"
MF "v[184:187],a[200:203],a[64:67],v[184:187]\n"
"s_add_u32 m0,s62,0\n"
"s_add_u32 s82,s66,2359296\n"
TR "a[220:221],v1 offset:36872\n"
MF "v[192:195],a[200:203],a[68:71],v[192:195]\n"
"s_sub_u32 s15,s82,7864320\n"
"s_min_u32 s82,s82,s15\n"
TR "a[222:223],v1 offset:37384\n"
MF "v[192:195],a[204:207],a[108:111],v[192:195]\n"
LD "v6,s[28:31],s82 offen lds\n"
MF "v[200:203],a[204:207],a[112:115],v[200:203]\n"
CV "v245,v234,v235\n"
CV "v244,v232,v233\n"
MF "v[200:203],a[200:203],a[72:75],v[200:203]\n"
"v_fma_f32 v224,v224,s54,v14\n"
"v_fma_f32 v225,v225,s54,v14\n"
MF "v[208:211],a[200:203],a[76:79],v[208:211]\n"
"buffer_store_dwordx4 v[244:247],v9,s[40:43],s75 offen offset:3072 sc1\n"
MF "v[208:211],a[204:207],a[116:119],v[208:211]\n"
LD "v11,s[28:31],s82 offen offset:1024 lds\n"
MF "v[208:211],a[208:211],a[156:159],v[208:211]\n"
TR "a[200:201],v1 offset:49160\n"
"v_fma_f32 v226,v226,s54,v14\n"
MF "v[208:211],a[212:215],a[196:199],v[208:211]\n"
TR "a[202:203],v1 offset:49672\n"
"v_fma_f32 v227,v227,s54,v14\n"
MF "v[200:203],a[212:215],a[192:195],v[200:203]\n"
"v_fma_f32 v228,v228,s54,v14\n"
"v_fma_f32 v229,v229,s54,v14\n"
MF "v[200:203],a[208:211],a[152:155],v[200:203]\n"
TR "a[204:205],v1 offset:53256\n"
"v_fma_f32 v230,v230,s54,v14\n"
MF "v[192:195],a[208:211],a[148:151],v[192:195]\n"
TR "a[206:207],v1 offset:53768\n"
"v_fma_f32 v231,v231,s54,v14\n"
MF "v[192:195],a[212:215],a[188:191],v[192:195]\n"
CV "v243,v230,v231\n"
CV "v242,v228,v229\n"
MF "v[184:187],a[212:215],a[184:187],v[184:187]\n"
CV "v241,v226,v227\n"
CV "v240,v224,v225\n"
MF "v[184:187],a[208:211],a[144:147],v[184:187]\n"
"buffer_store_dwordx4 v[240:243],v9,s[40:43],s75 offen offset:2048 sc1\n"
MF "v[176:179],a[208:211],a[140:143],v[176:179]\n"
LD "v12,s[28:31],s82 offen offset:2048 lds\n"
MF "v[176:179],a[212:215],a[180:183],v[176:179]\n"
"s_waitcnt lgkmcnt(4)\n"
"v_fma_f32 v216,v216,s54,v14\n"
"v_fma_f32 v217,v217,s54,v14\n"
MF "v[140:143],a[216:219],a[60:63],v[140:143]\n"
TR "a[208:209],v1 offset:32768\n"
"v_fma_f32 v218,v218,s54,v14\n"
MF "v[140:143],a[220:223],a[100:103],v[140:143]\n"
TR "a[210:211],v1 offset:33280\n"
"v_fma_f32 v219,v219,s54,v14\n"
MF "v[148:151],a[220:223],a[104:107],v[148:151]\n"
"v_fma_f32 v220,v220,s54,v14\n"
"v_fma_f32 v221,v221,s54,v14\n"
MF "v[148:151],a[216:219],a[64:67],v[148:151]\n"
TR "a[212:213],v1 offset:36864\n"
"v_fma_f32 v222,v222,s54,v14\n"
MF "v[156:159],a[216:219],a[68:71],v[156:159]\n"
TR "a[214:215],v1 offset:37376\n"
"v_fma_f32 v223,v223,s54,v14\n"
MF "v[156:159],a[220:223],a[108:111],v[156:159]\n"
CV "v239,v222,v223\n"
CV "v238,v220,v221\n"
MF "v[164:167],a[220:223],a[112:115],v[164:167]\n"
CV "v237,v218,v219\n"
CV "v236,v216,v217\n"
MF "v[164:167],a[216:219],a[72:75],v[164:167]\n"
LD "v13,s[28:31],s82 offen offset:3072 lds\n"
MF "v[172:175],a[216:219],a[76:79],v[172:175]\n"
"buffer_store_dwordx4 v[236:239],v9,s[40:43],s75 offen offset:1024 sc1\n"
MF "v[172:175],a[220:223],a[116:119],v[172:175]\n"
"s_waitcnt lgkmcnt(4)\n"
"v_fma_f32 v208,v208,s54,v14\n"
"v_fma_f32 v209,v209,s54,v14\n"
MF "v[140:143],a[200:203],a[140:143],v[140:143]\n"
TR "a[216:217],v1 offset:49152\n"
"v_fma_f32 v210,v210,s54,v14\n"
MF "v[140:143],a[204:207],a[180:183],v[140:143]\n"
TR "a[218:219],v1 offset:49664\n"
"v_fma_f32 v211,v211,s54,v14\n"
MF "v[148:151],a[204:207],a[184:187],v[148:151]\n"
"v_fma_f32 v212,v212,s54,v14\n"
"v_fma_f32 v213,v213,s54,v14\n"
MF "v[148:151],a[200:203],a[144:147],v[148:151]\n"
TR "a[220:221],v1 offset:53248\n"
"v_fma_f32 v214,v214,s54,v14\n"
MF "v[156:159],a[200:203],a[148:151],v[156:159]\n"
TR "a[222:223],v1 offset:53760\n"
"v_fma_f32 v215,v215,s54,v14\n"
MF "v[156:159],a[204:207],a[188:191],v[156:159]\n"
CV "v235,v214,v215\n"
CV "v234,v212,v213\n"
MF "v[164:167],a[204:207],a[192:195],v[164:167]\n"
CV "v233,v210,v211\n"
CV "v232,v208,v209\n"
MF "v[164:167],a[200:203],a[152:155],v[164:167]\n"
"v_fma_f32 v200,v200,s54,v14\n"
"v_fma_f32 v201,v201,s54,v14\n"
MF "v[172:175],a[200:203],a[156:159],v[172:175]\n"
"buffer_store_dwordx4 v[232:235],v9,s[40:43],s75 offen sc1\n"
MF "v[172:175],a[204:207],a[196:199],v[172:175]\n"
"s_waitcnt lgkmcnt(0)\n"
"v_fma_f32 v202,v202,s54,v14\n"
"v_fma_f32 v203,v203,s54,v14\n"
MF "v[136:139],a[208:211],a[60:63],v[136:139]\n"
"s_add_u32 m0,s62,16384\n"
"s_add_u32 s82,s66,2359424\n"
TR "a[200:201],v0 offset:32776\n"
MF "v[136:139],a[212:215],a[100:103],v[136:139]\n"
"s_sub_u32 s15,s82,7864320\n"
"s_min_u32 s82,s82,s15\n"
TR "a[202:203],v0 offset:33288\n"
MF "v[144:147],a[212:215],a[104:107],v[144:147]\n"
LD "v6,s[28:31],s82 offen lds\n"
MF "v[144:147],a[208:211],a[64:67],v[144:147]\n"
TR "a[204:205],v0 offset:36872\n"
"v_fma_f32 v204,v204,s54,v14\n"
MF "v[152:155],a[208:211],a[68:71],v[152:155]\n"
TR "a[206:207],v0 offset:37384\n"
"v_fma_f32 v205,v205,s54,v14\n"
MF "v[152:155],a[212:215],a[108:111],v[152:155]\n"
"v_fma_f32 v206,v206,s54,v14\n"
"v_fma_f32 v207,v207,s54,v14\n"
MF "v[160:163],a[212:215],a[112:115],v[160:163]\n"
CV "v231,v206,v207\n"
CV "v230,v204,v205\n"
MF "v[160:163],a[208:211],a[72:75],v[160:163]\n"
CV "v229,v202,v203\n"
CV "v228,v200,v201\n"
MF "v[168:171],a[208:211],a[76:79],v[168:171]\n"
AW "a84,v228\n"
AW "a85,v229\n"
MF "v[168:171],a[212:215],a[116:119],v[168:171]\n"
LD "v11,s[28:31],s82 offen offset:1024 lds\n"
MF "v[168:171],a[216:219],a[156:159],v[168:171]\n"
TR "a[208:209],v0 offset:49160\n"
AW "a86,v230\n"
MF "v[168:171],a[220:223],a[196:199],v[168:171]\n"
TR "a[210:211],v0 offset:49672\n"
AW "a87,v231\n"
MF "v[160:163],a[220:223],a[192:195],v[160:163]\n"
"v_fma_f32 v192,v192,s54,v14\n"
"v_fma_f32 v193,v193,s54,v14\n"
MF "v[160:163],a[216:219],a[152:155],v[160:163]\n"
TR "a[212:213],v0 offset:53256\n"
"v_fma_f32 v194,v194,s54,v14\n"
MF "v[152:155],a[216:219],a[148:151],v[152:155]\n"
TR "a[214:215],v0 offset:53768\n"
"v_fma_f32 v195,v195,s54,v14\n"
MF "v[152:155],a[220:223],a[188:191],v[152:155]\n"
"v_fma_f32 v196,v196,s54,v14\n"
"v_fma_f32 v197,v197,s54,v14\n"
MF "v[144:147],a[220:223],a[184:187],v[144:147]\n"
"v_fma_f32 v198,v198,s54,v14\n"
"v_fma_f32 v199,v199,s54,v14\n"
MF "v[144:147],a[216:219],a[144:147],v[144:147]\n"
CV "v227,v198,v199\n"
CV "v226,v196,v197\n"
MF "v[136:139],a[216:219],a[140:143],v[136:139]\n"
LD "v12,s[28:31],s82 offen offset:2048 lds\n"
MF "v[136:139],a[220:223],a[180:183],v[136:139]\n"
"s_waitcnt lgkmcnt(4)\n"
CV "v225,v194,v195\n"
CV "v224,v192,v193\n"
MF "v[100:103],a[200:203],a[60:63],v[100:103]\n"
TR "a[216:217],v0 offset:32768\n"
AW "a80,v224\n"
MF "v[100:103],a[204:207],a[100:103],v[100:103]\n"
TR "a[218:219],v0 offset:33280\n"
AW "a81,v225\n"
MF "v[108:111],a[204:207],a[104:107],v[108:111]\n"
AW "a82,v226\n"
AW "a83,v227\n"
MF "v[108:111],a[200:203],a[64:67],v[108:111]\n"
TR "a[220:221],v0 offset:36864\n"
"v_fma_f32 v184,v184,s54,v14\n"
MF "v[116:119],a[200:203],a[68:71],v[116:119]\n"
TR "a[222:223],v0 offset:37376\n"
"v_fma_f32 v185,v185,s54,v14\n"
MF "v[116:119],a[204:207],a[108:111],v[116:119]\n"
"v_fma_f32 v186,v186,s54,v14\n"
"v_fma_f32 v187,v187,s54,v14\n"
MF "v[124:127],a[204:207],a[112:115],v[124:127]\n"
"v_fma_f32 v188,v188,s54,v14\n"
"v_fma_f32 v189,v189,s54,v14\n"
MF "v[124:127],a[200:203],a[72:75],v[124:127]\n"
LD "v13,s[28:31],s82 offen offset:3072 lds\n"
MF "v[132:135],a[200:203],a[76:79],v[132:135]\n"
"v_fma_f32 v190,v190,s54,v14\n"
"v_fma_f32 v191,v191,s54,v14\n"
MF "v[132:135],a[204:207],a[116:119],v[132:135]\n"
"s_waitcnt lgkmcnt(4)\n"
CV "v223,v190,v191\n"
CV "v222,v188,v189\n"
MF "v[100:103],a[208:211],a[140:143],v[100:103]\n"
TR "a[200:201],v0 offset:49152\n"
CV "v221,v186,v187\n"
MF "v[100:103],a[212:215],a[180:183],v[100:103]\n"
TR "a[202:203],v0 offset:49664\n"
CV "v220,v184,v185\n"
MF "v[108:111],a[212:215],a[184:187],v[108:111]\n"
AW "a56,v220\n"
AW "a57,v221\n"
MF "v[108:111],a[208:211],a[144:147],v[108:111]\n"
TR "a[204:205],v0 offset:53248\n"
AW "a58,v222\n"
MF "v[116:119],a[208:211],a[148:151],v[116:119]\n"
TR "a[206:207],v0 offset:53760\n"
AW "a59,v223\n"
MF "v[116:119],a[212:215],a[188:191],v[116:119]\n"
"v_fma_f32 v176,v176,s54,v14\n"
"v_fma_f32 v177,v177,s54,v14\n"
MF "v[124:127],a[212:215],a[192:195],v[124:127]\n"
"v_add_u32 v0,0xc000,v0\n"
"v_fma_f32 v178,v178,s54,v14\n"
MF "v[124:127],a[208:211],a[152:155],v[124:127]\n"
"v_add_u32 v1,0xc000,v1\n"
"v_fma_f32 v179,v179,s54,v14\n"
MF "v[132:135],a[208:211],a[156:159],v[132:135]\n"
"v_fma_f32 v180,v180,s54,v14\n"
"v_fma_f32 v181,v181,s54,v14\n"
MF "v[132:135],a[212:215],a[196:199],v[132:135]\n"
"s_waitcnt vmcnt(14) lgkmcnt(0)\n"
"s_barrier\n"
MF "v[96:99],a[216:219],a[60:63],v[96:99]\n"
"s_add_u32 m0,s62,32768\n"
"s_add_u32 s82,s66,2359552\n"
TR "a[208:209],v1 offset:16392\n"
MF "v[96:99],a[220:223],a[100:103],v[96:99]\n"
"s_sub_u32 s15,s82,7864320\n"
"s_min_u32 s82,s82,s15\n"
TR "a[210:211],v1 offset:16904\n"
MF "v[104:107],a[220:223],a[104:107],v[104:107]\n"
LD "v6,s[28:31],s82 offen lds\n"
MF "v[104:107],a[216:219],a[64:67],v[104:107]\n"
TR "a[212:213],v1 offset:20488\n"
"v_fma_f32 v182,v182,s54,v14\n"
MF "v[112:115],a[216:219],a[68:71],v[112:115]\n"
TR "a[214:215],v1 offset:21000\n"
"v_fma_f32 v183,v183,s54,v14\n"
MF "v[112:115],a[220:223],a[108:111],v[112:115]\n"
CV "v219,v182,v183\n"
CV "v218,v180,v181\n"
MF "v[120:123],a[220:223],a[112:115],v[120:123]\n"
CV "v217,v178,v179\n"
CV "v216,v176,v177\n"
MF "v[120:123],a[216:219],a[72:75],v[120:123]\n"
AW "a52,v216\n"
AW "a53,v217\n"
MF "v[128:131],a[216:219],a[76:79],v[128:131]\n"
AW "a54,v218\n"
AW "a55,v219\n"
MF "v[128:131],a[220:223],a[116:119],v[128:131]\n"
LD "v11,s[28:31],s82 offen offset:1024 lds\n"
MF "v[128:131],a[200:203],a[156:159],v[128:131]\n"
TR "a[216:217],v1 offset:32776\n"
"v_fma_f32 v168,v168,s54,v14\n"
MF "v[128:131],a[204:207],a[196:199],v[128:131]\n"
TR "a[218:219],v1 offset:33288\n"
"v_fma_f32 v169,v169,s54,v14\n"
MF "v[120:123],a[204:207],a[192:195],v[120:123]\n"
"v_fma_f32 v170,v170,s54,v14\n"
"v_fma_f32 v171,v171,s54,v14\n"
MF "v[120:123],a[200:203],a[152:155],v[120:123]\n"
TR "a[220:221],v1 offset:36872\n"
"v_fma_f32 v172,v172,s54,v14\n"
MF "v[112:115],a[200:203],a[148:151],v[112:115]\n"
TR "a[222:223],v1 offset:37384\n"
"v_fma_f32 v173,v173,s54,v14\n"
MF "v[112:115],a[204:207],a[188:191],v[112:115]\n"
"v_fma_f32 v174,v174,s54,v14\n"
"v_fma_f32 v175,v175,s54,v14\n"
MF "v[104:107],a[204:207],a[184:187],v[104:107]\n"
CV "v215,v174,v175\n"
CV "v214,v172,v173\n"
MF "v[104:107],a[200:203],a[144:147],v[104:107]\n"
CV "v213,v170,v171\n"
CV "v212,v168,v169\n"
MF "v[96:99],a[200:203],a[140:143],v[96:99]\n"
LD "v12,s[28:31],s82 offen offset:2048 lds\n"
MF "v[96:99],a[204:207],a[180:183],v[96:99]\n"
"s_waitcnt lgkmcnt(4)\n"
AW "a48,v212\n"
AW "a49,v213\n"
MF "v[60:63],a[208:211],a[60:63],v[60:63]\n"
TR "a[200:201],v1 offset:16384\n"
AW "a50,v214\n"
MF "v[60:63],a[212:215],a[100:103],v[60:63]\n"
TR "a[202:203],v1 offset:16896\n"
AW "a51,v215\n"
MF "v[68:71],a[212:215],a[104:107],v[68:71]\n"
"v_fma_f32 v160,v160,s54,v14\n"
"v_fma_f32 v161,v161,s54,v14\n"
MF "v[68:71],a[208:211],a[64:67],v[68:71]\n"
TR "a[204:205],v1 offset:20480\n"
"v_fma_f32 v162,v162,s54,v14\n"
MF "v[76:79],a[208:211],a[68:71],v[76:79]\n"
TR "a[206:207],v1 offset:20992\n"
"v_fma_f32 v163,v163,s54,v14\n"
MF "v[76:79],a[212:215],a[108:111],v[76:79]\n"
"v_fma_f32 v164,v164,s54,v14\n"
"v_fma_f32 v165,v165,s54,v14\n"
MF "v[84:87],a[212:215],a[112:115],v[84:87]\n"
"v_fma_f32 v166,v166,s54,v14\n"
"v_fma_f32 v167,v167,s54,v14\n"
MF "v[84:87],a[208:211],a[72:75],v[84:87]\n"
LD "v13,s[28:31],s82 offen offset:3072 lds\n"
MF "v[92:95],a[208:211],a[76:79],v[92:95]\n"
CV "v211,v166,v167\n"
CV "v210,v164,v165\n"
MF "v[92:95],a[212:215],a[116:119],v[92:95]\n"
"s_waitcnt lgkmcnt(4)\n"
CV "v209,v162,v163\n"
CV "v208,v160,v161\n"
MF "v[60:63],a[216:219],a[140:143],v[60:63]\n"
TR "a[208:209],v1 offset:32768\n"
AW "a44,v208\n"
MF "v[60:63],a[220:223],a[180:183],v[60:63]\n"
TR "a[210:211],v1 offset:33280\n"
AW "a45,v209\n"
MF "v[68:71],a[220:223],a[184:187],v[68:71]\n"
AW "a46,v210\n"
AW "a47,v211\n"
MF "v[68:71],a[216:219],a[144:147],v[68:71]\n"
TR "a[212:213],v1 offset:36864\n"
"v_fma_f32 v152,v152,s54,v14\n"
MF "v[76:79],a[216:219],a[148:151],v[76:79]\n"
TR "a[214:215],v1 offset:37376\n"
"v_fma_f32 v153,v153,s54,v14\n"
MF "v[76:79],a[220:223],a[188:191],v[76:79]\n"
"v_fma_f32 v154,v154,s54,v14\n"
"v_fma_f32 v155,v155,s54,v14\n"
MF "v[84:87],a[220:223],a[192:195],v[84:87]\n"
"v_fma_f32 v156,v156,s54,v14\n"
"v_fma_f32 v157,v157,s54,v14\n"
MF "v[84:87],a[216:219],a[152:155],v[84:87]\n"
"v_fma_f32 v158,v158,s54,v14\n"
"v_fma_f32 v159,v159,s54,v14\n"
MF "v[92:95],a[216:219],a[156:159],v[92:95]\n"
CV "v207,v158,v159\n"
CV "v206,v156,v157\n"
MF "v[92:95],a[220:223],a[196:199],v[92:95]\n"
"s_waitcnt lgkmcnt(0)\n"
CV "v205,v154,v155\n"
CV "v204,v152,v153\n"
MF "v[56:59],a[200:203],a[60:63],v[56:59]\n"
TR "a[216:217],v0 offset:16392\n"
AW "a40,v204\n"
MF "v[56:59],a[204:207],a[100:103],v[56:59]\n"
TR "a[218:219],v0 offset:16904\n"
AW "a41,v205\n"
MF "v[64:67],a[204:207],a[104:107],v[64:67]\n"
AW "a42,v206\n"
AW "a43,v207\n"
MF "v[64:67],a[200:203],a[64:67],v[64:67]\n"
TR "a[220:221],v0 offset:20488\n"
"v_fma_f32 v144,v144,s54,v14\n"
MF "v[72:75],a[200:203],a[68:71],v[72:75]\n"
TR "a[222:223],v0 offset:21000\n"
"v_fma_f32 v145,v145,s54,v14\n"
MF "v[72:75],a[204:207],a[108:111],v[72:75]\n"
"v_fma_f32 v146,v146,s54,v14\n"
"v_fma_f32 v147,v147,s54,v14\n"
MF "v[80:83],a[204:207],a[112:115],v[80:83]\n"
"v_fma_f32 v148,v148,s54,v14\n"
"v_fma_f32 v149,v149,s54,v14\n"
MF "v[80:83],a[200:203],a[72:75],v[80:83]\n"
"v_fma_f32 v150,v150,s54,v14\n"
"v_fma_f32 v151,v151,s54,v14\n"
MF "v[88:91],a[200:203],a[76:79],v[88:91]\n"
CV "v203,v150,v151\n"
CV "v202,v148,v149\n"
MF "v[88:91],a[204:207],a[116:119],v[88:91]\n"
CV "v201,v146,v147\n"
CV "v200,v144,v145\n"
MF "v[88:91],a[208:211],a[156:159],v[88:91]\n"
TR "a[200:201],v0 offset:32776\n"
AW "a16,v200\n"
MF "v[88:91],a[212:215],a[196:199],v[88:91]\n"
TR "a[202:203],v0 offset:33288\n"
AW "a17,v201\n"
MF "v[80:83],a[212:215],a[192:195],v[80:83]\n"
AW "a18,v202\n"
AW "a19,v203\n"
MF "v[80:83],a[208:211],a[152:155],v[80:83]\n"
TR "a[204:205],v0 offset:36872\n"
"v_fma_f32 v136,v136,s54,v14\n"
MF "v[72:75],a[208:211],a[148:151],v[72:75]\n"
TR "a[206:207],v0 offset:37384\n"
"v_fma_f32 v137,v137,s54,v14\n"
MF "v[72:75],a[212:215],a[188:191],v[72:75]\n"
"v_fma_f32 v138,v138,s54,v14\n"
"v_fma_f32 v139,v139,s54,v14\n"
MF "v[64:67],a[212:215],a[184:187],v[64:67]\n"
"v_fma_f32 v140,v140,s54,v14\n"
"v_fma_f32 v141,v141,s54,v14\n"
MF "v[64:67],a[208:211],a[144:147],v[64:67]\n"
"v_fma_f32 v142,v142,s54,v14\n"
"v_fma_f32 v143,v143,s54,v14\n"
MF "v[56:59],a[208:211],a[140:143],v[56:59]\n"
CV "v199,v142,v143\n"
CV "v198,v140,v141\n"
MF "v[56:59],a[212:215],a[180:183],v[56:59]\n"
"s_waitcnt lgkmcnt(4)\n"
CV "v197,v138,v139\n"
CV "v196,v136,v137\n"
MF "v[20:23],a[216:219],a[60:63],v[20:23]\n"
TR "a[208:209],v0 offset:16384\n"
AW "a12,v196\n"
MF "v[20:23],a[220:223],a[100:103],v[20:23]\n"
TR "a[210:211],v0 offset:16896\n"
AW "a13,v197\n"
MF "v[28:31],a[220:223],a[104:107],v[28:31]\n"
AW "a14,v198\n"
AW "a15,v199\n"
MF "v[28:31],a[216:219],a[64:67],v[28:31]\n"
TR "a[212:213],v0 offset:20480\n"
"v_fma_f32 v128,v128,s54,v14\n"
MF "v[36:39],a[216:219],a[68:71],v[36:39]\n"
TR "a[214:215],v0 offset:20992\n"
"v_fma_f32 v129,v129,s54,v14\n"
MF "v[36:39],a[220:223],a[108:111],v[36:39]\n"
"v_fma_f32 v130,v130,s54,v14\n"
"v_fma_f32 v131,v131,s54,v14\n"
MF "v[44:47],a[220:223],a[112:115],v[44:47]\n"
"v_fma_f32 v132,v132,s54,v14\n"
"v_fma_f32 v133,v133,s54,v14\n"
MF "v[44:47],a[216:219],a[72:75],v[44:47]\n"
"v_fma_f32 v134,v134,s54,v14\n"
"v_fma_f32 v135,v135,s54,v14\n"
MF "v[52:55],a[216:219],a[76:79],v[52:55]\n"
CV "v195,v134,v135\n"
CV "v194,v132,v133\n"
MF "v[52:55],a[220:223],a[116:119],v[52:55]\n"
"s_waitcnt lgkmcnt(4)\n"
CV "v193,v130,v131\n"
CV "v192,v128,v129\n"
MF "v[20:23],a[200:203],a[140:143],v[20:23]\n"
TR "a[216:217],v0 offset:32768\n"
AW "a8,v192\n"
MF "v[20:23],a[204:207],a[180:183],v[20:23]\n"
TR "a[218:219],v0 offset:33280\n"
AW "a9,v193\n"
MF "v[28:31],a[204:207],a[184:187],v[28:31]\n"
AW "a10,v194\n"
AW "a11,v195\n"
MF "v[28:31],a[200:203],a[144:147],v[28:31]\n"
TR "a[220:221],v0 offset:36864\n"
"v_fma_f32 v120,v120,s54,v14\n"
MF "v[36:39],a[200:203],a[148:151],v[36:39]\n"
TR "a[222:223],v0 offset:37376\n"
"v_fma_f32 v121,v121,s54,v14\n"
MF "v[36:39],a[204:207],a[188:191],v[36:39]\n"
"v_fma_f32 v122,v122,s54,v14\n"
"v_fma_f32 v123,v123,s54,v14\n"
MF "v[44:47],a[204:207],a[192:195],v[44:47]\n"
"v_fma_f32 v124,v124,s54,v14\n"
"v_fma_f32 v125,v125,s54,v14\n"
MF "v[44:47],a[200:203],a[152:155],v[44:47]\n"
"v_fma_f32 v126,v126,s54,v14\n"
"v_fma_f32 v127,v127,s54,v14\n"
MF "v[52:55],a[200:203],a[156:159],v[52:55]\n"
CV "v191,v126,v127\n"
CV "v190,v124,v125\n"
MF "v[52:55],a[204:207],a[196:199],v[52:55]\n"
"s_waitcnt lgkmcnt(0)\n"
"s_barrier\n"
MF "v[16:19],a[208:211],a[60:63],v[16:19]\n"
CV "v189,v122,v123\n"
CV "v188,v120,v121\n"
MF "v[16:19],a[212:215],a[100:103],v[16:19]\n"
AW "a4,v188\n"
AW "a5,v189\n"
MF "v[24:27],a[212:215],a[104:107],v[24:27]\n"
AW "a6,v190\n"
AW "a7,v191\n"
MF "v[24:27],a[208:211],a[64:67],v[24:27]\n"
"v_fma_f32 v112,v112,s54,v14\n"
"v_fma_f32 v113,v113,s54,v14\n"
MF "v[32:35],a[208:211],a[68:71],v[32:35]\n"
"v_fma_f32 v114,v114,s54,v14\n"
"v_fma_f32 v115,v115,s54,v14\n"
MF "v[32:35],a[212:215],a[108:111],v[32:35]\n"
"v_fma_f32 v116,v116,s54,v14\n"
"v_fma_f32 v117,v117,s54,v14\n"
MF "v[40:43],a[212:215],a[112:115],v[40:43]\n"
"v_fma_f32 v118,v118,s54,v14\n"
"v_fma_f32 v119,v119,s54,v14\n"
MF "v[40:43],a[208:211],a[72:75],v[40:43]\n"
CV "v187,v118,v119\n"
CV "v186,v116,v117\n"
MF "v[48:51],a[208:211],a[76:79],v[48:51]\n"
CV "v185,v114,v115\n"
CV "v184,v112,v113\n"
MF "v[48:51],a[212:215],a[116:119],v[48:51]\n"
AW "a0,v184\n"
AW "a1,v185\n"
MF "v[48:51],a[216:219],a[156:159],v[48:51]\n"
AW "a2,v186\n"
AW "a3,v187\n"
MF "v[48:51],a[220:223],a[196:199],v[48:51]\n"
"v_fma_f32 v104,v104,s54,v14\n"
"v_fma_f32 v105,v105,s54,v14\n"
MF "v[40:43],a[220:223],a[192:195],v[40:43]\n"
"v_fma_f32 v106,v106,s54,v14\n"
"v_fma_f32 v107,v107,s54,v14\n"
MF "v[40:43],a[216:219],a[152:155],v[40:43]\n"
"v_fma_f32 v108,v108,s54,v14\n"
"v_fma_f32 v109,v109,s54,v14\n"
MF "v[32:35],a[216:219],a[148:151],v[32:35]\n"
"v_fma_f32 v110,v110,s54,v14\n"
"v_fma_f32 v111,v111,s54,v14\n"
MF "v[32:35],a[220:223],a[188:191],v[32:35]\n"
CV "v183,v110,v111\n"
CV "v182,v108,v109\n"
MF "v[24:27],a[220:223],a[184:187],v[24:27]\n"
CV "v181,v106,v107\n"
CV "v180,v104,v105\n"
MF "v[24:27],a[216:219],a[144:147],v[24:27]\n"
AW "a36,v180\n"
AW "a37,v181\n"
MF "v[16:19],a[216:219],a[140:143],v[16:19]\n"
AW "a38,v182\n"
AW "a39,v183\n"
MF "v[16:19],a[220:223],a[180:183],v[16:19]\n"
"s_mov_b32 s57,1\n"
"s_mov_b32 s69,99\n"
AR "v0,a240\n"
AR "v2,a241\n"
AR "v4,a242\n"
AR "v6,a243\n"
"v_xor_b32 v1,64,v0\n"
"v_xor_b32 v3,64,v2\n"
"v_xor_b32 v5,64,v4\n"
"v_add_u32 v11,97280,v6\n"
"v_add_u32 v12,194560,v6\n"
"v_add_u32 v13,291840,v6\n"
"s_memtime s[52:53]\n"
"s_memrealtime s[100:101]\n"
"s_waitcnt lgkmcnt(0)\n"
"s_sub_u32 s53,s52,s80\n"
"s_cmp_gt_u32 s53,30000\n"
"s_cselect_b32 s101,1,0\n"
"s_cmp_eq_u32 s87,2\n"
"s_cselect_b32 s87,s101,s87\n"
"s_cmp_eq_u32 s87,1\n"
"s_mov_b64 vcc,-1\n"
"s_waitcnt vmcnt(10) lgkmcnt(0)\n"
"s_barrier\n"
"s_nop 0\n"
TR "a[200:201],v0\n"
TR "a[202:203],v0 offset:512\n"
TR "a[204:205],v0 offset:4096\n"
TR "a[206:207],v0 offset:4608\n"
"ds_read_b128 a[60:63],v2\n"
"ds_read_b128 a[100:103],v3\n"
"ds_read_b128 a[64:67],v2 offset:2048\n"
"ds_read_b128 a[104:107],v3 offset:2048\n"
"ds_read_b128 a[68:71],v2 offset:4096\n"
"ds_read_b128 a[108:111],v3 offset:4096\n"
"ds_read_b128 a[72:75],v2 offset:6144\n"
"ds_read_b128 a[112:115],v3 offset:6144\n"
"ds_read_b128 a[76:79],v2 offset:8192\n"
"ds_read_b128 a[116:119],v3 offset:8192\n"
TR "a[208:209],v0 offset:8\n"
TR "a[210:211],v0 offset:520\n"
TR "a[212:213],v0 offset:4104\n"
TR "a[214:215],v0 offset:4616\n"
"v_pk_fma_f32 v[96:97],v[96:97],s[54:55],v[14:15] op_sel_hi:[1,1,0]\n"
"v_pk_fma_f32 v[98:99],v[98:99],s[54:55],v[14:15] op_sel_hi:[1,1,0]\n"
"v_pk_fma_f32 v[100:101],v[100:101],s[54:55],v[14:15] op_sel_hi:[1,1,0]\n"
"v_pk_fma_f32 v[102:103],v[102:103],s[54:55],v[14:15] op_sel_hi:[1,1,0]\n"
CV "v179,v102,v103\n"
CV "v178,v100,v101\n"
CV "v177,v98,v99\n"
CV "v176,v96,v97\n"
AW "a32,v176\n"
AW "a33,v177\n"
AW "a34,v178\n"
AW "a35,v179\n"
"v_pk_fma_f32 v[88:89],v[88:89],s[54:55],v[14:15] op_sel_hi:[1,1,0]\n"
"v_pk_fma_f32 v[90:91],v[90:91],s[54:55],v[14:15] op_sel_hi:[1,1,0]\n"
"v_pk_fma_f32 v[92:93],v[92:93],s[54:55],v[14:15] op_sel_hi:[1,1,0]\n"
"v_pk_fma_f32 v[94:95],v[94:95],s[54:55],v[14:15] op_sel_hi:[1,1,0]\n"
CV "v175,v94,v95\n"
CV "v174,v92,v93\n"
CV "v173,v90,v91\n"
CV "v172,v88,v89\n"
AW "a28,v172\n"
AW "a29,v173\n"
AW "a30,v174\n"
AW "a31,v175\n"
"v_pk_fma_f32 v[80:81],v[80:81],s[54:55],v[14:15] op_sel_hi:[1,1,0]\n"
"v_pk_fma_f32 v[82:83],v[82:83],s[54:55],v[14:15] op_sel_hi:[1,1,0]\n"
"v_pk_fma_f32 v[84:85],v[84:85],s[54:55],v[14:15] op_sel_hi:[1,1,0]\n"
"v_pk_fma_f32 v[86:87],v[86:87],s[54:55],v[14:15] op_sel_hi:[1,1,0]\n"
CV "v171,v86,v87\n"
CV "v170,v84,v85\n"
CV "v169,v82,v83\n"
CV "v168,v80,v81\n"
AW "a24,v168\n"
AW "a25,v169\n"
AW "a26,v170\n"
AW "a27,v171\n"
"v_pk_fma_f32 v[72:73],v[72:73],s[54:55],v[14:15] op_sel_hi:[1,1,0]\n"
"v_pk_fma_f32 v[74:75],v[74:75],s[54:55],v[14:15] op_sel_hi:[1,1,0]\n"
"v_pk_fma_f32 v[76:77],v[76:77],s[54:55],v[14:15] op_sel_hi:[1,1,0]\n"
"v_pk_fma_f32 v[78:79],v[78:79],s[54:55],v[14:15] op_sel_hi:[1,1,0]\n"
CV "v167,v78,v79\n"
CV "v166,v76,v77\n"
CV "v165,v74,v75\n"
CV "v164,v72,v73\n"
AW "a20,v164\n"
AW "a21,v165\n"
AW "a22,v166\n"
AW "a23,v167\n"
"v_pk_fma_f32 v[64:65],v[64:65],s[54:55],v[14:15] op_sel_hi:[1,1,0]\n"
"v_pk_fma_f32 v[66:67],v[66:67],s[54:55],v[14:15] op_sel_hi:[1,1,0]\n"
"v_pk_fma_f32 v[68:69],v[68:69],s[54:55],v[14:15] op_sel_hi:[1,1,0]\n"
"v_pk_fma_f32 v[70:71],v[70:71],s[54:55],v[14:15] op_sel_hi:[1,1,0]\n"
CV "v163,v70,v71\n"
CV "v162,v68,v69\n"
CV "v161,v66,v67\n"
CV "v160,v64,v65\n"
AW "a176,v160\n"
AW "a177,v161\n"
AW "a178,v162\n"
AW "a179,v163\n"
"v_pk_fma_f32 v[56:57],v[56:57],s[54:55],v[14:15] op_sel_hi:[1,1,0]\n"
"v_pk_fma_f32 v[58:59],v[58:59],s[54:55],v[14:15] op_sel_hi:[1,1,0]\n"
"v_pk_fma_f32 v[60:61],v[60:61],s[54:55],v[14:15] op_sel_hi:[1,1,0]\n"
"v_pk_fma_f32 v[62:63],v[62:63],s[54:55],v[14:15] op_sel_hi:[1,1,0]\n"
CV "v159,v62,v63\n"
CV "v158,v60,v61\n"
CV "v157,v58,v59\n"
CV "v156,v56,v57\n"
AW "a172,v156\n"
AW "a173,v157\n"
AW "a174,v158\n"
AW "a175,v159\n"
"v_pk_mul_f32 v[48:49],v[48:49],s[54:55]\n"
"v_pk_mul_f32 v[50:51],v[50:51],s[54:55]\n"
"v_pk_mul_f32 v[52:53],v[52:53],s[54:55]\n"
"v_pk_mul_f32 v[54:55],v[54:55],s[54:55]\n"
CV "v155,v54,v55\n"
CV "v154,v52,v53\n"
CV "v153,v50,v51\n"
CV "v152,v48,v49\n"
AW "a168,v152\n"
AW "a169,v153\n"
AW "a170,v154\n"
AW "a171,v155\n"
"v_pk_mul_f32 v[40:41],v[40:41],s[54:55]\n"
"v_pk_mul_f32 v[42:43],v[42:43],s[54:55]\n"
"v_pk_mul_f32 v[44:45],v[44:45],s[54:55]\n"
"v_pk_mul_f32 v[46:47],v[46:47],s[54:55]\n"
CV "v151,v46,v47\n"
CV "v150,v44,v45\n"
CV "v149,v42,v43\n"
CV "v148,v40,v41\n"
AW "a120,v148\n"
AW "a121,v149\n"
AW "a122,v150\n"
AW "a123,v151\n"
"v_pk_mul_f32 v[32:33],v[32:33],s[54:55]\n"
"v_pk_mul_f32 v[34:35],v[34:35],s[54:55]\n"
"v_pk_mul_f32 v[36:37],v[36:37],s[54:55]\n"
"v_pk_mul_f32 v[38:39],v[38:39],s[54:55]\n"
CV "v147,v38,v39\n"
CV "v146,v36,v37\n"
CV "v145,v34,v35\n"
CV "v144,v32,v33\n"
AW "a96,v144\n"
AW "a97,v145\n"
AW "a98,v146\n"
AW "a99,v147\n"
"v_pk_mul_f32 v[24:25],v[24:25],s[54:55]\n"
"v_pk_mul_f32 v[26:27],v[26:27],s[54:55]\n"
"v_pk_mul_f32 v[28:29],v[28:29],s[54:55]\n"
"v_pk_mul_f32 v[30:31],v[30:31],s[54:55]\n"
CV "v143,v30,v31\n"
CV "v142,v28,v29\n"
CV "v141,v26,v27\n"
CV "v140,v24,v25\n"
AW "a92,v140\n"
AW "a93,v141\n"
AW "a94,v142\n"
AW "a95,v143\n"
"v_pk_mul_f32 v[16:17],v[16:17],s[54:55]\n"
"v_pk_mul_f32 v[18:19],v[18:19],s[54:55]\n"
"v_pk_mul_f32 v[20:21],v[20:21],s[54:55]\n"
"v_pk_mul_f32 v[22:23],v[22:23],s[54:55]\n"
CV "v139,v22,v23\n"
CV "v138,v20,v21\n"
CV "v137,v18,v19\n"
CV "v136,v16,v17\n"
AW "a88,v136\n"
AW "a89,v137\n"
AW "a90,v138\n"
AW "a91,v139\n"
"s_waitcnt lgkmcnt(0)\n"
"s_branch .Lq0\n"
".Lpsl:\n"
"s_sleep 1\n"
".Lpsub:\n"
"s_memrealtime s[100:101]\n"
"s_waitcnt lgkmcnt(0)\n"
"s_sub_u32 s101,s21,s100\n"
"s_cmp_gt_i32 s101,0\n"
"s_cbranch_scc1 .Lpsl\n"
"s_add_u32 s21,s100,s97\n"
"s_setpc_b64 s[52:53]\n"
".Lstub_6:\n"
"v_cmp_le_f32 vcc,0x47800000,v10\n"
"s_cmp_eq_u32 s61,1\n"
"s_cbranch_scc0 .Lhook_6\n"
"s_sub_u32 s58,s71,s70\n"
"s_add_u32 s72,s72,s58\n"
"s_add_u32 s16,s16,s58\n"
"s_add_u32 s17,s17,s58\n"
"s_cmp_eq_u32 s57,0\n"
"s_cselect_b32 s34,0,s34\n"
"s_cselect_b32 s64,163840,s64\n"
"s_mov_b64 exec,0\n"
"s_cmp_eq_u32 s57,0\n"
"s_cbranch_scc1 .Lipa2\n"
"s_cmp_eq_u32 s56,0\n"
"s_cbranch_scc1 .Lep\n"
"s_cmp_eq_u32 s56,1\n"
"s_cbranch_scc1 .Lt0e\n"
"s_cmp_eq_u32 s56,2\n"
"s_cbranch_scc1 .Lmid\n"
"s_branch .Laloop\n"
".Lipa2:\n"
"s_cmp_eq_u32 s56,0\n"
"s_cbranch_scc1 .Lq0\n"
"s_cmp_eq_u32 s56,1\n"
"s_cbranch_scc1 .LattB\n"
"s_cmp_eq_u32 s56,2\n"
"s_cbranch_scc1 .Lt0k\n"
"s_branch .Lmid\n"
".Lipr2:\n"
"s_mov_b64 exec,-1\n"
".Lhook_6:\n"
"s_cmp_eq_u32 s77,0\n"
"s_cbranch_scc0 .Lnop_6\n"
"s_call_b64 s[52:53],.Lpsub\n"
".Lnop_6:\n"
"s_cbranch_vccz .Lback_6\n"
"s_call_b64 s[52:53],.Lslow_1\n"
"s_branch .Lback_6\n"
".Lstub_7:\n"
"v_cmp_le_f32 vcc,0x47800000,v10\n"
"s_cmp_eq_u32 s61,1\n"
"s_cbranch_scc0 .Lhook_7\n"
"s_sub_u32 s58,s71,s70\n"
"s_add_u32 s73,s73,s58\n"
"s_add_u32 s18,s18,s58\n"
"s_add_u32 s19,s19,s58\n"
"s_cmp_eq_u32 s57,0\n"
"s_cselect_b32 s38,0,s38\n"
"s_cselect_b32 s65,163840,s65\n"
".Lhook_7:\n"
"s_cmp_eq_u32 s77,0\n"
"s_cbranch_scc0 .Lnop_7\n"
"s_call_b64 s[52:53],.Lpsub\n"
"s_cmp_eq_u32 s61,4\n"
"s_cselect_b32 s77,0x47800000,s77\n"
"s_cselect_b32 s95,0x47800000,s95\n"
".Lnop_7:\n"
"s_cbranch_vccz .Lback_7\n"
"s_call_b64 s[52:53],.Lslow_0\n"
"s_branch .Lback_7\n"
".Lstub_8:\n"
"s_call_b64 s[52:53],.Lslow_1\n"
"s_branch .Lback_8\n"
".Lstub_11:\n"
"v_cmp_le_f32 vcc,0x47800000,v10\n"
"s_cmp_eq_u32 s77,0\n"
"s_cbranch_scc0 .Lnop_11\n"
"s_call_b64 s[52:53],.Lpsub\n"
".Lnop_11:\n"
"s_cbranch_vccz .Lback_11\n"
"s_call_b64 s[52:53],.Lslow_1\n"
"s_branch .Lback_11\n"
".Lstub_12:\n"
"v_cmp_le_f32 vcc,0x47800000,v10\n"
"s_cmp_eq_u32 s77,0\n"
"s_cbranch_scc0 .Lnop_12\n"
"s_call_b64 s[52:53],.Lpsub\n"
".Lnop_12:\n"
"s_cbranch_vccz .Lback_12\n"
"s_call_b64 s[52:53],.Lslow_0\n"
"s_branch .Lback_12\n"
".Lstub_13:\n"
"v_cmp_le_f32 vcc,0x47800000,v10\n"
"s_cmp_eq_u32 s61,1\n"
"s_cbranch_scc0 .Lhook_13\n"
"s_mov_b64 exec,0\n"
"s_cmp_eq_u32 s56,0\n"
"s_cbranch_scc1 .Lrole1\n"
"s_cmp_eq_u32 s56,1\n"
"s_cbranch_scc1 .Lplast0\n"
"s_cmp_eq_u32 s56,2\n"
"s_cbranch_scc1 .Lplast1\n"
"s_branch .Ljobs\n"
".Lipr3:\n"
"s_mov_b64 exec,-1\n"
"s_mov_b32 s97,0\n"
".Lhook_13:\n"
"s_cmp_eq_u32 s77,0\n"
"s_cbranch_scc0 .Lnop_13\n"
"s_call_b64 s[52:53],.Lpsub\n"
".Lnop_13:\n"
"s_cbranch_vccz .Lback_13\n"
"s_call_b64 s[52:53],.Lslow_1\n"
"s_branch .Lback_13\n"
".Lstub_14:\n"
"v_cmp_le_f32 vcc,0x47800000,v10\n"
"s_cmp_eq_u32 s77,0\n"
"s_cbranch_scc0 .Lnop_14\n"
"s_call_b64 s[52:53],.Lpsub\n"
".Lnop_14:\n"
"s_cbranch_vccz .Lback_14\n"
"s_call_b64 s[52:53],.Lslow_0\n"
"s_branch .Lback_14\n"
".Lstub_15:\n"
"v_cmp_le_f32 vcc,0x47800000,v10\n"
"s_cmp_eq_u32 s61,1\n"
"s_cbranch_scc0 .Lhook_15\n"
"s_mov_b32 s34,0\n"
"s_mov_b32 s64,163840\n"
".Lhook_15:\n"
"s_cmp_eq_u32 s77,0\n"
"s_cbranch_scc0 .Lnop_15\n"
"s_call_b64 s[52:53],.Lpsub\n"
".Lnop_15:\n"
"s_cbranch_vccz .Lback_15\n"
"s_call_b64 s[52:53],.Lslow_1\n"
"s_branch .Lback_15\n"
".Lstub_16:\n"
"v_cmp_le_f32 vcc,0x47800000,v10\n"
"s_cmp_eq_u32 s77,0\n"
"s_cbranch_scc0 .Lnop_16\n"
"s_call_b64 s[52:53],.Lpsub\n"
".Lnop_16:\n"
"s_cbranch_vccz .Lback_16\n"
"s_call_b64 s[52:53],.Lslow_0\n"
"s_branch .Lback_16\n"
".Lstub_17:\n"
"s_call_b64 s[52:53],.Lslow_1\n"
"s_branch .Lback_17\n"
".Lslow_0:\n"
"s_nop 7\n"
"s_nop 7\n"
"v_and_b32 v10,240,v9\n"
"v_mul_u32_u24 v10,24,v10\n"
"v_lshrrev_b32 v11,4,v9\n"
"v_and_or_b32 v10,v11,48,v10\n"
"s_mov_b32 s59,s34\n"
"s_mov_b32 s34,s20\n"
"s_add_u32 s58,s74,0\n"
"s_nop 3\n"
LD "v[52:55],v10,s[32:35],s58 offen\n"
"s_waitcnt vmcnt(0)\n"
MF "v[72:75],v[52:55],v[136:139],v[32:35]\n"
MF "v[76:79],v[52:55],v[140:143],v[36:39]\n"
MF "v[80:83],v[52:55],v[144:147],v[40:43]\n"
MF "v[84:87],v[52:55],v[148:151],v[44:47]\n"
MF "v[88:91],v[52:55],v[152:155],v[48:51]\n"
"s_add_u32 s58,s74,0\n"
"s_nop 3\n"
LD "v[52:55],v10,s[32:35],s58 offen offset:64\n"
"s_waitcnt vmcnt(0)\n"
MF "v[72:75],v[52:55],v[156:159],v[72:75]\n"
MF "v[76:79],v[52:55],v[160:163],v[76:79]\n"
MF "v[80:83],v[52:55],v[164:167],v[80:83]\n"
MF "v[84:87],v[52:55],v[168:171],v[84:87]\n"
MF "v[88:91],v[52:55],v[172:175],v[88:91]\n"
"s_add_u32 s58,s74,0\n"
"s_nop 3\n"
LD "v[52:55],v10,s[32:35],s58 offen offset:128\n"
"s_waitcnt vmcnt(0)\n"
MF "v[72:75],v[52:55],v[176:179],v[72:75]\n"
MF "v[76:79],v[52:55],v[180:183],v[76:79]\n"
MF "v[80:83],v[52:55],v[184:187],v[80:83]\n"
MF "v[84:87],v[52:55],v[188:191],v[84:87]\n"
MF "v[88:91],v[52:55],v[192:195],v[88:91]\n"
"s_add_u32 s58,s74,0\n"
"s_nop 3\n"
LD "v[52:55],v10,s[32:35],s58 offen offset:192\n"
"s_waitcnt vmcnt(0)\n"
MF "v[72:75],v[52:55],v[196:199],v[72:75]\n"
MF "v[76:79],v[52:55],v[200:203],v[76:79]\n"
MF "v[80:83],v[52:55],v[204:207],v[80:83]\n"
MF "v[84:87],v[52:55],v[208:211],v[84:87]\n"
MF "v[88:91],v[52:55],v[212:215],v[88:91]\n"
"s_add_u32 s58,s74,0\n"
"s_nop 3\n"
LD "v[52:55],v10,s[32:35],s58 offen offset:256\n"
"s_waitcnt vmcnt(0)\n"
MF "v[72:75],v[52:55],v[216:219],v[72:75]\n"
MF "v[76:79],v[52:55],v[220:223],v[76:79]\n"
MF "v[80:83],v[52:55],v[224:227],v[80:83]\n"
MF "v[84:87],v[52:55],v[228:231],v[84:87]\n"
MF "v[88:91],v[52:55],v[232:235],v[88:91]\n"
"s_add_u32 s58,s74,0\n"
"s_nop 3\n"
LD "v[52:55],v10,s[32:35],s58 offen offset:320\n"
"s_waitcnt vmcnt(0)\n"
MF "v[72:75],v[52:55],v[236:239],v[72:75]\n"
MF "v[76:79],v[52:55],v[240:243],v[76:79]\n"
MF "v[80:83],v[52:55],v[244:247],v[80:83]\n"
MF "v[84:87],v[52:55],v[248:251],v[84:87]\n"
MF "v[88:91],v[52:55],v[252:255],v[88:91]\n"
"s_add_u32 s58,s74,6144\n"
"s_nop 3\n"
LD "v[52:55],v10,s[32:35],s58 offen\n"
"s_waitcnt vmcnt(0)\n"
MF "v[112:115],v[52:55],v[136:139],v[32:35]\n"
MF "v[116:119],v[52:55],v[140:143],v[36:39]\n"
MF "v[120:123],v[52:55],v[144:147],v[40:43]\n"
MF "v[124:127],v[52:55],v[148:151],v[44:47]\n"
MF "v[128:131],v[52:55],v[152:155],v[48:51]\n"
"s_add_u32 s58,s74,6144\n"
"s_nop 3\n"
LD "v[52:55],v10,s[32:35],s58 offen offset:64\n"
"s_waitcnt vmcnt(0)\n"
MF "v[112:115],v[52:55],v[156:159],v[112:115]\n"
MF "v[116:119],v[52:55],v[160:163],v[116:119]\n"
MF "v[120:123],v[52:55],v[164:167],v[120:123]\n"
MF "v[124:127],v[52:55],v[168:171],v[124:127]\n"
MF "v[128:131],v[52:55],v[172:175],v[128:131]\n"
"s_add_u32 s58,s74,6144\n"
"s_nop 3\n"
LD "v[52:55],v10,s[32:35],s58 offen offset:128\n"
"s_waitcnt vmcnt(0)\n"
MF "v[112:115],v[52:55],v[176:179],v[112:115]\n"
MF "v[116:119],v[52:55],v[180:183],v[116:119]\n"
MF "v[120:123],v[52:55],v[184:187],v[120:123]\n"
MF "v[124:127],v[52:55],v[188:191],v[124:127]\n"
MF "v[128:131],v[52:55],v[192:195],v[128:131]\n"
"s_add_u32 s58,s74,6144\n"
"s_nop 3\n"
LD "v[52:55],v10,s[32:35],s58 offen offset:192\n"
"s_waitcnt vmcnt(0)\n"
MF "v[112:115],v[52:55],v[196:199],v[112:115]\n"
MF "v[116:119],v[52:55],v[200:203],v[116:119]\n"
MF "v[120:123],v[52:55],v[204:207],v[120:123]\n"
MF "v[124:127],v[52:55],v[208:211],v[124:127]\n"
MF "v[128:131],v[52:55],v[212:215],v[128:131]\n"
"s_add_u32 s58,s74,6144\n"
"s_nop 3\n"
LD "v[52:55],v10,s[32:35],s58 offen offset:256\n"
"s_waitcnt vmcnt(0)\n"
MF "v[112:115],v[52:55],v[216:219],v[112:115]\n"
MF "v[116:119],v[52:55],v[220:223],v[116:119]\n"
MF "v[120:123],v[52:55],v[224:227],v[120:123]\n"
MF "v[124:127],v[52:55],v[228:231],v[124:127]\n"
MF "v[128:131],v[52:55],v[232:235],v[128:131]\n"
"s_add_u32 s58,s74,6144\n"
"s_nop 3\n"
LD "v[52:55],v10,s[32:35],s58 offen offset:320\n"
"s_waitcnt vmcnt(0)\n"
MF "v[112:115],v[52:55],v[236:239],v[112:115]\n"
MF "v[116:119],v[52:55],v[240:243],v[116:119]\n"
MF "v[120:123],v[52:55],v[244:247],v[120:123]\n"
MF "v[124:127],v[52:55],v[248:251],v[124:127]\n"
MF "v[128:131],v[52:55],v[252:255],v[128:131]\n"
"s_nop 7\n"
"s_mov_b32 s34,s59\n"
"v_max3_f32 v10,v72,v73,v74\n"
"s_nop 0\n"
"v_max3_f32 v11,v75,v112,v113\n"
"v_max3_f32 v10,v10,v114,v115\n"
"v_max_f32 v10,v10,v11\n"
"v_mov_b32 v11,v10\n"
"s_nop 1\n"
"v_permlane16_swap_b32 v10,v11\n"
"v_max_f32 v10,v10,v11\n"
"v_mov_b32 v11,v10\n"
"s_nop 1\n"
"v_permlane32_swap_b32 v10,v11\n"
"v_max_f32 v10,v10,v11\n"
"v_add_f32 v10,0x41300000,v10\n"
"v_max_f32 v10,0,v10\n"
"v_exp_f32 v11,-v10\n"
AR "v12,a0\n"
"v_subrev_f32 v12,s13,v12\n"
"v_fma_f32 v12,v12,v11,s13\n"
AW "a0,v12\n"
AR "v12,a20\n"
"v_subrev_f32 v12,s13,v12\n"
"v_fma_f32 v12,v12,v11,s13\n"
AW "a20,v12\n"
AR "v12,a40\n"
"v_subrev_f32 v12,s13,v12\n"
"v_fma_f32 v12,v12,v11,s13\n"
AW "a40,v12\n"
AR "v12,a60\n"
"v_subrev_f32 v12,s13,v12\n"
"v_fma_f32 v12,v12,v11,s13\n"
AW "a60,v12\n"
AR "v12,a80\n"
"v_subrev_f32 v12,s13,v12\n"
"v_fma_f32 v12,v12,v11,s13\n"
AW "a80,v12\n"
AR "v12,a100\n"
"v_subrev_f32 v12,s13,v12\n"
"v_fma_f32 v12,v12,v11,s13\n"
AW "a100,v12\n"
AR "v12,a120\n"
"v_subrev_f32 v12,s13,v12\n"
"v_fma_f32 v12,v12,v11,s13\n"
AW "a120,v12\n"
AR "v12,a140\n"
"v_subrev_f32 v12,s13,v12\n"
"v_fma_f32 v12,v12,v11,s13\n"
AW "a140,v12\n"
AR "v12,a160\n"
"v_subrev_f32 v12,s13,v12\n"
"v_fma_f32 v12,v12,v11,s13\n"
AW "a160,v12\n"
AR "v12,a180\n"
"v_subrev_f32 v12,s13,v12\n"
"v_fma_f32 v12,v12,v11,s13\n"
AW "a180,v12\n"
AR "v12,a200\n"
"v_subrev_f32 v12,s13,v12\n"
"v_fma_f32 v12,v12,v11,s13\n"
AW "a200,v12\n"
AR "v12,a220\n"
"v_subrev_f32 v12,s13,v12\n"
"v_fma_f32 v12,v12,v11,s13\n"
AW "a220,v12\n"
AR "v12,a1\n"
"v_subrev_f32 v12,s13,v12\n"
"v_fma_f32 v12,v12,v11,s13\n"
AW "a1,v12\n"
AR "v12,a21\n"
"v_subrev_f32 v12,s13,v12\n"
"v_fma_f32 v12,v12,v11,s13\n"
AW "a21,v12\n"
AR "v12,a41\n"
"v_subrev_f32 v12,s13,v12\n"
"v_fma_f32 v12,v12,v11,s13\n"
AW "a41,v12\n"
AR "v12,a61\n"
"v_subrev_f32 v12,s13,v12\n"
"v_fma_f32 v12,v12,v11,s13\n"
AW "a61,v12\n"
AR "v12,a81\n"
"v_subrev_f32 v12,s13,v12\n"
"v_fma_f32 v12,v12,v11,s13\n"
AW "a81,v12\n"
AR "v12,a101\n"
"v_subrev_f32 v12,s13,v12\n"
"v_fma_f32 v12,v12,v11,s13\n"
AW "a101,v12\n"
AR "v12,a121\n"
"v_subrev_f32 v12,s13,v12\n"
"v_fma_f32 v12,v12,v11,s13\n"
AW "a121,v12\n"
AR "v12,a141\n"
"v_subrev_f32 v12,s13,v12\n"
"v_fma_f32 v12,v12,v11,s13\n"
AW "a141,v12\n"
AR "v12,a161\n"
"v_subrev_f32 v12,s13,v12\n"
"v_fma_f32 v12,v12,v11,s13\n"
AW "a161,v12\n"
AR "v12,a181\n"
"v_subrev_f32 v12,s13,v12\n"
"v_fma_f32 v12,v12,v11,s13\n"
AW "a181,v12\n"
AR "v12,a201\n"
"v_subrev_f32 v12,s13,v12\n"
"v_fma_f32 v12,v12,v11,s13\n"
AW "a201,v12\n"
AR "v12,a221\n"
"v_subrev_f32 v12,s13,v12\n"
"v_fma_f32 v12,v12,v11,s13\n"
AW "a221,v12\n"
AR "v12,a2\n"
"v_subrev_f32 v12,s13,v12\n"
"v_fma_f32 v12,v12,v11,s13\n"
AW "a2,v12\n"
AR "v12,a22\n"
"v_subrev_f32 v12,s13,v12\n"
"v_fma_f32 v12,v12,v11,s13\n"
AW "a22,v12\n"
AR "v12,a42\n"
"v_subrev_f32 v12,s13,v12\n"
"v_fma_f32 v12,v12,v11,s13\n"
AW "a42,v12\n"
AR "v12,a62\n"
"v_subrev_f32 v12,s13,v12\n"
"v_fma_f32 v12,v12,v11,s13\n"
AW "a62,v12\n"
AR "v12,a82\n"
"v_subrev_f32 v12,s13,v12\n"
"v_fma_f32 v12,v12,v11,s13\n"
AW "a82,v12\n"
AR "v12,a102\n"
"v_subrev_f32 v12,s13,v12\n"
"v_fma_f32 v12,v12,v11,s13\n"
AW "a102,v12\n"
AR "v12,a122\n"
"v_subrev_f32 v12,s13,v12\n"
"v_fma_f32 v12,v12,v11,s13\n"
AW "a122,v12\n"
AR "v12,a142\n"
"v_subrev_f32 v12,s13,v12\n"
"v_fma_f32 v12,v12,v11,s13\n"
AW "a142,v12\n"
AR "v12,a162\n"
"v_subrev_f32 v12,s13,v12\n"
"v_fma_f32 v12,v12,v11,s13\n"
AW "a162,v12\n"
AR "v12,a182\n"
"v_subrev_f32 v12,s13,v12\n"
"v_fma_f32 v12,v12,v11,s13\n"
AW "a182,v12\n"
AR "v12,a202\n"
"v_subrev_f32 v12,s13,v12\n"
"v_fma_f32 v12,v12,v11,s13\n"
AW "a202,v12\n"
AR "v12,a222\n"
"v_subrev_f32 v12,s13,v12\n"
"v_fma_f32 v12,v12,v11,s13\n"
AW "a222,v12\n"
AR "v12,a3\n"
"v_subrev_f32 v12,s13,v12\n"
"v_fma_f32 v12,v12,v11,s13\n"
AW "a3,v12\n"
AR "v12,a23\n"
"v_subrev_f32 v12,s13,v12\n"
"v_fma_f32 v12,v12,v11,s13\n"
AW "a23,v12\n"
AR "v12,a43\n"
"v_subrev_f32 v12,s13,v12\n"
"v_fma_f32 v12,v12,v11,s13\n"
AW "a43,v12\n"
AR "v12,a63\n"
"v_subrev_f32 v12,s13,v12\n"
"v_fma_f32 v12,v12,v11,s13\n"
AW "a63,v12\n"
AR "v12,a83\n"
"v_subrev_f32 v12,s13,v12\n"
"v_fma_f32 v12,v12,v11,s13\n"
AW "a83,v12\n"
AR "v12,a103\n"
"v_subrev_f32 v12,s13,v12\n"
"v_fma_f32 v12,v12,v11,s13\n"
AW "a103,v12\n"
AR "v12,a123\n"
"v_subrev_f32 v12,s13,v12\n"
"v_fma_f32 v12,v12,v11,s13\n"
AW "a123,v12\n"
AR "v12,a143\n"
"v_subrev_f32 v12,s13,v12\n"
"v_fma_f32 v12,v12,v11,s13\n"
AW "a143,v12\n"
AR "v12,a163\n"
"v_subrev_f32 v12,s13,v12\n"
"v_fma_f32 v12,v12,v11,s13\n"
AW "a163,v12\n"
AR "v12,a183\n"
"v_subrev_f32 v12,s13,v12\n"
"v_fma_f32 v12,v12,v11,s13\n"
AW "a183,v12\n"
AR "v12,a203\n"
"v_subrev_f32 v12,s13,v12\n"
"v_fma_f32 v12,v12,v11,s13\n"
AW "a203,v12\n"
AR "v12,a223\n"
"v_subrev_f32 v12,s13,v12\n"
"v_fma_f32 v12,v12,v11,s13\n"
AW "a223,v12\n"
"v_mul_f32 v52,v57,v11\n"
"v_sub_f32 v32,v32,v10\n"
"v_sub_f32 v33,v33,v10\n"
"v_sub_f32 v34,v34,v10\n"
"v_sub_f32 v35,v35,v10\n"
"v_sub_f32 v92,v92,v10\n"
"v_sub_f32 v93,v93,v10\n"
"v_sub_f32 v94,v94,v10\n"
"v_sub_f32 v95,v95,v10\n"
"v_sub_f32 v72,v72,v10\n"
"v_sub_f32 v73,v73,v10\n"
"v_sub_f32 v74,v74,v10\n"
"v_sub_f32 v75,v75,v10\n"
"v_sub_f32 v112,v112,v10\n"
"v_sub_f32 v113,v113,v10\n"
"v_sub_f32 v114,v114,v10\n"
"v_sub_f32 v115,v115,v10\n"
"v_exp_f32 v72,v72\n"
"v_exp_f32 v73,v73\n"
"v_exp_f32 v74,v74\n"
"v_exp_f32 v75,v75\n"
"v_exp_f32 v112,v112\n"
"v_exp_f32 v113,v113\n"
"v_exp_f32 v114,v114\n"
"v_exp_f32 v115,v115\n"
"v_add_f32 v52,v52,v72\n"
"v_add_f32 v52,v52,v73\n"
"v_add_f32 v52,v52,v74\n"
"v_add_f32 v52,v52,v75\n"
"v_add_f32 v52,v52,v112\n"
"v_add_f32 v52,v52,v113\n"
"v_add_f32 v52,v52,v114\n"
"v_add_f32_e64 v52,v52,v115\n"
CV "v72,v72,v73\n"
CV "v73,v74,v75\n"
CV "v74,v112,v113\n"
CV "v75,v114,v115\n"
"v_max3_f32 v10,v76,v77,v78\n"
"v_max3_f32 v11,v79,v116,v117\n"
"v_max3_f32 v10,v10,v118,v119\n"
"v_max_f32 v10,v10,v11\n"
"v_mov_b32 v11,v10\n"
"s_nop 1\n"
"v_permlane16_swap_b32 v10,v11\n"
"v_max_f32 v10,v10,v11\n"
"v_mov_b32 v11,v10\n"
"s_nop 1\n"
"v_permlane32_swap_b32 v10,v11\n"
"v_max_f32 v10,v10,v11\n"
"v_add_f32 v10,0x41300000,v10\n"
"v_max_f32 v10,0,v10\n"
"v_exp_f32 v11,-v10\n"
AR "v12,a4\n"
"v_subrev_f32 v12,s13,v12\n"
"v_fma_f32 v12,v12,v11,s13\n"
AW "a4,v12\n"
AR "v12,a24\n"
"v_subrev_f32 v12,s13,v12\n"
"v_fma_f32 v12,v12,v11,s13\n"
AW "a24,v12\n"
AR "v12,a44\n"
"v_subrev_f32 v12,s13,v12\n"
"v_fma_f32 v12,v12,v11,s13\n"
AW "a44,v12\n"
AR "v12,a64\n"
"v_subrev_f32 v12,s13,v12\n"
"v_fma_f32 v12,v12,v11,s13\n"
AW "a64,v12\n"
AR "v12,a84\n"
"v_subrev_f32 v12,s13,v12\n"
"v_fma_f32 v12,v12,v11,s13\n"
AW "a84,v12\n"
AR "v12,a104\n"
"v_subrev_f32 v12,s13,v12\n"
"v_fma_f32 v12,v12,v11,s13\n"
AW "a104,v12\n"
AR "v12,a124\n"
"v_subrev_f32 v12,s13,v12\n"
"v_fma_f32 v12,v12,v11,s13\n"
AW "a124,v12\n"
AR "v12,a144\n"
"v_subrev_f32 v12,s13,v12\n"
"v_fma_f32 v12,v12,v11,s13\n"
AW "a144,v12\n"
AR "v12,a164\n"
"v_subrev_f32 v12,s13,v12\n"
"v_fma_f32 v12,v12,v11,s13\n"
AW "a164,v12\n"
AR "v12,a184\n"
"v_subrev_f32 v12,s13,v12\n"
"v_fma_f32 v12,v12,v11,s13\n"
AW "a184,v12\n"
AR "v12,a204\n"
"v_subrev_f32 v12,s13,v12\n"
"v_fma_f32 v12,v12,v11,s13\n"
AW "a204,v12\n"
AR "v12,a224\n"
"v_subrev_f32 v12,s13,v12\n"
"v_fma_f32 v12,v12,v11,s13\n"
AW "a224,v12\n"
AR "v12,a5\n"
"v_subrev_f32 v12,s13,v12\n"
"v_fma_f32 v12,v12,v11,s13\n"
AW "a5,v12\n"
AR "v12,a25\n"
"v_subrev_f32 v12,s13,v12\n"
"v_fma_f32 v12,v12,v11,s13\n"
AW "a25,v12\n"
AR "v12,a45\n"
"v_subrev_f32 v12,s13,v12\n"
"v_fma_f32 v12,v12,v11,s13\n"
AW "a45,v12\n"
AR "v12,a65\n"
"v_subrev_f32 v12,s13,v12\n"
"v_fma_f32 v12,v12,v11,s13\n"
AW "a65,v12\n"
AR "v12,a85\n"
"v_subrev_f32 v12,s13,v12\n"
"v_fma_f32 v12,v12,v11,s13\n"
AW "a85,v12\n"
AR "v12,a105\n"
"v_subrev_f32 v12,s13,v12\n"
"v_fma_f32 v12,v12,v11,s13\n"
AW "a105,v12\n"
AR "v12,a125\n"
"v_subrev_f32 v12,s13,v12\n"
"v_fma_f32 v12,v12,v11,s13\n"
AW "a125,v12\n"
AR "v12,a145\n"
"v_subrev_f32 v12,s13,v12\n"
"v_fma_f32 v12,v12,v11,s13\n"
AW "a145,v12\n"
AR "v12,a165\n"
"v_subrev_f32 v12,s13,v12\n"
"v_fma_f32 v12,v12,v11,s13\n"
AW "a165,v12\n"
AR "v12,a185\n"
"v_subrev_f32 v12,s13,v12\n"
"v_fma_f32 v12,v12,v11,s13\n"
AW "a185,v12\n"
AR "v12,a205\n"
"v_subrev_f32 v12,s13,v12\n"
"v_fma_f32 v12,v12,v11,s13\n"
AW "a205,v12\n"
AR "v12,a225\n"
"v_subrev_f32 v12,s13,v12\n"
"v_fma_f32 v12,v12,v11,s13\n"
AW "a225,v12\n"
AR "v12,a6\n"
"v_subrev_f32 v12,s13,v12\n"
"v_fma_f32 v12,v12,v11,s13\n"
AW "a6,v12\n"
AR "v12,a26\n"
"v_subrev_f32 v12,s13,v12\n"
"v_fma_f32 v12,v12,v11,s13\n"
AW "a26,v12\n"
AR "v12,a46\n"
"v_subrev_f32 v12,s13,v12\n"
"v_fma_f32 v12,v12,v11,s13\n"
AW "a46,v12\n"
AR "v12,a66\n"
"v_subrev_f32 v12,s13,v12\n"
"v_fma_f32 v12,v12,v11,s13\n"
AW "a66,v12\n"
AR "v12,a86\n"
"v_subrev_f32 v12,s13,v12\n"
"v_fma_f32 v12,v12,v11,s13\n"
AW "a86,v12\n"
AR "v12,a106\n"
"v_subrev_f32 v12,s13,v12\n"
"v_fma_f32 v12,v12,v11,s13\n"
AW "a106,v12\n"
AR "v12,a126\n"
"v_subrev_f32 v12,s13,v12\n"
"v_fma_f32 v12,v12,v11,s13\n"
AW "a126,v12\n"
AR "v12,a146\n"
"v_subrev_f32 v12,s13,v12\n"
"v_fma_f32 v12,v12,v11,s13\n"
AW "a146,v12\n"
AR "v12,a166\n"
"v_subrev_f32 v12,s13,v12\n"
"v_fma_f32 v12,v12,v11,s13\n"
AW "a166,v12\n"
AR "v12,a186\n"
"v_subrev_f32 v12,s13,v12\n"
"v_fma_f32 v12,v12,v11,s13\n"
AW "a186,v12\n"
AR "v12,a206\n"
"v_subrev_f32 v12,s13,v12\n"
"v_fma_f32 v12,v12,v11,s13\n"
AW "a206,v12\n"
AR "v12,a226\n"
"v_subrev_f32 v12,s13,v12\n"
"v_fma_f32 v12,v12,v11,s13\n"
AW "a226,v12\n"
AR "v12,a7\n"
"v_subrev_f32 v12,s13,v12\n"
"v_fma_f32 v12,v12,v11,s13\n"
AW "a7,v12\n"
AR "v12,a27\n"
"v_subrev_f32 v12,s13,v12\n"
"v_fma_f32 v12,v12,v11,s13\n"
AW "a27,v12\n"
AR "v12,a47\n"
"v_subrev_f32 v12,s13,v12\n"
"v_fma_f32 v12,v12,v11,s13\n"
AW "a47,v12\n"
AR "v12,a67\n"
"v_subrev_f32 v12,s13,v12\n"
"v_fma_f32 v12,v12,v11,s13\n"
AW "a67,v12\n"
AR "v12,a87\n"
"v_subrev_f32 v12,s13,v12\n"
"v_fma_f32 v12,v12,v11,s13\n"
AW "a87,v12\n"
AR "v12,a107\n"
"v_subrev_f32 v12,s13,v12\n"
"v_fma_f32 v12,v12,v11,s13\n"
AW "a107,v12\n"
AR "v12,a127\n"
"v_subrev_f32 v12,s13,v12\n"
"v_fma_f32 v12,v12,v11,s13\n"
AW "a127,v12\n"
AR "v12,a147\n"
"v_subrev_f32 v12,s13,v12\n"
"v_fma_f32 v12,v12,v11,s13\n"
AW "a147,v12\n"
AR "v12,a167\n"
"v_subrev_f32 v12,s13,v12\n"
"v_fma_f32 v12,v12,v11,s13\n"
AW "a167,v12\n"
AR "v12,a187\n"
"v_subrev_f32 v12,s13,v12\n"
"v_fma_f32 v12,v12,v11,s13\n"
AW "a187,v12\n"
AR "v12,a207\n"
"v_subrev_f32 v12,s13,v12\n"
"v_fma_f32 v12,v12,v11,s13\n"
AW "a207,v12\n"
AR "v12,a227\n"
"v_subrev_f32 v12,s13,v12\n"
"v_fma_f32 v12,v12,v11,s13\n"
AW "a227,v12\n"
"v_mul_f32 v53,v58,v11\n"
"v_sub_f32 v36,v36,v10\n"
"v_sub_f32 v37,v37,v10\n"
"v_sub_f32 v38,v38,v10\n"
"v_sub_f32 v39,v39,v10\n"
"v_sub_f32 v96,v96,v10\n"
"v_sub_f32 v97,v97,v10\n"
"v_sub_f32 v98,v98,v10\n"
"v_sub_f32 v99,v99,v10\n"
"v_sub_f32 v76,v76,v10\n"
"v_sub_f32 v77,v77,v10\n"
"v_sub_f32 v78,v78,v10\n"
"v_sub_f32 v79,v79,v10\n"
"v_sub_f32 v116,v116,v10\n"
"v_sub_f32 v117,v117,v10\n"
"v_sub_f32 v118,v118,v10\n"
"v_sub_f32 v119,v119,v10\n"
"v_exp_f32 v76,v76\n"
"v_exp_f32 v77,v77\n"
"v_exp_f32 v78,v78\n"
"v_exp_f32 v79,v79\n"
"v_exp_f32 v116,v116\n"
"v_exp_f32 v117,v117\n"
"v_exp_f32 v118,v118\n"
"v_exp_f32 v119,v119\n"
"v_add_f32 v53,v53,v76\n"
"v_add_f32 v53,v53,v77\n"
"v_add_f32 v53,v53,v78\n"
"v_add_f32 v53,v53,v79\n"
"v_add_f32 v53,v53,v116\n"
"v_add_f32 v53,v53,v117\n"
"v_add_f32 v53,v53,v118\n"
"v_add_f32_e64 v53,v53,v119\n"
CV "v76,v76,v77\n"
CV "v77,v78,v79\n"
CV "v78,v116,v117\n"
CV "v79,v118,v119\n"
"v_max3_f32 v10,v80,v81,v82\n"
"v_max3_f32 v11,v83,v120,v121\n"
"v_max3_f32 v10,v10,v122,v123\n"
"v_max_f32 v10,v10,v11\n"
"v_mov_b32 v11,v10\n"
"s_nop 1\n"
"v_permlane16_swap_b32 v10,v11\n"
"v_max_f32 v10,v10,v11\n"
"v_mov_b32 v11,v10\n"
"s_nop 1\n"
"v_permlane32_swap_b32 v10,v11\n"
"v_max_f32_e64 v10,v10,v11\n"
"v_add_f32 v10,0x41300000,v10\n"
"v_max_f32 v10,0,v10\n"
"v_exp_f32 v11,-v10\n"
AR "v12,a8\n"
"v_subrev_f32 v12,s13,v12\n"
"v_fma_f32 v12,v12,v11,s13\n"
AW "a8,v12\n"
AR "v12,a28\n"
"v_subrev_f32 v12,s13,v12\n"
"v_fma_f32 v12,v12,v11,s13\n"
AW "a28,v12\n"
AR "v12,a48\n"
"v_subrev_f32 v12,s13,v12\n"
"v_fma_f32 v12,v12,v11,s13\n"
AW "a48,v12\n"
AR "v12,a68\n"
"v_subrev_f32 v12,s13,v12\n"
"v_fma_f32 v12,v12,v11,s13\n"
AW "a68,v12\n"
AR "v12,a88\n"
"v_subrev_f32 v12,s13,v12\n"
"v_fma_f32 v12,v12,v11,s13\n"
AW "a88,v12\n"
AR "v12,a108\n"
"v_subrev_f32 v12,s13,v12\n"
"v_fma_f32 v12,v12,v11,s13\n"
AW "a108,v12\n"
AR "v12,a128\n"
"v_subrev_f32 v12,s13,v12\n"
"v_fma_f32 v12,v12,v11,s13\n"
AW "a128,v12\n"
AR "v12,a148\n"
"v_subrev_f32 v12,s13,v12\n"
"v_fma_f32 v12,v12,v11,s13\n"
AW "a148,v12\n"
AR "v12,a168\n"
"v_subrev_f32 v12,s13,v12\n"
"v_fma_f32 v12,v12,v11,s13\n"
AW "a168,v12\n"
AR "v12,a188\n"
"v_subrev_f32 v12,s13,v12\n"
"v_fma_f32 v12,v12,v11,s13\n"
AW "a188,v12\n"
AR "v12,a208\n"
"v_subrev_f32 v12,s13,v12\n"
"v_fma_f32 v12,v12,v11,s13\n"
AW "a208,v12\n"
AR "v12,a228\n"
"v_subrev_f32 v12,s13,v12\n"
"v_fma_f32 v12,v12,v11,s13\n"
AW "a228,v12\n"
AR "v12,a9\n"
"v_subrev_f32 v12,s13,v12\n"
"v_fma_f32 v12,v12,v11,s13\n"
AW "a9,v12\n"
AR "v12,a29\n"
"v_subrev_f32 v12,s13,v12\n"
"v_fma_f32 v12,v12,v11,s13\n"
AW "a29,v12\n"
AR "v12,a49\n"
"v_subrev_f32 v12,s13,v12\n"
"v_fma_f32 v12,v12,v11,s13\n"
AW "a49,v12\n"
AR "v12,a69\n"
"v_subrev_f32 v12,s13,v12\n"
"v_fma_f32 v12,v12,v11,s13\n"
AW "a69,v12\n"
AR "v12,a89\n"
"v_subrev_f32 v12,s13,v12\n"
"v_fma_f32 v12,v12,v11,s13\n"
AW "a89,v12\n"
AR "v12,a109\n"
"v_subrev_f32 v12,s13,v12\n"
"v_fma_f32 v12,v12,v11,s13\n"
AW "a109,v12\n"
AR "v12,a129\n"
"v_subrev_f32 v12,s13,v12\n"
"v_fma_f32 v12,v12,v11,s13\n"
AW "a129,v12\n"
AR "v12,a149\n"
"v_subrev_f32 v12,s13,v12\n"
"v_fma_f32 v12,v12,v11,s13\n"
AW "a149,v12\n"
AR "v12,a169\n"
"v_subrev_f32 v12,s13,v12\n"
"v_fma_f32 v12,v12,v11,s13\n"
AW "a169,v12\n"
AR "v12,a189\n"
"v_subrev_f32 v12,s13,v12\n"
"v_fma_f32 v12,v12,v11,s13\n"
AW "a189,v12\n"
AR "v12,a209\n"
"v_subrev_f32 v12,s13,v12\n"
"v_fma_f32 v12,v12,v11,s13\n"
AW "a209,v12\n"
AR "v12,a229\n"
"v_subrev_f32 v12,s13,v12\n"
"v_fma_f32 v12,v12,v11,s13\n"
AW "a229,v12\n"
AR "v12,a10\n"
"v_subrev_f32 v12,s13,v12\n"
"v_fma_f32 v12,v12,v11,s13\n"
AW "a10,v12\n"
AR "v12,a30\n"
"v_subrev_f32 v12,s13,v12\n"
"v_fma_f32 v12,v12,v11,s13\n"
AW "a30,v12\n"
AR "v12,a50\n"
"v_subrev_f32 v12,s13,v12\n"
"v_fma_f32 v12,v12,v11,s13\n"
AW "a50,v12\n"
AR "v12,a70\n"
"v_subrev_f32 v12,s13,v12\n"
"v_fma_f32 v12,v12,v11,s13\n"
AW "a70,v12\n"
AR "v12,a90\n"
"v_subrev_f32 v12,s13,v12\n"
"v_fma_f32 v12,v12,v11,s13\n"
AW "a90,v12\n"
AR "v12,a110\n"
"v_subrev_f32 v12,s13,v12\n"
"v_fma_f32 v12,v12,v11,s13\n"
AW "a110,v12\n"
AR "v12,a130\n"
"v_subrev_f32 v12,s13,v12\n"
"v_fma_f32 v12,v12,v11,s13\n"
AW "a130,v12\n"
AR "v12,a150\n"
"v_subrev_f32 v12,s13,v12\n"
"v_fma_f32 v12,v12,v11,s13\n"
AW "a150,v12\n"
AR "v12,a170\n"
"v_subrev_f32 v12,s13,v12\n"
"v_fma_f32 v12,v12,v11,s13\n"
AW "a170,v12\n"
AR "v12,a190\n"
"v_subrev_f32 v12,s13,v12\n"
"v_fma_f32 v12,v12,v11,s13\n"
AW "a190,v12\n"
AR "v12,a210\n"
"v_subrev_f32 v12,s13,v12\n"
"v_fma_f32 v12,v12,v11,s13\n"
AW "a210,v12\n"
AR "v12,a230\n"
"v_subrev_f32 v12,s13,v12\n"
"v_fma_f32 v12,v12,v11,s13\n"
AW "a230,v12\n"
AR "v12,a11\n"
"v_subrev_f32 v12,s13,v12\n"
"v_fma_f32 v12,v12,v11,s13\n"
AW "a11,v12\n"
AR "v12,a31\n"
"v_subrev_f32 v12,s13,v12\n"
"v_fma_f32 v12,v12,v11,s13\n"
AW "a31,v12\n"
AR "v12,a51\n"
"v_subrev_f32 v12,s13,v12\n"
"v_fma_f32 v12,v12,v11,s13\n"
AW "a51,v12\n"
AR "v12,a71\n"
"v_subrev_f32 v12,s13,v12\n"
"v_fma_f32 v12,v12,v11,s13\n"
AW "a71,v12\n"
AR "v12,a91\n"
"v_subrev_f32 v12,s13,v12\n"
"v_fma_f32 v12,v12,v11,s13\n"
AW "a91,v12\n"
AR "v12,a111\n"
"v_subrev_f32 v12,s13,v12\n"
"v_fma_f32 v12,v12,v11,s13\n"
AW "a111,v12\n"
AR "v12,a131\n"
"v_subrev_f32 v12,s13,v12\n"
"v_fma_f32 v12,v12,v11,s13\n"
AW "a131,v12\n"
AR "v12,a151\n"
"v_subrev_f32 v12,s13,v12\n"
"v_fma_f32 v12,v12,v11,s13\n"
AW "a151,v12\n"
AR "v12,a171\n"
"v_subrev_f32 v12,s13,v12\n"
"v_fma_f32 v12,v12,v11,s13\n"
AW "a171,v12\n"
AR "v12,a191\n"
"v_subrev_f32 v12,s13,v12\n"
"v_fma_f32 v12,v12,v11,s13\n"
AW "a191,v12\n"
AR "v12,a211\n"
"v_subrev_f32 v12,s13,v12\n"
"v_fma_f32 v12,v12,v11,s13\n"
AW "a211,v12\n"
AR "v12,a231\n"
"v_subrev_f32 v12,s13,v12\n"
"v_fma_f32 v12,v12,v11,s13\n"
AW "a231,v12\n"
"v_mul_f32 v54,v59,v11\n"
"v_sub_f32 v40,v40,v10\n"
"v_sub_f32 v41,v41,v10\n"
"v_sub_f32 v42,v42,v10\n"
"v_sub_f32 v43,v43,v10\n"
"v_sub_f32 v100,v100,v10\n"
"v_sub_f32 v101,v101,v10\n"
"v_sub_f32 v102,v102,v10\n"
"v_sub_f32 v103,v103,v10\n"
"v_sub_f32 v80,v80,v10\n"
"v_sub_f32 v81,v81,v10\n"
"v_sub_f32 v82,v82,v10\n"
"v_sub_f32 v83,v83,v10\n"
"v_sub_f32 v120,v120,v10\n"
"v_sub_f32 v121,v121,v10\n"
"v_sub_f32 v122,v122,v10\n"
"v_sub_f32 v123,v123,v10\n"
"v_exp_f32 v80,v80\n"
"v_exp_f32 v81,v81\n"
"v_exp_f32 v82,v82\n"
"v_exp_f32 v83,v83\n"
"v_exp_f32 v120,v120\n"
"v_exp_f32 v121,v121\n"
"v_exp_f32 v122,v122\n"
"v_exp_f32 v123,v123\n"
"v_add_f32 v54,v54,v80\n"
"v_add_f32 v54,v54,v81\n"
"v_add_f32 v54,v54,v82\n"
"v_add_f32 v54,v54,v83\n"
"v_add_f32 v54,v54,v120\n"
"v_add_f32 v54,v54,v121\n"
"v_add_f32 v54,v54,v122\n"
"v_add_f32 v54,v54,v123\n"
CV "v80,v80,v81\n"
CV "v81,v82,v83\n"
CV "v82,v120,v121\n"
CV "v83,v122,v123\n"
"v_max3_f32 v10,v84,v85,v86\n"
"v_max3_f32 v11,v87,v124,v125\n"
"v_max3_f32 v10,v10,v126,v127\n"
"v_max_f32 v10,v10,v11\n"
"v_mov_b32 v11,v10\n"
"s_nop 1\n"
"v_permlane16_swap_b32 v10,v11\n"
"v_max_f32 v10,v10,v11\n"
"v_mov_b32 v11,v10\n"
"s_nop 1\n"
"v_permlane32_swap_b32 v10,v11\n"
"v_max_f32 v10,v10,v11\n"
"v_add_f32 v10,0x41300000,v10\n"
"v_max_f32 v10,0,v10\n"
"v_exp_f32 v11,-v10\n"
AR "v12,a12\n"
"v_subrev_f32 v12,s13,v12\n"
"v_fma_f32 v12,v12,v11,s13\n"
AW "a12,v12\n"
AR "v12,a32\n"
"v_subrev_f32 v12,s13,v12\n"
"v_fma_f32 v12,v12,v11,s13\n"
AW "a32,v12\n"
AR "v12,a52\n"
"v_subrev_f32 v12,s13,v12\n"
"v_fma_f32 v12,v12,v11,s13\n"
AW "a52,v12\n"
AR "v12,a72\n"
"v_subrev_f32 v12,s13,v12\n"
"v_fma_f32 v12,v12,v11,s13\n"
AW "a72,v12\n"
AR "v12,a92\n"
"v_subrev_f32 v12,s13,v12\n"
"v_fma_f32 v12,v12,v11,s13\n"
AW "a92,v12\n"
AR "v12,a112\n"
"v_subrev_f32 v12,s13,v12\n"
"v_fma_f32 v12,v12,v11,s13\n"
AW "a112,v12\n"
AR "v12,a132\n"
"v_subrev_f32 v12,s13,v12\n"
"v_fma_f32 v12,v12,v11,s13\n"
AW "a132,v12\n"
AR "v12,a152\n"
"v_subrev_f32 v12,s13,v12\n"
"v_fma_f32 v12,v12,v11,s13\n"
AW "a152,v12\n"
AR "v12,a172\n"
"v_subrev_f32 v12,s13,v12\n"
"v_fma_f32 v12,v12,v11,s13\n"
AW "a172,v12\n"
AR "v12,a192\n"
"v_subrev_f32 v12,s13,v12\n"
"v_fma_f32 v12,v12,v11,s13\n"
AW "a192,v12\n"
AR "v12,a212\n"
"v_subrev_f32 v12,s13,v12\n"
"v_fma_f32 v12,v12,v11,s13\n"
AW "a212,v12\n"
AR "v12,a232\n"
"v_subrev_f32 v12,s13,v12\n"
"v_fma_f32 v12,v12,v11,s13\n"
AW "a232,v12\n"
AR "v12,a13\n"
"v_subrev_f32 v12,s13,v12\n"
"v_fma_f32 v12,v12,v11,s13\n"
AW "a13,v12\n"
AR "v12,a33\n"
"v_subrev_f32 v12,s13,v12\n"
"v_fma_f32 v12,v12,v11,s13\n"
AW "a33,v12\n"
AR "v12,a53\n"
"v_subrev_f32 v12,s13,v12\n"
"v_fma_f32 v12,v12,v11,s13\n"
AW "a53,v12\n"
AR "v12,a73\n"
"v_subrev_f32 v12,s13,v12\n"
"v_fma_f32 v12,v12,v11,s13\n"
AW "a73,v12\n"
AR "v12,a93\n"
"v_subrev_f32 v12,s13,v12\n"
"v_fma_f32 v12,v12,v11,s13\n"
AW "a93,v12\n"
AR "v12,a113\n"
"v_subrev_f32 v12,s13,v12\n"
"v_fma_f32 v12,v12,v11,s13\n"
AW "a113,v12\n"
AR "v12,a133\n"
"v_subrev_f32 v12,s13,v12\n"
"v_fma_f32 v12,v12,v11,s13\n"
AW "a133,v12\n"
AR "v12,a153\n"
"v_subrev_f32 v12,s13,v12\n"
"v_fma_f32 v12,v12,v11,s13\n"
AW "a153,v12\n"
AR "v12,a173\n"
"v_subrev_f32 v12,s13,v12\n"
"v_fma_f32 v12,v12,v11,s13\n"
AW "a173,v12\n"
AR "v12,a193\n"
"v_subrev_f32 v12,s13,v12\n"
"v_fma_f32 v12,v12,v11,s13\n"
AW "a193,v12\n"
AR "v12,a213\n"
"v_subrev_f32 v12,s13,v12\n"
"v_fma_f32 v12,v12,v11,s13\n"
AW "a213,v12\n"
AR "v12,a233\n"
"v_subrev_f32 v12,s13,v12\n"
"v_fma_f32 v12,v12,v11,s13\n"
AW "a233,v12\n"
AR "v12,a14\n"
"v_subrev_f32 v12,s13,v12\n"
"v_fma_f32 v12,v12,v11,s13\n"
AW "a14,v12\n"
AR "v12,a34\n"
"v_subrev_f32 v12,s13,v12\n"
"v_fma_f32 v12,v12,v11,s13\n"
AW "a34,v12\n"
AR "v12,a54\n"
"v_subrev_f32 v12,s13,v12\n"
"v_fma_f32 v12,v12,v11,s13\n"
AW "a54,v12\n"
AR "v12,a74\n"
"v_subrev_f32 v12,s13,v12\n"
"v_fma_f32 v12,v12,v11,s13\n"
AW "a74,v12\n"
AR "v12,a94\n"
"v_subrev_f32 v12,s13,v12\n"
"v_fma_f32 v12,v12,v11,s13\n"
AW "a94,v12\n"
AR "v12,a114\n"
"v_subrev_f32 v12,s13,v12\n"
"v_fma_f32 v12,v12,v11,s13\n"
AW "a114,v12\n"
AR "v12,a134\n"
"v_subrev_f32 v12,s13,v12\n"
"v_fma_f32 v12,v12,v11,s13\n"
AW "a134,v12\n"
AR "v12,a154\n"
"v_subrev_f32 v12,s13,v12\n"
"v_fma_f32 v12,v12,v11,s13\n"
AW "a154,v12\n"
AR "v12,a174\n"
"v_subrev_f32 v12,s13,v12\n"
"v_fma_f32 v12,v12,v11,s13\n"
AW "a174,v12\n"
AR "v12,a194\n"
"v_subrev_f32 v12,s13,v12\n"
"v_fma_f32 v12,v12,v11,s13\n"
AW "a194,v12\n"
AR "v12,a214\n"
"v_subrev_f32 v12,s13,v12\n"
"v_fma_f32 v12,v12,v11,s13\n"
AW "a214,v12\n"
AR "v12,a234\n"
"v_subrev_f32 v12,s13,v12\n"
"v_fma_f32 v12,v12,v11,s13\n"
AW "a234,v12\n"
AR "v12,a15\n"
"v_subrev_f32 v12,s13,v12\n"
"v_fma_f32 v12,v12,v11,s13\n"
AW "a15,v12\n"
AR "v12,a35\n"
"v_subrev_f32 v12,s13,v12\n"
"v_fma_f32 v12,v12,v11,s13\n"
AW "a35,v12\n"
AR "v12,a55\n"
"v_subrev_f32 v12,s13,v12\n"
"v_fma_f32 v12,v12,v11,s13\n"
AW "a55,v12\n"
AR "v12,a75\n"
"v_subrev_f32 v12,s13,v12\n"
"v_fma_f32 v12,v12,v11,s13\n"
AW "a75,v12\n"
AR "v12,a95\n"
"v_subrev_f32 v12,s13,v12\n"
"v_fma_f32 v12,v12,v11,s13\n"
AW "a95,v12\n"
AR "v12,a115\n"
"v_subrev_f32 v12,s13,v12\n"
"v_fma_f32 v12,v12,v11,s13\n"
AW "a115,v12\n"
AR "v12,a135\n"
"v_subrev_f32 v12,s13,v12\n"
"v_fma_f32 v12,v12,v11,s13\n"
AW "a135,v12\n"
AR "v12,a155\n"
"v_subrev_f32 v12,s13,v12\n"
"v_fma_f32 v12,v12,v11,s13\n"
AW "a155,v12\n"
AR "v12,a175\n"
"v_subrev_f32 v12,s13,v12\n"
"v_fma_f32 v12,v12,v11,s13\n"
AW "a175,v12\n"
AR "v12,a195\n"
"v_subrev_f32 v12,s13,v12\n"
"v_fma_f32 v12,v12,v11,s13\n"
AW "a195,v12\n"
AR "v12,a215\n"
"v_subrev_f32 v12,s13,v12\n"
"v_fma_f32 v12,v12,v11,s13\n"
AW "a215,v12\n"
AR "v12,a235\n"
"v_subrev_f32 v12,s13,v12\n"
"v_fma_f32 v12,v12,v11,s13\n"
AW "a235,v12\n"
"v_mul_f32 v55,v60,v11\n"
"v_sub_f32 v44,v44,v10\n"
"v_sub_f32 v45,v45,v10\n"
"v_sub_f32 v46,v46,v10\n"
"v_sub_f32 v47,v47,v10\n"
"v_sub_f32 v104,v104,v10\n"
"v_sub_f32 v105,v105,v10\n"
"v_sub_f32 v106,v106,v10\n"
"v_sub_f32 v107,v107,v10\n"
"v_sub_f32 v84,v84,v10\n"
"v_sub_f32 v85,v85,v10\n"
"v_sub_f32 v86,v86,v10\n"
"v_sub_f32 v87,v87,v10\n"
"v_sub_f32 v124,v124,v10\n"
"v_sub_f32 v125,v125,v10\n"
"v_sub_f32 v126,v126,v10\n"
"v_sub_f32 v127,v127,v10\n"
"v_exp_f32 v84,v84\n"
"v_exp_f32 v85,v85\n"
"v_exp_f32 v86,v86\n"
"v_exp_f32 v87,v87\n"
"v_exp_f32 v124,v124\n"
"v_exp_f32 v125,v125\n"
"v_exp_f32 v126,v126\n"
"v_exp_f32 v127,v127\n"
"v_add_f32 v55,v55,v84\n"
"v_add_f32 v55,v55,v85\n"
"v_add_f32 v55,v55,v86\n"
"v_add_f32 v55,v55,v87\n"
"v_add_f32 v55,v55,v124\n"
"v_add_f32 v55,v55,v125\n"
"v_add_f32 v55,v55,v126\n"
"v_add_f32_e64 v55,v55,v127\n"
CV "v84,v84,v85\n"
CV "v85,v86,v87\n"
CV "v86,v124,v125\n"
CV "v87,v126,v127\n"
"v_max3_f32 v10,v88,v89,v90\n"
"v_max3_f32 v11,v91,v128,v129\n"
"v_max3_f32 v10,v10,v130,v131\n"
"v_max_f32 v10,v10,v11\n"
"v_mov_b32 v11,v10\n"
"s_nop 1\n"
"v_permlane16_swap_b32 v10,v11\n"
"v_max_f32 v10,v10,v11\n"
"v_mov_b32 v11,v10\n"
"s_nop 1\n"
"v_permlane32_swap_b32 v10,v11\n"
"v_max_f32 v10,v10,v11\n"
"v_add_f32 v10,0x41300000,v10\n"
"v_max_f32 v10,0,v10\n"
"v_exp_f32 v11,-v10\n"
AR "v12,a16\n"
"v_subrev_f32 v12,s13,v12\n"
"v_fma_f32 v12,v12,v11,s13\n"
AW "a16,v12\n"
AR "v12,a36\n"
"v_subrev_f32 v12,s13,v12\n"
"v_fma_f32 v12,v12,v11,s13\n"
AW "a36,v12\n"
AR "v12,a56\n"
"v_subrev_f32 v12,s13,v12\n"
"v_fma_f32 v12,v12,v11,s13\n"
AW "a56,v12\n"
AR "v12,a76\n"
"v_subrev_f32 v12,s13,v12\n"
"v_fma_f32 v12,v12,v11,s13\n"
AW "a76,v12\n"
AR "v12,a96\n"
"v_subrev_f32 v12,s13,v12\n"
"v_fma_f32 v12,v12,v11,s13\n"
AW "a96,v12\n"
AR "v12,a116\n"
"v_subrev_f32 v12,s13,v12\n"
"v_fma_f32 v12,v12,v11,s13\n"
AW "a116,v12\n"
AR "v12,a136\n"
"v_subrev_f32 v12,s13,v12\n"
"v_fma_f32 v12,v12,v11,s13\n"
AW "a136,v12\n"
AR "v12,a156\n"
"v_subrev_f32 v12,s13,v12\n"
"v_fma_f32 v12,v12,v11,s13\n"
AW "a156,v12\n"
AR "v12,a176\n"
"v_subrev_f32 v12,s13,v12\n"
"v_fma_f32 v12,v12,v11,s13\n"
AW "a176,v12\n"
AR "v12,a196\n"
"v_subrev_f32 v12,s13,v12\n"
"v_fma_f32 v12,v12,v11,s13\n"
AW "a196,v12\n"
AR "v12,a216\n"
"v_subrev_f32 v12,s13,v12\n"
"v_fma_f32 v12,v12,v11,s13\n"
AW "a216,v12\n"
AR "v12,a236\n"
"v_subrev_f32 v12,s13,v12\n"
"v_fma_f32 v12,v12,v11,s13\n"
AW "a236,v12\n"
AR "v12,a17\n"
"v_subrev_f32 v12,s13,v12\n"
"v_fma_f32 v12,v12,v11,s13\n"
AW "a17,v12\n"
AR "v12,a37\n"
"v_subrev_f32 v12,s13,v12\n"
"v_fma_f32 v12,v12,v11,s13\n"
AW "a37,v12\n"
AR "v12,a57\n"
"v_subrev_f32 v12,s13,v12\n"
"v_fma_f32 v12,v12,v11,s13\n"
AW "a57,v12\n"
AR "v12,a77\n"
"v_subrev_f32 v12,s13,v12\n"
"v_fma_f32 v12,v12,v11,s13\n"
AW "a77,v12\n"
AR "v12,a97\n"
"v_subrev_f32 v12,s13,v12\n"
"v_fma_f32 v12,v12,v11,s13\n"
AW "a97,v12\n"
AR "v12,a117\n"
"v_subrev_f32 v12,s13,v12\n"
"v_fma_f32 v12,v12,v11,s13\n"
AW "a117,v12\n"
AR "v12,a137\n"
"v_subrev_f32 v12,s13,v12\n"
"v_fma_f32 v12,v12,v11,s13\n"
AW "a137,v12\n"
AR "v12,a157\n"
"v_subrev_f32 v12,s13,v12\n"
"v_fma_f32 v12,v12,v11,s13\n"
AW "a157,v12\n"
AR "v12,a177\n"
"v_subrev_f32 v12,s13,v12\n"
"v_fma_f32 v12,v12,v11,s13\n"
AW "a177,v12\n"
AR "v12,a197\n"
"v_subrev_f32 v12,s13,v12\n"
"v_fma_f32 v12,v12,v11,s13\n"
AW "a197,v12\n"
AR "v12,a217\n"
"v_subrev_f32 v12,s13,v12\n"
"v_fma_f32 v12,v12,v11,s13\n"
AW "a217,v12\n"
AR "v12,a237\n"
"v_subrev_f32 v12,s13,v12\n"
"v_fma_f32 v12,v12,v11,s13\n"
AW "a237,v12\n"
AR "v12,a18\n"
"v_subrev_f32 v12,s13,v12\n"
"v_fma_f32 v12,v12,v11,s13\n"
AW "a18,v12\n"
AR "v12,a38\n"
"v_subrev_f32 v12,s13,v12\n"
"v_fma_f32 v12,v12,v11,s13\n"
AW "a38,v12\n"
AR "v12,a58\n"
"v_subrev_f32 v12,s13,v12\n"
"v_fma_f32 v12,v12,v11,s13\n"
AW "a58,v12\n"
AR "v12,a78\n"
"v_subrev_f32 v12,s13,v12\n"
"v_fma_f32 v12,v12,v11,s13\n"
AW "a78,v12\n"
AR "v12,a98\n"
"v_subrev_f32 v12,s13,v12\n"
"v_fma_f32 v12,v12,v11,s13\n"
AW "a98,v12\n"
AR "v12,a118\n"
"v_subrev_f32 v12,s13,v12\n"
"v_fma_f32 v12,v12,v11,s13\n"
AW "a118,v12\n"
AR "v12,a138\n"
"v_subrev_f32 v12,s13,v12\n"
"v_fma_f32 v12,v12,v11,s13\n"
AW "a138,v12\n"
AR "v12,a158\n"
"v_subrev_f32 v12,s13,v12\n"
"v_fma_f32 v12,v12,v11,s13\n"
AW "a158,v12\n"
AR "v12,a178\n"
"v_subrev_f32 v12,s13,v12\n"
"v_fma_f32 v12,v12,v11,s13\n"
AW "a178,v12\n"
AR "v12,a198\n"
"v_subrev_f32 v12,s13,v12\n"
"v_fma_f32 v12,v12,v11,s13\n"
AW "a198,v12\n"
AR "v12,a218\n"
"v_subrev_f32 v12,s13,v12\n"
"v_fma_f32 v12,v12,v11,s13\n"
AW "a218,v12\n"
AR "v12,a238\n"
"v_subrev_f32 v12,s13,v12\n"
"v_fma_f32 v12,v12,v11,s13\n"
AW "a238,v12\n"
AR "v12,a19\n"
"v_subrev_f32 v12,s13,v12\n"
"v_fma_f32 v12,v12,v11,s13\n"
AW "a19,v12\n"
AR "v12,a39\n"
"v_subrev_f32 v12,s13,v12\n"
"v_fma_f32 v12,v12,v11,s13\n"
AW "a39,v12\n"
AR "v12,a59\n"
"v_subrev_f32 v12,s13,v12\n"
"v_fma_f32 v12,v12,v11,s13\n"
AW "a59,v12\n"
AR "v12,a79\n"
"v_subrev_f32 v12,s13,v12\n"
"v_fma_f32 v12,v12,v11,s13\n"
AW "a79,v12\n"
AR "v12,a99\n"
"v_subrev_f32 v12,s13,v12\n"
"v_fma_f32 v12,v12,v11,s13\n"
AW "a99,v12\n"
AR "v12,a119\n"
"v_subrev_f32 v12,s13,v12\n"
"v_fma_f32 v12,v12,v11,s13\n"
AW "a119,v12\n"
AR "v12,a139\n"
"v_subrev_f32 v12,s13,v12\n"
"v_fma_f32 v12,v12,v11,s13\n"
AW "a139,v12\n"
AR "v12,a159\n"
"v_subrev_f32 v12,s13,v12\n"
"v_fma_f32 v12,v12,v11,s13\n"
AW "a159,v12\n"
AR "v12,a179\n"
"v_subrev_f32 v12,s13,v12\n"
"v_fma_f32 v12,v12,v11,s13\n"
AW "a179,v12\n"
AR "v12,a199\n"
"v_subrev_f32 v12,s13,v12\n"
"v_fma_f32 v12,v12,v11,s13\n"
AW "a199,v12\n"
AR "v12,a219\n"
"v_subrev_f32 v12,s13,v12\n"
"v_fma_f32 v12,v12,v11,s13\n"
AW "a219,v12\n"
AR "v12,a239\n"
"v_subrev_f32 v12,s13,v12\n"
"v_fma_f32 v12,v12,v11,s13\n"
AW "a239,v12\n"
"v_mul_f32 v56,v61,v11\n"
"v_sub_f32 v48,v48,v10\n"
"v_sub_f32 v49,v49,v10\n"
"v_sub_f32 v50,v50,v10\n"
"v_sub_f32 v51,v51,v10\n"
"v_sub_f32 v108,v108,v10\n"
"v_sub_f32 v109,v109,v10\n"
"v_sub_f32 v110,v110,v10\n"
"v_sub_f32 v111,v111,v10\n"
"v_sub_f32 v88,v88,v10\n"
"v_sub_f32 v89,v89,v10\n"
"v_sub_f32 v90,v90,v10\n"
"v_sub_f32 v91,v91,v10\n"
"v_sub_f32 v128,v128,v10\n"
"v_sub_f32 v129,v129,v10\n"
"v_sub_f32 v130,v130,v10\n"
"v_sub_f32 v131,v131,v10\n"
"v_exp_f32 v88,v88\n"
"v_exp_f32 v89,v89\n"
"v_exp_f32 v90,v90\n"
"v_exp_f32 v91,v91\n"
"v_exp_f32 v128,v128\n"
"v_exp_f32 v129,v129\n"
"v_exp_f32 v130,v130\n"
"v_exp_f32 v131,v131\n"
"v_add_f32 v56,v56,v88\n"
"v_add_f32 v56,v56,v89\n"
"v_add_f32 v56,v56,v90\n"
"v_add_f32 v56,v56,v91\n"
"v_add_f32 v56,v56,v128\n"
"v_add_f32 v56,v56,v129\n"
"v_add_f32 v56,v56,v130\n"
"v_add_f32_e64 v56,v56,v131\n"
CV "v88,v88,v89\n"
CV "v89,v90,v91\n"
CV "v90,v128,v129\n"
CV "v91,v130,v131\n"
"s_nop 7\n"
"s_setpc_b64 s[52:53]\n"
".Lslow_1:\n"
"s_nop 7\n"
"s_nop 7\n"
"v_and_b32 v10,240,v9\n"
"v_mul_u32_u24 v10,24,v10\n"
"v_lshrrev_b32 v11,4,v9\n"
"v_and_or_b32 v10,v11,48,v10\n"
"s_mov_b32 s59,s34\n"
"s_mov_b32 s34,s20\n"
"s_add_u32 s58,s74,0\n"
"s_nop 3\n"
LD "v[58:61],v10,s[32:35],s58 offen\n"
"s_waitcnt vmcnt(0)\n"
MF "v[92:95],v[58:61],v[136:139],v[32:35]\n"
MF "v[96:99],v[58:61],v[140:143],v[36:39]\n"
MF "v[100:103],v[58:61],v[144:147],v[40:43]\n"
MF "v[104:107],v[58:61],v[148:151],v[44:47]\n"
MF "v[108:111],v[58:61],v[152:155],v[48:51]\n"
"s_add_u32 s58,s74,0\n"
"s_nop 3\n"
LD "v[58:61],v10,s[32:35],s58 offen offset:64\n"
"s_waitcnt vmcnt(0)\n"
MF "v[92:95],v[58:61],v[156:159],v[92:95]\n"
MF "v[96:99],v[58:61],v[160:163],v[96:99]\n"
MF "v[100:103],v[58:61],v[164:167],v[100:103]\n"
MF "v[104:107],v[58:61],v[168:171],v[104:107]\n"
MF "v[108:111],v[58:61],v[172:175],v[108:111]\n"
"s_add_u32 s58,s74,0\n"
"s_nop 3\n"
LD "v[58:61],v10,s[32:35],s58 offen offset:128\n"
"s_waitcnt vmcnt(0)\n"
MF "v[92:95],v[58:61],v[176:179],v[92:95]\n"
MF "v[96:99],v[58:61],v[180:183],v[96:99]\n"
MF "v[100:103],v[58:61],v[184:187],v[100:103]\n"
MF "v[104:107],v[58:61],v[188:191],v[104:107]\n"
MF "v[108:111],v[58:61],v[192:195],v[108:111]\n"
"s_add_u32 s58,s74,0\n"
"s_nop 3\n"
LD "v[58:61],v10,s[32:35],s58 offen offset:192\n"
"s_waitcnt vmcnt(0)\n"
MF "v[92:95],v[58:61],v[196:199],v[92:95]\n"
MF "v[96:99],v[58:61],v[200:203],v[96:99]\n"
MF "v[100:103],v[58:61],v[204:207],v[100:103]\n"
MF "v[104:107],v[58:61],v[208:211],v[104:107]\n"
MF "v[108:111],v[58:61],v[212:215],v[108:111]\n"
"s_add_u32 s58,s74,0\n"
"s_nop 3\n"
LD "v[58:61],v10,s[32:35],s58 offen offset:256\n"
"s_waitcnt vmcnt(0)\n"
MF "v[92:95],v[58:61],v[216:219],v[92:95]\n"
MF "v[96:99],v[58:61],v[220:223],v[96:99]\n"
MF "v[100:103],v[58:61],v[224:227],v[100:103]\n"
MF "v[104:107],v[58:61],v[228:231],v[104:107]\n"
MF "v[108:111],v[58:61],v[232:235],v[108:111]\n"
"s_add_u32 s58,s74,0\n"
"s_nop 3\n"
LD "v[58:61],v10,s[32:35],s58 offen offset:320\n"
"s_waitcnt vmcnt(0)\n"
MF "v[92:95],v[58:61],v[236:239],v[92:95]\n"
MF "v[96:99],v[58:61],v[240:243],v[96:99]\n"
MF "v[100:103],v[58:61],v[244:247],v[100:103]\n"
MF "v[104:107],v[58:61],v[248:251],v[104:107]\n"
MF "v[108:111],v[58:61],v[252:255],v[108:111]\n"
"s_add_u32 s58,s74,6144\n"
"s_nop 3\n"
LD "v[58:61],v10,s[32:35],s58 offen\n"
"s_waitcnt vmcnt(0)\n"
MF "v[112:115],v[58:61],v[136:139],v[32:35]\n"
MF "v[116:119],v[58:61],v[140:143],v[36:39]\n"
MF "v[120:123],v[58:61],v[144:147],v[40:43]\n"
MF "v[124:127],v[58:61],v[148:151],v[44:47]\n"
MF "v[128:131],v[58:61],v[152:155],v[48:51]\n"
"s_add_u32 s58,s74,6144\n"
"s_nop 3\n"
LD "v[58:61],v10,s[32:35],s58 offen offset:64\n"
"s_waitcnt vmcnt(0)\n"
MF "v[112:115],v[58:61],v[156:159],v[112:115]\n"
MF "v[116:119],v[58:61],v[160:163],v[116:119]\n"
MF "v[120:123],v[58:61],v[164:167],v[120:123]\n"
MF "v[124:127],v[58:61],v[168:171],v[124:127]\n"
MF "v[128:131],v[58:61],v[172:175],v[128:131]\n"
"s_add_u32 s58,s74,6144\n"
"s_nop 3\n"
LD "v[58:61],v10,s[32:35],s58 offen offset:128\n"
"s_waitcnt vmcnt(0)\n"
MF "v[112:115],v[58:61],v[176:179],v[112:115]\n"
MF "v[116:119],v[58:61],v[180:183],v[116:119]\n"
MF "v[120:123],v[58:61],v[184:187],v[120:123]\n"
MF "v[124:127],v[58:61],v[188:191],v[124:127]\n"
MF "v[128:131],v[58:61],v[192:195],v[128:131]\n"
"s_add_u32 s58,s74,6144\n"
"s_nop 3\n"
LD "v[58:61],v10,s[32:35],s58 offen offset:192\n"
"s_waitcnt vmcnt(0)\n"
MF "v[112:115],v[58:61],v[196:199],v[112:115]\n"
MF "v[116:119],v[58:61],v[200:203],v[116:119]\n"
MF "v[120:123],v[58:61],v[204:207],v[120:123]\n"
MF "v[124:127],v[58:61],v[208:211],v[124:127]\n"
MF "v[128:131],v[58:61],v[212:215],v[128:131]\n"
"s_add_u32 s58,s74,6144\n"
"s_nop 3\n"
LD "v[58:61],v10,s[32:35],s58 offen offset:256\n"
"s_waitcnt vmcnt(0)\n"
MF "v[112:115],v[58:61],v[216:219],v[112:115]\n"
MF "v[116:119],v[58:61],v[220:223],v[116:119]\n"
MF "v[120:123],v[58:61],v[224:227],v[120:123]\n"
MF "v[124:127],v[58:61],v[228:231],v[124:127]\n"
MF "v[128:131],v[58:61],v[232:235],v[128:131]\n"
"s_add_u32 s58,s74,6144\n"
"s_nop 3\n"
LD "v[58:61],v10,s[32:35],s58 offen offset:320\n"
"s_waitcnt vmcnt(0)\n"
MF "v[112:115],v[58:61],v[236:239],v[112:115]\n"
MF "v[116:119],v[58:61],v[240:243],v[116:119]\n"
MF "v[120:123],v[58:61],v[244:247],v[120:123]\n"
MF "v[124:127],v[58:61],v[248:251],v[124:127]\n"
MF "v[128:131],v[58:61],v[252:255],v[128:131]\n"
"s_nop 7\n"
"s_mov_b32 s34,s59\n"
"v_max3_f32 v10,v92,v93,v94\n"
"s_nop 0\n"
"v_max3_f32 v11,v95,v112,v113\n"
"v_max3_f32 v10,v10,v114,v115\n"
"v_max_f32 v10,v10,v11\n"
"v_mov_b32 v11,v10\n"
"s_nop 1\n"
"v_permlane16_swap_b32 v10,v11\n"
"v_max_f32 v10,v10,v11\n"
"v_mov_b32 v11,v10\n"
"s_nop 1\n"
"v_permlane32_swap_b32 v10,v11\n"
"v_max_f32 v10,v10,v11\n"
"v_add_f32 v10,0x41300000,v10\n"
"v_max_f32_e64 v10,0,v10\n"
"v_exp_f32 v11,-v10\n"
AR "v12,a0\n"
"v_subrev_f32 v12,s13,v12\n"
"v_fma_f32 v12,v12,v11,s13\n"
AW "a0,v12\n"
AR "v12,a20\n"
"v_subrev_f32 v12,s13,v12\n"
"v_fma_f32 v12,v12,v11,s13\n"
AW "a20,v12\n"
AR "v12,a40\n"
"v_subrev_f32 v12,s13,v12\n"
"v_fma_f32 v12,v12,v11,s13\n"
AW "a40,v12\n"
AR "v12,a60\n"
"v_subrev_f32 v12,s13,v12\n"
"v_fma_f32 v12,v12,v11,s13\n"
AW "a60,v12\n"
AR "v12,a80\n"
"v_subrev_f32 v12,s13,v12\n"
"v_fma_f32 v12,v12,v11,s13\n"
AW "a80,v12\n"
AR "v12,a100\n"
"v_subrev_f32 v12,s13,v12\n"
"v_fma_f32 v12,v12,v11,s13\n"
AW "a100,v12\n"
AR "v12,a120\n"
"v_subrev_f32 v12,s13,v12\n"
"v_fma_f32 v12,v12,v11,s13\n"
AW "a120,v12\n"
AR "v12,a140\n"
"v_subrev_f32 v12,s13,v12\n"
"v_fma_f32 v12,v12,v11,s13\n"
AW "a140,v12\n"
AR "v12,a160\n"
"v_subrev_f32 v12,s13,v12\n"
"v_fma_f32 v12,v12,v11,s13\n"
AW "a160,v12\n"
AR "v12,a180\n"
"v_subrev_f32 v12,s13,v12\n"
"v_fma_f32 v12,v12,v11,s13\n"
AW "a180,v12\n"
AR "v12,a200\n"
"v_subrev_f32 v12,s13,v12\n"
"v_fma_f32 v12,v12,v11,s13\n"
AW "a200,v12\n"
AR "v12,a220\n"
"v_subrev_f32 v12,s13,v12\n"
"v_fma_f32 v12,v12,v11,s13\n"
AW "a220,v12\n"
AR "v12,a1\n"
"v_subrev_f32 v12,s13,v12\n"
"v_fma_f32 v12,v12,v11,s13\n"
AW "a1,v12\n"
AR "v12,a21\n"
"v_subrev_f32 v12,s13,v12\n"
"v_fma_f32 v12,v12,v11,s13\n"
AW "a21,v12\n"
AR "v12,a41\n"
"v_subrev_f32 v12,s13,v12\n"
"v_fma_f32 v12,v12,v11,s13\n"
AW "a41,v12\n"
AR "v12,a61\n"
"v_subrev_f32 v12,s13,v12\n"
"v_fma_f32 v12,v12,v11,s13\n"
AW "a61,v12\n"
AR "v12,a81\n"
"v_subrev_f32 v12,s13,v12\n"
"v_fma_f32 v12,v12,v11,s13\n"
AW "a81,v12\n"
AR "v12,a101\n"
"v_subrev_f32 v12,s13,v12\n"
"v_fma_f32 v12,v12,v11,s13\n"
AW "a101,v12\n"
AR "v12,a121\n"
"v_subrev_f32 v12,s13,v12\n"
"v_fma_f32 v12,v12,v11,s13\n"
AW "a121,v12\n"
AR "v12,a141\n"
"v_subrev_f32 v12,s13,v12\n"
"v_fma_f32 v12,v12,v11,s13\n"
AW "a141,v12\n"
AR "v12,a161\n"
"v_subrev_f32 v12,s13,v12\n"
"v_fma_f32 v12,v12,v11,s13\n"
AW "a161,v12\n"
AR "v12,a181\n"
"v_subrev_f32 v12,s13,v12\n"
"v_fma_f32 v12,v12,v11,s13\n"
AW "a181,v12\n"
AR "v12,a201\n"
"v_subrev_f32 v12,s13,v12\n"
"v_fma_f32 v12,v12,v11,s13\n"
AW "a201,v12\n"
AR "v12,a221\n"
"v_subrev_f32 v12,s13,v12\n"
"v_fma_f32 v12,v12,v11,s13\n"
AW "a221,v12\n"
AR "v12,a2\n"
"v_subrev_f32 v12,s13,v12\n"
"v_fma_f32 v12,v12,v11,s13\n"
AW "a2,v12\n"
AR "v12,a22\n"
"v_subrev_f32 v12,s13,v12\n"
"v_fma_f32 v12,v12,v11,s13\n"
AW "a22,v12\n"
AR "v12,a42\n"
"v_subrev_f32 v12,s13,v12\n"
"v_fma_f32 v12,v12,v11,s13\n"
AW "a42,v12\n"
AR "v12,a62\n"
"v_subrev_f32 v12,s13,v12\n"
"v_fma_f32 v12,v12,v11,s13\n"
AW "a62,v12\n"
AR "v12,a82\n"
"v_subrev_f32 v12,s13,v12\n"
"v_fma_f32 v12,v12,v11,s13\n"
AW "a82,v12\n"
AR "v12,a102\n"
"v_subrev_f32 v12,s13,v12\n"
"v_fma_f32 v12,v12,v11,s13\n"
AW "a102,v12\n"
AR "v12,a122\n"
"v_subrev_f32 v12,s13,v12\n"
"v_fma_f32 v12,v12,v11,s13\n"
AW "a122,v12\n"
AR "v12,a142\n"
"v_subrev_f32 v12,s13,v12\n"
"v_fma_f32 v12,v12,v11,s13\n"
AW "a142,v12\n"
AR "v12,a162\n"
"v_subrev_f32 v12,s13,v12\n"
"v_fma_f32 v12,v12,v11,s13\n"
AW "a162,v12\n"
AR "v12,a182\n"
"v_subrev_f32 v12,s13,v12\n"
"v_fma_f32 v12,v12,v11,s13\n"
AW "a182,v12\n"
AR "v12,a202\n"
"v_subrev_f32 v12,s13,v12\n"
"v_fma_f32 v12,v12,v11,s13\n"
AW "a202,v12\n"
AR "v12,a222\n"
"v_subrev_f32 v12,s13,v12\n"
"v_fma_f32 v12,v12,v11,s13\n"
AW "a222,v12\n"
AR "v12,a3\n"
"v_subrev_f32 v12,s13,v12\n"
"v_fma_f32 v12,v12,v11,s13\n"
AW "a3,v12\n"
AR "v12,a23\n"
"v_subrev_f32 v12,s13,v12\n"
"v_fma_f32 v12,v12,v11,s13\n"
AW "a23,v12\n"
AR "v12,a43\n"
"v_subrev_f32 v12,s13,v12\n"
"v_fma_f32 v12,v12,v11,s13\n"
AW "a43,v12\n"
AR "v12,a63\n"
"v_subrev_f32 v12,s13,v12\n"
"v_fma_f32 v12,v12,v11,s13\n"
AW "a63,v12\n"
AR "v12,a83\n"
"v_subrev_f32 v12,s13,v12\n"
"v_fma_f32 v12,v12,v11,s13\n"
AW "a83,v12\n"
AR "v12,a103\n"
"v_subrev_f32 v12,s13,v12\n"
"v_fma_f32 v12,v12,v11,s13\n"
AW "a103,v12\n"
AR "v12,a123\n"
"v_subrev_f32 v12,s13,v12\n"
"v_fma_f32 v12,v12,v11,s13\n"
AW "a123,v12\n"
AR "v12,a143\n"
"v_subrev_f32 v12,s13,v12\n"
"v_fma_f32 v12,v12,v11,s13\n"
AW "a143,v12\n"
AR "v12,a163\n"
"v_subrev_f32 v12,s13,v12\n"
"v_fma_f32 v12,v12,v11,s13\n"
AW "a163,v12\n"
AR "v12,a183\n"
"v_subrev_f32 v12,s13,v12\n"
"v_fma_f32 v12,v12,v11,s13\n"
AW "a183,v12\n"
AR "v12,a203\n"
"v_subrev_f32 v12,s13,v12\n"
"v_fma_f32 v12,v12,v11,s13\n"
AW "a203,v12\n"
AR "v12,a223\n"
"v_subrev_f32 v12,s13,v12\n"
"v_fma_f32 v12,v12,v11,s13\n"
AW "a223,v12\n"
"v_mul_f32 v57,v52,v11\n"
"v_sub_f32 v32,v32,v10\n"
"v_sub_f32 v33,v33,v10\n"
"v_sub_f32 v34,v34,v10\n"
"v_sub_f32 v35,v35,v10\n"
"v_sub_f32 v72,v72,v10\n"
"v_sub_f32 v73,v73,v10\n"
"v_sub_f32 v74,v74,v10\n"
"v_sub_f32 v75,v75,v10\n"
"v_sub_f32 v92,v92,v10\n"
"v_sub_f32 v93,v93,v10\n"
"v_sub_f32 v94,v94,v10\n"
"v_sub_f32 v95,v95,v10\n"
"v_sub_f32 v112,v112,v10\n"
"v_sub_f32 v113,v113,v10\n"
"v_sub_f32 v114,v114,v10\n"
"v_sub_f32 v115,v115,v10\n"
"v_exp_f32 v92,v92\n"
"v_exp_f32 v93,v93\n"
"v_exp_f32 v94,v94\n"
"v_exp_f32 v95,v95\n"
"v_exp_f32 v112,v112\n"
"v_exp_f32 v113,v113\n"
"v_exp_f32 v114,v114\n"
"v_exp_f32 v115,v115\n"
"v_add_f32 v57,v57,v92\n"
"v_add_f32 v57,v57,v93\n"
"v_add_f32 v57,v57,v94\n"
"v_add_f32 v57,v57,v95\n"
"v_add_f32 v57,v57,v112\n"
"v_add_f32 v57,v57,v113\n"
"v_add_f32 v57,v57,v114\n"
"v_add_f32_e64 v57,v57,v115\n"
CV "v92,v92,v93\n"
CV "v93,v94,v95\n"
CV "v94,v112,v113\n"
CV "v95,v114,v115\n"
"v_max3_f32 v10,v96,v97,v98\n"
"v_max3_f32 v11,v99,v116,v117\n"
"v_max3_f32 v10,v10,v118,v119\n"
"v_max_f32 v10,v10,v11\n"
"v_mov_b32 v11,v10\n"
"s_nop 1\n"
"v_permlane16_swap_b32 v10,v11\n"
"v_max_f32 v10,v10,v11\n"
"v_mov_b32 v11,v10\n"
"s_nop 1\n"
"v_permlane32_swap_b32 v10,v11\n"
"v_max_f32 v10,v10,v11\n"
"v_add_f32 v10,0x41300000,v10\n"
"v_max_f32 v10,0,v10\n"
"v_exp_f32 v11,-v10\n"
AR "v12,a4\n"
"v_subrev_f32 v12,s13,v12\n"
"v_fma_f32 v12,v12,v11,s13\n"
AW "a4,v12\n"
AR "v12,a24\n"
"v_subrev_f32 v12,s13,v12\n"
"v_fma_f32 v12,v12,v11,s13\n"
AW "a24,v12\n"
AR "v12,a44\n"
"v_subrev_f32 v12,s13,v12\n"
"v_fma_f32 v12,v12,v11,s13\n"
AW "a44,v12\n"
AR "v12,a64\n"
"v_subrev_f32 v12,s13,v12\n"
"v_fma_f32 v12,v12,v11,s13\n"
AW "a64,v12\n"
AR "v12,a84\n"
"v_subrev_f32 v12,s13,v12\n"
"v_fma_f32 v12,v12,v11,s13\n"
AW "a84,v12\n"
AR "v12,a104\n"
"v_subrev_f32 v12,s13,v12\n"
"v_fma_f32 v12,v12,v11,s13\n"
AW "a104,v12\n"
AR "v12,a124\n"
"v_subrev_f32 v12,s13,v12\n"
"v_fma_f32 v12,v12,v11,s13\n"
AW "a124,v12\n"
AR "v12,a144\n"
"v_subrev_f32 v12,s13,v12\n"
"v_fma_f32 v12,v12,v11,s13\n"
AW "a144,v12\n"
AR "v12,a164\n"
"v_subrev_f32 v12,s13,v12\n"
"v_fma_f32 v12,v12,v11,s13\n"
AW "a164,v12\n"
AR "v12,a184\n"
"v_subrev_f32 v12,s13,v12\n"
"v_fma_f32 v12,v12,v11,s13\n"
AW "a184,v12\n"
AR "v12,a204\n"
"v_subrev_f32 v12,s13,v12\n"
"v_fma_f32 v12,v12,v11,s13\n"
AW "a204,v12\n"
AR "v12,a224\n"
"v_subrev_f32 v12,s13,v12\n"
"v_fma_f32 v12,v12,v11,s13\n"
AW "a224,v12\n"
AR "v12,a5\n"
"v_subrev_f32 v12,s13,v12\n"
"v_fma_f32 v12,v12,v11,s13\n"
AW "a5,v12\n"
AR "v12,a25\n"
"v_subrev_f32 v12,s13,v12\n"
"v_fma_f32 v12,v12,v11,s13\n"
AW "a25,v12\n"
AR "v12,a45\n"
"v_subrev_f32 v12,s13,v12\n"
"v_fma_f32 v12,v12,v11,s13\n"
AW "a45,v12\n"
AR "v12,a65\n"
"v_subrev_f32 v12,s13,v12\n"
"v_fma_f32 v12,v12,v11,s13\n"
AW "a65,v12\n"
AR "v12,a85\n"
"v_subrev_f32 v12,s13,v12\n"
"v_fma_f32 v12,v12,v11,s13\n"
AW "a85,v12\n"
AR "v12,a105\n"
"v_subrev_f32 v12,s13,v12\n"
"v_fma_f32 v12,v12,v11,s13\n"
AW "a105,v12\n"
AR "v12,a125\n"
"v_subrev_f32 v12,s13,v12\n"
"v_fma_f32 v12,v12,v11,s13\n"
AW "a125,v12\n"
AR "v12,a145\n"
"v_subrev_f32 v12,s13,v12\n"
"v_fma_f32 v12,v12,v11,s13\n"
AW "a145,v12\n"
AR "v12,a165\n"
"v_subrev_f32 v12,s13,v12\n"
"v_fma_f32 v12,v12,v11,s13\n"
AW "a165,v12\n"
AR "v12,a185\n"
"v_subrev_f32 v12,s13,v12\n"
"v_fma_f32 v12,v12,v11,s13\n"
AW "a185,v12\n"
AR "v12,a205\n"
"v_subrev_f32 v12,s13,v12\n"
"v_fma_f32 v12,v12,v11,s13\n"
AW "a205,v12\n"
AR "v12,a225\n"
"v_subrev_f32 v12,s13,v12\n"
"v_fma_f32 v12,v12,v11,s13\n"
AW "a225,v12\n"
AR "v12,a6\n"
"v_subrev_f32 v12,s13,v12\n"
"v_fma_f32 v12,v12,v11,s13\n"
AW "a6,v12\n"
AR "v12,a26\n"
"v_subrev_f32 v12,s13,v12\n"
"v_fma_f32 v12,v12,v11,s13\n"
AW "a26,v12\n"
AR "v12,a46\n"
"v_subrev_f32 v12,s13,v12\n"
"v_fma_f32 v12,v12,v11,s13\n"
AW "a46,v12\n"
AR "v12,a66\n"
"v_subrev_f32 v12,s13,v12\n"
"v_fma_f32 v12,v12,v11,s13\n"
AW "a66,v12\n"
AR "v12,a86\n"
"v_subrev_f32 v12,s13,v12\n"
"v_fma_f32 v12,v12,v11,s13\n"
AW "a86,v12\n"
AR "v12,a106\n"
"v_subrev_f32 v12,s13,v12\n"
"v_fma_f32 v12,v12,v11,s13\n"
AW "a106,v12\n"
AR "v12,a126\n"
"v_subrev_f32 v12,s13,v12\n"
"v_fma_f32 v12,v12,v11,s13\n"
AW "a126,v12\n"
AR "v12,a146\n"
"v_subrev_f32 v12,s13,v12\n"
"v_fma_f32 v12,v12,v11,s13\n"
AW "a146,v12\n"
AR "v12,a166\n"
"v_subrev_f32 v12,s13,v12\n"
"v_fma_f32 v12,v12,v11,s13\n"
AW "a166,v12\n"
AR "v12,a186\n"
"v_subrev_f32 v12,s13,v12\n"
"v_fma_f32 v12,v12,v11,s13\n"
AW "a186,v12\n"
AR "v12,a206\n"
"v_subrev_f32 v12,s13,v12\n"
"v_fma_f32 v12,v12,v11,s13\n"
AW "a206,v12\n"
AR "v12,a226\n"
"v_subrev_f32 v12,s13,v12\n"
"v_fma_f32 v12,v12,v11,s13\n"
AW "a226,v12\n"
AR "v12,a7\n"
"v_subrev_f32 v12,s13,v12\n"
"v_fma_f32 v12,v12,v11,s13\n"
AW "a7,v12\n"
AR "v12,a27\n"
"v_subrev_f32 v12,s13,v12\n"
"v_fma_f32 v12,v12,v11,s13\n"
AW "a27,v12\n"
AR "v12,a47\n"
"v_subrev_f32 v12,s13,v12\n"
"v_fma_f32 v12,v12,v11,s13\n"
AW "a47,v12\n"
AR "v12,a67\n"
"v_subrev_f32 v12,s13,v12\n"
"v_fma_f32 v12,v12,v11,s13\n"
AW "a67,v12\n"
AR "v12,a87\n"
"v_subrev_f32 v12,s13,v12\n"
"v_fma_f32 v12,v12,v11,s13\n"
AW "a87,v12\n"
AR "v12,a107\n"
"v_subrev_f32 v12,s13,v12\n"
"v_fma_f32 v12,v12,v11,s13\n"
AW "a107,v12\n"
AR "v12,a127\n"
"v_subrev_f32 v12,s13,v12\n"
"v_fma_f32 v12,v12,v11,s13\n"
AW "a127,v12\n"
AR "v12,a147\n"
"v_subrev_f32 v12,s13,v12\n"
"v_fma_f32 v12,v12,v11,s13\n"
AW "a147,v12\n"
AR "v12,a167\n"
"v_subrev_f32 v12,s13,v12\n"
"v_fma_f32 v12,v12,v11,s13\n"
AW "a167,v12\n"
AR "v12,a187\n"
"v_subrev_f32 v12,s13,v12\n"
"v_fma_f32 v12,v12,v11,s13\n"
AW "a187,v12\n"
AR "v12,a207\n"
"v_subrev_f32 v12,s13,v12\n"
"v_fma_f32 v12,v12,v11,s13\n"
AW "a207,v12\n"
AR "v12,a227\n"
"v_subrev_f32 v12,s13,v12\n"
"v_fma_f32 v12,v12,v11,s13\n"
AW "a227,v12\n"
"v_mul_f32 v58,v53,v11\n"
"v_sub_f32 v36,v36,v10\n"
"v_sub_f32 v37,v37,v10\n"
"v_sub_f32 v38,v38,v10\n"
"v_sub_f32 v39,v39,v10\n"
"v_sub_f32 v76,v76,v10\n"
"v_sub_f32 v77,v77,v10\n"
"v_sub_f32 v78,v78,v10\n"
"v_sub_f32 v79,v79,v10\n"
"v_sub_f32 v96,v96,v10\n"
"v_sub_f32 v97,v97,v10\n"
"v_sub_f32 v98,v98,v10\n"
"v_sub_f32 v99,v99,v10\n"
"v_sub_f32 v116,v116,v10\n"
"v_sub_f32 v117,v117,v10\n"
"v_sub_f32 v118,v118,v10\n"
"v_sub_f32 v119,v119,v10\n"
"v_exp_f32 v96,v96\n"
"v_exp_f32 v97,v97\n"
"v_exp_f32 v98,v98\n"
"v_exp_f32 v99,v99\n"
"v_exp_f32 v116,v116\n"
"v_exp_f32 v117,v117\n"
"v_exp_f32 v118,v118\n"
"v_exp_f32 v119,v119\n"
"v_add_f32 v58,v58,v96\n"
"v_add_f32 v58,v58,v97\n"
"v_add_f32 v58,v58,v98\n"
"v_add_f32 v58,v58,v99\n"
"v_add_f32 v58,v58,v116\n"
"v_add_f32 v58,v58,v117\n"
"v_add_f32 v58,v58,v118\n"
"v_add_f32_e64 v58,v58,v119\n"
CV "v96,v96,v97\n"
CV "v97,v98,v99\n"
CV "v98,v116,v117\n"
CV "v99,v118,v119\n"
"v_max3_f32 v10,v100,v101,v102\n"
"v_max3_f32 v11,v103,v120,v121\n"
"v_max3_f32 v10,v10,v122,v123\n"
"v_max_f32 v10,v10,v11\n"
"v_mov_b32 v11,v10\n"
"s_nop 1\n"
"v_permlane16_swap_b32 v10,v11\n"
"v_max_f32 v10,v10,v11\n"
"v_mov_b32 v11,v10\n"
"s_nop 1\n"
"v_permlane32_swap_b32 v10,v11\n"
"v_max_f32_e64 v10,v10,v11\n"
"v_add_f32 v10,0x41300000,v10\n"
"v_max_f32 v10,0,v10\n"
"v_exp_f32 v11,-v10\n"
AR "v12,a8\n"
"v_subrev_f32 v12,s13,v12\n"
"v_fma_f32 v12,v12,v11,s13\n"
AW "a8,v12\n"
AR "v12,a28\n"
"v_subrev_f32 v12,s13,v12\n"
"v_fma_f32 v12,v12,v11,s13\n"
AW "a28,v12\n"
AR "v12,a48\n"
"v_subrev_f32 v12,s13,v12\n"
"v_fma_f32 v12,v12,v11,s13\n"
AW "a48,v12\n"
AR "v12,a68\n"
"v_subrev_f32 v12,s13,v12\n"
"v_fma_f32 v12,v12,v11,s13\n"
AW "a68,v12\n"
AR "v12,a88\n"
"v_subrev_f32 v12,s13,v12\n"
"v_fma_f32 v12,v12,v11,s13\n"
AW "a88,v12\n"
AR "v12,a108\n"
"v_subrev_f32 v12,s13,v12\n"
"v_fma_f32 v12,v12,v11,s13\n"
AW "a108,v12\n"
AR "v12,a128\n"
"v_subrev_f32 v12,s13,v12\n"
"v_fma_f32 v12,v12,v11,s13\n"
AW "a128,v12\n"
AR "v12,a148\n"
"v_subrev_f32 v12,s13,v12\n"
"v_fma_f32 v12,v12,v11,s13\n"
AW "a148,v12\n"
AR "v12,a168\n"
"v_subrev_f32 v12,s13,v12\n"
"v_fma_f32 v12,v12,v11,s13\n"
AW "a168,v12\n"
AR "v12,a188\n"
"v_subrev_f32 v12,s13,v12\n"
"v_fma_f32 v12,v12,v11,s13\n"
AW "a188,v12\n"
AR "v12,a208\n"
"v_subrev_f32 v12,s13,v12\n"
"v_fma_f32 v12,v12,v11,s13\n"
AW "a208,v12\n"
AR "v12,a228\n"
"v_subrev_f32 v12,s13,v12\n"
"v_fma_f32 v12,v12,v11,s13\n"
AW "a228,v12\n"
AR "v12,a9\n"
"v_subrev_f32 v12,s13,v12\n"
"v_fma_f32 v12,v12,v11,s13\n"
AW "a9,v12\n"
AR "v12,a29\n"
"v_subrev_f32 v12,s13,v12\n"
"v_fma_f32 v12,v12,v11,s13\n"
AW "a29,v12\n"
AR "v12,a49\n"
"v_subrev_f32 v12,s13,v12\n"
"v_fma_f32 v12,v12,v11,s13\n"
AW "a49,v12\n"
AR "v12,a69\n"
"v_subrev_f32 v12,s13,v12\n"
"v_fma_f32 v12,v12,v11,s13\n"
AW "a69,v12\n"
AR "v12,a89\n"
"v_subrev_f32 v12,s13,v12\n"
"v_fma_f32 v12,v12,v11,s13\n"
AW "a89,v12\n"
AR "v12,a109\n"
"v_subrev_f32 v12,s13,v12\n"
"v_fma_f32 v12,v12,v11,s13\n"
AW "a109,v12\n"
AR "v12,a129\n"
"v_subrev_f32 v12,s13,v12\n"
"v_fma_f32 v12,v12,v11,s13\n"
AW "a129,v12\n"
AR "v12,a149\n"
"v_subrev_f32 v12,s13,v12\n"
"v_fma_f32 v12,v12,v11,s13\n"
AW "a149,v12\n"
AR "v12,a169\n"
"v_subrev_f32 v12,s13,v12\n"
"v_fma_f32 v12,v12,v11,s13\n"
AW "a169,v12\n"
AR "v12,a189\n"
"v_subrev_f32 v12,s13,v12\n"
"v_fma_f32 v12,v12,v11,s13\n"
AW "a189,v12\n"
AR "v12,a209\n"
"v_subrev_f32 v12,s13,v12\n"
"v_fma_f32 v12,v12,v11,s13\n"
AW "a209,v12\n"
AR "v12,a229\n"
"v_subrev_f32 v12,s13,v12\n"
"v_fma_f32 v12,v12,v11,s13\n"
AW "a229,v12\n"
AR "v12,a10\n"
"v_subrev_f32 v12,s13,v12\n"
"v_fma_f32 v12,v12,v11,s13\n"
AW "a10,v12\n"
AR "v12,a30\n"
"v_subrev_f32 v12,s13,v12\n"
"v_fma_f32 v12,v12,v11,s13\n"
AW "a30,v12\n"
AR "v12,a50\n"
"v_subrev_f32 v12,s13,v12\n"
"v_fma_f32 v12,v12,v11,s13\n"
AW "a50,v12\n"
AR "v12,a70\n"
"v_subrev_f32 v12,s13,v12\n"
"v_fma_f32 v12,v12,v11,s13\n"
AW "a70,v12\n"
AR "v12,a90\n"
"v_subrev_f32 v12,s13,v12\n"
"v_fma_f32 v12,v12,v11,s13\n"
AW "a90,v12\n"
AR "v12,a110\n"
"v_subrev_f32 v12,s13,v12\n"
"v_fma_f32 v12,v12,v11,s13\n"
AW "a110,v12\n"
AR "v12,a130\n"
"v_subrev_f32 v12,s13,v12\n"
"v_fma_f32 v12,v12,v11,s13\n"
AW "a130,v12\n"
AR "v12,a150\n"
"v_subrev_f32 v12,s13,v12\n"
"v_fma_f32 v12,v12,v11,s13\n"
AW "a150,v12\n"
AR "v12,a170\n"
"v_subrev_f32 v12,s13,v12\n"
"v_fma_f32 v12,v12,v11,s13\n"
AW "a170,v12\n"
AR "v12,a190\n"
"v_subrev_f32 v12,s13,v12\n"
"v_fma_f32 v12,v12,v11,s13\n"
AW "a190,v12\n"
AR "v12,a210\n"
"v_subrev_f32 v12,s13,v12\n"
"v_fma_f32 v12,v12,v11,s13\n"
AW "a210,v12\n"
AR "v12,a230\n"
"v_subrev_f32 v12,s13,v12\n"
"v_fma_f32 v12,v12,v11,s13\n"
AW "a230,v12\n"
AR "v12,a11\n"
"v_subrev_f32 v12,s13,v12\n"
"v_fma_f32 v12,v12,v11,s13\n"
AW "a11,v12\n"
AR "v12,a31\n"
"v_subrev_f32 v12,s13,v12\n"
"v_fma_f32 v12,v12,v11,s13\n"
AW "a31,v12\n"
AR "v12,a51\n"
"v_subrev_f32 v12,s13,v12\n"
"v_fma_f32 v12,v12,v11,s13\n"
AW "a51,v12\n"
AR "v12,a71\n"
"v_subrev_f32 v12,s13,v12\n"
"v_fma_f32 v12,v12,v11,s13\n"
AW "a71,v12\n"
AR "v12,a91\n"
"v_subrev_f32 v12,s13,v12\n"
"v_fma_f32 v12,v12,v11,s13\n"
AW "a91,v12\n"
AR "v12,a111\n"
"v_subrev_f32 v12,s13,v12\n"
"v_fma_f32 v12,v12,v11,s13\n"
AW "a111,v12\n"
AR "v12,a131\n"
"v_subrev_f32 v12,s13,v12\n"
"v_fma_f32 v12,v12,v11,s13\n"
AW "a131,v12\n"
AR "v12,a151\n"
"v_subrev_f32 v12,s13,v12\n"
"v_fma_f32 v12,v12,v11,s13\n"
AW "a151,v12\n"
AR "v12,a171\n"
"v_subrev_f32 v12,s13,v12\n"
"v_fma_f32 v12,v12,v11,s13\n"
AW "a171,v12\n"
AR "v12,a191\n"
"v_subrev_f32 v12,s13,v12\n"
"v_fma_f32 v12,v12,v11,s13\n"
AW "a191,v12\n"
AR "v12,a211\n"
"v_subrev_f32 v12,s13,v12\n"
"v_fma_f32 v12,v12,v11,s13\n"
AW "a211,v12\n"
AR "v12,a231\n"
"v_subrev_f32 v12,s13,v12\n"
"v_fma_f32 v12,v12,v11,s13\n"
AW "a231,v12\n"
"v_mul_f32 v59,v54,v11\n"
"v_sub_f32 v40,v40,v10\n"
"v_sub_f32 v41,v41,v10\n"
"v_sub_f32 v42,v42,v10\n"
"v_sub_f32 v43,v43,v10\n"
"v_sub_f32 v80,v80,v10\n"
"v_sub_f32 v81,v81,v10\n"
"v_sub_f32 v82,v82,v10\n"
"v_sub_f32 v83,v83,v10\n"
"v_sub_f32 v100,v100,v10\n"
"v_sub_f32 v101,v101,v10\n"
"v_sub_f32 v102,v102,v10\n"
"v_sub_f32 v103,v103,v10\n"
"v_sub_f32 v120,v120,v10\n"
"v_sub_f32 v121,v121,v10\n"
"v_sub_f32 v122,v122,v10\n"
"v_sub_f32 v123,v123,v10\n"
"v_exp_f32 v100,v100\n"
"v_exp_f32 v101,v101\n"
"v_exp_f32 v102,v102\n"
"v_exp_f32 v103,v103\n"
"v_exp_f32 v120,v120\n"
"v_exp_f32 v121,v121\n"
"v_exp_f32 v122,v122\n"
"v_exp_f32 v123,v123\n"
"v_add_f32 v59,v59,v100\n"
"v_add_f32 v59,v59,v101\n"
"v_add_f32 v59,v59,v102\n"
"v_add_f32 v59,v59,v103\n"
"v_add_f32 v59,v59,v120\n"
"v_add_f32 v59,v59,v121\n"
"v_add_f32 v59,v59,v122\n"
"v_add_f32 v59,v59,v123\n"
CV "v100,v100,v101\n"
CV "v101,v102,v103\n"
CV "v102,v120,v121\n"
CV "v103,v122,v123\n"
"v_max3_f32 v10,v104,v105,v106\n"
"v_max3_f32 v11,v107,v124,v125\n"
"v_max3_f32 v10,v10,v126,v127\n"
"v_max_f32 v10,v10,v11\n"
"v_mov_b32 v11,v10\n"
"s_nop 1\n"
"v_permlane16_swap_b32 v10,v11\n"
"v_max_f32 v10,v10,v11\n"
"v_mov_b32 v11,v10\n"
"s_nop 1\n"
"v_permlane32_swap_b32 v10,v11\n"
"v_max_f32 v10,v10,v11\n"
"v_add_f32 v10,0x41300000,v10\n"
"v_max_f32 v10,0,v10\n"
"v_exp_f32 v11,-v10\n"
AR "v12,a12\n"
"v_subrev_f32 v12,s13,v12\n"
"v_fma_f32 v12,v12,v11,s13\n"
AW "a12,v12\n"
AR "v12,a32\n"
"v_subrev_f32 v12,s13,v12\n"
"v_fma_f32 v12,v12,v11,s13\n"
AW "a32,v12\n"
AR "v12,a52\n"
"v_subrev_f32 v12,s13,v12\n"
"v_fma_f32 v12,v12,v11,s13\n"
AW "a52,v12\n"
AR "v12,a72\n"
"v_subrev_f32 v12,s13,v12\n"
"v_fma_f32 v12,v12,v11,s13\n"
AW "a72,v12\n"
AR "v12,a92\n"
"v_subrev_f32 v12,s13,v12\n"
"v_fma_f32 v12,v12,v11,s13\n"
AW "a92,v12\n"
AR "v12,a112\n"
"v_subrev_f32 v12,s13,v12\n"
"v_fma_f32 v12,v12,v11,s13\n"
AW "a112,v12\n"
AR "v12,a132\n"
"v_subrev_f32 v12,s13,v12\n"
"v_fma_f32 v12,v12,v11,s13\n"
AW "a132,v12\n"
AR "v12,a152\n"
"v_subrev_f32 v12,s13,v12\n"
"v_fma_f32 v12,v12,v11,s13\n"
AW "a152,v12\n"
AR "v12,a172\n"
"v_subrev_f32 v12,s13,v12\n"
"v_fma_f32 v12,v12,v11,s13\n"
AW "a172,v12\n"
AR "v12,a192\n"
"v_subrev_f32 v12,s13,v12\n"
"v_fma_f32 v12,v12,v11,s13\n"
AW "a192,v12\n"
AR "v12,a212\n"
"v_subrev_f32 v12,s13,v12\n"
"v_fma_f32 v12,v12,v11,s13\n"
AW "a212,v12\n"
AR "v12,a232\n"
"v_subrev_f32 v12,s13,v12\n"
"v_fma_f32 v12,v12,v11,s13\n"
AW "a232,v12\n"
AR "v12,a13\n"
"v_subrev_f32 v12,s13,v12\n"
"v_fma_f32 v12,v12,v11,s13\n"
AW "a13,v12\n"
AR "v12,a33\n"
"v_subrev_f32 v12,s13,v12\n"
"v_fma_f32 v12,v12,v11,s13\n"
AW "a33,v12\n"
AR "v12,a53\n"
"v_subrev_f32 v12,s13,v12\n"
"v_fma_f32 v12,v12,v11,s13\n"
AW "a53,v12\n"
AR "v12,a73\n"
"v_subrev_f32 v12,s13,v12\n"
"v_fma_f32 v12,v12,v11,s13\n"
AW "a73,v12\n"
AR "v12,a93\n"
"v_subrev_f32 v12,s13,v12\n"
"v_fma_f32 v12,v12,v11,s13\n"
AW "a93,v12\n"
AR "v12,a113\n"
"v_subrev_f32 v12,s13,v12\n"
"v_fma_f32 v12,v12,v11,s13\n"
AW "a113,v12\n"
AR "v12,a133\n"
"v_subrev_f32 v12,s13,v12\n"
"v_fma_f32 v12,v12,v11,s13\n"
AW "a133,v12\n"
AR "v12,a153\n"
"v_subrev_f32 v12,s13,v12\n"
"v_fma_f32 v12,v12,v11,s13\n"
AW "a153,v12\n"
AR "v12,a173\n"
"v_subrev_f32 v12,s13,v12\n"
"v_fma_f32 v12,v12,v11,s13\n"
AW "a173,v12\n"
AR "v12,a193\n"
"v_subrev_f32 v12,s13,v12\n"
"v_fma_f32 v12,v12,v11,s13\n"
AW "a193,v12\n"
AR "v12,a213\n"
"v_subrev_f32 v12,s13,v12\n"
"v_fma_f32 v12,v12,v11,s13\n"
AW "a213,v12\n"
AR "v12,a233\n"
"v_subrev_f32 v12,s13,v12\n"
"v_fma_f32 v12,v12,v11,s13\n"
AW "a233,v12\n"
AR "v12,a14\n"
"v_subrev_f32 v12,s13,v12\n"
"v_fma_f32 v12,v12,v11,s13\n"
AW "a14,v12\n"
AR "v12,a34\n"
"v_subrev_f32 v12,s13,v12\n"
"v_fma_f32 v12,v12,v11,s13\n"
AW "a34,v12\n"
AR "v12,a54\n"
"v_subrev_f32 v12,s13,v12\n"
"v_fma_f32 v12,v12,v11,s13\n"
AW "a54,v12\n"
AR "v12,a74\n"
"v_subrev_f32 v12,s13,v12\n"
"v_fma_f32 v12,v12,v11,s13\n"
AW "a74,v12\n"
AR "v12,a94\n"
"v_subrev_f32 v12,s13,v12\n"
"v_fma_f32 v12,v12,v11,s13\n"
AW "a94,v12\n"
AR "v12,a114\n"
"v_subrev_f32 v12,s13,v12\n"
"v_fma_f32 v12,v12,v11,s13\n"
AW "a114,v12\n"
AR "v12,a134\n"
"v_subrev_f32 v12,s13,v12\n"
"v_fma_f32 v12,v12,v11,s13\n"
AW "a134,v12\n"
AR "v12,a154\n"
"v_subrev_f32 v12,s13,v12\n"
"v_fma_f32 v12,v12,v11,s13\n"
AW "a154,v12\n"
AR "v12,a174\n"
"v_subrev_f32 v12,s13,v12\n"
"v_fma_f32 v12,v12,v11,s13\n"
AW "a174,v12\n"
AR "v12,a194\n"
"v_subrev_f32 v12,s13,v12\n"
"v_fma_f32 v12,v12,v11,s13\n"
AW "a194,v12\n"
AR "v12,a214\n"
"v_subrev_f32 v12,s13,v12\n"
"v_fma_f32 v12,v12,v11,s13\n"
AW "a214,v12\n"
AR "v12,a234\n"
"v_subrev_f32 v12,s13,v12\n"
"v_fma_f32 v12,v12,v11,s13\n"
AW "a234,v12\n"
AR "v12,a15\n"
"v_subrev_f32 v12,s13,v12\n"
"v_fma_f32 v12,v12,v11,s13\n"
AW "a15,v12\n"
AR "v12,a35\n"
"v_subrev_f32 v12,s13,v12\n"
"v_fma_f32 v12,v12,v11,s13\n"
AW "a35,v12\n"
AR "v12,a55\n"
"v_subrev_f32 v12,s13,v12\n"
"v_fma_f32 v12,v12,v11,s13\n"
AW "a55,v12\n"
AR "v12,a75\n"
"v_subrev_f32 v12,s13,v12\n"
"v_fma_f32 v12,v12,v11,s13\n"
AW "a75,v12\n"
AR "v12,a95\n"
"v_subrev_f32 v12,s13,v12\n"
"v_fma_f32 v12,v12,v11,s13\n"
AW "a95,v12\n"
AR "v12,a115\n"
"v_subrev_f32 v12,s13,v12\n"
"v_fma_f32 v12,v12,v11,s13\n"
AW "a115,v12\n"
AR "v12,a135\n"
"v_subrev_f32 v12,s13,v12\n"
"v_fma_f32 v12,v12,v11,s13\n"
AW "a135,v12\n"
AR "v12,a155\n"
"v_subrev_f32 v12,s13,v12\n"
"v_fma_f32 v12,v12,v11,s13\n"
AW "a155,v12\n"
AR "v12,a175\n"
"v_subrev_f32 v12,s13,v12\n"
"v_fma_f32 v12,v12,v11,s13\n"
AW "a175,v12\n"
AR "v12,a195\n"
"v_subrev_f32 v12,s13,v12\n"
"v_fma_f32 v12,v12,v11,s13\n"
AW "a195,v12\n"
AR "v12,a215\n"
"v_subrev_f32 v12,s13,v12\n"
"v_fma_f32 v12,v12,v11,s13\n"
AW "a215,v12\n"
AR "v12,a235\n"
"v_subrev_f32 v12,s13,v12\n"
"v_fma_f32 v12,v12,v11,s13\n"
AW "a235,v12\n"
"v_mul_f32 v60,v55,v11\n"
"v_sub_f32 v44,v44,v10\n"
"v_sub_f32 v45,v45,v10\n"
"v_sub_f32 v46,v46,v10\n"
"v_sub_f32 v47,v47,v10\n"
"v_sub_f32 v84,v84,v10\n"
"v_sub_f32 v85,v85,v10\n"
"v_sub_f32 v86,v86,v10\n"
"v_sub_f32 v87,v87,v10\n"
"v_sub_f32 v104,v104,v10\n"
"v_sub_f32 v105,v105,v10\n"
"v_sub_f32 v106,v106,v10\n"
"v_sub_f32 v107,v107,v10\n"
"v_sub_f32 v124,v124,v10\n"
"v_sub_f32 v125,v125,v10\n"
"v_sub_f32 v126,v126,v10\n"
"v_sub_f32 v127,v127,v10\n"
"v_exp_f32 v104,v104\n"
"v_exp_f32 v105,v105\n"
"v_exp_f32 v106,v106\n"
"v_exp_f32 v107,v107\n"
"v_exp_f32 v124,v124\n"
"v_exp_f32 v125,v125\n"
"v_exp_f32 v126,v126\n"
"v_exp_f32 v127,v127\n"
"v_add_f32 v60,v60,v104\n"
"v_add_f32 v60,v60,v105\n"
"v_add_f32 v60,v60,v106\n"
"v_add_f32 v60,v60,v107\n"
"v_add_f32 v60,v60,v124\n"
"v_add_f32 v60,v60,v125\n"
"v_add_f32 v60,v60,v126\n"
"v_add_f32_e64 v60,v60,v127\n"
CV "v104,v104,v105\n"
CV "v105,v106,v107\n"
CV "v106,v124,v125\n"
CV "v107,v126,v127\n"
"v_max3_f32 v10,v108,v109,v110\n"
"v_max3_f32 v11,v111,v128,v129\n"
"v_max3_f32 v10,v10,v130,v131\n"
"v_max_f32 v10,v10,v11\n"
"v_mov_b32 v11,v10\n"
"s_nop 1\n"
"v_permlane16_swap_b32 v10,v11\n"
"v_max_f32 v10,v10,v11\n"
"v_mov_b32 v11,v10\n"
"s_nop 1\n"
"v_permlane32_swap_b32 v10,v11\n"
"v_max_f32 v10,v10,v11\n"
"v_add_f32 v10,0x41300000,v10\n"
"v_max_f32 v10,0,v10\n"
"v_exp_f32 v11,-v10\n"
AR "v12,a16\n"
"v_subrev_f32 v12,s13,v12\n"
"v_fma_f32 v12,v12,v11,s13\n"
AW "a16,v12\n"
AR "v12,a36\n"
"v_subrev_f32 v12,s13,v12\n"
"v_fma_f32 v12,v12,v11,s13\n"
AW "a36,v12\n"
AR "v12,a56\n"
"v_subrev_f32 v12,s13,v12\n"
"v_fma_f32 v12,v12,v11,s13\n"
AW "a56,v12\n"
AR "v12,a76\n"
"v_subrev_f32 v12,s13,v12\n"
"v_fma_f32 v12,v12,v11,s13\n"
AW "a76,v12\n"
AR "v12,a96\n"
"v_subrev_f32 v12,s13,v12\n"
"v_fma_f32 v12,v12,v11,s13\n"
AW "a96,v12\n"
AR "v12,a116\n"
"v_subrev_f32 v12,s13,v12\n"
"v_fma_f32 v12,v12,v11,s13\n"
AW "a116,v12\n"
AR "v12,a136\n"
"v_subrev_f32 v12,s13,v12\n"
"v_fma_f32 v12,v12,v11,s13\n"
AW "a136,v12\n"
AR "v12,a156\n"
"v_subrev_f32 v12,s13,v12\n"
"v_fma_f32 v12,v12,v11,s13\n"
AW "a156,v12\n"
AR "v12,a176\n"
"v_subrev_f32 v12,s13,v12\n"
"v_fma_f32 v12,v12,v11,s13\n"
AW "a176,v12\n"
AR "v12,a196\n"
"v_subrev_f32 v12,s13,v12\n"
"v_fma_f32 v12,v12,v11,s13\n"
AW "a196,v12\n"
AR "v12,a216\n"
"v_subrev_f32 v12,s13,v12\n"
"v_fma_f32 v12,v12,v11,s13\n"
AW "a216,v12\n"
AR "v12,a236\n"
"v_subrev_f32 v12,s13,v12\n"
"v_fma_f32 v12,v12,v11,s13\n"
AW "a236,v12\n"
AR "v12,a17\n"
"v_subrev_f32 v12,s13,v12\n"
"v_fma_f32 v12,v12,v11,s13\n"
AW "a17,v12\n"
AR "v12,a37\n"
"v_subrev_f32 v12,s13,v12\n"
"v_fma_f32 v12,v12,v11,s13\n"
AW "a37,v12\n"
AR "v12,a57\n"
"v_subrev_f32 v12,s13,v12\n"
"v_fma_f32 v12,v12,v11,s13\n"
AW "a57,v12\n"
AR "v12,a77\n"
"v_subrev_f32 v12,s13,v12\n"
"v_fma_f32 v12,v12,v11,s13\n"
AW "a77,v12\n"
AR "v12,a97\n"
"v_subrev_f32 v12,s13,v12\n"
"v_fma_f32 v12,v12,v11,s13\n"
AW "a97,v12\n"
AR "v12,a117\n"
"v_subrev_f32 v12,s13,v12\n"
"v_fma_f32 v12,v12,v11,s13\n"
AW "a117,v12\n"
AR "v12,a137\n"
"v_subrev_f32 v12,s13,v12\n"
"v_fma_f32 v12,v12,v11,s13\n"
AW "a137,v12\n"
AR "v12,a157\n"
"v_subrev_f32 v12,s13,v12\n"
"v_fma_f32 v12,v12,v11,s13\n"
AW "a157,v12\n"
AR "v12,a177\n"
"v_subrev_f32 v12,s13,v12\n"
"v_fma_f32 v12,v12,v11,s13\n"
AW "a177,v12\n"
AR "v12,a197\n"
"v_subrev_f32 v12,s13,v12\n"
"v_fma_f32 v12,v12,v11,s13\n"
AW "a197,v12\n"
AR "v12,a217\n"
"v_subrev_f32 v12,s13,v12\n"
"v_fma_f32 v12,v12,v11,s13\n"
AW "a217,v12\n"
AR "v12,a237\n"
"v_subrev_f32 v12,s13,v12\n"
"v_fma_f32 v12,v12,v11,s13\n"
AW "a237,v12\n"
AR "v12,a18\n"
"v_subrev_f32 v12,s13,v12\n"
"v_fma_f32 v12,v12,v11,s13\n"
AW "a18,v12\n"
AR "v12,a38\n"
"v_subrev_f32 v12,s13,v12\n"
"v_fma_f32 v12,v12,v11,s13\n"
AW "a38,v12\n"
AR "v12,a58\n"
"v_subrev_f32 v12,s13,v12\n"
"v_fma_f32 v12,v12,v11,s13\n"
AW "a58,v12\n"
AR "v12,a78\n"
"v_subrev_f32 v12,s13,v12\n"
"v_fma_f32 v12,v12,v11,s13\n"
AW "a78,v12\n"
AR "v12,a98\n"
"v_subrev_f32 v12,s13,v12\n"
"v_fma_f32 v12,v12,v11,s13\n"
AW "a98,v12\n"
AR "v12,a118\n"
"v_subrev_f32 v12,s13,v12\n"
"v_fma_f32 v12,v12,v11,s13\n"
AW "a118,v12\n"
AR "v12,a138\n"
"v_subrev_f32 v12,s13,v12\n"
"v_fma_f32 v12,v12,v11,s13\n"
AW "a138,v12\n"
AR "v12,a158\n"
"v_subrev_f32 v12,s13,v12\n"
"v_fma_f32 v12,v12,v11,s13\n"
AW "a158,v12\n"
AR "v12,a178\n"
"v_subrev_f32 v12,s13,v12\n"
"v_fma_f32 v12,v12,v11,s13\n"
AW "a178,v12\n"
AR "v12,a198\n"
"v_subrev_f32 v12,s13,v12\n"
"v_fma_f32 v12,v12,v11,s13\n"
AW "a198,v12\n"
AR "v12,a218\n"
"v_subrev_f32 v12,s13,v12\n"
"v_fma_f32 v12,v12,v11,s13\n"
AW "a218,v12\n"
AR "v12,a238\n"
"v_subrev_f32 v12,s13,v12\n"
"v_fma_f32 v12,v12,v11,s13\n"
AW "a238,v12\n"
AR "v12,a19\n"
"v_subrev_f32 v12,s13,v12\n"
"v_fma_f32 v12,v12,v11,s13\n"
AW "a19,v12\n"
AR "v12,a39\n"
"v_subrev_f32 v12,s13,v12\n"
"v_fma_f32 v12,v12,v11,s13\n"
AW "a39,v12\n"
AR "v12,a59\n"
"v_subrev_f32 v12,s13,v12\n"
"v_fma_f32 v12,v12,v11,s13\n"
AW "a59,v12\n"
AR "v12,a79\n"
"v_subrev_f32 v12,s13,v12\n"
"v_fma_f32 v12,v12,v11,s13\n"
AW "a79,v12\n"
AR "v12,a99\n"
"v_subrev_f32 v12,s13,v12\n"
"v_fma_f32 v12,v12,v11,s13\n"
AW "a99,v12\n"
AR "v12,a119\n"
"v_subrev_f32 v12,s13,v12\n"
"v_fma_f32 v12,v12,v11,s13\n"
AW "a119,v12\n"
AR "v12,a139\n"
"v_subrev_f32 v12,s13,v12\n"
"v_fma_f32 v12,v12,v11,s13\n"
AW "a139,v12\n"
AR "v12,a159\n"
"v_subrev_f32 v12,s13,v12\n"
"v_fma_f32 v12,v12,v11,s13\n"
AW "a159,v12\n"
AR "v12,a179\n"
"v_subrev_f32 v12,s13,v12\n"
"v_fma_f32 v12,v12,v11,s13\n"
AW "a179,v12\n"
AR "v12,a199\n"
"v_subrev_f32 v12,s13,v12\n"
"v_fma_f32 v12,v12,v11,s13\n"
AW "a199,v12\n"
AR "v12,a219\n"
"v_subrev_f32 v12,s13,v12\n"
"v_fma_f32 v12,v12,v11,s13\n"
AW "a219,v12\n"
AR "v12,a239\n"
"v_subrev_f32 v12,s13,v12\n"
"v_fma_f32 v12,v12,v11,s13\n"
AW "a239,v12\n"
"v_mul_f32 v61,v56,v11\n"
"v_sub_f32 v48,v48,v10\n"
"v_sub_f32 v49,v49,v10\n"
"v_sub_f32 v50,v50,v10\n"
"v_sub_f32 v51,v51,v10\n"
"v_sub_f32 v88,v88,v10\n"
"v_sub_f32 v89,v89,v10\n"
"v_sub_f32 v90,v90,v10\n"
"v_sub_f32 v91,v91,v10\n"
"v_sub_f32 v108,v108,v10\n"
"v_sub_f32 v109,v109,v10\n"
"v_sub_f32 v110,v110,v10\n"
"v_sub_f32 v111,v111,v10\n"
"v_sub_f32 v128,v128,v10\n"
"v_sub_f32 v129,v129,v10\n"
"v_sub_f32 v130,v130,v10\n"
"v_sub_f32 v131,v131,v10\n"
"v_exp_f32 v108,v108\n"
"v_exp_f32 v109,v109\n"
"v_exp_f32 v110,v110\n"
"v_exp_f32 v111,v111\n"
"v_exp_f32 v128,v128\n"
"v_exp_f32 v129,v129\n"
"v_exp_f32 v130,v130\n"
"v_exp_f32 v131,v131\n"
"v_add_f32 v61,v61,v108\n"
"v_add_f32 v61,v61,v109\n"
"v_add_f32 v61,v61,v110\n"
"v_add_f32 v61,v61,v111\n"
"v_add_f32 v61,v61,v128\n"
"v_add_f32 v61,v61,v129\n"
"v_add_f32 v61,v61,v130\n"
"v_add_f32_e64 v61,v61,v131\n"
CV "v108,v108,v109\n"
CV "v109,v110,v111\n"
CV "v110,v128,v129\n"
CV "v111,v130,v131\n"
"s_nop 7\n"
"s_setpc_b64 s[52:53]\n"
".Lackspin_q:\n"
"s_cmp_eq_u32 s98,0\n"
"s_cbranch_scc1 .Lackok_q\n"
"s_sub_u32 s98,s98,1\n"
"s_sleep 8\n"
"v_bfe_u32 v14,v9,4,5\n"
"v_lshlrev_b32 v14,6,v14\n"
"s_lshl_b32 s58,s78,2\n"
"s_add_u32 s58,s58,s86\n"
"buffer_load_dword v10,v14,s[40:43],s58 offen offset:128 sc1\n"
"s_waitcnt vmcnt(0)\n"
"s_branch .Lack_q\n"
".Lcode1:\n"
".Ldone:\n"
    : "+{v0}"(tid), "+{s23}"(bid), "+{s[20:21]}"(ka)
    :
    : "v1", "v2", "v3", "v4", "v5", "v6", "v7", "v8", "v9", "v10", "v11", "v12", "v13", "v14", "v15", "v16", "v17", "v18", "v19", "v20", "v21", "v22", "v23", "v24", "v25", "v26", "v27", "v28", "v29", "v30", "v31", "v32", "v33", "v34", "v35", "v36", "v37", "v38", "v39", "v40", "v41", "v42", "v43", "v44", "v45", "v46", "v47", "v48", "v49", "v50", "v51", "v52", "v53", "v54", "v55", "v56", "v57", "v58", "v59", "v60", "v61", "v62", "v63", "v64", "v65", "v66", "v67", "v68", "v69", "v70", "v71", "v72", "v73", "v74", "v75", "v76", "v77", "v78", "v79", "v80", "v81", "v82", "v83", "v84", "v85", "v86", "v87", "v88", "v89", "v90", "v91", "v92", "v93", "v94", "v95", "v96", "v97", "v98", "v99", "v100", "v101", "v102", "v103", "v104", "v105", "v106", "v107", "v108", "v109", "v110", "v111", "v112", "v113", "v114", "v115", "v116", "v117", "v118", "v119", "v120", "v121", "v122", "v123", "v124", "v125", "v126", "v127", "v128", "v129", "v130", "v131", "v132", "v133", "v134", "v135", "v136", "v137", "v138", "v139", "v140", "v141", "v142", "v143", "v144", "v145", "v146", "v147", "v148", "v149", "v150", "v151", "v152", "v153", "v154", "v155", "v156", "v157", "v158", "v159", "v160", "v161", "v162", "v163", "v164", "v165", "v166", "v167", "v168", "v169", "v170", "v171", "v172", "v173", "v174", "v175", "v176", "v177", "v178", "v179", "v180", "v181", "v182", "v183", "v184", "v185", "v186", "v187", "v188", "v189", "v190", "v191", "v192", "v193", "v194", "v195", "v196", "v197", "v198", "v199", "v200", "v201", "v202", "v203", "v204", "v205", "v206", "v207", "v208", "v209", "v210", "v211", "v212", "v213", "v214", "v215", "v216", "v217", "v218", "v219", "v220", "v221", "v222", "v223", "v224", "v225", "v226", "v227", "v228", "v229", "v230", "v231", "v232", "v233", "v234", "v235", "v236", "v237", "v238", "v239", "v240", "v241", "v242", "v243", "v244", "v245", "v246", "v247", "v248", "v249", "v250", "v251", "v252", "v253", "v254", "v255", "a0", "a1", "a2", "a3", "a4", "a5", "a6", "a7", "a8", "a9", "a10", "a11", "a12", "a13", "a14", "a15", "a16", "a17", "a18", "a19", "a20", "a21", "a22", "a23", "a24", "a25", "a26", "a27", "a28", "a29", "a30", "a31", "a32", "a33", "a34", "a35", "a36", "a37", "a38", "a39", "a40", "a41", "a42", "a43", "a44", "a45", "a46", "a47", "a48", "a49", "a50", "a51", "a52", "a53", "a54", "a55", "a56", "a57", "a58", "a59", "a60", "a61", "a62", "a63", "a64", "a65", "a66", "a67", "a68", "a69", "a70", "a71", "a72", "a73", "a74", "a75", "a76", "a77", "a78", "a79", "a80", "a81", "a82", "a83", "a84", "a85", "a86", "a87", "a88", "a89", "a90", "a91", "a92", "a93", "a94", "a95", "a96", "a97", "a98", "a99", "a100", "a101", "a102", "a103", "a104", "a105", "a106", "a107", "a108", "a109", "a110", "a111", "a112", "a113", "a114", "a115", "a116", "a117", "a118", "a119", "a120", "a121", "a122", "a123", "a124", "a125", "a126", "a127", "a128", "a129", "a130", "a131", "a132", "a133", "a134", "a135", "a136", "a137", "a138", "a139", "a140", "a141", "a142", "a143", "a144", "a145", "a146", "a147", "a148", "a149", "a150", "a151", "a152", "a153", "a154", "a155", "a156", "a157", "a158", "a159", "a160", "a161", "a162", "a163", "a164", "a165", "a166", "a167", "a168", "a169", "a170", "a171", "a172", "a173", "a174", "a175", "a176", "a177", "a178", "a179", "a180", "a181", "a182", "a183", "a184", "a185", "a186", "a187", "a188", "a189", "a190", "a191", "a192", "a193", "a194", "a195", "a196", "a197", "a198", "a199", "a200", "a201", "a202", "a203", "a204", "a205", "a206", "a207", "a208", "a209", "a210", "a211", "a212", "a213", "a214", "a215", "a216", "a217", "a218", "a219", "a220", "a221", "a222", "a223", "a224", "a225", "a226", "a227", "a228", "a229", "a230", "a231", "a232", "a233", "a234", "a235", "a236", "a237", "a238", "a239", "a240", "a241", "a242", "a243", "a244", "a245", "a246", "a247", "a248", "a249", "a250", "a251", "a252", "a253", "a254", "a255", "s0", "s1", "s2", "s3", "s4", "s5", "s6", "s7", "s8", "s9", "s10", "s11", "s12", "s13", "s14", "s15", "s16", "s17", "s18", "s19", "s22", "s24", "s25", "s26", "s27", "s28", "s29", "s30", "s31", "s32", "s33", "s34", "s35", "s36", "s37", "s38", "s39", "s40", "s41", "s42", "s43", "s44", "s45", "s46", "s47", "s48", "s49", "s50", "s51", "s52", "s53", "s54", "s55", "s56", "s57", "s58", "s59", "s60", "s61", "s62", "s63", "s64", "s65", "s66", "s67", "s68", "s69", "s70", "s71", "s72", "s73", "s74", "s75", "s76", "s77", "s78", "s79", "s80", "s81", "s82", "s83", "s84", "s85", "s86", "s87", "s88", "s89", "s90", "s91", "s92", "s93", "s94", "s95", "s96", "s97", "s98", "s99", "s100", "s101", "vcc", "scc", "memory");
}

extern "C" void kernel_launch(void* const* d_in, const int* in_sizes, int n_in, void* d_out, int out_size, void* d_ws, size_t ws_size, hipStream_t stream) {
  Ptrs P;
  for (int br = 0; br < 2; ++br) { P.x[br] = d_in[5 * br]; P.k[br] = d_in[5 * br + 1]; P.v[br] = d_in[5 * br + 2]; P.wq[br] = d_in[5 * br + 3]; P.wo[br] = d_in[5 * br + 4]; }
  (void)hipFuncSetAttribute((const void*)fused_kernel, hipFuncAttributeMaxDynamicSharedMemorySize, LDS1);
  fused_kernel<<<256, 256, LDS1, stream>>>(P, (char*)d_ws, (char*)d_out);
}
```

```cpp
#include <hip/hip_runtime.h>
#include <hip/hip_bf16.h>

#define MF "v_mfma_f32_16x16x32_bf16 "
#define TR "ds_read_b64_tr_b16 "
#define AR "v_accvgpr_read_b32 "
#define AW "v_accvgpr_write_b32 "
#define CV "v_cvt_pk_bf16_f32 "
#define LD "buffer_load_dwordx4 "
constexpr unsigned LDS1 = 160 * 1024;

struct Ptrs { const void* x[2]; const void* k[2]; const void* v[2]; const void* wq[2]; const void* wo[2]; };

__global__ __launch_bounds__(256, 1) __attribute__((aligned(4096))) void fused_kernel(Ptrs P, char* __restrict__ ws, char* __restrict__ out) {
  unsigned tid = threadIdx.x, bid = blockIdx.x;
  unsigned long long ka = (unsigned long long)__builtin_amdgcn_kernarg_segment_ptr();
  asm volatile(
".p2align 5\n"
".Lcode0:\n"
"s_and_b32 s58,s23,4\n"
"s_lshl_b32 s58,s58,1\n"
"s_load_dwordx2 s[28:29],s[20:21],s58 offset:48\n"
"s_load_dwordx2 s[24:25],s[20:21],s58\n"
"s_load_dwordx2 s[32:33],s[20:21],s58 offset:16\n"
"s_load_dwordx2 s[36:37],s[20:21],s58 offset:32\n"
"s_load_dwordx2 s[48:49],s[20:21],s58 offset:64\n"
"s_memtime s[100:101]\n"
"s_load_dwordx2 s[40:41],s[20:21],0x50\n"
"s_load_dwordx2 s[44:45],s[20:21],0x58\n"
"v_readfirstlane_b32 s22,v0\n"
"v_and_b32 v9,63,v0\n"
"v_bfe_u32 v10,v0,3,3\n"
"v_and_b32 v11,7,v0\n"
"s_lshr_b32 s22,s22,6\n"
"s_or_b32 s22,s22,s58\n"
"s_and_b32 s58,s23,1\n"
"s_lshl_b32 s58,s58,3\n"
"s_lshr_b32 s59,s23,5\n"
"s_add_u32 s59,s59,s58\n"
"s_mul_i32 s59,s59,4112\n"
"s_or_b32 s22,s22,s59\n"
"s_and_b32 s59,s23,2\n"
"s_lshl_b32 s59,s59,1\n"
"s_bfe_u32 s60,s23,0x20003\n"
"s_or_b32 s59,s59,s60\n"
"s_lshl_b32 s60,s59,20\n"
"s_or_b32 s22,s22,s60\n"
"s_or_b32 s59,s59,s58\n"
"s_mul_i32 s59,s59,65792\n"
"s_xor_b32 s59,s59,524288\n"
"s_or_b32 s22,s22,s59\n"
"v_lshrrev_b32 v6,2,v0\n"
"v_and_b32 v6,4,v6\n"
"v_xor_b32 v6,v6,v11\n"
"v_lshlrev_b32 v6,4,v6\n"
"s_mov_b32 s58,12288\n"
"v_mad_u32_u24 v6,v10,s58,v6\n"
"v_bfe_u32 v4,v0,4,3\n"
"v_xor_b32 v4,v4,v11\n"
"v_lshlrev_b32 v4,4,v4\n"
"s_mov_b32 s58,1280\n"
"v_mad_u32_u24 v4,v10,s58,v4\n"
"v_lshlrev_b32 v9,4,v9\n"
"s_mov_b32 s26,3276800\n"
"s_mov_b32 s27,0x00020000\n"
"s_mov_b32 s30,7864320\n"
"s_mov_b32 s31,0x00020000\n"
"s_mov_b32 s34,0\n"
"s_mov_b32 s35,0x00020000\n"
"s_mov_b32 s38,0\n"
"s_mov_b32 s39,0x00020000\n"
"s_mov_b32 s42,268435456\n"
"s_mov_b32 s43,0x00020000\n"
"s_mov_b32 s46,6553600\n"
"s_mov_b32 s47,0x00020000\n"
"s_mov_b32 s50,7864320\n"
"s_mov_b32 s51,0x00020000\n"
"s_mov_b32 s98,20000\n"
"s_bfe_u32 s56,s22,0x20000\n"
"s_bfe_u32 s72,s22,0x10003\n"
"s_lshr_b32 s73,s56,1\n"
"s_and_b32 s74,s56,1\n"
"s_mul_i32 s62,s56,4096\n"
"s_mul_i32 s63,s56,5120\n"
"s_add_u32 s63,s63,98304\n"
"s_bfe_u32 s58,s22,0x40004\n"
"s_bfe_u32 s59,s22,0x40008\n"
"s_mul_i32 s68,s58,204800\n"
"s_mul_i32 s60,s56,51200\n"
"s_add_u32 s68,s68,s60\n"
"s_lshl_b32 s59,s59,1\n"
"s_add_u32 s59,s59,s73\n"
"s_mul_i32 s66,s59,384\n"
"s_mul_i32 s60,s74,393216\n"
"s_add_u32 s66,s66,s60\n"
"s_lshr_b32 s58,s22,6\n"
"s_xor_b32 s58,s58,s22\n"
"s_bfe_u32 s58,s58,0x10004\n"
"s_mul_i32 s81,s58,640\n"
"s_mul_i32 s60,s81,6144\n"
"s_add_u32 s66,s66,s60\n"
"v_xor_b32 v19,64,v4\n"
"v_add_u32 v16,97280,v6\n"
"v_add_u32 v17,194560,v6\n"
"v_add_u32 v18,291840,v6\n"
"s_waitcnt lgkmcnt(0)\n"
"s_mov_b32 s80,s100\n"
"s_mov_b32 s87,2\n"
"s_add_u32 m0,s62,0\n"
"s_add_u32 s82,s66,0\n"
"s_sub_u32 s15,s82,7864320\n"
"s_min_u32 s82,s82,s15\n"
LD "v6,s[28:31],s82 offen lds\n"
LD "v16,s[28:31],s82 offen offset:1024 lds\n"
LD "v17,s[28:31],s82 offen offset:2048 lds\n"
LD "v18,s[28:31],s82 offen offset:3072 lds\n"
"s_add_u32 s14,s81,0\n"
"s_sub_u32 s15,s14,1280\n"
"s_min_u32 s14,s14,s15\n"
"s_add_u32 s14,s14,s68\n"
"s_add_u32 s84,s14,0\n"
"s_add_u32 m0,s63,0\n"
"s_nop 0\n"
LD "v4,s[24:27],s84 offen lds\n"
"s_add_u32 s84,s14,9216\n"
LD "v19,s[24:27],s84 offen offset:1024 lds\n"
"s_add_u32 s84,s14,18432\n"
LD "v4,s[24:27],s84 offen offset:2048 lds\n"
"s_add_u32 s84,s14,27648\n"
LD "v19,s[24:27],s84 offen offset:3072 lds\n"
"s_add_u32 s84,s14,40960\n"
"s_add_u32 m0,s63,4096\n"
"s_nop 0\n"
LD "v4,s[24:27],s84 offen lds\n"
"s_lshr_b32 s58,s23,3\n"
"s_mul_i32 s59,s58,7\n"
"s_and_b32 s60,s23,7\n"
"s_mul_i32 s60,s60,3\n"
"s_add_u32 s59,s59,s60\n"
"s_and_b32 s59,s59,15\n"
"s_lshl_b32 s59,s59,8\n"
"s_or_b32 s58,s58,s59\n"
"s_and_b32 s59,s23,3\n"
"s_lshl_b32 s59,s59,5\n"
"s_or_b32 s58,s58,s59\n"
"s_bfe_u32 s59,s23,0x10001\n"
"s_lshl_b32 s59,s59,12\n"
"s_or_b32 s58,s58,s59\n"
"s_lshl_b32 s23,s23,16\n"
"s_or_b32 s23,s23,s58\n"
"v_bfe_u32 v20,v0,4,2\n"
"v_mul_u32_u24 v21,384,v10\n"
"v_xor_b32_e64 v22,v11,v20\n"
"v_lshl_add_u32 v7,v22,4,v21\n"
"s_add_u32 m0,s62,16384\n"
"s_add_u32 s82,s66,128\n"
"s_sub_u32 s15,s82,7864320\n"
"s_min_u32 s82,s82,s15\n"
LD "v6,s[28:31],s82 offen lds\n"
"s_lshl_b32 s58,s74,6\n"
"v_xor_b32 v7,s58,v7\n"
"v_lshlrev_b32 v22,1,v20\n"
"v_xor_b32 v22,v11,v22\n"
"v_lshl_add_u32 v8,v22,4,v21\n"
"v_bfe_u32 v21,v0,1,3\n"
"v_xor_b32 v21,v21,v20\n"
"v_lshlrev_b32_e64 v21,4,v21\n"
"v_lshl_add_u32 v21,v11,7,v21\n"
"v_bfe_u32 v22,v0,3,1\n"
"s_mul_i32 s58,s73,12288\n"
"v_mul_u32_u24 v23,3072,v22\n"
"v_add3_u32 v1,v23,v21,s58\n"
"s_mul_i32 s59,s74,10240\n"
"s_add_u32 s59,s59,98304\n"
"v_lshl_add_u32 v2,v22,10,v21\n"
"v_add_u32 v2,s59,v2\n"
"v_bfe_u32 v21,v0,2,3\n"
"v_lshlrev_b32 v21,7,v21\n"
"v_bfe_u32 v23,v0,3,2\n"
LD "v16,s[28:31],s82 offen offset:1024 lds\n"
"v_lshl_add_u32 v21,v23,5,v21\n"
"v_and_b32_e64 v23,3,v0\n"
"v_lshl_add_u32 v21,v23,3,v21\n"
"v_bfe_u32 v23,v0,5,1\n"
"v_mul_u32_u24 v3,3072,v23\n"
"v_add3_u32 v3,v3,v21,s58\n"
"s_bfe_u32 s58,s22,0x40004\n"
"s_mul_i32 s58,s58,409600\n"
"s_mul_i32 s59,s72,1280\n"
"s_add_u32 s58,s58,s59\n"
"v_and_b32 v21,15,v0\n"
"v_mul_u32_u24 v21,2560,v21\n"
"v_lshl_add_u32 v21,v23,4,v21\n"
"v_bfe_u32 v23,v0,4,1\n"
"v_lshl_add_u32 v21,v23,5,v21\n"
"v_add_u32 v12,s58,v21\n"
"v_bfe_u32 v21,v0,2,2\n"
"v_lshlrev_b32 v21,7,v21\n"
"v_lshl_add_u32 v21,v20,10,v21\n"
"v_lshl_add_u32 v21,v22,6,v21\n"
LD "v17,s[28:31],s82 offen offset:2048 lds\n"
"v_and_b32_e64 v23,3,v0\n"
"v_lshl_add_u32 v21,v23,4,v21\n"
"s_lshl_b32 s58,s73,13\n"
"v_add_u32 v0,s58,v21\n"
"s_getpc_b64 s[52:53]\n"
".Lcts:\n"
"s_sub_u32 s52,s52,.Lcts-.Lcode0\n"
"s_subb_u32 s53,s53,0\n"
"s_bfe_u32 s58,s23,0x20000\n"
"s_lshl_b32 s58,s58,2\n"
"s_add_u32 s58,s58,s56\n"
"s_lshl_b32 s58,s58,13\n"
"v_lshlrev_b32 v15,3,v9\n"
"v_add_u32_e64 v15,s58,v15\n"
"v_min_u32 v15,.Lcode1-.Lcode0,v15\n"
"s_bfe_u32 s58,s22,0x4000c\n"
"s_bfe_u32 s59,s22,0x40010\n"
"s_mul_i32 s69,s58,204800\n"
"s_mul_i32 s60,s56,51200\n"
LD "v18,s[28:31],s82 offen offset:3072 lds\n"
"s_add_u32 s69,s69,s60\n"
"s_lshl_b32 s59,s59,1\n"
"s_add_u32 s59,s59,s73\n"
"s_mul_i32 s67,s59,384\n"
"s_mul_i32 s60,s74,393216\n"
"s_add_u32 s67,s67,s60\n"
"s_mul_i32 s60,s81,6144\n"
"s_add_u32 s67,s67,s60\n"
"s_bfe_u32 s58,s22,0x40004\n"
"s_bfe_u32 s59,s22,0x40008\n"
"s_lshl_b32 s59,s59,1\n"
"s_add_u32 s88,s59,s74\n"
"s_mul_i32 s88,s88,245760\n"
"s_add_u32 s59,s59,s73\n"
"s_lshl_b32 s70,s58,5\n"
"s_add_u32 s70,s70,s59\n"
"s_mul_i32 s70,s70,196608\n"
"s_bfe_u32 s58,s22,0x4000c\n"
"s_bfe_u32 s59,s22,0x40010\n"
"s_lshl_b32 s59,s59,1\n"
"s_add_u32 m0,s62,32768\n"
"s_add_u32 s82,s66,256\n"
"s_sub_u32 s15,s82,7864320\n"
"s_min_u32 s82,s82,s15\n"
LD "v6,s[28:31],s82 offen lds\n"
"s_add_u32 s89,s59,s74\n"
"s_mul_i32 s89,s89,245760\n"
"s_add_u32 s59,s59,s73\n"
"s_lshl_b32 s71,s58,5\n"
"s_add_u32 s71,s71,s59\n"
"s_mul_i32 s71,s71,196608\n"
"s_bitcmp1_b32 s23,12\n"
"s_cselect_b32 s58,s71,s70\n"
"s_cselect_b32 s71,s70,s71\n"
"s_mov_b32 s70,s58\n"
"s_cselect_b32 s58,s89,s88\n"
"s_cselect_b32 s89,s88,s89\n"
"s_mov_b32 s88,s58\n"
"s_cselect_b32 s69,98,1\n"
"s_mov_b32 s54,1037384597\n"
"s_mov_b32 s55,1037384597\n"
"s_bfe_u32 s58,s22,0x40004\n"
"s_lshl_b32 s59,s72,4\n"
"s_add_u32 s59,s59,s58\n"
"s_lshl_b32 s86,s59,12\n"
LD "v16,s[28:31],s82 offen offset:1024 lds\n"
"s_add_u32 s86,s86,100663296\n"
"s_mul_i32 s93,s59,1638400\n"
"s_add_u32 s93,s93,33554432\n"
"s_bfe_u32 s58,s22,0x30014\n"
"s_lshl_b32 s78,s58,1\n"
"s_add_u32 s78,s78,s74\n"
"s_lshl_b32 s94,s78,2\n"
"s_add_u32 s94,s94,s86\n"
"s_lshl_b32 s92,s58,2\n"
"s_lshr_b32 s92,0x31752064,s92\n"
"s_and_b32 s92,s92,7\n"
"s_lshl_b32 s92,s92,2\n"
"s_add_u32 s92,s92,s56\n"
"s_mul_i32 s91,s58,204800\n"
"s_add_u32 s91,s91,s93\n"
"s_mul_i32 s75,s56,30720\n"
"s_add_u32 s75,s75,s91\n"
"s_add_u32 s75,s75,0\n"
"s_mul_i32 s58,s74,5120\n"
"s_add_u32 s91,s91,s58\n"
LD "v17,s[28:31],s82 offen offset:2048 lds\n"
"s_mov_b32 s90,0xf0000000\n"
"s_mov_b32 s77,0xf0000000\n"
"s_mov_b32 s85,0xf0000000\n"
AW "a240,v0\n"
AW "a244,v1\n"
AW "a241,v2\n"
AW "a245,v3\n"
AW "a242,v4\n"
AW "a243,v6\n"
AW "a252,v12\n"
"s_mov_b32 s57,0\n"
"s_mov_b32 s83,0\n"
LD "v18,s[28:31],s82 offen offset:3072 lds\n"
"global_load_dword a249,v15,s[52:53]\n"
AR "v0,a240\n"
AR "v2,a241\n"
AR "v4,a242\n"
AR "v6,a243\n"
"v_xor_b32 v1,64,v0\n"
"v_xor_b32 v3,64,v2\n"
"v_xor_b32 v5,64,v4\n"
"v_add_u32 v11,97280,v6\n"
"v_add_u32 v12,194560,v6\n"
"v_add_u32 v13,291840,v6\n"
"s_mov_b64 vcc,0\n"
"s_waitcnt vmcnt(9) lgkmcnt(0)\n"
"s_barrier\n"
TR "a[200:201],v0\n"
TR "a[202:203],v0 offset:512\n"
TR "a[204:205],v0 offset:4096\n"
TR "a[206:207],v0 offset:4608\n"
"ds_read_b128 a[60:63],v2\n"
"ds_read_b128 a[100:103],v3\n"
"ds_read_b128 a[64:67],v2 offset:2048\n"
"ds_read_b128 a[104:107],v3 offset:2048\n"
"ds_read_b128 a[68:71],v2 offset:4096\n"
"ds_read_b128 a[108:111],v3 offset:4096\n"
"ds_read_b128 a[72:75],v2 offset:6144\n"
"ds_read_b128 a[112:115],v3 offset:6144\n"
"ds_read_b128 a[76:79],v2 offset:8192\n"
"ds_read_b128 a[116:119],v3 offset:8192\n"
TR "a[208:209],v0 offset:8\n"
TR "a[210:211],v0 offset:520\n"
TR "a[212:213],v0 offset:4104\n"
TR "a[214:215],v0 offset:4616\n"
"s_waitcnt lgkmcnt(0)\n"
MF "v[16:19],a[200:203],a[60:63],0\n"
TR "a[216:217],v1\n"
MF "v[16:19],a[204:207],a[100:103],v[16:19]\n"
"s_add_u32 s14,s81,128\n"
TR "a[218:219],v1 offset:512\n"
MF "v[24:27],a[204:207],a[104:107],0\n"
"s_sub_u32 s15,s14,1280\n"
"v_lshrrev_b32 v15,5,v9\n"
"v_min_u32 v15,11,v15\n"
MF "v[24:27],a[200:203],a[64:67],v[24:27]\n"
"s_min_u32 s14,s14,s15\n"
TR "a[220:221],v1 offset:4096\n"
"v_mul_u32_u24 v15,160,v15\n"
MF "v[32:35],a[200:203],a[68:71],0\n"
"s_add_u32 s14,s14,s68\n"
TR "a[222:223],v1 offset:4608\n"
"v_and_or_b32 v15,v9,16,v15\n"
MF "v[32:35],a[204:207],a[108:111],v[32:35]\n"
"s_add_u32 s84,s14,0\n"
"v_max3_f32 v220,|v16|,|v17|,|v18|\n"
"v_max_f32_e64 v220,v220,|v19|\n"
MF "v[40:43],a[204:207],a[112:115],0\n"
"s_add_u32 m0,s63,20480\n"
"v_readlane_b32 s12,v220,3\n"
"v_readlane_b32 s6,v220,22\n"
MF "v[40:43],a[200:203],a[72:75],v[40:43]\n"
"s_min_u32 s12,s12,s6\n"
"v_readlane_b32 s7,v220,41\n"
"v_readlane_b32 s8,v220,60\n"
MF "v[48:51],a[200:203],a[76:79],0\n"
"s_min_u32 s7,s7,s8\n"
"v_lshlrev_b32 v15,3,v15\n"
MF "v[48:51],a[204:207],a[116:119],v[48:51]\n"
LD "v4,s[24:27],s84 offen lds\n"
MF "v[52:55],a[212:215],a[116:119],0\n"
"s_min_u32 s12,s12,s7\n"
TR "a[200:201],v1 offset:8\n"
MF "v[52:55],a[208:211],a[76:79],v[52:55]\n"
"s_and_b32 s12,s12,0x7f800000\n"
TR "a[202:203],v1 offset:520\n"
MF "v[44:47],a[208:211],a[72:75],0\n"
"s_sub_u32 s12,s12,0x14000000\n"
MF "v[44:47],a[212:215],a[112:115],v[44:47]\n"
"s_cmp_lt_u32 s12,0x50000000\n"
"s_cselect_b32 s12,s12,0xe8400000\n"
TR "a[204:205],v1 offset:4104\n"
MF "v[36:39],a[212:215],a[108:111],0\n"
"s_add_u32 s12,s12,0x17c00000\n"
TR "a[206:207],v1 offset:4616\n"
MF "v[36:39],a[208:211],a[68:71],v[36:39]\n"
"s_bfe_u32 s60,s23,0x70000\n"
"v_mov_b32 v220,s12\n"
"v_mov_b32 v221,s12\n"
MF "v[28:31],a[208:211],a[64:67],0\n"
"s_add_u32 s84,s14,9216\n"
"v_mov_b32 v222,s12\n"
"v_mov_b32 v223,s12\n"
MF "v[28:31],a[212:215],a[104:107],v[28:31]\n"
LD "v5,s[24:27],s84 offen offset:1024 lds\n"
MF "v[20:23],a[212:215],a[100:103],0\n"
"s_lshl_b32 s60,s60,2\n"
MF "v[20:23],a[208:211],a[60:63],v[20:23]\n"
"s_waitcnt vmcnt(7) lgkmcnt(0)\n"
"s_barrier\n"
MF "v[56:59],a[216:219],a[60:63],v[220:223]\n"
"s_add_u32 s60,s60,s56\n"
TR "a[208:209],v0 offset:16384\n"
MF "v[56:59],a[220:223],a[100:103],v[56:59]\n"
"s_add_u32 s84,s14,18432\n"
TR "a[210:211],v0 offset:16896\n"
MF "v[64:67],a[220:223],a[104:107],v[220:223]\n"
LD "v4,s[24:27],s84 offen offset:2048 lds\n"
MF "v[64:67],a[216:219],a[64:67],v[64:67]\n"
"s_mul_i32 s60,s60,15360\n"
TR "a[212:213],v0 offset:20480\n"
MF "v[72:75],a[216:219],a[68:71],v[220:223]\n"
TR "a[214:215],v0 offset:20992\n"
MF "v[72:75],a[220:223],a[108:111],v[72:75]\n"
MF "v[80:83],a[220:223],a[112:115],v[220:223]\n"
"s_add_u32 s84,s14,27648\n"
MF "v[80:83],a[216:219],a[72:75],v[80:83]\n"
LD "v5,s[24:27],s84 offen offset:3072 lds\n"
MF "v[88:91],a[216:219],a[76:79],v[220:223]\n"
MF "v[88:91],a[220:223],a[116:119],v[88:91]\n"
MF "v[92:95],a[204:207],a[116:119],v[220:223]\n"
"s_add_u32 s84,s14,40960\n"
TR "a[216:217],v0 offset:16392\n"
MF "v[92:95],a[200:203],a[76:79],v[92:95]\n"
"s_add_u32 m0,s63,24576\n"
TR "a[218:219],v0 offset:16904\n"
MF "v[84:87],a[200:203],a[72:75],v[220:223]\n"
LD "v4,s[24:27],s84 offen lds\n"
MF "v[84:87],a[204:207],a[112:115],v[84:87]\n"
"s_add_u32 m0,s62,49152\n"
TR "a[220:221],v0 offset:20488\n"
MF "v[76:79],a[204:207],a[108:111],v[220:223]\n"
"s_add_u32 s82,s66,786432\n"
TR "a[222:223],v0 offset:21000\n"
MF "v[76:79],a[200:203],a[68:71],v[76:79]\n"
"s_sub_u32 s15,s82,7864320\n"
MF "v[68:71],a[200:203],a[64:67],v[220:223]\n"
"s_min_u32 s82,s82,s15\n"
MF "v[68:71],a[204:207],a[104:107],v[68:71]\n"
LD "v6,s[28:31],s82 offen lds\n"
MF "v[60:63],a[204:207],a[100:103],v[220:223]\n"
MF "v[60:63],a[200:203],a[60:63],v[60:63]\n"
"s_waitcnt lgkmcnt(4)\n"
MF "v[96:99],a[208:211],a[60:63],v[220:223]\n"
TR "a[200:201],v1 offset:16384\n"
MF "v[96:99],a[212:215],a[100:103],v[96:99]\n"
TR "a[202:203],v1 offset:16896\n"
MF "v[104:107],a[212:215],a[104:107],v[220:223]\n"
LD "v11,s[28:31],s82 offen offset:1024 lds\n"
MF "v[104:107],a[208:211],a[64:67],v[104:107]\n"
TR "a[204:205],v1 offset:20480\n"
MF "v[112:115],a[208:211],a[68:71],v[220:223]\n"
TR "a[206:207],v1 offset:20992\n"
MF "v[112:115],a[212:215],a[108:111],v[112:115]\n"
MF "v[120:123],a[212:215],a[112:115],v[220:223]\n"
MF "v[120:123],a[208:211],a[72:75],v[120:123]\n"
LD "v12,s[28:31],s82 offen offset:2048 lds\n"
MF "v[128:131],a[208:211],a[76:79],v[220:223]\n"
MF "v[128:131],a[212:215],a[116:119],v[128:131]\n"
"s_waitcnt lgkmcnt(4)\n"
MF "v[132:135],a[220:223],a[116:119],v[220:223]\n"
TR "a[208:209],v1 offset:16392\n"
MF "v[132:135],a[216:219],a[76:79],v[132:135]\n"
TR "a[210:211],v1 offset:16904\n"
MF "v[124:127],a[216:219],a[72:75],v[220:223]\n"
LD "v13,s[28:31],s82 offen offset:3072 lds\n"
MF "v[124:127],a[220:223],a[112:115],v[124:127]\n"
"s_add_u32 m0,s62,65536\n"
TR "a[212:213],v1 offset:20488\n"
MF "v[116:119],a[220:223],a[108:111],v[220:223]\n"
"s_add_u32 s82,s66,786560\n"
TR "a[214:215],v1 offset:21000\n"
MF "v[116:119],a[216:219],a[68:71],v[116:119]\n"
"s_sub_u32 s15,s82,7864320\n"
MF "v[108:111],a[216:219],a[64:67],v[220:223]\n"
"s_min_u32 s82,s82,s15\n"
MF "v[108:111],a[220:223],a[104:107],v[108:111]\n"
LD "v6,s[28:31],s82 offen lds\n"
MF "v[100:103],a[220:223],a[100:103],v[220:223]\n"
MF "v[100:103],a[216:219],a[60:63],v[100:103]\n"
"s_waitcnt vmcnt(5) lgkmcnt(0)\n"
"s_barrier\n"
MF "v[136:139],a[200:203],a[60:63],v[220:223]\n"
TR "a[216:217],v0 offset:32768\n"
MF "v[136:139],a[204:207],a[100:103],v[136:139]\n"
TR "a[218:219],v0 offset:33280\n"
MF "v[144:147],a[204:207],a[104:107],v[220:223]\n"
"ds_read_b128 a[140:143],v2 offset:20480\n"
MF "v[144:147],a[200:203],a[64:67],v[144:147]\n"
TR "a[220:221],v0 offset:36864\n"
MF "v[152:155],a[200:203],a[68:71],v[220:223]\n"
TR "a[222:223],v0 offset:37376\n"
MF "v[152:155],a[204:207],a[108:111],v[152:155]\n"
LD "v11,s[28:31],s82 offen offset:1024 lds\n"
MF "v[160:163],a[204:207],a[112:115],v[220:223]\n"
"ds_read_b128 a[180:183],v3 offset:20480\n"
MF "v[160:163],a[200:203],a[72:75],v[160:163]\n"
MF "v[168:171],a[200:203],a[76:79],v[220:223]\n"
MF "v[168:171],a[204:207],a[116:119],v[168:171]\n"
LD "v12,s[28:31],s82 offen offset:2048 lds\n"
MF "v[172:175],a[212:215],a[116:119],v[220:223]\n"
TR "a[200:201],v0 offset:32776\n"
MF "v[172:175],a[208:211],a[76:79],v[172:175]\n"
TR "a[202:203],v0 offset:33288\n"
MF "v[164:167],a[208:211],a[72:75],v[220:223]\n"
"ds_read_b128 a[144:147],v2 offset:22528\n"
MF "v[164:167],a[212:215],a[112:115],v[164:167]\n"
TR "a[204:205],v0 offset:36872\n"
MF "v[156:159],a[212:215],a[108:111],v[220:223]\n"
TR "a[206:207],v0 offset:37384\n"
MF "v[156:159],a[208:211],a[68:71],v[156:159]\n"
LD "v13,s[28:31],s82 offen offset:3072 lds\n"
MF "v[148:151],a[208:211],a[64:67],v[220:223]\n"
"ds_read_b128 a[184:187],v3 offset:22528\n"
MF "v[148:151],a[212:215],a[104:107],v[148:151]\n"
"s_add_u32 m0,s62,81920\n"
MF "v[140:143],a[212:215],a[100:103],v[220:223]\n"
"s_add_u32 s82,s66,786688\n"
"s_sub_u32 s15,s82,7864320\n"
"s_min_u32 s82,s82,s15\n"
MF "v[140:143],a[208:211],a[60:63],v[140:143]\n"
"s_waitcnt lgkmcnt(7)\n"
LD "v6,s[28:31],s82 offen lds\n"
MF "v[176:179],a[216:219],a[60:63],v[220:223]\n"
TR "a[208:209],v1 offset:32768\n"
MF "v[176:179],a[220:223],a[100:103],v[176:179]\n"
TR "a[210:211],v1 offset:33280\n"
MF "v[184:187],a[220:223],a[104:107],v[220:223]\n"
"ds_read_b128 a[148:151],v2 offset:24576\n"
MF "v[184:187],a[216:219],a[64:67],v[184:187]\n"
TR "a[212:213],v1 offset:36864\n"
MF "v[192:195],a[216:219],a[68:71],v[220:223]\n"
TR "a[214:215],v1 offset:37376\n"
MF "v[192:195],a[220:223],a[108:111],v[192:195]\n"
LD "v11,s[28:31],s82 offen offset:1024 lds\n"
MF "v[200:203],a[220:223],a[112:115],v[220:223]\n"
"ds_read_b128 a[188:191],v3 offset:24576\n"
MF "v[200:203],a[216:219],a[72:75],v[200:203]\n"
MF "v[208:211],a[216:219],a[76:79],v[220:223]\n"
MF "v[208:211],a[220:223],a[116:119],v[208:211]\n"
"s_waitcnt lgkmcnt(7)\n"
LD "v12,s[28:31],s82 offen offset:2048 lds\n"
MF "v[212:215],a[204:207],a[116:119],v[220:223]\n"
TR "a[216:217],v1 offset:32776\n"
MF "v[212:215],a[200:203],a[76:79],v[212:215]\n"
TR "a[218:219],v1 offset:33288\n"
MF "v[204:207],a[200:203],a[72:75],v[220:223]\n"
"ds_read_b128 a[152:155],v2 offset:26624\n"
MF "v[204:207],a[204:207],a[112:115],v[204:207]\n"
TR "a[220:221],v1 offset:36872\n"
MF "v[196:199],a[204:207],a[108:111],v[220:223]\n"
TR "a[222:223],v1 offset:37384\n"
MF "v[196:199],a[200:203],a[68:71],v[196:199]\n"
LD "v13,s[28:31],s82 offen offset:3072 lds\n"
MF "v[188:191],a[200:203],a[64:67],v[220:223]\n"
"v_add_u32 v0,0xc000,v0\n"
MF "v[188:191],a[204:207],a[104:107],v[188:191]\n"
"v_add_u32 v1,0xc000,v1\n"
MF "v[180:183],a[204:207],a[100:103],v[220:223]\n"
"s_add_u32 s14,s81,256\n"
"s_mov_b64 exec,0\n"
"s_bitcmp1_b32 s23,12\n"
"s_cbranch_scc1 .Lisy1\n"
"s_cmp_eq_u32 s56,0\n"
"s_cbranch_scc1 .Lpg8b\n"
"s_cmp_eq_u32 s56,1\n"
"s_cbranch_scc1 .Lpg9b\n"
"s_cmp_eq_u32 s56,2\n"
"s_cbranch_scc1 .Lpgt0ex_b\n"
"s_branch .Lpg10b\n"
".Lisy1:\n"
"s_cmp_eq_u32 s56,0\n"
"s_cbranch_scc1 .Lpgq0_c\n"
"s_cmp_eq_u32 s56,1\n"
"s_cbranch_scc1 .Lpg4c\n"
"s_cmp_eq_u32 s56,2\n"
"s_cbranch_scc1 .Lpg5c\n"
"s_branch .Lpg6c\n"
".Lisr1:\n"
"s_mov_b64 exec,-1\n"
".Lisx1:\n"
MF "v[180:183],a[200:203],a[60:63],v[180:183]\n"
"s_waitcnt vmcnt(8) lgkmcnt(0)\n"
"s_barrier\n"
MF "v[216:219],a[208:211],a[60:63],v[220:223]\n"
"s_sub_u32 s15,s14,1280\n"
TR "a[200:201],v0\n"
MF "v[216:219],a[212:215],a[100:103],v[216:219]\n"
"s_min_u32 s14,s14,s15\n"
TR "a[202:203],v0 offset:512\n"
MF "v[224:227],a[212:215],a[104:107],v[220:223]\n"
"s_add_u32 s14,s14,s68\n"
"ds_read_b128 a[192:195],v3 offset:26624\n"
MF "v[224:227],a[208:211],a[64:67],v[224:227]\n"
"s_add_u32 s84,s14,0\n"
TR "a[204:205],v0 offset:4096\n"
MF "v[232:235],a[208:211],a[68:71],v[220:223]\n"
"s_add_u32 m0,s63,40960\n"
TR "a[206:207],v0 offset:4608\n"
MF "v[232:235],a[212:215],a[108:111],v[232:235]\n"
LD "v4,s[24:27],s84 offen lds\n"
MF "v[240:243],a[212:215],a[112:115],v[220:223]\n"
"ds_read_b128 a[156:159],v2 offset:28672\n"
MF "v[240:243],a[208:211],a[72:75],v[240:243]\n"
"ds_read_b128 a[196:199],v3 offset:28672\n"
MF "v[248:251],a[208:211],a[76:79],v[220:223]\n"
"s_add_u32 s84,s14,9216\n"
MF "v[248:251],a[212:215],a[116:119],v[248:251]\n"
LD "v5,s[24:27],s84 offen offset:1024 lds\n"
MF "v[252:255],a[220:223],a[116:119],v[220:223]\n"
TR "a[208:209],v0 offset:8\n"
MF "v[252:255],a[216:219],a[76:79],v[252:255]\n"
TR "a[210:211],v0 offset:520\n"
MF "v[244:247],a[216:219],a[72:75],v[220:223]\n"
MF "v[244:247],a[220:223],a[112:115],v[244:247]\n"
TR "a[212:213],v0 offset:4104\n"
MF "v[236:239],a[220:223],a[108:111],v[220:223]\n"
"s_add_u32 s84,s14,18432\n"
TR "a[214:215],v0 offset:4616\n"
MF "v[236:239],a[216:219],a[68:71],v[236:239]\n"
LD "v4,s[24:27],s84 offen offset:2048 lds\n"
MF "v[228:231],a[216:219],a[64:67],v[220:223]\n"
MF "v[228:231],a[220:223],a[104:107],v[228:231]\n"
MF "v[220:223],a[220:223],a[100:103],v[220:223]\n"
".Lpg1a:\n"
"s_cbranch_execz .Lpg7a\n"
"s_add_u32 s84,s14,27648\n"
MF "v[220:223],a[216:219],a[60:63],v[220:223]\n"
"s_waitcnt lgkmcnt(4)\n"
LD "v5,s[24:27],s84 offen offset:3072 lds\n"
MF "v[16:19],a[200:203],a[140:143],v[16:19]\n"
TR "a[216:217],v1\n"
MF "v[16:19],a[204:207],a[180:183],v[16:19]\n"
TR "a[218:219],v1 offset:512\n"
MF "v[24:27],a[204:207],a[184:187],v[24:27]\n"
MF "v[24:27],a[200:203],a[144:147],v[24:27]\n"
"s_add_u32 s84,s14,40960\n"
TR "a[220:221],v1 offset:4096\n"
MF "v[32:35],a[200:203],a[148:151],v[32:35]\n"
"s_add_u32 m0,s63,45056\n"
TR "a[222:223],v1 offset:4608\n"
MF "v[32:35],a[204:207],a[188:191],v[32:35]\n"
LD "v4,s[24:27],s84 offen lds\n"
MF "v[40:43],a[204:207],a[192:195],v[40:43]\n"
MF "v[40:43],a[200:203],a[152:155],v[40:43]\n"
"s_add_u32 m0,s62,0\n"
MF "v[48:51],a[200:203],a[156:159],v[48:51]\n"
"s_add_u32 s82,s66,1572864\n"
"s_sub_u32 s15,s82,7864320\n"
"s_min_u32 s82,s82,s15\n"
MF "v[48:51],a[204:207],a[196:199],v[48:51]\n"
"s_waitcnt lgkmcnt(4)\n"
LD "v6,s[28:31],s82 offen lds\n"
MF "v[52:55],a[212:215],a[196:199],v[52:55]\n"
TR "a[200:201],v1 offset:8\n"
MF "v[52:55],a[208:211],a[156:159],v[52:55]\n"
TR "a[202:203],v1 offset:520\n"
MF "v[44:47],a[208:211],a[152:155],v[44:47]\n"
MF "v[44:47],a[212:215],a[192:195],v[44:47]\n"
TR "a[204:205],v1 offset:4104\n"
MF "v[36:39],a[212:215],a[188:191],v[36:39]\n"
TR "a[206:207],v1 offset:4616\n"
MF "v[36:39],a[208:211],a[148:151],v[36:39]\n"
LD "v11,s[28:31],s82 offen offset:1024 lds\n"
MF "v[28:31],a[208:211],a[144:147],v[28:31]\n"
MF "v[28:31],a[212:215],a[184:187],v[28:31]\n"
MF "v[20:23],a[212:215],a[180:183],v[20:23]\n"
LD "v12,s[28:31],s82 offen offset:2048 lds\n"
MF "v[20:23],a[208:211],a[140:143],v[20:23]\n"
"s_waitcnt vmcnt(12) lgkmcnt(0)\n"
"s_barrier\n"
"v_bfe_u32 v14,v9,4,4\n"
"v_lshlrev_b32 v14,2,v14\n"
"buffer_load_dword v10,v14,s[40:43],s86 offen sc1\n"
".Lhs2:\n"
MF "v[56:59],a[216:219],a[140:143],v[56:59]\n"
TR "a[208:209],v0 offset:16384\n"
MF "v[56:59],a[220:223],a[180:183],v[56:59]\n"
TR "a[210:211],v0 offset:16896\n"
MF "v[64:67],a[220:223],a[184:187],v[64:67]\n"
LD "v13,s[28:31],s82 offen offset:3072 lds\n"
MF "v[64:67],a[216:219],a[144:147],v[64:67]\n"
TR "a[212:213],v0 offset:20480\n"
MF "v[72:75],a[216:219],a[148:151],v[72:75]\n"
TR "a[214:215],v0 offset:20992\n"
MF "v[72:75],a[220:223],a[188:191],v[72:75]\n"
"s_add_u32 s14,s81,384\n"
"s_sub_u32 s15,s14,1280\n"
"s_min_u32 s14,s14,s15\n"
MF "v[80:83],a[220:223],a[192:195],v[80:83]\n"
"s_add_u32 s14,s14,s68\n"
"s_add_u32 s84,s14,0\n"
"s_add_u32 m0,s63,0\n"
MF "v[80:83],a[216:219],a[152:155],v[80:83]\n"
LD "v4,s[24:27],s84 offen lds\n"
MF "v[88:91],a[216:219],a[156:159],v[88:91]\n"
MF "v[88:91],a[220:223],a[196:199],v[88:91]\n"
MF "v[92:95],a[204:207],a[196:199],v[92:95]\n"
TR "a[216:217],v0 offset:16392\n"
MF "v[92:95],a[200:203],a[156:159],v[92:95]\n"
"s_add_u32 s84,s14,9216\n"
TR "a[218:219],v0 offset:16904\n"
MF "v[84:87],a[200:203],a[152:155],v[84:87]\n"
LD "v5,s[24:27],s84 offen offset:1024 lds\n"
MF "v[84:87],a[204:207],a[192:195],v[84:87]\n"
"s_add_u32 m0,s62,16384\n"
TR "a[220:221],v0 offset:20488\n"
MF "v[76:79],a[204:207],a[188:191],v[76:79]\n"
"s_add_u32 s82,s66,1572992\n"
TR "a[222:223],v0 offset:21000\n"
MF "v[76:79],a[200:203],a[148:151],v[76:79]\n"
"s_sub_u32 s15,s82,7864320\n"
MF "v[68:71],a[200:203],a[144:147],v[68:71]\n"
"s_min_u32 s82,s82,s15\n"
MF "v[68:71],a[204:207],a[184:187],v[68:71]\n"
LD "v6,s[28:31],s82 offen lds\n"
MF "v[60:63],a[204:207],a[180:183],v[60:63]\n"
MF "v[60:63],a[200:203],a[140:143],v[60:63]\n"
"s_waitcnt lgkmcnt(4)\n"
MF "v[96:99],a[208:211],a[140:143],v[96:99]\n"
TR "a[200:201],v1 offset:16384\n"
MF "v[96:99],a[212:215],a[180:183],v[96:99]\n"
TR "a[202:203],v1 offset:16896\n"
MF "v[104:107],a[212:215],a[184:187],v[104:107]\n"
LD "v11,s[28:31],s82 offen offset:1024 lds\n"
MF "v[104:107],a[208:211],a[144:147],v[104:107]\n"
TR "a[204:205],v1 offset:20480\n"
MF "v[112:115],a[208:211],a[148:151],v[112:115]\n"
TR "a[206:207],v1 offset:20992\n"
MF "v[112:115],a[212:215],a[188:191],v[112:115]\n"
MF "v[120:123],a[212:215],a[192:195],v[120:123]\n"
MF "v[120:123],a[208:211],a[152:155],v[120:123]\n"
LD "v12,s[28:31],s82 offen offset:2048 lds\n"
MF "v[128:131],a[208:211],a[156:159],v[128:131]\n"
MF "v[128:131],a[212:215],a[196:199],v[128:131]\n"
"s_waitcnt lgkmcnt(4)\n"
MF "v[132:135],a[220:223],a[196:199],v[132:135]\n"
TR "a[208:209],v1 offset:16392\n"
MF "v[132:135],a[216:219],a[156:159],v[132:135]\n"
TR "a[210:211],v1 offset:16904\n"
MF "v[124:127],a[216:219],a[152:155],v[124:127]\n"
LD "v13,s[28:31],s82 offen offset:3072 lds\n"
MF "v[124:127],a[220:223],a[192:195],v[124:127]\n"
TR "a[212:213],v1 offset:20488\n"
MF "v[116:119],a[220:223],a[188:191],v[116:119]\n"
TR "a[214:215],v1 offset:21000\n"
MF "v[116:119],a[216:219],a[148:151],v[116:119]\n"
"s_add_u32 s84,s14,20480\n"
MF "v[108:111],a[216:219],a[144:147],v[108:111]\n"
"s_add_u32 m0,s63,2048\n"
MF "v[108:111],a[220:223],a[184:187],v[108:111]\n"
LD "v4,s[24:27],s84 offen lds\n"
MF "v[100:103],a[220:223],a[180:183],v[100:103]\n"
MF "v[100:103],a[216:219],a[140:143],v[100:103]\n"
"s_waitcnt vmcnt(11) lgkmcnt(0)\n"
"s_barrier\n"
MF "v[136:139],a[200:203],a[140:143],v[136:139]\n"
TR "a[216:217],v0 offset:32768\n"
MF "v[136:139],a[204:207],a[180:183],v[136:139]\n"
TR "a[218:219],v0 offset:33280\n"
MF "v[144:147],a[204:207],a[184:187],v[144:147]\n"
"ds_read_b128 a[60:63],v2 offset:40960\n"
MF "v[144:147],a[200:203],a[144:147],v[144:147]\n"
TR "a[220:221],v0 offset:36864\n"
MF "v[152:155],a[200:203],a[148:151],v[152:155]\n"
"s_add_u32 s84,s14,29696\n"
TR "a[222:223],v0 offset:37376\n"
MF "v[152:155],a[204:207],a[188:191],v[152:155]\n"
LD "v5,s[24:27],s84 offen offset:1024 lds\n"
MF "v[160:163],a[204:207],a[192:195],v[160:163]\n"
"ds_read_b128 a[100:103],v3 offset:40960\n"
MF "v[160:163],a[200:203],a[152:155],v[160:163]\n"
"s_add_u32 m0,s62,32768\n"
MF "v[168:171],a[200:203],a[156:159],v[168:171]\n"
"s_add_u32 s82,s66,1573120\n"
"s_sub_u32 s15,s82,7864320\n"
"s_min_u32 s82,s82,s15\n"
MF "v[168:171],a[204:207],a[196:199],v[168:171]\n"
LD "v6,s[28:31],s82 offen lds\n"
MF "v[172:175],a[212:215],a[196:199],v[172:175]\n"
TR "a[200:201],v0 offset:32776\n"
MF "v[172:175],a[208:211],a[156:159],v[172:175]\n"
TR "a[202:203],v0 offset:33288\n"
MF "v[164:167],a[208:211],a[152:155],v[164:167]\n"
"ds_read_b128 a[64:67],v2 offset:43008\n"
MF "v[164:167],a[212:215],a[192:195],v[164:167]\n"
TR "a[204:205],v0 offset:36872\n"
MF "v[156:159],a[212:215],a[188:191],v[156:159]\n"
TR "a[206:207],v0 offset:37384\n"
MF "v[156:159],a[208:211],a[148:151],v[156:159]\n"
LD "v11,s[28:31],s82 offen offset:1024 lds\n"
MF "v[148:151],a[208:211],a[144:147],v[148:151]\n"
"ds_read_b128 a[104:107],v3 offset:43008\n"
MF "v[148:151],a[212:215],a[184:187],v[148:151]\n"
MF "v[140:143],a[212:215],a[180:183],v[140:143]\n"
MF "v[140:143],a[208:211],a[140:143],v[140:143]\n"
"s_waitcnt lgkmcnt(7)\n"
LD "v12,s[28:31],s82 offen offset:2048 lds\n"
MF "v[176:179],a[216:219],a[140:143],v[176:179]\n"
TR "a[208:209],v1 offset:32768\n"
MF "v[176:179],a[220:223],a[180:183],v[176:179]\n"
TR "a[210:211],v1 offset:33280\n"
MF "v[184:187],a[220:223],a[184:187],v[184:187]\n"
"ds_read_b128 a[68:71],v2 offset:45056\n"
MF "v[184:187],a[216:219],a[144:147],v[184:187]\n"
TR "a[212:213],v1 offset:36864\n"
MF "v[192:195],a[216:219],a[148:151],v[192:195]\n"
TR "a[214:215],v1 offset:37376\n"
MF "v[192:195],a[220:223],a[188:191],v[192:195]\n"
LD "v13,s[28:31],s82 offen offset:3072 lds\n"
MF "v[200:203],a[220:223],a[192:195],v[200:203]\n"
"ds_read_b128 a[108:111],v3 offset:45056\n"
MF "v[200:203],a[216:219],a[152:155],v[200:203]\n"
"s_add_u32 s84,s14,40960\n"
MF "v[208:211],a[216:219],a[156:159],v[208:211]\n"
"s_add_u32 m0,s63,4096\n"
MF "v[208:211],a[220:223],a[196:199],v[208:211]\n"
"s_waitcnt lgkmcnt(7)\n"
LD "v4,s[24:27],s84 offen lds\n"
MF "v[212:215],a[204:207],a[196:199],v[212:215]\n"
"s_sub_u32 s66,s66,s83\n"
TR "a[216:217],v1 offset:32776\n"
MF "v[212:215],a[200:203],a[156:159],v[212:215]\n"
"s_add_u32 m0,s62,49152\n"
TR "a[218:219],v1 offset:33288\n"
MF "v[204:207],a[200:203],a[152:155],v[204:207]\n"
"s_add_u32 s82,s66,2359296\n"
"ds_read_b128 a[72:75],v2 offset:47104\n"
MF "v[204:207],a[204:207],a[192:195],v[204:207]\n"
"s_sub_u32 s15,s82,7864320\n"
TR "a[220:221],v1 offset:36872\n"
MF "v[196:199],a[204:207],a[188:191],v[196:199]\n"
"s_min_u32 s82,s82,s15\n"
TR "a[222:223],v1 offset:37384\n"
MF "v[196:199],a[200:203],a[148:151],v[196:199]\n"
LD "v6,s[28:31],s82 offen lds\n"
MF "v[188:191],a[200:203],a[144:147],v[188:191]\n"
"v_add_u32 v0,0xffff4000,v0\n"
MF "v[188:191],a[204:207],a[184:187],v[188:191]\n"
"v_add_u32 v1,0xffff4000,v1\n"
MF "v[180:183],a[204:207],a[180:183],v[180:183]\n"
LD "v11,s[28:31],s82 offen offset:1024 lds\n"
MF "v[180:183],a[200:203],a[140:143],v[180:183]\n"
"s_waitcnt vmcnt(15) lgkmcnt(0)\n"
"s_barrier\n"
MF "v[216:219],a[208:211],a[140:143],v[216:219]\n"
TR "a[200:201],v0\n"
MF "v[216:219],a[212:215],a[180:183],v[216:219]\n"
TR "a[202:203],v0 offset:512\n"
MF "v[224:227],a[212:215],a[184:187],v[224:227]\n"
"ds_read_b128 a[112:115],v3 offset:47104\n"
MF "v[224:227],a[208:211],a[144:147],v[224:227]\n"
TR "a[204:205],v0 offset:4096\n"
MF "v[232:235],a[208:211],a[148:151],v[232:235]\n"
TR "a[206:207],v0 offset:4608\n"
MF "v[232:235],a[212:215],a[188:191],v[232:235]\n"
LD "v12,s[28:31],s82 offen offset:2048 lds\n"
MF "v[240:243],a[212:215],a[192:195],v[240:243]\n"
"ds_read_b128 a[76:79],v2 offset:49152\n"
MF "v[240:243],a[208:211],a[152:155],v[240:243]\n"
"ds_read_b128 a[116:119],v3 offset:49152\n"
MF "v[248:251],a[208:211],a[156:159],v[248:251]\n"
MF "v[248:251],a[212:215],a[196:199],v[248:251]\n"
LD "v13,s[28:31],s82 offen offset:3072 lds\n"
MF "v[252:255],a[220:223],a[196:199],v[252:255]\n"
TR "a[208:209],v0 offset:8\n"
MF "v[252:255],a[216:219],a[156:159],v[252:255]\n"
TR "a[210:211],v0 offset:520\n"
MF "v[244:247],a[216:219],a[152:155],v[244:247]\n"
"s_add_u32 s14,s81,512\n"
"s_sub_u32 s15,s14,1280\n"
MF "v[244:247],a[220:223],a[192:195],v[244:247]\n"
"s_min_u32 s14,s14,s15\n"
"s_add_u32 s14,s14,s68\n"
TR "a[212:213],v0 offset:4104\n"
MF "v[236:239],a[220:223],a[188:191],v[236:239]\n"
"s_add_u32 s84,s14,0\n"
"s_add_u32 m0,s63,20480\n"
TR "a[214:215],v0 offset:4616\n"
MF "v[236:239],a[216:219],a[148:151],v[236:239]\n"
LD "v4,s[24:27],s84 offen lds\n"
MF "v[228:231],a[216:219],a[144:147],v[228:231]\n"
MF "v[228:231],a[220:223],a[184:187],v[228:231]\n"
MF "v[220:223],a[220:223],a[180:183],v[220:223]\n"
"s_add_u32 s84,s14,9216\n"
MF "v[220:223],a[216:219],a[140:143],v[220:223]\n"
"s_waitcnt lgkmcnt(4)\n"
LD "v5,s[24:27],s84 offen offset:1024 lds\n"
MF "v[16:19],a[200:203],a[60:63],v[16:19]\n"
TR "a[216:217],v1\n"
MF "v[16:19],a[204:207],a[100:103],v[16:19]\n"
"s_add_u32 m0,s62,65536\n"
TR "a[218:219],v1 offset:512\n"
MF "v[24:27],a[204:207],a[104:107],v[24:27]\n"
"s_add_u32 s82,s66,2359424\n"
MF "v[24:27],a[200:203],a[64:67],v[24:27]\n"
"s_sub_u32 s15,s82,7864320\n"
TR "a[220:221],v1 offset:4096\n"
MF "v[32:35],a[200:203],a[68:71],v[32:35]\n"
"s_min_u32 s82,s82,s15\n"
TR "a[222:223],v1 offset:4608\n"
MF "v[32:35],a[204:207],a[108:111],v[32:35]\n"
LD "v6,s[28:31],s82 offen lds\n"
MF "v[40:43],a[204:207],a[112:115],v[40:43]\n"
MF "v[40:43],a[200:203],a[72:75],v[40:43]\n"
MF "v[48:51],a[200:203],a[76:79],v[48:51]\n"
MF "v[48:51],a[204:207],a[116:119],v[48:51]\n"
"s_waitcnt lgkmcnt(4)\n"
LD "v11,s[28:31],s82 offen offset:1024 lds\n"
MF "v[52:55],a[212:215],a[116:119],v[52:55]\n"
TR "a[200:201],v1 offset:8\n"
MF "v[52:55],a[208:211],a[76:79],v[52:55]\n"
TR "a[202:203],v1 offset:520\n"
MF "v[44:47],a[208:211],a[72:75],v[44:47]\n"
MF "v[44:47],a[212:215],a[112:115],v[44:47]\n"
TR "a[204:205],v1 offset:4104\n"
MF "v[36:39],a[212:215],a[108:111],v[36:39]\n"
TR "a[206:207],v1 offset:4616\n"
MF "v[36:39],a[208:211],a[68:71],v[36:39]\n"
LD "v12,s[28:31],s82 offen offset:2048 lds\n"
MF "v[28:31],a[208:211],a[64:67],v[28:31]\n"
MF "v[28:31],a[212:215],a[104:107],v[28:31]\n"
MF "v[20:23],a[212:215],a[100:103],v[20:23]\n"
LD "v13,s[28:31],s82 offen offset:3072 lds\n"
MF "v[20:23],a[208:211],a[60:63],v[20:23]\n"
"s_waitcnt vmcnt(17) lgkmcnt(0)\n"
"s_barrier\n"
MF "v[56:59],a[216:219],a[60:63],v[56:59]\n"
"s_add_u32 s84,s14,20480\n"
TR "a[208:209],v0 offset:16384\n"
MF "v[56:59],a[220:223],a[100:103],v[56:59]\n"
"s_add_u32 m0,s63,22528\n"
TR "a[210:211],v0 offset:16896\n"
MF "v[64:67],a[220:223],a[104:107],v[64:67]\n"
LD "v4,s[24:27],s84 offen lds\n"
MF "v[64:67],a[216:219],a[64:67],v[64:67]\n"
TR "a[212:213],v0 offset:20480\n"
MF "v[72:75],a[216:219],a[68:71],v[72:75]\n"
TR "a[214:215],v0 offset:20992\n"
MF "v[72:75],a[220:223],a[108:111],v[72:75]\n"
MF "v[80:83],a[220:223],a[112:115],v[80:83]\n"
"s_add_u32 s84,s14,29696\n"
MF "v[80:83],a[216:219],a[72:75],v[80:83]\n"
LD "v5,s[24:27],s84 offen offset:1024 lds\n"
MF "v[88:91],a[216:219],a[76:79],v[88:91]\n"
"s_add_u32 m0,s62,81920\n"
MF "v[88:91],a[220:223],a[116:119],v[88:91]\n"
"s_add_u32 s82,s66,2359552\n"
MF "v[92:95],a[204:207],a[116:119],v[92:95]\n"
"s_sub_u32 s15,s82,7864320\n"
TR "a[216:217],v0 offset:16392\n"
MF "v[92:95],a[200:203],a[76:79],v[92:95]\n"
"s_min_u32 s82,s82,s15\n"
TR "a[218:219],v0 offset:16904\n"
MF "v[84:87],a[200:203],a[72:75],v[84:87]\n"
LD "v6,s[28:31],s82 offen lds\n"
MF "v[84:87],a[204:207],a[112:115],v[84:87]\n"
TR "a[220:221],v0 offset:20488\n"
MF "v[76:79],a[204:207],a[108:111],v[76:79]\n"
TR "a[222:223],v0 offset:21000\n"
MF "v[76:79],a[200:203],a[68:71],v[76:79]\n"
MF "v[68:71],a[200:203],a[64:67],v[68:71]\n"
MF "v[68:71],a[204:207],a[104:107],v[68:71]\n"
LD "v11,s[28:31],s82 offen offset:1024 lds\n"
MF "v[60:63],a[204:207],a[100:103],v[60:63]\n"
MF "v[60:63],a[200:203],a[60:63],v[60:63]\n"
"s_waitcnt lgkmcnt(4)\n"
MF "v[96:99],a[208:211],a[60:63],v[96:99]\n"
TR "a[200:201],v1 offset:16384\n"
MF "v[96:99],a[212:215],a[100:103],v[96:99]\n"
TR "a[202:203],v1 offset:16896\n"
MF "v[104:107],a[212:215],a[104:107],v[104:107]\n"
LD "v12,s[28:31],s82 offen offset:2048 lds\n"
MF "v[104:107],a[208:211],a[64:67],v[104:107]\n"
TR "a[204:205],v1 offset:20480\n"
MF "v[112:115],a[208:211],a[68:71],v[112:115]\n"
TR "a[206:207],v1 offset:20992\n"
MF "v[112:115],a[212:215],a[108:111],v[112:115]\n"
MF "v[120:123],a[212:215],a[112:115],v[120:123]\n"
MF "v[120:123],a[208:211],a[72:75],v[120:123]\n"
LD "v13,s[28:31],s82 offen offset:3072 lds\n"
MF "v[128:131],a[208:211],a[76:79],v[128:131]\n"
MF "v[128:131],a[212:215],a[116:119],v[128:131]\n"
"s_waitcnt lgkmcnt(4)\n"
MF "v[132:135],a[220:223],a[116:119],v[132:135]\n"
"s_add_u32 s84,s14,40960\n"
TR "a[208:209],v1 offset:16392\n"
MF "v[132:135],a[216:219],a[76:79],v[132:135]\n"
"s_add_u32 m0,s63,24576\n"
TR "a[210:211],v1 offset:16904\n"
MF "v[124:127],a[216:219],a[72:75],v[124:127]\n"
LD "v4,s[24:27],s84 offen lds\n"
MF "v[124:127],a[220:223],a[112:115],v[124:127]\n"
"s_add_u32 m0,s62,0\n"
TR "a[212:213],v1 offset:20488\n"
MF "v[116:119],a[220:223],a[108:111],v[116:119]\n"
"s_add_u32 s82,s66,3145728\n"
TR "a[214:215],v1 offset:21000\n"
MF "v[116:119],a[216:219],a[68:71],v[116:119]\n"
"s_sub_u32 s15,s82,7864320\n"
MF "v[108:111],a[216:219],a[64:67],v[108:111]\n"
"s_min_u32 s82,s82,s15\n"
MF "v[108:111],a[220:223],a[104:107],v[108:111]\n"
LD "v6,s[28:31],s82 offen lds\n"
MF "v[100:103],a[220:223],a[100:103],v[100:103]\n"
MF "v[100:103],a[216:219],a[60:63],v[100:103]\n"
"s_waitcnt vmcnt(18) lgkmcnt(0)\n"
"s_barrier\n"
MF "v[136:139],a[200:203],a[60:63],v[136:139]\n"
TR "a[216:217],v0 offset:32768\n"
MF "v[136:139],a[204:207],a[100:103],v[136:139]\n"
TR "a[218:219],v0 offset:33280\n"
MF "v[144:147],a[204:207],a[104:107],v[144:147]\n"
"ds_read_b128 a[140:143],v2\n"
MF "v[144:147],a[200:203],a[64:67],v[144:147]\n"
TR "a[220:221],v0 offset:36864\n"
MF "v[152:155],a[200:203],a[68:71],v[152:155]\n"
TR "a[222:223],v0 offset:37376\n"
MF "v[152:155],a[204:207],a[108:111],v[152:155]\n"
LD "v11,s[28:31],s82 offen offset:1024 lds\n"
MF "v[160:163],a[204:207],a[112:115],v[160:163]\n"
"ds_read_b128 a[180:183],v3\n"
MF "v[160:163],a[200:203],a[72:75],v[160:163]\n"
MF "v[168:171],a[200:203],a[76:79],v[168:171]\n"
MF "v[168:171],a[204:207],a[116:119],v[168:171]\n"
LD "v12,s[28:31],s82 offen offset:2048 lds\n"
MF "v[172:175],a[212:215],a[116:119],v[172:175]\n"
TR "a[200:201],v0 offset:32776\n"
MF "v[172:175],a[208:211],a[76:79],v[172:175]\n"
TR "a[202:203],v0 offset:33288\n"
MF "v[164:167],a[208:211],a[72:75],v[164:167]\n"
"ds_read_b128 a[144:147],v2 offset:2048\n"
MF "v[164:167],a[212:215],a[112:115],v[164:167]\n"
TR "a[204:205],v0 offset:36872\n"
MF "v[156:159],a[212:215],a[108:111],v[156:159]\n"
TR "a[206:207],v0 offset:37384\n"
MF "v[156:159],a[208:211],a[68:71],v[156:159]\n"
LD "v13,s[28:31],s82 offen offset:3072 lds\n"
MF "v[148:151],a[208:211],a[64:67],v[148:151]\n"
"ds_read_b128 a[184:187],v3 offset:2048\n"
MF "v[148:151],a[212:215],a[104:107],v[148:151]\n"
"s_add_u32 s14,s81,640\n"
"s_sub_u32 s15,s14,1280\n"
"s_min_u32 s14,s14,s15\n"
MF "v[140:143],a[212:215],a[100:103],v[140:143]\n"
"s_add_u32 s14,s14,s68\n"
"s_add_u32 s84,s14,0\n"
"s_add_u32 m0,s63,40960\n"
MF "v[140:143],a[208:211],a[60:63],v[140:143]\n"
"s_waitcnt lgkmcnt(7)\n"
LD "v4,s[24:27],s84 offen lds\n"
MF "v[176:179],a[216:219],a[60:63],v[176:179]\n"
TR "a[208:209],v1 offset:32768\n"
MF "v[176:179],a[220:223],a[100:103],v[176:179]\n"
TR "a[210:211],v1 offset:33280\n"
MF "v[184:187],a[220:223],a[104:107],v[184:187]\n"
"ds_read_b128 a[148:151],v2 offset:4096\n"
MF "v[184:187],a[216:219],a[64:67],v[184:187]\n"
TR "a[212:213],v1 offset:36864\n"
MF "v[192:195],a[216:219],a[68:71],v[192:195]\n"
"s_add_u32 s84,s14,9216\n"
TR "a[214:215],v1 offset:37376\n"
MF "v[192:195],a[220:223],a[108:111],v[192:195]\n"
LD "v5,s[24:27],s84 offen offset:1024 lds\n"
MF "v[200:203],a[220:223],a[112:115],v[200:203]\n"
"ds_read_b128 a[188:191],v3 offset:4096\n"
MF "v[200:203],a[216:219],a[72:75],v[200:203]\n"
"s_add_u32 m0,s62,16384\n"
MF "v[208:211],a[216:219],a[76:79],v[208:211]\n"
"s_add_u32 s82,s66,3145856\n"
"s_sub_u32 s15,s82,7864320\n"
"s_min_u32 s82,s82,s15\n"
MF "v[208:211],a[220:223],a[116:119],v[208:211]\n"
"s_waitcnt lgkmcnt(7)\n"
LD "v6,s[28:31],s82 offen lds\n"
MF "v[212:215],a[204:207],a[116:119],v[212:215]\n"
TR "a[216:217],v1 offset:32776\n"
MF "v[212:215],a[200:203],a[76:79],v[212:215]\n"
TR "a[218:219],v1 offset:33288\n"
MF "v[204:207],a[200:203],a[72:75],v[204:207]\n"
"ds_read_b128 a[152:155],v2 offset:6144\n"
MF "v[204:207],a[204:207],a[112:115],v[204:207]\n"
TR "a[220:221],v1 offset:36872\n"
MF "v[196:199],a[204:207],a[108:111],v[196:199]\n"
TR "a[222:223],v1 offset:37384\n"
MF "v[196:199],a[200:203],a[68:71],v[196:199]\n"
LD "v11,s[28:31],s82 offen offset:1024 lds\n"
MF "v[188:191],a[200:203],a[64:67],v[188:191]\n"
"v_add_u32 v0,0xc000,v0\n"
MF "v[188:191],a[204:207],a[104:107],v[188:191]\n"
"v_add_u32 v1,0xc000,v1\n"
MF "v[180:183],a[204:207],a[100:103],v[180:183]\n"
LD "v12,s[28:31],s82 offen offset:2048 lds\n"
MF "v[180:183],a[200:203],a[60:63],v[180:183]\n"
"s_waitcnt vmcnt(22) lgkmcnt(0)\n"
"s_barrier\n"
MF "v[216:219],a[208:211],a[60:63],v[216:219]\n"
TR "a[200:201],v0\n"
MF "v[216:219],a[212:215],a[100:103],v[216:219]\n"
TR "a[202:203],v0 offset:512\n"
MF "v[224:227],a[212:215],a[104:107],v[224:227]\n"
"ds_read_b128 a[192:195],v3 offset:6144\n"
MF "v[224:227],a[208:211],a[64:67],v[224:227]\n"
TR "a[204:205],v0 offset:4096\n"
MF "v[232:235],a[208:211],a[68:71],v[232:235]\n"
TR "a[206:207],v0 offset:4608\n"
MF "v[232:235],a[212:215],a[108:111],v[232:235]\n"
LD "v13,s[28:31],s82 offen offset:3072 lds\n"
MF "v[240:243],a[212:215],a[112:115],v[240:243]\n"
"ds_read_b128 a[156:159],v2 offset:8192\n"
MF "v[240:243],a[208:211],a[72:75],v[240:243]\n"
"s_add_u32 s84,s14,20480\n"
"ds_read_b128 a[196:199],v3 offset:8192\n"
MF "v[248:251],a[208:211],a[76:79],v[248:251]\n"
"s_add_u32 m0,s63,43008\n"
MF "v[248:251],a[212:215],a[116:119],v[248:251]\n"
LD "v4,s[24:27],s84 offen lds\n"
MF "v[252:255],a[220:223],a[116:119],v[252:255]\n"
TR "a[208:209],v0 offset:8\n"
MF "v[252:255],a[216:219],a[76:79],v[252:255]\n"
TR "a[210:211],v0 offset:520\n"
MF "v[244:247],a[216:219],a[72:75],v[244:247]\n"
MF "v[244:247],a[220:223],a[112:115],v[244:247]\n"
TR "a[212:213],v0 offset:4104\n"
MF "v[236:239],a[220:223],a[108:111],v[236:239]\n"
"s_add_u32 s84,s14,29696\n"
TR "a[214:215],v0 offset:4616\n"
MF "v[236:239],a[216:219],a[68:71],v[236:239]\n"
LD "v5,s[24:27],s84 offen offset:1024 lds\n"
MF "v[228:231],a[216:219],a[64:67],v[228:231]\n"
MF "v[228:231],a[220:223],a[104:107],v[228:231]\n"
"s_add_u32 m0,s62,32768\n"
MF "v[220:223],a[220:223],a[100:103],v[220:223]\n"
"s_add_u32 s82,s66,3145984\n"
"s_sub_u32 s15,s82,7864320\n"
"s_min_u32 s82,s82,s15\n"
MF "v[220:223],a[216:219],a[60:63],v[220:223]\n"
"s_waitcnt lgkmcnt(4)\n"
LD "v6,s[28:31],s82 offen lds\n"
MF "v[16:19],a[200:203],a[140:143],v[16:19]\n"
TR "a[216:217],v1\n"
MF "v[16:19],a[204:207],a[180:183],v[16:19]\n"
TR "a[218:219],v1 offset:512\n"
MF "v[24:27],a[204:207],a[184:187],v[24:27]\n"
MF "v[24:27],a[200:203],a[144:147],v[24:27]\n"
TR "a[220:221],v1 offset:4096\n"
MF "v[32:35],a[200:203],a[148:151],v[32:35]\n"
TR "a[222:223],v1 offset:4608\n"
MF "v[32:35],a[204:207],a[188:191],v[32:35]\n"
LD "v11,s[28:31],s82 offen offset:1024 lds\n"
MF "v[40:43],a[204:207],a[192:195],v[40:43]\n"
MF "v[40:43],a[200:203],a[152:155],v[40:43]\n"
MF "v[48:51],a[200:203],a[156:159],v[48:51]\n"
MF "v[48:51],a[204:207],a[196:199],v[48:51]\n"
"s_waitcnt lgkmcnt(4)\n"
LD "v12,s[28:31],s82 offen offset:2048 lds\n"
MF "v[52:55],a[212:215],a[196:199],v[52:55]\n"
TR "a[200:201],v1 offset:8\n"
MF "v[52:55],a[208:211],a[156:159],v[52:55]\n"
TR "a[202:203],v1 offset:520\n"
MF "v[44:47],a[208:211],a[152:155],v[44:47]\n"
MF "v[44:47],a[212:215],a[192:195],v[44:47]\n"
TR "a[204:205],v1 offset:4104\n"
MF "v[36:39],a[212:215],a[188:191],v[36:39]\n"
".Lpg2a:\n"
"s_cbranch_execz .Lisr1\n"
TR "a[206:207],v1 offset:4616\n"
MF "v[36:39],a[208:211],a[148:151],v[36:39]\n"
LD "v13,s[28:31],s82 offen offset:3072 lds\n"
MF "v[28:31],a[208:211],a[144:147],v[28:31]\n"
"s_add_u32 s84,s14,40960\n"
MF "v[28:31],a[212:215],a[184:187],v[28:31]\n"
"s_add_u32 m0,s63,45056\n"
MF "v[20:23],a[212:215],a[180:183],v[20:23]\n"
LD "v4,s[24:27],s84 offen lds\n"
MF "v[20:23],a[208:211],a[140:143],v[20:23]\n"
"s_waitcnt vmcnt(24) lgkmcnt(0)\n"
"s_barrier\n"
AW "a253,v10\n"
"v_add_u32 v10,1,v10\n"
"s_bfe_u32 s58,s22,0x30014\n"
"s_lshl_b32 s58,s58,2\n"
"s_add_u32 s58,s58,s56\n"
"s_lshl_b32 s58,s58,6\n"
"s_add_u32 s58,s58,s86\n"
"v_readlane_b32 s76,v10,s78\n"
"buffer_store_dword v10,v14,s[40:43],s58 offen offset:128 sc1\n"
".Lhs5:\n"
MF "v[56:59],a[216:219],a[140:143],v[56:59]\n"
"s_add_u32 m0,s62,49152\n"
"s_add_u32 s82,s66,3932160\n"
TR "a[208:209],v0 offset:16384\n"
MF "v[56:59],a[220:223],a[180:183],v[56:59]\n"
"s_sub_u32 s15,s82,7864320\n"
"s_min_u32 s82,s82,s15\n"
TR "a[210:211],v0 offset:16896\n"
MF "v[64:67],a[220:223],a[184:187],v[64:67]\n"
LD "v6,s[28:31],s82 offen lds\n"
MF "v[64:67],a[216:219],a[144:147],v[64:67]\n"
TR "a[212:213],v0 offset:20480\n"
MF "v[72:75],a[216:219],a[148:151],v[72:75]\n"
TR "a[214:215],v0 offset:20992\n"
MF "v[72:75],a[220:223],a[188:191],v[72:75]\n"
"buffer_load_dword a249,v15,s[48:51],s60 offen offset:1024\n"
MF "v[80:83],a[220:223],a[192:195],v[80:83]\n"
"s_bfe_u32 s59,s23,0x30003\n"
MF "v[80:83],a[216:219],a[152:155],v[80:83]\n"
LD "v11,s[28:31],s82 offen offset:1024 lds\n"
MF "v[88:91],a[216:219],a[156:159],v[88:91]\n"
"s_add_u32 s59,s59,1\n"
MF "v[88:91],a[220:223],a[196:199],v[88:91]\n"
"s_lshl_b32 s59,s59,7\n"
MF "v[92:95],a[204:207],a[196:199],v[92:95]\n"
"s_and_b32 s59,s59,768\n"
TR "a[216:217],v0 offset:16392\n"
MF "v[92:95],a[200:203],a[156:159],v[92:95]\n"
"s_add_u32 s59,s59,s60\n"
TR "a[218:219],v0 offset:16904\n"
MF "v[84:87],a[200:203],a[152:155],v[84:87]\n"
LD "v12,s[28:31],s82 offen offset:2048 lds\n"
MF "v[84:87],a[204:207],a[192:195],v[84:87]\n"
TR "a[220:221],v0 offset:20488\n"
MF "v[76:79],a[204:207],a[188:191],v[76:79]\n"
TR "a[222:223],v0 offset:21000\n"
MF "v[76:79],a[200:203],a[148:151],v[76:79]\n"
MF "v[68:71],a[200:203],a[144:147],v[68:71]\n"
MF "v[68:71],a[204:207],a[184:187],v[68:71]\n"
LD "v13,s[28:31],s82 offen offset:3072 lds\n"
MF "v[60:63],a[204:207],a[180:183],v[60:63]\n"
MF "v[60:63],a[200:203],a[140:143],v[60:63]\n"
"s_add_u32 s14,s81,768\n"
"s_sub_u32 s15,s14,1280\n"
"s_waitcnt lgkmcnt(4)\n"
MF "v[96:99],a[208:211],a[140:143],v[96:99]\n"
"s_min_u32 s14,s14,s15\n"
"s_add_u32 s14,s14,s68\n"
TR "a[200:201],v1 offset:16384\n"
MF "v[96:99],a[212:215],a[180:183],v[96:99]\n"
"s_add_u32 s84,s14,0\n"
"s_add_u32 m0,s63,0\n"
TR "a[202:203],v1 offset:16896\n"
MF "v[104:107],a[212:215],a[184:187],v[104:107]\n"
LD "v4,s[24:27],s84 offen lds\n"
MF "v[104:107],a[208:211],a[144:147],v[104:107]\n"
TR "a[204:205],v1 offset:20480\n"
MF "v[112:115],a[208:211],a[148:151],v[112:115]\n"
TR "a[206:207],v1 offset:20992\n"
MF "v[112:115],a[212:215],a[188:191],v[112:115]\n"
MF "v[120:123],a[212:215],a[192:195],v[120:123]\n"
"s_add_u32 s84,s14,9216\n"
MF "v[120:123],a[208:211],a[152:155],v[120:123]\n"
LD "v5,s[24:27],s84 offen offset:1024 lds\n"
MF "v[128:131],a[208:211],a[156:159],v[128:131]\n"
MF "v[128:131],a[212:215],a[196:199],v[128:131]\n"
"s_waitcnt lgkmcnt(4)\n"
MF "v[132:135],a[220:223],a[196:199],v[132:135]\n"
TR "a[208:209],v1 offset:16392\n"
MF "v[132:135],a[216:219],a[156:159],v[132:135]\n"
TR "a[210:211],v1 offset:16904\n"
MF "v[124:127],a[216:219],a[152:155],v[124:127]\n"
MF "v[124:127],a[220:223],a[192:195],v[124:127]\n"
TR "a[212:213],v1 offset:20488\n"
MF "v[116:119],a[220:223],a[188:191],v[116:119]\n"
TR "a[214:215],v1 offset:21000\n"
MF "v[116:119],a[216:219],a[148:151],v[116:119]\n"
MF "v[108:111],a[216:219],a[144:147],v[108:111]\n"
MF "v[108:111],a[220:223],a[184:187],v[108:111]\n"
MF "v[100:103],a[220:223],a[180:183],v[100:103]\n"
MF "v[100:103],a[216:219],a[140:143],v[100:103]\n"
"s_waitcnt vmcnt(24) lgkmcnt(0)\n"
"s_barrier\n"
MF "v[136:139],a[200:203],a[140:143],v[136:139]\n"
TR "a[216:217],v0 offset:32768\n"
MF "v[136:139],a[204:207],a[180:183],v[136:139]\n"
"s_add_u32 m0,s62,65536\n"
TR "a[218:219],v0 offset:33280\n"
MF "v[144:147],a[204:207],a[184:187],v[144:147]\n"
"s_add_u32 s82,s66,3932288\n"
"ds_read_b128 a[60:63],v2 offset:20480\n"
MF "v[144:147],a[200:203],a[144:147],v[144:147]\n"
"s_sub_u32 s15,s82,7864320\n"
TR "a[220:221],v0 offset:36864\n"
MF "v[152:155],a[200:203],a[148:151],v[152:155]\n"
"s_min_u32 s82,s82,s15\n"
TR "a[222:223],v0 offset:37376\n"
MF "v[152:155],a[204:207],a[188:191],v[152:155]\n"
LD "v6,s[28:31],s82 offen lds\n"
MF "v[160:163],a[204:207],a[192:195],v[160:163]\n"
"ds_read_b128 a[100:103],v3 offset:20480\n"
MF "v[160:163],a[200:203],a[152:155],v[160:163]\n"
MF "v[168:171],a[200:203],a[156:159],v[168:171]\n"
MF "v[168:171],a[204:207],a[196:199],v[168:171]\n"
LD "v11,s[28:31],s82 offen offset:1024 lds\n"
MF "v[172:175],a[212:215],a[196:199],v[172:175]\n"
TR "a[200:201],v0 offset:32776\n"
MF "v[172:175],a[208:211],a[156:159],v[172:175]\n"
TR "a[202:203],v0 offset:33288\n"
MF "v[164:167],a[208:211],a[152:155],v[164:167]\n"
"ds_read_b128 a[64:67],v2 offset:22528\n"
MF "v[164:167],a[212:215],a[192:195],v[164:167]\n"
TR "a[204:205],v0 offset:36872\n"
MF "v[156:159],a[212:215],a[188:191],v[156:159]\n"
TR "a[206:207],v0 offset:37384\n"
MF "v[156:159],a[208:211],a[148:151],v[156:159]\n"
LD "v12,s[28:31],s82 offen offset:2048 lds\n"
MF "v[148:151],a[208:211],a[144:147],v[148:151]\n"
"ds_read_b128 a[104:107],v3 offset:22528\n"
MF "v[148:151],a[212:215],a[184:187],v[148:151]\n"
MF "v[140:143],a[212:215],a[180:183],v[140:143]\n"
MF "v[140:143],a[208:211],a[140:143],v[140:143]\n"
"s_waitcnt lgkmcnt(7)\n"
LD "v13,s[28:31],s82 offen offset:3072 lds\n"
MF "v[176:179],a[216:219],a[140:143],v[176:179]\n"
TR "a[208:209],v1 offset:32768\n"
MF "v[176:179],a[220:223],a[180:183],v[176:179]\n"
TR "a[210:211],v1 offset:33280\n"
MF "v[184:187],a[220:223],a[184:187],v[184:187]\n"
"ds_read_b128 a[68:71],v2 offset:24576\n"
MF "v[184:187],a[216:219],a[144:147],v[184:187]\n"
"s_add_u32 s84,s14,20480\n"
TR "a[212:213],v1 offset:36864\n"
MF "v[192:195],a[216:219],a[148:151],v[192:195]\n"
"s_add_u32 m0,s63,2048\n"
TR "a[214:215],v1 offset:37376\n"
MF "v[192:195],a[220:223],a[188:191],v[192:195]\n"
LD "v4,s[24:27],s84 offen lds\n"
MF "v[200:203],a[220:223],a[192:195],v[200:203]\n"
"ds_read_b128 a[108:111],v3 offset:24576\n"
MF "v[200:203],a[216:219],a[152:155],v[200:203]\n"
MF "v[208:211],a[216:219],a[156:159],v[208:211]\n"
"s_add_u32 s84,s14,29696\n"
MF "v[208:211],a[220:223],a[196:199],v[208:211]\n"
"s_waitcnt lgkmcnt(7)\n"
LD "v5,s[24:27],s84 offen offset:1024 lds\n"
MF "v[212:215],a[204:207],a[196:199],v[212:215]\n"
TR "a[216:217],v1 offset:32776\n"
MF "v[212:215],a[200:203],a[156:159],v[212:215]\n"
TR "a[218:219],v1 offset:33288\n"
MF "v[204:207],a[200:203],a[152:155],v[204:207]\n"
"ds_read_b128 a[72:75],v2 offset:26624\n"
MF "v[204:207],a[204:207],a[192:195],v[204:207]\n"
TR "a[220:221],v1 offset:36872\n"
MF "v[196:199],a[204:207],a[188:191],v[196:199]\n"
TR "a[222:223],v1 offset:37384\n"
MF "v[196:199],a[200:203],a[148:151],v[196:199]\n"
MF "v[188:191],a[200:203],a[144:147],v[188:191]\n"
"v_add_u32 v0,0xffff4000,v0\n"
MF "v[188:191],a[204:207],a[184:187],v[188:191]\n"
"v_add_u32 v1,0xffff4000,v1\n"
MF "v[180:183],a[204:207],a[180:183],v[180:183]\n"
MF "v[180:183],a[200:203],a[140:143],v[180:183]\n"
"s_waitcnt vmcnt(26) lgkmcnt(0)\n"
"s_barrier\n"
MF "v[216:219],a[208:211],a[140:143],v[216:219]\n"
TR "a[200:201],v0\n"
MF "v[216:219],a[212:215],a[180:183],v[216:219]\n"
"s_add_u32 m0,s62,81920\n"
TR "a[202:203],v0 offset:512\n"
MF "v[224:227],a[212:215],a[184:187],v[224:227]\n"
"s_add_u32 s82,s66,3932416\n"
"ds_read_b128 a[112:115],v3 offset:26624\n"
MF "v[224:227],a[208:211],a[144:147],v[224:227]\n"
"s_sub_u32 s15,s82,7864320\n"
TR "a[204:205],v0 offset:4096\n"
MF "v[232:235],a[208:211],a[148:151],v[232:235]\n"
"s_min_u32 s82,s82,s15\n"
TR "a[206:207],v0 offset:4608\n"
MF "v[232:235],a[212:215],a[188:191],v[232:235]\n"
LD "v6,s[28:31],s82 offen lds\n"
MF "v[240:243],a[212:215],a[192:195],v[240:243]\n"
"ds_read_b128 a[76:79],v2 offset:28672\n"
MF "v[240:243],a[208:211],a[152:155],v[240:243]\n"
"ds_read_b128 a[116:119],v3 offset:28672\n"
MF "v[248:251],a[208:211],a[156:159],v[248:251]\n"
MF "v[248:251],a[212:215],a[196:199],v[248:251]\n"
LD "v11,s[28:31],s82 offen offset:1024 lds\n"
MF "v[252:255],a[220:223],a[196:199],v[252:255]\n"
TR "a[208:209],v0 offset:8\n"
MF "v[252:255],a[216:219],a[156:159],v[252:255]\n"
TR "a[210:211],v0 offset:520\n"
MF "v[244:247],a[216:219],a[152:155],v[244:247]\n"
MF "v[244:247],a[220:223],a[192:195],v[244:247]\n"
TR "a[212:213],v0 offset:4104\n"
MF "v[236:239],a[220:223],a[188:191],v[236:239]\n"
TR "a[214:215],v0 offset:4616\n"
MF "v[236:239],a[216:219],a[148:151],v[236:239]\n"
LD "v12,s[28:31],s82 offen offset:2048 lds\n"
MF "v[228:231],a[216:219],a[144:147],v[228:231]\n"
MF "v[228:231],a[220:223],a[184:187],v[228:231]\n"
MF "v[220:223],a[220:223],a[180:183],v[220:223]\n"
MF "v[220:223],a[216:219],a[140:143],v[220:223]\n"
"s_waitcnt lgkmcnt(4)\n"
LD "v13,s[28:31],s82 offen offset:3072 lds\n"
MF "v[16:19],a[200:203],a[60:63],v[16:19]\n"
TR "a[216:217],v1\n"
MF "v[16:19],a[204:207],a[100:103],v[16:19]\n"
TR "a[218:219],v1 offset:512\n"
MF "v[24:27],a[204:207],a[104:107],v[24:27]\n"
MF "v[24:27],a[200:203],a[64:67],v[24:27]\n"
"s_add_u32 s84,s14,40960\n"
TR "a[220:221],v1 offset:4096\n"
MF "v[32:35],a[200:203],a[68:71],v[32:35]\n"
"s_add_u32 m0,s63,4096\n"
TR "a[222:223],v1 offset:4608\n"
MF "v[32:35],a[204:207],a[108:111],v[32:35]\n"
LD "v4,s[24:27],s84 offen lds\n"
MF "v[40:43],a[204:207],a[112:115],v[40:43]\n"
MF "v[40:43],a[200:203],a[72:75],v[40:43]\n"
MF "v[48:51],a[200:203],a[76:79],v[48:51]\n"
MF "v[48:51],a[204:207],a[116:119],v[48:51]\n"
"s_waitcnt lgkmcnt(4)\n"
MF "v[52:55],a[212:215],a[116:119],v[52:55]\n"
TR "a[200:201],v1 offset:8\n"
MF "v[52:55],a[208:211],a[76:79],v[52:55]\n"
TR "a[202:203],v1 offset:520\n"
MF "v[44:47],a[208:211],a[72:75],v[44:47]\n"
MF "v[44:47],a[212:215],a[112:115],v[44:47]\n"
TR "a[204:205],v1 offset:4104\n"
MF "v[36:39],a[212:215],a[108:111],v[36:39]\n"
TR "a[206:207],v1 offset:4616\n"
MF "v[36:39],a[208:211],a[68:71],v[36:39]\n"
MF "v[28:31],a[208:211],a[64:67],v[28:31]\n"
MF "v[28:31],a[212:215],a[104:107],v[28:31]\n"
MF "v[20:23],a[212:215],a[100:103],v[20:23]\n"
MF "v[20:23],a[208:211],a[60:63],v[20:23]\n"
"s_waitcnt vmcnt(25) lgkmcnt(0)\n"
"s_barrier\n"
MF "v[56:59],a[216:219],a[60:63],v[56:59]\n"
"s_add_u32 m0,s62,0\n"
"s_add_u32 s82,s66,4718592\n"
TR "a[208:209],v0 offset:16384\n"
MF "v[56:59],a[220:223],a[100:103],v[56:59]\n"
"s_sub_u32 s15,s82,7864320\n"
"s_min_u32 s82,s82,s15\n"
TR "a[210:211],v0 offset:16896\n"
MF "v[64:67],a[220:223],a[104:107],v[64:67]\n"
LD "v6,s[28:31],s82 offen lds\n"
MF "v[64:67],a[216:219],a[64:67],v[64:67]\n"
TR "a[212:213],v0 offset:20480\n"
MF "v[72:75],a[216:219],a[68:71],v[72:75]\n"
TR "a[214:215],v0 offset:20992\n"
MF "v[72:75],a[220:223],a[108:111],v[72:75]\n"
"buffer_load_dword a249,v15,s[48:51],s59 offen\n"
MF "v[80:83],a[220:223],a[112:115],v[80:83]\n"
"s_bfe_u32 s59,s23,0x30003\n"
MF "v[80:83],a[216:219],a[72:75],v[80:83]\n"
LD "v11,s[28:31],s82 offen offset:1024 lds\n"
MF "v[88:91],a[216:219],a[76:79],v[88:91]\n"
"s_add_u32 s59,s59,3\n"
MF "v[88:91],a[220:223],a[116:119],v[88:91]\n"
"s_lshl_b32 s59,s59,7\n"
MF "v[92:95],a[204:207],a[116:119],v[92:95]\n"
"s_and_b32 s59,s59,768\n"
TR "a[216:217],v0 offset:16392\n"
MF "v[92:95],a[200:203],a[76:79],v[92:95]\n"
"s_add_u32 s59,s59,s60\n"
TR "a[218:219],v0 offset:16904\n"
MF "v[84:87],a[200:203],a[72:75],v[84:87]\n"
LD "v12,s[28:31],s82 offen offset:2048 lds\n"
MF "v[84:87],a[204:207],a[112:115],v[84:87]\n"
TR "a[220:221],v0 offset:20488\n"
MF "v[76:79],a[204:207],a[108:111],v[76:79]\n"
TR "a[222:223],v0 offset:21000\n"
MF "v[76:79],a[200:203],a[68:71],v[76:79]\n"
MF "v[68:71],a[200:203],a[64:67],v[68:71]\n"
MF "v[68:71],a[204:207],a[104:107],v[68:71]\n"
LD "v13,s[28:31],s82 offen offset:3072 lds\n"
MF "v[60:63],a[204:207],a[100:103],v[60:63]\n"
MF "v[60:63],a[200:203],a[60:63],v[60:63]\n"
"s_add_u32 s14,s81,896\n"
"s_sub_u32 s15,s14,1280\n"
"s_waitcnt lgkmcnt(4)\n"
MF "v[96:99],a[208:211],a[60:63],v[96:99]\n"
"s_min_u32 s14,s14,s15\n"
"s_add_u32 s14,s14,s68\n"
TR "a[200:201],v1 offset:16384\n"
MF "v[96:99],a[212:215],a[100:103],v[96:99]\n"
"s_add_u32 s84,s14,0\n"
"s_add_u32 m0,s63,20480\n"
TR "a[202:203],v1 offset:16896\n"
MF "v[104:107],a[212:215],a[104:107],v[104:107]\n"
LD "v4,s[24:27],s84 offen lds\n"
MF "v[104:107],a[208:211],a[64:67],v[104:107]\n"
TR "a[204:205],v1 offset:20480\n"
MF "v[112:115],a[208:211],a[68:71],v[112:115]\n"
TR "a[206:207],v1 offset:20992\n"
MF "v[112:115],a[212:215],a[108:111],v[112:115]\n"
MF "v[120:123],a[212:215],a[112:115],v[120:123]\n"
"s_add_u32 s84,s14,9216\n"
MF "v[120:123],a[208:211],a[72:75],v[120:123]\n"
LD "v5,s[24:27],s84 offen offset:1024 lds\n"
MF "v[128:131],a[208:211],a[76:79],v[128:131]\n"
MF "v[128:131],a[212:215],a[116:119],v[128:131]\n"
"s_waitcnt lgkmcnt(4)\n"
MF "v[132:135],a[220:223],a[116:119],v[132:135]\n"
TR "a[208:209],v1 offset:16392\n"
MF "v[132:135],a[216:219],a[76:79],v[132:135]\n"
TR "a[210:211],v1 offset:16904\n"
MF "v[124:127],a[216:219],a[72:75],v[124:127]\n"
MF "v[124:127],a[220:223],a[112:115],v[124:127]\n"
TR "a[212:213],v1 offset:20488\n"
MF "v[116:119],a[220:223],a[108:111],v[116:119]\n"
TR "a[214:215],v1 offset:21000\n"
MF "v[116:119],a[216:219],a[68:71],v[116:119]\n"
MF "v[108:111],a[216:219],a[64:67],v[108:111]\n"
MF "v[108:111],a[220:223],a[104:107],v[108:111]\n"
MF "v[100:103],a[220:223],a[100:103],v[100:103]\n"
MF "v[100:103],a[216:219],a[60:63],v[100:103]\n"
"s_waitcnt vmcnt(25) lgkmcnt(0)\n"
"s_barrier\n"
MF "v[136:139],a[200:203],a[60:63],v[136:139]\n"
TR "a[216:217],v0 offset:32768\n"
MF "v[136:139],a[204:207],a[100:103],v[136:139]\n"
"s_add_u32 m0,s62,16384\n"
TR "a[218:219],v0 offset:33280\n"
MF "v[144:147],a[204:207],a[104:107],v[144:147]\n"
"s_add_u32 s82,s66,4718720\n"
"ds_read_b128 a[140:143],v2 offset:40960\n"
MF "v[144:147],a[200:203],a[64:67],v[144:147]\n"
"s_sub_u32 s15,s82,7864320\n"
TR "a[220:221],v0 offset:36864\n"
MF "v[152:155],a[200:203],a[68:71],v[152:155]\n"
"s_min_u32 s82,s82,s15\n"
TR "a[222:223],v0 offset:37376\n"
MF "v[152:155],a[204:207],a[108:111],v[152:155]\n"
LD "v6,s[28:31],s82 offen lds\n"
MF "v[160:163],a[204:207],a[112:115],v[160:163]\n"
"ds_read_b128 a[180:183],v3 offset:40960\n"
MF "v[160:163],a[200:203],a[72:75],v[160:163]\n"
MF "v[168:171],a[200:203],a[76:79],v[168:171]\n"
MF "v[168:171],a[204:207],a[116:119],v[168:171]\n"
LD "v11,s[28:31],s82 offen offset:1024 lds\n"
MF "v[172:175],a[212:215],a[116:119],v[172:175]\n"
TR "a[200:201],v0 offset:32776\n"
MF "v[172:175],a[208:211],a[76:79],v[172:175]\n"
TR "a[202:203],v0 offset:33288\n"
MF "v[164:167],a[208:211],a[72:75],v[164:167]\n"
"ds_read_b128 a[144:147],v2 offset:43008\n"
MF "v[164:167],a[212:215],a[112:115],v[164:167]\n"
TR "a[204:205],v0 offset:36872\n"
MF "v[156:159],a[212:215],a[108:111],v[156:159]\n"
TR "a[206:207],v0 offset:37384\n"
MF "v[156:159],a[208:211],a[68:71],v[156:159]\n"
LD "v12,s[28:31],s82 offen offset:2048 lds\n"
MF "v[148:151],a[208:211],a[64:67],v[148:151]\n"
"ds_read_b128 a[184:187],v3 offset:43008\n"
MF "v[148:151],a[212:215],a[104:107],v[148:151]\n"
MF "v[140:143],a[212:215],a[100:103],v[140:143]\n"
MF "v[140:143],a[208:211],a[60:63],v[140:143]\n"
"s_waitcnt lgkmcnt(7)\n"
LD "v13,s[28:31],s82 offen offset:3072 lds\n"
MF "v[176:179],a[216:219],a[60:63],v[176:179]\n"
TR "a[208:209],v1 offset:32768\n"
MF "v[176:179],a[220:223],a[100:103],v[176:179]\n"
TR "a[210:211],v1 offset:33280\n"
MF "v[184:187],a[220:223],a[104:107],v[184:187]\n"
"ds_read_b128 a[148:151],v2 offset:45056\n"
MF "v[184:187],a[216:219],a[64:67],v[184:187]\n"
"s_add_u32 s84,s14,20480\n"
TR "a[212:213],v1 offset:36864\n"
MF "v[192:195],a[216:219],a[68:71],v[192:195]\n"
"s_add_u32 m0,s63,22528\n"
TR "a[214:215],v1 offset:37376\n"
MF "v[192:195],a[220:223],a[108:111],v[192:195]\n"
LD "v4,s[24:27],s84 offen lds\n"
MF "v[200:203],a[220:223],a[112:115],v[200:203]\n"
"ds_read_b128 a[188:191],v3 offset:45056\n"
MF "v[200:203],a[216:219],a[72:75],v[200:203]\n"
MF "v[208:211],a[216:219],a[76:79],v[208:211]\n"
"s_add_u32 s84,s14,29696\n"
MF "v[208:211],a[220:223],a[116:119],v[208:211]\n"
"s_waitcnt lgkmcnt(7)\n"
LD "v5,s[24:27],s84 offen offset:1024 lds\n"
MF "v[212:215],a[204:207],a[116:119],v[212:215]\n"
TR "a[216:217],v1 offset:32776\n"
MF "v[212:215],a[200:203],a[76:79],v[212:215]\n"
TR "a[218:219],v1 offset:33288\n"
MF "v[204:207],a[200:203],a[72:75],v[204:207]\n"
"ds_read_b128 a[152:155],v2 offset:47104\n"
MF "v[204:207],a[204:207],a[112:115],v[204:207]\n"
TR "a[220:221],v1 offset:36872\n"
MF "v[196:199],a[204:207],a[108:111],v[196:199]\n"
TR "a[222:223],v1 offset:37384\n"
MF "v[196:199],a[200:203],a[68:71],v[196:199]\n"
MF "v[188:191],a[200:203],a[64:67],v[188:191]\n"
"v_add_u32 v0,0xc000,v0\n"
MF "v[188:191],a[204:207],a[104:107],v[188:191]\n"
"v_add_u32 v1,0xc000,v1\n"
MF "v[180:183],a[204:207],a[100:103],v[180:183]\n"
MF "v[180:183],a[200:203],a[60:63],v[180:183]\n"
"s_waitcnt vmcnt(26) lgkmcnt(0)\n"
"s_barrier\n"
MF "v[216:219],a[208:211],a[60:63],v[216:219]\n"
TR "a[200:201],v0\n"
MF "v[216:219],a[212:215],a[100:103],v[216:219]\n"
"s_add_u32 m0,s62,32768\n"
TR "a[202:203],v0 offset:512\n"
MF "v[224:227],a[212:215],a[104:107],v[224:227]\n"
"s_add_u32 s82,s66,4718848\n"
"ds_read_b128 a[192:195],v3 offset:47104\n"
MF "v[224:227],a[208:211],a[64:67],v[224:227]\n"
"s_sub_u32 s15,s82,7864320\n"
TR "a[204:205],v0 offset:4096\n"
MF "v[232:235],a[208:211],a[68:71],v[232:235]\n"
"s_min_u32 s82,s82,s15\n"
TR "a[206:207],v0 offset:4608\n"
MF "v[232:235],a[212:215],a[108:111],v[232:235]\n"
LD "v6,s[28:31],s82 offen lds\n"
MF "v[240:243],a[212:215],a[112:115],v[240:243]\n"
"ds_read_b128 a[156:159],v2 offset:49152\n"
MF "v[240:243],a[208:211],a[72:75],v[240:243]\n"
"ds_read_b128 a[196:199],v3 offset:49152\n"
MF "v[248:251],a[208:211],a[76:79],v[248:251]\n"
MF "v[248:251],a[212:215],a[116:119],v[248:251]\n"
LD "v11,s[28:31],s82 offen offset:1024 lds\n"
MF "v[252:255],a[220:223],a[116:119],v[252:255]\n"
TR "a[208:209],v0 offset:8\n"
MF "v[252:255],a[216:219],a[76:79],v[252:255]\n"
TR "a[210:211],v0 offset:520\n"
MF "v[244:247],a[216:219],a[72:75],v[244:247]\n"
MF "v[244:247],a[220:223],a[112:115],v[244:247]\n"
TR "a[212:213],v0 offset:4104\n"
MF "v[236:239],a[220:223],a[108:111],v[236:239]\n"
TR "a[214:215],v0 offset:4616\n"
MF "v[236:239],a[216:219],a[68:71],v[236:239]\n"
LD "v12,s[28:31],s82 offen offset:2048 lds\n"
MF "v[228:231],a[216:219],a[64:67],v[228:231]\n"
MF "v[228:231],a[220:223],a[104:107],v[228:231]\n"
MF "v[220:223],a[220:223],a[100:103],v[220:223]\n"
MF "v[220:223],a[216:219],a[60:63],v[220:223]\n"
"s_waitcnt lgkmcnt(4)\n"
LD "v13,s[28:31],s82 offen offset:3072 lds\n"
MF "v[16:19],a[200:203],a[140:143],v[16:19]\n"
TR "a[216:217],v1\n"
MF "v[16:19],a[204:207],a[180:183],v[16:19]\n"
TR "a[218:219],v1 offset:512\n"
MF "v[24:27],a[204:207],a[184:187],v[24:27]\n"
MF "v[24:27],a[200:203],a[144:147],v[24:27]\n"
"s_add_u32 s84,s14,40960\n"
TR "a[220:221],v1 offset:4096\n"
MF "v[32:35],a[200:203],a[148:151],v[32:35]\n"
"s_add_u32 m0,s63,24576\n"
TR "a[222:223],v1 offset:4608\n"
MF "v[32:35],a[204:207],a[188:191],v[32:35]\n"
LD "v4,s[24:27],s84 offen lds\n"
MF "v[40:43],a[204:207],a[192:195],v[40:43]\n"
MF "v[40:43],a[200:203],a[152:155],v[40:43]\n"
MF "v[48:51],a[200:203],a[156:159],v[48:51]\n"
MF "v[48:51],a[204:207],a[196:199],v[48:51]\n"
"s_waitcnt lgkmcnt(4)\n"
MF "v[52:55],a[212:215],a[196:199],v[52:55]\n"
TR "a[200:201],v1 offset:8\n"
MF "v[52:55],a[208:211],a[156:159],v[52:55]\n"
TR "a[202:203],v1 offset:520\n"
MF "v[44:47],a[208:211],a[152:155],v[44:47]\n"
MF "v[44:47],a[212:215],a[192:195],v[44:47]\n"
TR "a[204:205],v1 offset:4104\n"
MF "v[36:39],a[212:215],a[188:191],v[36:39]\n"
TR "a[206:207],v1 offset:4616\n"
MF "v[36:39],a[208:211],a[148:151],v[36:39]\n"
MF "v[28:31],a[208:211],a[144:147],v[28:31]\n"
MF "v[28:31],a[212:215],a[184:187],v[28:31]\n"
MF "v[20:23],a[212:215],a[180:183],v[20:23]\n"
MF "v[20:23],a[208:211],a[140:143],v[20:23]\n"
"s_waitcnt vmcnt(25) lgkmcnt(0)\n"
"s_barrier\n"
MF "v[56:59],a[216:219],a[140:143],v[56:59]\n"
"s_add_u32 m0,s62,49152\n"
"s_add_u32 s82,s66,5505024\n"
TR "a[208:209],v0 offset:16384\n"
MF "v[56:59],a[220:223],a[180:183],v[56:59]\n"
"s_sub_u32 s15,s82,7864320\n"
"s_min_u32 s82,s82,s15\n"
TR "a[210:211],v0 offset:16896\n"
MF "v[64:67],a[220:223],a[184:187],v[64:67]\n"
LD "v6,s[28:31],s82 offen lds\n"
MF "v[64:67],a[216:219],a[144:147],v[64:67]\n"
TR "a[212:213],v0 offset:20480\n"
MF "v[72:75],a[216:219],a[148:151],v[72:75]\n"
TR "a[214:215],v0 offset:20992\n"
MF "v[72:75],a[220:223],a[188:191],v[72:75]\n"
"buffer_load_dword a249,v15,s[48:51],s59 offen\n"
MF "v[80:83],a[220:223],a[192:195],v[80:83]\n"
"s_bfe_u32 s59,s23,0x30003\n"
MF "v[80:83],a[216:219],a[152:155],v[80:83]\n"
LD "v11,s[28:31],s82 offen offset:1024 lds\n"
MF "v[88:91],a[216:219],a[156:159],v[88:91]\n"
"s_add_u32 s59,s59,5\n"
MF "v[88:91],a[220:223],a[196:199],v[88:91]\n"
"s_lshl_b32 s59,s59,7\n"
MF "v[92:95],a[204:207],a[196:199],v[92:95]\n"
"s_and_b32 s59,s59,768\n"
TR "a[216:217],v0 offset:16392\n"
MF "v[92:95],a[200:203],a[156:159],v[92:95]\n"
"s_add_u32 s59,s59,s60\n"
TR "a[218:219],v0 offset:16904\n"
MF "v[84:87],a[200:203],a[152:155],v[84:87]\n"
LD "v12,s[28:31],s82 offen offset:2048 lds\n"
MF "v[84:87],a[204:207],a[192:195],v[84:87]\n"
TR "a[220:221],v0 offset:20488\n"
MF "v[76:79],a[204:207],a[188:191],v[76:79]\n"
TR "a[222:223],v0 offset:21000\n"
MF "v[76:79],a[200:203],a[148:151],v[76:79]\n"
MF "v[68:71],a[200:203],a[144:147],v[68:71]\n"
MF "v[68:71],a[204:207],a[184:187],v[68:71]\n"
".Lpg3a:\n"
"s_cbranch_execz .Lisr1\n"
LD "v13,s[28:31],s82 offen offset:3072 lds\n"
MF "v[60:63],a[204:207],a[180:183],v[60:63]\n"
MF "v[60:63],a[200:203],a[140:143],v[60:63]\n"
"s_add_u32 s14,s81,1024\n"
"s_sub_u32 s15,s14,1280\n"
"s_waitcnt lgkmcnt(4)\n"
MF "v[96:99],a[208:211],a[140:143],v[96:99]\n"
"s_min_u32 s14,s14,s15\n"
"s_add_u32 s14,s14,s68\n"
TR "a[200:201],v1 offset:16384\n"
MF "v[96:99],a[212:215],a[180:183],v[96:99]\n"
"s_add_u32 s84,s14,0\n"
"s_add_u32 m0,s63,40960\n"
TR "a[202:203],v1 offset:16896\n"
MF "v[104:107],a[212:215],a[184:187],v[104:107]\n"
LD "v4,s[24:27],s84 offen lds\n"
MF "v[104:107],a[208:211],a[144:147],v[104:107]\n"
TR "a[204:205],v1 offset:20480\n"
MF "v[112:115],a[208:211],a[148:151],v[112:115]\n"
TR "a[206:207],v1 offset:20992\n"
MF "v[112:115],a[212:215],a[188:191],v[112:115]\n"
MF "v[120:123],a[212:215],a[192:195],v[120:123]\n"
"s_add_u32 s84,s14,9216\n"
MF "v[120:123],a[208:211],a[152:155],v[120:123]\n"
LD "v5,s[24:27],s84 offen offset:1024 lds\n"
MF "v[128:131],a[208:211],a[156:159],v[128:131]\n"
MF "v[128:131],a[212:215],a[196:199],v[128:131]\n"
"s_waitcnt lgkmcnt(4)\n"
MF "v[132:135],a[220:223],a[196:199],v[132:135]\n"
TR "a[208:209],v1 offset:16392\n"
MF "v[132:135],a[216:219],a[156:159],v[132:135]\n"
TR "a[210:211],v1 offset:16904\n"
MF "v[124:127],a[216:219],a[152:155],v[124:127]\n"
MF "v[124:127],a[220:223],a[192:195],v[124:127]\n"
TR "a[212:213],v1 offset:20488\n"
MF "v[116:119],a[220:223],a[188:191],v[116:119]\n"
TR "a[214:215],v1 offset:21000\n"
MF "v[116:119],a[216:219],a[148:151],v[116:119]\n"
MF "v[108:111],a[216:219],a[144:147],v[108:111]\n"
MF "v[108:111],a[220:223],a[184:187],v[108:111]\n"
MF "v[100:103],a[220:223],a[180:183],v[100:103]\n"
MF "v[100:103],a[216:219],a[140:143],v[100:103]\n"
"s_waitcnt vmcnt(25) lgkmcnt(0)\n"
"s_barrier\n"
MF "v[136:139],a[200:203],a[140:143],v[136:139]\n"
TR "a[216:217],v0 offset:32768\n"
MF "v[136:139],a[204:207],a[180:183],v[136:139]\n"
"s_add_u32 m0,s62,65536\n"
TR "a[218:219],v0 offset:33280\n"
MF "v[144:147],a[204:207],a[184:187],v[144:147]\n"
"s_add_u32 s82,s66,5505152\n"
"ds_read_b128 a[60:63],v2\n"
MF "v[144:147],a[200:203],a[144:147],v[144:147]\n"
"s_sub_u32 s15,s82,7864320\n"
TR "a[220:221],v0 offset:36864\n"
MF "v[152:155],a[200:203],a[148:151],v[152:155]\n"
"s_min_u32 s82,s82,s15\n"
TR "a[222:223],v0 offset:37376\n"
MF "v[152:155],a[204:207],a[188:191],v[152:155]\n"
LD "v6,s[28:31],s82 offen lds\n"
MF "v[160:163],a[204:207],a[192:195],v[160:163]\n"
"ds_read_b128 a[100:103],v3\n"
MF "v[160:163],a[200:203],a[152:155],v[160:163]\n"
MF "v[168:171],a[200:203],a[156:159],v[168:171]\n"
MF "v[168:171],a[204:207],a[196:199],v[168:171]\n"
LD "v11,s[28:31],s82 offen offset:1024 lds\n"
MF "v[172:175],a[212:215],a[196:199],v[172:175]\n"
TR "a[200:201],v0 offset:32776\n"
MF "v[172:175],a[208:211],a[156:159],v[172:175]\n"
TR "a[202:203],v0 offset:33288\n"
MF "v[164:167],a[208:211],a[152:155],v[164:167]\n"
"ds_read_b128 a[64:67],v2 offset:2048\n"
MF "v[164:167],a[212:215],a[192:195],v[164:167]\n"
TR "a[204:205],v0 offset:36872\n"
MF "v[156:159],a[212:215],a[188:191],v[156:159]\n"
TR "a[206:207],v0 offset:37384\n"
MF "v[156:159],a[208:211],a[148:151],v[156:159]\n"
LD "v12,s[28:31],s82 offen offset:2048 lds\n"
MF "v[148:151],a[208:211],a[144:147],v[148:151]\n"
"ds_read_b128 a[104:107],v3 offset:2048\n"
MF "v[148:151],a[212:215],a[184:187],v[148:151]\n"
MF "v[140:143],a[212:215],a[180:183],v[140:143]\n"
MF "v[140:143],a[208:211],a[140:143],v[140:143]\n"
"s_waitcnt lgkmcnt(7)\n"
LD "v13,s[28:31],s82 offen offset:3072 lds\n"
MF "v[176:179],a[216:219],a[140:143],v[176:179]\n"
TR "a[208:209],v1 offset:32768\n"
MF "v[176:179],a[220:223],a[180:183],v[176:179]\n"
TR "a[210:211],v1 offset:33280\n"
MF "v[184:187],a[220:223],a[184:187],v[184:187]\n"
"ds_read_b128 a[68:71],v2 offset:4096\n"
MF "v[184:187],a[216:219],a[144:147],v[184:187]\n"
"s_add_u32 s84,s14,20480\n"
TR "a[212:213],v1 offset:36864\n"
MF "v[192:195],a[216:219],a[148:151],v[192:195]\n"
"s_add_u32 m0,s63,43008\n"
TR "a[214:215],v1 offset:37376\n"
MF "v[192:195],a[220:223],a[188:191],v[192:195]\n"
LD "v4,s[24:27],s84 offen lds\n"
MF "v[200:203],a[220:223],a[192:195],v[200:203]\n"
"ds_read_b128 a[108:111],v3 offset:4096\n"
MF "v[200:203],a[216:219],a[152:155],v[200:203]\n"
MF "v[208:211],a[216:219],a[156:159],v[208:211]\n"
"s_add_u32 s84,s14,29696\n"
MF "v[208:211],a[220:223],a[196:199],v[208:211]\n"
"s_waitcnt lgkmcnt(7)\n"
LD "v5,s[24:27],s84 offen offset:1024 lds\n"
MF "v[212:215],a[204:207],a[196:199],v[212:215]\n"
TR "a[216:217],v1 offset:32776\n"
MF "v[212:215],a[200:203],a[156:159],v[212:215]\n"
TR "a[218:219],v1 offset:33288\n"
MF "v[204:207],a[200:203],a[152:155],v[204:207]\n"
"ds_read_b128 a[72:75],v2 offset:6144\n"
MF "v[204:207],a[204:207],a[192:195],v[204:207]\n"
TR "a[220:221],v1 offset:36872\n"
MF "v[196:199],a[204:207],a[188:191],v[196:199]\n"
TR "a[222:223],v1 offset:37384\n"
MF "v[196:199],a[200:203],a[148:151],v[196:199]\n"
MF "v[188:191],a[200:203],a[144:147],v[188:191]\n"
"v_add_u32 v0,0xffff4000,v0\n"
MF "v[188:191],a[204:207],a[184:187],v[188:191]\n"
"v_add_u32 v1,0xffff4000,v1\n"
MF "v[180:183],a[204:207],a[180:183],v[180:183]\n"
MF "v[180:183],a[200:203],a[140:143],v[180:183]\n"
"s_waitcnt vmcnt(26) lgkmcnt(0)\n"
"s_barrier\n"
MF "v[216:219],a[208:211],a[140:143],v[216:219]\n"
TR "a[200:201],v0\n"
MF "v[216:219],a[212:215],a[180:183],v[216:219]\n"
"s_add_u32 m0,s62,81920\n"
TR "a[202:203],v0 offset:512\n"
MF "v[224:227],a[212:215],a[184:187],v[224:227]\n"
"s_add_u32 s82,s66,5505280\n"
"ds_read_b128 a[112:115],v3 offset:6144\n"
MF "v[224:227],a[208:211],a[144:147],v[224:227]\n"
"s_sub_u32 s15,s82,7864320\n"
TR "a[204:205],v0 offset:4096\n"
MF "v[232:235],a[208:211],a[148:151],v[232:235]\n"
"s_min_u32 s82,s82,s15\n"
TR "a[206:207],v0 offset:4608\n"
MF "v[232:235],a[212:215],a[188:191],v[232:235]\n"
LD "v6,s[28:31],s82 offen lds\n"
MF "v[240:243],a[212:215],a[192:195],v[240:243]\n"
"ds_read_b128 a[76:79],v2 offset:8192\n"
MF "v[240:243],a[208:211],a[152:155],v[240:243]\n"
"ds_read_b128 a[116:119],v3 offset:8192\n"
MF "v[248:251],a[208:211],a[156:159],v[248:251]\n"
MF "v[248:251],a[212:215],a[196:199],v[248:251]\n"
LD "v11,s[28:31],s82 offen offset:1024 lds\n"
MF "v[252:255],a[220:223],a[196:199],v[252:255]\n"
TR "a[208:209],v0 offset:8\n"
MF "v[252:255],a[216:219],a[156:159],v[252:255]\n"
TR "a[210:211],v0 offset:520\n"
MF "v[244:247],a[216:219],a[152:155],v[244:247]\n"
MF "v[244:247],a[220:223],a[192:195],v[244:247]\n"
TR "a[212:213],v0 offset:4104\n"
MF "v[236:239],a[220:223],a[188:191],v[236:239]\n"
TR "a[214:215],v0 offset:4616\n"
MF "v[236:239],a[216:219],a[148:151],v[236:239]\n"
LD "v12,s[28:31],s82 offen offset:2048 lds\n"
MF "v[228:231],a[216:219],a[144:147],v[228:231]\n"
MF "v[228:231],a[220:223],a[184:187],v[228:231]\n"
MF "v[220:223],a[220:223],a[180:183],v[220:223]\n"
MF "v[220:223],a[216:219],a[140:143],v[220:223]\n"
"s_waitcnt lgkmcnt(4)\n"
LD "v13,s[28:31],s82 offen offset:3072 lds\n"
MF "v[16:19],a[200:203],a[60:63],v[16:19]\n"
TR "a[216:217],v1\n"
MF "v[16:19],a[204:207],a[100:103],v[16:19]\n"
TR "a[218:219],v1 offset:512\n"
MF "v[24:27],a[204:207],a[104:107],v[24:27]\n"
MF "v[24:27],a[200:203],a[64:67],v[24:27]\n"
"s_add_u32 s84,s14,40960\n"
TR "a[220:221],v1 offset:4096\n"
MF "v[32:35],a[200:203],a[68:71],v[32:35]\n"
"s_add_u32 m0,s63,45056\n"
TR "a[222:223],v1 offset:4608\n"
MF "v[32:35],a[204:207],a[108:111],v[32:35]\n"
LD "v4,s[24:27],s84 offen lds\n"
MF "v[40:43],a[204:207],a[112:115],v[40:43]\n"
MF "v[40:43],a[200:203],a[72:75],v[40:43]\n"
MF "v[48:51],a[200:203],a[76:79],v[48:51]\n"
MF "v[48:51],a[204:207],a[116:119],v[48:51]\n"
"s_waitcnt lgkmcnt(4)\n"
MF "v[52:55],a[212:215],a[116:119],v[52:55]\n"
TR "a[200:201],v1 offset:8\n"
MF "v[52:55],a[208:211],a[76:79],v[52:55]\n"
TR "a[202:203],v1 offset:520\n"
MF "v[44:47],a[208:211],a[72:75],v[44:47]\n"
MF "v[44:47],a[212:215],a[112:115],v[44:47]\n"
TR "a[204:205],v1 offset:4104\n"
MF "v[36:39],a[212:215],a[108:111],v[36:39]\n"
TR "a[206:207],v1 offset:4616\n"
MF "v[36:39],a[208:211],a[68:71],v[36:39]\n"
MF "v[28:31],a[208:211],a[64:67],v[28:31]\n"
MF "v[28:31],a[212:215],a[104:107],v[28:31]\n"
MF "v[20:23],a[212:215],a[100:103],v[20:23]\n"
MF "v[20:23],a[208:211],a[60:63],v[20:23]\n"
"s_waitcnt vmcnt(25) lgkmcnt(0)\n"
"s_barrier\n"
MF "v[56:59],a[216:219],a[60:63],v[56:59]\n"
"s_add_u32 m0,s62,0\n"
"s_add_u32 s82,s66,6291712\n"
TR "a[208:209],v0 offset:16384\n"
MF "v[56:59],a[220:223],a[100:103],v[56:59]\n"
"s_sub_u32 s15,s82,7864320\n"
"s_min_u32 s82,s82,s15\n"
TR "a[210:211],v0 offset:16896\n"
MF "v[64:67],a[220:223],a[104:107],v[64:67]\n"
LD "v6,s[28:31],s82 offen lds\n"
MF "v[64:67],a[216:219],a[64:67],v[64:67]\n"
TR "a[212:213],v0 offset:20480\n"
MF "v[72:75],a[216:219],a[68:71],v[72:75]\n"
TR "a[214:215],v0 offset:20992\n"
MF "v[72:75],a[220:223],a[108:111],v[72:75]\n"
"buffer_load_dword a249,v15,s[48:51],s59 offen\n"
MF "v[80:83],a[220:223],a[112:115],v[80:83]\n"
MF "v[80:83],a[216:219],a[72:75],v[80:83]\n"
LD "v11,s[28:31],s82 offen offset:1024 lds\n"
MF "v[88:91],a[216:219],a[76:79],v[88:91]\n"
MF "v[88:91],a[220:223],a[116:119],v[88:91]\n"
MF "v[92:95],a[204:207],a[116:119],v[92:95]\n"
TR "a[216:217],v0 offset:16392\n"
MF "v[92:95],a[200:203],a[76:79],v[92:95]\n"
TR "a[218:219],v0 offset:16904\n"
MF "v[84:87],a[200:203],a[72:75],v[84:87]\n"
LD "v12,s[28:31],s82 offen offset:2048 lds\n"
MF "v[84:87],a[204:207],a[112:115],v[84:87]\n"
TR "a[220:221],v0 offset:20488\n"
MF "v[76:79],a[204:207],a[108:111],v[76:79]\n"
TR "a[222:223],v0 offset:21000\n"
MF "v[76:79],a[200:203],a[68:71],v[76:79]\n"
MF "v[68:71],a[200:203],a[64:67],v[68:71]\n"
MF "v[68:71],a[204:207],a[104:107],v[68:71]\n"
LD "v13,s[28:31],s82 offen offset:3072 lds\n"
MF "v[60:63],a[204:207],a[100:103],v[60:63]\n"
MF "v[60:63],a[200:203],a[60:63],v[60:63]\n"
"s_add_u32 s14,s81,1152\n"
"s_sub_u32 s15,s14,1280\n"
"s_waitcnt lgkmcnt(4)\n"
MF "v[96:99],a[208:211],a[60:63],v[96:99]\n"
"s_min_u32 s14,s14,s15\n"
"s_add_u32 s14,s14,s68\n"
TR "a[200:201],v1 offset:16384\n"
MF "v[96:99],a[212:215],a[100:103],v[96:99]\n"
"s_add_u32 s84,s14,0\n"
"s_add_u32 m0,s63,0\n"
TR "a[202:203],v1 offset:16896\n"
MF "v[104:107],a[212:215],a[104:107],v[104:107]\n"
LD "v4,s[24:27],s84 offen lds\n"
MF "v[104:107],a[208:211],a[64:67],v[104:107]\n"
TR "a[204:205],v1 offset:20480\n"
MF "v[112:115],a[208:211],a[68:71],v[112:115]\n"
TR "a[206:207],v1 offset:20992\n"
MF "v[112:115],a[212:215],a[108:111],v[112:115]\n"
MF "v[120:123],a[212:215],a[112:115],v[120:123]\n"
"s_add_u32 s84,s14,9216\n"
MF "v[120:123],a[208:211],a[72:75],v[120:123]\n"
LD "v5,s[24:27],s84 offen offset:1024 lds\n"
MF "v[128:131],a[208:211],a[76:79],v[128:131]\n"
MF "v[128:131],a[212:215],a[116:119],v[128:131]\n"
"s_waitcnt lgkmcnt(4)\n"
MF "v[132:135],a[220:223],a[116:119],v[132:135]\n"
TR "a[208:209],v1 offset:16392\n"
MF "v[132:135],a[216:219],a[76:79],v[132:135]\n"
"s_add_u32 s84,s14,18432\n"
TR "a[210:211],v1 offset:16904\n"
MF "v[124:127],a[216:219],a[72:75],v[124:127]\n"
LD "v4,s[24:27],s84 offen offset:2048 lds\n"
MF "v[124:127],a[220:223],a[112:115],v[124:127]\n"
TR "a[212:213],v1 offset:20488\n"
MF "v[116:119],a[220:223],a[108:111],v[116:119]\n"
TR "a[214:215],v1 offset:21000\n"
MF "v[116:119],a[216:219],a[68:71],v[116:119]\n"
MF "v[108:111],a[216:219],a[64:67],v[108:111]\n"
MF "v[108:111],a[220:223],a[104:107],v[108:111]\n"
MF "v[100:103],a[220:223],a[100:103],v[100:103]\n"
MF "v[100:103],a[216:219],a[60:63],v[100:103]\n"
"s_waitcnt vmcnt(26) lgkmcnt(0)\n"
"s_barrier\n"
MF "v[136:139],a[200:203],a[60:63],v[136:139]\n"
TR "a[216:217],v0 offset:32768\n"
MF "v[136:139],a[204:207],a[100:103],v[136:139]\n"
"s_add_u32 m0,s62,16384\n"
TR "a[218:219],v0 offset:33280\n"
MF "v[144:147],a[204:207],a[104:107],v[144:147]\n"
"s_add_u32 s82,s66,7078144\n"
"ds_read_b128 a[140:143],v2 offset:20480\n"
MF "v[144:147],a[200:203],a[64:67],v[144:147]\n"
"s_sub_u32 s15,s82,7864320\n"
TR "a[220:221],v0 offset:36864\n"
MF "v[152:155],a[200:203],a[68:71],v[152:155]\n"
"s_min_u32 s82,s82,s15\n"
TR "a[222:223],v0 offset:37376\n"
MF "v[152:155],a[204:207],a[108:111],v[152:155]\n"
LD "v6,s[28:31],s82 offen lds\n"
MF "v[160:163],a[204:207],a[112:115],v[160:163]\n"
"ds_read_b128 a[180:183],v3 offset:20480\n"
MF "v[160:163],a[200:203],a[72:75],v[160:163]\n"
MF "v[168:171],a[200:203],a[76:79],v[168:171]\n"
MF "v[168:171],a[204:207],a[116:119],v[168:171]\n"
LD "v11,s[28:31],s82 offen offset:1024 lds\n"
MF "v[172:175],a[212:215],a[116:119],v[172:175]\n"
TR "a[200:201],v0 offset:32776\n"
MF "v[172:175],a[208:211],a[76:79],v[172:175]\n"
TR "a[202:203],v0 offset:33288\n"
MF "v[164:167],a[208:211],a[72:75],v[164:167]\n"
"ds_read_b128 a[144:147],v2 offset:22528\n"
MF "v[164:167],a[212:215],a[112:115],v[164:167]\n"
TR "a[204:205],v0 offset:36872\n"
MF "v[156:159],a[212:215],a[108:111],v[156:159]\n"
TR "a[206:207],v0 offset:37384\n"
MF "v[156:159],a[208:211],a[68:71],v[156:159]\n"
LD "v12,s[28:31],s82 offen offset:2048 lds\n"
MF "v[148:151],a[208:211],a[64:67],v[148:151]\n"
"ds_read_b128 a[184:187],v3 offset:22528\n"
MF "v[148:151],a[212:215],a[104:107],v[148:151]\n"
MF "v[140:143],a[212:215],a[100:103],v[140:143]\n"
MF "v[140:143],a[208:211],a[60:63],v[140:143]\n"
"s_waitcnt lgkmcnt(7)\n"
LD "v13,s[28:31],s82 offen offset:3072 lds\n"
MF "v[176:179],a[216:219],a[60:63],v[176:179]\n"
TR "a[208:209],v1 offset:32768\n"
MF "v[176:179],a[220:223],a[100:103],v[176:179]\n"
TR "a[210:211],v1 offset:33280\n"
MF "v[184:187],a[220:223],a[104:107],v[184:187]\n"
"ds_read_b128 a[148:151],v2 offset:24576\n"
MF "v[184:187],a[216:219],a[64:67],v[184:187]\n"
"s_add_u32 s84,s14,30720\n"
TR "a[212:213],v1 offset:36864\n"
MF "v[192:195],a[216:219],a[68:71],v[192:195]\n"
"s_add_u32 m0,s63,3072\n"
TR "a[214:215],v1 offset:37376\n"
MF "v[192:195],a[220:223],a[108:111],v[192:195]\n"
LD "v5,s[24:27],s84 offen lds\n"
MF "v[200:203],a[220:223],a[112:115],v[200:203]\n"
"ds_read_b128 a[188:191],v3 offset:24576\n"
MF "v[200:203],a[216:219],a[72:75],v[200:203]\n"
MF "v[208:211],a[216:219],a[76:79],v[208:211]\n"
"s_add_u32 s84,s14,39936\n"
MF "v[208:211],a[220:223],a[116:119],v[208:211]\n"
"s_waitcnt lgkmcnt(7)\n"
LD "v4,s[24:27],s84 offen offset:1024 lds\n"
MF "v[212:215],a[204:207],a[116:119],v[212:215]\n"
TR "a[216:217],v1 offset:32776\n"
MF "v[212:215],a[200:203],a[76:79],v[212:215]\n"
TR "a[218:219],v1 offset:33288\n"
MF "v[204:207],a[200:203],a[72:75],v[204:207]\n"
"ds_read_b128 a[152:155],v2 offset:26624\n"
MF "v[204:207],a[204:207],a[112:115],v[204:207]\n"
TR "a[220:221],v1 offset:36872\n"
MF "v[196:199],a[204:207],a[108:111],v[196:199]\n"
TR "a[222:223],v1 offset:37384\n"
MF "v[196:199],a[200:203],a[68:71],v[196:199]\n"
MF "v[188:191],a[200:203],a[64:67],v[188:191]\n"
"v_add_u32 v0,0xc000,v0\n"
MF "v[188:191],a[204:207],a[104:107],v[188:191]\n"
"v_add_u32 v1,0xc000,v1\n"
MF "v[180:183],a[204:207],a[100:103],v[180:183]\n"
"s_branch .Lqj\n"
".Lpgq0_c:\n"
"s_cbranch_execz .Lpgqy_c\n"
".Lpgq0_f:\n"
"s_cbranch_execz .Lpg12f\n"
".Lq0:\n"
MF "v[16:19],a[200:203],a[60:63],0\n"
"s_add_u32 m0,s62,49152\n"
"s_add_u32 s82,s66,786432\n"
TR "a[216:217],v1\n"
MF "v[16:19],a[204:207],a[100:103],v[16:19]\n"
"s_sub_u32 s15,s82,7864320\n"
"s_min_u32 s82,s82,s15\n"
TR "a[218:219],v1 offset:512\n"
MF "v[24:27],a[204:207],a[104:107],0\n"
LD "v6,s[28:31],s82 offen lds\n"
MF "v[24:27],a[200:203],a[64:67],v[24:27]\n"
TR "a[220:221],v1 offset:4096\n"
MF "v[32:35],a[200:203],a[68:71],0\n"
TR "a[222:223],v1 offset:4608\n"
MF "v[32:35],a[204:207],a[108:111],v[32:35]\n"
"v_max3_f32 v220,|v16|,|v17|,|v18|\n"
"v_max_f32_e64 v220,v220,|v19|\n"
MF "v[40:43],a[204:207],a[112:115],0\n"
"v_readlane_b32 s12,v220,3\n"
"v_readlane_b32 s6,v220,22\n"
MF "v[40:43],a[200:203],a[72:75],v[40:43]\n"
"s_min_u32 s12,s12,s6\n"
"v_readlane_b32 s7,v220,41\n"
"v_readlane_b32 s8,v220,60\n"
MF "v[48:51],a[200:203],a[76:79],0\n"
"s_min_u32 s7,s7,s8\n"
MF "v[48:51],a[204:207],a[116:119],v[48:51]\n"
LD "v11,s[28:31],s82 offen offset:1024 lds\n"
MF "v[52:55],a[212:215],a[116:119],0\n"
"s_min_u32 s12,s12,s7\n"
TR "a[200:201],v1 offset:8\n"
MF "v[52:55],a[208:211],a[76:79],v[52:55]\n"
"s_and_b32 s12,s12,0x7f800000\n"
TR "a[202:203],v1 offset:520\n"
MF "v[44:47],a[208:211],a[72:75],0\n"
"s_sub_u32 s12,s12,0x14000000\n"
MF "v[44:47],a[212:215],a[112:115],v[44:47]\n"
"s_cmp_lt_u32 s12,0x50000000\n"
"s_cselect_b32 s12,s12,0xe8400000\n"
TR "a[204:205],v1 offset:4104\n"
MF "v[36:39],a[212:215],a[108:111],0\n"
"s_add_u32 s12,s12,0x17c00000\n"
TR "a[206:207],v1 offset:4616\n"
MF "v[36:39],a[208:211],a[68:71],v[36:39]\n"
"v_mov_b32 v220,s12\n"
"v_mov_b32 v221,s12\n"
MF "v[28:31],a[208:211],a[64:67],0\n"
"v_mov_b32 v222,s12\n"
"v_mov_b32 v223,s12\n"
MF "v[28:31],a[212:215],a[104:107],v[28:31]\n"
LD "v12,s[28:31],s82 offen offset:2048 lds\n"
MF "v[20:23],a[212:215],a[100:103],0\n"
MF "v[20:23],a[208:211],a[60:63],v[20:23]\n"
"s_waitcnt vmcnt(7) lgkmcnt(0)\n"
"s_barrier\n"
MF "v[56:59],a[216:219],a[60:63],v[220:223]\n"
TR "a[208:209],v0 offset:16384\n"
MF "v[56:59],a[220:223],a[100:103],v[56:59]\n"
TR "a[210:211],v0 offset:16896\n"
MF "v[64:67],a[220:223],a[104:107],v[220:223]\n"
LD "v13,s[28:31],s82 offen offset:3072 lds\n"
MF "v[64:67],a[216:219],a[64:67],v[64:67]\n"
"s_add_u32 m0,s62,65536\n"
TR "a[212:213],v0 offset:20480\n"
MF "v[72:75],a[216:219],a[68:71],v[220:223]\n"
"s_add_u32 s82,s66,786560\n"
TR "a[214:215],v0 offset:20992\n"
MF "v[72:75],a[220:223],a[108:111],v[72:75]\n"
"s_sub_u32 s15,s82,7864320\n"
MF "v[80:83],a[220:223],a[112:115],v[220:223]\n"
"s_min_u32 s82,s82,s15\n"
MF "v[80:83],a[216:219],a[72:75],v[80:83]\n"
LD "v6,s[28:31],s82 offen lds\n"
MF "v[88:91],a[216:219],a[76:79],v[220:223]\n"
MF "v[88:91],a[220:223],a[116:119],v[88:91]\n"
MF "v[92:95],a[204:207],a[116:119],v[220:223]\n"
TR "a[216:217],v0 offset:16392\n"
MF "v[92:95],a[200:203],a[76:79],v[92:95]\n"
TR "a[218:219],v0 offset:16904\n"
MF "v[84:87],a[200:203],a[72:75],v[220:223]\n"
LD "v11,s[28:31],s82 offen offset:1024 lds\n"
MF "v[84:87],a[204:207],a[112:115],v[84:87]\n"
TR "a[220:221],v0 offset:20488\n"
MF "v[76:79],a[204:207],a[108:111],v[220:223]\n"
TR "a[222:223],v0 offset:21000\n"
MF "v[76:79],a[200:203],a[68:71],v[76:79]\n"
MF "v[68:71],a[200:203],a[64:67],v[220:223]\n"
MF "v[68:71],a[204:207],a[104:107],v[68:71]\n"
LD "v12,s[28:31],s82 offen offset:2048 lds\n"
MF "v[60:63],a[204:207],a[100:103],v[220:223]\n"
MF "v[60:63],a[200:203],a[60:63],v[60:63]\n"
"s_waitcnt lgkmcnt(4)\n"
MF "v[96:99],a[208:211],a[60:63],v[220:223]\n"
TR "a[200:201],v1 offset:16384\n"
MF "v[96:99],a[212:215],a[100:103],v[96:99]\n"
TR "a[202:203],v1 offset:16896\n"
MF "v[104:107],a[212:215],a[104:107],v[220:223]\n"
LD "v13,s[28:31],s82 offen offset:3072 lds\n"
MF "v[104:107],a[208:211],a[64:67],v[104:107]\n"
"s_add_u32 m0,s62,81920\n"
TR "a[204:205],v1 offset:20480\n"
MF "v[112:115],a[208:211],a[68:71],v[220:223]\n"
"s_add_u32 s82,s66,786688\n"
TR "a[206:207],v1 offset:20992\n"
MF "v[112:115],a[212:215],a[108:111],v[112:115]\n"
"s_sub_u32 s15,s82,7864320\n"
MF "v[120:123],a[212:215],a[112:115],v[220:223]\n"
"s_min_u32 s82,s82,s15\n"
MF "v[120:123],a[208:211],a[72:75],v[120:123]\n"
LD "v6,s[28:31],s82 offen lds\n"
MF "v[128:131],a[208:211],a[76:79],v[220:223]\n"
MF "v[128:131],a[212:215],a[116:119],v[128:131]\n"
"s_waitcnt lgkmcnt(4)\n"
MF "v[132:135],a[220:223],a[116:119],v[220:223]\n"
TR "a[208:209],v1 offset:16392\n"
MF "v[132:135],a[216:219],a[76:79],v[132:135]\n"
TR "a[210:211],v1 offset:16904\n"
MF "v[124:127],a[216:219],a[72:75],v[220:223]\n"
LD "v11,s[28:31],s82 offen offset:1024 lds\n"
MF "v[124:127],a[220:223],a[112:115],v[124:127]\n"
TR "a[212:213],v1 offset:20488\n"
MF "v[116:119],a[220:223],a[108:111],v[220:223]\n"
TR "a[214:215],v1 offset:21000\n"
MF "v[116:119],a[216:219],a[68:71],v[116:119]\n"
MF "v[108:111],a[216:219],a[64:67],v[220:223]\n"
MF "v[108:111],a[220:223],a[104:107],v[108:111]\n"
LD "v12,s[28:31],s82 offen offset:2048 lds\n"
MF "v[100:103],a[220:223],a[100:103],v[220:223]\n"
MF "v[100:103],a[216:219],a[60:63],v[100:103]\n"
"s_waitcnt vmcnt(11) lgkmcnt(0)\n"
"s_barrier\n"
MF "v[136:139],a[200:203],a[60:63],v[220:223]\n"
TR "a[216:217],v0 offset:32768\n"
MF "v[136:139],a[204:207],a[100:103],v[136:139]\n"
TR "a[218:219],v0 offset:33280\n"
MF "v[144:147],a[204:207],a[104:107],v[220:223]\n"
"ds_read_b128 a[140:143],v2 offset:20480\n"
MF "v[144:147],a[200:203],a[64:67],v[144:147]\n"
TR "a[220:221],v0 offset:36864\n"
MF "v[152:155],a[200:203],a[68:71],v[220:223]\n"
TR "a[222:223],v0 offset:37376\n"
MF "v[152:155],a[204:207],a[108:111],v[152:155]\n"
LD "v13,s[28:31],s82 offen offset:3072 lds\n"
MF "v[160:163],a[204:207],a[112:115],v[220:223]\n"
"ds_read_b128 a[180:183],v3 offset:20480\n"
MF "v[160:163],a[200:203],a[72:75],v[160:163]\n"
"s_add_u32 m0,s62,0\n"
MF "v[168:171],a[200:203],a[76:79],v[220:223]\n"
"s_add_u32 s82,s66,1572864\n"
"s_sub_u32 s15,s82,7864320\n"
"s_min_u32 s82,s82,s15\n"
MF "v[168:171],a[204:207],a[116:119],v[168:171]\n"
LD "v6,s[28:31],s82 offen lds\n"
MF "v[172:175],a[212:215],a[116:119],v[220:223]\n"
TR "a[200:201],v0 offset:32776\n"
MF "v[172:175],a[208:211],a[76:79],v[172:175]\n"
TR "a[202:203],v0 offset:33288\n"
MF "v[164:167],a[208:211],a[72:75],v[220:223]\n"
"ds_read_b128 a[144:147],v2 offset:22528\n"
MF "v[164:167],a[212:215],a[112:115],v[164:167]\n"
TR "a[204:205],v0 offset:36872\n"
MF "v[156:159],a[212:215],a[108:111],v[220:223]\n"
TR "a[206:207],v0 offset:37384\n"
MF "v[156:159],a[208:211],a[68:71],v[156:159]\n"
LD "v11,s[28:31],s82 offen offset:1024 lds\n"
MF "v[148:151],a[208:211],a[64:67],v[220:223]\n"
"ds_read_b128 a[184:187],v3 offset:22528\n"
MF "v[148:151],a[212:215],a[104:107],v[148:151]\n"
MF "v[140:143],a[212:215],a[100:103],v[220:223]\n"
MF "v[140:143],a[208:211],a[60:63],v[140:143]\n"
"s_waitcnt lgkmcnt(7)\n"
LD "v12,s[28:31],s82 offen offset:2048 lds\n"
MF "v[176:179],a[216:219],a[60:63],v[220:223]\n"
TR "a[208:209],v1 offset:32768\n"
MF "v[176:179],a[220:223],a[100:103],v[176:179]\n"
TR "a[210:211],v1 offset:33280\n"
MF "v[184:187],a[220:223],a[104:107],v[220:223]\n"
".Lpg4c:\n"
"s_cbranch_execz .Lpg30c\n"
".Lpg4f:\n"
"s_cbranch_execz .LpgattB_f\n"
"ds_read_b128 a[148:151],v2 offset:24576\n"
MF "v[184:187],a[216:219],a[64:67],v[184:187]\n"
TR "a[212:213],v1 offset:36864\n"
MF "v[192:195],a[216:219],a[68:71],v[220:223]\n"
TR "a[214:215],v1 offset:37376\n"
MF "v[192:195],a[220:223],a[108:111],v[192:195]\n"
LD "v13,s[28:31],s82 offen offset:3072 lds\n"
MF "v[200:203],a[220:223],a[112:115],v[220:223]\n"
"ds_read_b128 a[188:191],v3 offset:24576\n"
MF "v[200:203],a[216:219],a[72:75],v[200:203]\n"
"s_add_u32 s14,s81,384\n"
"s_sub_u32 s15,s14,1280\n"
"s_min_u32 s14,s14,s15\n"
MF "v[208:211],a[216:219],a[76:79],v[220:223]\n"
"s_add_u32 s14,s14,s68\n"
"s_add_u32 s84,s14,0\n"
"s_add_u32 m0,s63,0\n"
MF "v[208:211],a[220:223],a[116:119],v[208:211]\n"
"s_waitcnt lgkmcnt(7)\n"
LD "v4,s[24:27],s84 offen lds\n"
MF "v[212:215],a[204:207],a[116:119],v[220:223]\n"
TR "a[216:217],v1 offset:32776\n"
MF "v[212:215],a[200:203],a[76:79],v[212:215]\n"
TR "a[218:219],v1 offset:33288\n"
MF "v[204:207],a[200:203],a[72:75],v[220:223]\n"
"ds_read_b128 a[152:155],v2 offset:26624\n"
MF "v[204:207],a[204:207],a[112:115],v[204:207]\n"
TR "a[220:221],v1 offset:36872\n"
MF "v[196:199],a[204:207],a[108:111],v[220:223]\n"
"s_add_u32 s84,s14,9216\n"
TR "a[222:223],v1 offset:37384\n"
MF "v[196:199],a[200:203],a[68:71],v[196:199]\n"
LD "v5,s[24:27],s84 offen offset:1024 lds\n"
MF "v[188:191],a[200:203],a[64:67],v[220:223]\n"
"s_add_u32 m0,s62,16384\n"
"s_add_u32 s82,s66,1572992\n"
"v_add_u32 v0,0xc000,v0\n"
MF "v[188:191],a[204:207],a[104:107],v[188:191]\n"
"s_sub_u32 s15,s82,7864320\n"
"s_min_u32 s82,s82,s15\n"
"v_add_u32 v1,0xc000,v1\n"
MF "v[180:183],a[204:207],a[100:103],v[220:223]\n"
LD "v6,s[28:31],s82 offen lds\n"
MF "v[180:183],a[200:203],a[60:63],v[180:183]\n"
"s_waitcnt vmcnt(15) lgkmcnt(0)\n"
"s_barrier\n"
MF "v[216:219],a[208:211],a[60:63],v[220:223]\n"
TR "a[200:201],v0\n"
MF "v[216:219],a[212:215],a[100:103],v[216:219]\n"
TR "a[202:203],v0 offset:512\n"
MF "v[224:227],a[212:215],a[104:107],v[220:223]\n"
"ds_read_b128 a[192:195],v3 offset:26624\n"
MF "v[224:227],a[208:211],a[64:67],v[224:227]\n"
TR "a[204:205],v0 offset:4096\n"
MF "v[232:235],a[208:211],a[68:71],v[220:223]\n"
TR "a[206:207],v0 offset:4608\n"
MF "v[232:235],a[212:215],a[108:111],v[232:235]\n"
LD "v11,s[28:31],s82 offen offset:1024 lds\n"
MF "v[240:243],a[212:215],a[112:115],v[220:223]\n"
"ds_read_b128 a[156:159],v2 offset:28672\n"
MF "v[240:243],a[208:211],a[72:75],v[240:243]\n"
"ds_read_b128 a[196:199],v3 offset:28672\n"
MF "v[248:251],a[208:211],a[76:79],v[220:223]\n"
MF "v[248:251],a[212:215],a[116:119],v[248:251]\n"
LD "v12,s[28:31],s82 offen offset:2048 lds\n"
MF "v[252:255],a[220:223],a[116:119],v[220:223]\n"
TR "a[208:209],v0 offset:8\n"
MF "v[252:255],a[216:219],a[76:79],v[252:255]\n"
TR "a[210:211],v0 offset:520\n"
MF "v[244:247],a[216:219],a[72:75],v[220:223]\n"
MF "v[244:247],a[220:223],a[112:115],v[244:247]\n"
TR "a[212:213],v0 offset:4104\n"
MF "v[236:239],a[220:223],a[108:111],v[220:223]\n"
TR "a[214:215],v0 offset:4616\n"
MF "v[236:239],a[216:219],a[68:71],v[236:239]\n"
LD "v13,s[28:31],s82 offen offset:3072 lds\n"
MF "v[228:231],a[216:219],a[64:67],v[220:223]\n"
MF "v[228:231],a[220:223],a[104:107],v[228:231]\n"
"s_add_u32 s84,s14,20480\n"
MF "v[220:223],a[220:223],a[100:103],v[220:223]\n"
"s_add_u32 m0,s63,2048\n"
MF "v[220:223],a[216:219],a[60:63],v[220:223]\n"
"s_waitcnt lgkmcnt(4)\n"
LD "v4,s[24:27],s84 offen lds\n"
MF "v[16:19],a[200:203],a[140:143],v[16:19]\n"
TR "a[216:217],v1\n"
MF "v[16:19],a[204:207],a[180:183],v[16:19]\n"
TR "a[218:219],v1 offset:512\n"
MF "v[24:27],a[204:207],a[184:187],v[24:27]\n"
MF "v[24:27],a[200:203],a[144:147],v[24:27]\n"
TR "a[220:221],v1 offset:4096\n"
MF "v[32:35],a[200:203],a[148:151],v[32:35]\n"
"s_add_u32 s84,s14,29696\n"
TR "a[222:223],v1 offset:4608\n"
MF "v[32:35],a[204:207],a[188:191],v[32:35]\n"
LD "v5,s[24:27],s84 offen offset:1024 lds\n"
MF "v[40:43],a[204:207],a[192:195],v[40:43]\n"
MF "v[40:43],a[200:203],a[152:155],v[40:43]\n"
"s_add_u32 m0,s62,32768\n"
MF "v[48:51],a[200:203],a[156:159],v[48:51]\n"
"s_add_u32 s82,s66,1573120\n"
"s_sub_u32 s15,s82,7864320\n"
"s_min_u32 s82,s82,s15\n"
MF "v[48:51],a[204:207],a[196:199],v[48:51]\n"
"s_waitcnt lgkmcnt(4)\n"
LD "v6,s[28:31],s82 offen lds\n"
MF "v[52:55],a[212:215],a[196:199],v[52:55]\n"
TR "a[200:201],v1 offset:8\n"
MF "v[52:55],a[208:211],a[156:159],v[52:55]\n"
TR "a[202:203],v1 offset:520\n"
MF "v[44:47],a[208:211],a[152:155],v[44:47]\n"
MF "v[44:47],a[212:215],a[192:195],v[44:47]\n"
TR "a[204:205],v1 offset:4104\n"
MF "v[36:39],a[212:215],a[188:191],v[36:39]\n"
TR "a[206:207],v1 offset:4616\n"
MF "v[36:39],a[208:211],a[148:151],v[36:39]\n"
LD "v11,s[28:31],s82 offen offset:1024 lds\n"
MF "v[28:31],a[208:211],a[144:147],v[28:31]\n"
MF "v[28:31],a[212:215],a[184:187],v[28:31]\n"
MF "v[20:23],a[212:215],a[180:183],v[20:23]\n"
LD "v12,s[28:31],s82 offen offset:2048 lds\n"
"s_bitcmp0_b32 s23,12\n"
"s_cbranch_scc1 .Lisx2\n"
"s_mov_b64 exec,0\n"
"s_cmp_eq_u32 s56,0\n"
"s_cbranch_scc1 .Lpg11d\n"
"s_cmp_eq_u32 s56,1\n"
"s_cbranch_scc1 .Lpg10d\n"
"s_cmp_eq_u32 s56,2\n"
"s_cbranch_scc1 .Lpgt0kx_d\n"
"s_branch .Lpg8d\n"
".Lisr2:\n"
"s_mov_b64 exec,-1\n"
".Lisx2:\n"
MF "v[20:23],a[208:211],a[140:143],v[20:23]\n"
"s_waitcnt vmcnt(19) lgkmcnt(0)\n"
"s_barrier\n"
MF "v[56:59],a[216:219],a[140:143],v[56:59]\n"
TR "a[208:209],v0 offset:16384\n"
MF "v[56:59],a[220:223],a[180:183],v[56:59]\n"
TR "a[210:211],v0 offset:16896\n"
MF "v[64:67],a[220:223],a[184:187],v[64:67]\n"
LD "v13,s[28:31],s82 offen offset:3072 lds\n"
MF "v[64:67],a[216:219],a[144:147],v[64:67]\n"
TR "a[212:213],v0 offset:20480\n"
MF "v[72:75],a[216:219],a[148:151],v[72:75]\n"
TR "a[214:215],v0 offset:20992\n"
MF "v[72:75],a[220:223],a[188:191],v[72:75]\n"
"s_add_u32 s84,s14,40960\n"
MF "v[80:83],a[220:223],a[192:195],v[80:83]\n"
"s_add_u32 m0,s63,4096\n"
MF "v[80:83],a[216:219],a[152:155],v[80:83]\n"
LD "v4,s[24:27],s84 offen lds\n"
MF "v[88:91],a[216:219],a[156:159],v[88:91]\n"
MF "v[88:91],a[220:223],a[196:199],v[88:91]\n"
"s_sub_u32 s66,s66,s83\n"
MF "v[92:95],a[204:207],a[196:199],v[92:95]\n"
"s_add_u32 m0,s62,49152\n"
"s_add_u32 s82,s66,2359296\n"
TR "a[216:217],v0 offset:16392\n"
MF "v[92:95],a[200:203],a[156:159],v[92:95]\n"
"s_sub_u32 s15,s82,7864320\n"
"s_min_u32 s82,s82,s15\n"
TR "a[218:219],v0 offset:16904\n"
MF "v[84:87],a[200:203],a[152:155],v[84:87]\n"
LD "v6,s[28:31],s82 offen lds\n"
MF "v[84:87],a[204:207],a[192:195],v[84:87]\n"
TR "a[220:221],v0 offset:20488\n"
MF "v[76:79],a[204:207],a[188:191],v[76:79]\n"
TR "a[222:223],v0 offset:21000\n"
MF "v[76:79],a[200:203],a[148:151],v[76:79]\n"
MF "v[68:71],a[200:203],a[144:147],v[68:71]\n"
MF "v[68:71],a[204:207],a[184:187],v[68:71]\n"
LD "v11,s[28:31],s82 offen offset:1024 lds\n"
MF "v[60:63],a[204:207],a[180:183],v[60:63]\n"
MF "v[60:63],a[200:203],a[140:143],v[60:63]\n"
"s_waitcnt lgkmcnt(4)\n"
MF "v[96:99],a[208:211],a[140:143],v[96:99]\n"
TR "a[200:201],v1 offset:16384\n"
MF "v[96:99],a[212:215],a[180:183],v[96:99]\n"
TR "a[202:203],v1 offset:16896\n"
MF "v[104:107],a[212:215],a[184:187],v[104:107]\n"
LD "v12,s[28:31],s82 offen offset:2048 lds\n"
MF "v[104:107],a[208:211],a[144:147],v[104:107]\n"
TR "a[204:205],v1 offset:20480\n"
MF "v[112:115],a[208:211],a[148:151],v[112:115]\n"
TR "a[206:207],v1 offset:20992\n"
MF "v[112:115],a[212:215],a[188:191],v[112:115]\n"
MF "v[120:123],a[212:215],a[192:195],v[120:123]\n"
MF "v[120:123],a[208:211],a[152:155],v[120:123]\n"
LD "v13,s[28:31],s82 offen offset:3072 lds\n"
MF "v[128:131],a[208:211],a[156:159],v[128:131]\n"
MF "v[128:131],a[212:215],a[196:199],v[128:131]\n"
"s_add_u32 s14,s81,512\n"
"s_sub_u32 s15,s14,1280\n"
"s_waitcnt lgkmcnt(4)\n"
MF "v[132:135],a[220:223],a[196:199],v[132:135]\n"
"s_min_u32 s14,s14,s15\n"
"s_add_u32 s14,s14,s68\n"
TR "a[208:209],v1 offset:16392\n"
MF "v[132:135],a[216:219],a[156:159],v[132:135]\n"
"s_add_u32 s84,s14,0\n"
"s_add_u32 m0,s63,20480\n"
TR "a[210:211],v1 offset:16904\n"
MF "v[124:127],a[216:219],a[152:155],v[124:127]\n"
LD "v4,s[24:27],s84 offen lds\n"
MF "v[124:127],a[220:223],a[192:195],v[124:127]\n"
TR "a[212:213],v1 offset:20488\n"
MF "v[116:119],a[220:223],a[188:191],v[116:119]\n"
TR "a[214:215],v1 offset:21000\n"
MF "v[116:119],a[216:219],a[148:151],v[116:119]\n"
MF "v[108:111],a[216:219],a[144:147],v[108:111]\n"
"s_add_u32 s84,s14,9216\n"
MF "v[108:111],a[220:223],a[184:187],v[108:111]\n"
LD "v5,s[24:27],s84 offen offset:1024 lds\n"
MF "v[100:103],a[220:223],a[180:183],v[100:103]\n"
MF "v[100:103],a[216:219],a[140:143],v[100:103]\n"
"s_waitcnt vmcnt(23) lgkmcnt(0)\n"
"s_barrier\n"
MF "v[136:139],a[200:203],a[140:143],v[136:139]\n"
TR "a[216:217],v0 offset:32768\n"
MF "v[136:139],a[204:207],a[180:183],v[136:139]\n"
"s_add_u32 m0,s62,65536\n"
TR "a[218:219],v0 offset:33280\n"
MF "v[144:147],a[204:207],a[184:187],v[144:147]\n"
"s_add_u32 s82,s66,2359424\n"
"ds_read_b128 a[60:63],v2 offset:40960\n"
MF "v[144:147],a[200:203],a[144:147],v[144:147]\n"
"s_sub_u32 s15,s82,7864320\n"
TR "a[220:221],v0 offset:36864\n"
MF "v[152:155],a[200:203],a[148:151],v[152:155]\n"
"s_min_u32 s82,s82,s15\n"
TR "a[222:223],v0 offset:37376\n"
MF "v[152:155],a[204:207],a[188:191],v[152:155]\n"
LD "v6,s[28:31],s82 offen lds\n"
MF "v[160:163],a[204:207],a[192:195],v[160:163]\n"
"ds_read_b128 a[100:103],v3 offset:40960\n"
MF "v[160:163],a[200:203],a[152:155],v[160:163]\n"
LD "v11,s[28:31],s82 offen offset:1024 lds\n"
MF "v[168:171],a[200:203],a[156:159],v[168:171]\n"
MF "v[168:171],a[204:207],a[196:199],v[168:171]\n"
LD "v12,s[28:31],s82 offen offset:2048 lds\n"
MF "v[172:175],a[212:215],a[196:199],v[172:175]\n"
TR "a[200:201],v0 offset:32776\n"
MF "v[172:175],a[208:211],a[156:159],v[172:175]\n"
TR "a[202:203],v0 offset:33288\n"
MF "v[164:167],a[208:211],a[152:155],v[164:167]\n"
"ds_read_b128 a[64:67],v2 offset:43008\n"
MF "v[164:167],a[212:215],a[192:195],v[164:167]\n"
TR "a[204:205],v0 offset:36872\n"
MF "v[156:159],a[212:215],a[188:191],v[156:159]\n"
TR "a[206:207],v0 offset:37384\n"
MF "v[156:159],a[208:211],a[148:151],v[156:159]\n"
MF "v[148:151],a[208:211],a[144:147],v[148:151]\n"
"ds_read_b128 a[104:107],v3 offset:43008\n"
MF "v[148:151],a[212:215],a[184:187],v[148:151]\n"
MF "v[140:143],a[212:215],a[180:183],v[140:143]\n"
LD "v13,s[28:31],s82 offen offset:3072 lds\n"
MF "v[140:143],a[208:211],a[140:143],v[140:143]\n"
"s_waitcnt lgkmcnt(7)\n"
MF "v[176:179],a[216:219],a[140:143],v[176:179]\n"
TR "a[208:209],v1 offset:32768\n"
MF "v[176:179],a[220:223],a[180:183],v[176:179]\n"
TR "a[210:211],v1 offset:33280\n"
MF "v[184:187],a[220:223],a[184:187],v[184:187]\n"
"ds_read_b128 a[68:71],v2 offset:45056\n"
MF "v[184:187],a[216:219],a[144:147],v[184:187]\n"
"s_add_u32 s84,s14,20480\n"
TR "a[212:213],v1 offset:36864\n"
MF "v[192:195],a[216:219],a[148:151],v[192:195]\n"
"s_add_u32 m0,s63,22528\n"
TR "a[214:215],v1 offset:37376\n"
MF "v[192:195],a[220:223],a[188:191],v[192:195]\n"
LD "v4,s[24:27],s84 offen lds\n"
MF "v[200:203],a[220:223],a[192:195],v[200:203]\n"
"ds_read_b128 a[108:111],v3 offset:45056\n"
MF "v[200:203],a[216:219],a[152:155],v[200:203]\n"
MF "v[208:211],a[216:219],a[156:159],v[208:211]\n"
"s_add_u32 s84,s14,29696\n"
MF "v[208:211],a[220:223],a[196:199],v[208:211]\n"
"s_waitcnt lgkmcnt(7)\n"
LD "v5,s[24:27],s84 offen offset:1024 lds\n"
MF "v[212:215],a[204:207],a[196:199],v[212:215]\n"
TR "a[216:217],v1 offset:32776\n"
MF "v[212:215],a[200:203],a[156:159],v[212:215]\n"
TR "a[218:219],v1 offset:33288\n"
MF "v[204:207],a[200:203],a[152:155],v[204:207]\n"
"ds_read_b128 a[72:75],v2 offset:47104\n"
MF "v[204:207],a[204:207],a[192:195],v[204:207]\n"
TR "a[220:221],v1 offset:36872\n"
MF "v[196:199],a[204:207],a[188:191],v[196:199]\n"
TR "a[222:223],v1 offset:37384\n"
MF "v[196:199],a[200:203],a[148:151],v[196:199]\n"
MF "v[188:191],a[200:203],a[144:147],v[188:191]\n"
"v_add_u32 v0,0xffff4000,v0\n"
MF "v[188:191],a[204:207],a[184:187],v[188:191]\n"
"v_add_u32 v1,0xffff4000,v1\n"
MF "v[180:183],a[204:207],a[180:183],v[180:183]\n"
MF "v[180:183],a[200:203],a[140:143],v[180:183]\n"
"s_waitcnt vmcnt(25) lgkmcnt(0)\n"
"s_barrier\n"
MF "v[216:219],a[208:211],a[140:143],v[216:219]\n"
TR "a[200:201],v0\n"
MF "v[216:219],a[212:215],a[180:183],v[216:219]\n"
"s_add_u32 m0,s62,81920\n"
TR "a[202:203],v0 offset:512\n"
MF "v[224:227],a[212:215],a[184:187],v[224:227]\n"
"s_add_u32 s82,s66,2359552\n"
"ds_read_b128 a[112:115],v3 offset:47104\n"
MF "v[224:227],a[208:211],a[144:147],v[224:227]\n"
"s_sub_u32 s15,s82,7864320\n"
TR "a[204:205],v0 offset:4096\n"
MF "v[232:235],a[208:211],a[148:151],v[232:235]\n"
"s_min_u32 s82,s82,s15\n"
TR "a[206:207],v0 offset:4608\n"
MF "v[232:235],a[212:215],a[188:191],v[232:235]\n"
LD "v6,s[28:31],s82 offen lds\n"
MF "v[240:243],a[212:215],a[192:195],v[240:243]\n"
"ds_read_b128 a[76:79],v2 offset:49152\n"
MF "v[240:243],a[208:211],a[152:155],v[240:243]\n"
"ds_read_b128 a[116:119],v3 offset:49152\n"
MF "v[248:251],a[208:211],a[156:159],v[248:251]\n"
LD "v11,s[28:31],s82 offen offset:1024 lds\n"
MF "v[248:251],a[212:215],a[196:199],v[248:251]\n"
MF "v[252:255],a[220:223],a[196:199],v[252:255]\n"
TR "a[208:209],v0 offset:8\n"
MF "v[252:255],a[216:219],a[156:159],v[252:255]\n"
TR "a[210:211],v0 offset:520\n"
MF "v[244:247],a[216:219],a[152:155],v[244:247]\n"
MF "v[244:247],a[220:223],a[192:195],v[244:247]\n"
TR "a[212:213],v0 offset:4104\n"
MF "v[236:239],a[220:223],a[188:191],v[236:239]\n"
TR "a[214:215],v0 offset:4616\n"
MF "v[236:239],a[216:219],a[148:151],v[236:239]\n"
LD "v12,s[28:31],s82 offen offset:2048 lds\n"
MF "v[228:231],a[216:219],a[144:147],v[228:231]\n"
MF "v[228:231],a[220:223],a[184:187],v[228:231]\n"
MF "v[220:223],a[220:223],a[180:183],v[220:223]\n"
MF "v[220:223],a[216:219],a[140:143],v[220:223]\n"
"s_waitcnt lgkmcnt(4)\n"
MF "v[16:19],a[200:203],a[60:63],v[16:19]\n"
TR "a[216:217],v1\n"
MF "v[16:19],a[204:207],a[100:103],v[16:19]\n"
TR "a[218:219],v1 offset:512\n"
MF "v[24:27],a[204:207],a[104:107],v[24:27]\n"
LD "v13,s[28:31],s82 offen offset:3072 lds\n"
MF "v[24:27],a[200:203],a[64:67],v[24:27]\n"
TR "a[220:221],v1 offset:4096\n"
MF "v[32:35],a[200:203],a[68:71],v[32:35]\n"
TR "a[222:223],v1 offset:4608\n"
MF "v[32:35],a[204:207],a[108:111],v[32:35]\n"
MF "v[40:43],a[204:207],a[112:115],v[40:43]\n"
MF "v[40:43],a[200:203],a[72:75],v[40:43]\n"
"s_add_u32 s84,s14,40960\n"
MF "v[48:51],a[200:203],a[76:79],v[48:51]\n"
"s_add_u32 m0,s63,24576\n"
MF "v[48:51],a[204:207],a[116:119],v[48:51]\n"
"s_waitcnt lgkmcnt(4)\n"
LD "v4,s[24:27],s84 offen lds\n"
MF "v[52:55],a[212:215],a[116:119],v[52:55]\n"
TR "a[200:201],v1 offset:8\n"
MF "v[52:55],a[208:211],a[76:79],v[52:55]\n"
TR "a[202:203],v1 offset:520\n"
MF "v[44:47],a[208:211],a[72:75],v[44:47]\n"
MF "v[44:47],a[212:215],a[112:115],v[44:47]\n"
TR "a[204:205],v1 offset:4104\n"
MF "v[36:39],a[212:215],a[108:111],v[36:39]\n"
TR "a[206:207],v1 offset:4616\n"
MF "v[36:39],a[208:211],a[68:71],v[36:39]\n"
MF "v[28:31],a[208:211],a[64:67],v[28:31]\n"
MF "v[28:31],a[212:215],a[104:107],v[28:31]\n"
MF "v[20:23],a[212:215],a[100:103],v[20:23]\n"
MF "v[20:23],a[208:211],a[60:63],v[20:23]\n"
"s_waitcnt vmcnt(24) lgkmcnt(0)\n"
"s_barrier\n"
MF "v[56:59],a[216:219],a[60:63],v[56:59]\n"
"s_add_u32 m0,s62,0\n"
"s_add_u32 s82,s66,3145728\n"
TR "a[208:209],v0 offset:16384\n"
MF "v[56:59],a[220:223],a[100:103],v[56:59]\n"
"s_sub_u32 s15,s82,7864320\n"
"s_min_u32 s82,s82,s15\n"
TR "a[210:211],v0 offset:16896\n"
MF "v[64:67],a[220:223],a[104:107],v[64:67]\n"
LD "v6,s[28:31],s82 offen lds\n"
MF "v[64:67],a[216:219],a[64:67],v[64:67]\n"
TR "a[212:213],v0 offset:20480\n"
MF "v[72:75],a[216:219],a[68:71],v[72:75]\n"
TR "a[214:215],v0 offset:20992\n"
MF "v[72:75],a[220:223],a[108:111],v[72:75]\n"
MF "v[80:83],a[220:223],a[112:115],v[80:83]\n"
LD "v11,s[28:31],s82 offen offset:1024 lds\n"
MF "v[80:83],a[216:219],a[72:75],v[80:83]\n"
MF "v[88:91],a[216:219],a[76:79],v[88:91]\n"
MF "v[88:91],a[220:223],a[116:119],v[88:91]\n"
MF "v[92:95],a[204:207],a[116:119],v[92:95]\n"
TR "a[216:217],v0 offset:16392\n"
MF "v[92:95],a[200:203],a[76:79],v[92:95]\n"
TR "a[218:219],v0 offset:16904\n"
MF "v[84:87],a[200:203],a[72:75],v[84:87]\n"
LD "v12,s[28:31],s82 offen offset:2048 lds\n"
MF "v[84:87],a[204:207],a[112:115],v[84:87]\n"
TR "a[220:221],v0 offset:20488\n"
MF "v[76:79],a[204:207],a[108:111],v[76:79]\n"
TR "a[222:223],v0 offset:21000\n"
MF "v[76:79],a[200:203],a[68:71],v[76:79]\n"
MF "v[68:71],a[200:203],a[64:67],v[68:71]\n"
MF "v[68:71],a[204:207],a[104:107],v[68:71]\n"
MF "v[60:63],a[204:207],a[100:103],v[60:63]\n"
LD "v13,s[28:31],s82 offen offset:3072 lds\n"
MF "v[60:63],a[200:203],a[60:63],v[60:63]\n"
"s_waitcnt lgkmcnt(4)\n"
MF "v[96:99],a[208:211],a[60:63],v[96:99]\n"
TR "a[200:201],v1 offset:16384\n"
MF "v[96:99],a[212:215],a[100:103],v[96:99]\n"
TR "a[202:203],v1 offset:16896\n"
MF "v[104:107],a[212:215],a[104:107],v[104:107]\n"
"s_add_u32 s14,s81,640\n"
"s_sub_u32 s15,s14,1280\n"
MF "v[104:107],a[208:211],a[64:67],v[104:107]\n"
"s_min_u32 s14,s14,s15\n"
"s_add_u32 s14,s14,s68\n"
TR "a[204:205],v1 offset:20480\n"
MF "v[112:115],a[208:211],a[68:71],v[112:115]\n"
"s_add_u32 s84,s14,0\n"
"s_add_u32 m0,s63,40960\n"
TR "a[206:207],v1 offset:20992\n"
MF "v[112:115],a[212:215],a[108:111],v[112:115]\n"
LD "v4,s[24:27],s84 offen lds\n"
MF "v[120:123],a[212:215],a[112:115],v[120:123]\n"
MF "v[120:123],a[208:211],a[72:75],v[120:123]\n"
MF "v[128:131],a[208:211],a[76:79],v[128:131]\n"
"s_add_u32 s84,s14,9216\n"
MF "v[128:131],a[212:215],a[116:119],v[128:131]\n"
"s_waitcnt lgkmcnt(4)\n"
LD "v5,s[24:27],s84 offen offset:1024 lds\n"
MF "v[132:135],a[220:223],a[116:119],v[132:135]\n"
TR "a[208:209],v1 offset:16392\n"
MF "v[132:135],a[216:219],a[76:79],v[132:135]\n"
TR "a[210:211],v1 offset:16904\n"
MF "v[124:127],a[216:219],a[72:75],v[124:127]\n"
MF "v[124:127],a[220:223],a[112:115],v[124:127]\n"
TR "a[212:213],v1 offset:20488\n"
MF "v[116:119],a[220:223],a[108:111],v[116:119]\n"
TR "a[214:215],v1 offset:21000\n"
MF "v[116:119],a[216:219],a[68:71],v[116:119]\n"
MF "v[108:111],a[216:219],a[64:67],v[108:111]\n"
MF "v[108:111],a[220:223],a[104:107],v[108:111]\n"
MF "v[100:103],a[220:223],a[100:103],v[100:103]\n"
MF "v[100:103],a[216:219],a[60:63],v[100:103]\n"
"s_waitcnt vmcnt(23) lgkmcnt(0)\n"
"s_barrier\n"
MF "v[136:139],a[200:203],a[60:63],v[136:139]\n"
TR "a[216:217],v0 offset:32768\n"
MF "v[136:139],a[204:207],a[100:103],v[136:139]\n"
"s_add_u32 m0,s62,16384\n"
TR "a[218:219],v0 offset:33280\n"
MF "v[144:147],a[204:207],a[104:107],v[144:147]\n"
"s_add_u32 s82,s66,3145856\n"
"ds_read_b128 a[140:143],v2\n"
MF "v[144:147],a[200:203],a[64:67],v[144:147]\n"
"s_sub_u32 s15,s82,7864320\n"
TR "a[220:221],v0 offset:36864\n"
MF "v[152:155],a[200:203],a[68:71],v[152:155]\n"
"s_min_u32 s82,s82,s15\n"
TR "a[222:223],v0 offset:37376\n"
MF "v[152:155],a[204:207],a[108:111],v[152:155]\n"
LD "v6,s[28:31],s82 offen lds\n"
MF "v[160:163],a[204:207],a[112:115],v[160:163]\n"
"ds_read_b128 a[180:183],v3\n"
MF "v[160:163],a[200:203],a[72:75],v[160:163]\n"
LD "v11,s[28:31],s82 offen offset:1024 lds\n"
MF "v[168:171],a[200:203],a[76:79],v[168:171]\n"
MF "v[168:171],a[204:207],a[116:119],v[168:171]\n"
LD "v12,s[28:31],s82 offen offset:2048 lds\n"
MF "v[172:175],a[212:215],a[116:119],v[172:175]\n"
TR "a[200:201],v0 offset:32776\n"
MF "v[172:175],a[208:211],a[76:79],v[172:175]\n"
TR "a[202:203],v0 offset:33288\n"
MF "v[164:167],a[208:211],a[72:75],v[164:167]\n"
"ds_read_b128 a[144:147],v2 offset:2048\n"
MF "v[164:167],a[212:215],a[112:115],v[164:167]\n"
TR "a[204:205],v0 offset:36872\n"
MF "v[156:159],a[212:215],a[108:111],v[156:159]\n"
TR "a[206:207],v0 offset:37384\n"
MF "v[156:159],a[208:211],a[68:71],v[156:159]\n"
MF "v[148:151],a[208:211],a[64:67],v[148:151]\n"
"ds_read_b128 a[184:187],v3 offset:2048\n"
MF "v[148:151],a[212:215],a[104:107],v[148:151]\n"
MF "v[140:143],a[212:215],a[100:103],v[140:143]\n"
LD "v13,s[28:31],s82 offen offset:3072 lds\n"
MF "v[140:143],a[208:211],a[60:63],v[140:143]\n"
"s_waitcnt lgkmcnt(7)\n"
MF "v[176:179],a[216:219],a[60:63],v[176:179]\n"
TR "a[208:209],v1 offset:32768\n"
MF "v[176:179],a[220:223],a[100:103],v[176:179]\n"
TR "a[210:211],v1 offset:33280\n"
MF "v[184:187],a[220:223],a[104:107],v[184:187]\n"
"ds_read_b128 a[148:151],v2 offset:4096\n"
MF "v[184:187],a[216:219],a[64:67],v[184:187]\n"
"s_add_u32 s84,s14,20480\n"
TR "a[212:213],v1 offset:36864\n"
MF "v[192:195],a[216:219],a[68:71],v[192:195]\n"
"s_add_u32 m0,s63,43008\n"
TR "a[214:215],v1 offset:37376\n"
MF "v[192:195],a[220:223],a[108:111],v[192:195]\n"
LD "v4,s[24:27],s84 offen lds\n"
MF "v[200:203],a[220:223],a[112:115],v[200:203]\n"
"ds_read_b128 a[188:191],v3 offset:4096\n"
MF "v[200:203],a[216:219],a[72:75],v[200:203]\n"
MF "v[208:211],a[216:219],a[76:79],v[208:211]\n"
"s_add_u32 s84,s14,29696\n"
MF "v[208:211],a[220:223],a[116:119],v[208:211]\n"
"s_waitcnt lgkmcnt(7)\n"
LD "v5,s[24:27],s84 offen offset:1024 lds\n"
MF "v[212:215],a[204:207],a[116:119],v[212:215]\n"
TR "a[216:217],v1 offset:32776\n"
MF "v[212:215],a[200:203],a[76:79],v[212:215]\n"
TR "a[218:219],v1 offset:33288\n"
MF "v[204:207],a[200:203],a[72:75],v[204:207]\n"
"ds_read_b128 a[152:155],v2 offset:6144\n"
MF "v[204:207],a[204:207],a[112:115],v[204:207]\n"
TR "a[220:221],v1 offset:36872\n"
MF "v[196:199],a[204:207],a[108:111],v[196:199]\n"
TR "a[222:223],v1 offset:37384\n"
MF "v[196:199],a[200:203],a[68:71],v[196:199]\n"
MF "v[188:191],a[200:203],a[64:67],v[188:191]\n"
"v_add_u32 v0,0xc000,v0\n"
MF "v[188:191],a[204:207],a[104:107],v[188:191]\n"
"v_add_u32 v1,0xc000,v1\n"
MF "v[180:183],a[204:207],a[100:103],v[180:183]\n"
MF "v[180:183],a[200:203],a[60:63],v[180:183]\n"
"s_waitcnt vmcnt(25) lgkmcnt(0)\n"
"s_barrier\n"
MF "v[216:219],a[208:211],a[60:63],v[216:219]\n"
TR "a[200:201],v0\n"
MF "v[216:219],a[212:215],a[100:103],v[216:219]\n"
"s_add_u32 m0,s62,32768\n"
TR "a[202:203],v0 offset:512\n"
MF "v[224:227],a[212:215],a[104:107],v[224:227]\n"
".Lpg5c:\n"
"s_cbranch_execz .Lpg31c\n"
".Lpg5f:\n"
"s_cbranch_execz .Lpgt0k_f\n"
"s_add_u32 s82,s66,3145984\n"
"ds_read_b128 a[192:195],v3 offset:6144\n"
MF "v[224:227],a[208:211],a[64:67],v[224:227]\n"
"s_sub_u32 s15,s82,7864320\n"
TR "a[204:205],v0 offset:4096\n"
MF "v[232:235],a[208:211],a[68:71],v[232:235]\n"
"s_min_u32 s82,s82,s15\n"
TR "a[206:207],v0 offset:4608\n"
MF "v[232:235],a[212:215],a[108:111],v[232:235]\n"
LD "v6,s[28:31],s82 offen lds\n"
MF "v[240:243],a[212:215],a[112:115],v[240:243]\n"
"ds_read_b128 a[156:159],v2 offset:8192\n"
MF "v[240:243],a[208:211],a[72:75],v[240:243]\n"
"ds_read_b128 a[196:199],v3 offset:8192\n"
MF "v[248:251],a[208:211],a[76:79],v[248:251]\n"
LD "v11,s[28:31],s82 offen offset:1024 lds\n"
MF "v[248:251],a[212:215],a[116:119],v[248:251]\n"
MF "v[252:255],a[220:223],a[116:119],v[252:255]\n"
TR "a[208:209],v0 offset:8\n"
MF "v[252:255],a[216:219],a[76:79],v[252:255]\n"
TR "a[210:211],v0 offset:520\n"
MF "v[244:247],a[216:219],a[72:75],v[244:247]\n"
MF "v[244:247],a[220:223],a[112:115],v[244:247]\n"
TR "a[212:213],v0 offset:4104\n"
MF "v[236:239],a[220:223],a[108:111],v[236:239]\n"
TR "a[214:215],v0 offset:4616\n"
MF "v[236:239],a[216:219],a[68:71],v[236:239]\n"
LD "v12,s[28:31],s82 offen offset:2048 lds\n"
MF "v[228:231],a[216:219],a[64:67],v[228:231]\n"
MF "v[228:231],a[220:223],a[104:107],v[228:231]\n"
MF "v[220:223],a[220:223],a[100:103],v[220:223]\n"
MF "v[220:223],a[216:219],a[60:63],v[220:223]\n"
"s_waitcnt lgkmcnt(4)\n"
MF "v[16:19],a[200:203],a[140:143],v[16:19]\n"
TR "a[216:217],v1\n"
MF "v[16:19],a[204:207],a[180:183],v[16:19]\n"
TR "a[218:219],v1 offset:512\n"
MF "v[24:27],a[204:207],a[184:187],v[24:27]\n"
LD "v13,s[28:31],s82 offen offset:3072 lds\n"
MF "v[24:27],a[200:203],a[144:147],v[24:27]\n"
TR "a[220:221],v1 offset:4096\n"
MF "v[32:35],a[200:203],a[148:151],v[32:35]\n"
TR "a[222:223],v1 offset:4608\n"
MF "v[32:35],a[204:207],a[188:191],v[32:35]\n"
MF "v[40:43],a[204:207],a[192:195],v[40:43]\n"
MF "v[40:43],a[200:203],a[152:155],v[40:43]\n"
"s_add_u32 s84,s14,40960\n"
MF "v[48:51],a[200:203],a[156:159],v[48:51]\n"
"s_add_u32 m0,s63,45056\n"
MF "v[48:51],a[204:207],a[196:199],v[48:51]\n"
"s_waitcnt lgkmcnt(4)\n"
LD "v4,s[24:27],s84 offen lds\n"
MF "v[52:55],a[212:215],a[196:199],v[52:55]\n"
TR "a[200:201],v1 offset:8\n"
MF "v[52:55],a[208:211],a[156:159],v[52:55]\n"
TR "a[202:203],v1 offset:520\n"
MF "v[44:47],a[208:211],a[152:155],v[44:47]\n"
MF "v[44:47],a[212:215],a[192:195],v[44:47]\n"
TR "a[204:205],v1 offset:4104\n"
MF "v[36:39],a[212:215],a[188:191],v[36:39]\n"
TR "a[206:207],v1 offset:4616\n"
MF "v[36:39],a[208:211],a[148:151],v[36:39]\n"
MF "v[28:31],a[208:211],a[144:147],v[28:31]\n"
MF "v[28:31],a[212:215],a[184:187],v[28:31]\n"
MF "v[20:23],a[212:215],a[180:183],v[20:23]\n"
MF "v[20:23],a[208:211],a[140:143],v[20:23]\n"
"s_waitcnt vmcnt(24) lgkmcnt(0)\n"
"s_barrier\n"
MF "v[56:59],a[216:219],a[140:143],v[56:59]\n"
"s_add_u32 m0,s62,49152\n"
"s_add_u32 s82,s66,3932160\n"
TR "a[208:209],v0 offset:16384\n"
MF "v[56:59],a[220:223],a[180:183],v[56:59]\n"
"s_sub_u32 s15,s82,7864320\n"
"s_min_u32 s82,s82,s15\n"
TR "a[210:211],v0 offset:16896\n"
MF "v[64:67],a[220:223],a[184:187],v[64:67]\n"
LD "v6,s[28:31],s82 offen lds\n"
MF "v[64:67],a[216:219],a[144:147],v[64:67]\n"
TR "a[212:213],v0 offset:20480\n"
MF "v[72:75],a[216:219],a[148:151],v[72:75]\n"
TR "a[214:215],v0 offset:20992\n"
MF "v[72:75],a[220:223],a[188:191],v[72:75]\n"
MF "v[80:83],a[220:223],a[192:195],v[80:83]\n"
LD "v11,s[28:31],s82 offen offset:1024 lds\n"
MF "v[80:83],a[216:219],a[152:155],v[80:83]\n"
MF "v[88:91],a[216:219],a[156:159],v[88:91]\n"
MF "v[88:91],a[220:223],a[196:199],v[88:91]\n"
MF "v[92:95],a[204:207],a[196:199],v[92:95]\n"
TR "a[216:217],v0 offset:16392\n"
MF "v[92:95],a[200:203],a[156:159],v[92:95]\n"
TR "a[218:219],v0 offset:16904\n"
MF "v[84:87],a[200:203],a[152:155],v[84:87]\n"
LD "v12,s[28:31],s82 offen offset:2048 lds\n"
MF "v[84:87],a[204:207],a[192:195],v[84:87]\n"
TR "a[220:221],v0 offset:20488\n"
MF "v[76:79],a[204:207],a[188:191],v[76:79]\n"
TR "a[222:223],v0 offset:21000\n"
MF "v[76:79],a[200:203],a[148:151],v[76:79]\n"
MF "v[68:71],a[200:203],a[144:147],v[68:71]\n"
MF "v[68:71],a[204:207],a[184:187],v[68:71]\n"
MF "v[60:63],a[204:207],a[180:183],v[60:63]\n"
LD "v13,s[28:31],s82 offen offset:3072 lds\n"
MF "v[60:63],a[200:203],a[140:143],v[60:63]\n"
"s_waitcnt lgkmcnt(4)\n"
MF "v[96:99],a[208:211],a[140:143],v[96:99]\n"
TR "a[200:201],v1 offset:16384\n"
MF "v[96:99],a[212:215],a[180:183],v[96:99]\n"
TR "a[202:203],v1 offset:16896\n"
MF "v[104:107],a[212:215],a[184:187],v[104:107]\n"
"s_add_u32 s14,s81,768\n"
"s_sub_u32 s15,s14,1280\n"
MF "v[104:107],a[208:211],a[144:147],v[104:107]\n"
"s_min_u32 s14,s14,s15\n"
"s_add_u32 s14,s14,s68\n"
TR "a[204:205],v1 offset:20480\n"
MF "v[112:115],a[208:211],a[148:151],v[112:115]\n"
"s_add_u32 s84,s14,0\n"
"s_add_u32 m0,s63,0\n"
TR "a[206:207],v1 offset:20992\n"
MF "v[112:115],a[212:215],a[188:191],v[112:115]\n"
LD "v4,s[24:27],s84 offen lds\n"
MF "v[120:123],a[212:215],a[192:195],v[120:123]\n"
MF "v[120:123],a[208:211],a[152:155],v[120:123]\n"
MF "v[128:131],a[208:211],a[156:159],v[128:131]\n"
"s_add_u32 s84,s14,9216\n"
MF "v[128:131],a[212:215],a[196:199],v[128:131]\n"
"s_waitcnt lgkmcnt(4)\n"
LD "v5,s[24:27],s84 offen offset:1024 lds\n"
MF "v[132:135],a[220:223],a[196:199],v[132:135]\n"
TR "a[208:209],v1 offset:16392\n"
MF "v[132:135],a[216:219],a[156:159],v[132:135]\n"
TR "a[210:211],v1 offset:16904\n"
MF "v[124:127],a[216:219],a[152:155],v[124:127]\n"
MF "v[124:127],a[220:223],a[192:195],v[124:127]\n"
TR "a[212:213],v1 offset:20488\n"
MF "v[116:119],a[220:223],a[188:191],v[116:119]\n"
TR "a[214:215],v1 offset:21000\n"
MF "v[116:119],a[216:219],a[148:151],v[116:119]\n"
MF "v[108:111],a[216:219],a[144:147],v[108:111]\n"
MF "v[108:111],a[220:223],a[184:187],v[108:111]\n"
MF "v[100:103],a[220:223],a[180:183],v[100:103]\n"
MF "v[100:103],a[216:219],a[140:143],v[100:103]\n"
"s_waitcnt vmcnt(23) lgkmcnt(0)\n"
"s_barrier\n"
MF "v[136:139],a[200:203],a[140:143],v[136:139]\n"
TR "a[216:217],v0 offset:32768\n"
MF "v[136:139],a[204:207],a[180:183],v[136:139]\n"
"s_add_u32 m0,s62,65536\n"
TR "a[218:219],v0 offset:33280\n"
MF "v[144:147],a[204:207],a[184:187],v[144:147]\n"
"s_add_u32 s82,s66,3932288\n"
"ds_read_b128 a[60:63],v2 offset:20480\n"
MF "v[144:147],a[200:203],a[144:147],v[144:147]\n"
"s_sub_u32 s15,s82,7864320\n"
TR "a[220:221],v0 offset:36864\n"
MF "v[152:155],a[200:203],a[148:151],v[152:155]\n"
"s_min_u32 s82,s82,s15\n"
TR "a[222:223],v0 offset:37376\n"
MF "v[152:155],a[204:207],a[188:191],v[152:155]\n"
LD "v6,s[28:31],s82 offen lds\n"
MF "v[160:163],a[204:207],a[192:195],v[160:163]\n"
"ds_read_b128 a[100:103],v3 offset:20480\n"
MF "v[160:163],a[200:203],a[152:155],v[160:163]\n"
LD "v11,s[28:31],s82 offen offset:1024 lds\n"
MF "v[168:171],a[200:203],a[156:159],v[168:171]\n"
MF "v[168:171],a[204:207],a[196:199],v[168:171]\n"
LD "v12,s[28:31],s82 offen offset:2048 lds\n"
MF "v[172:175],a[212:215],a[196:199],v[172:175]\n"
TR "a[200:201],v0 offset:32776\n"
MF "v[172:175],a[208:211],a[156:159],v[172:175]\n"
TR "a[202:203],v0 offset:33288\n"
MF "v[164:167],a[208:211],a[152:155],v[164:167]\n"
"ds_read_b128 a[64:67],v2 offset:22528\n"
MF "v[164:167],a[212:215],a[192:195],v[164:167]\n"
TR "a[204:205],v0 offset:36872\n"
MF "v[156:159],a[212:215],a[188:191],v[156:159]\n"
TR "a[206:207],v0 offset:37384\n"
MF "v[156:159],a[208:211],a[148:151],v[156:159]\n"
MF "v[148:151],a[208:211],a[144:147],v[148:151]\n"
"ds_read_b128 a[104:107],v3 offset:22528\n"
MF "v[148:151],a[212:215],a[184:187],v[148:151]\n"
MF "v[140:143],a[212:215],a[180:183],v[140:143]\n"
LD "v13,s[28:31],s82 offen offset:3072 lds\n"
MF "v[140:143],a[208:211],a[140:143],v[140:143]\n"
"s_waitcnt lgkmcnt(7)\n"
MF "v[176:179],a[216:219],a[140:143],v[176:179]\n"
TR "a[208:209],v1 offset:32768\n"
MF "v[176:179],a[220:223],a[180:183],v[176:179]\n"
TR "a[210:211],v1 offset:33280\n"
MF "v[184:187],a[220:223],a[184:187],v[184:187]\n"
"ds_read_b128 a[68:71],v2 offset:24576\n"
MF "v[184:187],a[216:219],a[144:147],v[184:187]\n"
"s_add_u32 s84,s14,20480\n"
TR "a[212:213],v1 offset:36864\n"
MF "v[192:195],a[216:219],a[148:151],v[192:195]\n"
"s_add_u32 m0,s63,2048\n"
TR "a[214:215],v1 offset:37376\n"
MF "v[192:195],a[220:223],a[188:191],v[192:195]\n"
LD "v4,s[24:27],s84 offen lds\n"
MF "v[200:203],a[220:223],a[192:195],v[200:203]\n"
"ds_read_b128 a[108:111],v3 offset:24576\n"
MF "v[200:203],a[216:219],a[152:155],v[200:203]\n"
MF "v[208:211],a[216:219],a[156:159],v[208:211]\n"
"s_add_u32 s84,s14,29696\n"
MF "v[208:211],a[220:223],a[196:199],v[208:211]\n"
"s_waitcnt lgkmcnt(7)\n"
LD "v5,s[24:27],s84 offen offset:1024 lds\n"
MF "v[212:215],a[204:207],a[196:199],v[212:215]\n"
TR "a[216:217],v1 offset:32776\n"
MF "v[212:215],a[200:203],a[156:159],v[212:215]\n"
TR "a[218:219],v1 offset:33288\n"
MF "v[204:207],a[200:203],a[152:155],v[204:207]\n"
"ds_read_b128 a[72:75],v2 offset:26624\n"
MF "v[204:207],a[204:207],a[192:195],v[204:207]\n"
TR "a[220:221],v1 offset:36872\n"
MF "v[196:199],a[204:207],a[188:191],v[196:199]\n"
TR "a[222:223],v1 offset:37384\n"
MF "v[196:199],a[200:203],a[148:151],v[196:199]\n"
MF "v[188:191],a[200:203],a[144:147],v[188:191]\n"
"v_add_u32 v0,0xffff4000,v0\n"
MF "v[188:191],a[204:207],a[184:187],v[188:191]\n"
"v_add_u32 v1,0xffff4000,v1\n"
MF "v[180:183],a[204:207],a[180:183],v[180:183]\n"
MF "v[180:183],a[200:203],a[140:143],v[180:183]\n"
"s_waitcnt vmcnt(25) lgkmcnt(0)\n"
"s_barrier\n"
MF "v[216:219],a[208:211],a[140:143],v[216:219]\n"
TR "a[200:201],v0\n"
MF "v[216:219],a[212:215],a[180:183],v[216:219]\n"
"s_add_u32 m0,s62,81920\n"
TR "a[202:203],v0 offset:512\n"
MF "v[224:227],a[212:215],a[184:187],v[224:227]\n"
"s_add_u32 s82,s66,3932416\n"
"ds_read_b128 a[112:115],v3 offset:26624\n"
MF "v[224:227],a[208:211],a[144:147],v[224:227]\n"
"s_sub_u32 s15,s82,7864320\n"
TR "a[204:205],v0 offset:4096\n"
MF "v[232:235],a[208:211],a[148:151],v[232:235]\n"
"s_min_u32 s82,s82,s15\n"
TR "a[206:207],v0 offset:4608\n"
MF "v[232:235],a[212:215],a[188:191],v[232:235]\n"
LD "v6,s[28:31],s82 offen lds\n"
MF "v[240:243],a[212:215],a[192:195],v[240:243]\n"
"ds_read_b128 a[76:79],v2 offset:28672\n"
MF "v[240:243],a[208:211],a[152:155],v[240:243]\n"
"ds_read_b128 a[116:119],v3 offset:28672\n"
MF "v[248:251],a[208:211],a[156:159],v[248:251]\n"
LD "v11,s[28:31],s82 offen offset:1024 lds\n"
MF "v[248:251],a[212:215],a[196:199],v[248:251]\n"
MF "v[252:255],a[220:223],a[196:199],v[252:255]\n"
TR "a[208:209],v0 offset:8\n"
MF "v[252:255],a[216:219],a[156:159],v[252:255]\n"
TR "a[210:211],v0 offset:520\n"
MF "v[244:247],a[216:219],a[152:155],v[244:247]\n"
MF "v[244:247],a[220:223],a[192:195],v[244:247]\n"
TR "a[212:213],v0 offset:4104\n"
MF "v[236:239],a[220:223],a[188:191],v[236:239]\n"
TR "a[214:215],v0 offset:4616\n"
MF "v[236:239],a[216:219],a[148:151],v[236:239]\n"
LD "v12,s[28:31],s82 offen offset:2048 lds\n"
MF "v[228:231],a[216:219],a[144:147],v[228:231]\n"
MF "v[228:231],a[220:223],a[184:187],v[228:231]\n"
MF "v[220:223],a[220:223],a[180:183],v[220:223]\n"
MF "v[220:223],a[216:219],a[140:143],v[220:223]\n"
"s_waitcnt lgkmcnt(4)\n"
MF "v[16:19],a[200:203],a[60:63],v[16:19]\n"
TR "a[216:217],v1\n"
MF "v[16:19],a[204:207],a[100:103],v[16:19]\n"
TR "a[218:219],v1 offset:512\n"
MF "v[24:27],a[204:207],a[104:107],v[24:27]\n"
LD "v13,s[28:31],s82 offen offset:3072 lds\n"
MF "v[24:27],a[200:203],a[64:67],v[24:27]\n"
TR "a[220:221],v1 offset:4096\n"
MF "v[32:35],a[200:203],a[68:71],v[32:35]\n"
TR "a[222:223],v1 offset:4608\n"
MF "v[32:35],a[204:207],a[108:111],v[32:35]\n"
MF "v[40:43],a[204:207],a[112:115],v[40:43]\n"
MF "v[40:43],a[200:203],a[72:75],v[40:43]\n"
"s_add_u32 s84,s14,40960\n"
MF "v[48:51],a[200:203],a[76:79],v[48:51]\n"
"s_add_u32 m0,s63,4096\n"
MF "v[48:51],a[204:207],a[116:119],v[48:51]\n"
"s_waitcnt lgkmcnt(4)\n"
LD "v4,s[24:27],s84 offen lds\n"
MF "v[52:55],a[212:215],a[116:119],v[52:55]\n"
TR "a[200:201],v1 offset:8\n"
MF "v[52:55],a[208:211],a[76:79],v[52:55]\n"
TR "a[202:203],v1 offset:520\n"
MF "v[44:47],a[208:211],a[72:75],v[44:47]\n"
MF "v[44:47],a[212:215],a[112:115],v[44:47]\n"
TR "a[204:205],v1 offset:4104\n"
MF "v[36:39],a[212:215],a[108:111],v[36:39]\n"
TR "a[206:207],v1 offset:4616\n"
MF "v[36:39],a[208:211],a[68:71],v[36:39]\n"
MF "v[28:31],a[208:211],a[64:67],v[28:31]\n"
MF "v[28:31],a[212:215],a[104:107],v[28:31]\n"
MF "v[20:23],a[212:215],a[100:103],v[20:23]\n"
MF "v[20:23],a[208:211],a[60:63],v[20:23]\n"
"s_waitcnt vmcnt(24) lgkmcnt(0)\n"
"s_barrier\n"
MF "v[56:59],a[216:219],a[60:63],v[56:59]\n"
"s_add_u32 m0,s62,0\n"
"s_add_u32 s82,s66,4718592\n"
TR "a[208:209],v0 offset:16384\n"
MF "v[56:59],a[220:223],a[100:103],v[56:59]\n"
"s_sub_u32 s15,s82,7864320\n"
"s_min_u32 s82,s82,s15\n"
TR "a[210:211],v0 offset:16896\n"
MF "v[64:67],a[220:223],a[104:107],v[64:67]\n"
LD "v6,s[28:31],s82 offen lds\n"
MF "v[64:67],a[216:219],a[64:67],v[64:67]\n"
TR "a[212:213],v0 offset:20480\n"
MF "v[72:75],a[216:219],a[68:71],v[72:75]\n"
TR "a[214:215],v0 offset:20992\n"
MF "v[72:75],a[220:223],a[108:111],v[72:75]\n"
MF "v[80:83],a[220:223],a[112:115],v[80:83]\n"
LD "v11,s[28:31],s82 offen offset:1024 lds\n"
MF "v[80:83],a[216:219],a[72:75],v[80:83]\n"
MF "v[88:91],a[216:219],a[76:79],v[88:91]\n"
MF "v[88:91],a[220:223],a[116:119],v[88:91]\n"
MF "v[92:95],a[204:207],a[116:119],v[92:95]\n"
TR "a[216:217],v0 offset:16392\n"
MF "v[92:95],a[200:203],a[76:79],v[92:95]\n"
TR "a[218:219],v0 offset:16904\n"
MF "v[84:87],a[200:203],a[72:75],v[84:87]\n"
LD "v12,s[28:31],s82 offen offset:2048 lds\n"
MF "v[84:87],a[204:207],a[112:115],v[84:87]\n"
TR "a[220:221],v0 offset:20488\n"
MF "v[76:79],a[204:207],a[108:111],v[76:79]\n"
TR "a[222:223],v0 offset:21000\n"
MF "v[76:79],a[200:203],a[68:71],v[76:79]\n"
MF "v[68:71],a[200:203],a[64:67],v[68:71]\n"
MF "v[68:71],a[204:207],a[104:107],v[68:71]\n"
MF "v[60:63],a[204:207],a[100:103],v[60:63]\n"
LD "v13,s[28:31],s82 offen offset:3072 lds\n"
MF "v[60:63],a[200:203],a[60:63],v[60:63]\n"
"s_waitcnt lgkmcnt(4)\n"
MF "v[96:99],a[208:211],a[60:63],v[96:99]\n"
TR "a[200:201],v1 offset:16384\n"
MF "v[96:99],a[212:215],a[100:103],v[96:99]\n"
TR "a[202:203],v1 offset:16896\n"
MF "v[104:107],a[212:215],a[104:107],v[104:107]\n"
"s_add_u32 s14,s81,896\n"
"s_sub_u32 s15,s14,1280\n"
MF "v[104:107],a[208:211],a[64:67],v[104:107]\n"
"s_min_u32 s14,s14,s15\n"
"s_add_u32 s14,s14,s68\n"
TR "a[204:205],v1 offset:20480\n"
MF "v[112:115],a[208:211],a[68:71],v[112:115]\n"
"s_add_u32 s84,s14,0\n"
"s_add_u32 m0,s63,20480\n"
TR "a[206:207],v1 offset:20992\n"
MF "v[112:115],a[212:215],a[108:111],v[112:115]\n"
LD "v4,s[24:27],s84 offen lds\n"
MF "v[120:123],a[212:215],a[112:115],v[120:123]\n"
MF "v[120:123],a[208:211],a[72:75],v[120:123]\n"
MF "v[128:131],a[208:211],a[76:79],v[128:131]\n"
"s_add_u32 s84,s14,9216\n"
MF "v[128:131],a[212:215],a[116:119],v[128:131]\n"
"s_waitcnt lgkmcnt(4)\n"
LD "v5,s[24:27],s84 offen offset:1024 lds\n"
MF "v[132:135],a[220:223],a[116:119],v[132:135]\n"
TR "a[208:209],v1 offset:16392\n"
MF "v[132:135],a[216:219],a[76:79],v[132:135]\n"
TR "a[210:211],v1 offset:16904\n"
MF "v[124:127],a[216:219],a[72:75],v[124:127]\n"
MF "v[124:127],a[220:223],a[112:115],v[124:127]\n"
TR "a[212:213],v1 offset:20488\n"
MF "v[116:119],a[220:223],a[108:111],v[116:119]\n"
TR "a[214:215],v1 offset:21000\n"
MF "v[116:119],a[216:219],a[68:71],v[116:119]\n"
MF "v[108:111],a[216:219],a[64:67],v[108:111]\n"
MF "v[108:111],a[220:223],a[104:107],v[108:111]\n"
MF "v[100:103],a[220:223],a[100:103],v[100:103]\n"
MF "v[100:103],a[216:219],a[60:63],v[100:103]\n"
"s_waitcnt vmcnt(23) lgkmcnt(0)\n"
"s_barrier\n"
MF "v[136:139],a[200:203],a[60:63],v[136:139]\n"
TR "a[216:217],v0 offset:32768\n"
MF "v[136:139],a[204:207],a[100:103],v[136:139]\n"
"s_add_u32 m0,s62,16384\n"
TR "a[218:219],v0 offset:33280\n"
MF "v[144:147],a[204:207],a[104:107],v[144:147]\n"
"s_add_u32 s82,s66,4718720\n"
"ds_read_b128 a[140:143],v2 offset:40960\n"
MF "v[144:147],a[200:203],a[64:67],v[144:147]\n"
"s_sub_u32 s15,s82,7864320\n"
TR "a[220:221],v0 offset:36864\n"
MF "v[152:155],a[200:203],a[68:71],v[152:155]\n"
"s_min_u32 s82,s82,s15\n"
TR "a[222:223],v0 offset:37376\n"
MF "v[152:155],a[204:207],a[108:111],v[152:155]\n"
LD "v6,s[28:31],s82 offen lds\n"
MF "v[160:163],a[204:207],a[112:115],v[160:163]\n"
"ds_read_b128 a[180:183],v3 offset:40960\n"
MF "v[160:163],a[200:203],a[72:75],v[160:163]\n"
LD "v11,s[28:31],s82 offen offset:1024 lds\n"
MF "v[168:171],a[200:203],a[76:79],v[168:171]\n"
MF "v[168:171],a[204:207],a[116:119],v[168:171]\n"
LD "v12,s[28:31],s82 offen offset:2048 lds\n"
MF "v[172:175],a[212:215],a[116:119],v[172:175]\n"
TR "a[200:201],v0 offset:32776\n"
MF "v[172:175],a[208:211],a[76:79],v[172:175]\n"
TR "a[202:203],v0 offset:33288\n"
MF "v[164:167],a[208:211],a[72:75],v[164:167]\n"
"ds_read_b128 a[144:147],v2 offset:43008\n"
MF "v[164:167],a[212:215],a[112:115],v[164:167]\n"
TR "a[204:205],v0 offset:36872\n"
MF "v[156:159],a[212:215],a[108:111],v[156:159]\n"
TR "a[206:207],v0 offset:37384\n"
MF "v[156:159],a[208:211],a[68:71],v[156:159]\n"
MF "v[148:151],a[208:211],a[64:67],v[148:151]\n"
"ds_read_b128 a[184:187],v3 offset:43008\n"
MF "v[148:151],a[212:215],a[104:107],v[148:151]\n"
MF "v[140:143],a[212:215],a[100:103],v[140:143]\n"
LD "v13,s[28:31],s82 offen offset:3072 lds\n"
MF "v[140:143],a[208:211],a[60:63],v[140:143]\n"
"s_waitcnt lgkmcnt(7)\n"
MF "v[176:179],a[216:219],a[60:63],v[176:179]\n"
TR "a[208:209],v1 offset:32768\n"
MF "v[176:179],a[220:223],a[100:103],v[176:179]\n"
TR "a[210:211],v1 offset:33280\n"
MF "v[184:187],a[220:223],a[104:107],v[184:187]\n"
"ds_read_b128 a[148:151],v2 offset:45056\n"
MF "v[184:187],a[216:219],a[64:67],v[184:187]\n"
"s_add_u32 s84,s14,20480\n"
TR "a[212:213],v1 offset:36864\n"
MF "v[192:195],a[216:219],a[68:71],v[192:195]\n"
"s_add_u32 m0,s63,22528\n"
TR "a[214:215],v1 offset:37376\n"
MF "v[192:195],a[220:223],a[108:111],v[192:195]\n"
LD "v4,s[24:27],s84 offen lds\n"
MF "v[200:203],a[220:223],a[112:115],v[200:203]\n"
"ds_read_b128 a[188:191],v3 offset:45056\n"
MF "v[200:203],a[216:219],a[72:75],v[200:203]\n"
MF "v[208:211],a[216:219],a[76:79],v[208:211]\n"
"s_add_u32 s84,s14,29696\n"
MF "v[208:211],a[220:223],a[116:119],v[208:211]\n"
"s_waitcnt lgkmcnt(7)\n"
LD "v5,s[24:27],s84 offen offset:1024 lds\n"
MF "v[212:215],a[204:207],a[116:119],v[212:215]\n"
TR "a[216:217],v1 offset:32776\n"
MF "v[212:215],a[200:203],a[76:79],v[212:215]\n"
TR "a[218:219],v1 offset:33288\n"
MF "v[204:207],a[200:203],a[72:75],v[204:207]\n"
"ds_read_b128 a[152:155],v2 offset:47104\n"
MF "v[204:207],a[204:207],a[112:115],v[204:207]\n"
TR "a[220:221],v1 offset:36872\n"
MF "v[196:199],a[204:207],a[108:111],v[196:199]\n"
TR "a[222:223],v1 offset:37384\n"
MF "v[196:199],a[200:203],a[68:71],v[196:199]\n"
MF "v[188:191],a[200:203],a[64:67],v[188:191]\n"
"v_add_u32 v0,0xc000,v0\n"
MF "v[188:191],a[204:207],a[104:107],v[188:191]\n"
"v_add_u32 v1,0xc000,v1\n"
MF "v[180:183],a[204:207],a[100:103],v[180:183]\n"
MF "v[180:183],a[200:203],a[60:63],v[180:183]\n"
"s_waitcnt vmcnt(25) lgkmcnt(0)\n"
"s_barrier\n"
MF "v[216:219],a[208:211],a[60:63],v[216:219]\n"
TR "a[200:201],v0\n"
MF "v[216:219],a[212:215],a[100:103],v[216:219]\n"
"s_add_u32 m0,s62,32768\n"
TR "a[202:203],v0 offset:512\n"
MF "v[224:227],a[212:215],a[104:107],v[224:227]\n"
"s_add_u32 s82,s66,4718848\n"
"ds_read_b128 a[192:195],v3 offset:47104\n"
MF "v[224:227],a[208:211],a[64:67],v[224:227]\n"
"s_sub_u32 s15,s82,7864320\n"
TR "a[204:205],v0 offset:4096\n"
MF "v[232:235],a[208:211],a[68:71],v[232:235]\n"
"s_min_u32 s82,s82,s15\n"
TR "a[206:207],v0 offset:4608\n"
MF "v[232:235],a[212:215],a[108:111],v[232:235]\n"
LD "v6,s[28:31],s82 offen lds\n"
MF "v[240:243],a[212:215],a[112:115],v[240:243]\n"
"ds_read_b128 a[156:159],v2 offset:49152\n"
MF "v[240:243],a[208:211],a[72:75],v[240:243]\n"
"ds_read_b128 a[196:199],v3 offset:49152\n"
MF "v[248:251],a[208:211],a[76:79],v[248:251]\n"
LD "v11,s[28:31],s82 offen offset:1024 lds\n"
MF "v[248:251],a[212:215],a[116:119],v[248:251]\n"
MF "v[252:255],a[220:223],a[116:119],v[252:255]\n"
TR "a[208:209],v0 offset:8\n"
MF "v[252:255],a[216:219],a[76:79],v[252:255]\n"
TR "a[210:211],v0 offset:520\n"
MF "v[244:247],a[216:219],a[72:75],v[244:247]\n"
MF "v[244:247],a[220:223],a[112:115],v[244:247]\n"
TR "a[212:213],v0 offset:4104\n"
MF "v[236:239],a[220:223],a[108:111],v[236:239]\n"
TR "a[214:215],v0 offset:4616\n"
MF "v[236:239],a[216:219],a[68:71],v[236:239]\n"
LD "v12,s[28:31],s82 offen offset:2048 lds\n"
MF "v[228:231],a[216:219],a[64:67],v[228:231]\n"
MF "v[228:231],a[220:223],a[104:107],v[228:231]\n"
MF "v[220:223],a[220:223],a[100:103],v[220:223]\n"
MF "v[220:223],a[216:219],a[60:63],v[220:223]\n"
"s_waitcnt lgkmcnt(4)\n"
MF "v[16:19],a[200:203],a[140:143],v[16:19]\n"
TR "a[216:217],v1\n"
MF "v[16:19],a[204:207],a[180:183],v[16:19]\n"
TR "a[218:219],v1 offset:512\n"
MF "v[24:27],a[204:207],a[184:187],v[24:27]\n"
LD "v13,s[28:31],s82 offen offset:3072 lds\n"
MF "v[24:27],a[200:203],a[144:147],v[24:27]\n"
TR "a[220:221],v1 offset:4096\n"
MF "v[32:35],a[200:203],a[148:151],v[32:35]\n"
TR "a[222:223],v1 offset:4608\n"
MF "v[32:35],a[204:207],a[188:191],v[32:35]\n"
MF "v[40:43],a[204:207],a[192:195],v[40:43]\n"
MF "v[40:43],a[200:203],a[152:155],v[40:43]\n"
"s_add_u32 s84,s14,40960\n"
MF "v[48:51],a[200:203],a[156:159],v[48:51]\n"
"s_add_u32 m0,s63,24576\n"
MF "v[48:51],a[204:207],a[196:199],v[48:51]\n"
"s_waitcnt lgkmcnt(4)\n"
LD "v4,s[24:27],s84 offen lds\n"
MF "v[52:55],a[212:215],a[196:199],v[52:55]\n"
TR "a[200:201],v1 offset:8\n"
MF "v[52:55],a[208:211],a[156:159],v[52:55]\n"
TR "a[202:203],v1 offset:520\n"
MF "v[44:47],a[208:211],a[152:155],v[44:47]\n"
MF "v[44:47],a[212:215],a[192:195],v[44:47]\n"
TR "a[204:205],v1 offset:4104\n"
MF "v[36:39],a[212:215],a[188:191],v[36:39]\n"
TR "a[206:207],v1 offset:4616\n"
MF "v[36:39],a[208:211],a[148:151],v[36:39]\n"
MF "v[28:31],a[208:211],a[144:147],v[28:31]\n"
MF "v[28:31],a[212:215],a[184:187],v[28:31]\n"
MF "v[20:23],a[212:215],a[180:183],v[20:23]\n"
MF "v[20:23],a[208:211],a[140:143],v[20:23]\n"
".Lpg6c:\n"
"s_cbranch_execz .Lpg1a\n"
".Lpg6f:\n"
"s_cbranch_execz .Lpg15e\n"
"s_waitcnt vmcnt(24) lgkmcnt(0)\n"
"s_barrier\n"
MF "v[56:59],a[216:219],a[140:143],v[56:59]\n"
"s_add_u32 m0,s62,49152\n"
"s_add_u32 s82,s66,5505024\n"
TR "a[208:209],v0 offset:16384\n"
MF "v[56:59],a[220:223],a[180:183],v[56:59]\n"
"s_sub_u32 s15,s82,7864320\n"
"s_min_u32 s82,s82,s15\n"
TR "a[210:211],v0 offset:16896\n"
MF "v[64:67],a[220:223],a[184:187],v[64:67]\n"
LD "v6,s[28:31],s82 offen lds\n"
MF "v[64:67],a[216:219],a[144:147],v[64:67]\n"
TR "a[212:213],v0 offset:20480\n"
MF "v[72:75],a[216:219],a[148:151],v[72:75]\n"
TR "a[214:215],v0 offset:20992\n"
MF "v[72:75],a[220:223],a[188:191],v[72:75]\n"
MF "v[80:83],a[220:223],a[192:195],v[80:83]\n"
LD "v11,s[28:31],s82 offen offset:1024 lds\n"
MF "v[80:83],a[216:219],a[152:155],v[80:83]\n"
MF "v[88:91],a[216:219],a[156:159],v[88:91]\n"
MF "v[88:91],a[220:223],a[196:199],v[88:91]\n"
MF "v[92:95],a[204:207],a[196:199],v[92:95]\n"
TR "a[216:217],v0 offset:16392\n"
MF "v[92:95],a[200:203],a[156:159],v[92:95]\n"
TR "a[218:219],v0 offset:16904\n"
MF "v[84:87],a[200:203],a[152:155],v[84:87]\n"
LD "v12,s[28:31],s82 offen offset:2048 lds\n"
MF "v[84:87],a[204:207],a[192:195],v[84:87]\n"
TR "a[220:221],v0 offset:20488\n"
MF "v[76:79],a[204:207],a[188:191],v[76:79]\n"
TR "a[222:223],v0 offset:21000\n"
MF "v[76:79],a[200:203],a[148:151],v[76:79]\n"
MF "v[68:71],a[200:203],a[144:147],v[68:71]\n"
MF "v[68:71],a[204:207],a[184:187],v[68:71]\n"
MF "v[60:63],a[204:207],a[180:183],v[60:63]\n"
LD "v13,s[28:31],s82 offen offset:3072 lds\n"
MF "v[60:63],a[200:203],a[140:143],v[60:63]\n"
"s_waitcnt lgkmcnt(4)\n"
MF "v[96:99],a[208:211],a[140:143],v[96:99]\n"
TR "a[200:201],v1 offset:16384\n"
MF "v[96:99],a[212:215],a[180:183],v[96:99]\n"
TR "a[202:203],v1 offset:16896\n"
MF "v[104:107],a[212:215],a[184:187],v[104:107]\n"
"s_add_u32 s14,s81,1024\n"
"s_sub_u32 s15,s14,1280\n"
MF "v[104:107],a[208:211],a[144:147],v[104:107]\n"
"s_min_u32 s14,s14,s15\n"
"s_add_u32 s14,s14,s68\n"
TR "a[204:205],v1 offset:20480\n"
MF "v[112:115],a[208:211],a[148:151],v[112:115]\n"
"s_add_u32 s84,s14,0\n"
"s_add_u32 m0,s63,40960\n"
TR "a[206:207],v1 offset:20992\n"
MF "v[112:115],a[212:215],a[188:191],v[112:115]\n"
LD "v4,s[24:27],s84 offen lds\n"
MF "v[120:123],a[212:215],a[192:195],v[120:123]\n"
MF "v[120:123],a[208:211],a[152:155],v[120:123]\n"
MF "v[128:131],a[208:211],a[156:159],v[128:131]\n"
"s_add_u32 s84,s14,9216\n"
MF "v[128:131],a[212:215],a[196:199],v[128:131]\n"
"s_waitcnt lgkmcnt(4)\n"
LD "v5,s[24:27],s84 offen offset:1024 lds\n"
MF "v[132:135],a[220:223],a[196:199],v[132:135]\n"
TR "a[208:209],v1 offset:16392\n"
MF "v[132:135],a[216:219],a[156:159],v[132:135]\n"
TR "a[210:211],v1 offset:16904\n"
MF "v[124:127],a[216:219],a[152:155],v[124:127]\n"
MF "v[124:127],a[220:223],a[192:195],v[124:127]\n"
TR "a[212:213],v1 offset:20488\n"
MF "v[116:119],a[220:223],a[188:191],v[116:119]\n"
TR "a[214:215],v1 offset:21000\n"
MF "v[116:119],a[216:219],a[148:151],v[116:119]\n"
MF "v[108:111],a[216:219],a[144:147],v[108:111]\n"
MF "v[108:111],a[220:223],a[184:187],v[108:111]\n"
MF "v[100:103],a[220:223],a[180:183],v[100:103]\n"
MF "v[100:103],a[216:219],a[140:143],v[100:103]\n"
"s_waitcnt vmcnt(23) lgkmcnt(0)\n"
"s_barrier\n"
"s_cmp_eq_u32 s57,1\n"
"s_cbranch_scc0 .Lhs12\n"
"v_bfe_u32 v14,v9,4,5\n"
"v_lshlrev_b32 v14,6,v14\n"
"s_lshl_b32 s58,s78,2\n"
"s_add_u32 s58,s58,s86\n"
"buffer_load_dword v10,v14,s[40:43],s58 offen offset:128 sc1\n"
".Lhs12:\n"
MF "v[136:139],a[200:203],a[140:143],v[136:139]\n"
TR "a[216:217],v0 offset:32768\n"
MF "v[136:139],a[204:207],a[180:183],v[136:139]\n"
"s_add_u32 m0,s62,65536\n"
TR "a[218:219],v0 offset:33280\n"
MF "v[144:147],a[204:207],a[184:187],v[144:147]\n"
"s_add_u32 s82,s66,5505152\n"
"ds_read_b128 a[60:63],v2\n"
MF "v[144:147],a[200:203],a[144:147],v[144:147]\n"
"s_sub_u32 s15,s82,7864320\n"
TR "a[220:221],v0 offset:36864\n"
MF "v[152:155],a[200:203],a[148:151],v[152:155]\n"
"s_min_u32 s82,s82,s15\n"
TR "a[222:223],v0 offset:37376\n"
MF "v[152:155],a[204:207],a[188:191],v[152:155]\n"
LD "v6,s[28:31],s82 offen lds\n"
MF "v[160:163],a[204:207],a[192:195],v[160:163]\n"
"ds_read_b128 a[100:103],v3\n"
MF "v[160:163],a[200:203],a[152:155],v[160:163]\n"
LD "v11,s[28:31],s82 offen offset:1024 lds\n"
MF "v[168:171],a[200:203],a[156:159],v[168:171]\n"
MF "v[168:171],a[204:207],a[196:199],v[168:171]\n"
LD "v12,s[28:31],s82 offen offset:2048 lds\n"
MF "v[172:175],a[212:215],a[196:199],v[172:175]\n"
TR "a[200:201],v0 offset:32776\n"
MF "v[172:175],a[208:211],a[156:159],v[172:175]\n"
TR "a[202:203],v0 offset:33288\n"
MF "v[164:167],a[208:211],a[152:155],v[164:167]\n"
"ds_read_b128 a[64:67],v2 offset:2048\n"
MF "v[164:167],a[212:215],a[192:195],v[164:167]\n"
TR "a[204:205],v0 offset:36872\n"
MF "v[156:159],a[212:215],a[188:191],v[156:159]\n"
TR "a[206:207],v0 offset:37384\n"
MF "v[156:159],a[208:211],a[148:151],v[156:159]\n"
MF "v[148:151],a[208:211],a[144:147],v[148:151]\n"
"ds_read_b128 a[104:107],v3 offset:2048\n"
MF "v[148:151],a[212:215],a[184:187],v[148:151]\n"
MF "v[140:143],a[212:215],a[180:183],v[140:143]\n"
LD "v13,s[28:31],s82 offen offset:3072 lds\n"
MF "v[140:143],a[208:211],a[140:143],v[140:143]\n"
"s_waitcnt lgkmcnt(7)\n"
MF "v[176:179],a[216:219],a[140:143],v[176:179]\n"
TR "a[208:209],v1 offset:32768\n"
MF "v[176:179],a[220:223],a[180:183],v[176:179]\n"
TR "a[210:211],v1 offset:33280\n"
MF "v[184:187],a[220:223],a[184:187],v[184:187]\n"
"ds_read_b128 a[68:71],v2 offset:4096\n"
MF "v[184:187],a[216:219],a[144:147],v[184:187]\n"
"s_add_u32 s84,s14,20480\n"
TR "a[212:213],v1 offset:36864\n"
MF "v[192:195],a[216:219],a[148:151],v[192:195]\n"
"s_add_u32 m0,s63,43008\n"
TR "a[214:215],v1 offset:37376\n"
MF "v[192:195],a[220:223],a[188:191],v[192:195]\n"
LD "v4,s[24:27],s84 offen lds\n"
MF "v[200:203],a[220:223],a[192:195],v[200:203]\n"
"ds_read_b128 a[108:111],v3 offset:4096\n"
MF "v[200:203],a[216:219],a[152:155],v[200:203]\n"
MF "v[208:211],a[216:219],a[156:159],v[208:211]\n"
"s_add_u32 s84,s14,29696\n"
MF "v[208:211],a[220:223],a[196:199],v[208:211]\n"
"s_waitcnt lgkmcnt(7)\n"
LD "v5,s[24:27],s84 offen offset:1024 lds\n"
MF "v[212:215],a[204:207],a[196:199],v[212:215]\n"
TR "a[216:217],v1 offset:32776\n"
MF "v[212:215],a[200:203],a[156:159],v[212:215]\n"
TR "a[218:219],v1 offset:33288\n"
MF "v[204:207],a[200:203],a[152:155],v[204:207]\n"
"ds_read_b128 a[72:75],v2 offset:6144\n"
MF "v[204:207],a[204:207],a[192:195],v[204:207]\n"
TR "a[220:221],v1 offset:36872\n"
MF "v[196:199],a[204:207],a[188:191],v[196:199]\n"
TR "a[222:223],v1 offset:37384\n"
MF "v[196:199],a[200:203],a[148:151],v[196:199]\n"
MF "v[188:191],a[200:203],a[144:147],v[188:191]\n"
"v_add_u32 v0,0xffff4000,v0\n"
MF "v[188:191],a[204:207],a[184:187],v[188:191]\n"
"v_add_u32 v1,0xffff4000,v1\n"
MF "v[180:183],a[204:207],a[180:183],v[180:183]\n"
MF "v[180:183],a[200:203],a[140:143],v[180:183]\n"
"s_waitcnt vmcnt(25) lgkmcnt(0)\n"
"s_barrier\n"
MF "v[216:219],a[208:211],a[140:143],v[216:219]\n"
TR "a[200:201],v0\n"
MF "v[216:219],a[212:215],a[180:183],v[216:219]\n"
"s_add_u32 m0,s62,81920\n"
TR "a[202:203],v0 offset:512\n"
MF "v[224:227],a[212:215],a[184:187],v[224:227]\n"
"s_add_u32 s82,s66,5505280\n"
"ds_read_b128 a[112:115],v3 offset:6144\n"
MF "v[224:227],a[208:211],a[144:147],v[224:227]\n"
"s_sub_u32 s15,s82,7864320\n"
TR "a[204:205],v0 offset:4096\n"
MF "v[232:235],a[208:211],a[148:151],v[232:235]\n"
"s_min_u32 s82,s82,s15\n"
TR "a[206:207],v0 offset:4608\n"
MF "v[232:235],a[212:215],a[188:191],v[232:235]\n"
LD "v6,s[28:31],s82 offen lds\n"
MF "v[240:243],a[212:215],a[192:195],v[240:243]\n"
"ds_read_b128 a[76:79],v2 offset:8192\n"
MF "v[240:243],a[208:211],a[152:155],v[240:243]\n"
"ds_read_b128 a[116:119],v3 offset:8192\n"
MF "v[248:251],a[208:211],a[156:159],v[248:251]\n"
LD "v11,s[28:31],s82 offen offset:1024 lds\n"
MF "v[248:251],a[212:215],a[196:199],v[248:251]\n"
MF "v[252:255],a[220:223],a[196:199],v[252:255]\n"
TR "a[208:209],v0 offset:8\n"
MF "v[252:255],a[216:219],a[156:159],v[252:255]\n"
TR "a[210:211],v0 offset:520\n"
MF "v[244:247],a[216:219],a[152:155],v[244:247]\n"
MF "v[244:247],a[220:223],a[192:195],v[244:247]\n"
TR "a[212:213],v0 offset:4104\n"
MF "v[236:239],a[220:223],a[188:191],v[236:239]\n"
TR "a[214:215],v0 offset:4616\n"
MF "v[236:239],a[216:219],a[148:151],v[236:239]\n"
LD "v12,s[28:31],s82 offen offset:2048 lds\n"
MF "v[228:231],a[216:219],a[144:147],v[228:231]\n"
MF "v[228:231],a[220:223],a[184:187],v[228:231]\n"
MF "v[220:223],a[220:223],a[180:183],v[220:223]\n"
MF "v[220:223],a[216:219],a[140:143],v[220:223]\n"
"s_waitcnt lgkmcnt(4)\n"
MF "v[16:19],a[200:203],a[60:63],v[16:19]\n"
TR "a[216:217],v1\n"
MF "v[16:19],a[204:207],a[100:103],v[16:19]\n"
TR "a[218:219],v1 offset:512\n"
MF "v[24:27],a[204:207],a[104:107],v[24:27]\n"
LD "v13,s[28:31],s82 offen offset:3072 lds\n"
MF "v[24:27],a[200:203],a[64:67],v[24:27]\n"
TR "a[220:221],v1 offset:4096\n"
MF "v[32:35],a[200:203],a[68:71],v[32:35]\n"
TR "a[222:223],v1 offset:4608\n"
MF "v[32:35],a[204:207],a[108:111],v[32:35]\n"
MF "v[40:43],a[204:207],a[112:115],v[40:43]\n"
MF "v[40:43],a[200:203],a[72:75],v[40:43]\n"
"s_add_u32 s84,s14,40960\n"
MF "v[48:51],a[200:203],a[76:79],v[48:51]\n"
"s_add_u32 m0,s63,45056\n"
MF "v[48:51],a[204:207],a[116:119],v[48:51]\n"
"s_waitcnt lgkmcnt(4)\n"
LD "v4,s[24:27],s84 offen lds\n"
MF "v[52:55],a[212:215],a[116:119],v[52:55]\n"
TR "a[200:201],v1 offset:8\n"
MF "v[52:55],a[208:211],a[76:79],v[52:55]\n"
TR "a[202:203],v1 offset:520\n"
MF "v[44:47],a[208:211],a[72:75],v[44:47]\n"
MF "v[44:47],a[212:215],a[112:115],v[44:47]\n"
TR "a[204:205],v1 offset:4104\n"
MF "v[36:39],a[212:215],a[108:111],v[36:39]\n"
TR "a[206:207],v1 offset:4616\n"
MF "v[36:39],a[208:211],a[68:71],v[36:39]\n"
MF "v[28:31],a[208:211],a[64:67],v[28:31]\n"
MF "v[28:31],a[212:215],a[104:107],v[28:31]\n"
MF "v[20:23],a[212:215],a[100:103],v[20:23]\n"
MF "v[20:23],a[208:211],a[60:63],v[20:23]\n"
"s_waitcnt vmcnt(24) lgkmcnt(0)\n"
"s_barrier\n"
MF "v[56:59],a[216:219],a[60:63],v[56:59]\n"
"s_add_u32 m0,s62,0\n"
"s_add_u32 s82,s66,6291712\n"
TR "a[208:209],v0 offset:16384\n"
MF "v[56:59],a[220:223],a[100:103],v[56:59]\n"
"s_sub_u32 s15,s82,7864320\n"
"s_min_u32 s82,s82,s15\n"
TR "a[210:211],v0 offset:16896\n"
MF "v[64:67],a[220:223],a[104:107],v[64:67]\n"
LD "v6,s[28:31],s82 offen lds\n"
MF "v[64:67],a[216:219],a[64:67],v[64:67]\n"
TR "a[212:213],v0 offset:20480\n"
MF "v[72:75],a[216:219],a[68:71],v[72:75]\n"
TR "a[214:215],v0 offset:20992\n"
MF "v[72:75],a[220:223],a[108:111],v[72:75]\n"
MF "v[80:83],a[220:223],a[112:115],v[80:83]\n"
LD "v11,s[28:31],s82 offen offset:1024 lds\n"
MF "v[80:83],a[216:219],a[72:75],v[80:83]\n"
MF "v[88:91],a[216:219],a[76:79],v[88:91]\n"
MF "v[88:91],a[220:223],a[116:119],v[88:91]\n"
MF "v[92:95],a[204:207],a[116:119],v[92:95]\n"
TR "a[216:217],v0 offset:16392\n"
MF "v[92:95],a[200:203],a[76:79],v[92:95]\n"
TR "a[218:219],v0 offset:16904\n"
MF "v[84:87],a[200:203],a[72:75],v[84:87]\n"
LD "v12,s[28:31],s82 offen offset:2048 lds\n"
MF "v[84:87],a[204:207],a[112:115],v[84:87]\n"
TR "a[220:221],v0 offset:20488\n"
MF "v[76:79],a[204:207],a[108:111],v[76:79]\n"
TR "a[222:223],v0 offset:21000\n"
MF "v[76:79],a[200:203],a[68:71],v[76:79]\n"
MF "v[68:71],a[200:203],a[64:67],v[68:71]\n"
LD "v13,s[28:31],s82 offen offset:3072 lds\n"
MF "v[68:71],a[204:207],a[104:107],v[68:71]\n"
MF "v[60:63],a[204:207],a[100:103],v[60:63]\n"
MF "v[60:63],a[200:203],a[60:63],v[60:63]\n"
"s_add_u32 s14,s81,1152\n"
"s_sub_u32 s15,s14,1280\n"
"s_waitcnt lgkmcnt(4)\n"
MF "v[96:99],a[208:211],a[60:63],v[96:99]\n"
"s_min_u32 s14,s14,s15\n"
"s_add_u32 s14,s14,s68\n"
TR "a[200:201],v1 offset:16384\n"
MF "v[96:99],a[212:215],a[100:103],v[96:99]\n"
"s_add_u32 s84,s14,0\n"
"s_add_u32 m0,s63,0\n"
TR "a[202:203],v1 offset:16896\n"
MF "v[104:107],a[212:215],a[104:107],v[104:107]\n"
LD "v4,s[24:27],s84 offen lds\n"
MF "v[104:107],a[208:211],a[64:67],v[104:107]\n"
TR "a[204:205],v1 offset:20480\n"
MF "v[112:115],a[208:211],a[68:71],v[112:115]\n"
TR "a[206:207],v1 offset:20992\n"
MF "v[112:115],a[212:215],a[108:111],v[112:115]\n"
"s_add_u32 s84,s14,9216\n"
MF "v[120:123],a[212:215],a[112:115],v[120:123]\n"
LD "v5,s[24:27],s84 offen offset:1024 lds\n"
MF "v[120:123],a[208:211],a[72:75],v[120:123]\n"
MF "v[128:131],a[208:211],a[76:79],v[128:131]\n"
MF "v[128:131],a[212:215],a[116:119],v[128:131]\n"
"s_waitcnt lgkmcnt(4)\n"
MF "v[132:135],a[220:223],a[116:119],v[132:135]\n"
TR "a[208:209],v1 offset:16392\n"
MF "v[132:135],a[216:219],a[76:79],v[132:135]\n"
"s_add_u32 s84,s14,18432\n"
TR "a[210:211],v1 offset:16904\n"
MF "v[124:127],a[216:219],a[72:75],v[124:127]\n"
LD "v4,s[24:27],s84 offen offset:2048 lds\n"
MF "v[124:127],a[220:223],a[112:115],v[124:127]\n"
TR "a[212:213],v1 offset:20488\n"
MF "v[116:119],a[220:223],a[108:111],v[116:119]\n"
TR "a[214:215],v1 offset:21000\n"
MF "v[116:119],a[216:219],a[68:71],v[116:119]\n"
MF "v[108:111],a[216:219],a[64:67],v[108:111]\n"
MF "v[108:111],a[220:223],a[104:107],v[108:111]\n"
MF "v[100:103],a[220:223],a[100:103],v[100:103]\n"
MF "v[100:103],a[216:219],a[60:63],v[100:103]\n"
"s_waitcnt vmcnt(24) lgkmcnt(0)\n"
"s_barrier\n"
MF "v[136:139],a[200:203],a[60:63],v[136:139]\n"
TR "a[216:217],v0 offset:32768\n"
MF "v[136:139],a[204:207],a[100:103],v[136:139]\n"
"s_add_u32 m0,s62,16384\n"
TR "a[218:219],v0 offset:33280\n"
MF "v[144:147],a[204:207],a[104:107],v[144:147]\n"
"s_add_u32 s82,s66,7078144\n"
"ds_read_b128 a[140:143],v2 offset:20480\n"
MF "v[144:147],a[200:203],a[64:67],v[144:147]\n"
"s_sub_u32 s15,s82,7864320\n"
TR "a[220:221],v0 offset:36864\n"
MF "v[152:155],a[200:203],a[68:71],v[152:155]\n"
"s_min_u32 s82,s82,s15\n"
TR "a[222:223],v0 offset:37376\n"
MF "v[152:155],a[204:207],a[108:111],v[152:155]\n"
LD "v6,s[28:31],s82 offen lds\n"
MF "v[160:163],a[204:207],a[112:115],v[160:163]\n"
"ds_read_b128 a[180:183],v3 offset:20480\n"
MF "v[160:163],a[200:203],a[72:75],v[160:163]\n"
LD "v11,s[28:31],s82 offen offset:1024 lds\n"
MF "v[168:171],a[200:203],a[76:79],v[168:171]\n"
MF "v[168:171],a[204:207],a[116:119],v[168:171]\n"
LD "v12,s[28:31],s82 offen offset:2048 lds\n"
MF "v[172:175],a[212:215],a[116:119],v[172:175]\n"
TR "a[200:201],v0 offset:32776\n"
MF "v[172:175],a[208:211],a[76:79],v[172:175]\n"
TR "a[202:203],v0 offset:33288\n"
MF "v[164:167],a[208:211],a[72:75],v[164:167]\n"
"ds_read_b128 a[144:147],v2 offset:22528\n"
MF "v[164:167],a[212:215],a[112:115],v[164:167]\n"
TR "a[204:205],v0 offset:36872\n"
MF "v[156:159],a[212:215],a[108:111],v[156:159]\n"
TR "a[206:207],v0 offset:37384\n"
MF "v[156:159],a[208:211],a[68:71],v[156:159]\n"
MF "v[148:151],a[208:211],a[64:67],v[148:151]\n"
"ds_read_b128 a[184:187],v3 offset:22528\n"
MF "v[148:151],a[212:215],a[104:107],v[148:151]\n"
MF "v[140:143],a[212:215],a[100:103],v[140:143]\n"
LD "v13,s[28:31],s82 offen offset:3072 lds\n"
MF "v[140:143],a[208:211],a[60:63],v[140:143]\n"
"s_waitcnt lgkmcnt(7)\n"
MF "v[176:179],a[216:219],a[60:63],v[176:179]\n"
TR "a[208:209],v1 offset:32768\n"
MF "v[176:179],a[220:223],a[100:103],v[176:179]\n"
TR "a[210:211],v1 offset:33280\n"
MF "v[184:187],a[220:223],a[104:107],v[184:187]\n"
"ds_read_b128 a[148:151],v2 offset:24576\n"
MF "v[184:187],a[216:219],a[64:67],v[184:187]\n"
"s_add_u32 s84,s14,30720\n"
TR "a[212:213],v1 offset:36864\n"
MF "v[192:195],a[216:219],a[68:71],v[192:195]\n"
"s_add_u32 m0,s63,3072\n"
TR "a[214:215],v1 offset:37376\n"
MF "v[192:195],a[220:223],a[108:111],v[192:195]\n"
LD "v5,s[24:27],s84 offen lds\n"
MF "v[200:203],a[220:223],a[112:115],v[200:203]\n"
"ds_read_b128 a[188:191],v3 offset:24576\n"
MF "v[200:203],a[216:219],a[72:75],v[200:203]\n"
MF "v[208:211],a[216:219],a[76:79],v[208:211]\n"
"s_add_u32 s84,s14,39936\n"
MF "v[208:211],a[220:223],a[116:119],v[208:211]\n"
"s_waitcnt lgkmcnt(7)\n"
LD "v4,s[24:27],s84 offen offset:1024 lds\n"
MF "v[212:215],a[204:207],a[116:119],v[212:215]\n"
TR "a[216:217],v1 offset:32776\n"
MF "v[212:215],a[200:203],a[76:79],v[212:215]\n"
TR "a[218:219],v1 offset:33288\n"
MF "v[204:207],a[200:203],a[72:75],v[204:207]\n"
"ds_read_b128 a[152:155],v2 offset:26624\n"
MF "v[204:207],a[204:207],a[112:115],v[204:207]\n"
TR "a[220:221],v1 offset:36872\n"
MF "v[196:199],a[204:207],a[108:111],v[196:199]\n"
TR "a[222:223],v1 offset:37384\n"
MF "v[196:199],a[200:203],a[68:71],v[196:199]\n"
MF "v[188:191],a[200:203],a[64:67],v[188:191]\n"
"v_add_u32 v0,0xc000,v0\n"
MF "v[188:191],a[204:207],a[104:107],v[188:191]\n"
"v_add_u32 v1,0xc000,v1\n"
MF "v[180:183],a[204:207],a[100:103],v[180:183]\n"
".Lpgqj_a:\n"
"s_cbranch_execz .Lisr1\n"
".Lqj:\n"
MF "v[180:183],a[200:203],a[60:63],v[180:183]\n"
"s_waitcnt vmcnt(26) lgkmcnt(0)\n"
"s_barrier\n"
"s_getpc_b64 s[52:53]\n"
".Lctq:\n"
"s_sub_u32 s52,s52,.Lctq-.Lcode0\n"
"s_subb_u32 s53,s53,0\n"
"s_bfe_u32 s58,s23,0x20000\n"
"s_lshl_b32 s58,s58,2\n"
"s_add_u32 s58,s58,s56\n"
"s_lshl_b32 s58,s58,13\n"
"v_lshlrev_b32 v15,3,v9\n"
"v_add_u32 v15,s58,v15\n"
"v_min_u32 v15,.Lcode1-.Lcode0,v15\n"
"global_load_dword a249,v15,s[52:53]\n"
MF "v[216:219],a[208:211],a[60:63],v[216:219]\n"
TR "a[200:201],v0\n"
MF "v[216:219],a[212:215],a[100:103],v[216:219]\n"
"s_add_u32 m0,s62,32768\n"
TR "a[202:203],v0 offset:512\n"
MF "v[224:227],a[212:215],a[104:107],v[224:227]\n"
"s_add_u32 s82,s66,6291584\n"
"ds_read_b128 a[192:195],v3 offset:26624\n"
MF "v[224:227],a[208:211],a[64:67],v[224:227]\n"
"s_sub_u32 s15,s82,7864320\n"
TR "a[204:205],v0 offset:4096\n"
MF "v[232:235],a[208:211],a[68:71],v[232:235]\n"
"s_min_u32 s82,s82,s15\n"
TR "a[206:207],v0 offset:4608\n"
MF "v[232:235],a[212:215],a[108:111],v[232:235]\n"
LD "v6,s[28:31],s82 offen lds\n"
MF "v[240:243],a[212:215],a[112:115],v[240:243]\n"
"ds_read_b128 a[156:159],v2 offset:28672\n"
MF "v[240:243],a[208:211],a[72:75],v[240:243]\n"
"ds_read_b128 a[196:199],v3 offset:28672\n"
MF "v[248:251],a[208:211],a[76:79],v[248:251]\n"
MF "v[248:251],a[212:215],a[116:119],v[248:251]\n"
LD "v11,s[28:31],s82 offen offset:1024 lds\n"
MF "v[252:255],a[220:223],a[116:119],v[252:255]\n"
TR "a[208:209],v0 offset:8\n"
MF "v[252:255],a[216:219],a[76:79],v[252:255]\n"
TR "a[210:211],v0 offset:520\n"
MF "v[244:247],a[216:219],a[72:75],v[244:247]\n"
MF "v[244:247],a[220:223],a[112:115],v[244:247]\n"
TR "a[212:213],v0 offset:4104\n"
MF "v[236:239],a[220:223],a[108:111],v[236:239]\n"
TR "a[214:215],v0 offset:4616\n"
MF "v[236:239],a[216:219],a[68:71],v[236:239]\n"
MF "v[228:231],a[216:219],a[64:67],v[228:231]\n"
MF "v[228:231],a[220:223],a[104:107],v[228:231]\n"
MF "v[220:223],a[220:223],a[100:103],v[220:223]\n"
LD "v12,s[28:31],s82 offen offset:2048 lds\n"
MF "v[220:223],a[216:219],a[60:63],v[220:223]\n"
"s_waitcnt lgkmcnt(4)\n"
MF "v[16:19],a[200:203],a[140:143],v[16:19]\n"
TR "a[216:217],v1\n"
MF "v[16:19],a[204:207],a[180:183],v[16:19]\n"
TR "a[218:219],v1 offset:512\n"
MF "v[24:27],a[204:207],a[184:187],v[24:27]\n"
MF "v[24:27],a[200:203],a[144:147],v[24:27]\n"
TR "a[220:221],v1 offset:4096\n"
MF "v[32:35],a[200:203],a[148:151],v[32:35]\n"
TR "a[222:223],v1 offset:4608\n"
MF "v[32:35],a[204:207],a[188:191],v[32:35]\n"
MF "v[40:43],a[204:207],a[192:195],v[40:43]\n"
MF "v[40:43],a[200:203],a[152:155],v[40:43]\n"
LD "v13,s[28:31],s82 offen offset:3072 lds\n"
MF "v[48:51],a[200:203],a[156:159],v[48:51]\n"
MF "v[48:51],a[204:207],a[196:199],v[48:51]\n"
"s_waitcnt lgkmcnt(4)\n"
MF "v[52:55],a[212:215],a[196:199],v[52:55]\n"
TR "a[200:201],v1 offset:8\n"
MF "v[52:55],a[208:211],a[156:159],v[52:55]\n"
TR "a[202:203],v1 offset:520\n"
MF "v[44:47],a[208:211],a[152:155],v[44:47]\n"
MF "v[44:47],a[212:215],a[192:195],v[44:47]\n"
TR "a[204:205],v1 offset:4104\n"
MF "v[36:39],a[212:215],a[188:191],v[36:39]\n"
TR "a[206:207],v1 offset:4616\n"
MF "v[36:39],a[208:211],a[148:151],v[36:39]\n"
MF "v[28:31],a[208:211],a[144:147],v[28:31]\n"
MF "v[28:31],a[212:215],a[184:187],v[28:31]\n"
MF "v[20:23],a[212:215],a[180:183],v[20:23]\n"
MF "v[20:23],a[208:211],a[140:143],v[20:23]\n"
"s_waitcnt vmcnt(25) lgkmcnt(0)\n"
"s_barrier\n"
MF "v[56:59],a[216:219],a[140:143],v[56:59]\n"
"s_add_u32 m0,s62,49152\n"
"s_add_u32 s82,s66,7078016\n"
TR "a[208:209],v0 offset:16384\n"
MF "v[56:59],a[220:223],a[180:183],v[56:59]\n"
"s_sub_u32 s15,s82,7864320\n"
"s_min_u32 s82,s82,s15\n"
TR "a[210:211],v0 offset:16896\n"
MF "v[64:67],a[220:223],a[184:187],v[64:67]\n"
LD "v6,s[28:31],s82 offen lds\n"
MF "v[64:67],a[216:219],a[144:147],v[64:67]\n"
TR "a[212:213],v0 offset:20480\n"
"v_lshrrev_b32 v15,8,v9\n"
MF "v[72:75],a[216:219],a[148:151],v[72:75]\n"
TR "a[214:215],v0 offset:20992\n"
"v_mul_u32_u24 v15,48960,v15\n"
MF "v[72:75],a[220:223],a[188:191],v[72:75]\n"
"s_lshr_b32 s7,s56,1\n"
"v_add_u32 v15,v15,v9\n"
MF "v[80:83],a[220:223],a[192:195],v[80:83]\n"
"s_and_b32 s8,s56,1\n"
MF "v[80:83],a[216:219],a[152:155],v[80:83]\n"
"s_sub_u32 s7,s7,s8\n"
MF "v[88:91],a[216:219],a[156:159],v[88:91]\n"
"s_mul_i32 s7,s7,245760\n"
MF "v[88:91],a[220:223],a[196:199],v[88:91]\n"
LD "v11,s[28:31],s82 offen offset:1024 lds\n"
MF "v[92:95],a[204:207],a[196:199],v[92:95]\n"
"s_add_u32 s7,s7,s89\n"
TR "a[216:217],v0 offset:16392\n"
MF "v[92:95],a[200:203],a[156:159],v[92:95]\n"
TR "a[218:219],v0 offset:16904\n"
MF "v[84:87],a[200:203],a[152:155],v[84:87]\n"
MF "v[84:87],a[204:207],a[192:195],v[84:87]\n"
TR "a[220:221],v0 offset:20488\n"
MF "v[76:79],a[204:207],a[188:191],v[76:79]\n"
TR "a[222:223],v0 offset:21000\n"
MF "v[76:79],a[200:203],a[148:151],v[76:79]\n"
MF "v[68:71],a[200:203],a[144:147],v[68:71]\n"
MF "v[68:71],a[204:207],a[184:187],v[68:71]\n"
MF "v[60:63],a[204:207],a[180:183],v[60:63]\n"
LD "v12,s[28:31],s82 offen offset:2048 lds\n"
MF "v[60:63],a[200:203],a[140:143],v[60:63]\n"
"s_waitcnt lgkmcnt(4)\n"
MF "v[96:99],a[208:211],a[140:143],v[96:99]\n"
TR "a[200:201],v1 offset:16384\n"
MF "v[96:99],a[212:215],a[180:183],v[96:99]\n"
TR "a[202:203],v1 offset:16896\n"
MF "v[104:107],a[212:215],a[184:187],v[104:107]\n"
MF "v[104:107],a[208:211],a[144:147],v[104:107]\n"
TR "a[204:205],v1 offset:20480\n"
MF "v[112:115],a[208:211],a[148:151],v[112:115]\n"
TR "a[206:207],v1 offset:20992\n"
MF "v[112:115],a[212:215],a[188:191],v[112:115]\n"
MF "v[120:123],a[212:215],a[192:195],v[120:123]\n"
".Lpg7a:\n"
"s_cbranch_execz .Lisr1\n"
MF "v[120:123],a[208:211],a[152:155],v[120:123]\n"
LD "v13,s[28:31],s82 offen offset:3072 lds\n"
MF "v[128:131],a[208:211],a[156:159],v[128:131]\n"
MF "v[128:131],a[212:215],a[196:199],v[128:131]\n"
"s_waitcnt lgkmcnt(4)\n"
MF "v[132:135],a[220:223],a[196:199],v[132:135]\n"
TR "a[208:209],v1 offset:16392\n"
MF "v[132:135],a[216:219],a[156:159],v[132:135]\n"
TR "a[210:211],v1 offset:16904\n"
MF "v[124:127],a[216:219],a[152:155],v[124:127]\n"
MF "v[124:127],a[220:223],a[192:195],v[124:127]\n"
TR "a[212:213],v1 offset:20488\n"
MF "v[116:119],a[220:223],a[188:191],v[116:119]\n"
TR "a[214:215],v1 offset:21000\n"
MF "v[116:119],a[216:219],a[148:151],v[116:119]\n"
MF "v[108:111],a[216:219],a[144:147],v[108:111]\n"
MF "v[108:111],a[220:223],a[184:187],v[108:111]\n"
MF "v[100:103],a[220:223],a[180:183],v[100:103]\n"
MF "v[100:103],a[216:219],a[140:143],v[100:103]\n"
"s_waitcnt vmcnt(22) lgkmcnt(0)\n"
"s_barrier\n"
"s_cmp_eq_u32 s57,1\n"
"s_cbranch_scc0 .Lhs15\n"
".Lack_q:\n"
"v_cmp_ne_u32 vcc,s76,v10\n"
"s_cbranch_vccnz .Lackspin_q\n"
".Lackok_q:\n"
".Lhs15:\n"
MF "v[136:139],a[200:203],a[140:143],v[136:139]\n"
"s_and_b32 s58,s56,1\n"
TR "a[216:217],v0 offset:32768\n"
MF "v[136:139],a[204:207],a[180:183],v[136:139]\n"
"s_add_u32 m0,s62,65536\n"
TR "a[218:219],v0 offset:33280\n"
MF "v[144:147],a[204:207],a[184:187],v[144:147]\n"
"s_add_u32 s82,s66,6291456\n"
"ds_read_b128 a[60:63],v2 offset:40960\n"
MF "v[144:147],a[200:203],a[144:147],v[144:147]\n"
"s_sub_u32 s15,s82,7864320\n"
TR "a[220:221],v0 offset:36864\n"
MF "v[152:155],a[200:203],a[148:151],v[152:155]\n"
"s_min_u32 s82,s82,s15\n"
TR "a[222:223],v0 offset:37376\n"
MF "v[152:155],a[204:207],a[188:191],v[152:155]\n"
LD "v6,s[28:31],s82 offen lds\n"
MF "v[160:163],a[204:207],a[192:195],v[160:163]\n"
"s_mul_i32 s59,s58,3072\n"
"ds_read_b128 a[100:103],v3 offset:40960\n"
MF "v[160:163],a[200:203],a[152:155],v[160:163]\n"
"s_add_u32 s16,s70,s59\n"
MF "v[168:171],a[200:203],a[156:159],v[168:171]\n"
"s_mul_i32 s59,s58,6144\n"
MF "v[168:171],a[204:207],a[196:199],v[168:171]\n"
LD "v11,s[28:31],s82 offen offset:1024 lds\n"
MF "v[172:175],a[212:215],a[196:199],v[172:175]\n"
"s_add_u32 s18,s70,s59\n"
TR "a[200:201],v0 offset:32776\n"
MF "v[172:175],a[208:211],a[156:159],v[172:175]\n"
"s_add_u32 s17,s16,196608\n"
TR "a[202:203],v0 offset:33288\n"
MF "v[164:167],a[208:211],a[152:155],v[164:167]\n"
"s_add_u32 s19,s18,196608\n"
"ds_read_b128 a[64:67],v2 offset:43008\n"
MF "v[164:167],a[212:215],a[192:195],v[164:167]\n"
"s_add_u32 s20,s70,196608\n"
TR "a[204:205],v0 offset:36872\n"
MF "v[156:159],a[212:215],a[188:191],v[156:159]\n"
"s_bfe_u32 s58,s23,0x40008\n"
TR "a[206:207],v0 offset:37384\n"
MF "v[156:159],a[208:211],a[148:151],v[156:159]\n"
"s_lshr_b32 s59,s56,1\n"
MF "v[148:151],a[208:211],a[144:147],v[148:151]\n"
"s_mul_i32 s59,s59,5\n"
"ds_read_b128 a[104:107],v3 offset:43008\n"
MF "v[148:151],a[212:215],a[184:187],v[148:151]\n"
"s_add_u32 s58,s58,s59\n"
MF "v[140:143],a[212:215],a[180:183],v[140:143]\n"
LD "v12,s[28:31],s82 offen offset:2048 lds\n"
MF "v[140:143],a[208:211],a[140:143],v[140:143]\n"
"s_waitcnt lgkmcnt(7)\n"
MF "v[176:179],a[216:219],a[140:143],v[176:179]\n"
"s_and_b32 s58,s58,15\n"
TR "a[208:209],v1 offset:32768\n"
MF "v[176:179],a[220:223],a[180:183],v[176:179]\n"
"s_mul_i32 s58,s58,12288\n"
TR "a[210:211],v1 offset:33280\n"
MF "v[184:187],a[220:223],a[184:187],v[184:187]\n"
"s_add_u32 s72,s16,s58\n"
"ds_read_b128 a[68:71],v2 offset:45056\n"
MF "v[184:187],a[216:219],a[144:147],v[184:187]\n"
"s_add_u32 s73,s18,s58\n"
TR "a[212:213],v1 offset:36864\n"
MF "v[192:195],a[216:219],a[148:151],v[192:195]\n"
"s_add_u32 s74,s70,s58\n"
TR "a[214:215],v1 offset:37376\n"
MF "v[192:195],a[220:223],a[188:191],v[192:195]\n"
"s_add_u32 s74,s74,12288\n"
MF "v[200:203],a[220:223],a[192:195],v[200:203]\n"
"s_cmp_eq_u32 s74,s20\n"
"s_cselect_b32 s74,s70,s74\n"
"ds_read_b128 a[108:111],v3 offset:45056\n"
MF "v[200:203],a[216:219],a[152:155],v[200:203]\n"
LD "v13,s[28:31],s82 offen offset:3072 lds\n"
MF "v[208:211],a[216:219],a[156:159],v[208:211]\n"
"s_lshr_b32 s58,s56,1\n"
MF "v[208:211],a[220:223],a[196:199],v[208:211]\n"
"s_waitcnt lgkmcnt(7)\n"
MF "v[212:215],a[204:207],a[196:199],v[212:215]\n"
"s_mul_i32 s58,s58,6144\n"
TR "a[216:217],v1 offset:32776\n"
MF "v[212:215],a[200:203],a[156:159],v[212:215]\n"
"s_mul_i32 s64,s56,3072\n"
TR "a[218:219],v1 offset:33288\n"
MF "v[204:207],a[200:203],a[152:155],v[204:207]\n"
"s_add_u32 s64,s64,s58\n"
"ds_read_b128 a[72:75],v2 offset:47104\n"
MF "v[204:207],a[204:207],a[192:195],v[204:207]\n"
"s_mul_i32 s65,s56,6144\n"
TR "a[220:221],v1 offset:36872\n"
MF "v[196:199],a[204:207],a[188:191],v[196:199]\n"
"s_sub_u32 s58,s71,s70\n"
TR "a[222:223],v1 offset:37384\n"
MF "v[196:199],a[200:203],a[148:151],v[196:199]\n"
"s_add_u32 s96,s74,s58\n"
MF "v[188:191],a[200:203],a[144:147],v[188:191]\n"
"s_sub_u32 s58,s16,s70\n"
"v_add_u32 v0,0xffff4000,v0\n"
MF "v[188:191],a[204:207],a[184:187],v[188:191]\n"
"s_add_u32 s96,s96,s58\n"
"v_add_u32 v1,0xffff4000,v1\n"
MF "v[180:183],a[204:207],a[180:183],v[180:183]\n"
"s_max_u32 s60,s70,s71\n"
MF "v[180:183],a[200:203],a[140:143],v[180:183]\n"
"s_waitcnt vmcnt(13) lgkmcnt(0)\n"
"s_barrier\n"
MF "v[216:219],a[208:211],a[140:143],v[216:219]\n"
"s_add_u32 s34,s60,196608\n"
TR "a[200:201],v1 offset:8\n"
MF "v[216:219],a[212:215],a[180:183],v[216:219]\n"
"s_add_u32 m0,s62,81920\n"
TR "a[202:203],v1 offset:520\n"
"v_mov_b32 v14,s12\n"
MF "v[224:227],a[212:215],a[184:187],v[224:227]\n"
"s_add_u32 s82,s66,7077888\n"
"ds_read_b128 a[112:115],v3 offset:47104\n"
"v_mul_f32 v14,0xbdd53b95,v14\n"
MF "v[224:227],a[208:211],a[144:147],v[224:227]\n"
"s_sub_u32 s15,s82,7864320\n"
TR "a[204:205],v1 offset:4104\n"
MF "v[232:235],a[208:211],a[148:151],v[232:235]\n"
"s_min_u32 s82,s82,s15\n"
TR "a[206:207],v1 offset:4616\n"
MF "v[232:235],a[212:215],a[188:191],v[232:235]\n"
LD "v6,s[28:31],s82 offen lds\n"
MF "v[240:243],a[212:215],a[192:195],v[240:243]\n"
"s_add_u32 s38,s60,196608\n"
"ds_read_b128 a[76:79],v2 offset:49152\n"
MF "v[240:243],a[208:211],a[152:155],v[240:243]\n"
"ds_read_b128 a[116:119],v3 offset:49152\n"
MF "v[248:251],a[208:211],a[156:159],v[248:251]\n"
MF "v[248:251],a[212:215],a[196:199],v[248:251]\n"
LD "v11,s[28:31],s82 offen offset:1024 lds\n"
MF "v[220:223],a[216:219],a[140:143],v[220:223]\n"
TR "a[208:209],v1 offset:16392\n"
MF "v[220:223],a[220:223],a[180:183],v[220:223]\n"
TR "a[210:211],v1 offset:16904\n"
MF "v[228:231],a[220:223],a[184:187],v[228:231]\n"
"ds_read_b128 a[140:143],v2\n"
MF "v[228:231],a[216:219],a[144:147],v[228:231]\n"
TR "a[212:213],v1 offset:20488\n"
MF "v[236:239],a[216:219],a[148:151],v[236:239]\n"
TR "a[214:215],v1 offset:21000\n"
MF "v[236:239],a[220:223],a[188:191],v[236:239]\n"
"ds_read_b128 a[180:183],v3\n"
MF "v[244:247],a[220:223],a[192:195],v[244:247]\n"
"ds_read_b128 a[144:147],v2 offset:2048\n"
MF "v[244:247],a[216:219],a[152:155],v[244:247]\n"
"ds_read_b128 a[184:187],v3 offset:2048\n"
MF "v[252:255],a[216:219],a[156:159],v[252:255]\n"
"ds_read_b128 a[148:151],v2 offset:4096\n"
MF "v[252:255],a[220:223],a[196:199],v[252:255]\n"
"s_waitcnt lgkmcnt(9)\n"
"ds_read_b128 a[188:191],v3 offset:4096\n"
MF "v[220:223],a[200:203],a[60:63],v[220:223]\n"
TR "a[216:217],v1\n"
MF "v[220:223],a[204:207],a[100:103],v[220:223]\n"
TR "a[218:219],v1 offset:512\n"
MF "v[228:231],a[204:207],a[104:107],v[228:231]\n"
"ds_read_b128 a[152:155],v2 offset:6144\n"
MF "v[228:231],a[200:203],a[64:67],v[228:231]\n"
TR "a[220:221],v1 offset:4096\n"
MF "v[236:239],a[200:203],a[68:71],v[236:239]\n"
TR "a[222:223],v1 offset:4608\n"
MF "v[236:239],a[204:207],a[108:111],v[236:239]\n"
"ds_read_b128 a[192:195],v3 offset:6144\n"
MF "v[244:247],a[204:207],a[112:115],v[244:247]\n"
"ds_read_b128 a[156:159],v2 offset:8192\n"
MF "v[244:247],a[200:203],a[72:75],v[244:247]\n"
"ds_read_b128 a[196:199],v3 offset:8192\n"
MF "v[252:255],a[200:203],a[76:79],v[252:255]\n"
LD "v12,s[28:31],s82 offen offset:2048 lds\n"
MF "v[252:255],a[204:207],a[116:119],v[252:255]\n"
"s_waitcnt lgkmcnt(8)\n"
LD "v13,s[28:31],s82 offen offset:3072 lds\n"
MF "v[220:223],a[208:211],a[140:143],v[220:223]\n"
TR "a[200:201],v1 offset:16384\n"
MF "v[220:223],a[212:215],a[180:183],v[220:223]\n"
TR "a[202:203],v1 offset:16896\n"
MF "v[228:231],a[212:215],a[184:187],v[228:231]\n"
MF "v[228:231],a[208:211],a[144:147],v[228:231]\n"
TR "a[204:205],v1 offset:20480\n"
MF "v[236:239],a[208:211],a[148:151],v[236:239]\n"
TR "a[206:207],v1 offset:20992\n"
MF "v[236:239],a[212:215],a[188:191],v[236:239]\n"
"s_waitcnt lgkmcnt(4)\n"
MF "v[244:247],a[212:215],a[192:195],v[244:247]\n"
MF "v[244:247],a[208:211],a[152:155],v[244:247]\n"
MF "v[252:255],a[208:211],a[156:159],v[252:255]\n"
MF "v[252:255],a[212:215],a[196:199],v[252:255]\n"
"s_waitcnt lgkmcnt(0)\n"
"s_cmp_eq_u32 s69,98\n"
"s_cbranch_scc1 .Lqy\n"
".Lqf:\n"
MF "v[216:219],a[216:219],a[60:63],v[216:219]\n"
TR "a[208:209],v0 offset:8\n"
MF "v[216:219],a[220:223],a[100:103],v[216:219]\n"
TR "a[210:211],v0 offset:520\n"
MF "v[224:227],a[220:223],a[104:107],v[224:227]\n"
"buffer_load_dword a224,v15,s[36:39],s70 offen offset:2688 nt\n"
MF "v[224:227],a[216:219],a[64:67],v[224:227]\n"
TR "a[212:213],v0 offset:4104\n"
MF "v[232:235],a[216:219],a[68:71],v[232:235]\n"
TR "a[214:215],v0 offset:4616\n"
MF "v[232:235],a[220:223],a[108:111],v[232:235]\n"
"buffer_load_dword a225,v15,s[36:39],s71 offen offset:2688 nt\n"
MF "v[240:243],a[220:223],a[112:115],v[240:243]\n"
MF "v[240:243],a[216:219],a[72:75],v[240:243]\n"
"buffer_load_dword a226,v15,s[48:51],s7 offen offset:2688\n"
MF "v[248:251],a[216:219],a[76:79],v[248:251]\n"
MF "v[248:251],a[220:223],a[116:119],v[248:251]\n"
MF "v[248:251],a[200:203],a[156:159],v[248:251]\n"
TR "a[216:217],v0 offset:16392\n"
MF "v[248:251],a[204:207],a[196:199],v[248:251]\n"
TR "a[218:219],v0 offset:16904\n"
MF "v[240:243],a[204:207],a[192:195],v[240:243]\n"
MF "v[240:243],a[200:203],a[152:155],v[240:243]\n"
TR "a[220:221],v0 offset:20488\n"
MF "v[232:235],a[200:203],a[148:151],v[232:235]\n"
TR "a[222:223],v0 offset:21000\n"
MF "v[232:235],a[204:207],a[188:191],v[232:235]\n"
MF "v[224:227],a[204:207],a[184:187],v[224:227]\n"
MF "v[224:227],a[200:203],a[144:147],v[224:227]\n"
"v_fma_f32 v248,v248,s54,v14\n"
"v_fma_f32 v249,v249,s54,v14\n"
MF "v[216:219],a[200:203],a[140:143],v[216:219]\n"
"v_fma_f32 v250,v250,s54,v14\n"
"v_fma_f32 v251,v251,s54,v14\n"
MF "v[216:219],a[204:207],a[180:183],v[216:219]\n"
"s_waitcnt lgkmcnt(4)\n"
"v_fma_f32 v252,v252,s54,v14\n"
"v_fma_f32 v253,v253,s54,v14\n"
MF "v[180:183],a[208:211],a[60:63],v[180:183]\n"
TR "a[200:201],v0\n"
"v_fma_f32 v254,v254,s54,v14\n"
MF "v[180:183],a[212:215],a[100:103],v[180:183]\n"
TR "a[202:203],v0 offset:512\n"
"v_fma_f32 v255,v255,s54,v14\n"
MF "v[188:191],a[212:215],a[104:107],v[188:191]\n"
CV "v255,v254,v255\n"
CV "v254,v252,v253\n"
MF "v[188:191],a[208:211],a[64:67],v[188:191]\n"
TR "a[204:205],v0 offset:4096\n"
CV "v253,v250,v251\n"
MF "v[196:199],a[208:211],a[68:71],v[196:199]\n"
TR "a[206:207],v0 offset:4608\n"
CV "v252,v248,v249\n"
MF "v[196:199],a[212:215],a[108:111],v[196:199]\n"
"v_fma_f32 v240,v240,s54,v14\n"
"v_fma_f32 v241,v241,s54,v14\n"
MF "v[204:207],a[212:215],a[112:115],v[204:207]\n"
"v_fma_f32 v242,v242,s54,v14\n"
"v_fma_f32 v243,v243,s54,v14\n"
MF "v[204:207],a[208:211],a[72:75],v[204:207]\n"
"v_fma_f32 v244,v244,s54,v14\n"
"v_fma_f32 v245,v245,s54,v14\n"
MF "v[212:215],a[208:211],a[76:79],v[212:215]\n"
"v_fma_f32 v246,v246,s54,v14\n"
"v_fma_f32 v247,v247,s54,v14\n"
MF "v[212:215],a[212:215],a[116:119],v[212:215]\n"
"s_waitcnt lgkmcnt(4)\n"
CV "v251,v246,v247\n"
CV "v250,v244,v245\n"
MF "v[180:183],a[216:219],a[140:143],v[180:183]\n"
TR "a[208:209],v0 offset:16384\n"
CV "v249,v242,v243\n"
MF "v[180:183],a[220:223],a[180:183],v[180:183]\n"
TR "a[210:211],v0 offset:16896\n"
CV "v248,v240,v241\n"
MF "v[188:191],a[220:223],a[184:187],v[188:191]\n"
"v_fma_f32 v232,v232,s54,v14\n"
"v_fma_f32 v233,v233,s54,v14\n"
MF "v[188:191],a[216:219],a[144:147],v[188:191]\n"
TR "a[212:213],v0 offset:20480\n"
"v_fma_f32 v234,v234,s54,v14\n"
MF "v[196:199],a[216:219],a[148:151],v[196:199]\n"
TR "a[214:215],v0 offset:20992\n"
"v_fma_f32 v235,v235,s54,v14\n"
MF "v[196:199],a[220:223],a[188:191],v[196:199]\n"
"v_fma_f32 v236,v236,s54,v14\n"
"v_fma_f32 v237,v237,s54,v14\n"
MF "v[204:207],a[220:223],a[192:195],v[204:207]\n"
"v_fma_f32 v238,v238,s54,v14\n"
"v_fma_f32 v239,v239,s54,v14\n"
MF "v[204:207],a[216:219],a[152:155],v[204:207]\n"
CV "v247,v238,v239\n"
CV "v246,v236,v237\n"
MF "v[212:215],a[216:219],a[156:159],v[212:215]\n"
CV "v245,v234,v235\n"
CV "v244,v232,v233\n"
MF "v[212:215],a[220:223],a[196:199],v[212:215]\n"
"s_waitcnt vmcnt(11) lgkmcnt(0)\n"
"s_barrier\n"
MF "v[176:179],a[200:203],a[60:63],v[176:179]\n"
TR "a[216:217],v1 offset:32776\n"
"v_fma_f32 v224,v224,s54,v14\n"
MF "v[176:179],a[204:207],a[100:103],v[176:179]\n"
"s_add_u32 m0,s64,0\n"
TR "a[218:219],v1 offset:33288\n"
"v_fma_f32 v225,v225,s54,v14\n"
MF "v[184:187],a[204:207],a[104:107],v[184:187]\n"
LD "v7,s[32:35],s72 offen nt lds\n"
MF "v[184:187],a[200:203],a[64:67],v[184:187]\n"
TR "a[220:221],v1 offset:36872\n"
"v_fma_f32 v226,v226,s54,v14\n"
MF "v[192:195],a[200:203],a[68:71],v[192:195]\n"
TR "a[222:223],v1 offset:37384\n"
"v_fma_f32 v227,v227,s54,v14\n"
MF "v[192:195],a[204:207],a[108:111],v[192:195]\n"
"s_add_u32 m0,s64,896\n"
"v_fma_f32 v228,v228,s54,v14\n"
"v_fma_f32 v229,v229,s54,v14\n"
MF "v[200:203],a[204:207],a[112:115],v[200:203]\n"
LD "v7,s[32:35],s72 offen offset:128 nt lds\n"
MF "v[200:203],a[200:203],a[72:75],v[200:203]\n"
"v_fma_f32 v230,v230,s54,v14\n"
"v_fma_f32 v231,v231,s54,v14\n"
MF "v[208:211],a[200:203],a[76:79],v[208:211]\n"
CV "v243,v230,v231\n"
CV "v242,v228,v229\n"
MF "v[208:211],a[204:207],a[116:119],v[208:211]\n"
CV "v241,v226,v227\n"
CV "v240,v224,v225\n"
MF "v[208:211],a[208:211],a[156:159],v[208:211]\n"
TR "a[200:201],v1 offset:49160\n"
"v_fma_f32 v216,v216,s54,v14\n"
MF "v[208:211],a[212:215],a[196:199],v[208:211]\n"
"s_add_u32 m0,s64,1792\n"
TR "a[202:203],v1 offset:49672\n"
"v_fma_f32 v217,v217,s54,v14\n"
MF "v[200:203],a[212:215],a[192:195],v[200:203]\n"
LD "v7,s[32:35],s72 offen offset:256 nt lds\n"
MF "v[200:203],a[208:211],a[152:155],v[200:203]\n"
TR "a[204:205],v1 offset:53256\n"
"v_fma_f32 v218,v218,s54,v14\n"
MF "v[192:195],a[208:211],a[148:151],v[192:195]\n"
TR "a[206:207],v1 offset:53768\n"
"v_fma_f32 v219,v219,s54,v14\n"
MF "v[192:195],a[212:215],a[188:191],v[192:195]\n"
"v_fma_f32 v220,v220,s54,v14\n"
"v_fma_f32 v221,v221,s54,v14\n"
MF "v[184:187],a[212:215],a[184:187],v[184:187]\n"
"s_add_u32 s72,s72,6144\n"
"v_fma_f32 v222,v222,s54,v14\n"
"v_fma_f32 v223,v223,s54,v14\n"
MF "v[184:187],a[208:211],a[144:147],v[184:187]\n"
"s_add_u32 m0,s64,6144\n"
CV "v239,v222,v223\n"
CV "v238,v220,v221\n"
MF "v[176:179],a[208:211],a[140:143],v[176:179]\n"
LD "v7,s[32:35],s72 offen nt lds\n"
MF "v[176:179],a[212:215],a[180:183],v[176:179]\n"
"s_waitcnt lgkmcnt(4)\n"
CV "v237,v218,v219\n"
CV "v236,v216,v217\n"
MF "v[140:143],a[216:219],a[60:63],v[140:143]\n"
TR "a[208:209],v1 offset:32768\n"
"v_fma_f32 v208,v208,s54,v14\n"
MF "v[140:143],a[220:223],a[100:103],v[140:143]\n"
TR "a[210:211],v1 offset:33280\n"
"v_fma_f32 v209,v209,s54,v14\n"
MF "v[148:151],a[220:223],a[104:107],v[148:151]\n"
"v_fma_f32 v210,v210,s54,v14\n"
"v_fma_f32 v211,v211,s54,v14\n"
MF "v[148:151],a[216:219],a[64:67],v[148:151]\n"
TR "a[212:213],v1 offset:36864\n"
"v_fma_f32 v212,v212,s54,v14\n"
MF "v[156:159],a[216:219],a[68:71],v[156:159]\n"
"s_add_u32 m0,s64,7040\n"
TR "a[214:215],v1 offset:37376\n"
"v_fma_f32 v213,v213,s54,v14\n"
MF "v[156:159],a[220:223],a[108:111],v[156:159]\n"
LD "v7,s[32:35],s72 offen offset:128 nt lds\n"
MF "v[164:167],a[220:223],a[112:115],v[164:167]\n"
"v_fma_f32 v214,v214,s54,v14\n"
"v_fma_f32 v215,v215,s54,v14\n"
MF "v[164:167],a[216:219],a[72:75],v[164:167]\n"
CV "v235,v214,v215\n"
CV "v234,v212,v213\n"
MF "v[172:175],a[216:219],a[76:79],v[172:175]\n"
"s_add_u32 m0,s64,7936\n"
CV "v233,v210,v211\n"
CV "v232,v208,v209\n"
MF "v[172:175],a[220:223],a[116:119],v[172:175]\n"
"s_waitcnt lgkmcnt(4)\n"
LD "v7,s[32:35],s72 offen offset:256 nt lds\n"
MF "v[140:143],a[200:203],a[140:143],v[140:143]\n"
TR "a[216:217],v1 offset:49152\n"
"v_fma_f32 v200,v200,s54,v14\n"
MF "v[140:143],a[204:207],a[180:183],v[140:143]\n"
TR "a[218:219],v1 offset:49664\n"
"v_fma_f32 v201,v201,s54,v14\n"
MF "v[148:151],a[204:207],a[184:187],v[148:151]\n"
"v_fma_f32 v202,v202,s54,v14\n"
"v_fma_f32 v203,v203,s54,v14\n"
MF "v[148:151],a[200:203],a[144:147],v[148:151]\n"
TR "a[220:221],v1 offset:53248\n"
"v_fma_f32 v204,v204,s54,v14\n"
MF "v[156:159],a[200:203],a[148:151],v[156:159]\n"
TR "a[222:223],v1 offset:53760\n"
"v_fma_f32 v205,v205,s54,v14\n"
MF "v[156:159],a[204:207],a[188:191],v[156:159]\n"
"v_fma_f32 v206,v206,s54,v14\n"
"v_fma_f32 v207,v207,s54,v14\n"
MF "v[164:167],a[204:207],a[192:195],v[164:167]\n"
CV "v231,v206,v207\n"
CV "v230,v204,v205\n"
MF "v[164:167],a[200:203],a[152:155],v[164:167]\n"
CV "v229,v202,v203\n"
CV "v228,v200,v201\n"
MF "v[172:175],a[200:203],a[156:159],v[172:175]\n"
"v_fma_f32 v192,v192,s54,v14\n"
"v_fma_f32 v193,v193,s54,v14\n"
MF "v[172:175],a[204:207],a[196:199],v[172:175]\n"
"s_waitcnt lgkmcnt(0)\n"
"v_fma_f32 v194,v194,s54,v14\n"
"v_fma_f32 v195,v195,s54,v14\n"
MF "v[136:139],a[208:211],a[60:63],v[136:139]\n"
TR "a[200:201],v0 offset:32776\n"
"v_fma_f32 v196,v196,s54,v14\n"
MF "v[136:139],a[212:215],a[100:103],v[136:139]\n"
TR "a[202:203],v0 offset:33288\n"
"v_fma_f32 v197,v197,s54,v14\n"
MF "v[144:147],a[212:215],a[104:107],v[144:147]\n"
"v_fma_f32 v198,v198,s54,v14\n"
"v_fma_f32 v199,v199,s54,v14\n"
MF "v[144:147],a[208:211],a[64:67],v[144:147]\n"
TR "a[204:205],v0 offset:36872\n"
CV "v227,v198,v199\n"
MF "v[152:155],a[208:211],a[68:71],v[152:155]\n"
TR "a[206:207],v0 offset:37384\n"
CV "v226,v196,v197\n"
MF "v[152:155],a[212:215],a[108:111],v[152:155]\n"
CV "v225,v194,v195\n"
CV "v224,v192,v193\n"
MF "v[160:163],a[212:215],a[112:115],v[160:163]\n"
"v_fma_f32 v184,v184,s54,v14\n"
"v_fma_f32 v185,v185,s54,v14\n"
MF "v[160:163],a[208:211],a[72:75],v[160:163]\n"
"v_fma_f32 v186,v186,s54,v14\n"
"v_fma_f32 v187,v187,s54,v14\n"
MF "v[168:171],a[208:211],a[76:79],v[168:171]\n"
"v_fma_f32 v188,v188,s54,v14\n"
"v_fma_f32 v189,v189,s54,v14\n"
MF "v[168:171],a[212:215],a[116:119],v[168:171]\n"
"v_fma_f32 v190,v190,s54,v14\n"
"v_fma_f32 v191,v191,s54,v14\n"
MF "v[168:171],a[216:219],a[156:159],v[168:171]\n"
TR "a[208:209],v0 offset:49160\n"
CV "v223,v190,v191\n"
MF "v[168:171],a[220:223],a[196:199],v[168:171]\n"
TR "a[210:211],v0 offset:49672\n"
CV "v222,v188,v189\n"
MF "v[160:163],a[220:223],a[192:195],v[160:163]\n"
CV "v221,v186,v187\n"
CV "v220,v184,v185\n"
MF "v[160:163],a[216:219],a[152:155],v[160:163]\n"
TR "a[212:213],v0 offset:53256\n"
"v_fma_f32 v176,v176,s54,v14\n"
MF "v[152:155],a[216:219],a[148:151],v[152:155]\n"
TR "a[214:215],v0 offset:53768\n"
"v_fma_f32 v177,v177,s54,v14\n"
MF "v[152:155],a[220:223],a[188:191],v[152:155]\n"
"v_fma_f32 v178,v178,s54,v14\n"
"v_fma_f32 v179,v179,s54,v14\n"
MF "v[144:147],a[220:223],a[184:187],v[144:147]\n"
"v_fma_f32 v180,v180,s54,v14\n"
"v_fma_f32 v181,v181,s54,v14\n"
MF "v[144:147],a[216:219],a[144:147],v[144:147]\n"
"v_fma_f32 v182,v182,s54,v14\n"
"v_fma_f32 v183,v183,s54,v14\n"
MF "v[136:139],a[216:219],a[140:143],v[136:139]\n"
CV "v219,v182,v183\n"
CV "v218,v180,v181\n"
MF "v[136:139],a[220:223],a[180:183],v[136:139]\n"
"s_waitcnt lgkmcnt(4)\n"
CV "v217,v178,v179\n"
CV "v216,v176,v177\n"
MF "v[100:103],a[200:203],a[60:63],v[100:103]\n"
TR "a[216:217],v0 offset:32768\n"
"v_fma_f32 v168,v168,s54,v14\n"
MF "v[100:103],a[204:207],a[100:103],v[100:103]\n"
TR "a[218:219],v0 offset:33280\n"
"v_fma_f32 v169,v169,s54,v14\n"
MF "v[108:111],a[204:207],a[104:107],v[108:111]\n"
"v_fma_f32 v170,v170,s54,v14\n"
"v_fma_f32 v171,v171,s54,v14\n"
MF "v[108:111],a[200:203],a[64:67],v[108:111]\n"
TR "a[220:221],v0 offset:36864\n"
"v_fma_f32 v172,v172,s54,v14\n"
MF "v[116:119],a[200:203],a[68:71],v[116:119]\n"
TR "a[222:223],v0 offset:37376\n"
"v_fma_f32 v173,v173,s54,v14\n"
MF "v[116:119],a[204:207],a[108:111],v[116:119]\n"
"v_fma_f32 v174,v174,s54,v14\n"
"v_fma_f32 v175,v175,s54,v14\n"
MF "v[124:127],a[204:207],a[112:115],v[124:127]\n"
CV "v215,v174,v175\n"
CV "v214,v172,v173\n"
MF "v[124:127],a[200:203],a[72:75],v[124:127]\n"
CV "v213,v170,v171\n"
CV "v212,v168,v169\n"
MF "v[132:135],a[200:203],a[76:79],v[132:135]\n"
"v_fma_f32 v160,v160,s54,v14\n"
"v_fma_f32 v161,v161,s54,v14\n"
MF "v[132:135],a[204:207],a[116:119],v[132:135]\n"
"s_waitcnt lgkmcnt(4)\n"
"v_fma_f32 v162,v162,s54,v14\n"
"v_fma_f32 v163,v163,s54,v14\n"
MF "v[100:103],a[208:211],a[140:143],v[100:103]\n"
TR "a[200:201],v0 offset:49152\n"
"v_fma_f32 v164,v164,s54,v14\n"
MF "v[100:103],a[212:215],a[180:183],v[100:103]\n"
".Lpg8b:\n"
"s_cbranch_execz .Lpg11b\n"
".Lpg8d:\n"
"s_cbranch_execz .Lpgpsl_d\n"
TR "a[202:203],v0 offset:49664\n"
"v_fma_f32 v165,v165,s54,v14\n"
MF "v[108:111],a[212:215],a[184:187],v[108:111]\n"
"v_fma_f32 v166,v166,s54,v14\n"
"v_fma_f32 v167,v167,s54,v14\n"
MF "v[108:111],a[208:211],a[144:147],v[108:111]\n"
TR "a[204:205],v0 offset:53248\n"
CV "v211,v166,v167\n"
MF "v[116:119],a[208:211],a[148:151],v[116:119]\n"
TR "a[206:207],v0 offset:53760\n"
CV "v210,v164,v165\n"
MF "v[116:119],a[212:215],a[188:191],v[116:119]\n"
CV "v209,v162,v163\n"
CV "v208,v160,v161\n"
MF "v[124:127],a[212:215],a[192:195],v[124:127]\n"
"v_add_u32 v0,0xc000,v0\n"
"v_fma_f32 v152,v152,s54,v14\n"
MF "v[124:127],a[208:211],a[152:155],v[124:127]\n"
"v_add_u32 v1,0xc000,v1\n"
"v_fma_f32 v153,v153,s54,v14\n"
MF "v[132:135],a[208:211],a[156:159],v[132:135]\n"
"v_fma_f32 v154,v154,s54,v14\n"
"v_fma_f32 v155,v155,s54,v14\n"
MF "v[132:135],a[212:215],a[196:199],v[132:135]\n"
"s_waitcnt vmcnt(9) lgkmcnt(0)\n"
"s_barrier\n"
MF "v[96:99],a[216:219],a[60:63],v[96:99]\n"
TR "a[208:209],v1 offset:16392\n"
"v_fma_f32 v156,v156,s54,v14\n"
MF "v[96:99],a[220:223],a[100:103],v[96:99]\n"
TR "a[210:211],v1 offset:16904\n"
"v_fma_f32 v157,v157,s54,v14\n"
MF "v[104:107],a[220:223],a[104:107],v[104:107]\n"
"s_add_u32 s72,s72,6144\n"
"v_fma_f32 v158,v158,s54,v14\n"
"v_fma_f32 v159,v159,s54,v14\n"
MF "v[104:107],a[216:219],a[64:67],v[104:107]\n"
"s_cmp_eq_u32 s72,s17\n"
"s_cselect_b32 s72,s16,s72\n"
TR "a[212:213],v1 offset:20488\n"
MF "v[112:115],a[216:219],a[68:71],v[112:115]\n"
"s_add_u32 m0,s64,24576\n"
TR "a[214:215],v1 offset:21000\n"
CV "v207,v158,v159\n"
MF "v[112:115],a[220:223],a[108:111],v[112:115]\n"
LD "v7,s[32:35],s72 offen nt lds\n"
MF "v[120:123],a[220:223],a[112:115],v[120:123]\n"
"s_add_u32 m0,s64,25472\n"
CV "v206,v156,v157\n"
CV "v205,v154,v155\n"
MF "v[120:123],a[216:219],a[72:75],v[120:123]\n"
LD "v7,s[32:35],s72 offen offset:128 nt lds\n"
MF "v[128:131],a[216:219],a[76:79],v[128:131]\n"
CV "v204,v152,v153\n"
"v_fma_f32 v144,v144,s54,v14\n"
MF "v[128:131],a[220:223],a[116:119],v[128:131]\n"
"v_fma_f32 v145,v145,s54,v14\n"
"v_fma_f32 v146,v146,s54,v14\n"
MF "v[128:131],a[200:203],a[156:159],v[128:131]\n"
TR "a[216:217],v1 offset:32776\n"
"v_fma_f32 v147,v147,s54,v14\n"
MF "v[128:131],a[204:207],a[196:199],v[128:131]\n"
"s_add_u32 m0,s64,26368\n"
TR "a[218:219],v1 offset:33288\n"
"v_fma_f32 v148,v148,s54,v14\n"
MF "v[120:123],a[204:207],a[192:195],v[120:123]\n"
LD "v7,s[32:35],s72 offen offset:256 nt lds\n"
MF "v[120:123],a[200:203],a[152:155],v[120:123]\n"
TR "a[220:221],v1 offset:36872\n"
"v_fma_f32 v149,v149,s54,v14\n"
MF "v[112:115],a[200:203],a[148:151],v[112:115]\n"
TR "a[222:223],v1 offset:37384\n"
"v_fma_f32 v150,v150,s54,v14\n"
MF "v[112:115],a[204:207],a[188:191],v[112:115]\n"
"v_fma_f32 v151,v151,s54,v14\n"
CV "v203,v150,v151\n"
MF "v[104:107],a[204:207],a[184:187],v[104:107]\n"
"s_add_u32 s72,s72,6144\n"
CV "v202,v148,v149\n"
CV "v201,v146,v147\n"
MF "v[104:107],a[200:203],a[144:147],v[104:107]\n"
"s_add_u32 m0,s64,30720\n"
CV "v200,v144,v145\n"
"v_fma_f32 v136,v136,s54,v14\n"
MF "v[96:99],a[200:203],a[140:143],v[96:99]\n"
LD "v7,s[32:35],s72 offen nt lds\n"
MF "v[96:99],a[204:207],a[180:183],v[96:99]\n"
"s_waitcnt lgkmcnt(4)\n"
"v_fma_f32 v137,v137,s54,v14\n"
"v_fma_f32 v138,v138,s54,v14\n"
MF "v[60:63],a[208:211],a[60:63],v[60:63]\n"
TR "a[200:201],v1 offset:16384\n"
"v_fma_f32 v139,v139,s54,v14\n"
MF "v[60:63],a[212:215],a[100:103],v[60:63]\n"
TR "a[202:203],v1 offset:16896\n"
"v_fma_f32 v140,v140,s54,v14\n"
MF "v[68:71],a[212:215],a[104:107],v[68:71]\n"
"v_fma_f32 v141,v141,s54,v14\n"
"v_fma_f32 v142,v142,s54,v14\n"
MF "v[68:71],a[208:211],a[64:67],v[68:71]\n"
TR "a[204:205],v1 offset:20480\n"
"v_fma_f32 v143,v143,s54,v14\n"
MF "v[76:79],a[208:211],a[68:71],v[76:79]\n"
"s_add_u32 m0,s64,31616\n"
TR "a[206:207],v1 offset:20992\n"
CV "v199,v142,v143\n"
MF "v[76:79],a[212:215],a[108:111],v[76:79]\n"
LD "v7,s[32:35],s72 offen offset:128 nt lds\n"
MF "v[84:87],a[212:215],a[112:115],v[84:87]\n"
CV "v198,v140,v141\n"
CV "v197,v138,v139\n"
MF "v[84:87],a[208:211],a[72:75],v[84:87]\n"
CV "v196,v136,v137\n"
"v_fma_f32 v128,v128,s54,v14\n"
MF "v[92:95],a[208:211],a[76:79],v[92:95]\n"
"s_add_u32 m0,s64,32512\n"
"v_fma_f32 v129,v129,s54,v14\n"
"v_fma_f32 v130,v130,s54,v14\n"
MF "v[92:95],a[212:215],a[116:119],v[92:95]\n"
"s_waitcnt lgkmcnt(4)\n"
LD "v7,s[32:35],s72 offen offset:256 nt lds\n"
MF "v[60:63],a[216:219],a[140:143],v[60:63]\n"
TR "a[208:209],v1 offset:32768\n"
"v_fma_f32 v131,v131,s54,v14\n"
MF "v[60:63],a[220:223],a[180:183],v[60:63]\n"
TR "a[210:211],v1 offset:33280\n"
"v_fma_f32 v132,v132,s54,v14\n"
MF "v[68:71],a[220:223],a[184:187],v[68:71]\n"
"v_fma_f32 v133,v133,s54,v14\n"
"v_fma_f32 v134,v134,s54,v14\n"
MF "v[68:71],a[216:219],a[144:147],v[68:71]\n"
TR "a[212:213],v1 offset:36864\n"
"v_fma_f32 v135,v135,s54,v14\n"
MF "v[76:79],a[216:219],a[148:151],v[76:79]\n"
TR "a[214:215],v1 offset:37376\n"
CV "v195,v134,v135\n"
MF "v[76:79],a[220:223],a[188:191],v[76:79]\n"
CV "v194,v132,v133\n"
CV "v193,v130,v131\n"
MF "v[84:87],a[220:223],a[192:195],v[84:87]\n"
CV "v192,v128,v129\n"
"v_fma_f32 v120,v120,s54,v14\n"
MF "v[84:87],a[216:219],a[152:155],v[84:87]\n"
"v_fma_f32 v121,v121,s54,v14\n"
"v_fma_f32 v122,v122,s54,v14\n"
MF "v[92:95],a[216:219],a[156:159],v[92:95]\n"
"v_fma_f32 v123,v123,s54,v14\n"
"v_fma_f32 v124,v124,s54,v14\n"
MF "v[92:95],a[220:223],a[196:199],v[92:95]\n"
"s_waitcnt lgkmcnt(0)\n"
"v_fma_f32 v125,v125,s54,v14\n"
"v_fma_f32 v126,v126,s54,v14\n"
MF "v[56:59],a[200:203],a[60:63],v[56:59]\n"
TR "a[216:217],v0 offset:16392\n"
"v_fma_f32 v127,v127,s54,v14\n"
MF "v[56:59],a[204:207],a[100:103],v[56:59]\n"
TR "a[218:219],v0 offset:16904\n"
CV "v191,v126,v127\n"
MF "v[64:67],a[204:207],a[104:107],v[64:67]\n"
CV "v190,v124,v125\n"
CV "v189,v122,v123\n"
MF "v[64:67],a[200:203],a[64:67],v[64:67]\n"
TR "a[220:221],v0 offset:20488\n"
CV "v188,v120,v121\n"
MF "v[72:75],a[200:203],a[68:71],v[72:75]\n"
TR "a[222:223],v0 offset:21000\n"
"v_fma_f32 v112,v112,s54,v14\n"
MF "v[72:75],a[204:207],a[108:111],v[72:75]\n"
"v_fma_f32 v113,v113,s54,v14\n"
"v_fma_f32 v114,v114,s54,v14\n"
MF "v[80:83],a[204:207],a[112:115],v[80:83]\n"
"v_fma_f32 v115,v115,s54,v14\n"
"v_fma_f32 v116,v116,s54,v14\n"
MF "v[80:83],a[200:203],a[72:75],v[80:83]\n"
"v_fma_f32 v117,v117,s54,v14\n"
"v_fma_f32 v118,v118,s54,v14\n"
MF "v[88:91],a[200:203],a[76:79],v[88:91]\n"
"v_fma_f32 v119,v119,s54,v14\n"
CV "v187,v118,v119\n"
MF "v[88:91],a[204:207],a[116:119],v[88:91]\n"
CV "v186,v116,v117\n"
CV "v185,v114,v115\n"
MF "v[88:91],a[208:211],a[156:159],v[88:91]\n"
TR "a[200:201],v0 offset:32776\n"
CV "v184,v112,v113\n"
MF "v[88:91],a[212:215],a[196:199],v[88:91]\n"
TR "a[202:203],v0 offset:33288\n"
"v_fma_f32 v104,v104,s54,v14\n"
MF "v[80:83],a[212:215],a[192:195],v[80:83]\n"
"v_fma_f32 v105,v105,s54,v14\n"
"v_fma_f32 v106,v106,s54,v14\n"
MF "v[80:83],a[208:211],a[152:155],v[80:83]\n"
TR "a[204:205],v0 offset:36872\n"
"v_fma_f32 v107,v107,s54,v14\n"
MF "v[72:75],a[208:211],a[148:151],v[72:75]\n"
TR "a[206:207],v0 offset:37384\n"
"v_fma_f32 v108,v108,s54,v14\n"
MF "v[72:75],a[212:215],a[188:191],v[72:75]\n"
"v_fma_f32 v109,v109,s54,v14\n"
"v_fma_f32 v110,v110,s54,v14\n"
MF "v[64:67],a[212:215],a[184:187],v[64:67]\n"
"v_fma_f32 v111,v111,s54,v14\n"
CV "v183,v110,v111\n"
MF "v[64:67],a[208:211],a[144:147],v[64:67]\n"
CV "v182,v108,v109\n"
CV "v181,v106,v107\n"
MF "v[56:59],a[208:211],a[140:143],v[56:59]\n"
CV "v180,v104,v105\n"
"v_fma_f32 v96,v96,s54,v14\n"
MF "v[56:59],a[212:215],a[180:183],v[56:59]\n"
"s_waitcnt lgkmcnt(4)\n"
"v_fma_f32 v97,v97,s54,v14\n"
"v_fma_f32 v98,v98,s54,v14\n"
MF "v[20:23],a[216:219],a[60:63],v[20:23]\n"
TR "a[208:209],v0 offset:16384\n"
"v_fma_f32 v99,v99,s54,v14\n"
MF "v[20:23],a[220:223],a[100:103],v[20:23]\n"
TR "a[210:211],v0 offset:16896\n"
"v_fma_f32 v100,v100,s54,v14\n"
MF "v[28:31],a[220:223],a[104:107],v[28:31]\n"
"v_fma_f32 v101,v101,s54,v14\n"
"v_fma_f32 v102,v102,s54,v14\n"
MF "v[28:31],a[216:219],a[64:67],v[28:31]\n"
TR "a[212:213],v0 offset:20480\n"
"v_fma_f32 v103,v103,s54,v14\n"
MF "v[36:39],a[216:219],a[68:71],v[36:39]\n"
TR "a[214:215],v0 offset:20992\n"
CV "v179,v102,v103\n"
MF "v[36:39],a[220:223],a[108:111],v[36:39]\n"
CV "v178,v100,v101\n"
CV "v177,v98,v99\n"
MF "v[44:47],a[220:223],a[112:115],v[44:47]\n"
CV "v176,v96,v97\n"
"v_fma_f32 v88,v88,s54,v14\n"
MF "v[44:47],a[216:219],a[72:75],v[44:47]\n"
"v_fma_f32 v89,v89,s54,v14\n"
"v_fma_f32 v90,v90,s54,v14\n"
MF "v[52:55],a[216:219],a[76:79],v[52:55]\n"
"v_fma_f32 v91,v91,s54,v14\n"
"v_fma_f32 v92,v92,s54,v14\n"
MF "v[52:55],a[220:223],a[116:119],v[52:55]\n"
"s_waitcnt lgkmcnt(4)\n"
"v_fma_f32 v93,v93,s54,v14\n"
"v_fma_f32 v94,v94,s54,v14\n"
MF "v[20:23],a[200:203],a[140:143],v[20:23]\n"
TR "a[216:217],v0 offset:32768\n"
"v_fma_f32 v95,v95,s54,v14\n"
MF "v[20:23],a[204:207],a[180:183],v[20:23]\n"
TR "a[218:219],v0 offset:33280\n"
CV "v175,v94,v95\n"
MF "v[28:31],a[204:207],a[184:187],v[28:31]\n"
CV "v174,v92,v93\n"
CV "v173,v90,v91\n"
MF "v[28:31],a[200:203],a[144:147],v[28:31]\n"
TR "a[220:221],v0 offset:36864\n"
CV "v172,v88,v89\n"
MF "v[36:39],a[200:203],a[148:151],v[36:39]\n"
TR "a[222:223],v0 offset:37376\n"
"v_fma_f32 v80,v80,s54,v14\n"
MF "v[36:39],a[204:207],a[188:191],v[36:39]\n"
"v_fma_f32 v81,v81,s54,v14\n"
"v_fma_f32 v82,v82,s54,v14\n"
MF "v[44:47],a[204:207],a[192:195],v[44:47]\n"
"v_fma_f32 v83,v83,s54,v14\n"
"v_fma_f32 v84,v84,s54,v14\n"
MF "v[44:47],a[200:203],a[152:155],v[44:47]\n"
"v_fma_f32 v85,v85,s54,v14\n"
"v_fma_f32 v86,v86,s54,v14\n"
MF "v[52:55],a[200:203],a[156:159],v[52:55]\n"
"v_fma_f32 v87,v87,s54,v14\n"
CV "v171,v86,v87\n"
MF "v[52:55],a[204:207],a[196:199],v[52:55]\n"
"s_waitcnt lgkmcnt(0)\n"
"s_barrier\n"
MF "v[16:19],a[208:211],a[60:63],v[16:19]\n"
"s_add_u32 m0,s65,49152\n"
CV "v170,v84,v85\n"
CV "v169,v82,v83\n"
MF "v[16:19],a[212:215],a[100:103],v[16:19]\n"
LD "v8,s[36:39],s73 offen nt lds\n"
MF "v[24:27],a[212:215],a[104:107],v[24:27]\n"
"s_add_u32 m0,s65,50048\n"
CV "v168,v80,v81\n"
"v_fma_f32 v72,v72,s54,v14\n"
MF "v[24:27],a[208:211],a[64:67],v[24:27]\n"
LD "v8,s[36:39],s73 offen offset:128 nt lds\n"
MF "v[32:35],a[208:211],a[68:71],v[32:35]\n"
"v_fma_f32 v73,v73,s54,v14\n"
"v_fma_f32 v74,v74,s54,v14\n"
MF "v[32:35],a[212:215],a[108:111],v[32:35]\n"
"s_add_u32 m0,s65,50944\n"
"v_fma_f32 v75,v75,s54,v14\n"
"v_fma_f32 v76,v76,s54,v14\n"
MF "v[40:43],a[212:215],a[112:115],v[40:43]\n"
LD "v8,s[36:39],s73 offen offset:256 nt lds\n"
MF "v[40:43],a[208:211],a[72:75],v[40:43]\n"
"v_fma_f32 v77,v77,s54,v14\n"
"v_fma_f32 v78,v78,s54,v14\n"
MF "v[48:51],a[208:211],a[76:79],v[48:51]\n"
"s_add_u32 m0,s65,49152\n"
"v_fma_f32 v79,v79,s54,v14\n"
CV "v167,v78,v79\n"
MF "v[48:51],a[212:215],a[116:119],v[48:51]\n"
LD "v8,s[36:39],s73 offen offset:3072 nt lds\n"
MF "v[48:51],a[216:219],a[156:159],v[48:51]\n"
"s_add_u32 m0,s65,50048\n"
CV "v166,v76,v77\n"
CV "v165,v74,v75\n"
MF "v[48:51],a[220:223],a[196:199],v[48:51]\n"
LD "v8,s[36:39],s73 offen offset:3200 nt lds\n"
MF "v[40:43],a[220:223],a[192:195],v[40:43]\n"
CV "v164,v72,v73\n"
"v_fma_f32 v64,v64,s54,v14\n"
MF "v[40:43],a[216:219],a[152:155],v[40:43]\n"
"s_add_u32 m0,s65,50944\n"
"v_fma_f32 v65,v65,s54,v14\n"
"v_fma_f32 v66,v66,s54,v14\n"
MF "v[32:35],a[216:219],a[148:151],v[32:35]\n"
LD "v8,s[36:39],s73 offen offset:3328 nt lds\n"
MF "v[32:35],a[220:223],a[188:191],v[32:35]\n"
"v_fma_f32 v67,v67,s54,v14\n"
"v_fma_f32 v68,v68,s54,v14\n"
MF "v[24:27],a[220:223],a[184:187],v[24:27]\n"
"v_fma_f32 v69,v69,s54,v14\n"
"v_fma_f32 v70,v70,s54,v14\n"
MF "v[24:27],a[216:219],a[144:147],v[24:27]\n"
"v_fma_f32 v71,v71,s54,v14\n"
CV "v163,v70,v71\n"
MF "v[16:19],a[216:219],a[140:143],v[16:19]\n"
CV "v162,v68,v69\n"
CV "v161,v66,v67\n"
MF "v[16:19],a[220:223],a[180:183],v[16:19]\n"
CV "v160,v64,v65\n"
"v_pk_fma_f32 v[56:57],v[56:57],s[54:55],v[14:15] op_sel_hi:[1,1,0]\n"
"v_pk_fma_f32 v[58:59],v[58:59],s[54:55],v[14:15] op_sel_hi:[1,1,0]\n"
"v_pk_fma_f32 v[60:61],v[60:61],s[54:55],v[14:15] op_sel_hi:[1,1,0]\n"
"v_pk_fma_f32 v[62:63],v[62:63],s[54:55],v[14:15] op_sel_hi:[1,1,0]\n"
CV "v159,v62,v63\n"
CV "v158,v60,v61\n"
CV "v157,v58,v59\n"
CV "v156,v56,v57\n"
"v_pk_mul_f32 v[48:49],v[48:49],s[54:55]\n"
"v_pk_mul_f32 v[50:51],v[50:51],s[54:55]\n"
"v_pk_mul_f32 v[52:53],v[52:53],s[54:55]\n"
"v_pk_mul_f32 v[54:55],v[54:55],s[54:55]\n"
CV "v155,v54,v55\n"
CV "v154,v52,v53\n"
CV "v153,v50,v51\n"
CV "v152,v48,v49\n"
"v_pk_mul_f32 v[40:41],v[40:41],s[54:55]\n"
"v_pk_mul_f32 v[42:43],v[42:43],s[54:55]\n"
"v_pk_mul_f32 v[44:45],v[44:45],s[54:55]\n"
"v_pk_mul_f32 v[46:47],v[46:47],s[54:55]\n"
CV "v151,v46,v47\n"
CV "v150,v44,v45\n"
CV "v149,v42,v43\n"
CV "v148,v40,v41\n"
"v_pk_mul_f32 v[32:33],v[32:33],s[54:55]\n"
"v_pk_mul_f32 v[34:35],v[34:35],s[54:55]\n"
"v_pk_mul_f32 v[36:37],v[36:37],s[54:55]\n"
"v_pk_mul_f32 v[38:39],v[38:39],s[54:55]\n"
CV "v147,v38,v39\n"
CV "v146,v36,v37\n"
CV "v145,v34,v35\n"
CV "v144,v32,v33\n"
"v_pk_mul_f32 v[24:25],v[24:25],s[54:55]\n"
"v_pk_mul_f32 v[26:27],v[26:27],s[54:55]\n"
"v_pk_mul_f32 v[28:29],v[28:29],s[54:55]\n"
"v_pk_mul_f32 v[30:31],v[30:31],s[54:55]\n"
CV "v143,v30,v31\n"
CV "v142,v28,v29\n"
CV "v141,v26,v27\n"
CV "v140,v24,v25\n"
"v_pk_mul_f32 v[16:17],v[16:17],s[54:55]\n"
"v_pk_mul_f32 v[18:19],v[18:19],s[54:55]\n"
"v_pk_mul_f32 v[20:21],v[20:21],s[54:55]\n"
"v_pk_mul_f32 v[22:23],v[22:23],s[54:55]\n"
CV "v139,v22,v23\n"
CV "v138,v20,v21\n"
CV "v137,v18,v19\n"
CV "v136,v16,v17\n"
"s_cmp_eq_u32 s57,s69\n"
"s_cbranch_scc1 .LattB\n"
AR "v0,a244\n"
AR "v2,a245\n"
"v_xor_b32 v1,64,v0\n"
"v_add_u32 v2,49152,v2\n"
"v_xor_b32 v3,32,v2\n"
"v_xor_b32 v4,64,v2\n"
"v_xor_b32 v5,96,v2\n"
"s_mov_b32 s61,7\n"
"s_memtime s[52:53]\n"
"s_memrealtime s[100:101]\n"
"s_waitcnt lgkmcnt(0)\n"
"s_sub_u32 s53,s52,s80\n"
"s_cmp_gt_u32 s53,30000\n"
"s_cselect_b32 s101,1,0\n"
"s_cmp_eq_u32 s87,2\n"
"s_cselect_b32 s87,s101,s87\n"
"s_cmp_eq_u32 s87,1\n"
"s_cmp_eq_u32 s57,0\n"
"s_mov_b32 s101,0\n"
"s_cselect_b32 s101,130,s101\n"
"s_cmp_eq_u32 s87,1\n"
"s_cselect_b32 s97,s101,0\n"
"s_cselect_b32 s21,0,0\n"
"s_add_u32 s21,s21,s100\n"
".Latt2x:\n"
"s_cmp_eq_u32 s97,0\n"
"s_cselect_b32 s95,0x47800000,0\n"
"s_mov_b32 s77,s95\n"
"s_waitcnt vmcnt(12) lgkmcnt(0)\n"
"s_barrier\n"
"ds_read_b128 v[16:19],v0\n"
"ds_read_b128 v[20:23],v1\n"
"ds_read_b128 v[24:27],v0 offset:1024\n"
"s_cmp_eq_u32 s57,1\n"
"s_cbranch_scc1 .Lt0kx\n"
".Lt0nx:\n"
"s_waitcnt lgkmcnt(2)\n"
MF "v[72:75],v[16:19],v[136:139],0\n"
"s_add_u32 s73,s73,12288\n"
"ds_read_b128 v[28:31],v1 offset:1024\n"
AR "v10,a224\n"
MF "v[76:79],v[16:19],v[140:143],0\n"
"s_cmp_eq_u32 s73,s19\n"
"s_cselect_b32 s73,s18,s73\n"
"s_add_u32 m0,s65,73728\n"
MF "v[80:83],v[16:19],v[144:147],0\n"
LD "v8,s[36:39],s73 offen nt lds\n"
MF "v[84:87],v[16:19],v[148:151],0\n"
"v_max_f32_dpp v10,|v10|,|v10| quad_perm:[1,0,3,2] row_mask:0xf bank_mask:0xf\n"
AR "v11,a225\n"
MF "v[88:91],v[16:19],v[152:155],0\n"
"s_waitcnt lgkmcnt(2)\n"
"ds_read_b128 v[16:19],v0 offset:2048\n"
AR "v12,a226\n"
MF "v[72:75],v[20:23],v[156:159],v[72:75]\n"
"s_add_u32 m0,s65,74624\n"
"v_max_f32_dpp v10,v10,v10 quad_perm:[2,3,0,1] row_mask:0xf bank_mask:0xf\n"
"v_max_f32_dpp v11,|v11|,|v11| quad_perm:[1,0,3,2] row_mask:0xf bank_mask:0xf\n"
MF "v[76:79],v[20:23],v[160:163],v[76:79]\n"
LD "v8,s[36:39],s73 offen offset:128 nt lds\n"
MF "v[80:83],v[20:23],v[164:167],v[80:83]\n"
"v_max_f32_dpp v10,v10,v10 row_half_mirror row_mask:0xf bank_mask:0xf\n"
"v_max_f32_dpp v11,v11,v11 quad_perm:[2,3,0,1] row_mask:0xf bank_mask:0xf\n"
MF "v[84:87],v[20:23],v[168:171],v[84:87]\n"
"v_readlane_b32 s13,v10,0\n"
"v_readlane_b32 s0,v10,24\n"
MF "v[88:91],v[20:23],v[172:175],v[88:91]\n"
"s_waitcnt lgkmcnt(2)\n"
"ds_read_b128 v[20:23],v1 offset:2048\n"
"v_readlane_b32 s1,v10,32\n"
MF "v[72:75],v[24:27],v[176:179],v[72:75]\n"
"s_add_u32 m0,s65,75520\n"
"v_readlane_b32 s2,v10,56\n"
"v_max_f32_dpp v11,v11,v11 row_half_mirror row_mask:0xf bank_mask:0xf\n"
MF "v[76:79],v[24:27],v[180:183],v[76:79]\n"
LD "v8,s[36:39],s73 offen offset:256 nt lds\n"
MF "v[80:83],v[24:27],v[184:187],v[80:83]\n"
"s_min_u32 s13,s13,s0\n"
"v_readlane_b32 s11,v11,0\n"
"v_readlane_b32 s3,v11,24\n"
MF "v[84:87],v[24:27],v[188:191],v[84:87]\n"
"s_min_u32 s1,s1,s2\n"
"v_readlane_b32 s4,v11,32\n"
"v_readlane_b32 s5,v11,56\n"
MF "v[88:91],v[24:27],v[192:195],v[88:91]\n"
"s_waitcnt lgkmcnt(2)\n"
"ds_read_b128 v[24:27],v0 offset:6144\n"
"v_max_f32_dpp v12,|v12|,|v12| quad_perm:[1,0,3,2] row_mask:0xf bank_mask:0xf\n"
MF "v[72:75],v[28:31],v[196:199],v[72:75]\n"
"s_add_u32 m0,s65,73728\n"
MF "v[76:79],v[28:31],v[200:203],v[76:79]\n"
LD "v8,s[36:39],s73 offen offset:3072 nt lds\n"
MF "v[80:83],v[28:31],v[204:207],v[80:83]\n"
"s_min_u32 s13,s13,s1\n"
"v_max_f32_dpp v12,v12,v12 quad_perm:[2,3,0,1] row_mask:0xf bank_mask:0xf\n"
MF "v[84:87],v[28:31],v[208:211],v[84:87]\n"
"s_and_b32 s13,s13,0x7f800000\n"
MF "v[88:91],v[28:31],v[212:215],v[88:91]\n"
"s_waitcnt lgkmcnt(2)\n"
"ds_read_b128 v[28:31],v1 offset:6144\n"
"v_max_f32_dpp v12,v12,v12 row_half_mirror row_mask:0xf bank_mask:0xf\n"
MF "v[72:75],v[16:19],v[216:219],v[72:75]\n"
"s_add_u32 m0,s65,74624\n"
"v_readlane_b32 s10,v12,0\n"
"v_readlane_b32 s6,v12,24\n"
MF "v[76:79],v[16:19],v[220:223],v[76:79]\n"
LD "v8,s[36:39],s73 offen offset:3200 nt lds\n"
MF "v[80:83],v[16:19],v[224:227],v[80:83]\n"
"s_sub_u32 s13,s13,0x14000000\n"
"v_readlane_b32 s7,v12,32\n"
"v_readlane_b32 s8,v12,56\n"
MF "v[84:87],v[16:19],v[228:231],v[84:87]\n"
"s_cmp_lt_u32 s13,0x50000000\n"
"s_cselect_b32 s13,s13,0xed400000\n"
MF "v[88:91],v[16:19],v[232:235],v[88:91]\n"
"s_waitcnt lgkmcnt(2)\n"
"ds_read_b128 v[16:19],v0 offset:7168\n"
MF "v[72:75],v[20:23],v[236:239],v[72:75]\n"
"s_add_u32 m0,s65,75520\n"
MF "v[76:79],v[20:23],v[240:243],v[76:79]\n"
LD "v8,s[36:39],s73 offen offset:3328 nt lds\n"
MF "v[80:83],v[20:23],v[244:247],v[80:83]\n"
"s_add_u32 s13,s13,0x12c00000\n"
MF "v[84:87],v[20:23],v[248:251],v[84:87]\n"
"s_min_u32 s11,s11,s3\n"
MF "v[88:91],v[20:23],v[252:255],v[88:91]\n"
"s_waitcnt lgkmcnt(2)\n"
"ds_read_b128 v[20:23],v1 offset:7168\n"
MF "v[112:115],v[24:27],v[136:139],0\n"
"s_min_u32 s4,s4,s5\n"
MF "v[116:119],v[24:27],v[140:143],0\n"
"s_min_u32 s11,s11,s4\n"
MF "v[120:123],v[24:27],v[144:147],0\n"
"s_and_b32 s11,s11,0x7f800000\n"
MF "v[124:127],v[24:27],v[148:151],0\n"
"s_sub_u32 s11,s11,0x14000000\n"
MF "v[128:131],v[24:27],v[152:155],0\n"
"s_waitcnt lgkmcnt(2)\n"
"ds_read_b128 v[24:27],v0 offset:8192\n"
MF "v[112:115],v[28:31],v[156:159],v[112:115]\n"
"s_min_u32 s10,s10,s6\n"
"v_max3_f32 v52,v72,v73,v74\n"
"v_max_f32 v52,v52,v75\n"
MF "v[116:119],v[28:31],v[160:163],v[116:119]\n"
"s_min_u32 s7,s7,s8\n"
"v_max3_f32 v53,v76,v77,v78\n"
"v_max_f32 v53,v53,v79\n"
MF "v[120:123],v[28:31],v[164:167],v[120:123]\n"
"s_min_u32 s10,s10,s7\n"
"v_max3_f32 v54,v80,v81,v82\n"
"v_max_f32 v54,v54,v83\n"
MF "v[124:127],v[28:31],v[168:171],v[124:127]\n"
"v_max3_f32 v55,v84,v85,v86\n"
"v_max_f32 v55,v55,v87\n"
MF "v[128:131],v[28:31],v[172:175],v[128:131]\n"
"s_waitcnt lgkmcnt(2)\n"
"ds_read_b128 v[28:31],v1 offset:8192\n"
"v_max3_f32 v56,v88,v89,v90\n"
MF "v[112:115],v[16:19],v[176:179],v[112:115]\n"
".Lpg9b:\n"
"s_cbranch_execz .Lpgpsl_b\n"
"s_cmp_lt_u32 s11,0x50000000\n"
"s_cselect_b32 s11,s11,0xed400000\n"
"v_max_f32 v56,v56,v91\n"
MF "v[116:119],v[16:19],v[180:183],v[116:119]\n"
"s_add_u32 s11,s11,0x12c00000\n"
MF "v[120:123],v[16:19],v[184:187],v[120:123]\n"
MF "v[124:127],v[16:19],v[188:191],v[124:127]\n"
MF "v[128:131],v[16:19],v[192:195],v[128:131]\n"
"s_waitcnt lgkmcnt(2)\n"
MF "v[112:115],v[20:23],v[196:199],v[112:115]\n"
MF "v[116:119],v[20:23],v[200:203],v[116:119]\n"
"s_waitcnt lgkmcnt(0)\n"
"s_barrier\n"
MF "v[120:123],v[20:23],v[204:207],v[120:123]\n"
"s_add_u32 s72,s72,6144\n"
MF "v[124:127],v[20:23],v[208:211],v[124:127]\n"
"s_cmp_eq_u32 s72,s17\n"
"s_cselect_b32 s72,s16,s72\n"
"s_add_u32 m0,s64,0\n"
MF "v[128:131],v[20:23],v[212:215],v[128:131]\n"
LD "v7,s[32:35],s72 offen nt lds\n"
MF "v[112:115],v[24:27],v[216:219],v[112:115]\n"
"s_add_u32 m0,s64,896\n"
MF "v[116:119],v[24:27],v[220:223],v[116:119]\n"
LD "v7,s[32:35],s72 offen offset:128 nt lds\n"
MF "v[120:123],v[24:27],v[224:227],v[120:123]\n"
"s_add_u32 m0,s64,1792\n"
MF "v[124:127],v[24:27],v[228:231],v[124:127]\n"
LD "v7,s[32:35],s72 offen offset:256 nt lds\n"
MF "v[128:131],v[24:27],v[232:235],v[128:131]\n"
"s_add_u32 s72,s72,6144\n"
"s_add_u32 m0,s64,6144\n"
MF "v[112:115],v[28:31],v[236:239],v[112:115]\n"
LD "v7,s[32:35],s72 offen nt lds\n"
MF "v[116:119],v[28:31],v[240:243],v[116:119]\n"
"s_add_u32 m0,s64,7040\n"
MF "v[120:123],v[28:31],v[244:247],v[120:123]\n"
LD "v7,s[32:35],s72 offen offset:128 nt lds\n"
MF "v[124:127],v[28:31],v[248:251],v[124:127]\n"
"s_add_u32 m0,s64,7936\n"
MF "v[128:131],v[28:31],v[252:255],v[128:131]\n"
LD "v7,s[32:35],s72 offen offset:256 nt lds\n"
"s_branch .Lt0ex\n"
".Lpgt0kx_d:\n"
"s_cbranch_execz .Lisr2\n"
".Lt0kx:\n"
"s_lshl_b32 s58,s56,10\n"
"s_add_u32 s58,s58,73728\n"
"v_add_u32 v32,s58,v9\n"
"v_add_u32 v33,65536,v32\n"
"s_waitcnt lgkmcnt(2)\n"
MF "v[72:75],v[16:19],v[136:139],0\n"
"s_add_u32 s73,s73,12288\n"
"ds_read_b128 v[28:31],v1 offset:1024\n"
AR "v10,a224\n"
MF "v[76:79],v[16:19],v[140:143],0\n"
"s_cmp_eq_u32 s73,s19\n"
"s_cselect_b32 s73,s18,s73\n"
"s_add_u32 m0,s65,73728\n"
MF "v[80:83],v[16:19],v[144:147],0\n"
LD "v8,s[36:39],s73 offen nt lds\n"
MF "v[84:87],v[16:19],v[148:151],0\n"
"v_max_f32_dpp v10,|v10|,|v10| quad_perm:[1,0,3,2] row_mask:0xf bank_mask:0xf\n"
AR "v11,a225\n"
MF "v[88:91],v[16:19],v[152:155],0\n"
"s_waitcnt lgkmcnt(2)\n"
"ds_read_b128 v[16:19],v0 offset:2048\n"
AR "v12,a226\n"
MF "v[72:75],v[20:23],v[156:159],v[72:75]\n"
"s_add_u32 m0,s65,74624\n"
"v_max_f32_dpp v10,v10,v10 quad_perm:[2,3,0,1] row_mask:0xf bank_mask:0xf\n"
"v_max_f32_dpp v11,|v11|,|v11| quad_perm:[1,0,3,2] row_mask:0xf bank_mask:0xf\n"
MF "v[76:79],v[20:23],v[160:163],v[76:79]\n"
LD "v8,s[36:39],s73 offen offset:128 nt lds\n"
MF "v[80:83],v[20:23],v[164:167],v[80:83]\n"
"v_max_f32_dpp v10,v10,v10 row_half_mirror row_mask:0xf bank_mask:0xf\n"
"v_max_f32_dpp v11,v11,v11 quad_perm:[2,3,0,1] row_mask:0xf bank_mask:0xf\n"
MF "v[84:87],v[20:23],v[168:171],v[84:87]\n"
"v_readlane_b32 s13,v10,0\n"
"v_readlane_b32 s0,v10,24\n"
MF "v[88:91],v[20:23],v[172:175],v[88:91]\n"
"s_waitcnt lgkmcnt(2)\n"
"ds_read_b128 v[20:23],v1 offset:2048\n"
"v_readlane_b32 s1,v10,32\n"
MF "v[72:75],v[24:27],v[176:179],v[72:75]\n"
"s_add_u32 m0,s65,75520\n"
"v_readlane_b32 s2,v10,56\n"
"v_max_f32_dpp v11,v11,v11 row_half_mirror row_mask:0xf bank_mask:0xf\n"
MF "v[76:79],v[24:27],v[180:183],v[76:79]\n"
LD "v8,s[36:39],s73 offen offset:256 nt lds\n"
MF "v[80:83],v[24:27],v[184:187],v[80:83]\n"
"s_min_u32 s13,s13,s0\n"
"v_readlane_b32 s11,v11,0\n"
"v_readlane_b32 s3,v11,24\n"
MF "v[84:87],v[24:27],v[188:191],v[84:87]\n"
"s_min_u32 s1,s1,s2\n"
"v_readlane_b32 s4,v11,32\n"
"v_readlane_b32 s5,v11,56\n"
MF "v[88:91],v[24:27],v[192:195],v[88:91]\n"
"s_waitcnt lgkmcnt(2)\n"
"ds_read_b128 v[24:27],v0 offset:6144\n"
"v_max_f32_dpp v12,|v12|,|v12| quad_perm:[1,0,3,2] row_mask:0xf bank_mask:0xf\n"
MF "v[72:75],v[28:31],v[196:199],v[72:75]\n"
"s_add_u32 m0,s65,73728\n"
"ds_write_b128 v32,a[0:3] offset:24576\n"
MF "v[76:79],v[28:31],v[200:203],v[76:79]\n"
LD "v8,s[36:39],s73 offen offset:3072 nt lds\n"
MF "v[80:83],v[28:31],v[204:207],v[80:83]\n"
"s_min_u32 s13,s13,s1\n"
"v_max_f32_dpp v12,v12,v12 quad_perm:[2,3,0,1] row_mask:0xf bank_mask:0xf\n"
AR "v62,a20\n"
MF "v[84:87],v[28:31],v[208:211],v[84:87]\n"
"s_and_b32 s13,s13,0x7f800000\n"
"ds_write_b128 v32,a[4:7] offset:28672\n"
MF "v[88:91],v[28:31],v[212:215],v[88:91]\n"
"s_waitcnt lgkmcnt(4)\n"
"ds_read_b128 v[28:31],v1 offset:6144\n"
"v_max_f32_dpp v12,v12,v12 row_half_mirror row_mask:0xf bank_mask:0xf\n"
MF "v[72:75],v[16:19],v[216:219],v[72:75]\n"
"s_add_u32 m0,s65,74624\n"
"v_readlane_b32 s10,v12,0\n"
"v_readlane_b32 s6,v12,24\n"
MF "v[76:79],v[16:19],v[220:223],v[76:79]\n"
LD "v8,s[36:39],s73 offen offset:3200 nt lds\n"
MF "v[80:83],v[16:19],v[224:227],v[80:83]\n"
"s_sub_u32 s13,s13,0x14000000\n"
"v_readlane_b32 s7,v12,32\n"
"v_readlane_b32 s8,v12,56\n"
MF "v[84:87],v[16:19],v[228:231],v[84:87]\n"
"s_cmp_lt_u32 s13,0x50000000\n"
"s_cselect_b32 s13,s13,0xed400000\n"
AR "v63,a21\n"
MF "v[88:91],v[16:19],v[232:235],v[88:91]\n"
"s_waitcnt lgkmcnt(4)\n"
"ds_read_b128 v[16:19],v0 offset:7168\n"
AR "v64,a22\n"
MF "v[72:75],v[20:23],v[236:239],v[72:75]\n"
"s_add_u32 m0,s65,75520\n"
"ds_write_b128 v32,a[8:11] offset:32768\n"
MF "v[76:79],v[20:23],v[240:243],v[76:79]\n"
LD "v8,s[36:39],s73 offen offset:3328 nt lds\n"
MF "v[80:83],v[20:23],v[244:247],v[80:83]\n"
"s_add_u32 s13,s13,0x12c00000\n"
"ds_write_b128 v32,a[12:15] offset:36864\n"
MF "v[84:87],v[20:23],v[248:251],v[84:87]\n"
"s_min_u32 s11,s11,s3\n"
"ds_write_b128 v32,a[16:19] offset:40960\n"
MF "v[88:91],v[20:23],v[252:255],v[88:91]\n"
"s_waitcnt lgkmcnt(7)\n"
"ds_read_b128 v[20:23],v1 offset:7168\n"
AR "v65,a23\n"
MF "v[112:115],v[24:27],v[136:139],0\n"
"s_min_u32 s4,s4,s5\n"
"ds_write_b128 v32,a[40:43] offset:45056\n"
MF "v[116:119],v[24:27],v[140:143],0\n"
"s_min_u32 s11,s11,s4\n"
"ds_write_b128 v32,a[44:47] offset:49152\n"
MF "v[120:123],v[24:27],v[144:147],0\n"
"s_and_b32 s11,s11,0x7f800000\n"
"ds_write_b128 v32,a[48:51] offset:53248\n"
MF "v[124:127],v[24:27],v[148:151],0\n"
"s_sub_u32 s11,s11,0x14000000\n"
"ds_write_b128 v32,a[52:55] offset:57344\n"
MF "v[128:131],v[24:27],v[152:155],0\n"
"s_waitcnt lgkmcnt(9)\n"
"ds_read_b128 v[24:27],v0 offset:8192\n"
AR "v66,a24\n"
MF "v[112:115],v[28:31],v[156:159],v[112:115]\n"
"s_min_u32 s10,s10,s6\n"
"v_max3_f32 v52,v72,v73,v74\n"
"v_max_f32 v52,v52,v75\n"
MF "v[116:119],v[28:31],v[160:163],v[116:119]\n"
"s_min_u32 s7,s7,s8\n"
"v_max3_f32 v53,v76,v77,v78\n"
"v_max_f32 v53,v53,v79\n"
MF "v[120:123],v[28:31],v[164:167],v[120:123]\n"
"s_min_u32 s10,s10,s7\n"
"v_max3_f32 v54,v80,v81,v82\n"
"v_max_f32 v54,v54,v83\n"
MF "v[124:127],v[28:31],v[168:171],v[124:127]\n"
"v_max3_f32 v55,v84,v85,v86\n"
"v_max_f32 v55,v55,v87\n"
MF "v[128:131],v[28:31],v[172:175],v[128:131]\n"
"s_waitcnt lgkmcnt(9)\n"
"ds_read_b128 v[28:31],v1 offset:8192\n"
"v_max3_f32 v56,v88,v89,v90\n"
MF "v[112:115],v[16:19],v[176:179],v[112:115]\n"
"s_cmp_lt_u32 s11,0x50000000\n"
"s_cselect_b32 s11,s11,0xed400000\n"
"v_max_f32 v56,v56,v91\n"
MF "v[116:119],v[16:19],v[180:183],v[116:119]\n"
"s_add_u32 s11,s11,0x12c00000\n"
"ds_write_b128 v32,a[56:59] offset:61440\n"
MF "v[120:123],v[16:19],v[184:187],v[120:123]\n"
"ds_write_b128 v33,a[80:83]\n"
MF "v[124:127],v[16:19],v[188:191],v[124:127]\n"
"ds_write_b128 v33,a[84:87] offset:4096\n"
MF "v[128:131],v[16:19],v[192:195],v[128:131]\n"
"s_waitcnt lgkmcnt(9)\n"
"ds_write_b128 v33,a[88:91] offset:8192\n"
MF "v[112:115],v[20:23],v[196:199],v[112:115]\n"
AR "v67,a25\n"
AR "v68,a26\n"
MF "v[116:119],v[20:23],v[200:203],v[116:119]\n"
"s_waitcnt lgkmcnt(0)\n"
"s_barrier\n"
MF "v[120:123],v[20:23],v[204:207],v[120:123]\n"
"s_add_u32 s72,s72,6144\n"
"ds_write_b128 v33,a[92:95] offset:12288\n"
MF "v[124:127],v[20:23],v[208:211],v[124:127]\n"
"s_cmp_eq_u32 s72,s17\n"
"s_cselect_b32 s72,s16,s72\n"
"s_add_u32 m0,s64,0\n"
MF "v[128:131],v[20:23],v[212:215],v[128:131]\n"
LD "v7,s[32:35],s72 offen nt lds\n"
MF "v[112:115],v[24:27],v[216:219],v[112:115]\n"
"s_add_u32 m0,s64,896\n"
"ds_write_b128 v33,a[96:99] offset:16384\n"
MF "v[116:119],v[24:27],v[220:223],v[116:119]\n"
LD "v7,s[32:35],s72 offen offset:128 nt lds\n"
MF "v[120:123],v[24:27],v[224:227],v[120:123]\n"
"s_add_u32 m0,s64,1792\n"
AR "v69,a27\n"
AR "v70,a28\n"
MF "v[124:127],v[24:27],v[228:231],v[124:127]\n"
LD "v7,s[32:35],s72 offen offset:256 nt lds\n"
MF "v[128:131],v[24:27],v[232:235],v[128:131]\n"
"s_add_u32 s72,s72,6144\n"
"s_add_u32 m0,s64,6144\n"
AR "v71,a29\n"
MF "v[112:115],v[28:31],v[236:239],v[112:115]\n"
LD "v7,s[32:35],s72 offen nt lds\n"
MF "v[116:119],v[28:31],v[240:243],v[116:119]\n"
"s_add_u32 m0,s64,7040\n"
"ds_write_b128 v33,a[120:123] offset:20480\n"
MF "v[120:123],v[28:31],v[244:247],v[120:123]\n"
LD "v7,s[32:35],s72 offen offset:128 nt lds\n"
MF "v[124:127],v[28:31],v[248:251],v[124:127]\n"
"s_add_u32 m0,s64,7936\n"
AR "v132,a30\n"
AR "v133,a31\n"
MF "v[128:131],v[28:31],v[252:255],v[128:131]\n"
LD "v7,s[32:35],s72 offen offset:256 nt lds\n"
AR "v134,a32\n"
AR "v135,a33\n"
AR "v6,a34\n"
AR "v13,a35\n"
AR "v14,a36\n"
AR "v15,a37\n"
"v_accvgpr_mov_b32 a254,a38\n"
"v_accvgpr_mov_b32 a255,a39\n"
"v_accvgpr_mov_b32 a240,a168\n"
"v_accvgpr_mov_b32 a241,a169\n"
"v_accvgpr_mov_b32 a242,a170\n"
"v_accvgpr_mov_b32 a243,a171\n"
"v_accvgpr_mov_b32 a244,a172\n"
"v_accvgpr_mov_b32 a245,a173\n"
"v_accvgpr_mov_b32 a246,a174\n"
"v_accvgpr_mov_b32 a247,a175\n"
"v_accvgpr_mov_b32 a248,a176\n"
"v_accvgpr_mov_b32 a249,a177\n"
"v_accvgpr_mov_b32 a250,a178\n"
"v_accvgpr_mov_b32 a251,a179\n"
".Lpgt0ex_b:\n"
"s_cbranch_execz .Lpg2a\n"
".Lt0ex:\n"
"s_nop 1\n"
"s_nop 0\n"
"v_max3_f32 v52,v52,v112,v113\n"
"v_max3_f32 v53,v53,v116,v117\n"
"v_max3_f32 v54,v54,v120,v121\n"
"v_max3_f32 v55,v55,v124,v125\n"
"v_max3_f32 v56,v56,v128,v129\n"
"v_max3_f32 v52,v52,v114,v115\n"
"v_max3_f32 v53,v53,v118,v119\n"
"v_max3_f32 v54,v54,v122,v123\n"
"v_max3_f32 v55,v55,v126,v127\n"
"v_max3_f32 v56,v56,v130,v131\n"
"v_mov_b32 v57,v52\n"
"v_mov_b32 v58,v53\n"
"v_mov_b32 v59,v54\n"
"v_mov_b32 v60,v55\n"
"v_mov_b32 v61,v56\n"
"v_permlane16_swap_b32 v52,v57\n"
"v_permlane16_swap_b32 v53,v58\n"
"v_permlane16_swap_b32 v54,v59\n"
"v_permlane16_swap_b32 v55,v60\n"
"v_permlane16_swap_b32 v56,v61\n"
"v_max_f32 v52,v52,v57\n"
"v_max_f32 v53,v53,v58\n"
"v_max_f32 v54,v54,v59\n"
"v_max_f32 v55,v55,v60\n"
"v_max_f32 v56,v56,v61\n"
"v_mov_b32 v57,v52\n"
"v_mov_b32 v58,v53\n"
"v_mov_b32 v59,v54\n"
"v_mov_b32 v60,v55\n"
"v_mov_b32 v61,v56\n"
"v_permlane32_swap_b32 v52,v57\n"
"v_permlane32_swap_b32 v53,v58\n"
"v_permlane32_swap_b32 v54,v59\n"
"v_permlane32_swap_b32 v55,v60\n"
"v_permlane32_swap_b32 v56,v61\n"
"v_max_f32 v52,v52,v57\n"
"v_max_f32 v53,v53,v58\n"
"v_max_f32 v54,v54,v59\n"
"v_max_f32 v55,v55,v60\n"
"v_max_f32 v56,v56,v61\n"
"v_sub_f32 v32,0xc1300000,v52\n"
"v_sub_f32 v36,0xc1300000,v53\n"
"v_sub_f32 v40,0xc1300000,v54\n"
"v_sub_f32 v44,0xc1300000,v55\n"
"v_sub_f32 v48,0xc1300000,v56\n"
"v_mov_b32 v33,v32\n"
"v_mov_b32 v34,v32\n"
"v_mov_b32 v35,v32\n"
"v_mov_b32 v37,v36\n"
"v_mov_b32 v38,v36\n"
"v_mov_b32 v39,v36\n"
"v_mov_b32 v41,v40\n"
"v_mov_b32 v42,v40\n"
"v_mov_b32 v43,v40\n"
"v_mov_b32 v45,v44\n"
"v_mov_b32 v46,v44\n"
"v_mov_b32 v47,v44\n"
"v_mov_b32 v49,v48\n"
"v_mov_b32 v50,v48\n"
"v_mov_b32 v51,v48\n"
"v_add_f32 v72,v72,v32\n"
"v_add_f32 v73,v73,v32\n"
"v_add_f32 v74,v74,v32\n"
"v_add_f32 v75,v75,v32\n"
"v_add_f32 v112,v112,v32\n"
"v_add_f32 v113,v113,v32\n"
"v_add_f32 v114,v114,v32\n"
"v_add_f32 v115,v115,v32\n"
"v_exp_f32 v72,v72\n"
"v_exp_f32 v73,v73\n"
"v_exp_f32 v74,v74\n"
"v_exp_f32 v75,v75\n"
"v_exp_f32 v112,v112\n"
"v_exp_f32 v113,v113\n"
"v_exp_f32 v114,v114\n"
"v_exp_f32 v115,v115\n"
"v_add_f32 v52,v72,v73\n"
"v_add_f32 v52,v52,v74\n"
"v_add_f32 v52,v52,v75\n"
"v_add_f32 v52,v52,v112\n"
"v_add_f32 v52,v52,v113\n"
"v_add_f32 v52,v52,v114\n"
"v_add_f32 v52,v52,v115\n"
CV "v72,v72,v73\n"
CV "v73,v74,v75\n"
CV "v74,v112,v113\n"
CV "v75,v114,v115\n"
"v_add_f32 v76,v76,v36\n"
"v_add_f32 v77,v77,v36\n"
"v_add_f32 v78,v78,v36\n"
"v_add_f32 v79,v79,v36\n"
"v_add_f32 v116,v116,v36\n"
"v_add_f32 v117,v117,v36\n"
"v_add_f32 v118,v118,v36\n"
"v_add_f32 v119,v119,v36\n"
"v_exp_f32 v76,v76\n"
"v_exp_f32 v77,v77\n"
"v_exp_f32 v78,v78\n"
"v_exp_f32 v79,v79\n"
"v_exp_f32 v116,v116\n"
"v_exp_f32 v117,v117\n"
"v_exp_f32 v118,v118\n"
"v_exp_f32 v119,v119\n"
"v_add_f32 v53,v76,v77\n"
"v_add_f32 v53,v53,v78\n"
"v_add_f32 v53,v53,v79\n"
"v_add_f32 v53,v53,v116\n"
"v_add_f32 v53,v53,v117\n"
"v_add_f32 v53,v53,v118\n"
"v_add_f32_e64 v53,v53,v119\n"
CV "v76,v76,v77\n"
CV "v77,v78,v79\n"
CV "v78,v116,v117\n"
CV "v79,v118,v119\n"
"v_add_f32 v80,v80,v40\n"
"v_add_f32 v81,v81,v40\n"
"v_add_f32 v82,v82,v40\n"
"v_add_f32 v83,v83,v40\n"
"v_add_f32 v120,v120,v40\n"
"v_add_f32 v121,v121,v40\n"
"v_add_f32 v122,v122,v40\n"
"v_add_f32 v123,v123,v40\n"
"v_exp_f32 v80,v80\n"
"v_exp_f32 v81,v81\n"
"v_exp_f32 v82,v82\n"
"v_exp_f32 v83,v83\n"
"v_exp_f32 v120,v120\n"
"v_exp_f32 v121,v121\n"
"v_exp_f32 v122,v122\n"
"v_exp_f32 v123,v123\n"
"v_add_f32 v54,v80,v81\n"
"v_add_f32 v54,v54,v82\n"
"v_add_f32 v54,v54,v83\n"
"v_add_f32 v54,v54,v120\n"
"v_add_f32 v54,v54,v121\n"
"v_add_f32 v54,v54,v122\n"
"v_add_f32_e64 v54,v54,v123\n"
CV "v80,v80,v81\n"
CV "v81,v82,v83\n"
CV "v82,v120,v121\n"
CV "v83,v122,v123\n"
"v_add_f32 v84,v84,v44\n"
"v_add_f32 v85,v85,v44\n"
"v_add_f32 v86,v86,v44\n"
"v_add_f32 v87,v87,v44\n"
"v_add_f32 v124,v124,v44\n"
"v_add_f32 v125,v125,v44\n"
"v_add_f32 v126,v126,v44\n"
"v_add_f32 v127,v127,v44\n"
"v_mov_b32 v55,0\n"
"v_add_f32 v88,v88,v48\n"
"v_add_f32 v89,v89,v48\n"
"v_add_f32 v90,v90,v48\n"
"v_add_f32 v91,v91,v48\n"
"v_add_f32 v128,v128,v48\n"
"v_add_f32 v129,v129,v48\n"
"v_add_f32 v130,v130,v48\n"
"v_add_f32 v131,v131,v48\n"
"v_mov_b32 v56,0\n"
"s_waitcnt vmcnt(18) lgkmcnt(0)\n"
"s_barrier\n"
"ds_read_b128 v[16:19],v0 offset:24576\n"
"ds_read_b128 v[20:23],v1 offset:24576\n"
AW "a220,s13\n"
AW "a221,s13\n"
AW "a222,s13\n"
AW "a223,s13\n"
"s_waitcnt lgkmcnt(1)\n"
MF "v[92:95],v[16:19],v[136:139],v[32:35]\n"
"s_nop 0\n"
"ds_read_b128 v[24:27],v0 offset:25600\n"
"ds_read_b128 v[28:31],v1 offset:25600\n"
"s_waitcnt lgkmcnt(2)\n"
MF "v[92:95],v[20:23],v[156:159],v[92:95]\n"
"v_exp_f32 v84,v84\n"
MF "v[96:99],v[20:23],v[160:163],v[36:39]\n"
"v_exp_f32 v85,v85\n"
MF "v[96:99],v[16:19],v[140:143],v[96:99]\n"
"v_exp_f32 v86,v86\n"
MF "v[100:103],v[16:19],v[144:147],v[40:43]\n"
"v_exp_f32 v87,v87\n"
MF "v[100:103],v[20:23],v[164:167],v[100:103]\n"
"v_add_f32 v55,v55,v84\n"
"v_add_f32 v55,v55,v85\n"
MF "v[104:107],v[20:23],v[168:171],v[44:47]\n"
CV "v84,v84,v85\n"
"v_add_f32 v55,v55,v86\n"
MF "v[104:107],v[16:19],v[148:151],v[104:107]\n"
"v_add_f32 v55,v55,v87\n"
CV "v85,v86,v87\n"
MF "v[108:111],v[16:19],v[152:155],v[48:51]\n"
"v_exp_f32 v124,v124\n"
MF "v[108:111],v[20:23],v[172:175],v[108:111]\n"
"s_waitcnt lgkmcnt(0)\n"
"ds_read_b128 v[16:19],v0 offset:26624\n"
"ds_read_b128 v[20:23],v1 offset:26624\n"
MF "v[108:111],v[24:27],v[192:195],v[108:111]\n"
"v_exp_f32 v125,v125\n"
MF "v[108:111],v[28:31],v[212:215],v[108:111]\n"
"v_exp_f32 v126,v126\n"
MF "v[104:107],v[28:31],v[208:211],v[104:107]\n"
"v_exp_f32 v127,v127\n"
MF "v[104:107],v[24:27],v[188:191],v[104:107]\n"
"v_add_f32 v55,v55,v124\n"
"v_add_f32 v55,v55,v125\n"
MF "v[100:103],v[24:27],v[184:187],v[100:103]\n"
CV "v86,v124,v125\n"
"v_add_f32 v55,v55,v126\n"
MF "v[100:103],v[28:31],v[204:207],v[100:103]\n"
"v_add_f32 v55,v55,v127\n"
CV "v87,v126,v127\n"
MF "v[96:99],v[28:31],v[200:203],v[96:99]\n"
"v_exp_f32 v88,v88\n"
MF "v[96:99],v[24:27],v[180:183],v[96:99]\n"
"v_exp_f32 v89,v89\n"
MF "v[92:95],v[24:27],v[176:179],v[92:95]\n"
"v_exp_f32 v90,v90\n"
MF "v[92:95],v[28:31],v[196:199],v[92:95]\n"
"s_waitcnt lgkmcnt(0)\n"
"ds_read_b128 v[24:27],v0 offset:30720\n"
"ds_read_b128 v[28:31],v1 offset:30720\n"
MF "v[92:95],v[16:19],v[216:219],v[92:95]\n"
"v_exp_f32 v91,v91\n"
MF "v[92:95],v[20:23],v[236:239],v[92:95]\n"
"v_add_f32 v56,v56,v88\n"
"v_add_f32 v56,v56,v89\n"
MF "v[96:99],v[20:23],v[240:243],v[96:99]\n"
CV "v88,v88,v89\n"
"v_add_f32 v56,v56,v90\n"
MF "v[96:99],v[16:19],v[220:223],v[96:99]\n"
"v_add_f32 v56,v56,v91\n"
CV "v89,v90,v91\n"
MF "v[100:103],v[16:19],v[224:227],v[100:103]\n"
"v_exp_f32 v128,v128\n"
MF "v[100:103],v[20:23],v[244:247],v[100:103]\n"
"v_exp_f32 v129,v129\n"
MF "v[104:107],v[20:23],v[248:251],v[104:107]\n"
"v_exp_f32 v130,v130\n"
MF "v[104:107],v[16:19],v[228:231],v[104:107]\n"
"v_exp_f32 v131,v131\n"
MF "v[108:111],v[16:19],v[232:235],v[108:111]\n"
"v_add_f32 v56,v56,v128\n"
"v_add_f32 v56,v56,v129\n"
MF "v[108:111],v[20:23],v[252:255],v[108:111]\n"
"s_waitcnt lgkmcnt(0)\n"
"ds_read_b128 v[16:19],v0 offset:31744\n"
"ds_read_b128 v[20:23],v1 offset:31744\n"
MF "v[112:115],v[24:27],v[136:139],v[32:35]\n"
CV "v90,v128,v129\n"
"v_add_f32 v56,v56,v130\n"
MF "v[112:115],v[28:31],v[156:159],v[112:115]\n"
"v_add_f32 v56,v56,v131\n"
CV "v91,v130,v131\n"
MF "v[116:119],v[28:31],v[160:163],v[36:39]\n"
"v_exp_f32 v92,v92\n"
MF "v[116:119],v[24:27],v[140:143],v[116:119]\n"
"v_exp_f32 v93,v93\n"
MF "v[120:123],v[24:27],v[144:147],v[40:43]\n"
"v_exp_f32 v94,v94\n"
MF "v[120:123],v[28:31],v[164:167],v[120:123]\n"
"v_exp_f32 v95,v95\n"
MF "v[124:127],v[28:31],v[168:171],v[44:47]\n"
"v_add_f32 v57,v52,v92\n"
"v_add_f32 v57,v57,v93\n"
MF "v[124:127],v[24:27],v[148:151],v[124:127]\n"
CV "v92,v92,v93\n"
"v_add_f32 v57,v57,v94\n"
MF "v[128:131],v[24:27],v[152:155],v[48:51]\n"
"v_add_f32 v57,v57,v95\n"
CV "v93,v94,v95\n"
MF "v[128:131],v[28:31],v[172:175],v[128:131]\n"
"s_waitcnt lgkmcnt(0)\n"
"ds_read_b128 v[24:27],v0 offset:32768\n"
"ds_read_b128 v[28:31],v1 offset:32768\n"
MF "v[128:131],v[16:19],v[192:195],v[128:131]\n"
"v_exp_f32 v96,v96\n"
MF "v[128:131],v[20:23],v[212:215],v[128:131]\n"
"v_exp_f32 v97,v97\n"
MF "v[124:127],v[20:23],v[208:211],v[124:127]\n"
"v_exp_f32 v98,v98\n"
MF "v[124:127],v[16:19],v[188:191],v[124:127]\n"
"v_exp_f32 v99,v99\n"
MF "v[120:123],v[16:19],v[184:187],v[120:123]\n"
"v_add_f32 v58,v53,v96\n"
"v_add_f32 v58,v58,v97\n"
MF "v[120:123],v[20:23],v[204:207],v[120:123]\n"
CV "v96,v96,v97\n"
"v_add_f32 v58,v58,v98\n"
MF "v[116:119],v[20:23],v[200:203],v[116:119]\n"
"v_add_f32 v58,v58,v99\n"
CV "v97,v98,v99\n"
MF "v[116:119],v[16:19],v[180:183],v[116:119]\n"
"s_waitcnt vmcnt(12) lgkmcnt(0)\n"
"s_barrier\n"
MF "v[112:115],v[16:19],v[176:179],v[112:115]\n"
"v_exp_f32 v100,v100\n"
MF "v[112:115],v[20:23],v[196:199],v[112:115]\n"
"s_nop 0\n"
TR "v[16:17],v2\n"
TR "v[18:19],v2 offset:6144\n"
MF "v[112:115],v[24:27],v[216:219],v[112:115]\n"
"s_add_u32 s72,s72,6144\n"
TR "v[20:21],v3\n"
TR "v[22:23],v3 offset:6144\n"
MF "v[112:115],v[28:31],v[236:239],v[112:115]\n"
"s_cmp_eq_u32 s72,s17\n"
"s_cselect_b32 s72,s16,s72\n"
"s_add_u32 m0,s64,24576\n"
MF "v[116:119],v[28:31],v[240:243],v[116:119]\n"
LD "v7,s[32:35],s72 offen nt lds\n"
MF "v[116:119],v[24:27],v[220:223],v[116:119]\n"
"s_add_u32 m0,s64,25472\n"
"v_exp_f32 v101,v101\n"
MF "v[120:123],v[24:27],v[224:227],v[120:123]\n"
LD "v7,s[32:35],s72 offen offset:128 nt lds\n"
MF "v[120:123],v[28:31],v[244:247],v[120:123]\n"
"v_exp_f32 v102,v102\n"
MF "v[124:127],v[28:31],v[248:251],v[124:127]\n"
"s_add_u32 m0,s64,26368\n"
"v_exp_f32 v103,v103\n"
MF "v[124:127],v[24:27],v[228:231],v[124:127]\n"
LD "v7,s[32:35],s72 offen offset:256 nt lds\n"
MF "v[128:131],v[24:27],v[232:235],v[128:131]\n"
"s_add_u32 s72,s72,6144\n"
"v_add_f32 v59,v54,v100\n"
"v_add_f32 v59,v59,v101\n"
MF "v[128:131],v[28:31],v[252:255],v[128:131]\n"
"s_waitcnt lgkmcnt(0)\n"
TR "v[24:25],v4\n"
TR "v[26:27],v4 offset:6144\n"
MF "a[0:3],v[16:19],v[72:75],a[220:223]\n"
"s_add_u32 m0,s64,30720\n"
TR "v[28:29],v5\n"
TR "v[30:31],v5 offset:6144\n"
MF "a[20:23],v[20:23],v[72:75],a[220:223]\n"
LD "v7,s[32:35],s72 offen nt lds\n"
MF "a[24:27],v[20:23],v[76:79],a[220:223]\n"
CV "v100,v100,v101\n"
"v_add_f32 v59,v59,v102\n"
MF "a[4:7],v[16:19],v[76:79],a[220:223]\n"
"s_add_u32 m0,s64,31616\n"
"v_add_f32 v59,v59,v103\n"
CV "v101,v102,v103\n"
MF "a[8:11],v[16:19],v[80:83],a[220:223]\n"
LD "v7,s[32:35],s72 offen offset:128 nt lds\n"
MF "a[28:31],v[20:23],v[80:83],a[220:223]\n"
"v_exp_f32 v104,v104\n"
MF "a[32:35],v[20:23],v[84:87],a[220:223]\n"
"s_add_u32 m0,s64,32512\n"
"v_exp_f32 v105,v105\n"
MF "a[12:15],v[16:19],v[84:87],a[220:223]\n"
LD "v7,s[32:35],s72 offen offset:256 nt lds\n"
MF "a[16:19],v[16:19],v[88:91],a[220:223]\n"
"v_exp_f32 v106,v106\n"
MF "a[36:39],v[20:23],v[88:91],a[220:223]\n"
"s_waitcnt lgkmcnt(0)\n"
TR "v[16:17],v2 offset:1024\n"
TR "v[18:19],v2 offset:7168\n"
MF "a[56:59],v[24:27],v[88:91],a[220:223]\n"
"s_nop 0\n"
TR "v[20:21],v3 offset:1024\n"
TR "v[22:23],v3 offset:7168\n"
MF "a[76:79],v[28:31],v[88:91],a[220:223]\n"
"v_exp_f32 v107,v107\n"
MF "a[72:75],v[28:31],v[84:87],a[220:223]\n"
"v_add_f32 v60,v55,v104\n"
"v_add_f32 v60,v60,v105\n"
MF "a[52:55],v[24:27],v[84:87],a[220:223]\n"
CV "v104,v104,v105\n"
"v_add_f32 v60,v60,v106\n"
MF "a[48:51],v[24:27],v[80:83],a[220:223]\n"
"v_add_f32 v60,v60,v107\n"
CV "v105,v106,v107\n"
MF "a[68:71],v[28:31],v[80:83],a[220:223]\n"
"v_exp_f32 v108,v108\n"
MF "a[64:67],v[28:31],v[76:79],a[220:223]\n"
"v_exp_f32 v109,v109\n"
MF "a[44:47],v[24:27],v[76:79],a[220:223]\n"
"v_exp_f32 v110,v110\n"
MF "a[40:43],v[24:27],v[72:75],a[220:223]\n"
"v_exp_f32 v111,v111\n"
MF "a[60:63],v[28:31],v[72:75],a[220:223]\n"
"s_waitcnt lgkmcnt(0)\n"
TR "v[24:25],v4 offset:1024\n"
TR "v[26:27],v4 offset:7168\n"
MF "a[80:83],v[16:19],v[72:75],a[220:223]\n"
".Lpg10b:\n"
"s_cbranch_execz .Lpg1a\n"
".Lpg10d:\n"
"s_cbranch_execz .Lisr2\n"
"s_nop 0\n"
TR "v[28:29],v5 offset:1024\n"
TR "v[30:31],v5 offset:7168\n"
MF "a[100:103],v[20:23],v[72:75],a[220:223]\n"
"v_add_f32 v61,v56,v108\n"
"v_add_f32 v61,v61,v109\n"
MF "a[104:107],v[20:23],v[76:79],a[220:223]\n"
CV "v108,v108,v109\n"
"v_add_f32 v61,v61,v110\n"
MF "a[84:87],v[16:19],v[76:79],a[220:223]\n"
"v_add_f32 v61,v61,v111\n"
CV "v109,v110,v111\n"
MF "a[88:91],v[16:19],v[80:83],a[220:223]\n"
"v_exp_f32 v112,v112\n"
MF "a[108:111],v[20:23],v[80:83],a[220:223]\n"
"v_exp_f32 v113,v113\n"
MF "a[112:115],v[20:23],v[84:87],a[220:223]\n"
"v_exp_f32 v114,v114\n"
MF "a[92:95],v[16:19],v[84:87],a[220:223]\n"
"v_exp_f32 v115,v115\n"
MF "a[96:99],v[16:19],v[88:91],a[220:223]\n"
"v_add_f32 v57,v57,v112\n"
"v_add_f32 v57,v57,v113\n"
MF "a[116:119],v[20:23],v[88:91],a[220:223]\n"
"s_waitcnt lgkmcnt(0)\n"
TR "v[16:17],v2 offset:2048\n"
TR "v[18:19],v2 offset:8192\n"
MF "a[136:139],v[24:27],v[88:91],a[220:223]\n"
"s_nop 0\n"
TR "v[20:21],v3 offset:2048\n"
TR "v[22:23],v3 offset:8192\n"
MF "a[156:159],v[28:31],v[88:91],a[220:223]\n"
CV "v94,v112,v113\n"
"v_add_f32 v57,v57,v114\n"
MF "a[152:155],v[28:31],v[84:87],a[220:223]\n"
"v_add_f32 v57,v57,v115\n"
CV "v95,v114,v115\n"
MF "a[132:135],v[24:27],v[84:87],a[220:223]\n"
"v_exp_f32 v116,v116\n"
MF "a[128:131],v[24:27],v[80:83],a[220:223]\n"
"v_exp_f32 v117,v117\n"
MF "a[148:151],v[28:31],v[80:83],a[220:223]\n"
"v_exp_f32 v118,v118\n"
MF "a[144:147],v[28:31],v[76:79],a[220:223]\n"
"v_exp_f32 v119,v119\n"
MF "a[124:127],v[24:27],v[76:79],a[220:223]\n"
"v_add_f32 v58,v58,v116\n"
"v_add_f32 v58,v58,v117\n"
MF "a[120:123],v[24:27],v[72:75],a[220:223]\n"
CV "v98,v116,v117\n"
"v_add_f32 v58,v58,v118\n"
MF "a[140:143],v[28:31],v[72:75],a[220:223]\n"
"s_waitcnt lgkmcnt(0)\n"
TR "v[24:25],v4 offset:2048\n"
TR "v[26:27],v4 offset:8192\n"
MF "a[160:163],v[16:19],v[72:75],a[220:223]\n"
"s_nop 0\n"
TR "v[28:29],v5 offset:2048\n"
TR "v[30:31],v5 offset:8192\n"
MF "a[180:183],v[20:23],v[72:75],a[220:223]\n"
"v_add_f32 v58,v58,v119\n"
CV "v99,v118,v119\n"
MF "a[184:187],v[20:23],v[76:79],a[220:223]\n"
"v_exp_f32 v120,v120\n"
MF "a[164:167],v[16:19],v[76:79],a[220:223]\n"
"v_exp_f32 v121,v121\n"
MF "a[168:171],v[16:19],v[80:83],a[220:223]\n"
"v_exp_f32 v122,v122\n"
MF "a[188:191],v[20:23],v[80:83],a[220:223]\n"
"v_exp_f32 v123,v123\n"
MF "a[192:195],v[20:23],v[84:87],a[220:223]\n"
"v_add_f32 v59,v59,v120\n"
"v_add_f32 v59,v59,v121\n"
MF "a[172:175],v[16:19],v[84:87],a[220:223]\n"
CV "v102,v120,v121\n"
"v_add_f32 v59,v59,v122\n"
MF "a[176:179],v[16:19],v[88:91],a[220:223]\n"
"s_waitcnt vmcnt(6) lgkmcnt(0)\n"
"s_barrier\n"
MF "a[196:199],v[20:23],v[88:91],a[220:223]\n"
"s_add_u32 s73,s73,12288\n"
"ds_read_b128 v[16:19],v0\n"
"ds_read_b128 v[20:23],v1\n"
MF "a[216:219],v[24:27],v[88:91],a[220:223]\n"
"s_cmp_eq_u32 s73,s19\n"
"s_cselect_b32 s73,s18,s73\n"
"s_add_u32 m0,s65,49152\n"
MF "a[236:239],v[28:31],v[88:91],a[220:223]\n"
LD "v8,s[36:39],s73 offen nt lds\n"
MF "a[232:235],v[28:31],v[84:87],a[220:223]\n"
"s_add_u32 m0,s65,50048\n"
"v_add_f32 v59,v59,v123\n"
CV "v103,v122,v123\n"
MF "a[212:215],v[24:27],v[84:87],a[220:223]\n"
LD "v8,s[36:39],s73 offen offset:128 nt lds\n"
MF "a[208:211],v[24:27],v[80:83],a[220:223]\n"
"s_add_u32 m0,s65,50944\n"
MF "a[228:231],v[28:31],v[80:83],a[220:223]\n"
LD "v8,s[36:39],s73 offen offset:256 nt lds\n"
MF "a[224:227],v[28:31],v[76:79],a[220:223]\n"
MF "a[204:207],v[24:27],v[76:79],a[220:223]\n"
MF "a[200:203],v[24:27],v[72:75],a[220:223]\n"
MF "a[220:223],v[28:31],v[72:75],a[220:223]\n"
"s_waitcnt lgkmcnt(0)\n"
".Laloopx:\n"
MF "v[72:75],v[16:19],v[136:139],v[32:35]\n"
"s_nop 0\n"
"ds_read_b128 v[24:27],v0 offset:1024\n"
"ds_read_b128 v[28:31],v1 offset:1024\n"
MF "v[72:75],v[20:23],v[156:159],v[72:75]\n"
MF "v[76:79],v[20:23],v[160:163],v[36:39]\n"
MF "v[76:79],v[16:19],v[140:143],v[76:79]\n"
MF "v[80:83],v[16:19],v[144:147],v[40:43]\n"
MF "v[80:83],v[20:23],v[164:167],v[80:83]\n"
MF "v[84:87],v[20:23],v[168:171],v[44:47]\n"
MF "v[84:87],v[16:19],v[148:151],v[84:87]\n"
"s_add_u32 m0,s65,49152\n"
MF "v[88:91],v[16:19],v[152:155],v[48:51]\n"
LD "v8,s[36:39],s73 offen offset:3072 nt lds\n"
MF "v[88:91],v[20:23],v[172:175],v[88:91]\n"
"s_waitcnt lgkmcnt(0)\n"
"ds_read_b128 v[16:19],v0 offset:2048\n"
"ds_read_b128 v[20:23],v1 offset:2048\n"
MF "v[88:91],v[24:27],v[192:195],v[88:91]\n"
"v_exp_f32 v124,v124\n"
MF "v[88:91],v[28:31],v[212:215],v[88:91]\n"
"s_add_u32 m0,s65,50048\n"
"v_exp_f32 v125,v125\n"
MF "v[84:87],v[28:31],v[208:211],v[84:87]\n"
LD "v8,s[36:39],s73 offen offset:3200 nt lds\n"
MF "v[84:87],v[24:27],v[188:191],v[84:87]\n"
"v_exp_f32 v126,v126\n"
MF "v[80:83],v[24:27],v[184:187],v[80:83]\n"
"v_exp_f32 v127,v127\n"
MF "v[80:83],v[28:31],v[204:207],v[80:83]\n"
"s_add_u32 m0,s65,50944\n"
"v_add_f32 v60,v60,v124\n"
"v_add_f32 v60,v60,v125\n"
MF "v[76:79],v[28:31],v[200:203],v[76:79]\n"
LD "v8,s[36:39],s73 offen offset:3328 nt lds\n"
MF "v[76:79],v[24:27],v[180:183],v[76:79]\n"
CV "v106,v124,v125\n"
"v_add_f32 v60,v60,v126\n"
MF "v[72:75],v[24:27],v[176:179],v[72:75]\n"
"v_add_f32 v60,v60,v127\n"
CV "v107,v126,v127\n"
MF "v[72:75],v[28:31],v[196:199],v[72:75]\n"
"s_waitcnt lgkmcnt(0)\n"
"ds_read_b128 v[24:27],v0 offset:6144\n"
"ds_read_b128 v[28:31],v1 offset:6144\n"
MF "v[72:75],v[16:19],v[216:219],v[72:75]\n"
"v_exp_f32 v128,v128\n"
MF "v[72:75],v[20:23],v[236:239],v[72:75]\n"
"v_exp_f32 v129,v129\n"
MF "v[76:79],v[20:23],v[240:243],v[76:79]\n"
"v_exp_f32 v130,v130\n"
MF "v[76:79],v[16:19],v[220:223],v[76:79]\n"
"v_exp_f32 v131,v131\n"
MF "v[80:83],v[16:19],v[224:227],v[80:83]\n"
"v_add_f32 v61,v61,v128\n"
"v_add_f32 v61,v61,v129\n"
MF "v[80:83],v[20:23],v[244:247],v[80:83]\n"
CV "v110,v128,v129\n"
"v_add_f32 v61,v61,v130\n"
MF "v[84:87],v[20:23],v[248:251],v[84:87]\n"
"v_add_f32 v61,v61,v131\n"
CV "v111,v130,v131\n"
MF "v[84:87],v[16:19],v[228:231],v[84:87]\n"
"v_max3_f32 v10,v57,v58,v59\n"
"v_max3_f32 v10,v10,v60,v61\n"
MF "v[88:91],v[16:19],v[232:235],v[88:91]\n"
"v_cmp_ngt_f32 vcc,s95,v10\n"
MF "v[88:91],v[20:23],v[252:255],v[88:91]\n"
"s_waitcnt lgkmcnt(0)\n"
"s_cbranch_vccnz .Lstub_6\n"
".Lback_6:\n"
MF "v[112:115],v[24:27],v[136:139],v[32:35]\n"
"s_add_u32 s74,s74,12288\n"
"ds_read_b128 v[16:19],v0 offset:7168\n"
"ds_read_b128 v[20:23],v1 offset:7168\n"
MF "v[112:115],v[28:31],v[156:159],v[112:115]\n"
"s_cmp_eq_u32 s74,s20\n"
"s_cselect_b32 s74,s70,s74\n"
MF "v[116:119],v[28:31],v[160:163],v[36:39]\n"
"v_exp_f32 v72,v72\n"
MF "v[116:119],v[24:27],v[140:143],v[116:119]\n"
"v_exp_f32 v73,v73\n"
MF "v[120:123],v[24:27],v[144:147],v[40:43]\n"
"v_exp_f32 v74,v74\n"
MF "v[120:123],v[28:31],v[164:167],v[120:123]\n"
"v_exp_f32 v75,v75\n"
MF "v[124:127],v[28:31],v[168:171],v[44:47]\n"
"v_add_f32 v52,v57,v72\n"
"v_add_f32 v52,v52,v73\n"
MF "v[124:127],v[24:27],v[148:151],v[124:127]\n"
CV "v72,v72,v73\n"
"v_add_f32 v52,v52,v74\n"
MF "v[128:131],v[24:27],v[152:155],v[48:51]\n"
"v_add_f32 v52,v52,v75\n"
CV "v73,v74,v75\n"
MF "v[128:131],v[28:31],v[172:175],v[128:131]\n"
"s_waitcnt lgkmcnt(0)\n"
"ds_read_b128 v[24:27],v0 offset:8192\n"
"ds_read_b128 v[28:31],v1 offset:8192\n"
MF "v[128:131],v[16:19],v[192:195],v[128:131]\n"
"v_exp_f32 v76,v76\n"
MF "v[128:131],v[20:23],v[212:215],v[128:131]\n"
"v_exp_f32 v77,v77\n"
MF "v[124:127],v[20:23],v[208:211],v[124:127]\n"
"v_exp_f32 v78,v78\n"
MF "v[124:127],v[16:19],v[188:191],v[124:127]\n"
"v_exp_f32 v79,v79\n"
MF "v[120:123],v[16:19],v[184:187],v[120:123]\n"
"v_add_f32 v53,v58,v76\n"
"v_add_f32 v53,v53,v77\n"
MF "v[120:123],v[20:23],v[204:207],v[120:123]\n"
CV "v76,v76,v77\n"
"v_add_f32 v53,v53,v78\n"
MF "v[116:119],v[20:23],v[200:203],v[116:119]\n"
"v_add_f32 v53,v53,v79\n"
CV "v77,v78,v79\n"
MF "v[116:119],v[16:19],v[180:183],v[116:119]\n"
"s_waitcnt vmcnt(12) lgkmcnt(0)\n"
"s_barrier\n"
MF "v[112:115],v[16:19],v[176:179],v[112:115]\n"
"v_exp_f32 v80,v80\n"
MF "v[112:115],v[20:23],v[196:199],v[112:115]\n"
"s_nop 0\n"
TR "v[16:17],v2 offset:24576\n"
TR "v[18:19],v2 offset:30720\n"
MF "v[112:115],v[24:27],v[216:219],v[112:115]\n"
"s_add_u32 s72,s72,6144\n"
TR "v[20:21],v3 offset:24576\n"
TR "v[22:23],v3 offset:30720\n"
MF "v[112:115],v[28:31],v[236:239],v[112:115]\n"
"s_cmp_eq_u32 s72,s17\n"
"s_cselect_b32 s72,s16,s72\n"
"s_add_u32 m0,s64,0\n"
MF "v[116:119],v[28:31],v[240:243],v[116:119]\n"
LD "v7,s[32:35],s72 offen nt lds\n"
MF "v[116:119],v[24:27],v[220:223],v[116:119]\n"
"s_add_u32 m0,s64,896\n"
"v_exp_f32 v81,v81\n"
MF "v[120:123],v[24:27],v[224:227],v[120:123]\n"
LD "v7,s[32:35],s72 offen offset:128 nt lds\n"
MF "v[120:123],v[28:31],v[244:247],v[120:123]\n"
"v_exp_f32 v82,v82\n"
MF "v[124:127],v[28:31],v[248:251],v[124:127]\n"
"s_add_u32 m0,s64,1792\n"
"v_exp_f32 v83,v83\n"
MF "v[124:127],v[24:27],v[228:231],v[124:127]\n"
LD "v7,s[32:35],s72 offen offset:256 nt lds\n"
MF "v[128:131],v[24:27],v[232:235],v[128:131]\n"
"s_add_u32 s72,s72,6144\n"
"v_add_f32 v54,v59,v80\n"
"v_add_f32 v54,v54,v81\n"
MF "v[128:131],v[28:31],v[252:255],v[128:131]\n"
"s_waitcnt lgkmcnt(0)\n"
TR "v[24:25],v4 offset:24576\n"
TR "v[26:27],v4 offset:30720\n"
MF "a[0:3],v[16:19],v[92:95],a[0:3]\n"
"s_add_u32 m0,s64,6144\n"
TR "v[28:29],v5 offset:24576\n"
TR "v[30:31],v5 offset:30720\n"
MF "a[20:23],v[20:23],v[92:95],a[20:23]\n"
LD "v7,s[32:35],s72 offen nt lds\n"
MF "a[24:27],v[20:23],v[96:99],a[24:27]\n"
CV "v80,v80,v81\n"
"v_add_f32 v54,v54,v82\n"
MF "a[4:7],v[16:19],v[96:99],a[4:7]\n"
"s_add_u32 m0,s64,7040\n"
"v_add_f32 v54,v54,v83\n"
CV "v81,v82,v83\n"
MF "a[8:11],v[16:19],v[100:103],a[8:11]\n"
LD "v7,s[32:35],s72 offen offset:128 nt lds\n"
MF "a[28:31],v[20:23],v[100:103],a[28:31]\n"
"v_exp_f32 v84,v84\n"
MF "a[32:35],v[20:23],v[104:107],a[32:35]\n"
"s_add_u32 m0,s64,7936\n"
"v_exp_f32 v85,v85\n"
MF "a[12:15],v[16:19],v[104:107],a[12:15]\n"
LD "v7,s[32:35],s72 offen offset:256 nt lds\n"
MF "a[16:19],v[16:19],v[108:111],a[16:19]\n"
"v_exp_f32 v86,v86\n"
MF "a[36:39],v[20:23],v[108:111],a[36:39]\n"
"s_waitcnt lgkmcnt(0)\n"
TR "v[16:17],v2 offset:25600\n"
TR "v[18:19],v2 offset:31744\n"
MF "a[56:59],v[24:27],v[108:111],a[56:59]\n"
"s_nop 0\n"
TR "v[20:21],v3 offset:25600\n"
TR "v[22:23],v3 offset:31744\n"
MF "a[76:79],v[28:31],v[108:111],a[76:79]\n"
"v_exp_f32 v87,v87\n"
MF "a[72:75],v[28:31],v[104:107],a[72:75]\n"
"v_add_f32 v55,v60,v84\n"
"v_add_f32 v55,v55,v85\n"
MF "a[52:55],v[24:27],v[104:107],a[52:55]\n"
CV "v84,v84,v85\n"
"v_add_f32 v55,v55,v86\n"
MF "a[48:51],v[24:27],v[100:103],a[48:51]\n"
"v_add_f32 v55,v55,v87\n"
CV "v85,v86,v87\n"
MF "a[68:71],v[28:31],v[100:103],a[68:71]\n"
"v_exp_f32 v88,v88\n"
MF "a[64:67],v[28:31],v[96:99],a[64:67]\n"
"v_exp_f32 v89,v89\n"
MF "a[44:47],v[24:27],v[96:99],a[44:47]\n"
"v_exp_f32 v90,v90\n"
MF "a[40:43],v[24:27],v[92:95],a[40:43]\n"
"v_exp_f32 v91,v91\n"
MF "a[60:63],v[28:31],v[92:95],a[60:63]\n"
"s_waitcnt lgkmcnt(0)\n"
TR "v[24:25],v4 offset:25600\n"
TR "v[26:27],v4 offset:31744\n"
MF "a[80:83],v[16:19],v[92:95],a[80:83]\n"
"s_nop 0\n"
TR "v[28:29],v5 offset:25600\n"
TR "v[30:31],v5 offset:31744\n"
MF "a[100:103],v[20:23],v[92:95],a[100:103]\n"
"v_add_f32 v56,v61,v88\n"
"v_add_f32 v56,v56,v89\n"
MF "a[104:107],v[20:23],v[96:99],a[104:107]\n"
CV "v88,v88,v89\n"
"v_add_f32 v56,v56,v90\n"
MF "a[84:87],v[16:19],v[96:99],a[84:87]\n"
"v_add_f32 v56,v56,v91\n"
CV "v89,v90,v91\n"
MF "a[88:91],v[16:19],v[100:103],a[88:91]\n"
"v_exp_f32 v112,v112\n"
MF "a[108:111],v[20:23],v[100:103],a[108:111]\n"
"v_exp_f32 v113,v113\n"
MF "a[112:115],v[20:23],v[104:107],a[112:115]\n"
"v_exp_f32 v114,v114\n"
MF "a[92:95],v[16:19],v[104:107],a[92:95]\n"
"v_exp_f32 v115,v115\n"
MF "a[96:99],v[16:19],v[108:111],a[96:99]\n"
"v_add_f32 v52,v52,v112\n"
"v_add_f32 v52,v52,v113\n"
MF "a[116:119],v[20:23],v[108:111],a[116:119]\n"
"s_waitcnt lgkmcnt(0)\n"
TR "v[16:17],v2 offset:26624\n"
TR "v[18:19],v2 offset:32768\n"
MF "a[136:139],v[24:27],v[108:111],a[136:139]\n"
"s_nop 0\n"
TR "v[20:21],v3 offset:26624\n"
TR "v[22:23],v3 offset:32768\n"
MF "a[156:159],v[28:31],v[108:111],a[156:159]\n"
CV "v74,v112,v113\n"
"v_add_f32 v52,v52,v114\n"
MF "a[152:155],v[28:31],v[104:107],a[152:155]\n"
"v_add_f32 v52,v52,v115\n"
CV "v75,v114,v115\n"
MF "a[132:135],v[24:27],v[104:107],a[132:135]\n"
"v_exp_f32 v116,v116\n"
MF "a[128:131],v[24:27],v[100:103],a[128:131]\n"
"v_exp_f32 v117,v117\n"
MF "a[148:151],v[28:31],v[100:103],a[148:151]\n"
"v_exp_f32 v118,v118\n"
MF "a[144:147],v[28:31],v[96:99],a[144:147]\n"
"v_exp_f32 v119,v119\n"
MF "a[124:127],v[24:27],v[96:99],a[124:127]\n"
"v_add_f32 v53,v53,v116\n"
"v_add_f32 v53,v53,v117\n"
MF "a[120:123],v[24:27],v[92:95],a[120:123]\n"
CV "v78,v116,v117\n"
"v_add_f32 v53,v53,v118\n"
MF "a[140:143],v[28:31],v[92:95],a[140:143]\n"
"s_waitcnt lgkmcnt(0)\n"
TR "v[24:25],v4 offset:26624\n"
TR "v[26:27],v4 offset:32768\n"
MF "a[160:163],v[16:19],v[92:95],a[160:163]\n"
"s_nop 0\n"
TR "v[28:29],v5 offset:26624\n"
TR "v[30:31],v5 offset:32768\n"
MF "a[180:183],v[20:23],v[92:95],a[180:183]\n"
"v_add_f32 v53,v53,v119\n"
CV "v79,v118,v119\n"
MF "a[184:187],v[20:23],v[96:99],a[184:187]\n"
"v_exp_f32 v120,v120\n"
MF "a[164:167],v[16:19],v[96:99],a[164:167]\n"
"v_exp_f32 v121,v121\n"
MF "a[168:171],v[16:19],v[100:103],a[168:171]\n"
"v_exp_f32 v122,v122\n"
MF "a[188:191],v[20:23],v[100:103],a[188:191]\n"
"v_exp_f32 v123,v123\n"
MF "a[192:195],v[20:23],v[104:107],a[192:195]\n"
"v_add_f32 v54,v54,v120\n"
"v_add_f32 v54,v54,v121\n"
MF "a[172:175],v[16:19],v[104:107],a[172:175]\n"
CV "v82,v120,v121\n"
"v_add_f32 v54,v54,v122\n"
MF "a[176:179],v[16:19],v[108:111],a[176:179]\n"
"s_waitcnt vmcnt(12) lgkmcnt(0)\n"
"s_barrier\n"
MF "a[196:199],v[20:23],v[108:111],a[196:199]\n"
"s_add_u32 s73,s73,12288\n"
"ds_read_b128 v[16:19],v0 offset:24576\n"
"ds_read_b128 v[20:23],v1 offset:24576\n"
MF "a[216:219],v[24:27],v[108:111],a[216:219]\n"
"s_cmp_eq_u32 s73,s19\n"
"s_cselect_b32 s73,s18,s73\n"
"s_add_u32 m0,s65,73728\n"
MF "a[236:239],v[28:31],v[108:111],a[236:239]\n"
LD "v8,s[36:39],s73 offen nt lds\n"
MF "a[232:235],v[28:31],v[104:107],a[232:235]\n"
"s_add_u32 m0,s65,74624\n"
"v_add_f32 v54,v54,v123\n"
CV "v83,v122,v123\n"
MF "a[212:215],v[24:27],v[104:107],a[212:215]\n"
LD "v8,s[36:39],s73 offen offset:128 nt lds\n"
MF "a[208:211],v[24:27],v[100:103],a[208:211]\n"
"s_add_u32 m0,s65,75520\n"
MF "a[228:231],v[28:31],v[100:103],a[228:231]\n"
LD "v8,s[36:39],s73 offen offset:256 nt lds\n"
MF "a[224:227],v[28:31],v[96:99],a[224:227]\n"
MF "a[204:207],v[24:27],v[96:99],a[204:207]\n"
MF "a[200:203],v[24:27],v[92:95],a[200:203]\n"
MF "a[220:223],v[28:31],v[92:95],a[220:223]\n"
"s_waitcnt lgkmcnt(0)\n"
MF "v[92:95],v[16:19],v[136:139],v[32:35]\n"
"s_add_u32 m0,s65,73728\n"
"ds_read_b128 v[24:27],v0 offset:25600\n"
"ds_read_b128 v[28:31],v1 offset:25600\n"
MF "v[92:95],v[20:23],v[156:159],v[92:95]\n"
LD "v8,s[36:39],s73 offen offset:3072 nt lds\n"
MF "v[96:99],v[20:23],v[160:163],v[36:39]\n"
"v_exp_f32 v124,v124\n"
MF "v[96:99],v[16:19],v[140:143],v[96:99]\n"
"v_exp_f32 v125,v125\n"
MF "v[100:103],v[16:19],v[144:147],v[40:43]\n"
"s_add_u32 m0,s65,74624\n"
"v_exp_f32 v126,v126\n"
MF "v[100:103],v[20:23],v[164:167],v[100:103]\n"
LD "v8,s[36:39],s73 offen offset:3200 nt lds\n"
MF "v[104:107],v[20:23],v[168:171],v[44:47]\n"
"v_exp_f32 v127,v127\n"
MF "v[104:107],v[16:19],v[148:151],v[104:107]\n"
"s_add_u32 m0,s65,75520\n"
"v_add_f32 v55,v55,v124\n"
"v_add_f32 v55,v55,v125\n"
MF "v[108:111],v[16:19],v[152:155],v[48:51]\n"
LD "v8,s[36:39],s73 offen offset:3328 nt lds\n"
MF "v[108:111],v[20:23],v[172:175],v[108:111]\n"
"s_waitcnt lgkmcnt(0)\n"
"ds_read_b128 v[16:19],v0 offset:26624\n"
"ds_read_b128 v[20:23],v1 offset:26624\n"
MF "v[108:111],v[24:27],v[192:195],v[108:111]\n"
CV "v86,v124,v125\n"
"v_add_f32 v55,v55,v126\n"
MF "v[108:111],v[28:31],v[212:215],v[108:111]\n"
"v_add_f32 v55,v55,v127\n"
CV "v87,v126,v127\n"
MF "v[104:107],v[28:31],v[208:211],v[104:107]\n"
"v_exp_f32 v128,v128\n"
MF "v[104:107],v[24:27],v[188:191],v[104:107]\n"
"v_exp_f32 v129,v129\n"
MF "v[100:103],v[24:27],v[184:187],v[100:103]\n"
"v_exp_f32 v130,v130\n"
MF "v[100:103],v[28:31],v[204:207],v[100:103]\n"
"v_exp_f32 v131,v131\n"
MF "v[96:99],v[28:31],v[200:203],v[96:99]\n"
"v_add_f32 v56,v56,v128\n"
"v_add_f32 v56,v56,v129\n"
MF "v[96:99],v[24:27],v[180:183],v[96:99]\n"
CV "v90,v128,v129\n"
"v_add_f32 v56,v56,v130\n"
MF "v[92:95],v[24:27],v[176:179],v[92:95]\n"
"v_add_f32 v56,v56,v131\n"
CV "v91,v130,v131\n"
MF "v[92:95],v[28:31],v[196:199],v[92:95]\n"
"s_waitcnt lgkmcnt(0)\n"
"ds_read_b128 v[24:27],v0 offset:30720\n"
"ds_read_b128 v[28:31],v1 offset:30720\n"
MF "v[92:95],v[16:19],v[216:219],v[92:95]\n"
MF "v[92:95],v[20:23],v[236:239],v[92:95]\n"
MF "v[96:99],v[20:23],v[240:243],v[96:99]\n"
MF "v[96:99],v[16:19],v[220:223],v[96:99]\n"
MF "v[100:103],v[16:19],v[224:227],v[100:103]\n"
MF "v[100:103],v[20:23],v[244:247],v[100:103]\n"
MF "v[104:107],v[20:23],v[248:251],v[104:107]\n"
MF "v[104:107],v[16:19],v[228:231],v[104:107]\n"
"v_max3_f32 v10,v52,v53,v54\n"
"v_max3_f32 v10,v10,v55,v56\n"
MF "v[108:111],v[16:19],v[232:235],v[108:111]\n"
"v_cmp_ngt_f32 vcc,s95,v10\n"
MF "v[108:111],v[20:23],v[252:255],v[108:111]\n"
"s_waitcnt lgkmcnt(0)\n"
"s_cbranch_vccnz .Lstub_7\n"
".Lback_7:\n"
MF "v[112:115],v[24:27],v[136:139],v[32:35]\n"
"s_add_u32 s74,s74,12288\n"
"ds_read_b128 v[16:19],v0 offset:31744\n"
"ds_read_b128 v[20:23],v1 offset:31744\n"
MF "v[112:115],v[28:31],v[156:159],v[112:115]\n"
"s_cmp_eq_u32 s74,s20\n"
"s_cselect_b32 s74,s70,s74\n"
MF "v[116:119],v[28:31],v[160:163],v[36:39]\n"
"v_exp_f32 v92,v92\n"
MF "v[116:119],v[24:27],v[140:143],v[116:119]\n"
"v_exp_f32 v93,v93\n"
MF "v[120:123],v[24:27],v[144:147],v[40:43]\n"
"v_exp_f32 v94,v94\n"
MF "v[120:123],v[28:31],v[164:167],v[120:123]\n"
"v_exp_f32 v95,v95\n"
MF "v[124:127],v[28:31],v[168:171],v[44:47]\n"
"v_add_f32 v57,v52,v92\n"
"v_add_f32 v57,v57,v93\n"
MF "v[124:127],v[24:27],v[148:151],v[124:127]\n"
CV "v92,v92,v93\n"
"v_add_f32 v57,v57,v94\n"
MF "v[128:131],v[24:27],v[152:155],v[48:51]\n"
"v_add_f32 v57,v57,v95\n"
CV "v93,v94,v95\n"
MF "v[128:131],v[28:31],v[172:175],v[128:131]\n"
"s_waitcnt lgkmcnt(0)\n"
"ds_read_b128 v[24:27],v0 offset:32768\n"
"ds_read_b128 v[28:31],v1 offset:32768\n"
MF "v[128:131],v[16:19],v[192:195],v[128:131]\n"
"s_sub_u32 s61,s61,1\n"
"v_exp_f32 v96,v96\n"
MF "v[128:131],v[20:23],v[212:215],v[128:131]\n"
"s_cmp_eq_u32 s61,1\n"
"s_cselect_b32 s95,0,s95\n"
"v_add_f32 v58,v53,v96\n"
MF "v[124:127],v[20:23],v[208:211],v[124:127]\n"
"v_exp_f32 v97,v97\n"
MF "v[124:127],v[16:19],v[188:191],v[124:127]\n"
"v_exp_f32 v98,v98\n"
MF "v[120:123],v[16:19],v[184:187],v[120:123]\n"
"v_exp_f32 v99,v99\n"
MF "v[120:123],v[20:23],v[204:207],v[120:123]\n"
"v_add_f32 v58,v58,v97\n"
CV "v96,v96,v97\n"
MF "v[116:119],v[20:23],v[200:203],v[116:119]\n"
"v_add_f32 v58,v58,v98\n"
"v_add_f32 v58,v58,v99\n"
MF "v[116:119],v[16:19],v[180:183],v[116:119]\n"
"s_waitcnt vmcnt(12) lgkmcnt(0)\n"
"s_barrier\n"
MF "v[112:115],v[16:19],v[176:179],v[112:115]\n"
CV "v97,v98,v99\n"
MF "v[112:115],v[20:23],v[196:199],v[112:115]\n"
"s_nop 0\n"
TR "v[16:17],v2\n"
TR "v[18:19],v2 offset:6144\n"
MF "v[112:115],v[24:27],v[216:219],v[112:115]\n"
"s_add_u32 s72,s72,6144\n"
TR "v[20:21],v3\n"
TR "v[22:23],v3 offset:6144\n"
MF "v[112:115],v[28:31],v[236:239],v[112:115]\n"
"s_cmp_eq_u32 s72,s17\n"
"s_cselect_b32 s72,s16,s72\n"
"s_add_u32 m0,s64,24576\n"
MF "v[116:119],v[28:31],v[240:243],v[116:119]\n"
LD "v7,s[32:35],s72 offen nt lds\n"
MF "v[116:119],v[24:27],v[220:223],v[116:119]\n"
"s_add_u32 m0,s64,25472\n"
"v_exp_f32 v100,v100\n"
MF "v[120:123],v[24:27],v[224:227],v[120:123]\n"
LD "v7,s[32:35],s72 offen offset:128 nt lds\n"
MF "v[120:123],v[28:31],v[244:247],v[120:123]\n"
"v_exp_f32 v101,v101\n"
MF "v[124:127],v[28:31],v[248:251],v[124:127]\n"
"s_add_u32 m0,s64,26368\n"
"v_exp_f32 v102,v102\n"
MF "v[124:127],v[24:27],v[228:231],v[124:127]\n"
LD "v7,s[32:35],s72 offen offset:256 nt lds\n"
MF "v[128:131],v[24:27],v[232:235],v[128:131]\n"
"s_add_u32 s72,s72,6144\n"
"v_exp_f32 v103,v103\n"
MF "v[128:131],v[28:31],v[252:255],v[128:131]\n"
"s_waitcnt lgkmcnt(0)\n"
TR "v[24:25],v4\n"
TR "v[26:27],v4 offset:6144\n"
MF "a[0:3],v[16:19],v[72:75],a[0:3]\n"
"s_add_u32 m0,s64,30720\n"
TR "v[28:29],v5\n"
TR "v[30:31],v5 offset:6144\n"
MF "a[20:23],v[20:23],v[72:75],a[20:23]\n"
LD "v7,s[32:35],s72 offen nt lds\n"
MF "a[24:27],v[20:23],v[76:79],a[24:27]\n"
"v_add_f32 v59,v54,v100\n"
"v_add_f32 v59,v59,v101\n"
MF "a[4:7],v[16:19],v[76:79],a[4:7]\n"
"s_add_u32 m0,s64,31616\n"
CV "v100,v100,v101\n"
"v_add_f32 v59,v59,v102\n"
MF "a[8:11],v[16:19],v[80:83],a[8:11]\n"
LD "v7,s[32:35],s72 offen offset:128 nt lds\n"
MF "a[28:31],v[20:23],v[80:83],a[28:31]\n"
"v_add_f32 v59,v59,v103\n"
CV "v101,v102,v103\n"
MF "a[32:35],v[20:23],v[84:87],a[32:35]\n"
"s_add_u32 m0,s64,32512\n"
"v_exp_f32 v104,v104\n"
MF "a[12:15],v[16:19],v[84:87],a[12:15]\n"
LD "v7,s[32:35],s72 offen offset:256 nt lds\n"
MF "a[16:19],v[16:19],v[88:91],a[16:19]\n"
"v_exp_f32 v105,v105\n"
MF "a[36:39],v[20:23],v[88:91],a[36:39]\n"
"s_waitcnt lgkmcnt(0)\n"
TR "v[16:17],v2 offset:1024\n"
TR "v[18:19],v2 offset:7168\n"
MF "a[56:59],v[24:27],v[88:91],a[56:59]\n"
"s_nop 0\n"
TR "v[20:21],v3 offset:1024\n"
TR "v[22:23],v3 offset:7168\n"
MF "a[76:79],v[28:31],v[88:91],a[76:79]\n"
"v_exp_f32 v106,v106\n"
MF "a[72:75],v[28:31],v[84:87],a[72:75]\n"
"v_exp_f32 v107,v107\n"
MF "a[52:55],v[24:27],v[84:87],a[52:55]\n"
"v_add_f32 v60,v55,v104\n"
"v_add_f32 v60,v60,v105\n"
MF "a[48:51],v[24:27],v[80:83],a[48:51]\n"
CV "v104,v104,v105\n"
"v_add_f32 v60,v60,v106\n"
MF "a[68:71],v[28:31],v[80:83],a[68:71]\n"
".Lpg11b:\n"
"s_cbranch_execz .Lpgqj_a\n"
".Lpg11d:\n"
"s_cbranch_execz .Lisr2\n"
"v_add_f32 v60,v60,v107\n"
CV "v105,v106,v107\n"
MF "a[64:67],v[28:31],v[76:79],a[64:67]\n"
"v_exp_f32 v108,v108\n"
MF "a[44:47],v[24:27],v[76:79],a[44:47]\n"
"v_exp_f32 v109,v109\n"
MF "a[40:43],v[24:27],v[72:75],a[40:43]\n"
"v_exp_f32 v110,v110\n"
MF "a[60:63],v[28:31],v[72:75],a[60:63]\n"
"s_waitcnt lgkmcnt(0)\n"
TR "v[24:25],v4 offset:1024\n"
TR "v[26:27],v4 offset:7168\n"
MF "a[80:83],v[16:19],v[72:75],a[80:83]\n"
"s_nop 0\n"
TR "v[28:29],v5 offset:1024\n"
TR "v[30:31],v5 offset:7168\n"
MF "a[100:103],v[20:23],v[72:75],a[100:103]\n"
"v_exp_f32 v111,v111\n"
MF "a[104:107],v[20:23],v[76:79],a[104:107]\n"
"v_add_f32 v61,v56,v108\n"
"v_add_f32 v61,v61,v109\n"
MF "a[84:87],v[16:19],v[76:79],a[84:87]\n"
CV "v108,v108,v109\n"
"v_add_f32 v61,v61,v110\n"
MF "a[88:91],v[16:19],v[80:83],a[88:91]\n"
"v_add_f32 v61,v61,v111\n"
CV "v109,v110,v111\n"
MF "a[108:111],v[20:23],v[80:83],a[108:111]\n"
"v_exp_f32 v112,v112\n"
MF "a[112:115],v[20:23],v[84:87],a[112:115]\n"
"v_exp_f32 v113,v113\n"
MF "a[92:95],v[16:19],v[84:87],a[92:95]\n"
"v_exp_f32 v114,v114\n"
MF "a[96:99],v[16:19],v[88:91],a[96:99]\n"
"v_exp_f32 v115,v115\n"
MF "a[116:119],v[20:23],v[88:91],a[116:119]\n"
"s_waitcnt lgkmcnt(0)\n"
TR "v[16:17],v2 offset:2048\n"
TR "v[18:19],v2 offset:8192\n"
MF "a[136:139],v[24:27],v[88:91],a[136:139]\n"
"s_nop 0\n"
TR "v[20:21],v3 offset:2048\n"
TR "v[22:23],v3 offset:8192\n"
MF "a[156:159],v[28:31],v[88:91],a[156:159]\n"
"v_add_f32 v57,v57,v112\n"
"v_add_f32 v57,v57,v113\n"
MF "a[152:155],v[28:31],v[84:87],a[152:155]\n"
CV "v94,v112,v113\n"
"v_add_f32 v57,v57,v114\n"
MF "a[132:135],v[24:27],v[84:87],a[132:135]\n"
"v_add_f32 v57,v57,v115\n"
CV "v95,v114,v115\n"
MF "a[128:131],v[24:27],v[80:83],a[128:131]\n"
"v_exp_f32 v116,v116\n"
MF "a[148:151],v[28:31],v[80:83],a[148:151]\n"
"v_exp_f32 v117,v117\n"
MF "a[144:147],v[28:31],v[76:79],a[144:147]\n"
"v_exp_f32 v118,v118\n"
MF "a[124:127],v[24:27],v[76:79],a[124:127]\n"
"v_exp_f32 v119,v119\n"
MF "a[120:123],v[24:27],v[72:75],a[120:123]\n"
"v_add_f32 v58,v58,v116\n"
"v_add_f32 v58,v58,v117\n"
MF "a[140:143],v[28:31],v[72:75],a[140:143]\n"
"s_waitcnt lgkmcnt(0)\n"
TR "v[24:25],v4 offset:2048\n"
TR "v[26:27],v4 offset:8192\n"
MF "a[160:163],v[16:19],v[72:75],a[160:163]\n"
"s_nop 0\n"
TR "v[28:29],v5 offset:2048\n"
TR "v[30:31],v5 offset:8192\n"
MF "a[180:183],v[20:23],v[72:75],a[180:183]\n"
CV "v98,v116,v117\n"
"v_add_f32 v58,v58,v118\n"
MF "a[184:187],v[20:23],v[76:79],a[184:187]\n"
"v_add_f32 v58,v58,v119\n"
CV "v99,v118,v119\n"
MF "a[164:167],v[16:19],v[76:79],a[164:167]\n"
"v_exp_f32 v120,v120\n"
MF "a[168:171],v[16:19],v[80:83],a[168:171]\n"
"v_exp_f32 v121,v121\n"
MF "a[188:191],v[20:23],v[80:83],a[188:191]\n"
"v_exp_f32 v122,v122\n"
MF "a[192:195],v[20:23],v[84:87],a[192:195]\n"
"v_exp_f32 v123,v123\n"
MF "a[172:175],v[16:19],v[84:87],a[172:175]\n"
"v_add_f32 v59,v59,v120\n"
"v_add_f32 v59,v59,v121\n"
MF "a[176:179],v[16:19],v[88:91],a[176:179]\n"
"s_waitcnt vmcnt(12) lgkmcnt(0)\n"
"s_barrier\n"
MF "a[196:199],v[20:23],v[88:91],a[196:199]\n"
"s_add_u32 s73,s73,12288\n"
"ds_read_b128 v[16:19],v0\n"
"ds_read_b128 v[20:23],v1\n"
MF "a[216:219],v[24:27],v[88:91],a[216:219]\n"
"s_cmp_eq_u32 s73,s19\n"
"s_cselect_b32 s73,s18,s73\n"
"s_add_u32 m0,s65,49152\n"
MF "a[236:239],v[28:31],v[88:91],a[236:239]\n"
LD "v8,s[36:39],s73 offen nt lds\n"
MF "a[232:235],v[28:31],v[84:87],a[232:235]\n"
"s_add_u32 m0,s65,50048\n"
CV "v102,v120,v121\n"
"v_add_f32 v59,v59,v122\n"
MF "a[212:215],v[24:27],v[84:87],a[212:215]\n"
LD "v8,s[36:39],s73 offen offset:128 nt lds\n"
MF "a[208:211],v[24:27],v[80:83],a[208:211]\n"
"s_add_u32 m0,s65,50944\n"
"v_add_f32 v59,v59,v123\n"
CV "v103,v122,v123\n"
MF "a[228:231],v[28:31],v[80:83],a[228:231]\n"
LD "v8,s[36:39],s73 offen offset:256 nt lds\n"
MF "a[224:227],v[28:31],v[76:79],a[224:227]\n"
MF "a[204:207],v[24:27],v[76:79],a[204:207]\n"
"s_waitcnt lgkmcnt(0)\n"
MF "a[200:203],v[24:27],v[72:75],a[200:203]\n"
"s_cmp_lg_u32 s61,0\n"
MF "a[220:223],v[28:31],v[72:75],a[220:223]\n"
"s_cbranch_scc1 .Laloopx\n"
".Lepi1:\n"
"v_exp_f32 v124,v124\n"
"v_exp_f32 v125,v125\n"
"v_exp_f32 v126,v126\n"
"v_exp_f32 v127,v127\n"
"v_exp_f32 v128,v128\n"
"v_exp_f32 v129,v129\n"
"v_exp_f32 v130,v130\n"
"v_exp_f32 v131,v131\n"
"v_add_f32 v60,v60,v124\n"
"v_add_f32 v61,v61,v128\n"
"v_add_f32 v60,v60,v125\n"
"v_add_f32 v61,v61,v129\n"
"v_add_f32 v60,v60,v126\n"
"v_add_f32 v61,v61,v130\n"
"v_add_f32 v60,v60,v127\n"
"v_add_f32_e64 v61,v61,v131\n"
CV "v106,v124,v125\n"
CV "v107,v126,v127\n"
CV "v110,v128,v129\n"
CV "v111,v130,v131\n"
"v_max3_f32 v10,v57,v58,v59\n"
"v_max3_f32 v10,v10,v60,v61\n"
"v_cmp_le_f32 vcc,0x47800000,v10\n"
"s_cbranch_vccnz .Lstub_8\n"
".Lback_8:\n"
"s_waitcnt vmcnt(9) lgkmcnt(0)\n"
"s_barrier\n"
"s_cmp_eq_u32 s57,0\n"
"s_cbranch_scc0 .Lep\n"
".Lepx:\n"
"s_nop 0\n"
"s_add_u32 s66,s67,4718592\n"
"s_sub_u32 s81,s81,384\n"
"s_mov_b32 s83,7077888\n"
AR "v122,a243\n"
"v_add_u32 v123,97280,v122\n"
"v_add_u32 v124,194560,v122\n"
"v_add_u32 v125,291840,v122\n"
TR "v[136:137],v2 offset:24576\n"
TR "v[138:139],v2 offset:30720\n"
TR "v[140:141],v3 offset:24576\n"
TR "v[142:143],v3 offset:30720\n"
TR "v[144:145],v4 offset:24576\n"
TR "v[146:147],v4 offset:30720\n"
TR "v[148:149],v5 offset:24576\n"
TR "v[150:151],v5 offset:30720\n"
TR "v[152:153],v2 offset:25600\n"
TR "v[154:155],v2 offset:31744\n"
TR "v[156:157],v3 offset:25600\n"
TR "v[158:159],v3 offset:31744\n"
TR "v[160:161],v4 offset:25600\n"
TR "v[162:163],v4 offset:31744\n"
TR "v[164:165],v5 offset:25600\n"
TR "v[166:167],v5 offset:31744\n"
TR "v[168:169],v2 offset:26624\n"
TR "v[170:171],v2 offset:32768\n"
TR "v[172:173],v3 offset:26624\n"
TR "v[174:175],v3 offset:32768\n"
TR "v[176:177],v4 offset:26624\n"
TR "v[178:179],v4 offset:32768\n"
TR "v[180:181],v5 offset:26624\n"
TR "v[182:183],v5 offset:32768\n"
"v_mov_b32 v52,v57\n"
"v_mov_b32 v53,v58\n"
"v_mov_b32 v54,v59\n"
"v_mov_b32 v55,v60\n"
"v_mov_b32 v56,v61\n"
"s_add_u32 m0,s62,0\n"
"s_add_u32 s82,s66,2359296\n"
"s_sub_u32 s15,s82,7864320\n"
"s_min_u32 s82,s82,s15\n"
LD "v122,s[28:31],s82 offen lds\n"
LD "v123,s[28:31],s82 offen offset:1024 lds\n"
"v_permlane16_swap_b32 v57,v52\n"
"v_permlane16_swap_b32 v58,v53\n"
"v_permlane16_swap_b32 v59,v54\n"
"v_permlane16_swap_b32 v60,v55\n"
"v_permlane16_swap_b32 v61,v56\n"
"v_add_f32 v57,v57,v52\n"
"v_add_f32 v58,v58,v53\n"
"v_add_f32 v59,v59,v54\n"
"v_add_f32 v60,v60,v55\n"
"v_add_f32 v61,v61,v56\n"
"v_mov_b32 v52,v57\n"
"v_mov_b32 v53,v58\n"
"v_mov_b32 v54,v59\n"
"v_mov_b32 v55,v60\n"
"v_mov_b32 v56,v61\n"
LD "v124,s[28:31],s82 offen offset:2048 lds\n"
LD "v125,s[28:31],s82 offen offset:3072 lds\n"
"v_permlane32_swap_b32 v57,v52\n"
"v_permlane32_swap_b32 v58,v53\n"
"v_permlane32_swap_b32 v59,v54\n"
"v_permlane32_swap_b32 v60,v55\n"
"v_permlane32_swap_b32 v61,v56\n"
"v_add_f32 v57,v57,v52\n"
"v_add_f32 v58,v58,v53\n"
"v_add_f32 v59,v59,v54\n"
"v_add_f32 v60,v60,v55\n"
"v_add_f32 v61,v61,v56\n"
"s_waitcnt lgkmcnt(15)\n"
MF "a[0:3],v[136:139],v[92:95],a[0:3]\n"
MF "a[20:23],v[140:143],v[92:95],a[20:23]\n"
"v_rcp_f32 v57,v57\n"
"v_rcp_f32 v58,v58\n"
"v_rcp_f32 v59,v59\n"
"v_rcp_f32 v60,v60\n"
"v_rcp_f32 v61,v61\n"
MF "a[4:7],v[136:139],v[96:99],a[4:7]\n"
"v_mul_f32_e64 v52,-s13,v57\n"
"v_mul_f32_e64 v53,-s13,v58\n"
"v_mul_f32_e64 v54,-s13,v59\n"
"v_mul_f32_e64 v55,-s13,v60\n"
"v_mul_f32_e64 v56,-s13,v61\n"
AR "v72,a0\n"
AR "v73,a1\n"
AR "v74,a2\n"
MF "a[24:27],v[140:143],v[96:99],a[24:27]\n"
AR "v75,a3\n"
AR "v76,a20\n"
AR "v77,a21\n"
AR "v78,a22\n"
AR "v79,a23\n"
"v_pk_fma_f32 v[72:73],v[72:73],v[56:57],v[52:53] op_sel:[0,1,0] op_sel_hi:[1,1,0]\n"
"v_pk_fma_f32 v[74:75],v[74:75],v[56:57],v[52:53] op_sel:[0,1,0] op_sel_hi:[1,1,0]\n"
"v_pk_fma_f32 v[76:77],v[76:77],v[56:57],v[52:53] op_sel:[0,1,0] op_sel_hi:[1,1,0]\n"
"v_pk_fma_f32 v[78:79],v[78:79],v[56:57],v[52:53] op_sel:[0,1,0] op_sel_hi:[1,1,0]\n"
CV "v112,v72,v73\n"
CV "v113,v74,v75\n"
CV "v114,v76,v77\n"
CV "v115,v78,v79\n"
MF "a[8:11],v[136:139],v[100:103],a[8:11]\n"
AW "a0,v112\n"
AW "a1,v113\n"
AW "a2,v114\n"
AW "a3,v115\n"
"s_add_u32 m0,s62,16384\n"
"s_add_u32 s82,s66,2359424\n"
"s_sub_u32 s15,s82,7864320\n"
"s_min_u32 s82,s82,s15\n"
"s_nop 0\n"
LD "v122,s[28:31],s82 offen lds\n"
AR "v80,a4\n"
AR "v81,a5\n"
AR "v82,a6\n"
MF "a[28:31],v[140:143],v[100:103],a[28:31]\n"
AR "v83,a7\n"
AR "v84,a24\n"
AR "v85,a25\n"
AR "v86,a26\n"
AR "v87,a27\n"
"v_pk_fma_f32 v[80:81],v[80:81],v[58:59],v[52:53] op_sel:[0,0,1] op_sel_hi:[1,0,1]\n"
"v_pk_fma_f32 v[82:83],v[82:83],v[58:59],v[52:53] op_sel:[0,0,1] op_sel_hi:[1,0,1]\n"
"v_pk_fma_f32 v[84:85],v[84:85],v[58:59],v[52:53] op_sel:[0,0,1] op_sel_hi:[1,0,1]\n"
"v_pk_fma_f32 v[86:87],v[86:87],v[58:59],v[52:53] op_sel:[0,0,1] op_sel_hi:[1,0,1]\n"
CV "v116,v80,v81\n"
CV "v117,v82,v83\n"
CV "v118,v84,v85\n"
CV "v119,v86,v87\n"
MF "a[12:15],v[136:139],v[104:107],a[12:15]\n"
AW "a4,v116\n"
AW "a5,v117\n"
AW "a6,v118\n"
AW "a7,v119\n"
LD "v123,s[28:31],s82 offen offset:1024 lds\n"
AR "v72,a8\n"
AR "v73,a9\n"
AR "v74,a10\n"
MF "a[32:35],v[140:143],v[104:107],a[32:35]\n"
AR "v75,a11\n"
AR "v76,a28\n"
AR "v77,a29\n"
AR "v78,a30\n"
AR "v79,a31\n"
"v_pk_fma_f32 v[72:73],v[72:73],v[58:59],v[54:55] op_sel:[0,1,0] op_sel_hi:[1,1,0]\n"
"v_pk_fma_f32 v[74:75],v[74:75],v[58:59],v[54:55] op_sel:[0,1,0] op_sel_hi:[1,1,0]\n"
"v_pk_fma_f32 v[76:77],v[76:77],v[58:59],v[54:55] op_sel:[0,1,0] op_sel_hi:[1,1,0]\n"
"v_pk_fma_f32 v[78:79],v[78:79],v[58:59],v[54:55] op_sel:[0,1,0] op_sel_hi:[1,1,0]\n"
CV "v112,v72,v73\n"
CV "v113,v74,v75\n"
CV "v114,v76,v77\n"
CV "v115,v78,v79\n"
MF "a[16:19],v[136:139],v[108:111],a[16:19]\n"
AW "a8,v112\n"
AW "a9,v113\n"
AW "a10,v114\n"
AW "a11,v115\n"
"s_waitcnt lgkmcnt(0)\n"
"s_barrier\n"
AR "v80,a12\n"
AR "v81,a13\n"
AR "v82,a14\n"
MF "a[36:39],v[140:143],v[108:111],a[36:39]\n"
AR "v83,a15\n"
AR "v84,a32\n"
AR "v85,a33\n"
AR "v86,a34\n"
AR "v87,a35\n"
"v_pk_fma_f32 v[80:81],v[80:81],v[60:61],v[54:55] op_sel:[0,0,1] op_sel_hi:[1,0,1]\n"
"v_pk_fma_f32 v[82:83],v[82:83],v[60:61],v[54:55] op_sel:[0,0,1] op_sel_hi:[1,0,1]\n"
"v_pk_fma_f32 v[84:85],v[84:85],v[60:61],v[54:55] op_sel:[0,0,1] op_sel_hi:[1,0,1]\n"
"v_pk_fma_f32 v[86:87],v[86:87],v[60:61],v[54:55] op_sel:[0,0,1] op_sel_hi:[1,0,1]\n"
CV "v116,v80,v81\n"
CV "v117,v82,v83\n"
CV "v118,v84,v85\n"
CV "v119,v86,v87\n"
MF "a[40:43],v[144:147],v[92:95],a[40:43]\n"
AW "a12,v116\n"
AW "a13,v117\n"
AW "a14,v118\n"
AW "a15,v119\n"
LD "v124,s[28:31],s82 offen offset:2048 lds\n"
AR "v72,a16\n"
AR "v73,a17\n"
AR "v74,a18\n"
MF "a[60:63],v[148:151],v[92:95],a[60:63]\n"
AR "v75,a19\n"
AR "v76,a36\n"
AR "v77,a37\n"
AR "v78,a38\n"
AR "v79,a39\n"
"v_pk_fma_f32 v[72:73],v[72:73],v[60:61],v[56:57] op_sel:[0,1,0] op_sel_hi:[1,1,0]\n"
"v_pk_fma_f32 v[74:75],v[74:75],v[60:61],v[56:57] op_sel:[0,1,0] op_sel_hi:[1,1,0]\n"
"v_pk_fma_f32 v[76:77],v[76:77],v[60:61],v[56:57] op_sel:[0,1,0] op_sel_hi:[1,1,0]\n"
"v_pk_fma_f32 v[78:79],v[78:79],v[60:61],v[56:57] op_sel:[0,1,0] op_sel_hi:[1,1,0]\n"
CV "v112,v72,v73\n"
CV "v113,v74,v75\n"
CV "v114,v76,v77\n"
CV "v115,v78,v79\n"
MF "a[44:47],v[144:147],v[96:99],a[44:47]\n"
AW "a16,v112\n"
AW "a17,v113\n"
AW "a18,v114\n"
AW "a19,v115\n"
LD "v125,s[28:31],s82 offen offset:3072 lds\n"
AR "v80,a40\n"
AR "v81,a41\n"
AR "v82,a42\n"
MF "a[64:67],v[148:151],v[96:99],a[64:67]\n"
AR "v83,a43\n"
AR "v84,a60\n"
AR "v85,a61\n"
AR "v86,a62\n"
AR "v87,a63\n"
"v_pk_fma_f32 v[80:81],v[80:81],v[56:57],v[52:53] op_sel:[0,1,0] op_sel_hi:[1,1,0]\n"
"v_pk_fma_f32 v[82:83],v[82:83],v[56:57],v[52:53] op_sel:[0,1,0] op_sel_hi:[1,1,0]\n"
"v_pk_fma_f32 v[84:85],v[84:85],v[56:57],v[52:53] op_sel:[0,1,0] op_sel_hi:[1,1,0]\n"
"v_pk_fma_f32 v[86:87],v[86:87],v[56:57],v[52:53] op_sel:[0,1,0] op_sel_hi:[1,1,0]\n"
CV "v116,v80,v81\n"
CV "v117,v82,v83\n"
CV "v118,v84,v85\n"
CV "v119,v86,v87\n"
MF "a[48:51],v[144:147],v[100:103],a[48:51]\n"
AW "a40,v116\n"
AW "a41,v117\n"
AW "a42,v118\n"
AW "a43,v119\n"
AR "v72,a44\n"
AR "v73,a45\n"
AR "v74,a46\n"
MF "a[68:71],v[148:151],v[100:103],a[68:71]\n"
AR "v75,a47\n"
AR "v76,a64\n"
AR "v77,a65\n"
AR "v78,a66\n"
AR "v79,a67\n"
"v_pk_fma_f32 v[72:73],v[72:73],v[58:59],v[52:53] op_sel:[0,0,1] op_sel_hi:[1,0,1]\n"
"v_pk_fma_f32 v[74:75],v[74:75],v[58:59],v[52:53] op_sel:[0,0,1] op_sel_hi:[1,0,1]\n"
"v_pk_fma_f32 v[76:77],v[76:77],v[58:59],v[52:53] op_sel:[0,0,1] op_sel_hi:[1,0,1]\n"
"v_pk_fma_f32 v[78:79],v[78:79],v[58:59],v[52:53] op_sel:[0,0,1] op_sel_hi:[1,0,1]\n"
CV "v112,v72,v73\n"
CV "v113,v74,v75\n"
CV "v114,v76,v77\n"
CV "v115,v78,v79\n"
MF "a[52:55],v[144:147],v[104:107],a[52:55]\n"
AW "a44,v112\n"
AW "a45,v113\n"
AW "a46,v114\n"
AW "a47,v115\n"
"s_add_u32 m0,s62,32768\n"
"s_add_u32 s82,s66,2359552\n"
"s_sub_u32 s15,s82,7864320\n"
"s_min_u32 s82,s82,s15\n"
"s_nop 0\n"
LD "v122,s[28:31],s82 offen lds\n"
AR "v80,a48\n"
AR "v81,a49\n"
AR "v82,a50\n"
MF "a[72:75],v[148:151],v[104:107],a[72:75]\n"
AR "v83,a51\n"
AR "v84,a68\n"
AR "v85,a69\n"
AR "v86,a70\n"
AR "v87,a71\n"
"v_pk_fma_f32 v[80:81],v[80:81],v[58:59],v[54:55] op_sel:[0,1,0] op_sel_hi:[1,1,0]\n"
"v_pk_fma_f32 v[82:83],v[82:83],v[58:59],v[54:55] op_sel:[0,1,0] op_sel_hi:[1,1,0]\n"
"v_pk_fma_f32 v[84:85],v[84:85],v[58:59],v[54:55] op_sel:[0,1,0] op_sel_hi:[1,1,0]\n"
"v_pk_fma_f32 v[86:87],v[86:87],v[58:59],v[54:55] op_sel:[0,1,0] op_sel_hi:[1,1,0]\n"
CV "v116,v80,v81\n"
CV "v117,v82,v83\n"
CV "v118,v84,v85\n"
CV "v119,v86,v87\n"
MF "a[56:59],v[144:147],v[108:111],a[56:59]\n"
AW "a48,v116\n"
AW "a49,v117\n"
AW "a50,v118\n"
AW "a51,v119\n"
LD "v123,s[28:31],s82 offen offset:1024 lds\n"
AR "v72,a52\n"
AR "v73,a53\n"
AR "v74,a54\n"
MF "a[76:79],v[148:151],v[108:111],a[76:79]\n"
AR "v75,a55\n"
AR "v76,a72\n"
AR "v77,a73\n"
AR "v78,a74\n"
AR "v79,a75\n"
"v_pk_fma_f32 v[72:73],v[72:73],v[60:61],v[54:55] op_sel:[0,0,1] op_sel_hi:[1,0,1]\n"
"v_pk_fma_f32 v[74:75],v[74:75],v[60:61],v[54:55] op_sel:[0,0,1] op_sel_hi:[1,0,1]\n"
"v_pk_fma_f32 v[76:77],v[76:77],v[60:61],v[54:55] op_sel:[0,0,1] op_sel_hi:[1,0,1]\n"
"v_pk_fma_f32 v[78:79],v[78:79],v[60:61],v[54:55] op_sel:[0,0,1] op_sel_hi:[1,0,1]\n"
CV "v112,v72,v73\n"
CV "v113,v74,v75\n"
CV "v114,v76,v77\n"
CV "v115,v78,v79\n"
MF "a[80:83],v[152:155],v[92:95],a[80:83]\n"
AW "a52,v112\n"
AW "a53,v113\n"
AW "a54,v114\n"
AW "a55,v115\n"
AR "v80,a56\n"
AR "v81,a57\n"
AR "v82,a58\n"
MF "a[100:103],v[156:159],v[92:95],a[100:103]\n"
AR "v83,a59\n"
AR "v84,a76\n"
AR "v85,a77\n"
AR "v86,a78\n"
AR "v87,a79\n"
"v_pk_fma_f32 v[80:81],v[80:81],v[60:61],v[56:57] op_sel:[0,1,0] op_sel_hi:[1,1,0]\n"
"v_pk_fma_f32 v[82:83],v[82:83],v[60:61],v[56:57] op_sel:[0,1,0] op_sel_hi:[1,1,0]\n"
"v_pk_fma_f32 v[84:85],v[84:85],v[60:61],v[56:57] op_sel:[0,1,0] op_sel_hi:[1,1,0]\n"
"v_pk_fma_f32 v[86:87],v[86:87],v[60:61],v[56:57] op_sel:[0,1,0] op_sel_hi:[1,1,0]\n"
CV "v116,v80,v81\n"
CV "v117,v82,v83\n"
CV "v118,v84,v85\n"
CV "v119,v86,v87\n"
MF "a[84:87],v[152:155],v[96:99],a[84:87]\n"
AW "a56,v116\n"
AW "a57,v117\n"
AW "a58,v118\n"
AW "a59,v119\n"
LD "v124,s[28:31],s82 offen offset:2048 lds\n"
AR "v72,a80\n"
AR "v73,a81\n"
AR "v74,a82\n"
MF "a[104:107],v[156:159],v[96:99],a[104:107]\n"
AR "v75,a83\n"
AR "v76,a100\n"
AR "v77,a101\n"
AR "v78,a102\n"
AR "v79,a103\n"
"v_pk_fma_f32 v[72:73],v[72:73],v[56:57],v[52:53] op_sel:[0,1,0] op_sel_hi:[1,1,0]\n"
"v_pk_fma_f32 v[74:75],v[74:75],v[56:57],v[52:53] op_sel:[0,1,0] op_sel_hi:[1,1,0]\n"
"v_pk_fma_f32 v[76:77],v[76:77],v[56:57],v[52:53] op_sel:[0,1,0] op_sel_hi:[1,1,0]\n"
"v_pk_fma_f32 v[78:79],v[78:79],v[56:57],v[52:53] op_sel:[0,1,0] op_sel_hi:[1,1,0]\n"
CV "v112,v72,v73\n"
CV "v113,v74,v75\n"
CV "v114,v76,v77\n"
CV "v115,v78,v79\n"
MF "a[88:91],v[152:155],v[100:103],a[88:91]\n"
AW "a80,v112\n"
AW "a81,v113\n"
AW "a82,v114\n"
AW "a83,v115\n"
LD "v125,s[28:31],s82 offen offset:3072 lds\n"
AR "v80,a84\n"
AR "v81,a85\n"
AR "v82,a86\n"
MF "a[108:111],v[156:159],v[100:103],a[108:111]\n"
AR "v83,a87\n"
AR "v84,a104\n"
AR "v85,a105\n"
AR "v86,a106\n"
AR "v87,a107\n"
"v_pk_fma_f32 v[80:81],v[80:81],v[58:59],v[52:53] op_sel:[0,0,1] op_sel_hi:[1,0,1]\n"
"v_pk_fma_f32 v[82:83],v[82:83],v[58:59],v[52:53] op_sel:[0,0,1] op_sel_hi:[1,0,1]\n"
"v_pk_fma_f32 v[84:85],v[84:85],v[58:59],v[52:53] op_sel:[0,0,1] op_sel_hi:[1,0,1]\n"
"v_pk_fma_f32 v[86:87],v[86:87],v[58:59],v[52:53] op_sel:[0,0,1] op_sel_hi:[1,0,1]\n"
CV "v116,v80,v81\n"
CV "v117,v82,v83\n"
CV "v118,v84,v85\n"
CV "v119,v86,v87\n"
MF "a[92:95],v[152:155],v[104:107],a[92:95]\n"
AW "a84,v116\n"
AW "a85,v117\n"
AW "a86,v118\n"
AW "a87,v119\n"
AR "v72,a88\n"
AR "v73,a89\n"
AR "v74,a90\n"
MF "a[112:115],v[156:159],v[104:107],a[112:115]\n"
AR "v75,a91\n"
AR "v76,a108\n"
AR "v77,a109\n"
AR "v78,a110\n"
AR "v79,a111\n"
"v_pk_fma_f32 v[72:73],v[72:73],v[58:59],v[54:55] op_sel:[0,1,0] op_sel_hi:[1,1,0]\n"
"v_pk_fma_f32 v[74:75],v[74:75],v[58:59],v[54:55] op_sel:[0,1,0] op_sel_hi:[1,1,0]\n"
"v_pk_fma_f32 v[76:77],v[76:77],v[58:59],v[54:55] op_sel:[0,1,0] op_sel_hi:[1,1,0]\n"
"v_pk_fma_f32 v[78:79],v[78:79],v[58:59],v[54:55] op_sel:[0,1,0] op_sel_hi:[1,1,0]\n"
CV "v112,v72,v73\n"
CV "v113,v74,v75\n"
CV "v114,v76,v77\n"
CV "v115,v78,v79\n"
MF "a[96:99],v[152:155],v[108:111],a[96:99]\n"
AW "a88,v112\n"
AW "a89,v113\n"
AW "a90,v114\n"
AW "a91,v115\n"
AR "v80,a92\n"
AR "v81,a93\n"
AR "v82,a94\n"
MF "a[116:119],v[156:159],v[108:111],a[116:119]\n"
AR "v83,a95\n"
AR "v84,a112\n"
AR "v85,a113\n"
AR "v86,a114\n"
AR "v87,a115\n"
"v_pk_fma_f32 v[80:81],v[80:81],v[60:61],v[54:55] op_sel:[0,0,1] op_sel_hi:[1,0,1]\n"
"v_pk_fma_f32 v[82:83],v[82:83],v[60:61],v[54:55] op_sel:[0,0,1] op_sel_hi:[1,0,1]\n"
"v_pk_fma_f32 v[84:85],v[84:85],v[60:61],v[54:55] op_sel:[0,0,1] op_sel_hi:[1,0,1]\n"
"v_pk_fma_f32 v[86:87],v[86:87],v[60:61],v[54:55] op_sel:[0,0,1] op_sel_hi:[1,0,1]\n"
CV "v116,v80,v81\n"
CV "v117,v82,v83\n"
CV "v118,v84,v85\n"
CV "v119,v86,v87\n"
MF "a[120:123],v[160:163],v[92:95],a[120:123]\n"
AW "a92,v116\n"
AW "a93,v117\n"
AW "a94,v118\n"
AW "a95,v119\n"
AR "v72,a96\n"
AR "v73,a97\n"
AR "v74,a98\n"
MF "a[140:143],v[164:167],v[92:95],a[140:143]\n"
AR "v75,a99\n"
AR "v76,a116\n"
AR "v77,a117\n"
".Lpg12f:\n"
"s_cbranch_execz .Lisr3\n"
"s_nop 0\n"
AR "v78,a118\n"
AR "v79,a119\n"
"v_pk_fma_f32 v[72:73],v[72:73],v[60:61],v[56:57] op_sel:[0,1,0] op_sel_hi:[1,1,0]\n"
"v_pk_fma_f32 v[74:75],v[74:75],v[60:61],v[56:57] op_sel:[0,1,0] op_sel_hi:[1,1,0]\n"
"v_pk_fma_f32 v[76:77],v[76:77],v[60:61],v[56:57] op_sel:[0,1,0] op_sel_hi:[1,1,0]\n"
"v_pk_fma_f32 v[78:79],v[78:79],v[60:61],v[56:57] op_sel:[0,1,0] op_sel_hi:[1,1,0]\n"
CV "v112,v72,v73\n"
CV "v113,v74,v75\n"
CV "v114,v76,v77\n"
CV "v115,v78,v79\n"
MF "a[124:127],v[160:163],v[96:99],a[124:127]\n"
AW "a96,v112\n"
AW "a97,v113\n"
AW "a98,v114\n"
AW "a99,v115\n"
AR "v80,a120\n"
AR "v81,a121\n"
AR "v82,a122\n"
MF "a[144:147],v[164:167],v[96:99],a[144:147]\n"
AR "v83,a123\n"
AR "v84,a140\n"
AR "v85,a141\n"
AR "v86,a142\n"
AR "v87,a143\n"
"v_pk_fma_f32 v[80:81],v[80:81],v[56:57],v[52:53] op_sel:[0,1,0] op_sel_hi:[1,1,0]\n"
"v_pk_fma_f32 v[82:83],v[82:83],v[56:57],v[52:53] op_sel:[0,1,0] op_sel_hi:[1,1,0]\n"
"v_pk_fma_f32 v[84:85],v[84:85],v[56:57],v[52:53] op_sel:[0,1,0] op_sel_hi:[1,1,0]\n"
"v_pk_fma_f32 v[86:87],v[86:87],v[56:57],v[52:53] op_sel:[0,1,0] op_sel_hi:[1,1,0]\n"
CV "v116,v80,v81\n"
CV "v117,v82,v83\n"
CV "v118,v84,v85\n"
CV "v119,v86,v87\n"
MF "a[128:131],v[160:163],v[100:103],a[128:131]\n"
AW "a120,v116\n"
AW "a121,v117\n"
AW "a122,v118\n"
AW "a123,v119\n"
AR "v72,a124\n"
AR "v73,a125\n"
AR "v74,a126\n"
MF "a[148:151],v[164:167],v[100:103],a[148:151]\n"
AR "v75,a127\n"
AR "v76,a144\n"
AR "v77,a145\n"
AR "v78,a146\n"
AR "v79,a147\n"
"v_pk_fma_f32 v[72:73],v[72:73],v[58:59],v[52:53] op_sel:[0,0,1] op_sel_hi:[1,0,1]\n"
"v_pk_fma_f32 v[74:75],v[74:75],v[58:59],v[52:53] op_sel:[0,0,1] op_sel_hi:[1,0,1]\n"
"v_pk_fma_f32 v[76:77],v[76:77],v[58:59],v[52:53] op_sel:[0,0,1] op_sel_hi:[1,0,1]\n"
"v_pk_fma_f32 v[78:79],v[78:79],v[58:59],v[52:53] op_sel:[0,0,1] op_sel_hi:[1,0,1]\n"
CV "v112,v72,v73\n"
CV "v113,v74,v75\n"
CV "v114,v76,v77\n"
CV "v115,v78,v79\n"
MF "a[132:135],v[160:163],v[104:107],a[132:135]\n"
AW "a124,v112\n"
AW "a125,v113\n"
AW "a126,v114\n"
AW "a127,v115\n"
AR "v80,a128\n"
AR "v81,a129\n"
AR "v82,a130\n"
MF "a[152:155],v[164:167],v[104:107],a[152:155]\n"
AR "v83,a131\n"
AR "v84,a148\n"
AR "v85,a149\n"
AR "v86,a150\n"
AR "v87,a151\n"
"v_pk_fma_f32 v[80:81],v[80:81],v[58:59],v[54:55] op_sel:[0,1,0] op_sel_hi:[1,1,0]\n"
"v_pk_fma_f32 v[82:83],v[82:83],v[58:59],v[54:55] op_sel:[0,1,0] op_sel_hi:[1,1,0]\n"
"v_pk_fma_f32 v[84:85],v[84:85],v[58:59],v[54:55] op_sel:[0,1,0] op_sel_hi:[1,1,0]\n"
"v_pk_fma_f32 v[86:87],v[86:87],v[58:59],v[54:55] op_sel:[0,1,0] op_sel_hi:[1,1,0]\n"
CV "v116,v80,v81\n"
CV "v117,v82,v83\n"
CV "v118,v84,v85\n"
CV "v119,v86,v87\n"
MF "a[136:139],v[160:163],v[108:111],a[136:139]\n"
AW "a128,v116\n"
AW "a129,v117\n"
AW "a130,v118\n"
AW "a131,v119\n"
AR "v72,a132\n"
AR "v73,a133\n"
AR "v74,a134\n"
MF "a[156:159],v[164:167],v[108:111],a[156:159]\n"
AR "v75,a135\n"
AR "v76,a152\n"
AR "v77,a153\n"
AR "v78,a154\n"
AR "v79,a155\n"
"v_pk_fma_f32 v[72:73],v[72:73],v[60:61],v[54:55] op_sel:[0,0,1] op_sel_hi:[1,0,1]\n"
"v_pk_fma_f32 v[74:75],v[74:75],v[60:61],v[54:55] op_sel:[0,0,1] op_sel_hi:[1,0,1]\n"
"v_pk_fma_f32 v[76:77],v[76:77],v[60:61],v[54:55] op_sel:[0,0,1] op_sel_hi:[1,0,1]\n"
"v_pk_fma_f32 v[78:79],v[78:79],v[60:61],v[54:55] op_sel:[0,0,1] op_sel_hi:[1,0,1]\n"
CV "v112,v72,v73\n"
CV "v113,v74,v75\n"
CV "v114,v76,v77\n"
CV "v115,v78,v79\n"
MF "a[160:163],v[168:171],v[92:95],a[160:163]\n"
AW "a132,v112\n"
AW "a133,v113\n"
AW "a134,v114\n"
AW "a135,v115\n"
AR "v80,a136\n"
AR "v81,a137\n"
AR "v82,a138\n"
MF "a[180:183],v[172:175],v[92:95],a[180:183]\n"
AR "v83,a139\n"
AR "v84,a156\n"
AR "v85,a157\n"
AR "v86,a158\n"
AR "v87,a159\n"
"v_pk_fma_f32 v[80:81],v[80:81],v[60:61],v[56:57] op_sel:[0,1,0] op_sel_hi:[1,1,0]\n"
"v_pk_fma_f32 v[82:83],v[82:83],v[60:61],v[56:57] op_sel:[0,1,0] op_sel_hi:[1,1,0]\n"
"v_pk_fma_f32 v[84:85],v[84:85],v[60:61],v[56:57] op_sel:[0,1,0] op_sel_hi:[1,1,0]\n"
"v_pk_fma_f32 v[86:87],v[86:87],v[60:61],v[56:57] op_sel:[0,1,0] op_sel_hi:[1,1,0]\n"
CV "v116,v80,v81\n"
CV "v117,v82,v83\n"
CV "v118,v84,v85\n"
CV "v119,v86,v87\n"
MF "a[164:167],v[168:171],v[96:99],a[164:167]\n"
AW "a136,v116\n"
AW "a137,v117\n"
AW "a138,v118\n"
AW "a139,v119\n"
AR "v72,a160\n"
AR "v73,a161\n"
AR "v74,a162\n"
MF "a[184:187],v[172:175],v[96:99],a[184:187]\n"
AR "v75,a163\n"
AR "v76,a180\n"
AR "v77,a181\n"
AR "v78,a182\n"
AR "v79,a183\n"
"v_pk_fma_f32 v[72:73],v[72:73],v[56:57],v[52:53] op_sel:[0,1,0] op_sel_hi:[1,1,0]\n"
"v_pk_fma_f32 v[74:75],v[74:75],v[56:57],v[52:53] op_sel:[0,1,0] op_sel_hi:[1,1,0]\n"
"v_pk_fma_f32 v[76:77],v[76:77],v[56:57],v[52:53] op_sel:[0,1,0] op_sel_hi:[1,1,0]\n"
"v_pk_fma_f32 v[78:79],v[78:79],v[56:57],v[52:53] op_sel:[0,1,0] op_sel_hi:[1,1,0]\n"
CV "v112,v72,v73\n"
CV "v113,v74,v75\n"
CV "v114,v76,v77\n"
CV "v115,v78,v79\n"
MF "a[168:171],v[168:171],v[100:103],a[168:171]\n"
AW "a160,v112\n"
AW "a161,v113\n"
AW "a162,v114\n"
AW "a163,v115\n"
AR "v80,a164\n"
AR "v81,a165\n"
AR "v82,a166\n"
MF "a[188:191],v[172:175],v[100:103],a[188:191]\n"
AR "v83,a167\n"
AR "v84,a184\n"
AR "v85,a185\n"
AR "v86,a186\n"
AR "v87,a187\n"
"v_pk_fma_f32 v[80:81],v[80:81],v[58:59],v[52:53] op_sel:[0,0,1] op_sel_hi:[1,0,1]\n"
"v_pk_fma_f32 v[82:83],v[82:83],v[58:59],v[52:53] op_sel:[0,0,1] op_sel_hi:[1,0,1]\n"
"v_pk_fma_f32 v[84:85],v[84:85],v[58:59],v[52:53] op_sel:[0,0,1] op_sel_hi:[1,0,1]\n"
"v_pk_fma_f32 v[86:87],v[86:87],v[58:59],v[52:53] op_sel:[0,0,1] op_sel_hi:[1,0,1]\n"
CV "v116,v80,v81\n"
CV "v117,v82,v83\n"
CV "v118,v84,v85\n"
CV "v119,v86,v87\n"
MF "a[172:175],v[168:171],v[104:107],a[172:175]\n"
AW "a164,v116\n"
AW "a165,v117\n"
AW "a166,v118\n"
AW "a167,v119\n"
AR "v72,a168\n"
AR "v73,a169\n"
AR "v74,a170\n"
MF "a[192:195],v[172:175],v[104:107],a[192:195]\n"
AR "v75,a171\n"
AR "v76,a188\n"
AR "v77,a189\n"
AR "v78,a190\n"
AR "v79,a191\n"
"v_pk_fma_f32 v[72:73],v[72:73],v[58:59],v[54:55] op_sel:[0,1,0] op_sel_hi:[1,1,0]\n"
"v_pk_fma_f32 v[74:75],v[74:75],v[58:59],v[54:55] op_sel:[0,1,0] op_sel_hi:[1,1,0]\n"
"v_pk_fma_f32 v[76:77],v[76:77],v[58:59],v[54:55] op_sel:[0,1,0] op_sel_hi:[1,1,0]\n"
"v_pk_fma_f32 v[78:79],v[78:79],v[58:59],v[54:55] op_sel:[0,1,0] op_sel_hi:[1,1,0]\n"
CV "v112,v72,v73\n"
CV "v113,v74,v75\n"
CV "v114,v76,v77\n"
CV "v115,v78,v79\n"
MF "a[176:179],v[168:171],v[108:111],a[176:179]\n"
AW "a168,v112\n"
AW "a169,v113\n"
AW "a170,v114\n"
AW "a171,v115\n"
AR "v80,a172\n"
AR "v81,a173\n"
AR "v82,a174\n"
MF "a[196:199],v[172:175],v[108:111],a[196:199]\n"
AR "v83,a175\n"
AR "v84,a192\n"
AR "v85,a193\n"
AR "v86,a194\n"
AR "v87,a195\n"
"v_pk_fma_f32 v[80:81],v[80:81],v[60:61],v[54:55] op_sel:[0,0,1] op_sel_hi:[1,0,1]\n"
"v_pk_fma_f32 v[82:83],v[82:83],v[60:61],v[54:55] op_sel:[0,0,1] op_sel_hi:[1,0,1]\n"
"v_pk_fma_f32 v[84:85],v[84:85],v[60:61],v[54:55] op_sel:[0,0,1] op_sel_hi:[1,0,1]\n"
"v_pk_fma_f32 v[86:87],v[86:87],v[60:61],v[54:55] op_sel:[0,0,1] op_sel_hi:[1,0,1]\n"
CV "v116,v80,v81\n"
CV "v117,v82,v83\n"
CV "v118,v84,v85\n"
CV "v119,v86,v87\n"
MF "a[200:203],v[176:179],v[92:95],a[200:203]\n"
AW "a172,v116\n"
AW "a173,v117\n"
AW "a174,v118\n"
AW "a175,v119\n"
AR "v72,a176\n"
AR "v73,a177\n"
AR "v74,a178\n"
MF "a[220:223],v[180:183],v[92:95],a[220:223]\n"
AR "v75,a179\n"
AR "v76,a196\n"
AR "v77,a197\n"
AR "v78,a198\n"
AR "v79,a199\n"
"v_pk_fma_f32 v[72:73],v[72:73],v[60:61],v[56:57] op_sel:[0,1,0] op_sel_hi:[1,1,0]\n"
"v_pk_fma_f32 v[74:75],v[74:75],v[60:61],v[56:57] op_sel:[0,1,0] op_sel_hi:[1,1,0]\n"
"v_pk_fma_f32 v[76:77],v[76:77],v[60:61],v[56:57] op_sel:[0,1,0] op_sel_hi:[1,1,0]\n"
"v_pk_fma_f32 v[78:79],v[78:79],v[60:61],v[56:57] op_sel:[0,1,0] op_sel_hi:[1,1,0]\n"
CV "v112,v72,v73\n"
CV "v113,v74,v75\n"
CV "v114,v76,v77\n"
CV "v115,v78,v79\n"
MF "a[204:207],v[176:179],v[96:99],a[204:207]\n"
AW "a176,v112\n"
AW "a177,v113\n"
AW "a178,v114\n"
AW "a179,v115\n"
AR "v80,a200\n"
AR "v81,a201\n"
AR "v82,a202\n"
MF "a[224:227],v[180:183],v[96:99],a[224:227]\n"
AR "v83,a203\n"
AR "v84,a220\n"
AR "v85,a221\n"
AR "v86,a222\n"
AR "v87,a223\n"
"v_pk_fma_f32 v[80:81],v[80:81],v[56:57],v[52:53] op_sel:[0,1,0] op_sel_hi:[1,1,0]\n"
"v_pk_fma_f32 v[82:83],v[82:83],v[56:57],v[52:53] op_sel:[0,1,0] op_sel_hi:[1,1,0]\n"
"v_pk_fma_f32 v[84:85],v[84:85],v[56:57],v[52:53] op_sel:[0,1,0] op_sel_hi:[1,1,0]\n"
"v_pk_fma_f32 v[86:87],v[86:87],v[56:57],v[52:53] op_sel:[0,1,0] op_sel_hi:[1,1,0]\n"
CV "v116,v80,v81\n"
CV "v117,v82,v83\n"
CV "v118,v84,v85\n"
CV "v119,v86,v87\n"
MF "a[208:211],v[176:179],v[100:103],a[208:211]\n"
AW "a20,v116\n"
AW "a21,v117\n"
AW "a22,v118\n"
AW "a23,v119\n"
AR "v72,a204\n"
AR "v73,a205\n"
AR "v74,a206\n"
MF "a[228:231],v[180:183],v[100:103],a[228:231]\n"
AR "v75,a207\n"
AR "v76,a224\n"
AR "v77,a225\n"
AR "v78,a226\n"
AR "v79,a227\n"
"v_pk_fma_f32 v[72:73],v[72:73],v[58:59],v[52:53] op_sel:[0,0,1] op_sel_hi:[1,0,1]\n"
"v_pk_fma_f32 v[74:75],v[74:75],v[58:59],v[52:53] op_sel:[0,0,1] op_sel_hi:[1,0,1]\n"
"v_pk_fma_f32 v[76:77],v[76:77],v[58:59],v[52:53] op_sel:[0,0,1] op_sel_hi:[1,0,1]\n"
"v_pk_fma_f32 v[78:79],v[78:79],v[58:59],v[52:53] op_sel:[0,0,1] op_sel_hi:[1,0,1]\n"
CV "v112,v72,v73\n"
CV "v113,v74,v75\n"
CV "v114,v76,v77\n"
CV "v115,v78,v79\n"
MF "a[212:215],v[176:179],v[104:107],a[212:215]\n"
AW "a24,v112\n"
AW "a25,v113\n"
AW "a26,v114\n"
AW "a27,v115\n"
AR "v80,a208\n"
AR "v81,a209\n"
AR "v82,a210\n"
MF "a[232:235],v[180:183],v[104:107],a[232:235]\n"
AR "v83,a211\n"
AR "v84,a228\n"
AR "v85,a229\n"
AR "v86,a230\n"
AR "v87,a231\n"
"v_pk_fma_f32 v[80:81],v[80:81],v[58:59],v[54:55] op_sel:[0,1,0] op_sel_hi:[1,1,0]\n"
"v_pk_fma_f32 v[82:83],v[82:83],v[58:59],v[54:55] op_sel:[0,1,0] op_sel_hi:[1,1,0]\n"
"v_pk_fma_f32 v[84:85],v[84:85],v[58:59],v[54:55] op_sel:[0,1,0] op_sel_hi:[1,1,0]\n"
"v_pk_fma_f32 v[86:87],v[86:87],v[58:59],v[54:55] op_sel:[0,1,0] op_sel_hi:[1,1,0]\n"
CV "v116,v80,v81\n"
CV "v117,v82,v83\n"
CV "v118,v84,v85\n"
CV "v119,v86,v87\n"
MF "a[216:219],v[176:179],v[108:111],a[216:219]\n"
AW "a28,v116\n"
AW "a29,v117\n"
AW "a30,v118\n"
AW "a31,v119\n"
AR "v72,a212\n"
AR "v73,a213\n"
AR "v74,a214\n"
MF "a[236:239],v[180:183],v[108:111],a[236:239]\n"
AR "v75,a215\n"
AR "v76,a232\n"
AR "v77,a233\n"
AR "v78,a234\n"
AR "v79,a235\n"
"v_pk_fma_f32 v[72:73],v[72:73],v[60:61],v[54:55] op_sel:[0,0,1] op_sel_hi:[1,0,1]\n"
"v_pk_fma_f32 v[74:75],v[74:75],v[60:61],v[54:55] op_sel:[0,0,1] op_sel_hi:[1,0,1]\n"
"v_pk_fma_f32 v[76:77],v[76:77],v[60:61],v[54:55] op_sel:[0,0,1] op_sel_hi:[1,0,1]\n"
"v_pk_fma_f32 v[78:79],v[78:79],v[60:61],v[54:55] op_sel:[0,0,1] op_sel_hi:[1,0,1]\n"
CV "v112,v72,v73\n"
CV "v113,v74,v75\n"
CV "v114,v76,v77\n"
CV "v115,v78,v79\n"
AW "a32,v112\n"
AW "a33,v113\n"
AW "a34,v114\n"
AW "a35,v115\n"
AR "v80,a216\n"
AR "v81,a217\n"
AR "v82,a218\n"
AR "v83,a219\n"
AR "v84,a236\n"
AR "v85,a237\n"
AR "v86,a238\n"
AR "v87,a239\n"
"v_pk_fma_f32 v[80:81],v[80:81],v[60:61],v[56:57] op_sel:[0,1,0] op_sel_hi:[1,1,0]\n"
"v_pk_fma_f32 v[82:83],v[82:83],v[60:61],v[56:57] op_sel:[0,1,0] op_sel_hi:[1,1,0]\n"
"v_pk_fma_f32 v[84:85],v[84:85],v[60:61],v[56:57] op_sel:[0,1,0] op_sel_hi:[1,1,0]\n"
"v_pk_fma_f32 v[86:87],v[86:87],v[60:61],v[56:57] op_sel:[0,1,0] op_sel_hi:[1,1,0]\n"
CV "v116,v80,v81\n"
CV "v117,v82,v83\n"
CV "v118,v84,v85\n"
CV "v119,v86,v87\n"
AW "a36,v116\n"
AW "a37,v117\n"
AW "a38,v118\n"
AW "a39,v119\n"
"s_mov_b32 s13,s11\n"
"s_mov_b32 s70,s71\n"
"s_mov_b32 s57,1\n"
AR "v0,a240\n"
AR "v2,a241\n"
AR "v4,a242\n"
AR "v6,a243\n"
"v_xor_b32 v1,64,v0\n"
"v_xor_b32 v3,64,v2\n"
"v_xor_b32 v5,64,v4\n"
"v_add_u32 v11,97280,v6\n"
"v_add_u32 v12,194560,v6\n"
"v_add_u32 v13,291840,v6\n"
"s_mov_b64 vcc,-1\n"
"s_waitcnt vmcnt(8) lgkmcnt(0)\n"
"s_barrier\n"
"s_nop 0\n"
TR "a[200:201],v0\n"
TR "a[202:203],v0 offset:512\n"
TR "a[204:205],v0 offset:4096\n"
TR "a[206:207],v0 offset:4608\n"
"ds_read_b128 a[60:63],v2\n"
"ds_read_b128 a[100:103],v3\n"
"ds_read_b128 a[64:67],v2 offset:2048\n"
"ds_read_b128 a[104:107],v3 offset:2048\n"
"ds_read_b128 a[68:71],v2 offset:4096\n"
"ds_read_b128 a[108:111],v3 offset:4096\n"
"ds_read_b128 a[72:75],v2 offset:6144\n"
"ds_read_b128 a[112:115],v3 offset:6144\n"
"ds_read_b128 a[76:79],v2 offset:8192\n"
"ds_read_b128 a[116:119],v3 offset:8192\n"
TR "a[208:209],v0 offset:8\n"
TR "a[210:211],v0 offset:520\n"
TR "a[212:213],v0 offset:4104\n"
TR "a[214:215],v0 offset:4616\n"
"s_waitcnt lgkmcnt(0)\n"
"s_branch .Lq0\n"
".Lpgep_g:\n"
"s_cbranch_execz .Lisr3\n"
".Lep:\n"
"s_lshl_b32 s58,s56,10\n"
"s_add_u32 s58,s58,73728\n"
"v_add_u32 v120,s58,v9\n"
"v_add_u32 v121,65536,v120\n"
"s_sub_u32 s59,s71,s70\n"
TR "v[136:137],v2 offset:24576\n"
TR "v[138:139],v2 offset:30720\n"
TR "v[140:141],v3 offset:24576\n"
TR "v[142:143],v3 offset:30720\n"
TR "v[144:145],v4 offset:24576\n"
TR "v[146:147],v4 offset:30720\n"
TR "v[148:149],v5 offset:24576\n"
TR "v[150:151],v5 offset:30720\n"
TR "v[152:153],v2 offset:25600\n"
TR "v[154:155],v2 offset:31744\n"
TR "v[156:157],v3 offset:25600\n"
TR "v[158:159],v3 offset:31744\n"
TR "v[160:161],v4 offset:25600\n"
TR "v[162:163],v4 offset:31744\n"
TR "v[164:165],v5 offset:25600\n"
TR "v[166:167],v5 offset:31744\n"
TR "v[168:169],v2 offset:26624\n"
TR "v[170:171],v2 offset:32768\n"
TR "v[172:173],v3 offset:26624\n"
TR "v[174:175],v3 offset:32768\n"
TR "v[176:177],v4 offset:26624\n"
TR "v[178:179],v4 offset:32768\n"
TR "v[180:181],v5 offset:26624\n"
TR "v[182:183],v5 offset:32768\n"
"v_mov_b32 v52,v57\n"
"v_mov_b32 v53,v58\n"
"v_mov_b32 v54,v59\n"
"v_mov_b32 v55,v60\n"
"v_mov_b32 v56,v61\n"
"s_add_u32 m0,s65,49152\n"
"s_nop 0\n"
LD "v8,s[36:39],s73 offen offset:3072 nt lds\n"
"s_add_u32 m0,s65,50048\n"
"s_nop 0\n"
LD "v8,s[36:39],s73 offen offset:3200 nt lds\n"
"v_permlane16_swap_b32 v57,v52\n"
"v_permlane16_swap_b32 v58,v53\n"
"v_permlane16_swap_b32 v59,v54\n"
"v_permlane16_swap_b32 v60,v55\n"
"v_permlane16_swap_b32 v61,v56\n"
"v_add_f32 v57,v57,v52\n"
"v_add_f32 v58,v58,v53\n"
"v_add_f32 v59,v59,v54\n"
"v_add_f32 v60,v60,v55\n"
"v_add_f32 v61,v61,v56\n"
"v_mov_b32 v52,v57\n"
"v_mov_b32 v53,v58\n"
"v_mov_b32 v54,v59\n"
"v_mov_b32 v55,v60\n"
"v_mov_b32 v56,v61\n"
"s_add_u32 m0,s65,50944\n"
"s_nop 0\n"
LD "v8,s[36:39],s73 offen offset:3328 nt lds\n"
"s_sub_u32 s74,s96,s16\n"
"s_add_u32 s74,s74,s71\n"
"s_add_u32 s20,s20,s59\n"
"s_mov_b32 s70,s71\n"
LD "v[232:235],v9,s[40:43],s75 offen sc1\n"
"v_permlane32_swap_b32 v57,v52\n"
"v_permlane32_swap_b32 v58,v53\n"
"v_permlane32_swap_b32 v59,v54\n"
"v_permlane32_swap_b32 v60,v55\n"
"v_permlane32_swap_b32 v61,v56\n"
"v_add_f32 v57,v57,v52\n"
"v_add_f32 v58,v58,v53\n"
"v_add_f32 v59,v59,v54\n"
"v_add_f32 v60,v60,v55\n"
"v_add_f32 v61,v61,v56\n"
"s_waitcnt lgkmcnt(15)\n"
MF "a[0:3],v[136:139],v[92:95],a[0:3]\n"
MF "a[20:23],v[140:143],v[92:95],a[20:23]\n"
"v_rcp_f32 v57,v57\n"
"v_rcp_f32 v58,v58\n"
"v_rcp_f32 v59,v59\n"
"v_rcp_f32 v60,v60\n"
"v_rcp_f32_e64 v61,v61\n"
MF "a[4:7],v[136:139],v[96:99],a[4:7]\n"
"v_mul_f32_e64 v52,-s13,v57\n"
"v_mul_f32_e64 v53,-s13,v58\n"
"v_mul_f32_e64 v54,-s13,v59\n"
"v_mul_f32_e64 v55,-s13,v60\n"
"v_mul_f32_e64 v56,-s13,v61\n"
AR "v72,a0\n"
AR "v73,a1\n"
AR "v74,a2\n"
MF "a[24:27],v[140:143],v[96:99],a[24:27]\n"
AR "v75,a3\n"
AR "v76,a20\n"
AR "v77,a21\n"
AR "v78,a22\n"
AR "v79,a23\n"
"v_pk_fma_f32 v[72:73],v[72:73],v[56:57],v[52:53] op_sel:[0,1,0] op_sel_hi:[1,1,0]\n"
"v_pk_fma_f32 v[74:75],v[74:75],v[56:57],v[52:53] op_sel:[0,1,0] op_sel_hi:[1,1,0]\n"
"v_pk_fma_f32 v[76:77],v[76:77],v[56:57],v[52:53] op_sel:[0,1,0] op_sel_hi:[1,1,0]\n"
"v_pk_fma_f32 v[78:79],v[78:79],v[56:57],v[52:53] op_sel:[0,1,0] op_sel_hi:[1,1,0]\n"
CV "v112,v72,v73\n"
CV "v113,v74,v75\n"
CV "v114,v76,v77\n"
CV "v115,v78,v79\n"
MF "a[8:11],v[136:139],v[100:103],a[8:11]\n"
"ds_read_b128 v[184:187],v120 offset:24576\n"
"ds_write_b128 v120,v[112:115] offset:24576\n"
LD "v[236:239],v9,s[40:43],s75 offen offset:1024 sc1\n"
AR "v80,a4\n"
AR "v81,a5\n"
AR "v82,a6\n"
MF "a[28:31],v[140:143],v[100:103],a[28:31]\n"
AR "v83,a7\n"
AR "v84,a24\n"
AR "v85,a25\n"
AR "v86,a26\n"
AR "v87,a27\n"
"v_pk_fma_f32 v[80:81],v[80:81],v[58:59],v[52:53] op_sel:[0,0,1] op_sel_hi:[1,0,1]\n"
"v_pk_fma_f32 v[82:83],v[82:83],v[58:59],v[52:53] op_sel:[0,0,1] op_sel_hi:[1,0,1]\n"
"v_pk_fma_f32 v[84:85],v[84:85],v[58:59],v[52:53] op_sel:[0,0,1] op_sel_hi:[1,0,1]\n"
"v_pk_fma_f32 v[86:87],v[86:87],v[58:59],v[52:53] op_sel:[0,0,1] op_sel_hi:[1,0,1]\n"
CV "v116,v80,v81\n"
CV "v117,v82,v83\n"
CV "v118,v84,v85\n"
CV "v119,v86,v87\n"
MF "a[12:15],v[136:139],v[104:107],a[12:15]\n"
"ds_read_b128 v[188:191],v120 offset:28672\n"
"ds_write_b128 v120,v[116:119] offset:28672\n"
AR "v72,a8\n"
AR "v73,a9\n"
AR "v74,a10\n"
MF "a[32:35],v[140:143],v[104:107],a[32:35]\n"
AR "v75,a11\n"
AR "v76,a28\n"
AR "v77,a29\n"
AR "v78,a30\n"
AR "v79,a31\n"
"v_pk_fma_f32 v[72:73],v[72:73],v[58:59],v[54:55] op_sel:[0,1,0] op_sel_hi:[1,1,0]\n"
"v_pk_fma_f32 v[74:75],v[74:75],v[58:59],v[54:55] op_sel:[0,1,0] op_sel_hi:[1,1,0]\n"
"v_pk_fma_f32 v[76:77],v[76:77],v[58:59],v[54:55] op_sel:[0,1,0] op_sel_hi:[1,1,0]\n"
"v_pk_fma_f32 v[78:79],v[78:79],v[58:59],v[54:55] op_sel:[0,1,0] op_sel_hi:[1,1,0]\n"
CV "v112,v72,v73\n"
CV "v113,v74,v75\n"
CV "v114,v76,v77\n"
CV "v115,v78,v79\n"
MF "a[16:19],v[136:139],v[108:111],a[16:19]\n"
"ds_read_b128 v[192:195],v120 offset:32768\n"
"ds_write_b128 v120,v[112:115] offset:32768\n"
"s_waitcnt lgkmcnt(0)\n"
"s_barrier\n"
LD "v[240:243],v9,s[40:43],s75 offen offset:2048 sc1\n"
AR "v80,a12\n"
AR "v81,a13\n"
AR "v82,a14\n"
MF "a[36:39],v[140:143],v[108:111],a[36:39]\n"
AR "v83,a15\n"
AR "v84,a32\n"
AR "v85,a33\n"
AR "v86,a34\n"
".Lpg13g:\n"
"s_cbranch_execz .Lisr3\n"
"s_nop 0\n"
AR "v87,a35\n"
"v_pk_fma_f32 v[80:81],v[80:81],v[60:61],v[54:55] op_sel:[0,0,1] op_sel_hi:[1,0,1]\n"
"v_pk_fma_f32 v[82:83],v[82:83],v[60:61],v[54:55] op_sel:[0,0,1] op_sel_hi:[1,0,1]\n"
"v_pk_fma_f32 v[84:85],v[84:85],v[60:61],v[54:55] op_sel:[0,0,1] op_sel_hi:[1,0,1]\n"
"v_pk_fma_f32 v[86:87],v[86:87],v[60:61],v[54:55] op_sel:[0,0,1] op_sel_hi:[1,0,1]\n"
CV "v116,v80,v81\n"
CV "v117,v82,v83\n"
CV "v118,v84,v85\n"
CV "v119,v86,v87\n"
MF "a[40:43],v[144:147],v[92:95],a[40:43]\n"
"ds_read_b128 v[196:199],v120 offset:36864\n"
"ds_write_b128 v120,v[116:119] offset:36864\n"
LD "v[244:247],v9,s[40:43],s75 offen offset:3072 sc1\n"
AR "v72,a16\n"
AR "v73,a17\n"
AR "v74,a18\n"
MF "a[60:63],v[148:151],v[92:95],a[60:63]\n"
AR "v75,a19\n"
AR "v76,a36\n"
AR "v77,a37\n"
AR "v78,a38\n"
AR "v79,a39\n"
"v_pk_fma_f32 v[72:73],v[72:73],v[60:61],v[56:57] op_sel:[0,1,0] op_sel_hi:[1,1,0]\n"
"v_pk_fma_f32 v[74:75],v[74:75],v[60:61],v[56:57] op_sel:[0,1,0] op_sel_hi:[1,1,0]\n"
"v_pk_fma_f32 v[76:77],v[76:77],v[60:61],v[56:57] op_sel:[0,1,0] op_sel_hi:[1,1,0]\n"
"v_pk_fma_f32 v[78:79],v[78:79],v[60:61],v[56:57] op_sel:[0,1,0] op_sel_hi:[1,1,0]\n"
CV "v112,v72,v73\n"
CV "v113,v74,v75\n"
CV "v114,v76,v77\n"
CV "v115,v78,v79\n"
MF "a[44:47],v[144:147],v[96:99],a[44:47]\n"
"ds_read_b128 v[200:203],v120 offset:40960\n"
"ds_write_b128 v120,v[112:115] offset:40960\n"
AR "v80,a40\n"
AR "v81,a41\n"
AR "v82,a42\n"
MF "a[64:67],v[148:151],v[96:99],a[64:67]\n"
AR "v83,a43\n"
AR "v84,a60\n"
AR "v85,a61\n"
AR "v86,a62\n"
AR "v87,a63\n"
"v_pk_fma_f32 v[80:81],v[80:81],v[56:57],v[52:53] op_sel:[0,1,0] op_sel_hi:[1,1,0]\n"
"v_pk_fma_f32 v[82:83],v[82:83],v[56:57],v[52:53] op_sel:[0,1,0] op_sel_hi:[1,1,0]\n"
"v_pk_fma_f32 v[84:85],v[84:85],v[56:57],v[52:53] op_sel:[0,1,0] op_sel_hi:[1,1,0]\n"
"v_pk_fma_f32 v[86:87],v[86:87],v[56:57],v[52:53] op_sel:[0,1,0] op_sel_hi:[1,1,0]\n"
CV "v116,v80,v81\n"
CV "v117,v82,v83\n"
CV "v118,v84,v85\n"
CV "v119,v86,v87\n"
MF "a[48:51],v[144:147],v[100:103],a[48:51]\n"
"ds_read_b128 v[204:207],v120 offset:45056\n"
"ds_write_b128 v120,v[116:119] offset:45056\n"
"s_add_u32 s58,s75,4096\n"
LD "v[248:251],v9,s[40:43],s58 offen sc1\n"
AR "v72,a44\n"
AR "v73,a45\n"
AR "v74,a46\n"
MF "a[68:71],v[148:151],v[100:103],a[68:71]\n"
AR "v75,a47\n"
AR "v76,a64\n"
AR "v77,a65\n"
AR "v78,a66\n"
AR "v79,a67\n"
"v_pk_fma_f32 v[72:73],v[72:73],v[58:59],v[52:53] op_sel:[0,0,1] op_sel_hi:[1,0,1]\n"
"v_pk_fma_f32 v[74:75],v[74:75],v[58:59],v[52:53] op_sel:[0,0,1] op_sel_hi:[1,0,1]\n"
"v_pk_fma_f32 v[76:77],v[76:77],v[58:59],v[52:53] op_sel:[0,0,1] op_sel_hi:[1,0,1]\n"
"v_pk_fma_f32 v[78:79],v[78:79],v[58:59],v[52:53] op_sel:[0,0,1] op_sel_hi:[1,0,1]\n"
CV "v112,v72,v73\n"
CV "v113,v74,v75\n"
CV "v114,v76,v77\n"
CV "v115,v78,v79\n"
MF "a[52:55],v[144:147],v[104:107],a[52:55]\n"
"ds_read_b128 v[208:211],v120 offset:49152\n"
"ds_write_b128 v120,v[112:115] offset:49152\n"
LD "v[252:255],v9,s[40:43],s58 offen offset:1024 sc1\n"
AR "v80,a48\n"
AR "v81,a49\n"
AR "v82,a50\n"
MF "a[72:75],v[148:151],v[104:107],a[72:75]\n"
AR "v83,a51\n"
AR "v84,a68\n"
AR "v85,a69\n"
AR "v86,a70\n"
AR "v87,a71\n"
"v_pk_fma_f32 v[80:81],v[80:81],v[58:59],v[54:55] op_sel:[0,1,0] op_sel_hi:[1,1,0]\n"
"v_pk_fma_f32 v[82:83],v[82:83],v[58:59],v[54:55] op_sel:[0,1,0] op_sel_hi:[1,1,0]\n"
"v_pk_fma_f32 v[84:85],v[84:85],v[58:59],v[54:55] op_sel:[0,1,0] op_sel_hi:[1,1,0]\n"
"v_pk_fma_f32 v[86:87],v[86:87],v[58:59],v[54:55] op_sel:[0,1,0] op_sel_hi:[1,1,0]\n"
CV "v116,v80,v81\n"
CV "v117,v82,v83\n"
CV "v118,v84,v85\n"
CV "v119,v86,v87\n"
MF "a[56:59],v[144:147],v[108:111],a[56:59]\n"
"ds_read_b128 v[212:215],v120 offset:53248\n"
"ds_write_b128 v120,v[116:119] offset:53248\n"
AR "v72,a52\n"
AR "v73,a53\n"
AR "v74,a54\n"
MF "a[76:79],v[148:151],v[108:111],a[76:79]\n"
AR "v75,a55\n"
AR "v76,a72\n"
AR "v77,a73\n"
AR "v78,a74\n"
AR "v79,a75\n"
"v_pk_fma_f32 v[72:73],v[72:73],v[60:61],v[54:55] op_sel:[0,0,1] op_sel_hi:[1,0,1]\n"
"v_pk_fma_f32 v[74:75],v[74:75],v[60:61],v[54:55] op_sel:[0,0,1] op_sel_hi:[1,0,1]\n"
"v_pk_fma_f32 v[76:77],v[76:77],v[60:61],v[54:55] op_sel:[0,0,1] op_sel_hi:[1,0,1]\n"
"v_pk_fma_f32 v[78:79],v[78:79],v[60:61],v[54:55] op_sel:[0,0,1] op_sel_hi:[1,0,1]\n"
CV "v112,v72,v73\n"
CV "v113,v74,v75\n"
CV "v114,v76,v77\n"
CV "v115,v78,v79\n"
MF "a[80:83],v[152:155],v[92:95],a[80:83]\n"
"ds_read_b128 v[216:219],v120 offset:57344\n"
"ds_write_b128 v120,v[112:115] offset:57344\n"
AR "v80,a56\n"
AR "v81,a57\n"
AR "v82,a58\n"
MF "a[100:103],v[156:159],v[92:95],a[100:103]\n"
AR "v83,a59\n"
AR "v84,a76\n"
AR "v85,a77\n"
AR "v86,a78\n"
AR "v87,a79\n"
"v_pk_fma_f32 v[80:81],v[80:81],v[60:61],v[56:57] op_sel:[0,1,0] op_sel_hi:[1,1,0]\n"
"v_pk_fma_f32 v[82:83],v[82:83],v[60:61],v[56:57] op_sel:[0,1,0] op_sel_hi:[1,1,0]\n"
"v_pk_fma_f32 v[84:85],v[84:85],v[60:61],v[56:57] op_sel:[0,1,0] op_sel_hi:[1,1,0]\n"
"v_pk_fma_f32 v[86:87],v[86:87],v[60:61],v[56:57] op_sel:[0,1,0] op_sel_hi:[1,1,0]\n"
CV "v116,v80,v81\n"
CV "v117,v82,v83\n"
CV "v118,v84,v85\n"
CV "v119,v86,v87\n"
MF "a[84:87],v[152:155],v[96:99],a[84:87]\n"
"ds_read_b128 v[220:223],v120 offset:61440\n"
"ds_write_b128 v120,v[116:119] offset:61440\n"
AR "v72,a80\n"
AR "v73,a81\n"
AR "v74,a82\n"
MF "a[104:107],v[156:159],v[96:99],a[104:107]\n"
AR "v75,a83\n"
AR "v76,a100\n"
AR "v77,a101\n"
AR "v78,a102\n"
AR "v79,a103\n"
"v_pk_fma_f32 v[72:73],v[72:73],v[56:57],v[52:53] op_sel:[0,1,0] op_sel_hi:[1,1,0]\n"
"v_pk_fma_f32 v[74:75],v[74:75],v[56:57],v[52:53] op_sel:[0,1,0] op_sel_hi:[1,1,0]\n"
"v_pk_fma_f32 v[76:77],v[76:77],v[56:57],v[52:53] op_sel:[0,1,0] op_sel_hi:[1,1,0]\n"
"v_pk_fma_f32 v[78:79],v[78:79],v[56:57],v[52:53] op_sel:[0,1,0] op_sel_hi:[1,1,0]\n"
CV "v112,v72,v73\n"
CV "v113,v74,v75\n"
CV "v114,v76,v77\n"
CV "v115,v78,v79\n"
MF "a[88:91],v[152:155],v[100:103],a[88:91]\n"
"ds_read_b128 v[224:227],v121\n"
"ds_write_b128 v121,v[112:115]\n"
AR "v80,a84\n"
AR "v81,a85\n"
AR "v82,a86\n"
MF "a[108:111],v[156:159],v[100:103],a[108:111]\n"
AR "v83,a87\n"
AR "v84,a104\n"
AR "v85,a105\n"
AR "v86,a106\n"
AR "v87,a107\n"
"v_pk_fma_f32 v[80:81],v[80:81],v[58:59],v[52:53] op_sel:[0,0,1] op_sel_hi:[1,0,1]\n"
"v_pk_fma_f32 v[82:83],v[82:83],v[58:59],v[52:53] op_sel:[0,0,1] op_sel_hi:[1,0,1]\n"
"v_pk_fma_f32 v[84:85],v[84:85],v[58:59],v[52:53] op_sel:[0,0,1] op_sel_hi:[1,0,1]\n"
"v_pk_fma_f32 v[86:87],v[86:87],v[58:59],v[52:53] op_sel:[0,0,1] op_sel_hi:[1,0,1]\n"
CV "v116,v80,v81\n"
CV "v117,v82,v83\n"
CV "v118,v84,v85\n"
CV "v119,v86,v87\n"
MF "a[92:95],v[152:155],v[104:107],a[92:95]\n"
"ds_read_b128 v[228:231],v121 offset:4096\n"
"ds_write_b128 v121,v[116:119] offset:4096\n"
AR "v72,a88\n"
AR "v73,a89\n"
AR "v74,a90\n"
MF "a[112:115],v[156:159],v[104:107],a[112:115]\n"
AR "v75,a91\n"
AR "v76,a108\n"
AR "v77,a109\n"
AR "v78,a110\n"
AR "v79,a111\n"
"v_pk_fma_f32 v[72:73],v[72:73],v[58:59],v[54:55] op_sel:[0,1,0] op_sel_hi:[1,1,0]\n"
"v_pk_fma_f32 v[74:75],v[74:75],v[58:59],v[54:55] op_sel:[0,1,0] op_sel_hi:[1,1,0]\n"
"v_pk_fma_f32 v[76:77],v[76:77],v[58:59],v[54:55] op_sel:[0,1,0] op_sel_hi:[1,1,0]\n"
"v_pk_fma_f32 v[78:79],v[78:79],v[58:59],v[54:55] op_sel:[0,1,0] op_sel_hi:[1,1,0]\n"
CV "v112,v72,v73\n"
CV "v113,v74,v75\n"
CV "v114,v76,v77\n"
CV "v115,v78,v79\n"
MF "a[96:99],v[152:155],v[108:111],a[96:99]\n"
"ds_read_b128 v[136:139],v121 offset:8192\n"
"ds_write_b128 v121,v[112:115] offset:8192\n"
AR "v80,a92\n"
AR "v81,a93\n"
AR "v82,a94\n"
MF "a[116:119],v[156:159],v[108:111],a[116:119]\n"
AR "v83,a95\n"
AR "v84,a112\n"
AR "v85,a113\n"
AR "v86,a114\n"
AR "v87,a115\n"
"v_pk_fma_f32 v[80:81],v[80:81],v[60:61],v[54:55] op_sel:[0,0,1] op_sel_hi:[1,0,1]\n"
"v_pk_fma_f32 v[82:83],v[82:83],v[60:61],v[54:55] op_sel:[0,0,1] op_sel_hi:[1,0,1]\n"
"v_pk_fma_f32 v[84:85],v[84:85],v[60:61],v[54:55] op_sel:[0,0,1] op_sel_hi:[1,0,1]\n"
"v_pk_fma_f32 v[86:87],v[86:87],v[60:61],v[54:55] op_sel:[0,0,1] op_sel_hi:[1,0,1]\n"
CV "v116,v80,v81\n"
CV "v117,v82,v83\n"
CV "v118,v84,v85\n"
CV "v119,v86,v87\n"
MF "a[120:123],v[160:163],v[92:95],a[120:123]\n"
"ds_read_b128 v[140:143],v121 offset:12288\n"
"ds_write_b128 v121,v[116:119] offset:12288\n"
AR "v72,a96\n"
AR "v73,a97\n"
AR "v74,a98\n"
MF "a[140:143],v[164:167],v[92:95],a[140:143]\n"
AR "v75,a99\n"
AR "v76,a116\n"
AR "v77,a117\n"
AR "v78,a118\n"
AR "v79,a119\n"
"v_pk_fma_f32 v[72:73],v[72:73],v[60:61],v[56:57] op_sel:[0,1,0] op_sel_hi:[1,1,0]\n"
"v_pk_fma_f32 v[74:75],v[74:75],v[60:61],v[56:57] op_sel:[0,1,0] op_sel_hi:[1,1,0]\n"
"v_pk_fma_f32 v[76:77],v[76:77],v[60:61],v[56:57] op_sel:[0,1,0] op_sel_hi:[1,1,0]\n"
"v_pk_fma_f32 v[78:79],v[78:79],v[60:61],v[56:57] op_sel:[0,1,0] op_sel_hi:[1,1,0]\n"
CV "v112,v72,v73\n"
CV "v113,v74,v75\n"
CV "v114,v76,v77\n"
CV "v115,v78,v79\n"
MF "a[124:127],v[160:163],v[96:99],a[124:127]\n"
"ds_read_b128 v[144:147],v121 offset:16384\n"
"ds_write_b128 v121,v[112:115] offset:16384\n"
AR "v80,a120\n"
AR "v81,a121\n"
AR "v82,a122\n"
MF "a[144:147],v[164:167],v[96:99],a[144:147]\n"
AR "v83,a123\n"
AR "v84,a140\n"
AR "v85,a141\n"
AR "v86,a142\n"
AR "v87,a143\n"
"v_pk_fma_f32 v[80:81],v[80:81],v[56:57],v[52:53] op_sel:[0,1,0] op_sel_hi:[1,1,0]\n"
"v_pk_fma_f32 v[82:83],v[82:83],v[56:57],v[52:53] op_sel:[0,1,0] op_sel_hi:[1,1,0]\n"
"v_pk_fma_f32 v[84:85],v[84:85],v[56:57],v[52:53] op_sel:[0,1,0] op_sel_hi:[1,1,0]\n"
"v_pk_fma_f32 v[86:87],v[86:87],v[56:57],v[52:53] op_sel:[0,1,0] op_sel_hi:[1,1,0]\n"
CV "v116,v80,v81\n"
CV "v117,v82,v83\n"
CV "v118,v84,v85\n"
CV "v119,v86,v87\n"
MF "a[128:131],v[160:163],v[100:103],a[128:131]\n"
"ds_read_b128 v[148:151],v121 offset:20480\n"
"ds_write_b128 v121,v[116:119] offset:20480\n"
AR "v72,a124\n"
AR "v73,a125\n"
AR "v74,a126\n"
MF "a[148:151],v[164:167],v[100:103],a[148:151]\n"
AR "v75,a127\n"
AR "v76,a144\n"
AR "v77,a145\n"
AR "v78,a146\n"
AR "v79,a147\n"
"v_pk_fma_f32 v[72:73],v[72:73],v[58:59],v[52:53] op_sel:[0,0,1] op_sel_hi:[1,0,1]\n"
"v_pk_fma_f32 v[74:75],v[74:75],v[58:59],v[52:53] op_sel:[0,0,1] op_sel_hi:[1,0,1]\n"
"v_pk_fma_f32 v[76:77],v[76:77],v[58:59],v[52:53] op_sel:[0,0,1] op_sel_hi:[1,0,1]\n"
"v_pk_fma_f32 v[78:79],v[78:79],v[58:59],v[52:53] op_sel:[0,0,1] op_sel_hi:[1,0,1]\n"
CV "v112,v72,v73\n"
CV "v113,v74,v75\n"
CV "v114,v76,v77\n"
CV "v115,v78,v79\n"
MF "a[132:135],v[160:163],v[104:107],a[132:135]\n"
"ds_write_b128 v120,v[112:115]\n"
AR "v80,a128\n"
AR "v81,a129\n"
AR "v82,a130\n"
MF "a[152:155],v[164:167],v[104:107],a[152:155]\n"
AR "v83,a131\n"
AR "v84,a148\n"
AR "v85,a149\n"
AR "v86,a150\n"
AR "v87,a151\n"
"v_pk_fma_f32 v[80:81],v[80:81],v[58:59],v[54:55] op_sel:[0,1,0] op_sel_hi:[1,1,0]\n"
"v_pk_fma_f32 v[82:83],v[82:83],v[58:59],v[54:55] op_sel:[0,1,0] op_sel_hi:[1,1,0]\n"
"v_pk_fma_f32 v[84:85],v[84:85],v[58:59],v[54:55] op_sel:[0,1,0] op_sel_hi:[1,1,0]\n"
"v_pk_fma_f32 v[86:87],v[86:87],v[58:59],v[54:55] op_sel:[0,1,0] op_sel_hi:[1,1,0]\n"
CV "v116,v80,v81\n"
CV "v117,v82,v83\n"
CV "v118,v84,v85\n"
CV "v119,v86,v87\n"
MF "a[136:139],v[160:163],v[108:111],a[136:139]\n"
"ds_write_b128 v120,v[116:119] offset:4096\n"
AR "v72,a132\n"
AR "v73,a133\n"
AR "v74,a134\n"
MF "a[156:159],v[164:167],v[108:111],a[156:159]\n"
AR "v75,a135\n"
AR "v76,a152\n"
AR "v77,a153\n"
AR "v78,a154\n"
AR "v79,a155\n"
"v_pk_fma_f32 v[72:73],v[72:73],v[60:61],v[54:55] op_sel:[0,0,1] op_sel_hi:[1,0,1]\n"
"v_pk_fma_f32 v[74:75],v[74:75],v[60:61],v[54:55] op_sel:[0,0,1] op_sel_hi:[1,0,1]\n"
"v_pk_fma_f32 v[76:77],v[76:77],v[60:61],v[54:55] op_sel:[0,0,1] op_sel_hi:[1,0,1]\n"
"v_pk_fma_f32 v[78:79],v[78:79],v[60:61],v[54:55] op_sel:[0,0,1] op_sel_hi:[1,0,1]\n"
CV "v112,v72,v73\n"
CV "v113,v74,v75\n"
CV "v114,v76,v77\n"
CV "v115,v78,v79\n"
MF "a[160:163],v[168:171],v[92:95],a[160:163]\n"
"ds_write_b128 v120,v[112:115] offset:8192\n"
AR "v80,a136\n"
AR "v81,a137\n"
AR "v82,a138\n"
MF "a[180:183],v[172:175],v[92:95],a[180:183]\n"
AR "v83,a139\n"
AR "v84,a156\n"
AR "v85,a157\n"
AR "v86,a158\n"
AR "v87,a159\n"
"v_pk_fma_f32 v[80:81],v[80:81],v[60:61],v[56:57] op_sel:[0,1,0] op_sel_hi:[1,1,0]\n"
"v_pk_fma_f32 v[82:83],v[82:83],v[60:61],v[56:57] op_sel:[0,1,0] op_sel_hi:[1,1,0]\n"
"v_pk_fma_f32 v[84:85],v[84:85],v[60:61],v[56:57] op_sel:[0,1,0] op_sel_hi:[1,1,0]\n"
"v_pk_fma_f32 v[86:87],v[86:87],v[60:61],v[56:57] op_sel:[0,1,0] op_sel_hi:[1,1,0]\n"
CV "v116,v80,v81\n"
CV "v117,v82,v83\n"
CV "v118,v84,v85\n"
CV "v119,v86,v87\n"
MF "a[164:167],v[168:171],v[96:99],a[164:167]\n"
"ds_write_b128 v120,v[116:119] offset:12288\n"
AR "v72,a160\n"
AR "v73,a161\n"
AR "v74,a162\n"
MF "a[184:187],v[172:175],v[96:99],a[184:187]\n"
AR "v75,a163\n"
AR "v76,a180\n"
AR "v77,a181\n"
AR "v78,a182\n"
AR "v79,a183\n"
"v_pk_fma_f32 v[72:73],v[72:73],v[56:57],v[52:53] op_sel:[0,1,0] op_sel_hi:[1,1,0]\n"
"v_pk_fma_f32 v[74:75],v[74:75],v[56:57],v[52:53] op_sel:[0,1,0] op_sel_hi:[1,1,0]\n"
"v_pk_fma_f32 v[76:77],v[76:77],v[56:57],v[52:53] op_sel:[0,1,0] op_sel_hi:[1,1,0]\n"
"v_pk_fma_f32 v[78:79],v[78:79],v[56:57],v[52:53] op_sel:[0,1,0] op_sel_hi:[1,1,0]\n"
CV "v112,v72,v73\n"
CV "v113,v74,v75\n"
CV "v114,v76,v77\n"
CV "v115,v78,v79\n"
MF "a[168:171],v[168:171],v[100:103],a[168:171]\n"
"ds_write_b128 v120,v[112:115] offset:16384\n"
AR "v80,a164\n"
AR "v81,a165\n"
AR "v82,a166\n"
MF "a[188:191],v[172:175],v[100:103],a[188:191]\n"
AR "v83,a167\n"
AR "v84,a184\n"
AR "v85,a185\n"
AR "v86,a186\n"
AR "v87,a187\n"
"v_pk_fma_f32 v[80:81],v[80:81],v[58:59],v[52:53] op_sel:[0,0,1] op_sel_hi:[1,0,1]\n"
"v_pk_fma_f32 v[82:83],v[82:83],v[58:59],v[52:53] op_sel:[0,0,1] op_sel_hi:[1,0,1]\n"
"v_pk_fma_f32 v[84:85],v[84:85],v[58:59],v[52:53] op_sel:[0,0,1] op_sel_hi:[1,0,1]\n"
"v_pk_fma_f32 v[86:87],v[86:87],v[58:59],v[52:53] op_sel:[0,0,1] op_sel_hi:[1,0,1]\n"
CV "v116,v80,v81\n"
CV "v117,v82,v83\n"
CV "v118,v84,v85\n"
CV "v119,v86,v87\n"
MF "a[172:175],v[168:171],v[104:107],a[172:175]\n"
"ds_write_b128 v120,v[116:119] offset:20480\n"
AR "v72,a168\n"
AR "v73,a169\n"
AR "v74,a170\n"
MF "a[192:195],v[172:175],v[104:107],a[192:195]\n"
AR "v75,a171\n"
AR "v76,a188\n"
AR "v77,a189\n"
AR "v78,a190\n"
AR "v79,a191\n"
"v_pk_fma_f32 v[72:73],v[72:73],v[58:59],v[54:55] op_sel:[0,1,0] op_sel_hi:[1,1,0]\n"
"v_pk_fma_f32 v[74:75],v[74:75],v[58:59],v[54:55] op_sel:[0,1,0] op_sel_hi:[1,1,0]\n"
"v_pk_fma_f32 v[76:77],v[76:77],v[58:59],v[54:55] op_sel:[0,1,0] op_sel_hi:[1,1,0]\n"
"v_pk_fma_f32 v[78:79],v[78:79],v[58:59],v[54:55] op_sel:[0,1,0] op_sel_hi:[1,1,0]\n"
CV "v112,v72,v73\n"
CV "v113,v74,v75\n"
CV "v114,v76,v77\n"
CV "v115,v78,v79\n"
MF "a[176:179],v[168:171],v[108:111],a[176:179]\n"
AR "v152,a240\n"
AW "a240,v112\n"
AR "v153,a241\n"
AW "a241,v113\n"
AR "v154,a242\n"
AW "a242,v114\n"
AR "v155,a243\n"
AW "a243,v115\n"
AR "v80,a172\n"
AR "v81,a173\n"
AR "v82,a174\n"
MF "a[196:199],v[172:175],v[108:111],a[196:199]\n"
AR "v83,a175\n"
AR "v84,a192\n"
AR "v85,a193\n"
AR "v86,a194\n"
AR "v87,a195\n"
"v_pk_fma_f32 v[80:81],v[80:81],v[60:61],v[54:55] op_sel:[0,0,1] op_sel_hi:[1,0,1]\n"
"v_pk_fma_f32 v[82:83],v[82:83],v[60:61],v[54:55] op_sel:[0,0,1] op_sel_hi:[1,0,1]\n"
"v_pk_fma_f32 v[84:85],v[84:85],v[60:61],v[54:55] op_sel:[0,0,1] op_sel_hi:[1,0,1]\n"
"v_pk_fma_f32 v[86:87],v[86:87],v[60:61],v[54:55] op_sel:[0,0,1] op_sel_hi:[1,0,1]\n"
CV "v116,v80,v81\n"
CV "v117,v82,v83\n"
CV "v118,v84,v85\n"
CV "v119,v86,v87\n"
MF "a[200:203],v[176:179],v[92:95],a[200:203]\n"
AR "v156,a244\n"
AW "a244,v116\n"
AR "v157,a245\n"
AW "a245,v117\n"
AR "v158,a246\n"
AW "a246,v118\n"
AR "v159,a247\n"
AW "a247,v119\n"
AR "v72,a176\n"
AR "v73,a177\n"
AR "v74,a178\n"
MF "a[220:223],v[180:183],v[92:95],a[220:223]\n"
AR "v75,a179\n"
AR "v76,a196\n"
AR "v77,a197\n"
AR "v78,a198\n"
AR "v79,a199\n"
"v_pk_fma_f32 v[72:73],v[72:73],v[60:61],v[56:57] op_sel:[0,1,0] op_sel_hi:[1,1,0]\n"
"v_pk_fma_f32 v[74:75],v[74:75],v[60:61],v[56:57] op_sel:[0,1,0] op_sel_hi:[1,1,0]\n"
"v_pk_fma_f32 v[76:77],v[76:77],v[60:61],v[56:57] op_sel:[0,1,0] op_sel_hi:[1,1,0]\n"
"v_pk_fma_f32 v[78:79],v[78:79],v[60:61],v[56:57] op_sel:[0,1,0] op_sel_hi:[1,1,0]\n"
CV "v112,v72,v73\n"
CV "v113,v74,v75\n"
CV "v114,v76,v77\n"
CV "v115,v78,v79\n"
MF "a[204:207],v[176:179],v[96:99],a[204:207]\n"
AR "v160,a248\n"
AW "a248,v112\n"
AR "v161,a249\n"
AW "a249,v113\n"
AR "v162,a250\n"
AW "a250,v114\n"
AR "v163,a251\n"
AW "a251,v115\n"
AR "v80,a200\n"
AR "v81,a201\n"
AR "v82,a202\n"
MF "a[224:227],v[180:183],v[96:99],a[224:227]\n"
AR "v83,a203\n"
AR "v84,a220\n"
AR "v85,a221\n"
AR "v86,a222\n"
AR "v87,a223\n"
"v_pk_fma_f32 v[80:81],v[80:81],v[56:57],v[52:53] op_sel:[0,1,0] op_sel_hi:[1,1,0]\n"
"v_pk_fma_f32 v[82:83],v[82:83],v[56:57],v[52:53] op_sel:[0,1,0] op_sel_hi:[1,1,0]\n"
"v_pk_fma_f32 v[84:85],v[84:85],v[56:57],v[52:53] op_sel:[0,1,0] op_sel_hi:[1,1,0]\n"
"v_pk_fma_f32 v[86:87],v[86:87],v[56:57],v[52:53] op_sel:[0,1,0] op_sel_hi:[1,1,0]\n"
CV "v116,v80,v81\n"
CV "v117,v82,v83\n"
CV "v118,v84,v85\n"
CV "v119,v86,v87\n"
MF "a[208:211],v[176:179],v[100:103],a[208:211]\n"
"v_mov_b32 v164,v62\n"
"v_mov_b32 v62,v116\n"
"v_mov_b32 v165,v63\n"
"v_mov_b32 v63,v117\n"
"v_mov_b32 v166,v64\n"
"v_mov_b32 v64,v118\n"
"v_mov_b32 v167,v65\n"
"v_mov_b32 v65,v119\n"
AR "v72,a204\n"
AR "v73,a205\n"
AR "v74,a206\n"
MF "a[228:231],v[180:183],v[100:103],a[228:231]\n"
AR "v75,a207\n"
AR "v76,a224\n"
AR "v77,a225\n"
AR "v78,a226\n"
AR "v79,a227\n"
"v_pk_fma_f32 v[72:73],v[72:73],v[58:59],v[52:53] op_sel:[0,0,1] op_sel_hi:[1,0,1]\n"
"v_pk_fma_f32 v[74:75],v[74:75],v[58:59],v[52:53] op_sel:[0,0,1] op_sel_hi:[1,0,1]\n"
"v_pk_fma_f32 v[76:77],v[76:77],v[58:59],v[52:53] op_sel:[0,0,1] op_sel_hi:[1,0,1]\n"
"v_pk_fma_f32 v[78:79],v[78:79],v[58:59],v[52:53] op_sel:[0,0,1] op_sel_hi:[1,0,1]\n"
CV "v112,v72,v73\n"
CV "v113,v74,v75\n"
CV "v114,v76,v77\n"
CV "v115,v78,v79\n"
MF "a[212:215],v[176:179],v[104:107],a[212:215]\n"
"v_mov_b32 v168,v66\n"
"v_mov_b32 v66,v112\n"
"v_mov_b32 v169,v67\n"
"v_mov_b32 v67,v113\n"
"v_mov_b32 v170,v68\n"
"v_mov_b32 v68,v114\n"
"v_mov_b32 v171,v69\n"
"v_mov_b32 v69,v115\n"
AR "v80,a208\n"
AR "v81,a209\n"
AR "v82,a210\n"
MF "a[232:235],v[180:183],v[104:107],a[232:235]\n"
AR "v83,a211\n"
AR "v84,a228\n"
AR "v85,a229\n"
AR "v86,a230\n"
AR "v87,a231\n"
"v_pk_fma_f32 v[80:81],v[80:81],v[58:59],v[54:55] op_sel:[0,1,0] op_sel_hi:[1,1,0]\n"
"v_pk_fma_f32 v[82:83],v[82:83],v[58:59],v[54:55] op_sel:[0,1,0] op_sel_hi:[1,1,0]\n"
"v_pk_fma_f32 v[84:85],v[84:85],v[58:59],v[54:55] op_sel:[0,1,0] op_sel_hi:[1,1,0]\n"
"v_pk_fma_f32 v[86:87],v[86:87],v[58:59],v[54:55] op_sel:[0,1,0] op_sel_hi:[1,1,0]\n"
CV "v116,v80,v81\n"
CV "v117,v82,v83\n"
CV "v118,v84,v85\n"
CV "v119,v86,v87\n"
MF "a[216:219],v[176:179],v[108:111],a[216:219]\n"
"v_mov_b32 v172,v70\n"
"v_mov_b32 v70,v116\n"
"v_mov_b32 v173,v71\n"
".Lpg14g:\n"
"s_cbranch_execz .Lisr3\n"
"s_nop 0\n"
"v_mov_b32 v71,v117\n"
"v_mov_b32 v174,v132\n"
"v_mov_b32 v132,v118\n"
"v_mov_b32 v175,v133\n"
"v_mov_b32 v133,v119\n"
AR "v72,a212\n"
AR "v73,a213\n"
AR "v74,a214\n"
MF "a[236:239],v[180:183],v[108:111],a[236:239]\n"
AR "v75,a215\n"
AR "v76,a232\n"
AR "v77,a233\n"
AR "v78,a234\n"
AR "v79,a235\n"
"v_pk_fma_f32 v[72:73],v[72:73],v[60:61],v[54:55] op_sel:[0,0,1] op_sel_hi:[1,0,1]\n"
"v_pk_fma_f32 v[74:75],v[74:75],v[60:61],v[54:55] op_sel:[0,0,1] op_sel_hi:[1,0,1]\n"
"v_pk_fma_f32 v[76:77],v[76:77],v[60:61],v[54:55] op_sel:[0,0,1] op_sel_hi:[1,0,1]\n"
"v_pk_fma_f32 v[78:79],v[78:79],v[60:61],v[54:55] op_sel:[0,0,1] op_sel_hi:[1,0,1]\n"
CV "v112,v72,v73\n"
CV "v113,v74,v75\n"
CV "v114,v76,v77\n"
CV "v115,v78,v79\n"
"v_mov_b32 v176,v134\n"
"v_mov_b32 v134,v112\n"
"v_mov_b32 v177,v135\n"
"v_mov_b32 v135,v113\n"
"v_mov_b32 v178,v6\n"
"v_mov_b32 v6,v114\n"
"v_mov_b32 v179,v13\n"
"v_mov_b32 v13,v115\n"
AR "v80,a216\n"
AR "v81,a217\n"
AR "v82,a218\n"
AR "v83,a219\n"
AR "v84,a236\n"
AR "v85,a237\n"
AR "v86,a238\n"
AR "v87,a239\n"
"v_pk_fma_f32 v[80:81],v[80:81],v[60:61],v[56:57] op_sel:[0,1,0] op_sel_hi:[1,1,0]\n"
"v_pk_fma_f32 v[82:83],v[82:83],v[60:61],v[56:57] op_sel:[0,1,0] op_sel_hi:[1,1,0]\n"
"v_pk_fma_f32 v[84:85],v[84:85],v[60:61],v[56:57] op_sel:[0,1,0] op_sel_hi:[1,1,0]\n"
"v_pk_fma_f32 v[86:87],v[86:87],v[60:61],v[56:57] op_sel:[0,1,0] op_sel_hi:[1,1,0]\n"
CV "v116,v80,v81\n"
CV "v117,v82,v83\n"
CV "v118,v84,v85\n"
CV "v119,v86,v87\n"
"v_mov_b32 v180,v14\n"
"v_mov_b32 v14,v116\n"
"v_mov_b32 v181,v15\n"
"v_mov_b32 v15,v117\n"
AR "v182,a254\n"
AW "a254,v118\n"
AR "v183,a255\n"
AW "a255,v119\n"
"s_mov_b32 s13,s11\n"
"v_subrev_u32 v2,49152,v2\n"
"v_subrev_u32 v3,49152,v3\n"
"v_subrev_u32 v4,49152,v4\n"
"v_subrev_u32 v5,49152,v5\n"
"s_mov_b32 s57,2\n"
"s_mov_b32 s69,1\n"
"s_mov_b32 s61,3\n"
"s_memrealtime s[100:101]\n"
"s_waitcnt lgkmcnt(0)\n"
"s_cmp_eq_u32 s87,1\n"
"s_cselect_b32 s97,0,0\n"
"s_cselect_b32 s21,215,0\n"
"s_add_u32 s21,s21,s100\n"
"s_waitcnt vmcnt(0)\n"
"s_branch .Latt2\n"
".LpgattB_f:\n"
"s_cbranch_execz .Lisr3\n"
".LattB:\n"
AR "v0,a244\n"
AR "v2,a245\n"
"v_xor_b32 v1,64,v0\n"
"v_xor_b32 v3,32,v2\n"
"v_xor_b32 v4,64,v2\n"
"v_xor_b32 v5,96,v2\n"
"s_mov_b32 s61,3\n"
"s_memtime s[52:53]\n"
"s_memrealtime s[100:101]\n"
"s_waitcnt lgkmcnt(0)\n"
"s_sub_u32 s53,s52,s80\n"
"s_cmp_gt_u32 s53,30000\n"
"s_cselect_b32 s101,1,0\n"
"s_cmp_eq_u32 s87,2\n"
"s_cselect_b32 s87,s101,s87\n"
"s_cmp_eq_u32 s87,1\n"
"s_cselect_b32 s97,0,0\n"
"s_cselect_b32 s21,215,0\n"
"s_add_u32 s21,s21,s100\n"
".Latt2:\n"
"s_cmp_eq_u32 s97,0\n"
"s_cselect_b32 s95,0x47800000,0\n"
"s_mov_b32 s77,s95\n"
"s_waitcnt vmcnt(12) lgkmcnt(0)\n"
"s_barrier\n"
"ds_read_b128 v[16:19],v0\n"
"ds_read_b128 v[20:23],v1\n"
"ds_read_b128 v[24:27],v0 offset:1024\n"
"s_cmp_eq_u32 s57,1\n"
"s_cbranch_scc1 .Lt0k\n"
".Lt0n:\n"
"s_waitcnt lgkmcnt(2)\n"
MF "v[72:75],v[16:19],v[136:139],0\n"
"ds_read_b128 v[28:31],v1 offset:1024\n"
MF "v[76:79],v[16:19],v[140:143],0\n"
MF "v[80:83],v[16:19],v[144:147],0\n"
MF "v[84:87],v[16:19],v[148:151],0\n"
MF "v[88:91],v[16:19],v[152:155],0\n"
"s_waitcnt lgkmcnt(2)\n"
"ds_read_b128 v[16:19],v0 offset:2048\n"
MF "v[72:75],v[20:23],v[156:159],v[72:75]\n"
MF "v[76:79],v[20:23],v[160:163],v[76:79]\n"
MF "v[80:83],v[20:23],v[164:167],v[80:83]\n"
MF "v[84:87],v[20:23],v[168:171],v[84:87]\n"
MF "v[88:91],v[20:23],v[172:175],v[88:91]\n"
"s_waitcnt lgkmcnt(2)\n"
"ds_read_b128 v[20:23],v1 offset:2048\n"
MF "v[72:75],v[24:27],v[176:179],v[72:75]\n"
MF "v[76:79],v[24:27],v[180:183],v[76:79]\n"
MF "v[80:83],v[24:27],v[184:187],v[80:83]\n"
MF "v[84:87],v[24:27],v[188:191],v[84:87]\n"
MF "v[88:91],v[24:27],v[192:195],v[88:91]\n"
"s_waitcnt lgkmcnt(2)\n"
"ds_read_b128 v[24:27],v0 offset:6144\n"
MF "v[72:75],v[28:31],v[196:199],v[72:75]\n"
MF "v[76:79],v[28:31],v[200:203],v[76:79]\n"
MF "v[80:83],v[28:31],v[204:207],v[80:83]\n"
MF "v[84:87],v[28:31],v[208:211],v[84:87]\n"
MF "v[88:91],v[28:31],v[212:215],v[88:91]\n"
"s_waitcnt lgkmcnt(2)\n"
"ds_read_b128 v[28:31],v1 offset:6144\n"
MF "v[72:75],v[16:19],v[216:219],v[72:75]\n"
MF "v[76:79],v[16:19],v[220:223],v[76:79]\n"
MF "v[80:83],v[16:19],v[224:227],v[80:83]\n"
MF "v[84:87],v[16:19],v[228:231],v[84:87]\n"
MF "v[88:91],v[16:19],v[232:235],v[88:91]\n"
"s_waitcnt lgkmcnt(2)\n"
"ds_read_b128 v[16:19],v0 offset:7168\n"
MF "v[72:75],v[20:23],v[236:239],v[72:75]\n"
MF "v[76:79],v[20:23],v[240:243],v[76:79]\n"
MF "v[80:83],v[20:23],v[244:247],v[80:83]\n"
MF "v[84:87],v[20:23],v[248:251],v[84:87]\n"
MF "v[88:91],v[20:23],v[252:255],v[88:91]\n"
"s_waitcnt lgkmcnt(2)\n"
"ds_read_b128 v[20:23],v1 offset:7168\n"
MF "v[112:115],v[24:27],v[136:139],0\n"
MF "v[116:119],v[24:27],v[140:143],0\n"
MF "v[120:123],v[24:27],v[144:147],0\n"
MF "v[124:127],v[24:27],v[148:151],0\n"
MF "v[128:131],v[24:27],v[152:155],0\n"
"s_waitcnt lgkmcnt(2)\n"
"ds_read_b128 v[24:27],v0 offset:8192\n"
MF "v[112:115],v[28:31],v[156:159],v[112:115]\n"
"v_max3_f32 v52,v72,v73,v74\n"
"v_max_f32 v52,v52,v75\n"
MF "v[116:119],v[28:31],v[160:163],v[116:119]\n"
"v_max3_f32 v53,v76,v77,v78\n"
"v_max_f32 v53,v53,v79\n"
MF "v[120:123],v[28:31],v[164:167],v[120:123]\n"
"v_max3_f32 v54,v80,v81,v82\n"
"v_max_f32 v54,v54,v83\n"
MF "v[124:127],v[28:31],v[168:171],v[124:127]\n"
"v_max3_f32 v55,v84,v85,v86\n"
"v_max_f32 v55,v55,v87\n"
MF "v[128:131],v[28:31],v[172:175],v[128:131]\n"
"s_waitcnt lgkmcnt(2)\n"
"ds_read_b128 v[28:31],v1 offset:8192\n"
"v_max3_f32 v56,v88,v89,v90\n"
MF "v[112:115],v[16:19],v[176:179],v[112:115]\n"
"v_max_f32 v56,v56,v91\n"
MF "v[116:119],v[16:19],v[180:183],v[116:119]\n"
MF "v[120:123],v[16:19],v[184:187],v[120:123]\n"
MF "v[124:127],v[16:19],v[188:191],v[124:127]\n"
MF "v[128:131],v[16:19],v[192:195],v[128:131]\n"
"s_waitcnt lgkmcnt(2)\n"
MF "v[112:115],v[20:23],v[196:199],v[112:115]\n"
MF "v[116:119],v[20:23],v[200:203],v[116:119]\n"
"s_waitcnt lgkmcnt(0)\n"
"s_barrier\n"
MF "v[120:123],v[20:23],v[204:207],v[120:123]\n"
"s_add_u32 s72,s72,6144\n"
MF "v[124:127],v[20:23],v[208:211],v[124:127]\n"
"s_cmp_eq_u32 s72,s17\n"
"s_cselect_b32 s72,s16,s72\n"
"s_add_u32 m0,s64,0\n"
MF "v[128:131],v[20:23],v[212:215],v[128:131]\n"
LD "v7,s[32:35],s72 offen nt lds\n"
MF "v[112:115],v[24:27],v[216:219],v[112:115]\n"
"s_add_u32 m0,s64,896\n"
MF "v[116:119],v[24:27],v[220:223],v[116:119]\n"
LD "v7,s[32:35],s72 offen offset:128 nt lds\n"
MF "v[120:123],v[24:27],v[224:227],v[120:123]\n"
"s_add_u32 m0,s64,1792\n"
MF "v[124:127],v[24:27],v[228:231],v[124:127]\n"
LD "v7,s[32:35],s72 offen offset:256 nt lds\n"
MF "v[128:131],v[24:27],v[232:235],v[128:131]\n"
"s_add_u32 s72,s72,6144\n"
"s_add_u32 m0,s64,6144\n"
MF "v[112:115],v[28:31],v[236:239],v[112:115]\n"
LD "v7,s[32:35],s72 offen nt lds\n"
MF "v[116:119],v[28:31],v[240:243],v[116:119]\n"
"s_add_u32 m0,s64,7040\n"
MF "v[120:123],v[28:31],v[244:247],v[120:123]\n"
LD "v7,s[32:35],s72 offen offset:128 nt lds\n"
MF "v[124:127],v[28:31],v[248:251],v[124:127]\n"
"s_add_u32 m0,s64,7936\n"
MF "v[128:131],v[28:31],v[252:255],v[128:131]\n"
LD "v7,s[32:35],s72 offen offset:256 nt lds\n"
"s_branch .Lt0e\n"
".Lpgt0k_f:\n"
"s_cbranch_execz .Lisr3\n"
".Lt0k:\n"
"s_lshl_b32 s58,s56,10\n"
"s_add_u32 s58,s58,73728\n"
"v_add_u32 v32,s58,v9\n"
"v_add_u32 v33,65536,v32\n"
"s_waitcnt lgkmcnt(2)\n"
MF "v[72:75],v[16:19],v[136:139],0\n"
"ds_read_b128 v[28:31],v1 offset:1024\n"
AR "v62,a20\n"
MF "v[76:79],v[16:19],v[140:143],0\n"
"ds_write_b128 v32,a[0:3] offset:24576\n"
MF "v[80:83],v[16:19],v[144:147],0\n"
"ds_write_b128 v32,a[4:7] offset:28672\n"
MF "v[84:87],v[16:19],v[148:151],0\n"
AR "v63,a21\n"
AR "v64,a22\n"
MF "v[88:91],v[16:19],v[152:155],0\n"
"s_waitcnt lgkmcnt(4)\n"
"ds_read_b128 v[16:19],v0 offset:2048\n"
AR "v65,a23\n"
MF "v[72:75],v[20:23],v[156:159],v[72:75]\n"
"ds_write_b128 v32,a[8:11] offset:32768\n"
MF "v[76:79],v[20:23],v[160:163],v[76:79]\n"
AR "v66,a24\n"
AR "v67,a25\n"
MF "v[80:83],v[20:23],v[164:167],v[80:83]\n"
"ds_write_b128 v32,a[12:15] offset:36864\n"
MF "v[84:87],v[20:23],v[168:171],v[84:87]\n"
AR "v68,a26\n"
AR "v69,a27\n"
MF "v[88:91],v[20:23],v[172:175],v[88:91]\n"
"s_waitcnt lgkmcnt(6)\n"
"ds_read_b128 v[20:23],v1 offset:2048\n"
AR "v70,a28\n"
MF "v[72:75],v[24:27],v[176:179],v[72:75]\n"
"ds_write_b128 v32,a[16:19] offset:40960\n"
MF "v[76:79],v[24:27],v[180:183],v[76:79]\n"
AR "v71,a29\n"
AR "v132,a30\n"
MF "v[80:83],v[24:27],v[184:187],v[80:83]\n"
AR "v133,a31\n"
AR "v134,a32\n"
MF "v[84:87],v[24:27],v[188:191],v[84:87]\n"
"ds_write_b128 v32,a[40:43] offset:45056\n"
MF "v[88:91],v[24:27],v[192:195],v[88:91]\n"
"s_waitcnt lgkmcnt(8)\n"
"ds_read_b128 v[24:27],v0 offset:6144\n"
AR "v135,a33\n"
MF "v[72:75],v[28:31],v[196:199],v[72:75]\n"
"ds_write_b128 v32,a[44:47] offset:49152\n"
MF "v[76:79],v[28:31],v[200:203],v[76:79]\n"
AR "v6,a34\n"
AR "v13,a35\n"
MF "v[80:83],v[28:31],v[204:207],v[80:83]\n"
AR "v14,a36\n"
AR "v15,a37\n"
MF "v[84:87],v[28:31],v[208:211],v[84:87]\n"
"ds_write_b128 v32,a[48:51] offset:53248\n"
MF "v[88:91],v[28:31],v[212:215],v[88:91]\n"
"s_waitcnt lgkmcnt(8)\n"
"ds_read_b128 v[28:31],v1 offset:6144\n"
"v_accvgpr_mov_b32 a254,a38\n"
MF "v[72:75],v[16:19],v[216:219],v[72:75]\n"
"v_accvgpr_mov_b32 a255,a39\n"
"v_accvgpr_mov_b32 a240,a168\n"
MF "v[76:79],v[16:19],v[220:223],v[76:79]\n"
"ds_write_b128 v32,a[52:55] offset:57344\n"
MF "v[80:83],v[16:19],v[224:227],v[80:83]\n"
"v_accvgpr_mov_b32 a241,a169\n"
"v_accvgpr_mov_b32 a242,a170\n"
MF "v[84:87],v[16:19],v[228:231],v[84:87]\n"
"ds_write_b128 v32,a[56:59] offset:61440\n"
MF "v[88:91],v[16:19],v[232:235],v[88:91]\n"
"s_waitcnt lgkmcnt(8)\n"
"ds_read_b128 v[16:19],v0 offset:7168\n"
"v_accvgpr_mov_b32 a243,a171\n"
MF "v[72:75],v[20:23],v[236:239],v[72:75]\n"
"v_accvgpr_mov_b32 a244,a172\n"
"v_accvgpr_mov_b32 a245,a173\n"
MF "v[76:79],v[20:23],v[240:243],v[76:79]\n"
"ds_write_b128 v33,a[80:83]\n"
MF "v[80:83],v[20:23],v[244:247],v[80:83]\n"
"v_accvgpr_mov_b32 a246,a174\n"
"v_accvgpr_mov_b32 a247,a175\n"
MF "v[84:87],v[20:23],v[248:251],v[84:87]\n"
"v_accvgpr_mov_b32 a248,a176\n"
"v_accvgpr_mov_b32 a249,a177\n"
MF "v[88:91],v[20:23],v[252:255],v[88:91]\n"
"s_waitcnt lgkmcnt(7)\n"
"ds_read_b128 v[20:23],v1 offset:7168\n"
"v_accvgpr_mov_b32 a250,a178\n"
MF "v[112:115],v[24:27],v[136:139],0\n"
"ds_write_b128 v33,a[84:87] offset:4096\n"
MF "v[116:119],v[24:27],v[140:143],0\n"
"ds_write_b128 v33,a[88:91] offset:8192\n"
MF "v[120:123],v[24:27],v[144:147],0\n"
"v_accvgpr_mov_b32 a251,a179\n"
MF "v[124:127],v[24:27],v[148:151],0\n"
MF "v[128:131],v[24:27],v[152:155],0\n"
"s_waitcnt lgkmcnt(7)\n"
"ds_read_b128 v[24:27],v0 offset:8192\n"
MF "v[112:115],v[28:31],v[156:159],v[112:115]\n"
"v_max3_f32 v52,v72,v73,v74\n"
"v_max_f32 v52,v52,v75\n"
MF "v[116:119],v[28:31],v[160:163],v[116:119]\n"
"v_max3_f32 v53,v76,v77,v78\n"
"v_max_f32 v53,v53,v79\n"
MF "v[120:123],v[28:31],v[164:167],v[120:123]\n"
"v_max3_f32 v54,v80,v81,v82\n"
"v_max_f32 v54,v54,v83\n"
MF "v[124:127],v[28:31],v[168:171],v[124:127]\n"
"v_max3_f32 v55,v84,v85,v86\n"
"v_max_f32 v55,v55,v87\n"
MF "v[128:131],v[28:31],v[172:175],v[128:131]\n"
"s_waitcnt lgkmcnt(5)\n"
"ds_read_b128 v[28:31],v1 offset:8192\n"
"v_max3_f32 v56,v88,v89,v90\n"
MF "v[112:115],v[16:19],v[176:179],v[112:115]\n"
"v_max_f32 v56,v56,v91\n"
MF "v[116:119],v[16:19],v[180:183],v[116:119]\n"
"ds_write_b128 v33,a[92:95] offset:12288\n"
MF "v[120:123],v[16:19],v[184:187],v[120:123]\n"
"ds_write_b128 v33,a[96:99] offset:16384\n"
MF "v[124:127],v[16:19],v[188:191],v[124:127]\n"
"ds_write_b128 v33,a[120:123] offset:20480\n"
MF "v[128:131],v[16:19],v[192:195],v[128:131]\n"
"s_waitcnt lgkmcnt(7)\n"
"ds_write_b128 v32,a[124:127]\n"
MF "v[112:115],v[20:23],v[196:199],v[112:115]\n"
"ds_write_b128 v32,a[128:131] offset:4096\n"
MF "v[116:119],v[20:23],v[200:203],v[116:119]\n"
"s_waitcnt lgkmcnt(0)\n"
"s_barrier\n"
MF "v[120:123],v[20:23],v[204:207],v[120:123]\n"
"s_add_u32 s72,s72,6144\n"
"ds_write_b128 v32,a[132:135] offset:8192\n"
MF "v[124:127],v[20:23],v[208:211],v[124:127]\n"
"s_cmp_eq_u32 s72,s17\n"
"s_cselect_b32 s72,s16,s72\n"
"s_add_u32 m0,s64,0\n"
MF "v[128:131],v[20:23],v[212:215],v[128:131]\n"
LD "v7,s[32:35],s72 offen nt lds\n"
MF "v[112:115],v[24:27],v[216:219],v[112:115]\n"
"s_add_u32 m0,s64,896\n"
"ds_write_b128 v32,a[136:139] offset:12288\n"
MF "v[116:119],v[24:27],v[220:223],v[116:119]\n"
LD "v7,s[32:35],s72 offen offset:128 nt lds\n"
MF "v[120:123],v[24:27],v[224:227],v[120:123]\n"
"s_add_u32 m0,s64,1792\n"
"ds_write_b128 v32,a[160:163] offset:16384\n"
MF "v[124:127],v[24:27],v[228:231],v[124:127]\n"
LD "v7,s[32:35],s72 offen offset:256 nt lds\n"
MF "v[128:131],v[24:27],v[232:235],v[128:131]\n"
"s_add_u32 s72,s72,6144\n"
"s_add_u32 m0,s64,6144\n"
MF "v[112:115],v[28:31],v[236:239],v[112:115]\n"
LD "v7,s[32:35],s72 offen nt lds\n"
MF "v[116:119],v[28:31],v[240:243],v[116:119]\n"
"s_add_u32 m0,s64,7040\n"
"ds_write_b128 v32,a[164:167] offset:20480\n"
MF "v[120:123],v[28:31],v[244:247],v[120:123]\n"
LD "v7,s[32:35],s72 offen offset:128 nt lds\n"
MF "v[124:127],v[28:31],v[248:251],v[124:127]\n"
"s_add_u32 m0,s64,7936\n"
MF "v[128:131],v[28:31],v[252:255],v[128:131]\n"
LD "v7,s[32:35],s72 offen offset:256 nt lds\n"
".Lpgt0e_g:\n"
"s_cbranch_execz .Lpg15e\n"
".Lt0e:\n"
"s_nop 1\n"
"v_max3_f32 v52,v52,v112,v113\n"
"v_max3_f32 v53,v53,v116,v117\n"
"v_max3_f32 v54,v54,v120,v121\n"
"s_nop 0\n"
"v_max3_f32 v55,v55,v124,v125\n"
"s_nop 0\n"
"v_max3_f32 v56,v56,v128,v129\n"
"v_max3_f32 v52,v52,v114,v115\n"
"v_max3_f32 v53,v53,v118,v119\n"
"v_max3_f32 v54,v54,v122,v123\n"
"v_max3_f32 v55,v55,v126,v127\n"
"v_max3_f32 v56,v56,v130,v131\n"
"v_mov_b32 v57,v52\n"
"v_mov_b32 v58,v53\n"
"v_mov_b32 v59,v54\n"
"v_mov_b32 v60,v55\n"
"v_mov_b32 v61,v56\n"
"v_permlane16_swap_b32 v52,v57\n"
"v_permlane16_swap_b32 v53,v58\n"
"v_permlane16_swap_b32 v54,v59\n"
"v_permlane16_swap_b32 v55,v60\n"
"v_permlane16_swap_b32 v56,v61\n"
"v_max_f32 v52,v52,v57\n"
"v_max_f32 v53,v53,v58\n"
"v_max_f32 v54,v54,v59\n"
"v_max_f32 v55,v55,v60\n"
"v_max_f32 v56,v56,v61\n"
"v_mov_b32 v57,v52\n"
"v_mov_b32 v58,v53\n"
"v_mov_b32 v59,v54\n"
"v_mov_b32 v60,v55\n"
"v_mov_b32 v61,v56\n"
"v_permlane32_swap_b32 v52,v57\n"
"v_permlane32_swap_b32 v53,v58\n"
"v_permlane32_swap_b32 v54,v59\n"
"v_permlane32_swap_b32 v55,v60\n"
"v_permlane32_swap_b32 v56,v61\n"
"v_max_f32 v52,v52,v57\n"
"v_max_f32 v53,v53,v58\n"
"v_max_f32 v54,v54,v59\n"
"v_max_f32 v55,v55,v60\n"
"v_max_f32_e64 v56,v56,v61\n"
"v_sub_f32 v32,0xc1300000,v52\n"
"v_sub_f32 v36,0xc1300000,v53\n"
"v_sub_f32 v40,0xc1300000,v54\n"
"v_sub_f32 v44,0xc1300000,v55\n"
"v_sub_f32 v48,0xc1300000,v56\n"
"v_mov_b32 v33,v32\n"
"v_mov_b32 v34,v32\n"
"v_mov_b32 v35,v32\n"
"v_mov_b32 v37,v36\n"
"v_mov_b32 v38,v36\n"
"v_mov_b32 v39,v36\n"
"v_mov_b32 v41,v40\n"
"v_mov_b32 v42,v40\n"
"v_mov_b32 v43,v40\n"
"v_mov_b32 v45,v44\n"
"v_mov_b32 v46,v44\n"
"v_mov_b32 v47,v44\n"
"v_mov_b32 v49,v48\n"
"v_mov_b32 v50,v48\n"
"v_mov_b32 v51,v48\n"
"v_add_f32 v72,v72,v32\n"
"v_add_f32 v73,v73,v32\n"
"v_add_f32 v74,v74,v32\n"
"v_add_f32 v75,v75,v32\n"
"v_add_f32 v112,v112,v32\n"
"v_add_f32 v113,v113,v32\n"
"v_add_f32 v114,v114,v32\n"
"v_add_f32 v115,v115,v32\n"
"v_exp_f32 v72,v72\n"
"v_exp_f32 v73,v73\n"
"v_exp_f32 v74,v74\n"
"v_exp_f32 v75,v75\n"
"v_exp_f32 v112,v112\n"
"v_exp_f32 v113,v113\n"
"v_exp_f32 v114,v114\n"
"v_exp_f32 v115,v115\n"
"v_add_f32 v52,v72,v73\n"
"v_add_f32 v52,v52,v74\n"
"v_add_f32 v52,v52,v75\n"
"v_add_f32 v52,v52,v112\n"
"v_add_f32 v52,v52,v113\n"
"v_add_f32 v52,v52,v114\n"
"v_add_f32 v52,v52,v115\n"
CV "v72,v72,v73\n"
CV "v73,v74,v75\n"
CV "v74,v112,v113\n"
CV "v75,v114,v115\n"
"v_add_f32 v76,v76,v36\n"
"v_add_f32 v77,v77,v36\n"
"v_add_f32 v78,v78,v36\n"
"v_add_f32 v79,v79,v36\n"
"v_add_f32 v116,v116,v36\n"
"v_add_f32 v117,v117,v36\n"
"v_add_f32 v118,v118,v36\n"
"v_add_f32 v119,v119,v36\n"
"v_exp_f32 v76,v76\n"
"v_exp_f32 v77,v77\n"
"v_exp_f32 v78,v78\n"
"v_exp_f32 v79,v79\n"
"v_exp_f32 v116,v116\n"
"v_exp_f32 v117,v117\n"
"v_exp_f32 v118,v118\n"
"v_exp_f32 v119,v119\n"
"v_add_f32 v53,v76,v77\n"
"v_add_f32 v53,v53,v78\n"
"v_add_f32 v53,v53,v79\n"
"v_add_f32 v53,v53,v116\n"
"v_add_f32 v53,v53,v117\n"
"v_add_f32 v53,v53,v118\n"
"v_add_f32_e64 v53,v53,v119\n"
CV "v76,v76,v77\n"
CV "v77,v78,v79\n"
CV "v78,v116,v117\n"
CV "v79,v118,v119\n"
"v_add_f32 v80,v80,v40\n"
"v_add_f32 v81,v81,v40\n"
"v_add_f32 v82,v82,v40\n"
"v_add_f32 v83,v83,v40\n"
"v_add_f32 v120,v120,v40\n"
"v_add_f32 v121,v121,v40\n"
"v_add_f32 v122,v122,v40\n"
"v_add_f32 v123,v123,v40\n"
"v_exp_f32 v80,v80\n"
"v_exp_f32 v81,v81\n"
"v_exp_f32 v82,v82\n"
"v_exp_f32 v83,v83\n"
"v_exp_f32 v120,v120\n"
"v_exp_f32 v121,v121\n"
"v_exp_f32 v122,v122\n"
"v_exp_f32 v123,v123\n"
"v_add_f32 v54,v80,v81\n"
"v_add_f32 v54,v54,v82\n"
"v_add_f32 v54,v54,v83\n"
"v_add_f32 v54,v54,v120\n"
"v_add_f32 v54,v54,v121\n"
"v_add_f32 v54,v54,v122\n"
"v_add_f32_e64 v54,v54,v123\n"
CV "v80,v80,v81\n"
CV "v81,v82,v83\n"
CV "v82,v120,v121\n"
CV "v83,v122,v123\n"
"v_add_f32 v84,v84,v44\n"
"v_add_f32 v85,v85,v44\n"
"v_add_f32 v86,v86,v44\n"
"v_add_f32 v87,v87,v44\n"
"v_add_f32 v124,v124,v44\n"
"v_add_f32 v125,v125,v44\n"
"v_add_f32 v126,v126,v44\n"
"v_add_f32 v127,v127,v44\n"
"v_mov_b32 v55,0\n"
"v_add_f32 v88,v88,v48\n"
"v_add_f32 v89,v89,v48\n"
"v_add_f32 v90,v90,v48\n"
"v_add_f32 v91,v91,v48\n"
"v_add_f32 v128,v128,v48\n"
"v_add_f32 v129,v129,v48\n"
"v_add_f32 v130,v130,v48\n"
"v_add_f32 v131,v131,v48\n"
"v_mov_b32 v56,0\n"
"s_waitcnt vmcnt(12) lgkmcnt(0)\n"
"s_barrier\n"
"ds_read_b128 v[16:19],v0 offset:24576\n"
"ds_read_b128 v[20:23],v1 offset:24576\n"
AW "a220,s13\n"
AW "a221,s13\n"
AW "a222,s13\n"
AW "a223,s13\n"
"s_waitcnt lgkmcnt(1)\n"
MF "v[92:95],v[16:19],v[136:139],v[32:35]\n"
"s_nop 0\n"
"ds_read_b128 v[24:27],v0 offset:25600\n"
"ds_read_b128 v[28:31],v1 offset:25600\n"
"s_waitcnt lgkmcnt(2)\n"
MF "v[92:95],v[20:23],v[156:159],v[92:95]\n"
"v_exp_f32 v84,v84\n"
MF "v[96:99],v[20:23],v[160:163],v[36:39]\n"
"v_exp_f32 v85,v85\n"
MF "v[96:99],v[16:19],v[140:143],v[96:99]\n"
"v_exp_f32 v86,v86\n"
MF "v[100:103],v[16:19],v[144:147],v[40:43]\n"
"v_exp_f32 v87,v87\n"
MF "v[100:103],v[20:23],v[164:167],v[100:103]\n"
"v_add_f32 v55,v55,v84\n"
"v_add_f32 v55,v55,v85\n"
MF "v[104:107],v[20:23],v[168:171],v[44:47]\n"
CV "v84,v84,v85\n"
"v_add_f32 v55,v55,v86\n"
MF "v[104:107],v[16:19],v[148:151],v[104:107]\n"
"v_add_f32 v55,v55,v87\n"
CV "v85,v86,v87\n"
MF "v[108:111],v[16:19],v[152:155],v[48:51]\n"
"v_exp_f32 v124,v124\n"
MF "v[108:111],v[20:23],v[172:175],v[108:111]\n"
"s_waitcnt lgkmcnt(0)\n"
"ds_read_b128 v[16:19],v0 offset:26624\n"
"ds_read_b128 v[20:23],v1 offset:26624\n"
MF "v[108:111],v[24:27],v[192:195],v[108:111]\n"
"v_exp_f32 v125,v125\n"
MF "v[108:111],v[28:31],v[212:215],v[108:111]\n"
"v_exp_f32 v126,v126\n"
MF "v[104:107],v[28:31],v[208:211],v[104:107]\n"
"v_exp_f32 v127,v127\n"
MF "v[104:107],v[24:27],v[188:191],v[104:107]\n"
"v_add_f32 v55,v55,v124\n"
"v_add_f32 v55,v55,v125\n"
MF "v[100:103],v[24:27],v[184:187],v[100:103]\n"
CV "v86,v124,v125\n"
"v_add_f32 v55,v55,v126\n"
MF "v[100:103],v[28:31],v[204:207],v[100:103]\n"
"v_add_f32 v55,v55,v127\n"
CV "v87,v126,v127\n"
MF "v[96:99],v[28:31],v[200:203],v[96:99]\n"
"v_exp_f32 v88,v88\n"
MF "v[96:99],v[24:27],v[180:183],v[96:99]\n"
"v_exp_f32 v89,v89\n"
MF "v[92:95],v[24:27],v[176:179],v[92:95]\n"
"v_exp_f32 v90,v90\n"
MF "v[92:95],v[28:31],v[196:199],v[92:95]\n"
"s_waitcnt lgkmcnt(0)\n"
"ds_read_b128 v[24:27],v0 offset:30720\n"
"ds_read_b128 v[28:31],v1 offset:30720\n"
MF "v[92:95],v[16:19],v[216:219],v[92:95]\n"
"v_exp_f32 v91,v91\n"
MF "v[92:95],v[20:23],v[236:239],v[92:95]\n"
"v_add_f32 v56,v56,v88\n"
"v_add_f32 v56,v56,v89\n"
MF "v[96:99],v[20:23],v[240:243],v[96:99]\n"
CV "v88,v88,v89\n"
"v_add_f32 v56,v56,v90\n"
MF "v[96:99],v[16:19],v[220:223],v[96:99]\n"
"v_add_f32 v56,v56,v91\n"
CV "v89,v90,v91\n"
MF "v[100:103],v[16:19],v[224:227],v[100:103]\n"
"v_exp_f32 v128,v128\n"
MF "v[100:103],v[20:23],v[244:247],v[100:103]\n"
"v_exp_f32 v129,v129\n"
MF "v[104:107],v[20:23],v[248:251],v[104:107]\n"
"v_exp_f32 v130,v130\n"
MF "v[104:107],v[16:19],v[228:231],v[104:107]\n"
"v_exp_f32 v131,v131\n"
MF "v[108:111],v[16:19],v[232:235],v[108:111]\n"
"v_add_f32 v56,v56,v128\n"
"v_add_f32 v56,v56,v129\n"
MF "v[108:111],v[20:23],v[252:255],v[108:111]\n"
"s_waitcnt lgkmcnt(0)\n"
"ds_read_b128 v[16:19],v0 offset:31744\n"
"ds_read_b128 v[20:23],v1 offset:31744\n"
MF "v[112:115],v[24:27],v[136:139],v[32:35]\n"
CV "v90,v128,v129\n"
"v_add_f32 v56,v56,v130\n"
MF "v[112:115],v[28:31],v[156:159],v[112:115]\n"
"v_add_f32 v56,v56,v131\n"
CV "v91,v130,v131\n"
MF "v[116:119],v[28:31],v[160:163],v[36:39]\n"
"v_exp_f32 v92,v92\n"
MF "v[116:119],v[24:27],v[140:143],v[116:119]\n"
"v_exp_f32 v93,v93\n"
MF "v[120:123],v[24:27],v[144:147],v[40:43]\n"
"v_exp_f32 v94,v94\n"
MF "v[120:123],v[28:31],v[164:167],v[120:123]\n"
"v_exp_f32 v95,v95\n"
MF "v[124:127],v[28:31],v[168:171],v[44:47]\n"
"v_add_f32 v57,v52,v92\n"
"v_add_f32 v57,v57,v93\n"
MF "v[124:127],v[24:27],v[148:151],v[124:127]\n"
CV "v92,v92,v93\n"
"v_add_f32 v57,v57,v94\n"
MF "v[128:131],v[24:27],v[152:155],v[48:51]\n"
"v_add_f32 v57,v57,v95\n"
CV "v93,v94,v95\n"
MF "v[128:131],v[28:31],v[172:175],v[128:131]\n"
"s_waitcnt lgkmcnt(0)\n"
"ds_read_b128 v[24:27],v0 offset:32768\n"
"ds_read_b128 v[28:31],v1 offset:32768\n"
MF "v[128:131],v[16:19],v[192:195],v[128:131]\n"
"v_exp_f32 v96,v96\n"
MF "v[128:131],v[20:23],v[212:215],v[128:131]\n"
".Lpg15e:\n"
"s_cbranch_execz .Lisr3\n"
"v_exp_f32 v97,v97\n"
MF "v[124:127],v[20:23],v[208:211],v[124:127]\n"
"v_exp_f32 v98,v98\n"
MF "v[124:127],v[16:19],v[188:191],v[124:127]\n"
"v_exp_f32 v99,v99\n"
MF "v[120:123],v[16:19],v[184:187],v[120:123]\n"
"v_add_f32 v58,v53,v96\n"
"v_add_f32 v58,v58,v97\n"
MF "v[120:123],v[20:23],v[204:207],v[120:123]\n"
CV "v96,v96,v97\n"
"v_add_f32 v58,v58,v98\n"
MF "v[116:119],v[20:23],v[200:203],v[116:119]\n"
"v_add_f32 v58,v58,v99\n"
CV "v97,v98,v99\n"
MF "v[116:119],v[16:19],v[180:183],v[116:119]\n"
"s_waitcnt vmcnt(6) lgkmcnt(0)\n"
"s_barrier\n"
MF "v[112:115],v[16:19],v[176:179],v[112:115]\n"
"v_exp_f32 v100,v100\n"
MF "v[112:115],v[20:23],v[196:199],v[112:115]\n"
"s_nop 0\n"
TR "v[16:17],v2 offset:49152\n"
TR "v[18:19],v2 offset:55296\n"
MF "v[112:115],v[24:27],v[216:219],v[112:115]\n"
"s_add_u32 s73,s73,12288\n"
TR "v[20:21],v3 offset:49152\n"
TR "v[22:23],v3 offset:55296\n"
MF "v[112:115],v[28:31],v[236:239],v[112:115]\n"
"s_cmp_eq_u32 s73,s19\n"
"s_cselect_b32 s73,s18,s73\n"
"s_add_u32 m0,s65,24576\n"
MF "v[116:119],v[28:31],v[240:243],v[116:119]\n"
LD "v8,s[36:39],s73 offen nt lds\n"
MF "v[116:119],v[24:27],v[220:223],v[116:119]\n"
"s_add_u32 m0,s65,25472\n"
"v_exp_f32 v101,v101\n"
MF "v[120:123],v[24:27],v[224:227],v[120:123]\n"
LD "v8,s[36:39],s73 offen offset:128 nt lds\n"
MF "v[120:123],v[28:31],v[244:247],v[120:123]\n"
"v_exp_f32 v102,v102\n"
MF "v[124:127],v[28:31],v[248:251],v[124:127]\n"
"s_add_u32 m0,s65,26368\n"
"v_exp_f32 v103,v103\n"
MF "v[124:127],v[24:27],v[228:231],v[124:127]\n"
LD "v8,s[36:39],s73 offen offset:256 nt lds\n"
MF "v[128:131],v[24:27],v[232:235],v[128:131]\n"
"v_add_f32 v59,v54,v100\n"
"v_add_f32 v59,v59,v101\n"
MF "v[128:131],v[28:31],v[252:255],v[128:131]\n"
"s_waitcnt lgkmcnt(0)\n"
TR "v[24:25],v4 offset:49152\n"
TR "v[26:27],v4 offset:55296\n"
MF "a[0:3],v[16:19],v[72:75],a[220:223]\n"
"s_add_u32 m0,s65,24576\n"
TR "v[28:29],v5 offset:49152\n"
TR "v[30:31],v5 offset:55296\n"
MF "a[20:23],v[20:23],v[72:75],a[220:223]\n"
LD "v8,s[36:39],s73 offen offset:3072 nt lds\n"
MF "a[24:27],v[20:23],v[76:79],a[220:223]\n"
CV "v100,v100,v101\n"
"v_add_f32 v59,v59,v102\n"
MF "a[4:7],v[16:19],v[76:79],a[220:223]\n"
"s_add_u32 m0,s65,25472\n"
"v_add_f32 v59,v59,v103\n"
CV "v101,v102,v103\n"
MF "a[8:11],v[16:19],v[80:83],a[220:223]\n"
LD "v8,s[36:39],s73 offen offset:3200 nt lds\n"
MF "a[28:31],v[20:23],v[80:83],a[220:223]\n"
"v_exp_f32 v104,v104\n"
MF "a[32:35],v[20:23],v[84:87],a[220:223]\n"
"s_add_u32 m0,s65,26368\n"
"v_exp_f32 v105,v105\n"
MF "a[12:15],v[16:19],v[84:87],a[220:223]\n"
LD "v8,s[36:39],s73 offen offset:3328 nt lds\n"
MF "a[16:19],v[16:19],v[88:91],a[220:223]\n"
"v_exp_f32 v106,v106\n"
MF "a[36:39],v[20:23],v[88:91],a[220:223]\n"
"s_waitcnt lgkmcnt(0)\n"
TR "v[16:17],v2 offset:50176\n"
TR "v[18:19],v2 offset:56320\n"
MF "a[56:59],v[24:27],v[88:91],a[220:223]\n"
"s_nop 0\n"
TR "v[20:21],v3 offset:50176\n"
TR "v[22:23],v3 offset:56320\n"
MF "a[76:79],v[28:31],v[88:91],a[220:223]\n"
"v_exp_f32 v107,v107\n"
MF "a[72:75],v[28:31],v[84:87],a[220:223]\n"
"v_add_f32 v60,v55,v104\n"
"v_add_f32 v60,v60,v105\n"
MF "a[52:55],v[24:27],v[84:87],a[220:223]\n"
CV "v104,v104,v105\n"
"v_add_f32 v60,v60,v106\n"
MF "a[48:51],v[24:27],v[80:83],a[220:223]\n"
"v_add_f32 v60,v60,v107\n"
CV "v105,v106,v107\n"
MF "a[68:71],v[28:31],v[80:83],a[220:223]\n"
"v_exp_f32 v108,v108\n"
MF "a[64:67],v[28:31],v[76:79],a[220:223]\n"
"v_exp_f32 v109,v109\n"
MF "a[44:47],v[24:27],v[76:79],a[220:223]\n"
"v_exp_f32 v110,v110\n"
MF "a[40:43],v[24:27],v[72:75],a[220:223]\n"
"v_exp_f32 v111,v111\n"
MF "a[60:63],v[28:31],v[72:75],a[220:223]\n"
"s_waitcnt lgkmcnt(0)\n"
TR "v[24:25],v4 offset:50176\n"
TR "v[26:27],v4 offset:56320\n"
MF "a[80:83],v[16:19],v[72:75],a[220:223]\n"
"s_nop 0\n"
TR "v[28:29],v5 offset:50176\n"
TR "v[30:31],v5 offset:56320\n"
MF "a[100:103],v[20:23],v[72:75],a[220:223]\n"
"v_add_f32 v61,v56,v108\n"
"v_add_f32 v61,v61,v109\n"
MF "a[104:107],v[20:23],v[76:79],a[220:223]\n"
CV "v108,v108,v109\n"
"v_add_f32 v61,v61,v110\n"
MF "a[84:87],v[16:19],v[76:79],a[220:223]\n"
"v_add_f32 v61,v61,v111\n"
CV "v109,v110,v111\n"
MF "a[88:91],v[16:19],v[80:83],a[220:223]\n"
"v_exp_f32 v112,v112\n"
MF "a[108:111],v[20:23],v[80:83],a[220:223]\n"
"v_exp_f32 v113,v113\n"
MF "a[112:115],v[20:23],v[84:87],a[220:223]\n"
"v_exp_f32 v114,v114\n"
MF "a[92:95],v[16:19],v[84:87],a[220:223]\n"
"v_exp_f32 v115,v115\n"
MF "a[96:99],v[16:19],v[88:91],a[220:223]\n"
"v_add_f32 v57,v57,v112\n"
"v_add_f32 v57,v57,v113\n"
MF "a[116:119],v[20:23],v[88:91],a[220:223]\n"
"s_waitcnt lgkmcnt(0)\n"
TR "v[16:17],v2 offset:51200\n"
TR "v[18:19],v2 offset:57344\n"
MF "a[136:139],v[24:27],v[88:91],a[220:223]\n"
"s_nop 0\n"
TR "v[20:21],v3 offset:51200\n"
TR "v[22:23],v3 offset:57344\n"
MF "a[156:159],v[28:31],v[88:91],a[220:223]\n"
CV "v94,v112,v113\n"
"v_add_f32 v57,v57,v114\n"
MF "a[152:155],v[28:31],v[84:87],a[220:223]\n"
"v_add_f32 v57,v57,v115\n"
CV "v95,v114,v115\n"
MF "a[132:135],v[24:27],v[84:87],a[220:223]\n"
"v_exp_f32 v116,v116\n"
MF "a[128:131],v[24:27],v[80:83],a[220:223]\n"
"v_exp_f32 v117,v117\n"
MF "a[148:151],v[28:31],v[80:83],a[220:223]\n"
"v_exp_f32 v118,v118\n"
MF "a[144:147],v[28:31],v[76:79],a[220:223]\n"
"v_exp_f32 v119,v119\n"
MF "a[124:127],v[24:27],v[76:79],a[220:223]\n"
"v_add_f32 v58,v58,v116\n"
"v_add_f32 v58,v58,v117\n"
MF "a[120:123],v[24:27],v[72:75],a[220:223]\n"
CV "v98,v116,v117\n"
"v_add_f32 v58,v58,v118\n"
MF "a[140:143],v[28:31],v[72:75],a[220:223]\n"
"s_waitcnt lgkmcnt(0)\n"
TR "v[24:25],v4 offset:51200\n"
TR "v[26:27],v4 offset:57344\n"
MF "a[160:163],v[16:19],v[72:75],a[220:223]\n"
"s_nop 0\n"
TR "v[28:29],v5 offset:51200\n"
TR "v[30:31],v5 offset:57344\n"
MF "a[180:183],v[20:23],v[72:75],a[220:223]\n"
"v_add_f32 v58,v58,v119\n"
CV "v99,v118,v119\n"
MF "a[184:187],v[20:23],v[76:79],a[220:223]\n"
"v_exp_f32 v120,v120\n"
MF "a[164:167],v[16:19],v[76:79],a[220:223]\n"
"v_exp_f32 v121,v121\n"
MF "a[168:171],v[16:19],v[80:83],a[220:223]\n"
"v_exp_f32 v122,v122\n"
MF "a[188:191],v[20:23],v[80:83],a[220:223]\n"
"v_exp_f32 v123,v123\n"
MF "a[192:195],v[20:23],v[84:87],a[220:223]\n"
"v_add_f32 v59,v59,v120\n"
"v_add_f32 v59,v59,v121\n"
MF "a[172:175],v[16:19],v[84:87],a[220:223]\n"
CV "v102,v120,v121\n"
"v_add_f32 v59,v59,v122\n"
MF "a[176:179],v[16:19],v[88:91],a[220:223]\n"
"s_waitcnt vmcnt(6) lgkmcnt(0)\n"
"s_barrier\n"
MF "a[196:199],v[20:23],v[88:91],a[220:223]\n"
"s_add_u32 s72,s72,6144\n"
"ds_read_b128 v[16:19],v0\n"
"ds_read_b128 v[20:23],v1\n"
MF "a[216:219],v[24:27],v[88:91],a[220:223]\n"
"s_cmp_eq_u32 s72,s17\n"
"s_cselect_b32 s72,s16,s72\n"
"s_add_u32 m0,s64,49152\n"
MF "a[236:239],v[28:31],v[88:91],a[220:223]\n"
LD "v7,s[32:35],s72 offen nt lds\n"
MF "a[232:235],v[28:31],v[84:87],a[220:223]\n"
"s_add_u32 m0,s64,50048\n"
"v_add_f32 v59,v59,v123\n"
CV "v103,v122,v123\n"
MF "a[212:215],v[24:27],v[84:87],a[220:223]\n"
LD "v7,s[32:35],s72 offen offset:128 nt lds\n"
MF "a[208:211],v[24:27],v[80:83],a[220:223]\n"
"s_add_u32 m0,s64,50944\n"
MF "a[228:231],v[28:31],v[80:83],a[220:223]\n"
LD "v7,s[32:35],s72 offen offset:256 nt lds\n"
MF "a[224:227],v[28:31],v[76:79],a[220:223]\n"
MF "a[204:207],v[24:27],v[76:79],a[220:223]\n"
MF "a[200:203],v[24:27],v[72:75],a[220:223]\n"
MF "a[220:223],v[28:31],v[72:75],a[220:223]\n"
"s_waitcnt lgkmcnt(0)\n"
"s_branch .Lmid\n"
".Laloop:\n"
MF "v[72:75],v[16:19],v[136:139],v[32:35]\n"
"s_nop 0\n"
"ds_read_b128 v[24:27],v0 offset:25600\n"
"ds_read_b128 v[28:31],v1 offset:25600\n"
MF "v[72:75],v[20:23],v[156:159],v[72:75]\n"
MF "v[76:79],v[20:23],v[160:163],v[36:39]\n"
MF "v[76:79],v[16:19],v[140:143],v[76:79]\n"
MF "v[80:83],v[16:19],v[144:147],v[40:43]\n"
MF "v[80:83],v[20:23],v[164:167],v[80:83]\n"
MF "v[84:87],v[20:23],v[168:171],v[44:47]\n"
MF "v[84:87],v[16:19],v[148:151],v[84:87]\n"
"s_add_u32 s72,s72,6144\n"
"s_add_u32 m0,s64,6144\n"
MF "v[88:91],v[16:19],v[152:155],v[48:51]\n"
LD "v7,s[32:35],s72 offen nt lds\n"
MF "v[88:91],v[20:23],v[172:175],v[88:91]\n"
"s_waitcnt lgkmcnt(0)\n"
"ds_read_b128 v[16:19],v0 offset:26624\n"
"ds_read_b128 v[20:23],v1 offset:26624\n"
MF "v[88:91],v[24:27],v[192:195],v[88:91]\n"
"v_exp_f32 v124,v124\n"
MF "v[88:91],v[28:31],v[212:215],v[88:91]\n"
"s_add_u32 m0,s64,7040\n"
"v_exp_f32 v125,v125\n"
MF "v[84:87],v[28:31],v[208:211],v[84:87]\n"
LD "v7,s[32:35],s72 offen offset:128 nt lds\n"
MF "v[84:87],v[24:27],v[188:191],v[84:87]\n"
"v_exp_f32 v126,v126\n"
MF "v[80:83],v[24:27],v[184:187],v[80:83]\n"
"v_exp_f32 v127,v127\n"
MF "v[80:83],v[28:31],v[204:207],v[80:83]\n"
"s_add_u32 m0,s64,7936\n"
"v_add_f32 v60,v60,v124\n"
"v_add_f32 v60,v60,v125\n"
MF "v[76:79],v[28:31],v[200:203],v[76:79]\n"
LD "v7,s[32:35],s72 offen offset:256 nt lds\n"
MF "v[76:79],v[24:27],v[180:183],v[76:79]\n"
CV "v106,v124,v125\n"
"v_add_f32 v60,v60,v126\n"
MF "v[72:75],v[24:27],v[176:179],v[72:75]\n"
"v_add_f32 v60,v60,v127\n"
CV "v107,v126,v127\n"
MF "v[72:75],v[28:31],v[196:199],v[72:75]\n"
"s_waitcnt lgkmcnt(0)\n"
"ds_read_b128 v[24:27],v0 offset:30720\n"
"ds_read_b128 v[28:31],v1 offset:30720\n"
MF "v[72:75],v[16:19],v[216:219],v[72:75]\n"
"v_exp_f32 v128,v128\n"
MF "v[72:75],v[20:23],v[236:239],v[72:75]\n"
"v_exp_f32 v129,v129\n"
MF "v[76:79],v[20:23],v[240:243],v[76:79]\n"
"v_exp_f32 v130,v130\n"
MF "v[76:79],v[16:19],v[220:223],v[76:79]\n"
"v_exp_f32 v131,v131\n"
MF "v[80:83],v[16:19],v[224:227],v[80:83]\n"
"v_add_f32 v61,v61,v128\n"
"v_add_f32 v61,v61,v129\n"
MF "v[80:83],v[20:23],v[244:247],v[80:83]\n"
CV "v110,v128,v129\n"
"v_add_f32 v61,v61,v130\n"
MF "v[84:87],v[20:23],v[248:251],v[84:87]\n"
"v_add_f32 v61,v61,v131\n"
CV "v111,v130,v131\n"
MF "v[84:87],v[16:19],v[228:231],v[84:87]\n"
"v_max3_f32 v10,v57,v58,v59\n"
"v_max3_f32 v10,v10,v60,v61\n"
MF "v[88:91],v[16:19],v[232:235],v[88:91]\n"
"v_cmp_ngt_f32 vcc,s77,v10\n"
MF "v[88:91],v[20:23],v[252:255],v[88:91]\n"
"s_waitcnt lgkmcnt(0)\n"
"s_cbranch_vccnz .Lstub_11\n"
".Lback_11:\n"
MF "v[112:115],v[24:27],v[136:139],v[32:35]\n"
"s_add_u32 s74,s74,12288\n"
"ds_read_b128 v[16:19],v0 offset:31744\n"
"ds_read_b128 v[20:23],v1 offset:31744\n"
MF "v[112:115],v[28:31],v[156:159],v[112:115]\n"
"s_cmp_eq_u32 s74,s20\n"
"s_cselect_b32 s74,s70,s74\n"
MF "v[116:119],v[28:31],v[160:163],v[36:39]\n"
"v_exp_f32 v72,v72\n"
MF "v[116:119],v[24:27],v[140:143],v[116:119]\n"
"v_exp_f32 v73,v73\n"
MF "v[120:123],v[24:27],v[144:147],v[40:43]\n"
"v_exp_f32 v74,v74\n"
MF "v[120:123],v[28:31],v[164:167],v[120:123]\n"
"v_exp_f32 v75,v75\n"
MF "v[124:127],v[28:31],v[168:171],v[44:47]\n"
"v_add_f32 v52,v57,v72\n"
"v_add_f32 v52,v52,v73\n"
MF "v[124:127],v[24:27],v[148:151],v[124:127]\n"
CV "v72,v72,v73\n"
"v_add_f32 v52,v52,v74\n"
MF "v[128:131],v[24:27],v[152:155],v[48:51]\n"
"v_add_f32 v52,v52,v75\n"
CV "v73,v74,v75\n"
MF "v[128:131],v[28:31],v[172:175],v[128:131]\n"
"s_waitcnt lgkmcnt(0)\n"
"ds_read_b128 v[24:27],v0 offset:32768\n"
"ds_read_b128 v[28:31],v1 offset:32768\n"
MF "v[128:131],v[16:19],v[192:195],v[128:131]\n"
"v_exp_f32 v76,v76\n"
MF "v[128:131],v[20:23],v[212:215],v[128:131]\n"
"v_exp_f32 v77,v77\n"
MF "v[124:127],v[20:23],v[208:211],v[124:127]\n"
"v_exp_f32 v78,v78\n"
MF "v[124:127],v[16:19],v[188:191],v[124:127]\n"
"v_exp_f32 v79,v79\n"
MF "v[120:123],v[16:19],v[184:187],v[120:123]\n"
"v_add_f32 v53,v58,v76\n"
"v_add_f32 v53,v53,v77\n"
MF "v[120:123],v[20:23],v[204:207],v[120:123]\n"
CV "v76,v76,v77\n"
"v_add_f32 v53,v53,v78\n"
MF "v[116:119],v[20:23],v[200:203],v[116:119]\n"
"v_add_f32 v53,v53,v79\n"
CV "v77,v78,v79\n"
MF "v[116:119],v[16:19],v[180:183],v[116:119]\n"
"s_waitcnt vmcnt(6) lgkmcnt(0)\n"
"s_barrier\n"
MF "v[112:115],v[16:19],v[176:179],v[112:115]\n"
"v_exp_f32 v80,v80\n"
MF "v[112:115],v[20:23],v[196:199],v[112:115]\n"
"s_nop 0\n"
TR "v[16:17],v2 offset:49152\n"
TR "v[18:19],v2 offset:55296\n"
MF "v[112:115],v[24:27],v[216:219],v[112:115]\n"
"s_add_u32 s73,s73,12288\n"
TR "v[20:21],v3 offset:49152\n"
TR "v[22:23],v3 offset:55296\n"
MF "v[112:115],v[28:31],v[236:239],v[112:115]\n"
"s_cmp_eq_u32 s73,s19\n"
"s_cselect_b32 s73,s18,s73\n"
"s_add_u32 m0,s65,24576\n"
MF "v[116:119],v[28:31],v[240:243],v[116:119]\n"
LD "v8,s[36:39],s73 offen nt lds\n"
MF "v[116:119],v[24:27],v[220:223],v[116:119]\n"
"s_add_u32 m0,s65,25472\n"
"v_exp_f32 v81,v81\n"
MF "v[120:123],v[24:27],v[224:227],v[120:123]\n"
LD "v8,s[36:39],s73 offen offset:128 nt lds\n"
MF "v[120:123],v[28:31],v[244:247],v[120:123]\n"
"v_exp_f32 v82,v82\n"
MF "v[124:127],v[28:31],v[248:251],v[124:127]\n"
"s_add_u32 m0,s65,26368\n"
"v_exp_f32 v83,v83\n"
MF "v[124:127],v[24:27],v[228:231],v[124:127]\n"
LD "v8,s[36:39],s73 offen offset:256 nt lds\n"
MF "v[128:131],v[24:27],v[232:235],v[128:131]\n"
"v_add_f32 v54,v59,v80\n"
"v_add_f32 v54,v54,v81\n"
MF "v[128:131],v[28:31],v[252:255],v[128:131]\n"
"s_waitcnt lgkmcnt(0)\n"
TR "v[24:25],v4 offset:49152\n"
TR "v[26:27],v4 offset:55296\n"
MF "a[0:3],v[16:19],v[92:95],a[0:3]\n"
"s_add_u32 m0,s65,24576\n"
TR "v[28:29],v5 offset:49152\n"
TR "v[30:31],v5 offset:55296\n"
MF "a[20:23],v[20:23],v[92:95],a[20:23]\n"
LD "v8,s[36:39],s73 offen offset:3072 nt lds\n"
MF "a[24:27],v[20:23],v[96:99],a[24:27]\n"
CV "v80,v80,v81\n"
"v_add_f32 v54,v54,v82\n"
MF "a[4:7],v[16:19],v[96:99],a[4:7]\n"
"s_add_u32 m0,s65,25472\n"
"v_add_f32 v54,v54,v83\n"
CV "v81,v82,v83\n"
MF "a[8:11],v[16:19],v[100:103],a[8:11]\n"
LD "v8,s[36:39],s73 offen offset:3200 nt lds\n"
MF "a[28:31],v[20:23],v[100:103],a[28:31]\n"
"v_exp_f32 v84,v84\n"
MF "a[32:35],v[20:23],v[104:107],a[32:35]\n"
"s_add_u32 m0,s65,26368\n"
"v_exp_f32 v85,v85\n"
MF "a[12:15],v[16:19],v[104:107],a[12:15]\n"
LD "v8,s[36:39],s73 offen offset:3328 nt lds\n"
MF "a[16:19],v[16:19],v[108:111],a[16:19]\n"
"v_exp_f32 v86,v86\n"
MF "a[36:39],v[20:23],v[108:111],a[36:39]\n"
"s_waitcnt lgkmcnt(0)\n"
TR "v[16:17],v2 offset:50176\n"
TR "v[18:19],v2 offset:56320\n"
MF "a[56:59],v[24:27],v[108:111],a[56:59]\n"
"s_nop 0\n"
TR "v[20:21],v3 offset:50176\n"
TR "v[22:23],v3 offset:56320\n"
MF "a[76:79],v[28:31],v[108:111],a[76:79]\n"
"v_exp_f32 v87,v87\n"
MF "a[72:75],v[28:31],v[104:107],a[72:75]\n"
"v_add_f32 v55,v60,v84\n"
"v_add_f32 v55,v55,v85\n"
MF "a[52:55],v[24:27],v[104:107],a[52:55]\n"
CV "v84,v84,v85\n"
"v_add_f32 v55,v55,v86\n"
MF "a[48:51],v[24:27],v[100:103],a[48:51]\n"
"v_add_f32 v55,v55,v87\n"
CV "v85,v86,v87\n"
MF "a[68:71],v[28:31],v[100:103],a[68:71]\n"
"v_exp_f32 v88,v88\n"
MF "a[64:67],v[28:31],v[96:99],a[64:67]\n"
"v_exp_f32 v89,v89\n"
MF "a[44:47],v[24:27],v[96:99],a[44:47]\n"
"v_exp_f32 v90,v90\n"
MF "a[40:43],v[24:27],v[92:95],a[40:43]\n"
"v_exp_f32 v91,v91\n"
MF "a[60:63],v[28:31],v[92:95],a[60:63]\n"
"s_waitcnt lgkmcnt(0)\n"
TR "v[24:25],v4 offset:50176\n"
TR "v[26:27],v4 offset:56320\n"
MF "a[80:83],v[16:19],v[92:95],a[80:83]\n"
"s_nop 0\n"
TR "v[28:29],v5 offset:50176\n"
TR "v[30:31],v5 offset:56320\n"
MF "a[100:103],v[20:23],v[92:95],a[100:103]\n"
"v_add_f32 v56,v61,v88\n"
"v_add_f32 v56,v56,v89\n"
MF "a[104:107],v[20:23],v[96:99],a[104:107]\n"
CV "v88,v88,v89\n"
"v_add_f32 v56,v56,v90\n"
MF "a[84:87],v[16:19],v[96:99],a[84:87]\n"
"v_add_f32 v56,v56,v91\n"
CV "v89,v90,v91\n"
MF "a[88:91],v[16:19],v[100:103],a[88:91]\n"
"v_exp_f32 v112,v112\n"
MF "a[108:111],v[20:23],v[100:103],a[108:111]\n"
"v_exp_f32 v113,v113\n"
MF "a[112:115],v[20:23],v[104:107],a[112:115]\n"
"v_exp_f32 v114,v114\n"
MF "a[92:95],v[16:19],v[104:107],a[92:95]\n"
"v_exp_f32 v115,v115\n"
MF "a[96:99],v[16:19],v[108:111],a[96:99]\n"
"v_add_f32 v52,v52,v112\n"
"v_add_f32 v52,v52,v113\n"
MF "a[116:119],v[20:23],v[108:111],a[116:119]\n"
"s_waitcnt lgkmcnt(0)\n"
TR "v[16:17],v2 offset:51200\n"
TR "v[18:19],v2 offset:57344\n"
MF "a[136:139],v[24:27],v[108:111],a[136:139]\n"
"s_nop 0\n"
TR "v[20:21],v3 offset:51200\n"
TR "v[22:23],v3 offset:57344\n"
MF "a[156:159],v[28:31],v[108:111],a[156:159]\n"
CV "v74,v112,v113\n"
"v_add_f32 v52,v52,v114\n"
MF "a[152:155],v[28:31],v[104:107],a[152:155]\n"
"v_add_f32 v52,v52,v115\n"
CV "v75,v114,v115\n"
MF "a[132:135],v[24:27],v[104:107],a[132:135]\n"
"v_exp_f32 v116,v116\n"
MF "a[128:131],v[24:27],v[100:103],a[128:131]\n"
"v_exp_f32 v117,v117\n"
MF "a[148:151],v[28:31],v[100:103],a[148:151]\n"
"v_exp_f32 v118,v118\n"
MF "a[144:147],v[28:31],v[96:99],a[144:147]\n"
"v_exp_f32 v119,v119\n"
MF "a[124:127],v[24:27],v[96:99],a[124:127]\n"
"v_add_f32 v53,v53,v116\n"
"v_add_f32 v53,v53,v117\n"
MF "a[120:123],v[24:27],v[92:95],a[120:123]\n"
CV "v78,v116,v117\n"
"v_add_f32 v53,v53,v118\n"
MF "a[140:143],v[28:31],v[92:95],a[140:143]\n"
"s_waitcnt lgkmcnt(0)\n"
TR "v[24:25],v4 offset:51200\n"
TR "v[26:27],v4 offset:57344\n"
MF "a[160:163],v[16:19],v[92:95],a[160:163]\n"
"s_nop 0\n"
TR "v[28:29],v5 offset:51200\n"
TR "v[30:31],v5 offset:57344\n"
MF "a[180:183],v[20:23],v[92:95],a[180:183]\n"
"v_add_f32 v53,v53,v119\n"
CV "v79,v118,v119\n"
MF "a[184:187],v[20:23],v[96:99],a[184:187]\n"
"v_exp_f32 v120,v120\n"
MF "a[164:167],v[16:19],v[96:99],a[164:167]\n"
"v_exp_f32 v121,v121\n"
MF "a[168:171],v[16:19],v[100:103],a[168:171]\n"
"v_exp_f32 v122,v122\n"
MF "a[188:191],v[20:23],v[100:103],a[188:191]\n"
"v_exp_f32 v123,v123\n"
MF "a[192:195],v[20:23],v[104:107],a[192:195]\n"
"v_add_f32 v54,v54,v120\n"
"v_add_f32 v54,v54,v121\n"
MF "a[172:175],v[16:19],v[104:107],a[172:175]\n"
CV "v82,v120,v121\n"
"v_add_f32 v54,v54,v122\n"
MF "a[176:179],v[16:19],v[108:111],a[176:179]\n"
"s_waitcnt vmcnt(6) lgkmcnt(0)\n"
"s_barrier\n"
MF "a[196:199],v[20:23],v[108:111],a[196:199]\n"
"s_add_u32 s72,s72,6144\n"
"ds_read_b128 v[16:19],v0\n"
"ds_read_b128 v[20:23],v1\n"
MF "a[216:219],v[24:27],v[108:111],a[216:219]\n"
"s_cmp_eq_u32 s72,s17\n"
"s_cselect_b32 s72,s16,s72\n"
"s_add_u32 m0,s64,49152\n"
MF "a[236:239],v[28:31],v[108:111],a[236:239]\n"
LD "v7,s[32:35],s72 offen nt lds\n"
MF "a[232:235],v[28:31],v[104:107],a[232:235]\n"
"s_add_u32 m0,s64,50048\n"
"v_add_f32 v54,v54,v123\n"
CV "v83,v122,v123\n"
MF "a[212:215],v[24:27],v[104:107],a[212:215]\n"
LD "v7,s[32:35],s72 offen offset:128 nt lds\n"
MF "a[208:211],v[24:27],v[100:103],a[208:211]\n"
"s_add_u32 m0,s64,50944\n"
MF "a[228:231],v[28:31],v[100:103],a[228:231]\n"
LD "v7,s[32:35],s72 offen offset:256 nt lds\n"
MF "a[224:227],v[28:31],v[96:99],a[224:227]\n"
MF "a[204:207],v[24:27],v[96:99],a[204:207]\n"
MF "a[200:203],v[24:27],v[92:95],a[200:203]\n"
MF "a[220:223],v[28:31],v[92:95],a[220:223]\n"
"s_waitcnt lgkmcnt(0)\n"
MF "v[92:95],v[16:19],v[136:139],v[32:35]\n"
"s_add_u32 s72,s72,6144\n"
"ds_read_b128 v[24:27],v0 offset:1024\n"
"ds_read_b128 v[28:31],v1 offset:1024\n"
MF "v[92:95],v[20:23],v[156:159],v[92:95]\n"
"s_add_u32 m0,s64,55296\n"
"v_exp_f32 v124,v124\n"
MF "v[96:99],v[20:23],v[160:163],v[36:39]\n"
LD "v7,s[32:35],s72 offen nt lds\n"
MF "v[96:99],v[16:19],v[140:143],v[96:99]\n"
"v_exp_f32 v125,v125\n"
MF "v[100:103],v[16:19],v[144:147],v[40:43]\n"
"s_add_u32 m0,s64,56192\n"
"v_exp_f32 v126,v126\n"
MF "v[100:103],v[20:23],v[164:167],v[100:103]\n"
LD "v7,s[32:35],s72 offen offset:128 nt lds\n"
MF "v[104:107],v[20:23],v[168:171],v[44:47]\n"
"v_exp_f32 v127,v127\n"
MF "v[104:107],v[16:19],v[148:151],v[104:107]\n"
"s_add_u32 m0,s64,57088\n"
"v_add_f32 v55,v55,v124\n"
"v_add_f32 v55,v55,v125\n"
MF "v[108:111],v[16:19],v[152:155],v[48:51]\n"
LD "v7,s[32:35],s72 offen offset:256 nt lds\n"
MF "v[108:111],v[20:23],v[172:175],v[108:111]\n"
"s_waitcnt lgkmcnt(0)\n"
"ds_read_b128 v[16:19],v0 offset:2048\n"
"ds_read_b128 v[20:23],v1 offset:2048\n"
MF "v[108:111],v[24:27],v[192:195],v[108:111]\n"
CV "v86,v124,v125\n"
"v_add_f32 v55,v55,v126\n"
MF "v[108:111],v[28:31],v[212:215],v[108:111]\n"
"v_add_f32 v55,v55,v127\n"
CV "v87,v126,v127\n"
MF "v[104:107],v[28:31],v[208:211],v[104:107]\n"
"v_exp_f32 v128,v128\n"
MF "v[104:107],v[24:27],v[188:191],v[104:107]\n"
"v_exp_f32 v129,v129\n"
MF "v[100:103],v[24:27],v[184:187],v[100:103]\n"
"v_exp_f32 v130,v130\n"
MF "v[100:103],v[28:31],v[204:207],v[100:103]\n"
"v_exp_f32 v131,v131\n"
MF "v[96:99],v[28:31],v[200:203],v[96:99]\n"
"v_add_f32 v56,v56,v128\n"
"v_add_f32 v56,v56,v129\n"
MF "v[96:99],v[24:27],v[180:183],v[96:99]\n"
CV "v90,v128,v129\n"
"v_add_f32 v56,v56,v130\n"
MF "v[92:95],v[24:27],v[176:179],v[92:95]\n"
"v_add_f32 v56,v56,v131\n"
CV "v91,v130,v131\n"
MF "v[92:95],v[28:31],v[196:199],v[92:95]\n"
"s_waitcnt lgkmcnt(0)\n"
"ds_read_b128 v[24:27],v0 offset:6144\n"
"ds_read_b128 v[28:31],v1 offset:6144\n"
MF "v[92:95],v[16:19],v[216:219],v[92:95]\n"
MF "v[92:95],v[20:23],v[236:239],v[92:95]\n"
MF "v[96:99],v[20:23],v[240:243],v[96:99]\n"
MF "v[96:99],v[16:19],v[220:223],v[96:99]\n"
MF "v[100:103],v[16:19],v[224:227],v[100:103]\n"
MF "v[100:103],v[20:23],v[244:247],v[100:103]\n"
MF "v[104:107],v[20:23],v[248:251],v[104:107]\n"
MF "v[104:107],v[16:19],v[228:231],v[104:107]\n"
"v_max3_f32 v10,v52,v53,v54\n"
"v_max3_f32 v10,v10,v55,v56\n"
MF "v[108:111],v[16:19],v[232:235],v[108:111]\n"
"v_cmp_ngt_f32 vcc,s77,v10\n"
MF "v[108:111],v[20:23],v[252:255],v[108:111]\n"
"s_waitcnt lgkmcnt(0)\n"
"s_cbranch_vccnz .Lstub_12\n"
".Lback_12:\n"
MF "v[112:115],v[24:27],v[136:139],v[32:35]\n"
"s_add_u32 s74,s74,12288\n"
"ds_read_b128 v[16:19],v0 offset:7168\n"
"ds_read_b128 v[20:23],v1 offset:7168\n"
MF "v[112:115],v[28:31],v[156:159],v[112:115]\n"
"s_cmp_eq_u32 s74,s20\n"
"s_cselect_b32 s74,s70,s74\n"
MF "v[116:119],v[28:31],v[160:163],v[36:39]\n"
"v_exp_f32 v92,v92\n"
MF "v[116:119],v[24:27],v[140:143],v[116:119]\n"
"v_exp_f32 v93,v93\n"
MF "v[120:123],v[24:27],v[144:147],v[40:43]\n"
"v_exp_f32 v94,v94\n"
MF "v[120:123],v[28:31],v[164:167],v[120:123]\n"
"v_exp_f32 v95,v95\n"
MF "v[124:127],v[28:31],v[168:171],v[44:47]\n"
"v_add_f32 v57,v52,v92\n"
"v_add_f32 v57,v57,v93\n"
MF "v[124:127],v[24:27],v[148:151],v[124:127]\n"
CV "v92,v92,v93\n"
"v_add_f32 v57,v57,v94\n"
MF "v[128:131],v[24:27],v[152:155],v[48:51]\n"
"v_add_f32 v57,v57,v95\n"
CV "v93,v94,v95\n"
MF "v[128:131],v[28:31],v[172:175],v[128:131]\n"
"s_waitcnt lgkmcnt(0)\n"
"ds_read_b128 v[24:27],v0 offset:8192\n"
"ds_read_b128 v[28:31],v1 offset:8192\n"
MF "v[128:131],v[16:19],v[192:195],v[128:131]\n"
"v_exp_f32 v96,v96\n"
MF "v[128:131],v[20:23],v[212:215],v[128:131]\n"
"v_exp_f32 v97,v97\n"
MF "v[124:127],v[20:23],v[208:211],v[124:127]\n"
"v_exp_f32 v98,v98\n"
MF "v[124:127],v[16:19],v[188:191],v[124:127]\n"
"v_exp_f32 v99,v99\n"
MF "v[120:123],v[16:19],v[184:187],v[120:123]\n"
"v_add_f32 v58,v53,v96\n"
"v_add_f32 v58,v58,v97\n"
MF "v[120:123],v[20:23],v[204:207],v[120:123]\n"
CV "v96,v96,v97\n"
"v_add_f32 v58,v58,v98\n"
MF "v[116:119],v[20:23],v[200:203],v[116:119]\n"
"v_add_f32 v58,v58,v99\n"
CV "v97,v98,v99\n"
MF "v[116:119],v[16:19],v[180:183],v[116:119]\n"
"s_waitcnt vmcnt(6) lgkmcnt(0)\n"
"s_barrier\n"
MF "v[112:115],v[16:19],v[176:179],v[112:115]\n"
"v_exp_f32 v100,v100\n"
MF "v[112:115],v[20:23],v[196:199],v[112:115]\n"
"s_nop 0\n"
TR "v[16:17],v2 offset:24576\n"
TR "v[18:19],v2 offset:30720\n"
MF "v[112:115],v[24:27],v[216:219],v[112:115]\n"
"s_add_u32 s73,s73,12288\n"
TR "v[20:21],v3 offset:24576\n"
TR "v[22:23],v3 offset:30720\n"
MF "v[112:115],v[28:31],v[236:239],v[112:115]\n"
"s_cmp_eq_u32 s73,s19\n"
"s_cselect_b32 s73,s18,s73\n"
"s_add_u32 m0,s65,0\n"
MF "v[116:119],v[28:31],v[240:243],v[116:119]\n"
LD "v8,s[36:39],s73 offen nt lds\n"
MF "v[116:119],v[24:27],v[220:223],v[116:119]\n"
"s_add_u32 m0,s65,896\n"
"v_exp_f32 v101,v101\n"
MF "v[120:123],v[24:27],v[224:227],v[120:123]\n"
LD "v8,s[36:39],s73 offen offset:128 nt lds\n"
MF "v[120:123],v[28:31],v[244:247],v[120:123]\n"
"v_exp_f32 v102,v102\n"
MF "v[124:127],v[28:31],v[248:251],v[124:127]\n"
"s_add_u32 m0,s65,1792\n"
"v_exp_f32 v103,v103\n"
MF "v[124:127],v[24:27],v[228:231],v[124:127]\n"
LD "v8,s[36:39],s73 offen offset:256 nt lds\n"
MF "v[128:131],v[24:27],v[232:235],v[128:131]\n"
"v_add_f32 v59,v54,v100\n"
"v_add_f32 v59,v59,v101\n"
MF "v[128:131],v[28:31],v[252:255],v[128:131]\n"
"s_waitcnt lgkmcnt(0)\n"
TR "v[24:25],v4 offset:24576\n"
TR "v[26:27],v4 offset:30720\n"
MF "a[0:3],v[16:19],v[72:75],a[0:3]\n"
"s_add_u32 m0,s65,0\n"
TR "v[28:29],v5 offset:24576\n"
TR "v[30:31],v5 offset:30720\n"
MF "a[20:23],v[20:23],v[72:75],a[20:23]\n"
LD "v8,s[36:39],s73 offen offset:3072 nt lds\n"
MF "a[24:27],v[20:23],v[76:79],a[24:27]\n"
CV "v100,v100,v101\n"
"v_add_f32 v59,v59,v102\n"
MF "a[4:7],v[16:19],v[76:79],a[4:7]\n"
"s_add_u32 m0,s65,896\n"
"v_add_f32 v59,v59,v103\n"
CV "v101,v102,v103\n"
MF "a[8:11],v[16:19],v[80:83],a[8:11]\n"
LD "v8,s[36:39],s73 offen offset:3200 nt lds\n"
MF "a[28:31],v[20:23],v[80:83],a[28:31]\n"
"v_exp_f32 v104,v104\n"
MF "a[32:35],v[20:23],v[84:87],a[32:35]\n"
"s_add_u32 m0,s65,1792\n"
"v_exp_f32 v105,v105\n"
MF "a[12:15],v[16:19],v[84:87],a[12:15]\n"
LD "v8,s[36:39],s73 offen offset:3328 nt lds\n"
MF "a[16:19],v[16:19],v[88:91],a[16:19]\n"
"v_exp_f32 v106,v106\n"
MF "a[36:39],v[20:23],v[88:91],a[36:39]\n"
"s_waitcnt lgkmcnt(0)\n"
TR "v[16:17],v2 offset:25600\n"
TR "v[18:19],v2 offset:31744\n"
MF "a[56:59],v[24:27],v[88:91],a[56:59]\n"
"s_nop 0\n"
TR "v[20:21],v3 offset:25600\n"
TR "v[22:23],v3 offset:31744\n"
MF "a[76:79],v[28:31],v[88:91],a[76:79]\n"
"v_exp_f32 v107,v107\n"
MF "a[72:75],v[28:31],v[84:87],a[72:75]\n"
"v_add_f32 v60,v55,v104\n"
"v_add_f32 v60,v60,v105\n"
MF "a[52:55],v[24:27],v[84:87],a[52:55]\n"
CV "v104,v104,v105\n"
"v_add_f32 v60,v60,v106\n"
MF "a[48:51],v[24:27],v[80:83],a[48:51]\n"
"v_add_f32 v60,v60,v107\n"
CV "v105,v106,v107\n"
MF "a[68:71],v[28:31],v[80:83],a[68:71]\n"
"v_exp_f32 v108,v108\n"
MF "a[64:67],v[28:31],v[76:79],a[64:67]\n"
"v_exp_f32 v109,v109\n"
MF "a[44:47],v[24:27],v[76:79],a[44:47]\n"
"v_exp_f32 v110,v110\n"
MF "a[40:43],v[24:27],v[72:75],a[40:43]\n"
"v_exp_f32 v111,v111\n"
MF "a[60:63],v[28:31],v[72:75],a[60:63]\n"
"s_waitcnt lgkmcnt(0)\n"
TR "v[24:25],v4 offset:25600\n"
TR "v[26:27],v4 offset:31744\n"
MF "a[80:83],v[16:19],v[72:75],a[80:83]\n"
"s_nop 0\n"
TR "v[28:29],v5 offset:25600\n"
TR "v[30:31],v5 offset:31744\n"
MF "a[100:103],v[20:23],v[72:75],a[100:103]\n"
"v_add_f32 v61,v56,v108\n"
"v_add_f32 v61,v61,v109\n"
MF "a[104:107],v[20:23],v[76:79],a[104:107]\n"
CV "v108,v108,v109\n"
"v_add_f32 v61,v61,v110\n"
MF "a[84:87],v[16:19],v[76:79],a[84:87]\n"
"v_add_f32 v61,v61,v111\n"
CV "v109,v110,v111\n"
MF "a[88:91],v[16:19],v[80:83],a[88:91]\n"
"v_exp_f32 v112,v112\n"
MF "a[108:111],v[20:23],v[80:83],a[108:111]\n"
"v_exp_f32 v113,v113\n"
MF "a[112:115],v[20:23],v[84:87],a[112:115]\n"
"v_exp_f32 v114,v114\n"
MF "a[92:95],v[16:19],v[84:87],a[92:95]\n"
"v_exp_f32 v115,v115\n"
MF "a[96:99],v[16:19],v[88:91],a[96:99]\n"
"v_add_f32 v57,v57,v112\n"
"v_add_f32 v57,v57,v113\n"
MF "a[116:119],v[20:23],v[88:91],a[116:119]\n"
"s_waitcnt lgkmcnt(0)\n"
TR "v[16:17],v2 offset:26624\n"
TR "v[18:19],v2 offset:32768\n"
MF "a[136:139],v[24:27],v[88:91],a[136:139]\n"
"s_nop 0\n"
TR "v[20:21],v3 offset:26624\n"
TR "v[22:23],v3 offset:32768\n"
MF "a[156:159],v[28:31],v[88:91],a[156:159]\n"
CV "v94,v112,v113\n"
"v_add_f32 v57,v57,v114\n"
MF "a[152:155],v[28:31],v[84:87],a[152:155]\n"
"v_add_f32 v57,v57,v115\n"
CV "v95,v114,v115\n"
MF "a[132:135],v[24:27],v[84:87],a[132:135]\n"
"v_exp_f32 v116,v116\n"
MF "a[128:131],v[24:27],v[80:83],a[128:131]\n"
"v_exp_f32 v117,v117\n"
MF "a[148:151],v[28:31],v[80:83],a[148:151]\n"
"v_exp_f32 v118,v118\n"
MF "a[144:147],v[28:31],v[76:79],a[144:147]\n"
"v_exp_f32 v119,v119\n"
MF "a[124:127],v[24:27],v[76:79],a[124:127]\n"
"v_add_f32 v58,v58,v116\n"
"v_add_f32 v58,v58,v117\n"
MF "a[120:123],v[24:27],v[72:75],a[120:123]\n"
CV "v98,v116,v117\n"
"v_add_f32 v58,v58,v118\n"
MF "a[140:143],v[28:31],v[72:75],a[140:143]\n"
"s_waitcnt lgkmcnt(0)\n"
TR "v[24:25],v4 offset:26624\n"
TR "v[26:27],v4 offset:32768\n"
MF "a[160:163],v[16:19],v[72:75],a[160:163]\n"
"s_nop 0\n"
TR "v[28:29],v5 offset:26624\n"
TR "v[30:31],v5 offset:32768\n"
MF "a[180:183],v[20:23],v[72:75],a[180:183]\n"
"v_add_f32 v58,v58,v119\n"
CV "v99,v118,v119\n"
MF "a[184:187],v[20:23],v[76:79],a[184:187]\n"
"v_exp_f32 v120,v120\n"
MF "a[164:167],v[16:19],v[76:79],a[164:167]\n"
"v_exp_f32 v121,v121\n"
MF "a[168:171],v[16:19],v[80:83],a[168:171]\n"
"v_exp_f32 v122,v122\n"
MF "a[188:191],v[20:23],v[80:83],a[188:191]\n"
"v_exp_f32 v123,v123\n"
MF "a[192:195],v[20:23],v[84:87],a[192:195]\n"
"v_add_f32 v59,v59,v120\n"
"v_add_f32 v59,v59,v121\n"
MF "a[172:175],v[16:19],v[84:87],a[172:175]\n"
CV "v102,v120,v121\n"
"v_add_f32 v59,v59,v122\n"
MF "a[176:179],v[16:19],v[88:91],a[176:179]\n"
"s_waitcnt vmcnt(6) lgkmcnt(0)\n"
"s_barrier\n"
MF "a[196:199],v[20:23],v[88:91],a[196:199]\n"
"s_add_u32 s72,s72,6144\n"
"ds_read_b128 v[16:19],v0 offset:49152\n"
"ds_read_b128 v[20:23],v1 offset:49152\n"
MF "a[216:219],v[24:27],v[88:91],a[216:219]\n"
"s_cmp_eq_u32 s72,s17\n"
"s_cselect_b32 s72,s16,s72\n"
"s_add_u32 m0,s64,24576\n"
MF "a[236:239],v[28:31],v[88:91],a[236:239]\n"
LD "v7,s[32:35],s72 offen nt lds\n"
MF "a[232:235],v[28:31],v[84:87],a[232:235]\n"
"s_add_u32 m0,s64,25472\n"
"v_add_f32 v59,v59,v123\n"
CV "v103,v122,v123\n"
MF "a[212:215],v[24:27],v[84:87],a[212:215]\n"
LD "v7,s[32:35],s72 offen offset:128 nt lds\n"
MF "a[208:211],v[24:27],v[80:83],a[208:211]\n"
"s_add_u32 m0,s64,26368\n"
MF "a[228:231],v[28:31],v[80:83],a[228:231]\n"
LD "v7,s[32:35],s72 offen offset:256 nt lds\n"
MF "a[224:227],v[28:31],v[76:79],a[224:227]\n"
MF "a[204:207],v[24:27],v[76:79],a[204:207]\n"
MF "a[200:203],v[24:27],v[72:75],a[200:203]\n"
MF "a[220:223],v[28:31],v[72:75],a[220:223]\n"
"s_waitcnt lgkmcnt(0)\n"
MF "v[72:75],v[16:19],v[136:139],v[32:35]\n"
"s_add_u32 s72,s72,6144\n"
"ds_read_b128 v[24:27],v0 offset:50176\n"
"ds_read_b128 v[28:31],v1 offset:50176\n"
MF "v[72:75],v[20:23],v[156:159],v[72:75]\n"
"s_add_u32 m0,s64,30720\n"
"v_exp_f32 v124,v124\n"
MF "v[76:79],v[20:23],v[160:163],v[36:39]\n"
LD "v7,s[32:35],s72 offen nt lds\n"
MF "v[76:79],v[16:19],v[140:143],v[76:79]\n"
"v_exp_f32 v125,v125\n"
MF "v[80:83],v[16:19],v[144:147],v[40:43]\n"
"s_add_u32 m0,s64,31616\n"
"v_exp_f32 v126,v126\n"
MF "v[80:83],v[20:23],v[164:167],v[80:83]\n"
LD "v7,s[32:35],s72 offen offset:128 nt lds\n"
MF "v[84:87],v[20:23],v[168:171],v[44:47]\n"
"v_exp_f32 v127,v127\n"
MF "v[84:87],v[16:19],v[148:151],v[84:87]\n"
"s_add_u32 m0,s64,32512\n"
"v_add_f32 v60,v60,v124\n"
"v_add_f32 v60,v60,v125\n"
MF "v[88:91],v[16:19],v[152:155],v[48:51]\n"
LD "v7,s[32:35],s72 offen offset:256 nt lds\n"
MF "v[88:91],v[20:23],v[172:175],v[88:91]\n"
"s_waitcnt lgkmcnt(0)\n"
"ds_read_b128 v[16:19],v0 offset:51200\n"
"ds_read_b128 v[20:23],v1 offset:51200\n"
MF "v[88:91],v[24:27],v[192:195],v[88:91]\n"
CV "v106,v124,v125\n"
"v_add_f32 v60,v60,v126\n"
MF "v[88:91],v[28:31],v[212:215],v[88:91]\n"
"v_add_f32 v60,v60,v127\n"
CV "v107,v126,v127\n"
MF "v[84:87],v[28:31],v[208:211],v[84:87]\n"
"v_exp_f32 v128,v128\n"
MF "v[84:87],v[24:27],v[188:191],v[84:87]\n"
"v_exp_f32 v129,v129\n"
MF "v[80:83],v[24:27],v[184:187],v[80:83]\n"
"v_exp_f32 v130,v130\n"
MF "v[80:83],v[28:31],v[204:207],v[80:83]\n"
"v_exp_f32 v131,v131\n"
MF "v[76:79],v[28:31],v[200:203],v[76:79]\n"
"v_add_f32 v61,v61,v128\n"
"v_add_f32 v61,v61,v129\n"
MF "v[76:79],v[24:27],v[180:183],v[76:79]\n"
CV "v110,v128,v129\n"
"v_add_f32 v61,v61,v130\n"
MF "v[72:75],v[24:27],v[176:179],v[72:75]\n"
"v_add_f32 v61,v61,v131\n"
CV "v111,v130,v131\n"
MF "v[72:75],v[28:31],v[196:199],v[72:75]\n"
"s_waitcnt lgkmcnt(0)\n"
"ds_read_b128 v[24:27],v0 offset:55296\n"
"ds_read_b128 v[28:31],v1 offset:55296\n"
MF "v[72:75],v[16:19],v[216:219],v[72:75]\n"
MF "v[72:75],v[20:23],v[236:239],v[72:75]\n"
MF "v[76:79],v[20:23],v[240:243],v[76:79]\n"
MF "v[76:79],v[16:19],v[220:223],v[76:79]\n"
MF "v[80:83],v[16:19],v[224:227],v[80:83]\n"
MF "v[80:83],v[20:23],v[244:247],v[80:83]\n"
MF "v[84:87],v[20:23],v[248:251],v[84:87]\n"
MF "v[84:87],v[16:19],v[228:231],v[84:87]\n"
"v_max3_f32 v10,v57,v58,v59\n"
"v_max3_f32 v10,v10,v60,v61\n"
MF "v[88:91],v[16:19],v[232:235],v[88:91]\n"
"v_cmp_ngt_f32 vcc,s95,v10\n"
MF "v[88:91],v[20:23],v[252:255],v[88:91]\n"
"s_waitcnt lgkmcnt(0)\n"
"s_cbranch_vccnz .Lstub_13\n"
".Lback_13:\n"
MF "v[112:115],v[24:27],v[136:139],v[32:35]\n"
"s_add_u32 s74,s74,12288\n"
"ds_read_b128 v[16:19],v0 offset:56320\n"
"ds_read_b128 v[20:23],v1 offset:56320\n"
MF "v[112:115],v[28:31],v[156:159],v[112:115]\n"
"s_cmp_eq_u32 s74,s20\n"
"s_cselect_b32 s74,s70,s74\n"
MF "v[116:119],v[28:31],v[160:163],v[36:39]\n"
"v_exp_f32 v72,v72\n"
MF "v[116:119],v[24:27],v[140:143],v[116:119]\n"
"v_exp_f32 v73,v73\n"
MF "v[120:123],v[24:27],v[144:147],v[40:43]\n"
"v_exp_f32 v74,v74\n"
MF "v[120:123],v[28:31],v[164:167],v[120:123]\n"
"v_exp_f32 v75,v75\n"
MF "v[124:127],v[28:31],v[168:171],v[44:47]\n"
"v_add_f32 v52,v57,v72\n"
"v_add_f32 v52,v52,v73\n"
MF "v[124:127],v[24:27],v[148:151],v[124:127]\n"
CV "v72,v72,v73\n"
"v_add_f32 v52,v52,v74\n"
MF "v[128:131],v[24:27],v[152:155],v[48:51]\n"
"v_add_f32 v52,v52,v75\n"
CV "v73,v74,v75\n"
MF "v[128:131],v[28:31],v[172:175],v[128:131]\n"
"s_waitcnt lgkmcnt(0)\n"
"ds_read_b128 v[24:27],v0 offset:57344\n"
"ds_read_b128 v[28:31],v1 offset:57344\n"
MF "v[128:131],v[16:19],v[192:195],v[128:131]\n"
"v_exp_f32 v76,v76\n"
MF "v[128:131],v[20:23],v[212:215],v[128:131]\n"
"v_exp_f32 v77,v77\n"
MF "v[124:127],v[20:23],v[208:211],v[124:127]\n"
"v_exp_f32 v78,v78\n"
MF "v[124:127],v[16:19],v[188:191],v[124:127]\n"
"v_exp_f32 v79,v79\n"
MF "v[120:123],v[16:19],v[184:187],v[120:123]\n"
"v_add_f32 v53,v58,v76\n"
"v_add_f32 v53,v53,v77\n"
MF "v[120:123],v[20:23],v[204:207],v[120:123]\n"
CV "v76,v76,v77\n"
"v_add_f32 v53,v53,v78\n"
MF "v[116:119],v[20:23],v[200:203],v[116:119]\n"
"v_add_f32 v53,v53,v79\n"
CV "v77,v78,v79\n"
MF "v[116:119],v[16:19],v[180:183],v[116:119]\n"
"s_waitcnt vmcnt(6) lgkmcnt(0)\n"
"s_barrier\n"
MF "v[112:115],v[16:19],v[176:179],v[112:115]\n"
"v_exp_f32 v80,v80\n"
MF "v[112:115],v[20:23],v[196:199],v[112:115]\n"
"s_nop 0\n"
TR "v[16:17],v2\n"
TR "v[18:19],v2 offset:6144\n"
MF "v[112:115],v[24:27],v[216:219],v[112:115]\n"
"s_add_u32 s73,s73,12288\n"
TR "v[20:21],v3\n"
TR "v[22:23],v3 offset:6144\n"
MF "v[112:115],v[28:31],v[236:239],v[112:115]\n"
"s_cmp_eq_u32 s73,s19\n"
"s_cselect_b32 s73,s18,s73\n"
"s_add_u32 m0,s65,49152\n"
MF "v[116:119],v[28:31],v[240:243],v[116:119]\n"
LD "v8,s[36:39],s73 offen nt lds\n"
MF "v[116:119],v[24:27],v[220:223],v[116:119]\n"
"s_add_u32 m0,s65,50048\n"
"v_exp_f32 v81,v81\n"
MF "v[120:123],v[24:27],v[224:227],v[120:123]\n"
LD "v8,s[36:39],s73 offen offset:128 nt lds\n"
MF "v[120:123],v[28:31],v[244:247],v[120:123]\n"
"v_exp_f32 v82,v82\n"
MF "v[124:127],v[28:31],v[248:251],v[124:127]\n"
"s_add_u32 m0,s65,50944\n"
"v_exp_f32 v83,v83\n"
MF "v[124:127],v[24:27],v[228:231],v[124:127]\n"
LD "v8,s[36:39],s73 offen offset:256 nt lds\n"
MF "v[128:131],v[24:27],v[232:235],v[128:131]\n"
"v_add_f32 v54,v59,v80\n"
"v_add_f32 v54,v54,v81\n"
MF "v[128:131],v[28:31],v[252:255],v[128:131]\n"
"s_waitcnt lgkmcnt(0)\n"
TR "v[24:25],v4\n"
TR "v[26:27],v4 offset:6144\n"
MF "a[0:3],v[16:19],v[92:95],a[0:3]\n"
"s_add_u32 m0,s65,49152\n"
TR "v[28:29],v5\n"
TR "v[30:31],v5 offset:6144\n"
MF "a[20:23],v[20:23],v[92:95],a[20:23]\n"
LD "v8,s[36:39],s73 offen offset:3072 nt lds\n"
MF "a[24:27],v[20:23],v[96:99],a[24:27]\n"
CV "v80,v80,v81\n"
"v_add_f32 v54,v54,v82\n"
MF "a[4:7],v[16:19],v[96:99],a[4:7]\n"
"s_add_u32 m0,s65,50048\n"
"v_add_f32 v54,v54,v83\n"
CV "v81,v82,v83\n"
MF "a[8:11],v[16:19],v[100:103],a[8:11]\n"
LD "v8,s[36:39],s73 offen offset:3200 nt lds\n"
MF "a[28:31],v[20:23],v[100:103],a[28:31]\n"
"v_exp_f32 v84,v84\n"
MF "a[32:35],v[20:23],v[104:107],a[32:35]\n"
"s_add_u32 m0,s65,50944\n"
"v_exp_f32 v85,v85\n"
MF "a[12:15],v[16:19],v[104:107],a[12:15]\n"
LD "v8,s[36:39],s73 offen offset:3328 nt lds\n"
MF "a[16:19],v[16:19],v[108:111],a[16:19]\n"
"v_exp_f32 v86,v86\n"
MF "a[36:39],v[20:23],v[108:111],a[36:39]\n"
"s_waitcnt lgkmcnt(0)\n"
TR "v[16:17],v2 offset:1024\n"
TR "v[18:19],v2 offset:7168\n"
MF "a[56:59],v[24:27],v[108:111],a[56:59]\n"
"s_nop 0\n"
TR "v[20:21],v3 offset:1024\n"
TR "v[22:23],v3 offset:7168\n"
MF "a[76:79],v[28:31],v[108:111],a[76:79]\n"
"v_exp_f32 v87,v87\n"
MF "a[72:75],v[28:31],v[104:107],a[72:75]\n"
"v_add_f32 v55,v60,v84\n"
"v_add_f32 v55,v55,v85\n"
MF "a[52:55],v[24:27],v[104:107],a[52:55]\n"
CV "v84,v84,v85\n"
"v_add_f32 v55,v55,v86\n"
MF "a[48:51],v[24:27],v[100:103],a[48:51]\n"
"v_add_f32 v55,v55,v87\n"
CV "v85,v86,v87\n"
MF "a[68:71],v[28:31],v[100:103],a[68:71]\n"
"v_exp_f32 v88,v88\n"
MF "a[64:67],v[28:31],v[96:99],a[64:67]\n"
"v_exp_f32 v89,v89\n"
MF "a[44:47],v[24:27],v[96:99],a[44:47]\n"
"v_exp_f32 v90,v90\n"
MF "a[40:43],v[24:27],v[92:95],a[40:43]\n"
"v_exp_f32 v91,v91\n"
MF "a[60:63],v[28:31],v[92:95],a[60:63]\n"
"s_waitcnt lgkmcnt(0)\n"
TR "v[24:25],v4 offset:1024\n"
TR "v[26:27],v4 offset:7168\n"
MF "a[80:83],v[16:19],v[92:95],a[80:83]\n"
"s_nop 0\n"
TR "v[28:29],v5 offset:1024\n"
TR "v[30:31],v5 offset:7168\n"
MF "a[100:103],v[20:23],v[92:95],a[100:103]\n"
"v_add_f32 v56,v61,v88\n"
"v_add_f32 v56,v56,v89\n"
MF "a[104:107],v[20:23],v[96:99],a[104:107]\n"
CV "v88,v88,v89\n"
"v_add_f32 v56,v56,v90\n"
MF "a[84:87],v[16:19],v[96:99],a[84:87]\n"
"v_add_f32 v56,v56,v91\n"
CV "v89,v90,v91\n"
MF "a[88:91],v[16:19],v[100:103],a[88:91]\n"
"v_exp_f32 v112,v112\n"
MF "a[108:111],v[20:23],v[100:103],a[108:111]\n"
"v_exp_f32 v113,v113\n"
MF "a[112:115],v[20:23],v[104:107],a[112:115]\n"
"v_exp_f32 v114,v114\n"
MF "a[92:95],v[16:19],v[104:107],a[92:95]\n"
"v_exp_f32 v115,v115\n"
MF "a[96:99],v[16:19],v[108:111],a[96:99]\n"
"v_add_f32 v52,v52,v112\n"
"v_add_f32 v52,v52,v113\n"
MF "a[116:119],v[20:23],v[108:111],a[116:119]\n"
"s_waitcnt lgkmcnt(0)\n"
TR "v[16:17],v2 offset:2048\n"
TR "v[18:19],v2 offset:8192\n"
MF "a[136:139],v[24:27],v[108:111],a[136:139]\n"
"s_nop 0\n"
TR "v[20:21],v3 offset:2048\n"
TR "v[22:23],v3 offset:8192\n"
MF "a[156:159],v[28:31],v[108:111],a[156:159]\n"
CV "v74,v112,v113\n"
"v_add_f32 v52,v52,v114\n"
MF "a[152:155],v[28:31],v[104:107],a[152:155]\n"
"v_add_f32 v52,v52,v115\n"
CV "v75,v114,v115\n"
MF "a[132:135],v[24:27],v[104:107],a[132:135]\n"
"v_exp_f32 v116,v116\n"
MF "a[128:131],v[24:27],v[100:103],a[128:131]\n"
"v_exp_f32 v117,v117\n"
MF "a[148:151],v[28:31],v[100:103],a[148:151]\n"
"v_exp_f32 v118,v118\n"
MF "a[144:147],v[28:31],v[96:99],a[144:147]\n"
"v_exp_f32 v119,v119\n"
MF "a[124:127],v[24:27],v[96:99],a[124:127]\n"
"v_add_f32 v53,v53,v116\n"
"v_add_f32 v53,v53,v117\n"
MF "a[120:123],v[24:27],v[92:95],a[120:123]\n"
CV "v78,v116,v117\n"
"v_add_f32 v53,v53,v118\n"
MF "a[140:143],v[28:31],v[92:95],a[140:143]\n"
"s_waitcnt lgkmcnt(0)\n"
TR "v[24:25],v4 offset:2048\n"
TR "v[26:27],v4 offset:8192\n"
MF "a[160:163],v[16:19],v[92:95],a[160:163]\n"
"s_nop 0\n"
TR "v[28:29],v5 offset:2048\n"
TR "v[30:31],v5 offset:8192\n"
MF "a[180:183],v[20:23],v[92:95],a[180:183]\n"
"v_add_f32 v53,v53,v119\n"
CV "v79,v118,v119\n"
MF "a[184:187],v[20:23],v[96:99],a[184:187]\n"
"v_exp_f32 v120,v120\n"
MF "a[164:167],v[16:19],v[96:99],a[164:167]\n"
"v_exp_f32 v121,v121\n"
MF "a[168:171],v[16:19],v[100:103],a[168:171]\n"
"v_exp_f32 v122,v122\n"
MF "a[188:191],v[20:23],v[100:103],a[188:191]\n"
"v_exp_f32 v123,v123\n"
MF "a[192:195],v[20:23],v[104:107],a[192:195]\n"
"v_add_f32 v54,v54,v120\n"
"v_add_f32 v54,v54,v121\n"
MF "a[172:175],v[16:19],v[104:107],a[172:175]\n"
CV "v82,v120,v121\n"
"v_add_f32 v54,v54,v122\n"
MF "a[176:179],v[16:19],v[108:111],a[176:179]\n"
"s_waitcnt vmcnt(6) lgkmcnt(0)\n"
"s_barrier\n"
MF "a[196:199],v[20:23],v[108:111],a[196:199]\n"
"s_add_u32 s72,s72,6144\n"
"ds_read_b128 v[16:19],v0 offset:24576\n"
"ds_read_b128 v[20:23],v1 offset:24576\n"
MF "a[216:219],v[24:27],v[108:111],a[216:219]\n"
"s_cmp_eq_u32 s72,s17\n"
"s_cselect_b32 s72,s16,s72\n"
"s_add_u32 m0,s64,0\n"
MF "a[236:239],v[28:31],v[108:111],a[236:239]\n"
LD "v7,s[32:35],s72 offen nt lds\n"
MF "a[232:235],v[28:31],v[104:107],a[232:235]\n"
"s_add_u32 m0,s64,896\n"
"v_add_f32 v54,v54,v123\n"
CV "v83,v122,v123\n"
MF "a[212:215],v[24:27],v[104:107],a[212:215]\n"
LD "v7,s[32:35],s72 offen offset:128 nt lds\n"
MF "a[208:211],v[24:27],v[100:103],a[208:211]\n"
"s_add_u32 m0,s64,1792\n"
MF "a[228:231],v[28:31],v[100:103],a[228:231]\n"
LD "v7,s[32:35],s72 offen offset:256 nt lds\n"
MF "a[224:227],v[28:31],v[96:99],a[224:227]\n"
MF "a[204:207],v[24:27],v[96:99],a[204:207]\n"
MF "a[200:203],v[24:27],v[92:95],a[200:203]\n"
MF "a[220:223],v[28:31],v[92:95],a[220:223]\n"
"s_waitcnt lgkmcnt(0)\n"
MF "v[92:95],v[16:19],v[136:139],v[32:35]\n"
"s_add_u32 s72,s72,6144\n"
"ds_read_b128 v[24:27],v0 offset:25600\n"
"ds_read_b128 v[28:31],v1 offset:25600\n"
MF "v[92:95],v[20:23],v[156:159],v[92:95]\n"
"s_add_u32 m0,s64,6144\n"
"v_exp_f32 v124,v124\n"
MF "v[96:99],v[20:23],v[160:163],v[36:39]\n"
LD "v7,s[32:35],s72 offen nt lds\n"
MF "v[96:99],v[16:19],v[140:143],v[96:99]\n"
"v_exp_f32 v125,v125\n"
MF "v[100:103],v[16:19],v[144:147],v[40:43]\n"
"s_add_u32 m0,s64,7040\n"
"v_exp_f32 v126,v126\n"
MF "v[100:103],v[20:23],v[164:167],v[100:103]\n"
LD "v7,s[32:35],s72 offen offset:128 nt lds\n"
MF "v[104:107],v[20:23],v[168:171],v[44:47]\n"
"v_exp_f32 v127,v127\n"
MF "v[104:107],v[16:19],v[148:151],v[104:107]\n"
"s_add_u32 m0,s64,7936\n"
"v_add_f32 v55,v55,v124\n"
"v_add_f32 v55,v55,v125\n"
MF "v[108:111],v[16:19],v[152:155],v[48:51]\n"
LD "v7,s[32:35],s72 offen offset:256 nt lds\n"
MF "v[108:111],v[20:23],v[172:175],v[108:111]\n"
"s_waitcnt lgkmcnt(0)\n"
"ds_read_b128 v[16:19],v0 offset:26624\n"
"ds_read_b128 v[20:23],v1 offset:26624\n"
MF "v[108:111],v[24:27],v[192:195],v[108:111]\n"
CV "v86,v124,v125\n"
"v_add_f32 v55,v55,v126\n"
MF "v[108:111],v[28:31],v[212:215],v[108:111]\n"
"v_add_f32 v55,v55,v127\n"
CV "v87,v126,v127\n"
MF "v[104:107],v[28:31],v[208:211],v[104:107]\n"
"v_exp_f32 v128,v128\n"
MF "v[104:107],v[24:27],v[188:191],v[104:107]\n"
"v_exp_f32 v129,v129\n"
MF "v[100:103],v[24:27],v[184:187],v[100:103]\n"
"v_exp_f32 v130,v130\n"
MF "v[100:103],v[28:31],v[204:207],v[100:103]\n"
"v_exp_f32 v131,v131\n"
MF "v[96:99],v[28:31],v[200:203],v[96:99]\n"
"v_add_f32 v56,v56,v128\n"
"v_add_f32 v56,v56,v129\n"
MF "v[96:99],v[24:27],v[180:183],v[96:99]\n"
CV "v90,v128,v129\n"
"v_add_f32 v56,v56,v130\n"
MF "v[92:95],v[24:27],v[176:179],v[92:95]\n"
"v_add_f32 v56,v56,v131\n"
CV "v91,v130,v131\n"
MF "v[92:95],v[28:31],v[196:199],v[92:95]\n"
"s_waitcnt lgkmcnt(0)\n"
"ds_read_b128 v[24:27],v0 offset:30720\n"
"ds_read_b128 v[28:31],v1 offset:30720\n"
MF "v[92:95],v[16:19],v[216:219],v[92:95]\n"
MF "v[92:95],v[20:23],v[236:239],v[92:95]\n"
MF "v[96:99],v[20:23],v[240:243],v[96:99]\n"
MF "v[96:99],v[16:19],v[220:223],v[96:99]\n"
MF "v[100:103],v[16:19],v[224:227],v[100:103]\n"
MF "v[100:103],v[20:23],v[244:247],v[100:103]\n"
MF "v[104:107],v[20:23],v[248:251],v[104:107]\n"
MF "v[104:107],v[16:19],v[228:231],v[104:107]\n"
"v_max3_f32 v10,v52,v53,v54\n"
"v_max3_f32 v10,v10,v55,v56\n"
MF "v[108:111],v[16:19],v[232:235],v[108:111]\n"
"v_cmp_ngt_f32 vcc,s77,v10\n"
MF "v[108:111],v[20:23],v[252:255],v[108:111]\n"
"s_waitcnt lgkmcnt(0)\n"
"s_cbranch_vccnz .Lstub_14\n"
".Lback_14:\n"
MF "v[112:115],v[24:27],v[136:139],v[32:35]\n"
"s_add_u32 s74,s74,12288\n"
"ds_read_b128 v[16:19],v0 offset:31744\n"
"ds_read_b128 v[20:23],v1 offset:31744\n"
MF "v[112:115],v[28:31],v[156:159],v[112:115]\n"
"s_cmp_eq_u32 s74,s20\n"
"s_cselect_b32 s74,s70,s74\n"
MF "v[116:119],v[28:31],v[160:163],v[36:39]\n"
"v_exp_f32 v92,v92\n"
MF "v[116:119],v[24:27],v[140:143],v[116:119]\n"
"v_exp_f32 v93,v93\n"
MF "v[120:123],v[24:27],v[144:147],v[40:43]\n"
"v_exp_f32 v94,v94\n"
MF "v[120:123],v[28:31],v[164:167],v[120:123]\n"
"v_exp_f32 v95,v95\n"
MF "v[124:127],v[28:31],v[168:171],v[44:47]\n"
"v_add_f32 v57,v52,v92\n"
"v_add_f32 v57,v57,v93\n"
MF "v[124:127],v[24:27],v[148:151],v[124:127]\n"
CV "v92,v92,v93\n"
"v_add_f32 v57,v57,v94\n"
MF "v[128:131],v[24:27],v[152:155],v[48:51]\n"
"v_add_f32 v57,v57,v95\n"
CV "v93,v94,v95\n"
MF "v[128:131],v[28:31],v[172:175],v[128:131]\n"
"s_waitcnt lgkmcnt(0)\n"
"ds_read_b128 v[24:27],v0 offset:32768\n"
"ds_read_b128 v[28:31],v1 offset:32768\n"
MF "v[128:131],v[16:19],v[192:195],v[128:131]\n"
"v_exp_f32 v96,v96\n"
MF "v[128:131],v[20:23],v[212:215],v[128:131]\n"
"v_exp_f32 v97,v97\n"
MF "v[124:127],v[20:23],v[208:211],v[124:127]\n"
"v_exp_f32 v98,v98\n"
MF "v[124:127],v[16:19],v[188:191],v[124:127]\n"
"v_exp_f32 v99,v99\n"
MF "v[120:123],v[16:19],v[184:187],v[120:123]\n"
"v_add_f32 v58,v53,v96\n"
"v_add_f32 v58,v58,v97\n"
MF "v[120:123],v[20:23],v[204:207],v[120:123]\n"
CV "v96,v96,v97\n"
"v_add_f32 v58,v58,v98\n"
MF "v[116:119],v[20:23],v[200:203],v[116:119]\n"
"v_add_f32 v58,v58,v99\n"
CV "v97,v98,v99\n"
MF "v[116:119],v[16:19],v[180:183],v[116:119]\n"
"s_waitcnt vmcnt(6) lgkmcnt(0)\n"
"s_barrier\n"
MF "v[112:115],v[16:19],v[176:179],v[112:115]\n"
"v_exp_f32 v100,v100\n"
MF "v[112:115],v[20:23],v[196:199],v[112:115]\n"
"s_nop 0\n"
TR "v[16:17],v2 offset:49152\n"
TR "v[18:19],v2 offset:55296\n"
MF "v[112:115],v[24:27],v[216:219],v[112:115]\n"
"s_add_u32 s73,s73,12288\n"
TR "v[20:21],v3 offset:49152\n"
TR "v[22:23],v3 offset:55296\n"
MF "v[112:115],v[28:31],v[236:239],v[112:115]\n"
"s_cmp_eq_u32 s73,s19\n"
"s_cselect_b32 s73,s18,s73\n"
"s_add_u32 m0,s65,24576\n"
MF "v[116:119],v[28:31],v[240:243],v[116:119]\n"
LD "v8,s[36:39],s73 offen nt lds\n"
MF "v[116:119],v[24:27],v[220:223],v[116:119]\n"
"s_add_u32 m0,s65,25472\n"
"v_exp_f32 v101,v101\n"
MF "v[120:123],v[24:27],v[224:227],v[120:123]\n"
LD "v8,s[36:39],s73 offen offset:128 nt lds\n"
MF "v[120:123],v[28:31],v[244:247],v[120:123]\n"
"v_exp_f32 v102,v102\n"
MF "v[124:127],v[28:31],v[248:251],v[124:127]\n"
"s_add_u32 m0,s65,26368\n"
"v_exp_f32 v103,v103\n"
MF "v[124:127],v[24:27],v[228:231],v[124:127]\n"
LD "v8,s[36:39],s73 offen offset:256 nt lds\n"
MF "v[128:131],v[24:27],v[232:235],v[128:131]\n"
"v_add_f32 v59,v54,v100\n"
"v_add_f32 v59,v59,v101\n"
MF "v[128:131],v[28:31],v[252:255],v[128:131]\n"
"s_waitcnt lgkmcnt(0)\n"
TR "v[24:25],v4 offset:49152\n"
TR "v[26:27],v4 offset:55296\n"
MF "a[0:3],v[16:19],v[72:75],a[0:3]\n"
"s_add_u32 m0,s65,24576\n"
TR "v[28:29],v5 offset:49152\n"
TR "v[30:31],v5 offset:55296\n"
MF "a[20:23],v[20:23],v[72:75],a[20:23]\n"
LD "v8,s[36:39],s73 offen offset:3072 nt lds\n"
MF "a[24:27],v[20:23],v[76:79],a[24:27]\n"
CV "v100,v100,v101\n"
"v_add_f32 v59,v59,v102\n"
MF "a[4:7],v[16:19],v[76:79],a[4:7]\n"
"s_add_u32 m0,s65,25472\n"
"v_add_f32 v59,v59,v103\n"
CV "v101,v102,v103\n"
MF "a[8:11],v[16:19],v[80:83],a[8:11]\n"
LD "v8,s[36:39],s73 offen offset:3200 nt lds\n"
MF "a[28:31],v[20:23],v[80:83],a[28:31]\n"
"v_exp_f32 v104,v104\n"
MF "a[32:35],v[20:23],v[84:87],a[32:35]\n"
"s_add_u32 m0,s65,26368\n"
"v_exp_f32 v105,v105\n"
MF "a[12:15],v[16:19],v[84:87],a[12:15]\n"
LD "v8,s[36:39],s73 offen offset:3328 nt lds\n"
MF "a[16:19],v[16:19],v[88:91],a[16:19]\n"
"v_exp_f32 v106,v106\n"
MF "a[36:39],v[20:23],v[88:91],a[36:39]\n"
"s_waitcnt lgkmcnt(0)\n"
TR "v[16:17],v2 offset:50176\n"
TR "v[18:19],v2 offset:56320\n"
MF "a[56:59],v[24:27],v[88:91],a[56:59]\n"
"s_nop 0\n"
TR "v[20:21],v3 offset:50176\n"
TR "v[22:23],v3 offset:56320\n"
MF "a[76:79],v[28:31],v[88:91],a[76:79]\n"
"v_exp_f32 v107,v107\n"
MF "a[72:75],v[28:31],v[84:87],a[72:75]\n"
"v_add_f32 v60,v55,v104\n"
"v_add_f32 v60,v60,v105\n"
MF "a[52:55],v[24:27],v[84:87],a[52:55]\n"
CV "v104,v104,v105\n"
"v_add_f32 v60,v60,v106\n"
MF "a[48:51],v[24:27],v[80:83],a[48:51]\n"
"v_add_f32 v60,v60,v107\n"
CV "v105,v106,v107\n"
MF "a[68:71],v[28:31],v[80:83],a[68:71]\n"
"v_exp_f32 v108,v108\n"
MF "a[64:67],v[28:31],v[76:79],a[64:67]\n"
"v_exp_f32 v109,v109\n"
MF "a[44:47],v[24:27],v[76:79],a[44:47]\n"
"v_exp_f32 v110,v110\n"
MF "a[40:43],v[24:27],v[72:75],a[40:43]\n"
"v_exp_f32 v111,v111\n"
MF "a[60:63],v[28:31],v[72:75],a[60:63]\n"
"s_waitcnt lgkmcnt(0)\n"
TR "v[24:25],v4 offset:50176\n"
TR "v[26:27],v4 offset:56320\n"
MF "a[80:83],v[16:19],v[72:75],a[80:83]\n"
"s_nop 0\n"
TR "v[28:29],v5 offset:50176\n"
TR "v[30:31],v5 offset:56320\n"
MF "a[100:103],v[20:23],v[72:75],a[100:103]\n"
"v_add_f32 v61,v56,v108\n"
"v_add_f32 v61,v61,v109\n"
MF "a[104:107],v[20:23],v[76:79],a[104:107]\n"
CV "v108,v108,v109\n"
"v_add_f32 v61,v61,v110\n"
MF "a[84:87],v[16:19],v[76:79],a[84:87]\n"
"v_add_f32 v61,v61,v111\n"
CV "v109,v110,v111\n"
MF "a[88:91],v[16:19],v[80:83],a[88:91]\n"
"v_exp_f32 v112,v112\n"
MF "a[108:111],v[20:23],v[80:83],a[108:111]\n"
"v_exp_f32 v113,v113\n"
MF "a[112:115],v[20:23],v[84:87],a[112:115]\n"
"v_exp_f32 v114,v114\n"
MF "a[92:95],v[16:19],v[84:87],a[92:95]\n"
"v_exp_f32 v115,v115\n"
MF "a[96:99],v[16:19],v[88:91],a[96:99]\n"
"v_add_f32 v57,v57,v112\n"
"v_add_f32 v57,v57,v113\n"
MF "a[116:119],v[20:23],v[88:91],a[116:119]\n"
"s_waitcnt lgkmcnt(0)\n"
TR "v[16:17],v2 offset:51200\n"
TR "v[18:19],v2 offset:57344\n"
MF "a[136:139],v[24:27],v[88:91],a[136:139]\n"
"s_nop 0\n"
TR "v[20:21],v3 offset:51200\n"
TR "v[22:23],v3 offset:57344\n"
MF "a[156:159],v[28:31],v[88:91],a[156:159]\n"
CV "v94,v112,v113\n"
"v_add_f32 v57,v57,v114\n"
MF "a[152:155],v[28:31],v[84:87],a[152:155]\n"
"v_add_f32 v57,v57,v115\n"
CV "v95,v114,v115\n"
MF "a[132:135],v[24:27],v[84:87],a[132:135]\n"
"v_exp_f32 v116,v116\n"
MF "a[128:131],v[24:27],v[80:83],a[128:131]\n"
"v_exp_f32 v117,v117\n"
MF "a[148:151],v[28:31],v[80:83],a[148:151]\n"
"v_exp_f32 v118,v118\n"
MF "a[144:147],v[28:31],v[76:79],a[144:147]\n"
"v_exp_f32 v119,v119\n"
MF "a[124:127],v[24:27],v[76:79],a[124:127]\n"
"v_add_f32 v58,v58,v116\n"
"v_add_f32 v58,v58,v117\n"
MF "a[120:123],v[24:27],v[72:75],a[120:123]\n"
CV "v98,v116,v117\n"
"v_add_f32 v58,v58,v118\n"
MF "a[140:143],v[28:31],v[72:75],a[140:143]\n"
"s_waitcnt lgkmcnt(0)\n"
TR "v[24:25],v4 offset:51200\n"
TR "v[26:27],v4 offset:57344\n"
MF "a[160:163],v[16:19],v[72:75],a[160:163]\n"
"s_nop 0\n"
TR "v[28:29],v5 offset:51200\n"
TR "v[30:31],v5 offset:57344\n"
MF "a[180:183],v[20:23],v[72:75],a[180:183]\n"
"v_add_f32 v58,v58,v119\n"
CV "v99,v118,v119\n"
MF "a[184:187],v[20:23],v[76:79],a[184:187]\n"
"v_exp_f32 v120,v120\n"
MF "a[164:167],v[16:19],v[76:79],a[164:167]\n"
"v_exp_f32 v121,v121\n"
MF "a[168:171],v[16:19],v[80:83],a[168:171]\n"
"v_exp_f32 v122,v122\n"
MF "a[188:191],v[20:23],v[80:83],a[188:191]\n"
"v_exp_f32 v123,v123\n"
MF "a[192:195],v[20:23],v[84:87],a[192:195]\n"
"v_add_f32 v59,v59,v120\n"
"v_add_f32 v59,v59,v121\n"
MF "a[172:175],v[16:19],v[84:87],a[172:175]\n"
CV "v102,v120,v121\n"
"v_add_f32 v59,v59,v122\n"
MF "a[176:179],v[16:19],v[88:91],a[176:179]\n"
"s_waitcnt vmcnt(6) lgkmcnt(0)\n"
"s_barrier\n"
MF "a[196:199],v[20:23],v[88:91],a[196:199]\n"
"s_add_u32 s72,s72,6144\n"
"ds_read_b128 v[16:19],v0\n"
"ds_read_b128 v[20:23],v1\n"
MF "a[216:219],v[24:27],v[88:91],a[216:219]\n"
"s_cmp_eq_u32 s72,s17\n"
"s_cselect_b32 s72,s16,s72\n"
"s_add_u32 m0,s64,49152\n"
MF "a[236:239],v[28:31],v[88:91],a[236:239]\n"
LD "v7,s[32:35],s72 offen nt lds\n"
MF "a[232:235],v[28:31],v[84:87],a[232:235]\n"
"s_add_u32 m0,s64,50048\n"
"v_add_f32 v59,v59,v123\n"
CV "v103,v122,v123\n"
MF "a[212:215],v[24:27],v[84:87],a[212:215]\n"
LD "v7,s[32:35],s72 offen offset:128 nt lds\n"
MF "a[208:211],v[24:27],v[80:83],a[208:211]\n"
"s_add_u32 m0,s64,50944\n"
MF "a[228:231],v[28:31],v[80:83],a[228:231]\n"
LD "v7,s[32:35],s72 offen offset:256 nt lds\n"
MF "a[224:227],v[28:31],v[76:79],a[224:227]\n"
MF "a[204:207],v[24:27],v[76:79],a[204:207]\n"
MF "a[200:203],v[24:27],v[72:75],a[200:203]\n"
MF "a[220:223],v[28:31],v[72:75],a[220:223]\n"
"s_waitcnt lgkmcnt(0)\n"
".Lmid:\n"
MF "v[72:75],v[16:19],v[136:139],v[32:35]\n"
"s_add_u32 s72,s72,6144\n"
"ds_read_b128 v[24:27],v0 offset:1024\n"
"ds_read_b128 v[28:31],v1 offset:1024\n"
MF "v[72:75],v[20:23],v[156:159],v[72:75]\n"
"s_add_u32 m0,s64,55296\n"
"v_exp_f32 v124,v124\n"
MF "v[76:79],v[20:23],v[160:163],v[36:39]\n"
LD "v7,s[32:35],s72 offen nt lds\n"
MF "v[76:79],v[16:19],v[140:143],v[76:79]\n"
"v_exp_f32 v125,v125\n"
MF "v[80:83],v[16:19],v[144:147],v[40:43]\n"
"s_add_u32 m0,s64,56192\n"
"v_exp_f32 v126,v126\n"
MF "v[80:83],v[20:23],v[164:167],v[80:83]\n"
LD "v7,s[32:35],s72 offen offset:128 nt lds\n"
MF "v[84:87],v[20:23],v[168:171],v[44:47]\n"
"v_exp_f32 v127,v127\n"
MF "v[84:87],v[16:19],v[148:151],v[84:87]\n"
"s_add_u32 m0,s64,57088\n"
"v_add_f32 v60,v60,v124\n"
"v_add_f32 v60,v60,v125\n"
MF "v[88:91],v[16:19],v[152:155],v[48:51]\n"
LD "v7,s[32:35],s72 offen offset:256 nt lds\n"
MF "v[88:91],v[20:23],v[172:175],v[88:91]\n"
"s_waitcnt lgkmcnt(0)\n"
"ds_read_b128 v[16:19],v0 offset:2048\n"
"ds_read_b128 v[20:23],v1 offset:2048\n"
MF "v[88:91],v[24:27],v[192:195],v[88:91]\n"
CV "v106,v124,v125\n"
"v_add_f32 v60,v60,v126\n"
MF "v[88:91],v[28:31],v[212:215],v[88:91]\n"
"v_add_f32 v60,v60,v127\n"
CV "v107,v126,v127\n"
MF "v[84:87],v[28:31],v[208:211],v[84:87]\n"
"v_exp_f32 v128,v128\n"
MF "v[84:87],v[24:27],v[188:191],v[84:87]\n"
"v_exp_f32 v129,v129\n"
MF "v[80:83],v[24:27],v[184:187],v[80:83]\n"
"v_exp_f32 v130,v130\n"
MF "v[80:83],v[28:31],v[204:207],v[80:83]\n"
"v_exp_f32 v131,v131\n"
MF "v[76:79],v[28:31],v[200:203],v[76:79]\n"
"v_add_f32 v61,v61,v128\n"
"v_add_f32 v61,v61,v129\n"
MF "v[76:79],v[24:27],v[180:183],v[76:79]\n"
CV "v110,v128,v129\n"
"v_add_f32 v61,v61,v130\n"
MF "v[72:75],v[24:27],v[176:179],v[72:75]\n"
"v_add_f32 v61,v61,v131\n"
CV "v111,v130,v131\n"
MF "v[72:75],v[28:31],v[196:199],v[72:75]\n"
"s_waitcnt lgkmcnt(0)\n"
"ds_read_b128 v[24:27],v0 offset:6144\n"
"ds_read_b128 v[28:31],v1 offset:6144\n"
MF "v[72:75],v[16:19],v[216:219],v[72:75]\n"
MF "v[72:75],v[20:23],v[236:239],v[72:75]\n"
MF "v[76:79],v[20:23],v[240:243],v[76:79]\n"
MF "v[76:79],v[16:19],v[220:223],v[76:79]\n"
MF "v[80:83],v[16:19],v[224:227],v[80:83]\n"
MF "v[80:83],v[20:23],v[244:247],v[80:83]\n"
MF "v[84:87],v[20:23],v[248:251],v[84:87]\n"
MF "v[84:87],v[16:19],v[228:231],v[84:87]\n"
"v_max3_f32 v10,v57,v58,v59\n"
"v_max3_f32 v10,v10,v60,v61\n"
MF "v[88:91],v[16:19],v[232:235],v[88:91]\n"
"v_cmp_ngt_f32 vcc,s95,v10\n"
MF "v[88:91],v[20:23],v[252:255],v[88:91]\n"
"s_waitcnt lgkmcnt(0)\n"
"s_cbranch_vccnz .Lstub_15\n"
".Lback_15:\n"
MF "v[112:115],v[24:27],v[136:139],v[32:35]\n"
"s_add_u32 s74,s74,12288\n"
"ds_read_b128 v[16:19],v0 offset:7168\n"
"ds_read_b128 v[20:23],v1 offset:7168\n"
MF "v[112:115],v[28:31],v[156:159],v[112:115]\n"
"s_cmp_eq_u32 s74,s20\n"
"s_cselect_b32 s74,s70,s74\n"
MF "v[116:119],v[28:31],v[160:163],v[36:39]\n"
"v_exp_f32 v72,v72\n"
MF "v[116:119],v[24:27],v[140:143],v[116:119]\n"
"v_exp_f32 v73,v73\n"
MF "v[120:123],v[24:27],v[144:147],v[40:43]\n"
"v_exp_f32 v74,v74\n"
MF "v[120:123],v[28:31],v[164:167],v[120:123]\n"
"v_exp_f32 v75,v75\n"
MF "v[124:127],v[28:31],v[168:171],v[44:47]\n"
"v_add_f32 v52,v57,v72\n"
"v_add_f32 v52,v52,v73\n"
MF "v[124:127],v[24:27],v[148:151],v[124:127]\n"
CV "v72,v72,v73\n"
"v_add_f32 v52,v52,v74\n"
MF "v[128:131],v[24:27],v[152:155],v[48:51]\n"
"v_add_f32 v52,v52,v75\n"
CV "v73,v74,v75\n"
MF "v[128:131],v[28:31],v[172:175],v[128:131]\n"
"s_waitcnt lgkmcnt(0)\n"
"ds_read_b128 v[24:27],v0 offset:8192\n"
"ds_read_b128 v[28:31],v1 offset:8192\n"
MF "v[128:131],v[16:19],v[192:195],v[128:131]\n"
"v_exp_f32 v76,v76\n"
MF "v[128:131],v[20:23],v[212:215],v[128:131]\n"
"v_exp_f32 v77,v77\n"
MF "v[124:127],v[20:23],v[208:211],v[124:127]\n"
"v_exp_f32 v78,v78\n"
MF "v[124:127],v[16:19],v[188:191],v[124:127]\n"
"v_exp_f32 v79,v79\n"
MF "v[120:123],v[16:19],v[184:187],v[120:123]\n"
"v_add_f32 v53,v58,v76\n"
"v_add_f32 v53,v53,v77\n"
MF "v[120:123],v[20:23],v[204:207],v[120:123]\n"
CV "v76,v76,v77\n"
"v_add_f32 v53,v53,v78\n"
MF "v[116:119],v[20:23],v[200:203],v[116:119]\n"
"v_add_f32 v53,v53,v79\n"
CV "v77,v78,v79\n"
MF "v[116:119],v[16:19],v[180:183],v[116:119]\n"
"s_waitcnt vmcnt(6) lgkmcnt(0)\n"
"s_barrier\n"
MF "v[112:115],v[16:19],v[176:179],v[112:115]\n"
"v_exp_f32 v80,v80\n"
MF "v[112:115],v[20:23],v[196:199],v[112:115]\n"
"s_nop 0\n"
TR "v[16:17],v2 offset:24576\n"
TR "v[18:19],v2 offset:30720\n"
MF "v[112:115],v[24:27],v[216:219],v[112:115]\n"
"s_add_u32 s73,s73,12288\n"
TR "v[20:21],v3 offset:24576\n"
TR "v[22:23],v3 offset:30720\n"
MF "v[112:115],v[28:31],v[236:239],v[112:115]\n"
"s_cmp_eq_u32 s73,s19\n"
"s_cselect_b32 s73,s18,s73\n"
"s_add_u32 m0,s65,0\n"
MF "v[116:119],v[28:31],v[240:243],v[116:119]\n"
LD "v8,s[36:39],s73 offen nt lds\n"
MF "v[116:119],v[24:27],v[220:223],v[116:119]\n"
"s_add_u32 m0,s65,896\n"
"v_exp_f32 v81,v81\n"
MF "v[120:123],v[24:27],v[224:227],v[120:123]\n"
LD "v8,s[36:39],s73 offen offset:128 nt lds\n"
MF "v[120:123],v[28:31],v[244:247],v[120:123]\n"
"v_exp_f32 v82,v82\n"
MF "v[124:127],v[28:31],v[248:251],v[124:127]\n"
"s_add_u32 m0,s65,1792\n"
"v_exp_f32 v83,v83\n"
MF "v[124:127],v[24:27],v[228:231],v[124:127]\n"
LD "v8,s[36:39],s73 offen offset:256 nt lds\n"
MF "v[128:131],v[24:27],v[232:235],v[128:131]\n"
"v_add_f32 v54,v59,v80\n"
"v_add_f32 v54,v54,v81\n"
MF "v[128:131],v[28:31],v[252:255],v[128:131]\n"
"s_waitcnt lgkmcnt(0)\n"
TR "v[24:25],v4 offset:24576\n"
TR "v[26:27],v4 offset:30720\n"
MF "a[0:3],v[16:19],v[92:95],a[0:3]\n"
"s_add_u32 m0,s65,0\n"
TR "v[28:29],v5 offset:24576\n"
TR "v[30:31],v5 offset:30720\n"
MF "a[20:23],v[20:23],v[92:95],a[20:23]\n"
LD "v8,s[36:39],s73 offen offset:3072 nt lds\n"
MF "a[24:27],v[20:23],v[96:99],a[24:27]\n"
CV "v80,v80,v81\n"
"v_add_f32 v54,v54,v82\n"
MF "a[4:7],v[16:19],v[96:99],a[4:7]\n"
"s_add_u32 m0,s65,896\n"
"v_add_f32 v54,v54,v83\n"
CV "v81,v82,v83\n"
MF "a[8:11],v[16:19],v[100:103],a[8:11]\n"
LD "v8,s[36:39],s73 offen offset:3200 nt lds\n"
MF "a[28:31],v[20:23],v[100:103],a[28:31]\n"
"v_exp_f32 v84,v84\n"
MF "a[32:35],v[20:23],v[104:107],a[32:35]\n"
"s_add_u32 m0,s65,1792\n"
"v_exp_f32 v85,v85\n"
MF "a[12:15],v[16:19],v[104:107],a[12:15]\n"
LD "v8,s[36:39],s73 offen offset:3328 nt lds\n"
MF "a[16:19],v[16:19],v[108:111],a[16:19]\n"
"v_exp_f32 v86,v86\n"
MF "a[36:39],v[20:23],v[108:111],a[36:39]\n"
"s_waitcnt lgkmcnt(0)\n"
TR "v[16:17],v2 offset:25600\n"
TR "v[18:19],v2 offset:31744\n"
MF "a[56:59],v[24:27],v[108:111],a[56:59]\n"
"s_nop 0\n"
TR "v[20:21],v3 offset:25600\n"
TR "v[22:23],v3 offset:31744\n"
MF "a[76:79],v[28:31],v[108:111],a[76:79]\n"
"v_exp_f32 v87,v87\n"
MF "a[72:75],v[28:31],v[104:107],a[72:75]\n"
"v_add_f32 v55,v60,v84\n"
"v_add_f32 v55,v55,v85\n"
MF "a[52:55],v[24:27],v[104:107],a[52:55]\n"
CV "v84,v84,v85\n"
"v_add_f32 v55,v55,v86\n"
MF "a[48:51],v[24:27],v[100:103],a[48:51]\n"
"v_add_f32 v55,v55,v87\n"
CV "v85,v86,v87\n"
MF "a[68:71],v[28:31],v[100:103],a[68:71]\n"
"v_exp_f32 v88,v88\n"
MF "a[64:67],v[28:31],v[96:99],a[64:67]\n"
"v_exp_f32 v89,v89\n"
MF "a[44:47],v[24:27],v[96:99],a[44:47]\n"
"v_exp_f32 v90,v90\n"
MF "a[40:43],v[24:27],v[92:95],a[40:43]\n"
"v_exp_f32 v91,v91\n"
MF "a[60:63],v[28:31],v[92:95],a[60:63]\n"
"s_waitcnt lgkmcnt(0)\n"
TR "v[24:25],v4 offset:25600\n"
TR "v[26:27],v4 offset:31744\n"
MF "a[80:83],v[16:19],v[92:95],a[80:83]\n"
"s_nop 0\n"
TR "v[28:29],v5 offset:25600\n"
TR "v[30:31],v5 offset:31744\n"
MF "a[100:103],v[20:23],v[92:95],a[100:103]\n"
"v_add_f32 v56,v61,v88\n"
"v_add_f32 v56,v56,v89\n"
MF "a[104:107],v[20:23],v[96:99],a[104:107]\n"
CV "v88,v88,v89\n"
"v_add_f32 v56,v56,v90\n"
MF "a[84:87],v[16:19],v[96:99],a[84:87]\n"
"v_add_f32 v56,v56,v91\n"
CV "v89,v90,v91\n"
MF "a[88:91],v[16:19],v[100:103],a[88:91]\n"
"v_exp_f32 v112,v112\n"
MF "a[108:111],v[20:23],v[100:103],a[108:111]\n"
"v_exp_f32 v113,v113\n"
MF "a[112:115],v[20:23],v[104:107],a[112:115]\n"
"v_exp_f32 v114,v114\n"
MF "a[92:95],v[16:19],v[104:107],a[92:95]\n"
"v_exp_f32 v115,v115\n"
MF "a[96:99],v[16:19],v[108:111],a[96:99]\n"
"v_add_f32 v52,v52,v112\n"
"v_add_f32 v52,v52,v113\n"
MF "a[116:119],v[20:23],v[108:111],a[116:119]\n"
"s_waitcnt lgkmcnt(0)\n"
TR "v[16:17],v2 offset:26624\n"
TR "v[18:19],v2 offset:32768\n"
MF "a[136:139],v[24:27],v[108:111],a[136:139]\n"
"s_nop 0\n"
TR "v[20:21],v3 offset:26624\n"
TR "v[22:23],v3 offset:32768\n"
MF "a[156:159],v[28:31],v[108:111],a[156:159]\n"
CV "v74,v112,v113\n"
"v_add_f32 v52,v52,v114\n"
MF "a[152:155],v[28:31],v[104:107],a[152:155]\n"
"v_add_f32 v52,v52,v115\n"
CV "v75,v114,v115\n"
MF "a[132:135],v[24:27],v[104:107],a[132:135]\n"
"v_exp_f32 v116,v116\n"
MF "a[128:131],v[24:27],v[100:103],a[128:131]\n"
"v_exp_f32 v117,v117\n"
MF "a[148:151],v[28:31],v[100:103],a[148:151]\n"
"v_exp_f32 v118,v118\n"
MF "a[144:147],v[28:31],v[96:99],a[144:147]\n"
"v_exp_f32 v119,v119\n"
MF "a[124:127],v[24:27],v[96:99],a[124:127]\n"
"v_add_f32 v53,v53,v116\n"
"v_add_f32 v53,v53,v117\n"
MF "a[120:123],v[24:27],v[92:95],a[120:123]\n"
CV "v78,v116,v117\n"
"v_add_f32 v53,v53,v118\n"
MF "a[140:143],v[28:31],v[92:95],a[140:143]\n"
"s_waitcnt lgkmcnt(0)\n"
TR "v[24:25],v4 offset:26624\n"
TR "v[26:27],v4 offset:32768\n"
MF "a[160:163],v[16:19],v[92:95],a[160:163]\n"
"s_nop 0\n"
TR "v[28:29],v5 offset:26624\n"
TR "v[30:31],v5 offset:32768\n"
MF "a[180:183],v[20:23],v[92:95],a[180:183]\n"
"v_add_f32 v53,v53,v119\n"
CV "v79,v118,v119\n"
MF "a[184:187],v[20:23],v[96:99],a[184:187]\n"
"v_exp_f32 v120,v120\n"
MF "a[164:167],v[16:19],v[96:99],a[164:167]\n"
"v_exp_f32 v121,v121\n"
MF "a[168:171],v[16:19],v[100:103],a[168:171]\n"
"v_exp_f32 v122,v122\n"
MF "a[188:191],v[20:23],v[100:103],a[188:191]\n"
"v_exp_f32 v123,v123\n"
MF "a[192:195],v[20:23],v[104:107],a[192:195]\n"
"v_add_f32 v54,v54,v120\n"
"v_add_f32 v54,v54,v121\n"
MF "a[172:175],v[16:19],v[104:107],a[172:175]\n"
CV "v82,v120,v121\n"
"v_add_f32 v54,v54,v122\n"
MF "a[176:179],v[16:19],v[108:111],a[176:179]\n"
"s_waitcnt vmcnt(6) lgkmcnt(0)\n"
"s_barrier\n"
MF "a[196:199],v[20:23],v[108:111],a[196:199]\n"
"s_add_u32 s72,s72,6144\n"
"ds_read_b128 v[16:19],v0 offset:49152\n"
"ds_read_b128 v[20:23],v1 offset:49152\n"
MF "a[216:219],v[24:27],v[108:111],a[216:219]\n"
"s_cmp_eq_u32 s72,s17\n"
"s_cselect_b32 s72,s16,s72\n"
"s_add_u32 m0,s64,24576\n"
MF "a[236:239],v[28:31],v[108:111],a[236:239]\n"
LD "v7,s[32:35],s72 offen nt lds\n"
MF "a[232:235],v[28:31],v[104:107],a[232:235]\n"
"s_add_u32 m0,s64,25472\n"
"v_add_f32 v54,v54,v123\n"
CV "v83,v122,v123\n"
MF "a[212:215],v[24:27],v[104:107],a[212:215]\n"
LD "v7,s[32:35],s72 offen offset:128 nt lds\n"
MF "a[208:211],v[24:27],v[100:103],a[208:211]\n"
"s_add_u32 m0,s64,26368\n"
MF "a[228:231],v[28:31],v[100:103],a[228:231]\n"
LD "v7,s[32:35],s72 offen offset:256 nt lds\n"
MF "a[224:227],v[28:31],v[96:99],a[224:227]\n"
MF "a[204:207],v[24:27],v[96:99],a[204:207]\n"
MF "a[200:203],v[24:27],v[92:95],a[200:203]\n"
MF "a[220:223],v[28:31],v[92:95],a[220:223]\n"
"s_waitcnt lgkmcnt(0)\n"
MF "v[92:95],v[16:19],v[136:139],v[32:35]\n"
"s_add_u32 s72,s72,6144\n"
"ds_read_b128 v[24:27],v0 offset:50176\n"
"ds_read_b128 v[28:31],v1 offset:50176\n"
MF "v[92:95],v[20:23],v[156:159],v[92:95]\n"
"s_add_u32 m0,s64,30720\n"
"v_exp_f32 v124,v124\n"
MF "v[96:99],v[20:23],v[160:163],v[36:39]\n"
LD "v7,s[32:35],s72 offen nt lds\n"
MF "v[96:99],v[16:19],v[140:143],v[96:99]\n"
"v_exp_f32 v125,v125\n"
MF "v[100:103],v[16:19],v[144:147],v[40:43]\n"
"s_add_u32 m0,s64,31616\n"
"v_exp_f32 v126,v126\n"
MF "v[100:103],v[20:23],v[164:167],v[100:103]\n"
LD "v7,s[32:35],s72 offen offset:128 nt lds\n"
MF "v[104:107],v[20:23],v[168:171],v[44:47]\n"
"v_exp_f32 v127,v127\n"
MF "v[104:107],v[16:19],v[148:151],v[104:107]\n"
"s_add_u32 m0,s64,32512\n"
"v_add_f32 v55,v55,v124\n"
"v_add_f32 v55,v55,v125\n"
MF "v[108:111],v[16:19],v[152:155],v[48:51]\n"
LD "v7,s[32:35],s72 offen offset:256 nt lds\n"
MF "v[108:111],v[20:23],v[172:175],v[108:111]\n"
"s_waitcnt lgkmcnt(0)\n"
"ds_read_b128 v[16:19],v0 offset:51200\n"
"ds_read_b128 v[20:23],v1 offset:51200\n"
MF "v[108:111],v[24:27],v[192:195],v[108:111]\n"
CV "v86,v124,v125\n"
"v_add_f32 v55,v55,v126\n"
MF "v[108:111],v[28:31],v[212:215],v[108:111]\n"
"v_add_f32 v55,v55,v127\n"
CV "v87,v126,v127\n"
MF "v[104:107],v[28:31],v[208:211],v[104:107]\n"
"v_exp_f32 v128,v128\n"
MF "v[104:107],v[24:27],v[188:191],v[104:107]\n"
"v_exp_f32 v129,v129\n"
MF "v[100:103],v[24:27],v[184:187],v[100:103]\n"
"v_exp_f32 v130,v130\n"
MF "v[100:103],v[28:31],v[204:207],v[100:103]\n"
"v_exp_f32 v131,v131\n"
MF "v[96:99],v[28:31],v[200:203],v[96:99]\n"
"v_add_f32 v56,v56,v128\n"
"v_add_f32 v56,v56,v129\n"
MF "v[96:99],v[24:27],v[180:183],v[96:99]\n"
CV "v90,v128,v129\n"
"v_add_f32 v56,v56,v130\n"
MF "v[92:95],v[24:27],v[176:179],v[92:95]\n"
"v_add_f32 v56,v56,v131\n"
CV "v91,v130,v131\n"
MF "v[92:95],v[28:31],v[196:199],v[92:95]\n"
"s_waitcnt lgkmcnt(0)\n"
"ds_read_b128 v[24:27],v0 offset:55296\n"
"ds_read_b128 v[28:31],v1 offset:55296\n"
MF "v[92:95],v[16:19],v[216:219],v[92:95]\n"
MF "v[92:95],v[20:23],v[236:239],v[92:95]\n"
MF "v[96:99],v[20:23],v[240:243],v[96:99]\n"
MF "v[96:99],v[16:19],v[220:223],v[96:99]\n"
MF "v[100:103],v[16:19],v[224:227],v[100:103]\n"
MF "v[100:103],v[20:23],v[244:247],v[100:103]\n"
MF "v[104:107],v[20:23],v[248:251],v[104:107]\n"
MF "v[104:107],v[16:19],v[228:231],v[104:107]\n"
"v_max3_f32 v10,v52,v53,v54\n"
"v_max3_f32 v10,v10,v55,v56\n"
MF "v[108:111],v[16:19],v[232:235],v[108:111]\n"
"v_cmp_ngt_f32 vcc,s77,v10\n"
MF "v[108:111],v[20:23],v[252:255],v[108:111]\n"
"s_waitcnt lgkmcnt(0)\n"
"s_cbranch_vccnz .Lstub_16\n"
".Lback_16:\n"
MF "v[112:115],v[24:27],v[136:139],v[32:35]\n"
"s_add_u32 s74,s74,12288\n"
"ds_read_b128 v[16:19],v0 offset:56320\n"
"ds_read_b128 v[20:23],v1 offset:56320\n"
MF "v[112:115],v[28:31],v[156:159],v[112:115]\n"
"s_cmp_eq_u32 s74,s20\n"
"s_cselect_b32 s74,s70,s74\n"
MF "v[116:119],v[28:31],v[160:163],v[36:39]\n"
"v_exp_f32 v92,v92\n"
MF "v[116:119],v[24:27],v[140:143],v[116:119]\n"
"v_exp_f32 v93,v93\n"
MF "v[120:123],v[24:27],v[144:147],v[40:43]\n"
"v_exp_f32 v94,v94\n"
MF "v[120:123],v[28:31],v[164:167],v[120:123]\n"
"v_exp_f32 v95,v95\n"
MF "v[124:127],v[28:31],v[168:171],v[44:47]\n"
"v_add_f32 v57,v52,v92\n"
"v_add_f32 v57,v57,v93\n"
MF "v[124:127],v[24:27],v[148:151],v[124:127]\n"
CV "v92,v92,v93\n"
"v_add_f32 v57,v57,v94\n"
MF "v[128:131],v[24:27],v[152:155],v[48:51]\n"
"v_add_f32 v57,v57,v95\n"
CV "v93,v94,v95\n"
MF "v[128:131],v[28:31],v[172:175],v[128:131]\n"
"s_waitcnt lgkmcnt(0)\n"
"ds_read_b128 v[24:27],v0 offset:57344\n"
"ds_read_b128 v[28:31],v1 offset:57344\n"
MF "v[128:131],v[16:19],v[192:195],v[128:131]\n"
"s_sub_u32 s61,s61,1\n"
"v_exp_f32 v96,v96\n"
MF "v[128:131],v[20:23],v[212:215],v[128:131]\n"
"s_cmp_eq_u32 s61,1\n"
"s_cselect_b32 s95,0,s95\n"
"v_add_f32 v58,v53,v96\n"
MF "v[124:127],v[20:23],v[208:211],v[124:127]\n"
"v_exp_f32 v97,v97\n"
MF "v[124:127],v[16:19],v[188:191],v[124:127]\n"
"v_exp_f32 v98,v98\n"
MF "v[120:123],v[16:19],v[184:187],v[120:123]\n"
"v_exp_f32 v99,v99\n"
MF "v[120:123],v[20:23],v[204:207],v[120:123]\n"
"v_add_f32 v58,v58,v97\n"
CV "v96,v96,v97\n"
MF "v[116:119],v[20:23],v[200:203],v[116:119]\n"
"v_add_f32 v58,v58,v98\n"
"v_add_f32 v58,v58,v99\n"
MF "v[116:119],v[16:19],v[180:183],v[116:119]\n"
"s_waitcnt vmcnt(6) lgkmcnt(0)\n"
"s_barrier\n"
MF "v[112:115],v[16:19],v[176:179],v[112:115]\n"
CV "v97,v98,v99\n"
MF "v[112:115],v[20:23],v[196:199],v[112:115]\n"
"s_nop 0\n"
TR "v[16:17],v2\n"
TR "v[18:19],v2 offset:6144\n"
MF "v[112:115],v[24:27],v[216:219],v[112:115]\n"
"s_add_u32 s73,s73,12288\n"
TR "v[20:21],v3\n"
TR "v[22:23],v3 offset:6144\n"
MF "v[112:115],v[28:31],v[236:239],v[112:115]\n"
"s_cmp_eq_u32 s73,s19\n"
"s_cselect_b32 s73,s18,s73\n"
"s_add_u32 m0,s65,49152\n"
MF "v[116:119],v[28:31],v[240:243],v[116:119]\n"
LD "v8,s[36:39],s73 offen nt lds\n"
MF "v[116:119],v[24:27],v[220:223],v[116:119]\n"
"s_add_u32 m0,s65,50048\n"
"v_exp_f32 v100,v100\n"
MF "v[120:123],v[24:27],v[224:227],v[120:123]\n"
LD "v8,s[36:39],s73 offen offset:128 nt lds\n"
MF "v[120:123],v[28:31],v[244:247],v[120:123]\n"
"v_exp_f32 v101,v101\n"
MF "v[124:127],v[28:31],v[248:251],v[124:127]\n"
"s_add_u32 m0,s65,50944\n"
"v_exp_f32 v102,v102\n"
MF "v[124:127],v[24:27],v[228:231],v[124:127]\n"
LD "v8,s[36:39],s73 offen offset:256 nt lds\n"
MF "v[128:131],v[24:27],v[232:235],v[128:131]\n"
"v_exp_f32 v103,v103\n"
MF "v[128:131],v[28:31],v[252:255],v[128:131]\n"
"s_waitcnt lgkmcnt(0)\n"
TR "v[24:25],v4\n"
TR "v[26:27],v4 offset:6144\n"
MF "a[0:3],v[16:19],v[72:75],a[0:3]\n"
"s_add_u32 m0,s65,49152\n"
TR "v[28:29],v5\n"
TR "v[30:31],v5 offset:6144\n"
MF "a[20:23],v[20:23],v[72:75],a[20:23]\n"
LD "v8,s[36:39],s73 offen offset:3072 nt lds\n"
MF "a[24:27],v[20:23],v[76:79],a[24:27]\n"
"v_add_f32 v59,v54,v100\n"
"v_add_f32 v59,v59,v101\n"
MF "a[4:7],v[16:19],v[76:79],a[4:7]\n"
"s_add_u32 m0,s65,50048\n"
CV "v100,v100,v101\n"
"v_add_f32 v59,v59,v102\n"
MF "a[8:11],v[16:19],v[80:83],a[8:11]\n"
LD "v8,s[36:39],s73 offen offset:3200 nt lds\n"
MF "a[28:31],v[20:23],v[80:83],a[28:31]\n"
"v_add_f32 v59,v59,v103\n"
CV "v101,v102,v103\n"
MF "a[32:35],v[20:23],v[84:87],a[32:35]\n"
"s_add_u32 m0,s65,50944\n"
"v_exp_f32 v104,v104\n"
MF "a[12:15],v[16:19],v[84:87],a[12:15]\n"
LD "v8,s[36:39],s73 offen offset:3328 nt lds\n"
MF "a[16:19],v[16:19],v[88:91],a[16:19]\n"
"v_exp_f32 v105,v105\n"
MF "a[36:39],v[20:23],v[88:91],a[36:39]\n"
"s_waitcnt lgkmcnt(0)\n"
TR "v[16:17],v2 offset:1024\n"
TR "v[18:19],v2 offset:7168\n"
MF "a[56:59],v[24:27],v[88:91],a[56:59]\n"
"s_nop 0\n"
TR "v[20:21],v3 offset:1024\n"
TR "v[22:23],v3 offset:7168\n"
MF "a[76:79],v[28:31],v[88:91],a[76:79]\n"
"v_exp_f32 v106,v106\n"
MF "a[72:75],v[28:31],v[84:87],a[72:75]\n"
"v_exp_f32 v107,v107\n"
MF "a[52:55],v[24:27],v[84:87],a[52:55]\n"
"v_add_f32 v60,v55,v104\n"
"v_add_f32 v60,v60,v105\n"
MF "a[48:51],v[24:27],v[80:83],a[48:51]\n"
CV "v104,v104,v105\n"
"v_add_f32 v60,v60,v106\n"
MF "a[68:71],v[28:31],v[80:83],a[68:71]\n"
"v_add_f32 v60,v60,v107\n"
CV "v105,v106,v107\n"
MF "a[64:67],v[28:31],v[76:79],a[64:67]\n"
"v_exp_f32 v108,v108\n"
MF "a[44:47],v[24:27],v[76:79],a[44:47]\n"
"v_exp_f32 v109,v109\n"
MF "a[40:43],v[24:27],v[72:75],a[40:43]\n"
"v_exp_f32 v110,v110\n"
MF "a[60:63],v[28:31],v[72:75],a[60:63]\n"
"s_waitcnt lgkmcnt(0)\n"
TR "v[24:25],v4 offset:1024\n"
TR "v[26:27],v4 offset:7168\n"
MF "a[80:83],v[16:19],v[72:75],a[80:83]\n"
"s_nop 0\n"
TR "v[28:29],v5 offset:1024\n"
TR "v[30:31],v5 offset:7168\n"
MF "a[100:103],v[20:23],v[72:75],a[100:103]\n"
"v_exp_f32 v111,v111\n"
MF "a[104:107],v[20:23],v[76:79],a[104:107]\n"
"v_add_f32 v61,v56,v108\n"
"v_add_f32 v61,v61,v109\n"
MF "a[84:87],v[16:19],v[76:79],a[84:87]\n"
CV "v108,v108,v109\n"
"v_add_f32 v61,v61,v110\n"
MF "a[88:91],v[16:19],v[80:83],a[88:91]\n"
"v_add_f32 v61,v61,v111\n"
CV "v109,v110,v111\n"
MF "a[108:111],v[20:23],v[80:83],a[108:111]\n"
"v_exp_f32 v112,v112\n"
MF "a[112:115],v[20:23],v[84:87],a[112:115]\n"
"v_exp_f32 v113,v113\n"
MF "a[92:95],v[16:19],v[84:87],a[92:95]\n"
"v_exp_f32 v114,v114\n"
MF "a[96:99],v[16:19],v[88:91],a[96:99]\n"
"v_exp_f32 v115,v115\n"
MF "a[116:119],v[20:23],v[88:91],a[116:119]\n"
"s_waitcnt lgkmcnt(0)\n"
TR "v[16:17],v2 offset:2048\n"
TR "v[18:19],v2 offset:8192\n"
MF "a[136:139],v[24:27],v[88:91],a[136:139]\n"
"s_nop 0\n"
TR "v[20:21],v3 offset:2048\n"
TR "v[22:23],v3 offset:8192\n"
MF "a[156:159],v[28:31],v[88:91],a[156:159]\n"
"v_add_f32 v57,v57,v112\n"
"v_add_f32 v57,v57,v113\n"
MF "a[152:155],v[28:31],v[84:87],a[152:155]\n"
CV "v94,v112,v113\n"
"v_add_f32 v57,v57,v114\n"
MF "a[132:135],v[24:27],v[84:87],a[132:135]\n"
"v_add_f32 v57,v57,v115\n"
CV "v95,v114,v115\n"
MF "a[128:131],v[24:27],v[80:83],a[128:131]\n"
"v_exp_f32 v116,v116\n"
MF "a[148:151],v[28:31],v[80:83],a[148:151]\n"
"v_exp_f32 v117,v117\n"
MF "a[144:147],v[28:31],v[76:79],a[144:147]\n"
"v_exp_f32 v118,v118\n"
MF "a[124:127],v[24:27],v[76:79],a[124:127]\n"
"v_exp_f32 v119,v119\n"
MF "a[120:123],v[24:27],v[72:75],a[120:123]\n"
"v_add_f32 v58,v58,v116\n"
"v_add_f32 v58,v58,v117\n"
MF "a[140:143],v[28:31],v[72:75],a[140:143]\n"
"s_waitcnt lgkmcnt(0)\n"
TR "v[24:25],v4 offset:2048\n"
TR "v[26:27],v4 offset:8192\n"
MF "a[160:163],v[16:19],v[72:75],a[160:163]\n"
"s_nop 0\n"
TR "v[28:29],v5 offset:2048\n"
TR "v[30:31],v5 offset:8192\n"
MF "a[180:183],v[20:23],v[72:75],a[180:183]\n"
CV "v98,v116,v117\n"
"v_add_f32 v58,v58,v118\n"
MF "a[184:187],v[20:23],v[76:79],a[184:187]\n"
"v_add_f32 v58,v58,v119\n"
CV "v99,v118,v119\n"
MF "a[164:167],v[16:19],v[76:79],a[164:167]\n"
"v_exp_f32 v120,v120\n"
MF "a[168:171],v[16:19],v[80:83],a[168:171]\n"
"v_exp_f32 v121,v121\n"
MF "a[188:191],v[20:23],v[80:83],a[188:191]\n"
"v_exp_f32 v122,v122\n"
MF "a[192:195],v[20:23],v[84:87],a[192:195]\n"
"v_exp_f32 v123,v123\n"
MF "a[172:175],v[16:19],v[84:87],a[172:175]\n"
"v_add_f32 v59,v59,v120\n"
"v_add_f32 v59,v59,v121\n"
MF "a[176:179],v[16:19],v[88:91],a[176:179]\n"
"s_waitcnt vmcnt(6) lgkmcnt(0)\n"
"s_barrier\n"
MF "a[196:199],v[20:23],v[88:91],a[196:199]\n"
"s_add_u32 s72,s72,6144\n"
"ds_read_b128 v[16:19],v0 offset:24576\n"
"ds_read_b128 v[20:23],v1 offset:24576\n"
MF "a[216:219],v[24:27],v[88:91],a[216:219]\n"
"s_cmp_eq_u32 s72,s17\n"
"s_cselect_b32 s72,s16,s72\n"
"s_add_u32 m0,s64,0\n"
MF "a[236:239],v[28:31],v[88:91],a[236:239]\n"
LD "v7,s[32:35],s72 offen nt lds\n"
MF "a[232:235],v[28:31],v[84:87],a[232:235]\n"
"s_add_u32 m0,s64,896\n"
CV "v102,v120,v121\n"
"v_add_f32 v59,v59,v122\n"
MF "a[212:215],v[24:27],v[84:87],a[212:215]\n"
LD "v7,s[32:35],s72 offen offset:128 nt lds\n"
MF "a[208:211],v[24:27],v[80:83],a[208:211]\n"
"s_add_u32 m0,s64,1792\n"
"v_add_f32 v59,v59,v123\n"
CV "v103,v122,v123\n"
MF "a[228:231],v[28:31],v[80:83],a[228:231]\n"
LD "v7,s[32:35],s72 offen offset:256 nt lds\n"
MF "a[224:227],v[28:31],v[76:79],a[224:227]\n"
MF "a[204:207],v[24:27],v[76:79],a[204:207]\n"
"s_waitcnt lgkmcnt(0)\n"
MF "a[200:203],v[24:27],v[72:75],a[200:203]\n"
"s_cmp_lg_u32 s61,0\n"
MF "a[220:223],v[28:31],v[72:75],a[220:223]\n"
"s_cbranch_scc1 .Laloop\n"
".Lepi2:\n"
"v_exp_f32 v124,v124\n"
"v_exp_f32 v125,v125\n"
"v_exp_f32 v126,v126\n"
"v_exp_f32 v127,v127\n"
"v_exp_f32 v128,v128\n"
"v_exp_f32 v129,v129\n"
"v_exp_f32 v130,v130\n"
"v_exp_f32 v131,v131\n"
"v_add_f32 v60,v60,v124\n"
"v_add_f32 v61,v61,v128\n"
"v_add_f32 v60,v60,v125\n"
"v_add_f32 v61,v61,v129\n"
"v_add_f32 v60,v60,v126\n"
"v_add_f32 v61,v61,v130\n"
"v_add_f32 v60,v60,v127\n"
"v_add_f32 v61,v61,v131\n"
CV "v106,v124,v125\n"
CV "v107,v126,v127\n"
CV "v110,v128,v129\n"
CV "v111,v130,v131\n"
"v_max3_f32 v10,v57,v58,v59\n"
"v_max3_f32 v10,v10,v60,v61\n"
"v_cmp_le_f32 vcc,0x47800000,v10\n"
"s_cbranch_vccnz .Lstub_17\n"
".Lback_17:\n"
"s_waitcnt vmcnt(3) lgkmcnt(0)\n"
"s_barrier\n"
"s_and_b32 s58,s56,1\n"
"s_mul_i32 s79,s58,12288\n"
"s_lshr_b32 s58,s56,1\n"
"s_mul_i32 s72,s58,61440\n"
"s_mul_i32 s73,s58,6144\n"
"s_add_u32 s73,s73,s79\n"
"v_lshrrev_b32_e64 v192,7,v9\n"
"v_mul_u32_u24 v192,896,v192\n"
"v_add_u32_e64 v192,v192,v8\n"
"s_add_u32 s88,s88,1024\n"
"s_add_u32 s89,s89,1024\n"
"s_add_u32 s72,s72,122880\n"
"s_lshl_b32 s58,s56,10\n"
"s_add_u32 s58,s58,73728\n"
"v_add_u32 v236,s58,v9\n"
"v_add_u32 v237,65536,v236\n"
TR "v[160:161],v4 offset:50176\n"
TR "v[162:163],v4 offset:56320\n"
TR "v[164:165],v5 offset:50176\n"
TR "v[166:167],v5 offset:56320\n"
TR "v[168:169],v2 offset:51200\n"
TR "v[170:171],v2 offset:57344\n"
TR "v[172:173],v3 offset:51200\n"
TR "v[174:175],v3 offset:57344\n"
TR "v[176:177],v4 offset:51200\n"
TR "v[178:179],v4 offset:57344\n"
TR "v[180:181],v5 offset:51200\n"
TR "v[182:183],v5 offset:57344\n"
TR "v[136:137],v2 offset:49152\n"
TR "v[138:139],v2 offset:55296\n"
TR "v[140:141],v3 offset:49152\n"
TR "v[142:143],v3 offset:55296\n"
TR "v[144:145],v4 offset:49152\n"
TR "v[146:147],v4 offset:55296\n"
TR "v[148:149],v5 offset:49152\n"
TR "v[150:151],v5 offset:55296\n"
TR "v[152:153],v2 offset:50176\n"
TR "v[154:155],v2 offset:56320\n"
TR "v[156:157],v3 offset:50176\n"
TR "v[158:159],v3 offset:56320\n"
"v_mov_b32 v52,v57\n"
"v_mov_b32 v53,v58\n"
"v_mov_b32 v54,v59\n"
"v_mov_b32 v55,v60\n"
"v_mov_b32 v56,v61\n"
"s_add_u32 m0,s73,24576\n"
"s_add_u32 s80,s89,s72\n"
LD "v192,s[48:51],s80 offen lds\n"
"v_add_u32 v193,9216,v192\n"
LD "v193,s[48:51],s80 offen offset:1024 lds\n"
"v_permlane16_swap_b32 v57,v52\n"
"v_permlane16_swap_b32 v58,v53\n"
"v_permlane16_swap_b32 v59,v54\n"
"v_permlane16_swap_b32 v60,v55\n"
"v_permlane16_swap_b32 v61,v56\n"
"v_add_f32 v57,v57,v52\n"
"v_add_f32 v58,v58,v53\n"
"v_add_f32 v59,v59,v54\n"
"v_add_f32 v60,v60,v55\n"
"v_add_f32 v61,v61,v56\n"
"v_mov_b32 v52,v57\n"
"v_mov_b32 v53,v58\n"
"v_mov_b32 v54,v59\n"
"v_mov_b32 v55,v60\n"
"v_mov_b32_e64 v56,v61\n"
"v_add_u32 v194,18432,v192\n"
LD "v194,s[48:51],s80 offen offset:2048 lds\n"
"v_add_u32 v195,27648,v192\n"
LD "v195,s[48:51],s80 offen offset:3072 lds\n"
"v_permlane32_swap_b32 v57,v52\n"
"v_permlane32_swap_b32 v58,v53\n"
"v_permlane32_swap_b32 v59,v54\n"
"v_permlane32_swap_b32 v60,v55\n"
"v_permlane32_swap_b32 v61,v56\n"
"v_add_f32 v57,v57,v52\n"
"v_add_f32 v58,v58,v53\n"
"v_add_f32 v59,v59,v54\n"
"v_add_f32 v60,v60,v55\n"
"v_add_f32 v61,v61,v56\n"
"s_waitcnt lgkmcnt(15)\n"
MF "a[120:123],v[160:163],v[92:95],a[120:123]\n"
MF "a[140:143],v[164:167],v[92:95],a[140:143]\n"
"v_rcp_f32 v208,v57\n"
"v_rcp_f32 v209,v58\n"
"v_rcp_f32 v210,v59\n"
"v_rcp_f32 v211,v60\n"
"v_rcp_f32 v212,v61\n"
MF "a[124:127],v[160:163],v[96:99],a[124:127]\n"
"v_mul_f32_e64 v214,-s13,v208\n"
"v_mul_f32_e64 v215,-s13,v209\n"
"v_mul_f32_e64 v216,-s13,v210\n"
"v_mul_f32_e64 v217,-s13,v211\n"
"v_mul_f32_e64 v218,-s13,v212\n"
"s_add_u32 m0,s73,28672\n"
"v_add_u32 v196,40960,v192\n"
LD "v196,s[48:51],s80 offen lds\n"
AR "v72,a120\n"
AR "v73,a121\n"
AR "v74,a122\n"
MF "a[144:147],v[164:167],v[96:99],a[144:147]\n"
AR "v75,a123\n"
AR "v76,a140\n"
AR "v77,a141\n"
AR "v78,a142\n"
AR "v79,a143\n"
"v_pk_fma_f32 v[72:73],v[72:73],v[208:209],v[214:215] op_sel:[0,0,0] op_sel_hi:[1,0,0]\n"
"v_pk_fma_f32 v[74:75],v[74:75],v[208:209],v[214:215] op_sel:[0,0,0] op_sel_hi:[1,0,0]\n"
"v_pk_fma_f32 v[76:77],v[76:77],v[208:209],v[214:215] op_sel:[0,0,0] op_sel_hi:[1,0,0]\n"
"v_pk_fma_f32 v[78:79],v[78:79],v[208:209],v[214:215] op_sel:[0,0,0] op_sel_hi:[1,0,0]\n"
CV "v112,v72,v73\n"
CV "v113,v74,v75\n"
CV "v114,v76,v77\n"
CV "v115,v78,v79\n"
MF "a[128:131],v[160:163],v[100:103],a[128:131]\n"
"ds_read_b128 a[140:143],v237 offset:20480\n"
AW "a120,v112\n"
AW "a121,v113\n"
AW "a122,v114\n"
AW "a123,v115\n"
"s_waitcnt lgkmcnt(7)\n"
MF "a[44:47],v[144:147],v[96:99],a[44:47]\n"
"v_add_u32 v197,50176,v192\n"
LD "v197,s[48:51],s80 offen offset:1024 lds\n"
AR "v80,a124\n"
AR "v81,a125\n"
AR "v82,a126\n"
MF "a[148:151],v[164:167],v[100:103],a[148:151]\n"
AR "v83,a127\n"
AR "v84,a144\n"
AR "v85,a145\n"
"s_nop 0\n"
AR "v86,a146\n"
AR "v87,a147\n"
"v_pk_fma_f32 v[80:81],v[80:81],v[208:209],v[214:215] op_sel:[0,1,1] op_sel_hi:[1,1,1]\n"
"v_pk_fma_f32 v[82:83],v[82:83],v[208:209],v[214:215] op_sel:[0,1,1] op_sel_hi:[1,1,1]\n"
"v_pk_fma_f32 v[84:85],v[84:85],v[208:209],v[214:215] op_sel:[0,1,1] op_sel_hi:[1,1,1]\n"
"v_pk_fma_f32 v[86:87],v[86:87],v[208:209],v[214:215] op_sel:[0,1,1] op_sel_hi:[1,1,1]\n"
CV "v116,v80,v81\n"
CV "v117,v82,v83\n"
CV "v118,v84,v85\n"
CV "v119,v86,v87\n"
MF "a[132:135],v[160:163],v[104:107],a[132:135]\n"
"ds_read_b128 a[144:147],v236\n"
AW "a124,v116\n"
AW "a125,v117\n"
AW "a126,v118\n"
AW "a127,v119\n"
"s_waitcnt lgkmcnt(6)\n"
MF "a[64:67],v[148:151],v[96:99],a[64:67]\n"
"s_add_u32 m0,s73,0\n"
"s_add_u32 s80,s88,s72\n"
"s_sub_u32 s72,s72,122880\n"
"s_nop 0\n"
LD "v192,s[48:51],s80 offen lds\n"
AR "v72,a128\n"
AR "v73,a129\n"
AR "v74,a130\n"
MF "a[152:155],v[164:167],v[104:107],a[152:155]\n"
AR "v75,a131\n"
AR "v76,a148\n"
AR "v77,a149\n"
AR "v78,a150\n"
AR "v79,a151\n"
"v_pk_fma_f32 v[72:73],v[72:73],v[210:211],v[216:217] op_sel:[0,0,0] op_sel_hi:[1,0,0]\n"
"v_pk_fma_f32 v[74:75],v[74:75],v[210:211],v[216:217] op_sel:[0,0,0] op_sel_hi:[1,0,0]\n"
"v_pk_fma_f32 v[76:77],v[76:77],v[210:211],v[216:217] op_sel:[0,0,0] op_sel_hi:[1,0,0]\n"
"v_pk_fma_f32 v[78:79],v[78:79],v[210:211],v[216:217] op_sel:[0,0,0] op_sel_hi:[1,0,0]\n"
LD "v193,s[48:51],s80 offen offset:1024 lds\n"
CV "v112,v72,v73\n"
CV "v113,v74,v75\n"
CV "v114,v76,v77\n"
CV "v115,v78,v79\n"
MF "a[136:139],v[160:163],v[108:111],a[136:139]\n"
"ds_read_b128 a[148:151],v236 offset:4096\n"
AW "a128,v112\n"
AW "a129,v113\n"
AW "a130,v114\n"
AW "a131,v115\n"
"s_waitcnt lgkmcnt(5)\n"
MF "a[84:87],v[152:155],v[96:99],a[84:87]\n"
LD "v194,s[48:51],s80 offen offset:2048 lds\n"
AR "v80,a132\n"
AR "v81,a133\n"
AR "v82,a134\n"
MF "a[156:159],v[164:167],v[108:111],a[156:159]\n"
AR "v83,a135\n"
AR "v84,a152\n"
AR "v85,a153\n"
"s_nop 0\n"
AR "v86,a154\n"
AR "v87,a155\n"
"v_pk_fma_f32 v[80:81],v[80:81],v[210:211],v[216:217] op_sel:[0,1,1] op_sel_hi:[1,1,1]\n"
"v_pk_fma_f32 v[82:83],v[82:83],v[210:211],v[216:217] op_sel:[0,1,1] op_sel_hi:[1,1,1]\n"
"v_pk_fma_f32 v[84:85],v[84:85],v[210:211],v[216:217] op_sel:[0,1,1] op_sel_hi:[1,1,1]\n"
"v_pk_fma_f32 v[86:87],v[86:87],v[210:211],v[216:217] op_sel:[0,1,1] op_sel_hi:[1,1,1]\n"
CV "v116,v80,v81\n"
CV "v117,v82,v83\n"
CV "v118,v84,v85\n"
CV "v119,v86,v87\n"
MF "a[160:163],v[168:171],v[92:95],a[160:163]\n"
"ds_read_b128 a[152:155],v236 offset:8192\n"
AW "a132,v116\n"
AW "a133,v117\n"
AW "a134,v118\n"
AW "a135,v119\n"
"s_waitcnt lgkmcnt(4)\n"
MF "a[104:107],v[156:159],v[96:99],a[104:107]\n"
LD "v195,s[48:51],s80 offen offset:3072 lds\n"
AR "v72,a136\n"
AR "v73,a137\n"
AR "v74,a138\n"
MF "a[180:183],v[172:175],v[92:95],a[180:183]\n"
AR "v75,a139\n"
AR "v76,a156\n"
AR "v77,a157\n"
"s_nop 0\n"
AR "v78,a158\n"
AR "v79,a159\n"
"v_pk_fma_f32 v[72:73],v[72:73],v[212:213],v[218:219] op_sel:[0,0,0] op_sel_hi:[1,0,0]\n"
"v_pk_fma_f32 v[74:75],v[74:75],v[212:213],v[218:219] op_sel:[0,0,0] op_sel_hi:[1,0,0]\n"
"v_pk_fma_f32 v[76:77],v[76:77],v[212:213],v[218:219] op_sel:[0,0,0] op_sel_hi:[1,0,0]\n"
"v_pk_fma_f32 v[78:79],v[78:79],v[212:213],v[218:219] op_sel:[0,0,0] op_sel_hi:[1,0,0]\n"
CV "v112,v72,v73\n"
CV "v113,v74,v75\n"
CV "v114,v76,v77\n"
CV "v115,v78,v79\n"
MF "a[164:167],v[168:171],v[96:99],a[164:167]\n"
"ds_read_b128 a[156:159],v236 offset:12288\n"
AW "a136,v112\n"
AW "a137,v113\n"
AW "a138,v114\n"
AW "a139,v115\n"
"v_max3_f32 v10,|v72|,|v73|,|v74|\n"
"s_lshl_b32 s58,s56,10\n"
"s_add_u32 s58,s58,159744\n"
"v_max3_f32 v10,v10,|v75|,|v76|\n"
"v_max3_f32 v10,v10,|v77|,|v78|\n"
"v_max3_f32 v10,v10,|v78|,|v79|\n"
"v_mul_f32 v10,s10,v10\n"
"v_lshrrev_b32_e64 v11,2,v9\n"
"v_readlane_b32 s12,v10,3\n"
"v_readlane_b32 s6,v10,22\n"
"v_readlane_b32 s7,v10,41\n"
"v_readlane_b32 s8,v10,60\n"
"v_add_u32 v11,s58,v11\n"
"s_min_u32 s12,s12,s6\n"
"s_min_u32 s7,s7,s8\n"
"s_min_u32 s12,s12,s7\n"
"s_and_b32 s12,s12,0x7f800000\n"
"s_sub_u32 s12,s12,0x14000000\n"
"s_cmp_lt_u32 s12,0x50000000\n"
"s_cselect_b32 s12,s12,0xe7400000\n"
"s_add_u32 s12,s12,0x18c00000\n"
"v_mov_b32 v10,s12\n"
"v_or_b32 v12,0x80000000,v10\n"
"ds_write2st64_b32 v11,v10,v12 offset1:1\n"
MF "a[8:11],v[136:139],v[100:103],a[8:11]\n"
"s_add_u32 m0,s73,4096\n"
"s_nop 0\n"
LD "v196,s[48:51],s80 offen lds\n"
AR "v80,a160\n"
AR "v81,a161\n"
AR "v82,a162\n"
MF "a[184:187],v[172:175],v[96:99],a[184:187]\n"
AR "v83,a163\n"
AR "v84,a180\n"
AR "v85,a181\n"
AR "v86,a182\n"
AR "v87,a183\n"
"v_pk_fma_f32 v[80:81],v[80:81],v[208:209],v[214:215] op_sel:[0,0,0] op_sel_hi:[1,0,0]\n"
"v_pk_fma_f32 v[82:83],v[82:83],v[208:209],v[214:215] op_sel:[0,0,0] op_sel_hi:[1,0,0]\n"
"v_pk_fma_f32 v[84:85],v[84:85],v[208:209],v[214:215] op_sel:[0,0,0] op_sel_hi:[1,0,0]\n"
"v_pk_fma_f32 v[86:87],v[86:87],v[208:209],v[214:215] op_sel:[0,0,0] op_sel_hi:[1,0,0]\n"
LD "v197,s[48:51],s80 offen offset:1024 lds\n"
CV "v116,v80,v81\n"
CV "v117,v82,v83\n"
CV "v118,v84,v85\n"
CV "v119,v86,v87\n"
MF "a[168:171],v[168:171],v[100:103],a[168:171]\n"
"ds_read_b128 a[180:183],v236 offset:16384\n"
AW "a160,v116\n"
AW "a161,v117\n"
AW "a162,v118\n"
AW "a163,v119\n"
MF "a[28:31],v[140:143],v[100:103],a[28:31]\n"
AR "v72,a164\n"
AR "v73,a165\n"
AR "v74,a166\n"
MF "a[188:191],v[172:175],v[100:103],a[188:191]\n"
AR "v75,a167\n"
AR "v76,a184\n"
AR "v77,a185\n"
AR "v78,a186\n"
AR "v79,a187\n"
"v_pk_fma_f32 v[72:73],v[72:73],v[208:209],v[214:215] op_sel:[0,1,1] op_sel_hi:[1,1,1]\n"
"v_pk_fma_f32 v[74:75],v[74:75],v[208:209],v[214:215] op_sel:[0,1,1] op_sel_hi:[1,1,1]\n"
"v_pk_fma_f32 v[76:77],v[76:77],v[208:209],v[214:215] op_sel:[0,1,1] op_sel_hi:[1,1,1]\n"
"v_pk_fma_f32 v[78:79],v[78:79],v[208:209],v[214:215] op_sel:[0,1,1] op_sel_hi:[1,1,1]\n"
CV "v112,v72,v73\n"
CV "v113,v74,v75\n"
CV "v114,v76,v77\n"
CV "v115,v78,v79\n"
MF "a[172:175],v[168:171],v[104:107],a[172:175]\n"
"ds_read_b128 a[184:187],v236 offset:20480\n"
AW "a164,v112\n"
AW "a165,v113\n"
AW "a166,v114\n"
AW "a167,v115\n"
MF "a[48:51],v[144:147],v[100:103],a[48:51]\n"
"s_waitcnt lgkmcnt(0)\n"
"s_barrier\n"
"s_add_u32 m0,s73,49152\n"
"s_add_u32 s80,s89,s72\n"
"s_nop 0\n"
LD "v192,s[48:51],s80 offen lds\n"
AR "v80,a168\n"
AR "v81,a169\n"
AR "v82,a170\n"
MF "a[192:195],v[172:175],v[104:107],a[192:195]\n"
AR "v83,a171\n"
AR "v84,a188\n"
AR "v85,a189\n"
AR "v86,a190\n"
AR "v87,a191\n"
"v_pk_fma_f32 v[80:81],v[80:81],v[210:211],v[216:217] op_sel:[0,0,0] op_sel_hi:[1,0,0]\n"
"v_pk_fma_f32 v[82:83],v[82:83],v[210:211],v[216:217] op_sel:[0,0,0] op_sel_hi:[1,0,0]\n"
"v_pk_fma_f32 v[84:85],v[84:85],v[210:211],v[216:217] op_sel:[0,0,0] op_sel_hi:[1,0,0]\n"
"v_pk_fma_f32 v[86:87],v[86:87],v[210:211],v[216:217] op_sel:[0,0,0] op_sel_hi:[1,0,0]\n"
LD "v193,s[48:51],s80 offen offset:1024 lds\n"
".Lpg19h:\n"
"s_cbranch_execz .Lpg23h\n"
"s_nop 0\n"
CV "v116,v80,v81\n"
CV "v117,v82,v83\n"
CV "v118,v84,v85\n"
CV "v119,v86,v87\n"
MF "a[176:179],v[168:171],v[108:111],a[176:179]\n"
"v_accvgpr_mov_b32 a188,a240\n"
"v_accvgpr_mov_b32 a189,a241\n"
"v_accvgpr_mov_b32 a190,a242\n"
"v_accvgpr_mov_b32 a191,a243\n"
AW "a168,v116\n"
AW "a169,v117\n"
AW "a170,v118\n"
AW "a171,v119\n"
MF "a[68:71],v[148:151],v[100:103],a[68:71]\n"
LD "v194,s[48:51],s80 offen offset:2048 lds\n"
AR "v72,a172\n"
AR "v73,a173\n"
AR "v74,a174\n"
MF "a[196:199],v[172:175],v[108:111],a[196:199]\n"
AR "v75,a175\n"
AR "v76,a192\n"
AR "v77,a193\n"
AR "v78,a194\n"
AR "v79,a195\n"
"v_pk_fma_f32 v[72:73],v[72:73],v[210:211],v[216:217] op_sel:[0,1,1] op_sel_hi:[1,1,1]\n"
"v_pk_fma_f32 v[74:75],v[74:75],v[210:211],v[216:217] op_sel:[0,1,1] op_sel_hi:[1,1,1]\n"
"v_pk_fma_f32 v[76:77],v[76:77],v[210:211],v[216:217] op_sel:[0,1,1] op_sel_hi:[1,1,1]\n"
"v_pk_fma_f32 v[78:79],v[78:79],v[210:211],v[216:217] op_sel:[0,1,1] op_sel_hi:[1,1,1]\n"
LD "v195,s[48:51],s80 offen offset:3072 lds\n"
CV "v112,v72,v73\n"
CV "v113,v74,v75\n"
CV "v114,v76,v77\n"
CV "v115,v78,v79\n"
MF "a[200:203],v[176:179],v[92:95],a[200:203]\n"
"v_accvgpr_mov_b32 a192,a244\n"
"v_accvgpr_mov_b32 a193,a245\n"
"v_accvgpr_mov_b32 a194,a246\n"
"v_accvgpr_mov_b32 a195,a247\n"
AW "a172,v112\n"
AW "a173,v113\n"
AW "a174,v114\n"
AW "a175,v115\n"
MF "a[88:91],v[152:155],v[100:103],a[88:91]\n"
"s_add_u32 m0,s73,53248\n"
"s_nop 0\n"
LD "v196,s[48:51],s80 offen lds\n"
AR "v80,a176\n"
AR "v81,a177\n"
AR "v82,a178\n"
MF "a[220:223],v[180:183],v[92:95],a[220:223]\n"
AR "v83,a179\n"
AR "v84,a196\n"
AR "v85,a197\n"
"s_nop 0\n"
AR "v86,a198\n"
AR "v87,a199\n"
"v_pk_fma_f32 v[80:81],v[80:81],v[212:213],v[218:219] op_sel:[0,0,0] op_sel_hi:[1,0,0]\n"
"v_pk_fma_f32 v[82:83],v[82:83],v[212:213],v[218:219] op_sel:[0,0,0] op_sel_hi:[1,0,0]\n"
"v_pk_fma_f32 v[84:85],v[84:85],v[212:213],v[218:219] op_sel:[0,0,0] op_sel_hi:[1,0,0]\n"
"v_pk_fma_f32 v[86:87],v[86:87],v[212:213],v[218:219] op_sel:[0,0,0] op_sel_hi:[1,0,0]\n"
CV "v116,v80,v81\n"
CV "v117,v82,v83\n"
CV "v118,v84,v85\n"
CV "v119,v86,v87\n"
MF "a[204:207],v[176:179],v[96:99],a[204:207]\n"
"v_accvgpr_mov_b32 a196,a248\n"
"v_accvgpr_mov_b32 a197,a249\n"
"v_accvgpr_mov_b32 a198,a250\n"
"v_accvgpr_mov_b32 a199,a251\n"
AW "a176,v116\n"
AW "a177,v117\n"
AW "a178,v118\n"
AW "a179,v119\n"
MF "a[108:111],v[156:159],v[100:103],a[108:111]\n"
LD "v197,s[48:51],s80 offen offset:1024 lds\n"
AR "v72,a200\n"
AR "v73,a201\n"
AR "v74,a202\n"
MF "a[224:227],v[180:183],v[96:99],a[224:227]\n"
AR "v75,a203\n"
AR "v76,a220\n"
AR "v77,a221\n"
AR "v78,a222\n"
AR "v79,a223\n"
"v_pk_fma_f32 v[72:73],v[72:73],v[208:209],v[214:215] op_sel:[0,0,0] op_sel_hi:[1,0,0]\n"
"v_pk_fma_f32 v[74:75],v[74:75],v[208:209],v[214:215] op_sel:[0,0,0] op_sel_hi:[1,0,0]\n"
"v_pk_fma_f32 v[76:77],v[76:77],v[208:209],v[214:215] op_sel:[0,0,0] op_sel_hi:[1,0,0]\n"
"v_pk_fma_f32 v[78:79],v[78:79],v[208:209],v[214:215] op_sel:[0,0,0] op_sel_hi:[1,0,0]\n"
CV "v112,v72,v73\n"
CV "v113,v74,v75\n"
CV "v114,v76,v77\n"
CV "v115,v78,v79\n"
MF "a[208:211],v[176:179],v[100:103],a[208:211]\n"
AW "a220,v62\n"
AW "a221,v63\n"
AW "a222,v64\n"
AW "a223,v65\n"
AW "a200,v112\n"
AW "a201,v113\n"
AW "a202,v114\n"
AW "a203,v115\n"
MF "a[12:15],v[136:139],v[104:107],a[12:15]\n"
"s_add_u32 m0,s73,73728\n"
"s_add_u32 s80,s88,s72\n"
LD "v192,s[48:51],s80 offen lds\n"
AR "v80,a204\n"
AR "v81,a205\n"
AR "v82,a206\n"
MF "a[228:231],v[180:183],v[100:103],a[228:231]\n"
AR "v83,a207\n"
AR "v84,a224\n"
AR "v85,a225\n"
"s_nop 0\n"
AR "v86,a226\n"
AR "v87,a227\n"
"v_pk_fma_f32 v[80:81],v[80:81],v[208:209],v[214:215] op_sel:[0,1,1] op_sel_hi:[1,1,1]\n"
"v_pk_fma_f32 v[82:83],v[82:83],v[208:209],v[214:215] op_sel:[0,1,1] op_sel_hi:[1,1,1]\n"
"v_pk_fma_f32 v[84:85],v[84:85],v[208:209],v[214:215] op_sel:[0,1,1] op_sel_hi:[1,1,1]\n"
"v_pk_fma_f32 v[86:87],v[86:87],v[208:209],v[214:215] op_sel:[0,1,1] op_sel_hi:[1,1,1]\n"
LD "v193,s[48:51],s80 offen offset:1024 lds\n"
CV "v116,v80,v81\n"
CV "v117,v82,v83\n"
CV "v118,v84,v85\n"
CV "v119,v86,v87\n"
MF "a[212:215],v[176:179],v[104:107],a[212:215]\n"
AW "a224,v66\n"
AW "a225,v67\n"
AW "a226,v68\n"
AW "a227,v69\n"
AW "a204,v116\n"
AW "a205,v117\n"
AW "a206,v118\n"
AW "a207,v119\n"
MF "a[32:35],v[140:143],v[104:107],a[32:35]\n"
LD "v194,s[48:51],s80 offen offset:2048 lds\n"
AR "v72,a208\n"
AR "v73,a209\n"
AR "v74,a210\n"
MF "a[232:235],v[180:183],v[104:107],a[232:235]\n"
AR "v75,a211\n"
AR "v76,a228\n"
AR "v77,a229\n"
AR "v78,a230\n"
AR "v79,a231\n"
"v_pk_fma_f32 v[72:73],v[72:73],v[210:211],v[216:217] op_sel:[0,0,0] op_sel_hi:[1,0,0]\n"
"v_pk_fma_f32 v[74:75],v[74:75],v[210:211],v[216:217] op_sel:[0,0,0] op_sel_hi:[1,0,0]\n"
"v_pk_fma_f32 v[76:77],v[76:77],v[210:211],v[216:217] op_sel:[0,0,0] op_sel_hi:[1,0,0]\n"
"v_pk_fma_f32 v[78:79],v[78:79],v[210:211],v[216:217] op_sel:[0,0,0] op_sel_hi:[1,0,0]\n"
CV "v112,v72,v73\n"
CV "v113,v74,v75\n"
CV "v114,v76,v77\n"
CV "v115,v78,v79\n"
MF "a[216:219],v[176:179],v[108:111],a[216:219]\n"
AW "a228,v70\n"
AW "a229,v71\n"
AW "a230,v132\n"
AW "a231,v133\n"
AW "a208,v112\n"
AW "a209,v113\n"
AW "a210,v114\n"
AW "a211,v115\n"
MF "a[52:55],v[144:147],v[104:107],a[52:55]\n"
LD "v195,s[48:51],s80 offen offset:3072 lds\n"
AR "v80,a212\n"
AR "v81,a213\n"
AR "v82,a214\n"
MF "a[236:239],v[180:183],v[108:111],a[236:239]\n"
AR "v83,a215\n"
AR "v84,a232\n"
AR "v85,a233\n"
AR "v86,a234\n"
AR "v87,a235\n"
"v_pk_fma_f32 v[80:81],v[80:81],v[210:211],v[216:217] op_sel:[0,1,1] op_sel_hi:[1,1,1]\n"
"v_pk_fma_f32 v[82:83],v[82:83],v[210:211],v[216:217] op_sel:[0,1,1] op_sel_hi:[1,1,1]\n"
"v_pk_fma_f32 v[84:85],v[84:85],v[210:211],v[216:217] op_sel:[0,1,1] op_sel_hi:[1,1,1]\n"
"v_pk_fma_f32 v[86:87],v[86:87],v[210:211],v[216:217] op_sel:[0,1,1] op_sel_hi:[1,1,1]\n"
CV "v116,v80,v81\n"
CV "v117,v82,v83\n"
CV "v118,v84,v85\n"
CV "v119,v86,v87\n"
MF "a[0:3],v[136:139],v[92:95],a[0:3]\n"
AW "a232,v134\n"
AW "a233,v135\n"
AW "a234,v6\n"
AW "a235,v13\n"
AW "a212,v116\n"
AW "a213,v117\n"
AW "a214,v118\n"
AW "a215,v119\n"
MF "a[72:75],v[148:151],v[104:107],a[72:75]\n"
"s_add_u32 m0,s73,77824\n"
"s_nop 0\n"
LD "v196,s[48:51],s80 offen lds\n"
AR "v72,a216\n"
AR "v73,a217\n"
AR "v74,a218\n"
MF "a[20:23],v[140:143],v[92:95],a[20:23]\n"
AR "v75,a219\n"
AR "v76,a236\n"
AR "v77,a237\n"
"s_nop 0\n"
AR "v78,a238\n"
AR "v79,a239\n"
"v_pk_fma_f32 v[72:73],v[72:73],v[212:213],v[218:219] op_sel:[0,0,0] op_sel_hi:[1,0,0]\n"
"v_pk_fma_f32 v[74:75],v[74:75],v[212:213],v[218:219] op_sel:[0,0,0] op_sel_hi:[1,0,0]\n"
"v_pk_fma_f32 v[76:77],v[76:77],v[212:213],v[218:219] op_sel:[0,0,0] op_sel_hi:[1,0,0]\n"
"v_pk_fma_f32 v[78:79],v[78:79],v[212:213],v[218:219] op_sel:[0,0,0] op_sel_hi:[1,0,0]\n"
LD "v197,s[48:51],s80 offen offset:1024 lds\n"
CV "v112,v72,v73\n"
CV "v113,v74,v75\n"
CV "v114,v76,v77\n"
CV "v115,v78,v79\n"
MF "a[40:43],v[144:147],v[92:95],a[40:43]\n"
AW "a236,v14\n"
AW "a237,v15\n"
"v_accvgpr_mov_b32 a238,a254\n"
"v_accvgpr_mov_b32 a239,a255\n"
AW "a216,v112\n"
AW "a217,v113\n"
AW "a218,v114\n"
AW "a219,v115\n"
MF "a[92:95],v[152:155],v[104:107],a[92:95]\n"
AR "v80,a0\n"
AR "v81,a1\n"
AR "v82,a2\n"
MF "a[60:63],v[148:151],v[92:95],a[60:63]\n"
AR "v83,a3\n"
AR "v84,a20\n"
AR "v85,a21\n"
AR "v86,a22\n"
AR "v87,a23\n"
"v_pk_fma_f32 v[80:81],v[80:81],v[208:209],v[214:215] op_sel:[0,0,0] op_sel_hi:[1,0,0]\n"
"v_pk_fma_f32 v[82:83],v[82:83],v[208:209],v[214:215] op_sel:[0,0,0] op_sel_hi:[1,0,0]\n"
"v_pk_fma_f32 v[84:85],v[84:85],v[208:209],v[214:215] op_sel:[0,0,0] op_sel_hi:[1,0,0]\n"
"v_pk_fma_f32 v[86:87],v[86:87],v[208:209],v[214:215] op_sel:[0,0,0] op_sel_hi:[1,0,0]\n"
CV "v116,v80,v81\n"
CV "v117,v82,v83\n"
CV "v118,v84,v85\n"
CV "v119,v86,v87\n"
MF "a[80:83],v[152:155],v[92:95],a[80:83]\n"
"ds_read_b128 a[20:23],v236 offset:24576\n"
AW "a0,v116\n"
AW "a1,v117\n"
AW "a2,v118\n"
AW "a3,v119\n"
MF "a[112:115],v[156:159],v[104:107],a[112:115]\n"
AR "v72,a40\n"
AR "v73,a41\n"
AR "v74,a42\n"
MF "a[100:103],v[156:159],v[92:95],a[100:103]\n"
AR "v75,a43\n"
AR "v76,a60\n"
AR "v77,a61\n"
AR "v78,a62\n"
AR "v79,a63\n"
"v_pk_fma_f32 v[72:73],v[72:73],v[208:209],v[214:215] op_sel:[0,0,0] op_sel_hi:[1,0,0]\n"
"v_pk_fma_f32 v[74:75],v[74:75],v[208:209],v[214:215] op_sel:[0,0,0] op_sel_hi:[1,0,0]\n"
"v_pk_fma_f32 v[76:77],v[76:77],v[208:209],v[214:215] op_sel:[0,0,0] op_sel_hi:[1,0,0]\n"
"v_pk_fma_f32 v[78:79],v[78:79],v[208:209],v[214:215] op_sel:[0,0,0] op_sel_hi:[1,0,0]\n"
CV "v112,v72,v73\n"
CV "v113,v74,v75\n"
CV "v114,v76,v77\n"
CV "v115,v78,v79\n"
MF "a[4:7],v[136:139],v[96:99],a[4:7]\n"
"ds_read_b128 a[60:63],v236 offset:45056\n"
AW "a40,v112\n"
AW "a41,v113\n"
AW "a42,v114\n"
AW "a43,v115\n"
MF "a[16:19],v[136:139],v[108:111],a[16:19]\n"
AR "v80,a80\n"
AR "v81,a81\n"
AR "v82,a82\n"
MF "a[24:27],v[140:143],v[96:99],a[24:27]\n"
AR "v83,a83\n"
AR "v84,a100\n"
AR "v85,a101\n"
AR "v86,a102\n"
AR "v87,a103\n"
"v_pk_fma_f32 v[80:81],v[80:81],v[208:209],v[214:215] op_sel:[0,0,0] op_sel_hi:[1,0,0]\n"
"v_pk_fma_f32 v[82:83],v[82:83],v[208:209],v[214:215] op_sel:[0,0,0] op_sel_hi:[1,0,0]\n"
"v_pk_fma_f32 v[84:85],v[84:85],v[208:209],v[214:215] op_sel:[0,0,0] op_sel_hi:[1,0,0]\n"
"v_pk_fma_f32 v[86:87],v[86:87],v[208:209],v[214:215] op_sel:[0,0,0] op_sel_hi:[1,0,0]\n"
CV "v116,v80,v81\n"
CV "v117,v82,v83\n"
CV "v118,v84,v85\n"
CV "v119,v86,v87\n"
MF "a[36:39],v[140:143],v[108:111],a[36:39]\n"
"ds_read_b128 a[100:103],v237\n"
AW "a80,v116\n"
AW "a81,v117\n"
AW "a82,v118\n"
AW "a83,v119\n"
MF "a[56:59],v[144:147],v[108:111],a[56:59]\n"
AR "v72,a4\n"
AR "v73,a5\n"
AR "v74,a6\n"
MF "a[76:79],v[148:151],v[108:111],a[76:79]\n"
AR "v75,a7\n"
AR "v76,a24\n"
AR "v77,a25\n"
AR "v78,a26\n"
AR "v79,a27\n"
"v_pk_fma_f32 v[72:73],v[72:73],v[208:209],v[214:215] op_sel:[0,1,1] op_sel_hi:[1,1,1]\n"
"v_pk_fma_f32 v[74:75],v[74:75],v[208:209],v[214:215] op_sel:[0,1,1] op_sel_hi:[1,1,1]\n"
"v_pk_fma_f32 v[76:77],v[76:77],v[208:209],v[214:215] op_sel:[0,1,1] op_sel_hi:[1,1,1]\n"
"v_pk_fma_f32 v[78:79],v[78:79],v[208:209],v[214:215] op_sel:[0,1,1] op_sel_hi:[1,1,1]\n"
CV "v112,v72,v73\n"
CV "v113,v74,v75\n"
CV "v114,v76,v77\n"
CV "v115,v78,v79\n"
MF "a[96:99],v[152:155],v[108:111],a[96:99]\n"
"ds_read_b128 a[24:27],v236 offset:28672\n"
AW "a4,v112\n"
AW "a5,v113\n"
AW "a6,v114\n"
AW "a7,v115\n"
MF "a[116:119],v[156:159],v[108:111],a[116:119]\n"
"s_mov_b32 s13,s11\n"
"v_and_b32 v0,512,v9\n"
"v_lshlrev_b32 v0,2,v0\n"
"v_sub_u32_e64 v0,v2,v0\n"
"s_bfe_u32 s58,s22,0x10014\n"
"s_lshl_b32 s58,s58,5\n"
"v_xor_b32 v0,s58,v0\n"
"s_and_b32 s58,s56,1\n"
"s_lshl_b32 s58,s58,10\n"
"s_lshr_b32 s59,s56,1\n"
"s_lshl_b32 s59,s59,8\n"
"s_add_u32 s58,s58,s59\n"
"s_add_u32 s58,s58,159744\n"
"v_mov_b32 v252,s58\n"
"v_xor_b32 v1,32,v0\n"
"v_xor_b32 v2,64,v0\n"
"v_xor_b32 v3,96,v0\n"
"v_add_u32 v4,73728,v0\n"
"v_add_u32 v5,73728,v1\n"
"v_add_u32 v6,73728,v2\n"
"v_add_u32 v7,73728,v3\n"
"s_waitcnt vmcnt(12) lgkmcnt(0)\n"
"s_barrier\n"
"ds_read_b128 v[252:255],v252\n"
TR "v[176:177],v0 offset:24576\n"
TR "v[178:179],v0 offset:26624\n"
TR "v[180:181],v0 offset:28672\n"
TR "v[182:183],v0 offset:30720\n"
TR "v[184:185],v0 offset:32768\n"
TR "v[186:187],v0 offset:34816\n"
"s_waitcnt lgkmcnt(0)\n"
"s_cmp_ge_u32 s56,2\n"
"s_cbranch_scc1 .Lrole1\n"
MF "v[16:19],a[120:123],v[176:179],v[252:255]\n"
"s_mov_b32 s65,128\n"
TR "v[188:189],v1 offset:24576\n"
AR "v220,a44\n"
MF "v[16:19],a[160:163],v[180:183],v[16:19]\n"
"s_mov_b32 s9,0x3f80\n"
TR "v[190:191],v1 offset:26624\n"
AR "v221,a45\n"
MF "v[16:19],a[200:203],v[184:187],v[16:19]\n"
"s_add_u32 s89,s89,s65\n"
AR "v222,a46\n"
AR "v223,a47\n"
MF "v[24:27],a[204:207],v[184:187],v[252:255]\n"
"s_bfe_u32 s58,s22,0x30014\n"
TR "a[240:241],v1 offset:28672\n"
AR "v224,a64\n"
MF "v[24:27],a[164:167],v[180:183],v[24:27]\n"
"s_add_u32 s61,s58,1\n"
TR "a[242:243],v1 offset:30720\n"
AR "v225,a65\n"
MF "v[24:27],a[124:127],v[176:179],v[24:27]\n"
"s_and_b32 s61,s61,7\n"
AR "v226,a66\n"
AR "v227,a67\n"
MF "v[32:35],a[128:131],v[176:179],v[252:255]\n"
"s_mov_b32 s64,9\n"
TR "a[244:245],v1 offset:32768\n"
"ds_read_b128 a[64:67],v236 offset:49152\n"
MF "v[32:35],a[168:171],v[180:183],v[32:35]\n"
"s_xor_b32 s67,s78,8\n"
TR "a[246:247],v1 offset:34816\n"
"v_fma_f32 v220,v220,v209,v215\n"
MF "v[32:35],a[208:211],v[184:187],v[32:35]\n"
"s_mov_b32 s71,204800\n"
"v_fma_f32 v221,v221,v209,v215\n"
"v_fma_f32 v222,v222,v209,v215\n"
MF "v[40:43],a[212:215],v[184:187],v[252:255]\n"
"s_mov_b32 s66,204800\n"
"v_fma_f32 v223,v223,v209,v215\n"
"v_fma_f32 v224,v224,v209,v215\n"
MF "v[40:43],a[172:175],v[180:183],v[40:43]\n"
"s_and_b32 s73,s56,1\n"
"v_fma_f32 v225,v225,v209,v215\n"
"v_fma_f32 v226,v226,v209,v215\n"
MF "v[40:43],a[132:135],v[176:179],v[40:43]\n"
"s_mul_i32 s73,s73,5120\n"
"v_fma_f32 v227,v227,v209,v215\n"
CV "v220,v220,v221\n"
MF "v[48:51],a[136:139],v[176:179],v[252:255]\n"
"s_add_u32 s73,s73,s93\n"
CV "v221,v222,v223\n"
CV "v222,v224,v225\n"
MF "v[48:51],a[176:179],v[180:183],v[48:51]\n"
"s_xor_b32 s58,s58,4\n"
CV "v223,v226,v227\n"
AW "a44,v220\n"
MF "v[48:51],a[216:219],v[184:187],v[48:51]\n"
"s_waitcnt lgkmcnt(0)\n"
AW "a45,v221\n"
AW "a46,v222\n"
MF "v[52:55],a[216:219],a[244:247],v[252:255]\n"
"s_mul_i32 s70,s58,204800\n"
TR "v[176:177],v2 offset:24576\n"
AW "a47,v223\n"
MF "v[52:55],a[176:179],a[240:243],v[52:55]\n"
"s_add_u32 s70,s70,s73\n"
TR "v[178:179],v2 offset:26624\n"
AR "v228,a84\n"
MF "v[52:55],a[136:139],v[188:191],v[52:55]\n"
"s_sub_u32 s70,s70,40960\n"
AR "v229,a85\n"
AR "v230,a86\n"
MF "v[44:47],a[132:135],v[188:191],v[252:255]\n"
"s_mov_b32 s85,1\n"
TR "v[180:181],v2 offset:28672\n"
AR "v231,a87\n"
MF "v[44:47],a[172:175],a[240:243],v[44:47]\n"
"s_mov_b32 s83,0xf0000000\n"
TR "v[182:183],v2 offset:30720\n"
AR "v232,a104\n"
MF "v[44:47],a[212:215],a[244:247],v[44:47]\n"
"s_mov_b32 s82,0xf0000000\n"
AR "v233,a105\n"
AR "v234,a106\n"
MF "v[36:39],a[208:211],a[244:247],v[252:255]\n"
"s_add_u32 s58,s79,73728\n"
TR "v[184:185],v2 offset:32768\n"
AR "v235,a107\n"
MF "v[36:39],a[168:171],a[240:243],v[36:39]\n"
"s_nop 0\n"
TR "v[186:187],v2 offset:34816\n"
"ds_read_b128 a[104:107],v237 offset:4096\n"
MF "v[36:39],a[128:131],v[188:191],v[36:39]\n"
"v_fma_f32 v228,v228,v209,v215\n"
"v_fma_f32 v229,v229,v209,v215\n"
MF "v[28:31],a[124:127],v[188:191],v[252:255]\n"
"v_fma_f32 v230,v230,v209,v215\n"
"v_fma_f32 v231,v231,v209,v215\n"
MF "v[28:31],a[164:167],a[240:243],v[28:31]\n"
"v_fma_f32 v232,v232,v209,v215\n"
"v_fma_f32 v233,v233,v209,v215\n"
MF "v[28:31],a[204:207],a[244:247],v[28:31]\n"
"v_fma_f32 v234,v234,v209,v215\n"
"v_fma_f32 v235,v235,v209,v215\n"
MF "v[20:23],a[200:203],a[244:247],v[252:255]\n"
CV "v228,v228,v229\n"
CV "v229,v230,v231\n"
MF "v[20:23],a[160:163],a[240:243],v[20:23]\n"
CV "v230,v232,v233\n"
CV "v231,v234,v235\n"
MF "v[20:23],a[120:123],v[188:191],v[20:23]\n"
"s_waitcnt lgkmcnt(1)\n"
AW "a84,v228\n"
AW "a85,v229\n"
MF "v[56:59],a[120:123],v[176:179],v[252:255]\n"
TR "v[188:189],v3 offset:24576\n"
AW "a86,v230\n"
MF "v[56:59],a[160:163],v[180:183],v[56:59]\n"
TR "v[190:191],v3 offset:26624\n"
AW "a87,v231\n"
MF "v[56:59],a[200:203],v[184:187],v[56:59]\n"
AR "v220,a8\n"
AR "v221,a9\n"
MF "v[64:67],a[204:207],v[184:187],v[252:255]\n"
TR "a[240:241],v3 offset:28672\n"
AR "v222,a10\n"
MF "v[64:67],a[164:167],v[180:183],v[64:67]\n"
TR "a[242:243],v3 offset:30720\n"
AR "v223,a11\n"
MF "v[64:67],a[124:127],v[176:179],v[64:67]\n"
AR "v224,a28\n"
AR "v225,a29\n"
MF "v[72:75],a[128:131],v[176:179],v[252:255]\n"
TR "a[244:245],v3 offset:32768\n"
AR "v226,a30\n"
MF "v[72:75],a[168:171],v[180:183],v[72:75]\n"
TR "a[246:247],v3 offset:34816\n"
AR "v227,a31\n"
MF "v[72:75],a[208:211],v[184:187],v[72:75]\n"
"ds_read_b128 a[28:31],v236 offset:32768\n"
"v_fma_f32 v220,v220,v210,v216\n"
MF "v[80:83],a[212:215],v[184:187],v[252:255]\n"
"v_fma_f32 v221,v221,v210,v216\n"
"v_fma_f32 v222,v222,v210,v216\n"
MF "v[80:83],a[172:175],v[180:183],v[80:83]\n"
"v_fma_f32 v223,v223,v210,v216\n"
"v_fma_f32 v224,v224,v210,v216\n"
MF "v[80:83],a[132:135],v[176:179],v[80:83]\n"
"v_fma_f32 v225,v225,v210,v216\n"
"v_fma_f32 v226,v226,v210,v216\n"
MF "v[88:91],a[136:139],v[176:179],v[252:255]\n"
"v_fma_f32 v227,v227,v210,v216\n"
CV "v220,v220,v221\n"
MF "v[88:91],a[176:179],v[180:183],v[88:91]\n"
CV "v221,v222,v223\n"
CV "v222,v224,v225\n"
MF "v[88:91],a[216:219],v[184:187],v[88:91]\n"
"s_waitcnt vmcnt(0) lgkmcnt(0)\n"
"s_barrier\n"
MF "v[92:95],a[216:219],a[244:247],v[252:255]\n"
TR "v[176:177],v0\n"
CV "v223,v226,v227\n"
MF "v[92:95],a[176:179],a[240:243],v[92:95]\n"
TR "v[178:179],v0 offset:2048\n"
AW "a8,v220\n"
MF "v[92:95],a[136:139],v[188:191],v[92:95]\n"
TR "v[96:97],v0 offset:49152\n"
AW "a9,v221\n"
MF "v[84:87],a[132:135],v[188:191],v[252:255]\n"
TR "v[180:181],v0 offset:4096\n"
AW "a10,v222\n"
MF "v[84:87],a[172:175],a[240:243],v[84:87]\n"
TR "v[182:183],v0 offset:6144\n"
AW "a11,v223\n"
MF "v[84:87],a[212:215],a[244:247],v[84:87]\n"
TR "v[98:99],v0 offset:51200\n"
AR "v228,a48\n"
MF "v[76:79],a[208:211],a[244:247],v[252:255]\n"
TR "v[184:185],v0 offset:8192\n"
AR "v229,a49\n"
MF "v[76:79],a[168:171],a[240:243],v[76:79]\n"
TR "v[186:187],v0 offset:10240\n"
AR "v230,a50\n"
MF "v[76:79],a[128:131],v[188:191],v[76:79]\n"
TR "v[100:101],v0 offset:53248\n"
AR "v231,a51\n"
MF "v[68:71],a[124:127],v[188:191],v[252:255]\n"
".Lpg20h:\n"
"s_cbranch_execz .Lpg24h\n"
TR "v[102:103],v0 offset:55296\n"
AR "v232,a68\n"
MF "v[68:71],a[164:167],a[240:243],v[68:71]\n"
TR "v[104:105],v0 offset:57344\n"
AR "v233,a69\n"
MF "v[68:71],a[204:207],a[244:247],v[68:71]\n"
TR "v[106:107],v0 offset:59392\n"
AR "v234,a70\n"
MF "v[60:63],a[200:203],a[244:247],v[252:255]\n"
TR "v[108:109],v4\n"
AR "v235,a71\n"
MF "v[60:63],a[160:163],a[240:243],v[60:63]\n"
"s_nop 0\n"
TR "v[110:111],v4 offset:2048\n"
"ds_read_b128 a[68:71],v236 offset:53248\n"
MF "v[60:63],a[120:123],v[188:191],v[60:63]\n"
"s_waitcnt lgkmcnt(7)\n"
TR "v[112:113],v4 offset:4096\n"
"v_fma_f32 v228,v228,v210,v216\n"
MF "v[16:19],a[140:143],v[176:179],v[16:19]\n"
TR "v[188:189],v1\n"
"v_fma_f32 v229,v229,v210,v216\n"
MF "v[16:19],a[180:183],v[180:183],v[16:19]\n"
TR "v[190:191],v1 offset:2048\n"
"v_fma_f32 v230,v230,v210,v216\n"
MF "v[16:19],a[220:223],v[184:187],v[16:19]\n"
TR "v[114:115],v4 offset:6144\n"
"v_fma_f32 v231,v231,v210,v216\n"
MF "v[24:27],a[224:227],v[184:187],v[24:27]\n"
TR "a[240:241],v1 offset:4096\n"
"v_fma_f32 v232,v232,v210,v216\n"
MF "v[24:27],a[184:187],v[180:183],v[24:27]\n"
TR "a[242:243],v1 offset:6144\n"
"v_fma_f32 v233,v233,v210,v216\n"
MF "v[24:27],a[144:147],v[176:179],v[24:27]\n"
TR "v[116:117],v4 offset:8192\n"
"v_fma_f32 v234,v234,v210,v216\n"
MF "v[32:35],a[148:151],v[176:179],v[32:35]\n"
TR "a[244:245],v1 offset:8192\n"
"v_fma_f32 v235,v235,v210,v216\n"
MF "v[32:35],a[188:191],v[180:183],v[32:35]\n"
TR "a[246:247],v1 offset:10240\n"
CV "v228,v228,v229\n"
MF "v[32:35],a[228:231],v[184:187],v[32:35]\n"
TR "v[118:119],v4 offset:10240\n"
CV "v229,v230,v231\n"
MF "v[40:43],a[232:235],v[184:187],v[40:43]\n"
TR "v[120:121],v1 offset:49152\n"
CV "v230,v232,v233\n"
MF "v[40:43],a[192:195],v[180:183],v[40:43]\n"
TR "v[122:123],v1 offset:51200\n"
CV "v231,v234,v235\n"
MF "v[40:43],a[152:155],v[176:179],v[40:43]\n"
TR "v[124:125],v1 offset:53248\n"
AW "a48,v228\n"
MF "v[48:51],a[156:159],v[176:179],v[48:51]\n"
TR "v[126:127],v1 offset:55296\n"
AW "a49,v229\n"
MF "v[48:51],a[196:199],v[180:183],v[48:51]\n"
TR "v[128:129],v1 offset:57344\n"
AW "a50,v230\n"
MF "v[48:51],a[236:239],v[184:187],v[48:51]\n"
"s_waitcnt lgkmcnt(6)\n"
TR "v[130:131],v1 offset:59392\n"
AW "a51,v231\n"
MF "v[52:55],a[236:239],a[244:247],v[52:55]\n"
TR "v[176:177],v2\n"
AR "v220,a88\n"
MF "v[52:55],a[196:199],a[240:243],v[52:55]\n"
TR "v[178:179],v2 offset:2048\n"
AR "v221,a89\n"
MF "v[52:55],a[156:159],v[188:191],v[52:55]\n"
TR "v[132:133],v5\n"
AR "v222,a90\n"
MF "v[44:47],a[152:155],v[188:191],v[44:47]\n"
TR "v[180:181],v2 offset:4096\n"
AR "v223,a91\n"
MF "v[44:47],a[192:195],a[240:243],v[44:47]\n"
TR "v[182:183],v2 offset:6144\n"
AR "v224,a108\n"
MF "v[44:47],a[232:235],a[244:247],v[44:47]\n"
TR "v[134:135],v5 offset:2048\n"
AR "v225,a109\n"
MF "v[36:39],a[228:231],a[244:247],v[36:39]\n"
TR "v[184:185],v2 offset:8192\n"
AR "v226,a110\n"
MF "v[36:39],a[188:191],a[240:243],v[36:39]\n"
TR "v[186:187],v2 offset:10240\n"
AR "v227,a111\n"
MF "v[36:39],a[148:151],v[188:191],v[36:39]\n"
"s_nop 0\n"
TR "v[136:137],v5 offset:4096\n"
"ds_read_b128 a[108:111],v237 offset:8192\n"
MF "v[28:31],a[144:147],v[188:191],v[28:31]\n"
TR "v[138:139],v5 offset:6144\n"
"v_fma_f32 v220,v220,v210,v216\n"
MF "v[28:31],a[184:187],a[240:243],v[28:31]\n"
TR "v[140:141],v5 offset:8192\n"
"v_fma_f32 v221,v221,v210,v216\n"
MF "v[28:31],a[224:227],a[244:247],v[28:31]\n"
TR "v[142:143],v5 offset:10240\n"
"v_fma_f32 v222,v222,v210,v216\n"
MF "v[20:23],a[220:223],a[244:247],v[20:23]\n"
TR "v[144:145],v2 offset:49152\n"
"v_fma_f32 v223,v223,v210,v216\n"
MF "v[20:23],a[180:183],a[240:243],v[20:23]\n"
TR "v[146:147],v2 offset:51200\n"
"v_fma_f32 v224,v224,v210,v216\n"
MF "v[20:23],a[140:143],v[188:191],v[20:23]\n"
"s_waitcnt lgkmcnt(7)\n"
TR "v[148:149],v2 offset:53248\n"
"v_fma_f32 v225,v225,v210,v216\n"
MF "v[56:59],a[140:143],v[176:179],v[56:59]\n"
TR "v[188:189],v3\n"
"v_add_u32 v198,59392,v192\n"
MF "v[56:59],a[180:183],v[180:183],v[56:59]\n"
TR "v[190:191],v3 offset:2048\n"
"v_add_u32 v199,68608,v192\n"
MF "v[56:59],a[220:223],v[184:187],v[56:59]\n"
TR "v[150:151],v2 offset:55296\n"
"v_add_u32 v200,81920,v192\n"
"v_add_u32 v201,91136,v192\n"
MF "v[64:67],a[224:227],v[184:187],v[64:67]\n"
TR "a[240:241],v3 offset:4096\n"
"v_add_u32 v202,100352,v192\n"
MF "v[64:67],a[184:187],v[180:183],v[64:67]\n"
TR "a[242:243],v3 offset:6144\n"
"v_add_u32 v203,109568,v192\n"
MF "v[64:67],a[144:147],v[176:179],v[64:67]\n"
TR "v[152:153],v2 offset:57344\n"
"v_fma_f32 v226,v226,v210,v216\n"
MF "v[72:75],a[148:151],v[176:179],v[72:75]\n"
TR "a[244:245],v3 offset:8192\n"
"v_fma_f32 v227,v227,v210,v216\n"
MF "v[72:75],a[188:191],v[180:183],v[72:75]\n"
TR "a[246:247],v3 offset:10240\n"
CV "v220,v220,v221\n"
MF "v[72:75],a[228:231],v[184:187],v[72:75]\n"
TR "v[154:155],v2 offset:59392\n"
CV "v221,v222,v223\n"
MF "v[80:83],a[232:235],v[184:187],v[80:83]\n"
TR "v[156:157],v6\n"
CV "v222,v224,v225\n"
MF "v[80:83],a[192:195],v[180:183],v[80:83]\n"
TR "v[158:159],v6 offset:2048\n"
CV "v223,v226,v227\n"
MF "v[80:83],a[152:155],v[176:179],v[80:83]\n"
TR "v[160:161],v6 offset:4096\n"
AW "a88,v220\n"
MF "v[88:91],a[156:159],v[176:179],v[88:91]\n"
TR "v[162:163],v6 offset:6144\n"
AW "a89,v221\n"
MF "v[88:91],a[196:199],v[180:183],v[88:91]\n"
TR "v[164:165],v6 offset:8192\n"
AW "a90,v222\n"
MF "v[88:91],a[236:239],v[184:187],v[88:91]\n"
"s_waitcnt lgkmcnt(0)\n"
"s_barrier\n"
MF "v[92:95],a[236:239],a[244:247],v[92:95]\n"
"s_add_u32 s80,s89,122880\n"
"s_add_u32 m0,s79,0\n"
TR "v[166:167],v6 offset:10240\n"
MF "v[92:95],a[196:199],a[240:243],v[92:95]\n"
LD "v192,s[48:51],s80 offen lds\n"
MF "v[92:95],a[156:159],v[188:191],v[92:95]\n"
TR "v[168:169],v3 offset:49152\n"
AW "a91,v223\n"
MF "v[84:87],a[152:155],v[188:191],v[84:87]\n"
TR "v[170:171],v3 offset:51200\n"
AR "v228,a12\n"
MF "v[84:87],a[192:195],a[240:243],v[84:87]\n"
TR "v[172:173],v3 offset:53248\n"
AR "v229,a13\n"
MF "v[84:87],a[232:235],a[244:247],v[84:87]\n"
LD "v193,s[48:51],s80 offen offset:1024 lds\n"
MF "v[76:79],a[228:231],a[244:247],v[76:79]\n"
TR "v[174:175],v3 offset:55296\n"
AR "v230,a14\n"
MF "v[76:79],a[188:191],a[240:243],v[76:79]\n"
TR "v[204:205],v3 offset:57344\n"
AR "v231,a15\n"
MF "v[76:79],a[148:151],v[188:191],v[76:79]\n"
TR "v[206:207],v3 offset:59392\n"
AR "v232,a32\n"
MF "v[68:71],a[144:147],v[188:191],v[68:71]\n"
TR "v[244:245],v7\n"
AR "v233,a33\n"
MF "v[68:71],a[184:187],a[240:243],v[68:71]\n"
LD "v194,s[48:51],s80 offen offset:2048 lds\n"
MF "v[68:71],a[224:227],a[244:247],v[68:71]\n"
TR "v[246:247],v7 offset:2048\n"
AR "v234,a34\n"
MF "v[60:63],a[220:223],a[244:247],v[60:63]\n"
TR "v[248:249],v7 offset:4096\n"
AR "v235,a35\n"
MF "v[60:63],a[180:183],a[240:243],v[60:63]\n"
"s_nop 0\n"
TR "v[250:251],v7 offset:6144\n"
"ds_read_b128 a[32:35],v236 offset:36864\n"
MF "v[60:63],a[140:143],v[188:191],v[60:63]\n"
LD "v195,s[48:51],s80 offen offset:3072 lds\n"
MF "v[16:19],a[0:3],v[96:99],v[16:19]\n"
TR "v[238:239],v7 offset:8192\n"
"v_fma_f32 v228,v228,v211,v217\n"
MF "v[16:19],a[40:43],v[100:103],v[16:19]\n"
TR "v[240:241],v7 offset:10240\n"
"v_fma_f32 v229,v229,v211,v217\n"
MF "v[16:19],a[80:83],v[104:107],v[16:19]\n"
"v_fma_f32 v230,v230,v211,v217\n"
"v_fma_f32 v231,v231,v211,v217\n"
MF "v[16:19],a[20:23],v[108:111],v[16:19]\n"
"s_add_u32 m0,s79,4096\n"
"v_fma_f32 v232,v232,v211,v217\n"
"v_fma_f32 v233,v233,v211,v217\n"
MF "v[16:19],a[60:63],v[112:115],v[16:19]\n"
LD "v196,s[48:51],s80 offen lds\n"
MF "v[16:19],a[100:103],v[116:119],v[16:19]\n"
"v_fma_f32 v234,v234,v211,v217\n"
"v_fma_f32 v235,v235,v211,v217\n"
MF "v[20:23],a[100:103],v[140:143],v[20:23]\n"
CV "v228,v228,v229\n"
CV "v229,v230,v231\n"
MF "v[20:23],a[60:63],v[136:139],v[20:23]\n"
CV "v230,v232,v233\n"
CV "v231,v234,v235\n"
MF "v[20:23],a[20:23],v[132:135],v[20:23]\n"
LD "v197,s[48:51],s80 offen offset:1024 lds\n"
MF "v[20:23],a[80:83],v[128:131],v[20:23]\n"
AW "a12,v228\n"
AW "a13,v229\n"
MF "v[20:23],a[40:43],v[124:127],v[20:23]\n"
AW "a14,v230\n"
AW "a15,v231\n"
MF "v[20:23],a[0:3],v[120:123],v[20:23]\n"
"s_waitcnt lgkmcnt(13)\n"
AR "v220,a52\n"
AR "v221,a53\n"
MF "v[56:59],a[0:3],v[144:147],v[56:59]\n"
AR "v222,a54\n"
AR "v223,a55\n"
MF "v[56:59],a[40:43],v[148:151],v[56:59]\n"
LD "v198,s[48:51],s80 offen offset:2048 lds\n"
MF "v[56:59],a[80:83],v[152:155],v[56:59]\n"
AR "v224,a72\n"
AR "v225,a73\n"
MF "v[56:59],a[20:23],v[156:159],v[56:59]\n"
AR "v226,a74\n"
AR "v227,a75\n"
MF "v[56:59],a[60:63],v[160:163],v[56:59]\n"
"ds_read_b128 a[72:75],v236 offset:57344\n"
"v_fma_f32 v220,v220,v211,v217\n"
MF "v[56:59],a[100:103],v[164:167],v[56:59]\n"
"s_waitcnt lgkmcnt(1)\n"
"v_fma_f32 v221,v221,v211,v217\n"
"v_fma_f32 v222,v222,v211,v217\n"
MF "v[60:63],a[100:103],v[238:241],v[60:63]\n"
LD "v199,s[48:51],s80 offen offset:3072 lds\n"
MF "v[60:63],a[60:63],v[248:251],v[60:63]\n"
"v_fma_f32 v223,v223,v211,v217\n"
"v_fma_f32 v224,v224,v211,v217\n"
MF "v[60:63],a[20:23],v[244:247],v[60:63]\n"
"v_fma_f32 v225,v225,v211,v217\n"
"v_fma_f32 v226,v226,v211,v217\n"
MF "v[60:63],a[80:83],v[204:207],v[60:63]\n"
"s_add_u32 m0,s79,8192\n"
"v_fma_f32 v227,v227,v211,v217\n"
CV "v220,v220,v221\n"
MF "v[60:63],a[40:43],v[172:175],v[60:63]\n"
LD "v200,s[48:51],s80 offen lds\n"
MF "v[60:63],a[0:3],v[168:171],v[60:63]\n"
CV "v221,v222,v223\n"
CV "v222,v224,v225\n"
MF "v[68:71],a[4:7],v[168:171],v[68:71]\n"
CV "v223,v226,v227\n"
AW "a52,v220\n"
MF "v[68:71],a[44:47],v[172:175],v[68:71]\n"
AW "a53,v221\n"
AW "a54,v222\n"
MF "v[68:71],a[84:87],v[204:207],v[68:71]\n"
AW "a55,v223\n"
AR "v228,a92\n"
MF "v[68:71],a[24:27],v[244:247],v[68:71]\n"
LD "v201,s[48:51],s80 offen offset:1024 lds\n"
MF "v[68:71],a[64:67],v[248:251],v[68:71]\n"
AR "v229,a93\n"
AR "v230,a94\n"
MF "v[68:71],a[104:107],v[238:241],v[68:71]\n"
AR "v231,a95\n"
AR "v232,a112\n"
MF "v[64:67],a[104:107],v[164:167],v[64:67]\n"
"s_waitcnt lgkmcnt(0)\n"
"s_barrier\n"
MF "v[64:67],a[64:67],v[160:163],v[64:67]\n"
LD "v202,s[48:51],s80 offen offset:2048 lds\n"
MF "v[64:67],a[24:27],v[156:159],v[64:67]\n"
AR "v233,a113\n"
AR "v234,a114\n"
MF "v[64:67],a[84:87],v[152:155],v[64:67]\n"
AR "v235,a115\n"
"v_fma_f32 v228,v228,v211,v217\n"
MF "v[64:67],a[44:47],v[148:151],v[64:67]\n"
"ds_read_b128 a[112:115],v237 offset:12288\n"
"v_fma_f32 v229,v229,v211,v217\n"
MF "v[64:67],a[4:7],v[144:147],v[64:67]\n"
"v_fma_f32 v230,v230,v211,v217\n"
"v_fma_f32 v231,v231,v211,v217\n"
MF "v[28:31],a[4:7],v[120:123],v[28:31]\n"
LD "v203,s[48:51],s80 offen offset:3072 lds\n"
MF "v[28:31],a[44:47],v[124:127],v[28:31]\n"
"v_fma_f32 v232,v232,v211,v217\n"
"v_fma_f32 v233,v233,v211,v217\n"
MF "v[28:31],a[84:87],v[128:131],v[28:31]\n"
"v_fma_f32 v234,v234,v211,v217\n"
"v_fma_f32 v235,v235,v211,v217\n"
MF "v[28:31],a[24:27],v[132:135],v[28:31]\n"
CV "v228,v228,v229\n"
CV "v229,v230,v231\n"
MF "v[28:31],a[64:67],v[136:139],v[28:31]\n"
CV "v230,v232,v233\n"
CV "v231,v234,v235\n"
MF "v[28:31],a[104:107],v[140:143],v[28:31]\n"
"s_add_u32 s88,s88,s65\n"
"s_add_u32 m0,s79,49152\n"
AW "a92,v228\n"
MF "v[24:27],a[104:107],v[116:119],v[24:27]\n"
LD "v192,s[48:51],s88 offen lds\n"
MF "v[24:27],a[64:67],v[112:115],v[24:27]\n"
AW "a93,v229\n"
AW "a94,v230\n"
MF "v[24:27],a[24:27],v[108:111],v[24:27]\n"
AW "a95,v231\n"
AR "v220,a16\n"
MF "v[24:27],a[84:87],v[104:107],v[24:27]\n"
AR "v221,a17\n"
AR "v222,a18\n"
MF "v[24:27],a[44:47],v[100:103],v[24:27]\n"
LD "v193,s[48:51],s88 offen offset:1024 lds\n"
MF "v[24:27],a[4:7],v[96:99],v[24:27]\n"
AR "v223,a19\n"
AR "v224,a36\n"
MF "v[32:35],a[8:11],v[96:99],v[32:35]\n"
AR "v225,a37\n"
AR "v226,a38\n"
MF "v[32:35],a[48:51],v[100:103],v[32:35]\n"
AR "v227,a39\n"
"v_fma_f32 v220,v220,v212,v218\n"
MF "v[32:35],a[88:91],v[104:107],v[32:35]\n"
"ds_read_b128 a[36:39],v236 offset:40960\n"
"v_fma_f32 v221,v221,v212,v218\n"
MF "v[32:35],a[28:31],v[108:111],v[32:35]\n"
LD "v194,s[48:51],s88 offen offset:2048 lds\n"
MF "v[32:35],a[68:71],v[112:115],v[32:35]\n"
"v_fma_f32 v222,v222,v212,v218\n"
"v_fma_f32 v223,v223,v212,v218\n"
MF "v[32:35],a[108:111],v[116:119],v[32:35]\n"
"v_fma_f32 v224,v224,v212,v218\n"
"v_fma_f32 v225,v225,v212,v218\n"
MF "v[36:39],a[108:111],v[140:143],v[36:39]\n"
"v_fma_f32 v226,v226,v212,v218\n"
"v_fma_f32 v227,v227,v212,v218\n"
MF "v[36:39],a[68:71],v[136:139],v[36:39]\n"
CV "v220,v220,v221\n"
CV "v221,v222,v223\n"
MF "v[36:39],a[28:31],v[132:135],v[36:39]\n"
LD "v195,s[48:51],s88 offen offset:3072 lds\n"
MF "v[36:39],a[88:91],v[128:131],v[36:39]\n"
CV "v222,v224,v225\n"
CV "v223,v226,v227\n"
MF "v[36:39],a[48:51],v[124:127],v[36:39]\n"
AW "a16,v220\n"
AW "a17,v221\n"
MF "v[36:39],a[8:11],v[120:123],v[36:39]\n"
AW "a18,v222\n"
AW "a19,v223\n"
MF "v[72:75],a[8:11],v[144:147],v[72:75]\n"
"s_add_u32 m0,s79,53248\n"
AR "v228,a56\n"
AR "v229,a57\n"
MF "v[72:75],a[48:51],v[148:151],v[72:75]\n"
LD "v196,s[48:51],s88 offen lds\n"
MF "v[72:75],a[88:91],v[152:155],v[72:75]\n"
AR "v230,a58\n"
AR "v231,a59\n"
MF "v[72:75],a[28:31],v[156:159],v[72:75]\n"
AR "v232,a76\n"
AR "v233,a77\n"
MF "v[72:75],a[68:71],v[160:163],v[72:75]\n"
AR "v234,a78\n"
AR "v235,a79\n"
MF "v[72:75],a[108:111],v[164:167],v[72:75]\n"
"ds_read_b128 a[76:79],v236 offset:61440\n"
"v_fma_f32 v228,v228,v212,v218\n"
MF "v[76:79],a[108:111],v[238:241],v[76:79]\n"
LD "v197,s[48:51],s88 offen offset:1024 lds\n"
MF "v[76:79],a[68:71],v[248:251],v[76:79]\n"
"v_fma_f32 v229,v229,v212,v218\n"
"v_fma_f32 v230,v230,v212,v218\n"
MF "v[76:79],a[28:31],v[244:247],v[76:79]\n"
"v_fma_f32 v231,v231,v212,v218\n"
"v_fma_f32 v232,v232,v212,v218\n"
MF "v[76:79],a[88:91],v[204:207],v[76:79]\n"
"v_fma_f32 v233,v233,v212,v218\n"
"v_fma_f32 v234,v234,v212,v218\n"
MF "v[76:79],a[48:51],v[172:175],v[76:79]\n"
"v_fma_f32 v235,v235,v212,v218\n"
CV "v228,v228,v229\n"
MF "v[76:79],a[8:11],v[168:171],v[76:79]\n"
"s_waitcnt lgkmcnt(2)\n"
LD "v198,s[48:51],s88 offen offset:2048 lds\n"
MF "v[84:87],a[12:15],v[168:171],v[84:87]\n"
CV "v229,v230,v231\n"
CV "v230,v232,v233\n"
MF "v[84:87],a[52:55],v[172:175],v[84:87]\n"
CV "v231,v234,v235\n"
AW "a56,v228\n"
MF "v[84:87],a[92:95],v[204:207],v[84:87]\n"
AW "a57,v229\n"
AW "a58,v230\n"
MF "v[84:87],a[32:35],v[244:247],v[84:87]\n"
AW "a59,v231\n"
AR "v220,a96\n"
MF "v[84:87],a[72:75],v[248:251],v[84:87]\n"
LD "v199,s[48:51],s88 offen offset:3072 lds\n"
MF "v[84:87],a[112:115],v[238:241],v[84:87]\n"
AR "v221,a97\n"
AR "v222,a98\n"
MF "v[80:83],a[112:115],v[164:167],v[80:83]\n"
AR "v223,a99\n"
AR "v224,a116\n"
MF "v[80:83],a[72:75],v[160:163],v[80:83]\n"
AR "v225,a117\n"
AR "v226,a118\n"
MF "v[80:83],a[32:35],v[156:159],v[80:83]\n"
"s_add_u32 m0,s79,57344\n"
AR "v227,a119\n"
"v_fma_f32 v220,v220,v212,v218\n"
MF "v[80:83],a[92:95],v[152:155],v[80:83]\n"
LD "v200,s[48:51],s88 offen lds\n"
MF "v[80:83],a[52:55],v[148:151],v[80:83]\n"
"ds_read_b128 a[116:119],v237 offset:16384\n"
"v_fma_f32 v221,v221,v212,v218\n"
MF "v[80:83],a[12:15],v[144:147],v[80:83]\n"
"v_fma_f32 v222,v222,v212,v218\n"
"v_fma_f32 v223,v223,v212,v218\n"
MF "v[44:47],a[12:15],v[120:123],v[44:47]\n"
"v_fma_f32 v224,v224,v212,v218\n"
"v_fma_f32 v225,v225,v212,v218\n"
MF "v[44:47],a[52:55],v[124:127],v[44:47]\n"
"v_fma_f32 v226,v226,v212,v218\n"
"v_fma_f32 v227,v227,v212,v218\n"
MF "v[44:47],a[92:95],v[128:131],v[44:47]\n"
LD "v201,s[48:51],s88 offen offset:1024 lds\n"
MF "v[44:47],a[32:35],v[132:135],v[44:47]\n"
CV "v220,v220,v221\n"
CV "v221,v222,v223\n"
MF "v[44:47],a[72:75],v[136:139],v[44:47]\n"
CV "v222,v224,v225\n"
CV "v223,v226,v227\n"
MF "v[44:47],a[112:115],v[140:143],v[44:47]\n"
AW "a96,v220\n"
AW "a97,v221\n"
MF "v[40:43],a[112:115],v[116:119],v[40:43]\n"
AW "a98,v222\n"
AW "a99,v223\n"
MF "v[40:43],a[72:75],v[112:115],v[40:43]\n"
LD "v202,s[48:51],s88 offen offset:2048 lds\n"
MF "v[40:43],a[32:35],v[108:111],v[40:43]\n"
AR "v8,a252\n"
AR "v11,a253\n"
MF "v[40:43],a[92:95],v[104:107],v[40:43]\n"
"v_bfe_u32 v224,v9,5,3\n"
"v_bfe_u32 v225,v9,8,2\n"
MF "v[40:43],a[52:55],v[100:103],v[40:43]\n"
"v_lshlrev_b32 v225,1,v225\n"
"v_and_b32 v226,16,v9\n"
MF "v[40:43],a[12:15],v[96:99],v[40:43]\n"
"s_waitcnt lgkmcnt(0)\n"
"v_lshlrev_b32_e64 v226,v226,s9\n"
"v_sub_u32 v224,v224,v225\n"
MF "v[48:51],a[16:19],v[96:99],v[48:51]\n"
LD "v203,s[48:51],s88 offen offset:3072 lds\n"
MF "v[48:51],a[56:59],v[100:103],v[48:51]\n"
"v_cmp_eq_u32 vcc,0,v224\n"
MF "v[48:51],a[96:99],v[104:107],v[48:51]\n"
"s_nop 0\n"
"v_cndmask_b32 v220,0,v226,vcc\n"
"v_cmp_eq_u32 vcc,1,v224\n"
MF "v[48:51],a[36:39],v[108:111],v[48:51]\n"
"s_nop 0\n"
"v_cndmask_b32 v221,0,v226,vcc\n"
AW "a248,0\n"
MF "v[48:51],a[76:79],v[112:115],v[48:51]\n"
AW "a249,0\n"
AW "a250,v220\n"
MF "v[48:51],a[116:119],v[116:119],v[48:51]\n"
AW "a251,v221\n"
AW "a252,v220\n"
MF "v[52:55],a[116:119],v[140:143],v[52:55]\n"
AW "a253,v221\n"
AW "a254,0\n"
MF "v[52:55],a[76:79],v[136:139],v[52:55]\n"
AW "a255,0\n"
"v_bfe_u32 v12,v9,4,4\n"
MF "v[52:55],a[36:39],v[132:135],v[52:55]\n"
"s_waitcnt lgkmcnt(0)\n"
"s_barrier\n"
MF "v[52:55],a[96:99],v[128:131],v[52:55]\n"
"s_add_u32 s80,s88,122880\n"
TR "v[176:177],v0 offset:24576\n"
"v_lshlrev_b32 v12,2,v12\n"
MF "v[52:55],a[56:59],v[124:127],v[52:55]\n"
"s_add_u32 m0,s79,98304\n"
TR "v[178:179],v0 offset:26624\n"
"v_lshrrev_b32 v13,2,v9\n"
MF "v[52:55],a[16:19],v[120:123],v[52:55]\n"
LD "v192,s[48:51],s80 offen lds\n"
MF "v[88:91],a[16:19],v[144:147],v[88:91]\n"
"s_nop 0\n"
TR "v[180:181],v0 offset:28672\n"
"ds_write_addtid_b32 v16 offset:24576\n"
MF "v[88:91],a[56:59],v[148:151],v[88:91]\n"
"s_nop 0\n"
TR "v[182:183],v0 offset:30720\n"
"ds_write_addtid_b32 v17 offset:24832\n"
MF "v[88:91],a[96:99],v[152:155],v[88:91]\n"
LD "v193,s[48:51],s80 offen offset:1024 lds\n"
MF "v[88:91],a[36:39],v[156:159],v[88:91]\n"
".Lpg21h:\n"
"s_cbranch_execz .Lpgrole1_h\n"
"s_nop 0\n"
TR "v[184:185],v0 offset:32768\n"
"ds_write_addtid_b32 v18 offset:25088\n"
MF "v[88:91],a[76:79],v[160:163],v[88:91]\n"
"s_nop 0\n"
TR "v[186:187],v0 offset:34816\n"
"ds_write_addtid_b32 v19 offset:25344\n"
MF "v[88:91],a[116:119],v[164:167],v[88:91]\n"
"s_nop 0\n"
"ds_write_addtid_b32 v20 offset:25600\n"
"ds_write_addtid_b32 v21 offset:25856\n"
MF "v[92:95],a[116:119],v[238:241],v[92:95]\n"
LD "v194,s[48:51],s80 offen offset:2048 lds\n"
MF "v[92:95],a[76:79],v[248:251],v[92:95]\n"
"s_nop 0\n"
"ds_write_addtid_b32 v22 offset:26112\n"
"ds_write_addtid_b32 v23 offset:26368\n"
MF "v[92:95],a[36:39],v[244:247],v[92:95]\n"
"s_nop 0\n"
"ds_write_addtid_b32 v24 offset:26624\n"
"ds_write_addtid_b32 v25 offset:26880\n"
MF "v[92:95],a[96:99],v[204:207],v[92:95]\n"
"s_nop 0\n"
"ds_write_addtid_b32 v26 offset:27136\n"
"ds_write_addtid_b32 v27 offset:27392\n"
MF "v[92:95],a[56:59],v[172:175],v[92:95]\n"
"v_add_u32 v13,s58,v13\n"
"v_add_u32 v4,24576,v4\n"
MF "v[92:95],a[16:19],v[168:171],v[92:95]\n"
"s_waitcnt lgkmcnt(9)\n"
"v_add_u32 v5,24576,v5\n"
"v_add_u32 v6,24576,v6\n"
"v_add_u32 v7,24576,v7\n"
".Lpl0:\n"
MF "v[96:99],a[0:3],v[176:179],v[252:255]\n"
"s_nop 0\n"
TR "v[188:189],v1 offset:24576\n"
"ds_write_addtid_b32 v28 offset:27648\n"
MF "v[96:99],a[40:43],v[180:183],v[96:99]\n"
"s_nop 0\n"
TR "v[190:191],v1 offset:26624\n"
"ds_write_addtid_b32 v29 offset:27904\n"
MF "v[96:99],a[80:83],v[184:187],v[96:99]\n"
LD "v195,s[48:51],s80 offen offset:3072 lds\n"
MF "v[104:107],a[84:87],v[184:187],v[252:255]\n"
"s_add_u32 s60,s64,1\n"
TR "a[240:241],v1 offset:28672\n"
"ds_write_addtid_b32 v30 offset:28160\n"
MF "v[104:107],a[44:47],v[180:183],v[104:107]\n"
"s_add_u32 m0,s79,102400\n"
TR "a[242:243],v1 offset:30720\n"
"ds_write_addtid_b32 v31 offset:24320\n"
MF "v[104:107],a[4:7],v[176:179],v[104:107]\n"
LD "v196,s[48:51],s80 offen lds\n"
MF "v[112:115],a[8:11],v[176:179],v[252:255]\n"
"s_cmp_eq_u32 s60,8\n"
"s_cselect_b32 s60,0,s60\n"
TR "a[244:245],v1 offset:32768\n"
MF "v[112:115],a[48:51],v[180:183],v[112:115]\n"
"s_cmp_eq_u32 s60,10\n"
"s_cselect_b32 s60,s61,s60\n"
TR "a[246:247],v1 offset:34816\n"
MF "v[112:115],a[88:91],v[184:187],v[112:115]\n"
"s_sub_u32 s65,s60,s64\n"
"ds_write_addtid_b32 v32 offset:24576\n"
"ds_write_addtid_b32 v33 offset:24832\n"
MF "v[120:123],a[92:95],v[184:187],v[252:255]\n"
"s_lshl_b32 s65,s65,7\n"
"ds_write_addtid_b32 v34 offset:25088\n"
"ds_write_addtid_b32 v35 offset:25344\n"
MF "v[120:123],a[52:55],v[180:183],v[120:123]\n"
LD "v197,s[48:51],s80 offen offset:1024 lds\n"
MF "v[120:123],a[12:15],v[176:179],v[120:123]\n"
"s_mov_b32 s64,s60\n"
"ds_write_addtid_b32 v36 offset:25600\n"
"ds_write_addtid_b32 v37 offset:25856\n"
MF "v[128:131],a[16:19],v[176:179],v[252:255]\n"
"s_nop 0\n"
"ds_write_addtid_b32 v38 offset:26112\n"
"ds_write_addtid_b32 v39 offset:26368\n"
MF "v[128:131],a[56:59],v[180:183],v[128:131]\n"
"s_nop 0\n"
"ds_write_addtid_b32 v40 offset:26624\n"
"ds_write_addtid_b32 v41 offset:26880\n"
MF "v[128:131],a[96:99],v[184:187],v[128:131]\n"
"s_waitcnt lgkmcnt(10)\n"
LD "v198,s[48:51],s80 offen offset:2048 lds\n"
MF "v[132:135],a[96:99],a[244:247],v[252:255]\n"
"s_nop 0\n"
TR "v[176:177],v2 offset:24576\n"
"ds_write_addtid_b32 v42 offset:27136\n"
MF "v[132:135],a[56:59],a[240:243],v[132:135]\n"
"s_nop 0\n"
TR "v[178:179],v2 offset:26624\n"
"ds_write_addtid_b32 v43 offset:27392\n"
MF "v[132:135],a[16:19],v[188:191],v[132:135]\n"
"s_nop 0\n"
"ds_write_addtid_b32 v44 offset:27648\n"
"ds_write_addtid_b32 v45 offset:27904\n"
MF "v[124:127],a[12:15],v[188:191],v[252:255]\n"
"s_nop 0\n"
TR "v[180:181],v2 offset:28672\n"
"ds_write_addtid_b32 v46 offset:28160\n"
MF "v[124:127],a[52:55],a[240:243],v[124:127]\n"
"s_nop 0\n"
TR "v[182:183],v2 offset:30720\n"
"ds_write_addtid_b32 v47 offset:28416\n"
MF "v[124:127],a[92:95],a[244:247],v[124:127]\n"
LD "v199,s[48:51],s80 offen offset:3072 lds\n"
MF "v[116:119],a[88:91],a[244:247],v[252:255]\n"
"s_nop 0\n"
TR "v[184:185],v2 offset:32768\n"
"ds_write_addtid_b32 v48 offset:28672\n"
MF "v[116:119],a[48:51],a[240:243],v[116:119]\n"
"s_add_u32 m0,s79,106496\n"
TR "v[186:187],v2 offset:34816\n"
"ds_write_addtid_b32 v49 offset:24832\n"
MF "v[116:119],a[8:11],v[188:191],v[116:119]\n"
LD "v200,s[48:51],s80 offen lds\n"
MF "v[108:111],a[4:7],v[188:191],v[252:255]\n"
"s_nop 0\n"
"ds_write_addtid_b32 v50 offset:25088\n"
"ds_write_addtid_b32 v51 offset:25344\n"
MF "v[108:111],a[44:47],a[240:243],v[108:111]\n"
"s_nop 0\n"
"ds_write_addtid_b32 v52 offset:25600\n"
"ds_write_addtid_b32 v53 offset:25856\n"
MF "v[108:111],a[84:87],a[244:247],v[108:111]\n"
"s_nop 0\n"
"ds_write_addtid_b32 v54 offset:26112\n"
"ds_write_addtid_b32 v55 offset:26368\n"
MF "v[100:103],a[80:83],a[244:247],v[252:255]\n"
MF "v[100:103],a[40:43],a[240:243],v[100:103]\n"
LD "v201,s[48:51],s80 offen offset:1024 lds\n"
MF "v[100:103],a[0:3],v[188:191],v[100:103]\n"
"s_waitcnt lgkmcnt(7)\n"
MF "v[136:139],a[0:3],v[176:179],v[252:255]\n"
TR "v[188:189],v3 offset:24576\n"
MF "v[136:139],a[40:43],v[180:183],v[136:139]\n"
TR "v[190:191],v3 offset:26624\n"
MF "v[136:139],a[80:83],v[184:187],v[136:139]\n"
LD "v202,s[48:51],s80 offen offset:2048 lds\n"
MF "v[144:147],a[84:87],v[184:187],v[252:255]\n"
TR "a[240:241],v3 offset:28672\n"
MF "v[144:147],a[44:47],v[180:183],v[144:147]\n"
TR "a[242:243],v3 offset:30720\n"
MF "v[144:147],a[4:7],v[176:179],v[144:147]\n"
LD "v203,s[48:51],s80 offen offset:3072 lds\n"
MF "v[152:155],a[8:11],v[176:179],v[252:255]\n"
TR "a[244:245],v3 offset:32768\n"
MF "v[152:155],a[48:51],v[180:183],v[152:155]\n"
TR "a[246:247],v3 offset:34816\n"
MF "v[152:155],a[88:91],v[184:187],v[152:155]\n"
MF "v[160:163],a[92:95],v[184:187],v[252:255]\n"
MF "v[160:163],a[52:55],v[180:183],v[160:163]\n"
MF "v[160:163],a[12:15],v[176:179],v[160:163]\n"
MF "v[168:171],a[16:19],v[176:179],v[252:255]\n"
MF "v[168:171],a[56:59],v[180:183],v[168:171]\n"
MF "v[168:171],a[96:99],v[184:187],v[168:171]\n"
"s_waitcnt vmcnt(24) lgkmcnt(0)\n"
"s_barrier\n"
MF "v[172:175],a[96:99],a[244:247],v[252:255]\n"
"s_add_u32 s89,s89,s65\n"
TR "v[176:177],v0\n"
MF "v[172:175],a[56:59],a[240:243],v[172:175]\n"
"s_add_u32 m0,s79,24576\n"
TR "v[178:179],v0 offset:2048\n"
MF "v[172:175],a[16:19],v[188:191],v[172:175]\n"
LD "v192,s[48:51],s89 offen lds\n"
MF "v[164:167],a[12:15],v[188:191],v[252:255]\n"
TR "v[180:181],v0 offset:4096\n"
MF "v[164:167],a[52:55],a[240:243],v[164:167]\n"
TR "v[182:183],v0 offset:6144\n"
MF "v[164:167],a[92:95],a[244:247],v[164:167]\n"
LD "v193,s[48:51],s89 offen offset:1024 lds\n"
MF "v[156:159],a[88:91],a[244:247],v[252:255]\n"
TR "v[184:185],v0 offset:8192\n"
MF "v[156:159],a[48:51],a[240:243],v[156:159]\n"
"s_nop 0\n"
TR "v[186:187],v0 offset:10240\n"
"ds_write_addtid_b32 v56 offset:49152\n"
MF "v[156:159],a[8:11],v[188:191],v[156:159]\n"
"s_nop 0\n"
"ds_write_addtid_b32 v57 offset:49408\n"
"ds_write_addtid_b32 v58 offset:49664\n"
MF "v[148:151],a[4:7],v[188:191],v[252:255]\n"
LD "v194,s[48:51],s89 offen offset:2048 lds\n"
MF "v[148:151],a[44:47],a[240:243],v[148:151]\n"
"buffer_load_dword v10,v12,s[40:43],s86 offen sc1\n"
MF "v[148:151],a[84:87],a[244:247],v[148:151]\n"
"s_nop 0\n"
"ds_write_addtid_b32 v59 offset:49920\n"
"ds_write_addtid_b32 v60 offset:50176\n"
MF "v[140:143],a[80:83],a[244:247],v[252:255]\n"
"s_nop 0\n"
"ds_write_addtid_b32 v61 offset:50432\n"
"ds_write_addtid_b32 v62 offset:50688\n"
MF "v[140:143],a[40:43],a[240:243],v[140:143]\n"
"s_nop 0\n"
"ds_write_addtid_b32 v63 offset:50944\n"
"ds_write_addtid_b32 v64 offset:51200\n"
MF "v[140:143],a[0:3],v[188:191],v[140:143]\n"
"s_waitcnt lgkmcnt(9)\n"
LD "v195,s[48:51],s89 offen offset:3072 lds\n"
MF "v[96:99],a[120:123],v[176:179],v[96:99]\n"
"s_nop 0\n"
TR "v[188:189],v1\n"
"ds_write_addtid_b32 v65 offset:51456\n"
MF "v[96:99],a[160:163],v[180:183],v[96:99]\n"
"s_nop 0\n"
TR "v[190:191],v1 offset:2048\n"
"ds_write_addtid_b32 v66 offset:51712\n"
MF "v[96:99],a[200:203],v[184:187],v[96:99]\n"
"s_nop 0\n"
"ds_write_addtid_b32 v67 offset:51968\n"
"ds_write_addtid_b32 v68 offset:52224\n"
MF "v[104:107],a[204:207],v[184:187],v[104:107]\n"
"s_nop 0\n"
TR "a[240:241],v1 offset:4096\n"
"ds_write_addtid_b32 v69 offset:52480\n"
MF "v[104:107],a[164:167],v[180:183],v[104:107]\n"
"s_add_u32 m0,s79,28672\n"
TR "a[242:243],v1 offset:6144\n"
"ds_write_addtid_b32 v70 offset:48640\n"
MF "v[104:107],a[124:127],v[176:179],v[104:107]\n"
LD "v196,s[48:51],s89 offen lds\n"
MF "v[112:115],a[128:131],v[176:179],v[112:115]\n"
"s_nop 0\n"
TR "a[244:245],v1 offset:8192\n"
"ds_write_addtid_b32 v71 offset:48896\n"
MF "v[112:115],a[168:171],v[180:183],v[112:115]\n"
"s_nop 0\n"
TR "a[246:247],v1 offset:10240\n"
"ds_write_addtid_b32 v72 offset:49152\n"
MF "v[112:115],a[208:211],v[184:187],v[112:115]\n"
LD "v197,s[48:51],s89 offen offset:1024 lds\n"
MF "v[120:123],a[212:215],v[184:187],v[120:123]\n"
"s_nop 0\n"
"ds_write_addtid_b32 v73 offset:49408\n"
"ds_write_addtid_b32 v74 offset:49664\n"
MF "v[120:123],a[172:175],v[180:183],v[120:123]\n"
"s_nop 0\n"
"ds_write_addtid_b32 v75 offset:49920\n"
"ds_write_addtid_b32 v76 offset:50176\n"
MF "v[120:123],a[132:135],v[176:179],v[120:123]\n"
"s_nop 0\n"
"ds_write_addtid_b32 v77 offset:50432\n"
"ds_write_addtid_b32 v78 offset:50688\n"
MF "v[128:131],a[136:139],v[176:179],v[128:131]\n"
"s_nop 0\n"
"ds_write_addtid_b32 v79 offset:50944\n"
"ds_write_addtid_b32 v80 offset:51200\n"
MF "v[128:131],a[176:179],v[180:183],v[128:131]\n"
LD "v198,s[48:51],s89 offen offset:2048 lds\n"
MF "v[128:131],a[216:219],v[184:187],v[128:131]\n"
"s_waitcnt lgkmcnt(9)\n"
"ds_write_addtid_b32 v81 offset:51456\n"
"ds_write_addtid_b32 v82 offset:51712\n"
MF "v[132:135],a[216:219],a[244:247],v[132:135]\n"
"s_nop 0\n"
TR "v[176:177],v2\n"
"ds_write_addtid_b32 v83 offset:51968\n"
MF "v[132:135],a[176:179],a[240:243],v[132:135]\n"
"s_nop 0\n"
TR "v[178:179],v2 offset:2048\n"
"ds_write_addtid_b32 v84 offset:52224\n"
MF "v[132:135],a[136:139],v[188:191],v[132:135]\n"
LD "v199,s[48:51],s89 offen offset:3072 lds\n"
MF "v[124:127],a[132:135],v[188:191],v[124:127]\n"
"s_nop 0\n"
TR "v[180:181],v2 offset:4096\n"
"ds_write_addtid_b32 v85 offset:52480\n"
MF "v[124:127],a[172:175],a[240:243],v[124:127]\n"
"s_nop 0\n"
TR "v[182:183],v2 offset:6144\n"
"ds_write_addtid_b32 v86 offset:52736\n"
MF "v[124:127],a[212:215],a[244:247],v[124:127]\n"
"s_nop 0\n"
"ds_write_addtid_b32 v87 offset:52992\n"
"ds_write_addtid_b32 v88 offset:53248\n"
MF "v[116:119],a[208:211],a[244:247],v[116:119]\n"
"s_nop 0\n"
TR "v[184:185],v2 offset:8192\n"
"ds_write_addtid_b32 v89 offset:53504\n"
MF "v[116:119],a[168:171],a[240:243],v[116:119]\n"
"s_add_u32 m0,s79,32768\n"
TR "v[186:187],v2 offset:10240\n"
"ds_write_addtid_b32 v90 offset:49664\n"
MF "v[116:119],a[128:131],v[188:191],v[116:119]\n"
LD "v200,s[48:51],s89 offen lds\n"
MF "v[108:111],a[124:127],v[188:191],v[108:111]\n"
"s_nop 0\n"
"ds_write_addtid_b32 v91 offset:49920\n"
"ds_write_addtid_b32 v92 offset:50176\n"
MF "v[108:111],a[164:167],a[240:243],v[108:111]\n"
"s_nop 0\n"
"ds_write_addtid_b32 v93 offset:50432\n"
"ds_write_addtid_b32 v94 offset:50688\n"
MF "v[108:111],a[204:207],a[244:247],v[108:111]\n"
"ds_write_addtid_b32 v95 offset:50944\n"
MF "v[100:103],a[200:203],a[244:247],v[100:103]\n"
LD "v201,s[48:51],s89 offen offset:1024 lds\n"
MF "v[100:103],a[160:163],a[240:243],v[100:103]\n"
MF "v[100:103],a[120:123],v[188:191],v[100:103]\n"
"s_waitcnt lgkmcnt(6)\n"
MF "v[136:139],a[120:123],v[176:179],v[136:139]\n"
TR "v[188:189],v3\n"
MF "v[136:139],a[160:163],v[180:183],v[136:139]\n"
TR "v[190:191],v3 offset:2048\n"
MF "v[136:139],a[200:203],v[184:187],v[136:139]\n"
LD "v202,s[48:51],s89 offen offset:2048 lds\n"
MF "v[144:147],a[204:207],v[184:187],v[144:147]\n"
TR "a[240:241],v3 offset:4096\n"
MF "v[144:147],a[164:167],v[180:183],v[144:147]\n"
TR "a[242:243],v3 offset:6144\n"
MF "v[144:147],a[124:127],v[176:179],v[144:147]\n"
LD "v203,s[48:51],s89 offen offset:3072 lds\n"
MF "v[152:155],a[128:131],v[176:179],v[152:155]\n"
TR "a[244:245],v3 offset:8192\n"
MF "v[152:155],a[168:171],v[180:183],v[152:155]\n"
TR "a[246:247],v3 offset:10240\n"
MF "v[152:155],a[208:211],v[184:187],v[152:155]\n"
MF "v[160:163],a[212:215],v[184:187],v[160:163]\n"
MF "v[160:163],a[172:175],v[180:183],v[160:163]\n"
MF "v[160:163],a[132:135],v[176:179],v[160:163]\n"
MF "v[168:171],a[136:139],v[176:179],v[168:171]\n"
MF "v[168:171],a[176:179],v[180:183],v[168:171]\n"
MF "v[168:171],a[216:219],v[184:187],v[168:171]\n"
"s_waitcnt vmcnt(25) lgkmcnt(0)\n"
"s_barrier\n"
MF "v[172:175],a[216:219],a[244:247],v[172:175]\n"
"s_add_u32 s80,s89,122880\n"
TR "v[176:177],v0 offset:49152\n"
MF "v[172:175],a[176:179],a[240:243],v[172:175]\n"
"s_add_u32 m0,s79,0\n"
"s_mul_i32 s95,s92,3277\n"
TR "v[178:179],v0 offset:51200\n"
MF "v[172:175],a[136:139],v[188:191],v[172:175]\n"
LD "v192,s[48:51],s80 offen lds\n"
MF "v[164:167],a[132:135],v[188:191],v[164:167]\n"
"s_lshr_b32 s95,s95,16\n"
"s_mul_i32 s59,s95,20\n"
TR "v[180:181],v0 offset:53248\n"
MF "v[164:167],a[172:175],a[240:243],v[164:167]\n"
"s_sub_u32 s59,s92,s59\n"
TR "v[182:183],v0 offset:55296\n"
MF "v[164:167],a[212:215],a[244:247],v[164:167]\n"
LD "v193,s[48:51],s80 offen offset:1024 lds\n"
MF "v[156:159],a[208:211],a[244:247],v[156:159]\n"
"s_cmp_ge_u32 s59,10\n"
"s_cselect_b32 s96,4294557760,0\n"
TR "v[184:185],v0 offset:57344\n"
MF "v[156:159],a[168:171],a[240:243],v[156:159]\n"
"s_add_u32 s96,s96,1024\n"
"s_mul_i32 s59,s59,40960\n"
TR "v[186:187],v0 offset:59392\n"
MF "v[156:159],a[128:131],v[188:191],v[156:159]\n"
"s_add_u32 s96,s96,s59\n"
"s_lshl_b32 s59,s95,7\n"
"s_add_u32 s96,s96,s59\n"
MF "v[148:151],a[124:127],v[188:191],v[148:151]\n"
"s_add_u32 s95,s95,3\n"
"s_cmp_ge_u32 s92,40\n"
"s_cselect_b32 s95,99,s95\n"
MF "v[148:151],a[164:167],a[240:243],v[148:151]\n"
LD "v194,s[48:51],s80 offen offset:2048 lds\n"
MF "v[148:151],a[204:207],a[244:247],v[148:151]\n"
"s_cmp_ge_u32 s85,5\n"
"s_cselect_b32 s95,99,s95\n"
"s_cselect_b32 s72,s85,0\n"
MF "v[140:143],a[200:203],a[244:247],v[140:143]\n"
MF "v[140:143],a[160:163],a[240:243],v[140:143]\n"
MF "v[140:143],a[120:123],v[188:191],v[140:143]\n"
"s_waitcnt lgkmcnt(0)\n"
LD "v195,s[48:51],s80 offen offset:3072 lds\n"
MF "v[96:99],a[20:23],v[176:179],v[96:99]\n"
TR "v[188:189],v1 offset:49152\n"
MF "v[96:99],a[60:63],v[180:183],v[96:99]\n"
TR "v[190:191],v1 offset:51200\n"
MF "v[96:99],a[100:103],v[184:187],v[96:99]\n"
MF "v[104:107],a[104:107],v[184:187],v[104:107]\n"
TR "a[240:241],v1 offset:53248\n"
MF "v[104:107],a[64:67],v[180:183],v[104:107]\n"
"s_add_u32 m0,s79,4096\n"
TR "a[242:243],v1 offset:55296\n"
MF "v[104:107],a[24:27],v[176:179],v[104:107]\n"
LD "v196,s[48:51],s80 offen lds\n"
MF "v[112:115],a[28:31],v[176:179],v[112:115]\n"
TR "a[244:245],v1 offset:57344\n"
MF "v[112:115],a[68:71],v[180:183],v[112:115]\n"
TR "a[246:247],v1 offset:59392\n"
MF "v[112:115],a[108:111],v[184:187],v[112:115]\n"
LD "v197,s[48:51],s80 offen offset:1024 lds\n"
MF "v[120:123],a[112:115],v[184:187],v[120:123]\n"
MF "v[120:123],a[72:75],v[180:183],v[120:123]\n"
MF "v[120:123],a[32:35],v[176:179],v[120:123]\n"
MF "v[128:131],a[36:39],v[176:179],v[128:131]\n"
MF "v[128:131],a[76:79],v[180:183],v[128:131]\n"
LD "v198,s[48:51],s80 offen offset:2048 lds\n"
MF "v[128:131],a[116:119],v[184:187],v[128:131]\n"
"s_waitcnt lgkmcnt(0)\n"
MF "v[132:135],a[116:119],a[244:247],v[132:135]\n"
TR "v[176:177],v2 offset:49152\n"
MF "v[132:135],a[76:79],a[240:243],v[132:135]\n"
TR "v[178:179],v2 offset:51200\n"
MF "v[132:135],a[36:39],v[188:191],v[132:135]\n"
LD "v199,s[48:51],s80 offen offset:3072 lds\n"
MF "v[124:127],a[32:35],v[188:191],v[124:127]\n"
TR "v[180:181],v2 offset:53248\n"
MF "v[124:127],a[72:75],a[240:243],v[124:127]\n"
TR "v[182:183],v2 offset:55296\n"
MF "v[124:127],a[112:115],a[244:247],v[124:127]\n"
MF "v[116:119],a[108:111],a[244:247],v[116:119]\n"
TR "v[184:185],v2 offset:57344\n"
MF "v[116:119],a[68:71],a[240:243],v[116:119]\n"
"s_add_u32 m0,s79,8192\n"
TR "v[186:187],v2 offset:59392\n"
MF "v[116:119],a[28:31],v[188:191],v[116:119]\n"
LD "v200,s[48:51],s80 offen lds\n"
MF "v[108:111],a[24:27],v[188:191],v[108:111]\n"
MF "v[108:111],a[64:67],a[240:243],v[108:111]\n"
MF "v[108:111],a[104:107],a[244:247],v[108:111]\n"
MF "v[100:103],a[100:103],a[244:247],v[100:103]\n"
LD "v201,s[48:51],s80 offen offset:1024 lds\n"
MF "v[100:103],a[60:63],a[240:243],v[100:103]\n"
"s_waitcnt vmcnt(19)\n"
"s_cmp_eq_u32 s83,0xf0000000\n"
MF "v[100:103],a[20:23],v[188:191],v[100:103]\n"
"s_cbranch_scc1 .Ljs5\n"
"s_cmp_eq_u32 s83,0xf0000010\n"
"s_cbranch_scc1 .Ljc5\n"
MF "v[248:251],v[210:213],a[250:253],0\n"
"v_mov_b32 v240,v208\n"
"v_mov_b32 v241,v209\n"
MF "v[244:247],v[214:217],a[250:253],0\n"
MF "v[248:251],v[218:221],a[250:253],v[248:251]\n"
MF "v[244:247],v[222:225],a[250:253],v[244:247]\n"
MF "v[248:251],v[226:229],a[250:253],v[248:251]\n"
MF "v[244:247],v[230:233],a[250:253],v[244:247]\n"
MF "v[248:251],v[234:237],a[250:253],v[248:251]\n"
MF "v[244:247],v[238:241],a[250:253],v[244:247]\n"
"s_branch .Ljs5\n"
".Ljc5:\n"
MF "v[96:99],a[252:255],v[208:211],v[96:99]\n"
MF "v[104:107],a[252:255],v[212:215],v[104:107]\n"
MF "v[112:115],a[252:255],v[216:219],v[112:115]\n"
MF "v[120:123],a[252:255],v[220:223],v[120:123]\n"
MF "v[128:131],a[252:255],v[224:227],v[128:131]\n"
MF "v[100:103],a[248:251],v[208:211],v[100:103]\n"
MF "v[108:111],a[248:251],v[212:215],v[108:111]\n"
MF "v[116:119],a[248:251],v[216:219],v[116:119]\n"
MF "v[124:127],a[248:251],v[220:223],v[124:127]\n"
MF "v[132:135],a[248:251],v[224:227],v[132:135]\n"
".Ljs5:\n"
"s_waitcnt lgkmcnt(0)\n"
MF "v[136:139],a[20:23],v[176:179],v[136:139]\n"
TR "v[188:189],v3 offset:49152\n"
"v_sub_u32 v10,v10,v11\n"
MF "v[136:139],a[60:63],v[180:183],v[136:139]\n"
"s_lshl_b32 s82,s92,10\n"
TR "v[190:191],v3 offset:51200\n"
"v_cmp_gt_u32 vcc,s95,v10\n"
MF "v[136:139],a[100:103],v[184:187],v[136:139]\n"
LD "v202,s[48:51],s80 offen offset:2048 lds\n"
MF "v[144:147],a[104:107],v[184:187],v[144:147]\n"
"s_add_u32 s82,s82,s93\n"
TR "a[240:241],v3 offset:53248\n"
CV "v244,v244,v245\n"
MF "v[144:147],a[64:67],v[180:183],v[144:147]\n"
TR "a[242:243],v3 offset:55296\n"
CV "v245,v246,v247\n"
MF "v[144:147],a[24:27],v[176:179],v[144:147]\n"
LD "v203,s[48:51],s80 offen offset:3072 lds\n"
MF "v[152:155],a[28:31],v[176:179],v[152:155]\n"
TR "a[244:245],v3 offset:57344\n"
CV "v246,v248,v249\n"
MF "v[152:155],a[68:71],v[180:183],v[152:155]\n"
TR "a[246:247],v3 offset:59392\n"
CV "v247,v250,v251\n"
MF "v[152:155],a[108:111],v[184:187],v[152:155]\n"
"s_cmp_eq_u64 vcc,0\n"
"s_cselect_b32 s82,s82,0xf0000000\n"
"s_cselect_b32 s84,s96,0xf0000000\n"
"s_cselect_b32 s59,32,0\n"
MF "v[160:163],a[112:115],v[184:187],v[160:163]\n"
"s_add_u32 s92,s92,s59\n"
".Lnf0:\n"
"v_readlane_b32 s58,v10,s67\n"
"s_cmp_lt_u32 s58,s72\n"
"s_cbranch_scc1 .Lsp0\n"
".Lnb0:\n"
MF "v[160:163],a[72:75],v[180:183],v[160:163]\n"
"s_cmp_ge_u32 s85,5\n"
"s_cselect_b32 s82,s70,s82\n"
"s_cselect_b32 s84,0xf0000010,s84\n"
MF "v[160:163],a[32:35],v[176:179],v[160:163]\n"
"s_cmp_ge_u32 s85,5\n"
"s_cselect_b32 s71,1024,s71\n"
"s_cselect_b32 s66,0x80000000,s66\n"
MF "v[168:171],a[36:39],v[176:179],v[168:171]\n"
"s_add_u32 s70,s70,20480\n"
"v_permlane16_swap_b32 v244,v246\n"
"v_permlane16_swap_b32 v245,v247\n"
MF "v[168:171],a[76:79],v[180:183],v[168:171]\n"
"buffer_store_dwordx4 v[244:247],v8,s[44:47],s83 offen sc1\n"
MF "v[168:171],a[116:119],v[184:187],v[168:171]\n"
"s_waitcnt vmcnt(26) lgkmcnt(0)\n"
"s_barrier\n"
MF "v[172:175],a[116:119],a[244:247],v[172:175]\n"
"s_add_u32 s88,s88,s65\n"
"s_mov_b32 s83,s84\n"
TR "v[176:177],v4\n"
MF "v[172:175],a[76:79],a[240:243],v[172:175]\n"
"s_add_u32 m0,s79,49152\n"
TR "v[178:179],v4 offset:2048\n"
MF "v[172:175],a[36:39],v[188:191],v[172:175]\n"
LD "v192,s[48:51],s88 offen lds\n"
MF "v[164:167],a[32:35],v[188:191],v[164:167]\n"
TR "v[180:181],v4 offset:4096\n"
MF "v[164:167],a[72:75],a[240:243],v[164:167]\n"
TR "v[182:183],v4 offset:6144\n"
MF "v[164:167],a[112:115],a[244:247],v[164:167]\n"
LD "v193,s[48:51],s88 offen offset:1024 lds\n"
MF "v[156:159],a[108:111],a[244:247],v[156:159]\n"
TR "v[184:185],v4 offset:8192\n"
MF "v[156:159],a[68:71],a[240:243],v[156:159]\n"
TR "v[186:187],v4 offset:10240\n"
MF "v[156:159],a[28:31],v[188:191],v[156:159]\n"
LD "v[208:211],v9,s[40:43],s82 offen sc1\n"
MF "v[148:151],a[24:27],v[188:191],v[148:151]\n"
MF "v[148:151],a[64:67],a[240:243],v[148:151]\n"
LD "v194,s[48:51],s88 offen offset:2048 lds\n"
MF "v[148:151],a[104:107],a[244:247],v[148:151]\n"
MF "v[140:143],a[100:103],a[244:247],v[140:143]\n"
MF "v[140:143],a[60:63],a[240:243],v[140:143]\n"
MF "v[140:143],a[20:23],v[188:191],v[140:143]\n"
"s_waitcnt lgkmcnt(0)\n"
LD "v195,s[48:51],s88 offen offset:3072 lds\n"
MF "v[96:99],a[140:143],v[176:179],v[96:99]\n"
TR "v[188:189],v5\n"
MF "v[96:99],a[180:183],v[180:183],v[96:99]\n"
"s_add_u32 s82,s82,s71\n"
TR "v[190:191],v5 offset:2048\n"
MF "v[96:99],a[220:223],v[184:187],v[96:99]\n"
LD "v[212:215],v9,s[40:43],s82 offen sc1\n"
MF "v[104:107],a[224:227],v[184:187],v[104:107]\n"
TR "a[240:241],v5 offset:4096\n"
MF "v[104:107],a[184:187],v[180:183],v[104:107]\n"
"s_add_u32 m0,s79,53248\n"
TR "a[242:243],v5 offset:6144\n"
MF "v[104:107],a[144:147],v[176:179],v[104:107]\n"
LD "v196,s[48:51],s88 offen lds\n"
MF "v[112:115],a[148:151],v[176:179],v[112:115]\n"
TR "a[244:245],v5 offset:8192\n"
MF "v[112:115],a[188:191],v[180:183],v[112:115]\n"
TR "a[246:247],v5 offset:10240\n"
MF "v[112:115],a[228:231],v[184:187],v[112:115]\n"
LD "v197,s[48:51],s88 offen offset:1024 lds\n"
MF "v[120:123],a[232:235],v[184:187],v[120:123]\n"
"s_add_u32 s82,s82,s71\n"
MF "v[120:123],a[192:195],v[180:183],v[120:123]\n"
LD "v[216:219],v9,s[40:43],s82 offen sc1\n"
MF "v[120:123],a[152:155],v[176:179],v[120:123]\n"
MF "v[128:131],a[156:159],v[176:179],v[128:131]\n"
MF "v[128:131],a[196:199],v[180:183],v[128:131]\n"
LD "v198,s[48:51],s88 offen offset:2048 lds\n"
MF "v[128:131],a[236:239],v[184:187],v[128:131]\n"
"s_waitcnt lgkmcnt(0)\n"
MF "v[132:135],a[236:239],a[244:247],v[132:135]\n"
".Lpg22h:\n"
"s_cbranch_execz .Lpg25h\n"
TR "v[176:177],v6\n"
MF "v[132:135],a[196:199],a[240:243],v[132:135]\n"
TR "v[178:179],v6 offset:2048\n"
MF "v[132:135],a[156:159],v[188:191],v[132:135]\n"
LD "v199,s[48:51],s88 offen offset:3072 lds\n"
MF "v[124:127],a[152:155],v[188:191],v[124:127]\n"
TR "v[180:181],v6 offset:4096\n"
MF "v[124:127],a[192:195],a[240:243],v[124:127]\n"
"s_add_u32 s82,s82,s71\n"
TR "v[182:183],v6 offset:6144\n"
MF "v[124:127],a[232:235],a[244:247],v[124:127]\n"
LD "v[220:223],v9,s[40:43],s82 offen sc1\n"
MF "v[116:119],a[228:231],a[244:247],v[116:119]\n"
TR "v[184:185],v6 offset:8192\n"
MF "v[116:119],a[188:191],a[240:243],v[116:119]\n"
"s_add_u32 m0,s79,57344\n"
TR "v[186:187],v6 offset:10240\n"
MF "v[116:119],a[148:151],v[188:191],v[116:119]\n"
LD "v200,s[48:51],s88 offen lds\n"
MF "v[108:111],a[144:147],v[188:191],v[108:111]\n"
MF "v[108:111],a[184:187],a[240:243],v[108:111]\n"
"s_add_u32 s82,s82,s71\n"
MF "v[108:111],a[224:227],a[244:247],v[108:111]\n"
LD "v[224:227],v9,s[40:43],s82 offen sc1\n"
MF "v[100:103],a[220:223],a[244:247],v[100:103]\n"
LD "v201,s[48:51],s88 offen offset:1024 lds\n"
MF "v[100:103],a[180:183],a[240:243],v[100:103]\n"
MF "v[100:103],a[140:143],v[188:191],v[100:103]\n"
"s_waitcnt lgkmcnt(0)\n"
MF "v[136:139],a[140:143],v[176:179],v[136:139]\n"
TR "v[188:189],v7\n"
MF "v[136:139],a[180:183],v[180:183],v[136:139]\n"
TR "v[190:191],v7 offset:2048\n"
MF "v[136:139],a[220:223],v[184:187],v[136:139]\n"
LD "v202,s[48:51],s88 offen offset:2048 lds\n"
MF "v[144:147],a[224:227],v[184:187],v[144:147]\n"
TR "a[240:241],v7 offset:4096\n"
MF "v[144:147],a[184:187],v[180:183],v[144:147]\n"
TR "a[242:243],v7 offset:6144\n"
MF "v[144:147],a[144:147],v[176:179],v[144:147]\n"
LD "v203,s[48:51],s88 offen offset:3072 lds\n"
MF "v[152:155],a[148:151],v[176:179],v[152:155]\n"
TR "a[244:245],v7 offset:8192\n"
MF "v[152:155],a[188:191],v[180:183],v[152:155]\n"
"s_add_u32 s82,s82,s66\n"
TR "a[246:247],v7 offset:10240\n"
MF "v[152:155],a[228:231],v[184:187],v[152:155]\n"
LD "v[228:231],v9,s[40:43],s82 offen sc1\n"
MF "v[160:163],a[232:235],v[184:187],v[160:163]\n"
MF "v[160:163],a[192:195],v[180:183],v[160:163]\n"
MF "v[160:163],a[152:155],v[176:179],v[160:163]\n"
MF "v[168:171],a[156:159],v[176:179],v[168:171]\n"
"s_add_u32 s82,s82,s71\n"
MF "v[168:171],a[196:199],v[180:183],v[168:171]\n"
LD "v[232:235],v9,s[40:43],s82 offen sc1\n"
MF "v[168:171],a[236:239],v[184:187],v[168:171]\n"
"s_waitcnt vmcnt(32) lgkmcnt(0)\n"
"s_barrier\n"
MF "v[172:175],a[236:239],a[244:247],v[172:175]\n"
"s_add_u32 s80,s88,122880\n"
TR "v[176:177],v0 offset:24576\n"
MF "v[172:175],a[196:199],a[240:243],v[172:175]\n"
"s_add_u32 m0,s79,98304\n"
"s_add_u32 s85,s85,1\n"
TR "v[178:179],v0 offset:26624\n"
MF "v[172:175],a[156:159],v[188:191],v[172:175]\n"
LD "v192,s[48:51],s80 offen lds\n"
MF "v[164:167],a[152:155],v[188:191],v[164:167]\n"
"s_nop 0\n"
TR "v[180:181],v0 offset:28672\n"
"ds_write_addtid_b32 v96 offset:24576\n"
MF "v[164:167],a[192:195],a[240:243],v[164:167]\n"
"s_nop 0\n"
TR "v[182:183],v0 offset:30720\n"
"ds_write_addtid_b32 v97 offset:24832\n"
MF "v[164:167],a[232:235],a[244:247],v[164:167]\n"
LD "v193,s[48:51],s80 offen offset:1024 lds\n"
MF "v[156:159],a[228:231],a[244:247],v[156:159]\n"
"s_nop 0\n"
TR "v[184:185],v0 offset:32768\n"
"ds_write_addtid_b32 v98 offset:25088\n"
MF "v[156:159],a[188:191],a[240:243],v[156:159]\n"
"s_add_u32 s82,s82,s71\n"
TR "v[186:187],v0 offset:34816\n"
"ds_write_addtid_b32 v99 offset:25344\n"
MF "v[156:159],a[148:151],v[188:191],v[156:159]\n"
LD "v[236:239],v9,s[40:43],s82 offen sc1\n"
MF "v[148:151],a[144:147],v[188:191],v[148:151]\n"
LD "v194,s[48:51],s80 offen offset:2048 lds\n"
MF "v[148:151],a[184:187],a[240:243],v[148:151]\n"
"s_nop 0\n"
"ds_write_addtid_b32 v100 offset:25600\n"
"ds_write_addtid_b32 v101 offset:25856\n"
MF "v[148:151],a[224:227],a[244:247],v[148:151]\n"
"s_nop 0\n"
"ds_write_addtid_b32 v102 offset:26112\n"
"ds_write_addtid_b32 v103 offset:26368\n"
MF "v[140:143],a[220:223],a[244:247],v[140:143]\n"
"s_nop 0\n"
"ds_write_addtid_b32 v104 offset:26624\n"
"ds_write_addtid_b32 v105 offset:26880\n"
MF "v[140:143],a[180:183],a[240:243],v[140:143]\n"
"s_nop 0\n"
"ds_write_addtid_b32 v106 offset:27136\n"
"ds_write_addtid_b32 v107 offset:27392\n"
MF "v[140:143],a[140:143],v[188:191],v[140:143]\n"
"s_waitcnt lgkmcnt(9)\n"
MF "v[16:19],a[0:3],v[176:179],v[252:255]\n"
"s_nop 0\n"
TR "v[188:189],v1 offset:24576\n"
"ds_write_addtid_b32 v108 offset:27648\n"
MF "v[16:19],a[40:43],v[180:183],v[16:19]\n"
"s_nop 0\n"
TR "v[190:191],v1 offset:26624\n"
"ds_write_addtid_b32 v109 offset:27904\n"
MF "v[16:19],a[80:83],v[184:187],v[16:19]\n"
LD "v195,s[48:51],s80 offen offset:3072 lds\n"
MF "v[24:27],a[84:87],v[184:187],v[252:255]\n"
"s_add_u32 s60,s64,1\n"
TR "a[240:241],v1 offset:28672\n"
"ds_write_addtid_b32 v110 offset:28160\n"
MF "v[24:27],a[44:47],v[180:183],v[24:27]\n"
"s_add_u32 m0,s79,102400\n"
TR "a[242:243],v1 offset:30720\n"
"ds_write_addtid_b32 v111 offset:24320\n"
MF "v[24:27],a[4:7],v[176:179],v[24:27]\n"
LD "v196,s[48:51],s80 offen lds\n"
MF "v[32:35],a[8:11],v[176:179],v[252:255]\n"
"s_cmp_eq_u32 s60,8\n"
"s_cselect_b32 s60,0,s60\n"
TR "a[244:245],v1 offset:32768\n"
MF "v[32:35],a[48:51],v[180:183],v[32:35]\n"
"s_cmp_eq_u32 s60,10\n"
"s_cselect_b32 s60,s61,s60\n"
TR "a[246:247],v1 offset:34816\n"
MF "v[32:35],a[88:91],v[184:187],v[32:35]\n"
"s_sub_u32 s65,s60,s64\n"
"ds_write_addtid_b32 v112 offset:24576\n"
"ds_write_addtid_b32 v113 offset:24832\n"
MF "v[40:43],a[92:95],v[184:187],v[252:255]\n"
"s_lshl_b32 s65,s65,7\n"
"ds_write_addtid_b32 v114 offset:25088\n"
"ds_write_addtid_b32 v115 offset:25344\n"
MF "v[40:43],a[52:55],v[180:183],v[40:43]\n"
LD "v197,s[48:51],s80 offen offset:1024 lds\n"
MF "v[40:43],a[12:15],v[176:179],v[40:43]\n"
"s_mov_b32 s64,s60\n"
"ds_write_addtid_b32 v116 offset:25600\n"
"ds_write_addtid_b32 v117 offset:25856\n"
MF "v[48:51],a[16:19],v[176:179],v[252:255]\n"
"s_nop 0\n"
"ds_write_addtid_b32 v118 offset:26112\n"
"ds_write_addtid_b32 v119 offset:26368\n"
MF "v[48:51],a[56:59],v[180:183],v[48:51]\n"
"s_nop 0\n"
"ds_write_addtid_b32 v120 offset:26624\n"
"ds_write_addtid_b32 v121 offset:26880\n"
MF "v[48:51],a[96:99],v[184:187],v[48:51]\n"
"s_waitcnt lgkmcnt(10)\n"
LD "v198,s[48:51],s80 offen offset:2048 lds\n"
MF "v[52:55],a[96:99],a[244:247],v[252:255]\n"
"s_nop 0\n"
TR "v[176:177],v2 offset:24576\n"
"ds_write_addtid_b32 v122 offset:27136\n"
MF "v[52:55],a[56:59],a[240:243],v[52:55]\n"
"s_nop 0\n"
TR "v[178:179],v2 offset:26624\n"
"ds_write_addtid_b32 v123 offset:27392\n"
MF "v[52:55],a[16:19],v[188:191],v[52:55]\n"
"s_nop 0\n"
"ds_write_addtid_b32 v124 offset:27648\n"
"ds_write_addtid_b32 v125 offset:27904\n"
MF "v[44:47],a[12:15],v[188:191],v[252:255]\n"
"s_nop 0\n"
TR "v[180:181],v2 offset:28672\n"
"ds_write_addtid_b32 v126 offset:28160\n"
MF "v[44:47],a[52:55],a[240:243],v[44:47]\n"
"s_nop 0\n"
TR "v[182:183],v2 offset:30720\n"
"ds_write_addtid_b32 v127 offset:28416\n"
MF "v[44:47],a[92:95],a[244:247],v[44:47]\n"
LD "v199,s[48:51],s80 offen offset:3072 lds\n"
MF "v[36:39],a[88:91],a[244:247],v[252:255]\n"
"s_nop 0\n"
TR "v[184:185],v2 offset:32768\n"
"ds_write_addtid_b32 v128 offset:28672\n"
MF "v[36:39],a[48:51],a[240:243],v[36:39]\n"
"s_add_u32 m0,s79,106496\n"
TR "v[186:187],v2 offset:34816\n"
"ds_write_addtid_b32 v129 offset:24832\n"
MF "v[36:39],a[8:11],v[188:191],v[36:39]\n"
LD "v200,s[48:51],s80 offen lds\n"
MF "v[28:31],a[4:7],v[188:191],v[252:255]\n"
"s_nop 0\n"
"ds_write_addtid_b32 v130 offset:25088\n"
"ds_write_addtid_b32 v131 offset:25344\n"
MF "v[28:31],a[44:47],a[240:243],v[28:31]\n"
"s_nop 0\n"
"ds_write_addtid_b32 v132 offset:25600\n"
"ds_write_addtid_b32 v133 offset:25856\n"
MF "v[28:31],a[84:87],a[244:247],v[28:31]\n"
"s_nop 0\n"
"ds_write_addtid_b32 v134 offset:26112\n"
"ds_write_addtid_b32 v135 offset:26368\n"
MF "v[20:23],a[80:83],a[244:247],v[252:255]\n"
MF "v[20:23],a[40:43],a[240:243],v[20:23]\n"
LD "v201,s[48:51],s80 offen offset:1024 lds\n"
MF "v[20:23],a[0:3],v[188:191],v[20:23]\n"
"s_waitcnt lgkmcnt(7)\n"
MF "v[56:59],a[0:3],v[176:179],v[252:255]\n"
TR "v[188:189],v3 offset:24576\n"
MF "v[56:59],a[40:43],v[180:183],v[56:59]\n"
TR "v[190:191],v3 offset:26624\n"
MF "v[56:59],a[80:83],v[184:187],v[56:59]\n"
LD "v202,s[48:51],s80 offen offset:2048 lds\n"
MF "v[64:67],a[84:87],v[184:187],v[252:255]\n"
TR "a[240:241],v3 offset:28672\n"
MF "v[64:67],a[44:47],v[180:183],v[64:67]\n"
TR "a[242:243],v3 offset:30720\n"
MF "v[64:67],a[4:7],v[176:179],v[64:67]\n"
LD "v203,s[48:51],s80 offen offset:3072 lds\n"
MF "v[72:75],a[8:11],v[176:179],v[252:255]\n"
TR "a[244:245],v3 offset:32768\n"
MF "v[72:75],a[48:51],v[180:183],v[72:75]\n"
TR "a[246:247],v3 offset:34816\n"
MF "v[72:75],a[88:91],v[184:187],v[72:75]\n"
MF "v[80:83],a[92:95],v[184:187],v[252:255]\n"
MF "v[80:83],a[52:55],v[180:183],v[80:83]\n"
MF "v[80:83],a[12:15],v[176:179],v[80:83]\n"
MF "v[88:91],a[16:19],v[176:179],v[252:255]\n"
MF "v[88:91],a[56:59],v[180:183],v[88:91]\n"
MF "v[88:91],a[96:99],v[184:187],v[88:91]\n"
"s_waitcnt vmcnt(33) lgkmcnt(0)\n"
"s_barrier\n"
MF "v[92:95],a[96:99],a[244:247],v[252:255]\n"
"s_add_u32 s89,s89,s65\n"
TR "v[176:177],v0\n"
MF "v[92:95],a[56:59],a[240:243],v[92:95]\n"
"s_add_u32 m0,s79,24576\n"
TR "v[178:179],v0 offset:2048\n"
MF "v[92:95],a[16:19],v[188:191],v[92:95]\n"
LD "v192,s[48:51],s89 offen lds\n"
MF "v[84:87],a[12:15],v[188:191],v[252:255]\n"
TR "v[180:181],v0 offset:4096\n"
MF "v[84:87],a[52:55],a[240:243],v[84:87]\n"
TR "v[182:183],v0 offset:6144\n"
MF "v[84:87],a[92:95],a[244:247],v[84:87]\n"
LD "v193,s[48:51],s89 offen offset:1024 lds\n"
MF "v[76:79],a[88:91],a[244:247],v[252:255]\n"
TR "v[184:185],v0 offset:8192\n"
MF "v[76:79],a[48:51],a[240:243],v[76:79]\n"
"s_nop 0\n"
TR "v[186:187],v0 offset:10240\n"
"ds_write_addtid_b32 v136 offset:49152\n"
MF "v[76:79],a[8:11],v[188:191],v[76:79]\n"
"s_nop 0\n"
"ds_write_addtid_b32 v137 offset:49408\n"
"ds_write_addtid_b32 v138 offset:49664\n"
MF "v[68:71],a[4:7],v[188:191],v[252:255]\n"
LD "v194,s[48:51],s89 offen offset:2048 lds\n"
MF "v[68:71],a[44:47],a[240:243],v[68:71]\n"
"buffer_load_dword v10,v12,s[40:43],s86 offen sc1\n"
MF "v[68:71],a[84:87],a[244:247],v[68:71]\n"
"s_nop 0\n"
"ds_write_addtid_b32 v139 offset:49920\n"
"ds_write_addtid_b32 v140 offset:50176\n"
MF "v[60:63],a[80:83],a[244:247],v[252:255]\n"
"s_nop 0\n"
"ds_write_addtid_b32 v141 offset:50432\n"
"ds_write_addtid_b32 v142 offset:50688\n"
MF "v[60:63],a[40:43],a[240:243],v[60:63]\n"
"s_nop 0\n"
"ds_write_addtid_b32 v143 offset:50944\n"
"ds_write_addtid_b32 v144 offset:51200\n"
MF "v[60:63],a[0:3],v[188:191],v[60:63]\n"
"s_waitcnt lgkmcnt(9)\n"
LD "v195,s[48:51],s89 offen offset:3072 lds\n"
MF "v[16:19],a[120:123],v[176:179],v[16:19]\n"
"s_nop 0\n"
TR "v[188:189],v1\n"
"ds_write_addtid_b32 v145 offset:51456\n"
MF "v[16:19],a[160:163],v[180:183],v[16:19]\n"
"s_nop 0\n"
TR "v[190:191],v1 offset:2048\n"
"ds_write_addtid_b32 v146 offset:51712\n"
MF "v[16:19],a[200:203],v[184:187],v[16:19]\n"
"s_nop 0\n"
"ds_write_addtid_b32 v147 offset:51968\n"
"ds_write_addtid_b32 v148 offset:52224\n"
MF "v[24:27],a[204:207],v[184:187],v[24:27]\n"
"s_nop 0\n"
TR "a[240:241],v1 offset:4096\n"
"ds_write_addtid_b32 v149 offset:52480\n"
MF "v[24:27],a[164:167],v[180:183],v[24:27]\n"
"s_add_u32 m0,s79,28672\n"
TR "a[242:243],v1 offset:6144\n"
"ds_write_addtid_b32 v150 offset:48640\n"
MF "v[24:27],a[124:127],v[176:179],v[24:27]\n"
LD "v196,s[48:51],s89 offen lds\n"
MF "v[32:35],a[128:131],v[176:179],v[32:35]\n"
"s_nop 0\n"
TR "a[244:245],v1 offset:8192\n"
"ds_write_addtid_b32 v151 offset:48896\n"
MF "v[32:35],a[168:171],v[180:183],v[32:35]\n"
"s_nop 0\n"
TR "a[246:247],v1 offset:10240\n"
"ds_write_addtid_b32 v152 offset:49152\n"
MF "v[32:35],a[208:211],v[184:187],v[32:35]\n"
LD "v197,s[48:51],s89 offen offset:1024 lds\n"
MF "v[40:43],a[212:215],v[184:187],v[40:43]\n"
"s_nop 0\n"
"ds_write_addtid_b32 v153 offset:49408\n"
"ds_write_addtid_b32 v154 offset:49664\n"
MF "v[40:43],a[172:175],v[180:183],v[40:43]\n"
"s_nop 0\n"
"ds_write_addtid_b32 v155 offset:49920\n"
"ds_write_addtid_b32 v156 offset:50176\n"
MF "v[40:43],a[132:135],v[176:179],v[40:43]\n"
"s_nop 0\n"
"ds_write_addtid_b32 v157 offset:50432\n"
"ds_write_addtid_b32 v158 offset:50688\n"
MF "v[48:51],a[136:139],v[176:179],v[48:51]\n"
"s_nop 0\n"
"ds_write_addtid_b32 v159 offset:50944\n"
"ds_write_addtid_b32 v160 offset:51200\n"
MF "v[48:51],a[176:179],v[180:183],v[48:51]\n"
LD "v198,s[48:51],s89 offen offset:2048 lds\n"
MF "v[48:51],a[216:219],v[184:187],v[48:51]\n"
"s_waitcnt lgkmcnt(9)\n"
"ds_write_addtid_b32 v161 offset:51456\n"
"ds_write_addtid_b32 v162 offset:51712\n"
MF "v[52:55],a[216:219],a[244:247],v[52:55]\n"
"s_nop 0\n"
TR "v[176:177],v2\n"
"ds_write_addtid_b32 v163 offset:51968\n"
MF "v[52:55],a[176:179],a[240:243],v[52:55]\n"
"s_nop 0\n"
TR "v[178:179],v2 offset:2048\n"
"ds_write_addtid_b32 v164 offset:52224\n"
MF "v[52:55],a[136:139],v[188:191],v[52:55]\n"
LD "v199,s[48:51],s89 offen offset:3072 lds\n"
MF "v[44:47],a[132:135],v[188:191],v[44:47]\n"
"s_nop 0\n"
TR "v[180:181],v2 offset:4096\n"
"ds_write_addtid_b32 v165 offset:52480\n"
MF "v[44:47],a[172:175],a[240:243],v[44:47]\n"
"s_nop 0\n"
TR "v[182:183],v2 offset:6144\n"
"ds_write_addtid_b32 v166 offset:52736\n"
MF "v[44:47],a[212:215],a[244:247],v[44:47]\n"
"s_nop 0\n"
"ds_write_addtid_b32 v167 offset:52992\n"
"ds_write_addtid_b32 v168 offset:53248\n"
MF "v[36:39],a[208:211],a[244:247],v[36:39]\n"
"s_nop 0\n"
TR "v[184:185],v2 offset:8192\n"
"ds_write_addtid_b32 v169 offset:53504\n"
MF "v[36:39],a[168:171],a[240:243],v[36:39]\n"
"s_add_u32 m0,s79,32768\n"
TR "v[186:187],v2 offset:10240\n"
"ds_write_addtid_b32 v170 offset:49664\n"
MF "v[36:39],a[128:131],v[188:191],v[36:39]\n"
LD "v200,s[48:51],s89 offen lds\n"
MF "v[28:31],a[124:127],v[188:191],v[28:31]\n"
"s_nop 0\n"
"ds_write_addtid_b32 v171 offset:49920\n"
"ds_write_addtid_b32 v172 offset:50176\n"
MF "v[28:31],a[164:167],a[240:243],v[28:31]\n"
"s_nop 0\n"
"ds_write_addtid_b32 v173 offset:50432\n"
"ds_write_addtid_b32 v174 offset:50688\n"
MF "v[28:31],a[204:207],a[244:247],v[28:31]\n"
"ds_write_addtid_b32 v175 offset:50944\n"
MF "v[20:23],a[200:203],a[244:247],v[20:23]\n"
LD "v201,s[48:51],s89 offen offset:1024 lds\n"
MF "v[20:23],a[160:163],a[240:243],v[20:23]\n"
MF "v[20:23],a[120:123],v[188:191],v[20:23]\n"
"s_waitcnt lgkmcnt(6)\n"
MF "v[56:59],a[120:123],v[176:179],v[56:59]\n"
TR "v[188:189],v3\n"
MF "v[56:59],a[160:163],v[180:183],v[56:59]\n"
TR "v[190:191],v3 offset:2048\n"
MF "v[56:59],a[200:203],v[184:187],v[56:59]\n"
LD "v202,s[48:51],s89 offen offset:2048 lds\n"
MF "v[64:67],a[204:207],v[184:187],v[64:67]\n"
TR "a[240:241],v3 offset:4096\n"
MF "v[64:67],a[164:167],v[180:183],v[64:67]\n"
TR "a[242:243],v3 offset:6144\n"
MF "v[64:67],a[124:127],v[176:179],v[64:67]\n"
LD "v203,s[48:51],s89 offen offset:3072 lds\n"
MF "v[72:75],a[128:131],v[176:179],v[72:75]\n"
TR "a[244:245],v3 offset:8192\n"
MF "v[72:75],a[168:171],v[180:183],v[72:75]\n"
TR "a[246:247],v3 offset:10240\n"
MF "v[72:75],a[208:211],v[184:187],v[72:75]\n"
MF "v[80:83],a[212:215],v[184:187],v[80:83]\n"
MF "v[80:83],a[172:175],v[180:183],v[80:83]\n"
MF "v[80:83],a[132:135],v[176:179],v[80:83]\n"
MF "v[88:91],a[136:139],v[176:179],v[88:91]\n"
MF "v[88:91],a[176:179],v[180:183],v[88:91]\n"
MF "v[88:91],a[216:219],v[184:187],v[88:91]\n"
"s_waitcnt vmcnt(28) lgkmcnt(0)\n"
"s_barrier\n"
MF "v[92:95],a[216:219],a[244:247],v[92:95]\n"
"s_add_u32 s80,s89,122880\n"
TR "v[176:177],v0 offset:49152\n"
MF "v[92:95],a[176:179],a[240:243],v[92:95]\n"
"s_add_u32 m0,s79,0\n"
"s_mul_i32 s95,s92,3277\n"
TR "v[178:179],v0 offset:51200\n"
MF "v[92:95],a[136:139],v[188:191],v[92:95]\n"
LD "v192,s[48:51],s80 offen lds\n"
MF "v[84:87],a[132:135],v[188:191],v[84:87]\n"
"s_lshr_b32 s95,s95,16\n"
"s_mul_i32 s59,s95,20\n"
TR "v[180:181],v0 offset:53248\n"
MF "v[84:87],a[172:175],a[240:243],v[84:87]\n"
"s_sub_u32 s59,s92,s59\n"
TR "v[182:183],v0 offset:55296\n"
MF "v[84:87],a[212:215],a[244:247],v[84:87]\n"
LD "v193,s[48:51],s80 offen offset:1024 lds\n"
MF "v[76:79],a[208:211],a[244:247],v[76:79]\n"
"s_cmp_ge_u32 s59,10\n"
"s_cselect_b32 s96,4294557760,0\n"
TR "v[184:185],v0 offset:57344\n"
MF "v[76:79],a[168:171],a[240:243],v[76:79]\n"
"s_add_u32 s96,s96,1024\n"
"s_mul_i32 s59,s59,40960\n"
TR "v[186:187],v0 offset:59392\n"
MF "v[76:79],a[128:131],v[188:191],v[76:79]\n"
"s_add_u32 s96,s96,s59\n"
"s_lshl_b32 s59,s95,7\n"
"s_add_u32 s96,s96,s59\n"
MF "v[68:71],a[124:127],v[188:191],v[68:71]\n"
"s_add_u32 s95,s95,3\n"
"s_cmp_ge_u32 s92,40\n"
"s_cselect_b32 s95,99,s95\n"
MF "v[68:71],a[164:167],a[240:243],v[68:71]\n"
LD "v194,s[48:51],s80 offen offset:2048 lds\n"
MF "v[68:71],a[204:207],a[244:247],v[68:71]\n"
"s_cmp_ge_u32 s85,5\n"
"s_cselect_b32 s95,99,s95\n"
"s_cselect_b32 s72,s85,0\n"
MF "v[60:63],a[200:203],a[244:247],v[60:63]\n"
MF "v[60:63],a[160:163],a[240:243],v[60:63]\n"
MF "v[60:63],a[120:123],v[188:191],v[60:63]\n"
"s_waitcnt lgkmcnt(0)\n"
LD "v195,s[48:51],s80 offen offset:3072 lds\n"
MF "v[16:19],a[20:23],v[176:179],v[16:19]\n"
TR "v[188:189],v1 offset:49152\n"
MF "v[16:19],a[60:63],v[180:183],v[16:19]\n"
TR "v[190:191],v1 offset:51200\n"
MF "v[16:19],a[100:103],v[184:187],v[16:19]\n"
MF "v[24:27],a[104:107],v[184:187],v[24:27]\n"
TR "a[240:241],v1 offset:53248\n"
MF "v[24:27],a[64:67],v[180:183],v[24:27]\n"
"s_add_u32 m0,s79,4096\n"
TR "a[242:243],v1 offset:55296\n"
MF "v[24:27],a[24:27],v[176:179],v[24:27]\n"
LD "v196,s[48:51],s80 offen lds\n"
MF "v[32:35],a[28:31],v[176:179],v[32:35]\n"
TR "a[244:245],v1 offset:57344\n"
MF "v[32:35],a[68:71],v[180:183],v[32:35]\n"
TR "a[246:247],v1 offset:59392\n"
MF "v[32:35],a[108:111],v[184:187],v[32:35]\n"
LD "v197,s[48:51],s80 offen offset:1024 lds\n"
MF "v[40:43],a[112:115],v[184:187],v[40:43]\n"
MF "v[40:43],a[72:75],v[180:183],v[40:43]\n"
MF "v[40:43],a[32:35],v[176:179],v[40:43]\n"
MF "v[48:51],a[36:39],v[176:179],v[48:51]\n"
MF "v[48:51],a[76:79],v[180:183],v[48:51]\n"
LD "v198,s[48:51],s80 offen offset:2048 lds\n"
MF "v[48:51],a[116:119],v[184:187],v[48:51]\n"
"s_waitcnt lgkmcnt(0)\n"
MF "v[52:55],a[116:119],a[244:247],v[52:55]\n"
TR "v[176:177],v2 offset:49152\n"
MF "v[52:55],a[76:79],a[240:243],v[52:55]\n"
TR "v[178:179],v2 offset:51200\n"
MF "v[52:55],a[36:39],v[188:191],v[52:55]\n"
LD "v199,s[48:51],s80 offen offset:3072 lds\n"
MF "v[44:47],a[32:35],v[188:191],v[44:47]\n"
TR "v[180:181],v2 offset:53248\n"
MF "v[44:47],a[72:75],a[240:243],v[44:47]\n"
TR "v[182:183],v2 offset:55296\n"
MF "v[44:47],a[112:115],a[244:247],v[44:47]\n"
MF "v[36:39],a[108:111],a[244:247],v[36:39]\n"
TR "v[184:185],v2 offset:57344\n"
MF "v[36:39],a[68:71],a[240:243],v[36:39]\n"
"s_add_u32 m0,s79,8192\n"
TR "v[186:187],v2 offset:59392\n"
MF "v[36:39],a[28:31],v[188:191],v[36:39]\n"
LD "v200,s[48:51],s80 offen lds\n"
MF "v[28:31],a[24:27],v[188:191],v[28:31]\n"
MF "v[28:31],a[64:67],a[240:243],v[28:31]\n"
MF "v[28:31],a[104:107],a[244:247],v[28:31]\n"
MF "v[20:23],a[100:103],a[244:247],v[20:23]\n"
LD "v201,s[48:51],s80 offen offset:1024 lds\n"
MF "v[20:23],a[60:63],a[240:243],v[20:23]\n"
"s_waitcnt vmcnt(19)\n"
"s_cmp_eq_u32 s83,0xf0000000\n"
MF "v[20:23],a[20:23],v[188:191],v[20:23]\n"
"s_cbranch_scc1 .Ljs7\n"
"s_cmp_eq_u32 s83,0xf0000010\n"
"s_cbranch_scc1 .Ljc7\n"
MF "v[248:251],v[210:213],a[250:253],0\n"
"v_mov_b32 v240,v208\n"
"v_mov_b32 v241,v209\n"
MF "v[244:247],v[214:217],a[250:253],0\n"
MF "v[248:251],v[218:221],a[250:253],v[248:251]\n"
MF "v[244:247],v[222:225],a[250:253],v[244:247]\n"
MF "v[248:251],v[226:229],a[250:253],v[248:251]\n"
MF "v[244:247],v[230:233],a[250:253],v[244:247]\n"
MF "v[248:251],v[234:237],a[250:253],v[248:251]\n"
MF "v[244:247],v[238:241],a[250:253],v[244:247]\n"
"s_branch .Ljs7\n"
".Ljc7:\n"
MF "v[16:19],a[252:255],v[208:211],v[16:19]\n"
MF "v[24:27],a[252:255],v[212:215],v[24:27]\n"
MF "v[32:35],a[252:255],v[216:219],v[32:35]\n"
MF "v[40:43],a[252:255],v[220:223],v[40:43]\n"
MF "v[48:51],a[252:255],v[224:227],v[48:51]\n"
MF "v[20:23],a[248:251],v[208:211],v[20:23]\n"
MF "v[28:31],a[248:251],v[212:215],v[28:31]\n"
MF "v[36:39],a[248:251],v[216:219],v[36:39]\n"
MF "v[44:47],a[248:251],v[220:223],v[44:47]\n"
MF "v[52:55],a[248:251],v[224:227],v[52:55]\n"
".Ljs7:\n"
"s_waitcnt lgkmcnt(0)\n"
MF "v[56:59],a[20:23],v[176:179],v[56:59]\n"
TR "v[188:189],v3 offset:49152\n"
"v_sub_u32 v10,v10,v11\n"
MF "v[56:59],a[60:63],v[180:183],v[56:59]\n"
"s_lshl_b32 s82,s92,10\n"
TR "v[190:191],v3 offset:51200\n"
"v_cmp_gt_u32 vcc,s95,v10\n"
MF "v[56:59],a[100:103],v[184:187],v[56:59]\n"
LD "v202,s[48:51],s80 offen offset:2048 lds\n"
MF "v[64:67],a[104:107],v[184:187],v[64:67]\n"
"s_add_u32 s82,s82,s93\n"
TR "a[240:241],v3 offset:53248\n"
CV "v244,v244,v245\n"
MF "v[64:67],a[64:67],v[180:183],v[64:67]\n"
TR "a[242:243],v3 offset:55296\n"
CV "v245,v246,v247\n"
MF "v[64:67],a[24:27],v[176:179],v[64:67]\n"
LD "v203,s[48:51],s80 offen offset:3072 lds\n"
MF "v[72:75],a[28:31],v[176:179],v[72:75]\n"
TR "a[244:245],v3 offset:57344\n"
CV "v246,v248,v249\n"
MF "v[72:75],a[68:71],v[180:183],v[72:75]\n"
TR "a[246:247],v3 offset:59392\n"
CV "v247,v250,v251\n"
MF "v[72:75],a[108:111],v[184:187],v[72:75]\n"
"s_cmp_eq_u64 vcc,0\n"
"s_cselect_b32 s82,s82,0xf0000000\n"
"s_cselect_b32 s84,s96,0xf0000000\n"
"s_cselect_b32 s59,32,0\n"
MF "v[80:83],a[112:115],v[184:187],v[80:83]\n"
"s_add_u32 s92,s92,s59\n"
".Lnf1:\n"
"v_readlane_b32 s58,v10,s67\n"
"s_cmp_lt_u32 s58,s72\n"
"s_cbranch_scc1 .Lsp1\n"
".Lnb1:\n"
MF "v[80:83],a[72:75],v[180:183],v[80:83]\n"
".Lpg23h:\n"
"s_cbranch_execz .Lpg26h\n"
"s_cmp_ge_u32 s85,5\n"
"s_cselect_b32 s82,s70,s82\n"
"s_cselect_b32 s84,0xf0000010,s84\n"
MF "v[80:83],a[32:35],v[176:179],v[80:83]\n"
"s_cmp_ge_u32 s85,5\n"
"s_cselect_b32 s71,1024,s71\n"
"s_cselect_b32 s66,0x80000000,s66\n"
MF "v[88:91],a[36:39],v[176:179],v[88:91]\n"
"s_add_u32 s70,s70,20480\n"
"v_permlane16_swap_b32 v244,v246\n"
"v_permlane16_swap_b32 v245,v247\n"
MF "v[88:91],a[76:79],v[180:183],v[88:91]\n"
"buffer_store_dwordx4 v[244:247],v8,s[44:47],s83 offen sc1\n"
MF "v[88:91],a[116:119],v[184:187],v[88:91]\n"
"s_waitcnt vmcnt(26) lgkmcnt(0)\n"
"s_barrier\n"
MF "v[92:95],a[116:119],a[244:247],v[92:95]\n"
"s_add_u32 s88,s88,s65\n"
"s_mov_b32 s83,s84\n"
TR "v[176:177],v4\n"
MF "v[92:95],a[76:79],a[240:243],v[92:95]\n"
"s_add_u32 m0,s79,49152\n"
TR "v[178:179],v4 offset:2048\n"
MF "v[92:95],a[36:39],v[188:191],v[92:95]\n"
LD "v192,s[48:51],s88 offen lds\n"
MF "v[84:87],a[32:35],v[188:191],v[84:87]\n"
TR "v[180:181],v4 offset:4096\n"
MF "v[84:87],a[72:75],a[240:243],v[84:87]\n"
TR "v[182:183],v4 offset:6144\n"
MF "v[84:87],a[112:115],a[244:247],v[84:87]\n"
LD "v193,s[48:51],s88 offen offset:1024 lds\n"
MF "v[76:79],a[108:111],a[244:247],v[76:79]\n"
TR "v[184:185],v4 offset:8192\n"
MF "v[76:79],a[68:71],a[240:243],v[76:79]\n"
TR "v[186:187],v4 offset:10240\n"
MF "v[76:79],a[28:31],v[188:191],v[76:79]\n"
LD "v[208:211],v9,s[40:43],s82 offen sc1\n"
MF "v[68:71],a[24:27],v[188:191],v[68:71]\n"
MF "v[68:71],a[64:67],a[240:243],v[68:71]\n"
LD "v194,s[48:51],s88 offen offset:2048 lds\n"
MF "v[68:71],a[104:107],a[244:247],v[68:71]\n"
MF "v[60:63],a[100:103],a[244:247],v[60:63]\n"
MF "v[60:63],a[60:63],a[240:243],v[60:63]\n"
MF "v[60:63],a[20:23],v[188:191],v[60:63]\n"
"s_waitcnt lgkmcnt(0)\n"
LD "v195,s[48:51],s88 offen offset:3072 lds\n"
MF "v[16:19],a[140:143],v[176:179],v[16:19]\n"
TR "v[188:189],v5\n"
MF "v[16:19],a[180:183],v[180:183],v[16:19]\n"
"s_add_u32 s82,s82,s71\n"
TR "v[190:191],v5 offset:2048\n"
MF "v[16:19],a[220:223],v[184:187],v[16:19]\n"
LD "v[212:215],v9,s[40:43],s82 offen sc1\n"
MF "v[24:27],a[224:227],v[184:187],v[24:27]\n"
TR "a[240:241],v5 offset:4096\n"
MF "v[24:27],a[184:187],v[180:183],v[24:27]\n"
"s_add_u32 m0,s79,53248\n"
TR "a[242:243],v5 offset:6144\n"
MF "v[24:27],a[144:147],v[176:179],v[24:27]\n"
LD "v196,s[48:51],s88 offen lds\n"
MF "v[32:35],a[148:151],v[176:179],v[32:35]\n"
TR "a[244:245],v5 offset:8192\n"
MF "v[32:35],a[188:191],v[180:183],v[32:35]\n"
TR "a[246:247],v5 offset:10240\n"
MF "v[32:35],a[228:231],v[184:187],v[32:35]\n"
LD "v197,s[48:51],s88 offen offset:1024 lds\n"
MF "v[40:43],a[232:235],v[184:187],v[40:43]\n"
"s_add_u32 s82,s82,s71\n"
MF "v[40:43],a[192:195],v[180:183],v[40:43]\n"
LD "v[216:219],v9,s[40:43],s82 offen sc1\n"
MF "v[40:43],a[152:155],v[176:179],v[40:43]\n"
MF "v[48:51],a[156:159],v[176:179],v[48:51]\n"
MF "v[48:51],a[196:199],v[180:183],v[48:51]\n"
LD "v198,s[48:51],s88 offen offset:2048 lds\n"
MF "v[48:51],a[236:239],v[184:187],v[48:51]\n"
"s_waitcnt lgkmcnt(0)\n"
MF "v[52:55],a[236:239],a[244:247],v[52:55]\n"
TR "v[176:177],v6\n"
MF "v[52:55],a[196:199],a[240:243],v[52:55]\n"
TR "v[178:179],v6 offset:2048\n"
MF "v[52:55],a[156:159],v[188:191],v[52:55]\n"
LD "v199,s[48:51],s88 offen offset:3072 lds\n"
MF "v[44:47],a[152:155],v[188:191],v[44:47]\n"
TR "v[180:181],v6 offset:4096\n"
MF "v[44:47],a[192:195],a[240:243],v[44:47]\n"
"s_add_u32 s82,s82,s71\n"
TR "v[182:183],v6 offset:6144\n"
MF "v[44:47],a[232:235],a[244:247],v[44:47]\n"
LD "v[220:223],v9,s[40:43],s82 offen sc1\n"
MF "v[36:39],a[228:231],a[244:247],v[36:39]\n"
TR "v[184:185],v6 offset:8192\n"
MF "v[36:39],a[188:191],a[240:243],v[36:39]\n"
"s_add_u32 m0,s79,57344\n"
TR "v[186:187],v6 offset:10240\n"
MF "v[36:39],a[148:151],v[188:191],v[36:39]\n"
LD "v200,s[48:51],s88 offen lds\n"
MF "v[28:31],a[144:147],v[188:191],v[28:31]\n"
MF "v[28:31],a[184:187],a[240:243],v[28:31]\n"
"s_add_u32 s82,s82,s71\n"
MF "v[28:31],a[224:227],a[244:247],v[28:31]\n"
LD "v[224:227],v9,s[40:43],s82 offen sc1\n"
MF "v[20:23],a[220:223],a[244:247],v[20:23]\n"
LD "v201,s[48:51],s88 offen offset:1024 lds\n"
MF "v[20:23],a[180:183],a[240:243],v[20:23]\n"
MF "v[20:23],a[140:143],v[188:191],v[20:23]\n"
"s_waitcnt lgkmcnt(0)\n"
MF "v[56:59],a[140:143],v[176:179],v[56:59]\n"
TR "v[188:189],v7\n"
MF "v[56:59],a[180:183],v[180:183],v[56:59]\n"
TR "v[190:191],v7 offset:2048\n"
MF "v[56:59],a[220:223],v[184:187],v[56:59]\n"
LD "v202,s[48:51],s88 offen offset:2048 lds\n"
MF "v[64:67],a[224:227],v[184:187],v[64:67]\n"
TR "a[240:241],v7 offset:4096\n"
MF "v[64:67],a[184:187],v[180:183],v[64:67]\n"
TR "a[242:243],v7 offset:6144\n"
MF "v[64:67],a[144:147],v[176:179],v[64:67]\n"
LD "v203,s[48:51],s88 offen offset:3072 lds\n"
MF "v[72:75],a[148:151],v[176:179],v[72:75]\n"
TR "a[244:245],v7 offset:8192\n"
MF "v[72:75],a[188:191],v[180:183],v[72:75]\n"
"s_add_u32 s82,s82,s66\n"
TR "a[246:247],v7 offset:10240\n"
MF "v[72:75],a[228:231],v[184:187],v[72:75]\n"
LD "v[228:231],v9,s[40:43],s82 offen sc1\n"
MF "v[80:83],a[232:235],v[184:187],v[80:83]\n"
MF "v[80:83],a[192:195],v[180:183],v[80:83]\n"
MF "v[80:83],a[152:155],v[176:179],v[80:83]\n"
MF "v[88:91],a[156:159],v[176:179],v[88:91]\n"
"s_add_u32 s82,s82,s71\n"
MF "v[88:91],a[196:199],v[180:183],v[88:91]\n"
LD "v[232:235],v9,s[40:43],s82 offen sc1\n"
MF "v[88:91],a[236:239],v[184:187],v[88:91]\n"
"s_waitcnt vmcnt(32) lgkmcnt(0)\n"
"s_barrier\n"
MF "v[92:95],a[236:239],a[244:247],v[92:95]\n"
"s_add_u32 s80,s88,122880\n"
TR "v[176:177],v0 offset:24576\n"
MF "v[92:95],a[196:199],a[240:243],v[92:95]\n"
"s_add_u32 m0,s79,98304\n"
"s_add_u32 s85,s85,1\n"
TR "v[178:179],v0 offset:26624\n"
MF "v[92:95],a[156:159],v[188:191],v[92:95]\n"
LD "v192,s[48:51],s80 offen lds\n"
MF "v[84:87],a[152:155],v[188:191],v[84:87]\n"
"s_nop 0\n"
TR "v[180:181],v0 offset:28672\n"
"ds_write_addtid_b32 v16 offset:24576\n"
MF "v[84:87],a[192:195],a[240:243],v[84:87]\n"
"s_nop 0\n"
TR "v[182:183],v0 offset:30720\n"
"ds_write_addtid_b32 v17 offset:24832\n"
MF "v[84:87],a[232:235],a[244:247],v[84:87]\n"
LD "v193,s[48:51],s80 offen offset:1024 lds\n"
MF "v[76:79],a[228:231],a[244:247],v[76:79]\n"
"s_nop 0\n"
TR "v[184:185],v0 offset:32768\n"
"ds_write_addtid_b32 v18 offset:25088\n"
MF "v[76:79],a[188:191],a[240:243],v[76:79]\n"
"s_add_u32 s82,s82,s71\n"
TR "v[186:187],v0 offset:34816\n"
"ds_write_addtid_b32 v19 offset:25344\n"
MF "v[76:79],a[148:151],v[188:191],v[76:79]\n"
LD "v[236:239],v9,s[40:43],s82 offen sc1\n"
MF "v[68:71],a[144:147],v[188:191],v[68:71]\n"
LD "v194,s[48:51],s80 offen offset:2048 lds\n"
MF "v[68:71],a[184:187],a[240:243],v[68:71]\n"
"s_nop 0\n"
"ds_write_addtid_b32 v20 offset:25600\n"
"ds_write_addtid_b32 v21 offset:25856\n"
MF "v[68:71],a[224:227],a[244:247],v[68:71]\n"
"s_nop 0\n"
"ds_write_addtid_b32 v22 offset:26112\n"
"ds_write_addtid_b32 v23 offset:26368\n"
MF "v[60:63],a[220:223],a[244:247],v[60:63]\n"
"s_nop 0\n"
"ds_write_addtid_b32 v24 offset:26624\n"
"ds_write_addtid_b32 v25 offset:26880\n"
MF "v[60:63],a[180:183],a[240:243],v[60:63]\n"
"s_nop 0\n"
"ds_write_addtid_b32 v26 offset:27136\n"
"ds_write_addtid_b32 v27 offset:27392\n"
MF "v[60:63],a[140:143],v[188:191],v[60:63]\n"
"s_waitcnt lgkmcnt(9)\n"
"s_cmp_lg_u32 s85,9\n"
"s_cbranch_scc1 .Lpl0\n"
".Lplast0:\n"
MF "v[96:99],v[176:179],a[0:3],v[252:255]\n"
"s_nop 0\n"
TR "v[188:189],v1 offset:24576\n"
TR "v[190:191],v1 offset:26624\n"
MF "v[96:99],v[180:183],a[40:43],v[96:99]\n"
"s_nop 0\n"
TR "a[240:241],v1 offset:28672\n"
TR "a[242:243],v1 offset:30720\n"
MF "v[96:99],v[184:187],a[80:83],v[96:99]\n"
"s_add_u32 s90,s91,163840\n"
TR "a[244:245],v1 offset:32768\n"
TR "a[246:247],v1 offset:34816\n"
MF "v[104:107],v[184:187],a[84:87],v[252:255]\n"
LD "v195,s[48:51],s80 offen offset:3072 lds\n"
MF "v[104:107],v[180:183],a[44:47],v[104:107]\n"
"s_nop 0\n"
"ds_write_addtid_b32 v28 offset:27648\n"
"ds_write_addtid_b32 v29 offset:27904\n"
MF "v[104:107],v[176:179],a[4:7],v[104:107]\n"
"s_nop 0\n"
"ds_write_addtid_b32 v30 offset:28160\n"
"ds_write_addtid_b32 v31 offset:28416\n"
MF "v[112:115],v[176:179],a[8:11],v[252:255]\n"
"s_nop 0\n"
"ds_write_addtid_b32 v32 offset:28672\n"
"ds_write_addtid_b32 v33 offset:28928\n"
MF "v[112:115],v[180:183],a[48:51],v[112:115]\n"
"s_add_u32 m0,s79,102400\n"
"s_nop 0\n"
"ds_write_addtid_b32 v34 offset:25088\n"
"ds_write_addtid_b32 v35 offset:25344\n"
MF "v[112:115],v[184:187],a[88:91],v[112:115]\n"
LD "v196,s[48:51],s80 offen lds\n"
MF "v[120:123],v[184:187],a[92:95],v[252:255]\n"
"s_nop 0\n"
"ds_write_addtid_b32 v36 offset:25600\n"
"ds_write_addtid_b32 v37 offset:25856\n"
MF "v[120:123],v[180:183],a[52:55],v[120:123]\n"
"ds_write_addtid_b32 v38 offset:26112\n"
MF "v[120:123],v[176:179],a[12:15],v[120:123]\n"
"ds_write_addtid_b32 v39 offset:26368\n"
MF "v[128:131],v[176:179],a[16:19],v[252:255]\n"
"ds_write_addtid_b32 v40 offset:26624\n"
MF "v[128:131],v[180:183],a[56:59],v[128:131]\n"
"ds_write_addtid_b32 v41 offset:26880\n"
MF "v[128:131],v[184:187],a[96:99],v[128:131]\n"
"s_waitcnt lgkmcnt(14)\n"
LD "v197,s[48:51],s80 offen offset:1024 lds\n"
MF "v[132:135],a[244:247],a[96:99],v[252:255]\n"
"s_nop 0\n"
TR "v[176:177],v2 offset:24576\n"
TR "v[178:179],v2 offset:26624\n"
MF "v[132:135],a[240:243],a[56:59],v[132:135]\n"
"s_nop 0\n"
TR "v[180:181],v2 offset:28672\n"
TR "v[182:183],v2 offset:30720\n"
MF "v[132:135],v[188:191],a[16:19],v[132:135]\n"
"s_nop 0\n"
TR "v[184:185],v2 offset:32768\n"
TR "v[186:187],v2 offset:34816\n"
MF "v[124:127],v[188:191],a[12:15],v[252:255]\n"
"s_nop 0\n"
"ds_write_addtid_b32 v42 offset:27136\n"
"ds_write_addtid_b32 v43 offset:27392\n"
MF "v[124:127],a[240:243],a[52:55],v[124:127]\n"
"s_nop 0\n"
"ds_write_addtid_b32 v44 offset:27648\n"
"ds_write_addtid_b32 v45 offset:27904\n"
MF "v[124:127],a[244:247],a[92:95],v[124:127]\n"
"s_nop 0\n"
"ds_write_addtid_b32 v46 offset:28160\n"
"ds_write_addtid_b32 v47 offset:28416\n"
MF "v[116:119],a[244:247],a[88:91],v[252:255]\n"
LD "v198,s[48:51],s80 offen offset:2048 lds\n"
MF "v[116:119],a[240:243],a[48:51],v[116:119]\n"
"s_nop 0\n"
"ds_write_addtid_b32 v48 offset:28672\n"
"ds_write_addtid_b32 v49 offset:28928\n"
MF "v[116:119],v[188:191],a[8:11],v[116:119]\n"
"s_nop 0\n"
"ds_write_addtid_b32 v50 offset:29184\n"
"ds_write_addtid_b32 v51 offset:29440\n"
MF "v[108:111],v[188:191],a[4:7],v[252:255]\n"
"ds_write_addtid_b32 v52 offset:29696\n"
MF "v[108:111],a[240:243],a[44:47],v[108:111]\n"
"ds_write_addtid_b32 v53 offset:29952\n"
MF "v[108:111],a[244:247],a[84:87],v[108:111]\n"
"ds_write_addtid_b32 v54 offset:30208\n"
MF "v[100:103],a[244:247],a[80:83],v[252:255]\n"
"ds_write_addtid_b32 v55 offset:30464\n"
MF "v[100:103],a[240:243],a[40:43],v[100:103]\n"
LD "v199,s[48:51],s80 offen offset:3072 lds\n"
MF "v[100:103],v[188:191],a[0:3],v[100:103]\n"
"s_waitcnt lgkmcnt(14)\n"
MF "v[136:139],v[176:179],a[0:3],v[252:255]\n"
"s_nop 0\n"
TR "v[188:189],v3 offset:24576\n"
TR "v[190:191],v3 offset:26624\n"
MF "v[136:139],v[180:183],a[40:43],v[136:139]\n"
"s_nop 0\n"
TR "a[240:241],v3 offset:28672\n"
TR "a[242:243],v3 offset:30720\n"
MF "v[136:139],v[184:187],a[80:83],v[136:139]\n"
"s_add_u32 m0,s79,106496\n"
TR "a[244:245],v3 offset:32768\n"
TR "a[246:247],v3 offset:34816\n"
MF "v[144:147],v[184:187],a[84:87],v[252:255]\n"
"s_waitcnt lgkmcnt(0)\n"
"s_barrier\n"
MF "v[144:147],v[180:183],a[44:47],v[144:147]\n"
LD "v200,s[48:51],s80 offen lds\n"
MF "v[144:147],v[176:179],a[4:7],v[144:147]\n"
"ds_read2st64_b32 v[16:17],v13 offset0:0 offset1:1\n"
MF "v[152:155],v[176:179],a[8:11],v[252:255]\n"
"ds_read2st64_b32 v[18:19],v13 offset0:2 offset1:3\n"
MF "v[152:155],v[180:183],a[48:51],v[152:155]\n"
"ds_read2st64_b32 v[20:21],v13 offset0:4 offset1:5\n"
MF "v[152:155],v[184:187],a[88:91],v[152:155]\n"
"ds_read2st64_b32 v[22:23],v13 offset0:6 offset1:7\n"
MF "v[160:163],v[184:187],a[92:95],v[252:255]\n"
"ds_read2st64_b32 v[24:25],v13 offset0:8 offset1:9\n"
MF "v[160:163],v[180:183],a[52:55],v[160:163]\n"
"ds_read2st64_b32 v[26:27],v13 offset0:10 offset1:11\n"
MF "v[160:163],v[176:179],a[12:15],v[160:163]\n"
LD "v201,s[48:51],s80 offen offset:1024 lds\n"
MF "v[168:171],v[176:179],a[16:19],v[252:255]\n"
"ds_read2st64_b32 v[28:29],v13 offset0:12 offset1:13\n"
MF "v[168:171],v[180:183],a[56:59],v[168:171]\n"
"s_waitcnt lgkmcnt(3)\n"
"ds_read2st64_b32 v[30:31],v13 offset0:14 offset1:15\n"
"v_add_f32 v56,v56,v16\n"
MF "v[168:171],v[184:187],a[96:99],v[168:171]\n"
"s_waitcnt vmcnt(31) lgkmcnt(0)\n"
"s_barrier\n"
MF "v[172:175],a[244:247],a[96:99],v[252:255]\n"
"s_nop 0\n"
TR "v[176:177],v0\n"
TR "v[178:179],v0 offset:2048\n"
MF "v[172:175],a[240:243],a[56:59],v[172:175]\n"
"s_nop 0\n"
TR "v[180:181],v0 offset:4096\n"
TR "v[182:183],v0 offset:6144\n"
MF "v[172:175],v[188:191],a[16:19],v[172:175]\n"
"s_nop 0\n"
TR "v[184:185],v0 offset:8192\n"
TR "v[186:187],v0 offset:10240\n"
MF "v[164:167],v[188:191],a[12:15],v[252:255]\n"
LD "v202,s[48:51],s80 offen offset:2048 lds\n"
MF "v[164:167],a[240:243],a[52:55],v[164:167]\n"
"ds_read2st64_b32 v[32:33],v13 offset0:16 offset1:17\n"
"v_add_f32 v57,v57,v17\n"
MF "v[164:167],a[244:247],a[92:95],v[164:167]\n"
"ds_read2st64_b32 v[34:35],v13 offset0:18 offset1:19\n"
"v_add_f32 v58,v58,v18\n"
MF "v[156:159],a[244:247],a[88:91],v[252:255]\n"
"ds_read2st64_b32 v[36:37],v13 offset0:20 offset1:21\n"
"v_add_f32 v59,v59,v19\n"
MF "v[156:159],a[240:243],a[48:51],v[156:159]\n"
"ds_read2st64_b32 v[38:39],v13 offset0:22 offset1:23\n"
"v_add_f32 v60,v60,v20\n"
MF "v[156:159],v[188:191],a[8:11],v[156:159]\n"
"ds_read2st64_b32 v[40:41],v13 offset0:24 offset1:25\n"
"v_add_f32 v61,v61,v21\n"
MF "v[148:151],v[188:191],a[4:7],v[252:255]\n"
LD "v203,s[48:51],s80 offen offset:3072 lds\n"
MF "v[148:151],a[240:243],a[44:47],v[148:151]\n"
"ds_read2st64_b32 v[42:43],v13 offset0:26 offset1:27\n"
"v_add_f32 v62,v62,v22\n"
MF "v[148:151],a[244:247],a[84:87],v[148:151]\n"
"ds_read2st64_b32 v[44:45],v13 offset0:28 offset1:29\n"
"v_add_f32 v63,v63,v23\n"
MF "v[140:143],a[244:247],a[80:83],v[252:255]\n"
"ds_read2st64_b32 v[46:47],v13 offset0:30 offset1:31\n"
CV "v56,v56,v57\n"
MF "v[140:143],a[240:243],a[40:43],v[140:143]\n"
"ds_read2st64_b32 v[48:49],v13 offset0:32 offset1:33\n"
CV "v57,v58,v59\n"
MF "v[140:143],v[188:191],a[0:3],v[140:143]\n"
"s_waitcnt lgkmcnt(9)\n"
"ds_read2st64_b32 v[50:51],v13 offset0:34 offset1:35\n"
CV "v58,v60,v61\n"
MF "v[96:99],v[176:179],a[120:123],v[96:99]\n"
"s_nop 0\n"
TR "v[188:189],v1\n"
TR "v[190:191],v1 offset:2048\n"
MF "v[96:99],v[180:183],a[160:163],v[96:99]\n"
"s_nop 0\n"
TR "a[240:241],v1 offset:4096\n"
TR "a[242:243],v1 offset:6144\n"
MF "v[96:99],v[184:187],a[200:203],v[96:99]\n"
"s_nop 0\n"
TR "a[244:245],v1 offset:8192\n"
TR "a[246:247],v1 offset:10240\n"
MF "v[104:107],v[184:187],a[204:207],v[104:107]\n"
"ds_read2st64_b32 v[52:53],v13 offset0:36 offset1:37\n"
CV "v59,v62,v63\n"
MF "v[104:107],v[180:183],a[164:167],v[104:107]\n"
"s_add_u32 s87,s90,10240\n"
"ds_read2st64_b32 v[54:55],v13 offset0:38 offset1:39\n"
"v_add_f32 v64,v64,v24\n"
MF "v[104:107],v[176:179],a[124:127],v[104:107]\n"
"buffer_store_dwordx4 v[56:59],v9,s[40:43],s87 offen sc1\n"
MF "v[112:115],v[176:179],a[128:131],v[112:115]\n"
"v_add_f32 v65,v65,v25\n"
"v_add_f32 v66,v66,v26\n"
MF "v[112:115],v[180:183],a[168:171],v[112:115]\n"
"v_add_f32 v67,v67,v27\n"
"v_add_f32 v68,v68,v28\n"
MF "v[112:115],v[184:187],a[208:211],v[112:115]\n"
"v_add_f32 v69,v69,v29\n"
"v_add_f32 v70,v70,v30\n"
MF "v[120:123],v[184:187],a[212:215],v[120:123]\n"
"v_add_f32 v71,v71,v31\n"
CV "v64,v64,v65\n"
MF "v[120:123],v[180:183],a[172:175],v[120:123]\n"
CV "v65,v66,v67\n"
CV "v66,v68,v69\n"
MF "v[120:123],v[176:179],a[132:135],v[120:123]\n"
"s_add_u32 s87,s90,11264\n"
"s_waitcnt lgkmcnt(14)\n"
CV "v67,v70,v71\n"
MF "v[128:131],v[176:179],a[136:139],v[128:131]\n"
"buffer_store_dwordx4 v[64:67],v9,s[40:43],s87 offen sc1\n"
MF "v[128:131],v[180:183],a[176:179],v[128:131]\n"
"v_add_f32 v72,v72,v32\n"
"v_add_f32 v73,v73,v33\n"
MF "v[128:131],v[184:187],a[216:219],v[128:131]\n"
"s_waitcnt lgkmcnt(2)\n"
"v_add_f32 v74,v74,v34\n"
"v_add_f32 v75,v75,v35\n"
MF "v[132:135],a[244:247],a[216:219],v[132:135]\n"
"s_nop 0\n"
TR "v[176:177],v2\n"
TR "v[178:179],v2 offset:2048\n"
MF "v[132:135],a[240:243],a[176:179],v[132:135]\n"
"s_nop 0\n"
TR "v[180:181],v2 offset:4096\n"
TR "v[182:183],v2 offset:6144\n"
MF "v[132:135],v[188:191],a[136:139],v[132:135]\n"
"s_nop 0\n"
TR "v[184:185],v2 offset:8192\n"
TR "v[186:187],v2 offset:10240\n"
MF "v[124:127],v[188:191],a[132:135],v[124:127]\n"
"v_add_f32 v76,v76,v36\n"
"v_add_f32 v77,v77,v37\n"
MF "v[124:127],a[240:243],a[172:175],v[124:127]\n"
"v_add_f32 v78,v78,v38\n"
"v_add_f32 v79,v79,v39\n"
MF "v[124:127],a[244:247],a[212:215],v[124:127]\n"
CV "v72,v72,v73\n"
CV "v73,v74,v75\n"
MF "v[116:119],a[244:247],a[208:211],v[116:119]\n"
"s_add_u32 s87,s90,12288\n"
CV "v74,v76,v77\n"
CV "v75,v78,v79\n"
MF "v[116:119],a[240:243],a[168:171],v[116:119]\n"
"buffer_store_dwordx4 v[72:75],v9,s[40:43],s87 offen sc1\n"
MF "v[116:119],v[188:191],a[128:131],v[116:119]\n"
"v_add_f32 v80,v80,v40\n"
"v_add_f32 v81,v81,v41\n"
MF "v[108:111],v[188:191],a[124:127],v[108:111]\n"
"v_add_f32 v82,v82,v42\n"
"v_add_f32 v83,v83,v43\n"
MF "v[108:111],a[240:243],a[164:167],v[108:111]\n"
"v_add_f32 v84,v84,v44\n"
"v_add_f32 v85,v85,v45\n"
MF "v[108:111],a[244:247],a[204:207],v[108:111]\n"
"v_add_f32 v86,v86,v46\n"
"v_add_f32 v87,v87,v47\n"
MF "v[100:103],a[244:247],a[200:203],v[100:103]\n"
CV "v80,v80,v81\n"
CV "v81,v82,v83\n"
MF "v[100:103],a[240:243],a[160:163],v[100:103]\n"
"s_add_u32 s87,s90,13312\n"
CV "v82,v84,v85\n"
CV "v83,v86,v87\n"
MF "v[100:103],v[188:191],a[120:123],v[100:103]\n"
"s_waitcnt lgkmcnt(0)\n"
"buffer_store_dwordx4 v[80:83],v9,s[40:43],s87 offen sc1\n"
MF "v[136:139],v[176:179],a[120:123],v[136:139]\n"
"s_nop 0\n"
TR "v[188:189],v3\n"
TR "v[190:191],v3 offset:2048\n"
MF "v[136:139],v[180:183],a[160:163],v[136:139]\n"
"s_nop 0\n"
TR "a[240:241],v3 offset:4096\n"
TR "a[242:243],v3 offset:6144\n"
MF "v[136:139],v[184:187],a[200:203],v[136:139]\n"
"s_nop 0\n"
TR "a[244:245],v3 offset:8192\n"
TR "a[246:247],v3 offset:10240\n"
MF "v[144:147],v[184:187],a[204:207],v[144:147]\n"
"v_add_f32 v88,v88,v48\n"
"v_add_f32 v89,v89,v49\n"
MF "v[144:147],v[180:183],a[164:167],v[144:147]\n"
"v_add_f32 v90,v90,v50\n"
"v_add_f32 v91,v91,v51\n"
MF "v[144:147],v[176:179],a[124:127],v[144:147]\n"
"v_add_f32 v92,v92,v52\n"
"v_add_f32 v93,v93,v53\n"
MF "v[152:155],v[176:179],a[128:131],v[152:155]\n"
"v_add_f32 v94,v94,v54\n"
"v_add_f32 v95,v95,v55\n"
MF "v[152:155],v[180:183],a[168:171],v[152:155]\n"
CV "v88,v88,v89\n"
CV "v89,v90,v91\n"
MF "v[152:155],v[184:187],a[208:211],v[152:155]\n"
"s_add_u32 s87,s90,14336\n"
CV "v90,v92,v93\n"
CV "v91,v94,v95\n"
MF "v[160:163],v[184:187],a[212:215],v[160:163]\n"
"buffer_store_dwordx4 v[88:91],v9,s[40:43],s87 offen sc1\n"
MF "v[160:163],v[180:183],a[172:175],v[160:163]\n"
MF "v[160:163],v[176:179],a[132:135],v[160:163]\n"
MF "v[168:171],v[176:179],a[136:139],v[168:171]\n"
MF "v[168:171],v[180:183],a[176:179],v[168:171]\n"
MF "v[168:171],v[184:187],a[216:219],v[168:171]\n"
"s_waitcnt vmcnt(20) lgkmcnt(0)\n"
"s_barrier\n"
MF "v[172:175],a[244:247],a[216:219],v[172:175]\n"
"s_nop 0\n"
TR "v[176:177],v0 offset:49152\n"
TR "v[178:179],v0 offset:51200\n"
MF "v[172:175],a[240:243],a[176:179],v[172:175]\n"
"s_nop 0\n"
TR "v[180:181],v0 offset:53248\n"
TR "v[182:183],v0 offset:55296\n"
MF "v[172:175],v[188:191],a[136:139],v[172:175]\n"
"s_nop 0\n"
TR "v[184:185],v0 offset:57344\n"
TR "v[186:187],v0 offset:59392\n"
MF "v[164:167],v[188:191],a[132:135],v[164:167]\n"
"buffer_load_dword v10,v12,s[40:43],s86 offen sc1\n"
MF "v[164:167],a[240:243],a[172:175],v[164:167]\n"
MF "v[164:167],a[244:247],a[212:215],v[164:167]\n"
MF "v[156:159],a[244:247],a[208:211],v[156:159]\n"
MF "v[156:159],a[240:243],a[168:171],v[156:159]\n"
MF "v[156:159],v[188:191],a[128:131],v[156:159]\n"
MF "v[148:151],v[188:191],a[124:127],v[148:151]\n"
MF "v[148:151],a[240:243],a[164:167],v[148:151]\n"
MF "v[148:151],a[244:247],a[204:207],v[148:151]\n"
MF "v[140:143],a[244:247],a[200:203],v[140:143]\n"
MF "v[140:143],a[240:243],a[160:163],v[140:143]\n"
MF "v[140:143],v[188:191],a[120:123],v[140:143]\n"
"s_waitcnt lgkmcnt(0)\n"
MF "v[96:99],v[176:179],a[20:23],v[96:99]\n"
"s_nop 0\n"
TR "v[188:189],v1 offset:49152\n"
TR "v[190:191],v1 offset:51200\n"
MF "v[96:99],v[180:183],a[60:63],v[96:99]\n"
"s_nop 0\n"
TR "a[240:241],v1 offset:53248\n"
TR "a[242:243],v1 offset:55296\n"
MF "v[96:99],v[184:187],a[100:103],v[96:99]\n"
"s_nop 0\n"
TR "a[244:245],v1 offset:57344\n"
TR "a[246:247],v1 offset:59392\n"
MF "v[104:107],v[184:187],a[104:107],v[104:107]\n"
MF "v[104:107],v[180:183],a[64:67],v[104:107]\n"
MF "v[104:107],v[176:179],a[24:27],v[104:107]\n"
"s_waitcnt vmcnt(16)\n"
MF "v[96:99],v[208:211],a[252:255],v[96:99]\n"
MF "v[104:107],v[212:215],a[252:255],v[104:107]\n"
MF "v[112:115],v[216:219],a[252:255],v[112:115]\n"
MF "v[120:123],v[220:223],a[252:255],v[120:123]\n"
MF "v[128:131],v[224:227],a[252:255],v[128:131]\n"
MF "v[100:103],v[208:211],a[248:251],v[100:103]\n"
MF "v[108:111],v[212:215],a[248:251],v[108:111]\n"
MF "v[116:119],v[216:219],a[248:251],v[116:119]\n"
MF "v[124:127],v[220:223],a[248:251],v[124:127]\n"
MF "v[132:135],v[224:227],a[248:251],v[132:135]\n"
MF "v[112:115],v[176:179],a[28:31],v[112:115]\n"
MF "v[112:115],v[180:183],a[68:71],v[112:115]\n"
MF "v[112:115],v[184:187],a[108:111],v[112:115]\n"
MF "v[120:123],v[184:187],a[112:115],v[120:123]\n"
MF "v[120:123],v[180:183],a[72:75],v[120:123]\n"
"s_bfe_u32 s87,s22,0x30014\n"
MF "v[120:123],v[176:179],a[32:35],v[120:123]\n"
"s_lshl_b32 s87,s87,7\n"
MF "v[128:131],v[176:179],a[36:39],v[128:131]\n"
"s_and_b32 s60,s56,1\n"
MF "v[128:131],v[180:183],a[76:79],v[128:131]\n"
"s_mul_i32 s60,s60,204800\n"
MF "v[128:131],v[184:187],a[116:119],v[128:131]\n"
"s_waitcnt lgkmcnt(0)\n"
MF "v[132:135],a[244:247],a[116:119],v[132:135]\n"
"s_add_u32 s87,s87,s60\n"
TR "v[176:177],v2 offset:49152\n"
TR "v[178:179],v2 offset:51200\n"
MF "v[132:135],a[240:243],a[76:79],v[132:135]\n"
".Lpg24h:\n"
"s_cbranch_execz .Lpg27h\n"
"s_mov_b32 s71,204800\n"
TR "v[180:181],v2 offset:53248\n"
TR "v[182:183],v2 offset:55296\n"
MF "v[132:135],v[188:191],a[36:39],v[132:135]\n"
"s_mov_b32 s66,204800\n"
TR "v[184:185],v2 offset:57344\n"
TR "v[186:187],v2 offset:59392\n"
MF "v[124:127],v[188:191],a[32:35],v[124:127]\n"
"s_add_u32 s72,s78,2\n"
MF "v[124:127],a[240:243],a[72:75],v[124:127]\n"
"s_and_b32 s72,s72,15\n"
MF "v[124:127],a[244:247],a[112:115],v[124:127]\n"
"s_bfe_u32 s60,s22,0x10014\n"
MF "v[116:119],a[244:247],a[108:111],v[116:119]\n"
"s_lshl_b32 s60,s60,5\n"
MF "v[116:119],a[240:243],a[68:71],v[116:119]\n"
"v_xor_b32 v242,s60,v8\n"
MF "v[116:119],v[188:191],a[28:31],v[116:119]\n"
MF "v[108:111],v[188:191],a[24:27],v[108:111]\n"
"s_add_u32 s16,s78,6\n"
MF "v[108:111],a[240:243],a[64:67],v[108:111]\n"
"s_and_b32 s16,s16,15\n"
MF "v[108:111],a[244:247],a[104:107],v[108:111]\n"
"s_add_u32 s17,s78,4\n"
MF "v[100:103],a[244:247],a[100:103],v[100:103]\n"
"s_and_b32 s17,s17,15\n"
MF "v[100:103],a[240:243],a[60:63],v[100:103]\n"
MF "v[100:103],v[188:191],a[20:23],v[100:103]\n"
"s_waitcnt lgkmcnt(0)\n"
MF "v[136:139],v[176:179],a[20:23],v[136:139]\n"
"s_nop 0\n"
TR "v[188:189],v3 offset:49152\n"
TR "v[190:191],v3 offset:51200\n"
MF "v[136:139],v[180:183],a[60:63],v[136:139]\n"
"s_nop 0\n"
TR "a[240:241],v3 offset:53248\n"
TR "a[242:243],v3 offset:55296\n"
MF "v[136:139],v[184:187],a[100:103],v[136:139]\n"
"s_nop 0\n"
TR "a[244:245],v3 offset:57344\n"
TR "a[246:247],v3 offset:59392\n"
MF "v[144:147],v[184:187],a[104:107],v[144:147]\n"
MF "v[144:147],v[180:183],a[64:67],v[144:147]\n"
MF "v[144:147],v[176:179],a[24:27],v[144:147]\n"
MF "v[152:155],v[176:179],a[28:31],v[152:155]\n"
MF "v[152:155],v[180:183],a[68:71],v[152:155]\n"
MF "v[152:155],v[184:187],a[108:111],v[152:155]\n"
MF "v[160:163],v[184:187],a[112:115],v[160:163]\n"
MF "v[160:163],v[180:183],a[72:75],v[160:163]\n"
MF "v[160:163],v[176:179],a[32:35],v[160:163]\n"
MF "v[168:171],v[176:179],a[36:39],v[168:171]\n"
MF "v[168:171],v[180:183],a[76:79],v[168:171]\n"
MF "v[168:171],v[184:187],a[116:119],v[168:171]\n"
"s_waitcnt vmcnt(0) lgkmcnt(0)\n"
"s_barrier\n"
MF "v[172:175],a[244:247],a[116:119],v[172:175]\n"
"v_sub_u32 v10,v10,v11\n"
MF "v[172:175],a[240:243],a[76:79],v[172:175]\n"
"s_nop 0\n"
TR "v[176:177],v4\n"
TR "v[178:179],v4 offset:2048\n"
MF "v[172:175],v[188:191],a[36:39],v[172:175]\n"
"s_nop 0\n"
TR "v[180:181],v4 offset:4096\n"
TR "v[182:183],v4 offset:6144\n"
MF "v[164:167],v[188:191],a[32:35],v[164:167]\n"
"s_nop 0\n"
TR "v[184:185],v4 offset:8192\n"
TR "v[186:187],v4 offset:10240\n"
MF "v[164:167],a[240:243],a[72:75],v[164:167]\n"
"v_readlane_b32 s16,v10,s16\n"
MF "v[164:167],a[244:247],a[112:115],v[164:167]\n"
"s_cmp_lt_u32 s16,9\n"
"s_cselect_b32 s16,0xf0000000,0\n"
MF "v[156:159],a[244:247],a[108:111],v[156:159]\n"
"v_readlane_b32 s17,v10,s17\n"
MF "v[156:159],a[240:243],a[68:71],v[156:159]\n"
"s_cmp_lt_u32 s17,10\n"
"s_cselect_b32 s17,0xf0000000,0\n"
MF "v[156:159],v[188:191],a[28:31],v[156:159]\n"
"s_bfe_u32 s62,s22,0x30014\n"
MF "v[148:151],v[188:191],a[24:27],v[148:151]\n"
"s_add_u32 s62,s62,3\n"
"s_and_b32 s62,s62,7\n"
"s_mul_i32 s62,s62,204800\n"
MF "v[148:151],a[240:243],a[64:67],v[148:151]\n"
"s_add_u32 s62,s62,s73\n"
"s_add_u32 s62,s62,133120\n"
"s_or_b32 s62,s62,s16\n"
MF "v[148:151],a[244:247],a[104:107],v[148:151]\n"
LD "v[16:19],v9,s[40:43],s62 offen sc1\n"
MF "v[140:143],a[244:247],a[100:103],v[140:143]\n"
LD "v[24:27],v9,s[40:43],s62 offen offset:1024 sc1\n"
MF "v[140:143],a[240:243],a[60:63],v[140:143]\n"
MF "v[140:143],v[188:191],a[20:23],v[140:143]\n"
"s_waitcnt lgkmcnt(0)\n"
LD "v[32:35],v9,s[40:43],s62 offen offset:2048 sc1\n"
MF "v[96:99],v[176:179],a[140:143],v[96:99]\n"
"s_nop 0\n"
TR "v[188:189],v5\n"
TR "v[190:191],v5 offset:2048\n"
MF "v[96:99],v[180:183],a[180:183],v[96:99]\n"
"s_nop 0\n"
TR "a[240:241],v5 offset:4096\n"
TR "a[242:243],v5 offset:6144\n"
MF "v[96:99],v[184:187],a[220:223],v[96:99]\n"
"s_nop 0\n"
TR "a[244:245],v5 offset:8192\n"
TR "a[246:247],v5 offset:10240\n"
MF "v[104:107],v[184:187],a[224:227],v[104:107]\n"
LD "v[40:43],v9,s[40:43],s62 offen offset:3072 sc1\n"
MF "v[104:107],v[180:183],a[184:187],v[104:107]\n"
MF "v[104:107],v[176:179],a[144:147],v[104:107]\n"
"s_add_u32 s62,s62,4096\n"
MF "v[112:115],v[176:179],a[148:151],v[112:115]\n"
LD "v[48:51],v9,s[40:43],s62 offen sc1\n"
MF "v[112:115],v[180:183],a[188:191],v[112:115]\n"
"s_bfe_u32 s62,s22,0x30014\n"
"ds_write_addtid_b32 v96 offset:16384\n"
"ds_write_addtid_b32 v97 offset:16640\n"
MF "v[112:115],v[184:187],a[228:231],v[112:115]\n"
"s_add_u32 s62,s62,2\n"
"s_and_b32 s62,s62,7\n"
"s_mul_i32 s62,s62,204800\n"
MF "v[120:123],v[184:187],a[232:235],v[120:123]\n"
"s_add_u32 s62,s62,s73\n"
"s_add_u32 s62,s62,153600\n"
"s_or_b32 s62,s62,s17\n"
MF "v[120:123],v[180:183],a[192:195],v[120:123]\n"
LD "v[20:23],v9,s[40:43],s62 offen sc1\n"
MF "v[120:123],v[176:179],a[152:155],v[120:123]\n"
"s_nop 0\n"
"ds_write_addtid_b32 v98 offset:16896\n"
"ds_write_addtid_b32 v99 offset:17152\n"
MF "v[128:131],v[176:179],a[156:159],v[128:131]\n"
LD "v[28:31],v9,s[40:43],s62 offen offset:1024 sc1\n"
MF "v[128:131],v[180:183],a[196:199],v[128:131]\n"
"s_nop 0\n"
"ds_write_addtid_b32 v104 offset:18432\n"
"ds_write_addtid_b32 v105 offset:18688\n"
MF "v[128:131],v[184:187],a[236:239],v[128:131]\n"
"s_waitcnt lgkmcnt(6)\n"
LD "v[36:39],v9,s[40:43],s62 offen offset:2048 sc1\n"
MF "v[100:103],v[188:191],a[140:143],v[100:103]\n"
"s_nop 0\n"
TR "v[176:177],v6\n"
TR "v[178:179],v6 offset:2048\n"
MF "v[100:103],a[240:243],a[180:183],v[100:103]\n"
"s_nop 0\n"
TR "v[180:181],v6 offset:4096\n"
TR "v[182:183],v6 offset:6144\n"
MF "v[100:103],a[244:247],a[220:223],v[100:103]\n"
"s_nop 0\n"
TR "v[184:185],v6 offset:8192\n"
TR "v[186:187],v6 offset:10240\n"
MF "v[108:111],a[244:247],a[224:227],v[108:111]\n"
LD "v[44:47],v9,s[40:43],s62 offen offset:3072 sc1\n"
MF "v[108:111],a[240:243],a[184:187],v[108:111]\n"
"s_nop 0\n"
"ds_write_addtid_b32 v106 offset:18944\n"
"ds_write_addtid_b32 v107 offset:19200\n"
MF "v[108:111],v[188:191],a[144:147],v[108:111]\n"
"s_add_u32 s62,s62,4096\n"
"ds_write_addtid_b32 v112 offset:20480\n"
"ds_write_addtid_b32 v113 offset:20736\n"
MF "v[116:119],v[188:191],a[148:151],v[116:119]\n"
LD "v[52:55],v9,s[40:43],s62 offen sc1\n"
MF "v[116:119],a[240:243],a[188:191],v[116:119]\n"
"s_nop 0\n"
"ds_write_addtid_b32 v114 offset:20992\n"
"ds_write_addtid_b32 v115 offset:21248\n"
MF "v[116:119],a[244:247],a[228:231],v[116:119]\n"
"s_nop 0\n"
"ds_write_addtid_b32 v120 offset:22528\n"
"ds_write_addtid_b32 v121 offset:22784\n"
MF "v[124:127],a[244:247],a[232:235],v[124:127]\n"
"s_nop 0\n"
"ds_write_addtid_b32 v122 offset:23040\n"
"ds_write_addtid_b32 v123 offset:23296\n"
MF "v[124:127],a[240:243],a[192:195],v[124:127]\n"
"s_nop 0\n"
"ds_write_addtid_b32 v128 offset:24576\n"
"ds_write_addtid_b32 v129 offset:24832\n"
MF "v[124:127],v[188:191],a[152:155],v[124:127]\n"
"buffer_load_dword v10,v12,s[40:43],s86 offen sc1\n"
MF "v[132:135],v[188:191],a[156:159],v[132:135]\n"
"s_nop 0\n"
"ds_write_addtid_b32 v130 offset:25088\n"
"ds_write_addtid_b32 v131 offset:25344\n"
MF "v[132:135],a[240:243],a[196:199],v[132:135]\n"
"s_nop 0\n"
"ds_write_addtid_b32 v100 offset:17408\n"
"ds_write_addtid_b32 v101 offset:17664\n"
MF "v[132:135],a[244:247],a[236:239],v[132:135]\n"
"s_waitcnt lgkmcnt(15)\n"
"ds_write_addtid_b32 v102 offset:17920\n"
"ds_write_addtid_b32 v103 offset:18176\n"
MF "v[136:139],v[176:179],a[140:143],v[136:139]\n"
"s_nop 0\n"
TR "v[188:189],v7\n"
TR "v[190:191],v7 offset:2048\n"
MF "v[136:139],v[180:183],a[180:183],v[136:139]\n"
"s_nop 0\n"
TR "a[240:241],v7 offset:4096\n"
TR "a[242:243],v7 offset:6144\n"
MF "v[136:139],v[184:187],a[220:223],v[136:139]\n"
"s_nop 0\n"
TR "a[244:245],v7 offset:8192\n"
TR "a[246:247],v7 offset:10240\n"
MF "v[144:147],v[184:187],a[224:227],v[144:147]\n"
"s_nop 0\n"
"ds_write_addtid_b32 v108 offset:19456\n"
"ds_write_addtid_b32 v109 offset:19712\n"
MF "v[144:147],v[180:183],a[184:187],v[144:147]\n"
"s_nop 0\n"
"ds_write_addtid_b32 v110 offset:19968\n"
"ds_write_addtid_b32 v111 offset:20224\n"
MF "v[144:147],v[176:179],a[144:147],v[144:147]\n"
"s_nop 0\n"
"ds_write_addtid_b32 v116 offset:21504\n"
"ds_write_addtid_b32 v117 offset:21760\n"
MF "v[152:155],v[176:179],a[148:151],v[152:155]\n"
"s_nop 0\n"
"ds_write_addtid_b32 v118 offset:22016\n"
"ds_write_addtid_b32 v119 offset:22272\n"
MF "v[152:155],v[180:183],a[188:191],v[152:155]\n"
"ds_write_addtid_b32 v124 offset:23552\n"
MF "v[152:155],v[184:187],a[228:231],v[152:155]\n"
"ds_write_addtid_b32 v125 offset:23808\n"
MF "v[160:163],v[184:187],a[232:235],v[160:163]\n"
"ds_write_addtid_b32 v126 offset:24064\n"
MF "v[160:163],v[180:183],a[192:195],v[160:163]\n"
"ds_write_addtid_b32 v127 offset:24320\n"
MF "v[160:163],v[176:179],a[152:155],v[160:163]\n"
"ds_write_addtid_b32 v132 offset:25600\n"
MF "v[168:171],v[176:179],a[156:159],v[168:171]\n"
"ds_write_addtid_b32 v133 offset:25856\n"
MF "v[168:171],v[180:183],a[196:199],v[168:171]\n"
"ds_write_addtid_b32 v134 offset:26112\n"
MF "v[168:171],v[184:187],a[236:239],v[168:171]\n"
"s_waitcnt lgkmcnt(15)\n"
"ds_write_addtid_b32 v135 offset:26368\n"
MF "v[140:143],v[188:191],a[140:143],v[140:143]\n"
MF "v[140:143],a[240:243],a[180:183],v[140:143]\n"
MF "v[140:143],a[244:247],a[220:223],v[140:143]\n"
MF "v[148:151],a[244:247],a[224:227],v[148:151]\n"
MF "v[148:151],a[240:243],a[184:187],v[148:151]\n"
"s_waitcnt lgkmcnt(0)\n"
"s_barrier\n"
MF "v[148:151],v[188:191],a[144:147],v[148:151]\n"
"ds_read2st64_b32 v[56:57],v13 offset0:0 offset1:1\n"
MF "v[156:159],v[188:191],a[148:151],v[156:159]\n"
"ds_read2st64_b32 v[58:59],v13 offset0:2 offset1:3\n"
MF "v[156:159],a[240:243],a[188:191],v[156:159]\n"
"ds_read2st64_b32 v[60:61],v13 offset0:4 offset1:5\n"
MF "v[156:159],a[244:247],a[228:231],v[156:159]\n"
"ds_read2st64_b32 v[62:63],v13 offset0:6 offset1:7\n"
MF "v[164:167],a[244:247],a[232:235],v[164:167]\n"
"ds_read2st64_b32 v[64:65],v13 offset0:8 offset1:9\n"
MF "v[164:167],a[240:243],a[192:195],v[164:167]\n"
"ds_read2st64_b32 v[66:67],v13 offset0:10 offset1:11\n"
MF "v[164:167],v[188:191],a[152:155],v[164:167]\n"
"ds_read2st64_b32 v[68:69],v13 offset0:12 offset1:13\n"
MF "v[172:175],v[188:191],a[156:159],v[172:175]\n"
"ds_read2st64_b32 v[70:71],v13 offset0:14 offset1:15\n"
MF "v[172:175],a[240:243],a[196:199],v[172:175]\n"
"ds_read2st64_b32 v[72:73],v13 offset0:16 offset1:17\n"
MF "v[172:175],a[244:247],a[236:239],v[172:175]\n"
"ds_read2st64_b32 v[74:75],v13 offset0:18 offset1:19\n"
"ds_read2st64_b32 v[76:77],v13 offset0:20 offset1:21\n"
"ds_read2st64_b32 v[78:79],v13 offset0:22 offset1:23\n"
"ds_read2st64_b32 v[80:81],v13 offset0:24 offset1:25\n"
"ds_read2st64_b32 v[82:83],v13 offset0:26 offset1:27\n"
"ds_read2st64_b32 v[84:85],v13 offset0:28 offset1:29\n"
"ds_read2st64_b32 v[86:87],v13 offset0:30 offset1:31\n"
"ds_read2st64_b32 v[88:89],v13 offset0:32 offset1:33\n"
"ds_read2st64_b32 v[90:91],v13 offset0:34 offset1:35\n"
"ds_read2st64_b32 v[92:93],v13 offset0:36 offset1:37\n"
"ds_read2st64_b32 v[94:95],v13 offset0:38 offset1:39\n"
"s_waitcnt vmcnt(0)\n"
"v_sub_u32 v10,v10,v11\n"
"s_cmp_eq_u32 s16,0\n"
"s_cbranch_scc1 .Lhave03\n"
"s_add_u32 s59,s78,6\n"
"s_and_b32 s59,s59,15\n"
"s_nop 3\n"
".Lnf2:\n"
"v_readlane_b32 s58,v10,s59\n"
"s_cmp_lt_u32 s58,9\n"
"s_cbranch_scc1 .Lsp2\n"
".Lnb2:\n"
"s_bfe_u32 s62,s22,0x30014\n"
"s_add_u32 s62,s62,3\n"
"s_and_b32 s62,s62,7\n"
"s_mul_i32 s62,s62,204800\n"
"s_add_u32 s62,s62,s73\n"
"s_add_u32 s62,s62,133120\n"
LD "v[16:19],v9,s[40:43],s62 offen sc1\n"
LD "v[24:27],v9,s[40:43],s62 offen offset:1024 sc1\n"
LD "v[32:35],v9,s[40:43],s62 offen offset:2048 sc1\n"
LD "v[40:43],v9,s[40:43],s62 offen offset:3072 sc1\n"
"s_add_u32 s62,s62,4096\n"
LD "v[48:51],v9,s[40:43],s62 offen sc1\n"
".Lhave03:\n"
"s_cmp_eq_u32 s17,0\n"
"s_cbranch_scc1 .Lhave02\n"
"s_add_u32 s59,s78,4\n"
"s_and_b32 s59,s59,15\n"
"s_nop 3\n"
".Lnf3:\n"
"v_readlane_b32 s58,v10,s59\n"
"s_cmp_lt_u32 s58,10\n"
"s_cbranch_scc1 .Lsp3\n"
".Lnb3:\n"
"s_bfe_u32 s62,s22,0x30014\n"
"s_add_u32 s62,s62,2\n"
"s_and_b32 s62,s62,7\n"
"s_mul_i32 s62,s62,204800\n"
"s_add_u32 s62,s62,s73\n"
"s_add_u32 s62,s62,153600\n"
LD "v[20:23],v9,s[40:43],s62 offen sc1\n"
LD "v[28:31],v9,s[40:43],s62 offen offset:1024 sc1\n"
LD "v[36:39],v9,s[40:43],s62 offen offset:2048 sc1\n"
LD "v[44:47],v9,s[40:43],s62 offen offset:3072 sc1\n"
"s_add_u32 s62,s62,4096\n"
LD "v[52:55],v9,s[40:43],s62 offen sc1\n"
".Lhave02:\n"
".Lnf4:\n"
"v_readlane_b32 s58,v10,s72\n"
"s_cmp_lt_u32 s58,11\n"
"s_cbranch_scc1 .Lsp4\n"
".Lnb4:\n"
"s_bfe_u32 s62,s22,0x30014\n"
"s_add_u32 s62,s62,1\n"
"s_and_b32 s62,s62,7\n"
"s_mul_i32 s62,s62,204800\n"
"s_add_u32 s62,s62,s73\n"
"s_add_u32 s62,s62,174080\n"
LD "v[208:211],v9,s[40:43],s62 offen sc1\n"
LD "v[212:215],v9,s[40:43],s62 offen offset:1024 sc1\n"
LD "v[216:219],v9,s[40:43],s62 offen offset:2048 sc1\n"
LD "v[220:223],v9,s[40:43],s62 offen offset:3072 sc1\n"
"s_add_u32 s62,s62,4096\n"
LD "v[224:227],v9,s[40:43],s62 offen sc1\n"
"s_waitcnt lgkmcnt(15)\n"
"v_pk_add_f32 v[136:137],v[136:137],v[56:57]\n"
"v_pk_add_f32 v[138:139],v[138:139],v[58:59]\n"
"v_pk_add_f32 v[140:141],v[140:141],v[60:61]\n"
"v_pk_add_f32 v[142:143],v[142:143],v[62:63]\n"
"v_pk_add_f32 v[144:145],v[144:145],v[64:65]\n"
"s_waitcnt lgkmcnt(14)\n"
"v_pk_add_f32 v[146:147],v[146:147],v[66:67]\n"
"s_waitcnt lgkmcnt(13)\n"
"v_pk_add_f32 v[148:149],v[148:149],v[68:69]\n"
"s_waitcnt lgkmcnt(12)\n"
"v_pk_add_f32 v[150:151],v[150:151],v[70:71]\n"
"s_waitcnt lgkmcnt(11)\n"
"v_pk_add_f32 v[152:153],v[152:153],v[72:73]\n"
"s_waitcnt lgkmcnt(10)\n"
"v_pk_add_f32 v[154:155],v[154:155],v[74:75]\n"
"s_waitcnt lgkmcnt(9)\n"
"v_pk_add_f32 v[156:157],v[156:157],v[76:77]\n"
"s_waitcnt lgkmcnt(8)\n"
"v_pk_add_f32 v[158:159],v[158:159],v[78:79]\n"
"s_waitcnt lgkmcnt(7)\n"
"v_pk_add_f32 v[160:161],v[160:161],v[80:81]\n"
"s_waitcnt lgkmcnt(6)\n"
"v_pk_add_f32 v[162:163],v[162:163],v[82:83]\n"
"s_waitcnt lgkmcnt(5)\n"
"v_pk_add_f32 v[164:165],v[164:165],v[84:85]\n"
"s_waitcnt lgkmcnt(4)\n"
"v_pk_add_f32 v[166:167],v[166:167],v[86:87]\n"
"s_waitcnt lgkmcnt(3)\n"
"v_pk_add_f32 v[168:169],v[168:169],v[88:89]\n"
"s_waitcnt lgkmcnt(2)\n"
"v_pk_add_f32 v[170:171],v[170:171],v[90:91]\n"
"s_waitcnt lgkmcnt(1)\n"
"v_pk_add_f32 v[172:173],v[172:173],v[92:93]\n"
"s_waitcnt lgkmcnt(0)\n"
"v_pk_add_f32 v[174:175],v[174:175],v[94:95]\n"
"s_waitcnt vmcnt(5)\n"
"s_nop 0\n"
MF "v[140:143],v[16:19],a[252:255],v[140:143]\n"
MF "v[148:151],v[24:27],a[252:255],v[148:151]\n"
MF "v[156:159],v[32:35],a[252:255],v[156:159]\n"
MF "v[164:167],v[40:43],a[252:255],v[164:167]\n"
MF "v[172:175],v[48:51],a[252:255],v[172:175]\n"
MF "v[136:139],v[16:19],a[248:251],v[136:139]\n"
MF "v[144:147],v[24:27],a[248:251],v[144:147]\n"
MF "v[152:155],v[32:35],a[248:251],v[152:155]\n"
MF "v[160:163],v[40:43],a[248:251],v[160:163]\n"
MF "v[168:171],v[48:51],a[248:251],v[168:171]\n"
MF "v[136:139],v[20:23],a[252:255],v[136:139]\n"
MF "v[144:147],v[28:31],a[252:255],v[144:147]\n"
MF "v[152:155],v[36:39],a[252:255],v[152:155]\n"
MF "v[160:163],v[44:47],a[252:255],v[160:163]\n"
MF "v[168:171],v[52:55],a[252:255],v[168:171]\n"
MF "v[140:143],v[20:23],a[248:251],v[140:143]\n"
MF "v[148:151],v[28:31],a[248:251],v[148:151]\n"
MF "v[156:159],v[36:39],a[248:251],v[156:159]\n"
MF "v[164:167],v[44:47],a[248:251],v[164:167]\n"
MF "v[172:175],v[52:55],a[248:251],v[172:175]\n"
"s_waitcnt vmcnt(0)\n"
MF "v[140:143],v[208:211],a[252:255],v[140:143]\n"
MF "v[136:139],v[208:211],a[248:251],v[136:139]\n"
MF "v[148:151],v[212:215],a[252:255],v[148:151]\n"
MF "v[144:147],v[212:215],a[248:251],v[144:147]\n"
MF "v[156:159],v[216:219],a[252:255],v[156:159]\n"
"s_nop 4\n"
CV "v136,v136,v137\n"
CV "v137,v138,v139\n"
MF "v[152:155],v[216:219],a[248:251],v[152:155]\n"
CV "v138,v140,v141\n"
CV "v139,v142,v143\n"
MF "v[164:167],v[220:223],a[252:255],v[164:167]\n"
CV "v144,v144,v145\n"
CV "v145,v146,v147\n"
MF "v[160:163],v[220:223],a[248:251],v[160:163]\n"
CV "v146,v148,v149\n"
CV "v147,v150,v151\n"
"v_permlane16_swap_b32 v136,v138\n"
"v_permlane16_swap_b32 v137,v139\n"
"buffer_store_dwordx4 v[136:139],v242,s[44:47],s87 offen offset:64 sc1\n"
"s_add_u32 s87,s87,40960\n"
MF "v[172:175],v[224:227],a[252:255],v[172:175]\n"
CV "v152,v152,v153\n"
CV "v153,v154,v155\n"
MF "v[168:171],v[224:227],a[248:251],v[168:171]\n"
CV "v154,v156,v157\n"
CV "v155,v158,v159\n"
"v_permlane16_swap_b32 v144,v146\n"
"v_permlane16_swap_b32 v145,v147\n"
"buffer_store_dwordx4 v[144:147],v242,s[44:47],s87 offen offset:64 sc1\n"
"s_add_u32 s87,s87,40960\n"
CV "v160,v160,v161\n"
CV "v161,v162,v163\n"
CV "v162,v164,v165\n"
CV "v163,v166,v167\n"
"v_permlane16_swap_b32 v152,v154\n"
"v_permlane16_swap_b32 v153,v155\n"
"buffer_store_dwordx4 v[152:155],v242,s[44:47],s87 offen offset:64 sc1\n"
"s_add_u32 s87,s87,40960\n"
CV "v168,v168,v169\n"
CV "v169,v170,v171\n"
CV "v170,v172,v173\n"
CV "v171,v174,v175\n"
"v_permlane16_swap_b32 v160,v162\n"
"v_permlane16_swap_b32 v161,v163\n"
"buffer_store_dwordx4 v[160:163],v242,s[44:47],s87 offen offset:64 sc1\n"
"s_add_u32 s87,s87,40960\n"
"v_permlane16_swap_b32 v168,v170\n"
"v_permlane16_swap_b32 v169,v171\n"
"buffer_store_dwordx4 v[168:171],v242,s[44:47],s87 offen offset:64 sc1\n"
"s_add_u32 s87,s87,40960\n"
"s_branch .Ljobs\n"
".Lpgrole1_h:\n"
"s_cbranch_execz .Lpg28h\n"
".Lrole1:\n"
MF "v[16:19],a[120:123],v[176:179],v[252:255]\n"
"s_mov_b32 s65,128\n"
TR "v[188:189],v1 offset:24576\n"
AR "v220,a44\n"
MF "v[16:19],a[160:163],v[180:183],v[16:19]\n"
"s_mov_b32 s9,0x3f80\n"
TR "v[190:191],v1 offset:26624\n"
AR "v221,a45\n"
MF "v[16:19],a[200:203],v[184:187],v[16:19]\n"
AR "v222,a46\n"
AR "v223,a47\n"
MF "v[24:27],a[204:207],v[184:187],v[252:255]\n"
"s_bfe_u32 s58,s22,0x30014\n"
TR "a[240:241],v1 offset:28672\n"
AR "v224,a64\n"
MF "v[24:27],a[164:167],v[180:183],v[24:27]\n"
"s_add_u32 s61,s58,1\n"
TR "a[242:243],v1 offset:30720\n"
AR "v225,a65\n"
MF "v[24:27],a[124:127],v[176:179],v[24:27]\n"
"s_and_b32 s61,s61,7\n"
AR "v226,a66\n"
AR "v227,a67\n"
MF "v[32:35],a[128:131],v[176:179],v[252:255]\n"
"s_mov_b32 s64,9\n"
TR "a[244:245],v1 offset:32768\n"
"ds_read_b128 a[64:67],v236 offset:49152\n"
MF "v[32:35],a[168:171],v[180:183],v[32:35]\n"
"s_xor_b32 s67,s78,8\n"
TR "a[246:247],v1 offset:34816\n"
"v_fma_f32 v220,v220,v209,v215\n"
MF "v[32:35],a[208:211],v[184:187],v[32:35]\n"
"s_mov_b32 s71,204800\n"
"v_fma_f32 v221,v221,v209,v215\n"
"v_fma_f32 v222,v222,v209,v215\n"
MF "v[40:43],a[212:215],v[184:187],v[252:255]\n"
"s_mov_b32 s66,204800\n"
"v_fma_f32 v223,v223,v209,v215\n"
"v_fma_f32 v224,v224,v209,v215\n"
MF "v[40:43],a[172:175],v[180:183],v[40:43]\n"
"s_and_b32 s73,s56,1\n"
"v_fma_f32 v225,v225,v209,v215\n"
"v_fma_f32 v226,v226,v209,v215\n"
MF "v[40:43],a[132:135],v[176:179],v[40:43]\n"
"s_mul_i32 s73,s73,5120\n"
"v_fma_f32 v227,v227,v209,v215\n"
CV "v220,v220,v221\n"
MF "v[48:51],a[136:139],v[176:179],v[252:255]\n"
"s_add_u32 s73,s73,s93\n"
CV "v221,v222,v223\n"
CV "v222,v224,v225\n"
MF "v[48:51],a[176:179],v[180:183],v[48:51]\n"
"s_xor_b32 s58,s58,4\n"
CV "v223,v226,v227\n"
AW "a44,v220\n"
MF "v[48:51],a[216:219],v[184:187],v[48:51]\n"
"s_waitcnt lgkmcnt(0)\n"
AW "a45,v221\n"
AW "a46,v222\n"
MF "v[52:55],a[216:219],a[244:247],v[252:255]\n"
"s_mul_i32 s70,s58,204800\n"
TR "v[176:177],v2 offset:24576\n"
AW "a47,v223\n"
MF "v[52:55],a[176:179],a[240:243],v[52:55]\n"
"s_add_u32 s70,s70,s73\n"
TR "v[178:179],v2 offset:26624\n"
AR "v228,a84\n"
MF "v[52:55],a[136:139],v[188:191],v[52:55]\n"
"s_sub_u32 s70,s70,30720\n"
AR "v229,a85\n"
AR "v230,a86\n"
MF "v[44:47],a[132:135],v[188:191],v[252:255]\n"
"s_mov_b32 s85,1\n"
TR "v[180:181],v2 offset:28672\n"
AR "v231,a87\n"
MF "v[44:47],a[172:175],a[240:243],v[44:47]\n"
"s_mov_b32 s83,0xf0000000\n"
TR "v[182:183],v2 offset:30720\n"
AR "v232,a104\n"
MF "v[44:47],a[212:215],a[244:247],v[44:47]\n"
"s_mov_b32 s82,0xf0000000\n"
AR "v233,a105\n"
AR "v234,a106\n"
MF "v[36:39],a[208:211],a[244:247],v[252:255]\n"
"s_add_u32 s58,s79,73728\n"
TR "v[184:185],v2 offset:32768\n"
AR "v235,a107\n"
MF "v[36:39],a[168:171],a[240:243],v[36:39]\n"
"s_mov_b32 s90,s91\n"
TR "v[186:187],v2 offset:34816\n"
"ds_read_b128 a[104:107],v237 offset:4096\n"
MF "v[36:39],a[128:131],v[188:191],v[36:39]\n"
"s_add_u32 s91,s91,20480\n"
"v_fma_f32 v228,v228,v209,v215\n"
"v_fma_f32 v229,v229,v209,v215\n"
MF "v[28:31],a[124:127],v[188:191],v[252:255]\n"
"s_bfe_u32 s74,s23,0x70000\n"
"v_fma_f32 v230,v230,v209,v215\n"
"v_fma_f32 v231,v231,v209,v215\n"
MF "v[28:31],a[164:167],a[240:243],v[28:31]\n"
"s_lshl_b32 s74,s74,1\n"
"v_fma_f32 v232,v232,v209,v215\n"
"v_fma_f32 v233,v233,v209,v215\n"
MF "v[28:31],a[204:207],a[244:247],v[28:31]\n"
"s_add_u32 s74,s74,s56\n"
"v_fma_f32 v234,v234,v209,v215\n"
"v_fma_f32 v235,v235,v209,v215\n"
MF "v[20:23],a[200:203],a[244:247],v[252:255]\n"
"s_sub_u32 s74,s74,2\n"
CV "v228,v228,v229\n"
CV "v229,v230,v231\n"
MF "v[20:23],a[160:163],a[240:243],v[20:23]\n"
"s_mul_i32 s74,s74,30720\n"
CV "v230,v232,v233\n"
CV "v231,v234,v235\n"
MF "v[20:23],a[120:123],v[188:191],v[20:23]\n"
"s_waitcnt lgkmcnt(1)\n"
AW "a84,v228\n"
AW "a85,v229\n"
MF "v[56:59],a[120:123],v[176:179],v[252:255]\n"
"s_bfe_u32 s68,s23,0x30003\n"
TR "v[188:189],v3 offset:24576\n"
"v_add_u32 v198,59392,v192\n"
MF "v[56:59],a[160:163],v[180:183],v[56:59]\n"
"s_add_u32 s68,s68,7\n"
TR "v[190:191],v3 offset:26624\n"
"v_add_u32 v199,68608,v192\n"
MF "v[56:59],a[200:203],v[184:187],v[56:59]\n"
".Lpg25h:\n"
"s_cbranch_execz .Lpg29h\n"
"s_lshl_b32 s68,s68,7\n"
"v_add_u32 v200,81920,v192\n"
"v_add_u32 v201,91136,v192\n"
MF "v[64:67],a[204:207],v[184:187],v[252:255]\n"
"s_and_b32 s68,s68,768\n"
TR "a[240:241],v3 offset:28672\n"
"v_add_u32 v202,100352,v192\n"
MF "v[64:67],a[164:167],v[180:183],v[64:67]\n"
TR "a[242:243],v3 offset:30720\n"
"v_add_u32 v203,109568,v192\n"
MF "v[64:67],a[124:127],v[176:179],v[64:67]\n"
AW "a86,v230\n"
AW "a87,v231\n"
MF "v[72:75],a[128:131],v[176:179],v[252:255]\n"
TR "a[244:245],v3 offset:32768\n"
AR "v220,a8\n"
MF "v[72:75],a[168:171],v[180:183],v[72:75]\n"
TR "a[246:247],v3 offset:34816\n"
AR "v221,a9\n"
MF "v[72:75],a[208:211],v[184:187],v[72:75]\n"
AR "v222,a10\n"
AR "v223,a11\n"
MF "v[80:83],a[212:215],v[184:187],v[252:255]\n"
AR "v224,a28\n"
AR "v225,a29\n"
MF "v[80:83],a[172:175],v[180:183],v[80:83]\n"
AR "v226,a30\n"
AR "v227,a31\n"
MF "v[80:83],a[132:135],v[176:179],v[80:83]\n"
"ds_read_b128 a[28:31],v236 offset:32768\n"
"v_fma_f32 v220,v220,v210,v216\n"
MF "v[88:91],a[136:139],v[176:179],v[252:255]\n"
"v_fma_f32 v221,v221,v210,v216\n"
"v_fma_f32 v222,v222,v210,v216\n"
MF "v[88:91],a[176:179],v[180:183],v[88:91]\n"
"v_fma_f32 v223,v223,v210,v216\n"
"v_fma_f32 v224,v224,v210,v216\n"
MF "v[88:91],a[216:219],v[184:187],v[88:91]\n"
"s_waitcnt vmcnt(0) lgkmcnt(0)\n"
"s_barrier\n"
MF "v[92:95],a[216:219],a[244:247],v[252:255]\n"
"s_add_u32 s89,s89,s65\n"
TR "v[176:177],v0\n"
"v_fma_f32 v225,v225,v210,v216\n"
MF "v[92:95],a[176:179],a[240:243],v[92:95]\n"
"s_add_u32 m0,s79,24576\n"
TR "v[178:179],v0 offset:2048\n"
"v_fma_f32 v226,v226,v210,v216\n"
MF "v[92:95],a[136:139],v[188:191],v[92:95]\n"
LD "v192,s[48:51],s89 offen lds\n"
MF "v[84:87],a[132:135],v[188:191],v[252:255]\n"
TR "v[180:181],v0 offset:4096\n"
"v_fma_f32 v227,v227,v210,v216\n"
MF "v[84:87],a[172:175],a[240:243],v[84:87]\n"
TR "v[182:183],v0 offset:6144\n"
CV "v220,v220,v221\n"
MF "v[84:87],a[212:215],a[244:247],v[84:87]\n"
LD "v193,s[48:51],s89 offen offset:1024 lds\n"
MF "v[76:79],a[208:211],a[244:247],v[252:255]\n"
TR "v[184:185],v0 offset:8192\n"
CV "v221,v222,v223\n"
MF "v[76:79],a[168:171],a[240:243],v[76:79]\n"
TR "v[186:187],v0 offset:10240\n"
CV "v222,v224,v225\n"
MF "v[76:79],a[128:131],v[188:191],v[76:79]\n"
TR "v[96:97],v0 offset:49152\n"
CV "v223,v226,v227\n"
MF "v[68:71],a[124:127],v[188:191],v[252:255]\n"
TR "v[98:99],v0 offset:51200\n"
AW "a8,v220\n"
MF "v[68:71],a[164:167],a[240:243],v[68:71]\n"
LD "v194,s[48:51],s89 offen offset:2048 lds\n"
MF "v[68:71],a[204:207],a[244:247],v[68:71]\n"
TR "v[100:101],v0 offset:53248\n"
AW "a9,v221\n"
MF "v[60:63],a[200:203],a[244:247],v[252:255]\n"
TR "v[102:103],v0 offset:55296\n"
AW "a10,v222\n"
MF "v[60:63],a[160:163],a[240:243],v[60:63]\n"
TR "v[104:105],v0 offset:57344\n"
AW "a11,v223\n"
MF "v[60:63],a[120:123],v[188:191],v[60:63]\n"
"s_waitcnt lgkmcnt(5)\n"
LD "v195,s[48:51],s89 offen offset:3072 lds\n"
MF "v[16:19],a[140:143],v[176:179],v[16:19]\n"
TR "v[188:189],v1\n"
AR "v228,a48\n"
MF "v[16:19],a[180:183],v[180:183],v[16:19]\n"
TR "v[190:191],v1 offset:2048\n"
AR "v229,a49\n"
MF "v[16:19],a[220:223],v[184:187],v[16:19]\n"
TR "v[106:107],v0 offset:59392\n"
AR "v230,a50\n"
MF "v[24:27],a[224:227],v[184:187],v[24:27]\n"
TR "a[240:241],v1 offset:4096\n"
AR "v231,a51\n"
MF "v[24:27],a[184:187],v[180:183],v[24:27]\n"
"s_add_u32 m0,s79,28672\n"
TR "a[242:243],v1 offset:6144\n"
AR "v232,a68\n"
MF "v[24:27],a[144:147],v[176:179],v[24:27]\n"
LD "v196,s[48:51],s89 offen lds\n"
MF "v[32:35],a[148:151],v[176:179],v[32:35]\n"
TR "a[244:245],v1 offset:8192\n"
AR "v233,a69\n"
MF "v[32:35],a[188:191],v[180:183],v[32:35]\n"
TR "a[246:247],v1 offset:10240\n"
AR "v234,a70\n"
MF "v[32:35],a[228:231],v[184:187],v[32:35]\n"
LD "v197,s[48:51],s89 offen offset:1024 lds\n"
MF "v[40:43],a[232:235],v[184:187],v[40:43]\n"
TR "v[108:109],v4\n"
AR "v235,a71\n"
MF "v[40:43],a[192:195],v[180:183],v[40:43]\n"
"s_nop 0\n"
TR "v[110:111],v4 offset:2048\n"
"ds_read_b128 a[68:71],v236 offset:53248\n"
MF "v[40:43],a[152:155],v[176:179],v[40:43]\n"
TR "v[112:113],v4 offset:4096\n"
"v_fma_f32 v228,v228,v210,v216\n"
MF "v[48:51],a[156:159],v[176:179],v[48:51]\n"
TR "v[114:115],v4 offset:6144\n"
"v_fma_f32 v229,v229,v210,v216\n"
MF "v[48:51],a[196:199],v[180:183],v[48:51]\n"
LD "v198,s[48:51],s89 offen offset:2048 lds\n"
MF "v[48:51],a[236:239],v[184:187],v[48:51]\n"
"s_waitcnt lgkmcnt(5)\n"
TR "v[116:117],v4 offset:8192\n"
"v_fma_f32 v230,v230,v210,v216\n"
MF "v[52:55],a[236:239],a[244:247],v[52:55]\n"
TR "v[176:177],v2\n"
"v_fma_f32 v231,v231,v210,v216\n"
MF "v[52:55],a[196:199],a[240:243],v[52:55]\n"
TR "v[178:179],v2 offset:2048\n"
"v_fma_f32 v232,v232,v210,v216\n"
MF "v[52:55],a[156:159],v[188:191],v[52:55]\n"
LD "v199,s[48:51],s89 offen offset:3072 lds\n"
MF "v[44:47],a[152:155],v[188:191],v[44:47]\n"
TR "v[180:181],v2 offset:4096\n"
"v_fma_f32 v233,v233,v210,v216\n"
MF "v[44:47],a[192:195],a[240:243],v[44:47]\n"
TR "v[182:183],v2 offset:6144\n"
"v_fma_f32 v234,v234,v210,v216\n"
MF "v[44:47],a[232:235],a[244:247],v[44:47]\n"
TR "v[118:119],v4 offset:10240\n"
"v_fma_f32 v235,v235,v210,v216\n"
MF "v[36:39],a[228:231],a[244:247],v[36:39]\n"
TR "v[184:185],v2 offset:8192\n"
CV "v228,v228,v229\n"
MF "v[36:39],a[188:191],a[240:243],v[36:39]\n"
"s_add_u32 m0,s79,32768\n"
TR "v[186:187],v2 offset:10240\n"
CV "v229,v230,v231\n"
MF "v[36:39],a[148:151],v[188:191],v[36:39]\n"
LD "v200,s[48:51],s89 offen lds\n"
MF "v[28:31],a[144:147],v[188:191],v[28:31]\n"
TR "v[120:121],v1 offset:49152\n"
CV "v230,v232,v233\n"
MF "v[28:31],a[184:187],a[240:243],v[28:31]\n"
TR "v[122:123],v1 offset:51200\n"
CV "v231,v234,v235\n"
MF "v[28:31],a[224:227],a[244:247],v[28:31]\n"
TR "v[124:125],v1 offset:53248\n"
AW "a48,v228\n"
MF "v[20:23],a[220:223],a[244:247],v[20:23]\n"
LD "v201,s[48:51],s89 offen offset:1024 lds\n"
MF "v[20:23],a[180:183],a[240:243],v[20:23]\n"
TR "v[126:127],v1 offset:55296\n"
AW "a49,v229\n"
MF "v[20:23],a[140:143],v[188:191],v[20:23]\n"
"s_waitcnt lgkmcnt(4)\n"
TR "v[128:129],v1 offset:57344\n"
AW "a50,v230\n"
MF "v[56:59],a[140:143],v[176:179],v[56:59]\n"
TR "v[188:189],v3\n"
AW "a51,v231\n"
MF "v[56:59],a[180:183],v[180:183],v[56:59]\n"
TR "v[190:191],v3 offset:2048\n"
AR "v220,a88\n"
MF "v[56:59],a[220:223],v[184:187],v[56:59]\n"
LD "v202,s[48:51],s89 offen offset:2048 lds\n"
MF "v[64:67],a[224:227],v[184:187],v[64:67]\n"
TR "a[240:241],v3 offset:4096\n"
AR "v221,a89\n"
MF "v[64:67],a[184:187],v[180:183],v[64:67]\n"
TR "a[242:243],v3 offset:6144\n"
AR "v222,a90\n"
MF "v[64:67],a[144:147],v[176:179],v[64:67]\n"
LD "v203,s[48:51],s89 offen offset:3072 lds\n"
MF "v[72:75],a[148:151],v[176:179],v[72:75]\n"
TR "a[244:245],v3 offset:8192\n"
AR "v223,a91\n"
MF "v[72:75],a[188:191],v[180:183],v[72:75]\n"
TR "a[246:247],v3 offset:10240\n"
AR "v224,a108\n"
MF "v[72:75],a[228:231],v[184:187],v[72:75]\n"
TR "v[130:131],v1 offset:59392\n"
AR "v225,a109\n"
MF "v[80:83],a[232:235],v[184:187],v[80:83]\n"
TR "v[132:133],v5\n"
AR "v226,a110\n"
MF "v[80:83],a[192:195],v[180:183],v[80:83]\n"
TR "v[134:135],v5 offset:2048\n"
AR "v227,a111\n"
MF "v[80:83],a[152:155],v[176:179],v[80:83]\n"
"s_nop 0\n"
TR "v[136:137],v5 offset:4096\n"
"ds_read_b128 a[108:111],v237 offset:8192\n"
MF "v[88:91],a[156:159],v[176:179],v[88:91]\n"
TR "v[138:139],v5 offset:6144\n"
"v_fma_f32 v220,v220,v210,v216\n"
MF "v[88:91],a[196:199],v[180:183],v[88:91]\n"
TR "v[140:141],v5 offset:8192\n"
"v_fma_f32 v221,v221,v210,v216\n"
MF "v[88:91],a[236:239],v[184:187],v[88:91]\n"
"s_waitcnt lgkmcnt(0)\n"
"s_barrier\n"
MF "v[92:95],a[236:239],a[244:247],v[92:95]\n"
TR "v[142:143],v5 offset:10240\n"
"v_fma_f32 v222,v222,v210,v216\n"
MF "v[92:95],a[196:199],a[240:243],v[92:95]\n"
TR "v[144:145],v2 offset:49152\n"
"v_fma_f32 v223,v223,v210,v216\n"
MF "v[92:95],a[156:159],v[188:191],v[92:95]\n"
TR "v[146:147],v2 offset:51200\n"
"v_fma_f32 v224,v224,v210,v216\n"
MF "v[84:87],a[152:155],v[188:191],v[84:87]\n"
TR "v[148:149],v2 offset:53248\n"
"v_fma_f32 v225,v225,v210,v216\n"
MF "v[84:87],a[192:195],a[240:243],v[84:87]\n"
TR "v[150:151],v2 offset:55296\n"
"v_fma_f32 v226,v226,v210,v216\n"
MF "v[84:87],a[232:235],a[244:247],v[84:87]\n"
TR "v[152:153],v2 offset:57344\n"
"v_fma_f32 v227,v227,v210,v216\n"
MF "v[76:79],a[228:231],a[244:247],v[76:79]\n"
TR "v[154:155],v2 offset:59392\n"
CV "v220,v220,v221\n"
MF "v[76:79],a[188:191],a[240:243],v[76:79]\n"
TR "v[156:157],v6\n"
CV "v221,v222,v223\n"
MF "v[76:79],a[148:151],v[188:191],v[76:79]\n"
TR "v[158:159],v6 offset:2048\n"
CV "v222,v224,v225\n"
MF "v[68:71],a[144:147],v[188:191],v[68:71]\n"
TR "v[160:161],v6 offset:4096\n"
CV "v223,v226,v227\n"
MF "v[68:71],a[184:187],a[240:243],v[68:71]\n"
TR "v[162:163],v6 offset:6144\n"
AW "a88,v220\n"
MF "v[68:71],a[224:227],a[244:247],v[68:71]\n"
TR "v[164:165],v6 offset:8192\n"
AW "a89,v221\n"
MF "v[60:63],a[220:223],a[244:247],v[60:63]\n"
TR "v[166:167],v6 offset:10240\n"
AW "a90,v222\n"
MF "v[60:63],a[180:183],a[240:243],v[60:63]\n"
TR "v[168:169],v3 offset:49152\n"
AW "a91,v223\n"
MF "v[60:63],a[140:143],v[188:191],v[60:63]\n"
TR "v[170:171],v3 offset:51200\n"
AR "v228,a12\n"
MF "v[16:19],a[0:3],v[96:99],v[16:19]\n"
TR "v[172:173],v3 offset:53248\n"
AR "v229,a13\n"
MF "v[16:19],a[40:43],v[100:103],v[16:19]\n"
TR "v[174:175],v3 offset:55296\n"
AR "v230,a14\n"
MF "v[16:19],a[80:83],v[104:107],v[16:19]\n"
TR "v[204:205],v3 offset:57344\n"
AR "v231,a15\n"
MF "v[16:19],a[20:23],v[108:111],v[16:19]\n"
TR "v[206:207],v3 offset:59392\n"
AR "v232,a32\n"
MF "v[16:19],a[60:63],v[112:115],v[16:19]\n"
TR "v[244:245],v7\n"
AR "v233,a33\n"
MF "v[16:19],a[100:103],v[116:119],v[16:19]\n"
"s_waitcnt lgkmcnt(15)\n"
TR "v[246:247],v7 offset:2048\n"
AR "v234,a34\n"
MF "v[20:23],a[100:103],v[140:143],v[20:23]\n"
TR "v[248:249],v7 offset:4096\n"
AR "v235,a35\n"
MF "v[20:23],a[60:63],v[136:139],v[20:23]\n"
"s_nop 0\n"
TR "v[250:251],v7 offset:6144\n"
"ds_read_b128 a[32:35],v236 offset:36864\n"
MF "v[20:23],a[20:23],v[132:135],v[20:23]\n"
TR "v[238:239],v7 offset:8192\n"
"v_fma_f32 v228,v228,v211,v217\n"
MF "v[20:23],a[80:83],v[128:131],v[20:23]\n"
TR "v[240:241],v7 offset:10240\n"
"v_fma_f32 v229,v229,v211,v217\n"
MF "v[20:23],a[40:43],v[124:127],v[20:23]\n"
"v_fma_f32 v230,v230,v211,v217\n"
"v_fma_f32 v231,v231,v211,v217\n"
MF "v[20:23],a[0:3],v[120:123],v[20:23]\n"
"s_waitcnt lgkmcnt(13)\n"
"v_fma_f32 v232,v232,v211,v217\n"
"v_fma_f32 v233,v233,v211,v217\n"
MF "v[56:59],a[0:3],v[144:147],v[56:59]\n"
"v_fma_f32 v234,v234,v211,v217\n"
"v_fma_f32 v235,v235,v211,v217\n"
MF "v[56:59],a[40:43],v[148:151],v[56:59]\n"
CV "v228,v228,v229\n"
CV "v229,v230,v231\n"
MF "v[56:59],a[80:83],v[152:155],v[56:59]\n"
CV "v230,v232,v233\n"
CV "v231,v234,v235\n"
MF "v[56:59],a[20:23],v[156:159],v[56:59]\n"
AW "a12,v228\n"
AW "a13,v229\n"
MF "v[56:59],a[60:63],v[160:163],v[56:59]\n"
AW "a14,v230\n"
AW "a15,v231\n"
MF "v[56:59],a[100:103],v[164:167],v[56:59]\n"
"s_waitcnt lgkmcnt(0)\n"
AR "v220,a52\n"
AR "v221,a53\n"
MF "v[60:63],a[100:103],v[238:241],v[60:63]\n"
AR "v222,a54\n"
AR "v223,a55\n"
MF "v[60:63],a[60:63],v[248:251],v[60:63]\n"
AR "v224,a72\n"
AR "v225,a73\n"
MF "v[60:63],a[20:23],v[244:247],v[60:63]\n"
AR "v226,a74\n"
AR "v227,a75\n"
MF "v[60:63],a[80:83],v[204:207],v[60:63]\n"
"ds_read_b128 a[72:75],v236 offset:57344\n"
"v_fma_f32 v220,v220,v211,v217\n"
MF "v[60:63],a[40:43],v[172:175],v[60:63]\n"
"v_fma_f32 v221,v221,v211,v217\n"
"v_fma_f32 v222,v222,v211,v217\n"
MF "v[60:63],a[0:3],v[168:171],v[60:63]\n"
"v_fma_f32 v223,v223,v211,v217\n"
"v_fma_f32 v224,v224,v211,v217\n"
MF "v[68:71],a[4:7],v[168:171],v[68:71]\n"
"v_fma_f32 v225,v225,v211,v217\n"
"v_fma_f32 v226,v226,v211,v217\n"
MF "v[68:71],a[44:47],v[172:175],v[68:71]\n"
"v_fma_f32 v227,v227,v211,v217\n"
CV "v220,v220,v221\n"
MF "v[68:71],a[84:87],v[204:207],v[68:71]\n"
CV "v221,v222,v223\n"
CV "v222,v224,v225\n"
MF "v[68:71],a[24:27],v[244:247],v[68:71]\n"
CV "v223,v226,v227\n"
AW "a52,v220\n"
MF "v[68:71],a[64:67],v[248:251],v[68:71]\n"
AW "a53,v221\n"
AW "a54,v222\n"
MF "v[68:71],a[104:107],v[238:241],v[68:71]\n"
AW "a55,v223\n"
AR "v228,a92\n"
MF "v[64:67],a[104:107],v[164:167],v[64:67]\n"
"s_waitcnt lgkmcnt(0)\n"
"s_barrier\n"
MF "v[64:67],a[64:67],v[160:163],v[64:67]\n"
AR "v229,a93\n"
AR "v230,a94\n"
MF "v[64:67],a[24:27],v[156:159],v[64:67]\n"
AR "v231,a95\n"
AR "v232,a112\n"
MF "v[64:67],a[84:87],v[152:155],v[64:67]\n"
AR "v233,a113\n"
AR "v234,a114\n"
MF "v[64:67],a[44:47],v[148:151],v[64:67]\n"
AR "v235,a115\n"
"v_fma_f32 v228,v228,v211,v217\n"
MF "v[64:67],a[4:7],v[144:147],v[64:67]\n"
"ds_read_b128 a[112:115],v237 offset:12288\n"
"v_fma_f32 v229,v229,v211,v217\n"
MF "v[28:31],a[4:7],v[120:123],v[28:31]\n"
"v_fma_f32 v230,v230,v211,v217\n"
"v_fma_f32 v231,v231,v211,v217\n"
MF "v[28:31],a[44:47],v[124:127],v[28:31]\n"
"v_fma_f32 v232,v232,v211,v217\n"
"v_fma_f32 v233,v233,v211,v217\n"
MF "v[28:31],a[84:87],v[128:131],v[28:31]\n"
"v_fma_f32 v234,v234,v211,v217\n"
"v_fma_f32 v235,v235,v211,v217\n"
MF "v[28:31],a[24:27],v[132:135],v[28:31]\n"
CV "v228,v228,v229\n"
CV "v229,v230,v231\n"
MF "v[28:31],a[64:67],v[136:139],v[28:31]\n"
CV "v230,v232,v233\n"
CV "v231,v234,v235\n"
MF "v[28:31],a[104:107],v[140:143],v[28:31]\n"
AW "a92,v228\n"
AW "a93,v229\n"
MF "v[24:27],a[104:107],v[116:119],v[24:27]\n"
AW "a94,v230\n"
AW "a95,v231\n"
MF "v[24:27],a[64:67],v[112:115],v[24:27]\n"
AR "v220,a16\n"
AR "v221,a17\n"
MF "v[24:27],a[24:27],v[108:111],v[24:27]\n"
AR "v222,a18\n"
AR "v223,a19\n"
MF "v[24:27],a[84:87],v[104:107],v[24:27]\n"
AR "v224,a36\n"
AR "v225,a37\n"
MF "v[24:27],a[44:47],v[100:103],v[24:27]\n"
AR "v226,a38\n"
AR "v227,a39\n"
MF "v[24:27],a[4:7],v[96:99],v[24:27]\n"
"ds_read_b128 a[36:39],v236 offset:40960\n"
"v_fma_f32 v220,v220,v212,v218\n"
MF "v[32:35],a[8:11],v[96:99],v[32:35]\n"
"v_fma_f32 v221,v221,v212,v218\n"
"v_fma_f32 v222,v222,v212,v218\n"
MF "v[32:35],a[48:51],v[100:103],v[32:35]\n"
"v_fma_f32 v223,v223,v212,v218\n"
"v_fma_f32 v224,v224,v212,v218\n"
MF "v[32:35],a[88:91],v[104:107],v[32:35]\n"
"v_fma_f32 v225,v225,v212,v218\n"
"v_fma_f32 v226,v226,v212,v218\n"
MF "v[32:35],a[28:31],v[108:111],v[32:35]\n"
"v_fma_f32 v227,v227,v212,v218\n"
CV "v220,v220,v221\n"
MF "v[32:35],a[68:71],v[112:115],v[32:35]\n"
CV "v221,v222,v223\n"
CV "v222,v224,v225\n"
MF "v[32:35],a[108:111],v[116:119],v[32:35]\n"
CV "v223,v226,v227\n"
AW "a16,v220\n"
MF "v[36:39],a[108:111],v[140:143],v[36:39]\n"
AW "a17,v221\n"
AW "a18,v222\n"
MF "v[36:39],a[68:71],v[136:139],v[36:39]\n"
AW "a19,v223\n"
AR "v228,a56\n"
MF "v[36:39],a[28:31],v[132:135],v[36:39]\n"
AR "v229,a57\n"
AR "v230,a58\n"
MF "v[36:39],a[88:91],v[128:131],v[36:39]\n"
AR "v231,a59\n"
AR "v232,a76\n"
MF "v[36:39],a[48:51],v[124:127],v[36:39]\n"
AR "v233,a77\n"
AR "v234,a78\n"
MF "v[36:39],a[8:11],v[120:123],v[36:39]\n"
AR "v235,a79\n"
"v_fma_f32 v228,v228,v212,v218\n"
MF "v[72:75],a[8:11],v[144:147],v[72:75]\n"
"ds_read_b128 a[76:79],v236 offset:61440\n"
"v_fma_f32 v229,v229,v212,v218\n"
MF "v[72:75],a[48:51],v[148:151],v[72:75]\n"
"v_fma_f32 v230,v230,v212,v218\n"
"v_fma_f32 v231,v231,v212,v218\n"
MF "v[72:75],a[88:91],v[152:155],v[72:75]\n"
"v_fma_f32 v232,v232,v212,v218\n"
"v_fma_f32 v233,v233,v212,v218\n"
MF "v[72:75],a[28:31],v[156:159],v[72:75]\n"
"v_fma_f32 v234,v234,v212,v218\n"
"v_fma_f32 v235,v235,v212,v218\n"
MF "v[72:75],a[68:71],v[160:163],v[72:75]\n"
CV "v228,v228,v229\n"
CV "v229,v230,v231\n"
MF "v[72:75],a[108:111],v[164:167],v[72:75]\n"
CV "v230,v232,v233\n"
CV "v231,v234,v235\n"
MF "v[76:79],a[108:111],v[238:241],v[76:79]\n"
AW "a56,v228\n"
AW "a57,v229\n"
MF "v[76:79],a[68:71],v[248:251],v[76:79]\n"
AW "a58,v230\n"
AW "a59,v231\n"
MF "v[76:79],a[28:31],v[244:247],v[76:79]\n"
AR "v220,a96\n"
AR "v221,a97\n"
MF "v[76:79],a[88:91],v[204:207],v[76:79]\n"
AR "v222,a98\n"
AR "v223,a99\n"
MF "v[76:79],a[48:51],v[172:175],v[76:79]\n"
AR "v224,a116\n"
AR "v225,a117\n"
MF "v[76:79],a[8:11],v[168:171],v[76:79]\n"
"s_waitcnt lgkmcnt(2)\n"
AR "v226,a118\n"
AR "v227,a119\n"
MF "v[84:87],a[12:15],v[168:171],v[84:87]\n"
"ds_read_b128 a[116:119],v237 offset:16384\n"
"v_fma_f32 v220,v220,v212,v218\n"
MF "v[84:87],a[52:55],v[172:175],v[84:87]\n"
"v_fma_f32 v221,v221,v212,v218\n"
"v_fma_f32 v222,v222,v212,v218\n"
MF "v[84:87],a[92:95],v[204:207],v[84:87]\n"
"v_fma_f32 v223,v223,v212,v218\n"
"v_fma_f32 v224,v224,v212,v218\n"
MF "v[84:87],a[32:35],v[244:247],v[84:87]\n"
"v_fma_f32 v225,v225,v212,v218\n"
"v_fma_f32 v226,v226,v212,v218\n"
MF "v[84:87],a[72:75],v[248:251],v[84:87]\n"
"v_fma_f32 v227,v227,v212,v218\n"
CV "v220,v220,v221\n"
MF "v[84:87],a[112:115],v[238:241],v[84:87]\n"
CV "v221,v222,v223\n"
CV "v222,v224,v225\n"
MF "v[80:83],a[112:115],v[164:167],v[80:83]\n"
CV "v223,v226,v227\n"
AW "a96,v220\n"
MF "v[80:83],a[72:75],v[160:163],v[80:83]\n"
AW "a97,v221\n"
AW "a98,v222\n"
MF "v[80:83],a[32:35],v[156:159],v[80:83]\n"
AW "a99,v223\n"
"v_mov_b32 v15,0\n"
MF "v[80:83],a[92:95],v[152:155],v[80:83]\n"
"v_lshrrev_b32 v242,5,v9\n"
"v_min_u32 v242,23,v242\n"
MF "v[80:83],a[52:55],v[148:151],v[80:83]\n"
"v_mul_u32_u24 v242,160,v242\n"
"v_and_or_b32 v242,v9,16,v242\n"
MF "v[80:83],a[12:15],v[144:147],v[80:83]\n"
"v_lshlrev_b32 v242,3,v242\n"
AR "v8,a252\n"
MF "v[44:47],a[12:15],v[120:123],v[44:47]\n"
AR "v11,a253\n"
"v_bfe_u32 v224,v9,5,3\n"
MF "v[44:47],a[52:55],v[124:127],v[44:47]\n"
".Lpg26h:\n"
"s_cbranch_execz .Lpgjobs_h\n"
"s_add_u32 s62,s74,s68\n"
"s_add_u32 s68,s68,256\n"
"s_and_b32 s68,s68,768\n"
MF "v[44:47],a[92:95],v[128:131],v[44:47]\n"
"buffer_load_dword v243,v242,s[48:51],s62 offen\n"
MF "v[44:47],a[32:35],v[132:135],v[44:47]\n"
"v_bfe_u32 v225,v9,8,2\n"
"v_lshlrev_b32 v225,1,v225\n"
MF "v[44:47],a[72:75],v[136:139],v[44:47]\n"
"v_mov_b32 v14,s76\n"
"s_add_u32 s76,s76,1\n"
MF "v[44:47],a[112:115],v[140:143],v[44:47]\n"
"buffer_store_dword v14,v15,s[40:43],s94 offen sc1\n"
MF "v[40:43],a[112:115],v[116:119],v[40:43]\n"
"v_and_b32 v226,16,v9\n"
"v_lshlrev_b32_e64 v226,v226,s9\n"
MF "v[40:43],a[72:75],v[112:115],v[40:43]\n"
"v_sub_u32 v224,v224,v225\n"
"v_cmp_eq_u32 vcc,0,v224\n"
MF "v[40:43],a[32:35],v[108:111],v[40:43]\n"
"s_nop 0\n"
"v_cndmask_b32 v220,0,v226,vcc\n"
"v_cmp_eq_u32 vcc,1,v224\n"
MF "v[40:43],a[92:95],v[104:107],v[40:43]\n"
"s_nop 0\n"
"v_cndmask_b32 v221,0,v226,vcc\n"
AW "a248,0\n"
MF "v[40:43],a[52:55],v[100:103],v[40:43]\n"
AW "a249,0\n"
AW "a250,v220\n"
MF "v[40:43],a[12:15],v[96:99],v[40:43]\n"
"s_waitcnt lgkmcnt(0)\n"
AW "a251,v221\n"
AW "a252,v220\n"
MF "v[48:51],a[16:19],v[96:99],v[48:51]\n"
AW "a253,v221\n"
AW "a254,0\n"
MF "v[48:51],a[56:59],v[100:103],v[48:51]\n"
AW "a255,0\n"
"v_bfe_u32 v12,v9,4,4\n"
MF "v[48:51],a[96:99],v[104:107],v[48:51]\n"
"v_lshlrev_b32 v12,2,v12\n"
"v_lshrrev_b32 v13,2,v9\n"
MF "v[48:51],a[36:39],v[108:111],v[48:51]\n"
"v_add_u32 v13,s58,v13\n"
"v_add_u32 v4,24576,v4\n"
MF "v[48:51],a[76:79],v[112:115],v[48:51]\n"
"v_add_u32 v5,24576,v5\n"
"v_add_u32 v6,24576,v6\n"
MF "v[48:51],a[116:119],v[116:119],v[48:51]\n"
"v_add_u32 v7,24576,v7\n"
MF "v[52:55],a[116:119],v[140:143],v[52:55]\n"
MF "v[52:55],a[76:79],v[136:139],v[52:55]\n"
MF "v[52:55],a[36:39],v[132:135],v[52:55]\n"
"s_waitcnt vmcnt(2) lgkmcnt(0)\n"
"s_barrier\n"
MF "v[52:55],a[96:99],v[128:131],v[52:55]\n"
TR "v[176:177],v0 offset:24576\n"
MF "v[52:55],a[56:59],v[124:127],v[52:55]\n"
TR "v[178:179],v0 offset:26624\n"
MF "v[52:55],a[16:19],v[120:123],v[52:55]\n"
MF "v[88:91],a[16:19],v[144:147],v[88:91]\n"
TR "v[180:181],v0 offset:28672\n"
MF "v[88:91],a[56:59],v[148:151],v[88:91]\n"
TR "v[182:183],v0 offset:30720\n"
MF "v[88:91],a[96:99],v[152:155],v[88:91]\n"
MF "v[88:91],a[36:39],v[156:159],v[88:91]\n"
TR "v[184:185],v0 offset:32768\n"
MF "v[88:91],a[76:79],v[160:163],v[88:91]\n"
TR "v[186:187],v0 offset:34816\n"
MF "v[88:91],a[116:119],v[164:167],v[88:91]\n"
MF "v[92:95],a[116:119],v[238:241],v[92:95]\n"
MF "v[92:95],a[76:79],v[248:251],v[92:95]\n"
MF "v[92:95],a[36:39],v[244:247],v[92:95]\n"
MF "v[92:95],a[96:99],v[204:207],v[92:95]\n"
MF "v[92:95],a[56:59],v[172:175],v[92:95]\n"
MF "v[92:95],a[16:19],v[168:171],v[92:95]\n"
"s_waitcnt lgkmcnt(0)\n"
".Lpl1:\n"
MF "v[96:99],a[0:3],v[176:179],v[252:255]\n"
TR "v[188:189],v1 offset:24576\n"
MF "v[96:99],a[40:43],v[180:183],v[96:99]\n"
"s_add_u32 s82,s82,s71\n"
TR "v[190:191],v1 offset:26624\n"
MF "v[96:99],a[80:83],v[184:187],v[96:99]\n"
LD "v[224:227],v9,s[40:43],s82 offen sc1\n"
MF "v[104:107],a[84:87],v[184:187],v[252:255]\n"
TR "a[240:241],v1 offset:28672\n"
MF "v[104:107],a[44:47],v[180:183],v[104:107]\n"
TR "a[242:243],v1 offset:30720\n"
MF "v[104:107],a[4:7],v[176:179],v[104:107]\n"
MF "v[112:115],a[8:11],v[176:179],v[252:255]\n"
TR "a[244:245],v1 offset:32768\n"
MF "v[112:115],a[48:51],v[180:183],v[112:115]\n"
TR "a[246:247],v1 offset:34816\n"
MF "v[112:115],a[88:91],v[184:187],v[112:115]\n"
MF "v[120:123],a[92:95],v[184:187],v[252:255]\n"
MF "v[120:123],a[52:55],v[180:183],v[120:123]\n"
"s_mul_i32 s95,s92,3277\n"
"s_lshr_b32 s95,s95,16\n"
"s_mul_i32 s59,s95,20\n"
MF "v[120:123],a[12:15],v[176:179],v[120:123]\n"
"s_sub_u32 s59,s92,s59\n"
"s_cmp_ge_u32 s59,10\n"
"s_cselect_b32 s96,4294557760,0\n"
MF "v[128:131],a[16:19],v[176:179],v[252:255]\n"
"s_add_u32 s82,s82,s66\n"
"s_add_u32 s96,s96,1024\n"
"s_mul_i32 s59,s59,40960\n"
MF "v[128:131],a[56:59],v[180:183],v[128:131]\n"
LD "v[228:231],v9,s[40:43],s82 offen sc1\n"
MF "v[128:131],a[96:99],v[184:187],v[128:131]\n"
"s_add_u32 s96,s96,s59\n"
"s_lshl_b32 s59,s95,7\n"
"s_waitcnt lgkmcnt(0)\n"
MF "v[132:135],a[96:99],a[244:247],v[252:255]\n"
"s_add_u32 s96,s96,s59\n"
"s_add_u32 s95,s95,3\n"
TR "v[176:177],v2 offset:24576\n"
MF "v[132:135],a[56:59],a[240:243],v[132:135]\n"
"s_cmp_ge_u32 s92,40\n"
"s_cselect_b32 s95,99,s95\n"
TR "v[178:179],v2 offset:26624\n"
MF "v[132:135],a[16:19],v[188:191],v[132:135]\n"
"s_cmp_ge_u32 s85,5\n"
"s_cselect_b32 s95,99,s95\n"
"s_cselect_b32 s72,s85,0\n"
MF "v[124:127],a[12:15],v[188:191],v[252:255]\n"
TR "v[180:181],v2 offset:28672\n"
MF "v[124:127],a[52:55],a[240:243],v[124:127]\n"
TR "v[182:183],v2 offset:30720\n"
MF "v[124:127],a[92:95],a[244:247],v[124:127]\n"
MF "v[116:119],a[88:91],a[244:247],v[252:255]\n"
TR "v[184:185],v2 offset:32768\n"
MF "v[116:119],a[48:51],a[240:243],v[116:119]\n"
"s_add_u32 s82,s82,s71\n"
TR "v[186:187],v2 offset:34816\n"
MF "v[116:119],a[8:11],v[188:191],v[116:119]\n"
LD "v[232:235],v9,s[40:43],s82 offen sc1\n"
MF "v[108:111],a[4:7],v[188:191],v[252:255]\n"
MF "v[108:111],a[44:47],a[240:243],v[108:111]\n"
MF "v[108:111],a[84:87],a[244:247],v[108:111]\n"
MF "v[100:103],a[80:83],a[244:247],v[252:255]\n"
MF "v[100:103],a[40:43],a[240:243],v[100:103]\n"
MF "v[100:103],a[0:3],v[188:191],v[100:103]\n"
"s_waitcnt lgkmcnt(0)\n"
MF "v[136:139],a[0:3],v[176:179],v[252:255]\n"
TR "v[188:189],v3 offset:24576\n"
MF "v[136:139],a[40:43],v[180:183],v[136:139]\n"
"s_add_u32 s82,s82,s71\n"
TR "v[190:191],v3 offset:26624\n"
MF "v[136:139],a[80:83],v[184:187],v[136:139]\n"
LD "v[236:239],v9,s[40:43],s82 offen sc1\n"
MF "v[144:147],a[84:87],v[184:187],v[252:255]\n"
TR "a[240:241],v3 offset:28672\n"
MF "v[144:147],a[44:47],v[180:183],v[144:147]\n"
TR "a[242:243],v3 offset:30720\n"
MF "v[144:147],a[4:7],v[176:179],v[144:147]\n"
MF "v[152:155],a[8:11],v[176:179],v[252:255]\n"
TR "a[244:245],v3 offset:32768\n"
MF "v[152:155],a[48:51],v[180:183],v[152:155]\n"
TR "a[246:247],v3 offset:34816\n"
MF "v[152:155],a[88:91],v[184:187],v[152:155]\n"
MF "v[160:163],a[92:95],v[184:187],v[252:255]\n"
MF "v[160:163],a[52:55],v[180:183],v[160:163]\n"
MF "v[160:163],a[12:15],v[176:179],v[160:163]\n"
MF "v[168:171],a[16:19],v[176:179],v[252:255]\n"
MF "v[168:171],a[56:59],v[180:183],v[168:171]\n"
MF "v[168:171],a[96:99],v[184:187],v[168:171]\n"
"s_waitcnt lgkmcnt(0)\n"
"s_barrier\n"
MF "v[172:175],a[96:99],a[244:247],v[252:255]\n"
TR "v[176:177],v0\n"
MF "v[172:175],a[56:59],a[240:243],v[172:175]\n"
TR "v[178:179],v0 offset:2048\n"
MF "v[172:175],a[16:19],v[188:191],v[172:175]\n"
"ds_read2st64_b32 v[192:193],v13 offset0:192 offset1:193\n"
MF "v[164:167],a[12:15],v[188:191],v[252:255]\n"
TR "v[180:181],v0 offset:4096\n"
MF "v[164:167],a[52:55],a[240:243],v[164:167]\n"
TR "v[182:183],v0 offset:6144\n"
MF "v[164:167],a[92:95],a[244:247],v[164:167]\n"
"ds_read2st64_b32 v[194:195],v13 offset0:194 offset1:195\n"
MF "v[156:159],a[88:91],a[244:247],v[252:255]\n"
TR "v[184:185],v0 offset:8192\n"
MF "v[156:159],a[48:51],a[240:243],v[156:159]\n"
TR "v[186:187],v0 offset:10240\n"
MF "v[156:159],a[8:11],v[188:191],v[156:159]\n"
"ds_read2st64_b32 v[196:197],v13 offset0:196 offset1:197\n"
MF "v[148:151],a[4:7],v[188:191],v[252:255]\n"
"ds_read2st64_b32 v[198:199],v13 offset0:198 offset1:199\n"
MF "v[148:151],a[44:47],a[240:243],v[148:151]\n"
"buffer_load_dword v10,v12,s[40:43],s86 offen sc1\n"
MF "v[148:151],a[84:87],a[244:247],v[148:151]\n"
MF "v[140:143],a[80:83],a[244:247],v[252:255]\n"
MF "v[140:143],a[40:43],a[240:243],v[140:143]\n"
MF "v[140:143],a[0:3],v[188:191],v[140:143]\n"
"s_waitcnt lgkmcnt(2)\n"
MF "v[96:99],a[120:123],v[176:179],v[96:99]\n"
"s_waitcnt lgkmcnt(0)\n"
TR "v[188:189],v1\n"
"v_add_f32 v16,v16,v192\n"
MF "v[96:99],a[160:163],v[180:183],v[96:99]\n"
TR "v[190:191],v1 offset:2048\n"
"v_add_f32 v17,v17,v193\n"
MF "v[96:99],a[200:203],v[184:187],v[96:99]\n"
"ds_read2st64_b32 v[200:201],v13 offset0:200 offset1:201\n"
"v_add_f32 v18,v18,v194\n"
"s_waitcnt vmcnt(9)\n"
"v_mov_b32 v14,s76\n"
"s_add_u32 s76,s76,1\n"
MF "v[104:107],a[204:207],v[184:187],v[104:107]\n"
TR "a[240:241],v1 offset:4096\n"
"v_add_f32 v19,v19,v195\n"
"buffer_store_dword v14,v15,s[40:43],s94 offen sc1\n"
MF "v[104:107],a[164:167],v[180:183],v[104:107]\n"
TR "a[242:243],v1 offset:6144\n"
"v_add_f32 v20,v20,v196\n"
MF "v[104:107],a[124:127],v[176:179],v[104:107]\n"
"ds_read2st64_b32 v[202:203],v13 offset0:202 offset1:203\n"
"v_add_f32 v21,v21,v197\n"
MF "v[112:115],a[128:131],v[176:179],v[112:115]\n"
TR "a[244:245],v1 offset:8192\n"
"v_add_f32 v22,v22,v198\n"
MF "v[112:115],a[168:171],v[180:183],v[112:115]\n"
TR "a[246:247],v1 offset:10240\n"
"v_add_f32 v23,v23,v199\n"
MF "v[112:115],a[208:211],v[184:187],v[112:115]\n"
"ds_read2st64_b32 v[204:205],v13 offset0:204 offset1:205\n"
CV "v16,v16,v17\n"
MF "v[120:123],a[212:215],v[184:187],v[120:123]\n"
"ds_read2st64_b32 v[206:207],v13 offset0:206 offset1:207\n"
CV "v17,v18,v19\n"
MF "v[120:123],a[172:175],v[180:183],v[120:123]\n"
"s_add_u32 s87,s90,0\n"
CV "v18,v20,v21\n"
CV "v19,v22,v23\n"
MF "v[120:123],a[132:135],v[176:179],v[120:123]\n"
MF "v[128:131],a[136:139],v[176:179],v[128:131]\n"
"buffer_store_dwordx4 v[16:19],v9,s[40:43],s87 offen sc1\n"
MF "v[128:131],a[176:179],v[180:183],v[128:131]\n"
MF "v[128:131],a[216:219],v[184:187],v[128:131]\n"
"s_waitcnt lgkmcnt(2)\n"
MF "v[132:135],a[216:219],a[244:247],v[132:135]\n"
"s_waitcnt lgkmcnt(0)\n"
TR "v[176:177],v2\n"
"v_add_f32 v24,v24,v200\n"
MF "v[132:135],a[176:179],a[240:243],v[132:135]\n"
TR "v[178:179],v2 offset:2048\n"
"v_add_f32 v25,v25,v201\n"
MF "v[132:135],a[136:139],v[188:191],v[132:135]\n"
"ds_read2st64_b32 v[192:193],v13 offset0:208 offset1:209\n"
"v_add_f32 v26,v26,v202\n"
MF "v[124:127],a[132:135],v[188:191],v[124:127]\n"
TR "v[180:181],v2 offset:4096\n"
"v_add_f32 v27,v27,v203\n"
MF "v[124:127],a[172:175],a[240:243],v[124:127]\n"
TR "v[182:183],v2 offset:6144\n"
"v_add_f32 v28,v28,v204\n"
MF "v[124:127],a[212:215],a[244:247],v[124:127]\n"
"ds_read2st64_b32 v[194:195],v13 offset0:210 offset1:211\n"
"v_add_f32 v29,v29,v205\n"
MF "v[116:119],a[208:211],a[244:247],v[116:119]\n"
TR "v[184:185],v2 offset:8192\n"
"v_add_f32 v30,v30,v206\n"
MF "v[116:119],a[168:171],a[240:243],v[116:119]\n"
TR "v[186:187],v2 offset:10240\n"
"v_add_f32 v31,v31,v207\n"
MF "v[116:119],a[128:131],v[188:191],v[116:119]\n"
"ds_read2st64_b32 v[196:197],v13 offset0:212 offset1:213\n"
CV "v24,v24,v25\n"
MF "v[108:111],a[124:127],v[188:191],v[108:111]\n"
"ds_read2st64_b32 v[198:199],v13 offset0:214 offset1:215\n"
CV "v25,v26,v27\n"
MF "v[108:111],a[164:167],a[240:243],v[108:111]\n"
"s_add_u32 s87,s90,1024\n"
CV "v26,v28,v29\n"
CV "v27,v30,v31\n"
MF "v[108:111],a[204:207],a[244:247],v[108:111]\n"
MF "v[100:103],a[200:203],a[244:247],v[100:103]\n"
"buffer_store_dwordx4 v[24:27],v9,s[40:43],s87 offen sc1\n"
MF "v[100:103],a[160:163],a[240:243],v[100:103]\n"
MF "v[100:103],a[120:123],v[188:191],v[100:103]\n"
"s_waitcnt lgkmcnt(2)\n"
MF "v[136:139],a[120:123],v[176:179],v[136:139]\n"
"s_waitcnt lgkmcnt(0)\n"
TR "v[188:189],v3\n"
"v_add_f32 v32,v32,v192\n"
MF "v[136:139],a[160:163],v[180:183],v[136:139]\n"
TR "v[190:191],v3 offset:2048\n"
"v_add_f32 v33,v33,v193\n"
MF "v[136:139],a[200:203],v[184:187],v[136:139]\n"
"ds_read2st64_b32 v[200:201],v13 offset0:216 offset1:217\n"
"v_add_f32 v34,v34,v194\n"
MF "v[144:147],a[204:207],v[184:187],v[144:147]\n"
TR "a[240:241],v3 offset:4096\n"
"v_add_f32 v35,v35,v195\n"
MF "v[144:147],a[164:167],v[180:183],v[144:147]\n"
TR "a[242:243],v3 offset:6144\n"
"v_add_f32 v36,v36,v196\n"
MF "v[144:147],a[124:127],v[176:179],v[144:147]\n"
"ds_read2st64_b32 v[202:203],v13 offset0:218 offset1:219\n"
"v_add_f32 v37,v37,v197\n"
MF "v[152:155],a[128:131],v[176:179],v[152:155]\n"
TR "a[244:245],v3 offset:8192\n"
"v_add_f32 v38,v38,v198\n"
MF "v[152:155],a[168:171],v[180:183],v[152:155]\n"
TR "a[246:247],v3 offset:10240\n"
"v_add_f32 v39,v39,v199\n"
MF "v[152:155],a[208:211],v[184:187],v[152:155]\n"
"ds_read2st64_b32 v[204:205],v13 offset0:220 offset1:221\n"
CV "v32,v32,v33\n"
MF "v[160:163],a[212:215],v[184:187],v[160:163]\n"
"ds_read2st64_b32 v[206:207],v13 offset0:222 offset1:223\n"
CV "v33,v34,v35\n"
MF "v[160:163],a[172:175],v[180:183],v[160:163]\n"
"s_add_u32 s87,s90,2048\n"
CV "v34,v36,v37\n"
CV "v35,v38,v39\n"
MF "v[160:163],a[132:135],v[176:179],v[160:163]\n"
MF "v[168:171],a[136:139],v[176:179],v[168:171]\n"
"buffer_store_dwordx4 v[32:35],v9,s[40:43],s87 offen sc1\n"
MF "v[168:171],a[176:179],v[180:183],v[168:171]\n"
MF "v[168:171],a[216:219],v[184:187],v[168:171]\n"
"s_waitcnt lgkmcnt(0)\n"
"s_barrier\n"
MF "v[172:175],a[216:219],a[244:247],v[172:175]\n"
TR "v[176:177],v0 offset:49152\n"
"v_add_f32 v40,v40,v200\n"
MF "v[172:175],a[176:179],a[240:243],v[172:175]\n"
TR "v[178:179],v0 offset:51200\n"
"v_add_f32 v41,v41,v201\n"
MF "v[172:175],a[136:139],v[188:191],v[172:175]\n"
"ds_read2st64_b32 v[192:193],v13 offset0:224 offset1:225\n"
"v_add_f32 v42,v42,v202\n"
MF "v[164:167],a[132:135],v[188:191],v[164:167]\n"
TR "v[180:181],v0 offset:53248\n"
"v_add_f32 v43,v43,v203\n"
MF "v[164:167],a[172:175],a[240:243],v[164:167]\n"
TR "v[182:183],v0 offset:55296\n"
"v_add_f32 v44,v44,v204\n"
MF "v[164:167],a[212:215],a[244:247],v[164:167]\n"
"ds_read2st64_b32 v[194:195],v13 offset0:226 offset1:227\n"
"v_add_f32 v45,v45,v205\n"
MF "v[156:159],a[208:211],a[244:247],v[156:159]\n"
TR "v[184:185],v0 offset:57344\n"
"v_add_f32 v46,v46,v206\n"
MF "v[156:159],a[168:171],a[240:243],v[156:159]\n"
TR "v[186:187],v0 offset:59392\n"
"v_add_f32 v47,v47,v207\n"
MF "v[156:159],a[128:131],v[188:191],v[156:159]\n"
"ds_read2st64_b32 v[196:197],v13 offset0:228 offset1:229\n"
CV "v40,v40,v41\n"
MF "v[148:151],a[124:127],v[188:191],v[148:151]\n"
"ds_read2st64_b32 v[198:199],v13 offset0:230 offset1:231\n"
CV "v41,v42,v43\n"
MF "v[148:151],a[164:167],a[240:243],v[148:151]\n"
"s_add_u32 s87,s90,3072\n"
CV "v42,v44,v45\n"
CV "v43,v46,v47\n"
MF "v[148:151],a[204:207],a[244:247],v[148:151]\n"
MF "v[140:143],a[200:203],a[244:247],v[140:143]\n"
"buffer_store_dwordx4 v[40:43],v9,s[40:43],s87 offen sc1\n"
MF "v[140:143],a[160:163],a[240:243],v[140:143]\n"
MF "v[140:143],a[120:123],v[188:191],v[140:143]\n"
"s_waitcnt lgkmcnt(2)\n"
MF "v[96:99],a[20:23],v[176:179],v[96:99]\n"
"s_waitcnt lgkmcnt(0)\n"
TR "v[188:189],v1 offset:49152\n"
"v_add_f32 v48,v48,v192\n"
MF "v[96:99],a[60:63],v[180:183],v[96:99]\n"
TR "v[190:191],v1 offset:51200\n"
"v_add_f32 v49,v49,v193\n"
MF "v[96:99],a[100:103],v[184:187],v[96:99]\n"
"ds_read2st64_b32 v[200:201],v13 offset0:0 offset1:1\n"
"v_add_f32 v50,v50,v194\n"
MF "v[104:107],a[104:107],v[184:187],v[104:107]\n"
TR "a[240:241],v1 offset:53248\n"
"v_add_f32 v51,v51,v195\n"
MF "v[104:107],a[64:67],v[180:183],v[104:107]\n"
TR "a[242:243],v1 offset:55296\n"
"v_add_f32 v52,v52,v196\n"
MF "v[104:107],a[24:27],v[176:179],v[104:107]\n"
"ds_read2st64_b32 v[202:203],v13 offset0:2 offset1:3\n"
"v_add_f32 v53,v53,v197\n"
MF "v[112:115],a[28:31],v[176:179],v[112:115]\n"
TR "a[244:245],v1 offset:57344\n"
"v_add_f32 v54,v54,v198\n"
MF "v[112:115],a[68:71],v[180:183],v[112:115]\n"
TR "a[246:247],v1 offset:59392\n"
"v_add_f32 v55,v55,v199\n"
MF "v[112:115],a[108:111],v[184:187],v[112:115]\n"
"ds_read2st64_b32 v[204:205],v13 offset0:4 offset1:5\n"
CV "v48,v48,v49\n"
MF "v[120:123],a[112:115],v[184:187],v[120:123]\n"
"ds_read2st64_b32 v[206:207],v13 offset0:6 offset1:7\n"
CV "v49,v50,v51\n"
MF "v[120:123],a[72:75],v[180:183],v[120:123]\n"
"s_add_u32 s87,s90,4096\n"
CV "v50,v52,v53\n"
CV "v51,v54,v55\n"
MF "v[120:123],a[32:35],v[176:179],v[120:123]\n"
MF "v[128:131],a[36:39],v[176:179],v[128:131]\n"
"buffer_store_dwordx4 v[48:51],v9,s[40:43],s87 offen sc1\n"
MF "v[128:131],a[76:79],v[180:183],v[128:131]\n"
MF "v[128:131],a[116:119],v[184:187],v[128:131]\n"
"s_waitcnt lgkmcnt(2)\n"
MF "v[132:135],a[116:119],a[244:247],v[132:135]\n"
"s_waitcnt lgkmcnt(0)\n"
TR "v[176:177],v2 offset:49152\n"
"v_add_f32 v56,v56,v200\n"
MF "v[132:135],a[76:79],a[240:243],v[132:135]\n"
TR "v[178:179],v2 offset:51200\n"
"v_add_f32 v57,v57,v201\n"
MF "v[132:135],a[36:39],v[188:191],v[132:135]\n"
"ds_read2st64_b32 v[192:193],v13 offset0:8 offset1:9\n"
"v_add_f32 v58,v58,v202\n"
MF "v[124:127],a[32:35],v[188:191],v[124:127]\n"
TR "v[180:181],v2 offset:53248\n"
"v_add_f32 v59,v59,v203\n"
MF "v[124:127],a[72:75],a[240:243],v[124:127]\n"
TR "v[182:183],v2 offset:55296\n"
"v_add_f32 v60,v60,v204\n"
MF "v[124:127],a[112:115],a[244:247],v[124:127]\n"
"ds_read2st64_b32 v[194:195],v13 offset0:10 offset1:11\n"
"v_add_f32 v61,v61,v205\n"
MF "v[116:119],a[108:111],a[244:247],v[116:119]\n"
TR "v[184:185],v2 offset:57344\n"
"v_add_f32 v62,v62,v206\n"
MF "v[116:119],a[68:71],a[240:243],v[116:119]\n"
TR "v[186:187],v2 offset:59392\n"
"v_add_f32 v63,v63,v207\n"
MF "v[116:119],a[28:31],v[188:191],v[116:119]\n"
"ds_read2st64_b32 v[196:197],v13 offset0:12 offset1:13\n"
CV "v56,v56,v57\n"
MF "v[108:111],a[24:27],v[188:191],v[108:111]\n"
"ds_read2st64_b32 v[198:199],v13 offset0:14 offset1:15\n"
CV "v57,v58,v59\n"
MF "v[108:111],a[64:67],a[240:243],v[108:111]\n"
"s_add_u32 s87,s90,10240\n"
CV "v58,v60,v61\n"
CV "v59,v62,v63\n"
MF "v[108:111],a[104:107],a[244:247],v[108:111]\n"
MF "v[100:103],a[100:103],a[244:247],v[100:103]\n"
"buffer_store_dwordx4 v[56:59],v9,s[40:43],s87 offen sc1\n"
MF "v[100:103],a[60:63],a[240:243],v[100:103]\n"
"s_waitcnt vmcnt(7)\n"
"s_cmp_eq_u32 s83,0xf0000000\n"
MF "v[100:103],a[20:23],v[188:191],v[100:103]\n"
"s_cbranch_scc1 .Ljs13\n"
"s_cmp_eq_u32 s83,0xf0000010\n"
"s_cbranch_scc1 .Ljc13\n"
MF "v[248:251],v[210:213],a[250:253],0\n"
"v_mov_b32 v240,v208\n"
"v_mov_b32 v241,v209\n"
MF "v[244:247],v[214:217],a[250:253],0\n"
MF "v[248:251],v[218:221],a[250:253],v[248:251]\n"
MF "v[244:247],v[222:225],a[250:253],v[244:247]\n"
MF "v[248:251],v[226:229],a[250:253],v[248:251]\n"
MF "v[244:247],v[230:233],a[250:253],v[244:247]\n"
MF "v[248:251],v[234:237],a[250:253],v[248:251]\n"
MF "v[244:247],v[238:241],a[250:253],v[244:247]\n"
"s_branch .Ljs13\n"
".Ljc13:\n"
MF "v[136:139],a[252:255],v[208:211],v[136:139]\n"
MF "v[144:147],a[252:255],v[212:215],v[144:147]\n"
MF "v[152:155],a[252:255],v[216:219],v[152:155]\n"
MF "v[160:163],a[252:255],v[220:223],v[160:163]\n"
MF "v[168:171],a[252:255],v[224:227],v[168:171]\n"
MF "v[140:143],a[248:251],v[208:211],v[140:143]\n"
MF "v[148:151],a[248:251],v[212:215],v[148:151]\n"
MF "v[156:159],a[248:251],v[216:219],v[156:159]\n"
MF "v[164:167],a[248:251],v[220:223],v[164:167]\n"
MF "v[172:175],a[248:251],v[224:227],v[172:175]\n"
".Ljs13:\n"
"s_waitcnt lgkmcnt(2)\n"
MF "v[136:139],a[20:23],v[176:179],v[136:139]\n"
"s_waitcnt lgkmcnt(0)\n"
TR "v[188:189],v3 offset:49152\n"
"v_add_f32 v64,v64,v192\n"
MF "v[136:139],a[60:63],v[180:183],v[136:139]\n"
TR "v[190:191],v3 offset:51200\n"
"v_add_f32 v65,v65,v193\n"
MF "v[136:139],a[100:103],v[184:187],v[136:139]\n"
"ds_read2st64_b32 v[200:201],v13 offset0:16 offset1:17\n"
"v_add_f32 v66,v66,v194\n"
MF "v[144:147],a[104:107],v[184:187],v[144:147]\n"
TR "a[240:241],v3 offset:53248\n"
"v_add_f32 v67,v67,v195\n"
MF "v[144:147],a[64:67],v[180:183],v[144:147]\n"
TR "a[242:243],v3 offset:55296\n"
"v_add_f32 v68,v68,v196\n"
MF "v[144:147],a[24:27],v[176:179],v[144:147]\n"
"ds_read2st64_b32 v[202:203],v13 offset0:18 offset1:19\n"
"v_add_f32 v69,v69,v197\n"
MF "v[152:155],a[28:31],v[176:179],v[152:155]\n"
TR "a[244:245],v3 offset:57344\n"
"v_add_f32 v70,v70,v198\n"
MF "v[152:155],a[68:71],v[180:183],v[152:155]\n"
TR "a[246:247],v3 offset:59392\n"
"v_add_f32 v71,v71,v199\n"
MF "v[152:155],a[108:111],v[184:187],v[152:155]\n"
"ds_read2st64_b32 v[204:205],v13 offset0:20 offset1:21\n"
CV "v64,v64,v65\n"
MF "v[160:163],a[112:115],v[184:187],v[160:163]\n"
"ds_read2st64_b32 v[206:207],v13 offset0:22 offset1:23\n"
CV "v65,v66,v67\n"
MF "v[160:163],a[72:75],v[180:183],v[160:163]\n"
"s_add_u32 s87,s90,11264\n"
CV "v66,v68,v69\n"
CV "v67,v70,v71\n"
MF "v[160:163],a[32:35],v[176:179],v[160:163]\n"
"s_lshl_b32 s82,s92,10\n"
"v_sub_u32 v10,v10,v11\n"
"v_cmp_gt_u32 vcc,s95,v10\n"
MF "v[168:171],a[36:39],v[176:179],v[168:171]\n"
"buffer_store_dwordx4 v[64:67],v9,s[40:43],s87 offen sc1\n"
MF "v[168:171],a[76:79],v[180:183],v[168:171]\n"
"s_add_u32 s82,s82,s93\n"
CV "v244,v244,v245\n"
CV "v245,v246,v247\n"
MF "v[168:171],a[116:119],v[184:187],v[168:171]\n"
"s_waitcnt lgkmcnt(0)\n"
"s_barrier\n"
MF "v[172:175],a[116:119],a[244:247],v[172:175]\n"
TR "v[176:177],v4\n"
"v_add_f32 v72,v72,v200\n"
MF "v[172:175],a[76:79],a[240:243],v[172:175]\n"
TR "v[178:179],v4 offset:2048\n"
"v_add_f32 v73,v73,v201\n"
MF "v[172:175],a[36:39],v[188:191],v[172:175]\n"
"ds_read2st64_b32 v[192:193],v13 offset0:24 offset1:25\n"
"v_add_f32 v74,v74,v202\n"
MF "v[164:167],a[32:35],v[188:191],v[164:167]\n"
TR "v[180:181],v4 offset:4096\n"
"v_add_f32 v75,v75,v203\n"
MF "v[164:167],a[72:75],a[240:243],v[164:167]\n"
TR "v[182:183],v4 offset:6144\n"
"v_add_f32 v76,v76,v204\n"
MF "v[164:167],a[112:115],a[244:247],v[164:167]\n"
"ds_read2st64_b32 v[194:195],v13 offset0:26 offset1:27\n"
"v_add_f32 v77,v77,v205\n"
MF "v[156:159],a[108:111],a[244:247],v[156:159]\n"
TR "v[184:185],v4 offset:8192\n"
"v_add_f32 v78,v78,v206\n"
MF "v[156:159],a[68:71],a[240:243],v[156:159]\n"
TR "v[186:187],v4 offset:10240\n"
"v_add_f32 v79,v79,v207\n"
MF "v[156:159],a[28:31],v[188:191],v[156:159]\n"
"ds_read2st64_b32 v[196:197],v13 offset0:28 offset1:29\n"
CV "v72,v72,v73\n"
MF "v[148:151],a[24:27],v[188:191],v[148:151]\n"
"ds_read2st64_b32 v[198:199],v13 offset0:30 offset1:31\n"
CV "v73,v74,v75\n"
MF "v[148:151],a[64:67],a[240:243],v[148:151]\n"
"s_add_u32 s87,s90,12288\n"
CV "v74,v76,v77\n"
CV "v75,v78,v79\n"
MF "v[148:151],a[104:107],a[244:247],v[148:151]\n"
"s_cmp_eq_u64 vcc,0\n"
"s_cselect_b32 s82,s82,0xf0000000\n"
"s_cselect_b32 s84,s96,0xf0000000\n"
"s_cselect_b32 s59,32,0\n"
MF "v[140:143],a[100:103],a[244:247],v[140:143]\n"
"buffer_store_dwordx4 v[72:75],v9,s[40:43],s87 offen sc1\n"
MF "v[140:143],a[60:63],a[240:243],v[140:143]\n"
"s_add_u32 s92,s92,s59\n"
".Lnf5:\n"
"v_readlane_b32 s58,v10,s67\n"
"s_cmp_lt_u32 s58,s72\n"
"s_cbranch_scc1 .Lsp5\n"
".Lnb5:\n"
MF "v[140:143],a[20:23],v[188:191],v[140:143]\n"
"s_waitcnt lgkmcnt(2)\n"
CV "v246,v248,v249\n"
CV "v247,v250,v251\n"
MF "v[96:99],a[140:143],v[176:179],v[96:99]\n"
"s_waitcnt lgkmcnt(0)\n"
TR "v[188:189],v5\n"
"v_add_f32 v80,v80,v192\n"
MF "v[96:99],a[180:183],v[180:183],v[96:99]\n"
TR "v[190:191],v5 offset:2048\n"
"v_add_f32 v81,v81,v193\n"
MF "v[96:99],a[220:223],v[184:187],v[96:99]\n"
"ds_read2st64_b32 v[200:201],v13 offset0:32 offset1:33\n"
"v_add_f32 v82,v82,v194\n"
MF "v[104:107],a[224:227],v[184:187],v[104:107]\n"
".Lpg27h:\n"
"s_cbranch_execz .Lisr4\n"
TR "a[240:241],v5 offset:4096\n"
"v_add_f32 v83,v83,v195\n"
MF "v[104:107],a[184:187],v[180:183],v[104:107]\n"
TR "a[242:243],v5 offset:6144\n"
"v_add_f32 v84,v84,v196\n"
MF "v[104:107],a[144:147],v[176:179],v[104:107]\n"
"ds_read2st64_b32 v[202:203],v13 offset0:34 offset1:35\n"
"v_add_f32 v85,v85,v197\n"
MF "v[112:115],a[148:151],v[176:179],v[112:115]\n"
TR "a[244:245],v5 offset:8192\n"
"v_add_f32 v86,v86,v198\n"
MF "v[112:115],a[188:191],v[180:183],v[112:115]\n"
TR "a[246:247],v5 offset:10240\n"
"v_add_f32 v87,v87,v199\n"
MF "v[112:115],a[228:231],v[184:187],v[112:115]\n"
"ds_read2st64_b32 v[204:205],v13 offset0:36 offset1:37\n"
CV "v80,v80,v81\n"
MF "v[120:123],a[232:235],v[184:187],v[120:123]\n"
"ds_read2st64_b32 v[206:207],v13 offset0:38 offset1:39\n"
CV "v81,v82,v83\n"
MF "v[120:123],a[192:195],v[180:183],v[120:123]\n"
"s_add_u32 s87,s90,13312\n"
CV "v82,v84,v85\n"
CV "v83,v86,v87\n"
MF "v[120:123],a[152:155],v[176:179],v[120:123]\n"
"s_cmp_ge_u32 s85,5\n"
"s_cselect_b32 s82,s70,s82\n"
"s_cselect_b32 s84,0xf0000010,s84\n"
MF "v[128:131],a[156:159],v[176:179],v[128:131]\n"
"buffer_store_dwordx4 v[80:83],v9,s[40:43],s87 offen sc1\n"
MF "v[128:131],a[196:199],v[180:183],v[128:131]\n"
"s_cmp_ge_u32 s85,5\n"
"s_cselect_b32 s71,1024,s71\n"
"s_cselect_b32 s66,0x80000000,s66\n"
MF "v[128:131],a[236:239],v[184:187],v[128:131]\n"
"s_add_u32 s70,s70,20480\n"
"s_waitcnt lgkmcnt(2)\n"
"v_permlane16_swap_b32 v244,v246\n"
MF "v[132:135],a[236:239],a[244:247],v[132:135]\n"
"s_waitcnt lgkmcnt(0)\n"
TR "v[176:177],v6\n"
"v_add_f32 v88,v88,v200\n"
MF "v[132:135],a[196:199],a[240:243],v[132:135]\n"
TR "v[178:179],v6 offset:2048\n"
"v_add_f32 v89,v89,v201\n"
MF "v[132:135],a[156:159],v[188:191],v[132:135]\n"
"v_add_f32 v90,v90,v202\n"
"v_add_f32 v91,v91,v203\n"
MF "v[124:127],a[152:155],v[188:191],v[124:127]\n"
TR "v[180:181],v6 offset:4096\n"
"v_add_f32 v92,v92,v204\n"
MF "v[124:127],a[192:195],a[240:243],v[124:127]\n"
TR "v[182:183],v6 offset:6144\n"
"v_add_f32 v93,v93,v205\n"
MF "v[124:127],a[232:235],a[244:247],v[124:127]\n"
"v_add_f32 v94,v94,v206\n"
"v_add_f32 v95,v95,v207\n"
MF "v[116:119],a[228:231],a[244:247],v[116:119]\n"
TR "v[184:185],v6 offset:8192\n"
CV "v88,v88,v89\n"
MF "v[116:119],a[188:191],a[240:243],v[116:119]\n"
TR "v[186:187],v6 offset:10240\n"
CV "v89,v90,v91\n"
MF "v[116:119],a[148:151],v[188:191],v[116:119]\n"
"s_add_u32 s87,s90,14336\n"
CV "v90,v92,v93\n"
CV "v91,v94,v95\n"
MF "v[108:111],a[144:147],v[188:191],v[108:111]\n"
"v_permlane16_swap_b32 v245,v247\n"
MF "v[108:111],a[184:187],a[240:243],v[108:111]\n"
"buffer_store_dwordx4 v[244:247],v8,s[44:47],s83 offen sc1\n"
MF "v[108:111],a[224:227],a[244:247],v[108:111]\n"
"s_mov_b32 s83,s84\n"
MF "v[100:103],a[220:223],a[244:247],v[100:103]\n"
"buffer_store_dwordx4 v[88:91],v9,s[40:43],s87 offen sc1\n"
MF "v[100:103],a[180:183],a[240:243],v[100:103]\n"
"s_mov_b32 s90,s91\n"
"s_add_u32 s91,s91,20480\n"
MF "v[100:103],a[140:143],v[188:191],v[100:103]\n"
"s_waitcnt lgkmcnt(0)\n"
LD "v[208:211],v9,s[40:43],s82 offen sc1\n"
MF "v[136:139],a[140:143],v[176:179],v[136:139]\n"
TR "v[188:189],v7\n"
MF "v[136:139],a[180:183],v[180:183],v[136:139]\n"
TR "v[190:191],v7 offset:2048\n"
MF "v[136:139],a[220:223],v[184:187],v[136:139]\n"
MF "v[144:147],a[224:227],v[184:187],v[144:147]\n"
TR "a[240:241],v7 offset:4096\n"
MF "v[144:147],a[184:187],v[180:183],v[144:147]\n"
"s_add_u32 s82,s82,s71\n"
TR "a[242:243],v7 offset:6144\n"
MF "v[144:147],a[144:147],v[176:179],v[144:147]\n"
LD "v[212:215],v9,s[40:43],s82 offen sc1\n"
MF "v[152:155],a[148:151],v[176:179],v[152:155]\n"
TR "a[244:245],v7 offset:8192\n"
MF "v[152:155],a[188:191],v[180:183],v[152:155]\n"
TR "a[246:247],v7 offset:10240\n"
MF "v[152:155],a[228:231],v[184:187],v[152:155]\n"
MF "v[160:163],a[232:235],v[184:187],v[160:163]\n"
MF "v[160:163],a[192:195],v[180:183],v[160:163]\n"
MF "v[160:163],a[152:155],v[176:179],v[160:163]\n"
"s_add_u32 s82,s82,s71\n"
MF "v[168:171],a[156:159],v[176:179],v[168:171]\n"
LD "v[216:219],v9,s[40:43],s82 offen sc1\n"
MF "v[168:171],a[196:199],v[180:183],v[168:171]\n"
MF "v[168:171],a[236:239],v[184:187],v[168:171]\n"
"s_waitcnt lgkmcnt(0)\n"
"s_barrier\n"
MF "v[172:175],a[236:239],a[244:247],v[172:175]\n"
TR "v[176:177],v0 offset:24576\n"
MF "v[172:175],a[196:199],a[240:243],v[172:175]\n"
"s_add_u32 s85,s85,1\n"
TR "v[178:179],v0 offset:26624\n"
MF "v[172:175],a[156:159],v[188:191],v[172:175]\n"
MF "v[164:167],a[152:155],v[188:191],v[164:167]\n"
TR "v[180:181],v0 offset:28672\n"
MF "v[164:167],a[192:195],a[240:243],v[164:167]\n"
"s_add_u32 s82,s82,s71\n"
TR "v[182:183],v0 offset:30720\n"
MF "v[164:167],a[232:235],a[244:247],v[164:167]\n"
LD "v[220:223],v9,s[40:43],s82 offen sc1\n"
MF "v[156:159],a[228:231],a[244:247],v[156:159]\n"
TR "v[184:185],v0 offset:32768\n"
MF "v[156:159],a[188:191],a[240:243],v[156:159]\n"
TR "v[186:187],v0 offset:34816\n"
MF "v[156:159],a[148:151],v[188:191],v[156:159]\n"
MF "v[148:151],a[144:147],v[188:191],v[148:151]\n"
MF "v[148:151],a[184:187],a[240:243],v[148:151]\n"
MF "v[148:151],a[224:227],a[244:247],v[148:151]\n"
MF "v[140:143],a[220:223],a[244:247],v[140:143]\n"
MF "v[140:143],a[180:183],a[240:243],v[140:143]\n"
MF "v[140:143],a[140:143],v[188:191],v[140:143]\n"
"s_waitcnt lgkmcnt(0)\n"
MF "v[16:19],a[0:3],v[176:179],v[252:255]\n"
TR "v[188:189],v1 offset:24576\n"
MF "v[16:19],a[40:43],v[180:183],v[16:19]\n"
"s_add_u32 s82,s82,s71\n"
TR "v[190:191],v1 offset:26624\n"
MF "v[16:19],a[80:83],v[184:187],v[16:19]\n"
LD "v[224:227],v9,s[40:43],s82 offen sc1\n"
MF "v[24:27],a[84:87],v[184:187],v[252:255]\n"
TR "a[240:241],v1 offset:28672\n"
MF "v[24:27],a[44:47],v[180:183],v[24:27]\n"
TR "a[242:243],v1 offset:30720\n"
MF "v[24:27],a[4:7],v[176:179],v[24:27]\n"
MF "v[32:35],a[8:11],v[176:179],v[252:255]\n"
TR "a[244:245],v1 offset:32768\n"
MF "v[32:35],a[48:51],v[180:183],v[32:35]\n"
TR "a[246:247],v1 offset:34816\n"
MF "v[32:35],a[88:91],v[184:187],v[32:35]\n"
MF "v[40:43],a[92:95],v[184:187],v[252:255]\n"
MF "v[40:43],a[52:55],v[180:183],v[40:43]\n"
"s_mul_i32 s95,s92,3277\n"
"s_lshr_b32 s95,s95,16\n"
"s_mul_i32 s59,s95,20\n"
MF "v[40:43],a[12:15],v[176:179],v[40:43]\n"
"s_sub_u32 s59,s92,s59\n"
"s_cmp_ge_u32 s59,10\n"
"s_cselect_b32 s96,4294557760,0\n"
MF "v[48:51],a[16:19],v[176:179],v[252:255]\n"
"s_add_u32 s82,s82,s66\n"
"s_add_u32 s96,s96,1024\n"
"s_mul_i32 s59,s59,40960\n"
MF "v[48:51],a[56:59],v[180:183],v[48:51]\n"
LD "v[228:231],v9,s[40:43],s82 offen sc1\n"
MF "v[48:51],a[96:99],v[184:187],v[48:51]\n"
"s_add_u32 s96,s96,s59\n"
"s_lshl_b32 s59,s95,7\n"
"s_waitcnt lgkmcnt(0)\n"
MF "v[52:55],a[96:99],a[244:247],v[252:255]\n"
"s_add_u32 s96,s96,s59\n"
"s_add_u32 s95,s95,3\n"
TR "v[176:177],v2 offset:24576\n"
MF "v[52:55],a[56:59],a[240:243],v[52:55]\n"
"s_cmp_ge_u32 s92,40\n"
"s_cselect_b32 s95,99,s95\n"
TR "v[178:179],v2 offset:26624\n"
MF "v[52:55],a[16:19],v[188:191],v[52:55]\n"
"s_cmp_ge_u32 s85,5\n"
"s_cselect_b32 s95,99,s95\n"
"s_cselect_b32 s72,s85,0\n"
MF "v[44:47],a[12:15],v[188:191],v[252:255]\n"
TR "v[180:181],v2 offset:28672\n"
MF "v[44:47],a[52:55],a[240:243],v[44:47]\n"
TR "v[182:183],v2 offset:30720\n"
MF "v[44:47],a[92:95],a[244:247],v[44:47]\n"
MF "v[36:39],a[88:91],a[244:247],v[252:255]\n"
TR "v[184:185],v2 offset:32768\n"
MF "v[36:39],a[48:51],a[240:243],v[36:39]\n"
"s_add_u32 s82,s82,s71\n"
TR "v[186:187],v2 offset:34816\n"
MF "v[36:39],a[8:11],v[188:191],v[36:39]\n"
LD "v[232:235],v9,s[40:43],s82 offen sc1\n"
MF "v[28:31],a[4:7],v[188:191],v[252:255]\n"
MF "v[28:31],a[44:47],a[240:243],v[28:31]\n"
MF "v[28:31],a[84:87],a[244:247],v[28:31]\n"
MF "v[20:23],a[80:83],a[244:247],v[252:255]\n"
MF "v[20:23],a[40:43],a[240:243],v[20:23]\n"
MF "v[20:23],a[0:3],v[188:191],v[20:23]\n"
"s_waitcnt lgkmcnt(0)\n"
MF "v[56:59],a[0:3],v[176:179],v[252:255]\n"
TR "v[188:189],v3 offset:24576\n"
MF "v[56:59],a[40:43],v[180:183],v[56:59]\n"
"s_add_u32 s82,s82,s71\n"
TR "v[190:191],v3 offset:26624\n"
MF "v[56:59],a[80:83],v[184:187],v[56:59]\n"
LD "v[236:239],v9,s[40:43],s82 offen sc1\n"
MF "v[64:67],a[84:87],v[184:187],v[252:255]\n"
TR "a[240:241],v3 offset:28672\n"
MF "v[64:67],a[44:47],v[180:183],v[64:67]\n"
TR "a[242:243],v3 offset:30720\n"
MF "v[64:67],a[4:7],v[176:179],v[64:67]\n"
MF "v[72:75],a[8:11],v[176:179],v[252:255]\n"
TR "a[244:245],v3 offset:32768\n"
MF "v[72:75],a[48:51],v[180:183],v[72:75]\n"
TR "a[246:247],v3 offset:34816\n"
MF "v[72:75],a[88:91],v[184:187],v[72:75]\n"
MF "v[80:83],a[92:95],v[184:187],v[252:255]\n"
MF "v[80:83],a[52:55],v[180:183],v[80:83]\n"
MF "v[80:83],a[12:15],v[176:179],v[80:83]\n"
MF "v[88:91],a[16:19],v[176:179],v[252:255]\n"
MF "v[88:91],a[56:59],v[180:183],v[88:91]\n"
MF "v[88:91],a[96:99],v[184:187],v[88:91]\n"
"s_waitcnt lgkmcnt(0)\n"
"s_barrier\n"
MF "v[92:95],a[96:99],a[244:247],v[252:255]\n"
TR "v[176:177],v0\n"
MF "v[92:95],a[56:59],a[240:243],v[92:95]\n"
TR "v[178:179],v0 offset:2048\n"
MF "v[92:95],a[16:19],v[188:191],v[92:95]\n"
"ds_read2st64_b32 v[192:193],v13 offset0:192 offset1:193\n"
MF "v[84:87],a[12:15],v[188:191],v[252:255]\n"
TR "v[180:181],v0 offset:4096\n"
MF "v[84:87],a[52:55],a[240:243],v[84:87]\n"
TR "v[182:183],v0 offset:6144\n"
MF "v[84:87],a[92:95],a[244:247],v[84:87]\n"
"ds_read2st64_b32 v[194:195],v13 offset0:194 offset1:195\n"
MF "v[76:79],a[88:91],a[244:247],v[252:255]\n"
TR "v[184:185],v0 offset:8192\n"
MF "v[76:79],a[48:51],a[240:243],v[76:79]\n"
TR "v[186:187],v0 offset:10240\n"
MF "v[76:79],a[8:11],v[188:191],v[76:79]\n"
"ds_read2st64_b32 v[196:197],v13 offset0:196 offset1:197\n"
MF "v[68:71],a[4:7],v[188:191],v[252:255]\n"
"ds_read2st64_b32 v[198:199],v13 offset0:198 offset1:199\n"
MF "v[68:71],a[44:47],a[240:243],v[68:71]\n"
"buffer_load_dword v10,v12,s[40:43],s86 offen sc1\n"
MF "v[68:71],a[84:87],a[244:247],v[68:71]\n"
MF "v[60:63],a[80:83],a[244:247],v[252:255]\n"
MF "v[60:63],a[40:43],a[240:243],v[60:63]\n"
MF "v[60:63],a[0:3],v[188:191],v[60:63]\n"
"s_waitcnt lgkmcnt(2)\n"
MF "v[16:19],a[120:123],v[176:179],v[16:19]\n"
"s_waitcnt lgkmcnt(0)\n"
TR "v[188:189],v1\n"
"v_add_f32 v96,v96,v192\n"
MF "v[16:19],a[160:163],v[180:183],v[16:19]\n"
TR "v[190:191],v1 offset:2048\n"
"v_add_f32 v97,v97,v193\n"
MF "v[16:19],a[200:203],v[184:187],v[16:19]\n"
"ds_read2st64_b32 v[200:201],v13 offset0:200 offset1:201\n"
"v_add_f32 v98,v98,v194\n"
"s_waitcnt vmcnt(9)\n"
"v_mov_b32 v14,s76\n"
"s_add_u32 s76,s76,1\n"
MF "v[24:27],a[204:207],v[184:187],v[24:27]\n"
TR "a[240:241],v1 offset:4096\n"
"v_add_f32 v99,v99,v195\n"
"buffer_store_dword v14,v15,s[40:43],s94 offen sc1\n"
MF "v[24:27],a[164:167],v[180:183],v[24:27]\n"
TR "a[242:243],v1 offset:6144\n"
"v_add_f32 v100,v100,v196\n"
MF "v[24:27],a[124:127],v[176:179],v[24:27]\n"
"ds_read2st64_b32 v[202:203],v13 offset0:202 offset1:203\n"
"v_add_f32 v101,v101,v197\n"
MF "v[32:35],a[128:131],v[176:179],v[32:35]\n"
TR "a[244:245],v1 offset:8192\n"
"v_add_f32 v102,v102,v198\n"
MF "v[32:35],a[168:171],v[180:183],v[32:35]\n"
TR "a[246:247],v1 offset:10240\n"
"v_add_f32 v103,v103,v199\n"
MF "v[32:35],a[208:211],v[184:187],v[32:35]\n"
"ds_read2st64_b32 v[204:205],v13 offset0:204 offset1:205\n"
CV "v96,v96,v97\n"
MF "v[40:43],a[212:215],v[184:187],v[40:43]\n"
"ds_read2st64_b32 v[206:207],v13 offset0:206 offset1:207\n"
CV "v97,v98,v99\n"
MF "v[40:43],a[172:175],v[180:183],v[40:43]\n"
"s_add_u32 s87,s90,0\n"
CV "v98,v100,v101\n"
CV "v99,v102,v103\n"
MF "v[40:43],a[132:135],v[176:179],v[40:43]\n"
MF "v[48:51],a[136:139],v[176:179],v[48:51]\n"
"buffer_store_dwordx4 v[96:99],v9,s[40:43],s87 offen sc1\n"
MF "v[48:51],a[176:179],v[180:183],v[48:51]\n"
MF "v[48:51],a[216:219],v[184:187],v[48:51]\n"
"s_waitcnt lgkmcnt(2)\n"
MF "v[52:55],a[216:219],a[244:247],v[52:55]\n"
"s_waitcnt lgkmcnt(0)\n"
TR "v[176:177],v2\n"
"v_add_f32 v104,v104,v200\n"
MF "v[52:55],a[176:179],a[240:243],v[52:55]\n"
TR "v[178:179],v2 offset:2048\n"
"v_add_f32 v105,v105,v201\n"
MF "v[52:55],a[136:139],v[188:191],v[52:55]\n"
"ds_read2st64_b32 v[192:193],v13 offset0:208 offset1:209\n"
"v_add_f32 v106,v106,v202\n"
MF "v[44:47],a[132:135],v[188:191],v[44:47]\n"
TR "v[180:181],v2 offset:4096\n"
"v_add_f32 v107,v107,v203\n"
MF "v[44:47],a[172:175],a[240:243],v[44:47]\n"
TR "v[182:183],v2 offset:6144\n"
"v_add_f32 v108,v108,v204\n"
MF "v[44:47],a[212:215],a[244:247],v[44:47]\n"
"ds_read2st64_b32 v[194:195],v13 offset0:210 offset1:211\n"
"v_add_f32 v109,v109,v205\n"
MF "v[36:39],a[208:211],a[244:247],v[36:39]\n"
TR "v[184:185],v2 offset:8192\n"
"v_add_f32 v110,v110,v206\n"
MF "v[36:39],a[168:171],a[240:243],v[36:39]\n"
TR "v[186:187],v2 offset:10240\n"
"v_add_f32 v111,v111,v207\n"
MF "v[36:39],a[128:131],v[188:191],v[36:39]\n"
"ds_read2st64_b32 v[196:197],v13 offset0:212 offset1:213\n"
CV "v104,v104,v105\n"
MF "v[28:31],a[124:127],v[188:191],v[28:31]\n"
"ds_read2st64_b32 v[198:199],v13 offset0:214 offset1:215\n"
CV "v105,v106,v107\n"
MF "v[28:31],a[164:167],a[240:243],v[28:31]\n"
"s_add_u32 s87,s90,1024\n"
CV "v106,v108,v109\n"
CV "v107,v110,v111\n"
MF "v[28:31],a[204:207],a[244:247],v[28:31]\n"
MF "v[20:23],a[200:203],a[244:247],v[20:23]\n"
"buffer_store_dwordx4 v[104:107],v9,s[40:43],s87 offen sc1\n"
MF "v[20:23],a[160:163],a[240:243],v[20:23]\n"
MF "v[20:23],a[120:123],v[188:191],v[20:23]\n"
"s_waitcnt lgkmcnt(2)\n"
MF "v[56:59],a[120:123],v[176:179],v[56:59]\n"
"s_waitcnt lgkmcnt(0)\n"
TR "v[188:189],v3\n"
"v_add_f32 v112,v112,v192\n"
MF "v[56:59],a[160:163],v[180:183],v[56:59]\n"
TR "v[190:191],v3 offset:2048\n"
"v_add_f32 v113,v113,v193\n"
MF "v[56:59],a[200:203],v[184:187],v[56:59]\n"
"ds_read2st64_b32 v[200:201],v13 offset0:216 offset1:217\n"
"v_add_f32 v114,v114,v194\n"
MF "v[64:67],a[204:207],v[184:187],v[64:67]\n"
TR "a[240:241],v3 offset:4096\n"
"v_add_f32 v115,v115,v195\n"
MF "v[64:67],a[164:167],v[180:183],v[64:67]\n"
TR "a[242:243],v3 offset:6144\n"
"v_add_f32 v116,v116,v196\n"
MF "v[64:67],a[124:127],v[176:179],v[64:67]\n"
"ds_read2st64_b32 v[202:203],v13 offset0:218 offset1:219\n"
"v_add_f32 v117,v117,v197\n"
MF "v[72:75],a[128:131],v[176:179],v[72:75]\n"
TR "a[244:245],v3 offset:8192\n"
"v_add_f32 v118,v118,v198\n"
MF "v[72:75],a[168:171],v[180:183],v[72:75]\n"
TR "a[246:247],v3 offset:10240\n"
"v_add_f32 v119,v119,v199\n"
MF "v[72:75],a[208:211],v[184:187],v[72:75]\n"
"ds_read2st64_b32 v[204:205],v13 offset0:220 offset1:221\n"
CV "v112,v112,v113\n"
MF "v[80:83],a[212:215],v[184:187],v[80:83]\n"
"ds_read2st64_b32 v[206:207],v13 offset0:222 offset1:223\n"
CV "v113,v114,v115\n"
MF "v[80:83],a[172:175],v[180:183],v[80:83]\n"
"s_add_u32 s87,s90,2048\n"
CV "v114,v116,v117\n"
CV "v115,v118,v119\n"
MF "v[80:83],a[132:135],v[176:179],v[80:83]\n"
MF "v[88:91],a[136:139],v[176:179],v[88:91]\n"
"buffer_store_dwordx4 v[112:115],v9,s[40:43],s87 offen sc1\n"
MF "v[88:91],a[176:179],v[180:183],v[88:91]\n"
MF "v[88:91],a[216:219],v[184:187],v[88:91]\n"
"s_waitcnt lgkmcnt(0)\n"
"s_barrier\n"
MF "v[92:95],a[216:219],a[244:247],v[92:95]\n"
TR "v[176:177],v0 offset:49152\n"
"v_add_f32 v120,v120,v200\n"
MF "v[92:95],a[176:179],a[240:243],v[92:95]\n"
TR "v[178:179],v0 offset:51200\n"
"v_add_f32 v121,v121,v201\n"
MF "v[92:95],a[136:139],v[188:191],v[92:95]\n"
"ds_read2st64_b32 v[192:193],v13 offset0:224 offset1:225\n"
"v_add_f32 v122,v122,v202\n"
MF "v[84:87],a[132:135],v[188:191],v[84:87]\n"
TR "v[180:181],v0 offset:53248\n"
"v_add_f32 v123,v123,v203\n"
MF "v[84:87],a[172:175],a[240:243],v[84:87]\n"
TR "v[182:183],v0 offset:55296\n"
"v_add_f32 v124,v124,v204\n"
MF "v[84:87],a[212:215],a[244:247],v[84:87]\n"
"ds_read2st64_b32 v[194:195],v13 offset0:226 offset1:227\n"
"v_add_f32 v125,v125,v205\n"
MF "v[76:79],a[208:211],a[244:247],v[76:79]\n"
TR "v[184:185],v0 offset:57344\n"
"v_add_f32 v126,v126,v206\n"
MF "v[76:79],a[168:171],a[240:243],v[76:79]\n"
TR "v[186:187],v0 offset:59392\n"
"v_add_f32 v127,v127,v207\n"
MF "v[76:79],a[128:131],v[188:191],v[76:79]\n"
"ds_read2st64_b32 v[196:197],v13 offset0:228 offset1:229\n"
CV "v120,v120,v121\n"
MF "v[68:71],a[124:127],v[188:191],v[68:71]\n"
"ds_read2st64_b32 v[198:199],v13 offset0:230 offset1:231\n"
CV "v121,v122,v123\n"
MF "v[68:71],a[164:167],a[240:243],v[68:71]\n"
"s_add_u32 s87,s90,3072\n"
CV "v122,v124,v125\n"
CV "v123,v126,v127\n"
MF "v[68:71],a[204:207],a[244:247],v[68:71]\n"
MF "v[60:63],a[200:203],a[244:247],v[60:63]\n"
"buffer_store_dwordx4 v[120:123],v9,s[40:43],s87 offen sc1\n"
MF "v[60:63],a[160:163],a[240:243],v[60:63]\n"
MF "v[60:63],a[120:123],v[188:191],v[60:63]\n"
"s_waitcnt lgkmcnt(2)\n"
MF "v[16:19],a[20:23],v[176:179],v[16:19]\n"
"s_waitcnt lgkmcnt(0)\n"
TR "v[188:189],v1 offset:49152\n"
"v_add_f32 v128,v128,v192\n"
MF "v[16:19],a[60:63],v[180:183],v[16:19]\n"
TR "v[190:191],v1 offset:51200\n"
"v_add_f32 v129,v129,v193\n"
MF "v[16:19],a[100:103],v[184:187],v[16:19]\n"
"ds_read2st64_b32 v[200:201],v13 offset0:0 offset1:1\n"
"v_add_f32 v130,v130,v194\n"
MF "v[24:27],a[104:107],v[184:187],v[24:27]\n"
TR "a[240:241],v1 offset:53248\n"
"v_add_f32 v131,v131,v195\n"
MF "v[24:27],a[64:67],v[180:183],v[24:27]\n"
TR "a[242:243],v1 offset:55296\n"
"v_add_f32 v132,v132,v196\n"
MF "v[24:27],a[24:27],v[176:179],v[24:27]\n"
"ds_read2st64_b32 v[202:203],v13 offset0:2 offset1:3\n"
"v_add_f32 v133,v133,v197\n"
MF "v[32:35],a[28:31],v[176:179],v[32:35]\n"
TR "a[244:245],v1 offset:57344\n"
"v_add_f32 v134,v134,v198\n"
MF "v[32:35],a[68:71],v[180:183],v[32:35]\n"
TR "a[246:247],v1 offset:59392\n"
"v_add_f32 v135,v135,v199\n"
MF "v[32:35],a[108:111],v[184:187],v[32:35]\n"
"ds_read2st64_b32 v[204:205],v13 offset0:4 offset1:5\n"
CV "v128,v128,v129\n"
MF "v[40:43],a[112:115],v[184:187],v[40:43]\n"
"ds_read2st64_b32 v[206:207],v13 offset0:6 offset1:7\n"
CV "v129,v130,v131\n"
MF "v[40:43],a[72:75],v[180:183],v[40:43]\n"
"s_add_u32 s87,s90,4096\n"
CV "v130,v132,v133\n"
CV "v131,v134,v135\n"
MF "v[40:43],a[32:35],v[176:179],v[40:43]\n"
MF "v[48:51],a[36:39],v[176:179],v[48:51]\n"
"buffer_store_dwordx4 v[128:131],v9,s[40:43],s87 offen sc1\n"
MF "v[48:51],a[76:79],v[180:183],v[48:51]\n"
MF "v[48:51],a[116:119],v[184:187],v[48:51]\n"
"s_waitcnt lgkmcnt(2)\n"
MF "v[52:55],a[116:119],a[244:247],v[52:55]\n"
"s_waitcnt lgkmcnt(0)\n"
TR "v[176:177],v2 offset:49152\n"
"v_add_f32 v136,v136,v200\n"
MF "v[52:55],a[76:79],a[240:243],v[52:55]\n"
TR "v[178:179],v2 offset:51200\n"
"v_add_f32 v137,v137,v201\n"
MF "v[52:55],a[36:39],v[188:191],v[52:55]\n"
"ds_read2st64_b32 v[192:193],v13 offset0:8 offset1:9\n"
"v_add_f32 v138,v138,v202\n"
MF "v[44:47],a[32:35],v[188:191],v[44:47]\n"
TR "v[180:181],v2 offset:53248\n"
"v_add_f32 v139,v139,v203\n"
MF "v[44:47],a[72:75],a[240:243],v[44:47]\n"
TR "v[182:183],v2 offset:55296\n"
"v_add_f32 v140,v140,v204\n"
MF "v[44:47],a[112:115],a[244:247],v[44:47]\n"
"ds_read2st64_b32 v[194:195],v13 offset0:10 offset1:11\n"
"v_add_f32 v141,v141,v205\n"
MF "v[36:39],a[108:111],a[244:247],v[36:39]\n"
TR "v[184:185],v2 offset:57344\n"
"v_add_f32 v142,v142,v206\n"
MF "v[36:39],a[68:71],a[240:243],v[36:39]\n"
TR "v[186:187],v2 offset:59392\n"
"v_add_f32 v143,v143,v207\n"
MF "v[36:39],a[28:31],v[188:191],v[36:39]\n"
"ds_read2st64_b32 v[196:197],v13 offset0:12 offset1:13\n"
CV "v136,v136,v137\n"
MF "v[28:31],a[24:27],v[188:191],v[28:31]\n"
"ds_read2st64_b32 v[198:199],v13 offset0:14 offset1:15\n"
CV "v137,v138,v139\n"
MF "v[28:31],a[64:67],a[240:243],v[28:31]\n"
"s_add_u32 s87,s90,10240\n"
CV "v138,v140,v141\n"
CV "v139,v142,v143\n"
MF "v[28:31],a[104:107],a[244:247],v[28:31]\n"
MF "v[20:23],a[100:103],a[244:247],v[20:23]\n"
"buffer_store_dwordx4 v[136:139],v9,s[40:43],s87 offen sc1\n"
MF "v[20:23],a[60:63],a[240:243],v[20:23]\n"
"s_waitcnt vmcnt(7)\n"
"s_cmp_eq_u32 s83,0xf0000000\n"
MF "v[20:23],a[20:23],v[188:191],v[20:23]\n"
"s_cbranch_scc1 .Ljs15\n"
"s_cmp_eq_u32 s83,0xf0000010\n"
"s_cbranch_scc1 .Ljc15\n"
MF "v[248:251],v[210:213],a[250:253],0\n"
"v_mov_b32 v240,v208\n"
"v_mov_b32 v241,v209\n"
MF "v[244:247],v[214:217],a[250:253],0\n"
MF "v[248:251],v[218:221],a[250:253],v[248:251]\n"
MF "v[244:247],v[222:225],a[250:253],v[244:247]\n"
MF "v[248:251],v[226:229],a[250:253],v[248:251]\n"
MF "v[244:247],v[230:233],a[250:253],v[244:247]\n"
MF "v[248:251],v[234:237],a[250:253],v[248:251]\n"
MF "v[244:247],v[238:241],a[250:253],v[244:247]\n"
"s_branch .Ljs15\n"
".Ljc15:\n"
MF "v[56:59],a[252:255],v[208:211],v[56:59]\n"
MF "v[64:67],a[252:255],v[212:215],v[64:67]\n"
MF "v[72:75],a[252:255],v[216:219],v[72:75]\n"
MF "v[80:83],a[252:255],v[220:223],v[80:83]\n"
MF "v[88:91],a[252:255],v[224:227],v[88:91]\n"
MF "v[60:63],a[248:251],v[208:211],v[60:63]\n"
MF "v[68:71],a[248:251],v[212:215],v[68:71]\n"
MF "v[76:79],a[248:251],v[216:219],v[76:79]\n"
MF "v[84:87],a[248:251],v[220:223],v[84:87]\n"
MF "v[92:95],a[248:251],v[224:227],v[92:95]\n"
".Ljs15:\n"
"s_waitcnt lgkmcnt(2)\n"
MF "v[56:59],a[20:23],v[176:179],v[56:59]\n"
"s_waitcnt lgkmcnt(0)\n"
TR "v[188:189],v3 offset:49152\n"
"v_add_f32 v144,v144,v192\n"
MF "v[56:59],a[60:63],v[180:183],v[56:59]\n"
TR "v[190:191],v3 offset:51200\n"
"v_add_f32 v145,v145,v193\n"
MF "v[56:59],a[100:103],v[184:187],v[56:59]\n"
"ds_read2st64_b32 v[200:201],v13 offset0:16 offset1:17\n"
"v_add_f32 v146,v146,v194\n"
MF "v[64:67],a[104:107],v[184:187],v[64:67]\n"
TR "a[240:241],v3 offset:53248\n"
"v_add_f32 v147,v147,v195\n"
MF "v[64:67],a[64:67],v[180:183],v[64:67]\n"
TR "a[242:243],v3 offset:55296\n"
"v_add_f32 v148,v148,v196\n"
MF "v[64:67],a[24:27],v[176:179],v[64:67]\n"
"ds_read2st64_b32 v[202:203],v13 offset0:18 offset1:19\n"
"v_add_f32 v149,v149,v197\n"
MF "v[72:75],a[28:31],v[176:179],v[72:75]\n"
TR "a[244:245],v3 offset:57344\n"
"v_add_f32 v150,v150,v198\n"
MF "v[72:75],a[68:71],v[180:183],v[72:75]\n"
TR "a[246:247],v3 offset:59392\n"
"v_add_f32 v151,v151,v199\n"
MF "v[72:75],a[108:111],v[184:187],v[72:75]\n"
"ds_read2st64_b32 v[204:205],v13 offset0:20 offset1:21\n"
CV "v144,v144,v145\n"
MF "v[80:83],a[112:115],v[184:187],v[80:83]\n"
"ds_read2st64_b32 v[206:207],v13 offset0:22 offset1:23\n"
CV "v145,v146,v147\n"
MF "v[80:83],a[72:75],v[180:183],v[80:83]\n"
"s_add_u32 s87,s90,11264\n"
CV "v146,v148,v149\n"
CV "v147,v150,v151\n"
MF "v[80:83],a[32:35],v[176:179],v[80:83]\n"
"s_lshl_b32 s82,s92,10\n"
"v_sub_u32 v10,v10,v11\n"
"v_cmp_gt_u32 vcc,s95,v10\n"
MF "v[88:91],a[36:39],v[176:179],v[88:91]\n"
"buffer_store_dwordx4 v[144:147],v9,s[40:43],s87 offen sc1\n"
MF "v[88:91],a[76:79],v[180:183],v[88:91]\n"
"s_add_u32 s82,s82,s93\n"
CV "v244,v244,v245\n"
CV "v245,v246,v247\n"
MF "v[88:91],a[116:119],v[184:187],v[88:91]\n"
"s_waitcnt lgkmcnt(0)\n"
"s_barrier\n"
MF "v[92:95],a[116:119],a[244:247],v[92:95]\n"
TR "v[176:177],v4\n"
"v_add_f32 v152,v152,v200\n"
MF "v[92:95],a[76:79],a[240:243],v[92:95]\n"
".Lpg28h:\n"
"s_cbranch_execz .Lisr4\n"
TR "v[178:179],v4 offset:2048\n"
"v_add_f32 v153,v153,v201\n"
MF "v[92:95],a[36:39],v[188:191],v[92:95]\n"
"ds_read2st64_b32 v[192:193],v13 offset0:24 offset1:25\n"
"v_add_f32 v154,v154,v202\n"
MF "v[84:87],a[32:35],v[188:191],v[84:87]\n"
TR "v[180:181],v4 offset:4096\n"
"v_add_f32 v155,v155,v203\n"
MF "v[84:87],a[72:75],a[240:243],v[84:87]\n"
TR "v[182:183],v4 offset:6144\n"
"v_add_f32 v156,v156,v204\n"
MF "v[84:87],a[112:115],a[244:247],v[84:87]\n"
"ds_read2st64_b32 v[194:195],v13 offset0:26 offset1:27\n"
"v_add_f32 v157,v157,v205\n"
MF "v[76:79],a[108:111],a[244:247],v[76:79]\n"
TR "v[184:185],v4 offset:8192\n"
"v_add_f32 v158,v158,v206\n"
MF "v[76:79],a[68:71],a[240:243],v[76:79]\n"
TR "v[186:187],v4 offset:10240\n"
"v_add_f32 v159,v159,v207\n"
MF "v[76:79],a[28:31],v[188:191],v[76:79]\n"
"ds_read2st64_b32 v[196:197],v13 offset0:28 offset1:29\n"
CV "v152,v152,v153\n"
MF "v[68:71],a[24:27],v[188:191],v[68:71]\n"
"ds_read2st64_b32 v[198:199],v13 offset0:30 offset1:31\n"
CV "v153,v154,v155\n"
MF "v[68:71],a[64:67],a[240:243],v[68:71]\n"
"s_add_u32 s87,s90,12288\n"
CV "v154,v156,v157\n"
CV "v155,v158,v159\n"
MF "v[68:71],a[104:107],a[244:247],v[68:71]\n"
"s_cmp_eq_u64 vcc,0\n"
"s_cselect_b32 s82,s82,0xf0000000\n"
"s_cselect_b32 s84,s96,0xf0000000\n"
"s_cselect_b32 s59,32,0\n"
MF "v[60:63],a[100:103],a[244:247],v[60:63]\n"
"buffer_store_dwordx4 v[152:155],v9,s[40:43],s87 offen sc1\n"
MF "v[60:63],a[60:63],a[240:243],v[60:63]\n"
"s_add_u32 s92,s92,s59\n"
".Lnf6:\n"
"v_readlane_b32 s58,v10,s67\n"
"s_cmp_lt_u32 s58,s72\n"
"s_cbranch_scc1 .Lsp6\n"
".Lnb6:\n"
MF "v[60:63],a[20:23],v[188:191],v[60:63]\n"
"s_waitcnt lgkmcnt(2)\n"
CV "v246,v248,v249\n"
CV "v247,v250,v251\n"
MF "v[16:19],a[140:143],v[176:179],v[16:19]\n"
"s_waitcnt lgkmcnt(0)\n"
TR "v[188:189],v5\n"
"v_add_f32 v160,v160,v192\n"
MF "v[16:19],a[180:183],v[180:183],v[16:19]\n"
TR "v[190:191],v5 offset:2048\n"
"v_add_f32 v161,v161,v193\n"
MF "v[16:19],a[220:223],v[184:187],v[16:19]\n"
"ds_read2st64_b32 v[200:201],v13 offset0:32 offset1:33\n"
"v_add_f32 v162,v162,v194\n"
MF "v[24:27],a[224:227],v[184:187],v[24:27]\n"
TR "a[240:241],v5 offset:4096\n"
"v_add_f32 v163,v163,v195\n"
MF "v[24:27],a[184:187],v[180:183],v[24:27]\n"
TR "a[242:243],v5 offset:6144\n"
"v_add_f32 v164,v164,v196\n"
MF "v[24:27],a[144:147],v[176:179],v[24:27]\n"
"ds_read2st64_b32 v[202:203],v13 offset0:34 offset1:35\n"
"v_add_f32 v165,v165,v197\n"
MF "v[32:35],a[148:151],v[176:179],v[32:35]\n"
TR "a[244:245],v5 offset:8192\n"
"v_add_f32 v166,v166,v198\n"
MF "v[32:35],a[188:191],v[180:183],v[32:35]\n"
TR "a[246:247],v5 offset:10240\n"
"v_add_f32 v167,v167,v199\n"
MF "v[32:35],a[228:231],v[184:187],v[32:35]\n"
"ds_read2st64_b32 v[204:205],v13 offset0:36 offset1:37\n"
CV "v160,v160,v161\n"
MF "v[40:43],a[232:235],v[184:187],v[40:43]\n"
"ds_read2st64_b32 v[206:207],v13 offset0:38 offset1:39\n"
CV "v161,v162,v163\n"
MF "v[40:43],a[192:195],v[180:183],v[40:43]\n"
"s_add_u32 s87,s90,13312\n"
CV "v162,v164,v165\n"
CV "v163,v166,v167\n"
MF "v[40:43],a[152:155],v[176:179],v[40:43]\n"
"s_cmp_ge_u32 s85,5\n"
"s_cselect_b32 s82,s70,s82\n"
"s_cselect_b32 s84,0xf0000010,s84\n"
MF "v[48:51],a[156:159],v[176:179],v[48:51]\n"
"buffer_store_dwordx4 v[160:163],v9,s[40:43],s87 offen sc1\n"
MF "v[48:51],a[196:199],v[180:183],v[48:51]\n"
"s_cmp_ge_u32 s85,5\n"
"s_cselect_b32 s71,1024,s71\n"
"s_cselect_b32 s66,0x80000000,s66\n"
MF "v[48:51],a[236:239],v[184:187],v[48:51]\n"
"s_add_u32 s70,s70,20480\n"
"s_waitcnt lgkmcnt(2)\n"
"v_permlane16_swap_b32 v244,v246\n"
MF "v[52:55],a[236:239],a[244:247],v[52:55]\n"
"s_waitcnt lgkmcnt(0)\n"
TR "v[176:177],v6\n"
"v_add_f32 v168,v168,v200\n"
MF "v[52:55],a[196:199],a[240:243],v[52:55]\n"
TR "v[178:179],v6 offset:2048\n"
"v_add_f32 v169,v169,v201\n"
MF "v[52:55],a[156:159],v[188:191],v[52:55]\n"
"v_add_f32 v170,v170,v202\n"
"v_add_f32 v171,v171,v203\n"
MF "v[44:47],a[152:155],v[188:191],v[44:47]\n"
TR "v[180:181],v6 offset:4096\n"
"v_add_f32 v172,v172,v204\n"
MF "v[44:47],a[192:195],a[240:243],v[44:47]\n"
TR "v[182:183],v6 offset:6144\n"
"v_add_f32 v173,v173,v205\n"
MF "v[44:47],a[232:235],a[244:247],v[44:47]\n"
"v_add_f32 v174,v174,v206\n"
"v_add_f32 v175,v175,v207\n"
MF "v[36:39],a[228:231],a[244:247],v[36:39]\n"
TR "v[184:185],v6 offset:8192\n"
CV "v168,v168,v169\n"
MF "v[36:39],a[188:191],a[240:243],v[36:39]\n"
TR "v[186:187],v6 offset:10240\n"
CV "v169,v170,v171\n"
MF "v[36:39],a[148:151],v[188:191],v[36:39]\n"
"s_add_u32 s87,s90,14336\n"
CV "v170,v172,v173\n"
CV "v171,v174,v175\n"
MF "v[28:31],a[144:147],v[188:191],v[28:31]\n"
"v_permlane16_swap_b32 v245,v247\n"
MF "v[28:31],a[184:187],a[240:243],v[28:31]\n"
"buffer_store_dwordx4 v[244:247],v8,s[44:47],s83 offen sc1\n"
MF "v[28:31],a[224:227],a[244:247],v[28:31]\n"
"s_mov_b32 s83,s84\n"
MF "v[20:23],a[220:223],a[244:247],v[20:23]\n"
"buffer_store_dwordx4 v[168:171],v9,s[40:43],s87 offen sc1\n"
MF "v[20:23],a[180:183],a[240:243],v[20:23]\n"
"s_mov_b32 s90,s91\n"
"s_add_u32 s91,s91,20480\n"
MF "v[20:23],a[140:143],v[188:191],v[20:23]\n"
"s_waitcnt lgkmcnt(0)\n"
LD "v[208:211],v9,s[40:43],s82 offen sc1\n"
MF "v[56:59],a[140:143],v[176:179],v[56:59]\n"
TR "v[188:189],v7\n"
MF "v[56:59],a[180:183],v[180:183],v[56:59]\n"
TR "v[190:191],v7 offset:2048\n"
MF "v[56:59],a[220:223],v[184:187],v[56:59]\n"
MF "v[64:67],a[224:227],v[184:187],v[64:67]\n"
TR "a[240:241],v7 offset:4096\n"
MF "v[64:67],a[184:187],v[180:183],v[64:67]\n"
"s_add_u32 s82,s82,s71\n"
TR "a[242:243],v7 offset:6144\n"
MF "v[64:67],a[144:147],v[176:179],v[64:67]\n"
LD "v[212:215],v9,s[40:43],s82 offen sc1\n"
MF "v[72:75],a[148:151],v[176:179],v[72:75]\n"
TR "a[244:245],v7 offset:8192\n"
MF "v[72:75],a[188:191],v[180:183],v[72:75]\n"
TR "a[246:247],v7 offset:10240\n"
MF "v[72:75],a[228:231],v[184:187],v[72:75]\n"
MF "v[80:83],a[232:235],v[184:187],v[80:83]\n"
MF "v[80:83],a[192:195],v[180:183],v[80:83]\n"
MF "v[80:83],a[152:155],v[176:179],v[80:83]\n"
"s_add_u32 s82,s82,s71\n"
MF "v[88:91],a[156:159],v[176:179],v[88:91]\n"
LD "v[216:219],v9,s[40:43],s82 offen sc1\n"
MF "v[88:91],a[196:199],v[180:183],v[88:91]\n"
MF "v[88:91],a[236:239],v[184:187],v[88:91]\n"
"s_waitcnt lgkmcnt(0)\n"
"s_barrier\n"
MF "v[92:95],a[236:239],a[244:247],v[92:95]\n"
TR "v[176:177],v0 offset:24576\n"
MF "v[92:95],a[196:199],a[240:243],v[92:95]\n"
"s_add_u32 s85,s85,1\n"
TR "v[178:179],v0 offset:26624\n"
MF "v[92:95],a[156:159],v[188:191],v[92:95]\n"
MF "v[84:87],a[152:155],v[188:191],v[84:87]\n"
TR "v[180:181],v0 offset:28672\n"
MF "v[84:87],a[192:195],a[240:243],v[84:87]\n"
"s_add_u32 s82,s82,s71\n"
TR "v[182:183],v0 offset:30720\n"
MF "v[84:87],a[232:235],a[244:247],v[84:87]\n"
LD "v[220:223],v9,s[40:43],s82 offen sc1\n"
MF "v[76:79],a[228:231],a[244:247],v[76:79]\n"
TR "v[184:185],v0 offset:32768\n"
MF "v[76:79],a[188:191],a[240:243],v[76:79]\n"
TR "v[186:187],v0 offset:34816\n"
MF "v[76:79],a[148:151],v[188:191],v[76:79]\n"
MF "v[68:71],a[144:147],v[188:191],v[68:71]\n"
MF "v[68:71],a[184:187],a[240:243],v[68:71]\n"
MF "v[68:71],a[224:227],a[244:247],v[68:71]\n"
MF "v[60:63],a[220:223],a[244:247],v[60:63]\n"
MF "v[60:63],a[180:183],a[240:243],v[60:63]\n"
MF "v[60:63],a[140:143],v[188:191],v[60:63]\n"
"s_waitcnt lgkmcnt(0)\n"
"s_cmp_lg_u32 s85,9\n"
"s_cbranch_scc1 .Lpl1\n"
".Lplast1:\n"
MF "v[96:99],v[176:179],a[0:3],v[252:255]\n"
"s_add_u32 m0,s79,73728\n"
TR "v[188:189],v1 offset:24576\n"
TR "v[190:191],v1 offset:26624\n"
MF "v[96:99],v[180:183],a[40:43],v[96:99]\n"
"s_nop 0\n"
TR "a[240:241],v1 offset:28672\n"
TR "a[242:243],v1 offset:30720\n"
MF "v[96:99],v[184:187],a[80:83],v[96:99]\n"
"s_add_u32 s82,s82,s71\n"
TR "a[244:245],v1 offset:32768\n"
TR "a[246:247],v1 offset:34816\n"
MF "v[104:107],v[184:187],a[84:87],v[252:255]\n"
LD "v[224:227],v9,s[40:43],s82 offen sc1\n"
MF "v[104:107],v[180:183],a[44:47],v[104:107]\n"
"s_nop 0\n"
"ds_write_addtid_b32 v56\n"
"ds_write_addtid_b32 v57 offset:256\n"
MF "v[104:107],v[176:179],a[4:7],v[104:107]\n"
"s_nop 0\n"
"ds_write_addtid_b32 v58 offset:512\n"
"ds_write_addtid_b32 v59 offset:768\n"
MF "v[112:115],v[176:179],a[8:11],v[252:255]\n"
"s_nop 0\n"
"ds_write_addtid_b32 v60 offset:1024\n"
"ds_write_addtid_b32 v61 offset:1280\n"
MF "v[112:115],v[180:183],a[48:51],v[112:115]\n"
"s_nop 0\n"
"ds_write_addtid_b32 v62 offset:1536\n"
"ds_write_addtid_b32 v63 offset:1792\n"
MF "v[112:115],v[184:187],a[88:91],v[112:115]\n"
"s_nop 0\n"
"ds_write_addtid_b32 v64 offset:2048\n"
"ds_write_addtid_b32 v65 offset:2304\n"
MF "v[120:123],v[184:187],a[92:95],v[252:255]\n"
"s_nop 0\n"
"ds_write_addtid_b32 v66 offset:2560\n"
"ds_write_addtid_b32 v67 offset:2816\n"
MF "v[120:123],v[180:183],a[52:55],v[120:123]\n"
"s_nop 0\n"
"ds_write_addtid_b32 v68 offset:3072\n"
"ds_write_addtid_b32 v69 offset:3328\n"
MF "v[120:123],v[176:179],a[12:15],v[120:123]\n"
"s_nop 0\n"
"ds_write_addtid_b32 v70 offset:3584\n"
"ds_write_addtid_b32 v71 offset:3840\n"
MF "v[128:131],v[176:179],a[16:19],v[252:255]\n"
"s_nop 0\n"
"ds_write_addtid_b32 v72 offset:4096\n"
"ds_write_addtid_b32 v73 offset:4352\n"
MF "v[128:131],v[180:183],a[56:59],v[128:131]\n"
"s_nop 0\n"
"ds_write_addtid_b32 v74 offset:4608\n"
"ds_write_addtid_b32 v75 offset:4864\n"
MF "v[128:131],v[184:187],a[96:99],v[128:131]\n"
"s_waitcnt lgkmcnt(15)\n"
"ds_write_addtid_b32 v76 offset:5120\n"
MF "v[132:135],a[244:247],a[96:99],v[252:255]\n"
"s_nop 0\n"
TR "v[176:177],v2 offset:24576\n"
TR "v[178:179],v2 offset:26624\n"
MF "v[132:135],a[240:243],a[56:59],v[132:135]\n"
"s_nop 0\n"
TR "v[180:181],v2 offset:28672\n"
TR "v[182:183],v2 offset:30720\n"
MF "v[132:135],v[188:191],a[16:19],v[132:135]\n"
"s_nop 0\n"
TR "v[184:185],v2 offset:32768\n"
TR "v[186:187],v2 offset:34816\n"
MF "v[124:127],v[188:191],a[12:15],v[252:255]\n"
"s_nop 0\n"
"ds_write_addtid_b32 v77 offset:5376\n"
"ds_write_addtid_b32 v78 offset:5632\n"
MF "v[124:127],a[240:243],a[52:55],v[124:127]\n"
"s_nop 0\n"
"ds_write_addtid_b32 v79 offset:5888\n"
"ds_write_addtid_b32 v80 offset:6144\n"
MF "v[124:127],a[244:247],a[92:95],v[124:127]\n"
"s_nop 0\n"
"ds_write_addtid_b32 v81 offset:6400\n"
"ds_write_addtid_b32 v82 offset:6656\n"
MF "v[116:119],a[244:247],a[88:91],v[252:255]\n"
"s_nop 0\n"
"ds_write_addtid_b32 v83 offset:6912\n"
"ds_write_addtid_b32 v84 offset:7168\n"
MF "v[116:119],a[240:243],a[48:51],v[116:119]\n"
"s_nop 0\n"
"ds_write_addtid_b32 v85 offset:7424\n"
"ds_write_addtid_b32 v86 offset:7680\n"
MF "v[116:119],v[188:191],a[8:11],v[116:119]\n"
"s_nop 0\n"
"ds_write_addtid_b32 v87 offset:7936\n"
"ds_write_addtid_b32 v88 offset:8192\n"
MF "v[108:111],v[188:191],a[4:7],v[252:255]\n"
"s_nop 0\n"
"ds_write_addtid_b32 v89 offset:8448\n"
"ds_write_addtid_b32 v90 offset:8704\n"
MF "v[108:111],a[240:243],a[44:47],v[108:111]\n"
"s_nop 0\n"
"ds_write_addtid_b32 v91 offset:8960\n"
"ds_write_addtid_b32 v92 offset:9216\n"
MF "v[108:111],a[244:247],a[84:87],v[108:111]\n"
"s_nop 0\n"
"ds_write_addtid_b32 v93 offset:9472\n"
"ds_write_addtid_b32 v94 offset:9728\n"
MF "v[100:103],a[244:247],a[80:83],v[252:255]\n"
"ds_write_addtid_b32 v95 offset:9984\n"
MF "v[100:103],a[240:243],a[40:43],v[100:103]\n"
MF "v[100:103],v[188:191],a[0:3],v[100:103]\n"
"s_waitcnt lgkmcnt(15)\n"
MF "v[136:139],v[176:179],a[0:3],v[252:255]\n"
"s_nop 0\n"
TR "v[188:189],v3 offset:24576\n"
TR "v[190:191],v3 offset:26624\n"
MF "v[136:139],v[180:183],a[40:43],v[136:139]\n"
"s_nop 0\n"
TR "a[240:241],v3 offset:28672\n"
TR "a[242:243],v3 offset:30720\n"
MF "v[136:139],v[184:187],a[80:83],v[136:139]\n"
"s_nop 0\n"
TR "a[244:245],v3 offset:32768\n"
TR "a[246:247],v3 offset:34816\n"
MF "v[144:147],v[184:187],a[84:87],v[252:255]\n"
"s_waitcnt lgkmcnt(0)\n"
"s_barrier\n"
MF "v[144:147],v[180:183],a[44:47],v[144:147]\n"
"ds_read2st64_b32 v[56:57],v13 offset0:192 offset1:193\n"
MF "v[144:147],v[176:179],a[4:7],v[144:147]\n"
"ds_read2st64_b32 v[58:59],v13 offset0:194 offset1:195\n"
MF "v[152:155],v[176:179],a[8:11],v[252:255]\n"
"ds_read2st64_b32 v[60:61],v13 offset0:196 offset1:197\n"
MF "v[152:155],v[180:183],a[48:51],v[152:155]\n"
"ds_read2st64_b32 v[62:63],v13 offset0:198 offset1:199\n"
MF "v[152:155],v[184:187],a[88:91],v[152:155]\n"
"ds_read2st64_b32 v[64:65],v13 offset0:200 offset1:201\n"
MF "v[160:163],v[184:187],a[92:95],v[252:255]\n"
"ds_read2st64_b32 v[66:67],v13 offset0:202 offset1:203\n"
MF "v[160:163],v[180:183],a[52:55],v[160:163]\n"
"ds_read2st64_b32 v[68:69],v13 offset0:204 offset1:205\n"
MF "v[160:163],v[176:179],a[12:15],v[160:163]\n"
"ds_read2st64_b32 v[70:71],v13 offset0:206 offset1:207\n"
MF "v[168:171],v[176:179],a[16:19],v[252:255]\n"
"s_waitcnt lgkmcnt(4)\n"
"ds_read2st64_b32 v[72:73],v13 offset0:208 offset1:209\n"
"v_add_f32 v16,v16,v56\n"
MF "v[168:171],v[180:183],a[56:59],v[168:171]\n"
"ds_read2st64_b32 v[74:75],v13 offset0:210 offset1:211\n"
"v_add_f32 v17,v17,v57\n"
MF "v[168:171],v[184:187],a[96:99],v[168:171]\n"
"s_waitcnt lgkmcnt(0)\n"
"s_barrier\n"
MF "v[172:175],a[244:247],a[96:99],v[252:255]\n"
"s_nop 0\n"
TR "v[176:177],v0\n"
TR "v[178:179],v0 offset:2048\n"
MF "v[172:175],a[240:243],a[56:59],v[172:175]\n"
"s_nop 0\n"
TR "v[180:181],v0 offset:4096\n"
TR "v[182:183],v0 offset:6144\n"
MF "v[172:175],v[188:191],a[16:19],v[172:175]\n"
"s_nop 0\n"
TR "v[184:185],v0 offset:8192\n"
TR "v[186:187],v0 offset:10240\n"
MF "v[164:167],v[188:191],a[12:15],v[252:255]\n"
"ds_read2st64_b32 v[76:77],v13 offset0:212 offset1:213\n"
"v_add_f32 v18,v18,v58\n"
MF "v[164:167],a[240:243],a[52:55],v[164:167]\n"
"ds_read2st64_b32 v[78:79],v13 offset0:214 offset1:215\n"
"v_add_f32 v19,v19,v59\n"
MF "v[164:167],a[244:247],a[92:95],v[164:167]\n"
"ds_read2st64_b32 v[80:81],v13 offset0:216 offset1:217\n"
"v_add_f32 v20,v20,v60\n"
MF "v[156:159],a[244:247],a[88:91],v[252:255]\n"
"ds_read2st64_b32 v[82:83],v13 offset0:218 offset1:219\n"
"v_add_f32 v21,v21,v61\n"
MF "v[156:159],a[240:243],a[48:51],v[156:159]\n"
"ds_read2st64_b32 v[84:85],v13 offset0:220 offset1:221\n"
"v_add_f32 v22,v22,v62\n"
MF "v[156:159],v[188:191],a[8:11],v[156:159]\n"
"ds_read2st64_b32 v[86:87],v13 offset0:222 offset1:223\n"
"v_add_f32 v23,v23,v63\n"
MF "v[148:151],v[188:191],a[4:7],v[252:255]\n"
"s_waitcnt vmcnt(5)\n"
"v_mov_b32 v14,s76\n"
"s_add_u32 s76,s76,1\n"
MF "v[148:151],a[240:243],a[44:47],v[148:151]\n"
"buffer_store_dword v14,v15,s[40:43],s94 offen sc1\n"
MF "v[148:151],a[244:247],a[84:87],v[148:151]\n"
"ds_read2st64_b32 v[88:89],v13 offset0:224 offset1:225\n"
CV "v16,v16,v17\n"
MF "v[140:143],a[244:247],a[80:83],v[252:255]\n"
"ds_read2st64_b32 v[90:91],v13 offset0:226 offset1:227\n"
CV "v17,v18,v19\n"
MF "v[140:143],a[240:243],a[40:43],v[140:143]\n"
"ds_read2st64_b32 v[92:93],v13 offset0:228 offset1:229\n"
CV "v18,v20,v21\n"
MF "v[140:143],v[188:191],a[0:3],v[140:143]\n"
"s_waitcnt lgkmcnt(9)\n"
"ds_read2st64_b32 v[94:95],v13 offset0:230 offset1:231\n"
CV "v19,v22,v23\n"
MF "v[96:99],v[176:179],a[120:123],v[96:99]\n"
"s_nop 0\n"
TR "v[188:189],v1\n"
TR "v[190:191],v1 offset:2048\n"
MF "v[96:99],v[180:183],a[160:163],v[96:99]\n"
"s_nop 0\n"
TR "a[240:241],v1 offset:4096\n"
TR "a[242:243],v1 offset:6144\n"
MF "v[96:99],v[184:187],a[200:203],v[96:99]\n"
"s_add_u32 s87,s90,0\n"
TR "a[244:245],v1 offset:8192\n"
TR "a[246:247],v1 offset:10240\n"
MF "v[104:107],v[184:187],a[204:207],v[104:107]\n"
"buffer_store_dwordx4 v[16:19],v9,s[40:43],s87 offen sc1\n"
MF "v[104:107],v[180:183],a[164:167],v[104:107]\n"
"v_add_f32 v24,v24,v64\n"
"v_add_f32 v25,v25,v65\n"
MF "v[104:107],v[176:179],a[124:127],v[104:107]\n"
"v_add_f32 v26,v26,v66\n"
"v_add_f32 v27,v27,v67\n"
MF "v[112:115],v[176:179],a[128:131],v[112:115]\n"
"v_add_f32 v28,v28,v68\n"
"v_add_f32 v29,v29,v69\n"
MF "v[112:115],v[180:183],a[168:171],v[112:115]\n"
"v_add_f32 v30,v30,v70\n"
"v_add_f32 v31,v31,v71\n"
MF "v[112:115],v[184:187],a[208:211],v[112:115]\n"
CV "v24,v24,v25\n"
CV "v25,v26,v27\n"
MF "v[120:123],v[184:187],a[212:215],v[120:123]\n"
"s_add_u32 s87,s90,1024\n"
CV "v26,v28,v29\n"
CV "v27,v30,v31\n"
MF "v[120:123],v[180:183],a[172:175],v[120:123]\n"
"s_waitcnt lgkmcnt(14)\n"
"buffer_store_dwordx4 v[24:27],v9,s[40:43],s87 offen sc1\n"
MF "v[120:123],v[176:179],a[132:135],v[120:123]\n"
"v_add_f32 v32,v32,v72\n"
"v_add_f32 v33,v33,v73\n"
MF "v[128:131],v[176:179],a[136:139],v[128:131]\n"
"v_add_f32 v34,v34,v74\n"
"v_add_f32 v35,v35,v75\n"
MF "v[128:131],v[180:183],a[176:179],v[128:131]\n"
"v_add_f32 v36,v36,v76\n"
"v_add_f32 v37,v37,v77\n"
MF "v[128:131],v[184:187],a[216:219],v[128:131]\n"
"s_waitcnt lgkmcnt(0)\n"
"v_add_f32 v38,v38,v78\n"
"v_add_f32 v39,v39,v79\n"
MF "v[132:135],a[244:247],a[216:219],v[132:135]\n"
"s_nop 0\n"
TR "v[176:177],v2\n"
TR "v[178:179],v2 offset:2048\n"
MF "v[132:135],a[240:243],a[176:179],v[132:135]\n"
"s_nop 0\n"
TR "v[180:181],v2 offset:4096\n"
TR "v[182:183],v2 offset:6144\n"
MF "v[132:135],v[188:191],a[136:139],v[132:135]\n"
"s_nop 0\n"
TR "v[184:185],v2 offset:8192\n"
TR "v[186:187],v2 offset:10240\n"
MF "v[124:127],v[188:191],a[132:135],v[124:127]\n"
CV "v32,v32,v33\n"
CV "v33,v34,v35\n"
MF "v[124:127],a[240:243],a[172:175],v[124:127]\n"
"s_add_u32 s87,s90,2048\n"
CV "v34,v36,v37\n"
CV "v35,v38,v39\n"
MF "v[124:127],a[244:247],a[212:215],v[124:127]\n"
"buffer_store_dwordx4 v[32:35],v9,s[40:43],s87 offen sc1\n"
MF "v[116:119],a[244:247],a[208:211],v[116:119]\n"
"v_add_f32 v40,v40,v80\n"
"v_add_f32 v41,v41,v81\n"
MF "v[116:119],a[240:243],a[168:171],v[116:119]\n"
"v_add_f32 v42,v42,v82\n"
"v_add_f32 v43,v43,v83\n"
MF "v[116:119],v[188:191],a[128:131],v[116:119]\n"
"v_add_f32 v44,v44,v84\n"
"v_add_f32 v45,v45,v85\n"
MF "v[108:111],v[188:191],a[124:127],v[108:111]\n"
"v_add_f32 v46,v46,v86\n"
"v_add_f32 v47,v47,v87\n"
MF "v[108:111],a[240:243],a[164:167],v[108:111]\n"
CV "v40,v40,v41\n"
CV "v41,v42,v43\n"
MF "v[108:111],a[244:247],a[204:207],v[108:111]\n"
"s_add_u32 s87,s90,3072\n"
CV "v42,v44,v45\n"
CV "v43,v46,v47\n"
MF "v[100:103],a[244:247],a[200:203],v[100:103]\n"
"buffer_store_dwordx4 v[40:43],v9,s[40:43],s87 offen sc1\n"
MF "v[100:103],a[240:243],a[160:163],v[100:103]\n"
"v_add_f32 v48,v48,v88\n"
"v_add_f32 v49,v49,v89\n"
MF "v[100:103],v[188:191],a[120:123],v[100:103]\n"
"s_waitcnt lgkmcnt(0)\n"
"v_add_f32 v50,v50,v90\n"
"v_add_f32 v51,v51,v91\n"
MF "v[136:139],v[176:179],a[120:123],v[136:139]\n"
"s_nop 0\n"
TR "v[188:189],v3\n"
TR "v[190:191],v3 offset:2048\n"
MF "v[136:139],v[180:183],a[160:163],v[136:139]\n"
"s_nop 0\n"
TR "a[240:241],v3 offset:4096\n"
TR "a[242:243],v3 offset:6144\n"
MF "v[136:139],v[184:187],a[200:203],v[136:139]\n"
"s_nop 0\n"
TR "a[244:245],v3 offset:8192\n"
TR "a[246:247],v3 offset:10240\n"
MF "v[144:147],v[184:187],a[204:207],v[144:147]\n"
"v_add_f32 v52,v52,v92\n"
"v_add_f32 v53,v53,v93\n"
MF "v[144:147],v[180:183],a[164:167],v[144:147]\n"
"v_add_f32 v54,v54,v94\n"
"v_add_f32 v55,v55,v95\n"
MF "v[144:147],v[176:179],a[124:127],v[144:147]\n"
CV "v48,v48,v49\n"
CV "v49,v50,v51\n"
MF "v[152:155],v[176:179],a[128:131],v[152:155]\n"
"s_add_u32 s87,s90,4096\n"
CV "v50,v52,v53\n"
CV "v51,v54,v55\n"
MF "v[152:155],v[180:183],a[168:171],v[152:155]\n"
"buffer_store_dwordx4 v[48:51],v9,s[40:43],s87 offen sc1\n"
MF "v[152:155],v[184:187],a[208:211],v[152:155]\n"
MF "v[160:163],v[184:187],a[212:215],v[160:163]\n"
MF "v[160:163],v[180:183],a[172:175],v[160:163]\n"
MF "v[160:163],v[176:179],a[132:135],v[160:163]\n"
MF "v[168:171],v[176:179],a[136:139],v[168:171]\n"
MF "v[168:171],v[180:183],a[176:179],v[168:171]\n"
MF "v[168:171],v[184:187],a[216:219],v[168:171]\n"
"s_waitcnt lgkmcnt(0)\n"
"s_barrier\n"
MF "v[172:175],a[244:247],a[216:219],v[172:175]\n"
"s_nop 0\n"
TR "v[176:177],v0 offset:49152\n"
TR "v[178:179],v0 offset:51200\n"
MF "v[172:175],a[240:243],a[176:179],v[172:175]\n"
"s_nop 0\n"
TR "v[180:181],v0 offset:53248\n"
TR "v[182:183],v0 offset:55296\n"
MF "v[172:175],v[188:191],a[136:139],v[172:175]\n"
"s_nop 0\n"
TR "v[184:185],v0 offset:57344\n"
TR "v[186:187],v0 offset:59392\n"
MF "v[164:167],v[188:191],a[132:135],v[164:167]\n"
"buffer_load_dword v10,v12,s[40:43],s86 offen sc1\n"
MF "v[164:167],a[240:243],a[172:175],v[164:167]\n"
MF "v[164:167],a[244:247],a[212:215],v[164:167]\n"
MF "v[156:159],a[244:247],a[208:211],v[156:159]\n"
MF "v[156:159],a[240:243],a[168:171],v[156:159]\n"
MF "v[156:159],v[188:191],a[128:131],v[156:159]\n"
MF "v[148:151],v[188:191],a[124:127],v[148:151]\n"
MF "v[148:151],a[240:243],a[164:167],v[148:151]\n"
MF "v[148:151],a[244:247],a[204:207],v[148:151]\n"
MF "v[140:143],a[244:247],a[200:203],v[140:143]\n"
MF "v[140:143],a[240:243],a[160:163],v[140:143]\n"
MF "v[140:143],v[188:191],a[120:123],v[140:143]\n"
"s_waitcnt lgkmcnt(0)\n"
MF "v[96:99],v[176:179],a[20:23],v[96:99]\n"
"s_nop 0\n"
TR "v[188:189],v1 offset:49152\n"
TR "v[190:191],v1 offset:51200\n"
MF "v[96:99],v[180:183],a[60:63],v[96:99]\n"
"s_nop 0\n"
TR "a[240:241],v1 offset:53248\n"
TR "a[242:243],v1 offset:55296\n"
MF "v[96:99],v[184:187],a[100:103],v[96:99]\n"
"s_nop 0\n"
TR "a[244:245],v1 offset:57344\n"
TR "a[246:247],v1 offset:59392\n"
MF "v[104:107],v[184:187],a[104:107],v[104:107]\n"
MF "v[104:107],v[180:183],a[64:67],v[104:107]\n"
MF "v[104:107],v[176:179],a[24:27],v[104:107]\n"
"s_waitcnt vmcnt(7)\n"
MF "v[136:139],v[208:211],a[252:255],v[136:139]\n"
MF "v[144:147],v[212:215],a[252:255],v[144:147]\n"
MF "v[152:155],v[216:219],a[252:255],v[152:155]\n"
MF "v[160:163],v[220:223],a[252:255],v[160:163]\n"
MF "v[168:171],v[224:227],a[252:255],v[168:171]\n"
MF "v[140:143],v[208:211],a[248:251],v[140:143]\n"
MF "v[148:151],v[212:215],a[248:251],v[148:151]\n"
MF "v[156:159],v[216:219],a[248:251],v[156:159]\n"
MF "v[164:167],v[220:223],a[248:251],v[164:167]\n"
MF "v[172:175],v[224:227],a[248:251],v[172:175]\n"
MF "v[112:115],v[176:179],a[28:31],v[112:115]\n"
MF "v[112:115],v[180:183],a[68:71],v[112:115]\n"
MF "v[112:115],v[184:187],a[108:111],v[112:115]\n"
MF "v[120:123],v[184:187],a[112:115],v[120:123]\n"
MF "v[120:123],v[180:183],a[72:75],v[120:123]\n"
"s_bfe_u32 s87,s22,0x30014\n"
MF "v[120:123],v[176:179],a[32:35],v[120:123]\n"
"s_lshl_b32 s87,s87,7\n"
MF "v[128:131],v[176:179],a[36:39],v[128:131]\n"
".Lpg29h:\n"
"s_cbranch_execz .Lisr4\n"
"s_and_b32 s60,s56,1\n"
MF "v[128:131],v[180:183],a[76:79],v[128:131]\n"
"s_mul_i32 s60,s60,204800\n"
MF "v[128:131],v[184:187],a[116:119],v[128:131]\n"
"s_waitcnt lgkmcnt(0)\n"
MF "v[132:135],a[244:247],a[116:119],v[132:135]\n"
"s_add_u32 s87,s87,s60\n"
TR "v[176:177],v2 offset:49152\n"
TR "v[178:179],v2 offset:51200\n"
MF "v[132:135],a[240:243],a[76:79],v[132:135]\n"
"s_mov_b32 s71,204800\n"
TR "v[180:181],v2 offset:53248\n"
TR "v[182:183],v2 offset:55296\n"
MF "v[132:135],v[188:191],a[36:39],v[132:135]\n"
"s_mov_b32 s66,204800\n"
TR "v[184:185],v2 offset:57344\n"
TR "v[186:187],v2 offset:59392\n"
MF "v[124:127],v[188:191],a[32:35],v[124:127]\n"
"s_add_u32 s72,s78,2\n"
MF "v[124:127],a[240:243],a[72:75],v[124:127]\n"
"s_and_b32 s72,s72,15\n"
MF "v[124:127],a[244:247],a[112:115],v[124:127]\n"
"s_bfe_u32 s60,s22,0x10014\n"
MF "v[116:119],a[244:247],a[108:111],v[116:119]\n"
"s_lshl_b32 s60,s60,5\n"
MF "v[116:119],a[240:243],a[68:71],v[116:119]\n"
"v_xor_b32 v242,s60,v8\n"
MF "v[116:119],v[188:191],a[28:31],v[116:119]\n"
MF "v[108:111],v[188:191],a[24:27],v[108:111]\n"
"s_add_u32 s16,s78,6\n"
MF "v[108:111],a[240:243],a[64:67],v[108:111]\n"
"s_and_b32 s16,s16,15\n"
MF "v[108:111],a[244:247],a[104:107],v[108:111]\n"
"s_add_u32 s17,s78,4\n"
MF "v[100:103],a[244:247],a[100:103],v[100:103]\n"
"s_and_b32 s17,s17,15\n"
MF "v[100:103],a[240:243],a[60:63],v[100:103]\n"
MF "v[100:103],v[188:191],a[20:23],v[100:103]\n"
"s_waitcnt lgkmcnt(0)\n"
MF "v[136:139],v[176:179],a[20:23],v[136:139]\n"
"s_nop 0\n"
TR "v[188:189],v3 offset:49152\n"
TR "v[190:191],v3 offset:51200\n"
MF "v[136:139],v[180:183],a[60:63],v[136:139]\n"
"s_nop 0\n"
TR "a[240:241],v3 offset:53248\n"
TR "a[242:243],v3 offset:55296\n"
MF "v[136:139],v[184:187],a[100:103],v[136:139]\n"
"s_nop 0\n"
TR "a[244:245],v3 offset:57344\n"
TR "a[246:247],v3 offset:59392\n"
MF "v[144:147],v[184:187],a[104:107],v[144:147]\n"
MF "v[144:147],v[180:183],a[64:67],v[144:147]\n"
"v_mov_b32 v14,s76\n"
"s_add_u32 s76,s76,1\n"
MF "v[144:147],v[176:179],a[24:27],v[144:147]\n"
MF "v[152:155],v[176:179],a[28:31],v[152:155]\n"
MF "v[152:155],v[180:183],a[68:71],v[152:155]\n"
MF "v[152:155],v[184:187],a[108:111],v[152:155]\n"
MF "v[160:163],v[184:187],a[112:115],v[160:163]\n"
MF "v[160:163],v[180:183],a[72:75],v[160:163]\n"
MF "v[160:163],v[176:179],a[32:35],v[160:163]\n"
MF "v[168:171],v[176:179],a[36:39],v[168:171]\n"
MF "v[168:171],v[180:183],a[76:79],v[168:171]\n"
MF "v[168:171],v[184:187],a[116:119],v[168:171]\n"
"s_waitcnt vmcnt(0) lgkmcnt(0)\n"
"s_barrier\n"
MF "v[172:175],a[244:247],a[116:119],v[172:175]\n"
"buffer_store_dword v14,v15,s[40:43],s94 offen sc1\n"
MF "v[172:175],a[240:243],a[76:79],v[172:175]\n"
"s_nop 0\n"
TR "v[176:177],v6\n"
TR "v[178:179],v6 offset:2048\n"
MF "v[172:175],v[188:191],a[36:39],v[172:175]\n"
"s_nop 0\n"
TR "v[180:181],v6 offset:4096\n"
TR "v[182:183],v6 offset:6144\n"
MF "v[164:167],v[188:191],a[32:35],v[164:167]\n"
"s_nop 0\n"
TR "v[184:185],v6 offset:8192\n"
TR "v[186:187],v6 offset:10240\n"
MF "v[164:167],a[240:243],a[72:75],v[164:167]\n"
"v_sub_u32 v10,v10,v11\n"
MF "v[164:167],a[244:247],a[112:115],v[164:167]\n"
"v_readlane_b32 s16,v10,s16\n"
MF "v[156:159],a[244:247],a[108:111],v[156:159]\n"
"s_cmp_lt_u32 s16,9\n"
"s_cselect_b32 s16,0xf0000000,0\n"
MF "v[156:159],a[240:243],a[68:71],v[156:159]\n"
"v_readlane_b32 s17,v10,s17\n"
MF "v[156:159],v[188:191],a[28:31],v[156:159]\n"
"s_cmp_lt_u32 s17,10\n"
"s_cselect_b32 s17,0xf0000000,0\n"
MF "v[148:151],v[188:191],a[24:27],v[148:151]\n"
"s_bfe_u32 s62,s22,0x30014\n"
MF "v[148:151],a[240:243],a[64:67],v[148:151]\n"
"s_add_u32 s62,s62,3\n"
"s_and_b32 s62,s62,7\n"
"s_mul_i32 s62,s62,204800\n"
MF "v[148:151],a[244:247],a[104:107],v[148:151]\n"
"s_add_u32 s62,s62,s73\n"
"s_add_u32 s62,s62,122880\n"
"s_or_b32 s62,s62,s16\n"
MF "v[140:143],a[244:247],a[100:103],v[140:143]\n"
LD "v[56:59],v9,s[40:43],s62 offen sc1\n"
MF "v[140:143],a[240:243],a[60:63],v[140:143]\n"
LD "v[64:67],v9,s[40:43],s62 offen offset:1024 sc1\n"
MF "v[140:143],v[188:191],a[20:23],v[140:143]\n"
"s_waitcnt lgkmcnt(0)\n"
LD "v[72:75],v9,s[40:43],s62 offen offset:2048 sc1\n"
MF "v[136:139],v[176:179],a[140:143],v[136:139]\n"
"s_nop 0\n"
TR "v[188:189],v7\n"
TR "v[190:191],v7 offset:2048\n"
MF "v[136:139],v[180:183],a[180:183],v[136:139]\n"
"s_nop 0\n"
TR "a[240:241],v7 offset:4096\n"
TR "a[242:243],v7 offset:6144\n"
MF "v[136:139],v[184:187],a[220:223],v[136:139]\n"
"s_nop 0\n"
TR "a[244:245],v7 offset:8192\n"
TR "a[246:247],v7 offset:10240\n"
MF "v[144:147],v[184:187],a[224:227],v[144:147]\n"
LD "v[80:83],v9,s[40:43],s62 offen offset:3072 sc1\n"
MF "v[144:147],v[180:183],a[184:187],v[144:147]\n"
MF "v[144:147],v[176:179],a[144:147],v[144:147]\n"
"s_add_u32 s62,s62,4096\n"
MF "v[152:155],v[176:179],a[148:151],v[152:155]\n"
LD "v[88:91],v9,s[40:43],s62 offen sc1\n"
MF "v[152:155],v[180:183],a[188:191],v[152:155]\n"
"s_bfe_u32 s62,s22,0x30014\n"
"ds_write_addtid_b32 v136\n"
"ds_write_addtid_b32 v137 offset:256\n"
MF "v[152:155],v[184:187],a[228:231],v[152:155]\n"
"s_add_u32 s62,s62,2\n"
"s_and_b32 s62,s62,7\n"
"s_mul_i32 s62,s62,204800\n"
MF "v[160:163],v[184:187],a[232:235],v[160:163]\n"
"s_add_u32 s62,s62,s73\n"
"s_add_u32 s62,s62,143360\n"
"s_or_b32 s62,s62,s17\n"
MF "v[160:163],v[180:183],a[192:195],v[160:163]\n"
LD "v[60:63],v9,s[40:43],s62 offen sc1\n"
MF "v[160:163],v[176:179],a[152:155],v[160:163]\n"
"s_nop 0\n"
"ds_write_addtid_b32 v138 offset:512\n"
"ds_write_addtid_b32 v139 offset:768\n"
MF "v[168:171],v[176:179],a[156:159],v[168:171]\n"
LD "v[68:71],v9,s[40:43],s62 offen offset:1024 sc1\n"
MF "v[168:171],v[180:183],a[196:199],v[168:171]\n"
"s_nop 0\n"
"ds_write_addtid_b32 v144 offset:2048\n"
"ds_write_addtid_b32 v145 offset:2304\n"
MF "v[168:171],v[184:187],a[236:239],v[168:171]\n"
"s_waitcnt lgkmcnt(6)\n"
LD "v[76:79],v9,s[40:43],s62 offen offset:2048 sc1\n"
MF "v[140:143],v[188:191],a[140:143],v[140:143]\n"
"s_nop 0\n"
TR "v[176:177],v4\n"
TR "v[178:179],v4 offset:2048\n"
MF "v[140:143],a[240:243],a[180:183],v[140:143]\n"
"s_nop 0\n"
TR "v[180:181],v4 offset:4096\n"
TR "v[182:183],v4 offset:6144\n"
MF "v[140:143],a[244:247],a[220:223],v[140:143]\n"
"s_nop 0\n"
TR "v[184:185],v4 offset:8192\n"
TR "v[186:187],v4 offset:10240\n"
MF "v[148:151],a[244:247],a[224:227],v[148:151]\n"
LD "v[84:87],v9,s[40:43],s62 offen offset:3072 sc1\n"
MF "v[148:151],a[240:243],a[184:187],v[148:151]\n"
"s_nop 0\n"
"ds_write_addtid_b32 v146 offset:2560\n"
"ds_write_addtid_b32 v147 offset:2816\n"
MF "v[148:151],v[188:191],a[144:147],v[148:151]\n"
"s_add_u32 s62,s62,4096\n"
"ds_write_addtid_b32 v152 offset:4096\n"
"ds_write_addtid_b32 v153 offset:4352\n"
MF "v[156:159],v[188:191],a[148:151],v[156:159]\n"
LD "v[92:95],v9,s[40:43],s62 offen sc1\n"
MF "v[156:159],a[240:243],a[188:191],v[156:159]\n"
"s_nop 0\n"
"ds_write_addtid_b32 v154 offset:4608\n"
"ds_write_addtid_b32 v155 offset:4864\n"
MF "v[156:159],a[244:247],a[228:231],v[156:159]\n"
"s_nop 0\n"
"ds_write_addtid_b32 v160 offset:6144\n"
"ds_write_addtid_b32 v161 offset:6400\n"
MF "v[164:167],a[244:247],a[232:235],v[164:167]\n"
"s_nop 0\n"
"ds_write_addtid_b32 v162 offset:6656\n"
"ds_write_addtid_b32 v163 offset:6912\n"
MF "v[164:167],a[240:243],a[192:195],v[164:167]\n"
"s_nop 0\n"
"ds_write_addtid_b32 v168 offset:8192\n"
"ds_write_addtid_b32 v169 offset:8448\n"
MF "v[164:167],v[188:191],a[152:155],v[164:167]\n"
"buffer_load_dword v10,v12,s[40:43],s86 offen sc1\n"
MF "v[172:175],v[188:191],a[156:159],v[172:175]\n"
"s_nop 0\n"
"ds_write_addtid_b32 v170 offset:8704\n"
"ds_write_addtid_b32 v171 offset:8960\n"
MF "v[172:175],a[240:243],a[196:199],v[172:175]\n"
"s_nop 0\n"
"ds_write_addtid_b32 v140 offset:1024\n"
"ds_write_addtid_b32 v141 offset:1280\n"
MF "v[172:175],a[244:247],a[236:239],v[172:175]\n"
"s_waitcnt lgkmcnt(15)\n"
"ds_write_addtid_b32 v142 offset:1536\n"
"ds_write_addtid_b32 v143 offset:1792\n"
MF "v[96:99],v[176:179],a[140:143],v[96:99]\n"
"s_nop 0\n"
TR "v[188:189],v5\n"
TR "v[190:191],v5 offset:2048\n"
MF "v[96:99],v[180:183],a[180:183],v[96:99]\n"
"s_nop 0\n"
TR "a[240:241],v5 offset:4096\n"
TR "a[242:243],v5 offset:6144\n"
MF "v[96:99],v[184:187],a[220:223],v[96:99]\n"
"s_nop 0\n"
TR "a[244:245],v5 offset:8192\n"
TR "a[246:247],v5 offset:10240\n"
MF "v[104:107],v[184:187],a[224:227],v[104:107]\n"
"s_nop 0\n"
"ds_write_addtid_b32 v148 offset:3072\n"
"ds_write_addtid_b32 v149 offset:3328\n"
MF "v[104:107],v[180:183],a[184:187],v[104:107]\n"
"s_nop 0\n"
"ds_write_addtid_b32 v150 offset:3584\n"
"ds_write_addtid_b32 v151 offset:3840\n"
MF "v[104:107],v[176:179],a[144:147],v[104:107]\n"
"s_nop 0\n"
"ds_write_addtid_b32 v156 offset:5120\n"
"ds_write_addtid_b32 v157 offset:5376\n"
MF "v[112:115],v[176:179],a[148:151],v[112:115]\n"
"s_nop 0\n"
"ds_write_addtid_b32 v158 offset:5632\n"
"ds_write_addtid_b32 v159 offset:5888\n"
MF "v[112:115],v[180:183],a[188:191],v[112:115]\n"
"ds_write_addtid_b32 v164 offset:7168\n"
MF "v[112:115],v[184:187],a[228:231],v[112:115]\n"
"ds_write_addtid_b32 v165 offset:7424\n"
MF "v[120:123],v[184:187],a[232:235],v[120:123]\n"
"ds_write_addtid_b32 v166 offset:7680\n"
MF "v[120:123],v[180:183],a[192:195],v[120:123]\n"
"ds_write_addtid_b32 v167 offset:7936\n"
MF "v[120:123],v[176:179],a[152:155],v[120:123]\n"
"ds_write_addtid_b32 v172 offset:9216\n"
MF "v[128:131],v[176:179],a[156:159],v[128:131]\n"
"ds_write_addtid_b32 v173 offset:9472\n"
MF "v[128:131],v[180:183],a[196:199],v[128:131]\n"
"ds_write_addtid_b32 v174 offset:9728\n"
MF "v[128:131],v[184:187],a[236:239],v[128:131]\n"
"s_waitcnt lgkmcnt(15)\n"
"ds_write_addtid_b32 v175 offset:9984\n"
MF "v[100:103],v[188:191],a[140:143],v[100:103]\n"
MF "v[100:103],a[240:243],a[180:183],v[100:103]\n"
MF "v[100:103],a[244:247],a[220:223],v[100:103]\n"
MF "v[108:111],a[244:247],a[224:227],v[108:111]\n"
MF "v[108:111],a[240:243],a[184:187],v[108:111]\n"
"s_waitcnt lgkmcnt(0)\n"
"s_barrier\n"
MF "v[108:111],v[188:191],a[144:147],v[108:111]\n"
"ds_read2st64_b32 v[16:17],v13 offset0:192 offset1:193\n"
MF "v[116:119],v[188:191],a[148:151],v[116:119]\n"
"ds_read2st64_b32 v[18:19],v13 offset0:194 offset1:195\n"
MF "v[116:119],a[240:243],a[188:191],v[116:119]\n"
"ds_read2st64_b32 v[20:21],v13 offset0:196 offset1:197\n"
MF "v[116:119],a[244:247],a[228:231],v[116:119]\n"
"ds_read2st64_b32 v[22:23],v13 offset0:198 offset1:199\n"
MF "v[124:127],a[244:247],a[232:235],v[124:127]\n"
"ds_read2st64_b32 v[24:25],v13 offset0:200 offset1:201\n"
MF "v[124:127],a[240:243],a[192:195],v[124:127]\n"
"ds_read2st64_b32 v[26:27],v13 offset0:202 offset1:203\n"
MF "v[124:127],v[188:191],a[152:155],v[124:127]\n"
"ds_read2st64_b32 v[28:29],v13 offset0:204 offset1:205\n"
MF "v[132:135],v[188:191],a[156:159],v[132:135]\n"
"ds_read2st64_b32 v[30:31],v13 offset0:206 offset1:207\n"
MF "v[132:135],a[240:243],a[196:199],v[132:135]\n"
"ds_read2st64_b32 v[32:33],v13 offset0:208 offset1:209\n"
MF "v[132:135],a[244:247],a[236:239],v[132:135]\n"
"ds_read2st64_b32 v[34:35],v13 offset0:210 offset1:211\n"
"ds_read2st64_b32 v[36:37],v13 offset0:212 offset1:213\n"
"ds_read2st64_b32 v[38:39],v13 offset0:214 offset1:215\n"
"ds_read2st64_b32 v[40:41],v13 offset0:216 offset1:217\n"
"ds_read2st64_b32 v[42:43],v13 offset0:218 offset1:219\n"
"ds_read2st64_b32 v[44:45],v13 offset0:220 offset1:221\n"
"ds_read2st64_b32 v[46:47],v13 offset0:222 offset1:223\n"
"ds_read2st64_b32 v[48:49],v13 offset0:224 offset1:225\n"
"ds_read2st64_b32 v[50:51],v13 offset0:226 offset1:227\n"
"ds_read2st64_b32 v[52:53],v13 offset0:228 offset1:229\n"
"ds_read2st64_b32 v[54:55],v13 offset0:230 offset1:231\n"
"s_waitcnt vmcnt(0)\n"
"v_sub_u32 v10,v10,v11\n"
"s_cmp_eq_u32 s16,0\n"
"s_cbranch_scc1 .Lhave13\n"
"s_add_u32 s59,s78,6\n"
"s_and_b32 s59,s59,15\n"
"s_nop 3\n"
".Lnf7:\n"
"v_readlane_b32 s58,v10,s59\n"
"s_cmp_lt_u32 s58,9\n"
"s_cbranch_scc1 .Lsp7\n"
".Lnb7:\n"
"s_bfe_u32 s62,s22,0x30014\n"
"s_add_u32 s62,s62,3\n"
"s_and_b32 s62,s62,7\n"
"s_mul_i32 s62,s62,204800\n"
"s_add_u32 s62,s62,s73\n"
"s_add_u32 s62,s62,122880\n"
LD "v[56:59],v9,s[40:43],s62 offen sc1\n"
LD "v[64:67],v9,s[40:43],s62 offen offset:1024 sc1\n"
LD "v[72:75],v9,s[40:43],s62 offen offset:2048 sc1\n"
LD "v[80:83],v9,s[40:43],s62 offen offset:3072 sc1\n"
"s_add_u32 s62,s62,4096\n"
LD "v[88:91],v9,s[40:43],s62 offen sc1\n"
".Lhave13:\n"
"s_cmp_eq_u32 s17,0\n"
"s_cbranch_scc1 .Lhave12\n"
"s_add_u32 s59,s78,4\n"
"s_and_b32 s59,s59,15\n"
"s_nop 3\n"
".Lnf8:\n"
"v_readlane_b32 s58,v10,s59\n"
"s_cmp_lt_u32 s58,10\n"
"s_cbranch_scc1 .Lsp8\n"
".Lnb8:\n"
"s_bfe_u32 s62,s22,0x30014\n"
"s_add_u32 s62,s62,2\n"
"s_and_b32 s62,s62,7\n"
"s_mul_i32 s62,s62,204800\n"
"s_add_u32 s62,s62,s73\n"
"s_add_u32 s62,s62,143360\n"
LD "v[60:63],v9,s[40:43],s62 offen sc1\n"
LD "v[68:71],v9,s[40:43],s62 offen offset:1024 sc1\n"
LD "v[76:79],v9,s[40:43],s62 offen offset:2048 sc1\n"
LD "v[84:87],v9,s[40:43],s62 offen offset:3072 sc1\n"
"s_add_u32 s62,s62,4096\n"
LD "v[92:95],v9,s[40:43],s62 offen sc1\n"
".Lhave12:\n"
".Lnf9:\n"
"v_readlane_b32 s58,v10,s72\n"
"s_cmp_lt_u32 s58,11\n"
"s_cbranch_scc1 .Lsp9\n"
".Lnb9:\n"
"s_bfe_u32 s62,s22,0x30014\n"
"s_add_u32 s62,s62,1\n"
"s_and_b32 s62,s62,7\n"
"s_mul_i32 s62,s62,204800\n"
"s_add_u32 s62,s62,s73\n"
"s_add_u32 s62,s62,163840\n"
LD "v[208:211],v9,s[40:43],s62 offen sc1\n"
LD "v[212:215],v9,s[40:43],s62 offen offset:1024 sc1\n"
LD "v[216:219],v9,s[40:43],s62 offen offset:2048 sc1\n"
LD "v[220:223],v9,s[40:43],s62 offen offset:3072 sc1\n"
"s_add_u32 s62,s62,4096\n"
LD "v[224:227],v9,s[40:43],s62 offen sc1\n"
"s_waitcnt lgkmcnt(15)\n"
"v_pk_add_f32 v[96:97],v[96:97],v[16:17]\n"
"v_pk_add_f32 v[98:99],v[98:99],v[18:19]\n"
"v_pk_add_f32 v[100:101],v[100:101],v[20:21]\n"
"v_pk_add_f32 v[102:103],v[102:103],v[22:23]\n"
"v_pk_add_f32 v[104:105],v[104:105],v[24:25]\n"
"s_waitcnt lgkmcnt(14)\n"
"v_pk_add_f32 v[106:107],v[106:107],v[26:27]\n"
"s_waitcnt lgkmcnt(13)\n"
"v_pk_add_f32 v[108:109],v[108:109],v[28:29]\n"
"s_waitcnt lgkmcnt(12)\n"
"v_pk_add_f32 v[110:111],v[110:111],v[30:31]\n"
"s_waitcnt lgkmcnt(11)\n"
"v_pk_add_f32 v[112:113],v[112:113],v[32:33]\n"
"s_waitcnt lgkmcnt(10)\n"
"v_pk_add_f32 v[114:115],v[114:115],v[34:35]\n"
"s_waitcnt lgkmcnt(9)\n"
"v_pk_add_f32 v[116:117],v[116:117],v[36:37]\n"
"s_waitcnt lgkmcnt(8)\n"
"v_pk_add_f32 v[118:119],v[118:119],v[38:39]\n"
"s_waitcnt lgkmcnt(7)\n"
"v_pk_add_f32 v[120:121],v[120:121],v[40:41]\n"
"s_waitcnt lgkmcnt(6)\n"
"v_pk_add_f32 v[122:123],v[122:123],v[42:43]\n"
"s_waitcnt lgkmcnt(5)\n"
"v_pk_add_f32 v[124:125],v[124:125],v[44:45]\n"
"s_waitcnt lgkmcnt(4)\n"
"v_pk_add_f32 v[126:127],v[126:127],v[46:47]\n"
"s_waitcnt lgkmcnt(3)\n"
"v_pk_add_f32 v[128:129],v[128:129],v[48:49]\n"
"s_waitcnt lgkmcnt(2)\n"
"v_pk_add_f32 v[130:131],v[130:131],v[50:51]\n"
"s_waitcnt lgkmcnt(1)\n"
"v_pk_add_f32 v[132:133],v[132:133],v[52:53]\n"
"s_waitcnt lgkmcnt(0)\n"
"v_pk_add_f32 v[134:135],v[134:135],v[54:55]\n"
"s_waitcnt vmcnt(5)\n"
"s_nop 0\n"
MF "v[100:103],v[56:59],a[252:255],v[100:103]\n"
MF "v[108:111],v[64:67],a[252:255],v[108:111]\n"
MF "v[116:119],v[72:75],a[252:255],v[116:119]\n"
MF "v[124:127],v[80:83],a[252:255],v[124:127]\n"
MF "v[132:135],v[88:91],a[252:255],v[132:135]\n"
MF "v[96:99],v[56:59],a[248:251],v[96:99]\n"
MF "v[104:107],v[64:67],a[248:251],v[104:107]\n"
MF "v[112:115],v[72:75],a[248:251],v[112:115]\n"
MF "v[120:123],v[80:83],a[248:251],v[120:123]\n"
MF "v[128:131],v[88:91],a[248:251],v[128:131]\n"
MF "v[96:99],v[60:63],a[252:255],v[96:99]\n"
MF "v[104:107],v[68:71],a[252:255],v[104:107]\n"
MF "v[112:115],v[76:79],a[252:255],v[112:115]\n"
MF "v[120:123],v[84:87],a[252:255],v[120:123]\n"
MF "v[128:131],v[92:95],a[252:255],v[128:131]\n"
MF "v[100:103],v[60:63],a[248:251],v[100:103]\n"
MF "v[108:111],v[68:71],a[248:251],v[108:111]\n"
MF "v[116:119],v[76:79],a[248:251],v[116:119]\n"
MF "v[124:127],v[84:87],a[248:251],v[124:127]\n"
MF "v[132:135],v[92:95],a[248:251],v[132:135]\n"
"s_waitcnt vmcnt(0)\n"
MF "v[100:103],v[208:211],a[252:255],v[100:103]\n"
MF "v[96:99],v[208:211],a[248:251],v[96:99]\n"
MF "v[108:111],v[212:215],a[252:255],v[108:111]\n"
MF "v[104:107],v[212:215],a[248:251],v[104:107]\n"
MF "v[116:119],v[216:219],a[252:255],v[116:119]\n"
"s_nop 4\n"
CV "v96,v96,v97\n"
CV "v97,v98,v99\n"
MF "v[112:115],v[216:219],a[248:251],v[112:115]\n"
CV "v98,v100,v101\n"
CV "v99,v102,v103\n"
MF "v[124:127],v[220:223],a[252:255],v[124:127]\n"
CV "v104,v104,v105\n"
CV "v105,v106,v107\n"
MF "v[120:123],v[220:223],a[248:251],v[120:123]\n"
CV "v106,v108,v109\n"
CV "v107,v110,v111\n"
"v_permlane16_swap_b32 v96,v98\n"
"v_permlane16_swap_b32 v97,v99\n"
"buffer_store_dwordx4 v[96:99],v242,s[44:47],s87 offen sc1\n"
"s_add_u32 s87,s87,40960\n"
MF "v[132:135],v[224:227],a[252:255],v[132:135]\n"
CV "v112,v112,v113\n"
CV "v113,v114,v115\n"
MF "v[128:131],v[224:227],a[248:251],v[128:131]\n"
CV "v114,v116,v117\n"
CV "v115,v118,v119\n"
"v_permlane16_swap_b32 v104,v106\n"
"v_permlane16_swap_b32 v105,v107\n"
"buffer_store_dwordx4 v[104:107],v242,s[44:47],s87 offen sc1\n"
"s_add_u32 s87,s87,40960\n"
CV "v120,v120,v121\n"
CV "v121,v122,v123\n"
CV "v122,v124,v125\n"
CV "v123,v126,v127\n"
"v_permlane16_swap_b32 v112,v114\n"
"v_permlane16_swap_b32 v113,v115\n"
"buffer_store_dwordx4 v[112:115],v242,s[44:47],s87 offen sc1\n"
"s_add_u32 s87,s87,40960\n"
CV "v128,v128,v129\n"
CV "v129,v130,v131\n"
CV "v130,v132,v133\n"
CV "v131,v134,v135\n"
"v_permlane16_swap_b32 v120,v122\n"
"v_permlane16_swap_b32 v121,v123\n"
"buffer_store_dwordx4 v[120:123],v242,s[44:47],s87 offen sc1\n"
"s_add_u32 s87,s87,40960\n"
"v_permlane16_swap_b32 v128,v130\n"
"v_permlane16_swap_b32 v129,v131\n"
"buffer_store_dwordx4 v[128:131],v242,s[44:47],s87 offen sc1\n"
"s_add_u32 s87,s87,40960\n"
"s_branch .Ljobs\n"
".Lpgjobs_h:\n"
"s_cbranch_execz .Lisr4\n"
".Ljobs:\n"
"s_cmp_ge_u32 s92,40\n"
"s_cbranch_scc1 .Ldone\n"
"buffer_load_dword v10,v12,s[40:43],s86 offen sc1\n"
"s_mul_i32 s95,s92,3277\n"
"s_lshr_b32 s95,s95,16\n"
"s_mul_i32 s59,s95,20\n"
"s_sub_u32 s59,s92,s59\n"
"s_cmp_ge_u32 s59,10\n"
"s_cselect_b32 s96,4294557760,0\n"
"s_add_u32 s96,s96,1024\n"
"s_mul_i32 s59,s59,40960\n"
"s_add_u32 s96,s96,s59\n"
"s_lshl_b32 s59,s95,7\n"
"s_add_u32 s96,s96,s59\n"
"s_add_u32 s95,s95,3\n"
"s_waitcnt vmcnt(0)\n"
".Ljtry:\n"
"s_cmp_eq_u32 s98,0\n"
"s_cselect_b32 s95,0,s95\n"
"v_sub_u32 v10,v10,v11\n"
"v_cmp_gt_u32 vcc,s95,v10\n"
"s_lshl_b32 s82,s92,10\n"
"s_add_u32 s82,s82,s93\n"
"s_cmp_eq_u64 vcc,0\n"
"s_cselect_b32 s82,s82,0xf0000000\n"
"s_cselect_b32 s84,s96,0xf0000000\n"
"s_cselect_b32 s59,32,0\n"
"s_add_u32 s92,s92,s59\n"
"s_cmp_eq_u32 s59,0\n"
"s_cbranch_scc0 .Ljgo\n"
"s_sub_u32 s98,s98,1\n"
"s_sleep 2\n"
"s_branch .Ljobs\n"
".Ljgo:\n"
"s_mov_b32 s83,s84\n"
LD "v[208:211],v9,s[40:43],s82 offen sc1\n"
"s_add_u32 s82,s82,s71\n"
LD "v[212:215],v9,s[40:43],s82 offen sc1\n"
"s_add_u32 s82,s82,s71\n"
LD "v[216:219],v9,s[40:43],s82 offen sc1\n"
"s_add_u32 s82,s82,s71\n"
LD "v[220:223],v9,s[40:43],s82 offen sc1\n"
"s_add_u32 s82,s82,s71\n"
LD "v[224:227],v9,s[40:43],s82 offen sc1\n"
"s_add_u32 s82,s82,s66\n"
LD "v[228:231],v9,s[40:43],s82 offen sc1\n"
"s_add_u32 s82,s82,s71\n"
LD "v[232:235],v9,s[40:43],s82 offen sc1\n"
"s_add_u32 s82,s82,s71\n"
LD "v[236:239],v9,s[40:43],s82 offen sc1\n"
"buffer_load_dword v10,v12,s[40:43],s86 offen sc1\n"
"s_waitcnt vmcnt(0)\n"
".Ljfin:\n"
"s_nop 1\n"
"v_mov_b32 v240,v208\n"
"v_mov_b32 v241,v209\n"
MF "v[248:251],v[210:213],a[250:253],0\n"
MF "v[244:247],v[214:217],a[250:253],0\n"
MF "v[248:251],v[218:221],a[250:253],v[248:251]\n"
MF "v[244:247],v[222:225],a[250:253],v[244:247]\n"
MF "v[248:251],v[226:229],a[250:253],v[248:251]\n"
MF "v[244:247],v[230:233],a[250:253],v[244:247]\n"
MF "v[248:251],v[234:237],a[250:253],v[248:251]\n"
MF "v[244:247],v[238:241],a[250:253],v[244:247]\n"
"s_nop 7\n"
CV "v244,v244,v245\n"
CV "v245,v246,v247\n"
CV "v246,v248,v249\n"
CV "v247,v250,v251\n"
"s_nop 0\n"
"v_permlane16_swap_b32 v244,v246\n"
"v_permlane16_swap_b32 v245,v247\n"
"buffer_store_dwordx4 v[244:247],v8,s[44:47],s83 offen sc1\n"
"s_cmp_ge_u32 s92,40\n"
"s_cbranch_scc1 .Ldone\n"
"s_mul_i32 s95,s92,3277\n"
"s_lshr_b32 s95,s95,16\n"
"s_mul_i32 s59,s95,20\n"
"s_sub_u32 s59,s92,s59\n"
"s_cmp_ge_u32 s59,10\n"
"s_cselect_b32 s96,4294557760,0\n"
"s_add_u32 s96,s96,1024\n"
"s_mul_i32 s59,s59,40960\n"
"s_add_u32 s96,s96,s59\n"
"s_lshl_b32 s59,s95,7\n"
"s_add_u32 s96,s96,s59\n"
"s_add_u32 s95,s95,3\n"
"s_branch .Ljtry\n"
".Lool:\n"
".Lsp0:\n"
"s_cmp_eq_u32 s98,0\n"
"s_cbranch_scc1 .Lnb0\n"
"s_sub_u32 s98,s98,1\n"
"s_sleep 2\n"
"buffer_load_dword v10,v12,s[40:43],s86 offen sc1\n"
"s_waitcnt vmcnt(0)\n"
"v_sub_u32 v10,v10,v11\n"
"s_nop 1\n"
"s_branch .Lnf0\n"
".Lsp1:\n"
"s_cmp_eq_u32 s98,0\n"
"s_cbranch_scc1 .Lnb1\n"
"s_sub_u32 s98,s98,1\n"
"s_sleep 2\n"
"buffer_load_dword v10,v12,s[40:43],s86 offen sc1\n"
"s_waitcnt vmcnt(0)\n"
"v_sub_u32 v10,v10,v11\n"
"s_nop 1\n"
"s_branch .Lnf1\n"
".Lsp2:\n"
"s_cmp_eq_u32 s98,0\n"
"s_cbranch_scc1 .Lnb2\n"
"s_sub_u32 s98,s98,1\n"
"s_sleep 2\n"
"buffer_load_dword v10,v12,s[40:43],s86 offen sc1\n"
"s_waitcnt vmcnt(0)\n"
"v_sub_u32 v10,v10,v11\n"
"s_nop 1\n"
"s_branch .Lnf2\n"
".Lsp3:\n"
"s_cmp_eq_u32 s98,0\n"
"s_cbranch_scc1 .Lnb3\n"
"s_sub_u32 s98,s98,1\n"
"s_sleep 2\n"
"buffer_load_dword v10,v12,s[40:43],s86 offen sc1\n"
"s_waitcnt vmcnt(0)\n"
"v_sub_u32 v10,v10,v11\n"
"s_nop 1\n"
"s_branch .Lnf3\n"
".Lsp4:\n"
"s_cmp_eq_u32 s98,0\n"
"s_cbranch_scc1 .Lnb4\n"
"s_sub_u32 s98,s98,1\n"
"s_sleep 2\n"
"buffer_load_dword v10,v12,s[40:43],s86 offen sc1\n"
"s_waitcnt vmcnt(0)\n"
"v_sub_u32 v10,v10,v11\n"
"s_nop 1\n"
"s_branch .Lnf4\n"
".Lsp5:\n"
"s_cmp_eq_u32 s98,0\n"
"s_cbranch_scc1 .Lnb5\n"
"s_sub_u32 s98,s98,1\n"
"s_sleep 2\n"
"buffer_load_dword v10,v12,s[40:43],s86 offen sc1\n"
"s_waitcnt vmcnt(0)\n"
"v_sub_u32 v10,v10,v11\n"
"s_nop 1\n"
"s_branch .Lnf5\n"
".Lsp6:\n"
"s_cmp_eq_u32 s98,0\n"
"s_cbranch_scc1 .Lnb6\n"
"s_sub_u32 s98,s98,1\n"
"s_sleep 2\n"
"buffer_load_dword v10,v12,s[40:43],s86 offen sc1\n"
"s_waitcnt vmcnt(0)\n"
"v_sub_u32 v10,v10,v11\n"
"s_nop 1\n"
"s_branch .Lnf6\n"
".Lsp7:\n"
"s_cmp_eq_u32 s98,0\n"
"s_cbranch_scc1 .Lnb7\n"
"s_sub_u32 s98,s98,1\n"
"s_sleep 2\n"
"buffer_load_dword v10,v12,s[40:43],s86 offen sc1\n"
"s_waitcnt vmcnt(0)\n"
"v_sub_u32 v10,v10,v11\n"
"s_nop 1\n"
"s_branch .Lnf7\n"
".Lsp8:\n"
"s_cmp_eq_u32 s98,0\n"
"s_cbranch_scc1 .Lnb8\n"
"s_sub_u32 s98,s98,1\n"
"s_sleep 2\n"
"buffer_load_dword v10,v12,s[40:43],s86 offen sc1\n"
"s_waitcnt vmcnt(0)\n"
"v_sub_u32 v10,v10,v11\n"
"s_nop 1\n"
"s_branch .Lnf8\n"
".Lsp9:\n"
"s_cmp_eq_u32 s98,0\n"
"s_cbranch_scc1 .Lnb9\n"
"s_sub_u32 s98,s98,1\n"
"s_sleep 2\n"
"buffer_load_dword v10,v12,s[40:43],s86 offen sc1\n"
"s_waitcnt vmcnt(0)\n"
"v_sub_u32 v10,v10,v11\n"
"s_nop 1\n"
"s_branch .Lnf9\n"
".Lpgqy_c:\n"
"s_cbranch_execz .Lpgqj_a\n"
".Lqy:\n"
MF "v[216:219],a[216:219],a[60:63],v[216:219]\n"
".Lpg30c:\n"
"s_cbranch_execz .Lpg3a\n"
TR "a[208:209],v0 offset:8\n"
MF "v[216:219],a[220:223],a[100:103],v[216:219]\n"
TR "a[210:211],v0 offset:520\n"
MF "v[224:227],a[220:223],a[104:107],v[224:227]\n"
MF "v[224:227],a[216:219],a[64:67],v[224:227]\n"
TR "a[212:213],v0 offset:4104\n"
MF "v[232:235],a[216:219],a[68:71],v[232:235]\n"
TR "a[214:215],v0 offset:4616\n"
MF "v[232:235],a[220:223],a[108:111],v[232:235]\n"
MF "v[240:243],a[220:223],a[112:115],v[240:243]\n"
MF "v[240:243],a[216:219],a[72:75],v[240:243]\n"
MF "v[248:251],a[216:219],a[76:79],v[248:251]\n"
MF "v[248:251],a[220:223],a[116:119],v[248:251]\n"
MF "v[248:251],a[200:203],a[156:159],v[248:251]\n"
TR "a[216:217],v0 offset:16392\n"
MF "v[248:251],a[204:207],a[196:199],v[248:251]\n"
TR "a[218:219],v0 offset:16904\n"
MF "v[240:243],a[204:207],a[192:195],v[240:243]\n"
MF "v[240:243],a[200:203],a[152:155],v[240:243]\n"
TR "a[220:221],v0 offset:20488\n"
MF "v[232:235],a[200:203],a[148:151],v[232:235]\n"
TR "a[222:223],v0 offset:21000\n"
MF "v[232:235],a[204:207],a[188:191],v[232:235]\n"
MF "v[224:227],a[204:207],a[184:187],v[224:227]\n"
MF "v[224:227],a[200:203],a[144:147],v[224:227]\n"
"v_fma_f32 v248,v248,s54,v14\n"
"v_fma_f32 v249,v249,s54,v14\n"
MF "v[216:219],a[200:203],a[140:143],v[216:219]\n"
"v_fma_f32 v250,v250,s54,v14\n"
"v_fma_f32 v251,v251,s54,v14\n"
MF "v[216:219],a[204:207],a[180:183],v[216:219]\n"
"s_waitcnt lgkmcnt(4)\n"
"v_fma_f32 v252,v252,s54,v14\n"
"v_fma_f32 v253,v253,s54,v14\n"
MF "v[180:183],a[208:211],a[60:63],v[180:183]\n"
TR "a[200:201],v0\n"
"v_fma_f32 v254,v254,s54,v14\n"
MF "v[180:183],a[212:215],a[100:103],v[180:183]\n"
TR "a[202:203],v0 offset:512\n"
"v_fma_f32 v255,v255,s54,v14\n"
MF "v[188:191],a[212:215],a[104:107],v[188:191]\n"
CV "v255,v254,v255\n"
CV "v254,v252,v253\n"
MF "v[188:191],a[208:211],a[64:67],v[188:191]\n"
TR "a[204:205],v0 offset:4096\n"
CV "v253,v250,v251\n"
MF "v[196:199],a[208:211],a[68:71],v[196:199]\n"
TR "a[206:207],v0 offset:4608\n"
CV "v252,v248,v249\n"
MF "v[196:199],a[212:215],a[108:111],v[196:199]\n"
"s_add_u32 s58,s75,4096\n"
"v_fma_f32 v240,v240,s54,v14\n"
"v_fma_f32 v241,v241,s54,v14\n"
MF "v[204:207],a[212:215],a[112:115],v[204:207]\n"
"buffer_store_dwordx4 v[252:255],v9,s[40:43],s58 offen offset:1024 sc1\n"
MF "v[204:207],a[208:211],a[72:75],v[204:207]\n"
"v_fma_f32 v242,v242,s54,v14\n"
"v_fma_f32 v243,v243,s54,v14\n"
MF "v[212:215],a[208:211],a[76:79],v[212:215]\n"
"v_fma_f32 v244,v244,s54,v14\n"
"v_fma_f32 v245,v245,s54,v14\n"
MF "v[212:215],a[212:215],a[116:119],v[212:215]\n"
"s_waitcnt lgkmcnt(4)\n"
"v_fma_f32 v246,v246,s54,v14\n"
"v_fma_f32 v247,v247,s54,v14\n"
MF "v[180:183],a[216:219],a[140:143],v[180:183]\n"
TR "a[208:209],v0 offset:16384\n"
CV "v251,v246,v247\n"
MF "v[180:183],a[220:223],a[180:183],v[180:183]\n"
TR "a[210:211],v0 offset:16896\n"
CV "v250,v244,v245\n"
MF "v[188:191],a[220:223],a[184:187],v[188:191]\n"
CV "v249,v242,v243\n"
CV "v248,v240,v241\n"
MF "v[188:191],a[216:219],a[144:147],v[188:191]\n"
TR "a[212:213],v0 offset:20480\n"
"v_fma_f32 v232,v232,s54,v14\n"
MF "v[196:199],a[216:219],a[148:151],v[196:199]\n"
TR "a[214:215],v0 offset:20992\n"
"v_fma_f32 v233,v233,s54,v14\n"
MF "v[196:199],a[220:223],a[188:191],v[196:199]\n"
"buffer_store_dwordx4 v[248:251],v9,s[40:43],s58 offen sc1\n"
MF "v[204:207],a[220:223],a[192:195],v[204:207]\n"
"v_fma_f32 v234,v234,s54,v14\n"
"v_fma_f32 v235,v235,s54,v14\n"
MF "v[204:207],a[216:219],a[152:155],v[204:207]\n"
"v_fma_f32 v236,v236,s54,v14\n"
"v_fma_f32 v237,v237,s54,v14\n"
MF "v[212:215],a[216:219],a[156:159],v[212:215]\n"
"v_fma_f32 v238,v238,s54,v14\n"
"v_fma_f32 v239,v239,s54,v14\n"
MF "v[212:215],a[220:223],a[196:199],v[212:215]\n"
"s_waitcnt vmcnt(10) lgkmcnt(0)\n"
"s_barrier\n"
MF "v[176:179],a[200:203],a[60:63],v[176:179]\n"
TR "a[216:217],v1 offset:32776\n"
CV "v247,v238,v239\n"
MF "v[176:179],a[204:207],a[100:103],v[176:179]\n"
TR "a[218:219],v1 offset:33288\n"
CV "v246,v236,v237\n"
MF "v[184:187],a[204:207],a[104:107],v[184:187]\n"
"s_add_u32 s66,s67,4718592\n"
"s_sub_u32 s81,s81,384\n"
"s_mov_b32 s83,7077888\n"
MF "v[184:187],a[200:203],a[64:67],v[184:187]\n"
"s_add_u32 m0,s62,0\n"
"s_add_u32 s82,s66,2359296\n"
TR "a[220:221],v1 offset:36872\n"
MF "v[192:195],a[200:203],a[68:71],v[192:195]\n"
"s_sub_u32 s15,s82,7864320\n"
"s_min_u32 s82,s82,s15\n"
TR "a[222:223],v1 offset:37384\n"
MF "v[192:195],a[204:207],a[108:111],v[192:195]\n"
LD "v6,s[28:31],s82 offen lds\n"
MF "v[200:203],a[204:207],a[112:115],v[200:203]\n"
CV "v245,v234,v235\n"
CV "v244,v232,v233\n"
MF "v[200:203],a[200:203],a[72:75],v[200:203]\n"
"v_fma_f32 v224,v224,s54,v14\n"
"v_fma_f32 v225,v225,s54,v14\n"
MF "v[208:211],a[200:203],a[76:79],v[208:211]\n"
"buffer_store_dwordx4 v[244:247],v9,s[40:43],s75 offen offset:3072 sc1\n"
MF "v[208:211],a[204:207],a[116:119],v[208:211]\n"
LD "v11,s[28:31],s82 offen offset:1024 lds\n"
MF "v[208:211],a[208:211],a[156:159],v[208:211]\n"
TR "a[200:201],v1 offset:49160\n"
"v_fma_f32 v226,v226,s54,v14\n"
MF "v[208:211],a[212:215],a[196:199],v[208:211]\n"
TR "a[202:203],v1 offset:49672\n"
"v_fma_f32 v227,v227,s54,v14\n"
MF "v[200:203],a[212:215],a[192:195],v[200:203]\n"
"v_fma_f32 v228,v228,s54,v14\n"
"v_fma_f32 v229,v229,s54,v14\n"
MF "v[200:203],a[208:211],a[152:155],v[200:203]\n"
TR "a[204:205],v1 offset:53256\n"
"v_fma_f32 v230,v230,s54,v14\n"
MF "v[192:195],a[208:211],a[148:151],v[192:195]\n"
TR "a[206:207],v1 offset:53768\n"
"v_fma_f32 v231,v231,s54,v14\n"
MF "v[192:195],a[212:215],a[188:191],v[192:195]\n"
CV "v243,v230,v231\n"
CV "v242,v228,v229\n"
MF "v[184:187],a[212:215],a[184:187],v[184:187]\n"
CV "v241,v226,v227\n"
CV "v240,v224,v225\n"
MF "v[184:187],a[208:211],a[144:147],v[184:187]\n"
"buffer_store_dwordx4 v[240:243],v9,s[40:43],s75 offen offset:2048 sc1\n"
MF "v[176:179],a[208:211],a[140:143],v[176:179]\n"
LD "v12,s[28:31],s82 offen offset:2048 lds\n"
MF "v[176:179],a[212:215],a[180:183],v[176:179]\n"
"s_waitcnt lgkmcnt(4)\n"
"v_fma_f32 v216,v216,s54,v14\n"
"v_fma_f32 v217,v217,s54,v14\n"
MF "v[140:143],a[216:219],a[60:63],v[140:143]\n"
TR "a[208:209],v1 offset:32768\n"
"v_fma_f32 v218,v218,s54,v14\n"
MF "v[140:143],a[220:223],a[100:103],v[140:143]\n"
TR "a[210:211],v1 offset:33280\n"
"v_fma_f32 v219,v219,s54,v14\n"
MF "v[148:151],a[220:223],a[104:107],v[148:151]\n"
"v_fma_f32 v220,v220,s54,v14\n"
"v_fma_f32 v221,v221,s54,v14\n"
MF "v[148:151],a[216:219],a[64:67],v[148:151]\n"
TR "a[212:213],v1 offset:36864\n"
"v_fma_f32 v222,v222,s54,v14\n"
MF "v[156:159],a[216:219],a[68:71],v[156:159]\n"
TR "a[214:215],v1 offset:37376\n"
"v_fma_f32 v223,v223,s54,v14\n"
MF "v[156:159],a[220:223],a[108:111],v[156:159]\n"
CV "v239,v222,v223\n"
CV "v238,v220,v221\n"
MF "v[164:167],a[220:223],a[112:115],v[164:167]\n"
CV "v237,v218,v219\n"
CV "v236,v216,v217\n"
MF "v[164:167],a[216:219],a[72:75],v[164:167]\n"
LD "v13,s[28:31],s82 offen offset:3072 lds\n"
MF "v[172:175],a[216:219],a[76:79],v[172:175]\n"
"buffer_store_dwordx4 v[236:239],v9,s[40:43],s75 offen offset:1024 sc1\n"
MF "v[172:175],a[220:223],a[116:119],v[172:175]\n"
"s_waitcnt lgkmcnt(4)\n"
"v_fma_f32 v208,v208,s54,v14\n"
"v_fma_f32 v209,v209,s54,v14\n"
MF "v[140:143],a[200:203],a[140:143],v[140:143]\n"
TR "a[216:217],v1 offset:49152\n"
"v_fma_f32 v210,v210,s54,v14\n"
MF "v[140:143],a[204:207],a[180:183],v[140:143]\n"
TR "a[218:219],v1 offset:49664\n"
"v_fma_f32 v211,v211,s54,v14\n"
MF "v[148:151],a[204:207],a[184:187],v[148:151]\n"
"v_fma_f32 v212,v212,s54,v14\n"
"v_fma_f32 v213,v213,s54,v14\n"
MF "v[148:151],a[200:203],a[144:147],v[148:151]\n"
TR "a[220:221],v1 offset:53248\n"
"v_fma_f32 v214,v214,s54,v14\n"
MF "v[156:159],a[200:203],a[148:151],v[156:159]\n"
TR "a[222:223],v1 offset:53760\n"
"v_fma_f32 v215,v215,s54,v14\n"
MF "v[156:159],a[204:207],a[188:191],v[156:159]\n"
CV "v235,v214,v215\n"
CV "v234,v212,v213\n"
MF "v[164:167],a[204:207],a[192:195],v[164:167]\n"
CV "v233,v210,v211\n"
CV "v232,v208,v209\n"
MF "v[164:167],a[200:203],a[152:155],v[164:167]\n"
"v_fma_f32 v200,v200,s54,v14\n"
"v_fma_f32 v201,v201,s54,v14\n"
MF "v[172:175],a[200:203],a[156:159],v[172:175]\n"
"buffer_store_dwordx4 v[232:235],v9,s[40:43],s75 offen sc1\n"
MF "v[172:175],a[204:207],a[196:199],v[172:175]\n"
"s_waitcnt lgkmcnt(0)\n"
"v_fma_f32 v202,v202,s54,v14\n"
"v_fma_f32 v203,v203,s54,v14\n"
MF "v[136:139],a[208:211],a[60:63],v[136:139]\n"
"s_add_u32 m0,s62,16384\n"
"s_add_u32 s82,s66,2359424\n"
TR "a[200:201],v0 offset:32776\n"
MF "v[136:139],a[212:215],a[100:103],v[136:139]\n"
"s_sub_u32 s15,s82,7864320\n"
"s_min_u32 s82,s82,s15\n"
TR "a[202:203],v0 offset:33288\n"
MF "v[144:147],a[212:215],a[104:107],v[144:147]\n"
LD "v6,s[28:31],s82 offen lds\n"
MF "v[144:147],a[208:211],a[64:67],v[144:147]\n"
TR "a[204:205],v0 offset:36872\n"
"v_fma_f32 v204,v204,s54,v14\n"
MF "v[152:155],a[208:211],a[68:71],v[152:155]\n"
TR "a[206:207],v0 offset:37384\n"
"v_fma_f32 v205,v205,s54,v14\n"
MF "v[152:155],a[212:215],a[108:111],v[152:155]\n"
"v_fma_f32 v206,v206,s54,v14\n"
"v_fma_f32 v207,v207,s54,v14\n"
MF "v[160:163],a[212:215],a[112:115],v[160:163]\n"
CV "v231,v206,v207\n"
CV "v230,v204,v205\n"
MF "v[160:163],a[208:211],a[72:75],v[160:163]\n"
CV "v229,v202,v203\n"
CV "v228,v200,v201\n"
MF "v[168:171],a[208:211],a[76:79],v[168:171]\n"
AW "a84,v228\n"
AW "a85,v229\n"
MF "v[168:171],a[212:215],a[116:119],v[168:171]\n"
LD "v11,s[28:31],s82 offen offset:1024 lds\n"
MF "v[168:171],a[216:219],a[156:159],v[168:171]\n"
TR "a[208:209],v0 offset:49160\n"
AW "a86,v230\n"
MF "v[168:171],a[220:223],a[196:199],v[168:171]\n"
TR "a[210:211],v0 offset:49672\n"
AW "a87,v231\n"
MF "v[160:163],a[220:223],a[192:195],v[160:163]\n"
"v_fma_f32 v192,v192,s54,v14\n"
"v_fma_f32 v193,v193,s54,v14\n"
MF "v[160:163],a[216:219],a[152:155],v[160:163]\n"
TR "a[212:213],v0 offset:53256\n"
"v_fma_f32 v194,v194,s54,v14\n"
MF "v[152:155],a[216:219],a[148:151],v[152:155]\n"
TR "a[214:215],v0 offset:53768\n"
"v_fma_f32 v195,v195,s54,v14\n"
MF "v[152:155],a[220:223],a[188:191],v[152:155]\n"
"v_fma_f32 v196,v196,s54,v14\n"
"v_fma_f32 v197,v197,s54,v14\n"
MF "v[144:147],a[220:223],a[184:187],v[144:147]\n"
"v_fma_f32 v198,v198,s54,v14\n"
"v_fma_f32 v199,v199,s54,v14\n"
MF "v[144:147],a[216:219],a[144:147],v[144:147]\n"
CV "v227,v198,v199\n"
CV "v226,v196,v197\n"
MF "v[136:139],a[216:219],a[140:143],v[136:139]\n"
LD "v12,s[28:31],s82 offen offset:2048 lds\n"
MF "v[136:139],a[220:223],a[180:183],v[136:139]\n"
"s_waitcnt lgkmcnt(4)\n"
CV "v225,v194,v195\n"
CV "v224,v192,v193\n"
MF "v[100:103],a[200:203],a[60:63],v[100:103]\n"
TR "a[216:217],v0 offset:32768\n"
AW "a80,v224\n"
MF "v[100:103],a[204:207],a[100:103],v[100:103]\n"
TR "a[218:219],v0 offset:33280\n"
AW "a81,v225\n"
MF "v[108:111],a[204:207],a[104:107],v[108:111]\n"
AW "a82,v226\n"
AW "a83,v227\n"
MF "v[108:111],a[200:203],a[64:67],v[108:111]\n"
TR "a[220:221],v0 offset:36864\n"
"v_fma_f32 v184,v184,s54,v14\n"
MF "v[116:119],a[200:203],a[68:71],v[116:119]\n"
TR "a[222:223],v0 offset:37376\n"
"v_fma_f32 v185,v185,s54,v14\n"
MF "v[116:119],a[204:207],a[108:111],v[116:119]\n"
"v_fma_f32 v186,v186,s54,v14\n"
"v_fma_f32 v187,v187,s54,v14\n"
MF "v[124:127],a[204:207],a[112:115],v[124:127]\n"
"v_fma_f32 v188,v188,s54,v14\n"
"v_fma_f32 v189,v189,s54,v14\n"
MF "v[124:127],a[200:203],a[72:75],v[124:127]\n"
LD "v13,s[28:31],s82 offen offset:3072 lds\n"
MF "v[132:135],a[200:203],a[76:79],v[132:135]\n"
"v_fma_f32 v190,v190,s54,v14\n"
"v_fma_f32 v191,v191,s54,v14\n"
MF "v[132:135],a[204:207],a[116:119],v[132:135]\n"
"s_waitcnt lgkmcnt(4)\n"
CV "v223,v190,v191\n"
CV "v222,v188,v189\n"
MF "v[100:103],a[208:211],a[140:143],v[100:103]\n"
TR "a[200:201],v0 offset:49152\n"
CV "v221,v186,v187\n"
MF "v[100:103],a[212:215],a[180:183],v[100:103]\n"
TR "a[202:203],v0 offset:49664\n"
CV "v220,v184,v185\n"
MF "v[108:111],a[212:215],a[184:187],v[108:111]\n"
AW "a56,v220\n"
AW "a57,v221\n"
MF "v[108:111],a[208:211],a[144:147],v[108:111]\n"
TR "a[204:205],v0 offset:53248\n"
AW "a58,v222\n"
MF "v[116:119],a[208:211],a[148:151],v[116:119]\n"
TR "a[206:207],v0 offset:53760\n"
AW "a59,v223\n"
MF "v[116:119],a[212:215],a[188:191],v[116:119]\n"
"v_fma_f32 v176,v176,s54,v14\n"
"v_fma_f32 v177,v177,s54,v14\n"
MF "v[124:127],a[212:215],a[192:195],v[124:127]\n"
"v_add_u32 v0,0xc000,v0\n"
"v_fma_f32 v178,v178,s54,v14\n"
MF "v[124:127],a[208:211],a[152:155],v[124:127]\n"
"v_add_u32 v1,0xc000,v1\n"
"v_fma_f32 v179,v179,s54,v14\n"
MF "v[132:135],a[208:211],a[156:159],v[132:135]\n"
"v_fma_f32 v180,v180,s54,v14\n"
"v_fma_f32 v181,v181,s54,v14\n"
MF "v[132:135],a[212:215],a[196:199],v[132:135]\n"
"s_waitcnt vmcnt(14) lgkmcnt(0)\n"
"s_barrier\n"
MF "v[96:99],a[216:219],a[60:63],v[96:99]\n"
"s_add_u32 m0,s62,32768\n"
"s_add_u32 s82,s66,2359552\n"
TR "a[208:209],v1 offset:16392\n"
MF "v[96:99],a[220:223],a[100:103],v[96:99]\n"
"s_sub_u32 s15,s82,7864320\n"
"s_min_u32 s82,s82,s15\n"
TR "a[210:211],v1 offset:16904\n"
MF "v[104:107],a[220:223],a[104:107],v[104:107]\n"
LD "v6,s[28:31],s82 offen lds\n"
MF "v[104:107],a[216:219],a[64:67],v[104:107]\n"
TR "a[212:213],v1 offset:20488\n"
"v_fma_f32 v182,v182,s54,v14\n"
MF "v[112:115],a[216:219],a[68:71],v[112:115]\n"
TR "a[214:215],v1 offset:21000\n"
"v_fma_f32 v183,v183,s54,v14\n"
MF "v[112:115],a[220:223],a[108:111],v[112:115]\n"
CV "v219,v182,v183\n"
CV "v218,v180,v181\n"
MF "v[120:123],a[220:223],a[112:115],v[120:123]\n"
CV "v217,v178,v179\n"
CV "v216,v176,v177\n"
MF "v[120:123],a[216:219],a[72:75],v[120:123]\n"
AW "a52,v216\n"
AW "a53,v217\n"
MF "v[128:131],a[216:219],a[76:79],v[128:131]\n"
AW "a54,v218\n"
AW "a55,v219\n"
MF "v[128:131],a[220:223],a[116:119],v[128:131]\n"
LD "v11,s[28:31],s82 offen offset:1024 lds\n"
MF "v[128:131],a[200:203],a[156:159],v[128:131]\n"
TR "a[216:217],v1 offset:32776\n"
"v_fma_f32 v168,v168,s54,v14\n"
MF "v[128:131],a[204:207],a[196:199],v[128:131]\n"
TR "a[218:219],v1 offset:33288\n"
"v_fma_f32 v169,v169,s54,v14\n"
MF "v[120:123],a[204:207],a[192:195],v[120:123]\n"
"v_fma_f32 v170,v170,s54,v14\n"
"v_fma_f32 v171,v171,s54,v14\n"
MF "v[120:123],a[200:203],a[152:155],v[120:123]\n"
TR "a[220:221],v1 offset:36872\n"
"v_fma_f32 v172,v172,s54,v14\n"
MF "v[112:115],a[200:203],a[148:151],v[112:115]\n"
TR "a[222:223],v1 offset:37384\n"
"v_fma_f32 v173,v173,s54,v14\n"
MF "v[112:115],a[204:207],a[188:191],v[112:115]\n"
"v_fma_f32 v174,v174,s54,v14\n"
"v_fma_f32 v175,v175,s54,v14\n"
MF "v[104:107],a[204:207],a[184:187],v[104:107]\n"
CV "v215,v174,v175\n"
CV "v214,v172,v173\n"
MF "v[104:107],a[200:203],a[144:147],v[104:107]\n"
CV "v213,v170,v171\n"
CV "v212,v168,v169\n"
MF "v[96:99],a[200:203],a[140:143],v[96:99]\n"
LD "v12,s[28:31],s82 offen offset:2048 lds\n"
MF "v[96:99],a[204:207],a[180:183],v[96:99]\n"
"s_waitcnt lgkmcnt(4)\n"
AW "a48,v212\n"
AW "a49,v213\n"
MF "v[60:63],a[208:211],a[60:63],v[60:63]\n"
TR "a[200:201],v1 offset:16384\n"
AW "a50,v214\n"
MF "v[60:63],a[212:215],a[100:103],v[60:63]\n"
TR "a[202:203],v1 offset:16896\n"
AW "a51,v215\n"
MF "v[68:71],a[212:215],a[104:107],v[68:71]\n"
"v_fma_f32 v160,v160,s54,v14\n"
"v_fma_f32 v161,v161,s54,v14\n"
MF "v[68:71],a[208:211],a[64:67],v[68:71]\n"
TR "a[204:205],v1 offset:20480\n"
"v_fma_f32 v162,v162,s54,v14\n"
MF "v[76:79],a[208:211],a[68:71],v[76:79]\n"
TR "a[206:207],v1 offset:20992\n"
"v_fma_f32 v163,v163,s54,v14\n"
MF "v[76:79],a[212:215],a[108:111],v[76:79]\n"
"v_fma_f32 v164,v164,s54,v14\n"
"v_fma_f32 v165,v165,s54,v14\n"
MF "v[84:87],a[212:215],a[112:115],v[84:87]\n"
"v_fma_f32 v166,v166,s54,v14\n"
"v_fma_f32 v167,v167,s54,v14\n"
MF "v[84:87],a[208:211],a[72:75],v[84:87]\n"
LD "v13,s[28:31],s82 offen offset:3072 lds\n"
MF "v[92:95],a[208:211],a[76:79],v[92:95]\n"
CV "v211,v166,v167\n"
CV "v210,v164,v165\n"
MF "v[92:95],a[212:215],a[116:119],v[92:95]\n"
"s_waitcnt lgkmcnt(4)\n"
CV "v209,v162,v163\n"
CV "v208,v160,v161\n"
MF "v[60:63],a[216:219],a[140:143],v[60:63]\n"
TR "a[208:209],v1 offset:32768\n"
AW "a44,v208\n"
MF "v[60:63],a[220:223],a[180:183],v[60:63]\n"
TR "a[210:211],v1 offset:33280\n"
AW "a45,v209\n"
MF "v[68:71],a[220:223],a[184:187],v[68:71]\n"
AW "a46,v210\n"
AW "a47,v211\n"
MF "v[68:71],a[216:219],a[144:147],v[68:71]\n"
TR "a[212:213],v1 offset:36864\n"
"v_fma_f32 v152,v152,s54,v14\n"
MF "v[76:79],a[216:219],a[148:151],v[76:79]\n"
TR "a[214:215],v1 offset:37376\n"
"v_fma_f32 v153,v153,s54,v14\n"
MF "v[76:79],a[220:223],a[188:191],v[76:79]\n"
"v_fma_f32 v154,v154,s54,v14\n"
"v_fma_f32 v155,v155,s54,v14\n"
MF "v[84:87],a[220:223],a[192:195],v[84:87]\n"
"v_fma_f32 v156,v156,s54,v14\n"
"v_fma_f32 v157,v157,s54,v14\n"
MF "v[84:87],a[216:219],a[152:155],v[84:87]\n"
"v_fma_f32 v158,v158,s54,v14\n"
"v_fma_f32 v159,v159,s54,v14\n"
MF "v[92:95],a[216:219],a[156:159],v[92:95]\n"
CV "v207,v158,v159\n"
CV "v206,v156,v157\n"
MF "v[92:95],a[220:223],a[196:199],v[92:95]\n"
"s_waitcnt lgkmcnt(0)\n"
CV "v205,v154,v155\n"
CV "v204,v152,v153\n"
MF "v[56:59],a[200:203],a[60:63],v[56:59]\n"
TR "a[216:217],v0 offset:16392\n"
AW "a40,v204\n"
MF "v[56:59],a[204:207],a[100:103],v[56:59]\n"
TR "a[218:219],v0 offset:16904\n"
AW "a41,v205\n"
MF "v[64:67],a[204:207],a[104:107],v[64:67]\n"
AW "a42,v206\n"
AW "a43,v207\n"
MF "v[64:67],a[200:203],a[64:67],v[64:67]\n"
TR "a[220:221],v0 offset:20488\n"
"v_fma_f32 v144,v144,s54,v14\n"
MF "v[72:75],a[200:203],a[68:71],v[72:75]\n"
TR "a[222:223],v0 offset:21000\n"
"v_fma_f32 v145,v145,s54,v14\n"
MF "v[72:75],a[204:207],a[108:111],v[72:75]\n"
"v_fma_f32 v146,v146,s54,v14\n"
"v_fma_f32 v147,v147,s54,v14\n"
MF "v[80:83],a[204:207],a[112:115],v[80:83]\n"
"v_fma_f32 v148,v148,s54,v14\n"
"v_fma_f32 v149,v149,s54,v14\n"
MF "v[80:83],a[200:203],a[72:75],v[80:83]\n"
"v_fma_f32 v150,v150,s54,v14\n"
"v_fma_f32 v151,v151,s54,v14\n"
MF "v[88:91],a[200:203],a[76:79],v[88:91]\n"
CV "v203,v150,v151\n"
CV "v202,v148,v149\n"
MF "v[88:91],a[204:207],a[116:119],v[88:91]\n"
CV "v201,v146,v147\n"
CV "v200,v144,v145\n"
MF "v[88:91],a[208:211],a[156:159],v[88:91]\n"
TR "a[200:201],v0 offset:32776\n"
AW "a16,v200\n"
MF "v[88:91],a[212:215],a[196:199],v[88:91]\n"
TR "a[202:203],v0 offset:33288\n"
AW "a17,v201\n"
MF "v[80:83],a[212:215],a[192:195],v[80:83]\n"
AW "a18,v202\n"
AW "a19,v203\n"
MF "v[80:83],a[208:211],a[152:155],v[80:83]\n"
TR "a[204:205],v0 offset:36872\n"
"v_fma_f32 v136,v136,s54,v14\n"
MF "v[72:75],a[208:211],a[148:151],v[72:75]\n"
TR "a[206:207],v0 offset:37384\n"
"v_fma_f32 v137,v137,s54,v14\n"
MF "v[72:75],a[212:215],a[188:191],v[72:75]\n"
"v_fma_f32 v138,v138,s54,v14\n"
"v_fma_f32 v139,v139,s54,v14\n"
MF "v[64:67],a[212:215],a[184:187],v[64:67]\n"
"v_fma_f32 v140,v140,s54,v14\n"
"v_fma_f32 v141,v141,s54,v14\n"
MF "v[64:67],a[208:211],a[144:147],v[64:67]\n"
"v_fma_f32 v142,v142,s54,v14\n"
"v_fma_f32 v143,v143,s54,v14\n"
MF "v[56:59],a[208:211],a[140:143],v[56:59]\n"
CV "v199,v142,v143\n"
CV "v198,v140,v141\n"
MF "v[56:59],a[212:215],a[180:183],v[56:59]\n"
"s_waitcnt lgkmcnt(4)\n"
CV "v197,v138,v139\n"
CV "v196,v136,v137\n"
MF "v[20:23],a[216:219],a[60:63],v[20:23]\n"
TR "a[208:209],v0 offset:16384\n"
AW "a12,v196\n"
MF "v[20:23],a[220:223],a[100:103],v[20:23]\n"
TR "a[210:211],v0 offset:16896\n"
AW "a13,v197\n"
MF "v[28:31],a[220:223],a[104:107],v[28:31]\n"
".Lpg31c:\n"
"s_cbranch_execz .Lpg2a\n"
AW "a14,v198\n"
AW "a15,v199\n"
MF "v[28:31],a[216:219],a[64:67],v[28:31]\n"
TR "a[212:213],v0 offset:20480\n"
"v_fma_f32 v128,v128,s54,v14\n"
MF "v[36:39],a[216:219],a[68:71],v[36:39]\n"
TR "a[214:215],v0 offset:20992\n"
"v_fma_f32 v129,v129,s54,v14\n"
MF "v[36:39],a[220:223],a[108:111],v[36:39]\n"
"v_fma_f32 v130,v130,s54,v14\n"
"v_fma_f32 v131,v131,s54,v14\n"
MF "v[44:47],a[220:223],a[112:115],v[44:47]\n"
"v_fma_f32 v132,v132,s54,v14\n"
"v_fma_f32 v133,v133,s54,v14\n"
MF "v[44:47],a[216:219],a[72:75],v[44:47]\n"
"v_fma_f32 v134,v134,s54,v14\n"
"v_fma_f32 v135,v135,s54,v14\n"
MF "v[52:55],a[216:219],a[76:79],v[52:55]\n"
CV "v195,v134,v135\n"
CV "v194,v132,v133\n"
MF "v[52:55],a[220:223],a[116:119],v[52:55]\n"
"s_waitcnt lgkmcnt(4)\n"
CV "v193,v130,v131\n"
CV "v192,v128,v129\n"
MF "v[20:23],a[200:203],a[140:143],v[20:23]\n"
TR "a[216:217],v0 offset:32768\n"
AW "a8,v192\n"
MF "v[20:23],a[204:207],a[180:183],v[20:23]\n"
TR "a[218:219],v0 offset:33280\n"
AW "a9,v193\n"
MF "v[28:31],a[204:207],a[184:187],v[28:31]\n"
AW "a10,v194\n"
AW "a11,v195\n"
MF "v[28:31],a[200:203],a[144:147],v[28:31]\n"
TR "a[220:221],v0 offset:36864\n"
"v_fma_f32 v120,v120,s54,v14\n"
MF "v[36:39],a[200:203],a[148:151],v[36:39]\n"
TR "a[222:223],v0 offset:37376\n"
"v_fma_f32 v121,v121,s54,v14\n"
MF "v[36:39],a[204:207],a[188:191],v[36:39]\n"
"v_fma_f32 v122,v122,s54,v14\n"
"v_fma_f32 v123,v123,s54,v14\n"
MF "v[44:47],a[204:207],a[192:195],v[44:47]\n"
"v_fma_f32 v124,v124,s54,v14\n"
"v_fma_f32 v125,v125,s54,v14\n"
MF "v[44:47],a[200:203],a[152:155],v[44:47]\n"
"v_fma_f32 v126,v126,s54,v14\n"
"v_fma_f32 v127,v127,s54,v14\n"
MF "v[52:55],a[200:203],a[156:159],v[52:55]\n"
CV "v191,v126,v127\n"
CV "v190,v124,v125\n"
MF "v[52:55],a[204:207],a[196:199],v[52:55]\n"
"s_waitcnt lgkmcnt(0)\n"
"s_barrier\n"
MF "v[16:19],a[208:211],a[60:63],v[16:19]\n"
CV "v189,v122,v123\n"
CV "v188,v120,v121\n"
MF "v[16:19],a[212:215],a[100:103],v[16:19]\n"
AW "a4,v188\n"
AW "a5,v189\n"
MF "v[24:27],a[212:215],a[104:107],v[24:27]\n"
AW "a6,v190\n"
AW "a7,v191\n"
MF "v[24:27],a[208:211],a[64:67],v[24:27]\n"
"v_fma_f32 v112,v112,s54,v14\n"
"v_fma_f32 v113,v113,s54,v14\n"
MF "v[32:35],a[208:211],a[68:71],v[32:35]\n"
"v_fma_f32 v114,v114,s54,v14\n"
"v_fma_f32 v115,v115,s54,v14\n"
MF "v[32:35],a[212:215],a[108:111],v[32:35]\n"
"v_fma_f32 v116,v116,s54,v14\n"
"v_fma_f32 v117,v117,s54,v14\n"
MF "v[40:43],a[212:215],a[112:115],v[40:43]\n"
"v_fma_f32 v118,v118,s54,v14\n"
"v_fma_f32 v119,v119,s54,v14\n"
MF "v[40:43],a[208:211],a[72:75],v[40:43]\n"
CV "v187,v118,v119\n"
CV "v186,v116,v117\n"
MF "v[48:51],a[208:211],a[76:79],v[48:51]\n"
CV "v185,v114,v115\n"
CV "v184,v112,v113\n"
MF "v[48:51],a[212:215],a[116:119],v[48:51]\n"
AW "a0,v184\n"
AW "a1,v185\n"
MF "v[48:51],a[216:219],a[156:159],v[48:51]\n"
AW "a2,v186\n"
AW "a3,v187\n"
MF "v[48:51],a[220:223],a[196:199],v[48:51]\n"
"v_fma_f32 v104,v104,s54,v14\n"
"v_fma_f32 v105,v105,s54,v14\n"
MF "v[40:43],a[220:223],a[192:195],v[40:43]\n"
"v_fma_f32 v106,v106,s54,v14\n"
"v_fma_f32 v107,v107,s54,v14\n"
MF "v[40:43],a[216:219],a[152:155],v[40:43]\n"
"v_fma_f32 v108,v108,s54,v14\n"
"v_fma_f32 v109,v109,s54,v14\n"
MF "v[32:35],a[216:219],a[148:151],v[32:35]\n"
"v_fma_f32 v110,v110,s54,v14\n"
"v_fma_f32 v111,v111,s54,v14\n"
MF "v[32:35],a[220:223],a[188:191],v[32:35]\n"
CV "v183,v110,v111\n"
CV "v182,v108,v109\n"
MF "v[24:27],a[220:223],a[184:187],v[24:27]\n"
CV "v181,v106,v107\n"
CV "v180,v104,v105\n"
MF "v[24:27],a[216:219],a[144:147],v[24:27]\n"
AW "a36,v180\n"
AW "a37,v181\n"
MF "v[16:19],a[216:219],a[140:143],v[16:19]\n"
AW "a38,v182\n"
AW "a39,v183\n"
MF "v[16:19],a[220:223],a[180:183],v[16:19]\n"
"s_mov_b32 s57,1\n"
"s_mov_b32 s69,99\n"
AR "v0,a240\n"
AR "v2,a241\n"
AR "v4,a242\n"
AR "v6,a243\n"
"v_xor_b32 v1,64,v0\n"
"v_xor_b32 v3,64,v2\n"
"v_xor_b32 v5,64,v4\n"
"v_add_u32 v11,97280,v6\n"
"v_add_u32 v12,194560,v6\n"
"v_add_u32 v13,291840,v6\n"
"s_memtime s[52:53]\n"
"s_memrealtime s[100:101]\n"
"s_waitcnt lgkmcnt(0)\n"
"s_sub_u32 s53,s52,s80\n"
"s_cmp_gt_u32 s53,30000\n"
"s_cselect_b32 s101,1,0\n"
"s_cmp_eq_u32 s87,2\n"
"s_cselect_b32 s87,s101,s87\n"
"s_cmp_eq_u32 s87,1\n"
"s_mov_b64 vcc,-1\n"
"s_waitcnt vmcnt(10) lgkmcnt(0)\n"
"s_barrier\n"
"s_nop 0\n"
TR "a[200:201],v0\n"
TR "a[202:203],v0 offset:512\n"
TR "a[204:205],v0 offset:4096\n"
TR "a[206:207],v0 offset:4608\n"
"ds_read_b128 a[60:63],v2\n"
"ds_read_b128 a[100:103],v3\n"
"ds_read_b128 a[64:67],v2 offset:2048\n"
"ds_read_b128 a[104:107],v3 offset:2048\n"
"ds_read_b128 a[68:71],v2 offset:4096\n"
"ds_read_b128 a[108:111],v3 offset:4096\n"
"ds_read_b128 a[72:75],v2 offset:6144\n"
"ds_read_b128 a[112:115],v3 offset:6144\n"
"ds_read_b128 a[76:79],v2 offset:8192\n"
"ds_read_b128 a[116:119],v3 offset:8192\n"
TR "a[208:209],v0 offset:8\n"
TR "a[210:211],v0 offset:520\n"
TR "a[212:213],v0 offset:4104\n"
TR "a[214:215],v0 offset:4616\n"
"v_pk_fma_f32 v[96:97],v[96:97],s[54:55],v[14:15] op_sel_hi:[1,1,0]\n"
"v_pk_fma_f32 v[98:99],v[98:99],s[54:55],v[14:15] op_sel_hi:[1,1,0]\n"
"v_pk_fma_f32 v[100:101],v[100:101],s[54:55],v[14:15] op_sel_hi:[1,1,0]\n"
"v_pk_fma_f32 v[102:103],v[102:103],s[54:55],v[14:15] op_sel_hi:[1,1,0]\n"
CV "v179,v102,v103\n"
CV "v178,v100,v101\n"
CV "v177,v98,v99\n"
CV "v176,v96,v97\n"
AW "a32,v176\n"
AW "a33,v177\n"
AW "a34,v178\n"
AW "a35,v179\n"
"v_pk_fma_f32 v[88:89],v[88:89],s[54:55],v[14:15] op_sel_hi:[1,1,0]\n"
"v_pk_fma_f32 v[90:91],v[90:91],s[54:55],v[14:15] op_sel_hi:[1,1,0]\n"
"v_pk_fma_f32 v[92:93],v[92:93],s[54:55],v[14:15] op_sel_hi:[1,1,0]\n"
"v_pk_fma_f32 v[94:95],v[94:95],s[54:55],v[14:15] op_sel_hi:[1,1,0]\n"
CV "v175,v94,v95\n"
CV "v174,v92,v93\n"
CV "v173,v90,v91\n"
CV "v172,v88,v89\n"
AW "a28,v172\n"
AW "a29,v173\n"
AW "a30,v174\n"
AW "a31,v175\n"
"v_pk_fma_f32 v[80:81],v[80:81],s[54:55],v[14:15] op_sel_hi:[1,1,0]\n"
"v_pk_fma_f32 v[82:83],v[82:83],s[54:55],v[14:15] op_sel_hi:[1,1,0]\n"
"v_pk_fma_f32 v[84:85],v[84:85],s[54:55],v[14:15] op_sel_hi:[1,1,0]\n"
"v_pk_fma_f32 v[86:87],v[86:87],s[54:55],v[14:15] op_sel_hi:[1,1,0]\n"
CV "v171,v86,v87\n"
CV "v170,v84,v85\n"
CV "v169,v82,v83\n"
CV "v168,v80,v81\n"
AW "a24,v168\n"
AW "a25,v169\n"
AW "a26,v170\n"
AW "a27,v171\n"
"v_pk_fma_f32 v[72:73],v[72:73],s[54:55],v[14:15] op_sel_hi:[1,1,0]\n"
"v_pk_fma_f32 v[74:75],v[74:75],s[54:55],v[14:15] op_sel_hi:[1,1,0]\n"
"v_pk_fma_f32 v[76:77],v[76:77],s[54:55],v[14:15] op_sel_hi:[1,1,0]\n"
"v_pk_fma_f32 v[78:79],v[78:79],s[54:55],v[14:15] op_sel_hi:[1,1,0]\n"
CV "v167,v78,v79\n"
CV "v166,v76,v77\n"
CV "v165,v74,v75\n"
CV "v164,v72,v73\n"
AW "a20,v164\n"
AW "a21,v165\n"
AW "a22,v166\n"
AW "a23,v167\n"
"v_pk_fma_f32 v[64:65],v[64:65],s[54:55],v[14:15] op_sel_hi:[1,1,0]\n"
"v_pk_fma_f32 v[66:67],v[66:67],s[54:55],v[14:15] op_sel_hi:[1,1,0]\n"
"v_pk_fma_f32 v[68:69],v[68:69],s[54:55],v[14:15] op_sel_hi:[1,1,0]\n"
"v_pk_fma_f32 v[70:71],v[70:71],s[54:55],v[14:15] op_sel_hi:[1,1,0]\n"
CV "v163,v70,v71\n"
CV "v162,v68,v69\n"
CV "v161,v66,v67\n"
CV "v160,v64,v65\n"
AW "a176,v160\n"
AW "a177,v161\n"
AW "a178,v162\n"
AW "a179,v163\n"
"v_pk_fma_f32 v[56:57],v[56:57],s[54:55],v[14:15] op_sel_hi:[1,1,0]\n"
"v_pk_fma_f32 v[58:59],v[58:59],s[54:55],v[14:15] op_sel_hi:[1,1,0]\n"
"v_pk_fma_f32 v[60:61],v[60:61],s[54:55],v[14:15] op_sel_hi:[1,1,0]\n"
"v_pk_fma_f32 v[62:63],v[62:63],s[54:55],v[14:15] op_sel_hi:[1,1,0]\n"
CV "v159,v62,v63\n"
CV "v158,v60,v61\n"
CV "v157,v58,v59\n"
CV "v156,v56,v57\n"
AW "a172,v156\n"
AW "a173,v157\n"
AW "a174,v158\n"
AW "a175,v159\n"
"v_pk_mul_f32 v[48:49],v[48:49],s[54:55]\n"
"v_pk_mul_f32 v[50:51],v[50:51],s[54:55]\n"
"v_pk_mul_f32 v[52:53],v[52:53],s[54:55]\n"
"v_pk_mul_f32 v[54:55],v[54:55],s[54:55]\n"
CV "v155,v54,v55\n"
CV "v154,v52,v53\n"
CV "v153,v50,v51\n"
CV "v152,v48,v49\n"
AW "a168,v152\n"
AW "a169,v153\n"
AW "a170,v154\n"
AW "a171,v155\n"
"v_pk_mul_f32 v[40:41],v[40:41],s[54:55]\n"
"v_pk_mul_f32 v[42:43],v[42:43],s[54:55]\n"
"v_pk_mul_f32 v[44:45],v[44:45],s[54:55]\n"
"v_pk_mul_f32 v[46:47],v[46:47],s[54:55]\n"
CV "v151,v46,v47\n"
CV "v150,v44,v45\n"
CV "v149,v42,v43\n"
CV "v148,v40,v41\n"
AW "a120,v148\n"
AW "a121,v149\n"
AW "a122,v150\n"
AW "a123,v151\n"
"v_pk_mul_f32 v[32:33],v[32:33],s[54:55]\n"
"v_pk_mul_f32 v[34:35],v[34:35],s[54:55]\n"
"v_pk_mul_f32 v[36:37],v[36:37],s[54:55]\n"
"v_pk_mul_f32 v[38:39],v[38:39],s[54:55]\n"
CV "v147,v38,v39\n"
CV "v146,v36,v37\n"
CV "v145,v34,v35\n"
CV "v144,v32,v33\n"
AW "a96,v144\n"
AW "a97,v145\n"
AW "a98,v146\n"
AW "a99,v147\n"
"v_pk_mul_f32 v[24:25],v[24:25],s[54:55]\n"
"v_pk_mul_f32 v[26:27],v[26:27],s[54:55]\n"
"v_pk_mul_f32 v[28:29],v[28:29],s[54:55]\n"
"v_pk_mul_f32 v[30:31],v[30:31],s[54:55]\n"
CV "v143,v30,v31\n"
CV "v142,v28,v29\n"
CV "v141,v26,v27\n"
CV "v140,v24,v25\n"
AW "a92,v140\n"
AW "a93,v141\n"
AW "a94,v142\n"
AW "a95,v143\n"
"v_pk_mul_f32 v[16:17],v[16:17],s[54:55]\n"
"v_pk_mul_f32 v[18:19],v[18:19],s[54:55]\n"
"v_pk_mul_f32 v[20:21],v[20:21],s[54:55]\n"
"v_pk_mul_f32 v[22:23],v[22:23],s[54:55]\n"
CV "v139,v22,v23\n"
CV "v138,v20,v21\n"
CV "v137,v18,v19\n"
CV "v136,v16,v17\n"
AW "a88,v136\n"
AW "a89,v137\n"
AW "a90,v138\n"
AW "a91,v139\n"
"s_waitcnt lgkmcnt(0)\n"
"s_branch .Lq0\n"
".Lpgpsl_b:\n"
"s_cbranch_execz .Lpg3a\n"
".Lpgpsl_d:\n"
"s_cbranch_execz .Lisr2\n"
".Lpsl:\n"
"s_sleep 1\n"
".Lpsub:\n"
"s_memrealtime s[100:101]\n"
"s_waitcnt lgkmcnt(0)\n"
"s_sub_u32 s101,s21,s100\n"
"s_cmp_gt_i32 s101,0\n"
"s_cbranch_scc1 .Lpsl\n"
"s_add_u32 s21,s100,s97\n"
"s_setpc_b64 s[52:53]\n"
".Lstub_6:\n"
"v_cmp_le_f32 vcc,0x47800000,v10\n"
"s_cmp_eq_u32 s61,1\n"
"s_cbranch_scc0 .Lhook_6\n"
"s_sub_u32 s58,s71,s70\n"
"s_add_u32 s72,s72,s58\n"
"s_add_u32 s16,s16,s58\n"
"s_add_u32 s17,s17,s58\n"
"s_cmp_eq_u32 s57,0\n"
"s_cselect_b32 s34,0,s34\n"
"s_cselect_b32 s64,163840,s64\n"
"s_mov_b64 exec,0\n"
"s_bitcmp1_b32 s23,12\n"
"s_cbranch_scc1 .Lisy3\n"
"s_cmp_eq_u32 s56,0\n"
"s_cbranch_scc1 .Lpgq0_f\n"
"s_cmp_eq_u32 s56,1\n"
"s_cbranch_scc1 .Lpg4f\n"
"s_cmp_eq_u32 s56,2\n"
"s_cbranch_scc1 .Lpg5f\n"
"s_branch .Lpg6f\n"
".Lisy3:\n"
"s_cmp_eq_u32 s56,0\n"
"s_cbranch_scc1 .Lpgep_g\n"
"s_cmp_eq_u32 s56,1\n"
"s_cbranch_scc1 .Lpg13g\n"
"s_cmp_eq_u32 s56,2\n"
"s_cbranch_scc1 .Lpg14g\n"
"s_branch .Lpgt0e_g\n"
".Lisr3:\n"
"s_mov_b64 exec,-1\n"
".Lisx3:\n"
".Lhook_6:\n"
"s_cmp_eq_u32 s77,0\n"
"s_cbranch_scc0 .Lnop_6\n"
"s_call_b64 s[52:53],.Lpsub\n"
".Lnop_6:\n"
"s_cbranch_vccz .Lback_6\n"
"s_call_b64 s[52:53],.Lslow_1\n"
"s_branch .Lback_6\n"
".Lstub_7:\n"
"v_cmp_le_f32 vcc,0x47800000,v10\n"
"s_cmp_eq_u32 s61,1\n"
"s_cbranch_scc0 .Lhook_7\n"
"s_sub_u32 s58,s71,s70\n"
"s_add_u32 s73,s73,s58\n"
"s_add_u32 s18,s18,s58\n"
"s_add_u32 s19,s19,s58\n"
"s_cmp_eq_u32 s57,0\n"
"s_cselect_b32 s38,0,s38\n"
"s_cselect_b32 s65,163840,s65\n"
".Lhook_7:\n"
"s_cmp_eq_u32 s77,0\n"
"s_cbranch_scc0 .Lnop_7\n"
"s_call_b64 s[52:53],.Lpsub\n"
"s_cmp_eq_u32 s61,4\n"
"s_cselect_b32 s77,0x47800000,s77\n"
"s_cselect_b32 s95,0x47800000,s95\n"
".Lnop_7:\n"
"s_cbranch_vccz .Lback_7\n"
"s_call_b64 s[52:53],.Lslow_0\n"
"s_branch .Lback_7\n"
".Lstub_8:\n"
"s_call_b64 s[52:53],.Lslow_1\n"
"s_branch .Lback_8\n"
".Lstub_11:\n"
"v_cmp_le_f32 vcc,0x47800000,v10\n"
"s_cmp_eq_u32 s77,0\n"
"s_cbranch_scc0 .Lnop_11\n"
"s_call_b64 s[52:53],.Lpsub\n"
".Lnop_11:\n"
"s_cbranch_vccz .Lback_11\n"
"s_call_b64 s[52:53],.Lslow_1\n"
"s_branch .Lback_11\n"
".Lstub_12:\n"
"v_cmp_le_f32 vcc,0x47800000,v10\n"
"s_cmp_eq_u32 s77,0\n"
"s_cbranch_scc0 .Lnop_12\n"
"s_call_b64 s[52:53],.Lpsub\n"
".Lnop_12:\n"
"s_cbranch_vccz .Lback_12\n"
"s_call_b64 s[52:53],.Lslow_0\n"
"s_branch .Lback_12\n"
".Lstub_13:\n"
"v_cmp_le_f32 vcc,0x47800000,v10\n"
"s_cmp_eq_u32 s61,1\n"
"s_cbranch_scc0 .Lhook_13\n"
"s_mov_b64 exec,0\n"
"s_cmp_eq_u32 s56,0\n"
"s_cbranch_scc1 .Lpg22h\n"
"s_cmp_eq_u32 s56,1\n"
"s_cbranch_scc1 .Lpg21h\n"
"s_cmp_eq_u32 s56,2\n"
"s_cbranch_scc1 .Lpg20h\n"
"s_branch .Lpg19h\n"
".Lisr4:\n"
"s_mov_b64 exec,-1\n"
".Lisx4:\n"
"s_mov_b32 s97,0\n"
".Lhook_13:\n"
"s_cmp_eq_u32 s77,0\n"
"s_cbranch_scc0 .Lnop_13\n"
"s_call_b64 s[52:53],.Lpsub\n"
".Lnop_13:\n"
"s_cbranch_vccz .Lback_13\n"
"s_call_b64 s[52:53],.Lslow_1\n"
"s_branch .Lback_13\n"
".Lstub_14:\n"
"v_cmp_le_f32 vcc,0x47800000,v10\n"
"s_cmp_eq_u32 s77,0\n"
"s_cbranch_scc0 .Lnop_14\n"
"s_call_b64 s[52:53],.Lpsub\n"
".Lnop_14:\n"
"s_cbranch_vccz .Lback_14\n"
"s_call_b64 s[52:53],.Lslow_0\n"
"s_branch .Lback_14\n"
".Lstub_15:\n"
"v_cmp_le_f32 vcc,0x47800000,v10\n"
"s_cmp_eq_u32 s61,1\n"
"s_cbranch_scc0 .Lhook_15\n"
"s_mov_b32 s34,0\n"
"s_mov_b32 s64,163840\n"
".Lhook_15:\n"
"s_cmp_eq_u32 s77,0\n"
"s_cbranch_scc0 .Lnop_15\n"
"s_call_b64 s[52:53],.Lpsub\n"
".Lnop_15:\n"
"s_cbranch_vccz .Lback_15\n"
"s_call_b64 s[52:53],.Lslow_1\n"
"s_branch .Lback_15\n"
".Lstub_16:\n"
"v_cmp_le_f32 vcc,0x47800000,v10\n"
"s_cmp_eq_u32 s77,0\n"
"s_cbranch_scc0 .Lnop_16\n"
"s_call_b64 s[52:53],.Lpsub\n"
".Lnop_16:\n"
"s_cbranch_vccz .Lback_16\n"
"s_call_b64 s[52:53],.Lslow_0\n"
"s_branch .Lback_16\n"
".Lstub_17:\n"
"s_call_b64 s[52:53],.Lslow_1\n"
"s_branch .Lback_17\n"
".Lslow_0:\n"
"s_nop 7\n"
"s_nop 7\n"
"v_and_b32 v10,240,v9\n"
"v_mul_u32_u24 v10,24,v10\n"
"v_lshrrev_b32 v11,4,v9\n"
"v_and_or_b32 v10,v11,48,v10\n"
"s_mov_b32 s59,s34\n"
"s_mov_b32 s34,s20\n"
"s_add_u32 s58,s74,0\n"
"s_nop 3\n"
LD "v[52:55],v10,s[32:35],s58 offen\n"
"s_waitcnt vmcnt(0)\n"
MF "v[72:75],v[52:55],v[136:139],v[32:35]\n"
MF "v[76:79],v[52:55],v[140:143],v[36:39]\n"
MF "v[80:83],v[52:55],v[144:147],v[40:43]\n"
MF "v[84:87],v[52:55],v[148:151],v[44:47]\n"
MF "v[88:91],v[52:55],v[152:155],v[48:51]\n"
"s_add_u32 s58,s74,0\n"
"s_nop 3\n"
LD "v[52:55],v10,s[32:35],s58 offen offset:64\n"
"s_waitcnt vmcnt(0)\n"
MF "v[72:75],v[52:55],v[156:159],v[72:75]\n"
MF "v[76:79],v[52:55],v[160:163],v[76:79]\n"
MF "v[80:83],v[52:55],v[164:167],v[80:83]\n"
MF "v[84:87],v[52:55],v[168:171],v[84:87]\n"
MF "v[88:91],v[52:55],v[172:175],v[88:91]\n"
"s_add_u32 s58,s74,0\n"
"s_nop 3\n"
LD "v[52:55],v10,s[32:35],s58 offen offset:128\n"
"s_waitcnt vmcnt(0)\n"
MF "v[72:75],v[52:55],v[176:179],v[72:75]\n"
MF "v[76:79],v[52:55],v[180:183],v[76:79]\n"
MF "v[80:83],v[52:55],v[184:187],v[80:83]\n"
MF "v[84:87],v[52:55],v[188:191],v[84:87]\n"
MF "v[88:91],v[52:55],v[192:195],v[88:91]\n"
"s_add_u32 s58,s74,0\n"
"s_nop 3\n"
LD "v[52:55],v10,s[32:35],s58 offen offset:192\n"
"s_waitcnt vmcnt(0)\n"
MF "v[72:75],v[52:55],v[196:199],v[72:75]\n"
MF "v[76:79],v[52:55],v[200:203],v[76:79]\n"
MF "v[80:83],v[52:55],v[204:207],v[80:83]\n"
MF "v[84:87],v[52:55],v[208:211],v[84:87]\n"
MF "v[88:91],v[52:55],v[212:215],v[88:91]\n"
"s_add_u32 s58,s74,0\n"
"s_nop 3\n"
LD "v[52:55],v10,s[32:35],s58 offen offset:256\n"
"s_waitcnt vmcnt(0)\n"
MF "v[72:75],v[52:55],v[216:219],v[72:75]\n"
MF "v[76:79],v[52:55],v[220:223],v[76:79]\n"
MF "v[80:83],v[52:55],v[224:227],v[80:83]\n"
MF "v[84:87],v[52:55],v[228:231],v[84:87]\n"
MF "v[88:91],v[52:55],v[232:235],v[88:91]\n"
"s_add_u32 s58,s74,0\n"
"s_nop 3\n"
LD "v[52:55],v10,s[32:35],s58 offen offset:320\n"
"s_waitcnt vmcnt(0)\n"
MF "v[72:75],v[52:55],v[236:239],v[72:75]\n"
MF "v[76:79],v[52:55],v[240:243],v[76:79]\n"
MF "v[80:83],v[52:55],v[244:247],v[80:83]\n"
MF "v[84:87],v[52:55],v[248:251],v[84:87]\n"
MF "v[88:91],v[52:55],v[252:255],v[88:91]\n"
"s_add_u32 s58,s74,6144\n"
"s_nop 3\n"
LD "v[52:55],v10,s[32:35],s58 offen\n"
"s_waitcnt vmcnt(0)\n"
MF "v[112:115],v[52:55],v[136:139],v[32:35]\n"
MF "v[116:119],v[52:55],v[140:143],v[36:39]\n"
MF "v[120:123],v[52:55],v[144:147],v[40:43]\n"
MF "v[124:127],v[52:55],v[148:151],v[44:47]\n"
MF "v[128:131],v[52:55],v[152:155],v[48:51]\n"
"s_add_u32 s58,s74,6144\n"
"s_nop 3\n"
LD "v[52:55],v10,s[32:35],s58 offen offset:64\n"
"s_waitcnt vmcnt(0)\n"
MF "v[112:115],v[52:55],v[156:159],v[112:115]\n"
MF "v[116:119],v[52:55],v[160:163],v[116:119]\n"
MF "v[120:123],v[52:55],v[164:167],v[120:123]\n"
MF "v[124:127],v[52:55],v[168:171],v[124:127]\n"
MF "v[128:131],v[52:55],v[172:175],v[128:131]\n"
"s_add_u32 s58,s74,6144\n"
"s_nop 3\n"
LD "v[52:55],v10,s[32:35],s58 offen offset:128\n"
"s_waitcnt vmcnt(0)\n"
MF "v[112:115],v[52:55],v[176:179],v[112:115]\n"
MF "v[116:119],v[52:55],v[180:183],v[116:119]\n"
MF "v[120:123],v[52:55],v[184:187],v[120:123]\n"
MF "v[124:127],v[52:55],v[188:191],v[124:127]\n"
MF "v[128:131],v[52:55],v[192:195],v[128:131]\n"
"s_add_u32 s58,s74,6144\n"
"s_nop 3\n"
LD "v[52:55],v10,s[32:35],s58 offen offset:192\n"
"s_waitcnt vmcnt(0)\n"
MF "v[112:115],v[52:55],v[196:199],v[112:115]\n"
MF "v[116:119],v[52:55],v[200:203],v[116:119]\n"
MF "v[120:123],v[52:55],v[204:207],v[120:123]\n"
MF "v[124:127],v[52:55],v[208:211],v[124:127]\n"
MF "v[128:131],v[52:55],v[212:215],v[128:131]\n"
"s_add_u32 s58,s74,6144\n"
"s_nop 3\n"
LD "v[52:55],v10,s[32:35],s58 offen offset:256\n"
"s_waitcnt vmcnt(0)\n"
MF "v[112:115],v[52:55],v[216:219],v[112:115]\n"
MF "v[116:119],v[52:55],v[220:223],v[116:119]\n"
MF "v[120:123],v[52:55],v[224:227],v[120:123]\n"
MF "v[124:127],v[52:55],v[228:231],v[124:127]\n"
MF "v[128:131],v[52:55],v[232:235],v[128:131]\n"
"s_add_u32 s58,s74,6144\n"
"s_nop 3\n"
LD "v[52:55],v10,s[32:35],s58 offen offset:320\n"
"s_waitcnt vmcnt(0)\n"
MF "v[112:115],v[52:55],v[236:239],v[112:115]\n"
MF "v[116:119],v[52:55],v[240:243],v[116:119]\n"
MF "v[120:123],v[52:55],v[244:247],v[120:123]\n"
MF "v[124:127],v[52:55],v[248:251],v[124:127]\n"
MF "v[128:131],v[52:55],v[252:255],v[128:131]\n"
"s_nop 7\n"
"s_mov_b32 s34,s59\n"
"v_max3_f32 v10,v72,v73,v74\n"
"s_nop 0\n"
"v_max3_f32 v11,v75,v112,v113\n"
"v_max3_f32 v10,v10,v114,v115\n"
"v_max_f32 v10,v10,v11\n"
"v_mov_b32 v11,v10\n"
"s_nop 1\n"
"v_permlane16_swap_b32 v10,v11\n"
"v_max_f32 v10,v10,v11\n"
"v_mov_b32 v11,v10\n"
"s_nop 1\n"
"v_permlane32_swap_b32 v10,v11\n"
"v_max_f32 v10,v10,v11\n"
"v_add_f32 v10,0x41300000,v10\n"
"v_max_f32 v10,0,v10\n"
"v_exp_f32 v11,-v10\n"
AR "v12,a0\n"
"v_subrev_f32 v12,s13,v12\n"
"v_fma_f32 v12,v12,v11,s13\n"
AW "a0,v12\n"
AR "v12,a20\n"
"v_subrev_f32 v12,s13,v12\n"
"v_fma_f32 v12,v12,v11,s13\n"
AW "a20,v12\n"
AR "v12,a40\n"
"v_subrev_f32 v12,s13,v12\n"
"v_fma_f32 v12,v12,v11,s13\n"
AW "a40,v12\n"
AR "v12,a60\n"
"v_subrev_f32 v12,s13,v12\n"
"v_fma_f32 v12,v12,v11,s13\n"
AW "a60,v12\n"
AR "v12,a80\n"
"v_subrev_f32 v12,s13,v12\n"
"v_fma_f32 v12,v12,v11,s13\n"
AW "a80,v12\n"
AR "v12,a100\n"
"v_subrev_f32 v12,s13,v12\n"
"v_fma_f32 v12,v12,v11,s13\n"
AW "a100,v12\n"
AR "v12,a120\n"
"v_subrev_f32 v12,s13,v12\n"
"v_fma_f32 v12,v12,v11,s13\n"
AW "a120,v12\n"
AR "v12,a140\n"
"v_subrev_f32 v12,s13,v12\n"
"v_fma_f32 v12,v12,v11,s13\n"
AW "a140,v12\n"
AR "v12,a160\n"
"v_subrev_f32 v12,s13,v12\n"
"v_fma_f32 v12,v12,v11,s13\n"
AW "a160,v12\n"
AR "v12,a180\n"
"v_subrev_f32 v12,s13,v12\n"
"v_fma_f32 v12,v12,v11,s13\n"
AW "a180,v12\n"
AR "v12,a200\n"
"v_subrev_f32 v12,s13,v12\n"
"v_fma_f32 v12,v12,v11,s13\n"
AW "a200,v12\n"
AR "v12,a220\n"
"v_subrev_f32 v12,s13,v12\n"
"v_fma_f32 v12,v12,v11,s13\n"
AW "a220,v12\n"
AR "v12,a1\n"
"v_subrev_f32 v12,s13,v12\n"
"v_fma_f32 v12,v12,v11,s13\n"
AW "a1,v12\n"
AR "v12,a21\n"
"v_subrev_f32 v12,s13,v12\n"
"v_fma_f32 v12,v12,v11,s13\n"
AW "a21,v12\n"
AR "v12,a41\n"
"v_subrev_f32 v12,s13,v12\n"
"v_fma_f32 v12,v12,v11,s13\n"
AW "a41,v12\n"
AR "v12,a61\n"
"v_subrev_f32 v12,s13,v12\n"
"v_fma_f32 v12,v12,v11,s13\n"
AW "a61,v12\n"
AR "v12,a81\n"
"v_subrev_f32 v12,s13,v12\n"
"v_fma_f32 v12,v12,v11,s13\n"
AW "a81,v12\n"
AR "v12,a101\n"
"v_subrev_f32 v12,s13,v12\n"
"v_fma_f32 v12,v12,v11,s13\n"
AW "a101,v12\n"
AR "v12,a121\n"
"v_subrev_f32 v12,s13,v12\n"
"v_fma_f32 v12,v12,v11,s13\n"
AW "a121,v12\n"
AR "v12,a141\n"
"v_subrev_f32 v12,s13,v12\n"
"v_fma_f32 v12,v12,v11,s13\n"
AW "a141,v12\n"
AR "v12,a161\n"
"v_subrev_f32 v12,s13,v12\n"
"v_fma_f32 v12,v12,v11,s13\n"
AW "a161,v12\n"
AR "v12,a181\n"
"v_subrev_f32 v12,s13,v12\n"
"v_fma_f32 v12,v12,v11,s13\n"
AW "a181,v12\n"
AR "v12,a201\n"
"v_subrev_f32 v12,s13,v12\n"
"v_fma_f32 v12,v12,v11,s13\n"
AW "a201,v12\n"
AR "v12,a221\n"
"v_subrev_f32 v12,s13,v12\n"
"v_fma_f32 v12,v12,v11,s13\n"
AW "a221,v12\n"
AR "v12,a2\n"
"v_subrev_f32 v12,s13,v12\n"
"v_fma_f32 v12,v12,v11,s13\n"
AW "a2,v12\n"
AR "v12,a22\n"
"v_subrev_f32 v12,s13,v12\n"
"v_fma_f32 v12,v12,v11,s13\n"
AW "a22,v12\n"
AR "v12,a42\n"
"v_subrev_f32 v12,s13,v12\n"
"v_fma_f32 v12,v12,v11,s13\n"
AW "a42,v12\n"
AR "v12,a62\n"
"v_subrev_f32 v12,s13,v12\n"
"v_fma_f32 v12,v12,v11,s13\n"
AW "a62,v12\n"
AR "v12,a82\n"
"v_subrev_f32 v12,s13,v12\n"
"v_fma_f32 v12,v12,v11,s13\n"
AW "a82,v12\n"
AR "v12,a102\n"
"v_subrev_f32 v12,s13,v12\n"
"v_fma_f32 v12,v12,v11,s13\n"
AW "a102,v12\n"
AR "v12,a122\n"
"v_subrev_f32 v12,s13,v12\n"
"v_fma_f32 v12,v12,v11,s13\n"
AW "a122,v12\n"
AR "v12,a142\n"
"v_subrev_f32 v12,s13,v12\n"
"v_fma_f32 v12,v12,v11,s13\n"
AW "a142,v12\n"
AR "v12,a162\n"
"v_subrev_f32 v12,s13,v12\n"
"v_fma_f32 v12,v12,v11,s13\n"
AW "a162,v12\n"
AR "v12,a182\n"
"v_subrev_f32 v12,s13,v12\n"
"v_fma_f32 v12,v12,v11,s13\n"
AW "a182,v12\n"
AR "v12,a202\n"
"v_subrev_f32 v12,s13,v12\n"
"v_fma_f32 v12,v12,v11,s13\n"
AW "a202,v12\n"
AR "v12,a222\n"
"v_subrev_f32 v12,s13,v12\n"
"v_fma_f32 v12,v12,v11,s13\n"
AW "a222,v12\n"
AR "v12,a3\n"
"v_subrev_f32 v12,s13,v12\n"
"v_fma_f32 v12,v12,v11,s13\n"
AW "a3,v12\n"
AR "v12,a23\n"
"v_subrev_f32 v12,s13,v12\n"
"v_fma_f32 v12,v12,v11,s13\n"
AW "a23,v12\n"
AR "v12,a43\n"
"v_subrev_f32 v12,s13,v12\n"
"v_fma_f32 v12,v12,v11,s13\n"
AW "a43,v12\n"
AR "v12,a63\n"
"v_subrev_f32 v12,s13,v12\n"
"v_fma_f32 v12,v12,v11,s13\n"
AW "a63,v12\n"
AR "v12,a83\n"
"v_subrev_f32 v12,s13,v12\n"
"v_fma_f32 v12,v12,v11,s13\n"
AW "a83,v12\n"
AR "v12,a103\n"
"v_subrev_f32 v12,s13,v12\n"
"v_fma_f32 v12,v12,v11,s13\n"
AW "a103,v12\n"
AR "v12,a123\n"
"v_subrev_f32 v12,s13,v12\n"
"v_fma_f32 v12,v12,v11,s13\n"
AW "a123,v12\n"
AR "v12,a143\n"
"v_subrev_f32 v12,s13,v12\n"
"v_fma_f32 v12,v12,v11,s13\n"
AW "a143,v12\n"
AR "v12,a163\n"
"v_subrev_f32 v12,s13,v12\n"
"v_fma_f32 v12,v12,v11,s13\n"
AW "a163,v12\n"
AR "v12,a183\n"
"v_subrev_f32 v12,s13,v12\n"
"v_fma_f32 v12,v12,v11,s13\n"
AW "a183,v12\n"
AR "v12,a203\n"
"v_subrev_f32 v12,s13,v12\n"
"v_fma_f32 v12,v12,v11,s13\n"
AW "a203,v12\n"
AR "v12,a223\n"
"v_subrev_f32 v12,s13,v12\n"
"v_fma_f32 v12,v12,v11,s13\n"
AW "a223,v12\n"
"v_mul_f32 v52,v57,v11\n"
"v_sub_f32 v32,v32,v10\n"
"v_sub_f32 v33,v33,v10\n"
"v_sub_f32 v34,v34,v10\n"
"v_sub_f32 v35,v35,v10\n"
"v_sub_f32 v92,v92,v10\n"
"v_sub_f32 v93,v93,v10\n"
"v_sub_f32 v94,v94,v10\n"
"v_sub_f32 v95,v95,v10\n"
"v_sub_f32 v72,v72,v10\n"
"v_sub_f32 v73,v73,v10\n"
"v_sub_f32 v74,v74,v10\n"
"v_sub_f32 v75,v75,v10\n"
"v_sub_f32 v112,v112,v10\n"
"v_sub_f32 v113,v113,v10\n"
"v_sub_f32 v114,v114,v10\n"
"v_sub_f32 v115,v115,v10\n"
"v_exp_f32 v72,v72\n"
"v_exp_f32 v73,v73\n"
"v_exp_f32 v74,v74\n"
"v_exp_f32 v75,v75\n"
"v_exp_f32 v112,v112\n"
"v_exp_f32 v113,v113\n"
"v_exp_f32 v114,v114\n"
"v_exp_f32 v115,v115\n"
"v_add_f32 v52,v52,v72\n"
"v_add_f32 v52,v52,v73\n"
"v_add_f32 v52,v52,v74\n"
"v_add_f32 v52,v52,v75\n"
"v_add_f32 v52,v52,v112\n"
"v_add_f32 v52,v52,v113\n"
"v_add_f32 v52,v52,v114\n"
"v_add_f32_e64 v52,v52,v115\n"
CV "v72,v72,v73\n"
CV "v73,v74,v75\n"
CV "v74,v112,v113\n"
CV "v75,v114,v115\n"
"v_max3_f32 v10,v76,v77,v78\n"
"v_max3_f32 v11,v79,v116,v117\n"
"v_max3_f32 v10,v10,v118,v119\n"
"v_max_f32 v10,v10,v11\n"
"v_mov_b32 v11,v10\n"
"s_nop 1\n"
"v_permlane16_swap_b32 v10,v11\n"
"v_max_f32 v10,v10,v11\n"
"v_mov_b32 v11,v10\n"
"s_nop 1\n"
"v_permlane32_swap_b32 v10,v11\n"
"v_max_f32_e64 v10,v10,v11\n"
"v_add_f32 v10,0x41300000,v10\n"
"v_max_f32 v10,0,v10\n"
"v_exp_f32 v11,-v10\n"
AR "v12,a4\n"
"v_subrev_f32 v12,s13,v12\n"
"v_fma_f32 v12,v12,v11,s13\n"
AW "a4,v12\n"
AR "v12,a24\n"
"v_subrev_f32 v12,s13,v12\n"
"v_fma_f32 v12,v12,v11,s13\n"
AW "a24,v12\n"
AR "v12,a44\n"
"v_subrev_f32 v12,s13,v12\n"
"v_fma_f32 v12,v12,v11,s13\n"
AW "a44,v12\n"
AR "v12,a64\n"
"v_subrev_f32 v12,s13,v12\n"
"v_fma_f32 v12,v12,v11,s13\n"
AW "a64,v12\n"
AR "v12,a84\n"
"v_subrev_f32 v12,s13,v12\n"
"v_fma_f32 v12,v12,v11,s13\n"
AW "a84,v12\n"
AR "v12,a104\n"
"v_subrev_f32 v12,s13,v12\n"
"v_fma_f32 v12,v12,v11,s13\n"
AW "a104,v12\n"
AR "v12,a124\n"
"v_subrev_f32 v12,s13,v12\n"
"v_fma_f32 v12,v12,v11,s13\n"
AW "a124,v12\n"
AR "v12,a144\n"
"v_subrev_f32 v12,s13,v12\n"
"v_fma_f32 v12,v12,v11,s13\n"
AW "a144,v12\n"
AR "v12,a164\n"
"v_subrev_f32 v12,s13,v12\n"
"v_fma_f32 v12,v12,v11,s13\n"
AW "a164,v12\n"
AR "v12,a184\n"
"v_subrev_f32 v12,s13,v12\n"
"v_fma_f32 v12,v12,v11,s13\n"
AW "a184,v12\n"
AR "v12,a204\n"
"v_subrev_f32 v12,s13,v12\n"
"v_fma_f32 v12,v12,v11,s13\n"
AW "a204,v12\n"
AR "v12,a224\n"
"v_subrev_f32 v12,s13,v12\n"
"v_fma_f32 v12,v12,v11,s13\n"
AW "a224,v12\n"
AR "v12,a5\n"
"v_subrev_f32 v12,s13,v12\n"
"v_fma_f32 v12,v12,v11,s13\n"
AW "a5,v12\n"
AR "v12,a25\n"
"v_subrev_f32 v12,s13,v12\n"
"v_fma_f32 v12,v12,v11,s13\n"
AW "a25,v12\n"
AR "v12,a45\n"
"v_subrev_f32 v12,s13,v12\n"
"v_fma_f32 v12,v12,v11,s13\n"
AW "a45,v12\n"
AR "v12,a65\n"
"v_subrev_f32 v12,s13,v12\n"
"v_fma_f32 v12,v12,v11,s13\n"
AW "a65,v12\n"
AR "v12,a85\n"
"v_subrev_f32 v12,s13,v12\n"
"v_fma_f32 v12,v12,v11,s13\n"
AW "a85,v12\n"
AR "v12,a105\n"
"v_subrev_f32 v12,s13,v12\n"
"v_fma_f32 v12,v12,v11,s13\n"
AW "a105,v12\n"
AR "v12,a125\n"
"v_subrev_f32 v12,s13,v12\n"
"v_fma_f32 v12,v12,v11,s13\n"
AW "a125,v12\n"
AR "v12,a145\n"
"v_subrev_f32 v12,s13,v12\n"
"v_fma_f32 v12,v12,v11,s13\n"
AW "a145,v12\n"
AR "v12,a165\n"
"v_subrev_f32 v12,s13,v12\n"
"v_fma_f32 v12,v12,v11,s13\n"
AW "a165,v12\n"
AR "v12,a185\n"
"v_subrev_f32 v12,s13,v12\n"
"v_fma_f32 v12,v12,v11,s13\n"
AW "a185,v12\n"
AR "v12,a205\n"
"v_subrev_f32 v12,s13,v12\n"
"v_fma_f32 v12,v12,v11,s13\n"
AW "a205,v12\n"
AR "v12,a225\n"
"v_subrev_f32 v12,s13,v12\n"
"v_fma_f32 v12,v12,v11,s13\n"
AW "a225,v12\n"
AR "v12,a6\n"
"v_subrev_f32 v12,s13,v12\n"
"v_fma_f32 v12,v12,v11,s13\n"
AW "a6,v12\n"
AR "v12,a26\n"
"v_subrev_f32 v12,s13,v12\n"
"v_fma_f32 v12,v12,v11,s13\n"
AW "a26,v12\n"
AR "v12,a46\n"
"v_subrev_f32 v12,s13,v12\n"
"v_fma_f32 v12,v12,v11,s13\n"
AW "a46,v12\n"
AR "v12,a66\n"
"v_subrev_f32 v12,s13,v12\n"
"v_fma_f32 v12,v12,v11,s13\n"
AW "a66,v12\n"
AR "v12,a86\n"
"v_subrev_f32 v12,s13,v12\n"
"v_fma_f32 v12,v12,v11,s13\n"
AW "a86,v12\n"
AR "v12,a106\n"
"v_subrev_f32 v12,s13,v12\n"
"v_fma_f32 v12,v12,v11,s13\n"
AW "a106,v12\n"
AR "v12,a126\n"
"v_subrev_f32 v12,s13,v12\n"
"v_fma_f32 v12,v12,v11,s13\n"
AW "a126,v12\n"
AR "v12,a146\n"
"v_subrev_f32 v12,s13,v12\n"
"v_fma_f32 v12,v12,v11,s13\n"
AW "a146,v12\n"
AR "v12,a166\n"
"v_subrev_f32 v12,s13,v12\n"
"v_fma_f32 v12,v12,v11,s13\n"
AW "a166,v12\n"
AR "v12,a186\n"
"v_subrev_f32 v12,s13,v12\n"
"v_fma_f32 v12,v12,v11,s13\n"
AW "a186,v12\n"
AR "v12,a206\n"
"v_subrev_f32 v12,s13,v12\n"
"v_fma_f32 v12,v12,v11,s13\n"
AW "a206,v12\n"
AR "v12,a226\n"
"v_subrev_f32 v12,s13,v12\n"
"v_fma_f32 v12,v12,v11,s13\n"
AW "a226,v12\n"
AR "v12,a7\n"
"v_subrev_f32 v12,s13,v12\n"
"v_fma_f32 v12,v12,v11,s13\n"
AW "a7,v12\n"
AR "v12,a27\n"
"v_subrev_f32 v12,s13,v12\n"
"v_fma_f32 v12,v12,v11,s13\n"
AW "a27,v12\n"
AR "v12,a47\n"
"v_subrev_f32 v12,s13,v12\n"
"v_fma_f32 v12,v12,v11,s13\n"
AW "a47,v12\n"
AR "v12,a67\n"
"v_subrev_f32 v12,s13,v12\n"
"v_fma_f32 v12,v12,v11,s13\n"
AW "a67,v12\n"
AR "v12,a87\n"
"v_subrev_f32 v12,s13,v12\n"
"v_fma_f32 v12,v12,v11,s13\n"
AW "a87,v12\n"
AR "v12,a107\n"
"v_subrev_f32 v12,s13,v12\n"
"v_fma_f32 v12,v12,v11,s13\n"
AW "a107,v12\n"
AR "v12,a127\n"
"v_subrev_f32 v12,s13,v12\n"
"v_fma_f32 v12,v12,v11,s13\n"
AW "a127,v12\n"
AR "v12,a147\n"
"v_subrev_f32 v12,s13,v12\n"
"v_fma_f32 v12,v12,v11,s13\n"
AW "a147,v12\n"
AR "v12,a167\n"
"v_subrev_f32 v12,s13,v12\n"
"v_fma_f32 v12,v12,v11,s13\n"
AW "a167,v12\n"
AR "v12,a187\n"
"v_subrev_f32 v12,s13,v12\n"
"v_fma_f32 v12,v12,v11,s13\n"
AW "a187,v12\n"
AR "v12,a207\n"
"v_subrev_f32 v12,s13,v12\n"
"v_fma_f32 v12,v12,v11,s13\n"
AW "a207,v12\n"
AR "v12,a227\n"
"v_subrev_f32 v12,s13,v12\n"
"v_fma_f32 v12,v12,v11,s13\n"
AW "a227,v12\n"
"v_mul_f32 v53,v58,v11\n"
"v_sub_f32 v36,v36,v10\n"
"v_sub_f32 v37,v37,v10\n"
"v_sub_f32 v38,v38,v10\n"
"v_sub_f32 v39,v39,v10\n"
"v_sub_f32 v96,v96,v10\n"
"v_sub_f32 v97,v97,v10\n"
"v_sub_f32 v98,v98,v10\n"
"v_sub_f32 v99,v99,v10\n"
"v_sub_f32 v76,v76,v10\n"
"v_sub_f32 v77,v77,v10\n"
"v_sub_f32 v78,v78,v10\n"
"v_sub_f32 v79,v79,v10\n"
"v_sub_f32 v116,v116,v10\n"
"v_sub_f32 v117,v117,v10\n"
"v_sub_f32 v118,v118,v10\n"
"v_sub_f32 v119,v119,v10\n"
"v_exp_f32 v76,v76\n"
"v_exp_f32 v77,v77\n"
"v_exp_f32 v78,v78\n"
"v_exp_f32 v79,v79\n"
"v_exp_f32 v116,v116\n"
"v_exp_f32 v117,v117\n"
"v_exp_f32 v118,v118\n"
"v_exp_f32 v119,v119\n"
"v_add_f32 v53,v53,v76\n"
"v_add_f32 v53,v53,v77\n"
"v_add_f32 v53,v53,v78\n"
"v_add_f32 v53,v53,v79\n"
"v_add_f32 v53,v53,v116\n"
"v_add_f32 v53,v53,v117\n"
"v_add_f32 v53,v53,v118\n"
"v_add_f32 v53,v53,v119\n"
CV "v76,v76,v77\n"
CV "v77,v78,v79\n"
CV "v78,v116,v117\n"
CV "v79,v118,v119\n"
"v_max3_f32 v10,v80,v81,v82\n"
"v_max3_f32 v11,v83,v120,v121\n"
"v_max3_f32 v10,v10,v122,v123\n"
"v_max_f32 v10,v10,v11\n"
"v_mov_b32 v11,v10\n"
"s_nop 1\n"
"v_permlane16_swap_b32 v10,v11\n"
"v_max_f32 v10,v10,v11\n"
"v_mov_b32 v11,v10\n"
"s_nop 1\n"
"v_permlane32_swap_b32 v10,v11\n"
"v_max_f32 v10,v10,v11\n"
"v_add_f32 v10,0x41300000,v10\n"
"v_max_f32 v10,0,v10\n"
"v_exp_f32 v11,-v10\n"
AR "v12,a8\n"
"v_subrev_f32 v12,s13,v12\n"
"v_fma_f32 v12,v12,v11,s13\n"
AW "a8,v12\n"
AR "v12,a28\n"
"v_subrev_f32 v12,s13,v12\n"
"v_fma_f32 v12,v12,v11,s13\n"
AW "a28,v12\n"
AR "v12,a48\n"
"v_subrev_f32 v12,s13,v12\n"
"v_fma_f32 v12,v12,v11,s13\n"
AW "a48,v12\n"
AR "v12,a68\n"
"v_subrev_f32 v12,s13,v12\n"
"v_fma_f32 v12,v12,v11,s13\n"
AW "a68,v12\n"
AR "v12,a88\n"
"v_subrev_f32 v12,s13,v12\n"
"v_fma_f32 v12,v12,v11,s13\n"
AW "a88,v12\n"
AR "v12,a108\n"
"v_subrev_f32 v12,s13,v12\n"
"v_fma_f32 v12,v12,v11,s13\n"
AW "a108,v12\n"
AR "v12,a128\n"
"v_subrev_f32 v12,s13,v12\n"
"v_fma_f32 v12,v12,v11,s13\n"
AW "a128,v12\n"
AR "v12,a148\n"
"v_subrev_f32 v12,s13,v12\n"
"v_fma_f32 v12,v12,v11,s13\n"
AW "a148,v12\n"
AR "v12,a168\n"
"v_subrev_f32 v12,s13,v12\n"
"v_fma_f32 v12,v12,v11,s13\n"
AW "a168,v12\n"
AR "v12,a188\n"
"v_subrev_f32 v12,s13,v12\n"
"v_fma_f32 v12,v12,v11,s13\n"
AW "a188,v12\n"
AR "v12,a208\n"
"v_subrev_f32 v12,s13,v12\n"
"v_fma_f32 v12,v12,v11,s13\n"
AW "a208,v12\n"
AR "v12,a228\n"
"v_subrev_f32 v12,s13,v12\n"
"v_fma_f32 v12,v12,v11,s13\n"
AW "a228,v12\n"
AR "v12,a9\n"
"v_subrev_f32 v12,s13,v12\n"
"v_fma_f32 v12,v12,v11,s13\n"
AW "a9,v12\n"
AR "v12,a29\n"
"v_subrev_f32 v12,s13,v12\n"
"v_fma_f32 v12,v12,v11,s13\n"
AW "a29,v12\n"
AR "v12,a49\n"
"v_subrev_f32 v12,s13,v12\n"
"v_fma_f32 v12,v12,v11,s13\n"
AW "a49,v12\n"
AR "v12,a69\n"
"v_subrev_f32 v12,s13,v12\n"
"v_fma_f32 v12,v12,v11,s13\n"
AW "a69,v12\n"
AR "v12,a89\n"
"v_subrev_f32 v12,s13,v12\n"
"v_fma_f32 v12,v12,v11,s13\n"
AW "a89,v12\n"
AR "v12,a109\n"
"v_subrev_f32 v12,s13,v12\n"
"v_fma_f32 v12,v12,v11,s13\n"
AW "a109,v12\n"
AR "v12,a129\n"
"v_subrev_f32 v12,s13,v12\n"
"v_fma_f32 v12,v12,v11,s13\n"
AW "a129,v12\n"
AR "v12,a149\n"
"v_subrev_f32 v12,s13,v12\n"
"v_fma_f32 v12,v12,v11,s13\n"
AW "a149,v12\n"
AR "v12,a169\n"
"v_subrev_f32 v12,s13,v12\n"
"v_fma_f32 v12,v12,v11,s13\n"
AW "a169,v12\n"
AR "v12,a189\n"
"v_subrev_f32 v12,s13,v12\n"
"v_fma_f32 v12,v12,v11,s13\n"
AW "a189,v12\n"
AR "v12,a209\n"
"v_subrev_f32 v12,s13,v12\n"
"v_fma_f32 v12,v12,v11,s13\n"
AW "a209,v12\n"
AR "v12,a229\n"
"v_subrev_f32 v12,s13,v12\n"
"v_fma_f32 v12,v12,v11,s13\n"
AW "a229,v12\n"
AR "v12,a10\n"
"v_subrev_f32 v12,s13,v12\n"
"v_fma_f32 v12,v12,v11,s13\n"
AW "a10,v12\n"
AR "v12,a30\n"
"v_subrev_f32 v12,s13,v12\n"
"v_fma_f32 v12,v12,v11,s13\n"
AW "a30,v12\n"
AR "v12,a50\n"
"v_subrev_f32 v12,s13,v12\n"
"v_fma_f32 v12,v12,v11,s13\n"
AW "a50,v12\n"
AR "v12,a70\n"
"v_subrev_f32 v12,s13,v12\n"
"v_fma_f32 v12,v12,v11,s13\n"
AW "a70,v12\n"
AR "v12,a90\n"
"v_subrev_f32 v12,s13,v12\n"
"v_fma_f32 v12,v12,v11,s13\n"
AW "a90,v12\n"
AR "v12,a110\n"
"v_subrev_f32 v12,s13,v12\n"
"v_fma_f32 v12,v12,v11,s13\n"
AW "a110,v12\n"
AR "v12,a130\n"
"v_subrev_f32 v12,s13,v12\n"
"v_fma_f32 v12,v12,v11,s13\n"
AW "a130,v12\n"
AR "v12,a150\n"
"v_subrev_f32 v12,s13,v12\n"
"v_fma_f32 v12,v12,v11,s13\n"
AW "a150,v12\n"
AR "v12,a170\n"
"v_subrev_f32 v12,s13,v12\n"
"v_fma_f32 v12,v12,v11,s13\n"
AW "a170,v12\n"
AR "v12,a190\n"
"v_subrev_f32 v12,s13,v12\n"
"v_fma_f32 v12,v12,v11,s13\n"
AW "a190,v12\n"
AR "v12,a210\n"
"v_subrev_f32 v12,s13,v12\n"
"v_fma_f32 v12,v12,v11,s13\n"
AW "a210,v12\n"
AR "v12,a230\n"
"v_subrev_f32 v12,s13,v12\n"
"v_fma_f32 v12,v12,v11,s13\n"
AW "a230,v12\n"
AR "v12,a11\n"
"v_subrev_f32 v12,s13,v12\n"
"v_fma_f32 v12,v12,v11,s13\n"
AW "a11,v12\n"
AR "v12,a31\n"
"v_subrev_f32 v12,s13,v12\n"
"v_fma_f32 v12,v12,v11,s13\n"
AW "a31,v12\n"
AR "v12,a51\n"
"v_subrev_f32 v12,s13,v12\n"
"v_fma_f32 v12,v12,v11,s13\n"
AW "a51,v12\n"
AR "v12,a71\n"
"v_subrev_f32 v12,s13,v12\n"
"v_fma_f32 v12,v12,v11,s13\n"
AW "a71,v12\n"
AR "v12,a91\n"
"v_subrev_f32 v12,s13,v12\n"
"v_fma_f32 v12,v12,v11,s13\n"
AW "a91,v12\n"
AR "v12,a111\n"
"v_subrev_f32 v12,s13,v12\n"
"v_fma_f32 v12,v12,v11,s13\n"
AW "a111,v12\n"
AR "v12,a131\n"
"v_subrev_f32 v12,s13,v12\n"
"v_fma_f32 v12,v12,v11,s13\n"
AW "a131,v12\n"
AR "v12,a151\n"
"v_subrev_f32 v12,s13,v12\n"
"v_fma_f32 v12,v12,v11,s13\n"
AW "a151,v12\n"
AR "v12,a171\n"
"v_subrev_f32 v12,s13,v12\n"
"v_fma_f32 v12,v12,v11,s13\n"
AW "a171,v12\n"
AR "v12,a191\n"
"v_subrev_f32 v12,s13,v12\n"
"v_fma_f32 v12,v12,v11,s13\n"
AW "a191,v12\n"
AR "v12,a211\n"
"v_subrev_f32 v12,s13,v12\n"
"v_fma_f32 v12,v12,v11,s13\n"
AW "a211,v12\n"
AR "v12,a231\n"
"v_subrev_f32 v12,s13,v12\n"
"v_fma_f32 v12,v12,v11,s13\n"
AW "a231,v12\n"
"v_mul_f32 v54,v59,v11\n"
"v_sub_f32 v40,v40,v10\n"
"v_sub_f32 v41,v41,v10\n"
"v_sub_f32 v42,v42,v10\n"
"v_sub_f32 v43,v43,v10\n"
"v_sub_f32 v100,v100,v10\n"
"v_sub_f32 v101,v101,v10\n"
"v_sub_f32 v102,v102,v10\n"
"v_sub_f32 v103,v103,v10\n"
"v_sub_f32 v80,v80,v10\n"
"v_sub_f32 v81,v81,v10\n"
"v_sub_f32 v82,v82,v10\n"
"v_sub_f32 v83,v83,v10\n"
"v_sub_f32 v120,v120,v10\n"
"v_sub_f32 v121,v121,v10\n"
"v_sub_f32 v122,v122,v10\n"
"v_sub_f32 v123,v123,v10\n"
"v_exp_f32 v80,v80\n"
"v_exp_f32 v81,v81\n"
"v_exp_f32 v82,v82\n"
"v_exp_f32 v83,v83\n"
"v_exp_f32 v120,v120\n"
"v_exp_f32 v121,v121\n"
"v_exp_f32 v122,v122\n"
"v_exp_f32 v123,v123\n"
"v_add_f32 v54,v54,v80\n"
"v_add_f32 v54,v54,v81\n"
"v_add_f32 v54,v54,v82\n"
"v_add_f32 v54,v54,v83\n"
"v_add_f32 v54,v54,v120\n"
"v_add_f32 v54,v54,v121\n"
"v_add_f32 v54,v54,v122\n"
"v_add_f32_e64 v54,v54,v123\n"
CV "v80,v80,v81\n"
CV "v81,v82,v83\n"
CV "v82,v120,v121\n"
CV "v83,v122,v123\n"
"v_max3_f32 v10,v84,v85,v86\n"
"v_max3_f32 v11,v87,v124,v125\n"
"v_max3_f32 v10,v10,v126,v127\n"
"v_max_f32 v10,v10,v11\n"
"v_mov_b32 v11,v10\n"
"s_nop 1\n"
"v_permlane16_swap_b32 v10,v11\n"
"v_max_f32 v10,v10,v11\n"
"v_mov_b32 v11,v10\n"
"s_nop 1\n"
"v_permlane32_swap_b32 v10,v11\n"
"v_max_f32 v10,v10,v11\n"
"v_add_f32 v10,0x41300000,v10\n"
"v_max_f32 v10,0,v10\n"
"v_exp_f32 v11,-v10\n"
AR "v12,a12\n"
"v_subrev_f32 v12,s13,v12\n"
"v_fma_f32 v12,v12,v11,s13\n"
AW "a12,v12\n"
AR "v12,a32\n"
"v_subrev_f32 v12,s13,v12\n"
"v_fma_f32 v12,v12,v11,s13\n"
AW "a32,v12\n"
AR "v12,a52\n"
"v_subrev_f32 v12,s13,v12\n"
"v_fma_f32 v12,v12,v11,s13\n"
AW "a52,v12\n"
AR "v12,a72\n"
"v_subrev_f32 v12,s13,v12\n"
"v_fma_f32 v12,v12,v11,s13\n"
AW "a72,v12\n"
AR "v12,a92\n"
"v_subrev_f32 v12,s13,v12\n"
"v_fma_f32 v12,v12,v11,s13\n"
AW "a92,v12\n"
AR "v12,a112\n"
"v_subrev_f32 v12,s13,v12\n"
"v_fma_f32 v12,v12,v11,s13\n"
AW "a112,v12\n"
AR "v12,a132\n"
"v_subrev_f32 v12,s13,v12\n"
"v_fma_f32 v12,v12,v11,s13\n"
AW "a132,v12\n"
AR "v12,a152\n"
"v_subrev_f32 v12,s13,v12\n"
"v_fma_f32 v12,v12,v11,s13\n"
AW "a152,v12\n"
AR "v12,a172\n"
"v_subrev_f32 v12,s13,v12\n"
"v_fma_f32 v12,v12,v11,s13\n"
AW "a172,v12\n"
AR "v12,a192\n"
"v_subrev_f32 v12,s13,v12\n"
"v_fma_f32 v12,v12,v11,s13\n"
AW "a192,v12\n"
AR "v12,a212\n"
"v_subrev_f32 v12,s13,v12\n"
"v_fma_f32 v12,v12,v11,s13\n"
AW "a212,v12\n"
AR "v12,a232\n"
"v_subrev_f32 v12,s13,v12\n"
"v_fma_f32 v12,v12,v11,s13\n"
AW "a232,v12\n"
AR "v12,a13\n"
"v_subrev_f32 v12,s13,v12\n"
"v_fma_f32 v12,v12,v11,s13\n"
AW "a13,v12\n"
AR "v12,a33\n"
"v_subrev_f32 v12,s13,v12\n"
"v_fma_f32 v12,v12,v11,s13\n"
AW "a33,v12\n"
AR "v12,a53\n"
"v_subrev_f32 v12,s13,v12\n"
"v_fma_f32 v12,v12,v11,s13\n"
AW "a53,v12\n"
AR "v12,a73\n"
"v_subrev_f32 v12,s13,v12\n"
"v_fma_f32 v12,v12,v11,s13\n"
AW "a73,v12\n"
AR "v12,a93\n"
"v_subrev_f32 v12,s13,v12\n"
"v_fma_f32 v12,v12,v11,s13\n"
AW "a93,v12\n"
AR "v12,a113\n"
"v_subrev_f32 v12,s13,v12\n"
"v_fma_f32 v12,v12,v11,s13\n"
AW "a113,v12\n"
AR "v12,a133\n"
"v_subrev_f32 v12,s13,v12\n"
"v_fma_f32 v12,v12,v11,s13\n"
AW "a133,v12\n"
AR "v12,a153\n"
"v_subrev_f32 v12,s13,v12\n"
"v_fma_f32 v12,v12,v11,s13\n"
AW "a153,v12\n"
AR "v12,a173\n"
"v_subrev_f32 v12,s13,v12\n"
"v_fma_f32 v12,v12,v11,s13\n"
AW "a173,v12\n"
AR "v12,a193\n"
"v_subrev_f32 v12,s13,v12\n"
"v_fma_f32 v12,v12,v11,s13\n"
AW "a193,v12\n"
AR "v12,a213\n"
"v_subrev_f32 v12,s13,v12\n"
"v_fma_f32 v12,v12,v11,s13\n"
AW "a213,v12\n"
AR "v12,a233\n"
"v_subrev_f32 v12,s13,v12\n"
"v_fma_f32 v12,v12,v11,s13\n"
AW "a233,v12\n"
AR "v12,a14\n"
"v_subrev_f32 v12,s13,v12\n"
"v_fma_f32 v12,v12,v11,s13\n"
AW "a14,v12\n"
AR "v12,a34\n"
"v_subrev_f32 v12,s13,v12\n"
"v_fma_f32 v12,v12,v11,s13\n"
AW "a34,v12\n"
AR "v12,a54\n"
"v_subrev_f32 v12,s13,v12\n"
"v_fma_f32 v12,v12,v11,s13\n"
AW "a54,v12\n"
AR "v12,a74\n"
"v_subrev_f32 v12,s13,v12\n"
"v_fma_f32 v12,v12,v11,s13\n"
AW "a74,v12\n"
AR "v12,a94\n"
"v_subrev_f32 v12,s13,v12\n"
"v_fma_f32 v12,v12,v11,s13\n"
AW "a94,v12\n"
AR "v12,a114\n"
"v_subrev_f32 v12,s13,v12\n"
"v_fma_f32 v12,v12,v11,s13\n"
AW "a114,v12\n"
AR "v12,a134\n"
"v_subrev_f32 v12,s13,v12\n"
"v_fma_f32 v12,v12,v11,s13\n"
AW "a134,v12\n"
AR "v12,a154\n"
"v_subrev_f32 v12,s13,v12\n"
"v_fma_f32 v12,v12,v11,s13\n"
AW "a154,v12\n"
AR "v12,a174\n"
"v_subrev_f32 v12,s13,v12\n"
"v_fma_f32 v12,v12,v11,s13\n"
AW "a174,v12\n"
AR "v12,a194\n"
"v_subrev_f32 v12,s13,v12\n"
"v_fma_f32 v12,v12,v11,s13\n"
AW "a194,v12\n"
AR "v12,a214\n"
"v_subrev_f32 v12,s13,v12\n"
"v_fma_f32 v12,v12,v11,s13\n"
AW "a214,v12\n"
AR "v12,a234\n"
"v_subrev_f32 v12,s13,v12\n"
"v_fma_f32 v12,v12,v11,s13\n"
AW "a234,v12\n"
AR "v12,a15\n"
"v_subrev_f32 v12,s13,v12\n"
"v_fma_f32 v12,v12,v11,s13\n"
AW "a15,v12\n"
AR "v12,a35\n"
"v_subrev_f32 v12,s13,v12\n"
"v_fma_f32 v12,v12,v11,s13\n"
AW "a35,v12\n"
AR "v12,a55\n"
"v_subrev_f32 v12,s13,v12\n"
"v_fma_f32 v12,v12,v11,s13\n"
AW "a55,v12\n"
AR "v12,a75\n"
"v_subrev_f32 v12,s13,v12\n"
"v_fma_f32 v12,v12,v11,s13\n"
AW "a75,v12\n"
AR "v12,a95\n"
"v_subrev_f32 v12,s13,v12\n"
"v_fma_f32 v12,v12,v11,s13\n"
AW "a95,v12\n"
AR "v12,a115\n"
"v_subrev_f32 v12,s13,v12\n"
"v_fma_f32 v12,v12,v11,s13\n"
AW "a115,v12\n"
AR "v12,a135\n"
"v_subrev_f32 v12,s13,v12\n"
"v_fma_f32 v12,v12,v11,s13\n"
AW "a135,v12\n"
AR "v12,a155\n"
"v_subrev_f32 v12,s13,v12\n"
"v_fma_f32 v12,v12,v11,s13\n"
AW "a155,v12\n"
AR "v12,a175\n"
"v_subrev_f32 v12,s13,v12\n"
"v_fma_f32 v12,v12,v11,s13\n"
AW "a175,v12\n"
AR "v12,a195\n"
"v_subrev_f32 v12,s13,v12\n"
"v_fma_f32 v12,v12,v11,s13\n"
AW "a195,v12\n"
AR "v12,a215\n"
"v_subrev_f32 v12,s13,v12\n"
"v_fma_f32 v12,v12,v11,s13\n"
AW "a215,v12\n"
AR "v12,a235\n"
"v_subrev_f32 v12,s13,v12\n"
"v_fma_f32 v12,v12,v11,s13\n"
AW "a235,v12\n"
"v_mul_f32 v55,v60,v11\n"
"v_sub_f32 v44,v44,v10\n"
"v_sub_f32 v45,v45,v10\n"
"v_sub_f32 v46,v46,v10\n"
"v_sub_f32 v47,v47,v10\n"
"v_sub_f32 v104,v104,v10\n"
"v_sub_f32 v105,v105,v10\n"
"v_sub_f32 v106,v106,v10\n"
"v_sub_f32 v107,v107,v10\n"
"v_sub_f32 v84,v84,v10\n"
"v_sub_f32 v85,v85,v10\n"
"v_sub_f32 v86,v86,v10\n"
"v_sub_f32 v87,v87,v10\n"
"v_sub_f32 v124,v124,v10\n"
"v_sub_f32 v125,v125,v10\n"
"v_sub_f32 v126,v126,v10\n"
"v_sub_f32 v127,v127,v10\n"
"v_exp_f32 v84,v84\n"
"v_exp_f32 v85,v85\n"
"v_exp_f32 v86,v86\n"
"v_exp_f32 v87,v87\n"
"v_exp_f32 v124,v124\n"
"v_exp_f32 v125,v125\n"
"v_exp_f32 v126,v126\n"
"v_exp_f32 v127,v127\n"
"v_add_f32 v55,v55,v84\n"
"v_add_f32 v55,v55,v85\n"
"v_add_f32 v55,v55,v86\n"
"v_add_f32 v55,v55,v87\n"
"v_add_f32 v55,v55,v124\n"
"v_add_f32 v55,v55,v125\n"
"v_add_f32 v55,v55,v126\n"
"v_add_f32_e64 v55,v55,v127\n"
CV "v84,v84,v85\n"
CV "v85,v86,v87\n"
CV "v86,v124,v125\n"
CV "v87,v126,v127\n"
"v_max3_f32 v10,v88,v89,v90\n"
"v_max3_f32 v11,v91,v128,v129\n"
"v_max3_f32 v10,v10,v130,v131\n"
"v_max_f32 v10,v10,v11\n"
"v_mov_b32 v11,v10\n"
"s_nop 1\n"
"v_permlane16_swap_b32 v10,v11\n"
"v_max_f32 v10,v10,v11\n"
"v_mov_b32 v11,v10\n"
"s_nop 1\n"
"v_permlane32_swap_b32 v10,v11\n"
"v_max_f32 v10,v10,v11\n"
"v_add_f32 v10,0x41300000,v10\n"
"v_max_f32 v10,0,v10\n"
"v_exp_f32 v11,-v10\n"
AR "v12,a16\n"
"v_subrev_f32 v12,s13,v12\n"
"v_fma_f32 v12,v12,v11,s13\n"
AW "a16,v12\n"
AR "v12,a36\n"
"v_subrev_f32 v12,s13,v12\n"
"v_fma_f32 v12,v12,v11,s13\n"
AW "a36,v12\n"
AR "v12,a56\n"
"v_subrev_f32 v12,s13,v12\n"
"v_fma_f32 v12,v12,v11,s13\n"
AW "a56,v12\n"
AR "v12,a76\n"
"v_subrev_f32 v12,s13,v12\n"
"v_fma_f32 v12,v12,v11,s13\n"
AW "a76,v12\n"
AR "v12,a96\n"
"v_subrev_f32 v12,s13,v12\n"
"v_fma_f32 v12,v12,v11,s13\n"
AW "a96,v12\n"
AR "v12,a116\n"
"v_subrev_f32 v12,s13,v12\n"
"v_fma_f32 v12,v12,v11,s13\n"
AW "a116,v12\n"
AR "v12,a136\n"
"v_subrev_f32 v12,s13,v12\n"
"v_fma_f32 v12,v12,v11,s13\n"
AW "a136,v12\n"
AR "v12,a156\n"
"v_subrev_f32 v12,s13,v12\n"
"v_fma_f32 v12,v12,v11,s13\n"
AW "a156,v12\n"
AR "v12,a176\n"
"v_subrev_f32 v12,s13,v12\n"
"v_fma_f32 v12,v12,v11,s13\n"
AW "a176,v12\n"
AR "v12,a196\n"
"v_subrev_f32 v12,s13,v12\n"
"v_fma_f32 v12,v12,v11,s13\n"
AW "a196,v12\n"
AR "v12,a216\n"
"v_subrev_f32 v12,s13,v12\n"
"v_fma_f32 v12,v12,v11,s13\n"
AW "a216,v12\n"
AR "v12,a236\n"
"v_subrev_f32 v12,s13,v12\n"
"v_fma_f32 v12,v12,v11,s13\n"
AW "a236,v12\n"
AR "v12,a17\n"
"v_subrev_f32 v12,s13,v12\n"
"v_fma_f32 v12,v12,v11,s13\n"
AW "a17,v12\n"
AR "v12,a37\n"
"v_subrev_f32 v12,s13,v12\n"
"v_fma_f32 v12,v12,v11,s13\n"
AW "a37,v12\n"
AR "v12,a57\n"
"v_subrev_f32 v12,s13,v12\n"
"v_fma_f32 v12,v12,v11,s13\n"
AW "a57,v12\n"
AR "v12,a77\n"
"v_subrev_f32 v12,s13,v12\n"
"v_fma_f32 v12,v12,v11,s13\n"
AW "a77,v12\n"
AR "v12,a97\n"
"v_subrev_f32 v12,s13,v12\n"
"v_fma_f32 v12,v12,v11,s13\n"
AW "a97,v12\n"
AR "v12,a117\n"
"v_subrev_f32 v12,s13,v12\n"
"v_fma_f32 v12,v12,v11,s13\n"
AW "a117,v12\n"
AR "v12,a137\n"
"v_subrev_f32 v12,s13,v12\n"
"v_fma_f32 v12,v12,v11,s13\n"
AW "a137,v12\n"
AR "v12,a157\n"
"v_subrev_f32 v12,s13,v12\n"
"v_fma_f32 v12,v12,v11,s13\n"
AW "a157,v12\n"
AR "v12,a177\n"
"v_subrev_f32 v12,s13,v12\n"
"v_fma_f32 v12,v12,v11,s13\n"
AW "a177,v12\n"
AR "v12,a197\n"
"v_subrev_f32 v12,s13,v12\n"
"v_fma_f32 v12,v12,v11,s13\n"
AW "a197,v12\n"
AR "v12,a217\n"
"v_subrev_f32 v12,s13,v12\n"
"v_fma_f32 v12,v12,v11,s13\n"
AW "a217,v12\n"
AR "v12,a237\n"
"v_subrev_f32 v12,s13,v12\n"
"v_fma_f32 v12,v12,v11,s13\n"
AW "a237,v12\n"
AR "v12,a18\n"
"v_subrev_f32 v12,s13,v12\n"
"v_fma_f32 v12,v12,v11,s13\n"
AW "a18,v12\n"
AR "v12,a38\n"
"v_subrev_f32 v12,s13,v12\n"
"v_fma_f32 v12,v12,v11,s13\n"
AW "a38,v12\n"
AR "v12,a58\n"
"v_subrev_f32 v12,s13,v12\n"
"v_fma_f32 v12,v12,v11,s13\n"
AW "a58,v12\n"
AR "v12,a78\n"
"v_subrev_f32 v12,s13,v12\n"
"v_fma_f32 v12,v12,v11,s13\n"
AW "a78,v12\n"
AR "v12,a98\n"
"v_subrev_f32 v12,s13,v12\n"
"v_fma_f32 v12,v12,v11,s13\n"
AW "a98,v12\n"
AR "v12,a118\n"
"v_subrev_f32 v12,s13,v12\n"
"v_fma_f32 v12,v12,v11,s13\n"
AW "a118,v12\n"
AR "v12,a138\n"
"v_subrev_f32 v12,s13,v12\n"
"v_fma_f32 v12,v12,v11,s13\n"
AW "a138,v12\n"
AR "v12,a158\n"
"v_subrev_f32 v12,s13,v12\n"
"v_fma_f32 v12,v12,v11,s13\n"
AW "a158,v12\n"
AR "v12,a178\n"
"v_subrev_f32 v12,s13,v12\n"
"v_fma_f32 v12,v12,v11,s13\n"
AW "a178,v12\n"
AR "v12,a198\n"
"v_subrev_f32 v12,s13,v12\n"
"v_fma_f32 v12,v12,v11,s13\n"
AW "a198,v12\n"
AR "v12,a218\n"
"v_subrev_f32 v12,s13,v12\n"
"v_fma_f32 v12,v12,v11,s13\n"
AW "a218,v12\n"
AR "v12,a238\n"
"v_subrev_f32 v12,s13,v12\n"
"v_fma_f32 v12,v12,v11,s13\n"
AW "a238,v12\n"
AR "v12,a19\n"
"v_subrev_f32 v12,s13,v12\n"
"v_fma_f32 v12,v12,v11,s13\n"
AW "a19,v12\n"
AR "v12,a39\n"
"v_subrev_f32 v12,s13,v12\n"
"v_fma_f32 v12,v12,v11,s13\n"
AW "a39,v12\n"
AR "v12,a59\n"
"v_subrev_f32 v12,s13,v12\n"
"v_fma_f32 v12,v12,v11,s13\n"
AW "a59,v12\n"
AR "v12,a79\n"
"v_subrev_f32 v12,s13,v12\n"
"v_fma_f32 v12,v12,v11,s13\n"
AW "a79,v12\n"
AR "v12,a99\n"
"v_subrev_f32 v12,s13,v12\n"
"v_fma_f32 v12,v12,v11,s13\n"
AW "a99,v12\n"
AR "v12,a119\n"
"v_subrev_f32 v12,s13,v12\n"
"v_fma_f32 v12,v12,v11,s13\n"
AW "a119,v12\n"
AR "v12,a139\n"
"v_subrev_f32 v12,s13,v12\n"
"v_fma_f32 v12,v12,v11,s13\n"
AW "a139,v12\n"
AR "v12,a159\n"
"v_subrev_f32 v12,s13,v12\n"
"v_fma_f32 v12,v12,v11,s13\n"
AW "a159,v12\n"
AR "v12,a179\n"
"v_subrev_f32 v12,s13,v12\n"
"v_fma_f32 v12,v12,v11,s13\n"
AW "a179,v12\n"
AR "v12,a199\n"
"v_subrev_f32 v12,s13,v12\n"
"v_fma_f32 v12,v12,v11,s13\n"
AW "a199,v12\n"
AR "v12,a219\n"
"v_subrev_f32 v12,s13,v12\n"
"v_fma_f32 v12,v12,v11,s13\n"
AW "a219,v12\n"
AR "v12,a239\n"
"v_subrev_f32 v12,s13,v12\n"
"v_fma_f32 v12,v12,v11,s13\n"
AW "a239,v12\n"
"v_mul_f32 v56,v61,v11\n"
"v_sub_f32 v48,v48,v10\n"
"v_sub_f32 v49,v49,v10\n"
"v_sub_f32 v50,v50,v10\n"
"v_sub_f32 v51,v51,v10\n"
"v_sub_f32 v108,v108,v10\n"
"v_sub_f32 v109,v109,v10\n"
"v_sub_f32 v110,v110,v10\n"
"v_sub_f32 v111,v111,v10\n"
"v_sub_f32 v88,v88,v10\n"
"v_sub_f32 v89,v89,v10\n"
"v_sub_f32 v90,v90,v10\n"
"v_sub_f32 v91,v91,v10\n"
"v_sub_f32 v128,v128,v10\n"
"v_sub_f32 v129,v129,v10\n"
"v_sub_f32 v130,v130,v10\n"
"v_sub_f32 v131,v131,v10\n"
"v_exp_f32 v88,v88\n"
"v_exp_f32 v89,v89\n"
"v_exp_f32 v90,v90\n"
"v_exp_f32 v91,v91\n"
"v_exp_f32 v128,v128\n"
"v_exp_f32 v129,v129\n"
"v_exp_f32 v130,v130\n"
"v_exp_f32 v131,v131\n"
"v_add_f32 v56,v56,v88\n"
"v_add_f32 v56,v56,v89\n"
"v_add_f32 v56,v56,v90\n"
"v_add_f32 v56,v56,v91\n"
"v_add_f32 v56,v56,v128\n"
"v_add_f32 v56,v56,v129\n"
"v_add_f32 v56,v56,v130\n"
"v_add_f32_e64 v56,v56,v131\n"
CV "v88,v88,v89\n"
CV "v89,v90,v91\n"
CV "v90,v128,v129\n"
CV "v91,v130,v131\n"
"s_nop 7\n"
"s_setpc_b64 s[52:53]\n"
".Lslow_1:\n"
"s_nop 7\n"
"s_nop 7\n"
"v_and_b32 v10,240,v9\n"
"v_mul_u32_u24 v10,24,v10\n"
"v_lshrrev_b32 v11,4,v9\n"
"v_and_or_b32 v10,v11,48,v10\n"
"s_mov_b32 s59,s34\n"
"s_mov_b32 s34,s20\n"
"s_add_u32 s58,s74,0\n"
"s_nop 3\n"
LD "v[58:61],v10,s[32:35],s58 offen\n"
"s_waitcnt vmcnt(0)\n"
MF "v[92:95],v[58:61],v[136:139],v[32:35]\n"
MF "v[96:99],v[58:61],v[140:143],v[36:39]\n"
MF "v[100:103],v[58:61],v[144:147],v[40:43]\n"
MF "v[104:107],v[58:61],v[148:151],v[44:47]\n"
MF "v[108:111],v[58:61],v[152:155],v[48:51]\n"
"s_add_u32 s58,s74,0\n"
"s_nop 3\n"
LD "v[58:61],v10,s[32:35],s58 offen offset:64\n"
"s_waitcnt vmcnt(0)\n"
MF "v[92:95],v[58:61],v[156:159],v[92:95]\n"
MF "v[96:99],v[58:61],v[160:163],v[96:99]\n"
MF "v[100:103],v[58:61],v[164:167],v[100:103]\n"
MF "v[104:107],v[58:61],v[168:171],v[104:107]\n"
MF "v[108:111],v[58:61],v[172:175],v[108:111]\n"
"s_add_u32 s58,s74,0\n"
"s_nop 3\n"
LD "v[58:61],v10,s[32:35],s58 offen offset:128\n"
"s_waitcnt vmcnt(0)\n"
MF "v[92:95],v[58:61],v[176:179],v[92:95]\n"
MF "v[96:99],v[58:61],v[180:183],v[96:99]\n"
MF "v[100:103],v[58:61],v[184:187],v[100:103]\n"
MF "v[104:107],v[58:61],v[188:191],v[104:107]\n"
MF "v[108:111],v[58:61],v[192:195],v[108:111]\n"
"s_add_u32 s58,s74,0\n"
"s_nop 3\n"
LD "v[58:61],v10,s[32:35],s58 offen offset:192\n"
"s_waitcnt vmcnt(0)\n"
MF "v[92:95],v[58:61],v[196:199],v[92:95]\n"
MF "v[96:99],v[58:61],v[200:203],v[96:99]\n"
MF "v[100:103],v[58:61],v[204:207],v[100:103]\n"
MF "v[104:107],v[58:61],v[208:211],v[104:107]\n"
MF "v[108:111],v[58:61],v[212:215],v[108:111]\n"
"s_add_u32 s58,s74,0\n"
"s_nop 3\n"
LD "v[58:61],v10,s[32:35],s58 offen offset:256\n"
"s_waitcnt vmcnt(0)\n"
MF "v[92:95],v[58:61],v[216:219],v[92:95]\n"
MF "v[96:99],v[58:61],v[220:223],v[96:99]\n"
MF "v[100:103],v[58:61],v[224:227],v[100:103]\n"
MF "v[104:107],v[58:61],v[228:231],v[104:107]\n"
MF "v[108:111],v[58:61],v[232:235],v[108:111]\n"
"s_add_u32 s58,s74,0\n"
"s_nop 3\n"
LD "v[58:61],v10,s[32:35],s58 offen offset:320\n"
"s_waitcnt vmcnt(0)\n"
MF "v[92:95],v[58:61],v[236:239],v[92:95]\n"
MF "v[96:99],v[58:61],v[240:243],v[96:99]\n"
MF "v[100:103],v[58:61],v[244:247],v[100:103]\n"
MF "v[104:107],v[58:61],v[248:251],v[104:107]\n"
MF "v[108:111],v[58:61],v[252:255],v[108:111]\n"
"s_add_u32 s58,s74,6144\n"
"s_nop 3\n"
LD "v[58:61],v10,s[32:35],s58 offen\n"
"s_waitcnt vmcnt(0)\n"
MF "v[112:115],v[58:61],v[136:139],v[32:35]\n"
MF "v[116:119],v[58:61],v[140:143],v[36:39]\n"
MF "v[120:123],v[58:61],v[144:147],v[40:43]\n"
MF "v[124:127],v[58:61],v[148:151],v[44:47]\n"
MF "v[128:131],v[58:61],v[152:155],v[48:51]\n"
"s_add_u32 s58,s74,6144\n"
"s_nop 3\n"
LD "v[58:61],v10,s[32:35],s58 offen offset:64\n"
"s_waitcnt vmcnt(0)\n"
MF "v[112:115],v[58:61],v[156:159],v[112:115]\n"
MF "v[116:119],v[58:61],v[160:163],v[116:119]\n"
MF "v[120:123],v[58:61],v[164:167],v[120:123]\n"
MF "v[124:127],v[58:61],v[168:171],v[124:127]\n"
MF "v[128:131],v[58:61],v[172:175],v[128:131]\n"
"s_add_u32 s58,s74,6144\n"
"s_nop 3\n"
LD "v[58:61],v10,s[32:35],s58 offen offset:128\n"
"s_waitcnt vmcnt(0)\n"
MF "v[112:115],v[58:61],v[176:179],v[112:115]\n"
MF "v[116:119],v[58:61],v[180:183],v[116:119]\n"
MF "v[120:123],v[58:61],v[184:187],v[120:123]\n"
MF "v[124:127],v[58:61],v[188:191],v[124:127]\n"
MF "v[128:131],v[58:61],v[192:195],v[128:131]\n"
"s_add_u32 s58,s74,6144\n"
"s_nop 3\n"
LD "v[58:61],v10,s[32:35],s58 offen offset:192\n"
"s_waitcnt vmcnt(0)\n"
MF "v[112:115],v[58:61],v[196:199],v[112:115]\n"
MF "v[116:119],v[58:61],v[200:203],v[116:119]\n"
MF "v[120:123],v[58:61],v[204:207],v[120:123]\n"
MF "v[124:127],v[58:61],v[208:211],v[124:127]\n"
MF "v[128:131],v[58:61],v[212:215],v[128:131]\n"
"s_add_u32 s58,s74,6144\n"
"s_nop 3\n"
LD "v[58:61],v10,s[32:35],s58 offen offset:256\n"
"s_waitcnt vmcnt(0)\n"
MF "v[112:115],v[58:61],v[216:219],v[112:115]\n"
MF "v[116:119],v[58:61],v[220:223],v[116:119]\n"
MF "v[120:123],v[58:61],v[224:227],v[120:123]\n"
MF "v[124:127],v[58:61],v[228:231],v[124:127]\n"
MF "v[128:131],v[58:61],v[232:235],v[128:131]\n"
"s_add_u32 s58,s74,6144\n"
"s_nop 3\n"
LD "v[58:61],v10,s[32:35],s58 offen offset:320\n"
"s_waitcnt vmcnt(0)\n"
MF "v[112:115],v[58:61],v[236:239],v[112:115]\n"
MF "v[116:119],v[58:61],v[240:243],v[116:119]\n"
MF "v[120:123],v[58:61],v[244:247],v[120:123]\n"
MF "v[124:127],v[58:61],v[248:251],v[124:127]\n"
MF "v[128:131],v[58:61],v[252:255],v[128:131]\n"
"s_nop 7\n"
"s_mov_b32 s34,s59\n"
"v_max3_f32 v10,v92,v93,v94\n"
"s_nop 0\n"
"v_max3_f32 v11,v95,v112,v113\n"
"v_max3_f32 v10,v10,v114,v115\n"
"v_max_f32 v10,v10,v11\n"
"v_mov_b32 v11,v10\n"
"s_nop 1\n"
"v_permlane16_swap_b32 v10,v11\n"
"v_max_f32 v10,v10,v11\n"
"v_mov_b32 v11,v10\n"
"s_nop 1\n"
"v_permlane32_swap_b32 v10,v11\n"
"v_max_f32 v10,v10,v11\n"
"v_add_f32 v10,0x41300000,v10\n"
"v_max_f32 v10,0,v10\n"
"v_exp_f32 v11,-v10\n"
AR "v12,a0\n"
"v_subrev_f32 v12,s13,v12\n"
"v_fma_f32 v12,v12,v11,s13\n"
AW "a0,v12\n"
AR "v12,a20\n"
"v_subrev_f32 v12,s13,v12\n"
"v_fma_f32 v12,v12,v11,s13\n"
AW "a20,v12\n"
AR "v12,a40\n"
"v_subrev_f32 v12,s13,v12\n"
"v_fma_f32 v12,v12,v11,s13\n"
AW "a40,v12\n"
AR "v12,a60\n"
"v_subrev_f32 v12,s13,v12\n"
"v_fma_f32 v12,v12,v11,s13\n"
AW "a60,v12\n"
AR "v12,a80\n"
"v_subrev_f32 v12,s13,v12\n"
"v_fma_f32 v12,v12,v11,s13\n"
AW "a80,v12\n"
AR "v12,a100\n"
"v_subrev_f32 v12,s13,v12\n"
"v_fma_f32 v12,v12,v11,s13\n"
AW "a100,v12\n"
AR "v12,a120\n"
"v_subrev_f32 v12,s13,v12\n"
"v_fma_f32 v12,v12,v11,s13\n"
AW "a120,v12\n"
AR "v12,a140\n"
"v_subrev_f32 v12,s13,v12\n"
"v_fma_f32 v12,v12,v11,s13\n"
AW "a140,v12\n"
AR "v12,a160\n"
"v_subrev_f32 v12,s13,v12\n"
"v_fma_f32 v12,v12,v11,s13\n"
AW "a160,v12\n"
AR "v12,a180\n"
"v_subrev_f32 v12,s13,v12\n"
"v_fma_f32 v12,v12,v11,s13\n"
AW "a180,v12\n"
AR "v12,a200\n"
"v_subrev_f32 v12,s13,v12\n"
"v_fma_f32 v12,v12,v11,s13\n"
AW "a200,v12\n"
AR "v12,a220\n"
"v_subrev_f32 v12,s13,v12\n"
"v_fma_f32 v12,v12,v11,s13\n"
AW "a220,v12\n"
AR "v12,a1\n"
"v_subrev_f32 v12,s13,v12\n"
"v_fma_f32 v12,v12,v11,s13\n"
AW "a1,v12\n"
AR "v12,a21\n"
"v_subrev_f32 v12,s13,v12\n"
"v_fma_f32 v12,v12,v11,s13\n"
AW "a21,v12\n"
AR "v12,a41\n"
"v_subrev_f32 v12,s13,v12\n"
"v_fma_f32 v12,v12,v11,s13\n"
AW "a41,v12\n"
AR "v12,a61\n"
"v_subrev_f32 v12,s13,v12\n"
"v_fma_f32 v12,v12,v11,s13\n"
AW "a61,v12\n"
AR "v12,a81\n"
"v_subrev_f32 v12,s13,v12\n"
"v_fma_f32 v12,v12,v11,s13\n"
AW "a81,v12\n"
AR "v12,a101\n"
"v_subrev_f32 v12,s13,v12\n"
"v_fma_f32 v12,v12,v11,s13\n"
AW "a101,v12\n"
AR "v12,a121\n"
"v_subrev_f32 v12,s13,v12\n"
"v_fma_f32 v12,v12,v11,s13\n"
AW "a121,v12\n"
AR "v12,a141\n"
"v_subrev_f32 v12,s13,v12\n"
"v_fma_f32 v12,v12,v11,s13\n"
AW "a141,v12\n"
AR "v12,a161\n"
"v_subrev_f32 v12,s13,v12\n"
"v_fma_f32 v12,v12,v11,s13\n"
AW "a161,v12\n"
AR "v12,a181\n"
"v_subrev_f32 v12,s13,v12\n"
"v_fma_f32 v12,v12,v11,s13\n"
AW "a181,v12\n"
AR "v12,a201\n"
"v_subrev_f32 v12,s13,v12\n"
"v_fma_f32 v12,v12,v11,s13\n"
AW "a201,v12\n"
AR "v12,a221\n"
"v_subrev_f32 v12,s13,v12\n"
"v_fma_f32 v12,v12,v11,s13\n"
AW "a221,v12\n"
AR "v12,a2\n"
"v_subrev_f32 v12,s13,v12\n"
"v_fma_f32 v12,v12,v11,s13\n"
AW "a2,v12\n"
AR "v12,a22\n"
"v_subrev_f32 v12,s13,v12\n"
"v_fma_f32 v12,v12,v11,s13\n"
AW "a22,v12\n"
AR "v12,a42\n"
"v_subrev_f32 v12,s13,v12\n"
"v_fma_f32 v12,v12,v11,s13\n"
AW "a42,v12\n"
AR "v12,a62\n"
"v_subrev_f32 v12,s13,v12\n"
"v_fma_f32 v12,v12,v11,s13\n"
AW "a62,v12\n"
AR "v12,a82\n"
"v_subrev_f32 v12,s13,v12\n"
"v_fma_f32 v12,v12,v11,s13\n"
AW "a82,v12\n"
AR "v12,a102\n"
"v_subrev_f32 v12,s13,v12\n"
"v_fma_f32 v12,v12,v11,s13\n"
AW "a102,v12\n"
AR "v12,a122\n"
"v_subrev_f32 v12,s13,v12\n"
"v_fma_f32 v12,v12,v11,s13\n"
AW "a122,v12\n"
AR "v12,a142\n"
"v_subrev_f32 v12,s13,v12\n"
"v_fma_f32 v12,v12,v11,s13\n"
AW "a142,v12\n"
AR "v12,a162\n"
"v_subrev_f32 v12,s13,v12\n"
"v_fma_f32 v12,v12,v11,s13\n"
AW "a162,v12\n"
AR "v12,a182\n"
"v_subrev_f32 v12,s13,v12\n"
"v_fma_f32 v12,v12,v11,s13\n"
AW "a182,v12\n"
AR "v12,a202\n"
"v_subrev_f32 v12,s13,v12\n"
"v_fma_f32 v12,v12,v11,s13\n"
AW "a202,v12\n"
AR "v12,a222\n"
"v_subrev_f32 v12,s13,v12\n"
"v_fma_f32 v12,v12,v11,s13\n"
AW "a222,v12\n"
AR "v12,a3\n"
"v_subrev_f32 v12,s13,v12\n"
"v_fma_f32 v12,v12,v11,s13\n"
AW "a3,v12\n"
AR "v12,a23\n"
"v_subrev_f32 v12,s13,v12\n"
"v_fma_f32 v12,v12,v11,s13\n"
AW "a23,v12\n"
AR "v12,a43\n"
"v_subrev_f32 v12,s13,v12\n"
"v_fma_f32 v12,v12,v11,s13\n"
AW "a43,v12\n"
AR "v12,a63\n"
"v_subrev_f32 v12,s13,v12\n"
"v_fma_f32 v12,v12,v11,s13\n"
AW "a63,v12\n"
AR "v12,a83\n"
"v_subrev_f32 v12,s13,v12\n"
"v_fma_f32 v12,v12,v11,s13\n"
AW "a83,v12\n"
AR "v12,a103\n"
"v_subrev_f32 v12,s13,v12\n"
"v_fma_f32 v12,v12,v11,s13\n"
AW "a103,v12\n"
AR "v12,a123\n"
"v_subrev_f32 v12,s13,v12\n"
"v_fma_f32 v12,v12,v11,s13\n"
AW "a123,v12\n"
AR "v12,a143\n"
"v_subrev_f32 v12,s13,v12\n"
"v_fma_f32 v12,v12,v11,s13\n"
AW "a143,v12\n"
AR "v12,a163\n"
"v_subrev_f32 v12,s13,v12\n"
"v_fma_f32 v12,v12,v11,s13\n"
AW "a163,v12\n"
AR "v12,a183\n"
"v_subrev_f32 v12,s13,v12\n"
"v_fma_f32 v12,v12,v11,s13\n"
AW "a183,v12\n"
AR "v12,a203\n"
"v_subrev_f32 v12,s13,v12\n"
"v_fma_f32 v12,v12,v11,s13\n"
AW "a203,v12\n"
AR "v12,a223\n"
"v_subrev_f32 v12,s13,v12\n"
"v_fma_f32 v12,v12,v11,s13\n"
AW "a223,v12\n"
"v_mul_f32 v57,v52,v11\n"
"v_sub_f32 v32,v32,v10\n"
"v_sub_f32 v33,v33,v10\n"
"v_sub_f32 v34,v34,v10\n"
"v_sub_f32 v35,v35,v10\n"
"v_sub_f32 v72,v72,v10\n"
"v_sub_f32 v73,v73,v10\n"
"v_sub_f32 v74,v74,v10\n"
"v_sub_f32 v75,v75,v10\n"
"v_sub_f32 v92,v92,v10\n"
"v_sub_f32 v93,v93,v10\n"
"v_sub_f32 v94,v94,v10\n"
"v_sub_f32 v95,v95,v10\n"
"v_sub_f32 v112,v112,v10\n"
"v_sub_f32 v113,v113,v10\n"
"v_sub_f32 v114,v114,v10\n"
"v_sub_f32 v115,v115,v10\n"
"v_exp_f32 v92,v92\n"
"v_exp_f32 v93,v93\n"
"v_exp_f32 v94,v94\n"
"v_exp_f32 v95,v95\n"
"v_exp_f32 v112,v112\n"
"v_exp_f32 v113,v113\n"
"v_exp_f32 v114,v114\n"
"v_exp_f32 v115,v115\n"
"v_add_f32 v57,v57,v92\n"
"v_add_f32 v57,v57,v93\n"
"v_add_f32 v57,v57,v94\n"
"v_add_f32 v57,v57,v95\n"
"v_add_f32 v57,v57,v112\n"
"v_add_f32 v57,v57,v113\n"
"v_add_f32 v57,v57,v114\n"
"v_add_f32 v57,v57,v115\n"
CV "v92,v92,v93\n"
CV "v93,v94,v95\n"
CV "v94,v112,v113\n"
CV "v95,v114,v115\n"
"v_max3_f32 v10,v96,v97,v98\n"
"v_max3_f32 v11,v99,v116,v117\n"
"v_max3_f32 v10,v10,v118,v119\n"
"v_max_f32 v10,v10,v11\n"
"v_mov_b32 v11,v10\n"
"s_nop 1\n"
"v_permlane16_swap_b32 v10,v11\n"
"v_max_f32 v10,v10,v11\n"
"v_mov_b32 v11,v10\n"
"s_nop 1\n"
"v_permlane32_swap_b32 v10,v11\n"
"v_max_f32 v10,v10,v11\n"
"v_add_f32 v10,0x41300000,v10\n"
"v_max_f32 v10,0,v10\n"
"v_exp_f32 v11,-v10\n"
AR "v12,a4\n"
"v_subrev_f32 v12,s13,v12\n"
"v_fma_f32 v12,v12,v11,s13\n"
AW "a4,v12\n"
AR "v12,a24\n"
"v_subrev_f32 v12,s13,v12\n"
"v_fma_f32 v12,v12,v11,s13\n"
AW "a24,v12\n"
AR "v12,a44\n"
"v_subrev_f32 v12,s13,v12\n"
"v_fma_f32 v12,v12,v11,s13\n"
AW "a44,v12\n"
AR "v12,a64\n"
"v_subrev_f32 v12,s13,v12\n"
"v_fma_f32 v12,v12,v11,s13\n"
AW "a64,v12\n"
AR "v12,a84\n"
"v_subrev_f32 v12,s13,v12\n"
"v_fma_f32 v12,v12,v11,s13\n"
AW "a84,v12\n"
AR "v12,a104\n"
"v_subrev_f32 v12,s13,v12\n"
"v_fma_f32 v12,v12,v11,s13\n"
AW "a104,v12\n"
AR "v12,a124\n"
"v_subrev_f32 v12,s13,v12\n"
"v_fma_f32 v12,v12,v11,s13\n"
AW "a124,v12\n"
AR "v12,a144\n"
"v_subrev_f32 v12,s13,v12\n"
"v_fma_f32 v12,v12,v11,s13\n"
AW "a144,v12\n"
AR "v12,a164\n"
"v_subrev_f32 v12,s13,v12\n"
"v_fma_f32 v12,v12,v11,s13\n"
AW "a164,v12\n"
AR "v12,a184\n"
"v_subrev_f32 v12,s13,v12\n"
"v_fma_f32 v12,v12,v11,s13\n"
AW "a184,v12\n"
AR "v12,a204\n"
"v_subrev_f32 v12,s13,v12\n"
"v_fma_f32 v12,v12,v11,s13\n"
AW "a204,v12\n"
AR "v12,a224\n"
"v_subrev_f32 v12,s13,v12\n"
"v_fma_f32 v12,v12,v11,s13\n"
AW "a224,v12\n"
AR "v12,a5\n"
"v_subrev_f32 v12,s13,v12\n"
"v_fma_f32 v12,v12,v11,s13\n"
AW "a5,v12\n"
AR "v12,a25\n"
"v_subrev_f32 v12,s13,v12\n"
"v_fma_f32 v12,v12,v11,s13\n"
AW "a25,v12\n"
AR "v12,a45\n"
"v_subrev_f32 v12,s13,v12\n"
"v_fma_f32 v12,v12,v11,s13\n"
AW "a45,v12\n"
AR "v12,a65\n"
"v_subrev_f32 v12,s13,v12\n"
"v_fma_f32 v12,v12,v11,s13\n"
AW "a65,v12\n"
AR "v12,a85\n"
"v_subrev_f32 v12,s13,v12\n"
"v_fma_f32 v12,v12,v11,s13\n"
AW "a85,v12\n"
AR "v12,a105\n"
"v_subrev_f32 v12,s13,v12\n"
"v_fma_f32 v12,v12,v11,s13\n"
AW "a105,v12\n"
AR "v12,a125\n"
"v_subrev_f32 v12,s13,v12\n"
"v_fma_f32 v12,v12,v11,s13\n"
AW "a125,v12\n"
AR "v12,a145\n"
"v_subrev_f32 v12,s13,v12\n"
"v_fma_f32 v12,v12,v11,s13\n"
AW "a145,v12\n"
AR "v12,a165\n"
"v_subrev_f32 v12,s13,v12\n"
"v_fma_f32 v12,v12,v11,s13\n"
AW "a165,v12\n"
AR "v12,a185\n"
"v_subrev_f32 v12,s13,v12\n"
"v_fma_f32 v12,v12,v11,s13\n"
AW "a185,v12\n"
AR "v12,a205\n"
"v_subrev_f32 v12,s13,v12\n"
"v_fma_f32 v12,v12,v11,s13\n"
AW "a205,v12\n"
AR "v12,a225\n"
"v_subrev_f32 v12,s13,v12\n"
"v_fma_f32 v12,v12,v11,s13\n"
AW "a225,v12\n"
AR "v12,a6\n"
"v_subrev_f32 v12,s13,v12\n"
"v_fma_f32 v12,v12,v11,s13\n"
AW "a6,v12\n"
AR "v12,a26\n"
"v_subrev_f32 v12,s13,v12\n"
"v_fma_f32 v12,v12,v11,s13\n"
AW "a26,v12\n"
AR "v12,a46\n"
"v_subrev_f32 v12,s13,v12\n"
"v_fma_f32 v12,v12,v11,s13\n"
AW "a46,v12\n"
AR "v12,a66\n"
"v_subrev_f32 v12,s13,v12\n"
"v_fma_f32 v12,v12,v11,s13\n"
AW "a66,v12\n"
AR "v12,a86\n"
"v_subrev_f32 v12,s13,v12\n"
"v_fma_f32 v12,v12,v11,s13\n"
AW "a86,v12\n"
AR "v12,a106\n"
"v_subrev_f32 v12,s13,v12\n"
"v_fma_f32 v12,v12,v11,s13\n"
AW "a106,v12\n"
AR "v12,a126\n"
"v_subrev_f32 v12,s13,v12\n"
"v_fma_f32 v12,v12,v11,s13\n"
AW "a126,v12\n"
AR "v12,a146\n"
"v_subrev_f32 v12,s13,v12\n"
"v_fma_f32 v12,v12,v11,s13\n"
AW "a146,v12\n"
AR "v12,a166\n"
"v_subrev_f32 v12,s13,v12\n"
"v_fma_f32 v12,v12,v11,s13\n"
AW "a166,v12\n"
AR "v12,a186\n"
"v_subrev_f32 v12,s13,v12\n"
"v_fma_f32 v12,v12,v11,s13\n"
AW "a186,v12\n"
AR "v12,a206\n"
"v_subrev_f32 v12,s13,v12\n"
"v_fma_f32 v12,v12,v11,s13\n"
AW "a206,v12\n"
AR "v12,a226\n"
"v_subrev_f32 v12,s13,v12\n"
"v_fma_f32 v12,v12,v11,s13\n"
AW "a226,v12\n"
AR "v12,a7\n"
"v_subrev_f32 v12,s13,v12\n"
"v_fma_f32 v12,v12,v11,s13\n"
AW "a7,v12\n"
AR "v12,a27\n"
"v_subrev_f32 v12,s13,v12\n"
"v_fma_f32 v12,v12,v11,s13\n"
AW "a27,v12\n"
AR "v12,a47\n"
"v_subrev_f32 v12,s13,v12\n"
"v_fma_f32 v12,v12,v11,s13\n"
AW "a47,v12\n"
AR "v12,a67\n"
"v_subrev_f32 v12,s13,v12\n"
"v_fma_f32 v12,v12,v11,s13\n"
AW "a67,v12\n"
AR "v12,a87\n"
"v_subrev_f32 v12,s13,v12\n"
"v_fma_f32 v12,v12,v11,s13\n"
AW "a87,v12\n"
AR "v12,a107\n"
"v_subrev_f32 v12,s13,v12\n"
"v_fma_f32 v12,v12,v11,s13\n"
AW "a107,v12\n"
AR "v12,a127\n"
"v_subrev_f32 v12,s13,v12\n"
"v_fma_f32 v12,v12,v11,s13\n"
AW "a127,v12\n"
AR "v12,a147\n"
"v_subrev_f32 v12,s13,v12\n"
"v_fma_f32 v12,v12,v11,s13\n"
AW "a147,v12\n"
AR "v12,a167\n"
"v_subrev_f32 v12,s13,v12\n"
"v_fma_f32 v12,v12,v11,s13\n"
AW "a167,v12\n"
AR "v12,a187\n"
"v_subrev_f32 v12,s13,v12\n"
"v_fma_f32 v12,v12,v11,s13\n"
AW "a187,v12\n"
AR "v12,a207\n"
"v_subrev_f32 v12,s13,v12\n"
"v_fma_f32 v12,v12,v11,s13\n"
AW "a207,v12\n"
AR "v12,a227\n"
"v_subrev_f32 v12,s13,v12\n"
"v_fma_f32 v12,v12,v11,s13\n"
AW "a227,v12\n"
"v_mul_f32 v58,v53,v11\n"
"v_sub_f32 v36,v36,v10\n"
"v_sub_f32 v37,v37,v10\n"
"v_sub_f32 v38,v38,v10\n"
"v_sub_f32 v39,v39,v10\n"
"v_sub_f32 v76,v76,v10\n"
"v_sub_f32 v77,v77,v10\n"
"v_sub_f32 v78,v78,v10\n"
"v_sub_f32 v79,v79,v10\n"
"v_sub_f32 v96,v96,v10\n"
"v_sub_f32 v97,v97,v10\n"
"v_sub_f32 v98,v98,v10\n"
"v_sub_f32 v99,v99,v10\n"
"v_sub_f32 v116,v116,v10\n"
"v_sub_f32 v117,v117,v10\n"
"v_sub_f32 v118,v118,v10\n"
"v_sub_f32 v119,v119,v10\n"
"v_exp_f32 v96,v96\n"
"v_exp_f32 v97,v97\n"
"v_exp_f32 v98,v98\n"
"v_exp_f32 v99,v99\n"
"v_exp_f32 v116,v116\n"
"v_exp_f32 v117,v117\n"
"v_exp_f32 v118,v118\n"
"v_exp_f32 v119,v119\n"
"v_add_f32 v58,v58,v96\n"
"v_add_f32 v58,v58,v97\n"
"v_add_f32 v58,v58,v98\n"
"v_add_f32 v58,v58,v99\n"
"v_add_f32 v58,v58,v116\n"
"v_add_f32 v58,v58,v117\n"
"v_add_f32 v58,v58,v118\n"
"v_add_f32_e64 v58,v58,v119\n"
CV "v96,v96,v97\n"
CV "v97,v98,v99\n"
CV "v98,v116,v117\n"
CV "v99,v118,v119\n"
"v_max3_f32 v10,v100,v101,v102\n"
"v_max3_f32 v11,v103,v120,v121\n"
"v_max3_f32 v10,v10,v122,v123\n"
"v_max_f32 v10,v10,v11\n"
"v_mov_b32 v11,v10\n"
"s_nop 1\n"
"v_permlane16_swap_b32 v10,v11\n"
"v_max_f32 v10,v10,v11\n"
"v_mov_b32 v11,v10\n"
"s_nop 1\n"
"v_permlane32_swap_b32 v10,v11\n"
"v_max_f32 v10,v10,v11\n"
"v_add_f32 v10,0x41300000,v10\n"
"v_max_f32 v10,0,v10\n"
"v_exp_f32 v11,-v10\n"
AR "v12,a8\n"
"v_subrev_f32 v12,s13,v12\n"
"v_fma_f32 v12,v12,v11,s13\n"
AW "a8,v12\n"
AR "v12,a28\n"
"v_subrev_f32 v12,s13,v12\n"
"v_fma_f32 v12,v12,v11,s13\n"
AW "a28,v12\n"
AR "v12,a48\n"
"v_subrev_f32 v12,s13,v12\n"
"v_fma_f32 v12,v12,v11,s13\n"
AW "a48,v12\n"
AR "v12,a68\n"
"v_subrev_f32 v12,s13,v12\n"
"v_fma_f32 v12,v12,v11,s13\n"
AW "a68,v12\n"
AR "v12,a88\n"
"v_subrev_f32 v12,s13,v12\n"
"v_fma_f32 v12,v12,v11,s13\n"
AW "a88,v12\n"
AR "v12,a108\n"
"v_subrev_f32 v12,s13,v12\n"
"v_fma_f32 v12,v12,v11,s13\n"
AW "a108,v12\n"
AR "v12,a128\n"
"v_subrev_f32 v12,s13,v12\n"
"v_fma_f32 v12,v12,v11,s13\n"
AW "a128,v12\n"
AR "v12,a148\n"
"v_subrev_f32 v12,s13,v12\n"
"v_fma_f32 v12,v12,v11,s13\n"
AW "a148,v12\n"
AR "v12,a168\n"
"v_subrev_f32 v12,s13,v12\n"
"v_fma_f32 v12,v12,v11,s13\n"
AW "a168,v12\n"
AR "v12,a188\n"
"v_subrev_f32 v12,s13,v12\n"
"v_fma_f32 v12,v12,v11,s13\n"
AW "a188,v12\n"
AR "v12,a208\n"
"v_subrev_f32 v12,s13,v12\n"
"v_fma_f32 v12,v12,v11,s13\n"
AW "a208,v12\n"
AR "v12,a228\n"
"v_subrev_f32 v12,s13,v12\n"
"v_fma_f32 v12,v12,v11,s13\n"
AW "a228,v12\n"
AR "v12,a9\n"
"v_subrev_f32 v12,s13,v12\n"
"v_fma_f32 v12,v12,v11,s13\n"
AW "a9,v12\n"
AR "v12,a29\n"
"v_subrev_f32 v12,s13,v12\n"
"v_fma_f32 v12,v12,v11,s13\n"
AW "a29,v12\n"
AR "v12,a49\n"
"v_subrev_f32 v12,s13,v12\n"
"v_fma_f32 v12,v12,v11,s13\n"
AW "a49,v12\n"
AR "v12,a69\n"
"v_subrev_f32 v12,s13,v12\n"
"v_fma_f32 v12,v12,v11,s13\n"
AW "a69,v12\n"
AR "v12,a89\n"
"v_subrev_f32 v12,s13,v12\n"
"v_fma_f32 v12,v12,v11,s13\n"
AW "a89,v12\n"
AR "v12,a109\n"
"v_subrev_f32 v12,s13,v12\n"
"v_fma_f32 v12,v12,v11,s13\n"
AW "a109,v12\n"
AR "v12,a129\n"
"v_subrev_f32 v12,s13,v12\n"
"v_fma_f32 v12,v12,v11,s13\n"
AW "a129,v12\n"
AR "v12,a149\n"
"v_subrev_f32 v12,s13,v12\n"
"v_fma_f32 v12,v12,v11,s13\n"
AW "a149,v12\n"
AR "v12,a169\n"
"v_subrev_f32 v12,s13,v12\n"
"v_fma_f32 v12,v12,v11,s13\n"
AW "a169,v12\n"
AR "v12,a189\n"
"v_subrev_f32 v12,s13,v12\n"
"v_fma_f32 v12,v12,v11,s13\n"
AW "a189,v12\n"
AR "v12,a209\n"
"v_subrev_f32 v12,s13,v12\n"
"v_fma_f32 v12,v12,v11,s13\n"
AW "a209,v12\n"
AR "v12,a229\n"
"v_subrev_f32 v12,s13,v12\n"
"v_fma_f32 v12,v12,v11,s13\n"
AW "a229,v12\n"
AR "v12,a10\n"
"v_subrev_f32 v12,s13,v12\n"
"v_fma_f32 v12,v12,v11,s13\n"
AW "a10,v12\n"
AR "v12,a30\n"
"v_subrev_f32 v12,s13,v12\n"
"v_fma_f32 v12,v12,v11,s13\n"
AW "a30,v12\n"
AR "v12,a50\n"
"v_subrev_f32 v12,s13,v12\n"
"v_fma_f32 v12,v12,v11,s13\n"
AW "a50,v12\n"
AR "v12,a70\n"
"v_subrev_f32 v12,s13,v12\n"
"v_fma_f32 v12,v12,v11,s13\n"
AW "a70,v12\n"
AR "v12,a90\n"
"v_subrev_f32 v12,s13,v12\n"
"v_fma_f32 v12,v12,v11,s13\n"
AW "a90,v12\n"
AR "v12,a110\n"
"v_subrev_f32 v12,s13,v12\n"
"v_fma_f32 v12,v12,v11,s13\n"
AW "a110,v12\n"
AR "v12,a130\n"
"v_subrev_f32 v12,s13,v12\n"
"v_fma_f32 v12,v12,v11,s13\n"
AW "a130,v12\n"
AR "v12,a150\n"
"v_subrev_f32 v12,s13,v12\n"
"v_fma_f32 v12,v12,v11,s13\n"
AW "a150,v12\n"
AR "v12,a170\n"
"v_subrev_f32 v12,s13,v12\n"
"v_fma_f32 v12,v12,v11,s13\n"
AW "a170,v12\n"
AR "v12,a190\n"
"v_subrev_f32 v12,s13,v12\n"
"v_fma_f32 v12,v12,v11,s13\n"
AW "a190,v12\n"
AR "v12,a210\n"
"v_subrev_f32 v12,s13,v12\n"
"v_fma_f32 v12,v12,v11,s13\n"
AW "a210,v12\n"
AR "v12,a230\n"
"v_subrev_f32 v12,s13,v12\n"
"v_fma_f32 v12,v12,v11,s13\n"
AW "a230,v12\n"
AR "v12,a11\n"
"v_subrev_f32 v12,s13,v12\n"
"v_fma_f32 v12,v12,v11,s13\n"
AW "a11,v12\n"
AR "v12,a31\n"
"v_subrev_f32 v12,s13,v12\n"
"v_fma_f32 v12,v12,v11,s13\n"
AW "a31,v12\n"
AR "v12,a51\n"
"v_subrev_f32 v12,s13,v12\n"
"v_fma_f32 v12,v12,v11,s13\n"
AW "a51,v12\n"
AR "v12,a71\n"
"v_subrev_f32 v12,s13,v12\n"
"v_fma_f32 v12,v12,v11,s13\n"
AW "a71,v12\n"
AR "v12,a91\n"
"v_subrev_f32 v12,s13,v12\n"
"v_fma_f32 v12,v12,v11,s13\n"
AW "a91,v12\n"
AR "v12,a111\n"
"v_subrev_f32 v12,s13,v12\n"
"v_fma_f32 v12,v12,v11,s13\n"
AW "a111,v12\n"
AR "v12,a131\n"
"v_subrev_f32 v12,s13,v12\n"
"v_fma_f32 v12,v12,v11,s13\n"
AW "a131,v12\n"
AR "v12,a151\n"
"v_subrev_f32 v12,s13,v12\n"
"v_fma_f32 v12,v12,v11,s13\n"
AW "a151,v12\n"
AR "v12,a171\n"
"v_subrev_f32 v12,s13,v12\n"
"v_fma_f32 v12,v12,v11,s13\n"
AW "a171,v12\n"
AR "v12,a191\n"
"v_subrev_f32 v12,s13,v12\n"
"v_fma_f32 v12,v12,v11,s13\n"
AW "a191,v12\n"
AR "v12,a211\n"
"v_subrev_f32 v12,s13,v12\n"
"v_fma_f32 v12,v12,v11,s13\n"
AW "a211,v12\n"
AR "v12,a231\n"
"v_subrev_f32 v12,s13,v12\n"
"v_fma_f32 v12,v12,v11,s13\n"
AW "a231,v12\n"
"v_mul_f32 v59,v54,v11\n"
"v_sub_f32 v40,v40,v10\n"
"v_sub_f32 v41,v41,v10\n"
"v_sub_f32 v42,v42,v10\n"
"v_sub_f32 v43,v43,v10\n"
"v_sub_f32 v80,v80,v10\n"
"v_sub_f32 v81,v81,v10\n"
"v_sub_f32 v82,v82,v10\n"
"v_sub_f32 v83,v83,v10\n"
"v_sub_f32 v100,v100,v10\n"
"v_sub_f32 v101,v101,v10\n"
"v_sub_f32 v102,v102,v10\n"
"v_sub_f32 v103,v103,v10\n"
"v_sub_f32 v120,v120,v10\n"
"v_sub_f32 v121,v121,v10\n"
"v_sub_f32 v122,v122,v10\n"
"v_sub_f32 v123,v123,v10\n"
"v_exp_f32 v100,v100\n"
"v_exp_f32 v101,v101\n"
"v_exp_f32 v102,v102\n"
"v_exp_f32 v103,v103\n"
"v_exp_f32 v120,v120\n"
"v_exp_f32 v121,v121\n"
"v_exp_f32 v122,v122\n"
"v_exp_f32 v123,v123\n"
"v_add_f32 v59,v59,v100\n"
"v_add_f32 v59,v59,v101\n"
"v_add_f32 v59,v59,v102\n"
"v_add_f32 v59,v59,v103\n"
"v_add_f32 v59,v59,v120\n"
"v_add_f32 v59,v59,v121\n"
"v_add_f32 v59,v59,v122\n"
"v_add_f32_e64 v59,v59,v123\n"
CV "v100,v100,v101\n"
CV "v101,v102,v103\n"
CV "v102,v120,v121\n"
CV "v103,v122,v123\n"
"v_max3_f32 v10,v104,v105,v106\n"
"v_max3_f32 v11,v107,v124,v125\n"
"v_max3_f32 v10,v10,v126,v127\n"
"v_max_f32 v10,v10,v11\n"
"v_mov_b32 v11,v10\n"
"s_nop 1\n"
"v_permlane16_swap_b32 v10,v11\n"
"v_max_f32 v10,v10,v11\n"
"v_mov_b32 v11,v10\n"
"s_nop 1\n"
"v_permlane32_swap_b32 v10,v11\n"
"v_max_f32 v10,v10,v11\n"
"v_add_f32 v10,0x41300000,v10\n"
"v_max_f32 v10,0,v10\n"
"v_exp_f32 v11,-v10\n"
AR "v12,a12\n"
"v_subrev_f32 v12,s13,v12\n"
"v_fma_f32 v12,v12,v11,s13\n"
AW "a12,v12\n"
AR "v12,a32\n"
"v_subrev_f32 v12,s13,v12\n"
"v_fma_f32 v12,v12,v11,s13\n"
AW "a32,v12\n"
AR "v12,a52\n"
"v_subrev_f32 v12,s13,v12\n"
"v_fma_f32 v12,v12,v11,s13\n"
AW "a52,v12\n"
AR "v12,a72\n"
"v_subrev_f32 v12,s13,v12\n"
"v_fma_f32 v12,v12,v11,s13\n"
AW "a72,v12\n"
AR "v12,a92\n"
"v_subrev_f32 v12,s13,v12\n"
"v_fma_f32 v12,v12,v11,s13\n"
AW "a92,v12\n"
AR "v12,a112\n"
"v_subrev_f32 v12,s13,v12\n"
"v_fma_f32 v12,v12,v11,s13\n"
AW "a112,v12\n"
AR "v12,a132\n"
"v_subrev_f32 v12,s13,v12\n"
"v_fma_f32 v12,v12,v11,s13\n"
AW "a132,v12\n"
AR "v12,a152\n"
"v_subrev_f32 v12,s13,v12\n"
"v_fma_f32 v12,v12,v11,s13\n"
AW "a152,v12\n"
AR "v12,a172\n"
"v_subrev_f32 v12,s13,v12\n"
"v_fma_f32 v12,v12,v11,s13\n"
AW "a172,v12\n"
AR "v12,a192\n"
"v_subrev_f32 v12,s13,v12\n"
"v_fma_f32 v12,v12,v11,s13\n"
AW "a192,v12\n"
AR "v12,a212\n"
"v_subrev_f32 v12,s13,v12\n"
"v_fma_f32 v12,v12,v11,s13\n"
AW "a212,v12\n"
AR "v12,a232\n"
"v_subrev_f32 v12,s13,v12\n"
"v_fma_f32 v12,v12,v11,s13\n"
AW "a232,v12\n"
AR "v12,a13\n"
"v_subrev_f32 v12,s13,v12\n"
"v_fma_f32 v12,v12,v11,s13\n"
AW "a13,v12\n"
AR "v12,a33\n"
"v_subrev_f32 v12,s13,v12\n"
"v_fma_f32 v12,v12,v11,s13\n"
AW "a33,v12\n"
AR "v12,a53\n"
"v_subrev_f32 v12,s13,v12\n"
"v_fma_f32 v12,v12,v11,s13\n"
AW "a53,v12\n"
AR "v12,a73\n"
"v_subrev_f32 v12,s13,v12\n"
"v_fma_f32 v12,v12,v11,s13\n"
AW "a73,v12\n"
AR "v12,a93\n"
"v_subrev_f32 v12,s13,v12\n"
"v_fma_f32 v12,v12,v11,s13\n"
AW "a93,v12\n"
AR "v12,a113\n"
"v_subrev_f32 v12,s13,v12\n"
"v_fma_f32 v12,v12,v11,s13\n"
AW "a113,v12\n"
AR "v12,a133\n"
"v_subrev_f32 v12,s13,v12\n"
"v_fma_f32 v12,v12,v11,s13\n"
AW "a133,v12\n"
AR "v12,a153\n"
"v_subrev_f32 v12,s13,v12\n"
"v_fma_f32 v12,v12,v11,s13\n"
AW "a153,v12\n"
AR "v12,a173\n"
"v_subrev_f32 v12,s13,v12\n"
"v_fma_f32 v12,v12,v11,s13\n"
AW "a173,v12\n"
AR "v12,a193\n"
"v_subrev_f32 v12,s13,v12\n"
"v_fma_f32 v12,v12,v11,s13\n"
AW "a193,v12\n"
AR "v12,a213\n"
"v_subrev_f32 v12,s13,v12\n"
"v_fma_f32 v12,v12,v11,s13\n"
AW "a213,v12\n"
AR "v12,a233\n"
"v_subrev_f32 v12,s13,v12\n"
"v_fma_f32 v12,v12,v11,s13\n"
AW "a233,v12\n"
AR "v12,a14\n"
"v_subrev_f32 v12,s13,v12\n"
"v_fma_f32 v12,v12,v11,s13\n"
AW "a14,v12\n"
AR "v12,a34\n"
"v_subrev_f32 v12,s13,v12\n"
"v_fma_f32 v12,v12,v11,s13\n"
AW "a34,v12\n"
AR "v12,a54\n"
"v_subrev_f32 v12,s13,v12\n"
"v_fma_f32 v12,v12,v11,s13\n"
AW "a54,v12\n"
AR "v12,a74\n"
"v_subrev_f32 v12,s13,v12\n"
"v_fma_f32 v12,v12,v11,s13\n"
AW "a74,v12\n"
AR "v12,a94\n"
"v_subrev_f32 v12,s13,v12\n"
"v_fma_f32 v12,v12,v11,s13\n"
AW "a94,v12\n"
AR "v12,a114\n"
"v_subrev_f32 v12,s13,v12\n"
"v_fma_f32 v12,v12,v11,s13\n"
AW "a114,v12\n"
AR "v12,a134\n"
"v_subrev_f32 v12,s13,v12\n"
"v_fma_f32 v12,v12,v11,s13\n"
AW "a134,v12\n"
AR "v12,a154\n"
"v_subrev_f32 v12,s13,v12\n"
"v_fma_f32 v12,v12,v11,s13\n"
AW "a154,v12\n"
AR "v12,a174\n"
"v_subrev_f32 v12,s13,v12\n"
"v_fma_f32 v12,v12,v11,s13\n"
AW "a174,v12\n"
AR "v12,a194\n"
"v_subrev_f32 v12,s13,v12\n"
"v_fma_f32 v12,v12,v11,s13\n"
AW "a194,v12\n"
AR "v12,a214\n"
"v_subrev_f32 v12,s13,v12\n"
"v_fma_f32 v12,v12,v11,s13\n"
AW "a214,v12\n"
AR "v12,a234\n"
"v_subrev_f32 v12,s13,v12\n"
"v_fma_f32 v12,v12,v11,s13\n"
AW "a234,v12\n"
AR "v12,a15\n"
"v_subrev_f32 v12,s13,v12\n"
"v_fma_f32 v12,v12,v11,s13\n"
AW "a15,v12\n"
AR "v12,a35\n"
"v_subrev_f32 v12,s13,v12\n"
"v_fma_f32 v12,v12,v11,s13\n"
AW "a35,v12\n"
AR "v12,a55\n"
"v_subrev_f32 v12,s13,v12\n"
"v_fma_f32 v12,v12,v11,s13\n"
AW "a55,v12\n"
AR "v12,a75\n"
"v_subrev_f32 v12,s13,v12\n"
"v_fma_f32 v12,v12,v11,s13\n"
AW "a75,v12\n"
AR "v12,a95\n"
"v_subrev_f32 v12,s13,v12\n"
"v_fma_f32 v12,v12,v11,s13\n"
AW "a95,v12\n"
AR "v12,a115\n"
"v_subrev_f32 v12,s13,v12\n"
"v_fma_f32 v12,v12,v11,s13\n"
AW "a115,v12\n"
AR "v12,a135\n"
"v_subrev_f32 v12,s13,v12\n"
"v_fma_f32 v12,v12,v11,s13\n"
AW "a135,v12\n"
AR "v12,a155\n"
"v_subrev_f32 v12,s13,v12\n"
"v_fma_f32 v12,v12,v11,s13\n"
AW "a155,v12\n"
AR "v12,a175\n"
"v_subrev_f32 v12,s13,v12\n"
"v_fma_f32 v12,v12,v11,s13\n"
AW "a175,v12\n"
AR "v12,a195\n"
"v_subrev_f32 v12,s13,v12\n"
"v_fma_f32 v12,v12,v11,s13\n"
AW "a195,v12\n"
AR "v12,a215\n"
"v_subrev_f32 v12,s13,v12\n"
"v_fma_f32 v12,v12,v11,s13\n"
AW "a215,v12\n"
AR "v12,a235\n"
"v_subrev_f32 v12,s13,v12\n"
"v_fma_f32 v12,v12,v11,s13\n"
AW "a235,v12\n"
"v_mul_f32 v60,v55,v11\n"
"v_sub_f32 v44,v44,v10\n"
"v_sub_f32 v45,v45,v10\n"
"v_sub_f32 v46,v46,v10\n"
"v_sub_f32 v47,v47,v10\n"
"v_sub_f32 v84,v84,v10\n"
"v_sub_f32 v85,v85,v10\n"
"v_sub_f32 v86,v86,v10\n"
"v_sub_f32 v87,v87,v10\n"
"v_sub_f32 v104,v104,v10\n"
"v_sub_f32 v105,v105,v10\n"
"v_sub_f32 v106,v106,v10\n"
"v_sub_f32 v107,v107,v10\n"
"v_sub_f32 v124,v124,v10\n"
"v_sub_f32 v125,v125,v10\n"
"v_sub_f32 v126,v126,v10\n"
"v_sub_f32 v127,v127,v10\n"
"v_exp_f32 v104,v104\n"
"v_exp_f32 v105,v105\n"
"v_exp_f32 v106,v106\n"
"v_exp_f32 v107,v107\n"
"v_exp_f32 v124,v124\n"
"v_exp_f32 v125,v125\n"
"v_exp_f32 v126,v126\n"
"v_exp_f32 v127,v127\n"
"v_add_f32 v60,v60,v104\n"
"v_add_f32 v60,v60,v105\n"
"v_add_f32 v60,v60,v106\n"
"v_add_f32 v60,v60,v107\n"
"v_add_f32 v60,v60,v124\n"
"v_add_f32 v60,v60,v125\n"
"v_add_f32 v60,v60,v126\n"
"v_add_f32_e64 v60,v60,v127\n"
CV "v104,v104,v105\n"
CV "v105,v106,v107\n"
CV "v106,v124,v125\n"
CV "v107,v126,v127\n"
"v_max3_f32 v10,v108,v109,v110\n"
"v_max3_f32 v11,v111,v128,v129\n"
"v_max3_f32 v10,v10,v130,v131\n"
"v_max_f32 v10,v10,v11\n"
"v_mov_b32 v11,v10\n"
"s_nop 1\n"
"v_permlane16_swap_b32 v10,v11\n"
"v_max_f32 v10,v10,v11\n"
"v_mov_b32 v11,v10\n"
"s_nop 1\n"
"v_permlane32_swap_b32 v10,v11\n"
"v_max_f32_e64 v10,v10,v11\n"
"v_add_f32 v10,0x41300000,v10\n"
"v_max_f32 v10,0,v10\n"
"v_exp_f32 v11,-v10\n"
AR "v12,a16\n"
"v_subrev_f32 v12,s13,v12\n"
"v_fma_f32 v12,v12,v11,s13\n"
AW "a16,v12\n"
AR "v12,a36\n"
"v_subrev_f32 v12,s13,v12\n"
"v_fma_f32 v12,v12,v11,s13\n"
AW "a36,v12\n"
AR "v12,a56\n"
"v_subrev_f32 v12,s13,v12\n"
"v_fma_f32 v12,v12,v11,s13\n"
AW "a56,v12\n"
AR "v12,a76\n"
"v_subrev_f32 v12,s13,v12\n"
"v_fma_f32 v12,v12,v11,s13\n"
AW "a76,v12\n"
AR "v12,a96\n"
"v_subrev_f32 v12,s13,v12\n"
"v_fma_f32 v12,v12,v11,s13\n"
AW "a96,v12\n"
AR "v12,a116\n"
"v_subrev_f32 v12,s13,v12\n"
"v_fma_f32 v12,v12,v11,s13\n"
AW "a116,v12\n"
AR "v12,a136\n"
"v_subrev_f32 v12,s13,v12\n"
"v_fma_f32 v12,v12,v11,s13\n"
AW "a136,v12\n"
AR "v12,a156\n"
"v_subrev_f32 v12,s13,v12\n"
"v_fma_f32 v12,v12,v11,s13\n"
AW "a156,v12\n"
AR "v12,a176\n"
"v_subrev_f32 v12,s13,v12\n"
"v_fma_f32 v12,v12,v11,s13\n"
AW "a176,v12\n"
AR "v12,a196\n"
"v_subrev_f32 v12,s13,v12\n"
"v_fma_f32 v12,v12,v11,s13\n"
AW "a196,v12\n"
AR "v12,a216\n"
"v_subrev_f32 v12,s13,v12\n"
"v_fma_f32 v12,v12,v11,s13\n"
AW "a216,v12\n"
AR "v12,a236\n"
"v_subrev_f32 v12,s13,v12\n"
"v_fma_f32 v12,v12,v11,s13\n"
AW "a236,v12\n"
AR "v12,a17\n"
"v_subrev_f32 v12,s13,v12\n"
"v_fma_f32 v12,v12,v11,s13\n"
AW "a17,v12\n"
AR "v12,a37\n"
"v_subrev_f32 v12,s13,v12\n"
"v_fma_f32 v12,v12,v11,s13\n"
AW "a37,v12\n"
AR "v12,a57\n"
"v_subrev_f32 v12,s13,v12\n"
"v_fma_f32 v12,v12,v11,s13\n"
AW "a57,v12\n"
AR "v12,a77\n"
"v_subrev_f32 v12,s13,v12\n"
"v_fma_f32 v12,v12,v11,s13\n"
AW "a77,v12\n"
AR "v12,a97\n"
"v_subrev_f32 v12,s13,v12\n"
"v_fma_f32 v12,v12,v11,s13\n"
AW "a97,v12\n"
AR "v12,a117\n"
"v_subrev_f32 v12,s13,v12\n"
"v_fma_f32 v12,v12,v11,s13\n"
AW "a117,v12\n"
AR "v12,a137\n"
"v_subrev_f32 v12,s13,v12\n"
"v_fma_f32 v12,v12,v11,s13\n"
AW "a137,v12\n"
AR "v12,a157\n"
"v_subrev_f32 v12,s13,v12\n"
"v_fma_f32 v12,v12,v11,s13\n"
AW "a157,v12\n"
AR "v12,a177\n"
"v_subrev_f32 v12,s13,v12\n"
"v_fma_f32 v12,v12,v11,s13\n"
AW "a177,v12\n"
AR "v12,a197\n"
"v_subrev_f32 v12,s13,v12\n"
"v_fma_f32 v12,v12,v11,s13\n"
AW "a197,v12\n"
AR "v12,a217\n"
"v_subrev_f32 v12,s13,v12\n"
"v_fma_f32 v12,v12,v11,s13\n"
AW "a217,v12\n"
AR "v12,a237\n"
"v_subrev_f32 v12,s13,v12\n"
"v_fma_f32 v12,v12,v11,s13\n"
AW "a237,v12\n"
AR "v12,a18\n"
"v_subrev_f32 v12,s13,v12\n"
"v_fma_f32 v12,v12,v11,s13\n"
AW "a18,v12\n"
AR "v12,a38\n"
"v_subrev_f32 v12,s13,v12\n"
"v_fma_f32 v12,v12,v11,s13\n"
AW "a38,v12\n"
AR "v12,a58\n"
"v_subrev_f32 v12,s13,v12\n"
"v_fma_f32 v12,v12,v11,s13\n"
AW "a58,v12\n"
AR "v12,a78\n"
"v_subrev_f32 v12,s13,v12\n"
"v_fma_f32 v12,v12,v11,s13\n"
AW "a78,v12\n"
AR "v12,a98\n"
"v_subrev_f32 v12,s13,v12\n"
"v_fma_f32 v12,v12,v11,s13\n"
AW "a98,v12\n"
AR "v12,a118\n"
"v_subrev_f32 v12,s13,v12\n"
"v_fma_f32 v12,v12,v11,s13\n"
AW "a118,v12\n"
AR "v12,a138\n"
"v_subrev_f32 v12,s13,v12\n"
"v_fma_f32 v12,v12,v11,s13\n"
AW "a138,v12\n"
AR "v12,a158\n"
"v_subrev_f32 v12,s13,v12\n"
"v_fma_f32 v12,v12,v11,s13\n"
AW "a158,v12\n"
AR "v12,a178\n"
"v_subrev_f32 v12,s13,v12\n"
"v_fma_f32 v12,v12,v11,s13\n"
AW "a178,v12\n"
AR "v12,a198\n"
"v_subrev_f32 v12,s13,v12\n"
"v_fma_f32 v12,v12,v11,s13\n"
AW "a198,v12\n"
AR "v12,a218\n"
"v_subrev_f32 v12,s13,v12\n"
"v_fma_f32 v12,v12,v11,s13\n"
AW "a218,v12\n"
AR "v12,a238\n"
"v_subrev_f32 v12,s13,v12\n"
"v_fma_f32 v12,v12,v11,s13\n"
AW "a238,v12\n"
AR "v12,a19\n"
"v_subrev_f32 v12,s13,v12\n"
"v_fma_f32 v12,v12,v11,s13\n"
AW "a19,v12\n"
AR "v12,a39\n"
"v_subrev_f32 v12,s13,v12\n"
"v_fma_f32 v12,v12,v11,s13\n"
AW "a39,v12\n"
AR "v12,a59\n"
"v_subrev_f32 v12,s13,v12\n"
"v_fma_f32 v12,v12,v11,s13\n"
AW "a59,v12\n"
AR "v12,a79\n"
"v_subrev_f32 v12,s13,v12\n"
"v_fma_f32 v12,v12,v11,s13\n"
AW "a79,v12\n"
AR "v12,a99\n"
"v_subrev_f32 v12,s13,v12\n"
"v_fma_f32 v12,v12,v11,s13\n"
AW "a99,v12\n"
AR "v12,a119\n"
"v_subrev_f32 v12,s13,v12\n"
"v_fma_f32 v12,v12,v11,s13\n"
AW "a119,v12\n"
AR "v12,a139\n"
"v_subrev_f32 v12,s13,v12\n"
"v_fma_f32 v12,v12,v11,s13\n"
AW "a139,v12\n"
AR "v12,a159\n"
"v_subrev_f32 v12,s13,v12\n"
"v_fma_f32 v12,v12,v11,s13\n"
AW "a159,v12\n"
AR "v12,a179\n"
"v_subrev_f32 v12,s13,v12\n"
"v_fma_f32 v12,v12,v11,s13\n"
AW "a179,v12\n"
AR "v12,a199\n"
"v_subrev_f32 v12,s13,v12\n"
"v_fma_f32 v12,v12,v11,s13\n"
AW "a199,v12\n"
AR "v12,a219\n"
"v_subrev_f32 v12,s13,v12\n"
"v_fma_f32 v12,v12,v11,s13\n"
AW "a219,v12\n"
AR "v12,a239\n"
"v_subrev_f32 v12,s13,v12\n"
"v_fma_f32 v12,v12,v11,s13\n"
AW "a239,v12\n"
"v_mul_f32 v61,v56,v11\n"
"v_sub_f32 v48,v48,v10\n"
"v_sub_f32 v49,v49,v10\n"
"v_sub_f32 v50,v50,v10\n"
"v_sub_f32 v51,v51,v10\n"
"v_sub_f32 v88,v88,v10\n"
"v_sub_f32 v89,v89,v10\n"
"v_sub_f32 v90,v90,v10\n"
"v_sub_f32 v91,v91,v10\n"
"v_sub_f32 v108,v108,v10\n"
"v_sub_f32 v109,v109,v10\n"
"v_sub_f32 v110,v110,v10\n"
"v_sub_f32 v111,v111,v10\n"
"v_sub_f32 v128,v128,v10\n"
"v_sub_f32 v129,v129,v10\n"
"v_sub_f32 v130,v130,v10\n"
"v_sub_f32 v131,v131,v10\n"
"v_exp_f32 v108,v108\n"
"v_exp_f32 v109,v109\n"
"v_exp_f32 v110,v110\n"
"v_exp_f32 v111,v111\n"
"v_exp_f32 v128,v128\n"
"v_exp_f32 v129,v129\n"
"v_exp_f32 v130,v130\n"
"v_exp_f32 v131,v131\n"
"v_add_f32 v61,v61,v108\n"
"v_add_f32 v61,v61,v109\n"
"v_add_f32 v61,v61,v110\n"
"v_add_f32 v61,v61,v111\n"
"v_add_f32 v61,v61,v128\n"
"v_add_f32 v61,v61,v129\n"
"v_add_f32 v61,v61,v130\n"
"v_add_f32 v61,v61,v131\n"
CV "v108,v108,v109\n"
CV "v109,v110,v111\n"
CV "v110,v128,v129\n"
CV "v111,v130,v131\n"
"s_nop 7\n"
"s_setpc_b64 s[52:53]\n"
".Lackspin_q:\n"
"s_cmp_eq_u32 s98,0\n"
"s_cbranch_scc1 .Lackok_q\n"
"s_sub_u32 s98,s98,1\n"
"s_sleep 8\n"
"v_bfe_u32 v14,v9,4,5\n"
"v_lshlrev_b32 v14,6,v14\n"
"s_lshl_b32 s58,s78,2\n"
"s_add_u32 s58,s58,s86\n"
"buffer_load_dword v10,v14,s[40:43],s58 offen offset:128 sc1\n"
"s_waitcnt vmcnt(0)\n"
"s_branch .Lack_q\n"
".Lcode1:\n"
".Ldone:\n"
    : "+{v0}"(tid), "+{s23}"(bid), "+{s[20:21]}"(ka)
    :
    : "v1", "v2", "v3", "v4", "v5", "v6", "v7", "v8", "v9", "v10", "v11", "v12", "v13", "v14", "v15", "v16", "v17", "v18", "v19", "v20", "v21", "v22", "v23", "v24", "v25", "v26", "v27", "v28", "v29", "v30", "v31", "v32", "v33", "v34", "v35", "v36", "v37", "v38", "v39", "v40", "v41", "v42", "v43", "v44", "v45", "v46", "v47", "v48", "v49", "v50", "v51", "v52", "v53", "v54", "v55", "v56", "v57", "v58", "v59", "v60", "v61", "v62", "v63", "v64", "v65", "v66", "v67", "v68", "v69", "v70", "v71", "v72", "v73", "v74", "v75", "v76", "v77", "v78", "v79", "v80", "v81", "v82", "v83", "v84", "v85", "v86", "v87", "v88", "v89", "v90", "v91", "v92", "v93", "v94", "v95", "v96", "v97", "v98", "v99", "v100", "v101", "v102", "v103", "v104", "v105", "v106", "v107", "v108", "v109", "v110", "v111", "v112", "v113", "v114", "v115", "v116", "v117", "v118", "v119", "v120", "v121", "v122", "v123", "v124", "v125", "v126", "v127", "v128", "v129", "v130", "v131", "v132", "v133", "v134", "v135", "v136", "v137", "v138", "v139", "v140", "v141", "v142", "v143", "v144", "v145", "v146", "v147", "v148", "v149", "v150", "v151", "v152", "v153", "v154", "v155", "v156", "v157", "v158", "v159", "v160", "v161", "v162", "v163", "v164", "v165", "v166", "v167", "v168", "v169", "v170", "v171", "v172", "v173", "v174", "v175", "v176", "v177", "v178", "v179", "v180", "v181", "v182", "v183", "v184", "v185", "v186", "v187", "v188", "v189", "v190", "v191", "v192", "v193", "v194", "v195", "v196", "v197", "v198", "v199", "v200", "v201", "v202", "v203", "v204", "v205", "v206", "v207", "v208", "v209", "v210", "v211", "v212", "v213", "v214", "v215", "v216", "v217", "v218", "v219", "v220", "v221", "v222", "v223", "v224", "v225", "v226", "v227", "v228", "v229", "v230", "v231", "v232", "v233", "v234", "v235", "v236", "v237", "v238", "v239", "v240", "v241", "v242", "v243", "v244", "v245", "v246", "v247", "v248", "v249", "v250", "v251", "v252", "v253", "v254", "v255", "a0", "a1", "a2", "a3", "a4", "a5", "a6", "a7", "a8", "a9", "a10", "a11", "a12", "a13", "a14", "a15", "a16", "a17", "a18", "a19", "a20", "a21", "a22", "a23", "a24", "a25", "a26", "a27", "a28", "a29", "a30", "a31", "a32", "a33", "a34", "a35", "a36", "a37", "a38", "a39", "a40", "a41", "a42", "a43", "a44", "a45", "a46", "a47", "a48", "a49", "a50", "a51", "a52", "a53", "a54", "a55", "a56", "a57", "a58", "a59", "a60", "a61", "a62", "a63", "a64", "a65", "a66", "a67", "a68", "a69", "a70", "a71", "a72", "a73", "a74", "a75", "a76", "a77", "a78", "a79", "a80", "a81", "a82", "a83", "a84", "a85", "a86", "a87", "a88", "a89", "a90", "a91", "a92", "a93", "a94", "a95", "a96", "a97", "a98", "a99", "a100", "a101", "a102", "a103", "a104", "a105", "a106", "a107", "a108", "a109", "a110", "a111", "a112", "a113", "a114", "a115", "a116", "a117", "a118", "a119", "a120", "a121", "a122", "a123", "a124", "a125", "a126", "a127", "a128", "a129", "a130", "a131", "a132", "a133", "a134", "a135", "a136", "a137", "a138", "a139", "a140", "a141", "a142", "a143", "a144", "a145", "a146", "a147", "a148", "a149", "a150", "a151", "a152", "a153", "a154", "a155", "a156", "a157", "a158", "a159", "a160", "a161", "a162", "a163", "a164", "a165", "a166", "a167", "a168", "a169", "a170", "a171", "a172", "a173", "a174", "a175", "a176", "a177", "a178", "a179", "a180", "a181", "a182", "a183", "a184", "a185", "a186", "a187", "a188", "a189", "a190", "a191", "a192", "a193", "a194", "a195", "a196", "a197", "a198", "a199", "a200", "a201", "a202", "a203", "a204", "a205", "a206", "a207", "a208", "a209", "a210", "a211", "a212", "a213", "a214", "a215", "a216", "a217", "a218", "a219", "a220", "a221", "a222", "a223", "a224", "a225", "a226", "a227", "a228", "a229", "a230", "a231", "a232", "a233", "a234", "a235", "a236", "a237", "a238", "a239", "a240", "a241", "a242", "a243", "a244", "a245", "a246", "a247", "a248", "a249", "a250", "a251", "a252", "a253", "a254", "a255", "s0", "s1", "s2", "s3", "s4", "s5", "s6", "s7", "s8", "s9", "s10", "s11", "s12", "s13", "s14", "s15", "s16", "s17", "s18", "s19", "s22", "s24", "s25", "s26", "s27", "s28", "s29", "s30", "s31", "s32", "s33", "s34", "s35", "s36", "s37", "s38", "s39", "s40", "s41", "s42", "s43", "s44", "s45", "s46", "s47", "s48", "s49", "s50", "s51", "s52", "s53", "s54", "s55", "s56", "s57", "s58", "s59", "s60", "s61", "s62", "s63", "s64", "s65", "s66", "s67", "s68", "s69", "s70", "s71", "s72", "s73", "s74", "s75", "s76", "s77", "s78", "s79", "s80", "s81", "s82", "s83", "s84", "s85", "s86", "s87", "s88", "s89", "s90", "s91", "s92", "s93", "s94", "s95", "s96", "s97", "s98", "s99", "s100", "s101", "vcc", "scc", "memory");
}

extern "C" void kernel_launch(void* const* d_in, const int* in_sizes, int n_in, void* d_out, int out_size, void* d_ws, size_t ws_size, hipStream_t stream) {
  Ptrs P;
  for (int br = 0; br < 2; ++br) { P.x[br] = d_in[5 * br]; P.k[br] = d_in[5 * br + 1]; P.v[br] = d_in[5 * br + 2]; P.wq[br] = d_in[5 * br + 3]; P.wo[br] = d_in[5 * br + 4]; }
  (void)hipFuncSetAttribute((const void*)fused_kernel, hipFuncAttributeMaxDynamicSharedMemorySize, LDS1);
  fused_kernel<<<256, 256, LDS1, stream>>>(P, (char*)d_ws, (char*)d_out);
}
```

```cpp
#include <hip/hip_runtime.h>
#include <hip/hip_bf16.h>

#define MF "v_mfma_f32_16x16x32_bf16 "
#define TR "ds_read_b64_tr_b16 "
#define AR "v_accvgpr_read_b32 "
#define AW "v_accvgpr_write_b32 "
#define CV "v_cvt_pk_bf16_f32 "
#define LD "buffer_load_dwordx4 "
constexpr unsigned LDS1 = 160 * 1024;

struct Ptrs { const void* x[2]; const void* k[2]; const void* v[2]; const void* wq[2]; const void* wo[2]; };

__global__ __launch_bounds__(256, 1) __attribute__((aligned(4096))) void fused_kernel(Ptrs P, char* __restrict__ ws, char* __restrict__ out) {
  unsigned tid = threadIdx.x, bid = blockIdx.x;
  unsigned long long ka = (unsigned long long)__builtin_amdgcn_kernarg_segment_ptr();
  asm volatile(
".p2align 5\n"
".Lcode0:\n"
"s_and_b32 s58,s23,4\n"
"s_lshl_b32 s58,s58,1\n"
"s_load_dwordx2 s[28:29],s[20:21],s58 offset:48\n"
"s_load_dwordx2 s[24:25],s[20:21],s58\n"
"s_load_dwordx2 s[32:33],s[20:21],s58 offset:16\n"
"s_load_dwordx2 s[36:37],s[20:21],s58 offset:32\n"
"s_load_dwordx2 s[48:49],s[20:21],s58 offset:64\n"
"s_memtime s[100:101]\n"
"s_load_dwordx2 s[40:41],s[20:21],0x50\n"
"s_load_dwordx2 s[44:45],s[20:21],0x58\n"
"v_readfirstlane_b32 s22,v0\n"
"v_and_b32 v9,63,v0\n"
"v_bfe_u32 v10,v0,3,3\n"
"v_and_b32 v11,7,v0\n"
"s_lshr_b32 s22,s22,6\n"
"s_or_b32 s22,s22,s58\n"
"s_and_b32 s58,s23,1\n"
"s_lshl_b32 s58,s58,3\n"
"s_lshr_b32 s59,s23,5\n"
"s_add_u32 s59,s59,s58\n"
"s_mul_i32 s59,s59,4112\n"
"s_or_b32 s22,s22,s59\n"
"s_and_b32 s59,s23,2\n"
"s_lshl_b32 s59,s59,1\n"
"s_bfe_u32 s60,s23,0x20003\n"
"s_or_b32 s59,s59,s60\n"
"s_lshl_b32 s60,s59,20\n"
"s_or_b32 s22,s22,s60\n"
"s_or_b32 s59,s59,s58\n"
"s_mul_i32 s59,s59,65792\n"
"s_xor_b32 s59,s59,524288\n"
"s_or_b32 s22,s22,s59\n"
"v_lshrrev_b32 v6,2,v0\n"
"v_and_b32 v6,4,v6\n"
"v_xor_b32 v6,v6,v11\n"
"v_lshlrev_b32 v6,4,v6\n"
"s_mov_b32 s58,12288\n"
"v_mad_u32_u24 v6,v10,s58,v6\n"
"v_bfe_u32 v4,v0,4,3\n"
"v_xor_b32 v4,v4,v11\n"
"v_lshlrev_b32 v4,4,v4\n"
"s_mov_b32 s58,1280\n"
"v_mad_u32_u24 v4,v10,s58,v4\n"
"v_lshlrev_b32 v9,4,v9\n"
"s_mov_b32 s26,3276800\n"
"s_mov_b32 s27,0x00020000\n"
"s_mov_b32 s30,7864320\n"
"s_mov_b32 s31,0x00020000\n"
"s_mov_b32 s34,0\n"
"s_mov_b32 s35,0x00020000\n"
"s_mov_b32 s38,0\n"
"s_mov_b32 s39,0x00020000\n"
"s_mov_b32 s42,268435456\n"
"s_mov_b32 s43,0x00020000\n"
"s_mov_b32 s46,6553600\n"
"s_mov_b32 s47,0x00020000\n"
"s_mov_b32 s50,7864320\n"
"s_mov_b32 s51,0x00020000\n"
"s_mov_b32 s98,20000\n"
"s_bfe_u32 s56,s22,0x20000\n"
"s_bfe_u32 s72,s22,0x10003\n"
"s_lshr_b32 s73,s56,1\n"
"s_and_b32 s74,s56,1\n"
"s_mul_i32 s62,s56,4096\n"
"s_mul_i32 s63,s56,5120\n"
"s_add_u32 s63,s63,98304\n"
"s_bfe_u32 s58,s22,0x40004\n"
"s_bfe_u32 s59,s22,0x40008\n"
"s_mul_i32 s68,s58,204800\n"
"s_mul_i32 s60,s56,51200\n"
"s_add_u32 s68,s68,s60\n"
"s_lshl_b32 s59,s59,1\n"
"s_add_u32 s59,s59,s73\n"
"s_mul_i32 s66,s59,384\n"
"s_mul_i32 s60,s74,393216\n"
"s_add_u32 s66,s66,s60\n"
"s_lshr_b32 s58,s22,6\n"
"s_xor_b32 s58,s58,s22\n"
"s_bfe_u32 s58,s58,0x10004\n"
"s_mul_i32 s81,s58,640\n"
"s_mul_i32 s60,s81,6144\n"
"s_add_u32 s66,s66,s60\n"
"v_xor_b32 v19,64,v4\n"
"v_add_u32 v16,97280,v6\n"
"v_add_u32 v17,194560,v6\n"
"v_add_u32 v18,291840,v6\n"
"s_waitcnt lgkmcnt(0)\n"
"s_mov_b32 s80,s100\n"
"s_mov_b32 s87,2\n"
"s_add_u32 m0,s62,0\n"
"s_add_u32 s82,s66,0\n"
"s_sub_u32 s15,s82,7864320\n"
"s_min_u32 s82,s82,s15\n"
LD "v6,s[28:31],s82 offen lds\n"
LD "v16,s[28:31],s82 offen offset:1024 lds\n"
LD "v17,s[28:31],s82 offen offset:2048 lds\n"
LD "v18,s[28:31],s82 offen offset:3072 lds\n"
"s_add_u32 s14,s81,0\n"
"s_sub_u32 s15,s14,1280\n"
"s_min_u32 s14,s14,s15\n"
"s_add_u32 s14,s14,s68\n"
"s_add_u32 s84,s14,0\n"
"s_add_u32 m0,s63,0\n"
"s_nop 0\n"
LD "v4,s[24:27],s84 offen lds\n"
"s_add_u32 s84,s14,9216\n"
LD "v19,s[24:27],s84 offen offset:1024 lds\n"
"s_add_u32 s84,s14,18432\n"
LD "v4,s[24:27],s84 offen offset:2048 lds\n"
"s_add_u32 s84,s14,27648\n"
LD "v19,s[24:27],s84 offen offset:3072 lds\n"
"s_add_u32 s84,s14,40960\n"
"s_add_u32 m0,s63,4096\n"
"s_nop 0\n"
LD "v4,s[24:27],s84 offen lds\n"
"s_lshr_b32 s58,s23,3\n"
"s_mul_i32 s59,s58,7\n"
"s_and_b32 s60,s23,7\n"
"s_mul_i32 s60,s60,3\n"
"s_add_u32 s59,s59,s60\n"
"s_and_b32 s59,s59,15\n"
"s_lshl_b32 s59,s59,8\n"
"s_or_b32 s58,s58,s59\n"
"s_and_b32 s59,s23,3\n"
"s_lshl_b32 s59,s59,5\n"
"s_or_b32 s58,s58,s59\n"
"s_bfe_u32 s59,s23,0x10001\n"
"s_lshl_b32 s59,s59,12\n"
"s_or_b32 s58,s58,s59\n"
"s_lshl_b32 s23,s23,16\n"
"s_or_b32 s23,s23,s58\n"
"v_bfe_u32 v20,v0,4,2\n"
"v_mul_u32_u24 v21,384,v10\n"
"v_xor_b32_e64 v22,v11,v20\n"
"v_lshl_add_u32 v7,v22,4,v21\n"
"s_add_u32 m0,s62,16384\n"
"s_add_u32 s82,s66,128\n"
"s_sub_u32 s15,s82,7864320\n"
"s_min_u32 s82,s82,s15\n"
LD "v6,s[28:31],s82 offen lds\n"
"s_lshl_b32 s58,s74,6\n"
"v_xor_b32 v7,s58,v7\n"
"v_lshlrev_b32 v22,1,v20\n"
"v_xor_b32 v22,v11,v22\n"
"v_lshl_add_u32 v8,v22,4,v21\n"
"v_bfe_u32 v21,v0,1,3\n"
"v_xor_b32 v21,v21,v20\n"
"v_lshlrev_b32_e64 v21,4,v21\n"
"v_lshl_add_u32 v21,v11,7,v21\n"
"v_bfe_u32 v22,v0,3,1\n"
"s_mul_i32 s58,s73,12288\n"
"v_mul_u32_u24 v23,3072,v22\n"
"v_add3_u32 v1,v23,v21,s58\n"
"s_mul_i32 s59,s74,10240\n"
"s_add_u32 s59,s59,98304\n"
"v_lshl_add_u32 v2,v22,10,v21\n"
"v_add_u32 v2,s59,v2\n"
"v_bfe_u32 v21,v0,2,3\n"
"v_lshlrev_b32 v21,7,v21\n"
"v_bfe_u32 v23,v0,3,2\n"
LD "v16,s[28:31],s82 offen offset:1024 lds\n"
"v_lshl_add_u32 v21,v23,5,v21\n"
"v_and_b32_e64 v23,3,v0\n"
"v_lshl_add_u32 v21,v23,3,v21\n"
"v_bfe_u32 v23,v0,5,1\n"
"v_mul_u32_u24 v3,3072,v23\n"
"v_add3_u32 v3,v3,v21,s58\n"
"s_bfe_u32 s58,s22,0x40004\n"
"s_mul_i32 s58,s58,409600\n"
"s_mul_i32 s59,s72,1280\n"
"s_add_u32 s58,s58,s59\n"
"v_and_b32 v21,15,v0\n"
"v_mul_u32_u24 v21,2560,v21\n"
"v_lshl_add_u32 v21,v23,4,v21\n"
"v_bfe_u32 v23,v0,4,1\n"
"v_lshl_add_u32 v21,v23,5,v21\n"
"v_add_u32 v12,s58,v21\n"
"v_bfe_u32 v21,v0,2,2\n"
"v_lshlrev_b32 v21,7,v21\n"
"v_lshl_add_u32 v21,v20,10,v21\n"
"v_lshl_add_u32 v21,v22,6,v21\n"
LD "v17,s[28:31],s82 offen offset:2048 lds\n"
"v_and_b32_e64 v23,3,v0\n"
"v_lshl_add_u32 v21,v23,4,v21\n"
"s_lshl_b32 s58,s73,13\n"
"v_add_u32 v0,s58,v21\n"
"s_getpc_b64 s[52:53]\n"
".Lcts:\n"
"s_sub_u32 s52,s52,.Lcts-.Lcode0\n"
"s_subb_u32 s53,s53,0\n"
"s_bfe_u32 s58,s23,0x20000\n"
"s_lshl_b32 s58,s58,2\n"
"s_add_u32 s58,s58,s56\n"
"s_lshl_b32 s58,s58,13\n"
"v_lshlrev_b32 v15,3,v9\n"
"v_add_u32_e64 v15,s58,v15\n"
"v_min_u32 v15,.Lcode1-.Lcode0,v15\n"
"s_bfe_u32 s58,s22,0x4000c\n"
"s_bfe_u32 s59,s22,0x40010\n"
"s_mul_i32 s69,s58,204800\n"
"s_mul_i32 s60,s56,51200\n"
LD "v18,s[28:31],s82 offen offset:3072 lds\n"
"s_add_u32 s69,s69,s60\n"
"s_lshl_b32 s59,s59,1\n"
"s_add_u32 s59,s59,s73\n"
"s_mul_i32 s67,s59,384\n"
"s_mul_i32 s60,s74,393216\n"
"s_add_u32 s67,s67,s60\n"
"s_mul_i32 s60,s81,6144\n"
"s_add_u32 s67,s67,s60\n"
"s_bfe_u32 s58,s22,0x40004\n"
"s_bfe_u32 s59,s22,0x40008\n"
"s_lshl_b32 s59,s59,1\n"
"s_add_u32 s88,s59,s74\n"
"s_mul_i32 s88,s88,245760\n"
"s_add_u32 s59,s59,s73\n"
"s_lshl_b32 s70,s58,5\n"
"s_add_u32 s70,s70,s59\n"
"s_mul_i32 s70,s70,196608\n"
"s_bfe_u32 s58,s22,0x4000c\n"
"s_bfe_u32 s59,s22,0x40010\n"
"s_lshl_b32 s59,s59,1\n"
"s_add_u32 m0,s62,32768\n"
"s_add_u32 s82,s66,256\n"
"s_sub_u32 s15,s82,7864320\n"
"s_min_u32 s82,s82,s15\n"
LD "v6,s[28:31],s82 offen lds\n"
"s_add_u32 s89,s59,s74\n"
"s_mul_i32 s89,s89,245760\n"
"s_add_u32 s59,s59,s73\n"
"s_lshl_b32 s71,s58,5\n"
"s_add_u32 s71,s71,s59\n"
"s_mul_i32 s71,s71,196608\n"
"s_bitcmp1_b32 s23,12\n"
"s_cselect_b32 s58,s71,s70\n"
"s_cselect_b32 s71,s70,s71\n"
"s_mov_b32 s70,s58\n"
"s_cselect_b32 s58,s89,s88\n"
"s_cselect_b32 s89,s88,s89\n"
"s_mov_b32 s88,s58\n"
"s_cselect_b32 s69,98,1\n"
"s_mov_b32 s54,1037384597\n"
"s_mov_b32 s55,1037384597\n"
"s_bfe_u32 s58,s22,0x40004\n"
"s_lshl_b32 s59,s72,4\n"
"s_add_u32 s59,s59,s58\n"
"s_lshl_b32 s86,s59,12\n"
LD "v16,s[28:31],s82 offen offset:1024 lds\n"
"s_add_u32 s86,s86,100663296\n"
"s_mul_i32 s93,s59,1638400\n"
"s_add_u32 s93,s93,33554432\n"
"s_bfe_u32 s58,s22,0x30014\n"
"s_lshl_b32 s78,s58,1\n"
"s_add_u32 s78,s78,s74\n"
"s_lshl_b32 s94,s78,2\n"
"s_add_u32 s94,s94,s86\n"
"s_lshl_b32 s92,s58,2\n"
"s_lshr_b32 s92,0x31752064,s92\n"
"s_and_b32 s92,s92,7\n"
"s_lshl_b32 s92,s92,2\n"
"s_add_u32 s92,s92,s56\n"
"s_mul_i32 s91,s58,204800\n"
"s_add_u32 s91,s91,s93\n"
"s_mul_i32 s75,s56,30720\n"
"s_add_u32 s75,s75,s91\n"
"s_add_u32 s75,s75,0\n"
"s_mul_i32 s58,s74,5120\n"
"s_add_u32 s91,s91,s58\n"
LD "v17,s[28:31],s82 offen offset:2048 lds\n"
"s_mov_b32 s90,0xf0000000\n"
"s_mov_b32 s77,0xf0000000\n"
"s_mov_b32 s85,0xf0000000\n"
AW "a240,v0\n"
AW "a244,v1\n"
AW "a241,v2\n"
AW "a245,v3\n"
AW "a242,v4\n"
AW "a243,v6\n"
AW "a252,v12\n"
"s_mov_b32 s57,0\n"
"s_mov_b32 s83,0\n"
LD "v18,s[28:31],s82 offen offset:3072 lds\n"
"global_load_dword a249,v15,s[52:53]\n"
AR "v0,a240\n"
AR "v2,a241\n"
AR "v4,a242\n"
AR "v6,a243\n"
"v_xor_b32 v1,64,v0\n"
"v_xor_b32 v3,64,v2\n"
"v_xor_b32 v5,64,v4\n"
"v_add_u32 v11,97280,v6\n"
"v_add_u32 v12,194560,v6\n"
"v_add_u32 v13,291840,v6\n"
"s_mov_b64 vcc,0\n"
"s_waitcnt vmcnt(9) lgkmcnt(0)\n"
"s_barrier\n"
TR "a[200:201],v0\n"
TR "a[202:203],v0 offset:512\n"
TR "a[204:205],v0 offset:4096\n"
TR "a[206:207],v0 offset:4608\n"
"ds_read_b128 a[60:63],v2\n"
"ds_read_b128 a[100:103],v3\n"
"ds_read_b128 a[64:67],v2 offset:2048\n"
"ds_read_b128 a[104:107],v3 offset:2048\n"
"ds_read_b128 a[68:71],v2 offset:4096\n"
"ds_read_b128 a[108:111],v3 offset:4096\n"
"ds_read_b128 a[72:75],v2 offset:6144\n"
"ds_read_b128 a[112:115],v3 offset:6144\n"
"ds_read_b128 a[76:79],v2 offset:8192\n"
"ds_read_b128 a[116:119],v3 offset:8192\n"
TR "a[208:209],v0 offset:8\n"
TR "a[210:211],v0 offset:520\n"
TR "a[212:213],v0 offset:4104\n"
TR "a[214:215],v0 offset:4616\n"
"s_waitcnt lgkmcnt(0)\n"
MF "v[16:19],a[200:203],a[60:63],0\n"
TR "a[216:217],v1\n"
MF "v[16:19],a[204:207],a[100:103],v[16:19]\n"
"s_add_u32 s14,s81,128\n"
TR "a[218:219],v1 offset:512\n"
MF "v[24:27],a[204:207],a[104:107],0\n"
"s_sub_u32 s15,s14,1280\n"
"v_lshrrev_b32 v15,5,v9\n"
"v_min_u32 v15,11,v15\n"
MF "v[24:27],a[200:203],a[64:67],v[24:27]\n"
"s_min_u32 s14,s14,s15\n"
TR "a[220:221],v1 offset:4096\n"
"v_mul_u32_u24 v15,160,v15\n"
MF "v[32:35],a[200:203],a[68:71],0\n"
"s_add_u32 s14,s14,s68\n"
TR "a[222:223],v1 offset:4608\n"
"v_and_or_b32 v15,v9,16,v15\n"
MF "v[32:35],a[204:207],a[108:111],v[32:35]\n"
"s_add_u32 s84,s14,0\n"
"v_max3_f32 v220,|v16|,|v17|,|v18|\n"
"v_max_f32_e64 v220,v220,|v19|\n"
MF "v[40:43],a[204:207],a[112:115],0\n"
"s_add_u32 m0,s63,20480\n"
"v_readlane_b32 s12,v220,3\n"
"v_readlane_b32 s6,v220,22\n"
MF "v[40:43],a[200:203],a[72:75],v[40:43]\n"
"s_min_u32 s12,s12,s6\n"
"v_readlane_b32 s7,v220,41\n"
"v_readlane_b32 s8,v220,60\n"
MF "v[48:51],a[200:203],a[76:79],0\n"
"s_min_u32 s7,s7,s8\n"
"v_lshlrev_b32 v15,3,v15\n"
MF "v[48:51],a[204:207],a[116:119],v[48:51]\n"
LD "v4,s[24:27],s84 offen lds\n"
MF "v[52:55],a[212:215],a[116:119],0\n"
"s_min_u32 s12,s12,s7\n"
TR "a[200:201],v1 offset:8\n"
MF "v[52:55],a[208:211],a[76:79],v[52:55]\n"
"s_and_b32 s12,s12,0x7f800000\n"
TR "a[202:203],v1 offset:520\n"
MF "v[44:47],a[208:211],a[72:75],0\n"
"s_sub_u32 s12,s12,0x14000000\n"
MF "v[44:47],a[212:215],a[112:115],v[44:47]\n"
"s_cmp_lt_u32 s12,0x50000000\n"
"s_cselect_b32 s12,s12,0xe8400000\n"
TR "a[204:205],v1 offset:4104\n"
MF "v[36:39],a[212:215],a[108:111],0\n"
"s_add_u32 s12,s12,0x17c00000\n"
TR "a[206:207],v1 offset:4616\n"
MF "v[36:39],a[208:211],a[68:71],v[36:39]\n"
"s_bfe_u32 s60,s23,0x70000\n"
"v_mov_b32 v220,s12\n"
"v_mov_b32 v221,s12\n"
MF "v[28:31],a[208:211],a[64:67],0\n"
"s_add_u32 s84,s14,9216\n"
"v_mov_b32 v222,s12\n"
"v_mov_b32 v223,s12\n"
MF "v[28:31],a[212:215],a[104:107],v[28:31]\n"
LD "v5,s[24:27],s84 offen offset:1024 lds\n"
MF "v[20:23],a[212:215],a[100:103],0\n"
"s_lshl_b32 s60,s60,2\n"
MF "v[20:23],a[208:211],a[60:63],v[20:23]\n"
"s_waitcnt vmcnt(7) lgkmcnt(0)\n"
"s_barrier\n"
MF "v[56:59],a[216:219],a[60:63],v[220:223]\n"
"s_add_u32 s60,s60,s56\n"
TR "a[208:209],v0 offset:16384\n"
MF "v[56:59],a[220:223],a[100:103],v[56:59]\n"
"s_add_u32 s84,s14,18432\n"
TR "a[210:211],v0 offset:16896\n"
MF "v[64:67],a[220:223],a[104:107],v[220:223]\n"
LD "v4,s[24:27],s84 offen offset:2048 lds\n"
MF "v[64:67],a[216:219],a[64:67],v[64:67]\n"
"s_mul_i32 s60,s60,15360\n"
TR "a[212:213],v0 offset:20480\n"
MF "v[72:75],a[216:219],a[68:71],v[220:223]\n"
TR "a[214:215],v0 offset:20992\n"
MF "v[72:75],a[220:223],a[108:111],v[72:75]\n"
MF "v[80:83],a[220:223],a[112:115],v[220:223]\n"
"s_add_u32 s84,s14,27648\n"
MF "v[80:83],a[216:219],a[72:75],v[80:83]\n"
LD "v5,s[24:27],s84 offen offset:3072 lds\n"
MF "v[88:91],a[216:219],a[76:79],v[220:223]\n"
MF "v[88:91],a[220:223],a[116:119],v[88:91]\n"
MF "v[92:95],a[204:207],a[116:119],v[220:223]\n"
"s_add_u32 s84,s14,40960\n"
TR "a[216:217],v0 offset:16392\n"
MF "v[92:95],a[200:203],a[76:79],v[92:95]\n"
"s_add_u32 m0,s63,24576\n"
TR "a[218:219],v0 offset:16904\n"
MF "v[84:87],a[200:203],a[72:75],v[220:223]\n"
LD "v4,s[24:27],s84 offen lds\n"
MF "v[84:87],a[204:207],a[112:115],v[84:87]\n"
"s_add_u32 m0,s62,49152\n"
TR "a[220:221],v0 offset:20488\n"
MF "v[76:79],a[204:207],a[108:111],v[220:223]\n"
"s_add_u32 s82,s66,786432\n"
TR "a[222:223],v0 offset:21000\n"
MF "v[76:79],a[200:203],a[68:71],v[76:79]\n"
"s_sub_u32 s15,s82,7864320\n"
MF "v[68:71],a[200:203],a[64:67],v[220:223]\n"
"s_min_u32 s82,s82,s15\n"
MF "v[68:71],a[204:207],a[104:107],v[68:71]\n"
LD "v6,s[28:31],s82 offen lds\n"
MF "v[60:63],a[204:207],a[100:103],v[220:223]\n"
MF "v[60:63],a[200:203],a[60:63],v[60:63]\n"
"s_waitcnt lgkmcnt(4)\n"
MF "v[96:99],a[208:211],a[60:63],v[220:223]\n"
TR "a[200:201],v1 offset:16384\n"
MF "v[96:99],a[212:215],a[100:103],v[96:99]\n"
TR "a[202:203],v1 offset:16896\n"
MF "v[104:107],a[212:215],a[104:107],v[220:223]\n"
LD "v11,s[28:31],s82 offen offset:1024 lds\n"
MF "v[104:107],a[208:211],a[64:67],v[104:107]\n"
TR "a[204:205],v1 offset:20480\n"
MF "v[112:115],a[208:211],a[68:71],v[220:223]\n"
TR "a[206:207],v1 offset:20992\n"
MF "v[112:115],a[212:215],a[108:111],v[112:115]\n"
MF "v[120:123],a[212:215],a[112:115],v[220:223]\n"
MF "v[120:123],a[208:211],a[72:75],v[120:123]\n"
LD "v12,s[28:31],s82 offen offset:2048 lds\n"
MF "v[128:131],a[208:211],a[76:79],v[220:223]\n"
MF "v[128:131],a[212:215],a[116:119],v[128:131]\n"
"s_waitcnt lgkmcnt(4)\n"
MF "v[132:135],a[220:223],a[116:119],v[220:223]\n"
TR "a[208:209],v1 offset:16392\n"
MF "v[132:135],a[216:219],a[76:79],v[132:135]\n"
TR "a[210:211],v1 offset:16904\n"
MF "v[124:127],a[216:219],a[72:75],v[220:223]\n"
LD "v13,s[28:31],s82 offen offset:3072 lds\n"
MF "v[124:127],a[220:223],a[112:115],v[124:127]\n"
"s_add_u32 m0,s62,65536\n"
TR "a[212:213],v1 offset:20488\n"
MF "v[116:119],a[220:223],a[108:111],v[220:223]\n"
"s_add_u32 s82,s66,786560\n"
TR "a[214:215],v1 offset:21000\n"
MF "v[116:119],a[216:219],a[68:71],v[116:119]\n"
"s_sub_u32 s15,s82,7864320\n"
MF "v[108:111],a[216:219],a[64:67],v[220:223]\n"
"s_min_u32 s82,s82,s15\n"
MF "v[108:111],a[220:223],a[104:107],v[108:111]\n"
LD "v6,s[28:31],s82 offen lds\n"
MF "v[100:103],a[220:223],a[100:103],v[220:223]\n"
MF "v[100:103],a[216:219],a[60:63],v[100:103]\n"
"s_waitcnt vmcnt(5) lgkmcnt(0)\n"
"s_barrier\n"
MF "v[136:139],a[200:203],a[60:63],v[220:223]\n"
TR "a[216:217],v0 offset:32768\n"
MF "v[136:139],a[204:207],a[100:103],v[136:139]\n"
TR "a[218:219],v0 offset:33280\n"
MF "v[144:147],a[204:207],a[104:107],v[220:223]\n"
"ds_read_b128 a[140:143],v2 offset:20480\n"
MF "v[144:147],a[200:203],a[64:67],v[144:147]\n"
TR "a[220:221],v0 offset:36864\n"
MF "v[152:155],a[200:203],a[68:71],v[220:223]\n"
TR "a[222:223],v0 offset:37376\n"
MF "v[152:155],a[204:207],a[108:111],v[152:155]\n"
LD "v11,s[28:31],s82 offen offset:1024 lds\n"
MF "v[160:163],a[204:207],a[112:115],v[220:223]\n"
"ds_read_b128 a[180:183],v3 offset:20480\n"
MF "v[160:163],a[200:203],a[72:75],v[160:163]\n"
MF "v[168:171],a[200:203],a[76:79],v[220:223]\n"
MF "v[168:171],a[204:207],a[116:119],v[168:171]\n"
LD "v12,s[28:31],s82 offen offset:2048 lds\n"
MF "v[172:175],a[212:215],a[116:119],v[220:223]\n"
TR "a[200:201],v0 offset:32776\n"
MF "v[172:175],a[208:211],a[76:79],v[172:175]\n"
TR "a[202:203],v0 offset:33288\n"
MF "v[164:167],a[208:211],a[72:75],v[220:223]\n"
"ds_read_b128 a[144:147],v2 offset:22528\n"
MF "v[164:167],a[212:215],a[112:115],v[164:167]\n"
TR "a[204:205],v0 offset:36872\n"
MF "v[156:159],a[212:215],a[108:111],v[220:223]\n"
TR "a[206:207],v0 offset:37384\n"
MF "v[156:159],a[208:211],a[68:71],v[156:159]\n"
LD "v13,s[28:31],s82 offen offset:3072 lds\n"
MF "v[148:151],a[208:211],a[64:67],v[220:223]\n"
"ds_read_b128 a[184:187],v3 offset:22528\n"
MF "v[148:151],a[212:215],a[104:107],v[148:151]\n"
"s_add_u32 m0,s62,81920\n"
MF "v[140:143],a[212:215],a[100:103],v[220:223]\n"
"s_add_u32 s82,s66,786688\n"
"s_sub_u32 s15,s82,7864320\n"
"s_min_u32 s82,s82,s15\n"
MF "v[140:143],a[208:211],a[60:63],v[140:143]\n"
"s_waitcnt lgkmcnt(7)\n"
LD "v6,s[28:31],s82 offen lds\n"
MF "v[176:179],a[216:219],a[60:63],v[220:223]\n"
TR "a[208:209],v1 offset:32768\n"
MF "v[176:179],a[220:223],a[100:103],v[176:179]\n"
TR "a[210:211],v1 offset:33280\n"
MF "v[184:187],a[220:223],a[104:107],v[220:223]\n"
"ds_read_b128 a[148:151],v2 offset:24576\n"
MF "v[184:187],a[216:219],a[64:67],v[184:187]\n"
TR "a[212:213],v1 offset:36864\n"
MF "v[192:195],a[216:219],a[68:71],v[220:223]\n"
TR "a[214:215],v1 offset:37376\n"
MF "v[192:195],a[220:223],a[108:111],v[192:195]\n"
LD "v11,s[28:31],s82 offen offset:1024 lds\n"
MF "v[200:203],a[220:223],a[112:115],v[220:223]\n"
"ds_read_b128 a[188:191],v3 offset:24576\n"
MF "v[200:203],a[216:219],a[72:75],v[200:203]\n"
MF "v[208:211],a[216:219],a[76:79],v[220:223]\n"
MF "v[208:211],a[220:223],a[116:119],v[208:211]\n"
"s_waitcnt lgkmcnt(7)\n"
LD "v12,s[28:31],s82 offen offset:2048 lds\n"
MF "v[212:215],a[204:207],a[116:119],v[220:223]\n"
TR "a[216:217],v1 offset:32776\n"
MF "v[212:215],a[200:203],a[76:79],v[212:215]\n"
TR "a[218:219],v1 offset:33288\n"
MF "v[204:207],a[200:203],a[72:75],v[220:223]\n"
"ds_read_b128 a[152:155],v2 offset:26624\n"
MF "v[204:207],a[204:207],a[112:115],v[204:207]\n"
TR "a[220:221],v1 offset:36872\n"
MF "v[196:199],a[204:207],a[108:111],v[220:223]\n"
TR "a[222:223],v1 offset:37384\n"
MF "v[196:199],a[200:203],a[68:71],v[196:199]\n"
LD "v13,s[28:31],s82 offen offset:3072 lds\n"
MF "v[188:191],a[200:203],a[64:67],v[220:223]\n"
"v_add_u32 v0,0xc000,v0\n"
MF "v[188:191],a[204:207],a[104:107],v[188:191]\n"
"v_add_u32 v1,0xc000,v1\n"
MF "v[180:183],a[204:207],a[100:103],v[220:223]\n"
"s_add_u32 s14,s81,256\n"
"s_mov_b64 exec,0\n"
"s_bitcmp1_b32 s23,12\n"
"s_cbranch_scc1 .Lisy1\n"
"s_cmp_eq_u32 s56,0\n"
"s_cbranch_scc1 .Lpg8b\n"
"s_cmp_eq_u32 s56,1\n"
"s_cbranch_scc1 .Lpg9b\n"
"s_cmp_eq_u32 s56,2\n"
"s_cbranch_scc1 .Lpgt0ex_b\n"
"s_branch .Lpg10b\n"
".Lisy1:\n"
"s_cmp_eq_u32 s56,0\n"
"s_cbranch_scc1 .Lpgq0_c\n"
"s_cmp_eq_u32 s56,1\n"
"s_cbranch_scc1 .Lpg4c\n"
"s_cmp_eq_u32 s56,2\n"
"s_cbranch_scc1 .Lpg5c\n"
"s_branch .Lpg6c\n"
".Lisr1:\n"
"s_mov_b64 exec,-1\n"
".Lisx1:\n"
MF "v[180:183],a[200:203],a[60:63],v[180:183]\n"
"s_waitcnt vmcnt(8) lgkmcnt(0)\n"
"s_barrier\n"
MF "v[216:219],a[208:211],a[60:63],v[220:223]\n"
"s_sub_u32 s15,s14,1280\n"
TR "a[200:201],v0\n"
MF "v[216:219],a[212:215],a[100:103],v[216:219]\n"
"s_min_u32 s14,s14,s15\n"
TR "a[202:203],v0 offset:512\n"
MF "v[224:227],a[212:215],a[104:107],v[220:223]\n"
"s_add_u32 s14,s14,s68\n"
"ds_read_b128 a[192:195],v3 offset:26624\n"
MF "v[224:227],a[208:211],a[64:67],v[224:227]\n"
"s_add_u32 s84,s14,0\n"
TR "a[204:205],v0 offset:4096\n"
MF "v[232:235],a[208:211],a[68:71],v[220:223]\n"
"s_add_u32 m0,s63,40960\n"
TR "a[206:207],v0 offset:4608\n"
MF "v[232:235],a[212:215],a[108:111],v[232:235]\n"
LD "v4,s[24:27],s84 offen lds\n"
MF "v[240:243],a[212:215],a[112:115],v[220:223]\n"
"ds_read_b128 a[156:159],v2 offset:28672\n"
MF "v[240:243],a[208:211],a[72:75],v[240:243]\n"
"ds_read_b128 a[196:199],v3 offset:28672\n"
MF "v[248:251],a[208:211],a[76:79],v[220:223]\n"
"s_add_u32 s84,s14,9216\n"
MF "v[248:251],a[212:215],a[116:119],v[248:251]\n"
LD "v5,s[24:27],s84 offen offset:1024 lds\n"
MF "v[252:255],a[220:223],a[116:119],v[220:223]\n"
TR "a[208:209],v0 offset:8\n"
MF "v[252:255],a[216:219],a[76:79],v[252:255]\n"
TR "a[210:211],v0 offset:520\n"
MF "v[244:247],a[216:219],a[72:75],v[220:223]\n"
MF "v[244:247],a[220:223],a[112:115],v[244:247]\n"
TR "a[212:213],v0 offset:4104\n"
MF "v[236:239],a[220:223],a[108:111],v[220:223]\n"
"s_add_u32 s84,s14,18432\n"
TR "a[214:215],v0 offset:4616\n"
MF "v[236:239],a[216:219],a[68:71],v[236:239]\n"
LD "v4,s[24:27],s84 offen offset:2048 lds\n"
MF "v[228:231],a[216:219],a[64:67],v[220:223]\n"
MF "v[228:231],a[220:223],a[104:107],v[228:231]\n"
MF "v[220:223],a[220:223],a[100:103],v[220:223]\n"
".Lpg1a:\n"
"s_cbranch_execz .Lpg7a\n"
"s_add_u32 s84,s14,27648\n"
MF "v[220:223],a[216:219],a[60:63],v[220:223]\n"
"s_waitcnt lgkmcnt(4)\n"
LD "v5,s[24:27],s84 offen offset:3072 lds\n"
MF "v[16:19],a[200:203],a[140:143],v[16:19]\n"
TR "a[216:217],v1\n"
MF "v[16:19],a[204:207],a[180:183],v[16:19]\n"
TR "a[218:219],v1 offset:512\n"
MF "v[24:27],a[204:207],a[184:187],v[24:27]\n"
MF "v[24:27],a[200:203],a[144:147],v[24:27]\n"
"s_add_u32 s84,s14,40960\n"
TR "a[220:221],v1 offset:4096\n"
MF "v[32:35],a[200:203],a[148:151],v[32:35]\n"
"s_add_u32 m0,s63,45056\n"
TR "a[222:223],v1 offset:4608\n"
MF "v[32:35],a[204:207],a[188:191],v[32:35]\n"
LD "v4,s[24:27],s84 offen lds\n"
MF "v[40:43],a[204:207],a[192:195],v[40:43]\n"
MF "v[40:43],a[200:203],a[152:155],v[40:43]\n"
"s_add_u32 m0,s62,0\n"
MF "v[48:51],a[200:203],a[156:159],v[48:51]\n"
"s_add_u32 s82,s66,1572864\n"
"s_sub_u32 s15,s82,7864320\n"
"s_min_u32 s82,s82,s15\n"
MF "v[48:51],a[204:207],a[196:199],v[48:51]\n"
"s_waitcnt lgkmcnt(4)\n"
LD "v6,s[28:31],s82 offen lds\n"
MF "v[52:55],a[212:215],a[196:199],v[52:55]\n"
TR "a[200:201],v1 offset:8\n"
MF "v[52:55],a[208:211],a[156:159],v[52:55]\n"
TR "a[202:203],v1 offset:520\n"
MF "v[44:47],a[208:211],a[152:155],v[44:47]\n"
MF "v[44:47],a[212:215],a[192:195],v[44:47]\n"
TR "a[204:205],v1 offset:4104\n"
MF "v[36:39],a[212:215],a[188:191],v[36:39]\n"
TR "a[206:207],v1 offset:4616\n"
MF "v[36:39],a[208:211],a[148:151],v[36:39]\n"
LD "v11,s[28:31],s82 offen offset:1024 lds\n"
MF "v[28:31],a[208:211],a[144:147],v[28:31]\n"
MF "v[28:31],a[212:215],a[184:187],v[28:31]\n"
MF "v[20:23],a[212:215],a[180:183],v[20:23]\n"
LD "v12,s[28:31],s82 offen offset:2048 lds\n"
MF "v[20:23],a[208:211],a[140:143],v[20:23]\n"
"s_waitcnt vmcnt(12) lgkmcnt(0)\n"
"s_barrier\n"
"v_bfe_u32 v14,v9,4,4\n"
"v_lshlrev_b32 v14,2,v14\n"
"buffer_load_dword v10,v14,s[40:43],s86 offen sc1\n"
".Lhs2:\n"
MF "v[56:59],a[216:219],a[140:143],v[56:59]\n"
TR "a[208:209],v0 offset:16384\n"
MF "v[56:59],a[220:223],a[180:183],v[56:59]\n"
TR "a[210:211],v0 offset:16896\n"
MF "v[64:67],a[220:223],a[184:187],v[64:67]\n"
LD "v13,s[28:31],s82 offen offset:3072 lds\n"
MF "v[64:67],a[216:219],a[144:147],v[64:67]\n"
TR "a[212:213],v0 offset:20480\n"
MF "v[72:75],a[216:219],a[148:151],v[72:75]\n"
TR "a[214:215],v0 offset:20992\n"
MF "v[72:75],a[220:223],a[188:191],v[72:75]\n"
"s_add_u32 s14,s81,384\n"
"s_sub_u32 s15,s14,1280\n"
"s_min_u32 s14,s14,s15\n"
MF "v[80:83],a[220:223],a[192:195],v[80:83]\n"
"s_add_u32 s14,s14,s68\n"
"s_add_u32 s84,s14,0\n"
"s_add_u32 m0,s63,0\n"
MF "v[80:83],a[216:219],a[152:155],v[80:83]\n"
LD "v4,s[24:27],s84 offen lds\n"
MF "v[88:91],a[216:219],a[156:159],v[88:91]\n"
MF "v[88:91],a[220:223],a[196:199],v[88:91]\n"
MF "v[92:95],a[204:207],a[196:199],v[92:95]\n"
TR "a[216:217],v0 offset:16392\n"
MF "v[92:95],a[200:203],a[156:159],v[92:95]\n"
"s_add_u32 s84,s14,9216\n"
TR "a[218:219],v0 offset:16904\n"
MF "v[84:87],a[200:203],a[152:155],v[84:87]\n"
LD "v5,s[24:27],s84 offen offset:1024 lds\n"
MF "v[84:87],a[204:207],a[192:195],v[84:87]\n"
"s_add_u32 m0,s62,16384\n"
TR "a[220:221],v0 offset:20488\n"
MF "v[76:79],a[204:207],a[188:191],v[76:79]\n"
"s_add_u32 s82,s66,1572992\n"
TR "a[222:223],v0 offset:21000\n"
MF "v[76:79],a[200:203],a[148:151],v[76:79]\n"
"s_sub_u32 s15,s82,7864320\n"
MF "v[68:71],a[200:203],a[144:147],v[68:71]\n"
"s_min_u32 s82,s82,s15\n"
MF "v[68:71],a[204:207],a[184:187],v[68:71]\n"
LD "v6,s[28:31],s82 offen lds\n"
MF "v[60:63],a[204:207],a[180:183],v[60:63]\n"
MF "v[60:63],a[200:203],a[140:143],v[60:63]\n"
"s_waitcnt lgkmcnt(4)\n"
MF "v[96:99],a[208:211],a[140:143],v[96:99]\n"
TR "a[200:201],v1 offset:16384\n"
MF "v[96:99],a[212:215],a[180:183],v[96:99]\n"
TR "a[202:203],v1 offset:16896\n"
MF "v[104:107],a[212:215],a[184:187],v[104:107]\n"
LD "v11,s[28:31],s82 offen offset:1024 lds\n"
MF "v[104:107],a[208:211],a[144:147],v[104:107]\n"
TR "a[204:205],v1 offset:20480\n"
MF "v[112:115],a[208:211],a[148:151],v[112:115]\n"
TR "a[206:207],v1 offset:20992\n"
MF "v[112:115],a[212:215],a[188:191],v[112:115]\n"
MF "v[120:123],a[212:215],a[192:195],v[120:123]\n"
MF "v[120:123],a[208:211],a[152:155],v[120:123]\n"
LD "v12,s[28:31],s82 offen offset:2048 lds\n"
MF "v[128:131],a[208:211],a[156:159],v[128:131]\n"
MF "v[128:131],a[212:215],a[196:199],v[128:131]\n"
"s_waitcnt lgkmcnt(4)\n"
MF "v[132:135],a[220:223],a[196:199],v[132:135]\n"
TR "a[208:209],v1 offset:16392\n"
MF "v[132:135],a[216:219],a[156:159],v[132:135]\n"
TR "a[210:211],v1 offset:16904\n"
MF "v[124:127],a[216:219],a[152:155],v[124:127]\n"
LD "v13,s[28:31],s82 offen offset:3072 lds\n"
MF "v[124:127],a[220:223],a[192:195],v[124:127]\n"
TR "a[212:213],v1 offset:20488\n"
MF "v[116:119],a[220:223],a[188:191],v[116:119]\n"
TR "a[214:215],v1 offset:21000\n"
MF "v[116:119],a[216:219],a[148:151],v[116:119]\n"
"s_add_u32 s84,s14,20480\n"
MF "v[108:111],a[216:219],a[144:147],v[108:111]\n"
"s_add_u32 m0,s63,2048\n"
MF "v[108:111],a[220:223],a[184:187],v[108:111]\n"
LD "v4,s[24:27],s84 offen lds\n"
MF "v[100:103],a[220:223],a[180:183],v[100:103]\n"
MF "v[100:103],a[216:219],a[140:143],v[100:103]\n"
"s_waitcnt vmcnt(11) lgkmcnt(0)\n"
"s_barrier\n"
MF "v[136:139],a[200:203],a[140:143],v[136:139]\n"
TR "a[216:217],v0 offset:32768\n"
MF "v[136:139],a[204:207],a[180:183],v[136:139]\n"
TR "a[218:219],v0 offset:33280\n"
MF "v[144:147],a[204:207],a[184:187],v[144:147]\n"
"ds_read_b128 a[60:63],v2 offset:40960\n"
MF "v[144:147],a[200:203],a[144:147],v[144:147]\n"
TR "a[220:221],v0 offset:36864\n"
MF "v[152:155],a[200:203],a[148:151],v[152:155]\n"
"s_add_u32 s84,s14,29696\n"
TR "a[222:223],v0 offset:37376\n"
MF "v[152:155],a[204:207],a[188:191],v[152:155]\n"
LD "v5,s[24:27],s84 offen offset:1024 lds\n"
MF "v[160:163],a[204:207],a[192:195],v[160:163]\n"
"ds_read_b128 a[100:103],v3 offset:40960\n"
MF "v[160:163],a[200:203],a[152:155],v[160:163]\n"
"s_add_u32 m0,s62,32768\n"
MF "v[168:171],a[200:203],a[156:159],v[168:171]\n"
"s_add_u32 s82,s66,1573120\n"
"s_sub_u32 s15,s82,7864320\n"
"s_min_u32 s82,s82,s15\n"
MF "v[168:171],a[204:207],a[196:199],v[168:171]\n"
LD "v6,s[28:31],s82 offen lds\n"
MF "v[172:175],a[212:215],a[196:199],v[172:175]\n"
TR "a[200:201],v0 offset:32776\n"
MF "v[172:175],a[208:211],a[156:159],v[172:175]\n"
TR "a[202:203],v0 offset:33288\n"
MF "v[164:167],a[208:211],a[152:155],v[164:167]\n"
"ds_read_b128 a[64:67],v2 offset:43008\n"
MF "v[164:167],a[212:215],a[192:195],v[164:167]\n"
TR "a[204:205],v0 offset:36872\n"
MF "v[156:159],a[212:215],a[188:191],v[156:159]\n"
TR "a[206:207],v0 offset:37384\n"
MF "v[156:159],a[208:211],a[148:151],v[156:159]\n"
LD "v11,s[28:31],s82 offen offset:1024 lds\n"
MF "v[148:151],a[208:211],a[144:147],v[148:151]\n"
"ds_read_b128 a[104:107],v3 offset:43008\n"
MF "v[148:151],a[212:215],a[184:187],v[148:151]\n"
MF "v[140:143],a[212:215],a[180:183],v[140:143]\n"
MF "v[140:143],a[208:211],a[140:143],v[140:143]\n"
"s_waitcnt lgkmcnt(7)\n"
LD "v12,s[28:31],s82 offen offset:2048 lds\n"
MF "v[176:179],a[216:219],a[140:143],v[176:179]\n"
TR "a[208:209],v1 offset:32768\n"
MF "v[176:179],a[220:223],a[180:183],v[176:179]\n"
TR "a[210:211],v1 offset:33280\n"
MF "v[184:187],a[220:223],a[184:187],v[184:187]\n"
"ds_read_b128 a[68:71],v2 offset:45056\n"
MF "v[184:187],a[216:219],a[144:147],v[184:187]\n"
TR "a[212:213],v1 offset:36864\n"
MF "v[192:195],a[216:219],a[148:151],v[192:195]\n"
TR "a[214:215],v1 offset:37376\n"
MF "v[192:195],a[220:223],a[188:191],v[192:195]\n"
LD "v13,s[28:31],s82 offen offset:3072 lds\n"
MF "v[200:203],a[220:223],a[192:195],v[200:203]\n"
"ds_read_b128 a[108:111],v3 offset:45056\n"
MF "v[200:203],a[216:219],a[152:155],v[200:203]\n"
"s_add_u32 s84,s14,40960\n"
MF "v[208:211],a[216:219],a[156:159],v[208:211]\n"
"s_add_u32 m0,s63,4096\n"
MF "v[208:211],a[220:223],a[196:199],v[208:211]\n"
"s_waitcnt lgkmcnt(7)\n"
LD "v4,s[24:27],s84 offen lds\n"
MF "v[212:215],a[204:207],a[196:199],v[212:215]\n"
"s_sub_u32 s66,s66,s83\n"
TR "a[216:217],v1 offset:32776\n"
MF "v[212:215],a[200:203],a[156:159],v[212:215]\n"
"s_add_u32 m0,s62,49152\n"
TR "a[218:219],v1 offset:33288\n"
MF "v[204:207],a[200:203],a[152:155],v[204:207]\n"
"s_add_u32 s82,s66,2359296\n"
"ds_read_b128 a[72:75],v2 offset:47104\n"
MF "v[204:207],a[204:207],a[192:195],v[204:207]\n"
"s_sub_u32 s15,s82,7864320\n"
TR "a[220:221],v1 offset:36872\n"
MF "v[196:199],a[204:207],a[188:191],v[196:199]\n"
"s_min_u32 s82,s82,s15\n"
TR "a[222:223],v1 offset:37384\n"
MF "v[196:199],a[200:203],a[148:151],v[196:199]\n"
LD "v6,s[28:31],s82 offen lds\n"
MF "v[188:191],a[200:203],a[144:147],v[188:191]\n"
"v_add_u32 v0,0xffff4000,v0\n"
MF "v[188:191],a[204:207],a[184:187],v[188:191]\n"
"v_add_u32 v1,0xffff4000,v1\n"
MF "v[180:183],a[204:207],a[180:183],v[180:183]\n"
LD "v11,s[28:31],s82 offen offset:1024 lds\n"
MF "v[180:183],a[200:203],a[140:143],v[180:183]\n"
"s_waitcnt vmcnt(15) lgkmcnt(0)\n"
"s_barrier\n"
MF "v[216:219],a[208:211],a[140:143],v[216:219]\n"
TR "a[200:201],v0\n"
MF "v[216:219],a[212:215],a[180:183],v[216:219]\n"
TR "a[202:203],v0 offset:512\n"
MF "v[224:227],a[212:215],a[184:187],v[224:227]\n"
"ds_read_b128 a[112:115],v3 offset:47104\n"
MF "v[224:227],a[208:211],a[144:147],v[224:227]\n"
TR "a[204:205],v0 offset:4096\n"
MF "v[232:235],a[208:211],a[148:151],v[232:235]\n"
TR "a[206:207],v0 offset:4608\n"
MF "v[232:235],a[212:215],a[188:191],v[232:235]\n"
LD "v12,s[28:31],s82 offen offset:2048 lds\n"
MF "v[240:243],a[212:215],a[192:195],v[240:243]\n"
"ds_read_b128 a[76:79],v2 offset:49152\n"
MF "v[240:243],a[208:211],a[152:155],v[240:243]\n"
"ds_read_b128 a[116:119],v3 offset:49152\n"
MF "v[248:251],a[208:211],a[156:159],v[248:251]\n"
MF "v[248:251],a[212:215],a[196:199],v[248:251]\n"
LD "v13,s[28:31],s82 offen offset:3072 lds\n"
MF "v[252:255],a[220:223],a[196:199],v[252:255]\n"
TR "a[208:209],v0 offset:8\n"
MF "v[252:255],a[216:219],a[156:159],v[252:255]\n"
TR "a[210:211],v0 offset:520\n"
MF "v[244:247],a[216:219],a[152:155],v[244:247]\n"
"s_add_u32 s14,s81,512\n"
"s_sub_u32 s15,s14,1280\n"
MF "v[244:247],a[220:223],a[192:195],v[244:247]\n"
"s_min_u32 s14,s14,s15\n"
"s_add_u32 s14,s14,s68\n"
TR "a[212:213],v0 offset:4104\n"
MF "v[236:239],a[220:223],a[188:191],v[236:239]\n"
"s_add_u32 s84,s14,0\n"
"s_add_u32 m0,s63,20480\n"
TR "a[214:215],v0 offset:4616\n"
MF "v[236:239],a[216:219],a[148:151],v[236:239]\n"
LD "v4,s[24:27],s84 offen lds\n"
MF "v[228:231],a[216:219],a[144:147],v[228:231]\n"
MF "v[228:231],a[220:223],a[184:187],v[228:231]\n"
MF "v[220:223],a[220:223],a[180:183],v[220:223]\n"
"s_add_u32 s84,s14,9216\n"
MF "v[220:223],a[216:219],a[140:143],v[220:223]\n"
"s_waitcnt lgkmcnt(4)\n"
LD "v5,s[24:27],s84 offen offset:1024 lds\n"
MF "v[16:19],a[200:203],a[60:63],v[16:19]\n"
TR "a[216:217],v1\n"
MF "v[16:19],a[204:207],a[100:103],v[16:19]\n"
"s_add_u32 m0,s62,65536\n"
TR "a[218:219],v1 offset:512\n"
MF "v[24:27],a[204:207],a[104:107],v[24:27]\n"
"s_add_u32 s82,s66,2359424\n"
MF "v[24:27],a[200:203],a[64:67],v[24:27]\n"
"s_sub_u32 s15,s82,7864320\n"
TR "a[220:221],v1 offset:4096\n"
MF "v[32:35],a[200:203],a[68:71],v[32:35]\n"
"s_min_u32 s82,s82,s15\n"
TR "a[222:223],v1 offset:4608\n"
MF "v[32:35],a[204:207],a[108:111],v[32:35]\n"
LD "v6,s[28:31],s82 offen lds\n"
MF "v[40:43],a[204:207],a[112:115],v[40:43]\n"
MF "v[40:43],a[200:203],a[72:75],v[40:43]\n"
MF "v[48:51],a[200:203],a[76:79],v[48:51]\n"
MF "v[48:51],a[204:207],a[116:119],v[48:51]\n"
"s_waitcnt lgkmcnt(4)\n"
LD "v11,s[28:31],s82 offen offset:1024 lds\n"
MF "v[52:55],a[212:215],a[116:119],v[52:55]\n"
TR "a[200:201],v1 offset:8\n"
MF "v[52:55],a[208:211],a[76:79],v[52:55]\n"
TR "a[202:203],v1 offset:520\n"
MF "v[44:47],a[208:211],a[72:75],v[44:47]\n"
MF "v[44:47],a[212:215],a[112:115],v[44:47]\n"
TR "a[204:205],v1 offset:4104\n"
MF "v[36:39],a[212:215],a[108:111],v[36:39]\n"
TR "a[206:207],v1 offset:4616\n"
MF "v[36:39],a[208:211],a[68:71],v[36:39]\n"
LD "v12,s[28:31],s82 offen offset:2048 lds\n"
MF "v[28:31],a[208:211],a[64:67],v[28:31]\n"
MF "v[28:31],a[212:215],a[104:107],v[28:31]\n"
MF "v[20:23],a[212:215],a[100:103],v[20:23]\n"
LD "v13,s[28:31],s82 offen offset:3072 lds\n"
MF "v[20:23],a[208:211],a[60:63],v[20:23]\n"
"s_waitcnt vmcnt(17) lgkmcnt(0)\n"
"s_barrier\n"
MF "v[56:59],a[216:219],a[60:63],v[56:59]\n"
"s_add_u32 s84,s14,20480\n"
TR "a[208:209],v0 offset:16384\n"
MF "v[56:59],a[220:223],a[100:103],v[56:59]\n"
"s_add_u32 m0,s63,22528\n"
TR "a[210:211],v0 offset:16896\n"
MF "v[64:67],a[220:223],a[104:107],v[64:67]\n"
LD "v4,s[24:27],s84 offen lds\n"
MF "v[64:67],a[216:219],a[64:67],v[64:67]\n"
TR "a[212:213],v0 offset:20480\n"
MF "v[72:75],a[216:219],a[68:71],v[72:75]\n"
TR "a[214:215],v0 offset:20992\n"
MF "v[72:75],a[220:223],a[108:111],v[72:75]\n"
MF "v[80:83],a[220:223],a[112:115],v[80:83]\n"
"s_add_u32 s84,s14,29696\n"
MF "v[80:83],a[216:219],a[72:75],v[80:83]\n"
LD "v5,s[24:27],s84 offen offset:1024 lds\n"
MF "v[88:91],a[216:219],a[76:79],v[88:91]\n"
"s_add_u32 m0,s62,81920\n"
MF "v[88:91],a[220:223],a[116:119],v[88:91]\n"
"s_add_u32 s82,s66,2359552\n"
MF "v[92:95],a[204:207],a[116:119],v[92:95]\n"
"s_sub_u32 s15,s82,7864320\n"
TR "a[216:217],v0 offset:16392\n"
MF "v[92:95],a[200:203],a[76:79],v[92:95]\n"
"s_min_u32 s82,s82,s15\n"
TR "a[218:219],v0 offset:16904\n"
MF "v[84:87],a[200:203],a[72:75],v[84:87]\n"
LD "v6,s[28:31],s82 offen lds\n"
MF "v[84:87],a[204:207],a[112:115],v[84:87]\n"
TR "a[220:221],v0 offset:20488\n"
MF "v[76:79],a[204:207],a[108:111],v[76:79]\n"
TR "a[222:223],v0 offset:21000\n"
MF "v[76:79],a[200:203],a[68:71],v[76:79]\n"
MF "v[68:71],a[200:203],a[64:67],v[68:71]\n"
MF "v[68:71],a[204:207],a[104:107],v[68:71]\n"
LD "v11,s[28:31],s82 offen offset:1024 lds\n"
MF "v[60:63],a[204:207],a[100:103],v[60:63]\n"
MF "v[60:63],a[200:203],a[60:63],v[60:63]\n"
"s_waitcnt lgkmcnt(4)\n"
MF "v[96:99],a[208:211],a[60:63],v[96:99]\n"
TR "a[200:201],v1 offset:16384\n"
MF "v[96:99],a[212:215],a[100:103],v[96:99]\n"
TR "a[202:203],v1 offset:16896\n"
MF "v[104:107],a[212:215],a[104:107],v[104:107]\n"
LD "v12,s[28:31],s82 offen offset:2048 lds\n"
MF "v[104:107],a[208:211],a[64:67],v[104:107]\n"
TR "a[204:205],v1 offset:20480\n"
MF "v[112:115],a[208:211],a[68:71],v[112:115]\n"
TR "a[206:207],v1 offset:20992\n"
MF "v[112:115],a[212:215],a[108:111],v[112:115]\n"
MF "v[120:123],a[212:215],a[112:115],v[120:123]\n"
MF "v[120:123],a[208:211],a[72:75],v[120:123]\n"
LD "v13,s[28:31],s82 offen offset:3072 lds\n"
MF "v[128:131],a[208:211],a[76:79],v[128:131]\n"
MF "v[128:131],a[212:215],a[116:119],v[128:131]\n"
"s_waitcnt lgkmcnt(4)\n"
MF "v[132:135],a[220:223],a[116:119],v[132:135]\n"
"s_add_u32 s84,s14,40960\n"
TR "a[208:209],v1 offset:16392\n"
MF "v[132:135],a[216:219],a[76:79],v[132:135]\n"
"s_add_u32 m0,s63,24576\n"
TR "a[210:211],v1 offset:16904\n"
MF "v[124:127],a[216:219],a[72:75],v[124:127]\n"
LD "v4,s[24:27],s84 offen lds\n"
MF "v[124:127],a[220:223],a[112:115],v[124:127]\n"
"s_add_u32 m0,s62,0\n"
TR "a[212:213],v1 offset:20488\n"
MF "v[116:119],a[220:223],a[108:111],v[116:119]\n"
"s_add_u32 s82,s66,3145728\n"
TR "a[214:215],v1 offset:21000\n"
MF "v[116:119],a[216:219],a[68:71],v[116:119]\n"
"s_sub_u32 s15,s82,7864320\n"
MF "v[108:111],a[216:219],a[64:67],v[108:111]\n"
"s_min_u32 s82,s82,s15\n"
MF "v[108:111],a[220:223],a[104:107],v[108:111]\n"
LD "v6,s[28:31],s82 offen lds\n"
MF "v[100:103],a[220:223],a[100:103],v[100:103]\n"
MF "v[100:103],a[216:219],a[60:63],v[100:103]\n"
"s_waitcnt vmcnt(18) lgkmcnt(0)\n"
"s_barrier\n"
MF "v[136:139],a[200:203],a[60:63],v[136:139]\n"
TR "a[216:217],v0 offset:32768\n"
MF "v[136:139],a[204:207],a[100:103],v[136:139]\n"
TR "a[218:219],v0 offset:33280\n"
MF "v[144:147],a[204:207],a[104:107],v[144:147]\n"
"ds_read_b128 a[140:143],v2\n"
MF "v[144:147],a[200:203],a[64:67],v[144:147]\n"
TR "a[220:221],v0 offset:36864\n"
MF "v[152:155],a[200:203],a[68:71],v[152:155]\n"
TR "a[222:223],v0 offset:37376\n"
MF "v[152:155],a[204:207],a[108:111],v[152:155]\n"
LD "v11,s[28:31],s82 offen offset:1024 lds\n"
MF "v[160:163],a[204:207],a[112:115],v[160:163]\n"
"ds_read_b128 a[180:183],v3\n"
MF "v[160:163],a[200:203],a[72:75],v[160:163]\n"
MF "v[168:171],a[200:203],a[76:79],v[168:171]\n"
MF "v[168:171],a[204:207],a[116:119],v[168:171]\n"
LD "v12,s[28:31],s82 offen offset:2048 lds\n"
MF "v[172:175],a[212:215],a[116:119],v[172:175]\n"
TR "a[200:201],v0 offset:32776\n"
MF "v[172:175],a[208:211],a[76:79],v[172:175]\n"
TR "a[202:203],v0 offset:33288\n"
MF "v[164:167],a[208:211],a[72:75],v[164:167]\n"
"ds_read_b128 a[144:147],v2 offset:2048\n"
MF "v[164:167],a[212:215],a[112:115],v[164:167]\n"
TR "a[204:205],v0 offset:36872\n"
MF "v[156:159],a[212:215],a[108:111],v[156:159]\n"
TR "a[206:207],v0 offset:37384\n"
MF "v[156:159],a[208:211],a[68:71],v[156:159]\n"
LD "v13,s[28:31],s82 offen offset:3072 lds\n"
MF "v[148:151],a[208:211],a[64:67],v[148:151]\n"
"ds_read_b128 a[184:187],v3 offset:2048\n"
MF "v[148:151],a[212:215],a[104:107],v[148:151]\n"
"s_add_u32 s14,s81,640\n"
"s_sub_u32 s15,s14,1280\n"
"s_min_u32 s14,s14,s15\n"
MF "v[140:143],a[212:215],a[100:103],v[140:143]\n"
"s_add_u32 s14,s14,s68\n"
"s_add_u32 s84,s14,0\n"
"s_add_u32 m0,s63,40960\n"
MF "v[140:143],a[208:211],a[60:63],v[140:143]\n"
"s_waitcnt lgkmcnt(7)\n"
LD "v4,s[24:27],s84 offen lds\n"
MF "v[176:179],a[216:219],a[60:63],v[176:179]\n"
TR "a[208:209],v1 offset:32768\n"
MF "v[176:179],a[220:223],a[100:103],v[176:179]\n"
TR "a[210:211],v1 offset:33280\n"
MF "v[184:187],a[220:223],a[104:107],v[184:187]\n"
"ds_read_b128 a[148:151],v2 offset:4096\n"
MF "v[184:187],a[216:219],a[64:67],v[184:187]\n"
TR "a[212:213],v1 offset:36864\n"
MF "v[192:195],a[216:219],a[68:71],v[192:195]\n"
"s_add_u32 s84,s14,9216\n"
TR "a[214:215],v1 offset:37376\n"
MF "v[192:195],a[220:223],a[108:111],v[192:195]\n"
LD "v5,s[24:27],s84 offen offset:1024 lds\n"
MF "v[200:203],a[220:223],a[112:115],v[200:203]\n"
"ds_read_b128 a[188:191],v3 offset:4096\n"
MF "v[200:203],a[216:219],a[72:75],v[200:203]\n"
"s_add_u32 m0,s62,16384\n"
MF "v[208:211],a[216:219],a[76:79],v[208:211]\n"
"s_add_u32 s82,s66,3145856\n"
"s_sub_u32 s15,s82,7864320\n"
"s_min_u32 s82,s82,s15\n"
MF "v[208:211],a[220:223],a[116:119],v[208:211]\n"
"s_waitcnt lgkmcnt(7)\n"
LD "v6,s[28:31],s82 offen lds\n"
MF "v[212:215],a[204:207],a[116:119],v[212:215]\n"
TR "a[216:217],v1 offset:32776\n"
MF "v[212:215],a[200:203],a[76:79],v[212:215]\n"
TR "a[218:219],v1 offset:33288\n"
MF "v[204:207],a[200:203],a[72:75],v[204:207]\n"
"ds_read_b128 a[152:155],v2 offset:6144\n"
MF "v[204:207],a[204:207],a[112:115],v[204:207]\n"
TR "a[220:221],v1 offset:36872\n"
MF "v[196:199],a[204:207],a[108:111],v[196:199]\n"
TR "a[222:223],v1 offset:37384\n"
MF "v[196:199],a[200:203],a[68:71],v[196:199]\n"
LD "v11,s[28:31],s82 offen offset:1024 lds\n"
MF "v[188:191],a[200:203],a[64:67],v[188:191]\n"
"v_add_u32 v0,0xc000,v0\n"
MF "v[188:191],a[204:207],a[104:107],v[188:191]\n"
"v_add_u32 v1,0xc000,v1\n"
MF "v[180:183],a[204:207],a[100:103],v[180:183]\n"
LD "v12,s[28:31],s82 offen offset:2048 lds\n"
MF "v[180:183],a[200:203],a[60:63],v[180:183]\n"
"s_waitcnt vmcnt(22) lgkmcnt(0)\n"
"s_barrier\n"
MF "v[216:219],a[208:211],a[60:63],v[216:219]\n"
TR "a[200:201],v0\n"
MF "v[216:219],a[212:215],a[100:103],v[216:219]\n"
TR "a[202:203],v0 offset:512\n"
MF "v[224:227],a[212:215],a[104:107],v[224:227]\n"
"ds_read_b128 a[192:195],v3 offset:6144\n"
MF "v[224:227],a[208:211],a[64:67],v[224:227]\n"
TR "a[204:205],v0 offset:4096\n"
MF "v[232:235],a[208:211],a[68:71],v[232:235]\n"
TR "a[206:207],v0 offset:4608\n"
MF "v[232:235],a[212:215],a[108:111],v[232:235]\n"
LD "v13,s[28:31],s82 offen offset:3072 lds\n"
MF "v[240:243],a[212:215],a[112:115],v[240:243]\n"
"ds_read_b128 a[156:159],v2 offset:8192\n"
MF "v[240:243],a[208:211],a[72:75],v[240:243]\n"
"s_add_u32 s84,s14,20480\n"
"ds_read_b128 a[196:199],v3 offset:8192\n"
MF "v[248:251],a[208:211],a[76:79],v[248:251]\n"
"s_add_u32 m0,s63,43008\n"
MF "v[248:251],a[212:215],a[116:119],v[248:251]\n"
LD "v4,s[24:27],s84 offen lds\n"
MF "v[252:255],a[220:223],a[116:119],v[252:255]\n"
TR "a[208:209],v0 offset:8\n"
MF "v[252:255],a[216:219],a[76:79],v[252:255]\n"
TR "a[210:211],v0 offset:520\n"
MF "v[244:247],a[216:219],a[72:75],v[244:247]\n"
MF "v[244:247],a[220:223],a[112:115],v[244:247]\n"
TR "a[212:213],v0 offset:4104\n"
MF "v[236:239],a[220:223],a[108:111],v[236:239]\n"
"s_add_u32 s84,s14,29696\n"
TR "a[214:215],v0 offset:4616\n"
MF "v[236:239],a[216:219],a[68:71],v[236:239]\n"
LD "v5,s[24:27],s84 offen offset:1024 lds\n"
MF "v[228:231],a[216:219],a[64:67],v[228:231]\n"
MF "v[228:231],a[220:223],a[104:107],v[228:231]\n"
"s_add_u32 m0,s62,32768\n"
MF "v[220:223],a[220:223],a[100:103],v[220:223]\n"
"s_add_u32 s82,s66,3145984\n"
"s_sub_u32 s15,s82,7864320\n"
"s_min_u32 s82,s82,s15\n"
MF "v[220:223],a[216:219],a[60:63],v[220:223]\n"
"s_waitcnt lgkmcnt(4)\n"
LD "v6,s[28:31],s82 offen lds\n"
MF "v[16:19],a[200:203],a[140:143],v[16:19]\n"
TR "a[216:217],v1\n"
MF "v[16:19],a[204:207],a[180:183],v[16:19]\n"
TR "a[218:219],v1 offset:512\n"
MF "v[24:27],a[204:207],a[184:187],v[24:27]\n"
MF "v[24:27],a[200:203],a[144:147],v[24:27]\n"
TR "a[220:221],v1 offset:4096\n"
MF "v[32:35],a[200:203],a[148:151],v[32:35]\n"
TR "a[222:223],v1 offset:4608\n"
MF "v[32:35],a[204:207],a[188:191],v[32:35]\n"
LD "v11,s[28:31],s82 offen offset:1024 lds\n"
MF "v[40:43],a[204:207],a[192:195],v[40:43]\n"
MF "v[40:43],a[200:203],a[152:155],v[40:43]\n"
MF "v[48:51],a[200:203],a[156:159],v[48:51]\n"
MF "v[48:51],a[204:207],a[196:199],v[48:51]\n"
"s_waitcnt lgkmcnt(4)\n"
LD "v12,s[28:31],s82 offen offset:2048 lds\n"
MF "v[52:55],a[212:215],a[196:199],v[52:55]\n"
TR "a[200:201],v1 offset:8\n"
MF "v[52:55],a[208:211],a[156:159],v[52:55]\n"
TR "a[202:203],v1 offset:520\n"
MF "v[44:47],a[208:211],a[152:155],v[44:47]\n"
MF "v[44:47],a[212:215],a[192:195],v[44:47]\n"
TR "a[204:205],v1 offset:4104\n"
MF "v[36:39],a[212:215],a[188:191],v[36:39]\n"
".Lpg2a:\n"
"s_cbranch_execz .Lisr1\n"
TR "a[206:207],v1 offset:4616\n"
MF "v[36:39],a[208:211],a[148:151],v[36:39]\n"
LD "v13,s[28:31],s82 offen offset:3072 lds\n"
MF "v[28:31],a[208:211],a[144:147],v[28:31]\n"
"s_add_u32 s84,s14,40960\n"
MF "v[28:31],a[212:215],a[184:187],v[28:31]\n"
"s_add_u32 m0,s63,45056\n"
MF "v[20:23],a[212:215],a[180:183],v[20:23]\n"
LD "v4,s[24:27],s84 offen lds\n"
MF "v[20:23],a[208:211],a[140:143],v[20:23]\n"
"s_waitcnt vmcnt(24) lgkmcnt(0)\n"
"s_barrier\n"
AW "a253,v10\n"
"v_add_u32 v10,1,v10\n"
"s_bfe_u32 s58,s22,0x30014\n"
"s_lshl_b32 s58,s58,2\n"
"s_add_u32 s58,s58,s56\n"
"s_lshl_b32 s58,s58,6\n"
"s_add_u32 s58,s58,s86\n"
"v_readlane_b32 s76,v10,s78\n"
"buffer_store_dword v10,v14,s[40:43],s58 offen offset:128 sc1\n"
".Lhs5:\n"
MF "v[56:59],a[216:219],a[140:143],v[56:59]\n"
"s_add_u32 m0,s62,49152\n"
"s_add_u32 s82,s66,3932160\n"
TR "a[208:209],v0 offset:16384\n"
MF "v[56:59],a[220:223],a[180:183],v[56:59]\n"
"s_sub_u32 s15,s82,7864320\n"
"s_min_u32 s82,s82,s15\n"
TR "a[210:211],v0 offset:16896\n"
MF "v[64:67],a[220:223],a[184:187],v[64:67]\n"
LD "v6,s[28:31],s82 offen lds\n"
MF "v[64:67],a[216:219],a[144:147],v[64:67]\n"
TR "a[212:213],v0 offset:20480\n"
MF "v[72:75],a[216:219],a[148:151],v[72:75]\n"
TR "a[214:215],v0 offset:20992\n"
MF "v[72:75],a[220:223],a[188:191],v[72:75]\n"
"buffer_load_dword a249,v15,s[48:51],s60 offen offset:1024\n"
MF "v[80:83],a[220:223],a[192:195],v[80:83]\n"
"s_bfe_u32 s59,s23,0x30003\n"
MF "v[80:83],a[216:219],a[152:155],v[80:83]\n"
LD "v11,s[28:31],s82 offen offset:1024 lds\n"
MF "v[88:91],a[216:219],a[156:159],v[88:91]\n"
"s_add_u32 s59,s59,1\n"
MF "v[88:91],a[220:223],a[196:199],v[88:91]\n"
"s_lshl_b32 s59,s59,7\n"
MF "v[92:95],a[204:207],a[196:199],v[92:95]\n"
"s_and_b32 s59,s59,768\n"
TR "a[216:217],v0 offset:16392\n"
MF "v[92:95],a[200:203],a[156:159],v[92:95]\n"
"s_add_u32 s59,s59,s60\n"
TR "a[218:219],v0 offset:16904\n"
MF "v[84:87],a[200:203],a[152:155],v[84:87]\n"
LD "v12,s[28:31],s82 offen offset:2048 lds\n"
MF "v[84:87],a[204:207],a[192:195],v[84:87]\n"
TR "a[220:221],v0 offset:20488\n"
MF "v[76:79],a[204:207],a[188:191],v[76:79]\n"
TR "a[222:223],v0 offset:21000\n"
MF "v[76:79],a[200:203],a[148:151],v[76:79]\n"
MF "v[68:71],a[200:203],a[144:147],v[68:71]\n"
MF "v[68:71],a[204:207],a[184:187],v[68:71]\n"
LD "v13,s[28:31],s82 offen offset:3072 lds\n"
MF "v[60:63],a[204:207],a[180:183],v[60:63]\n"
MF "v[60:63],a[200:203],a[140:143],v[60:63]\n"
"s_add_u32 s14,s81,768\n"
"s_sub_u32 s15,s14,1280\n"
"s_waitcnt lgkmcnt(4)\n"
MF "v[96:99],a[208:211],a[140:143],v[96:99]\n"
"s_min_u32 s14,s14,s15\n"
"s_add_u32 s14,s14,s68\n"
TR "a[200:201],v1 offset:16384\n"
MF "v[96:99],a[212:215],a[180:183],v[96:99]\n"
"s_add_u32 s84,s14,0\n"
"s_add_u32 m0,s63,0\n"
TR "a[202:203],v1 offset:16896\n"
MF "v[104:107],a[212:215],a[184:187],v[104:107]\n"
LD "v4,s[24:27],s84 offen lds\n"
MF "v[104:107],a[208:211],a[144:147],v[104:107]\n"
TR "a[204:205],v1 offset:20480\n"
MF "v[112:115],a[208:211],a[148:151],v[112:115]\n"
TR "a[206:207],v1 offset:20992\n"
MF "v[112:115],a[212:215],a[188:191],v[112:115]\n"
MF "v[120:123],a[212:215],a[192:195],v[120:123]\n"
"s_add_u32 s84,s14,9216\n"
MF "v[120:123],a[208:211],a[152:155],v[120:123]\n"
LD "v5,s[24:27],s84 offen offset:1024 lds\n"
MF "v[128:131],a[208:211],a[156:159],v[128:131]\n"
MF "v[128:131],a[212:215],a[196:199],v[128:131]\n"
"s_waitcnt lgkmcnt(4)\n"
MF "v[132:135],a[220:223],a[196:199],v[132:135]\n"
TR "a[208:209],v1 offset:16392\n"
MF "v[132:135],a[216:219],a[156:159],v[132:135]\n"
TR "a[210:211],v1 offset:16904\n"
MF "v[124:127],a[216:219],a[152:155],v[124:127]\n"
MF "v[124:127],a[220:223],a[192:195],v[124:127]\n"
TR "a[212:213],v1 offset:20488\n"
MF "v[116:119],a[220:223],a[188:191],v[116:119]\n"
TR "a[214:215],v1 offset:21000\n"
MF "v[116:119],a[216:219],a[148:151],v[116:119]\n"
MF "v[108:111],a[216:219],a[144:147],v[108:111]\n"
MF "v[108:111],a[220:223],a[184:187],v[108:111]\n"
MF "v[100:103],a[220:223],a[180:183],v[100:103]\n"
MF "v[100:103],a[216:219],a[140:143],v[100:103]\n"
"s_waitcnt vmcnt(24) lgkmcnt(0)\n"
"s_barrier\n"
MF "v[136:139],a[200:203],a[140:143],v[136:139]\n"
TR "a[216:217],v0 offset:32768\n"
MF "v[136:139],a[204:207],a[180:183],v[136:139]\n"
"s_add_u32 m0,s62,65536\n"
TR "a[218:219],v0 offset:33280\n"
MF "v[144:147],a[204:207],a[184:187],v[144:147]\n"
"s_add_u32 s82,s66,3932288\n"
"ds_read_b128 a[60:63],v2 offset:20480\n"
MF "v[144:147],a[200:203],a[144:147],v[144:147]\n"
"s_sub_u32 s15,s82,7864320\n"
TR "a[220:221],v0 offset:36864\n"
MF "v[152:155],a[200:203],a[148:151],v[152:155]\n"
"s_min_u32 s82,s82,s15\n"
TR "a[222:223],v0 offset:37376\n"
MF "v[152:155],a[204:207],a[188:191],v[152:155]\n"
LD "v6,s[28:31],s82 offen lds\n"
MF "v[160:163],a[204:207],a[192:195],v[160:163]\n"
"ds_read_b128 a[100:103],v3 offset:20480\n"
MF "v[160:163],a[200:203],a[152:155],v[160:163]\n"
MF "v[168:171],a[200:203],a[156:159],v[168:171]\n"
MF "v[168:171],a[204:207],a[196:199],v[168:171]\n"
LD "v11,s[28:31],s82 offen offset:1024 lds\n"
MF "v[172:175],a[212:215],a[196:199],v[172:175]\n"
TR "a[200:201],v0 offset:32776\n"
MF "v[172:175],a[208:211],a[156:159],v[172:175]\n"
TR "a[202:203],v0 offset:33288\n"
MF "v[164:167],a[208:211],a[152:155],v[164:167]\n"
"ds_read_b128 a[64:67],v2 offset:22528\n"
MF "v[164:167],a[212:215],a[192:195],v[164:167]\n"
TR "a[204:205],v0 offset:36872\n"
MF "v[156:159],a[212:215],a[188:191],v[156:159]\n"
TR "a[206:207],v0 offset:37384\n"
MF "v[156:159],a[208:211],a[148:151],v[156:159]\n"
LD "v12,s[28:31],s82 offen offset:2048 lds\n"
MF "v[148:151],a[208:211],a[144:147],v[148:151]\n"
"ds_read_b128 a[104:107],v3 offset:22528\n"
MF "v[148:151],a[212:215],a[184:187],v[148:151]\n"
MF "v[140:143],a[212:215],a[180:183],v[140:143]\n"
MF "v[140:143],a[208:211],a[140:143],v[140:143]\n"
"s_waitcnt lgkmcnt(7)\n"
LD "v13,s[28:31],s82 offen offset:3072 lds\n"
MF "v[176:179],a[216:219],a[140:143],v[176:179]\n"
TR "a[208:209],v1 offset:32768\n"
MF "v[176:179],a[220:223],a[180:183],v[176:179]\n"
TR "a[210:211],v1 offset:33280\n"
MF "v[184:187],a[220:223],a[184:187],v[184:187]\n"
"ds_read_b128 a[68:71],v2 offset:24576\n"
MF "v[184:187],a[216:219],a[144:147],v[184:187]\n"
"s_add_u32 s84,s14,20480\n"
TR "a[212:213],v1 offset:36864\n"
MF "v[192:195],a[216:219],a[148:151],v[192:195]\n"
"s_add_u32 m0,s63,2048\n"
TR "a[214:215],v1 offset:37376\n"
MF "v[192:195],a[220:223],a[188:191],v[192:195]\n"
LD "v4,s[24:27],s84 offen lds\n"
MF "v[200:203],a[220:223],a[192:195],v[200:203]\n"
"ds_read_b128 a[108:111],v3 offset:24576\n"
MF "v[200:203],a[216:219],a[152:155],v[200:203]\n"
MF "v[208:211],a[216:219],a[156:159],v[208:211]\n"
"s_add_u32 s84,s14,29696\n"
MF "v[208:211],a[220:223],a[196:199],v[208:211]\n"
"s_waitcnt lgkmcnt(7)\n"
LD "v5,s[24:27],s84 offen offset:1024 lds\n"
MF "v[212:215],a[204:207],a[196:199],v[212:215]\n"
TR "a[216:217],v1 offset:32776\n"
MF "v[212:215],a[200:203],a[156:159],v[212:215]\n"
TR "a[218:219],v1 offset:33288\n"
MF "v[204:207],a[200:203],a[152:155],v[204:207]\n"
"ds_read_b128 a[72:75],v2 offset:26624\n"
MF "v[204:207],a[204:207],a[192:195],v[204:207]\n"
TR "a[220:221],v1 offset:36872\n"
MF "v[196:199],a[204:207],a[188:191],v[196:199]\n"
TR "a[222:223],v1 offset:37384\n"
MF "v[196:199],a[200:203],a[148:151],v[196:199]\n"
MF "v[188:191],a[200:203],a[144:147],v[188:191]\n"
"v_add_u32 v0,0xffff4000,v0\n"
MF "v[188:191],a[204:207],a[184:187],v[188:191]\n"
"v_add_u32 v1,0xffff4000,v1\n"
MF "v[180:183],a[204:207],a[180:183],v[180:183]\n"
MF "v[180:183],a[200:203],a[140:143],v[180:183]\n"
"s_waitcnt vmcnt(26) lgkmcnt(0)\n"
"s_barrier\n"
MF "v[216:219],a[208:211],a[140:143],v[216:219]\n"
TR "a[200:201],v0\n"
MF "v[216:219],a[212:215],a[180:183],v[216:219]\n"
"s_add_u32 m0,s62,81920\n"
TR "a[202:203],v0 offset:512\n"
MF "v[224:227],a[212:215],a[184:187],v[224:227]\n"
"s_add_u32 s82,s66,3932416\n"
"ds_read_b128 a[112:115],v3 offset:26624\n"
MF "v[224:227],a[208:211],a[144:147],v[224:227]\n"
"s_sub_u32 s15,s82,7864320\n"
TR "a[204:205],v0 offset:4096\n"
MF "v[232:235],a[208:211],a[148:151],v[232:235]\n"
"s_min_u32 s82,s82,s15\n"
TR "a[206:207],v0 offset:4608\n"
MF "v[232:235],a[212:215],a[188:191],v[232:235]\n"
LD "v6,s[28:31],s82 offen lds\n"
MF "v[240:243],a[212:215],a[192:195],v[240:243]\n"
"ds_read_b128 a[76:79],v2 offset:28672\n"
MF "v[240:243],a[208:211],a[152:155],v[240:243]\n"
"ds_read_b128 a[116:119],v3 offset:28672\n"
MF "v[248:251],a[208:211],a[156:159],v[248:251]\n"
MF "v[248:251],a[212:215],a[196:199],v[248:251]\n"
LD "v11,s[28:31],s82 offen offset:1024 lds\n"
MF "v[252:255],a[220:223],a[196:199],v[252:255]\n"
TR "a[208:209],v0 offset:8\n"
MF "v[252:255],a[216:219],a[156:159],v[252:255]\n"
TR "a[210:211],v0 offset:520\n"
MF "v[244:247],a[216:219],a[152:155],v[244:247]\n"
MF "v[244:247],a[220:223],a[192:195],v[244:247]\n"
TR "a[212:213],v0 offset:4104\n"
MF "v[236:239],a[220:223],a[188:191],v[236:239]\n"
TR "a[214:215],v0 offset:4616\n"
MF "v[236:239],a[216:219],a[148:151],v[236:239]\n"
LD "v12,s[28:31],s82 offen offset:2048 lds\n"
MF "v[228:231],a[216:219],a[144:147],v[228:231]\n"
MF "v[228:231],a[220:223],a[184:187],v[228:231]\n"
MF "v[220:223],a[220:223],a[180:183],v[220:223]\n"
MF "v[220:223],a[216:219],a[140:143],v[220:223]\n"
"s_waitcnt lgkmcnt(4)\n"
LD "v13,s[28:31],s82 offen offset:3072 lds\n"
MF "v[16:19],a[200:203],a[60:63],v[16:19]\n"
TR "a[216:217],v1\n"
MF "v[16:19],a[204:207],a[100:103],v[16:19]\n"
TR "a[218:219],v1 offset:512\n"
MF "v[24:27],a[204:207],a[104:107],v[24:27]\n"
MF "v[24:27],a[200:203],a[64:67],v[24:27]\n"
"s_add_u32 s84,s14,40960\n"
TR "a[220:221],v1 offset:4096\n"
MF "v[32:35],a[200:203],a[68:71],v[32:35]\n"
"s_add_u32 m0,s63,4096\n"
TR "a[222:223],v1 offset:4608\n"
MF "v[32:35],a[204:207],a[108:111],v[32:35]\n"
LD "v4,s[24:27],s84 offen lds\n"
MF "v[40:43],a[204:207],a[112:115],v[40:43]\n"
MF "v[40:43],a[200:203],a[72:75],v[40:43]\n"
MF "v[48:51],a[200:203],a[76:79],v[48:51]\n"
MF "v[48:51],a[204:207],a[116:119],v[48:51]\n"
"s_waitcnt lgkmcnt(4)\n"
MF "v[52:55],a[212:215],a[116:119],v[52:55]\n"
TR "a[200:201],v1 offset:8\n"
MF "v[52:55],a[208:211],a[76:79],v[52:55]\n"
TR "a[202:203],v1 offset:520\n"
MF "v[44:47],a[208:211],a[72:75],v[44:47]\n"
MF "v[44:47],a[212:215],a[112:115],v[44:47]\n"
TR "a[204:205],v1 offset:4104\n"
MF "v[36:39],a[212:215],a[108:111],v[36:39]\n"
TR "a[206:207],v1 offset:4616\n"
MF "v[36:39],a[208:211],a[68:71],v[36:39]\n"
MF "v[28:31],a[208:211],a[64:67],v[28:31]\n"
MF "v[28:31],a[212:215],a[104:107],v[28:31]\n"
MF "v[20:23],a[212:215],a[100:103],v[20:23]\n"
MF "v[20:23],a[208:211],a[60:63],v[20:23]\n"
"s_waitcnt vmcnt(25) lgkmcnt(0)\n"
"s_barrier\n"
MF "v[56:59],a[216:219],a[60:63],v[56:59]\n"
"s_add_u32 m0,s62,0\n"
"s_add_u32 s82,s66,4718592\n"
TR "a[208:209],v0 offset:16384\n"
MF "v[56:59],a[220:223],a[100:103],v[56:59]\n"
"s_sub_u32 s15,s82,7864320\n"
"s_min_u32 s82,s82,s15\n"
TR "a[210:211],v0 offset:16896\n"
MF "v[64:67],a[220:223],a[104:107],v[64:67]\n"
LD "v6,s[28:31],s82 offen lds\n"
MF "v[64:67],a[216:219],a[64:67],v[64:67]\n"
TR "a[212:213],v0 offset:20480\n"
MF "v[72:75],a[216:219],a[68:71],v[72:75]\n"
TR "a[214:215],v0 offset:20992\n"
MF "v[72:75],a[220:223],a[108:111],v[72:75]\n"
"buffer_load_dword a249,v15,s[48:51],s59 offen\n"
MF "v[80:83],a[220:223],a[112:115],v[80:83]\n"
"s_bfe_u32 s59,s23,0x30003\n"
MF "v[80:83],a[216:219],a[72:75],v[80:83]\n"
LD "v11,s[28:31],s82 offen offset:1024 lds\n"
MF "v[88:91],a[216:219],a[76:79],v[88:91]\n"
"s_add_u32 s59,s59,3\n"
MF "v[88:91],a[220:223],a[116:119],v[88:91]\n"
"s_lshl_b32 s59,s59,7\n"
MF "v[92:95],a[204:207],a[116:119],v[92:95]\n"
"s_and_b32 s59,s59,768\n"
TR "a[216:217],v0 offset:16392\n"
MF "v[92:95],a[200:203],a[76:79],v[92:95]\n"
"s_add_u32 s59,s59,s60\n"
TR "a[218:219],v0 offset:16904\n"
MF "v[84:87],a[200:203],a[72:75],v[84:87]\n"
LD "v12,s[28:31],s82 offen offset:2048 lds\n"
MF "v[84:87],a[204:207],a[112:115],v[84:87]\n"
TR "a[220:221],v0 offset:20488\n"
MF "v[76:79],a[204:207],a[108:111],v[76:79]\n"
TR "a[222:223],v0 offset:21000\n"
MF "v[76:79],a[200:203],a[68:71],v[76:79]\n"
MF "v[68:71],a[200:203],a[64:67],v[68:71]\n"
MF "v[68:71],a[204:207],a[104:107],v[68:71]\n"
LD "v13,s[28:31],s82 offen offset:3072 lds\n"
MF "v[60:63],a[204:207],a[100:103],v[60:63]\n"
MF "v[60:63],a[200:203],a[60:63],v[60:63]\n"
"s_add_u32 s14,s81,896\n"
"s_sub_u32 s15,s14,1280\n"
"s_waitcnt lgkmcnt(4)\n"
MF "v[96:99],a[208:211],a[60:63],v[96:99]\n"
"s_min_u32 s14,s14,s15\n"
"s_add_u32 s14,s14,s68\n"
TR "a[200:201],v1 offset:16384\n"
MF "v[96:99],a[212:215],a[100:103],v[96:99]\n"
"s_add_u32 s84,s14,0\n"
"s_add_u32 m0,s63,20480\n"
TR "a[202:203],v1 offset:16896\n"
MF "v[104:107],a[212:215],a[104:107],v[104:107]\n"
LD "v4,s[24:27],s84 offen lds\n"
MF "v[104:107],a[208:211],a[64:67],v[104:107]\n"
TR "a[204:205],v1 offset:20480\n"
MF "v[112:115],a[208:211],a[68:71],v[112:115]\n"
TR "a[206:207],v1 offset:20992\n"
MF "v[112:115],a[212:215],a[108:111],v[112:115]\n"
MF "v[120:123],a[212:215],a[112:115],v[120:123]\n"
"s_add_u32 s84,s14,9216\n"
MF "v[120:123],a[208:211],a[72:75],v[120:123]\n"
LD "v5,s[24:27],s84 offen offset:1024 lds\n"
MF "v[128:131],a[208:211],a[76:79],v[128:131]\n"
MF "v[128:131],a[212:215],a[116:119],v[128:131]\n"
"s_waitcnt lgkmcnt(4)\n"
MF "v[132:135],a[220:223],a[116:119],v[132:135]\n"
TR "a[208:209],v1 offset:16392\n"
MF "v[132:135],a[216:219],a[76:79],v[132:135]\n"
TR "a[210:211],v1 offset:16904\n"
MF "v[124:127],a[216:219],a[72:75],v[124:127]\n"
MF "v[124:127],a[220:223],a[112:115],v[124:127]\n"
TR "a[212:213],v1 offset:20488\n"
MF "v[116:119],a[220:223],a[108:111],v[116:119]\n"
TR "a[214:215],v1 offset:21000\n"
MF "v[116:119],a[216:219],a[68:71],v[116:119]\n"
MF "v[108:111],a[216:219],a[64:67],v[108:111]\n"
MF "v[108:111],a[220:223],a[104:107],v[108:111]\n"
MF "v[100:103],a[220:223],a[100:103],v[100:103]\n"
MF "v[100:103],a[216:219],a[60:63],v[100:103]\n"
"s_waitcnt vmcnt(25) lgkmcnt(0)\n"
"s_barrier\n"
MF "v[136:139],a[200:203],a[60:63],v[136:139]\n"
TR "a[216:217],v0 offset:32768\n"
MF "v[136:139],a[204:207],a[100:103],v[136:139]\n"
"s_add_u32 m0,s62,16384\n"
TR "a[218:219],v0 offset:33280\n"
MF "v[144:147],a[204:207],a[104:107],v[144:147]\n"
"s_add_u32 s82,s66,4718720\n"
"ds_read_b128 a[140:143],v2 offset:40960\n"
MF "v[144:147],a[200:203],a[64:67],v[144:147]\n"
"s_sub_u32 s15,s82,7864320\n"
TR "a[220:221],v0 offset:36864\n"
MF "v[152:155],a[200:203],a[68:71],v[152:155]\n"
"s_min_u32 s82,s82,s15\n"
TR "a[222:223],v0 offset:37376\n"
MF "v[152:155],a[204:207],a[108:111],v[152:155]\n"
LD "v6,s[28:31],s82 offen lds\n"
MF "v[160:163],a[204:207],a[112:115],v[160:163]\n"
"ds_read_b128 a[180:183],v3 offset:40960\n"
MF "v[160:163],a[200:203],a[72:75],v[160:163]\n"
MF "v[168:171],a[200:203],a[76:79],v[168:171]\n"
MF "v[168:171],a[204:207],a[116:119],v[168:171]\n"
LD "v11,s[28:31],s82 offen offset:1024 lds\n"
MF "v[172:175],a[212:215],a[116:119],v[172:175]\n"
TR "a[200:201],v0 offset:32776\n"
MF "v[172:175],a[208:211],a[76:79],v[172:175]\n"
TR "a[202:203],v0 offset:33288\n"
MF "v[164:167],a[208:211],a[72:75],v[164:167]\n"
"ds_read_b128 a[144:147],v2 offset:43008\n"
MF "v[164:167],a[212:215],a[112:115],v[164:167]\n"
TR "a[204:205],v0 offset:36872\n"
MF "v[156:159],a[212:215],a[108:111],v[156:159]\n"
TR "a[206:207],v0 offset:37384\n"
MF "v[156:159],a[208:211],a[68:71],v[156:159]\n"
LD "v12,s[28:31],s82 offen offset:2048 lds\n"
MF "v[148:151],a[208:211],a[64:67],v[148:151]\n"
"ds_read_b128 a[184:187],v3 offset:43008\n"
MF "v[148:151],a[212:215],a[104:107],v[148:151]\n"
MF "v[140:143],a[212:215],a[100:103],v[140:143]\n"
MF "v[140:143],a[208:211],a[60:63],v[140:143]\n"
"s_waitcnt lgkmcnt(7)\n"
LD "v13,s[28:31],s82 offen offset:3072 lds\n"
MF "v[176:179],a[216:219],a[60:63],v[176:179]\n"
TR "a[208:209],v1 offset:32768\n"
MF "v[176:179],a[220:223],a[100:103],v[176:179]\n"
TR "a[210:211],v1 offset:33280\n"
MF "v[184:187],a[220:223],a[104:107],v[184:187]\n"
"ds_read_b128 a[148:151],v2 offset:45056\n"
MF "v[184:187],a[216:219],a[64:67],v[184:187]\n"
"s_add_u32 s84,s14,20480\n"
TR "a[212:213],v1 offset:36864\n"
MF "v[192:195],a[216:219],a[68:71],v[192:195]\n"
"s_add_u32 m0,s63,22528\n"
TR "a[214:215],v1 offset:37376\n"
MF "v[192:195],a[220:223],a[108:111],v[192:195]\n"
LD "v4,s[24:27],s84 offen lds\n"
MF "v[200:203],a[220:223],a[112:115],v[200:203]\n"
"ds_read_b128 a[188:191],v3 offset:45056\n"
MF "v[200:203],a[216:219],a[72:75],v[200:203]\n"
MF "v[208:211],a[216:219],a[76:79],v[208:211]\n"
"s_add_u32 s84,s14,29696\n"
MF "v[208:211],a[220:223],a[116:119],v[208:211]\n"
"s_waitcnt lgkmcnt(7)\n"
LD "v5,s[24:27],s84 offen offset:1024 lds\n"
MF "v[212:215],a[204:207],a[116:119],v[212:215]\n"
TR "a[216:217],v1 offset:32776\n"
MF "v[212:215],a[200:203],a[76:79],v[212:215]\n"
TR "a[218:219],v1 offset:33288\n"
MF "v[204:207],a[200:203],a[72:75],v[204:207]\n"
"ds_read_b128 a[152:155],v2 offset:47104\n"
MF "v[204:207],a[204:207],a[112:115],v[204:207]\n"
TR "a[220:221],v1 offset:36872\n"
MF "v[196:199],a[204:207],a[108:111],v[196:199]\n"
TR "a[222:223],v1 offset:37384\n"
MF "v[196:199],a[200:203],a[68:71],v[196:199]\n"
MF "v[188:191],a[200:203],a[64:67],v[188:191]\n"
"v_add_u32 v0,0xc000,v0\n"
MF "v[188:191],a[204:207],a[104:107],v[188:191]\n"
"v_add_u32 v1,0xc000,v1\n"
MF "v[180:183],a[204:207],a[100:103],v[180:183]\n"
MF "v[180:183],a[200:203],a[60:63],v[180:183]\n"
"s_waitcnt vmcnt(26) lgkmcnt(0)\n"
"s_barrier\n"
MF "v[216:219],a[208:211],a[60:63],v[216:219]\n"
TR "a[200:201],v0\n"
MF "v[216:219],a[212:215],a[100:103],v[216:219]\n"
"s_add_u32 m0,s62,32768\n"
TR "a[202:203],v0 offset:512\n"
MF "v[224:227],a[212:215],a[104:107],v[224:227]\n"
"s_add_u32 s82,s66,4718848\n"
"ds_read_b128 a[192:195],v3 offset:47104\n"
MF "v[224:227],a[208:211],a[64:67],v[224:227]\n"
"s_sub_u32 s15,s82,7864320\n"
TR "a[204:205],v0 offset:4096\n"
MF "v[232:235],a[208:211],a[68:71],v[232:235]\n"
"s_min_u32 s82,s82,s15\n"
TR "a[206:207],v0 offset:4608\n"
MF "v[232:235],a[212:215],a[108:111],v[232:235]\n"
LD "v6,s[28:31],s82 offen lds\n"
MF "v[240:243],a[212:215],a[112:115],v[240:243]\n"
"ds_read_b128 a[156:159],v2 offset:49152\n"
MF "v[240:243],a[208:211],a[72:75],v[240:243]\n"
"ds_read_b128 a[196:199],v3 offset:49152\n"
MF "v[248:251],a[208:211],a[76:79],v[248:251]\n"
MF "v[248:251],a[212:215],a[116:119],v[248:251]\n"
LD "v11,s[28:31],s82 offen offset:1024 lds\n"
MF "v[252:255],a[220:223],a[116:119],v[252:255]\n"
TR "a[208:209],v0 offset:8\n"
MF "v[252:255],a[216:219],a[76:79],v[252:255]\n"
TR "a[210:211],v0 offset:520\n"
MF "v[244:247],a[216:219],a[72:75],v[244:247]\n"
MF "v[244:247],a[220:223],a[112:115],v[244:247]\n"
TR "a[212:213],v0 offset:4104\n"
MF "v[236:239],a[220:223],a[108:111],v[236:239]\n"
TR "a[214:215],v0 offset:4616\n"
MF "v[236:239],a[216:219],a[68:71],v[236:239]\n"
LD "v12,s[28:31],s82 offen offset:2048 lds\n"
MF "v[228:231],a[216:219],a[64:67],v[228:231]\n"
MF "v[228:231],a[220:223],a[104:107],v[228:231]\n"
MF "v[220:223],a[220:223],a[100:103],v[220:223]\n"
MF "v[220:223],a[216:219],a[60:63],v[220:223]\n"
"s_waitcnt lgkmcnt(4)\n"
LD "v13,s[28:31],s82 offen offset:3072 lds\n"
MF "v[16:19],a[200:203],a[140:143],v[16:19]\n"
TR "a[216:217],v1\n"
MF "v[16:19],a[204:207],a[180:183],v[16:19]\n"
TR "a[218:219],v1 offset:512\n"
MF "v[24:27],a[204:207],a[184:187],v[24:27]\n"
MF "v[24:27],a[200:203],a[144:147],v[24:27]\n"
"s_add_u32 s84,s14,40960\n"
TR "a[220:221],v1 offset:4096\n"
MF "v[32:35],a[200:203],a[148:151],v[32:35]\n"
"s_add_u32 m0,s63,24576\n"
TR "a[222:223],v1 offset:4608\n"
MF "v[32:35],a[204:207],a[188:191],v[32:35]\n"
LD "v4,s[24:27],s84 offen lds\n"
MF "v[40:43],a[204:207],a[192:195],v[40:43]\n"
MF "v[40:43],a[200:203],a[152:155],v[40:43]\n"
MF "v[48:51],a[200:203],a[156:159],v[48:51]\n"
MF "v[48:51],a[204:207],a[196:199],v[48:51]\n"
"s_waitcnt lgkmcnt(4)\n"
MF "v[52:55],a[212:215],a[196:199],v[52:55]\n"
TR "a[200:201],v1 offset:8\n"
MF "v[52:55],a[208:211],a[156:159],v[52:55]\n"
TR "a[202:203],v1 offset:520\n"
MF "v[44:47],a[208:211],a[152:155],v[44:47]\n"
MF "v[44:47],a[212:215],a[192:195],v[44:47]\n"
TR "a[204:205],v1 offset:4104\n"
MF "v[36:39],a[212:215],a[188:191],v[36:39]\n"
TR "a[206:207],v1 offset:4616\n"
MF "v[36:39],a[208:211],a[148:151],v[36:39]\n"
MF "v[28:31],a[208:211],a[144:147],v[28:31]\n"
MF "v[28:31],a[212:215],a[184:187],v[28:31]\n"
MF "v[20:23],a[212:215],a[180:183],v[20:23]\n"
MF "v[20:23],a[208:211],a[140:143],v[20:23]\n"
"s_waitcnt vmcnt(25) lgkmcnt(0)\n"
"s_barrier\n"
MF "v[56:59],a[216:219],a[140:143],v[56:59]\n"
"s_add_u32 m0,s62,49152\n"
"s_add_u32 s82,s66,5505024\n"
TR "a[208:209],v0 offset:16384\n"
MF "v[56:59],a[220:223],a[180:183],v[56:59]\n"
"s_sub_u32 s15,s82,7864320\n"
"s_min_u32 s82,s82,s15\n"
TR "a[210:211],v0 offset:16896\n"
MF "v[64:67],a[220:223],a[184:187],v[64:67]\n"
LD "v6,s[28:31],s82 offen lds\n"
MF "v[64:67],a[216:219],a[144:147],v[64:67]\n"
TR "a[212:213],v0 offset:20480\n"
MF "v[72:75],a[216:219],a[148:151],v[72:75]\n"
TR "a[214:215],v0 offset:20992\n"
MF "v[72:75],a[220:223],a[188:191],v[72:75]\n"
"buffer_load_dword a249,v15,s[48:51],s59 offen\n"
MF "v[80:83],a[220:223],a[192:195],v[80:83]\n"
"s_bfe_u32 s59,s23,0x30003\n"
MF "v[80:83],a[216:219],a[152:155],v[80:83]\n"
LD "v11,s[28:31],s82 offen offset:1024 lds\n"
MF "v[88:91],a[216:219],a[156:159],v[88:91]\n"
"s_add_u32 s59,s59,5\n"
MF "v[88:91],a[220:223],a[196:199],v[88:91]\n"
"s_lshl_b32 s59,s59,7\n"
MF "v[92:95],a[204:207],a[196:199],v[92:95]\n"
"s_and_b32 s59,s59,768\n"
TR "a[216:217],v0 offset:16392\n"
MF "v[92:95],a[200:203],a[156:159],v[92:95]\n"
"s_add_u32 s59,s59,s60\n"
TR "a[218:219],v0 offset:16904\n"
MF "v[84:87],a[200:203],a[152:155],v[84:87]\n"
LD "v12,s[28:31],s82 offen offset:2048 lds\n"
MF "v[84:87],a[204:207],a[192:195],v[84:87]\n"
TR "a[220:221],v0 offset:20488\n"
MF "v[76:79],a[204:207],a[188:191],v[76:79]\n"
TR "a[222:223],v0 offset:21000\n"
MF "v[76:79],a[200:203],a[148:151],v[76:79]\n"
MF "v[68:71],a[200:203],a[144:147],v[68:71]\n"
MF "v[68:71],a[204:207],a[184:187],v[68:71]\n"
".Lpg3a:\n"
"s_cbranch_execz .Lisr1\n"
LD "v13,s[28:31],s82 offen offset:3072 lds\n"
MF "v[60:63],a[204:207],a[180:183],v[60:63]\n"
MF "v[60:63],a[200:203],a[140:143],v[60:63]\n"
"s_add_u32 s14,s81,1024\n"
"s_sub_u32 s15,s14,1280\n"
"s_waitcnt lgkmcnt(4)\n"
MF "v[96:99],a[208:211],a[140:143],v[96:99]\n"
"s_min_u32 s14,s14,s15\n"
"s_add_u32 s14,s14,s68\n"
TR "a[200:201],v1 offset:16384\n"
MF "v[96:99],a[212:215],a[180:183],v[96:99]\n"
"s_add_u32 s84,s14,0\n"
"s_add_u32 m0,s63,40960\n"
TR "a[202:203],v1 offset:16896\n"
MF "v[104:107],a[212:215],a[184:187],v[104:107]\n"
LD "v4,s[24:27],s84 offen lds\n"
MF "v[104:107],a[208:211],a[144:147],v[104:107]\n"
TR "a[204:205],v1 offset:20480\n"
MF "v[112:115],a[208:211],a[148:151],v[112:115]\n"
TR "a[206:207],v1 offset:20992\n"
MF "v[112:115],a[212:215],a[188:191],v[112:115]\n"
MF "v[120:123],a[212:215],a[192:195],v[120:123]\n"
"s_add_u32 s84,s14,9216\n"
MF "v[120:123],a[208:211],a[152:155],v[120:123]\n"
LD "v5,s[24:27],s84 offen offset:1024 lds\n"
MF "v[128:131],a[208:211],a[156:159],v[128:131]\n"
MF "v[128:131],a[212:215],a[196:199],v[128:131]\n"
"s_waitcnt lgkmcnt(4)\n"
MF "v[132:135],a[220:223],a[196:199],v[132:135]\n"
TR "a[208:209],v1 offset:16392\n"
MF "v[132:135],a[216:219],a[156:159],v[132:135]\n"
TR "a[210:211],v1 offset:16904\n"
MF "v[124:127],a[216:219],a[152:155],v[124:127]\n"
MF "v[124:127],a[220:223],a[192:195],v[124:127]\n"
TR "a[212:213],v1 offset:20488\n"
MF "v[116:119],a[220:223],a[188:191],v[116:119]\n"
TR "a[214:215],v1 offset:21000\n"
MF "v[116:119],a[216:219],a[148:151],v[116:119]\n"
MF "v[108:111],a[216:219],a[144:147],v[108:111]\n"
MF "v[108:111],a[220:223],a[184:187],v[108:111]\n"
MF "v[100:103],a[220:223],a[180:183],v[100:103]\n"
MF "v[100:103],a[216:219],a[140:143],v[100:103]\n"
"s_waitcnt vmcnt(25) lgkmcnt(0)\n"
"s_barrier\n"
MF "v[136:139],a[200:203],a[140:143],v[136:139]\n"
TR "a[216:217],v0 offset:32768\n"
MF "v[136:139],a[204:207],a[180:183],v[136:139]\n"
"s_add_u32 m0,s62,65536\n"
TR "a[218:219],v0 offset:33280\n"
MF "v[144:147],a[204:207],a[184:187],v[144:147]\n"
"s_add_u32 s82,s66,5505152\n"
"ds_read_b128 a[60:63],v2\n"
MF "v[144:147],a[200:203],a[144:147],v[144:147]\n"
"s_sub_u32 s15,s82,7864320\n"
TR "a[220:221],v0 offset:36864\n"
MF "v[152:155],a[200:203],a[148:151],v[152:155]\n"
"s_min_u32 s82,s82,s15\n"
TR "a[222:223],v0 offset:37376\n"
MF "v[152:155],a[204:207],a[188:191],v[152:155]\n"
LD "v6,s[28:31],s82 offen lds\n"
MF "v[160:163],a[204:207],a[192:195],v[160:163]\n"
"ds_read_b128 a[100:103],v3\n"
MF "v[160:163],a[200:203],a[152:155],v[160:163]\n"
MF "v[168:171],a[200:203],a[156:159],v[168:171]\n"
MF "v[168:171],a[204:207],a[196:199],v[168:171]\n"
LD "v11,s[28:31],s82 offen offset:1024 lds\n"
MF "v[172:175],a[212:215],a[196:199],v[172:175]\n"
TR "a[200:201],v0 offset:32776\n"
MF "v[172:175],a[208:211],a[156:159],v[172:175]\n"
TR "a[202:203],v0 offset:33288\n"
MF "v[164:167],a[208:211],a[152:155],v[164:167]\n"
"ds_read_b128 a[64:67],v2 offset:2048\n"
MF "v[164:167],a[212:215],a[192:195],v[164:167]\n"
TR "a[204:205],v0 offset:36872\n"
MF "v[156:159],a[212:215],a[188:191],v[156:159]\n"
TR "a[206:207],v0 offset:37384\n"
MF "v[156:159],a[208:211],a[148:151],v[156:159]\n"
LD "v12,s[28:31],s82 offen offset:2048 lds\n"
MF "v[148:151],a[208:211],a[144:147],v[148:151]\n"
"ds_read_b128 a[104:107],v3 offset:2048\n"
MF "v[148:151],a[212:215],a[184:187],v[148:151]\n"
MF "v[140:143],a[212:215],a[180:183],v[140:143]\n"
MF "v[140:143],a[208:211],a[140:143],v[140:143]\n"
"s_waitcnt lgkmcnt(7)\n"
LD "v13,s[28:31],s82 offen offset:3072 lds\n"
MF "v[176:179],a[216:219],a[140:143],v[176:179]\n"
TR "a[208:209],v1 offset:32768\n"
MF "v[176:179],a[220:223],a[180:183],v[176:179]\n"
TR "a[210:211],v1 offset:33280\n"
MF "v[184:187],a[220:223],a[184:187],v[184:187]\n"
"ds_read_b128 a[68:71],v2 offset:4096\n"
MF "v[184:187],a[216:219],a[144:147],v[184:187]\n"
"s_add_u32 s84,s14,20480\n"
TR "a[212:213],v1 offset:36864\n"
MF "v[192:195],a[216:219],a[148:151],v[192:195]\n"
"s_add_u32 m0,s63,43008\n"
TR "a[214:215],v1 offset:37376\n"
MF "v[192:195],a[220:223],a[188:191],v[192:195]\n"
LD "v4,s[24:27],s84 offen lds\n"
MF "v[200:203],a[220:223],a[192:195],v[200:203]\n"
"ds_read_b128 a[108:111],v3 offset:4096\n"
MF "v[200:203],a[216:219],a[152:155],v[200:203]\n"
MF "v[208:211],a[216:219],a[156:159],v[208:211]\n"
"s_add_u32 s84,s14,29696\n"
MF "v[208:211],a[220:223],a[196:199],v[208:211]\n"
"s_waitcnt lgkmcnt(7)\n"
LD "v5,s[24:27],s84 offen offset:1024 lds\n"
MF "v[212:215],a[204:207],a[196:199],v[212:215]\n"
TR "a[216:217],v1 offset:32776\n"
MF "v[212:215],a[200:203],a[156:159],v[212:215]\n"
TR "a[218:219],v1 offset:33288\n"
MF "v[204:207],a[200:203],a[152:155],v[204:207]\n"
"ds_read_b128 a[72:75],v2 offset:6144\n"
MF "v[204:207],a[204:207],a[192:195],v[204:207]\n"
TR "a[220:221],v1 offset:36872\n"
MF "v[196:199],a[204:207],a[188:191],v[196:199]\n"
TR "a[222:223],v1 offset:37384\n"
MF "v[196:199],a[200:203],a[148:151],v[196:199]\n"
MF "v[188:191],a[200:203],a[144:147],v[188:191]\n"
"v_add_u32 v0,0xffff4000,v0\n"
MF "v[188:191],a[204:207],a[184:187],v[188:191]\n"
"v_add_u32 v1,0xffff4000,v1\n"
MF "v[180:183],a[204:207],a[180:183],v[180:183]\n"
MF "v[180:183],a[200:203],a[140:143],v[180:183]\n"
"s_waitcnt vmcnt(26) lgkmcnt(0)\n"
"s_barrier\n"
MF "v[216:219],a[208:211],a[140:143],v[216:219]\n"
TR "a[200:201],v0\n"
MF "v[216:219],a[212:215],a[180:183],v[216:219]\n"
"s_add_u32 m0,s62,81920\n"
TR "a[202:203],v0 offset:512\n"
MF "v[224:227],a[212:215],a[184:187],v[224:227]\n"
"s_add_u32 s82,s66,5505280\n"
"ds_read_b128 a[112:115],v3 offset:6144\n"
MF "v[224:227],a[208:211],a[144:147],v[224:227]\n"
"s_sub_u32 s15,s82,7864320\n"
TR "a[204:205],v0 offset:4096\n"
MF "v[232:235],a[208:211],a[148:151],v[232:235]\n"
"s_min_u32 s82,s82,s15\n"
TR "a[206:207],v0 offset:4608\n"
MF "v[232:235],a[212:215],a[188:191],v[232:235]\n"
LD "v6,s[28:31],s82 offen lds\n"
MF "v[240:243],a[212:215],a[192:195],v[240:243]\n"
"ds_read_b128 a[76:79],v2 offset:8192\n"
MF "v[240:243],a[208:211],a[152:155],v[240:243]\n"
"ds_read_b128 a[116:119],v3 offset:8192\n"
MF "v[248:251],a[208:211],a[156:159],v[248:251]\n"
MF "v[248:251],a[212:215],a[196:199],v[248:251]\n"
LD "v11,s[28:31],s82 offen offset:1024 lds\n"
MF "v[252:255],a[220:223],a[196:199],v[252:255]\n"
TR "a[208:209],v0 offset:8\n"
MF "v[252:255],a[216:219],a[156:159],v[252:255]\n"
TR "a[210:211],v0 offset:520\n"
MF "v[244:247],a[216:219],a[152:155],v[244:247]\n"
MF "v[244:247],a[220:223],a[192:195],v[244:247]\n"
TR "a[212:213],v0 offset:4104\n"
MF "v[236:239],a[220:223],a[188:191],v[236:239]\n"
TR "a[214:215],v0 offset:4616\n"
MF "v[236:239],a[216:219],a[148:151],v[236:239]\n"
LD "v12,s[28:31],s82 offen offset:2048 lds\n"
MF "v[228:231],a[216:219],a[144:147],v[228:231]\n"
MF "v[228:231],a[220:223],a[184:187],v[228:231]\n"
MF "v[220:223],a[220:223],a[180:183],v[220:223]\n"
MF "v[220:223],a[216:219],a[140:143],v[220:223]\n"
"s_waitcnt lgkmcnt(4)\n"
LD "v13,s[28:31],s82 offen offset:3072 lds\n"
MF "v[16:19],a[200:203],a[60:63],v[16:19]\n"
TR "a[216:217],v1\n"
MF "v[16:19],a[204:207],a[100:103],v[16:19]\n"
TR "a[218:219],v1 offset:512\n"
MF "v[24:27],a[204:207],a[104:107],v[24:27]\n"
MF "v[24:27],a[200:203],a[64:67],v[24:27]\n"
"s_add_u32 s84,s14,40960\n"
TR "a[220:221],v1 offset:4096\n"
MF "v[32:35],a[200:203],a[68:71],v[32:35]\n"
"s_add_u32 m0,s63,45056\n"
TR "a[222:223],v1 offset:4608\n"
MF "v[32:35],a[204:207],a[108:111],v[32:35]\n"
LD "v4,s[24:27],s84 offen lds\n"
MF "v[40:43],a[204:207],a[112:115],v[40:43]\n"
MF "v[40:43],a[200:203],a[72:75],v[40:43]\n"
MF "v[48:51],a[200:203],a[76:79],v[48:51]\n"
MF "v[48:51],a[204:207],a[116:119],v[48:51]\n"
"s_waitcnt lgkmcnt(4)\n"
MF "v[52:55],a[212:215],a[116:119],v[52:55]\n"
TR "a[200:201],v1 offset:8\n"
MF "v[52:55],a[208:211],a[76:79],v[52:55]\n"
TR "a[202:203],v1 offset:520\n"
MF "v[44:47],a[208:211],a[72:75],v[44:47]\n"
MF "v[44:47],a[212:215],a[112:115],v[44:47]\n"
TR "a[204:205],v1 offset:4104\n"
MF "v[36:39],a[212:215],a[108:111],v[36:39]\n"
TR "a[206:207],v1 offset:4616\n"
MF "v[36:39],a[208:211],a[68:71],v[36:39]\n"
MF "v[28:31],a[208:211],a[64:67],v[28:31]\n"
MF "v[28:31],a[212:215],a[104:107],v[28:31]\n"
MF "v[20:23],a[212:215],a[100:103],v[20:23]\n"
MF "v[20:23],a[208:211],a[60:63],v[20:23]\n"
"s_waitcnt vmcnt(25) lgkmcnt(0)\n"
"s_barrier\n"
MF "v[56:59],a[216:219],a[60:63],v[56:59]\n"
"s_add_u32 m0,s62,0\n"
"s_add_u32 s82,s66,6291712\n"
TR "a[208:209],v0 offset:16384\n"
MF "v[56:59],a[220:223],a[100:103],v[56:59]\n"
"s_sub_u32 s15,s82,7864320\n"
"s_min_u32 s82,s82,s15\n"
TR "a[210:211],v0 offset:16896\n"
MF "v[64:67],a[220:223],a[104:107],v[64:67]\n"
LD "v6,s[28:31],s82 offen lds\n"
MF "v[64:67],a[216:219],a[64:67],v[64:67]\n"
TR "a[212:213],v0 offset:20480\n"
MF "v[72:75],a[216:219],a[68:71],v[72:75]\n"
TR "a[214:215],v0 offset:20992\n"
MF "v[72:75],a[220:223],a[108:111],v[72:75]\n"
"buffer_load_dword a249,v15,s[48:51],s59 offen\n"
MF "v[80:83],a[220:223],a[112:115],v[80:83]\n"
MF "v[80:83],a[216:219],a[72:75],v[80:83]\n"
LD "v11,s[28:31],s82 offen offset:1024 lds\n"
MF "v[88:91],a[216:219],a[76:79],v[88:91]\n"
MF "v[88:91],a[220:223],a[116:119],v[88:91]\n"
MF "v[92:95],a[204:207],a[116:119],v[92:95]\n"
TR "a[216:217],v0 offset:16392\n"
MF "v[92:95],a[200:203],a[76:79],v[92:95]\n"
TR "a[218:219],v0 offset:16904\n"
MF "v[84:87],a[200:203],a[72:75],v[84:87]\n"
LD "v12,s[28:31],s82 offen offset:2048 lds\n"
MF "v[84:87],a[204:207],a[112:115],v[84:87]\n"
TR "a[220:221],v0 offset:20488\n"
MF "v[76:79],a[204:207],a[108:111],v[76:79]\n"
TR "a[222:223],v0 offset:21000\n"
MF "v[76:79],a[200:203],a[68:71],v[76:79]\n"
MF "v[68:71],a[200:203],a[64:67],v[68:71]\n"
MF "v[68:71],a[204:207],a[104:107],v[68:71]\n"
LD "v13,s[28:31],s82 offen offset:3072 lds\n"
MF "v[60:63],a[204:207],a[100:103],v[60:63]\n"
MF "v[60:63],a[200:203],a[60:63],v[60:63]\n"
"s_add_u32 s14,s81,1152\n"
"s_sub_u32 s15,s14,1280\n"
"s_waitcnt lgkmcnt(4)\n"
MF "v[96:99],a[208:211],a[60:63],v[96:99]\n"
"s_min_u32 s14,s14,s15\n"
"s_add_u32 s14,s14,s68\n"
TR "a[200:201],v1 offset:16384\n"
MF "v[96:99],a[212:215],a[100:103],v[96:99]\n"
"s_add_u32 s84,s14,0\n"
"s_add_u32 m0,s63,0\n"
TR "a[202:203],v1 offset:16896\n"
MF "v[104:107],a[212:215],a[104:107],v[104:107]\n"
LD "v4,s[24:27],s84 offen lds\n"
MF "v[104:107],a[208:211],a[64:67],v[104:107]\n"
TR "a[204:205],v1 offset:20480\n"
MF "v[112:115],a[208:211],a[68:71],v[112:115]\n"
TR "a[206:207],v1 offset:20992\n"
MF "v[112:115],a[212:215],a[108:111],v[112:115]\n"
MF "v[120:123],a[212:215],a[112:115],v[120:123]\n"
"s_add_u32 s84,s14,9216\n"
MF "v[120:123],a[208:211],a[72:75],v[120:123]\n"
LD "v5,s[24:27],s84 offen offset:1024 lds\n"
MF "v[128:131],a[208:211],a[76:79],v[128:131]\n"
MF "v[128:131],a[212:215],a[116:119],v[128:131]\n"
"s_waitcnt lgkmcnt(4)\n"
MF "v[132:135],a[220:223],a[116:119],v[132:135]\n"
TR "a[208:209],v1 offset:16392\n"
MF "v[132:135],a[216:219],a[76:79],v[132:135]\n"
"s_add_u32 s84,s14,18432\n"
TR "a[210:211],v1 offset:16904\n"
MF "v[124:127],a[216:219],a[72:75],v[124:127]\n"
LD "v4,s[24:27],s84 offen offset:2048 lds\n"
MF "v[124:127],a[220:223],a[112:115],v[124:127]\n"
TR "a[212:213],v1 offset:20488\n"
MF "v[116:119],a[220:223],a[108:111],v[116:119]\n"
TR "a[214:215],v1 offset:21000\n"
MF "v[116:119],a[216:219],a[68:71],v[116:119]\n"
MF "v[108:111],a[216:219],a[64:67],v[108:111]\n"
MF "v[108:111],a[220:223],a[104:107],v[108:111]\n"
MF "v[100:103],a[220:223],a[100:103],v[100:103]\n"
MF "v[100:103],a[216:219],a[60:63],v[100:103]\n"
"s_waitcnt vmcnt(26) lgkmcnt(0)\n"
"s_barrier\n"
MF "v[136:139],a[200:203],a[60:63],v[136:139]\n"
TR "a[216:217],v0 offset:32768\n"
MF "v[136:139],a[204:207],a[100:103],v[136:139]\n"
"s_add_u32 m0,s62,16384\n"
TR "a[218:219],v0 offset:33280\n"
MF "v[144:147],a[204:207],a[104:107],v[144:147]\n"
"s_add_u32 s82,s66,7078144\n"
"ds_read_b128 a[140:143],v2 offset:20480\n"
MF "v[144:147],a[200:203],a[64:67],v[144:147]\n"
"s_sub_u32 s15,s82,7864320\n"
TR "a[220:221],v0 offset:36864\n"
MF "v[152:155],a[200:203],a[68:71],v[152:155]\n"
"s_min_u32 s82,s82,s15\n"
TR "a[222:223],v0 offset:37376\n"
MF "v[152:155],a[204:207],a[108:111],v[152:155]\n"
LD "v6,s[28:31],s82 offen lds\n"
MF "v[160:163],a[204:207],a[112:115],v[160:163]\n"
"ds_read_b128 a[180:183],v3 offset:20480\n"
MF "v[160:163],a[200:203],a[72:75],v[160:163]\n"
MF "v[168:171],a[200:203],a[76:79],v[168:171]\n"
MF "v[168:171],a[204:207],a[116:119],v[168:171]\n"
LD "v11,s[28:31],s82 offen offset:1024 lds\n"
MF "v[172:175],a[212:215],a[116:119],v[172:175]\n"
TR "a[200:201],v0 offset:32776\n"
MF "v[172:175],a[208:211],a[76:79],v[172:175]\n"
TR "a[202:203],v0 offset:33288\n"
MF "v[164:167],a[208:211],a[72:75],v[164:167]\n"
"ds_read_b128 a[144:147],v2 offset:22528\n"
MF "v[164:167],a[212:215],a[112:115],v[164:167]\n"
TR "a[204:205],v0 offset:36872\n"
MF "v[156:159],a[212:215],a[108:111],v[156:159]\n"
TR "a[206:207],v0 offset:37384\n"
MF "v[156:159],a[208:211],a[68:71],v[156:159]\n"
LD "v12,s[28:31],s82 offen offset:2048 lds\n"
MF "v[148:151],a[208:211],a[64:67],v[148:151]\n"
"ds_read_b128 a[184:187],v3 offset:22528\n"
MF "v[148:151],a[212:215],a[104:107],v[148:151]\n"
MF "v[140:143],a[212:215],a[100:103],v[140:143]\n"
MF "v[140:143],a[208:211],a[60:63],v[140:143]\n"
"s_waitcnt lgkmcnt(7)\n"
LD "v13,s[28:31],s82 offen offset:3072 lds\n"
MF "v[176:179],a[216:219],a[60:63],v[176:179]\n"
TR "a[208:209],v1 offset:32768\n"
MF "v[176:179],a[220:223],a[100:103],v[176:179]\n"
TR "a[210:211],v1 offset:33280\n"
MF "v[184:187],a[220:223],a[104:107],v[184:187]\n"
"ds_read_b128 a[148:151],v2 offset:24576\n"
MF "v[184:187],a[216:219],a[64:67],v[184:187]\n"
"s_add_u32 s84,s14,30720\n"
TR "a[212:213],v1 offset:36864\n"
MF "v[192:195],a[216:219],a[68:71],v[192:195]\n"
"s_add_u32 m0,s63,3072\n"
TR "a[214:215],v1 offset:37376\n"
MF "v[192:195],a[220:223],a[108:111],v[192:195]\n"
LD "v5,s[24:27],s84 offen lds\n"
MF "v[200:203],a[220:223],a[112:115],v[200:203]\n"
"ds_read_b128 a[188:191],v3 offset:24576\n"
MF "v[200:203],a[216:219],a[72:75],v[200:203]\n"
MF "v[208:211],a[216:219],a[76:79],v[208:211]\n"
"s_add_u32 s84,s14,39936\n"
MF "v[208:211],a[220:223],a[116:119],v[208:211]\n"
"s_waitcnt lgkmcnt(7)\n"
LD "v4,s[24:27],s84 offen offset:1024 lds\n"
MF "v[212:215],a[204:207],a[116:119],v[212:215]\n"
TR "a[216:217],v1 offset:32776\n"
MF "v[212:215],a[200:203],a[76:79],v[212:215]\n"
TR "a[218:219],v1 offset:33288\n"
MF "v[204:207],a[200:203],a[72:75],v[204:207]\n"
"ds_read_b128 a[152:155],v2 offset:26624\n"
MF "v[204:207],a[204:207],a[112:115],v[204:207]\n"
TR "a[220:221],v1 offset:36872\n"
MF "v[196:199],a[204:207],a[108:111],v[196:199]\n"
TR "a[222:223],v1 offset:37384\n"
MF "v[196:199],a[200:203],a[68:71],v[196:199]\n"
MF "v[188:191],a[200:203],a[64:67],v[188:191]\n"
"v_add_u32 v0,0xc000,v0\n"
MF "v[188:191],a[204:207],a[104:107],v[188:191]\n"
"v_add_u32 v1,0xc000,v1\n"
MF "v[180:183],a[204:207],a[100:103],v[180:183]\n"
"s_branch .Lqj\n"
".Lpgq0_c:\n"
"s_cbranch_execz .Lpgqy_c\n"
".Lpgq0_f:\n"
"s_cbranch_execz .Lpg12f\n"
".Lq0:\n"
MF "v[16:19],a[200:203],a[60:63],0\n"
"s_add_u32 m0,s62,49152\n"
"s_add_u32 s82,s66,786432\n"
TR "a[216:217],v1\n"
MF "v[16:19],a[204:207],a[100:103],v[16:19]\n"
"s_sub_u32 s15,s82,7864320\n"
"s_min_u32 s82,s82,s15\n"
TR "a[218:219],v1 offset:512\n"
MF "v[24:27],a[204:207],a[104:107],0\n"
LD "v6,s[28:31],s82 offen lds\n"
MF "v[24:27],a[200:203],a[64:67],v[24:27]\n"
TR "a[220:221],v1 offset:4096\n"
MF "v[32:35],a[200:203],a[68:71],0\n"
TR "a[222:223],v1 offset:4608\n"
MF "v[32:35],a[204:207],a[108:111],v[32:35]\n"
"v_max3_f32 v220,|v16|,|v17|,|v18|\n"
"v_max_f32_e64 v220,v220,|v19|\n"
MF "v[40:43],a[204:207],a[112:115],0\n"
"v_readlane_b32 s12,v220,3\n"
"v_readlane_b32 s6,v220,22\n"
MF "v[40:43],a[200:203],a[72:75],v[40:43]\n"
"s_min_u32 s12,s12,s6\n"
"v_readlane_b32 s7,v220,41\n"
"v_readlane_b32 s8,v220,60\n"
MF "v[48:51],a[200:203],a[76:79],0\n"
"s_min_u32 s7,s7,s8\n"
MF "v[48:51],a[204:207],a[116:119],v[48:51]\n"
LD "v11,s[28:31],s82 offen offset:1024 lds\n"
MF "v[52:55],a[212:215],a[116:119],0\n"
"s_min_u32 s12,s12,s7\n"
TR "a[200:201],v1 offset:8\n"
MF "v[52:55],a[208:211],a[76:79],v[52:55]\n"
"s_and_b32 s12,s12,0x7f800000\n"
TR "a[202:203],v1 offset:520\n"
MF "v[44:47],a[208:211],a[72:75],0\n"
"s_sub_u32 s12,s12,0x14000000\n"
MF "v[44:47],a[212:215],a[112:115],v[44:47]\n"
"s_cmp_lt_u32 s12,0x50000000\n"
"s_cselect_b32 s12,s12,0xe8400000\n"
TR "a[204:205],v1 offset:4104\n"
MF "v[36:39],a[212:215],a[108:111],0\n"
"s_add_u32 s12,s12,0x17c00000\n"
TR "a[206:207],v1 offset:4616\n"
MF "v[36:39],a[208:211],a[68:71],v[36:39]\n"
"v_mov_b32 v220,s12\n"
"v_mov_b32 v221,s12\n"
MF "v[28:31],a[208:211],a[64:67],0\n"
"v_mov_b32 v222,s12\n"
"v_mov_b32 v223,s12\n"
MF "v[28:31],a[212:215],a[104:107],v[28:31]\n"
LD "v12,s[28:31],s82 offen offset:2048 lds\n"
MF "v[20:23],a[212:215],a[100:103],0\n"
MF "v[20:23],a[208:211],a[60:63],v[20:23]\n"
"s_waitcnt vmcnt(7) lgkmcnt(0)\n"
"s_barrier\n"
MF "v[56:59],a[216:219],a[60:63],v[220:223]\n"
TR "a[208:209],v0 offset:16384\n"
MF "v[56:59],a[220:223],a[100:103],v[56:59]\n"
TR "a[210:211],v0 offset:16896\n"
MF "v[64:67],a[220:223],a[104:107],v[220:223]\n"
LD "v13,s[28:31],s82 offen offset:3072 lds\n"
MF "v[64:67],a[216:219],a[64:67],v[64:67]\n"
"s_add_u32 m0,s62,65536\n"
TR "a[212:213],v0 offset:20480\n"
MF "v[72:75],a[216:219],a[68:71],v[220:223]\n"
"s_add_u32 s82,s66,786560\n"
TR "a[214:215],v0 offset:20992\n"
MF "v[72:75],a[220:223],a[108:111],v[72:75]\n"
"s_sub_u32 s15,s82,7864320\n"
MF "v[80:83],a[220:223],a[112:115],v[220:223]\n"
"s_min_u32 s82,s82,s15\n"
MF "v[80:83],a[216:219],a[72:75],v[80:83]\n"
LD "v6,s[28:31],s82 offen lds\n"
MF "v[88:91],a[216:219],a[76:79],v[220:223]\n"
MF "v[88:91],a[220:223],a[116:119],v[88:91]\n"
MF "v[92:95],a[204:207],a[116:119],v[220:223]\n"
TR "a[216:217],v0 offset:16392\n"
MF "v[92:95],a[200:203],a[76:79],v[92:95]\n"
TR "a[218:219],v0 offset:16904\n"
MF "v[84:87],a[200:203],a[72:75],v[220:223]\n"
LD "v11,s[28:31],s82 offen offset:1024 lds\n"
MF "v[84:87],a[204:207],a[112:115],v[84:87]\n"
TR "a[220:221],v0 offset:20488\n"
MF "v[76:79],a[204:207],a[108:111],v[220:223]\n"
TR "a[222:223],v0 offset:21000\n"
MF "v[76:79],a[200:203],a[68:71],v[76:79]\n"
MF "v[68:71],a[200:203],a[64:67],v[220:223]\n"
MF "v[68:71],a[204:207],a[104:107],v[68:71]\n"
LD "v12,s[28:31],s82 offen offset:2048 lds\n"
MF "v[60:63],a[204:207],a[100:103],v[220:223]\n"
MF "v[60:63],a[200:203],a[60:63],v[60:63]\n"
"s_waitcnt lgkmcnt(4)\n"
MF "v[96:99],a[208:211],a[60:63],v[220:223]\n"
TR "a[200:201],v1 offset:16384\n"
MF "v[96:99],a[212:215],a[100:103],v[96:99]\n"
TR "a[202:203],v1 offset:16896\n"
MF "v[104:107],a[212:215],a[104:107],v[220:223]\n"
LD "v13,s[28:31],s82 offen offset:3072 lds\n"
MF "v[104:107],a[208:211],a[64:67],v[104:107]\n"
"s_add_u32 m0,s62,81920\n"
TR "a[204:205],v1 offset:20480\n"
MF "v[112:115],a[208:211],a[68:71],v[220:223]\n"
"s_add_u32 s82,s66,786688\n"
TR "a[206:207],v1 offset:20992\n"
MF "v[112:115],a[212:215],a[108:111],v[112:115]\n"
"s_sub_u32 s15,s82,7864320\n"
MF "v[120:123],a[212:215],a[112:115],v[220:223]\n"
"s_min_u32 s82,s82,s15\n"
MF "v[120:123],a[208:211],a[72:75],v[120:123]\n"
LD "v6,s[28:31],s82 offen lds\n"
MF "v[128:131],a[208:211],a[76:79],v[220:223]\n"
MF "v[128:131],a[212:215],a[116:119],v[128:131]\n"
"s_waitcnt lgkmcnt(4)\n"
MF "v[132:135],a[220:223],a[116:119],v[220:223]\n"
TR "a[208:209],v1 offset:16392\n"
MF "v[132:135],a[216:219],a[76:79],v[132:135]\n"
TR "a[210:211],v1 offset:16904\n"
MF "v[124:127],a[216:219],a[72:75],v[220:223]\n"
LD "v11,s[28:31],s82 offen offset:1024 lds\n"
MF "v[124:127],a[220:223],a[112:115],v[124:127]\n"
TR "a[212:213],v1 offset:20488\n"
MF "v[116:119],a[220:223],a[108:111],v[220:223]\n"
TR "a[214:215],v1 offset:21000\n"
MF "v[116:119],a[216:219],a[68:71],v[116:119]\n"
MF "v[108:111],a[216:219],a[64:67],v[220:223]\n"
MF "v[108:111],a[220:223],a[104:107],v[108:111]\n"
LD "v12,s[28:31],s82 offen offset:2048 lds\n"
MF "v[100:103],a[220:223],a[100:103],v[220:223]\n"
MF "v[100:103],a[216:219],a[60:63],v[100:103]\n"
"s_waitcnt vmcnt(11) lgkmcnt(0)\n"
"s_barrier\n"
MF "v[136:139],a[200:203],a[60:63],v[220:223]\n"
TR "a[216:217],v0 offset:32768\n"
MF "v[136:139],a[204:207],a[100:103],v[136:139]\n"
TR "a[218:219],v0 offset:33280\n"
MF "v[144:147],a[204:207],a[104:107],v[220:223]\n"
"ds_read_b128 a[140:143],v2 offset:20480\n"
MF "v[144:147],a[200:203],a[64:67],v[144:147]\n"
TR "a[220:221],v0 offset:36864\n"
MF "v[152:155],a[200:203],a[68:71],v[220:223]\n"
TR "a[222:223],v0 offset:37376\n"
MF "v[152:155],a[204:207],a[108:111],v[152:155]\n"
LD "v13,s[28:31],s82 offen offset:3072 lds\n"
MF "v[160:163],a[204:207],a[112:115],v[220:223]\n"
"ds_read_b128 a[180:183],v3 offset:20480\n"
MF "v[160:163],a[200:203],a[72:75],v[160:163]\n"
"s_add_u32 m0,s62,0\n"
MF "v[168:171],a[200:203],a[76:79],v[220:223]\n"
"s_add_u32 s82,s66,1572864\n"
"s_sub_u32 s15,s82,7864320\n"
"s_min_u32 s82,s82,s15\n"
MF "v[168:171],a[204:207],a[116:119],v[168:171]\n"
LD "v6,s[28:31],s82 offen lds\n"
MF "v[172:175],a[212:215],a[116:119],v[220:223]\n"
TR "a[200:201],v0 offset:32776\n"
MF "v[172:175],a[208:211],a[76:79],v[172:175]\n"
TR "a[202:203],v0 offset:33288\n"
MF "v[164:167],a[208:211],a[72:75],v[220:223]\n"
"ds_read_b128 a[144:147],v2 offset:22528\n"
MF "v[164:167],a[212:215],a[112:115],v[164:167]\n"
TR "a[204:205],v0 offset:36872\n"
MF "v[156:159],a[212:215],a[108:111],v[220:223]\n"
TR "a[206:207],v0 offset:37384\n"
MF "v[156:159],a[208:211],a[68:71],v[156:159]\n"
LD "v11,s[28:31],s82 offen offset:1024 lds\n"
MF "v[148:151],a[208:211],a[64:67],v[220:223]\n"
"ds_read_b128 a[184:187],v3 offset:22528\n"
MF "v[148:151],a[212:215],a[104:107],v[148:151]\n"
MF "v[140:143],a[212:215],a[100:103],v[220:223]\n"
MF "v[140:143],a[208:211],a[60:63],v[140:143]\n"
"s_waitcnt lgkmcnt(7)\n"
LD "v12,s[28:31],s82 offen offset:2048 lds\n"
MF "v[176:179],a[216:219],a[60:63],v[220:223]\n"
TR "a[208:209],v1 offset:32768\n"
MF "v[176:179],a[220:223],a[100:103],v[176:179]\n"
TR "a[210:211],v1 offset:33280\n"
MF "v[184:187],a[220:223],a[104:107],v[220:223]\n"
".Lpg4c:\n"
"s_cbranch_execz .Lpg30c\n"
".Lpg4f:\n"
"s_cbranch_execz .LpgattB_f\n"
"ds_read_b128 a[148:151],v2 offset:24576\n"
MF "v[184:187],a[216:219],a[64:67],v[184:187]\n"
TR "a[212:213],v1 offset:36864\n"
MF "v[192:195],a[216:219],a[68:71],v[220:223]\n"
TR "a[214:215],v1 offset:37376\n"
MF "v[192:195],a[220:223],a[108:111],v[192:195]\n"
LD "v13,s[28:31],s82 offen offset:3072 lds\n"
MF "v[200:203],a[220:223],a[112:115],v[220:223]\n"
"ds_read_b128 a[188:191],v3 offset:24576\n"
MF "v[200:203],a[216:219],a[72:75],v[200:203]\n"
"s_add_u32 s14,s81,384\n"
"s_sub_u32 s15,s14,1280\n"
"s_min_u32 s14,s14,s15\n"
MF "v[208:211],a[216:219],a[76:79],v[220:223]\n"
"s_add_u32 s14,s14,s68\n"
"s_add_u32 s84,s14,0\n"
"s_add_u32 m0,s63,0\n"
MF "v[208:211],a[220:223],a[116:119],v[208:211]\n"
"s_waitcnt lgkmcnt(7)\n"
LD "v4,s[24:27],s84 offen lds\n"
MF "v[212:215],a[204:207],a[116:119],v[220:223]\n"
TR "a[216:217],v1 offset:32776\n"
MF "v[212:215],a[200:203],a[76:79],v[212:215]\n"
TR "a[218:219],v1 offset:33288\n"
MF "v[204:207],a[200:203],a[72:75],v[220:223]\n"
"ds_read_b128 a[152:155],v2 offset:26624\n"
MF "v[204:207],a[204:207],a[112:115],v[204:207]\n"
TR "a[220:221],v1 offset:36872\n"
MF "v[196:199],a[204:207],a[108:111],v[220:223]\n"
"s_add_u32 s84,s14,9216\n"
TR "a[222:223],v1 offset:37384\n"
MF "v[196:199],a[200:203],a[68:71],v[196:199]\n"
LD "v5,s[24:27],s84 offen offset:1024 lds\n"
MF "v[188:191],a[200:203],a[64:67],v[220:223]\n"
"s_add_u32 m0,s62,16384\n"
"s_add_u32 s82,s66,1572992\n"
"v_add_u32 v0,0xc000,v0\n"
MF "v[188:191],a[204:207],a[104:107],v[188:191]\n"
"s_sub_u32 s15,s82,7864320\n"
"s_min_u32 s82,s82,s15\n"
"v_add_u32 v1,0xc000,v1\n"
MF "v[180:183],a[204:207],a[100:103],v[220:223]\n"
LD "v6,s[28:31],s82 offen lds\n"
MF "v[180:183],a[200:203],a[60:63],v[180:183]\n"
"s_waitcnt vmcnt(15) lgkmcnt(0)\n"
"s_barrier\n"
MF "v[216:219],a[208:211],a[60:63],v[220:223]\n"
TR "a[200:201],v0\n"
MF "v[216:219],a[212:215],a[100:103],v[216:219]\n"
TR "a[202:203],v0 offset:512\n"
MF "v[224:227],a[212:215],a[104:107],v[220:223]\n"
"ds_read_b128 a[192:195],v3 offset:26624\n"
MF "v[224:227],a[208:211],a[64:67],v[224:227]\n"
TR "a[204:205],v0 offset:4096\n"
MF "v[232:235],a[208:211],a[68:71],v[220:223]\n"
TR "a[206:207],v0 offset:4608\n"
MF "v[232:235],a[212:215],a[108:111],v[232:235]\n"
LD "v11,s[28:31],s82 offen offset:1024 lds\n"
MF "v[240:243],a[212:215],a[112:115],v[220:223]\n"
"ds_read_b128 a[156:159],v2 offset:28672\n"
MF "v[240:243],a[208:211],a[72:75],v[240:243]\n"
"ds_read_b128 a[196:199],v3 offset:28672\n"
MF "v[248:251],a[208:211],a[76:79],v[220:223]\n"
MF "v[248:251],a[212:215],a[116:119],v[248:251]\n"
LD "v12,s[28:31],s82 offen offset:2048 lds\n"
MF "v[252:255],a[220:223],a[116:119],v[220:223]\n"
TR "a[208:209],v0 offset:8\n"
MF "v[252:255],a[216:219],a[76:79],v[252:255]\n"
TR "a[210:211],v0 offset:520\n"
MF "v[244:247],a[216:219],a[72:75],v[220:223]\n"
MF "v[244:247],a[220:223],a[112:115],v[244:247]\n"
TR "a[212:213],v0 offset:4104\n"
MF "v[236:239],a[220:223],a[108:111],v[220:223]\n"
TR "a[214:215],v0 offset:4616\n"
MF "v[236:239],a[216:219],a[68:71],v[236:239]\n"
LD "v13,s[28:31],s82 offen offset:3072 lds\n"
MF "v[228:231],a[216:219],a[64:67],v[220:223]\n"
MF "v[228:231],a[220:223],a[104:107],v[228:231]\n"
"s_add_u32 s84,s14,20480\n"
MF "v[220:223],a[220:223],a[100:103],v[220:223]\n"
"s_add_u32 m0,s63,2048\n"
MF "v[220:223],a[216:219],a[60:63],v[220:223]\n"
"s_waitcnt lgkmcnt(4)\n"
LD "v4,s[24:27],s84 offen lds\n"
MF "v[16:19],a[200:203],a[140:143],v[16:19]\n"
TR "a[216:217],v1\n"
MF "v[16:19],a[204:207],a[180:183],v[16:19]\n"
TR "a[218:219],v1 offset:512\n"
MF "v[24:27],a[204:207],a[184:187],v[24:27]\n"
MF "v[24:27],a[200:203],a[144:147],v[24:27]\n"
TR "a[220:221],v1 offset:4096\n"
MF "v[32:35],a[200:203],a[148:151],v[32:35]\n"
"s_add_u32 s84,s14,29696\n"
TR "a[222:223],v1 offset:4608\n"
MF "v[32:35],a[204:207],a[188:191],v[32:35]\n"
LD "v5,s[24:27],s84 offen offset:1024 lds\n"
MF "v[40:43],a[204:207],a[192:195],v[40:43]\n"
MF "v[40:43],a[200:203],a[152:155],v[40:43]\n"
"s_add_u32 m0,s62,32768\n"
MF "v[48:51],a[200:203],a[156:159],v[48:51]\n"
"s_add_u32 s82,s66,1573120\n"
"s_sub_u32 s15,s82,7864320\n"
"s_min_u32 s82,s82,s15\n"
MF "v[48:51],a[204:207],a[196:199],v[48:51]\n"
"s_waitcnt lgkmcnt(4)\n"
LD "v6,s[28:31],s82 offen lds\n"
MF "v[52:55],a[212:215],a[196:199],v[52:55]\n"
TR "a[200:201],v1 offset:8\n"
MF "v[52:55],a[208:211],a[156:159],v[52:55]\n"
TR "a[202:203],v1 offset:520\n"
MF "v[44:47],a[208:211],a[152:155],v[44:47]\n"
MF "v[44:47],a[212:215],a[192:195],v[44:47]\n"
TR "a[204:205],v1 offset:4104\n"
MF "v[36:39],a[212:215],a[188:191],v[36:39]\n"
TR "a[206:207],v1 offset:4616\n"
MF "v[36:39],a[208:211],a[148:151],v[36:39]\n"
LD "v11,s[28:31],s82 offen offset:1024 lds\n"
MF "v[28:31],a[208:211],a[144:147],v[28:31]\n"
MF "v[28:31],a[212:215],a[184:187],v[28:31]\n"
MF "v[20:23],a[212:215],a[180:183],v[20:23]\n"
LD "v12,s[28:31],s82 offen offset:2048 lds\n"
"s_bitcmp0_b32 s23,12\n"
"s_cbranch_scc1 .Lisx2\n"
"s_mov_b64 exec,0\n"
"s_cmp_eq_u32 s56,0\n"
"s_cbranch_scc1 .Lpg11d\n"
"s_cmp_eq_u32 s56,1\n"
"s_cbranch_scc1 .Lpg10d\n"
"s_cmp_eq_u32 s56,2\n"
"s_cbranch_scc1 .Lpgt0kx_d\n"
"s_branch .Lpg8d\n"
".Lisr2:\n"
"s_mov_b64 exec,-1\n"
".Lisx2:\n"
MF "v[20:23],a[208:211],a[140:143],v[20:23]\n"
"s_waitcnt vmcnt(19) lgkmcnt(0)\n"
"s_barrier\n"
MF "v[56:59],a[216:219],a[140:143],v[56:59]\n"
TR "a[208:209],v0 offset:16384\n"
MF "v[56:59],a[220:223],a[180:183],v[56:59]\n"
TR "a[210:211],v0 offset:16896\n"
MF "v[64:67],a[220:223],a[184:187],v[64:67]\n"
LD "v13,s[28:31],s82 offen offset:3072 lds\n"
MF "v[64:67],a[216:219],a[144:147],v[64:67]\n"
TR "a[212:213],v0 offset:20480\n"
MF "v[72:75],a[216:219],a[148:151],v[72:75]\n"
TR "a[214:215],v0 offset:20992\n"
MF "v[72:75],a[220:223],a[188:191],v[72:75]\n"
"s_add_u32 s84,s14,40960\n"
MF "v[80:83],a[220:223],a[192:195],v[80:83]\n"
"s_add_u32 m0,s63,4096\n"
MF "v[80:83],a[216:219],a[152:155],v[80:83]\n"
LD "v4,s[24:27],s84 offen lds\n"
MF "v[88:91],a[216:219],a[156:159],v[88:91]\n"
MF "v[88:91],a[220:223],a[196:199],v[88:91]\n"
"s_sub_u32 s66,s66,s83\n"
MF "v[92:95],a[204:207],a[196:199],v[92:95]\n"
"s_add_u32 m0,s62,49152\n"
"s_add_u32 s82,s66,2359296\n"
TR "a[216:217],v0 offset:16392\n"
MF "v[92:95],a[200:203],a[156:159],v[92:95]\n"
"s_sub_u32 s15,s82,7864320\n"
"s_min_u32 s82,s82,s15\n"
TR "a[218:219],v0 offset:16904\n"
MF "v[84:87],a[200:203],a[152:155],v[84:87]\n"
LD "v6,s[28:31],s82 offen lds\n"
MF "v[84:87],a[204:207],a[192:195],v[84:87]\n"
TR "a[220:221],v0 offset:20488\n"
MF "v[76:79],a[204:207],a[188:191],v[76:79]\n"
TR "a[222:223],v0 offset:21000\n"
MF "v[76:79],a[200:203],a[148:151],v[76:79]\n"
MF "v[68:71],a[200:203],a[144:147],v[68:71]\n"
MF "v[68:71],a[204:207],a[184:187],v[68:71]\n"
LD "v11,s[28:31],s82 offen offset:1024 lds\n"
MF "v[60:63],a[204:207],a[180:183],v[60:63]\n"
MF "v[60:63],a[200:203],a[140:143],v[60:63]\n"
"s_waitcnt lgkmcnt(4)\n"
MF "v[96:99],a[208:211],a[140:143],v[96:99]\n"
TR "a[200:201],v1 offset:16384\n"
MF "v[96:99],a[212:215],a[180:183],v[96:99]\n"
TR "a[202:203],v1 offset:16896\n"
MF "v[104:107],a[212:215],a[184:187],v[104:107]\n"
LD "v12,s[28:31],s82 offen offset:2048 lds\n"
MF "v[104:107],a[208:211],a[144:147],v[104:107]\n"
TR "a[204:205],v1 offset:20480\n"
MF "v[112:115],a[208:211],a[148:151],v[112:115]\n"
TR "a[206:207],v1 offset:20992\n"
MF "v[112:115],a[212:215],a[188:191],v[112:115]\n"
MF "v[120:123],a[212:215],a[192:195],v[120:123]\n"
MF "v[120:123],a[208:211],a[152:155],v[120:123]\n"
LD "v13,s[28:31],s82 offen offset:3072 lds\n"
MF "v[128:131],a[208:211],a[156:159],v[128:131]\n"
MF "v[128:131],a[212:215],a[196:199],v[128:131]\n"
"s_add_u32 s14,s81,512\n"
"s_sub_u32 s15,s14,1280\n"
"s_waitcnt lgkmcnt(4)\n"
MF "v[132:135],a[220:223],a[196:199],v[132:135]\n"
"s_min_u32 s14,s14,s15\n"
"s_add_u32 s14,s14,s68\n"
TR "a[208:209],v1 offset:16392\n"
MF "v[132:135],a[216:219],a[156:159],v[132:135]\n"
"s_add_u32 s84,s14,0\n"
"s_add_u32 m0,s63,20480\n"
TR "a[210:211],v1 offset:16904\n"
MF "v[124:127],a[216:219],a[152:155],v[124:127]\n"
LD "v4,s[24:27],s84 offen lds\n"
MF "v[124:127],a[220:223],a[192:195],v[124:127]\n"
TR "a[212:213],v1 offset:20488\n"
MF "v[116:119],a[220:223],a[188:191],v[116:119]\n"
TR "a[214:215],v1 offset:21000\n"
MF "v[116:119],a[216:219],a[148:151],v[116:119]\n"
MF "v[108:111],a[216:219],a[144:147],v[108:111]\n"
"s_add_u32 s84,s14,9216\n"
MF "v[108:111],a[220:223],a[184:187],v[108:111]\n"
LD "v5,s[24:27],s84 offen offset:1024 lds\n"
MF "v[100:103],a[220:223],a[180:183],v[100:103]\n"
MF "v[100:103],a[216:219],a[140:143],v[100:103]\n"
"s_waitcnt vmcnt(23) lgkmcnt(0)\n"
"s_barrier\n"
MF "v[136:139],a[200:203],a[140:143],v[136:139]\n"
TR "a[216:217],v0 offset:32768\n"
MF "v[136:139],a[204:207],a[180:183],v[136:139]\n"
"s_add_u32 m0,s62,65536\n"
TR "a[218:219],v0 offset:33280\n"
MF "v[144:147],a[204:207],a[184:187],v[144:147]\n"
"s_add_u32 s82,s66,2359424\n"
"ds_read_b128 a[60:63],v2 offset:40960\n"
MF "v[144:147],a[200:203],a[144:147],v[144:147]\n"
"s_sub_u32 s15,s82,7864320\n"
TR "a[220:221],v0 offset:36864\n"
MF "v[152:155],a[200:203],a[148:151],v[152:155]\n"
"s_min_u32 s82,s82,s15\n"
TR "a[222:223],v0 offset:37376\n"
MF "v[152:155],a[204:207],a[188:191],v[152:155]\n"
LD "v6,s[28:31],s82 offen lds\n"
MF "v[160:163],a[204:207],a[192:195],v[160:163]\n"
"ds_read_b128 a[100:103],v3 offset:40960\n"
MF "v[160:163],a[200:203],a[152:155],v[160:163]\n"
LD "v11,s[28:31],s82 offen offset:1024 lds\n"
MF "v[168:171],a[200:203],a[156:159],v[168:171]\n"
MF "v[168:171],a[204:207],a[196:199],v[168:171]\n"
LD "v12,s[28:31],s82 offen offset:2048 lds\n"
MF "v[172:175],a[212:215],a[196:199],v[172:175]\n"
TR "a[200:201],v0 offset:32776\n"
MF "v[172:175],a[208:211],a[156:159],v[172:175]\n"
TR "a[202:203],v0 offset:33288\n"
MF "v[164:167],a[208:211],a[152:155],v[164:167]\n"
"ds_read_b128 a[64:67],v2 offset:43008\n"
MF "v[164:167],a[212:215],a[192:195],v[164:167]\n"
TR "a[204:205],v0 offset:36872\n"
MF "v[156:159],a[212:215],a[188:191],v[156:159]\n"
TR "a[206:207],v0 offset:37384\n"
MF "v[156:159],a[208:211],a[148:151],v[156:159]\n"
MF "v[148:151],a[208:211],a[144:147],v[148:151]\n"
"ds_read_b128 a[104:107],v3 offset:43008\n"
MF "v[148:151],a[212:215],a[184:187],v[148:151]\n"
MF "v[140:143],a[212:215],a[180:183],v[140:143]\n"
LD "v13,s[28:31],s82 offen offset:3072 lds\n"
MF "v[140:143],a[208:211],a[140:143],v[140:143]\n"
"s_waitcnt lgkmcnt(7)\n"
MF "v[176:179],a[216:219],a[140:143],v[176:179]\n"
TR "a[208:209],v1 offset:32768\n"
MF "v[176:179],a[220:223],a[180:183],v[176:179]\n"
TR "a[210:211],v1 offset:33280\n"
MF "v[184:187],a[220:223],a[184:187],v[184:187]\n"
"ds_read_b128 a[68:71],v2 offset:45056\n"
MF "v[184:187],a[216:219],a[144:147],v[184:187]\n"
"s_add_u32 s84,s14,20480\n"
TR "a[212:213],v1 offset:36864\n"
MF "v[192:195],a[216:219],a[148:151],v[192:195]\n"
"s_add_u32 m0,s63,22528\n"
TR "a[214:215],v1 offset:37376\n"
MF "v[192:195],a[220:223],a[188:191],v[192:195]\n"
LD "v4,s[24:27],s84 offen lds\n"
MF "v[200:203],a[220:223],a[192:195],v[200:203]\n"
"ds_read_b128 a[108:111],v3 offset:45056\n"
MF "v[200:203],a[216:219],a[152:155],v[200:203]\n"
MF "v[208:211],a[216:219],a[156:159],v[208:211]\n"
"s_add_u32 s84,s14,29696\n"
MF "v[208:211],a[220:223],a[196:199],v[208:211]\n"
"s_waitcnt lgkmcnt(7)\n"
LD "v5,s[24:27],s84 offen offset:1024 lds\n"
MF "v[212:215],a[204:207],a[196:199],v[212:215]\n"
TR "a[216:217],v1 offset:32776\n"
MF "v[212:215],a[200:203],a[156:159],v[212:215]\n"
TR "a[218:219],v1 offset:33288\n"
MF "v[204:207],a[200:203],a[152:155],v[204:207]\n"
"ds_read_b128 a[72:75],v2 offset:47104\n"
MF "v[204:207],a[204:207],a[192:195],v[204:207]\n"
TR "a[220:221],v1 offset:36872\n"
MF "v[196:199],a[204:207],a[188:191],v[196:199]\n"
TR "a[222:223],v1 offset:37384\n"
MF "v[196:199],a[200:203],a[148:151],v[196:199]\n"
MF "v[188:191],a[200:203],a[144:147],v[188:191]\n"
"v_add_u32 v0,0xffff4000,v0\n"
MF "v[188:191],a[204:207],a[184:187],v[188:191]\n"
"v_add_u32 v1,0xffff4000,v1\n"
MF "v[180:183],a[204:207],a[180:183],v[180:183]\n"
MF "v[180:183],a[200:203],a[140:143],v[180:183]\n"
"s_waitcnt vmcnt(25) lgkmcnt(0)\n"
"s_barrier\n"
MF "v[216:219],a[208:211],a[140:143],v[216:219]\n"
TR "a[200:201],v0\n"
MF "v[216:219],a[212:215],a[180:183],v[216:219]\n"
"s_add_u32 m0,s62,81920\n"
TR "a[202:203],v0 offset:512\n"
MF "v[224:227],a[212:215],a[184:187],v[224:227]\n"
"s_add_u32 s82,s66,2359552\n"
"ds_read_b128 a[112:115],v3 offset:47104\n"
MF "v[224:227],a[208:211],a[144:147],v[224:227]\n"
"s_sub_u32 s15,s82,7864320\n"
TR "a[204:205],v0 offset:4096\n"
MF "v[232:235],a[208:211],a[148:151],v[232:235]\n"
"s_min_u32 s82,s82,s15\n"
TR "a[206:207],v0 offset:4608\n"
MF "v[232:235],a[212:215],a[188:191],v[232:235]\n"
LD "v6,s[28:31],s82 offen lds\n"
MF "v[240:243],a[212:215],a[192:195],v[240:243]\n"
"ds_read_b128 a[76:79],v2 offset:49152\n"
MF "v[240:243],a[208:211],a[152:155],v[240:243]\n"
"ds_read_b128 a[116:119],v3 offset:49152\n"
MF "v[248:251],a[208:211],a[156:159],v[248:251]\n"
LD "v11,s[28:31],s82 offen offset:1024 lds\n"
MF "v[248:251],a[212:215],a[196:199],v[248:251]\n"
MF "v[252:255],a[220:223],a[196:199],v[252:255]\n"
TR "a[208:209],v0 offset:8\n"
MF "v[252:255],a[216:219],a[156:159],v[252:255]\n"
TR "a[210:211],v0 offset:520\n"
MF "v[244:247],a[216:219],a[152:155],v[244:247]\n"
MF "v[244:247],a[220:223],a[192:195],v[244:247]\n"
TR "a[212:213],v0 offset:4104\n"
MF "v[236:239],a[220:223],a[188:191],v[236:239]\n"
TR "a[214:215],v0 offset:4616\n"
MF "v[236:239],a[216:219],a[148:151],v[236:239]\n"
LD "v12,s[28:31],s82 offen offset:2048 lds\n"
MF "v[228:231],a[216:219],a[144:147],v[228:231]\n"
MF "v[228:231],a[220:223],a[184:187],v[228:231]\n"
MF "v[220:223],a[220:223],a[180:183],v[220:223]\n"
MF "v[220:223],a[216:219],a[140:143],v[220:223]\n"
"s_waitcnt lgkmcnt(4)\n"
MF "v[16:19],a[200:203],a[60:63],v[16:19]\n"
TR "a[216:217],v1\n"
MF "v[16:19],a[204:207],a[100:103],v[16:19]\n"
TR "a[218:219],v1 offset:512\n"
MF "v[24:27],a[204:207],a[104:107],v[24:27]\n"
LD "v13,s[28:31],s82 offen offset:3072 lds\n"
MF "v[24:27],a[200:203],a[64:67],v[24:27]\n"
TR "a[220:221],v1 offset:4096\n"
MF "v[32:35],a[200:203],a[68:71],v[32:35]\n"
TR "a[222:223],v1 offset:4608\n"
MF "v[32:35],a[204:207],a[108:111],v[32:35]\n"
MF "v[40:43],a[204:207],a[112:115],v[40:43]\n"
MF "v[40:43],a[200:203],a[72:75],v[40:43]\n"
"s_add_u32 s84,s14,40960\n"
MF "v[48:51],a[200:203],a[76:79],v[48:51]\n"
"s_add_u32 m0,s63,24576\n"
MF "v[48:51],a[204:207],a[116:119],v[48:51]\n"
"s_waitcnt lgkmcnt(4)\n"
LD "v4,s[24:27],s84 offen lds\n"
MF "v[52:55],a[212:215],a[116:119],v[52:55]\n"
TR "a[200:201],v1 offset:8\n"
MF "v[52:55],a[208:211],a[76:79],v[52:55]\n"
TR "a[202:203],v1 offset:520\n"
MF "v[44:47],a[208:211],a[72:75],v[44:47]\n"
MF "v[44:47],a[212:215],a[112:115],v[44:47]\n"
TR "a[204:205],v1 offset:4104\n"
MF "v[36:39],a[212:215],a[108:111],v[36:39]\n"
TR "a[206:207],v1 offset:4616\n"
MF "v[36:39],a[208:211],a[68:71],v[36:39]\n"
MF "v[28:31],a[208:211],a[64:67],v[28:31]\n"
MF "v[28:31],a[212:215],a[104:107],v[28:31]\n"
MF "v[20:23],a[212:215],a[100:103],v[20:23]\n"
MF "v[20:23],a[208:211],a[60:63],v[20:23]\n"
"s_waitcnt vmcnt(24) lgkmcnt(0)\n"
"s_barrier\n"
MF "v[56:59],a[216:219],a[60:63],v[56:59]\n"
"s_add_u32 m0,s62,0\n"
"s_add_u32 s82,s66,3145728\n"
TR "a[208:209],v0 offset:16384\n"
MF "v[56:59],a[220:223],a[100:103],v[56:59]\n"
"s_sub_u32 s15,s82,7864320\n"
"s_min_u32 s82,s82,s15\n"
TR "a[210:211],v0 offset:16896\n"
MF "v[64:67],a[220:223],a[104:107],v[64:67]\n"
LD "v6,s[28:31],s82 offen lds\n"
MF "v[64:67],a[216:219],a[64:67],v[64:67]\n"
TR "a[212:213],v0 offset:20480\n"
MF "v[72:75],a[216:219],a[68:71],v[72:75]\n"
TR "a[214:215],v0 offset:20992\n"
MF "v[72:75],a[220:223],a[108:111],v[72:75]\n"
MF "v[80:83],a[220:223],a[112:115],v[80:83]\n"
LD "v11,s[28:31],s82 offen offset:1024 lds\n"
MF "v[80:83],a[216:219],a[72:75],v[80:83]\n"
MF "v[88:91],a[216:219],a[76:79],v[88:91]\n"
MF "v[88:91],a[220:223],a[116:119],v[88:91]\n"
MF "v[92:95],a[204:207],a[116:119],v[92:95]\n"
TR "a[216:217],v0 offset:16392\n"
MF "v[92:95],a[200:203],a[76:79],v[92:95]\n"
TR "a[218:219],v0 offset:16904\n"
MF "v[84:87],a[200:203],a[72:75],v[84:87]\n"
LD "v12,s[28:31],s82 offen offset:2048 lds\n"
MF "v[84:87],a[204:207],a[112:115],v[84:87]\n"
TR "a[220:221],v0 offset:20488\n"
MF "v[76:79],a[204:207],a[108:111],v[76:79]\n"
TR "a[222:223],v0 offset:21000\n"
MF "v[76:79],a[200:203],a[68:71],v[76:79]\n"
MF "v[68:71],a[200:203],a[64:67],v[68:71]\n"
MF "v[68:71],a[204:207],a[104:107],v[68:71]\n"
MF "v[60:63],a[204:207],a[100:103],v[60:63]\n"
LD "v13,s[28:31],s82 offen offset:3072 lds\n"
MF "v[60:63],a[200:203],a[60:63],v[60:63]\n"
"s_waitcnt lgkmcnt(4)\n"
MF "v[96:99],a[208:211],a[60:63],v[96:99]\n"
TR "a[200:201],v1 offset:16384\n"
MF "v[96:99],a[212:215],a[100:103],v[96:99]\n"
TR "a[202:203],v1 offset:16896\n"
MF "v[104:107],a[212:215],a[104:107],v[104:107]\n"
"s_add_u32 s14,s81,640\n"
"s_sub_u32 s15,s14,1280\n"
MF "v[104:107],a[208:211],a[64:67],v[104:107]\n"
"s_min_u32 s14,s14,s15\n"
"s_add_u32 s14,s14,s68\n"
TR "a[204:205],v1 offset:20480\n"
MF "v[112:115],a[208:211],a[68:71],v[112:115]\n"
"s_add_u32 s84,s14,0\n"
"s_add_u32 m0,s63,40960\n"
TR "a[206:207],v1 offset:20992\n"
MF "v[112:115],a[212:215],a[108:111],v[112:115]\n"
LD "v4,s[24:27],s84 offen lds\n"
MF "v[120:123],a[212:215],a[112:115],v[120:123]\n"
MF "v[120:123],a[208:211],a[72:75],v[120:123]\n"
MF "v[128:131],a[208:211],a[76:79],v[128:131]\n"
"s_add_u32 s84,s14,9216\n"
MF "v[128:131],a[212:215],a[116:119],v[128:131]\n"
"s_waitcnt lgkmcnt(4)\n"
LD "v5,s[24:27],s84 offen offset:1024 lds\n"
MF "v[132:135],a[220:223],a[116:119],v[132:135]\n"
TR "a[208:209],v1 offset:16392\n"
MF "v[132:135],a[216:219],a[76:79],v[132:135]\n"
TR "a[210:211],v1 offset:16904\n"
MF "v[124:127],a[216:219],a[72:75],v[124:127]\n"
MF "v[124:127],a[220:223],a[112:115],v[124:127]\n"
TR "a[212:213],v1 offset:20488\n"
MF "v[116:119],a[220:223],a[108:111],v[116:119]\n"
TR "a[214:215],v1 offset:21000\n"
MF "v[116:119],a[216:219],a[68:71],v[116:119]\n"
MF "v[108:111],a[216:219],a[64:67],v[108:111]\n"
MF "v[108:111],a[220:223],a[104:107],v[108:111]\n"
MF "v[100:103],a[220:223],a[100:103],v[100:103]\n"
MF "v[100:103],a[216:219],a[60:63],v[100:103]\n"
"s_waitcnt vmcnt(23) lgkmcnt(0)\n"
"s_barrier\n"
MF "v[136:139],a[200:203],a[60:63],v[136:139]\n"
TR "a[216:217],v0 offset:32768\n"
MF "v[136:139],a[204:207],a[100:103],v[136:139]\n"
"s_add_u32 m0,s62,16384\n"
TR "a[218:219],v0 offset:33280\n"
MF "v[144:147],a[204:207],a[104:107],v[144:147]\n"
"s_add_u32 s82,s66,3145856\n"
"ds_read_b128 a[140:143],v2\n"
MF "v[144:147],a[200:203],a[64:67],v[144:147]\n"
"s_sub_u32 s15,s82,7864320\n"
TR "a[220:221],v0 offset:36864\n"
MF "v[152:155],a[200:203],a[68:71],v[152:155]\n"
"s_min_u32 s82,s82,s15\n"
TR "a[222:223],v0 offset:37376\n"
MF "v[152:155],a[204:207],a[108:111],v[152:155]\n"
LD "v6,s[28:31],s82 offen lds\n"
MF "v[160:163],a[204:207],a[112:115],v[160:163]\n"
"ds_read_b128 a[180:183],v3\n"
MF "v[160:163],a[200:203],a[72:75],v[160:163]\n"
LD "v11,s[28:31],s82 offen offset:1024 lds\n"
MF "v[168:171],a[200:203],a[76:79],v[168:171]\n"
MF "v[168:171],a[204:207],a[116:119],v[168:171]\n"
LD "v12,s[28:31],s82 offen offset:2048 lds\n"
MF "v[172:175],a[212:215],a[116:119],v[172:175]\n"
TR "a[200:201],v0 offset:32776\n"
MF "v[172:175],a[208:211],a[76:79],v[172:175]\n"
TR "a[202:203],v0 offset:33288\n"
MF "v[164:167],a[208:211],a[72:75],v[164:167]\n"
"ds_read_b128 a[144:147],v2 offset:2048\n"
MF "v[164:167],a[212:215],a[112:115],v[164:167]\n"
TR "a[204:205],v0 offset:36872\n"
MF "v[156:159],a[212:215],a[108:111],v[156:159]\n"
TR "a[206:207],v0 offset:37384\n"
MF "v[156:159],a[208:211],a[68:71],v[156:159]\n"
MF "v[148:151],a[208:211],a[64:67],v[148:151]\n"
"ds_read_b128 a[184:187],v3 offset:2048\n"
MF "v[148:151],a[212:215],a[104:107],v[148:151]\n"
MF "v[140:143],a[212:215],a[100:103],v[140:143]\n"
LD "v13,s[28:31],s82 offen offset:3072 lds\n"
MF "v[140:143],a[208:211],a[60:63],v[140:143]\n"
"s_waitcnt lgkmcnt(7)\n"
MF "v[176:179],a[216:219],a[60:63],v[176:179]\n"
TR "a[208:209],v1 offset:32768\n"
MF "v[176:179],a[220:223],a[100:103],v[176:179]\n"
TR "a[210:211],v1 offset:33280\n"
MF "v[184:187],a[220:223],a[104:107],v[184:187]\n"
"ds_read_b128 a[148:151],v2 offset:4096\n"
MF "v[184:187],a[216:219],a[64:67],v[184:187]\n"
"s_add_u32 s84,s14,20480\n"
TR "a[212:213],v1 offset:36864\n"
MF "v[192:195],a[216:219],a[68:71],v[192:195]\n"
"s_add_u32 m0,s63,43008\n"
TR "a[214:215],v1 offset:37376\n"
MF "v[192:195],a[220:223],a[108:111],v[192:195]\n"
LD "v4,s[24:27],s84 offen lds\n"
MF "v[200:203],a[220:223],a[112:115],v[200:203]\n"
"ds_read_b128 a[188:191],v3 offset:4096\n"
MF "v[200:203],a[216:219],a[72:75],v[200:203]\n"
MF "v[208:211],a[216:219],a[76:79],v[208:211]\n"
"s_add_u32 s84,s14,29696\n"
MF "v[208:211],a[220:223],a[116:119],v[208:211]\n"
"s_waitcnt lgkmcnt(7)\n"
LD "v5,s[24:27],s84 offen offset:1024 lds\n"
MF "v[212:215],a[204:207],a[116:119],v[212:215]\n"
TR "a[216:217],v1 offset:32776\n"
MF "v[212:215],a[200:203],a[76:79],v[212:215]\n"
TR "a[218:219],v1 offset:33288\n"
MF "v[204:207],a[200:203],a[72:75],v[204:207]\n"
"ds_read_b128 a[152:155],v2 offset:6144\n"
MF "v[204:207],a[204:207],a[112:115],v[204:207]\n"
TR "a[220:221],v1 offset:36872\n"
MF "v[196:199],a[204:207],a[108:111],v[196:199]\n"
TR "a[222:223],v1 offset:37384\n"
MF "v[196:199],a[200:203],a[68:71],v[196:199]\n"
MF "v[188:191],a[200:203],a[64:67],v[188:191]\n"
"v_add_u32 v0,0xc000,v0\n"
MF "v[188:191],a[204:207],a[104:107],v[188:191]\n"
"v_add_u32 v1,0xc000,v1\n"
MF "v[180:183],a[204:207],a[100:103],v[180:183]\n"
MF "v[180:183],a[200:203],a[60:63],v[180:183]\n"
"s_waitcnt vmcnt(25) lgkmcnt(0)\n"
"s_barrier\n"
MF "v[216:219],a[208:211],a[60:63],v[216:219]\n"
TR "a[200:201],v0\n"
MF "v[216:219],a[212:215],a[100:103],v[216:219]\n"
"s_add_u32 m0,s62,32768\n"
TR "a[202:203],v0 offset:512\n"
MF "v[224:227],a[212:215],a[104:107],v[224:227]\n"
".Lpg5c:\n"
"s_cbranch_execz .Lpg31c\n"
".Lpg5f:\n"
"s_cbranch_execz .Lpgt0k_f\n"
"s_add_u32 s82,s66,3145984\n"
"ds_read_b128 a[192:195],v3 offset:6144\n"
MF "v[224:227],a[208:211],a[64:67],v[224:227]\n"
"s_sub_u32 s15,s82,7864320\n"
TR "a[204:205],v0 offset:4096\n"
MF "v[232:235],a[208:211],a[68:71],v[232:235]\n"
"s_min_u32 s82,s82,s15\n"
TR "a[206:207],v0 offset:4608\n"
MF "v[232:235],a[212:215],a[108:111],v[232:235]\n"
LD "v6,s[28:31],s82 offen lds\n"
MF "v[240:243],a[212:215],a[112:115],v[240:243]\n"
"ds_read_b128 a[156:159],v2 offset:8192\n"
MF "v[240:243],a[208:211],a[72:75],v[240:243]\n"
"ds_read_b128 a[196:199],v3 offset:8192\n"
MF "v[248:251],a[208:211],a[76:79],v[248:251]\n"
LD "v11,s[28:31],s82 offen offset:1024 lds\n"
MF "v[248:251],a[212:215],a[116:119],v[248:251]\n"
MF "v[252:255],a[220:223],a[116:119],v[252:255]\n"
TR "a[208:209],v0 offset:8\n"
MF "v[252:255],a[216:219],a[76:79],v[252:255]\n"
TR "a[210:211],v0 offset:520\n"
MF "v[244:247],a[216:219],a[72:75],v[244:247]\n"
MF "v[244:247],a[220:223],a[112:115],v[244:247]\n"
TR "a[212:213],v0 offset:4104\n"
MF "v[236:239],a[220:223],a[108:111],v[236:239]\n"
TR "a[214:215],v0 offset:4616\n"
MF "v[236:239],a[216:219],a[68:71],v[236:239]\n"
LD "v12,s[28:31],s82 offen offset:2048 lds\n"
MF "v[228:231],a[216:219],a[64:67],v[228:231]\n"
MF "v[228:231],a[220:223],a[104:107],v[228:231]\n"
MF "v[220:223],a[220:223],a[100:103],v[220:223]\n"
MF "v[220:223],a[216:219],a[60:63],v[220:223]\n"
"s_waitcnt lgkmcnt(4)\n"
MF "v[16:19],a[200:203],a[140:143],v[16:19]\n"
TR "a[216:217],v1\n"
MF "v[16:19],a[204:207],a[180:183],v[16:19]\n"
TR "a[218:219],v1 offset:512\n"
MF "v[24:27],a[204:207],a[184:187],v[24:27]\n"
LD "v13,s[28:31],s82 offen offset:3072 lds\n"
MF "v[24:27],a[200:203],a[144:147],v[24:27]\n"
TR "a[220:221],v1 offset:4096\n"
MF "v[32:35],a[200:203],a[148:151],v[32:35]\n"
TR "a[222:223],v1 offset:4608\n"
MF "v[32:35],a[204:207],a[188:191],v[32:35]\n"
MF "v[40:43],a[204:207],a[192:195],v[40:43]\n"
MF "v[40:43],a[200:203],a[152:155],v[40:43]\n"
"s_add_u32 s84,s14,40960\n"
MF "v[48:51],a[200:203],a[156:159],v[48:51]\n"
"s_add_u32 m0,s63,45056\n"
MF "v[48:51],a[204:207],a[196:199],v[48:51]\n"
"s_waitcnt lgkmcnt(4)\n"
LD "v4,s[24:27],s84 offen lds\n"
MF "v[52:55],a[212:215],a[196:199],v[52:55]\n"
TR "a[200:201],v1 offset:8\n"
MF "v[52:55],a[208:211],a[156:159],v[52:55]\n"
TR "a[202:203],v1 offset:520\n"
MF "v[44:47],a[208:211],a[152:155],v[44:47]\n"
MF "v[44:47],a[212:215],a[192:195],v[44:47]\n"
TR "a[204:205],v1 offset:4104\n"
MF "v[36:39],a[212:215],a[188:191],v[36:39]\n"
TR "a[206:207],v1 offset:4616\n"
MF "v[36:39],a[208:211],a[148:151],v[36:39]\n"
MF "v[28:31],a[208:211],a[144:147],v[28:31]\n"
MF "v[28:31],a[212:215],a[184:187],v[28:31]\n"
MF "v[20:23],a[212:215],a[180:183],v[20:23]\n"
MF "v[20:23],a[208:211],a[140:143],v[20:23]\n"
"s_waitcnt vmcnt(24) lgkmcnt(0)\n"
"s_barrier\n"
MF "v[56:59],a[216:219],a[140:143],v[56:59]\n"
"s_add_u32 m0,s62,49152\n"
"s_add_u32 s82,s66,3932160\n"
TR "a[208:209],v0 offset:16384\n"
MF "v[56:59],a[220:223],a[180:183],v[56:59]\n"
"s_sub_u32 s15,s82,7864320\n"
"s_min_u32 s82,s82,s15\n"
TR "a[210:211],v0 offset:16896\n"
MF "v[64:67],a[220:223],a[184:187],v[64:67]\n"
LD "v6,s[28:31],s82 offen lds\n"
MF "v[64:67],a[216:219],a[144:147],v[64:67]\n"
TR "a[212:213],v0 offset:20480\n"
MF "v[72:75],a[216:219],a[148:151],v[72:75]\n"
TR "a[214:215],v0 offset:20992\n"
MF "v[72:75],a[220:223],a[188:191],v[72:75]\n"
MF "v[80:83],a[220:223],a[192:195],v[80:83]\n"
LD "v11,s[28:31],s82 offen offset:1024 lds\n"
MF "v[80:83],a[216:219],a[152:155],v[80:83]\n"
MF "v[88:91],a[216:219],a[156:159],v[88:91]\n"
MF "v[88:91],a[220:223],a[196:199],v[88:91]\n"
MF "v[92:95],a[204:207],a[196:199],v[92:95]\n"
TR "a[216:217],v0 offset:16392\n"
MF "v[92:95],a[200:203],a[156:159],v[92:95]\n"
TR "a[218:219],v0 offset:16904\n"
MF "v[84:87],a[200:203],a[152:155],v[84:87]\n"
LD "v12,s[28:31],s82 offen offset:2048 lds\n"
MF "v[84:87],a[204:207],a[192:195],v[84:87]\n"
TR "a[220:221],v0 offset:20488\n"
MF "v[76:79],a[204:207],a[188:191],v[76:79]\n"
TR "a[222:223],v0 offset:21000\n"
MF "v[76:79],a[200:203],a[148:151],v[76:79]\n"
MF "v[68:71],a[200:203],a[144:147],v[68:71]\n"
MF "v[68:71],a[204:207],a[184:187],v[68:71]\n"
MF "v[60:63],a[204:207],a[180:183],v[60:63]\n"
LD "v13,s[28:31],s82 offen offset:3072 lds\n"
MF "v[60:63],a[200:203],a[140:143],v[60:63]\n"
"s_waitcnt lgkmcnt(4)\n"
MF "v[96:99],a[208:211],a[140:143],v[96:99]\n"
TR "a[200:201],v1 offset:16384\n"
MF "v[96:99],a[212:215],a[180:183],v[96:99]\n"
TR "a[202:203],v1 offset:16896\n"
MF "v[104:107],a[212:215],a[184:187],v[104:107]\n"
"s_add_u32 s14,s81,768\n"
"s_sub_u32 s15,s14,1280\n"
MF "v[104:107],a[208:211],a[144:147],v[104:107]\n"
"s_min_u32 s14,s14,s15\n"
"s_add_u32 s14,s14,s68\n"
TR "a[204:205],v1 offset:20480\n"
MF "v[112:115],a[208:211],a[148:151],v[112:115]\n"
"s_add_u32 s84,s14,0\n"
"s_add_u32 m0,s63,0\n"
TR "a[206:207],v1 offset:20992\n"
MF "v[112:115],a[212:215],a[188:191],v[112:115]\n"
LD "v4,s[24:27],s84 offen lds\n"
MF "v[120:123],a[212:215],a[192:195],v[120:123]\n"
MF "v[120:123],a[208:211],a[152:155],v[120:123]\n"
MF "v[128:131],a[208:211],a[156:159],v[128:131]\n"
"s_add_u32 s84,s14,9216\n"
MF "v[128:131],a[212:215],a[196:199],v[128:131]\n"
"s_waitcnt lgkmcnt(4)\n"
LD "v5,s[24:27],s84 offen offset:1024 lds\n"
MF "v[132:135],a[220:223],a[196:199],v[132:135]\n"
TR "a[208:209],v1 offset:16392\n"
MF "v[132:135],a[216:219],a[156:159],v[132:135]\n"
TR "a[210:211],v1 offset:16904\n"
MF "v[124:127],a[216:219],a[152:155],v[124:127]\n"
MF "v[124:127],a[220:223],a[192:195],v[124:127]\n"
TR "a[212:213],v1 offset:20488\n"
MF "v[116:119],a[220:223],a[188:191],v[116:119]\n"
TR "a[214:215],v1 offset:21000\n"
MF "v[116:119],a[216:219],a[148:151],v[116:119]\n"
MF "v[108:111],a[216:219],a[144:147],v[108:111]\n"
MF "v[108:111],a[220:223],a[184:187],v[108:111]\n"
MF "v[100:103],a[220:223],a[180:183],v[100:103]\n"
MF "v[100:103],a[216:219],a[140:143],v[100:103]\n"
"s_waitcnt vmcnt(23) lgkmcnt(0)\n"
"s_barrier\n"
MF "v[136:139],a[200:203],a[140:143],v[136:139]\n"
TR "a[216:217],v0 offset:32768\n"
MF "v[136:139],a[204:207],a[180:183],v[136:139]\n"
"s_add_u32 m0,s62,65536\n"
TR "a[218:219],v0 offset:33280\n"
MF "v[144:147],a[204:207],a[184:187],v[144:147]\n"
"s_add_u32 s82,s66,3932288\n"
"ds_read_b128 a[60:63],v2 offset:20480\n"
MF "v[144:147],a[200:203],a[144:147],v[144:147]\n"
"s_sub_u32 s15,s82,7864320\n"
TR "a[220:221],v0 offset:36864\n"
MF "v[152:155],a[200:203],a[148:151],v[152:155]\n"
"s_min_u32 s82,s82,s15\n"
TR "a[222:223],v0 offset:37376\n"
MF "v[152:155],a[204:207],a[188:191],v[152:155]\n"
LD "v6,s[28:31],s82 offen lds\n"
MF "v[160:163],a[204:207],a[192:195],v[160:163]\n"
"ds_read_b128 a[100:103],v3 offset:20480\n"
MF "v[160:163],a[200:203],a[152:155],v[160:163]\n"
LD "v11,s[28:31],s82 offen offset:1024 lds\n"
MF "v[168:171],a[200:203],a[156:159],v[168:171]\n"
MF "v[168:171],a[204:207],a[196:199],v[168:171]\n"
LD "v12,s[28:31],s82 offen offset:2048 lds\n"
MF "v[172:175],a[212:215],a[196:199],v[172:175]\n"
TR "a[200:201],v0 offset:32776\n"
MF "v[172:175],a[208:211],a[156:159],v[172:175]\n"
TR "a[202:203],v0 offset:33288\n"
MF "v[164:167],a[208:211],a[152:155],v[164:167]\n"
"ds_read_b128 a[64:67],v2 offset:22528\n"
MF "v[164:167],a[212:215],a[192:195],v[164:167]\n"
TR "a[204:205],v0 offset:36872\n"
MF "v[156:159],a[212:215],a[188:191],v[156:159]\n"
TR "a[206:207],v0 offset:37384\n"
MF "v[156:159],a[208:211],a[148:151],v[156:159]\n"
MF "v[148:151],a[208:211],a[144:147],v[148:151]\n"
"ds_read_b128 a[104:107],v3 offset:22528\n"
MF "v[148:151],a[212:215],a[184:187],v[148:151]\n"
MF "v[140:143],a[212:215],a[180:183],v[140:143]\n"
LD "v13,s[28:31],s82 offen offset:3072 lds\n"
MF "v[140:143],a[208:211],a[140:143],v[140:143]\n"
"s_waitcnt lgkmcnt(7)\n"
MF "v[176:179],a[216:219],a[140:143],v[176:179]\n"
TR "a[208:209],v1 offset:32768\n"
MF "v[176:179],a[220:223],a[180:183],v[176:179]\n"
TR "a[210:211],v1 offset:33280\n"
MF "v[184:187],a[220:223],a[184:187],v[184:187]\n"
"ds_read_b128 a[68:71],v2 offset:24576\n"
MF "v[184:187],a[216:219],a[144:147],v[184:187]\n"
"s_add_u32 s84,s14,20480\n"
TR "a[212:213],v1 offset:36864\n"
MF "v[192:195],a[216:219],a[148:151],v[192:195]\n"
"s_add_u32 m0,s63,2048\n"
TR "a[214:215],v1 offset:37376\n"
MF "v[192:195],a[220:223],a[188:191],v[192:195]\n"
LD "v4,s[24:27],s84 offen lds\n"
MF "v[200:203],a[220:223],a[192:195],v[200:203]\n"
"ds_read_b128 a[108:111],v3 offset:24576\n"
MF "v[200:203],a[216:219],a[152:155],v[200:203]\n"
MF "v[208:211],a[216:219],a[156:159],v[208:211]\n"
"s_add_u32 s84,s14,29696\n"
MF "v[208:211],a[220:223],a[196:199],v[208:211]\n"
"s_waitcnt lgkmcnt(7)\n"
LD "v5,s[24:27],s84 offen offset:1024 lds\n"
MF "v[212:215],a[204:207],a[196:199],v[212:215]\n"
TR "a[216:217],v1 offset:32776\n"
MF "v[212:215],a[200:203],a[156:159],v[212:215]\n"
TR "a[218:219],v1 offset:33288\n"
MF "v[204:207],a[200:203],a[152:155],v[204:207]\n"
"ds_read_b128 a[72:75],v2 offset:26624\n"
MF "v[204:207],a[204:207],a[192:195],v[204:207]\n"
TR "a[220:221],v1 offset:36872\n"
MF "v[196:199],a[204:207],a[188:191],v[196:199]\n"
TR "a[222:223],v1 offset:37384\n"
MF "v[196:199],a[200:203],a[148:151],v[196:199]\n"
MF "v[188:191],a[200:203],a[144:147],v[188:191]\n"
"v_add_u32 v0,0xffff4000,v0\n"
MF "v[188:191],a[204:207],a[184:187],v[188:191]\n"
"v_add_u32 v1,0xffff4000,v1\n"
MF "v[180:183],a[204:207],a[180:183],v[180:183]\n"
MF "v[180:183],a[200:203],a[140:143],v[180:183]\n"
"s_waitcnt vmcnt(25) lgkmcnt(0)\n"
"s_barrier\n"
MF "v[216:219],a[208:211],a[140:143],v[216:219]\n"
TR "a[200:201],v0\n"
MF "v[216:219],a[212:215],a[180:183],v[216:219]\n"
"s_add_u32 m0,s62,81920\n"
TR "a[202:203],v0 offset:512\n"
MF "v[224:227],a[212:215],a[184:187],v[224:227]\n"
"s_add_u32 s82,s66,3932416\n"
"ds_read_b128 a[112:115],v3 offset:26624\n"
MF "v[224:227],a[208:211],a[144:147],v[224:227]\n"
"s_sub_u32 s15,s82,7864320\n"
TR "a[204:205],v0 offset:4096\n"
MF "v[232:235],a[208:211],a[148:151],v[232:235]\n"
"s_min_u32 s82,s82,s15\n"
TR "a[206:207],v0 offset:4608\n"
MF "v[232:235],a[212:215],a[188:191],v[232:235]\n"
LD "v6,s[28:31],s82 offen lds\n"
MF "v[240:243],a[212:215],a[192:195],v[240:243]\n"
"ds_read_b128 a[76:79],v2 offset:28672\n"
MF "v[240:243],a[208:211],a[152:155],v[240:243]\n"
"ds_read_b128 a[116:119],v3 offset:28672\n"
MF "v[248:251],a[208:211],a[156:159],v[248:251]\n"
LD "v11,s[28:31],s82 offen offset:1024 lds\n"
MF "v[248:251],a[212:215],a[196:199],v[248:251]\n"
MF "v[252:255],a[220:223],a[196:199],v[252:255]\n"
TR "a[208:209],v0 offset:8\n"
MF "v[252:255],a[216:219],a[156:159],v[252:255]\n"
TR "a[210:211],v0 offset:520\n"
MF "v[244:247],a[216:219],a[152:155],v[244:247]\n"
MF "v[244:247],a[220:223],a[192:195],v[244:247]\n"
TR "a[212:213],v0 offset:4104\n"
MF "v[236:239],a[220:223],a[188:191],v[236:239]\n"
TR "a[214:215],v0 offset:4616\n"
MF "v[236:239],a[216:219],a[148:151],v[236:239]\n"
LD "v12,s[28:31],s82 offen offset:2048 lds\n"
MF "v[228:231],a[216:219],a[144:147],v[228:231]\n"
MF "v[228:231],a[220:223],a[184:187],v[228:231]\n"
MF "v[220:223],a[220:223],a[180:183],v[220:223]\n"
MF "v[220:223],a[216:219],a[140:143],v[220:223]\n"
"s_waitcnt lgkmcnt(4)\n"
MF "v[16:19],a[200:203],a[60:63],v[16:19]\n"
TR "a[216:217],v1\n"
MF "v[16:19],a[204:207],a[100:103],v[16:19]\n"
TR "a[218:219],v1 offset:512\n"
MF "v[24:27],a[204:207],a[104:107],v[24:27]\n"
LD "v13,s[28:31],s82 offen offset:3072 lds\n"
MF "v[24:27],a[200:203],a[64:67],v[24:27]\n"
TR "a[220:221],v1 offset:4096\n"
MF "v[32:35],a[200:203],a[68:71],v[32:35]\n"
TR "a[222:223],v1 offset:4608\n"
MF "v[32:35],a[204:207],a[108:111],v[32:35]\n"
MF "v[40:43],a[204:207],a[112:115],v[40:43]\n"
MF "v[40:43],a[200:203],a[72:75],v[40:43]\n"
"s_add_u32 s84,s14,40960\n"
MF "v[48:51],a[200:203],a[76:79],v[48:51]\n"
"s_add_u32 m0,s63,4096\n"
MF "v[48:51],a[204:207],a[116:119],v[48:51]\n"
"s_waitcnt lgkmcnt(4)\n"
LD "v4,s[24:27],s84 offen lds\n"
MF "v[52:55],a[212:215],a[116:119],v[52:55]\n"
TR "a[200:201],v1 offset:8\n"
MF "v[52:55],a[208:211],a[76:79],v[52:55]\n"
TR "a[202:203],v1 offset:520\n"
MF "v[44:47],a[208:211],a[72:75],v[44:47]\n"
MF "v[44:47],a[212:215],a[112:115],v[44:47]\n"
TR "a[204:205],v1 offset:4104\n"
MF "v[36:39],a[212:215],a[108:111],v[36:39]\n"
TR "a[206:207],v1 offset:4616\n"
MF "v[36:39],a[208:211],a[68:71],v[36:39]\n"
MF "v[28:31],a[208:211],a[64:67],v[28:31]\n"
MF "v[28:31],a[212:215],a[104:107],v[28:31]\n"
MF "v[20:23],a[212:215],a[100:103],v[20:23]\n"
MF "v[20:23],a[208:211],a[60:63],v[20:23]\n"
"s_waitcnt vmcnt(24) lgkmcnt(0)\n"
"s_barrier\n"
MF "v[56:59],a[216:219],a[60:63],v[56:59]\n"
"s_add_u32 m0,s62,0\n"
"s_add_u32 s82,s66,4718592\n"
TR "a[208:209],v0 offset:16384\n"
MF "v[56:59],a[220:223],a[100:103],v[56:59]\n"
"s_sub_u32 s15,s82,7864320\n"
"s_min_u32 s82,s82,s15\n"
TR "a[210:211],v0 offset:16896\n"
MF "v[64:67],a[220:223],a[104:107],v[64:67]\n"
LD "v6,s[28:31],s82 offen lds\n"
MF "v[64:67],a[216:219],a[64:67],v[64:67]\n"
TR "a[212:213],v0 offset:20480\n"
MF "v[72:75],a[216:219],a[68:71],v[72:75]\n"
TR "a[214:215],v0 offset:20992\n"
MF "v[72:75],a[220:223],a[108:111],v[72:75]\n"
MF "v[80:83],a[220:223],a[112:115],v[80:83]\n"
LD "v11,s[28:31],s82 offen offset:1024 lds\n"
MF "v[80:83],a[216:219],a[72:75],v[80:83]\n"
MF "v[88:91],a[216:219],a[76:79],v[88:91]\n"
MF "v[88:91],a[220:223],a[116:119],v[88:91]\n"
MF "v[92:95],a[204:207],a[116:119],v[92:95]\n"
TR "a[216:217],v0 offset:16392\n"
MF "v[92:95],a[200:203],a[76:79],v[92:95]\n"
TR "a[218:219],v0 offset:16904\n"
MF "v[84:87],a[200:203],a[72:75],v[84:87]\n"
LD "v12,s[28:31],s82 offen offset:2048 lds\n"
MF "v[84:87],a[204:207],a[112:115],v[84:87]\n"
TR "a[220:221],v0 offset:20488\n"
MF "v[76:79],a[204:207],a[108:111],v[76:79]\n"
TR "a[222:223],v0 offset:21000\n"
MF "v[76:79],a[200:203],a[68:71],v[76:79]\n"
MF "v[68:71],a[200:203],a[64:67],v[68:71]\n"
MF "v[68:71],a[204:207],a[104:107],v[68:71]\n"
MF "v[60:63],a[204:207],a[100:103],v[60:63]\n"
LD "v13,s[28:31],s82 offen offset:3072 lds\n"
MF "v[60:63],a[200:203],a[60:63],v[60:63]\n"
"s_waitcnt lgkmcnt(4)\n"
MF "v[96:99],a[208:211],a[60:63],v[96:99]\n"
TR "a[200:201],v1 offset:16384\n"
MF "v[96:99],a[212:215],a[100:103],v[96:99]\n"
TR "a[202:203],v1 offset:16896\n"
MF "v[104:107],a[212:215],a[104:107],v[104:107]\n"
"s_add_u32 s14,s81,896\n"
"s_sub_u32 s15,s14,1280\n"
MF "v[104:107],a[208:211],a[64:67],v[104:107]\n"
"s_min_u32 s14,s14,s15\n"
"s_add_u32 s14,s14,s68\n"
TR "a[204:205],v1 offset:20480\n"
MF "v[112:115],a[208:211],a[68:71],v[112:115]\n"
"s_add_u32 s84,s14,0\n"
"s_add_u32 m0,s63,20480\n"
TR "a[206:207],v1 offset:20992\n"
MF "v[112:115],a[212:215],a[108:111],v[112:115]\n"
LD "v4,s[24:27],s84 offen lds\n"
MF "v[120:123],a[212:215],a[112:115],v[120:123]\n"
MF "v[120:123],a[208:211],a[72:75],v[120:123]\n"
MF "v[128:131],a[208:211],a[76:79],v[128:131]\n"
"s_add_u32 s84,s14,9216\n"
MF "v[128:131],a[212:215],a[116:119],v[128:131]\n"
"s_waitcnt lgkmcnt(4)\n"
LD "v5,s[24:27],s84 offen offset:1024 lds\n"
MF "v[132:135],a[220:223],a[116:119],v[132:135]\n"
TR "a[208:209],v1 offset:16392\n"
MF "v[132:135],a[216:219],a[76:79],v[132:135]\n"
TR "a[210:211],v1 offset:16904\n"
MF "v[124:127],a[216:219],a[72:75],v[124:127]\n"
MF "v[124:127],a[220:223],a[112:115],v[124:127]\n"
TR "a[212:213],v1 offset:20488\n"
MF "v[116:119],a[220:223],a[108:111],v[116:119]\n"
TR "a[214:215],v1 offset:21000\n"
MF "v[116:119],a[216:219],a[68:71],v[116:119]\n"
MF "v[108:111],a[216:219],a[64:67],v[108:111]\n"
MF "v[108:111],a[220:223],a[104:107],v[108:111]\n"
MF "v[100:103],a[220:223],a[100:103],v[100:103]\n"
MF "v[100:103],a[216:219],a[60:63],v[100:103]\n"
"s_waitcnt vmcnt(23) lgkmcnt(0)\n"
"s_barrier\n"
MF "v[136:139],a[200:203],a[60:63],v[136:139]\n"
TR "a[216:217],v0 offset:32768\n"
MF "v[136:139],a[204:207],a[100:103],v[136:139]\n"
"s_add_u32 m0,s62,16384\n"
TR "a[218:219],v0 offset:33280\n"
MF "v[144:147],a[204:207],a[104:107],v[144:147]\n"
"s_add_u32 s82,s66,4718720\n"
"ds_read_b128 a[140:143],v2 offset:40960\n"
MF "v[144:147],a[200:203],a[64:67],v[144:147]\n"
"s_sub_u32 s15,s82,7864320\n"
TR "a[220:221],v0 offset:36864\n"
MF "v[152:155],a[200:203],a[68:71],v[152:155]\n"
"s_min_u32 s82,s82,s15\n"
TR "a[222:223],v0 offset:37376\n"
MF "v[152:155],a[204:207],a[108:111],v[152:155]\n"
LD "v6,s[28:31],s82 offen lds\n"
MF "v[160:163],a[204:207],a[112:115],v[160:163]\n"
"ds_read_b128 a[180:183],v3 offset:40960\n"
MF "v[160:163],a[200:203],a[72:75],v[160:163]\n"
LD "v11,s[28:31],s82 offen offset:1024 lds\n"
MF "v[168:171],a[200:203],a[76:79],v[168:171]\n"
MF "v[168:171],a[204:207],a[116:119],v[168:171]\n"
LD "v12,s[28:31],s82 offen offset:2048 lds\n"
MF "v[172:175],a[212:215],a[116:119],v[172:175]\n"
TR "a[200:201],v0 offset:32776\n"
MF "v[172:175],a[208:211],a[76:79],v[172:175]\n"
TR "a[202:203],v0 offset:33288\n"
MF "v[164:167],a[208:211],a[72:75],v[164:167]\n"
"ds_read_b128 a[144:147],v2 offset:43008\n"
MF "v[164:167],a[212:215],a[112:115],v[164:167]\n"
TR "a[204:205],v0 offset:36872\n"
MF "v[156:159],a[212:215],a[108:111],v[156:159]\n"
TR "a[206:207],v0 offset:37384\n"
MF "v[156:159],a[208:211],a[68:71],v[156:159]\n"
MF "v[148:151],a[208:211],a[64:67],v[148:151]\n"
"ds_read_b128 a[184:187],v3 offset:43008\n"
MF "v[148:151],a[212:215],a[104:107],v[148:151]\n"
MF "v[140:143],a[212:215],a[100:103],v[140:143]\n"
LD "v13,s[28:31],s82 offen offset:3072 lds\n"
MF "v[140:143],a[208:211],a[60:63],v[140:143]\n"
"s_waitcnt lgkmcnt(7)\n"
MF "v[176:179],a[216:219],a[60:63],v[176:179]\n"
TR "a[208:209],v1 offset:32768\n"
MF "v[176:179],a[220:223],a[100:103],v[176:179]\n"
TR "a[210:211],v1 offset:33280\n"
MF "v[184:187],a[220:223],a[104:107],v[184:187]\n"
"ds_read_b128 a[148:151],v2 offset:45056\n"
MF "v[184:187],a[216:219],a[64:67],v[184:187]\n"
"s_add_u32 s84,s14,20480\n"
TR "a[212:213],v1 offset:36864\n"
MF "v[192:195],a[216:219],a[68:71],v[192:195]\n"
"s_add_u32 m0,s63,22528\n"
TR "a[214:215],v1 offset:37376\n"
MF "v[192:195],a[220:223],a[108:111],v[192:195]\n"
LD "v4,s[24:27],s84 offen lds\n"
MF "v[200:203],a[220:223],a[112:115],v[200:203]\n"
"ds_read_b128 a[188:191],v3 offset:45056\n"
MF "v[200:203],a[216:219],a[72:75],v[200:203]\n"
MF "v[208:211],a[216:219],a[76:79],v[208:211]\n"
"s_add_u32 s84,s14,29696\n"
MF "v[208:211],a[220:223],a[116:119],v[208:211]\n"
"s_waitcnt lgkmcnt(7)\n"
LD "v5,s[24:27],s84 offen offset:1024 lds\n"
MF "v[212:215],a[204:207],a[116:119],v[212:215]\n"
TR "a[216:217],v1 offset:32776\n"
MF "v[212:215],a[200:203],a[76:79],v[212:215]\n"
TR "a[218:219],v1 offset:33288\n"
MF "v[204:207],a[200:203],a[72:75],v[204:207]\n"
"ds_read_b128 a[152:155],v2 offset:47104\n"
MF "v[204:207],a[204:207],a[112:115],v[204:207]\n"
TR "a[220:221],v1 offset:36872\n"
MF "v[196:199],a[204:207],a[108:111],v[196:199]\n"
TR "a[222:223],v1 offset:37384\n"
MF "v[196:199],a[200:203],a[68:71],v[196:199]\n"
MF "v[188:191],a[200:203],a[64:67],v[188:191]\n"
"v_add_u32 v0,0xc000,v0\n"
MF "v[188:191],a[204:207],a[104:107],v[188:191]\n"
"v_add_u32 v1,0xc000,v1\n"
MF "v[180:183],a[204:207],a[100:103],v[180:183]\n"
MF "v[180:183],a[200:203],a[60:63],v[180:183]\n"
"s_waitcnt vmcnt(25) lgkmcnt(0)\n"
"s_barrier\n"
MF "v[216:219],a[208:211],a[60:63],v[216:219]\n"
TR "a[200:201],v0\n"
MF "v[216:219],a[212:215],a[100:103],v[216:219]\n"
"s_add_u32 m0,s62,32768\n"
TR "a[202:203],v0 offset:512\n"
MF "v[224:227],a[212:215],a[104:107],v[224:227]\n"
"s_add_u32 s82,s66,4718848\n"
"ds_read_b128 a[192:195],v3 offset:47104\n"
MF "v[224:227],a[208:211],a[64:67],v[224:227]\n"
"s_sub_u32 s15,s82,7864320\n"
TR "a[204:205],v0 offset:4096\n"
MF "v[232:235],a[208:211],a[68:71],v[232:235]\n"
"s_min_u32 s82,s82,s15\n"
TR "a[206:207],v0 offset:4608\n"
MF "v[232:235],a[212:215],a[108:111],v[232:235]\n"
LD "v6,s[28:31],s82 offen lds\n"
MF "v[240:243],a[212:215],a[112:115],v[240:243]\n"
"ds_read_b128 a[156:159],v2 offset:49152\n"
MF "v[240:243],a[208:211],a[72:75],v[240:243]\n"
"ds_read_b128 a[196:199],v3 offset:49152\n"
MF "v[248:251],a[208:211],a[76:79],v[248:251]\n"
LD "v11,s[28:31],s82 offen offset:1024 lds\n"
MF "v[248:251],a[212:215],a[116:119],v[248:251]\n"
MF "v[252:255],a[220:223],a[116:119],v[252:255]\n"
TR "a[208:209],v0 offset:8\n"
MF "v[252:255],a[216:219],a[76:79],v[252:255]\n"
TR "a[210:211],v0 offset:520\n"
MF "v[244:247],a[216:219],a[72:75],v[244:247]\n"
MF "v[244:247],a[220:223],a[112:115],v[244:247]\n"
TR "a[212:213],v0 offset:4104\n"
MF "v[236:239],a[220:223],a[108:111],v[236:239]\n"
TR "a[214:215],v0 offset:4616\n"
MF "v[236:239],a[216:219],a[68:71],v[236:239]\n"
LD "v12,s[28:31],s82 offen offset:2048 lds\n"
MF "v[228:231],a[216:219],a[64:67],v[228:231]\n"
MF "v[228:231],a[220:223],a[104:107],v[228:231]\n"
MF "v[220:223],a[220:223],a[100:103],v[220:223]\n"
MF "v[220:223],a[216:219],a[60:63],v[220:223]\n"
"s_waitcnt lgkmcnt(4)\n"
MF "v[16:19],a[200:203],a[140:143],v[16:19]\n"
TR "a[216:217],v1\n"
MF "v[16:19],a[204:207],a[180:183],v[16:19]\n"
TR "a[218:219],v1 offset:512\n"
MF "v[24:27],a[204:207],a[184:187],v[24:27]\n"
LD "v13,s[28:31],s82 offen offset:3072 lds\n"
MF "v[24:27],a[200:203],a[144:147],v[24:27]\n"
TR "a[220:221],v1 offset:4096\n"
MF "v[32:35],a[200:203],a[148:151],v[32:35]\n"
TR "a[222:223],v1 offset:4608\n"
MF "v[32:35],a[204:207],a[188:191],v[32:35]\n"
MF "v[40:43],a[204:207],a[192:195],v[40:43]\n"
MF "v[40:43],a[200:203],a[152:155],v[40:43]\n"
"s_add_u32 s84,s14,40960\n"
MF "v[48:51],a[200:203],a[156:159],v[48:51]\n"
"s_add_u32 m0,s63,24576\n"
MF "v[48:51],a[204:207],a[196:199],v[48:51]\n"
"s_waitcnt lgkmcnt(4)\n"
LD "v4,s[24:27],s84 offen lds\n"
MF "v[52:55],a[212:215],a[196:199],v[52:55]\n"
TR "a[200:201],v1 offset:8\n"
MF "v[52:55],a[208:211],a[156:159],v[52:55]\n"
TR "a[202:203],v1 offset:520\n"
MF "v[44:47],a[208:211],a[152:155],v[44:47]\n"
MF "v[44:47],a[212:215],a[192:195],v[44:47]\n"
TR "a[204:205],v1 offset:4104\n"
MF "v[36:39],a[212:215],a[188:191],v[36:39]\n"
TR "a[206:207],v1 offset:4616\n"
MF "v[36:39],a[208:211],a[148:151],v[36:39]\n"
MF "v[28:31],a[208:211],a[144:147],v[28:31]\n"
MF "v[28:31],a[212:215],a[184:187],v[28:31]\n"
MF "v[20:23],a[212:215],a[180:183],v[20:23]\n"
MF "v[20:23],a[208:211],a[140:143],v[20:23]\n"
".Lpg6c:\n"
"s_cbranch_execz .Lpg1a\n"
".Lpg6f:\n"
"s_cbranch_execz .Lpg15e\n"
"s_waitcnt vmcnt(24) lgkmcnt(0)\n"
"s_barrier\n"
MF "v[56:59],a[216:219],a[140:143],v[56:59]\n"
"s_add_u32 m0,s62,49152\n"
"s_add_u32 s82,s66,5505024\n"
TR "a[208:209],v0 offset:16384\n"
MF "v[56:59],a[220:223],a[180:183],v[56:59]\n"
"s_sub_u32 s15,s82,7864320\n"
"s_min_u32 s82,s82,s15\n"
TR "a[210:211],v0 offset:16896\n"
MF "v[64:67],a[220:223],a[184:187],v[64:67]\n"
LD "v6,s[28:31],s82 offen lds\n"
MF "v[64:67],a[216:219],a[144:147],v[64:67]\n"
TR "a[212:213],v0 offset:20480\n"
MF "v[72:75],a[216:219],a[148:151],v[72:75]\n"
TR "a[214:215],v0 offset:20992\n"
MF "v[72:75],a[220:223],a[188:191],v[72:75]\n"
MF "v[80:83],a[220:223],a[192:195],v[80:83]\n"
LD "v11,s[28:31],s82 offen offset:1024 lds\n"
MF "v[80:83],a[216:219],a[152:155],v[80:83]\n"
MF "v[88:91],a[216:219],a[156:159],v[88:91]\n"
MF "v[88:91],a[220:223],a[196:199],v[88:91]\n"
MF "v[92:95],a[204:207],a[196:199],v[92:95]\n"
TR "a[216:217],v0 offset:16392\n"
MF "v[92:95],a[200:203],a[156:159],v[92:95]\n"
TR "a[218:219],v0 offset:16904\n"
MF "v[84:87],a[200:203],a[152:155],v[84:87]\n"
LD "v12,s[28:31],s82 offen offset:2048 lds\n"
MF "v[84:87],a[204:207],a[192:195],v[84:87]\n"
TR "a[220:221],v0 offset:20488\n"
MF "v[76:79],a[204:207],a[188:191],v[76:79]\n"
TR "a[222:223],v0 offset:21000\n"
MF "v[76:79],a[200:203],a[148:151],v[76:79]\n"
MF "v[68:71],a[200:203],a[144:147],v[68:71]\n"
MF "v[68:71],a[204:207],a[184:187],v[68:71]\n"
MF "v[60:63],a[204:207],a[180:183],v[60:63]\n"
LD "v13,s[28:31],s82 offen offset:3072 lds\n"
MF "v[60:63],a[200:203],a[140:143],v[60:63]\n"
"s_waitcnt lgkmcnt(4)\n"
MF "v[96:99],a[208:211],a[140:143],v[96:99]\n"
TR "a[200:201],v1 offset:16384\n"
MF "v[96:99],a[212:215],a[180:183],v[96:99]\n"
TR "a[202:203],v1 offset:16896\n"
MF "v[104:107],a[212:215],a[184:187],v[104:107]\n"
"s_add_u32 s14,s81,1024\n"
"s_sub_u32 s15,s14,1280\n"
MF "v[104:107],a[208:211],a[144:147],v[104:107]\n"
"s_min_u32 s14,s14,s15\n"
"s_add_u32 s14,s14,s68\n"
TR "a[204:205],v1 offset:20480\n"
MF "v[112:115],a[208:211],a[148:151],v[112:115]\n"
"s_add_u32 s84,s14,0\n"
"s_add_u32 m0,s63,40960\n"
TR "a[206:207],v1 offset:20992\n"
MF "v[112:115],a[212:215],a[188:191],v[112:115]\n"
LD "v4,s[24:27],s84 offen lds\n"
MF "v[120:123],a[212:215],a[192:195],v[120:123]\n"
MF "v[120:123],a[208:211],a[152:155],v[120:123]\n"
MF "v[128:131],a[208:211],a[156:159],v[128:131]\n"
"s_add_u32 s84,s14,9216\n"
MF "v[128:131],a[212:215],a[196:199],v[128:131]\n"
"s_waitcnt lgkmcnt(4)\n"
LD "v5,s[24:27],s84 offen offset:1024 lds\n"
MF "v[132:135],a[220:223],a[196:199],v[132:135]\n"
TR "a[208:209],v1 offset:16392\n"
MF "v[132:135],a[216:219],a[156:159],v[132:135]\n"
TR "a[210:211],v1 offset:16904\n"
MF "v[124:127],a[216:219],a[152:155],v[124:127]\n"
MF "v[124:127],a[220:223],a[192:195],v[124:127]\n"
TR "a[212:213],v1 offset:20488\n"
MF "v[116:119],a[220:223],a[188:191],v[116:119]\n"
TR "a[214:215],v1 offset:21000\n"
MF "v[116:119],a[216:219],a[148:151],v[116:119]\n"
MF "v[108:111],a[216:219],a[144:147],v[108:111]\n"
MF "v[108:111],a[220:223],a[184:187],v[108:111]\n"
MF "v[100:103],a[220:223],a[180:183],v[100:103]\n"
MF "v[100:103],a[216:219],a[140:143],v[100:103]\n"
"s_waitcnt vmcnt(23) lgkmcnt(0)\n"
"s_barrier\n"
"s_cmp_eq_u32 s57,1\n"
"s_cbranch_scc0 .Lhs12\n"
"v_bfe_u32 v14,v9,4,5\n"
"v_lshlrev_b32 v14,6,v14\n"
"s_lshl_b32 s58,s78,2\n"
"s_add_u32 s58,s58,s86\n"
"buffer_load_dword v10,v14,s[40:43],s58 offen offset:128 sc1\n"
".Lhs12:\n"
MF "v[136:139],a[200:203],a[140:143],v[136:139]\n"
TR "a[216:217],v0 offset:32768\n"
MF "v[136:139],a[204:207],a[180:183],v[136:139]\n"
"s_add_u32 m0,s62,65536\n"
TR "a[218:219],v0 offset:33280\n"
MF "v[144:147],a[204:207],a[184:187],v[144:147]\n"
"s_add_u32 s82,s66,5505152\n"
"ds_read_b128 a[60:63],v2\n"
MF "v[144:147],a[200:203],a[144:147],v[144:147]\n"
"s_sub_u32 s15,s82,7864320\n"
TR "a[220:221],v0 offset:36864\n"
MF "v[152:155],a[200:203],a[148:151],v[152:155]\n"
"s_min_u32 s82,s82,s15\n"
TR "a[222:223],v0 offset:37376\n"
MF "v[152:155],a[204:207],a[188:191],v[152:155]\n"
LD "v6,s[28:31],s82 offen lds\n"
MF "v[160:163],a[204:207],a[192:195],v[160:163]\n"
"ds_read_b128 a[100:103],v3\n"
MF "v[160:163],a[200:203],a[152:155],v[160:163]\n"
LD "v11,s[28:31],s82 offen offset:1024 lds\n"
MF "v[168:171],a[200:203],a[156:159],v[168:171]\n"
MF "v[168:171],a[204:207],a[196:199],v[168:171]\n"
LD "v12,s[28:31],s82 offen offset:2048 lds\n"
MF "v[172:175],a[212:215],a[196:199],v[172:175]\n"
TR "a[200:201],v0 offset:32776\n"
MF "v[172:175],a[208:211],a[156:159],v[172:175]\n"
TR "a[202:203],v0 offset:33288\n"
MF "v[164:167],a[208:211],a[152:155],v[164:167]\n"
"ds_read_b128 a[64:67],v2 offset:2048\n"
MF "v[164:167],a[212:215],a[192:195],v[164:167]\n"
TR "a[204:205],v0 offset:36872\n"
MF "v[156:159],a[212:215],a[188:191],v[156:159]\n"
TR "a[206:207],v0 offset:37384\n"
MF "v[156:159],a[208:211],a[148:151],v[156:159]\n"
MF "v[148:151],a[208:211],a[144:147],v[148:151]\n"
"ds_read_b128 a[104:107],v3 offset:2048\n"
MF "v[148:151],a[212:215],a[184:187],v[148:151]\n"
MF "v[140:143],a[212:215],a[180:183],v[140:143]\n"
LD "v13,s[28:31],s82 offen offset:3072 lds\n"
MF "v[140:143],a[208:211],a[140:143],v[140:143]\n"
"s_waitcnt lgkmcnt(7)\n"
MF "v[176:179],a[216:219],a[140:143],v[176:179]\n"
TR "a[208:209],v1 offset:32768\n"
MF "v[176:179],a[220:223],a[180:183],v[176:179]\n"
TR "a[210:211],v1 offset:33280\n"
MF "v[184:187],a[220:223],a[184:187],v[184:187]\n"
"ds_read_b128 a[68:71],v2 offset:4096\n"
MF "v[184:187],a[216:219],a[144:147],v[184:187]\n"
"s_add_u32 s84,s14,20480\n"
TR "a[212:213],v1 offset:36864\n"
MF "v[192:195],a[216:219],a[148:151],v[192:195]\n"
"s_add_u32 m0,s63,43008\n"
TR "a[214:215],v1 offset:37376\n"
MF "v[192:195],a[220:223],a[188:191],v[192:195]\n"
LD "v4,s[24:27],s84 offen lds\n"
MF "v[200:203],a[220:223],a[192:195],v[200:203]\n"
"ds_read_b128 a[108:111],v3 offset:4096\n"
MF "v[200:203],a[216:219],a[152:155],v[200:203]\n"
MF "v[208:211],a[216:219],a[156:159],v[208:211]\n"
"s_add_u32 s84,s14,29696\n"
MF "v[208:211],a[220:223],a[196:199],v[208:211]\n"
"s_waitcnt lgkmcnt(7)\n"
LD "v5,s[24:27],s84 offen offset:1024 lds\n"
MF "v[212:215],a[204:207],a[196:199],v[212:215]\n"
TR "a[216:217],v1 offset:32776\n"
MF "v[212:215],a[200:203],a[156:159],v[212:215]\n"
TR "a[218:219],v1 offset:33288\n"
MF "v[204:207],a[200:203],a[152:155],v[204:207]\n"
"ds_read_b128 a[72:75],v2 offset:6144\n"
MF "v[204:207],a[204:207],a[192:195],v[204:207]\n"
TR "a[220:221],v1 offset:36872\n"
MF "v[196:199],a[204:207],a[188:191],v[196:199]\n"
TR "a[222:223],v1 offset:37384\n"
MF "v[196:199],a[200:203],a[148:151],v[196:199]\n"
MF "v[188:191],a[200:203],a[144:147],v[188:191]\n"
"v_add_u32 v0,0xffff4000,v0\n"
MF "v[188:191],a[204:207],a[184:187],v[188:191]\n"
"v_add_u32 v1,0xffff4000,v1\n"
MF "v[180:183],a[204:207],a[180:183],v[180:183]\n"
MF "v[180:183],a[200:203],a[140:143],v[180:183]\n"
"s_waitcnt vmcnt(25) lgkmcnt(0)\n"
"s_barrier\n"
MF "v[216:219],a[208:211],a[140:143],v[216:219]\n"
TR "a[200:201],v0\n"
MF "v[216:219],a[212:215],a[180:183],v[216:219]\n"
"s_add_u32 m0,s62,81920\n"
TR "a[202:203],v0 offset:512\n"
MF "v[224:227],a[212:215],a[184:187],v[224:227]\n"
"s_add_u32 s82,s66,5505280\n"
"ds_read_b128 a[112:115],v3 offset:6144\n"
MF "v[224:227],a[208:211],a[144:147],v[224:227]\n"
"s_sub_u32 s15,s82,7864320\n"
TR "a[204:205],v0 offset:4096\n"
MF "v[232:235],a[208:211],a[148:151],v[232:235]\n"
"s_min_u32 s82,s82,s15\n"
TR "a[206:207],v0 offset:4608\n"
MF "v[232:235],a[212:215],a[188:191],v[232:235]\n"
LD "v6,s[28:31],s82 offen lds\n"
MF "v[240:243],a[212:215],a[192:195],v[240:243]\n"
"ds_read_b128 a[76:79],v2 offset:8192\n"
MF "v[240:243],a[208:211],a[152:155],v[240:243]\n"
"ds_read_b128 a[116:119],v3 offset:8192\n"
MF "v[248:251],a[208:211],a[156:159],v[248:251]\n"
LD "v11,s[28:31],s82 offen offset:1024 lds\n"
MF "v[248:251],a[212:215],a[196:199],v[248:251]\n"
MF "v[252:255],a[220:223],a[196:199],v[252:255]\n"
TR "a[208:209],v0 offset:8\n"
MF "v[252:255],a[216:219],a[156:159],v[252:255]\n"
TR "a[210:211],v0 offset:520\n"
MF "v[244:247],a[216:219],a[152:155],v[244:247]\n"
MF "v[244:247],a[220:223],a[192:195],v[244:247]\n"
TR "a[212:213],v0 offset:4104\n"
MF "v[236:239],a[220:223],a[188:191],v[236:239]\n"
TR "a[214:215],v0 offset:4616\n"
MF "v[236:239],a[216:219],a[148:151],v[236:239]\n"
LD "v12,s[28:31],s82 offen offset:2048 lds\n"
MF "v[228:231],a[216:219],a[144:147],v[228:231]\n"
MF "v[228:231],a[220:223],a[184:187],v[228:231]\n"
MF "v[220:223],a[220:223],a[180:183],v[220:223]\n"
MF "v[220:223],a[216:219],a[140:143],v[220:223]\n"
"s_waitcnt lgkmcnt(4)\n"
MF "v[16:19],a[200:203],a[60:63],v[16:19]\n"
TR "a[216:217],v1\n"
MF "v[16:19],a[204:207],a[100:103],v[16:19]\n"
TR "a[218:219],v1 offset:512\n"
MF "v[24:27],a[204:207],a[104:107],v[24:27]\n"
LD "v13,s[28:31],s82 offen offset:3072 lds\n"
MF "v[24:27],a[200:203],a[64:67],v[24:27]\n"
TR "a[220:221],v1 offset:4096\n"
MF "v[32:35],a[200:203],a[68:71],v[32:35]\n"
TR "a[222:223],v1 offset:4608\n"
MF "v[32:35],a[204:207],a[108:111],v[32:35]\n"
MF "v[40:43],a[204:207],a[112:115],v[40:43]\n"
MF "v[40:43],a[200:203],a[72:75],v[40:43]\n"
"s_add_u32 s84,s14,40960\n"
MF "v[48:51],a[200:203],a[76:79],v[48:51]\n"
"s_add_u32 m0,s63,45056\n"
MF "v[48:51],a[204:207],a[116:119],v[48:51]\n"
"s_waitcnt lgkmcnt(4)\n"
LD "v4,s[24:27],s84 offen lds\n"
MF "v[52:55],a[212:215],a[116:119],v[52:55]\n"
TR "a[200:201],v1 offset:8\n"
MF "v[52:55],a[208:211],a[76:79],v[52:55]\n"
TR "a[202:203],v1 offset:520\n"
MF "v[44:47],a[208:211],a[72:75],v[44:47]\n"
MF "v[44:47],a[212:215],a[112:115],v[44:47]\n"
TR "a[204:205],v1 offset:4104\n"
MF "v[36:39],a[212:215],a[108:111],v[36:39]\n"
TR "a[206:207],v1 offset:4616\n"
MF "v[36:39],a[208:211],a[68:71],v[36:39]\n"
MF "v[28:31],a[208:211],a[64:67],v[28:31]\n"
MF "v[28:31],a[212:215],a[104:107],v[28:31]\n"
MF "v[20:23],a[212:215],a[100:103],v[20:23]\n"
MF "v[20:23],a[208:211],a[60:63],v[20:23]\n"
"s_waitcnt vmcnt(24) lgkmcnt(0)\n"
"s_barrier\n"
MF "v[56:59],a[216:219],a[60:63],v[56:59]\n"
"s_add_u32 m0,s62,0\n"
"s_add_u32 s82,s66,6291712\n"
TR "a[208:209],v0 offset:16384\n"
MF "v[56:59],a[220:223],a[100:103],v[56:59]\n"
"s_sub_u32 s15,s82,7864320\n"
"s_min_u32 s82,s82,s15\n"
TR "a[210:211],v0 offset:16896\n"
MF "v[64:67],a[220:223],a[104:107],v[64:67]\n"
LD "v6,s[28:31],s82 offen lds\n"
MF "v[64:67],a[216:219],a[64:67],v[64:67]\n"
TR "a[212:213],v0 offset:20480\n"
MF "v[72:75],a[216:219],a[68:71],v[72:75]\n"
TR "a[214:215],v0 offset:20992\n"
MF "v[72:75],a[220:223],a[108:111],v[72:75]\n"
MF "v[80:83],a[220:223],a[112:115],v[80:83]\n"
LD "v11,s[28:31],s82 offen offset:1024 lds\n"
MF "v[80:83],a[216:219],a[72:75],v[80:83]\n"
MF "v[88:91],a[216:219],a[76:79],v[88:91]\n"
MF "v[88:91],a[220:223],a[116:119],v[88:91]\n"
MF "v[92:95],a[204:207],a[116:119],v[92:95]\n"
TR "a[216:217],v0 offset:16392\n"
MF "v[92:95],a[200:203],a[76:79],v[92:95]\n"
TR "a[218:219],v0 offset:16904\n"
MF "v[84:87],a[200:203],a[72:75],v[84:87]\n"
LD "v12,s[28:31],s82 offen offset:2048 lds\n"
MF "v[84:87],a[204:207],a[112:115],v[84:87]\n"
TR "a[220:221],v0 offset:20488\n"
MF "v[76:79],a[204:207],a[108:111],v[76:79]\n"
TR "a[222:223],v0 offset:21000\n"
MF "v[76:79],a[200:203],a[68:71],v[76:79]\n"
MF "v[68:71],a[200:203],a[64:67],v[68:71]\n"
LD "v13,s[28:31],s82 offen offset:3072 lds\n"
MF "v[68:71],a[204:207],a[104:107],v[68:71]\n"
MF "v[60:63],a[204:207],a[100:103],v[60:63]\n"
MF "v[60:63],a[200:203],a[60:63],v[60:63]\n"
"s_add_u32 s14,s81,1152\n"
"s_sub_u32 s15,s14,1280\n"
"s_waitcnt lgkmcnt(4)\n"
MF "v[96:99],a[208:211],a[60:63],v[96:99]\n"
"s_min_u32 s14,s14,s15\n"
"s_add_u32 s14,s14,s68\n"
TR "a[200:201],v1 offset:16384\n"
MF "v[96:99],a[212:215],a[100:103],v[96:99]\n"
"s_add_u32 s84,s14,0\n"
"s_add_u32 m0,s63,0\n"
TR "a[202:203],v1 offset:16896\n"
MF "v[104:107],a[212:215],a[104:107],v[104:107]\n"
LD "v4,s[24:27],s84 offen lds\n"
MF "v[104:107],a[208:211],a[64:67],v[104:107]\n"
TR "a[204:205],v1 offset:20480\n"
MF "v[112:115],a[208:211],a[68:71],v[112:115]\n"
TR "a[206:207],v1 offset:20992\n"
MF "v[112:115],a[212:215],a[108:111],v[112:115]\n"
"s_add_u32 s84,s14,9216\n"
MF "v[120:123],a[212:215],a[112:115],v[120:123]\n"
LD "v5,s[24:27],s84 offen offset:1024 lds\n"
MF "v[120:123],a[208:211],a[72:75],v[120:123]\n"
MF "v[128:131],a[208:211],a[76:79],v[128:131]\n"
MF "v[128:131],a[212:215],a[116:119],v[128:131]\n"
"s_waitcnt lgkmcnt(4)\n"
MF "v[132:135],a[220:223],a[116:119],v[132:135]\n"
TR "a[208:209],v1 offset:16392\n"
MF "v[132:135],a[216:219],a[76:79],v[132:135]\n"
"s_add_u32 s84,s14,18432\n"
TR "a[210:211],v1 offset:16904\n"
MF "v[124:127],a[216:219],a[72:75],v[124:127]\n"
LD "v4,s[24:27],s84 offen offset:2048 lds\n"
MF "v[124:127],a[220:223],a[112:115],v[124:127]\n"
TR "a[212:213],v1 offset:20488\n"
MF "v[116:119],a[220:223],a[108:111],v[116:119]\n"
TR "a[214:215],v1 offset:21000\n"
MF "v[116:119],a[216:219],a[68:71],v[116:119]\n"
MF "v[108:111],a[216:219],a[64:67],v[108:111]\n"
MF "v[108:111],a[220:223],a[104:107],v[108:111]\n"
MF "v[100:103],a[220:223],a[100:103],v[100:103]\n"
MF "v[100:103],a[216:219],a[60:63],v[100:103]\n"
"s_waitcnt vmcnt(24) lgkmcnt(0)\n"
"s_barrier\n"
MF "v[136:139],a[200:203],a[60:63],v[136:139]\n"
TR "a[216:217],v0 offset:32768\n"
MF "v[136:139],a[204:207],a[100:103],v[136:139]\n"
"s_add_u32 m0,s62,16384\n"
TR "a[218:219],v0 offset:33280\n"
MF "v[144:147],a[204:207],a[104:107],v[144:147]\n"
"s_add_u32 s82,s66,7078144\n"
"ds_read_b128 a[140:143],v2 offset:20480\n"
MF "v[144:147],a[200:203],a[64:67],v[144:147]\n"
"s_sub_u32 s15,s82,7864320\n"
TR "a[220:221],v0 offset:36864\n"
MF "v[152:155],a[200:203],a[68:71],v[152:155]\n"
"s_min_u32 s82,s82,s15\n"
TR "a[222:223],v0 offset:37376\n"
MF "v[152:155],a[204:207],a[108:111],v[152:155]\n"
LD "v6,s[28:31],s82 offen lds\n"
MF "v[160:163],a[204:207],a[112:115],v[160:163]\n"
"ds_read_b128 a[180:183],v3 offset:20480\n"
MF "v[160:163],a[200:203],a[72:75],v[160:163]\n"
LD "v11,s[28:31],s82 offen offset:1024 lds\n"
MF "v[168:171],a[200:203],a[76:79],v[168:171]\n"
MF "v[168:171],a[204:207],a[116:119],v[168:171]\n"
LD "v12,s[28:31],s82 offen offset:2048 lds\n"
MF "v[172:175],a[212:215],a[116:119],v[172:175]\n"
TR "a[200:201],v0 offset:32776\n"
MF "v[172:175],a[208:211],a[76:79],v[172:175]\n"
TR "a[202:203],v0 offset:33288\n"
MF "v[164:167],a[208:211],a[72:75],v[164:167]\n"
"ds_read_b128 a[144:147],v2 offset:22528\n"
MF "v[164:167],a[212:215],a[112:115],v[164:167]\n"
TR "a[204:205],v0 offset:36872\n"
MF "v[156:159],a[212:215],a[108:111],v[156:159]\n"
TR "a[206:207],v0 offset:37384\n"
MF "v[156:159],a[208:211],a[68:71],v[156:159]\n"
MF "v[148:151],a[208:211],a[64:67],v[148:151]\n"
"ds_read_b128 a[184:187],v3 offset:22528\n"
MF "v[148:151],a[212:215],a[104:107],v[148:151]\n"
MF "v[140:143],a[212:215],a[100:103],v[140:143]\n"
LD "v13,s[28:31],s82 offen offset:3072 lds\n"
MF "v[140:143],a[208:211],a[60:63],v[140:143]\n"
"s_waitcnt lgkmcnt(7)\n"
MF "v[176:179],a[216:219],a[60:63],v[176:179]\n"
TR "a[208:209],v1 offset:32768\n"
MF "v[176:179],a[220:223],a[100:103],v[176:179]\n"
TR "a[210:211],v1 offset:33280\n"
MF "v[184:187],a[220:223],a[104:107],v[184:187]\n"
"ds_read_b128 a[148:151],v2 offset:24576\n"
MF "v[184:187],a[216:219],a[64:67],v[184:187]\n"
"s_add_u32 s84,s14,30720\n"
TR "a[212:213],v1 offset:36864\n"
MF "v[192:195],a[216:219],a[68:71],v[192:195]\n"
"s_add_u32 m0,s63,3072\n"
TR "a[214:215],v1 offset:37376\n"
MF "v[192:195],a[220:223],a[108:111],v[192:195]\n"
LD "v5,s[24:27],s84 offen lds\n"
MF "v[200:203],a[220:223],a[112:115],v[200:203]\n"
"ds_read_b128 a[188:191],v3 offset:24576\n"
MF "v[200:203],a[216:219],a[72:75],v[200:203]\n"
MF "v[208:211],a[216:219],a[76:79],v[208:211]\n"
"s_add_u32 s84,s14,39936\n"
MF "v[208:211],a[220:223],a[116:119],v[208:211]\n"
"s_waitcnt lgkmcnt(7)\n"
LD "v4,s[24:27],s84 offen offset:1024 lds\n"
MF "v[212:215],a[204:207],a[116:119],v[212:215]\n"
TR "a[216:217],v1 offset:32776\n"
MF "v[212:215],a[200:203],a[76:79],v[212:215]\n"
TR "a[218:219],v1 offset:33288\n"
MF "v[204:207],a[200:203],a[72:75],v[204:207]\n"
"ds_read_b128 a[152:155],v2 offset:26624\n"
MF "v[204:207],a[204:207],a[112:115],v[204:207]\n"
TR "a[220:221],v1 offset:36872\n"
MF "v[196:199],a[204:207],a[108:111],v[196:199]\n"
TR "a[222:223],v1 offset:37384\n"
MF "v[196:199],a[200:203],a[68:71],v[196:199]\n"
MF "v[188:191],a[200:203],a[64:67],v[188:191]\n"
"v_add_u32 v0,0xc000,v0\n"
MF "v[188:191],a[204:207],a[104:107],v[188:191]\n"
"v_add_u32 v1,0xc000,v1\n"
MF "v[180:183],a[204:207],a[100:103],v[180:183]\n"
".Lpgqj_a:\n"
"s_cbranch_execz .Lisr1\n"
".Lqj:\n"
MF "v[180:183],a[200:203],a[60:63],v[180:183]\n"
"s_waitcnt vmcnt(26) lgkmcnt(0)\n"
"s_barrier\n"
"s_getpc_b64 s[52:53]\n"
".Lctq:\n"
"s_sub_u32 s52,s52,.Lctq-.Lcode0\n"
"s_subb_u32 s53,s53,0\n"
"s_bfe_u32 s58,s23,0x20000\n"
"s_lshl_b32 s58,s58,2\n"
"s_add_u32 s58,s58,s56\n"
"s_lshl_b32 s58,s58,13\n"
"v_lshlrev_b32 v15,3,v9\n"
"v_add_u32 v15,s58,v15\n"
"v_min_u32 v15,.Lcode1-.Lcode0,v15\n"
"global_load_dword a249,v15,s[52:53]\n"
MF "v[216:219],a[208:211],a[60:63],v[216:219]\n"
TR "a[200:201],v0\n"
MF "v[216:219],a[212:215],a[100:103],v[216:219]\n"
"s_add_u32 m0,s62,32768\n"
TR "a[202:203],v0 offset:512\n"
MF "v[224:227],a[212:215],a[104:107],v[224:227]\n"
"s_add_u32 s82,s66,6291584\n"
"ds_read_b128 a[192:195],v3 offset:26624\n"
MF "v[224:227],a[208:211],a[64:67],v[224:227]\n"
"s_sub_u32 s15,s82,7864320\n"
TR "a[204:205],v0 offset:4096\n"
MF "v[232:235],a[208:211],a[68:71],v[232:235]\n"
"s_min_u32 s82,s82,s15\n"
TR "a[206:207],v0 offset:4608\n"
MF "v[232:235],a[212:215],a[108:111],v[232:235]\n"
LD "v6,s[28:31],s82 offen lds\n"
MF "v[240:243],a[212:215],a[112:115],v[240:243]\n"
"ds_read_b128 a[156:159],v2 offset:28672\n"
MF "v[240:243],a[208:211],a[72:75],v[240:243]\n"
"ds_read_b128 a[196:199],v3 offset:28672\n"
MF "v[248:251],a[208:211],a[76:79],v[248:251]\n"
MF "v[248:251],a[212:215],a[116:119],v[248:251]\n"
LD "v11,s[28:31],s82 offen offset:1024 lds\n"
MF "v[252:255],a[220:223],a[116:119],v[252:255]\n"
TR "a[208:209],v0 offset:8\n"
MF "v[252:255],a[216:219],a[76:79],v[252:255]\n"
TR "a[210:211],v0 offset:520\n"
MF "v[244:247],a[216:219],a[72:75],v[244:247]\n"
MF "v[244:247],a[220:223],a[112:115],v[244:247]\n"
TR "a[212:213],v0 offset:4104\n"
MF "v[236:239],a[220:223],a[108:111],v[236:239]\n"
TR "a[214:215],v0 offset:4616\n"
MF "v[236:239],a[216:219],a[68:71],v[236:239]\n"
MF "v[228:231],a[216:219],a[64:67],v[228:231]\n"
MF "v[228:231],a[220:223],a[104:107],v[228:231]\n"
MF "v[220:223],a[220:223],a[100:103],v[220:223]\n"
LD "v12,s[28:31],s82 offen offset:2048 lds\n"
MF "v[220:223],a[216:219],a[60:63],v[220:223]\n"
"s_waitcnt lgkmcnt(4)\n"
MF "v[16:19],a[200:203],a[140:143],v[16:19]\n"
TR "a[216:217],v1\n"
MF "v[16:19],a[204:207],a[180:183],v[16:19]\n"
TR "a[218:219],v1 offset:512\n"
MF "v[24:27],a[204:207],a[184:187],v[24:27]\n"
MF "v[24:27],a[200:203],a[144:147],v[24:27]\n"
TR "a[220:221],v1 offset:4096\n"
MF "v[32:35],a[200:203],a[148:151],v[32:35]\n"
TR "a[222:223],v1 offset:4608\n"
MF "v[32:35],a[204:207],a[188:191],v[32:35]\n"
MF "v[40:43],a[204:207],a[192:195],v[40:43]\n"
MF "v[40:43],a[200:203],a[152:155],v[40:43]\n"
LD "v13,s[28:31],s82 offen offset:3072 lds\n"
MF "v[48:51],a[200:203],a[156:159],v[48:51]\n"
"s_memtime s[52:53]\n"
"s_memrealtime s[100:101]\n"
MF "v[48:51],a[204:207],a[196:199],v[48:51]\n"
"s_waitcnt lgkmcnt(4)\n"
MF "v[52:55],a[212:215],a[196:199],v[52:55]\n"
TR "a[200:201],v1 offset:8\n"
MF "v[52:55],a[208:211],a[156:159],v[52:55]\n"
TR "a[202:203],v1 offset:520\n"
MF "v[44:47],a[208:211],a[152:155],v[44:47]\n"
MF "v[44:47],a[212:215],a[192:195],v[44:47]\n"
TR "a[204:205],v1 offset:4104\n"
MF "v[36:39],a[212:215],a[188:191],v[36:39]\n"
TR "a[206:207],v1 offset:4616\n"
MF "v[36:39],a[208:211],a[148:151],v[36:39]\n"
MF "v[28:31],a[208:211],a[144:147],v[28:31]\n"
MF "v[28:31],a[212:215],a[184:187],v[28:31]\n"
MF "v[20:23],a[212:215],a[180:183],v[20:23]\n"
MF "v[20:23],a[208:211],a[140:143],v[20:23]\n"
"s_waitcnt vmcnt(25) lgkmcnt(0)\n"
"s_barrier\n"
MF "v[56:59],a[216:219],a[140:143],v[56:59]\n"
"s_add_u32 m0,s62,49152\n"
"s_add_u32 s82,s66,7078016\n"
TR "a[208:209],v0 offset:16384\n"
MF "v[56:59],a[220:223],a[180:183],v[56:59]\n"
"s_sub_u32 s15,s82,7864320\n"
"s_min_u32 s82,s82,s15\n"
TR "a[210:211],v0 offset:16896\n"
MF "v[64:67],a[220:223],a[184:187],v[64:67]\n"
LD "v6,s[28:31],s82 offen lds\n"
MF "v[64:67],a[216:219],a[144:147],v[64:67]\n"
"s_sub_u32 s53,s52,s80\n"
TR "a[212:213],v0 offset:20480\n"
"v_lshrrev_b32 v15,8,v9\n"
MF "v[72:75],a[216:219],a[148:151],v[72:75]\n"
TR "a[214:215],v0 offset:20992\n"
"v_mul_u32_u24 v15,48960,v15\n"
MF "v[72:75],a[220:223],a[188:191],v[72:75]\n"
"s_lshr_b32 s7,s56,1\n"
"v_add_u32 v15,v15,v9\n"
MF "v[80:83],a[220:223],a[192:195],v[80:83]\n"
"s_and_b32 s8,s56,1\n"
MF "v[80:83],a[216:219],a[152:155],v[80:83]\n"
"s_sub_u32 s7,s7,s8\n"
MF "v[88:91],a[216:219],a[156:159],v[88:91]\n"
"s_mul_i32 s7,s7,245760\n"
MF "v[88:91],a[220:223],a[196:199],v[88:91]\n"
LD "v11,s[28:31],s82 offen offset:1024 lds\n"
MF "v[92:95],a[204:207],a[196:199],v[92:95]\n"
"s_add_u32 s7,s7,s89\n"
TR "a[216:217],v0 offset:16392\n"
MF "v[92:95],a[200:203],a[156:159],v[92:95]\n"
"s_cmp_gt_u32 s53,24100\n"
"s_cselect_b32 s101,1,0\n"
TR "a[218:219],v0 offset:16904\n"
MF "v[84:87],a[200:203],a[152:155],v[84:87]\n"
"s_cmp_eq_u32 s87,2\n"
"s_cselect_b32 s87,s101,s87\n"
MF "v[84:87],a[204:207],a[192:195],v[84:87]\n"
TR "a[220:221],v0 offset:20488\n"
MF "v[76:79],a[204:207],a[188:191],v[76:79]\n"
TR "a[222:223],v0 offset:21000\n"
MF "v[76:79],a[200:203],a[148:151],v[76:79]\n"
"s_cmp_gt_u32 s87,s57\n"
"s_cselect_b32 s97,130,0\n"
"s_cselect_b32 s95,0,0x47800000\n"
MF "v[68:71],a[200:203],a[144:147],v[68:71]\n"
"s_mov_b32 s21,s100\n"
MF "v[68:71],a[204:207],a[184:187],v[68:71]\n"
"s_mov_b32 s77,s95\n"
MF "v[60:63],a[204:207],a[180:183],v[60:63]\n"
LD "v12,s[28:31],s82 offen offset:2048 lds\n"
MF "v[60:63],a[200:203],a[140:143],v[60:63]\n"
"s_waitcnt lgkmcnt(4)\n"
MF "v[96:99],a[208:211],a[140:143],v[96:99]\n"
TR "a[200:201],v1 offset:16384\n"
MF "v[96:99],a[212:215],a[180:183],v[96:99]\n"
".Lpg7a:\n"
"s_cbranch_execz .Lisr1\n"
TR "a[202:203],v1 offset:16896\n"
MF "v[104:107],a[212:215],a[184:187],v[104:107]\n"
MF "v[104:107],a[208:211],a[144:147],v[104:107]\n"
TR "a[204:205],v1 offset:20480\n"
MF "v[112:115],a[208:211],a[148:151],v[112:115]\n"
TR "a[206:207],v1 offset:20992\n"
MF "v[112:115],a[212:215],a[188:191],v[112:115]\n"
MF "v[120:123],a[212:215],a[192:195],v[120:123]\n"
MF "v[120:123],a[208:211],a[152:155],v[120:123]\n"
LD "v13,s[28:31],s82 offen offset:3072 lds\n"
MF "v[128:131],a[208:211],a[156:159],v[128:131]\n"
MF "v[128:131],a[212:215],a[196:199],v[128:131]\n"
"s_waitcnt lgkmcnt(4)\n"
MF "v[132:135],a[220:223],a[196:199],v[132:135]\n"
TR "a[208:209],v1 offset:16392\n"
MF "v[132:135],a[216:219],a[156:159],v[132:135]\n"
TR "a[210:211],v1 offset:16904\n"
MF "v[124:127],a[216:219],a[152:155],v[124:127]\n"
MF "v[124:127],a[220:223],a[192:195],v[124:127]\n"
TR "a[212:213],v1 offset:20488\n"
MF "v[116:119],a[220:223],a[188:191],v[116:119]\n"
TR "a[214:215],v1 offset:21000\n"
MF "v[116:119],a[216:219],a[148:151],v[116:119]\n"
MF "v[108:111],a[216:219],a[144:147],v[108:111]\n"
MF "v[108:111],a[220:223],a[184:187],v[108:111]\n"
MF "v[100:103],a[220:223],a[180:183],v[100:103]\n"
MF "v[100:103],a[216:219],a[140:143],v[100:103]\n"
"s_waitcnt vmcnt(22) lgkmcnt(0)\n"
"s_barrier\n"
"s_cmp_eq_u32 s57,1\n"
"s_cbranch_scc0 .Lhs15\n"
".Lack_q:\n"
"v_cmp_ne_u32 vcc,s76,v10\n"
"s_cbranch_vccnz .Lackspin_q\n"
".Lackok_q:\n"
".Lhs15:\n"
MF "v[136:139],a[200:203],a[140:143],v[136:139]\n"
"s_and_b32 s58,s56,1\n"
TR "a[216:217],v0 offset:32768\n"
MF "v[136:139],a[204:207],a[180:183],v[136:139]\n"
"s_add_u32 m0,s62,65536\n"
TR "a[218:219],v0 offset:33280\n"
MF "v[144:147],a[204:207],a[184:187],v[144:147]\n"
"s_add_u32 s82,s66,6291456\n"
"ds_read_b128 a[60:63],v2 offset:40960\n"
MF "v[144:147],a[200:203],a[144:147],v[144:147]\n"
"s_sub_u32 s15,s82,7864320\n"
TR "a[220:221],v0 offset:36864\n"
MF "v[152:155],a[200:203],a[148:151],v[152:155]\n"
"s_min_u32 s82,s82,s15\n"
TR "a[222:223],v0 offset:37376\n"
MF "v[152:155],a[204:207],a[188:191],v[152:155]\n"
LD "v6,s[28:31],s82 offen lds\n"
MF "v[160:163],a[204:207],a[192:195],v[160:163]\n"
"s_mul_i32 s59,s58,3072\n"
"ds_read_b128 a[100:103],v3 offset:40960\n"
MF "v[160:163],a[200:203],a[152:155],v[160:163]\n"
"s_add_u32 s16,s70,s59\n"
MF "v[168:171],a[200:203],a[156:159],v[168:171]\n"
"s_mul_i32 s59,s58,6144\n"
MF "v[168:171],a[204:207],a[196:199],v[168:171]\n"
LD "v11,s[28:31],s82 offen offset:1024 lds\n"
MF "v[172:175],a[212:215],a[196:199],v[172:175]\n"
"s_add_u32 s18,s70,s59\n"
TR "a[200:201],v0 offset:32776\n"
MF "v[172:175],a[208:211],a[156:159],v[172:175]\n"
"s_add_u32 s17,s16,196608\n"
TR "a[202:203],v0 offset:33288\n"
MF "v[164:167],a[208:211],a[152:155],v[164:167]\n"
"s_add_u32 s19,s18,196608\n"
"ds_read_b128 a[64:67],v2 offset:43008\n"
MF "v[164:167],a[212:215],a[192:195],v[164:167]\n"
"s_add_u32 s20,s70,196608\n"
TR "a[204:205],v0 offset:36872\n"
MF "v[156:159],a[212:215],a[188:191],v[156:159]\n"
"s_bfe_u32 s58,s23,0x40008\n"
TR "a[206:207],v0 offset:37384\n"
MF "v[156:159],a[208:211],a[148:151],v[156:159]\n"
"s_lshr_b32 s59,s56,1\n"
MF "v[148:151],a[208:211],a[144:147],v[148:151]\n"
"s_mul_i32 s59,s59,5\n"
"ds_read_b128 a[104:107],v3 offset:43008\n"
MF "v[148:151],a[212:215],a[184:187],v[148:151]\n"
"s_add_u32 s58,s58,s59\n"
MF "v[140:143],a[212:215],a[180:183],v[140:143]\n"
LD "v12,s[28:31],s82 offen offset:2048 lds\n"
MF "v[140:143],a[208:211],a[140:143],v[140:143]\n"
"s_waitcnt lgkmcnt(7)\n"
MF "v[176:179],a[216:219],a[140:143],v[176:179]\n"
"s_and_b32 s58,s58,15\n"
TR "a[208:209],v1 offset:32768\n"
MF "v[176:179],a[220:223],a[180:183],v[176:179]\n"
"s_mul_i32 s58,s58,12288\n"
TR "a[210:211],v1 offset:33280\n"
MF "v[184:187],a[220:223],a[184:187],v[184:187]\n"
"s_add_u32 s72,s16,s58\n"
"ds_read_b128 a[68:71],v2 offset:45056\n"
MF "v[184:187],a[216:219],a[144:147],v[184:187]\n"
"s_add_u32 s73,s18,s58\n"
TR "a[212:213],v1 offset:36864\n"
MF "v[192:195],a[216:219],a[148:151],v[192:195]\n"
"s_add_u32 s74,s70,s58\n"
TR "a[214:215],v1 offset:37376\n"
MF "v[192:195],a[220:223],a[188:191],v[192:195]\n"
"s_add_u32 s74,s74,12288\n"
MF "v[200:203],a[220:223],a[192:195],v[200:203]\n"
"s_cmp_eq_u32 s74,s20\n"
"s_cselect_b32 s74,s70,s74\n"
"ds_read_b128 a[108:111],v3 offset:45056\n"
MF "v[200:203],a[216:219],a[152:155],v[200:203]\n"
LD "v13,s[28:31],s82 offen offset:3072 lds\n"
MF "v[208:211],a[216:219],a[156:159],v[208:211]\n"
"s_lshr_b32 s58,s56,1\n"
MF "v[208:211],a[220:223],a[196:199],v[208:211]\n"
"s_waitcnt lgkmcnt(7)\n"
MF "v[212:215],a[204:207],a[196:199],v[212:215]\n"
"s_mul_i32 s58,s58,6144\n"
TR "a[216:217],v1 offset:32776\n"
MF "v[212:215],a[200:203],a[156:159],v[212:215]\n"
"s_mul_i32 s64,s56,3072\n"
TR "a[218:219],v1 offset:33288\n"
MF "v[204:207],a[200:203],a[152:155],v[204:207]\n"
"s_add_u32 s64,s64,s58\n"
"ds_read_b128 a[72:75],v2 offset:47104\n"
MF "v[204:207],a[204:207],a[192:195],v[204:207]\n"
"s_mul_i32 s65,s56,6144\n"
TR "a[220:221],v1 offset:36872\n"
MF "v[196:199],a[204:207],a[188:191],v[196:199]\n"
"s_sub_u32 s58,s71,s70\n"
TR "a[222:223],v1 offset:37384\n"
MF "v[196:199],a[200:203],a[148:151],v[196:199]\n"
"s_add_u32 s96,s74,s58\n"
MF "v[188:191],a[200:203],a[144:147],v[188:191]\n"
"s_sub_u32 s58,s16,s70\n"
"v_add_u32 v0,0xffff4000,v0\n"
MF "v[188:191],a[204:207],a[184:187],v[188:191]\n"
"s_add_u32 s96,s96,s58\n"
"v_add_u32 v1,0xffff4000,v1\n"
MF "v[180:183],a[204:207],a[180:183],v[180:183]\n"
"s_max_u32 s60,s70,s71\n"
MF "v[180:183],a[200:203],a[140:143],v[180:183]\n"
"s_waitcnt vmcnt(13) lgkmcnt(0)\n"
"s_barrier\n"
MF "v[216:219],a[208:211],a[140:143],v[216:219]\n"
"s_add_u32 s34,s60,196608\n"
TR "a[200:201],v1 offset:8\n"
MF "v[216:219],a[212:215],a[180:183],v[216:219]\n"
"s_add_u32 m0,s62,81920\n"
TR "a[202:203],v1 offset:520\n"
"v_mov_b32 v14,s12\n"
MF "v[224:227],a[212:215],a[184:187],v[224:227]\n"
"s_add_u32 s82,s66,7077888\n"
"ds_read_b128 a[112:115],v3 offset:47104\n"
"v_mul_f32 v14,0xbdd53b95,v14\n"
MF "v[224:227],a[208:211],a[144:147],v[224:227]\n"
"s_sub_u32 s15,s82,7864320\n"
TR "a[204:205],v1 offset:4104\n"
MF "v[232:235],a[208:211],a[148:151],v[232:235]\n"
"s_min_u32 s82,s82,s15\n"
TR "a[206:207],v1 offset:4616\n"
MF "v[232:235],a[212:215],a[188:191],v[232:235]\n"
LD "v6,s[28:31],s82 offen lds\n"
MF "v[240:243],a[212:215],a[192:195],v[240:243]\n"
"s_add_u32 s38,s60,196608\n"
"ds_read_b128 a[76:79],v2 offset:49152\n"
MF "v[240:243],a[208:211],a[152:155],v[240:243]\n"
"ds_read_b128 a[116:119],v3 offset:49152\n"
MF "v[248:251],a[208:211],a[156:159],v[248:251]\n"
MF "v[248:251],a[212:215],a[196:199],v[248:251]\n"
LD "v11,s[28:31],s82 offen offset:1024 lds\n"
MF "v[220:223],a[216:219],a[140:143],v[220:223]\n"
TR "a[208:209],v1 offset:16392\n"
MF "v[220:223],a[220:223],a[180:183],v[220:223]\n"
TR "a[210:211],v1 offset:16904\n"
MF "v[228:231],a[220:223],a[184:187],v[228:231]\n"
"ds_read_b128 a[140:143],v2\n"
MF "v[228:231],a[216:219],a[144:147],v[228:231]\n"
TR "a[212:213],v1 offset:20488\n"
MF "v[236:239],a[216:219],a[148:151],v[236:239]\n"
TR "a[214:215],v1 offset:21000\n"
MF "v[236:239],a[220:223],a[188:191],v[236:239]\n"
"ds_read_b128 a[180:183],v3\n"
MF "v[244:247],a[220:223],a[192:195],v[244:247]\n"
"ds_read_b128 a[144:147],v2 offset:2048\n"
MF "v[244:247],a[216:219],a[152:155],v[244:247]\n"
"ds_read_b128 a[184:187],v3 offset:2048\n"
MF "v[252:255],a[216:219],a[156:159],v[252:255]\n"
"ds_read_b128 a[148:151],v2 offset:4096\n"
MF "v[252:255],a[220:223],a[196:199],v[252:255]\n"
"s_waitcnt lgkmcnt(9)\n"
"ds_read_b128 a[188:191],v3 offset:4096\n"
MF "v[220:223],a[200:203],a[60:63],v[220:223]\n"
TR "a[216:217],v1\n"
MF "v[220:223],a[204:207],a[100:103],v[220:223]\n"
TR "a[218:219],v1 offset:512\n"
MF "v[228:231],a[204:207],a[104:107],v[228:231]\n"
"ds_read_b128 a[152:155],v2 offset:6144\n"
MF "v[228:231],a[200:203],a[64:67],v[228:231]\n"
TR "a[220:221],v1 offset:4096\n"
MF "v[236:239],a[200:203],a[68:71],v[236:239]\n"
TR "a[222:223],v1 offset:4608\n"
MF "v[236:239],a[204:207],a[108:111],v[236:239]\n"
"ds_read_b128 a[192:195],v3 offset:6144\n"
MF "v[244:247],a[204:207],a[112:115],v[244:247]\n"
"ds_read_b128 a[156:159],v2 offset:8192\n"
MF "v[244:247],a[200:203],a[72:75],v[244:247]\n"
"ds_read_b128 a[196:199],v3 offset:8192\n"
MF "v[252:255],a[200:203],a[76:79],v[252:255]\n"
LD "v12,s[28:31],s82 offen offset:2048 lds\n"
MF "v[252:255],a[204:207],a[116:119],v[252:255]\n"
"s_waitcnt lgkmcnt(8)\n"
LD "v13,s[28:31],s82 offen offset:3072 lds\n"
MF "v[220:223],a[208:211],a[140:143],v[220:223]\n"
TR "a[200:201],v1 offset:16384\n"
MF "v[220:223],a[212:215],a[180:183],v[220:223]\n"
TR "a[202:203],v1 offset:16896\n"
MF "v[228:231],a[212:215],a[184:187],v[228:231]\n"
MF "v[228:231],a[208:211],a[144:147],v[228:231]\n"
TR "a[204:205],v1 offset:20480\n"
MF "v[236:239],a[208:211],a[148:151],v[236:239]\n"
TR "a[206:207],v1 offset:20992\n"
MF "v[236:239],a[212:215],a[188:191],v[236:239]\n"
"s_waitcnt lgkmcnt(4)\n"
MF "v[244:247],a[212:215],a[192:195],v[244:247]\n"
MF "v[244:247],a[208:211],a[152:155],v[244:247]\n"
MF "v[252:255],a[208:211],a[156:159],v[252:255]\n"
MF "v[252:255],a[212:215],a[196:199],v[252:255]\n"
"s_waitcnt lgkmcnt(0)\n"
"s_cmp_eq_u32 s69,98\n"
"s_cbranch_scc1 .Lqy\n"
".Lqf:\n"
MF "v[216:219],a[216:219],a[60:63],v[216:219]\n"
TR "a[208:209],v0 offset:8\n"
MF "v[216:219],a[220:223],a[100:103],v[216:219]\n"
TR "a[210:211],v0 offset:520\n"
MF "v[224:227],a[220:223],a[104:107],v[224:227]\n"
"buffer_load_dword a224,v15,s[36:39],s70 offen offset:2688 nt\n"
MF "v[224:227],a[216:219],a[64:67],v[224:227]\n"
TR "a[212:213],v0 offset:4104\n"
MF "v[232:235],a[216:219],a[68:71],v[232:235]\n"
TR "a[214:215],v0 offset:4616\n"
MF "v[232:235],a[220:223],a[108:111],v[232:235]\n"
"buffer_load_dword a225,v15,s[36:39],s71 offen offset:2688 nt\n"
MF "v[240:243],a[220:223],a[112:115],v[240:243]\n"
MF "v[240:243],a[216:219],a[72:75],v[240:243]\n"
"buffer_load_dword a226,v15,s[48:51],s7 offen offset:2688\n"
MF "v[248:251],a[216:219],a[76:79],v[248:251]\n"
MF "v[248:251],a[220:223],a[116:119],v[248:251]\n"
MF "v[248:251],a[200:203],a[156:159],v[248:251]\n"
TR "a[216:217],v0 offset:16392\n"
MF "v[248:251],a[204:207],a[196:199],v[248:251]\n"
TR "a[218:219],v0 offset:16904\n"
MF "v[240:243],a[204:207],a[192:195],v[240:243]\n"
MF "v[240:243],a[200:203],a[152:155],v[240:243]\n"
TR "a[220:221],v0 offset:20488\n"
MF "v[232:235],a[200:203],a[148:151],v[232:235]\n"
TR "a[222:223],v0 offset:21000\n"
MF "v[232:235],a[204:207],a[188:191],v[232:235]\n"
MF "v[224:227],a[204:207],a[184:187],v[224:227]\n"
MF "v[224:227],a[200:203],a[144:147],v[224:227]\n"
"v_fma_f32 v248,v248,s54,v14\n"
"v_fma_f32 v249,v249,s54,v14\n"
MF "v[216:219],a[200:203],a[140:143],v[216:219]\n"
"v_fma_f32 v250,v250,s54,v14\n"
"v_fma_f32 v251,v251,s54,v14\n"
MF "v[216:219],a[204:207],a[180:183],v[216:219]\n"
"s_waitcnt lgkmcnt(4)\n"
"v_fma_f32 v252,v252,s54,v14\n"
"v_fma_f32 v253,v253,s54,v14\n"
MF "v[180:183],a[208:211],a[60:63],v[180:183]\n"
TR "a[200:201],v0\n"
"v_fma_f32 v254,v254,s54,v14\n"
MF "v[180:183],a[212:215],a[100:103],v[180:183]\n"
TR "a[202:203],v0 offset:512\n"
"v_fma_f32 v255,v255,s54,v14\n"
MF "v[188:191],a[212:215],a[104:107],v[188:191]\n"
CV "v255,v254,v255\n"
CV "v254,v252,v253\n"
MF "v[188:191],a[208:211],a[64:67],v[188:191]\n"
TR "a[204:205],v0 offset:4096\n"
CV "v253,v250,v251\n"
MF "v[196:199],a[208:211],a[68:71],v[196:199]\n"
TR "a[206:207],v0 offset:4608\n"
CV "v252,v248,v249\n"
MF "v[196:199],a[212:215],a[108:111],v[196:199]\n"
"v_fma_f32 v240,v240,s54,v14\n"
"v_fma_f32 v241,v241,s54,v14\n"
MF "v[204:207],a[212:215],a[112:115],v[204:207]\n"
"v_fma_f32 v242,v242,s54,v14\n"
"v_fma_f32 v243,v243,s54,v14\n"
MF "v[204:207],a[208:211],a[72:75],v[204:207]\n"
"v_fma_f32 v244,v244,s54,v14\n"
"v_fma_f32 v245,v245,s54,v14\n"
MF "v[212:215],a[208:211],a[76:79],v[212:215]\n"
"v_fma_f32 v246,v246,s54,v14\n"
"v_fma_f32 v247,v247,s54,v14\n"
MF "v[212:215],a[212:215],a[116:119],v[212:215]\n"
"s_waitcnt lgkmcnt(4)\n"
CV "v251,v246,v247\n"
CV "v250,v244,v245\n"
MF "v[180:183],a[216:219],a[140:143],v[180:183]\n"
TR "a[208:209],v0 offset:16384\n"
CV "v249,v242,v243\n"
MF "v[180:183],a[220:223],a[180:183],v[180:183]\n"
TR "a[210:211],v0 offset:16896\n"
CV "v248,v240,v241\n"
MF "v[188:191],a[220:223],a[184:187],v[188:191]\n"
"v_fma_f32 v232,v232,s54,v14\n"
"v_fma_f32 v233,v233,s54,v14\n"
MF "v[188:191],a[216:219],a[144:147],v[188:191]\n"
TR "a[212:213],v0 offset:20480\n"
"v_fma_f32 v234,v234,s54,v14\n"
MF "v[196:199],a[216:219],a[148:151],v[196:199]\n"
TR "a[214:215],v0 offset:20992\n"
"v_fma_f32 v235,v235,s54,v14\n"
MF "v[196:199],a[220:223],a[188:191],v[196:199]\n"
"v_fma_f32 v236,v236,s54,v14\n"
"v_fma_f32 v237,v237,s54,v14\n"
MF "v[204:207],a[220:223],a[192:195],v[204:207]\n"
"v_fma_f32 v238,v238,s54,v14\n"
"v_fma_f32 v239,v239,s54,v14\n"
MF "v[204:207],a[216:219],a[152:155],v[204:207]\n"
CV "v247,v238,v239\n"
CV "v246,v236,v237\n"
MF "v[212:215],a[216:219],a[156:159],v[212:215]\n"
CV "v245,v234,v235\n"
CV "v244,v232,v233\n"
MF "v[212:215],a[220:223],a[196:199],v[212:215]\n"
"s_waitcnt vmcnt(11) lgkmcnt(0)\n"
"s_barrier\n"
MF "v[176:179],a[200:203],a[60:63],v[176:179]\n"
TR "a[216:217],v1 offset:32776\n"
"v_fma_f32 v224,v224,s54,v14\n"
MF "v[176:179],a[204:207],a[100:103],v[176:179]\n"
"s_add_u32 m0,s64,0\n"
TR "a[218:219],v1 offset:33288\n"
"v_fma_f32 v225,v225,s54,v14\n"
MF "v[184:187],a[204:207],a[104:107],v[184:187]\n"
LD "v7,s[32:35],s72 offen nt lds\n"
MF "v[184:187],a[200:203],a[64:67],v[184:187]\n"
TR "a[220:221],v1 offset:36872\n"
"v_fma_f32 v226,v226,s54,v14\n"
MF "v[192:195],a[200:203],a[68:71],v[192:195]\n"
TR "a[222:223],v1 offset:37384\n"
"v_fma_f32 v227,v227,s54,v14\n"
MF "v[192:195],a[204:207],a[108:111],v[192:195]\n"
"s_add_u32 m0,s64,896\n"
"v_fma_f32 v228,v228,s54,v14\n"
"v_fma_f32 v229,v229,s54,v14\n"
MF "v[200:203],a[204:207],a[112:115],v[200:203]\n"
LD "v7,s[32:35],s72 offen offset:128 nt lds\n"
MF "v[200:203],a[200:203],a[72:75],v[200:203]\n"
"v_fma_f32 v230,v230,s54,v14\n"
"v_fma_f32 v231,v231,s54,v14\n"
MF "v[208:211],a[200:203],a[76:79],v[208:211]\n"
CV "v243,v230,v231\n"
CV "v242,v228,v229\n"
MF "v[208:211],a[204:207],a[116:119],v[208:211]\n"
CV "v241,v226,v227\n"
CV "v240,v224,v225\n"
MF "v[208:211],a[208:211],a[156:159],v[208:211]\n"
TR "a[200:201],v1 offset:49160\n"
"v_fma_f32 v216,v216,s54,v14\n"
MF "v[208:211],a[212:215],a[196:199],v[208:211]\n"
"s_add_u32 m0,s64,1792\n"
TR "a[202:203],v1 offset:49672\n"
"v_fma_f32 v217,v217,s54,v14\n"
MF "v[200:203],a[212:215],a[192:195],v[200:203]\n"
LD "v7,s[32:35],s72 offen offset:256 nt lds\n"
MF "v[200:203],a[208:211],a[152:155],v[200:203]\n"
TR "a[204:205],v1 offset:53256\n"
"v_fma_f32 v218,v218,s54,v14\n"
MF "v[192:195],a[208:211],a[148:151],v[192:195]\n"
TR "a[206:207],v1 offset:53768\n"
"v_fma_f32 v219,v219,s54,v14\n"
MF "v[192:195],a[212:215],a[188:191],v[192:195]\n"
"v_fma_f32 v220,v220,s54,v14\n"
"v_fma_f32 v221,v221,s54,v14\n"
MF "v[184:187],a[212:215],a[184:187],v[184:187]\n"
"s_add_u32 s72,s72,6144\n"
"v_fma_f32 v222,v222,s54,v14\n"
"v_fma_f32 v223,v223,s54,v14\n"
MF "v[184:187],a[208:211],a[144:147],v[184:187]\n"
"s_add_u32 m0,s64,6144\n"
CV "v239,v222,v223\n"
CV "v238,v220,v221\n"
MF "v[176:179],a[208:211],a[140:143],v[176:179]\n"
LD "v7,s[32:35],s72 offen nt lds\n"
MF "v[176:179],a[212:215],a[180:183],v[176:179]\n"
"s_waitcnt lgkmcnt(4)\n"
CV "v237,v218,v219\n"
CV "v236,v216,v217\n"
MF "v[140:143],a[216:219],a[60:63],v[140:143]\n"
TR "a[208:209],v1 offset:32768\n"
"v_fma_f32 v208,v208,s54,v14\n"
MF "v[140:143],a[220:223],a[100:103],v[140:143]\n"
TR "a[210:211],v1 offset:33280\n"
"v_fma_f32 v209,v209,s54,v14\n"
MF "v[148:151],a[220:223],a[104:107],v[148:151]\n"
"v_fma_f32 v210,v210,s54,v14\n"
"v_fma_f32 v211,v211,s54,v14\n"
MF "v[148:151],a[216:219],a[64:67],v[148:151]\n"
TR "a[212:213],v1 offset:36864\n"
"v_fma_f32 v212,v212,s54,v14\n"
MF "v[156:159],a[216:219],a[68:71],v[156:159]\n"
"s_add_u32 m0,s64,7040\n"
TR "a[214:215],v1 offset:37376\n"
"v_fma_f32 v213,v213,s54,v14\n"
MF "v[156:159],a[220:223],a[108:111],v[156:159]\n"
LD "v7,s[32:35],s72 offen offset:128 nt lds\n"
MF "v[164:167],a[220:223],a[112:115],v[164:167]\n"
"v_fma_f32 v214,v214,s54,v14\n"
"v_fma_f32 v215,v215,s54,v14\n"
MF "v[164:167],a[216:219],a[72:75],v[164:167]\n"
CV "v235,v214,v215\n"
CV "v234,v212,v213\n"
MF "v[172:175],a[216:219],a[76:79],v[172:175]\n"
"s_add_u32 m0,s64,7936\n"
CV "v233,v210,v211\n"
CV "v232,v208,v209\n"
MF "v[172:175],a[220:223],a[116:119],v[172:175]\n"
"s_waitcnt lgkmcnt(4)\n"
LD "v7,s[32:35],s72 offen offset:256 nt lds\n"
MF "v[140:143],a[200:203],a[140:143],v[140:143]\n"
TR "a[216:217],v1 offset:49152\n"
"v_fma_f32 v200,v200,s54,v14\n"
MF "v[140:143],a[204:207],a[180:183],v[140:143]\n"
TR "a[218:219],v1 offset:49664\n"
"v_fma_f32 v201,v201,s54,v14\n"
MF "v[148:151],a[204:207],a[184:187],v[148:151]\n"
"v_fma_f32 v202,v202,s54,v14\n"
"v_fma_f32 v203,v203,s54,v14\n"
MF "v[148:151],a[200:203],a[144:147],v[148:151]\n"
TR "a[220:221],v1 offset:53248\n"
"v_fma_f32 v204,v204,s54,v14\n"
MF "v[156:159],a[200:203],a[148:151],v[156:159]\n"
TR "a[222:223],v1 offset:53760\n"
"v_fma_f32 v205,v205,s54,v14\n"
MF "v[156:159],a[204:207],a[188:191],v[156:159]\n"
"v_fma_f32 v206,v206,s54,v14\n"
"v_fma_f32 v207,v207,s54,v14\n"
MF "v[164:167],a[204:207],a[192:195],v[164:167]\n"
CV "v231,v206,v207\n"
CV "v230,v204,v205\n"
MF "v[164:167],a[200:203],a[152:155],v[164:167]\n"
CV "v229,v202,v203\n"
CV "v228,v200,v201\n"
MF "v[172:175],a[200:203],a[156:159],v[172:175]\n"
"v_fma_f32 v192,v192,s54,v14\n"
"v_fma_f32 v193,v193,s54,v14\n"
MF "v[172:175],a[204:207],a[196:199],v[172:175]\n"
"s_waitcnt lgkmcnt(0)\n"
"v_fma_f32 v194,v194,s54,v14\n"
"v_fma_f32 v195,v195,s54,v14\n"
MF "v[136:139],a[208:211],a[60:63],v[136:139]\n"
TR "a[200:201],v0 offset:32776\n"
"v_fma_f32 v196,v196,s54,v14\n"
MF "v[136:139],a[212:215],a[100:103],v[136:139]\n"
TR "a[202:203],v0 offset:33288\n"
"v_fma_f32 v197,v197,s54,v14\n"
MF "v[144:147],a[212:215],a[104:107],v[144:147]\n"
"v_fma_f32 v198,v198,s54,v14\n"
"v_fma_f32 v199,v199,s54,v14\n"
MF "v[144:147],a[208:211],a[64:67],v[144:147]\n"
TR "a[204:205],v0 offset:36872\n"
CV "v227,v198,v199\n"
MF "v[152:155],a[208:211],a[68:71],v[152:155]\n"
TR "a[206:207],v0 offset:37384\n"
CV "v226,v196,v197\n"
MF "v[152:155],a[212:215],a[108:111],v[152:155]\n"
CV "v225,v194,v195\n"
CV "v224,v192,v193\n"
MF "v[160:163],a[212:215],a[112:115],v[160:163]\n"
"v_fma_f32 v184,v184,s54,v14\n"
"v_fma_f32 v185,v185,s54,v14\n"
MF "v[160:163],a[208:211],a[72:75],v[160:163]\n"
"v_fma_f32 v186,v186,s54,v14\n"
"v_fma_f32 v187,v187,s54,v14\n"
MF "v[168:171],a[208:211],a[76:79],v[168:171]\n"
"v_fma_f32 v188,v188,s54,v14\n"
"v_fma_f32 v189,v189,s54,v14\n"
MF "v[168:171],a[212:215],a[116:119],v[168:171]\n"
"v_fma_f32 v190,v190,s54,v14\n"
"v_fma_f32 v191,v191,s54,v14\n"
MF "v[168:171],a[216:219],a[156:159],v[168:171]\n"
TR "a[208:209],v0 offset:49160\n"
CV "v223,v190,v191\n"
MF "v[168:171],a[220:223],a[196:199],v[168:171]\n"
TR "a[210:211],v0 offset:49672\n"
CV "v222,v188,v189\n"
MF "v[160:163],a[220:223],a[192:195],v[160:163]\n"
CV "v221,v186,v187\n"
CV "v220,v184,v185\n"
MF "v[160:163],a[216:219],a[152:155],v[160:163]\n"
TR "a[212:213],v0 offset:53256\n"
"v_fma_f32 v176,v176,s54,v14\n"
MF "v[152:155],a[216:219],a[148:151],v[152:155]\n"
TR "a[214:215],v0 offset:53768\n"
"v_fma_f32 v177,v177,s54,v14\n"
MF "v[152:155],a[220:223],a[188:191],v[152:155]\n"
"v_fma_f32 v178,v178,s54,v14\n"
"v_fma_f32 v179,v179,s54,v14\n"
MF "v[144:147],a[220:223],a[184:187],v[144:147]\n"
"v_fma_f32 v180,v180,s54,v14\n"
"v_fma_f32 v181,v181,s54,v14\n"
MF "v[144:147],a[216:219],a[144:147],v[144:147]\n"
"v_fma_f32 v182,v182,s54,v14\n"
"v_fma_f32 v183,v183,s54,v14\n"
MF "v[136:139],a[216:219],a[140:143],v[136:139]\n"
CV "v219,v182,v183\n"
CV "v218,v180,v181\n"
MF "v[136:139],a[220:223],a[180:183],v[136:139]\n"
"s_waitcnt lgkmcnt(4)\n"
CV "v217,v178,v179\n"
CV "v216,v176,v177\n"
MF "v[100:103],a[200:203],a[60:63],v[100:103]\n"
TR "a[216:217],v0 offset:32768\n"
"v_fma_f32 v168,v168,s54,v14\n"
MF "v[100:103],a[204:207],a[100:103],v[100:103]\n"
TR "a[218:219],v0 offset:33280\n"
"v_fma_f32 v169,v169,s54,v14\n"
MF "v[108:111],a[204:207],a[104:107],v[108:111]\n"
"v_fma_f32 v170,v170,s54,v14\n"
"v_fma_f32 v171,v171,s54,v14\n"
MF "v[108:111],a[200:203],a[64:67],v[108:111]\n"
TR "a[220:221],v0 offset:36864\n"
"v_fma_f32 v172,v172,s54,v14\n"
MF "v[116:119],a[200:203],a[68:71],v[116:119]\n"
TR "a[222:223],v0 offset:37376\n"
"v_fma_f32 v173,v173,s54,v14\n"
MF "v[116:119],a[204:207],a[108:111],v[116:119]\n"
"v_fma_f32 v174,v174,s54,v14\n"
"v_fma_f32 v175,v175,s54,v14\n"
MF "v[124:127],a[204:207],a[112:115],v[124:127]\n"
CV "v215,v174,v175\n"
CV "v214,v172,v173\n"
MF "v[124:127],a[200:203],a[72:75],v[124:127]\n"
CV "v213,v170,v171\n"
CV "v212,v168,v169\n"
MF "v[132:135],a[200:203],a[76:79],v[132:135]\n"
".Lpg8b:\n"
"s_cbranch_execz .Lpg11b\n"
".Lpg8d:\n"
"s_cbranch_execz .Lpgpsl_d\n"
"v_fma_f32 v160,v160,s54,v14\n"
"v_fma_f32 v161,v161,s54,v14\n"
MF "v[132:135],a[204:207],a[116:119],v[132:135]\n"
"s_waitcnt lgkmcnt(4)\n"
"v_fma_f32 v162,v162,s54,v14\n"
"v_fma_f32 v163,v163,s54,v14\n"
MF "v[100:103],a[208:211],a[140:143],v[100:103]\n"
TR "a[200:201],v0 offset:49152\n"
"v_fma_f32 v164,v164,s54,v14\n"
MF "v[100:103],a[212:215],a[180:183],v[100:103]\n"
TR "a[202:203],v0 offset:49664\n"
"v_fma_f32 v165,v165,s54,v14\n"
MF "v[108:111],a[212:215],a[184:187],v[108:111]\n"
"v_fma_f32 v166,v166,s54,v14\n"
"v_fma_f32 v167,v167,s54,v14\n"
MF "v[108:111],a[208:211],a[144:147],v[108:111]\n"
TR "a[204:205],v0 offset:53248\n"
CV "v211,v166,v167\n"
MF "v[116:119],a[208:211],a[148:151],v[116:119]\n"
TR "a[206:207],v0 offset:53760\n"
CV "v210,v164,v165\n"
MF "v[116:119],a[212:215],a[188:191],v[116:119]\n"
CV "v209,v162,v163\n"
CV "v208,v160,v161\n"
MF "v[124:127],a[212:215],a[192:195],v[124:127]\n"
"v_add_u32 v0,0xc000,v0\n"
"v_fma_f32 v152,v152,s54,v14\n"
MF "v[124:127],a[208:211],a[152:155],v[124:127]\n"
"v_add_u32 v1,0xc000,v1\n"
"v_fma_f32 v153,v153,s54,v14\n"
MF "v[132:135],a[208:211],a[156:159],v[132:135]\n"
"v_fma_f32 v154,v154,s54,v14\n"
"v_fma_f32 v155,v155,s54,v14\n"
MF "v[132:135],a[212:215],a[196:199],v[132:135]\n"
"s_waitcnt vmcnt(9) lgkmcnt(0)\n"
"s_barrier\n"
MF "v[96:99],a[216:219],a[60:63],v[96:99]\n"
TR "a[208:209],v1 offset:16392\n"
"v_fma_f32 v156,v156,s54,v14\n"
MF "v[96:99],a[220:223],a[100:103],v[96:99]\n"
TR "a[210:211],v1 offset:16904\n"
"v_fma_f32 v157,v157,s54,v14\n"
MF "v[104:107],a[220:223],a[104:107],v[104:107]\n"
"s_add_u32 s72,s72,6144\n"
"v_fma_f32 v158,v158,s54,v14\n"
"v_fma_f32 v159,v159,s54,v14\n"
MF "v[104:107],a[216:219],a[64:67],v[104:107]\n"
"s_cmp_eq_u32 s72,s17\n"
"s_cselect_b32 s72,s16,s72\n"
TR "a[212:213],v1 offset:20488\n"
MF "v[112:115],a[216:219],a[68:71],v[112:115]\n"
"s_add_u32 m0,s64,24576\n"
TR "a[214:215],v1 offset:21000\n"
CV "v207,v158,v159\n"
MF "v[112:115],a[220:223],a[108:111],v[112:115]\n"
LD "v7,s[32:35],s72 offen nt lds\n"
MF "v[120:123],a[220:223],a[112:115],v[120:123]\n"
"s_add_u32 m0,s64,25472\n"
CV "v206,v156,v157\n"
CV "v205,v154,v155\n"
MF "v[120:123],a[216:219],a[72:75],v[120:123]\n"
LD "v7,s[32:35],s72 offen offset:128 nt lds\n"
MF "v[128:131],a[216:219],a[76:79],v[128:131]\n"
CV "v204,v152,v153\n"
"v_fma_f32 v144,v144,s54,v14\n"
MF "v[128:131],a[220:223],a[116:119],v[128:131]\n"
"v_fma_f32 v145,v145,s54,v14\n"
"v_fma_f32 v146,v146,s54,v14\n"
MF "v[128:131],a[200:203],a[156:159],v[128:131]\n"
TR "a[216:217],v1 offset:32776\n"
"v_fma_f32 v147,v147,s54,v14\n"
MF "v[128:131],a[204:207],a[196:199],v[128:131]\n"
"s_add_u32 m0,s64,26368\n"
TR "a[218:219],v1 offset:33288\n"
"v_fma_f32 v148,v148,s54,v14\n"
MF "v[120:123],a[204:207],a[192:195],v[120:123]\n"
LD "v7,s[32:35],s72 offen offset:256 nt lds\n"
MF "v[120:123],a[200:203],a[152:155],v[120:123]\n"
TR "a[220:221],v1 offset:36872\n"
"v_fma_f32 v149,v149,s54,v14\n"
MF "v[112:115],a[200:203],a[148:151],v[112:115]\n"
TR "a[222:223],v1 offset:37384\n"
"v_fma_f32 v150,v150,s54,v14\n"
MF "v[112:115],a[204:207],a[188:191],v[112:115]\n"
"v_fma_f32 v151,v151,s54,v14\n"
CV "v203,v150,v151\n"
MF "v[104:107],a[204:207],a[184:187],v[104:107]\n"
"s_add_u32 s72,s72,6144\n"
CV "v202,v148,v149\n"
CV "v201,v146,v147\n"
MF "v[104:107],a[200:203],a[144:147],v[104:107]\n"
"s_add_u32 m0,s64,30720\n"
CV "v200,v144,v145\n"
"v_fma_f32 v136,v136,s54,v14\n"
MF "v[96:99],a[200:203],a[140:143],v[96:99]\n"
LD "v7,s[32:35],s72 offen nt lds\n"
MF "v[96:99],a[204:207],a[180:183],v[96:99]\n"
"s_waitcnt lgkmcnt(4)\n"
"v_fma_f32 v137,v137,s54,v14\n"
"v_fma_f32 v138,v138,s54,v14\n"
MF "v[60:63],a[208:211],a[60:63],v[60:63]\n"
TR "a[200:201],v1 offset:16384\n"
"v_fma_f32 v139,v139,s54,v14\n"
MF "v[60:63],a[212:215],a[100:103],v[60:63]\n"
TR "a[202:203],v1 offset:16896\n"
"v_fma_f32 v140,v140,s54,v14\n"
MF "v[68:71],a[212:215],a[104:107],v[68:71]\n"
"v_fma_f32 v141,v141,s54,v14\n"
"v_fma_f32 v142,v142,s54,v14\n"
MF "v[68:71],a[208:211],a[64:67],v[68:71]\n"
TR "a[204:205],v1 offset:20480\n"
"v_fma_f32 v143,v143,s54,v14\n"
MF "v[76:79],a[208:211],a[68:71],v[76:79]\n"
"s_add_u32 m0,s64,31616\n"
TR "a[206:207],v1 offset:20992\n"
CV "v199,v142,v143\n"
MF "v[76:79],a[212:215],a[108:111],v[76:79]\n"
LD "v7,s[32:35],s72 offen offset:128 nt lds\n"
MF "v[84:87],a[212:215],a[112:115],v[84:87]\n"
CV "v198,v140,v141\n"
CV "v197,v138,v139\n"
MF "v[84:87],a[208:211],a[72:75],v[84:87]\n"
CV "v196,v136,v137\n"
"v_fma_f32 v128,v128,s54,v14\n"
MF "v[92:95],a[208:211],a[76:79],v[92:95]\n"
"s_add_u32 m0,s64,32512\n"
"v_fma_f32 v129,v129,s54,v14\n"
"v_fma_f32 v130,v130,s54,v14\n"
MF "v[92:95],a[212:215],a[116:119],v[92:95]\n"
"s_waitcnt lgkmcnt(4)\n"
LD "v7,s[32:35],s72 offen offset:256 nt lds\n"
MF "v[60:63],a[216:219],a[140:143],v[60:63]\n"
TR "a[208:209],v1 offset:32768\n"
"v_fma_f32 v131,v131,s54,v14\n"
MF "v[60:63],a[220:223],a[180:183],v[60:63]\n"
TR "a[210:211],v1 offset:33280\n"
"v_fma_f32 v132,v132,s54,v14\n"
MF "v[68:71],a[220:223],a[184:187],v[68:71]\n"
"v_fma_f32 v133,v133,s54,v14\n"
"v_fma_f32 v134,v134,s54,v14\n"
MF "v[68:71],a[216:219],a[144:147],v[68:71]\n"
TR "a[212:213],v1 offset:36864\n"
"v_fma_f32 v135,v135,s54,v14\n"
MF "v[76:79],a[216:219],a[148:151],v[76:79]\n"
TR "a[214:215],v1 offset:37376\n"
CV "v195,v134,v135\n"
MF "v[76:79],a[220:223],a[188:191],v[76:79]\n"
CV "v194,v132,v133\n"
CV "v193,v130,v131\n"
MF "v[84:87],a[220:223],a[192:195],v[84:87]\n"
CV "v192,v128,v129\n"
"v_fma_f32 v120,v120,s54,v14\n"
MF "v[84:87],a[216:219],a[152:155],v[84:87]\n"
"v_fma_f32 v121,v121,s54,v14\n"
"v_fma_f32 v122,v122,s54,v14\n"
MF "v[92:95],a[216:219],a[156:159],v[92:95]\n"
"v_fma_f32 v123,v123,s54,v14\n"
"v_fma_f32 v124,v124,s54,v14\n"
MF "v[92:95],a[220:223],a[196:199],v[92:95]\n"
"s_waitcnt lgkmcnt(0)\n"
"v_fma_f32 v125,v125,s54,v14\n"
"v_fma_f32 v126,v126,s54,v14\n"
MF "v[56:59],a[200:203],a[60:63],v[56:59]\n"
TR "a[216:217],v0 offset:16392\n"
"v_fma_f32 v127,v127,s54,v14\n"
MF "v[56:59],a[204:207],a[100:103],v[56:59]\n"
TR "a[218:219],v0 offset:16904\n"
CV "v191,v126,v127\n"
MF "v[64:67],a[204:207],a[104:107],v[64:67]\n"
CV "v190,v124,v125\n"
CV "v189,v122,v123\n"
MF "v[64:67],a[200:203],a[64:67],v[64:67]\n"
TR "a[220:221],v0 offset:20488\n"
CV "v188,v120,v121\n"
MF "v[72:75],a[200:203],a[68:71],v[72:75]\n"
TR "a[222:223],v0 offset:21000\n"
"v_fma_f32 v112,v112,s54,v14\n"
MF "v[72:75],a[204:207],a[108:111],v[72:75]\n"
"v_fma_f32 v113,v113,s54,v14\n"
"v_fma_f32 v114,v114,s54,v14\n"
MF "v[80:83],a[204:207],a[112:115],v[80:83]\n"
"v_fma_f32 v115,v115,s54,v14\n"
"v_fma_f32 v116,v116,s54,v14\n"
MF "v[80:83],a[200:203],a[72:75],v[80:83]\n"
"v_fma_f32 v117,v117,s54,v14\n"
"v_fma_f32 v118,v118,s54,v14\n"
MF "v[88:91],a[200:203],a[76:79],v[88:91]\n"
"v_fma_f32 v119,v119,s54,v14\n"
CV "v187,v118,v119\n"
MF "v[88:91],a[204:207],a[116:119],v[88:91]\n"
CV "v186,v116,v117\n"
CV "v185,v114,v115\n"
MF "v[88:91],a[208:211],a[156:159],v[88:91]\n"
TR "a[200:201],v0 offset:32776\n"
CV "v184,v112,v113\n"
MF "v[88:91],a[212:215],a[196:199],v[88:91]\n"
TR "a[202:203],v0 offset:33288\n"
"v_fma_f32 v104,v104,s54,v14\n"
MF "v[80:83],a[212:215],a[192:195],v[80:83]\n"
"v_fma_f32 v105,v105,s54,v14\n"
"v_fma_f32 v106,v106,s54,v14\n"
MF "v[80:83],a[208:211],a[152:155],v[80:83]\n"
TR "a[204:205],v0 offset:36872\n"
"v_fma_f32 v107,v107,s54,v14\n"
MF "v[72:75],a[208:211],a[148:151],v[72:75]\n"
TR "a[206:207],v0 offset:37384\n"
"v_fma_f32 v108,v108,s54,v14\n"
MF "v[72:75],a[212:215],a[188:191],v[72:75]\n"
"v_fma_f32 v109,v109,s54,v14\n"
"v_fma_f32 v110,v110,s54,v14\n"
MF "v[64:67],a[212:215],a[184:187],v[64:67]\n"
"v_fma_f32 v111,v111,s54,v14\n"
CV "v183,v110,v111\n"
MF "v[64:67],a[208:211],a[144:147],v[64:67]\n"
CV "v182,v108,v109\n"
CV "v181,v106,v107\n"
MF "v[56:59],a[208:211],a[140:143],v[56:59]\n"
CV "v180,v104,v105\n"
"v_fma_f32 v96,v96,s54,v14\n"
MF "v[56:59],a[212:215],a[180:183],v[56:59]\n"
"s_waitcnt lgkmcnt(4)\n"
"v_fma_f32 v97,v97,s54,v14\n"
"v_fma_f32 v98,v98,s54,v14\n"
MF "v[20:23],a[216:219],a[60:63],v[20:23]\n"
TR "a[208:209],v0 offset:16384\n"
"v_fma_f32 v99,v99,s54,v14\n"
MF "v[20:23],a[220:223],a[100:103],v[20:23]\n"
TR "a[210:211],v0 offset:16896\n"
"v_fma_f32 v100,v100,s54,v14\n"
MF "v[28:31],a[220:223],a[104:107],v[28:31]\n"
"v_fma_f32 v101,v101,s54,v14\n"
"v_fma_f32 v102,v102,s54,v14\n"
MF "v[28:31],a[216:219],a[64:67],v[28:31]\n"
TR "a[212:213],v0 offset:20480\n"
"v_fma_f32 v103,v103,s54,v14\n"
MF "v[36:39],a[216:219],a[68:71],v[36:39]\n"
TR "a[214:215],v0 offset:20992\n"
CV "v179,v102,v103\n"
MF "v[36:39],a[220:223],a[108:111],v[36:39]\n"
CV "v178,v100,v101\n"
CV "v177,v98,v99\n"
MF "v[44:47],a[220:223],a[112:115],v[44:47]\n"
CV "v176,v96,v97\n"
"v_fma_f32 v88,v88,s54,v14\n"
MF "v[44:47],a[216:219],a[72:75],v[44:47]\n"
"v_fma_f32 v89,v89,s54,v14\n"
"v_fma_f32 v90,v90,s54,v14\n"
MF "v[52:55],a[216:219],a[76:79],v[52:55]\n"
"v_fma_f32 v91,v91,s54,v14\n"
"v_fma_f32 v92,v92,s54,v14\n"
MF "v[52:55],a[220:223],a[116:119],v[52:55]\n"
"s_waitcnt lgkmcnt(4)\n"
"v_fma_f32 v93,v93,s54,v14\n"
"v_fma_f32 v94,v94,s54,v14\n"
MF "v[20:23],a[200:203],a[140:143],v[20:23]\n"
TR "a[216:217],v0 offset:32768\n"
"v_fma_f32 v95,v95,s54,v14\n"
MF "v[20:23],a[204:207],a[180:183],v[20:23]\n"
TR "a[218:219],v0 offset:33280\n"
CV "v175,v94,v95\n"
MF "v[28:31],a[204:207],a[184:187],v[28:31]\n"
CV "v174,v92,v93\n"
CV "v173,v90,v91\n"
MF "v[28:31],a[200:203],a[144:147],v[28:31]\n"
TR "a[220:221],v0 offset:36864\n"
CV "v172,v88,v89\n"
MF "v[36:39],a[200:203],a[148:151],v[36:39]\n"
TR "a[222:223],v0 offset:37376\n"
"v_fma_f32 v80,v80,s54,v14\n"
MF "v[36:39],a[204:207],a[188:191],v[36:39]\n"
"v_fma_f32 v81,v81,s54,v14\n"
"v_fma_f32 v82,v82,s54,v14\n"
MF "v[44:47],a[204:207],a[192:195],v[44:47]\n"
"v_fma_f32 v83,v83,s54,v14\n"
"v_fma_f32 v84,v84,s54,v14\n"
MF "v[44:47],a[200:203],a[152:155],v[44:47]\n"
"v_fma_f32 v85,v85,s54,v14\n"
"v_fma_f32 v86,v86,s54,v14\n"
MF "v[52:55],a[200:203],a[156:159],v[52:55]\n"
"v_fma_f32 v87,v87,s54,v14\n"
CV "v171,v86,v87\n"
MF "v[52:55],a[204:207],a[196:199],v[52:55]\n"
"s_waitcnt lgkmcnt(0)\n"
"s_barrier\n"
MF "v[16:19],a[208:211],a[60:63],v[16:19]\n"
"s_add_u32 m0,s65,49152\n"
CV "v170,v84,v85\n"
CV "v169,v82,v83\n"
MF "v[16:19],a[212:215],a[100:103],v[16:19]\n"
LD "v8,s[36:39],s73 offen nt lds\n"
MF "v[24:27],a[212:215],a[104:107],v[24:27]\n"
"s_add_u32 m0,s65,50048\n"
CV "v168,v80,v81\n"
"v_fma_f32 v72,v72,s54,v14\n"
MF "v[24:27],a[208:211],a[64:67],v[24:27]\n"
LD "v8,s[36:39],s73 offen offset:128 nt lds\n"
MF "v[32:35],a[208:211],a[68:71],v[32:35]\n"
"v_fma_f32 v73,v73,s54,v14\n"
"v_fma_f32 v74,v74,s54,v14\n"
MF "v[32:35],a[212:215],a[108:111],v[32:35]\n"
"s_add_u32 m0,s65,50944\n"
"v_fma_f32 v75,v75,s54,v14\n"
"v_fma_f32 v76,v76,s54,v14\n"
MF "v[40:43],a[212:215],a[112:115],v[40:43]\n"
LD "v8,s[36:39],s73 offen offset:256 nt lds\n"
MF "v[40:43],a[208:211],a[72:75],v[40:43]\n"
"v_fma_f32 v77,v77,s54,v14\n"
"v_fma_f32 v78,v78,s54,v14\n"
MF "v[48:51],a[208:211],a[76:79],v[48:51]\n"
"s_add_u32 m0,s65,49152\n"
"v_fma_f32 v79,v79,s54,v14\n"
CV "v167,v78,v79\n"
MF "v[48:51],a[212:215],a[116:119],v[48:51]\n"
LD "v8,s[36:39],s73 offen offset:3072 nt lds\n"
MF "v[48:51],a[216:219],a[156:159],v[48:51]\n"
"s_add_u32 m0,s65,50048\n"
CV "v166,v76,v77\n"
CV "v165,v74,v75\n"
MF "v[48:51],a[220:223],a[196:199],v[48:51]\n"
LD "v8,s[36:39],s73 offen offset:3200 nt lds\n"
MF "v[40:43],a[220:223],a[192:195],v[40:43]\n"
CV "v164,v72,v73\n"
"v_fma_f32 v64,v64,s54,v14\n"
MF "v[40:43],a[216:219],a[152:155],v[40:43]\n"
"s_add_u32 m0,s65,50944\n"
"v_fma_f32 v65,v65,s54,v14\n"
"v_fma_f32 v66,v66,s54,v14\n"
MF "v[32:35],a[216:219],a[148:151],v[32:35]\n"
LD "v8,s[36:39],s73 offen offset:3328 nt lds\n"
MF "v[32:35],a[220:223],a[188:191],v[32:35]\n"
"v_fma_f32 v67,v67,s54,v14\n"
"v_fma_f32 v68,v68,s54,v14\n"
MF "v[24:27],a[220:223],a[184:187],v[24:27]\n"
"v_fma_f32 v69,v69,s54,v14\n"
"v_fma_f32 v70,v70,s54,v14\n"
MF "v[24:27],a[216:219],a[144:147],v[24:27]\n"
"v_fma_f32 v71,v71,s54,v14\n"
CV "v163,v70,v71\n"
MF "v[16:19],a[216:219],a[140:143],v[16:19]\n"
CV "v162,v68,v69\n"
CV "v161,v66,v67\n"
MF "v[16:19],a[220:223],a[180:183],v[16:19]\n"
CV "v160,v64,v65\n"
AR "v0,a244\n"
"v_xor_b32 v1,64,v0\n"
"s_waitcnt vmcnt(12) lgkmcnt(0)\n"
"s_barrier\n"
"ds_read_b128 v[92:95],v0\n"
"ds_read_b128 v[96:99],v1\n"
"ds_read_b128 v[100:103],v0 offset:1024\n"
"v_pk_fma_f32 v[56:57],v[56:57],s[54:55],v[14:15] op_sel_hi:[1,1,0]\n"
"v_pk_fma_f32 v[58:59],v[58:59],s[54:55],v[14:15] op_sel_hi:[1,1,0]\n"
"v_pk_fma_f32 v[60:61],v[60:61],s[54:55],v[14:15] op_sel_hi:[1,1,0]\n"
"v_pk_fma_f32 v[62:63],v[62:63],s[54:55],v[14:15] op_sel_hi:[1,1,0]\n"
CV "v159,v62,v63\n"
CV "v158,v60,v61\n"
CV "v157,v58,v59\n"
CV "v156,v56,v57\n"
"v_pk_mul_f32 v[48:49],v[48:49],s[54:55]\n"
"v_pk_mul_f32 v[50:51],v[50:51],s[54:55]\n"
"v_pk_mul_f32 v[52:53],v[52:53],s[54:55]\n"
"v_pk_mul_f32 v[54:55],v[54:55],s[54:55]\n"
CV "v155,v54,v55\n"
CV "v154,v52,v53\n"
CV "v153,v50,v51\n"
CV "v152,v48,v49\n"
"v_pk_mul_f32 v[40:41],v[40:41],s[54:55]\n"
"v_pk_mul_f32 v[42:43],v[42:43],s[54:55]\n"
"v_pk_mul_f32 v[44:45],v[44:45],s[54:55]\n"
"v_pk_mul_f32 v[46:47],v[46:47],s[54:55]\n"
CV "v151,v46,v47\n"
CV "v150,v44,v45\n"
CV "v149,v42,v43\n"
CV "v148,v40,v41\n"
"v_pk_mul_f32 v[32:33],v[32:33],s[54:55]\n"
"v_pk_mul_f32 v[34:35],v[34:35],s[54:55]\n"
"v_pk_mul_f32 v[36:37],v[36:37],s[54:55]\n"
"v_pk_mul_f32 v[38:39],v[38:39],s[54:55]\n"
CV "v147,v38,v39\n"
CV "v146,v36,v37\n"
CV "v145,v34,v35\n"
CV "v144,v32,v33\n"
"v_pk_mul_f32 v[24:25],v[24:25],s[54:55]\n"
"v_pk_mul_f32 v[26:27],v[26:27],s[54:55]\n"
"v_pk_mul_f32 v[28:29],v[28:29],s[54:55]\n"
"v_pk_mul_f32 v[30:31],v[30:31],s[54:55]\n"
CV "v143,v30,v31\n"
CV "v142,v28,v29\n"
CV "v141,v26,v27\n"
CV "v140,v24,v25\n"
"v_pk_mul_f32 v[16:17],v[16:17],s[54:55]\n"
"v_pk_mul_f32 v[18:19],v[18:19],s[54:55]\n"
"v_pk_mul_f32 v[20:21],v[20:21],s[54:55]\n"
"v_pk_mul_f32 v[22:23],v[22:23],s[54:55]\n"
CV "v139,v22,v23\n"
CV "v138,v20,v21\n"
CV "v137,v18,v19\n"
CV "v136,v16,v17\n"
"s_cmp_eq_u32 s57,s69\n"
"s_cbranch_scc1 .LattB\n"
AR "v2,a245\n"
"v_add_u32 v2,49152,v2\n"
"v_xor_b32 v3,32,v2\n"
"v_xor_b32 v4,64,v2\n"
"v_xor_b32 v5,96,v2\n"
"s_mov_b32 s61,7\n"
".Latt2x:\n"
"s_cmp_eq_u32 s57,1\n"
"s_cbranch_scc1 .Lt0kx\n"
".Lt0nx:\n"
"s_waitcnt lgkmcnt(2)\n"
MF "v[72:75],v[92:95],v[136:139],0\n"
"s_add_u32 s73,s73,12288\n"
"ds_read_b128 v[104:107],v1 offset:1024\n"
AR "v10,a224\n"
MF "v[76:79],v[92:95],v[140:143],0\n"
"s_cmp_eq_u32 s73,s19\n"
"s_cselect_b32 s73,s18,s73\n"
"s_add_u32 m0,s65,73728\n"
MF "v[80:83],v[92:95],v[144:147],0\n"
LD "v8,s[36:39],s73 offen nt lds\n"
MF "v[84:87],v[92:95],v[148:151],0\n"
"v_max_f32_dpp v10,|v10|,|v10| quad_perm:[1,0,3,2] row_mask:0xf bank_mask:0xf\n"
AR "v11,a225\n"
MF "v[88:91],v[92:95],v[152:155],0\n"
"s_waitcnt lgkmcnt(2)\n"
"ds_read_b128 v[92:95],v0 offset:2048\n"
AR "v12,a226\n"
MF "v[72:75],v[96:99],v[156:159],v[72:75]\n"
"s_add_u32 m0,s65,74624\n"
"v_max_f32_dpp v10,v10,v10 quad_perm:[2,3,0,1] row_mask:0xf bank_mask:0xf\n"
"v_max_f32_dpp v11,|v11|,|v11| quad_perm:[1,0,3,2] row_mask:0xf bank_mask:0xf\n"
MF "v[76:79],v[96:99],v[160:163],v[76:79]\n"
LD "v8,s[36:39],s73 offen offset:128 nt lds\n"
MF "v[80:83],v[96:99],v[164:167],v[80:83]\n"
"v_max_f32_dpp v10,v10,v10 row_half_mirror row_mask:0xf bank_mask:0xf\n"
"v_max_f32_dpp v11,v11,v11 quad_perm:[2,3,0,1] row_mask:0xf bank_mask:0xf\n"
MF "v[84:87],v[96:99],v[168:171],v[84:87]\n"
"v_readlane_b32 s13,v10,0\n"
"v_readlane_b32 s0,v10,24\n"
MF "v[88:91],v[96:99],v[172:175],v[88:91]\n"
"s_waitcnt lgkmcnt(2)\n"
"ds_read_b128 v[96:99],v1 offset:2048\n"
"v_readlane_b32 s1,v10,32\n"
MF "v[72:75],v[100:103],v[176:179],v[72:75]\n"
"s_add_u32 m0,s65,75520\n"
"v_readlane_b32 s2,v10,56\n"
"v_max_f32_dpp v11,v11,v11 row_half_mirror row_mask:0xf bank_mask:0xf\n"
MF "v[76:79],v[100:103],v[180:183],v[76:79]\n"
LD "v8,s[36:39],s73 offen offset:256 nt lds\n"
MF "v[80:83],v[100:103],v[184:187],v[80:83]\n"
"s_min_u32 s13,s13,s0\n"
"v_readlane_b32 s11,v11,0\n"
"v_readlane_b32 s3,v11,24\n"
MF "v[84:87],v[100:103],v[188:191],v[84:87]\n"
"s_min_u32 s1,s1,s2\n"
"v_readlane_b32 s4,v11,32\n"
"v_readlane_b32 s5,v11,56\n"
MF "v[88:91],v[100:103],v[192:195],v[88:91]\n"
"s_waitcnt lgkmcnt(2)\n"
"ds_read_b128 v[100:103],v0 offset:6144\n"
"v_max_f32_dpp v12,|v12|,|v12| quad_perm:[1,0,3,2] row_mask:0xf bank_mask:0xf\n"
MF "v[72:75],v[104:107],v[196:199],v[72:75]\n"
"s_add_u32 m0,s65,73728\n"
MF "v[76:79],v[104:107],v[200:203],v[76:79]\n"
LD "v8,s[36:39],s73 offen offset:3072 nt lds\n"
MF "v[80:83],v[104:107],v[204:207],v[80:83]\n"
"s_min_u32 s13,s13,s1\n"
"v_max_f32_dpp v12,v12,v12 quad_perm:[2,3,0,1] row_mask:0xf bank_mask:0xf\n"
MF "v[84:87],v[104:107],v[208:211],v[84:87]\n"
"s_and_b32 s13,s13,0x7f800000\n"
MF "v[88:91],v[104:107],v[212:215],v[88:91]\n"
"s_waitcnt lgkmcnt(2)\n"
"ds_read_b128 v[104:107],v1 offset:6144\n"
"v_max_f32_dpp v12,v12,v12 row_half_mirror row_mask:0xf bank_mask:0xf\n"
MF "v[72:75],v[92:95],v[216:219],v[72:75]\n"
"s_add_u32 m0,s65,74624\n"
"v_readlane_b32 s10,v12,0\n"
"v_readlane_b32 s6,v12,24\n"
MF "v[76:79],v[92:95],v[220:223],v[76:79]\n"
LD "v8,s[36:39],s73 offen offset:3200 nt lds\n"
MF "v[80:83],v[92:95],v[224:227],v[80:83]\n"
"s_sub_u32 s13,s13,0x14000000\n"
"v_readlane_b32 s7,v12,32\n"
"v_readlane_b32 s8,v12,56\n"
MF "v[84:87],v[92:95],v[228:231],v[84:87]\n"
"s_cmp_lt_u32 s13,0x50000000\n"
"s_cselect_b32 s13,s13,0xed400000\n"
MF "v[88:91],v[92:95],v[232:235],v[88:91]\n"
"s_waitcnt lgkmcnt(2)\n"
"ds_read_b128 v[92:95],v0 offset:7168\n"
MF "v[72:75],v[96:99],v[236:239],v[72:75]\n"
"s_add_u32 m0,s65,75520\n"
MF "v[76:79],v[96:99],v[240:243],v[76:79]\n"
LD "v8,s[36:39],s73 offen offset:3328 nt lds\n"
MF "v[80:83],v[96:99],v[244:247],v[80:83]\n"
"s_add_u32 s13,s13,0x12c00000\n"
MF "v[84:87],v[96:99],v[248:251],v[84:87]\n"
"s_min_u32 s11,s11,s3\n"
MF "v[88:91],v[96:99],v[252:255],v[88:91]\n"
"s_waitcnt lgkmcnt(2)\n"
"ds_read_b128 v[96:99],v1 offset:7168\n"
MF "v[112:115],v[100:103],v[136:139],0\n"
"s_min_u32 s4,s4,s5\n"
MF "v[116:119],v[100:103],v[140:143],0\n"
"s_min_u32 s11,s11,s4\n"
MF "v[120:123],v[100:103],v[144:147],0\n"
"s_and_b32 s11,s11,0x7f800000\n"
MF "v[124:127],v[100:103],v[148:151],0\n"
"s_sub_u32 s11,s11,0x14000000\n"
MF "v[128:131],v[100:103],v[152:155],0\n"
"s_waitcnt lgkmcnt(2)\n"
"ds_read_b128 v[100:103],v0 offset:8192\n"
MF "v[112:115],v[104:107],v[156:159],v[112:115]\n"
"s_min_u32 s10,s10,s6\n"
"v_max3_f32 v52,v72,v73,v74\n"
"v_max_f32 v52,v52,v75\n"
MF "v[116:119],v[104:107],v[160:163],v[116:119]\n"
"s_min_u32 s7,s7,s8\n"
"v_max3_f32 v53,v76,v77,v78\n"
"v_max_f32 v53,v53,v79\n"
MF "v[120:123],v[104:107],v[164:167],v[120:123]\n"
"s_min_u32 s10,s10,s7\n"
"v_max3_f32 v54,v80,v81,v82\n"
"v_max_f32 v54,v54,v83\n"
MF "v[124:127],v[104:107],v[168:171],v[124:127]\n"
"v_max3_f32 v55,v84,v85,v86\n"
"v_max_f32 v55,v55,v87\n"
MF "v[128:131],v[104:107],v[172:175],v[128:131]\n"
"s_waitcnt lgkmcnt(2)\n"
"ds_read_b128 v[104:107],v1 offset:8192\n"
"v_max3_f32 v56,v88,v89,v90\n"
MF "v[112:115],v[92:95],v[176:179],v[112:115]\n"
"s_cmp_lt_u32 s11,0x50000000\n"
"s_cselect_b32 s11,s11,0xed400000\n"
"v_max_f32 v56,v56,v91\n"
MF "v[116:119],v[92:95],v[180:183],v[116:119]\n"
".Lpg9b:\n"
"s_cbranch_execz .Lpgpsl_b\n"
"s_add_u32 s11,s11,0x12c00000\n"
MF "v[120:123],v[92:95],v[184:187],v[120:123]\n"
MF "v[124:127],v[92:95],v[188:191],v[124:127]\n"
MF "v[128:131],v[92:95],v[192:195],v[128:131]\n"
"s_waitcnt lgkmcnt(2)\n"
MF "v[112:115],v[96:99],v[196:199],v[112:115]\n"
MF "v[116:119],v[96:99],v[200:203],v[116:119]\n"
"s_waitcnt lgkmcnt(0)\n"
"s_barrier\n"
MF "v[120:123],v[96:99],v[204:207],v[120:123]\n"
"s_add_u32 s72,s72,6144\n"
MF "v[124:127],v[96:99],v[208:211],v[124:127]\n"
"s_cmp_eq_u32 s72,s17\n"
"s_cselect_b32 s72,s16,s72\n"
"s_add_u32 m0,s64,0\n"
MF "v[128:131],v[96:99],v[212:215],v[128:131]\n"
LD "v7,s[32:35],s72 offen nt lds\n"
MF "v[112:115],v[100:103],v[216:219],v[112:115]\n"
"s_add_u32 m0,s64,896\n"
MF "v[116:119],v[100:103],v[220:223],v[116:119]\n"
LD "v7,s[32:35],s72 offen offset:128 nt lds\n"
MF "v[120:123],v[100:103],v[224:227],v[120:123]\n"
"s_add_u32 m0,s64,1792\n"
MF "v[124:127],v[100:103],v[228:231],v[124:127]\n"
LD "v7,s[32:35],s72 offen offset:256 nt lds\n"
MF "v[128:131],v[100:103],v[232:235],v[128:131]\n"
"s_add_u32 s72,s72,6144\n"
"s_add_u32 m0,s64,6144\n"
MF "v[112:115],v[104:107],v[236:239],v[112:115]\n"
LD "v7,s[32:35],s72 offen nt lds\n"
MF "v[116:119],v[104:107],v[240:243],v[116:119]\n"
"s_add_u32 m0,s64,7040\n"
MF "v[120:123],v[104:107],v[244:247],v[120:123]\n"
LD "v7,s[32:35],s72 offen offset:128 nt lds\n"
MF "v[124:127],v[104:107],v[248:251],v[124:127]\n"
"s_add_u32 m0,s64,7936\n"
MF "v[128:131],v[104:107],v[252:255],v[128:131]\n"
LD "v7,s[32:35],s72 offen offset:256 nt lds\n"
"s_branch .Lt0ex\n"
".Lpgt0kx_d:\n"
"s_cbranch_execz .Lisr2\n"
".Lt0kx:\n"
"s_lshl_b32 s58,s56,10\n"
"s_add_u32 s58,s58,73728\n"
"v_add_u32 v32,s58,v9\n"
"v_add_u32 v33,65536,v32\n"
"s_waitcnt lgkmcnt(2)\n"
MF "v[72:75],v[92:95],v[136:139],0\n"
"s_add_u32 s73,s73,12288\n"
"ds_read_b128 v[104:107],v1 offset:1024\n"
AR "v10,a224\n"
MF "v[76:79],v[92:95],v[140:143],0\n"
"s_cmp_eq_u32 s73,s19\n"
"s_cselect_b32 s73,s18,s73\n"
"s_add_u32 m0,s65,73728\n"
MF "v[80:83],v[92:95],v[144:147],0\n"
LD "v8,s[36:39],s73 offen nt lds\n"
MF "v[84:87],v[92:95],v[148:151],0\n"
"v_max_f32_dpp v10,|v10|,|v10| quad_perm:[1,0,3,2] row_mask:0xf bank_mask:0xf\n"
AR "v11,a225\n"
MF "v[88:91],v[92:95],v[152:155],0\n"
"s_waitcnt lgkmcnt(2)\n"
"ds_read_b128 v[92:95],v0 offset:2048\n"
AR "v12,a226\n"
MF "v[72:75],v[96:99],v[156:159],v[72:75]\n"
"s_add_u32 m0,s65,74624\n"
"v_max_f32_dpp v10,v10,v10 quad_perm:[2,3,0,1] row_mask:0xf bank_mask:0xf\n"
"v_max_f32_dpp v11,|v11|,|v11| quad_perm:[1,0,3,2] row_mask:0xf bank_mask:0xf\n"
MF "v[76:79],v[96:99],v[160:163],v[76:79]\n"
LD "v8,s[36:39],s73 offen offset:128 nt lds\n"
MF "v[80:83],v[96:99],v[164:167],v[80:83]\n"
"v_max_f32_dpp v10,v10,v10 row_half_mirror row_mask:0xf bank_mask:0xf\n"
"v_max_f32_dpp v11,v11,v11 quad_perm:[2,3,0,1] row_mask:0xf bank_mask:0xf\n"
MF "v[84:87],v[96:99],v[168:171],v[84:87]\n"
"v_readlane_b32 s13,v10,0\n"
"v_readlane_b32 s0,v10,24\n"
MF "v[88:91],v[96:99],v[172:175],v[88:91]\n"
"s_waitcnt lgkmcnt(2)\n"
"ds_read_b128 v[96:99],v1 offset:2048\n"
"v_readlane_b32 s1,v10,32\n"
MF "v[72:75],v[100:103],v[176:179],v[72:75]\n"
"s_add_u32 m0,s65,75520\n"
"v_readlane_b32 s2,v10,56\n"
"v_max_f32_dpp v11,v11,v11 row_half_mirror row_mask:0xf bank_mask:0xf\n"
MF "v[76:79],v[100:103],v[180:183],v[76:79]\n"
LD "v8,s[36:39],s73 offen offset:256 nt lds\n"
MF "v[80:83],v[100:103],v[184:187],v[80:83]\n"
"s_min_u32 s13,s13,s0\n"
"v_readlane_b32 s11,v11,0\n"
"v_readlane_b32 s3,v11,24\n"
MF "v[84:87],v[100:103],v[188:191],v[84:87]\n"
"s_min_u32 s1,s1,s2\n"
"v_readlane_b32 s4,v11,32\n"
"v_readlane_b32 s5,v11,56\n"
MF "v[88:91],v[100:103],v[192:195],v[88:91]\n"
"s_waitcnt lgkmcnt(2)\n"
"ds_read_b128 v[100:103],v0 offset:6144\n"
"v_max_f32_dpp v12,|v12|,|v12| quad_perm:[1,0,3,2] row_mask:0xf bank_mask:0xf\n"
MF "v[72:75],v[104:107],v[196:199],v[72:75]\n"
"s_add_u32 m0,s65,73728\n"
"ds_write_b128 v32,a[0:3] offset:24576\n"
MF "v[76:79],v[104:107],v[200:203],v[76:79]\n"
LD "v8,s[36:39],s73 offen offset:3072 nt lds\n"
MF "v[80:83],v[104:107],v[204:207],v[80:83]\n"
"s_min_u32 s13,s13,s1\n"
"v_max_f32_dpp v12,v12,v12 quad_perm:[2,3,0,1] row_mask:0xf bank_mask:0xf\n"
AR "v62,a20\n"
MF "v[84:87],v[104:107],v[208:211],v[84:87]\n"
"s_and_b32 s13,s13,0x7f800000\n"
"ds_write_b128 v32,a[4:7] offset:28672\n"
MF "v[88:91],v[104:107],v[212:215],v[88:91]\n"
"s_waitcnt lgkmcnt(4)\n"
"ds_read_b128 v[104:107],v1 offset:6144\n"
"v_max_f32_dpp v12,v12,v12 row_half_mirror row_mask:0xf bank_mask:0xf\n"
MF "v[72:75],v[92:95],v[216:219],v[72:75]\n"
"s_add_u32 m0,s65,74624\n"
"v_readlane_b32 s10,v12,0\n"
"v_readlane_b32 s6,v12,24\n"
MF "v[76:79],v[92:95],v[220:223],v[76:79]\n"
LD "v8,s[36:39],s73 offen offset:3200 nt lds\n"
MF "v[80:83],v[92:95],v[224:227],v[80:83]\n"
"s_sub_u32 s13,s13,0x14000000\n"
"v_readlane_b32 s7,v12,32\n"
"v_readlane_b32 s8,v12,56\n"
MF "v[84:87],v[92:95],v[228:231],v[84:87]\n"
"s_cmp_lt_u32 s13,0x50000000\n"
"s_cselect_b32 s13,s13,0xed400000\n"
AR "v63,a21\n"
MF "v[88:91],v[92:95],v[232:235],v[88:91]\n"
"s_waitcnt lgkmcnt(4)\n"
"ds_read_b128 v[92:95],v0 offset:7168\n"
AR "v64,a22\n"
MF "v[72:75],v[96:99],v[236:239],v[72:75]\n"
"s_add_u32 m0,s65,75520\n"
"ds_write_b128 v32,a[8:11] offset:32768\n"
MF "v[76:79],v[96:99],v[240:243],v[76:79]\n"
LD "v8,s[36:39],s73 offen offset:3328 nt lds\n"
MF "v[80:83],v[96:99],v[244:247],v[80:83]\n"
"s_add_u32 s13,s13,0x12c00000\n"
"ds_write_b128 v32,a[12:15] offset:36864\n"
MF "v[84:87],v[96:99],v[248:251],v[84:87]\n"
"s_min_u32 s11,s11,s3\n"
"ds_write_b128 v32,a[16:19] offset:40960\n"
MF "v[88:91],v[96:99],v[252:255],v[88:91]\n"
"s_waitcnt lgkmcnt(7)\n"
"ds_read_b128 v[96:99],v1 offset:7168\n"
AR "v65,a23\n"
MF "v[112:115],v[100:103],v[136:139],0\n"
"s_min_u32 s4,s4,s5\n"
"ds_write_b128 v32,a[40:43] offset:45056\n"
MF "v[116:119],v[100:103],v[140:143],0\n"
"s_min_u32 s11,s11,s4\n"
"ds_write_b128 v32,a[44:47] offset:49152\n"
MF "v[120:123],v[100:103],v[144:147],0\n"
"s_and_b32 s11,s11,0x7f800000\n"
"ds_write_b128 v32,a[48:51] offset:53248\n"
MF "v[124:127],v[100:103],v[148:151],0\n"
"s_sub_u32 s11,s11,0x14000000\n"
"ds_write_b128 v32,a[52:55] offset:57344\n"
MF "v[128:131],v[100:103],v[152:155],0\n"
"s_waitcnt lgkmcnt(9)\n"
"ds_read_b128 v[100:103],v0 offset:8192\n"
AR "v66,a24\n"
MF "v[112:115],v[104:107],v[156:159],v[112:115]\n"
"s_min_u32 s10,s10,s6\n"
"v_max3_f32 v52,v72,v73,v74\n"
"v_max_f32 v52,v52,v75\n"
MF "v[116:119],v[104:107],v[160:163],v[116:119]\n"
"s_min_u32 s7,s7,s8\n"
"v_max3_f32 v53,v76,v77,v78\n"
"v_max_f32 v53,v53,v79\n"
MF "v[120:123],v[104:107],v[164:167],v[120:123]\n"
"s_min_u32 s10,s10,s7\n"
"v_max3_f32 v54,v80,v81,v82\n"
"v_max_f32 v54,v54,v83\n"
MF "v[124:127],v[104:107],v[168:171],v[124:127]\n"
"v_max3_f32 v55,v84,v85,v86\n"
"v_max_f32 v55,v55,v87\n"
MF "v[128:131],v[104:107],v[172:175],v[128:131]\n"
"s_waitcnt lgkmcnt(9)\n"
"ds_read_b128 v[104:107],v1 offset:8192\n"
"v_max3_f32 v56,v88,v89,v90\n"
MF "v[112:115],v[92:95],v[176:179],v[112:115]\n"
"s_cmp_lt_u32 s11,0x50000000\n"
"s_cselect_b32 s11,s11,0xed400000\n"
"v_max_f32 v56,v56,v91\n"
MF "v[116:119],v[92:95],v[180:183],v[116:119]\n"
"s_add_u32 s11,s11,0x12c00000\n"
"ds_write_b128 v32,a[56:59] offset:61440\n"
MF "v[120:123],v[92:95],v[184:187],v[120:123]\n"
"ds_write_b128 v33,a[80:83]\n"
MF "v[124:127],v[92:95],v[188:191],v[124:127]\n"
"ds_write_b128 v33,a[84:87] offset:4096\n"
MF "v[128:131],v[92:95],v[192:195],v[128:131]\n"
"s_waitcnt lgkmcnt(9)\n"
"ds_write_b128 v33,a[88:91] offset:8192\n"
MF "v[112:115],v[96:99],v[196:199],v[112:115]\n"
AR "v67,a25\n"
AR "v68,a26\n"
MF "v[116:119],v[96:99],v[200:203],v[116:119]\n"
"s_waitcnt lgkmcnt(0)\n"
"s_barrier\n"
MF "v[120:123],v[96:99],v[204:207],v[120:123]\n"
"s_add_u32 s72,s72,6144\n"
"ds_write_b128 v33,a[92:95] offset:12288\n"
MF "v[124:127],v[96:99],v[208:211],v[124:127]\n"
"s_cmp_eq_u32 s72,s17\n"
"s_cselect_b32 s72,s16,s72\n"
"s_add_u32 m0,s64,0\n"
MF "v[128:131],v[96:99],v[212:215],v[128:131]\n"
LD "v7,s[32:35],s72 offen nt lds\n"
MF "v[112:115],v[100:103],v[216:219],v[112:115]\n"
"s_add_u32 m0,s64,896\n"
"ds_write_b128 v33,a[96:99] offset:16384\n"
MF "v[116:119],v[100:103],v[220:223],v[116:119]\n"
LD "v7,s[32:35],s72 offen offset:128 nt lds\n"
MF "v[120:123],v[100:103],v[224:227],v[120:123]\n"
"s_add_u32 m0,s64,1792\n"
AR "v69,a27\n"
AR "v70,a28\n"
MF "v[124:127],v[100:103],v[228:231],v[124:127]\n"
LD "v7,s[32:35],s72 offen offset:256 nt lds\n"
MF "v[128:131],v[100:103],v[232:235],v[128:131]\n"
"s_add_u32 s72,s72,6144\n"
"s_add_u32 m0,s64,6144\n"
AR "v71,a29\n"
MF "v[112:115],v[104:107],v[236:239],v[112:115]\n"
LD "v7,s[32:35],s72 offen nt lds\n"
MF "v[116:119],v[104:107],v[240:243],v[116:119]\n"
"s_add_u32 m0,s64,7040\n"
"ds_write_b128 v33,a[120:123] offset:20480\n"
MF "v[120:123],v[104:107],v[244:247],v[120:123]\n"
LD "v7,s[32:35],s72 offen offset:128 nt lds\n"
MF "v[124:127],v[104:107],v[248:251],v[124:127]\n"
"s_add_u32 m0,s64,7936\n"
AR "v132,a30\n"
AR "v133,a31\n"
MF "v[128:131],v[104:107],v[252:255],v[128:131]\n"
LD "v7,s[32:35],s72 offen offset:256 nt lds\n"
AR "v134,a32\n"
AR "v135,a33\n"
AR "v6,a34\n"
AR "v13,a35\n"
AR "v14,a36\n"
AR "v15,a37\n"
"v_accvgpr_mov_b32 a254,a38\n"
"v_accvgpr_mov_b32 a255,a39\n"
"v_accvgpr_mov_b32 a240,a168\n"
"v_accvgpr_mov_b32 a241,a169\n"
"v_accvgpr_mov_b32 a242,a170\n"
"v_accvgpr_mov_b32 a243,a171\n"
"v_accvgpr_mov_b32 a244,a172\n"
"v_accvgpr_mov_b32 a245,a173\n"
"v_accvgpr_mov_b32 a246,a174\n"
"v_accvgpr_mov_b32 a247,a175\n"
"v_accvgpr_mov_b32 a248,a176\n"
"v_accvgpr_mov_b32 a249,a177\n"
"v_accvgpr_mov_b32 a250,a178\n"
"v_accvgpr_mov_b32 a251,a179\n"
".Lpgt0ex_b:\n"
"s_cbranch_execz .Lpg2a\n"
".Lt0ex:\n"
"s_nop 1\n"
"v_max3_f32 v52,v52,v112,v113\n"
"v_max3_f32 v53,v53,v116,v117\n"
"v_max3_f32 v54,v54,v120,v121\n"
"v_max3_f32 v55,v55,v124,v125\n"
"v_max3_f32 v56,v56,v128,v129\n"
"v_max3_f32 v52,v52,v114,v115\n"
"v_max3_f32 v53,v53,v118,v119\n"
"v_max3_f32 v54,v54,v122,v123\n"
"v_max3_f32 v55,v55,v126,v127\n"
"v_max3_f32 v56,v56,v130,v131\n"
"v_mov_b32 v57,v52\n"
"v_mov_b32 v58,v53\n"
"v_mov_b32 v59,v54\n"
"v_mov_b32 v60,v55\n"
"v_mov_b32 v61,v56\n"
"v_permlane16_swap_b32 v52,v57\n"
"v_permlane16_swap_b32 v53,v58\n"
"v_permlane16_swap_b32 v54,v59\n"
"v_permlane16_swap_b32 v55,v60\n"
"v_permlane16_swap_b32 v56,v61\n"
"v_max_f32 v52,v52,v57\n"
"v_max_f32 v53,v53,v58\n"
"v_max_f32 v54,v54,v59\n"
"v_max_f32 v55,v55,v60\n"
"v_max_f32 v56,v56,v61\n"
"v_mov_b32 v57,v52\n"
"v_mov_b32 v58,v53\n"
"v_mov_b32 v59,v54\n"
"v_mov_b32 v60,v55\n"
"v_mov_b32 v61,v56\n"
"v_permlane32_swap_b32 v52,v57\n"
"v_permlane32_swap_b32 v53,v58\n"
"v_permlane32_swap_b32 v54,v59\n"
"v_permlane32_swap_b32 v55,v60\n"
"v_permlane32_swap_b32 v56,v61\n"
"v_max_f32 v52,v52,v57\n"
"v_max_f32 v53,v53,v58\n"
"v_max_f32 v54,v54,v59\n"
"v_max_f32 v55,v55,v60\n"
"v_max_f32 v56,v56,v61\n"
"v_sub_f32 v32,0xc1300000,v52\n"
"v_sub_f32 v36,0xc1300000,v53\n"
"v_sub_f32 v40,0xc1300000,v54\n"
"v_sub_f32 v44,0xc1300000,v55\n"
"v_sub_f32 v48,0xc1300000,v56\n"
"v_mov_b32_e64 v33,v32\n"
"v_pk_mov_b32 v[34:35],v[32:33],v[32:33]\n"
"v_mov_b32 v37,v36\n"
"v_pk_mov_b32 v[38:39],v[36:37],v[36:37]\n"
"v_mov_b32 v41,v40\n"
"v_pk_mov_b32 v[42:43],v[40:41],v[40:41]\n"
"v_mov_b32 v45,v44\n"
"v_pk_mov_b32 v[46:47],v[44:45],v[44:45]\n"
"v_mov_b32 v49,v48\n"
"v_pk_mov_b32 v[50:51],v[48:49],v[48:49]\n"
"v_pk_add_f32 v[72:73],v[72:73],v[32:33]\n"
"v_pk_add_f32 v[74:75],v[74:75],v[32:33]\n"
"v_pk_add_f32 v[112:113],v[112:113],v[32:33]\n"
"v_pk_add_f32 v[114:115],v[114:115],v[32:33]\n"
"v_exp_f32 v72,v72\n"
"v_exp_f32 v73,v73\n"
"v_exp_f32 v74,v74\n"
"v_exp_f32 v75,v75\n"
"v_exp_f32 v112,v112\n"
"v_exp_f32 v113,v113\n"
"v_exp_f32 v114,v114\n"
"v_exp_f32 v115,v115\n"
"v_pk_add_f32 v[10:11],v[72:73],v[74:75]\n"
"v_pk_add_f32 v[28:29],v[112:113],v[114:115]\n"
"v_pk_add_f32 v[10:11],v[10:11],v[28:29]\n"
"v_add_f32_e64 v52,v10,v11\n"
CV "v72,v72,v73\n"
CV "v73,v74,v75\n"
CV "v74,v112,v113\n"
CV "v75,v114,v115\n"
"v_pk_add_f32 v[76:77],v[76:77],v[36:37]\n"
"v_pk_add_f32 v[78:79],v[78:79],v[36:37]\n"
"v_pk_add_f32 v[116:117],v[116:117],v[36:37]\n"
"v_pk_add_f32 v[118:119],v[118:119],v[36:37]\n"
"v_exp_f32 v76,v76\n"
"v_exp_f32 v77,v77\n"
"v_exp_f32 v78,v78\n"
"v_exp_f32 v79,v79\n"
"v_exp_f32 v116,v116\n"
"v_exp_f32 v117,v117\n"
"v_exp_f32 v118,v118\n"
"v_exp_f32 v119,v119\n"
"v_pk_add_f32 v[10:11],v[76:77],v[78:79]\n"
"v_pk_add_f32 v[28:29],v[116:117],v[118:119]\n"
"v_pk_add_f32 v[10:11],v[10:11],v[28:29]\n"
"v_add_f32_e64 v53,v10,v11\n"
CV "v76,v76,v77\n"
CV "v77,v78,v79\n"
CV "v78,v116,v117\n"
CV "v79,v118,v119\n"
"v_pk_add_f32 v[80:81],v[80:81],v[40:41]\n"
"v_pk_add_f32 v[82:83],v[82:83],v[40:41]\n"
"v_pk_add_f32 v[120:121],v[120:121],v[40:41]\n"
"v_pk_add_f32 v[122:123],v[122:123],v[40:41]\n"
"v_exp_f32 v80,v80\n"
"v_exp_f32 v81,v81\n"
"v_exp_f32 v82,v82\n"
"v_exp_f32 v83,v83\n"
"v_exp_f32 v120,v120\n"
"v_exp_f32 v121,v121\n"
"v_exp_f32 v122,v122\n"
"v_exp_f32 v123,v123\n"
"v_pk_add_f32 v[10:11],v[80:81],v[82:83]\n"
"v_pk_add_f32 v[28:29],v[120:121],v[122:123]\n"
"v_pk_add_f32 v[10:11],v[10:11],v[28:29]\n"
"v_add_f32_e64 v54,v10,v11\n"
CV "v80,v80,v81\n"
CV "v81,v82,v83\n"
CV "v82,v120,v121\n"
CV "v83,v122,v123\n"
"v_pk_add_f32 v[84:85],v[84:85],v[44:45]\n"
"v_pk_add_f32 v[86:87],v[86:87],v[44:45]\n"
"v_pk_add_f32 v[124:125],v[124:125],v[44:45]\n"
"v_pk_add_f32 v[126:127],v[126:127],v[44:45]\n"
"v_mov_b32_e64 v55,0\n"
"v_pk_add_f32 v[88:89],v[88:89],v[48:49]\n"
"v_pk_add_f32 v[90:91],v[90:91],v[48:49]\n"
"v_pk_add_f32 v[128:129],v[128:129],v[48:49]\n"
"v_pk_add_f32 v[130:131],v[130:131],v[48:49]\n"
"v_mov_b32 v56,0\n"
"s_waitcnt vmcnt(18) lgkmcnt(0)\n"
"s_barrier\n"
"ds_read_b128 v[16:19],v0 offset:24576\n"
"ds_read_b128 v[20:23],v1 offset:24576\n"
AW "a220,s13\n"
AW "a221,s13\n"
AW "a222,s13\n"
AW "a223,s13\n"
"s_waitcnt lgkmcnt(1)\n"
MF "v[92:95],v[16:19],v[136:139],v[32:35]\n"
"s_nop 0\n"
"ds_read_b128 v[24:27],v0 offset:25600\n"
"ds_read_b128 v[28:31],v1 offset:25600\n"
"s_waitcnt lgkmcnt(2)\n"
MF "v[92:95],v[20:23],v[156:159],v[92:95]\n"
"v_exp_f32 v84,v84\n"
MF "v[96:99],v[20:23],v[160:163],v[36:39]\n"
"v_exp_f32 v85,v85\n"
MF "v[96:99],v[16:19],v[140:143],v[96:99]\n"
"v_exp_f32 v86,v86\n"
MF "v[100:103],v[16:19],v[144:147],v[40:43]\n"
"v_exp_f32 v87,v87\n"
MF "v[100:103],v[20:23],v[164:167],v[100:103]\n"
"v_add_f32 v55,v55,v84\n"
"v_add_f32 v55,v55,v85\n"
MF "v[104:107],v[20:23],v[168:171],v[44:47]\n"
CV "v84,v84,v85\n"
"v_add_f32 v55,v55,v86\n"
MF "v[104:107],v[16:19],v[148:151],v[104:107]\n"
"v_add_f32 v55,v55,v87\n"
CV "v85,v86,v87\n"
MF "v[108:111],v[16:19],v[152:155],v[48:51]\n"
"v_exp_f32 v124,v124\n"
MF "v[108:111],v[20:23],v[172:175],v[108:111]\n"
"s_waitcnt lgkmcnt(0)\n"
"ds_read_b128 v[16:19],v0 offset:26624\n"
"ds_read_b128 v[20:23],v1 offset:26624\n"
MF "v[108:111],v[24:27],v[192:195],v[108:111]\n"
"v_exp_f32 v125,v125\n"
MF "v[108:111],v[28:31],v[212:215],v[108:111]\n"
"v_exp_f32 v126,v126\n"
MF "v[104:107],v[28:31],v[208:211],v[104:107]\n"
"v_exp_f32 v127,v127\n"
MF "v[104:107],v[24:27],v[188:191],v[104:107]\n"
"v_add_f32 v55,v55,v124\n"
"v_add_f32 v55,v55,v125\n"
MF "v[100:103],v[24:27],v[184:187],v[100:103]\n"
CV "v86,v124,v125\n"
"v_add_f32 v55,v55,v126\n"
MF "v[100:103],v[28:31],v[204:207],v[100:103]\n"
"v_add_f32 v55,v55,v127\n"
CV "v87,v126,v127\n"
MF "v[96:99],v[28:31],v[200:203],v[96:99]\n"
"v_exp_f32 v88,v88\n"
MF "v[96:99],v[24:27],v[180:183],v[96:99]\n"
"v_exp_f32 v89,v89\n"
MF "v[92:95],v[24:27],v[176:179],v[92:95]\n"
"v_exp_f32 v90,v90\n"
MF "v[92:95],v[28:31],v[196:199],v[92:95]\n"
"s_waitcnt lgkmcnt(0)\n"
"ds_read_b128 v[24:27],v0 offset:30720\n"
"ds_read_b128 v[28:31],v1 offset:30720\n"
MF "v[92:95],v[16:19],v[216:219],v[92:95]\n"
"v_exp_f32 v91,v91\n"
MF "v[92:95],v[20:23],v[236:239],v[92:95]\n"
"v_add_f32 v56,v56,v88\n"
"v_add_f32 v56,v56,v89\n"
MF "v[96:99],v[20:23],v[240:243],v[96:99]\n"
CV "v88,v88,v89\n"
"v_add_f32 v56,v56,v90\n"
MF "v[96:99],v[16:19],v[220:223],v[96:99]\n"
"v_add_f32 v56,v56,v91\n"
CV "v89,v90,v91\n"
MF "v[100:103],v[16:19],v[224:227],v[100:103]\n"
"v_exp_f32 v128,v128\n"
MF "v[100:103],v[20:23],v[244:247],v[100:103]\n"
"v_exp_f32 v129,v129\n"
MF "v[104:107],v[20:23],v[248:251],v[104:107]\n"
"v_exp_f32 v130,v130\n"
MF "v[104:107],v[16:19],v[228:231],v[104:107]\n"
"v_exp_f32 v131,v131\n"
MF "v[108:111],v[16:19],v[232:235],v[108:111]\n"
"v_add_f32 v56,v56,v128\n"
"v_add_f32 v56,v56,v129\n"
MF "v[108:111],v[20:23],v[252:255],v[108:111]\n"
"s_waitcnt lgkmcnt(0)\n"
"ds_read_b128 v[16:19],v0 offset:31744\n"
"ds_read_b128 v[20:23],v1 offset:31744\n"
MF "v[112:115],v[24:27],v[136:139],v[32:35]\n"
CV "v90,v128,v129\n"
"v_add_f32 v56,v56,v130\n"
MF "v[112:115],v[28:31],v[156:159],v[112:115]\n"
"v_add_f32 v56,v56,v131\n"
CV "v91,v130,v131\n"
MF "v[116:119],v[28:31],v[160:163],v[36:39]\n"
"v_exp_f32 v92,v92\n"
MF "v[116:119],v[24:27],v[140:143],v[116:119]\n"
"v_exp_f32 v93,v93\n"
MF "v[120:123],v[24:27],v[144:147],v[40:43]\n"
"v_exp_f32 v94,v94\n"
MF "v[120:123],v[28:31],v[164:167],v[120:123]\n"
"v_exp_f32 v95,v95\n"
MF "v[124:127],v[28:31],v[168:171],v[44:47]\n"
"v_add_f32 v57,v52,v92\n"
"v_add_f32 v57,v57,v93\n"
MF "v[124:127],v[24:27],v[148:151],v[124:127]\n"
CV "v92,v92,v93\n"
"v_add_f32 v57,v57,v94\n"
MF "v[128:131],v[24:27],v[152:155],v[48:51]\n"
"v_add_f32 v57,v57,v95\n"
CV "v93,v94,v95\n"
MF "v[128:131],v[28:31],v[172:175],v[128:131]\n"
"s_waitcnt lgkmcnt(0)\n"
"ds_read_b128 v[24:27],v0 offset:32768\n"
"ds_read_b128 v[28:31],v1 offset:32768\n"
MF "v[128:131],v[16:19],v[192:195],v[128:131]\n"
"v_exp_f32 v96,v96\n"
MF "v[128:131],v[20:23],v[212:215],v[128:131]\n"
"v_exp_f32 v97,v97\n"
MF "v[124:127],v[20:23],v[208:211],v[124:127]\n"
"v_exp_f32 v98,v98\n"
MF "v[124:127],v[16:19],v[188:191],v[124:127]\n"
"v_exp_f32 v99,v99\n"
MF "v[120:123],v[16:19],v[184:187],v[120:123]\n"
"v_add_f32 v58,v53,v96\n"
"v_add_f32 v58,v58,v97\n"
MF "v[120:123],v[20:23],v[204:207],v[120:123]\n"
CV "v96,v96,v97\n"
"v_add_f32 v58,v58,v98\n"
MF "v[116:119],v[20:23],v[200:203],v[116:119]\n"
"v_add_f32 v58,v58,v99\n"
CV "v97,v98,v99\n"
MF "v[116:119],v[16:19],v[180:183],v[116:119]\n"
"s_waitcnt vmcnt(12) lgkmcnt(0)\n"
"s_barrier\n"
MF "v[112:115],v[16:19],v[176:179],v[112:115]\n"
"v_exp_f32 v100,v100\n"
MF "v[112:115],v[20:23],v[196:199],v[112:115]\n"
"s_nop 0\n"
TR "v[16:17],v2\n"
TR "v[18:19],v2 offset:6144\n"
MF "v[112:115],v[24:27],v[216:219],v[112:115]\n"
"s_add_u32 s72,s72,6144\n"
TR "v[20:21],v3\n"
TR "v[22:23],v3 offset:6144\n"
MF "v[112:115],v[28:31],v[236:239],v[112:115]\n"
"s_cmp_eq_u32 s72,s17\n"
"s_cselect_b32 s72,s16,s72\n"
"s_add_u32 m0,s64,24576\n"
MF "v[116:119],v[28:31],v[240:243],v[116:119]\n"
LD "v7,s[32:35],s72 offen nt lds\n"
MF "v[116:119],v[24:27],v[220:223],v[116:119]\n"
"s_add_u32 m0,s64,25472\n"
"v_exp_f32 v101,v101\n"
MF "v[120:123],v[24:27],v[224:227],v[120:123]\n"
LD "v7,s[32:35],s72 offen offset:128 nt lds\n"
MF "v[120:123],v[28:31],v[244:247],v[120:123]\n"
"v_exp_f32 v102,v102\n"
MF "v[124:127],v[28:31],v[248:251],v[124:127]\n"
"s_add_u32 m0,s64,26368\n"
"v_exp_f32 v103,v103\n"
MF "v[124:127],v[24:27],v[228:231],v[124:127]\n"
LD "v7,s[32:35],s72 offen offset:256 nt lds\n"
MF "v[128:131],v[24:27],v[232:235],v[128:131]\n"
"s_add_u32 s72,s72,6144\n"
"v_add_f32 v59,v54,v100\n"
"v_add_f32 v59,v59,v101\n"
MF "v[128:131],v[28:31],v[252:255],v[128:131]\n"
"s_waitcnt lgkmcnt(0)\n"
TR "v[24:25],v4\n"
TR "v[26:27],v4 offset:6144\n"
MF "a[0:3],v[16:19],v[72:75],a[220:223]\n"
"s_add_u32 m0,s64,30720\n"
TR "v[28:29],v5\n"
TR "v[30:31],v5 offset:6144\n"
MF "a[20:23],v[20:23],v[72:75],a[220:223]\n"
LD "v7,s[32:35],s72 offen nt lds\n"
MF "a[24:27],v[20:23],v[76:79],a[220:223]\n"
CV "v100,v100,v101\n"
"v_add_f32 v59,v59,v102\n"
MF "a[4:7],v[16:19],v[76:79],a[220:223]\n"
"s_add_u32 m0,s64,31616\n"
"v_add_f32 v59,v59,v103\n"
CV "v101,v102,v103\n"
MF "a[8:11],v[16:19],v[80:83],a[220:223]\n"
LD "v7,s[32:35],s72 offen offset:128 nt lds\n"
MF "a[28:31],v[20:23],v[80:83],a[220:223]\n"
"v_exp_f32 v104,v104\n"
MF "a[32:35],v[20:23],v[84:87],a[220:223]\n"
"s_add_u32 m0,s64,32512\n"
"v_exp_f32 v105,v105\n"
MF "a[12:15],v[16:19],v[84:87],a[220:223]\n"
LD "v7,s[32:35],s72 offen offset:256 nt lds\n"
MF "a[16:19],v[16:19],v[88:91],a[220:223]\n"
"v_exp_f32 v106,v106\n"
MF "a[36:39],v[20:23],v[88:91],a[220:223]\n"
"s_waitcnt lgkmcnt(0)\n"
TR "v[16:17],v2 offset:1024\n"
TR "v[18:19],v2 offset:7168\n"
MF "a[56:59],v[24:27],v[88:91],a[220:223]\n"
"s_nop 0\n"
TR "v[20:21],v3 offset:1024\n"
TR "v[22:23],v3 offset:7168\n"
MF "a[76:79],v[28:31],v[88:91],a[220:223]\n"
"v_exp_f32 v107,v107\n"
MF "a[72:75],v[28:31],v[84:87],a[220:223]\n"
"v_add_f32 v60,v55,v104\n"
"v_add_f32 v60,v60,v105\n"
MF "a[52:55],v[24:27],v[84:87],a[220:223]\n"
CV "v104,v104,v105\n"
"v_add_f32 v60,v60,v106\n"
MF "a[48:51],v[24:27],v[80:83],a[220:223]\n"
"v_add_f32 v60,v60,v107\n"
CV "v105,v106,v107\n"
MF "a[68:71],v[28:31],v[80:83],a[220:223]\n"
"v_exp_f32 v108,v108\n"
MF "a[64:67],v[28:31],v[76:79],a[220:223]\n"
"v_exp_f32 v109,v109\n"
MF "a[44:47],v[24:27],v[76:79],a[220:223]\n"
"v_exp_f32 v110,v110\n"
MF "a[40:43],v[24:27],v[72:75],a[220:223]\n"
"v_exp_f32 v111,v111\n"
MF "a[60:63],v[28:31],v[72:75],a[220:223]\n"
"s_waitcnt lgkmcnt(0)\n"
TR "v[24:25],v4 offset:1024\n"
TR "v[26:27],v4 offset:7168\n"
MF "a[80:83],v[16:19],v[72:75],a[220:223]\n"
".Lpg10b:\n"
"s_cbranch_execz .Lpg1a\n"
".Lpg10d:\n"
"s_cbranch_execz .Lisr2\n"
"s_nop 0\n"
TR "v[28:29],v5 offset:1024\n"
TR "v[30:31],v5 offset:7168\n"
MF "a[100:103],v[20:23],v[72:75],a[220:223]\n"
"v_add_f32 v61,v56,v108\n"
"v_add_f32 v61,v61,v109\n"
MF "a[104:107],v[20:23],v[76:79],a[220:223]\n"
CV "v108,v108,v109\n"
"v_add_f32 v61,v61,v110\n"
MF "a[84:87],v[16:19],v[76:79],a[220:223]\n"
"v_add_f32 v61,v61,v111\n"
CV "v109,v110,v111\n"
MF "a[88:91],v[16:19],v[80:83],a[220:223]\n"
"v_exp_f32 v112,v112\n"
MF "a[108:111],v[20:23],v[80:83],a[220:223]\n"
"v_exp_f32 v113,v113\n"
MF "a[112:115],v[20:23],v[84:87],a[220:223]\n"
"v_exp_f32 v114,v114\n"
MF "a[92:95],v[16:19],v[84:87],a[220:223]\n"
"v_exp_f32 v115,v115\n"
MF "a[96:99],v[16:19],v[88:91],a[220:223]\n"
"v_add_f32 v57,v57,v112\n"
"v_add_f32 v57,v57,v113\n"
MF "a[116:119],v[20:23],v[88:91],a[220:223]\n"
"s_waitcnt lgkmcnt(0)\n"
TR "v[16:17],v2 offset:2048\n"
TR "v[18:19],v2 offset:8192\n"
MF "a[136:139],v[24:27],v[88:91],a[220:223]\n"
"s_nop 0\n"
TR "v[20:21],v3 offset:2048\n"
TR "v[22:23],v3 offset:8192\n"
MF "a[156:159],v[28:31],v[88:91],a[220:223]\n"
CV "v94,v112,v113\n"
"v_add_f32 v57,v57,v114\n"
MF "a[152:155],v[28:31],v[84:87],a[220:223]\n"
"v_add_f32 v57,v57,v115\n"
CV "v95,v114,v115\n"
MF "a[132:135],v[24:27],v[84:87],a[220:223]\n"
"v_exp_f32 v116,v116\n"
MF "a[128:131],v[24:27],v[80:83],a[220:223]\n"
"v_exp_f32 v117,v117\n"
MF "a[148:151],v[28:31],v[80:83],a[220:223]\n"
"v_exp_f32 v118,v118\n"
MF "a[144:147],v[28:31],v[76:79],a[220:223]\n"
"v_exp_f32 v119,v119\n"
MF "a[124:127],v[24:27],v[76:79],a[220:223]\n"
"v_add_f32 v58,v58,v116\n"
"v_add_f32 v58,v58,v117\n"
MF "a[120:123],v[24:27],v[72:75],a[220:223]\n"
CV "v98,v116,v117\n"
"v_add_f32 v58,v58,v118\n"
MF "a[140:143],v[28:31],v[72:75],a[220:223]\n"
"s_waitcnt lgkmcnt(0)\n"
TR "v[24:25],v4 offset:2048\n"
TR "v[26:27],v4 offset:8192\n"
MF "a[160:163],v[16:19],v[72:75],a[220:223]\n"
"s_nop 0\n"
TR "v[28:29],v5 offset:2048\n"
TR "v[30:31],v5 offset:8192\n"
MF "a[180:183],v[20:23],v[72:75],a[220:223]\n"
"v_add_f32 v58,v58,v119\n"
CV "v99,v118,v119\n"
MF "a[184:187],v[20:23],v[76:79],a[220:223]\n"
"v_exp_f32 v120,v120\n"
MF "a[164:167],v[16:19],v[76:79],a[220:223]\n"
"v_exp_f32 v121,v121\n"
MF "a[168:171],v[16:19],v[80:83],a[220:223]\n"
"v_exp_f32 v122,v122\n"
MF "a[188:191],v[20:23],v[80:83],a[220:223]\n"
"v_exp_f32 v123,v123\n"
MF "a[192:195],v[20:23],v[84:87],a[220:223]\n"
"v_add_f32 v59,v59,v120\n"
"v_add_f32 v59,v59,v121\n"
MF "a[172:175],v[16:19],v[84:87],a[220:223]\n"
CV "v102,v120,v121\n"
"v_add_f32 v59,v59,v122\n"
MF "a[176:179],v[16:19],v[88:91],a[220:223]\n"
"s_waitcnt vmcnt(6) lgkmcnt(0)\n"
"s_barrier\n"
MF "a[196:199],v[20:23],v[88:91],a[220:223]\n"
"s_add_u32 s73,s73,12288\n"
"ds_read_b128 v[16:19],v0\n"
"ds_read_b128 v[20:23],v1\n"
MF "a[216:219],v[24:27],v[88:91],a[220:223]\n"
"s_cmp_eq_u32 s73,s19\n"
"s_cselect_b32 s73,s18,s73\n"
"s_add_u32 m0,s65,49152\n"
MF "a[236:239],v[28:31],v[88:91],a[220:223]\n"
LD "v8,s[36:39],s73 offen nt lds\n"
MF "a[232:235],v[28:31],v[84:87],a[220:223]\n"
"s_add_u32 m0,s65,50048\n"
"v_add_f32 v59,v59,v123\n"
CV "v103,v122,v123\n"
MF "a[212:215],v[24:27],v[84:87],a[220:223]\n"
LD "v8,s[36:39],s73 offen offset:128 nt lds\n"
MF "a[208:211],v[24:27],v[80:83],a[220:223]\n"
"s_add_u32 m0,s65,50944\n"
MF "a[228:231],v[28:31],v[80:83],a[220:223]\n"
LD "v8,s[36:39],s73 offen offset:256 nt lds\n"
MF "a[224:227],v[28:31],v[76:79],a[220:223]\n"
MF "a[204:207],v[24:27],v[76:79],a[220:223]\n"
MF "a[200:203],v[24:27],v[72:75],a[220:223]\n"
MF "a[220:223],v[28:31],v[72:75],a[220:223]\n"
"s_waitcnt lgkmcnt(0)\n"
".Laloopx:\n"
MF "v[72:75],v[16:19],v[136:139],v[32:35]\n"
"s_nop 0\n"
"ds_read_b128 v[24:27],v0 offset:1024\n"
"ds_read_b128 v[28:31],v1 offset:1024\n"
MF "v[72:75],v[20:23],v[156:159],v[72:75]\n"
MF "v[76:79],v[20:23],v[160:163],v[36:39]\n"
MF "v[76:79],v[16:19],v[140:143],v[76:79]\n"
MF "v[80:83],v[16:19],v[144:147],v[40:43]\n"
MF "v[80:83],v[20:23],v[164:167],v[80:83]\n"
MF "v[84:87],v[20:23],v[168:171],v[44:47]\n"
MF "v[84:87],v[16:19],v[148:151],v[84:87]\n"
"s_add_u32 m0,s65,49152\n"
MF "v[88:91],v[16:19],v[152:155],v[48:51]\n"
LD "v8,s[36:39],s73 offen offset:3072 nt lds\n"
MF "v[88:91],v[20:23],v[172:175],v[88:91]\n"
"s_waitcnt lgkmcnt(0)\n"
"ds_read_b128 v[16:19],v0 offset:2048\n"
"ds_read_b128 v[20:23],v1 offset:2048\n"
MF "v[88:91],v[24:27],v[192:195],v[88:91]\n"
"v_exp_f32 v124,v124\n"
MF "v[88:91],v[28:31],v[212:215],v[88:91]\n"
"s_add_u32 m0,s65,50048\n"
"v_exp_f32 v125,v125\n"
MF "v[84:87],v[28:31],v[208:211],v[84:87]\n"
LD "v8,s[36:39],s73 offen offset:3200 nt lds\n"
MF "v[84:87],v[24:27],v[188:191],v[84:87]\n"
"v_exp_f32 v126,v126\n"
MF "v[80:83],v[24:27],v[184:187],v[80:83]\n"
"v_exp_f32 v127,v127\n"
MF "v[80:83],v[28:31],v[204:207],v[80:83]\n"
"s_add_u32 m0,s65,50944\n"
"v_add_f32 v60,v60,v124\n"
"v_add_f32 v60,v60,v125\n"
MF "v[76:79],v[28:31],v[200:203],v[76:79]\n"
LD "v8,s[36:39],s73 offen offset:3328 nt lds\n"
MF "v[76:79],v[24:27],v[180:183],v[76:79]\n"
CV "v106,v124,v125\n"
"v_add_f32 v60,v60,v126\n"
MF "v[72:75],v[24:27],v[176:179],v[72:75]\n"
"v_add_f32 v60,v60,v127\n"
CV "v107,v126,v127\n"
MF "v[72:75],v[28:31],v[196:199],v[72:75]\n"
"s_waitcnt lgkmcnt(0)\n"
"ds_read_b128 v[24:27],v0 offset:6144\n"
"ds_read_b128 v[28:31],v1 offset:6144\n"
MF "v[72:75],v[16:19],v[216:219],v[72:75]\n"
"v_exp_f32 v128,v128\n"
MF "v[72:75],v[20:23],v[236:239],v[72:75]\n"
"v_exp_f32 v129,v129\n"
MF "v[76:79],v[20:23],v[240:243],v[76:79]\n"
"v_exp_f32 v130,v130\n"
MF "v[76:79],v[16:19],v[220:223],v[76:79]\n"
"v_exp_f32 v131,v131\n"
MF "v[80:83],v[16:19],v[224:227],v[80:83]\n"
"v_add_f32 v61,v61,v128\n"
"v_add_f32 v61,v61,v129\n"
MF "v[80:83],v[20:23],v[244:247],v[80:83]\n"
CV "v110,v128,v129\n"
"v_add_f32 v61,v61,v130\n"
MF "v[84:87],v[20:23],v[248:251],v[84:87]\n"
"v_add_f32 v61,v61,v131\n"
CV "v111,v130,v131\n"
MF "v[84:87],v[16:19],v[228:231],v[84:87]\n"
"v_max3_f32 v10,v57,v58,v59\n"
"v_max3_f32 v10,v10,v60,v61\n"
MF "v[88:91],v[16:19],v[232:235],v[88:91]\n"
"v_cmp_ngt_f32 vcc,s95,v10\n"
MF "v[88:91],v[20:23],v[252:255],v[88:91]\n"
"s_waitcnt lgkmcnt(0)\n"
"s_cbranch_vccnz .Lstub_6\n"
".Lback_6:\n"
MF "v[112:115],v[24:27],v[136:139],v[32:35]\n"
"s_add_u32 s74,s74,12288\n"
"ds_read_b128 v[16:19],v0 offset:7168\n"
"ds_read_b128 v[20:23],v1 offset:7168\n"
MF "v[112:115],v[28:31],v[156:159],v[112:115]\n"
"s_cmp_eq_u32 s74,s20\n"
"s_cselect_b32 s74,s70,s74\n"
MF "v[116:119],v[28:31],v[160:163],v[36:39]\n"
"v_exp_f32 v72,v72\n"
MF "v[116:119],v[24:27],v[140:143],v[116:119]\n"
"v_exp_f32 v73,v73\n"
MF "v[120:123],v[24:27],v[144:147],v[40:43]\n"
"v_exp_f32 v74,v74\n"
MF "v[120:123],v[28:31],v[164:167],v[120:123]\n"
"v_exp_f32 v75,v75\n"
MF "v[124:127],v[28:31],v[168:171],v[44:47]\n"
"v_add_f32 v52,v57,v72\n"
"v_add_f32 v52,v52,v73\n"
MF "v[124:127],v[24:27],v[148:151],v[124:127]\n"
CV "v72,v72,v73\n"
"v_add_f32 v52,v52,v74\n"
MF "v[128:131],v[24:27],v[152:155],v[48:51]\n"
"v_add_f32 v52,v52,v75\n"
CV "v73,v74,v75\n"
MF "v[128:131],v[28:31],v[172:175],v[128:131]\n"
"s_waitcnt lgkmcnt(0)\n"
"ds_read_b128 v[24:27],v0 offset:8192\n"
"ds_read_b128 v[28:31],v1 offset:8192\n"
MF "v[128:131],v[16:19],v[192:195],v[128:131]\n"
"v_exp_f32 v76,v76\n"
MF "v[128:131],v[20:23],v[212:215],v[128:131]\n"
"v_exp_f32 v77,v77\n"
MF "v[124:127],v[20:23],v[208:211],v[124:127]\n"
"v_exp_f32 v78,v78\n"
MF "v[124:127],v[16:19],v[188:191],v[124:127]\n"
"v_exp_f32 v79,v79\n"
MF "v[120:123],v[16:19],v[184:187],v[120:123]\n"
"v_add_f32 v53,v58,v76\n"
"v_add_f32 v53,v53,v77\n"
MF "v[120:123],v[20:23],v[204:207],v[120:123]\n"
CV "v76,v76,v77\n"
"v_add_f32 v53,v53,v78\n"
MF "v[116:119],v[20:23],v[200:203],v[116:119]\n"
"v_add_f32 v53,v53,v79\n"
CV "v77,v78,v79\n"
MF "v[116:119],v[16:19],v[180:183],v[116:119]\n"
"s_waitcnt vmcnt(12) lgkmcnt(0)\n"
"s_barrier\n"
MF "v[112:115],v[16:19],v[176:179],v[112:115]\n"
"v_exp_f32 v80,v80\n"
MF "v[112:115],v[20:23],v[196:199],v[112:115]\n"
"s_nop 0\n"
TR "v[16:17],v2 offset:24576\n"
TR "v[18:19],v2 offset:30720\n"
MF "v[112:115],v[24:27],v[216:219],v[112:115]\n"
"s_add_u32 s72,s72,6144\n"
TR "v[20:21],v3 offset:24576\n"
TR "v[22:23],v3 offset:30720\n"
MF "v[112:115],v[28:31],v[236:239],v[112:115]\n"
"s_cmp_eq_u32 s72,s17\n"
"s_cselect_b32 s72,s16,s72\n"
"s_add_u32 m0,s64,0\n"
MF "v[116:119],v[28:31],v[240:243],v[116:119]\n"
LD "v7,s[32:35],s72 offen nt lds\n"
MF "v[116:119],v[24:27],v[220:223],v[116:119]\n"
"s_add_u32 m0,s64,896\n"
"v_exp_f32 v81,v81\n"
MF "v[120:123],v[24:27],v[224:227],v[120:123]\n"
LD "v7,s[32:35],s72 offen offset:128 nt lds\n"
MF "v[120:123],v[28:31],v[244:247],v[120:123]\n"
"v_exp_f32 v82,v82\n"
MF "v[124:127],v[28:31],v[248:251],v[124:127]\n"
"s_add_u32 m0,s64,1792\n"
"v_exp_f32 v83,v83\n"
MF "v[124:127],v[24:27],v[228:231],v[124:127]\n"
LD "v7,s[32:35],s72 offen offset:256 nt lds\n"
MF "v[128:131],v[24:27],v[232:235],v[128:131]\n"
"s_add_u32 s72,s72,6144\n"
"v_add_f32 v54,v59,v80\n"
"v_add_f32 v54,v54,v81\n"
MF "v[128:131],v[28:31],v[252:255],v[128:131]\n"
"s_waitcnt lgkmcnt(0)\n"
TR "v[24:25],v4 offset:24576\n"
TR "v[26:27],v4 offset:30720\n"
MF "a[0:3],v[16:19],v[92:95],a[0:3]\n"
"s_add_u32 m0,s64,6144\n"
TR "v[28:29],v5 offset:24576\n"
TR "v[30:31],v5 offset:30720\n"
MF "a[20:23],v[20:23],v[92:95],a[20:23]\n"
LD "v7,s[32:35],s72 offen nt lds\n"
MF "a[24:27],v[20:23],v[96:99],a[24:27]\n"
CV "v80,v80,v81\n"
"v_add_f32 v54,v54,v82\n"
MF "a[4:7],v[16:19],v[96:99],a[4:7]\n"
"s_add_u32 m0,s64,7040\n"
"v_add_f32 v54,v54,v83\n"
CV "v81,v82,v83\n"
MF "a[8:11],v[16:19],v[100:103],a[8:11]\n"
LD "v7,s[32:35],s72 offen offset:128 nt lds\n"
MF "a[28:31],v[20:23],v[100:103],a[28:31]\n"
"v_exp_f32 v84,v84\n"
MF "a[32:35],v[20:23],v[104:107],a[32:35]\n"
"s_add_u32 m0,s64,7936\n"
"v_exp_f32 v85,v85\n"
MF "a[12:15],v[16:19],v[104:107],a[12:15]\n"
LD "v7,s[32:35],s72 offen offset:256 nt lds\n"
MF "a[16:19],v[16:19],v[108:111],a[16:19]\n"
"v_exp_f32 v86,v86\n"
MF "a[36:39],v[20:23],v[108:111],a[36:39]\n"
"s_waitcnt lgkmcnt(0)\n"
TR "v[16:17],v2 offset:25600\n"
TR "v[18:19],v2 offset:31744\n"
MF "a[56:59],v[24:27],v[108:111],a[56:59]\n"
"s_nop 0\n"
TR "v[20:21],v3 offset:25600\n"
TR "v[22:23],v3 offset:31744\n"
MF "a[76:79],v[28:31],v[108:111],a[76:79]\n"
"v_exp_f32 v87,v87\n"
MF "a[72:75],v[28:31],v[104:107],a[72:75]\n"
"v_add_f32 v55,v60,v84\n"
"v_add_f32 v55,v55,v85\n"
MF "a[52:55],v[24:27],v[104:107],a[52:55]\n"
CV "v84,v84,v85\n"
"v_add_f32 v55,v55,v86\n"
MF "a[48:51],v[24:27],v[100:103],a[48:51]\n"
"v_add_f32 v55,v55,v87\n"
CV "v85,v86,v87\n"
MF "a[68:71],v[28:31],v[100:103],a[68:71]\n"
"v_exp_f32 v88,v88\n"
MF "a[64:67],v[28:31],v[96:99],a[64:67]\n"
"v_exp_f32 v89,v89\n"
MF "a[44:47],v[24:27],v[96:99],a[44:47]\n"
"v_exp_f32 v90,v90\n"
MF "a[40:43],v[24:27],v[92:95],a[40:43]\n"
"v_exp_f32 v91,v91\n"
MF "a[60:63],v[28:31],v[92:95],a[60:63]\n"
"s_waitcnt lgkmcnt(0)\n"
TR "v[24:25],v4 offset:25600\n"
TR "v[26:27],v4 offset:31744\n"
MF "a[80:83],v[16:19],v[92:95],a[80:83]\n"
"s_nop 0\n"
TR "v[28:29],v5 offset:25600\n"
TR "v[30:31],v5 offset:31744\n"
MF "a[100:103],v[20:23],v[92:95],a[100:103]\n"
"v_add_f32 v56,v61,v88\n"
"v_add_f32 v56,v56,v89\n"
MF "a[104:107],v[20:23],v[96:99],a[104:107]\n"
CV "v88,v88,v89\n"
"v_add_f32 v56,v56,v90\n"
MF "a[84:87],v[16:19],v[96:99],a[84:87]\n"
"v_add_f32 v56,v56,v91\n"
CV "v89,v90,v91\n"
MF "a[88:91],v[16:19],v[100:103],a[88:91]\n"
"v_exp_f32 v112,v112\n"
MF "a[108:111],v[20:23],v[100:103],a[108:111]\n"
"v_exp_f32 v113,v113\n"
MF "a[112:115],v[20:23],v[104:107],a[112:115]\n"
"v_exp_f32 v114,v114\n"
MF "a[92:95],v[16:19],v[104:107],a[92:95]\n"
"v_exp_f32 v115,v115\n"
MF "a[96:99],v[16:19],v[108:111],a[96:99]\n"
"v_add_f32 v52,v52,v112\n"
"v_add_f32 v52,v52,v113\n"
MF "a[116:119],v[20:23],v[108:111],a[116:119]\n"
"s_waitcnt lgkmcnt(0)\n"
TR "v[16:17],v2 offset:26624\n"
TR "v[18:19],v2 offset:32768\n"
MF "a[136:139],v[24:27],v[108:111],a[136:139]\n"
"s_nop 0\n"
TR "v[20:21],v3 offset:26624\n"
TR "v[22:23],v3 offset:32768\n"
MF "a[156:159],v[28:31],v[108:111],a[156:159]\n"
CV "v74,v112,v113\n"
"v_add_f32 v52,v52,v114\n"
MF "a[152:155],v[28:31],v[104:107],a[152:155]\n"
"v_add_f32 v52,v52,v115\n"
CV "v75,v114,v115\n"
MF "a[132:135],v[24:27],v[104:107],a[132:135]\n"
"v_exp_f32 v116,v116\n"
MF "a[128:131],v[24:27],v[100:103],a[128:131]\n"
"v_exp_f32 v117,v117\n"
MF "a[148:151],v[28:31],v[100:103],a[148:151]\n"
"v_exp_f32 v118,v118\n"
MF "a[144:147],v[28:31],v[96:99],a[144:147]\n"
"v_exp_f32 v119,v119\n"
MF "a[124:127],v[24:27],v[96:99],a[124:127]\n"
"v_add_f32 v53,v53,v116\n"
"v_add_f32 v53,v53,v117\n"
MF "a[120:123],v[24:27],v[92:95],a[120:123]\n"
CV "v78,v116,v117\n"
"v_add_f32 v53,v53,v118\n"
MF "a[140:143],v[28:31],v[92:95],a[140:143]\n"
"s_waitcnt lgkmcnt(0)\n"
TR "v[24:25],v4 offset:26624\n"
TR "v[26:27],v4 offset:32768\n"
MF "a[160:163],v[16:19],v[92:95],a[160:163]\n"
"s_nop 0\n"
TR "v[28:29],v5 offset:26624\n"
TR "v[30:31],v5 offset:32768\n"
MF "a[180:183],v[20:23],v[92:95],a[180:183]\n"
"v_add_f32 v53,v53,v119\n"
CV "v79,v118,v119\n"
MF "a[184:187],v[20:23],v[96:99],a[184:187]\n"
"v_exp_f32 v120,v120\n"
MF "a[164:167],v[16:19],v[96:99],a[164:167]\n"
"v_exp_f32 v121,v121\n"
MF "a[168:171],v[16:19],v[100:103],a[168:171]\n"
"v_exp_f32 v122,v122\n"
MF "a[188:191],v[20:23],v[100:103],a[188:191]\n"
"v_exp_f32 v123,v123\n"
MF "a[192:195],v[20:23],v[104:107],a[192:195]\n"
"v_add_f32 v54,v54,v120\n"
"v_add_f32 v54,v54,v121\n"
MF "a[172:175],v[16:19],v[104:107],a[172:175]\n"
CV "v82,v120,v121\n"
"v_add_f32 v54,v54,v122\n"
MF "a[176:179],v[16:19],v[108:111],a[176:179]\n"
"s_waitcnt vmcnt(12) lgkmcnt(0)\n"
"s_barrier\n"
MF "a[196:199],v[20:23],v[108:111],a[196:199]\n"
"s_add_u32 s73,s73,12288\n"
"ds_read_b128 v[16:19],v0 offset:24576\n"
"ds_read_b128 v[20:23],v1 offset:24576\n"
MF "a[216:219],v[24:27],v[108:111],a[216:219]\n"
"s_cmp_eq_u32 s73,s19\n"
"s_cselect_b32 s73,s18,s73\n"
"s_add_u32 m0,s65,73728\n"
MF "a[236:239],v[28:31],v[108:111],a[236:239]\n"
LD "v8,s[36:39],s73 offen nt lds\n"
MF "a[232:235],v[28:31],v[104:107],a[232:235]\n"
"s_add_u32 m0,s65,74624\n"
"v_add_f32 v54,v54,v123\n"
CV "v83,v122,v123\n"
MF "a[212:215],v[24:27],v[104:107],a[212:215]\n"
LD "v8,s[36:39],s73 offen offset:128 nt lds\n"
MF "a[208:211],v[24:27],v[100:103],a[208:211]\n"
"s_add_u32 m0,s65,75520\n"
MF "a[228:231],v[28:31],v[100:103],a[228:231]\n"
LD "v8,s[36:39],s73 offen offset:256 nt lds\n"
MF "a[224:227],v[28:31],v[96:99],a[224:227]\n"
MF "a[204:207],v[24:27],v[96:99],a[204:207]\n"
MF "a[200:203],v[24:27],v[92:95],a[200:203]\n"
MF "a[220:223],v[28:31],v[92:95],a[220:223]\n"
"s_waitcnt lgkmcnt(0)\n"
MF "v[92:95],v[16:19],v[136:139],v[32:35]\n"
"s_add_u32 m0,s65,73728\n"
"ds_read_b128 v[24:27],v0 offset:25600\n"
"ds_read_b128 v[28:31],v1 offset:25600\n"
MF "v[92:95],v[20:23],v[156:159],v[92:95]\n"
LD "v8,s[36:39],s73 offen offset:3072 nt lds\n"
MF "v[96:99],v[20:23],v[160:163],v[36:39]\n"
"v_exp_f32 v124,v124\n"
MF "v[96:99],v[16:19],v[140:143],v[96:99]\n"
"v_exp_f32 v125,v125\n"
MF "v[100:103],v[16:19],v[144:147],v[40:43]\n"
"s_add_u32 m0,s65,74624\n"
"v_exp_f32 v126,v126\n"
MF "v[100:103],v[20:23],v[164:167],v[100:103]\n"
LD "v8,s[36:39],s73 offen offset:3200 nt lds\n"
MF "v[104:107],v[20:23],v[168:171],v[44:47]\n"
"v_exp_f32 v127,v127\n"
MF "v[104:107],v[16:19],v[148:151],v[104:107]\n"
"s_add_u32 m0,s65,75520\n"
"v_add_f32 v55,v55,v124\n"
"v_add_f32 v55,v55,v125\n"
MF "v[108:111],v[16:19],v[152:155],v[48:51]\n"
LD "v8,s[36:39],s73 offen offset:3328 nt lds\n"
MF "v[108:111],v[20:23],v[172:175],v[108:111]\n"
"s_waitcnt lgkmcnt(0)\n"
"ds_read_b128 v[16:19],v0 offset:26624\n"
"ds_read_b128 v[20:23],v1 offset:26624\n"
MF "v[108:111],v[24:27],v[192:195],v[108:111]\n"
CV "v86,v124,v125\n"
"v_add_f32 v55,v55,v126\n"
MF "v[108:111],v[28:31],v[212:215],v[108:111]\n"
"v_add_f32 v55,v55,v127\n"
CV "v87,v126,v127\n"
MF "v[104:107],v[28:31],v[208:211],v[104:107]\n"
"v_exp_f32 v128,v128\n"
MF "v[104:107],v[24:27],v[188:191],v[104:107]\n"
"v_exp_f32 v129,v129\n"
MF "v[100:103],v[24:27],v[184:187],v[100:103]\n"
"v_exp_f32 v130,v130\n"
MF "v[100:103],v[28:31],v[204:207],v[100:103]\n"
"v_exp_f32 v131,v131\n"
MF "v[96:99],v[28:31],v[200:203],v[96:99]\n"
"v_add_f32 v56,v56,v128\n"
"v_add_f32 v56,v56,v129\n"
MF "v[96:99],v[24:27],v[180:183],v[96:99]\n"
CV "v90,v128,v129\n"
"v_add_f32 v56,v56,v130\n"
MF "v[92:95],v[24:27],v[176:179],v[92:95]\n"
"v_add_f32 v56,v56,v131\n"
CV "v91,v130,v131\n"
MF "v[92:95],v[28:31],v[196:199],v[92:95]\n"
"s_waitcnt lgkmcnt(0)\n"
"ds_read_b128 v[24:27],v0 offset:30720\n"
"ds_read_b128 v[28:31],v1 offset:30720\n"
MF "v[92:95],v[16:19],v[216:219],v[92:95]\n"
MF "v[92:95],v[20:23],v[236:239],v[92:95]\n"
MF "v[96:99],v[20:23],v[240:243],v[96:99]\n"
MF "v[96:99],v[16:19],v[220:223],v[96:99]\n"
MF "v[100:103],v[16:19],v[224:227],v[100:103]\n"
MF "v[100:103],v[20:23],v[244:247],v[100:103]\n"
MF "v[104:107],v[20:23],v[248:251],v[104:107]\n"
MF "v[104:107],v[16:19],v[228:231],v[104:107]\n"
"v_max3_f32 v10,v52,v53,v54\n"
"v_max3_f32 v10,v10,v55,v56\n"
MF "v[108:111],v[16:19],v[232:235],v[108:111]\n"
"v_cmp_ngt_f32 vcc,s95,v10\n"
MF "v[108:111],v[20:23],v[252:255],v[108:111]\n"
"s_waitcnt lgkmcnt(0)\n"
"s_cbranch_vccnz .Lstub_7\n"
".Lback_7:\n"
MF "v[112:115],v[24:27],v[136:139],v[32:35]\n"
"s_add_u32 s74,s74,12288\n"
"ds_read_b128 v[16:19],v0 offset:31744\n"
"ds_read_b128 v[20:23],v1 offset:31744\n"
MF "v[112:115],v[28:31],v[156:159],v[112:115]\n"
"s_cmp_eq_u32 s74,s20\n"
"s_cselect_b32 s74,s70,s74\n"
MF "v[116:119],v[28:31],v[160:163],v[36:39]\n"
"v_exp_f32 v92,v92\n"
MF "v[116:119],v[24:27],v[140:143],v[116:119]\n"
"v_exp_f32 v93,v93\n"
MF "v[120:123],v[24:27],v[144:147],v[40:43]\n"
"v_exp_f32 v94,v94\n"
MF "v[120:123],v[28:31],v[164:167],v[120:123]\n"
"v_exp_f32 v95,v95\n"
MF "v[124:127],v[28:31],v[168:171],v[44:47]\n"
"v_add_f32 v57,v52,v92\n"
"v_add_f32 v57,v57,v93\n"
MF "v[124:127],v[24:27],v[148:151],v[124:127]\n"
CV "v92,v92,v93\n"
"v_add_f32 v57,v57,v94\n"
MF "v[128:131],v[24:27],v[152:155],v[48:51]\n"
"v_add_f32 v57,v57,v95\n"
CV "v93,v94,v95\n"
MF "v[128:131],v[28:31],v[172:175],v[128:131]\n"
"s_waitcnt lgkmcnt(0)\n"
"ds_read_b128 v[24:27],v0 offset:32768\n"
"ds_read_b128 v[28:31],v1 offset:32768\n"
MF "v[128:131],v[16:19],v[192:195],v[128:131]\n"
"s_sub_u32 s61,s61,1\n"
"v_exp_f32 v96,v96\n"
MF "v[128:131],v[20:23],v[212:215],v[128:131]\n"
"s_cmp_eq_u32 s61,1\n"
"s_cselect_b32 s95,0,s95\n"
"v_add_f32 v58,v53,v96\n"
MF "v[124:127],v[20:23],v[208:211],v[124:127]\n"
"v_exp_f32 v97,v97\n"
MF "v[124:127],v[16:19],v[188:191],v[124:127]\n"
"v_exp_f32 v98,v98\n"
MF "v[120:123],v[16:19],v[184:187],v[120:123]\n"
"v_exp_f32 v99,v99\n"
MF "v[120:123],v[20:23],v[204:207],v[120:123]\n"
"v_add_f32 v58,v58,v97\n"
CV "v96,v96,v97\n"
MF "v[116:119],v[20:23],v[200:203],v[116:119]\n"
"v_add_f32 v58,v58,v98\n"
"v_add_f32 v58,v58,v99\n"
MF "v[116:119],v[16:19],v[180:183],v[116:119]\n"
"s_waitcnt vmcnt(12) lgkmcnt(0)\n"
"s_barrier\n"
MF "v[112:115],v[16:19],v[176:179],v[112:115]\n"
CV "v97,v98,v99\n"
MF "v[112:115],v[20:23],v[196:199],v[112:115]\n"
"s_nop 0\n"
TR "v[16:17],v2\n"
TR "v[18:19],v2 offset:6144\n"
MF "v[112:115],v[24:27],v[216:219],v[112:115]\n"
"s_add_u32 s72,s72,6144\n"
TR "v[20:21],v3\n"
TR "v[22:23],v3 offset:6144\n"
MF "v[112:115],v[28:31],v[236:239],v[112:115]\n"
"s_cmp_eq_u32 s72,s17\n"
"s_cselect_b32 s72,s16,s72\n"
"s_add_u32 m0,s64,24576\n"
MF "v[116:119],v[28:31],v[240:243],v[116:119]\n"
LD "v7,s[32:35],s72 offen nt lds\n"
MF "v[116:119],v[24:27],v[220:223],v[116:119]\n"
"s_add_u32 m0,s64,25472\n"
"v_exp_f32 v100,v100\n"
MF "v[120:123],v[24:27],v[224:227],v[120:123]\n"
LD "v7,s[32:35],s72 offen offset:128 nt lds\n"
MF "v[120:123],v[28:31],v[244:247],v[120:123]\n"
"v_exp_f32 v101,v101\n"
MF "v[124:127],v[28:31],v[248:251],v[124:127]\n"
"s_add_u32 m0,s64,26368\n"
"v_exp_f32 v102,v102\n"
MF "v[124:127],v[24:27],v[228:231],v[124:127]\n"
LD "v7,s[32:35],s72 offen offset:256 nt lds\n"
MF "v[128:131],v[24:27],v[232:235],v[128:131]\n"
"s_add_u32 s72,s72,6144\n"
"v_exp_f32 v103,v103\n"
MF "v[128:131],v[28:31],v[252:255],v[128:131]\n"
"s_waitcnt lgkmcnt(0)\n"
TR "v[24:25],v4\n"
TR "v[26:27],v4 offset:6144\n"
MF "a[0:3],v[16:19],v[72:75],a[0:3]\n"
"s_add_u32 m0,s64,30720\n"
TR "v[28:29],v5\n"
TR "v[30:31],v5 offset:6144\n"
MF "a[20:23],v[20:23],v[72:75],a[20:23]\n"
LD "v7,s[32:35],s72 offen nt lds\n"
MF "a[24:27],v[20:23],v[76:79],a[24:27]\n"
"v_add_f32 v59,v54,v100\n"
"v_add_f32 v59,v59,v101\n"
MF "a[4:7],v[16:19],v[76:79],a[4:7]\n"
"s_add_u32 m0,s64,31616\n"
CV "v100,v100,v101\n"
"v_add_f32 v59,v59,v102\n"
MF "a[8:11],v[16:19],v[80:83],a[8:11]\n"
LD "v7,s[32:35],s72 offen offset:128 nt lds\n"
MF "a[28:31],v[20:23],v[80:83],a[28:31]\n"
"v_add_f32 v59,v59,v103\n"
CV "v101,v102,v103\n"
MF "a[32:35],v[20:23],v[84:87],a[32:35]\n"
"s_add_u32 m0,s64,32512\n"
"v_exp_f32 v104,v104\n"
MF "a[12:15],v[16:19],v[84:87],a[12:15]\n"
LD "v7,s[32:35],s72 offen offset:256 nt lds\n"
MF "a[16:19],v[16:19],v[88:91],a[16:19]\n"
"v_exp_f32 v105,v105\n"
MF "a[36:39],v[20:23],v[88:91],a[36:39]\n"
"s_waitcnt lgkmcnt(0)\n"
TR "v[16:17],v2 offset:1024\n"
TR "v[18:19],v2 offset:7168\n"
MF "a[56:59],v[24:27],v[88:91],a[56:59]\n"
"s_nop 0\n"
TR "v[20:21],v3 offset:1024\n"
TR "v[22:23],v3 offset:7168\n"
MF "a[76:79],v[28:31],v[88:91],a[76:79]\n"
"v_exp_f32 v106,v106\n"
MF "a[72:75],v[28:31],v[84:87],a[72:75]\n"
"v_exp_f32 v107,v107\n"
MF "a[52:55],v[24:27],v[84:87],a[52:55]\n"
"v_add_f32 v60,v55,v104\n"
"v_add_f32 v60,v60,v105\n"
MF "a[48:51],v[24:27],v[80:83],a[48:51]\n"
CV "v104,v104,v105\n"
"v_add_f32 v60,v60,v106\n"
MF "a[68:71],v[28:31],v[80:83],a[68:71]\n"
"v_add_f32 v60,v60,v107\n"
CV "v105,v106,v107\n"
MF "a[64:67],v[28:31],v[76:79],a[64:67]\n"
".Lpg11b:\n"
"s_cbranch_execz .Lpgqj_a\n"
".Lpg11d:\n"
"s_cbranch_execz .Lisr2\n"
"v_exp_f32 v108,v108\n"
MF "a[44:47],v[24:27],v[76:79],a[44:47]\n"
"v_exp_f32 v109,v109\n"
MF "a[40:43],v[24:27],v[72:75],a[40:43]\n"
"v_exp_f32 v110,v110\n"
MF "a[60:63],v[28:31],v[72:75],a[60:63]\n"
"s_waitcnt lgkmcnt(0)\n"
TR "v[24:25],v4 offset:1024\n"
TR "v[26:27],v4 offset:7168\n"
MF "a[80:83],v[16:19],v[72:75],a[80:83]\n"
"s_nop 0\n"
TR "v[28:29],v5 offset:1024\n"
TR "v[30:31],v5 offset:7168\n"
MF "a[100:103],v[20:23],v[72:75],a[100:103]\n"
"v_exp_f32 v111,v111\n"
MF "a[104:107],v[20:23],v[76:79],a[104:107]\n"
"v_add_f32 v61,v56,v108\n"
"v_add_f32 v61,v61,v109\n"
MF "a[84:87],v[16:19],v[76:79],a[84:87]\n"
CV "v108,v108,v109\n"
"v_add_f32 v61,v61,v110\n"
MF "a[88:91],v[16:19],v[80:83],a[88:91]\n"
"v_add_f32 v61,v61,v111\n"
CV "v109,v110,v111\n"
MF "a[108:111],v[20:23],v[80:83],a[108:111]\n"
"v_exp_f32 v112,v112\n"
MF "a[112:115],v[20:23],v[84:87],a[112:115]\n"
"v_exp_f32 v113,v113\n"
MF "a[92:95],v[16:19],v[84:87],a[92:95]\n"
"v_exp_f32 v114,v114\n"
MF "a[96:99],v[16:19],v[88:91],a[96:99]\n"
"v_exp_f32 v115,v115\n"
MF "a[116:119],v[20:23],v[88:91],a[116:119]\n"
"s_waitcnt lgkmcnt(0)\n"
TR "v[16:17],v2 offset:2048\n"
TR "v[18:19],v2 offset:8192\n"
MF "a[136:139],v[24:27],v[88:91],a[136:139]\n"
"s_nop 0\n"
TR "v[20:21],v3 offset:2048\n"
TR "v[22:23],v3 offset:8192\n"
MF "a[156:159],v[28:31],v[88:91],a[156:159]\n"
"v_add_f32 v57,v57,v112\n"
"v_add_f32 v57,v57,v113\n"
MF "a[152:155],v[28:31],v[84:87],a[152:155]\n"
CV "v94,v112,v113\n"
"v_add_f32 v57,v57,v114\n"
MF "a[132:135],v[24:27],v[84:87],a[132:135]\n"
"v_add_f32 v57,v57,v115\n"
CV "v95,v114,v115\n"
MF "a[128:131],v[24:27],v[80:83],a[128:131]\n"
"v_exp_f32 v116,v116\n"
MF "a[148:151],v[28:31],v[80:83],a[148:151]\n"
"v_exp_f32 v117,v117\n"
MF "a[144:147],v[28:31],v[76:79],a[144:147]\n"
"v_exp_f32 v118,v118\n"
MF "a[124:127],v[24:27],v[76:79],a[124:127]\n"
"v_exp_f32 v119,v119\n"
MF "a[120:123],v[24:27],v[72:75],a[120:123]\n"
"v_add_f32 v58,v58,v116\n"
"v_add_f32 v58,v58,v117\n"
MF "a[140:143],v[28:31],v[72:75],a[140:143]\n"
"s_waitcnt lgkmcnt(0)\n"
TR "v[24:25],v4 offset:2048\n"
TR "v[26:27],v4 offset:8192\n"
MF "a[160:163],v[16:19],v[72:75],a[160:163]\n"
"s_nop 0\n"
TR "v[28:29],v5 offset:2048\n"
TR "v[30:31],v5 offset:8192\n"
MF "a[180:183],v[20:23],v[72:75],a[180:183]\n"
CV "v98,v116,v117\n"
"v_add_f32 v58,v58,v118\n"
MF "a[184:187],v[20:23],v[76:79],a[184:187]\n"
"v_add_f32 v58,v58,v119\n"
CV "v99,v118,v119\n"
MF "a[164:167],v[16:19],v[76:79],a[164:167]\n"
"v_exp_f32 v120,v120\n"
MF "a[168:171],v[16:19],v[80:83],a[168:171]\n"
"v_exp_f32 v121,v121\n"
MF "a[188:191],v[20:23],v[80:83],a[188:191]\n"
"v_exp_f32 v122,v122\n"
MF "a[192:195],v[20:23],v[84:87],a[192:195]\n"
"v_exp_f32 v123,v123\n"
MF "a[172:175],v[16:19],v[84:87],a[172:175]\n"
"v_add_f32 v59,v59,v120\n"
"v_add_f32 v59,v59,v121\n"
MF "a[176:179],v[16:19],v[88:91],a[176:179]\n"
"s_waitcnt vmcnt(12) lgkmcnt(0)\n"
"s_barrier\n"
MF "a[196:199],v[20:23],v[88:91],a[196:199]\n"
"s_add_u32 s73,s73,12288\n"
"ds_read_b128 v[16:19],v0\n"
"ds_read_b128 v[20:23],v1\n"
MF "a[216:219],v[24:27],v[88:91],a[216:219]\n"
"s_cmp_eq_u32 s73,s19\n"
"s_cselect_b32 s73,s18,s73\n"
"s_add_u32 m0,s65,49152\n"
MF "a[236:239],v[28:31],v[88:91],a[236:239]\n"
LD "v8,s[36:39],s73 offen nt lds\n"
MF "a[232:235],v[28:31],v[84:87],a[232:235]\n"
"s_add_u32 m0,s65,50048\n"
CV "v102,v120,v121\n"
"v_add_f32 v59,v59,v122\n"
MF "a[212:215],v[24:27],v[84:87],a[212:215]\n"
LD "v8,s[36:39],s73 offen offset:128 nt lds\n"
MF "a[208:211],v[24:27],v[80:83],a[208:211]\n"
"s_add_u32 m0,s65,50944\n"
"v_add_f32 v59,v59,v123\n"
CV "v103,v122,v123\n"
MF "a[228:231],v[28:31],v[80:83],a[228:231]\n"
LD "v8,s[36:39],s73 offen offset:256 nt lds\n"
MF "a[224:227],v[28:31],v[76:79],a[224:227]\n"
MF "a[204:207],v[24:27],v[76:79],a[204:207]\n"
"s_waitcnt lgkmcnt(0)\n"
MF "a[200:203],v[24:27],v[72:75],a[200:203]\n"
"s_cmp_lg_u32 s61,0\n"
MF "a[220:223],v[28:31],v[72:75],a[220:223]\n"
"s_cbranch_scc1 .Laloopx\n"
".Lepi1:\n"
"v_exp_f32 v124,v124\n"
"v_exp_f32 v125,v125\n"
"v_exp_f32 v126,v126\n"
"v_exp_f32 v127,v127\n"
"v_exp_f32 v128,v128\n"
"v_exp_f32 v129,v129\n"
"v_exp_f32 v130,v130\n"
"v_exp_f32 v131,v131\n"
"v_add_f32 v60,v60,v124\n"
"v_add_f32 v61,v61,v128\n"
"v_add_f32 v60,v60,v125\n"
"v_add_f32 v61,v61,v129\n"
"v_add_f32 v60,v60,v126\n"
"v_add_f32 v61,v61,v130\n"
"v_add_f32 v60,v60,v127\n"
"v_add_f32 v61,v61,v131\n"
CV "v106,v124,v125\n"
CV "v107,v126,v127\n"
CV "v110,v128,v129\n"
CV "v111,v130,v131\n"
"v_max3_f32 v10,v57,v58,v59\n"
"v_max3_f32 v10,v10,v60,v61\n"
"v_cmp_le_f32 vcc,0x47800000,v10\n"
"s_cbranch_vccnz .Lstub_8\n"
".Lback_8:\n"
"s_waitcnt vmcnt(9) lgkmcnt(0)\n"
"s_barrier\n"
"s_cmp_eq_u32 s57,0\n"
"s_cbranch_scc0 .Lep\n"
".Lepx:\n"
"s_add_u32 s66,s67,4718592\n"
"s_sub_u32 s81,s81,384\n"
"s_mov_b32 s83,7077888\n"
AR "v122,a243\n"
"v_add_u32 v123,97280,v122\n"
"v_add_u32 v124,194560,v122\n"
"v_add_u32 v125,291840,v122\n"
"v_mov_b32 v10,1.0\n"
TR "v[136:137],v2 offset:24576\n"
TR "v[138:139],v2 offset:30720\n"
"v_mfma_f32_16x16x4_f32 v[16:19],v10,v57,0\n"
TR "v[140:141],v3 offset:24576\n"
TR "v[142:143],v3 offset:30720\n"
TR "v[144:145],v4 offset:24576\n"
TR "v[146:147],v4 offset:30720\n"
TR "v[148:149],v5 offset:24576\n"
TR "v[150:151],v5 offset:30720\n"
"v_mfma_f32_16x16x4_f32 v[20:23],v10,v58,0\n"
TR "v[152:153],v2 offset:25600\n"
TR "v[154:155],v2 offset:31744\n"
TR "v[156:157],v3 offset:25600\n"
TR "v[158:159],v3 offset:31744\n"
TR "v[160:161],v4 offset:25600\n"
TR "v[162:163],v4 offset:31744\n"
"v_mfma_f32_16x16x4_f32 v[24:27],v10,v59,0\n"
TR "v[164:165],v5 offset:25600\n"
TR "v[166:167],v5 offset:31744\n"
TR "v[168:169],v2 offset:26624\n"
TR "v[170:171],v2 offset:32768\n"
TR "v[172:173],v3 offset:26624\n"
TR "v[174:175],v3 offset:32768\n"
"v_mfma_f32_16x16x4_f32 v[28:31],v10,v60,0\n"
TR "v[176:177],v4 offset:26624\n"
TR "v[178:179],v4 offset:32768\n"
TR "v[180:181],v5 offset:26624\n"
TR "v[182:183],v5 offset:32768\n"
"v_mfma_f32_16x16x4_f32 v[32:35],v10,v61,0\n"
"s_add_u32 m0,s62,0\n"
"s_add_u32 s82,s66,2359296\n"
"s_sub_u32 s15,s82,7864320\n"
"s_min_u32 s82,s82,s15\n"
LD "v122,s[28:31],s82 offen lds\n"
LD "v123,s[28:31],s82 offen offset:1024 lds\n"
"s_waitcnt lgkmcnt(15)\n"
MF "a[0:3],v[136:139],v[92:95],a[0:3]\n"
MF "a[20:23],v[140:143],v[92:95],a[20:23]\n"
LD "v124,s[28:31],s82 offen offset:2048 lds\n"
LD "v125,s[28:31],s82 offen offset:3072 lds\n"
"v_rcp_f32 v57,v16\n"
"v_rcp_f32 v58,v20\n"
"v_rcp_f32 v59,v24\n"
"v_rcp_f32 v60,v28\n"
"v_rcp_f32 v61,v32\n"
MF "a[4:7],v[136:139],v[96:99],a[4:7]\n"
"v_mul_f32_e64 v52,-s13,v57\n"
"v_mul_f32_e64 v53,-s13,v58\n"
"v_mul_f32_e64 v54,-s13,v59\n"
"v_mul_f32_e64 v55,-s13,v60\n"
"v_mul_f32_e64 v56,-s13,v61\n"
AR "v72,a0\n"
AR "v73,a1\n"
AR "v74,a2\n"
MF "a[24:27],v[140:143],v[96:99],a[24:27]\n"
AR "v75,a3\n"
AR "v76,a20\n"
AR "v77,a21\n"
AR "v78,a22\n"
AR "v79,a23\n"
"v_pk_fma_f32 v[72:73],v[72:73],v[56:57],v[52:53] op_sel:[0,1,0] op_sel_hi:[1,1,0]\n"
"v_pk_fma_f32 v[74:75],v[74:75],v[56:57],v[52:53] op_sel:[0,1,0] op_sel_hi:[1,1,0]\n"
"v_pk_fma_f32 v[76:77],v[76:77],v[56:57],v[52:53] op_sel:[0,1,0] op_sel_hi:[1,1,0]\n"
"v_pk_fma_f32 v[78:79],v[78:79],v[56:57],v[52:53] op_sel:[0,1,0] op_sel_hi:[1,1,0]\n"
CV "v112,v72,v73\n"
CV "v113,v74,v75\n"
CV "v114,v76,v77\n"
CV "v115,v78,v79\n"
MF "a[8:11],v[136:139],v[100:103],a[8:11]\n"
AW "a0,v112\n"
AW "a1,v113\n"
AW "a2,v114\n"
AW "a3,v115\n"
"s_add_u32 m0,s62,16384\n"
"s_add_u32 s82,s66,2359424\n"
"s_sub_u32 s15,s82,7864320\n"
"s_min_u32 s82,s82,s15\n"
"s_nop 0\n"
LD "v122,s[28:31],s82 offen lds\n"
AR "v80,a4\n"
AR "v81,a5\n"
AR "v82,a6\n"
MF "a[28:31],v[140:143],v[100:103],a[28:31]\n"
AR "v83,a7\n"
AR "v84,a24\n"
AR "v85,a25\n"
AR "v86,a26\n"
AR "v87,a27\n"
"v_pk_fma_f32 v[80:81],v[80:81],v[58:59],v[52:53] op_sel:[0,0,1] op_sel_hi:[1,0,1]\n"
"v_pk_fma_f32 v[82:83],v[82:83],v[58:59],v[52:53] op_sel:[0,0,1] op_sel_hi:[1,0,1]\n"
"v_pk_fma_f32 v[84:85],v[84:85],v[58:59],v[52:53] op_sel:[0,0,1] op_sel_hi:[1,0,1]\n"
"v_pk_fma_f32 v[86:87],v[86:87],v[58:59],v[52:53] op_sel:[0,0,1] op_sel_hi:[1,0,1]\n"
CV "v116,v80,v81\n"
CV "v117,v82,v83\n"
CV "v118,v84,v85\n"
CV "v119,v86,v87\n"
MF "a[12:15],v[136:139],v[104:107],a[12:15]\n"
AW "a4,v116\n"
AW "a5,v117\n"
AW "a6,v118\n"
AW "a7,v119\n"
LD "v123,s[28:31],s82 offen offset:1024 lds\n"
AR "v72,a8\n"
AR "v73,a9\n"
AR "v74,a10\n"
MF "a[32:35],v[140:143],v[104:107],a[32:35]\n"
AR "v75,a11\n"
AR "v76,a28\n"
AR "v77,a29\n"
AR "v78,a30\n"
AR "v79,a31\n"
"v_pk_fma_f32 v[72:73],v[72:73],v[58:59],v[54:55] op_sel:[0,1,0] op_sel_hi:[1,1,0]\n"
"v_pk_fma_f32 v[74:75],v[74:75],v[58:59],v[54:55] op_sel:[0,1,0] op_sel_hi:[1,1,0]\n"
"v_pk_fma_f32 v[76:77],v[76:77],v[58:59],v[54:55] op_sel:[0,1,0] op_sel_hi:[1,1,0]\n"
"v_pk_fma_f32 v[78:79],v[78:79],v[58:59],v[54:55] op_sel:[0,1,0] op_sel_hi:[1,1,0]\n"
CV "v112,v72,v73\n"
CV "v113,v74,v75\n"
CV "v114,v76,v77\n"
CV "v115,v78,v79\n"
MF "a[16:19],v[136:139],v[108:111],a[16:19]\n"
AW "a8,v112\n"
AW "a9,v113\n"
AW "a10,v114\n"
AW "a11,v115\n"
"s_waitcnt lgkmcnt(0)\n"
"s_barrier\n"
AR "v80,a12\n"
AR "v81,a13\n"
AR "v82,a14\n"
MF "a[36:39],v[140:143],v[108:111],a[36:39]\n"
AR "v83,a15\n"
AR "v84,a32\n"
AR "v85,a33\n"
AR "v86,a34\n"
AR "v87,a35\n"
"v_pk_fma_f32 v[80:81],v[80:81],v[60:61],v[54:55] op_sel:[0,0,1] op_sel_hi:[1,0,1]\n"
"v_pk_fma_f32 v[82:83],v[82:83],v[60:61],v[54:55] op_sel:[0,0,1] op_sel_hi:[1,0,1]\n"
"v_pk_fma_f32 v[84:85],v[84:85],v[60:61],v[54:55] op_sel:[0,0,1] op_sel_hi:[1,0,1]\n"
"v_pk_fma_f32 v[86:87],v[86:87],v[60:61],v[54:55] op_sel:[0,0,1] op_sel_hi:[1,0,1]\n"
CV "v116,v80,v81\n"
CV "v117,v82,v83\n"
CV "v118,v84,v85\n"
CV "v119,v86,v87\n"
MF "a[40:43],v[144:147],v[92:95],a[40:43]\n"
AW "a12,v116\n"
AW "a13,v117\n"
AW "a14,v118\n"
AW "a15,v119\n"
LD "v124,s[28:31],s82 offen offset:2048 lds\n"
AR "v72,a16\n"
AR "v73,a17\n"
AR "v74,a18\n"
MF "a[60:63],v[148:151],v[92:95],a[60:63]\n"
AR "v75,a19\n"
AR "v76,a36\n"
AR "v77,a37\n"
AR "v78,a38\n"
AR "v79,a39\n"
"v_pk_fma_f32 v[72:73],v[72:73],v[60:61],v[56:57] op_sel:[0,1,0] op_sel_hi:[1,1,0]\n"
"v_pk_fma_f32 v[74:75],v[74:75],v[60:61],v[56:57] op_sel:[0,1,0] op_sel_hi:[1,1,0]\n"
"v_pk_fma_f32 v[76:77],v[76:77],v[60:61],v[56:57] op_sel:[0,1,0] op_sel_hi:[1,1,0]\n"
"v_pk_fma_f32 v[78:79],v[78:79],v[60:61],v[56:57] op_sel:[0,1,0] op_sel_hi:[1,1,0]\n"
CV "v112,v72,v73\n"
CV "v113,v74,v75\n"
CV "v114,v76,v77\n"
CV "v115,v78,v79\n"
MF "a[44:47],v[144:147],v[96:99],a[44:47]\n"
AW "a16,v112\n"
AW "a17,v113\n"
AW "a18,v114\n"
AW "a19,v115\n"
LD "v125,s[28:31],s82 offen offset:3072 lds\n"
AR "v80,a40\n"
AR "v81,a41\n"
AR "v82,a42\n"
MF "a[64:67],v[148:151],v[96:99],a[64:67]\n"
AR "v83,a43\n"
AR "v84,a60\n"
AR "v85,a61\n"
AR "v86,a62\n"
AR "v87,a63\n"
"v_pk_fma_f32 v[80:81],v[80:81],v[56:57],v[52:53] op_sel:[0,1,0] op_sel_hi:[1,1,0]\n"
"v_pk_fma_f32 v[82:83],v[82:83],v[56:57],v[52:53] op_sel:[0,1,0] op_sel_hi:[1,1,0]\n"
"v_pk_fma_f32 v[84:85],v[84:85],v[56:57],v[52:53] op_sel:[0,1,0] op_sel_hi:[1,1,0]\n"
"v_pk_fma_f32 v[86:87],v[86:87],v[56:57],v[52:53] op_sel:[0,1,0] op_sel_hi:[1,1,0]\n"
CV "v116,v80,v81\n"
CV "v117,v82,v83\n"
CV "v118,v84,v85\n"
CV "v119,v86,v87\n"
MF "a[48:51],v[144:147],v[100:103],a[48:51]\n"
AW "a40,v116\n"
AW "a41,v117\n"
AW "a42,v118\n"
AW "a43,v119\n"
AR "v72,a44\n"
AR "v73,a45\n"
AR "v74,a46\n"
MF "a[68:71],v[148:151],v[100:103],a[68:71]\n"
AR "v75,a47\n"
AR "v76,a64\n"
AR "v77,a65\n"
AR "v78,a66\n"
AR "v79,a67\n"
"v_pk_fma_f32 v[72:73],v[72:73],v[58:59],v[52:53] op_sel:[0,0,1] op_sel_hi:[1,0,1]\n"
"v_pk_fma_f32 v[74:75],v[74:75],v[58:59],v[52:53] op_sel:[0,0,1] op_sel_hi:[1,0,1]\n"
"v_pk_fma_f32 v[76:77],v[76:77],v[58:59],v[52:53] op_sel:[0,0,1] op_sel_hi:[1,0,1]\n"
"v_pk_fma_f32 v[78:79],v[78:79],v[58:59],v[52:53] op_sel:[0,0,1] op_sel_hi:[1,0,1]\n"
CV "v112,v72,v73\n"
CV "v113,v74,v75\n"
CV "v114,v76,v77\n"
CV "v115,v78,v79\n"
MF "a[52:55],v[144:147],v[104:107],a[52:55]\n"
AW "a44,v112\n"
AW "a45,v113\n"
AW "a46,v114\n"
AW "a47,v115\n"
"s_add_u32 m0,s62,32768\n"
"s_add_u32 s82,s66,2359552\n"
"s_sub_u32 s15,s82,7864320\n"
"s_min_u32 s82,s82,s15\n"
"s_nop 0\n"
LD "v122,s[28:31],s82 offen lds\n"
AR "v80,a48\n"
AR "v81,a49\n"
AR "v82,a50\n"
MF "a[72:75],v[148:151],v[104:107],a[72:75]\n"
AR "v83,a51\n"
AR "v84,a68\n"
AR "v85,a69\n"
AR "v86,a70\n"
AR "v87,a71\n"
"v_pk_fma_f32 v[80:81],v[80:81],v[58:59],v[54:55] op_sel:[0,1,0] op_sel_hi:[1,1,0]\n"
"v_pk_fma_f32 v[82:83],v[82:83],v[58:59],v[54:55] op_sel:[0,1,0] op_sel_hi:[1,1,0]\n"
"v_pk_fma_f32 v[84:85],v[84:85],v[58:59],v[54:55] op_sel:[0,1,0] op_sel_hi:[1,1,0]\n"
"v_pk_fma_f32 v[86:87],v[86:87],v[58:59],v[54:55] op_sel:[0,1,0] op_sel_hi:[1,1,0]\n"
CV "v116,v80,v81\n"
CV "v117,v82,v83\n"
CV "v118,v84,v85\n"
CV "v119,v86,v87\n"
MF "a[56:59],v[144:147],v[108:111],a[56:59]\n"
AW "a48,v116\n"
AW "a49,v117\n"
AW "a50,v118\n"
AW "a51,v119\n"
LD "v123,s[28:31],s82 offen offset:1024 lds\n"
AR "v72,a52\n"
AR "v73,a53\n"
AR "v74,a54\n"
MF "a[76:79],v[148:151],v[108:111],a[76:79]\n"
AR "v75,a55\n"
AR "v76,a72\n"
AR "v77,a73\n"
AR "v78,a74\n"
AR "v79,a75\n"
"v_pk_fma_f32 v[72:73],v[72:73],v[60:61],v[54:55] op_sel:[0,0,1] op_sel_hi:[1,0,1]\n"
"v_pk_fma_f32 v[74:75],v[74:75],v[60:61],v[54:55] op_sel:[0,0,1] op_sel_hi:[1,0,1]\n"
"v_pk_fma_f32 v[76:77],v[76:77],v[60:61],v[54:55] op_sel:[0,0,1] op_sel_hi:[1,0,1]\n"
"v_pk_fma_f32 v[78:79],v[78:79],v[60:61],v[54:55] op_sel:[0,0,1] op_sel_hi:[1,0,1]\n"
CV "v112,v72,v73\n"
CV "v113,v74,v75\n"
CV "v114,v76,v77\n"
CV "v115,v78,v79\n"
MF "a[80:83],v[152:155],v[92:95],a[80:83]\n"
AW "a52,v112\n"
AW "a53,v113\n"
AW "a54,v114\n"
AW "a55,v115\n"
AR "v80,a56\n"
AR "v81,a57\n"
AR "v82,a58\n"
MF "a[100:103],v[156:159],v[92:95],a[100:103]\n"
AR "v83,a59\n"
AR "v84,a76\n"
AR "v85,a77\n"
AR "v86,a78\n"
AR "v87,a79\n"
"v_pk_fma_f32 v[80:81],v[80:81],v[60:61],v[56:57] op_sel:[0,1,0] op_sel_hi:[1,1,0]\n"
"v_pk_fma_f32 v[82:83],v[82:83],v[60:61],v[56:57] op_sel:[0,1,0] op_sel_hi:[1,1,0]\n"
"v_pk_fma_f32 v[84:85],v[84:85],v[60:61],v[56:57] op_sel:[0,1,0] op_sel_hi:[1,1,0]\n"
"v_pk_fma_f32 v[86:87],v[86:87],v[60:61],v[56:57] op_sel:[0,1,0] op_sel_hi:[1,1,0]\n"
CV "v116,v80,v81\n"
CV "v117,v82,v83\n"
CV "v118,v84,v85\n"
CV "v119,v86,v87\n"
MF "a[84:87],v[152:155],v[96:99],a[84:87]\n"
AW "a56,v116\n"
AW "a57,v117\n"
AW "a58,v118\n"
AW "a59,v119\n"
LD "v124,s[28:31],s82 offen offset:2048 lds\n"
AR "v72,a80\n"
AR "v73,a81\n"
AR "v74,a82\n"
MF "a[104:107],v[156:159],v[96:99],a[104:107]\n"
AR "v75,a83\n"
AR "v76,a100\n"
AR "v77,a101\n"
AR "v78,a102\n"
AR "v79,a103\n"
"v_pk_fma_f32 v[72:73],v[72:73],v[56:57],v[52:53] op_sel:[0,1,0] op_sel_hi:[1,1,0]\n"
"v_pk_fma_f32 v[74:75],v[74:75],v[56:57],v[52:53] op_sel:[0,1,0] op_sel_hi:[1,1,0]\n"
"v_pk_fma_f32 v[76:77],v[76:77],v[56:57],v[52:53] op_sel:[0,1,0] op_sel_hi:[1,1,0]\n"
"v_pk_fma_f32 v[78:79],v[78:79],v[56:57],v[52:53] op_sel:[0,1,0] op_sel_hi:[1,1,0]\n"
CV "v112,v72,v73\n"
CV "v113,v74,v75\n"
CV "v114,v76,v77\n"
CV "v115,v78,v79\n"
MF "a[88:91],v[152:155],v[100:103],a[88:91]\n"
AW "a80,v112\n"
AW "a81,v113\n"
AW "a82,v114\n"
AW "a83,v115\n"
LD "v125,s[28:31],s82 offen offset:3072 lds\n"
AR "v80,a84\n"
AR "v81,a85\n"
AR "v82,a86\n"
MF "a[108:111],v[156:159],v[100:103],a[108:111]\n"
AR "v83,a87\n"
AR "v84,a104\n"
AR "v85,a105\n"
AR "v86,a106\n"
AR "v87,a107\n"
"v_pk_fma_f32 v[80:81],v[80:81],v[58:59],v[52:53] op_sel:[0,0,1] op_sel_hi:[1,0,1]\n"
"v_pk_fma_f32 v[82:83],v[82:83],v[58:59],v[52:53] op_sel:[0,0,1] op_sel_hi:[1,0,1]\n"
"v_pk_fma_f32 v[84:85],v[84:85],v[58:59],v[52:53] op_sel:[0,0,1] op_sel_hi:[1,0,1]\n"
"v_pk_fma_f32 v[86:87],v[86:87],v[58:59],v[52:53] op_sel:[0,0,1] op_sel_hi:[1,0,1]\n"
CV "v116,v80,v81\n"
CV "v117,v82,v83\n"
CV "v118,v84,v85\n"
CV "v119,v86,v87\n"
MF "a[92:95],v[152:155],v[104:107],a[92:95]\n"
AW "a84,v116\n"
AW "a85,v117\n"
AW "a86,v118\n"
AW "a87,v119\n"
AR "v72,a88\n"
AR "v73,a89\n"
AR "v74,a90\n"
MF "a[112:115],v[156:159],v[104:107],a[112:115]\n"
AR "v75,a91\n"
AR "v76,a108\n"
AR "v77,a109\n"
AR "v78,a110\n"
AR "v79,a111\n"
"v_pk_fma_f32 v[72:73],v[72:73],v[58:59],v[54:55] op_sel:[0,1,0] op_sel_hi:[1,1,0]\n"
"v_pk_fma_f32 v[74:75],v[74:75],v[58:59],v[54:55] op_sel:[0,1,0] op_sel_hi:[1,1,0]\n"
"v_pk_fma_f32 v[76:77],v[76:77],v[58:59],v[54:55] op_sel:[0,1,0] op_sel_hi:[1,1,0]\n"
"v_pk_fma_f32 v[78:79],v[78:79],v[58:59],v[54:55] op_sel:[0,1,0] op_sel_hi:[1,1,0]\n"
CV "v112,v72,v73\n"
CV "v113,v74,v75\n"
CV "v114,v76,v77\n"
CV "v115,v78,v79\n"
MF "a[96:99],v[152:155],v[108:111],a[96:99]\n"
AW "a88,v112\n"
AW "a89,v113\n"
AW "a90,v114\n"
AW "a91,v115\n"
AR "v80,a92\n"
AR "v81,a93\n"
AR "v82,a94\n"
MF "a[116:119],v[156:159],v[108:111],a[116:119]\n"
AR "v83,a95\n"
AR "v84,a112\n"
AR "v85,a113\n"
AR "v86,a114\n"
AR "v87,a115\n"
"v_pk_fma_f32 v[80:81],v[80:81],v[60:61],v[54:55] op_sel:[0,0,1] op_sel_hi:[1,0,1]\n"
"v_pk_fma_f32 v[82:83],v[82:83],v[60:61],v[54:55] op_sel:[0,0,1] op_sel_hi:[1,0,1]\n"
"v_pk_fma_f32 v[84:85],v[84:85],v[60:61],v[54:55] op_sel:[0,0,1] op_sel_hi:[1,0,1]\n"
"v_pk_fma_f32 v[86:87],v[86:87],v[60:61],v[54:55] op_sel:[0,0,1] op_sel_hi:[1,0,1]\n"
CV "v116,v80,v81\n"
CV "v117,v82,v83\n"
CV "v118,v84,v85\n"
CV "v119,v86,v87\n"
MF "a[120:123],v[160:163],v[92:95],a[120:123]\n"
AW "a92,v116\n"
AW "a93,v117\n"
AW "a94,v118\n"
AW "a95,v119\n"
AR "v72,a96\n"
AR "v73,a97\n"
AR "v74,a98\n"
MF "a[140:143],v[164:167],v[92:95],a[140:143]\n"
AR "v75,a99\n"
AR "v76,a116\n"
AR "v77,a117\n"
AR "v78,a118\n"
AR "v79,a119\n"
"v_pk_fma_f32 v[72:73],v[72:73],v[60:61],v[56:57] op_sel:[0,1,0] op_sel_hi:[1,1,0]\n"
"v_pk_fma_f32 v[74:75],v[74:75],v[60:61],v[56:57] op_sel:[0,1,0] op_sel_hi:[1,1,0]\n"
"v_pk_fma_f32 v[76:77],v[76:77],v[60:61],v[56:57] op_sel:[0,1,0] op_sel_hi:[1,1,0]\n"
"v_pk_fma_f32 v[78:79],v[78:79],v[60:61],v[56:57] op_sel:[0,1,0] op_sel_hi:[1,1,0]\n"
CV "v112,v72,v73\n"
CV "v113,v74,v75\n"
CV "v114,v76,v77\n"
CV "v115,v78,v79\n"
MF "a[124:127],v[160:163],v[96:99],a[124:127]\n"
".Lpg12f:\n"
"s_cbranch_execz .Lisr3\n"
AW "a96,v112\n"
AW "a97,v113\n"
AW "a98,v114\n"
"s_nop 0\n"
AW "a99,v115\n"
AR "v80,a120\n"
AR "v81,a121\n"
AR "v82,a122\n"
MF "a[144:147],v[164:167],v[96:99],a[144:147]\n"
AR "v83,a123\n"
AR "v84,a140\n"
AR "v85,a141\n"
AR "v86,a142\n"
AR "v87,a143\n"
"v_pk_fma_f32 v[80:81],v[80:81],v[56:57],v[52:53] op_sel:[0,1,0] op_sel_hi:[1,1,0]\n"
"v_pk_fma_f32 v[82:83],v[82:83],v[56:57],v[52:53] op_sel:[0,1,0] op_sel_hi:[1,1,0]\n"
"v_pk_fma_f32 v[84:85],v[84:85],v[56:57],v[52:53] op_sel:[0,1,0] op_sel_hi:[1,1,0]\n"
"v_pk_fma_f32 v[86:87],v[86:87],v[56:57],v[52:53] op_sel:[0,1,0] op_sel_hi:[1,1,0]\n"
CV "v116,v80,v81\n"
CV "v117,v82,v83\n"
CV "v118,v84,v85\n"
CV "v119,v86,v87\n"
MF "a[128:131],v[160:163],v[100:103],a[128:131]\n"
AW "a120,v116\n"
AW "a121,v117\n"
AW "a122,v118\n"
AW "a123,v119\n"
AR "v72,a124\n"
AR "v73,a125\n"
AR "v74,a126\n"
MF "a[148:151],v[164:167],v[100:103],a[148:151]\n"
AR "v75,a127\n"
AR "v76,a144\n"
AR "v77,a145\n"
AR "v78,a146\n"
AR "v79,a147\n"
"v_pk_fma_f32 v[72:73],v[72:73],v[58:59],v[52:53] op_sel:[0,0,1] op_sel_hi:[1,0,1]\n"
"v_pk_fma_f32 v[74:75],v[74:75],v[58:59],v[52:53] op_sel:[0,0,1] op_sel_hi:[1,0,1]\n"
"v_pk_fma_f32 v[76:77],v[76:77],v[58:59],v[52:53] op_sel:[0,0,1] op_sel_hi:[1,0,1]\n"
"v_pk_fma_f32 v[78:79],v[78:79],v[58:59],v[52:53] op_sel:[0,0,1] op_sel_hi:[1,0,1]\n"
CV "v112,v72,v73\n"
CV "v113,v74,v75\n"
CV "v114,v76,v77\n"
CV "v115,v78,v79\n"
MF "a[132:135],v[160:163],v[104:107],a[132:135]\n"
AW "a124,v112\n"
AW "a125,v113\n"
AW "a126,v114\n"
AW "a127,v115\n"
AR "v80,a128\n"
AR "v81,a129\n"
AR "v82,a130\n"
MF "a[152:155],v[164:167],v[104:107],a[152:155]\n"
AR "v83,a131\n"
AR "v84,a148\n"
AR "v85,a149\n"
AR "v86,a150\n"
AR "v87,a151\n"
"v_pk_fma_f32 v[80:81],v[80:81],v[58:59],v[54:55] op_sel:[0,1,0] op_sel_hi:[1,1,0]\n"
"v_pk_fma_f32 v[82:83],v[82:83],v[58:59],v[54:55] op_sel:[0,1,0] op_sel_hi:[1,1,0]\n"
"v_pk_fma_f32 v[84:85],v[84:85],v[58:59],v[54:55] op_sel:[0,1,0] op_sel_hi:[1,1,0]\n"
"v_pk_fma_f32 v[86:87],v[86:87],v[58:59],v[54:55] op_sel:[0,1,0] op_sel_hi:[1,1,0]\n"
CV "v116,v80,v81\n"
CV "v117,v82,v83\n"
CV "v118,v84,v85\n"
CV "v119,v86,v87\n"
MF "a[136:139],v[160:163],v[108:111],a[136:139]\n"
AW "a128,v116\n"
AW "a129,v117\n"
AW "a130,v118\n"
AW "a131,v119\n"
AR "v72,a132\n"
AR "v73,a133\n"
AR "v74,a134\n"
MF "a[156:159],v[164:167],v[108:111],a[156:159]\n"
AR "v75,a135\n"
AR "v76,a152\n"
AR "v77,a153\n"
AR "v78,a154\n"
AR "v79,a155\n"
"v_pk_fma_f32 v[72:73],v[72:73],v[60:61],v[54:55] op_sel:[0,0,1] op_sel_hi:[1,0,1]\n"
"v_pk_fma_f32 v[74:75],v[74:75],v[60:61],v[54:55] op_sel:[0,0,1] op_sel_hi:[1,0,1]\n"
"v_pk_fma_f32 v[76:77],v[76:77],v[60:61],v[54:55] op_sel:[0,0,1] op_sel_hi:[1,0,1]\n"
"v_pk_fma_f32 v[78:79],v[78:79],v[60:61],v[54:55] op_sel:[0,0,1] op_sel_hi:[1,0,1]\n"
CV "v112,v72,v73\n"
CV "v113,v74,v75\n"
CV "v114,v76,v77\n"
CV "v115,v78,v79\n"
MF "a[160:163],v[168:171],v[92:95],a[160:163]\n"
AW "a132,v112\n"
AW "a133,v113\n"
AW "a134,v114\n"
AW "a135,v115\n"
AR "v80,a136\n"
AR "v81,a137\n"
AR "v82,a138\n"
MF "a[180:183],v[172:175],v[92:95],a[180:183]\n"
AR "v83,a139\n"
AR "v84,a156\n"
AR "v85,a157\n"
AR "v86,a158\n"
AR "v87,a159\n"
"v_pk_fma_f32 v[80:81],v[80:81],v[60:61],v[56:57] op_sel:[0,1,0] op_sel_hi:[1,1,0]\n"
"v_pk_fma_f32 v[82:83],v[82:83],v[60:61],v[56:57] op_sel:[0,1,0] op_sel_hi:[1,1,0]\n"
"v_pk_fma_f32 v[84:85],v[84:85],v[60:61],v[56:57] op_sel:[0,1,0] op_sel_hi:[1,1,0]\n"
"v_pk_fma_f32 v[86:87],v[86:87],v[60:61],v[56:57] op_sel:[0,1,0] op_sel_hi:[1,1,0]\n"
CV "v116,v80,v81\n"
CV "v117,v82,v83\n"
CV "v118,v84,v85\n"
CV "v119,v86,v87\n"
MF "a[164:167],v[168:171],v[96:99],a[164:167]\n"
AW "a136,v116\n"
AW "a137,v117\n"
AW "a138,v118\n"
AW "a139,v119\n"
AR "v72,a160\n"
AR "v73,a161\n"
AR "v74,a162\n"
MF "a[184:187],v[172:175],v[96:99],a[184:187]\n"
AR "v75,a163\n"
AR "v76,a180\n"
AR "v77,a181\n"
AR "v78,a182\n"
AR "v79,a183\n"
"v_pk_fma_f32 v[72:73],v[72:73],v[56:57],v[52:53] op_sel:[0,1,0] op_sel_hi:[1,1,0]\n"
"v_pk_fma_f32 v[74:75],v[74:75],v[56:57],v[52:53] op_sel:[0,1,0] op_sel_hi:[1,1,0]\n"
"v_pk_fma_f32 v[76:77],v[76:77],v[56:57],v[52:53] op_sel:[0,1,0] op_sel_hi:[1,1,0]\n"
"v_pk_fma_f32 v[78:79],v[78:79],v[56:57],v[52:53] op_sel:[0,1,0] op_sel_hi:[1,1,0]\n"
CV "v112,v72,v73\n"
CV "v113,v74,v75\n"
CV "v114,v76,v77\n"
CV "v115,v78,v79\n"
MF "a[168:171],v[168:171],v[100:103],a[168:171]\n"
AW "a160,v112\n"
AW "a161,v113\n"
AW "a162,v114\n"
AW "a163,v115\n"
AR "v80,a164\n"
AR "v81,a165\n"
AR "v82,a166\n"
MF "a[188:191],v[172:175],v[100:103],a[188:191]\n"
AR "v83,a167\n"
AR "v84,a184\n"
AR "v85,a185\n"
AR "v86,a186\n"
AR "v87,a187\n"
"v_pk_fma_f32 v[80:81],v[80:81],v[58:59],v[52:53] op_sel:[0,0,1] op_sel_hi:[1,0,1]\n"
"v_pk_fma_f32 v[82:83],v[82:83],v[58:59],v[52:53] op_sel:[0,0,1] op_sel_hi:[1,0,1]\n"
"v_pk_fma_f32 v[84:85],v[84:85],v[58:59],v[52:53] op_sel:[0,0,1] op_sel_hi:[1,0,1]\n"
"v_pk_fma_f32 v[86:87],v[86:87],v[58:59],v[52:53] op_sel:[0,0,1] op_sel_hi:[1,0,1]\n"
CV "v116,v80,v81\n"
CV "v117,v82,v83\n"
CV "v118,v84,v85\n"
CV "v119,v86,v87\n"
MF "a[172:175],v[168:171],v[104:107],a[172:175]\n"
AW "a164,v116\n"
AW "a165,v117\n"
AW "a166,v118\n"
AW "a167,v119\n"
AR "v72,a168\n"
AR "v73,a169\n"
AR "v74,a170\n"
MF "a[192:195],v[172:175],v[104:107],a[192:195]\n"
AR "v75,a171\n"
AR "v76,a188\n"
AR "v77,a189\n"
AR "v78,a190\n"
AR "v79,a191\n"
"v_pk_fma_f32 v[72:73],v[72:73],v[58:59],v[54:55] op_sel:[0,1,0] op_sel_hi:[1,1,0]\n"
"v_pk_fma_f32 v[74:75],v[74:75],v[58:59],v[54:55] op_sel:[0,1,0] op_sel_hi:[1,1,0]\n"
"v_pk_fma_f32 v[76:77],v[76:77],v[58:59],v[54:55] op_sel:[0,1,0] op_sel_hi:[1,1,0]\n"
"v_pk_fma_f32 v[78:79],v[78:79],v[58:59],v[54:55] op_sel:[0,1,0] op_sel_hi:[1,1,0]\n"
CV "v112,v72,v73\n"
CV "v113,v74,v75\n"
CV "v114,v76,v77\n"
CV "v115,v78,v79\n"
MF "a[176:179],v[168:171],v[108:111],a[176:179]\n"
AW "a168,v112\n"
AW "a169,v113\n"
AW "a170,v114\n"
AW "a171,v115\n"
AR "v80,a172\n"
AR "v81,a173\n"
AR "v82,a174\n"
MF "a[196:199],v[172:175],v[108:111],a[196:199]\n"
AR "v83,a175\n"
AR "v84,a192\n"
AR "v85,a193\n"
AR "v86,a194\n"
AR "v87,a195\n"
"v_pk_fma_f32 v[80:81],v[80:81],v[60:61],v[54:55] op_sel:[0,0,1] op_sel_hi:[1,0,1]\n"
"v_pk_fma_f32 v[82:83],v[82:83],v[60:61],v[54:55] op_sel:[0,0,1] op_sel_hi:[1,0,1]\n"
"v_pk_fma_f32 v[84:85],v[84:85],v[60:61],v[54:55] op_sel:[0,0,1] op_sel_hi:[1,0,1]\n"
"v_pk_fma_f32 v[86:87],v[86:87],v[60:61],v[54:55] op_sel:[0,0,1] op_sel_hi:[1,0,1]\n"
CV "v116,v80,v81\n"
CV "v117,v82,v83\n"
CV "v118,v84,v85\n"
CV "v119,v86,v87\n"
MF "a[200:203],v[176:179],v[92:95],a[200:203]\n"
AW "a172,v116\n"
AW "a173,v117\n"
AW "a174,v118\n"
AW "a175,v119\n"
AR "v72,a176\n"
AR "v73,a177\n"
AR "v74,a178\n"
MF "a[220:223],v[180:183],v[92:95],a[220:223]\n"
AR "v75,a179\n"
AR "v76,a196\n"
AR "v77,a197\n"
AR "v78,a198\n"
AR "v79,a199\n"
"v_pk_fma_f32 v[72:73],v[72:73],v[60:61],v[56:57] op_sel:[0,1,0] op_sel_hi:[1,1,0]\n"
"v_pk_fma_f32 v[74:75],v[74:75],v[60:61],v[56:57] op_sel:[0,1,0] op_sel_hi:[1,1,0]\n"
"v_pk_fma_f32 v[76:77],v[76:77],v[60:61],v[56:57] op_sel:[0,1,0] op_sel_hi:[1,1,0]\n"
"v_pk_fma_f32 v[78:79],v[78:79],v[60:61],v[56:57] op_sel:[0,1,0] op_sel_hi:[1,1,0]\n"
CV "v112,v72,v73\n"
CV "v113,v74,v75\n"
CV "v114,v76,v77\n"
CV "v115,v78,v79\n"
MF "a[204:207],v[176:179],v[96:99],a[204:207]\n"
AW "a176,v112\n"
AW "a177,v113\n"
AW "a178,v114\n"
AW "a179,v115\n"
AR "v80,a200\n"
AR "v81,a201\n"
AR "v82,a202\n"
MF "a[224:227],v[180:183],v[96:99],a[224:227]\n"
AR "v83,a203\n"
AR "v84,a220\n"
AR "v85,a221\n"
AR "v86,a222\n"
AR "v87,a223\n"
"v_pk_fma_f32 v[80:81],v[80:81],v[56:57],v[52:53] op_sel:[0,1,0] op_sel_hi:[1,1,0]\n"
"v_pk_fma_f32 v[82:83],v[82:83],v[56:57],v[52:53] op_sel:[0,1,0] op_sel_hi:[1,1,0]\n"
"v_pk_fma_f32 v[84:85],v[84:85],v[56:57],v[52:53] op_sel:[0,1,0] op_sel_hi:[1,1,0]\n"
"v_pk_fma_f32 v[86:87],v[86:87],v[56:57],v[52:53] op_sel:[0,1,0] op_sel_hi:[1,1,0]\n"
CV "v116,v80,v81\n"
CV "v117,v82,v83\n"
CV "v118,v84,v85\n"
CV "v119,v86,v87\n"
MF "a[208:211],v[176:179],v[100:103],a[208:211]\n"
AW "a20,v116\n"
AW "a21,v117\n"
AW "a22,v118\n"
AW "a23,v119\n"
AR "v72,a204\n"
AR "v73,a205\n"
AR "v74,a206\n"
MF "a[228:231],v[180:183],v[100:103],a[228:231]\n"
AR "v75,a207\n"
AR "v76,a224\n"
AR "v77,a225\n"
AR "v78,a226\n"
AR "v79,a227\n"
"v_pk_fma_f32 v[72:73],v[72:73],v[58:59],v[52:53] op_sel:[0,0,1] op_sel_hi:[1,0,1]\n"
"v_pk_fma_f32 v[74:75],v[74:75],v[58:59],v[52:53] op_sel:[0,0,1] op_sel_hi:[1,0,1]\n"
"v_pk_fma_f32 v[76:77],v[76:77],v[58:59],v[52:53] op_sel:[0,0,1] op_sel_hi:[1,0,1]\n"
"v_pk_fma_f32 v[78:79],v[78:79],v[58:59],v[52:53] op_sel:[0,0,1] op_sel_hi:[1,0,1]\n"
CV "v112,v72,v73\n"
CV "v113,v74,v75\n"
CV "v114,v76,v77\n"
CV "v115,v78,v79\n"
MF "a[212:215],v[176:179],v[104:107],a[212:215]\n"
AW "a24,v112\n"
AW "a25,v113\n"
AW "a26,v114\n"
AW "a27,v115\n"
AR "v80,a208\n"
AR "v81,a209\n"
AR "v82,a210\n"
MF "a[232:235],v[180:183],v[104:107],a[232:235]\n"
AR "v83,a211\n"
AR "v84,a228\n"
AR "v85,a229\n"
AR "v86,a230\n"
AR "v87,a231\n"
"v_pk_fma_f32 v[80:81],v[80:81],v[58:59],v[54:55] op_sel:[0,1,0] op_sel_hi:[1,1,0]\n"
"v_pk_fma_f32 v[82:83],v[82:83],v[58:59],v[54:55] op_sel:[0,1,0] op_sel_hi:[1,1,0]\n"
"v_pk_fma_f32 v[84:85],v[84:85],v[58:59],v[54:55] op_sel:[0,1,0] op_sel_hi:[1,1,0]\n"
"v_pk_fma_f32 v[86:87],v[86:87],v[58:59],v[54:55] op_sel:[0,1,0] op_sel_hi:[1,1,0]\n"
CV "v116,v80,v81\n"
CV "v117,v82,v83\n"
CV "v118,v84,v85\n"
CV "v119,v86,v87\n"
MF "a[216:219],v[176:179],v[108:111],a[216:219]\n"
AW "a28,v116\n"
AW "a29,v117\n"
AW "a30,v118\n"
AW "a31,v119\n"
AR "v72,a212\n"
AR "v73,a213\n"
AR "v74,a214\n"
MF "a[236:239],v[180:183],v[108:111],a[236:239]\n"
AR "v75,a215\n"
AR "v76,a232\n"
AR "v77,a233\n"
AR "v78,a234\n"
AR "v79,a235\n"
"v_pk_fma_f32 v[72:73],v[72:73],v[60:61],v[54:55] op_sel:[0,0,1] op_sel_hi:[1,0,1]\n"
"v_pk_fma_f32 v[74:75],v[74:75],v[60:61],v[54:55] op_sel:[0,0,1] op_sel_hi:[1,0,1]\n"
"v_pk_fma_f32 v[76:77],v[76:77],v[60:61],v[54:55] op_sel:[0,0,1] op_sel_hi:[1,0,1]\n"
"v_pk_fma_f32 v[78:79],v[78:79],v[60:61],v[54:55] op_sel:[0,0,1] op_sel_hi:[1,0,1]\n"
CV "v112,v72,v73\n"
CV "v113,v74,v75\n"
CV "v114,v76,v77\n"
CV "v115,v78,v79\n"
AW "a32,v112\n"
AW "a33,v113\n"
AW "a34,v114\n"
AW "a35,v115\n"
AR "v80,a216\n"
AR "v81,a217\n"
AR "v82,a218\n"
AR "v83,a219\n"
AR "v84,a236\n"
AR "v85,a237\n"
AR "v86,a238\n"
AR "v87,a239\n"
"v_pk_fma_f32 v[80:81],v[80:81],v[60:61],v[56:57] op_sel:[0,1,0] op_sel_hi:[1,1,0]\n"
"v_pk_fma_f32 v[82:83],v[82:83],v[60:61],v[56:57] op_sel:[0,1,0] op_sel_hi:[1,1,0]\n"
"v_pk_fma_f32 v[84:85],v[84:85],v[60:61],v[56:57] op_sel:[0,1,0] op_sel_hi:[1,1,0]\n"
"v_pk_fma_f32 v[86:87],v[86:87],v[60:61],v[56:57] op_sel:[0,1,0] op_sel_hi:[1,1,0]\n"
CV "v116,v80,v81\n"
CV "v117,v82,v83\n"
CV "v118,v84,v85\n"
CV "v119,v86,v87\n"
AW "a36,v116\n"
AW "a37,v117\n"
AW "a38,v118\n"
AW "a39,v119\n"
"s_mov_b32 s13,s11\n"
"s_mov_b32 s70,s71\n"
"s_mov_b32 s57,1\n"
AR "v0,a240\n"
AR "v2,a241\n"
AR "v4,a242\n"
AR "v6,a243\n"
"v_xor_b32 v1,64,v0\n"
"v_xor_b32 v3,64,v2\n"
"v_xor_b32 v5,64,v4\n"
"v_add_u32 v11,97280,v6\n"
"v_add_u32 v12,194560,v6\n"
"v_add_u32 v13,291840,v6\n"
"s_mov_b64 vcc,-1\n"
"s_waitcnt vmcnt(8) lgkmcnt(0)\n"
"s_barrier\n"
"s_nop 0\n"
TR "a[200:201],v0\n"
TR "a[202:203],v0 offset:512\n"
TR "a[204:205],v0 offset:4096\n"
TR "a[206:207],v0 offset:4608\n"
"ds_read_b128 a[60:63],v2\n"
"ds_read_b128 a[100:103],v3\n"
"ds_read_b128 a[64:67],v2 offset:2048\n"
"ds_read_b128 a[104:107],v3 offset:2048\n"
"ds_read_b128 a[68:71],v2 offset:4096\n"
"ds_read_b128 a[108:111],v3 offset:4096\n"
"ds_read_b128 a[72:75],v2 offset:6144\n"
"ds_read_b128 a[112:115],v3 offset:6144\n"
"ds_read_b128 a[76:79],v2 offset:8192\n"
"ds_read_b128 a[116:119],v3 offset:8192\n"
TR "a[208:209],v0 offset:8\n"
TR "a[210:211],v0 offset:520\n"
TR "a[212:213],v0 offset:4104\n"
TR "a[214:215],v0 offset:4616\n"
"s_waitcnt lgkmcnt(0)\n"
"s_branch .Lq0\n"
".Lpgep_g:\n"
"s_cbranch_execz .Lisr3\n"
".Lep:\n"
"s_lshl_b32 s58,s56,10\n"
"s_add_u32 s58,s58,73728\n"
"v_add_u32 v120,s58,v9\n"
"v_add_u32 v121,65536,v120\n"
"s_sub_u32 s59,s71,s70\n"
"v_mov_b32 v10,1.0\n"
TR "v[136:137],v2 offset:24576\n"
TR "v[138:139],v2 offset:30720\n"
"v_mfma_f32_16x16x4_f32 v[16:19],v10,v57,0\n"
TR "v[140:141],v3 offset:24576\n"
TR "v[142:143],v3 offset:30720\n"
TR "v[144:145],v4 offset:24576\n"
TR "v[146:147],v4 offset:30720\n"
TR "v[148:149],v5 offset:24576\n"
TR "v[150:151],v5 offset:30720\n"
"v_mfma_f32_16x16x4_f32 v[20:23],v10,v58,0\n"
TR "v[152:153],v2 offset:25600\n"
TR "v[154:155],v2 offset:31744\n"
TR "v[156:157],v3 offset:25600\n"
TR "v[158:159],v3 offset:31744\n"
TR "v[160:161],v4 offset:25600\n"
TR "v[162:163],v4 offset:31744\n"
"v_mfma_f32_16x16x4_f32 v[24:27],v10,v59,0\n"
TR "v[164:165],v5 offset:25600\n"
TR "v[166:167],v5 offset:31744\n"
TR "v[168:169],v2 offset:26624\n"
TR "v[170:171],v2 offset:32768\n"
TR "v[172:173],v3 offset:26624\n"
TR "v[174:175],v3 offset:32768\n"
"v_mfma_f32_16x16x4_f32 v[28:31],v10,v60,0\n"
TR "v[176:177],v4 offset:26624\n"
TR "v[178:179],v4 offset:32768\n"
TR "v[180:181],v5 offset:26624\n"
TR "v[182:183],v5 offset:32768\n"
"v_mfma_f32_16x16x4_f32 v[32:35],v10,v61,0\n"
"s_add_u32 m0,s65,49152\n"
"s_nop 0\n"
LD "v8,s[36:39],s73 offen offset:3072 nt lds\n"
"s_add_u32 m0,s65,50048\n"
"s_nop 0\n"
LD "v8,s[36:39],s73 offen offset:3200 nt lds\n"
"s_waitcnt lgkmcnt(15)\n"
MF "a[0:3],v[136:139],v[92:95],a[0:3]\n"
MF "a[20:23],v[140:143],v[92:95],a[20:23]\n"
"s_add_u32 m0,s65,50944\n"
"s_nop 0\n"
LD "v8,s[36:39],s73 offen offset:3328 nt lds\n"
"s_sub_u32 s74,s96,s16\n"
"s_add_u32 s74,s74,s71\n"
"s_add_u32 s20,s20,s59\n"
"s_mov_b32 s70,s71\n"
LD "v[232:235],v9,s[40:43],s75 offen sc1\n"
"v_rcp_f32 v57,v16\n"
"v_rcp_f32 v58,v20\n"
"v_rcp_f32 v59,v24\n"
"v_rcp_f32 v60,v28\n"
"v_rcp_f32 v61,v32\n"
MF "a[4:7],v[136:139],v[96:99],a[4:7]\n"
"v_mul_f32_e64 v52,-s13,v57\n"
"v_mul_f32_e64 v53,-s13,v58\n"
"v_mul_f32_e64 v54,-s13,v59\n"
"v_mul_f32_e64 v55,-s13,v60\n"
"v_mul_f32_e64 v56,-s13,v61\n"
AR "v72,a0\n"
AR "v73,a1\n"
AR "v74,a2\n"
MF "a[24:27],v[140:143],v[96:99],a[24:27]\n"
AR "v75,a3\n"
AR "v76,a20\n"
AR "v77,a21\n"
AR "v78,a22\n"
AR "v79,a23\n"
"v_pk_fma_f32 v[72:73],v[72:73],v[56:57],v[52:53] op_sel:[0,1,0] op_sel_hi:[1,1,0]\n"
"v_pk_fma_f32 v[74:75],v[74:75],v[56:57],v[52:53] op_sel:[0,1,0] op_sel_hi:[1,1,0]\n"
"v_pk_fma_f32 v[76:77],v[76:77],v[56:57],v[52:53] op_sel:[0,1,0] op_sel_hi:[1,1,0]\n"
"v_pk_fma_f32 v[78:79],v[78:79],v[56:57],v[52:53] op_sel:[0,1,0] op_sel_hi:[1,1,0]\n"
CV "v112,v72,v73\n"
CV "v113,v74,v75\n"
CV "v114,v76,v77\n"
CV "v115,v78,v79\n"
MF "a[8:11],v[136:139],v[100:103],a[8:11]\n"
"ds_read_b128 v[184:187],v120 offset:24576\n"
"ds_write_b128 v120,v[112:115] offset:24576\n"
LD "v[236:239],v9,s[40:43],s75 offen offset:1024 sc1\n"
AR "v80,a4\n"
AR "v81,a5\n"
AR "v82,a6\n"
MF "a[28:31],v[140:143],v[100:103],a[28:31]\n"
AR "v83,a7\n"
AR "v84,a24\n"
AR "v85,a25\n"
AR "v86,a26\n"
AR "v87,a27\n"
"v_pk_fma_f32 v[80:81],v[80:81],v[58:59],v[52:53] op_sel:[0,0,1] op_sel_hi:[1,0,1]\n"
"v_pk_fma_f32 v[82:83],v[82:83],v[58:59],v[52:53] op_sel:[0,0,1] op_sel_hi:[1,0,1]\n"
"v_pk_fma_f32 v[84:85],v[84:85],v[58:59],v[52:53] op_sel:[0,0,1] op_sel_hi:[1,0,1]\n"
"v_pk_fma_f32 v[86:87],v[86:87],v[58:59],v[52:53] op_sel:[0,0,1] op_sel_hi:[1,0,1]\n"
CV "v116,v80,v81\n"
CV "v117,v82,v83\n"
CV "v118,v84,v85\n"
CV "v119,v86,v87\n"
MF "a[12:15],v[136:139],v[104:107],a[12:15]\n"
"ds_read_b128 v[188:191],v120 offset:28672\n"
"ds_write_b128 v120,v[116:119] offset:28672\n"
AR "v72,a8\n"
AR "v73,a9\n"
AR "v74,a10\n"
MF "a[32:35],v[140:143],v[104:107],a[32:35]\n"
AR "v75,a11\n"
AR "v76,a28\n"
AR "v77,a29\n"
AR "v78,a30\n"
AR "v79,a31\n"
"v_pk_fma_f32 v[72:73],v[72:73],v[58:59],v[54:55] op_sel:[0,1,0] op_sel_hi:[1,1,0]\n"
"v_pk_fma_f32 v[74:75],v[74:75],v[58:59],v[54:55] op_sel:[0,1,0] op_sel_hi:[1,1,0]\n"
"v_pk_fma_f32 v[76:77],v[76:77],v[58:59],v[54:55] op_sel:[0,1,0] op_sel_hi:[1,1,0]\n"
"v_pk_fma_f32 v[78:79],v[78:79],v[58:59],v[54:55] op_sel:[0,1,0] op_sel_hi:[1,1,0]\n"
CV "v112,v72,v73\n"
CV "v113,v74,v75\n"
CV "v114,v76,v77\n"
CV "v115,v78,v79\n"
MF "a[16:19],v[136:139],v[108:111],a[16:19]\n"
"ds_read_b128 v[192:195],v120 offset:32768\n"
"ds_write_b128 v120,v[112:115] offset:32768\n"
"s_waitcnt lgkmcnt(0)\n"
"s_barrier\n"
LD "v[240:243],v9,s[40:43],s75 offen offset:2048 sc1\n"
AR "v80,a12\n"
AR "v81,a13\n"
AR "v82,a14\n"
MF "a[36:39],v[140:143],v[108:111],a[36:39]\n"
AR "v83,a15\n"
AR "v84,a32\n"
AR "v85,a33\n"
AR "v86,a34\n"
AR "v87,a35\n"
"v_pk_fma_f32 v[80:81],v[80:81],v[60:61],v[54:55] op_sel:[0,0,1] op_sel_hi:[1,0,1]\n"
"v_pk_fma_f32 v[82:83],v[82:83],v[60:61],v[54:55] op_sel:[0,0,1] op_sel_hi:[1,0,1]\n"
"v_pk_fma_f32 v[84:85],v[84:85],v[60:61],v[54:55] op_sel:[0,0,1] op_sel_hi:[1,0,1]\n"
"v_pk_fma_f32 v[86:87],v[86:87],v[60:61],v[54:55] op_sel:[0,0,1] op_sel_hi:[1,0,1]\n"
CV "v116,v80,v81\n"
CV "v117,v82,v83\n"
CV "v118,v84,v85\n"
CV "v119,v86,v87\n"
MF "a[40:43],v[144:147],v[92:95],a[40:43]\n"
"ds_read_b128 v[196:199],v120 offset:36864\n"
"ds_write_b128 v120,v[116:119] offset:36864\n"
LD "v[244:247],v9,s[40:43],s75 offen offset:3072 sc1\n"
AR "v72,a16\n"
AR "v73,a17\n"
AR "v74,a18\n"
MF "a[60:63],v[148:151],v[92:95],a[60:63]\n"
AR "v75,a19\n"
AR "v76,a36\n"
AR "v77,a37\n"
AR "v78,a38\n"
AR "v79,a39\n"
"v_pk_fma_f32 v[72:73],v[72:73],v[60:61],v[56:57] op_sel:[0,1,0] op_sel_hi:[1,1,0]\n"
".Lpg13g:\n"
"s_cbranch_execz .Lisr3\n"
"s_nop 0\n"
"v_pk_fma_f32 v[74:75],v[74:75],v[60:61],v[56:57] op_sel:[0,1,0] op_sel_hi:[1,1,0]\n"
"v_pk_fma_f32 v[76:77],v[76:77],v[60:61],v[56:57] op_sel:[0,1,0] op_sel_hi:[1,1,0]\n"
"v_pk_fma_f32 v[78:79],v[78:79],v[60:61],v[56:57] op_sel:[0,1,0] op_sel_hi:[1,1,0]\n"
CV "v112,v72,v73\n"
CV "v113,v74,v75\n"
CV "v114,v76,v77\n"
CV "v115,v78,v79\n"
MF "a[44:47],v[144:147],v[96:99],a[44:47]\n"
"ds_read_b128 v[200:203],v120 offset:40960\n"
"ds_write_b128 v120,v[112:115] offset:40960\n"
AR "v80,a40\n"
AR "v81,a41\n"
AR "v82,a42\n"
MF "a[64:67],v[148:151],v[96:99],a[64:67]\n"
AR "v83,a43\n"
AR "v84,a60\n"
AR "v85,a61\n"
AR "v86,a62\n"
AR "v87,a63\n"
"v_pk_fma_f32 v[80:81],v[80:81],v[56:57],v[52:53] op_sel:[0,1,0] op_sel_hi:[1,1,0]\n"
"v_pk_fma_f32 v[82:83],v[82:83],v[56:57],v[52:53] op_sel:[0,1,0] op_sel_hi:[1,1,0]\n"
"v_pk_fma_f32 v[84:85],v[84:85],v[56:57],v[52:53] op_sel:[0,1,0] op_sel_hi:[1,1,0]\n"
"v_pk_fma_f32 v[86:87],v[86:87],v[56:57],v[52:53] op_sel:[0,1,0] op_sel_hi:[1,1,0]\n"
CV "v116,v80,v81\n"
CV "v117,v82,v83\n"
CV "v118,v84,v85\n"
CV "v119,v86,v87\n"
MF "a[48:51],v[144:147],v[100:103],a[48:51]\n"
"ds_read_b128 v[204:207],v120 offset:45056\n"
"ds_write_b128 v120,v[116:119] offset:45056\n"
"s_add_u32 s58,s75,4096\n"
LD "v[248:251],v9,s[40:43],s58 offen sc1\n"
AR "v72,a44\n"
AR "v73,a45\n"
AR "v74,a46\n"
MF "a[68:71],v[148:151],v[100:103],a[68:71]\n"
AR "v75,a47\n"
AR "v76,a64\n"
AR "v77,a65\n"
AR "v78,a66\n"
AR "v79,a67\n"
"v_pk_fma_f32 v[72:73],v[72:73],v[58:59],v[52:53] op_sel:[0,0,1] op_sel_hi:[1,0,1]\n"
"v_pk_fma_f32 v[74:75],v[74:75],v[58:59],v[52:53] op_sel:[0,0,1] op_sel_hi:[1,0,1]\n"
"v_pk_fma_f32 v[76:77],v[76:77],v[58:59],v[52:53] op_sel:[0,0,1] op_sel_hi:[1,0,1]\n"
"v_pk_fma_f32 v[78:79],v[78:79],v[58:59],v[52:53] op_sel:[0,0,1] op_sel_hi:[1,0,1]\n"
CV "v112,v72,v73\n"
CV "v113,v74,v75\n"
CV "v114,v76,v77\n"
CV "v115,v78,v79\n"
MF "a[52:55],v[144:147],v[104:107],a[52:55]\n"
"ds_read_b128 v[208:211],v120 offset:49152\n"
"ds_write_b128 v120,v[112:115] offset:49152\n"
LD "v[252:255],v9,s[40:43],s58 offen offset:1024 sc1\n"
AR "v80,a48\n"
AR "v81,a49\n"
AR "v82,a50\n"
MF "a[72:75],v[148:151],v[104:107],a[72:75]\n"
AR "v83,a51\n"
AR "v84,a68\n"
AR "v85,a69\n"
AR "v86,a70\n"
AR "v87,a71\n"
"v_pk_fma_f32 v[80:81],v[80:81],v[58:59],v[54:55] op_sel:[0,1,0] op_sel_hi:[1,1,0]\n"
"v_pk_fma_f32 v[82:83],v[82:83],v[58:59],v[54:55] op_sel:[0,1,0] op_sel_hi:[1,1,0]\n"
"v_pk_fma_f32 v[84:85],v[84:85],v[58:59],v[54:55] op_sel:[0,1,0] op_sel_hi:[1,1,0]\n"
"v_pk_fma_f32 v[86:87],v[86:87],v[58:59],v[54:55] op_sel:[0,1,0] op_sel_hi:[1,1,0]\n"
CV "v116,v80,v81\n"
CV "v117,v82,v83\n"
CV "v118,v84,v85\n"
CV "v119,v86,v87\n"
MF "a[56:59],v[144:147],v[108:111],a[56:59]\n"
"ds_read_b128 v[212:215],v120 offset:53248\n"
"ds_write_b128 v120,v[116:119] offset:53248\n"
AR "v72,a52\n"
AR "v73,a53\n"
AR "v74,a54\n"
MF "a[76:79],v[148:151],v[108:111],a[76:79]\n"
AR "v75,a55\n"
AR "v76,a72\n"
AR "v77,a73\n"
AR "v78,a74\n"
AR "v79,a75\n"
"v_pk_fma_f32 v[72:73],v[72:73],v[60:61],v[54:55] op_sel:[0,0,1] op_sel_hi:[1,0,1]\n"
"v_pk_fma_f32 v[74:75],v[74:75],v[60:61],v[54:55] op_sel:[0,0,1] op_sel_hi:[1,0,1]\n"
"v_pk_fma_f32 v[76:77],v[76:77],v[60:61],v[54:55] op_sel:[0,0,1] op_sel_hi:[1,0,1]\n"
"v_pk_fma_f32 v[78:79],v[78:79],v[60:61],v[54:55] op_sel:[0,0,1] op_sel_hi:[1,0,1]\n"
CV "v112,v72,v73\n"
CV "v113,v74,v75\n"
CV "v114,v76,v77\n"
CV "v115,v78,v79\n"
MF "a[80:83],v[152:155],v[92:95],a[80:83]\n"
"ds_read_b128 v[216:219],v120 offset:57344\n"
"ds_write_b128 v120,v[112:115] offset:57344\n"
AR "v80,a56\n"
AR "v81,a57\n"
AR "v82,a58\n"
MF "a[100:103],v[156:159],v[92:95],a[100:103]\n"
AR "v83,a59\n"
AR "v84,a76\n"
AR "v85,a77\n"
AR "v86,a78\n"
AR "v87,a79\n"
"v_pk_fma_f32 v[80:81],v[80:81],v[60:61],v[56:57] op_sel:[0,1,0] op_sel_hi:[1,1,0]\n"
"v_pk_fma_f32 v[82:83],v[82:83],v[60:61],v[56:57] op_sel:[0,1,0] op_sel_hi:[1,1,0]\n"
"v_pk_fma_f32 v[84:85],v[84:85],v[60:61],v[56:57] op_sel:[0,1,0] op_sel_hi:[1,1,0]\n"
"v_pk_fma_f32 v[86:87],v[86:87],v[60:61],v[56:57] op_sel:[0,1,0] op_sel_hi:[1,1,0]\n"
CV "v116,v80,v81\n"
CV "v117,v82,v83\n"
CV "v118,v84,v85\n"
CV "v119,v86,v87\n"
MF "a[84:87],v[152:155],v[96:99],a[84:87]\n"
"ds_read_b128 v[220:223],v120 offset:61440\n"
"ds_write_b128 v120,v[116:119] offset:61440\n"
AR "v72,a80\n"
AR "v73,a81\n"
AR "v74,a82\n"
MF "a[104:107],v[156:159],v[96:99],a[104:107]\n"
AR "v75,a83\n"
AR "v76,a100\n"
AR "v77,a101\n"
AR "v78,a102\n"
AR "v79,a103\n"
"v_pk_fma_f32 v[72:73],v[72:73],v[56:57],v[52:53] op_sel:[0,1,0] op_sel_hi:[1,1,0]\n"
"v_pk_fma_f32 v[74:75],v[74:75],v[56:57],v[52:53] op_sel:[0,1,0] op_sel_hi:[1,1,0]\n"
"v_pk_fma_f32 v[76:77],v[76:77],v[56:57],v[52:53] op_sel:[0,1,0] op_sel_hi:[1,1,0]\n"
"v_pk_fma_f32 v[78:79],v[78:79],v[56:57],v[52:53] op_sel:[0,1,0] op_sel_hi:[1,1,0]\n"
CV "v112,v72,v73\n"
CV "v113,v74,v75\n"
CV "v114,v76,v77\n"
CV "v115,v78,v79\n"
MF "a[88:91],v[152:155],v[100:103],a[88:91]\n"
"ds_read_b128 v[224:227],v121\n"
"ds_write_b128 v121,v[112:115]\n"
AR "v80,a84\n"
AR "v81,a85\n"
AR "v82,a86\n"
MF "a[108:111],v[156:159],v[100:103],a[108:111]\n"
AR "v83,a87\n"
AR "v84,a104\n"
AR "v85,a105\n"
AR "v86,a106\n"
AR "v87,a107\n"
"v_pk_fma_f32 v[80:81],v[80:81],v[58:59],v[52:53] op_sel:[0,0,1] op_sel_hi:[1,0,1]\n"
"v_pk_fma_f32 v[82:83],v[82:83],v[58:59],v[52:53] op_sel:[0,0,1] op_sel_hi:[1,0,1]\n"
"v_pk_fma_f32 v[84:85],v[84:85],v[58:59],v[52:53] op_sel:[0,0,1] op_sel_hi:[1,0,1]\n"
"v_pk_fma_f32 v[86:87],v[86:87],v[58:59],v[52:53] op_sel:[0,0,1] op_sel_hi:[1,0,1]\n"
CV "v116,v80,v81\n"
CV "v117,v82,v83\n"
CV "v118,v84,v85\n"
CV "v119,v86,v87\n"
MF "a[92:95],v[152:155],v[104:107],a[92:95]\n"
"ds_read_b128 v[228:231],v121 offset:4096\n"
"ds_write_b128 v121,v[116:119] offset:4096\n"
AR "v72,a88\n"
AR "v73,a89\n"
AR "v74,a90\n"
MF "a[112:115],v[156:159],v[104:107],a[112:115]\n"
AR "v75,a91\n"
AR "v76,a108\n"
AR "v77,a109\n"
AR "v78,a110\n"
AR "v79,a111\n"
"v_pk_fma_f32 v[72:73],v[72:73],v[58:59],v[54:55] op_sel:[0,1,0] op_sel_hi:[1,1,0]\n"
"v_pk_fma_f32 v[74:75],v[74:75],v[58:59],v[54:55] op_sel:[0,1,0] op_sel_hi:[1,1,0]\n"
"v_pk_fma_f32 v[76:77],v[76:77],v[58:59],v[54:55] op_sel:[0,1,0] op_sel_hi:[1,1,0]\n"
"v_pk_fma_f32 v[78:79],v[78:79],v[58:59],v[54:55] op_sel:[0,1,0] op_sel_hi:[1,1,0]\n"
CV "v112,v72,v73\n"
CV "v113,v74,v75\n"
CV "v114,v76,v77\n"
CV "v115,v78,v79\n"
MF "a[96:99],v[152:155],v[108:111],a[96:99]\n"
"ds_read_b128 v[136:139],v121 offset:8192\n"
"ds_write_b128 v121,v[112:115] offset:8192\n"
AR "v80,a92\n"
AR "v81,a93\n"
AR "v82,a94\n"
MF "a[116:119],v[156:159],v[108:111],a[116:119]\n"
AR "v83,a95\n"
AR "v84,a112\n"
AR "v85,a113\n"
AR "v86,a114\n"
AR "v87,a115\n"
"v_pk_fma_f32 v[80:81],v[80:81],v[60:61],v[54:55] op_sel:[0,0,1] op_sel_hi:[1,0,1]\n"
"v_pk_fma_f32 v[82:83],v[82:83],v[60:61],v[54:55] op_sel:[0,0,1] op_sel_hi:[1,0,1]\n"
"v_pk_fma_f32 v[84:85],v[84:85],v[60:61],v[54:55] op_sel:[0,0,1] op_sel_hi:[1,0,1]\n"
"v_pk_fma_f32 v[86:87],v[86:87],v[60:61],v[54:55] op_sel:[0,0,1] op_sel_hi:[1,0,1]\n"
CV "v116,v80,v81\n"
CV "v117,v82,v83\n"
CV "v118,v84,v85\n"
CV "v119,v86,v87\n"
MF "a[120:123],v[160:163],v[92:95],a[120:123]\n"
"ds_read_b128 v[140:143],v121 offset:12288\n"
"ds_write_b128 v121,v[116:119] offset:12288\n"
AR "v72,a96\n"
AR "v73,a97\n"
AR "v74,a98\n"
MF "a[140:143],v[164:167],v[92:95],a[140:143]\n"
AR "v75,a99\n"
AR "v76,a116\n"
AR "v77,a117\n"
AR "v78,a118\n"
AR "v79,a119\n"
"v_pk_fma_f32 v[72:73],v[72:73],v[60:61],v[56:57] op_sel:[0,1,0] op_sel_hi:[1,1,0]\n"
"v_pk_fma_f32 v[74:75],v[74:75],v[60:61],v[56:57] op_sel:[0,1,0] op_sel_hi:[1,1,0]\n"
"v_pk_fma_f32 v[76:77],v[76:77],v[60:61],v[56:57] op_sel:[0,1,0] op_sel_hi:[1,1,0]\n"
"v_pk_fma_f32 v[78:79],v[78:79],v[60:61],v[56:57] op_sel:[0,1,0] op_sel_hi:[1,1,0]\n"
CV "v112,v72,v73\n"
CV "v113,v74,v75\n"
CV "v114,v76,v77\n"
CV "v115,v78,v79\n"
MF "a[124:127],v[160:163],v[96:99],a[124:127]\n"
"ds_read_b128 v[144:147],v121 offset:16384\n"
"ds_write_b128 v121,v[112:115] offset:16384\n"
AR "v80,a120\n"
AR "v81,a121\n"
AR "v82,a122\n"
MF "a[144:147],v[164:167],v[96:99],a[144:147]\n"
AR "v83,a123\n"
AR "v84,a140\n"
AR "v85,a141\n"
AR "v86,a142\n"
AR "v87,a143\n"
"v_pk_fma_f32 v[80:81],v[80:81],v[56:57],v[52:53] op_sel:[0,1,0] op_sel_hi:[1,1,0]\n"
"v_pk_fma_f32 v[82:83],v[82:83],v[56:57],v[52:53] op_sel:[0,1,0] op_sel_hi:[1,1,0]\n"
"v_pk_fma_f32 v[84:85],v[84:85],v[56:57],v[52:53] op_sel:[0,1,0] op_sel_hi:[1,1,0]\n"
"v_pk_fma_f32 v[86:87],v[86:87],v[56:57],v[52:53] op_sel:[0,1,0] op_sel_hi:[1,1,0]\n"
CV "v116,v80,v81\n"
CV "v117,v82,v83\n"
CV "v118,v84,v85\n"
CV "v119,v86,v87\n"
MF "a[128:131],v[160:163],v[100:103],a[128:131]\n"
"ds_read_b128 v[148:151],v121 offset:20480\n"
"ds_write_b128 v121,v[116:119] offset:20480\n"
AR "v72,a124\n"
AR "v73,a125\n"
AR "v74,a126\n"
MF "a[148:151],v[164:167],v[100:103],a[148:151]\n"
AR "v75,a127\n"
AR "v76,a144\n"
AR "v77,a145\n"
AR "v78,a146\n"
AR "v79,a147\n"
"v_pk_fma_f32 v[72:73],v[72:73],v[58:59],v[52:53] op_sel:[0,0,1] op_sel_hi:[1,0,1]\n"
"v_pk_fma_f32 v[74:75],v[74:75],v[58:59],v[52:53] op_sel:[0,0,1] op_sel_hi:[1,0,1]\n"
"v_pk_fma_f32 v[76:77],v[76:77],v[58:59],v[52:53] op_sel:[0,0,1] op_sel_hi:[1,0,1]\n"
"v_pk_fma_f32 v[78:79],v[78:79],v[58:59],v[52:53] op_sel:[0,0,1] op_sel_hi:[1,0,1]\n"
CV "v112,v72,v73\n"
CV "v113,v74,v75\n"
CV "v114,v76,v77\n"
CV "v115,v78,v79\n"
MF "a[132:135],v[160:163],v[104:107],a[132:135]\n"
"ds_write_b128 v120,v[112:115]\n"
AR "v80,a128\n"
AR "v81,a129\n"
AR "v82,a130\n"
MF "a[152:155],v[164:167],v[104:107],a[152:155]\n"
AR "v83,a131\n"
AR "v84,a148\n"
AR "v85,a149\n"
AR "v86,a150\n"
AR "v87,a151\n"
"v_pk_fma_f32 v[80:81],v[80:81],v[58:59],v[54:55] op_sel:[0,1,0] op_sel_hi:[1,1,0]\n"
"v_pk_fma_f32 v[82:83],v[82:83],v[58:59],v[54:55] op_sel:[0,1,0] op_sel_hi:[1,1,0]\n"
"v_pk_fma_f32 v[84:85],v[84:85],v[58:59],v[54:55] op_sel:[0,1,0] op_sel_hi:[1,1,0]\n"
"v_pk_fma_f32 v[86:87],v[86:87],v[58:59],v[54:55] op_sel:[0,1,0] op_sel_hi:[1,1,0]\n"
CV "v116,v80,v81\n"
CV "v117,v82,v83\n"
CV "v118,v84,v85\n"
CV "v119,v86,v87\n"
MF "a[136:139],v[160:163],v[108:111],a[136:139]\n"
"ds_write_b128 v120,v[116:119] offset:4096\n"
AR "v72,a132\n"
AR "v73,a133\n"
AR "v74,a134\n"
MF "a[156:159],v[164:167],v[108:111],a[156:159]\n"
AR "v75,a135\n"
AR "v76,a152\n"
AR "v77,a153\n"
AR "v78,a154\n"
AR "v79,a155\n"
"v_pk_fma_f32 v[72:73],v[72:73],v[60:61],v[54:55] op_sel:[0,0,1] op_sel_hi:[1,0,1]\n"
"v_pk_fma_f32 v[74:75],v[74:75],v[60:61],v[54:55] op_sel:[0,0,1] op_sel_hi:[1,0,1]\n"
"v_pk_fma_f32 v[76:77],v[76:77],v[60:61],v[54:55] op_sel:[0,0,1] op_sel_hi:[1,0,1]\n"
"v_pk_fma_f32 v[78:79],v[78:79],v[60:61],v[54:55] op_sel:[0,0,1] op_sel_hi:[1,0,1]\n"
CV "v112,v72,v73\n"
CV "v113,v74,v75\n"
CV "v114,v76,v77\n"
CV "v115,v78,v79\n"
MF "a[160:163],v[168:171],v[92:95],a[160:163]\n"
"ds_write_b128 v120,v[112:115] offset:8192\n"
AR "v80,a136\n"
AR "v81,a137\n"
AR "v82,a138\n"
MF "a[180:183],v[172:175],v[92:95],a[180:183]\n"
AR "v83,a139\n"
AR "v84,a156\n"
AR "v85,a157\n"
AR "v86,a158\n"
AR "v87,a159\n"
"v_pk_fma_f32 v[80:81],v[80:81],v[60:61],v[56:57] op_sel:[0,1,0] op_sel_hi:[1,1,0]\n"
"v_pk_fma_f32 v[82:83],v[82:83],v[60:61],v[56:57] op_sel:[0,1,0] op_sel_hi:[1,1,0]\n"
"v_pk_fma_f32 v[84:85],v[84:85],v[60:61],v[56:57] op_sel:[0,1,0] op_sel_hi:[1,1,0]\n"
"v_pk_fma_f32 v[86:87],v[86:87],v[60:61],v[56:57] op_sel:[0,1,0] op_sel_hi:[1,1,0]\n"
CV "v116,v80,v81\n"
CV "v117,v82,v83\n"
CV "v118,v84,v85\n"
CV "v119,v86,v87\n"
MF "a[164:167],v[168:171],v[96:99],a[164:167]\n"
"ds_write_b128 v120,v[116:119] offset:12288\n"
AR "v72,a160\n"
AR "v73,a161\n"
AR "v74,a162\n"
MF "a[184:187],v[172:175],v[96:99],a[184:187]\n"
AR "v75,a163\n"
AR "v76,a180\n"
AR "v77,a181\n"
AR "v78,a182\n"
AR "v79,a183\n"
"v_pk_fma_f32 v[72:73],v[72:73],v[56:57],v[52:53] op_sel:[0,1,0] op_sel_hi:[1,1,0]\n"
"v_pk_fma_f32 v[74:75],v[74:75],v[56:57],v[52:53] op_sel:[0,1,0] op_sel_hi:[1,1,0]\n"
"v_pk_fma_f32 v[76:77],v[76:77],v[56:57],v[52:53] op_sel:[0,1,0] op_sel_hi:[1,1,0]\n"
"v_pk_fma_f32 v[78:79],v[78:79],v[56:57],v[52:53] op_sel:[0,1,0] op_sel_hi:[1,1,0]\n"
CV "v112,v72,v73\n"
CV "v113,v74,v75\n"
CV "v114,v76,v77\n"
CV "v115,v78,v79\n"
MF "a[168:171],v[168:171],v[100:103],a[168:171]\n"
"ds_write_b128 v120,v[112:115] offset:16384\n"
AR "v80,a164\n"
AR "v81,a165\n"
AR "v82,a166\n"
MF "a[188:191],v[172:175],v[100:103],a[188:191]\n"
AR "v83,a167\n"
AR "v84,a184\n"
AR "v85,a185\n"
AR "v86,a186\n"
AR "v87,a187\n"
"v_pk_fma_f32 v[80:81],v[80:81],v[58:59],v[52:53] op_sel:[0,0,1] op_sel_hi:[1,0,1]\n"
"v_pk_fma_f32 v[82:83],v[82:83],v[58:59],v[52:53] op_sel:[0,0,1] op_sel_hi:[1,0,1]\n"
"v_pk_fma_f32 v[84:85],v[84:85],v[58:59],v[52:53] op_sel:[0,0,1] op_sel_hi:[1,0,1]\n"
"v_pk_fma_f32 v[86:87],v[86:87],v[58:59],v[52:53] op_sel:[0,0,1] op_sel_hi:[1,0,1]\n"
CV "v116,v80,v81\n"
CV "v117,v82,v83\n"
CV "v118,v84,v85\n"
CV "v119,v86,v87\n"
MF "a[172:175],v[168:171],v[104:107],a[172:175]\n"
"ds_write_b128 v120,v[116:119] offset:20480\n"
AR "v72,a168\n"
AR "v73,a169\n"
AR "v74,a170\n"
MF "a[192:195],v[172:175],v[104:107],a[192:195]\n"
AR "v75,a171\n"
AR "v76,a188\n"
AR "v77,a189\n"
AR "v78,a190\n"
AR "v79,a191\n"
"v_pk_fma_f32 v[72:73],v[72:73],v[58:59],v[54:55] op_sel:[0,1,0] op_sel_hi:[1,1,0]\n"
"v_pk_fma_f32 v[74:75],v[74:75],v[58:59],v[54:55] op_sel:[0,1,0] op_sel_hi:[1,1,0]\n"
"v_pk_fma_f32 v[76:77],v[76:77],v[58:59],v[54:55] op_sel:[0,1,0] op_sel_hi:[1,1,0]\n"
"v_pk_fma_f32 v[78:79],v[78:79],v[58:59],v[54:55] op_sel:[0,1,0] op_sel_hi:[1,1,0]\n"
CV "v112,v72,v73\n"
CV "v113,v74,v75\n"
CV "v114,v76,v77\n"
CV "v115,v78,v79\n"
MF "a[176:179],v[168:171],v[108:111],a[176:179]\n"
AR "v152,a240\n"
AW "a240,v112\n"
AR "v153,a241\n"
AW "a241,v113\n"
AR "v154,a242\n"
AW "a242,v114\n"
AR "v155,a243\n"
AW "a243,v115\n"
AR "v80,a172\n"
AR "v81,a173\n"
AR "v82,a174\n"
MF "a[196:199],v[172:175],v[108:111],a[196:199]\n"
AR "v83,a175\n"
AR "v84,a192\n"
AR "v85,a193\n"
AR "v86,a194\n"
AR "v87,a195\n"
"v_pk_fma_f32 v[80:81],v[80:81],v[60:61],v[54:55] op_sel:[0,0,1] op_sel_hi:[1,0,1]\n"
"v_pk_fma_f32 v[82:83],v[82:83],v[60:61],v[54:55] op_sel:[0,0,1] op_sel_hi:[1,0,1]\n"
"v_pk_fma_f32 v[84:85],v[84:85],v[60:61],v[54:55] op_sel:[0,0,1] op_sel_hi:[1,0,1]\n"
"v_pk_fma_f32 v[86:87],v[86:87],v[60:61],v[54:55] op_sel:[0,0,1] op_sel_hi:[1,0,1]\n"
CV "v116,v80,v81\n"
CV "v117,v82,v83\n"
CV "v118,v84,v85\n"
CV "v119,v86,v87\n"
MF "a[200:203],v[176:179],v[92:95],a[200:203]\n"
AR "v156,a244\n"
AW "a244,v116\n"
AR "v157,a245\n"
AW "a245,v117\n"
AR "v158,a246\n"
AW "a246,v118\n"
AR "v159,a247\n"
AW "a247,v119\n"
AR "v72,a176\n"
AR "v73,a177\n"
AR "v74,a178\n"
MF "a[220:223],v[180:183],v[92:95],a[220:223]\n"
AR "v75,a179\n"
AR "v76,a196\n"
AR "v77,a197\n"
AR "v78,a198\n"
AR "v79,a199\n"
"v_pk_fma_f32 v[72:73],v[72:73],v[60:61],v[56:57] op_sel:[0,1,0] op_sel_hi:[1,1,0]\n"
"v_pk_fma_f32 v[74:75],v[74:75],v[60:61],v[56:57] op_sel:[0,1,0] op_sel_hi:[1,1,0]\n"
"v_pk_fma_f32 v[76:77],v[76:77],v[60:61],v[56:57] op_sel:[0,1,0] op_sel_hi:[1,1,0]\n"
"v_pk_fma_f32 v[78:79],v[78:79],v[60:61],v[56:57] op_sel:[0,1,0] op_sel_hi:[1,1,0]\n"
CV "v112,v72,v73\n"
CV "v113,v74,v75\n"
CV "v114,v76,v77\n"
CV "v115,v78,v79\n"
MF "a[204:207],v[176:179],v[96:99],a[204:207]\n"
AR "v160,a248\n"
AW "a248,v112\n"
AR "v161,a249\n"
AW "a249,v113\n"
AR "v162,a250\n"
AW "a250,v114\n"
AR "v163,a251\n"
AW "a251,v115\n"
AR "v80,a200\n"
AR "v81,a201\n"
AR "v82,a202\n"
MF "a[224:227],v[180:183],v[96:99],a[224:227]\n"
AR "v83,a203\n"
AR "v84,a220\n"
AR "v85,a221\n"
AR "v86,a222\n"
AR "v87,a223\n"
"v_pk_fma_f32 v[80:81],v[80:81],v[56:57],v[52:53] op_sel:[0,1,0] op_sel_hi:[1,1,0]\n"
"v_pk_fma_f32 v[82:83],v[82:83],v[56:57],v[52:53] op_sel:[0,1,0] op_sel_hi:[1,1,0]\n"
"v_pk_fma_f32 v[84:85],v[84:85],v[56:57],v[52:53] op_sel:[0,1,0] op_sel_hi:[1,1,0]\n"
"v_pk_fma_f32 v[86:87],v[86:87],v[56:57],v[52:53] op_sel:[0,1,0] op_sel_hi:[1,1,0]\n"
CV "v116,v80,v81\n"
CV "v117,v82,v83\n"
CV "v118,v84,v85\n"
CV "v119,v86,v87\n"
MF "a[208:211],v[176:179],v[100:103],a[208:211]\n"
"v_mov_b32 v164,v62\n"
"v_mov_b32 v62,v116\n"
"v_mov_b32 v165,v63\n"
"v_mov_b32 v63,v117\n"
"v_mov_b32 v166,v64\n"
"v_mov_b32 v64,v118\n"
"v_mov_b32 v167,v65\n"
"v_mov_b32 v65,v119\n"
AR "v72,a204\n"
AR "v73,a205\n"
AR "v74,a206\n"
MF "a[228:231],v[180:183],v[100:103],a[228:231]\n"
AR "v75,a207\n"
AR "v76,a224\n"
AR "v77,a225\n"
AR "v78,a226\n"
AR "v79,a227\n"
"v_pk_fma_f32 v[72:73],v[72:73],v[58:59],v[52:53] op_sel:[0,0,1] op_sel_hi:[1,0,1]\n"
"v_pk_fma_f32 v[74:75],v[74:75],v[58:59],v[52:53] op_sel:[0,0,1] op_sel_hi:[1,0,1]\n"
"v_pk_fma_f32 v[76:77],v[76:77],v[58:59],v[52:53] op_sel:[0,0,1] op_sel_hi:[1,0,1]\n"
"v_pk_fma_f32 v[78:79],v[78:79],v[58:59],v[52:53] op_sel:[0,0,1] op_sel_hi:[1,0,1]\n"
CV "v112,v72,v73\n"
CV "v113,v74,v75\n"
CV "v114,v76,v77\n"
CV "v115,v78,v79\n"
MF "a[212:215],v[176:179],v[104:107],a[212:215]\n"
"v_mov_b32 v168,v66\n"
"v_mov_b32 v66,v112\n"
"v_mov_b32 v169,v67\n"
"v_mov_b32 v67,v113\n"
"v_mov_b32 v170,v68\n"
"v_mov_b32 v68,v114\n"
"v_mov_b32 v171,v69\n"
"v_mov_b32 v69,v115\n"
AR "v80,a208\n"
AR "v81,a209\n"
AR "v82,a210\n"
MF "a[232:235],v[180:183],v[104:107],a[232:235]\n"
AR "v83,a211\n"
AR "v84,a228\n"
AR "v85,a229\n"
AR "v86,a230\n"
AR "v87,a231\n"
"v_pk_fma_f32 v[80:81],v[80:81],v[58:59],v[54:55] op_sel:[0,1,0] op_sel_hi:[1,1,0]\n"
"v_pk_fma_f32 v[82:83],v[82:83],v[58:59],v[54:55] op_sel:[0,1,0] op_sel_hi:[1,1,0]\n"
"v_pk_fma_f32 v[84:85],v[84:85],v[58:59],v[54:55] op_sel:[0,1,0] op_sel_hi:[1,1,0]\n"
"v_pk_fma_f32 v[86:87],v[86:87],v[58:59],v[54:55] op_sel:[0,1,0] op_sel_hi:[1,1,0]\n"
CV "v116,v80,v81\n"
CV "v117,v82,v83\n"
CV "v118,v84,v85\n"
CV "v119,v86,v87\n"
MF "a[216:219],v[176:179],v[108:111],a[216:219]\n"
"v_mov_b32 v172,v70\n"
"v_mov_b32 v70,v116\n"
"v_mov_b32 v173,v71\n"
"v_mov_b32 v71,v117\n"
"v_mov_b32 v174,v132\n"
"v_mov_b32 v132,v118\n"
"v_mov_b32 v175,v133\n"
"v_mov_b32 v133,v119\n"
AR "v72,a212\n"
AR "v73,a213\n"
AR "v74,a214\n"
MF "a[236:239],v[180:183],v[108:111],a[236:239]\n"
AR "v75,a215\n"
AR "v76,a232\n"
AR "v77,a233\n"
AR "v78,a234\n"
AR "v79,a235\n"
"v_pk_fma_f32 v[72:73],v[72:73],v[60:61],v[54:55] op_sel:[0,0,1] op_sel_hi:[1,0,1]\n"
"v_pk_fma_f32 v[74:75],v[74:75],v[60:61],v[54:55] op_sel:[0,0,1] op_sel_hi:[1,0,1]\n"
"v_pk_fma_f32 v[76:77],v[76:77],v[60:61],v[54:55] op_sel:[0,0,1] op_sel_hi:[1,0,1]\n"
"v_pk_fma_f32 v[78:79],v[78:79],v[60:61],v[54:55] op_sel:[0,0,1] op_sel_hi:[1,0,1]\n"
CV "v112,v72,v73\n"
CV "v113,v74,v75\n"
CV "v114,v76,v77\n"
CV "v115,v78,v79\n"
"s_waitcnt vmcnt(0) lgkmcnt(0)\n"
"s_barrier\n"
"ds_read_b128 v[92:95],v0\n"
"ds_read_b128 v[96:99],v1\n"
".Lpg14g:\n"
"s_cbranch_execz .Lisr3\n"
"s_nop 0\n"
"ds_read_b128 v[100:103],v0 offset:1024\n"
"v_mov_b32 v176,v134\n"
"v_mov_b32 v134,v112\n"
"v_mov_b32 v177,v135\n"
"v_mov_b32 v135,v113\n"
"v_mov_b32 v178,v6\n"
"v_mov_b32 v6,v114\n"
"v_mov_b32 v179,v13\n"
"v_mov_b32 v13,v115\n"
AR "v80,a216\n"
AR "v81,a217\n"
AR "v82,a218\n"
AR "v83,a219\n"
AR "v84,a236\n"
AR "v85,a237\n"
AR "v86,a238\n"
AR "v87,a239\n"
"v_pk_fma_f32 v[80:81],v[80:81],v[60:61],v[56:57] op_sel:[0,1,0] op_sel_hi:[1,1,0]\n"
"v_pk_fma_f32 v[82:83],v[82:83],v[60:61],v[56:57] op_sel:[0,1,0] op_sel_hi:[1,1,0]\n"
"v_pk_fma_f32 v[84:85],v[84:85],v[60:61],v[56:57] op_sel:[0,1,0] op_sel_hi:[1,1,0]\n"
"v_pk_fma_f32 v[86:87],v[86:87],v[60:61],v[56:57] op_sel:[0,1,0] op_sel_hi:[1,1,0]\n"
CV "v116,v80,v81\n"
CV "v117,v82,v83\n"
CV "v118,v84,v85\n"
CV "v119,v86,v87\n"
"v_mov_b32 v180,v14\n"
"v_mov_b32 v14,v116\n"
"v_mov_b32 v181,v15\n"
"v_mov_b32 v15,v117\n"
AR "v182,a254\n"
AW "a254,v118\n"
AR "v183,a255\n"
AW "a255,v119\n"
"s_mov_b32 s13,s11\n"
"v_subrev_u32 v2,49152,v2\n"
"v_subrev_u32 v3,49152,v3\n"
"v_subrev_u32 v4,49152,v4\n"
"v_subrev_u32 v5,49152,v5\n"
"s_mov_b32 s57,2\n"
"s_mov_b32 s69,1\n"
"s_mov_b32 s61,3\n"
"s_mov_b32 s97,0\n"
"s_mov_b32 s95,0x47800000\n"
"s_mov_b32 s77,0x47800000\n"
"s_branch .Latt2\n"
".LpgattB_f:\n"
"s_cbranch_execz .Lisr3\n"
".LattB:\n"
AR "v2,a245\n"
"v_xor_b32 v3,32,v2\n"
"v_xor_b32_e64 v4,64,v2\n"
"v_xor_b32 v5,96,v2\n"
"s_mov_b32 s61,3\n"
".Latt2:\n"
"s_cmp_eq_u32 s57,1\n"
"s_cbranch_scc1 .Lt0k\n"
".Lt0n:\n"
"s_waitcnt lgkmcnt(2)\n"
MF "v[72:75],v[92:95],v[136:139],0\n"
"ds_read_b128 v[104:107],v1 offset:1024\n"
MF "v[76:79],v[92:95],v[140:143],0\n"
MF "v[80:83],v[92:95],v[144:147],0\n"
MF "v[84:87],v[92:95],v[148:151],0\n"
MF "v[88:91],v[92:95],v[152:155],0\n"
"s_waitcnt lgkmcnt(2)\n"
"ds_read_b128 v[92:95],v0 offset:2048\n"
MF "v[72:75],v[96:99],v[156:159],v[72:75]\n"
MF "v[76:79],v[96:99],v[160:163],v[76:79]\n"
MF "v[80:83],v[96:99],v[164:167],v[80:83]\n"
MF "v[84:87],v[96:99],v[168:171],v[84:87]\n"
MF "v[88:91],v[96:99],v[172:175],v[88:91]\n"
"s_waitcnt lgkmcnt(2)\n"
"ds_read_b128 v[96:99],v1 offset:2048\n"
MF "v[72:75],v[100:103],v[176:179],v[72:75]\n"
MF "v[76:79],v[100:103],v[180:183],v[76:79]\n"
MF "v[80:83],v[100:103],v[184:187],v[80:83]\n"
MF "v[84:87],v[100:103],v[188:191],v[84:87]\n"
MF "v[88:91],v[100:103],v[192:195],v[88:91]\n"
"s_waitcnt lgkmcnt(2)\n"
"ds_read_b128 v[100:103],v0 offset:6144\n"
MF "v[72:75],v[104:107],v[196:199],v[72:75]\n"
MF "v[76:79],v[104:107],v[200:203],v[76:79]\n"
MF "v[80:83],v[104:107],v[204:207],v[80:83]\n"
MF "v[84:87],v[104:107],v[208:211],v[84:87]\n"
MF "v[88:91],v[104:107],v[212:215],v[88:91]\n"
"s_waitcnt lgkmcnt(2)\n"
"ds_read_b128 v[104:107],v1 offset:6144\n"
MF "v[72:75],v[92:95],v[216:219],v[72:75]\n"
MF "v[76:79],v[92:95],v[220:223],v[76:79]\n"
MF "v[80:83],v[92:95],v[224:227],v[80:83]\n"
MF "v[84:87],v[92:95],v[228:231],v[84:87]\n"
MF "v[88:91],v[92:95],v[232:235],v[88:91]\n"
"s_waitcnt lgkmcnt(2)\n"
"ds_read_b128 v[92:95],v0 offset:7168\n"
MF "v[72:75],v[96:99],v[236:239],v[72:75]\n"
MF "v[76:79],v[96:99],v[240:243],v[76:79]\n"
MF "v[80:83],v[96:99],v[244:247],v[80:83]\n"
MF "v[84:87],v[96:99],v[248:251],v[84:87]\n"
MF "v[88:91],v[96:99],v[252:255],v[88:91]\n"
"s_waitcnt lgkmcnt(2)\n"
"ds_read_b128 v[96:99],v1 offset:7168\n"
MF "v[112:115],v[100:103],v[136:139],0\n"
MF "v[116:119],v[100:103],v[140:143],0\n"
MF "v[120:123],v[100:103],v[144:147],0\n"
MF "v[124:127],v[100:103],v[148:151],0\n"
MF "v[128:131],v[100:103],v[152:155],0\n"
"s_waitcnt lgkmcnt(2)\n"
"ds_read_b128 v[100:103],v0 offset:8192\n"
MF "v[112:115],v[104:107],v[156:159],v[112:115]\n"
"v_max3_f32 v52,v72,v73,v74\n"
"v_max_f32 v52,v52,v75\n"
MF "v[116:119],v[104:107],v[160:163],v[116:119]\n"
"v_max3_f32 v53,v76,v77,v78\n"
"v_max_f32 v53,v53,v79\n"
MF "v[120:123],v[104:107],v[164:167],v[120:123]\n"
"v_max3_f32 v54,v80,v81,v82\n"
"v_max_f32 v54,v54,v83\n"
MF "v[124:127],v[104:107],v[168:171],v[124:127]\n"
"v_max3_f32 v55,v84,v85,v86\n"
"v_max_f32 v55,v55,v87\n"
MF "v[128:131],v[104:107],v[172:175],v[128:131]\n"
"s_waitcnt lgkmcnt(2)\n"
"ds_read_b128 v[104:107],v1 offset:8192\n"
"v_max3_f32 v56,v88,v89,v90\n"
MF "v[112:115],v[92:95],v[176:179],v[112:115]\n"
"v_max_f32 v56,v56,v91\n"
MF "v[116:119],v[92:95],v[180:183],v[116:119]\n"
MF "v[120:123],v[92:95],v[184:187],v[120:123]\n"
MF "v[124:127],v[92:95],v[188:191],v[124:127]\n"
MF "v[128:131],v[92:95],v[192:195],v[128:131]\n"
"s_waitcnt lgkmcnt(2)\n"
MF "v[112:115],v[96:99],v[196:199],v[112:115]\n"
MF "v[116:119],v[96:99],v[200:203],v[116:119]\n"
"s_waitcnt lgkmcnt(0)\n"
"s_barrier\n"
MF "v[120:123],v[96:99],v[204:207],v[120:123]\n"
"s_add_u32 s72,s72,6144\n"
MF "v[124:127],v[96:99],v[208:211],v[124:127]\n"
"s_cmp_eq_u32 s72,s17\n"
"s_cselect_b32 s72,s16,s72\n"
"s_add_u32 m0,s64,0\n"
MF "v[128:131],v[96:99],v[212:215],v[128:131]\n"
LD "v7,s[32:35],s72 offen nt lds\n"
MF "v[112:115],v[100:103],v[216:219],v[112:115]\n"
"s_add_u32 m0,s64,896\n"
MF "v[116:119],v[100:103],v[220:223],v[116:119]\n"
LD "v7,s[32:35],s72 offen offset:128 nt lds\n"
MF "v[120:123],v[100:103],v[224:227],v[120:123]\n"
"s_add_u32 m0,s64,1792\n"
MF "v[124:127],v[100:103],v[228:231],v[124:127]\n"
LD "v7,s[32:35],s72 offen offset:256 nt lds\n"
MF "v[128:131],v[100:103],v[232:235],v[128:131]\n"
"s_add_u32 s72,s72,6144\n"
"s_add_u32 m0,s64,6144\n"
MF "v[112:115],v[104:107],v[236:239],v[112:115]\n"
LD "v7,s[32:35],s72 offen nt lds\n"
MF "v[116:119],v[104:107],v[240:243],v[116:119]\n"
"s_add_u32 m0,s64,7040\n"
MF "v[120:123],v[104:107],v[244:247],v[120:123]\n"
LD "v7,s[32:35],s72 offen offset:128 nt lds\n"
MF "v[124:127],v[104:107],v[248:251],v[124:127]\n"
"s_add_u32 m0,s64,7936\n"
MF "v[128:131],v[104:107],v[252:255],v[128:131]\n"
LD "v7,s[32:35],s72 offen offset:256 nt lds\n"
"s_branch .Lt0e\n"
".Lpgt0k_f:\n"
"s_cbranch_execz .Lisr3\n"
".Lt0k:\n"
"s_lshl_b32 s58,s56,10\n"
"s_add_u32 s58,s58,73728\n"
"v_add_u32 v32,s58,v9\n"
"v_add_u32 v33,65536,v32\n"
"s_waitcnt lgkmcnt(2)\n"
MF "v[72:75],v[92:95],v[136:139],0\n"
"ds_read_b128 v[104:107],v1 offset:1024\n"
AR "v62,a20\n"
MF "v[76:79],v[92:95],v[140:143],0\n"
"ds_write_b128 v32,a[0:3] offset:24576\n"
MF "v[80:83],v[92:95],v[144:147],0\n"
"ds_write_b128 v32,a[4:7] offset:28672\n"
MF "v[84:87],v[92:95],v[148:151],0\n"
AR "v63,a21\n"
AR "v64,a22\n"
MF "v[88:91],v[92:95],v[152:155],0\n"
"s_waitcnt lgkmcnt(4)\n"
"ds_read_b128 v[92:95],v0 offset:2048\n"
AR "v65,a23\n"
MF "v[72:75],v[96:99],v[156:159],v[72:75]\n"
"ds_write_b128 v32,a[8:11] offset:32768\n"
MF "v[76:79],v[96:99],v[160:163],v[76:79]\n"
AR "v66,a24\n"
AR "v67,a25\n"
MF "v[80:83],v[96:99],v[164:167],v[80:83]\n"
"ds_write_b128 v32,a[12:15] offset:36864\n"
MF "v[84:87],v[96:99],v[168:171],v[84:87]\n"
AR "v68,a26\n"
AR "v69,a27\n"
MF "v[88:91],v[96:99],v[172:175],v[88:91]\n"
"s_waitcnt lgkmcnt(6)\n"
"ds_read_b128 v[96:99],v1 offset:2048\n"
AR "v70,a28\n"
MF "v[72:75],v[100:103],v[176:179],v[72:75]\n"
"ds_write_b128 v32,a[16:19] offset:40960\n"
MF "v[76:79],v[100:103],v[180:183],v[76:79]\n"
AR "v71,a29\n"
AR "v132,a30\n"
MF "v[80:83],v[100:103],v[184:187],v[80:83]\n"
AR "v133,a31\n"
AR "v134,a32\n"
MF "v[84:87],v[100:103],v[188:191],v[84:87]\n"
"ds_write_b128 v32,a[40:43] offset:45056\n"
MF "v[88:91],v[100:103],v[192:195],v[88:91]\n"
"s_waitcnt lgkmcnt(8)\n"
"ds_read_b128 v[100:103],v0 offset:6144\n"
AR "v135,a33\n"
MF "v[72:75],v[104:107],v[196:199],v[72:75]\n"
"ds_write_b128 v32,a[44:47] offset:49152\n"
MF "v[76:79],v[104:107],v[200:203],v[76:79]\n"
AR "v6,a34\n"
AR "v13,a35\n"
MF "v[80:83],v[104:107],v[204:207],v[80:83]\n"
AR "v14,a36\n"
AR "v15,a37\n"
MF "v[84:87],v[104:107],v[208:211],v[84:87]\n"
"ds_write_b128 v32,a[48:51] offset:53248\n"
MF "v[88:91],v[104:107],v[212:215],v[88:91]\n"
"s_waitcnt lgkmcnt(8)\n"
"ds_read_b128 v[104:107],v1 offset:6144\n"
"v_accvgpr_mov_b32 a254,a38\n"
MF "v[72:75],v[92:95],v[216:219],v[72:75]\n"
"v_accvgpr_mov_b32 a255,a39\n"
"v_accvgpr_mov_b32 a240,a168\n"
MF "v[76:79],v[92:95],v[220:223],v[76:79]\n"
"ds_write_b128 v32,a[52:55] offset:57344\n"
MF "v[80:83],v[92:95],v[224:227],v[80:83]\n"
"v_accvgpr_mov_b32 a241,a169\n"
"v_accvgpr_mov_b32 a242,a170\n"
MF "v[84:87],v[92:95],v[228:231],v[84:87]\n"
"ds_write_b128 v32,a[56:59] offset:61440\n"
MF "v[88:91],v[92:95],v[232:235],v[88:91]\n"
"s_waitcnt lgkmcnt(8)\n"
"ds_read_b128 v[92:95],v0 offset:7168\n"
"v_accvgpr_mov_b32 a243,a171\n"
MF "v[72:75],v[96:99],v[236:239],v[72:75]\n"
"v_accvgpr_mov_b32 a244,a172\n"
"v_accvgpr_mov_b32 a245,a173\n"
MF "v[76:79],v[96:99],v[240:243],v[76:79]\n"
"ds_write_b128 v33,a[80:83]\n"
MF "v[80:83],v[96:99],v[244:247],v[80:83]\n"
"v_accvgpr_mov_b32 a246,a174\n"
"v_accvgpr_mov_b32 a247,a175\n"
MF "v[84:87],v[96:99],v[248:251],v[84:87]\n"
"v_accvgpr_mov_b32 a248,a176\n"
"v_accvgpr_mov_b32 a249,a177\n"
MF "v[88:91],v[96:99],v[252:255],v[88:91]\n"
"s_waitcnt lgkmcnt(7)\n"
"ds_read_b128 v[96:99],v1 offset:7168\n"
"v_accvgpr_mov_b32 a250,a178\n"
MF "v[112:115],v[100:103],v[136:139],0\n"
"ds_write_b128 v33,a[84:87] offset:4096\n"
MF "v[116:119],v[100:103],v[140:143],0\n"
"ds_write_b128 v33,a[88:91] offset:8192\n"
MF "v[120:123],v[100:103],v[144:147],0\n"
"v_accvgpr_mov_b32 a251,a179\n"
MF "v[124:127],v[100:103],v[148:151],0\n"
MF "v[128:131],v[100:103],v[152:155],0\n"
"s_waitcnt lgkmcnt(7)\n"
"ds_read_b128 v[100:103],v0 offset:8192\n"
MF "v[112:115],v[104:107],v[156:159],v[112:115]\n"
"v_max3_f32 v52,v72,v73,v74\n"
"v_max_f32 v52,v52,v75\n"
MF "v[116:119],v[104:107],v[160:163],v[116:119]\n"
"v_max3_f32 v53,v76,v77,v78\n"
"v_max_f32 v53,v53,v79\n"
MF "v[120:123],v[104:107],v[164:167],v[120:123]\n"
"v_max3_f32 v54,v80,v81,v82\n"
"v_max_f32 v54,v54,v83\n"
MF "v[124:127],v[104:107],v[168:171],v[124:127]\n"
"v_max3_f32 v55,v84,v85,v86\n"
"v_max_f32 v55,v55,v87\n"
MF "v[128:131],v[104:107],v[172:175],v[128:131]\n"
"s_waitcnt lgkmcnt(5)\n"
"ds_read_b128 v[104:107],v1 offset:8192\n"
"v_max3_f32 v56,v88,v89,v90\n"
MF "v[112:115],v[92:95],v[176:179],v[112:115]\n"
"v_max_f32 v56,v56,v91\n"
MF "v[116:119],v[92:95],v[180:183],v[116:119]\n"
"ds_write_b128 v33,a[92:95] offset:12288\n"
MF "v[120:123],v[92:95],v[184:187],v[120:123]\n"
"ds_write_b128 v33,a[96:99] offset:16384\n"
MF "v[124:127],v[92:95],v[188:191],v[124:127]\n"
"ds_write_b128 v33,a[120:123] offset:20480\n"
MF "v[128:131],v[92:95],v[192:195],v[128:131]\n"
"s_waitcnt lgkmcnt(7)\n"
"ds_write_b128 v32,a[124:127]\n"
MF "v[112:115],v[96:99],v[196:199],v[112:115]\n"
"ds_write_b128 v32,a[128:131] offset:4096\n"
MF "v[116:119],v[96:99],v[200:203],v[116:119]\n"
"s_waitcnt lgkmcnt(0)\n"
"s_barrier\n"
MF "v[120:123],v[96:99],v[204:207],v[120:123]\n"
"s_add_u32 s72,s72,6144\n"
"ds_write_b128 v32,a[132:135] offset:8192\n"
MF "v[124:127],v[96:99],v[208:211],v[124:127]\n"
"s_cmp_eq_u32 s72,s17\n"
"s_cselect_b32 s72,s16,s72\n"
"s_add_u32 m0,s64,0\n"
MF "v[128:131],v[96:99],v[212:215],v[128:131]\n"
LD "v7,s[32:35],s72 offen nt lds\n"
MF "v[112:115],v[100:103],v[216:219],v[112:115]\n"
"s_add_u32 m0,s64,896\n"
"ds_write_b128 v32,a[136:139] offset:12288\n"
MF "v[116:119],v[100:103],v[220:223],v[116:119]\n"
LD "v7,s[32:35],s72 offen offset:128 nt lds\n"
MF "v[120:123],v[100:103],v[224:227],v[120:123]\n"
"s_add_u32 m0,s64,1792\n"
"ds_write_b128 v32,a[160:163] offset:16384\n"
MF "v[124:127],v[100:103],v[228:231],v[124:127]\n"
LD "v7,s[32:35],s72 offen offset:256 nt lds\n"
MF "v[128:131],v[100:103],v[232:235],v[128:131]\n"
"s_add_u32 s72,s72,6144\n"
"s_add_u32 m0,s64,6144\n"
MF "v[112:115],v[104:107],v[236:239],v[112:115]\n"
LD "v7,s[32:35],s72 offen nt lds\n"
MF "v[116:119],v[104:107],v[240:243],v[116:119]\n"
"s_add_u32 m0,s64,7040\n"
"ds_write_b128 v32,a[164:167] offset:20480\n"
MF "v[120:123],v[104:107],v[244:247],v[120:123]\n"
LD "v7,s[32:35],s72 offen offset:128 nt lds\n"
MF "v[124:127],v[104:107],v[248:251],v[124:127]\n"
"s_add_u32 m0,s64,7936\n"
MF "v[128:131],v[104:107],v[252:255],v[128:131]\n"
LD "v7,s[32:35],s72 offen offset:256 nt lds\n"
".Lpgt0e_g:\n"
"s_cbranch_execz .Lpg15e\n"
".Lt0e:\n"
"s_nop 1\n"
"v_max3_f32 v52,v52,v112,v113\n"
"v_max3_f32 v53,v53,v116,v117\n"
"v_max3_f32 v54,v54,v120,v121\n"
"s_nop 0\n"
"v_max3_f32 v55,v55,v124,v125\n"
"s_nop 0\n"
"v_max3_f32 v56,v56,v128,v129\n"
"v_max3_f32 v52,v52,v114,v115\n"
"v_max3_f32 v53,v53,v118,v119\n"
"v_max3_f32 v54,v54,v122,v123\n"
"v_max3_f32 v55,v55,v126,v127\n"
"v_max3_f32 v56,v56,v130,v131\n"
"v_mov_b32 v57,v52\n"
"v_mov_b32 v58,v53\n"
"v_mov_b32 v59,v54\n"
"v_mov_b32 v60,v55\n"
"v_mov_b32 v61,v56\n"
"v_permlane16_swap_b32 v52,v57\n"
"v_permlane16_swap_b32 v53,v58\n"
"v_permlane16_swap_b32 v54,v59\n"
"v_permlane16_swap_b32 v55,v60\n"
"v_permlane16_swap_b32 v56,v61\n"
"v_max_f32 v52,v52,v57\n"
"v_max_f32 v53,v53,v58\n"
"v_max_f32 v54,v54,v59\n"
"v_max_f32 v55,v55,v60\n"
"v_max_f32 v56,v56,v61\n"
"v_mov_b32 v57,v52\n"
"v_mov_b32 v58,v53\n"
"v_mov_b32 v59,v54\n"
"v_mov_b32 v60,v55\n"
"v_mov_b32 v61,v56\n"
"v_permlane32_swap_b32 v52,v57\n"
"v_permlane32_swap_b32 v53,v58\n"
"v_permlane32_swap_b32 v54,v59\n"
"v_permlane32_swap_b32 v55,v60\n"
"v_permlane32_swap_b32 v56,v61\n"
"v_max_f32 v52,v52,v57\n"
"v_max_f32 v53,v53,v58\n"
"v_max_f32 v54,v54,v59\n"
"v_max_f32 v55,v55,v60\n"
"v_max_f32_e64 v56,v56,v61\n"
"v_sub_f32 v32,0xc1300000,v52\n"
"v_sub_f32 v36,0xc1300000,v53\n"
"v_sub_f32 v40,0xc1300000,v54\n"
"v_sub_f32 v44,0xc1300000,v55\n"
"v_sub_f32 v48,0xc1300000,v56\n"
"v_mov_b32 v33,v32\n"
"v_pk_mov_b32 v[34:35],v[32:33],v[32:33]\n"
"v_mov_b32 v37,v36\n"
"v_pk_mov_b32 v[38:39],v[36:37],v[36:37]\n"
"v_mov_b32_e64 v41,v40\n"
"v_pk_mov_b32 v[42:43],v[40:41],v[40:41]\n"
"v_mov_b32 v45,v44\n"
"v_pk_mov_b32 v[46:47],v[44:45],v[44:45]\n"
"v_mov_b32 v49,v48\n"
"v_pk_mov_b32 v[50:51],v[48:49],v[48:49]\n"
"v_pk_add_f32 v[72:73],v[72:73],v[32:33]\n"
"v_pk_add_f32 v[74:75],v[74:75],v[32:33]\n"
"v_pk_add_f32 v[112:113],v[112:113],v[32:33]\n"
"v_pk_add_f32 v[114:115],v[114:115],v[32:33]\n"
"v_exp_f32 v72,v72\n"
"v_exp_f32 v73,v73\n"
"v_exp_f32 v74,v74\n"
"v_exp_f32 v75,v75\n"
"v_exp_f32 v112,v112\n"
"v_exp_f32 v113,v113\n"
"v_exp_f32 v114,v114\n"
"v_exp_f32 v115,v115\n"
"v_pk_add_f32 v[10:11],v[72:73],v[74:75]\n"
"v_pk_add_f32 v[28:29],v[112:113],v[114:115]\n"
"v_pk_add_f32 v[10:11],v[10:11],v[28:29]\n"
"v_add_f32_e64 v52,v10,v11\n"
CV "v72,v72,v73\n"
CV "v73,v74,v75\n"
CV "v74,v112,v113\n"
CV "v75,v114,v115\n"
"v_pk_add_f32 v[76:77],v[76:77],v[36:37]\n"
"v_pk_add_f32 v[78:79],v[78:79],v[36:37]\n"
"v_pk_add_f32 v[116:117],v[116:117],v[36:37]\n"
"v_pk_add_f32 v[118:119],v[118:119],v[36:37]\n"
"v_exp_f32 v76,v76\n"
"v_exp_f32 v77,v77\n"
"v_exp_f32 v78,v78\n"
"v_exp_f32 v79,v79\n"
"v_exp_f32 v116,v116\n"
"v_exp_f32 v117,v117\n"
"v_exp_f32 v118,v118\n"
"v_exp_f32 v119,v119\n"
"v_pk_add_f32 v[10:11],v[76:77],v[78:79]\n"
"v_pk_add_f32 v[28:29],v[116:117],v[118:119]\n"
"v_pk_add_f32 v[10:11],v[10:11],v[28:29]\n"
"v_add_f32_e64 v53,v10,v11\n"
CV "v76,v76,v77\n"
CV "v77,v78,v79\n"
CV "v78,v116,v117\n"
CV "v79,v118,v119\n"
"v_pk_add_f32 v[80:81],v[80:81],v[40:41]\n"
"v_pk_add_f32 v[82:83],v[82:83],v[40:41]\n"
"v_pk_add_f32 v[120:121],v[120:121],v[40:41]\n"
"v_pk_add_f32 v[122:123],v[122:123],v[40:41]\n"
"v_exp_f32 v80,v80\n"
"v_exp_f32 v81,v81\n"
"v_exp_f32 v82,v82\n"
"v_exp_f32 v83,v83\n"
"v_exp_f32 v120,v120\n"
"v_exp_f32 v121,v121\n"
"v_exp_f32 v122,v122\n"
"v_exp_f32 v123,v123\n"
"v_pk_add_f32 v[10:11],v[80:81],v[82:83]\n"
"v_pk_add_f32 v[28:29],v[120:121],v[122:123]\n"
"v_pk_add_f32 v[10:11],v[10:11],v[28:29]\n"
"v_add_f32_e64 v54,v10,v11\n"
CV "v80,v80,v81\n"
CV "v81,v82,v83\n"
CV "v82,v120,v121\n"
CV "v83,v122,v123\n"
"v_pk_add_f32 v[84:85],v[84:85],v[44:45]\n"
"v_pk_add_f32 v[86:87],v[86:87],v[44:45]\n"
"v_pk_add_f32 v[124:125],v[124:125],v[44:45]\n"
"v_pk_add_f32 v[126:127],v[126:127],v[44:45]\n"
"v_mov_b32_e64 v55,0\n"
"v_pk_add_f32 v[88:89],v[88:89],v[48:49]\n"
"v_pk_add_f32 v[90:91],v[90:91],v[48:49]\n"
"v_pk_add_f32 v[128:129],v[128:129],v[48:49]\n"
"v_pk_add_f32 v[130:131],v[130:131],v[48:49]\n"
"v_mov_b32 v56,0\n"
"s_waitcnt vmcnt(12) lgkmcnt(0)\n"
"s_barrier\n"
"ds_read_b128 v[16:19],v0 offset:24576\n"
"ds_read_b128 v[20:23],v1 offset:24576\n"
AW "a220,s13\n"
AW "a221,s13\n"
AW "a222,s13\n"
AW "a223,s13\n"
"s_waitcnt lgkmcnt(1)\n"
MF "v[92:95],v[16:19],v[136:139],v[32:35]\n"
"s_nop 0\n"
"ds_read_b128 v[24:27],v0 offset:25600\n"
"ds_read_b128 v[28:31],v1 offset:25600\n"
"s_waitcnt lgkmcnt(2)\n"
MF "v[92:95],v[20:23],v[156:159],v[92:95]\n"
"v_exp_f32 v84,v84\n"
MF "v[96:99],v[20:23],v[160:163],v[36:39]\n"
"v_exp_f32 v85,v85\n"
MF "v[96:99],v[16:19],v[140:143],v[96:99]\n"
"v_exp_f32 v86,v86\n"
MF "v[100:103],v[16:19],v[144:147],v[40:43]\n"
"v_exp_f32 v87,v87\n"
MF "v[100:103],v[20:23],v[164:167],v[100:103]\n"
"v_add_f32 v55,v55,v84\n"
"v_add_f32 v55,v55,v85\n"
MF "v[104:107],v[20:23],v[168:171],v[44:47]\n"
CV "v84,v84,v85\n"
"v_add_f32 v55,v55,v86\n"
MF "v[104:107],v[16:19],v[148:151],v[104:107]\n"
"v_add_f32 v55,v55,v87\n"
CV "v85,v86,v87\n"
MF "v[108:111],v[16:19],v[152:155],v[48:51]\n"
"v_exp_f32 v124,v124\n"
MF "v[108:111],v[20:23],v[172:175],v[108:111]\n"
"s_waitcnt lgkmcnt(0)\n"
"ds_read_b128 v[16:19],v0 offset:26624\n"
"ds_read_b128 v[20:23],v1 offset:26624\n"
MF "v[108:111],v[24:27],v[192:195],v[108:111]\n"
"v_exp_f32 v125,v125\n"
MF "v[108:111],v[28:31],v[212:215],v[108:111]\n"
"v_exp_f32 v126,v126\n"
MF "v[104:107],v[28:31],v[208:211],v[104:107]\n"
"v_exp_f32 v127,v127\n"
MF "v[104:107],v[24:27],v[188:191],v[104:107]\n"
"v_add_f32 v55,v55,v124\n"
"v_add_f32 v55,v55,v125\n"
MF "v[100:103],v[24:27],v[184:187],v[100:103]\n"
CV "v86,v124,v125\n"
"v_add_f32 v55,v55,v126\n"
MF "v[100:103],v[28:31],v[204:207],v[100:103]\n"
"v_add_f32 v55,v55,v127\n"
CV "v87,v126,v127\n"
MF "v[96:99],v[28:31],v[200:203],v[96:99]\n"
"v_exp_f32 v88,v88\n"
MF "v[96:99],v[24:27],v[180:183],v[96:99]\n"
"v_exp_f32 v89,v89\n"
MF "v[92:95],v[24:27],v[176:179],v[92:95]\n"
"v_exp_f32 v90,v90\n"
MF "v[92:95],v[28:31],v[196:199],v[92:95]\n"
"s_waitcnt lgkmcnt(0)\n"
"ds_read_b128 v[24:27],v0 offset:30720\n"
"ds_read_b128 v[28:31],v1 offset:30720\n"
MF "v[92:95],v[16:19],v[216:219],v[92:95]\n"
"v_exp_f32 v91,v91\n"
MF "v[92:95],v[20:23],v[236:239],v[92:95]\n"
"v_add_f32 v56,v56,v88\n"
"v_add_f32 v56,v56,v89\n"
MF "v[96:99],v[20:23],v[240:243],v[96:99]\n"
CV "v88,v88,v89\n"
"v_add_f32 v56,v56,v90\n"
MF "v[96:99],v[16:19],v[220:223],v[96:99]\n"
"v_add_f32 v56,v56,v91\n"
CV "v89,v90,v91\n"
MF "v[100:103],v[16:19],v[224:227],v[100:103]\n"
"v_exp_f32 v128,v128\n"
MF "v[100:103],v[20:23],v[244:247],v[100:103]\n"
"v_exp_f32 v129,v129\n"
MF "v[104:107],v[20:23],v[248:251],v[104:107]\n"
"v_exp_f32 v130,v130\n"
MF "v[104:107],v[16:19],v[228:231],v[104:107]\n"
"v_exp_f32 v131,v131\n"
MF "v[108:111],v[16:19],v[232:235],v[108:111]\n"
"v_add_f32 v56,v56,v128\n"
"v_add_f32 v56,v56,v129\n"
MF "v[108:111],v[20:23],v[252:255],v[108:111]\n"
"s_waitcnt lgkmcnt(0)\n"
"ds_read_b128 v[16:19],v0 offset:31744\n"
"ds_read_b128 v[20:23],v1 offset:31744\n"
MF "v[112:115],v[24:27],v[136:139],v[32:35]\n"
CV "v90,v128,v129\n"
"v_add_f32 v56,v56,v130\n"
MF "v[112:115],v[28:31],v[156:159],v[112:115]\n"
"v_add_f32 v56,v56,v131\n"
CV "v91,v130,v131\n"
MF "v[116:119],v[28:31],v[160:163],v[36:39]\n"
"v_exp_f32 v92,v92\n"
MF "v[116:119],v[24:27],v[140:143],v[116:119]\n"
"v_exp_f32 v93,v93\n"
MF "v[120:123],v[24:27],v[144:147],v[40:43]\n"
"v_exp_f32 v94,v94\n"
MF "v[120:123],v[28:31],v[164:167],v[120:123]\n"
"v_exp_f32 v95,v95\n"
MF "v[124:127],v[28:31],v[168:171],v[44:47]\n"
"v_add_f32 v57,v52,v92\n"
"v_add_f32 v57,v57,v93\n"
MF "v[124:127],v[24:27],v[148:151],v[124:127]\n"
CV "v92,v92,v93\n"
"v_add_f32 v57,v57,v94\n"
MF "v[128:131],v[24:27],v[152:155],v[48:51]\n"
"v_add_f32 v57,v57,v95\n"
CV "v93,v94,v95\n"
MF "v[128:131],v[28:31],v[172:175],v[128:131]\n"
"s_waitcnt lgkmcnt(0)\n"
"ds_read_b128 v[24:27],v0 offset:32768\n"
"ds_read_b128 v[28:31],v1 offset:32768\n"
MF "v[128:131],v[16:19],v[192:195],v[128:131]\n"
"v_exp_f32 v96,v96\n"
MF "v[128:131],v[20:23],v[212:215],v[128:131]\n"
"v_exp_f32 v97,v97\n"
MF "v[124:127],v[20:23],v[208:211],v[124:127]\n"
"v_exp_f32 v98,v98\n"
MF "v[124:127],v[16:19],v[188:191],v[124:127]\n"
"v_exp_f32 v99,v99\n"
MF "v[120:123],v[16:19],v[184:187],v[120:123]\n"
"v_add_f32 v58,v53,v96\n"
"v_add_f32 v58,v58,v97\n"
MF "v[120:123],v[20:23],v[204:207],v[120:123]\n"
CV "v96,v96,v97\n"
"v_add_f32 v58,v58,v98\n"
MF "v[116:119],v[20:23],v[200:203],v[116:119]\n"
"v_add_f32 v58,v58,v99\n"
CV "v97,v98,v99\n"
MF "v[116:119],v[16:19],v[180:183],v[116:119]\n"
"s_waitcnt vmcnt(6) lgkmcnt(0)\n"
"s_barrier\n"
MF "v[112:115],v[16:19],v[176:179],v[112:115]\n"
"v_exp_f32 v100,v100\n"
MF "v[112:115],v[20:23],v[196:199],v[112:115]\n"
"s_nop 0\n"
TR "v[16:17],v2 offset:49152\n"
TR "v[18:19],v2 offset:55296\n"
MF "v[112:115],v[24:27],v[216:219],v[112:115]\n"
"s_add_u32 s73,s73,12288\n"
TR "v[20:21],v3 offset:49152\n"
TR "v[22:23],v3 offset:55296\n"
MF "v[112:115],v[28:31],v[236:239],v[112:115]\n"
"s_cmp_eq_u32 s73,s19\n"
"s_cselect_b32 s73,s18,s73\n"
"s_add_u32 m0,s65,24576\n"
MF "v[116:119],v[28:31],v[240:243],v[116:119]\n"
LD "v8,s[36:39],s73 offen nt lds\n"
MF "v[116:119],v[24:27],v[220:223],v[116:119]\n"
"s_add_u32 m0,s65,25472\n"
"v_exp_f32 v101,v101\n"
MF "v[120:123],v[24:27],v[224:227],v[120:123]\n"
LD "v8,s[36:39],s73 offen offset:128 nt lds\n"
MF "v[120:123],v[28:31],v[244:247],v[120:123]\n"
"v_exp_f32 v102,v102\n"
MF "v[124:127],v[28:31],v[248:251],v[124:127]\n"
".Lpg15e:\n"
"s_cbranch_execz .Lisr3\n"
"s_add_u32 m0,s65,26368\n"
"v_exp_f32 v103,v103\n"
MF "v[124:127],v[24:27],v[228:231],v[124:127]\n"
LD "v8,s[36:39],s73 offen offset:256 nt lds\n"
MF "v[128:131],v[24:27],v[232:235],v[128:131]\n"
"v_add_f32 v59,v54,v100\n"
"v_add_f32 v59,v59,v101\n"
MF "v[128:131],v[28:31],v[252:255],v[128:131]\n"
"s_waitcnt lgkmcnt(0)\n"
TR "v[24:25],v4 offset:49152\n"
TR "v[26:27],v4 offset:55296\n"
MF "a[0:3],v[16:19],v[72:75],a[220:223]\n"
"s_add_u32 m0,s65,24576\n"
TR "v[28:29],v5 offset:49152\n"
TR "v[30:31],v5 offset:55296\n"
MF "a[20:23],v[20:23],v[72:75],a[220:223]\n"
LD "v8,s[36:39],s73 offen offset:3072 nt lds\n"
MF "a[24:27],v[20:23],v[76:79],a[220:223]\n"
CV "v100,v100,v101\n"
"v_add_f32 v59,v59,v102\n"
MF "a[4:7],v[16:19],v[76:79],a[220:223]\n"
"s_add_u32 m0,s65,25472\n"
"v_add_f32 v59,v59,v103\n"
CV "v101,v102,v103\n"
MF "a[8:11],v[16:19],v[80:83],a[220:223]\n"
LD "v8,s[36:39],s73 offen offset:3200 nt lds\n"
MF "a[28:31],v[20:23],v[80:83],a[220:223]\n"
"v_exp_f32 v104,v104\n"
MF "a[32:35],v[20:23],v[84:87],a[220:223]\n"
"s_add_u32 m0,s65,26368\n"
"v_exp_f32 v105,v105\n"
MF "a[12:15],v[16:19],v[84:87],a[220:223]\n"
LD "v8,s[36:39],s73 offen offset:3328 nt lds\n"
MF "a[16:19],v[16:19],v[88:91],a[220:223]\n"
"v_exp_f32 v106,v106\n"
MF "a[36:39],v[20:23],v[88:91],a[220:223]\n"
"s_waitcnt lgkmcnt(0)\n"
TR "v[16:17],v2 offset:50176\n"
TR "v[18:19],v2 offset:56320\n"
MF "a[56:59],v[24:27],v[88:91],a[220:223]\n"
"s_nop 0\n"
TR "v[20:21],v3 offset:50176\n"
TR "v[22:23],v3 offset:56320\n"
MF "a[76:79],v[28:31],v[88:91],a[220:223]\n"
"v_exp_f32 v107,v107\n"
MF "a[72:75],v[28:31],v[84:87],a[220:223]\n"
"v_add_f32 v60,v55,v104\n"
"v_add_f32 v60,v60,v105\n"
MF "a[52:55],v[24:27],v[84:87],a[220:223]\n"
CV "v104,v104,v105\n"
"v_add_f32 v60,v60,v106\n"
MF "a[48:51],v[24:27],v[80:83],a[220:223]\n"
"v_add_f32 v60,v60,v107\n"
CV "v105,v106,v107\n"
MF "a[68:71],v[28:31],v[80:83],a[220:223]\n"
"v_exp_f32 v108,v108\n"
MF "a[64:67],v[28:31],v[76:79],a[220:223]\n"
"v_exp_f32 v109,v109\n"
MF "a[44:47],v[24:27],v[76:79],a[220:223]\n"
"v_exp_f32 v110,v110\n"
MF "a[40:43],v[24:27],v[72:75],a[220:223]\n"
"v_exp_f32 v111,v111\n"
MF "a[60:63],v[28:31],v[72:75],a[220:223]\n"
"s_waitcnt lgkmcnt(0)\n"
TR "v[24:25],v4 offset:50176\n"
TR "v[26:27],v4 offset:56320\n"
MF "a[80:83],v[16:19],v[72:75],a[220:223]\n"
"s_nop 0\n"
TR "v[28:29],v5 offset:50176\n"
TR "v[30:31],v5 offset:56320\n"
MF "a[100:103],v[20:23],v[72:75],a[220:223]\n"
"v_add_f32 v61,v56,v108\n"
"v_add_f32 v61,v61,v109\n"
MF "a[104:107],v[20:23],v[76:79],a[220:223]\n"
CV "v108,v108,v109\n"
"v_add_f32 v61,v61,v110\n"
MF "a[84:87],v[16:19],v[76:79],a[220:223]\n"
"v_add_f32 v61,v61,v111\n"
CV "v109,v110,v111\n"
MF "a[88:91],v[16:19],v[80:83],a[220:223]\n"
"v_exp_f32 v112,v112\n"
MF "a[108:111],v[20:23],v[80:83],a[220:223]\n"
"v_exp_f32 v113,v113\n"
MF "a[112:115],v[20:23],v[84:87],a[220:223]\n"
"v_exp_f32 v114,v114\n"
MF "a[92:95],v[16:19],v[84:87],a[220:223]\n"
"v_exp_f32 v115,v115\n"
MF "a[96:99],v[16:19],v[88:91],a[220:223]\n"
"v_add_f32 v57,v57,v112\n"
"v_add_f32 v57,v57,v113\n"
MF "a[116:119],v[20:23],v[88:91],a[220:223]\n"
"s_waitcnt lgkmcnt(0)\n"
TR "v[16:17],v2 offset:51200\n"
TR "v[18:19],v2 offset:57344\n"
MF "a[136:139],v[24:27],v[88:91],a[220:223]\n"
"s_nop 0\n"
TR "v[20:21],v3 offset:51200\n"
TR "v[22:23],v3 offset:57344\n"
MF "a[156:159],v[28:31],v[88:91],a[220:223]\n"
CV "v94,v112,v113\n"
"v_add_f32 v57,v57,v114\n"
MF "a[152:155],v[28:31],v[84:87],a[220:223]\n"
"v_add_f32 v57,v57,v115\n"
CV "v95,v114,v115\n"
MF "a[132:135],v[24:27],v[84:87],a[220:223]\n"
"v_exp_f32 v116,v116\n"
MF "a[128:131],v[24:27],v[80:83],a[220:223]\n"
"v_exp_f32 v117,v117\n"
MF "a[148:151],v[28:31],v[80:83],a[220:223]\n"
"v_exp_f32 v118,v118\n"
MF "a[144:147],v[28:31],v[76:79],a[220:223]\n"
"v_exp_f32 v119,v119\n"
MF "a[124:127],v[24:27],v[76:79],a[220:223]\n"
"v_add_f32 v58,v58,v116\n"
"v_add_f32 v58,v58,v117\n"
MF "a[120:123],v[24:27],v[72:75],a[220:223]\n"
CV "v98,v116,v117\n"
"v_add_f32 v58,v58,v118\n"
MF "a[140:143],v[28:31],v[72:75],a[220:223]\n"
"s_waitcnt lgkmcnt(0)\n"
TR "v[24:25],v4 offset:51200\n"
TR "v[26:27],v4 offset:57344\n"
MF "a[160:163],v[16:19],v[72:75],a[220:223]\n"
"s_nop 0\n"
TR "v[28:29],v5 offset:51200\n"
TR "v[30:31],v5 offset:57344\n"
MF "a[180:183],v[20:23],v[72:75],a[220:223]\n"
"v_add_f32 v58,v58,v119\n"
CV "v99,v118,v119\n"
MF "a[184:187],v[20:23],v[76:79],a[220:223]\n"
"v_exp_f32 v120,v120\n"
MF "a[164:167],v[16:19],v[76:79],a[220:223]\n"
"v_exp_f32 v121,v121\n"
MF "a[168:171],v[16:19],v[80:83],a[220:223]\n"
"v_exp_f32 v122,v122\n"
MF "a[188:191],v[20:23],v[80:83],a[220:223]\n"
"v_exp_f32 v123,v123\n"
MF "a[192:195],v[20:23],v[84:87],a[220:223]\n"
"v_add_f32 v59,v59,v120\n"
"v_add_f32 v59,v59,v121\n"
MF "a[172:175],v[16:19],v[84:87],a[220:223]\n"
CV "v102,v120,v121\n"
"v_add_f32 v59,v59,v122\n"
MF "a[176:179],v[16:19],v[88:91],a[220:223]\n"
"s_waitcnt vmcnt(6) lgkmcnt(0)\n"
"s_barrier\n"
MF "a[196:199],v[20:23],v[88:91],a[220:223]\n"
"s_add_u32 s72,s72,6144\n"
"ds_read_b128 v[16:19],v0\n"
"ds_read_b128 v[20:23],v1\n"
MF "a[216:219],v[24:27],v[88:91],a[220:223]\n"
"s_cmp_eq_u32 s72,s17\n"
"s_cselect_b32 s72,s16,s72\n"
"s_add_u32 m0,s64,49152\n"
MF "a[236:239],v[28:31],v[88:91],a[220:223]\n"
LD "v7,s[32:35],s72 offen nt lds\n"
MF "a[232:235],v[28:31],v[84:87],a[220:223]\n"
"s_add_u32 m0,s64,50048\n"
"v_add_f32 v59,v59,v123\n"
CV "v103,v122,v123\n"
MF "a[212:215],v[24:27],v[84:87],a[220:223]\n"
LD "v7,s[32:35],s72 offen offset:128 nt lds\n"
MF "a[208:211],v[24:27],v[80:83],a[220:223]\n"
"s_add_u32 m0,s64,50944\n"
MF "a[228:231],v[28:31],v[80:83],a[220:223]\n"
LD "v7,s[32:35],s72 offen offset:256 nt lds\n"
MF "a[224:227],v[28:31],v[76:79],a[220:223]\n"
MF "a[204:207],v[24:27],v[76:79],a[220:223]\n"
MF "a[200:203],v[24:27],v[72:75],a[220:223]\n"
MF "a[220:223],v[28:31],v[72:75],a[220:223]\n"
"s_waitcnt lgkmcnt(0)\n"
"s_branch .Lmid\n"
".Laloop:\n"
MF "v[72:75],v[16:19],v[136:139],v[32:35]\n"
"s_nop 0\n"
"ds_read_b128 v[24:27],v0 offset:25600\n"
"ds_read_b128 v[28:31],v1 offset:25600\n"
MF "v[72:75],v[20:23],v[156:159],v[72:75]\n"
MF "v[76:79],v[20:23],v[160:163],v[36:39]\n"
MF "v[76:79],v[16:19],v[140:143],v[76:79]\n"
MF "v[80:83],v[16:19],v[144:147],v[40:43]\n"
MF "v[80:83],v[20:23],v[164:167],v[80:83]\n"
MF "v[84:87],v[20:23],v[168:171],v[44:47]\n"
MF "v[84:87],v[16:19],v[148:151],v[84:87]\n"
"s_add_u32 s72,s72,6144\n"
"s_add_u32 m0,s64,6144\n"
MF "v[88:91],v[16:19],v[152:155],v[48:51]\n"
LD "v7,s[32:35],s72 offen nt lds\n"
MF "v[88:91],v[20:23],v[172:175],v[88:91]\n"
"s_waitcnt lgkmcnt(0)\n"
"ds_read_b128 v[16:19],v0 offset:26624\n"
"ds_read_b128 v[20:23],v1 offset:26624\n"
MF "v[88:91],v[24:27],v[192:195],v[88:91]\n"
"v_exp_f32 v124,v124\n"
MF "v[88:91],v[28:31],v[212:215],v[88:91]\n"
"s_add_u32 m0,s64,7040\n"
"v_exp_f32 v125,v125\n"
MF "v[84:87],v[28:31],v[208:211],v[84:87]\n"
LD "v7,s[32:35],s72 offen offset:128 nt lds\n"
MF "v[84:87],v[24:27],v[188:191],v[84:87]\n"
"v_exp_f32 v126,v126\n"
MF "v[80:83],v[24:27],v[184:187],v[80:83]\n"
"v_exp_f32 v127,v127\n"
MF "v[80:83],v[28:31],v[204:207],v[80:83]\n"
"s_add_u32 m0,s64,7936\n"
"v_add_f32 v60,v60,v124\n"
"v_add_f32 v60,v60,v125\n"
MF "v[76:79],v[28:31],v[200:203],v[76:79]\n"
LD "v7,s[32:35],s72 offen offset:256 nt lds\n"
MF "v[76:79],v[24:27],v[180:183],v[76:79]\n"
CV "v106,v124,v125\n"
"v_add_f32 v60,v60,v126\n"
MF "v[72:75],v[24:27],v[176:179],v[72:75]\n"
"v_add_f32 v60,v60,v127\n"
CV "v107,v126,v127\n"
MF "v[72:75],v[28:31],v[196:199],v[72:75]\n"
"s_waitcnt lgkmcnt(0)\n"
"ds_read_b128 v[24:27],v0 offset:30720\n"
"ds_read_b128 v[28:31],v1 offset:30720\n"
MF "v[72:75],v[16:19],v[216:219],v[72:75]\n"
"v_exp_f32 v128,v128\n"
MF "v[72:75],v[20:23],v[236:239],v[72:75]\n"
"v_exp_f32 v129,v129\n"
MF "v[76:79],v[20:23],v[240:243],v[76:79]\n"
"v_exp_f32 v130,v130\n"
MF "v[76:79],v[16:19],v[220:223],v[76:79]\n"
"v_exp_f32 v131,v131\n"
MF "v[80:83],v[16:19],v[224:227],v[80:83]\n"
"v_add_f32 v61,v61,v128\n"
"v_add_f32 v61,v61,v129\n"
MF "v[80:83],v[20:23],v[244:247],v[80:83]\n"
CV "v110,v128,v129\n"
"v_add_f32 v61,v61,v130\n"
MF "v[84:87],v[20:23],v[248:251],v[84:87]\n"
"v_add_f32 v61,v61,v131\n"
CV "v111,v130,v131\n"
MF "v[84:87],v[16:19],v[228:231],v[84:87]\n"
"v_max3_f32 v10,v57,v58,v59\n"
"v_max3_f32 v10,v10,v60,v61\n"
MF "v[88:91],v[16:19],v[232:235],v[88:91]\n"
"v_cmp_ngt_f32 vcc,s77,v10\n"
MF "v[88:91],v[20:23],v[252:255],v[88:91]\n"
"s_waitcnt lgkmcnt(0)\n"
"s_cbranch_vccnz .Lstub_11\n"
".Lback_11:\n"
MF "v[112:115],v[24:27],v[136:139],v[32:35]\n"
"s_add_u32 s74,s74,12288\n"
"ds_read_b128 v[16:19],v0 offset:31744\n"
"ds_read_b128 v[20:23],v1 offset:31744\n"
MF "v[112:115],v[28:31],v[156:159],v[112:115]\n"
"s_cmp_eq_u32 s74,s20\n"
"s_cselect_b32 s74,s70,s74\n"
MF "v[116:119],v[28:31],v[160:163],v[36:39]\n"
"v_exp_f32 v72,v72\n"
MF "v[116:119],v[24:27],v[140:143],v[116:119]\n"
"v_exp_f32 v73,v73\n"
MF "v[120:123],v[24:27],v[144:147],v[40:43]\n"
"v_exp_f32 v74,v74\n"
MF "v[120:123],v[28:31],v[164:167],v[120:123]\n"
"v_exp_f32 v75,v75\n"
MF "v[124:127],v[28:31],v[168:171],v[44:47]\n"
"v_add_f32 v52,v57,v72\n"
"v_add_f32 v52,v52,v73\n"
MF "v[124:127],v[24:27],v[148:151],v[124:127]\n"
CV "v72,v72,v73\n"
"v_add_f32 v52,v52,v74\n"
MF "v[128:131],v[24:27],v[152:155],v[48:51]\n"
"v_add_f32 v52,v52,v75\n"
CV "v73,v74,v75\n"
MF "v[128:131],v[28:31],v[172:175],v[128:131]\n"
"s_waitcnt lgkmcnt(0)\n"
"ds_read_b128 v[24:27],v0 offset:32768\n"
"ds_read_b128 v[28:31],v1 offset:32768\n"
MF "v[128:131],v[16:19],v[192:195],v[128:131]\n"
"v_exp_f32 v76,v76\n"
MF "v[128:131],v[20:23],v[212:215],v[128:131]\n"
"v_exp_f32 v77,v77\n"
MF "v[124:127],v[20:23],v[208:211],v[124:127]\n"
"v_exp_f32 v78,v78\n"
MF "v[124:127],v[16:19],v[188:191],v[124:127]\n"
"v_exp_f32 v79,v79\n"
MF "v[120:123],v[16:19],v[184:187],v[120:123]\n"
"v_add_f32 v53,v58,v76\n"
"v_add_f32 v53,v53,v77\n"
MF "v[120:123],v[20:23],v[204:207],v[120:123]\n"
CV "v76,v76,v77\n"
"v_add_f32 v53,v53,v78\n"
MF "v[116:119],v[20:23],v[200:203],v[116:119]\n"
"v_add_f32 v53,v53,v79\n"
CV "v77,v78,v79\n"
MF "v[116:119],v[16:19],v[180:183],v[116:119]\n"
"s_waitcnt vmcnt(6) lgkmcnt(0)\n"
"s_barrier\n"
MF "v[112:115],v[16:19],v[176:179],v[112:115]\n"
"v_exp_f32 v80,v80\n"
MF "v[112:115],v[20:23],v[196:199],v[112:115]\n"
"s_nop 0\n"
TR "v[16:17],v2 offset:49152\n"
TR "v[18:19],v2 offset:55296\n"
MF "v[112:115],v[24:27],v[216:219],v[112:115]\n"
"s_add_u32 s73,s73,12288\n"
TR "v[20:21],v3 offset:49152\n"
TR "v[22:23],v3 offset:55296\n"
MF "v[112:115],v[28:31],v[236:239],v[112:115]\n"
"s_cmp_eq_u32 s73,s19\n"
"s_cselect_b32 s73,s18,s73\n"
"s_add_u32 m0,s65,24576\n"
MF "v[116:119],v[28:31],v[240:243],v[116:119]\n"
LD "v8,s[36:39],s73 offen nt lds\n"
MF "v[116:119],v[24:27],v[220:223],v[116:119]\n"
"s_add_u32 m0,s65,25472\n"
"v_exp_f32 v81,v81\n"
MF "v[120:123],v[24:27],v[224:227],v[120:123]\n"
LD "v8,s[36:39],s73 offen offset:128 nt lds\n"
MF "v[120:123],v[28:31],v[244:247],v[120:123]\n"
"v_exp_f32 v82,v82\n"
MF "v[124:127],v[28:31],v[248:251],v[124:127]\n"
"s_add_u32 m0,s65,26368\n"
"v_exp_f32 v83,v83\n"
MF "v[124:127],v[24:27],v[228:231],v[124:127]\n"
LD "v8,s[36:39],s73 offen offset:256 nt lds\n"
MF "v[128:131],v[24:27],v[232:235],v[128:131]\n"
"v_add_f32 v54,v59,v80\n"
"v_add_f32 v54,v54,v81\n"
MF "v[128:131],v[28:31],v[252:255],v[128:131]\n"
"s_waitcnt lgkmcnt(0)\n"
TR "v[24:25],v4 offset:49152\n"
TR "v[26:27],v4 offset:55296\n"
MF "a[0:3],v[16:19],v[92:95],a[0:3]\n"
"s_add_u32 m0,s65,24576\n"
TR "v[28:29],v5 offset:49152\n"
TR "v[30:31],v5 offset:55296\n"
MF "a[20:23],v[20:23],v[92:95],a[20:23]\n"
LD "v8,s[36:39],s73 offen offset:3072 nt lds\n"
MF "a[24:27],v[20:23],v[96:99],a[24:27]\n"
CV "v80,v80,v81\n"
"v_add_f32 v54,v54,v82\n"
MF "a[4:7],v[16:19],v[96:99],a[4:7]\n"
"s_add_u32 m0,s65,25472\n"
"v_add_f32 v54,v54,v83\n"
CV "v81,v82,v83\n"
MF "a[8:11],v[16:19],v[100:103],a[8:11]\n"
LD "v8,s[36:39],s73 offen offset:3200 nt lds\n"
MF "a[28:31],v[20:23],v[100:103],a[28:31]\n"
"v_exp_f32 v84,v84\n"
MF "a[32:35],v[20:23],v[104:107],a[32:35]\n"
"s_add_u32 m0,s65,26368\n"
"v_exp_f32 v85,v85\n"
MF "a[12:15],v[16:19],v[104:107],a[12:15]\n"
LD "v8,s[36:39],s73 offen offset:3328 nt lds\n"
MF "a[16:19],v[16:19],v[108:111],a[16:19]\n"
"v_exp_f32 v86,v86\n"
MF "a[36:39],v[20:23],v[108:111],a[36:39]\n"
"s_waitcnt lgkmcnt(0)\n"
TR "v[16:17],v2 offset:50176\n"
TR "v[18:19],v2 offset:56320\n"
MF "a[56:59],v[24:27],v[108:111],a[56:59]\n"
"s_nop 0\n"
TR "v[20:21],v3 offset:50176\n"
TR "v[22:23],v3 offset:56320\n"
MF "a[76:79],v[28:31],v[108:111],a[76:79]\n"
"v_exp_f32 v87,v87\n"
MF "a[72:75],v[28:31],v[104:107],a[72:75]\n"
"v_add_f32 v55,v60,v84\n"
"v_add_f32 v55,v55,v85\n"
MF "a[52:55],v[24:27],v[104:107],a[52:55]\n"
CV "v84,v84,v85\n"
"v_add_f32 v55,v55,v86\n"
MF "a[48:51],v[24:27],v[100:103],a[48:51]\n"
"v_add_f32 v55,v55,v87\n"
CV "v85,v86,v87\n"
MF "a[68:71],v[28:31],v[100:103],a[68:71]\n"
"v_exp_f32 v88,v88\n"
MF "a[64:67],v[28:31],v[96:99],a[64:67]\n"
"v_exp_f32 v89,v89\n"
MF "a[44:47],v[24:27],v[96:99],a[44:47]\n"
"v_exp_f32 v90,v90\n"
MF "a[40:43],v[24:27],v[92:95],a[40:43]\n"
"v_exp_f32 v91,v91\n"
MF "a[60:63],v[28:31],v[92:95],a[60:63]\n"
"s_waitcnt lgkmcnt(0)\n"
TR "v[24:25],v4 offset:50176\n"
TR "v[26:27],v4 offset:56320\n"
MF "a[80:83],v[16:19],v[92:95],a[80:83]\n"
"s_nop 0\n"
TR "v[28:29],v5 offset:50176\n"
TR "v[30:31],v5 offset:56320\n"
MF "a[100:103],v[20:23],v[92:95],a[100:103]\n"
"v_add_f32 v56,v61,v88\n"
"v_add_f32 v56,v56,v89\n"
MF "a[104:107],v[20:23],v[96:99],a[104:107]\n"
CV "v88,v88,v89\n"
"v_add_f32 v56,v56,v90\n"
MF "a[84:87],v[16:19],v[96:99],a[84:87]\n"
"v_add_f32 v56,v56,v91\n"
CV "v89,v90,v91\n"
MF "a[88:91],v[16:19],v[100:103],a[88:91]\n"
"v_exp_f32 v112,v112\n"
MF "a[108:111],v[20:23],v[100:103],a[108:111]\n"
"v_exp_f32 v113,v113\n"
MF "a[112:115],v[20:23],v[104:107],a[112:115]\n"
"v_exp_f32 v114,v114\n"
MF "a[92:95],v[16:19],v[104:107],a[92:95]\n"
"v_exp_f32 v115,v115\n"
MF "a[96:99],v[16:19],v[108:111],a[96:99]\n"
"v_add_f32 v52,v52,v112\n"
"v_add_f32 v52,v52,v113\n"
MF "a[116:119],v[20:23],v[108:111],a[116:119]\n"
"s_waitcnt lgkmcnt(0)\n"
TR "v[16:17],v2 offset:51200\n"
TR "v[18:19],v2 offset:57344\n"
MF "a[136:139],v[24:27],v[108:111],a[136:139]\n"
"s_nop 0\n"
TR "v[20:21],v3 offset:51200\n"
TR "v[22:23],v3 offset:57344\n"
MF "a[156:159],v[28:31],v[108:111],a[156:159]\n"
CV "v74,v112,v113\n"
"v_add_f32 v52,v52,v114\n"
MF "a[152:155],v[28:31],v[104:107],a[152:155]\n"
"v_add_f32 v52,v52,v115\n"
CV "v75,v114,v115\n"
MF "a[132:135],v[24:27],v[104:107],a[132:135]\n"
"v_exp_f32 v116,v116\n"
MF "a[128:131],v[24:27],v[100:103],a[128:131]\n"
"v_exp_f32 v117,v117\n"
MF "a[148:151],v[28:31],v[100:103],a[148:151]\n"
"v_exp_f32 v118,v118\n"
MF "a[144:147],v[28:31],v[96:99],a[144:147]\n"
"v_exp_f32 v119,v119\n"
MF "a[124:127],v[24:27],v[96:99],a[124:127]\n"
"v_add_f32 v53,v53,v116\n"
"v_add_f32 v53,v53,v117\n"
MF "a[120:123],v[24:27],v[92:95],a[120:123]\n"
CV "v78,v116,v117\n"
"v_add_f32 v53,v53,v118\n"
MF "a[140:143],v[28:31],v[92:95],a[140:143]\n"
"s_waitcnt lgkmcnt(0)\n"
TR "v[24:25],v4 offset:51200\n"
TR "v[26:27],v4 offset:57344\n"
MF "a[160:163],v[16:19],v[92:95],a[160:163]\n"
"s_nop 0\n"
TR "v[28:29],v5 offset:51200\n"
TR "v[30:31],v5 offset:57344\n"
MF "a[180:183],v[20:23],v[92:95],a[180:183]\n"
"v_add_f32 v53,v53,v119\n"
CV "v79,v118,v119\n"
MF "a[184:187],v[20:23],v[96:99],a[184:187]\n"
"v_exp_f32 v120,v120\n"
MF "a[164:167],v[16:19],v[96:99],a[164:167]\n"
"v_exp_f32 v121,v121\n"
MF "a[168:171],v[16:19],v[100:103],a[168:171]\n"
"v_exp_f32 v122,v122\n"
MF "a[188:191],v[20:23],v[100:103],a[188:191]\n"
"v_exp_f32 v123,v123\n"
MF "a[192:195],v[20:23],v[104:107],a[192:195]\n"
"v_add_f32 v54,v54,v120\n"
"v_add_f32 v54,v54,v121\n"
MF "a[172:175],v[16:19],v[104:107],a[172:175]\n"
CV "v82,v120,v121\n"
"v_add_f32 v54,v54,v122\n"
MF "a[176:179],v[16:19],v[108:111],a[176:179]\n"
"s_waitcnt vmcnt(6) lgkmcnt(0)\n"
"s_barrier\n"
MF "a[196:199],v[20:23],v[108:111],a[196:199]\n"
"s_add_u32 s72,s72,6144\n"
"ds_read_b128 v[16:19],v0\n"
"ds_read_b128 v[20:23],v1\n"
MF "a[216:219],v[24:27],v[108:111],a[216:219]\n"
"s_cmp_eq_u32 s72,s17\n"
"s_cselect_b32 s72,s16,s72\n"
"s_add_u32 m0,s64,49152\n"
MF "a[236:239],v[28:31],v[108:111],a[236:239]\n"
LD "v7,s[32:35],s72 offen nt lds\n"
MF "a[232:235],v[28:31],v[104:107],a[232:235]\n"
"s_add_u32 m0,s64,50048\n"
"v_add_f32 v54,v54,v123\n"
CV "v83,v122,v123\n"
MF "a[212:215],v[24:27],v[104:107],a[212:215]\n"
LD "v7,s[32:35],s72 offen offset:128 nt lds\n"
MF "a[208:211],v[24:27],v[100:103],a[208:211]\n"
"s_add_u32 m0,s64,50944\n"
MF "a[228:231],v[28:31],v[100:103],a[228:231]\n"
LD "v7,s[32:35],s72 offen offset:256 nt lds\n"
MF "a[224:227],v[28:31],v[96:99],a[224:227]\n"
MF "a[204:207],v[24:27],v[96:99],a[204:207]\n"
MF "a[200:203],v[24:27],v[92:95],a[200:203]\n"
MF "a[220:223],v[28:31],v[92:95],a[220:223]\n"
"s_waitcnt lgkmcnt(0)\n"
MF "v[92:95],v[16:19],v[136:139],v[32:35]\n"
"s_add_u32 s72,s72,6144\n"
"ds_read_b128 v[24:27],v0 offset:1024\n"
"ds_read_b128 v[28:31],v1 offset:1024\n"
MF "v[92:95],v[20:23],v[156:159],v[92:95]\n"
"s_add_u32 m0,s64,55296\n"
"v_exp_f32 v124,v124\n"
MF "v[96:99],v[20:23],v[160:163],v[36:39]\n"
LD "v7,s[32:35],s72 offen nt lds\n"
MF "v[96:99],v[16:19],v[140:143],v[96:99]\n"
"v_exp_f32 v125,v125\n"
MF "v[100:103],v[16:19],v[144:147],v[40:43]\n"
"s_add_u32 m0,s64,56192\n"
"v_exp_f32 v126,v126\n"
MF "v[100:103],v[20:23],v[164:167],v[100:103]\n"
LD "v7,s[32:35],s72 offen offset:128 nt lds\n"
MF "v[104:107],v[20:23],v[168:171],v[44:47]\n"
"v_exp_f32 v127,v127\n"
MF "v[104:107],v[16:19],v[148:151],v[104:107]\n"
"s_add_u32 m0,s64,57088\n"
"v_add_f32 v55,v55,v124\n"
"v_add_f32 v55,v55,v125\n"
MF "v[108:111],v[16:19],v[152:155],v[48:51]\n"
LD "v7,s[32:35],s72 offen offset:256 nt lds\n"
MF "v[108:111],v[20:23],v[172:175],v[108:111]\n"
"s_waitcnt lgkmcnt(0)\n"
"ds_read_b128 v[16:19],v0 offset:2048\n"
"ds_read_b128 v[20:23],v1 offset:2048\n"
MF "v[108:111],v[24:27],v[192:195],v[108:111]\n"
CV "v86,v124,v125\n"
"v_add_f32 v55,v55,v126\n"
MF "v[108:111],v[28:31],v[212:215],v[108:111]\n"
"v_add_f32 v55,v55,v127\n"
CV "v87,v126,v127\n"
MF "v[104:107],v[28:31],v[208:211],v[104:107]\n"
"v_exp_f32 v128,v128\n"
MF "v[104:107],v[24:27],v[188:191],v[104:107]\n"
"v_exp_f32 v129,v129\n"
MF "v[100:103],v[24:27],v[184:187],v[100:103]\n"
"v_exp_f32 v130,v130\n"
MF "v[100:103],v[28:31],v[204:207],v[100:103]\n"
"v_exp_f32 v131,v131\n"
MF "v[96:99],v[28:31],v[200:203],v[96:99]\n"
"v_add_f32 v56,v56,v128\n"
"v_add_f32 v56,v56,v129\n"
MF "v[96:99],v[24:27],v[180:183],v[96:99]\n"
CV "v90,v128,v129\n"
"v_add_f32 v56,v56,v130\n"
MF "v[92:95],v[24:27],v[176:179],v[92:95]\n"
"v_add_f32 v56,v56,v131\n"
CV "v91,v130,v131\n"
MF "v[92:95],v[28:31],v[196:199],v[92:95]\n"
"s_waitcnt lgkmcnt(0)\n"
"ds_read_b128 v[24:27],v0 offset:6144\n"
"ds_read_b128 v[28:31],v1 offset:6144\n"
MF "v[92:95],v[16:19],v[216:219],v[92:95]\n"
MF "v[92:95],v[20:23],v[236:239],v[92:95]\n"
MF "v[96:99],v[20:23],v[240:243],v[96:99]\n"
MF "v[96:99],v[16:19],v[220:223],v[96:99]\n"
MF "v[100:103],v[16:19],v[224:227],v[100:103]\n"
MF "v[100:103],v[20:23],v[244:247],v[100:103]\n"
MF "v[104:107],v[20:23],v[248:251],v[104:107]\n"
MF "v[104:107],v[16:19],v[228:231],v[104:107]\n"
"v_max3_f32 v10,v52,v53,v54\n"
"v_max3_f32 v10,v10,v55,v56\n"
MF "v[108:111],v[16:19],v[232:235],v[108:111]\n"
"v_cmp_ngt_f32 vcc,s77,v10\n"
MF "v[108:111],v[20:23],v[252:255],v[108:111]\n"
"s_waitcnt lgkmcnt(0)\n"
"s_cbranch_vccnz .Lstub_12\n"
".Lback_12:\n"
MF "v[112:115],v[24:27],v[136:139],v[32:35]\n"
"s_add_u32 s74,s74,12288\n"
"ds_read_b128 v[16:19],v0 offset:7168\n"
"ds_read_b128 v[20:23],v1 offset:7168\n"
MF "v[112:115],v[28:31],v[156:159],v[112:115]\n"
"s_cmp_eq_u32 s74,s20\n"
"s_cselect_b32 s74,s70,s74\n"
MF "v[116:119],v[28:31],v[160:163],v[36:39]\n"
"v_exp_f32 v92,v92\n"
MF "v[116:119],v[24:27],v[140:143],v[116:119]\n"
"v_exp_f32 v93,v93\n"
MF "v[120:123],v[24:27],v[144:147],v[40:43]\n"
"v_exp_f32 v94,v94\n"
MF "v[120:123],v[28:31],v[164:167],v[120:123]\n"
"v_exp_f32 v95,v95\n"
MF "v[124:127],v[28:31],v[168:171],v[44:47]\n"
"v_add_f32 v57,v52,v92\n"
"v_add_f32 v57,v57,v93\n"
MF "v[124:127],v[24:27],v[148:151],v[124:127]\n"
CV "v92,v92,v93\n"
"v_add_f32 v57,v57,v94\n"
MF "v[128:131],v[24:27],v[152:155],v[48:51]\n"
"v_add_f32 v57,v57,v95\n"
CV "v93,v94,v95\n"
MF "v[128:131],v[28:31],v[172:175],v[128:131]\n"
"s_waitcnt lgkmcnt(0)\n"
"ds_read_b128 v[24:27],v0 offset:8192\n"
"ds_read_b128 v[28:31],v1 offset:8192\n"
MF "v[128:131],v[16:19],v[192:195],v[128:131]\n"
"v_exp_f32 v96,v96\n"
MF "v[128:131],v[20:23],v[212:215],v[128:131]\n"
"v_exp_f32 v97,v97\n"
MF "v[124:127],v[20:23],v[208:211],v[124:127]\n"
"v_exp_f32 v98,v98\n"
MF "v[124:127],v[16:19],v[188:191],v[124:127]\n"
"v_exp_f32 v99,v99\n"
MF "v[120:123],v[16:19],v[184:187],v[120:123]\n"
"v_add_f32 v58,v53,v96\n"
"v_add_f32 v58,v58,v97\n"
MF "v[120:123],v[20:23],v[204:207],v[120:123]\n"
CV "v96,v96,v97\n"
"v_add_f32 v58,v58,v98\n"
MF "v[116:119],v[20:23],v[200:203],v[116:119]\n"
"v_add_f32 v58,v58,v99\n"
CV "v97,v98,v99\n"
MF "v[116:119],v[16:19],v[180:183],v[116:119]\n"
"s_waitcnt vmcnt(6) lgkmcnt(0)\n"
"s_barrier\n"
MF "v[112:115],v[16:19],v[176:179],v[112:115]\n"
"v_exp_f32 v100,v100\n"
MF "v[112:115],v[20:23],v[196:199],v[112:115]\n"
"s_nop 0\n"
TR "v[16:17],v2 offset:24576\n"
TR "v[18:19],v2 offset:30720\n"
MF "v[112:115],v[24:27],v[216:219],v[112:115]\n"
"s_add_u32 s73,s73,12288\n"
TR "v[20:21],v3 offset:24576\n"
TR "v[22:23],v3 offset:30720\n"
MF "v[112:115],v[28:31],v[236:239],v[112:115]\n"
"s_cmp_eq_u32 s73,s19\n"
"s_cselect_b32 s73,s18,s73\n"
"s_add_u32 m0,s65,0\n"
MF "v[116:119],v[28:31],v[240:243],v[116:119]\n"
LD "v8,s[36:39],s73 offen nt lds\n"
MF "v[116:119],v[24:27],v[220:223],v[116:119]\n"
"s_add_u32 m0,s65,896\n"
"v_exp_f32 v101,v101\n"
MF "v[120:123],v[24:27],v[224:227],v[120:123]\n"
LD "v8,s[36:39],s73 offen offset:128 nt lds\n"
MF "v[120:123],v[28:31],v[244:247],v[120:123]\n"
"v_exp_f32 v102,v102\n"
MF "v[124:127],v[28:31],v[248:251],v[124:127]\n"
"s_add_u32 m0,s65,1792\n"
"v_exp_f32 v103,v103\n"
MF "v[124:127],v[24:27],v[228:231],v[124:127]\n"
LD "v8,s[36:39],s73 offen offset:256 nt lds\n"
MF "v[128:131],v[24:27],v[232:235],v[128:131]\n"
"v_add_f32 v59,v54,v100\n"
"v_add_f32 v59,v59,v101\n"
MF "v[128:131],v[28:31],v[252:255],v[128:131]\n"
"s_waitcnt lgkmcnt(0)\n"
TR "v[24:25],v4 offset:24576\n"
TR "v[26:27],v4 offset:30720\n"
MF "a[0:3],v[16:19],v[72:75],a[0:3]\n"
"s_add_u32 m0,s65,0\n"
TR "v[28:29],v5 offset:24576\n"
TR "v[30:31],v5 offset:30720\n"
MF "a[20:23],v[20:23],v[72:75],a[20:23]\n"
LD "v8,s[36:39],s73 offen offset:3072 nt lds\n"
MF "a[24:27],v[20:23],v[76:79],a[24:27]\n"
CV "v100,v100,v101\n"
"v_add_f32 v59,v59,v102\n"
MF "a[4:7],v[16:19],v[76:79],a[4:7]\n"
"s_add_u32 m0,s65,896\n"
"v_add_f32 v59,v59,v103\n"
CV "v101,v102,v103\n"
MF "a[8:11],v[16:19],v[80:83],a[8:11]\n"
LD "v8,s[36:39],s73 offen offset:3200 nt lds\n"
MF "a[28:31],v[20:23],v[80:83],a[28:31]\n"
"v_exp_f32 v104,v104\n"
MF "a[32:35],v[20:23],v[84:87],a[32:35]\n"
"s_add_u32 m0,s65,1792\n"
"v_exp_f32 v105,v105\n"
MF "a[12:15],v[16:19],v[84:87],a[12:15]\n"
LD "v8,s[36:39],s73 offen offset:3328 nt lds\n"
MF "a[16:19],v[16:19],v[88:91],a[16:19]\n"
"v_exp_f32 v106,v106\n"
MF "a[36:39],v[20:23],v[88:91],a[36:39]\n"
"s_waitcnt lgkmcnt(0)\n"
TR "v[16:17],v2 offset:25600\n"
TR "v[18:19],v2 offset:31744\n"
MF "a[56:59],v[24:27],v[88:91],a[56:59]\n"
"s_nop 0\n"
TR "v[20:21],v3 offset:25600\n"
TR "v[22:23],v3 offset:31744\n"
MF "a[76:79],v[28:31],v[88:91],a[76:79]\n"
"v_exp_f32 v107,v107\n"
MF "a[72:75],v[28:31],v[84:87],a[72:75]\n"
"v_add_f32 v60,v55,v104\n"
"v_add_f32 v60,v60,v105\n"
MF "a[52:55],v[24:27],v[84:87],a[52:55]\n"
CV "v104,v104,v105\n"
"v_add_f32 v60,v60,v106\n"
MF "a[48:51],v[24:27],v[80:83],a[48:51]\n"
"v_add_f32 v60,v60,v107\n"
CV "v105,v106,v107\n"
MF "a[68:71],v[28:31],v[80:83],a[68:71]\n"
"v_exp_f32 v108,v108\n"
MF "a[64:67],v[28:31],v[76:79],a[64:67]\n"
"v_exp_f32 v109,v109\n"
MF "a[44:47],v[24:27],v[76:79],a[44:47]\n"
"v_exp_f32 v110,v110\n"
MF "a[40:43],v[24:27],v[72:75],a[40:43]\n"
"v_exp_f32 v111,v111\n"
MF "a[60:63],v[28:31],v[72:75],a[60:63]\n"
"s_waitcnt lgkmcnt(0)\n"
TR "v[24:25],v4 offset:25600\n"
TR "v[26:27],v4 offset:31744\n"
MF "a[80:83],v[16:19],v[72:75],a[80:83]\n"
"s_nop 0\n"
TR "v[28:29],v5 offset:25600\n"
TR "v[30:31],v5 offset:31744\n"
MF "a[100:103],v[20:23],v[72:75],a[100:103]\n"
"v_add_f32 v61,v56,v108\n"
"v_add_f32 v61,v61,v109\n"
MF "a[104:107],v[20:23],v[76:79],a[104:107]\n"
CV "v108,v108,v109\n"
"v_add_f32 v61,v61,v110\n"
MF "a[84:87],v[16:19],v[76:79],a[84:87]\n"
"v_add_f32 v61,v61,v111\n"
CV "v109,v110,v111\n"
MF "a[88:91],v[16:19],v[80:83],a[88:91]\n"
"v_exp_f32 v112,v112\n"
MF "a[108:111],v[20:23],v[80:83],a[108:111]\n"
"v_exp_f32 v113,v113\n"
MF "a[112:115],v[20:23],v[84:87],a[112:115]\n"
"v_exp_f32 v114,v114\n"
MF "a[92:95],v[16:19],v[84:87],a[92:95]\n"
"v_exp_f32 v115,v115\n"
MF "a[96:99],v[16:19],v[88:91],a[96:99]\n"
"v_add_f32 v57,v57,v112\n"
"v_add_f32 v57,v57,v113\n"
MF "a[116:119],v[20:23],v[88:91],a[116:119]\n"
"s_waitcnt lgkmcnt(0)\n"
TR "v[16:17],v2 offset:26624\n"
TR "v[18:19],v2 offset:32768\n"
MF "a[136:139],v[24:27],v[88:91],a[136:139]\n"
"s_nop 0\n"
TR "v[20:21],v3 offset:26624\n"
TR "v[22:23],v3 offset:32768\n"
MF "a[156:159],v[28:31],v[88:91],a[156:159]\n"
CV "v94,v112,v113\n"
"v_add_f32 v57,v57,v114\n"
MF "a[152:155],v[28:31],v[84:87],a[152:155]\n"
"v_add_f32 v57,v57,v115\n"
CV "v95,v114,v115\n"
MF "a[132:135],v[24:27],v[84:87],a[132:135]\n"
"v_exp_f32 v116,v116\n"
MF "a[128:131],v[24:27],v[80:83],a[128:131]\n"
"v_exp_f32 v117,v117\n"
MF "a[148:151],v[28:31],v[80:83],a[148:151]\n"
"v_exp_f32 v118,v118\n"
MF "a[144:147],v[28:31],v[76:79],a[144:147]\n"
"v_exp_f32 v119,v119\n"
MF "a[124:127],v[24:27],v[76:79],a[124:127]\n"
"v_add_f32 v58,v58,v116\n"
"v_add_f32 v58,v58,v117\n"
MF "a[120:123],v[24:27],v[72:75],a[120:123]\n"
CV "v98,v116,v117\n"
"v_add_f32 v58,v58,v118\n"
MF "a[140:143],v[28:31],v[72:75],a[140:143]\n"
"s_waitcnt lgkmcnt(0)\n"
TR "v[24:25],v4 offset:26624\n"
TR "v[26:27],v4 offset:32768\n"
MF "a[160:163],v[16:19],v[72:75],a[160:163]\n"
"s_nop 0\n"
TR "v[28:29],v5 offset:26624\n"
TR "v[30:31],v5 offset:32768\n"
MF "a[180:183],v[20:23],v[72:75],a[180:183]\n"
"v_add_f32 v58,v58,v119\n"
CV "v99,v118,v119\n"
MF "a[184:187],v[20:23],v[76:79],a[184:187]\n"
"v_exp_f32 v120,v120\n"
MF "a[164:167],v[16:19],v[76:79],a[164:167]\n"
"v_exp_f32 v121,v121\n"
MF "a[168:171],v[16:19],v[80:83],a[168:171]\n"
"v_exp_f32 v122,v122\n"
MF "a[188:191],v[20:23],v[80:83],a[188:191]\n"
"v_exp_f32 v123,v123\n"
MF "a[192:195],v[20:23],v[84:87],a[192:195]\n"
"v_add_f32 v59,v59,v120\n"
"v_add_f32 v59,v59,v121\n"
MF "a[172:175],v[16:19],v[84:87],a[172:175]\n"
CV "v102,v120,v121\n"
"v_add_f32 v59,v59,v122\n"
MF "a[176:179],v[16:19],v[88:91],a[176:179]\n"
"s_waitcnt vmcnt(6) lgkmcnt(0)\n"
"s_barrier\n"
MF "a[196:199],v[20:23],v[88:91],a[196:199]\n"
"s_add_u32 s72,s72,6144\n"
"ds_read_b128 v[16:19],v0 offset:49152\n"
"ds_read_b128 v[20:23],v1 offset:49152\n"
MF "a[216:219],v[24:27],v[88:91],a[216:219]\n"
"s_cmp_eq_u32 s72,s17\n"
"s_cselect_b32 s72,s16,s72\n"
"s_add_u32 m0,s64,24576\n"
MF "a[236:239],v[28:31],v[88:91],a[236:239]\n"
LD "v7,s[32:35],s72 offen nt lds\n"
MF "a[232:235],v[28:31],v[84:87],a[232:235]\n"
"s_add_u32 m0,s64,25472\n"
"v_add_f32 v59,v59,v123\n"
CV "v103,v122,v123\n"
MF "a[212:215],v[24:27],v[84:87],a[212:215]\n"
LD "v7,s[32:35],s72 offen offset:128 nt lds\n"
MF "a[208:211],v[24:27],v[80:83],a[208:211]\n"
"s_add_u32 m0,s64,26368\n"
MF "a[228:231],v[28:31],v[80:83],a[228:231]\n"
LD "v7,s[32:35],s72 offen offset:256 nt lds\n"
MF "a[224:227],v[28:31],v[76:79],a[224:227]\n"
MF "a[204:207],v[24:27],v[76:79],a[204:207]\n"
MF "a[200:203],v[24:27],v[72:75],a[200:203]\n"
MF "a[220:223],v[28:31],v[72:75],a[220:223]\n"
"s_waitcnt lgkmcnt(0)\n"
MF "v[72:75],v[16:19],v[136:139],v[32:35]\n"
"s_add_u32 s72,s72,6144\n"
"ds_read_b128 v[24:27],v0 offset:50176\n"
"ds_read_b128 v[28:31],v1 offset:50176\n"
MF "v[72:75],v[20:23],v[156:159],v[72:75]\n"
"s_add_u32 m0,s64,30720\n"
"v_exp_f32 v124,v124\n"
MF "v[76:79],v[20:23],v[160:163],v[36:39]\n"
LD "v7,s[32:35],s72 offen nt lds\n"
MF "v[76:79],v[16:19],v[140:143],v[76:79]\n"
"v_exp_f32 v125,v125\n"
MF "v[80:83],v[16:19],v[144:147],v[40:43]\n"
"s_add_u32 m0,s64,31616\n"
"v_exp_f32 v126,v126\n"
MF "v[80:83],v[20:23],v[164:167],v[80:83]\n"
LD "v7,s[32:35],s72 offen offset:128 nt lds\n"
MF "v[84:87],v[20:23],v[168:171],v[44:47]\n"
"v_exp_f32 v127,v127\n"
MF "v[84:87],v[16:19],v[148:151],v[84:87]\n"
"s_add_u32 m0,s64,32512\n"
"v_add_f32 v60,v60,v124\n"
"v_add_f32 v60,v60,v125\n"
MF "v[88:91],v[16:19],v[152:155],v[48:51]\n"
LD "v7,s[32:35],s72 offen offset:256 nt lds\n"
MF "v[88:91],v[20:23],v[172:175],v[88:91]\n"
"s_waitcnt lgkmcnt(0)\n"
"ds_read_b128 v[16:19],v0 offset:51200\n"
"ds_read_b128 v[20:23],v1 offset:51200\n"
MF "v[88:91],v[24:27],v[192:195],v[88:91]\n"
CV "v106,v124,v125\n"
"v_add_f32 v60,v60,v126\n"
MF "v[88:91],v[28:31],v[212:215],v[88:91]\n"
"v_add_f32 v60,v60,v127\n"
CV "v107,v126,v127\n"
MF "v[84:87],v[28:31],v[208:211],v[84:87]\n"
"v_exp_f32 v128,v128\n"
MF "v[84:87],v[24:27],v[188:191],v[84:87]\n"
"v_exp_f32 v129,v129\n"
MF "v[80:83],v[24:27],v[184:187],v[80:83]\n"
"v_exp_f32 v130,v130\n"
MF "v[80:83],v[28:31],v[204:207],v[80:83]\n"
"v_exp_f32 v131,v131\n"
MF "v[76:79],v[28:31],v[200:203],v[76:79]\n"
"v_add_f32 v61,v61,v128\n"
"v_add_f32 v61,v61,v129\n"
MF "v[76:79],v[24:27],v[180:183],v[76:79]\n"
CV "v110,v128,v129\n"
"v_add_f32 v61,v61,v130\n"
MF "v[72:75],v[24:27],v[176:179],v[72:75]\n"
"v_add_f32 v61,v61,v131\n"
CV "v111,v130,v131\n"
MF "v[72:75],v[28:31],v[196:199],v[72:75]\n"
"s_waitcnt lgkmcnt(0)\n"
"ds_read_b128 v[24:27],v0 offset:55296\n"
"ds_read_b128 v[28:31],v1 offset:55296\n"
MF "v[72:75],v[16:19],v[216:219],v[72:75]\n"
MF "v[72:75],v[20:23],v[236:239],v[72:75]\n"
MF "v[76:79],v[20:23],v[240:243],v[76:79]\n"
MF "v[76:79],v[16:19],v[220:223],v[76:79]\n"
MF "v[80:83],v[16:19],v[224:227],v[80:83]\n"
MF "v[80:83],v[20:23],v[244:247],v[80:83]\n"
MF "v[84:87],v[20:23],v[248:251],v[84:87]\n"
MF "v[84:87],v[16:19],v[228:231],v[84:87]\n"
"v_max3_f32 v10,v57,v58,v59\n"
"v_max3_f32 v10,v10,v60,v61\n"
MF "v[88:91],v[16:19],v[232:235],v[88:91]\n"
"v_cmp_ngt_f32 vcc,s95,v10\n"
MF "v[88:91],v[20:23],v[252:255],v[88:91]\n"
"s_waitcnt lgkmcnt(0)\n"
"s_cbranch_vccnz .Lstub_13\n"
".Lback_13:\n"
MF "v[112:115],v[24:27],v[136:139],v[32:35]\n"
"s_add_u32 s74,s74,12288\n"
"ds_read_b128 v[16:19],v0 offset:56320\n"
"ds_read_b128 v[20:23],v1 offset:56320\n"
MF "v[112:115],v[28:31],v[156:159],v[112:115]\n"
"s_cmp_eq_u32 s74,s20\n"
"s_cselect_b32 s74,s70,s74\n"
MF "v[116:119],v[28:31],v[160:163],v[36:39]\n"
"v_exp_f32 v72,v72\n"
MF "v[116:119],v[24:27],v[140:143],v[116:119]\n"
"v_exp_f32 v73,v73\n"
MF "v[120:123],v[24:27],v[144:147],v[40:43]\n"
"v_exp_f32 v74,v74\n"
MF "v[120:123],v[28:31],v[164:167],v[120:123]\n"
"v_exp_f32 v75,v75\n"
MF "v[124:127],v[28:31],v[168:171],v[44:47]\n"
"v_add_f32 v52,v57,v72\n"
"v_add_f32 v52,v52,v73\n"
MF "v[124:127],v[24:27],v[148:151],v[124:127]\n"
CV "v72,v72,v73\n"
"v_add_f32 v52,v52,v74\n"
MF "v[128:131],v[24:27],v[152:155],v[48:51]\n"
"v_add_f32 v52,v52,v75\n"
CV "v73,v74,v75\n"
MF "v[128:131],v[28:31],v[172:175],v[128:131]\n"
"s_waitcnt lgkmcnt(0)\n"
"ds_read_b128 v[24:27],v0 offset:57344\n"
"ds_read_b128 v[28:31],v1 offset:57344\n"
MF "v[128:131],v[16:19],v[192:195],v[128:131]\n"
"v_exp_f32 v76,v76\n"
MF "v[128:131],v[20:23],v[212:215],v[128:131]\n"
"v_exp_f32 v77,v77\n"
MF "v[124:127],v[20:23],v[208:211],v[124:127]\n"
"v_exp_f32 v78,v78\n"
MF "v[124:127],v[16:19],v[188:191],v[124:127]\n"
"v_exp_f32 v79,v79\n"
MF "v[120:123],v[16:19],v[184:187],v[120:123]\n"
"v_add_f32 v53,v58,v76\n"
"v_add_f32 v53,v53,v77\n"
MF "v[120:123],v[20:23],v[204:207],v[120:123]\n"
CV "v76,v76,v77\n"
"v_add_f32 v53,v53,v78\n"
MF "v[116:119],v[20:23],v[200:203],v[116:119]\n"
"v_add_f32 v53,v53,v79\n"
CV "v77,v78,v79\n"
MF "v[116:119],v[16:19],v[180:183],v[116:119]\n"
"s_waitcnt vmcnt(6) lgkmcnt(0)\n"
"s_barrier\n"
MF "v[112:115],v[16:19],v[176:179],v[112:115]\n"
"v_exp_f32 v80,v80\n"
MF "v[112:115],v[20:23],v[196:199],v[112:115]\n"
"s_nop 0\n"
TR "v[16:17],v2\n"
TR "v[18:19],v2 offset:6144\n"
MF "v[112:115],v[24:27],v[216:219],v[112:115]\n"
"s_add_u32 s73,s73,12288\n"
TR "v[20:21],v3\n"
TR "v[22:23],v3 offset:6144\n"
MF "v[112:115],v[28:31],v[236:239],v[112:115]\n"
"s_cmp_eq_u32 s73,s19\n"
"s_cselect_b32 s73,s18,s73\n"
"s_add_u32 m0,s65,49152\n"
MF "v[116:119],v[28:31],v[240:243],v[116:119]\n"
LD "v8,s[36:39],s73 offen nt lds\n"
MF "v[116:119],v[24:27],v[220:223],v[116:119]\n"
"s_add_u32 m0,s65,50048\n"
"v_exp_f32 v81,v81\n"
MF "v[120:123],v[24:27],v[224:227],v[120:123]\n"
LD "v8,s[36:39],s73 offen offset:128 nt lds\n"
MF "v[120:123],v[28:31],v[244:247],v[120:123]\n"
"v_exp_f32 v82,v82\n"
MF "v[124:127],v[28:31],v[248:251],v[124:127]\n"
"s_add_u32 m0,s65,50944\n"
"v_exp_f32 v83,v83\n"
MF "v[124:127],v[24:27],v[228:231],v[124:127]\n"
LD "v8,s[36:39],s73 offen offset:256 nt lds\n"
MF "v[128:131],v[24:27],v[232:235],v[128:131]\n"
"v_add_f32 v54,v59,v80\n"
"v_add_f32 v54,v54,v81\n"
MF "v[128:131],v[28:31],v[252:255],v[128:131]\n"
"s_waitcnt lgkmcnt(0)\n"
TR "v[24:25],v4\n"
TR "v[26:27],v4 offset:6144\n"
MF "a[0:3],v[16:19],v[92:95],a[0:3]\n"
"s_add_u32 m0,s65,49152\n"
TR "v[28:29],v5\n"
TR "v[30:31],v5 offset:6144\n"
MF "a[20:23],v[20:23],v[92:95],a[20:23]\n"
LD "v8,s[36:39],s73 offen offset:3072 nt lds\n"
MF "a[24:27],v[20:23],v[96:99],a[24:27]\n"
CV "v80,v80,v81\n"
"v_add_f32 v54,v54,v82\n"
MF "a[4:7],v[16:19],v[96:99],a[4:7]\n"
"s_add_u32 m0,s65,50048\n"
"v_add_f32 v54,v54,v83\n"
CV "v81,v82,v83\n"
MF "a[8:11],v[16:19],v[100:103],a[8:11]\n"
LD "v8,s[36:39],s73 offen offset:3200 nt lds\n"
MF "a[28:31],v[20:23],v[100:103],a[28:31]\n"
"v_exp_f32 v84,v84\n"
MF "a[32:35],v[20:23],v[104:107],a[32:35]\n"
"s_add_u32 m0,s65,50944\n"
"v_exp_f32 v85,v85\n"
MF "a[12:15],v[16:19],v[104:107],a[12:15]\n"
LD "v8,s[36:39],s73 offen offset:3328 nt lds\n"
MF "a[16:19],v[16:19],v[108:111],a[16:19]\n"
"v_exp_f32 v86,v86\n"
MF "a[36:39],v[20:23],v[108:111],a[36:39]\n"
"s_waitcnt lgkmcnt(0)\n"
TR "v[16:17],v2 offset:1024\n"
TR "v[18:19],v2 offset:7168\n"
MF "a[56:59],v[24:27],v[108:111],a[56:59]\n"
"s_nop 0\n"
TR "v[20:21],v3 offset:1024\n"
TR "v[22:23],v3 offset:7168\n"
MF "a[76:79],v[28:31],v[108:111],a[76:79]\n"
"v_exp_f32 v87,v87\n"
MF "a[72:75],v[28:31],v[104:107],a[72:75]\n"
"v_add_f32 v55,v60,v84\n"
"v_add_f32 v55,v55,v85\n"
MF "a[52:55],v[24:27],v[104:107],a[52:55]\n"
CV "v84,v84,v85\n"
"v_add_f32 v55,v55,v86\n"
MF "a[48:51],v[24:27],v[100:103],a[48:51]\n"
"v_add_f32 v55,v55,v87\n"
CV "v85,v86,v87\n"
MF "a[68:71],v[28:31],v[100:103],a[68:71]\n"
"v_exp_f32 v88,v88\n"
MF "a[64:67],v[28:31],v[96:99],a[64:67]\n"
"v_exp_f32 v89,v89\n"
MF "a[44:47],v[24:27],v[96:99],a[44:47]\n"
"v_exp_f32 v90,v90\n"
MF "a[40:43],v[24:27],v[92:95],a[40:43]\n"
"v_exp_f32 v91,v91\n"
MF "a[60:63],v[28:31],v[92:95],a[60:63]\n"
"s_waitcnt lgkmcnt(0)\n"
TR "v[24:25],v4 offset:1024\n"
TR "v[26:27],v4 offset:7168\n"
MF "a[80:83],v[16:19],v[92:95],a[80:83]\n"
"s_nop 0\n"
TR "v[28:29],v5 offset:1024\n"
TR "v[30:31],v5 offset:7168\n"
MF "a[100:103],v[20:23],v[92:95],a[100:103]\n"
"v_add_f32 v56,v61,v88\n"
"v_add_f32 v56,v56,v89\n"
MF "a[104:107],v[20:23],v[96:99],a[104:107]\n"
CV "v88,v88,v89\n"
"v_add_f32 v56,v56,v90\n"
MF "a[84:87],v[16:19],v[96:99],a[84:87]\n"
"v_add_f32 v56,v56,v91\n"
CV "v89,v90,v91\n"
MF "a[88:91],v[16:19],v[100:103],a[88:91]\n"
"v_exp_f32 v112,v112\n"
MF "a[108:111],v[20:23],v[100:103],a[108:111]\n"
"v_exp_f32 v113,v113\n"
MF "a[112:115],v[20:23],v[104:107],a[112:115]\n"
"v_exp_f32 v114,v114\n"
MF "a[92:95],v[16:19],v[104:107],a[92:95]\n"
"v_exp_f32 v115,v115\n"
MF "a[96:99],v[16:19],v[108:111],a[96:99]\n"
"v_add_f32 v52,v52,v112\n"
"v_add_f32 v52,v52,v113\n"
MF "a[116:119],v[20:23],v[108:111],a[116:119]\n"
"s_waitcnt lgkmcnt(0)\n"
TR "v[16:17],v2 offset:2048\n"
TR "v[18:19],v2 offset:8192\n"
MF "a[136:139],v[24:27],v[108:111],a[136:139]\n"
"s_nop 0\n"
TR "v[20:21],v3 offset:2048\n"
TR "v[22:23],v3 offset:8192\n"
MF "a[156:159],v[28:31],v[108:111],a[156:159]\n"
CV "v74,v112,v113\n"
"v_add_f32 v52,v52,v114\n"
MF "a[152:155],v[28:31],v[104:107],a[152:155]\n"
"v_add_f32 v52,v52,v115\n"
CV "v75,v114,v115\n"
MF "a[132:135],v[24:27],v[104:107],a[132:135]\n"
"v_exp_f32 v116,v116\n"
MF "a[128:131],v[24:27],v[100:103],a[128:131]\n"
"v_exp_f32 v117,v117\n"
MF "a[148:151],v[28:31],v[100:103],a[148:151]\n"
"v_exp_f32 v118,v118\n"
MF "a[144:147],v[28:31],v[96:99],a[144:147]\n"
"v_exp_f32 v119,v119\n"
MF "a[124:127],v[24:27],v[96:99],a[124:127]\n"
"v_add_f32 v53,v53,v116\n"
"v_add_f32 v53,v53,v117\n"
MF "a[120:123],v[24:27],v[92:95],a[120:123]\n"
CV "v78,v116,v117\n"
"v_add_f32 v53,v53,v118\n"
MF "a[140:143],v[28:31],v[92:95],a[140:143]\n"
"s_waitcnt lgkmcnt(0)\n"
TR "v[24:25],v4 offset:2048\n"
TR "v[26:27],v4 offset:8192\n"
MF "a[160:163],v[16:19],v[92:95],a[160:163]\n"
"s_nop 0\n"
TR "v[28:29],v5 offset:2048\n"
TR "v[30:31],v5 offset:8192\n"
MF "a[180:183],v[20:23],v[92:95],a[180:183]\n"
"v_add_f32 v53,v53,v119\n"
CV "v79,v118,v119\n"
MF "a[184:187],v[20:23],v[96:99],a[184:187]\n"
"v_exp_f32 v120,v120\n"
MF "a[164:167],v[16:19],v[96:99],a[164:167]\n"
"v_exp_f32 v121,v121\n"
MF "a[168:171],v[16:19],v[100:103],a[168:171]\n"
"v_exp_f32 v122,v122\n"
MF "a[188:191],v[20:23],v[100:103],a[188:191]\n"
"v_exp_f32 v123,v123\n"
MF "a[192:195],v[20:23],v[104:107],a[192:195]\n"
"v_add_f32 v54,v54,v120\n"
"v_add_f32 v54,v54,v121\n"
MF "a[172:175],v[16:19],v[104:107],a[172:175]\n"
CV "v82,v120,v121\n"
"v_add_f32 v54,v54,v122\n"
MF "a[176:179],v[16:19],v[108:111],a[176:179]\n"
"s_waitcnt vmcnt(6) lgkmcnt(0)\n"
"s_barrier\n"
MF "a[196:199],v[20:23],v[108:111],a[196:199]\n"
"s_add_u32 s72,s72,6144\n"
"ds_read_b128 v[16:19],v0 offset:24576\n"
"ds_read_b128 v[20:23],v1 offset:24576\n"
MF "a[216:219],v[24:27],v[108:111],a[216:219]\n"
"s_cmp_eq_u32 s72,s17\n"
"s_cselect_b32 s72,s16,s72\n"
"s_add_u32 m0,s64,0\n"
MF "a[236:239],v[28:31],v[108:111],a[236:239]\n"
LD "v7,s[32:35],s72 offen nt lds\n"
MF "a[232:235],v[28:31],v[104:107],a[232:235]\n"
"s_add_u32 m0,s64,896\n"
"v_add_f32 v54,v54,v123\n"
CV "v83,v122,v123\n"
MF "a[212:215],v[24:27],v[104:107],a[212:215]\n"
LD "v7,s[32:35],s72 offen offset:128 nt lds\n"
MF "a[208:211],v[24:27],v[100:103],a[208:211]\n"
"s_add_u32 m0,s64,1792\n"
MF "a[228:231],v[28:31],v[100:103],a[228:231]\n"
LD "v7,s[32:35],s72 offen offset:256 nt lds\n"
MF "a[224:227],v[28:31],v[96:99],a[224:227]\n"
MF "a[204:207],v[24:27],v[96:99],a[204:207]\n"
MF "a[200:203],v[24:27],v[92:95],a[200:203]\n"
MF "a[220:223],v[28:31],v[92:95],a[220:223]\n"
"s_waitcnt lgkmcnt(0)\n"
MF "v[92:95],v[16:19],v[136:139],v[32:35]\n"
"s_add_u32 s72,s72,6144\n"
"ds_read_b128 v[24:27],v0 offset:25600\n"
"ds_read_b128 v[28:31],v1 offset:25600\n"
MF "v[92:95],v[20:23],v[156:159],v[92:95]\n"
"s_add_u32 m0,s64,6144\n"
"v_exp_f32 v124,v124\n"
MF "v[96:99],v[20:23],v[160:163],v[36:39]\n"
LD "v7,s[32:35],s72 offen nt lds\n"
MF "v[96:99],v[16:19],v[140:143],v[96:99]\n"
"v_exp_f32 v125,v125\n"
MF "v[100:103],v[16:19],v[144:147],v[40:43]\n"
"s_add_u32 m0,s64,7040\n"
"v_exp_f32 v126,v126\n"
MF "v[100:103],v[20:23],v[164:167],v[100:103]\n"
LD "v7,s[32:35],s72 offen offset:128 nt lds\n"
MF "v[104:107],v[20:23],v[168:171],v[44:47]\n"
"v_exp_f32 v127,v127\n"
MF "v[104:107],v[16:19],v[148:151],v[104:107]\n"
"s_add_u32 m0,s64,7936\n"
"v_add_f32 v55,v55,v124\n"
"v_add_f32 v55,v55,v125\n"
MF "v[108:111],v[16:19],v[152:155],v[48:51]\n"
LD "v7,s[32:35],s72 offen offset:256 nt lds\n"
MF "v[108:111],v[20:23],v[172:175],v[108:111]\n"
"s_waitcnt lgkmcnt(0)\n"
"ds_read_b128 v[16:19],v0 offset:26624\n"
"ds_read_b128 v[20:23],v1 offset:26624\n"
MF "v[108:111],v[24:27],v[192:195],v[108:111]\n"
CV "v86,v124,v125\n"
"v_add_f32 v55,v55,v126\n"
MF "v[108:111],v[28:31],v[212:215],v[108:111]\n"
"v_add_f32 v55,v55,v127\n"
CV "v87,v126,v127\n"
MF "v[104:107],v[28:31],v[208:211],v[104:107]\n"
"v_exp_f32 v128,v128\n"
MF "v[104:107],v[24:27],v[188:191],v[104:107]\n"
"v_exp_f32 v129,v129\n"
MF "v[100:103],v[24:27],v[184:187],v[100:103]\n"
"v_exp_f32 v130,v130\n"
MF "v[100:103],v[28:31],v[204:207],v[100:103]\n"
"v_exp_f32 v131,v131\n"
MF "v[96:99],v[28:31],v[200:203],v[96:99]\n"
"v_add_f32 v56,v56,v128\n"
"v_add_f32 v56,v56,v129\n"
MF "v[96:99],v[24:27],v[180:183],v[96:99]\n"
CV "v90,v128,v129\n"
"v_add_f32 v56,v56,v130\n"
MF "v[92:95],v[24:27],v[176:179],v[92:95]\n"
"v_add_f32 v56,v56,v131\n"
CV "v91,v130,v131\n"
MF "v[92:95],v[28:31],v[196:199],v[92:95]\n"
"s_waitcnt lgkmcnt(0)\n"
"ds_read_b128 v[24:27],v0 offset:30720\n"
"ds_read_b128 v[28:31],v1 offset:30720\n"
MF "v[92:95],v[16:19],v[216:219],v[92:95]\n"
MF "v[92:95],v[20:23],v[236:239],v[92:95]\n"
MF "v[96:99],v[20:23],v[240:243],v[96:99]\n"
MF "v[96:99],v[16:19],v[220:223],v[96:99]\n"
MF "v[100:103],v[16:19],v[224:227],v[100:103]\n"
MF "v[100:103],v[20:23],v[244:247],v[100:103]\n"
MF "v[104:107],v[20:23],v[248:251],v[104:107]\n"
MF "v[104:107],v[16:19],v[228:231],v[104:107]\n"
"v_max3_f32 v10,v52,v53,v54\n"
"v_max3_f32 v10,v10,v55,v56\n"
MF "v[108:111],v[16:19],v[232:235],v[108:111]\n"
"v_cmp_ngt_f32 vcc,s77,v10\n"
MF "v[108:111],v[20:23],v[252:255],v[108:111]\n"
"s_waitcnt lgkmcnt(0)\n"
"s_cbranch_vccnz .Lstub_14\n"
".Lback_14:\n"
MF "v[112:115],v[24:27],v[136:139],v[32:35]\n"
"s_add_u32 s74,s74,12288\n"
"ds_read_b128 v[16:19],v0 offset:31744\n"
"ds_read_b128 v[20:23],v1 offset:31744\n"
MF "v[112:115],v[28:31],v[156:159],v[112:115]\n"
"s_cmp_eq_u32 s74,s20\n"
"s_cselect_b32 s74,s70,s74\n"
MF "v[116:119],v[28:31],v[160:163],v[36:39]\n"
"v_exp_f32 v92,v92\n"
MF "v[116:119],v[24:27],v[140:143],v[116:119]\n"
"v_exp_f32 v93,v93\n"
MF "v[120:123],v[24:27],v[144:147],v[40:43]\n"
"v_exp_f32 v94,v94\n"
MF "v[120:123],v[28:31],v[164:167],v[120:123]\n"
"v_exp_f32 v95,v95\n"
MF "v[124:127],v[28:31],v[168:171],v[44:47]\n"
"v_add_f32 v57,v52,v92\n"
"v_add_f32 v57,v57,v93\n"
MF "v[124:127],v[24:27],v[148:151],v[124:127]\n"
CV "v92,v92,v93\n"
"v_add_f32 v57,v57,v94\n"
MF "v[128:131],v[24:27],v[152:155],v[48:51]\n"
"v_add_f32 v57,v57,v95\n"
CV "v93,v94,v95\n"
MF "v[128:131],v[28:31],v[172:175],v[128:131]\n"
"s_waitcnt lgkmcnt(0)\n"
"ds_read_b128 v[24:27],v0 offset:32768\n"
"ds_read_b128 v[28:31],v1 offset:32768\n"
MF "v[128:131],v[16:19],v[192:195],v[128:131]\n"
"v_exp_f32 v96,v96\n"
MF "v[128:131],v[20:23],v[212:215],v[128:131]\n"
"v_exp_f32 v97,v97\n"
MF "v[124:127],v[20:23],v[208:211],v[124:127]\n"
"v_exp_f32 v98,v98\n"
MF "v[124:127],v[16:19],v[188:191],v[124:127]\n"
"v_exp_f32 v99,v99\n"
MF "v[120:123],v[16:19],v[184:187],v[120:123]\n"
"v_add_f32 v58,v53,v96\n"
"v_add_f32 v58,v58,v97\n"
MF "v[120:123],v[20:23],v[204:207],v[120:123]\n"
CV "v96,v96,v97\n"
"v_add_f32 v58,v58,v98\n"
MF "v[116:119],v[20:23],v[200:203],v[116:119]\n"
"v_add_f32 v58,v58,v99\n"
CV "v97,v98,v99\n"
MF "v[116:119],v[16:19],v[180:183],v[116:119]\n"
"s_waitcnt vmcnt(6) lgkmcnt(0)\n"
"s_barrier\n"
MF "v[112:115],v[16:19],v[176:179],v[112:115]\n"
"v_exp_f32 v100,v100\n"
MF "v[112:115],v[20:23],v[196:199],v[112:115]\n"
"s_nop 0\n"
TR "v[16:17],v2 offset:49152\n"
TR "v[18:19],v2 offset:55296\n"
MF "v[112:115],v[24:27],v[216:219],v[112:115]\n"
"s_add_u32 s73,s73,12288\n"
TR "v[20:21],v3 offset:49152\n"
TR "v[22:23],v3 offset:55296\n"
MF "v[112:115],v[28:31],v[236:239],v[112:115]\n"
"s_cmp_eq_u32 s73,s19\n"
"s_cselect_b32 s73,s18,s73\n"
"s_add_u32 m0,s65,24576\n"
MF "v[116:119],v[28:31],v[240:243],v[116:119]\n"
LD "v8,s[36:39],s73 offen nt lds\n"
MF "v[116:119],v[24:27],v[220:223],v[116:119]\n"
"s_add_u32 m0,s65,25472\n"
"v_exp_f32 v101,v101\n"
MF "v[120:123],v[24:27],v[224:227],v[120:123]\n"
LD "v8,s[36:39],s73 offen offset:128 nt lds\n"
MF "v[120:123],v[28:31],v[244:247],v[120:123]\n"
"v_exp_f32 v102,v102\n"
MF "v[124:127],v[28:31],v[248:251],v[124:127]\n"
"s_add_u32 m0,s65,26368\n"
"v_exp_f32 v103,v103\n"
MF "v[124:127],v[24:27],v[228:231],v[124:127]\n"
LD "v8,s[36:39],s73 offen offset:256 nt lds\n"
MF "v[128:131],v[24:27],v[232:235],v[128:131]\n"
"v_add_f32 v59,v54,v100\n"
"v_add_f32 v59,v59,v101\n"
MF "v[128:131],v[28:31],v[252:255],v[128:131]\n"
"s_waitcnt lgkmcnt(0)\n"
TR "v[24:25],v4 offset:49152\n"
TR "v[26:27],v4 offset:55296\n"
MF "a[0:3],v[16:19],v[72:75],a[0:3]\n"
"s_add_u32 m0,s65,24576\n"
TR "v[28:29],v5 offset:49152\n"
TR "v[30:31],v5 offset:55296\n"
MF "a[20:23],v[20:23],v[72:75],a[20:23]\n"
LD "v8,s[36:39],s73 offen offset:3072 nt lds\n"
MF "a[24:27],v[20:23],v[76:79],a[24:27]\n"
CV "v100,v100,v101\n"
"v_add_f32 v59,v59,v102\n"
MF "a[4:7],v[16:19],v[76:79],a[4:7]\n"
"s_add_u32 m0,s65,25472\n"
"v_add_f32 v59,v59,v103\n"
CV "v101,v102,v103\n"
MF "a[8:11],v[16:19],v[80:83],a[8:11]\n"
LD "v8,s[36:39],s73 offen offset:3200 nt lds\n"
MF "a[28:31],v[20:23],v[80:83],a[28:31]\n"
"v_exp_f32 v104,v104\n"
MF "a[32:35],v[20:23],v[84:87],a[32:35]\n"
"s_add_u32 m0,s65,26368\n"
"v_exp_f32 v105,v105\n"
MF "a[12:15],v[16:19],v[84:87],a[12:15]\n"
LD "v8,s[36:39],s73 offen offset:3328 nt lds\n"
MF "a[16:19],v[16:19],v[88:91],a[16:19]\n"
"v_exp_f32 v106,v106\n"
MF "a[36:39],v[20:23],v[88:91],a[36:39]\n"
"s_waitcnt lgkmcnt(0)\n"
TR "v[16:17],v2 offset:50176\n"
TR "v[18:19],v2 offset:56320\n"
MF "a[56:59],v[24:27],v[88:91],a[56:59]\n"
"s_nop 0\n"
TR "v[20:21],v3 offset:50176\n"
TR "v[22:23],v3 offset:56320\n"
MF "a[76:79],v[28:31],v[88:91],a[76:79]\n"
"v_exp_f32 v107,v107\n"
MF "a[72:75],v[28:31],v[84:87],a[72:75]\n"
"v_add_f32 v60,v55,v104\n"
"v_add_f32 v60,v60,v105\n"
MF "a[52:55],v[24:27],v[84:87],a[52:55]\n"
CV "v104,v104,v105\n"
"v_add_f32 v60,v60,v106\n"
MF "a[48:51],v[24:27],v[80:83],a[48:51]\n"
"v_add_f32 v60,v60,v107\n"
CV "v105,v106,v107\n"
MF "a[68:71],v[28:31],v[80:83],a[68:71]\n"
"v_exp_f32 v108,v108\n"
MF "a[64:67],v[28:31],v[76:79],a[64:67]\n"
"v_exp_f32 v109,v109\n"
MF "a[44:47],v[24:27],v[76:79],a[44:47]\n"
"v_exp_f32 v110,v110\n"
MF "a[40:43],v[24:27],v[72:75],a[40:43]\n"
"v_exp_f32 v111,v111\n"
MF "a[60:63],v[28:31],v[72:75],a[60:63]\n"
"s_waitcnt lgkmcnt(0)\n"
TR "v[24:25],v4 offset:50176\n"
TR "v[26:27],v4 offset:56320\n"
MF "a[80:83],v[16:19],v[72:75],a[80:83]\n"
"s_nop 0\n"
TR "v[28:29],v5 offset:50176\n"
TR "v[30:31],v5 offset:56320\n"
MF "a[100:103],v[20:23],v[72:75],a[100:103]\n"
"v_add_f32 v61,v56,v108\n"
"v_add_f32 v61,v61,v109\n"
MF "a[104:107],v[20:23],v[76:79],a[104:107]\n"
CV "v108,v108,v109\n"
"v_add_f32 v61,v61,v110\n"
MF "a[84:87],v[16:19],v[76:79],a[84:87]\n"
"v_add_f32 v61,v61,v111\n"
CV "v109,v110,v111\n"
MF "a[88:91],v[16:19],v[80:83],a[88:91]\n"
"v_exp_f32 v112,v112\n"
MF "a[108:111],v[20:23],v[80:83],a[108:111]\n"
"v_exp_f32 v113,v113\n"
MF "a[112:115],v[20:23],v[84:87],a[112:115]\n"
"v_exp_f32 v114,v114\n"
MF "a[92:95],v[16:19],v[84:87],a[92:95]\n"
"v_exp_f32 v115,v115\n"
MF "a[96:99],v[16:19],v[88:91],a[96:99]\n"
"v_add_f32 v57,v57,v112\n"
"v_add_f32 v57,v57,v113\n"
MF "a[116:119],v[20:23],v[88:91],a[116:119]\n"
"s_waitcnt lgkmcnt(0)\n"
TR "v[16:17],v2 offset:51200\n"
TR "v[18:19],v2 offset:57344\n"
MF "a[136:139],v[24:27],v[88:91],a[136:139]\n"
"s_nop 0\n"
TR "v[20:21],v3 offset:51200\n"
TR "v[22:23],v3 offset:57344\n"
MF "a[156:159],v[28:31],v[88:91],a[156:159]\n"
CV "v94,v112,v113\n"
"v_add_f32 v57,v57,v114\n"
MF "a[152:155],v[28:31],v[84:87],a[152:155]\n"
"v_add_f32 v57,v57,v115\n"
CV "v95,v114,v115\n"
MF "a[132:135],v[24:27],v[84:87],a[132:135]\n"
"v_exp_f32 v116,v116\n"
MF "a[128:131],v[24:27],v[80:83],a[128:131]\n"
"v_exp_f32 v117,v117\n"
MF "a[148:151],v[28:31],v[80:83],a[148:151]\n"
"v_exp_f32 v118,v118\n"
MF "a[144:147],v[28:31],v[76:79],a[144:147]\n"
"v_exp_f32 v119,v119\n"
MF "a[124:127],v[24:27],v[76:79],a[124:127]\n"
"v_add_f32 v58,v58,v116\n"
"v_add_f32 v58,v58,v117\n"
MF "a[120:123],v[24:27],v[72:75],a[120:123]\n"
CV "v98,v116,v117\n"
"v_add_f32 v58,v58,v118\n"
MF "a[140:143],v[28:31],v[72:75],a[140:143]\n"
"s_waitcnt lgkmcnt(0)\n"
TR "v[24:25],v4 offset:51200\n"
TR "v[26:27],v4 offset:57344\n"
MF "a[160:163],v[16:19],v[72:75],a[160:163]\n"
"s_nop 0\n"
TR "v[28:29],v5 offset:51200\n"
TR "v[30:31],v5 offset:57344\n"
MF "a[180:183],v[20:23],v[72:75],a[180:183]\n"
"v_add_f32 v58,v58,v119\n"
CV "v99,v118,v119\n"
MF "a[184:187],v[20:23],v[76:79],a[184:187]\n"
"v_exp_f32 v120,v120\n"
MF "a[164:167],v[16:19],v[76:79],a[164:167]\n"
"v_exp_f32 v121,v121\n"
MF "a[168:171],v[16:19],v[80:83],a[168:171]\n"
"v_exp_f32 v122,v122\n"
MF "a[188:191],v[20:23],v[80:83],a[188:191]\n"
"v_exp_f32 v123,v123\n"
MF "a[192:195],v[20:23],v[84:87],a[192:195]\n"
"v_add_f32 v59,v59,v120\n"
"v_add_f32 v59,v59,v121\n"
MF "a[172:175],v[16:19],v[84:87],a[172:175]\n"
CV "v102,v120,v121\n"
"v_add_f32 v59,v59,v122\n"
MF "a[176:179],v[16:19],v[88:91],a[176:179]\n"
"s_waitcnt vmcnt(6) lgkmcnt(0)\n"
"s_barrier\n"
MF "a[196:199],v[20:23],v[88:91],a[196:199]\n"
"s_add_u32 s72,s72,6144\n"
"ds_read_b128 v[16:19],v0\n"
"ds_read_b128 v[20:23],v1\n"
MF "a[216:219],v[24:27],v[88:91],a[216:219]\n"
"s_cmp_eq_u32 s72,s17\n"
"s_cselect_b32 s72,s16,s72\n"
"s_add_u32 m0,s64,49152\n"
MF "a[236:239],v[28:31],v[88:91],a[236:239]\n"
LD "v7,s[32:35],s72 offen nt lds\n"
MF "a[232:235],v[28:31],v[84:87],a[232:235]\n"
"s_add_u32 m0,s64,50048\n"
"v_add_f32 v59,v59,v123\n"
CV "v103,v122,v123\n"
MF "a[212:215],v[24:27],v[84:87],a[212:215]\n"
LD "v7,s[32:35],s72 offen offset:128 nt lds\n"
MF "a[208:211],v[24:27],v[80:83],a[208:211]\n"
"s_add_u32 m0,s64,50944\n"
MF "a[228:231],v[28:31],v[80:83],a[228:231]\n"
LD "v7,s[32:35],s72 offen offset:256 nt lds\n"
MF "a[224:227],v[28:31],v[76:79],a[224:227]\n"
MF "a[204:207],v[24:27],v[76:79],a[204:207]\n"
MF "a[200:203],v[24:27],v[72:75],a[200:203]\n"
MF "a[220:223],v[28:31],v[72:75],a[220:223]\n"
"s_waitcnt lgkmcnt(0)\n"
".Lmid:\n"
MF "v[72:75],v[16:19],v[136:139],v[32:35]\n"
"s_add_u32 s72,s72,6144\n"
"ds_read_b128 v[24:27],v0 offset:1024\n"
"ds_read_b128 v[28:31],v1 offset:1024\n"
MF "v[72:75],v[20:23],v[156:159],v[72:75]\n"
"s_add_u32 m0,s64,55296\n"
"v_exp_f32 v124,v124\n"
MF "v[76:79],v[20:23],v[160:163],v[36:39]\n"
LD "v7,s[32:35],s72 offen nt lds\n"
MF "v[76:79],v[16:19],v[140:143],v[76:79]\n"
"v_exp_f32 v125,v125\n"
MF "v[80:83],v[16:19],v[144:147],v[40:43]\n"
"s_add_u32 m0,s64,56192\n"
"v_exp_f32 v126,v126\n"
MF "v[80:83],v[20:23],v[164:167],v[80:83]\n"
LD "v7,s[32:35],s72 offen offset:128 nt lds\n"
MF "v[84:87],v[20:23],v[168:171],v[44:47]\n"
"v_exp_f32 v127,v127\n"
MF "v[84:87],v[16:19],v[148:151],v[84:87]\n"
"s_add_u32 m0,s64,57088\n"
"v_add_f32 v60,v60,v124\n"
"v_add_f32 v60,v60,v125\n"
MF "v[88:91],v[16:19],v[152:155],v[48:51]\n"
LD "v7,s[32:35],s72 offen offset:256 nt lds\n"
MF "v[88:91],v[20:23],v[172:175],v[88:91]\n"
"s_waitcnt lgkmcnt(0)\n"
"ds_read_b128 v[16:19],v0 offset:2048\n"
"ds_read_b128 v[20:23],v1 offset:2048\n"
MF "v[88:91],v[24:27],v[192:195],v[88:91]\n"
CV "v106,v124,v125\n"
"v_add_f32 v60,v60,v126\n"
MF "v[88:91],v[28:31],v[212:215],v[88:91]\n"
"v_add_f32 v60,v60,v127\n"
CV "v107,v126,v127\n"
MF "v[84:87],v[28:31],v[208:211],v[84:87]\n"
"v_exp_f32 v128,v128\n"
MF "v[84:87],v[24:27],v[188:191],v[84:87]\n"
"v_exp_f32 v129,v129\n"
MF "v[80:83],v[24:27],v[184:187],v[80:83]\n"
"v_exp_f32 v130,v130\n"
MF "v[80:83],v[28:31],v[204:207],v[80:83]\n"
"v_exp_f32 v131,v131\n"
MF "v[76:79],v[28:31],v[200:203],v[76:79]\n"
"v_add_f32 v61,v61,v128\n"
"v_add_f32 v61,v61,v129\n"
MF "v[76:79],v[24:27],v[180:183],v[76:79]\n"
CV "v110,v128,v129\n"
"v_add_f32 v61,v61,v130\n"
MF "v[72:75],v[24:27],v[176:179],v[72:75]\n"
"v_add_f32 v61,v61,v131\n"
CV "v111,v130,v131\n"
MF "v[72:75],v[28:31],v[196:199],v[72:75]\n"
"s_waitcnt lgkmcnt(0)\n"
"ds_read_b128 v[24:27],v0 offset:6144\n"
"ds_read_b128 v[28:31],v1 offset:6144\n"
MF "v[72:75],v[16:19],v[216:219],v[72:75]\n"
MF "v[72:75],v[20:23],v[236:239],v[72:75]\n"
MF "v[76:79],v[20:23],v[240:243],v[76:79]\n"
MF "v[76:79],v[16:19],v[220:223],v[76:79]\n"
MF "v[80:83],v[16:19],v[224:227],v[80:83]\n"
MF "v[80:83],v[20:23],v[244:247],v[80:83]\n"
MF "v[84:87],v[20:23],v[248:251],v[84:87]\n"
MF "v[84:87],v[16:19],v[228:231],v[84:87]\n"
"v_max3_f32 v10,v57,v58,v59\n"
"v_max3_f32 v10,v10,v60,v61\n"
MF "v[88:91],v[16:19],v[232:235],v[88:91]\n"
"v_cmp_ngt_f32 vcc,s95,v10\n"
MF "v[88:91],v[20:23],v[252:255],v[88:91]\n"
"s_waitcnt lgkmcnt(0)\n"
"s_cbranch_vccnz .Lstub_15\n"
".Lback_15:\n"
MF "v[112:115],v[24:27],v[136:139],v[32:35]\n"
"s_add_u32 s74,s74,12288\n"
"ds_read_b128 v[16:19],v0 offset:7168\n"
"ds_read_b128 v[20:23],v1 offset:7168\n"
MF "v[112:115],v[28:31],v[156:159],v[112:115]\n"
"s_cmp_eq_u32 s74,s20\n"
"s_cselect_b32 s74,s70,s74\n"
MF "v[116:119],v[28:31],v[160:163],v[36:39]\n"
"v_exp_f32 v72,v72\n"
MF "v[116:119],v[24:27],v[140:143],v[116:119]\n"
"v_exp_f32 v73,v73\n"
MF "v[120:123],v[24:27],v[144:147],v[40:43]\n"
"v_exp_f32 v74,v74\n"
MF "v[120:123],v[28:31],v[164:167],v[120:123]\n"
"v_exp_f32 v75,v75\n"
MF "v[124:127],v[28:31],v[168:171],v[44:47]\n"
"v_add_f32 v52,v57,v72\n"
"v_add_f32 v52,v52,v73\n"
MF "v[124:127],v[24:27],v[148:151],v[124:127]\n"
CV "v72,v72,v73\n"
"v_add_f32 v52,v52,v74\n"
MF "v[128:131],v[24:27],v[152:155],v[48:51]\n"
"v_add_f32 v52,v52,v75\n"
CV "v73,v74,v75\n"
MF "v[128:131],v[28:31],v[172:175],v[128:131]\n"
"s_waitcnt lgkmcnt(0)\n"
"ds_read_b128 v[24:27],v0 offset:8192\n"
"ds_read_b128 v[28:31],v1 offset:8192\n"
MF "v[128:131],v[16:19],v[192:195],v[128:131]\n"
"v_exp_f32 v76,v76\n"
MF "v[128:131],v[20:23],v[212:215],v[128:131]\n"
"v_exp_f32 v77,v77\n"
MF "v[124:127],v[20:23],v[208:211],v[124:127]\n"
"v_exp_f32 v78,v78\n"
MF "v[124:127],v[16:19],v[188:191],v[124:127]\n"
"v_exp_f32 v79,v79\n"
MF "v[120:123],v[16:19],v[184:187],v[120:123]\n"
"v_add_f32 v53,v58,v76\n"
"v_add_f32 v53,v53,v77\n"
MF "v[120:123],v[20:23],v[204:207],v[120:123]\n"
CV "v76,v76,v77\n"
"v_add_f32 v53,v53,v78\n"
MF "v[116:119],v[20:23],v[200:203],v[116:119]\n"
"v_add_f32 v53,v53,v79\n"
CV "v77,v78,v79\n"
MF "v[116:119],v[16:19],v[180:183],v[116:119]\n"
"s_waitcnt vmcnt(6) lgkmcnt(0)\n"
"s_barrier\n"
MF "v[112:115],v[16:19],v[176:179],v[112:115]\n"
"v_exp_f32 v80,v80\n"
MF "v[112:115],v[20:23],v[196:199],v[112:115]\n"
"s_nop 0\n"
TR "v[16:17],v2 offset:24576\n"
TR "v[18:19],v2 offset:30720\n"
MF "v[112:115],v[24:27],v[216:219],v[112:115]\n"
"s_add_u32 s73,s73,12288\n"
TR "v[20:21],v3 offset:24576\n"
TR "v[22:23],v3 offset:30720\n"
MF "v[112:115],v[28:31],v[236:239],v[112:115]\n"
"s_cmp_eq_u32 s73,s19\n"
"s_cselect_b32 s73,s18,s73\n"
"s_add_u32 m0,s65,0\n"
MF "v[116:119],v[28:31],v[240:243],v[116:119]\n"
LD "v8,s[36:39],s73 offen nt lds\n"
MF "v[116:119],v[24:27],v[220:223],v[116:119]\n"
"s_add_u32 m0,s65,896\n"
"v_exp_f32 v81,v81\n"
MF "v[120:123],v[24:27],v[224:227],v[120:123]\n"
LD "v8,s[36:39],s73 offen offset:128 nt lds\n"
MF "v[120:123],v[28:31],v[244:247],v[120:123]\n"
"v_exp_f32 v82,v82\n"
MF "v[124:127],v[28:31],v[248:251],v[124:127]\n"
"s_add_u32 m0,s65,1792\n"
"v_exp_f32 v83,v83\n"
MF "v[124:127],v[24:27],v[228:231],v[124:127]\n"
LD "v8,s[36:39],s73 offen offset:256 nt lds\n"
MF "v[128:131],v[24:27],v[232:235],v[128:131]\n"
"v_add_f32 v54,v59,v80\n"
"v_add_f32 v54,v54,v81\n"
MF "v[128:131],v[28:31],v[252:255],v[128:131]\n"
"s_waitcnt lgkmcnt(0)\n"
TR "v[24:25],v4 offset:24576\n"
TR "v[26:27],v4 offset:30720\n"
MF "a[0:3],v[16:19],v[92:95],a[0:3]\n"
"s_add_u32 m0,s65,0\n"
TR "v[28:29],v5 offset:24576\n"
TR "v[30:31],v5 offset:30720\n"
MF "a[20:23],v[20:23],v[92:95],a[20:23]\n"
LD "v8,s[36:39],s73 offen offset:3072 nt lds\n"
MF "a[24:27],v[20:23],v[96:99],a[24:27]\n"
CV "v80,v80,v81\n"
"v_add_f32 v54,v54,v82\n"
MF "a[4:7],v[16:19],v[96:99],a[4:7]\n"
"s_add_u32 m0,s65,896\n"
"v_add_f32 v54,v54,v83\n"
CV "v81,v82,v83\n"
MF "a[8:11],v[16:19],v[100:103],a[8:11]\n"
LD "v8,s[36:39],s73 offen offset:3200 nt lds\n"
MF "a[28:31],v[20:23],v[100:103],a[28:31]\n"
"v_exp_f32 v84,v84\n"
MF "a[32:35],v[20:23],v[104:107],a[32:35]\n"
"s_add_u32 m0,s65,1792\n"
"v_exp_f32 v85,v85\n"
MF "a[12:15],v[16:19],v[104:107],a[12:15]\n"
LD "v8,s[36:39],s73 offen offset:3328 nt lds\n"
MF "a[16:19],v[16:19],v[108:111],a[16:19]\n"
"v_exp_f32 v86,v86\n"
MF "a[36:39],v[20:23],v[108:111],a[36:39]\n"
"s_waitcnt lgkmcnt(0)\n"
TR "v[16:17],v2 offset:25600\n"
TR "v[18:19],v2 offset:31744\n"
MF "a[56:59],v[24:27],v[108:111],a[56:59]\n"
"s_nop 0\n"
TR "v[20:21],v3 offset:25600\n"
TR "v[22:23],v3 offset:31744\n"
MF "a[76:79],v[28:31],v[108:111],a[76:79]\n"
"v_exp_f32 v87,v87\n"
MF "a[72:75],v[28:31],v[104:107],a[72:75]\n"
"v_add_f32 v55,v60,v84\n"
"v_add_f32 v55,v55,v85\n"
MF "a[52:55],v[24:27],v[104:107],a[52:55]\n"
CV "v84,v84,v85\n"
"v_add_f32 v55,v55,v86\n"
MF "a[48:51],v[24:27],v[100:103],a[48:51]\n"
"v_add_f32 v55,v55,v87\n"
CV "v85,v86,v87\n"
MF "a[68:71],v[28:31],v[100:103],a[68:71]\n"
"v_exp_f32 v88,v88\n"
MF "a[64:67],v[28:31],v[96:99],a[64:67]\n"
"v_exp_f32 v89,v89\n"
MF "a[44:47],v[24:27],v[96:99],a[44:47]\n"
"v_exp_f32 v90,v90\n"
MF "a[40:43],v[24:27],v[92:95],a[40:43]\n"
"v_exp_f32 v91,v91\n"
MF "a[60:63],v[28:31],v[92:95],a[60:63]\n"
"s_waitcnt lgkmcnt(0)\n"
TR "v[24:25],v4 offset:25600\n"
TR "v[26:27],v4 offset:31744\n"
MF "a[80:83],v[16:19],v[92:95],a[80:83]\n"
"s_nop 0\n"
TR "v[28:29],v5 offset:25600\n"
TR "v[30:31],v5 offset:31744\n"
MF "a[100:103],v[20:23],v[92:95],a[100:103]\n"
"v_add_f32 v56,v61,v88\n"
"v_add_f32 v56,v56,v89\n"
MF "a[104:107],v[20:23],v[96:99],a[104:107]\n"
CV "v88,v88,v89\n"
"v_add_f32 v56,v56,v90\n"
MF "a[84:87],v[16:19],v[96:99],a[84:87]\n"
"v_add_f32 v56,v56,v91\n"
CV "v89,v90,v91\n"
MF "a[88:91],v[16:19],v[100:103],a[88:91]\n"
"v_exp_f32 v112,v112\n"
MF "a[108:111],v[20:23],v[100:103],a[108:111]\n"
"v_exp_f32 v113,v113\n"
MF "a[112:115],v[20:23],v[104:107],a[112:115]\n"
"v_exp_f32 v114,v114\n"
MF "a[92:95],v[16:19],v[104:107],a[92:95]\n"
"v_exp_f32 v115,v115\n"
MF "a[96:99],v[16:19],v[108:111],a[96:99]\n"
"v_add_f32 v52,v52,v112\n"
"v_add_f32 v52,v52,v113\n"
MF "a[116:119],v[20:23],v[108:111],a[116:119]\n"
"s_waitcnt lgkmcnt(0)\n"
TR "v[16:17],v2 offset:26624\n"
TR "v[18:19],v2 offset:32768\n"
MF "a[136:139],v[24:27],v[108:111],a[136:139]\n"
"s_nop 0\n"
TR "v[20:21],v3 offset:26624\n"
TR "v[22:23],v3 offset:32768\n"
MF "a[156:159],v[28:31],v[108:111],a[156:159]\n"
CV "v74,v112,v113\n"
"v_add_f32 v52,v52,v114\n"
MF "a[152:155],v[28:31],v[104:107],a[152:155]\n"
"v_add_f32 v52,v52,v115\n"
CV "v75,v114,v115\n"
MF "a[132:135],v[24:27],v[104:107],a[132:135]\n"
"v_exp_f32 v116,v116\n"
MF "a[128:131],v[24:27],v[100:103],a[128:131]\n"
"v_exp_f32 v117,v117\n"
MF "a[148:151],v[28:31],v[100:103],a[148:151]\n"
"v_exp_f32 v118,v118\n"
MF "a[144:147],v[28:31],v[96:99],a[144:147]\n"
"v_exp_f32 v119,v119\n"
MF "a[124:127],v[24:27],v[96:99],a[124:127]\n"
"v_add_f32 v53,v53,v116\n"
"v_add_f32 v53,v53,v117\n"
MF "a[120:123],v[24:27],v[92:95],a[120:123]\n"
CV "v78,v116,v117\n"
"v_add_f32 v53,v53,v118\n"
MF "a[140:143],v[28:31],v[92:95],a[140:143]\n"
"s_waitcnt lgkmcnt(0)\n"
TR "v[24:25],v4 offset:26624\n"
TR "v[26:27],v4 offset:32768\n"
MF "a[160:163],v[16:19],v[92:95],a[160:163]\n"
"s_nop 0\n"
TR "v[28:29],v5 offset:26624\n"
TR "v[30:31],v5 offset:32768\n"
MF "a[180:183],v[20:23],v[92:95],a[180:183]\n"
"v_add_f32 v53,v53,v119\n"
CV "v79,v118,v119\n"
MF "a[184:187],v[20:23],v[96:99],a[184:187]\n"
"v_exp_f32 v120,v120\n"
MF "a[164:167],v[16:19],v[96:99],a[164:167]\n"
"v_exp_f32 v121,v121\n"
MF "a[168:171],v[16:19],v[100:103],a[168:171]\n"
"v_exp_f32 v122,v122\n"
MF "a[188:191],v[20:23],v[100:103],a[188:191]\n"
"v_exp_f32 v123,v123\n"
MF "a[192:195],v[20:23],v[104:107],a[192:195]\n"
"v_add_f32 v54,v54,v120\n"
"v_add_f32 v54,v54,v121\n"
MF "a[172:175],v[16:19],v[104:107],a[172:175]\n"
CV "v82,v120,v121\n"
"v_add_f32 v54,v54,v122\n"
MF "a[176:179],v[16:19],v[108:111],a[176:179]\n"
"s_waitcnt vmcnt(6) lgkmcnt(0)\n"
"s_barrier\n"
MF "a[196:199],v[20:23],v[108:111],a[196:199]\n"
"s_add_u32 s72,s72,6144\n"
"ds_read_b128 v[16:19],v0 offset:49152\n"
"ds_read_b128 v[20:23],v1 offset:49152\n"
MF "a[216:219],v[24:27],v[108:111],a[216:219]\n"
"s_cmp_eq_u32 s72,s17\n"
"s_cselect_b32 s72,s16,s72\n"
"s_add_u32 m0,s64,24576\n"
MF "a[236:239],v[28:31],v[108:111],a[236:239]\n"
LD "v7,s[32:35],s72 offen nt lds\n"
MF "a[232:235],v[28:31],v[104:107],a[232:235]\n"
"s_add_u32 m0,s64,25472\n"
"v_add_f32 v54,v54,v123\n"
CV "v83,v122,v123\n"
MF "a[212:215],v[24:27],v[104:107],a[212:215]\n"
LD "v7,s[32:35],s72 offen offset:128 nt lds\n"
MF "a[208:211],v[24:27],v[100:103],a[208:211]\n"
"s_add_u32 m0,s64,26368\n"
MF "a[228:231],v[28:31],v[100:103],a[228:231]\n"
LD "v7,s[32:35],s72 offen offset:256 nt lds\n"
MF "a[224:227],v[28:31],v[96:99],a[224:227]\n"
MF "a[204:207],v[24:27],v[96:99],a[204:207]\n"
MF "a[200:203],v[24:27],v[92:95],a[200:203]\n"
MF "a[220:223],v[28:31],v[92:95],a[220:223]\n"
"s_waitcnt lgkmcnt(0)\n"
MF "v[92:95],v[16:19],v[136:139],v[32:35]\n"
"s_add_u32 s72,s72,6144\n"
"ds_read_b128 v[24:27],v0 offset:50176\n"
"ds_read_b128 v[28:31],v1 offset:50176\n"
MF "v[92:95],v[20:23],v[156:159],v[92:95]\n"
"s_add_u32 m0,s64,30720\n"
"v_exp_f32 v124,v124\n"
MF "v[96:99],v[20:23],v[160:163],v[36:39]\n"
LD "v7,s[32:35],s72 offen nt lds\n"
MF "v[96:99],v[16:19],v[140:143],v[96:99]\n"
"v_exp_f32 v125,v125\n"
MF "v[100:103],v[16:19],v[144:147],v[40:43]\n"
"s_add_u32 m0,s64,31616\n"
"v_exp_f32 v126,v126\n"
MF "v[100:103],v[20:23],v[164:167],v[100:103]\n"
LD "v7,s[32:35],s72 offen offset:128 nt lds\n"
MF "v[104:107],v[20:23],v[168:171],v[44:47]\n"
"v_exp_f32 v127,v127\n"
MF "v[104:107],v[16:19],v[148:151],v[104:107]\n"
"s_add_u32 m0,s64,32512\n"
"v_add_f32 v55,v55,v124\n"
"v_add_f32 v55,v55,v125\n"
MF "v[108:111],v[16:19],v[152:155],v[48:51]\n"
LD "v7,s[32:35],s72 offen offset:256 nt lds\n"
MF "v[108:111],v[20:23],v[172:175],v[108:111]\n"
"s_waitcnt lgkmcnt(0)\n"
"ds_read_b128 v[16:19],v0 offset:51200\n"
"ds_read_b128 v[20:23],v1 offset:51200\n"
MF "v[108:111],v[24:27],v[192:195],v[108:111]\n"
CV "v86,v124,v125\n"
"v_add_f32 v55,v55,v126\n"
MF "v[108:111],v[28:31],v[212:215],v[108:111]\n"
"v_add_f32 v55,v55,v127\n"
CV "v87,v126,v127\n"
MF "v[104:107],v[28:31],v[208:211],v[104:107]\n"
"v_exp_f32 v128,v128\n"
MF "v[104:107],v[24:27],v[188:191],v[104:107]\n"
"v_exp_f32 v129,v129\n"
MF "v[100:103],v[24:27],v[184:187],v[100:103]\n"
"v_exp_f32 v130,v130\n"
MF "v[100:103],v[28:31],v[204:207],v[100:103]\n"
"v_exp_f32 v131,v131\n"
MF "v[96:99],v[28:31],v[200:203],v[96:99]\n"
"v_add_f32 v56,v56,v128\n"
"v_add_f32 v56,v56,v129\n"
MF "v[96:99],v[24:27],v[180:183],v[96:99]\n"
CV "v90,v128,v129\n"
"v_add_f32 v56,v56,v130\n"
MF "v[92:95],v[24:27],v[176:179],v[92:95]\n"
"v_add_f32 v56,v56,v131\n"
CV "v91,v130,v131\n"
MF "v[92:95],v[28:31],v[196:199],v[92:95]\n"
"s_waitcnt lgkmcnt(0)\n"
"ds_read_b128 v[24:27],v0 offset:55296\n"
"ds_read_b128 v[28:31],v1 offset:55296\n"
MF "v[92:95],v[16:19],v[216:219],v[92:95]\n"
MF "v[92:95],v[20:23],v[236:239],v[92:95]\n"
MF "v[96:99],v[20:23],v[240:243],v[96:99]\n"
MF "v[96:99],v[16:19],v[220:223],v[96:99]\n"
MF "v[100:103],v[16:19],v[224:227],v[100:103]\n"
MF "v[100:103],v[20:23],v[244:247],v[100:103]\n"
MF "v[104:107],v[20:23],v[248:251],v[104:107]\n"
MF "v[104:107],v[16:19],v[228:231],v[104:107]\n"
"v_max3_f32 v10,v52,v53,v54\n"
"v_max3_f32 v10,v10,v55,v56\n"
MF "v[108:111],v[16:19],v[232:235],v[108:111]\n"
"v_cmp_ngt_f32 vcc,s77,v10\n"
MF "v[108:111],v[20:23],v[252:255],v[108:111]\n"
"s_waitcnt lgkmcnt(0)\n"
"s_cbranch_vccnz .Lstub_16\n"
".Lback_16:\n"
MF "v[112:115],v[24:27],v[136:139],v[32:35]\n"
"s_add_u32 s74,s74,12288\n"
"ds_read_b128 v[16:19],v0 offset:56320\n"
"ds_read_b128 v[20:23],v1 offset:56320\n"
MF "v[112:115],v[28:31],v[156:159],v[112:115]\n"
"s_cmp_eq_u32 s74,s20\n"
"s_cselect_b32 s74,s70,s74\n"
MF "v[116:119],v[28:31],v[160:163],v[36:39]\n"
"v_exp_f32 v92,v92\n"
MF "v[116:119],v[24:27],v[140:143],v[116:119]\n"
"v_exp_f32 v93,v93\n"
MF "v[120:123],v[24:27],v[144:147],v[40:43]\n"
"v_exp_f32 v94,v94\n"
MF "v[120:123],v[28:31],v[164:167],v[120:123]\n"
"v_exp_f32 v95,v95\n"
MF "v[124:127],v[28:31],v[168:171],v[44:47]\n"
"v_add_f32 v57,v52,v92\n"
"v_add_f32 v57,v57,v93\n"
MF "v[124:127],v[24:27],v[148:151],v[124:127]\n"
CV "v92,v92,v93\n"
"v_add_f32 v57,v57,v94\n"
MF "v[128:131],v[24:27],v[152:155],v[48:51]\n"
"v_add_f32 v57,v57,v95\n"
CV "v93,v94,v95\n"
MF "v[128:131],v[28:31],v[172:175],v[128:131]\n"
"s_waitcnt lgkmcnt(0)\n"
"ds_read_b128 v[24:27],v0 offset:57344\n"
"ds_read_b128 v[28:31],v1 offset:57344\n"
MF "v[128:131],v[16:19],v[192:195],v[128:131]\n"
"s_sub_u32 s61,s61,1\n"
"v_exp_f32 v96,v96\n"
MF "v[128:131],v[20:23],v[212:215],v[128:131]\n"
"s_cmp_eq_u32 s61,1\n"
"s_cselect_b32 s95,0,s95\n"
"v_add_f32 v58,v53,v96\n"
MF "v[124:127],v[20:23],v[208:211],v[124:127]\n"
"v_exp_f32 v97,v97\n"
MF "v[124:127],v[16:19],v[188:191],v[124:127]\n"
"v_exp_f32 v98,v98\n"
MF "v[120:123],v[16:19],v[184:187],v[120:123]\n"
"v_exp_f32 v99,v99\n"
MF "v[120:123],v[20:23],v[204:207],v[120:123]\n"
"v_add_f32 v58,v58,v97\n"
CV "v96,v96,v97\n"
MF "v[116:119],v[20:23],v[200:203],v[116:119]\n"
"v_add_f32 v58,v58,v98\n"
"v_add_f32 v58,v58,v99\n"
MF "v[116:119],v[16:19],v[180:183],v[116:119]\n"
"s_waitcnt vmcnt(6) lgkmcnt(0)\n"
"s_barrier\n"
MF "v[112:115],v[16:19],v[176:179],v[112:115]\n"
CV "v97,v98,v99\n"
MF "v[112:115],v[20:23],v[196:199],v[112:115]\n"
"s_nop 0\n"
TR "v[16:17],v2\n"
TR "v[18:19],v2 offset:6144\n"
MF "v[112:115],v[24:27],v[216:219],v[112:115]\n"
"s_add_u32 s73,s73,12288\n"
TR "v[20:21],v3\n"
TR "v[22:23],v3 offset:6144\n"
MF "v[112:115],v[28:31],v[236:239],v[112:115]\n"
"s_cmp_eq_u32 s73,s19\n"
"s_cselect_b32 s73,s18,s73\n"
"s_add_u32 m0,s65,49152\n"
MF "v[116:119],v[28:31],v[240:243],v[116:119]\n"
LD "v8,s[36:39],s73 offen nt lds\n"
MF "v[116:119],v[24:27],v[220:223],v[116:119]\n"
"s_add_u32 m0,s65,50048\n"
"v_exp_f32 v100,v100\n"
MF "v[120:123],v[24:27],v[224:227],v[120:123]\n"
LD "v8,s[36:39],s73 offen offset:128 nt lds\n"
MF "v[120:123],v[28:31],v[244:247],v[120:123]\n"
"v_exp_f32 v101,v101\n"
MF "v[124:127],v[28:31],v[248:251],v[124:127]\n"
"s_add_u32 m0,s65,50944\n"
"v_exp_f32 v102,v102\n"
MF "v[124:127],v[24:27],v[228:231],v[124:127]\n"
LD "v8,s[36:39],s73 offen offset:256 nt lds\n"
MF "v[128:131],v[24:27],v[232:235],v[128:131]\n"
"v_exp_f32 v103,v103\n"
MF "v[128:131],v[28:31],v[252:255],v[128:131]\n"
"s_waitcnt lgkmcnt(0)\n"
TR "v[24:25],v4\n"
TR "v[26:27],v4 offset:6144\n"
MF "a[0:3],v[16:19],v[72:75],a[0:3]\n"
"s_add_u32 m0,s65,49152\n"
TR "v[28:29],v5\n"
TR "v[30:31],v5 offset:6144\n"
MF "a[20:23],v[20:23],v[72:75],a[20:23]\n"
LD "v8,s[36:39],s73 offen offset:3072 nt lds\n"
MF "a[24:27],v[20:23],v[76:79],a[24:27]\n"
"v_add_f32 v59,v54,v100\n"
"v_add_f32 v59,v59,v101\n"
MF "a[4:7],v[16:19],v[76:79],a[4:7]\n"
"s_add_u32 m0,s65,50048\n"
CV "v100,v100,v101\n"
"v_add_f32 v59,v59,v102\n"
MF "a[8:11],v[16:19],v[80:83],a[8:11]\n"
LD "v8,s[36:39],s73 offen offset:3200 nt lds\n"
MF "a[28:31],v[20:23],v[80:83],a[28:31]\n"
"v_add_f32 v59,v59,v103\n"
CV "v101,v102,v103\n"
MF "a[32:35],v[20:23],v[84:87],a[32:35]\n"
"s_add_u32 m0,s65,50944\n"
"v_exp_f32 v104,v104\n"
MF "a[12:15],v[16:19],v[84:87],a[12:15]\n"
LD "v8,s[36:39],s73 offen offset:3328 nt lds\n"
MF "a[16:19],v[16:19],v[88:91],a[16:19]\n"
"v_exp_f32 v105,v105\n"
MF "a[36:39],v[20:23],v[88:91],a[36:39]\n"
"s_waitcnt lgkmcnt(0)\n"
TR "v[16:17],v2 offset:1024\n"
TR "v[18:19],v2 offset:7168\n"
MF "a[56:59],v[24:27],v[88:91],a[56:59]\n"
"s_nop 0\n"
TR "v[20:21],v3 offset:1024\n"
TR "v[22:23],v3 offset:7168\n"
MF "a[76:79],v[28:31],v[88:91],a[76:79]\n"
"v_exp_f32 v106,v106\n"
MF "a[72:75],v[28:31],v[84:87],a[72:75]\n"
"v_exp_f32 v107,v107\n"
MF "a[52:55],v[24:27],v[84:87],a[52:55]\n"
"v_add_f32 v60,v55,v104\n"
"v_add_f32 v60,v60,v105\n"
MF "a[48:51],v[24:27],v[80:83],a[48:51]\n"
CV "v104,v104,v105\n"
"v_add_f32 v60,v60,v106\n"
MF "a[68:71],v[28:31],v[80:83],a[68:71]\n"
"v_add_f32 v60,v60,v107\n"
CV "v105,v106,v107\n"
MF "a[64:67],v[28:31],v[76:79],a[64:67]\n"
"v_exp_f32 v108,v108\n"
MF "a[44:47],v[24:27],v[76:79],a[44:47]\n"
"v_exp_f32 v109,v109\n"
MF "a[40:43],v[24:27],v[72:75],a[40:43]\n"
"v_exp_f32 v110,v110\n"
MF "a[60:63],v[28:31],v[72:75],a[60:63]\n"
"s_waitcnt lgkmcnt(0)\n"
TR "v[24:25],v4 offset:1024\n"
TR "v[26:27],v4 offset:7168\n"
MF "a[80:83],v[16:19],v[72:75],a[80:83]\n"
"s_nop 0\n"
TR "v[28:29],v5 offset:1024\n"
TR "v[30:31],v5 offset:7168\n"
MF "a[100:103],v[20:23],v[72:75],a[100:103]\n"
"v_exp_f32 v111,v111\n"
MF "a[104:107],v[20:23],v[76:79],a[104:107]\n"
"v_add_f32 v61,v56,v108\n"
"v_add_f32 v61,v61,v109\n"
MF "a[84:87],v[16:19],v[76:79],a[84:87]\n"
CV "v108,v108,v109\n"
"v_add_f32 v61,v61,v110\n"
MF "a[88:91],v[16:19],v[80:83],a[88:91]\n"
"v_add_f32 v61,v61,v111\n"
CV "v109,v110,v111\n"
MF "a[108:111],v[20:23],v[80:83],a[108:111]\n"
"v_exp_f32 v112,v112\n"
MF "a[112:115],v[20:23],v[84:87],a[112:115]\n"
"v_exp_f32 v113,v113\n"
MF "a[92:95],v[16:19],v[84:87],a[92:95]\n"
"v_exp_f32 v114,v114\n"
MF "a[96:99],v[16:19],v[88:91],a[96:99]\n"
"v_exp_f32 v115,v115\n"
MF "a[116:119],v[20:23],v[88:91],a[116:119]\n"
"s_waitcnt lgkmcnt(0)\n"
TR "v[16:17],v2 offset:2048\n"
TR "v[18:19],v2 offset:8192\n"
MF "a[136:139],v[24:27],v[88:91],a[136:139]\n"
"s_nop 0\n"
TR "v[20:21],v3 offset:2048\n"
TR "v[22:23],v3 offset:8192\n"
MF "a[156:159],v[28:31],v[88:91],a[156:159]\n"
"v_add_f32 v57,v57,v112\n"
"v_add_f32 v57,v57,v113\n"
MF "a[152:155],v[28:31],v[84:87],a[152:155]\n"
CV "v94,v112,v113\n"
"v_add_f32 v57,v57,v114\n"
MF "a[132:135],v[24:27],v[84:87],a[132:135]\n"
"v_add_f32 v57,v57,v115\n"
CV "v95,v114,v115\n"
MF "a[128:131],v[24:27],v[80:83],a[128:131]\n"
"v_exp_f32 v116,v116\n"
MF "a[148:151],v[28:31],v[80:83],a[148:151]\n"
"v_exp_f32 v117,v117\n"
MF "a[144:147],v[28:31],v[76:79],a[144:147]\n"
"v_exp_f32 v118,v118\n"
MF "a[124:127],v[24:27],v[76:79],a[124:127]\n"
"v_exp_f32 v119,v119\n"
MF "a[120:123],v[24:27],v[72:75],a[120:123]\n"
"v_add_f32 v58,v58,v116\n"
"v_add_f32 v58,v58,v117\n"
MF "a[140:143],v[28:31],v[72:75],a[140:143]\n"
"s_waitcnt lgkmcnt(0)\n"
TR "v[24:25],v4 offset:2048\n"
TR "v[26:27],v4 offset:8192\n"
MF "a[160:163],v[16:19],v[72:75],a[160:163]\n"
"s_nop 0\n"
TR "v[28:29],v5 offset:2048\n"
TR "v[30:31],v5 offset:8192\n"
MF "a[180:183],v[20:23],v[72:75],a[180:183]\n"
CV "v98,v116,v117\n"
"v_add_f32 v58,v58,v118\n"
MF "a[184:187],v[20:23],v[76:79],a[184:187]\n"
"v_add_f32 v58,v58,v119\n"
CV "v99,v118,v119\n"
MF "a[164:167],v[16:19],v[76:79],a[164:167]\n"
"v_exp_f32 v120,v120\n"
MF "a[168:171],v[16:19],v[80:83],a[168:171]\n"
"v_exp_f32 v121,v121\n"
MF "a[188:191],v[20:23],v[80:83],a[188:191]\n"
"v_exp_f32 v122,v122\n"
MF "a[192:195],v[20:23],v[84:87],a[192:195]\n"
"v_exp_f32 v123,v123\n"
MF "a[172:175],v[16:19],v[84:87],a[172:175]\n"
"v_add_f32 v59,v59,v120\n"
"v_add_f32 v59,v59,v121\n"
MF "a[176:179],v[16:19],v[88:91],a[176:179]\n"
"s_waitcnt vmcnt(6) lgkmcnt(0)\n"
"s_barrier\n"
MF "a[196:199],v[20:23],v[88:91],a[196:199]\n"
"s_add_u32 s72,s72,6144\n"
"ds_read_b128 v[16:19],v0 offset:24576\n"
"ds_read_b128 v[20:23],v1 offset:24576\n"
MF "a[216:219],v[24:27],v[88:91],a[216:219]\n"
"s_cmp_eq_u32 s72,s17\n"
"s_cselect_b32 s72,s16,s72\n"
"s_add_u32 m0,s64,0\n"
MF "a[236:239],v[28:31],v[88:91],a[236:239]\n"
LD "v7,s[32:35],s72 offen nt lds\n"
MF "a[232:235],v[28:31],v[84:87],a[232:235]\n"
"s_add_u32 m0,s64,896\n"
CV "v102,v120,v121\n"
"v_add_f32 v59,v59,v122\n"
MF "a[212:215],v[24:27],v[84:87],a[212:215]\n"
LD "v7,s[32:35],s72 offen offset:128 nt lds\n"
MF "a[208:211],v[24:27],v[80:83],a[208:211]\n"
"s_add_u32 m0,s64,1792\n"
"v_add_f32 v59,v59,v123\n"
CV "v103,v122,v123\n"
MF "a[228:231],v[28:31],v[80:83],a[228:231]\n"
LD "v7,s[32:35],s72 offen offset:256 nt lds\n"
MF "a[224:227],v[28:31],v[76:79],a[224:227]\n"
MF "a[204:207],v[24:27],v[76:79],a[204:207]\n"
"s_waitcnt lgkmcnt(0)\n"
MF "a[200:203],v[24:27],v[72:75],a[200:203]\n"
"s_cmp_lg_u32 s61,0\n"
MF "a[220:223],v[28:31],v[72:75],a[220:223]\n"
"s_cbranch_scc1 .Laloop\n"
".Lepi2:\n"
"v_exp_f32 v124,v124\n"
"v_exp_f32 v125,v125\n"
"v_exp_f32 v126,v126\n"
"v_exp_f32 v127,v127\n"
"v_exp_f32 v128,v128\n"
"v_exp_f32 v129,v129\n"
"v_exp_f32 v130,v130\n"
"v_exp_f32 v131,v131\n"
"v_add_f32 v60,v60,v124\n"
"v_add_f32 v61,v61,v128\n"
"v_add_f32 v60,v60,v125\n"
"v_add_f32 v61,v61,v129\n"
"v_add_f32 v60,v60,v126\n"
"v_add_f32 v61,v61,v130\n"
"v_add_f32 v60,v60,v127\n"
"v_add_f32_e64 v61,v61,v131\n"
CV "v106,v124,v125\n"
CV "v107,v126,v127\n"
CV "v110,v128,v129\n"
CV "v111,v130,v131\n"
"v_max3_f32 v10,v57,v58,v59\n"
"v_max3_f32 v10,v10,v60,v61\n"
"v_cmp_le_f32 vcc,0x47800000,v10\n"
"s_cbranch_vccnz .Lstub_17\n"
".Lback_17:\n"
"s_waitcnt vmcnt(3) lgkmcnt(0)\n"
"s_barrier\n"
"s_and_b32 s58,s56,1\n"
"s_mul_i32 s79,s58,12288\n"
"s_lshr_b32 s58,s56,1\n"
"s_mul_i32 s72,s58,61440\n"
"s_mul_i32 s73,s58,6144\n"
"s_add_u32 s73,s73,s79\n"
"v_lshrrev_b32 v192,7,v9\n"
"v_mul_u32_u24 v192,896,v192\n"
"v_add_u32 v192,v192,v8\n"
"s_add_u32 s88,s88,1024\n"
"s_add_u32 s89,s89,1024\n"
"s_add_u32 s72,s72,122880\n"
"s_lshl_b32 s58,s56,10\n"
"s_add_u32 s58,s58,73728\n"
"v_add_u32 v236,s58,v9\n"
"v_add_u32 v237,65536,v236\n"
"v_mov_b32 v10,1.0\n"
TR "v[160:161],v4 offset:50176\n"
TR "v[162:163],v4 offset:56320\n"
"v_mfma_f32_16x16x4_f32 v[16:19],v10,v57,0\n"
TR "v[164:165],v5 offset:50176\n"
TR "v[166:167],v5 offset:56320\n"
TR "v[168:169],v2 offset:51200\n"
TR "v[170:171],v2 offset:57344\n"
TR "v[172:173],v3 offset:51200\n"
TR "v[174:175],v3 offset:57344\n"
"v_mfma_f32_16x16x4_f32 v[20:23],v10,v58,0\n"
TR "v[176:177],v4 offset:51200\n"
TR "v[178:179],v4 offset:57344\n"
TR "v[180:181],v5 offset:51200\n"
TR "v[182:183],v5 offset:57344\n"
TR "v[136:137],v2 offset:49152\n"
TR "v[138:139],v2 offset:55296\n"
"v_mfma_f32_16x16x4_f32 v[24:27],v10,v59,0\n"
TR "v[140:141],v3 offset:49152\n"
TR "v[142:143],v3 offset:55296\n"
TR "v[144:145],v4 offset:49152\n"
TR "v[146:147],v4 offset:55296\n"
TR "v[148:149],v5 offset:49152\n"
TR "v[150:151],v5 offset:55296\n"
"v_mfma_f32_16x16x4_f32 v[28:31],v10,v60,0\n"
TR "v[152:153],v2 offset:50176\n"
TR "v[154:155],v2 offset:56320\n"
TR "v[156:157],v3 offset:50176\n"
TR "v[158:159],v3 offset:56320\n"
"v_mfma_f32_16x16x4_f32 v[32:35],v10,v61,0\n"
"s_add_u32 m0,s73,24576\n"
"s_add_u32 s80,s89,s72\n"
LD "v192,s[48:51],s80 offen lds\n"
"v_add_u32 v193,9216,v192\n"
LD "v193,s[48:51],s80 offen offset:1024 lds\n"
"s_waitcnt lgkmcnt(15)\n"
MF "a[120:123],v[160:163],v[92:95],a[120:123]\n"
MF "a[140:143],v[164:167],v[92:95],a[140:143]\n"
"v_add_u32 v194,18432,v192\n"
LD "v194,s[48:51],s80 offen offset:2048 lds\n"
"v_add_u32 v195,27648,v192\n"
LD "v195,s[48:51],s80 offen offset:3072 lds\n"
"v_rcp_f32 v208,v16\n"
"v_rcp_f32 v209,v20\n"
"v_rcp_f32 v210,v24\n"
"v_rcp_f32 v211,v28\n"
"v_rcp_f32 v212,v32\n"
MF "a[124:127],v[160:163],v[96:99],a[124:127]\n"
"v_mul_f32_e64 v214,-s13,v208\n"
"v_mul_f32_e64 v215,-s13,v209\n"
"v_mul_f32_e64 v216,-s13,v210\n"
"v_mul_f32_e64 v217,-s13,v211\n"
"v_mul_f32_e64 v218,-s13,v212\n"
"s_add_u32 m0,s73,28672\n"
"v_add_u32 v196,40960,v192\n"
LD "v196,s[48:51],s80 offen lds\n"
AR "v72,a120\n"
AR "v73,a121\n"
AR "v74,a122\n"
MF "a[144:147],v[164:167],v[96:99],a[144:147]\n"
AR "v75,a123\n"
AR "v76,a140\n"
AR "v77,a141\n"
AR "v78,a142\n"
AR "v79,a143\n"
"v_pk_fma_f32 v[72:73],v[72:73],v[208:209],v[214:215] op_sel:[0,0,0] op_sel_hi:[1,0,0]\n"
"v_pk_fma_f32 v[74:75],v[74:75],v[208:209],v[214:215] op_sel:[0,0,0] op_sel_hi:[1,0,0]\n"
"v_pk_fma_f32 v[76:77],v[76:77],v[208:209],v[214:215] op_sel:[0,0,0] op_sel_hi:[1,0,0]\n"
"v_pk_fma_f32 v[78:79],v[78:79],v[208:209],v[214:215] op_sel:[0,0,0] op_sel_hi:[1,0,0]\n"
CV "v112,v72,v73\n"
CV "v113,v74,v75\n"
CV "v114,v76,v77\n"
CV "v115,v78,v79\n"
MF "a[128:131],v[160:163],v[100:103],a[128:131]\n"
"ds_read_b128 a[140:143],v237 offset:20480\n"
AW "a120,v112\n"
AW "a121,v113\n"
AW "a122,v114\n"
AW "a123,v115\n"
"s_waitcnt lgkmcnt(7)\n"
MF "a[44:47],v[144:147],v[96:99],a[44:47]\n"
"v_add_u32 v197,50176,v192\n"
LD "v197,s[48:51],s80 offen offset:1024 lds\n"
AR "v80,a124\n"
AR "v81,a125\n"
AR "v82,a126\n"
MF "a[148:151],v[164:167],v[100:103],a[148:151]\n"
AR "v83,a127\n"
AR "v84,a144\n"
AR "v85,a145\n"
"s_nop 0\n"
AR "v86,a146\n"
AR "v87,a147\n"
"v_pk_fma_f32 v[80:81],v[80:81],v[208:209],v[214:215] op_sel:[0,1,1] op_sel_hi:[1,1,1]\n"
"v_pk_fma_f32 v[82:83],v[82:83],v[208:209],v[214:215] op_sel:[0,1,1] op_sel_hi:[1,1,1]\n"
"v_pk_fma_f32 v[84:85],v[84:85],v[208:209],v[214:215] op_sel:[0,1,1] op_sel_hi:[1,1,1]\n"
"v_pk_fma_f32 v[86:87],v[86:87],v[208:209],v[214:215] op_sel:[0,1,1] op_sel_hi:[1,1,1]\n"
CV "v116,v80,v81\n"
CV "v117,v82,v83\n"
CV "v118,v84,v85\n"
CV "v119,v86,v87\n"
MF "a[132:135],v[160:163],v[104:107],a[132:135]\n"
"ds_read_b128 a[144:147],v236\n"
AW "a124,v116\n"
AW "a125,v117\n"
AW "a126,v118\n"
AW "a127,v119\n"
"s_waitcnt lgkmcnt(6)\n"
MF "a[64:67],v[148:151],v[96:99],a[64:67]\n"
"s_add_u32 m0,s73,0\n"
"s_add_u32 s80,s88,s72\n"
"s_sub_u32 s72,s72,122880\n"
"s_nop 0\n"
LD "v192,s[48:51],s80 offen lds\n"
AR "v72,a128\n"
AR "v73,a129\n"
AR "v74,a130\n"
MF "a[152:155],v[164:167],v[104:107],a[152:155]\n"
AR "v75,a131\n"
AR "v76,a148\n"
AR "v77,a149\n"
AR "v78,a150\n"
AR "v79,a151\n"
"v_pk_fma_f32 v[72:73],v[72:73],v[210:211],v[216:217] op_sel:[0,0,0] op_sel_hi:[1,0,0]\n"
"v_pk_fma_f32 v[74:75],v[74:75],v[210:211],v[216:217] op_sel:[0,0,0] op_sel_hi:[1,0,0]\n"
"v_pk_fma_f32 v[76:77],v[76:77],v[210:211],v[216:217] op_sel:[0,0,0] op_sel_hi:[1,0,0]\n"
"v_pk_fma_f32 v[78:79],v[78:79],v[210:211],v[216:217] op_sel:[0,0,0] op_sel_hi:[1,0,0]\n"
LD "v193,s[48:51],s80 offen offset:1024 lds\n"
CV "v112,v72,v73\n"
CV "v113,v74,v75\n"
CV "v114,v76,v77\n"
CV "v115,v78,v79\n"
MF "a[136:139],v[160:163],v[108:111],a[136:139]\n"
"ds_read_b128 a[148:151],v236 offset:4096\n"
AW "a128,v112\n"
AW "a129,v113\n"
AW "a130,v114\n"
AW "a131,v115\n"
"s_waitcnt lgkmcnt(5)\n"
MF "a[84:87],v[152:155],v[96:99],a[84:87]\n"
LD "v194,s[48:51],s80 offen offset:2048 lds\n"
AR "v80,a132\n"
AR "v81,a133\n"
AR "v82,a134\n"
MF "a[156:159],v[164:167],v[108:111],a[156:159]\n"
AR "v83,a135\n"
AR "v84,a152\n"
AR "v85,a153\n"
"s_nop 0\n"
AR "v86,a154\n"
AR "v87,a155\n"
"v_pk_fma_f32 v[80:81],v[80:81],v[210:211],v[216:217] op_sel:[0,1,1] op_sel_hi:[1,1,1]\n"
"v_pk_fma_f32 v[82:83],v[82:83],v[210:211],v[216:217] op_sel:[0,1,1] op_sel_hi:[1,1,1]\n"
"v_pk_fma_f32 v[84:85],v[84:85],v[210:211],v[216:217] op_sel:[0,1,1] op_sel_hi:[1,1,1]\n"
"v_pk_fma_f32 v[86:87],v[86:87],v[210:211],v[216:217] op_sel:[0,1,1] op_sel_hi:[1,1,1]\n"
CV "v116,v80,v81\n"
CV "v117,v82,v83\n"
CV "v118,v84,v85\n"
CV "v119,v86,v87\n"
MF "a[160:163],v[168:171],v[92:95],a[160:163]\n"
"ds_read_b128 a[152:155],v236 offset:8192\n"
AW "a132,v116\n"
AW "a133,v117\n"
AW "a134,v118\n"
AW "a135,v119\n"
"s_waitcnt lgkmcnt(4)\n"
MF "a[104:107],v[156:159],v[96:99],a[104:107]\n"
LD "v195,s[48:51],s80 offen offset:3072 lds\n"
AR "v72,a136\n"
AR "v73,a137\n"
AR "v74,a138\n"
MF "a[180:183],v[172:175],v[92:95],a[180:183]\n"
AR "v75,a139\n"
AR "v76,a156\n"
AR "v77,a157\n"
"s_nop 0\n"
AR "v78,a158\n"
AR "v79,a159\n"
"v_pk_fma_f32 v[72:73],v[72:73],v[212:213],v[218:219] op_sel:[0,0,0] op_sel_hi:[1,0,0]\n"
"v_pk_fma_f32 v[74:75],v[74:75],v[212:213],v[218:219] op_sel:[0,0,0] op_sel_hi:[1,0,0]\n"
"v_pk_fma_f32 v[76:77],v[76:77],v[212:213],v[218:219] op_sel:[0,0,0] op_sel_hi:[1,0,0]\n"
"v_pk_fma_f32 v[78:79],v[78:79],v[212:213],v[218:219] op_sel:[0,0,0] op_sel_hi:[1,0,0]\n"
CV "v112,v72,v73\n"
CV "v113,v74,v75\n"
CV "v114,v76,v77\n"
CV "v115,v78,v79\n"
MF "a[164:167],v[168:171],v[96:99],a[164:167]\n"
"ds_read_b128 a[156:159],v236 offset:12288\n"
AW "a136,v112\n"
AW "a137,v113\n"
AW "a138,v114\n"
AW "a139,v115\n"
"v_max3_f32 v10,|v72|,|v73|,|v74|\n"
"s_lshl_b32 s58,s56,10\n"
"s_add_u32 s58,s58,159744\n"
"v_max3_f32 v10,v10,|v75|,|v76|\n"
"v_max3_f32 v10,v10,|v77|,|v78|\n"
"v_max3_f32 v10,v10,|v78|,|v79|\n"
"v_mul_f32 v10,s10,v10\n"
"v_lshrrev_b32_e64 v11,2,v9\n"
"v_readlane_b32 s12,v10,3\n"
"v_readlane_b32 s6,v10,22\n"
"v_readlane_b32 s7,v10,41\n"
"v_readlane_b32 s8,v10,60\n"
"v_add_u32 v11,s58,v11\n"
"s_min_u32 s12,s12,s6\n"
"s_min_u32 s7,s7,s8\n"
"s_min_u32 s12,s12,s7\n"
"s_and_b32 s12,s12,0x7f800000\n"
"s_sub_u32 s12,s12,0x14000000\n"
"s_cmp_lt_u32 s12,0x50000000\n"
"s_cselect_b32 s12,s12,0xe7400000\n"
"s_add_u32 s12,s12,0x18c00000\n"
"v_mov_b32 v10,s12\n"
"v_or_b32 v12,0x80000000,v10\n"
"ds_write2st64_b32 v11,v10,v12 offset1:1\n"
MF "a[8:11],v[136:139],v[100:103],a[8:11]\n"
"s_add_u32 m0,s73,4096\n"
"s_nop 0\n"
LD "v196,s[48:51],s80 offen lds\n"
AR "v80,a160\n"
AR "v81,a161\n"
AR "v82,a162\n"
MF "a[184:187],v[172:175],v[96:99],a[184:187]\n"
AR "v83,a163\n"
AR "v84,a180\n"
AR "v85,a181\n"
AR "v86,a182\n"
AR "v87,a183\n"
"v_pk_fma_f32 v[80:81],v[80:81],v[208:209],v[214:215] op_sel:[0,0,0] op_sel_hi:[1,0,0]\n"
"v_pk_fma_f32 v[82:83],v[82:83],v[208:209],v[214:215] op_sel:[0,0,0] op_sel_hi:[1,0,0]\n"
"v_pk_fma_f32 v[84:85],v[84:85],v[208:209],v[214:215] op_sel:[0,0,0] op_sel_hi:[1,0,0]\n"
"v_pk_fma_f32 v[86:87],v[86:87],v[208:209],v[214:215] op_sel:[0,0,0] op_sel_hi:[1,0,0]\n"
LD "v197,s[48:51],s80 offen offset:1024 lds\n"
CV "v116,v80,v81\n"
CV "v117,v82,v83\n"
CV "v118,v84,v85\n"
CV "v119,v86,v87\n"
MF "a[168:171],v[168:171],v[100:103],a[168:171]\n"
"ds_read_b128 a[180:183],v236 offset:16384\n"
AW "a160,v116\n"
AW "a161,v117\n"
AW "a162,v118\n"
AW "a163,v119\n"
MF "a[28:31],v[140:143],v[100:103],a[28:31]\n"
AR "v72,a164\n"
AR "v73,a165\n"
AR "v74,a166\n"
MF "a[188:191],v[172:175],v[100:103],a[188:191]\n"
AR "v75,a167\n"
AR "v76,a184\n"
AR "v77,a185\n"
AR "v78,a186\n"
AR "v79,a187\n"
"v_pk_fma_f32 v[72:73],v[72:73],v[208:209],v[214:215] op_sel:[0,1,1] op_sel_hi:[1,1,1]\n"
"v_pk_fma_f32 v[74:75],v[74:75],v[208:209],v[214:215] op_sel:[0,1,1] op_sel_hi:[1,1,1]\n"
"v_pk_fma_f32 v[76:77],v[76:77],v[208:209],v[214:215] op_sel:[0,1,1] op_sel_hi:[1,1,1]\n"
"v_pk_fma_f32 v[78:79],v[78:79],v[208:209],v[214:215] op_sel:[0,1,1] op_sel_hi:[1,1,1]\n"
CV "v112,v72,v73\n"
CV "v113,v74,v75\n"
CV "v114,v76,v77\n"
CV "v115,v78,v79\n"
MF "a[172:175],v[168:171],v[104:107],a[172:175]\n"
"ds_read_b128 a[184:187],v236 offset:20480\n"
AW "a164,v112\n"
AW "a165,v113\n"
AW "a166,v114\n"
AW "a167,v115\n"
MF "a[48:51],v[144:147],v[100:103],a[48:51]\n"
"s_waitcnt lgkmcnt(0)\n"
"s_barrier\n"
"s_add_u32 m0,s73,49152\n"
"s_add_u32 s80,s89,s72\n"
"s_nop 0\n"
LD "v192,s[48:51],s80 offen lds\n"
AR "v80,a168\n"
AR "v81,a169\n"
AR "v82,a170\n"
MF "a[192:195],v[172:175],v[104:107],a[192:195]\n"
AR "v83,a171\n"
AR "v84,a188\n"
AR "v85,a189\n"
AR "v86,a190\n"
AR "v87,a191\n"
"v_pk_fma_f32 v[80:81],v[80:81],v[210:211],v[216:217] op_sel:[0,0,0] op_sel_hi:[1,0,0]\n"
"v_pk_fma_f32 v[82:83],v[82:83],v[210:211],v[216:217] op_sel:[0,0,0] op_sel_hi:[1,0,0]\n"
"v_pk_fma_f32 v[84:85],v[84:85],v[210:211],v[216:217] op_sel:[0,0,0] op_sel_hi:[1,0,0]\n"
"v_pk_fma_f32 v[86:87],v[86:87],v[210:211],v[216:217] op_sel:[0,0,0] op_sel_hi:[1,0,0]\n"
LD "v193,s[48:51],s80 offen offset:1024 lds\n"
CV "v116,v80,v81\n"
CV "v117,v82,v83\n"
CV "v118,v84,v85\n"
CV "v119,v86,v87\n"
MF "a[176:179],v[168:171],v[108:111],a[176:179]\n"
"v_accvgpr_mov_b32 a188,a240\n"
"v_accvgpr_mov_b32 a189,a241\n"
"v_accvgpr_mov_b32 a190,a242\n"
"v_accvgpr_mov_b32 a191,a243\n"
AW "a168,v116\n"
AW "a169,v117\n"
AW "a170,v118\n"
AW "a171,v119\n"
MF "a[68:71],v[148:151],v[100:103],a[68:71]\n"
LD "v194,s[48:51],s80 offen offset:2048 lds\n"
AR "v72,a172\n"
AR "v73,a173\n"
AR "v74,a174\n"
MF "a[196:199],v[172:175],v[108:111],a[196:199]\n"
AR "v75,a175\n"
AR "v76,a192\n"
AR "v77,a193\n"
AR "v78,a194\n"
AR "v79,a195\n"
"v_pk_fma_f32 v[72:73],v[72:73],v[210:211],v[216:217] op_sel:[0,1,1] op_sel_hi:[1,1,1]\n"
"v_pk_fma_f32 v[74:75],v[74:75],v[210:211],v[216:217] op_sel:[0,1,1] op_sel_hi:[1,1,1]\n"
"v_pk_fma_f32 v[76:77],v[76:77],v[210:211],v[216:217] op_sel:[0,1,1] op_sel_hi:[1,1,1]\n"
"v_pk_fma_f32 v[78:79],v[78:79],v[210:211],v[216:217] op_sel:[0,1,1] op_sel_hi:[1,1,1]\n"
LD "v195,s[48:51],s80 offen offset:3072 lds\n"
CV "v112,v72,v73\n"
CV "v113,v74,v75\n"
CV "v114,v76,v77\n"
CV "v115,v78,v79\n"
MF "a[200:203],v[176:179],v[92:95],a[200:203]\n"
"v_accvgpr_mov_b32 a192,a244\n"
"v_accvgpr_mov_b32 a193,a245\n"
"v_accvgpr_mov_b32 a194,a246\n"
"v_accvgpr_mov_b32 a195,a247\n"
AW "a172,v112\n"
AW "a173,v113\n"
AW "a174,v114\n"
AW "a175,v115\n"
MF "a[88:91],v[152:155],v[100:103],a[88:91]\n"
"s_add_u32 m0,s73,53248\n"
"s_nop 0\n"
LD "v196,s[48:51],s80 offen lds\n"
AR "v80,a176\n"
AR "v81,a177\n"
AR "v82,a178\n"
MF "a[220:223],v[180:183],v[92:95],a[220:223]\n"
".Lpg19h:\n"
"s_cbranch_execz .Lpg23h\n"
AR "v83,a179\n"
AR "v84,a196\n"
AR "v85,a197\n"
AR "v86,a198\n"
AR "v87,a199\n"
"v_pk_fma_f32 v[80:81],v[80:81],v[212:213],v[218:219] op_sel:[0,0,0] op_sel_hi:[1,0,0]\n"
"v_pk_fma_f32 v[82:83],v[82:83],v[212:213],v[218:219] op_sel:[0,0,0] op_sel_hi:[1,0,0]\n"
"v_pk_fma_f32 v[84:85],v[84:85],v[212:213],v[218:219] op_sel:[0,0,0] op_sel_hi:[1,0,0]\n"
"v_pk_fma_f32 v[86:87],v[86:87],v[212:213],v[218:219] op_sel:[0,0,0] op_sel_hi:[1,0,0]\n"
CV "v116,v80,v81\n"
CV "v117,v82,v83\n"
CV "v118,v84,v85\n"
CV "v119,v86,v87\n"
MF "a[204:207],v[176:179],v[96:99],a[204:207]\n"
"v_accvgpr_mov_b32 a196,a248\n"
"v_accvgpr_mov_b32 a197,a249\n"
"v_accvgpr_mov_b32 a198,a250\n"
"v_accvgpr_mov_b32 a199,a251\n"
AW "a176,v116\n"
AW "a177,v117\n"
AW "a178,v118\n"
AW "a179,v119\n"
MF "a[108:111],v[156:159],v[100:103],a[108:111]\n"
LD "v197,s[48:51],s80 offen offset:1024 lds\n"
AR "v72,a200\n"
AR "v73,a201\n"
AR "v74,a202\n"
MF "a[224:227],v[180:183],v[96:99],a[224:227]\n"
AR "v75,a203\n"
AR "v76,a220\n"
AR "v77,a221\n"
AR "v78,a222\n"
AR "v79,a223\n"
"v_pk_fma_f32 v[72:73],v[72:73],v[208:209],v[214:215] op_sel:[0,0,0] op_sel_hi:[1,0,0]\n"
"v_pk_fma_f32 v[74:75],v[74:75],v[208:209],v[214:215] op_sel:[0,0,0] op_sel_hi:[1,0,0]\n"
"v_pk_fma_f32 v[76:77],v[76:77],v[208:209],v[214:215] op_sel:[0,0,0] op_sel_hi:[1,0,0]\n"
"v_pk_fma_f32 v[78:79],v[78:79],v[208:209],v[214:215] op_sel:[0,0,0] op_sel_hi:[1,0,0]\n"
CV "v112,v72,v73\n"
CV "v113,v74,v75\n"
CV "v114,v76,v77\n"
CV "v115,v78,v79\n"
MF "a[208:211],v[176:179],v[100:103],a[208:211]\n"
AW "a220,v62\n"
AW "a221,v63\n"
AW "a222,v64\n"
AW "a223,v65\n"
AW "a200,v112\n"
AW "a201,v113\n"
AW "a202,v114\n"
AW "a203,v115\n"
MF "a[12:15],v[136:139],v[104:107],a[12:15]\n"
"s_add_u32 m0,s73,73728\n"
"s_add_u32 s80,s88,s72\n"
LD "v192,s[48:51],s80 offen lds\n"
AR "v80,a204\n"
AR "v81,a205\n"
AR "v82,a206\n"
MF "a[228:231],v[180:183],v[100:103],a[228:231]\n"
AR "v83,a207\n"
AR "v84,a224\n"
AR "v85,a225\n"
"s_nop 0\n"
AR "v86,a226\n"
AR "v87,a227\n"
"v_pk_fma_f32 v[80:81],v[80:81],v[208:209],v[214:215] op_sel:[0,1,1] op_sel_hi:[1,1,1]\n"
"v_pk_fma_f32 v[82:83],v[82:83],v[208:209],v[214:215] op_sel:[0,1,1] op_sel_hi:[1,1,1]\n"
"v_pk_fma_f32 v[84:85],v[84:85],v[208:209],v[214:215] op_sel:[0,1,1] op_sel_hi:[1,1,1]\n"
"v_pk_fma_f32 v[86:87],v[86:87],v[208:209],v[214:215] op_sel:[0,1,1] op_sel_hi:[1,1,1]\n"
LD "v193,s[48:51],s80 offen offset:1024 lds\n"
CV "v116,v80,v81\n"
CV "v117,v82,v83\n"
CV "v118,v84,v85\n"
CV "v119,v86,v87\n"
MF "a[212:215],v[176:179],v[104:107],a[212:215]\n"
AW "a224,v66\n"
AW "a225,v67\n"
AW "a226,v68\n"
AW "a227,v69\n"
AW "a204,v116\n"
AW "a205,v117\n"
AW "a206,v118\n"
AW "a207,v119\n"
MF "a[32:35],v[140:143],v[104:107],a[32:35]\n"
LD "v194,s[48:51],s80 offen offset:2048 lds\n"
AR "v72,a208\n"
AR "v73,a209\n"
AR "v74,a210\n"
MF "a[232:235],v[180:183],v[104:107],a[232:235]\n"
AR "v75,a211\n"
AR "v76,a228\n"
AR "v77,a229\n"
AR "v78,a230\n"
AR "v79,a231\n"
"v_pk_fma_f32 v[72:73],v[72:73],v[210:211],v[216:217] op_sel:[0,0,0] op_sel_hi:[1,0,0]\n"
"v_pk_fma_f32 v[74:75],v[74:75],v[210:211],v[216:217] op_sel:[0,0,0] op_sel_hi:[1,0,0]\n"
"v_pk_fma_f32 v[76:77],v[76:77],v[210:211],v[216:217] op_sel:[0,0,0] op_sel_hi:[1,0,0]\n"
"v_pk_fma_f32 v[78:79],v[78:79],v[210:211],v[216:217] op_sel:[0,0,0] op_sel_hi:[1,0,0]\n"
CV "v112,v72,v73\n"
CV "v113,v74,v75\n"
CV "v114,v76,v77\n"
CV "v115,v78,v79\n"
MF "a[216:219],v[176:179],v[108:111],a[216:219]\n"
AW "a228,v70\n"
AW "a229,v71\n"
AW "a230,v132\n"
AW "a231,v133\n"
AW "a208,v112\n"
AW "a209,v113\n"
AW "a210,v114\n"
AW "a211,v115\n"
MF "a[52:55],v[144:147],v[104:107],a[52:55]\n"
LD "v195,s[48:51],s80 offen offset:3072 lds\n"
AR "v80,a212\n"
AR "v81,a213\n"
AR "v82,a214\n"
MF "a[236:239],v[180:183],v[108:111],a[236:239]\n"
AR "v83,a215\n"
AR "v84,a232\n"
AR "v85,a233\n"
AR "v86,a234\n"
AR "v87,a235\n"
"v_pk_fma_f32 v[80:81],v[80:81],v[210:211],v[216:217] op_sel:[0,1,1] op_sel_hi:[1,1,1]\n"
"v_pk_fma_f32 v[82:83],v[82:83],v[210:211],v[216:217] op_sel:[0,1,1] op_sel_hi:[1,1,1]\n"
"v_pk_fma_f32 v[84:85],v[84:85],v[210:211],v[216:217] op_sel:[0,1,1] op_sel_hi:[1,1,1]\n"
"v_pk_fma_f32 v[86:87],v[86:87],v[210:211],v[216:217] op_sel:[0,1,1] op_sel_hi:[1,1,1]\n"
CV "v116,v80,v81\n"
CV "v117,v82,v83\n"
CV "v118,v84,v85\n"
CV "v119,v86,v87\n"
MF "a[0:3],v[136:139],v[92:95],a[0:3]\n"
AW "a232,v134\n"
AW "a233,v135\n"
AW "a234,v6\n"
AW "a235,v13\n"
AW "a212,v116\n"
AW "a213,v117\n"
AW "a214,v118\n"
AW "a215,v119\n"
MF "a[72:75],v[148:151],v[104:107],a[72:75]\n"
"s_add_u32 m0,s73,77824\n"
"s_nop 0\n"
LD "v196,s[48:51],s80 offen lds\n"
AR "v72,a216\n"
AR "v73,a217\n"
AR "v74,a218\n"
MF "a[20:23],v[140:143],v[92:95],a[20:23]\n"
AR "v75,a219\n"
AR "v76,a236\n"
AR "v77,a237\n"
"s_nop 0\n"
AR "v78,a238\n"
AR "v79,a239\n"
"v_pk_fma_f32 v[72:73],v[72:73],v[212:213],v[218:219] op_sel:[0,0,0] op_sel_hi:[1,0,0]\n"
"v_pk_fma_f32 v[74:75],v[74:75],v[212:213],v[218:219] op_sel:[0,0,0] op_sel_hi:[1,0,0]\n"
"v_pk_fma_f32 v[76:77],v[76:77],v[212:213],v[218:219] op_sel:[0,0,0] op_sel_hi:[1,0,0]\n"
"v_pk_fma_f32 v[78:79],v[78:79],v[212:213],v[218:219] op_sel:[0,0,0] op_sel_hi:[1,0,0]\n"
LD "v197,s[48:51],s80 offen offset:1024 lds\n"
CV "v112,v72,v73\n"
CV "v113,v74,v75\n"
CV "v114,v76,v77\n"
CV "v115,v78,v79\n"
MF "a[40:43],v[144:147],v[92:95],a[40:43]\n"
AW "a236,v14\n"
AW "a237,v15\n"
"v_accvgpr_mov_b32 a238,a254\n"
"v_accvgpr_mov_b32 a239,a255\n"
AW "a216,v112\n"
AW "a217,v113\n"
AW "a218,v114\n"
AW "a219,v115\n"
MF "a[92:95],v[152:155],v[104:107],a[92:95]\n"
AR "v80,a0\n"
AR "v81,a1\n"
AR "v82,a2\n"
MF "a[60:63],v[148:151],v[92:95],a[60:63]\n"
AR "v83,a3\n"
AR "v84,a20\n"
AR "v85,a21\n"
AR "v86,a22\n"
AR "v87,a23\n"
"v_pk_fma_f32 v[80:81],v[80:81],v[208:209],v[214:215] op_sel:[0,0,0] op_sel_hi:[1,0,0]\n"
"v_pk_fma_f32 v[82:83],v[82:83],v[208:209],v[214:215] op_sel:[0,0,0] op_sel_hi:[1,0,0]\n"
"v_pk_fma_f32 v[84:85],v[84:85],v[208:209],v[214:215] op_sel:[0,0,0] op_sel_hi:[1,0,0]\n"
"v_pk_fma_f32 v[86:87],v[86:87],v[208:209],v[214:215] op_sel:[0,0,0] op_sel_hi:[1,0,0]\n"
CV "v116,v80,v81\n"
CV "v117,v82,v83\n"
CV "v118,v84,v85\n"
CV "v119,v86,v87\n"
MF "a[80:83],v[152:155],v[92:95],a[80:83]\n"
"ds_read_b128 a[20:23],v236 offset:24576\n"
AW "a0,v116\n"
AW "a1,v117\n"
AW "a2,v118\n"
AW "a3,v119\n"
MF "a[112:115],v[156:159],v[104:107],a[112:115]\n"
AR "v72,a40\n"
AR "v73,a41\n"
AR "v74,a42\n"
MF "a[100:103],v[156:159],v[92:95],a[100:103]\n"
AR "v75,a43\n"
AR "v76,a60\n"
AR "v77,a61\n"
AR "v78,a62\n"
AR "v79,a63\n"
"v_pk_fma_f32 v[72:73],v[72:73],v[208:209],v[214:215] op_sel:[0,0,0] op_sel_hi:[1,0,0]\n"
"v_pk_fma_f32 v[74:75],v[74:75],v[208:209],v[214:215] op_sel:[0,0,0] op_sel_hi:[1,0,0]\n"
"v_pk_fma_f32 v[76:77],v[76:77],v[208:209],v[214:215] op_sel:[0,0,0] op_sel_hi:[1,0,0]\n"
"v_pk_fma_f32 v[78:79],v[78:79],v[208:209],v[214:215] op_sel:[0,0,0] op_sel_hi:[1,0,0]\n"
CV "v112,v72,v73\n"
CV "v113,v74,v75\n"
CV "v114,v76,v77\n"
CV "v115,v78,v79\n"
MF "a[4:7],v[136:139],v[96:99],a[4:7]\n"
"ds_read_b128 a[60:63],v236 offset:45056\n"
AW "a40,v112\n"
AW "a41,v113\n"
AW "a42,v114\n"
AW "a43,v115\n"
MF "a[16:19],v[136:139],v[108:111],a[16:19]\n"
AR "v80,a80\n"
AR "v81,a81\n"
AR "v82,a82\n"
MF "a[24:27],v[140:143],v[96:99],a[24:27]\n"
AR "v83,a83\n"
AR "v84,a100\n"
AR "v85,a101\n"
AR "v86,a102\n"
AR "v87,a103\n"
"v_pk_fma_f32 v[80:81],v[80:81],v[208:209],v[214:215] op_sel:[0,0,0] op_sel_hi:[1,0,0]\n"
"v_pk_fma_f32 v[82:83],v[82:83],v[208:209],v[214:215] op_sel:[0,0,0] op_sel_hi:[1,0,0]\n"
"v_pk_fma_f32 v[84:85],v[84:85],v[208:209],v[214:215] op_sel:[0,0,0] op_sel_hi:[1,0,0]\n"
"v_pk_fma_f32 v[86:87],v[86:87],v[208:209],v[214:215] op_sel:[0,0,0] op_sel_hi:[1,0,0]\n"
CV "v116,v80,v81\n"
CV "v117,v82,v83\n"
CV "v118,v84,v85\n"
CV "v119,v86,v87\n"
MF "a[36:39],v[140:143],v[108:111],a[36:39]\n"
"ds_read_b128 a[100:103],v237\n"
AW "a80,v116\n"
AW "a81,v117\n"
AW "a82,v118\n"
AW "a83,v119\n"
MF "a[56:59],v[144:147],v[108:111],a[56:59]\n"
AR "v72,a4\n"
AR "v73,a5\n"
AR "v74,a6\n"
MF "a[76:79],v[148:151],v[108:111],a[76:79]\n"
AR "v75,a7\n"
AR "v76,a24\n"
AR "v77,a25\n"
AR "v78,a26\n"
AR "v79,a27\n"
"v_pk_fma_f32 v[72:73],v[72:73],v[208:209],v[214:215] op_sel:[0,1,1] op_sel_hi:[1,1,1]\n"
"v_pk_fma_f32 v[74:75],v[74:75],v[208:209],v[214:215] op_sel:[0,1,1] op_sel_hi:[1,1,1]\n"
"v_pk_fma_f32 v[76:77],v[76:77],v[208:209],v[214:215] op_sel:[0,1,1] op_sel_hi:[1,1,1]\n"
"v_pk_fma_f32 v[78:79],v[78:79],v[208:209],v[214:215] op_sel:[0,1,1] op_sel_hi:[1,1,1]\n"
CV "v112,v72,v73\n"
CV "v113,v74,v75\n"
CV "v114,v76,v77\n"
CV "v115,v78,v79\n"
MF "a[96:99],v[152:155],v[108:111],a[96:99]\n"
"ds_read_b128 a[24:27],v236 offset:28672\n"
AW "a4,v112\n"
AW "a5,v113\n"
AW "a6,v114\n"
AW "a7,v115\n"
MF "a[116:119],v[156:159],v[108:111],a[116:119]\n"
"v_and_b32 v0,512,v9\n"
"v_lshlrev_b32 v0,2,v0\n"
"v_sub_u32 v0,v2,v0\n"
"s_bfe_u32 s58,s22,0x10014\n"
"s_lshl_b32 s58,s58,5\n"
"v_xor_b32 v0,s58,v0\n"
"s_and_b32 s58,s56,1\n"
"s_lshl_b32 s58,s58,10\n"
"s_lshr_b32 s59,s56,1\n"
"s_lshl_b32 s59,s59,8\n"
"s_add_u32 s58,s58,s59\n"
"s_add_u32 s58,s58,159744\n"
"v_mov_b32 v252,s58\n"
"v_xor_b32 v1,32,v0\n"
"v_xor_b32 v2,64,v0\n"
"v_xor_b32 v3,96,v0\n"
"v_add_u32 v4,73728,v0\n"
"v_add_u32 v5,73728,v1\n"
"v_add_u32 v6,73728,v2\n"
"v_add_u32 v7,73728,v3\n"
"s_waitcnt vmcnt(12) lgkmcnt(0)\n"
"s_barrier\n"
"ds_read_b128 v[252:255],v252\n"
TR "v[176:177],v0 offset:24576\n"
TR "v[178:179],v0 offset:26624\n"
TR "v[180:181],v0 offset:28672\n"
TR "v[182:183],v0 offset:30720\n"
TR "v[184:185],v0 offset:32768\n"
TR "v[186:187],v0 offset:34816\n"
"s_waitcnt lgkmcnt(0)\n"
"s_cmp_ge_u32 s56,2\n"
"s_cbranch_scc1 .Lrole1\n"
MF "v[16:19],a[120:123],v[176:179],v[252:255]\n"
"s_mov_b32 s65,128\n"
TR "v[188:189],v1 offset:24576\n"
AR "v220,a44\n"
MF "v[16:19],a[160:163],v[180:183],v[16:19]\n"
"s_mov_b32 s9,0x3f80\n"
TR "v[190:191],v1 offset:26624\n"
AR "v221,a45\n"
MF "v[16:19],a[200:203],v[184:187],v[16:19]\n"
"s_add_u32 s89,s89,s65\n"
AR "v222,a46\n"
AR "v223,a47\n"
MF "v[24:27],a[204:207],v[184:187],v[252:255]\n"
"s_bfe_u32 s58,s22,0x30014\n"
TR "a[240:241],v1 offset:28672\n"
AR "v224,a64\n"
MF "v[24:27],a[164:167],v[180:183],v[24:27]\n"
"s_add_u32 s61,s58,1\n"
TR "a[242:243],v1 offset:30720\n"
AR "v225,a65\n"
MF "v[24:27],a[124:127],v[176:179],v[24:27]\n"
"s_and_b32 s61,s61,7\n"
AR "v226,a66\n"
AR "v227,a67\n"
MF "v[32:35],a[128:131],v[176:179],v[252:255]\n"
"s_mov_b32 s64,9\n"
TR "a[244:245],v1 offset:32768\n"
"ds_read_b128 a[64:67],v236 offset:49152\n"
MF "v[32:35],a[168:171],v[180:183],v[32:35]\n"
"s_xor_b32 s67,s78,8\n"
TR "a[246:247],v1 offset:34816\n"
"v_fma_f32 v220,v220,v209,v215\n"
MF "v[32:35],a[208:211],v[184:187],v[32:35]\n"
"s_mov_b32 s71,204800\n"
"v_fma_f32 v221,v221,v209,v215\n"
"v_fma_f32 v222,v222,v209,v215\n"
MF "v[40:43],a[212:215],v[184:187],v[252:255]\n"
"s_mov_b32 s66,204800\n"
"v_fma_f32 v223,v223,v209,v215\n"
"v_fma_f32 v224,v224,v209,v215\n"
MF "v[40:43],a[172:175],v[180:183],v[40:43]\n"
"s_and_b32 s73,s56,1\n"
"v_fma_f32 v225,v225,v209,v215\n"
"v_fma_f32 v226,v226,v209,v215\n"
MF "v[40:43],a[132:135],v[176:179],v[40:43]\n"
"s_mul_i32 s73,s73,5120\n"
"v_fma_f32 v227,v227,v209,v215\n"
CV "v220,v220,v221\n"
MF "v[48:51],a[136:139],v[176:179],v[252:255]\n"
"s_add_u32 s73,s73,s93\n"
CV "v221,v222,v223\n"
CV "v222,v224,v225\n"
MF "v[48:51],a[176:179],v[180:183],v[48:51]\n"
"s_xor_b32 s58,s58,4\n"
CV "v223,v226,v227\n"
AW "a44,v220\n"
MF "v[48:51],a[216:219],v[184:187],v[48:51]\n"
"s_waitcnt lgkmcnt(0)\n"
AW "a45,v221\n"
AW "a46,v222\n"
MF "v[52:55],a[216:219],a[244:247],v[252:255]\n"
"s_mul_i32 s70,s58,204800\n"
TR "v[176:177],v2 offset:24576\n"
AW "a47,v223\n"
MF "v[52:55],a[176:179],a[240:243],v[52:55]\n"
"s_add_u32 s70,s70,s73\n"
TR "v[178:179],v2 offset:26624\n"
AR "v228,a84\n"
MF "v[52:55],a[136:139],v[188:191],v[52:55]\n"
"s_sub_u32 s70,s70,40960\n"
AR "v229,a85\n"
AR "v230,a86\n"
MF "v[44:47],a[132:135],v[188:191],v[252:255]\n"
"s_mov_b32 s85,1\n"
TR "v[180:181],v2 offset:28672\n"
AR "v231,a87\n"
MF "v[44:47],a[172:175],a[240:243],v[44:47]\n"
"s_mov_b32 s83,0xf0000000\n"
TR "v[182:183],v2 offset:30720\n"
AR "v232,a104\n"
MF "v[44:47],a[212:215],a[244:247],v[44:47]\n"
"s_mov_b32 s82,0xf0000000\n"
AR "v233,a105\n"
AR "v234,a106\n"
MF "v[36:39],a[208:211],a[244:247],v[252:255]\n"
"s_add_u32 s58,s79,73728\n"
TR "v[184:185],v2 offset:32768\n"
AR "v235,a107\n"
MF "v[36:39],a[168:171],a[240:243],v[36:39]\n"
"s_nop 0\n"
TR "v[186:187],v2 offset:34816\n"
"ds_read_b128 a[104:107],v237 offset:4096\n"
MF "v[36:39],a[128:131],v[188:191],v[36:39]\n"
"v_fma_f32 v228,v228,v209,v215\n"
"v_fma_f32 v229,v229,v209,v215\n"
MF "v[28:31],a[124:127],v[188:191],v[252:255]\n"
"v_fma_f32 v230,v230,v209,v215\n"
"v_fma_f32 v231,v231,v209,v215\n"
MF "v[28:31],a[164:167],a[240:243],v[28:31]\n"
"v_fma_f32 v232,v232,v209,v215\n"
"v_fma_f32 v233,v233,v209,v215\n"
MF "v[28:31],a[204:207],a[244:247],v[28:31]\n"
"v_fma_f32 v234,v234,v209,v215\n"
"v_fma_f32 v235,v235,v209,v215\n"
MF "v[20:23],a[200:203],a[244:247],v[252:255]\n"
CV "v228,v228,v229\n"
CV "v229,v230,v231\n"
MF "v[20:23],a[160:163],a[240:243],v[20:23]\n"
CV "v230,v232,v233\n"
CV "v231,v234,v235\n"
MF "v[20:23],a[120:123],v[188:191],v[20:23]\n"
"s_waitcnt lgkmcnt(1)\n"
AW "a84,v228\n"
AW "a85,v229\n"
MF "v[56:59],a[120:123],v[176:179],v[252:255]\n"
TR "v[188:189],v3 offset:24576\n"
AW "a86,v230\n"
MF "v[56:59],a[160:163],v[180:183],v[56:59]\n"
TR "v[190:191],v3 offset:26624\n"
AW "a87,v231\n"
MF "v[56:59],a[200:203],v[184:187],v[56:59]\n"
AR "v220,a8\n"
AR "v221,a9\n"
MF "v[64:67],a[204:207],v[184:187],v[252:255]\n"
TR "a[240:241],v3 offset:28672\n"
AR "v222,a10\n"
MF "v[64:67],a[164:167],v[180:183],v[64:67]\n"
TR "a[242:243],v3 offset:30720\n"
AR "v223,a11\n"
MF "v[64:67],a[124:127],v[176:179],v[64:67]\n"
AR "v224,a28\n"
AR "v225,a29\n"
MF "v[72:75],a[128:131],v[176:179],v[252:255]\n"
TR "a[244:245],v3 offset:32768\n"
AR "v226,a30\n"
MF "v[72:75],a[168:171],v[180:183],v[72:75]\n"
TR "a[246:247],v3 offset:34816\n"
AR "v227,a31\n"
MF "v[72:75],a[208:211],v[184:187],v[72:75]\n"
"ds_read_b128 a[28:31],v236 offset:32768\n"
"v_fma_f32 v220,v220,v210,v216\n"
MF "v[80:83],a[212:215],v[184:187],v[252:255]\n"
"v_fma_f32 v221,v221,v210,v216\n"
"v_fma_f32 v222,v222,v210,v216\n"
MF "v[80:83],a[172:175],v[180:183],v[80:83]\n"
"v_fma_f32 v223,v223,v210,v216\n"
"v_fma_f32 v224,v224,v210,v216\n"
MF "v[80:83],a[132:135],v[176:179],v[80:83]\n"
"v_fma_f32 v225,v225,v210,v216\n"
"v_fma_f32 v226,v226,v210,v216\n"
MF "v[88:91],a[136:139],v[176:179],v[252:255]\n"
"v_fma_f32 v227,v227,v210,v216\n"
CV "v220,v220,v221\n"
MF "v[88:91],a[176:179],v[180:183],v[88:91]\n"
CV "v221,v222,v223\n"
CV "v222,v224,v225\n"
MF "v[88:91],a[216:219],v[184:187],v[88:91]\n"
"s_waitcnt vmcnt(0) lgkmcnt(0)\n"
"s_barrier\n"
MF "v[92:95],a[216:219],a[244:247],v[252:255]\n"
TR "v[176:177],v0\n"
CV "v223,v226,v227\n"
MF "v[92:95],a[176:179],a[240:243],v[92:95]\n"
TR "v[178:179],v0 offset:2048\n"
AW "a8,v220\n"
MF "v[92:95],a[136:139],v[188:191],v[92:95]\n"
TR "v[96:97],v0 offset:49152\n"
AW "a9,v221\n"
MF "v[84:87],a[132:135],v[188:191],v[252:255]\n"
TR "v[180:181],v0 offset:4096\n"
AW "a10,v222\n"
MF "v[84:87],a[172:175],a[240:243],v[84:87]\n"
TR "v[182:183],v0 offset:6144\n"
AW "a11,v223\n"
MF "v[84:87],a[212:215],a[244:247],v[84:87]\n"
TR "v[98:99],v0 offset:51200\n"
AR "v228,a48\n"
MF "v[76:79],a[208:211],a[244:247],v[252:255]\n"
TR "v[184:185],v0 offset:8192\n"
AR "v229,a49\n"
MF "v[76:79],a[168:171],a[240:243],v[76:79]\n"
TR "v[186:187],v0 offset:10240\n"
AR "v230,a50\n"
MF "v[76:79],a[128:131],v[188:191],v[76:79]\n"
TR "v[100:101],v0 offset:53248\n"
AR "v231,a51\n"
MF "v[68:71],a[124:127],v[188:191],v[252:255]\n"
TR "v[102:103],v0 offset:55296\n"
AR "v232,a68\n"
MF "v[68:71],a[164:167],a[240:243],v[68:71]\n"
TR "v[104:105],v0 offset:57344\n"
AR "v233,a69\n"
MF "v[68:71],a[204:207],a[244:247],v[68:71]\n"
TR "v[106:107],v0 offset:59392\n"
AR "v234,a70\n"
MF "v[60:63],a[200:203],a[244:247],v[252:255]\n"
TR "v[108:109],v4\n"
AR "v235,a71\n"
MF "v[60:63],a[160:163],a[240:243],v[60:63]\n"
"s_nop 0\n"
TR "v[110:111],v4 offset:2048\n"
"ds_read_b128 a[68:71],v236 offset:53248\n"
MF "v[60:63],a[120:123],v[188:191],v[60:63]\n"
"s_waitcnt lgkmcnt(7)\n"
TR "v[112:113],v4 offset:4096\n"
"v_fma_f32 v228,v228,v210,v216\n"
MF "v[16:19],a[140:143],v[176:179],v[16:19]\n"
TR "v[188:189],v1\n"
"v_fma_f32 v229,v229,v210,v216\n"
MF "v[16:19],a[180:183],v[180:183],v[16:19]\n"
TR "v[190:191],v1 offset:2048\n"
"v_fma_f32 v230,v230,v210,v216\n"
MF "v[16:19],a[220:223],v[184:187],v[16:19]\n"
TR "v[114:115],v4 offset:6144\n"
"v_fma_f32 v231,v231,v210,v216\n"
MF "v[24:27],a[224:227],v[184:187],v[24:27]\n"
TR "a[240:241],v1 offset:4096\n"
"v_fma_f32 v232,v232,v210,v216\n"
MF "v[24:27],a[184:187],v[180:183],v[24:27]\n"
TR "a[242:243],v1 offset:6144\n"
"v_fma_f32 v233,v233,v210,v216\n"
MF "v[24:27],a[144:147],v[176:179],v[24:27]\n"
TR "v[116:117],v4 offset:8192\n"
"v_fma_f32 v234,v234,v210,v216\n"
MF "v[32:35],a[148:151],v[176:179],v[32:35]\n"
TR "a[244:245],v1 offset:8192\n"
"v_fma_f32 v235,v235,v210,v216\n"
MF "v[32:35],a[188:191],v[180:183],v[32:35]\n"
TR "a[246:247],v1 offset:10240\n"
CV "v228,v228,v229\n"
MF "v[32:35],a[228:231],v[184:187],v[32:35]\n"
TR "v[118:119],v4 offset:10240\n"
CV "v229,v230,v231\n"
MF "v[40:43],a[232:235],v[184:187],v[40:43]\n"
".Lpg20h:\n"
"s_cbranch_execz .Lpg24h\n"
TR "v[120:121],v1 offset:49152\n"
CV "v230,v232,v233\n"
MF "v[40:43],a[192:195],v[180:183],v[40:43]\n"
TR "v[122:123],v1 offset:51200\n"
CV "v231,v234,v235\n"
MF "v[40:43],a[152:155],v[176:179],v[40:43]\n"
TR "v[124:125],v1 offset:53248\n"
AW "a48,v228\n"
MF "v[48:51],a[156:159],v[176:179],v[48:51]\n"
TR "v[126:127],v1 offset:55296\n"
AW "a49,v229\n"
MF "v[48:51],a[196:199],v[180:183],v[48:51]\n"
TR "v[128:129],v1 offset:57344\n"
AW "a50,v230\n"
MF "v[48:51],a[236:239],v[184:187],v[48:51]\n"
"s_waitcnt lgkmcnt(6)\n"
TR "v[130:131],v1 offset:59392\n"
AW "a51,v231\n"
MF "v[52:55],a[236:239],a[244:247],v[52:55]\n"
TR "v[176:177],v2\n"
AR "v220,a88\n"
MF "v[52:55],a[196:199],a[240:243],v[52:55]\n"
TR "v[178:179],v2 offset:2048\n"
AR "v221,a89\n"
MF "v[52:55],a[156:159],v[188:191],v[52:55]\n"
TR "v[132:133],v5\n"
AR "v222,a90\n"
MF "v[44:47],a[152:155],v[188:191],v[44:47]\n"
TR "v[180:181],v2 offset:4096\n"
AR "v223,a91\n"
MF "v[44:47],a[192:195],a[240:243],v[44:47]\n"
TR "v[182:183],v2 offset:6144\n"
AR "v224,a108\n"
MF "v[44:47],a[232:235],a[244:247],v[44:47]\n"
TR "v[134:135],v5 offset:2048\n"
AR "v225,a109\n"
MF "v[36:39],a[228:231],a[244:247],v[36:39]\n"
TR "v[184:185],v2 offset:8192\n"
AR "v226,a110\n"
MF "v[36:39],a[188:191],a[240:243],v[36:39]\n"
TR "v[186:187],v2 offset:10240\n"
AR "v227,a111\n"
MF "v[36:39],a[148:151],v[188:191],v[36:39]\n"
"s_nop 0\n"
TR "v[136:137],v5 offset:4096\n"
"ds_read_b128 a[108:111],v237 offset:8192\n"
MF "v[28:31],a[144:147],v[188:191],v[28:31]\n"
TR "v[138:139],v5 offset:6144\n"
"v_fma_f32 v220,v220,v210,v216\n"
MF "v[28:31],a[184:187],a[240:243],v[28:31]\n"
TR "v[140:141],v5 offset:8192\n"
"v_fma_f32 v221,v221,v210,v216\n"
MF "v[28:31],a[224:227],a[244:247],v[28:31]\n"
TR "v[142:143],v5 offset:10240\n"
"v_fma_f32 v222,v222,v210,v216\n"
MF "v[20:23],a[220:223],a[244:247],v[20:23]\n"
TR "v[144:145],v2 offset:49152\n"
"v_fma_f32 v223,v223,v210,v216\n"
MF "v[20:23],a[180:183],a[240:243],v[20:23]\n"
TR "v[146:147],v2 offset:51200\n"
"v_fma_f32 v224,v224,v210,v216\n"
MF "v[20:23],a[140:143],v[188:191],v[20:23]\n"
"s_waitcnt lgkmcnt(7)\n"
TR "v[148:149],v2 offset:53248\n"
"v_fma_f32 v225,v225,v210,v216\n"
MF "v[56:59],a[140:143],v[176:179],v[56:59]\n"
TR "v[188:189],v3\n"
"v_add_u32 v198,59392,v192\n"
MF "v[56:59],a[180:183],v[180:183],v[56:59]\n"
TR "v[190:191],v3 offset:2048\n"
"v_add_u32 v199,68608,v192\n"
MF "v[56:59],a[220:223],v[184:187],v[56:59]\n"
TR "v[150:151],v2 offset:55296\n"
"v_add_u32 v200,81920,v192\n"
"v_add_u32 v201,91136,v192\n"
MF "v[64:67],a[224:227],v[184:187],v[64:67]\n"
TR "a[240:241],v3 offset:4096\n"
"v_add_u32 v202,100352,v192\n"
MF "v[64:67],a[184:187],v[180:183],v[64:67]\n"
TR "a[242:243],v3 offset:6144\n"
"v_add_u32 v203,109568,v192\n"
MF "v[64:67],a[144:147],v[176:179],v[64:67]\n"
TR "v[152:153],v2 offset:57344\n"
"v_fma_f32 v226,v226,v210,v216\n"
MF "v[72:75],a[148:151],v[176:179],v[72:75]\n"
TR "a[244:245],v3 offset:8192\n"
"v_fma_f32 v227,v227,v210,v216\n"
MF "v[72:75],a[188:191],v[180:183],v[72:75]\n"
TR "a[246:247],v3 offset:10240\n"
CV "v220,v220,v221\n"
MF "v[72:75],a[228:231],v[184:187],v[72:75]\n"
TR "v[154:155],v2 offset:59392\n"
CV "v221,v222,v223\n"
MF "v[80:83],a[232:235],v[184:187],v[80:83]\n"
TR "v[156:157],v6\n"
CV "v222,v224,v225\n"
MF "v[80:83],a[192:195],v[180:183],v[80:83]\n"
TR "v[158:159],v6 offset:2048\n"
CV "v223,v226,v227\n"
MF "v[80:83],a[152:155],v[176:179],v[80:83]\n"
TR "v[160:161],v6 offset:4096\n"
AW "a88,v220\n"
MF "v[88:91],a[156:159],v[176:179],v[88:91]\n"
TR "v[162:163],v6 offset:6144\n"
AW "a89,v221\n"
MF "v[88:91],a[196:199],v[180:183],v[88:91]\n"
TR "v[164:165],v6 offset:8192\n"
AW "a90,v222\n"
MF "v[88:91],a[236:239],v[184:187],v[88:91]\n"
"s_waitcnt lgkmcnt(0)\n"
"s_barrier\n"
MF "v[92:95],a[236:239],a[244:247],v[92:95]\n"
"s_add_u32 s80,s89,122880\n"
"s_add_u32 m0,s79,0\n"
TR "v[166:167],v6 offset:10240\n"
MF "v[92:95],a[196:199],a[240:243],v[92:95]\n"
LD "v192,s[48:51],s80 offen lds\n"
MF "v[92:95],a[156:159],v[188:191],v[92:95]\n"
TR "v[168:169],v3 offset:49152\n"
AW "a91,v223\n"
MF "v[84:87],a[152:155],v[188:191],v[84:87]\n"
TR "v[170:171],v3 offset:51200\n"
AR "v228,a12\n"
MF "v[84:87],a[192:195],a[240:243],v[84:87]\n"
TR "v[172:173],v3 offset:53248\n"
AR "v229,a13\n"
MF "v[84:87],a[232:235],a[244:247],v[84:87]\n"
LD "v193,s[48:51],s80 offen offset:1024 lds\n"
MF "v[76:79],a[228:231],a[244:247],v[76:79]\n"
TR "v[174:175],v3 offset:55296\n"
AR "v230,a14\n"
MF "v[76:79],a[188:191],a[240:243],v[76:79]\n"
TR "v[204:205],v3 offset:57344\n"
AR "v231,a15\n"
MF "v[76:79],a[148:151],v[188:191],v[76:79]\n"
TR "v[206:207],v3 offset:59392\n"
AR "v232,a32\n"
MF "v[68:71],a[144:147],v[188:191],v[68:71]\n"
TR "v[244:245],v7\n"
AR "v233,a33\n"
MF "v[68:71],a[184:187],a[240:243],v[68:71]\n"
LD "v194,s[48:51],s80 offen offset:2048 lds\n"
MF "v[68:71],a[224:227],a[244:247],v[68:71]\n"
TR "v[246:247],v7 offset:2048\n"
AR "v234,a34\n"
MF "v[60:63],a[220:223],a[244:247],v[60:63]\n"
TR "v[248:249],v7 offset:4096\n"
AR "v235,a35\n"
MF "v[60:63],a[180:183],a[240:243],v[60:63]\n"
"s_nop 0\n"
TR "v[250:251],v7 offset:6144\n"
"ds_read_b128 a[32:35],v236 offset:36864\n"
MF "v[60:63],a[140:143],v[188:191],v[60:63]\n"
LD "v195,s[48:51],s80 offen offset:3072 lds\n"
MF "v[16:19],a[0:3],v[96:99],v[16:19]\n"
TR "v[238:239],v7 offset:8192\n"
"v_fma_f32 v228,v228,v211,v217\n"
MF "v[16:19],a[40:43],v[100:103],v[16:19]\n"
TR "v[240:241],v7 offset:10240\n"
"v_fma_f32 v229,v229,v211,v217\n"
MF "v[16:19],a[80:83],v[104:107],v[16:19]\n"
"v_fma_f32 v230,v230,v211,v217\n"
"v_fma_f32 v231,v231,v211,v217\n"
MF "v[16:19],a[20:23],v[108:111],v[16:19]\n"
"s_add_u32 m0,s79,4096\n"
"v_fma_f32 v232,v232,v211,v217\n"
"v_fma_f32 v233,v233,v211,v217\n"
MF "v[16:19],a[60:63],v[112:115],v[16:19]\n"
LD "v196,s[48:51],s80 offen lds\n"
MF "v[16:19],a[100:103],v[116:119],v[16:19]\n"
"v_fma_f32 v234,v234,v211,v217\n"
"v_fma_f32 v235,v235,v211,v217\n"
MF "v[20:23],a[100:103],v[140:143],v[20:23]\n"
CV "v228,v228,v229\n"
CV "v229,v230,v231\n"
MF "v[20:23],a[60:63],v[136:139],v[20:23]\n"
CV "v230,v232,v233\n"
CV "v231,v234,v235\n"
MF "v[20:23],a[20:23],v[132:135],v[20:23]\n"
LD "v197,s[48:51],s80 offen offset:1024 lds\n"
MF "v[20:23],a[80:83],v[128:131],v[20:23]\n"
AW "a12,v228\n"
AW "a13,v229\n"
MF "v[20:23],a[40:43],v[124:127],v[20:23]\n"
AW "a14,v230\n"
AW "a15,v231\n"
MF "v[20:23],a[0:3],v[120:123],v[20:23]\n"
"s_waitcnt lgkmcnt(13)\n"
AR "v220,a52\n"
AR "v221,a53\n"
MF "v[56:59],a[0:3],v[144:147],v[56:59]\n"
AR "v222,a54\n"
AR "v223,a55\n"
MF "v[56:59],a[40:43],v[148:151],v[56:59]\n"
LD "v198,s[48:51],s80 offen offset:2048 lds\n"
MF "v[56:59],a[80:83],v[152:155],v[56:59]\n"
AR "v224,a72\n"
AR "v225,a73\n"
MF "v[56:59],a[20:23],v[156:159],v[56:59]\n"
AR "v226,a74\n"
AR "v227,a75\n"
MF "v[56:59],a[60:63],v[160:163],v[56:59]\n"
"ds_read_b128 a[72:75],v236 offset:57344\n"
"v_fma_f32 v220,v220,v211,v217\n"
MF "v[56:59],a[100:103],v[164:167],v[56:59]\n"
"s_waitcnt lgkmcnt(1)\n"
"v_fma_f32 v221,v221,v211,v217\n"
"v_fma_f32 v222,v222,v211,v217\n"
MF "v[60:63],a[100:103],v[238:241],v[60:63]\n"
LD "v199,s[48:51],s80 offen offset:3072 lds\n"
MF "v[60:63],a[60:63],v[248:251],v[60:63]\n"
"v_fma_f32 v223,v223,v211,v217\n"
"v_fma_f32 v224,v224,v211,v217\n"
MF "v[60:63],a[20:23],v[244:247],v[60:63]\n"
"v_fma_f32 v225,v225,v211,v217\n"
"v_fma_f32 v226,v226,v211,v217\n"
MF "v[60:63],a[80:83],v[204:207],v[60:63]\n"
"s_add_u32 m0,s79,8192\n"
"v_fma_f32 v227,v227,v211,v217\n"
CV "v220,v220,v221\n"
MF "v[60:63],a[40:43],v[172:175],v[60:63]\n"
LD "v200,s[48:51],s80 offen lds\n"
MF "v[60:63],a[0:3],v[168:171],v[60:63]\n"
CV "v221,v222,v223\n"
CV "v222,v224,v225\n"
MF "v[68:71],a[4:7],v[168:171],v[68:71]\n"
CV "v223,v226,v227\n"
AW "a52,v220\n"
MF "v[68:71],a[44:47],v[172:175],v[68:71]\n"
AW "a53,v221\n"
AW "a54,v222\n"
MF "v[68:71],a[84:87],v[204:207],v[68:71]\n"
AW "a55,v223\n"
AR "v228,a92\n"
MF "v[68:71],a[24:27],v[244:247],v[68:71]\n"
LD "v201,s[48:51],s80 offen offset:1024 lds\n"
MF "v[68:71],a[64:67],v[248:251],v[68:71]\n"
AR "v229,a93\n"
AR "v230,a94\n"
MF "v[68:71],a[104:107],v[238:241],v[68:71]\n"
AR "v231,a95\n"
AR "v232,a112\n"
MF "v[64:67],a[104:107],v[164:167],v[64:67]\n"
"s_waitcnt lgkmcnt(0)\n"
"s_barrier\n"
MF "v[64:67],a[64:67],v[160:163],v[64:67]\n"
LD "v202,s[48:51],s80 offen offset:2048 lds\n"
MF "v[64:67],a[24:27],v[156:159],v[64:67]\n"
AR "v233,a113\n"
AR "v234,a114\n"
MF "v[64:67],a[84:87],v[152:155],v[64:67]\n"
AR "v235,a115\n"
"v_fma_f32 v228,v228,v211,v217\n"
MF "v[64:67],a[44:47],v[148:151],v[64:67]\n"
"ds_read_b128 a[112:115],v237 offset:12288\n"
"v_fma_f32 v229,v229,v211,v217\n"
MF "v[64:67],a[4:7],v[144:147],v[64:67]\n"
"v_fma_f32 v230,v230,v211,v217\n"
"v_fma_f32 v231,v231,v211,v217\n"
MF "v[28:31],a[4:7],v[120:123],v[28:31]\n"
LD "v203,s[48:51],s80 offen offset:3072 lds\n"
MF "v[28:31],a[44:47],v[124:127],v[28:31]\n"
"v_fma_f32 v232,v232,v211,v217\n"
"v_fma_f32 v233,v233,v211,v217\n"
MF "v[28:31],a[84:87],v[128:131],v[28:31]\n"
"v_fma_f32 v234,v234,v211,v217\n"
"v_fma_f32 v235,v235,v211,v217\n"
MF "v[28:31],a[24:27],v[132:135],v[28:31]\n"
CV "v228,v228,v229\n"
CV "v229,v230,v231\n"
MF "v[28:31],a[64:67],v[136:139],v[28:31]\n"
CV "v230,v232,v233\n"
CV "v231,v234,v235\n"
MF "v[28:31],a[104:107],v[140:143],v[28:31]\n"
"s_add_u32 s88,s88,s65\n"
"s_add_u32 m0,s79,49152\n"
AW "a92,v228\n"
MF "v[24:27],a[104:107],v[116:119],v[24:27]\n"
LD "v192,s[48:51],s88 offen lds\n"
MF "v[24:27],a[64:67],v[112:115],v[24:27]\n"
AW "a93,v229\n"
AW "a94,v230\n"
MF "v[24:27],a[24:27],v[108:111],v[24:27]\n"
AW "a95,v231\n"
AR "v220,a16\n"
MF "v[24:27],a[84:87],v[104:107],v[24:27]\n"
AR "v221,a17\n"
AR "v222,a18\n"
MF "v[24:27],a[44:47],v[100:103],v[24:27]\n"
LD "v193,s[48:51],s88 offen offset:1024 lds\n"
MF "v[24:27],a[4:7],v[96:99],v[24:27]\n"
AR "v223,a19\n"
AR "v224,a36\n"
MF "v[32:35],a[8:11],v[96:99],v[32:35]\n"
AR "v225,a37\n"
AR "v226,a38\n"
MF "v[32:35],a[48:51],v[100:103],v[32:35]\n"
AR "v227,a39\n"
"v_fma_f32 v220,v220,v212,v218\n"
MF "v[32:35],a[88:91],v[104:107],v[32:35]\n"
"ds_read_b128 a[36:39],v236 offset:40960\n"
"v_fma_f32 v221,v221,v212,v218\n"
MF "v[32:35],a[28:31],v[108:111],v[32:35]\n"
LD "v194,s[48:51],s88 offen offset:2048 lds\n"
MF "v[32:35],a[68:71],v[112:115],v[32:35]\n"
"v_fma_f32 v222,v222,v212,v218\n"
"v_fma_f32 v223,v223,v212,v218\n"
MF "v[32:35],a[108:111],v[116:119],v[32:35]\n"
"v_fma_f32 v224,v224,v212,v218\n"
"v_fma_f32 v225,v225,v212,v218\n"
MF "v[36:39],a[108:111],v[140:143],v[36:39]\n"
"v_fma_f32 v226,v226,v212,v218\n"
"v_fma_f32 v227,v227,v212,v218\n"
MF "v[36:39],a[68:71],v[136:139],v[36:39]\n"
CV "v220,v220,v221\n"
CV "v221,v222,v223\n"
MF "v[36:39],a[28:31],v[132:135],v[36:39]\n"
LD "v195,s[48:51],s88 offen offset:3072 lds\n"
MF "v[36:39],a[88:91],v[128:131],v[36:39]\n"
CV "v222,v224,v225\n"
CV "v223,v226,v227\n"
MF "v[36:39],a[48:51],v[124:127],v[36:39]\n"
AW "a16,v220\n"
AW "a17,v221\n"
MF "v[36:39],a[8:11],v[120:123],v[36:39]\n"
AW "a18,v222\n"
AW "a19,v223\n"
MF "v[72:75],a[8:11],v[144:147],v[72:75]\n"
"s_add_u32 m0,s79,53248\n"
AR "v228,a56\n"
AR "v229,a57\n"
MF "v[72:75],a[48:51],v[148:151],v[72:75]\n"
LD "v196,s[48:51],s88 offen lds\n"
MF "v[72:75],a[88:91],v[152:155],v[72:75]\n"
AR "v230,a58\n"
AR "v231,a59\n"
MF "v[72:75],a[28:31],v[156:159],v[72:75]\n"
AR "v232,a76\n"
AR "v233,a77\n"
MF "v[72:75],a[68:71],v[160:163],v[72:75]\n"
AR "v234,a78\n"
AR "v235,a79\n"
MF "v[72:75],a[108:111],v[164:167],v[72:75]\n"
"ds_read_b128 a[76:79],v236 offset:61440\n"
"v_fma_f32 v228,v228,v212,v218\n"
MF "v[76:79],a[108:111],v[238:241],v[76:79]\n"
LD "v197,s[48:51],s88 offen offset:1024 lds\n"
MF "v[76:79],a[68:71],v[248:251],v[76:79]\n"
"v_fma_f32 v229,v229,v212,v218\n"
"v_fma_f32 v230,v230,v212,v218\n"
MF "v[76:79],a[28:31],v[244:247],v[76:79]\n"
"v_fma_f32 v231,v231,v212,v218\n"
"v_fma_f32 v232,v232,v212,v218\n"
MF "v[76:79],a[88:91],v[204:207],v[76:79]\n"
"v_fma_f32 v233,v233,v212,v218\n"
"v_fma_f32 v234,v234,v212,v218\n"
MF "v[76:79],a[48:51],v[172:175],v[76:79]\n"
"v_fma_f32 v235,v235,v212,v218\n"
CV "v228,v228,v229\n"
MF "v[76:79],a[8:11],v[168:171],v[76:79]\n"
"s_waitcnt lgkmcnt(2)\n"
LD "v198,s[48:51],s88 offen offset:2048 lds\n"
MF "v[84:87],a[12:15],v[168:171],v[84:87]\n"
CV "v229,v230,v231\n"
CV "v230,v232,v233\n"
MF "v[84:87],a[52:55],v[172:175],v[84:87]\n"
CV "v231,v234,v235\n"
AW "a56,v228\n"
MF "v[84:87],a[92:95],v[204:207],v[84:87]\n"
AW "a57,v229\n"
AW "a58,v230\n"
MF "v[84:87],a[32:35],v[244:247],v[84:87]\n"
AW "a59,v231\n"
AR "v220,a96\n"
MF "v[84:87],a[72:75],v[248:251],v[84:87]\n"
LD "v199,s[48:51],s88 offen offset:3072 lds\n"
MF "v[84:87],a[112:115],v[238:241],v[84:87]\n"
AR "v221,a97\n"
AR "v222,a98\n"
MF "v[80:83],a[112:115],v[164:167],v[80:83]\n"
AR "v223,a99\n"
AR "v224,a116\n"
MF "v[80:83],a[72:75],v[160:163],v[80:83]\n"
AR "v225,a117\n"
AR "v226,a118\n"
MF "v[80:83],a[32:35],v[156:159],v[80:83]\n"
"s_add_u32 m0,s79,57344\n"
AR "v227,a119\n"
"v_fma_f32 v220,v220,v212,v218\n"
MF "v[80:83],a[92:95],v[152:155],v[80:83]\n"
LD "v200,s[48:51],s88 offen lds\n"
MF "v[80:83],a[52:55],v[148:151],v[80:83]\n"
"ds_read_b128 a[116:119],v237 offset:16384\n"
"v_fma_f32 v221,v221,v212,v218\n"
MF "v[80:83],a[12:15],v[144:147],v[80:83]\n"
"v_fma_f32 v222,v222,v212,v218\n"
"v_fma_f32 v223,v223,v212,v218\n"
MF "v[44:47],a[12:15],v[120:123],v[44:47]\n"
"v_fma_f32 v224,v224,v212,v218\n"
"v_fma_f32 v225,v225,v212,v218\n"
MF "v[44:47],a[52:55],v[124:127],v[44:47]\n"
"v_fma_f32 v226,v226,v212,v218\n"
"v_fma_f32 v227,v227,v212,v218\n"
MF "v[44:47],a[92:95],v[128:131],v[44:47]\n"
LD "v201,s[48:51],s88 offen offset:1024 lds\n"
MF "v[44:47],a[32:35],v[132:135],v[44:47]\n"
CV "v220,v220,v221\n"
CV "v221,v222,v223\n"
MF "v[44:47],a[72:75],v[136:139],v[44:47]\n"
CV "v222,v224,v225\n"
CV "v223,v226,v227\n"
MF "v[44:47],a[112:115],v[140:143],v[44:47]\n"
AW "a96,v220\n"
AW "a97,v221\n"
MF "v[40:43],a[112:115],v[116:119],v[40:43]\n"
AW "a98,v222\n"
AW "a99,v223\n"
MF "v[40:43],a[72:75],v[112:115],v[40:43]\n"
LD "v202,s[48:51],s88 offen offset:2048 lds\n"
MF "v[40:43],a[32:35],v[108:111],v[40:43]\n"
AR "v8,a252\n"
AR "v11,a253\n"
MF "v[40:43],a[92:95],v[104:107],v[40:43]\n"
"v_bfe_u32 v224,v9,5,3\n"
"v_bfe_u32 v225,v9,8,2\n"
MF "v[40:43],a[52:55],v[100:103],v[40:43]\n"
"v_lshlrev_b32 v225,1,v225\n"
"v_and_b32 v226,16,v9\n"
MF "v[40:43],a[12:15],v[96:99],v[40:43]\n"
"s_waitcnt lgkmcnt(0)\n"
"v_lshlrev_b32_e64 v226,v226,s9\n"
"v_sub_u32 v224,v224,v225\n"
MF "v[48:51],a[16:19],v[96:99],v[48:51]\n"
LD "v203,s[48:51],s88 offen offset:3072 lds\n"
MF "v[48:51],a[56:59],v[100:103],v[48:51]\n"
"v_cmp_eq_u32 vcc,0,v224\n"
MF "v[48:51],a[96:99],v[104:107],v[48:51]\n"
"s_nop 0\n"
"v_cndmask_b32 v220,0,v226,vcc\n"
"v_cmp_eq_u32 vcc,1,v224\n"
MF "v[48:51],a[36:39],v[108:111],v[48:51]\n"
"s_nop 0\n"
"v_cndmask_b32 v221,0,v226,vcc\n"
AW "a248,0\n"
MF "v[48:51],a[76:79],v[112:115],v[48:51]\n"
AW "a249,0\n"
AW "a250,v220\n"
MF "v[48:51],a[116:119],v[116:119],v[48:51]\n"
AW "a251,v221\n"
AW "a252,v220\n"
MF "v[52:55],a[116:119],v[140:143],v[52:55]\n"
AW "a253,v221\n"
AW "a254,0\n"
MF "v[52:55],a[76:79],v[136:139],v[52:55]\n"
AW "a255,0\n"
"v_bfe_u32 v12,v9,4,4\n"
MF "v[52:55],a[36:39],v[132:135],v[52:55]\n"
"s_waitcnt lgkmcnt(0)\n"
"s_barrier\n"
MF "v[52:55],a[96:99],v[128:131],v[52:55]\n"
"s_add_u32 s80,s88,122880\n"
TR "v[176:177],v0 offset:24576\n"
"v_lshlrev_b32 v12,2,v12\n"
MF "v[52:55],a[56:59],v[124:127],v[52:55]\n"
"s_add_u32 m0,s79,98304\n"
TR "v[178:179],v0 offset:26624\n"
"v_lshrrev_b32 v13,2,v9\n"
MF "v[52:55],a[16:19],v[120:123],v[52:55]\n"
LD "v192,s[48:51],s80 offen lds\n"
MF "v[88:91],a[16:19],v[144:147],v[88:91]\n"
"s_nop 0\n"
TR "v[180:181],v0 offset:28672\n"
"ds_write_addtid_b32 v16 offset:24576\n"
MF "v[88:91],a[56:59],v[148:151],v[88:91]\n"
"s_nop 0\n"
TR "v[182:183],v0 offset:30720\n"
"ds_write_addtid_b32 v17 offset:24832\n"
MF "v[88:91],a[96:99],v[152:155],v[88:91]\n"
LD "v193,s[48:51],s80 offen offset:1024 lds\n"
MF "v[88:91],a[36:39],v[156:159],v[88:91]\n"
"s_nop 0\n"
TR "v[184:185],v0 offset:32768\n"
"ds_write_addtid_b32 v18 offset:25088\n"
MF "v[88:91],a[76:79],v[160:163],v[88:91]\n"
"s_nop 0\n"
TR "v[186:187],v0 offset:34816\n"
"ds_write_addtid_b32 v19 offset:25344\n"
MF "v[88:91],a[116:119],v[164:167],v[88:91]\n"
"s_nop 0\n"
"ds_write_addtid_b32 v20 offset:25600\n"
"ds_write_addtid_b32 v21 offset:25856\n"
MF "v[92:95],a[116:119],v[238:241],v[92:95]\n"
LD "v194,s[48:51],s80 offen offset:2048 lds\n"
MF "v[92:95],a[76:79],v[248:251],v[92:95]\n"
"s_nop 0\n"
"ds_write_addtid_b32 v22 offset:26112\n"
"ds_write_addtid_b32 v23 offset:26368\n"
MF "v[92:95],a[36:39],v[244:247],v[92:95]\n"
"s_nop 0\n"
"ds_write_addtid_b32 v24 offset:26624\n"
"ds_write_addtid_b32 v25 offset:26880\n"
MF "v[92:95],a[96:99],v[204:207],v[92:95]\n"
"s_nop 0\n"
"ds_write_addtid_b32 v26 offset:27136\n"
"ds_write_addtid_b32 v27 offset:27392\n"
MF "v[92:95],a[56:59],v[172:175],v[92:95]\n"
"v_add_u32 v13,s58,v13\n"
"v_add_u32 v4,24576,v4\n"
MF "v[92:95],a[16:19],v[168:171],v[92:95]\n"
"s_waitcnt lgkmcnt(9)\n"
"v_add_u32 v5,24576,v5\n"
"v_add_u32 v6,24576,v6\n"
"v_add_u32 v7,24576,v7\n"
".Lpl0:\n"
MF "v[96:99],a[0:3],v[176:179],v[252:255]\n"
"s_nop 0\n"
TR "v[188:189],v1 offset:24576\n"
"ds_write_addtid_b32 v28 offset:27648\n"
MF "v[96:99],a[40:43],v[180:183],v[96:99]\n"
"s_nop 0\n"
TR "v[190:191],v1 offset:26624\n"
"ds_write_addtid_b32 v29 offset:27904\n"
MF "v[96:99],a[80:83],v[184:187],v[96:99]\n"
LD "v195,s[48:51],s80 offen offset:3072 lds\n"
MF "v[104:107],a[84:87],v[184:187],v[252:255]\n"
"s_add_u32 s60,s64,1\n"
TR "a[240:241],v1 offset:28672\n"
"ds_write_addtid_b32 v30 offset:28160\n"
MF "v[104:107],a[44:47],v[180:183],v[104:107]\n"
"s_add_u32 m0,s79,102400\n"
TR "a[242:243],v1 offset:30720\n"
"ds_write_addtid_b32 v31 offset:24320\n"
MF "v[104:107],a[4:7],v[176:179],v[104:107]\n"
".Lpg21h:\n"
"s_cbranch_execz .Lpgrole1_h\n"
LD "v196,s[48:51],s80 offen lds\n"
MF "v[112:115],a[8:11],v[176:179],v[252:255]\n"
"s_cmp_eq_u32 s60,8\n"
"s_cselect_b32 s60,0,s60\n"
TR "a[244:245],v1 offset:32768\n"
MF "v[112:115],a[48:51],v[180:183],v[112:115]\n"
"s_cmp_eq_u32 s60,10\n"
"s_cselect_b32 s60,s61,s60\n"
TR "a[246:247],v1 offset:34816\n"
MF "v[112:115],a[88:91],v[184:187],v[112:115]\n"
"s_sub_u32 s65,s60,s64\n"
"ds_write_addtid_b32 v32 offset:24576\n"
"ds_write_addtid_b32 v33 offset:24832\n"
MF "v[120:123],a[92:95],v[184:187],v[252:255]\n"
"s_lshl_b32 s65,s65,7\n"
"ds_write_addtid_b32 v34 offset:25088\n"
"ds_write_addtid_b32 v35 offset:25344\n"
MF "v[120:123],a[52:55],v[180:183],v[120:123]\n"
LD "v197,s[48:51],s80 offen offset:1024 lds\n"
MF "v[120:123],a[12:15],v[176:179],v[120:123]\n"
"s_mov_b32 s64,s60\n"
"ds_write_addtid_b32 v36 offset:25600\n"
"ds_write_addtid_b32 v37 offset:25856\n"
MF "v[128:131],a[16:19],v[176:179],v[252:255]\n"
"s_nop 0\n"
"ds_write_addtid_b32 v38 offset:26112\n"
"ds_write_addtid_b32 v39 offset:26368\n"
MF "v[128:131],a[56:59],v[180:183],v[128:131]\n"
"s_nop 0\n"
"ds_write_addtid_b32 v40 offset:26624\n"
"ds_write_addtid_b32 v41 offset:26880\n"
MF "v[128:131],a[96:99],v[184:187],v[128:131]\n"
"s_waitcnt lgkmcnt(10)\n"
LD "v198,s[48:51],s80 offen offset:2048 lds\n"
MF "v[132:135],a[96:99],a[244:247],v[252:255]\n"
"s_nop 0\n"
TR "v[176:177],v2 offset:24576\n"
"ds_write_addtid_b32 v42 offset:27136\n"
MF "v[132:135],a[56:59],a[240:243],v[132:135]\n"
"s_nop 0\n"
TR "v[178:179],v2 offset:26624\n"
"ds_write_addtid_b32 v43 offset:27392\n"
MF "v[132:135],a[16:19],v[188:191],v[132:135]\n"
"s_nop 0\n"
"ds_write_addtid_b32 v44 offset:27648\n"
"ds_write_addtid_b32 v45 offset:27904\n"
MF "v[124:127],a[12:15],v[188:191],v[252:255]\n"
"s_nop 0\n"
TR "v[180:181],v2 offset:28672\n"
"ds_write_addtid_b32 v46 offset:28160\n"
MF "v[124:127],a[52:55],a[240:243],v[124:127]\n"
"s_nop 0\n"
TR "v[182:183],v2 offset:30720\n"
"ds_write_addtid_b32 v47 offset:28416\n"
MF "v[124:127],a[92:95],a[244:247],v[124:127]\n"
LD "v199,s[48:51],s80 offen offset:3072 lds\n"
MF "v[116:119],a[88:91],a[244:247],v[252:255]\n"
"s_nop 0\n"
TR "v[184:185],v2 offset:32768\n"
"ds_write_addtid_b32 v48 offset:28672\n"
MF "v[116:119],a[48:51],a[240:243],v[116:119]\n"
"s_add_u32 m0,s79,106496\n"
TR "v[186:187],v2 offset:34816\n"
"ds_write_addtid_b32 v49 offset:24832\n"
MF "v[116:119],a[8:11],v[188:191],v[116:119]\n"
LD "v200,s[48:51],s80 offen lds\n"
MF "v[108:111],a[4:7],v[188:191],v[252:255]\n"
"s_nop 0\n"
"ds_write_addtid_b32 v50 offset:25088\n"
"ds_write_addtid_b32 v51 offset:25344\n"
MF "v[108:111],a[44:47],a[240:243],v[108:111]\n"
"s_nop 0\n"
"ds_write_addtid_b32 v52 offset:25600\n"
"ds_write_addtid_b32 v53 offset:25856\n"
MF "v[108:111],a[84:87],a[244:247],v[108:111]\n"
"s_nop 0\n"
"ds_write_addtid_b32 v54 offset:26112\n"
"ds_write_addtid_b32 v55 offset:26368\n"
MF "v[100:103],a[80:83],a[244:247],v[252:255]\n"
MF "v[100:103],a[40:43],a[240:243],v[100:103]\n"
LD "v201,s[48:51],s80 offen offset:1024 lds\n"
MF "v[100:103],a[0:3],v[188:191],v[100:103]\n"
"s_waitcnt lgkmcnt(7)\n"
MF "v[136:139],a[0:3],v[176:179],v[252:255]\n"
TR "v[188:189],v3 offset:24576\n"
MF "v[136:139],a[40:43],v[180:183],v[136:139]\n"
TR "v[190:191],v3 offset:26624\n"
MF "v[136:139],a[80:83],v[184:187],v[136:139]\n"
LD "v202,s[48:51],s80 offen offset:2048 lds\n"
MF "v[144:147],a[84:87],v[184:187],v[252:255]\n"
TR "a[240:241],v3 offset:28672\n"
MF "v[144:147],a[44:47],v[180:183],v[144:147]\n"
TR "a[242:243],v3 offset:30720\n"
MF "v[144:147],a[4:7],v[176:179],v[144:147]\n"
LD "v203,s[48:51],s80 offen offset:3072 lds\n"
MF "v[152:155],a[8:11],v[176:179],v[252:255]\n"
TR "a[244:245],v3 offset:32768\n"
MF "v[152:155],a[48:51],v[180:183],v[152:155]\n"
TR "a[246:247],v3 offset:34816\n"
MF "v[152:155],a[88:91],v[184:187],v[152:155]\n"
MF "v[160:163],a[92:95],v[184:187],v[252:255]\n"
MF "v[160:163],a[52:55],v[180:183],v[160:163]\n"
MF "v[160:163],a[12:15],v[176:179],v[160:163]\n"
MF "v[168:171],a[16:19],v[176:179],v[252:255]\n"
MF "v[168:171],a[56:59],v[180:183],v[168:171]\n"
MF "v[168:171],a[96:99],v[184:187],v[168:171]\n"
"s_waitcnt vmcnt(24) lgkmcnt(0)\n"
"s_barrier\n"
MF "v[172:175],a[96:99],a[244:247],v[252:255]\n"
"s_add_u32 s89,s89,s65\n"
TR "v[176:177],v0\n"
MF "v[172:175],a[56:59],a[240:243],v[172:175]\n"
"s_add_u32 m0,s79,24576\n"
TR "v[178:179],v0 offset:2048\n"
MF "v[172:175],a[16:19],v[188:191],v[172:175]\n"
LD "v192,s[48:51],s89 offen lds\n"
MF "v[164:167],a[12:15],v[188:191],v[252:255]\n"
TR "v[180:181],v0 offset:4096\n"
MF "v[164:167],a[52:55],a[240:243],v[164:167]\n"
TR "v[182:183],v0 offset:6144\n"
MF "v[164:167],a[92:95],a[244:247],v[164:167]\n"
LD "v193,s[48:51],s89 offen offset:1024 lds\n"
MF "v[156:159],a[88:91],a[244:247],v[252:255]\n"
TR "v[184:185],v0 offset:8192\n"
MF "v[156:159],a[48:51],a[240:243],v[156:159]\n"
"s_nop 0\n"
TR "v[186:187],v0 offset:10240\n"
"ds_write_addtid_b32 v56 offset:49152\n"
MF "v[156:159],a[8:11],v[188:191],v[156:159]\n"
"s_nop 0\n"
"ds_write_addtid_b32 v57 offset:49408\n"
"ds_write_addtid_b32 v58 offset:49664\n"
MF "v[148:151],a[4:7],v[188:191],v[252:255]\n"
LD "v194,s[48:51],s89 offen offset:2048 lds\n"
MF "v[148:151],a[44:47],a[240:243],v[148:151]\n"
"buffer_load_dword v10,v12,s[40:43],s86 offen sc1\n"
MF "v[148:151],a[84:87],a[244:247],v[148:151]\n"
"s_nop 0\n"
"ds_write_addtid_b32 v59 offset:49920\n"
"ds_write_addtid_b32 v60 offset:50176\n"
MF "v[140:143],a[80:83],a[244:247],v[252:255]\n"
"s_nop 0\n"
"ds_write_addtid_b32 v61 offset:50432\n"
"ds_write_addtid_b32 v62 offset:50688\n"
MF "v[140:143],a[40:43],a[240:243],v[140:143]\n"
"s_nop 0\n"
"ds_write_addtid_b32 v63 offset:50944\n"
"ds_write_addtid_b32 v64 offset:51200\n"
MF "v[140:143],a[0:3],v[188:191],v[140:143]\n"
"s_waitcnt lgkmcnt(9)\n"
LD "v195,s[48:51],s89 offen offset:3072 lds\n"
MF "v[96:99],a[120:123],v[176:179],v[96:99]\n"
"s_nop 0\n"
TR "v[188:189],v1\n"
"ds_write_addtid_b32 v65 offset:51456\n"
MF "v[96:99],a[160:163],v[180:183],v[96:99]\n"
"s_nop 0\n"
TR "v[190:191],v1 offset:2048\n"
"ds_write_addtid_b32 v66 offset:51712\n"
MF "v[96:99],a[200:203],v[184:187],v[96:99]\n"
"s_nop 0\n"
"ds_write_addtid_b32 v67 offset:51968\n"
"ds_write_addtid_b32 v68 offset:52224\n"
MF "v[104:107],a[204:207],v[184:187],v[104:107]\n"
"s_nop 0\n"
TR "a[240:241],v1 offset:4096\n"
"ds_write_addtid_b32 v69 offset:52480\n"
MF "v[104:107],a[164:167],v[180:183],v[104:107]\n"
"s_add_u32 m0,s79,28672\n"
TR "a[242:243],v1 offset:6144\n"
"ds_write_addtid_b32 v70 offset:48640\n"
MF "v[104:107],a[124:127],v[176:179],v[104:107]\n"
LD "v196,s[48:51],s89 offen lds\n"
MF "v[112:115],a[128:131],v[176:179],v[112:115]\n"
"s_nop 0\n"
TR "a[244:245],v1 offset:8192\n"
"ds_write_addtid_b32 v71 offset:48896\n"
MF "v[112:115],a[168:171],v[180:183],v[112:115]\n"
"s_nop 0\n"
TR "a[246:247],v1 offset:10240\n"
"ds_write_addtid_b32 v72 offset:49152\n"
MF "v[112:115],a[208:211],v[184:187],v[112:115]\n"
LD "v197,s[48:51],s89 offen offset:1024 lds\n"
MF "v[120:123],a[212:215],v[184:187],v[120:123]\n"
"s_nop 0\n"
"ds_write_addtid_b32 v73 offset:49408\n"
"ds_write_addtid_b32 v74 offset:49664\n"
MF "v[120:123],a[172:175],v[180:183],v[120:123]\n"
"s_nop 0\n"
"ds_write_addtid_b32 v75 offset:49920\n"
"ds_write_addtid_b32 v76 offset:50176\n"
MF "v[120:123],a[132:135],v[176:179],v[120:123]\n"
"s_nop 0\n"
"ds_write_addtid_b32 v77 offset:50432\n"
"ds_write_addtid_b32 v78 offset:50688\n"
MF "v[128:131],a[136:139],v[176:179],v[128:131]\n"
"s_nop 0\n"
"ds_write_addtid_b32 v79 offset:50944\n"
"ds_write_addtid_b32 v80 offset:51200\n"
MF "v[128:131],a[176:179],v[180:183],v[128:131]\n"
LD "v198,s[48:51],s89 offen offset:2048 lds\n"
MF "v[128:131],a[216:219],v[184:187],v[128:131]\n"
"s_waitcnt lgkmcnt(9)\n"
"ds_write_addtid_b32 v81 offset:51456\n"
"ds_write_addtid_b32 v82 offset:51712\n"
MF "v[132:135],a[216:219],a[244:247],v[132:135]\n"
"s_nop 0\n"
TR "v[176:177],v2\n"
"ds_write_addtid_b32 v83 offset:51968\n"
MF "v[132:135],a[176:179],a[240:243],v[132:135]\n"
"s_nop 0\n"
TR "v[178:179],v2 offset:2048\n"
"ds_write_addtid_b32 v84 offset:52224\n"
MF "v[132:135],a[136:139],v[188:191],v[132:135]\n"
LD "v199,s[48:51],s89 offen offset:3072 lds\n"
MF "v[124:127],a[132:135],v[188:191],v[124:127]\n"
"s_nop 0\n"
TR "v[180:181],v2 offset:4096\n"
"ds_write_addtid_b32 v85 offset:52480\n"
MF "v[124:127],a[172:175],a[240:243],v[124:127]\n"
"s_nop 0\n"
TR "v[182:183],v2 offset:6144\n"
"ds_write_addtid_b32 v86 offset:52736\n"
MF "v[124:127],a[212:215],a[244:247],v[124:127]\n"
"s_nop 0\n"
"ds_write_addtid_b32 v87 offset:52992\n"
"ds_write_addtid_b32 v88 offset:53248\n"
MF "v[116:119],a[208:211],a[244:247],v[116:119]\n"
"s_nop 0\n"
TR "v[184:185],v2 offset:8192\n"
"ds_write_addtid_b32 v89 offset:53504\n"
MF "v[116:119],a[168:171],a[240:243],v[116:119]\n"
"s_add_u32 m0,s79,32768\n"
TR "v[186:187],v2 offset:10240\n"
"ds_write_addtid_b32 v90 offset:49664\n"
MF "v[116:119],a[128:131],v[188:191],v[116:119]\n"
LD "v200,s[48:51],s89 offen lds\n"
MF "v[108:111],a[124:127],v[188:191],v[108:111]\n"
"s_nop 0\n"
"ds_write_addtid_b32 v91 offset:49920\n"
"ds_write_addtid_b32 v92 offset:50176\n"
MF "v[108:111],a[164:167],a[240:243],v[108:111]\n"
"s_nop 0\n"
"ds_write_addtid_b32 v93 offset:50432\n"
"ds_write_addtid_b32 v94 offset:50688\n"
MF "v[108:111],a[204:207],a[244:247],v[108:111]\n"
"ds_write_addtid_b32 v95 offset:50944\n"
MF "v[100:103],a[200:203],a[244:247],v[100:103]\n"
LD "v201,s[48:51],s89 offen offset:1024 lds\n"
MF "v[100:103],a[160:163],a[240:243],v[100:103]\n"
MF "v[100:103],a[120:123],v[188:191],v[100:103]\n"
"s_waitcnt lgkmcnt(6)\n"
MF "v[136:139],a[120:123],v[176:179],v[136:139]\n"
TR "v[188:189],v3\n"
MF "v[136:139],a[160:163],v[180:183],v[136:139]\n"
TR "v[190:191],v3 offset:2048\n"
MF "v[136:139],a[200:203],v[184:187],v[136:139]\n"
LD "v202,s[48:51],s89 offen offset:2048 lds\n"
MF "v[144:147],a[204:207],v[184:187],v[144:147]\n"
TR "a[240:241],v3 offset:4096\n"
MF "v[144:147],a[164:167],v[180:183],v[144:147]\n"
TR "a[242:243],v3 offset:6144\n"
MF "v[144:147],a[124:127],v[176:179],v[144:147]\n"
LD "v203,s[48:51],s89 offen offset:3072 lds\n"
MF "v[152:155],a[128:131],v[176:179],v[152:155]\n"
TR "a[244:245],v3 offset:8192\n"
MF "v[152:155],a[168:171],v[180:183],v[152:155]\n"
TR "a[246:247],v3 offset:10240\n"
MF "v[152:155],a[208:211],v[184:187],v[152:155]\n"
MF "v[160:163],a[212:215],v[184:187],v[160:163]\n"
MF "v[160:163],a[172:175],v[180:183],v[160:163]\n"
MF "v[160:163],a[132:135],v[176:179],v[160:163]\n"
MF "v[168:171],a[136:139],v[176:179],v[168:171]\n"
MF "v[168:171],a[176:179],v[180:183],v[168:171]\n"
MF "v[168:171],a[216:219],v[184:187],v[168:171]\n"
"s_waitcnt vmcnt(25) lgkmcnt(0)\n"
"s_barrier\n"
MF "v[172:175],a[216:219],a[244:247],v[172:175]\n"
"s_add_u32 s80,s89,122880\n"
TR "v[176:177],v0 offset:49152\n"
MF "v[172:175],a[176:179],a[240:243],v[172:175]\n"
"s_add_u32 m0,s79,0\n"
"s_mul_i32 s95,s92,3277\n"
TR "v[178:179],v0 offset:51200\n"
MF "v[172:175],a[136:139],v[188:191],v[172:175]\n"
LD "v192,s[48:51],s80 offen lds\n"
MF "v[164:167],a[132:135],v[188:191],v[164:167]\n"
"s_lshr_b32 s95,s95,16\n"
"s_mul_i32 s59,s95,20\n"
TR "v[180:181],v0 offset:53248\n"
MF "v[164:167],a[172:175],a[240:243],v[164:167]\n"
"s_sub_u32 s59,s92,s59\n"
TR "v[182:183],v0 offset:55296\n"
MF "v[164:167],a[212:215],a[244:247],v[164:167]\n"
LD "v193,s[48:51],s80 offen offset:1024 lds\n"
MF "v[156:159],a[208:211],a[244:247],v[156:159]\n"
"s_cmp_ge_u32 s59,10\n"
"s_cselect_b32 s96,4294557760,0\n"
TR "v[184:185],v0 offset:57344\n"
MF "v[156:159],a[168:171],a[240:243],v[156:159]\n"
"s_add_u32 s96,s96,1024\n"
"s_mul_i32 s59,s59,40960\n"
TR "v[186:187],v0 offset:59392\n"
MF "v[156:159],a[128:131],v[188:191],v[156:159]\n"
"s_add_u32 s96,s96,s59\n"
"s_lshl_b32 s59,s95,7\n"
"s_add_u32 s96,s96,s59\n"
MF "v[148:151],a[124:127],v[188:191],v[148:151]\n"
"s_add_u32 s95,s95,3\n"
"s_cmp_ge_u32 s92,40\n"
"s_cselect_b32 s95,99,s95\n"
MF "v[148:151],a[164:167],a[240:243],v[148:151]\n"
LD "v194,s[48:51],s80 offen offset:2048 lds\n"
MF "v[148:151],a[204:207],a[244:247],v[148:151]\n"
"s_cmp_ge_u32 s85,5\n"
"s_cselect_b32 s95,99,s95\n"
"s_cselect_b32 s72,s85,0\n"
MF "v[140:143],a[200:203],a[244:247],v[140:143]\n"
MF "v[140:143],a[160:163],a[240:243],v[140:143]\n"
MF "v[140:143],a[120:123],v[188:191],v[140:143]\n"
"s_waitcnt lgkmcnt(0)\n"
LD "v195,s[48:51],s80 offen offset:3072 lds\n"
MF "v[96:99],a[20:23],v[176:179],v[96:99]\n"
TR "v[188:189],v1 offset:49152\n"
MF "v[96:99],a[60:63],v[180:183],v[96:99]\n"
TR "v[190:191],v1 offset:51200\n"
MF "v[96:99],a[100:103],v[184:187],v[96:99]\n"
MF "v[104:107],a[104:107],v[184:187],v[104:107]\n"
TR "a[240:241],v1 offset:53248\n"
MF "v[104:107],a[64:67],v[180:183],v[104:107]\n"
"s_add_u32 m0,s79,4096\n"
TR "a[242:243],v1 offset:55296\n"
MF "v[104:107],a[24:27],v[176:179],v[104:107]\n"
LD "v196,s[48:51],s80 offen lds\n"
MF "v[112:115],a[28:31],v[176:179],v[112:115]\n"
TR "a[244:245],v1 offset:57344\n"
MF "v[112:115],a[68:71],v[180:183],v[112:115]\n"
TR "a[246:247],v1 offset:59392\n"
MF "v[112:115],a[108:111],v[184:187],v[112:115]\n"
LD "v197,s[48:51],s80 offen offset:1024 lds\n"
MF "v[120:123],a[112:115],v[184:187],v[120:123]\n"
MF "v[120:123],a[72:75],v[180:183],v[120:123]\n"
MF "v[120:123],a[32:35],v[176:179],v[120:123]\n"
MF "v[128:131],a[36:39],v[176:179],v[128:131]\n"
MF "v[128:131],a[76:79],v[180:183],v[128:131]\n"
LD "v198,s[48:51],s80 offen offset:2048 lds\n"
MF "v[128:131],a[116:119],v[184:187],v[128:131]\n"
"s_waitcnt lgkmcnt(0)\n"
MF "v[132:135],a[116:119],a[244:247],v[132:135]\n"
TR "v[176:177],v2 offset:49152\n"
MF "v[132:135],a[76:79],a[240:243],v[132:135]\n"
TR "v[178:179],v2 offset:51200\n"
MF "v[132:135],a[36:39],v[188:191],v[132:135]\n"
LD "v199,s[48:51],s80 offen offset:3072 lds\n"
MF "v[124:127],a[32:35],v[188:191],v[124:127]\n"
TR "v[180:181],v2 offset:53248\n"
MF "v[124:127],a[72:75],a[240:243],v[124:127]\n"
TR "v[182:183],v2 offset:55296\n"
MF "v[124:127],a[112:115],a[244:247],v[124:127]\n"
MF "v[116:119],a[108:111],a[244:247],v[116:119]\n"
TR "v[184:185],v2 offset:57344\n"
MF "v[116:119],a[68:71],a[240:243],v[116:119]\n"
"s_add_u32 m0,s79,8192\n"
TR "v[186:187],v2 offset:59392\n"
MF "v[116:119],a[28:31],v[188:191],v[116:119]\n"
LD "v200,s[48:51],s80 offen lds\n"
MF "v[108:111],a[24:27],v[188:191],v[108:111]\n"
MF "v[108:111],a[64:67],a[240:243],v[108:111]\n"
MF "v[108:111],a[104:107],a[244:247],v[108:111]\n"
MF "v[100:103],a[100:103],a[244:247],v[100:103]\n"
LD "v201,s[48:51],s80 offen offset:1024 lds\n"
MF "v[100:103],a[60:63],a[240:243],v[100:103]\n"
"s_waitcnt vmcnt(19)\n"
"s_cmp_eq_u32 s83,0xf0000000\n"
MF "v[100:103],a[20:23],v[188:191],v[100:103]\n"
"s_cbranch_scc1 .Ljs5\n"
"s_cmp_eq_u32 s83,0xf0000010\n"
"s_cbranch_scc1 .Ljc5\n"
MF "v[248:251],v[210:213],a[250:253],0\n"
"v_mov_b32 v240,v208\n"
"v_mov_b32 v241,v209\n"
MF "v[244:247],v[214:217],a[250:253],0\n"
MF "v[248:251],v[218:221],a[250:253],v[248:251]\n"
MF "v[244:247],v[222:225],a[250:253],v[244:247]\n"
MF "v[248:251],v[226:229],a[250:253],v[248:251]\n"
MF "v[244:247],v[230:233],a[250:253],v[244:247]\n"
MF "v[248:251],v[234:237],a[250:253],v[248:251]\n"
MF "v[244:247],v[238:241],a[250:253],v[244:247]\n"
"s_branch .Ljs5\n"
".Ljc5:\n"
MF "v[96:99],a[252:255],v[208:211],v[96:99]\n"
MF "v[104:107],a[252:255],v[212:215],v[104:107]\n"
MF "v[112:115],a[252:255],v[216:219],v[112:115]\n"
MF "v[120:123],a[252:255],v[220:223],v[120:123]\n"
MF "v[128:131],a[252:255],v[224:227],v[128:131]\n"
MF "v[100:103],a[248:251],v[208:211],v[100:103]\n"
MF "v[108:111],a[248:251],v[212:215],v[108:111]\n"
MF "v[116:119],a[248:251],v[216:219],v[116:119]\n"
MF "v[124:127],a[248:251],v[220:223],v[124:127]\n"
MF "v[132:135],a[248:251],v[224:227],v[132:135]\n"
".Ljs5:\n"
"s_waitcnt lgkmcnt(0)\n"
MF "v[136:139],a[20:23],v[176:179],v[136:139]\n"
TR "v[188:189],v3 offset:49152\n"
"v_sub_u32 v10,v10,v11\n"
MF "v[136:139],a[60:63],v[180:183],v[136:139]\n"
"s_lshl_b32 s82,s92,10\n"
TR "v[190:191],v3 offset:51200\n"
"v_cmp_gt_u32 vcc,s95,v10\n"
MF "v[136:139],a[100:103],v[184:187],v[136:139]\n"
LD "v202,s[48:51],s80 offen offset:2048 lds\n"
MF "v[144:147],a[104:107],v[184:187],v[144:147]\n"
"s_add_u32 s82,s82,s93\n"
TR "a[240:241],v3 offset:53248\n"
CV "v244,v244,v245\n"
MF "v[144:147],a[64:67],v[180:183],v[144:147]\n"
TR "a[242:243],v3 offset:55296\n"
CV "v245,v246,v247\n"
MF "v[144:147],a[24:27],v[176:179],v[144:147]\n"
LD "v203,s[48:51],s80 offen offset:3072 lds\n"
MF "v[152:155],a[28:31],v[176:179],v[152:155]\n"
TR "a[244:245],v3 offset:57344\n"
CV "v246,v248,v249\n"
MF "v[152:155],a[68:71],v[180:183],v[152:155]\n"
TR "a[246:247],v3 offset:59392\n"
CV "v247,v250,v251\n"
MF "v[152:155],a[108:111],v[184:187],v[152:155]\n"
"s_cmp_eq_u64 vcc,0\n"
"s_cselect_b32 s82,s82,0xf0000000\n"
"s_cselect_b32 s84,s96,0xf0000000\n"
"s_cselect_b32 s59,32,0\n"
MF "v[160:163],a[112:115],v[184:187],v[160:163]\n"
"s_add_u32 s92,s92,s59\n"
".Lnf0:\n"
"v_readlane_b32 s58,v10,s67\n"
"s_cmp_lt_u32 s58,s72\n"
"s_cbranch_scc1 .Lsp0\n"
".Lnb0:\n"
MF "v[160:163],a[72:75],v[180:183],v[160:163]\n"
"s_cmp_ge_u32 s85,5\n"
"s_cselect_b32 s82,s70,s82\n"
"s_cselect_b32 s84,0xf0000010,s84\n"
MF "v[160:163],a[32:35],v[176:179],v[160:163]\n"
"s_cmp_ge_u32 s85,5\n"
"s_cselect_b32 s71,1024,s71\n"
"s_cselect_b32 s66,0x80000000,s66\n"
MF "v[168:171],a[36:39],v[176:179],v[168:171]\n"
"s_add_u32 s70,s70,20480\n"
"v_permlane16_swap_b32 v244,v246\n"
"v_permlane16_swap_b32 v245,v247\n"
MF "v[168:171],a[76:79],v[180:183],v[168:171]\n"
"buffer_store_dwordx4 v[244:247],v8,s[44:47],s83 offen sc1\n"
MF "v[168:171],a[116:119],v[184:187],v[168:171]\n"
"s_waitcnt vmcnt(26) lgkmcnt(0)\n"
"s_barrier\n"
MF "v[172:175],a[116:119],a[244:247],v[172:175]\n"
"s_add_u32 s88,s88,s65\n"
"s_mov_b32 s83,s84\n"
TR "v[176:177],v4\n"
MF "v[172:175],a[76:79],a[240:243],v[172:175]\n"
"s_add_u32 m0,s79,49152\n"
TR "v[178:179],v4 offset:2048\n"
MF "v[172:175],a[36:39],v[188:191],v[172:175]\n"
LD "v192,s[48:51],s88 offen lds\n"
MF "v[164:167],a[32:35],v[188:191],v[164:167]\n"
TR "v[180:181],v4 offset:4096\n"
MF "v[164:167],a[72:75],a[240:243],v[164:167]\n"
TR "v[182:183],v4 offset:6144\n"
MF "v[164:167],a[112:115],a[244:247],v[164:167]\n"
LD "v193,s[48:51],s88 offen offset:1024 lds\n"
MF "v[156:159],a[108:111],a[244:247],v[156:159]\n"
TR "v[184:185],v4 offset:8192\n"
MF "v[156:159],a[68:71],a[240:243],v[156:159]\n"
TR "v[186:187],v4 offset:10240\n"
MF "v[156:159],a[28:31],v[188:191],v[156:159]\n"
LD "v[208:211],v9,s[40:43],s82 offen sc1\n"
MF "v[148:151],a[24:27],v[188:191],v[148:151]\n"
MF "v[148:151],a[64:67],a[240:243],v[148:151]\n"
LD "v194,s[48:51],s88 offen offset:2048 lds\n"
MF "v[148:151],a[104:107],a[244:247],v[148:151]\n"
MF "v[140:143],a[100:103],a[244:247],v[140:143]\n"
MF "v[140:143],a[60:63],a[240:243],v[140:143]\n"
MF "v[140:143],a[20:23],v[188:191],v[140:143]\n"
"s_waitcnt lgkmcnt(0)\n"
LD "v195,s[48:51],s88 offen offset:3072 lds\n"
MF "v[96:99],a[140:143],v[176:179],v[96:99]\n"
TR "v[188:189],v5\n"
MF "v[96:99],a[180:183],v[180:183],v[96:99]\n"
"s_add_u32 s82,s82,s71\n"
TR "v[190:191],v5 offset:2048\n"
MF "v[96:99],a[220:223],v[184:187],v[96:99]\n"
LD "v[212:215],v9,s[40:43],s82 offen sc1\n"
MF "v[104:107],a[224:227],v[184:187],v[104:107]\n"
TR "a[240:241],v5 offset:4096\n"
MF "v[104:107],a[184:187],v[180:183],v[104:107]\n"
"s_add_u32 m0,s79,53248\n"
TR "a[242:243],v5 offset:6144\n"
MF "v[104:107],a[144:147],v[176:179],v[104:107]\n"
LD "v196,s[48:51],s88 offen lds\n"
MF "v[112:115],a[148:151],v[176:179],v[112:115]\n"
TR "a[244:245],v5 offset:8192\n"
MF "v[112:115],a[188:191],v[180:183],v[112:115]\n"
TR "a[246:247],v5 offset:10240\n"
MF "v[112:115],a[228:231],v[184:187],v[112:115]\n"
LD "v197,s[48:51],s88 offen offset:1024 lds\n"
MF "v[120:123],a[232:235],v[184:187],v[120:123]\n"
"s_add_u32 s82,s82,s71\n"
MF "v[120:123],a[192:195],v[180:183],v[120:123]\n"
LD "v[216:219],v9,s[40:43],s82 offen sc1\n"
MF "v[120:123],a[152:155],v[176:179],v[120:123]\n"
MF "v[128:131],a[156:159],v[176:179],v[128:131]\n"
MF "v[128:131],a[196:199],v[180:183],v[128:131]\n"
LD "v198,s[48:51],s88 offen offset:2048 lds\n"
MF "v[128:131],a[236:239],v[184:187],v[128:131]\n"
"s_waitcnt lgkmcnt(0)\n"
MF "v[132:135],a[236:239],a[244:247],v[132:135]\n"
TR "v[176:177],v6\n"
MF "v[132:135],a[196:199],a[240:243],v[132:135]\n"
TR "v[178:179],v6 offset:2048\n"
MF "v[132:135],a[156:159],v[188:191],v[132:135]\n"
LD "v199,s[48:51],s88 offen offset:3072 lds\n"
MF "v[124:127],a[152:155],v[188:191],v[124:127]\n"
TR "v[180:181],v6 offset:4096\n"
MF "v[124:127],a[192:195],a[240:243],v[124:127]\n"
"s_add_u32 s82,s82,s71\n"
TR "v[182:183],v6 offset:6144\n"
MF "v[124:127],a[232:235],a[244:247],v[124:127]\n"
LD "v[220:223],v9,s[40:43],s82 offen sc1\n"
MF "v[116:119],a[228:231],a[244:247],v[116:119]\n"
TR "v[184:185],v6 offset:8192\n"
MF "v[116:119],a[188:191],a[240:243],v[116:119]\n"
"s_add_u32 m0,s79,57344\n"
TR "v[186:187],v6 offset:10240\n"
MF "v[116:119],a[148:151],v[188:191],v[116:119]\n"
LD "v200,s[48:51],s88 offen lds\n"
MF "v[108:111],a[144:147],v[188:191],v[108:111]\n"
MF "v[108:111],a[184:187],a[240:243],v[108:111]\n"
"s_add_u32 s82,s82,s71\n"
MF "v[108:111],a[224:227],a[244:247],v[108:111]\n"
LD "v[224:227],v9,s[40:43],s82 offen sc1\n"
MF "v[100:103],a[220:223],a[244:247],v[100:103]\n"
LD "v201,s[48:51],s88 offen offset:1024 lds\n"
MF "v[100:103],a[180:183],a[240:243],v[100:103]\n"
MF "v[100:103],a[140:143],v[188:191],v[100:103]\n"
"s_waitcnt lgkmcnt(0)\n"
MF "v[136:139],a[140:143],v[176:179],v[136:139]\n"
TR "v[188:189],v7\n"
MF "v[136:139],a[180:183],v[180:183],v[136:139]\n"
TR "v[190:191],v7 offset:2048\n"
MF "v[136:139],a[220:223],v[184:187],v[136:139]\n"
LD "v202,s[48:51],s88 offen offset:2048 lds\n"
MF "v[144:147],a[224:227],v[184:187],v[144:147]\n"
TR "a[240:241],v7 offset:4096\n"
MF "v[144:147],a[184:187],v[180:183],v[144:147]\n"
TR "a[242:243],v7 offset:6144\n"
MF "v[144:147],a[144:147],v[176:179],v[144:147]\n"
LD "v203,s[48:51],s88 offen offset:3072 lds\n"
MF "v[152:155],a[148:151],v[176:179],v[152:155]\n"
TR "a[244:245],v7 offset:8192\n"
MF "v[152:155],a[188:191],v[180:183],v[152:155]\n"
"s_add_u32 s82,s82,s66\n"
TR "a[246:247],v7 offset:10240\n"
MF "v[152:155],a[228:231],v[184:187],v[152:155]\n"
LD "v[228:231],v9,s[40:43],s82 offen sc1\n"
MF "v[160:163],a[232:235],v[184:187],v[160:163]\n"
".Lpg22h:\n"
"s_cbranch_execz .Lpg25h\n"
MF "v[160:163],a[192:195],v[180:183],v[160:163]\n"
MF "v[160:163],a[152:155],v[176:179],v[160:163]\n"
MF "v[168:171],a[156:159],v[176:179],v[168:171]\n"
"s_add_u32 s82,s82,s71\n"
MF "v[168:171],a[196:199],v[180:183],v[168:171]\n"
LD "v[232:235],v9,s[40:43],s82 offen sc1\n"
MF "v[168:171],a[236:239],v[184:187],v[168:171]\n"
"s_waitcnt vmcnt(32) lgkmcnt(0)\n"
"s_barrier\n"
MF "v[172:175],a[236:239],a[244:247],v[172:175]\n"
"s_add_u32 s80,s88,122880\n"
TR "v[176:177],v0 offset:24576\n"
MF "v[172:175],a[196:199],a[240:243],v[172:175]\n"
"s_add_u32 m0,s79,98304\n"
"s_add_u32 s85,s85,1\n"
TR "v[178:179],v0 offset:26624\n"
MF "v[172:175],a[156:159],v[188:191],v[172:175]\n"
LD "v192,s[48:51],s80 offen lds\n"
MF "v[164:167],a[152:155],v[188:191],v[164:167]\n"
"s_nop 0\n"
TR "v[180:181],v0 offset:28672\n"
"ds_write_addtid_b32 v96 offset:24576\n"
MF "v[164:167],a[192:195],a[240:243],v[164:167]\n"
"s_nop 0\n"
TR "v[182:183],v0 offset:30720\n"
"ds_write_addtid_b32 v97 offset:24832\n"
MF "v[164:167],a[232:235],a[244:247],v[164:167]\n"
LD "v193,s[48:51],s80 offen offset:1024 lds\n"
MF "v[156:159],a[228:231],a[244:247],v[156:159]\n"
"s_nop 0\n"
TR "v[184:185],v0 offset:32768\n"
"ds_write_addtid_b32 v98 offset:25088\n"
MF "v[156:159],a[188:191],a[240:243],v[156:159]\n"
"s_add_u32 s82,s82,s71\n"
TR "v[186:187],v0 offset:34816\n"
"ds_write_addtid_b32 v99 offset:25344\n"
MF "v[156:159],a[148:151],v[188:191],v[156:159]\n"
LD "v[236:239],v9,s[40:43],s82 offen sc1\n"
MF "v[148:151],a[144:147],v[188:191],v[148:151]\n"
LD "v194,s[48:51],s80 offen offset:2048 lds\n"
MF "v[148:151],a[184:187],a[240:243],v[148:151]\n"
"s_nop 0\n"
"ds_write_addtid_b32 v100 offset:25600\n"
"ds_write_addtid_b32 v101 offset:25856\n"
MF "v[148:151],a[224:227],a[244:247],v[148:151]\n"
"s_nop 0\n"
"ds_write_addtid_b32 v102 offset:26112\n"
"ds_write_addtid_b32 v103 offset:26368\n"
MF "v[140:143],a[220:223],a[244:247],v[140:143]\n"
"s_nop 0\n"
"ds_write_addtid_b32 v104 offset:26624\n"
"ds_write_addtid_b32 v105 offset:26880\n"
MF "v[140:143],a[180:183],a[240:243],v[140:143]\n"
"s_nop 0\n"
"ds_write_addtid_b32 v106 offset:27136\n"
"ds_write_addtid_b32 v107 offset:27392\n"
MF "v[140:143],a[140:143],v[188:191],v[140:143]\n"
"s_waitcnt lgkmcnt(9)\n"
MF "v[16:19],a[0:3],v[176:179],v[252:255]\n"
"s_nop 0\n"
TR "v[188:189],v1 offset:24576\n"
"ds_write_addtid_b32 v108 offset:27648\n"
MF "v[16:19],a[40:43],v[180:183],v[16:19]\n"
"s_nop 0\n"
TR "v[190:191],v1 offset:26624\n"
"ds_write_addtid_b32 v109 offset:27904\n"
MF "v[16:19],a[80:83],v[184:187],v[16:19]\n"
LD "v195,s[48:51],s80 offen offset:3072 lds\n"
MF "v[24:27],a[84:87],v[184:187],v[252:255]\n"
"s_add_u32 s60,s64,1\n"
TR "a[240:241],v1 offset:28672\n"
"ds_write_addtid_b32 v110 offset:28160\n"
MF "v[24:27],a[44:47],v[180:183],v[24:27]\n"
"s_add_u32 m0,s79,102400\n"
TR "a[242:243],v1 offset:30720\n"
"ds_write_addtid_b32 v111 offset:24320\n"
MF "v[24:27],a[4:7],v[176:179],v[24:27]\n"
LD "v196,s[48:51],s80 offen lds\n"
MF "v[32:35],a[8:11],v[176:179],v[252:255]\n"
"s_cmp_eq_u32 s60,8\n"
"s_cselect_b32 s60,0,s60\n"
TR "a[244:245],v1 offset:32768\n"
MF "v[32:35],a[48:51],v[180:183],v[32:35]\n"
"s_cmp_eq_u32 s60,10\n"
"s_cselect_b32 s60,s61,s60\n"
TR "a[246:247],v1 offset:34816\n"
MF "v[32:35],a[88:91],v[184:187],v[32:35]\n"
"s_sub_u32 s65,s60,s64\n"
"ds_write_addtid_b32 v112 offset:24576\n"
"ds_write_addtid_b32 v113 offset:24832\n"
MF "v[40:43],a[92:95],v[184:187],v[252:255]\n"
"s_lshl_b32 s65,s65,7\n"
"ds_write_addtid_b32 v114 offset:25088\n"
"ds_write_addtid_b32 v115 offset:25344\n"
MF "v[40:43],a[52:55],v[180:183],v[40:43]\n"
LD "v197,s[48:51],s80 offen offset:1024 lds\n"
MF "v[40:43],a[12:15],v[176:179],v[40:43]\n"
"s_mov_b32 s64,s60\n"
"ds_write_addtid_b32 v116 offset:25600\n"
"ds_write_addtid_b32 v117 offset:25856\n"
MF "v[48:51],a[16:19],v[176:179],v[252:255]\n"
"s_nop 0\n"
"ds_write_addtid_b32 v118 offset:26112\n"
"ds_write_addtid_b32 v119 offset:26368\n"
MF "v[48:51],a[56:59],v[180:183],v[48:51]\n"
"s_nop 0\n"
"ds_write_addtid_b32 v120 offset:26624\n"
"ds_write_addtid_b32 v121 offset:26880\n"
MF "v[48:51],a[96:99],v[184:187],v[48:51]\n"
"s_waitcnt lgkmcnt(10)\n"
LD "v198,s[48:51],s80 offen offset:2048 lds\n"
MF "v[52:55],a[96:99],a[244:247],v[252:255]\n"
"s_nop 0\n"
TR "v[176:177],v2 offset:24576\n"
"ds_write_addtid_b32 v122 offset:27136\n"
MF "v[52:55],a[56:59],a[240:243],v[52:55]\n"
"s_nop 0\n"
TR "v[178:179],v2 offset:26624\n"
"ds_write_addtid_b32 v123 offset:27392\n"
MF "v[52:55],a[16:19],v[188:191],v[52:55]\n"
"s_nop 0\n"
"ds_write_addtid_b32 v124 offset:27648\n"
"ds_write_addtid_b32 v125 offset:27904\n"
MF "v[44:47],a[12:15],v[188:191],v[252:255]\n"
"s_nop 0\n"
TR "v[180:181],v2 offset:28672\n"
"ds_write_addtid_b32 v126 offset:28160\n"
MF "v[44:47],a[52:55],a[240:243],v[44:47]\n"
"s_nop 0\n"
TR "v[182:183],v2 offset:30720\n"
"ds_write_addtid_b32 v127 offset:28416\n"
MF "v[44:47],a[92:95],a[244:247],v[44:47]\n"
LD "v199,s[48:51],s80 offen offset:3072 lds\n"
MF "v[36:39],a[88:91],a[244:247],v[252:255]\n"
"s_nop 0\n"
TR "v[184:185],v2 offset:32768\n"
"ds_write_addtid_b32 v128 offset:28672\n"
MF "v[36:39],a[48:51],a[240:243],v[36:39]\n"
"s_add_u32 m0,s79,106496\n"
TR "v[186:187],v2 offset:34816\n"
"ds_write_addtid_b32 v129 offset:24832\n"
MF "v[36:39],a[8:11],v[188:191],v[36:39]\n"
LD "v200,s[48:51],s80 offen lds\n"
MF "v[28:31],a[4:7],v[188:191],v[252:255]\n"
"s_nop 0\n"
"ds_write_addtid_b32 v130 offset:25088\n"
"ds_write_addtid_b32 v131 offset:25344\n"
MF "v[28:31],a[44:47],a[240:243],v[28:31]\n"
"s_nop 0\n"
"ds_write_addtid_b32 v132 offset:25600\n"
"ds_write_addtid_b32 v133 offset:25856\n"
MF "v[28:31],a[84:87],a[244:247],v[28:31]\n"
"s_nop 0\n"
"ds_write_addtid_b32 v134 offset:26112\n"
"ds_write_addtid_b32 v135 offset:26368\n"
MF "v[20:23],a[80:83],a[244:247],v[252:255]\n"
MF "v[20:23],a[40:43],a[240:243],v[20:23]\n"
LD "v201,s[48:51],s80 offen offset:1024 lds\n"
MF "v[20:23],a[0:3],v[188:191],v[20:23]\n"
"s_waitcnt lgkmcnt(7)\n"
MF "v[56:59],a[0:3],v[176:179],v[252:255]\n"
TR "v[188:189],v3 offset:24576\n"
MF "v[56:59],a[40:43],v[180:183],v[56:59]\n"
TR "v[190:191],v3 offset:26624\n"
MF "v[56:59],a[80:83],v[184:187],v[56:59]\n"
LD "v202,s[48:51],s80 offen offset:2048 lds\n"
MF "v[64:67],a[84:87],v[184:187],v[252:255]\n"
TR "a[240:241],v3 offset:28672\n"
MF "v[64:67],a[44:47],v[180:183],v[64:67]\n"
TR "a[242:243],v3 offset:30720\n"
MF "v[64:67],a[4:7],v[176:179],v[64:67]\n"
LD "v203,s[48:51],s80 offen offset:3072 lds\n"
MF "v[72:75],a[8:11],v[176:179],v[252:255]\n"
TR "a[244:245],v3 offset:32768\n"
MF "v[72:75],a[48:51],v[180:183],v[72:75]\n"
TR "a[246:247],v3 offset:34816\n"
MF "v[72:75],a[88:91],v[184:187],v[72:75]\n"
MF "v[80:83],a[92:95],v[184:187],v[252:255]\n"
MF "v[80:83],a[52:55],v[180:183],v[80:83]\n"
MF "v[80:83],a[12:15],v[176:179],v[80:83]\n"
MF "v[88:91],a[16:19],v[176:179],v[252:255]\n"
MF "v[88:91],a[56:59],v[180:183],v[88:91]\n"
MF "v[88:91],a[96:99],v[184:187],v[88:91]\n"
"s_waitcnt vmcnt(33) lgkmcnt(0)\n"
"s_barrier\n"
MF "v[92:95],a[96:99],a[244:247],v[252:255]\n"
"s_add_u32 s89,s89,s65\n"
TR "v[176:177],v0\n"
MF "v[92:95],a[56:59],a[240:243],v[92:95]\n"
"s_add_u32 m0,s79,24576\n"
TR "v[178:179],v0 offset:2048\n"
MF "v[92:95],a[16:19],v[188:191],v[92:95]\n"
LD "v192,s[48:51],s89 offen lds\n"
MF "v[84:87],a[12:15],v[188:191],v[252:255]\n"
TR "v[180:181],v0 offset:4096\n"
MF "v[84:87],a[52:55],a[240:243],v[84:87]\n"
TR "v[182:183],v0 offset:6144\n"
MF "v[84:87],a[92:95],a[244:247],v[84:87]\n"
LD "v193,s[48:51],s89 offen offset:1024 lds\n"
MF "v[76:79],a[88:91],a[244:247],v[252:255]\n"
TR "v[184:185],v0 offset:8192\n"
MF "v[76:79],a[48:51],a[240:243],v[76:79]\n"
"s_nop 0\n"
TR "v[186:187],v0 offset:10240\n"
"ds_write_addtid_b32 v136 offset:49152\n"
MF "v[76:79],a[8:11],v[188:191],v[76:79]\n"
"s_nop 0\n"
"ds_write_addtid_b32 v137 offset:49408\n"
"ds_write_addtid_b32 v138 offset:49664\n"
MF "v[68:71],a[4:7],v[188:191],v[252:255]\n"
LD "v194,s[48:51],s89 offen offset:2048 lds\n"
MF "v[68:71],a[44:47],a[240:243],v[68:71]\n"
"buffer_load_dword v10,v12,s[40:43],s86 offen sc1\n"
MF "v[68:71],a[84:87],a[244:247],v[68:71]\n"
"s_nop 0\n"
"ds_write_addtid_b32 v139 offset:49920\n"
"ds_write_addtid_b32 v140 offset:50176\n"
MF "v[60:63],a[80:83],a[244:247],v[252:255]\n"
"s_nop 0\n"
"ds_write_addtid_b32 v141 offset:50432\n"
"ds_write_addtid_b32 v142 offset:50688\n"
MF "v[60:63],a[40:43],a[240:243],v[60:63]\n"
"s_nop 0\n"
"ds_write_addtid_b32 v143 offset:50944\n"
"ds_write_addtid_b32 v144 offset:51200\n"
MF "v[60:63],a[0:3],v[188:191],v[60:63]\n"
"s_waitcnt lgkmcnt(9)\n"
LD "v195,s[48:51],s89 offen offset:3072 lds\n"
MF "v[16:19],a[120:123],v[176:179],v[16:19]\n"
"s_nop 0\n"
TR "v[188:189],v1\n"
"ds_write_addtid_b32 v145 offset:51456\n"
MF "v[16:19],a[160:163],v[180:183],v[16:19]\n"
"s_nop 0\n"
TR "v[190:191],v1 offset:2048\n"
"ds_write_addtid_b32 v146 offset:51712\n"
MF "v[16:19],a[200:203],v[184:187],v[16:19]\n"
"s_nop 0\n"
"ds_write_addtid_b32 v147 offset:51968\n"
"ds_write_addtid_b32 v148 offset:52224\n"
MF "v[24:27],a[204:207],v[184:187],v[24:27]\n"
"s_nop 0\n"
TR "a[240:241],v1 offset:4096\n"
"ds_write_addtid_b32 v149 offset:52480\n"
MF "v[24:27],a[164:167],v[180:183],v[24:27]\n"
"s_add_u32 m0,s79,28672\n"
TR "a[242:243],v1 offset:6144\n"
"ds_write_addtid_b32 v150 offset:48640\n"
MF "v[24:27],a[124:127],v[176:179],v[24:27]\n"
LD "v196,s[48:51],s89 offen lds\n"
MF "v[32:35],a[128:131],v[176:179],v[32:35]\n"
"s_nop 0\n"
TR "a[244:245],v1 offset:8192\n"
"ds_write_addtid_b32 v151 offset:48896\n"
MF "v[32:35],a[168:171],v[180:183],v[32:35]\n"
"s_nop 0\n"
TR "a[246:247],v1 offset:10240\n"
"ds_write_addtid_b32 v152 offset:49152\n"
MF "v[32:35],a[208:211],v[184:187],v[32:35]\n"
LD "v197,s[48:51],s89 offen offset:1024 lds\n"
MF "v[40:43],a[212:215],v[184:187],v[40:43]\n"
"s_nop 0\n"
"ds_write_addtid_b32 v153 offset:49408\n"
"ds_write_addtid_b32 v154 offset:49664\n"
MF "v[40:43],a[172:175],v[180:183],v[40:43]\n"
"s_nop 0\n"
"ds_write_addtid_b32 v155 offset:49920\n"
"ds_write_addtid_b32 v156 offset:50176\n"
MF "v[40:43],a[132:135],v[176:179],v[40:43]\n"
"s_nop 0\n"
"ds_write_addtid_b32 v157 offset:50432\n"
"ds_write_addtid_b32 v158 offset:50688\n"
MF "v[48:51],a[136:139],v[176:179],v[48:51]\n"
"s_nop 0\n"
"ds_write_addtid_b32 v159 offset:50944\n"
"ds_write_addtid_b32 v160 offset:51200\n"
MF "v[48:51],a[176:179],v[180:183],v[48:51]\n"
LD "v198,s[48:51],s89 offen offset:2048 lds\n"
MF "v[48:51],a[216:219],v[184:187],v[48:51]\n"
"s_waitcnt lgkmcnt(9)\n"
"ds_write_addtid_b32 v161 offset:51456\n"
"ds_write_addtid_b32 v162 offset:51712\n"
MF "v[52:55],a[216:219],a[244:247],v[52:55]\n"
"s_nop 0\n"
TR "v[176:177],v2\n"
"ds_write_addtid_b32 v163 offset:51968\n"
MF "v[52:55],a[176:179],a[240:243],v[52:55]\n"
"s_nop 0\n"
TR "v[178:179],v2 offset:2048\n"
"ds_write_addtid_b32 v164 offset:52224\n"
MF "v[52:55],a[136:139],v[188:191],v[52:55]\n"
LD "v199,s[48:51],s89 offen offset:3072 lds\n"
MF "v[44:47],a[132:135],v[188:191],v[44:47]\n"
"s_nop 0\n"
TR "v[180:181],v2 offset:4096\n"
"ds_write_addtid_b32 v165 offset:52480\n"
MF "v[44:47],a[172:175],a[240:243],v[44:47]\n"
"s_nop 0\n"
TR "v[182:183],v2 offset:6144\n"
"ds_write_addtid_b32 v166 offset:52736\n"
MF "v[44:47],a[212:215],a[244:247],v[44:47]\n"
"s_nop 0\n"
"ds_write_addtid_b32 v167 offset:52992\n"
"ds_write_addtid_b32 v168 offset:53248\n"
MF "v[36:39],a[208:211],a[244:247],v[36:39]\n"
"s_nop 0\n"
TR "v[184:185],v2 offset:8192\n"
"ds_write_addtid_b32 v169 offset:53504\n"
MF "v[36:39],a[168:171],a[240:243],v[36:39]\n"
"s_add_u32 m0,s79,32768\n"
TR "v[186:187],v2 offset:10240\n"
"ds_write_addtid_b32 v170 offset:49664\n"
MF "v[36:39],a[128:131],v[188:191],v[36:39]\n"
LD "v200,s[48:51],s89 offen lds\n"
MF "v[28:31],a[124:127],v[188:191],v[28:31]\n"
"s_nop 0\n"
"ds_write_addtid_b32 v171 offset:49920\n"
"ds_write_addtid_b32 v172 offset:50176\n"
MF "v[28:31],a[164:167],a[240:243],v[28:31]\n"
"s_nop 0\n"
"ds_write_addtid_b32 v173 offset:50432\n"
"ds_write_addtid_b32 v174 offset:50688\n"
MF "v[28:31],a[204:207],a[244:247],v[28:31]\n"
"ds_write_addtid_b32 v175 offset:50944\n"
MF "v[20:23],a[200:203],a[244:247],v[20:23]\n"
LD "v201,s[48:51],s89 offen offset:1024 lds\n"
MF "v[20:23],a[160:163],a[240:243],v[20:23]\n"
MF "v[20:23],a[120:123],v[188:191],v[20:23]\n"
"s_waitcnt lgkmcnt(6)\n"
MF "v[56:59],a[120:123],v[176:179],v[56:59]\n"
TR "v[188:189],v3\n"
MF "v[56:59],a[160:163],v[180:183],v[56:59]\n"
TR "v[190:191],v3 offset:2048\n"
MF "v[56:59],a[200:203],v[184:187],v[56:59]\n"
LD "v202,s[48:51],s89 offen offset:2048 lds\n"
MF "v[64:67],a[204:207],v[184:187],v[64:67]\n"
TR "a[240:241],v3 offset:4096\n"
MF "v[64:67],a[164:167],v[180:183],v[64:67]\n"
TR "a[242:243],v3 offset:6144\n"
MF "v[64:67],a[124:127],v[176:179],v[64:67]\n"
LD "v203,s[48:51],s89 offen offset:3072 lds\n"
MF "v[72:75],a[128:131],v[176:179],v[72:75]\n"
TR "a[244:245],v3 offset:8192\n"
MF "v[72:75],a[168:171],v[180:183],v[72:75]\n"
TR "a[246:247],v3 offset:10240\n"
MF "v[72:75],a[208:211],v[184:187],v[72:75]\n"
MF "v[80:83],a[212:215],v[184:187],v[80:83]\n"
MF "v[80:83],a[172:175],v[180:183],v[80:83]\n"
MF "v[80:83],a[132:135],v[176:179],v[80:83]\n"
MF "v[88:91],a[136:139],v[176:179],v[88:91]\n"
MF "v[88:91],a[176:179],v[180:183],v[88:91]\n"
MF "v[88:91],a[216:219],v[184:187],v[88:91]\n"
"s_waitcnt vmcnt(28) lgkmcnt(0)\n"
"s_barrier\n"
MF "v[92:95],a[216:219],a[244:247],v[92:95]\n"
"s_add_u32 s80,s89,122880\n"
TR "v[176:177],v0 offset:49152\n"
MF "v[92:95],a[176:179],a[240:243],v[92:95]\n"
"s_add_u32 m0,s79,0\n"
"s_mul_i32 s95,s92,3277\n"
TR "v[178:179],v0 offset:51200\n"
MF "v[92:95],a[136:139],v[188:191],v[92:95]\n"
LD "v192,s[48:51],s80 offen lds\n"
MF "v[84:87],a[132:135],v[188:191],v[84:87]\n"
"s_lshr_b32 s95,s95,16\n"
"s_mul_i32 s59,s95,20\n"
TR "v[180:181],v0 offset:53248\n"
MF "v[84:87],a[172:175],a[240:243],v[84:87]\n"
"s_sub_u32 s59,s92,s59\n"
TR "v[182:183],v0 offset:55296\n"
MF "v[84:87],a[212:215],a[244:247],v[84:87]\n"
LD "v193,s[48:51],s80 offen offset:1024 lds\n"
MF "v[76:79],a[208:211],a[244:247],v[76:79]\n"
"s_cmp_ge_u32 s59,10\n"
"s_cselect_b32 s96,4294557760,0\n"
TR "v[184:185],v0 offset:57344\n"
MF "v[76:79],a[168:171],a[240:243],v[76:79]\n"
"s_add_u32 s96,s96,1024\n"
"s_mul_i32 s59,s59,40960\n"
TR "v[186:187],v0 offset:59392\n"
MF "v[76:79],a[128:131],v[188:191],v[76:79]\n"
"s_add_u32 s96,s96,s59\n"
"s_lshl_b32 s59,s95,7\n"
"s_add_u32 s96,s96,s59\n"
MF "v[68:71],a[124:127],v[188:191],v[68:71]\n"
"s_add_u32 s95,s95,3\n"
"s_cmp_ge_u32 s92,40\n"
"s_cselect_b32 s95,99,s95\n"
MF "v[68:71],a[164:167],a[240:243],v[68:71]\n"
LD "v194,s[48:51],s80 offen offset:2048 lds\n"
MF "v[68:71],a[204:207],a[244:247],v[68:71]\n"
"s_cmp_ge_u32 s85,5\n"
"s_cselect_b32 s95,99,s95\n"
"s_cselect_b32 s72,s85,0\n"
MF "v[60:63],a[200:203],a[244:247],v[60:63]\n"
MF "v[60:63],a[160:163],a[240:243],v[60:63]\n"
MF "v[60:63],a[120:123],v[188:191],v[60:63]\n"
"s_waitcnt lgkmcnt(0)\n"
LD "v195,s[48:51],s80 offen offset:3072 lds\n"
MF "v[16:19],a[20:23],v[176:179],v[16:19]\n"
TR "v[188:189],v1 offset:49152\n"
MF "v[16:19],a[60:63],v[180:183],v[16:19]\n"
TR "v[190:191],v1 offset:51200\n"
MF "v[16:19],a[100:103],v[184:187],v[16:19]\n"
MF "v[24:27],a[104:107],v[184:187],v[24:27]\n"
TR "a[240:241],v1 offset:53248\n"
MF "v[24:27],a[64:67],v[180:183],v[24:27]\n"
"s_add_u32 m0,s79,4096\n"
TR "a[242:243],v1 offset:55296\n"
MF "v[24:27],a[24:27],v[176:179],v[24:27]\n"
LD "v196,s[48:51],s80 offen lds\n"
MF "v[32:35],a[28:31],v[176:179],v[32:35]\n"
TR "a[244:245],v1 offset:57344\n"
MF "v[32:35],a[68:71],v[180:183],v[32:35]\n"
TR "a[246:247],v1 offset:59392\n"
MF "v[32:35],a[108:111],v[184:187],v[32:35]\n"
LD "v197,s[48:51],s80 offen offset:1024 lds\n"
MF "v[40:43],a[112:115],v[184:187],v[40:43]\n"
MF "v[40:43],a[72:75],v[180:183],v[40:43]\n"
MF "v[40:43],a[32:35],v[176:179],v[40:43]\n"
MF "v[48:51],a[36:39],v[176:179],v[48:51]\n"
MF "v[48:51],a[76:79],v[180:183],v[48:51]\n"
LD "v198,s[48:51],s80 offen offset:2048 lds\n"
MF "v[48:51],a[116:119],v[184:187],v[48:51]\n"
"s_waitcnt lgkmcnt(0)\n"
MF "v[52:55],a[116:119],a[244:247],v[52:55]\n"
TR "v[176:177],v2 offset:49152\n"
MF "v[52:55],a[76:79],a[240:243],v[52:55]\n"
TR "v[178:179],v2 offset:51200\n"
MF "v[52:55],a[36:39],v[188:191],v[52:55]\n"
LD "v199,s[48:51],s80 offen offset:3072 lds\n"
MF "v[44:47],a[32:35],v[188:191],v[44:47]\n"
TR "v[180:181],v2 offset:53248\n"
MF "v[44:47],a[72:75],a[240:243],v[44:47]\n"
TR "v[182:183],v2 offset:55296\n"
MF "v[44:47],a[112:115],a[244:247],v[44:47]\n"
MF "v[36:39],a[108:111],a[244:247],v[36:39]\n"
TR "v[184:185],v2 offset:57344\n"
MF "v[36:39],a[68:71],a[240:243],v[36:39]\n"
"s_add_u32 m0,s79,8192\n"
TR "v[186:187],v2 offset:59392\n"
MF "v[36:39],a[28:31],v[188:191],v[36:39]\n"
LD "v200,s[48:51],s80 offen lds\n"
MF "v[28:31],a[24:27],v[188:191],v[28:31]\n"
MF "v[28:31],a[64:67],a[240:243],v[28:31]\n"
MF "v[28:31],a[104:107],a[244:247],v[28:31]\n"
MF "v[20:23],a[100:103],a[244:247],v[20:23]\n"
LD "v201,s[48:51],s80 offen offset:1024 lds\n"
MF "v[20:23],a[60:63],a[240:243],v[20:23]\n"
"s_waitcnt vmcnt(19)\n"
"s_cmp_eq_u32 s83,0xf0000000\n"
MF "v[20:23],a[20:23],v[188:191],v[20:23]\n"
"s_cbranch_scc1 .Ljs7\n"
"s_cmp_eq_u32 s83,0xf0000010\n"
"s_cbranch_scc1 .Ljc7\n"
MF "v[248:251],v[210:213],a[250:253],0\n"
"v_mov_b32 v240,v208\n"
"v_mov_b32 v241,v209\n"
MF "v[244:247],v[214:217],a[250:253],0\n"
MF "v[248:251],v[218:221],a[250:253],v[248:251]\n"
MF "v[244:247],v[222:225],a[250:253],v[244:247]\n"
MF "v[248:251],v[226:229],a[250:253],v[248:251]\n"
MF "v[244:247],v[230:233],a[250:253],v[244:247]\n"
MF "v[248:251],v[234:237],a[250:253],v[248:251]\n"
MF "v[244:247],v[238:241],a[250:253],v[244:247]\n"
"s_branch .Ljs7\n"
".Ljc7:\n"
MF "v[16:19],a[252:255],v[208:211],v[16:19]\n"
MF "v[24:27],a[252:255],v[212:215],v[24:27]\n"
MF "v[32:35],a[252:255],v[216:219],v[32:35]\n"
MF "v[40:43],a[252:255],v[220:223],v[40:43]\n"
MF "v[48:51],a[252:255],v[224:227],v[48:51]\n"
MF "v[20:23],a[248:251],v[208:211],v[20:23]\n"
MF "v[28:31],a[248:251],v[212:215],v[28:31]\n"
MF "v[36:39],a[248:251],v[216:219],v[36:39]\n"
MF "v[44:47],a[248:251],v[220:223],v[44:47]\n"
MF "v[52:55],a[248:251],v[224:227],v[52:55]\n"
".Ljs7:\n"
"s_waitcnt lgkmcnt(0)\n"
MF "v[56:59],a[20:23],v[176:179],v[56:59]\n"
TR "v[188:189],v3 offset:49152\n"
"v_sub_u32 v10,v10,v11\n"
MF "v[56:59],a[60:63],v[180:183],v[56:59]\n"
"s_lshl_b32 s82,s92,10\n"
TR "v[190:191],v3 offset:51200\n"
"v_cmp_gt_u32 vcc,s95,v10\n"
MF "v[56:59],a[100:103],v[184:187],v[56:59]\n"
LD "v202,s[48:51],s80 offen offset:2048 lds\n"
MF "v[64:67],a[104:107],v[184:187],v[64:67]\n"
"s_add_u32 s82,s82,s93\n"
TR "a[240:241],v3 offset:53248\n"
CV "v244,v244,v245\n"
MF "v[64:67],a[64:67],v[180:183],v[64:67]\n"
TR "a[242:243],v3 offset:55296\n"
CV "v245,v246,v247\n"
MF "v[64:67],a[24:27],v[176:179],v[64:67]\n"
LD "v203,s[48:51],s80 offen offset:3072 lds\n"
MF "v[72:75],a[28:31],v[176:179],v[72:75]\n"
TR "a[244:245],v3 offset:57344\n"
CV "v246,v248,v249\n"
MF "v[72:75],a[68:71],v[180:183],v[72:75]\n"
TR "a[246:247],v3 offset:59392\n"
CV "v247,v250,v251\n"
MF "v[72:75],a[108:111],v[184:187],v[72:75]\n"
"s_cmp_eq_u64 vcc,0\n"
"s_cselect_b32 s82,s82,0xf0000000\n"
"s_cselect_b32 s84,s96,0xf0000000\n"
"s_cselect_b32 s59,32,0\n"
MF "v[80:83],a[112:115],v[184:187],v[80:83]\n"
"s_add_u32 s92,s92,s59\n"
".Lnf1:\n"
"v_readlane_b32 s58,v10,s67\n"
"s_cmp_lt_u32 s58,s72\n"
"s_cbranch_scc1 .Lsp1\n"
".Lnb1:\n"
MF "v[80:83],a[72:75],v[180:183],v[80:83]\n"
"s_cmp_ge_u32 s85,5\n"
"s_cselect_b32 s82,s70,s82\n"
"s_cselect_b32 s84,0xf0000010,s84\n"
MF "v[80:83],a[32:35],v[176:179],v[80:83]\n"
"s_cmp_ge_u32 s85,5\n"
"s_cselect_b32 s71,1024,s71\n"
"s_cselect_b32 s66,0x80000000,s66\n"
MF "v[88:91],a[36:39],v[176:179],v[88:91]\n"
"s_add_u32 s70,s70,20480\n"
"v_permlane16_swap_b32 v244,v246\n"
"v_permlane16_swap_b32 v245,v247\n"
MF "v[88:91],a[76:79],v[180:183],v[88:91]\n"
"buffer_store_dwordx4 v[244:247],v8,s[44:47],s83 offen sc1\n"
MF "v[88:91],a[116:119],v[184:187],v[88:91]\n"
"s_waitcnt vmcnt(26) lgkmcnt(0)\n"
"s_barrier\n"
MF "v[92:95],a[116:119],a[244:247],v[92:95]\n"
"s_add_u32 s88,s88,s65\n"
"s_mov_b32 s83,s84\n"
TR "v[176:177],v4\n"
MF "v[92:95],a[76:79],a[240:243],v[92:95]\n"
"s_add_u32 m0,s79,49152\n"
TR "v[178:179],v4 offset:2048\n"
MF "v[92:95],a[36:39],v[188:191],v[92:95]\n"
LD "v192,s[48:51],s88 offen lds\n"
MF "v[84:87],a[32:35],v[188:191],v[84:87]\n"
TR "v[180:181],v4 offset:4096\n"
MF "v[84:87],a[72:75],a[240:243],v[84:87]\n"
TR "v[182:183],v4 offset:6144\n"
MF "v[84:87],a[112:115],a[244:247],v[84:87]\n"
LD "v193,s[48:51],s88 offen offset:1024 lds\n"
MF "v[76:79],a[108:111],a[244:247],v[76:79]\n"
TR "v[184:185],v4 offset:8192\n"
MF "v[76:79],a[68:71],a[240:243],v[76:79]\n"
TR "v[186:187],v4 offset:10240\n"
MF "v[76:79],a[28:31],v[188:191],v[76:79]\n"
LD "v[208:211],v9,s[40:43],s82 offen sc1\n"
MF "v[68:71],a[24:27],v[188:191],v[68:71]\n"
MF "v[68:71],a[64:67],a[240:243],v[68:71]\n"
LD "v194,s[48:51],s88 offen offset:2048 lds\n"
MF "v[68:71],a[104:107],a[244:247],v[68:71]\n"
MF "v[60:63],a[100:103],a[244:247],v[60:63]\n"
MF "v[60:63],a[60:63],a[240:243],v[60:63]\n"
MF "v[60:63],a[20:23],v[188:191],v[60:63]\n"
"s_waitcnt lgkmcnt(0)\n"
LD "v195,s[48:51],s88 offen offset:3072 lds\n"
MF "v[16:19],a[140:143],v[176:179],v[16:19]\n"
TR "v[188:189],v5\n"
MF "v[16:19],a[180:183],v[180:183],v[16:19]\n"
"s_add_u32 s82,s82,s71\n"
TR "v[190:191],v5 offset:2048\n"
MF "v[16:19],a[220:223],v[184:187],v[16:19]\n"
LD "v[212:215],v9,s[40:43],s82 offen sc1\n"
MF "v[24:27],a[224:227],v[184:187],v[24:27]\n"
".Lpg23h:\n"
"s_cbranch_execz .Lpg26h\n"
TR "a[240:241],v5 offset:4096\n"
MF "v[24:27],a[184:187],v[180:183],v[24:27]\n"
"s_add_u32 m0,s79,53248\n"
TR "a[242:243],v5 offset:6144\n"
MF "v[24:27],a[144:147],v[176:179],v[24:27]\n"
LD "v196,s[48:51],s88 offen lds\n"
MF "v[32:35],a[148:151],v[176:179],v[32:35]\n"
TR "a[244:245],v5 offset:8192\n"
MF "v[32:35],a[188:191],v[180:183],v[32:35]\n"
TR "a[246:247],v5 offset:10240\n"
MF "v[32:35],a[228:231],v[184:187],v[32:35]\n"
LD "v197,s[48:51],s88 offen offset:1024 lds\n"
MF "v[40:43],a[232:235],v[184:187],v[40:43]\n"
"s_add_u32 s82,s82,s71\n"
MF "v[40:43],a[192:195],v[180:183],v[40:43]\n"
LD "v[216:219],v9,s[40:43],s82 offen sc1\n"
MF "v[40:43],a[152:155],v[176:179],v[40:43]\n"
MF "v[48:51],a[156:159],v[176:179],v[48:51]\n"
MF "v[48:51],a[196:199],v[180:183],v[48:51]\n"
LD "v198,s[48:51],s88 offen offset:2048 lds\n"
MF "v[48:51],a[236:239],v[184:187],v[48:51]\n"
"s_waitcnt lgkmcnt(0)\n"
MF "v[52:55],a[236:239],a[244:247],v[52:55]\n"
TR "v[176:177],v6\n"
MF "v[52:55],a[196:199],a[240:243],v[52:55]\n"
TR "v[178:179],v6 offset:2048\n"
MF "v[52:55],a[156:159],v[188:191],v[52:55]\n"
LD "v199,s[48:51],s88 offen offset:3072 lds\n"
MF "v[44:47],a[152:155],v[188:191],v[44:47]\n"
TR "v[180:181],v6 offset:4096\n"
MF "v[44:47],a[192:195],a[240:243],v[44:47]\n"
"s_add_u32 s82,s82,s71\n"
TR "v[182:183],v6 offset:6144\n"
MF "v[44:47],a[232:235],a[244:247],v[44:47]\n"
LD "v[220:223],v9,s[40:43],s82 offen sc1\n"
MF "v[36:39],a[228:231],a[244:247],v[36:39]\n"
TR "v[184:185],v6 offset:8192\n"
MF "v[36:39],a[188:191],a[240:243],v[36:39]\n"
"s_add_u32 m0,s79,57344\n"
TR "v[186:187],v6 offset:10240\n"
MF "v[36:39],a[148:151],v[188:191],v[36:39]\n"
LD "v200,s[48:51],s88 offen lds\n"
MF "v[28:31],a[144:147],v[188:191],v[28:31]\n"
MF "v[28:31],a[184:187],a[240:243],v[28:31]\n"
"s_add_u32 s82,s82,s71\n"
MF "v[28:31],a[224:227],a[244:247],v[28:31]\n"
LD "v[224:227],v9,s[40:43],s82 offen sc1\n"
MF "v[20:23],a[220:223],a[244:247],v[20:23]\n"
LD "v201,s[48:51],s88 offen offset:1024 lds\n"
MF "v[20:23],a[180:183],a[240:243],v[20:23]\n"
MF "v[20:23],a[140:143],v[188:191],v[20:23]\n"
"s_waitcnt lgkmcnt(0)\n"
MF "v[56:59],a[140:143],v[176:179],v[56:59]\n"
TR "v[188:189],v7\n"
MF "v[56:59],a[180:183],v[180:183],v[56:59]\n"
TR "v[190:191],v7 offset:2048\n"
MF "v[56:59],a[220:223],v[184:187],v[56:59]\n"
LD "v202,s[48:51],s88 offen offset:2048 lds\n"
MF "v[64:67],a[224:227],v[184:187],v[64:67]\n"
TR "a[240:241],v7 offset:4096\n"
MF "v[64:67],a[184:187],v[180:183],v[64:67]\n"
TR "a[242:243],v7 offset:6144\n"
MF "v[64:67],a[144:147],v[176:179],v[64:67]\n"
LD "v203,s[48:51],s88 offen offset:3072 lds\n"
MF "v[72:75],a[148:151],v[176:179],v[72:75]\n"
TR "a[244:245],v7 offset:8192\n"
MF "v[72:75],a[188:191],v[180:183],v[72:75]\n"
"s_add_u32 s82,s82,s66\n"
TR "a[246:247],v7 offset:10240\n"
MF "v[72:75],a[228:231],v[184:187],v[72:75]\n"
LD "v[228:231],v9,s[40:43],s82 offen sc1\n"
MF "v[80:83],a[232:235],v[184:187],v[80:83]\n"
MF "v[80:83],a[192:195],v[180:183],v[80:83]\n"
MF "v[80:83],a[152:155],v[176:179],v[80:83]\n"
MF "v[88:91],a[156:159],v[176:179],v[88:91]\n"
"s_add_u32 s82,s82,s71\n"
MF "v[88:91],a[196:199],v[180:183],v[88:91]\n"
LD "v[232:235],v9,s[40:43],s82 offen sc1\n"
MF "v[88:91],a[236:239],v[184:187],v[88:91]\n"
"s_waitcnt vmcnt(32) lgkmcnt(0)\n"
"s_barrier\n"
MF "v[92:95],a[236:239],a[244:247],v[92:95]\n"
"s_add_u32 s80,s88,122880\n"
TR "v[176:177],v0 offset:24576\n"
MF "v[92:95],a[196:199],a[240:243],v[92:95]\n"
"s_add_u32 m0,s79,98304\n"
"s_add_u32 s85,s85,1\n"
TR "v[178:179],v0 offset:26624\n"
MF "v[92:95],a[156:159],v[188:191],v[92:95]\n"
LD "v192,s[48:51],s80 offen lds\n"
MF "v[84:87],a[152:155],v[188:191],v[84:87]\n"
"s_nop 0\n"
TR "v[180:181],v0 offset:28672\n"
"ds_write_addtid_b32 v16 offset:24576\n"
MF "v[84:87],a[192:195],a[240:243],v[84:87]\n"
"s_nop 0\n"
TR "v[182:183],v0 offset:30720\n"
"ds_write_addtid_b32 v17 offset:24832\n"
MF "v[84:87],a[232:235],a[244:247],v[84:87]\n"
LD "v193,s[48:51],s80 offen offset:1024 lds\n"
MF "v[76:79],a[228:231],a[244:247],v[76:79]\n"
"s_nop 0\n"
TR "v[184:185],v0 offset:32768\n"
"ds_write_addtid_b32 v18 offset:25088\n"
MF "v[76:79],a[188:191],a[240:243],v[76:79]\n"
"s_add_u32 s82,s82,s71\n"
TR "v[186:187],v0 offset:34816\n"
"ds_write_addtid_b32 v19 offset:25344\n"
MF "v[76:79],a[148:151],v[188:191],v[76:79]\n"
LD "v[236:239],v9,s[40:43],s82 offen sc1\n"
MF "v[68:71],a[144:147],v[188:191],v[68:71]\n"
LD "v194,s[48:51],s80 offen offset:2048 lds\n"
MF "v[68:71],a[184:187],a[240:243],v[68:71]\n"
"s_nop 0\n"
"ds_write_addtid_b32 v20 offset:25600\n"
"ds_write_addtid_b32 v21 offset:25856\n"
MF "v[68:71],a[224:227],a[244:247],v[68:71]\n"
"s_nop 0\n"
"ds_write_addtid_b32 v22 offset:26112\n"
"ds_write_addtid_b32 v23 offset:26368\n"
MF "v[60:63],a[220:223],a[244:247],v[60:63]\n"
"s_nop 0\n"
"ds_write_addtid_b32 v24 offset:26624\n"
"ds_write_addtid_b32 v25 offset:26880\n"
MF "v[60:63],a[180:183],a[240:243],v[60:63]\n"
"s_nop 0\n"
"ds_write_addtid_b32 v26 offset:27136\n"
"ds_write_addtid_b32 v27 offset:27392\n"
MF "v[60:63],a[140:143],v[188:191],v[60:63]\n"
"s_waitcnt lgkmcnt(9)\n"
"s_cmp_lg_u32 s85,9\n"
"s_cbranch_scc1 .Lpl0\n"
".Lplast0:\n"
MF "v[96:99],v[176:179],a[0:3],v[252:255]\n"
"s_nop 0\n"
TR "v[188:189],v1 offset:24576\n"
TR "v[190:191],v1 offset:26624\n"
MF "v[96:99],v[180:183],a[40:43],v[96:99]\n"
"s_nop 0\n"
TR "a[240:241],v1 offset:28672\n"
TR "a[242:243],v1 offset:30720\n"
MF "v[96:99],v[184:187],a[80:83],v[96:99]\n"
"s_add_u32 s90,s91,163840\n"
TR "a[244:245],v1 offset:32768\n"
TR "a[246:247],v1 offset:34816\n"
MF "v[104:107],v[184:187],a[84:87],v[252:255]\n"
LD "v195,s[48:51],s80 offen offset:3072 lds\n"
MF "v[104:107],v[180:183],a[44:47],v[104:107]\n"
"s_nop 0\n"
"ds_write_addtid_b32 v28 offset:27648\n"
"ds_write_addtid_b32 v29 offset:27904\n"
MF "v[104:107],v[176:179],a[4:7],v[104:107]\n"
"s_nop 0\n"
"ds_write_addtid_b32 v30 offset:28160\n"
"ds_write_addtid_b32 v31 offset:28416\n"
MF "v[112:115],v[176:179],a[8:11],v[252:255]\n"
"s_nop 0\n"
"ds_write_addtid_b32 v32 offset:28672\n"
"ds_write_addtid_b32 v33 offset:28928\n"
MF "v[112:115],v[180:183],a[48:51],v[112:115]\n"
"s_add_u32 m0,s79,102400\n"
"s_nop 0\n"
"ds_write_addtid_b32 v34 offset:25088\n"
"ds_write_addtid_b32 v35 offset:25344\n"
MF "v[112:115],v[184:187],a[88:91],v[112:115]\n"
LD "v196,s[48:51],s80 offen lds\n"
MF "v[120:123],v[184:187],a[92:95],v[252:255]\n"
"s_nop 0\n"
"ds_write_addtid_b32 v36 offset:25600\n"
"ds_write_addtid_b32 v37 offset:25856\n"
MF "v[120:123],v[180:183],a[52:55],v[120:123]\n"
"ds_write_addtid_b32 v38 offset:26112\n"
MF "v[120:123],v[176:179],a[12:15],v[120:123]\n"
"ds_write_addtid_b32 v39 offset:26368\n"
MF "v[128:131],v[176:179],a[16:19],v[252:255]\n"
"ds_write_addtid_b32 v40 offset:26624\n"
MF "v[128:131],v[180:183],a[56:59],v[128:131]\n"
"ds_write_addtid_b32 v41 offset:26880\n"
MF "v[128:131],v[184:187],a[96:99],v[128:131]\n"
"s_waitcnt lgkmcnt(14)\n"
LD "v197,s[48:51],s80 offen offset:1024 lds\n"
MF "v[132:135],a[244:247],a[96:99],v[252:255]\n"
"s_nop 0\n"
TR "v[176:177],v2 offset:24576\n"
TR "v[178:179],v2 offset:26624\n"
MF "v[132:135],a[240:243],a[56:59],v[132:135]\n"
"s_nop 0\n"
TR "v[180:181],v2 offset:28672\n"
TR "v[182:183],v2 offset:30720\n"
MF "v[132:135],v[188:191],a[16:19],v[132:135]\n"
"s_nop 0\n"
TR "v[184:185],v2 offset:32768\n"
TR "v[186:187],v2 offset:34816\n"
MF "v[124:127],v[188:191],a[12:15],v[252:255]\n"
"s_nop 0\n"
"ds_write_addtid_b32 v42 offset:27136\n"
"ds_write_addtid_b32 v43 offset:27392\n"
MF "v[124:127],a[240:243],a[52:55],v[124:127]\n"
"s_nop 0\n"
"ds_write_addtid_b32 v44 offset:27648\n"
"ds_write_addtid_b32 v45 offset:27904\n"
MF "v[124:127],a[244:247],a[92:95],v[124:127]\n"
"s_nop 0\n"
"ds_write_addtid_b32 v46 offset:28160\n"
"ds_write_addtid_b32 v47 offset:28416\n"
MF "v[116:119],a[244:247],a[88:91],v[252:255]\n"
LD "v198,s[48:51],s80 offen offset:2048 lds\n"
MF "v[116:119],a[240:243],a[48:51],v[116:119]\n"
"s_nop 0\n"
"ds_write_addtid_b32 v48 offset:28672\n"
"ds_write_addtid_b32 v49 offset:28928\n"
MF "v[116:119],v[188:191],a[8:11],v[116:119]\n"
"s_nop 0\n"
"ds_write_addtid_b32 v50 offset:29184\n"
"ds_write_addtid_b32 v51 offset:29440\n"
MF "v[108:111],v[188:191],a[4:7],v[252:255]\n"
"ds_write_addtid_b32 v52 offset:29696\n"
MF "v[108:111],a[240:243],a[44:47],v[108:111]\n"
"ds_write_addtid_b32 v53 offset:29952\n"
MF "v[108:111],a[244:247],a[84:87],v[108:111]\n"
"ds_write_addtid_b32 v54 offset:30208\n"
MF "v[100:103],a[244:247],a[80:83],v[252:255]\n"
"ds_write_addtid_b32 v55 offset:30464\n"
MF "v[100:103],a[240:243],a[40:43],v[100:103]\n"
LD "v199,s[48:51],s80 offen offset:3072 lds\n"
MF "v[100:103],v[188:191],a[0:3],v[100:103]\n"
"s_waitcnt lgkmcnt(14)\n"
MF "v[136:139],v[176:179],a[0:3],v[252:255]\n"
"s_nop 0\n"
TR "v[188:189],v3 offset:24576\n"
TR "v[190:191],v3 offset:26624\n"
MF "v[136:139],v[180:183],a[40:43],v[136:139]\n"
"s_nop 0\n"
TR "a[240:241],v3 offset:28672\n"
TR "a[242:243],v3 offset:30720\n"
MF "v[136:139],v[184:187],a[80:83],v[136:139]\n"
"s_add_u32 m0,s79,106496\n"
TR "a[244:245],v3 offset:32768\n"
TR "a[246:247],v3 offset:34816\n"
MF "v[144:147],v[184:187],a[84:87],v[252:255]\n"
"s_waitcnt lgkmcnt(0)\n"
"s_barrier\n"
MF "v[144:147],v[180:183],a[44:47],v[144:147]\n"
LD "v200,s[48:51],s80 offen lds\n"
MF "v[144:147],v[176:179],a[4:7],v[144:147]\n"
"ds_read2st64_b32 v[16:17],v13 offset0:0 offset1:1\n"
MF "v[152:155],v[176:179],a[8:11],v[252:255]\n"
"ds_read2st64_b32 v[18:19],v13 offset0:2 offset1:3\n"
MF "v[152:155],v[180:183],a[48:51],v[152:155]\n"
"ds_read2st64_b32 v[20:21],v13 offset0:4 offset1:5\n"
MF "v[152:155],v[184:187],a[88:91],v[152:155]\n"
"ds_read2st64_b32 v[22:23],v13 offset0:6 offset1:7\n"
MF "v[160:163],v[184:187],a[92:95],v[252:255]\n"
"ds_read2st64_b32 v[24:25],v13 offset0:8 offset1:9\n"
MF "v[160:163],v[180:183],a[52:55],v[160:163]\n"
"ds_read2st64_b32 v[26:27],v13 offset0:10 offset1:11\n"
MF "v[160:163],v[176:179],a[12:15],v[160:163]\n"
LD "v201,s[48:51],s80 offen offset:1024 lds\n"
MF "v[168:171],v[176:179],a[16:19],v[252:255]\n"
"ds_read2st64_b32 v[28:29],v13 offset0:12 offset1:13\n"
MF "v[168:171],v[180:183],a[56:59],v[168:171]\n"
"s_waitcnt lgkmcnt(3)\n"
"ds_read2st64_b32 v[30:31],v13 offset0:14 offset1:15\n"
"v_add_f32 v56,v56,v16\n"
MF "v[168:171],v[184:187],a[96:99],v[168:171]\n"
"s_waitcnt vmcnt(31) lgkmcnt(0)\n"
"s_barrier\n"
MF "v[172:175],a[244:247],a[96:99],v[252:255]\n"
"s_nop 0\n"
TR "v[176:177],v0\n"
TR "v[178:179],v0 offset:2048\n"
MF "v[172:175],a[240:243],a[56:59],v[172:175]\n"
"s_nop 0\n"
TR "v[180:181],v0 offset:4096\n"
TR "v[182:183],v0 offset:6144\n"
MF "v[172:175],v[188:191],a[16:19],v[172:175]\n"
"s_nop 0\n"
TR "v[184:185],v0 offset:8192\n"
TR "v[186:187],v0 offset:10240\n"
MF "v[164:167],v[188:191],a[12:15],v[252:255]\n"
LD "v202,s[48:51],s80 offen offset:2048 lds\n"
MF "v[164:167],a[240:243],a[52:55],v[164:167]\n"
"ds_read2st64_b32 v[32:33],v13 offset0:16 offset1:17\n"
"v_add_f32 v57,v57,v17\n"
MF "v[164:167],a[244:247],a[92:95],v[164:167]\n"
"ds_read2st64_b32 v[34:35],v13 offset0:18 offset1:19\n"
"v_add_f32 v58,v58,v18\n"
MF "v[156:159],a[244:247],a[88:91],v[252:255]\n"
"ds_read2st64_b32 v[36:37],v13 offset0:20 offset1:21\n"
"v_add_f32 v59,v59,v19\n"
MF "v[156:159],a[240:243],a[48:51],v[156:159]\n"
"ds_read2st64_b32 v[38:39],v13 offset0:22 offset1:23\n"
"v_add_f32 v60,v60,v20\n"
MF "v[156:159],v[188:191],a[8:11],v[156:159]\n"
"ds_read2st64_b32 v[40:41],v13 offset0:24 offset1:25\n"
"v_add_f32 v61,v61,v21\n"
MF "v[148:151],v[188:191],a[4:7],v[252:255]\n"
LD "v203,s[48:51],s80 offen offset:3072 lds\n"
MF "v[148:151],a[240:243],a[44:47],v[148:151]\n"
"ds_read2st64_b32 v[42:43],v13 offset0:26 offset1:27\n"
"v_add_f32 v62,v62,v22\n"
MF "v[148:151],a[244:247],a[84:87],v[148:151]\n"
"ds_read2st64_b32 v[44:45],v13 offset0:28 offset1:29\n"
"v_add_f32 v63,v63,v23\n"
MF "v[140:143],a[244:247],a[80:83],v[252:255]\n"
"ds_read2st64_b32 v[46:47],v13 offset0:30 offset1:31\n"
CV "v56,v56,v57\n"
MF "v[140:143],a[240:243],a[40:43],v[140:143]\n"
"ds_read2st64_b32 v[48:49],v13 offset0:32 offset1:33\n"
CV "v57,v58,v59\n"
MF "v[140:143],v[188:191],a[0:3],v[140:143]\n"
"s_waitcnt lgkmcnt(9)\n"
"ds_read2st64_b32 v[50:51],v13 offset0:34 offset1:35\n"
CV "v58,v60,v61\n"
MF "v[96:99],v[176:179],a[120:123],v[96:99]\n"
"s_nop 0\n"
TR "v[188:189],v1\n"
TR "v[190:191],v1 offset:2048\n"
MF "v[96:99],v[180:183],a[160:163],v[96:99]\n"
"s_nop 0\n"
TR "a[240:241],v1 offset:4096\n"
TR "a[242:243],v1 offset:6144\n"
MF "v[96:99],v[184:187],a[200:203],v[96:99]\n"
"s_nop 0\n"
TR "a[244:245],v1 offset:8192\n"
TR "a[246:247],v1 offset:10240\n"
MF "v[104:107],v[184:187],a[204:207],v[104:107]\n"
"ds_read2st64_b32 v[52:53],v13 offset0:36 offset1:37\n"
CV "v59,v62,v63\n"
MF "v[104:107],v[180:183],a[164:167],v[104:107]\n"
"s_add_u32 s87,s90,10240\n"
"ds_read2st64_b32 v[54:55],v13 offset0:38 offset1:39\n"
"v_add_f32 v64,v64,v24\n"
MF "v[104:107],v[176:179],a[124:127],v[104:107]\n"
"buffer_store_dwordx4 v[56:59],v9,s[40:43],s87 offen sc1\n"
MF "v[112:115],v[176:179],a[128:131],v[112:115]\n"
"v_add_f32 v65,v65,v25\n"
"v_add_f32 v66,v66,v26\n"
MF "v[112:115],v[180:183],a[168:171],v[112:115]\n"
"v_add_f32 v67,v67,v27\n"
"v_add_f32 v68,v68,v28\n"
MF "v[112:115],v[184:187],a[208:211],v[112:115]\n"
"v_add_f32 v69,v69,v29\n"
"v_add_f32 v70,v70,v30\n"
MF "v[120:123],v[184:187],a[212:215],v[120:123]\n"
"v_add_f32 v71,v71,v31\n"
CV "v64,v64,v65\n"
MF "v[120:123],v[180:183],a[172:175],v[120:123]\n"
CV "v65,v66,v67\n"
CV "v66,v68,v69\n"
MF "v[120:123],v[176:179],a[132:135],v[120:123]\n"
"s_add_u32 s87,s90,11264\n"
"s_waitcnt lgkmcnt(14)\n"
CV "v67,v70,v71\n"
MF "v[128:131],v[176:179],a[136:139],v[128:131]\n"
"buffer_store_dwordx4 v[64:67],v9,s[40:43],s87 offen sc1\n"
MF "v[128:131],v[180:183],a[176:179],v[128:131]\n"
"v_add_f32 v72,v72,v32\n"
"v_add_f32 v73,v73,v33\n"
MF "v[128:131],v[184:187],a[216:219],v[128:131]\n"
"s_waitcnt lgkmcnt(2)\n"
"v_add_f32 v74,v74,v34\n"
"v_add_f32 v75,v75,v35\n"
MF "v[132:135],a[244:247],a[216:219],v[132:135]\n"
"s_nop 0\n"
TR "v[176:177],v2\n"
TR "v[178:179],v2 offset:2048\n"
MF "v[132:135],a[240:243],a[176:179],v[132:135]\n"
"s_nop 0\n"
TR "v[180:181],v2 offset:4096\n"
TR "v[182:183],v2 offset:6144\n"
MF "v[132:135],v[188:191],a[136:139],v[132:135]\n"
"s_nop 0\n"
TR "v[184:185],v2 offset:8192\n"
TR "v[186:187],v2 offset:10240\n"
MF "v[124:127],v[188:191],a[132:135],v[124:127]\n"
"v_add_f32 v76,v76,v36\n"
"v_add_f32 v77,v77,v37\n"
MF "v[124:127],a[240:243],a[172:175],v[124:127]\n"
"v_add_f32 v78,v78,v38\n"
"v_add_f32 v79,v79,v39\n"
MF "v[124:127],a[244:247],a[212:215],v[124:127]\n"
CV "v72,v72,v73\n"
CV "v73,v74,v75\n"
MF "v[116:119],a[244:247],a[208:211],v[116:119]\n"
"s_add_u32 s87,s90,12288\n"
CV "v74,v76,v77\n"
CV "v75,v78,v79\n"
MF "v[116:119],a[240:243],a[168:171],v[116:119]\n"
"buffer_store_dwordx4 v[72:75],v9,s[40:43],s87 offen sc1\n"
MF "v[116:119],v[188:191],a[128:131],v[116:119]\n"
"v_add_f32 v80,v80,v40\n"
"v_add_f32 v81,v81,v41\n"
MF "v[108:111],v[188:191],a[124:127],v[108:111]\n"
"v_add_f32 v82,v82,v42\n"
"v_add_f32 v83,v83,v43\n"
MF "v[108:111],a[240:243],a[164:167],v[108:111]\n"
"v_add_f32 v84,v84,v44\n"
"v_add_f32 v85,v85,v45\n"
MF "v[108:111],a[244:247],a[204:207],v[108:111]\n"
"v_add_f32 v86,v86,v46\n"
"v_add_f32 v87,v87,v47\n"
MF "v[100:103],a[244:247],a[200:203],v[100:103]\n"
CV "v80,v80,v81\n"
CV "v81,v82,v83\n"
MF "v[100:103],a[240:243],a[160:163],v[100:103]\n"
"s_add_u32 s87,s90,13312\n"
CV "v82,v84,v85\n"
CV "v83,v86,v87\n"
MF "v[100:103],v[188:191],a[120:123],v[100:103]\n"
"s_waitcnt lgkmcnt(0)\n"
"buffer_store_dwordx4 v[80:83],v9,s[40:43],s87 offen sc1\n"
MF "v[136:139],v[176:179],a[120:123],v[136:139]\n"
"s_nop 0\n"
TR "v[188:189],v3\n"
TR "v[190:191],v3 offset:2048\n"
MF "v[136:139],v[180:183],a[160:163],v[136:139]\n"
"s_nop 0\n"
TR "a[240:241],v3 offset:4096\n"
TR "a[242:243],v3 offset:6144\n"
MF "v[136:139],v[184:187],a[200:203],v[136:139]\n"
"s_nop 0\n"
TR "a[244:245],v3 offset:8192\n"
TR "a[246:247],v3 offset:10240\n"
MF "v[144:147],v[184:187],a[204:207],v[144:147]\n"
"v_add_f32 v88,v88,v48\n"
"v_add_f32 v89,v89,v49\n"
MF "v[144:147],v[180:183],a[164:167],v[144:147]\n"
"v_add_f32 v90,v90,v50\n"
"v_add_f32 v91,v91,v51\n"
MF "v[144:147],v[176:179],a[124:127],v[144:147]\n"
"v_add_f32 v92,v92,v52\n"
"v_add_f32 v93,v93,v53\n"
MF "v[152:155],v[176:179],a[128:131],v[152:155]\n"
"v_add_f32 v94,v94,v54\n"
"v_add_f32 v95,v95,v55\n"
MF "v[152:155],v[180:183],a[168:171],v[152:155]\n"
CV "v88,v88,v89\n"
CV "v89,v90,v91\n"
MF "v[152:155],v[184:187],a[208:211],v[152:155]\n"
"s_add_u32 s87,s90,14336\n"
CV "v90,v92,v93\n"
CV "v91,v94,v95\n"
MF "v[160:163],v[184:187],a[212:215],v[160:163]\n"
"buffer_store_dwordx4 v[88:91],v9,s[40:43],s87 offen sc1\n"
MF "v[160:163],v[180:183],a[172:175],v[160:163]\n"
MF "v[160:163],v[176:179],a[132:135],v[160:163]\n"
MF "v[168:171],v[176:179],a[136:139],v[168:171]\n"
MF "v[168:171],v[180:183],a[176:179],v[168:171]\n"
MF "v[168:171],v[184:187],a[216:219],v[168:171]\n"
"s_waitcnt vmcnt(20) lgkmcnt(0)\n"
"s_barrier\n"
MF "v[172:175],a[244:247],a[216:219],v[172:175]\n"
"s_nop 0\n"
TR "v[176:177],v0 offset:49152\n"
TR "v[178:179],v0 offset:51200\n"
MF "v[172:175],a[240:243],a[176:179],v[172:175]\n"
"s_nop 0\n"
TR "v[180:181],v0 offset:53248\n"
TR "v[182:183],v0 offset:55296\n"
MF "v[172:175],v[188:191],a[136:139],v[172:175]\n"
"s_nop 0\n"
TR "v[184:185],v0 offset:57344\n"
TR "v[186:187],v0 offset:59392\n"
MF "v[164:167],v[188:191],a[132:135],v[164:167]\n"
"buffer_load_dword v10,v12,s[40:43],s86 offen sc1\n"
MF "v[164:167],a[240:243],a[172:175],v[164:167]\n"
MF "v[164:167],a[244:247],a[212:215],v[164:167]\n"
MF "v[156:159],a[244:247],a[208:211],v[156:159]\n"
MF "v[156:159],a[240:243],a[168:171],v[156:159]\n"
MF "v[156:159],v[188:191],a[128:131],v[156:159]\n"
MF "v[148:151],v[188:191],a[124:127],v[148:151]\n"
MF "v[148:151],a[240:243],a[164:167],v[148:151]\n"
MF "v[148:151],a[244:247],a[204:207],v[148:151]\n"
MF "v[140:143],a[244:247],a[200:203],v[140:143]\n"
MF "v[140:143],a[240:243],a[160:163],v[140:143]\n"
MF "v[140:143],v[188:191],a[120:123],v[140:143]\n"
"s_waitcnt lgkmcnt(0)\n"
MF "v[96:99],v[176:179],a[20:23],v[96:99]\n"
"s_nop 0\n"
TR "v[188:189],v1 offset:49152\n"
TR "v[190:191],v1 offset:51200\n"
MF "v[96:99],v[180:183],a[60:63],v[96:99]\n"
"s_nop 0\n"
TR "a[240:241],v1 offset:53248\n"
TR "a[242:243],v1 offset:55296\n"
MF "v[96:99],v[184:187],a[100:103],v[96:99]\n"
"s_nop 0\n"
TR "a[244:245],v1 offset:57344\n"
TR "a[246:247],v1 offset:59392\n"
MF "v[104:107],v[184:187],a[104:107],v[104:107]\n"
MF "v[104:107],v[180:183],a[64:67],v[104:107]\n"
MF "v[104:107],v[176:179],a[24:27],v[104:107]\n"
"s_waitcnt vmcnt(16)\n"
MF "v[96:99],v[208:211],a[252:255],v[96:99]\n"
MF "v[104:107],v[212:215],a[252:255],v[104:107]\n"
MF "v[112:115],v[216:219],a[252:255],v[112:115]\n"
MF "v[120:123],v[220:223],a[252:255],v[120:123]\n"
MF "v[128:131],v[224:227],a[252:255],v[128:131]\n"
MF "v[100:103],v[208:211],a[248:251],v[100:103]\n"
MF "v[108:111],v[212:215],a[248:251],v[108:111]\n"
MF "v[116:119],v[216:219],a[248:251],v[116:119]\n"
MF "v[124:127],v[220:223],a[248:251],v[124:127]\n"
MF "v[132:135],v[224:227],a[248:251],v[132:135]\n"
MF "v[112:115],v[176:179],a[28:31],v[112:115]\n"
MF "v[112:115],v[180:183],a[68:71],v[112:115]\n"
MF "v[112:115],v[184:187],a[108:111],v[112:115]\n"
MF "v[120:123],v[184:187],a[112:115],v[120:123]\n"
MF "v[120:123],v[180:183],a[72:75],v[120:123]\n"
"s_bfe_u32 s87,s22,0x30014\n"
MF "v[120:123],v[176:179],a[32:35],v[120:123]\n"
"s_lshl_b32 s87,s87,7\n"
MF "v[128:131],v[176:179],a[36:39],v[128:131]\n"
"s_and_b32 s60,s56,1\n"
MF "v[128:131],v[180:183],a[76:79],v[128:131]\n"
"s_mul_i32 s60,s60,204800\n"
MF "v[128:131],v[184:187],a[116:119],v[128:131]\n"
"s_waitcnt lgkmcnt(0)\n"
MF "v[132:135],a[244:247],a[116:119],v[132:135]\n"
"s_add_u32 s87,s87,s60\n"
TR "v[176:177],v2 offset:49152\n"
TR "v[178:179],v2 offset:51200\n"
MF "v[132:135],a[240:243],a[76:79],v[132:135]\n"
"s_mov_b32 s71,204800\n"
TR "v[180:181],v2 offset:53248\n"
TR "v[182:183],v2 offset:55296\n"
MF "v[132:135],v[188:191],a[36:39],v[132:135]\n"
"s_mov_b32 s66,204800\n"
TR "v[184:185],v2 offset:57344\n"
TR "v[186:187],v2 offset:59392\n"
MF "v[124:127],v[188:191],a[32:35],v[124:127]\n"
"s_add_u32 s72,s78,2\n"
MF "v[124:127],a[240:243],a[72:75],v[124:127]\n"
"s_and_b32 s72,s72,15\n"
MF "v[124:127],a[244:247],a[112:115],v[124:127]\n"
"s_bfe_u32 s60,s22,0x10014\n"
MF "v[116:119],a[244:247],a[108:111],v[116:119]\n"
"s_lshl_b32 s60,s60,5\n"
MF "v[116:119],a[240:243],a[68:71],v[116:119]\n"
"v_xor_b32 v242,s60,v8\n"
MF "v[116:119],v[188:191],a[28:31],v[116:119]\n"
MF "v[108:111],v[188:191],a[24:27],v[108:111]\n"
"s_add_u32 s16,s78,6\n"
MF "v[108:111],a[240:243],a[64:67],v[108:111]\n"
"s_and_b32 s16,s16,15\n"
MF "v[108:111],a[244:247],a[104:107],v[108:111]\n"
"s_add_u32 s17,s78,4\n"
MF "v[100:103],a[244:247],a[100:103],v[100:103]\n"
"s_and_b32 s17,s17,15\n"
MF "v[100:103],a[240:243],a[60:63],v[100:103]\n"
MF "v[100:103],v[188:191],a[20:23],v[100:103]\n"
"s_waitcnt lgkmcnt(0)\n"
MF "v[136:139],v[176:179],a[20:23],v[136:139]\n"
"s_nop 0\n"
TR "v[188:189],v3 offset:49152\n"
TR "v[190:191],v3 offset:51200\n"
MF "v[136:139],v[180:183],a[60:63],v[136:139]\n"
"s_nop 0\n"
TR "a[240:241],v3 offset:53248\n"
TR "a[242:243],v3 offset:55296\n"
MF "v[136:139],v[184:187],a[100:103],v[136:139]\n"
"s_nop 0\n"
TR "a[244:245],v3 offset:57344\n"
TR "a[246:247],v3 offset:59392\n"
MF "v[144:147],v[184:187],a[104:107],v[144:147]\n"
MF "v[144:147],v[180:183],a[64:67],v[144:147]\n"
MF "v[144:147],v[176:179],a[24:27],v[144:147]\n"
MF "v[152:155],v[176:179],a[28:31],v[152:155]\n"
MF "v[152:155],v[180:183],a[68:71],v[152:155]\n"
MF "v[152:155],v[184:187],a[108:111],v[152:155]\n"
MF "v[160:163],v[184:187],a[112:115],v[160:163]\n"
MF "v[160:163],v[180:183],a[72:75],v[160:163]\n"
MF "v[160:163],v[176:179],a[32:35],v[160:163]\n"
MF "v[168:171],v[176:179],a[36:39],v[168:171]\n"
MF "v[168:171],v[180:183],a[76:79],v[168:171]\n"
".Lpg24h:\n"
"s_cbranch_execz .Lpg27h\n"
MF "v[168:171],v[184:187],a[116:119],v[168:171]\n"
"s_waitcnt vmcnt(0) lgkmcnt(0)\n"
"s_barrier\n"
MF "v[172:175],a[244:247],a[116:119],v[172:175]\n"
"v_sub_u32 v10,v10,v11\n"
MF "v[172:175],a[240:243],a[76:79],v[172:175]\n"
"s_nop 0\n"
TR "v[176:177],v4\n"
TR "v[178:179],v4 offset:2048\n"
MF "v[172:175],v[188:191],a[36:39],v[172:175]\n"
"s_nop 0\n"
TR "v[180:181],v4 offset:4096\n"
TR "v[182:183],v4 offset:6144\n"
MF "v[164:167],v[188:191],a[32:35],v[164:167]\n"
"s_nop 0\n"
TR "v[184:185],v4 offset:8192\n"
TR "v[186:187],v4 offset:10240\n"
MF "v[164:167],a[240:243],a[72:75],v[164:167]\n"
"v_readlane_b32 s16,v10,s16\n"
MF "v[164:167],a[244:247],a[112:115],v[164:167]\n"
"s_cmp_lt_u32 s16,9\n"
"s_cselect_b32 s16,0xf0000000,0\n"
MF "v[156:159],a[244:247],a[108:111],v[156:159]\n"
"v_readlane_b32 s17,v10,s17\n"
MF "v[156:159],a[240:243],a[68:71],v[156:159]\n"
"s_cmp_lt_u32 s17,10\n"
"s_cselect_b32 s17,0xf0000000,0\n"
MF "v[156:159],v[188:191],a[28:31],v[156:159]\n"
"s_bfe_u32 s62,s22,0x30014\n"
MF "v[148:151],v[188:191],a[24:27],v[148:151]\n"
"s_add_u32 s62,s62,3\n"
"s_and_b32 s62,s62,7\n"
"s_mul_i32 s62,s62,204800\n"
MF "v[148:151],a[240:243],a[64:67],v[148:151]\n"
"s_add_u32 s62,s62,s73\n"
"s_add_u32 s62,s62,133120\n"
"s_or_b32 s62,s62,s16\n"
MF "v[148:151],a[244:247],a[104:107],v[148:151]\n"
LD "v[16:19],v9,s[40:43],s62 offen sc1\n"
MF "v[140:143],a[244:247],a[100:103],v[140:143]\n"
LD "v[24:27],v9,s[40:43],s62 offen offset:1024 sc1\n"
MF "v[140:143],a[240:243],a[60:63],v[140:143]\n"
MF "v[140:143],v[188:191],a[20:23],v[140:143]\n"
"s_waitcnt lgkmcnt(0)\n"
LD "v[32:35],v9,s[40:43],s62 offen offset:2048 sc1\n"
MF "v[96:99],v[176:179],a[140:143],v[96:99]\n"
"s_nop 0\n"
TR "v[188:189],v5\n"
TR "v[190:191],v5 offset:2048\n"
MF "v[96:99],v[180:183],a[180:183],v[96:99]\n"
"s_nop 0\n"
TR "a[240:241],v5 offset:4096\n"
TR "a[242:243],v5 offset:6144\n"
MF "v[96:99],v[184:187],a[220:223],v[96:99]\n"
"s_nop 0\n"
TR "a[244:245],v5 offset:8192\n"
TR "a[246:247],v5 offset:10240\n"
MF "v[104:107],v[184:187],a[224:227],v[104:107]\n"
LD "v[40:43],v9,s[40:43],s62 offen offset:3072 sc1\n"
MF "v[104:107],v[180:183],a[184:187],v[104:107]\n"
MF "v[104:107],v[176:179],a[144:147],v[104:107]\n"
"s_add_u32 s62,s62,4096\n"
MF "v[112:115],v[176:179],a[148:151],v[112:115]\n"
LD "v[48:51],v9,s[40:43],s62 offen sc1\n"
MF "v[112:115],v[180:183],a[188:191],v[112:115]\n"
"s_bfe_u32 s62,s22,0x30014\n"
"ds_write_addtid_b32 v96 offset:16384\n"
"ds_write_addtid_b32 v97 offset:16640\n"
MF "v[112:115],v[184:187],a[228:231],v[112:115]\n"
"s_add_u32 s62,s62,2\n"
"s_and_b32 s62,s62,7\n"
"s_mul_i32 s62,s62,204800\n"
MF "v[120:123],v[184:187],a[232:235],v[120:123]\n"
"s_add_u32 s62,s62,s73\n"
"s_add_u32 s62,s62,153600\n"
"s_or_b32 s62,s62,s17\n"
MF "v[120:123],v[180:183],a[192:195],v[120:123]\n"
LD "v[20:23],v9,s[40:43],s62 offen sc1\n"
MF "v[120:123],v[176:179],a[152:155],v[120:123]\n"
"s_nop 0\n"
"ds_write_addtid_b32 v98 offset:16896\n"
"ds_write_addtid_b32 v99 offset:17152\n"
MF "v[128:131],v[176:179],a[156:159],v[128:131]\n"
LD "v[28:31],v9,s[40:43],s62 offen offset:1024 sc1\n"
MF "v[128:131],v[180:183],a[196:199],v[128:131]\n"
"s_nop 0\n"
"ds_write_addtid_b32 v104 offset:18432\n"
"ds_write_addtid_b32 v105 offset:18688\n"
MF "v[128:131],v[184:187],a[236:239],v[128:131]\n"
"s_waitcnt lgkmcnt(6)\n"
LD "v[36:39],v9,s[40:43],s62 offen offset:2048 sc1\n"
MF "v[100:103],v[188:191],a[140:143],v[100:103]\n"
"s_nop 0\n"
TR "v[176:177],v6\n"
TR "v[178:179],v6 offset:2048\n"
MF "v[100:103],a[240:243],a[180:183],v[100:103]\n"
"s_nop 0\n"
TR "v[180:181],v6 offset:4096\n"
TR "v[182:183],v6 offset:6144\n"
MF "v[100:103],a[244:247],a[220:223],v[100:103]\n"
"s_nop 0\n"
TR "v[184:185],v6 offset:8192\n"
TR "v[186:187],v6 offset:10240\n"
MF "v[108:111],a[244:247],a[224:227],v[108:111]\n"
LD "v[44:47],v9,s[40:43],s62 offen offset:3072 sc1\n"
MF "v[108:111],a[240:243],a[184:187],v[108:111]\n"
"s_nop 0\n"
"ds_write_addtid_b32 v106 offset:18944\n"
"ds_write_addtid_b32 v107 offset:19200\n"
MF "v[108:111],v[188:191],a[144:147],v[108:111]\n"
"s_add_u32 s62,s62,4096\n"
"ds_write_addtid_b32 v112 offset:20480\n"
"ds_write_addtid_b32 v113 offset:20736\n"
MF "v[116:119],v[188:191],a[148:151],v[116:119]\n"
LD "v[52:55],v9,s[40:43],s62 offen sc1\n"
MF "v[116:119],a[240:243],a[188:191],v[116:119]\n"
"s_nop 0\n"
"ds_write_addtid_b32 v114 offset:20992\n"
"ds_write_addtid_b32 v115 offset:21248\n"
MF "v[116:119],a[244:247],a[228:231],v[116:119]\n"
"s_nop 0\n"
"ds_write_addtid_b32 v120 offset:22528\n"
"ds_write_addtid_b32 v121 offset:22784\n"
MF "v[124:127],a[244:247],a[232:235],v[124:127]\n"
"s_nop 0\n"
"ds_write_addtid_b32 v122 offset:23040\n"
"ds_write_addtid_b32 v123 offset:23296\n"
MF "v[124:127],a[240:243],a[192:195],v[124:127]\n"
"s_nop 0\n"
"ds_write_addtid_b32 v128 offset:24576\n"
"ds_write_addtid_b32 v129 offset:24832\n"
MF "v[124:127],v[188:191],a[152:155],v[124:127]\n"
"buffer_load_dword v10,v12,s[40:43],s86 offen sc1\n"
MF "v[132:135],v[188:191],a[156:159],v[132:135]\n"
"s_nop 0\n"
"ds_write_addtid_b32 v130 offset:25088\n"
"ds_write_addtid_b32 v131 offset:25344\n"
MF "v[132:135],a[240:243],a[196:199],v[132:135]\n"
"s_nop 0\n"
"ds_write_addtid_b32 v100 offset:17408\n"
"ds_write_addtid_b32 v101 offset:17664\n"
MF "v[132:135],a[244:247],a[236:239],v[132:135]\n"
"s_waitcnt lgkmcnt(15)\n"
"ds_write_addtid_b32 v102 offset:17920\n"
"ds_write_addtid_b32 v103 offset:18176\n"
MF "v[136:139],v[176:179],a[140:143],v[136:139]\n"
"s_nop 0\n"
TR "v[188:189],v7\n"
TR "v[190:191],v7 offset:2048\n"
MF "v[136:139],v[180:183],a[180:183],v[136:139]\n"
"s_nop 0\n"
TR "a[240:241],v7 offset:4096\n"
TR "a[242:243],v7 offset:6144\n"
MF "v[136:139],v[184:187],a[220:223],v[136:139]\n"
"s_nop 0\n"
TR "a[244:245],v7 offset:8192\n"
TR "a[246:247],v7 offset:10240\n"
MF "v[144:147],v[184:187],a[224:227],v[144:147]\n"
"s_nop 0\n"
"ds_write_addtid_b32 v108 offset:19456\n"
"ds_write_addtid_b32 v109 offset:19712\n"
MF "v[144:147],v[180:183],a[184:187],v[144:147]\n"
"s_nop 0\n"
"ds_write_addtid_b32 v110 offset:19968\n"
"ds_write_addtid_b32 v111 offset:20224\n"
MF "v[144:147],v[176:179],a[144:147],v[144:147]\n"
"s_nop 0\n"
"ds_write_addtid_b32 v116 offset:21504\n"
"ds_write_addtid_b32 v117 offset:21760\n"
MF "v[152:155],v[176:179],a[148:151],v[152:155]\n"
"s_nop 0\n"
"ds_write_addtid_b32 v118 offset:22016\n"
"ds_write_addtid_b32 v119 offset:22272\n"
MF "v[152:155],v[180:183],a[188:191],v[152:155]\n"
"ds_write_addtid_b32 v124 offset:23552\n"
MF "v[152:155],v[184:187],a[228:231],v[152:155]\n"
"ds_write_addtid_b32 v125 offset:23808\n"
MF "v[160:163],v[184:187],a[232:235],v[160:163]\n"
"ds_write_addtid_b32 v126 offset:24064\n"
MF "v[160:163],v[180:183],a[192:195],v[160:163]\n"
"ds_write_addtid_b32 v127 offset:24320\n"
MF "v[160:163],v[176:179],a[152:155],v[160:163]\n"
"ds_write_addtid_b32 v132 offset:25600\n"
MF "v[168:171],v[176:179],a[156:159],v[168:171]\n"
"ds_write_addtid_b32 v133 offset:25856\n"
MF "v[168:171],v[180:183],a[196:199],v[168:171]\n"
"ds_write_addtid_b32 v134 offset:26112\n"
MF "v[168:171],v[184:187],a[236:239],v[168:171]\n"
"s_waitcnt lgkmcnt(15)\n"
"ds_write_addtid_b32 v135 offset:26368\n"
MF "v[140:143],v[188:191],a[140:143],v[140:143]\n"
MF "v[140:143],a[240:243],a[180:183],v[140:143]\n"
MF "v[140:143],a[244:247],a[220:223],v[140:143]\n"
MF "v[148:151],a[244:247],a[224:227],v[148:151]\n"
MF "v[148:151],a[240:243],a[184:187],v[148:151]\n"
"s_waitcnt lgkmcnt(0)\n"
"s_barrier\n"
MF "v[148:151],v[188:191],a[144:147],v[148:151]\n"
"ds_read2st64_b32 v[56:57],v13 offset0:0 offset1:1\n"
MF "v[156:159],v[188:191],a[148:151],v[156:159]\n"
"ds_read2st64_b32 v[58:59],v13 offset0:2 offset1:3\n"
MF "v[156:159],a[240:243],a[188:191],v[156:159]\n"
"ds_read2st64_b32 v[60:61],v13 offset0:4 offset1:5\n"
MF "v[156:159],a[244:247],a[228:231],v[156:159]\n"
"ds_read2st64_b32 v[62:63],v13 offset0:6 offset1:7\n"
MF "v[164:167],a[244:247],a[232:235],v[164:167]\n"
"ds_read2st64_b32 v[64:65],v13 offset0:8 offset1:9\n"
MF "v[164:167],a[240:243],a[192:195],v[164:167]\n"
"ds_read2st64_b32 v[66:67],v13 offset0:10 offset1:11\n"
MF "v[164:167],v[188:191],a[152:155],v[164:167]\n"
"ds_read2st64_b32 v[68:69],v13 offset0:12 offset1:13\n"
MF "v[172:175],v[188:191],a[156:159],v[172:175]\n"
"ds_read2st64_b32 v[70:71],v13 offset0:14 offset1:15\n"
MF "v[172:175],a[240:243],a[196:199],v[172:175]\n"
"ds_read2st64_b32 v[72:73],v13 offset0:16 offset1:17\n"
MF "v[172:175],a[244:247],a[236:239],v[172:175]\n"
"ds_read2st64_b32 v[74:75],v13 offset0:18 offset1:19\n"
"ds_read2st64_b32 v[76:77],v13 offset0:20 offset1:21\n"
"ds_read2st64_b32 v[78:79],v13 offset0:22 offset1:23\n"
"ds_read2st64_b32 v[80:81],v13 offset0:24 offset1:25\n"
"ds_read2st64_b32 v[82:83],v13 offset0:26 offset1:27\n"
"ds_read2st64_b32 v[84:85],v13 offset0:28 offset1:29\n"
"ds_read2st64_b32 v[86:87],v13 offset0:30 offset1:31\n"
"ds_read2st64_b32 v[88:89],v13 offset0:32 offset1:33\n"
"ds_read2st64_b32 v[90:91],v13 offset0:34 offset1:35\n"
"ds_read2st64_b32 v[92:93],v13 offset0:36 offset1:37\n"
"ds_read2st64_b32 v[94:95],v13 offset0:38 offset1:39\n"
"s_waitcnt vmcnt(0)\n"
"v_sub_u32 v10,v10,v11\n"
"s_cmp_eq_u32 s16,0\n"
"s_cbranch_scc1 .Lhave03\n"
"s_add_u32 s59,s78,6\n"
"s_and_b32 s59,s59,15\n"
"s_nop 3\n"
".Lnf2:\n"
"v_readlane_b32 s58,v10,s59\n"
"s_cmp_lt_u32 s58,9\n"
"s_cbranch_scc1 .Lsp2\n"
".Lnb2:\n"
"s_bfe_u32 s62,s22,0x30014\n"
"s_add_u32 s62,s62,3\n"
"s_and_b32 s62,s62,7\n"
"s_mul_i32 s62,s62,204800\n"
"s_add_u32 s62,s62,s73\n"
"s_add_u32 s62,s62,133120\n"
LD "v[16:19],v9,s[40:43],s62 offen sc1\n"
LD "v[24:27],v9,s[40:43],s62 offen offset:1024 sc1\n"
LD "v[32:35],v9,s[40:43],s62 offen offset:2048 sc1\n"
LD "v[40:43],v9,s[40:43],s62 offen offset:3072 sc1\n"
"s_add_u32 s62,s62,4096\n"
LD "v[48:51],v9,s[40:43],s62 offen sc1\n"
".Lhave03:\n"
"s_cmp_eq_u32 s17,0\n"
"s_cbranch_scc1 .Lhave02\n"
"s_add_u32 s59,s78,4\n"
"s_and_b32 s59,s59,15\n"
"s_nop 3\n"
".Lnf3:\n"
"v_readlane_b32 s58,v10,s59\n"
"s_cmp_lt_u32 s58,10\n"
"s_cbranch_scc1 .Lsp3\n"
".Lnb3:\n"
"s_bfe_u32 s62,s22,0x30014\n"
"s_add_u32 s62,s62,2\n"
"s_and_b32 s62,s62,7\n"
"s_mul_i32 s62,s62,204800\n"
"s_add_u32 s62,s62,s73\n"
"s_add_u32 s62,s62,153600\n"
LD "v[20:23],v9,s[40:43],s62 offen sc1\n"
LD "v[28:31],v9,s[40:43],s62 offen offset:1024 sc1\n"
LD "v[36:39],v9,s[40:43],s62 offen offset:2048 sc1\n"
LD "v[44:47],v9,s[40:43],s62 offen offset:3072 sc1\n"
"s_add_u32 s62,s62,4096\n"
LD "v[52:55],v9,s[40:43],s62 offen sc1\n"
".Lhave02:\n"
".Lnf4:\n"
"v_readlane_b32 s58,v10,s72\n"
"s_cmp_lt_u32 s58,11\n"
"s_cbranch_scc1 .Lsp4\n"
".Lnb4:\n"
"s_bfe_u32 s62,s22,0x30014\n"
"s_add_u32 s62,s62,1\n"
"s_and_b32 s62,s62,7\n"
"s_mul_i32 s62,s62,204800\n"
"s_add_u32 s62,s62,s73\n"
"s_add_u32 s62,s62,174080\n"
LD "v[208:211],v9,s[40:43],s62 offen sc1\n"
LD "v[212:215],v9,s[40:43],s62 offen offset:1024 sc1\n"
LD "v[216:219],v9,s[40:43],s62 offen offset:2048 sc1\n"
LD "v[220:223],v9,s[40:43],s62 offen offset:3072 sc1\n"
"s_add_u32 s62,s62,4096\n"
LD "v[224:227],v9,s[40:43],s62 offen sc1\n"
"s_waitcnt lgkmcnt(15)\n"
"v_pk_add_f32 v[136:137],v[136:137],v[56:57]\n"
"v_pk_add_f32 v[138:139],v[138:139],v[58:59]\n"
"v_pk_add_f32 v[140:141],v[140:141],v[60:61]\n"
"v_pk_add_f32 v[142:143],v[142:143],v[62:63]\n"
"v_pk_add_f32 v[144:145],v[144:145],v[64:65]\n"
"s_waitcnt lgkmcnt(14)\n"
"v_pk_add_f32 v[146:147],v[146:147],v[66:67]\n"
"s_waitcnt lgkmcnt(13)\n"
"v_pk_add_f32 v[148:149],v[148:149],v[68:69]\n"
"s_waitcnt lgkmcnt(12)\n"
"v_pk_add_f32 v[150:151],v[150:151],v[70:71]\n"
"s_waitcnt lgkmcnt(11)\n"
"v_pk_add_f32 v[152:153],v[152:153],v[72:73]\n"
"s_waitcnt lgkmcnt(10)\n"
"v_pk_add_f32 v[154:155],v[154:155],v[74:75]\n"
"s_waitcnt lgkmcnt(9)\n"
"v_pk_add_f32 v[156:157],v[156:157],v[76:77]\n"
"s_waitcnt lgkmcnt(8)\n"
"v_pk_add_f32 v[158:159],v[158:159],v[78:79]\n"
"s_waitcnt lgkmcnt(7)\n"
"v_pk_add_f32 v[160:161],v[160:161],v[80:81]\n"
"s_waitcnt lgkmcnt(6)\n"
"v_pk_add_f32 v[162:163],v[162:163],v[82:83]\n"
"s_waitcnt lgkmcnt(5)\n"
"v_pk_add_f32 v[164:165],v[164:165],v[84:85]\n"
"s_waitcnt lgkmcnt(4)\n"
"v_pk_add_f32 v[166:167],v[166:167],v[86:87]\n"
"s_waitcnt lgkmcnt(3)\n"
"v_pk_add_f32 v[168:169],v[168:169],v[88:89]\n"
"s_waitcnt lgkmcnt(2)\n"
"v_pk_add_f32 v[170:171],v[170:171],v[90:91]\n"
"s_waitcnt lgkmcnt(1)\n"
"v_pk_add_f32 v[172:173],v[172:173],v[92:93]\n"
"s_waitcnt lgkmcnt(0)\n"
"v_pk_add_f32 v[174:175],v[174:175],v[94:95]\n"
"s_waitcnt vmcnt(5)\n"
"s_nop 0\n"
MF "v[140:143],v[16:19],a[252:255],v[140:143]\n"
MF "v[148:151],v[24:27],a[252:255],v[148:151]\n"
MF "v[156:159],v[32:35],a[252:255],v[156:159]\n"
MF "v[164:167],v[40:43],a[252:255],v[164:167]\n"
MF "v[172:175],v[48:51],a[252:255],v[172:175]\n"
MF "v[136:139],v[16:19],a[248:251],v[136:139]\n"
MF "v[144:147],v[24:27],a[248:251],v[144:147]\n"
MF "v[152:155],v[32:35],a[248:251],v[152:155]\n"
MF "v[160:163],v[40:43],a[248:251],v[160:163]\n"
MF "v[168:171],v[48:51],a[248:251],v[168:171]\n"
MF "v[136:139],v[20:23],a[252:255],v[136:139]\n"
MF "v[144:147],v[28:31],a[252:255],v[144:147]\n"
MF "v[152:155],v[36:39],a[252:255],v[152:155]\n"
MF "v[160:163],v[44:47],a[252:255],v[160:163]\n"
MF "v[168:171],v[52:55],a[252:255],v[168:171]\n"
MF "v[140:143],v[20:23],a[248:251],v[140:143]\n"
MF "v[148:151],v[28:31],a[248:251],v[148:151]\n"
MF "v[156:159],v[36:39],a[248:251],v[156:159]\n"
MF "v[164:167],v[44:47],a[248:251],v[164:167]\n"
MF "v[172:175],v[52:55],a[248:251],v[172:175]\n"
"s_waitcnt vmcnt(0)\n"
MF "v[140:143],v[208:211],a[252:255],v[140:143]\n"
MF "v[136:139],v[208:211],a[248:251],v[136:139]\n"
MF "v[148:151],v[212:215],a[252:255],v[148:151]\n"
MF "v[144:147],v[212:215],a[248:251],v[144:147]\n"
MF "v[156:159],v[216:219],a[252:255],v[156:159]\n"
"s_nop 4\n"
CV "v136,v136,v137\n"
CV "v137,v138,v139\n"
MF "v[152:155],v[216:219],a[248:251],v[152:155]\n"
CV "v138,v140,v141\n"
CV "v139,v142,v143\n"
MF "v[164:167],v[220:223],a[252:255],v[164:167]\n"
CV "v144,v144,v145\n"
CV "v145,v146,v147\n"
MF "v[160:163],v[220:223],a[248:251],v[160:163]\n"
CV "v146,v148,v149\n"
CV "v147,v150,v151\n"
"v_permlane16_swap_b32 v136,v138\n"
"v_permlane16_swap_b32 v137,v139\n"
"buffer_store_dwordx4 v[136:139],v242,s[44:47],s87 offen offset:64 sc1\n"
"s_add_u32 s87,s87,40960\n"
MF "v[172:175],v[224:227],a[252:255],v[172:175]\n"
CV "v152,v152,v153\n"
CV "v153,v154,v155\n"
MF "v[168:171],v[224:227],a[248:251],v[168:171]\n"
CV "v154,v156,v157\n"
CV "v155,v158,v159\n"
"v_permlane16_swap_b32 v144,v146\n"
"v_permlane16_swap_b32 v145,v147\n"
"buffer_store_dwordx4 v[144:147],v242,s[44:47],s87 offen offset:64 sc1\n"
"s_add_u32 s87,s87,40960\n"
CV "v160,v160,v161\n"
CV "v161,v162,v163\n"
CV "v162,v164,v165\n"
CV "v163,v166,v167\n"
"v_permlane16_swap_b32 v152,v154\n"
"v_permlane16_swap_b32 v153,v155\n"
"buffer_store_dwordx4 v[152:155],v242,s[44:47],s87 offen offset:64 sc1\n"
"s_add_u32 s87,s87,40960\n"
CV "v168,v168,v169\n"
CV "v169,v170,v171\n"
CV "v170,v172,v173\n"
CV "v171,v174,v175\n"
"v_permlane16_swap_b32 v160,v162\n"
"v_permlane16_swap_b32 v161,v163\n"
"buffer_store_dwordx4 v[160:163],v242,s[44:47],s87 offen offset:64 sc1\n"
"s_add_u32 s87,s87,40960\n"
"v_permlane16_swap_b32 v168,v170\n"
"v_permlane16_swap_b32 v169,v171\n"
"buffer_store_dwordx4 v[168:171],v242,s[44:47],s87 offen offset:64 sc1\n"
"s_add_u32 s87,s87,40960\n"
"s_branch .Ljobs\n"
".Lpgrole1_h:\n"
"s_cbranch_execz .Lpg28h\n"
".Lrole1:\n"
MF "v[16:19],a[120:123],v[176:179],v[252:255]\n"
"s_mov_b32 s65,128\n"
TR "v[188:189],v1 offset:24576\n"
AR "v220,a44\n"
MF "v[16:19],a[160:163],v[180:183],v[16:19]\n"
"s_mov_b32 s9,0x3f80\n"
TR "v[190:191],v1 offset:26624\n"
AR "v221,a45\n"
MF "v[16:19],a[200:203],v[184:187],v[16:19]\n"
AR "v222,a46\n"
AR "v223,a47\n"
MF "v[24:27],a[204:207],v[184:187],v[252:255]\n"
"s_bfe_u32 s58,s22,0x30014\n"
TR "a[240:241],v1 offset:28672\n"
AR "v224,a64\n"
MF "v[24:27],a[164:167],v[180:183],v[24:27]\n"
"s_add_u32 s61,s58,1\n"
TR "a[242:243],v1 offset:30720\n"
AR "v225,a65\n"
MF "v[24:27],a[124:127],v[176:179],v[24:27]\n"
"s_and_b32 s61,s61,7\n"
AR "v226,a66\n"
AR "v227,a67\n"
MF "v[32:35],a[128:131],v[176:179],v[252:255]\n"
"s_mov_b32 s64,9\n"
TR "a[244:245],v1 offset:32768\n"
"ds_read_b128 a[64:67],v236 offset:49152\n"
MF "v[32:35],a[168:171],v[180:183],v[32:35]\n"
"s_xor_b32 s67,s78,8\n"
TR "a[246:247],v1 offset:34816\n"
"v_fma_f32 v220,v220,v209,v215\n"
MF "v[32:35],a[208:211],v[184:187],v[32:35]\n"
"s_mov_b32 s71,204800\n"
"v_fma_f32 v221,v221,v209,v215\n"
"v_fma_f32 v222,v222,v209,v215\n"
MF "v[40:43],a[212:215],v[184:187],v[252:255]\n"
"s_mov_b32 s66,204800\n"
"v_fma_f32 v223,v223,v209,v215\n"
"v_fma_f32 v224,v224,v209,v215\n"
MF "v[40:43],a[172:175],v[180:183],v[40:43]\n"
"s_and_b32 s73,s56,1\n"
"v_fma_f32 v225,v225,v209,v215\n"
"v_fma_f32 v226,v226,v209,v215\n"
MF "v[40:43],a[132:135],v[176:179],v[40:43]\n"
"s_mul_i32 s73,s73,5120\n"
"v_fma_f32 v227,v227,v209,v215\n"
CV "v220,v220,v221\n"
MF "v[48:51],a[136:139],v[176:179],v[252:255]\n"
"s_add_u32 s73,s73,s93\n"
CV "v221,v222,v223\n"
CV "v222,v224,v225\n"
MF "v[48:51],a[176:179],v[180:183],v[48:51]\n"
"s_xor_b32 s58,s58,4\n"
CV "v223,v226,v227\n"
AW "a44,v220\n"
MF "v[48:51],a[216:219],v[184:187],v[48:51]\n"
"s_waitcnt lgkmcnt(0)\n"
AW "a45,v221\n"
AW "a46,v222\n"
MF "v[52:55],a[216:219],a[244:247],v[252:255]\n"
"s_mul_i32 s70,s58,204800\n"
TR "v[176:177],v2 offset:24576\n"
AW "a47,v223\n"
MF "v[52:55],a[176:179],a[240:243],v[52:55]\n"
"s_add_u32 s70,s70,s73\n"
TR "v[178:179],v2 offset:26624\n"
AR "v228,a84\n"
MF "v[52:55],a[136:139],v[188:191],v[52:55]\n"
"s_sub_u32 s70,s70,30720\n"
AR "v229,a85\n"
AR "v230,a86\n"
MF "v[44:47],a[132:135],v[188:191],v[252:255]\n"
"s_mov_b32 s85,1\n"
TR "v[180:181],v2 offset:28672\n"
AR "v231,a87\n"
MF "v[44:47],a[172:175],a[240:243],v[44:47]\n"
"s_mov_b32 s83,0xf0000000\n"
TR "v[182:183],v2 offset:30720\n"
AR "v232,a104\n"
MF "v[44:47],a[212:215],a[244:247],v[44:47]\n"
"s_mov_b32 s82,0xf0000000\n"
AR "v233,a105\n"
AR "v234,a106\n"
MF "v[36:39],a[208:211],a[244:247],v[252:255]\n"
"s_add_u32 s58,s79,73728\n"
TR "v[184:185],v2 offset:32768\n"
AR "v235,a107\n"
MF "v[36:39],a[168:171],a[240:243],v[36:39]\n"
"s_mov_b32 s90,s91\n"
TR "v[186:187],v2 offset:34816\n"
"ds_read_b128 a[104:107],v237 offset:4096\n"
MF "v[36:39],a[128:131],v[188:191],v[36:39]\n"
"s_add_u32 s91,s91,20480\n"
"v_fma_f32 v228,v228,v209,v215\n"
"v_fma_f32 v229,v229,v209,v215\n"
MF "v[28:31],a[124:127],v[188:191],v[252:255]\n"
"s_bfe_u32 s74,s23,0x70000\n"
"v_fma_f32 v230,v230,v209,v215\n"
"v_fma_f32 v231,v231,v209,v215\n"
MF "v[28:31],a[164:167],a[240:243],v[28:31]\n"
"s_lshl_b32 s74,s74,1\n"
"v_fma_f32 v232,v232,v209,v215\n"
"v_fma_f32 v233,v233,v209,v215\n"
MF "v[28:31],a[204:207],a[244:247],v[28:31]\n"
"s_add_u32 s74,s74,s56\n"
"v_fma_f32 v234,v234,v209,v215\n"
"v_fma_f32 v235,v235,v209,v215\n"
MF "v[20:23],a[200:203],a[244:247],v[252:255]\n"
"s_sub_u32 s74,s74,2\n"
CV "v228,v228,v229\n"
CV "v229,v230,v231\n"
MF "v[20:23],a[160:163],a[240:243],v[20:23]\n"
"s_mul_i32 s74,s74,30720\n"
CV "v230,v232,v233\n"
CV "v231,v234,v235\n"
MF "v[20:23],a[120:123],v[188:191],v[20:23]\n"
"s_waitcnt lgkmcnt(1)\n"
AW "a84,v228\n"
AW "a85,v229\n"
MF "v[56:59],a[120:123],v[176:179],v[252:255]\n"
"s_bfe_u32 s68,s23,0x30003\n"
TR "v[188:189],v3 offset:24576\n"
"v_add_u32 v198,59392,v192\n"
MF "v[56:59],a[160:163],v[180:183],v[56:59]\n"
"s_add_u32 s68,s68,7\n"
TR "v[190:191],v3 offset:26624\n"
"v_add_u32 v199,68608,v192\n"
MF "v[56:59],a[200:203],v[184:187],v[56:59]\n"
"s_lshl_b32 s68,s68,7\n"
"v_add_u32 v200,81920,v192\n"
"v_add_u32 v201,91136,v192\n"
MF "v[64:67],a[204:207],v[184:187],v[252:255]\n"
"s_and_b32 s68,s68,768\n"
TR "a[240:241],v3 offset:28672\n"
"v_add_u32 v202,100352,v192\n"
MF "v[64:67],a[164:167],v[180:183],v[64:67]\n"
TR "a[242:243],v3 offset:30720\n"
"v_add_u32 v203,109568,v192\n"
MF "v[64:67],a[124:127],v[176:179],v[64:67]\n"
AW "a86,v230\n"
AW "a87,v231\n"
MF "v[72:75],a[128:131],v[176:179],v[252:255]\n"
TR "a[244:245],v3 offset:32768\n"
AR "v220,a8\n"
MF "v[72:75],a[168:171],v[180:183],v[72:75]\n"
TR "a[246:247],v3 offset:34816\n"
AR "v221,a9\n"
MF "v[72:75],a[208:211],v[184:187],v[72:75]\n"
AR "v222,a10\n"
AR "v223,a11\n"
MF "v[80:83],a[212:215],v[184:187],v[252:255]\n"
AR "v224,a28\n"
AR "v225,a29\n"
MF "v[80:83],a[172:175],v[180:183],v[80:83]\n"
AR "v226,a30\n"
AR "v227,a31\n"
MF "v[80:83],a[132:135],v[176:179],v[80:83]\n"
"ds_read_b128 a[28:31],v236 offset:32768\n"
"v_fma_f32 v220,v220,v210,v216\n"
MF "v[88:91],a[136:139],v[176:179],v[252:255]\n"
"v_fma_f32 v221,v221,v210,v216\n"
"v_fma_f32 v222,v222,v210,v216\n"
MF "v[88:91],a[176:179],v[180:183],v[88:91]\n"
"v_fma_f32 v223,v223,v210,v216\n"
"v_fma_f32 v224,v224,v210,v216\n"
MF "v[88:91],a[216:219],v[184:187],v[88:91]\n"
"s_waitcnt vmcnt(0) lgkmcnt(0)\n"
"s_barrier\n"
MF "v[92:95],a[216:219],a[244:247],v[252:255]\n"
"s_add_u32 s89,s89,s65\n"
TR "v[176:177],v0\n"
"v_fma_f32 v225,v225,v210,v216\n"
MF "v[92:95],a[176:179],a[240:243],v[92:95]\n"
"s_add_u32 m0,s79,24576\n"
TR "v[178:179],v0 offset:2048\n"
"v_fma_f32 v226,v226,v210,v216\n"
MF "v[92:95],a[136:139],v[188:191],v[92:95]\n"
".Lpg25h:\n"
"s_cbranch_execz .Lpg29h\n"
LD "v192,s[48:51],s89 offen lds\n"
MF "v[84:87],a[132:135],v[188:191],v[252:255]\n"
TR "v[180:181],v0 offset:4096\n"
"v_fma_f32 v227,v227,v210,v216\n"
MF "v[84:87],a[172:175],a[240:243],v[84:87]\n"
TR "v[182:183],v0 offset:6144\n"
CV "v220,v220,v221\n"
MF "v[84:87],a[212:215],a[244:247],v[84:87]\n"
LD "v193,s[48:51],s89 offen offset:1024 lds\n"
MF "v[76:79],a[208:211],a[244:247],v[252:255]\n"
TR "v[184:185],v0 offset:8192\n"
CV "v221,v222,v223\n"
MF "v[76:79],a[168:171],a[240:243],v[76:79]\n"
TR "v[186:187],v0 offset:10240\n"
CV "v222,v224,v225\n"
MF "v[76:79],a[128:131],v[188:191],v[76:79]\n"
TR "v[96:97],v0 offset:49152\n"
CV "v223,v226,v227\n"
MF "v[68:71],a[124:127],v[188:191],v[252:255]\n"
TR "v[98:99],v0 offset:51200\n"
AW "a8,v220\n"
MF "v[68:71],a[164:167],a[240:243],v[68:71]\n"
LD "v194,s[48:51],s89 offen offset:2048 lds\n"
MF "v[68:71],a[204:207],a[244:247],v[68:71]\n"
TR "v[100:101],v0 offset:53248\n"
AW "a9,v221\n"
MF "v[60:63],a[200:203],a[244:247],v[252:255]\n"
TR "v[102:103],v0 offset:55296\n"
AW "a10,v222\n"
MF "v[60:63],a[160:163],a[240:243],v[60:63]\n"
TR "v[104:105],v0 offset:57344\n"
AW "a11,v223\n"
MF "v[60:63],a[120:123],v[188:191],v[60:63]\n"
"s_waitcnt lgkmcnt(5)\n"
LD "v195,s[48:51],s89 offen offset:3072 lds\n"
MF "v[16:19],a[140:143],v[176:179],v[16:19]\n"
TR "v[188:189],v1\n"
AR "v228,a48\n"
MF "v[16:19],a[180:183],v[180:183],v[16:19]\n"
TR "v[190:191],v1 offset:2048\n"
AR "v229,a49\n"
MF "v[16:19],a[220:223],v[184:187],v[16:19]\n"
TR "v[106:107],v0 offset:59392\n"
AR "v230,a50\n"
MF "v[24:27],a[224:227],v[184:187],v[24:27]\n"
TR "a[240:241],v1 offset:4096\n"
AR "v231,a51\n"
MF "v[24:27],a[184:187],v[180:183],v[24:27]\n"
"s_add_u32 m0,s79,28672\n"
TR "a[242:243],v1 offset:6144\n"
AR "v232,a68\n"
MF "v[24:27],a[144:147],v[176:179],v[24:27]\n"
LD "v196,s[48:51],s89 offen lds\n"
MF "v[32:35],a[148:151],v[176:179],v[32:35]\n"
TR "a[244:245],v1 offset:8192\n"
AR "v233,a69\n"
MF "v[32:35],a[188:191],v[180:183],v[32:35]\n"
TR "a[246:247],v1 offset:10240\n"
AR "v234,a70\n"
MF "v[32:35],a[228:231],v[184:187],v[32:35]\n"
LD "v197,s[48:51],s89 offen offset:1024 lds\n"
MF "v[40:43],a[232:235],v[184:187],v[40:43]\n"
TR "v[108:109],v4\n"
AR "v235,a71\n"
MF "v[40:43],a[192:195],v[180:183],v[40:43]\n"
"s_nop 0\n"
TR "v[110:111],v4 offset:2048\n"
"ds_read_b128 a[68:71],v236 offset:53248\n"
MF "v[40:43],a[152:155],v[176:179],v[40:43]\n"
TR "v[112:113],v4 offset:4096\n"
"v_fma_f32 v228,v228,v210,v216\n"
MF "v[48:51],a[156:159],v[176:179],v[48:51]\n"
TR "v[114:115],v4 offset:6144\n"
"v_fma_f32 v229,v229,v210,v216\n"
MF "v[48:51],a[196:199],v[180:183],v[48:51]\n"
LD "v198,s[48:51],s89 offen offset:2048 lds\n"
MF "v[48:51],a[236:239],v[184:187],v[48:51]\n"
"s_waitcnt lgkmcnt(5)\n"
TR "v[116:117],v4 offset:8192\n"
"v_fma_f32 v230,v230,v210,v216\n"
MF "v[52:55],a[236:239],a[244:247],v[52:55]\n"
TR "v[176:177],v2\n"
"v_fma_f32 v231,v231,v210,v216\n"
MF "v[52:55],a[196:199],a[240:243],v[52:55]\n"
TR "v[178:179],v2 offset:2048\n"
"v_fma_f32 v232,v232,v210,v216\n"
MF "v[52:55],a[156:159],v[188:191],v[52:55]\n"
LD "v199,s[48:51],s89 offen offset:3072 lds\n"
MF "v[44:47],a[152:155],v[188:191],v[44:47]\n"
TR "v[180:181],v2 offset:4096\n"
"v_fma_f32 v233,v233,v210,v216\n"
MF "v[44:47],a[192:195],a[240:243],v[44:47]\n"
TR "v[182:183],v2 offset:6144\n"
"v_fma_f32 v234,v234,v210,v216\n"
MF "v[44:47],a[232:235],a[244:247],v[44:47]\n"
TR "v[118:119],v4 offset:10240\n"
"v_fma_f32 v235,v235,v210,v216\n"
MF "v[36:39],a[228:231],a[244:247],v[36:39]\n"
TR "v[184:185],v2 offset:8192\n"
CV "v228,v228,v229\n"
MF "v[36:39],a[188:191],a[240:243],v[36:39]\n"
"s_add_u32 m0,s79,32768\n"
TR "v[186:187],v2 offset:10240\n"
CV "v229,v230,v231\n"
MF "v[36:39],a[148:151],v[188:191],v[36:39]\n"
LD "v200,s[48:51],s89 offen lds\n"
MF "v[28:31],a[144:147],v[188:191],v[28:31]\n"
TR "v[120:121],v1 offset:49152\n"
CV "v230,v232,v233\n"
MF "v[28:31],a[184:187],a[240:243],v[28:31]\n"
TR "v[122:123],v1 offset:51200\n"
CV "v231,v234,v235\n"
MF "v[28:31],a[224:227],a[244:247],v[28:31]\n"
TR "v[124:125],v1 offset:53248\n"
AW "a48,v228\n"
MF "v[20:23],a[220:223],a[244:247],v[20:23]\n"
LD "v201,s[48:51],s89 offen offset:1024 lds\n"
MF "v[20:23],a[180:183],a[240:243],v[20:23]\n"
TR "v[126:127],v1 offset:55296\n"
AW "a49,v229\n"
MF "v[20:23],a[140:143],v[188:191],v[20:23]\n"
"s_waitcnt lgkmcnt(4)\n"
TR "v[128:129],v1 offset:57344\n"
AW "a50,v230\n"
MF "v[56:59],a[140:143],v[176:179],v[56:59]\n"
TR "v[188:189],v3\n"
AW "a51,v231\n"
MF "v[56:59],a[180:183],v[180:183],v[56:59]\n"
TR "v[190:191],v3 offset:2048\n"
AR "v220,a88\n"
MF "v[56:59],a[220:223],v[184:187],v[56:59]\n"
LD "v202,s[48:51],s89 offen offset:2048 lds\n"
MF "v[64:67],a[224:227],v[184:187],v[64:67]\n"
TR "a[240:241],v3 offset:4096\n"
AR "v221,a89\n"
MF "v[64:67],a[184:187],v[180:183],v[64:67]\n"
TR "a[242:243],v3 offset:6144\n"
AR "v222,a90\n"
MF "v[64:67],a[144:147],v[176:179],v[64:67]\n"
LD "v203,s[48:51],s89 offen offset:3072 lds\n"
MF "v[72:75],a[148:151],v[176:179],v[72:75]\n"
TR "a[244:245],v3 offset:8192\n"
AR "v223,a91\n"
MF "v[72:75],a[188:191],v[180:183],v[72:75]\n"
TR "a[246:247],v3 offset:10240\n"
AR "v224,a108\n"
MF "v[72:75],a[228:231],v[184:187],v[72:75]\n"
TR "v[130:131],v1 offset:59392\n"
AR "v225,a109\n"
MF "v[80:83],a[232:235],v[184:187],v[80:83]\n"
TR "v[132:133],v5\n"
AR "v226,a110\n"
MF "v[80:83],a[192:195],v[180:183],v[80:83]\n"
TR "v[134:135],v5 offset:2048\n"
AR "v227,a111\n"
MF "v[80:83],a[152:155],v[176:179],v[80:83]\n"
"s_nop 0\n"
TR "v[136:137],v5 offset:4096\n"
"ds_read_b128 a[108:111],v237 offset:8192\n"
MF "v[88:91],a[156:159],v[176:179],v[88:91]\n"
TR "v[138:139],v5 offset:6144\n"
"v_fma_f32 v220,v220,v210,v216\n"
MF "v[88:91],a[196:199],v[180:183],v[88:91]\n"
TR "v[140:141],v5 offset:8192\n"
"v_fma_f32 v221,v221,v210,v216\n"
MF "v[88:91],a[236:239],v[184:187],v[88:91]\n"
"s_waitcnt lgkmcnt(0)\n"
"s_barrier\n"
MF "v[92:95],a[236:239],a[244:247],v[92:95]\n"
TR "v[142:143],v5 offset:10240\n"
"v_fma_f32 v222,v222,v210,v216\n"
MF "v[92:95],a[196:199],a[240:243],v[92:95]\n"
TR "v[144:145],v2 offset:49152\n"
"v_fma_f32 v223,v223,v210,v216\n"
MF "v[92:95],a[156:159],v[188:191],v[92:95]\n"
TR "v[146:147],v2 offset:51200\n"
"v_fma_f32 v224,v224,v210,v216\n"
MF "v[84:87],a[152:155],v[188:191],v[84:87]\n"
TR "v[148:149],v2 offset:53248\n"
"v_fma_f32 v225,v225,v210,v216\n"
MF "v[84:87],a[192:195],a[240:243],v[84:87]\n"
TR "v[150:151],v2 offset:55296\n"
"v_fma_f32 v226,v226,v210,v216\n"
MF "v[84:87],a[232:235],a[244:247],v[84:87]\n"
TR "v[152:153],v2 offset:57344\n"
"v_fma_f32 v227,v227,v210,v216\n"
MF "v[76:79],a[228:231],a[244:247],v[76:79]\n"
TR "v[154:155],v2 offset:59392\n"
CV "v220,v220,v221\n"
MF "v[76:79],a[188:191],a[240:243],v[76:79]\n"
TR "v[156:157],v6\n"
CV "v221,v222,v223\n"
MF "v[76:79],a[148:151],v[188:191],v[76:79]\n"
TR "v[158:159],v6 offset:2048\n"
CV "v222,v224,v225\n"
MF "v[68:71],a[144:147],v[188:191],v[68:71]\n"
TR "v[160:161],v6 offset:4096\n"
CV "v223,v226,v227\n"
MF "v[68:71],a[184:187],a[240:243],v[68:71]\n"
TR "v[162:163],v6 offset:6144\n"
AW "a88,v220\n"
MF "v[68:71],a[224:227],a[244:247],v[68:71]\n"
TR "v[164:165],v6 offset:8192\n"
AW "a89,v221\n"
MF "v[60:63],a[220:223],a[244:247],v[60:63]\n"
TR "v[166:167],v6 offset:10240\n"
AW "a90,v222\n"
MF "v[60:63],a[180:183],a[240:243],v[60:63]\n"
TR "v[168:169],v3 offset:49152\n"
AW "a91,v223\n"
MF "v[60:63],a[140:143],v[188:191],v[60:63]\n"
TR "v[170:171],v3 offset:51200\n"
AR "v228,a12\n"
MF "v[16:19],a[0:3],v[96:99],v[16:19]\n"
TR "v[172:173],v3 offset:53248\n"
AR "v229,a13\n"
MF "v[16:19],a[40:43],v[100:103],v[16:19]\n"
TR "v[174:175],v3 offset:55296\n"
AR "v230,a14\n"
MF "v[16:19],a[80:83],v[104:107],v[16:19]\n"
TR "v[204:205],v3 offset:57344\n"
AR "v231,a15\n"
MF "v[16:19],a[20:23],v[108:111],v[16:19]\n"
TR "v[206:207],v3 offset:59392\n"
AR "v232,a32\n"
MF "v[16:19],a[60:63],v[112:115],v[16:19]\n"
TR "v[244:245],v7\n"
AR "v233,a33\n"
MF "v[16:19],a[100:103],v[116:119],v[16:19]\n"
"s_waitcnt lgkmcnt(15)\n"
TR "v[246:247],v7 offset:2048\n"
AR "v234,a34\n"
MF "v[20:23],a[100:103],v[140:143],v[20:23]\n"
TR "v[248:249],v7 offset:4096\n"
AR "v235,a35\n"
MF "v[20:23],a[60:63],v[136:139],v[20:23]\n"
"s_nop 0\n"
TR "v[250:251],v7 offset:6144\n"
"ds_read_b128 a[32:35],v236 offset:36864\n"
MF "v[20:23],a[20:23],v[132:135],v[20:23]\n"
TR "v[238:239],v7 offset:8192\n"
"v_fma_f32 v228,v228,v211,v217\n"
MF "v[20:23],a[80:83],v[128:131],v[20:23]\n"
TR "v[240:241],v7 offset:10240\n"
"v_fma_f32 v229,v229,v211,v217\n"
MF "v[20:23],a[40:43],v[124:127],v[20:23]\n"
"v_fma_f32 v230,v230,v211,v217\n"
"v_fma_f32 v231,v231,v211,v217\n"
MF "v[20:23],a[0:3],v[120:123],v[20:23]\n"
"s_waitcnt lgkmcnt(13)\n"
"v_fma_f32 v232,v232,v211,v217\n"
"v_fma_f32 v233,v233,v211,v217\n"
MF "v[56:59],a[0:3],v[144:147],v[56:59]\n"
"v_fma_f32 v234,v234,v211,v217\n"
"v_fma_f32 v235,v235,v211,v217\n"
MF "v[56:59],a[40:43],v[148:151],v[56:59]\n"
CV "v228,v228,v229\n"
CV "v229,v230,v231\n"
MF "v[56:59],a[80:83],v[152:155],v[56:59]\n"
CV "v230,v232,v233\n"
CV "v231,v234,v235\n"
MF "v[56:59],a[20:23],v[156:159],v[56:59]\n"
AW "a12,v228\n"
AW "a13,v229\n"
MF "v[56:59],a[60:63],v[160:163],v[56:59]\n"
AW "a14,v230\n"
AW "a15,v231\n"
MF "v[56:59],a[100:103],v[164:167],v[56:59]\n"
"s_waitcnt lgkmcnt(0)\n"
AR "v220,a52\n"
AR "v221,a53\n"
MF "v[60:63],a[100:103],v[238:241],v[60:63]\n"
AR "v222,a54\n"
AR "v223,a55\n"
MF "v[60:63],a[60:63],v[248:251],v[60:63]\n"
AR "v224,a72\n"
AR "v225,a73\n"
MF "v[60:63],a[20:23],v[244:247],v[60:63]\n"
AR "v226,a74\n"
AR "v227,a75\n"
MF "v[60:63],a[80:83],v[204:207],v[60:63]\n"
"ds_read_b128 a[72:75],v236 offset:57344\n"
"v_fma_f32 v220,v220,v211,v217\n"
MF "v[60:63],a[40:43],v[172:175],v[60:63]\n"
"v_fma_f32 v221,v221,v211,v217\n"
"v_fma_f32 v222,v222,v211,v217\n"
MF "v[60:63],a[0:3],v[168:171],v[60:63]\n"
"v_fma_f32 v223,v223,v211,v217\n"
"v_fma_f32 v224,v224,v211,v217\n"
MF "v[68:71],a[4:7],v[168:171],v[68:71]\n"
"v_fma_f32 v225,v225,v211,v217\n"
"v_fma_f32 v226,v226,v211,v217\n"
MF "v[68:71],a[44:47],v[172:175],v[68:71]\n"
"v_fma_f32 v227,v227,v211,v217\n"
CV "v220,v220,v221\n"
MF "v[68:71],a[84:87],v[204:207],v[68:71]\n"
CV "v221,v222,v223\n"
CV "v222,v224,v225\n"
MF "v[68:71],a[24:27],v[244:247],v[68:71]\n"
CV "v223,v226,v227\n"
AW "a52,v220\n"
MF "v[68:71],a[64:67],v[248:251],v[68:71]\n"
AW "a53,v221\n"
AW "a54,v222\n"
MF "v[68:71],a[104:107],v[238:241],v[68:71]\n"
AW "a55,v223\n"
AR "v228,a92\n"
MF "v[64:67],a[104:107],v[164:167],v[64:67]\n"
"s_waitcnt lgkmcnt(0)\n"
"s_barrier\n"
MF "v[64:67],a[64:67],v[160:163],v[64:67]\n"
AR "v229,a93\n"
AR "v230,a94\n"
MF "v[64:67],a[24:27],v[156:159],v[64:67]\n"
AR "v231,a95\n"
AR "v232,a112\n"
MF "v[64:67],a[84:87],v[152:155],v[64:67]\n"
AR "v233,a113\n"
AR "v234,a114\n"
MF "v[64:67],a[44:47],v[148:151],v[64:67]\n"
AR "v235,a115\n"
"v_fma_f32 v228,v228,v211,v217\n"
MF "v[64:67],a[4:7],v[144:147],v[64:67]\n"
"ds_read_b128 a[112:115],v237 offset:12288\n"
"v_fma_f32 v229,v229,v211,v217\n"
MF "v[28:31],a[4:7],v[120:123],v[28:31]\n"
"v_fma_f32 v230,v230,v211,v217\n"
"v_fma_f32 v231,v231,v211,v217\n"
MF "v[28:31],a[44:47],v[124:127],v[28:31]\n"
"v_fma_f32 v232,v232,v211,v217\n"
"v_fma_f32 v233,v233,v211,v217\n"
MF "v[28:31],a[84:87],v[128:131],v[28:31]\n"
"v_fma_f32 v234,v234,v211,v217\n"
"v_fma_f32 v235,v235,v211,v217\n"
MF "v[28:31],a[24:27],v[132:135],v[28:31]\n"
CV "v228,v228,v229\n"
CV "v229,v230,v231\n"
MF "v[28:31],a[64:67],v[136:139],v[28:31]\n"
CV "v230,v232,v233\n"
CV "v231,v234,v235\n"
MF "v[28:31],a[104:107],v[140:143],v[28:31]\n"
AW "a92,v228\n"
AW "a93,v229\n"
MF "v[24:27],a[104:107],v[116:119],v[24:27]\n"
AW "a94,v230\n"
AW "a95,v231\n"
MF "v[24:27],a[64:67],v[112:115],v[24:27]\n"
AR "v220,a16\n"
AR "v221,a17\n"
MF "v[24:27],a[24:27],v[108:111],v[24:27]\n"
AR "v222,a18\n"
AR "v223,a19\n"
MF "v[24:27],a[84:87],v[104:107],v[24:27]\n"
AR "v224,a36\n"
AR "v225,a37\n"
MF "v[24:27],a[44:47],v[100:103],v[24:27]\n"
AR "v226,a38\n"
AR "v227,a39\n"
MF "v[24:27],a[4:7],v[96:99],v[24:27]\n"
"ds_read_b128 a[36:39],v236 offset:40960\n"
"v_fma_f32 v220,v220,v212,v218\n"
MF "v[32:35],a[8:11],v[96:99],v[32:35]\n"
"v_fma_f32 v221,v221,v212,v218\n"
"v_fma_f32 v222,v222,v212,v218\n"
MF "v[32:35],a[48:51],v[100:103],v[32:35]\n"
"v_fma_f32 v223,v223,v212,v218\n"
"v_fma_f32 v224,v224,v212,v218\n"
MF "v[32:35],a[88:91],v[104:107],v[32:35]\n"
"v_fma_f32 v225,v225,v212,v218\n"
"v_fma_f32 v226,v226,v212,v218\n"
MF "v[32:35],a[28:31],v[108:111],v[32:35]\n"
"v_fma_f32 v227,v227,v212,v218\n"
CV "v220,v220,v221\n"
MF "v[32:35],a[68:71],v[112:115],v[32:35]\n"
CV "v221,v222,v223\n"
CV "v222,v224,v225\n"
MF "v[32:35],a[108:111],v[116:119],v[32:35]\n"
CV "v223,v226,v227\n"
AW "a16,v220\n"
MF "v[36:39],a[108:111],v[140:143],v[36:39]\n"
AW "a17,v221\n"
AW "a18,v222\n"
MF "v[36:39],a[68:71],v[136:139],v[36:39]\n"
AW "a19,v223\n"
AR "v228,a56\n"
MF "v[36:39],a[28:31],v[132:135],v[36:39]\n"
AR "v229,a57\n"
AR "v230,a58\n"
MF "v[36:39],a[88:91],v[128:131],v[36:39]\n"
AR "v231,a59\n"
AR "v232,a76\n"
MF "v[36:39],a[48:51],v[124:127],v[36:39]\n"
AR "v233,a77\n"
AR "v234,a78\n"
MF "v[36:39],a[8:11],v[120:123],v[36:39]\n"
AR "v235,a79\n"
"v_fma_f32 v228,v228,v212,v218\n"
MF "v[72:75],a[8:11],v[144:147],v[72:75]\n"
"ds_read_b128 a[76:79],v236 offset:61440\n"
"v_fma_f32 v229,v229,v212,v218\n"
MF "v[72:75],a[48:51],v[148:151],v[72:75]\n"
"v_fma_f32 v230,v230,v212,v218\n"
"v_fma_f32 v231,v231,v212,v218\n"
MF "v[72:75],a[88:91],v[152:155],v[72:75]\n"
"v_fma_f32 v232,v232,v212,v218\n"
"v_fma_f32 v233,v233,v212,v218\n"
MF "v[72:75],a[28:31],v[156:159],v[72:75]\n"
"v_fma_f32 v234,v234,v212,v218\n"
"v_fma_f32 v235,v235,v212,v218\n"
MF "v[72:75],a[68:71],v[160:163],v[72:75]\n"
CV "v228,v228,v229\n"
CV "v229,v230,v231\n"
MF "v[72:75],a[108:111],v[164:167],v[72:75]\n"
CV "v230,v232,v233\n"
CV "v231,v234,v235\n"
MF "v[76:79],a[108:111],v[238:241],v[76:79]\n"
AW "a56,v228\n"
AW "a57,v229\n"
MF "v[76:79],a[68:71],v[248:251],v[76:79]\n"
AW "a58,v230\n"
AW "a59,v231\n"
MF "v[76:79],a[28:31],v[244:247],v[76:79]\n"
AR "v220,a96\n"
AR "v221,a97\n"
MF "v[76:79],a[88:91],v[204:207],v[76:79]\n"
AR "v222,a98\n"
AR "v223,a99\n"
MF "v[76:79],a[48:51],v[172:175],v[76:79]\n"
AR "v224,a116\n"
AR "v225,a117\n"
MF "v[76:79],a[8:11],v[168:171],v[76:79]\n"
"s_waitcnt lgkmcnt(2)\n"
AR "v226,a118\n"
AR "v227,a119\n"
MF "v[84:87],a[12:15],v[168:171],v[84:87]\n"
"ds_read_b128 a[116:119],v237 offset:16384\n"
"v_fma_f32 v220,v220,v212,v218\n"
MF "v[84:87],a[52:55],v[172:175],v[84:87]\n"
"v_fma_f32 v221,v221,v212,v218\n"
"v_fma_f32 v222,v222,v212,v218\n"
MF "v[84:87],a[92:95],v[204:207],v[84:87]\n"
"v_fma_f32 v223,v223,v212,v218\n"
"v_fma_f32 v224,v224,v212,v218\n"
MF "v[84:87],a[32:35],v[244:247],v[84:87]\n"
"v_fma_f32 v225,v225,v212,v218\n"
"v_fma_f32 v226,v226,v212,v218\n"
MF "v[84:87],a[72:75],v[248:251],v[84:87]\n"
"v_fma_f32 v227,v227,v212,v218\n"
CV "v220,v220,v221\n"
MF "v[84:87],a[112:115],v[238:241],v[84:87]\n"
CV "v221,v222,v223\n"
CV "v222,v224,v225\n"
MF "v[80:83],a[112:115],v[164:167],v[80:83]\n"
CV "v223,v226,v227\n"
AW "a96,v220\n"
MF "v[80:83],a[72:75],v[160:163],v[80:83]\n"
AW "a97,v221\n"
AW "a98,v222\n"
MF "v[80:83],a[32:35],v[156:159],v[80:83]\n"
AW "a99,v223\n"
"v_mov_b32 v15,0\n"
MF "v[80:83],a[92:95],v[152:155],v[80:83]\n"
"v_lshrrev_b32 v242,5,v9\n"
"v_min_u32 v242,23,v242\n"
MF "v[80:83],a[52:55],v[148:151],v[80:83]\n"
"v_mul_u32_u24 v242,160,v242\n"
"v_and_or_b32 v242,v9,16,v242\n"
MF "v[80:83],a[12:15],v[144:147],v[80:83]\n"
"v_lshlrev_b32 v242,3,v242\n"
AR "v8,a252\n"
MF "v[44:47],a[12:15],v[120:123],v[44:47]\n"
AR "v11,a253\n"
"v_bfe_u32 v224,v9,5,3\n"
MF "v[44:47],a[52:55],v[124:127],v[44:47]\n"
"s_add_u32 s62,s74,s68\n"
"s_add_u32 s68,s68,256\n"
"s_and_b32 s68,s68,768\n"
MF "v[44:47],a[92:95],v[128:131],v[44:47]\n"
"buffer_load_dword v243,v242,s[48:51],s62 offen\n"
MF "v[44:47],a[32:35],v[132:135],v[44:47]\n"
"v_bfe_u32 v225,v9,8,2\n"
"v_lshlrev_b32 v225,1,v225\n"
MF "v[44:47],a[72:75],v[136:139],v[44:47]\n"
"v_mov_b32 v14,s76\n"
"s_add_u32 s76,s76,1\n"
MF "v[44:47],a[112:115],v[140:143],v[44:47]\n"
"buffer_store_dword v14,v15,s[40:43],s94 offen sc1\n"
MF "v[40:43],a[112:115],v[116:119],v[40:43]\n"
"v_and_b32 v226,16,v9\n"
"v_lshlrev_b32_e64 v226,v226,s9\n"
MF "v[40:43],a[72:75],v[112:115],v[40:43]\n"
"v_sub_u32 v224,v224,v225\n"
"v_cmp_eq_u32 vcc,0,v224\n"
MF "v[40:43],a[32:35],v[108:111],v[40:43]\n"
"s_nop 0\n"
"v_cndmask_b32 v220,0,v226,vcc\n"
"v_cmp_eq_u32 vcc,1,v224\n"
MF "v[40:43],a[92:95],v[104:107],v[40:43]\n"
"s_nop 0\n"
"v_cndmask_b32 v221,0,v226,vcc\n"
AW "a248,0\n"
MF "v[40:43],a[52:55],v[100:103],v[40:43]\n"
AW "a249,0\n"
AW "a250,v220\n"
MF "v[40:43],a[12:15],v[96:99],v[40:43]\n"
"s_waitcnt lgkmcnt(0)\n"
AW "a251,v221\n"
AW "a252,v220\n"
MF "v[48:51],a[16:19],v[96:99],v[48:51]\n"
AW "a253,v221\n"
AW "a254,0\n"
MF "v[48:51],a[56:59],v[100:103],v[48:51]\n"
AW "a255,0\n"
"v_bfe_u32 v12,v9,4,4\n"
MF "v[48:51],a[96:99],v[104:107],v[48:51]\n"
"v_lshlrev_b32 v12,2,v12\n"
"v_lshrrev_b32 v13,2,v9\n"
MF "v[48:51],a[36:39],v[108:111],v[48:51]\n"
"v_add_u32 v13,s58,v13\n"
"v_add_u32 v4,24576,v4\n"
MF "v[48:51],a[76:79],v[112:115],v[48:51]\n"
"v_add_u32 v5,24576,v5\n"
"v_add_u32 v6,24576,v6\n"
MF "v[48:51],a[116:119],v[116:119],v[48:51]\n"
"v_add_u32 v7,24576,v7\n"
MF "v[52:55],a[116:119],v[140:143],v[52:55]\n"
MF "v[52:55],a[76:79],v[136:139],v[52:55]\n"
MF "v[52:55],a[36:39],v[132:135],v[52:55]\n"
".Lpg26h:\n"
"s_cbranch_execz .Lpgjobs_h\n"
"s_waitcnt vmcnt(2) lgkmcnt(0)\n"
"s_barrier\n"
MF "v[52:55],a[96:99],v[128:131],v[52:55]\n"
TR "v[176:177],v0 offset:24576\n"
MF "v[52:55],a[56:59],v[124:127],v[52:55]\n"
TR "v[178:179],v0 offset:26624\n"
MF "v[52:55],a[16:19],v[120:123],v[52:55]\n"
MF "v[88:91],a[16:19],v[144:147],v[88:91]\n"
TR "v[180:181],v0 offset:28672\n"
MF "v[88:91],a[56:59],v[148:151],v[88:91]\n"
TR "v[182:183],v0 offset:30720\n"
MF "v[88:91],a[96:99],v[152:155],v[88:91]\n"
MF "v[88:91],a[36:39],v[156:159],v[88:91]\n"
TR "v[184:185],v0 offset:32768\n"
MF "v[88:91],a[76:79],v[160:163],v[88:91]\n"
TR "v[186:187],v0 offset:34816\n"
MF "v[88:91],a[116:119],v[164:167],v[88:91]\n"
MF "v[92:95],a[116:119],v[238:241],v[92:95]\n"
MF "v[92:95],a[76:79],v[248:251],v[92:95]\n"
MF "v[92:95],a[36:39],v[244:247],v[92:95]\n"
MF "v[92:95],a[96:99],v[204:207],v[92:95]\n"
MF "v[92:95],a[56:59],v[172:175],v[92:95]\n"
MF "v[92:95],a[16:19],v[168:171],v[92:95]\n"
"s_waitcnt lgkmcnt(0)\n"
".Lpl1:\n"
MF "v[96:99],a[0:3],v[176:179],v[252:255]\n"
TR "v[188:189],v1 offset:24576\n"
MF "v[96:99],a[40:43],v[180:183],v[96:99]\n"
"s_add_u32 s82,s82,s71\n"
TR "v[190:191],v1 offset:26624\n"
MF "v[96:99],a[80:83],v[184:187],v[96:99]\n"
LD "v[224:227],v9,s[40:43],s82 offen sc1\n"
MF "v[104:107],a[84:87],v[184:187],v[252:255]\n"
TR "a[240:241],v1 offset:28672\n"
MF "v[104:107],a[44:47],v[180:183],v[104:107]\n"
TR "a[242:243],v1 offset:30720\n"
MF "v[104:107],a[4:7],v[176:179],v[104:107]\n"
MF "v[112:115],a[8:11],v[176:179],v[252:255]\n"
TR "a[244:245],v1 offset:32768\n"
MF "v[112:115],a[48:51],v[180:183],v[112:115]\n"
TR "a[246:247],v1 offset:34816\n"
MF "v[112:115],a[88:91],v[184:187],v[112:115]\n"
MF "v[120:123],a[92:95],v[184:187],v[252:255]\n"
MF "v[120:123],a[52:55],v[180:183],v[120:123]\n"
"s_mul_i32 s95,s92,3277\n"
"s_lshr_b32 s95,s95,16\n"
"s_mul_i32 s59,s95,20\n"
MF "v[120:123],a[12:15],v[176:179],v[120:123]\n"
"s_sub_u32 s59,s92,s59\n"
"s_cmp_ge_u32 s59,10\n"
"s_cselect_b32 s96,4294557760,0\n"
MF "v[128:131],a[16:19],v[176:179],v[252:255]\n"
"s_add_u32 s82,s82,s66\n"
"s_add_u32 s96,s96,1024\n"
"s_mul_i32 s59,s59,40960\n"
MF "v[128:131],a[56:59],v[180:183],v[128:131]\n"
LD "v[228:231],v9,s[40:43],s82 offen sc1\n"
MF "v[128:131],a[96:99],v[184:187],v[128:131]\n"
"s_add_u32 s96,s96,s59\n"
"s_lshl_b32 s59,s95,7\n"
"s_waitcnt lgkmcnt(0)\n"
MF "v[132:135],a[96:99],a[244:247],v[252:255]\n"
"s_add_u32 s96,s96,s59\n"
"s_add_u32 s95,s95,3\n"
TR "v[176:177],v2 offset:24576\n"
MF "v[132:135],a[56:59],a[240:243],v[132:135]\n"
"s_cmp_ge_u32 s92,40\n"
"s_cselect_b32 s95,99,s95\n"
TR "v[178:179],v2 offset:26624\n"
MF "v[132:135],a[16:19],v[188:191],v[132:135]\n"
"s_cmp_ge_u32 s85,5\n"
"s_cselect_b32 s95,99,s95\n"
"s_cselect_b32 s72,s85,0\n"
MF "v[124:127],a[12:15],v[188:191],v[252:255]\n"
TR "v[180:181],v2 offset:28672\n"
MF "v[124:127],a[52:55],a[240:243],v[124:127]\n"
TR "v[182:183],v2 offset:30720\n"
MF "v[124:127],a[92:95],a[244:247],v[124:127]\n"
MF "v[116:119],a[88:91],a[244:247],v[252:255]\n"
TR "v[184:185],v2 offset:32768\n"
MF "v[116:119],a[48:51],a[240:243],v[116:119]\n"
"s_add_u32 s82,s82,s71\n"
TR "v[186:187],v2 offset:34816\n"
MF "v[116:119],a[8:11],v[188:191],v[116:119]\n"
LD "v[232:235],v9,s[40:43],s82 offen sc1\n"
MF "v[108:111],a[4:7],v[188:191],v[252:255]\n"
MF "v[108:111],a[44:47],a[240:243],v[108:111]\n"
MF "v[108:111],a[84:87],a[244:247],v[108:111]\n"
MF "v[100:103],a[80:83],a[244:247],v[252:255]\n"
MF "v[100:103],a[40:43],a[240:243],v[100:103]\n"
MF "v[100:103],a[0:3],v[188:191],v[100:103]\n"
"s_waitcnt lgkmcnt(0)\n"
MF "v[136:139],a[0:3],v[176:179],v[252:255]\n"
TR "v[188:189],v3 offset:24576\n"
MF "v[136:139],a[40:43],v[180:183],v[136:139]\n"
"s_add_u32 s82,s82,s71\n"
TR "v[190:191],v3 offset:26624\n"
MF "v[136:139],a[80:83],v[184:187],v[136:139]\n"
LD "v[236:239],v9,s[40:43],s82 offen sc1\n"
MF "v[144:147],a[84:87],v[184:187],v[252:255]\n"
TR "a[240:241],v3 offset:28672\n"
MF "v[144:147],a[44:47],v[180:183],v[144:147]\n"
TR "a[242:243],v3 offset:30720\n"
MF "v[144:147],a[4:7],v[176:179],v[144:147]\n"
MF "v[152:155],a[8:11],v[176:179],v[252:255]\n"
TR "a[244:245],v3 offset:32768\n"
MF "v[152:155],a[48:51],v[180:183],v[152:155]\n"
TR "a[246:247],v3 offset:34816\n"
MF "v[152:155],a[88:91],v[184:187],v[152:155]\n"
MF "v[160:163],a[92:95],v[184:187],v[252:255]\n"
MF "v[160:163],a[52:55],v[180:183],v[160:163]\n"
MF "v[160:163],a[12:15],v[176:179],v[160:163]\n"
MF "v[168:171],a[16:19],v[176:179],v[252:255]\n"
MF "v[168:171],a[56:59],v[180:183],v[168:171]\n"
MF "v[168:171],a[96:99],v[184:187],v[168:171]\n"
"s_waitcnt lgkmcnt(0)\n"
"s_barrier\n"
MF "v[172:175],a[96:99],a[244:247],v[252:255]\n"
TR "v[176:177],v0\n"
MF "v[172:175],a[56:59],a[240:243],v[172:175]\n"
TR "v[178:179],v0 offset:2048\n"
MF "v[172:175],a[16:19],v[188:191],v[172:175]\n"
"ds_read2st64_b32 v[192:193],v13 offset0:192 offset1:193\n"
MF "v[164:167],a[12:15],v[188:191],v[252:255]\n"
TR "v[180:181],v0 offset:4096\n"
MF "v[164:167],a[52:55],a[240:243],v[164:167]\n"
TR "v[182:183],v0 offset:6144\n"
MF "v[164:167],a[92:95],a[244:247],v[164:167]\n"
"ds_read2st64_b32 v[194:195],v13 offset0:194 offset1:195\n"
MF "v[156:159],a[88:91],a[244:247],v[252:255]\n"
TR "v[184:185],v0 offset:8192\n"
MF "v[156:159],a[48:51],a[240:243],v[156:159]\n"
TR "v[186:187],v0 offset:10240\n"
MF "v[156:159],a[8:11],v[188:191],v[156:159]\n"
"ds_read2st64_b32 v[196:197],v13 offset0:196 offset1:197\n"
MF "v[148:151],a[4:7],v[188:191],v[252:255]\n"
"ds_read2st64_b32 v[198:199],v13 offset0:198 offset1:199\n"
MF "v[148:151],a[44:47],a[240:243],v[148:151]\n"
"buffer_load_dword v10,v12,s[40:43],s86 offen sc1\n"
MF "v[148:151],a[84:87],a[244:247],v[148:151]\n"
MF "v[140:143],a[80:83],a[244:247],v[252:255]\n"
MF "v[140:143],a[40:43],a[240:243],v[140:143]\n"
MF "v[140:143],a[0:3],v[188:191],v[140:143]\n"
"s_waitcnt lgkmcnt(2)\n"
MF "v[96:99],a[120:123],v[176:179],v[96:99]\n"
"s_waitcnt lgkmcnt(0)\n"
TR "v[188:189],v1\n"
"v_add_f32 v16,v16,v192\n"
MF "v[96:99],a[160:163],v[180:183],v[96:99]\n"
TR "v[190:191],v1 offset:2048\n"
"v_add_f32 v17,v17,v193\n"
MF "v[96:99],a[200:203],v[184:187],v[96:99]\n"
"ds_read2st64_b32 v[200:201],v13 offset0:200 offset1:201\n"
"v_add_f32 v18,v18,v194\n"
"s_waitcnt vmcnt(9)\n"
"v_mov_b32 v14,s76\n"
"s_add_u32 s76,s76,1\n"
MF "v[104:107],a[204:207],v[184:187],v[104:107]\n"
TR "a[240:241],v1 offset:4096\n"
"v_add_f32 v19,v19,v195\n"
"buffer_store_dword v14,v15,s[40:43],s94 offen sc1\n"
MF "v[104:107],a[164:167],v[180:183],v[104:107]\n"
TR "a[242:243],v1 offset:6144\n"
"v_add_f32 v20,v20,v196\n"
MF "v[104:107],a[124:127],v[176:179],v[104:107]\n"
"ds_read2st64_b32 v[202:203],v13 offset0:202 offset1:203\n"
"v_add_f32 v21,v21,v197\n"
MF "v[112:115],a[128:131],v[176:179],v[112:115]\n"
TR "a[244:245],v1 offset:8192\n"
"v_add_f32 v22,v22,v198\n"
MF "v[112:115],a[168:171],v[180:183],v[112:115]\n"
TR "a[246:247],v1 offset:10240\n"
"v_add_f32 v23,v23,v199\n"
MF "v[112:115],a[208:211],v[184:187],v[112:115]\n"
"ds_read2st64_b32 v[204:205],v13 offset0:204 offset1:205\n"
CV "v16,v16,v17\n"
MF "v[120:123],a[212:215],v[184:187],v[120:123]\n"
"ds_read2st64_b32 v[206:207],v13 offset0:206 offset1:207\n"
CV "v17,v18,v19\n"
MF "v[120:123],a[172:175],v[180:183],v[120:123]\n"
"s_add_u32 s87,s90,0\n"
CV "v18,v20,v21\n"
CV "v19,v22,v23\n"
MF "v[120:123],a[132:135],v[176:179],v[120:123]\n"
MF "v[128:131],a[136:139],v[176:179],v[128:131]\n"
"buffer_store_dwordx4 v[16:19],v9,s[40:43],s87 offen sc1\n"
MF "v[128:131],a[176:179],v[180:183],v[128:131]\n"
MF "v[128:131],a[216:219],v[184:187],v[128:131]\n"
"s_waitcnt lgkmcnt(2)\n"
MF "v[132:135],a[216:219],a[244:247],v[132:135]\n"
"s_waitcnt lgkmcnt(0)\n"
TR "v[176:177],v2\n"
"v_add_f32 v24,v24,v200\n"
MF "v[132:135],a[176:179],a[240:243],v[132:135]\n"
TR "v[178:179],v2 offset:2048\n"
"v_add_f32 v25,v25,v201\n"
MF "v[132:135],a[136:139],v[188:191],v[132:135]\n"
"ds_read2st64_b32 v[192:193],v13 offset0:208 offset1:209\n"
"v_add_f32 v26,v26,v202\n"
MF "v[124:127],a[132:135],v[188:191],v[124:127]\n"
TR "v[180:181],v2 offset:4096\n"
"v_add_f32 v27,v27,v203\n"
MF "v[124:127],a[172:175],a[240:243],v[124:127]\n"
TR "v[182:183],v2 offset:6144\n"
"v_add_f32 v28,v28,v204\n"
MF "v[124:127],a[212:215],a[244:247],v[124:127]\n"
"ds_read2st64_b32 v[194:195],v13 offset0:210 offset1:211\n"
"v_add_f32 v29,v29,v205\n"
MF "v[116:119],a[208:211],a[244:247],v[116:119]\n"
TR "v[184:185],v2 offset:8192\n"
"v_add_f32 v30,v30,v206\n"
MF "v[116:119],a[168:171],a[240:243],v[116:119]\n"
TR "v[186:187],v2 offset:10240\n"
"v_add_f32 v31,v31,v207\n"
MF "v[116:119],a[128:131],v[188:191],v[116:119]\n"
"ds_read2st64_b32 v[196:197],v13 offset0:212 offset1:213\n"
CV "v24,v24,v25\n"
MF "v[108:111],a[124:127],v[188:191],v[108:111]\n"
"ds_read2st64_b32 v[198:199],v13 offset0:214 offset1:215\n"
CV "v25,v26,v27\n"
MF "v[108:111],a[164:167],a[240:243],v[108:111]\n"
"s_add_u32 s87,s90,1024\n"
CV "v26,v28,v29\n"
CV "v27,v30,v31\n"
MF "v[108:111],a[204:207],a[244:247],v[108:111]\n"
MF "v[100:103],a[200:203],a[244:247],v[100:103]\n"
"buffer_store_dwordx4 v[24:27],v9,s[40:43],s87 offen sc1\n"
MF "v[100:103],a[160:163],a[240:243],v[100:103]\n"
MF "v[100:103],a[120:123],v[188:191],v[100:103]\n"
"s_waitcnt lgkmcnt(2)\n"
MF "v[136:139],a[120:123],v[176:179],v[136:139]\n"
"s_waitcnt lgkmcnt(0)\n"
TR "v[188:189],v3\n"
"v_add_f32 v32,v32,v192\n"
MF "v[136:139],a[160:163],v[180:183],v[136:139]\n"
TR "v[190:191],v3 offset:2048\n"
"v_add_f32 v33,v33,v193\n"
MF "v[136:139],a[200:203],v[184:187],v[136:139]\n"
"ds_read2st64_b32 v[200:201],v13 offset0:216 offset1:217\n"
"v_add_f32 v34,v34,v194\n"
MF "v[144:147],a[204:207],v[184:187],v[144:147]\n"
TR "a[240:241],v3 offset:4096\n"
"v_add_f32 v35,v35,v195\n"
MF "v[144:147],a[164:167],v[180:183],v[144:147]\n"
TR "a[242:243],v3 offset:6144\n"
"v_add_f32 v36,v36,v196\n"
MF "v[144:147],a[124:127],v[176:179],v[144:147]\n"
"ds_read2st64_b32 v[202:203],v13 offset0:218 offset1:219\n"
"v_add_f32 v37,v37,v197\n"
MF "v[152:155],a[128:131],v[176:179],v[152:155]\n"
TR "a[244:245],v3 offset:8192\n"
"v_add_f32 v38,v38,v198\n"
MF "v[152:155],a[168:171],v[180:183],v[152:155]\n"
TR "a[246:247],v3 offset:10240\n"
"v_add_f32 v39,v39,v199\n"
MF "v[152:155],a[208:211],v[184:187],v[152:155]\n"
"ds_read2st64_b32 v[204:205],v13 offset0:220 offset1:221\n"
CV "v32,v32,v33\n"
MF "v[160:163],a[212:215],v[184:187],v[160:163]\n"
"ds_read2st64_b32 v[206:207],v13 offset0:222 offset1:223\n"
CV "v33,v34,v35\n"
MF "v[160:163],a[172:175],v[180:183],v[160:163]\n"
"s_add_u32 s87,s90,2048\n"
CV "v34,v36,v37\n"
CV "v35,v38,v39\n"
MF "v[160:163],a[132:135],v[176:179],v[160:163]\n"
MF "v[168:171],a[136:139],v[176:179],v[168:171]\n"
"buffer_store_dwordx4 v[32:35],v9,s[40:43],s87 offen sc1\n"
MF "v[168:171],a[176:179],v[180:183],v[168:171]\n"
MF "v[168:171],a[216:219],v[184:187],v[168:171]\n"
"s_waitcnt lgkmcnt(0)\n"
"s_barrier\n"
MF "v[172:175],a[216:219],a[244:247],v[172:175]\n"
TR "v[176:177],v0 offset:49152\n"
"v_add_f32 v40,v40,v200\n"
MF "v[172:175],a[176:179],a[240:243],v[172:175]\n"
TR "v[178:179],v0 offset:51200\n"
"v_add_f32 v41,v41,v201\n"
MF "v[172:175],a[136:139],v[188:191],v[172:175]\n"
"ds_read2st64_b32 v[192:193],v13 offset0:224 offset1:225\n"
"v_add_f32 v42,v42,v202\n"
MF "v[164:167],a[132:135],v[188:191],v[164:167]\n"
TR "v[180:181],v0 offset:53248\n"
"v_add_f32 v43,v43,v203\n"
MF "v[164:167],a[172:175],a[240:243],v[164:167]\n"
TR "v[182:183],v0 offset:55296\n"
"v_add_f32 v44,v44,v204\n"
MF "v[164:167],a[212:215],a[244:247],v[164:167]\n"
"ds_read2st64_b32 v[194:195],v13 offset0:226 offset1:227\n"
"v_add_f32 v45,v45,v205\n"
MF "v[156:159],a[208:211],a[244:247],v[156:159]\n"
TR "v[184:185],v0 offset:57344\n"
"v_add_f32 v46,v46,v206\n"
MF "v[156:159],a[168:171],a[240:243],v[156:159]\n"
TR "v[186:187],v0 offset:59392\n"
"v_add_f32 v47,v47,v207\n"
MF "v[156:159],a[128:131],v[188:191],v[156:159]\n"
"ds_read2st64_b32 v[196:197],v13 offset0:228 offset1:229\n"
CV "v40,v40,v41\n"
MF "v[148:151],a[124:127],v[188:191],v[148:151]\n"
"ds_read2st64_b32 v[198:199],v13 offset0:230 offset1:231\n"
CV "v41,v42,v43\n"
MF "v[148:151],a[164:167],a[240:243],v[148:151]\n"
"s_add_u32 s87,s90,3072\n"
CV "v42,v44,v45\n"
CV "v43,v46,v47\n"
MF "v[148:151],a[204:207],a[244:247],v[148:151]\n"
MF "v[140:143],a[200:203],a[244:247],v[140:143]\n"
"buffer_store_dwordx4 v[40:43],v9,s[40:43],s87 offen sc1\n"
MF "v[140:143],a[160:163],a[240:243],v[140:143]\n"
MF "v[140:143],a[120:123],v[188:191],v[140:143]\n"
"s_waitcnt lgkmcnt(2)\n"
MF "v[96:99],a[20:23],v[176:179],v[96:99]\n"
"s_waitcnt lgkmcnt(0)\n"
TR "v[188:189],v1 offset:49152\n"
"v_add_f32 v48,v48,v192\n"
MF "v[96:99],a[60:63],v[180:183],v[96:99]\n"
TR "v[190:191],v1 offset:51200\n"
"v_add_f32 v49,v49,v193\n"
MF "v[96:99],a[100:103],v[184:187],v[96:99]\n"
"ds_read2st64_b32 v[200:201],v13 offset0:0 offset1:1\n"
"v_add_f32 v50,v50,v194\n"
MF "v[104:107],a[104:107],v[184:187],v[104:107]\n"
TR "a[240:241],v1 offset:53248\n"
"v_add_f32 v51,v51,v195\n"
MF "v[104:107],a[64:67],v[180:183],v[104:107]\n"
TR "a[242:243],v1 offset:55296\n"
"v_add_f32 v52,v52,v196\n"
MF "v[104:107],a[24:27],v[176:179],v[104:107]\n"
"ds_read2st64_b32 v[202:203],v13 offset0:2 offset1:3\n"
"v_add_f32 v53,v53,v197\n"
MF "v[112:115],a[28:31],v[176:179],v[112:115]\n"
TR "a[244:245],v1 offset:57344\n"
"v_add_f32 v54,v54,v198\n"
MF "v[112:115],a[68:71],v[180:183],v[112:115]\n"
TR "a[246:247],v1 offset:59392\n"
"v_add_f32 v55,v55,v199\n"
MF "v[112:115],a[108:111],v[184:187],v[112:115]\n"
"ds_read2st64_b32 v[204:205],v13 offset0:4 offset1:5\n"
CV "v48,v48,v49\n"
MF "v[120:123],a[112:115],v[184:187],v[120:123]\n"
"ds_read2st64_b32 v[206:207],v13 offset0:6 offset1:7\n"
CV "v49,v50,v51\n"
MF "v[120:123],a[72:75],v[180:183],v[120:123]\n"
"s_add_u32 s87,s90,4096\n"
CV "v50,v52,v53\n"
CV "v51,v54,v55\n"
MF "v[120:123],a[32:35],v[176:179],v[120:123]\n"
MF "v[128:131],a[36:39],v[176:179],v[128:131]\n"
"buffer_store_dwordx4 v[48:51],v9,s[40:43],s87 offen sc1\n"
MF "v[128:131],a[76:79],v[180:183],v[128:131]\n"
MF "v[128:131],a[116:119],v[184:187],v[128:131]\n"
"s_waitcnt lgkmcnt(2)\n"
MF "v[132:135],a[116:119],a[244:247],v[132:135]\n"
"s_waitcnt lgkmcnt(0)\n"
TR "v[176:177],v2 offset:49152\n"
"v_add_f32 v56,v56,v200\n"
MF "v[132:135],a[76:79],a[240:243],v[132:135]\n"
TR "v[178:179],v2 offset:51200\n"
"v_add_f32 v57,v57,v201\n"
MF "v[132:135],a[36:39],v[188:191],v[132:135]\n"
"ds_read2st64_b32 v[192:193],v13 offset0:8 offset1:9\n"
"v_add_f32 v58,v58,v202\n"
MF "v[124:127],a[32:35],v[188:191],v[124:127]\n"
TR "v[180:181],v2 offset:53248\n"
"v_add_f32 v59,v59,v203\n"
MF "v[124:127],a[72:75],a[240:243],v[124:127]\n"
TR "v[182:183],v2 offset:55296\n"
"v_add_f32 v60,v60,v204\n"
MF "v[124:127],a[112:115],a[244:247],v[124:127]\n"
"ds_read2st64_b32 v[194:195],v13 offset0:10 offset1:11\n"
"v_add_f32 v61,v61,v205\n"
MF "v[116:119],a[108:111],a[244:247],v[116:119]\n"
TR "v[184:185],v2 offset:57344\n"
"v_add_f32 v62,v62,v206\n"
MF "v[116:119],a[68:71],a[240:243],v[116:119]\n"
TR "v[186:187],v2 offset:59392\n"
"v_add_f32 v63,v63,v207\n"
MF "v[116:119],a[28:31],v[188:191],v[116:119]\n"
"ds_read2st64_b32 v[196:197],v13 offset0:12 offset1:13\n"
CV "v56,v56,v57\n"
MF "v[108:111],a[24:27],v[188:191],v[108:111]\n"
"ds_read2st64_b32 v[198:199],v13 offset0:14 offset1:15\n"
CV "v57,v58,v59\n"
MF "v[108:111],a[64:67],a[240:243],v[108:111]\n"
"s_add_u32 s87,s90,10240\n"
CV "v58,v60,v61\n"
CV "v59,v62,v63\n"
MF "v[108:111],a[104:107],a[244:247],v[108:111]\n"
MF "v[100:103],a[100:103],a[244:247],v[100:103]\n"
"buffer_store_dwordx4 v[56:59],v9,s[40:43],s87 offen sc1\n"
MF "v[100:103],a[60:63],a[240:243],v[100:103]\n"
"s_waitcnt vmcnt(7)\n"
"s_cmp_eq_u32 s83,0xf0000000\n"
MF "v[100:103],a[20:23],v[188:191],v[100:103]\n"
"s_cbranch_scc1 .Ljs13\n"
"s_cmp_eq_u32 s83,0xf0000010\n"
"s_cbranch_scc1 .Ljc13\n"
MF "v[248:251],v[210:213],a[250:253],0\n"
"v_mov_b32 v240,v208\n"
"v_mov_b32 v241,v209\n"
MF "v[244:247],v[214:217],a[250:253],0\n"
MF "v[248:251],v[218:221],a[250:253],v[248:251]\n"
MF "v[244:247],v[222:225],a[250:253],v[244:247]\n"
MF "v[248:251],v[226:229],a[250:253],v[248:251]\n"
MF "v[244:247],v[230:233],a[250:253],v[244:247]\n"
MF "v[248:251],v[234:237],a[250:253],v[248:251]\n"
MF "v[244:247],v[238:241],a[250:253],v[244:247]\n"
"s_branch .Ljs13\n"
".Ljc13:\n"
MF "v[136:139],a[252:255],v[208:211],v[136:139]\n"
MF "v[144:147],a[252:255],v[212:215],v[144:147]\n"
MF "v[152:155],a[252:255],v[216:219],v[152:155]\n"
MF "v[160:163],a[252:255],v[220:223],v[160:163]\n"
MF "v[168:171],a[252:255],v[224:227],v[168:171]\n"
MF "v[140:143],a[248:251],v[208:211],v[140:143]\n"
MF "v[148:151],a[248:251],v[212:215],v[148:151]\n"
MF "v[156:159],a[248:251],v[216:219],v[156:159]\n"
MF "v[164:167],a[248:251],v[220:223],v[164:167]\n"
MF "v[172:175],a[248:251],v[224:227],v[172:175]\n"
".Ljs13:\n"
"s_waitcnt lgkmcnt(2)\n"
MF "v[136:139],a[20:23],v[176:179],v[136:139]\n"
"s_waitcnt lgkmcnt(0)\n"
TR "v[188:189],v3 offset:49152\n"
"v_add_f32 v64,v64,v192\n"
MF "v[136:139],a[60:63],v[180:183],v[136:139]\n"
TR "v[190:191],v3 offset:51200\n"
"v_add_f32 v65,v65,v193\n"
MF "v[136:139],a[100:103],v[184:187],v[136:139]\n"
"ds_read2st64_b32 v[200:201],v13 offset0:16 offset1:17\n"
"v_add_f32 v66,v66,v194\n"
MF "v[144:147],a[104:107],v[184:187],v[144:147]\n"
TR "a[240:241],v3 offset:53248\n"
"v_add_f32 v67,v67,v195\n"
MF "v[144:147],a[64:67],v[180:183],v[144:147]\n"
TR "a[242:243],v3 offset:55296\n"
"v_add_f32 v68,v68,v196\n"
MF "v[144:147],a[24:27],v[176:179],v[144:147]\n"
"ds_read2st64_b32 v[202:203],v13 offset0:18 offset1:19\n"
"v_add_f32 v69,v69,v197\n"
MF "v[152:155],a[28:31],v[176:179],v[152:155]\n"
TR "a[244:245],v3 offset:57344\n"
"v_add_f32 v70,v70,v198\n"
MF "v[152:155],a[68:71],v[180:183],v[152:155]\n"
TR "a[246:247],v3 offset:59392\n"
"v_add_f32 v71,v71,v199\n"
MF "v[152:155],a[108:111],v[184:187],v[152:155]\n"
"ds_read2st64_b32 v[204:205],v13 offset0:20 offset1:21\n"
CV "v64,v64,v65\n"
MF "v[160:163],a[112:115],v[184:187],v[160:163]\n"
"ds_read2st64_b32 v[206:207],v13 offset0:22 offset1:23\n"
CV "v65,v66,v67\n"
MF "v[160:163],a[72:75],v[180:183],v[160:163]\n"
"s_add_u32 s87,s90,11264\n"
CV "v66,v68,v69\n"
CV "v67,v70,v71\n"
MF "v[160:163],a[32:35],v[176:179],v[160:163]\n"
"s_lshl_b32 s82,s92,10\n"
"v_sub_u32 v10,v10,v11\n"
"v_cmp_gt_u32 vcc,s95,v10\n"
MF "v[168:171],a[36:39],v[176:179],v[168:171]\n"
"buffer_store_dwordx4 v[64:67],v9,s[40:43],s87 offen sc1\n"
MF "v[168:171],a[76:79],v[180:183],v[168:171]\n"
"s_add_u32 s82,s82,s93\n"
CV "v244,v244,v245\n"
CV "v245,v246,v247\n"
MF "v[168:171],a[116:119],v[184:187],v[168:171]\n"
"s_waitcnt lgkmcnt(0)\n"
"s_barrier\n"
MF "v[172:175],a[116:119],a[244:247],v[172:175]\n"
TR "v[176:177],v4\n"
"v_add_f32 v72,v72,v200\n"
MF "v[172:175],a[76:79],a[240:243],v[172:175]\n"
TR "v[178:179],v4 offset:2048\n"
"v_add_f32 v73,v73,v201\n"
MF "v[172:175],a[36:39],v[188:191],v[172:175]\n"
"ds_read2st64_b32 v[192:193],v13 offset0:24 offset1:25\n"
"v_add_f32 v74,v74,v202\n"
MF "v[164:167],a[32:35],v[188:191],v[164:167]\n"
TR "v[180:181],v4 offset:4096\n"
"v_add_f32 v75,v75,v203\n"
MF "v[164:167],a[72:75],a[240:243],v[164:167]\n"
TR "v[182:183],v4 offset:6144\n"
"v_add_f32 v76,v76,v204\n"
MF "v[164:167],a[112:115],a[244:247],v[164:167]\n"
"ds_read2st64_b32 v[194:195],v13 offset0:26 offset1:27\n"
"v_add_f32 v77,v77,v205\n"
MF "v[156:159],a[108:111],a[244:247],v[156:159]\n"
TR "v[184:185],v4 offset:8192\n"
"v_add_f32 v78,v78,v206\n"
MF "v[156:159],a[68:71],a[240:243],v[156:159]\n"
TR "v[186:187],v4 offset:10240\n"
"v_add_f32 v79,v79,v207\n"
MF "v[156:159],a[28:31],v[188:191],v[156:159]\n"
"ds_read2st64_b32 v[196:197],v13 offset0:28 offset1:29\n"
CV "v72,v72,v73\n"
MF "v[148:151],a[24:27],v[188:191],v[148:151]\n"
"ds_read2st64_b32 v[198:199],v13 offset0:30 offset1:31\n"
CV "v73,v74,v75\n"
MF "v[148:151],a[64:67],a[240:243],v[148:151]\n"
"s_add_u32 s87,s90,12288\n"
CV "v74,v76,v77\n"
CV "v75,v78,v79\n"
MF "v[148:151],a[104:107],a[244:247],v[148:151]\n"
"s_cmp_eq_u64 vcc,0\n"
"s_cselect_b32 s82,s82,0xf0000000\n"
"s_cselect_b32 s84,s96,0xf0000000\n"
"s_cselect_b32 s59,32,0\n"
MF "v[140:143],a[100:103],a[244:247],v[140:143]\n"
"buffer_store_dwordx4 v[72:75],v9,s[40:43],s87 offen sc1\n"
MF "v[140:143],a[60:63],a[240:243],v[140:143]\n"
"s_add_u32 s92,s92,s59\n"
".Lnf5:\n"
"v_readlane_b32 s58,v10,s67\n"
"s_cmp_lt_u32 s58,s72\n"
"s_cbranch_scc1 .Lsp5\n"
".Lnb5:\n"
MF "v[140:143],a[20:23],v[188:191],v[140:143]\n"
"s_waitcnt lgkmcnt(2)\n"
CV "v246,v248,v249\n"
CV "v247,v250,v251\n"
MF "v[96:99],a[140:143],v[176:179],v[96:99]\n"
"s_waitcnt lgkmcnt(0)\n"
TR "v[188:189],v5\n"
"v_add_f32 v80,v80,v192\n"
MF "v[96:99],a[180:183],v[180:183],v[96:99]\n"
TR "v[190:191],v5 offset:2048\n"
"v_add_f32 v81,v81,v193\n"
MF "v[96:99],a[220:223],v[184:187],v[96:99]\n"
"ds_read2st64_b32 v[200:201],v13 offset0:32 offset1:33\n"
"v_add_f32 v82,v82,v194\n"
MF "v[104:107],a[224:227],v[184:187],v[104:107]\n"
TR "a[240:241],v5 offset:4096\n"
"v_add_f32 v83,v83,v195\n"
MF "v[104:107],a[184:187],v[180:183],v[104:107]\n"
TR "a[242:243],v5 offset:6144\n"
"v_add_f32 v84,v84,v196\n"
MF "v[104:107],a[144:147],v[176:179],v[104:107]\n"
"ds_read2st64_b32 v[202:203],v13 offset0:34 offset1:35\n"
"v_add_f32 v85,v85,v197\n"
MF "v[112:115],a[148:151],v[176:179],v[112:115]\n"
TR "a[244:245],v5 offset:8192\n"
"v_add_f32 v86,v86,v198\n"
MF "v[112:115],a[188:191],v[180:183],v[112:115]\n"
TR "a[246:247],v5 offset:10240\n"
"v_add_f32 v87,v87,v199\n"
MF "v[112:115],a[228:231],v[184:187],v[112:115]\n"
"ds_read2st64_b32 v[204:205],v13 offset0:36 offset1:37\n"
CV "v80,v80,v81\n"
MF "v[120:123],a[232:235],v[184:187],v[120:123]\n"
"ds_read2st64_b32 v[206:207],v13 offset0:38 offset1:39\n"
CV "v81,v82,v83\n"
MF "v[120:123],a[192:195],v[180:183],v[120:123]\n"
"s_add_u32 s87,s90,13312\n"
CV "v82,v84,v85\n"
CV "v83,v86,v87\n"
MF "v[120:123],a[152:155],v[176:179],v[120:123]\n"
"s_cmp_ge_u32 s85,5\n"
"s_cselect_b32 s82,s70,s82\n"
"s_cselect_b32 s84,0xf0000010,s84\n"
MF "v[128:131],a[156:159],v[176:179],v[128:131]\n"
"buffer_store_dwordx4 v[80:83],v9,s[40:43],s87 offen sc1\n"
MF "v[128:131],a[196:199],v[180:183],v[128:131]\n"
"s_cmp_ge_u32 s85,5\n"
"s_cselect_b32 s71,1024,s71\n"
"s_cselect_b32 s66,0x80000000,s66\n"
MF "v[128:131],a[236:239],v[184:187],v[128:131]\n"
"s_add_u32 s70,s70,20480\n"
"s_waitcnt lgkmcnt(2)\n"
"v_permlane16_swap_b32 v244,v246\n"
MF "v[132:135],a[236:239],a[244:247],v[132:135]\n"
"s_waitcnt lgkmcnt(0)\n"
TR "v[176:177],v6\n"
"v_add_f32 v88,v88,v200\n"
MF "v[132:135],a[196:199],a[240:243],v[132:135]\n"
TR "v[178:179],v6 offset:2048\n"
"v_add_f32 v89,v89,v201\n"
MF "v[132:135],a[156:159],v[188:191],v[132:135]\n"
"v_add_f32 v90,v90,v202\n"
"v_add_f32 v91,v91,v203\n"
MF "v[124:127],a[152:155],v[188:191],v[124:127]\n"
TR "v[180:181],v6 offset:4096\n"
"v_add_f32 v92,v92,v204\n"
MF "v[124:127],a[192:195],a[240:243],v[124:127]\n"
TR "v[182:183],v6 offset:6144\n"
"v_add_f32 v93,v93,v205\n"
MF "v[124:127],a[232:235],a[244:247],v[124:127]\n"
".Lpg27h:\n"
"s_cbranch_execz .Lisr4\n"
"v_add_f32 v94,v94,v206\n"
"v_add_f32 v95,v95,v207\n"
MF "v[116:119],a[228:231],a[244:247],v[116:119]\n"
TR "v[184:185],v6 offset:8192\n"
CV "v88,v88,v89\n"
MF "v[116:119],a[188:191],a[240:243],v[116:119]\n"
TR "v[186:187],v6 offset:10240\n"
CV "v89,v90,v91\n"
MF "v[116:119],a[148:151],v[188:191],v[116:119]\n"
"s_add_u32 s87,s90,14336\n"
CV "v90,v92,v93\n"
CV "v91,v94,v95\n"
MF "v[108:111],a[144:147],v[188:191],v[108:111]\n"
"v_permlane16_swap_b32 v245,v247\n"
MF "v[108:111],a[184:187],a[240:243],v[108:111]\n"
"buffer_store_dwordx4 v[244:247],v8,s[44:47],s83 offen sc1\n"
MF "v[108:111],a[224:227],a[244:247],v[108:111]\n"
"s_mov_b32 s83,s84\n"
MF "v[100:103],a[220:223],a[244:247],v[100:103]\n"
"buffer_store_dwordx4 v[88:91],v9,s[40:43],s87 offen sc1\n"
MF "v[100:103],a[180:183],a[240:243],v[100:103]\n"
"s_mov_b32 s90,s91\n"
"s_add_u32 s91,s91,20480\n"
MF "v[100:103],a[140:143],v[188:191],v[100:103]\n"
"s_waitcnt lgkmcnt(0)\n"
LD "v[208:211],v9,s[40:43],s82 offen sc1\n"
MF "v[136:139],a[140:143],v[176:179],v[136:139]\n"
TR "v[188:189],v7\n"
MF "v[136:139],a[180:183],v[180:183],v[136:139]\n"
TR "v[190:191],v7 offset:2048\n"
MF "v[136:139],a[220:223],v[184:187],v[136:139]\n"
MF "v[144:147],a[224:227],v[184:187],v[144:147]\n"
TR "a[240:241],v7 offset:4096\n"
MF "v[144:147],a[184:187],v[180:183],v[144:147]\n"
"s_add_u32 s82,s82,s71\n"
TR "a[242:243],v7 offset:6144\n"
MF "v[144:147],a[144:147],v[176:179],v[144:147]\n"
LD "v[212:215],v9,s[40:43],s82 offen sc1\n"
MF "v[152:155],a[148:151],v[176:179],v[152:155]\n"
TR "a[244:245],v7 offset:8192\n"
MF "v[152:155],a[188:191],v[180:183],v[152:155]\n"
TR "a[246:247],v7 offset:10240\n"
MF "v[152:155],a[228:231],v[184:187],v[152:155]\n"
MF "v[160:163],a[232:235],v[184:187],v[160:163]\n"
MF "v[160:163],a[192:195],v[180:183],v[160:163]\n"
MF "v[160:163],a[152:155],v[176:179],v[160:163]\n"
"s_add_u32 s82,s82,s71\n"
MF "v[168:171],a[156:159],v[176:179],v[168:171]\n"
LD "v[216:219],v9,s[40:43],s82 offen sc1\n"
MF "v[168:171],a[196:199],v[180:183],v[168:171]\n"
MF "v[168:171],a[236:239],v[184:187],v[168:171]\n"
"s_waitcnt lgkmcnt(0)\n"
"s_barrier\n"
MF "v[172:175],a[236:239],a[244:247],v[172:175]\n"
TR "v[176:177],v0 offset:24576\n"
MF "v[172:175],a[196:199],a[240:243],v[172:175]\n"
"s_add_u32 s85,s85,1\n"
TR "v[178:179],v0 offset:26624\n"
MF "v[172:175],a[156:159],v[188:191],v[172:175]\n"
MF "v[164:167],a[152:155],v[188:191],v[164:167]\n"
TR "v[180:181],v0 offset:28672\n"
MF "v[164:167],a[192:195],a[240:243],v[164:167]\n"
"s_add_u32 s82,s82,s71\n"
TR "v[182:183],v0 offset:30720\n"
MF "v[164:167],a[232:235],a[244:247],v[164:167]\n"
LD "v[220:223],v9,s[40:43],s82 offen sc1\n"
MF "v[156:159],a[228:231],a[244:247],v[156:159]\n"
TR "v[184:185],v0 offset:32768\n"
MF "v[156:159],a[188:191],a[240:243],v[156:159]\n"
TR "v[186:187],v0 offset:34816\n"
MF "v[156:159],a[148:151],v[188:191],v[156:159]\n"
MF "v[148:151],a[144:147],v[188:191],v[148:151]\n"
MF "v[148:151],a[184:187],a[240:243],v[148:151]\n"
MF "v[148:151],a[224:227],a[244:247],v[148:151]\n"
MF "v[140:143],a[220:223],a[244:247],v[140:143]\n"
MF "v[140:143],a[180:183],a[240:243],v[140:143]\n"
MF "v[140:143],a[140:143],v[188:191],v[140:143]\n"
"s_waitcnt lgkmcnt(0)\n"
MF "v[16:19],a[0:3],v[176:179],v[252:255]\n"
TR "v[188:189],v1 offset:24576\n"
MF "v[16:19],a[40:43],v[180:183],v[16:19]\n"
"s_add_u32 s82,s82,s71\n"
TR "v[190:191],v1 offset:26624\n"
MF "v[16:19],a[80:83],v[184:187],v[16:19]\n"
LD "v[224:227],v9,s[40:43],s82 offen sc1\n"
MF "v[24:27],a[84:87],v[184:187],v[252:255]\n"
TR "a[240:241],v1 offset:28672\n"
MF "v[24:27],a[44:47],v[180:183],v[24:27]\n"
TR "a[242:243],v1 offset:30720\n"
MF "v[24:27],a[4:7],v[176:179],v[24:27]\n"
MF "v[32:35],a[8:11],v[176:179],v[252:255]\n"
TR "a[244:245],v1 offset:32768\n"
MF "v[32:35],a[48:51],v[180:183],v[32:35]\n"
TR "a[246:247],v1 offset:34816\n"
MF "v[32:35],a[88:91],v[184:187],v[32:35]\n"
MF "v[40:43],a[92:95],v[184:187],v[252:255]\n"
MF "v[40:43],a[52:55],v[180:183],v[40:43]\n"
"s_mul_i32 s95,s92,3277\n"
"s_lshr_b32 s95,s95,16\n"
"s_mul_i32 s59,s95,20\n"
MF "v[40:43],a[12:15],v[176:179],v[40:43]\n"
"s_sub_u32 s59,s92,s59\n"
"s_cmp_ge_u32 s59,10\n"
"s_cselect_b32 s96,4294557760,0\n"
MF "v[48:51],a[16:19],v[176:179],v[252:255]\n"
"s_add_u32 s82,s82,s66\n"
"s_add_u32 s96,s96,1024\n"
"s_mul_i32 s59,s59,40960\n"
MF "v[48:51],a[56:59],v[180:183],v[48:51]\n"
LD "v[228:231],v9,s[40:43],s82 offen sc1\n"
MF "v[48:51],a[96:99],v[184:187],v[48:51]\n"
"s_add_u32 s96,s96,s59\n"
"s_lshl_b32 s59,s95,7\n"
"s_waitcnt lgkmcnt(0)\n"
MF "v[52:55],a[96:99],a[244:247],v[252:255]\n"
"s_add_u32 s96,s96,s59\n"
"s_add_u32 s95,s95,3\n"
TR "v[176:177],v2 offset:24576\n"
MF "v[52:55],a[56:59],a[240:243],v[52:55]\n"
"s_cmp_ge_u32 s92,40\n"
"s_cselect_b32 s95,99,s95\n"
TR "v[178:179],v2 offset:26624\n"
MF "v[52:55],a[16:19],v[188:191],v[52:55]\n"
"s_cmp_ge_u32 s85,5\n"
"s_cselect_b32 s95,99,s95\n"
"s_cselect_b32 s72,s85,0\n"
MF "v[44:47],a[12:15],v[188:191],v[252:255]\n"
TR "v[180:181],v2 offset:28672\n"
MF "v[44:47],a[52:55],a[240:243],v[44:47]\n"
TR "v[182:183],v2 offset:30720\n"
MF "v[44:47],a[92:95],a[244:247],v[44:47]\n"
MF "v[36:39],a[88:91],a[244:247],v[252:255]\n"
TR "v[184:185],v2 offset:32768\n"
MF "v[36:39],a[48:51],a[240:243],v[36:39]\n"
"s_add_u32 s82,s82,s71\n"
TR "v[186:187],v2 offset:34816\n"
MF "v[36:39],a[8:11],v[188:191],v[36:39]\n"
LD "v[232:235],v9,s[40:43],s82 offen sc1\n"
MF "v[28:31],a[4:7],v[188:191],v[252:255]\n"
MF "v[28:31],a[44:47],a[240:243],v[28:31]\n"
MF "v[28:31],a[84:87],a[244:247],v[28:31]\n"
MF "v[20:23],a[80:83],a[244:247],v[252:255]\n"
MF "v[20:23],a[40:43],a[240:243],v[20:23]\n"
MF "v[20:23],a[0:3],v[188:191],v[20:23]\n"
"s_waitcnt lgkmcnt(0)\n"
MF "v[56:59],a[0:3],v[176:179],v[252:255]\n"
TR "v[188:189],v3 offset:24576\n"
MF "v[56:59],a[40:43],v[180:183],v[56:59]\n"
"s_add_u32 s82,s82,s71\n"
TR "v[190:191],v3 offset:26624\n"
MF "v[56:59],a[80:83],v[184:187],v[56:59]\n"
LD "v[236:239],v9,s[40:43],s82 offen sc1\n"
MF "v[64:67],a[84:87],v[184:187],v[252:255]\n"
TR "a[240:241],v3 offset:28672\n"
MF "v[64:67],a[44:47],v[180:183],v[64:67]\n"
TR "a[242:243],v3 offset:30720\n"
MF "v[64:67],a[4:7],v[176:179],v[64:67]\n"
MF "v[72:75],a[8:11],v[176:179],v[252:255]\n"
TR "a[244:245],v3 offset:32768\n"
MF "v[72:75],a[48:51],v[180:183],v[72:75]\n"
TR "a[246:247],v3 offset:34816\n"
MF "v[72:75],a[88:91],v[184:187],v[72:75]\n"
MF "v[80:83],a[92:95],v[184:187],v[252:255]\n"
MF "v[80:83],a[52:55],v[180:183],v[80:83]\n"
MF "v[80:83],a[12:15],v[176:179],v[80:83]\n"
MF "v[88:91],a[16:19],v[176:179],v[252:255]\n"
MF "v[88:91],a[56:59],v[180:183],v[88:91]\n"
MF "v[88:91],a[96:99],v[184:187],v[88:91]\n"
"s_waitcnt lgkmcnt(0)\n"
"s_barrier\n"
MF "v[92:95],a[96:99],a[244:247],v[252:255]\n"
TR "v[176:177],v0\n"
MF "v[92:95],a[56:59],a[240:243],v[92:95]\n"
TR "v[178:179],v0 offset:2048\n"
MF "v[92:95],a[16:19],v[188:191],v[92:95]\n"
"ds_read2st64_b32 v[192:193],v13 offset0:192 offset1:193\n"
MF "v[84:87],a[12:15],v[188:191],v[252:255]\n"
TR "v[180:181],v0 offset:4096\n"
MF "v[84:87],a[52:55],a[240:243],v[84:87]\n"
TR "v[182:183],v0 offset:6144\n"
MF "v[84:87],a[92:95],a[244:247],v[84:87]\n"
"ds_read2st64_b32 v[194:195],v13 offset0:194 offset1:195\n"
MF "v[76:79],a[88:91],a[244:247],v[252:255]\n"
TR "v[184:185],v0 offset:8192\n"
MF "v[76:79],a[48:51],a[240:243],v[76:79]\n"
TR "v[186:187],v0 offset:10240\n"
MF "v[76:79],a[8:11],v[188:191],v[76:79]\n"
"ds_read2st64_b32 v[196:197],v13 offset0:196 offset1:197\n"
MF "v[68:71],a[4:7],v[188:191],v[252:255]\n"
"ds_read2st64_b32 v[198:199],v13 offset0:198 offset1:199\n"
MF "v[68:71],a[44:47],a[240:243],v[68:71]\n"
"buffer_load_dword v10,v12,s[40:43],s86 offen sc1\n"
MF "v[68:71],a[84:87],a[244:247],v[68:71]\n"
MF "v[60:63],a[80:83],a[244:247],v[252:255]\n"
MF "v[60:63],a[40:43],a[240:243],v[60:63]\n"
MF "v[60:63],a[0:3],v[188:191],v[60:63]\n"
"s_waitcnt lgkmcnt(2)\n"
MF "v[16:19],a[120:123],v[176:179],v[16:19]\n"
"s_waitcnt lgkmcnt(0)\n"
TR "v[188:189],v1\n"
"v_add_f32 v96,v96,v192\n"
MF "v[16:19],a[160:163],v[180:183],v[16:19]\n"
TR "v[190:191],v1 offset:2048\n"
"v_add_f32 v97,v97,v193\n"
MF "v[16:19],a[200:203],v[184:187],v[16:19]\n"
"ds_read2st64_b32 v[200:201],v13 offset0:200 offset1:201\n"
"v_add_f32 v98,v98,v194\n"
"s_waitcnt vmcnt(9)\n"
"v_mov_b32 v14,s76\n"
"s_add_u32 s76,s76,1\n"
MF "v[24:27],a[204:207],v[184:187],v[24:27]\n"
TR "a[240:241],v1 offset:4096\n"
"v_add_f32 v99,v99,v195\n"
"buffer_store_dword v14,v15,s[40:43],s94 offen sc1\n"
MF "v[24:27],a[164:167],v[180:183],v[24:27]\n"
TR "a[242:243],v1 offset:6144\n"
"v_add_f32 v100,v100,v196\n"
MF "v[24:27],a[124:127],v[176:179],v[24:27]\n"
"ds_read2st64_b32 v[202:203],v13 offset0:202 offset1:203\n"
"v_add_f32 v101,v101,v197\n"
MF "v[32:35],a[128:131],v[176:179],v[32:35]\n"
TR "a[244:245],v1 offset:8192\n"
"v_add_f32 v102,v102,v198\n"
MF "v[32:35],a[168:171],v[180:183],v[32:35]\n"
TR "a[246:247],v1 offset:10240\n"
"v_add_f32 v103,v103,v199\n"
MF "v[32:35],a[208:211],v[184:187],v[32:35]\n"
"ds_read2st64_b32 v[204:205],v13 offset0:204 offset1:205\n"
CV "v96,v96,v97\n"
MF "v[40:43],a[212:215],v[184:187],v[40:43]\n"
"ds_read2st64_b32 v[206:207],v13 offset0:206 offset1:207\n"
CV "v97,v98,v99\n"
MF "v[40:43],a[172:175],v[180:183],v[40:43]\n"
"s_add_u32 s87,s90,0\n"
CV "v98,v100,v101\n"
CV "v99,v102,v103\n"
MF "v[40:43],a[132:135],v[176:179],v[40:43]\n"
MF "v[48:51],a[136:139],v[176:179],v[48:51]\n"
"buffer_store_dwordx4 v[96:99],v9,s[40:43],s87 offen sc1\n"
MF "v[48:51],a[176:179],v[180:183],v[48:51]\n"
MF "v[48:51],a[216:219],v[184:187],v[48:51]\n"
"s_waitcnt lgkmcnt(2)\n"
MF "v[52:55],a[216:219],a[244:247],v[52:55]\n"
"s_waitcnt lgkmcnt(0)\n"
TR "v[176:177],v2\n"
"v_add_f32 v104,v104,v200\n"
MF "v[52:55],a[176:179],a[240:243],v[52:55]\n"
TR "v[178:179],v2 offset:2048\n"
"v_add_f32 v105,v105,v201\n"
MF "v[52:55],a[136:139],v[188:191],v[52:55]\n"
"ds_read2st64_b32 v[192:193],v13 offset0:208 offset1:209\n"
"v_add_f32 v106,v106,v202\n"
MF "v[44:47],a[132:135],v[188:191],v[44:47]\n"
TR "v[180:181],v2 offset:4096\n"
"v_add_f32 v107,v107,v203\n"
MF "v[44:47],a[172:175],a[240:243],v[44:47]\n"
TR "v[182:183],v2 offset:6144\n"
"v_add_f32 v108,v108,v204\n"
MF "v[44:47],a[212:215],a[244:247],v[44:47]\n"
"ds_read2st64_b32 v[194:195],v13 offset0:210 offset1:211\n"
"v_add_f32 v109,v109,v205\n"
MF "v[36:39],a[208:211],a[244:247],v[36:39]\n"
TR "v[184:185],v2 offset:8192\n"
"v_add_f32 v110,v110,v206\n"
MF "v[36:39],a[168:171],a[240:243],v[36:39]\n"
TR "v[186:187],v2 offset:10240\n"
"v_add_f32 v111,v111,v207\n"
MF "v[36:39],a[128:131],v[188:191],v[36:39]\n"
"ds_read2st64_b32 v[196:197],v13 offset0:212 offset1:213\n"
CV "v104,v104,v105\n"
MF "v[28:31],a[124:127],v[188:191],v[28:31]\n"
"ds_read2st64_b32 v[198:199],v13 offset0:214 offset1:215\n"
CV "v105,v106,v107\n"
MF "v[28:31],a[164:167],a[240:243],v[28:31]\n"
"s_add_u32 s87,s90,1024\n"
CV "v106,v108,v109\n"
CV "v107,v110,v111\n"
MF "v[28:31],a[204:207],a[244:247],v[28:31]\n"
MF "v[20:23],a[200:203],a[244:247],v[20:23]\n"
"buffer_store_dwordx4 v[104:107],v9,s[40:43],s87 offen sc1\n"
MF "v[20:23],a[160:163],a[240:243],v[20:23]\n"
MF "v[20:23],a[120:123],v[188:191],v[20:23]\n"
"s_waitcnt lgkmcnt(2)\n"
MF "v[56:59],a[120:123],v[176:179],v[56:59]\n"
"s_waitcnt lgkmcnt(0)\n"
TR "v[188:189],v3\n"
"v_add_f32 v112,v112,v192\n"
MF "v[56:59],a[160:163],v[180:183],v[56:59]\n"
TR "v[190:191],v3 offset:2048\n"
"v_add_f32 v113,v113,v193\n"
MF "v[56:59],a[200:203],v[184:187],v[56:59]\n"
"ds_read2st64_b32 v[200:201],v13 offset0:216 offset1:217\n"
"v_add_f32 v114,v114,v194\n"
MF "v[64:67],a[204:207],v[184:187],v[64:67]\n"
TR "a[240:241],v3 offset:4096\n"
"v_add_f32 v115,v115,v195\n"
MF "v[64:67],a[164:167],v[180:183],v[64:67]\n"
TR "a[242:243],v3 offset:6144\n"
"v_add_f32 v116,v116,v196\n"
MF "v[64:67],a[124:127],v[176:179],v[64:67]\n"
"ds_read2st64_b32 v[202:203],v13 offset0:218 offset1:219\n"
"v_add_f32 v117,v117,v197\n"
MF "v[72:75],a[128:131],v[176:179],v[72:75]\n"
TR "a[244:245],v3 offset:8192\n"
"v_add_f32 v118,v118,v198\n"
MF "v[72:75],a[168:171],v[180:183],v[72:75]\n"
TR "a[246:247],v3 offset:10240\n"
"v_add_f32 v119,v119,v199\n"
MF "v[72:75],a[208:211],v[184:187],v[72:75]\n"
"ds_read2st64_b32 v[204:205],v13 offset0:220 offset1:221\n"
CV "v112,v112,v113\n"
MF "v[80:83],a[212:215],v[184:187],v[80:83]\n"
"ds_read2st64_b32 v[206:207],v13 offset0:222 offset1:223\n"
CV "v113,v114,v115\n"
MF "v[80:83],a[172:175],v[180:183],v[80:83]\n"
"s_add_u32 s87,s90,2048\n"
CV "v114,v116,v117\n"
CV "v115,v118,v119\n"
MF "v[80:83],a[132:135],v[176:179],v[80:83]\n"
MF "v[88:91],a[136:139],v[176:179],v[88:91]\n"
"buffer_store_dwordx4 v[112:115],v9,s[40:43],s87 offen sc1\n"
MF "v[88:91],a[176:179],v[180:183],v[88:91]\n"
MF "v[88:91],a[216:219],v[184:187],v[88:91]\n"
"s_waitcnt lgkmcnt(0)\n"
"s_barrier\n"
MF "v[92:95],a[216:219],a[244:247],v[92:95]\n"
TR "v[176:177],v0 offset:49152\n"
"v_add_f32 v120,v120,v200\n"
MF "v[92:95],a[176:179],a[240:243],v[92:95]\n"
TR "v[178:179],v0 offset:51200\n"
"v_add_f32 v121,v121,v201\n"
MF "v[92:95],a[136:139],v[188:191],v[92:95]\n"
"ds_read2st64_b32 v[192:193],v13 offset0:224 offset1:225\n"
"v_add_f32 v122,v122,v202\n"
MF "v[84:87],a[132:135],v[188:191],v[84:87]\n"
TR "v[180:181],v0 offset:53248\n"
"v_add_f32 v123,v123,v203\n"
MF "v[84:87],a[172:175],a[240:243],v[84:87]\n"
TR "v[182:183],v0 offset:55296\n"
"v_add_f32 v124,v124,v204\n"
MF "v[84:87],a[212:215],a[244:247],v[84:87]\n"
"ds_read2st64_b32 v[194:195],v13 offset0:226 offset1:227\n"
"v_add_f32 v125,v125,v205\n"
MF "v[76:79],a[208:211],a[244:247],v[76:79]\n"
TR "v[184:185],v0 offset:57344\n"
"v_add_f32 v126,v126,v206\n"
MF "v[76:79],a[168:171],a[240:243],v[76:79]\n"
TR "v[186:187],v0 offset:59392\n"
"v_add_f32 v127,v127,v207\n"
MF "v[76:79],a[128:131],v[188:191],v[76:79]\n"
"ds_read2st64_b32 v[196:197],v13 offset0:228 offset1:229\n"
CV "v120,v120,v121\n"
MF "v[68:71],a[124:127],v[188:191],v[68:71]\n"
"ds_read2st64_b32 v[198:199],v13 offset0:230 offset1:231\n"
CV "v121,v122,v123\n"
MF "v[68:71],a[164:167],a[240:243],v[68:71]\n"
"s_add_u32 s87,s90,3072\n"
CV "v122,v124,v125\n"
CV "v123,v126,v127\n"
MF "v[68:71],a[204:207],a[244:247],v[68:71]\n"
MF "v[60:63],a[200:203],a[244:247],v[60:63]\n"
"buffer_store_dwordx4 v[120:123],v9,s[40:43],s87 offen sc1\n"
MF "v[60:63],a[160:163],a[240:243],v[60:63]\n"
MF "v[60:63],a[120:123],v[188:191],v[60:63]\n"
"s_waitcnt lgkmcnt(2)\n"
MF "v[16:19],a[20:23],v[176:179],v[16:19]\n"
"s_waitcnt lgkmcnt(0)\n"
TR "v[188:189],v1 offset:49152\n"
"v_add_f32 v128,v128,v192\n"
MF "v[16:19],a[60:63],v[180:183],v[16:19]\n"
TR "v[190:191],v1 offset:51200\n"
"v_add_f32 v129,v129,v193\n"
MF "v[16:19],a[100:103],v[184:187],v[16:19]\n"
"ds_read2st64_b32 v[200:201],v13 offset0:0 offset1:1\n"
"v_add_f32 v130,v130,v194\n"
MF "v[24:27],a[104:107],v[184:187],v[24:27]\n"
TR "a[240:241],v1 offset:53248\n"
"v_add_f32 v131,v131,v195\n"
MF "v[24:27],a[64:67],v[180:183],v[24:27]\n"
TR "a[242:243],v1 offset:55296\n"
"v_add_f32 v132,v132,v196\n"
MF "v[24:27],a[24:27],v[176:179],v[24:27]\n"
"ds_read2st64_b32 v[202:203],v13 offset0:2 offset1:3\n"
"v_add_f32 v133,v133,v197\n"
MF "v[32:35],a[28:31],v[176:179],v[32:35]\n"
TR "a[244:245],v1 offset:57344\n"
"v_add_f32 v134,v134,v198\n"
MF "v[32:35],a[68:71],v[180:183],v[32:35]\n"
TR "a[246:247],v1 offset:59392\n"
"v_add_f32 v135,v135,v199\n"
MF "v[32:35],a[108:111],v[184:187],v[32:35]\n"
"ds_read2st64_b32 v[204:205],v13 offset0:4 offset1:5\n"
CV "v128,v128,v129\n"
MF "v[40:43],a[112:115],v[184:187],v[40:43]\n"
"ds_read2st64_b32 v[206:207],v13 offset0:6 offset1:7\n"
CV "v129,v130,v131\n"
MF "v[40:43],a[72:75],v[180:183],v[40:43]\n"
"s_add_u32 s87,s90,4096\n"
CV "v130,v132,v133\n"
CV "v131,v134,v135\n"
MF "v[40:43],a[32:35],v[176:179],v[40:43]\n"
MF "v[48:51],a[36:39],v[176:179],v[48:51]\n"
"buffer_store_dwordx4 v[128:131],v9,s[40:43],s87 offen sc1\n"
MF "v[48:51],a[76:79],v[180:183],v[48:51]\n"
MF "v[48:51],a[116:119],v[184:187],v[48:51]\n"
"s_waitcnt lgkmcnt(2)\n"
MF "v[52:55],a[116:119],a[244:247],v[52:55]\n"
"s_waitcnt lgkmcnt(0)\n"
TR "v[176:177],v2 offset:49152\n"
"v_add_f32 v136,v136,v200\n"
MF "v[52:55],a[76:79],a[240:243],v[52:55]\n"
TR "v[178:179],v2 offset:51200\n"
"v_add_f32 v137,v137,v201\n"
MF "v[52:55],a[36:39],v[188:191],v[52:55]\n"
"ds_read2st64_b32 v[192:193],v13 offset0:8 offset1:9\n"
"v_add_f32 v138,v138,v202\n"
MF "v[44:47],a[32:35],v[188:191],v[44:47]\n"
TR "v[180:181],v2 offset:53248\n"
"v_add_f32 v139,v139,v203\n"
MF "v[44:47],a[72:75],a[240:243],v[44:47]\n"
TR "v[182:183],v2 offset:55296\n"
"v_add_f32 v140,v140,v204\n"
MF "v[44:47],a[112:115],a[244:247],v[44:47]\n"
"ds_read2st64_b32 v[194:195],v13 offset0:10 offset1:11\n"
"v_add_f32 v141,v141,v205\n"
MF "v[36:39],a[108:111],a[244:247],v[36:39]\n"
TR "v[184:185],v2 offset:57344\n"
"v_add_f32 v142,v142,v206\n"
MF "v[36:39],a[68:71],a[240:243],v[36:39]\n"
TR "v[186:187],v2 offset:59392\n"
"v_add_f32 v143,v143,v207\n"
MF "v[36:39],a[28:31],v[188:191],v[36:39]\n"
"ds_read2st64_b32 v[196:197],v13 offset0:12 offset1:13\n"
CV "v136,v136,v137\n"
MF "v[28:31],a[24:27],v[188:191],v[28:31]\n"
"ds_read2st64_b32 v[198:199],v13 offset0:14 offset1:15\n"
CV "v137,v138,v139\n"
MF "v[28:31],a[64:67],a[240:243],v[28:31]\n"
"s_add_u32 s87,s90,10240\n"
CV "v138,v140,v141\n"
CV "v139,v142,v143\n"
MF "v[28:31],a[104:107],a[244:247],v[28:31]\n"
MF "v[20:23],a[100:103],a[244:247],v[20:23]\n"
"buffer_store_dwordx4 v[136:139],v9,s[40:43],s87 offen sc1\n"
MF "v[20:23],a[60:63],a[240:243],v[20:23]\n"
"s_waitcnt vmcnt(7)\n"
"s_cmp_eq_u32 s83,0xf0000000\n"
MF "v[20:23],a[20:23],v[188:191],v[20:23]\n"
"s_cbranch_scc1 .Ljs15\n"
"s_cmp_eq_u32 s83,0xf0000010\n"
"s_cbranch_scc1 .Ljc15\n"
MF "v[248:251],v[210:213],a[250:253],0\n"
"v_mov_b32 v240,v208\n"
"v_mov_b32 v241,v209\n"
MF "v[244:247],v[214:217],a[250:253],0\n"
MF "v[248:251],v[218:221],a[250:253],v[248:251]\n"
MF "v[244:247],v[222:225],a[250:253],v[244:247]\n"
MF "v[248:251],v[226:229],a[250:253],v[248:251]\n"
MF "v[244:247],v[230:233],a[250:253],v[244:247]\n"
MF "v[248:251],v[234:237],a[250:253],v[248:251]\n"
MF "v[244:247],v[238:241],a[250:253],v[244:247]\n"
"s_branch .Ljs15\n"
".Ljc15:\n"
MF "v[56:59],a[252:255],v[208:211],v[56:59]\n"
MF "v[64:67],a[252:255],v[212:215],v[64:67]\n"
MF "v[72:75],a[252:255],v[216:219],v[72:75]\n"
MF "v[80:83],a[252:255],v[220:223],v[80:83]\n"
MF "v[88:91],a[252:255],v[224:227],v[88:91]\n"
MF "v[60:63],a[248:251],v[208:211],v[60:63]\n"
MF "v[68:71],a[248:251],v[212:215],v[68:71]\n"
MF "v[76:79],a[248:251],v[216:219],v[76:79]\n"
MF "v[84:87],a[248:251],v[220:223],v[84:87]\n"
MF "v[92:95],a[248:251],v[224:227],v[92:95]\n"
".Ljs15:\n"
"s_waitcnt lgkmcnt(2)\n"
MF "v[56:59],a[20:23],v[176:179],v[56:59]\n"
"s_waitcnt lgkmcnt(0)\n"
TR "v[188:189],v3 offset:49152\n"
"v_add_f32 v144,v144,v192\n"
MF "v[56:59],a[60:63],v[180:183],v[56:59]\n"
TR "v[190:191],v3 offset:51200\n"
"v_add_f32 v145,v145,v193\n"
MF "v[56:59],a[100:103],v[184:187],v[56:59]\n"
"ds_read2st64_b32 v[200:201],v13 offset0:16 offset1:17\n"
"v_add_f32 v146,v146,v194\n"
MF "v[64:67],a[104:107],v[184:187],v[64:67]\n"
TR "a[240:241],v3 offset:53248\n"
"v_add_f32 v147,v147,v195\n"
MF "v[64:67],a[64:67],v[180:183],v[64:67]\n"
TR "a[242:243],v3 offset:55296\n"
"v_add_f32 v148,v148,v196\n"
MF "v[64:67],a[24:27],v[176:179],v[64:67]\n"
"ds_read2st64_b32 v[202:203],v13 offset0:18 offset1:19\n"
"v_add_f32 v149,v149,v197\n"
MF "v[72:75],a[28:31],v[176:179],v[72:75]\n"
TR "a[244:245],v3 offset:57344\n"
"v_add_f32 v150,v150,v198\n"
MF "v[72:75],a[68:71],v[180:183],v[72:75]\n"
TR "a[246:247],v3 offset:59392\n"
"v_add_f32 v151,v151,v199\n"
MF "v[72:75],a[108:111],v[184:187],v[72:75]\n"
"ds_read2st64_b32 v[204:205],v13 offset0:20 offset1:21\n"
CV "v144,v144,v145\n"
MF "v[80:83],a[112:115],v[184:187],v[80:83]\n"
"ds_read2st64_b32 v[206:207],v13 offset0:22 offset1:23\n"
CV "v145,v146,v147\n"
MF "v[80:83],a[72:75],v[180:183],v[80:83]\n"
"s_add_u32 s87,s90,11264\n"
CV "v146,v148,v149\n"
CV "v147,v150,v151\n"
MF "v[80:83],a[32:35],v[176:179],v[80:83]\n"
"s_lshl_b32 s82,s92,10\n"
"v_sub_u32 v10,v10,v11\n"
"v_cmp_gt_u32 vcc,s95,v10\n"
MF "v[88:91],a[36:39],v[176:179],v[88:91]\n"
"buffer_store_dwordx4 v[144:147],v9,s[40:43],s87 offen sc1\n"
MF "v[88:91],a[76:79],v[180:183],v[88:91]\n"
"s_add_u32 s82,s82,s93\n"
CV "v244,v244,v245\n"
CV "v245,v246,v247\n"
MF "v[88:91],a[116:119],v[184:187],v[88:91]\n"
"s_waitcnt lgkmcnt(0)\n"
"s_barrier\n"
MF "v[92:95],a[116:119],a[244:247],v[92:95]\n"
TR "v[176:177],v4\n"
"v_add_f32 v152,v152,v200\n"
MF "v[92:95],a[76:79],a[240:243],v[92:95]\n"
TR "v[178:179],v4 offset:2048\n"
"v_add_f32 v153,v153,v201\n"
MF "v[92:95],a[36:39],v[188:191],v[92:95]\n"
"ds_read2st64_b32 v[192:193],v13 offset0:24 offset1:25\n"
"v_add_f32 v154,v154,v202\n"
MF "v[84:87],a[32:35],v[188:191],v[84:87]\n"
TR "v[180:181],v4 offset:4096\n"
"v_add_f32 v155,v155,v203\n"
MF "v[84:87],a[72:75],a[240:243],v[84:87]\n"
TR "v[182:183],v4 offset:6144\n"
"v_add_f32 v156,v156,v204\n"
MF "v[84:87],a[112:115],a[244:247],v[84:87]\n"
"ds_read2st64_b32 v[194:195],v13 offset0:26 offset1:27\n"
"v_add_f32 v157,v157,v205\n"
MF "v[76:79],a[108:111],a[244:247],v[76:79]\n"
TR "v[184:185],v4 offset:8192\n"
"v_add_f32 v158,v158,v206\n"
MF "v[76:79],a[68:71],a[240:243],v[76:79]\n"
TR "v[186:187],v4 offset:10240\n"
"v_add_f32 v159,v159,v207\n"
MF "v[76:79],a[28:31],v[188:191],v[76:79]\n"
"ds_read2st64_b32 v[196:197],v13 offset0:28 offset1:29\n"
CV "v152,v152,v153\n"
MF "v[68:71],a[24:27],v[188:191],v[68:71]\n"
"ds_read2st64_b32 v[198:199],v13 offset0:30 offset1:31\n"
CV "v153,v154,v155\n"
MF "v[68:71],a[64:67],a[240:243],v[68:71]\n"
"s_add_u32 s87,s90,12288\n"
CV "v154,v156,v157\n"
CV "v155,v158,v159\n"
MF "v[68:71],a[104:107],a[244:247],v[68:71]\n"
"s_cmp_eq_u64 vcc,0\n"
"s_cselect_b32 s82,s82,0xf0000000\n"
"s_cselect_b32 s84,s96,0xf0000000\n"
"s_cselect_b32 s59,32,0\n"
MF "v[60:63],a[100:103],a[244:247],v[60:63]\n"
"buffer_store_dwordx4 v[152:155],v9,s[40:43],s87 offen sc1\n"
MF "v[60:63],a[60:63],a[240:243],v[60:63]\n"
"s_add_u32 s92,s92,s59\n"
".Lnf6:\n"
"v_readlane_b32 s58,v10,s67\n"
"s_cmp_lt_u32 s58,s72\n"
"s_cbranch_scc1 .Lsp6\n"
".Lnb6:\n"
MF "v[60:63],a[20:23],v[188:191],v[60:63]\n"
"s_waitcnt lgkmcnt(2)\n"
CV "v246,v248,v249\n"
CV "v247,v250,v251\n"
MF "v[16:19],a[140:143],v[176:179],v[16:19]\n"
"s_waitcnt lgkmcnt(0)\n"
TR "v[188:189],v5\n"
"v_add_f32 v160,v160,v192\n"
MF "v[16:19],a[180:183],v[180:183],v[16:19]\n"
TR "v[190:191],v5 offset:2048\n"
"v_add_f32 v161,v161,v193\n"
MF "v[16:19],a[220:223],v[184:187],v[16:19]\n"
".Lpg28h:\n"
"s_cbranch_execz .Lisr4\n"
"ds_read2st64_b32 v[200:201],v13 offset0:32 offset1:33\n"
"v_add_f32 v162,v162,v194\n"
MF "v[24:27],a[224:227],v[184:187],v[24:27]\n"
TR "a[240:241],v5 offset:4096\n"
"v_add_f32 v163,v163,v195\n"
MF "v[24:27],a[184:187],v[180:183],v[24:27]\n"
TR "a[242:243],v5 offset:6144\n"
"v_add_f32 v164,v164,v196\n"
MF "v[24:27],a[144:147],v[176:179],v[24:27]\n"
"ds_read2st64_b32 v[202:203],v13 offset0:34 offset1:35\n"
"v_add_f32 v165,v165,v197\n"
MF "v[32:35],a[148:151],v[176:179],v[32:35]\n"
TR "a[244:245],v5 offset:8192\n"
"v_add_f32 v166,v166,v198\n"
MF "v[32:35],a[188:191],v[180:183],v[32:35]\n"
TR "a[246:247],v5 offset:10240\n"
"v_add_f32 v167,v167,v199\n"
MF "v[32:35],a[228:231],v[184:187],v[32:35]\n"
"ds_read2st64_b32 v[204:205],v13 offset0:36 offset1:37\n"
CV "v160,v160,v161\n"
MF "v[40:43],a[232:235],v[184:187],v[40:43]\n"
"ds_read2st64_b32 v[206:207],v13 offset0:38 offset1:39\n"
CV "v161,v162,v163\n"
MF "v[40:43],a[192:195],v[180:183],v[40:43]\n"
"s_add_u32 s87,s90,13312\n"
CV "v162,v164,v165\n"
CV "v163,v166,v167\n"
MF "v[40:43],a[152:155],v[176:179],v[40:43]\n"
"s_cmp_ge_u32 s85,5\n"
"s_cselect_b32 s82,s70,s82\n"
"s_cselect_b32 s84,0xf0000010,s84\n"
MF "v[48:51],a[156:159],v[176:179],v[48:51]\n"
"buffer_store_dwordx4 v[160:163],v9,s[40:43],s87 offen sc1\n"
MF "v[48:51],a[196:199],v[180:183],v[48:51]\n"
"s_cmp_ge_u32 s85,5\n"
"s_cselect_b32 s71,1024,s71\n"
"s_cselect_b32 s66,0x80000000,s66\n"
MF "v[48:51],a[236:239],v[184:187],v[48:51]\n"
"s_add_u32 s70,s70,20480\n"
"s_waitcnt lgkmcnt(2)\n"
"v_permlane16_swap_b32 v244,v246\n"
MF "v[52:55],a[236:239],a[244:247],v[52:55]\n"
"s_waitcnt lgkmcnt(0)\n"
TR "v[176:177],v6\n"
"v_add_f32 v168,v168,v200\n"
MF "v[52:55],a[196:199],a[240:243],v[52:55]\n"
TR "v[178:179],v6 offset:2048\n"
"v_add_f32 v169,v169,v201\n"
MF "v[52:55],a[156:159],v[188:191],v[52:55]\n"
"v_add_f32 v170,v170,v202\n"
"v_add_f32 v171,v171,v203\n"
MF "v[44:47],a[152:155],v[188:191],v[44:47]\n"
TR "v[180:181],v6 offset:4096\n"
"v_add_f32 v172,v172,v204\n"
MF "v[44:47],a[192:195],a[240:243],v[44:47]\n"
TR "v[182:183],v6 offset:6144\n"
"v_add_f32 v173,v173,v205\n"
MF "v[44:47],a[232:235],a[244:247],v[44:47]\n"
"v_add_f32 v174,v174,v206\n"
"v_add_f32 v175,v175,v207\n"
MF "v[36:39],a[228:231],a[244:247],v[36:39]\n"
TR "v[184:185],v6 offset:8192\n"
CV "v168,v168,v169\n"
MF "v[36:39],a[188:191],a[240:243],v[36:39]\n"
TR "v[186:187],v6 offset:10240\n"
CV "v169,v170,v171\n"
MF "v[36:39],a[148:151],v[188:191],v[36:39]\n"
"s_add_u32 s87,s90,14336\n"
CV "v170,v172,v173\n"
CV "v171,v174,v175\n"
MF "v[28:31],a[144:147],v[188:191],v[28:31]\n"
"v_permlane16_swap_b32 v245,v247\n"
MF "v[28:31],a[184:187],a[240:243],v[28:31]\n"
"buffer_store_dwordx4 v[244:247],v8,s[44:47],s83 offen sc1\n"
MF "v[28:31],a[224:227],a[244:247],v[28:31]\n"
"s_mov_b32 s83,s84\n"
MF "v[20:23],a[220:223],a[244:247],v[20:23]\n"
"buffer_store_dwordx4 v[168:171],v9,s[40:43],s87 offen sc1\n"
MF "v[20:23],a[180:183],a[240:243],v[20:23]\n"
"s_mov_b32 s90,s91\n"
"s_add_u32 s91,s91,20480\n"
MF "v[20:23],a[140:143],v[188:191],v[20:23]\n"
"s_waitcnt lgkmcnt(0)\n"
LD "v[208:211],v9,s[40:43],s82 offen sc1\n"
MF "v[56:59],a[140:143],v[176:179],v[56:59]\n"
TR "v[188:189],v7\n"
MF "v[56:59],a[180:183],v[180:183],v[56:59]\n"
TR "v[190:191],v7 offset:2048\n"
MF "v[56:59],a[220:223],v[184:187],v[56:59]\n"
MF "v[64:67],a[224:227],v[184:187],v[64:67]\n"
TR "a[240:241],v7 offset:4096\n"
MF "v[64:67],a[184:187],v[180:183],v[64:67]\n"
"s_add_u32 s82,s82,s71\n"
TR "a[242:243],v7 offset:6144\n"
MF "v[64:67],a[144:147],v[176:179],v[64:67]\n"
LD "v[212:215],v9,s[40:43],s82 offen sc1\n"
MF "v[72:75],a[148:151],v[176:179],v[72:75]\n"
TR "a[244:245],v7 offset:8192\n"
MF "v[72:75],a[188:191],v[180:183],v[72:75]\n"
TR "a[246:247],v7 offset:10240\n"
MF "v[72:75],a[228:231],v[184:187],v[72:75]\n"
MF "v[80:83],a[232:235],v[184:187],v[80:83]\n"
MF "v[80:83],a[192:195],v[180:183],v[80:83]\n"
MF "v[80:83],a[152:155],v[176:179],v[80:83]\n"
"s_add_u32 s82,s82,s71\n"
MF "v[88:91],a[156:159],v[176:179],v[88:91]\n"
LD "v[216:219],v9,s[40:43],s82 offen sc1\n"
MF "v[88:91],a[196:199],v[180:183],v[88:91]\n"
MF "v[88:91],a[236:239],v[184:187],v[88:91]\n"
"s_waitcnt lgkmcnt(0)\n"
"s_barrier\n"
MF "v[92:95],a[236:239],a[244:247],v[92:95]\n"
TR "v[176:177],v0 offset:24576\n"
MF "v[92:95],a[196:199],a[240:243],v[92:95]\n"
"s_add_u32 s85,s85,1\n"
TR "v[178:179],v0 offset:26624\n"
MF "v[92:95],a[156:159],v[188:191],v[92:95]\n"
MF "v[84:87],a[152:155],v[188:191],v[84:87]\n"
TR "v[180:181],v0 offset:28672\n"
MF "v[84:87],a[192:195],a[240:243],v[84:87]\n"
"s_add_u32 s82,s82,s71\n"
TR "v[182:183],v0 offset:30720\n"
MF "v[84:87],a[232:235],a[244:247],v[84:87]\n"
LD "v[220:223],v9,s[40:43],s82 offen sc1\n"
MF "v[76:79],a[228:231],a[244:247],v[76:79]\n"
TR "v[184:185],v0 offset:32768\n"
MF "v[76:79],a[188:191],a[240:243],v[76:79]\n"
TR "v[186:187],v0 offset:34816\n"
MF "v[76:79],a[148:151],v[188:191],v[76:79]\n"
MF "v[68:71],a[144:147],v[188:191],v[68:71]\n"
MF "v[68:71],a[184:187],a[240:243],v[68:71]\n"
MF "v[68:71],a[224:227],a[244:247],v[68:71]\n"
MF "v[60:63],a[220:223],a[244:247],v[60:63]\n"
MF "v[60:63],a[180:183],a[240:243],v[60:63]\n"
MF "v[60:63],a[140:143],v[188:191],v[60:63]\n"
"s_waitcnt lgkmcnt(0)\n"
"s_cmp_lg_u32 s85,9\n"
"s_cbranch_scc1 .Lpl1\n"
".Lplast1:\n"
MF "v[96:99],v[176:179],a[0:3],v[252:255]\n"
"s_add_u32 m0,s79,73728\n"
TR "v[188:189],v1 offset:24576\n"
TR "v[190:191],v1 offset:26624\n"
MF "v[96:99],v[180:183],a[40:43],v[96:99]\n"
"s_nop 0\n"
TR "a[240:241],v1 offset:28672\n"
TR "a[242:243],v1 offset:30720\n"
MF "v[96:99],v[184:187],a[80:83],v[96:99]\n"
"s_add_u32 s82,s82,s71\n"
TR "a[244:245],v1 offset:32768\n"
TR "a[246:247],v1 offset:34816\n"
MF "v[104:107],v[184:187],a[84:87],v[252:255]\n"
LD "v[224:227],v9,s[40:43],s82 offen sc1\n"
MF "v[104:107],v[180:183],a[44:47],v[104:107]\n"
"s_nop 0\n"
"ds_write_addtid_b32 v56\n"
"ds_write_addtid_b32 v57 offset:256\n"
MF "v[104:107],v[176:179],a[4:7],v[104:107]\n"
"s_nop 0\n"
"ds_write_addtid_b32 v58 offset:512\n"
"ds_write_addtid_b32 v59 offset:768\n"
MF "v[112:115],v[176:179],a[8:11],v[252:255]\n"
"s_nop 0\n"
"ds_write_addtid_b32 v60 offset:1024\n"
"ds_write_addtid_b32 v61 offset:1280\n"
MF "v[112:115],v[180:183],a[48:51],v[112:115]\n"
"s_nop 0\n"
"ds_write_addtid_b32 v62 offset:1536\n"
"ds_write_addtid_b32 v63 offset:1792\n"
MF "v[112:115],v[184:187],a[88:91],v[112:115]\n"
"s_nop 0\n"
"ds_write_addtid_b32 v64 offset:2048\n"
"ds_write_addtid_b32 v65 offset:2304\n"
MF "v[120:123],v[184:187],a[92:95],v[252:255]\n"
"s_nop 0\n"
"ds_write_addtid_b32 v66 offset:2560\n"
"ds_write_addtid_b32 v67 offset:2816\n"
MF "v[120:123],v[180:183],a[52:55],v[120:123]\n"
"s_nop 0\n"
"ds_write_addtid_b32 v68 offset:3072\n"
"ds_write_addtid_b32 v69 offset:3328\n"
MF "v[120:123],v[176:179],a[12:15],v[120:123]\n"
"s_nop 0\n"
"ds_write_addtid_b32 v70 offset:3584\n"
"ds_write_addtid_b32 v71 offset:3840\n"
MF "v[128:131],v[176:179],a[16:19],v[252:255]\n"
"s_nop 0\n"
"ds_write_addtid_b32 v72 offset:4096\n"
"ds_write_addtid_b32 v73 offset:4352\n"
MF "v[128:131],v[180:183],a[56:59],v[128:131]\n"
"s_nop 0\n"
"ds_write_addtid_b32 v74 offset:4608\n"
"ds_write_addtid_b32 v75 offset:4864\n"
MF "v[128:131],v[184:187],a[96:99],v[128:131]\n"
"s_waitcnt lgkmcnt(15)\n"
"ds_write_addtid_b32 v76 offset:5120\n"
MF "v[132:135],a[244:247],a[96:99],v[252:255]\n"
"s_nop 0\n"
TR "v[176:177],v2 offset:24576\n"
TR "v[178:179],v2 offset:26624\n"
MF "v[132:135],a[240:243],a[56:59],v[132:135]\n"
"s_nop 0\n"
TR "v[180:181],v2 offset:28672\n"
TR "v[182:183],v2 offset:30720\n"
MF "v[132:135],v[188:191],a[16:19],v[132:135]\n"
"s_nop 0\n"
TR "v[184:185],v2 offset:32768\n"
TR "v[186:187],v2 offset:34816\n"
MF "v[124:127],v[188:191],a[12:15],v[252:255]\n"
"s_nop 0\n"
"ds_write_addtid_b32 v77 offset:5376\n"
"ds_write_addtid_b32 v78 offset:5632\n"
MF "v[124:127],a[240:243],a[52:55],v[124:127]\n"
"s_nop 0\n"
"ds_write_addtid_b32 v79 offset:5888\n"
"ds_write_addtid_b32 v80 offset:6144\n"
MF "v[124:127],a[244:247],a[92:95],v[124:127]\n"
"s_nop 0\n"
"ds_write_addtid_b32 v81 offset:6400\n"
"ds_write_addtid_b32 v82 offset:6656\n"
MF "v[116:119],a[244:247],a[88:91],v[252:255]\n"
"s_nop 0\n"
"ds_write_addtid_b32 v83 offset:6912\n"
"ds_write_addtid_b32 v84 offset:7168\n"
MF "v[116:119],a[240:243],a[48:51],v[116:119]\n"
"s_nop 0\n"
"ds_write_addtid_b32 v85 offset:7424\n"
"ds_write_addtid_b32 v86 offset:7680\n"
MF "v[116:119],v[188:191],a[8:11],v[116:119]\n"
"s_nop 0\n"
"ds_write_addtid_b32 v87 offset:7936\n"
"ds_write_addtid_b32 v88 offset:8192\n"
MF "v[108:111],v[188:191],a[4:7],v[252:255]\n"
"s_nop 0\n"
"ds_write_addtid_b32 v89 offset:8448\n"
"ds_write_addtid_b32 v90 offset:8704\n"
MF "v[108:111],a[240:243],a[44:47],v[108:111]\n"
"s_nop 0\n"
"ds_write_addtid_b32 v91 offset:8960\n"
"ds_write_addtid_b32 v92 offset:9216\n"
MF "v[108:111],a[244:247],a[84:87],v[108:111]\n"
"s_nop 0\n"
"ds_write_addtid_b32 v93 offset:9472\n"
"ds_write_addtid_b32 v94 offset:9728\n"
MF "v[100:103],a[244:247],a[80:83],v[252:255]\n"
"ds_write_addtid_b32 v95 offset:9984\n"
MF "v[100:103],a[240:243],a[40:43],v[100:103]\n"
MF "v[100:103],v[188:191],a[0:3],v[100:103]\n"
"s_waitcnt lgkmcnt(15)\n"
MF "v[136:139],v[176:179],a[0:3],v[252:255]\n"
"s_nop 0\n"
TR "v[188:189],v3 offset:24576\n"
TR "v[190:191],v3 offset:26624\n"
MF "v[136:139],v[180:183],a[40:43],v[136:139]\n"
"s_nop 0\n"
TR "a[240:241],v3 offset:28672\n"
TR "a[242:243],v3 offset:30720\n"
MF "v[136:139],v[184:187],a[80:83],v[136:139]\n"
"s_nop 0\n"
TR "a[244:245],v3 offset:32768\n"
TR "a[246:247],v3 offset:34816\n"
MF "v[144:147],v[184:187],a[84:87],v[252:255]\n"
"s_waitcnt lgkmcnt(0)\n"
"s_barrier\n"
MF "v[144:147],v[180:183],a[44:47],v[144:147]\n"
"ds_read2st64_b32 v[56:57],v13 offset0:192 offset1:193\n"
MF "v[144:147],v[176:179],a[4:7],v[144:147]\n"
"ds_read2st64_b32 v[58:59],v13 offset0:194 offset1:195\n"
MF "v[152:155],v[176:179],a[8:11],v[252:255]\n"
"ds_read2st64_b32 v[60:61],v13 offset0:196 offset1:197\n"
MF "v[152:155],v[180:183],a[48:51],v[152:155]\n"
"ds_read2st64_b32 v[62:63],v13 offset0:198 offset1:199\n"
MF "v[152:155],v[184:187],a[88:91],v[152:155]\n"
"ds_read2st64_b32 v[64:65],v13 offset0:200 offset1:201\n"
MF "v[160:163],v[184:187],a[92:95],v[252:255]\n"
"ds_read2st64_b32 v[66:67],v13 offset0:202 offset1:203\n"
MF "v[160:163],v[180:183],a[52:55],v[160:163]\n"
"ds_read2st64_b32 v[68:69],v13 offset0:204 offset1:205\n"
MF "v[160:163],v[176:179],a[12:15],v[160:163]\n"
"ds_read2st64_b32 v[70:71],v13 offset0:206 offset1:207\n"
MF "v[168:171],v[176:179],a[16:19],v[252:255]\n"
"s_waitcnt lgkmcnt(4)\n"
"ds_read2st64_b32 v[72:73],v13 offset0:208 offset1:209\n"
"v_add_f32 v16,v16,v56\n"
MF "v[168:171],v[180:183],a[56:59],v[168:171]\n"
"ds_read2st64_b32 v[74:75],v13 offset0:210 offset1:211\n"
"v_add_f32 v17,v17,v57\n"
MF "v[168:171],v[184:187],a[96:99],v[168:171]\n"
"s_waitcnt lgkmcnt(0)\n"
"s_barrier\n"
MF "v[172:175],a[244:247],a[96:99],v[252:255]\n"
"s_nop 0\n"
TR "v[176:177],v0\n"
TR "v[178:179],v0 offset:2048\n"
MF "v[172:175],a[240:243],a[56:59],v[172:175]\n"
"s_nop 0\n"
TR "v[180:181],v0 offset:4096\n"
TR "v[182:183],v0 offset:6144\n"
MF "v[172:175],v[188:191],a[16:19],v[172:175]\n"
"s_nop 0\n"
TR "v[184:185],v0 offset:8192\n"
TR "v[186:187],v0 offset:10240\n"
MF "v[164:167],v[188:191],a[12:15],v[252:255]\n"
"ds_read2st64_b32 v[76:77],v13 offset0:212 offset1:213\n"
"v_add_f32 v18,v18,v58\n"
MF "v[164:167],a[240:243],a[52:55],v[164:167]\n"
"ds_read2st64_b32 v[78:79],v13 offset0:214 offset1:215\n"
"v_add_f32 v19,v19,v59\n"
MF "v[164:167],a[244:247],a[92:95],v[164:167]\n"
"ds_read2st64_b32 v[80:81],v13 offset0:216 offset1:217\n"
"v_add_f32 v20,v20,v60\n"
MF "v[156:159],a[244:247],a[88:91],v[252:255]\n"
"ds_read2st64_b32 v[82:83],v13 offset0:218 offset1:219\n"
"v_add_f32 v21,v21,v61\n"
MF "v[156:159],a[240:243],a[48:51],v[156:159]\n"
"ds_read2st64_b32 v[84:85],v13 offset0:220 offset1:221\n"
"v_add_f32 v22,v22,v62\n"
MF "v[156:159],v[188:191],a[8:11],v[156:159]\n"
"ds_read2st64_b32 v[86:87],v13 offset0:222 offset1:223\n"
"v_add_f32 v23,v23,v63\n"
MF "v[148:151],v[188:191],a[4:7],v[252:255]\n"
"s_waitcnt vmcnt(5)\n"
"v_mov_b32 v14,s76\n"
"s_add_u32 s76,s76,1\n"
MF "v[148:151],a[240:243],a[44:47],v[148:151]\n"
"buffer_store_dword v14,v15,s[40:43],s94 offen sc1\n"
MF "v[148:151],a[244:247],a[84:87],v[148:151]\n"
"ds_read2st64_b32 v[88:89],v13 offset0:224 offset1:225\n"
CV "v16,v16,v17\n"
MF "v[140:143],a[244:247],a[80:83],v[252:255]\n"
"ds_read2st64_b32 v[90:91],v13 offset0:226 offset1:227\n"
CV "v17,v18,v19\n"
MF "v[140:143],a[240:243],a[40:43],v[140:143]\n"
"ds_read2st64_b32 v[92:93],v13 offset0:228 offset1:229\n"
CV "v18,v20,v21\n"
MF "v[140:143],v[188:191],a[0:3],v[140:143]\n"
"s_waitcnt lgkmcnt(9)\n"
"ds_read2st64_b32 v[94:95],v13 offset0:230 offset1:231\n"
CV "v19,v22,v23\n"
MF "v[96:99],v[176:179],a[120:123],v[96:99]\n"
"s_nop 0\n"
TR "v[188:189],v1\n"
TR "v[190:191],v1 offset:2048\n"
MF "v[96:99],v[180:183],a[160:163],v[96:99]\n"
"s_nop 0\n"
TR "a[240:241],v1 offset:4096\n"
TR "a[242:243],v1 offset:6144\n"
MF "v[96:99],v[184:187],a[200:203],v[96:99]\n"
"s_add_u32 s87,s90,0\n"
TR "a[244:245],v1 offset:8192\n"
TR "a[246:247],v1 offset:10240\n"
MF "v[104:107],v[184:187],a[204:207],v[104:107]\n"
"buffer_store_dwordx4 v[16:19],v9,s[40:43],s87 offen sc1\n"
MF "v[104:107],v[180:183],a[164:167],v[104:107]\n"
"v_add_f32 v24,v24,v64\n"
"v_add_f32 v25,v25,v65\n"
MF "v[104:107],v[176:179],a[124:127],v[104:107]\n"
"v_add_f32 v26,v26,v66\n"
"v_add_f32 v27,v27,v67\n"
MF "v[112:115],v[176:179],a[128:131],v[112:115]\n"
"v_add_f32 v28,v28,v68\n"
"v_add_f32 v29,v29,v69\n"
MF "v[112:115],v[180:183],a[168:171],v[112:115]\n"
"v_add_f32 v30,v30,v70\n"
"v_add_f32 v31,v31,v71\n"
MF "v[112:115],v[184:187],a[208:211],v[112:115]\n"
CV "v24,v24,v25\n"
CV "v25,v26,v27\n"
MF "v[120:123],v[184:187],a[212:215],v[120:123]\n"
"s_add_u32 s87,s90,1024\n"
CV "v26,v28,v29\n"
CV "v27,v30,v31\n"
MF "v[120:123],v[180:183],a[172:175],v[120:123]\n"
"s_waitcnt lgkmcnt(14)\n"
"buffer_store_dwordx4 v[24:27],v9,s[40:43],s87 offen sc1\n"
MF "v[120:123],v[176:179],a[132:135],v[120:123]\n"
"v_add_f32 v32,v32,v72\n"
"v_add_f32 v33,v33,v73\n"
MF "v[128:131],v[176:179],a[136:139],v[128:131]\n"
"v_add_f32 v34,v34,v74\n"
"v_add_f32 v35,v35,v75\n"
MF "v[128:131],v[180:183],a[176:179],v[128:131]\n"
"v_add_f32 v36,v36,v76\n"
"v_add_f32 v37,v37,v77\n"
MF "v[128:131],v[184:187],a[216:219],v[128:131]\n"
"s_waitcnt lgkmcnt(0)\n"
"v_add_f32 v38,v38,v78\n"
"v_add_f32 v39,v39,v79\n"
MF "v[132:135],a[244:247],a[216:219],v[132:135]\n"
"s_nop 0\n"
TR "v[176:177],v2\n"
TR "v[178:179],v2 offset:2048\n"
MF "v[132:135],a[240:243],a[176:179],v[132:135]\n"
"s_nop 0\n"
TR "v[180:181],v2 offset:4096\n"
TR "v[182:183],v2 offset:6144\n"
MF "v[132:135],v[188:191],a[136:139],v[132:135]\n"
"s_nop 0\n"
TR "v[184:185],v2 offset:8192\n"
TR "v[186:187],v2 offset:10240\n"
MF "v[124:127],v[188:191],a[132:135],v[124:127]\n"
CV "v32,v32,v33\n"
CV "v33,v34,v35\n"
MF "v[124:127],a[240:243],a[172:175],v[124:127]\n"
"s_add_u32 s87,s90,2048\n"
CV "v34,v36,v37\n"
CV "v35,v38,v39\n"
MF "v[124:127],a[244:247],a[212:215],v[124:127]\n"
"buffer_store_dwordx4 v[32:35],v9,s[40:43],s87 offen sc1\n"
MF "v[116:119],a[244:247],a[208:211],v[116:119]\n"
"v_add_f32 v40,v40,v80\n"
"v_add_f32 v41,v41,v81\n"
MF "v[116:119],a[240:243],a[168:171],v[116:119]\n"
"v_add_f32 v42,v42,v82\n"
"v_add_f32 v43,v43,v83\n"
MF "v[116:119],v[188:191],a[128:131],v[116:119]\n"
"v_add_f32 v44,v44,v84\n"
"v_add_f32 v45,v45,v85\n"
MF "v[108:111],v[188:191],a[124:127],v[108:111]\n"
"v_add_f32 v46,v46,v86\n"
"v_add_f32 v47,v47,v87\n"
MF "v[108:111],a[240:243],a[164:167],v[108:111]\n"
CV "v40,v40,v41\n"
CV "v41,v42,v43\n"
MF "v[108:111],a[244:247],a[204:207],v[108:111]\n"
"s_add_u32 s87,s90,3072\n"
CV "v42,v44,v45\n"
CV "v43,v46,v47\n"
MF "v[100:103],a[244:247],a[200:203],v[100:103]\n"
"buffer_store_dwordx4 v[40:43],v9,s[40:43],s87 offen sc1\n"
MF "v[100:103],a[240:243],a[160:163],v[100:103]\n"
"v_add_f32 v48,v48,v88\n"
"v_add_f32 v49,v49,v89\n"
MF "v[100:103],v[188:191],a[120:123],v[100:103]\n"
"s_waitcnt lgkmcnt(0)\n"
"v_add_f32 v50,v50,v90\n"
"v_add_f32 v51,v51,v91\n"
MF "v[136:139],v[176:179],a[120:123],v[136:139]\n"
"s_nop 0\n"
TR "v[188:189],v3\n"
TR "v[190:191],v3 offset:2048\n"
MF "v[136:139],v[180:183],a[160:163],v[136:139]\n"
"s_nop 0\n"
TR "a[240:241],v3 offset:4096\n"
TR "a[242:243],v3 offset:6144\n"
MF "v[136:139],v[184:187],a[200:203],v[136:139]\n"
"s_nop 0\n"
TR "a[244:245],v3 offset:8192\n"
TR "a[246:247],v3 offset:10240\n"
MF "v[144:147],v[184:187],a[204:207],v[144:147]\n"
"v_add_f32 v52,v52,v92\n"
"v_add_f32 v53,v53,v93\n"
MF "v[144:147],v[180:183],a[164:167],v[144:147]\n"
"v_add_f32 v54,v54,v94\n"
"v_add_f32 v55,v55,v95\n"
MF "v[144:147],v[176:179],a[124:127],v[144:147]\n"
CV "v48,v48,v49\n"
CV "v49,v50,v51\n"
MF "v[152:155],v[176:179],a[128:131],v[152:155]\n"
"s_add_u32 s87,s90,4096\n"
CV "v50,v52,v53\n"
CV "v51,v54,v55\n"
MF "v[152:155],v[180:183],a[168:171],v[152:155]\n"
"buffer_store_dwordx4 v[48:51],v9,s[40:43],s87 offen sc1\n"
MF "v[152:155],v[184:187],a[208:211],v[152:155]\n"
MF "v[160:163],v[184:187],a[212:215],v[160:163]\n"
MF "v[160:163],v[180:183],a[172:175],v[160:163]\n"
MF "v[160:163],v[176:179],a[132:135],v[160:163]\n"
MF "v[168:171],v[176:179],a[136:139],v[168:171]\n"
MF "v[168:171],v[180:183],a[176:179],v[168:171]\n"
MF "v[168:171],v[184:187],a[216:219],v[168:171]\n"
"s_waitcnt lgkmcnt(0)\n"
"s_barrier\n"
MF "v[172:175],a[244:247],a[216:219],v[172:175]\n"
"s_nop 0\n"
TR "v[176:177],v0 offset:49152\n"
TR "v[178:179],v0 offset:51200\n"
MF "v[172:175],a[240:243],a[176:179],v[172:175]\n"
"s_nop 0\n"
TR "v[180:181],v0 offset:53248\n"
TR "v[182:183],v0 offset:55296\n"
MF "v[172:175],v[188:191],a[136:139],v[172:175]\n"
"s_nop 0\n"
TR "v[184:185],v0 offset:57344\n"
TR "v[186:187],v0 offset:59392\n"
MF "v[164:167],v[188:191],a[132:135],v[164:167]\n"
"buffer_load_dword v10,v12,s[40:43],s86 offen sc1\n"
MF "v[164:167],a[240:243],a[172:175],v[164:167]\n"
MF "v[164:167],a[244:247],a[212:215],v[164:167]\n"
MF "v[156:159],a[244:247],a[208:211],v[156:159]\n"
MF "v[156:159],a[240:243],a[168:171],v[156:159]\n"
MF "v[156:159],v[188:191],a[128:131],v[156:159]\n"
MF "v[148:151],v[188:191],a[124:127],v[148:151]\n"
MF "v[148:151],a[240:243],a[164:167],v[148:151]\n"
MF "v[148:151],a[244:247],a[204:207],v[148:151]\n"
MF "v[140:143],a[244:247],a[200:203],v[140:143]\n"
MF "v[140:143],a[240:243],a[160:163],v[140:143]\n"
MF "v[140:143],v[188:191],a[120:123],v[140:143]\n"
"s_waitcnt lgkmcnt(0)\n"
MF "v[96:99],v[176:179],a[20:23],v[96:99]\n"
"s_nop 0\n"
TR "v[188:189],v1 offset:49152\n"
TR "v[190:191],v1 offset:51200\n"
MF "v[96:99],v[180:183],a[60:63],v[96:99]\n"
"s_nop 0\n"
TR "a[240:241],v1 offset:53248\n"
TR "a[242:243],v1 offset:55296\n"
MF "v[96:99],v[184:187],a[100:103],v[96:99]\n"
"s_nop 0\n"
TR "a[244:245],v1 offset:57344\n"
TR "a[246:247],v1 offset:59392\n"
MF "v[104:107],v[184:187],a[104:107],v[104:107]\n"
MF "v[104:107],v[180:183],a[64:67],v[104:107]\n"
MF "v[104:107],v[176:179],a[24:27],v[104:107]\n"
"s_waitcnt vmcnt(7)\n"
MF "v[136:139],v[208:211],a[252:255],v[136:139]\n"
MF "v[144:147],v[212:215],a[252:255],v[144:147]\n"
MF "v[152:155],v[216:219],a[252:255],v[152:155]\n"
MF "v[160:163],v[220:223],a[252:255],v[160:163]\n"
MF "v[168:171],v[224:227],a[252:255],v[168:171]\n"
MF "v[140:143],v[208:211],a[248:251],v[140:143]\n"
MF "v[148:151],v[212:215],a[248:251],v[148:151]\n"
MF "v[156:159],v[216:219],a[248:251],v[156:159]\n"
MF "v[164:167],v[220:223],a[248:251],v[164:167]\n"
MF "v[172:175],v[224:227],a[248:251],v[172:175]\n"
MF "v[112:115],v[176:179],a[28:31],v[112:115]\n"
MF "v[112:115],v[180:183],a[68:71],v[112:115]\n"
MF "v[112:115],v[184:187],a[108:111],v[112:115]\n"
MF "v[120:123],v[184:187],a[112:115],v[120:123]\n"
MF "v[120:123],v[180:183],a[72:75],v[120:123]\n"
"s_bfe_u32 s87,s22,0x30014\n"
MF "v[120:123],v[176:179],a[32:35],v[120:123]\n"
"s_lshl_b32 s87,s87,7\n"
MF "v[128:131],v[176:179],a[36:39],v[128:131]\n"
"s_and_b32 s60,s56,1\n"
MF "v[128:131],v[180:183],a[76:79],v[128:131]\n"
"s_mul_i32 s60,s60,204800\n"
MF "v[128:131],v[184:187],a[116:119],v[128:131]\n"
"s_waitcnt lgkmcnt(0)\n"
MF "v[132:135],a[244:247],a[116:119],v[132:135]\n"
"s_add_u32 s87,s87,s60\n"
TR "v[176:177],v2 offset:49152\n"
TR "v[178:179],v2 offset:51200\n"
MF "v[132:135],a[240:243],a[76:79],v[132:135]\n"
"s_mov_b32 s71,204800\n"
TR "v[180:181],v2 offset:53248\n"
TR "v[182:183],v2 offset:55296\n"
MF "v[132:135],v[188:191],a[36:39],v[132:135]\n"
"s_mov_b32 s66,204800\n"
TR "v[184:185],v2 offset:57344\n"
TR "v[186:187],v2 offset:59392\n"
MF "v[124:127],v[188:191],a[32:35],v[124:127]\n"
"s_add_u32 s72,s78,2\n"
MF "v[124:127],a[240:243],a[72:75],v[124:127]\n"
"s_and_b32 s72,s72,15\n"
MF "v[124:127],a[244:247],a[112:115],v[124:127]\n"
"s_bfe_u32 s60,s22,0x10014\n"
MF "v[116:119],a[244:247],a[108:111],v[116:119]\n"
"s_lshl_b32 s60,s60,5\n"
MF "v[116:119],a[240:243],a[68:71],v[116:119]\n"
"v_xor_b32 v242,s60,v8\n"
MF "v[116:119],v[188:191],a[28:31],v[116:119]\n"
MF "v[108:111],v[188:191],a[24:27],v[108:111]\n"
"s_add_u32 s16,s78,6\n"
MF "v[108:111],a[240:243],a[64:67],v[108:111]\n"
"s_and_b32 s16,s16,15\n"
MF "v[108:111],a[244:247],a[104:107],v[108:111]\n"
"s_add_u32 s17,s78,4\n"
MF "v[100:103],a[244:247],a[100:103],v[100:103]\n"
"s_and_b32 s17,s17,15\n"
MF "v[100:103],a[240:243],a[60:63],v[100:103]\n"
MF "v[100:103],v[188:191],a[20:23],v[100:103]\n"
"s_waitcnt lgkmcnt(0)\n"
MF "v[136:139],v[176:179],a[20:23],v[136:139]\n"
"s_nop 0\n"
TR "v[188:189],v3 offset:49152\n"
TR "v[190:191],v3 offset:51200\n"
MF "v[136:139],v[180:183],a[60:63],v[136:139]\n"
"s_nop 0\n"
TR "a[240:241],v3 offset:53248\n"
TR "a[242:243],v3 offset:55296\n"
MF "v[136:139],v[184:187],a[100:103],v[136:139]\n"
"s_nop 0\n"
TR "a[244:245],v3 offset:57344\n"
TR "a[246:247],v3 offset:59392\n"
MF "v[144:147],v[184:187],a[104:107],v[144:147]\n"
MF "v[144:147],v[180:183],a[64:67],v[144:147]\n"
"v_mov_b32 v14,s76\n"
"s_add_u32 s76,s76,1\n"
MF "v[144:147],v[176:179],a[24:27],v[144:147]\n"
".Lpg29h:\n"
"s_cbranch_execz .Lisr4\n"
MF "v[152:155],v[176:179],a[28:31],v[152:155]\n"
MF "v[152:155],v[180:183],a[68:71],v[152:155]\n"
MF "v[152:155],v[184:187],a[108:111],v[152:155]\n"
MF "v[160:163],v[184:187],a[112:115],v[160:163]\n"
MF "v[160:163],v[180:183],a[72:75],v[160:163]\n"
MF "v[160:163],v[176:179],a[32:35],v[160:163]\n"
MF "v[168:171],v[176:179],a[36:39],v[168:171]\n"
MF "v[168:171],v[180:183],a[76:79],v[168:171]\n"
MF "v[168:171],v[184:187],a[116:119],v[168:171]\n"
"s_waitcnt vmcnt(0) lgkmcnt(0)\n"
"s_barrier\n"
MF "v[172:175],a[244:247],a[116:119],v[172:175]\n"
"buffer_store_dword v14,v15,s[40:43],s94 offen sc1\n"
MF "v[172:175],a[240:243],a[76:79],v[172:175]\n"
"s_nop 0\n"
TR "v[176:177],v6\n"
TR "v[178:179],v6 offset:2048\n"
MF "v[172:175],v[188:191],a[36:39],v[172:175]\n"
"s_nop 0\n"
TR "v[180:181],v6 offset:4096\n"
TR "v[182:183],v6 offset:6144\n"
MF "v[164:167],v[188:191],a[32:35],v[164:167]\n"
"s_nop 0\n"
TR "v[184:185],v6 offset:8192\n"
TR "v[186:187],v6 offset:10240\n"
MF "v[164:167],a[240:243],a[72:75],v[164:167]\n"
"v_sub_u32 v10,v10,v11\n"
MF "v[164:167],a[244:247],a[112:115],v[164:167]\n"
"v_readlane_b32 s16,v10,s16\n"
MF "v[156:159],a[244:247],a[108:111],v[156:159]\n"
"s_cmp_lt_u32 s16,9\n"
"s_cselect_b32 s16,0xf0000000,0\n"
MF "v[156:159],a[240:243],a[68:71],v[156:159]\n"
"v_readlane_b32 s17,v10,s17\n"
MF "v[156:159],v[188:191],a[28:31],v[156:159]\n"
"s_cmp_lt_u32 s17,10\n"
"s_cselect_b32 s17,0xf0000000,0\n"
MF "v[148:151],v[188:191],a[24:27],v[148:151]\n"
"s_bfe_u32 s62,s22,0x30014\n"
MF "v[148:151],a[240:243],a[64:67],v[148:151]\n"
"s_add_u32 s62,s62,3\n"
"s_and_b32 s62,s62,7\n"
"s_mul_i32 s62,s62,204800\n"
MF "v[148:151],a[244:247],a[104:107],v[148:151]\n"
"s_add_u32 s62,s62,s73\n"
"s_add_u32 s62,s62,122880\n"
"s_or_b32 s62,s62,s16\n"
MF "v[140:143],a[244:247],a[100:103],v[140:143]\n"
LD "v[56:59],v9,s[40:43],s62 offen sc1\n"
MF "v[140:143],a[240:243],a[60:63],v[140:143]\n"
LD "v[64:67],v9,s[40:43],s62 offen offset:1024 sc1\n"
MF "v[140:143],v[188:191],a[20:23],v[140:143]\n"
"s_waitcnt lgkmcnt(0)\n"
LD "v[72:75],v9,s[40:43],s62 offen offset:2048 sc1\n"
MF "v[136:139],v[176:179],a[140:143],v[136:139]\n"
"s_nop 0\n"
TR "v[188:189],v7\n"
TR "v[190:191],v7 offset:2048\n"
MF "v[136:139],v[180:183],a[180:183],v[136:139]\n"
"s_nop 0\n"
TR "a[240:241],v7 offset:4096\n"
TR "a[242:243],v7 offset:6144\n"
MF "v[136:139],v[184:187],a[220:223],v[136:139]\n"
"s_nop 0\n"
TR "a[244:245],v7 offset:8192\n"
TR "a[246:247],v7 offset:10240\n"
MF "v[144:147],v[184:187],a[224:227],v[144:147]\n"
LD "v[80:83],v9,s[40:43],s62 offen offset:3072 sc1\n"
MF "v[144:147],v[180:183],a[184:187],v[144:147]\n"
MF "v[144:147],v[176:179],a[144:147],v[144:147]\n"
"s_add_u32 s62,s62,4096\n"
MF "v[152:155],v[176:179],a[148:151],v[152:155]\n"
LD "v[88:91],v9,s[40:43],s62 offen sc1\n"
MF "v[152:155],v[180:183],a[188:191],v[152:155]\n"
"s_bfe_u32 s62,s22,0x30014\n"
"ds_write_addtid_b32 v136\n"
"ds_write_addtid_b32 v137 offset:256\n"
MF "v[152:155],v[184:187],a[228:231],v[152:155]\n"
"s_add_u32 s62,s62,2\n"
"s_and_b32 s62,s62,7\n"
"s_mul_i32 s62,s62,204800\n"
MF "v[160:163],v[184:187],a[232:235],v[160:163]\n"
"s_add_u32 s62,s62,s73\n"
"s_add_u32 s62,s62,143360\n"
"s_or_b32 s62,s62,s17\n"
MF "v[160:163],v[180:183],a[192:195],v[160:163]\n"
LD "v[60:63],v9,s[40:43],s62 offen sc1\n"
MF "v[160:163],v[176:179],a[152:155],v[160:163]\n"
"s_nop 0\n"
"ds_write_addtid_b32 v138 offset:512\n"
"ds_write_addtid_b32 v139 offset:768\n"
MF "v[168:171],v[176:179],a[156:159],v[168:171]\n"
LD "v[68:71],v9,s[40:43],s62 offen offset:1024 sc1\n"
MF "v[168:171],v[180:183],a[196:199],v[168:171]\n"
"s_nop 0\n"
"ds_write_addtid_b32 v144 offset:2048\n"
"ds_write_addtid_b32 v145 offset:2304\n"
MF "v[168:171],v[184:187],a[236:239],v[168:171]\n"
"s_waitcnt lgkmcnt(6)\n"
LD "v[76:79],v9,s[40:43],s62 offen offset:2048 sc1\n"
MF "v[140:143],v[188:191],a[140:143],v[140:143]\n"
"s_nop 0\n"
TR "v[176:177],v4\n"
TR "v[178:179],v4 offset:2048\n"
MF "v[140:143],a[240:243],a[180:183],v[140:143]\n"
"s_nop 0\n"
TR "v[180:181],v4 offset:4096\n"
TR "v[182:183],v4 offset:6144\n"
MF "v[140:143],a[244:247],a[220:223],v[140:143]\n"
"s_nop 0\n"
TR "v[184:185],v4 offset:8192\n"
TR "v[186:187],v4 offset:10240\n"
MF "v[148:151],a[244:247],a[224:227],v[148:151]\n"
LD "v[84:87],v9,s[40:43],s62 offen offset:3072 sc1\n"
MF "v[148:151],a[240:243],a[184:187],v[148:151]\n"
"s_nop 0\n"
"ds_write_addtid_b32 v146 offset:2560\n"
"ds_write_addtid_b32 v147 offset:2816\n"
MF "v[148:151],v[188:191],a[144:147],v[148:151]\n"
"s_add_u32 s62,s62,4096\n"
"ds_write_addtid_b32 v152 offset:4096\n"
"ds_write_addtid_b32 v153 offset:4352\n"
MF "v[156:159],v[188:191],a[148:151],v[156:159]\n"
LD "v[92:95],v9,s[40:43],s62 offen sc1\n"
MF "v[156:159],a[240:243],a[188:191],v[156:159]\n"
"s_nop 0\n"
"ds_write_addtid_b32 v154 offset:4608\n"
"ds_write_addtid_b32 v155 offset:4864\n"
MF "v[156:159],a[244:247],a[228:231],v[156:159]\n"
"s_nop 0\n"
"ds_write_addtid_b32 v160 offset:6144\n"
"ds_write_addtid_b32 v161 offset:6400\n"
MF "v[164:167],a[244:247],a[232:235],v[164:167]\n"
"s_nop 0\n"
"ds_write_addtid_b32 v162 offset:6656\n"
"ds_write_addtid_b32 v163 offset:6912\n"
MF "v[164:167],a[240:243],a[192:195],v[164:167]\n"
"s_nop 0\n"
"ds_write_addtid_b32 v168 offset:8192\n"
"ds_write_addtid_b32 v169 offset:8448\n"
MF "v[164:167],v[188:191],a[152:155],v[164:167]\n"
"buffer_load_dword v10,v12,s[40:43],s86 offen sc1\n"
MF "v[172:175],v[188:191],a[156:159],v[172:175]\n"
"s_nop 0\n"
"ds_write_addtid_b32 v170 offset:8704\n"
"ds_write_addtid_b32 v171 offset:8960\n"
MF "v[172:175],a[240:243],a[196:199],v[172:175]\n"
"s_nop 0\n"
"ds_write_addtid_b32 v140 offset:1024\n"
"ds_write_addtid_b32 v141 offset:1280\n"
MF "v[172:175],a[244:247],a[236:239],v[172:175]\n"
"s_waitcnt lgkmcnt(15)\n"
"ds_write_addtid_b32 v142 offset:1536\n"
"ds_write_addtid_b32 v143 offset:1792\n"
MF "v[96:99],v[176:179],a[140:143],v[96:99]\n"
"s_nop 0\n"
TR "v[188:189],v5\n"
TR "v[190:191],v5 offset:2048\n"
MF "v[96:99],v[180:183],a[180:183],v[96:99]\n"
"s_nop 0\n"
TR "a[240:241],v5 offset:4096\n"
TR "a[242:243],v5 offset:6144\n"
MF "v[96:99],v[184:187],a[220:223],v[96:99]\n"
"s_nop 0\n"
TR "a[244:245],v5 offset:8192\n"
TR "a[246:247],v5 offset:10240\n"
MF "v[104:107],v[184:187],a[224:227],v[104:107]\n"
"s_nop 0\n"
"ds_write_addtid_b32 v148 offset:3072\n"
"ds_write_addtid_b32 v149 offset:3328\n"
MF "v[104:107],v[180:183],a[184:187],v[104:107]\n"
"s_nop 0\n"
"ds_write_addtid_b32 v150 offset:3584\n"
"ds_write_addtid_b32 v151 offset:3840\n"
MF "v[104:107],v[176:179],a[144:147],v[104:107]\n"
"s_nop 0\n"
"ds_write_addtid_b32 v156 offset:5120\n"
"ds_write_addtid_b32 v157 offset:5376\n"
MF "v[112:115],v[176:179],a[148:151],v[112:115]\n"
"s_nop 0\n"
"ds_write_addtid_b32 v158 offset:5632\n"
"ds_write_addtid_b32 v159 offset:5888\n"
MF "v[112:115],v[180:183],a[188:191],v[112:115]\n"
"ds_write_addtid_b32 v164 offset:7168\n"
MF "v[112:115],v[184:187],a[228:231],v[112:115]\n"
"ds_write_addtid_b32 v165 offset:7424\n"
MF "v[120:123],v[184:187],a[232:235],v[120:123]\n"
"ds_write_addtid_b32 v166 offset:7680\n"
MF "v[120:123],v[180:183],a[192:195],v[120:123]\n"
"ds_write_addtid_b32 v167 offset:7936\n"
MF "v[120:123],v[176:179],a[152:155],v[120:123]\n"
"ds_write_addtid_b32 v172 offset:9216\n"
MF "v[128:131],v[176:179],a[156:159],v[128:131]\n"
"ds_write_addtid_b32 v173 offset:9472\n"
MF "v[128:131],v[180:183],a[196:199],v[128:131]\n"
"ds_write_addtid_b32 v174 offset:9728\n"
MF "v[128:131],v[184:187],a[236:239],v[128:131]\n"
"s_waitcnt lgkmcnt(15)\n"
"ds_write_addtid_b32 v175 offset:9984\n"
MF "v[100:103],v[188:191],a[140:143],v[100:103]\n"
MF "v[100:103],a[240:243],a[180:183],v[100:103]\n"
MF "v[100:103],a[244:247],a[220:223],v[100:103]\n"
MF "v[108:111],a[244:247],a[224:227],v[108:111]\n"
MF "v[108:111],a[240:243],a[184:187],v[108:111]\n"
"s_waitcnt lgkmcnt(0)\n"
"s_barrier\n"
MF "v[108:111],v[188:191],a[144:147],v[108:111]\n"
"ds_read2st64_b32 v[16:17],v13 offset0:192 offset1:193\n"
MF "v[116:119],v[188:191],a[148:151],v[116:119]\n"
"ds_read2st64_b32 v[18:19],v13 offset0:194 offset1:195\n"
MF "v[116:119],a[240:243],a[188:191],v[116:119]\n"
"ds_read2st64_b32 v[20:21],v13 offset0:196 offset1:197\n"
MF "v[116:119],a[244:247],a[228:231],v[116:119]\n"
"ds_read2st64_b32 v[22:23],v13 offset0:198 offset1:199\n"
MF "v[124:127],a[244:247],a[232:235],v[124:127]\n"
"ds_read2st64_b32 v[24:25],v13 offset0:200 offset1:201\n"
MF "v[124:127],a[240:243],a[192:195],v[124:127]\n"
"ds_read2st64_b32 v[26:27],v13 offset0:202 offset1:203\n"
MF "v[124:127],v[188:191],a[152:155],v[124:127]\n"
"ds_read2st64_b32 v[28:29],v13 offset0:204 offset1:205\n"
MF "v[132:135],v[188:191],a[156:159],v[132:135]\n"
"ds_read2st64_b32 v[30:31],v13 offset0:206 offset1:207\n"
MF "v[132:135],a[240:243],a[196:199],v[132:135]\n"
"ds_read2st64_b32 v[32:33],v13 offset0:208 offset1:209\n"
MF "v[132:135],a[244:247],a[236:239],v[132:135]\n"
"ds_read2st64_b32 v[34:35],v13 offset0:210 offset1:211\n"
"ds_read2st64_b32 v[36:37],v13 offset0:212 offset1:213\n"
"ds_read2st64_b32 v[38:39],v13 offset0:214 offset1:215\n"
"ds_read2st64_b32 v[40:41],v13 offset0:216 offset1:217\n"
"ds_read2st64_b32 v[42:43],v13 offset0:218 offset1:219\n"
"ds_read2st64_b32 v[44:45],v13 offset0:220 offset1:221\n"
"ds_read2st64_b32 v[46:47],v13 offset0:222 offset1:223\n"
"ds_read2st64_b32 v[48:49],v13 offset0:224 offset1:225\n"
"ds_read2st64_b32 v[50:51],v13 offset0:226 offset1:227\n"
"ds_read2st64_b32 v[52:53],v13 offset0:228 offset1:229\n"
"ds_read2st64_b32 v[54:55],v13 offset0:230 offset1:231\n"
"s_waitcnt vmcnt(0)\n"
"v_sub_u32 v10,v10,v11\n"
"s_cmp_eq_u32 s16,0\n"
"s_cbranch_scc1 .Lhave13\n"
"s_add_u32 s59,s78,6\n"
"s_and_b32 s59,s59,15\n"
"s_nop 3\n"
".Lnf7:\n"
"v_readlane_b32 s58,v10,s59\n"
"s_cmp_lt_u32 s58,9\n"
"s_cbranch_scc1 .Lsp7\n"
".Lnb7:\n"
"s_bfe_u32 s62,s22,0x30014\n"
"s_add_u32 s62,s62,3\n"
"s_and_b32 s62,s62,7\n"
"s_mul_i32 s62,s62,204800\n"
"s_add_u32 s62,s62,s73\n"
"s_add_u32 s62,s62,122880\n"
LD "v[56:59],v9,s[40:43],s62 offen sc1\n"
LD "v[64:67],v9,s[40:43],s62 offen offset:1024 sc1\n"
LD "v[72:75],v9,s[40:43],s62 offen offset:2048 sc1\n"
LD "v[80:83],v9,s[40:43],s62 offen offset:3072 sc1\n"
"s_add_u32 s62,s62,4096\n"
LD "v[88:91],v9,s[40:43],s62 offen sc1\n"
".Lhave13:\n"
"s_cmp_eq_u32 s17,0\n"
"s_cbranch_scc1 .Lhave12\n"
"s_add_u32 s59,s78,4\n"
"s_and_b32 s59,s59,15\n"
"s_nop 3\n"
".Lnf8:\n"
"v_readlane_b32 s58,v10,s59\n"
"s_cmp_lt_u32 s58,10\n"
"s_cbranch_scc1 .Lsp8\n"
".Lnb8:\n"
"s_bfe_u32 s62,s22,0x30014\n"
"s_add_u32 s62,s62,2\n"
"s_and_b32 s62,s62,7\n"
"s_mul_i32 s62,s62,204800\n"
"s_add_u32 s62,s62,s73\n"
"s_add_u32 s62,s62,143360\n"
LD "v[60:63],v9,s[40:43],s62 offen sc1\n"
LD "v[68:71],v9,s[40:43],s62 offen offset:1024 sc1\n"
LD "v[76:79],v9,s[40:43],s62 offen offset:2048 sc1\n"
LD "v[84:87],v9,s[40:43],s62 offen offset:3072 sc1\n"
"s_add_u32 s62,s62,4096\n"
LD "v[92:95],v9,s[40:43],s62 offen sc1\n"
".Lhave12:\n"
".Lnf9:\n"
"v_readlane_b32 s58,v10,s72\n"
"s_cmp_lt_u32 s58,11\n"
"s_cbranch_scc1 .Lsp9\n"
".Lnb9:\n"
"s_bfe_u32 s62,s22,0x30014\n"
"s_add_u32 s62,s62,1\n"
"s_and_b32 s62,s62,7\n"
"s_mul_i32 s62,s62,204800\n"
"s_add_u32 s62,s62,s73\n"
"s_add_u32 s62,s62,163840\n"
LD "v[208:211],v9,s[40:43],s62 offen sc1\n"
LD "v[212:215],v9,s[40:43],s62 offen offset:1024 sc1\n"
LD "v[216:219],v9,s[40:43],s62 offen offset:2048 sc1\n"
LD "v[220:223],v9,s[40:43],s62 offen offset:3072 sc1\n"
"s_add_u32 s62,s62,4096\n"
LD "v[224:227],v9,s[40:43],s62 offen sc1\n"
"s_waitcnt lgkmcnt(15)\n"
"v_pk_add_f32 v[96:97],v[96:97],v[16:17]\n"
"v_pk_add_f32 v[98:99],v[98:99],v[18:19]\n"
"v_pk_add_f32 v[100:101],v[100:101],v[20:21]\n"
"v_pk_add_f32 v[102:103],v[102:103],v[22:23]\n"
"v_pk_add_f32 v[104:105],v[104:105],v[24:25]\n"
"s_waitcnt lgkmcnt(14)\n"
"v_pk_add_f32 v[106:107],v[106:107],v[26:27]\n"
"s_waitcnt lgkmcnt(13)\n"
"v_pk_add_f32 v[108:109],v[108:109],v[28:29]\n"
"s_waitcnt lgkmcnt(12)\n"
"v_pk_add_f32 v[110:111],v[110:111],v[30:31]\n"
"s_waitcnt lgkmcnt(11)\n"
"v_pk_add_f32 v[112:113],v[112:113],v[32:33]\n"
"s_waitcnt lgkmcnt(10)\n"
"v_pk_add_f32 v[114:115],v[114:115],v[34:35]\n"
"s_waitcnt lgkmcnt(9)\n"
"v_pk_add_f32 v[116:117],v[116:117],v[36:37]\n"
"s_waitcnt lgkmcnt(8)\n"
"v_pk_add_f32 v[118:119],v[118:119],v[38:39]\n"
"s_waitcnt lgkmcnt(7)\n"
"v_pk_add_f32 v[120:121],v[120:121],v[40:41]\n"
"s_waitcnt lgkmcnt(6)\n"
"v_pk_add_f32 v[122:123],v[122:123],v[42:43]\n"
"s_waitcnt lgkmcnt(5)\n"
"v_pk_add_f32 v[124:125],v[124:125],v[44:45]\n"
"s_waitcnt lgkmcnt(4)\n"
"v_pk_add_f32 v[126:127],v[126:127],v[46:47]\n"
"s_waitcnt lgkmcnt(3)\n"
"v_pk_add_f32 v[128:129],v[128:129],v[48:49]\n"
"s_waitcnt lgkmcnt(2)\n"
"v_pk_add_f32 v[130:131],v[130:131],v[50:51]\n"
"s_waitcnt lgkmcnt(1)\n"
"v_pk_add_f32 v[132:133],v[132:133],v[52:53]\n"
"s_waitcnt lgkmcnt(0)\n"
"v_pk_add_f32 v[134:135],v[134:135],v[54:55]\n"
"s_waitcnt vmcnt(5)\n"
"s_nop 0\n"
MF "v[100:103],v[56:59],a[252:255],v[100:103]\n"
MF "v[108:111],v[64:67],a[252:255],v[108:111]\n"
MF "v[116:119],v[72:75],a[252:255],v[116:119]\n"
MF "v[124:127],v[80:83],a[252:255],v[124:127]\n"
MF "v[132:135],v[88:91],a[252:255],v[132:135]\n"
MF "v[96:99],v[56:59],a[248:251],v[96:99]\n"
MF "v[104:107],v[64:67],a[248:251],v[104:107]\n"
MF "v[112:115],v[72:75],a[248:251],v[112:115]\n"
MF "v[120:123],v[80:83],a[248:251],v[120:123]\n"
MF "v[128:131],v[88:91],a[248:251],v[128:131]\n"
MF "v[96:99],v[60:63],a[252:255],v[96:99]\n"
MF "v[104:107],v[68:71],a[252:255],v[104:107]\n"
MF "v[112:115],v[76:79],a[252:255],v[112:115]\n"
MF "v[120:123],v[84:87],a[252:255],v[120:123]\n"
MF "v[128:131],v[92:95],a[252:255],v[128:131]\n"
MF "v[100:103],v[60:63],a[248:251],v[100:103]\n"
MF "v[108:111],v[68:71],a[248:251],v[108:111]\n"
MF "v[116:119],v[76:79],a[248:251],v[116:119]\n"
MF "v[124:127],v[84:87],a[248:251],v[124:127]\n"
MF "v[132:135],v[92:95],a[248:251],v[132:135]\n"
"s_waitcnt vmcnt(0)\n"
MF "v[100:103],v[208:211],a[252:255],v[100:103]\n"
MF "v[96:99],v[208:211],a[248:251],v[96:99]\n"
MF "v[108:111],v[212:215],a[252:255],v[108:111]\n"
MF "v[104:107],v[212:215],a[248:251],v[104:107]\n"
MF "v[116:119],v[216:219],a[252:255],v[116:119]\n"
"s_nop 4\n"
CV "v96,v96,v97\n"
CV "v97,v98,v99\n"
MF "v[112:115],v[216:219],a[248:251],v[112:115]\n"
CV "v98,v100,v101\n"
CV "v99,v102,v103\n"
MF "v[124:127],v[220:223],a[252:255],v[124:127]\n"
CV "v104,v104,v105\n"
CV "v105,v106,v107\n"
MF "v[120:123],v[220:223],a[248:251],v[120:123]\n"
CV "v106,v108,v109\n"
CV "v107,v110,v111\n"
"v_permlane16_swap_b32 v96,v98\n"
"v_permlane16_swap_b32 v97,v99\n"
"buffer_store_dwordx4 v[96:99],v242,s[44:47],s87 offen sc1\n"
"s_add_u32 s87,s87,40960\n"
MF "v[132:135],v[224:227],a[252:255],v[132:135]\n"
CV "v112,v112,v113\n"
CV "v113,v114,v115\n"
MF "v[128:131],v[224:227],a[248:251],v[128:131]\n"
CV "v114,v116,v117\n"
CV "v115,v118,v119\n"
"v_permlane16_swap_b32 v104,v106\n"
"v_permlane16_swap_b32 v105,v107\n"
"buffer_store_dwordx4 v[104:107],v242,s[44:47],s87 offen sc1\n"
"s_add_u32 s87,s87,40960\n"
CV "v120,v120,v121\n"
CV "v121,v122,v123\n"
CV "v122,v124,v125\n"
CV "v123,v126,v127\n"
"v_permlane16_swap_b32 v112,v114\n"
"v_permlane16_swap_b32 v113,v115\n"
"buffer_store_dwordx4 v[112:115],v242,s[44:47],s87 offen sc1\n"
"s_add_u32 s87,s87,40960\n"
CV "v128,v128,v129\n"
CV "v129,v130,v131\n"
CV "v130,v132,v133\n"
CV "v131,v134,v135\n"
"v_permlane16_swap_b32 v120,v122\n"
"v_permlane16_swap_b32 v121,v123\n"
"buffer_store_dwordx4 v[120:123],v242,s[44:47],s87 offen sc1\n"
"s_add_u32 s87,s87,40960\n"
"v_permlane16_swap_b32 v128,v130\n"
"v_permlane16_swap_b32 v129,v131\n"
"buffer_store_dwordx4 v[128:131],v242,s[44:47],s87 offen sc1\n"
"s_add_u32 s87,s87,40960\n"
"s_branch .Ljobs\n"
".Lpgjobs_h:\n"
"s_cbranch_execz .Lisr4\n"
".Ljobs:\n"
"s_cmp_ge_u32 s92,40\n"
"s_cbranch_scc1 .Ldone\n"
"buffer_load_dword v10,v12,s[40:43],s86 offen sc1\n"
"s_mul_i32 s95,s92,3277\n"
"s_lshr_b32 s95,s95,16\n"
"s_mul_i32 s59,s95,20\n"
"s_sub_u32 s59,s92,s59\n"
"s_cmp_ge_u32 s59,10\n"
"s_cselect_b32 s96,4294557760,0\n"
"s_add_u32 s96,s96,1024\n"
"s_mul_i32 s59,s59,40960\n"
"s_add_u32 s96,s96,s59\n"
"s_lshl_b32 s59,s95,7\n"
"s_add_u32 s96,s96,s59\n"
"s_add_u32 s95,s95,3\n"
"s_waitcnt vmcnt(0)\n"
".Ljtry:\n"
"s_cmp_eq_u32 s98,0\n"
"s_cselect_b32 s95,0,s95\n"
"v_sub_u32 v10,v10,v11\n"
"v_cmp_gt_u32 vcc,s95,v10\n"
"s_lshl_b32 s82,s92,10\n"
"s_add_u32 s82,s82,s93\n"
"s_cmp_eq_u64 vcc,0\n"
"s_cselect_b32 s82,s82,0xf0000000\n"
"s_cselect_b32 s84,s96,0xf0000000\n"
"s_cselect_b32 s59,32,0\n"
"s_add_u32 s92,s92,s59\n"
"s_cmp_eq_u32 s59,0\n"
"s_cbranch_scc0 .Ljgo\n"
"s_sub_u32 s98,s98,1\n"
"s_sleep 2\n"
"s_branch .Ljobs\n"
".Ljgo:\n"
"s_mov_b32 s83,s84\n"
LD "v[208:211],v9,s[40:43],s82 offen sc1\n"
"s_add_u32 s82,s82,s71\n"
LD "v[212:215],v9,s[40:43],s82 offen sc1\n"
"s_add_u32 s82,s82,s71\n"
LD "v[216:219],v9,s[40:43],s82 offen sc1\n"
"s_add_u32 s82,s82,s71\n"
LD "v[220:223],v9,s[40:43],s82 offen sc1\n"
"s_add_u32 s82,s82,s71\n"
LD "v[224:227],v9,s[40:43],s82 offen sc1\n"
"s_add_u32 s82,s82,s66\n"
LD "v[228:231],v9,s[40:43],s82 offen sc1\n"
"s_add_u32 s82,s82,s71\n"
LD "v[232:235],v9,s[40:43],s82 offen sc1\n"
"s_add_u32 s82,s82,s71\n"
LD "v[236:239],v9,s[40:43],s82 offen sc1\n"
"buffer_load_dword v10,v12,s[40:43],s86 offen sc1\n"
"s_waitcnt vmcnt(0)\n"
".Ljfin:\n"
"s_nop 1\n"
"v_mov_b32 v240,v208\n"
"v_mov_b32 v241,v209\n"
MF "v[248:251],v[210:213],a[250:253],0\n"
MF "v[244:247],v[214:217],a[250:253],0\n"
MF "v[248:251],v[218:221],a[250:253],v[248:251]\n"
MF "v[244:247],v[222:225],a[250:253],v[244:247]\n"
MF "v[248:251],v[226:229],a[250:253],v[248:251]\n"
MF "v[244:247],v[230:233],a[250:253],v[244:247]\n"
MF "v[248:251],v[234:237],a[250:253],v[248:251]\n"
MF "v[244:247],v[238:241],a[250:253],v[244:247]\n"
"s_nop 7\n"
CV "v244,v244,v245\n"
CV "v245,v246,v247\n"
CV "v246,v248,v249\n"
CV "v247,v250,v251\n"
"s_nop 0\n"
"v_permlane16_swap_b32 v244,v246\n"
"v_permlane16_swap_b32 v245,v247\n"
"buffer_store_dwordx4 v[244:247],v8,s[44:47],s83 offen sc1\n"
"s_cmp_ge_u32 s92,40\n"
"s_cbranch_scc1 .Ldone\n"
"s_mul_i32 s95,s92,3277\n"
"s_lshr_b32 s95,s95,16\n"
"s_mul_i32 s59,s95,20\n"
"s_sub_u32 s59,s92,s59\n"
"s_cmp_ge_u32 s59,10\n"
"s_cselect_b32 s96,4294557760,0\n"
"s_add_u32 s96,s96,1024\n"
"s_mul_i32 s59,s59,40960\n"
"s_add_u32 s96,s96,s59\n"
"s_lshl_b32 s59,s95,7\n"
"s_add_u32 s96,s96,s59\n"
"s_add_u32 s95,s95,3\n"
"s_branch .Ljtry\n"
".Lool:\n"
".Lsp0:\n"
"s_cmp_eq_u32 s98,0\n"
"s_cbranch_scc1 .Lnb0\n"
"s_sub_u32 s98,s98,1\n"
"s_sleep 2\n"
"buffer_load_dword v10,v12,s[40:43],s86 offen sc1\n"
"s_waitcnt vmcnt(0)\n"
"v_sub_u32 v10,v10,v11\n"
"s_nop 1\n"
"s_branch .Lnf0\n"
".Lsp1:\n"
"s_cmp_eq_u32 s98,0\n"
"s_cbranch_scc1 .Lnb1\n"
"s_sub_u32 s98,s98,1\n"
"s_sleep 2\n"
"buffer_load_dword v10,v12,s[40:43],s86 offen sc1\n"
"s_waitcnt vmcnt(0)\n"
"v_sub_u32 v10,v10,v11\n"
"s_nop 1\n"
"s_branch .Lnf1\n"
".Lsp2:\n"
"s_cmp_eq_u32 s98,0\n"
"s_cbranch_scc1 .Lnb2\n"
"s_sub_u32 s98,s98,1\n"
"s_sleep 2\n"
"buffer_load_dword v10,v12,s[40:43],s86 offen sc1\n"
"s_waitcnt vmcnt(0)\n"
"v_sub_u32 v10,v10,v11\n"
"s_nop 1\n"
"s_branch .Lnf2\n"
".Lsp3:\n"
"s_cmp_eq_u32 s98,0\n"
"s_cbranch_scc1 .Lnb3\n"
"s_sub_u32 s98,s98,1\n"
"s_sleep 2\n"
"buffer_load_dword v10,v12,s[40:43],s86 offen sc1\n"
"s_waitcnt vmcnt(0)\n"
"v_sub_u32 v10,v10,v11\n"
"s_nop 1\n"
"s_branch .Lnf3\n"
".Lsp4:\n"
"s_cmp_eq_u32 s98,0\n"
"s_cbranch_scc1 .Lnb4\n"
"s_sub_u32 s98,s98,1\n"
"s_sleep 2\n"
"buffer_load_dword v10,v12,s[40:43],s86 offen sc1\n"
"s_waitcnt vmcnt(0)\n"
"v_sub_u32 v10,v10,v11\n"
"s_nop 1\n"
"s_branch .Lnf4\n"
".Lsp5:\n"
"s_cmp_eq_u32 s98,0\n"
"s_cbranch_scc1 .Lnb5\n"
"s_sub_u32 s98,s98,1\n"
"s_sleep 2\n"
"buffer_load_dword v10,v12,s[40:43],s86 offen sc1\n"
"s_waitcnt vmcnt(0)\n"
"v_sub_u32 v10,v10,v11\n"
"s_nop 1\n"
"s_branch .Lnf5\n"
".Lsp6:\n"
"s_cmp_eq_u32 s98,0\n"
"s_cbranch_scc1 .Lnb6\n"
"s_sub_u32 s98,s98,1\n"
"s_sleep 2\n"
"buffer_load_dword v10,v12,s[40:43],s86 offen sc1\n"
"s_waitcnt vmcnt(0)\n"
"v_sub_u32 v10,v10,v11\n"
"s_nop 1\n"
"s_branch .Lnf6\n"
".Lsp7:\n"
"s_cmp_eq_u32 s98,0\n"
"s_cbranch_scc1 .Lnb7\n"
"s_sub_u32 s98,s98,1\n"
"s_sleep 2\n"
"buffer_load_dword v10,v12,s[40:43],s86 offen sc1\n"
"s_waitcnt vmcnt(0)\n"
"v_sub_u32 v10,v10,v11\n"
"s_nop 1\n"
"s_branch .Lnf7\n"
".Lsp8:\n"
"s_cmp_eq_u32 s98,0\n"
"s_cbranch_scc1 .Lnb8\n"
"s_sub_u32 s98,s98,1\n"
"s_sleep 2\n"
"buffer_load_dword v10,v12,s[40:43],s86 offen sc1\n"
"s_waitcnt vmcnt(0)\n"
"v_sub_u32 v10,v10,v11\n"
"s_nop 1\n"
"s_branch .Lnf8\n"
".Lsp9:\n"
"s_cmp_eq_u32 s98,0\n"
"s_cbranch_scc1 .Lnb9\n"
"s_sub_u32 s98,s98,1\n"
"s_sleep 2\n"
"buffer_load_dword v10,v12,s[40:43],s86 offen sc1\n"
"s_waitcnt vmcnt(0)\n"
"v_sub_u32 v10,v10,v11\n"
"s_nop 1\n"
"s_branch .Lnf9\n"
".Lpgqy_c:\n"
"s_cbranch_execz .Lpgqj_a\n"
".Lqy:\n"
MF "v[216:219],a[216:219],a[60:63],v[216:219]\n"
TR "a[208:209],v0 offset:8\n"
MF "v[216:219],a[220:223],a[100:103],v[216:219]\n"
TR "a[210:211],v0 offset:520\n"
MF "v[224:227],a[220:223],a[104:107],v[224:227]\n"
MF "v[224:227],a[216:219],a[64:67],v[224:227]\n"
TR "a[212:213],v0 offset:4104\n"
MF "v[232:235],a[216:219],a[68:71],v[232:235]\n"
TR "a[214:215],v0 offset:4616\n"
MF "v[232:235],a[220:223],a[108:111],v[232:235]\n"
MF "v[240:243],a[220:223],a[112:115],v[240:243]\n"
MF "v[240:243],a[216:219],a[72:75],v[240:243]\n"
MF "v[248:251],a[216:219],a[76:79],v[248:251]\n"
MF "v[248:251],a[220:223],a[116:119],v[248:251]\n"
MF "v[248:251],a[200:203],a[156:159],v[248:251]\n"
TR "a[216:217],v0 offset:16392\n"
MF "v[248:251],a[204:207],a[196:199],v[248:251]\n"
TR "a[218:219],v0 offset:16904\n"
MF "v[240:243],a[204:207],a[192:195],v[240:243]\n"
MF "v[240:243],a[200:203],a[152:155],v[240:243]\n"
TR "a[220:221],v0 offset:20488\n"
MF "v[232:235],a[200:203],a[148:151],v[232:235]\n"
TR "a[222:223],v0 offset:21000\n"
MF "v[232:235],a[204:207],a[188:191],v[232:235]\n"
MF "v[224:227],a[204:207],a[184:187],v[224:227]\n"
MF "v[224:227],a[200:203],a[144:147],v[224:227]\n"
"v_fma_f32 v248,v248,s54,v14\n"
"v_fma_f32 v249,v249,s54,v14\n"
MF "v[216:219],a[200:203],a[140:143],v[216:219]\n"
"v_fma_f32 v250,v250,s54,v14\n"
"v_fma_f32 v251,v251,s54,v14\n"
MF "v[216:219],a[204:207],a[180:183],v[216:219]\n"
"s_waitcnt lgkmcnt(4)\n"
"v_fma_f32 v252,v252,s54,v14\n"
"v_fma_f32 v253,v253,s54,v14\n"
MF "v[180:183],a[208:211],a[60:63],v[180:183]\n"
TR "a[200:201],v0\n"
"v_fma_f32 v254,v254,s54,v14\n"
MF "v[180:183],a[212:215],a[100:103],v[180:183]\n"
TR "a[202:203],v0 offset:512\n"
"v_fma_f32 v255,v255,s54,v14\n"
MF "v[188:191],a[212:215],a[104:107],v[188:191]\n"
CV "v255,v254,v255\n"
CV "v254,v252,v253\n"
MF "v[188:191],a[208:211],a[64:67],v[188:191]\n"
TR "a[204:205],v0 offset:4096\n"
CV "v253,v250,v251\n"
MF "v[196:199],a[208:211],a[68:71],v[196:199]\n"
TR "a[206:207],v0 offset:4608\n"
CV "v252,v248,v249\n"
MF "v[196:199],a[212:215],a[108:111],v[196:199]\n"
".Lpg30c:\n"
"s_cbranch_execz .Lpg3a\n"
"s_add_u32 s58,s75,4096\n"
"v_fma_f32 v240,v240,s54,v14\n"
"v_fma_f32 v241,v241,s54,v14\n"
MF "v[204:207],a[212:215],a[112:115],v[204:207]\n"
"buffer_store_dwordx4 v[252:255],v9,s[40:43],s58 offen offset:1024 sc1\n"
MF "v[204:207],a[208:211],a[72:75],v[204:207]\n"
"v_fma_f32 v242,v242,s54,v14\n"
"v_fma_f32 v243,v243,s54,v14\n"
MF "v[212:215],a[208:211],a[76:79],v[212:215]\n"
"v_fma_f32 v244,v244,s54,v14\n"
"v_fma_f32 v245,v245,s54,v14\n"
MF "v[212:215],a[212:215],a[116:119],v[212:215]\n"
"s_waitcnt lgkmcnt(4)\n"
"v_fma_f32 v246,v246,s54,v14\n"
"v_fma_f32 v247,v247,s54,v14\n"
MF "v[180:183],a[216:219],a[140:143],v[180:183]\n"
TR "a[208:209],v0 offset:16384\n"
CV "v251,v246,v247\n"
MF "v[180:183],a[220:223],a[180:183],v[180:183]\n"
TR "a[210:211],v0 offset:16896\n"
CV "v250,v244,v245\n"
MF "v[188:191],a[220:223],a[184:187],v[188:191]\n"
CV "v249,v242,v243\n"
CV "v248,v240,v241\n"
MF "v[188:191],a[216:219],a[144:147],v[188:191]\n"
TR "a[212:213],v0 offset:20480\n"
"v_fma_f32 v232,v232,s54,v14\n"
MF "v[196:199],a[216:219],a[148:151],v[196:199]\n"
TR "a[214:215],v0 offset:20992\n"
"v_fma_f32 v233,v233,s54,v14\n"
MF "v[196:199],a[220:223],a[188:191],v[196:199]\n"
"buffer_store_dwordx4 v[248:251],v9,s[40:43],s58 offen sc1\n"
MF "v[204:207],a[220:223],a[192:195],v[204:207]\n"
"v_fma_f32 v234,v234,s54,v14\n"
"v_fma_f32 v235,v235,s54,v14\n"
MF "v[204:207],a[216:219],a[152:155],v[204:207]\n"
"v_fma_f32 v236,v236,s54,v14\n"
"v_fma_f32 v237,v237,s54,v14\n"
MF "v[212:215],a[216:219],a[156:159],v[212:215]\n"
"v_fma_f32 v238,v238,s54,v14\n"
"v_fma_f32 v239,v239,s54,v14\n"
MF "v[212:215],a[220:223],a[196:199],v[212:215]\n"
"s_waitcnt vmcnt(10) lgkmcnt(0)\n"
"s_barrier\n"
MF "v[176:179],a[200:203],a[60:63],v[176:179]\n"
TR "a[216:217],v1 offset:32776\n"
CV "v247,v238,v239\n"
MF "v[176:179],a[204:207],a[100:103],v[176:179]\n"
TR "a[218:219],v1 offset:33288\n"
CV "v246,v236,v237\n"
MF "v[184:187],a[204:207],a[104:107],v[184:187]\n"
"s_add_u32 s66,s67,4718592\n"
"s_sub_u32 s81,s81,384\n"
"s_mov_b32 s83,7077888\n"
MF "v[184:187],a[200:203],a[64:67],v[184:187]\n"
"s_add_u32 m0,s62,0\n"
"s_add_u32 s82,s66,2359296\n"
TR "a[220:221],v1 offset:36872\n"
MF "v[192:195],a[200:203],a[68:71],v[192:195]\n"
"s_sub_u32 s15,s82,7864320\n"
"s_min_u32 s82,s82,s15\n"
TR "a[222:223],v1 offset:37384\n"
MF "v[192:195],a[204:207],a[108:111],v[192:195]\n"
LD "v6,s[28:31],s82 offen lds\n"
MF "v[200:203],a[204:207],a[112:115],v[200:203]\n"
CV "v245,v234,v235\n"
CV "v244,v232,v233\n"
MF "v[200:203],a[200:203],a[72:75],v[200:203]\n"
"v_fma_f32 v224,v224,s54,v14\n"
"v_fma_f32 v225,v225,s54,v14\n"
MF "v[208:211],a[200:203],a[76:79],v[208:211]\n"
"buffer_store_dwordx4 v[244:247],v9,s[40:43],s75 offen offset:3072 sc1\n"
MF "v[208:211],a[204:207],a[116:119],v[208:211]\n"
LD "v11,s[28:31],s82 offen offset:1024 lds\n"
MF "v[208:211],a[208:211],a[156:159],v[208:211]\n"
TR "a[200:201],v1 offset:49160\n"
"v_fma_f32 v226,v226,s54,v14\n"
MF "v[208:211],a[212:215],a[196:199],v[208:211]\n"
TR "a[202:203],v1 offset:49672\n"
"v_fma_f32 v227,v227,s54,v14\n"
MF "v[200:203],a[212:215],a[192:195],v[200:203]\n"
"v_fma_f32 v228,v228,s54,v14\n"
"v_fma_f32 v229,v229,s54,v14\n"
MF "v[200:203],a[208:211],a[152:155],v[200:203]\n"
TR "a[204:205],v1 offset:53256\n"
"v_fma_f32 v230,v230,s54,v14\n"
MF "v[192:195],a[208:211],a[148:151],v[192:195]\n"
TR "a[206:207],v1 offset:53768\n"
"v_fma_f32 v231,v231,s54,v14\n"
MF "v[192:195],a[212:215],a[188:191],v[192:195]\n"
CV "v243,v230,v231\n"
CV "v242,v228,v229\n"
MF "v[184:187],a[212:215],a[184:187],v[184:187]\n"
CV "v241,v226,v227\n"
CV "v240,v224,v225\n"
MF "v[184:187],a[208:211],a[144:147],v[184:187]\n"
"buffer_store_dwordx4 v[240:243],v9,s[40:43],s75 offen offset:2048 sc1\n"
MF "v[176:179],a[208:211],a[140:143],v[176:179]\n"
LD "v12,s[28:31],s82 offen offset:2048 lds\n"
MF "v[176:179],a[212:215],a[180:183],v[176:179]\n"
"s_waitcnt lgkmcnt(4)\n"
"v_fma_f32 v216,v216,s54,v14\n"
"v_fma_f32 v217,v217,s54,v14\n"
MF "v[140:143],a[216:219],a[60:63],v[140:143]\n"
TR "a[208:209],v1 offset:32768\n"
"v_fma_f32 v218,v218,s54,v14\n"
MF "v[140:143],a[220:223],a[100:103],v[140:143]\n"
TR "a[210:211],v1 offset:33280\n"
"v_fma_f32 v219,v219,s54,v14\n"
MF "v[148:151],a[220:223],a[104:107],v[148:151]\n"
"v_fma_f32 v220,v220,s54,v14\n"
"v_fma_f32 v221,v221,s54,v14\n"
MF "v[148:151],a[216:219],a[64:67],v[148:151]\n"
TR "a[212:213],v1 offset:36864\n"
"v_fma_f32 v222,v222,s54,v14\n"
MF "v[156:159],a[216:219],a[68:71],v[156:159]\n"
TR "a[214:215],v1 offset:37376\n"
"v_fma_f32 v223,v223,s54,v14\n"
MF "v[156:159],a[220:223],a[108:111],v[156:159]\n"
CV "v239,v222,v223\n"
CV "v238,v220,v221\n"
MF "v[164:167],a[220:223],a[112:115],v[164:167]\n"
CV "v237,v218,v219\n"
CV "v236,v216,v217\n"
MF "v[164:167],a[216:219],a[72:75],v[164:167]\n"
LD "v13,s[28:31],s82 offen offset:3072 lds\n"
MF "v[172:175],a[216:219],a[76:79],v[172:175]\n"
"buffer_store_dwordx4 v[236:239],v9,s[40:43],s75 offen offset:1024 sc1\n"
MF "v[172:175],a[220:223],a[116:119],v[172:175]\n"
"s_waitcnt lgkmcnt(4)\n"
"v_fma_f32 v208,v208,s54,v14\n"
"v_fma_f32 v209,v209,s54,v14\n"
MF "v[140:143],a[200:203],a[140:143],v[140:143]\n"
TR "a[216:217],v1 offset:49152\n"
"v_fma_f32 v210,v210,s54,v14\n"
MF "v[140:143],a[204:207],a[180:183],v[140:143]\n"
TR "a[218:219],v1 offset:49664\n"
"v_fma_f32 v211,v211,s54,v14\n"
MF "v[148:151],a[204:207],a[184:187],v[148:151]\n"
"v_fma_f32 v212,v212,s54,v14\n"
"v_fma_f32 v213,v213,s54,v14\n"
MF "v[148:151],a[200:203],a[144:147],v[148:151]\n"
TR "a[220:221],v1 offset:53248\n"
"v_fma_f32 v214,v214,s54,v14\n"
MF "v[156:159],a[200:203],a[148:151],v[156:159]\n"
TR "a[222:223],v1 offset:53760\n"
"v_fma_f32 v215,v215,s54,v14\n"
MF "v[156:159],a[204:207],a[188:191],v[156:159]\n"
CV "v235,v214,v215\n"
CV "v234,v212,v213\n"
MF "v[164:167],a[204:207],a[192:195],v[164:167]\n"
CV "v233,v210,v211\n"
CV "v232,v208,v209\n"
MF "v[164:167],a[200:203],a[152:155],v[164:167]\n"
"v_fma_f32 v200,v200,s54,v14\n"
"v_fma_f32 v201,v201,s54,v14\n"
MF "v[172:175],a[200:203],a[156:159],v[172:175]\n"
"buffer_store_dwordx4 v[232:235],v9,s[40:43],s75 offen sc1\n"
MF "v[172:175],a[204:207],a[196:199],v[172:175]\n"
"s_waitcnt lgkmcnt(0)\n"
"v_fma_f32 v202,v202,s54,v14\n"
"v_fma_f32 v203,v203,s54,v14\n"
MF "v[136:139],a[208:211],a[60:63],v[136:139]\n"
"s_add_u32 m0,s62,16384\n"
"s_add_u32 s82,s66,2359424\n"
TR "a[200:201],v0 offset:32776\n"
MF "v[136:139],a[212:215],a[100:103],v[136:139]\n"
"s_sub_u32 s15,s82,7864320\n"
"s_min_u32 s82,s82,s15\n"
TR "a[202:203],v0 offset:33288\n"
MF "v[144:147],a[212:215],a[104:107],v[144:147]\n"
LD "v6,s[28:31],s82 offen lds\n"
MF "v[144:147],a[208:211],a[64:67],v[144:147]\n"
TR "a[204:205],v0 offset:36872\n"
"v_fma_f32 v204,v204,s54,v14\n"
MF "v[152:155],a[208:211],a[68:71],v[152:155]\n"
TR "a[206:207],v0 offset:37384\n"
"v_fma_f32 v205,v205,s54,v14\n"
MF "v[152:155],a[212:215],a[108:111],v[152:155]\n"
"v_fma_f32 v206,v206,s54,v14\n"
"v_fma_f32 v207,v207,s54,v14\n"
MF "v[160:163],a[212:215],a[112:115],v[160:163]\n"
CV "v231,v206,v207\n"
CV "v230,v204,v205\n"
MF "v[160:163],a[208:211],a[72:75],v[160:163]\n"
CV "v229,v202,v203\n"
CV "v228,v200,v201\n"
MF "v[168:171],a[208:211],a[76:79],v[168:171]\n"
AW "a84,v228\n"
AW "a85,v229\n"
MF "v[168:171],a[212:215],a[116:119],v[168:171]\n"
LD "v11,s[28:31],s82 offen offset:1024 lds\n"
MF "v[168:171],a[216:219],a[156:159],v[168:171]\n"
TR "a[208:209],v0 offset:49160\n"
AW "a86,v230\n"
MF "v[168:171],a[220:223],a[196:199],v[168:171]\n"
TR "a[210:211],v0 offset:49672\n"
AW "a87,v231\n"
MF "v[160:163],a[220:223],a[192:195],v[160:163]\n"
"v_fma_f32 v192,v192,s54,v14\n"
"v_fma_f32 v193,v193,s54,v14\n"
MF "v[160:163],a[216:219],a[152:155],v[160:163]\n"
TR "a[212:213],v0 offset:53256\n"
"v_fma_f32 v194,v194,s54,v14\n"
MF "v[152:155],a[216:219],a[148:151],v[152:155]\n"
TR "a[214:215],v0 offset:53768\n"
"v_fma_f32 v195,v195,s54,v14\n"
MF "v[152:155],a[220:223],a[188:191],v[152:155]\n"
"v_fma_f32 v196,v196,s54,v14\n"
"v_fma_f32 v197,v197,s54,v14\n"
MF "v[144:147],a[220:223],a[184:187],v[144:147]\n"
"v_fma_f32 v198,v198,s54,v14\n"
"v_fma_f32 v199,v199,s54,v14\n"
MF "v[144:147],a[216:219],a[144:147],v[144:147]\n"
CV "v227,v198,v199\n"
CV "v226,v196,v197\n"
MF "v[136:139],a[216:219],a[140:143],v[136:139]\n"
LD "v12,s[28:31],s82 offen offset:2048 lds\n"
MF "v[136:139],a[220:223],a[180:183],v[136:139]\n"
"s_waitcnt lgkmcnt(4)\n"
CV "v225,v194,v195\n"
CV "v224,v192,v193\n"
MF "v[100:103],a[200:203],a[60:63],v[100:103]\n"
TR "a[216:217],v0 offset:32768\n"
AW "a80,v224\n"
MF "v[100:103],a[204:207],a[100:103],v[100:103]\n"
TR "a[218:219],v0 offset:33280\n"
AW "a81,v225\n"
MF "v[108:111],a[204:207],a[104:107],v[108:111]\n"
AW "a82,v226\n"
AW "a83,v227\n"
MF "v[108:111],a[200:203],a[64:67],v[108:111]\n"
TR "a[220:221],v0 offset:36864\n"
"v_fma_f32 v184,v184,s54,v14\n"
MF "v[116:119],a[200:203],a[68:71],v[116:119]\n"
TR "a[222:223],v0 offset:37376\n"
"v_fma_f32 v185,v185,s54,v14\n"
MF "v[116:119],a[204:207],a[108:111],v[116:119]\n"
"v_fma_f32 v186,v186,s54,v14\n"
"v_fma_f32 v187,v187,s54,v14\n"
MF "v[124:127],a[204:207],a[112:115],v[124:127]\n"
"v_fma_f32 v188,v188,s54,v14\n"
"v_fma_f32 v189,v189,s54,v14\n"
MF "v[124:127],a[200:203],a[72:75],v[124:127]\n"
LD "v13,s[28:31],s82 offen offset:3072 lds\n"
MF "v[132:135],a[200:203],a[76:79],v[132:135]\n"
"v_fma_f32 v190,v190,s54,v14\n"
"v_fma_f32 v191,v191,s54,v14\n"
MF "v[132:135],a[204:207],a[116:119],v[132:135]\n"
"s_waitcnt lgkmcnt(4)\n"
CV "v223,v190,v191\n"
CV "v222,v188,v189\n"
MF "v[100:103],a[208:211],a[140:143],v[100:103]\n"
TR "a[200:201],v0 offset:49152\n"
CV "v221,v186,v187\n"
MF "v[100:103],a[212:215],a[180:183],v[100:103]\n"
TR "a[202:203],v0 offset:49664\n"
CV "v220,v184,v185\n"
MF "v[108:111],a[212:215],a[184:187],v[108:111]\n"
AW "a56,v220\n"
AW "a57,v221\n"
MF "v[108:111],a[208:211],a[144:147],v[108:111]\n"
TR "a[204:205],v0 offset:53248\n"
AW "a58,v222\n"
MF "v[116:119],a[208:211],a[148:151],v[116:119]\n"
TR "a[206:207],v0 offset:53760\n"
AW "a59,v223\n"
MF "v[116:119],a[212:215],a[188:191],v[116:119]\n"
"v_fma_f32 v176,v176,s54,v14\n"
"v_fma_f32 v177,v177,s54,v14\n"
MF "v[124:127],a[212:215],a[192:195],v[124:127]\n"
"v_add_u32 v0,0xc000,v0\n"
"v_fma_f32 v178,v178,s54,v14\n"
MF "v[124:127],a[208:211],a[152:155],v[124:127]\n"
"v_add_u32 v1,0xc000,v1\n"
"v_fma_f32 v179,v179,s54,v14\n"
MF "v[132:135],a[208:211],a[156:159],v[132:135]\n"
"v_fma_f32 v180,v180,s54,v14\n"
"v_fma_f32 v181,v181,s54,v14\n"
MF "v[132:135],a[212:215],a[196:199],v[132:135]\n"
"s_waitcnt vmcnt(14) lgkmcnt(0)\n"
"s_barrier\n"
MF "v[96:99],a[216:219],a[60:63],v[96:99]\n"
"s_add_u32 m0,s62,32768\n"
"s_add_u32 s82,s66,2359552\n"
TR "a[208:209],v1 offset:16392\n"
MF "v[96:99],a[220:223],a[100:103],v[96:99]\n"
"s_sub_u32 s15,s82,7864320\n"
"s_min_u32 s82,s82,s15\n"
TR "a[210:211],v1 offset:16904\n"
MF "v[104:107],a[220:223],a[104:107],v[104:107]\n"
LD "v6,s[28:31],s82 offen lds\n"
MF "v[104:107],a[216:219],a[64:67],v[104:107]\n"
TR "a[212:213],v1 offset:20488\n"
"v_fma_f32 v182,v182,s54,v14\n"
MF "v[112:115],a[216:219],a[68:71],v[112:115]\n"
TR "a[214:215],v1 offset:21000\n"
"v_fma_f32 v183,v183,s54,v14\n"
MF "v[112:115],a[220:223],a[108:111],v[112:115]\n"
CV "v219,v182,v183\n"
CV "v218,v180,v181\n"
MF "v[120:123],a[220:223],a[112:115],v[120:123]\n"
CV "v217,v178,v179\n"
CV "v216,v176,v177\n"
MF "v[120:123],a[216:219],a[72:75],v[120:123]\n"
AW "a52,v216\n"
AW "a53,v217\n"
MF "v[128:131],a[216:219],a[76:79],v[128:131]\n"
AW "a54,v218\n"
AW "a55,v219\n"
MF "v[128:131],a[220:223],a[116:119],v[128:131]\n"
LD "v11,s[28:31],s82 offen offset:1024 lds\n"
MF "v[128:131],a[200:203],a[156:159],v[128:131]\n"
TR "a[216:217],v1 offset:32776\n"
"v_fma_f32 v168,v168,s54,v14\n"
MF "v[128:131],a[204:207],a[196:199],v[128:131]\n"
TR "a[218:219],v1 offset:33288\n"
"v_fma_f32 v169,v169,s54,v14\n"
MF "v[120:123],a[204:207],a[192:195],v[120:123]\n"
"v_fma_f32 v170,v170,s54,v14\n"
"v_fma_f32 v171,v171,s54,v14\n"
MF "v[120:123],a[200:203],a[152:155],v[120:123]\n"
TR "a[220:221],v1 offset:36872\n"
"v_fma_f32 v172,v172,s54,v14\n"
MF "v[112:115],a[200:203],a[148:151],v[112:115]\n"
TR "a[222:223],v1 offset:37384\n"
"v_fma_f32 v173,v173,s54,v14\n"
MF "v[112:115],a[204:207],a[188:191],v[112:115]\n"
"v_fma_f32 v174,v174,s54,v14\n"
"v_fma_f32 v175,v175,s54,v14\n"
MF "v[104:107],a[204:207],a[184:187],v[104:107]\n"
CV "v215,v174,v175\n"
CV "v214,v172,v173\n"
MF "v[104:107],a[200:203],a[144:147],v[104:107]\n"
CV "v213,v170,v171\n"
CV "v212,v168,v169\n"
MF "v[96:99],a[200:203],a[140:143],v[96:99]\n"
LD "v12,s[28:31],s82 offen offset:2048 lds\n"
MF "v[96:99],a[204:207],a[180:183],v[96:99]\n"
"s_waitcnt lgkmcnt(4)\n"
AW "a48,v212\n"
AW "a49,v213\n"
MF "v[60:63],a[208:211],a[60:63],v[60:63]\n"
TR "a[200:201],v1 offset:16384\n"
AW "a50,v214\n"
MF "v[60:63],a[212:215],a[100:103],v[60:63]\n"
TR "a[202:203],v1 offset:16896\n"
AW "a51,v215\n"
MF "v[68:71],a[212:215],a[104:107],v[68:71]\n"
"v_fma_f32 v160,v160,s54,v14\n"
"v_fma_f32 v161,v161,s54,v14\n"
MF "v[68:71],a[208:211],a[64:67],v[68:71]\n"
TR "a[204:205],v1 offset:20480\n"
"v_fma_f32 v162,v162,s54,v14\n"
MF "v[76:79],a[208:211],a[68:71],v[76:79]\n"
TR "a[206:207],v1 offset:20992\n"
"v_fma_f32 v163,v163,s54,v14\n"
MF "v[76:79],a[212:215],a[108:111],v[76:79]\n"
"v_fma_f32 v164,v164,s54,v14\n"
"v_fma_f32 v165,v165,s54,v14\n"
MF "v[84:87],a[212:215],a[112:115],v[84:87]\n"
"v_fma_f32 v166,v166,s54,v14\n"
"v_fma_f32 v167,v167,s54,v14\n"
MF "v[84:87],a[208:211],a[72:75],v[84:87]\n"
LD "v13,s[28:31],s82 offen offset:3072 lds\n"
MF "v[92:95],a[208:211],a[76:79],v[92:95]\n"
CV "v211,v166,v167\n"
CV "v210,v164,v165\n"
MF "v[92:95],a[212:215],a[116:119],v[92:95]\n"
"s_waitcnt lgkmcnt(4)\n"
CV "v209,v162,v163\n"
CV "v208,v160,v161\n"
MF "v[60:63],a[216:219],a[140:143],v[60:63]\n"
TR "a[208:209],v1 offset:32768\n"
AW "a44,v208\n"
MF "v[60:63],a[220:223],a[180:183],v[60:63]\n"
TR "a[210:211],v1 offset:33280\n"
AW "a45,v209\n"
MF "v[68:71],a[220:223],a[184:187],v[68:71]\n"
AW "a46,v210\n"
AW "a47,v211\n"
MF "v[68:71],a[216:219],a[144:147],v[68:71]\n"
TR "a[212:213],v1 offset:36864\n"
"v_fma_f32 v152,v152,s54,v14\n"
MF "v[76:79],a[216:219],a[148:151],v[76:79]\n"
TR "a[214:215],v1 offset:37376\n"
"v_fma_f32 v153,v153,s54,v14\n"
MF "v[76:79],a[220:223],a[188:191],v[76:79]\n"
"v_fma_f32 v154,v154,s54,v14\n"
"v_fma_f32 v155,v155,s54,v14\n"
MF "v[84:87],a[220:223],a[192:195],v[84:87]\n"
"v_fma_f32 v156,v156,s54,v14\n"
"v_fma_f32 v157,v157,s54,v14\n"
MF "v[84:87],a[216:219],a[152:155],v[84:87]\n"
"v_fma_f32 v158,v158,s54,v14\n"
"v_fma_f32 v159,v159,s54,v14\n"
MF "v[92:95],a[216:219],a[156:159],v[92:95]\n"
CV "v207,v158,v159\n"
CV "v206,v156,v157\n"
MF "v[92:95],a[220:223],a[196:199],v[92:95]\n"
"s_waitcnt lgkmcnt(0)\n"
CV "v205,v154,v155\n"
CV "v204,v152,v153\n"
MF "v[56:59],a[200:203],a[60:63],v[56:59]\n"
TR "a[216:217],v0 offset:16392\n"
AW "a40,v204\n"
MF "v[56:59],a[204:207],a[100:103],v[56:59]\n"
TR "a[218:219],v0 offset:16904\n"
AW "a41,v205\n"
MF "v[64:67],a[204:207],a[104:107],v[64:67]\n"
AW "a42,v206\n"
AW "a43,v207\n"
MF "v[64:67],a[200:203],a[64:67],v[64:67]\n"
TR "a[220:221],v0 offset:20488\n"
"v_fma_f32 v144,v144,s54,v14\n"
MF "v[72:75],a[200:203],a[68:71],v[72:75]\n"
TR "a[222:223],v0 offset:21000\n"
"v_fma_f32 v145,v145,s54,v14\n"
MF "v[72:75],a[204:207],a[108:111],v[72:75]\n"
"v_fma_f32 v146,v146,s54,v14\n"
"v_fma_f32 v147,v147,s54,v14\n"
MF "v[80:83],a[204:207],a[112:115],v[80:83]\n"
"v_fma_f32 v148,v148,s54,v14\n"
"v_fma_f32 v149,v149,s54,v14\n"
MF "v[80:83],a[200:203],a[72:75],v[80:83]\n"
"v_fma_f32 v150,v150,s54,v14\n"
"v_fma_f32 v151,v151,s54,v14\n"
MF "v[88:91],a[200:203],a[76:79],v[88:91]\n"
CV "v203,v150,v151\n"
CV "v202,v148,v149\n"
MF "v[88:91],a[204:207],a[116:119],v[88:91]\n"
CV "v201,v146,v147\n"
CV "v200,v144,v145\n"
MF "v[88:91],a[208:211],a[156:159],v[88:91]\n"
TR "a[200:201],v0 offset:32776\n"
AW "a16,v200\n"
MF "v[88:91],a[212:215],a[196:199],v[88:91]\n"
TR "a[202:203],v0 offset:33288\n"
AW "a17,v201\n"
MF "v[80:83],a[212:215],a[192:195],v[80:83]\n"
AW "a18,v202\n"
AW "a19,v203\n"
MF "v[80:83],a[208:211],a[152:155],v[80:83]\n"
TR "a[204:205],v0 offset:36872\n"
"v_fma_f32 v136,v136,s54,v14\n"
MF "v[72:75],a[208:211],a[148:151],v[72:75]\n"
TR "a[206:207],v0 offset:37384\n"
"v_fma_f32 v137,v137,s54,v14\n"
MF "v[72:75],a[212:215],a[188:191],v[72:75]\n"
"v_fma_f32 v138,v138,s54,v14\n"
"v_fma_f32 v139,v139,s54,v14\n"
MF "v[64:67],a[212:215],a[184:187],v[64:67]\n"
"v_fma_f32 v140,v140,s54,v14\n"
"v_fma_f32 v141,v141,s54,v14\n"
MF "v[64:67],a[208:211],a[144:147],v[64:67]\n"
"v_fma_f32 v142,v142,s54,v14\n"
"v_fma_f32 v143,v143,s54,v14\n"
MF "v[56:59],a[208:211],a[140:143],v[56:59]\n"
CV "v199,v142,v143\n"
CV "v198,v140,v141\n"
MF "v[56:59],a[212:215],a[180:183],v[56:59]\n"
"s_waitcnt lgkmcnt(4)\n"
CV "v197,v138,v139\n"
CV "v196,v136,v137\n"
MF "v[20:23],a[216:219],a[60:63],v[20:23]\n"
TR "a[208:209],v0 offset:16384\n"
AW "a12,v196\n"
MF "v[20:23],a[220:223],a[100:103],v[20:23]\n"
TR "a[210:211],v0 offset:16896\n"
AW "a13,v197\n"
MF "v[28:31],a[220:223],a[104:107],v[28:31]\n"
AW "a14,v198\n"
AW "a15,v199\n"
MF "v[28:31],a[216:219],a[64:67],v[28:31]\n"
TR "a[212:213],v0 offset:20480\n"
"v_fma_f32 v128,v128,s54,v14\n"
MF "v[36:39],a[216:219],a[68:71],v[36:39]\n"
TR "a[214:215],v0 offset:20992\n"
"v_fma_f32 v129,v129,s54,v14\n"
MF "v[36:39],a[220:223],a[108:111],v[36:39]\n"
"v_fma_f32 v130,v130,s54,v14\n"
"v_fma_f32 v131,v131,s54,v14\n"
MF "v[44:47],a[220:223],a[112:115],v[44:47]\n"
"v_fma_f32 v132,v132,s54,v14\n"
"v_fma_f32 v133,v133,s54,v14\n"
MF "v[44:47],a[216:219],a[72:75],v[44:47]\n"
"v_fma_f32 v134,v134,s54,v14\n"
"v_fma_f32 v135,v135,s54,v14\n"
MF "v[52:55],a[216:219],a[76:79],v[52:55]\n"
CV "v195,v134,v135\n"
CV "v194,v132,v133\n"
MF "v[52:55],a[220:223],a[116:119],v[52:55]\n"
"s_waitcnt lgkmcnt(4)\n"
CV "v193,v130,v131\n"
CV "v192,v128,v129\n"
MF "v[20:23],a[200:203],a[140:143],v[20:23]\n"
TR "a[216:217],v0 offset:32768\n"
AW "a8,v192\n"
MF "v[20:23],a[204:207],a[180:183],v[20:23]\n"
TR "a[218:219],v0 offset:33280\n"
AW "a9,v193\n"
MF "v[28:31],a[204:207],a[184:187],v[28:31]\n"
AW "a10,v194\n"
AW "a11,v195\n"
MF "v[28:31],a[200:203],a[144:147],v[28:31]\n"
TR "a[220:221],v0 offset:36864\n"
"v_fma_f32 v120,v120,s54,v14\n"
MF "v[36:39],a[200:203],a[148:151],v[36:39]\n"
TR "a[222:223],v0 offset:37376\n"
"v_fma_f32 v121,v121,s54,v14\n"
MF "v[36:39],a[204:207],a[188:191],v[36:39]\n"
"v_fma_f32 v122,v122,s54,v14\n"
"v_fma_f32 v123,v123,s54,v14\n"
MF "v[44:47],a[204:207],a[192:195],v[44:47]\n"
"v_fma_f32 v124,v124,s54,v14\n"
"v_fma_f32 v125,v125,s54,v14\n"
MF "v[44:47],a[200:203],a[152:155],v[44:47]\n"
"v_fma_f32 v126,v126,s54,v14\n"
"v_fma_f32 v127,v127,s54,v14\n"
MF "v[52:55],a[200:203],a[156:159],v[52:55]\n"
".Lpg31c:\n"
"s_cbranch_execz .Lpg2a\n"
CV "v191,v126,v127\n"
CV "v190,v124,v125\n"
MF "v[52:55],a[204:207],a[196:199],v[52:55]\n"
"s_waitcnt lgkmcnt(0)\n"
"s_barrier\n"
MF "v[16:19],a[208:211],a[60:63],v[16:19]\n"
CV "v189,v122,v123\n"
CV "v188,v120,v121\n"
MF "v[16:19],a[212:215],a[100:103],v[16:19]\n"
AW "a4,v188\n"
AW "a5,v189\n"
MF "v[24:27],a[212:215],a[104:107],v[24:27]\n"
AW "a6,v190\n"
AW "a7,v191\n"
MF "v[24:27],a[208:211],a[64:67],v[24:27]\n"
"v_fma_f32 v112,v112,s54,v14\n"
"v_fma_f32 v113,v113,s54,v14\n"
MF "v[32:35],a[208:211],a[68:71],v[32:35]\n"
"v_fma_f32 v114,v114,s54,v14\n"
"v_fma_f32 v115,v115,s54,v14\n"
MF "v[32:35],a[212:215],a[108:111],v[32:35]\n"
"v_fma_f32 v116,v116,s54,v14\n"
"v_fma_f32 v117,v117,s54,v14\n"
MF "v[40:43],a[212:215],a[112:115],v[40:43]\n"
"v_fma_f32 v118,v118,s54,v14\n"
"v_fma_f32 v119,v119,s54,v14\n"
MF "v[40:43],a[208:211],a[72:75],v[40:43]\n"
CV "v187,v118,v119\n"
CV "v186,v116,v117\n"
MF "v[48:51],a[208:211],a[76:79],v[48:51]\n"
CV "v185,v114,v115\n"
CV "v184,v112,v113\n"
MF "v[48:51],a[212:215],a[116:119],v[48:51]\n"
AW "a0,v184\n"
AW "a1,v185\n"
MF "v[48:51],a[216:219],a[156:159],v[48:51]\n"
AW "a2,v186\n"
AW "a3,v187\n"
MF "v[48:51],a[220:223],a[196:199],v[48:51]\n"
"v_fma_f32 v104,v104,s54,v14\n"
"v_fma_f32 v105,v105,s54,v14\n"
MF "v[40:43],a[220:223],a[192:195],v[40:43]\n"
"v_fma_f32 v106,v106,s54,v14\n"
"v_fma_f32 v107,v107,s54,v14\n"
MF "v[40:43],a[216:219],a[152:155],v[40:43]\n"
"v_fma_f32 v108,v108,s54,v14\n"
"v_fma_f32 v109,v109,s54,v14\n"
MF "v[32:35],a[216:219],a[148:151],v[32:35]\n"
"v_fma_f32 v110,v110,s54,v14\n"
"v_fma_f32 v111,v111,s54,v14\n"
MF "v[32:35],a[220:223],a[188:191],v[32:35]\n"
CV "v183,v110,v111\n"
CV "v182,v108,v109\n"
MF "v[24:27],a[220:223],a[184:187],v[24:27]\n"
CV "v181,v106,v107\n"
CV "v180,v104,v105\n"
MF "v[24:27],a[216:219],a[144:147],v[24:27]\n"
AW "a36,v180\n"
AW "a37,v181\n"
MF "v[16:19],a[216:219],a[140:143],v[16:19]\n"
AW "a38,v182\n"
AW "a39,v183\n"
MF "v[16:19],a[220:223],a[180:183],v[16:19]\n"
"s_mov_b32 s57,1\n"
"s_mov_b32 s69,99\n"
AR "v0,a240\n"
AR "v2,a241\n"
AR "v4,a242\n"
AR "v6,a243\n"
"v_xor_b32 v1,64,v0\n"
"v_xor_b32 v3,64,v2\n"
"v_xor_b32 v5,64,v4\n"
"v_add_u32 v11,97280,v6\n"
"v_add_u32 v12,194560,v6\n"
"v_add_u32 v13,291840,v6\n"
"s_mov_b64 vcc,-1\n"
"s_waitcnt vmcnt(10) lgkmcnt(0)\n"
"s_barrier\n"
"s_nop 0\n"
TR "a[200:201],v0\n"
TR "a[202:203],v0 offset:512\n"
TR "a[204:205],v0 offset:4096\n"
TR "a[206:207],v0 offset:4608\n"
"ds_read_b128 a[60:63],v2\n"
"ds_read_b128 a[100:103],v3\n"
"ds_read_b128 a[64:67],v2 offset:2048\n"
"ds_read_b128 a[104:107],v3 offset:2048\n"
"ds_read_b128 a[68:71],v2 offset:4096\n"
"ds_read_b128 a[108:111],v3 offset:4096\n"
"ds_read_b128 a[72:75],v2 offset:6144\n"
"ds_read_b128 a[112:115],v3 offset:6144\n"
"ds_read_b128 a[76:79],v2 offset:8192\n"
"ds_read_b128 a[116:119],v3 offset:8192\n"
TR "a[208:209],v0 offset:8\n"
TR "a[210:211],v0 offset:520\n"
TR "a[212:213],v0 offset:4104\n"
TR "a[214:215],v0 offset:4616\n"
"v_pk_fma_f32 v[96:97],v[96:97],s[54:55],v[14:15] op_sel_hi:[1,1,0]\n"
"v_pk_fma_f32 v[98:99],v[98:99],s[54:55],v[14:15] op_sel_hi:[1,1,0]\n"
"v_pk_fma_f32 v[100:101],v[100:101],s[54:55],v[14:15] op_sel_hi:[1,1,0]\n"
"v_pk_fma_f32 v[102:103],v[102:103],s[54:55],v[14:15] op_sel_hi:[1,1,0]\n"
CV "v179,v102,v103\n"
CV "v178,v100,v101\n"
CV "v177,v98,v99\n"
CV "v176,v96,v97\n"
AW "a32,v176\n"
AW "a33,v177\n"
AW "a34,v178\n"
AW "a35,v179\n"
"v_pk_fma_f32 v[88:89],v[88:89],s[54:55],v[14:15] op_sel_hi:[1,1,0]\n"
"v_pk_fma_f32 v[90:91],v[90:91],s[54:55],v[14:15] op_sel_hi:[1,1,0]\n"
"v_pk_fma_f32 v[92:93],v[92:93],s[54:55],v[14:15] op_sel_hi:[1,1,0]\n"
"v_pk_fma_f32 v[94:95],v[94:95],s[54:55],v[14:15] op_sel_hi:[1,1,0]\n"
CV "v175,v94,v95\n"
CV "v174,v92,v93\n"
CV "v173,v90,v91\n"
CV "v172,v88,v89\n"
AW "a28,v172\n"
AW "a29,v173\n"
AW "a30,v174\n"
AW "a31,v175\n"
"v_pk_fma_f32 v[80:81],v[80:81],s[54:55],v[14:15] op_sel_hi:[1,1,0]\n"
"v_pk_fma_f32 v[82:83],v[82:83],s[54:55],v[14:15] op_sel_hi:[1,1,0]\n"
"v_pk_fma_f32 v[84:85],v[84:85],s[54:55],v[14:15] op_sel_hi:[1,1,0]\n"
"v_pk_fma_f32 v[86:87],v[86:87],s[54:55],v[14:15] op_sel_hi:[1,1,0]\n"
CV "v171,v86,v87\n"
CV "v170,v84,v85\n"
CV "v169,v82,v83\n"
CV "v168,v80,v81\n"
AW "a24,v168\n"
AW "a25,v169\n"
AW "a26,v170\n"
AW "a27,v171\n"
"v_pk_fma_f32 v[72:73],v[72:73],s[54:55],v[14:15] op_sel_hi:[1,1,0]\n"
"v_pk_fma_f32 v[74:75],v[74:75],s[54:55],v[14:15] op_sel_hi:[1,1,0]\n"
"v_pk_fma_f32 v[76:77],v[76:77],s[54:55],v[14:15] op_sel_hi:[1,1,0]\n"
"v_pk_fma_f32 v[78:79],v[78:79],s[54:55],v[14:15] op_sel_hi:[1,1,0]\n"
CV "v167,v78,v79\n"
CV "v166,v76,v77\n"
CV "v165,v74,v75\n"
CV "v164,v72,v73\n"
AW "a20,v164\n"
AW "a21,v165\n"
AW "a22,v166\n"
AW "a23,v167\n"
"v_pk_fma_f32 v[64:65],v[64:65],s[54:55],v[14:15] op_sel_hi:[1,1,0]\n"
"v_pk_fma_f32 v[66:67],v[66:67],s[54:55],v[14:15] op_sel_hi:[1,1,0]\n"
"v_pk_fma_f32 v[68:69],v[68:69],s[54:55],v[14:15] op_sel_hi:[1,1,0]\n"
"v_pk_fma_f32 v[70:71],v[70:71],s[54:55],v[14:15] op_sel_hi:[1,1,0]\n"
CV "v163,v70,v71\n"
CV "v162,v68,v69\n"
CV "v161,v66,v67\n"
CV "v160,v64,v65\n"
AW "a176,v160\n"
AW "a177,v161\n"
AW "a178,v162\n"
AW "a179,v163\n"
"v_pk_fma_f32 v[56:57],v[56:57],s[54:55],v[14:15] op_sel_hi:[1,1,0]\n"
"v_pk_fma_f32 v[58:59],v[58:59],s[54:55],v[14:15] op_sel_hi:[1,1,0]\n"
"v_pk_fma_f32 v[60:61],v[60:61],s[54:55],v[14:15] op_sel_hi:[1,1,0]\n"
"v_pk_fma_f32 v[62:63],v[62:63],s[54:55],v[14:15] op_sel_hi:[1,1,0]\n"
CV "v159,v62,v63\n"
CV "v158,v60,v61\n"
CV "v157,v58,v59\n"
CV "v156,v56,v57\n"
AW "a172,v156\n"
AW "a173,v157\n"
AW "a174,v158\n"
AW "a175,v159\n"
"v_pk_mul_f32 v[48:49],v[48:49],s[54:55]\n"
"v_pk_mul_f32 v[50:51],v[50:51],s[54:55]\n"
"v_pk_mul_f32 v[52:53],v[52:53],s[54:55]\n"
"v_pk_mul_f32 v[54:55],v[54:55],s[54:55]\n"
CV "v155,v54,v55\n"
CV "v154,v52,v53\n"
CV "v153,v50,v51\n"
CV "v152,v48,v49\n"
AW "a168,v152\n"
AW "a169,v153\n"
AW "a170,v154\n"
AW "a171,v155\n"
"v_pk_mul_f32 v[40:41],v[40:41],s[54:55]\n"
"v_pk_mul_f32 v[42:43],v[42:43],s[54:55]\n"
"v_pk_mul_f32 v[44:45],v[44:45],s[54:55]\n"
"v_pk_mul_f32 v[46:47],v[46:47],s[54:55]\n"
CV "v151,v46,v47\n"
CV "v150,v44,v45\n"
CV "v149,v42,v43\n"
CV "v148,v40,v41\n"
AW "a120,v148\n"
AW "a121,v149\n"
AW "a122,v150\n"
AW "a123,v151\n"
"v_pk_mul_f32 v[32:33],v[32:33],s[54:55]\n"
"v_pk_mul_f32 v[34:35],v[34:35],s[54:55]\n"
"v_pk_mul_f32 v[36:37],v[36:37],s[54:55]\n"
"v_pk_mul_f32 v[38:39],v[38:39],s[54:55]\n"
CV "v147,v38,v39\n"
CV "v146,v36,v37\n"
CV "v145,v34,v35\n"
CV "v144,v32,v33\n"
AW "a96,v144\n"
AW "a97,v145\n"
AW "a98,v146\n"
AW "a99,v147\n"
"v_pk_mul_f32 v[24:25],v[24:25],s[54:55]\n"
"v_pk_mul_f32 v[26:27],v[26:27],s[54:55]\n"
"v_pk_mul_f32 v[28:29],v[28:29],s[54:55]\n"
"v_pk_mul_f32 v[30:31],v[30:31],s[54:55]\n"
CV "v143,v30,v31\n"
CV "v142,v28,v29\n"
CV "v141,v26,v27\n"
CV "v140,v24,v25\n"
AW "a92,v140\n"
AW "a93,v141\n"
AW "a94,v142\n"
AW "a95,v143\n"
"v_pk_mul_f32 v[16:17],v[16:17],s[54:55]\n"
"v_pk_mul_f32 v[18:19],v[18:19],s[54:55]\n"
"v_pk_mul_f32 v[20:21],v[20:21],s[54:55]\n"
"v_pk_mul_f32 v[22:23],v[22:23],s[54:55]\n"
CV "v139,v22,v23\n"
CV "v138,v20,v21\n"
CV "v137,v18,v19\n"
CV "v136,v16,v17\n"
AW "a88,v136\n"
AW "a89,v137\n"
AW "a90,v138\n"
AW "a91,v139\n"
"s_waitcnt lgkmcnt(0)\n"
"s_branch .Lq0\n"
".Lpgpsl_b:\n"
"s_cbranch_execz .Lpg3a\n"
".Lpgpsl_d:\n"
"s_cbranch_execz .Lisr2\n"
".Lpsl:\n"
"s_sleep 1\n"
".Lpsub:\n"
"s_memrealtime s[100:101]\n"
"s_waitcnt lgkmcnt(0)\n"
"s_sub_u32 s101,s21,s100\n"
"s_cmp_gt_i32 s101,0\n"
"s_cbranch_scc1 .Lpsl\n"
"s_add_u32 s21,s100,s97\n"
"s_setpc_b64 s[52:53]\n"
".Lstub_6:\n"
"v_cmp_le_f32 vcc,0x47800000,v10\n"
"s_cmp_eq_u32 s61,1\n"
"s_cbranch_scc0 .Lhook_6\n"
"s_sub_u32 s58,s71,s70\n"
"s_add_u32 s72,s72,s58\n"
"s_add_u32 s16,s16,s58\n"
"s_add_u32 s17,s17,s58\n"
"s_cmp_eq_u32 s57,0\n"
"s_cselect_b32 s34,0,s34\n"
"s_cselect_b32 s64,163840,s64\n"
"s_mov_b64 exec,0\n"
"s_bitcmp1_b32 s23,12\n"
"s_cbranch_scc1 .Lisy3\n"
"s_cmp_eq_u32 s56,0\n"
"s_cbranch_scc1 .Lpgq0_f\n"
"s_cmp_eq_u32 s56,1\n"
"s_cbranch_scc1 .Lpg4f\n"
"s_cmp_eq_u32 s56,2\n"
"s_cbranch_scc1 .Lpg5f\n"
"s_branch .Lpg6f\n"
".Lisy3:\n"
"s_cmp_eq_u32 s56,0\n"
"s_cbranch_scc1 .Lpgep_g\n"
"s_cmp_eq_u32 s56,1\n"
"s_cbranch_scc1 .Lpg13g\n"
"s_cmp_eq_u32 s56,2\n"
"s_cbranch_scc1 .Lpg14g\n"
"s_branch .Lpgt0e_g\n"
".Lisr3:\n"
"s_mov_b64 exec,-1\n"
".Lisx3:\n"
".Lhook_6:\n"
"s_cmp_eq_u32 s77,0\n"
"s_cbranch_scc0 .Lnop_6\n"
"s_call_b64 s[52:53],.Lpsub\n"
".Lnop_6:\n"
"s_cbranch_vccz .Lback_6\n"
"s_call_b64 s[52:53],.Lslow_1\n"
"s_branch .Lback_6\n"
".Lstub_7:\n"
"v_cmp_le_f32 vcc,0x47800000,v10\n"
"s_cmp_eq_u32 s61,1\n"
"s_cbranch_scc0 .Lhook_7\n"
"s_sub_u32 s58,s71,s70\n"
"s_add_u32 s73,s73,s58\n"
"s_add_u32 s18,s18,s58\n"
"s_add_u32 s19,s19,s58\n"
"s_cmp_eq_u32 s57,0\n"
"s_cselect_b32 s38,0,s38\n"
"s_cselect_b32 s65,163840,s65\n"
".Lhook_7:\n"
"s_cmp_eq_u32 s77,0\n"
"s_cbranch_scc0 .Lnop_7\n"
"s_call_b64 s[52:53],.Lpsub\n"
"s_cmp_eq_u32 s61,4\n"
"s_cselect_b32 s77,0x47800000,s77\n"
"s_cselect_b32 s95,0x47800000,s95\n"
".Lnop_7:\n"
"s_cbranch_vccz .Lback_7\n"
"s_call_b64 s[52:53],.Lslow_0\n"
"s_branch .Lback_7\n"
".Lstub_8:\n"
"s_call_b64 s[52:53],.Lslow_1\n"
"s_branch .Lback_8\n"
".Lstub_11:\n"
"v_cmp_le_f32 vcc,0x47800000,v10\n"
"s_cmp_eq_u32 s77,0\n"
"s_cbranch_scc0 .Lnop_11\n"
"s_call_b64 s[52:53],.Lpsub\n"
".Lnop_11:\n"
"s_cbranch_vccz .Lback_11\n"
"s_call_b64 s[52:53],.Lslow_1\n"
"s_branch .Lback_11\n"
".Lstub_12:\n"
"v_cmp_le_f32 vcc,0x47800000,v10\n"
"s_cmp_eq_u32 s77,0\n"
"s_cbranch_scc0 .Lnop_12\n"
"s_call_b64 s[52:53],.Lpsub\n"
".Lnop_12:\n"
"s_cbranch_vccz .Lback_12\n"
"s_call_b64 s[52:53],.Lslow_0\n"
"s_branch .Lback_12\n"
".Lstub_13:\n"
"v_cmp_le_f32 vcc,0x47800000,v10\n"
"s_cmp_eq_u32 s61,1\n"
"s_cbranch_scc0 .Lhook_13\n"
"s_mov_b64 exec,0\n"
"s_cmp_eq_u32 s56,0\n"
"s_cbranch_scc1 .Lpg22h\n"
"s_cmp_eq_u32 s56,1\n"
"s_cbranch_scc1 .Lpg21h\n"
"s_cmp_eq_u32 s56,2\n"
"s_cbranch_scc1 .Lpg20h\n"
"s_branch .Lpg19h\n"
".Lisr4:\n"
"s_mov_b64 exec,-1\n"
".Lisx4:\n"
"s_mov_b32 s97,0\n"
".Lhook_13:\n"
"s_cmp_eq_u32 s77,0\n"
"s_cbranch_scc0 .Lnop_13\n"
"s_call_b64 s[52:53],.Lpsub\n"
".Lnop_13:\n"
"s_cbranch_vccz .Lback_13\n"
"s_call_b64 s[52:53],.Lslow_1\n"
"s_branch .Lback_13\n"
".Lstub_14:\n"
"v_cmp_le_f32 vcc,0x47800000,v10\n"
"s_cmp_eq_u32 s77,0\n"
"s_cbranch_scc0 .Lnop_14\n"
"s_call_b64 s[52:53],.Lpsub\n"
".Lnop_14:\n"
"s_cbranch_vccz .Lback_14\n"
"s_call_b64 s[52:53],.Lslow_0\n"
"s_branch .Lback_14\n"
".Lstub_15:\n"
"v_cmp_le_f32 vcc,0x47800000,v10\n"
"s_cmp_eq_u32 s61,1\n"
"s_cbranch_scc0 .Lhook_15\n"
"s_mov_b32 s34,0\n"
"s_mov_b32 s64,163840\n"
".Lhook_15:\n"
"s_cmp_eq_u32 s77,0\n"
"s_cbranch_scc0 .Lnop_15\n"
"s_call_b64 s[52:53],.Lpsub\n"
".Lnop_15:\n"
"s_cbranch_vccz .Lback_15\n"
"s_call_b64 s[52:53],.Lslow_1\n"
"s_branch .Lback_15\n"
".Lstub_16:\n"
"v_cmp_le_f32 vcc,0x47800000,v10\n"
"s_cmp_eq_u32 s77,0\n"
"s_cbranch_scc0 .Lnop_16\n"
"s_call_b64 s[52:53],.Lpsub\n"
".Lnop_16:\n"
"s_cbranch_vccz .Lback_16\n"
"s_call_b64 s[52:53],.Lslow_0\n"
"s_branch .Lback_16\n"
".Lstub_17:\n"
"s_call_b64 s[52:53],.Lslow_1\n"
"s_branch .Lback_17\n"
".Lslow_0:\n"
"s_nop 7\n"
"s_nop 7\n"
"v_and_b32 v10,240,v9\n"
"v_mul_u32_u24 v10,24,v10\n"
"v_lshrrev_b32_e64 v11,4,v9\n"
"v_and_or_b32 v10,v11,48,v10\n"
"s_mov_b32 s59,s34\n"
"s_mov_b32 s34,s20\n"
"s_add_u32 s58,s74,0\n"
"s_nop 3\n"
LD "v[52:55],v10,s[32:35],s58 offen\n"
"s_waitcnt vmcnt(0)\n"
MF "v[72:75],v[52:55],v[136:139],v[32:35]\n"
MF "v[76:79],v[52:55],v[140:143],v[36:39]\n"
MF "v[80:83],v[52:55],v[144:147],v[40:43]\n"
MF "v[84:87],v[52:55],v[148:151],v[44:47]\n"
MF "v[88:91],v[52:55],v[152:155],v[48:51]\n"
"s_add_u32 s58,s74,0\n"
"s_nop 3\n"
LD "v[52:55],v10,s[32:35],s58 offen offset:64\n"
"s_waitcnt vmcnt(0)\n"
MF "v[72:75],v[52:55],v[156:159],v[72:75]\n"
MF "v[76:79],v[52:55],v[160:163],v[76:79]\n"
MF "v[80:83],v[52:55],v[164:167],v[80:83]\n"
MF "v[84:87],v[52:55],v[168:171],v[84:87]\n"
MF "v[88:91],v[52:55],v[172:175],v[88:91]\n"
"s_add_u32 s58,s74,0\n"
"s_nop 3\n"
LD "v[52:55],v10,s[32:35],s58 offen offset:128\n"
"s_waitcnt vmcnt(0)\n"
MF "v[72:75],v[52:55],v[176:179],v[72:75]\n"
MF "v[76:79],v[52:55],v[180:183],v[76:79]\n"
MF "v[80:83],v[52:55],v[184:187],v[80:83]\n"
MF "v[84:87],v[52:55],v[188:191],v[84:87]\n"
MF "v[88:91],v[52:55],v[192:195],v[88:91]\n"
"s_add_u32 s58,s74,0\n"
"s_nop 3\n"
LD "v[52:55],v10,s[32:35],s58 offen offset:192\n"
"s_waitcnt vmcnt(0)\n"
MF "v[72:75],v[52:55],v[196:199],v[72:75]\n"
MF "v[76:79],v[52:55],v[200:203],v[76:79]\n"
MF "v[80:83],v[52:55],v[204:207],v[80:83]\n"
MF "v[84:87],v[52:55],v[208:211],v[84:87]\n"
MF "v[88:91],v[52:55],v[212:215],v[88:91]\n"
"s_add_u32 s58,s74,0\n"
"s_nop 3\n"
LD "v[52:55],v10,s[32:35],s58 offen offset:256\n"
"s_waitcnt vmcnt(0)\n"
MF "v[72:75],v[52:55],v[216:219],v[72:75]\n"
MF "v[76:79],v[52:55],v[220:223],v[76:79]\n"
MF "v[80:83],v[52:55],v[224:227],v[80:83]\n"
MF "v[84:87],v[52:55],v[228:231],v[84:87]\n"
MF "v[88:91],v[52:55],v[232:235],v[88:91]\n"
"s_add_u32 s58,s74,0\n"
"s_nop 3\n"
LD "v[52:55],v10,s[32:35],s58 offen offset:320\n"
"s_waitcnt vmcnt(0)\n"
MF "v[72:75],v[52:55],v[236:239],v[72:75]\n"
MF "v[76:79],v[52:55],v[240:243],v[76:79]\n"
MF "v[80:83],v[52:55],v[244:247],v[80:83]\n"
MF "v[84:87],v[52:55],v[248:251],v[84:87]\n"
MF "v[88:91],v[52:55],v[252:255],v[88:91]\n"
"s_add_u32 s58,s74,6144\n"
"s_nop 3\n"
LD "v[52:55],v10,s[32:35],s58 offen\n"
"s_waitcnt vmcnt(0)\n"
MF "v[112:115],v[52:55],v[136:139],v[32:35]\n"
MF "v[116:119],v[52:55],v[140:143],v[36:39]\n"
MF "v[120:123],v[52:55],v[144:147],v[40:43]\n"
MF "v[124:127],v[52:55],v[148:151],v[44:47]\n"
MF "v[128:131],v[52:55],v[152:155],v[48:51]\n"
"s_add_u32 s58,s74,6144\n"
"s_nop 3\n"
LD "v[52:55],v10,s[32:35],s58 offen offset:64\n"
"s_waitcnt vmcnt(0)\n"
MF "v[112:115],v[52:55],v[156:159],v[112:115]\n"
MF "v[116:119],v[52:55],v[160:163],v[116:119]\n"
MF "v[120:123],v[52:55],v[164:167],v[120:123]\n"
MF "v[124:127],v[52:55],v[168:171],v[124:127]\n"
MF "v[128:131],v[52:55],v[172:175],v[128:131]\n"
"s_add_u32 s58,s74,6144\n"
"s_nop 3\n"
LD "v[52:55],v10,s[32:35],s58 offen offset:128\n"
"s_waitcnt vmcnt(0)\n"
MF "v[112:115],v[52:55],v[176:179],v[112:115]\n"
MF "v[116:119],v[52:55],v[180:183],v[116:119]\n"
MF "v[120:123],v[52:55],v[184:187],v[120:123]\n"
MF "v[124:127],v[52:55],v[188:191],v[124:127]\n"
MF "v[128:131],v[52:55],v[192:195],v[128:131]\n"
"s_add_u32 s58,s74,6144\n"
"s_nop 3\n"
LD "v[52:55],v10,s[32:35],s58 offen offset:192\n"
"s_waitcnt vmcnt(0)\n"
MF "v[112:115],v[52:55],v[196:199],v[112:115]\n"
MF "v[116:119],v[52:55],v[200:203],v[116:119]\n"
MF "v[120:123],v[52:55],v[204:207],v[120:123]\n"
MF "v[124:127],v[52:55],v[208:211],v[124:127]\n"
MF "v[128:131],v[52:55],v[212:215],v[128:131]\n"
"s_add_u32 s58,s74,6144\n"
"s_nop 3\n"
LD "v[52:55],v10,s[32:35],s58 offen offset:256\n"
"s_waitcnt vmcnt(0)\n"
MF "v[112:115],v[52:55],v[216:219],v[112:115]\n"
MF "v[116:119],v[52:55],v[220:223],v[116:119]\n"
MF "v[120:123],v[52:55],v[224:227],v[120:123]\n"
MF "v[124:127],v[52:55],v[228:231],v[124:127]\n"
MF "v[128:131],v[52:55],v[232:235],v[128:131]\n"
"s_add_u32 s58,s74,6144\n"
"s_nop 3\n"
LD "v[52:55],v10,s[32:35],s58 offen offset:320\n"
"s_waitcnt vmcnt(0)\n"
MF "v[112:115],v[52:55],v[236:239],v[112:115]\n"
MF "v[116:119],v[52:55],v[240:243],v[116:119]\n"
MF "v[120:123],v[52:55],v[244:247],v[120:123]\n"
MF "v[124:127],v[52:55],v[248:251],v[124:127]\n"
MF "v[128:131],v[52:55],v[252:255],v[128:131]\n"
"s_nop 7\n"
"s_mov_b32 s34,s59\n"
"v_max3_f32 v10,v72,v73,v74\n"
"s_nop 0\n"
"v_max3_f32 v11,v75,v112,v113\n"
"v_max3_f32 v10,v10,v114,v115\n"
"v_max_f32 v10,v10,v11\n"
"v_mov_b32 v11,v10\n"
"s_nop 1\n"
"v_permlane16_swap_b32 v10,v11\n"
"v_max_f32 v10,v10,v11\n"
"v_mov_b32 v11,v10\n"
"s_nop 1\n"
"v_permlane32_swap_b32 v10,v11\n"
"v_max_f32 v10,v10,v11\n"
"v_add_f32 v10,0x41300000,v10\n"
"v_max_f32 v10,0,v10\n"
"v_exp_f32 v11,-v10\n"
AR "v12,a0\n"
"v_subrev_f32 v12,s13,v12\n"
"v_fma_f32 v12,v12,v11,s13\n"
AW "a0,v12\n"
AR "v12,a20\n"
"v_subrev_f32 v12,s13,v12\n"
"v_fma_f32 v12,v12,v11,s13\n"
AW "a20,v12\n"
AR "v12,a40\n"
"v_subrev_f32 v12,s13,v12\n"
"v_fma_f32 v12,v12,v11,s13\n"
AW "a40,v12\n"
AR "v12,a60\n"
"v_subrev_f32 v12,s13,v12\n"
"v_fma_f32 v12,v12,v11,s13\n"
AW "a60,v12\n"
AR "v12,a80\n"
"v_subrev_f32 v12,s13,v12\n"
"v_fma_f32 v12,v12,v11,s13\n"
AW "a80,v12\n"
AR "v12,a100\n"
"v_subrev_f32 v12,s13,v12\n"
"v_fma_f32 v12,v12,v11,s13\n"
AW "a100,v12\n"
AR "v12,a120\n"
"v_subrev_f32 v12,s13,v12\n"
"v_fma_f32 v12,v12,v11,s13\n"
AW "a120,v12\n"
AR "v12,a140\n"
"v_subrev_f32 v12,s13,v12\n"
"v_fma_f32 v12,v12,v11,s13\n"
AW "a140,v12\n"
AR "v12,a160\n"
"v_subrev_f32 v12,s13,v12\n"
"v_fma_f32 v12,v12,v11,s13\n"
AW "a160,v12\n"
AR "v12,a180\n"
"v_subrev_f32 v12,s13,v12\n"
"v_fma_f32 v12,v12,v11,s13\n"
AW "a180,v12\n"
AR "v12,a200\n"
"v_subrev_f32 v12,s13,v12\n"
"v_fma_f32 v12,v12,v11,s13\n"
AW "a200,v12\n"
AR "v12,a220\n"
"v_subrev_f32 v12,s13,v12\n"
"v_fma_f32 v12,v12,v11,s13\n"
AW "a220,v12\n"
AR "v12,a1\n"
"v_subrev_f32 v12,s13,v12\n"
"v_fma_f32 v12,v12,v11,s13\n"
AW "a1,v12\n"
AR "v12,a21\n"
"v_subrev_f32 v12,s13,v12\n"
"v_fma_f32 v12,v12,v11,s13\n"
AW "a21,v12\n"
AR "v12,a41\n"
"v_subrev_f32 v12,s13,v12\n"
"v_fma_f32 v12,v12,v11,s13\n"
AW "a41,v12\n"
AR "v12,a61\n"
"v_subrev_f32 v12,s13,v12\n"
"v_fma_f32 v12,v12,v11,s13\n"
AW "a61,v12\n"
AR "v12,a81\n"
"v_subrev_f32 v12,s13,v12\n"
"v_fma_f32 v12,v12,v11,s13\n"
AW "a81,v12\n"
AR "v12,a101\n"
"v_subrev_f32 v12,s13,v12\n"
"v_fma_f32 v12,v12,v11,s13\n"
AW "a101,v12\n"
AR "v12,a121\n"
"v_subrev_f32 v12,s13,v12\n"
"v_fma_f32 v12,v12,v11,s13\n"
AW "a121,v12\n"
AR "v12,a141\n"
"v_subrev_f32 v12,s13,v12\n"
"v_fma_f32 v12,v12,v11,s13\n"
AW "a141,v12\n"
AR "v12,a161\n"
"v_subrev_f32 v12,s13,v12\n"
"v_fma_f32 v12,v12,v11,s13\n"
AW "a161,v12\n"
AR "v12,a181\n"
"v_subrev_f32 v12,s13,v12\n"
"v_fma_f32 v12,v12,v11,s13\n"
AW "a181,v12\n"
AR "v12,a201\n"
"v_subrev_f32 v12,s13,v12\n"
"v_fma_f32 v12,v12,v11,s13\n"
AW "a201,v12\n"
AR "v12,a221\n"
"v_subrev_f32 v12,s13,v12\n"
"v_fma_f32 v12,v12,v11,s13\n"
AW "a221,v12\n"
AR "v12,a2\n"
"v_subrev_f32 v12,s13,v12\n"
"v_fma_f32 v12,v12,v11,s13\n"
AW "a2,v12\n"
AR "v12,a22\n"
"v_subrev_f32 v12,s13,v12\n"
"v_fma_f32 v12,v12,v11,s13\n"
AW "a22,v12\n"
AR "v12,a42\n"
"v_subrev_f32 v12,s13,v12\n"
"v_fma_f32 v12,v12,v11,s13\n"
AW "a42,v12\n"
AR "v12,a62\n"
"v_subrev_f32 v12,s13,v12\n"
"v_fma_f32 v12,v12,v11,s13\n"
AW "a62,v12\n"
AR "v12,a82\n"
"v_subrev_f32 v12,s13,v12\n"
"v_fma_f32 v12,v12,v11,s13\n"
AW "a82,v12\n"
AR "v12,a102\n"
"v_subrev_f32 v12,s13,v12\n"
"v_fma_f32 v12,v12,v11,s13\n"
AW "a102,v12\n"
AR "v12,a122\n"
"v_subrev_f32 v12,s13,v12\n"
"v_fma_f32 v12,v12,v11,s13\n"
AW "a122,v12\n"
AR "v12,a142\n"
"v_subrev_f32 v12,s13,v12\n"
"v_fma_f32 v12,v12,v11,s13\n"
AW "a142,v12\n"
AR "v12,a162\n"
"v_subrev_f32 v12,s13,v12\n"
"v_fma_f32 v12,v12,v11,s13\n"
AW "a162,v12\n"
AR "v12,a182\n"
"v_subrev_f32 v12,s13,v12\n"
"v_fma_f32 v12,v12,v11,s13\n"
AW "a182,v12\n"
AR "v12,a202\n"
"v_subrev_f32 v12,s13,v12\n"
"v_fma_f32 v12,v12,v11,s13\n"
AW "a202,v12\n"
AR "v12,a222\n"
"v_subrev_f32 v12,s13,v12\n"
"v_fma_f32 v12,v12,v11,s13\n"
AW "a222,v12\n"
AR "v12,a3\n"
"v_subrev_f32 v12,s13,v12\n"
"v_fma_f32 v12,v12,v11,s13\n"
AW "a3,v12\n"
AR "v12,a23\n"
"v_subrev_f32 v12,s13,v12\n"
"v_fma_f32 v12,v12,v11,s13\n"
AW "a23,v12\n"
AR "v12,a43\n"
"v_subrev_f32 v12,s13,v12\n"
"v_fma_f32 v12,v12,v11,s13\n"
AW "a43,v12\n"
AR "v12,a63\n"
"v_subrev_f32 v12,s13,v12\n"
"v_fma_f32 v12,v12,v11,s13\n"
AW "a63,v12\n"
AR "v12,a83\n"
"v_subrev_f32 v12,s13,v12\n"
"v_fma_f32 v12,v12,v11,s13\n"
AW "a83,v12\n"
AR "v12,a103\n"
"v_subrev_f32 v12,s13,v12\n"
"v_fma_f32 v12,v12,v11,s13\n"
AW "a103,v12\n"
AR "v12,a123\n"
"v_subrev_f32 v12,s13,v12\n"
"v_fma_f32 v12,v12,v11,s13\n"
AW "a123,v12\n"
AR "v12,a143\n"
"v_subrev_f32 v12,s13,v12\n"
"v_fma_f32 v12,v12,v11,s13\n"
AW "a143,v12\n"
AR "v12,a163\n"
"v_subrev_f32 v12,s13,v12\n"
"v_fma_f32 v12,v12,v11,s13\n"
AW "a163,v12\n"
AR "v12,a183\n"
"v_subrev_f32 v12,s13,v12\n"
"v_fma_f32 v12,v12,v11,s13\n"
AW "a183,v12\n"
AR "v12,a203\n"
"v_subrev_f32 v12,s13,v12\n"
"v_fma_f32 v12,v12,v11,s13\n"
AW "a203,v12\n"
AR "v12,a223\n"
"v_subrev_f32 v12,s13,v12\n"
"v_fma_f32 v12,v12,v11,s13\n"
AW "a223,v12\n"
"v_mul_f32 v52,v57,v11\n"
"v_sub_f32 v32,v32,v10\n"
"v_sub_f32 v33,v33,v10\n"
"v_sub_f32 v34,v34,v10\n"
"v_sub_f32 v35,v35,v10\n"
"v_sub_f32 v92,v92,v10\n"
"v_sub_f32 v93,v93,v10\n"
"v_sub_f32 v94,v94,v10\n"
"v_sub_f32 v95,v95,v10\n"
"v_sub_f32 v72,v72,v10\n"
"v_sub_f32 v73,v73,v10\n"
"v_sub_f32 v74,v74,v10\n"
"v_sub_f32 v75,v75,v10\n"
"v_sub_f32 v112,v112,v10\n"
"v_sub_f32 v113,v113,v10\n"
"v_sub_f32 v114,v114,v10\n"
"v_sub_f32 v115,v115,v10\n"
"v_exp_f32 v72,v72\n"
"v_exp_f32 v73,v73\n"
"v_exp_f32 v74,v74\n"
"v_exp_f32 v75,v75\n"
"v_exp_f32 v112,v112\n"
"v_exp_f32 v113,v113\n"
"v_exp_f32 v114,v114\n"
"v_exp_f32 v115,v115\n"
"v_add_f32 v52,v52,v72\n"
"v_add_f32 v52,v52,v73\n"
"v_add_f32 v52,v52,v74\n"
"v_add_f32 v52,v52,v75\n"
"v_add_f32 v52,v52,v112\n"
"v_add_f32 v52,v52,v113\n"
"v_add_f32 v52,v52,v114\n"
"v_add_f32 v52,v52,v115\n"
CV "v72,v72,v73\n"
CV "v73,v74,v75\n"
CV "v74,v112,v113\n"
CV "v75,v114,v115\n"
"v_max3_f32 v10,v76,v77,v78\n"
"v_max3_f32 v11,v79,v116,v117\n"
"v_max3_f32 v10,v10,v118,v119\n"
"v_max_f32 v10,v10,v11\n"
"v_mov_b32 v11,v10\n"
"s_nop 1\n"
"v_permlane16_swap_b32 v10,v11\n"
"v_max_f32 v10,v10,v11\n"
"v_mov_b32 v11,v10\n"
"s_nop 1\n"
"v_permlane32_swap_b32 v10,v11\n"
"v_max_f32 v10,v10,v11\n"
"v_add_f32 v10,0x41300000,v10\n"
"v_max_f32 v10,0,v10\n"
"v_exp_f32 v11,-v10\n"
AR "v12,a4\n"
"v_subrev_f32 v12,s13,v12\n"
"v_fma_f32 v12,v12,v11,s13\n"
AW "a4,v12\n"
AR "v12,a24\n"
"v_subrev_f32 v12,s13,v12\n"
"v_fma_f32 v12,v12,v11,s13\n"
AW "a24,v12\n"
AR "v12,a44\n"
"v_subrev_f32 v12,s13,v12\n"
"v_fma_f32 v12,v12,v11,s13\n"
AW "a44,v12\n"
AR "v12,a64\n"
"v_subrev_f32 v12,s13,v12\n"
"v_fma_f32 v12,v12,v11,s13\n"
AW "a64,v12\n"
AR "v12,a84\n"
"v_subrev_f32 v12,s13,v12\n"
"v_fma_f32 v12,v12,v11,s13\n"
AW "a84,v12\n"
AR "v12,a104\n"
"v_subrev_f32 v12,s13,v12\n"
"v_fma_f32 v12,v12,v11,s13\n"
AW "a104,v12\n"
AR "v12,a124\n"
"v_subrev_f32 v12,s13,v12\n"
"v_fma_f32 v12,v12,v11,s13\n"
AW "a124,v12\n"
AR "v12,a144\n"
"v_subrev_f32 v12,s13,v12\n"
"v_fma_f32 v12,v12,v11,s13\n"
AW "a144,v12\n"
AR "v12,a164\n"
"v_subrev_f32 v12,s13,v12\n"
"v_fma_f32 v12,v12,v11,s13\n"
AW "a164,v12\n"
AR "v12,a184\n"
"v_subrev_f32 v12,s13,v12\n"
"v_fma_f32 v12,v12,v11,s13\n"
AW "a184,v12\n"
AR "v12,a204\n"
"v_subrev_f32 v12,s13,v12\n"
"v_fma_f32 v12,v12,v11,s13\n"
AW "a204,v12\n"
AR "v12,a224\n"
"v_subrev_f32 v12,s13,v12\n"
"v_fma_f32 v12,v12,v11,s13\n"
AW "a224,v12\n"
AR "v12,a5\n"
"v_subrev_f32 v12,s13,v12\n"
"v_fma_f32 v12,v12,v11,s13\n"
AW "a5,v12\n"
AR "v12,a25\n"
"v_subrev_f32 v12,s13,v12\n"
"v_fma_f32 v12,v12,v11,s13\n"
AW "a25,v12\n"
AR "v12,a45\n"
"v_subrev_f32 v12,s13,v12\n"
"v_fma_f32 v12,v12,v11,s13\n"
AW "a45,v12\n"
AR "v12,a65\n"
"v_subrev_f32 v12,s13,v12\n"
"v_fma_f32 v12,v12,v11,s13\n"
AW "a65,v12\n"
AR "v12,a85\n"
"v_subrev_f32 v12,s13,v12\n"
"v_fma_f32 v12,v12,v11,s13\n"
AW "a85,v12\n"
AR "v12,a105\n"
"v_subrev_f32 v12,s13,v12\n"
"v_fma_f32 v12,v12,v11,s13\n"
AW "a105,v12\n"
AR "v12,a125\n"
"v_subrev_f32 v12,s13,v12\n"
"v_fma_f32 v12,v12,v11,s13\n"
AW "a125,v12\n"
AR "v12,a145\n"
"v_subrev_f32 v12,s13,v12\n"
"v_fma_f32 v12,v12,v11,s13\n"
AW "a145,v12\n"
AR "v12,a165\n"
"v_subrev_f32 v12,s13,v12\n"
"v_fma_f32 v12,v12,v11,s13\n"
AW "a165,v12\n"
AR "v12,a185\n"
"v_subrev_f32 v12,s13,v12\n"
"v_fma_f32 v12,v12,v11,s13\n"
AW "a185,v12\n"
AR "v12,a205\n"
"v_subrev_f32 v12,s13,v12\n"
"v_fma_f32 v12,v12,v11,s13\n"
AW "a205,v12\n"
AR "v12,a225\n"
"v_subrev_f32 v12,s13,v12\n"
"v_fma_f32 v12,v12,v11,s13\n"
AW "a225,v12\n"
AR "v12,a6\n"
"v_subrev_f32 v12,s13,v12\n"
"v_fma_f32 v12,v12,v11,s13\n"
AW "a6,v12\n"
AR "v12,a26\n"
"v_subrev_f32 v12,s13,v12\n"
"v_fma_f32 v12,v12,v11,s13\n"
AW "a26,v12\n"
AR "v12,a46\n"
"v_subrev_f32 v12,s13,v12\n"
"v_fma_f32 v12,v12,v11,s13\n"
AW "a46,v12\n"
AR "v12,a66\n"
"v_subrev_f32 v12,s13,v12\n"
"v_fma_f32 v12,v12,v11,s13\n"
AW "a66,v12\n"
AR "v12,a86\n"
"v_subrev_f32 v12,s13,v12\n"
"v_fma_f32 v12,v12,v11,s13\n"
AW "a86,v12\n"
AR "v12,a106\n"
"v_subrev_f32 v12,s13,v12\n"
"v_fma_f32 v12,v12,v11,s13\n"
AW "a106,v12\n"
AR "v12,a126\n"
"v_subrev_f32 v12,s13,v12\n"
"v_fma_f32 v12,v12,v11,s13\n"
AW "a126,v12\n"
AR "v12,a146\n"
"v_subrev_f32 v12,s13,v12\n"
"v_fma_f32 v12,v12,v11,s13\n"
AW "a146,v12\n"
AR "v12,a166\n"
"v_subrev_f32 v12,s13,v12\n"
"v_fma_f32 v12,v12,v11,s13\n"
AW "a166,v12\n"
AR "v12,a186\n"
"v_subrev_f32 v12,s13,v12\n"
"v_fma_f32 v12,v12,v11,s13\n"
AW "a186,v12\n"
AR "v12,a206\n"
"v_subrev_f32 v12,s13,v12\n"
"v_fma_f32 v12,v12,v11,s13\n"
AW "a206,v12\n"
AR "v12,a226\n"
"v_subrev_f32 v12,s13,v12\n"
"v_fma_f32 v12,v12,v11,s13\n"
AW "a226,v12\n"
AR "v12,a7\n"
"v_subrev_f32 v12,s13,v12\n"
"v_fma_f32 v12,v12,v11,s13\n"
AW "a7,v12\n"
AR "v12,a27\n"
"v_subrev_f32 v12,s13,v12\n"
"v_fma_f32 v12,v12,v11,s13\n"
AW "a27,v12\n"
AR "v12,a47\n"
"v_subrev_f32 v12,s13,v12\n"
"v_fma_f32 v12,v12,v11,s13\n"
AW "a47,v12\n"
AR "v12,a67\n"
"v_subrev_f32 v12,s13,v12\n"
"v_fma_f32 v12,v12,v11,s13\n"
AW "a67,v12\n"
AR "v12,a87\n"
"v_subrev_f32 v12,s13,v12\n"
"v_fma_f32 v12,v12,v11,s13\n"
AW "a87,v12\n"
AR "v12,a107\n"
"v_subrev_f32 v12,s13,v12\n"
"v_fma_f32 v12,v12,v11,s13\n"
AW "a107,v12\n"
AR "v12,a127\n"
"v_subrev_f32 v12,s13,v12\n"
"v_fma_f32 v12,v12,v11,s13\n"
AW "a127,v12\n"
AR "v12,a147\n"
"v_subrev_f32 v12,s13,v12\n"
"v_fma_f32 v12,v12,v11,s13\n"
AW "a147,v12\n"
AR "v12,a167\n"
"v_subrev_f32 v12,s13,v12\n"
"v_fma_f32 v12,v12,v11,s13\n"
AW "a167,v12\n"
AR "v12,a187\n"
"v_subrev_f32 v12,s13,v12\n"
"v_fma_f32 v12,v12,v11,s13\n"
AW "a187,v12\n"
AR "v12,a207\n"
"v_subrev_f32 v12,s13,v12\n"
"v_fma_f32 v12,v12,v11,s13\n"
AW "a207,v12\n"
AR "v12,a227\n"
"v_subrev_f32 v12,s13,v12\n"
"v_fma_f32 v12,v12,v11,s13\n"
AW "a227,v12\n"
"v_mul_f32 v53,v58,v11\n"
"v_sub_f32 v36,v36,v10\n"
"v_sub_f32 v37,v37,v10\n"
"v_sub_f32 v38,v38,v10\n"
"v_sub_f32 v39,v39,v10\n"
"v_sub_f32 v96,v96,v10\n"
"v_sub_f32 v97,v97,v10\n"
"v_sub_f32 v98,v98,v10\n"
"v_sub_f32 v99,v99,v10\n"
"v_sub_f32 v76,v76,v10\n"
"v_sub_f32 v77,v77,v10\n"
"v_sub_f32 v78,v78,v10\n"
"v_sub_f32 v79,v79,v10\n"
"v_sub_f32 v116,v116,v10\n"
"v_sub_f32 v117,v117,v10\n"
"v_sub_f32 v118,v118,v10\n"
"v_sub_f32 v119,v119,v10\n"
"v_exp_f32 v76,v76\n"
"v_exp_f32 v77,v77\n"
"v_exp_f32 v78,v78\n"
"v_exp_f32 v79,v79\n"
"v_exp_f32 v116,v116\n"
"v_exp_f32 v117,v117\n"
"v_exp_f32 v118,v118\n"
"v_exp_f32 v119,v119\n"
"v_add_f32 v53,v53,v76\n"
"v_add_f32 v53,v53,v77\n"
"v_add_f32 v53,v53,v78\n"
"v_add_f32 v53,v53,v79\n"
"v_add_f32 v53,v53,v116\n"
"v_add_f32 v53,v53,v117\n"
"v_add_f32 v53,v53,v118\n"
"v_add_f32_e64 v53,v53,v119\n"
CV "v76,v76,v77\n"
CV "v77,v78,v79\n"
CV "v78,v116,v117\n"
CV "v79,v118,v119\n"
"v_max3_f32 v10,v80,v81,v82\n"
"v_max3_f32 v11,v83,v120,v121\n"
"v_max3_f32 v10,v10,v122,v123\n"
"v_max_f32 v10,v10,v11\n"
"v_mov_b32 v11,v10\n"
"s_nop 1\n"
"v_permlane16_swap_b32 v10,v11\n"
"v_max_f32 v10,v10,v11\n"
"v_mov_b32 v11,v10\n"
"s_nop 1\n"
"v_permlane32_swap_b32 v10,v11\n"
"v_max_f32 v10,v10,v11\n"
"v_add_f32 v10,0x41300000,v10\n"
"v_max_f32 v10,0,v10\n"
"v_exp_f32 v11,-v10\n"
AR "v12,a8\n"
"v_subrev_f32 v12,s13,v12\n"
"v_fma_f32 v12,v12,v11,s13\n"
AW "a8,v12\n"
AR "v12,a28\n"
"v_subrev_f32 v12,s13,v12\n"
"v_fma_f32 v12,v12,v11,s13\n"
AW "a28,v12\n"
AR "v12,a48\n"
"v_subrev_f32 v12,s13,v12\n"
"v_fma_f32 v12,v12,v11,s13\n"
AW "a48,v12\n"
AR "v12,a68\n"
"v_subrev_f32 v12,s13,v12\n"
"v_fma_f32 v12,v12,v11,s13\n"
AW "a68,v12\n"
AR "v12,a88\n"
"v_subrev_f32 v12,s13,v12\n"
"v_fma_f32 v12,v12,v11,s13\n"
AW "a88,v12\n"
AR "v12,a108\n"
"v_subrev_f32 v12,s13,v12\n"
"v_fma_f32 v12,v12,v11,s13\n"
AW "a108,v12\n"
AR "v12,a128\n"
"v_subrev_f32 v12,s13,v12\n"
"v_fma_f32 v12,v12,v11,s13\n"
AW "a128,v12\n"
AR "v12,a148\n"
"v_subrev_f32 v12,s13,v12\n"
"v_fma_f32 v12,v12,v11,s13\n"
AW "a148,v12\n"
AR "v12,a168\n"
"v_subrev_f32 v12,s13,v12\n"
"v_fma_f32 v12,v12,v11,s13\n"
AW "a168,v12\n"
AR "v12,a188\n"
"v_subrev_f32 v12,s13,v12\n"
"v_fma_f32 v12,v12,v11,s13\n"
AW "a188,v12\n"
AR "v12,a208\n"
"v_subrev_f32 v12,s13,v12\n"
"v_fma_f32 v12,v12,v11,s13\n"
AW "a208,v12\n"
AR "v12,a228\n"
"v_subrev_f32 v12,s13,v12\n"
"v_fma_f32 v12,v12,v11,s13\n"
AW "a228,v12\n"
AR "v12,a9\n"
"v_subrev_f32 v12,s13,v12\n"
"v_fma_f32 v12,v12,v11,s13\n"
AW "a9,v12\n"
AR "v12,a29\n"
"v_subrev_f32 v12,s13,v12\n"
"v_fma_f32 v12,v12,v11,s13\n"
AW "a29,v12\n"
AR "v12,a49\n"
"v_subrev_f32 v12,s13,v12\n"
"v_fma_f32 v12,v12,v11,s13\n"
AW "a49,v12\n"
AR "v12,a69\n"
"v_subrev_f32 v12,s13,v12\n"
"v_fma_f32 v12,v12,v11,s13\n"
AW "a69,v12\n"
AR "v12,a89\n"
"v_subrev_f32 v12,s13,v12\n"
"v_fma_f32 v12,v12,v11,s13\n"
AW "a89,v12\n"
AR "v12,a109\n"
"v_subrev_f32 v12,s13,v12\n"
"v_fma_f32 v12,v12,v11,s13\n"
AW "a109,v12\n"
AR "v12,a129\n"
"v_subrev_f32 v12,s13,v12\n"
"v_fma_f32 v12,v12,v11,s13\n"
AW "a129,v12\n"
AR "v12,a149\n"
"v_subrev_f32 v12,s13,v12\n"
"v_fma_f32 v12,v12,v11,s13\n"
AW "a149,v12\n"
AR "v12,a169\n"
"v_subrev_f32 v12,s13,v12\n"
"v_fma_f32 v12,v12,v11,s13\n"
AW "a169,v12\n"
AR "v12,a189\n"
"v_subrev_f32 v12,s13,v12\n"
"v_fma_f32 v12,v12,v11,s13\n"
AW "a189,v12\n"
AR "v12,a209\n"
"v_subrev_f32 v12,s13,v12\n"
"v_fma_f32 v12,v12,v11,s13\n"
AW "a209,v12\n"
AR "v12,a229\n"
"v_subrev_f32 v12,s13,v12\n"
"v_fma_f32 v12,v12,v11,s13\n"
AW "a229,v12\n"
AR "v12,a10\n"
"v_subrev_f32 v12,s13,v12\n"
"v_fma_f32 v12,v12,v11,s13\n"
AW "a10,v12\n"
AR "v12,a30\n"
"v_subrev_f32 v12,s13,v12\n"
"v_fma_f32 v12,v12,v11,s13\n"
AW "a30,v12\n"
AR "v12,a50\n"
"v_subrev_f32 v12,s13,v12\n"
"v_fma_f32 v12,v12,v11,s13\n"
AW "a50,v12\n"
AR "v12,a70\n"
"v_subrev_f32 v12,s13,v12\n"
"v_fma_f32 v12,v12,v11,s13\n"
AW "a70,v12\n"
AR "v12,a90\n"
"v_subrev_f32 v12,s13,v12\n"
"v_fma_f32 v12,v12,v11,s13\n"
AW "a90,v12\n"
AR "v12,a110\n"
"v_subrev_f32 v12,s13,v12\n"
"v_fma_f32 v12,v12,v11,s13\n"
AW "a110,v12\n"
AR "v12,a130\n"
"v_subrev_f32 v12,s13,v12\n"
"v_fma_f32 v12,v12,v11,s13\n"
AW "a130,v12\n"
AR "v12,a150\n"
"v_subrev_f32 v12,s13,v12\n"
"v_fma_f32 v12,v12,v11,s13\n"
AW "a150,v12\n"
AR "v12,a170\n"
"v_subrev_f32 v12,s13,v12\n"
"v_fma_f32 v12,v12,v11,s13\n"
AW "a170,v12\n"
AR "v12,a190\n"
"v_subrev_f32 v12,s13,v12\n"
"v_fma_f32 v12,v12,v11,s13\n"
AW "a190,v12\n"
AR "v12,a210\n"
"v_subrev_f32 v12,s13,v12\n"
"v_fma_f32 v12,v12,v11,s13\n"
AW "a210,v12\n"
AR "v12,a230\n"
"v_subrev_f32 v12,s13,v12\n"
"v_fma_f32 v12,v12,v11,s13\n"
AW "a230,v12\n"
AR "v12,a11\n"
"v_subrev_f32 v12,s13,v12\n"
"v_fma_f32 v12,v12,v11,s13\n"
AW "a11,v12\n"
AR "v12,a31\n"
"v_subrev_f32 v12,s13,v12\n"
"v_fma_f32 v12,v12,v11,s13\n"
AW "a31,v12\n"
AR "v12,a51\n"
"v_subrev_f32 v12,s13,v12\n"
"v_fma_f32 v12,v12,v11,s13\n"
AW "a51,v12\n"
AR "v12,a71\n"
"v_subrev_f32 v12,s13,v12\n"
"v_fma_f32 v12,v12,v11,s13\n"
AW "a71,v12\n"
AR "v12,a91\n"
"v_subrev_f32 v12,s13,v12\n"
"v_fma_f32 v12,v12,v11,s13\n"
AW "a91,v12\n"
AR "v12,a111\n"
"v_subrev_f32 v12,s13,v12\n"
"v_fma_f32 v12,v12,v11,s13\n"
AW "a111,v12\n"
AR "v12,a131\n"
"v_subrev_f32 v12,s13,v12\n"
"v_fma_f32 v12,v12,v11,s13\n"
AW "a131,v12\n"
AR "v12,a151\n"
"v_subrev_f32 v12,s13,v12\n"
"v_fma_f32 v12,v12,v11,s13\n"
AW "a151,v12\n"
AR "v12,a171\n"
"v_subrev_f32 v12,s13,v12\n"
"v_fma_f32 v12,v12,v11,s13\n"
AW "a171,v12\n"
AR "v12,a191\n"
"v_subrev_f32 v12,s13,v12\n"
"v_fma_f32 v12,v12,v11,s13\n"
AW "a191,v12\n"
AR "v12,a211\n"
"v_subrev_f32 v12,s13,v12\n"
"v_fma_f32 v12,v12,v11,s13\n"
AW "a211,v12\n"
AR "v12,a231\n"
"v_subrev_f32 v12,s13,v12\n"
"v_fma_f32 v12,v12,v11,s13\n"
AW "a231,v12\n"
"v_mul_f32 v54,v59,v11\n"
"v_sub_f32 v40,v40,v10\n"
"v_sub_f32 v41,v41,v10\n"
"v_sub_f32 v42,v42,v10\n"
"v_sub_f32 v43,v43,v10\n"
"v_sub_f32 v100,v100,v10\n"
"v_sub_f32 v101,v101,v10\n"
"v_sub_f32 v102,v102,v10\n"
"v_sub_f32 v103,v103,v10\n"
"v_sub_f32 v80,v80,v10\n"
"v_sub_f32 v81,v81,v10\n"
"v_sub_f32 v82,v82,v10\n"
"v_sub_f32 v83,v83,v10\n"
"v_sub_f32 v120,v120,v10\n"
"v_sub_f32 v121,v121,v10\n"
"v_sub_f32 v122,v122,v10\n"
"v_sub_f32 v123,v123,v10\n"
"v_exp_f32 v80,v80\n"
"v_exp_f32 v81,v81\n"
"v_exp_f32 v82,v82\n"
"v_exp_f32 v83,v83\n"
"v_exp_f32 v120,v120\n"
"v_exp_f32 v121,v121\n"
"v_exp_f32 v122,v122\n"
"v_exp_f32 v123,v123\n"
"v_add_f32 v54,v54,v80\n"
"v_add_f32 v54,v54,v81\n"
"v_add_f32 v54,v54,v82\n"
"v_add_f32 v54,v54,v83\n"
"v_add_f32 v54,v54,v120\n"
"v_add_f32 v54,v54,v121\n"
"v_add_f32 v54,v54,v122\n"
"v_add_f32_e64 v54,v54,v123\n"
CV "v80,v80,v81\n"
CV "v81,v82,v83\n"
CV "v82,v120,v121\n"
CV "v83,v122,v123\n"
"v_max3_f32 v10,v84,v85,v86\n"
"v_max3_f32 v11,v87,v124,v125\n"
"v_max3_f32 v10,v10,v126,v127\n"
"v_max_f32 v10,v10,v11\n"
"v_mov_b32 v11,v10\n"
"s_nop 1\n"
"v_permlane16_swap_b32 v10,v11\n"
"v_max_f32 v10,v10,v11\n"
"v_mov_b32 v11,v10\n"
"s_nop 1\n"
"v_permlane32_swap_b32 v10,v11\n"
"v_max_f32 v10,v10,v11\n"
"v_add_f32 v10,0x41300000,v10\n"
"v_max_f32 v10,0,v10\n"
"v_exp_f32 v11,-v10\n"
AR "v12,a12\n"
"v_subrev_f32 v12,s13,v12\n"
"v_fma_f32 v12,v12,v11,s13\n"
AW "a12,v12\n"
AR "v12,a32\n"
"v_subrev_f32 v12,s13,v12\n"
"v_fma_f32 v12,v12,v11,s13\n"
AW "a32,v12\n"
AR "v12,a52\n"
"v_subrev_f32 v12,s13,v12\n"
"v_fma_f32 v12,v12,v11,s13\n"
AW "a52,v12\n"
AR "v12,a72\n"
"v_subrev_f32 v12,s13,v12\n"
"v_fma_f32 v12,v12,v11,s13\n"
AW "a72,v12\n"
AR "v12,a92\n"
"v_subrev_f32 v12,s13,v12\n"
"v_fma_f32 v12,v12,v11,s13\n"
AW "a92,v12\n"
AR "v12,a112\n"
"v_subrev_f32 v12,s13,v12\n"
"v_fma_f32 v12,v12,v11,s13\n"
AW "a112,v12\n"
AR "v12,a132\n"
"v_subrev_f32 v12,s13,v12\n"
"v_fma_f32 v12,v12,v11,s13\n"
AW "a132,v12\n"
AR "v12,a152\n"
"v_subrev_f32 v12,s13,v12\n"
"v_fma_f32 v12,v12,v11,s13\n"
AW "a152,v12\n"
AR "v12,a172\n"
"v_subrev_f32 v12,s13,v12\n"
"v_fma_f32 v12,v12,v11,s13\n"
AW "a172,v12\n"
AR "v12,a192\n"
"v_subrev_f32 v12,s13,v12\n"
"v_fma_f32 v12,v12,v11,s13\n"
AW "a192,v12\n"
AR "v12,a212\n"
"v_subrev_f32 v12,s13,v12\n"
"v_fma_f32 v12,v12,v11,s13\n"
AW "a212,v12\n"
AR "v12,a232\n"
"v_subrev_f32 v12,s13,v12\n"
"v_fma_f32 v12,v12,v11,s13\n"
AW "a232,v12\n"
AR "v12,a13\n"
"v_subrev_f32 v12,s13,v12\n"
"v_fma_f32 v12,v12,v11,s13\n"
AW "a13,v12\n"
AR "v12,a33\n"
"v_subrev_f32 v12,s13,v12\n"
"v_fma_f32 v12,v12,v11,s13\n"
AW "a33,v12\n"
AR "v12,a53\n"
"v_subrev_f32 v12,s13,v12\n"
"v_fma_f32 v12,v12,v11,s13\n"
AW "a53,v12\n"
AR "v12,a73\n"
"v_subrev_f32 v12,s13,v12\n"
"v_fma_f32 v12,v12,v11,s13\n"
AW "a73,v12\n"
AR "v12,a93\n"
"v_subrev_f32 v12,s13,v12\n"
"v_fma_f32 v12,v12,v11,s13\n"
AW "a93,v12\n"
AR "v12,a113\n"
"v_subrev_f32 v12,s13,v12\n"
"v_fma_f32 v12,v12,v11,s13\n"
AW "a113,v12\n"
AR "v12,a133\n"
"v_subrev_f32 v12,s13,v12\n"
"v_fma_f32 v12,v12,v11,s13\n"
AW "a133,v12\n"
AR "v12,a153\n"
"v_subrev_f32 v12,s13,v12\n"
"v_fma_f32 v12,v12,v11,s13\n"
AW "a153,v12\n"
AR "v12,a173\n"
"v_subrev_f32 v12,s13,v12\n"
"v_fma_f32 v12,v12,v11,s13\n"
AW "a173,v12\n"
AR "v12,a193\n"
"v_subrev_f32 v12,s13,v12\n"
"v_fma_f32 v12,v12,v11,s13\n"
AW "a193,v12\n"
AR "v12,a213\n"
"v_subrev_f32 v12,s13,v12\n"
"v_fma_f32 v12,v12,v11,s13\n"
AW "a213,v12\n"
AR "v12,a233\n"
"v_subrev_f32 v12,s13,v12\n"
"v_fma_f32 v12,v12,v11,s13\n"
AW "a233,v12\n"
AR "v12,a14\n"
"v_subrev_f32 v12,s13,v12\n"
"v_fma_f32 v12,v12,v11,s13\n"
AW "a14,v12\n"
AR "v12,a34\n"
"v_subrev_f32 v12,s13,v12\n"
"v_fma_f32 v12,v12,v11,s13\n"
AW "a34,v12\n"
AR "v12,a54\n"
"v_subrev_f32 v12,s13,v12\n"
"v_fma_f32 v12,v12,v11,s13\n"
AW "a54,v12\n"
AR "v12,a74\n"
"v_subrev_f32 v12,s13,v12\n"
"v_fma_f32 v12,v12,v11,s13\n"
AW "a74,v12\n"
AR "v12,a94\n"
"v_subrev_f32 v12,s13,v12\n"
"v_fma_f32 v12,v12,v11,s13\n"
AW "a94,v12\n"
AR "v12,a114\n"
"v_subrev_f32 v12,s13,v12\n"
"v_fma_f32 v12,v12,v11,s13\n"
AW "a114,v12\n"
AR "v12,a134\n"
"v_subrev_f32 v12,s13,v12\n"
"v_fma_f32 v12,v12,v11,s13\n"
AW "a134,v12\n"
AR "v12,a154\n"
"v_subrev_f32 v12,s13,v12\n"
"v_fma_f32 v12,v12,v11,s13\n"
AW "a154,v12\n"
AR "v12,a174\n"
"v_subrev_f32 v12,s13,v12\n"
"v_fma_f32 v12,v12,v11,s13\n"
AW "a174,v12\n"
AR "v12,a194\n"
"v_subrev_f32 v12,s13,v12\n"
"v_fma_f32 v12,v12,v11,s13\n"
AW "a194,v12\n"
AR "v12,a214\n"
"v_subrev_f32 v12,s13,v12\n"
"v_fma_f32 v12,v12,v11,s13\n"
AW "a214,v12\n"
AR "v12,a234\n"
"v_subrev_f32 v12,s13,v12\n"
"v_fma_f32 v12,v12,v11,s13\n"
AW "a234,v12\n"
AR "v12,a15\n"
"v_subrev_f32 v12,s13,v12\n"
"v_fma_f32 v12,v12,v11,s13\n"
AW "a15,v12\n"
AR "v12,a35\n"
"v_subrev_f32 v12,s13,v12\n"
"v_fma_f32 v12,v12,v11,s13\n"
AW "a35,v12\n"
AR "v12,a55\n"
"v_subrev_f32 v12,s13,v12\n"
"v_fma_f32 v12,v12,v11,s13\n"
AW "a55,v12\n"
AR "v12,a75\n"
"v_subrev_f32 v12,s13,v12\n"
"v_fma_f32 v12,v12,v11,s13\n"
AW "a75,v12\n"
AR "v12,a95\n"
"v_subrev_f32 v12,s13,v12\n"
"v_fma_f32 v12,v12,v11,s13\n"
AW "a95,v12\n"
AR "v12,a115\n"
"v_subrev_f32 v12,s13,v12\n"
"v_fma_f32 v12,v12,v11,s13\n"
AW "a115,v12\n"
AR "v12,a135\n"
"v_subrev_f32 v12,s13,v12\n"
"v_fma_f32 v12,v12,v11,s13\n"
AW "a135,v12\n"
AR "v12,a155\n"
"v_subrev_f32 v12,s13,v12\n"
"v_fma_f32 v12,v12,v11,s13\n"
AW "a155,v12\n"
AR "v12,a175\n"
"v_subrev_f32 v12,s13,v12\n"
"v_fma_f32 v12,v12,v11,s13\n"
AW "a175,v12\n"
AR "v12,a195\n"
"v_subrev_f32 v12,s13,v12\n"
"v_fma_f32 v12,v12,v11,s13\n"
AW "a195,v12\n"
AR "v12,a215\n"
"v_subrev_f32 v12,s13,v12\n"
"v_fma_f32 v12,v12,v11,s13\n"
AW "a215,v12\n"
AR "v12,a235\n"
"v_subrev_f32 v12,s13,v12\n"
"v_fma_f32 v12,v12,v11,s13\n"
AW "a235,v12\n"
"v_mul_f32 v55,v60,v11\n"
"v_sub_f32 v44,v44,v10\n"
"v_sub_f32 v45,v45,v10\n"
"v_sub_f32 v46,v46,v10\n"
"v_sub_f32 v47,v47,v10\n"
"v_sub_f32 v104,v104,v10\n"
"v_sub_f32 v105,v105,v10\n"
"v_sub_f32 v106,v106,v10\n"
"v_sub_f32 v107,v107,v10\n"
"v_sub_f32 v84,v84,v10\n"
"v_sub_f32 v85,v85,v10\n"
"v_sub_f32 v86,v86,v10\n"
"v_sub_f32 v87,v87,v10\n"
"v_sub_f32 v124,v124,v10\n"
"v_sub_f32 v125,v125,v10\n"
"v_sub_f32 v126,v126,v10\n"
"v_sub_f32 v127,v127,v10\n"
"v_exp_f32 v84,v84\n"
"v_exp_f32 v85,v85\n"
"v_exp_f32 v86,v86\n"
"v_exp_f32 v87,v87\n"
"v_exp_f32 v124,v124\n"
"v_exp_f32 v125,v125\n"
"v_exp_f32 v126,v126\n"
"v_exp_f32 v127,v127\n"
"v_add_f32 v55,v55,v84\n"
"v_add_f32 v55,v55,v85\n"
"v_add_f32 v55,v55,v86\n"
"v_add_f32 v55,v55,v87\n"
"v_add_f32 v55,v55,v124\n"
"v_add_f32 v55,v55,v125\n"
"v_add_f32 v55,v55,v126\n"
"v_add_f32_e64 v55,v55,v127\n"
CV "v84,v84,v85\n"
CV "v85,v86,v87\n"
CV "v86,v124,v125\n"
CV "v87,v126,v127\n"
"v_max3_f32 v10,v88,v89,v90\n"
"v_max3_f32 v11,v91,v128,v129\n"
"v_max3_f32 v10,v10,v130,v131\n"
"v_max_f32 v10,v10,v11\n"
"v_mov_b32 v11,v10\n"
"s_nop 1\n"
"v_permlane16_swap_b32 v10,v11\n"
"v_max_f32 v10,v10,v11\n"
"v_mov_b32 v11,v10\n"
"s_nop 1\n"
"v_permlane32_swap_b32 v10,v11\n"
"v_max_f32_e64 v10,v10,v11\n"
"v_add_f32 v10,0x41300000,v10\n"
"v_max_f32 v10,0,v10\n"
"v_exp_f32 v11,-v10\n"
AR "v12,a16\n"
"v_subrev_f32 v12,s13,v12\n"
"v_fma_f32 v12,v12,v11,s13\n"
AW "a16,v12\n"
AR "v12,a36\n"
"v_subrev_f32 v12,s13,v12\n"
"v_fma_f32 v12,v12,v11,s13\n"
AW "a36,v12\n"
AR "v12,a56\n"
"v_subrev_f32 v12,s13,v12\n"
"v_fma_f32 v12,v12,v11,s13\n"
AW "a56,v12\n"
AR "v12,a76\n"
"v_subrev_f32 v12,s13,v12\n"
"v_fma_f32 v12,v12,v11,s13\n"
AW "a76,v12\n"
AR "v12,a96\n"
"v_subrev_f32 v12,s13,v12\n"
"v_fma_f32 v12,v12,v11,s13\n"
AW "a96,v12\n"
AR "v12,a116\n"
"v_subrev_f32 v12,s13,v12\n"
"v_fma_f32 v12,v12,v11,s13\n"
AW "a116,v12\n"
AR "v12,a136\n"
"v_subrev_f32 v12,s13,v12\n"
"v_fma_f32 v12,v12,v11,s13\n"
AW "a136,v12\n"
AR "v12,a156\n"
"v_subrev_f32 v12,s13,v12\n"
"v_fma_f32 v12,v12,v11,s13\n"
AW "a156,v12\n"
AR "v12,a176\n"
"v_subrev_f32 v12,s13,v12\n"
"v_fma_f32 v12,v12,v11,s13\n"
AW "a176,v12\n"
AR "v12,a196\n"
"v_subrev_f32 v12,s13,v12\n"
"v_fma_f32 v12,v12,v11,s13\n"
AW "a196,v12\n"
AR "v12,a216\n"
"v_subrev_f32 v12,s13,v12\n"
"v_fma_f32 v12,v12,v11,s13\n"
AW "a216,v12\n"
AR "v12,a236\n"
"v_subrev_f32 v12,s13,v12\n"
"v_fma_f32 v12,v12,v11,s13\n"
AW "a236,v12\n"
AR "v12,a17\n"
"v_subrev_f32 v12,s13,v12\n"
"v_fma_f32 v12,v12,v11,s13\n"
AW "a17,v12\n"
AR "v12,a37\n"
"v_subrev_f32 v12,s13,v12\n"
"v_fma_f32 v12,v12,v11,s13\n"
AW "a37,v12\n"
AR "v12,a57\n"
"v_subrev_f32 v12,s13,v12\n"
"v_fma_f32 v12,v12,v11,s13\n"
AW "a57,v12\n"
AR "v12,a77\n"
"v_subrev_f32 v12,s13,v12\n"
"v_fma_f32 v12,v12,v11,s13\n"
AW "a77,v12\n"
AR "v12,a97\n"
"v_subrev_f32 v12,s13,v12\n"
"v_fma_f32 v12,v12,v11,s13\n"
AW "a97,v12\n"
AR "v12,a117\n"
"v_subrev_f32 v12,s13,v12\n"
"v_fma_f32 v12,v12,v11,s13\n"
AW "a117,v12\n"
AR "v12,a137\n"
"v_subrev_f32 v12,s13,v12\n"
"v_fma_f32 v12,v12,v11,s13\n"
AW "a137,v12\n"
AR "v12,a157\n"
"v_subrev_f32 v12,s13,v12\n"
"v_fma_f32 v12,v12,v11,s13\n"
AW "a157,v12\n"
AR "v12,a177\n"
"v_subrev_f32 v12,s13,v12\n"
"v_fma_f32 v12,v12,v11,s13\n"
AW "a177,v12\n"
AR "v12,a197\n"
"v_subrev_f32 v12,s13,v12\n"
"v_fma_f32 v12,v12,v11,s13\n"
AW "a197,v12\n"
AR "v12,a217\n"
"v_subrev_f32 v12,s13,v12\n"
"v_fma_f32 v12,v12,v11,s13\n"
AW "a217,v12\n"
AR "v12,a237\n"
"v_subrev_f32 v12,s13,v12\n"
"v_fma_f32 v12,v12,v11,s13\n"
AW "a237,v12\n"
AR "v12,a18\n"
"v_subrev_f32 v12,s13,v12\n"
"v_fma_f32 v12,v12,v11,s13\n"
AW "a18,v12\n"
AR "v12,a38\n"
"v_subrev_f32 v12,s13,v12\n"
"v_fma_f32 v12,v12,v11,s13\n"
AW "a38,v12\n"
AR "v12,a58\n"
"v_subrev_f32 v12,s13,v12\n"
"v_fma_f32 v12,v12,v11,s13\n"
AW "a58,v12\n"
AR "v12,a78\n"
"v_subrev_f32 v12,s13,v12\n"
"v_fma_f32 v12,v12,v11,s13\n"
AW "a78,v12\n"
AR "v12,a98\n"
"v_subrev_f32 v12,s13,v12\n"
"v_fma_f32 v12,v12,v11,s13\n"
AW "a98,v12\n"
AR "v12,a118\n"
"v_subrev_f32 v12,s13,v12\n"
"v_fma_f32 v12,v12,v11,s13\n"
AW "a118,v12\n"
AR "v12,a138\n"
"v_subrev_f32 v12,s13,v12\n"
"v_fma_f32 v12,v12,v11,s13\n"
AW "a138,v12\n"
AR "v12,a158\n"
"v_subrev_f32 v12,s13,v12\n"
"v_fma_f32 v12,v12,v11,s13\n"
AW "a158,v12\n"
AR "v12,a178\n"
"v_subrev_f32 v12,s13,v12\n"
"v_fma_f32 v12,v12,v11,s13\n"
AW "a178,v12\n"
AR "v12,a198\n"
"v_subrev_f32 v12,s13,v12\n"
"v_fma_f32 v12,v12,v11,s13\n"
AW "a198,v12\n"
AR "v12,a218\n"
"v_subrev_f32 v12,s13,v12\n"
"v_fma_f32 v12,v12,v11,s13\n"
AW "a218,v12\n"
AR "v12,a238\n"
"v_subrev_f32 v12,s13,v12\n"
"v_fma_f32 v12,v12,v11,s13\n"
AW "a238,v12\n"
AR "v12,a19\n"
"v_subrev_f32 v12,s13,v12\n"
"v_fma_f32 v12,v12,v11,s13\n"
AW "a19,v12\n"
AR "v12,a39\n"
"v_subrev_f32 v12,s13,v12\n"
"v_fma_f32 v12,v12,v11,s13\n"
AW "a39,v12\n"
AR "v12,a59\n"
"v_subrev_f32 v12,s13,v12\n"
"v_fma_f32 v12,v12,v11,s13\n"
AW "a59,v12\n"
AR "v12,a79\n"
"v_subrev_f32 v12,s13,v12\n"
"v_fma_f32 v12,v12,v11,s13\n"
AW "a79,v12\n"
AR "v12,a99\n"
"v_subrev_f32 v12,s13,v12\n"
"v_fma_f32 v12,v12,v11,s13\n"
AW "a99,v12\n"
AR "v12,a119\n"
"v_subrev_f32 v12,s13,v12\n"
"v_fma_f32 v12,v12,v11,s13\n"
AW "a119,v12\n"
AR "v12,a139\n"
"v_subrev_f32 v12,s13,v12\n"
"v_fma_f32 v12,v12,v11,s13\n"
AW "a139,v12\n"
AR "v12,a159\n"
"v_subrev_f32 v12,s13,v12\n"
"v_fma_f32 v12,v12,v11,s13\n"
AW "a159,v12\n"
AR "v12,a179\n"
"v_subrev_f32 v12,s13,v12\n"
"v_fma_f32 v12,v12,v11,s13\n"
AW "a179,v12\n"
AR "v12,a199\n"
"v_subrev_f32 v12,s13,v12\n"
"v_fma_f32 v12,v12,v11,s13\n"
AW "a199,v12\n"
AR "v12,a219\n"
"v_subrev_f32 v12,s13,v12\n"
"v_fma_f32 v12,v12,v11,s13\n"
AW "a219,v12\n"
AR "v12,a239\n"
"v_subrev_f32 v12,s13,v12\n"
"v_fma_f32 v12,v12,v11,s13\n"
AW "a239,v12\n"
"v_mul_f32 v56,v61,v11\n"
"v_sub_f32 v48,v48,v10\n"
"v_sub_f32 v49,v49,v10\n"
"v_sub_f32 v50,v50,v10\n"
"v_sub_f32 v51,v51,v10\n"
"v_sub_f32 v108,v108,v10\n"
"v_sub_f32 v109,v109,v10\n"
"v_sub_f32 v110,v110,v10\n"
"v_sub_f32 v111,v111,v10\n"
"v_sub_f32 v88,v88,v10\n"
"v_sub_f32 v89,v89,v10\n"
"v_sub_f32 v90,v90,v10\n"
"v_sub_f32 v91,v91,v10\n"
"v_sub_f32 v128,v128,v10\n"
"v_sub_f32 v129,v129,v10\n"
"v_sub_f32 v130,v130,v10\n"
"v_sub_f32 v131,v131,v10\n"
"v_exp_f32 v88,v88\n"
"v_exp_f32 v89,v89\n"
"v_exp_f32 v90,v90\n"
"v_exp_f32 v91,v91\n"
"v_exp_f32 v128,v128\n"
"v_exp_f32 v129,v129\n"
"v_exp_f32 v130,v130\n"
"v_exp_f32 v131,v131\n"
"v_add_f32 v56,v56,v88\n"
"v_add_f32 v56,v56,v89\n"
"v_add_f32 v56,v56,v90\n"
"v_add_f32 v56,v56,v91\n"
"v_add_f32 v56,v56,v128\n"
"v_add_f32 v56,v56,v129\n"
"v_add_f32 v56,v56,v130\n"
"v_add_f32 v56,v56,v131\n"
CV "v88,v88,v89\n"
CV "v89,v90,v91\n"
CV "v90,v128,v129\n"
CV "v91,v130,v131\n"
"s_nop 7\n"
"s_setpc_b64 s[52:53]\n"
".Lslow_1:\n"
"s_nop 7\n"
"s_nop 7\n"
"v_and_b32 v10,240,v9\n"
"v_mul_u32_u24 v10,24,v10\n"
"v_lshrrev_b32 v11,4,v9\n"
"v_and_or_b32 v10,v11,48,v10\n"
"s_mov_b32 s59,s34\n"
"s_mov_b32 s34,s20\n"
"s_add_u32 s58,s74,0\n"
"s_nop 3\n"
LD "v[58:61],v10,s[32:35],s58 offen\n"
"s_waitcnt vmcnt(0)\n"
MF "v[92:95],v[58:61],v[136:139],v[32:35]\n"
MF "v[96:99],v[58:61],v[140:143],v[36:39]\n"
MF "v[100:103],v[58:61],v[144:147],v[40:43]\n"
MF "v[104:107],v[58:61],v[148:151],v[44:47]\n"
MF "v[108:111],v[58:61],v[152:155],v[48:51]\n"
"s_add_u32 s58,s74,0\n"
"s_nop 3\n"
LD "v[58:61],v10,s[32:35],s58 offen offset:64\n"
"s_waitcnt vmcnt(0)\n"
MF "v[92:95],v[58:61],v[156:159],v[92:95]\n"
MF "v[96:99],v[58:61],v[160:163],v[96:99]\n"
MF "v[100:103],v[58:61],v[164:167],v[100:103]\n"
MF "v[104:107],v[58:61],v[168:171],v[104:107]\n"
MF "v[108:111],v[58:61],v[172:175],v[108:111]\n"
"s_add_u32 s58,s74,0\n"
"s_nop 3\n"
LD "v[58:61],v10,s[32:35],s58 offen offset:128\n"
"s_waitcnt vmcnt(0)\n"
MF "v[92:95],v[58:61],v[176:179],v[92:95]\n"
MF "v[96:99],v[58:61],v[180:183],v[96:99]\n"
MF "v[100:103],v[58:61],v[184:187],v[100:103]\n"
MF "v[104:107],v[58:61],v[188:191],v[104:107]\n"
MF "v[108:111],v[58:61],v[192:195],v[108:111]\n"
"s_add_u32 s58,s74,0\n"
"s_nop 3\n"
LD "v[58:61],v10,s[32:35],s58 offen offset:192\n"
"s_waitcnt vmcnt(0)\n"
MF "v[92:95],v[58:61],v[196:199],v[92:95]\n"
MF "v[96:99],v[58:61],v[200:203],v[96:99]\n"
MF "v[100:103],v[58:61],v[204:207],v[100:103]\n"
MF "v[104:107],v[58:61],v[208:211],v[104:107]\n"
MF "v[108:111],v[58:61],v[212:215],v[108:111]\n"
"s_add_u32 s58,s74,0\n"
"s_nop 3\n"
LD "v[58:61],v10,s[32:35],s58 offen offset:256\n"
"s_waitcnt vmcnt(0)\n"
MF "v[92:95],v[58:61],v[216:219],v[92:95]\n"
MF "v[96:99],v[58:61],v[220:223],v[96:99]\n"
MF "v[100:103],v[58:61],v[224:227],v[100:103]\n"
MF "v[104:107],v[58:61],v[228:231],v[104:107]\n"
MF "v[108:111],v[58:61],v[232:235],v[108:111]\n"
"s_add_u32 s58,s74,0\n"
"s_nop 3\n"
LD "v[58:61],v10,s[32:35],s58 offen offset:320\n"
"s_waitcnt vmcnt(0)\n"
MF "v[92:95],v[58:61],v[236:239],v[92:95]\n"
MF "v[96:99],v[58:61],v[240:243],v[96:99]\n"
MF "v[100:103],v[58:61],v[244:247],v[100:103]\n"
MF "v[104:107],v[58:61],v[248:251],v[104:107]\n"
MF "v[108:111],v[58:61],v[252:255],v[108:111]\n"
"s_add_u32 s58,s74,6144\n"
"s_nop 3\n"
LD "v[58:61],v10,s[32:35],s58 offen\n"
"s_waitcnt vmcnt(0)\n"
MF "v[112:115],v[58:61],v[136:139],v[32:35]\n"
MF "v[116:119],v[58:61],v[140:143],v[36:39]\n"
MF "v[120:123],v[58:61],v[144:147],v[40:43]\n"
MF "v[124:127],v[58:61],v[148:151],v[44:47]\n"
MF "v[128:131],v[58:61],v[152:155],v[48:51]\n"
"s_add_u32 s58,s74,6144\n"
"s_nop 3\n"
LD "v[58:61],v10,s[32:35],s58 offen offset:64\n"
"s_waitcnt vmcnt(0)\n"
MF "v[112:115],v[58:61],v[156:159],v[112:115]\n"
MF "v[116:119],v[58:61],v[160:163],v[116:119]\n"
MF "v[120:123],v[58:61],v[164:167],v[120:123]\n"
MF "v[124:127],v[58:61],v[168:171],v[124:127]\n"
MF "v[128:131],v[58:61],v[172:175],v[128:131]\n"
"s_add_u32 s58,s74,6144\n"
"s_nop 3\n"
LD "v[58:61],v10,s[32:35],s58 offen offset:128\n"
"s_waitcnt vmcnt(0)\n"
MF "v[112:115],v[58:61],v[176:179],v[112:115]\n"
MF "v[116:119],v[58:61],v[180:183],v[116:119]\n"
MF "v[120:123],v[58:61],v[184:187],v[120:123]\n"
MF "v[124:127],v[58:61],v[188:191],v[124:127]\n"
MF "v[128:131],v[58:61],v[192:195],v[128:131]\n"
"s_add_u32 s58,s74,6144\n"
"s_nop 3\n"
LD "v[58:61],v10,s[32:35],s58 offen offset:192\n"
"s_waitcnt vmcnt(0)\n"
MF "v[112:115],v[58:61],v[196:199],v[112:115]\n"
MF "v[116:119],v[58:61],v[200:203],v[116:119]\n"
MF "v[120:123],v[58:61],v[204:207],v[120:123]\n"
MF "v[124:127],v[58:61],v[208:211],v[124:127]\n"
MF "v[128:131],v[58:61],v[212:215],v[128:131]\n"
"s_add_u32 s58,s74,6144\n"
"s_nop 3\n"
LD "v[58:61],v10,s[32:35],s58 offen offset:256\n"
"s_waitcnt vmcnt(0)\n"
MF "v[112:115],v[58:61],v[216:219],v[112:115]\n"
MF "v[116:119],v[58:61],v[220:223],v[116:119]\n"
MF "v[120:123],v[58:61],v[224:227],v[120:123]\n"
MF "v[124:127],v[58:61],v[228:231],v[124:127]\n"
MF "v[128:131],v[58:61],v[232:235],v[128:131]\n"
"s_add_u32 s58,s74,6144\n"
"s_nop 3\n"
LD "v[58:61],v10,s[32:35],s58 offen offset:320\n"
"s_waitcnt vmcnt(0)\n"
MF "v[112:115],v[58:61],v[236:239],v[112:115]\n"
MF "v[116:119],v[58:61],v[240:243],v[116:119]\n"
MF "v[120:123],v[58:61],v[244:247],v[120:123]\n"
MF "v[124:127],v[58:61],v[248:251],v[124:127]\n"
MF "v[128:131],v[58:61],v[252:255],v[128:131]\n"
"s_nop 7\n"
"s_mov_b32 s34,s59\n"
"v_max3_f32 v10,v92,v93,v94\n"
"s_nop 0\n"
"v_max3_f32 v11,v95,v112,v113\n"
"v_max3_f32 v10,v10,v114,v115\n"
"v_max_f32 v10,v10,v11\n"
"v_mov_b32 v11,v10\n"
"s_nop 1\n"
"v_permlane16_swap_b32 v10,v11\n"
"v_max_f32 v10,v10,v11\n"
"v_mov_b32 v11,v10\n"
"s_nop 1\n"
"v_permlane32_swap_b32 v10,v11\n"
"v_max_f32 v10,v10,v11\n"
"v_add_f32 v10,0x41300000,v10\n"
"v_max_f32 v10,0,v10\n"
"v_exp_f32 v11,-v10\n"
AR "v12,a0\n"
"v_subrev_f32 v12,s13,v12\n"
"v_fma_f32 v12,v12,v11,s13\n"
AW "a0,v12\n"
AR "v12,a20\n"
"v_subrev_f32 v12,s13,v12\n"
"v_fma_f32 v12,v12,v11,s13\n"
AW "a20,v12\n"
AR "v12,a40\n"
"v_subrev_f32 v12,s13,v12\n"
"v_fma_f32 v12,v12,v11,s13\n"
AW "a40,v12\n"
AR "v12,a60\n"
"v_subrev_f32 v12,s13,v12\n"
"v_fma_f32 v12,v12,v11,s13\n"
AW "a60,v12\n"
AR "v12,a80\n"
"v_subrev_f32 v12,s13,v12\n"
"v_fma_f32 v12,v12,v11,s13\n"
AW "a80,v12\n"
AR "v12,a100\n"
"v_subrev_f32 v12,s13,v12\n"
"v_fma_f32 v12,v12,v11,s13\n"
AW "a100,v12\n"
AR "v12,a120\n"
"v_subrev_f32 v12,s13,v12\n"
"v_fma_f32 v12,v12,v11,s13\n"
AW "a120,v12\n"
AR "v12,a140\n"
"v_subrev_f32 v12,s13,v12\n"
"v_fma_f32 v12,v12,v11,s13\n"
AW "a140,v12\n"
AR "v12,a160\n"
"v_subrev_f32 v12,s13,v12\n"
"v_fma_f32 v12,v12,v11,s13\n"
AW "a160,v12\n"
AR "v12,a180\n"
"v_subrev_f32 v12,s13,v12\n"
"v_fma_f32 v12,v12,v11,s13\n"
AW "a180,v12\n"
AR "v12,a200\n"
"v_subrev_f32 v12,s13,v12\n"
"v_fma_f32 v12,v12,v11,s13\n"
AW "a200,v12\n"
AR "v12,a220\n"
"v_subrev_f32 v12,s13,v12\n"
"v_fma_f32 v12,v12,v11,s13\n"
AW "a220,v12\n"
AR "v12,a1\n"
"v_subrev_f32 v12,s13,v12\n"
"v_fma_f32 v12,v12,v11,s13\n"
AW "a1,v12\n"
AR "v12,a21\n"
"v_subrev_f32 v12,s13,v12\n"
"v_fma_f32 v12,v12,v11,s13\n"
AW "a21,v12\n"
AR "v12,a41\n"
"v_subrev_f32 v12,s13,v12\n"
"v_fma_f32 v12,v12,v11,s13\n"
AW "a41,v12\n"
AR "v12,a61\n"
"v_subrev_f32 v12,s13,v12\n"
"v_fma_f32 v12,v12,v11,s13\n"
AW "a61,v12\n"
AR "v12,a81\n"
"v_subrev_f32 v12,s13,v12\n"
"v_fma_f32 v12,v12,v11,s13\n"
AW "a81,v12\n"
AR "v12,a101\n"
"v_subrev_f32 v12,s13,v12\n"
"v_fma_f32 v12,v12,v11,s13\n"
AW "a101,v12\n"
AR "v12,a121\n"
"v_subrev_f32 v12,s13,v12\n"
"v_fma_f32 v12,v12,v11,s13\n"
AW "a121,v12\n"
AR "v12,a141\n"
"v_subrev_f32 v12,s13,v12\n"
"v_fma_f32 v12,v12,v11,s13\n"
AW "a141,v12\n"
AR "v12,a161\n"
"v_subrev_f32 v12,s13,v12\n"
"v_fma_f32 v12,v12,v11,s13\n"
AW "a161,v12\n"
AR "v12,a181\n"
"v_subrev_f32 v12,s13,v12\n"
"v_fma_f32 v12,v12,v11,s13\n"
AW "a181,v12\n"
AR "v12,a201\n"
"v_subrev_f32 v12,s13,v12\n"
"v_fma_f32 v12,v12,v11,s13\n"
AW "a201,v12\n"
AR "v12,a221\n"
"v_subrev_f32 v12,s13,v12\n"
"v_fma_f32 v12,v12,v11,s13\n"
AW "a221,v12\n"
AR "v12,a2\n"
"v_subrev_f32 v12,s13,v12\n"
"v_fma_f32 v12,v12,v11,s13\n"
AW "a2,v12\n"
AR "v12,a22\n"
"v_subrev_f32 v12,s13,v12\n"
"v_fma_f32 v12,v12,v11,s13\n"
AW "a22,v12\n"
AR "v12,a42\n"
"v_subrev_f32 v12,s13,v12\n"
"v_fma_f32 v12,v12,v11,s13\n"
AW "a42,v12\n"
AR "v12,a62\n"
"v_subrev_f32 v12,s13,v12\n"
"v_fma_f32 v12,v12,v11,s13\n"
AW "a62,v12\n"
AR "v12,a82\n"
"v_subrev_f32 v12,s13,v12\n"
"v_fma_f32 v12,v12,v11,s13\n"
AW "a82,v12\n"
AR "v12,a102\n"
"v_subrev_f32 v12,s13,v12\n"
"v_fma_f32 v12,v12,v11,s13\n"
AW "a102,v12\n"
AR "v12,a122\n"
"v_subrev_f32 v12,s13,v12\n"
"v_fma_f32 v12,v12,v11,s13\n"
AW "a122,v12\n"
AR "v12,a142\n"
"v_subrev_f32 v12,s13,v12\n"
"v_fma_f32 v12,v12,v11,s13\n"
AW "a142,v12\n"
AR "v12,a162\n"
"v_subrev_f32 v12,s13,v12\n"
"v_fma_f32 v12,v12,v11,s13\n"
AW "a162,v12\n"
AR "v12,a182\n"
"v_subrev_f32 v12,s13,v12\n"
"v_fma_f32 v12,v12,v11,s13\n"
AW "a182,v12\n"
AR "v12,a202\n"
"v_subrev_f32 v12,s13,v12\n"
"v_fma_f32 v12,v12,v11,s13\n"
AW "a202,v12\n"
AR "v12,a222\n"
"v_subrev_f32 v12,s13,v12\n"
"v_fma_f32 v12,v12,v11,s13\n"
AW "a222,v12\n"
AR "v12,a3\n"
"v_subrev_f32 v12,s13,v12\n"
"v_fma_f32 v12,v12,v11,s13\n"
AW "a3,v12\n"
AR "v12,a23\n"
"v_subrev_f32 v12,s13,v12\n"
"v_fma_f32 v12,v12,v11,s13\n"
AW "a23,v12\n"
AR "v12,a43\n"
"v_subrev_f32 v12,s13,v12\n"
"v_fma_f32 v12,v12,v11,s13\n"
AW "a43,v12\n"
AR "v12,a63\n"
"v_subrev_f32 v12,s13,v12\n"
"v_fma_f32 v12,v12,v11,s13\n"
AW "a63,v12\n"
AR "v12,a83\n"
"v_subrev_f32 v12,s13,v12\n"
"v_fma_f32 v12,v12,v11,s13\n"
AW "a83,v12\n"
AR "v12,a103\n"
"v_subrev_f32 v12,s13,v12\n"
"v_fma_f32 v12,v12,v11,s13\n"
AW "a103,v12\n"
AR "v12,a123\n"
"v_subrev_f32 v12,s13,v12\n"
"v_fma_f32 v12,v12,v11,s13\n"
AW "a123,v12\n"
AR "v12,a143\n"
"v_subrev_f32 v12,s13,v12\n"
"v_fma_f32 v12,v12,v11,s13\n"
AW "a143,v12\n"
AR "v12,a163\n"
"v_subrev_f32 v12,s13,v12\n"
"v_fma_f32 v12,v12,v11,s13\n"
AW "a163,v12\n"
AR "v12,a183\n"
"v_subrev_f32 v12,s13,v12\n"
"v_fma_f32 v12,v12,v11,s13\n"
AW "a183,v12\n"
AR "v12,a203\n"
"v_subrev_f32 v12,s13,v12\n"
"v_fma_f32 v12,v12,v11,s13\n"
AW "a203,v12\n"
AR "v12,a223\n"
"v_subrev_f32 v12,s13,v12\n"
"v_fma_f32 v12,v12,v11,s13\n"
AW "a223,v12\n"
"v_mul_f32 v57,v52,v11\n"
"v_sub_f32 v32,v32,v10\n"
"v_sub_f32 v33,v33,v10\n"
"v_sub_f32 v34,v34,v10\n"
"v_sub_f32 v35,v35,v10\n"
"v_sub_f32 v72,v72,v10\n"
"v_sub_f32 v73,v73,v10\n"
"v_sub_f32 v74,v74,v10\n"
"v_sub_f32 v75,v75,v10\n"
"v_sub_f32 v92,v92,v10\n"
"v_sub_f32 v93,v93,v10\n"
"v_sub_f32 v94,v94,v10\n"
"v_sub_f32 v95,v95,v10\n"
"v_sub_f32 v112,v112,v10\n"
"v_sub_f32 v113,v113,v10\n"
"v_sub_f32 v114,v114,v10\n"
"v_sub_f32 v115,v115,v10\n"
"v_exp_f32 v92,v92\n"
"v_exp_f32 v93,v93\n"
"v_exp_f32 v94,v94\n"
"v_exp_f32 v95,v95\n"
"v_exp_f32 v112,v112\n"
"v_exp_f32 v113,v113\n"
"v_exp_f32 v114,v114\n"
"v_exp_f32 v115,v115\n"
"v_add_f32 v57,v57,v92\n"
"v_add_f32 v57,v57,v93\n"
"v_add_f32 v57,v57,v94\n"
"v_add_f32 v57,v57,v95\n"
"v_add_f32 v57,v57,v112\n"
"v_add_f32 v57,v57,v113\n"
"v_add_f32 v57,v57,v114\n"
"v_add_f32 v57,v57,v115\n"
CV "v92,v92,v93\n"
CV "v93,v94,v95\n"
CV "v94,v112,v113\n"
CV "v95,v114,v115\n"
"v_max3_f32 v10,v96,v97,v98\n"
"v_max3_f32 v11,v99,v116,v117\n"
"v_max3_f32 v10,v10,v118,v119\n"
"v_max_f32 v10,v10,v11\n"
"v_mov_b32 v11,v10\n"
"s_nop 1\n"
"v_permlane16_swap_b32 v10,v11\n"
"v_max_f32 v10,v10,v11\n"
"v_mov_b32 v11,v10\n"
"s_nop 1\n"
"v_permlane32_swap_b32 v10,v11\n"
"v_max_f32 v10,v10,v11\n"
"v_add_f32 v10,0x41300000,v10\n"
"v_max_f32 v10,0,v10\n"
"v_exp_f32 v11,-v10\n"
AR "v12,a4\n"
"v_subrev_f32 v12,s13,v12\n"
"v_fma_f32 v12,v12,v11,s13\n"
AW "a4,v12\n"
AR "v12,a24\n"
"v_subrev_f32 v12,s13,v12\n"
"v_fma_f32 v12,v12,v11,s13\n"
AW "a24,v12\n"
AR "v12,a44\n"
"v_subrev_f32 v12,s13,v12\n"
"v_fma_f32 v12,v12,v11,s13\n"
AW "a44,v12\n"
AR "v12,a64\n"
"v_subrev_f32 v12,s13,v12\n"
"v_fma_f32 v12,v12,v11,s13\n"
AW "a64,v12\n"
AR "v12,a84\n"
"v_subrev_f32 v12,s13,v12\n"
"v_fma_f32 v12,v12,v11,s13\n"
AW "a84,v12\n"
AR "v12,a104\n"
"v_subrev_f32 v12,s13,v12\n"
"v_fma_f32 v12,v12,v11,s13\n"
AW "a104,v12\n"
AR "v12,a124\n"
"v_subrev_f32 v12,s13,v12\n"
"v_fma_f32 v12,v12,v11,s13\n"
AW "a124,v12\n"
AR "v12,a144\n"
"v_subrev_f32 v12,s13,v12\n"
"v_fma_f32 v12,v12,v11,s13\n"
AW "a144,v12\n"
AR "v12,a164\n"
"v_subrev_f32 v12,s13,v12\n"
"v_fma_f32 v12,v12,v11,s13\n"
AW "a164,v12\n"
AR "v12,a184\n"
"v_subrev_f32 v12,s13,v12\n"
"v_fma_f32 v12,v12,v11,s13\n"
AW "a184,v12\n"
AR "v12,a204\n"
"v_subrev_f32 v12,s13,v12\n"
"v_fma_f32 v12,v12,v11,s13\n"
AW "a204,v12\n"
AR "v12,a224\n"
"v_subrev_f32 v12,s13,v12\n"
"v_fma_f32 v12,v12,v11,s13\n"
AW "a224,v12\n"
AR "v12,a5\n"
"v_subrev_f32 v12,s13,v12\n"
"v_fma_f32 v12,v12,v11,s13\n"
AW "a5,v12\n"
AR "v12,a25\n"
"v_subrev_f32 v12,s13,v12\n"
"v_fma_f32 v12,v12,v11,s13\n"
AW "a25,v12\n"
AR "v12,a45\n"
"v_subrev_f32 v12,s13,v12\n"
"v_fma_f32 v12,v12,v11,s13\n"
AW "a45,v12\n"
AR "v12,a65\n"
"v_subrev_f32 v12,s13,v12\n"
"v_fma_f32 v12,v12,v11,s13\n"
AW "a65,v12\n"
AR "v12,a85\n"
"v_subrev_f32 v12,s13,v12\n"
"v_fma_f32 v12,v12,v11,s13\n"
AW "a85,v12\n"
AR "v12,a105\n"
"v_subrev_f32 v12,s13,v12\n"
"v_fma_f32 v12,v12,v11,s13\n"
AW "a105,v12\n"
AR "v12,a125\n"
"v_subrev_f32 v12,s13,v12\n"
"v_fma_f32 v12,v12,v11,s13\n"
AW "a125,v12\n"
AR "v12,a145\n"
"v_subrev_f32 v12,s13,v12\n"
"v_fma_f32 v12,v12,v11,s13\n"
AW "a145,v12\n"
AR "v12,a165\n"
"v_subrev_f32 v12,s13,v12\n"
"v_fma_f32 v12,v12,v11,s13\n"
AW "a165,v12\n"
AR "v12,a185\n"
"v_subrev_f32 v12,s13,v12\n"
"v_fma_f32 v12,v12,v11,s13\n"
AW "a185,v12\n"
AR "v12,a205\n"
"v_subrev_f32 v12,s13,v12\n"
"v_fma_f32 v12,v12,v11,s13\n"
AW "a205,v12\n"
AR "v12,a225\n"
"v_subrev_f32 v12,s13,v12\n"
"v_fma_f32 v12,v12,v11,s13\n"
AW "a225,v12\n"
AR "v12,a6\n"
"v_subrev_f32 v12,s13,v12\n"
"v_fma_f32 v12,v12,v11,s13\n"
AW "a6,v12\n"
AR "v12,a26\n"
"v_subrev_f32 v12,s13,v12\n"
"v_fma_f32 v12,v12,v11,s13\n"
AW "a26,v12\n"
AR "v12,a46\n"
"v_subrev_f32 v12,s13,v12\n"
"v_fma_f32 v12,v12,v11,s13\n"
AW "a46,v12\n"
AR "v12,a66\n"
"v_subrev_f32 v12,s13,v12\n"
"v_fma_f32 v12,v12,v11,s13\n"
AW "a66,v12\n"
AR "v12,a86\n"
"v_subrev_f32 v12,s13,v12\n"
"v_fma_f32 v12,v12,v11,s13\n"
AW "a86,v12\n"
AR "v12,a106\n"
"v_subrev_f32 v12,s13,v12\n"
"v_fma_f32 v12,v12,v11,s13\n"
AW "a106,v12\n"
AR "v12,a126\n"
"v_subrev_f32 v12,s13,v12\n"
"v_fma_f32 v12,v12,v11,s13\n"
AW "a126,v12\n"
AR "v12,a146\n"
"v_subrev_f32 v12,s13,v12\n"
"v_fma_f32 v12,v12,v11,s13\n"
AW "a146,v12\n"
AR "v12,a166\n"
"v_subrev_f32 v12,s13,v12\n"
"v_fma_f32 v12,v12,v11,s13\n"
AW "a166,v12\n"
AR "v12,a186\n"
"v_subrev_f32 v12,s13,v12\n"
"v_fma_f32 v12,v12,v11,s13\n"
AW "a186,v12\n"
AR "v12,a206\n"
"v_subrev_f32 v12,s13,v12\n"
"v_fma_f32 v12,v12,v11,s13\n"
AW "a206,v12\n"
AR "v12,a226\n"
"v_subrev_f32 v12,s13,v12\n"
"v_fma_f32 v12,v12,v11,s13\n"
AW "a226,v12\n"
AR "v12,a7\n"
"v_subrev_f32 v12,s13,v12\n"
"v_fma_f32 v12,v12,v11,s13\n"
AW "a7,v12\n"
AR "v12,a27\n"
"v_subrev_f32 v12,s13,v12\n"
"v_fma_f32 v12,v12,v11,s13\n"
AW "a27,v12\n"
AR "v12,a47\n"
"v_subrev_f32 v12,s13,v12\n"
"v_fma_f32 v12,v12,v11,s13\n"
AW "a47,v12\n"
AR "v12,a67\n"
"v_subrev_f32 v12,s13,v12\n"
"v_fma_f32 v12,v12,v11,s13\n"
AW "a67,v12\n"
AR "v12,a87\n"
"v_subrev_f32 v12,s13,v12\n"
"v_fma_f32 v12,v12,v11,s13\n"
AW "a87,v12\n"
AR "v12,a107\n"
"v_subrev_f32 v12,s13,v12\n"
"v_fma_f32 v12,v12,v11,s13\n"
AW "a107,v12\n"
AR "v12,a127\n"
"v_subrev_f32 v12,s13,v12\n"
"v_fma_f32 v12,v12,v11,s13\n"
AW "a127,v12\n"
AR "v12,a147\n"
"v_subrev_f32 v12,s13,v12\n"
"v_fma_f32 v12,v12,v11,s13\n"
AW "a147,v12\n"
AR "v12,a167\n"
"v_subrev_f32 v12,s13,v12\n"
"v_fma_f32 v12,v12,v11,s13\n"
AW "a167,v12\n"
AR "v12,a187\n"
"v_subrev_f32 v12,s13,v12\n"
"v_fma_f32 v12,v12,v11,s13\n"
AW "a187,v12\n"
AR "v12,a207\n"
"v_subrev_f32 v12,s13,v12\n"
"v_fma_f32 v12,v12,v11,s13\n"
AW "a207,v12\n"
AR "v12,a227\n"
"v_subrev_f32 v12,s13,v12\n"
"v_fma_f32 v12,v12,v11,s13\n"
AW "a227,v12\n"
"v_mul_f32 v58,v53,v11\n"
"v_sub_f32 v36,v36,v10\n"
"v_sub_f32 v37,v37,v10\n"
"v_sub_f32 v38,v38,v10\n"
"v_sub_f32 v39,v39,v10\n"
"v_sub_f32 v76,v76,v10\n"
"v_sub_f32 v77,v77,v10\n"
"v_sub_f32 v78,v78,v10\n"
"v_sub_f32 v79,v79,v10\n"
"v_sub_f32 v96,v96,v10\n"
"v_sub_f32 v97,v97,v10\n"
"v_sub_f32 v98,v98,v10\n"
"v_sub_f32 v99,v99,v10\n"
"v_sub_f32 v116,v116,v10\n"
"v_sub_f32 v117,v117,v10\n"
"v_sub_f32 v118,v118,v10\n"
"v_sub_f32 v119,v119,v10\n"
"v_exp_f32 v96,v96\n"
"v_exp_f32 v97,v97\n"
"v_exp_f32 v98,v98\n"
"v_exp_f32 v99,v99\n"
"v_exp_f32 v116,v116\n"
"v_exp_f32 v117,v117\n"
"v_exp_f32 v118,v118\n"
"v_exp_f32 v119,v119\n"
"v_add_f32 v58,v58,v96\n"
"v_add_f32 v58,v58,v97\n"
"v_add_f32 v58,v58,v98\n"
"v_add_f32 v58,v58,v99\n"
"v_add_f32 v58,v58,v116\n"
"v_add_f32 v58,v58,v117\n"
"v_add_f32 v58,v58,v118\n"
"v_add_f32_e64 v58,v58,v119\n"
CV "v96,v96,v97\n"
CV "v97,v98,v99\n"
CV "v98,v116,v117\n"
CV "v99,v118,v119\n"
"v_max3_f32 v10,v100,v101,v102\n"
"v_max3_f32 v11,v103,v120,v121\n"
"v_max3_f32 v10,v10,v122,v123\n"
"v_max_f32 v10,v10,v11\n"
"v_mov_b32 v11,v10\n"
"s_nop 1\n"
"v_permlane16_swap_b32 v10,v11\n"
"v_max_f32 v10,v10,v11\n"
"v_mov_b32 v11,v10\n"
"s_nop 1\n"
"v_permlane32_swap_b32 v10,v11\n"
"v_max_f32 v10,v10,v11\n"
"v_add_f32 v10,0x41300000,v10\n"
"v_max_f32 v10,0,v10\n"
"v_exp_f32 v11,-v10\n"
AR "v12,a8\n"
"v_subrev_f32 v12,s13,v12\n"
"v_fma_f32 v12,v12,v11,s13\n"
AW "a8,v12\n"
AR "v12,a28\n"
"v_subrev_f32 v12,s13,v12\n"
"v_fma_f32 v12,v12,v11,s13\n"
AW "a28,v12\n"
AR "v12,a48\n"
"v_subrev_f32 v12,s13,v12\n"
"v_fma_f32 v12,v12,v11,s13\n"
AW "a48,v12\n"
AR "v12,a68\n"
"v_subrev_f32 v12,s13,v12\n"
"v_fma_f32 v12,v12,v11,s13\n"
AW "a68,v12\n"
AR "v12,a88\n"
"v_subrev_f32 v12,s13,v12\n"
"v_fma_f32 v12,v12,v11,s13\n"
AW "a88,v12\n"
AR "v12,a108\n"
"v_subrev_f32 v12,s13,v12\n"
"v_fma_f32 v12,v12,v11,s13\n"
AW "a108,v12\n"
AR "v12,a128\n"
"v_subrev_f32 v12,s13,v12\n"
"v_fma_f32 v12,v12,v11,s13\n"
AW "a128,v12\n"
AR "v12,a148\n"
"v_subrev_f32 v12,s13,v12\n"
"v_fma_f32 v12,v12,v11,s13\n"
AW "a148,v12\n"
AR "v12,a168\n"
"v_subrev_f32 v12,s13,v12\n"
"v_fma_f32 v12,v12,v11,s13\n"
AW "a168,v12\n"
AR "v12,a188\n"
"v_subrev_f32 v12,s13,v12\n"
"v_fma_f32 v12,v12,v11,s13\n"
AW "a188,v12\n"
AR "v12,a208\n"
"v_subrev_f32 v12,s13,v12\n"
"v_fma_f32 v12,v12,v11,s13\n"
AW "a208,v12\n"
AR "v12,a228\n"
"v_subrev_f32 v12,s13,v12\n"
"v_fma_f32 v12,v12,v11,s13\n"
AW "a228,v12\n"
AR "v12,a9\n"
"v_subrev_f32 v12,s13,v12\n"
"v_fma_f32 v12,v12,v11,s13\n"
AW "a9,v12\n"
AR "v12,a29\n"
"v_subrev_f32 v12,s13,v12\n"
"v_fma_f32 v12,v12,v11,s13\n"
AW "a29,v12\n"
AR "v12,a49\n"
"v_subrev_f32 v12,s13,v12\n"
"v_fma_f32 v12,v12,v11,s13\n"
AW "a49,v12\n"
AR "v12,a69\n"
"v_subrev_f32 v12,s13,v12\n"
"v_fma_f32 v12,v12,v11,s13\n"
AW "a69,v12\n"
AR "v12,a89\n"
"v_subrev_f32 v12,s13,v12\n"
"v_fma_f32 v12,v12,v11,s13\n"
AW "a89,v12\n"
AR "v12,a109\n"
"v_subrev_f32 v12,s13,v12\n"
"v_fma_f32 v12,v12,v11,s13\n"
AW "a109,v12\n"
AR "v12,a129\n"
"v_subrev_f32 v12,s13,v12\n"
"v_fma_f32 v12,v12,v11,s13\n"
AW "a129,v12\n"
AR "v12,a149\n"
"v_subrev_f32 v12,s13,v12\n"
"v_fma_f32 v12,v12,v11,s13\n"
AW "a149,v12\n"
AR "v12,a169\n"
"v_subrev_f32 v12,s13,v12\n"
"v_fma_f32 v12,v12,v11,s13\n"
AW "a169,v12\n"
AR "v12,a189\n"
"v_subrev_f32 v12,s13,v12\n"
"v_fma_f32 v12,v12,v11,s13\n"
AW "a189,v12\n"
AR "v12,a209\n"
"v_subrev_f32 v12,s13,v12\n"
"v_fma_f32 v12,v12,v11,s13\n"
AW "a209,v12\n"
AR "v12,a229\n"
"v_subrev_f32 v12,s13,v12\n"
"v_fma_f32 v12,v12,v11,s13\n"
AW "a229,v12\n"
AR "v12,a10\n"
"v_subrev_f32 v12,s13,v12\n"
"v_fma_f32 v12,v12,v11,s13\n"
AW "a10,v12\n"
AR "v12,a30\n"
"v_subrev_f32 v12,s13,v12\n"
"v_fma_f32 v12,v12,v11,s13\n"
AW "a30,v12\n"
AR "v12,a50\n"
"v_subrev_f32 v12,s13,v12\n"
"v_fma_f32 v12,v12,v11,s13\n"
AW "a50,v12\n"
AR "v12,a70\n"
"v_subrev_f32 v12,s13,v12\n"
"v_fma_f32 v12,v12,v11,s13\n"
AW "a70,v12\n"
AR "v12,a90\n"
"v_subrev_f32 v12,s13,v12\n"
"v_fma_f32 v12,v12,v11,s13\n"
AW "a90,v12\n"
AR "v12,a110\n"
"v_subrev_f32 v12,s13,v12\n"
"v_fma_f32 v12,v12,v11,s13\n"
AW "a110,v12\n"
AR "v12,a130\n"
"v_subrev_f32 v12,s13,v12\n"
"v_fma_f32 v12,v12,v11,s13\n"
AW "a130,v12\n"
AR "v12,a150\n"
"v_subrev_f32 v12,s13,v12\n"
"v_fma_f32 v12,v12,v11,s13\n"
AW "a150,v12\n"
AR "v12,a170\n"
"v_subrev_f32 v12,s13,v12\n"
"v_fma_f32 v12,v12,v11,s13\n"
AW "a170,v12\n"
AR "v12,a190\n"
"v_subrev_f32 v12,s13,v12\n"
"v_fma_f32 v12,v12,v11,s13\n"
AW "a190,v12\n"
AR "v12,a210\n"
"v_subrev_f32 v12,s13,v12\n"
"v_fma_f32 v12,v12,v11,s13\n"
AW "a210,v12\n"
AR "v12,a230\n"
"v_subrev_f32 v12,s13,v12\n"
"v_fma_f32 v12,v12,v11,s13\n"
AW "a230,v12\n"
AR "v12,a11\n"
"v_subrev_f32 v12,s13,v12\n"
"v_fma_f32 v12,v12,v11,s13\n"
AW "a11,v12\n"
AR "v12,a31\n"
"v_subrev_f32 v12,s13,v12\n"
"v_fma_f32 v12,v12,v11,s13\n"
AW "a31,v12\n"
AR "v12,a51\n"
"v_subrev_f32 v12,s13,v12\n"
"v_fma_f32 v12,v12,v11,s13\n"
AW "a51,v12\n"
AR "v12,a71\n"
"v_subrev_f32 v12,s13,v12\n"
"v_fma_f32 v12,v12,v11,s13\n"
AW "a71,v12\n"
AR "v12,a91\n"
"v_subrev_f32 v12,s13,v12\n"
"v_fma_f32 v12,v12,v11,s13\n"
AW "a91,v12\n"
AR "v12,a111\n"
"v_subrev_f32 v12,s13,v12\n"
"v_fma_f32 v12,v12,v11,s13\n"
AW "a111,v12\n"
AR "v12,a131\n"
"v_subrev_f32 v12,s13,v12\n"
"v_fma_f32 v12,v12,v11,s13\n"
AW "a131,v12\n"
AR "v12,a151\n"
"v_subrev_f32 v12,s13,v12\n"
"v_fma_f32 v12,v12,v11,s13\n"
AW "a151,v12\n"
AR "v12,a171\n"
"v_subrev_f32 v12,s13,v12\n"
"v_fma_f32 v12,v12,v11,s13\n"
AW "a171,v12\n"
AR "v12,a191\n"
"v_subrev_f32 v12,s13,v12\n"
"v_fma_f32 v12,v12,v11,s13\n"
AW "a191,v12\n"
AR "v12,a211\n"
"v_subrev_f32 v12,s13,v12\n"
"v_fma_f32 v12,v12,v11,s13\n"
AW "a211,v12\n"
AR "v12,a231\n"
"v_subrev_f32 v12,s13,v12\n"
"v_fma_f32 v12,v12,v11,s13\n"
AW "a231,v12\n"
"v_mul_f32 v59,v54,v11\n"
"v_sub_f32 v40,v40,v10\n"
"v_sub_f32 v41,v41,v10\n"
"v_sub_f32 v42,v42,v10\n"
"v_sub_f32 v43,v43,v10\n"
"v_sub_f32 v80,v80,v10\n"
"v_sub_f32 v81,v81,v10\n"
"v_sub_f32 v82,v82,v10\n"
"v_sub_f32 v83,v83,v10\n"
"v_sub_f32 v100,v100,v10\n"
"v_sub_f32 v101,v101,v10\n"
"v_sub_f32 v102,v102,v10\n"
"v_sub_f32 v103,v103,v10\n"
"v_sub_f32 v120,v120,v10\n"
"v_sub_f32 v121,v121,v10\n"
"v_sub_f32 v122,v122,v10\n"
"v_sub_f32 v123,v123,v10\n"
"v_exp_f32 v100,v100\n"
"v_exp_f32 v101,v101\n"
"v_exp_f32 v102,v102\n"
"v_exp_f32 v103,v103\n"
"v_exp_f32 v120,v120\n"
"v_exp_f32 v121,v121\n"
"v_exp_f32 v122,v122\n"
"v_exp_f32 v123,v123\n"
"v_add_f32 v59,v59,v100\n"
"v_add_f32 v59,v59,v101\n"
"v_add_f32 v59,v59,v102\n"
"v_add_f32 v59,v59,v103\n"
"v_add_f32 v59,v59,v120\n"
"v_add_f32 v59,v59,v121\n"
"v_add_f32 v59,v59,v122\n"
"v_add_f32_e64 v59,v59,v123\n"
CV "v100,v100,v101\n"
CV "v101,v102,v103\n"
CV "v102,v120,v121\n"
CV "v103,v122,v123\n"
"v_max3_f32 v10,v104,v105,v106\n"
"v_max3_f32 v11,v107,v124,v125\n"
"v_max3_f32 v10,v10,v126,v127\n"
"v_max_f32 v10,v10,v11\n"
"v_mov_b32 v11,v10\n"
"s_nop 1\n"
"v_permlane16_swap_b32 v10,v11\n"
"v_max_f32 v10,v10,v11\n"
"v_mov_b32 v11,v10\n"
"s_nop 1\n"
"v_permlane32_swap_b32 v10,v11\n"
"v_max_f32_e64 v10,v10,v11\n"
"v_add_f32 v10,0x41300000,v10\n"
"v_max_f32 v10,0,v10\n"
"v_exp_f32 v11,-v10\n"
AR "v12,a12\n"
"v_subrev_f32 v12,s13,v12\n"
"v_fma_f32 v12,v12,v11,s13\n"
AW "a12,v12\n"
AR "v12,a32\n"
"v_subrev_f32 v12,s13,v12\n"
"v_fma_f32 v12,v12,v11,s13\n"
AW "a32,v12\n"
AR "v12,a52\n"
"v_subrev_f32 v12,s13,v12\n"
"v_fma_f32 v12,v12,v11,s13\n"
AW "a52,v12\n"
AR "v12,a72\n"
"v_subrev_f32 v12,s13,v12\n"
"v_fma_f32 v12,v12,v11,s13\n"
AW "a72,v12\n"
AR "v12,a92\n"
"v_subrev_f32 v12,s13,v12\n"
"v_fma_f32 v12,v12,v11,s13\n"
AW "a92,v12\n"
AR "v12,a112\n"
"v_subrev_f32 v12,s13,v12\n"
"v_fma_f32 v12,v12,v11,s13\n"
AW "a112,v12\n"
AR "v12,a132\n"
"v_subrev_f32 v12,s13,v12\n"
"v_fma_f32 v12,v12,v11,s13\n"
AW "a132,v12\n"
AR "v12,a152\n"
"v_subrev_f32 v12,s13,v12\n"
"v_fma_f32 v12,v12,v11,s13\n"
AW "a152,v12\n"
AR "v12,a172\n"
"v_subrev_f32 v12,s13,v12\n"
"v_fma_f32 v12,v12,v11,s13\n"
AW "a172,v12\n"
AR "v12,a192\n"
"v_subrev_f32 v12,s13,v12\n"
"v_fma_f32 v12,v12,v11,s13\n"
AW "a192,v12\n"
AR "v12,a212\n"
"v_subrev_f32 v12,s13,v12\n"
"v_fma_f32 v12,v12,v11,s13\n"
AW "a212,v12\n"
AR "v12,a232\n"
"v_subrev_f32 v12,s13,v12\n"
"v_fma_f32 v12,v12,v11,s13\n"
AW "a232,v12\n"
AR "v12,a13\n"
"v_subrev_f32 v12,s13,v12\n"
"v_fma_f32 v12,v12,v11,s13\n"
AW "a13,v12\n"
AR "v12,a33\n"
"v_subrev_f32 v12,s13,v12\n"
"v_fma_f32 v12,v12,v11,s13\n"
AW "a33,v12\n"
AR "v12,a53\n"
"v_subrev_f32 v12,s13,v12\n"
"v_fma_f32 v12,v12,v11,s13\n"
AW "a53,v12\n"
AR "v12,a73\n"
"v_subrev_f32 v12,s13,v12\n"
"v_fma_f32 v12,v12,v11,s13\n"
AW "a73,v12\n"
AR "v12,a93\n"
"v_subrev_f32 v12,s13,v12\n"
"v_fma_f32 v12,v12,v11,s13\n"
AW "a93,v12\n"
AR "v12,a113\n"
"v_subrev_f32 v12,s13,v12\n"
"v_fma_f32 v12,v12,v11,s13\n"
AW "a113,v12\n"
AR "v12,a133\n"
"v_subrev_f32 v12,s13,v12\n"
"v_fma_f32 v12,v12,v11,s13\n"
AW "a133,v12\n"
AR "v12,a153\n"
"v_subrev_f32 v12,s13,v12\n"
"v_fma_f32 v12,v12,v11,s13\n"
AW "a153,v12\n"
AR "v12,a173\n"
"v_subrev_f32 v12,s13,v12\n"
"v_fma_f32 v12,v12,v11,s13\n"
AW "a173,v12\n"
AR "v12,a193\n"
"v_subrev_f32 v12,s13,v12\n"
"v_fma_f32 v12,v12,v11,s13\n"
AW "a193,v12\n"
AR "v12,a213\n"
"v_subrev_f32 v12,s13,v12\n"
"v_fma_f32 v12,v12,v11,s13\n"
AW "a213,v12\n"
AR "v12,a233\n"
"v_subrev_f32 v12,s13,v12\n"
"v_fma_f32 v12,v12,v11,s13\n"
AW "a233,v12\n"
AR "v12,a14\n"
"v_subrev_f32 v12,s13,v12\n"
"v_fma_f32 v12,v12,v11,s13\n"
AW "a14,v12\n"
AR "v12,a34\n"
"v_subrev_f32 v12,s13,v12\n"
"v_fma_f32 v12,v12,v11,s13\n"
AW "a34,v12\n"
AR "v12,a54\n"
"v_subrev_f32 v12,s13,v12\n"
"v_fma_f32 v12,v12,v11,s13\n"
AW "a54,v12\n"
AR "v12,a74\n"
"v_subrev_f32 v12,s13,v12\n"
"v_fma_f32 v12,v12,v11,s13\n"
AW "a74,v12\n"
AR "v12,a94\n"
"v_subrev_f32 v12,s13,v12\n"
"v_fma_f32 v12,v12,v11,s13\n"
AW "a94,v12\n"
AR "v12,a114\n"
"v_subrev_f32 v12,s13,v12\n"
"v_fma_f32 v12,v12,v11,s13\n"
AW "a114,v12\n"
AR "v12,a134\n"
"v_subrev_f32 v12,s13,v12\n"
"v_fma_f32 v12,v12,v11,s13\n"
AW "a134,v12\n"
AR "v12,a154\n"
"v_subrev_f32 v12,s13,v12\n"
"v_fma_f32 v12,v12,v11,s13\n"
AW "a154,v12\n"
AR "v12,a174\n"
"v_subrev_f32 v12,s13,v12\n"
"v_fma_f32 v12,v12,v11,s13\n"
AW "a174,v12\n"
AR "v12,a194\n"
"v_subrev_f32 v12,s13,v12\n"
"v_fma_f32 v12,v12,v11,s13\n"
AW "a194,v12\n"
AR "v12,a214\n"
"v_subrev_f32 v12,s13,v12\n"
"v_fma_f32 v12,v12,v11,s13\n"
AW "a214,v12\n"
AR "v12,a234\n"
"v_subrev_f32 v12,s13,v12\n"
"v_fma_f32 v12,v12,v11,s13\n"
AW "a234,v12\n"
AR "v12,a15\n"
"v_subrev_f32 v12,s13,v12\n"
"v_fma_f32 v12,v12,v11,s13\n"
AW "a15,v12\n"
AR "v12,a35\n"
"v_subrev_f32 v12,s13,v12\n"
"v_fma_f32 v12,v12,v11,s13\n"
AW "a35,v12\n"
AR "v12,a55\n"
"v_subrev_f32 v12,s13,v12\n"
"v_fma_f32 v12,v12,v11,s13\n"
AW "a55,v12\n"
AR "v12,a75\n"
"v_subrev_f32 v12,s13,v12\n"
"v_fma_f32 v12,v12,v11,s13\n"
AW "a75,v12\n"
AR "v12,a95\n"
"v_subrev_f32 v12,s13,v12\n"
"v_fma_f32 v12,v12,v11,s13\n"
AW "a95,v12\n"
AR "v12,a115\n"
"v_subrev_f32 v12,s13,v12\n"
"v_fma_f32 v12,v12,v11,s13\n"
AW "a115,v12\n"
AR "v12,a135\n"
"v_subrev_f32 v12,s13,v12\n"
"v_fma_f32 v12,v12,v11,s13\n"
AW "a135,v12\n"
AR "v12,a155\n"
"v_subrev_f32 v12,s13,v12\n"
"v_fma_f32 v12,v12,v11,s13\n"
AW "a155,v12\n"
AR "v12,a175\n"
"v_subrev_f32 v12,s13,v12\n"
"v_fma_f32 v12,v12,v11,s13\n"
AW "a175,v12\n"
AR "v12,a195\n"
"v_subrev_f32 v12,s13,v12\n"
"v_fma_f32 v12,v12,v11,s13\n"
AW "a195,v12\n"
AR "v12,a215\n"
"v_subrev_f32 v12,s13,v12\n"
"v_fma_f32 v12,v12,v11,s13\n"
AW "a215,v12\n"
AR "v12,a235\n"
"v_subrev_f32 v12,s13,v12\n"
"v_fma_f32 v12,v12,v11,s13\n"
AW "a235,v12\n"
"v_mul_f32 v60,v55,v11\n"
"v_sub_f32 v44,v44,v10\n"
"v_sub_f32 v45,v45,v10\n"
"v_sub_f32 v46,v46,v10\n"
"v_sub_f32 v47,v47,v10\n"
"v_sub_f32 v84,v84,v10\n"
"v_sub_f32 v85,v85,v10\n"
"v_sub_f32 v86,v86,v10\n"
"v_sub_f32 v87,v87,v10\n"
"v_sub_f32 v104,v104,v10\n"
"v_sub_f32 v105,v105,v10\n"
"v_sub_f32 v106,v106,v10\n"
"v_sub_f32 v107,v107,v10\n"
"v_sub_f32 v124,v124,v10\n"
"v_sub_f32 v125,v125,v10\n"
"v_sub_f32 v126,v126,v10\n"
"v_sub_f32 v127,v127,v10\n"
"v_exp_f32 v104,v104\n"
"v_exp_f32 v105,v105\n"
"v_exp_f32 v106,v106\n"
"v_exp_f32 v107,v107\n"
"v_exp_f32 v124,v124\n"
"v_exp_f32 v125,v125\n"
"v_exp_f32 v126,v126\n"
"v_exp_f32 v127,v127\n"
"v_add_f32 v60,v60,v104\n"
"v_add_f32 v60,v60,v105\n"
"v_add_f32 v60,v60,v106\n"
"v_add_f32 v60,v60,v107\n"
"v_add_f32 v60,v60,v124\n"
"v_add_f32 v60,v60,v125\n"
"v_add_f32 v60,v60,v126\n"
"v_add_f32 v60,v60,v127\n"
CV "v104,v104,v105\n"
CV "v105,v106,v107\n"
CV "v106,v124,v125\n"
CV "v107,v126,v127\n"
"v_max3_f32 v10,v108,v109,v110\n"
"v_max3_f32 v11,v111,v128,v129\n"
"v_max3_f32 v10,v10,v130,v131\n"
"v_max_f32 v10,v10,v11\n"
"v_mov_b32 v11,v10\n"
"s_nop 1\n"
"v_permlane16_swap_b32 v10,v11\n"
"v_max_f32 v10,v10,v11\n"
"v_mov_b32 v11,v10\n"
"s_nop 1\n"
"v_permlane32_swap_b32 v10,v11\n"
"v_max_f32 v10,v10,v11\n"
"v_add_f32 v10,0x41300000,v10\n"
"v_max_f32 v10,0,v10\n"
"v_exp_f32 v11,-v10\n"
AR "v12,a16\n"
"v_subrev_f32 v12,s13,v12\n"
"v_fma_f32 v12,v12,v11,s13\n"
AW "a16,v12\n"
AR "v12,a36\n"
"v_subrev_f32 v12,s13,v12\n"
"v_fma_f32 v12,v12,v11,s13\n"
AW "a36,v12\n"
AR "v12,a56\n"
"v_subrev_f32 v12,s13,v12\n"
"v_fma_f32 v12,v12,v11,s13\n"
AW "a56,v12\n"
AR "v12,a76\n"
"v_subrev_f32 v12,s13,v12\n"
"v_fma_f32 v12,v12,v11,s13\n"
AW "a76,v12\n"
AR "v12,a96\n"
"v_subrev_f32 v12,s13,v12\n"
"v_fma_f32 v12,v12,v11,s13\n"
AW "a96,v12\n"
AR "v12,a116\n"
"v_subrev_f32 v12,s13,v12\n"
"v_fma_f32 v12,v12,v11,s13\n"
AW "a116,v12\n"
AR "v12,a136\n"
"v_subrev_f32 v12,s13,v12\n"
"v_fma_f32 v12,v12,v11,s13\n"
AW "a136,v12\n"
AR "v12,a156\n"
"v_subrev_f32 v12,s13,v12\n"
"v_fma_f32 v12,v12,v11,s13\n"
AW "a156,v12\n"
AR "v12,a176\n"
"v_subrev_f32 v12,s13,v12\n"
"v_fma_f32 v12,v12,v11,s13\n"
AW "a176,v12\n"
AR "v12,a196\n"
"v_subrev_f32 v12,s13,v12\n"
"v_fma_f32 v12,v12,v11,s13\n"
AW "a196,v12\n"
AR "v12,a216\n"
"v_subrev_f32 v12,s13,v12\n"
"v_fma_f32 v12,v12,v11,s13\n"
AW "a216,v12\n"
AR "v12,a236\n"
"v_subrev_f32 v12,s13,v12\n"
"v_fma_f32 v12,v12,v11,s13\n"
AW "a236,v12\n"
AR "v12,a17\n"
"v_subrev_f32 v12,s13,v12\n"
"v_fma_f32 v12,v12,v11,s13\n"
AW "a17,v12\n"
AR "v12,a37\n"
"v_subrev_f32 v12,s13,v12\n"
"v_fma_f32 v12,v12,v11,s13\n"
AW "a37,v12\n"
AR "v12,a57\n"
"v_subrev_f32 v12,s13,v12\n"
"v_fma_f32 v12,v12,v11,s13\n"
AW "a57,v12\n"
AR "v12,a77\n"
"v_subrev_f32 v12,s13,v12\n"
"v_fma_f32 v12,v12,v11,s13\n"
AW "a77,v12\n"
AR "v12,a97\n"
"v_subrev_f32 v12,s13,v12\n"
"v_fma_f32 v12,v12,v11,s13\n"
AW "a97,v12\n"
AR "v12,a117\n"
"v_subrev_f32 v12,s13,v12\n"
"v_fma_f32 v12,v12,v11,s13\n"
AW "a117,v12\n"
AR "v12,a137\n"
"v_subrev_f32 v12,s13,v12\n"
"v_fma_f32 v12,v12,v11,s13\n"
AW "a137,v12\n"
AR "v12,a157\n"
"v_subrev_f32 v12,s13,v12\n"
"v_fma_f32 v12,v12,v11,s13\n"
AW "a157,v12\n"
AR "v12,a177\n"
"v_subrev_f32 v12,s13,v12\n"
"v_fma_f32 v12,v12,v11,s13\n"
AW "a177,v12\n"
AR "v12,a197\n"
"v_subrev_f32 v12,s13,v12\n"
"v_fma_f32 v12,v12,v11,s13\n"
AW "a197,v12\n"
AR "v12,a217\n"
"v_subrev_f32 v12,s13,v12\n"
"v_fma_f32 v12,v12,v11,s13\n"
AW "a217,v12\n"
AR "v12,a237\n"
"v_subrev_f32 v12,s13,v12\n"
"v_fma_f32 v12,v12,v11,s13\n"
AW "a237,v12\n"
AR "v12,a18\n"
"v_subrev_f32 v12,s13,v12\n"
"v_fma_f32 v12,v12,v11,s13\n"
AW "a18,v12\n"
AR "v12,a38\n"
"v_subrev_f32 v12,s13,v12\n"
"v_fma_f32 v12,v12,v11,s13\n"
AW "a38,v12\n"
AR "v12,a58\n"
"v_subrev_f32 v12,s13,v12\n"
"v_fma_f32 v12,v12,v11,s13\n"
AW "a58,v12\n"
AR "v12,a78\n"
"v_subrev_f32 v12,s13,v12\n"
"v_fma_f32 v12,v12,v11,s13\n"
AW "a78,v12\n"
AR "v12,a98\n"
"v_subrev_f32 v12,s13,v12\n"
"v_fma_f32 v12,v12,v11,s13\n"
AW "a98,v12\n"
AR "v12,a118\n"
"v_subrev_f32 v12,s13,v12\n"
"v_fma_f32 v12,v12,v11,s13\n"
AW "a118,v12\n"
AR "v12,a138\n"
"v_subrev_f32 v12,s13,v12\n"
"v_fma_f32 v12,v12,v11,s13\n"
AW "a138,v12\n"
AR "v12,a158\n"
"v_subrev_f32 v12,s13,v12\n"
"v_fma_f32 v12,v12,v11,s13\n"
AW "a158,v12\n"
AR "v12,a178\n"
"v_subrev_f32 v12,s13,v12\n"
"v_fma_f32 v12,v12,v11,s13\n"
AW "a178,v12\n"
AR "v12,a198\n"
"v_subrev_f32 v12,s13,v12\n"
"v_fma_f32 v12,v12,v11,s13\n"
AW "a198,v12\n"
AR "v12,a218\n"
"v_subrev_f32 v12,s13,v12\n"
"v_fma_f32 v12,v12,v11,s13\n"
AW "a218,v12\n"
AR "v12,a238\n"
"v_subrev_f32 v12,s13,v12\n"
"v_fma_f32 v12,v12,v11,s13\n"
AW "a238,v12\n"
AR "v12,a19\n"
"v_subrev_f32 v12,s13,v12\n"
"v_fma_f32 v12,v12,v11,s13\n"
AW "a19,v12\n"
AR "v12,a39\n"
"v_subrev_f32 v12,s13,v12\n"
"v_fma_f32 v12,v12,v11,s13\n"
AW "a39,v12\n"
AR "v12,a59\n"
"v_subrev_f32 v12,s13,v12\n"
"v_fma_f32 v12,v12,v11,s13\n"
AW "a59,v12\n"
AR "v12,a79\n"
"v_subrev_f32 v12,s13,v12\n"
"v_fma_f32 v12,v12,v11,s13\n"
AW "a79,v12\n"
AR "v12,a99\n"
"v_subrev_f32 v12,s13,v12\n"
"v_fma_f32 v12,v12,v11,s13\n"
AW "a99,v12\n"
AR "v12,a119\n"
"v_subrev_f32 v12,s13,v12\n"
"v_fma_f32 v12,v12,v11,s13\n"
AW "a119,v12\n"
AR "v12,a139\n"
"v_subrev_f32 v12,s13,v12\n"
"v_fma_f32 v12,v12,v11,s13\n"
AW "a139,v12\n"
AR "v12,a159\n"
"v_subrev_f32 v12,s13,v12\n"
"v_fma_f32 v12,v12,v11,s13\n"
AW "a159,v12\n"
AR "v12,a179\n"
"v_subrev_f32 v12,s13,v12\n"
"v_fma_f32 v12,v12,v11,s13\n"
AW "a179,v12\n"
AR "v12,a199\n"
"v_subrev_f32 v12,s13,v12\n"
"v_fma_f32 v12,v12,v11,s13\n"
AW "a199,v12\n"
AR "v12,a219\n"
"v_subrev_f32 v12,s13,v12\n"
"v_fma_f32 v12,v12,v11,s13\n"
AW "a219,v12\n"
AR "v12,a239\n"
"v_subrev_f32 v12,s13,v12\n"
"v_fma_f32 v12,v12,v11,s13\n"
AW "a239,v12\n"
"v_mul_f32 v61,v56,v11\n"
"v_sub_f32 v48,v48,v10\n"
"v_sub_f32 v49,v49,v10\n"
"v_sub_f32 v50,v50,v10\n"
"v_sub_f32 v51,v51,v10\n"
"v_sub_f32 v88,v88,v10\n"
"v_sub_f32 v89,v89,v10\n"
"v_sub_f32 v90,v90,v10\n"
"v_sub_f32 v91,v91,v10\n"
"v_sub_f32 v108,v108,v10\n"
"v_sub_f32 v109,v109,v10\n"
"v_sub_f32 v110,v110,v10\n"
"v_sub_f32 v111,v111,v10\n"
"v_sub_f32 v128,v128,v10\n"
"v_sub_f32 v129,v129,v10\n"
"v_sub_f32 v130,v130,v10\n"
"v_sub_f32 v131,v131,v10\n"
"v_exp_f32 v108,v108\n"
"v_exp_f32 v109,v109\n"
"v_exp_f32 v110,v110\n"
"v_exp_f32 v111,v111\n"
"v_exp_f32 v128,v128\n"
"v_exp_f32 v129,v129\n"
"v_exp_f32 v130,v130\n"
"v_exp_f32 v131,v131\n"
"v_add_f32 v61,v61,v108\n"
"v_add_f32 v61,v61,v109\n"
"v_add_f32 v61,v61,v110\n"
"v_add_f32 v61,v61,v111\n"
"v_add_f32 v61,v61,v128\n"
"v_add_f32 v61,v61,v129\n"
"v_add_f32 v61,v61,v130\n"
"v_add_f32_e64 v61,v61,v131\n"
CV "v108,v108,v109\n"
CV "v109,v110,v111\n"
CV "v110,v128,v129\n"
CV "v111,v130,v131\n"
"s_nop 7\n"
"s_setpc_b64 s[52:53]\n"
".Lackspin_q:\n"
"s_cmp_eq_u32 s98,0\n"
"s_cbranch_scc1 .Lackok_q\n"
"s_sub_u32 s98,s98,1\n"
"s_sleep 8\n"
"v_bfe_u32 v14,v9,4,5\n"
"v_lshlrev_b32 v14,6,v14\n"
"s_lshl_b32 s58,s78,2\n"
"s_add_u32 s58,s58,s86\n"
"buffer_load_dword v10,v14,s[40:43],s58 offen offset:128 sc1\n"
"s_waitcnt vmcnt(0)\n"
"s_branch .Lack_q\n"
".Lcode1:\n"
".Ldone:\n"
    : "+{v0}"(tid), "+{s23}"(bid), "+{s[20:21]}"(ka)
    :
    : "v1", "v2", "v3", "v4", "v5", "v6", "v7", "v8", "v9", "v10", "v11", "v12", "v13", "v14", "v15", "v16", "v17", "v18", "v19", "v20", "v21", "v22", "v23", "v24", "v25", "v26", "v27", "v28", "v29", "v30", "v31", "v32", "v33", "v34", "v35", "v36", "v37", "v38", "v39", "v40", "v41", "v42", "v43", "v44", "v45", "v46", "v47", "v48", "v49", "v50", "v51", "v52", "v53", "v54", "v55", "v56", "v57", "v58", "v59", "v60", "v61", "v62", "v63", "v64", "v65", "v66", "v67", "v68", "v69", "v70", "v71", "v72", "v73", "v74", "v75", "v76", "v77", "v78", "v79", "v80", "v81", "v82", "v83", "v84", "v85", "v86", "v87", "v88", "v89", "v90", "v91", "v92", "v93", "v94", "v95", "v96", "v97", "v98", "v99", "v100", "v101", "v102", "v103", "v104", "v105", "v106", "v107", "v108", "v109", "v110", "v111", "v112", "v113", "v114", "v115", "v116", "v117", "v118", "v119", "v120", "v121", "v122", "v123", "v124", "v125", "v126", "v127", "v128", "v129", "v130", "v131", "v132", "v133", "v134", "v135", "v136", "v137", "v138", "v139", "v140", "v141", "v142", "v143", "v144", "v145", "v146", "v147", "v148", "v149", "v150", "v151", "v152", "v153", "v154", "v155", "v156", "v157", "v158", "v159", "v160", "v161", "v162", "v163", "v164", "v165", "v166", "v167", "v168", "v169", "v170", "v171", "v172", "v173", "v174", "v175", "v176", "v177", "v178", "v179", "v180", "v181", "v182", "v183", "v184", "v185", "v186", "v187", "v188", "v189", "v190", "v191", "v192", "v193", "v194", "v195", "v196", "v197", "v198", "v199", "v200", "v201", "v202", "v203", "v204", "v205", "v206", "v207", "v208", "v209", "v210", "v211", "v212", "v213", "v214", "v215", "v216", "v217", "v218", "v219", "v220", "v221", "v222", "v223", "v224", "v225", "v226", "v227", "v228", "v229", "v230", "v231", "v232", "v233", "v234", "v235", "v236", "v237", "v238", "v239", "v240", "v241", "v242", "v243", "v244", "v245", "v246", "v247", "v248", "v249", "v250", "v251", "v252", "v253", "v254", "v255", "a0", "a1", "a2", "a3", "a4", "a5", "a6", "a7", "a8", "a9", "a10", "a11", "a12", "a13", "a14", "a15", "a16", "a17", "a18", "a19", "a20", "a21", "a22", "a23", "a24", "a25", "a26", "a27", "a28", "a29", "a30", "a31", "a32", "a33", "a34", "a35", "a36", "a37", "a38", "a39", "a40", "a41", "a42", "a43", "a44", "a45", "a46", "a47", "a48", "a49", "a50", "a51", "a52", "a53", "a54", "a55", "a56", "a57", "a58", "a59", "a60", "a61", "a62", "a63", "a64", "a65", "a66", "a67", "a68", "a69", "a70", "a71", "a72", "a73", "a74", "a75", "a76", "a77", "a78", "a79", "a80", "a81", "a82", "a83", "a84", "a85", "a86", "a87", "a88", "a89", "a90", "a91", "a92", "a93", "a94", "a95", "a96", "a97", "a98", "a99", "a100", "a101", "a102", "a103", "a104", "a105", "a106", "a107", "a108", "a109", "a110", "a111", "a112", "a113", "a114", "a115", "a116", "a117", "a118", "a119", "a120", "a121", "a122", "a123", "a124", "a125", "a126", "a127", "a128", "a129", "a130", "a131", "a132", "a133", "a134", "a135", "a136", "a137", "a138", "a139", "a140", "a141", "a142", "a143", "a144", "a145", "a146", "a147", "a148", "a149", "a150", "a151", "a152", "a153", "a154", "a155", "a156", "a157", "a158", "a159", "a160", "a161", "a162", "a163", "a164", "a165", "a166", "a167", "a168", "a169", "a170", "a171", "a172", "a173", "a174", "a175", "a176", "a177", "a178", "a179", "a180", "a181", "a182", "a183", "a184", "a185", "a186", "a187", "a188", "a189", "a190", "a191", "a192", "a193", "a194", "a195", "a196", "a197", "a198", "a199", "a200", "a201", "a202", "a203", "a204", "a205", "a206", "a207", "a208", "a209", "a210", "a211", "a212", "a213", "a214", "a215", "a216", "a217", "a218", "a219", "a220", "a221", "a222", "a223", "a224", "a225", "a226", "a227", "a228", "a229", "a230", "a231", "a232", "a233", "a234", "a235", "a236", "a237", "a238", "a239", "a240", "a241", "a242", "a243", "a244", "a245", "a246", "a247", "a248", "a249", "a250", "a251", "a252", "a253", "a254", "a255", "s0", "s1", "s2", "s3", "s4", "s5", "s6", "s7", "s8", "s9", "s10", "s11", "s12", "s13", "s14", "s15", "s16", "s17", "s18", "s19", "s22", "s24", "s25", "s26", "s27", "s28", "s29", "s30", "s31", "s32", "s33", "s34", "s35", "s36", "s37", "s38", "s39", "s40", "s41", "s42", "s43", "s44", "s45", "s46", "s47", "s48", "s49", "s50", "s51", "s52", "s53", "s54", "s55", "s56", "s57", "s58", "s59", "s60", "s61", "s62", "s63", "s64", "s65", "s66", "s67", "s68", "s69", "s70", "s71", "s72", "s73", "s74", "s75", "s76", "s77", "s78", "s79", "s80", "s81", "s82", "s83", "s84", "s85", "s86", "s87", "s88", "s89", "s90", "s91", "s92", "s93", "s94", "s95", "s96", "s97", "s98", "s99", "s100", "s101", "vcc", "scc", "memory");
}

extern "C" void kernel_launch(void* const* d_in, const int* in_sizes, int n_in, void* d_out, int out_size, void* d_ws, size_t ws_size, hipStream_t stream) {
  Ptrs P;
  for (int br = 0; br < 2; ++br) { P.x[br] = d_in[5 * br]; P.k[br] = d_in[5 * br + 1]; P.v[br] = d_in[5 * br + 2]; P.wq[br] = d_in[5 * br + 3]; P.wo[br] = d_in[5 * br + 4]; }
  (void)hipFuncSetAttribute((const void*)fused_kernel, hipFuncAttributeMaxDynamicSharedMemorySize, LDS1);
  fused_kernel<<<256, 256, LDS1, stream>>>(P, (char*)d_ws, (char*)d_out);
}
```

```cpp
#include <hip/hip_runtime.h>
#include <hip/hip_bf16.h>

#define MF "v_mfma_f32_16x16x32_bf16 "
#define TR "ds_read_b64_tr_b16 "
#define AR "v_accvgpr_read_b32 "
#define AW "v_accvgpr_write_b32 "
#define CV "v_cvt_pk_bf16_f32 "
#define LD "buffer_load_dwordx4 "
constexpr unsigned LDS1 = 160 * 1024;

struct Ptrs { const void* x[2]; const void* k[2]; const void* v[2]; const void* wq[2]; const void* wo[2]; };

__global__ __launch_bounds__(256, 1) __attribute__((aligned(4096))) void fused_kernel(Ptrs P, char* __restrict__ ws, char* __restrict__ out) {
  unsigned tid = threadIdx.x, bid = blockIdx.x;
  unsigned long long ka = (unsigned long long)__builtin_amdgcn_kernarg_segment_ptr();
  asm volatile(
".p2align 5\n"
".Lcode0:\n"
"s_and_b32 s58,s23,4\n"
"s_lshl_b32 s58,s58,1\n"
"s_load_dwordx2 s[28:29],s[20:21],s58 offset:48\n"
"s_load_dwordx2 s[24:25],s[20:21],s58\n"
"s_load_dwordx2 s[32:33],s[20:21],s58 offset:16\n"
"s_load_dwordx2 s[36:37],s[20:21],s58 offset:32\n"
"s_load_dwordx2 s[48:49],s[20:21],s58 offset:64\n"
"s_memtime s[100:101]\n"
"s_load_dwordx2 s[40:41],s[20:21],0x50\n"
"s_load_dwordx2 s[44:45],s[20:21],0x58\n"
"v_readfirstlane_b32 s22,v0\n"
"v_and_b32 v9,63,v0\n"
"v_bfe_u32 v10,v0,3,3\n"
"v_and_b32 v11,7,v0\n"
"s_lshr_b32 s22,s22,6\n"
"s_or_b32 s22,s22,s58\n"
"s_and_b32 s58,s23,1\n"
"s_lshl_b32 s58,s58,3\n"
"s_lshr_b32 s59,s23,5\n"
"s_add_u32 s59,s59,s58\n"
"s_mul_i32 s59,s59,4112\n"
"s_or_b32 s22,s22,s59\n"
"s_and_b32 s59,s23,2\n"
"s_lshl_b32 s59,s59,1\n"
"s_bfe_u32 s60,s23,0x20003\n"
"s_or_b32 s59,s59,s60\n"
"s_lshl_b32 s60,s59,20\n"
"s_or_b32 s22,s22,s60\n"
"s_or_b32 s59,s59,s58\n"
"s_mul_i32 s59,s59,65792\n"
"s_xor_b32 s59,s59,524288\n"
"s_or_b32 s22,s22,s59\n"
"v_lshrrev_b32 v6,2,v0\n"
"v_and_b32 v6,4,v6\n"
"v_xor_b32 v6,v6,v11\n"
"v_lshlrev_b32 v6,4,v6\n"
"s_mov_b32 s58,12288\n"
"v_mad_u32_u24 v6,v10,s58,v6\n"
"v_bfe_u32 v4,v0,4,3\n"
"v_xor_b32 v4,v4,v11\n"
"v_lshlrev_b32 v4,4,v4\n"
"s_mov_b32 s58,1280\n"
"v_mad_u32_u24 v4,v10,s58,v4\n"
"v_lshlrev_b32 v9,4,v9\n"
"s_mov_b32 s26,3276800\n"
"s_mov_b32 s27,0x00020000\n"
"s_mov_b32 s30,7864320\n"
"s_mov_b32 s31,0x00020000\n"
"s_mov_b32 s34,0\n"
"s_mov_b32 s35,0x00020000\n"
"s_mov_b32 s38,0\n"
"s_mov_b32 s39,0x00020000\n"
"s_mov_b32 s42,268435456\n"
"s_mov_b32 s43,0x00020000\n"
"s_mov_b32 s46,6553600\n"
"s_mov_b32 s47,0x00020000\n"
"s_mov_b32 s50,7864320\n"
"s_mov_b32 s51,0x00020000\n"
"s_mov_b32 s98,20000\n"
"s_bfe_u32 s56,s22,0x20000\n"
"s_bfe_u32 s72,s22,0x10003\n"
"s_lshr_b32 s73,s56,1\n"
"s_and_b32 s74,s56,1\n"
"s_mul_i32 s62,s56,4096\n"
"s_mul_i32 s63,s56,5120\n"
"s_add_u32 s63,s63,98304\n"
"s_bfe_u32 s58,s22,0x40004\n"
"s_bfe_u32 s59,s22,0x40008\n"
"s_mul_i32 s68,s58,204800\n"
"s_mul_i32 s60,s56,51200\n"
"s_add_u32 s68,s68,s60\n"
"s_lshl_b32 s59,s59,1\n"
"s_add_u32 s59,s59,s73\n"
"s_mul_i32 s66,s59,384\n"
"s_mul_i32 s60,s74,393216\n"
"s_add_u32 s66,s66,s60\n"
"s_lshr_b32 s58,s22,6\n"
"s_xor_b32 s58,s58,s22\n"
"s_bfe_u32 s58,s58,0x10004\n"
"s_mul_i32 s81,s58,640\n"
"s_mul_i32 s60,s81,6144\n"
"s_add_u32 s66,s66,s60\n"
"v_xor_b32 v19,64,v4\n"
"v_add_u32 v16,97280,v6\n"
"v_add_u32 v17,194560,v6\n"
"v_add_u32 v18,291840,v6\n"
"s_waitcnt lgkmcnt(0)\n"
"s_mov_b32 s80,s100\n"
"s_mov_b32 s87,2\n"
"s_add_u32 m0,s62,0\n"
"s_add_u32 s82,s66,0\n"
"s_sub_u32 s15,s82,7864320\n"
"s_min_u32 s82,s82,s15\n"
LD "v6,s[28:31],s82 offen lds\n"
LD "v16,s[28:31],s82 offen offset:1024 lds\n"
LD "v17,s[28:31],s82 offen offset:2048 lds\n"
LD "v18,s[28:31],s82 offen offset:3072 lds\n"
"s_add_u32 s14,s81,0\n"
"s_sub_u32 s15,s14,1280\n"
"s_min_u32 s14,s14,s15\n"
"s_add_u32 s14,s14,s68\n"
"s_add_u32 s84,s14,0\n"
"s_add_u32 m0,s63,0\n"
"s_nop 0\n"
LD "v4,s[24:27],s84 offen lds\n"
"s_add_u32 s84,s14,9216\n"
LD "v19,s[24:27],s84 offen offset:1024 lds\n"
"s_add_u32 s84,s14,18432\n"
LD "v4,s[24:27],s84 offen offset:2048 lds\n"
"s_add_u32 s84,s14,27648\n"
LD "v19,s[24:27],s84 offen offset:3072 lds\n"
"s_add_u32 s84,s14,40960\n"
"s_add_u32 m0,s63,4096\n"
"s_nop 0\n"
LD "v4,s[24:27],s84 offen lds\n"
"s_lshr_b32 s58,s23,3\n"
"s_mul_i32 s59,s58,7\n"
"s_and_b32 s60,s23,7\n"
"s_mul_i32 s60,s60,3\n"
"s_add_u32 s59,s59,s60\n"
"s_and_b32 s59,s59,15\n"
"s_lshl_b32 s59,s59,8\n"
"s_or_b32 s58,s58,s59\n"
"s_and_b32 s59,s23,3\n"
"s_lshl_b32 s59,s59,5\n"
"s_or_b32 s58,s58,s59\n"
"s_bfe_u32 s59,s23,0x10001\n"
"s_lshl_b32 s59,s59,12\n"
"s_or_b32 s58,s58,s59\n"
"s_lshl_b32 s23,s23,16\n"
"s_or_b32 s23,s23,s58\n"
"v_bfe_u32 v20,v0,4,2\n"
"v_mul_u32_u24 v21,384,v10\n"
"v_xor_b32_e64 v22,v11,v20\n"
"v_lshl_add_u32 v7,v22,4,v21\n"
"s_add_u32 m0,s62,16384\n"
"s_add_u32 s82,s66,128\n"
"s_sub_u32 s15,s82,7864320\n"
"s_min_u32 s82,s82,s15\n"
LD "v6,s[28:31],s82 offen lds\n"
"s_lshl_b32 s58,s74,6\n"
"v_xor_b32 v7,s58,v7\n"
"v_lshlrev_b32 v22,1,v20\n"
"v_xor_b32 v22,v11,v22\n"
"v_lshl_add_u32 v8,v22,4,v21\n"
"v_bfe_u32 v21,v0,1,3\n"
"v_xor_b32 v21,v21,v20\n"
"v_lshlrev_b32_e64 v21,4,v21\n"
"v_lshl_add_u32 v21,v11,7,v21\n"
"v_bfe_u32 v22,v0,3,1\n"
"s_mul_i32 s58,s73,12288\n"
"v_mul_u32_u24 v23,3072,v22\n"
"v_add3_u32 v1,v23,v21,s58\n"
"s_mul_i32 s59,s74,10240\n"
"s_add_u32 s59,s59,98304\n"
"v_lshl_add_u32 v2,v22,10,v21\n"
"v_add_u32 v2,s59,v2\n"
"v_bfe_u32 v21,v0,2,3\n"
"v_lshlrev_b32 v21,7,v21\n"
"v_bfe_u32 v23,v0,3,2\n"
LD "v16,s[28:31],s82 offen offset:1024 lds\n"
"v_lshl_add_u32 v21,v23,5,v21\n"
"v_and_b32_e64 v23,3,v0\n"
"v_lshl_add_u32 v21,v23,3,v21\n"
"v_bfe_u32 v23,v0,5,1\n"
"v_mul_u32_u24 v3,3072,v23\n"
"v_add3_u32 v3,v3,v21,s58\n"
"s_bfe_u32 s58,s22,0x40004\n"
"s_mul_i32 s58,s58,409600\n"
"s_mul_i32 s59,s72,1280\n"
"s_add_u32 s58,s58,s59\n"
"v_and_b32 v21,15,v0\n"
"v_mul_u32_u24 v21,2560,v21\n"
"v_lshl_add_u32 v21,v23,4,v21\n"
"v_bfe_u32 v23,v0,4,1\n"
"v_lshl_add_u32 v21,v23,5,v21\n"
"v_add_u32 v12,s58,v21\n"
"v_bfe_u32 v21,v0,2,2\n"
"v_lshlrev_b32 v21,7,v21\n"
"v_lshl_add_u32 v21,v20,10,v21\n"
"v_lshl_add_u32 v21,v22,6,v21\n"
LD "v17,s[28:31],s82 offen offset:2048 lds\n"
"v_and_b32_e64 v23,3,v0\n"
"v_lshl_add_u32 v21,v23,4,v21\n"
"s_lshl_b32 s58,s73,13\n"
"v_add_u32 v0,s58,v21\n"
"s_getpc_b64 s[52:53]\n"
".Lcts:\n"
"s_sub_u32 s52,s52,.Lcts-.Lcode0\n"
"s_subb_u32 s53,s53,0\n"
"s_bfe_u32 s58,s23,0x30000\n"
"s_lshl_b32 s58,s58,2\n"
"s_add_u32 s58,s58,s56\n"
"s_lshl_b32 s58,s58,13\n"
"v_lshlrev_b32 v15,3,v9\n"
"v_add_u32_e64 v15,s58,v15\n"
"v_min_u32 v15,.Lcode1-.Lcode0,v15\n"
"s_bfe_u32 s58,s22,0x4000c\n"
"s_bfe_u32 s59,s22,0x40010\n"
"s_mul_i32 s69,s58,204800\n"
"s_mul_i32 s60,s56,51200\n"
LD "v18,s[28:31],s82 offen offset:3072 lds\n"
"s_add_u32 s69,s69,s60\n"
"s_lshl_b32 s59,s59,1\n"
"s_add_u32 s59,s59,s73\n"
"s_mul_i32 s67,s59,384\n"
"s_mul_i32 s60,s74,393216\n"
"s_add_u32 s67,s67,s60\n"
"s_mul_i32 s60,s81,6144\n"
"s_add_u32 s67,s67,s60\n"
"s_bfe_u32 s58,s22,0x40004\n"
"s_bfe_u32 s59,s22,0x40008\n"
"s_lshl_b32 s59,s59,1\n"
"s_add_u32 s88,s59,s74\n"
"s_mul_i32 s88,s88,245760\n"
"s_add_u32 s59,s59,s73\n"
"s_lshl_b32 s70,s58,5\n"
"s_add_u32 s70,s70,s59\n"
"s_mul_i32 s70,s70,196608\n"
"s_bfe_u32 s58,s22,0x4000c\n"
"s_bfe_u32 s59,s22,0x40010\n"
"s_lshl_b32 s59,s59,1\n"
"s_add_u32 m0,s62,32768\n"
"s_add_u32 s82,s66,256\n"
"s_sub_u32 s15,s82,7864320\n"
"s_min_u32 s82,s82,s15\n"
LD "v6,s[28:31],s82 offen lds\n"
"s_add_u32 s89,s59,s74\n"
"s_mul_i32 s89,s89,245760\n"
"s_add_u32 s59,s59,s73\n"
"s_lshl_b32 s71,s58,5\n"
"s_add_u32 s71,s71,s59\n"
"s_mul_i32 s71,s71,196608\n"
"s_bitcmp1_b32 s23,12\n"
"s_cselect_b32 s58,s71,s70\n"
"s_cselect_b32 s71,s70,s71\n"
"s_mov_b32 s70,s58\n"
"s_cselect_b32 s58,s89,s88\n"
"s_cselect_b32 s89,s88,s89\n"
"s_mov_b32 s88,s58\n"
"s_cselect_b32 s69,98,1\n"
"s_mov_b32 s54,1037384597\n"
"s_mov_b32 s55,1037384597\n"
"s_bfe_u32 s58,s22,0x40004\n"
"s_lshl_b32 s59,s72,4\n"
"s_add_u32 s59,s59,s58\n"
"s_lshl_b32 s86,s59,12\n"
LD "v16,s[28:31],s82 offen offset:1024 lds\n"
"s_add_u32 s86,s86,100663296\n"
"s_mul_i32 s93,s59,1638400\n"
"s_add_u32 s93,s93,33554432\n"
"s_bfe_u32 s58,s22,0x30014\n"
"s_lshl_b32 s78,s58,1\n"
"s_add_u32 s78,s78,s74\n"
"s_lshl_b32 s94,s78,2\n"
"s_add_u32 s94,s94,s86\n"
"s_lshl_b32 s92,s58,2\n"
"s_lshr_b32 s92,0x31752064,s92\n"
"s_and_b32 s92,s92,7\n"
"s_lshl_b32 s92,s92,2\n"
"s_add_u32 s92,s92,s56\n"
"s_mul_i32 s91,s58,204800\n"
"s_add_u32 s91,s91,s93\n"
"s_mul_i32 s75,s56,30720\n"
"s_add_u32 s75,s75,s91\n"
"s_add_u32 s75,s75,0\n"
"s_mul_i32 s58,s74,5120\n"
"s_add_u32 s91,s91,s58\n"
LD "v17,s[28:31],s82 offen offset:2048 lds\n"
"s_mov_b32 s90,0xf0000000\n"
"s_mov_b32 s77,0xf0000000\n"
"s_mov_b32 s85,0xf0000000\n"
AW "a240,v0\n"
AW "a244,v1\n"
AW "a241,v2\n"
AW "a245,v3\n"
AW "a242,v4\n"
AW "a243,v6\n"
AW "a252,v12\n"
"s_mov_b32 s57,0\n"
"s_mov_b32 s83,0\n"
LD "v18,s[28:31],s82 offen offset:3072 lds\n"
"global_load_dword a249,v15,s[52:53]\n"
AR "v0,a240\n"
AR "v2,a241\n"
AR "v4,a242\n"
AR "v6,a243\n"
"v_xor_b32 v1,64,v0\n"
"v_xor_b32 v3,64,v2\n"
"v_xor_b32 v5,64,v4\n"
"v_add_u32 v11,97280,v6\n"
"v_add_u32 v12,194560,v6\n"
"v_add_u32 v13,291840,v6\n"
"s_mov_b64 vcc,0\n"
"s_waitcnt vmcnt(9) lgkmcnt(0)\n"
"s_barrier\n"
TR "a[200:201],v0\n"
TR "a[202:203],v0 offset:512\n"
TR "a[204:205],v0 offset:4096\n"
TR "a[206:207],v0 offset:4608\n"
"ds_read_b128 a[60:63],v2\n"
"ds_read_b128 a[100:103],v3\n"
"ds_read_b128 a[64:67],v2 offset:2048\n"
"ds_read_b128 a[104:107],v3 offset:2048\n"
"ds_read_b128 a[68:71],v2 offset:4096\n"
"ds_read_b128 a[108:111],v3 offset:4096\n"
"ds_read_b128 a[72:75],v2 offset:6144\n"
"ds_read_b128 a[112:115],v3 offset:6144\n"
"ds_read_b128 a[76:79],v2 offset:8192\n"
"ds_read_b128 a[116:119],v3 offset:8192\n"
TR "a[208:209],v0 offset:8\n"
TR "a[210:211],v0 offset:520\n"
TR "a[212:213],v0 offset:4104\n"
TR "a[214:215],v0 offset:4616\n"
"s_waitcnt lgkmcnt(0)\n"
MF "v[16:19],a[200:203],a[60:63],0\n"
TR "a[216:217],v1\n"
MF "v[16:19],a[204:207],a[100:103],v[16:19]\n"
"s_add_u32 s14,s81,128\n"
TR "a[218:219],v1 offset:512\n"
MF "v[24:27],a[204:207],a[104:107],0\n"
"s_sub_u32 s15,s14,1280\n"
"v_lshrrev_b32 v15,5,v9\n"
"v_min_u32 v15,11,v15\n"
MF "v[24:27],a[200:203],a[64:67],v[24:27]\n"
"s_min_u32 s14,s14,s15\n"
TR "a[220:221],v1 offset:4096\n"
"v_mul_u32_u24 v15,160,v15\n"
MF "v[32:35],a[200:203],a[68:71],0\n"
"s_add_u32 s14,s14,s68\n"
TR "a[222:223],v1 offset:4608\n"
"v_and_or_b32 v15,v9,16,v15\n"
MF "v[32:35],a[204:207],a[108:111],v[32:35]\n"
"s_add_u32 s84,s14,0\n"
"v_max3_f32 v220,|v16|,|v17|,|v18|\n"
"v_max_f32_e64 v220,v220,|v19|\n"
MF "v[40:43],a[204:207],a[112:115],0\n"
"s_add_u32 m0,s63,20480\n"
"v_readlane_b32 s12,v220,3\n"
"v_readlane_b32 s6,v220,22\n"
MF "v[40:43],a[200:203],a[72:75],v[40:43]\n"
"s_min_u32 s12,s12,s6\n"
"v_readlane_b32 s7,v220,41\n"
"v_readlane_b32 s8,v220,60\n"
MF "v[48:51],a[200:203],a[76:79],0\n"
"s_min_u32 s7,s7,s8\n"
"v_lshlrev_b32 v15,3,v15\n"
MF "v[48:51],a[204:207],a[116:119],v[48:51]\n"
LD "v4,s[24:27],s84 offen lds\n"
MF "v[52:55],a[212:215],a[116:119],0\n"
"s_min_u32 s12,s12,s7\n"
TR "a[200:201],v1 offset:8\n"
MF "v[52:55],a[208:211],a[76:79],v[52:55]\n"
"s_and_b32 s12,s12,0x7f800000\n"
TR "a[202:203],v1 offset:520\n"
MF "v[44:47],a[208:211],a[72:75],0\n"
"s_sub_u32 s12,s12,0x14000000\n"
MF "v[44:47],a[212:215],a[112:115],v[44:47]\n"
"s_cmp_lt_u32 s12,0x50000000\n"
"s_cselect_b32 s12,s12,0xe8400000\n"
TR "a[204:205],v1 offset:4104\n"
MF "v[36:39],a[212:215],a[108:111],0\n"
"s_add_u32 s12,s12,0x17c00000\n"
TR "a[206:207],v1 offset:4616\n"
MF "v[36:39],a[208:211],a[68:71],v[36:39]\n"
"s_bfe_u32 s60,s23,0x70000\n"
"v_mov_b32 v220,s12\n"
"v_mov_b32 v221,s12\n"
MF "v[28:31],a[208:211],a[64:67],0\n"
"s_add_u32 s84,s14,9216\n"
"v_mov_b32 v222,s12\n"
"v_mov_b32 v223,s12\n"
MF "v[28:31],a[212:215],a[104:107],v[28:31]\n"
LD "v5,s[24:27],s84 offen offset:1024 lds\n"
MF "v[20:23],a[212:215],a[100:103],0\n"
"s_lshl_b32 s60,s60,2\n"
MF "v[20:23],a[208:211],a[60:63],v[20:23]\n"
"s_waitcnt vmcnt(7) lgkmcnt(0)\n"
"s_barrier\n"
MF "v[56:59],a[216:219],a[60:63],v[220:223]\n"
"s_add_u32 s60,s60,s56\n"
TR "a[208:209],v0 offset:16384\n"
MF "v[56:59],a[220:223],a[100:103],v[56:59]\n"
"s_add_u32 s84,s14,18432\n"
TR "a[210:211],v0 offset:16896\n"
MF "v[64:67],a[220:223],a[104:107],v[220:223]\n"
LD "v4,s[24:27],s84 offen offset:2048 lds\n"
MF "v[64:67],a[216:219],a[64:67],v[64:67]\n"
"s_mul_i32 s60,s60,15360\n"
TR "a[212:213],v0 offset:20480\n"
MF "v[72:75],a[216:219],a[68:71],v[220:223]\n"
TR "a[214:215],v0 offset:20992\n"
MF "v[72:75],a[220:223],a[108:111],v[72:75]\n"
MF "v[80:83],a[220:223],a[112:115],v[220:223]\n"
"s_add_u32 s84,s14,27648\n"
MF "v[80:83],a[216:219],a[72:75],v[80:83]\n"
LD "v5,s[24:27],s84 offen offset:3072 lds\n"
MF "v[88:91],a[216:219],a[76:79],v[220:223]\n"
MF "v[88:91],a[220:223],a[116:119],v[88:91]\n"
MF "v[92:95],a[204:207],a[116:119],v[220:223]\n"
"s_add_u32 s84,s14,40960\n"
TR "a[216:217],v0 offset:16392\n"
MF "v[92:95],a[200:203],a[76:79],v[92:95]\n"
"s_add_u32 m0,s63,24576\n"
TR "a[218:219],v0 offset:16904\n"
MF "v[84:87],a[200:203],a[72:75],v[220:223]\n"
LD "v4,s[24:27],s84 offen lds\n"
MF "v[84:87],a[204:207],a[112:115],v[84:87]\n"
"s_add_u32 m0,s62,49152\n"
TR "a[220:221],v0 offset:20488\n"
MF "v[76:79],a[204:207],a[108:111],v[220:223]\n"
"s_add_u32 s82,s66,786432\n"
TR "a[222:223],v0 offset:21000\n"
MF "v[76:79],a[200:203],a[68:71],v[76:79]\n"
"s_sub_u32 s15,s82,7864320\n"
MF "v[68:71],a[200:203],a[64:67],v[220:223]\n"
"s_min_u32 s82,s82,s15\n"
MF "v[68:71],a[204:207],a[104:107],v[68:71]\n"
LD "v6,s[28:31],s82 offen lds\n"
MF "v[60:63],a[204:207],a[100:103],v[220:223]\n"
MF "v[60:63],a[200:203],a[60:63],v[60:63]\n"
"s_waitcnt lgkmcnt(4)\n"
MF "v[96:99],a[208:211],a[60:63],v[220:223]\n"
TR "a[200:201],v1 offset:16384\n"
MF "v[96:99],a[212:215],a[100:103],v[96:99]\n"
TR "a[202:203],v1 offset:16896\n"
MF "v[104:107],a[212:215],a[104:107],v[220:223]\n"
LD "v11,s[28:31],s82 offen offset:1024 lds\n"
MF "v[104:107],a[208:211],a[64:67],v[104:107]\n"
TR "a[204:205],v1 offset:20480\n"
MF "v[112:115],a[208:211],a[68:71],v[220:223]\n"
TR "a[206:207],v1 offset:20992\n"
MF "v[112:115],a[212:215],a[108:111],v[112:115]\n"
MF "v[120:123],a[212:215],a[112:115],v[220:223]\n"
MF "v[120:123],a[208:211],a[72:75],v[120:123]\n"
LD "v12,s[28:31],s82 offen offset:2048 lds\n"
MF "v[128:131],a[208:211],a[76:79],v[220:223]\n"
MF "v[128:131],a[212:215],a[116:119],v[128:131]\n"
"s_waitcnt lgkmcnt(4)\n"
MF "v[132:135],a[220:223],a[116:119],v[220:223]\n"
TR "a[208:209],v1 offset:16392\n"
MF "v[132:135],a[216:219],a[76:79],v[132:135]\n"
TR "a[210:211],v1 offset:16904\n"
MF "v[124:127],a[216:219],a[72:75],v[220:223]\n"
LD "v13,s[28:31],s82 offen offset:3072 lds\n"
MF "v[124:127],a[220:223],a[112:115],v[124:127]\n"
"s_add_u32 m0,s62,65536\n"
TR "a[212:213],v1 offset:20488\n"
MF "v[116:119],a[220:223],a[108:111],v[220:223]\n"
"s_add_u32 s82,s66,786560\n"
TR "a[214:215],v1 offset:21000\n"
MF "v[116:119],a[216:219],a[68:71],v[116:119]\n"
"s_sub_u32 s15,s82,7864320\n"
MF "v[108:111],a[216:219],a[64:67],v[220:223]\n"
"s_min_u32 s82,s82,s15\n"
MF "v[108:111],a[220:223],a[104:107],v[108:111]\n"
LD "v6,s[28:31],s82 offen lds\n"
MF "v[100:103],a[220:223],a[100:103],v[220:223]\n"
MF "v[100:103],a[216:219],a[60:63],v[100:103]\n"
"s_waitcnt vmcnt(5) lgkmcnt(0)\n"
"s_barrier\n"
MF "v[136:139],a[200:203],a[60:63],v[220:223]\n"
TR "a[216:217],v0 offset:32768\n"
MF "v[136:139],a[204:207],a[100:103],v[136:139]\n"
TR "a[218:219],v0 offset:33280\n"
MF "v[144:147],a[204:207],a[104:107],v[220:223]\n"
"ds_read_b128 a[140:143],v2 offset:20480\n"
MF "v[144:147],a[200:203],a[64:67],v[144:147]\n"
TR "a[220:221],v0 offset:36864\n"
MF "v[152:155],a[200:203],a[68:71],v[220:223]\n"
TR "a[222:223],v0 offset:37376\n"
MF "v[152:155],a[204:207],a[108:111],v[152:155]\n"
LD "v11,s[28:31],s82 offen offset:1024 lds\n"
MF "v[160:163],a[204:207],a[112:115],v[220:223]\n"
"ds_read_b128 a[180:183],v3 offset:20480\n"
MF "v[160:163],a[200:203],a[72:75],v[160:163]\n"
MF "v[168:171],a[200:203],a[76:79],v[220:223]\n"
MF "v[168:171],a[204:207],a[116:119],v[168:171]\n"
LD "v12,s[28:31],s82 offen offset:2048 lds\n"
MF "v[172:175],a[212:215],a[116:119],v[220:223]\n"
TR "a[200:201],v0 offset:32776\n"
MF "v[172:175],a[208:211],a[76:79],v[172:175]\n"
TR "a[202:203],v0 offset:33288\n"
MF "v[164:167],a[208:211],a[72:75],v[220:223]\n"
"ds_read_b128 a[144:147],v2 offset:22528\n"
MF "v[164:167],a[212:215],a[112:115],v[164:167]\n"
TR "a[204:205],v0 offset:36872\n"
MF "v[156:159],a[212:215],a[108:111],v[220:223]\n"
TR "a[206:207],v0 offset:37384\n"
MF "v[156:159],a[208:211],a[68:71],v[156:159]\n"
LD "v13,s[28:31],s82 offen offset:3072 lds\n"
MF "v[148:151],a[208:211],a[64:67],v[220:223]\n"
"ds_read_b128 a[184:187],v3 offset:22528\n"
MF "v[148:151],a[212:215],a[104:107],v[148:151]\n"
"s_add_u32 m0,s62,81920\n"
MF "v[140:143],a[212:215],a[100:103],v[220:223]\n"
"s_add_u32 s82,s66,786688\n"
"s_sub_u32 s15,s82,7864320\n"
"s_min_u32 s82,s82,s15\n"
MF "v[140:143],a[208:211],a[60:63],v[140:143]\n"
"s_waitcnt lgkmcnt(7)\n"
LD "v6,s[28:31],s82 offen lds\n"
MF "v[176:179],a[216:219],a[60:63],v[220:223]\n"
TR "a[208:209],v1 offset:32768\n"
MF "v[176:179],a[220:223],a[100:103],v[176:179]\n"
TR "a[210:211],v1 offset:33280\n"
MF "v[184:187],a[220:223],a[104:107],v[220:223]\n"
"ds_read_b128 a[148:151],v2 offset:24576\n"
MF "v[184:187],a[216:219],a[64:67],v[184:187]\n"
TR "a[212:213],v1 offset:36864\n"
MF "v[192:195],a[216:219],a[68:71],v[220:223]\n"
TR "a[214:215],v1 offset:37376\n"
MF "v[192:195],a[220:223],a[108:111],v[192:195]\n"
LD "v11,s[28:31],s82 offen offset:1024 lds\n"
MF "v[200:203],a[220:223],a[112:115],v[220:223]\n"
"ds_read_b128 a[188:191],v3 offset:24576\n"
MF "v[200:203],a[216:219],a[72:75],v[200:203]\n"
MF "v[208:211],a[216:219],a[76:79],v[220:223]\n"
MF "v[208:211],a[220:223],a[116:119],v[208:211]\n"
"s_waitcnt lgkmcnt(7)\n"
LD "v12,s[28:31],s82 offen offset:2048 lds\n"
MF "v[212:215],a[204:207],a[116:119],v[220:223]\n"
TR "a[216:217],v1 offset:32776\n"
MF "v[212:215],a[200:203],a[76:79],v[212:215]\n"
TR "a[218:219],v1 offset:33288\n"
MF "v[204:207],a[200:203],a[72:75],v[220:223]\n"
"ds_read_b128 a[152:155],v2 offset:26624\n"
MF "v[204:207],a[204:207],a[112:115],v[204:207]\n"
TR "a[220:221],v1 offset:36872\n"
MF "v[196:199],a[204:207],a[108:111],v[220:223]\n"
TR "a[222:223],v1 offset:37384\n"
MF "v[196:199],a[200:203],a[68:71],v[196:199]\n"
LD "v13,s[28:31],s82 offen offset:3072 lds\n"
MF "v[188:191],a[200:203],a[64:67],v[220:223]\n"
"v_add_u32 v0,0xc000,v0\n"
MF "v[188:191],a[204:207],a[104:107],v[188:191]\n"
"v_add_u32 v1,0xc000,v1\n"
MF "v[180:183],a[204:207],a[100:103],v[220:223]\n"
"s_add_u32 s14,s81,256\n"
"s_mov_b64 exec,0\n"
"s_bitcmp1_b32 s23,12\n"
"s_cbranch_scc1 .Lisy1\n"
"s_cmp_eq_u32 s56,0\n"
"s_cbranch_scc1 .Lpg8b\n"
"s_cmp_eq_u32 s56,1\n"
"s_cbranch_scc1 .Lpg9b\n"
"s_cmp_eq_u32 s56,2\n"
"s_cbranch_scc1 .Lpgt0ex_b\n"
"s_branch .Lpg10b\n"
".Lisy1:\n"
"s_cmp_eq_u32 s56,0\n"
"s_cbranch_scc1 .Lpgq0_c\n"
"s_cmp_eq_u32 s56,1\n"
"s_cbranch_scc1 .Lpg4c\n"
"s_cmp_eq_u32 s56,2\n"
"s_cbranch_scc1 .Lpg5c\n"
"s_branch .Lpg6c\n"
".Lisr1:\n"
"s_mov_b64 exec,-1\n"
".Lisx1:\n"
MF "v[180:183],a[200:203],a[60:63],v[180:183]\n"
"s_waitcnt vmcnt(8) lgkmcnt(0)\n"
"s_barrier\n"
MF "v[216:219],a[208:211],a[60:63],v[220:223]\n"
"s_sub_u32 s15,s14,1280\n"
TR "a[200:201],v0\n"
MF "v[216:219],a[212:215],a[100:103],v[216:219]\n"
"s_min_u32 s14,s14,s15\n"
TR "a[202:203],v0 offset:512\n"
MF "v[224:227],a[212:215],a[104:107],v[220:223]\n"
"s_add_u32 s14,s14,s68\n"
"ds_read_b128 a[192:195],v3 offset:26624\n"
MF "v[224:227],a[208:211],a[64:67],v[224:227]\n"
"s_add_u32 s84,s14,0\n"
TR "a[204:205],v0 offset:4096\n"
MF "v[232:235],a[208:211],a[68:71],v[220:223]\n"
"s_add_u32 m0,s63,40960\n"
TR "a[206:207],v0 offset:4608\n"
MF "v[232:235],a[212:215],a[108:111],v[232:235]\n"
LD "v4,s[24:27],s84 offen lds\n"
MF "v[240:243],a[212:215],a[112:115],v[220:223]\n"
"ds_read_b128 a[156:159],v2 offset:28672\n"
MF "v[240:243],a[208:211],a[72:75],v[240:243]\n"
"ds_read_b128 a[196:199],v3 offset:28672\n"
MF "v[248:251],a[208:211],a[76:79],v[220:223]\n"
"s_add_u32 s84,s14,9216\n"
MF "v[248:251],a[212:215],a[116:119],v[248:251]\n"
LD "v5,s[24:27],s84 offen offset:1024 lds\n"
MF "v[252:255],a[220:223],a[116:119],v[220:223]\n"
TR "a[208:209],v0 offset:8\n"
MF "v[252:255],a[216:219],a[76:79],v[252:255]\n"
TR "a[210:211],v0 offset:520\n"
MF "v[244:247],a[216:219],a[72:75],v[220:223]\n"
MF "v[244:247],a[220:223],a[112:115],v[244:247]\n"
TR "a[212:213],v0 offset:4104\n"
MF "v[236:239],a[220:223],a[108:111],v[220:223]\n"
"s_add_u32 s84,s14,18432\n"
TR "a[214:215],v0 offset:4616\n"
MF "v[236:239],a[216:219],a[68:71],v[236:239]\n"
LD "v4,s[24:27],s84 offen offset:2048 lds\n"
MF "v[228:231],a[216:219],a[64:67],v[220:223]\n"
MF "v[228:231],a[220:223],a[104:107],v[228:231]\n"
MF "v[220:223],a[220:223],a[100:103],v[220:223]\n"
".Lpg1a:\n"
"s_cbranch_execz .Lpg7a\n"
"s_add_u32 s84,s14,27648\n"
MF "v[220:223],a[216:219],a[60:63],v[220:223]\n"
"s_waitcnt lgkmcnt(4)\n"
LD "v5,s[24:27],s84 offen offset:3072 lds\n"
MF "v[16:19],a[200:203],a[140:143],v[16:19]\n"
TR "a[216:217],v1\n"
MF "v[16:19],a[204:207],a[180:183],v[16:19]\n"
TR "a[218:219],v1 offset:512\n"
MF "v[24:27],a[204:207],a[184:187],v[24:27]\n"
MF "v[24:27],a[200:203],a[144:147],v[24:27]\n"
"s_add_u32 s84,s14,40960\n"
TR "a[220:221],v1 offset:4096\n"
MF "v[32:35],a[200:203],a[148:151],v[32:35]\n"
"s_add_u32 m0,s63,45056\n"
TR "a[222:223],v1 offset:4608\n"
MF "v[32:35],a[204:207],a[188:191],v[32:35]\n"
LD "v4,s[24:27],s84 offen lds\n"
MF "v[40:43],a[204:207],a[192:195],v[40:43]\n"
MF "v[40:43],a[200:203],a[152:155],v[40:43]\n"
"s_add_u32 m0,s62,0\n"
MF "v[48:51],a[200:203],a[156:159],v[48:51]\n"
"s_add_u32 s82,s66,1572864\n"
"s_sub_u32 s15,s82,7864320\n"
"s_min_u32 s82,s82,s15\n"
MF "v[48:51],a[204:207],a[196:199],v[48:51]\n"
"s_waitcnt lgkmcnt(4)\n"
LD "v6,s[28:31],s82 offen lds\n"
MF "v[52:55],a[212:215],a[196:199],v[52:55]\n"
TR "a[200:201],v1 offset:8\n"
MF "v[52:55],a[208:211],a[156:159],v[52:55]\n"
TR "a[202:203],v1 offset:520\n"
MF "v[44:47],a[208:211],a[152:155],v[44:47]\n"
MF "v[44:47],a[212:215],a[192:195],v[44:47]\n"
TR "a[204:205],v1 offset:4104\n"
MF "v[36:39],a[212:215],a[188:191],v[36:39]\n"
TR "a[206:207],v1 offset:4616\n"
MF "v[36:39],a[208:211],a[148:151],v[36:39]\n"
LD "v11,s[28:31],s82 offen offset:1024 lds\n"
MF "v[28:31],a[208:211],a[144:147],v[28:31]\n"
MF "v[28:31],a[212:215],a[184:187],v[28:31]\n"
MF "v[20:23],a[212:215],a[180:183],v[20:23]\n"
LD "v12,s[28:31],s82 offen offset:2048 lds\n"
MF "v[20:23],a[208:211],a[140:143],v[20:23]\n"
"s_waitcnt vmcnt(12) lgkmcnt(0)\n"
"s_barrier\n"
"v_bfe_u32 v14,v9,4,4\n"
"v_lshlrev_b32 v14,2,v14\n"
"buffer_load_dword v10,v14,s[40:43],s86 offen sc1\n"
".Lhs2:\n"
MF "v[56:59],a[216:219],a[140:143],v[56:59]\n"
TR "a[208:209],v0 offset:16384\n"
MF "v[56:59],a[220:223],a[180:183],v[56:59]\n"
TR "a[210:211],v0 offset:16896\n"
MF "v[64:67],a[220:223],a[184:187],v[64:67]\n"
LD "v13,s[28:31],s82 offen offset:3072 lds\n"
MF "v[64:67],a[216:219],a[144:147],v[64:67]\n"
TR "a[212:213],v0 offset:20480\n"
MF "v[72:75],a[216:219],a[148:151],v[72:75]\n"
TR "a[214:215],v0 offset:20992\n"
MF "v[72:75],a[220:223],a[188:191],v[72:75]\n"
"s_add_u32 s14,s81,384\n"
"s_sub_u32 s15,s14,1280\n"
"s_min_u32 s14,s14,s15\n"
MF "v[80:83],a[220:223],a[192:195],v[80:83]\n"
"s_add_u32 s14,s14,s68\n"
"s_add_u32 s84,s14,0\n"
"s_add_u32 m0,s63,0\n"
MF "v[80:83],a[216:219],a[152:155],v[80:83]\n"
LD "v4,s[24:27],s84 offen lds\n"
MF "v[88:91],a[216:219],a[156:159],v[88:91]\n"
MF "v[88:91],a[220:223],a[196:199],v[88:91]\n"
MF "v[92:95],a[204:207],a[196:199],v[92:95]\n"
TR "a[216:217],v0 offset:16392\n"
MF "v[92:95],a[200:203],a[156:159],v[92:95]\n"
"s_add_u32 s84,s14,9216\n"
TR "a[218:219],v0 offset:16904\n"
MF "v[84:87],a[200:203],a[152:155],v[84:87]\n"
LD "v5,s[24:27],s84 offen offset:1024 lds\n"
MF "v[84:87],a[204:207],a[192:195],v[84:87]\n"
"s_add_u32 m0,s62,16384\n"
TR "a[220:221],v0 offset:20488\n"
MF "v[76:79],a[204:207],a[188:191],v[76:79]\n"
"s_add_u32 s82,s66,1572992\n"
TR "a[222:223],v0 offset:21000\n"
MF "v[76:79],a[200:203],a[148:151],v[76:79]\n"
"s_sub_u32 s15,s82,7864320\n"
MF "v[68:71],a[200:203],a[144:147],v[68:71]\n"
"s_min_u32 s82,s82,s15\n"
MF "v[68:71],a[204:207],a[184:187],v[68:71]\n"
LD "v6,s[28:31],s82 offen lds\n"
MF "v[60:63],a[204:207],a[180:183],v[60:63]\n"
MF "v[60:63],a[200:203],a[140:143],v[60:63]\n"
"s_waitcnt lgkmcnt(4)\n"
MF "v[96:99],a[208:211],a[140:143],v[96:99]\n"
TR "a[200:201],v1 offset:16384\n"
MF "v[96:99],a[212:215],a[180:183],v[96:99]\n"
TR "a[202:203],v1 offset:16896\n"
MF "v[104:107],a[212:215],a[184:187],v[104:107]\n"
LD "v11,s[28:31],s82 offen offset:1024 lds\n"
MF "v[104:107],a[208:211],a[144:147],v[104:107]\n"
TR "a[204:205],v1 offset:20480\n"
MF "v[112:115],a[208:211],a[148:151],v[112:115]\n"
TR "a[206:207],v1 offset:20992\n"
MF "v[112:115],a[212:215],a[188:191],v[112:115]\n"
MF "v[120:123],a[212:215],a[192:195],v[120:123]\n"
MF "v[120:123],a[208:211],a[152:155],v[120:123]\n"
LD "v12,s[28:31],s82 offen offset:2048 lds\n"
MF "v[128:131],a[208:211],a[156:159],v[128:131]\n"
MF "v[128:131],a[212:215],a[196:199],v[128:131]\n"
"s_waitcnt lgkmcnt(4)\n"
MF "v[132:135],a[220:223],a[196:199],v[132:135]\n"
TR "a[208:209],v1 offset:16392\n"
MF "v[132:135],a[216:219],a[156:159],v[132:135]\n"
TR "a[210:211],v1 offset:16904\n"
MF "v[124:127],a[216:219],a[152:155],v[124:127]\n"
LD "v13,s[28:31],s82 offen offset:3072 lds\n"
MF "v[124:127],a[220:223],a[192:195],v[124:127]\n"
TR "a[212:213],v1 offset:20488\n"
MF "v[116:119],a[220:223],a[188:191],v[116:119]\n"
TR "a[214:215],v1 offset:21000\n"
MF "v[116:119],a[216:219],a[148:151],v[116:119]\n"
"s_add_u32 s84,s14,20480\n"
MF "v[108:111],a[216:219],a[144:147],v[108:111]\n"
"s_add_u32 m0,s63,2048\n"
MF "v[108:111],a[220:223],a[184:187],v[108:111]\n"
LD "v4,s[24:27],s84 offen lds\n"
MF "v[100:103],a[220:223],a[180:183],v[100:103]\n"
MF "v[100:103],a[216:219],a[140:143],v[100:103]\n"
"s_waitcnt vmcnt(11) lgkmcnt(0)\n"
"s_barrier\n"
MF "v[136:139],a[200:203],a[140:143],v[136:139]\n"
TR "a[216:217],v0 offset:32768\n"
MF "v[136:139],a[204:207],a[180:183],v[136:139]\n"
TR "a[218:219],v0 offset:33280\n"
MF "v[144:147],a[204:207],a[184:187],v[144:147]\n"
"ds_read_b128 a[60:63],v2 offset:40960\n"
MF "v[144:147],a[200:203],a[144:147],v[144:147]\n"
TR "a[220:221],v0 offset:36864\n"
MF "v[152:155],a[200:203],a[148:151],v[152:155]\n"
"s_add_u32 s84,s14,29696\n"
TR "a[222:223],v0 offset:37376\n"
MF "v[152:155],a[204:207],a[188:191],v[152:155]\n"
LD "v5,s[24:27],s84 offen offset:1024 lds\n"
MF "v[160:163],a[204:207],a[192:195],v[160:163]\n"
"ds_read_b128 a[100:103],v3 offset:40960\n"
MF "v[160:163],a[200:203],a[152:155],v[160:163]\n"
"s_add_u32 m0,s62,32768\n"
MF "v[168:171],a[200:203],a[156:159],v[168:171]\n"
"s_add_u32 s82,s66,1573120\n"
"s_sub_u32 s15,s82,7864320\n"
"s_min_u32 s82,s82,s15\n"
MF "v[168:171],a[204:207],a[196:199],v[168:171]\n"
LD "v6,s[28:31],s82 offen lds\n"
MF "v[172:175],a[212:215],a[196:199],v[172:175]\n"
TR "a[200:201],v0 offset:32776\n"
MF "v[172:175],a[208:211],a[156:159],v[172:175]\n"
TR "a[202:203],v0 offset:33288\n"
MF "v[164:167],a[208:211],a[152:155],v[164:167]\n"
"ds_read_b128 a[64:67],v2 offset:43008\n"
MF "v[164:167],a[212:215],a[192:195],v[164:167]\n"
TR "a[204:205],v0 offset:36872\n"
MF "v[156:159],a[212:215],a[188:191],v[156:159]\n"
TR "a[206:207],v0 offset:37384\n"
MF "v[156:159],a[208:211],a[148:151],v[156:159]\n"
LD "v11,s[28:31],s82 offen offset:1024 lds\n"
MF "v[148:151],a[208:211],a[144:147],v[148:151]\n"
"ds_read_b128 a[104:107],v3 offset:43008\n"
MF "v[148:151],a[212:215],a[184:187],v[148:151]\n"
MF "v[140:143],a[212:215],a[180:183],v[140:143]\n"
MF "v[140:143],a[208:211],a[140:143],v[140:143]\n"
"s_waitcnt lgkmcnt(7)\n"
LD "v12,s[28:31],s82 offen offset:2048 lds\n"
MF "v[176:179],a[216:219],a[140:143],v[176:179]\n"
TR "a[208:209],v1 offset:32768\n"
MF "v[176:179],a[220:223],a[180:183],v[176:179]\n"
TR "a[210:211],v1 offset:33280\n"
MF "v[184:187],a[220:223],a[184:187],v[184:187]\n"
"ds_read_b128 a[68:71],v2 offset:45056\n"
MF "v[184:187],a[216:219],a[144:147],v[184:187]\n"
TR "a[212:213],v1 offset:36864\n"
MF "v[192:195],a[216:219],a[148:151],v[192:195]\n"
TR "a[214:215],v1 offset:37376\n"
MF "v[192:195],a[220:223],a[188:191],v[192:195]\n"
LD "v13,s[28:31],s82 offen offset:3072 lds\n"
MF "v[200:203],a[220:223],a[192:195],v[200:203]\n"
"ds_read_b128 a[108:111],v3 offset:45056\n"
MF "v[200:203],a[216:219],a[152:155],v[200:203]\n"
"s_add_u32 s84,s14,40960\n"
MF "v[208:211],a[216:219],a[156:159],v[208:211]\n"
"s_add_u32 m0,s63,4096\n"
MF "v[208:211],a[220:223],a[196:199],v[208:211]\n"
"s_waitcnt lgkmcnt(7)\n"
LD "v4,s[24:27],s84 offen lds\n"
MF "v[212:215],a[204:207],a[196:199],v[212:215]\n"
"s_sub_u32 s66,s66,s83\n"
TR "a[216:217],v1 offset:32776\n"
MF "v[212:215],a[200:203],a[156:159],v[212:215]\n"
"s_add_u32 m0,s62,49152\n"
TR "a[218:219],v1 offset:33288\n"
MF "v[204:207],a[200:203],a[152:155],v[204:207]\n"
"s_add_u32 s82,s66,2359296\n"
"ds_read_b128 a[72:75],v2 offset:47104\n"
MF "v[204:207],a[204:207],a[192:195],v[204:207]\n"
"s_sub_u32 s15,s82,7864320\n"
TR "a[220:221],v1 offset:36872\n"
MF "v[196:199],a[204:207],a[188:191],v[196:199]\n"
"s_min_u32 s82,s82,s15\n"
TR "a[222:223],v1 offset:37384\n"
MF "v[196:199],a[200:203],a[148:151],v[196:199]\n"
LD "v6,s[28:31],s82 offen lds\n"
MF "v[188:191],a[200:203],a[144:147],v[188:191]\n"
"v_add_u32 v0,0xffff4000,v0\n"
MF "v[188:191],a[204:207],a[184:187],v[188:191]\n"
"v_add_u32 v1,0xffff4000,v1\n"
MF "v[180:183],a[204:207],a[180:183],v[180:183]\n"
LD "v11,s[28:31],s82 offen offset:1024 lds\n"
MF "v[180:183],a[200:203],a[140:143],v[180:183]\n"
"s_waitcnt vmcnt(15) lgkmcnt(0)\n"
"s_barrier\n"
MF "v[216:219],a[208:211],a[140:143],v[216:219]\n"
TR "a[200:201],v0\n"
MF "v[216:219],a[212:215],a[180:183],v[216:219]\n"
TR "a[202:203],v0 offset:512\n"
MF "v[224:227],a[212:215],a[184:187],v[224:227]\n"
"ds_read_b128 a[112:115],v3 offset:47104\n"
MF "v[224:227],a[208:211],a[144:147],v[224:227]\n"
TR "a[204:205],v0 offset:4096\n"
MF "v[232:235],a[208:211],a[148:151],v[232:235]\n"
TR "a[206:207],v0 offset:4608\n"
MF "v[232:235],a[212:215],a[188:191],v[232:235]\n"
LD "v12,s[28:31],s82 offen offset:2048 lds\n"
MF "v[240:243],a[212:215],a[192:195],v[240:243]\n"
"ds_read_b128 a[76:79],v2 offset:49152\n"
MF "v[240:243],a[208:211],a[152:155],v[240:243]\n"
"ds_read_b128 a[116:119],v3 offset:49152\n"
MF "v[248:251],a[208:211],a[156:159],v[248:251]\n"
MF "v[248:251],a[212:215],a[196:199],v[248:251]\n"
LD "v13,s[28:31],s82 offen offset:3072 lds\n"
MF "v[252:255],a[220:223],a[196:199],v[252:255]\n"
TR "a[208:209],v0 offset:8\n"
MF "v[252:255],a[216:219],a[156:159],v[252:255]\n"
TR "a[210:211],v0 offset:520\n"
MF "v[244:247],a[216:219],a[152:155],v[244:247]\n"
"s_add_u32 s14,s81,512\n"
"s_sub_u32 s15,s14,1280\n"
MF "v[244:247],a[220:223],a[192:195],v[244:247]\n"
"s_min_u32 s14,s14,s15\n"
"s_add_u32 s14,s14,s68\n"
TR "a[212:213],v0 offset:4104\n"
MF "v[236:239],a[220:223],a[188:191],v[236:239]\n"
"s_add_u32 s84,s14,0\n"
"s_add_u32 m0,s63,20480\n"
TR "a[214:215],v0 offset:4616\n"
MF "v[236:239],a[216:219],a[148:151],v[236:239]\n"
LD "v4,s[24:27],s84 offen lds\n"
MF "v[228:231],a[216:219],a[144:147],v[228:231]\n"
MF "v[228:231],a[220:223],a[184:187],v[228:231]\n"
MF "v[220:223],a[220:223],a[180:183],v[220:223]\n"
"s_add_u32 s84,s14,9216\n"
MF "v[220:223],a[216:219],a[140:143],v[220:223]\n"
"s_waitcnt lgkmcnt(4)\n"
LD "v5,s[24:27],s84 offen offset:1024 lds\n"
MF "v[16:19],a[200:203],a[60:63],v[16:19]\n"
TR "a[216:217],v1\n"
MF "v[16:19],a[204:207],a[100:103],v[16:19]\n"
"s_add_u32 m0,s62,65536\n"
TR "a[218:219],v1 offset:512\n"
MF "v[24:27],a[204:207],a[104:107],v[24:27]\n"
"s_add_u32 s82,s66,2359424\n"
MF "v[24:27],a[200:203],a[64:67],v[24:27]\n"
"s_sub_u32 s15,s82,7864320\n"
TR "a[220:221],v1 offset:4096\n"
MF "v[32:35],a[200:203],a[68:71],v[32:35]\n"
"s_min_u32 s82,s82,s15\n"
TR "a[222:223],v1 offset:4608\n"
MF "v[32:35],a[204:207],a[108:111],v[32:35]\n"
LD "v6,s[28:31],s82 offen lds\n"
MF "v[40:43],a[204:207],a[112:115],v[40:43]\n"
MF "v[40:43],a[200:203],a[72:75],v[40:43]\n"
MF "v[48:51],a[200:203],a[76:79],v[48:51]\n"
MF "v[48:51],a[204:207],a[116:119],v[48:51]\n"
"s_waitcnt lgkmcnt(4)\n"
LD "v11,s[28:31],s82 offen offset:1024 lds\n"
MF "v[52:55],a[212:215],a[116:119],v[52:55]\n"
TR "a[200:201],v1 offset:8\n"
MF "v[52:55],a[208:211],a[76:79],v[52:55]\n"
TR "a[202:203],v1 offset:520\n"
MF "v[44:47],a[208:211],a[72:75],v[44:47]\n"
MF "v[44:47],a[212:215],a[112:115],v[44:47]\n"
TR "a[204:205],v1 offset:4104\n"
MF "v[36:39],a[212:215],a[108:111],v[36:39]\n"
TR "a[206:207],v1 offset:4616\n"
MF "v[36:39],a[208:211],a[68:71],v[36:39]\n"
LD "v12,s[28:31],s82 offen offset:2048 lds\n"
MF "v[28:31],a[208:211],a[64:67],v[28:31]\n"
MF "v[28:31],a[212:215],a[104:107],v[28:31]\n"
MF "v[20:23],a[212:215],a[100:103],v[20:23]\n"
LD "v13,s[28:31],s82 offen offset:3072 lds\n"
MF "v[20:23],a[208:211],a[60:63],v[20:23]\n"
"s_waitcnt vmcnt(17) lgkmcnt(0)\n"
"s_barrier\n"
MF "v[56:59],a[216:219],a[60:63],v[56:59]\n"
"s_add_u32 s84,s14,20480\n"
TR "a[208:209],v0 offset:16384\n"
MF "v[56:59],a[220:223],a[100:103],v[56:59]\n"
"s_add_u32 m0,s63,22528\n"
TR "a[210:211],v0 offset:16896\n"
MF "v[64:67],a[220:223],a[104:107],v[64:67]\n"
LD "v4,s[24:27],s84 offen lds\n"
MF "v[64:67],a[216:219],a[64:67],v[64:67]\n"
TR "a[212:213],v0 offset:20480\n"
MF "v[72:75],a[216:219],a[68:71],v[72:75]\n"
TR "a[214:215],v0 offset:20992\n"
MF "v[72:75],a[220:223],a[108:111],v[72:75]\n"
MF "v[80:83],a[220:223],a[112:115],v[80:83]\n"
"s_add_u32 s84,s14,29696\n"
MF "v[80:83],a[216:219],a[72:75],v[80:83]\n"
LD "v5,s[24:27],s84 offen offset:1024 lds\n"
MF "v[88:91],a[216:219],a[76:79],v[88:91]\n"
"s_add_u32 m0,s62,81920\n"
MF "v[88:91],a[220:223],a[116:119],v[88:91]\n"
"s_add_u32 s82,s66,2359552\n"
MF "v[92:95],a[204:207],a[116:119],v[92:95]\n"
"s_sub_u32 s15,s82,7864320\n"
TR "a[216:217],v0 offset:16392\n"
MF "v[92:95],a[200:203],a[76:79],v[92:95]\n"
"s_min_u32 s82,s82,s15\n"
TR "a[218:219],v0 offset:16904\n"
MF "v[84:87],a[200:203],a[72:75],v[84:87]\n"
LD "v6,s[28:31],s82 offen lds\n"
MF "v[84:87],a[204:207],a[112:115],v[84:87]\n"
TR "a[220:221],v0 offset:20488\n"
MF "v[76:79],a[204:207],a[108:111],v[76:79]\n"
TR "a[222:223],v0 offset:21000\n"
MF "v[76:79],a[200:203],a[68:71],v[76:79]\n"
MF "v[68:71],a[200:203],a[64:67],v[68:71]\n"
MF "v[68:71],a[204:207],a[104:107],v[68:71]\n"
LD "v11,s[28:31],s82 offen offset:1024 lds\n"
MF "v[60:63],a[204:207],a[100:103],v[60:63]\n"
MF "v[60:63],a[200:203],a[60:63],v[60:63]\n"
"s_waitcnt lgkmcnt(4)\n"
MF "v[96:99],a[208:211],a[60:63],v[96:99]\n"
TR "a[200:201],v1 offset:16384\n"
MF "v[96:99],a[212:215],a[100:103],v[96:99]\n"
TR "a[202:203],v1 offset:16896\n"
MF "v[104:107],a[212:215],a[104:107],v[104:107]\n"
LD "v12,s[28:31],s82 offen offset:2048 lds\n"
MF "v[104:107],a[208:211],a[64:67],v[104:107]\n"
TR "a[204:205],v1 offset:20480\n"
MF "v[112:115],a[208:211],a[68:71],v[112:115]\n"
TR "a[206:207],v1 offset:20992\n"
MF "v[112:115],a[212:215],a[108:111],v[112:115]\n"
MF "v[120:123],a[212:215],a[112:115],v[120:123]\n"
MF "v[120:123],a[208:211],a[72:75],v[120:123]\n"
LD "v13,s[28:31],s82 offen offset:3072 lds\n"
MF "v[128:131],a[208:211],a[76:79],v[128:131]\n"
MF "v[128:131],a[212:215],a[116:119],v[128:131]\n"
"s_waitcnt lgkmcnt(4)\n"
MF "v[132:135],a[220:223],a[116:119],v[132:135]\n"
"s_add_u32 s84,s14,40960\n"
TR "a[208:209],v1 offset:16392\n"
MF "v[132:135],a[216:219],a[76:79],v[132:135]\n"
"s_add_u32 m0,s63,24576\n"
TR "a[210:211],v1 offset:16904\n"
MF "v[124:127],a[216:219],a[72:75],v[124:127]\n"
LD "v4,s[24:27],s84 offen lds\n"
MF "v[124:127],a[220:223],a[112:115],v[124:127]\n"
"s_add_u32 m0,s62,0\n"
TR "a[212:213],v1 offset:20488\n"
MF "v[116:119],a[220:223],a[108:111],v[116:119]\n"
"s_add_u32 s82,s66,3145728\n"
TR "a[214:215],v1 offset:21000\n"
MF "v[116:119],a[216:219],a[68:71],v[116:119]\n"
"s_sub_u32 s15,s82,7864320\n"
MF "v[108:111],a[216:219],a[64:67],v[108:111]\n"
"s_min_u32 s82,s82,s15\n"
MF "v[108:111],a[220:223],a[104:107],v[108:111]\n"
LD "v6,s[28:31],s82 offen lds\n"
MF "v[100:103],a[220:223],a[100:103],v[100:103]\n"
MF "v[100:103],a[216:219],a[60:63],v[100:103]\n"
"s_waitcnt vmcnt(18) lgkmcnt(0)\n"
"s_barrier\n"
MF "v[136:139],a[200:203],a[60:63],v[136:139]\n"
TR "a[216:217],v0 offset:32768\n"
MF "v[136:139],a[204:207],a[100:103],v[136:139]\n"
TR "a[218:219],v0 offset:33280\n"
MF "v[144:147],a[204:207],a[104:107],v[144:147]\n"
"ds_read_b128 a[140:143],v2\n"
MF "v[144:147],a[200:203],a[64:67],v[144:147]\n"
TR "a[220:221],v0 offset:36864\n"
MF "v[152:155],a[200:203],a[68:71],v[152:155]\n"
TR "a[222:223],v0 offset:37376\n"
MF "v[152:155],a[204:207],a[108:111],v[152:155]\n"
LD "v11,s[28:31],s82 offen offset:1024 lds\n"
MF "v[160:163],a[204:207],a[112:115],v[160:163]\n"
"ds_read_b128 a[180:183],v3\n"
MF "v[160:163],a[200:203],a[72:75],v[160:163]\n"
MF "v[168:171],a[200:203],a[76:79],v[168:171]\n"
MF "v[168:171],a[204:207],a[116:119],v[168:171]\n"
LD "v12,s[28:31],s82 offen offset:2048 lds\n"
MF "v[172:175],a[212:215],a[116:119],v[172:175]\n"
TR "a[200:201],v0 offset:32776\n"
MF "v[172:175],a[208:211],a[76:79],v[172:175]\n"
TR "a[202:203],v0 offset:33288\n"
MF "v[164:167],a[208:211],a[72:75],v[164:167]\n"
"ds_read_b128 a[144:147],v2 offset:2048\n"
MF "v[164:167],a[212:215],a[112:115],v[164:167]\n"
TR "a[204:205],v0 offset:36872\n"
MF "v[156:159],a[212:215],a[108:111],v[156:159]\n"
TR "a[206:207],v0 offset:37384\n"
MF "v[156:159],a[208:211],a[68:71],v[156:159]\n"
LD "v13,s[28:31],s82 offen offset:3072 lds\n"
MF "v[148:151],a[208:211],a[64:67],v[148:151]\n"
"ds_read_b128 a[184:187],v3 offset:2048\n"
MF "v[148:151],a[212:215],a[104:107],v[148:151]\n"
"s_add_u32 s14,s81,640\n"
"s_sub_u32 s15,s14,1280\n"
"s_min_u32 s14,s14,s15\n"
MF "v[140:143],a[212:215],a[100:103],v[140:143]\n"
"s_add_u32 s14,s14,s68\n"
"s_add_u32 s84,s14,0\n"
"s_add_u32 m0,s63,40960\n"
MF "v[140:143],a[208:211],a[60:63],v[140:143]\n"
"s_waitcnt lgkmcnt(7)\n"
LD "v4,s[24:27],s84 offen lds\n"
MF "v[176:179],a[216:219],a[60:63],v[176:179]\n"
TR "a[208:209],v1 offset:32768\n"
MF "v[176:179],a[220:223],a[100:103],v[176:179]\n"
TR "a[210:211],v1 offset:33280\n"
MF "v[184:187],a[220:223],a[104:107],v[184:187]\n"
"ds_read_b128 a[148:151],v2 offset:4096\n"
MF "v[184:187],a[216:219],a[64:67],v[184:187]\n"
TR "a[212:213],v1 offset:36864\n"
MF "v[192:195],a[216:219],a[68:71],v[192:195]\n"
"s_add_u32 s84,s14,9216\n"
TR "a[214:215],v1 offset:37376\n"
MF "v[192:195],a[220:223],a[108:111],v[192:195]\n"
LD "v5,s[24:27],s84 offen offset:1024 lds\n"
MF "v[200:203],a[220:223],a[112:115],v[200:203]\n"
"ds_read_b128 a[188:191],v3 offset:4096\n"
MF "v[200:203],a[216:219],a[72:75],v[200:203]\n"
"s_add_u32 m0,s62,16384\n"
MF "v[208:211],a[216:219],a[76:79],v[208:211]\n"
"s_add_u32 s82,s66,3145856\n"
"s_sub_u32 s15,s82,7864320\n"
"s_min_u32 s82,s82,s15\n"
MF "v[208:211],a[220:223],a[116:119],v[208:211]\n"
"s_waitcnt lgkmcnt(7)\n"
LD "v6,s[28:31],s82 offen lds\n"
MF "v[212:215],a[204:207],a[116:119],v[212:215]\n"
TR "a[216:217],v1 offset:32776\n"
MF "v[212:215],a[200:203],a[76:79],v[212:215]\n"
TR "a[218:219],v1 offset:33288\n"
MF "v[204:207],a[200:203],a[72:75],v[204:207]\n"
"ds_read_b128 a[152:155],v2 offset:6144\n"
MF "v[204:207],a[204:207],a[112:115],v[204:207]\n"
TR "a[220:221],v1 offset:36872\n"
MF "v[196:199],a[204:207],a[108:111],v[196:199]\n"
TR "a[222:223],v1 offset:37384\n"
MF "v[196:199],a[200:203],a[68:71],v[196:199]\n"
LD "v11,s[28:31],s82 offen offset:1024 lds\n"
MF "v[188:191],a[200:203],a[64:67],v[188:191]\n"
"v_add_u32 v0,0xc000,v0\n"
MF "v[188:191],a[204:207],a[104:107],v[188:191]\n"
"v_add_u32 v1,0xc000,v1\n"
MF "v[180:183],a[204:207],a[100:103],v[180:183]\n"
LD "v12,s[28:31],s82 offen offset:2048 lds\n"
MF "v[180:183],a[200:203],a[60:63],v[180:183]\n"
"s_waitcnt vmcnt(22) lgkmcnt(0)\n"
"s_barrier\n"
MF "v[216:219],a[208:211],a[60:63],v[216:219]\n"
TR "a[200:201],v0\n"
MF "v[216:219],a[212:215],a[100:103],v[216:219]\n"
TR "a[202:203],v0 offset:512\n"
MF "v[224:227],a[212:215],a[104:107],v[224:227]\n"
"ds_read_b128 a[192:195],v3 offset:6144\n"
MF "v[224:227],a[208:211],a[64:67],v[224:227]\n"
TR "a[204:205],v0 offset:4096\n"
MF "v[232:235],a[208:211],a[68:71],v[232:235]\n"
TR "a[206:207],v0 offset:4608\n"
MF "v[232:235],a[212:215],a[108:111],v[232:235]\n"
LD "v13,s[28:31],s82 offen offset:3072 lds\n"
MF "v[240:243],a[212:215],a[112:115],v[240:243]\n"
"ds_read_b128 a[156:159],v2 offset:8192\n"
MF "v[240:243],a[208:211],a[72:75],v[240:243]\n"
"s_add_u32 s84,s14,20480\n"
"ds_read_b128 a[196:199],v3 offset:8192\n"
MF "v[248:251],a[208:211],a[76:79],v[248:251]\n"
"s_add_u32 m0,s63,43008\n"
MF "v[248:251],a[212:215],a[116:119],v[248:251]\n"
LD "v4,s[24:27],s84 offen lds\n"
MF "v[252:255],a[220:223],a[116:119],v[252:255]\n"
TR "a[208:209],v0 offset:8\n"
MF "v[252:255],a[216:219],a[76:79],v[252:255]\n"
TR "a[210:211],v0 offset:520\n"
MF "v[244:247],a[216:219],a[72:75],v[244:247]\n"
MF "v[244:247],a[220:223],a[112:115],v[244:247]\n"
TR "a[212:213],v0 offset:4104\n"
MF "v[236:239],a[220:223],a[108:111],v[236:239]\n"
"s_add_u32 s84,s14,29696\n"
TR "a[214:215],v0 offset:4616\n"
MF "v[236:239],a[216:219],a[68:71],v[236:239]\n"
LD "v5,s[24:27],s84 offen offset:1024 lds\n"
MF "v[228:231],a[216:219],a[64:67],v[228:231]\n"
MF "v[228:231],a[220:223],a[104:107],v[228:231]\n"
"s_add_u32 m0,s62,32768\n"
MF "v[220:223],a[220:223],a[100:103],v[220:223]\n"
"s_add_u32 s82,s66,3145984\n"
"s_sub_u32 s15,s82,7864320\n"
"s_min_u32 s82,s82,s15\n"
MF "v[220:223],a[216:219],a[60:63],v[220:223]\n"
"s_waitcnt lgkmcnt(4)\n"
LD "v6,s[28:31],s82 offen lds\n"
MF "v[16:19],a[200:203],a[140:143],v[16:19]\n"
TR "a[216:217],v1\n"
MF "v[16:19],a[204:207],a[180:183],v[16:19]\n"
TR "a[218:219],v1 offset:512\n"
MF "v[24:27],a[204:207],a[184:187],v[24:27]\n"
MF "v[24:27],a[200:203],a[144:147],v[24:27]\n"
TR "a[220:221],v1 offset:4096\n"
MF "v[32:35],a[200:203],a[148:151],v[32:35]\n"
TR "a[222:223],v1 offset:4608\n"
MF "v[32:35],a[204:207],a[188:191],v[32:35]\n"
LD "v11,s[28:31],s82 offen offset:1024 lds\n"
MF "v[40:43],a[204:207],a[192:195],v[40:43]\n"
MF "v[40:43],a[200:203],a[152:155],v[40:43]\n"
MF "v[48:51],a[200:203],a[156:159],v[48:51]\n"
MF "v[48:51],a[204:207],a[196:199],v[48:51]\n"
"s_waitcnt lgkmcnt(4)\n"
LD "v12,s[28:31],s82 offen offset:2048 lds\n"
MF "v[52:55],a[212:215],a[196:199],v[52:55]\n"
TR "a[200:201],v1 offset:8\n"
MF "v[52:55],a[208:211],a[156:159],v[52:55]\n"
TR "a[202:203],v1 offset:520\n"
MF "v[44:47],a[208:211],a[152:155],v[44:47]\n"
MF "v[44:47],a[212:215],a[192:195],v[44:47]\n"
TR "a[204:205],v1 offset:4104\n"
MF "v[36:39],a[212:215],a[188:191],v[36:39]\n"
".Lpg2a:\n"
"s_cbranch_execz .Lisr1\n"
TR "a[206:207],v1 offset:4616\n"
MF "v[36:39],a[208:211],a[148:151],v[36:39]\n"
LD "v13,s[28:31],s82 offen offset:3072 lds\n"
MF "v[28:31],a[208:211],a[144:147],v[28:31]\n"
"s_add_u32 s84,s14,40960\n"
MF "v[28:31],a[212:215],a[184:187],v[28:31]\n"
"s_add_u32 m0,s63,45056\n"
MF "v[20:23],a[212:215],a[180:183],v[20:23]\n"
LD "v4,s[24:27],s84 offen lds\n"
MF "v[20:23],a[208:211],a[140:143],v[20:23]\n"
"s_waitcnt vmcnt(24) lgkmcnt(0)\n"
"s_barrier\n"
AW "a253,v10\n"
"v_add_u32 v10,1,v10\n"
"s_bfe_u32 s58,s22,0x30014\n"
"s_lshl_b32 s58,s58,2\n"
"s_add_u32 s58,s58,s56\n"
"s_lshl_b32 s58,s58,6\n"
"s_add_u32 s58,s58,s86\n"
"v_readlane_b32 s76,v10,s78\n"
"buffer_store_dword v10,v14,s[40:43],s58 offen offset:128 sc1\n"
".Lhs5:\n"
MF "v[56:59],a[216:219],a[140:143],v[56:59]\n"
"s_add_u32 m0,s62,49152\n"
"s_add_u32 s82,s66,3932160\n"
TR "a[208:209],v0 offset:16384\n"
MF "v[56:59],a[220:223],a[180:183],v[56:59]\n"
"s_sub_u32 s15,s82,7864320\n"
"s_min_u32 s82,s82,s15\n"
TR "a[210:211],v0 offset:16896\n"
MF "v[64:67],a[220:223],a[184:187],v[64:67]\n"
LD "v6,s[28:31],s82 offen lds\n"
MF "v[64:67],a[216:219],a[144:147],v[64:67]\n"
TR "a[212:213],v0 offset:20480\n"
MF "v[72:75],a[216:219],a[148:151],v[72:75]\n"
TR "a[214:215],v0 offset:20992\n"
MF "v[72:75],a[220:223],a[188:191],v[72:75]\n"
"buffer_load_dword a249,v15,s[48:51],s60 offen offset:1024\n"
MF "v[80:83],a[220:223],a[192:195],v[80:83]\n"
"s_bfe_u32 s59,s23,0x30003\n"
MF "v[80:83],a[216:219],a[152:155],v[80:83]\n"
LD "v11,s[28:31],s82 offen offset:1024 lds\n"
MF "v[88:91],a[216:219],a[156:159],v[88:91]\n"
"s_add_u32 s59,s59,1\n"
MF "v[88:91],a[220:223],a[196:199],v[88:91]\n"
"s_lshl_b32 s59,s59,7\n"
MF "v[92:95],a[204:207],a[196:199],v[92:95]\n"
"s_and_b32 s59,s59,768\n"
TR "a[216:217],v0 offset:16392\n"
MF "v[92:95],a[200:203],a[156:159],v[92:95]\n"
"s_add_u32 s59,s59,s60\n"
TR "a[218:219],v0 offset:16904\n"
MF "v[84:87],a[200:203],a[152:155],v[84:87]\n"
LD "v12,s[28:31],s82 offen offset:2048 lds\n"
MF "v[84:87],a[204:207],a[192:195],v[84:87]\n"
TR "a[220:221],v0 offset:20488\n"
MF "v[76:79],a[204:207],a[188:191],v[76:79]\n"
TR "a[222:223],v0 offset:21000\n"
MF "v[76:79],a[200:203],a[148:151],v[76:79]\n"
MF "v[68:71],a[200:203],a[144:147],v[68:71]\n"
MF "v[68:71],a[204:207],a[184:187],v[68:71]\n"
LD "v13,s[28:31],s82 offen offset:3072 lds\n"
MF "v[60:63],a[204:207],a[180:183],v[60:63]\n"
MF "v[60:63],a[200:203],a[140:143],v[60:63]\n"
"s_add_u32 s14,s81,768\n"
"s_sub_u32 s15,s14,1280\n"
"s_waitcnt lgkmcnt(4)\n"
MF "v[96:99],a[208:211],a[140:143],v[96:99]\n"
"s_min_u32 s14,s14,s15\n"
"s_add_u32 s14,s14,s68\n"
TR "a[200:201],v1 offset:16384\n"
MF "v[96:99],a[212:215],a[180:183],v[96:99]\n"
"s_add_u32 s84,s14,0\n"
"s_add_u32 m0,s63,0\n"
TR "a[202:203],v1 offset:16896\n"
MF "v[104:107],a[212:215],a[184:187],v[104:107]\n"
LD "v4,s[24:27],s84 offen lds\n"
MF "v[104:107],a[208:211],a[144:147],v[104:107]\n"
TR "a[204:205],v1 offset:20480\n"
MF "v[112:115],a[208:211],a[148:151],v[112:115]\n"
TR "a[206:207],v1 offset:20992\n"
MF "v[112:115],a[212:215],a[188:191],v[112:115]\n"
MF "v[120:123],a[212:215],a[192:195],v[120:123]\n"
"s_add_u32 s84,s14,9216\n"
MF "v[120:123],a[208:211],a[152:155],v[120:123]\n"
LD "v5,s[24:27],s84 offen offset:1024 lds\n"
MF "v[128:131],a[208:211],a[156:159],v[128:131]\n"
MF "v[128:131],a[212:215],a[196:199],v[128:131]\n"
"s_waitcnt lgkmcnt(4)\n"
MF "v[132:135],a[220:223],a[196:199],v[132:135]\n"
TR "a[208:209],v1 offset:16392\n"
MF "v[132:135],a[216:219],a[156:159],v[132:135]\n"
TR "a[210:211],v1 offset:16904\n"
MF "v[124:127],a[216:219],a[152:155],v[124:127]\n"
MF "v[124:127],a[220:223],a[192:195],v[124:127]\n"
TR "a[212:213],v1 offset:20488\n"
MF "v[116:119],a[220:223],a[188:191],v[116:119]\n"
TR "a[214:215],v1 offset:21000\n"
MF "v[116:119],a[216:219],a[148:151],v[116:119]\n"
MF "v[108:111],a[216:219],a[144:147],v[108:111]\n"
MF "v[108:111],a[220:223],a[184:187],v[108:111]\n"
MF "v[100:103],a[220:223],a[180:183],v[100:103]\n"
MF "v[100:103],a[216:219],a[140:143],v[100:103]\n"
"s_waitcnt vmcnt(24) lgkmcnt(0)\n"
"s_barrier\n"
MF "v[136:139],a[200:203],a[140:143],v[136:139]\n"
TR "a[216:217],v0 offset:32768\n"
MF "v[136:139],a[204:207],a[180:183],v[136:139]\n"
"s_add_u32 m0,s62,65536\n"
TR "a[218:219],v0 offset:33280\n"
MF "v[144:147],a[204:207],a[184:187],v[144:147]\n"
"s_add_u32 s82,s66,3932288\n"
"ds_read_b128 a[60:63],v2 offset:20480\n"
MF "v[144:147],a[200:203],a[144:147],v[144:147]\n"
"s_sub_u32 s15,s82,7864320\n"
TR "a[220:221],v0 offset:36864\n"
MF "v[152:155],a[200:203],a[148:151],v[152:155]\n"
"s_min_u32 s82,s82,s15\n"
TR "a[222:223],v0 offset:37376\n"
MF "v[152:155],a[204:207],a[188:191],v[152:155]\n"
LD "v6,s[28:31],s82 offen lds\n"
MF "v[160:163],a[204:207],a[192:195],v[160:163]\n"
"ds_read_b128 a[100:103],v3 offset:20480\n"
MF "v[160:163],a[200:203],a[152:155],v[160:163]\n"
MF "v[168:171],a[200:203],a[156:159],v[168:171]\n"
MF "v[168:171],a[204:207],a[196:199],v[168:171]\n"
LD "v11,s[28:31],s82 offen offset:1024 lds\n"
MF "v[172:175],a[212:215],a[196:199],v[172:175]\n"
TR "a[200:201],v0 offset:32776\n"
MF "v[172:175],a[208:211],a[156:159],v[172:175]\n"
TR "a[202:203],v0 offset:33288\n"
MF "v[164:167],a[208:211],a[152:155],v[164:167]\n"
"ds_read_b128 a[64:67],v2 offset:22528\n"
MF "v[164:167],a[212:215],a[192:195],v[164:167]\n"
TR "a[204:205],v0 offset:36872\n"
MF "v[156:159],a[212:215],a[188:191],v[156:159]\n"
TR "a[206:207],v0 offset:37384\n"
MF "v[156:159],a[208:211],a[148:151],v[156:159]\n"
LD "v12,s[28:31],s82 offen offset:2048 lds\n"
MF "v[148:151],a[208:211],a[144:147],v[148:151]\n"
"ds_read_b128 a[104:107],v3 offset:22528\n"
MF "v[148:151],a[212:215],a[184:187],v[148:151]\n"
MF "v[140:143],a[212:215],a[180:183],v[140:143]\n"
MF "v[140:143],a[208:211],a[140:143],v[140:143]\n"
"s_waitcnt lgkmcnt(7)\n"
LD "v13,s[28:31],s82 offen offset:3072 lds\n"
MF "v[176:179],a[216:219],a[140:143],v[176:179]\n"
TR "a[208:209],v1 offset:32768\n"
MF "v[176:179],a[220:223],a[180:183],v[176:179]\n"
TR "a[210:211],v1 offset:33280\n"
MF "v[184:187],a[220:223],a[184:187],v[184:187]\n"
"ds_read_b128 a[68:71],v2 offset:24576\n"
MF "v[184:187],a[216:219],a[144:147],v[184:187]\n"
"s_add_u32 s84,s14,20480\n"
TR "a[212:213],v1 offset:36864\n"
MF "v[192:195],a[216:219],a[148:151],v[192:195]\n"
"s_add_u32 m0,s63,2048\n"
TR "a[214:215],v1 offset:37376\n"
MF "v[192:195],a[220:223],a[188:191],v[192:195]\n"
LD "v4,s[24:27],s84 offen lds\n"
MF "v[200:203],a[220:223],a[192:195],v[200:203]\n"
"ds_read_b128 a[108:111],v3 offset:24576\n"
MF "v[200:203],a[216:219],a[152:155],v[200:203]\n"
MF "v[208:211],a[216:219],a[156:159],v[208:211]\n"
"s_add_u32 s84,s14,29696\n"
MF "v[208:211],a[220:223],a[196:199],v[208:211]\n"
"s_waitcnt lgkmcnt(7)\n"
LD "v5,s[24:27],s84 offen offset:1024 lds\n"
MF "v[212:215],a[204:207],a[196:199],v[212:215]\n"
TR "a[216:217],v1 offset:32776\n"
MF "v[212:215],a[200:203],a[156:159],v[212:215]\n"
TR "a[218:219],v1 offset:33288\n"
MF "v[204:207],a[200:203],a[152:155],v[204:207]\n"
"ds_read_b128 a[72:75],v2 offset:26624\n"
MF "v[204:207],a[204:207],a[192:195],v[204:207]\n"
TR "a[220:221],v1 offset:36872\n"
MF "v[196:199],a[204:207],a[188:191],v[196:199]\n"
TR "a[222:223],v1 offset:37384\n"
MF "v[196:199],a[200:203],a[148:151],v[196:199]\n"
MF "v[188:191],a[200:203],a[144:147],v[188:191]\n"
"v_add_u32 v0,0xffff4000,v0\n"
MF "v[188:191],a[204:207],a[184:187],v[188:191]\n"
"v_add_u32 v1,0xffff4000,v1\n"
MF "v[180:183],a[204:207],a[180:183],v[180:183]\n"
MF "v[180:183],a[200:203],a[140:143],v[180:183]\n"
"s_waitcnt vmcnt(26) lgkmcnt(0)\n"
"s_barrier\n"
MF "v[216:219],a[208:211],a[140:143],v[216:219]\n"
TR "a[200:201],v0\n"
MF "v[216:219],a[212:215],a[180:183],v[216:219]\n"
"s_add_u32 m0,s62,81920\n"
TR "a[202:203],v0 offset:512\n"
MF "v[224:227],a[212:215],a[184:187],v[224:227]\n"
"s_add_u32 s82,s66,3932416\n"
"ds_read_b128 a[112:115],v3 offset:26624\n"
MF "v[224:227],a[208:211],a[144:147],v[224:227]\n"
"s_sub_u32 s15,s82,7864320\n"
TR "a[204:205],v0 offset:4096\n"
MF "v[232:235],a[208:211],a[148:151],v[232:235]\n"
"s_min_u32 s82,s82,s15\n"
TR "a[206:207],v0 offset:4608\n"
MF "v[232:235],a[212:215],a[188:191],v[232:235]\n"
LD "v6,s[28:31],s82 offen lds\n"
MF "v[240:243],a[212:215],a[192:195],v[240:243]\n"
"ds_read_b128 a[76:79],v2 offset:28672\n"
MF "v[240:243],a[208:211],a[152:155],v[240:243]\n"
"ds_read_b128 a[116:119],v3 offset:28672\n"
MF "v[248:251],a[208:211],a[156:159],v[248:251]\n"
MF "v[248:251],a[212:215],a[196:199],v[248:251]\n"
LD "v11,s[28:31],s82 offen offset:1024 lds\n"
MF "v[252:255],a[220:223],a[196:199],v[252:255]\n"
TR "a[208:209],v0 offset:8\n"
MF "v[252:255],a[216:219],a[156:159],v[252:255]\n"
TR "a[210:211],v0 offset:520\n"
MF "v[244:247],a[216:219],a[152:155],v[244:247]\n"
MF "v[244:247],a[220:223],a[192:195],v[244:247]\n"
TR "a[212:213],v0 offset:4104\n"
MF "v[236:239],a[220:223],a[188:191],v[236:239]\n"
TR "a[214:215],v0 offset:4616\n"
MF "v[236:239],a[216:219],a[148:151],v[236:239]\n"
LD "v12,s[28:31],s82 offen offset:2048 lds\n"
MF "v[228:231],a[216:219],a[144:147],v[228:231]\n"
MF "v[228:231],a[220:223],a[184:187],v[228:231]\n"
MF "v[220:223],a[220:223],a[180:183],v[220:223]\n"
MF "v[220:223],a[216:219],a[140:143],v[220:223]\n"
"s_waitcnt lgkmcnt(4)\n"
LD "v13,s[28:31],s82 offen offset:3072 lds\n"
MF "v[16:19],a[200:203],a[60:63],v[16:19]\n"
TR "a[216:217],v1\n"
MF "v[16:19],a[204:207],a[100:103],v[16:19]\n"
TR "a[218:219],v1 offset:512\n"
MF "v[24:27],a[204:207],a[104:107],v[24:27]\n"
MF "v[24:27],a[200:203],a[64:67],v[24:27]\n"
"s_add_u32 s84,s14,40960\n"
TR "a[220:221],v1 offset:4096\n"
MF "v[32:35],a[200:203],a[68:71],v[32:35]\n"
"s_add_u32 m0,s63,4096\n"
TR "a[222:223],v1 offset:4608\n"
MF "v[32:35],a[204:207],a[108:111],v[32:35]\n"
LD "v4,s[24:27],s84 offen lds\n"
MF "v[40:43],a[204:207],a[112:115],v[40:43]\n"
MF "v[40:43],a[200:203],a[72:75],v[40:43]\n"
MF "v[48:51],a[200:203],a[76:79],v[48:51]\n"
MF "v[48:51],a[204:207],a[116:119],v[48:51]\n"
"s_waitcnt lgkmcnt(4)\n"
MF "v[52:55],a[212:215],a[116:119],v[52:55]\n"
TR "a[200:201],v1 offset:8\n"
MF "v[52:55],a[208:211],a[76:79],v[52:55]\n"
TR "a[202:203],v1 offset:520\n"
MF "v[44:47],a[208:211],a[72:75],v[44:47]\n"
MF "v[44:47],a[212:215],a[112:115],v[44:47]\n"
TR "a[204:205],v1 offset:4104\n"
MF "v[36:39],a[212:215],a[108:111],v[36:39]\n"
TR "a[206:207],v1 offset:4616\n"
MF "v[36:39],a[208:211],a[68:71],v[36:39]\n"
MF "v[28:31],a[208:211],a[64:67],v[28:31]\n"
MF "v[28:31],a[212:215],a[104:107],v[28:31]\n"
MF "v[20:23],a[212:215],a[100:103],v[20:23]\n"
MF "v[20:23],a[208:211],a[60:63],v[20:23]\n"
"s_waitcnt vmcnt(25) lgkmcnt(0)\n"
"s_barrier\n"
MF "v[56:59],a[216:219],a[60:63],v[56:59]\n"
"s_add_u32 m0,s62,0\n"
"s_add_u32 s82,s66,4718592\n"
TR "a[208:209],v0 offset:16384\n"
MF "v[56:59],a[220:223],a[100:103],v[56:59]\n"
"s_sub_u32 s15,s82,7864320\n"
"s_min_u32 s82,s82,s15\n"
TR "a[210:211],v0 offset:16896\n"
MF "v[64:67],a[220:223],a[104:107],v[64:67]\n"
LD "v6,s[28:31],s82 offen lds\n"
MF "v[64:67],a[216:219],a[64:67],v[64:67]\n"
TR "a[212:213],v0 offset:20480\n"
MF "v[72:75],a[216:219],a[68:71],v[72:75]\n"
TR "a[214:215],v0 offset:20992\n"
MF "v[72:75],a[220:223],a[108:111],v[72:75]\n"
"buffer_load_dword a249,v15,s[48:51],s59 offen\n"
MF "v[80:83],a[220:223],a[112:115],v[80:83]\n"
"s_bfe_u32 s59,s23,0x30003\n"
MF "v[80:83],a[216:219],a[72:75],v[80:83]\n"
LD "v11,s[28:31],s82 offen offset:1024 lds\n"
MF "v[88:91],a[216:219],a[76:79],v[88:91]\n"
"s_add_u32 s59,s59,3\n"
MF "v[88:91],a[220:223],a[116:119],v[88:91]\n"
"s_lshl_b32 s59,s59,7\n"
MF "v[92:95],a[204:207],a[116:119],v[92:95]\n"
"s_and_b32 s59,s59,768\n"
TR "a[216:217],v0 offset:16392\n"
MF "v[92:95],a[200:203],a[76:79],v[92:95]\n"
"s_add_u32 s59,s59,s60\n"
TR "a[218:219],v0 offset:16904\n"
MF "v[84:87],a[200:203],a[72:75],v[84:87]\n"
LD "v12,s[28:31],s82 offen offset:2048 lds\n"
MF "v[84:87],a[204:207],a[112:115],v[84:87]\n"
TR "a[220:221],v0 offset:20488\n"
MF "v[76:79],a[204:207],a[108:111],v[76:79]\n"
TR "a[222:223],v0 offset:21000\n"
MF "v[76:79],a[200:203],a[68:71],v[76:79]\n"
MF "v[68:71],a[200:203],a[64:67],v[68:71]\n"
MF "v[68:71],a[204:207],a[104:107],v[68:71]\n"
LD "v13,s[28:31],s82 offen offset:3072 lds\n"
MF "v[60:63],a[204:207],a[100:103],v[60:63]\n"
MF "v[60:63],a[200:203],a[60:63],v[60:63]\n"
"s_add_u32 s14,s81,896\n"
"s_sub_u32 s15,s14,1280\n"
"s_waitcnt lgkmcnt(4)\n"
MF "v[96:99],a[208:211],a[60:63],v[96:99]\n"
"s_min_u32 s14,s14,s15\n"
"s_add_u32 s14,s14,s68\n"
TR "a[200:201],v1 offset:16384\n"
MF "v[96:99],a[212:215],a[100:103],v[96:99]\n"
"s_add_u32 s84,s14,0\n"
"s_add_u32 m0,s63,20480\n"
TR "a[202:203],v1 offset:16896\n"
MF "v[104:107],a[212:215],a[104:107],v[104:107]\n"
LD "v4,s[24:27],s84 offen lds\n"
MF "v[104:107],a[208:211],a[64:67],v[104:107]\n"
TR "a[204:205],v1 offset:20480\n"
MF "v[112:115],a[208:211],a[68:71],v[112:115]\n"
TR "a[206:207],v1 offset:20992\n"
MF "v[112:115],a[212:215],a[108:111],v[112:115]\n"
MF "v[120:123],a[212:215],a[112:115],v[120:123]\n"
"s_add_u32 s84,s14,9216\n"
MF "v[120:123],a[208:211],a[72:75],v[120:123]\n"
LD "v5,s[24:27],s84 offen offset:1024 lds\n"
MF "v[128:131],a[208:211],a[76:79],v[128:131]\n"
MF "v[128:131],a[212:215],a[116:119],v[128:131]\n"
"s_waitcnt lgkmcnt(4)\n"
MF "v[132:135],a[220:223],a[116:119],v[132:135]\n"
TR "a[208:209],v1 offset:16392\n"
MF "v[132:135],a[216:219],a[76:79],v[132:135]\n"
TR "a[210:211],v1 offset:16904\n"
MF "v[124:127],a[216:219],a[72:75],v[124:127]\n"
MF "v[124:127],a[220:223],a[112:115],v[124:127]\n"
TR "a[212:213],v1 offset:20488\n"
MF "v[116:119],a[220:223],a[108:111],v[116:119]\n"
TR "a[214:215],v1 offset:21000\n"
MF "v[116:119],a[216:219],a[68:71],v[116:119]\n"
MF "v[108:111],a[216:219],a[64:67],v[108:111]\n"
MF "v[108:111],a[220:223],a[104:107],v[108:111]\n"
MF "v[100:103],a[220:223],a[100:103],v[100:103]\n"
MF "v[100:103],a[216:219],a[60:63],v[100:103]\n"
"s_waitcnt vmcnt(25) lgkmcnt(0)\n"
"s_barrier\n"
MF "v[136:139],a[200:203],a[60:63],v[136:139]\n"
TR "a[216:217],v0 offset:32768\n"
MF "v[136:139],a[204:207],a[100:103],v[136:139]\n"
"s_add_u32 m0,s62,16384\n"
TR "a[218:219],v0 offset:33280\n"
MF "v[144:147],a[204:207],a[104:107],v[144:147]\n"
"s_add_u32 s82,s66,4718720\n"
"ds_read_b128 a[140:143],v2 offset:40960\n"
MF "v[144:147],a[200:203],a[64:67],v[144:147]\n"
"s_sub_u32 s15,s82,7864320\n"
TR "a[220:221],v0 offset:36864\n"
MF "v[152:155],a[200:203],a[68:71],v[152:155]\n"
"s_min_u32 s82,s82,s15\n"
TR "a[222:223],v0 offset:37376\n"
MF "v[152:155],a[204:207],a[108:111],v[152:155]\n"
LD "v6,s[28:31],s82 offen lds\n"
MF "v[160:163],a[204:207],a[112:115],v[160:163]\n"
"ds_read_b128 a[180:183],v3 offset:40960\n"
MF "v[160:163],a[200:203],a[72:75],v[160:163]\n"
MF "v[168:171],a[200:203],a[76:79],v[168:171]\n"
MF "v[168:171],a[204:207],a[116:119],v[168:171]\n"
LD "v11,s[28:31],s82 offen offset:1024 lds\n"
MF "v[172:175],a[212:215],a[116:119],v[172:175]\n"
TR "a[200:201],v0 offset:32776\n"
MF "v[172:175],a[208:211],a[76:79],v[172:175]\n"
TR "a[202:203],v0 offset:33288\n"
MF "v[164:167],a[208:211],a[72:75],v[164:167]\n"
"ds_read_b128 a[144:147],v2 offset:43008\n"
MF "v[164:167],a[212:215],a[112:115],v[164:167]\n"
TR "a[204:205],v0 offset:36872\n"
MF "v[156:159],a[212:215],a[108:111],v[156:159]\n"
TR "a[206:207],v0 offset:37384\n"
MF "v[156:159],a[208:211],a[68:71],v[156:159]\n"
LD "v12,s[28:31],s82 offen offset:2048 lds\n"
MF "v[148:151],a[208:211],a[64:67],v[148:151]\n"
"ds_read_b128 a[184:187],v3 offset:43008\n"
MF "v[148:151],a[212:215],a[104:107],v[148:151]\n"
MF "v[140:143],a[212:215],a[100:103],v[140:143]\n"
MF "v[140:143],a[208:211],a[60:63],v[140:143]\n"
"s_waitcnt lgkmcnt(7)\n"
LD "v13,s[28:31],s82 offen offset:3072 lds\n"
MF "v[176:179],a[216:219],a[60:63],v[176:179]\n"
TR "a[208:209],v1 offset:32768\n"
MF "v[176:179],a[220:223],a[100:103],v[176:179]\n"
TR "a[210:211],v1 offset:33280\n"
MF "v[184:187],a[220:223],a[104:107],v[184:187]\n"
"ds_read_b128 a[148:151],v2 offset:45056\n"
MF "v[184:187],a[216:219],a[64:67],v[184:187]\n"
"s_add_u32 s84,s14,20480\n"
TR "a[212:213],v1 offset:36864\n"
MF "v[192:195],a[216:219],a[68:71],v[192:195]\n"
"s_add_u32 m0,s63,22528\n"
TR "a[214:215],v1 offset:37376\n"
MF "v[192:195],a[220:223],a[108:111],v[192:195]\n"
LD "v4,s[24:27],s84 offen lds\n"
MF "v[200:203],a[220:223],a[112:115],v[200:203]\n"
"ds_read_b128 a[188:191],v3 offset:45056\n"
MF "v[200:203],a[216:219],a[72:75],v[200:203]\n"
MF "v[208:211],a[216:219],a[76:79],v[208:211]\n"
"s_add_u32 s84,s14,29696\n"
MF "v[208:211],a[220:223],a[116:119],v[208:211]\n"
"s_waitcnt lgkmcnt(7)\n"
LD "v5,s[24:27],s84 offen offset:1024 lds\n"
MF "v[212:215],a[204:207],a[116:119],v[212:215]\n"
TR "a[216:217],v1 offset:32776\n"
MF "v[212:215],a[200:203],a[76:79],v[212:215]\n"
TR "a[218:219],v1 offset:33288\n"
MF "v[204:207],a[200:203],a[72:75],v[204:207]\n"
"ds_read_b128 a[152:155],v2 offset:47104\n"
MF "v[204:207],a[204:207],a[112:115],v[204:207]\n"
TR "a[220:221],v1 offset:36872\n"
MF "v[196:199],a[204:207],a[108:111],v[196:199]\n"
TR "a[222:223],v1 offset:37384\n"
MF "v[196:199],a[200:203],a[68:71],v[196:199]\n"
MF "v[188:191],a[200:203],a[64:67],v[188:191]\n"
"v_add_u32 v0,0xc000,v0\n"
MF "v[188:191],a[204:207],a[104:107],v[188:191]\n"
"v_add_u32 v1,0xc000,v1\n"
MF "v[180:183],a[204:207],a[100:103],v[180:183]\n"
MF "v[180:183],a[200:203],a[60:63],v[180:183]\n"
"s_waitcnt vmcnt(26) lgkmcnt(0)\n"
"s_barrier\n"
MF "v[216:219],a[208:211],a[60:63],v[216:219]\n"
TR "a[200:201],v0\n"
MF "v[216:219],a[212:215],a[100:103],v[216:219]\n"
"s_add_u32 m0,s62,32768\n"
TR "a[202:203],v0 offset:512\n"
MF "v[224:227],a[212:215],a[104:107],v[224:227]\n"
"s_add_u32 s82,s66,4718848\n"
"ds_read_b128 a[192:195],v3 offset:47104\n"
MF "v[224:227],a[208:211],a[64:67],v[224:227]\n"
"s_sub_u32 s15,s82,7864320\n"
TR "a[204:205],v0 offset:4096\n"
MF "v[232:235],a[208:211],a[68:71],v[232:235]\n"
"s_min_u32 s82,s82,s15\n"
TR "a[206:207],v0 offset:4608\n"
MF "v[232:235],a[212:215],a[108:111],v[232:235]\n"
LD "v6,s[28:31],s82 offen lds\n"
MF "v[240:243],a[212:215],a[112:115],v[240:243]\n"
"ds_read_b128 a[156:159],v2 offset:49152\n"
MF "v[240:243],a[208:211],a[72:75],v[240:243]\n"
"ds_read_b128 a[196:199],v3 offset:49152\n"
MF "v[248:251],a[208:211],a[76:79],v[248:251]\n"
MF "v[248:251],a[212:215],a[116:119],v[248:251]\n"
LD "v11,s[28:31],s82 offen offset:1024 lds\n"
MF "v[252:255],a[220:223],a[116:119],v[252:255]\n"
TR "a[208:209],v0 offset:8\n"
MF "v[252:255],a[216:219],a[76:79],v[252:255]\n"
TR "a[210:211],v0 offset:520\n"
MF "v[244:247],a[216:219],a[72:75],v[244:247]\n"
MF "v[244:247],a[220:223],a[112:115],v[244:247]\n"
TR "a[212:213],v0 offset:4104\n"
MF "v[236:239],a[220:223],a[108:111],v[236:239]\n"
TR "a[214:215],v0 offset:4616\n"
MF "v[236:239],a[216:219],a[68:71],v[236:239]\n"
LD "v12,s[28:31],s82 offen offset:2048 lds\n"
MF "v[228:231],a[216:219],a[64:67],v[228:231]\n"
MF "v[228:231],a[220:223],a[104:107],v[228:231]\n"
MF "v[220:223],a[220:223],a[100:103],v[220:223]\n"
MF "v[220:223],a[216:219],a[60:63],v[220:223]\n"
"s_waitcnt lgkmcnt(4)\n"
LD "v13,s[28:31],s82 offen offset:3072 lds\n"
MF "v[16:19],a[200:203],a[140:143],v[16:19]\n"
TR "a[216:217],v1\n"
MF "v[16:19],a[204:207],a[180:183],v[16:19]\n"
TR "a[218:219],v1 offset:512\n"
MF "v[24:27],a[204:207],a[184:187],v[24:27]\n"
MF "v[24:27],a[200:203],a[144:147],v[24:27]\n"
"s_add_u32 s84,s14,40960\n"
TR "a[220:221],v1 offset:4096\n"
MF "v[32:35],a[200:203],a[148:151],v[32:35]\n"
"s_add_u32 m0,s63,24576\n"
TR "a[222:223],v1 offset:4608\n"
MF "v[32:35],a[204:207],a[188:191],v[32:35]\n"
LD "v4,s[24:27],s84 offen lds\n"
MF "v[40:43],a[204:207],a[192:195],v[40:43]\n"
MF "v[40:43],a[200:203],a[152:155],v[40:43]\n"
MF "v[48:51],a[200:203],a[156:159],v[48:51]\n"
MF "v[48:51],a[204:207],a[196:199],v[48:51]\n"
"s_waitcnt lgkmcnt(4)\n"
MF "v[52:55],a[212:215],a[196:199],v[52:55]\n"
TR "a[200:201],v1 offset:8\n"
MF "v[52:55],a[208:211],a[156:159],v[52:55]\n"
TR "a[202:203],v1 offset:520\n"
MF "v[44:47],a[208:211],a[152:155],v[44:47]\n"
MF "v[44:47],a[212:215],a[192:195],v[44:47]\n"
TR "a[204:205],v1 offset:4104\n"
MF "v[36:39],a[212:215],a[188:191],v[36:39]\n"
TR "a[206:207],v1 offset:4616\n"
MF "v[36:39],a[208:211],a[148:151],v[36:39]\n"
MF "v[28:31],a[208:211],a[144:147],v[28:31]\n"
MF "v[28:31],a[212:215],a[184:187],v[28:31]\n"
MF "v[20:23],a[212:215],a[180:183],v[20:23]\n"
MF "v[20:23],a[208:211],a[140:143],v[20:23]\n"
"s_waitcnt vmcnt(25) lgkmcnt(0)\n"
"s_barrier\n"
MF "v[56:59],a[216:219],a[140:143],v[56:59]\n"
"s_add_u32 m0,s62,49152\n"
"s_add_u32 s82,s66,5505024\n"
TR "a[208:209],v0 offset:16384\n"
MF "v[56:59],a[220:223],a[180:183],v[56:59]\n"
"s_sub_u32 s15,s82,7864320\n"
"s_min_u32 s82,s82,s15\n"
TR "a[210:211],v0 offset:16896\n"
MF "v[64:67],a[220:223],a[184:187],v[64:67]\n"
LD "v6,s[28:31],s82 offen lds\n"
MF "v[64:67],a[216:219],a[144:147],v[64:67]\n"
TR "a[212:213],v0 offset:20480\n"
MF "v[72:75],a[216:219],a[148:151],v[72:75]\n"
TR "a[214:215],v0 offset:20992\n"
MF "v[72:75],a[220:223],a[188:191],v[72:75]\n"
"buffer_load_dword a249,v15,s[48:51],s59 offen\n"
MF "v[80:83],a[220:223],a[192:195],v[80:83]\n"
"s_bfe_u32 s59,s23,0x30003\n"
MF "v[80:83],a[216:219],a[152:155],v[80:83]\n"
LD "v11,s[28:31],s82 offen offset:1024 lds\n"
MF "v[88:91],a[216:219],a[156:159],v[88:91]\n"
"s_add_u32 s59,s59,5\n"
MF "v[88:91],a[220:223],a[196:199],v[88:91]\n"
"s_lshl_b32 s59,s59,7\n"
MF "v[92:95],a[204:207],a[196:199],v[92:95]\n"
"s_and_b32 s59,s59,768\n"
TR "a[216:217],v0 offset:16392\n"
MF "v[92:95],a[200:203],a[156:159],v[92:95]\n"
"s_add_u32 s59,s59,s60\n"
TR "a[218:219],v0 offset:16904\n"
MF "v[84:87],a[200:203],a[152:155],v[84:87]\n"
LD "v12,s[28:31],s82 offen offset:2048 lds\n"
MF "v[84:87],a[204:207],a[192:195],v[84:87]\n"
TR "a[220:221],v0 offset:20488\n"
MF "v[76:79],a[204:207],a[188:191],v[76:79]\n"
TR "a[222:223],v0 offset:21000\n"
MF "v[76:79],a[200:203],a[148:151],v[76:79]\n"
MF "v[68:71],a[200:203],a[144:147],v[68:71]\n"
MF "v[68:71],a[204:207],a[184:187],v[68:71]\n"
".Lpg3a:\n"
"s_cbranch_execz .Lisr1\n"
LD "v13,s[28:31],s82 offen offset:3072 lds\n"
MF "v[60:63],a[204:207],a[180:183],v[60:63]\n"
MF "v[60:63],a[200:203],a[140:143],v[60:63]\n"
"s_add_u32 s14,s81,1024\n"
"s_sub_u32 s15,s14,1280\n"
"s_waitcnt lgkmcnt(4)\n"
MF "v[96:99],a[208:211],a[140:143],v[96:99]\n"
"s_min_u32 s14,s14,s15\n"
"s_add_u32 s14,s14,s68\n"
TR "a[200:201],v1 offset:16384\n"
MF "v[96:99],a[212:215],a[180:183],v[96:99]\n"
"s_add_u32 s84,s14,0\n"
"s_add_u32 m0,s63,40960\n"
TR "a[202:203],v1 offset:16896\n"
MF "v[104:107],a[212:215],a[184:187],v[104:107]\n"
LD "v4,s[24:27],s84 offen lds\n"
MF "v[104:107],a[208:211],a[144:147],v[104:107]\n"
TR "a[204:205],v1 offset:20480\n"
MF "v[112:115],a[208:211],a[148:151],v[112:115]\n"
TR "a[206:207],v1 offset:20992\n"
MF "v[112:115],a[212:215],a[188:191],v[112:115]\n"
MF "v[120:123],a[212:215],a[192:195],v[120:123]\n"
"s_add_u32 s84,s14,9216\n"
MF "v[120:123],a[208:211],a[152:155],v[120:123]\n"
LD "v5,s[24:27],s84 offen offset:1024 lds\n"
MF "v[128:131],a[208:211],a[156:159],v[128:131]\n"
MF "v[128:131],a[212:215],a[196:199],v[128:131]\n"
"s_waitcnt lgkmcnt(4)\n"
MF "v[132:135],a[220:223],a[196:199],v[132:135]\n"
TR "a[208:209],v1 offset:16392\n"
MF "v[132:135],a[216:219],a[156:159],v[132:135]\n"
TR "a[210:211],v1 offset:16904\n"
MF "v[124:127],a[216:219],a[152:155],v[124:127]\n"
MF "v[124:127],a[220:223],a[192:195],v[124:127]\n"
TR "a[212:213],v1 offset:20488\n"
MF "v[116:119],a[220:223],a[188:191],v[116:119]\n"
TR "a[214:215],v1 offset:21000\n"
MF "v[116:119],a[216:219],a[148:151],v[116:119]\n"
MF "v[108:111],a[216:219],a[144:147],v[108:111]\n"
MF "v[108:111],a[220:223],a[184:187],v[108:111]\n"
MF "v[100:103],a[220:223],a[180:183],v[100:103]\n"
MF "v[100:103],a[216:219],a[140:143],v[100:103]\n"
"s_waitcnt vmcnt(25) lgkmcnt(0)\n"
"s_barrier\n"
MF "v[136:139],a[200:203],a[140:143],v[136:139]\n"
TR "a[216:217],v0 offset:32768\n"
MF "v[136:139],a[204:207],a[180:183],v[136:139]\n"
"s_add_u32 m0,s62,65536\n"
TR "a[218:219],v0 offset:33280\n"
MF "v[144:147],a[204:207],a[184:187],v[144:147]\n"
"s_add_u32 s82,s66,5505152\n"
"ds_read_b128 a[60:63],v2\n"
MF "v[144:147],a[200:203],a[144:147],v[144:147]\n"
"s_sub_u32 s15,s82,7864320\n"
TR "a[220:221],v0 offset:36864\n"
MF "v[152:155],a[200:203],a[148:151],v[152:155]\n"
"s_min_u32 s82,s82,s15\n"
TR "a[222:223],v0 offset:37376\n"
MF "v[152:155],a[204:207],a[188:191],v[152:155]\n"
LD "v6,s[28:31],s82 offen lds\n"
MF "v[160:163],a[204:207],a[192:195],v[160:163]\n"
"ds_read_b128 a[100:103],v3\n"
MF "v[160:163],a[200:203],a[152:155],v[160:163]\n"
MF "v[168:171],a[200:203],a[156:159],v[168:171]\n"
MF "v[168:171],a[204:207],a[196:199],v[168:171]\n"
LD "v11,s[28:31],s82 offen offset:1024 lds\n"
MF "v[172:175],a[212:215],a[196:199],v[172:175]\n"
TR "a[200:201],v0 offset:32776\n"
MF "v[172:175],a[208:211],a[156:159],v[172:175]\n"
TR "a[202:203],v0 offset:33288\n"
MF "v[164:167],a[208:211],a[152:155],v[164:167]\n"
"ds_read_b128 a[64:67],v2 offset:2048\n"
MF "v[164:167],a[212:215],a[192:195],v[164:167]\n"
TR "a[204:205],v0 offset:36872\n"
MF "v[156:159],a[212:215],a[188:191],v[156:159]\n"
TR "a[206:207],v0 offset:37384\n"
MF "v[156:159],a[208:211],a[148:151],v[156:159]\n"
LD "v12,s[28:31],s82 offen offset:2048 lds\n"
MF "v[148:151],a[208:211],a[144:147],v[148:151]\n"
"ds_read_b128 a[104:107],v3 offset:2048\n"
MF "v[148:151],a[212:215],a[184:187],v[148:151]\n"
MF "v[140:143],a[212:215],a[180:183],v[140:143]\n"
MF "v[140:143],a[208:211],a[140:143],v[140:143]\n"
"s_waitcnt lgkmcnt(7)\n"
LD "v13,s[28:31],s82 offen offset:3072 lds\n"
MF "v[176:179],a[216:219],a[140:143],v[176:179]\n"
TR "a[208:209],v1 offset:32768\n"
MF "v[176:179],a[220:223],a[180:183],v[176:179]\n"
TR "a[210:211],v1 offset:33280\n"
MF "v[184:187],a[220:223],a[184:187],v[184:187]\n"
"ds_read_b128 a[68:71],v2 offset:4096\n"
MF "v[184:187],a[216:219],a[144:147],v[184:187]\n"
"s_add_u32 s84,s14,20480\n"
TR "a[212:213],v1 offset:36864\n"
MF "v[192:195],a[216:219],a[148:151],v[192:195]\n"
"s_add_u32 m0,s63,43008\n"
TR "a[214:215],v1 offset:37376\n"
MF "v[192:195],a[220:223],a[188:191],v[192:195]\n"
LD "v4,s[24:27],s84 offen lds\n"
MF "v[200:203],a[220:223],a[192:195],v[200:203]\n"
"ds_read_b128 a[108:111],v3 offset:4096\n"
MF "v[200:203],a[216:219],a[152:155],v[200:203]\n"
MF "v[208:211],a[216:219],a[156:159],v[208:211]\n"
"s_add_u32 s84,s14,29696\n"
MF "v[208:211],a[220:223],a[196:199],v[208:211]\n"
"s_waitcnt lgkmcnt(7)\n"
LD "v5,s[24:27],s84 offen offset:1024 lds\n"
MF "v[212:215],a[204:207],a[196:199],v[212:215]\n"
TR "a[216:217],v1 offset:32776\n"
MF "v[212:215],a[200:203],a[156:159],v[212:215]\n"
TR "a[218:219],v1 offset:33288\n"
MF "v[204:207],a[200:203],a[152:155],v[204:207]\n"
"ds_read_b128 a[72:75],v2 offset:6144\n"
MF "v[204:207],a[204:207],a[192:195],v[204:207]\n"
TR "a[220:221],v1 offset:36872\n"
MF "v[196:199],a[204:207],a[188:191],v[196:199]\n"
TR "a[222:223],v1 offset:37384\n"
MF "v[196:199],a[200:203],a[148:151],v[196:199]\n"
MF "v[188:191],a[200:203],a[144:147],v[188:191]\n"
"v_add_u32 v0,0xffff4000,v0\n"
MF "v[188:191],a[204:207],a[184:187],v[188:191]\n"
"v_add_u32 v1,0xffff4000,v1\n"
MF "v[180:183],a[204:207],a[180:183],v[180:183]\n"
MF "v[180:183],a[200:203],a[140:143],v[180:183]\n"
"s_waitcnt vmcnt(26) lgkmcnt(0)\n"
"s_barrier\n"
MF "v[216:219],a[208:211],a[140:143],v[216:219]\n"
TR "a[200:201],v0\n"
MF "v[216:219],a[212:215],a[180:183],v[216:219]\n"
"s_add_u32 m0,s62,81920\n"
TR "a[202:203],v0 offset:512\n"
MF "v[224:227],a[212:215],a[184:187],v[224:227]\n"
"s_add_u32 s82,s66,5505280\n"
"ds_read_b128 a[112:115],v3 offset:6144\n"
MF "v[224:227],a[208:211],a[144:147],v[224:227]\n"
"s_sub_u32 s15,s82,7864320\n"
TR "a[204:205],v0 offset:4096\n"
MF "v[232:235],a[208:211],a[148:151],v[232:235]\n"
"s_min_u32 s82,s82,s15\n"
TR "a[206:207],v0 offset:4608\n"
MF "v[232:235],a[212:215],a[188:191],v[232:235]\n"
LD "v6,s[28:31],s82 offen lds\n"
MF "v[240:243],a[212:215],a[192:195],v[240:243]\n"
"ds_read_b128 a[76:79],v2 offset:8192\n"
MF "v[240:243],a[208:211],a[152:155],v[240:243]\n"
"ds_read_b128 a[116:119],v3 offset:8192\n"
MF "v[248:251],a[208:211],a[156:159],v[248:251]\n"
MF "v[248:251],a[212:215],a[196:199],v[248:251]\n"
LD "v11,s[28:31],s82 offen offset:1024 lds\n"
MF "v[252:255],a[220:223],a[196:199],v[252:255]\n"
TR "a[208:209],v0 offset:8\n"
MF "v[252:255],a[216:219],a[156:159],v[252:255]\n"
TR "a[210:211],v0 offset:520\n"
MF "v[244:247],a[216:219],a[152:155],v[244:247]\n"
MF "v[244:247],a[220:223],a[192:195],v[244:247]\n"
TR "a[212:213],v0 offset:4104\n"
MF "v[236:239],a[220:223],a[188:191],v[236:239]\n"
TR "a[214:215],v0 offset:4616\n"
MF "v[236:239],a[216:219],a[148:151],v[236:239]\n"
LD "v12,s[28:31],s82 offen offset:2048 lds\n"
MF "v[228:231],a[216:219],a[144:147],v[228:231]\n"
MF "v[228:231],a[220:223],a[184:187],v[228:231]\n"
MF "v[220:223],a[220:223],a[180:183],v[220:223]\n"
MF "v[220:223],a[216:219],a[140:143],v[220:223]\n"
"s_waitcnt lgkmcnt(4)\n"
LD "v13,s[28:31],s82 offen offset:3072 lds\n"
MF "v[16:19],a[200:203],a[60:63],v[16:19]\n"
TR "a[216:217],v1\n"
MF "v[16:19],a[204:207],a[100:103],v[16:19]\n"
TR "a[218:219],v1 offset:512\n"
MF "v[24:27],a[204:207],a[104:107],v[24:27]\n"
MF "v[24:27],a[200:203],a[64:67],v[24:27]\n"
"s_add_u32 s84,s14,40960\n"
TR "a[220:221],v1 offset:4096\n"
MF "v[32:35],a[200:203],a[68:71],v[32:35]\n"
"s_add_u32 m0,s63,45056\n"
TR "a[222:223],v1 offset:4608\n"
MF "v[32:35],a[204:207],a[108:111],v[32:35]\n"
LD "v4,s[24:27],s84 offen lds\n"
MF "v[40:43],a[204:207],a[112:115],v[40:43]\n"
MF "v[40:43],a[200:203],a[72:75],v[40:43]\n"
MF "v[48:51],a[200:203],a[76:79],v[48:51]\n"
MF "v[48:51],a[204:207],a[116:119],v[48:51]\n"
"s_waitcnt lgkmcnt(4)\n"
MF "v[52:55],a[212:215],a[116:119],v[52:55]\n"
TR "a[200:201],v1 offset:8\n"
MF "v[52:55],a[208:211],a[76:79],v[52:55]\n"
TR "a[202:203],v1 offset:520\n"
MF "v[44:47],a[208:211],a[72:75],v[44:47]\n"
MF "v[44:47],a[212:215],a[112:115],v[44:47]\n"
TR "a[204:205],v1 offset:4104\n"
MF "v[36:39],a[212:215],a[108:111],v[36:39]\n"
TR "a[206:207],v1 offset:4616\n"
MF "v[36:39],a[208:211],a[68:71],v[36:39]\n"
MF "v[28:31],a[208:211],a[64:67],v[28:31]\n"
MF "v[28:31],a[212:215],a[104:107],v[28:31]\n"
MF "v[20:23],a[212:215],a[100:103],v[20:23]\n"
MF "v[20:23],a[208:211],a[60:63],v[20:23]\n"
"s_waitcnt vmcnt(25) lgkmcnt(0)\n"
"s_barrier\n"
MF "v[56:59],a[216:219],a[60:63],v[56:59]\n"
"s_add_u32 m0,s62,0\n"
"s_add_u32 s82,s66,6291712\n"
TR "a[208:209],v0 offset:16384\n"
MF "v[56:59],a[220:223],a[100:103],v[56:59]\n"
"s_sub_u32 s15,s82,7864320\n"
"s_min_u32 s82,s82,s15\n"
TR "a[210:211],v0 offset:16896\n"
MF "v[64:67],a[220:223],a[104:107],v[64:67]\n"
LD "v6,s[28:31],s82 offen lds\n"
MF "v[64:67],a[216:219],a[64:67],v[64:67]\n"
TR "a[212:213],v0 offset:20480\n"
MF "v[72:75],a[216:219],a[68:71],v[72:75]\n"
TR "a[214:215],v0 offset:20992\n"
MF "v[72:75],a[220:223],a[108:111],v[72:75]\n"
"buffer_load_dword a249,v15,s[48:51],s59 offen\n"
MF "v[80:83],a[220:223],a[112:115],v[80:83]\n"
MF "v[80:83],a[216:219],a[72:75],v[80:83]\n"
LD "v11,s[28:31],s82 offen offset:1024 lds\n"
MF "v[88:91],a[216:219],a[76:79],v[88:91]\n"
MF "v[88:91],a[220:223],a[116:119],v[88:91]\n"
MF "v[92:95],a[204:207],a[116:119],v[92:95]\n"
TR "a[216:217],v0 offset:16392\n"
MF "v[92:95],a[200:203],a[76:79],v[92:95]\n"
TR "a[218:219],v0 offset:16904\n"
MF "v[84:87],a[200:203],a[72:75],v[84:87]\n"
LD "v12,s[28:31],s82 offen offset:2048 lds\n"
MF "v[84:87],a[204:207],a[112:115],v[84:87]\n"
TR "a[220:221],v0 offset:20488\n"
MF "v[76:79],a[204:207],a[108:111],v[76:79]\n"
TR "a[222:223],v0 offset:21000\n"
MF "v[76:79],a[200:203],a[68:71],v[76:79]\n"
MF "v[68:71],a[200:203],a[64:67],v[68:71]\n"
MF "v[68:71],a[204:207],a[104:107],v[68:71]\n"
LD "v13,s[28:31],s82 offen offset:3072 lds\n"
MF "v[60:63],a[204:207],a[100:103],v[60:63]\n"
MF "v[60:63],a[200:203],a[60:63],v[60:63]\n"
"s_add_u32 s14,s81,1152\n"
"s_sub_u32 s15,s14,1280\n"
"s_waitcnt lgkmcnt(4)\n"
MF "v[96:99],a[208:211],a[60:63],v[96:99]\n"
"s_min_u32 s14,s14,s15\n"
"s_add_u32 s14,s14,s68\n"
TR "a[200:201],v1 offset:16384\n"
MF "v[96:99],a[212:215],a[100:103],v[96:99]\n"
"s_add_u32 s84,s14,0\n"
"s_add_u32 m0,s63,0\n"
TR "a[202:203],v1 offset:16896\n"
MF "v[104:107],a[212:215],a[104:107],v[104:107]\n"
LD "v4,s[24:27],s84 offen lds\n"
MF "v[104:107],a[208:211],a[64:67],v[104:107]\n"
TR "a[204:205],v1 offset:20480\n"
MF "v[112:115],a[208:211],a[68:71],v[112:115]\n"
TR "a[206:207],v1 offset:20992\n"
MF "v[112:115],a[212:215],a[108:111],v[112:115]\n"
MF "v[120:123],a[212:215],a[112:115],v[120:123]\n"
"s_add_u32 s84,s14,9216\n"
MF "v[120:123],a[208:211],a[72:75],v[120:123]\n"
LD "v5,s[24:27],s84 offen offset:1024 lds\n"
MF "v[128:131],a[208:211],a[76:79],v[128:131]\n"
MF "v[128:131],a[212:215],a[116:119],v[128:131]\n"
"s_waitcnt lgkmcnt(4)\n"
MF "v[132:135],a[220:223],a[116:119],v[132:135]\n"
TR "a[208:209],v1 offset:16392\n"
MF "v[132:135],a[216:219],a[76:79],v[132:135]\n"
"s_add_u32 s84,s14,18432\n"
TR "a[210:211],v1 offset:16904\n"
MF "v[124:127],a[216:219],a[72:75],v[124:127]\n"
LD "v4,s[24:27],s84 offen offset:2048 lds\n"
MF "v[124:127],a[220:223],a[112:115],v[124:127]\n"
TR "a[212:213],v1 offset:20488\n"
MF "v[116:119],a[220:223],a[108:111],v[116:119]\n"
TR "a[214:215],v1 offset:21000\n"
MF "v[116:119],a[216:219],a[68:71],v[116:119]\n"
MF "v[108:111],a[216:219],a[64:67],v[108:111]\n"
MF "v[108:111],a[220:223],a[104:107],v[108:111]\n"
MF "v[100:103],a[220:223],a[100:103],v[100:103]\n"
MF "v[100:103],a[216:219],a[60:63],v[100:103]\n"
"s_waitcnt vmcnt(26) lgkmcnt(0)\n"
"s_barrier\n"
MF "v[136:139],a[200:203],a[60:63],v[136:139]\n"
TR "a[216:217],v0 offset:32768\n"
MF "v[136:139],a[204:207],a[100:103],v[136:139]\n"
"s_add_u32 m0,s62,16384\n"
TR "a[218:219],v0 offset:33280\n"
MF "v[144:147],a[204:207],a[104:107],v[144:147]\n"
"s_add_u32 s82,s66,7078144\n"
"ds_read_b128 a[140:143],v2 offset:20480\n"
MF "v[144:147],a[200:203],a[64:67],v[144:147]\n"
"s_sub_u32 s15,s82,7864320\n"
TR "a[220:221],v0 offset:36864\n"
MF "v[152:155],a[200:203],a[68:71],v[152:155]\n"
"s_min_u32 s82,s82,s15\n"
TR "a[222:223],v0 offset:37376\n"
MF "v[152:155],a[204:207],a[108:111],v[152:155]\n"
LD "v6,s[28:31],s82 offen lds\n"
MF "v[160:163],a[204:207],a[112:115],v[160:163]\n"
"ds_read_b128 a[180:183],v3 offset:20480\n"
MF "v[160:163],a[200:203],a[72:75],v[160:163]\n"
MF "v[168:171],a[200:203],a[76:79],v[168:171]\n"
MF "v[168:171],a[204:207],a[116:119],v[168:171]\n"
LD "v11,s[28:31],s82 offen offset:1024 lds\n"
MF "v[172:175],a[212:215],a[116:119],v[172:175]\n"
TR "a[200:201],v0 offset:32776\n"
MF "v[172:175],a[208:211],a[76:79],v[172:175]\n"
TR "a[202:203],v0 offset:33288\n"
MF "v[164:167],a[208:211],a[72:75],v[164:167]\n"
"ds_read_b128 a[144:147],v2 offset:22528\n"
MF "v[164:167],a[212:215],a[112:115],v[164:167]\n"
TR "a[204:205],v0 offset:36872\n"
MF "v[156:159],a[212:215],a[108:111],v[156:159]\n"
TR "a[206:207],v0 offset:37384\n"
MF "v[156:159],a[208:211],a[68:71],v[156:159]\n"
LD "v12,s[28:31],s82 offen offset:2048 lds\n"
MF "v[148:151],a[208:211],a[64:67],v[148:151]\n"
"ds_read_b128 a[184:187],v3 offset:22528\n"
MF "v[148:151],a[212:215],a[104:107],v[148:151]\n"
MF "v[140:143],a[212:215],a[100:103],v[140:143]\n"
MF "v[140:143],a[208:211],a[60:63],v[140:143]\n"
"s_waitcnt lgkmcnt(7)\n"
LD "v13,s[28:31],s82 offen offset:3072 lds\n"
MF "v[176:179],a[216:219],a[60:63],v[176:179]\n"
TR "a[208:209],v1 offset:32768\n"
MF "v[176:179],a[220:223],a[100:103],v[176:179]\n"
TR "a[210:211],v1 offset:33280\n"
MF "v[184:187],a[220:223],a[104:107],v[184:187]\n"
"ds_read_b128 a[148:151],v2 offset:24576\n"
MF "v[184:187],a[216:219],a[64:67],v[184:187]\n"
"s_add_u32 s84,s14,30720\n"
TR "a[212:213],v1 offset:36864\n"
MF "v[192:195],a[216:219],a[68:71],v[192:195]\n"
"s_add_u32 m0,s63,3072\n"
TR "a[214:215],v1 offset:37376\n"
MF "v[192:195],a[220:223],a[108:111],v[192:195]\n"
LD "v5,s[24:27],s84 offen lds\n"
MF "v[200:203],a[220:223],a[112:115],v[200:203]\n"
"ds_read_b128 a[188:191],v3 offset:24576\n"
MF "v[200:203],a[216:219],a[72:75],v[200:203]\n"
MF "v[208:211],a[216:219],a[76:79],v[208:211]\n"
"s_add_u32 s84,s14,39936\n"
MF "v[208:211],a[220:223],a[116:119],v[208:211]\n"
"s_waitcnt lgkmcnt(7)\n"
LD "v4,s[24:27],s84 offen offset:1024 lds\n"
MF "v[212:215],a[204:207],a[116:119],v[212:215]\n"
TR "a[216:217],v1 offset:32776\n"
MF "v[212:215],a[200:203],a[76:79],v[212:215]\n"
TR "a[218:219],v1 offset:33288\n"
MF "v[204:207],a[200:203],a[72:75],v[204:207]\n"
"ds_read_b128 a[152:155],v2 offset:26624\n"
MF "v[204:207],a[204:207],a[112:115],v[204:207]\n"
TR "a[220:221],v1 offset:36872\n"
MF "v[196:199],a[204:207],a[108:111],v[196:199]\n"
TR "a[222:223],v1 offset:37384\n"
MF "v[196:199],a[200:203],a[68:71],v[196:199]\n"
MF "v[188:191],a[200:203],a[64:67],v[188:191]\n"
"v_add_u32 v0,0xc000,v0\n"
MF "v[188:191],a[204:207],a[104:107],v[188:191]\n"
"v_add_u32 v1,0xc000,v1\n"
MF "v[180:183],a[204:207],a[100:103],v[180:183]\n"
"s_branch .Lqj\n"
".Lpgq0_c:\n"
"s_cbranch_execz .Lpgqy_c\n"
".Lpgq0_f:\n"
"s_cbranch_execz .Lpg12f\n"
".Lq0:\n"
MF "v[16:19],a[200:203],a[60:63],0\n"
"s_add_u32 m0,s62,49152\n"
"s_add_u32 s82,s66,786432\n"
TR "a[216:217],v1\n"
MF "v[16:19],a[204:207],a[100:103],v[16:19]\n"
"s_sub_u32 s15,s82,7864320\n"
"s_min_u32 s82,s82,s15\n"
TR "a[218:219],v1 offset:512\n"
MF "v[24:27],a[204:207],a[104:107],0\n"
LD "v6,s[28:31],s82 offen lds\n"
MF "v[24:27],a[200:203],a[64:67],v[24:27]\n"
TR "a[220:221],v1 offset:4096\n"
MF "v[32:35],a[200:203],a[68:71],0\n"
TR "a[222:223],v1 offset:4608\n"
MF "v[32:35],a[204:207],a[108:111],v[32:35]\n"
"v_max3_f32 v220,|v16|,|v17|,|v18|\n"
"v_max_f32_e64 v220,v220,|v19|\n"
MF "v[40:43],a[204:207],a[112:115],0\n"
"v_readlane_b32 s12,v220,3\n"
"v_readlane_b32 s6,v220,22\n"
MF "v[40:43],a[200:203],a[72:75],v[40:43]\n"
"s_min_u32 s12,s12,s6\n"
"v_readlane_b32 s7,v220,41\n"
"v_readlane_b32 s8,v220,60\n"
MF "v[48:51],a[200:203],a[76:79],0\n"
"s_min_u32 s7,s7,s8\n"
MF "v[48:51],a[204:207],a[116:119],v[48:51]\n"
LD "v11,s[28:31],s82 offen offset:1024 lds\n"
MF "v[52:55],a[212:215],a[116:119],0\n"
"s_min_u32 s12,s12,s7\n"
TR "a[200:201],v1 offset:8\n"
MF "v[52:55],a[208:211],a[76:79],v[52:55]\n"
"s_and_b32 s12,s12,0x7f800000\n"
TR "a[202:203],v1 offset:520\n"
MF "v[44:47],a[208:211],a[72:75],0\n"
"s_sub_u32 s12,s12,0x14000000\n"
MF "v[44:47],a[212:215],a[112:115],v[44:47]\n"
"s_cmp_lt_u32 s12,0x50000000\n"
"s_cselect_b32 s12,s12,0xe8400000\n"
TR "a[204:205],v1 offset:4104\n"
MF "v[36:39],a[212:215],a[108:111],0\n"
"s_add_u32 s12,s12,0x17c00000\n"
TR "a[206:207],v1 offset:4616\n"
MF "v[36:39],a[208:211],a[68:71],v[36:39]\n"
"v_mov_b32 v220,s12\n"
"v_mov_b32 v221,s12\n"
MF "v[28:31],a[208:211],a[64:67],0\n"
"v_mov_b32 v222,s12\n"
"v_mov_b32 v223,s12\n"
MF "v[28:31],a[212:215],a[104:107],v[28:31]\n"
LD "v12,s[28:31],s82 offen offset:2048 lds\n"
MF "v[20:23],a[212:215],a[100:103],0\n"
MF "v[20:23],a[208:211],a[60:63],v[20:23]\n"
"s_waitcnt vmcnt(7) lgkmcnt(0)\n"
"s_barrier\n"
MF "v[56:59],a[216:219],a[60:63],v[220:223]\n"
TR "a[208:209],v0 offset:16384\n"
MF "v[56:59],a[220:223],a[100:103],v[56:59]\n"
TR "a[210:211],v0 offset:16896\n"
MF "v[64:67],a[220:223],a[104:107],v[220:223]\n"
LD "v13,s[28:31],s82 offen offset:3072 lds\n"
MF "v[64:67],a[216:219],a[64:67],v[64:67]\n"
"s_add_u32 m0,s62,65536\n"
TR "a[212:213],v0 offset:20480\n"
MF "v[72:75],a[216:219],a[68:71],v[220:223]\n"
"s_add_u32 s82,s66,786560\n"
TR "a[214:215],v0 offset:20992\n"
MF "v[72:75],a[220:223],a[108:111],v[72:75]\n"
"s_sub_u32 s15,s82,7864320\n"
MF "v[80:83],a[220:223],a[112:115],v[220:223]\n"
"s_min_u32 s82,s82,s15\n"
MF "v[80:83],a[216:219],a[72:75],v[80:83]\n"
LD "v6,s[28:31],s82 offen lds\n"
MF "v[88:91],a[216:219],a[76:79],v[220:223]\n"
MF "v[88:91],a[220:223],a[116:119],v[88:91]\n"
MF "v[92:95],a[204:207],a[116:119],v[220:223]\n"
TR "a[216:217],v0 offset:16392\n"
MF "v[92:95],a[200:203],a[76:79],v[92:95]\n"
TR "a[218:219],v0 offset:16904\n"
MF "v[84:87],a[200:203],a[72:75],v[220:223]\n"
LD "v11,s[28:31],s82 offen offset:1024 lds\n"
MF "v[84:87],a[204:207],a[112:115],v[84:87]\n"
TR "a[220:221],v0 offset:20488\n"
MF "v[76:79],a[204:207],a[108:111],v[220:223]\n"
TR "a[222:223],v0 offset:21000\n"
MF "v[76:79],a[200:203],a[68:71],v[76:79]\n"
MF "v[68:71],a[200:203],a[64:67],v[220:223]\n"
MF "v[68:71],a[204:207],a[104:107],v[68:71]\n"
LD "v12,s[28:31],s82 offen offset:2048 lds\n"
MF "v[60:63],a[204:207],a[100:103],v[220:223]\n"
MF "v[60:63],a[200:203],a[60:63],v[60:63]\n"
"s_waitcnt lgkmcnt(4)\n"
MF "v[96:99],a[208:211],a[60:63],v[220:223]\n"
TR "a[200:201],v1 offset:16384\n"
MF "v[96:99],a[212:215],a[100:103],v[96:99]\n"
TR "a[202:203],v1 offset:16896\n"
MF "v[104:107],a[212:215],a[104:107],v[220:223]\n"
LD "v13,s[28:31],s82 offen offset:3072 lds\n"
MF "v[104:107],a[208:211],a[64:67],v[104:107]\n"
"s_add_u32 m0,s62,81920\n"
TR "a[204:205],v1 offset:20480\n"
MF "v[112:115],a[208:211],a[68:71],v[220:223]\n"
"s_add_u32 s82,s66,786688\n"
TR "a[206:207],v1 offset:20992\n"
MF "v[112:115],a[212:215],a[108:111],v[112:115]\n"
"s_sub_u32 s15,s82,7864320\n"
MF "v[120:123],a[212:215],a[112:115],v[220:223]\n"
"s_min_u32 s82,s82,s15\n"
MF "v[120:123],a[208:211],a[72:75],v[120:123]\n"
LD "v6,s[28:31],s82 offen lds\n"
MF "v[128:131],a[208:211],a[76:79],v[220:223]\n"
MF "v[128:131],a[212:215],a[116:119],v[128:131]\n"
"s_waitcnt lgkmcnt(4)\n"
MF "v[132:135],a[220:223],a[116:119],v[220:223]\n"
TR "a[208:209],v1 offset:16392\n"
MF "v[132:135],a[216:219],a[76:79],v[132:135]\n"
TR "a[210:211],v1 offset:16904\n"
MF "v[124:127],a[216:219],a[72:75],v[220:223]\n"
LD "v11,s[28:31],s82 offen offset:1024 lds\n"
MF "v[124:127],a[220:223],a[112:115],v[124:127]\n"
TR "a[212:213],v1 offset:20488\n"
MF "v[116:119],a[220:223],a[108:111],v[220:223]\n"
TR "a[214:215],v1 offset:21000\n"
MF "v[116:119],a[216:219],a[68:71],v[116:119]\n"
MF "v[108:111],a[216:219],a[64:67],v[220:223]\n"
MF "v[108:111],a[220:223],a[104:107],v[108:111]\n"
LD "v12,s[28:31],s82 offen offset:2048 lds\n"
MF "v[100:103],a[220:223],a[100:103],v[220:223]\n"
MF "v[100:103],a[216:219],a[60:63],v[100:103]\n"
"s_waitcnt vmcnt(11) lgkmcnt(0)\n"
"s_barrier\n"
MF "v[136:139],a[200:203],a[60:63],v[220:223]\n"
TR "a[216:217],v0 offset:32768\n"
MF "v[136:139],a[204:207],a[100:103],v[136:139]\n"
TR "a[218:219],v0 offset:33280\n"
MF "v[144:147],a[204:207],a[104:107],v[220:223]\n"
"ds_read_b128 a[140:143],v2 offset:20480\n"
MF "v[144:147],a[200:203],a[64:67],v[144:147]\n"
TR "a[220:221],v0 offset:36864\n"
MF "v[152:155],a[200:203],a[68:71],v[220:223]\n"
TR "a[222:223],v0 offset:37376\n"
MF "v[152:155],a[204:207],a[108:111],v[152:155]\n"
LD "v13,s[28:31],s82 offen offset:3072 lds\n"
MF "v[160:163],a[204:207],a[112:115],v[220:223]\n"
"ds_read_b128 a[180:183],v3 offset:20480\n"
MF "v[160:163],a[200:203],a[72:75],v[160:163]\n"
"s_add_u32 m0,s62,0\n"
MF "v[168:171],a[200:203],a[76:79],v[220:223]\n"
"s_add_u32 s82,s66,1572864\n"
"s_sub_u32 s15,s82,7864320\n"
"s_min_u32 s82,s82,s15\n"
MF "v[168:171],a[204:207],a[116:119],v[168:171]\n"
LD "v6,s[28:31],s82 offen lds\n"
MF "v[172:175],a[212:215],a[116:119],v[220:223]\n"
TR "a[200:201],v0 offset:32776\n"
MF "v[172:175],a[208:211],a[76:79],v[172:175]\n"
TR "a[202:203],v0 offset:33288\n"
MF "v[164:167],a[208:211],a[72:75],v[220:223]\n"
"ds_read_b128 a[144:147],v2 offset:22528\n"
MF "v[164:167],a[212:215],a[112:115],v[164:167]\n"
TR "a[204:205],v0 offset:36872\n"
MF "v[156:159],a[212:215],a[108:111],v[220:223]\n"
TR "a[206:207],v0 offset:37384\n"
MF "v[156:159],a[208:211],a[68:71],v[156:159]\n"
LD "v11,s[28:31],s82 offen offset:1024 lds\n"
MF "v[148:151],a[208:211],a[64:67],v[220:223]\n"
"ds_read_b128 a[184:187],v3 offset:22528\n"
MF "v[148:151],a[212:215],a[104:107],v[148:151]\n"
MF "v[140:143],a[212:215],a[100:103],v[220:223]\n"
MF "v[140:143],a[208:211],a[60:63],v[140:143]\n"
"s_waitcnt lgkmcnt(7)\n"
LD "v12,s[28:31],s82 offen offset:2048 lds\n"
MF "v[176:179],a[216:219],a[60:63],v[220:223]\n"
TR "a[208:209],v1 offset:32768\n"
MF "v[176:179],a[220:223],a[100:103],v[176:179]\n"
TR "a[210:211],v1 offset:33280\n"
MF "v[184:187],a[220:223],a[104:107],v[220:223]\n"
".Lpg4c:\n"
"s_cbranch_execz .Lpg13c\n"
".Lpg4f:\n"
"s_cbranch_execz .LpgattB_f\n"
"ds_read_b128 a[148:151],v2 offset:24576\n"
MF "v[184:187],a[216:219],a[64:67],v[184:187]\n"
TR "a[212:213],v1 offset:36864\n"
MF "v[192:195],a[216:219],a[68:71],v[220:223]\n"
TR "a[214:215],v1 offset:37376\n"
MF "v[192:195],a[220:223],a[108:111],v[192:195]\n"
LD "v13,s[28:31],s82 offen offset:3072 lds\n"
MF "v[200:203],a[220:223],a[112:115],v[220:223]\n"
"ds_read_b128 a[188:191],v3 offset:24576\n"
MF "v[200:203],a[216:219],a[72:75],v[200:203]\n"
"s_add_u32 s14,s81,384\n"
"s_sub_u32 s15,s14,1280\n"
"s_min_u32 s14,s14,s15\n"
MF "v[208:211],a[216:219],a[76:79],v[220:223]\n"
"s_add_u32 s14,s14,s68\n"
"s_add_u32 s84,s14,0\n"
"s_add_u32 m0,s63,0\n"
MF "v[208:211],a[220:223],a[116:119],v[208:211]\n"
"s_waitcnt lgkmcnt(7)\n"
LD "v4,s[24:27],s84 offen lds\n"
MF "v[212:215],a[204:207],a[116:119],v[220:223]\n"
TR "a[216:217],v1 offset:32776\n"
MF "v[212:215],a[200:203],a[76:79],v[212:215]\n"
TR "a[218:219],v1 offset:33288\n"
MF "v[204:207],a[200:203],a[72:75],v[220:223]\n"
"ds_read_b128 a[152:155],v2 offset:26624\n"
MF "v[204:207],a[204:207],a[112:115],v[204:207]\n"
TR "a[220:221],v1 offset:36872\n"
MF "v[196:199],a[204:207],a[108:111],v[220:223]\n"
"s_add_u32 s84,s14,9216\n"
TR "a[222:223],v1 offset:37384\n"
MF "v[196:199],a[200:203],a[68:71],v[196:199]\n"
LD "v5,s[24:27],s84 offen offset:1024 lds\n"
MF "v[188:191],a[200:203],a[64:67],v[220:223]\n"
"s_add_u32 m0,s62,16384\n"
"s_add_u32 s82,s66,1572992\n"
"v_add_u32 v0,0xc000,v0\n"
MF "v[188:191],a[204:207],a[104:107],v[188:191]\n"
"s_sub_u32 s15,s82,7864320\n"
"s_min_u32 s82,s82,s15\n"
"v_add_u32 v1,0xc000,v1\n"
MF "v[180:183],a[204:207],a[100:103],v[220:223]\n"
LD "v6,s[28:31],s82 offen lds\n"
MF "v[180:183],a[200:203],a[60:63],v[180:183]\n"
"s_waitcnt vmcnt(15) lgkmcnt(0)\n"
"s_barrier\n"
MF "v[216:219],a[208:211],a[60:63],v[220:223]\n"
TR "a[200:201],v0\n"
MF "v[216:219],a[212:215],a[100:103],v[216:219]\n"
TR "a[202:203],v0 offset:512\n"
MF "v[224:227],a[212:215],a[104:107],v[220:223]\n"
"ds_read_b128 a[192:195],v3 offset:26624\n"
MF "v[224:227],a[208:211],a[64:67],v[224:227]\n"
TR "a[204:205],v0 offset:4096\n"
MF "v[232:235],a[208:211],a[68:71],v[220:223]\n"
TR "a[206:207],v0 offset:4608\n"
MF "v[232:235],a[212:215],a[108:111],v[232:235]\n"
LD "v11,s[28:31],s82 offen offset:1024 lds\n"
MF "v[240:243],a[212:215],a[112:115],v[220:223]\n"
"ds_read_b128 a[156:159],v2 offset:28672\n"
MF "v[240:243],a[208:211],a[72:75],v[240:243]\n"
"ds_read_b128 a[196:199],v3 offset:28672\n"
MF "v[248:251],a[208:211],a[76:79],v[220:223]\n"
MF "v[248:251],a[212:215],a[116:119],v[248:251]\n"
LD "v12,s[28:31],s82 offen offset:2048 lds\n"
MF "v[252:255],a[220:223],a[116:119],v[220:223]\n"
TR "a[208:209],v0 offset:8\n"
MF "v[252:255],a[216:219],a[76:79],v[252:255]\n"
TR "a[210:211],v0 offset:520\n"
MF "v[244:247],a[216:219],a[72:75],v[220:223]\n"
MF "v[244:247],a[220:223],a[112:115],v[244:247]\n"
TR "a[212:213],v0 offset:4104\n"
MF "v[236:239],a[220:223],a[108:111],v[220:223]\n"
TR "a[214:215],v0 offset:4616\n"
MF "v[236:239],a[216:219],a[68:71],v[236:239]\n"
LD "v13,s[28:31],s82 offen offset:3072 lds\n"
MF "v[228:231],a[216:219],a[64:67],v[220:223]\n"
MF "v[228:231],a[220:223],a[104:107],v[228:231]\n"
"s_add_u32 s84,s14,20480\n"
MF "v[220:223],a[220:223],a[100:103],v[220:223]\n"
"s_add_u32 m0,s63,2048\n"
MF "v[220:223],a[216:219],a[60:63],v[220:223]\n"
"s_waitcnt lgkmcnt(4)\n"
LD "v4,s[24:27],s84 offen lds\n"
MF "v[16:19],a[200:203],a[140:143],v[16:19]\n"
TR "a[216:217],v1\n"
MF "v[16:19],a[204:207],a[180:183],v[16:19]\n"
TR "a[218:219],v1 offset:512\n"
MF "v[24:27],a[204:207],a[184:187],v[24:27]\n"
MF "v[24:27],a[200:203],a[144:147],v[24:27]\n"
TR "a[220:221],v1 offset:4096\n"
MF "v[32:35],a[200:203],a[148:151],v[32:35]\n"
"s_add_u32 s84,s14,29696\n"
TR "a[222:223],v1 offset:4608\n"
MF "v[32:35],a[204:207],a[188:191],v[32:35]\n"
LD "v5,s[24:27],s84 offen offset:1024 lds\n"
MF "v[40:43],a[204:207],a[192:195],v[40:43]\n"
MF "v[40:43],a[200:203],a[152:155],v[40:43]\n"
"s_add_u32 m0,s62,32768\n"
MF "v[48:51],a[200:203],a[156:159],v[48:51]\n"
"s_add_u32 s82,s66,1573120\n"
"s_sub_u32 s15,s82,7864320\n"
"s_min_u32 s82,s82,s15\n"
MF "v[48:51],a[204:207],a[196:199],v[48:51]\n"
"s_waitcnt lgkmcnt(4)\n"
LD "v6,s[28:31],s82 offen lds\n"
MF "v[52:55],a[212:215],a[196:199],v[52:55]\n"
TR "a[200:201],v1 offset:8\n"
MF "v[52:55],a[208:211],a[156:159],v[52:55]\n"
TR "a[202:203],v1 offset:520\n"
MF "v[44:47],a[208:211],a[152:155],v[44:47]\n"
MF "v[44:47],a[212:215],a[192:195],v[44:47]\n"
TR "a[204:205],v1 offset:4104\n"
MF "v[36:39],a[212:215],a[188:191],v[36:39]\n"
TR "a[206:207],v1 offset:4616\n"
MF "v[36:39],a[208:211],a[148:151],v[36:39]\n"
LD "v11,s[28:31],s82 offen offset:1024 lds\n"
MF "v[28:31],a[208:211],a[144:147],v[28:31]\n"
MF "v[28:31],a[212:215],a[184:187],v[28:31]\n"
MF "v[20:23],a[212:215],a[180:183],v[20:23]\n"
LD "v12,s[28:31],s82 offen offset:2048 lds\n"
"s_bitcmp0_b32 s23,12\n"
"s_cbranch_scc1 .Lisx2\n"
"s_mov_b64 exec,0\n"
"s_cmp_eq_u32 s56,0\n"
"s_cbranch_scc1 .Lpg11d\n"
"s_cmp_eq_u32 s56,1\n"
"s_cbranch_scc1 .Lpg10d\n"
"s_cmp_eq_u32 s56,2\n"
"s_cbranch_scc1 .Lpgt0kx_d\n"
"s_branch .Lpg8d\n"
".Lisr2:\n"
"s_mov_b64 exec,-1\n"
".Lisx2:\n"
MF "v[20:23],a[208:211],a[140:143],v[20:23]\n"
"s_waitcnt vmcnt(19) lgkmcnt(0)\n"
"s_barrier\n"
MF "v[56:59],a[216:219],a[140:143],v[56:59]\n"
TR "a[208:209],v0 offset:16384\n"
MF "v[56:59],a[220:223],a[180:183],v[56:59]\n"
TR "a[210:211],v0 offset:16896\n"
MF "v[64:67],a[220:223],a[184:187],v[64:67]\n"
LD "v13,s[28:31],s82 offen offset:3072 lds\n"
MF "v[64:67],a[216:219],a[144:147],v[64:67]\n"
TR "a[212:213],v0 offset:20480\n"
MF "v[72:75],a[216:219],a[148:151],v[72:75]\n"
TR "a[214:215],v0 offset:20992\n"
MF "v[72:75],a[220:223],a[188:191],v[72:75]\n"
"s_add_u32 s84,s14,40960\n"
MF "v[80:83],a[220:223],a[192:195],v[80:83]\n"
"s_add_u32 m0,s63,4096\n"
MF "v[80:83],a[216:219],a[152:155],v[80:83]\n"
LD "v4,s[24:27],s84 offen lds\n"
MF "v[88:91],a[216:219],a[156:159],v[88:91]\n"
MF "v[88:91],a[220:223],a[196:199],v[88:91]\n"
"s_sub_u32 s66,s66,s83\n"
MF "v[92:95],a[204:207],a[196:199],v[92:95]\n"
"s_add_u32 m0,s62,49152\n"
"s_add_u32 s82,s66,2359296\n"
TR "a[216:217],v0 offset:16392\n"
MF "v[92:95],a[200:203],a[156:159],v[92:95]\n"
"s_sub_u32 s15,s82,7864320\n"
"s_min_u32 s82,s82,s15\n"
TR "a[218:219],v0 offset:16904\n"
MF "v[84:87],a[200:203],a[152:155],v[84:87]\n"
LD "v6,s[28:31],s82 offen lds\n"
MF "v[84:87],a[204:207],a[192:195],v[84:87]\n"
TR "a[220:221],v0 offset:20488\n"
MF "v[76:79],a[204:207],a[188:191],v[76:79]\n"
TR "a[222:223],v0 offset:21000\n"
MF "v[76:79],a[200:203],a[148:151],v[76:79]\n"
MF "v[68:71],a[200:203],a[144:147],v[68:71]\n"
MF "v[68:71],a[204:207],a[184:187],v[68:71]\n"
LD "v11,s[28:31],s82 offen offset:1024 lds\n"
MF "v[60:63],a[204:207],a[180:183],v[60:63]\n"
MF "v[60:63],a[200:203],a[140:143],v[60:63]\n"
"s_waitcnt lgkmcnt(4)\n"
MF "v[96:99],a[208:211],a[140:143],v[96:99]\n"
TR "a[200:201],v1 offset:16384\n"
MF "v[96:99],a[212:215],a[180:183],v[96:99]\n"
TR "a[202:203],v1 offset:16896\n"
MF "v[104:107],a[212:215],a[184:187],v[104:107]\n"
LD "v12,s[28:31],s82 offen offset:2048 lds\n"
MF "v[104:107],a[208:211],a[144:147],v[104:107]\n"
TR "a[204:205],v1 offset:20480\n"
MF "v[112:115],a[208:211],a[148:151],v[112:115]\n"
TR "a[206:207],v1 offset:20992\n"
MF "v[112:115],a[212:215],a[188:191],v[112:115]\n"
MF "v[120:123],a[212:215],a[192:195],v[120:123]\n"
MF "v[120:123],a[208:211],a[152:155],v[120:123]\n"
LD "v13,s[28:31],s82 offen offset:3072 lds\n"
MF "v[128:131],a[208:211],a[156:159],v[128:131]\n"
MF "v[128:131],a[212:215],a[196:199],v[128:131]\n"
"s_add_u32 s14,s81,512\n"
"s_sub_u32 s15,s14,1280\n"
"s_waitcnt lgkmcnt(4)\n"
MF "v[132:135],a[220:223],a[196:199],v[132:135]\n"
"s_min_u32 s14,s14,s15\n"
"s_add_u32 s14,s14,s68\n"
TR "a[208:209],v1 offset:16392\n"
MF "v[132:135],a[216:219],a[156:159],v[132:135]\n"
"s_add_u32 s84,s14,0\n"
"s_add_u32 m0,s63,20480\n"
TR "a[210:211],v1 offset:16904\n"
MF "v[124:127],a[216:219],a[152:155],v[124:127]\n"
LD "v4,s[24:27],s84 offen lds\n"
MF "v[124:127],a[220:223],a[192:195],v[124:127]\n"
TR "a[212:213],v1 offset:20488\n"
MF "v[116:119],a[220:223],a[188:191],v[116:119]\n"
TR "a[214:215],v1 offset:21000\n"
MF "v[116:119],a[216:219],a[148:151],v[116:119]\n"
MF "v[108:111],a[216:219],a[144:147],v[108:111]\n"
"s_add_u32 s84,s14,9216\n"
MF "v[108:111],a[220:223],a[184:187],v[108:111]\n"
LD "v5,s[24:27],s84 offen offset:1024 lds\n"
MF "v[100:103],a[220:223],a[180:183],v[100:103]\n"
MF "v[100:103],a[216:219],a[140:143],v[100:103]\n"
"s_waitcnt vmcnt(23) lgkmcnt(0)\n"
"s_barrier\n"
MF "v[136:139],a[200:203],a[140:143],v[136:139]\n"
TR "a[216:217],v0 offset:32768\n"
MF "v[136:139],a[204:207],a[180:183],v[136:139]\n"
"s_add_u32 m0,s62,65536\n"
TR "a[218:219],v0 offset:33280\n"
MF "v[144:147],a[204:207],a[184:187],v[144:147]\n"
"s_add_u32 s82,s66,2359424\n"
"ds_read_b128 a[60:63],v2 offset:40960\n"
MF "v[144:147],a[200:203],a[144:147],v[144:147]\n"
"s_sub_u32 s15,s82,7864320\n"
TR "a[220:221],v0 offset:36864\n"
MF "v[152:155],a[200:203],a[148:151],v[152:155]\n"
"s_min_u32 s82,s82,s15\n"
TR "a[222:223],v0 offset:37376\n"
MF "v[152:155],a[204:207],a[188:191],v[152:155]\n"
LD "v6,s[28:31],s82 offen lds\n"
MF "v[160:163],a[204:207],a[192:195],v[160:163]\n"
"ds_read_b128 a[100:103],v3 offset:40960\n"
MF "v[160:163],a[200:203],a[152:155],v[160:163]\n"
LD "v11,s[28:31],s82 offen offset:1024 lds\n"
MF "v[168:171],a[200:203],a[156:159],v[168:171]\n"
MF "v[168:171],a[204:207],a[196:199],v[168:171]\n"
LD "v12,s[28:31],s82 offen offset:2048 lds\n"
MF "v[172:175],a[212:215],a[196:199],v[172:175]\n"
TR "a[200:201],v0 offset:32776\n"
MF "v[172:175],a[208:211],a[156:159],v[172:175]\n"
TR "a[202:203],v0 offset:33288\n"
MF "v[164:167],a[208:211],a[152:155],v[164:167]\n"
"ds_read_b128 a[64:67],v2 offset:43008\n"
MF "v[164:167],a[212:215],a[192:195],v[164:167]\n"
TR "a[204:205],v0 offset:36872\n"
MF "v[156:159],a[212:215],a[188:191],v[156:159]\n"
TR "a[206:207],v0 offset:37384\n"
MF "v[156:159],a[208:211],a[148:151],v[156:159]\n"
MF "v[148:151],a[208:211],a[144:147],v[148:151]\n"
"ds_read_b128 a[104:107],v3 offset:43008\n"
MF "v[148:151],a[212:215],a[184:187],v[148:151]\n"
MF "v[140:143],a[212:215],a[180:183],v[140:143]\n"
LD "v13,s[28:31],s82 offen offset:3072 lds\n"
MF "v[140:143],a[208:211],a[140:143],v[140:143]\n"
"s_waitcnt lgkmcnt(7)\n"
MF "v[176:179],a[216:219],a[140:143],v[176:179]\n"
TR "a[208:209],v1 offset:32768\n"
MF "v[176:179],a[220:223],a[180:183],v[176:179]\n"
TR "a[210:211],v1 offset:33280\n"
MF "v[184:187],a[220:223],a[184:187],v[184:187]\n"
"ds_read_b128 a[68:71],v2 offset:45056\n"
MF "v[184:187],a[216:219],a[144:147],v[184:187]\n"
"s_add_u32 s84,s14,20480\n"
TR "a[212:213],v1 offset:36864\n"
MF "v[192:195],a[216:219],a[148:151],v[192:195]\n"
"s_add_u32 m0,s63,22528\n"
TR "a[214:215],v1 offset:37376\n"
MF "v[192:195],a[220:223],a[188:191],v[192:195]\n"
LD "v4,s[24:27],s84 offen lds\n"
MF "v[200:203],a[220:223],a[192:195],v[200:203]\n"
"ds_read_b128 a[108:111],v3 offset:45056\n"
MF "v[200:203],a[216:219],a[152:155],v[200:203]\n"
MF "v[208:211],a[216:219],a[156:159],v[208:211]\n"
"s_add_u32 s84,s14,29696\n"
MF "v[208:211],a[220:223],a[196:199],v[208:211]\n"
"s_waitcnt lgkmcnt(7)\n"
LD "v5,s[24:27],s84 offen offset:1024 lds\n"
MF "v[212:215],a[204:207],a[196:199],v[212:215]\n"
TR "a[216:217],v1 offset:32776\n"
MF "v[212:215],a[200:203],a[156:159],v[212:215]\n"
TR "a[218:219],v1 offset:33288\n"
MF "v[204:207],a[200:203],a[152:155],v[204:207]\n"
"ds_read_b128 a[72:75],v2 offset:47104\n"
MF "v[204:207],a[204:207],a[192:195],v[204:207]\n"
TR "a[220:221],v1 offset:36872\n"
MF "v[196:199],a[204:207],a[188:191],v[196:199]\n"
TR "a[222:223],v1 offset:37384\n"
MF "v[196:199],a[200:203],a[148:151],v[196:199]\n"
MF "v[188:191],a[200:203],a[144:147],v[188:191]\n"
"v_add_u32 v0,0xffff4000,v0\n"
MF "v[188:191],a[204:207],a[184:187],v[188:191]\n"
"v_add_u32 v1,0xffff4000,v1\n"
MF "v[180:183],a[204:207],a[180:183],v[180:183]\n"
MF "v[180:183],a[200:203],a[140:143],v[180:183]\n"
"s_waitcnt vmcnt(25) lgkmcnt(0)\n"
"s_barrier\n"
MF "v[216:219],a[208:211],a[140:143],v[216:219]\n"
TR "a[200:201],v0\n"
MF "v[216:219],a[212:215],a[180:183],v[216:219]\n"
"s_add_u32 m0,s62,81920\n"
TR "a[202:203],v0 offset:512\n"
MF "v[224:227],a[212:215],a[184:187],v[224:227]\n"
"s_add_u32 s82,s66,2359552\n"
"ds_read_b128 a[112:115],v3 offset:47104\n"
MF "v[224:227],a[208:211],a[144:147],v[224:227]\n"
"s_sub_u32 s15,s82,7864320\n"
TR "a[204:205],v0 offset:4096\n"
MF "v[232:235],a[208:211],a[148:151],v[232:235]\n"
"s_min_u32 s82,s82,s15\n"
TR "a[206:207],v0 offset:4608\n"
MF "v[232:235],a[212:215],a[188:191],v[232:235]\n"
LD "v6,s[28:31],s82 offen lds\n"
MF "v[240:243],a[212:215],a[192:195],v[240:243]\n"
"ds_read_b128 a[76:79],v2 offset:49152\n"
MF "v[240:243],a[208:211],a[152:155],v[240:243]\n"
"ds_read_b128 a[116:119],v3 offset:49152\n"
MF "v[248:251],a[208:211],a[156:159],v[248:251]\n"
LD "v11,s[28:31],s82 offen offset:1024 lds\n"
MF "v[248:251],a[212:215],a[196:199],v[248:251]\n"
MF "v[252:255],a[220:223],a[196:199],v[252:255]\n"
TR "a[208:209],v0 offset:8\n"
MF "v[252:255],a[216:219],a[156:159],v[252:255]\n"
TR "a[210:211],v0 offset:520\n"
MF "v[244:247],a[216:219],a[152:155],v[244:247]\n"
MF "v[244:247],a[220:223],a[192:195],v[244:247]\n"
TR "a[212:213],v0 offset:4104\n"
MF "v[236:239],a[220:223],a[188:191],v[236:239]\n"
TR "a[214:215],v0 offset:4616\n"
MF "v[236:239],a[216:219],a[148:151],v[236:239]\n"
LD "v12,s[28:31],s82 offen offset:2048 lds\n"
MF "v[228:231],a[216:219],a[144:147],v[228:231]\n"
MF "v[228:231],a[220:223],a[184:187],v[228:231]\n"
MF "v[220:223],a[220:223],a[180:183],v[220:223]\n"
MF "v[220:223],a[216:219],a[140:143],v[220:223]\n"
"s_waitcnt lgkmcnt(4)\n"
MF "v[16:19],a[200:203],a[60:63],v[16:19]\n"
TR "a[216:217],v1\n"
MF "v[16:19],a[204:207],a[100:103],v[16:19]\n"
TR "a[218:219],v1 offset:512\n"
MF "v[24:27],a[204:207],a[104:107],v[24:27]\n"
LD "v13,s[28:31],s82 offen offset:3072 lds\n"
MF "v[24:27],a[200:203],a[64:67],v[24:27]\n"
TR "a[220:221],v1 offset:4096\n"
MF "v[32:35],a[200:203],a[68:71],v[32:35]\n"
TR "a[222:223],v1 offset:4608\n"
MF "v[32:35],a[204:207],a[108:111],v[32:35]\n"
MF "v[40:43],a[204:207],a[112:115],v[40:43]\n"
MF "v[40:43],a[200:203],a[72:75],v[40:43]\n"
"s_add_u32 s84,s14,40960\n"
MF "v[48:51],a[200:203],a[76:79],v[48:51]\n"
"s_add_u32 m0,s63,24576\n"
MF "v[48:51],a[204:207],a[116:119],v[48:51]\n"
"s_waitcnt lgkmcnt(4)\n"
LD "v4,s[24:27],s84 offen lds\n"
MF "v[52:55],a[212:215],a[116:119],v[52:55]\n"
TR "a[200:201],v1 offset:8\n"
MF "v[52:55],a[208:211],a[76:79],v[52:55]\n"
TR "a[202:203],v1 offset:520\n"
MF "v[44:47],a[208:211],a[72:75],v[44:47]\n"
MF "v[44:47],a[212:215],a[112:115],v[44:47]\n"
TR "a[204:205],v1 offset:4104\n"
MF "v[36:39],a[212:215],a[108:111],v[36:39]\n"
TR "a[206:207],v1 offset:4616\n"
MF "v[36:39],a[208:211],a[68:71],v[36:39]\n"
MF "v[28:31],a[208:211],a[64:67],v[28:31]\n"
MF "v[28:31],a[212:215],a[104:107],v[28:31]\n"
MF "v[20:23],a[212:215],a[100:103],v[20:23]\n"
MF "v[20:23],a[208:211],a[60:63],v[20:23]\n"
"s_waitcnt vmcnt(24) lgkmcnt(0)\n"
"s_barrier\n"
MF "v[56:59],a[216:219],a[60:63],v[56:59]\n"
"s_add_u32 m0,s62,0\n"
"s_add_u32 s82,s66,3145728\n"
TR "a[208:209],v0 offset:16384\n"
MF "v[56:59],a[220:223],a[100:103],v[56:59]\n"
"s_sub_u32 s15,s82,7864320\n"
"s_min_u32 s82,s82,s15\n"
TR "a[210:211],v0 offset:16896\n"
MF "v[64:67],a[220:223],a[104:107],v[64:67]\n"
LD "v6,s[28:31],s82 offen lds\n"
MF "v[64:67],a[216:219],a[64:67],v[64:67]\n"
TR "a[212:213],v0 offset:20480\n"
MF "v[72:75],a[216:219],a[68:71],v[72:75]\n"
TR "a[214:215],v0 offset:20992\n"
MF "v[72:75],a[220:223],a[108:111],v[72:75]\n"
MF "v[80:83],a[220:223],a[112:115],v[80:83]\n"
LD "v11,s[28:31],s82 offen offset:1024 lds\n"
MF "v[80:83],a[216:219],a[72:75],v[80:83]\n"
MF "v[88:91],a[216:219],a[76:79],v[88:91]\n"
MF "v[88:91],a[220:223],a[116:119],v[88:91]\n"
MF "v[92:95],a[204:207],a[116:119],v[92:95]\n"
TR "a[216:217],v0 offset:16392\n"
MF "v[92:95],a[200:203],a[76:79],v[92:95]\n"
TR "a[218:219],v0 offset:16904\n"
MF "v[84:87],a[200:203],a[72:75],v[84:87]\n"
LD "v12,s[28:31],s82 offen offset:2048 lds\n"
MF "v[84:87],a[204:207],a[112:115],v[84:87]\n"
TR "a[220:221],v0 offset:20488\n"
MF "v[76:79],a[204:207],a[108:111],v[76:79]\n"
TR "a[222:223],v0 offset:21000\n"
MF "v[76:79],a[200:203],a[68:71],v[76:79]\n"
MF "v[68:71],a[200:203],a[64:67],v[68:71]\n"
MF "v[68:71],a[204:207],a[104:107],v[68:71]\n"
MF "v[60:63],a[204:207],a[100:103],v[60:63]\n"
LD "v13,s[28:31],s82 offen offset:3072 lds\n"
MF "v[60:63],a[200:203],a[60:63],v[60:63]\n"
"s_waitcnt lgkmcnt(4)\n"
MF "v[96:99],a[208:211],a[60:63],v[96:99]\n"
TR "a[200:201],v1 offset:16384\n"
MF "v[96:99],a[212:215],a[100:103],v[96:99]\n"
TR "a[202:203],v1 offset:16896\n"
MF "v[104:107],a[212:215],a[104:107],v[104:107]\n"
"s_add_u32 s14,s81,640\n"
"s_sub_u32 s15,s14,1280\n"
MF "v[104:107],a[208:211],a[64:67],v[104:107]\n"
"s_min_u32 s14,s14,s15\n"
"s_add_u32 s14,s14,s68\n"
TR "a[204:205],v1 offset:20480\n"
MF "v[112:115],a[208:211],a[68:71],v[112:115]\n"
"s_add_u32 s84,s14,0\n"
"s_add_u32 m0,s63,40960\n"
TR "a[206:207],v1 offset:20992\n"
MF "v[112:115],a[212:215],a[108:111],v[112:115]\n"
LD "v4,s[24:27],s84 offen lds\n"
MF "v[120:123],a[212:215],a[112:115],v[120:123]\n"
MF "v[120:123],a[208:211],a[72:75],v[120:123]\n"
MF "v[128:131],a[208:211],a[76:79],v[128:131]\n"
"s_add_u32 s84,s14,9216\n"
MF "v[128:131],a[212:215],a[116:119],v[128:131]\n"
"s_waitcnt lgkmcnt(4)\n"
LD "v5,s[24:27],s84 offen offset:1024 lds\n"
MF "v[132:135],a[220:223],a[116:119],v[132:135]\n"
TR "a[208:209],v1 offset:16392\n"
MF "v[132:135],a[216:219],a[76:79],v[132:135]\n"
TR "a[210:211],v1 offset:16904\n"
MF "v[124:127],a[216:219],a[72:75],v[124:127]\n"
MF "v[124:127],a[220:223],a[112:115],v[124:127]\n"
TR "a[212:213],v1 offset:20488\n"
MF "v[116:119],a[220:223],a[108:111],v[116:119]\n"
TR "a[214:215],v1 offset:21000\n"
MF "v[116:119],a[216:219],a[68:71],v[116:119]\n"
MF "v[108:111],a[216:219],a[64:67],v[108:111]\n"
MF "v[108:111],a[220:223],a[104:107],v[108:111]\n"
MF "v[100:103],a[220:223],a[100:103],v[100:103]\n"
MF "v[100:103],a[216:219],a[60:63],v[100:103]\n"
"s_waitcnt vmcnt(23) lgkmcnt(0)\n"
"s_barrier\n"
MF "v[136:139],a[200:203],a[60:63],v[136:139]\n"
TR "a[216:217],v0 offset:32768\n"
MF "v[136:139],a[204:207],a[100:103],v[136:139]\n"
"s_add_u32 m0,s62,16384\n"
TR "a[218:219],v0 offset:33280\n"
MF "v[144:147],a[204:207],a[104:107],v[144:147]\n"
"s_add_u32 s82,s66,3145856\n"
"ds_read_b128 a[140:143],v2\n"
MF "v[144:147],a[200:203],a[64:67],v[144:147]\n"
"s_sub_u32 s15,s82,7864320\n"
TR "a[220:221],v0 offset:36864\n"
MF "v[152:155],a[200:203],a[68:71],v[152:155]\n"
"s_min_u32 s82,s82,s15\n"
TR "a[222:223],v0 offset:37376\n"
MF "v[152:155],a[204:207],a[108:111],v[152:155]\n"
LD "v6,s[28:31],s82 offen lds\n"
MF "v[160:163],a[204:207],a[112:115],v[160:163]\n"
"ds_read_b128 a[180:183],v3\n"
MF "v[160:163],a[200:203],a[72:75],v[160:163]\n"
LD "v11,s[28:31],s82 offen offset:1024 lds\n"
MF "v[168:171],a[200:203],a[76:79],v[168:171]\n"
MF "v[168:171],a[204:207],a[116:119],v[168:171]\n"
LD "v12,s[28:31],s82 offen offset:2048 lds\n"
MF "v[172:175],a[212:215],a[116:119],v[172:175]\n"
TR "a[200:201],v0 offset:32776\n"
MF "v[172:175],a[208:211],a[76:79],v[172:175]\n"
TR "a[202:203],v0 offset:33288\n"
MF "v[164:167],a[208:211],a[72:75],v[164:167]\n"
"ds_read_b128 a[144:147],v2 offset:2048\n"
MF "v[164:167],a[212:215],a[112:115],v[164:167]\n"
TR "a[204:205],v0 offset:36872\n"
MF "v[156:159],a[212:215],a[108:111],v[156:159]\n"
TR "a[206:207],v0 offset:37384\n"
MF "v[156:159],a[208:211],a[68:71],v[156:159]\n"
MF "v[148:151],a[208:211],a[64:67],v[148:151]\n"
"ds_read_b128 a[184:187],v3 offset:2048\n"
MF "v[148:151],a[212:215],a[104:107],v[148:151]\n"
MF "v[140:143],a[212:215],a[100:103],v[140:143]\n"
LD "v13,s[28:31],s82 offen offset:3072 lds\n"
MF "v[140:143],a[208:211],a[60:63],v[140:143]\n"
"s_waitcnt lgkmcnt(7)\n"
MF "v[176:179],a[216:219],a[60:63],v[176:179]\n"
TR "a[208:209],v1 offset:32768\n"
MF "v[176:179],a[220:223],a[100:103],v[176:179]\n"
TR "a[210:211],v1 offset:33280\n"
MF "v[184:187],a[220:223],a[104:107],v[184:187]\n"
"ds_read_b128 a[148:151],v2 offset:4096\n"
MF "v[184:187],a[216:219],a[64:67],v[184:187]\n"
"s_add_u32 s84,s14,20480\n"
TR "a[212:213],v1 offset:36864\n"
MF "v[192:195],a[216:219],a[68:71],v[192:195]\n"
"s_add_u32 m0,s63,43008\n"
TR "a[214:215],v1 offset:37376\n"
MF "v[192:195],a[220:223],a[108:111],v[192:195]\n"
LD "v4,s[24:27],s84 offen lds\n"
MF "v[200:203],a[220:223],a[112:115],v[200:203]\n"
"ds_read_b128 a[188:191],v3 offset:4096\n"
MF "v[200:203],a[216:219],a[72:75],v[200:203]\n"
MF "v[208:211],a[216:219],a[76:79],v[208:211]\n"
"s_add_u32 s84,s14,29696\n"
MF "v[208:211],a[220:223],a[116:119],v[208:211]\n"
"s_waitcnt lgkmcnt(7)\n"
LD "v5,s[24:27],s84 offen offset:1024 lds\n"
MF "v[212:215],a[204:207],a[116:119],v[212:215]\n"
TR "a[216:217],v1 offset:32776\n"
MF "v[212:215],a[200:203],a[76:79],v[212:215]\n"
TR "a[218:219],v1 offset:33288\n"
MF "v[204:207],a[200:203],a[72:75],v[204:207]\n"
"ds_read_b128 a[152:155],v2 offset:6144\n"
MF "v[204:207],a[204:207],a[112:115],v[204:207]\n"
TR "a[220:221],v1 offset:36872\n"
MF "v[196:199],a[204:207],a[108:111],v[196:199]\n"
TR "a[222:223],v1 offset:37384\n"
MF "v[196:199],a[200:203],a[68:71],v[196:199]\n"
MF "v[188:191],a[200:203],a[64:67],v[188:191]\n"
"v_add_u32 v0,0xc000,v0\n"
MF "v[188:191],a[204:207],a[104:107],v[188:191]\n"
"v_add_u32 v1,0xc000,v1\n"
MF "v[180:183],a[204:207],a[100:103],v[180:183]\n"
MF "v[180:183],a[200:203],a[60:63],v[180:183]\n"
"s_waitcnt vmcnt(25) lgkmcnt(0)\n"
"s_barrier\n"
MF "v[216:219],a[208:211],a[60:63],v[216:219]\n"
TR "a[200:201],v0\n"
MF "v[216:219],a[212:215],a[100:103],v[216:219]\n"
"s_add_u32 m0,s62,32768\n"
TR "a[202:203],v0 offset:512\n"
MF "v[224:227],a[212:215],a[104:107],v[224:227]\n"
".Lpg5c:\n"
"s_cbranch_execz .Lpg14c\n"
".Lpg5f:\n"
"s_cbranch_execz .Lpgt0k_f\n"
"s_add_u32 s82,s66,3145984\n"
"ds_read_b128 a[192:195],v3 offset:6144\n"
MF "v[224:227],a[208:211],a[64:67],v[224:227]\n"
"s_sub_u32 s15,s82,7864320\n"
TR "a[204:205],v0 offset:4096\n"
MF "v[232:235],a[208:211],a[68:71],v[232:235]\n"
"s_min_u32 s82,s82,s15\n"
TR "a[206:207],v0 offset:4608\n"
MF "v[232:235],a[212:215],a[108:111],v[232:235]\n"
LD "v6,s[28:31],s82 offen lds\n"
MF "v[240:243],a[212:215],a[112:115],v[240:243]\n"
"ds_read_b128 a[156:159],v2 offset:8192\n"
MF "v[240:243],a[208:211],a[72:75],v[240:243]\n"
"ds_read_b128 a[196:199],v3 offset:8192\n"
MF "v[248:251],a[208:211],a[76:79],v[248:251]\n"
LD "v11,s[28:31],s82 offen offset:1024 lds\n"
MF "v[248:251],a[212:215],a[116:119],v[248:251]\n"
MF "v[252:255],a[220:223],a[116:119],v[252:255]\n"
TR "a[208:209],v0 offset:8\n"
MF "v[252:255],a[216:219],a[76:79],v[252:255]\n"
TR "a[210:211],v0 offset:520\n"
MF "v[244:247],a[216:219],a[72:75],v[244:247]\n"
MF "v[244:247],a[220:223],a[112:115],v[244:247]\n"
TR "a[212:213],v0 offset:4104\n"
MF "v[236:239],a[220:223],a[108:111],v[236:239]\n"
TR "a[214:215],v0 offset:4616\n"
MF "v[236:239],a[216:219],a[68:71],v[236:239]\n"
LD "v12,s[28:31],s82 offen offset:2048 lds\n"
MF "v[228:231],a[216:219],a[64:67],v[228:231]\n"
MF "v[228:231],a[220:223],a[104:107],v[228:231]\n"
MF "v[220:223],a[220:223],a[100:103],v[220:223]\n"
MF "v[220:223],a[216:219],a[60:63],v[220:223]\n"
"s_waitcnt lgkmcnt(4)\n"
MF "v[16:19],a[200:203],a[140:143],v[16:19]\n"
TR "a[216:217],v1\n"
MF "v[16:19],a[204:207],a[180:183],v[16:19]\n"
TR "a[218:219],v1 offset:512\n"
MF "v[24:27],a[204:207],a[184:187],v[24:27]\n"
LD "v13,s[28:31],s82 offen offset:3072 lds\n"
MF "v[24:27],a[200:203],a[144:147],v[24:27]\n"
TR "a[220:221],v1 offset:4096\n"
MF "v[32:35],a[200:203],a[148:151],v[32:35]\n"
TR "a[222:223],v1 offset:4608\n"
MF "v[32:35],a[204:207],a[188:191],v[32:35]\n"
MF "v[40:43],a[204:207],a[192:195],v[40:43]\n"
MF "v[40:43],a[200:203],a[152:155],v[40:43]\n"
"s_add_u32 s84,s14,40960\n"
MF "v[48:51],a[200:203],a[156:159],v[48:51]\n"
"s_add_u32 m0,s63,45056\n"
MF "v[48:51],a[204:207],a[196:199],v[48:51]\n"
"s_waitcnt lgkmcnt(4)\n"
LD "v4,s[24:27],s84 offen lds\n"
MF "v[52:55],a[212:215],a[196:199],v[52:55]\n"
TR "a[200:201],v1 offset:8\n"
MF "v[52:55],a[208:211],a[156:159],v[52:55]\n"
TR "a[202:203],v1 offset:520\n"
MF "v[44:47],a[208:211],a[152:155],v[44:47]\n"
MF "v[44:47],a[212:215],a[192:195],v[44:47]\n"
TR "a[204:205],v1 offset:4104\n"
MF "v[36:39],a[212:215],a[188:191],v[36:39]\n"
TR "a[206:207],v1 offset:4616\n"
MF "v[36:39],a[208:211],a[148:151],v[36:39]\n"
MF "v[28:31],a[208:211],a[144:147],v[28:31]\n"
MF "v[28:31],a[212:215],a[184:187],v[28:31]\n"
MF "v[20:23],a[212:215],a[180:183],v[20:23]\n"
MF "v[20:23],a[208:211],a[140:143],v[20:23]\n"
"s_waitcnt vmcnt(24) lgkmcnt(0)\n"
"s_barrier\n"
MF "v[56:59],a[216:219],a[140:143],v[56:59]\n"
"s_add_u32 m0,s62,49152\n"
"s_add_u32 s82,s66,3932160\n"
TR "a[208:209],v0 offset:16384\n"
MF "v[56:59],a[220:223],a[180:183],v[56:59]\n"
"s_sub_u32 s15,s82,7864320\n"
"s_min_u32 s82,s82,s15\n"
TR "a[210:211],v0 offset:16896\n"
MF "v[64:67],a[220:223],a[184:187],v[64:67]\n"
LD "v6,s[28:31],s82 offen lds\n"
MF "v[64:67],a[216:219],a[144:147],v[64:67]\n"
TR "a[212:213],v0 offset:20480\n"
MF "v[72:75],a[216:219],a[148:151],v[72:75]\n"
TR "a[214:215],v0 offset:20992\n"
MF "v[72:75],a[220:223],a[188:191],v[72:75]\n"
MF "v[80:83],a[220:223],a[192:195],v[80:83]\n"
LD "v11,s[28:31],s82 offen offset:1024 lds\n"
MF "v[80:83],a[216:219],a[152:155],v[80:83]\n"
MF "v[88:91],a[216:219],a[156:159],v[88:91]\n"
MF "v[88:91],a[220:223],a[196:199],v[88:91]\n"
MF "v[92:95],a[204:207],a[196:199],v[92:95]\n"
TR "a[216:217],v0 offset:16392\n"
MF "v[92:95],a[200:203],a[156:159],v[92:95]\n"
TR "a[218:219],v0 offset:16904\n"
MF "v[84:87],a[200:203],a[152:155],v[84:87]\n"
LD "v12,s[28:31],s82 offen offset:2048 lds\n"
MF "v[84:87],a[204:207],a[192:195],v[84:87]\n"
TR "a[220:221],v0 offset:20488\n"
MF "v[76:79],a[204:207],a[188:191],v[76:79]\n"
TR "a[222:223],v0 offset:21000\n"
MF "v[76:79],a[200:203],a[148:151],v[76:79]\n"
MF "v[68:71],a[200:203],a[144:147],v[68:71]\n"
MF "v[68:71],a[204:207],a[184:187],v[68:71]\n"
MF "v[60:63],a[204:207],a[180:183],v[60:63]\n"
LD "v13,s[28:31],s82 offen offset:3072 lds\n"
MF "v[60:63],a[200:203],a[140:143],v[60:63]\n"
"s_waitcnt lgkmcnt(4)\n"
MF "v[96:99],a[208:211],a[140:143],v[96:99]\n"
TR "a[200:201],v1 offset:16384\n"
MF "v[96:99],a[212:215],a[180:183],v[96:99]\n"
TR "a[202:203],v1 offset:16896\n"
MF "v[104:107],a[212:215],a[184:187],v[104:107]\n"
"s_add_u32 s14,s81,768\n"
"s_sub_u32 s15,s14,1280\n"
MF "v[104:107],a[208:211],a[144:147],v[104:107]\n"
"s_min_u32 s14,s14,s15\n"
"s_add_u32 s14,s14,s68\n"
TR "a[204:205],v1 offset:20480\n"
MF "v[112:115],a[208:211],a[148:151],v[112:115]\n"
"s_add_u32 s84,s14,0\n"
"s_add_u32 m0,s63,0\n"
TR "a[206:207],v1 offset:20992\n"
MF "v[112:115],a[212:215],a[188:191],v[112:115]\n"
LD "v4,s[24:27],s84 offen lds\n"
MF "v[120:123],a[212:215],a[192:195],v[120:123]\n"
MF "v[120:123],a[208:211],a[152:155],v[120:123]\n"
MF "v[128:131],a[208:211],a[156:159],v[128:131]\n"
"s_add_u32 s84,s14,9216\n"
MF "v[128:131],a[212:215],a[196:199],v[128:131]\n"
"s_waitcnt lgkmcnt(4)\n"
LD "v5,s[24:27],s84 offen offset:1024 lds\n"
MF "v[132:135],a[220:223],a[196:199],v[132:135]\n"
TR "a[208:209],v1 offset:16392\n"
MF "v[132:135],a[216:219],a[156:159],v[132:135]\n"
TR "a[210:211],v1 offset:16904\n"
MF "v[124:127],a[216:219],a[152:155],v[124:127]\n"
MF "v[124:127],a[220:223],a[192:195],v[124:127]\n"
TR "a[212:213],v1 offset:20488\n"
MF "v[116:119],a[220:223],a[188:191],v[116:119]\n"
TR "a[214:215],v1 offset:21000\n"
MF "v[116:119],a[216:219],a[148:151],v[116:119]\n"
MF "v[108:111],a[216:219],a[144:147],v[108:111]\n"
MF "v[108:111],a[220:223],a[184:187],v[108:111]\n"
MF "v[100:103],a[220:223],a[180:183],v[100:103]\n"
MF "v[100:103],a[216:219],a[140:143],v[100:103]\n"
"s_waitcnt vmcnt(23) lgkmcnt(0)\n"
"s_barrier\n"
MF "v[136:139],a[200:203],a[140:143],v[136:139]\n"
TR "a[216:217],v0 offset:32768\n"
MF "v[136:139],a[204:207],a[180:183],v[136:139]\n"
"s_add_u32 m0,s62,65536\n"
TR "a[218:219],v0 offset:33280\n"
MF "v[144:147],a[204:207],a[184:187],v[144:147]\n"
"s_add_u32 s82,s66,3932288\n"
"ds_read_b128 a[60:63],v2 offset:20480\n"
MF "v[144:147],a[200:203],a[144:147],v[144:147]\n"
"s_sub_u32 s15,s82,7864320\n"
TR "a[220:221],v0 offset:36864\n"
MF "v[152:155],a[200:203],a[148:151],v[152:155]\n"
"s_min_u32 s82,s82,s15\n"
TR "a[222:223],v0 offset:37376\n"
MF "v[152:155],a[204:207],a[188:191],v[152:155]\n"
LD "v6,s[28:31],s82 offen lds\n"
MF "v[160:163],a[204:207],a[192:195],v[160:163]\n"
"ds_read_b128 a[100:103],v3 offset:20480\n"
MF "v[160:163],a[200:203],a[152:155],v[160:163]\n"
LD "v11,s[28:31],s82 offen offset:1024 lds\n"
MF "v[168:171],a[200:203],a[156:159],v[168:171]\n"
MF "v[168:171],a[204:207],a[196:199],v[168:171]\n"
LD "v12,s[28:31],s82 offen offset:2048 lds\n"
MF "v[172:175],a[212:215],a[196:199],v[172:175]\n"
TR "a[200:201],v0 offset:32776\n"
MF "v[172:175],a[208:211],a[156:159],v[172:175]\n"
TR "a[202:203],v0 offset:33288\n"
MF "v[164:167],a[208:211],a[152:155],v[164:167]\n"
"ds_read_b128 a[64:67],v2 offset:22528\n"
MF "v[164:167],a[212:215],a[192:195],v[164:167]\n"
TR "a[204:205],v0 offset:36872\n"
MF "v[156:159],a[212:215],a[188:191],v[156:159]\n"
TR "a[206:207],v0 offset:37384\n"
MF "v[156:159],a[208:211],a[148:151],v[156:159]\n"
MF "v[148:151],a[208:211],a[144:147],v[148:151]\n"
"ds_read_b128 a[104:107],v3 offset:22528\n"
MF "v[148:151],a[212:215],a[184:187],v[148:151]\n"
MF "v[140:143],a[212:215],a[180:183],v[140:143]\n"
LD "v13,s[28:31],s82 offen offset:3072 lds\n"
MF "v[140:143],a[208:211],a[140:143],v[140:143]\n"
"s_waitcnt lgkmcnt(7)\n"
MF "v[176:179],a[216:219],a[140:143],v[176:179]\n"
TR "a[208:209],v1 offset:32768\n"
MF "v[176:179],a[220:223],a[180:183],v[176:179]\n"
TR "a[210:211],v1 offset:33280\n"
MF "v[184:187],a[220:223],a[184:187],v[184:187]\n"
"ds_read_b128 a[68:71],v2 offset:24576\n"
MF "v[184:187],a[216:219],a[144:147],v[184:187]\n"
"s_add_u32 s84,s14,20480\n"
TR "a[212:213],v1 offset:36864\n"
MF "v[192:195],a[216:219],a[148:151],v[192:195]\n"
"s_add_u32 m0,s63,2048\n"
TR "a[214:215],v1 offset:37376\n"
MF "v[192:195],a[220:223],a[188:191],v[192:195]\n"
LD "v4,s[24:27],s84 offen lds\n"
MF "v[200:203],a[220:223],a[192:195],v[200:203]\n"
"ds_read_b128 a[108:111],v3 offset:24576\n"
MF "v[200:203],a[216:219],a[152:155],v[200:203]\n"
MF "v[208:211],a[216:219],a[156:159],v[208:211]\n"
"s_add_u32 s84,s14,29696\n"
MF "v[208:211],a[220:223],a[196:199],v[208:211]\n"
"s_waitcnt lgkmcnt(7)\n"
LD "v5,s[24:27],s84 offen offset:1024 lds\n"
MF "v[212:215],a[204:207],a[196:199],v[212:215]\n"
TR "a[216:217],v1 offset:32776\n"
MF "v[212:215],a[200:203],a[156:159],v[212:215]\n"
TR "a[218:219],v1 offset:33288\n"
MF "v[204:207],a[200:203],a[152:155],v[204:207]\n"
"ds_read_b128 a[72:75],v2 offset:26624\n"
MF "v[204:207],a[204:207],a[192:195],v[204:207]\n"
TR "a[220:221],v1 offset:36872\n"
MF "v[196:199],a[204:207],a[188:191],v[196:199]\n"
TR "a[222:223],v1 offset:37384\n"
MF "v[196:199],a[200:203],a[148:151],v[196:199]\n"
MF "v[188:191],a[200:203],a[144:147],v[188:191]\n"
"v_add_u32 v0,0xffff4000,v0\n"
MF "v[188:191],a[204:207],a[184:187],v[188:191]\n"
"v_add_u32 v1,0xffff4000,v1\n"
MF "v[180:183],a[204:207],a[180:183],v[180:183]\n"
MF "v[180:183],a[200:203],a[140:143],v[180:183]\n"
"s_waitcnt vmcnt(25) lgkmcnt(0)\n"
"s_barrier\n"
MF "v[216:219],a[208:211],a[140:143],v[216:219]\n"
TR "a[200:201],v0\n"
MF "v[216:219],a[212:215],a[180:183],v[216:219]\n"
"s_add_u32 m0,s62,81920\n"
TR "a[202:203],v0 offset:512\n"
MF "v[224:227],a[212:215],a[184:187],v[224:227]\n"
"s_add_u32 s82,s66,3932416\n"
"ds_read_b128 a[112:115],v3 offset:26624\n"
MF "v[224:227],a[208:211],a[144:147],v[224:227]\n"
"s_sub_u32 s15,s82,7864320\n"
TR "a[204:205],v0 offset:4096\n"
MF "v[232:235],a[208:211],a[148:151],v[232:235]\n"
"s_min_u32 s82,s82,s15\n"
TR "a[206:207],v0 offset:4608\n"
MF "v[232:235],a[212:215],a[188:191],v[232:235]\n"
LD "v6,s[28:31],s82 offen lds\n"
MF "v[240:243],a[212:215],a[192:195],v[240:243]\n"
"ds_read_b128 a[76:79],v2 offset:28672\n"
MF "v[240:243],a[208:211],a[152:155],v[240:243]\n"
"ds_read_b128 a[116:119],v3 offset:28672\n"
MF "v[248:251],a[208:211],a[156:159],v[248:251]\n"
LD "v11,s[28:31],s82 offen offset:1024 lds\n"
MF "v[248:251],a[212:215],a[196:199],v[248:251]\n"
MF "v[252:255],a[220:223],a[196:199],v[252:255]\n"
TR "a[208:209],v0 offset:8\n"
MF "v[252:255],a[216:219],a[156:159],v[252:255]\n"
TR "a[210:211],v0 offset:520\n"
MF "v[244:247],a[216:219],a[152:155],v[244:247]\n"
MF "v[244:247],a[220:223],a[192:195],v[244:247]\n"
TR "a[212:213],v0 offset:4104\n"
MF "v[236:239],a[220:223],a[188:191],v[236:239]\n"
TR "a[214:215],v0 offset:4616\n"
MF "v[236:239],a[216:219],a[148:151],v[236:239]\n"
LD "v12,s[28:31],s82 offen offset:2048 lds\n"
MF "v[228:231],a[216:219],a[144:147],v[228:231]\n"
MF "v[228:231],a[220:223],a[184:187],v[228:231]\n"
MF "v[220:223],a[220:223],a[180:183],v[220:223]\n"
MF "v[220:223],a[216:219],a[140:143],v[220:223]\n"
"s_waitcnt lgkmcnt(4)\n"
MF "v[16:19],a[200:203],a[60:63],v[16:19]\n"
TR "a[216:217],v1\n"
MF "v[16:19],a[204:207],a[100:103],v[16:19]\n"
TR "a[218:219],v1 offset:512\n"
MF "v[24:27],a[204:207],a[104:107],v[24:27]\n"
LD "v13,s[28:31],s82 offen offset:3072 lds\n"
MF "v[24:27],a[200:203],a[64:67],v[24:27]\n"
TR "a[220:221],v1 offset:4096\n"
MF "v[32:35],a[200:203],a[68:71],v[32:35]\n"
TR "a[222:223],v1 offset:4608\n"
MF "v[32:35],a[204:207],a[108:111],v[32:35]\n"
MF "v[40:43],a[204:207],a[112:115],v[40:43]\n"
MF "v[40:43],a[200:203],a[72:75],v[40:43]\n"
"s_add_u32 s84,s14,40960\n"
MF "v[48:51],a[200:203],a[76:79],v[48:51]\n"
"s_add_u32 m0,s63,4096\n"
MF "v[48:51],a[204:207],a[116:119],v[48:51]\n"
"s_waitcnt lgkmcnt(4)\n"
LD "v4,s[24:27],s84 offen lds\n"
MF "v[52:55],a[212:215],a[116:119],v[52:55]\n"
TR "a[200:201],v1 offset:8\n"
MF "v[52:55],a[208:211],a[76:79],v[52:55]\n"
TR "a[202:203],v1 offset:520\n"
MF "v[44:47],a[208:211],a[72:75],v[44:47]\n"
MF "v[44:47],a[212:215],a[112:115],v[44:47]\n"
TR "a[204:205],v1 offset:4104\n"
MF "v[36:39],a[212:215],a[108:111],v[36:39]\n"
TR "a[206:207],v1 offset:4616\n"
MF "v[36:39],a[208:211],a[68:71],v[36:39]\n"
MF "v[28:31],a[208:211],a[64:67],v[28:31]\n"
MF "v[28:31],a[212:215],a[104:107],v[28:31]\n"
MF "v[20:23],a[212:215],a[100:103],v[20:23]\n"
MF "v[20:23],a[208:211],a[60:63],v[20:23]\n"
"s_waitcnt vmcnt(24) lgkmcnt(0)\n"
"s_barrier\n"
MF "v[56:59],a[216:219],a[60:63],v[56:59]\n"
"s_add_u32 m0,s62,0\n"
"s_add_u32 s82,s66,4718592\n"
TR "a[208:209],v0 offset:16384\n"
MF "v[56:59],a[220:223],a[100:103],v[56:59]\n"
"s_sub_u32 s15,s82,7864320\n"
"s_min_u32 s82,s82,s15\n"
TR "a[210:211],v0 offset:16896\n"
MF "v[64:67],a[220:223],a[104:107],v[64:67]\n"
LD "v6,s[28:31],s82 offen lds\n"
MF "v[64:67],a[216:219],a[64:67],v[64:67]\n"
TR "a[212:213],v0 offset:20480\n"
MF "v[72:75],a[216:219],a[68:71],v[72:75]\n"
TR "a[214:215],v0 offset:20992\n"
MF "v[72:75],a[220:223],a[108:111],v[72:75]\n"
MF "v[80:83],a[220:223],a[112:115],v[80:83]\n"
LD "v11,s[28:31],s82 offen offset:1024 lds\n"
MF "v[80:83],a[216:219],a[72:75],v[80:83]\n"
MF "v[88:91],a[216:219],a[76:79],v[88:91]\n"
MF "v[88:91],a[220:223],a[116:119],v[88:91]\n"
MF "v[92:95],a[204:207],a[116:119],v[92:95]\n"
TR "a[216:217],v0 offset:16392\n"
MF "v[92:95],a[200:203],a[76:79],v[92:95]\n"
TR "a[218:219],v0 offset:16904\n"
MF "v[84:87],a[200:203],a[72:75],v[84:87]\n"
LD "v12,s[28:31],s82 offen offset:2048 lds\n"
MF "v[84:87],a[204:207],a[112:115],v[84:87]\n"
TR "a[220:221],v0 offset:20488\n"
MF "v[76:79],a[204:207],a[108:111],v[76:79]\n"
TR "a[222:223],v0 offset:21000\n"
MF "v[76:79],a[200:203],a[68:71],v[76:79]\n"
MF "v[68:71],a[200:203],a[64:67],v[68:71]\n"
MF "v[68:71],a[204:207],a[104:107],v[68:71]\n"
MF "v[60:63],a[204:207],a[100:103],v[60:63]\n"
LD "v13,s[28:31],s82 offen offset:3072 lds\n"
MF "v[60:63],a[200:203],a[60:63],v[60:63]\n"
"s_waitcnt lgkmcnt(4)\n"
MF "v[96:99],a[208:211],a[60:63],v[96:99]\n"
TR "a[200:201],v1 offset:16384\n"
MF "v[96:99],a[212:215],a[100:103],v[96:99]\n"
TR "a[202:203],v1 offset:16896\n"
MF "v[104:107],a[212:215],a[104:107],v[104:107]\n"
"s_add_u32 s14,s81,896\n"
"s_sub_u32 s15,s14,1280\n"
MF "v[104:107],a[208:211],a[64:67],v[104:107]\n"
"s_min_u32 s14,s14,s15\n"
"s_add_u32 s14,s14,s68\n"
TR "a[204:205],v1 offset:20480\n"
MF "v[112:115],a[208:211],a[68:71],v[112:115]\n"
"s_add_u32 s84,s14,0\n"
"s_add_u32 m0,s63,20480\n"
TR "a[206:207],v1 offset:20992\n"
MF "v[112:115],a[212:215],a[108:111],v[112:115]\n"
LD "v4,s[24:27],s84 offen lds\n"
MF "v[120:123],a[212:215],a[112:115],v[120:123]\n"
MF "v[120:123],a[208:211],a[72:75],v[120:123]\n"
MF "v[128:131],a[208:211],a[76:79],v[128:131]\n"
"s_add_u32 s84,s14,9216\n"
MF "v[128:131],a[212:215],a[116:119],v[128:131]\n"
"s_waitcnt lgkmcnt(4)\n"
LD "v5,s[24:27],s84 offen offset:1024 lds\n"
MF "v[132:135],a[220:223],a[116:119],v[132:135]\n"
TR "a[208:209],v1 offset:16392\n"
MF "v[132:135],a[216:219],a[76:79],v[132:135]\n"
TR "a[210:211],v1 offset:16904\n"
MF "v[124:127],a[216:219],a[72:75],v[124:127]\n"
MF "v[124:127],a[220:223],a[112:115],v[124:127]\n"
TR "a[212:213],v1 offset:20488\n"
MF "v[116:119],a[220:223],a[108:111],v[116:119]\n"
TR "a[214:215],v1 offset:21000\n"
MF "v[116:119],a[216:219],a[68:71],v[116:119]\n"
MF "v[108:111],a[216:219],a[64:67],v[108:111]\n"
MF "v[108:111],a[220:223],a[104:107],v[108:111]\n"
MF "v[100:103],a[220:223],a[100:103],v[100:103]\n"
MF "v[100:103],a[216:219],a[60:63],v[100:103]\n"
"s_waitcnt vmcnt(23) lgkmcnt(0)\n"
"s_barrier\n"
MF "v[136:139],a[200:203],a[60:63],v[136:139]\n"
TR "a[216:217],v0 offset:32768\n"
MF "v[136:139],a[204:207],a[100:103],v[136:139]\n"
"s_add_u32 m0,s62,16384\n"
TR "a[218:219],v0 offset:33280\n"
MF "v[144:147],a[204:207],a[104:107],v[144:147]\n"
"s_add_u32 s82,s66,4718720\n"
"ds_read_b128 a[140:143],v2 offset:40960\n"
MF "v[144:147],a[200:203],a[64:67],v[144:147]\n"
"s_sub_u32 s15,s82,7864320\n"
TR "a[220:221],v0 offset:36864\n"
MF "v[152:155],a[200:203],a[68:71],v[152:155]\n"
"s_min_u32 s82,s82,s15\n"
TR "a[222:223],v0 offset:37376\n"
MF "v[152:155],a[204:207],a[108:111],v[152:155]\n"
LD "v6,s[28:31],s82 offen lds\n"
MF "v[160:163],a[204:207],a[112:115],v[160:163]\n"
"ds_read_b128 a[180:183],v3 offset:40960\n"
MF "v[160:163],a[200:203],a[72:75],v[160:163]\n"
LD "v11,s[28:31],s82 offen offset:1024 lds\n"
MF "v[168:171],a[200:203],a[76:79],v[168:171]\n"
MF "v[168:171],a[204:207],a[116:119],v[168:171]\n"
LD "v12,s[28:31],s82 offen offset:2048 lds\n"
MF "v[172:175],a[212:215],a[116:119],v[172:175]\n"
TR "a[200:201],v0 offset:32776\n"
MF "v[172:175],a[208:211],a[76:79],v[172:175]\n"
TR "a[202:203],v0 offset:33288\n"
MF "v[164:167],a[208:211],a[72:75],v[164:167]\n"
"ds_read_b128 a[144:147],v2 offset:43008\n"
MF "v[164:167],a[212:215],a[112:115],v[164:167]\n"
TR "a[204:205],v0 offset:36872\n"
MF "v[156:159],a[212:215],a[108:111],v[156:159]\n"
TR "a[206:207],v0 offset:37384\n"
MF "v[156:159],a[208:211],a[68:71],v[156:159]\n"
MF "v[148:151],a[208:211],a[64:67],v[148:151]\n"
"ds_read_b128 a[184:187],v3 offset:43008\n"
MF "v[148:151],a[212:215],a[104:107],v[148:151]\n"
MF "v[140:143],a[212:215],a[100:103],v[140:143]\n"
LD "v13,s[28:31],s82 offen offset:3072 lds\n"
MF "v[140:143],a[208:211],a[60:63],v[140:143]\n"
"s_waitcnt lgkmcnt(7)\n"
MF "v[176:179],a[216:219],a[60:63],v[176:179]\n"
TR "a[208:209],v1 offset:32768\n"
MF "v[176:179],a[220:223],a[100:103],v[176:179]\n"
TR "a[210:211],v1 offset:33280\n"
MF "v[184:187],a[220:223],a[104:107],v[184:187]\n"
"ds_read_b128 a[148:151],v2 offset:45056\n"
MF "v[184:187],a[216:219],a[64:67],v[184:187]\n"
"s_add_u32 s84,s14,20480\n"
TR "a[212:213],v1 offset:36864\n"
MF "v[192:195],a[216:219],a[68:71],v[192:195]\n"
"s_add_u32 m0,s63,22528\n"
TR "a[214:215],v1 offset:37376\n"
MF "v[192:195],a[220:223],a[108:111],v[192:195]\n"
LD "v4,s[24:27],s84 offen lds\n"
MF "v[200:203],a[220:223],a[112:115],v[200:203]\n"
"ds_read_b128 a[188:191],v3 offset:45056\n"
MF "v[200:203],a[216:219],a[72:75],v[200:203]\n"
MF "v[208:211],a[216:219],a[76:79],v[208:211]\n"
"s_add_u32 s84,s14,29696\n"
MF "v[208:211],a[220:223],a[116:119],v[208:211]\n"
"s_waitcnt lgkmcnt(7)\n"
LD "v5,s[24:27],s84 offen offset:1024 lds\n"
MF "v[212:215],a[204:207],a[116:119],v[212:215]\n"
TR "a[216:217],v1 offset:32776\n"
MF "v[212:215],a[200:203],a[76:79],v[212:215]\n"
TR "a[218:219],v1 offset:33288\n"
MF "v[204:207],a[200:203],a[72:75],v[204:207]\n"
"ds_read_b128 a[152:155],v2 offset:47104\n"
MF "v[204:207],a[204:207],a[112:115],v[204:207]\n"
TR "a[220:221],v1 offset:36872\n"
MF "v[196:199],a[204:207],a[108:111],v[196:199]\n"
TR "a[222:223],v1 offset:37384\n"
MF "v[196:199],a[200:203],a[68:71],v[196:199]\n"
MF "v[188:191],a[200:203],a[64:67],v[188:191]\n"
"v_add_u32 v0,0xc000,v0\n"
MF "v[188:191],a[204:207],a[104:107],v[188:191]\n"
"v_add_u32 v1,0xc000,v1\n"
MF "v[180:183],a[204:207],a[100:103],v[180:183]\n"
MF "v[180:183],a[200:203],a[60:63],v[180:183]\n"
"s_waitcnt vmcnt(25) lgkmcnt(0)\n"
"s_barrier\n"
MF "v[216:219],a[208:211],a[60:63],v[216:219]\n"
TR "a[200:201],v0\n"
MF "v[216:219],a[212:215],a[100:103],v[216:219]\n"
"s_add_u32 m0,s62,32768\n"
TR "a[202:203],v0 offset:512\n"
MF "v[224:227],a[212:215],a[104:107],v[224:227]\n"
"s_add_u32 s82,s66,4718848\n"
"ds_read_b128 a[192:195],v3 offset:47104\n"
MF "v[224:227],a[208:211],a[64:67],v[224:227]\n"
"s_sub_u32 s15,s82,7864320\n"
TR "a[204:205],v0 offset:4096\n"
MF "v[232:235],a[208:211],a[68:71],v[232:235]\n"
"s_min_u32 s82,s82,s15\n"
TR "a[206:207],v0 offset:4608\n"
MF "v[232:235],a[212:215],a[108:111],v[232:235]\n"
LD "v6,s[28:31],s82 offen lds\n"
MF "v[240:243],a[212:215],a[112:115],v[240:243]\n"
"ds_read_b128 a[156:159],v2 offset:49152\n"
MF "v[240:243],a[208:211],a[72:75],v[240:243]\n"
"ds_read_b128 a[196:199],v3 offset:49152\n"
MF "v[248:251],a[208:211],a[76:79],v[248:251]\n"
LD "v11,s[28:31],s82 offen offset:1024 lds\n"
MF "v[248:251],a[212:215],a[116:119],v[248:251]\n"
MF "v[252:255],a[220:223],a[116:119],v[252:255]\n"
TR "a[208:209],v0 offset:8\n"
MF "v[252:255],a[216:219],a[76:79],v[252:255]\n"
TR "a[210:211],v0 offset:520\n"
MF "v[244:247],a[216:219],a[72:75],v[244:247]\n"
MF "v[244:247],a[220:223],a[112:115],v[244:247]\n"
TR "a[212:213],v0 offset:4104\n"
MF "v[236:239],a[220:223],a[108:111],v[236:239]\n"
TR "a[214:215],v0 offset:4616\n"
MF "v[236:239],a[216:219],a[68:71],v[236:239]\n"
LD "v12,s[28:31],s82 offen offset:2048 lds\n"
MF "v[228:231],a[216:219],a[64:67],v[228:231]\n"
MF "v[228:231],a[220:223],a[104:107],v[228:231]\n"
MF "v[220:223],a[220:223],a[100:103],v[220:223]\n"
MF "v[220:223],a[216:219],a[60:63],v[220:223]\n"
"s_waitcnt lgkmcnt(4)\n"
MF "v[16:19],a[200:203],a[140:143],v[16:19]\n"
TR "a[216:217],v1\n"
MF "v[16:19],a[204:207],a[180:183],v[16:19]\n"
TR "a[218:219],v1 offset:512\n"
MF "v[24:27],a[204:207],a[184:187],v[24:27]\n"
LD "v13,s[28:31],s82 offen offset:3072 lds\n"
MF "v[24:27],a[200:203],a[144:147],v[24:27]\n"
TR "a[220:221],v1 offset:4096\n"
MF "v[32:35],a[200:203],a[148:151],v[32:35]\n"
TR "a[222:223],v1 offset:4608\n"
MF "v[32:35],a[204:207],a[188:191],v[32:35]\n"
MF "v[40:43],a[204:207],a[192:195],v[40:43]\n"
MF "v[40:43],a[200:203],a[152:155],v[40:43]\n"
"s_add_u32 s84,s14,40960\n"
MF "v[48:51],a[200:203],a[156:159],v[48:51]\n"
"s_add_u32 m0,s63,24576\n"
MF "v[48:51],a[204:207],a[196:199],v[48:51]\n"
"s_waitcnt lgkmcnt(4)\n"
LD "v4,s[24:27],s84 offen lds\n"
MF "v[52:55],a[212:215],a[196:199],v[52:55]\n"
TR "a[200:201],v1 offset:8\n"
MF "v[52:55],a[208:211],a[156:159],v[52:55]\n"
TR "a[202:203],v1 offset:520\n"
MF "v[44:47],a[208:211],a[152:155],v[44:47]\n"
MF "v[44:47],a[212:215],a[192:195],v[44:47]\n"
TR "a[204:205],v1 offset:4104\n"
MF "v[36:39],a[212:215],a[188:191],v[36:39]\n"
TR "a[206:207],v1 offset:4616\n"
MF "v[36:39],a[208:211],a[148:151],v[36:39]\n"
MF "v[28:31],a[208:211],a[144:147],v[28:31]\n"
MF "v[28:31],a[212:215],a[184:187],v[28:31]\n"
MF "v[20:23],a[212:215],a[180:183],v[20:23]\n"
MF "v[20:23],a[208:211],a[140:143],v[20:23]\n"
".Lpg6c:\n"
"s_cbranch_execz .Lpg1a\n"
".Lpg6f:\n"
"s_cbranch_execz .Lpg16f\n"
"s_waitcnt vmcnt(24) lgkmcnt(0)\n"
"s_barrier\n"
MF "v[56:59],a[216:219],a[140:143],v[56:59]\n"
"s_add_u32 m0,s62,49152\n"
"s_add_u32 s82,s66,5505024\n"
TR "a[208:209],v0 offset:16384\n"
MF "v[56:59],a[220:223],a[180:183],v[56:59]\n"
"s_sub_u32 s15,s82,7864320\n"
"s_min_u32 s82,s82,s15\n"
TR "a[210:211],v0 offset:16896\n"
MF "v[64:67],a[220:223],a[184:187],v[64:67]\n"
LD "v6,s[28:31],s82 offen lds\n"
MF "v[64:67],a[216:219],a[144:147],v[64:67]\n"
TR "a[212:213],v0 offset:20480\n"
MF "v[72:75],a[216:219],a[148:151],v[72:75]\n"
TR "a[214:215],v0 offset:20992\n"
MF "v[72:75],a[220:223],a[188:191],v[72:75]\n"
MF "v[80:83],a[220:223],a[192:195],v[80:83]\n"
LD "v11,s[28:31],s82 offen offset:1024 lds\n"
MF "v[80:83],a[216:219],a[152:155],v[80:83]\n"
MF "v[88:91],a[216:219],a[156:159],v[88:91]\n"
MF "v[88:91],a[220:223],a[196:199],v[88:91]\n"
MF "v[92:95],a[204:207],a[196:199],v[92:95]\n"
TR "a[216:217],v0 offset:16392\n"
MF "v[92:95],a[200:203],a[156:159],v[92:95]\n"
TR "a[218:219],v0 offset:16904\n"
MF "v[84:87],a[200:203],a[152:155],v[84:87]\n"
LD "v12,s[28:31],s82 offen offset:2048 lds\n"
MF "v[84:87],a[204:207],a[192:195],v[84:87]\n"
TR "a[220:221],v0 offset:20488\n"
MF "v[76:79],a[204:207],a[188:191],v[76:79]\n"
TR "a[222:223],v0 offset:21000\n"
MF "v[76:79],a[200:203],a[148:151],v[76:79]\n"
MF "v[68:71],a[200:203],a[144:147],v[68:71]\n"
MF "v[68:71],a[204:207],a[184:187],v[68:71]\n"
MF "v[60:63],a[204:207],a[180:183],v[60:63]\n"
LD "v13,s[28:31],s82 offen offset:3072 lds\n"
MF "v[60:63],a[200:203],a[140:143],v[60:63]\n"
"s_waitcnt lgkmcnt(4)\n"
MF "v[96:99],a[208:211],a[140:143],v[96:99]\n"
TR "a[200:201],v1 offset:16384\n"
MF "v[96:99],a[212:215],a[180:183],v[96:99]\n"
TR "a[202:203],v1 offset:16896\n"
MF "v[104:107],a[212:215],a[184:187],v[104:107]\n"
"s_add_u32 s14,s81,1024\n"
"s_sub_u32 s15,s14,1280\n"
MF "v[104:107],a[208:211],a[144:147],v[104:107]\n"
"s_min_u32 s14,s14,s15\n"
"s_add_u32 s14,s14,s68\n"
TR "a[204:205],v1 offset:20480\n"
MF "v[112:115],a[208:211],a[148:151],v[112:115]\n"
"s_add_u32 s84,s14,0\n"
"s_add_u32 m0,s63,40960\n"
TR "a[206:207],v1 offset:20992\n"
MF "v[112:115],a[212:215],a[188:191],v[112:115]\n"
LD "v4,s[24:27],s84 offen lds\n"
MF "v[120:123],a[212:215],a[192:195],v[120:123]\n"
MF "v[120:123],a[208:211],a[152:155],v[120:123]\n"
MF "v[128:131],a[208:211],a[156:159],v[128:131]\n"
"s_add_u32 s84,s14,9216\n"
MF "v[128:131],a[212:215],a[196:199],v[128:131]\n"
"s_waitcnt lgkmcnt(4)\n"
LD "v5,s[24:27],s84 offen offset:1024 lds\n"
MF "v[132:135],a[220:223],a[196:199],v[132:135]\n"
TR "a[208:209],v1 offset:16392\n"
MF "v[132:135],a[216:219],a[156:159],v[132:135]\n"
TR "a[210:211],v1 offset:16904\n"
MF "v[124:127],a[216:219],a[152:155],v[124:127]\n"
MF "v[124:127],a[220:223],a[192:195],v[124:127]\n"
TR "a[212:213],v1 offset:20488\n"
MF "v[116:119],a[220:223],a[188:191],v[116:119]\n"
TR "a[214:215],v1 offset:21000\n"
MF "v[116:119],a[216:219],a[148:151],v[116:119]\n"
MF "v[108:111],a[216:219],a[144:147],v[108:111]\n"
MF "v[108:111],a[220:223],a[184:187],v[108:111]\n"
MF "v[100:103],a[220:223],a[180:183],v[100:103]\n"
MF "v[100:103],a[216:219],a[140:143],v[100:103]\n"
"s_waitcnt vmcnt(23) lgkmcnt(0)\n"
"s_barrier\n"
"s_cmp_eq_u32 s57,1\n"
"s_cbranch_scc0 .Lhs12\n"
"v_bfe_u32 v14,v9,4,5\n"
"v_lshlrev_b32 v14,6,v14\n"
"s_lshl_b32 s58,s78,2\n"
"s_add_u32 s58,s58,s86\n"
"buffer_load_dword v10,v14,s[40:43],s58 offen offset:128 sc1\n"
".Lhs12:\n"
MF "v[136:139],a[200:203],a[140:143],v[136:139]\n"
TR "a[216:217],v0 offset:32768\n"
MF "v[136:139],a[204:207],a[180:183],v[136:139]\n"
"s_add_u32 m0,s62,65536\n"
TR "a[218:219],v0 offset:33280\n"
MF "v[144:147],a[204:207],a[184:187],v[144:147]\n"
"s_add_u32 s82,s66,5505152\n"
"ds_read_b128 a[60:63],v2\n"
MF "v[144:147],a[200:203],a[144:147],v[144:147]\n"
"s_sub_u32 s15,s82,7864320\n"
TR "a[220:221],v0 offset:36864\n"
MF "v[152:155],a[200:203],a[148:151],v[152:155]\n"
"s_min_u32 s82,s82,s15\n"
TR "a[222:223],v0 offset:37376\n"
MF "v[152:155],a[204:207],a[188:191],v[152:155]\n"
LD "v6,s[28:31],s82 offen lds\n"
MF "v[160:163],a[204:207],a[192:195],v[160:163]\n"
"ds_read_b128 a[100:103],v3\n"
MF "v[160:163],a[200:203],a[152:155],v[160:163]\n"
LD "v11,s[28:31],s82 offen offset:1024 lds\n"
MF "v[168:171],a[200:203],a[156:159],v[168:171]\n"
MF "v[168:171],a[204:207],a[196:199],v[168:171]\n"
LD "v12,s[28:31],s82 offen offset:2048 lds\n"
MF "v[172:175],a[212:215],a[196:199],v[172:175]\n"
TR "a[200:201],v0 offset:32776\n"
MF "v[172:175],a[208:211],a[156:159],v[172:175]\n"
TR "a[202:203],v0 offset:33288\n"
MF "v[164:167],a[208:211],a[152:155],v[164:167]\n"
"ds_read_b128 a[64:67],v2 offset:2048\n"
MF "v[164:167],a[212:215],a[192:195],v[164:167]\n"
TR "a[204:205],v0 offset:36872\n"
MF "v[156:159],a[212:215],a[188:191],v[156:159]\n"
TR "a[206:207],v0 offset:37384\n"
MF "v[156:159],a[208:211],a[148:151],v[156:159]\n"
MF "v[148:151],a[208:211],a[144:147],v[148:151]\n"
"ds_read_b128 a[104:107],v3 offset:2048\n"
MF "v[148:151],a[212:215],a[184:187],v[148:151]\n"
MF "v[140:143],a[212:215],a[180:183],v[140:143]\n"
LD "v13,s[28:31],s82 offen offset:3072 lds\n"
MF "v[140:143],a[208:211],a[140:143],v[140:143]\n"
"s_waitcnt lgkmcnt(7)\n"
MF "v[176:179],a[216:219],a[140:143],v[176:179]\n"
TR "a[208:209],v1 offset:32768\n"
MF "v[176:179],a[220:223],a[180:183],v[176:179]\n"
TR "a[210:211],v1 offset:33280\n"
MF "v[184:187],a[220:223],a[184:187],v[184:187]\n"
"ds_read_b128 a[68:71],v2 offset:4096\n"
MF "v[184:187],a[216:219],a[144:147],v[184:187]\n"
"s_add_u32 s84,s14,20480\n"
TR "a[212:213],v1 offset:36864\n"
MF "v[192:195],a[216:219],a[148:151],v[192:195]\n"
"s_add_u32 m0,s63,43008\n"
TR "a[214:215],v1 offset:37376\n"
MF "v[192:195],a[220:223],a[188:191],v[192:195]\n"
LD "v4,s[24:27],s84 offen lds\n"
MF "v[200:203],a[220:223],a[192:195],v[200:203]\n"
"ds_read_b128 a[108:111],v3 offset:4096\n"
MF "v[200:203],a[216:219],a[152:155],v[200:203]\n"
MF "v[208:211],a[216:219],a[156:159],v[208:211]\n"
"s_add_u32 s84,s14,29696\n"
MF "v[208:211],a[220:223],a[196:199],v[208:211]\n"
"s_waitcnt lgkmcnt(7)\n"
LD "v5,s[24:27],s84 offen offset:1024 lds\n"
MF "v[212:215],a[204:207],a[196:199],v[212:215]\n"
TR "a[216:217],v1 offset:32776\n"
MF "v[212:215],a[200:203],a[156:159],v[212:215]\n"
TR "a[218:219],v1 offset:33288\n"
MF "v[204:207],a[200:203],a[152:155],v[204:207]\n"
"ds_read_b128 a[72:75],v2 offset:6144\n"
MF "v[204:207],a[204:207],a[192:195],v[204:207]\n"
TR "a[220:221],v1 offset:36872\n"
MF "v[196:199],a[204:207],a[188:191],v[196:199]\n"
TR "a[222:223],v1 offset:37384\n"
MF "v[196:199],a[200:203],a[148:151],v[196:199]\n"
MF "v[188:191],a[200:203],a[144:147],v[188:191]\n"
"v_add_u32 v0,0xffff4000,v0\n"
MF "v[188:191],a[204:207],a[184:187],v[188:191]\n"
"v_add_u32 v1,0xffff4000,v1\n"
MF "v[180:183],a[204:207],a[180:183],v[180:183]\n"
MF "v[180:183],a[200:203],a[140:143],v[180:183]\n"
"s_waitcnt vmcnt(25) lgkmcnt(0)\n"
"s_barrier\n"
MF "v[216:219],a[208:211],a[140:143],v[216:219]\n"
TR "a[200:201],v0\n"
MF "v[216:219],a[212:215],a[180:183],v[216:219]\n"
"s_add_u32 m0,s62,81920\n"
TR "a[202:203],v0 offset:512\n"
MF "v[224:227],a[212:215],a[184:187],v[224:227]\n"
"s_add_u32 s82,s66,5505280\n"
"ds_read_b128 a[112:115],v3 offset:6144\n"
MF "v[224:227],a[208:211],a[144:147],v[224:227]\n"
"s_sub_u32 s15,s82,7864320\n"
TR "a[204:205],v0 offset:4096\n"
MF "v[232:235],a[208:211],a[148:151],v[232:235]\n"
"s_min_u32 s82,s82,s15\n"
TR "a[206:207],v0 offset:4608\n"
MF "v[232:235],a[212:215],a[188:191],v[232:235]\n"
LD "v6,s[28:31],s82 offen lds\n"
MF "v[240:243],a[212:215],a[192:195],v[240:243]\n"
"ds_read_b128 a[76:79],v2 offset:8192\n"
MF "v[240:243],a[208:211],a[152:155],v[240:243]\n"
"ds_read_b128 a[116:119],v3 offset:8192\n"
MF "v[248:251],a[208:211],a[156:159],v[248:251]\n"
LD "v11,s[28:31],s82 offen offset:1024 lds\n"
MF "v[248:251],a[212:215],a[196:199],v[248:251]\n"
MF "v[252:255],a[220:223],a[196:199],v[252:255]\n"
TR "a[208:209],v0 offset:8\n"
MF "v[252:255],a[216:219],a[156:159],v[252:255]\n"
TR "a[210:211],v0 offset:520\n"
MF "v[244:247],a[216:219],a[152:155],v[244:247]\n"
MF "v[244:247],a[220:223],a[192:195],v[244:247]\n"
TR "a[212:213],v0 offset:4104\n"
MF "v[236:239],a[220:223],a[188:191],v[236:239]\n"
TR "a[214:215],v0 offset:4616\n"
MF "v[236:239],a[216:219],a[148:151],v[236:239]\n"
LD "v12,s[28:31],s82 offen offset:2048 lds\n"
MF "v[228:231],a[216:219],a[144:147],v[228:231]\n"
MF "v[228:231],a[220:223],a[184:187],v[228:231]\n"
MF "v[220:223],a[220:223],a[180:183],v[220:223]\n"
MF "v[220:223],a[216:219],a[140:143],v[220:223]\n"
"s_waitcnt lgkmcnt(4)\n"
MF "v[16:19],a[200:203],a[60:63],v[16:19]\n"
TR "a[216:217],v1\n"
MF "v[16:19],a[204:207],a[100:103],v[16:19]\n"
TR "a[218:219],v1 offset:512\n"
MF "v[24:27],a[204:207],a[104:107],v[24:27]\n"
LD "v13,s[28:31],s82 offen offset:3072 lds\n"
MF "v[24:27],a[200:203],a[64:67],v[24:27]\n"
TR "a[220:221],v1 offset:4096\n"
MF "v[32:35],a[200:203],a[68:71],v[32:35]\n"
TR "a[222:223],v1 offset:4608\n"
MF "v[32:35],a[204:207],a[108:111],v[32:35]\n"
MF "v[40:43],a[204:207],a[112:115],v[40:43]\n"
MF "v[40:43],a[200:203],a[72:75],v[40:43]\n"
"s_add_u32 s84,s14,40960\n"
MF "v[48:51],a[200:203],a[76:79],v[48:51]\n"
"s_add_u32 m0,s63,45056\n"
MF "v[48:51],a[204:207],a[116:119],v[48:51]\n"
"s_waitcnt lgkmcnt(4)\n"
LD "v4,s[24:27],s84 offen lds\n"
MF "v[52:55],a[212:215],a[116:119],v[52:55]\n"
TR "a[200:201],v1 offset:8\n"
MF "v[52:55],a[208:211],a[76:79],v[52:55]\n"
TR "a[202:203],v1 offset:520\n"
MF "v[44:47],a[208:211],a[72:75],v[44:47]\n"
MF "v[44:47],a[212:215],a[112:115],v[44:47]\n"
TR "a[204:205],v1 offset:4104\n"
MF "v[36:39],a[212:215],a[108:111],v[36:39]\n"
TR "a[206:207],v1 offset:4616\n"
MF "v[36:39],a[208:211],a[68:71],v[36:39]\n"
MF "v[28:31],a[208:211],a[64:67],v[28:31]\n"
MF "v[28:31],a[212:215],a[104:107],v[28:31]\n"
MF "v[20:23],a[212:215],a[100:103],v[20:23]\n"
MF "v[20:23],a[208:211],a[60:63],v[20:23]\n"
"s_waitcnt vmcnt(24) lgkmcnt(0)\n"
"s_barrier\n"
MF "v[56:59],a[216:219],a[60:63],v[56:59]\n"
"s_add_u32 m0,s62,0\n"
"s_add_u32 s82,s66,6291712\n"
TR "a[208:209],v0 offset:16384\n"
MF "v[56:59],a[220:223],a[100:103],v[56:59]\n"
"s_sub_u32 s15,s82,7864320\n"
"s_min_u32 s82,s82,s15\n"
TR "a[210:211],v0 offset:16896\n"
MF "v[64:67],a[220:223],a[104:107],v[64:67]\n"
LD "v6,s[28:31],s82 offen lds\n"
MF "v[64:67],a[216:219],a[64:67],v[64:67]\n"
TR "a[212:213],v0 offset:20480\n"
MF "v[72:75],a[216:219],a[68:71],v[72:75]\n"
TR "a[214:215],v0 offset:20992\n"
MF "v[72:75],a[220:223],a[108:111],v[72:75]\n"
MF "v[80:83],a[220:223],a[112:115],v[80:83]\n"
LD "v11,s[28:31],s82 offen offset:1024 lds\n"
MF "v[80:83],a[216:219],a[72:75],v[80:83]\n"
MF "v[88:91],a[216:219],a[76:79],v[88:91]\n"
MF "v[88:91],a[220:223],a[116:119],v[88:91]\n"
MF "v[92:95],a[204:207],a[116:119],v[92:95]\n"
TR "a[216:217],v0 offset:16392\n"
MF "v[92:95],a[200:203],a[76:79],v[92:95]\n"
TR "a[218:219],v0 offset:16904\n"
MF "v[84:87],a[200:203],a[72:75],v[84:87]\n"
LD "v12,s[28:31],s82 offen offset:2048 lds\n"
MF "v[84:87],a[204:207],a[112:115],v[84:87]\n"
TR "a[220:221],v0 offset:20488\n"
MF "v[76:79],a[204:207],a[108:111],v[76:79]\n"
TR "a[222:223],v0 offset:21000\n"
MF "v[76:79],a[200:203],a[68:71],v[76:79]\n"
MF "v[68:71],a[200:203],a[64:67],v[68:71]\n"
LD "v13,s[28:31],s82 offen offset:3072 lds\n"
MF "v[68:71],a[204:207],a[104:107],v[68:71]\n"
MF "v[60:63],a[204:207],a[100:103],v[60:63]\n"
MF "v[60:63],a[200:203],a[60:63],v[60:63]\n"
"s_add_u32 s14,s81,1152\n"
"s_sub_u32 s15,s14,1280\n"
"s_waitcnt lgkmcnt(4)\n"
MF "v[96:99],a[208:211],a[60:63],v[96:99]\n"
"s_min_u32 s14,s14,s15\n"
"s_add_u32 s14,s14,s68\n"
TR "a[200:201],v1 offset:16384\n"
MF "v[96:99],a[212:215],a[100:103],v[96:99]\n"
"s_add_u32 s84,s14,0\n"
"s_add_u32 m0,s63,0\n"
TR "a[202:203],v1 offset:16896\n"
MF "v[104:107],a[212:215],a[104:107],v[104:107]\n"
LD "v4,s[24:27],s84 offen lds\n"
MF "v[104:107],a[208:211],a[64:67],v[104:107]\n"
TR "a[204:205],v1 offset:20480\n"
MF "v[112:115],a[208:211],a[68:71],v[112:115]\n"
TR "a[206:207],v1 offset:20992\n"
MF "v[112:115],a[212:215],a[108:111],v[112:115]\n"
"s_add_u32 s84,s14,9216\n"
MF "v[120:123],a[212:215],a[112:115],v[120:123]\n"
LD "v5,s[24:27],s84 offen offset:1024 lds\n"
MF "v[120:123],a[208:211],a[72:75],v[120:123]\n"
MF "v[128:131],a[208:211],a[76:79],v[128:131]\n"
MF "v[128:131],a[212:215],a[116:119],v[128:131]\n"
"s_waitcnt lgkmcnt(4)\n"
MF "v[132:135],a[220:223],a[116:119],v[132:135]\n"
TR "a[208:209],v1 offset:16392\n"
MF "v[132:135],a[216:219],a[76:79],v[132:135]\n"
"s_add_u32 s84,s14,18432\n"
TR "a[210:211],v1 offset:16904\n"
MF "v[124:127],a[216:219],a[72:75],v[124:127]\n"
LD "v4,s[24:27],s84 offen offset:2048 lds\n"
MF "v[124:127],a[220:223],a[112:115],v[124:127]\n"
TR "a[212:213],v1 offset:20488\n"
MF "v[116:119],a[220:223],a[108:111],v[116:119]\n"
TR "a[214:215],v1 offset:21000\n"
MF "v[116:119],a[216:219],a[68:71],v[116:119]\n"
MF "v[108:111],a[216:219],a[64:67],v[108:111]\n"
MF "v[108:111],a[220:223],a[104:107],v[108:111]\n"
MF "v[100:103],a[220:223],a[100:103],v[100:103]\n"
MF "v[100:103],a[216:219],a[60:63],v[100:103]\n"
"s_waitcnt vmcnt(24) lgkmcnt(0)\n"
"s_barrier\n"
MF "v[136:139],a[200:203],a[60:63],v[136:139]\n"
TR "a[216:217],v0 offset:32768\n"
MF "v[136:139],a[204:207],a[100:103],v[136:139]\n"
"s_add_u32 m0,s62,16384\n"
TR "a[218:219],v0 offset:33280\n"
MF "v[144:147],a[204:207],a[104:107],v[144:147]\n"
"s_add_u32 s82,s66,7078144\n"
"ds_read_b128 a[140:143],v2 offset:20480\n"
MF "v[144:147],a[200:203],a[64:67],v[144:147]\n"
"s_sub_u32 s15,s82,7864320\n"
TR "a[220:221],v0 offset:36864\n"
MF "v[152:155],a[200:203],a[68:71],v[152:155]\n"
"s_min_u32 s82,s82,s15\n"
TR "a[222:223],v0 offset:37376\n"
MF "v[152:155],a[204:207],a[108:111],v[152:155]\n"
LD "v6,s[28:31],s82 offen lds\n"
MF "v[160:163],a[204:207],a[112:115],v[160:163]\n"
"ds_read_b128 a[180:183],v3 offset:20480\n"
MF "v[160:163],a[200:203],a[72:75],v[160:163]\n"
LD "v11,s[28:31],s82 offen offset:1024 lds\n"
MF "v[168:171],a[200:203],a[76:79],v[168:171]\n"
MF "v[168:171],a[204:207],a[116:119],v[168:171]\n"
LD "v12,s[28:31],s82 offen offset:2048 lds\n"
MF "v[172:175],a[212:215],a[116:119],v[172:175]\n"
TR "a[200:201],v0 offset:32776\n"
MF "v[172:175],a[208:211],a[76:79],v[172:175]\n"
TR "a[202:203],v0 offset:33288\n"
MF "v[164:167],a[208:211],a[72:75],v[164:167]\n"
"ds_read_b128 a[144:147],v2 offset:22528\n"
MF "v[164:167],a[212:215],a[112:115],v[164:167]\n"
TR "a[204:205],v0 offset:36872\n"
MF "v[156:159],a[212:215],a[108:111],v[156:159]\n"
TR "a[206:207],v0 offset:37384\n"
MF "v[156:159],a[208:211],a[68:71],v[156:159]\n"
MF "v[148:151],a[208:211],a[64:67],v[148:151]\n"
"ds_read_b128 a[184:187],v3 offset:22528\n"
MF "v[148:151],a[212:215],a[104:107],v[148:151]\n"
MF "v[140:143],a[212:215],a[100:103],v[140:143]\n"
LD "v13,s[28:31],s82 offen offset:3072 lds\n"
MF "v[140:143],a[208:211],a[60:63],v[140:143]\n"
"s_waitcnt lgkmcnt(7)\n"
MF "v[176:179],a[216:219],a[60:63],v[176:179]\n"
TR "a[208:209],v1 offset:32768\n"
MF "v[176:179],a[220:223],a[100:103],v[176:179]\n"
TR "a[210:211],v1 offset:33280\n"
MF "v[184:187],a[220:223],a[104:107],v[184:187]\n"
"ds_read_b128 a[148:151],v2 offset:24576\n"
MF "v[184:187],a[216:219],a[64:67],v[184:187]\n"
"s_add_u32 s84,s14,30720\n"
TR "a[212:213],v1 offset:36864\n"
MF "v[192:195],a[216:219],a[68:71],v[192:195]\n"
"s_add_u32 m0,s63,3072\n"
TR "a[214:215],v1 offset:37376\n"
MF "v[192:195],a[220:223],a[108:111],v[192:195]\n"
LD "v5,s[24:27],s84 offen lds\n"
MF "v[200:203],a[220:223],a[112:115],v[200:203]\n"
"ds_read_b128 a[188:191],v3 offset:24576\n"
MF "v[200:203],a[216:219],a[72:75],v[200:203]\n"
MF "v[208:211],a[216:219],a[76:79],v[208:211]\n"
"s_add_u32 s84,s14,39936\n"
MF "v[208:211],a[220:223],a[116:119],v[208:211]\n"
"s_waitcnt lgkmcnt(7)\n"
LD "v4,s[24:27],s84 offen offset:1024 lds\n"
MF "v[212:215],a[204:207],a[116:119],v[212:215]\n"
TR "a[216:217],v1 offset:32776\n"
MF "v[212:215],a[200:203],a[76:79],v[212:215]\n"
TR "a[218:219],v1 offset:33288\n"
MF "v[204:207],a[200:203],a[72:75],v[204:207]\n"
"ds_read_b128 a[152:155],v2 offset:26624\n"
MF "v[204:207],a[204:207],a[112:115],v[204:207]\n"
TR "a[220:221],v1 offset:36872\n"
MF "v[196:199],a[204:207],a[108:111],v[196:199]\n"
TR "a[222:223],v1 offset:37384\n"
MF "v[196:199],a[200:203],a[68:71],v[196:199]\n"
MF "v[188:191],a[200:203],a[64:67],v[188:191]\n"
"v_add_u32 v0,0xc000,v0\n"
MF "v[188:191],a[204:207],a[104:107],v[188:191]\n"
"v_add_u32 v1,0xc000,v1\n"
MF "v[180:183],a[204:207],a[100:103],v[180:183]\n"
".Lpgqj_a:\n"
"s_cbranch_execz .Lisr1\n"
".Lqj:\n"
MF "v[180:183],a[200:203],a[60:63],v[180:183]\n"
"s_waitcnt vmcnt(26) lgkmcnt(0)\n"
"s_barrier\n"
"s_getpc_b64 s[52:53]\n"
".Lctq:\n"
"s_sub_u32 s52,s52,.Lctq-.Lcode0\n"
"s_subb_u32 s53,s53,0\n"
"s_bfe_u32 s58,s23,0x30000\n"
"s_lshl_b32 s58,s58,2\n"
"s_add_u32 s58,s58,s56\n"
"s_lshl_b32 s58,s58,13\n"
"v_lshlrev_b32 v15,3,v9\n"
"v_add_u32 v15,s58,v15\n"
"v_min_u32 v15,.Lcode1-.Lcode0,v15\n"
"global_load_dword a249,v15,s[52:53]\n"
MF "v[216:219],a[208:211],a[60:63],v[216:219]\n"
TR "a[200:201],v0\n"
MF "v[216:219],a[212:215],a[100:103],v[216:219]\n"
"s_add_u32 m0,s62,32768\n"
TR "a[202:203],v0 offset:512\n"
MF "v[224:227],a[212:215],a[104:107],v[224:227]\n"
"s_add_u32 s82,s66,6291584\n"
"ds_read_b128 a[192:195],v3 offset:26624\n"
MF "v[224:227],a[208:211],a[64:67],v[224:227]\n"
"s_sub_u32 s15,s82,7864320\n"
TR "a[204:205],v0 offset:4096\n"
MF "v[232:235],a[208:211],a[68:71],v[232:235]\n"
"s_min_u32 s82,s82,s15\n"
TR "a[206:207],v0 offset:4608\n"
MF "v[232:235],a[212:215],a[108:111],v[232:235]\n"
LD "v6,s[28:31],s82 offen lds\n"
MF "v[240:243],a[212:215],a[112:115],v[240:243]\n"
"ds_read_b128 a[156:159],v2 offset:28672\n"
MF "v[240:243],a[208:211],a[72:75],v[240:243]\n"
"ds_read_b128 a[196:199],v3 offset:28672\n"
MF "v[248:251],a[208:211],a[76:79],v[248:251]\n"
MF "v[248:251],a[212:215],a[116:119],v[248:251]\n"
LD "v11,s[28:31],s82 offen offset:1024 lds\n"
MF "v[252:255],a[220:223],a[116:119],v[252:255]\n"
TR "a[208:209],v0 offset:8\n"
MF "v[252:255],a[216:219],a[76:79],v[252:255]\n"
TR "a[210:211],v0 offset:520\n"
MF "v[244:247],a[216:219],a[72:75],v[244:247]\n"
MF "v[244:247],a[220:223],a[112:115],v[244:247]\n"
TR "a[212:213],v0 offset:4104\n"
MF "v[236:239],a[220:223],a[108:111],v[236:239]\n"
TR "a[214:215],v0 offset:4616\n"
MF "v[236:239],a[216:219],a[68:71],v[236:239]\n"
MF "v[228:231],a[216:219],a[64:67],v[228:231]\n"
MF "v[228:231],a[220:223],a[104:107],v[228:231]\n"
MF "v[220:223],a[220:223],a[100:103],v[220:223]\n"
LD "v12,s[28:31],s82 offen offset:2048 lds\n"
MF "v[220:223],a[216:219],a[60:63],v[220:223]\n"
"s_waitcnt lgkmcnt(4)\n"
MF "v[16:19],a[200:203],a[140:143],v[16:19]\n"
TR "a[216:217],v1\n"
MF "v[16:19],a[204:207],a[180:183],v[16:19]\n"
TR "a[218:219],v1 offset:512\n"
MF "v[24:27],a[204:207],a[184:187],v[24:27]\n"
MF "v[24:27],a[200:203],a[144:147],v[24:27]\n"
TR "a[220:221],v1 offset:4096\n"
MF "v[32:35],a[200:203],a[148:151],v[32:35]\n"
TR "a[222:223],v1 offset:4608\n"
MF "v[32:35],a[204:207],a[188:191],v[32:35]\n"
MF "v[40:43],a[204:207],a[192:195],v[40:43]\n"
MF "v[40:43],a[200:203],a[152:155],v[40:43]\n"
LD "v13,s[28:31],s82 offen offset:3072 lds\n"
MF "v[48:51],a[200:203],a[156:159],v[48:51]\n"
"s_memtime s[52:53]\n"
"s_memrealtime s[100:101]\n"
MF "v[48:51],a[204:207],a[196:199],v[48:51]\n"
"s_waitcnt lgkmcnt(4)\n"
MF "v[52:55],a[212:215],a[196:199],v[52:55]\n"
TR "a[200:201],v1 offset:8\n"
MF "v[52:55],a[208:211],a[156:159],v[52:55]\n"
TR "a[202:203],v1 offset:520\n"
MF "v[44:47],a[208:211],a[152:155],v[44:47]\n"
MF "v[44:47],a[212:215],a[192:195],v[44:47]\n"
TR "a[204:205],v1 offset:4104\n"
MF "v[36:39],a[212:215],a[188:191],v[36:39]\n"
TR "a[206:207],v1 offset:4616\n"
MF "v[36:39],a[208:211],a[148:151],v[36:39]\n"
MF "v[28:31],a[208:211],a[144:147],v[28:31]\n"
MF "v[28:31],a[212:215],a[184:187],v[28:31]\n"
MF "v[20:23],a[212:215],a[180:183],v[20:23]\n"
MF "v[20:23],a[208:211],a[140:143],v[20:23]\n"
"s_waitcnt vmcnt(25) lgkmcnt(0)\n"
"s_barrier\n"
MF "v[56:59],a[216:219],a[140:143],v[56:59]\n"
"s_add_u32 m0,s62,49152\n"
"s_add_u32 s82,s66,7078016\n"
TR "a[208:209],v0 offset:16384\n"
MF "v[56:59],a[220:223],a[180:183],v[56:59]\n"
"s_sub_u32 s15,s82,7864320\n"
"s_min_u32 s82,s82,s15\n"
TR "a[210:211],v0 offset:16896\n"
MF "v[64:67],a[220:223],a[184:187],v[64:67]\n"
LD "v6,s[28:31],s82 offen lds\n"
MF "v[64:67],a[216:219],a[144:147],v[64:67]\n"
"s_sub_u32 s53,s52,s80\n"
TR "a[212:213],v0 offset:20480\n"
"v_lshrrev_b32 v15,8,v9\n"
MF "v[72:75],a[216:219],a[148:151],v[72:75]\n"
TR "a[214:215],v0 offset:20992\n"
"v_mul_u32_u24 v15,48960,v15\n"
MF "v[72:75],a[220:223],a[188:191],v[72:75]\n"
"s_lshr_b32 s7,s56,1\n"
"v_add_u32 v15,v15,v9\n"
MF "v[80:83],a[220:223],a[192:195],v[80:83]\n"
"s_and_b32 s8,s56,1\n"
MF "v[80:83],a[216:219],a[152:155],v[80:83]\n"
"s_sub_u32 s7,s7,s8\n"
MF "v[88:91],a[216:219],a[156:159],v[88:91]\n"
"s_mul_i32 s7,s7,245760\n"
MF "v[88:91],a[220:223],a[196:199],v[88:91]\n"
LD "v11,s[28:31],s82 offen offset:1024 lds\n"
MF "v[92:95],a[204:207],a[196:199],v[92:95]\n"
"s_add_u32 s7,s7,s89\n"
TR "a[216:217],v0 offset:16392\n"
MF "v[92:95],a[200:203],a[156:159],v[92:95]\n"
"s_cmp_gt_u32 s53,24100\n"
"s_cselect_b32 s101,1,0\n"
TR "a[218:219],v0 offset:16904\n"
MF "v[84:87],a[200:203],a[152:155],v[84:87]\n"
"s_cmp_eq_u32 s87,2\n"
"s_cselect_b32 s87,s101,s87\n"
MF "v[84:87],a[204:207],a[192:195],v[84:87]\n"
TR "a[220:221],v0 offset:20488\n"
MF "v[76:79],a[204:207],a[188:191],v[76:79]\n"
TR "a[222:223],v0 offset:21000\n"
MF "v[76:79],a[200:203],a[148:151],v[76:79]\n"
"s_cmp_gt_u32 s87,s57\n"
"s_cselect_b32 s97,130,0\n"
"s_cselect_b32 s95,0,0x47800000\n"
MF "v[68:71],a[200:203],a[144:147],v[68:71]\n"
"s_mov_b32 s21,s100\n"
MF "v[68:71],a[204:207],a[184:187],v[68:71]\n"
"s_mov_b32 s77,s95\n"
MF "v[60:63],a[204:207],a[180:183],v[60:63]\n"
LD "v12,s[28:31],s82 offen offset:2048 lds\n"
MF "v[60:63],a[200:203],a[140:143],v[60:63]\n"
"s_waitcnt lgkmcnt(4)\n"
MF "v[96:99],a[208:211],a[140:143],v[96:99]\n"
TR "a[200:201],v1 offset:16384\n"
MF "v[96:99],a[212:215],a[180:183],v[96:99]\n"
".Lpg7a:\n"
"s_cbranch_execz .Lisr1\n"
TR "a[202:203],v1 offset:16896\n"
MF "v[104:107],a[212:215],a[184:187],v[104:107]\n"
MF "v[104:107],a[208:211],a[144:147],v[104:107]\n"
TR "a[204:205],v1 offset:20480\n"
MF "v[112:115],a[208:211],a[148:151],v[112:115]\n"
TR "a[206:207],v1 offset:20992\n"
MF "v[112:115],a[212:215],a[188:191],v[112:115]\n"
MF "v[120:123],a[212:215],a[192:195],v[120:123]\n"
MF "v[120:123],a[208:211],a[152:155],v[120:123]\n"
LD "v13,s[28:31],s82 offen offset:3072 lds\n"
MF "v[128:131],a[208:211],a[156:159],v[128:131]\n"
MF "v[128:131],a[212:215],a[196:199],v[128:131]\n"
"s_waitcnt lgkmcnt(4)\n"
MF "v[132:135],a[220:223],a[196:199],v[132:135]\n"
TR "a[208:209],v1 offset:16392\n"
MF "v[132:135],a[216:219],a[156:159],v[132:135]\n"
TR "a[210:211],v1 offset:16904\n"
MF "v[124:127],a[216:219],a[152:155],v[124:127]\n"
MF "v[124:127],a[220:223],a[192:195],v[124:127]\n"
TR "a[212:213],v1 offset:20488\n"
MF "v[116:119],a[220:223],a[188:191],v[116:119]\n"
TR "a[214:215],v1 offset:21000\n"
MF "v[116:119],a[216:219],a[148:151],v[116:119]\n"
MF "v[108:111],a[216:219],a[144:147],v[108:111]\n"
MF "v[108:111],a[220:223],a[184:187],v[108:111]\n"
MF "v[100:103],a[220:223],a[180:183],v[100:103]\n"
MF "v[100:103],a[216:219],a[140:143],v[100:103]\n"
"s_waitcnt vmcnt(22) lgkmcnt(0)\n"
"s_barrier\n"
"s_cmp_eq_u32 s57,1\n"
"s_cbranch_scc0 .Lhs15\n"
".Lack_q:\n"
"v_cmp_ne_u32 vcc,s76,v10\n"
"s_cbranch_vccnz .Lackspin_q\n"
".Lackok_q:\n"
".Lhs15:\n"
MF "v[136:139],a[200:203],a[140:143],v[136:139]\n"
"s_and_b32 s58,s56,1\n"
TR "a[216:217],v0 offset:32768\n"
MF "v[136:139],a[204:207],a[180:183],v[136:139]\n"
"s_add_u32 m0,s62,65536\n"
TR "a[218:219],v0 offset:33280\n"
MF "v[144:147],a[204:207],a[184:187],v[144:147]\n"
"s_add_u32 s82,s66,6291456\n"
"ds_read_b128 a[60:63],v2 offset:40960\n"
MF "v[144:147],a[200:203],a[144:147],v[144:147]\n"
"s_sub_u32 s15,s82,7864320\n"
TR "a[220:221],v0 offset:36864\n"
MF "v[152:155],a[200:203],a[148:151],v[152:155]\n"
"s_min_u32 s82,s82,s15\n"
TR "a[222:223],v0 offset:37376\n"
MF "v[152:155],a[204:207],a[188:191],v[152:155]\n"
LD "v6,s[28:31],s82 offen lds\n"
MF "v[160:163],a[204:207],a[192:195],v[160:163]\n"
"s_mul_i32 s59,s58,3072\n"
"ds_read_b128 a[100:103],v3 offset:40960\n"
MF "v[160:163],a[200:203],a[152:155],v[160:163]\n"
"s_add_u32 s16,s70,s59\n"
MF "v[168:171],a[200:203],a[156:159],v[168:171]\n"
"s_mul_i32 s59,s58,6144\n"
MF "v[168:171],a[204:207],a[196:199],v[168:171]\n"
LD "v11,s[28:31],s82 offen offset:1024 lds\n"
MF "v[172:175],a[212:215],a[196:199],v[172:175]\n"
"s_add_u32 s18,s70,s59\n"
TR "a[200:201],v0 offset:32776\n"
MF "v[172:175],a[208:211],a[156:159],v[172:175]\n"
"s_add_u32 s17,s16,196608\n"
TR "a[202:203],v0 offset:33288\n"
MF "v[164:167],a[208:211],a[152:155],v[164:167]\n"
"s_add_u32 s19,s18,196608\n"
"ds_read_b128 a[64:67],v2 offset:43008\n"
MF "v[164:167],a[212:215],a[192:195],v[164:167]\n"
"s_add_u32 s20,s70,196608\n"
TR "a[204:205],v0 offset:36872\n"
MF "v[156:159],a[212:215],a[188:191],v[156:159]\n"
"s_bfe_u32 s58,s23,0x40008\n"
TR "a[206:207],v0 offset:37384\n"
MF "v[156:159],a[208:211],a[148:151],v[156:159]\n"
"s_lshr_b32 s59,s56,1\n"
MF "v[148:151],a[208:211],a[144:147],v[148:151]\n"
"s_mul_i32 s59,s59,5\n"
"ds_read_b128 a[104:107],v3 offset:43008\n"
MF "v[148:151],a[212:215],a[184:187],v[148:151]\n"
"s_add_u32 s58,s58,s59\n"
MF "v[140:143],a[212:215],a[180:183],v[140:143]\n"
LD "v12,s[28:31],s82 offen offset:2048 lds\n"
MF "v[140:143],a[208:211],a[140:143],v[140:143]\n"
"s_waitcnt lgkmcnt(7)\n"
MF "v[176:179],a[216:219],a[140:143],v[176:179]\n"
"s_and_b32 s58,s58,15\n"
TR "a[208:209],v1 offset:32768\n"
MF "v[176:179],a[220:223],a[180:183],v[176:179]\n"
"s_mul_i32 s58,s58,12288\n"
TR "a[210:211],v1 offset:33280\n"
MF "v[184:187],a[220:223],a[184:187],v[184:187]\n"
"s_add_u32 s72,s16,s58\n"
"ds_read_b128 a[68:71],v2 offset:45056\n"
MF "v[184:187],a[216:219],a[144:147],v[184:187]\n"
"s_add_u32 s73,s18,s58\n"
TR "a[212:213],v1 offset:36864\n"
MF "v[192:195],a[216:219],a[148:151],v[192:195]\n"
"s_add_u32 s74,s70,s58\n"
TR "a[214:215],v1 offset:37376\n"
MF "v[192:195],a[220:223],a[188:191],v[192:195]\n"
"s_add_u32 s74,s74,12288\n"
MF "v[200:203],a[220:223],a[192:195],v[200:203]\n"
"s_cmp_eq_u32 s74,s20\n"
"s_cselect_b32 s74,s70,s74\n"
"ds_read_b128 a[108:111],v3 offset:45056\n"
MF "v[200:203],a[216:219],a[152:155],v[200:203]\n"
LD "v13,s[28:31],s82 offen offset:3072 lds\n"
MF "v[208:211],a[216:219],a[156:159],v[208:211]\n"
"s_lshr_b32 s58,s56,1\n"
MF "v[208:211],a[220:223],a[196:199],v[208:211]\n"
"s_waitcnt lgkmcnt(7)\n"
MF "v[212:215],a[204:207],a[196:199],v[212:215]\n"
"s_mul_i32 s58,s58,6144\n"
TR "a[216:217],v1 offset:32776\n"
MF "v[212:215],a[200:203],a[156:159],v[212:215]\n"
"s_mul_i32 s64,s56,3072\n"
TR "a[218:219],v1 offset:33288\n"
MF "v[204:207],a[200:203],a[152:155],v[204:207]\n"
"s_add_u32 s64,s64,s58\n"
"ds_read_b128 a[72:75],v2 offset:47104\n"
MF "v[204:207],a[204:207],a[192:195],v[204:207]\n"
"s_mul_i32 s65,s56,6144\n"
TR "a[220:221],v1 offset:36872\n"
MF "v[196:199],a[204:207],a[188:191],v[196:199]\n"
"s_sub_u32 s58,s71,s70\n"
TR "a[222:223],v1 offset:37384\n"
MF "v[196:199],a[200:203],a[148:151],v[196:199]\n"
"s_add_u32 s96,s74,s58\n"
MF "v[188:191],a[200:203],a[144:147],v[188:191]\n"
"s_sub_u32 s58,s16,s70\n"
"v_add_u32 v0,0xffff4000,v0\n"
MF "v[188:191],a[204:207],a[184:187],v[188:191]\n"
"s_add_u32 s96,s96,s58\n"
"v_add_u32 v1,0xffff4000,v1\n"
MF "v[180:183],a[204:207],a[180:183],v[180:183]\n"
"s_max_u32 s60,s70,s71\n"
MF "v[180:183],a[200:203],a[140:143],v[180:183]\n"
"s_waitcnt vmcnt(13) lgkmcnt(0)\n"
"s_barrier\n"
MF "v[216:219],a[208:211],a[140:143],v[216:219]\n"
"s_add_u32 s34,s60,196608\n"
TR "a[200:201],v1 offset:8\n"
MF "v[216:219],a[212:215],a[180:183],v[216:219]\n"
"s_add_u32 m0,s62,81920\n"
TR "a[202:203],v1 offset:520\n"
"v_mov_b32 v14,s12\n"
MF "v[224:227],a[212:215],a[184:187],v[224:227]\n"
"s_add_u32 s82,s66,7077888\n"
"ds_read_b128 a[112:115],v3 offset:47104\n"
"v_mul_f32 v14,0xbdd53b95,v14\n"
MF "v[224:227],a[208:211],a[144:147],v[224:227]\n"
"s_sub_u32 s15,s82,7864320\n"
TR "a[204:205],v1 offset:4104\n"
MF "v[232:235],a[208:211],a[148:151],v[232:235]\n"
"s_min_u32 s82,s82,s15\n"
TR "a[206:207],v1 offset:4616\n"
MF "v[232:235],a[212:215],a[188:191],v[232:235]\n"
LD "v6,s[28:31],s82 offen lds\n"
MF "v[240:243],a[212:215],a[192:195],v[240:243]\n"
"s_add_u32 s38,s60,196608\n"
"ds_read_b128 a[76:79],v2 offset:49152\n"
MF "v[240:243],a[208:211],a[152:155],v[240:243]\n"
"ds_read_b128 a[116:119],v3 offset:49152\n"
MF "v[248:251],a[208:211],a[156:159],v[248:251]\n"
MF "v[248:251],a[212:215],a[196:199],v[248:251]\n"
LD "v11,s[28:31],s82 offen offset:1024 lds\n"
MF "v[220:223],a[216:219],a[140:143],v[220:223]\n"
TR "a[208:209],v1 offset:16392\n"
MF "v[220:223],a[220:223],a[180:183],v[220:223]\n"
TR "a[210:211],v1 offset:16904\n"
MF "v[228:231],a[220:223],a[184:187],v[228:231]\n"
"ds_read_b128 a[140:143],v2\n"
MF "v[228:231],a[216:219],a[144:147],v[228:231]\n"
TR "a[212:213],v1 offset:20488\n"
MF "v[236:239],a[216:219],a[148:151],v[236:239]\n"
TR "a[214:215],v1 offset:21000\n"
MF "v[236:239],a[220:223],a[188:191],v[236:239]\n"
"ds_read_b128 a[180:183],v3\n"
MF "v[244:247],a[220:223],a[192:195],v[244:247]\n"
"ds_read_b128 a[144:147],v2 offset:2048\n"
MF "v[244:247],a[216:219],a[152:155],v[244:247]\n"
"ds_read_b128 a[184:187],v3 offset:2048\n"
MF "v[252:255],a[216:219],a[156:159],v[252:255]\n"
"ds_read_b128 a[148:151],v2 offset:4096\n"
MF "v[252:255],a[220:223],a[196:199],v[252:255]\n"
"s_waitcnt lgkmcnt(9)\n"
"ds_read_b128 a[188:191],v3 offset:4096\n"
MF "v[220:223],a[200:203],a[60:63],v[220:223]\n"
TR "a[216:217],v1\n"
MF "v[220:223],a[204:207],a[100:103],v[220:223]\n"
TR "a[218:219],v1 offset:512\n"
MF "v[228:231],a[204:207],a[104:107],v[228:231]\n"
"ds_read_b128 a[152:155],v2 offset:6144\n"
MF "v[228:231],a[200:203],a[64:67],v[228:231]\n"
TR "a[220:221],v1 offset:4096\n"
MF "v[236:239],a[200:203],a[68:71],v[236:239]\n"
TR "a[222:223],v1 offset:4608\n"
MF "v[236:239],a[204:207],a[108:111],v[236:239]\n"
"ds_read_b128 a[192:195],v3 offset:6144\n"
MF "v[244:247],a[204:207],a[112:115],v[244:247]\n"
"ds_read_b128 a[156:159],v2 offset:8192\n"
MF "v[244:247],a[200:203],a[72:75],v[244:247]\n"
"ds_read_b128 a[196:199],v3 offset:8192\n"
MF "v[252:255],a[200:203],a[76:79],v[252:255]\n"
LD "v12,s[28:31],s82 offen offset:2048 lds\n"
MF "v[252:255],a[204:207],a[116:119],v[252:255]\n"
"s_waitcnt lgkmcnt(8)\n"
LD "v13,s[28:31],s82 offen offset:3072 lds\n"
MF "v[220:223],a[208:211],a[140:143],v[220:223]\n"
TR "a[200:201],v1 offset:16384\n"
MF "v[220:223],a[212:215],a[180:183],v[220:223]\n"
TR "a[202:203],v1 offset:16896\n"
MF "v[228:231],a[212:215],a[184:187],v[228:231]\n"
MF "v[228:231],a[208:211],a[144:147],v[228:231]\n"
TR "a[204:205],v1 offset:20480\n"
MF "v[236:239],a[208:211],a[148:151],v[236:239]\n"
TR "a[206:207],v1 offset:20992\n"
MF "v[236:239],a[212:215],a[188:191],v[236:239]\n"
"s_waitcnt lgkmcnt(4)\n"
MF "v[244:247],a[212:215],a[192:195],v[244:247]\n"
MF "v[244:247],a[208:211],a[152:155],v[244:247]\n"
MF "v[252:255],a[208:211],a[156:159],v[252:255]\n"
MF "v[252:255],a[212:215],a[196:199],v[252:255]\n"
"s_waitcnt lgkmcnt(0)\n"
"s_cmp_eq_u32 s69,98\n"
"s_cbranch_scc1 .Lqy\n"
".Lqf:\n"
MF "v[216:219],a[216:219],a[60:63],v[216:219]\n"
TR "a[208:209],v0 offset:8\n"
MF "v[216:219],a[220:223],a[100:103],v[216:219]\n"
TR "a[210:211],v0 offset:520\n"
MF "v[224:227],a[220:223],a[104:107],v[224:227]\n"
"buffer_load_dword a224,v15,s[36:39],s70 offen offset:2688 nt\n"
MF "v[224:227],a[216:219],a[64:67],v[224:227]\n"
TR "a[212:213],v0 offset:4104\n"
MF "v[232:235],a[216:219],a[68:71],v[232:235]\n"
TR "a[214:215],v0 offset:4616\n"
MF "v[232:235],a[220:223],a[108:111],v[232:235]\n"
"buffer_load_dword a225,v15,s[36:39],s71 offen offset:2688 nt\n"
MF "v[240:243],a[220:223],a[112:115],v[240:243]\n"
MF "v[240:243],a[216:219],a[72:75],v[240:243]\n"
"buffer_load_dword a226,v15,s[48:51],s7 offen offset:2688\n"
MF "v[248:251],a[216:219],a[76:79],v[248:251]\n"
MF "v[248:251],a[220:223],a[116:119],v[248:251]\n"
MF "v[248:251],a[200:203],a[156:159],v[248:251]\n"
TR "a[216:217],v0 offset:16392\n"
MF "v[248:251],a[204:207],a[196:199],v[248:251]\n"
TR "a[218:219],v0 offset:16904\n"
MF "v[240:243],a[204:207],a[192:195],v[240:243]\n"
MF "v[240:243],a[200:203],a[152:155],v[240:243]\n"
TR "a[220:221],v0 offset:20488\n"
MF "v[232:235],a[200:203],a[148:151],v[232:235]\n"
TR "a[222:223],v0 offset:21000\n"
MF "v[232:235],a[204:207],a[188:191],v[232:235]\n"
MF "v[224:227],a[204:207],a[184:187],v[224:227]\n"
MF "v[224:227],a[200:203],a[144:147],v[224:227]\n"
"v_fma_f32 v248,v248,s54,v14\n"
"v_fma_f32 v249,v249,s54,v14\n"
MF "v[216:219],a[200:203],a[140:143],v[216:219]\n"
"v_fma_f32 v250,v250,s54,v14\n"
"v_fma_f32 v251,v251,s54,v14\n"
MF "v[216:219],a[204:207],a[180:183],v[216:219]\n"
"s_waitcnt lgkmcnt(4)\n"
"v_fma_f32 v252,v252,s54,v14\n"
"v_fma_f32 v253,v253,s54,v14\n"
MF "v[180:183],a[208:211],a[60:63],v[180:183]\n"
TR "a[200:201],v0\n"
"v_fma_f32 v254,v254,s54,v14\n"
MF "v[180:183],a[212:215],a[100:103],v[180:183]\n"
TR "a[202:203],v0 offset:512\n"
"v_fma_f32 v255,v255,s54,v14\n"
MF "v[188:191],a[212:215],a[104:107],v[188:191]\n"
CV "v255,v254,v255\n"
CV "v254,v252,v253\n"
MF "v[188:191],a[208:211],a[64:67],v[188:191]\n"
TR "a[204:205],v0 offset:4096\n"
CV "v253,v250,v251\n"
MF "v[196:199],a[208:211],a[68:71],v[196:199]\n"
TR "a[206:207],v0 offset:4608\n"
CV "v252,v248,v249\n"
MF "v[196:199],a[212:215],a[108:111],v[196:199]\n"
"v_fma_f32 v240,v240,s54,v14\n"
"v_fma_f32 v241,v241,s54,v14\n"
MF "v[204:207],a[212:215],a[112:115],v[204:207]\n"
"v_fma_f32 v242,v242,s54,v14\n"
"v_fma_f32 v243,v243,s54,v14\n"
MF "v[204:207],a[208:211],a[72:75],v[204:207]\n"
"v_fma_f32 v244,v244,s54,v14\n"
"v_fma_f32 v245,v245,s54,v14\n"
MF "v[212:215],a[208:211],a[76:79],v[212:215]\n"
"v_fma_f32 v246,v246,s54,v14\n"
"v_fma_f32 v247,v247,s54,v14\n"
MF "v[212:215],a[212:215],a[116:119],v[212:215]\n"
"s_waitcnt lgkmcnt(4)\n"
CV "v251,v246,v247\n"
CV "v250,v244,v245\n"
MF "v[180:183],a[216:219],a[140:143],v[180:183]\n"
TR "a[208:209],v0 offset:16384\n"
CV "v249,v242,v243\n"
MF "v[180:183],a[220:223],a[180:183],v[180:183]\n"
TR "a[210:211],v0 offset:16896\n"
CV "v248,v240,v241\n"
MF "v[188:191],a[220:223],a[184:187],v[188:191]\n"
"v_fma_f32 v232,v232,s54,v14\n"
"v_fma_f32 v233,v233,s54,v14\n"
MF "v[188:191],a[216:219],a[144:147],v[188:191]\n"
TR "a[212:213],v0 offset:20480\n"
"v_fma_f32 v234,v234,s54,v14\n"
MF "v[196:199],a[216:219],a[148:151],v[196:199]\n"
TR "a[214:215],v0 offset:20992\n"
"v_fma_f32 v235,v235,s54,v14\n"
MF "v[196:199],a[220:223],a[188:191],v[196:199]\n"
"v_fma_f32 v236,v236,s54,v14\n"
"v_fma_f32 v237,v237,s54,v14\n"
MF "v[204:207],a[220:223],a[192:195],v[204:207]\n"
"v_fma_f32 v238,v238,s54,v14\n"
"v_fma_f32 v239,v239,s54,v14\n"
MF "v[204:207],a[216:219],a[152:155],v[204:207]\n"
CV "v247,v238,v239\n"
CV "v246,v236,v237\n"
MF "v[212:215],a[216:219],a[156:159],v[212:215]\n"
CV "v245,v234,v235\n"
CV "v244,v232,v233\n"
MF "v[212:215],a[220:223],a[196:199],v[212:215]\n"
"s_waitcnt vmcnt(11) lgkmcnt(0)\n"
"s_barrier\n"
MF "v[176:179],a[200:203],a[60:63],v[176:179]\n"
TR "a[216:217],v1 offset:32776\n"
"v_fma_f32 v224,v224,s54,v14\n"
MF "v[176:179],a[204:207],a[100:103],v[176:179]\n"
"s_add_u32 m0,s64,0\n"
TR "a[218:219],v1 offset:33288\n"
"v_fma_f32 v225,v225,s54,v14\n"
MF "v[184:187],a[204:207],a[104:107],v[184:187]\n"
LD "v7,s[32:35],s72 offen nt lds\n"
MF "v[184:187],a[200:203],a[64:67],v[184:187]\n"
TR "a[220:221],v1 offset:36872\n"
"v_fma_f32 v226,v226,s54,v14\n"
MF "v[192:195],a[200:203],a[68:71],v[192:195]\n"
TR "a[222:223],v1 offset:37384\n"
"v_fma_f32 v227,v227,s54,v14\n"
MF "v[192:195],a[204:207],a[108:111],v[192:195]\n"
"s_add_u32 m0,s64,896\n"
"v_fma_f32 v228,v228,s54,v14\n"
"v_fma_f32 v229,v229,s54,v14\n"
MF "v[200:203],a[204:207],a[112:115],v[200:203]\n"
LD "v7,s[32:35],s72 offen offset:128 nt lds\n"
MF "v[200:203],a[200:203],a[72:75],v[200:203]\n"
"v_fma_f32 v230,v230,s54,v14\n"
"v_fma_f32 v231,v231,s54,v14\n"
MF "v[208:211],a[200:203],a[76:79],v[208:211]\n"
CV "v243,v230,v231\n"
CV "v242,v228,v229\n"
MF "v[208:211],a[204:207],a[116:119],v[208:211]\n"
CV "v241,v226,v227\n"
CV "v240,v224,v225\n"
MF "v[208:211],a[208:211],a[156:159],v[208:211]\n"
TR "a[200:201],v1 offset:49160\n"
"v_fma_f32 v216,v216,s54,v14\n"
MF "v[208:211],a[212:215],a[196:199],v[208:211]\n"
"s_add_u32 m0,s64,1792\n"
TR "a[202:203],v1 offset:49672\n"
"v_fma_f32 v217,v217,s54,v14\n"
MF "v[200:203],a[212:215],a[192:195],v[200:203]\n"
LD "v7,s[32:35],s72 offen offset:256 nt lds\n"
MF "v[200:203],a[208:211],a[152:155],v[200:203]\n"
TR "a[204:205],v1 offset:53256\n"
"v_fma_f32 v218,v218,s54,v14\n"
MF "v[192:195],a[208:211],a[148:151],v[192:195]\n"
TR "a[206:207],v1 offset:53768\n"
"v_fma_f32 v219,v219,s54,v14\n"
MF "v[192:195],a[212:215],a[188:191],v[192:195]\n"
"v_fma_f32 v220,v220,s54,v14\n"
"v_fma_f32 v221,v221,s54,v14\n"
MF "v[184:187],a[212:215],a[184:187],v[184:187]\n"
"s_add_u32 s72,s72,6144\n"
"v_fma_f32 v222,v222,s54,v14\n"
"v_fma_f32 v223,v223,s54,v14\n"
MF "v[184:187],a[208:211],a[144:147],v[184:187]\n"
"s_add_u32 m0,s64,6144\n"
CV "v239,v222,v223\n"
CV "v238,v220,v221\n"
MF "v[176:179],a[208:211],a[140:143],v[176:179]\n"
LD "v7,s[32:35],s72 offen nt lds\n"
MF "v[176:179],a[212:215],a[180:183],v[176:179]\n"
"s_waitcnt lgkmcnt(4)\n"
CV "v237,v218,v219\n"
CV "v236,v216,v217\n"
MF "v[140:143],a[216:219],a[60:63],v[140:143]\n"
TR "a[208:209],v1 offset:32768\n"
"v_fma_f32 v208,v208,s54,v14\n"
MF "v[140:143],a[220:223],a[100:103],v[140:143]\n"
TR "a[210:211],v1 offset:33280\n"
"v_fma_f32 v209,v209,s54,v14\n"
MF "v[148:151],a[220:223],a[104:107],v[148:151]\n"
"v_fma_f32 v210,v210,s54,v14\n"
"v_fma_f32 v211,v211,s54,v14\n"
MF "v[148:151],a[216:219],a[64:67],v[148:151]\n"
TR "a[212:213],v1 offset:36864\n"
"v_fma_f32 v212,v212,s54,v14\n"
MF "v[156:159],a[216:219],a[68:71],v[156:159]\n"
"s_add_u32 m0,s64,7040\n"
TR "a[214:215],v1 offset:37376\n"
"v_fma_f32 v213,v213,s54,v14\n"
MF "v[156:159],a[220:223],a[108:111],v[156:159]\n"
LD "v7,s[32:35],s72 offen offset:128 nt lds\n"
MF "v[164:167],a[220:223],a[112:115],v[164:167]\n"
"v_fma_f32 v214,v214,s54,v14\n"
"v_fma_f32 v215,v215,s54,v14\n"
MF "v[164:167],a[216:219],a[72:75],v[164:167]\n"
CV "v235,v214,v215\n"
CV "v234,v212,v213\n"
MF "v[172:175],a[216:219],a[76:79],v[172:175]\n"
"s_add_u32 m0,s64,7936\n"
CV "v233,v210,v211\n"
CV "v232,v208,v209\n"
MF "v[172:175],a[220:223],a[116:119],v[172:175]\n"
"s_waitcnt lgkmcnt(4)\n"
LD "v7,s[32:35],s72 offen offset:256 nt lds\n"
MF "v[140:143],a[200:203],a[140:143],v[140:143]\n"
TR "a[216:217],v1 offset:49152\n"
"v_fma_f32 v200,v200,s54,v14\n"
MF "v[140:143],a[204:207],a[180:183],v[140:143]\n"
TR "a[218:219],v1 offset:49664\n"
"v_fma_f32 v201,v201,s54,v14\n"
MF "v[148:151],a[204:207],a[184:187],v[148:151]\n"
"v_fma_f32 v202,v202,s54,v14\n"
"v_fma_f32 v203,v203,s54,v14\n"
MF "v[148:151],a[200:203],a[144:147],v[148:151]\n"
TR "a[220:221],v1 offset:53248\n"
"v_fma_f32 v204,v204,s54,v14\n"
MF "v[156:159],a[200:203],a[148:151],v[156:159]\n"
TR "a[222:223],v1 offset:53760\n"
"v_fma_f32 v205,v205,s54,v14\n"
MF "v[156:159],a[204:207],a[188:191],v[156:159]\n"
"v_fma_f32 v206,v206,s54,v14\n"
"v_fma_f32 v207,v207,s54,v14\n"
MF "v[164:167],a[204:207],a[192:195],v[164:167]\n"
CV "v231,v206,v207\n"
CV "v230,v204,v205\n"
MF "v[164:167],a[200:203],a[152:155],v[164:167]\n"
CV "v229,v202,v203\n"
CV "v228,v200,v201\n"
MF "v[172:175],a[200:203],a[156:159],v[172:175]\n"
"v_fma_f32 v192,v192,s54,v14\n"
"v_fma_f32 v193,v193,s54,v14\n"
MF "v[172:175],a[204:207],a[196:199],v[172:175]\n"
"s_waitcnt lgkmcnt(0)\n"
"v_fma_f32 v194,v194,s54,v14\n"
"v_fma_f32 v195,v195,s54,v14\n"
MF "v[136:139],a[208:211],a[60:63],v[136:139]\n"
TR "a[200:201],v0 offset:32776\n"
"v_fma_f32 v196,v196,s54,v14\n"
MF "v[136:139],a[212:215],a[100:103],v[136:139]\n"
TR "a[202:203],v0 offset:33288\n"
"v_fma_f32 v197,v197,s54,v14\n"
MF "v[144:147],a[212:215],a[104:107],v[144:147]\n"
"v_fma_f32 v198,v198,s54,v14\n"
"v_fma_f32 v199,v199,s54,v14\n"
MF "v[144:147],a[208:211],a[64:67],v[144:147]\n"
TR "a[204:205],v0 offset:36872\n"
CV "v227,v198,v199\n"
MF "v[152:155],a[208:211],a[68:71],v[152:155]\n"
TR "a[206:207],v0 offset:37384\n"
CV "v226,v196,v197\n"
MF "v[152:155],a[212:215],a[108:111],v[152:155]\n"
CV "v225,v194,v195\n"
CV "v224,v192,v193\n"
MF "v[160:163],a[212:215],a[112:115],v[160:163]\n"
"v_fma_f32 v184,v184,s54,v14\n"
"v_fma_f32 v185,v185,s54,v14\n"
MF "v[160:163],a[208:211],a[72:75],v[160:163]\n"
"v_fma_f32 v186,v186,s54,v14\n"
"v_fma_f32 v187,v187,s54,v14\n"
MF "v[168:171],a[208:211],a[76:79],v[168:171]\n"
"v_fma_f32 v188,v188,s54,v14\n"
"v_fma_f32 v189,v189,s54,v14\n"
MF "v[168:171],a[212:215],a[116:119],v[168:171]\n"
"v_fma_f32 v190,v190,s54,v14\n"
"v_fma_f32 v191,v191,s54,v14\n"
MF "v[168:171],a[216:219],a[156:159],v[168:171]\n"
TR "a[208:209],v0 offset:49160\n"
CV "v223,v190,v191\n"
MF "v[168:171],a[220:223],a[196:199],v[168:171]\n"
TR "a[210:211],v0 offset:49672\n"
CV "v222,v188,v189\n"
MF "v[160:163],a[220:223],a[192:195],v[160:163]\n"
CV "v221,v186,v187\n"
CV "v220,v184,v185\n"
MF "v[160:163],a[216:219],a[152:155],v[160:163]\n"
TR "a[212:213],v0 offset:53256\n"
"v_fma_f32 v176,v176,s54,v14\n"
MF "v[152:155],a[216:219],a[148:151],v[152:155]\n"
TR "a[214:215],v0 offset:53768\n"
"v_fma_f32 v177,v177,s54,v14\n"
MF "v[152:155],a[220:223],a[188:191],v[152:155]\n"
"v_fma_f32 v178,v178,s54,v14\n"
"v_fma_f32 v179,v179,s54,v14\n"
MF "v[144:147],a[220:223],a[184:187],v[144:147]\n"
"v_fma_f32 v180,v180,s54,v14\n"
"v_fma_f32 v181,v181,s54,v14\n"
MF "v[144:147],a[216:219],a[144:147],v[144:147]\n"
"v_fma_f32 v182,v182,s54,v14\n"
"v_fma_f32 v183,v183,s54,v14\n"
MF "v[136:139],a[216:219],a[140:143],v[136:139]\n"
CV "v219,v182,v183\n"
CV "v218,v180,v181\n"
MF "v[136:139],a[220:223],a[180:183],v[136:139]\n"
"s_waitcnt lgkmcnt(4)\n"
CV "v217,v178,v179\n"
CV "v216,v176,v177\n"
MF "v[100:103],a[200:203],a[60:63],v[100:103]\n"
TR "a[216:217],v0 offset:32768\n"
"v_fma_f32 v168,v168,s54,v14\n"
MF "v[100:103],a[204:207],a[100:103],v[100:103]\n"
TR "a[218:219],v0 offset:33280\n"
"v_fma_f32 v169,v169,s54,v14\n"
MF "v[108:111],a[204:207],a[104:107],v[108:111]\n"
"v_fma_f32 v170,v170,s54,v14\n"
"v_fma_f32 v171,v171,s54,v14\n"
MF "v[108:111],a[200:203],a[64:67],v[108:111]\n"
TR "a[220:221],v0 offset:36864\n"
"v_fma_f32 v172,v172,s54,v14\n"
MF "v[116:119],a[200:203],a[68:71],v[116:119]\n"
TR "a[222:223],v0 offset:37376\n"
"v_fma_f32 v173,v173,s54,v14\n"
MF "v[116:119],a[204:207],a[108:111],v[116:119]\n"
"v_fma_f32 v174,v174,s54,v14\n"
"v_fma_f32 v175,v175,s54,v14\n"
MF "v[124:127],a[204:207],a[112:115],v[124:127]\n"
CV "v215,v174,v175\n"
CV "v214,v172,v173\n"
MF "v[124:127],a[200:203],a[72:75],v[124:127]\n"
CV "v213,v170,v171\n"
CV "v212,v168,v169\n"
MF "v[132:135],a[200:203],a[76:79],v[132:135]\n"
".Lpg8b:\n"
"s_cbranch_execz .Lpg11b\n"
".Lpg8d:\n"
"s_cbranch_execz .Lpgpsl_d\n"
"v_fma_f32 v160,v160,s54,v14\n"
"v_fma_f32 v161,v161,s54,v14\n"
MF "v[132:135],a[204:207],a[116:119],v[132:135]\n"
"s_waitcnt lgkmcnt(4)\n"
"v_fma_f32 v162,v162,s54,v14\n"
"v_fma_f32 v163,v163,s54,v14\n"
MF "v[100:103],a[208:211],a[140:143],v[100:103]\n"
TR "a[200:201],v0 offset:49152\n"
"v_fma_f32 v164,v164,s54,v14\n"
MF "v[100:103],a[212:215],a[180:183],v[100:103]\n"
TR "a[202:203],v0 offset:49664\n"
"v_fma_f32 v165,v165,s54,v14\n"
MF "v[108:111],a[212:215],a[184:187],v[108:111]\n"
"v_fma_f32 v166,v166,s54,v14\n"
"v_fma_f32 v167,v167,s54,v14\n"
MF "v[108:111],a[208:211],a[144:147],v[108:111]\n"
TR "a[204:205],v0 offset:53248\n"
CV "v211,v166,v167\n"
MF "v[116:119],a[208:211],a[148:151],v[116:119]\n"
TR "a[206:207],v0 offset:53760\n"
CV "v210,v164,v165\n"
MF "v[116:119],a[212:215],a[188:191],v[116:119]\n"
CV "v209,v162,v163\n"
CV "v208,v160,v161\n"
MF "v[124:127],a[212:215],a[192:195],v[124:127]\n"
"v_add_u32 v0,0xc000,v0\n"
"v_fma_f32 v152,v152,s54,v14\n"
MF "v[124:127],a[208:211],a[152:155],v[124:127]\n"
"v_add_u32 v1,0xc000,v1\n"
"v_fma_f32 v153,v153,s54,v14\n"
MF "v[132:135],a[208:211],a[156:159],v[132:135]\n"
"v_fma_f32 v154,v154,s54,v14\n"
"v_fma_f32 v155,v155,s54,v14\n"
MF "v[132:135],a[212:215],a[196:199],v[132:135]\n"
"s_waitcnt vmcnt(9) lgkmcnt(0)\n"
"s_barrier\n"
MF "v[96:99],a[216:219],a[60:63],v[96:99]\n"
TR "a[208:209],v1 offset:16392\n"
"v_fma_f32 v156,v156,s54,v14\n"
MF "v[96:99],a[220:223],a[100:103],v[96:99]\n"
TR "a[210:211],v1 offset:16904\n"
"v_fma_f32 v157,v157,s54,v14\n"
MF "v[104:107],a[220:223],a[104:107],v[104:107]\n"
"s_add_u32 s72,s72,6144\n"
"v_fma_f32 v158,v158,s54,v14\n"
"v_fma_f32 v159,v159,s54,v14\n"
MF "v[104:107],a[216:219],a[64:67],v[104:107]\n"
"s_cmp_eq_u32 s72,s17\n"
"s_cselect_b32 s72,s16,s72\n"
TR "a[212:213],v1 offset:20488\n"
MF "v[112:115],a[216:219],a[68:71],v[112:115]\n"
"s_add_u32 m0,s64,24576\n"
TR "a[214:215],v1 offset:21000\n"
CV "v207,v158,v159\n"
MF "v[112:115],a[220:223],a[108:111],v[112:115]\n"
LD "v7,s[32:35],s72 offen nt lds\n"
MF "v[120:123],a[220:223],a[112:115],v[120:123]\n"
"s_add_u32 m0,s64,25472\n"
CV "v206,v156,v157\n"
CV "v205,v154,v155\n"
MF "v[120:123],a[216:219],a[72:75],v[120:123]\n"
LD "v7,s[32:35],s72 offen offset:128 nt lds\n"
MF "v[128:131],a[216:219],a[76:79],v[128:131]\n"
CV "v204,v152,v153\n"
"v_fma_f32 v144,v144,s54,v14\n"
MF "v[128:131],a[220:223],a[116:119],v[128:131]\n"
"v_fma_f32 v145,v145,s54,v14\n"
"v_fma_f32 v146,v146,s54,v14\n"
MF "v[128:131],a[200:203],a[156:159],v[128:131]\n"
TR "a[216:217],v1 offset:32776\n"
"v_fma_f32 v147,v147,s54,v14\n"
MF "v[128:131],a[204:207],a[196:199],v[128:131]\n"
"s_add_u32 m0,s64,26368\n"
TR "a[218:219],v1 offset:33288\n"
"v_fma_f32 v148,v148,s54,v14\n"
MF "v[120:123],a[204:207],a[192:195],v[120:123]\n"
LD "v7,s[32:35],s72 offen offset:256 nt lds\n"
MF "v[120:123],a[200:203],a[152:155],v[120:123]\n"
TR "a[220:221],v1 offset:36872\n"
"v_fma_f32 v149,v149,s54,v14\n"
MF "v[112:115],a[200:203],a[148:151],v[112:115]\n"
TR "a[222:223],v1 offset:37384\n"
"v_fma_f32 v150,v150,s54,v14\n"
MF "v[112:115],a[204:207],a[188:191],v[112:115]\n"
"v_fma_f32 v151,v151,s54,v14\n"
CV "v203,v150,v151\n"
MF "v[104:107],a[204:207],a[184:187],v[104:107]\n"
"s_add_u32 s72,s72,6144\n"
CV "v202,v148,v149\n"
CV "v201,v146,v147\n"
MF "v[104:107],a[200:203],a[144:147],v[104:107]\n"
"s_add_u32 m0,s64,30720\n"
CV "v200,v144,v145\n"
"v_fma_f32 v136,v136,s54,v14\n"
MF "v[96:99],a[200:203],a[140:143],v[96:99]\n"
LD "v7,s[32:35],s72 offen nt lds\n"
MF "v[96:99],a[204:207],a[180:183],v[96:99]\n"
"s_waitcnt lgkmcnt(4)\n"
"v_fma_f32 v137,v137,s54,v14\n"
"v_fma_f32 v138,v138,s54,v14\n"
MF "v[60:63],a[208:211],a[60:63],v[60:63]\n"
TR "a[200:201],v1 offset:16384\n"
"v_fma_f32 v139,v139,s54,v14\n"
MF "v[60:63],a[212:215],a[100:103],v[60:63]\n"
TR "a[202:203],v1 offset:16896\n"
"v_fma_f32 v140,v140,s54,v14\n"
MF "v[68:71],a[212:215],a[104:107],v[68:71]\n"
"v_fma_f32 v141,v141,s54,v14\n"
"v_fma_f32 v142,v142,s54,v14\n"
MF "v[68:71],a[208:211],a[64:67],v[68:71]\n"
TR "a[204:205],v1 offset:20480\n"
"v_fma_f32 v143,v143,s54,v14\n"
MF "v[76:79],a[208:211],a[68:71],v[76:79]\n"
"s_add_u32 m0,s64,31616\n"
TR "a[206:207],v1 offset:20992\n"
CV "v199,v142,v143\n"
MF "v[76:79],a[212:215],a[108:111],v[76:79]\n"
LD "v7,s[32:35],s72 offen offset:128 nt lds\n"
MF "v[84:87],a[212:215],a[112:115],v[84:87]\n"
CV "v198,v140,v141\n"
CV "v197,v138,v139\n"
MF "v[84:87],a[208:211],a[72:75],v[84:87]\n"
CV "v196,v136,v137\n"
"v_fma_f32 v128,v128,s54,v14\n"
MF "v[92:95],a[208:211],a[76:79],v[92:95]\n"
"s_add_u32 m0,s64,32512\n"
"v_fma_f32 v129,v129,s54,v14\n"
"v_fma_f32 v130,v130,s54,v14\n"
MF "v[92:95],a[212:215],a[116:119],v[92:95]\n"
"s_waitcnt lgkmcnt(4)\n"
LD "v7,s[32:35],s72 offen offset:256 nt lds\n"
MF "v[60:63],a[216:219],a[140:143],v[60:63]\n"
TR "a[208:209],v1 offset:32768\n"
"v_fma_f32 v131,v131,s54,v14\n"
MF "v[60:63],a[220:223],a[180:183],v[60:63]\n"
TR "a[210:211],v1 offset:33280\n"
"v_fma_f32 v132,v132,s54,v14\n"
MF "v[68:71],a[220:223],a[184:187],v[68:71]\n"
"v_fma_f32 v133,v133,s54,v14\n"
"v_fma_f32 v134,v134,s54,v14\n"
MF "v[68:71],a[216:219],a[144:147],v[68:71]\n"
TR "a[212:213],v1 offset:36864\n"
"v_fma_f32 v135,v135,s54,v14\n"
MF "v[76:79],a[216:219],a[148:151],v[76:79]\n"
TR "a[214:215],v1 offset:37376\n"
CV "v195,v134,v135\n"
MF "v[76:79],a[220:223],a[188:191],v[76:79]\n"
CV "v194,v132,v133\n"
CV "v193,v130,v131\n"
MF "v[84:87],a[220:223],a[192:195],v[84:87]\n"
CV "v192,v128,v129\n"
"v_fma_f32 v120,v120,s54,v14\n"
MF "v[84:87],a[216:219],a[152:155],v[84:87]\n"
"v_fma_f32 v121,v121,s54,v14\n"
"v_fma_f32 v122,v122,s54,v14\n"
MF "v[92:95],a[216:219],a[156:159],v[92:95]\n"
"v_fma_f32 v123,v123,s54,v14\n"
"v_fma_f32 v124,v124,s54,v14\n"
MF "v[92:95],a[220:223],a[196:199],v[92:95]\n"
"s_waitcnt lgkmcnt(0)\n"
"v_fma_f32 v125,v125,s54,v14\n"
"v_fma_f32 v126,v126,s54,v14\n"
MF "v[56:59],a[200:203],a[60:63],v[56:59]\n"
TR "a[216:217],v0 offset:16392\n"
"v_fma_f32 v127,v127,s54,v14\n"
MF "v[56:59],a[204:207],a[100:103],v[56:59]\n"
TR "a[218:219],v0 offset:16904\n"
CV "v191,v126,v127\n"
MF "v[64:67],a[204:207],a[104:107],v[64:67]\n"
CV "v190,v124,v125\n"
CV "v189,v122,v123\n"
MF "v[64:67],a[200:203],a[64:67],v[64:67]\n"
TR "a[220:221],v0 offset:20488\n"
CV "v188,v120,v121\n"
MF "v[72:75],a[200:203],a[68:71],v[72:75]\n"
TR "a[222:223],v0 offset:21000\n"
"v_fma_f32 v112,v112,s54,v14\n"
MF "v[72:75],a[204:207],a[108:111],v[72:75]\n"
"v_fma_f32 v113,v113,s54,v14\n"
"v_fma_f32 v114,v114,s54,v14\n"
MF "v[80:83],a[204:207],a[112:115],v[80:83]\n"
"v_fma_f32 v115,v115,s54,v14\n"
"v_fma_f32 v116,v116,s54,v14\n"
MF "v[80:83],a[200:203],a[72:75],v[80:83]\n"
"v_fma_f32 v117,v117,s54,v14\n"
"v_fma_f32 v118,v118,s54,v14\n"
MF "v[88:91],a[200:203],a[76:79],v[88:91]\n"
"v_fma_f32 v119,v119,s54,v14\n"
CV "v187,v118,v119\n"
MF "v[88:91],a[204:207],a[116:119],v[88:91]\n"
CV "v186,v116,v117\n"
CV "v185,v114,v115\n"
MF "v[88:91],a[208:211],a[156:159],v[88:91]\n"
TR "a[200:201],v0 offset:32776\n"
CV "v184,v112,v113\n"
MF "v[88:91],a[212:215],a[196:199],v[88:91]\n"
TR "a[202:203],v0 offset:33288\n"
"v_fma_f32 v104,v104,s54,v14\n"
MF "v[80:83],a[212:215],a[192:195],v[80:83]\n"
"v_fma_f32 v105,v105,s54,v14\n"
"v_fma_f32 v106,v106,s54,v14\n"
MF "v[80:83],a[208:211],a[152:155],v[80:83]\n"
TR "a[204:205],v0 offset:36872\n"
"v_fma_f32 v107,v107,s54,v14\n"
MF "v[72:75],a[208:211],a[148:151],v[72:75]\n"
TR "a[206:207],v0 offset:37384\n"
"v_fma_f32 v108,v108,s54,v14\n"
MF "v[72:75],a[212:215],a[188:191],v[72:75]\n"
"v_fma_f32 v109,v109,s54,v14\n"
"v_fma_f32 v110,v110,s54,v14\n"
MF "v[64:67],a[212:215],a[184:187],v[64:67]\n"
"v_fma_f32 v111,v111,s54,v14\n"
CV "v183,v110,v111\n"
MF "v[64:67],a[208:211],a[144:147],v[64:67]\n"
CV "v182,v108,v109\n"
CV "v181,v106,v107\n"
MF "v[56:59],a[208:211],a[140:143],v[56:59]\n"
CV "v180,v104,v105\n"
"v_fma_f32 v96,v96,s54,v14\n"
MF "v[56:59],a[212:215],a[180:183],v[56:59]\n"
"s_waitcnt lgkmcnt(4)\n"
"v_fma_f32 v97,v97,s54,v14\n"
"v_fma_f32 v98,v98,s54,v14\n"
MF "v[20:23],a[216:219],a[60:63],v[20:23]\n"
TR "a[208:209],v0 offset:16384\n"
"v_fma_f32 v99,v99,s54,v14\n"
MF "v[20:23],a[220:223],a[100:103],v[20:23]\n"
TR "a[210:211],v0 offset:16896\n"
"v_fma_f32 v100,v100,s54,v14\n"
MF "v[28:31],a[220:223],a[104:107],v[28:31]\n"
"v_fma_f32 v101,v101,s54,v14\n"
"v_fma_f32 v102,v102,s54,v14\n"
MF "v[28:31],a[216:219],a[64:67],v[28:31]\n"
TR "a[212:213],v0 offset:20480\n"
"v_fma_f32 v103,v103,s54,v14\n"
MF "v[36:39],a[216:219],a[68:71],v[36:39]\n"
TR "a[214:215],v0 offset:20992\n"
CV "v179,v102,v103\n"
MF "v[36:39],a[220:223],a[108:111],v[36:39]\n"
CV "v178,v100,v101\n"
CV "v177,v98,v99\n"
MF "v[44:47],a[220:223],a[112:115],v[44:47]\n"
CV "v176,v96,v97\n"
"v_fma_f32 v88,v88,s54,v14\n"
MF "v[44:47],a[216:219],a[72:75],v[44:47]\n"
"v_fma_f32 v89,v89,s54,v14\n"
"v_fma_f32 v90,v90,s54,v14\n"
MF "v[52:55],a[216:219],a[76:79],v[52:55]\n"
"v_fma_f32 v91,v91,s54,v14\n"
"v_fma_f32 v92,v92,s54,v14\n"
MF "v[52:55],a[220:223],a[116:119],v[52:55]\n"
"s_waitcnt lgkmcnt(4)\n"
"v_fma_f32 v93,v93,s54,v14\n"
"v_fma_f32 v94,v94,s54,v14\n"
MF "v[20:23],a[200:203],a[140:143],v[20:23]\n"
TR "a[216:217],v0 offset:32768\n"
"v_fma_f32 v95,v95,s54,v14\n"
MF "v[20:23],a[204:207],a[180:183],v[20:23]\n"
TR "a[218:219],v0 offset:33280\n"
CV "v175,v94,v95\n"
MF "v[28:31],a[204:207],a[184:187],v[28:31]\n"
CV "v174,v92,v93\n"
CV "v173,v90,v91\n"
MF "v[28:31],a[200:203],a[144:147],v[28:31]\n"
TR "a[220:221],v0 offset:36864\n"
CV "v172,v88,v89\n"
MF "v[36:39],a[200:203],a[148:151],v[36:39]\n"
TR "a[222:223],v0 offset:37376\n"
"v_fma_f32 v80,v80,s54,v14\n"
MF "v[36:39],a[204:207],a[188:191],v[36:39]\n"
"v_fma_f32 v81,v81,s54,v14\n"
"v_fma_f32 v82,v82,s54,v14\n"
MF "v[44:47],a[204:207],a[192:195],v[44:47]\n"
"v_fma_f32 v83,v83,s54,v14\n"
"v_fma_f32 v84,v84,s54,v14\n"
MF "v[44:47],a[200:203],a[152:155],v[44:47]\n"
"v_fma_f32 v85,v85,s54,v14\n"
"v_fma_f32 v86,v86,s54,v14\n"
MF "v[52:55],a[200:203],a[156:159],v[52:55]\n"
"v_fma_f32 v87,v87,s54,v14\n"
CV "v171,v86,v87\n"
MF "v[52:55],a[204:207],a[196:199],v[52:55]\n"
"s_waitcnt lgkmcnt(0)\n"
"s_barrier\n"
MF "v[16:19],a[208:211],a[60:63],v[16:19]\n"
"s_add_u32 m0,s65,49152\n"
CV "v170,v84,v85\n"
CV "v169,v82,v83\n"
MF "v[16:19],a[212:215],a[100:103],v[16:19]\n"
LD "v8,s[36:39],s73 offen nt lds\n"
MF "v[24:27],a[212:215],a[104:107],v[24:27]\n"
"s_add_u32 m0,s65,50048\n"
CV "v168,v80,v81\n"
"v_fma_f32 v72,v72,s54,v14\n"
MF "v[24:27],a[208:211],a[64:67],v[24:27]\n"
LD "v8,s[36:39],s73 offen offset:128 nt lds\n"
MF "v[32:35],a[208:211],a[68:71],v[32:35]\n"
"v_fma_f32 v73,v73,s54,v14\n"
"v_fma_f32 v74,v74,s54,v14\n"
MF "v[32:35],a[212:215],a[108:111],v[32:35]\n"
"s_add_u32 m0,s65,50944\n"
"v_fma_f32 v75,v75,s54,v14\n"
"v_fma_f32 v76,v76,s54,v14\n"
MF "v[40:43],a[212:215],a[112:115],v[40:43]\n"
LD "v8,s[36:39],s73 offen offset:256 nt lds\n"
MF "v[40:43],a[208:211],a[72:75],v[40:43]\n"
"v_fma_f32 v77,v77,s54,v14\n"
"v_fma_f32 v78,v78,s54,v14\n"
MF "v[48:51],a[208:211],a[76:79],v[48:51]\n"
"s_add_u32 m0,s65,49152\n"
"v_fma_f32 v79,v79,s54,v14\n"
CV "v167,v78,v79\n"
MF "v[48:51],a[212:215],a[116:119],v[48:51]\n"
LD "v8,s[36:39],s73 offen offset:3072 nt lds\n"
MF "v[48:51],a[216:219],a[156:159],v[48:51]\n"
"s_add_u32 m0,s65,50048\n"
CV "v166,v76,v77\n"
CV "v165,v74,v75\n"
MF "v[48:51],a[220:223],a[196:199],v[48:51]\n"
LD "v8,s[36:39],s73 offen offset:3200 nt lds\n"
MF "v[40:43],a[220:223],a[192:195],v[40:43]\n"
CV "v164,v72,v73\n"
"v_fma_f32 v64,v64,s54,v14\n"
MF "v[40:43],a[216:219],a[152:155],v[40:43]\n"
"s_add_u32 m0,s65,50944\n"
"v_fma_f32 v65,v65,s54,v14\n"
"v_fma_f32 v66,v66,s54,v14\n"
MF "v[32:35],a[216:219],a[148:151],v[32:35]\n"
LD "v8,s[36:39],s73 offen offset:3328 nt lds\n"
MF "v[32:35],a[220:223],a[188:191],v[32:35]\n"
"v_fma_f32 v67,v67,s54,v14\n"
"v_fma_f32 v68,v68,s54,v14\n"
MF "v[24:27],a[220:223],a[184:187],v[24:27]\n"
"v_fma_f32 v69,v69,s54,v14\n"
"v_fma_f32 v70,v70,s54,v14\n"
MF "v[24:27],a[216:219],a[144:147],v[24:27]\n"
"v_fma_f32 v71,v71,s54,v14\n"
CV "v163,v70,v71\n"
MF "v[16:19],a[216:219],a[140:143],v[16:19]\n"
CV "v162,v68,v69\n"
CV "v161,v66,v67\n"
MF "v[16:19],a[220:223],a[180:183],v[16:19]\n"
CV "v160,v64,v65\n"
AR "v0,a244\n"
"v_xor_b32 v1,64,v0\n"
"s_waitcnt vmcnt(12) lgkmcnt(0)\n"
"s_barrier\n"
"ds_read_b128 v[92:95],v0\n"
"ds_read_b128 v[96:99],v1\n"
"ds_read_b128 v[100:103],v0 offset:1024\n"
"v_pk_fma_f32 v[56:57],v[56:57],s[54:55],v[14:15] op_sel_hi:[1,1,0]\n"
"v_pk_fma_f32 v[58:59],v[58:59],s[54:55],v[14:15] op_sel_hi:[1,1,0]\n"
"v_pk_fma_f32 v[60:61],v[60:61],s[54:55],v[14:15] op_sel_hi:[1,1,0]\n"
"v_pk_fma_f32 v[62:63],v[62:63],s[54:55],v[14:15] op_sel_hi:[1,1,0]\n"
CV "v159,v62,v63\n"
CV "v158,v60,v61\n"
CV "v157,v58,v59\n"
CV "v156,v56,v57\n"
"v_pk_mul_f32 v[48:49],v[48:49],s[54:55]\n"
"v_pk_mul_f32 v[50:51],v[50:51],s[54:55]\n"
"v_pk_mul_f32 v[52:53],v[52:53],s[54:55]\n"
"v_pk_mul_f32 v[54:55],v[54:55],s[54:55]\n"
CV "v155,v54,v55\n"
CV "v154,v52,v53\n"
CV "v153,v50,v51\n"
CV "v152,v48,v49\n"
"v_pk_mul_f32 v[40:41],v[40:41],s[54:55]\n"
"v_pk_mul_f32 v[42:43],v[42:43],s[54:55]\n"
"v_pk_mul_f32 v[44:45],v[44:45],s[54:55]\n"
"v_pk_mul_f32 v[46:47],v[46:47],s[54:55]\n"
CV "v151,v46,v47\n"
CV "v150,v44,v45\n"
CV "v149,v42,v43\n"
CV "v148,v40,v41\n"
"v_pk_mul_f32 v[32:33],v[32:33],s[54:55]\n"
"v_pk_mul_f32 v[34:35],v[34:35],s[54:55]\n"
"v_pk_mul_f32 v[36:37],v[36:37],s[54:55]\n"
"v_pk_mul_f32 v[38:39],v[38:39],s[54:55]\n"
CV "v147,v38,v39\n"
CV "v146,v36,v37\n"
CV "v145,v34,v35\n"
CV "v144,v32,v33\n"
"v_pk_mul_f32 v[24:25],v[24:25],s[54:55]\n"
"v_pk_mul_f32 v[26:27],v[26:27],s[54:55]\n"
"v_pk_mul_f32 v[28:29],v[28:29],s[54:55]\n"
"v_pk_mul_f32 v[30:31],v[30:31],s[54:55]\n"
CV "v143,v30,v31\n"
CV "v142,v28,v29\n"
CV "v141,v26,v27\n"
CV "v140,v24,v25\n"
"v_pk_mul_f32 v[16:17],v[16:17],s[54:55]\n"
"v_pk_mul_f32 v[18:19],v[18:19],s[54:55]\n"
"v_pk_mul_f32 v[20:21],v[20:21],s[54:55]\n"
"v_pk_mul_f32 v[22:23],v[22:23],s[54:55]\n"
CV "v139,v22,v23\n"
CV "v138,v20,v21\n"
CV "v137,v18,v19\n"
CV "v136,v16,v17\n"
"s_cmp_eq_u32 s57,s69\n"
"s_cbranch_scc1 .LattB\n"
AR "v2,a245\n"
"v_add_u32 v2,49152,v2\n"
"v_xor_b32 v3,32,v2\n"
"v_xor_b32 v4,64,v2\n"
"v_xor_b32 v5,96,v2\n"
"s_mov_b32 s61,7\n"
".Latt2x:\n"
"s_cmp_eq_u32 s57,1\n"
"s_cbranch_scc1 .Lt0kx\n"
".Lt0nx:\n"
"s_waitcnt lgkmcnt(2)\n"
MF "v[72:75],v[92:95],v[136:139],0\n"
"s_add_u32 s73,s73,12288\n"
"ds_read_b128 v[104:107],v1 offset:1024\n"
AR "v10,a224\n"
MF "v[76:79],v[92:95],v[140:143],0\n"
"s_cmp_eq_u32 s73,s19\n"
"s_cselect_b32 s73,s18,s73\n"
"s_add_u32 m0,s65,73728\n"
MF "v[80:83],v[92:95],v[144:147],0\n"
LD "v8,s[36:39],s73 offen nt lds\n"
MF "v[84:87],v[92:95],v[148:151],0\n"
"v_max_f32_dpp v10,|v10|,|v10| quad_perm:[1,0,3,2] row_mask:0xf bank_mask:0xf\n"
AR "v11,a225\n"
MF "v[88:91],v[92:95],v[152:155],0\n"
"s_waitcnt lgkmcnt(2)\n"
"ds_read_b128 v[92:95],v0 offset:2048\n"
AR "v12,a226\n"
MF "v[72:75],v[96:99],v[156:159],v[72:75]\n"
"s_add_u32 m0,s65,74624\n"
"v_max_f32_dpp v10,v10,v10 quad_perm:[2,3,0,1] row_mask:0xf bank_mask:0xf\n"
"v_max_f32_dpp v11,|v11|,|v11| quad_perm:[1,0,3,2] row_mask:0xf bank_mask:0xf\n"
MF "v[76:79],v[96:99],v[160:163],v[76:79]\n"
LD "v8,s[36:39],s73 offen offset:128 nt lds\n"
MF "v[80:83],v[96:99],v[164:167],v[80:83]\n"
"v_max_f32_dpp v10,v10,v10 row_half_mirror row_mask:0xf bank_mask:0xf\n"
"v_max_f32_dpp v11,v11,v11 quad_perm:[2,3,0,1] row_mask:0xf bank_mask:0xf\n"
MF "v[84:87],v[96:99],v[168:171],v[84:87]\n"
"v_readlane_b32 s13,v10,0\n"
"v_readlane_b32 s0,v10,24\n"
MF "v[88:91],v[96:99],v[172:175],v[88:91]\n"
"s_waitcnt lgkmcnt(2)\n"
"ds_read_b128 v[96:99],v1 offset:2048\n"
"v_readlane_b32 s1,v10,32\n"
MF "v[72:75],v[100:103],v[176:179],v[72:75]\n"
"s_add_u32 m0,s65,75520\n"
"v_readlane_b32 s2,v10,56\n"
"v_max_f32_dpp v11,v11,v11 row_half_mirror row_mask:0xf bank_mask:0xf\n"
MF "v[76:79],v[100:103],v[180:183],v[76:79]\n"
LD "v8,s[36:39],s73 offen offset:256 nt lds\n"
MF "v[80:83],v[100:103],v[184:187],v[80:83]\n"
"s_min_u32 s13,s13,s0\n"
"v_readlane_b32 s11,v11,0\n"
"v_readlane_b32 s3,v11,24\n"
MF "v[84:87],v[100:103],v[188:191],v[84:87]\n"
"s_min_u32 s1,s1,s2\n"
"v_readlane_b32 s4,v11,32\n"
"v_readlane_b32 s5,v11,56\n"
MF "v[88:91],v[100:103],v[192:195],v[88:91]\n"
"s_waitcnt lgkmcnt(2)\n"
"ds_read_b128 v[100:103],v0 offset:6144\n"
"v_max_f32_dpp v12,|v12|,|v12| quad_perm:[1,0,3,2] row_mask:0xf bank_mask:0xf\n"
MF "v[72:75],v[104:107],v[196:199],v[72:75]\n"
"s_add_u32 m0,s65,73728\n"
MF "v[76:79],v[104:107],v[200:203],v[76:79]\n"
LD "v8,s[36:39],s73 offen offset:3072 nt lds\n"
MF "v[80:83],v[104:107],v[204:207],v[80:83]\n"
"s_min_u32 s13,s13,s1\n"
"v_max_f32_dpp v12,v12,v12 quad_perm:[2,3,0,1] row_mask:0xf bank_mask:0xf\n"
MF "v[84:87],v[104:107],v[208:211],v[84:87]\n"
"s_and_b32 s13,s13,0x7f800000\n"
MF "v[88:91],v[104:107],v[212:215],v[88:91]\n"
"s_waitcnt lgkmcnt(2)\n"
"ds_read_b128 v[104:107],v1 offset:6144\n"
"v_max_f32_dpp v12,v12,v12 row_half_mirror row_mask:0xf bank_mask:0xf\n"
MF "v[72:75],v[92:95],v[216:219],v[72:75]\n"
"s_add_u32 m0,s65,74624\n"
"v_readlane_b32 s10,v12,0\n"
"v_readlane_b32 s6,v12,24\n"
MF "v[76:79],v[92:95],v[220:223],v[76:79]\n"
LD "v8,s[36:39],s73 offen offset:3200 nt lds\n"
MF "v[80:83],v[92:95],v[224:227],v[80:83]\n"
"s_sub_u32 s13,s13,0x14000000\n"
"v_readlane_b32 s7,v12,32\n"
"v_readlane_b32 s8,v12,56\n"
MF "v[84:87],v[92:95],v[228:231],v[84:87]\n"
"s_cmp_lt_u32 s13,0x50000000\n"
"s_cselect_b32 s13,s13,0xed400000\n"
MF "v[88:91],v[92:95],v[232:235],v[88:91]\n"
"s_waitcnt lgkmcnt(2)\n"
"ds_read_b128 v[92:95],v0 offset:7168\n"
MF "v[72:75],v[96:99],v[236:239],v[72:75]\n"
"s_add_u32 m0,s65,75520\n"
MF "v[76:79],v[96:99],v[240:243],v[76:79]\n"
LD "v8,s[36:39],s73 offen offset:3328 nt lds\n"
MF "v[80:83],v[96:99],v[244:247],v[80:83]\n"
"s_add_u32 s13,s13,0x12c00000\n"
MF "v[84:87],v[96:99],v[248:251],v[84:87]\n"
"s_min_u32 s11,s11,s3\n"
MF "v[88:91],v[96:99],v[252:255],v[88:91]\n"
"s_waitcnt lgkmcnt(2)\n"
"ds_read_b128 v[96:99],v1 offset:7168\n"
MF "v[112:115],v[100:103],v[136:139],0\n"
"s_min_u32 s4,s4,s5\n"
MF "v[116:119],v[100:103],v[140:143],0\n"
"s_min_u32 s11,s11,s4\n"
MF "v[120:123],v[100:103],v[144:147],0\n"
"s_and_b32 s11,s11,0x7f800000\n"
MF "v[124:127],v[100:103],v[148:151],0\n"
"s_sub_u32 s11,s11,0x14000000\n"
MF "v[128:131],v[100:103],v[152:155],0\n"
"s_waitcnt lgkmcnt(2)\n"
"ds_read_b128 v[100:103],v0 offset:8192\n"
MF "v[112:115],v[104:107],v[156:159],v[112:115]\n"
"s_min_u32 s10,s10,s6\n"
"v_max3_f32 v52,v72,v73,v74\n"
"v_max_f32 v52,v52,v75\n"
MF "v[116:119],v[104:107],v[160:163],v[116:119]\n"
"s_min_u32 s7,s7,s8\n"
"v_max3_f32 v53,v76,v77,v78\n"
"v_max_f32 v53,v53,v79\n"
MF "v[120:123],v[104:107],v[164:167],v[120:123]\n"
"s_min_u32 s10,s10,s7\n"
"v_max3_f32 v54,v80,v81,v82\n"
"v_max_f32 v54,v54,v83\n"
MF "v[124:127],v[104:107],v[168:171],v[124:127]\n"
"v_max3_f32 v55,v84,v85,v86\n"
"v_max_f32 v55,v55,v87\n"
MF "v[128:131],v[104:107],v[172:175],v[128:131]\n"
"s_waitcnt lgkmcnt(2)\n"
"ds_read_b128 v[104:107],v1 offset:8192\n"
"v_max3_f32 v56,v88,v89,v90\n"
MF "v[112:115],v[92:95],v[176:179],v[112:115]\n"
"s_cmp_lt_u32 s11,0x50000000\n"
"s_cselect_b32 s11,s11,0xed400000\n"
"v_max_f32 v56,v56,v91\n"
MF "v[116:119],v[92:95],v[180:183],v[116:119]\n"
".Lpg9b:\n"
"s_cbranch_execz .Lpgpsl_b\n"
"s_add_u32 s11,s11,0x12c00000\n"
MF "v[120:123],v[92:95],v[184:187],v[120:123]\n"
MF "v[124:127],v[92:95],v[188:191],v[124:127]\n"
MF "v[128:131],v[92:95],v[192:195],v[128:131]\n"
"s_waitcnt lgkmcnt(2)\n"
MF "v[112:115],v[96:99],v[196:199],v[112:115]\n"
MF "v[116:119],v[96:99],v[200:203],v[116:119]\n"
"s_waitcnt lgkmcnt(0)\n"
"s_barrier\n"
MF "v[120:123],v[96:99],v[204:207],v[120:123]\n"
"s_add_u32 s72,s72,6144\n"
MF "v[124:127],v[96:99],v[208:211],v[124:127]\n"
"s_cmp_eq_u32 s72,s17\n"
"s_cselect_b32 s72,s16,s72\n"
"s_add_u32 m0,s64,0\n"
MF "v[128:131],v[96:99],v[212:215],v[128:131]\n"
LD "v7,s[32:35],s72 offen nt lds\n"
MF "v[112:115],v[100:103],v[216:219],v[112:115]\n"
"s_add_u32 m0,s64,896\n"
MF "v[116:119],v[100:103],v[220:223],v[116:119]\n"
LD "v7,s[32:35],s72 offen offset:128 nt lds\n"
MF "v[120:123],v[100:103],v[224:227],v[120:123]\n"
"s_add_u32 m0,s64,1792\n"
MF "v[124:127],v[100:103],v[228:231],v[124:127]\n"
LD "v7,s[32:35],s72 offen offset:256 nt lds\n"
MF "v[128:131],v[100:103],v[232:235],v[128:131]\n"
"s_add_u32 s72,s72,6144\n"
"s_add_u32 m0,s64,6144\n"
MF "v[112:115],v[104:107],v[236:239],v[112:115]\n"
LD "v7,s[32:35],s72 offen nt lds\n"
MF "v[116:119],v[104:107],v[240:243],v[116:119]\n"
"s_add_u32 m0,s64,7040\n"
MF "v[120:123],v[104:107],v[244:247],v[120:123]\n"
LD "v7,s[32:35],s72 offen offset:128 nt lds\n"
MF "v[124:127],v[104:107],v[248:251],v[124:127]\n"
"s_add_u32 m0,s64,7936\n"
MF "v[128:131],v[104:107],v[252:255],v[128:131]\n"
LD "v7,s[32:35],s72 offen offset:256 nt lds\n"
"s_branch .Lt0ex\n"
".Lpgt0kx_d:\n"
"s_cbranch_execz .Lisr2\n"
".Lt0kx:\n"
"s_lshl_b32 s58,s56,10\n"
"s_add_u32 s58,s58,73728\n"
"v_add_u32 v32,s58,v9\n"
"v_add_u32 v33,65536,v32\n"
"s_waitcnt lgkmcnt(2)\n"
MF "v[72:75],v[92:95],v[136:139],0\n"
"s_add_u32 s73,s73,12288\n"
"ds_read_b128 v[104:107],v1 offset:1024\n"
AR "v10,a224\n"
MF "v[76:79],v[92:95],v[140:143],0\n"
"s_cmp_eq_u32 s73,s19\n"
"s_cselect_b32 s73,s18,s73\n"
"s_add_u32 m0,s65,73728\n"
MF "v[80:83],v[92:95],v[144:147],0\n"
LD "v8,s[36:39],s73 offen nt lds\n"
MF "v[84:87],v[92:95],v[148:151],0\n"
"v_max_f32_dpp v10,|v10|,|v10| quad_perm:[1,0,3,2] row_mask:0xf bank_mask:0xf\n"
AR "v11,a225\n"
MF "v[88:91],v[92:95],v[152:155],0\n"
"s_waitcnt lgkmcnt(2)\n"
"ds_read_b128 v[92:95],v0 offset:2048\n"
AR "v12,a226\n"
MF "v[72:75],v[96:99],v[156:159],v[72:75]\n"
"s_add_u32 m0,s65,74624\n"
"v_max_f32_dpp v10,v10,v10 quad_perm:[2,3,0,1] row_mask:0xf bank_mask:0xf\n"
"v_max_f32_dpp v11,|v11|,|v11| quad_perm:[1,0,3,2] row_mask:0xf bank_mask:0xf\n"
MF "v[76:79],v[96:99],v[160:163],v[76:79]\n"
LD "v8,s[36:39],s73 offen offset:128 nt lds\n"
MF "v[80:83],v[96:99],v[164:167],v[80:83]\n"
"v_max_f32_dpp v10,v10,v10 row_half_mirror row_mask:0xf bank_mask:0xf\n"
"v_max_f32_dpp v11,v11,v11 quad_perm:[2,3,0,1] row_mask:0xf bank_mask:0xf\n"
MF "v[84:87],v[96:99],v[168:171],v[84:87]\n"
"v_readlane_b32 s13,v10,0\n"
"v_readlane_b32 s0,v10,24\n"
MF "v[88:91],v[96:99],v[172:175],v[88:91]\n"
"s_waitcnt lgkmcnt(2)\n"
"ds_read_b128 v[96:99],v1 offset:2048\n"
"v_readlane_b32 s1,v10,32\n"
MF "v[72:75],v[100:103],v[176:179],v[72:75]\n"
"s_add_u32 m0,s65,75520\n"
"v_readlane_b32 s2,v10,56\n"
"v_max_f32_dpp v11,v11,v11 row_half_mirror row_mask:0xf bank_mask:0xf\n"
MF "v[76:79],v[100:103],v[180:183],v[76:79]\n"
LD "v8,s[36:39],s73 offen offset:256 nt lds\n"
MF "v[80:83],v[100:103],v[184:187],v[80:83]\n"
"s_min_u32 s13,s13,s0\n"
"v_readlane_b32 s11,v11,0\n"
"v_readlane_b32 s3,v11,24\n"
MF "v[84:87],v[100:103],v[188:191],v[84:87]\n"
"s_min_u32 s1,s1,s2\n"
"v_readlane_b32 s4,v11,32\n"
"v_readlane_b32 s5,v11,56\n"
MF "v[88:91],v[100:103],v[192:195],v[88:91]\n"
"s_waitcnt lgkmcnt(2)\n"
"ds_read_b128 v[100:103],v0 offset:6144\n"
"v_max_f32_dpp v12,|v12|,|v12| quad_perm:[1,0,3,2] row_mask:0xf bank_mask:0xf\n"
MF "v[72:75],v[104:107],v[196:199],v[72:75]\n"
"s_add_u32 m0,s65,73728\n"
"ds_write_b128 v32,a[0:3] offset:24576\n"
MF "v[76:79],v[104:107],v[200:203],v[76:79]\n"
LD "v8,s[36:39],s73 offen offset:3072 nt lds\n"
MF "v[80:83],v[104:107],v[204:207],v[80:83]\n"
"s_min_u32 s13,s13,s1\n"
"v_max_f32_dpp v12,v12,v12 quad_perm:[2,3,0,1] row_mask:0xf bank_mask:0xf\n"
AR "v62,a20\n"
MF "v[84:87],v[104:107],v[208:211],v[84:87]\n"
"s_and_b32 s13,s13,0x7f800000\n"
"ds_write_b128 v32,a[4:7] offset:28672\n"
MF "v[88:91],v[104:107],v[212:215],v[88:91]\n"
"s_waitcnt lgkmcnt(4)\n"
"ds_read_b128 v[104:107],v1 offset:6144\n"
"v_max_f32_dpp v12,v12,v12 row_half_mirror row_mask:0xf bank_mask:0xf\n"
MF "v[72:75],v[92:95],v[216:219],v[72:75]\n"
"s_add_u32 m0,s65,74624\n"
"v_readlane_b32 s10,v12,0\n"
"v_readlane_b32 s6,v12,24\n"
MF "v[76:79],v[92:95],v[220:223],v[76:79]\n"
LD "v8,s[36:39],s73 offen offset:3200 nt lds\n"
MF "v[80:83],v[92:95],v[224:227],v[80:83]\n"
"s_sub_u32 s13,s13,0x14000000\n"
"v_readlane_b32 s7,v12,32\n"
"v_readlane_b32 s8,v12,56\n"
MF "v[84:87],v[92:95],v[228:231],v[84:87]\n"
"s_cmp_lt_u32 s13,0x50000000\n"
"s_cselect_b32 s13,s13,0xed400000\n"
AR "v63,a21\n"
MF "v[88:91],v[92:95],v[232:235],v[88:91]\n"
"s_waitcnt lgkmcnt(4)\n"
"ds_read_b128 v[92:95],v0 offset:7168\n"
AR "v64,a22\n"
MF "v[72:75],v[96:99],v[236:239],v[72:75]\n"
"s_add_u32 m0,s65,75520\n"
"ds_write_b128 v32,a[8:11] offset:32768\n"
MF "v[76:79],v[96:99],v[240:243],v[76:79]\n"
LD "v8,s[36:39],s73 offen offset:3328 nt lds\n"
MF "v[80:83],v[96:99],v[244:247],v[80:83]\n"
"s_add_u32 s13,s13,0x12c00000\n"
"ds_write_b128 v32,a[12:15] offset:36864\n"
MF "v[84:87],v[96:99],v[248:251],v[84:87]\n"
"s_min_u32 s11,s11,s3\n"
"ds_write_b128 v32,a[16:19] offset:40960\n"
MF "v[88:91],v[96:99],v[252:255],v[88:91]\n"
"s_waitcnt lgkmcnt(7)\n"
"ds_read_b128 v[96:99],v1 offset:7168\n"
AR "v65,a23\n"
MF "v[112:115],v[100:103],v[136:139],0\n"
"s_min_u32 s4,s4,s5\n"
"ds_write_b128 v32,a[40:43] offset:45056\n"
MF "v[116:119],v[100:103],v[140:143],0\n"
"s_min_u32 s11,s11,s4\n"
"ds_write_b128 v32,a[44:47] offset:49152\n"
MF "v[120:123],v[100:103],v[144:147],0\n"
"s_and_b32 s11,s11,0x7f800000\n"
"ds_write_b128 v32,a[48:51] offset:53248\n"
MF "v[124:127],v[100:103],v[148:151],0\n"
"s_sub_u32 s11,s11,0x14000000\n"
"ds_write_b128 v32,a[52:55] offset:57344\n"
MF "v[128:131],v[100:103],v[152:155],0\n"
"s_waitcnt lgkmcnt(9)\n"
"ds_read_b128 v[100:103],v0 offset:8192\n"
AR "v66,a24\n"
MF "v[112:115],v[104:107],v[156:159],v[112:115]\n"
"s_min_u32 s10,s10,s6\n"
"v_max3_f32 v52,v72,v73,v74\n"
"v_max_f32 v52,v52,v75\n"
MF "v[116:119],v[104:107],v[160:163],v[116:119]\n"
"s_min_u32 s7,s7,s8\n"
"v_max3_f32 v53,v76,v77,v78\n"
"v_max_f32 v53,v53,v79\n"
MF "v[120:123],v[104:107],v[164:167],v[120:123]\n"
"s_min_u32 s10,s10,s7\n"
"v_max3_f32 v54,v80,v81,v82\n"
"v_max_f32 v54,v54,v83\n"
MF "v[124:127],v[104:107],v[168:171],v[124:127]\n"
"v_max3_f32 v55,v84,v85,v86\n"
"v_max_f32 v55,v55,v87\n"
MF "v[128:131],v[104:107],v[172:175],v[128:131]\n"
"s_waitcnt lgkmcnt(9)\n"
"ds_read_b128 v[104:107],v1 offset:8192\n"
"v_max3_f32 v56,v88,v89,v90\n"
MF "v[112:115],v[92:95],v[176:179],v[112:115]\n"
"s_cmp_lt_u32 s11,0x50000000\n"
"s_cselect_b32 s11,s11,0xed400000\n"
"v_max_f32 v56,v56,v91\n"
MF "v[116:119],v[92:95],v[180:183],v[116:119]\n"
"s_add_u32 s11,s11,0x12c00000\n"
"ds_write_b128 v32,a[56:59] offset:61440\n"
MF "v[120:123],v[92:95],v[184:187],v[120:123]\n"
"ds_write_b128 v33,a[80:83]\n"
MF "v[124:127],v[92:95],v[188:191],v[124:127]\n"
"ds_write_b128 v33,a[84:87] offset:4096\n"
MF "v[128:131],v[92:95],v[192:195],v[128:131]\n"
"s_waitcnt lgkmcnt(9)\n"
"ds_write_b128 v33,a[88:91] offset:8192\n"
MF "v[112:115],v[96:99],v[196:199],v[112:115]\n"
AR "v67,a25\n"
AR "v68,a26\n"
MF "v[116:119],v[96:99],v[200:203],v[116:119]\n"
"s_waitcnt lgkmcnt(0)\n"
"s_barrier\n"
MF "v[120:123],v[96:99],v[204:207],v[120:123]\n"
"s_add_u32 s72,s72,6144\n"
"ds_write_b128 v33,a[92:95] offset:12288\n"
MF "v[124:127],v[96:99],v[208:211],v[124:127]\n"
"s_cmp_eq_u32 s72,s17\n"
"s_cselect_b32 s72,s16,s72\n"
"s_add_u32 m0,s64,0\n"
MF "v[128:131],v[96:99],v[212:215],v[128:131]\n"
LD "v7,s[32:35],s72 offen nt lds\n"
MF "v[112:115],v[100:103],v[216:219],v[112:115]\n"
"s_add_u32 m0,s64,896\n"
"ds_write_b128 v33,a[96:99] offset:16384\n"
MF "v[116:119],v[100:103],v[220:223],v[116:119]\n"
LD "v7,s[32:35],s72 offen offset:128 nt lds\n"
MF "v[120:123],v[100:103],v[224:227],v[120:123]\n"
"s_add_u32 m0,s64,1792\n"
AR "v69,a27\n"
AR "v70,a28\n"
MF "v[124:127],v[100:103],v[228:231],v[124:127]\n"
LD "v7,s[32:35],s72 offen offset:256 nt lds\n"
MF "v[128:131],v[100:103],v[232:235],v[128:131]\n"
"s_add_u32 s72,s72,6144\n"
"s_add_u32 m0,s64,6144\n"
AR "v71,a29\n"
MF "v[112:115],v[104:107],v[236:239],v[112:115]\n"
LD "v7,s[32:35],s72 offen nt lds\n"
MF "v[116:119],v[104:107],v[240:243],v[116:119]\n"
"s_add_u32 m0,s64,7040\n"
"ds_write_b128 v33,a[120:123] offset:20480\n"
MF "v[120:123],v[104:107],v[244:247],v[120:123]\n"
LD "v7,s[32:35],s72 offen offset:128 nt lds\n"
MF "v[124:127],v[104:107],v[248:251],v[124:127]\n"
"s_add_u32 m0,s64,7936\n"
AR "v132,a30\n"
AR "v133,a31\n"
MF "v[128:131],v[104:107],v[252:255],v[128:131]\n"
LD "v7,s[32:35],s72 offen offset:256 nt lds\n"
AR "v134,a32\n"
AR "v135,a33\n"
AR "v6,a34\n"
AR "v13,a35\n"
AR "v14,a36\n"
AR "v15,a37\n"
"v_accvgpr_mov_b32 a254,a38\n"
"v_accvgpr_mov_b32 a255,a39\n"
"v_accvgpr_mov_b32 a240,a168\n"
"v_accvgpr_mov_b32 a241,a169\n"
"v_accvgpr_mov_b32 a242,a170\n"
"v_accvgpr_mov_b32 a243,a171\n"
"v_accvgpr_mov_b32 a244,a172\n"
"v_accvgpr_mov_b32 a245,a173\n"
"v_accvgpr_mov_b32 a246,a174\n"
"v_accvgpr_mov_b32 a247,a175\n"
"v_accvgpr_mov_b32 a248,a176\n"
"v_accvgpr_mov_b32 a249,a177\n"
"v_accvgpr_mov_b32 a250,a178\n"
"v_accvgpr_mov_b32 a251,a179\n"
".Lpgt0ex_b:\n"
"s_cbranch_execz .Lpg2a\n"
".Lt0ex:\n"
"s_nop 1\n"
"v_max3_f32 v52,v52,v112,v113\n"
"v_max3_f32 v53,v53,v116,v117\n"
"v_max3_f32 v54,v54,v120,v121\n"
"v_max3_f32 v55,v55,v124,v125\n"
"v_max3_f32 v56,v56,v128,v129\n"
"v_max3_f32 v52,v52,v114,v115\n"
"v_max3_f32 v53,v53,v118,v119\n"
"v_max3_f32 v54,v54,v122,v123\n"
"v_max3_f32 v55,v55,v126,v127\n"
"v_max3_f32 v56,v56,v130,v131\n"
"v_mov_b32 v57,v52\n"
"v_mov_b32 v58,v53\n"
"v_mov_b32 v59,v54\n"
"v_mov_b32 v60,v55\n"
"v_mov_b32 v61,v56\n"
"v_permlane16_swap_b32 v52,v57\n"
"v_permlane16_swap_b32 v53,v58\n"
"v_permlane16_swap_b32 v54,v59\n"
"v_permlane16_swap_b32 v55,v60\n"
"v_permlane16_swap_b32 v56,v61\n"
"v_max_f32 v52,v52,v57\n"
"v_max_f32 v53,v53,v58\n"
"v_max_f32 v54,v54,v59\n"
"v_max_f32 v55,v55,v60\n"
"v_max_f32 v56,v56,v61\n"
"v_mov_b32 v57,v52\n"
"v_mov_b32 v58,v53\n"
"v_mov_b32 v59,v54\n"
"v_mov_b32 v60,v55\n"
"v_mov_b32 v61,v56\n"
"v_permlane32_swap_b32 v52,v57\n"
"v_permlane32_swap_b32 v53,v58\n"
"v_permlane32_swap_b32 v54,v59\n"
"v_permlane32_swap_b32 v55,v60\n"
"v_permlane32_swap_b32 v56,v61\n"
"v_max_f32 v52,v52,v57\n"
"v_max_f32 v53,v53,v58\n"
"v_max_f32 v54,v54,v59\n"
"v_max_f32 v55,v55,v60\n"
"v_max_f32 v56,v56,v61\n"
"v_sub_f32 v32,0xc1300000,v52\n"
"v_sub_f32 v36,0xc1300000,v53\n"
"v_sub_f32 v40,0xc1300000,v54\n"
"v_sub_f32 v44,0xc1300000,v55\n"
"v_sub_f32 v48,0xc1300000,v56\n"
"v_mov_b32_e64 v33,v32\n"
"v_pk_mov_b32 v[34:35],v[32:33],v[32:33]\n"
"v_mov_b32 v37,v36\n"
"v_pk_mov_b32 v[38:39],v[36:37],v[36:37]\n"
"v_mov_b32 v41,v40\n"
"v_pk_mov_b32 v[42:43],v[40:41],v[40:41]\n"
"v_mov_b32 v45,v44\n"
"v_pk_mov_b32 v[46:47],v[44:45],v[44:45]\n"
"v_mov_b32 v49,v48\n"
"v_pk_mov_b32 v[50:51],v[48:49],v[48:49]\n"
"v_pk_add_f32 v[72:73],v[72:73],v[32:33]\n"
"v_pk_add_f32 v[74:75],v[74:75],v[32:33]\n"
"v_pk_add_f32 v[112:113],v[112:113],v[32:33]\n"
"v_pk_add_f32 v[114:115],v[114:115],v[32:33]\n"
"v_exp_f32 v72,v72\n"
"v_exp_f32 v73,v73\n"
"v_exp_f32 v74,v74\n"
"v_exp_f32 v75,v75\n"
"v_exp_f32 v112,v112\n"
"v_exp_f32 v113,v113\n"
"v_exp_f32 v114,v114\n"
"v_exp_f32 v115,v115\n"
"v_pk_add_f32 v[10:11],v[72:73],v[74:75]\n"
"v_pk_add_f32 v[28:29],v[112:113],v[114:115]\n"
"v_pk_add_f32 v[10:11],v[10:11],v[28:29]\n"
"v_add_f32_e64 v52,v10,v11\n"
CV "v72,v72,v73\n"
CV "v73,v74,v75\n"
CV "v74,v112,v113\n"
CV "v75,v114,v115\n"
"v_pk_add_f32 v[76:77],v[76:77],v[36:37]\n"
"v_pk_add_f32 v[78:79],v[78:79],v[36:37]\n"
"v_pk_add_f32 v[116:117],v[116:117],v[36:37]\n"
"v_pk_add_f32 v[118:119],v[118:119],v[36:37]\n"
"v_exp_f32 v76,v76\n"
"v_exp_f32 v77,v77\n"
"v_exp_f32 v78,v78\n"
"v_exp_f32 v79,v79\n"
"v_exp_f32 v116,v116\n"
"v_exp_f32 v117,v117\n"
"v_exp_f32 v118,v118\n"
"v_exp_f32 v119,v119\n"
"v_pk_add_f32 v[10:11],v[76:77],v[78:79]\n"
"v_pk_add_f32 v[28:29],v[116:117],v[118:119]\n"
"v_pk_add_f32 v[10:11],v[10:11],v[28:29]\n"
"v_add_f32_e64 v53,v10,v11\n"
CV "v76,v76,v77\n"
CV "v77,v78,v79\n"
CV "v78,v116,v117\n"
CV "v79,v118,v119\n"
"v_pk_add_f32 v[80:81],v[80:81],v[40:41]\n"
"v_pk_add_f32 v[82:83],v[82:83],v[40:41]\n"
"v_pk_add_f32 v[120:121],v[120:121],v[40:41]\n"
"v_pk_add_f32 v[122:123],v[122:123],v[40:41]\n"
"v_exp_f32 v80,v80\n"
"v_exp_f32 v81,v81\n"
"v_exp_f32 v82,v82\n"
"v_exp_f32 v83,v83\n"
"v_exp_f32 v120,v120\n"
"v_exp_f32 v121,v121\n"
"v_exp_f32 v122,v122\n"
"v_exp_f32 v123,v123\n"
"v_pk_add_f32 v[10:11],v[80:81],v[82:83]\n"
"v_pk_add_f32 v[28:29],v[120:121],v[122:123]\n"
"v_pk_add_f32 v[10:11],v[10:11],v[28:29]\n"
"v_add_f32_e64 v54,v10,v11\n"
CV "v80,v80,v81\n"
CV "v81,v82,v83\n"
CV "v82,v120,v121\n"
CV "v83,v122,v123\n"
"v_pk_add_f32 v[84:85],v[84:85],v[44:45]\n"
"v_pk_add_f32 v[86:87],v[86:87],v[44:45]\n"
"v_pk_add_f32 v[124:125],v[124:125],v[44:45]\n"
"v_pk_add_f32 v[126:127],v[126:127],v[44:45]\n"
"v_mov_b32_e64 v55,0\n"
"v_pk_add_f32 v[88:89],v[88:89],v[48:49]\n"
"v_pk_add_f32 v[90:91],v[90:91],v[48:49]\n"
"v_pk_add_f32 v[128:129],v[128:129],v[48:49]\n"
"v_pk_add_f32 v[130:131],v[130:131],v[48:49]\n"
"v_mov_b32 v56,0\n"
"s_waitcnt vmcnt(18) lgkmcnt(0)\n"
"s_barrier\n"
"ds_read_b128 v[16:19],v0 offset:24576\n"
"ds_read_b128 v[20:23],v1 offset:24576\n"
AW "a220,s13\n"
AW "a221,s13\n"
AW "a222,s13\n"
AW "a223,s13\n"
"s_waitcnt lgkmcnt(1)\n"
MF "v[92:95],v[16:19],v[136:139],v[32:35]\n"
"s_nop 0\n"
"ds_read_b128 v[24:27],v0 offset:25600\n"
"ds_read_b128 v[28:31],v1 offset:25600\n"
"s_waitcnt lgkmcnt(2)\n"
MF "v[92:95],v[20:23],v[156:159],v[92:95]\n"
"v_exp_f32 v84,v84\n"
MF "v[96:99],v[20:23],v[160:163],v[36:39]\n"
"v_exp_f32 v85,v85\n"
MF "v[96:99],v[16:19],v[140:143],v[96:99]\n"
"v_exp_f32 v86,v86\n"
MF "v[100:103],v[16:19],v[144:147],v[40:43]\n"
"v_exp_f32 v87,v87\n"
MF "v[100:103],v[20:23],v[164:167],v[100:103]\n"
"v_add_f32 v55,v55,v84\n"
"v_add_f32 v55,v55,v85\n"
MF "v[104:107],v[20:23],v[168:171],v[44:47]\n"
CV "v84,v84,v85\n"
"v_add_f32 v55,v55,v86\n"
MF "v[104:107],v[16:19],v[148:151],v[104:107]\n"
"v_add_f32 v55,v55,v87\n"
CV "v85,v86,v87\n"
MF "v[108:111],v[16:19],v[152:155],v[48:51]\n"
"v_exp_f32 v124,v124\n"
MF "v[108:111],v[20:23],v[172:175],v[108:111]\n"
"s_waitcnt lgkmcnt(0)\n"
"ds_read_b128 v[16:19],v0 offset:26624\n"
"ds_read_b128 v[20:23],v1 offset:26624\n"
MF "v[108:111],v[24:27],v[192:195],v[108:111]\n"
"v_exp_f32 v125,v125\n"
MF "v[108:111],v[28:31],v[212:215],v[108:111]\n"
"v_exp_f32 v126,v126\n"
MF "v[104:107],v[28:31],v[208:211],v[104:107]\n"
"v_exp_f32 v127,v127\n"
MF "v[104:107],v[24:27],v[188:191],v[104:107]\n"
"v_add_f32 v55,v55,v124\n"
"v_add_f32 v55,v55,v125\n"
MF "v[100:103],v[24:27],v[184:187],v[100:103]\n"
CV "v86,v124,v125\n"
"v_add_f32 v55,v55,v126\n"
MF "v[100:103],v[28:31],v[204:207],v[100:103]\n"
"v_add_f32 v55,v55,v127\n"
CV "v87,v126,v127\n"
MF "v[96:99],v[28:31],v[200:203],v[96:99]\n"
"v_exp_f32 v88,v88\n"
MF "v[96:99],v[24:27],v[180:183],v[96:99]\n"
"v_exp_f32 v89,v89\n"
MF "v[92:95],v[24:27],v[176:179],v[92:95]\n"
"v_exp_f32 v90,v90\n"
MF "v[92:95],v[28:31],v[196:199],v[92:95]\n"
"s_waitcnt lgkmcnt(0)\n"
"ds_read_b128 v[24:27],v0 offset:30720\n"
"ds_read_b128 v[28:31],v1 offset:30720\n"
MF "v[92:95],v[16:19],v[216:219],v[92:95]\n"
"v_exp_f32 v91,v91\n"
MF "v[92:95],v[20:23],v[236:239],v[92:95]\n"
"v_add_f32 v56,v56,v88\n"
"v_add_f32 v56,v56,v89\n"
MF "v[96:99],v[20:23],v[240:243],v[96:99]\n"
CV "v88,v88,v89\n"
"v_add_f32 v56,v56,v90\n"
MF "v[96:99],v[16:19],v[220:223],v[96:99]\n"
"v_add_f32 v56,v56,v91\n"
CV "v89,v90,v91\n"
MF "v[100:103],v[16:19],v[224:227],v[100:103]\n"
"v_exp_f32 v128,v128\n"
MF "v[100:103],v[20:23],v[244:247],v[100:103]\n"
"v_exp_f32 v129,v129\n"
MF "v[104:107],v[20:23],v[248:251],v[104:107]\n"
"v_exp_f32 v130,v130\n"
MF "v[104:107],v[16:19],v[228:231],v[104:107]\n"
"v_exp_f32 v131,v131\n"
MF "v[108:111],v[16:19],v[232:235],v[108:111]\n"
"v_add_f32 v56,v56,v128\n"
"v_add_f32 v56,v56,v129\n"
MF "v[108:111],v[20:23],v[252:255],v[108:111]\n"
"s_waitcnt lgkmcnt(0)\n"
"ds_read_b128 v[16:19],v0 offset:31744\n"
"ds_read_b128 v[20:23],v1 offset:31744\n"
MF "v[112:115],v[24:27],v[136:139],v[32:35]\n"
CV "v90,v128,v129\n"
"v_add_f32 v56,v56,v130\n"
MF "v[112:115],v[28:31],v[156:159],v[112:115]\n"
"v_add_f32 v56,v56,v131\n"
CV "v91,v130,v131\n"
MF "v[116:119],v[28:31],v[160:163],v[36:39]\n"
"v_exp_f32 v92,v92\n"
MF "v[116:119],v[24:27],v[140:143],v[116:119]\n"
"v_exp_f32 v93,v93\n"
MF "v[120:123],v[24:27],v[144:147],v[40:43]\n"
"v_exp_f32 v94,v94\n"
MF "v[120:123],v[28:31],v[164:167],v[120:123]\n"
"v_exp_f32 v95,v95\n"
MF "v[124:127],v[28:31],v[168:171],v[44:47]\n"
"v_add_f32 v57,v52,v92\n"
"v_add_f32 v57,v57,v93\n"
MF "v[124:127],v[24:27],v[148:151],v[124:127]\n"
CV "v92,v92,v93\n"
"v_add_f32 v57,v57,v94\n"
MF "v[128:131],v[24:27],v[152:155],v[48:51]\n"
"v_add_f32 v57,v57,v95\n"
CV "v93,v94,v95\n"
MF "v[128:131],v[28:31],v[172:175],v[128:131]\n"
"s_waitcnt lgkmcnt(0)\n"
"ds_read_b128 v[24:27],v0 offset:32768\n"
"ds_read_b128 v[28:31],v1 offset:32768\n"
MF "v[128:131],v[16:19],v[192:195],v[128:131]\n"
"v_exp_f32 v96,v96\n"
MF "v[128:131],v[20:23],v[212:215],v[128:131]\n"
"v_exp_f32 v97,v97\n"
MF "v[124:127],v[20:23],v[208:211],v[124:127]\n"
"v_exp_f32 v98,v98\n"
MF "v[124:127],v[16:19],v[188:191],v[124:127]\n"
"v_exp_f32 v99,v99\n"
MF "v[120:123],v[16:19],v[184:187],v[120:123]\n"
"v_add_f32 v58,v53,v96\n"
"v_add_f32 v58,v58,v97\n"
MF "v[120:123],v[20:23],v[204:207],v[120:123]\n"
CV "v96,v96,v97\n"
"v_add_f32 v58,v58,v98\n"
MF "v[116:119],v[20:23],v[200:203],v[116:119]\n"
"v_add_f32 v58,v58,v99\n"
CV "v97,v98,v99\n"
MF "v[116:119],v[16:19],v[180:183],v[116:119]\n"
"s_waitcnt vmcnt(12) lgkmcnt(0)\n"
"s_barrier\n"
MF "v[112:115],v[16:19],v[176:179],v[112:115]\n"
"v_exp_f32 v100,v100\n"
MF "v[112:115],v[20:23],v[196:199],v[112:115]\n"
"s_nop 0\n"
TR "v[16:17],v2\n"
TR "v[18:19],v2 offset:6144\n"
MF "v[112:115],v[24:27],v[216:219],v[112:115]\n"
"s_add_u32 s72,s72,6144\n"
TR "v[20:21],v3\n"
TR "v[22:23],v3 offset:6144\n"
MF "v[112:115],v[28:31],v[236:239],v[112:115]\n"
"s_cmp_eq_u32 s72,s17\n"
"s_cselect_b32 s72,s16,s72\n"
"s_add_u32 m0,s64,24576\n"
MF "v[116:119],v[28:31],v[240:243],v[116:119]\n"
LD "v7,s[32:35],s72 offen nt lds\n"
MF "v[116:119],v[24:27],v[220:223],v[116:119]\n"
"s_add_u32 m0,s64,25472\n"
"v_exp_f32 v101,v101\n"
MF "v[120:123],v[24:27],v[224:227],v[120:123]\n"
LD "v7,s[32:35],s72 offen offset:128 nt lds\n"
MF "v[120:123],v[28:31],v[244:247],v[120:123]\n"
"v_exp_f32 v102,v102\n"
MF "v[124:127],v[28:31],v[248:251],v[124:127]\n"
"s_add_u32 m0,s64,26368\n"
"v_exp_f32 v103,v103\n"
MF "v[124:127],v[24:27],v[228:231],v[124:127]\n"
LD "v7,s[32:35],s72 offen offset:256 nt lds\n"
MF "v[128:131],v[24:27],v[232:235],v[128:131]\n"
"s_add_u32 s72,s72,6144\n"
"v_add_f32 v59,v54,v100\n"
"v_add_f32 v59,v59,v101\n"
MF "v[128:131],v[28:31],v[252:255],v[128:131]\n"
"s_waitcnt lgkmcnt(0)\n"
TR "v[24:25],v4\n"
TR "v[26:27],v4 offset:6144\n"
MF "a[0:3],v[16:19],v[72:75],a[220:223]\n"
"s_add_u32 m0,s64,30720\n"
TR "v[28:29],v5\n"
TR "v[30:31],v5 offset:6144\n"
MF "a[20:23],v[20:23],v[72:75],a[220:223]\n"
LD "v7,s[32:35],s72 offen nt lds\n"
MF "a[24:27],v[20:23],v[76:79],a[220:223]\n"
CV "v100,v100,v101\n"
"v_add_f32 v59,v59,v102\n"
MF "a[4:7],v[16:19],v[76:79],a[220:223]\n"
"s_add_u32 m0,s64,31616\n"
"v_add_f32 v59,v59,v103\n"
CV "v101,v102,v103\n"
MF "a[8:11],v[16:19],v[80:83],a[220:223]\n"
LD "v7,s[32:35],s72 offen offset:128 nt lds\n"
MF "a[28:31],v[20:23],v[80:83],a[220:223]\n"
"v_exp_f32 v104,v104\n"
MF "a[32:35],v[20:23],v[84:87],a[220:223]\n"
"s_add_u32 m0,s64,32512\n"
"v_exp_f32 v105,v105\n"
MF "a[12:15],v[16:19],v[84:87],a[220:223]\n"
LD "v7,s[32:35],s72 offen offset:256 nt lds\n"
MF "a[16:19],v[16:19],v[88:91],a[220:223]\n"
"v_exp_f32 v106,v106\n"
MF "a[36:39],v[20:23],v[88:91],a[220:223]\n"
"s_waitcnt lgkmcnt(0)\n"
TR "v[16:17],v2 offset:1024\n"
TR "v[18:19],v2 offset:7168\n"
MF "a[56:59],v[24:27],v[88:91],a[220:223]\n"
"s_nop 0\n"
TR "v[20:21],v3 offset:1024\n"
TR "v[22:23],v3 offset:7168\n"
MF "a[76:79],v[28:31],v[88:91],a[220:223]\n"
"v_exp_f32 v107,v107\n"
MF "a[72:75],v[28:31],v[84:87],a[220:223]\n"
"v_add_f32 v60,v55,v104\n"
"v_add_f32 v60,v60,v105\n"
MF "a[52:55],v[24:27],v[84:87],a[220:223]\n"
CV "v104,v104,v105\n"
"v_add_f32 v60,v60,v106\n"
MF "a[48:51],v[24:27],v[80:83],a[220:223]\n"
"v_add_f32 v60,v60,v107\n"
CV "v105,v106,v107\n"
MF "a[68:71],v[28:31],v[80:83],a[220:223]\n"
"v_exp_f32 v108,v108\n"
MF "a[64:67],v[28:31],v[76:79],a[220:223]\n"
"v_exp_f32 v109,v109\n"
MF "a[44:47],v[24:27],v[76:79],a[220:223]\n"
"v_exp_f32 v110,v110\n"
MF "a[40:43],v[24:27],v[72:75],a[220:223]\n"
"v_exp_f32 v111,v111\n"
MF "a[60:63],v[28:31],v[72:75],a[220:223]\n"
"s_waitcnt lgkmcnt(0)\n"
TR "v[24:25],v4 offset:1024\n"
TR "v[26:27],v4 offset:7168\n"
MF "a[80:83],v[16:19],v[72:75],a[220:223]\n"
".Lpg10b:\n"
"s_cbranch_execz .Lpg1a\n"
".Lpg10d:\n"
"s_cbranch_execz .Lisr2\n"
"s_nop 0\n"
TR "v[28:29],v5 offset:1024\n"
TR "v[30:31],v5 offset:7168\n"
MF "a[100:103],v[20:23],v[72:75],a[220:223]\n"
"v_add_f32 v61,v56,v108\n"
"v_add_f32 v61,v61,v109\n"
MF "a[104:107],v[20:23],v[76:79],a[220:223]\n"
CV "v108,v108,v109\n"
"v_add_f32 v61,v61,v110\n"
MF "a[84:87],v[16:19],v[76:79],a[220:223]\n"
"v_add_f32 v61,v61,v111\n"
CV "v109,v110,v111\n"
MF "a[88:91],v[16:19],v[80:83],a[220:223]\n"
"v_exp_f32 v112,v112\n"
MF "a[108:111],v[20:23],v[80:83],a[220:223]\n"
"v_exp_f32 v113,v113\n"
MF "a[112:115],v[20:23],v[84:87],a[220:223]\n"
"v_exp_f32 v114,v114\n"
MF "a[92:95],v[16:19],v[84:87],a[220:223]\n"
"v_exp_f32 v115,v115\n"
MF "a[96:99],v[16:19],v[88:91],a[220:223]\n"
"v_add_f32 v57,v57,v112\n"
"v_add_f32 v57,v57,v113\n"
MF "a[116:119],v[20:23],v[88:91],a[220:223]\n"
"s_waitcnt lgkmcnt(0)\n"
TR "v[16:17],v2 offset:2048\n"
TR "v[18:19],v2 offset:8192\n"
MF "a[136:139],v[24:27],v[88:91],a[220:223]\n"
"s_nop 0\n"
TR "v[20:21],v3 offset:2048\n"
TR "v[22:23],v3 offset:8192\n"
MF "a[156:159],v[28:31],v[88:91],a[220:223]\n"
CV "v94,v112,v113\n"
"v_add_f32 v57,v57,v114\n"
MF "a[152:155],v[28:31],v[84:87],a[220:223]\n"
"v_add_f32 v57,v57,v115\n"
CV "v95,v114,v115\n"
MF "a[132:135],v[24:27],v[84:87],a[220:223]\n"
"v_exp_f32 v116,v116\n"
MF "a[128:131],v[24:27],v[80:83],a[220:223]\n"
"v_exp_f32 v117,v117\n"
MF "a[148:151],v[28:31],v[80:83],a[220:223]\n"
"v_exp_f32 v118,v118\n"
MF "a[144:147],v[28:31],v[76:79],a[220:223]\n"
"v_exp_f32 v119,v119\n"
MF "a[124:127],v[24:27],v[76:79],a[220:223]\n"
"v_add_f32 v58,v58,v116\n"
"v_add_f32 v58,v58,v117\n"
MF "a[120:123],v[24:27],v[72:75],a[220:223]\n"
CV "v98,v116,v117\n"
"v_add_f32 v58,v58,v118\n"
MF "a[140:143],v[28:31],v[72:75],a[220:223]\n"
"s_waitcnt lgkmcnt(0)\n"
TR "v[24:25],v4 offset:2048\n"
TR "v[26:27],v4 offset:8192\n"
MF "a[160:163],v[16:19],v[72:75],a[220:223]\n"
"s_nop 0\n"
TR "v[28:29],v5 offset:2048\n"
TR "v[30:31],v5 offset:8192\n"
MF "a[180:183],v[20:23],v[72:75],a[220:223]\n"
"v_add_f32 v58,v58,v119\n"
CV "v99,v118,v119\n"
MF "a[184:187],v[20:23],v[76:79],a[220:223]\n"
"v_exp_f32 v120,v120\n"
MF "a[164:167],v[16:19],v[76:79],a[220:223]\n"
"v_exp_f32 v121,v121\n"
MF "a[168:171],v[16:19],v[80:83],a[220:223]\n"
"v_exp_f32 v122,v122\n"
MF "a[188:191],v[20:23],v[80:83],a[220:223]\n"
"v_exp_f32 v123,v123\n"
MF "a[192:195],v[20:23],v[84:87],a[220:223]\n"
"v_add_f32 v59,v59,v120\n"
"v_add_f32 v59,v59,v121\n"
MF "a[172:175],v[16:19],v[84:87],a[220:223]\n"
CV "v102,v120,v121\n"
"v_add_f32 v59,v59,v122\n"
MF "a[176:179],v[16:19],v[88:91],a[220:223]\n"
"s_waitcnt vmcnt(6) lgkmcnt(0)\n"
"s_barrier\n"
MF "a[196:199],v[20:23],v[88:91],a[220:223]\n"
"s_add_u32 s73,s73,12288\n"
"ds_read_b128 v[16:19],v0\n"
"ds_read_b128 v[20:23],v1\n"
MF "a[216:219],v[24:27],v[88:91],a[220:223]\n"
"s_cmp_eq_u32 s73,s19\n"
"s_cselect_b32 s73,s18,s73\n"
"s_add_u32 m0,s65,49152\n"
MF "a[236:239],v[28:31],v[88:91],a[220:223]\n"
LD "v8,s[36:39],s73 offen nt lds\n"
MF "a[232:235],v[28:31],v[84:87],a[220:223]\n"
"s_add_u32 m0,s65,50048\n"
"v_add_f32 v59,v59,v123\n"
CV "v103,v122,v123\n"
MF "a[212:215],v[24:27],v[84:87],a[220:223]\n"
LD "v8,s[36:39],s73 offen offset:128 nt lds\n"
MF "a[208:211],v[24:27],v[80:83],a[220:223]\n"
"s_add_u32 m0,s65,50944\n"
MF "a[228:231],v[28:31],v[80:83],a[220:223]\n"
LD "v8,s[36:39],s73 offen offset:256 nt lds\n"
MF "a[224:227],v[28:31],v[76:79],a[220:223]\n"
MF "a[204:207],v[24:27],v[76:79],a[220:223]\n"
MF "a[200:203],v[24:27],v[72:75],a[220:223]\n"
MF "a[220:223],v[28:31],v[72:75],a[220:223]\n"
"s_waitcnt lgkmcnt(0)\n"
".Laloopx:\n"
MF "v[72:75],v[16:19],v[136:139],v[32:35]\n"
"s_nop 0\n"
"ds_read_b128 v[24:27],v0 offset:1024\n"
"ds_read_b128 v[28:31],v1 offset:1024\n"
MF "v[72:75],v[20:23],v[156:159],v[72:75]\n"
MF "v[76:79],v[20:23],v[160:163],v[36:39]\n"
MF "v[76:79],v[16:19],v[140:143],v[76:79]\n"
MF "v[80:83],v[16:19],v[144:147],v[40:43]\n"
MF "v[80:83],v[20:23],v[164:167],v[80:83]\n"
MF "v[84:87],v[20:23],v[168:171],v[44:47]\n"
MF "v[84:87],v[16:19],v[148:151],v[84:87]\n"
"s_add_u32 m0,s65,49152\n"
MF "v[88:91],v[16:19],v[152:155],v[48:51]\n"
LD "v8,s[36:39],s73 offen offset:3072 nt lds\n"
MF "v[88:91],v[20:23],v[172:175],v[88:91]\n"
"s_waitcnt lgkmcnt(0)\n"
"ds_read_b128 v[16:19],v0 offset:2048\n"
"ds_read_b128 v[20:23],v1 offset:2048\n"
MF "v[88:91],v[24:27],v[192:195],v[88:91]\n"
"v_exp_f32 v124,v124\n"
MF "v[88:91],v[28:31],v[212:215],v[88:91]\n"
"s_add_u32 m0,s65,50048\n"
"v_exp_f32 v125,v125\n"
MF "v[84:87],v[28:31],v[208:211],v[84:87]\n"
LD "v8,s[36:39],s73 offen offset:3200 nt lds\n"
MF "v[84:87],v[24:27],v[188:191],v[84:87]\n"
"v_exp_f32 v126,v126\n"
MF "v[80:83],v[24:27],v[184:187],v[80:83]\n"
"v_exp_f32 v127,v127\n"
MF "v[80:83],v[28:31],v[204:207],v[80:83]\n"
"s_add_u32 m0,s65,50944\n"
"v_add_f32 v60,v60,v124\n"
"v_add_f32 v60,v60,v125\n"
MF "v[76:79],v[28:31],v[200:203],v[76:79]\n"
LD "v8,s[36:39],s73 offen offset:3328 nt lds\n"
MF "v[76:79],v[24:27],v[180:183],v[76:79]\n"
CV "v106,v124,v125\n"
"v_add_f32 v60,v60,v126\n"
MF "v[72:75],v[24:27],v[176:179],v[72:75]\n"
"v_add_f32 v60,v60,v127\n"
CV "v107,v126,v127\n"
MF "v[72:75],v[28:31],v[196:199],v[72:75]\n"
"s_waitcnt lgkmcnt(0)\n"
"ds_read_b128 v[24:27],v0 offset:6144\n"
"ds_read_b128 v[28:31],v1 offset:6144\n"
MF "v[72:75],v[16:19],v[216:219],v[72:75]\n"
"v_exp_f32 v128,v128\n"
MF "v[72:75],v[20:23],v[236:239],v[72:75]\n"
"v_exp_f32 v129,v129\n"
MF "v[76:79],v[20:23],v[240:243],v[76:79]\n"
"v_exp_f32 v130,v130\n"
MF "v[76:79],v[16:19],v[220:223],v[76:79]\n"
"v_exp_f32 v131,v131\n"
MF "v[80:83],v[16:19],v[224:227],v[80:83]\n"
"v_add_f32 v61,v61,v128\n"
"v_add_f32 v61,v61,v129\n"
MF "v[80:83],v[20:23],v[244:247],v[80:83]\n"
CV "v110,v128,v129\n"
"v_add_f32 v61,v61,v130\n"
MF "v[84:87],v[20:23],v[248:251],v[84:87]\n"
"v_add_f32 v61,v61,v131\n"
CV "v111,v130,v131\n"
MF "v[84:87],v[16:19],v[228:231],v[84:87]\n"
"v_max3_f32 v10,v57,v58,v59\n"
"v_max3_f32 v10,v10,v60,v61\n"
MF "v[88:91],v[16:19],v[232:235],v[88:91]\n"
"v_cmp_ngt_f32 vcc,s95,v10\n"
MF "v[88:91],v[20:23],v[252:255],v[88:91]\n"
"s_waitcnt lgkmcnt(0)\n"
"s_cbranch_vccnz .Lstub_6\n"
".Lback_6:\n"
MF "v[112:115],v[24:27],v[136:139],v[32:35]\n"
"s_add_u32 s74,s74,12288\n"
"ds_read_b128 v[16:19],v0 offset:7168\n"
"ds_read_b128 v[20:23],v1 offset:7168\n"
MF "v[112:115],v[28:31],v[156:159],v[112:115]\n"
"s_cmp_eq_u32 s74,s20\n"
"s_cselect_b32 s74,s70,s74\n"
MF "v[116:119],v[28:31],v[160:163],v[36:39]\n"
"v_exp_f32 v72,v72\n"
MF "v[116:119],v[24:27],v[140:143],v[116:119]\n"
"v_exp_f32 v73,v73\n"
MF "v[120:123],v[24:27],v[144:147],v[40:43]\n"
"v_exp_f32 v74,v74\n"
MF "v[120:123],v[28:31],v[164:167],v[120:123]\n"
"v_exp_f32 v75,v75\n"
MF "v[124:127],v[28:31],v[168:171],v[44:47]\n"
"v_add_f32 v52,v57,v72\n"
"v_add_f32 v52,v52,v73\n"
MF "v[124:127],v[24:27],v[148:151],v[124:127]\n"
CV "v72,v72,v73\n"
"v_add_f32 v52,v52,v74\n"
MF "v[128:131],v[24:27],v[152:155],v[48:51]\n"
"v_add_f32 v52,v52,v75\n"
CV "v73,v74,v75\n"
MF "v[128:131],v[28:31],v[172:175],v[128:131]\n"
"s_waitcnt lgkmcnt(0)\n"
"ds_read_b128 v[24:27],v0 offset:8192\n"
"ds_read_b128 v[28:31],v1 offset:8192\n"
MF "v[128:131],v[16:19],v[192:195],v[128:131]\n"
"v_exp_f32 v76,v76\n"
MF "v[128:131],v[20:23],v[212:215],v[128:131]\n"
"v_exp_f32 v77,v77\n"
MF "v[124:127],v[20:23],v[208:211],v[124:127]\n"
"v_exp_f32 v78,v78\n"
MF "v[124:127],v[16:19],v[188:191],v[124:127]\n"
"v_exp_f32 v79,v79\n"
MF "v[120:123],v[16:19],v[184:187],v[120:123]\n"
"v_add_f32 v53,v58,v76\n"
"v_add_f32 v53,v53,v77\n"
MF "v[120:123],v[20:23],v[204:207],v[120:123]\n"
CV "v76,v76,v77\n"
"v_add_f32 v53,v53,v78\n"
MF "v[116:119],v[20:23],v[200:203],v[116:119]\n"
"v_add_f32 v53,v53,v79\n"
CV "v77,v78,v79\n"
MF "v[116:119],v[16:19],v[180:183],v[116:119]\n"
"s_waitcnt vmcnt(12) lgkmcnt(0)\n"
"s_barrier\n"
MF "v[112:115],v[16:19],v[176:179],v[112:115]\n"
"v_exp_f32 v80,v80\n"
MF "v[112:115],v[20:23],v[196:199],v[112:115]\n"
"s_nop 0\n"
TR "v[16:17],v2 offset:24576\n"
TR "v[18:19],v2 offset:30720\n"
MF "v[112:115],v[24:27],v[216:219],v[112:115]\n"
"s_add_u32 s72,s72,6144\n"
TR "v[20:21],v3 offset:24576\n"
TR "v[22:23],v3 offset:30720\n"
MF "v[112:115],v[28:31],v[236:239],v[112:115]\n"
"s_cmp_eq_u32 s72,s17\n"
"s_cselect_b32 s72,s16,s72\n"
"s_add_u32 m0,s64,0\n"
MF "v[116:119],v[28:31],v[240:243],v[116:119]\n"
LD "v7,s[32:35],s72 offen nt lds\n"
MF "v[116:119],v[24:27],v[220:223],v[116:119]\n"
"s_add_u32 m0,s64,896\n"
"v_exp_f32 v81,v81\n"
MF "v[120:123],v[24:27],v[224:227],v[120:123]\n"
LD "v7,s[32:35],s72 offen offset:128 nt lds\n"
MF "v[120:123],v[28:31],v[244:247],v[120:123]\n"
"v_exp_f32 v82,v82\n"
MF "v[124:127],v[28:31],v[248:251],v[124:127]\n"
"s_add_u32 m0,s64,1792\n"
"v_exp_f32 v83,v83\n"
MF "v[124:127],v[24:27],v[228:231],v[124:127]\n"
LD "v7,s[32:35],s72 offen offset:256 nt lds\n"
MF "v[128:131],v[24:27],v[232:235],v[128:131]\n"
"s_add_u32 s72,s72,6144\n"
"v_add_f32 v54,v59,v80\n"
"v_add_f32 v54,v54,v81\n"
MF "v[128:131],v[28:31],v[252:255],v[128:131]\n"
"s_waitcnt lgkmcnt(0)\n"
TR "v[24:25],v4 offset:24576\n"
TR "v[26:27],v4 offset:30720\n"
MF "a[0:3],v[16:19],v[92:95],a[0:3]\n"
"s_add_u32 m0,s64,6144\n"
TR "v[28:29],v5 offset:24576\n"
TR "v[30:31],v5 offset:30720\n"
MF "a[20:23],v[20:23],v[92:95],a[20:23]\n"
LD "v7,s[32:35],s72 offen nt lds\n"
MF "a[24:27],v[20:23],v[96:99],a[24:27]\n"
CV "v80,v80,v81\n"
"v_add_f32 v54,v54,v82\n"
MF "a[4:7],v[16:19],v[96:99],a[4:7]\n"
"s_add_u32 m0,s64,7040\n"
"v_add_f32 v54,v54,v83\n"
CV "v81,v82,v83\n"
MF "a[8:11],v[16:19],v[100:103],a[8:11]\n"
LD "v7,s[32:35],s72 offen offset:128 nt lds\n"
MF "a[28:31],v[20:23],v[100:103],a[28:31]\n"
"v_exp_f32 v84,v84\n"
MF "a[32:35],v[20:23],v[104:107],a[32:35]\n"
"s_add_u32 m0,s64,7936\n"
"v_exp_f32 v85,v85\n"
MF "a[12:15],v[16:19],v[104:107],a[12:15]\n"
LD "v7,s[32:35],s72 offen offset:256 nt lds\n"
MF "a[16:19],v[16:19],v[108:111],a[16:19]\n"
"v_exp_f32 v86,v86\n"
MF "a[36:39],v[20:23],v[108:111],a[36:39]\n"
"s_waitcnt lgkmcnt(0)\n"
TR "v[16:17],v2 offset:25600\n"
TR "v[18:19],v2 offset:31744\n"
MF "a[56:59],v[24:27],v[108:111],a[56:59]\n"
"s_nop 0\n"
TR "v[20:21],v3 offset:25600\n"
TR "v[22:23],v3 offset:31744\n"
MF "a[76:79],v[28:31],v[108:111],a[76:79]\n"
"v_exp_f32 v87,v87\n"
MF "a[72:75],v[28:31],v[104:107],a[72:75]\n"
"v_add_f32 v55,v60,v84\n"
"v_add_f32 v55,v55,v85\n"
MF "a[52:55],v[24:27],v[104:107],a[52:55]\n"
CV "v84,v84,v85\n"
"v_add_f32 v55,v55,v86\n"
MF "a[48:51],v[24:27],v[100:103],a[48:51]\n"
"v_add_f32 v55,v55,v87\n"
CV "v85,v86,v87\n"
MF "a[68:71],v[28:31],v[100:103],a[68:71]\n"
"v_exp_f32 v88,v88\n"
MF "a[64:67],v[28:31],v[96:99],a[64:67]\n"
"v_exp_f32 v89,v89\n"
MF "a[44:47],v[24:27],v[96:99],a[44:47]\n"
"v_exp_f32 v90,v90\n"
MF "a[40:43],v[24:27],v[92:95],a[40:43]\n"
"v_exp_f32 v91,v91\n"
MF "a[60:63],v[28:31],v[92:95],a[60:63]\n"
"s_waitcnt lgkmcnt(0)\n"
TR "v[24:25],v4 offset:25600\n"
TR "v[26:27],v4 offset:31744\n"
MF "a[80:83],v[16:19],v[92:95],a[80:83]\n"
"s_nop 0\n"
TR "v[28:29],v5 offset:25600\n"
TR "v[30:31],v5 offset:31744\n"
MF "a[100:103],v[20:23],v[92:95],a[100:103]\n"
"v_add_f32 v56,v61,v88\n"
"v_add_f32 v56,v56,v89\n"
MF "a[104:107],v[20:23],v[96:99],a[104:107]\n"
CV "v88,v88,v89\n"
"v_add_f32 v56,v56,v90\n"
MF "a[84:87],v[16:19],v[96:99],a[84:87]\n"
"v_add_f32 v56,v56,v91\n"
CV "v89,v90,v91\n"
MF "a[88:91],v[16:19],v[100:103],a[88:91]\n"
"v_exp_f32 v112,v112\n"
MF "a[108:111],v[20:23],v[100:103],a[108:111]\n"
"v_exp_f32 v113,v113\n"
MF "a[112:115],v[20:23],v[104:107],a[112:115]\n"
"v_exp_f32 v114,v114\n"
MF "a[92:95],v[16:19],v[104:107],a[92:95]\n"
"v_exp_f32 v115,v115\n"
MF "a[96:99],v[16:19],v[108:111],a[96:99]\n"
"v_add_f32 v52,v52,v112\n"
"v_add_f32 v52,v52,v113\n"
MF "a[116:119],v[20:23],v[108:111],a[116:119]\n"
"s_waitcnt lgkmcnt(0)\n"
TR "v[16:17],v2 offset:26624\n"
TR "v[18:19],v2 offset:32768\n"
MF "a[136:139],v[24:27],v[108:111],a[136:139]\n"
"s_nop 0\n"
TR "v[20:21],v3 offset:26624\n"
TR "v[22:23],v3 offset:32768\n"
MF "a[156:159],v[28:31],v[108:111],a[156:159]\n"
CV "v74,v112,v113\n"
"v_add_f32 v52,v52,v114\n"
MF "a[152:155],v[28:31],v[104:107],a[152:155]\n"
"v_add_f32 v52,v52,v115\n"
CV "v75,v114,v115\n"
MF "a[132:135],v[24:27],v[104:107],a[132:135]\n"
"v_exp_f32 v116,v116\n"
MF "a[128:131],v[24:27],v[100:103],a[128:131]\n"
"v_exp_f32 v117,v117\n"
MF "a[148:151],v[28:31],v[100:103],a[148:151]\n"
"v_exp_f32 v118,v118\n"
MF "a[144:147],v[28:31],v[96:99],a[144:147]\n"
"v_exp_f32 v119,v119\n"
MF "a[124:127],v[24:27],v[96:99],a[124:127]\n"
"v_add_f32 v53,v53,v116\n"
"v_add_f32 v53,v53,v117\n"
MF "a[120:123],v[24:27],v[92:95],a[120:123]\n"
CV "v78,v116,v117\n"
"v_add_f32 v53,v53,v118\n"
MF "a[140:143],v[28:31],v[92:95],a[140:143]\n"
"s_waitcnt lgkmcnt(0)\n"
TR "v[24:25],v4 offset:26624\n"
TR "v[26:27],v4 offset:32768\n"
MF "a[160:163],v[16:19],v[92:95],a[160:163]\n"
"s_nop 0\n"
TR "v[28:29],v5 offset:26624\n"
TR "v[30:31],v5 offset:32768\n"
MF "a[180:183],v[20:23],v[92:95],a[180:183]\n"
"v_add_f32 v53,v53,v119\n"
CV "v79,v118,v119\n"
MF "a[184:187],v[20:23],v[96:99],a[184:187]\n"
"v_exp_f32 v120,v120\n"
MF "a[164:167],v[16:19],v[96:99],a[164:167]\n"
"v_exp_f32 v121,v121\n"
MF "a[168:171],v[16:19],v[100:103],a[168:171]\n"
"v_exp_f32 v122,v122\n"
MF "a[188:191],v[20:23],v[100:103],a[188:191]\n"
"v_exp_f32 v123,v123\n"
MF "a[192:195],v[20:23],v[104:107],a[192:195]\n"
"v_add_f32 v54,v54,v120\n"
"v_add_f32 v54,v54,v121\n"
MF "a[172:175],v[16:19],v[104:107],a[172:175]\n"
CV "v82,v120,v121\n"
"v_add_f32 v54,v54,v122\n"
MF "a[176:179],v[16:19],v[108:111],a[176:179]\n"
"s_waitcnt vmcnt(12) lgkmcnt(0)\n"
"s_barrier\n"
MF "a[196:199],v[20:23],v[108:111],a[196:199]\n"
"s_add_u32 s73,s73,12288\n"
"ds_read_b128 v[16:19],v0 offset:24576\n"
"ds_read_b128 v[20:23],v1 offset:24576\n"
MF "a[216:219],v[24:27],v[108:111],a[216:219]\n"
"s_cmp_eq_u32 s73,s19\n"
"s_cselect_b32 s73,s18,s73\n"
"s_add_u32 m0,s65,73728\n"
MF "a[236:239],v[28:31],v[108:111],a[236:239]\n"
LD "v8,s[36:39],s73 offen nt lds\n"
MF "a[232:235],v[28:31],v[104:107],a[232:235]\n"
"s_add_u32 m0,s65,74624\n"
"v_add_f32 v54,v54,v123\n"
CV "v83,v122,v123\n"
MF "a[212:215],v[24:27],v[104:107],a[212:215]\n"
LD "v8,s[36:39],s73 offen offset:128 nt lds\n"
MF "a[208:211],v[24:27],v[100:103],a[208:211]\n"
"s_add_u32 m0,s65,75520\n"
MF "a[228:231],v[28:31],v[100:103],a[228:231]\n"
LD "v8,s[36:39],s73 offen offset:256 nt lds\n"
MF "a[224:227],v[28:31],v[96:99],a[224:227]\n"
MF "a[204:207],v[24:27],v[96:99],a[204:207]\n"
MF "a[200:203],v[24:27],v[92:95],a[200:203]\n"
MF "a[220:223],v[28:31],v[92:95],a[220:223]\n"
"s_waitcnt lgkmcnt(0)\n"
MF "v[92:95],v[16:19],v[136:139],v[32:35]\n"
"s_add_u32 m0,s65,73728\n"
"ds_read_b128 v[24:27],v0 offset:25600\n"
"ds_read_b128 v[28:31],v1 offset:25600\n"
MF "v[92:95],v[20:23],v[156:159],v[92:95]\n"
LD "v8,s[36:39],s73 offen offset:3072 nt lds\n"
MF "v[96:99],v[20:23],v[160:163],v[36:39]\n"
"v_exp_f32 v124,v124\n"
MF "v[96:99],v[16:19],v[140:143],v[96:99]\n"
"v_exp_f32 v125,v125\n"
MF "v[100:103],v[16:19],v[144:147],v[40:43]\n"
"s_add_u32 m0,s65,74624\n"
"v_exp_f32 v126,v126\n"
MF "v[100:103],v[20:23],v[164:167],v[100:103]\n"
LD "v8,s[36:39],s73 offen offset:3200 nt lds\n"
MF "v[104:107],v[20:23],v[168:171],v[44:47]\n"
"v_exp_f32 v127,v127\n"
MF "v[104:107],v[16:19],v[148:151],v[104:107]\n"
"s_add_u32 m0,s65,75520\n"
"v_add_f32 v55,v55,v124\n"
"v_add_f32 v55,v55,v125\n"
MF "v[108:111],v[16:19],v[152:155],v[48:51]\n"
LD "v8,s[36:39],s73 offen offset:3328 nt lds\n"
MF "v[108:111],v[20:23],v[172:175],v[108:111]\n"
"s_waitcnt lgkmcnt(0)\n"
"ds_read_b128 v[16:19],v0 offset:26624\n"
"ds_read_b128 v[20:23],v1 offset:26624\n"
MF "v[108:111],v[24:27],v[192:195],v[108:111]\n"
CV "v86,v124,v125\n"
"v_add_f32 v55,v55,v126\n"
MF "v[108:111],v[28:31],v[212:215],v[108:111]\n"
"v_add_f32 v55,v55,v127\n"
CV "v87,v126,v127\n"
MF "v[104:107],v[28:31],v[208:211],v[104:107]\n"
"v_exp_f32 v128,v128\n"
MF "v[104:107],v[24:27],v[188:191],v[104:107]\n"
"v_exp_f32 v129,v129\n"
MF "v[100:103],v[24:27],v[184:187],v[100:103]\n"
"v_exp_f32 v130,v130\n"
MF "v[100:103],v[28:31],v[204:207],v[100:103]\n"
"v_exp_f32 v131,v131\n"
MF "v[96:99],v[28:31],v[200:203],v[96:99]\n"
"v_add_f32 v56,v56,v128\n"
"v_add_f32 v56,v56,v129\n"
MF "v[96:99],v[24:27],v[180:183],v[96:99]\n"
CV "v90,v128,v129\n"
"v_add_f32 v56,v56,v130\n"
MF "v[92:95],v[24:27],v[176:179],v[92:95]\n"
"v_add_f32 v56,v56,v131\n"
CV "v91,v130,v131\n"
MF "v[92:95],v[28:31],v[196:199],v[92:95]\n"
"s_waitcnt lgkmcnt(0)\n"
"ds_read_b128 v[24:27],v0 offset:30720\n"
"ds_read_b128 v[28:31],v1 offset:30720\n"
MF "v[92:95],v[16:19],v[216:219],v[92:95]\n"
MF "v[92:95],v[20:23],v[236:239],v[92:95]\n"
MF "v[96:99],v[20:23],v[240:243],v[96:99]\n"
MF "v[96:99],v[16:19],v[220:223],v[96:99]\n"
MF "v[100:103],v[16:19],v[224:227],v[100:103]\n"
MF "v[100:103],v[20:23],v[244:247],v[100:103]\n"
MF "v[104:107],v[20:23],v[248:251],v[104:107]\n"
MF "v[104:107],v[16:19],v[228:231],v[104:107]\n"
"v_max3_f32 v10,v52,v53,v54\n"
"v_max3_f32 v10,v10,v55,v56\n"
MF "v[108:111],v[16:19],v[232:235],v[108:111]\n"
"v_cmp_ngt_f32 vcc,s95,v10\n"
MF "v[108:111],v[20:23],v[252:255],v[108:111]\n"
"s_waitcnt lgkmcnt(0)\n"
"s_cbranch_vccnz .Lstub_7\n"
".Lback_7:\n"
MF "v[112:115],v[24:27],v[136:139],v[32:35]\n"
"s_add_u32 s74,s74,12288\n"
"ds_read_b128 v[16:19],v0 offset:31744\n"
"ds_read_b128 v[20:23],v1 offset:31744\n"
MF "v[112:115],v[28:31],v[156:159],v[112:115]\n"
"s_cmp_eq_u32 s74,s20\n"
"s_cselect_b32 s74,s70,s74\n"
MF "v[116:119],v[28:31],v[160:163],v[36:39]\n"
"v_exp_f32 v92,v92\n"
MF "v[116:119],v[24:27],v[140:143],v[116:119]\n"
"v_exp_f32 v93,v93\n"
MF "v[120:123],v[24:27],v[144:147],v[40:43]\n"
"v_exp_f32 v94,v94\n"
MF "v[120:123],v[28:31],v[164:167],v[120:123]\n"
"v_exp_f32 v95,v95\n"
MF "v[124:127],v[28:31],v[168:171],v[44:47]\n"
"v_add_f32 v57,v52,v92\n"
"v_add_f32 v57,v57,v93\n"
MF "v[124:127],v[24:27],v[148:151],v[124:127]\n"
CV "v92,v92,v93\n"
"v_add_f32 v57,v57,v94\n"
MF "v[128:131],v[24:27],v[152:155],v[48:51]\n"
"v_add_f32 v57,v57,v95\n"
CV "v93,v94,v95\n"
MF "v[128:131],v[28:31],v[172:175],v[128:131]\n"
"s_waitcnt lgkmcnt(0)\n"
"ds_read_b128 v[24:27],v0 offset:32768\n"
"ds_read_b128 v[28:31],v1 offset:32768\n"
MF "v[128:131],v[16:19],v[192:195],v[128:131]\n"
"s_sub_u32 s61,s61,1\n"
"v_exp_f32 v96,v96\n"
MF "v[128:131],v[20:23],v[212:215],v[128:131]\n"
"s_cmp_eq_u32 s61,1\n"
"s_cselect_b32 s95,0,s95\n"
"v_add_f32 v58,v53,v96\n"
MF "v[124:127],v[20:23],v[208:211],v[124:127]\n"
"v_exp_f32 v97,v97\n"
MF "v[124:127],v[16:19],v[188:191],v[124:127]\n"
"v_exp_f32 v98,v98\n"
MF "v[120:123],v[16:19],v[184:187],v[120:123]\n"
"v_exp_f32 v99,v99\n"
MF "v[120:123],v[20:23],v[204:207],v[120:123]\n"
"v_add_f32 v58,v58,v97\n"
CV "v96,v96,v97\n"
MF "v[116:119],v[20:23],v[200:203],v[116:119]\n"
"v_add_f32 v58,v58,v98\n"
"v_add_f32 v58,v58,v99\n"
MF "v[116:119],v[16:19],v[180:183],v[116:119]\n"
"s_waitcnt vmcnt(12) lgkmcnt(0)\n"
"s_barrier\n"
MF "v[112:115],v[16:19],v[176:179],v[112:115]\n"
CV "v97,v98,v99\n"
MF "v[112:115],v[20:23],v[196:199],v[112:115]\n"
"s_nop 0\n"
TR "v[16:17],v2\n"
TR "v[18:19],v2 offset:6144\n"
MF "v[112:115],v[24:27],v[216:219],v[112:115]\n"
"s_add_u32 s72,s72,6144\n"
TR "v[20:21],v3\n"
TR "v[22:23],v3 offset:6144\n"
MF "v[112:115],v[28:31],v[236:239],v[112:115]\n"
"s_cmp_eq_u32 s72,s17\n"
"s_cselect_b32 s72,s16,s72\n"
"s_add_u32 m0,s64,24576\n"
MF "v[116:119],v[28:31],v[240:243],v[116:119]\n"
LD "v7,s[32:35],s72 offen nt lds\n"
MF "v[116:119],v[24:27],v[220:223],v[116:119]\n"
"s_add_u32 m0,s64,25472\n"
"v_exp_f32 v100,v100\n"
MF "v[120:123],v[24:27],v[224:227],v[120:123]\n"
LD "v7,s[32:35],s72 offen offset:128 nt lds\n"
MF "v[120:123],v[28:31],v[244:247],v[120:123]\n"
"v_exp_f32 v101,v101\n"
MF "v[124:127],v[28:31],v[248:251],v[124:127]\n"
"s_add_u32 m0,s64,26368\n"
"v_exp_f32 v102,v102\n"
MF "v[124:127],v[24:27],v[228:231],v[124:127]\n"
LD "v7,s[32:35],s72 offen offset:256 nt lds\n"
MF "v[128:131],v[24:27],v[232:235],v[128:131]\n"
"s_add_u32 s72,s72,6144\n"
"v_exp_f32 v103,v103\n"
MF "v[128:131],v[28:31],v[252:255],v[128:131]\n"
"s_waitcnt lgkmcnt(0)\n"
TR "v[24:25],v4\n"
TR "v[26:27],v4 offset:6144\n"
MF "a[0:3],v[16:19],v[72:75],a[0:3]\n"
"s_add_u32 m0,s64,30720\n"
TR "v[28:29],v5\n"
TR "v[30:31],v5 offset:6144\n"
MF "a[20:23],v[20:23],v[72:75],a[20:23]\n"
LD "v7,s[32:35],s72 offen nt lds\n"
MF "a[24:27],v[20:23],v[76:79],a[24:27]\n"
"v_add_f32 v59,v54,v100\n"
"v_add_f32 v59,v59,v101\n"
MF "a[4:7],v[16:19],v[76:79],a[4:7]\n"
"s_add_u32 m0,s64,31616\n"
CV "v100,v100,v101\n"
"v_add_f32 v59,v59,v102\n"
MF "a[8:11],v[16:19],v[80:83],a[8:11]\n"
LD "v7,s[32:35],s72 offen offset:128 nt lds\n"
MF "a[28:31],v[20:23],v[80:83],a[28:31]\n"
"v_add_f32 v59,v59,v103\n"
CV "v101,v102,v103\n"
MF "a[32:35],v[20:23],v[84:87],a[32:35]\n"
"s_add_u32 m0,s64,32512\n"
"v_exp_f32 v104,v104\n"
MF "a[12:15],v[16:19],v[84:87],a[12:15]\n"
LD "v7,s[32:35],s72 offen offset:256 nt lds\n"
MF "a[16:19],v[16:19],v[88:91],a[16:19]\n"
"v_exp_f32 v105,v105\n"
MF "a[36:39],v[20:23],v[88:91],a[36:39]\n"
"s_waitcnt lgkmcnt(0)\n"
TR "v[16:17],v2 offset:1024\n"
TR "v[18:19],v2 offset:7168\n"
MF "a[56:59],v[24:27],v[88:91],a[56:59]\n"
"s_nop 0\n"
TR "v[20:21],v3 offset:1024\n"
TR "v[22:23],v3 offset:7168\n"
MF "a[76:79],v[28:31],v[88:91],a[76:79]\n"
"v_exp_f32 v106,v106\n"
MF "a[72:75],v[28:31],v[84:87],a[72:75]\n"
"v_exp_f32 v107,v107\n"
MF "a[52:55],v[24:27],v[84:87],a[52:55]\n"
"v_add_f32 v60,v55,v104\n"
"v_add_f32 v60,v60,v105\n"
MF "a[48:51],v[24:27],v[80:83],a[48:51]\n"
CV "v104,v104,v105\n"
"v_add_f32 v60,v60,v106\n"
MF "a[68:71],v[28:31],v[80:83],a[68:71]\n"
"v_add_f32 v60,v60,v107\n"
CV "v105,v106,v107\n"
MF "a[64:67],v[28:31],v[76:79],a[64:67]\n"
".Lpg11b:\n"
"s_cbranch_execz .Lpgqj_a\n"
".Lpg11d:\n"
"s_cbranch_execz .Lisr2\n"
"v_exp_f32 v108,v108\n"
MF "a[44:47],v[24:27],v[76:79],a[44:47]\n"
"v_exp_f32 v109,v109\n"
MF "a[40:43],v[24:27],v[72:75],a[40:43]\n"
"v_exp_f32 v110,v110\n"
MF "a[60:63],v[28:31],v[72:75],a[60:63]\n"
"s_waitcnt lgkmcnt(0)\n"
TR "v[24:25],v4 offset:1024\n"
TR "v[26:27],v4 offset:7168\n"
MF "a[80:83],v[16:19],v[72:75],a[80:83]\n"
"s_nop 0\n"
TR "v[28:29],v5 offset:1024\n"
TR "v[30:31],v5 offset:7168\n"
MF "a[100:103],v[20:23],v[72:75],a[100:103]\n"
"v_exp_f32 v111,v111\n"
MF "a[104:107],v[20:23],v[76:79],a[104:107]\n"
"v_add_f32 v61,v56,v108\n"
"v_add_f32 v61,v61,v109\n"
MF "a[84:87],v[16:19],v[76:79],a[84:87]\n"
CV "v108,v108,v109\n"
"v_add_f32 v61,v61,v110\n"
MF "a[88:91],v[16:19],v[80:83],a[88:91]\n"
"v_add_f32 v61,v61,v111\n"
CV "v109,v110,v111\n"
MF "a[108:111],v[20:23],v[80:83],a[108:111]\n"
"v_exp_f32 v112,v112\n"
MF "a[112:115],v[20:23],v[84:87],a[112:115]\n"
"v_exp_f32 v113,v113\n"
MF "a[92:95],v[16:19],v[84:87],a[92:95]\n"
"v_exp_f32 v114,v114\n"
MF "a[96:99],v[16:19],v[88:91],a[96:99]\n"
"v_exp_f32 v115,v115\n"
MF "a[116:119],v[20:23],v[88:91],a[116:119]\n"
"s_waitcnt lgkmcnt(0)\n"
TR "v[16:17],v2 offset:2048\n"
TR "v[18:19],v2 offset:8192\n"
MF "a[136:139],v[24:27],v[88:91],a[136:139]\n"
"s_nop 0\n"
TR "v[20:21],v3 offset:2048\n"
TR "v[22:23],v3 offset:8192\n"
MF "a[156:159],v[28:31],v[88:91],a[156:159]\n"
"v_add_f32 v57,v57,v112\n"
"v_add_f32 v57,v57,v113\n"
MF "a[152:155],v[28:31],v[84:87],a[152:155]\n"
CV "v94,v112,v113\n"
"v_add_f32 v57,v57,v114\n"
MF "a[132:135],v[24:27],v[84:87],a[132:135]\n"
"v_add_f32 v57,v57,v115\n"
CV "v95,v114,v115\n"
MF "a[128:131],v[24:27],v[80:83],a[128:131]\n"
"v_exp_f32 v116,v116\n"
MF "a[148:151],v[28:31],v[80:83],a[148:151]\n"
"v_exp_f32 v117,v117\n"
MF "a[144:147],v[28:31],v[76:79],a[144:147]\n"
"v_exp_f32 v118,v118\n"
MF "a[124:127],v[24:27],v[76:79],a[124:127]\n"
"v_exp_f32 v119,v119\n"
MF "a[120:123],v[24:27],v[72:75],a[120:123]\n"
"v_add_f32 v58,v58,v116\n"
"v_add_f32 v58,v58,v117\n"
MF "a[140:143],v[28:31],v[72:75],a[140:143]\n"
"s_waitcnt lgkmcnt(0)\n"
TR "v[24:25],v4 offset:2048\n"
TR "v[26:27],v4 offset:8192\n"
MF "a[160:163],v[16:19],v[72:75],a[160:163]\n"
"s_nop 0\n"
TR "v[28:29],v5 offset:2048\n"
TR "v[30:31],v5 offset:8192\n"
MF "a[180:183],v[20:23],v[72:75],a[180:183]\n"
CV "v98,v116,v117\n"
"v_add_f32 v58,v58,v118\n"
MF "a[184:187],v[20:23],v[76:79],a[184:187]\n"
"v_add_f32 v58,v58,v119\n"
CV "v99,v118,v119\n"
MF "a[164:167],v[16:19],v[76:79],a[164:167]\n"
"v_exp_f32 v120,v120\n"
MF "a[168:171],v[16:19],v[80:83],a[168:171]\n"
"v_exp_f32 v121,v121\n"
MF "a[188:191],v[20:23],v[80:83],a[188:191]\n"
"v_exp_f32 v122,v122\n"
MF "a[192:195],v[20:23],v[84:87],a[192:195]\n"
"v_exp_f32 v123,v123\n"
MF "a[172:175],v[16:19],v[84:87],a[172:175]\n"
"v_add_f32 v59,v59,v120\n"
"v_add_f32 v59,v59,v121\n"
MF "a[176:179],v[16:19],v[88:91],a[176:179]\n"
"s_waitcnt vmcnt(12) lgkmcnt(0)\n"
"s_barrier\n"
MF "a[196:199],v[20:23],v[88:91],a[196:199]\n"
"s_add_u32 s73,s73,12288\n"
"ds_read_b128 v[16:19],v0\n"
"ds_read_b128 v[20:23],v1\n"
MF "a[216:219],v[24:27],v[88:91],a[216:219]\n"
"s_cmp_eq_u32 s73,s19\n"
"s_cselect_b32 s73,s18,s73\n"
"s_add_u32 m0,s65,49152\n"
MF "a[236:239],v[28:31],v[88:91],a[236:239]\n"
LD "v8,s[36:39],s73 offen nt lds\n"
MF "a[232:235],v[28:31],v[84:87],a[232:235]\n"
"s_add_u32 m0,s65,50048\n"
CV "v102,v120,v121\n"
"v_add_f32 v59,v59,v122\n"
MF "a[212:215],v[24:27],v[84:87],a[212:215]\n"
LD "v8,s[36:39],s73 offen offset:128 nt lds\n"
MF "a[208:211],v[24:27],v[80:83],a[208:211]\n"
"s_add_u32 m0,s65,50944\n"
"v_add_f32 v59,v59,v123\n"
CV "v103,v122,v123\n"
MF "a[228:231],v[28:31],v[80:83],a[228:231]\n"
LD "v8,s[36:39],s73 offen offset:256 nt lds\n"
MF "a[224:227],v[28:31],v[76:79],a[224:227]\n"
MF "a[204:207],v[24:27],v[76:79],a[204:207]\n"
"s_waitcnt lgkmcnt(0)\n"
MF "a[200:203],v[24:27],v[72:75],a[200:203]\n"
"s_cmp_lg_u32 s61,0\n"
MF "a[220:223],v[28:31],v[72:75],a[220:223]\n"
"s_cbranch_scc1 .Laloopx\n"
".Lepi1:\n"
"v_exp_f32 v124,v124\n"
"v_exp_f32 v125,v125\n"
"v_exp_f32 v126,v126\n"
"v_exp_f32 v127,v127\n"
"v_exp_f32 v128,v128\n"
"v_exp_f32 v129,v129\n"
"v_exp_f32 v130,v130\n"
"v_exp_f32 v131,v131\n"
"v_add_f32 v60,v60,v124\n"
"v_add_f32 v61,v61,v128\n"
"v_add_f32 v60,v60,v125\n"
"v_add_f32 v61,v61,v129\n"
"v_add_f32 v60,v60,v126\n"
"v_add_f32 v61,v61,v130\n"
"v_add_f32 v60,v60,v127\n"
"v_add_f32 v61,v61,v131\n"
CV "v106,v124,v125\n"
CV "v107,v126,v127\n"
CV "v110,v128,v129\n"
CV "v111,v130,v131\n"
"v_max3_f32 v10,v57,v58,v59\n"
"v_max3_f32 v10,v10,v60,v61\n"
"v_cmp_le_f32 vcc,0x47800000,v10\n"
"s_cbranch_vccnz .Lstub_8\n"
".Lback_8:\n"
"s_waitcnt vmcnt(9) lgkmcnt(0)\n"
"s_barrier\n"
"s_cmp_eq_u32 s57,0\n"
"s_cbranch_scc0 .Lep\n"
".Lepx:\n"
"s_add_u32 s66,s67,4718592\n"
"s_sub_u32 s81,s81,384\n"
"s_mov_b32 s83,7077888\n"
AR "v122,a243\n"
"v_add_u32 v123,97280,v122\n"
"v_add_u32 v124,194560,v122\n"
"v_add_u32 v125,291840,v122\n"
"v_mov_b32 v10,1.0\n"
TR "v[136:137],v2 offset:24576\n"
TR "v[138:139],v2 offset:30720\n"
"v_mfma_f32_16x16x4_f32 v[16:19],v10,v57,0\n"
TR "v[140:141],v3 offset:24576\n"
TR "v[142:143],v3 offset:30720\n"
TR "v[144:145],v4 offset:24576\n"
TR "v[146:147],v4 offset:30720\n"
TR "v[148:149],v5 offset:24576\n"
TR "v[150:151],v5 offset:30720\n"
"v_mfma_f32_16x16x4_f32 v[20:23],v10,v58,0\n"
TR "v[152:153],v2 offset:25600\n"
TR "v[154:155],v2 offset:31744\n"
TR "v[156:157],v3 offset:25600\n"
TR "v[158:159],v3 offset:31744\n"
TR "v[160:161],v4 offset:25600\n"
TR "v[162:163],v4 offset:31744\n"
"v_mfma_f32_16x16x4_f32 v[24:27],v10,v59,0\n"
TR "v[164:165],v5 offset:25600\n"
TR "v[166:167],v5 offset:31744\n"
TR "v[168:169],v2 offset:26624\n"
TR "v[170:171],v2 offset:32768\n"
TR "v[172:173],v3 offset:26624\n"
TR "v[174:175],v3 offset:32768\n"
"v_mfma_f32_16x16x4_f32 v[28:31],v10,v60,0\n"
TR "v[176:177],v4 offset:26624\n"
TR "v[178:179],v4 offset:32768\n"
TR "v[180:181],v5 offset:26624\n"
TR "v[182:183],v5 offset:32768\n"
"v_mfma_f32_16x16x4_f32 v[32:35],v10,v61,0\n"
"s_add_u32 m0,s62,0\n"
"s_add_u32 s82,s66,2359296\n"
"s_sub_u32 s15,s82,7864320\n"
"s_min_u32 s82,s82,s15\n"
LD "v122,s[28:31],s82 offen lds\n"
LD "v123,s[28:31],s82 offen offset:1024 lds\n"
"s_waitcnt lgkmcnt(15)\n"
MF "a[0:3],v[136:139],v[92:95],a[0:3]\n"
MF "a[20:23],v[140:143],v[92:95],a[20:23]\n"
LD "v124,s[28:31],s82 offen offset:2048 lds\n"
LD "v125,s[28:31],s82 offen offset:3072 lds\n"
"v_rcp_f32 v57,v16\n"
"v_rcp_f32 v58,v20\n"
"v_rcp_f32 v59,v24\n"
"v_rcp_f32 v60,v28\n"
"v_rcp_f32 v61,v32\n"
MF "a[4:7],v[136:139],v[96:99],a[4:7]\n"
"v_mul_f32_e64 v52,-s13,v57\n"
"v_mul_f32_e64 v53,-s13,v58\n"
"v_mul_f32_e64 v54,-s13,v59\n"
"v_mul_f32_e64 v55,-s13,v60\n"
"v_mul_f32_e64 v56,-s13,v61\n"
AR "v72,a0\n"
AR "v73,a1\n"
AR "v74,a2\n"
MF "a[24:27],v[140:143],v[96:99],a[24:27]\n"
AR "v75,a3\n"
AR "v76,a20\n"
AR "v77,a21\n"
AR "v78,a22\n"
AR "v79,a23\n"
"v_pk_fma_f32 v[72:73],v[72:73],v[56:57],v[52:53] op_sel:[0,1,0] op_sel_hi:[1,1,0]\n"
"v_pk_fma_f32 v[74:75],v[74:75],v[56:57],v[52:53] op_sel:[0,1,0] op_sel_hi:[1,1,0]\n"
"v_pk_fma_f32 v[76:77],v[76:77],v[56:57],v[52:53] op_sel:[0,1,0] op_sel_hi:[1,1,0]\n"
"v_pk_fma_f32 v[78:79],v[78:79],v[56:57],v[52:53] op_sel:[0,1,0] op_sel_hi:[1,1,0]\n"
CV "v112,v72,v73\n"
CV "v113,v74,v75\n"
CV "v114,v76,v77\n"
CV "v115,v78,v79\n"
MF "a[8:11],v[136:139],v[100:103],a[8:11]\n"
AW "a0,v112\n"
AW "a1,v113\n"
AW "a2,v114\n"
AW "a3,v115\n"
"s_add_u32 m0,s62,16384\n"
"s_add_u32 s82,s66,2359424\n"
"s_sub_u32 s15,s82,7864320\n"
"s_min_u32 s82,s82,s15\n"
"s_nop 0\n"
LD "v122,s[28:31],s82 offen lds\n"
AR "v80,a4\n"
AR "v81,a5\n"
AR "v82,a6\n"
MF "a[28:31],v[140:143],v[100:103],a[28:31]\n"
AR "v83,a7\n"
AR "v84,a24\n"
AR "v85,a25\n"
AR "v86,a26\n"
AR "v87,a27\n"
"v_pk_fma_f32 v[80:81],v[80:81],v[58:59],v[52:53] op_sel:[0,0,1] op_sel_hi:[1,0,1]\n"
"v_pk_fma_f32 v[82:83],v[82:83],v[58:59],v[52:53] op_sel:[0,0,1] op_sel_hi:[1,0,1]\n"
"v_pk_fma_f32 v[84:85],v[84:85],v[58:59],v[52:53] op_sel:[0,0,1] op_sel_hi:[1,0,1]\n"
"v_pk_fma_f32 v[86:87],v[86:87],v[58:59],v[52:53] op_sel:[0,0,1] op_sel_hi:[1,0,1]\n"
CV "v116,v80,v81\n"
CV "v117,v82,v83\n"
CV "v118,v84,v85\n"
CV "v119,v86,v87\n"
MF "a[12:15],v[136:139],v[104:107],a[12:15]\n"
AW "a4,v116\n"
AW "a5,v117\n"
AW "a6,v118\n"
AW "a7,v119\n"
LD "v123,s[28:31],s82 offen offset:1024 lds\n"
AR "v72,a8\n"
AR "v73,a9\n"
AR "v74,a10\n"
MF "a[32:35],v[140:143],v[104:107],a[32:35]\n"
AR "v75,a11\n"
AR "v76,a28\n"
AR "v77,a29\n"
AR "v78,a30\n"
AR "v79,a31\n"
"v_pk_fma_f32 v[72:73],v[72:73],v[58:59],v[54:55] op_sel:[0,1,0] op_sel_hi:[1,1,0]\n"
"v_pk_fma_f32 v[74:75],v[74:75],v[58:59],v[54:55] op_sel:[0,1,0] op_sel_hi:[1,1,0]\n"
"v_pk_fma_f32 v[76:77],v[76:77],v[58:59],v[54:55] op_sel:[0,1,0] op_sel_hi:[1,1,0]\n"
"v_pk_fma_f32 v[78:79],v[78:79],v[58:59],v[54:55] op_sel:[0,1,0] op_sel_hi:[1,1,0]\n"
CV "v112,v72,v73\n"
CV "v113,v74,v75\n"
CV "v114,v76,v77\n"
CV "v115,v78,v79\n"
MF "a[16:19],v[136:139],v[108:111],a[16:19]\n"
AW "a8,v112\n"
AW "a9,v113\n"
AW "a10,v114\n"
AW "a11,v115\n"
"s_waitcnt lgkmcnt(0)\n"
"s_barrier\n"
AR "v80,a12\n"
AR "v81,a13\n"
AR "v82,a14\n"
MF "a[36:39],v[140:143],v[108:111],a[36:39]\n"
AR "v83,a15\n"
AR "v84,a32\n"
AR "v85,a33\n"
AR "v86,a34\n"
AR "v87,a35\n"
"v_pk_fma_f32 v[80:81],v[80:81],v[60:61],v[54:55] op_sel:[0,0,1] op_sel_hi:[1,0,1]\n"
"v_pk_fma_f32 v[82:83],v[82:83],v[60:61],v[54:55] op_sel:[0,0,1] op_sel_hi:[1,0,1]\n"
"v_pk_fma_f32 v[84:85],v[84:85],v[60:61],v[54:55] op_sel:[0,0,1] op_sel_hi:[1,0,1]\n"
"v_pk_fma_f32 v[86:87],v[86:87],v[60:61],v[54:55] op_sel:[0,0,1] op_sel_hi:[1,0,1]\n"
CV "v116,v80,v81\n"
CV "v117,v82,v83\n"
CV "v118,v84,v85\n"
CV "v119,v86,v87\n"
MF "a[40:43],v[144:147],v[92:95],a[40:43]\n"
AW "a12,v116\n"
AW "a13,v117\n"
AW "a14,v118\n"
AW "a15,v119\n"
LD "v124,s[28:31],s82 offen offset:2048 lds\n"
AR "v72,a16\n"
AR "v73,a17\n"
AR "v74,a18\n"
MF "a[60:63],v[148:151],v[92:95],a[60:63]\n"
AR "v75,a19\n"
AR "v76,a36\n"
AR "v77,a37\n"
AR "v78,a38\n"
AR "v79,a39\n"
"v_pk_fma_f32 v[72:73],v[72:73],v[60:61],v[56:57] op_sel:[0,1,0] op_sel_hi:[1,1,0]\n"
"v_pk_fma_f32 v[74:75],v[74:75],v[60:61],v[56:57] op_sel:[0,1,0] op_sel_hi:[1,1,0]\n"
"v_pk_fma_f32 v[76:77],v[76:77],v[60:61],v[56:57] op_sel:[0,1,0] op_sel_hi:[1,1,0]\n"
"v_pk_fma_f32 v[78:79],v[78:79],v[60:61],v[56:57] op_sel:[0,1,0] op_sel_hi:[1,1,0]\n"
CV "v112,v72,v73\n"
CV "v113,v74,v75\n"
CV "v114,v76,v77\n"
CV "v115,v78,v79\n"
MF "a[44:47],v[144:147],v[96:99],a[44:47]\n"
AW "a16,v112\n"
AW "a17,v113\n"
AW "a18,v114\n"
AW "a19,v115\n"
LD "v125,s[28:31],s82 offen offset:3072 lds\n"
AR "v80,a40\n"
AR "v81,a41\n"
AR "v82,a42\n"
MF "a[64:67],v[148:151],v[96:99],a[64:67]\n"
AR "v83,a43\n"
AR "v84,a60\n"
AR "v85,a61\n"
AR "v86,a62\n"
AR "v87,a63\n"
"v_pk_fma_f32 v[80:81],v[80:81],v[56:57],v[52:53] op_sel:[0,1,0] op_sel_hi:[1,1,0]\n"
"v_pk_fma_f32 v[82:83],v[82:83],v[56:57],v[52:53] op_sel:[0,1,0] op_sel_hi:[1,1,0]\n"
"v_pk_fma_f32 v[84:85],v[84:85],v[56:57],v[52:53] op_sel:[0,1,0] op_sel_hi:[1,1,0]\n"
"v_pk_fma_f32 v[86:87],v[86:87],v[56:57],v[52:53] op_sel:[0,1,0] op_sel_hi:[1,1,0]\n"
CV "v116,v80,v81\n"
CV "v117,v82,v83\n"
CV "v118,v84,v85\n"
CV "v119,v86,v87\n"
MF "a[48:51],v[144:147],v[100:103],a[48:51]\n"
AW "a40,v116\n"
AW "a41,v117\n"
AW "a42,v118\n"
AW "a43,v119\n"
AR "v72,a44\n"
AR "v73,a45\n"
AR "v74,a46\n"
MF "a[68:71],v[148:151],v[100:103],a[68:71]\n"
AR "v75,a47\n"
AR "v76,a64\n"
AR "v77,a65\n"
AR "v78,a66\n"
AR "v79,a67\n"
"v_pk_fma_f32 v[72:73],v[72:73],v[58:59],v[52:53] op_sel:[0,0,1] op_sel_hi:[1,0,1]\n"
"v_pk_fma_f32 v[74:75],v[74:75],v[58:59],v[52:53] op_sel:[0,0,1] op_sel_hi:[1,0,1]\n"
"v_pk_fma_f32 v[76:77],v[76:77],v[58:59],v[52:53] op_sel:[0,0,1] op_sel_hi:[1,0,1]\n"
"v_pk_fma_f32 v[78:79],v[78:79],v[58:59],v[52:53] op_sel:[0,0,1] op_sel_hi:[1,0,1]\n"
CV "v112,v72,v73\n"
CV "v113,v74,v75\n"
CV "v114,v76,v77\n"
CV "v115,v78,v79\n"
MF "a[52:55],v[144:147],v[104:107],a[52:55]\n"
AW "a44,v112\n"
AW "a45,v113\n"
AW "a46,v114\n"
AW "a47,v115\n"
"s_add_u32 m0,s62,32768\n"
"s_add_u32 s82,s66,2359552\n"
"s_sub_u32 s15,s82,7864320\n"
"s_min_u32 s82,s82,s15\n"
"s_nop 0\n"
LD "v122,s[28:31],s82 offen lds\n"
AR "v80,a48\n"
AR "v81,a49\n"
AR "v82,a50\n"
MF "a[72:75],v[148:151],v[104:107],a[72:75]\n"
AR "v83,a51\n"
AR "v84,a68\n"
AR "v85,a69\n"
AR "v86,a70\n"
AR "v87,a71\n"
"v_pk_fma_f32 v[80:81],v[80:81],v[58:59],v[54:55] op_sel:[0,1,0] op_sel_hi:[1,1,0]\n"
"v_pk_fma_f32 v[82:83],v[82:83],v[58:59],v[54:55] op_sel:[0,1,0] op_sel_hi:[1,1,0]\n"
"v_pk_fma_f32 v[84:85],v[84:85],v[58:59],v[54:55] op_sel:[0,1,0] op_sel_hi:[1,1,0]\n"
"v_pk_fma_f32 v[86:87],v[86:87],v[58:59],v[54:55] op_sel:[0,1,0] op_sel_hi:[1,1,0]\n"
CV "v116,v80,v81\n"
CV "v117,v82,v83\n"
CV "v118,v84,v85\n"
CV "v119,v86,v87\n"
MF "a[56:59],v[144:147],v[108:111],a[56:59]\n"
AW "a48,v116\n"
AW "a49,v117\n"
AW "a50,v118\n"
AW "a51,v119\n"
LD "v123,s[28:31],s82 offen offset:1024 lds\n"
AR "v72,a52\n"
AR "v73,a53\n"
AR "v74,a54\n"
MF "a[76:79],v[148:151],v[108:111],a[76:79]\n"
AR "v75,a55\n"
AR "v76,a72\n"
AR "v77,a73\n"
AR "v78,a74\n"
AR "v79,a75\n"
"v_pk_fma_f32 v[72:73],v[72:73],v[60:61],v[54:55] op_sel:[0,0,1] op_sel_hi:[1,0,1]\n"
"v_pk_fma_f32 v[74:75],v[74:75],v[60:61],v[54:55] op_sel:[0,0,1] op_sel_hi:[1,0,1]\n"
"v_pk_fma_f32 v[76:77],v[76:77],v[60:61],v[54:55] op_sel:[0,0,1] op_sel_hi:[1,0,1]\n"
"v_pk_fma_f32 v[78:79],v[78:79],v[60:61],v[54:55] op_sel:[0,0,1] op_sel_hi:[1,0,1]\n"
CV "v112,v72,v73\n"
CV "v113,v74,v75\n"
CV "v114,v76,v77\n"
CV "v115,v78,v79\n"
MF "a[80:83],v[152:155],v[92:95],a[80:83]\n"
AW "a52,v112\n"
AW "a53,v113\n"
AW "a54,v114\n"
AW "a55,v115\n"
AR "v80,a56\n"
AR "v81,a57\n"
AR "v82,a58\n"
MF "a[100:103],v[156:159],v[92:95],a[100:103]\n"
AR "v83,a59\n"
AR "v84,a76\n"
AR "v85,a77\n"
AR "v86,a78\n"
AR "v87,a79\n"
"v_pk_fma_f32 v[80:81],v[80:81],v[60:61],v[56:57] op_sel:[0,1,0] op_sel_hi:[1,1,0]\n"
"v_pk_fma_f32 v[82:83],v[82:83],v[60:61],v[56:57] op_sel:[0,1,0] op_sel_hi:[1,1,0]\n"
"v_pk_fma_f32 v[84:85],v[84:85],v[60:61],v[56:57] op_sel:[0,1,0] op_sel_hi:[1,1,0]\n"
"v_pk_fma_f32 v[86:87],v[86:87],v[60:61],v[56:57] op_sel:[0,1,0] op_sel_hi:[1,1,0]\n"
CV "v116,v80,v81\n"
CV "v117,v82,v83\n"
CV "v118,v84,v85\n"
CV "v119,v86,v87\n"
MF "a[84:87],v[152:155],v[96:99],a[84:87]\n"
AW "a56,v116\n"
AW "a57,v117\n"
AW "a58,v118\n"
AW "a59,v119\n"
LD "v124,s[28:31],s82 offen offset:2048 lds\n"
AR "v72,a80\n"
AR "v73,a81\n"
AR "v74,a82\n"
MF "a[104:107],v[156:159],v[96:99],a[104:107]\n"
AR "v75,a83\n"
AR "v76,a100\n"
AR "v77,a101\n"
AR "v78,a102\n"
AR "v79,a103\n"
"v_pk_fma_f32 v[72:73],v[72:73],v[56:57],v[52:53] op_sel:[0,1,0] op_sel_hi:[1,1,0]\n"
"v_pk_fma_f32 v[74:75],v[74:75],v[56:57],v[52:53] op_sel:[0,1,0] op_sel_hi:[1,1,0]\n"
"v_pk_fma_f32 v[76:77],v[76:77],v[56:57],v[52:53] op_sel:[0,1,0] op_sel_hi:[1,1,0]\n"
"v_pk_fma_f32 v[78:79],v[78:79],v[56:57],v[52:53] op_sel:[0,1,0] op_sel_hi:[1,1,0]\n"
CV "v112,v72,v73\n"
CV "v113,v74,v75\n"
CV "v114,v76,v77\n"
CV "v115,v78,v79\n"
MF "a[88:91],v[152:155],v[100:103],a[88:91]\n"
AW "a80,v112\n"
AW "a81,v113\n"
AW "a82,v114\n"
AW "a83,v115\n"
LD "v125,s[28:31],s82 offen offset:3072 lds\n"
AR "v80,a84\n"
AR "v81,a85\n"
AR "v82,a86\n"
MF "a[108:111],v[156:159],v[100:103],a[108:111]\n"
AR "v83,a87\n"
AR "v84,a104\n"
AR "v85,a105\n"
AR "v86,a106\n"
AR "v87,a107\n"
"v_pk_fma_f32 v[80:81],v[80:81],v[58:59],v[52:53] op_sel:[0,0,1] op_sel_hi:[1,0,1]\n"
"v_pk_fma_f32 v[82:83],v[82:83],v[58:59],v[52:53] op_sel:[0,0,1] op_sel_hi:[1,0,1]\n"
"v_pk_fma_f32 v[84:85],v[84:85],v[58:59],v[52:53] op_sel:[0,0,1] op_sel_hi:[1,0,1]\n"
"v_pk_fma_f32 v[86:87],v[86:87],v[58:59],v[52:53] op_sel:[0,0,1] op_sel_hi:[1,0,1]\n"
CV "v116,v80,v81\n"
CV "v117,v82,v83\n"
CV "v118,v84,v85\n"
CV "v119,v86,v87\n"
MF "a[92:95],v[152:155],v[104:107],a[92:95]\n"
AW "a84,v116\n"
AW "a85,v117\n"
AW "a86,v118\n"
AW "a87,v119\n"
AR "v72,a88\n"
AR "v73,a89\n"
AR "v74,a90\n"
MF "a[112:115],v[156:159],v[104:107],a[112:115]\n"
AR "v75,a91\n"
AR "v76,a108\n"
AR "v77,a109\n"
AR "v78,a110\n"
AR "v79,a111\n"
"v_pk_fma_f32 v[72:73],v[72:73],v[58:59],v[54:55] op_sel:[0,1,0] op_sel_hi:[1,1,0]\n"
"v_pk_fma_f32 v[74:75],v[74:75],v[58:59],v[54:55] op_sel:[0,1,0] op_sel_hi:[1,1,0]\n"
"v_pk_fma_f32 v[76:77],v[76:77],v[58:59],v[54:55] op_sel:[0,1,0] op_sel_hi:[1,1,0]\n"
"v_pk_fma_f32 v[78:79],v[78:79],v[58:59],v[54:55] op_sel:[0,1,0] op_sel_hi:[1,1,0]\n"
CV "v112,v72,v73\n"
CV "v113,v74,v75\n"
CV "v114,v76,v77\n"
CV "v115,v78,v79\n"
MF "a[96:99],v[152:155],v[108:111],a[96:99]\n"
AW "a88,v112\n"
AW "a89,v113\n"
AW "a90,v114\n"
AW "a91,v115\n"
AR "v80,a92\n"
AR "v81,a93\n"
AR "v82,a94\n"
MF "a[116:119],v[156:159],v[108:111],a[116:119]\n"
AR "v83,a95\n"
AR "v84,a112\n"
AR "v85,a113\n"
AR "v86,a114\n"
AR "v87,a115\n"
"v_pk_fma_f32 v[80:81],v[80:81],v[60:61],v[54:55] op_sel:[0,0,1] op_sel_hi:[1,0,1]\n"
"v_pk_fma_f32 v[82:83],v[82:83],v[60:61],v[54:55] op_sel:[0,0,1] op_sel_hi:[1,0,1]\n"
"v_pk_fma_f32 v[84:85],v[84:85],v[60:61],v[54:55] op_sel:[0,0,1] op_sel_hi:[1,0,1]\n"
"v_pk_fma_f32 v[86:87],v[86:87],v[60:61],v[54:55] op_sel:[0,0,1] op_sel_hi:[1,0,1]\n"
CV "v116,v80,v81\n"
CV "v117,v82,v83\n"
CV "v118,v84,v85\n"
CV "v119,v86,v87\n"
MF "a[120:123],v[160:163],v[92:95],a[120:123]\n"
AW "a92,v116\n"
AW "a93,v117\n"
AW "a94,v118\n"
AW "a95,v119\n"
AR "v72,a96\n"
AR "v73,a97\n"
AR "v74,a98\n"
MF "a[140:143],v[164:167],v[92:95],a[140:143]\n"
AR "v75,a99\n"
AR "v76,a116\n"
AR "v77,a117\n"
AR "v78,a118\n"
AR "v79,a119\n"
"v_pk_fma_f32 v[72:73],v[72:73],v[60:61],v[56:57] op_sel:[0,1,0] op_sel_hi:[1,1,0]\n"
"v_pk_fma_f32 v[74:75],v[74:75],v[60:61],v[56:57] op_sel:[0,1,0] op_sel_hi:[1,1,0]\n"
"v_pk_fma_f32 v[76:77],v[76:77],v[60:61],v[56:57] op_sel:[0,1,0] op_sel_hi:[1,1,0]\n"
"v_pk_fma_f32 v[78:79],v[78:79],v[60:61],v[56:57] op_sel:[0,1,0] op_sel_hi:[1,1,0]\n"
CV "v112,v72,v73\n"
CV "v113,v74,v75\n"
CV "v114,v76,v77\n"
CV "v115,v78,v79\n"
MF "a[124:127],v[160:163],v[96:99],a[124:127]\n"
".Lpg12f:\n"
"s_cbranch_execz .Lisr3\n"
AW "a96,v112\n"
AW "a97,v113\n"
AW "a98,v114\n"
"s_nop 0\n"
AW "a99,v115\n"
AR "v80,a120\n"
AR "v81,a121\n"
AR "v82,a122\n"
MF "a[144:147],v[164:167],v[96:99],a[144:147]\n"
AR "v83,a123\n"
AR "v84,a140\n"
AR "v85,a141\n"
AR "v86,a142\n"
AR "v87,a143\n"
"v_pk_fma_f32 v[80:81],v[80:81],v[56:57],v[52:53] op_sel:[0,1,0] op_sel_hi:[1,1,0]\n"
"v_pk_fma_f32 v[82:83],v[82:83],v[56:57],v[52:53] op_sel:[0,1,0] op_sel_hi:[1,1,0]\n"
"v_pk_fma_f32 v[84:85],v[84:85],v[56:57],v[52:53] op_sel:[0,1,0] op_sel_hi:[1,1,0]\n"
"v_pk_fma_f32 v[86:87],v[86:87],v[56:57],v[52:53] op_sel:[0,1,0] op_sel_hi:[1,1,0]\n"
CV "v116,v80,v81\n"
CV "v117,v82,v83\n"
CV "v118,v84,v85\n"
CV "v119,v86,v87\n"
MF "a[128:131],v[160:163],v[100:103],a[128:131]\n"
AW "a120,v116\n"
AW "a121,v117\n"
AW "a122,v118\n"
AW "a123,v119\n"
AR "v72,a124\n"
AR "v73,a125\n"
AR "v74,a126\n"
MF "a[148:151],v[164:167],v[100:103],a[148:151]\n"
AR "v75,a127\n"
AR "v76,a144\n"
AR "v77,a145\n"
AR "v78,a146\n"
AR "v79,a147\n"
"v_pk_fma_f32 v[72:73],v[72:73],v[58:59],v[52:53] op_sel:[0,0,1] op_sel_hi:[1,0,1]\n"
"v_pk_fma_f32 v[74:75],v[74:75],v[58:59],v[52:53] op_sel:[0,0,1] op_sel_hi:[1,0,1]\n"
"v_pk_fma_f32 v[76:77],v[76:77],v[58:59],v[52:53] op_sel:[0,0,1] op_sel_hi:[1,0,1]\n"
"v_pk_fma_f32 v[78:79],v[78:79],v[58:59],v[52:53] op_sel:[0,0,1] op_sel_hi:[1,0,1]\n"
CV "v112,v72,v73\n"
CV "v113,v74,v75\n"
CV "v114,v76,v77\n"
CV "v115,v78,v79\n"
MF "a[132:135],v[160:163],v[104:107],a[132:135]\n"
AW "a124,v112\n"
AW "a125,v113\n"
AW "a126,v114\n"
AW "a127,v115\n"
AR "v80,a128\n"
AR "v81,a129\n"
AR "v82,a130\n"
MF "a[152:155],v[164:167],v[104:107],a[152:155]\n"
AR "v83,a131\n"
AR "v84,a148\n"
AR "v85,a149\n"
AR "v86,a150\n"
AR "v87,a151\n"
"v_pk_fma_f32 v[80:81],v[80:81],v[58:59],v[54:55] op_sel:[0,1,0] op_sel_hi:[1,1,0]\n"
"v_pk_fma_f32 v[82:83],v[82:83],v[58:59],v[54:55] op_sel:[0,1,0] op_sel_hi:[1,1,0]\n"
"v_pk_fma_f32 v[84:85],v[84:85],v[58:59],v[54:55] op_sel:[0,1,0] op_sel_hi:[1,1,0]\n"
"v_pk_fma_f32 v[86:87],v[86:87],v[58:59],v[54:55] op_sel:[0,1,0] op_sel_hi:[1,1,0]\n"
CV "v116,v80,v81\n"
CV "v117,v82,v83\n"
CV "v118,v84,v85\n"
CV "v119,v86,v87\n"
MF "a[136:139],v[160:163],v[108:111],a[136:139]\n"
AW "a128,v116\n"
AW "a129,v117\n"
AW "a130,v118\n"
AW "a131,v119\n"
AR "v72,a132\n"
AR "v73,a133\n"
AR "v74,a134\n"
MF "a[156:159],v[164:167],v[108:111],a[156:159]\n"
AR "v75,a135\n"
AR "v76,a152\n"
AR "v77,a153\n"
AR "v78,a154\n"
AR "v79,a155\n"
"v_pk_fma_f32 v[72:73],v[72:73],v[60:61],v[54:55] op_sel:[0,0,1] op_sel_hi:[1,0,1]\n"
"v_pk_fma_f32 v[74:75],v[74:75],v[60:61],v[54:55] op_sel:[0,0,1] op_sel_hi:[1,0,1]\n"
"v_pk_fma_f32 v[76:77],v[76:77],v[60:61],v[54:55] op_sel:[0,0,1] op_sel_hi:[1,0,1]\n"
"v_pk_fma_f32 v[78:79],v[78:79],v[60:61],v[54:55] op_sel:[0,0,1] op_sel_hi:[1,0,1]\n"
CV "v112,v72,v73\n"
CV "v113,v74,v75\n"
CV "v114,v76,v77\n"
CV "v115,v78,v79\n"
MF "a[160:163],v[168:171],v[92:95],a[160:163]\n"
AW "a132,v112\n"
AW "a133,v113\n"
AW "a134,v114\n"
AW "a135,v115\n"
AR "v80,a136\n"
AR "v81,a137\n"
AR "v82,a138\n"
MF "a[180:183],v[172:175],v[92:95],a[180:183]\n"
AR "v83,a139\n"
AR "v84,a156\n"
AR "v85,a157\n"
AR "v86,a158\n"
AR "v87,a159\n"
"v_pk_fma_f32 v[80:81],v[80:81],v[60:61],v[56:57] op_sel:[0,1,0] op_sel_hi:[1,1,0]\n"
"v_pk_fma_f32 v[82:83],v[82:83],v[60:61],v[56:57] op_sel:[0,1,0] op_sel_hi:[1,1,0]\n"
"v_pk_fma_f32 v[84:85],v[84:85],v[60:61],v[56:57] op_sel:[0,1,0] op_sel_hi:[1,1,0]\n"
"v_pk_fma_f32 v[86:87],v[86:87],v[60:61],v[56:57] op_sel:[0,1,0] op_sel_hi:[1,1,0]\n"
CV "v116,v80,v81\n"
CV "v117,v82,v83\n"
CV "v118,v84,v85\n"
CV "v119,v86,v87\n"
MF "a[164:167],v[168:171],v[96:99],a[164:167]\n"
AW "a136,v116\n"
AW "a137,v117\n"
AW "a138,v118\n"
AW "a139,v119\n"
AR "v72,a160\n"
AR "v73,a161\n"
AR "v74,a162\n"
MF "a[184:187],v[172:175],v[96:99],a[184:187]\n"
AR "v75,a163\n"
AR "v76,a180\n"
AR "v77,a181\n"
AR "v78,a182\n"
AR "v79,a183\n"
"v_pk_fma_f32 v[72:73],v[72:73],v[56:57],v[52:53] op_sel:[0,1,0] op_sel_hi:[1,1,0]\n"
"v_pk_fma_f32 v[74:75],v[74:75],v[56:57],v[52:53] op_sel:[0,1,0] op_sel_hi:[1,1,0]\n"
"v_pk_fma_f32 v[76:77],v[76:77],v[56:57],v[52:53] op_sel:[0,1,0] op_sel_hi:[1,1,0]\n"
"v_pk_fma_f32 v[78:79],v[78:79],v[56:57],v[52:53] op_sel:[0,1,0] op_sel_hi:[1,1,0]\n"
CV "v112,v72,v73\n"
CV "v113,v74,v75\n"
CV "v114,v76,v77\n"
CV "v115,v78,v79\n"
MF "a[168:171],v[168:171],v[100:103],a[168:171]\n"
AW "a160,v112\n"
AW "a161,v113\n"
AW "a162,v114\n"
AW "a163,v115\n"
AR "v80,a164\n"
AR "v81,a165\n"
AR "v82,a166\n"
MF "a[188:191],v[172:175],v[100:103],a[188:191]\n"
AR "v83,a167\n"
AR "v84,a184\n"
AR "v85,a185\n"
AR "v86,a186\n"
AR "v87,a187\n"
"v_pk_fma_f32 v[80:81],v[80:81],v[58:59],v[52:53] op_sel:[0,0,1] op_sel_hi:[1,0,1]\n"
"v_pk_fma_f32 v[82:83],v[82:83],v[58:59],v[52:53] op_sel:[0,0,1] op_sel_hi:[1,0,1]\n"
"v_pk_fma_f32 v[84:85],v[84:85],v[58:59],v[52:53] op_sel:[0,0,1] op_sel_hi:[1,0,1]\n"
"v_pk_fma_f32 v[86:87],v[86:87],v[58:59],v[52:53] op_sel:[0,0,1] op_sel_hi:[1,0,1]\n"
CV "v116,v80,v81\n"
CV "v117,v82,v83\n"
CV "v118,v84,v85\n"
CV "v119,v86,v87\n"
MF "a[172:175],v[168:171],v[104:107],a[172:175]\n"
AW "a164,v116\n"
AW "a165,v117\n"
AW "a166,v118\n"
AW "a167,v119\n"
AR "v72,a168\n"
AR "v73,a169\n"
AR "v74,a170\n"
MF "a[192:195],v[172:175],v[104:107],a[192:195]\n"
AR "v75,a171\n"
AR "v76,a188\n"
AR "v77,a189\n"
AR "v78,a190\n"
AR "v79,a191\n"
"v_pk_fma_f32 v[72:73],v[72:73],v[58:59],v[54:55] op_sel:[0,1,0] op_sel_hi:[1,1,0]\n"
"v_pk_fma_f32 v[74:75],v[74:75],v[58:59],v[54:55] op_sel:[0,1,0] op_sel_hi:[1,1,0]\n"
"v_pk_fma_f32 v[76:77],v[76:77],v[58:59],v[54:55] op_sel:[0,1,0] op_sel_hi:[1,1,0]\n"
"v_pk_fma_f32 v[78:79],v[78:79],v[58:59],v[54:55] op_sel:[0,1,0] op_sel_hi:[1,1,0]\n"
CV "v112,v72,v73\n"
CV "v113,v74,v75\n"
CV "v114,v76,v77\n"
CV "v115,v78,v79\n"
MF "a[176:179],v[168:171],v[108:111],a[176:179]\n"
AW "a168,v112\n"
AW "a169,v113\n"
AW "a170,v114\n"
AW "a171,v115\n"
AR "v80,a172\n"
AR "v81,a173\n"
AR "v82,a174\n"
MF "a[196:199],v[172:175],v[108:111],a[196:199]\n"
AR "v83,a175\n"
AR "v84,a192\n"
AR "v85,a193\n"
AR "v86,a194\n"
AR "v87,a195\n"
"v_pk_fma_f32 v[80:81],v[80:81],v[60:61],v[54:55] op_sel:[0,0,1] op_sel_hi:[1,0,1]\n"
"v_pk_fma_f32 v[82:83],v[82:83],v[60:61],v[54:55] op_sel:[0,0,1] op_sel_hi:[1,0,1]\n"
"v_pk_fma_f32 v[84:85],v[84:85],v[60:61],v[54:55] op_sel:[0,0,1] op_sel_hi:[1,0,1]\n"
"v_pk_fma_f32 v[86:87],v[86:87],v[60:61],v[54:55] op_sel:[0,0,1] op_sel_hi:[1,0,1]\n"
CV "v116,v80,v81\n"
CV "v117,v82,v83\n"
CV "v118,v84,v85\n"
CV "v119,v86,v87\n"
MF "a[200:203],v[176:179],v[92:95],a[200:203]\n"
AW "a172,v116\n"
AW "a173,v117\n"
AW "a174,v118\n"
AW "a175,v119\n"
AR "v72,a176\n"
AR "v73,a177\n"
AR "v74,a178\n"
MF "a[220:223],v[180:183],v[92:95],a[220:223]\n"
AR "v75,a179\n"
AR "v76,a196\n"
AR "v77,a197\n"
AR "v78,a198\n"
AR "v79,a199\n"
"v_pk_fma_f32 v[72:73],v[72:73],v[60:61],v[56:57] op_sel:[0,1,0] op_sel_hi:[1,1,0]\n"
"v_pk_fma_f32 v[74:75],v[74:75],v[60:61],v[56:57] op_sel:[0,1,0] op_sel_hi:[1,1,0]\n"
"v_pk_fma_f32 v[76:77],v[76:77],v[60:61],v[56:57] op_sel:[0,1,0] op_sel_hi:[1,1,0]\n"
"v_pk_fma_f32 v[78:79],v[78:79],v[60:61],v[56:57] op_sel:[0,1,0] op_sel_hi:[1,1,0]\n"
CV "v112,v72,v73\n"
CV "v113,v74,v75\n"
CV "v114,v76,v77\n"
CV "v115,v78,v79\n"
MF "a[204:207],v[176:179],v[96:99],a[204:207]\n"
AW "a176,v112\n"
AW "a177,v113\n"
AW "a178,v114\n"
AW "a179,v115\n"
AR "v80,a200\n"
AR "v81,a201\n"
AR "v82,a202\n"
MF "a[224:227],v[180:183],v[96:99],a[224:227]\n"
AR "v83,a203\n"
AR "v84,a220\n"
AR "v85,a221\n"
AR "v86,a222\n"
AR "v87,a223\n"
"v_pk_fma_f32 v[80:81],v[80:81],v[56:57],v[52:53] op_sel:[0,1,0] op_sel_hi:[1,1,0]\n"
"v_pk_fma_f32 v[82:83],v[82:83],v[56:57],v[52:53] op_sel:[0,1,0] op_sel_hi:[1,1,0]\n"
"v_pk_fma_f32 v[84:85],v[84:85],v[56:57],v[52:53] op_sel:[0,1,0] op_sel_hi:[1,1,0]\n"
"v_pk_fma_f32 v[86:87],v[86:87],v[56:57],v[52:53] op_sel:[0,1,0] op_sel_hi:[1,1,0]\n"
CV "v116,v80,v81\n"
CV "v117,v82,v83\n"
CV "v118,v84,v85\n"
CV "v119,v86,v87\n"
MF "a[208:211],v[176:179],v[100:103],a[208:211]\n"
AW "a20,v116\n"
AW "a21,v117\n"
AW "a22,v118\n"
AW "a23,v119\n"
AR "v72,a204\n"
AR "v73,a205\n"
AR "v74,a206\n"
MF "a[228:231],v[180:183],v[100:103],a[228:231]\n"
AR "v75,a207\n"
AR "v76,a224\n"
AR "v77,a225\n"
AR "v78,a226\n"
AR "v79,a227\n"
"v_pk_fma_f32 v[72:73],v[72:73],v[58:59],v[52:53] op_sel:[0,0,1] op_sel_hi:[1,0,1]\n"
"v_pk_fma_f32 v[74:75],v[74:75],v[58:59],v[52:53] op_sel:[0,0,1] op_sel_hi:[1,0,1]\n"
"v_pk_fma_f32 v[76:77],v[76:77],v[58:59],v[52:53] op_sel:[0,0,1] op_sel_hi:[1,0,1]\n"
"v_pk_fma_f32 v[78:79],v[78:79],v[58:59],v[52:53] op_sel:[0,0,1] op_sel_hi:[1,0,1]\n"
CV "v112,v72,v73\n"
CV "v113,v74,v75\n"
CV "v114,v76,v77\n"
CV "v115,v78,v79\n"
MF "a[212:215],v[176:179],v[104:107],a[212:215]\n"
AW "a24,v112\n"
AW "a25,v113\n"
AW "a26,v114\n"
AW "a27,v115\n"
AR "v80,a208\n"
AR "v81,a209\n"
AR "v82,a210\n"
MF "a[232:235],v[180:183],v[104:107],a[232:235]\n"
AR "v83,a211\n"
AR "v84,a228\n"
AR "v85,a229\n"
AR "v86,a230\n"
AR "v87,a231\n"
"v_pk_fma_f32 v[80:81],v[80:81],v[58:59],v[54:55] op_sel:[0,1,0] op_sel_hi:[1,1,0]\n"
"v_pk_fma_f32 v[82:83],v[82:83],v[58:59],v[54:55] op_sel:[0,1,0] op_sel_hi:[1,1,0]\n"
"v_pk_fma_f32 v[84:85],v[84:85],v[58:59],v[54:55] op_sel:[0,1,0] op_sel_hi:[1,1,0]\n"
"v_pk_fma_f32 v[86:87],v[86:87],v[58:59],v[54:55] op_sel:[0,1,0] op_sel_hi:[1,1,0]\n"
CV "v116,v80,v81\n"
CV "v117,v82,v83\n"
CV "v118,v84,v85\n"
CV "v119,v86,v87\n"
MF "a[216:219],v[176:179],v[108:111],a[216:219]\n"
AW "a28,v116\n"
AW "a29,v117\n"
AW "a30,v118\n"
AW "a31,v119\n"
AR "v72,a212\n"
AR "v73,a213\n"
AR "v74,a214\n"
MF "a[236:239],v[180:183],v[108:111],a[236:239]\n"
AR "v75,a215\n"
AR "v76,a232\n"
AR "v77,a233\n"
AR "v78,a234\n"
AR "v79,a235\n"
"v_pk_fma_f32 v[72:73],v[72:73],v[60:61],v[54:55] op_sel:[0,0,1] op_sel_hi:[1,0,1]\n"
"v_pk_fma_f32 v[74:75],v[74:75],v[60:61],v[54:55] op_sel:[0,0,1] op_sel_hi:[1,0,1]\n"
"v_pk_fma_f32 v[76:77],v[76:77],v[60:61],v[54:55] op_sel:[0,0,1] op_sel_hi:[1,0,1]\n"
"v_pk_fma_f32 v[78:79],v[78:79],v[60:61],v[54:55] op_sel:[0,0,1] op_sel_hi:[1,0,1]\n"
CV "v112,v72,v73\n"
CV "v113,v74,v75\n"
CV "v114,v76,v77\n"
CV "v115,v78,v79\n"
AW "a32,v112\n"
AW "a33,v113\n"
AW "a34,v114\n"
AW "a35,v115\n"
AR "v80,a216\n"
AR "v81,a217\n"
AR "v82,a218\n"
AR "v83,a219\n"
AR "v84,a236\n"
AR "v85,a237\n"
AR "v86,a238\n"
AR "v87,a239\n"
"v_pk_fma_f32 v[80:81],v[80:81],v[60:61],v[56:57] op_sel:[0,1,0] op_sel_hi:[1,1,0]\n"
"v_pk_fma_f32 v[82:83],v[82:83],v[60:61],v[56:57] op_sel:[0,1,0] op_sel_hi:[1,1,0]\n"
"v_pk_fma_f32 v[84:85],v[84:85],v[60:61],v[56:57] op_sel:[0,1,0] op_sel_hi:[1,1,0]\n"
"v_pk_fma_f32 v[86:87],v[86:87],v[60:61],v[56:57] op_sel:[0,1,0] op_sel_hi:[1,1,0]\n"
CV "v116,v80,v81\n"
CV "v117,v82,v83\n"
CV "v118,v84,v85\n"
CV "v119,v86,v87\n"
AW "a36,v116\n"
AW "a37,v117\n"
AW "a38,v118\n"
AW "a39,v119\n"
"s_mov_b32 s13,s11\n"
"s_mov_b32 s70,s71\n"
"s_mov_b32 s57,1\n"
AR "v0,a240\n"
AR "v2,a241\n"
AR "v4,a242\n"
AR "v6,a243\n"
"v_xor_b32 v1,64,v0\n"
"v_xor_b32 v3,64,v2\n"
"v_xor_b32 v5,64,v4\n"
"v_add_u32 v11,97280,v6\n"
"v_add_u32 v12,194560,v6\n"
"v_add_u32 v13,291840,v6\n"
"s_mov_b64 vcc,-1\n"
"s_waitcnt vmcnt(8) lgkmcnt(0)\n"
"s_barrier\n"
"s_nop 0\n"
TR "a[200:201],v0\n"
TR "a[202:203],v0 offset:512\n"
TR "a[204:205],v0 offset:4096\n"
TR "a[206:207],v0 offset:4608\n"
"ds_read_b128 a[60:63],v2\n"
"ds_read_b128 a[100:103],v3\n"
"ds_read_b128 a[64:67],v2 offset:2048\n"
"ds_read_b128 a[104:107],v3 offset:2048\n"
"ds_read_b128 a[68:71],v2 offset:4096\n"
"ds_read_b128 a[108:111],v3 offset:4096\n"
"ds_read_b128 a[72:75],v2 offset:6144\n"
"ds_read_b128 a[112:115],v3 offset:6144\n"
"ds_read_b128 a[76:79],v2 offset:8192\n"
"ds_read_b128 a[116:119],v3 offset:8192\n"
TR "a[208:209],v0 offset:8\n"
TR "a[210:211],v0 offset:520\n"
TR "a[212:213],v0 offset:4104\n"
TR "a[214:215],v0 offset:4616\n"
"s_waitcnt lgkmcnt(0)\n"
"s_branch .Lq0\n"
".Lpgqy_c:\n"
"s_cbranch_execz .Lpgqj_a\n"
".Lqy:\n"
MF "v[216:219],a[216:219],a[60:63],v[216:219]\n"
TR "a[208:209],v0 offset:8\n"
MF "v[216:219],a[220:223],a[100:103],v[216:219]\n"
TR "a[210:211],v0 offset:520\n"
MF "v[224:227],a[220:223],a[104:107],v[224:227]\n"
MF "v[224:227],a[216:219],a[64:67],v[224:227]\n"
TR "a[212:213],v0 offset:4104\n"
MF "v[232:235],a[216:219],a[68:71],v[232:235]\n"
TR "a[214:215],v0 offset:4616\n"
MF "v[232:235],a[220:223],a[108:111],v[232:235]\n"
MF "v[240:243],a[220:223],a[112:115],v[240:243]\n"
MF "v[240:243],a[216:219],a[72:75],v[240:243]\n"
MF "v[248:251],a[216:219],a[76:79],v[248:251]\n"
MF "v[248:251],a[220:223],a[116:119],v[248:251]\n"
MF "v[248:251],a[200:203],a[156:159],v[248:251]\n"
TR "a[216:217],v0 offset:16392\n"
MF "v[248:251],a[204:207],a[196:199],v[248:251]\n"
TR "a[218:219],v0 offset:16904\n"
MF "v[240:243],a[204:207],a[192:195],v[240:243]\n"
MF "v[240:243],a[200:203],a[152:155],v[240:243]\n"
TR "a[220:221],v0 offset:20488\n"
MF "v[232:235],a[200:203],a[148:151],v[232:235]\n"
TR "a[222:223],v0 offset:21000\n"
MF "v[232:235],a[204:207],a[188:191],v[232:235]\n"
MF "v[224:227],a[204:207],a[184:187],v[224:227]\n"
MF "v[224:227],a[200:203],a[144:147],v[224:227]\n"
"v_fma_f32 v248,v248,s54,v14\n"
"v_fma_f32 v249,v249,s54,v14\n"
MF "v[216:219],a[200:203],a[140:143],v[216:219]\n"
"v_fma_f32 v250,v250,s54,v14\n"
"v_fma_f32 v251,v251,s54,v14\n"
MF "v[216:219],a[204:207],a[180:183],v[216:219]\n"
"s_waitcnt lgkmcnt(4)\n"
"v_fma_f32 v252,v252,s54,v14\n"
"v_fma_f32 v253,v253,s54,v14\n"
MF "v[180:183],a[208:211],a[60:63],v[180:183]\n"
TR "a[200:201],v0\n"
"v_fma_f32 v254,v254,s54,v14\n"
MF "v[180:183],a[212:215],a[100:103],v[180:183]\n"
TR "a[202:203],v0 offset:512\n"
"v_fma_f32 v255,v255,s54,v14\n"
MF "v[188:191],a[212:215],a[104:107],v[188:191]\n"
CV "v255,v254,v255\n"
CV "v254,v252,v253\n"
MF "v[188:191],a[208:211],a[64:67],v[188:191]\n"
TR "a[204:205],v0 offset:4096\n"
CV "v253,v250,v251\n"
MF "v[196:199],a[208:211],a[68:71],v[196:199]\n"
TR "a[206:207],v0 offset:4608\n"
CV "v252,v248,v249\n"
MF "v[196:199],a[212:215],a[108:111],v[196:199]\n"
"s_add_u32 s58,s75,4096\n"
"v_fma_f32 v240,v240,s54,v14\n"
"v_fma_f32 v241,v241,s54,v14\n"
MF "v[204:207],a[212:215],a[112:115],v[204:207]\n"
"buffer_store_dwordx4 v[252:255],v9,s[40:43],s58 offen offset:1024 sc1\n"
MF "v[204:207],a[208:211],a[72:75],v[204:207]\n"
"v_fma_f32 v242,v242,s54,v14\n"
"v_fma_f32 v243,v243,s54,v14\n"
MF "v[212:215],a[208:211],a[76:79],v[212:215]\n"
"v_fma_f32 v244,v244,s54,v14\n"
"v_fma_f32 v245,v245,s54,v14\n"
MF "v[212:215],a[212:215],a[116:119],v[212:215]\n"
"s_waitcnt lgkmcnt(4)\n"
"v_fma_f32 v246,v246,s54,v14\n"
"v_fma_f32 v247,v247,s54,v14\n"
MF "v[180:183],a[216:219],a[140:143],v[180:183]\n"
TR "a[208:209],v0 offset:16384\n"
CV "v251,v246,v247\n"
MF "v[180:183],a[220:223],a[180:183],v[180:183]\n"
TR "a[210:211],v0 offset:16896\n"
CV "v250,v244,v245\n"
MF "v[188:191],a[220:223],a[184:187],v[188:191]\n"
CV "v249,v242,v243\n"
CV "v248,v240,v241\n"
MF "v[188:191],a[216:219],a[144:147],v[188:191]\n"
TR "a[212:213],v0 offset:20480\n"
"v_fma_f32 v232,v232,s54,v14\n"
MF "v[196:199],a[216:219],a[148:151],v[196:199]\n"
TR "a[214:215],v0 offset:20992\n"
"v_fma_f32 v233,v233,s54,v14\n"
MF "v[196:199],a[220:223],a[188:191],v[196:199]\n"
"buffer_store_dwordx4 v[248:251],v9,s[40:43],s58 offen sc1\n"
MF "v[204:207],a[220:223],a[192:195],v[204:207]\n"
"v_fma_f32 v234,v234,s54,v14\n"
"v_fma_f32 v235,v235,s54,v14\n"
MF "v[204:207],a[216:219],a[152:155],v[204:207]\n"
"v_fma_f32 v236,v236,s54,v14\n"
"v_fma_f32 v237,v237,s54,v14\n"
MF "v[212:215],a[216:219],a[156:159],v[212:215]\n"
"v_fma_f32 v238,v238,s54,v14\n"
"v_fma_f32 v239,v239,s54,v14\n"
MF "v[212:215],a[220:223],a[196:199],v[212:215]\n"
"s_waitcnt vmcnt(10) lgkmcnt(0)\n"
"s_barrier\n"
MF "v[176:179],a[200:203],a[60:63],v[176:179]\n"
TR "a[216:217],v1 offset:32776\n"
CV "v247,v238,v239\n"
MF "v[176:179],a[204:207],a[100:103],v[176:179]\n"
TR "a[218:219],v1 offset:33288\n"
CV "v246,v236,v237\n"
MF "v[184:187],a[204:207],a[104:107],v[184:187]\n"
"s_add_u32 s66,s67,4718592\n"
"s_sub_u32 s81,s81,384\n"
"s_mov_b32 s83,7077888\n"
MF "v[184:187],a[200:203],a[64:67],v[184:187]\n"
"s_add_u32 m0,s62,0\n"
"s_add_u32 s82,s66,2359296\n"
TR "a[220:221],v1 offset:36872\n"
MF "v[192:195],a[200:203],a[68:71],v[192:195]\n"
"s_sub_u32 s15,s82,7864320\n"
"s_min_u32 s82,s82,s15\n"
TR "a[222:223],v1 offset:37384\n"
MF "v[192:195],a[204:207],a[108:111],v[192:195]\n"
LD "v6,s[28:31],s82 offen lds\n"
MF "v[200:203],a[204:207],a[112:115],v[200:203]\n"
CV "v245,v234,v235\n"
CV "v244,v232,v233\n"
MF "v[200:203],a[200:203],a[72:75],v[200:203]\n"
"v_fma_f32 v224,v224,s54,v14\n"
"v_fma_f32 v225,v225,s54,v14\n"
MF "v[208:211],a[200:203],a[76:79],v[208:211]\n"
"buffer_store_dwordx4 v[244:247],v9,s[40:43],s75 offen offset:3072 sc1\n"
MF "v[208:211],a[204:207],a[116:119],v[208:211]\n"
LD "v11,s[28:31],s82 offen offset:1024 lds\n"
MF "v[208:211],a[208:211],a[156:159],v[208:211]\n"
TR "a[200:201],v1 offset:49160\n"
"v_fma_f32 v226,v226,s54,v14\n"
MF "v[208:211],a[212:215],a[196:199],v[208:211]\n"
TR "a[202:203],v1 offset:49672\n"
"v_fma_f32 v227,v227,s54,v14\n"
MF "v[200:203],a[212:215],a[192:195],v[200:203]\n"
"v_fma_f32 v228,v228,s54,v14\n"
"v_fma_f32 v229,v229,s54,v14\n"
MF "v[200:203],a[208:211],a[152:155],v[200:203]\n"
TR "a[204:205],v1 offset:53256\n"
"v_fma_f32 v230,v230,s54,v14\n"
MF "v[192:195],a[208:211],a[148:151],v[192:195]\n"
TR "a[206:207],v1 offset:53768\n"
"v_fma_f32 v231,v231,s54,v14\n"
MF "v[192:195],a[212:215],a[188:191],v[192:195]\n"
CV "v243,v230,v231\n"
CV "v242,v228,v229\n"
MF "v[184:187],a[212:215],a[184:187],v[184:187]\n"
CV "v241,v226,v227\n"
CV "v240,v224,v225\n"
MF "v[184:187],a[208:211],a[144:147],v[184:187]\n"
"buffer_store_dwordx4 v[240:243],v9,s[40:43],s75 offen offset:2048 sc1\n"
MF "v[176:179],a[208:211],a[140:143],v[176:179]\n"
LD "v12,s[28:31],s82 offen offset:2048 lds\n"
MF "v[176:179],a[212:215],a[180:183],v[176:179]\n"
"s_waitcnt lgkmcnt(4)\n"
"v_fma_f32 v216,v216,s54,v14\n"
"v_fma_f32 v217,v217,s54,v14\n"
MF "v[140:143],a[216:219],a[60:63],v[140:143]\n"
".Lpg13c:\n"
"s_cbranch_execz .Lpg3a\n"
TR "a[208:209],v1 offset:32768\n"
"v_fma_f32 v218,v218,s54,v14\n"
MF "v[140:143],a[220:223],a[100:103],v[140:143]\n"
TR "a[210:211],v1 offset:33280\n"
"v_fma_f32 v219,v219,s54,v14\n"
MF "v[148:151],a[220:223],a[104:107],v[148:151]\n"
"v_fma_f32 v220,v220,s54,v14\n"
"v_fma_f32 v221,v221,s54,v14\n"
MF "v[148:151],a[216:219],a[64:67],v[148:151]\n"
TR "a[212:213],v1 offset:36864\n"
"v_fma_f32 v222,v222,s54,v14\n"
MF "v[156:159],a[216:219],a[68:71],v[156:159]\n"
TR "a[214:215],v1 offset:37376\n"
"v_fma_f32 v223,v223,s54,v14\n"
MF "v[156:159],a[220:223],a[108:111],v[156:159]\n"
CV "v239,v222,v223\n"
CV "v238,v220,v221\n"
MF "v[164:167],a[220:223],a[112:115],v[164:167]\n"
CV "v237,v218,v219\n"
CV "v236,v216,v217\n"
MF "v[164:167],a[216:219],a[72:75],v[164:167]\n"
LD "v13,s[28:31],s82 offen offset:3072 lds\n"
MF "v[172:175],a[216:219],a[76:79],v[172:175]\n"
"buffer_store_dwordx4 v[236:239],v9,s[40:43],s75 offen offset:1024 sc1\n"
MF "v[172:175],a[220:223],a[116:119],v[172:175]\n"
"s_waitcnt lgkmcnt(4)\n"
"v_fma_f32 v208,v208,s54,v14\n"
"v_fma_f32 v209,v209,s54,v14\n"
MF "v[140:143],a[200:203],a[140:143],v[140:143]\n"
TR "a[216:217],v1 offset:49152\n"
"v_fma_f32 v210,v210,s54,v14\n"
MF "v[140:143],a[204:207],a[180:183],v[140:143]\n"
TR "a[218:219],v1 offset:49664\n"
"v_fma_f32 v211,v211,s54,v14\n"
MF "v[148:151],a[204:207],a[184:187],v[148:151]\n"
"v_fma_f32 v212,v212,s54,v14\n"
"v_fma_f32 v213,v213,s54,v14\n"
MF "v[148:151],a[200:203],a[144:147],v[148:151]\n"
TR "a[220:221],v1 offset:53248\n"
"v_fma_f32 v214,v214,s54,v14\n"
MF "v[156:159],a[200:203],a[148:151],v[156:159]\n"
TR "a[222:223],v1 offset:53760\n"
"v_fma_f32 v215,v215,s54,v14\n"
MF "v[156:159],a[204:207],a[188:191],v[156:159]\n"
CV "v235,v214,v215\n"
CV "v234,v212,v213\n"
MF "v[164:167],a[204:207],a[192:195],v[164:167]\n"
CV "v233,v210,v211\n"
CV "v232,v208,v209\n"
MF "v[164:167],a[200:203],a[152:155],v[164:167]\n"
"v_fma_f32 v200,v200,s54,v14\n"
"v_fma_f32 v201,v201,s54,v14\n"
MF "v[172:175],a[200:203],a[156:159],v[172:175]\n"
"buffer_store_dwordx4 v[232:235],v9,s[40:43],s75 offen sc1\n"
MF "v[172:175],a[204:207],a[196:199],v[172:175]\n"
"s_waitcnt lgkmcnt(0)\n"
"v_fma_f32 v202,v202,s54,v14\n"
"v_fma_f32 v203,v203,s54,v14\n"
MF "v[136:139],a[208:211],a[60:63],v[136:139]\n"
"s_add_u32 m0,s62,16384\n"
"s_add_u32 s82,s66,2359424\n"
TR "a[200:201],v0 offset:32776\n"
MF "v[136:139],a[212:215],a[100:103],v[136:139]\n"
"s_sub_u32 s15,s82,7864320\n"
"s_min_u32 s82,s82,s15\n"
TR "a[202:203],v0 offset:33288\n"
MF "v[144:147],a[212:215],a[104:107],v[144:147]\n"
LD "v6,s[28:31],s82 offen lds\n"
MF "v[144:147],a[208:211],a[64:67],v[144:147]\n"
TR "a[204:205],v0 offset:36872\n"
"v_fma_f32 v204,v204,s54,v14\n"
MF "v[152:155],a[208:211],a[68:71],v[152:155]\n"
TR "a[206:207],v0 offset:37384\n"
"v_fma_f32 v205,v205,s54,v14\n"
MF "v[152:155],a[212:215],a[108:111],v[152:155]\n"
"v_fma_f32 v206,v206,s54,v14\n"
"v_fma_f32 v207,v207,s54,v14\n"
MF "v[160:163],a[212:215],a[112:115],v[160:163]\n"
CV "v231,v206,v207\n"
CV "v230,v204,v205\n"
MF "v[160:163],a[208:211],a[72:75],v[160:163]\n"
CV "v229,v202,v203\n"
CV "v228,v200,v201\n"
MF "v[168:171],a[208:211],a[76:79],v[168:171]\n"
AW "a84,v228\n"
AW "a85,v229\n"
MF "v[168:171],a[212:215],a[116:119],v[168:171]\n"
LD "v11,s[28:31],s82 offen offset:1024 lds\n"
MF "v[168:171],a[216:219],a[156:159],v[168:171]\n"
TR "a[208:209],v0 offset:49160\n"
AW "a86,v230\n"
MF "v[168:171],a[220:223],a[196:199],v[168:171]\n"
TR "a[210:211],v0 offset:49672\n"
AW "a87,v231\n"
MF "v[160:163],a[220:223],a[192:195],v[160:163]\n"
"v_fma_f32 v192,v192,s54,v14\n"
"v_fma_f32 v193,v193,s54,v14\n"
MF "v[160:163],a[216:219],a[152:155],v[160:163]\n"
TR "a[212:213],v0 offset:53256\n"
"v_fma_f32 v194,v194,s54,v14\n"
MF "v[152:155],a[216:219],a[148:151],v[152:155]\n"
TR "a[214:215],v0 offset:53768\n"
"v_fma_f32 v195,v195,s54,v14\n"
MF "v[152:155],a[220:223],a[188:191],v[152:155]\n"
"v_fma_f32 v196,v196,s54,v14\n"
"v_fma_f32 v197,v197,s54,v14\n"
MF "v[144:147],a[220:223],a[184:187],v[144:147]\n"
"v_fma_f32 v198,v198,s54,v14\n"
"v_fma_f32 v199,v199,s54,v14\n"
MF "v[144:147],a[216:219],a[144:147],v[144:147]\n"
CV "v227,v198,v199\n"
CV "v226,v196,v197\n"
MF "v[136:139],a[216:219],a[140:143],v[136:139]\n"
LD "v12,s[28:31],s82 offen offset:2048 lds\n"
MF "v[136:139],a[220:223],a[180:183],v[136:139]\n"
"s_waitcnt lgkmcnt(4)\n"
CV "v225,v194,v195\n"
CV "v224,v192,v193\n"
MF "v[100:103],a[200:203],a[60:63],v[100:103]\n"
TR "a[216:217],v0 offset:32768\n"
AW "a80,v224\n"
MF "v[100:103],a[204:207],a[100:103],v[100:103]\n"
TR "a[218:219],v0 offset:33280\n"
AW "a81,v225\n"
MF "v[108:111],a[204:207],a[104:107],v[108:111]\n"
AW "a82,v226\n"
AW "a83,v227\n"
MF "v[108:111],a[200:203],a[64:67],v[108:111]\n"
TR "a[220:221],v0 offset:36864\n"
"v_fma_f32 v184,v184,s54,v14\n"
MF "v[116:119],a[200:203],a[68:71],v[116:119]\n"
TR "a[222:223],v0 offset:37376\n"
"v_fma_f32 v185,v185,s54,v14\n"
MF "v[116:119],a[204:207],a[108:111],v[116:119]\n"
"v_fma_f32 v186,v186,s54,v14\n"
"v_fma_f32 v187,v187,s54,v14\n"
MF "v[124:127],a[204:207],a[112:115],v[124:127]\n"
"v_fma_f32 v188,v188,s54,v14\n"
"v_fma_f32 v189,v189,s54,v14\n"
MF "v[124:127],a[200:203],a[72:75],v[124:127]\n"
LD "v13,s[28:31],s82 offen offset:3072 lds\n"
MF "v[132:135],a[200:203],a[76:79],v[132:135]\n"
"v_fma_f32 v190,v190,s54,v14\n"
"v_fma_f32 v191,v191,s54,v14\n"
MF "v[132:135],a[204:207],a[116:119],v[132:135]\n"
"s_waitcnt lgkmcnt(4)\n"
CV "v223,v190,v191\n"
CV "v222,v188,v189\n"
MF "v[100:103],a[208:211],a[140:143],v[100:103]\n"
TR "a[200:201],v0 offset:49152\n"
CV "v221,v186,v187\n"
MF "v[100:103],a[212:215],a[180:183],v[100:103]\n"
TR "a[202:203],v0 offset:49664\n"
CV "v220,v184,v185\n"
MF "v[108:111],a[212:215],a[184:187],v[108:111]\n"
AW "a56,v220\n"
AW "a57,v221\n"
MF "v[108:111],a[208:211],a[144:147],v[108:111]\n"
TR "a[204:205],v0 offset:53248\n"
AW "a58,v222\n"
MF "v[116:119],a[208:211],a[148:151],v[116:119]\n"
TR "a[206:207],v0 offset:53760\n"
AW "a59,v223\n"
MF "v[116:119],a[212:215],a[188:191],v[116:119]\n"
"v_fma_f32 v176,v176,s54,v14\n"
"v_fma_f32 v177,v177,s54,v14\n"
MF "v[124:127],a[212:215],a[192:195],v[124:127]\n"
"v_add_u32 v0,0xc000,v0\n"
"v_fma_f32 v178,v178,s54,v14\n"
MF "v[124:127],a[208:211],a[152:155],v[124:127]\n"
"v_add_u32 v1,0xc000,v1\n"
"v_fma_f32 v179,v179,s54,v14\n"
MF "v[132:135],a[208:211],a[156:159],v[132:135]\n"
"v_fma_f32 v180,v180,s54,v14\n"
"v_fma_f32 v181,v181,s54,v14\n"
MF "v[132:135],a[212:215],a[196:199],v[132:135]\n"
"s_waitcnt vmcnt(14) lgkmcnt(0)\n"
"s_barrier\n"
MF "v[96:99],a[216:219],a[60:63],v[96:99]\n"
"s_add_u32 m0,s62,32768\n"
"s_add_u32 s82,s66,2359552\n"
TR "a[208:209],v1 offset:16392\n"
MF "v[96:99],a[220:223],a[100:103],v[96:99]\n"
"s_sub_u32 s15,s82,7864320\n"
"s_min_u32 s82,s82,s15\n"
TR "a[210:211],v1 offset:16904\n"
MF "v[104:107],a[220:223],a[104:107],v[104:107]\n"
LD "v6,s[28:31],s82 offen lds\n"
MF "v[104:107],a[216:219],a[64:67],v[104:107]\n"
TR "a[212:213],v1 offset:20488\n"
"v_fma_f32 v182,v182,s54,v14\n"
MF "v[112:115],a[216:219],a[68:71],v[112:115]\n"
TR "a[214:215],v1 offset:21000\n"
"v_fma_f32 v183,v183,s54,v14\n"
MF "v[112:115],a[220:223],a[108:111],v[112:115]\n"
CV "v219,v182,v183\n"
CV "v218,v180,v181\n"
MF "v[120:123],a[220:223],a[112:115],v[120:123]\n"
CV "v217,v178,v179\n"
CV "v216,v176,v177\n"
MF "v[120:123],a[216:219],a[72:75],v[120:123]\n"
AW "a52,v216\n"
AW "a53,v217\n"
MF "v[128:131],a[216:219],a[76:79],v[128:131]\n"
AW "a54,v218\n"
AW "a55,v219\n"
MF "v[128:131],a[220:223],a[116:119],v[128:131]\n"
LD "v11,s[28:31],s82 offen offset:1024 lds\n"
MF "v[128:131],a[200:203],a[156:159],v[128:131]\n"
TR "a[216:217],v1 offset:32776\n"
"v_fma_f32 v168,v168,s54,v14\n"
MF "v[128:131],a[204:207],a[196:199],v[128:131]\n"
TR "a[218:219],v1 offset:33288\n"
"v_fma_f32 v169,v169,s54,v14\n"
MF "v[120:123],a[204:207],a[192:195],v[120:123]\n"
"v_fma_f32 v170,v170,s54,v14\n"
"v_fma_f32 v171,v171,s54,v14\n"
MF "v[120:123],a[200:203],a[152:155],v[120:123]\n"
TR "a[220:221],v1 offset:36872\n"
"v_fma_f32 v172,v172,s54,v14\n"
MF "v[112:115],a[200:203],a[148:151],v[112:115]\n"
TR "a[222:223],v1 offset:37384\n"
"v_fma_f32 v173,v173,s54,v14\n"
MF "v[112:115],a[204:207],a[188:191],v[112:115]\n"
"v_fma_f32 v174,v174,s54,v14\n"
"v_fma_f32 v175,v175,s54,v14\n"
MF "v[104:107],a[204:207],a[184:187],v[104:107]\n"
CV "v215,v174,v175\n"
CV "v214,v172,v173\n"
MF "v[104:107],a[200:203],a[144:147],v[104:107]\n"
CV "v213,v170,v171\n"
CV "v212,v168,v169\n"
MF "v[96:99],a[200:203],a[140:143],v[96:99]\n"
LD "v12,s[28:31],s82 offen offset:2048 lds\n"
MF "v[96:99],a[204:207],a[180:183],v[96:99]\n"
"s_waitcnt lgkmcnt(4)\n"
AW "a48,v212\n"
AW "a49,v213\n"
MF "v[60:63],a[208:211],a[60:63],v[60:63]\n"
TR "a[200:201],v1 offset:16384\n"
AW "a50,v214\n"
MF "v[60:63],a[212:215],a[100:103],v[60:63]\n"
TR "a[202:203],v1 offset:16896\n"
AW "a51,v215\n"
MF "v[68:71],a[212:215],a[104:107],v[68:71]\n"
"v_fma_f32 v160,v160,s54,v14\n"
"v_fma_f32 v161,v161,s54,v14\n"
MF "v[68:71],a[208:211],a[64:67],v[68:71]\n"
TR "a[204:205],v1 offset:20480\n"
"v_fma_f32 v162,v162,s54,v14\n"
MF "v[76:79],a[208:211],a[68:71],v[76:79]\n"
TR "a[206:207],v1 offset:20992\n"
"v_fma_f32 v163,v163,s54,v14\n"
MF "v[76:79],a[212:215],a[108:111],v[76:79]\n"
"v_fma_f32 v164,v164,s54,v14\n"
"v_fma_f32 v165,v165,s54,v14\n"
MF "v[84:87],a[212:215],a[112:115],v[84:87]\n"
"v_fma_f32 v166,v166,s54,v14\n"
"v_fma_f32 v167,v167,s54,v14\n"
MF "v[84:87],a[208:211],a[72:75],v[84:87]\n"
LD "v13,s[28:31],s82 offen offset:3072 lds\n"
MF "v[92:95],a[208:211],a[76:79],v[92:95]\n"
CV "v211,v166,v167\n"
CV "v210,v164,v165\n"
MF "v[92:95],a[212:215],a[116:119],v[92:95]\n"
"s_waitcnt lgkmcnt(4)\n"
CV "v209,v162,v163\n"
CV "v208,v160,v161\n"
MF "v[60:63],a[216:219],a[140:143],v[60:63]\n"
TR "a[208:209],v1 offset:32768\n"
AW "a44,v208\n"
MF "v[60:63],a[220:223],a[180:183],v[60:63]\n"
TR "a[210:211],v1 offset:33280\n"
AW "a45,v209\n"
MF "v[68:71],a[220:223],a[184:187],v[68:71]\n"
AW "a46,v210\n"
AW "a47,v211\n"
MF "v[68:71],a[216:219],a[144:147],v[68:71]\n"
TR "a[212:213],v1 offset:36864\n"
"v_fma_f32 v152,v152,s54,v14\n"
MF "v[76:79],a[216:219],a[148:151],v[76:79]\n"
TR "a[214:215],v1 offset:37376\n"
"v_fma_f32 v153,v153,s54,v14\n"
MF "v[76:79],a[220:223],a[188:191],v[76:79]\n"
"v_fma_f32 v154,v154,s54,v14\n"
"v_fma_f32 v155,v155,s54,v14\n"
MF "v[84:87],a[220:223],a[192:195],v[84:87]\n"
"v_fma_f32 v156,v156,s54,v14\n"
"v_fma_f32 v157,v157,s54,v14\n"
MF "v[84:87],a[216:219],a[152:155],v[84:87]\n"
"v_fma_f32 v158,v158,s54,v14\n"
"v_fma_f32 v159,v159,s54,v14\n"
MF "v[92:95],a[216:219],a[156:159],v[92:95]\n"
CV "v207,v158,v159\n"
CV "v206,v156,v157\n"
MF "v[92:95],a[220:223],a[196:199],v[92:95]\n"
"s_waitcnt lgkmcnt(0)\n"
CV "v205,v154,v155\n"
CV "v204,v152,v153\n"
MF "v[56:59],a[200:203],a[60:63],v[56:59]\n"
TR "a[216:217],v0 offset:16392\n"
AW "a40,v204\n"
MF "v[56:59],a[204:207],a[100:103],v[56:59]\n"
TR "a[218:219],v0 offset:16904\n"
AW "a41,v205\n"
MF "v[64:67],a[204:207],a[104:107],v[64:67]\n"
AW "a42,v206\n"
AW "a43,v207\n"
MF "v[64:67],a[200:203],a[64:67],v[64:67]\n"
TR "a[220:221],v0 offset:20488\n"
"v_fma_f32 v144,v144,s54,v14\n"
MF "v[72:75],a[200:203],a[68:71],v[72:75]\n"
TR "a[222:223],v0 offset:21000\n"
"v_fma_f32 v145,v145,s54,v14\n"
MF "v[72:75],a[204:207],a[108:111],v[72:75]\n"
"v_fma_f32 v146,v146,s54,v14\n"
"v_fma_f32 v147,v147,s54,v14\n"
MF "v[80:83],a[204:207],a[112:115],v[80:83]\n"
"v_fma_f32 v148,v148,s54,v14\n"
"v_fma_f32 v149,v149,s54,v14\n"
MF "v[80:83],a[200:203],a[72:75],v[80:83]\n"
"v_fma_f32 v150,v150,s54,v14\n"
"v_fma_f32 v151,v151,s54,v14\n"
MF "v[88:91],a[200:203],a[76:79],v[88:91]\n"
CV "v203,v150,v151\n"
CV "v202,v148,v149\n"
MF "v[88:91],a[204:207],a[116:119],v[88:91]\n"
CV "v201,v146,v147\n"
CV "v200,v144,v145\n"
MF "v[88:91],a[208:211],a[156:159],v[88:91]\n"
TR "a[200:201],v0 offset:32776\n"
AW "a16,v200\n"
MF "v[88:91],a[212:215],a[196:199],v[88:91]\n"
TR "a[202:203],v0 offset:33288\n"
AW "a17,v201\n"
MF "v[80:83],a[212:215],a[192:195],v[80:83]\n"
AW "a18,v202\n"
AW "a19,v203\n"
MF "v[80:83],a[208:211],a[152:155],v[80:83]\n"
TR "a[204:205],v0 offset:36872\n"
"v_fma_f32 v136,v136,s54,v14\n"
MF "v[72:75],a[208:211],a[148:151],v[72:75]\n"
TR "a[206:207],v0 offset:37384\n"
"v_fma_f32 v137,v137,s54,v14\n"
MF "v[72:75],a[212:215],a[188:191],v[72:75]\n"
"v_fma_f32 v138,v138,s54,v14\n"
"v_fma_f32 v139,v139,s54,v14\n"
MF "v[64:67],a[212:215],a[184:187],v[64:67]\n"
"v_fma_f32 v140,v140,s54,v14\n"
"v_fma_f32 v141,v141,s54,v14\n"
MF "v[64:67],a[208:211],a[144:147],v[64:67]\n"
"v_fma_f32 v142,v142,s54,v14\n"
"v_fma_f32 v143,v143,s54,v14\n"
MF "v[56:59],a[208:211],a[140:143],v[56:59]\n"
CV "v199,v142,v143\n"
CV "v198,v140,v141\n"
MF "v[56:59],a[212:215],a[180:183],v[56:59]\n"
"s_waitcnt lgkmcnt(4)\n"
CV "v197,v138,v139\n"
CV "v196,v136,v137\n"
MF "v[20:23],a[216:219],a[60:63],v[20:23]\n"
TR "a[208:209],v0 offset:16384\n"
AW "a12,v196\n"
MF "v[20:23],a[220:223],a[100:103],v[20:23]\n"
TR "a[210:211],v0 offset:16896\n"
AW "a13,v197\n"
MF "v[28:31],a[220:223],a[104:107],v[28:31]\n"
AW "a14,v198\n"
AW "a15,v199\n"
MF "v[28:31],a[216:219],a[64:67],v[28:31]\n"
TR "a[212:213],v0 offset:20480\n"
"v_fma_f32 v128,v128,s54,v14\n"
MF "v[36:39],a[216:219],a[68:71],v[36:39]\n"
TR "a[214:215],v0 offset:20992\n"
"v_fma_f32 v129,v129,s54,v14\n"
MF "v[36:39],a[220:223],a[108:111],v[36:39]\n"
"v_fma_f32 v130,v130,s54,v14\n"
"v_fma_f32 v131,v131,s54,v14\n"
MF "v[44:47],a[220:223],a[112:115],v[44:47]\n"
"v_fma_f32 v132,v132,s54,v14\n"
"v_fma_f32 v133,v133,s54,v14\n"
MF "v[44:47],a[216:219],a[72:75],v[44:47]\n"
"v_fma_f32 v134,v134,s54,v14\n"
"v_fma_f32 v135,v135,s54,v14\n"
MF "v[52:55],a[216:219],a[76:79],v[52:55]\n"
CV "v195,v134,v135\n"
CV "v194,v132,v133\n"
MF "v[52:55],a[220:223],a[116:119],v[52:55]\n"
"s_waitcnt lgkmcnt(4)\n"
CV "v193,v130,v131\n"
CV "v192,v128,v129\n"
MF "v[20:23],a[200:203],a[140:143],v[20:23]\n"
TR "a[216:217],v0 offset:32768\n"
AW "a8,v192\n"
MF "v[20:23],a[204:207],a[180:183],v[20:23]\n"
TR "a[218:219],v0 offset:33280\n"
AW "a9,v193\n"
MF "v[28:31],a[204:207],a[184:187],v[28:31]\n"
AW "a10,v194\n"
AW "a11,v195\n"
MF "v[28:31],a[200:203],a[144:147],v[28:31]\n"
TR "a[220:221],v0 offset:36864\n"
"v_fma_f32 v120,v120,s54,v14\n"
MF "v[36:39],a[200:203],a[148:151],v[36:39]\n"
TR "a[222:223],v0 offset:37376\n"
"v_fma_f32 v121,v121,s54,v14\n"
MF "v[36:39],a[204:207],a[188:191],v[36:39]\n"
"v_fma_f32 v122,v122,s54,v14\n"
"v_fma_f32 v123,v123,s54,v14\n"
MF "v[44:47],a[204:207],a[192:195],v[44:47]\n"
"v_fma_f32 v124,v124,s54,v14\n"
"v_fma_f32 v125,v125,s54,v14\n"
MF "v[44:47],a[200:203],a[152:155],v[44:47]\n"
"v_fma_f32 v126,v126,s54,v14\n"
"v_fma_f32 v127,v127,s54,v14\n"
MF "v[52:55],a[200:203],a[156:159],v[52:55]\n"
CV "v191,v126,v127\n"
CV "v190,v124,v125\n"
MF "v[52:55],a[204:207],a[196:199],v[52:55]\n"
"s_waitcnt lgkmcnt(0)\n"
"s_barrier\n"
MF "v[16:19],a[208:211],a[60:63],v[16:19]\n"
CV "v189,v122,v123\n"
CV "v188,v120,v121\n"
MF "v[16:19],a[212:215],a[100:103],v[16:19]\n"
AW "a4,v188\n"
AW "a5,v189\n"
MF "v[24:27],a[212:215],a[104:107],v[24:27]\n"
AW "a6,v190\n"
AW "a7,v191\n"
MF "v[24:27],a[208:211],a[64:67],v[24:27]\n"
"v_fma_f32 v112,v112,s54,v14\n"
"v_fma_f32 v113,v113,s54,v14\n"
MF "v[32:35],a[208:211],a[68:71],v[32:35]\n"
"v_fma_f32 v114,v114,s54,v14\n"
"v_fma_f32 v115,v115,s54,v14\n"
MF "v[32:35],a[212:215],a[108:111],v[32:35]\n"
"v_fma_f32 v116,v116,s54,v14\n"
"v_fma_f32 v117,v117,s54,v14\n"
MF "v[40:43],a[212:215],a[112:115],v[40:43]\n"
"v_fma_f32 v118,v118,s54,v14\n"
"v_fma_f32 v119,v119,s54,v14\n"
MF "v[40:43],a[208:211],a[72:75],v[40:43]\n"
CV "v187,v118,v119\n"
CV "v186,v116,v117\n"
MF "v[48:51],a[208:211],a[76:79],v[48:51]\n"
CV "v185,v114,v115\n"
CV "v184,v112,v113\n"
MF "v[48:51],a[212:215],a[116:119],v[48:51]\n"
AW "a0,v184\n"
AW "a1,v185\n"
MF "v[48:51],a[216:219],a[156:159],v[48:51]\n"
AW "a2,v186\n"
AW "a3,v187\n"
MF "v[48:51],a[220:223],a[196:199],v[48:51]\n"
"v_fma_f32 v104,v104,s54,v14\n"
"v_fma_f32 v105,v105,s54,v14\n"
MF "v[40:43],a[220:223],a[192:195],v[40:43]\n"
"v_fma_f32 v106,v106,s54,v14\n"
"v_fma_f32 v107,v107,s54,v14\n"
MF "v[40:43],a[216:219],a[152:155],v[40:43]\n"
"v_fma_f32 v108,v108,s54,v14\n"
"v_fma_f32 v109,v109,s54,v14\n"
MF "v[32:35],a[216:219],a[148:151],v[32:35]\n"
"v_fma_f32 v110,v110,s54,v14\n"
"v_fma_f32 v111,v111,s54,v14\n"
MF "v[32:35],a[220:223],a[188:191],v[32:35]\n"
CV "v183,v110,v111\n"
CV "v182,v108,v109\n"
MF "v[24:27],a[220:223],a[184:187],v[24:27]\n"
CV "v181,v106,v107\n"
CV "v180,v104,v105\n"
MF "v[24:27],a[216:219],a[144:147],v[24:27]\n"
AW "a36,v180\n"
AW "a37,v181\n"
MF "v[16:19],a[216:219],a[140:143],v[16:19]\n"
AW "a38,v182\n"
AW "a39,v183\n"
MF "v[16:19],a[220:223],a[180:183],v[16:19]\n"
"s_mov_b32 s57,1\n"
"s_mov_b32 s69,99\n"
AR "v0,a240\n"
AR "v2,a241\n"
AR "v4,a242\n"
AR "v6,a243\n"
"v_xor_b32 v1,64,v0\n"
"v_xor_b32 v3,64,v2\n"
"v_xor_b32 v5,64,v4\n"
"v_add_u32 v11,97280,v6\n"
"v_add_u32 v12,194560,v6\n"
"v_add_u32 v13,291840,v6\n"
"s_mov_b64 vcc,-1\n"
"s_waitcnt vmcnt(10) lgkmcnt(0)\n"
"s_barrier\n"
"s_nop 0\n"
TR "a[200:201],v0\n"
TR "a[202:203],v0 offset:512\n"
TR "a[204:205],v0 offset:4096\n"
TR "a[206:207],v0 offset:4608\n"
"ds_read_b128 a[60:63],v2\n"
"ds_read_b128 a[100:103],v3\n"
"ds_read_b128 a[64:67],v2 offset:2048\n"
"ds_read_b128 a[104:107],v3 offset:2048\n"
"ds_read_b128 a[68:71],v2 offset:4096\n"
"ds_read_b128 a[108:111],v3 offset:4096\n"
"ds_read_b128 a[72:75],v2 offset:6144\n"
"ds_read_b128 a[112:115],v3 offset:6144\n"
"ds_read_b128 a[76:79],v2 offset:8192\n"
"ds_read_b128 a[116:119],v3 offset:8192\n"
TR "a[208:209],v0 offset:8\n"
TR "a[210:211],v0 offset:520\n"
TR "a[212:213],v0 offset:4104\n"
TR "a[214:215],v0 offset:4616\n"
"v_pk_fma_f32 v[96:97],v[96:97],s[54:55],v[14:15] op_sel_hi:[1,1,0]\n"
"v_pk_fma_f32 v[98:99],v[98:99],s[54:55],v[14:15] op_sel_hi:[1,1,0]\n"
"v_pk_fma_f32 v[100:101],v[100:101],s[54:55],v[14:15] op_sel_hi:[1,1,0]\n"
"v_pk_fma_f32 v[102:103],v[102:103],s[54:55],v[14:15] op_sel_hi:[1,1,0]\n"
CV "v179,v102,v103\n"
CV "v178,v100,v101\n"
CV "v177,v98,v99\n"
".Lpg14c:\n"
"s_cbranch_execz .Lpg2a\n"
"s_nop 0\n"
CV "v176,v96,v97\n"
AW "a32,v176\n"
AW "a33,v177\n"
AW "a34,v178\n"
AW "a35,v179\n"
"v_pk_fma_f32 v[88:89],v[88:89],s[54:55],v[14:15] op_sel_hi:[1,1,0]\n"
"v_pk_fma_f32 v[90:91],v[90:91],s[54:55],v[14:15] op_sel_hi:[1,1,0]\n"
"v_pk_fma_f32 v[92:93],v[92:93],s[54:55],v[14:15] op_sel_hi:[1,1,0]\n"
"v_pk_fma_f32 v[94:95],v[94:95],s[54:55],v[14:15] op_sel_hi:[1,1,0]\n"
CV "v175,v94,v95\n"
CV "v174,v92,v93\n"
CV "v173,v90,v91\n"
CV "v172,v88,v89\n"
AW "a28,v172\n"
AW "a29,v173\n"
AW "a30,v174\n"
AW "a31,v175\n"
"v_pk_fma_f32 v[80:81],v[80:81],s[54:55],v[14:15] op_sel_hi:[1,1,0]\n"
"v_pk_fma_f32 v[82:83],v[82:83],s[54:55],v[14:15] op_sel_hi:[1,1,0]\n"
"v_pk_fma_f32 v[84:85],v[84:85],s[54:55],v[14:15] op_sel_hi:[1,1,0]\n"
"v_pk_fma_f32 v[86:87],v[86:87],s[54:55],v[14:15] op_sel_hi:[1,1,0]\n"
CV "v171,v86,v87\n"
CV "v170,v84,v85\n"
CV "v169,v82,v83\n"
CV "v168,v80,v81\n"
AW "a24,v168\n"
AW "a25,v169\n"
AW "a26,v170\n"
AW "a27,v171\n"
"v_pk_fma_f32 v[72:73],v[72:73],s[54:55],v[14:15] op_sel_hi:[1,1,0]\n"
"v_pk_fma_f32 v[74:75],v[74:75],s[54:55],v[14:15] op_sel_hi:[1,1,0]\n"
"v_pk_fma_f32 v[76:77],v[76:77],s[54:55],v[14:15] op_sel_hi:[1,1,0]\n"
"v_pk_fma_f32 v[78:79],v[78:79],s[54:55],v[14:15] op_sel_hi:[1,1,0]\n"
CV "v167,v78,v79\n"
CV "v166,v76,v77\n"
CV "v165,v74,v75\n"
CV "v164,v72,v73\n"
AW "a20,v164\n"
AW "a21,v165\n"
AW "a22,v166\n"
AW "a23,v167\n"
"v_pk_fma_f32 v[64:65],v[64:65],s[54:55],v[14:15] op_sel_hi:[1,1,0]\n"
"v_pk_fma_f32 v[66:67],v[66:67],s[54:55],v[14:15] op_sel_hi:[1,1,0]\n"
"v_pk_fma_f32 v[68:69],v[68:69],s[54:55],v[14:15] op_sel_hi:[1,1,0]\n"
"v_pk_fma_f32 v[70:71],v[70:71],s[54:55],v[14:15] op_sel_hi:[1,1,0]\n"
CV "v163,v70,v71\n"
CV "v162,v68,v69\n"
CV "v161,v66,v67\n"
CV "v160,v64,v65\n"
AW "a176,v160\n"
AW "a177,v161\n"
AW "a178,v162\n"
AW "a179,v163\n"
"v_pk_fma_f32 v[56:57],v[56:57],s[54:55],v[14:15] op_sel_hi:[1,1,0]\n"
"v_pk_fma_f32 v[58:59],v[58:59],s[54:55],v[14:15] op_sel_hi:[1,1,0]\n"
"v_pk_fma_f32 v[60:61],v[60:61],s[54:55],v[14:15] op_sel_hi:[1,1,0]\n"
"v_pk_fma_f32 v[62:63],v[62:63],s[54:55],v[14:15] op_sel_hi:[1,1,0]\n"
CV "v159,v62,v63\n"
CV "v158,v60,v61\n"
CV "v157,v58,v59\n"
CV "v156,v56,v57\n"
AW "a172,v156\n"
AW "a173,v157\n"
AW "a174,v158\n"
AW "a175,v159\n"
"v_pk_mul_f32 v[48:49],v[48:49],s[54:55]\n"
"v_pk_mul_f32 v[50:51],v[50:51],s[54:55]\n"
"v_pk_mul_f32 v[52:53],v[52:53],s[54:55]\n"
"v_pk_mul_f32 v[54:55],v[54:55],s[54:55]\n"
CV "v155,v54,v55\n"
CV "v154,v52,v53\n"
CV "v153,v50,v51\n"
CV "v152,v48,v49\n"
AW "a168,v152\n"
AW "a169,v153\n"
AW "a170,v154\n"
AW "a171,v155\n"
"v_pk_mul_f32 v[40:41],v[40:41],s[54:55]\n"
"v_pk_mul_f32 v[42:43],v[42:43],s[54:55]\n"
"v_pk_mul_f32 v[44:45],v[44:45],s[54:55]\n"
"v_pk_mul_f32 v[46:47],v[46:47],s[54:55]\n"
CV "v151,v46,v47\n"
CV "v150,v44,v45\n"
CV "v149,v42,v43\n"
CV "v148,v40,v41\n"
AW "a120,v148\n"
AW "a121,v149\n"
AW "a122,v150\n"
AW "a123,v151\n"
"v_pk_mul_f32 v[32:33],v[32:33],s[54:55]\n"
"v_pk_mul_f32 v[34:35],v[34:35],s[54:55]\n"
"v_pk_mul_f32 v[36:37],v[36:37],s[54:55]\n"
"v_pk_mul_f32 v[38:39],v[38:39],s[54:55]\n"
CV "v147,v38,v39\n"
CV "v146,v36,v37\n"
CV "v145,v34,v35\n"
CV "v144,v32,v33\n"
AW "a96,v144\n"
AW "a97,v145\n"
AW "a98,v146\n"
AW "a99,v147\n"
"v_pk_mul_f32 v[24:25],v[24:25],s[54:55]\n"
"v_pk_mul_f32 v[26:27],v[26:27],s[54:55]\n"
"v_pk_mul_f32 v[28:29],v[28:29],s[54:55]\n"
"v_pk_mul_f32 v[30:31],v[30:31],s[54:55]\n"
CV "v143,v30,v31\n"
CV "v142,v28,v29\n"
CV "v141,v26,v27\n"
CV "v140,v24,v25\n"
AW "a92,v140\n"
AW "a93,v141\n"
AW "a94,v142\n"
AW "a95,v143\n"
"v_pk_mul_f32 v[16:17],v[16:17],s[54:55]\n"
"v_pk_mul_f32 v[18:19],v[18:19],s[54:55]\n"
"v_pk_mul_f32 v[20:21],v[20:21],s[54:55]\n"
"v_pk_mul_f32 v[22:23],v[22:23],s[54:55]\n"
CV "v139,v22,v23\n"
CV "v138,v20,v21\n"
CV "v137,v18,v19\n"
CV "v136,v16,v17\n"
AW "a88,v136\n"
AW "a89,v137\n"
AW "a90,v138\n"
AW "a91,v139\n"
"s_waitcnt lgkmcnt(0)\n"
"s_branch .Lq0\n"
".Lpgpsl_b:\n"
"s_cbranch_execz .Lpg3a\n"
".Lpgpsl_d:\n"
"s_cbranch_execz .Lisr2\n"
".Lpsl:\n"
"s_sleep 1\n"
".Lpsub:\n"
"s_memrealtime s[100:101]\n"
"s_waitcnt lgkmcnt(0)\n"
"s_sub_u32 s101,s21,s100\n"
"s_cmp_gt_i32 s101,0\n"
"s_cbranch_scc1 .Lpsl\n"
"s_add_u32 s21,s100,s97\n"
"s_setpc_b64 s[52:53]\n"
".Lstub_6:\n"
"v_cmp_le_f32 vcc,0x47800000,v10\n"
"s_cmp_eq_u32 s61,1\n"
"s_cbranch_scc0 .Lhook_6\n"
"s_sub_u32 s58,s71,s70\n"
"s_add_u32 s72,s72,s58\n"
"s_add_u32 s16,s16,s58\n"
"s_add_u32 s17,s17,s58\n"
"s_cmp_eq_u32 s57,0\n"
"s_cselect_b32 s34,0,s34\n"
"s_cselect_b32 s64,163840,s64\n"
"s_mov_b64 exec,0\n"
"s_bitcmp1_b32 s23,12\n"
"s_cbranch_scc1 .Lisy3\n"
"s_cmp_eq_u32 s56,0\n"
"s_cbranch_scc1 .Lpgq0_f\n"
"s_cmp_eq_u32 s56,1\n"
"s_cbranch_scc1 .Lpg4f\n"
"s_cmp_eq_u32 s56,2\n"
"s_cbranch_scc1 .Lpg5f\n"
"s_branch .Lpg6f\n"
".Lisy3:\n"
"s_cmp_eq_u32 s56,0\n"
"s_cbranch_scc1 .Lpgep_g\n"
"s_cmp_eq_u32 s56,1\n"
"s_cbranch_scc1 .Lpg15g\n"
"s_cmp_eq_u32 s56,2\n"
"s_cbranch_scc1 .Lpgt0e_g\n"
"s_branch .Lisr3\n"
".Lisr3:\n"
"s_mov_b64 exec,-1\n"
".Lisx3:\n"
".Lhook_6:\n"
"s_cmp_eq_u32 s77,0\n"
"s_cbranch_scc0 .Lnop_6\n"
"s_call_b64 s[52:53],.Lpsub\n"
".Lnop_6:\n"
"s_cbranch_vccz .Lback_6\n"
"s_call_b64 s[52:53],.Lslow_1\n"
"s_branch .Lback_6\n"
".Lstub_7:\n"
"v_cmp_le_f32 vcc,0x47800000,v10\n"
"s_cmp_eq_u32 s61,1\n"
"s_cbranch_scc0 .Lhook_7\n"
"s_sub_u32 s58,s71,s70\n"
"s_add_u32 s73,s73,s58\n"
"s_add_u32 s18,s18,s58\n"
"s_add_u32 s19,s19,s58\n"
"s_cmp_eq_u32 s57,0\n"
"s_cselect_b32 s38,0,s38\n"
"s_cselect_b32 s65,163840,s65\n"
".Lhook_7:\n"
"s_cmp_eq_u32 s77,0\n"
"s_cbranch_scc0 .Lnop_7\n"
"s_call_b64 s[52:53],.Lpsub\n"
"s_cmp_eq_u32 s61,4\n"
"s_cselect_b32 s77,0x47800000,s77\n"
"s_cselect_b32 s95,0x47800000,s95\n"
".Lnop_7:\n"
"s_cbranch_vccz .Lback_7\n"
"s_call_b64 s[52:53],.Lslow_0\n"
"s_branch .Lback_7\n"
".Lstub_8:\n"
"s_call_b64 s[52:53],.Lslow_1\n"
"s_branch .Lback_8\n"
".Lstub_11:\n"
"v_cmp_le_f32 vcc,0x47800000,v10\n"
"s_cmp_eq_u32 s77,0\n"
"s_cbranch_scc0 .Lnop_11\n"
"s_call_b64 s[52:53],.Lpsub\n"
".Lnop_11:\n"
"s_cbranch_vccz .Lback_11\n"
"s_call_b64 s[52:53],.Lslow_1\n"
"s_branch .Lback_11\n"
".Lstub_12:\n"
"v_cmp_le_f32 vcc,0x47800000,v10\n"
"s_cmp_eq_u32 s77,0\n"
"s_cbranch_scc0 .Lnop_12\n"
"s_call_b64 s[52:53],.Lpsub\n"
".Lnop_12:\n"
"s_cbranch_vccz .Lback_12\n"
"s_call_b64 s[52:53],.Lslow_0\n"
"s_branch .Lback_12\n"
".Lstub_13:\n"
"v_cmp_le_f32 vcc,0x47800000,v10\n"
"s_cmp_eq_u32 s61,1\n"
"s_cbranch_scc0 .Lhook_13\n"
"s_mov_b64 exec,0\n"
"s_cmp_eq_u32 s56,0\n"
"s_cbranch_scc1 .Lpg24h\n"
"s_cmp_eq_u32 s56,1\n"
"s_cbranch_scc1 .Lpg23h\n"
"s_cmp_eq_u32 s56,2\n"
"s_cbranch_scc1 .Lpg22h\n"
"s_branch .Lpg21h\n"
".Lisr4:\n"
"s_mov_b64 exec,-1\n"
".Lisx4:\n"
"s_mov_b32 s97,0\n"
".Lhook_13:\n"
"s_cmp_eq_u32 s77,0\n"
"s_cbranch_scc0 .Lnop_13\n"
"s_call_b64 s[52:53],.Lpsub\n"
".Lnop_13:\n"
"s_cbranch_vccz .Lback_13\n"
"s_call_b64 s[52:53],.Lslow_1\n"
"s_branch .Lback_13\n"
".Lstub_14:\n"
"v_cmp_le_f32 vcc,0x47800000,v10\n"
"s_cmp_eq_u32 s77,0\n"
"s_cbranch_scc0 .Lnop_14\n"
"s_call_b64 s[52:53],.Lpsub\n"
".Lnop_14:\n"
"s_cbranch_vccz .Lback_14\n"
"s_call_b64 s[52:53],.Lslow_0\n"
"s_branch .Lback_14\n"
".Lstub_15:\n"
"v_cmp_le_f32 vcc,0x47800000,v10\n"
"s_cmp_eq_u32 s61,1\n"
"s_cbranch_scc0 .Lhook_15\n"
"s_mov_b32 s34,0\n"
"s_mov_b32 s64,163840\n"
".Lhook_15:\n"
"s_cmp_eq_u32 s77,0\n"
"s_cbranch_scc0 .Lnop_15\n"
"s_call_b64 s[52:53],.Lpsub\n"
".Lnop_15:\n"
"s_cbranch_vccz .Lback_15\n"
"s_call_b64 s[52:53],.Lslow_1\n"
"s_branch .Lback_15\n"
".Lstub_16:\n"
"v_cmp_le_f32 vcc,0x47800000,v10\n"
"s_cmp_eq_u32 s77,0\n"
"s_cbranch_scc0 .Lnop_16\n"
"s_call_b64 s[52:53],.Lpsub\n"
".Lnop_16:\n"
"s_cbranch_vccz .Lback_16\n"
"s_call_b64 s[52:53],.Lslow_0\n"
"s_branch .Lback_16\n"
".Lstub_17:\n"
"s_call_b64 s[52:53],.Lslow_1\n"
"s_branch .Lback_17\n"
".Lackspin_q:\n"
"s_cmp_eq_u32 s98,0\n"
"s_cbranch_scc1 .Lackok_q\n"
"s_sub_u32 s98,s98,1\n"
"s_sleep 8\n"
"v_bfe_u32 v14,v9,4,5\n"
"v_lshlrev_b32 v14,6,v14\n"
"s_lshl_b32 s58,s78,2\n"
"s_add_u32 s58,s58,s86\n"
"buffer_load_dword v10,v14,s[40:43],s58 offen offset:128 sc1\n"
"s_waitcnt vmcnt(0)\n"
"s_branch .Lack_q\n"
".Lpgep_g:\n"
"s_cbranch_execz .Lisr3\n"
".Lep:\n"
"s_lshl_b32 s58,s56,10\n"
"s_add_u32 s58,s58,73728\n"
"v_add_u32 v120,s58,v9\n"
"v_add_u32 v121,65536,v120\n"
"s_sub_u32 s59,s71,s70\n"
"v_mov_b32 v10,1.0\n"
TR "v[136:137],v2 offset:24576\n"
TR "v[138:139],v2 offset:30720\n"
"v_mfma_f32_16x16x4_f32 v[16:19],v10,v57,0\n"
TR "v[140:141],v3 offset:24576\n"
TR "v[142:143],v3 offset:30720\n"
TR "v[144:145],v4 offset:24576\n"
TR "v[146:147],v4 offset:30720\n"
TR "v[148:149],v5 offset:24576\n"
TR "v[150:151],v5 offset:30720\n"
"v_mfma_f32_16x16x4_f32 v[20:23],v10,v58,0\n"
TR "v[152:153],v2 offset:25600\n"
TR "v[154:155],v2 offset:31744\n"
TR "v[156:157],v3 offset:25600\n"
TR "v[158:159],v3 offset:31744\n"
TR "v[160:161],v4 offset:25600\n"
TR "v[162:163],v4 offset:31744\n"
"v_mfma_f32_16x16x4_f32 v[24:27],v10,v59,0\n"
TR "v[164:165],v5 offset:25600\n"
TR "v[166:167],v5 offset:31744\n"
TR "v[168:169],v2 offset:26624\n"
TR "v[170:171],v2 offset:32768\n"
TR "v[172:173],v3 offset:26624\n"
TR "v[174:175],v3 offset:32768\n"
"v_mfma_f32_16x16x4_f32 v[28:31],v10,v60,0\n"
TR "v[176:177],v4 offset:26624\n"
TR "v[178:179],v4 offset:32768\n"
TR "v[180:181],v5 offset:26624\n"
TR "v[182:183],v5 offset:32768\n"
"v_mfma_f32_16x16x4_f32 v[32:35],v10,v61,0\n"
"s_add_u32 m0,s65,49152\n"
"s_nop 0\n"
LD "v8,s[36:39],s73 offen offset:3072 nt lds\n"
"s_add_u32 m0,s65,50048\n"
"s_nop 0\n"
LD "v8,s[36:39],s73 offen offset:3200 nt lds\n"
"s_waitcnt lgkmcnt(15)\n"
MF "a[0:3],v[136:139],v[92:95],a[0:3]\n"
MF "a[20:23],v[140:143],v[92:95],a[20:23]\n"
"s_add_u32 m0,s65,50944\n"
"s_nop 0\n"
LD "v8,s[36:39],s73 offen offset:3328 nt lds\n"
"s_sub_u32 s74,s96,s16\n"
"s_add_u32 s74,s74,s71\n"
"s_add_u32 s20,s20,s59\n"
"s_mov_b32 s70,s71\n"
LD "v[232:235],v9,s[40:43],s75 offen sc1\n"
"v_rcp_f32 v57,v16\n"
"v_rcp_f32 v58,v20\n"
"v_rcp_f32 v59,v24\n"
"v_rcp_f32 v60,v28\n"
"v_rcp_f32 v61,v32\n"
MF "a[4:7],v[136:139],v[96:99],a[4:7]\n"
"v_mul_f32_e64 v52,-s13,v57\n"
"v_mul_f32_e64 v53,-s13,v58\n"
"v_mul_f32_e64 v54,-s13,v59\n"
"v_mul_f32_e64 v55,-s13,v60\n"
"v_mul_f32_e64 v56,-s13,v61\n"
AR "v72,a0\n"
AR "v73,a1\n"
AR "v74,a2\n"
MF "a[24:27],v[140:143],v[96:99],a[24:27]\n"
AR "v75,a3\n"
AR "v76,a20\n"
AR "v77,a21\n"
AR "v78,a22\n"
AR "v79,a23\n"
"v_pk_fma_f32 v[72:73],v[72:73],v[56:57],v[52:53] op_sel:[0,1,0] op_sel_hi:[1,1,0]\n"
"v_pk_fma_f32 v[74:75],v[74:75],v[56:57],v[52:53] op_sel:[0,1,0] op_sel_hi:[1,1,0]\n"
"v_pk_fma_f32 v[76:77],v[76:77],v[56:57],v[52:53] op_sel:[0,1,0] op_sel_hi:[1,1,0]\n"
"v_pk_fma_f32 v[78:79],v[78:79],v[56:57],v[52:53] op_sel:[0,1,0] op_sel_hi:[1,1,0]\n"
CV "v112,v72,v73\n"
CV "v113,v74,v75\n"
CV "v114,v76,v77\n"
CV "v115,v78,v79\n"
MF "a[8:11],v[136:139],v[100:103],a[8:11]\n"
"ds_read_b128 v[184:187],v120 offset:24576\n"
"ds_write_b128 v120,v[112:115] offset:24576\n"
LD "v[236:239],v9,s[40:43],s75 offen offset:1024 sc1\n"
AR "v80,a4\n"
AR "v81,a5\n"
AR "v82,a6\n"
MF "a[28:31],v[140:143],v[100:103],a[28:31]\n"
AR "v83,a7\n"
AR "v84,a24\n"
AR "v85,a25\n"
AR "v86,a26\n"
AR "v87,a27\n"
"v_pk_fma_f32 v[80:81],v[80:81],v[58:59],v[52:53] op_sel:[0,0,1] op_sel_hi:[1,0,1]\n"
"v_pk_fma_f32 v[82:83],v[82:83],v[58:59],v[52:53] op_sel:[0,0,1] op_sel_hi:[1,0,1]\n"
"v_pk_fma_f32 v[84:85],v[84:85],v[58:59],v[52:53] op_sel:[0,0,1] op_sel_hi:[1,0,1]\n"
"v_pk_fma_f32 v[86:87],v[86:87],v[58:59],v[52:53] op_sel:[0,0,1] op_sel_hi:[1,0,1]\n"
CV "v116,v80,v81\n"
CV "v117,v82,v83\n"
CV "v118,v84,v85\n"
CV "v119,v86,v87\n"
MF "a[12:15],v[136:139],v[104:107],a[12:15]\n"
"ds_read_b128 v[188:191],v120 offset:28672\n"
"ds_write_b128 v120,v[116:119] offset:28672\n"
AR "v72,a8\n"
AR "v73,a9\n"
AR "v74,a10\n"
MF "a[32:35],v[140:143],v[104:107],a[32:35]\n"
AR "v75,a11\n"
AR "v76,a28\n"
AR "v77,a29\n"
AR "v78,a30\n"
AR "v79,a31\n"
"v_pk_fma_f32 v[72:73],v[72:73],v[58:59],v[54:55] op_sel:[0,1,0] op_sel_hi:[1,1,0]\n"
"v_pk_fma_f32 v[74:75],v[74:75],v[58:59],v[54:55] op_sel:[0,1,0] op_sel_hi:[1,1,0]\n"
"v_pk_fma_f32 v[76:77],v[76:77],v[58:59],v[54:55] op_sel:[0,1,0] op_sel_hi:[1,1,0]\n"
"v_pk_fma_f32 v[78:79],v[78:79],v[58:59],v[54:55] op_sel:[0,1,0] op_sel_hi:[1,1,0]\n"
CV "v112,v72,v73\n"
CV "v113,v74,v75\n"
CV "v114,v76,v77\n"
CV "v115,v78,v79\n"
MF "a[16:19],v[136:139],v[108:111],a[16:19]\n"
"ds_read_b128 v[192:195],v120 offset:32768\n"
"ds_write_b128 v120,v[112:115] offset:32768\n"
"s_waitcnt lgkmcnt(0)\n"
"s_barrier\n"
LD "v[240:243],v9,s[40:43],s75 offen offset:2048 sc1\n"
AR "v80,a12\n"
AR "v81,a13\n"
AR "v82,a14\n"
MF "a[36:39],v[140:143],v[108:111],a[36:39]\n"
AR "v83,a15\n"
AR "v84,a32\n"
AR "v85,a33\n"
AR "v86,a34\n"
AR "v87,a35\n"
"v_pk_fma_f32 v[80:81],v[80:81],v[60:61],v[54:55] op_sel:[0,0,1] op_sel_hi:[1,0,1]\n"
"v_pk_fma_f32 v[82:83],v[82:83],v[60:61],v[54:55] op_sel:[0,0,1] op_sel_hi:[1,0,1]\n"
"v_pk_fma_f32 v[84:85],v[84:85],v[60:61],v[54:55] op_sel:[0,0,1] op_sel_hi:[1,0,1]\n"
"v_pk_fma_f32 v[86:87],v[86:87],v[60:61],v[54:55] op_sel:[0,0,1] op_sel_hi:[1,0,1]\n"
CV "v116,v80,v81\n"
CV "v117,v82,v83\n"
CV "v118,v84,v85\n"
CV "v119,v86,v87\n"
MF "a[40:43],v[144:147],v[92:95],a[40:43]\n"
"ds_read_b128 v[196:199],v120 offset:36864\n"
"ds_write_b128 v120,v[116:119] offset:36864\n"
LD "v[244:247],v9,s[40:43],s75 offen offset:3072 sc1\n"
AR "v72,a16\n"
AR "v73,a17\n"
AR "v74,a18\n"
MF "a[60:63],v[148:151],v[92:95],a[60:63]\n"
AR "v75,a19\n"
AR "v76,a36\n"
AR "v77,a37\n"
AR "v78,a38\n"
AR "v79,a39\n"
"v_pk_fma_f32 v[72:73],v[72:73],v[60:61],v[56:57] op_sel:[0,1,0] op_sel_hi:[1,1,0]\n"
"v_pk_fma_f32 v[74:75],v[74:75],v[60:61],v[56:57] op_sel:[0,1,0] op_sel_hi:[1,1,0]\n"
"v_pk_fma_f32 v[76:77],v[76:77],v[60:61],v[56:57] op_sel:[0,1,0] op_sel_hi:[1,1,0]\n"
"v_pk_fma_f32 v[78:79],v[78:79],v[60:61],v[56:57] op_sel:[0,1,0] op_sel_hi:[1,1,0]\n"
CV "v112,v72,v73\n"
CV "v113,v74,v75\n"
CV "v114,v76,v77\n"
CV "v115,v78,v79\n"
MF "a[44:47],v[144:147],v[96:99],a[44:47]\n"
"ds_read_b128 v[200:203],v120 offset:40960\n"
"ds_write_b128 v120,v[112:115] offset:40960\n"
AR "v80,a40\n"
AR "v81,a41\n"
AR "v82,a42\n"
MF "a[64:67],v[148:151],v[96:99],a[64:67]\n"
AR "v83,a43\n"
AR "v84,a60\n"
AR "v85,a61\n"
AR "v86,a62\n"
AR "v87,a63\n"
"v_pk_fma_f32 v[80:81],v[80:81],v[56:57],v[52:53] op_sel:[0,1,0] op_sel_hi:[1,1,0]\n"
"v_pk_fma_f32 v[82:83],v[82:83],v[56:57],v[52:53] op_sel:[0,1,0] op_sel_hi:[1,1,0]\n"
"v_pk_fma_f32 v[84:85],v[84:85],v[56:57],v[52:53] op_sel:[0,1,0] op_sel_hi:[1,1,0]\n"
"v_pk_fma_f32 v[86:87],v[86:87],v[56:57],v[52:53] op_sel:[0,1,0] op_sel_hi:[1,1,0]\n"
CV "v116,v80,v81\n"
CV "v117,v82,v83\n"
CV "v118,v84,v85\n"
CV "v119,v86,v87\n"
MF "a[48:51],v[144:147],v[100:103],a[48:51]\n"
"ds_read_b128 v[204:207],v120 offset:45056\n"
"ds_write_b128 v120,v[116:119] offset:45056\n"
"s_add_u32 s58,s75,4096\n"
LD "v[248:251],v9,s[40:43],s58 offen sc1\n"
AR "v72,a44\n"
AR "v73,a45\n"
AR "v74,a46\n"
MF "a[68:71],v[148:151],v[100:103],a[68:71]\n"
AR "v75,a47\n"
AR "v76,a64\n"
AR "v77,a65\n"
AR "v78,a66\n"
AR "v79,a67\n"
"v_pk_fma_f32 v[72:73],v[72:73],v[58:59],v[52:53] op_sel:[0,0,1] op_sel_hi:[1,0,1]\n"
"v_pk_fma_f32 v[74:75],v[74:75],v[58:59],v[52:53] op_sel:[0,0,1] op_sel_hi:[1,0,1]\n"
"v_pk_fma_f32 v[76:77],v[76:77],v[58:59],v[52:53] op_sel:[0,0,1] op_sel_hi:[1,0,1]\n"
"v_pk_fma_f32 v[78:79],v[78:79],v[58:59],v[52:53] op_sel:[0,0,1] op_sel_hi:[1,0,1]\n"
CV "v112,v72,v73\n"
CV "v113,v74,v75\n"
CV "v114,v76,v77\n"
CV "v115,v78,v79\n"
MF "a[52:55],v[144:147],v[104:107],a[52:55]\n"
"ds_read_b128 v[208:211],v120 offset:49152\n"
"ds_write_b128 v120,v[112:115] offset:49152\n"
LD "v[252:255],v9,s[40:43],s58 offen offset:1024 sc1\n"
AR "v80,a48\n"
AR "v81,a49\n"
AR "v82,a50\n"
MF "a[72:75],v[148:151],v[104:107],a[72:75]\n"
AR "v83,a51\n"
AR "v84,a68\n"
AR "v85,a69\n"
AR "v86,a70\n"
AR "v87,a71\n"
"v_pk_fma_f32 v[80:81],v[80:81],v[58:59],v[54:55] op_sel:[0,1,0] op_sel_hi:[1,1,0]\n"
"v_pk_fma_f32 v[82:83],v[82:83],v[58:59],v[54:55] op_sel:[0,1,0] op_sel_hi:[1,1,0]\n"
"v_pk_fma_f32 v[84:85],v[84:85],v[58:59],v[54:55] op_sel:[0,1,0] op_sel_hi:[1,1,0]\n"
"v_pk_fma_f32 v[86:87],v[86:87],v[58:59],v[54:55] op_sel:[0,1,0] op_sel_hi:[1,1,0]\n"
CV "v116,v80,v81\n"
CV "v117,v82,v83\n"
CV "v118,v84,v85\n"
CV "v119,v86,v87\n"
MF "a[56:59],v[144:147],v[108:111],a[56:59]\n"
"ds_read_b128 v[212:215],v120 offset:53248\n"
"ds_write_b128 v120,v[116:119] offset:53248\n"
AR "v72,a52\n"
AR "v73,a53\n"
AR "v74,a54\n"
MF "a[76:79],v[148:151],v[108:111],a[76:79]\n"
AR "v75,a55\n"
AR "v76,a72\n"
AR "v77,a73\n"
AR "v78,a74\n"
AR "v79,a75\n"
"v_pk_fma_f32 v[72:73],v[72:73],v[60:61],v[54:55] op_sel:[0,0,1] op_sel_hi:[1,0,1]\n"
"v_pk_fma_f32 v[74:75],v[74:75],v[60:61],v[54:55] op_sel:[0,0,1] op_sel_hi:[1,0,1]\n"
"v_pk_fma_f32 v[76:77],v[76:77],v[60:61],v[54:55] op_sel:[0,0,1] op_sel_hi:[1,0,1]\n"
"v_pk_fma_f32 v[78:79],v[78:79],v[60:61],v[54:55] op_sel:[0,0,1] op_sel_hi:[1,0,1]\n"
CV "v112,v72,v73\n"
CV "v113,v74,v75\n"
CV "v114,v76,v77\n"
CV "v115,v78,v79\n"
MF "a[80:83],v[152:155],v[92:95],a[80:83]\n"
"ds_read_b128 v[216:219],v120 offset:57344\n"
"ds_write_b128 v120,v[112:115] offset:57344\n"
AR "v80,a56\n"
AR "v81,a57\n"
AR "v82,a58\n"
MF "a[100:103],v[156:159],v[92:95],a[100:103]\n"
AR "v83,a59\n"
AR "v84,a76\n"
AR "v85,a77\n"
AR "v86,a78\n"
AR "v87,a79\n"
"v_pk_fma_f32 v[80:81],v[80:81],v[60:61],v[56:57] op_sel:[0,1,0] op_sel_hi:[1,1,0]\n"
"v_pk_fma_f32 v[82:83],v[82:83],v[60:61],v[56:57] op_sel:[0,1,0] op_sel_hi:[1,1,0]\n"
"v_pk_fma_f32 v[84:85],v[84:85],v[60:61],v[56:57] op_sel:[0,1,0] op_sel_hi:[1,1,0]\n"
"v_pk_fma_f32 v[86:87],v[86:87],v[60:61],v[56:57] op_sel:[0,1,0] op_sel_hi:[1,1,0]\n"
CV "v116,v80,v81\n"
CV "v117,v82,v83\n"
CV "v118,v84,v85\n"
CV "v119,v86,v87\n"
MF "a[84:87],v[152:155],v[96:99],a[84:87]\n"
"ds_read_b128 v[220:223],v120 offset:61440\n"
"ds_write_b128 v120,v[116:119] offset:61440\n"
AR "v72,a80\n"
AR "v73,a81\n"
AR "v74,a82\n"
MF "a[104:107],v[156:159],v[96:99],a[104:107]\n"
AR "v75,a83\n"
AR "v76,a100\n"
AR "v77,a101\n"
AR "v78,a102\n"
AR "v79,a103\n"
"v_pk_fma_f32 v[72:73],v[72:73],v[56:57],v[52:53] op_sel:[0,1,0] op_sel_hi:[1,1,0]\n"
"v_pk_fma_f32 v[74:75],v[74:75],v[56:57],v[52:53] op_sel:[0,1,0] op_sel_hi:[1,1,0]\n"
"v_pk_fma_f32 v[76:77],v[76:77],v[56:57],v[52:53] op_sel:[0,1,0] op_sel_hi:[1,1,0]\n"
"v_pk_fma_f32 v[78:79],v[78:79],v[56:57],v[52:53] op_sel:[0,1,0] op_sel_hi:[1,1,0]\n"
CV "v112,v72,v73\n"
CV "v113,v74,v75\n"
CV "v114,v76,v77\n"
CV "v115,v78,v79\n"
MF "a[88:91],v[152:155],v[100:103],a[88:91]\n"
"ds_read_b128 v[224:227],v121\n"
"ds_write_b128 v121,v[112:115]\n"
AR "v80,a84\n"
AR "v81,a85\n"
AR "v82,a86\n"
MF "a[108:111],v[156:159],v[100:103],a[108:111]\n"
AR "v83,a87\n"
AR "v84,a104\n"
AR "v85,a105\n"
AR "v86,a106\n"
AR "v87,a107\n"
"v_pk_fma_f32 v[80:81],v[80:81],v[58:59],v[52:53] op_sel:[0,0,1] op_sel_hi:[1,0,1]\n"
"v_pk_fma_f32 v[82:83],v[82:83],v[58:59],v[52:53] op_sel:[0,0,1] op_sel_hi:[1,0,1]\n"
"v_pk_fma_f32 v[84:85],v[84:85],v[58:59],v[52:53] op_sel:[0,0,1] op_sel_hi:[1,0,1]\n"
"v_pk_fma_f32 v[86:87],v[86:87],v[58:59],v[52:53] op_sel:[0,0,1] op_sel_hi:[1,0,1]\n"
CV "v116,v80,v81\n"
CV "v117,v82,v83\n"
CV "v118,v84,v85\n"
CV "v119,v86,v87\n"
MF "a[92:95],v[152:155],v[104:107],a[92:95]\n"
"ds_read_b128 v[228:231],v121 offset:4096\n"
"ds_write_b128 v121,v[116:119] offset:4096\n"
AR "v72,a88\n"
AR "v73,a89\n"
AR "v74,a90\n"
MF "a[112:115],v[156:159],v[104:107],a[112:115]\n"
AR "v75,a91\n"
AR "v76,a108\n"
AR "v77,a109\n"
".Lpg15g:\n"
"s_cbranch_execz .Lisr3\n"
"s_nop 0\n"
AR "v78,a110\n"
AR "v79,a111\n"
"v_pk_fma_f32 v[72:73],v[72:73],v[58:59],v[54:55] op_sel:[0,1,0] op_sel_hi:[1,1,0]\n"
"v_pk_fma_f32 v[74:75],v[74:75],v[58:59],v[54:55] op_sel:[0,1,0] op_sel_hi:[1,1,0]\n"
"v_pk_fma_f32 v[76:77],v[76:77],v[58:59],v[54:55] op_sel:[0,1,0] op_sel_hi:[1,1,0]\n"
"v_pk_fma_f32 v[78:79],v[78:79],v[58:59],v[54:55] op_sel:[0,1,0] op_sel_hi:[1,1,0]\n"
CV "v112,v72,v73\n"
CV "v113,v74,v75\n"
CV "v114,v76,v77\n"
CV "v115,v78,v79\n"
MF "a[96:99],v[152:155],v[108:111],a[96:99]\n"
"ds_read_b128 v[136:139],v121 offset:8192\n"
"ds_write_b128 v121,v[112:115] offset:8192\n"
AR "v80,a92\n"
AR "v81,a93\n"
AR "v82,a94\n"
MF "a[116:119],v[156:159],v[108:111],a[116:119]\n"
AR "v83,a95\n"
AR "v84,a112\n"
AR "v85,a113\n"
AR "v86,a114\n"
AR "v87,a115\n"
"v_pk_fma_f32 v[80:81],v[80:81],v[60:61],v[54:55] op_sel:[0,0,1] op_sel_hi:[1,0,1]\n"
"v_pk_fma_f32 v[82:83],v[82:83],v[60:61],v[54:55] op_sel:[0,0,1] op_sel_hi:[1,0,1]\n"
"v_pk_fma_f32 v[84:85],v[84:85],v[60:61],v[54:55] op_sel:[0,0,1] op_sel_hi:[1,0,1]\n"
"v_pk_fma_f32 v[86:87],v[86:87],v[60:61],v[54:55] op_sel:[0,0,1] op_sel_hi:[1,0,1]\n"
CV "v116,v80,v81\n"
CV "v117,v82,v83\n"
CV "v118,v84,v85\n"
CV "v119,v86,v87\n"
MF "a[120:123],v[160:163],v[92:95],a[120:123]\n"
"ds_read_b128 v[140:143],v121 offset:12288\n"
"ds_write_b128 v121,v[116:119] offset:12288\n"
AR "v72,a96\n"
AR "v73,a97\n"
AR "v74,a98\n"
MF "a[140:143],v[164:167],v[92:95],a[140:143]\n"
AR "v75,a99\n"
AR "v76,a116\n"
AR "v77,a117\n"
AR "v78,a118\n"
AR "v79,a119\n"
"v_pk_fma_f32 v[72:73],v[72:73],v[60:61],v[56:57] op_sel:[0,1,0] op_sel_hi:[1,1,0]\n"
"v_pk_fma_f32 v[74:75],v[74:75],v[60:61],v[56:57] op_sel:[0,1,0] op_sel_hi:[1,1,0]\n"
"v_pk_fma_f32 v[76:77],v[76:77],v[60:61],v[56:57] op_sel:[0,1,0] op_sel_hi:[1,1,0]\n"
"v_pk_fma_f32 v[78:79],v[78:79],v[60:61],v[56:57] op_sel:[0,1,0] op_sel_hi:[1,1,0]\n"
CV "v112,v72,v73\n"
CV "v113,v74,v75\n"
CV "v114,v76,v77\n"
CV "v115,v78,v79\n"
MF "a[124:127],v[160:163],v[96:99],a[124:127]\n"
"ds_read_b128 v[144:147],v121 offset:16384\n"
"ds_write_b128 v121,v[112:115] offset:16384\n"
AR "v80,a120\n"
AR "v81,a121\n"
AR "v82,a122\n"
MF "a[144:147],v[164:167],v[96:99],a[144:147]\n"
AR "v83,a123\n"
AR "v84,a140\n"
AR "v85,a141\n"
AR "v86,a142\n"
AR "v87,a143\n"
"v_pk_fma_f32 v[80:81],v[80:81],v[56:57],v[52:53] op_sel:[0,1,0] op_sel_hi:[1,1,0]\n"
"v_pk_fma_f32 v[82:83],v[82:83],v[56:57],v[52:53] op_sel:[0,1,0] op_sel_hi:[1,1,0]\n"
"v_pk_fma_f32 v[84:85],v[84:85],v[56:57],v[52:53] op_sel:[0,1,0] op_sel_hi:[1,1,0]\n"
"v_pk_fma_f32 v[86:87],v[86:87],v[56:57],v[52:53] op_sel:[0,1,0] op_sel_hi:[1,1,0]\n"
CV "v116,v80,v81\n"
CV "v117,v82,v83\n"
CV "v118,v84,v85\n"
CV "v119,v86,v87\n"
MF "a[128:131],v[160:163],v[100:103],a[128:131]\n"
"ds_read_b128 v[148:151],v121 offset:20480\n"
"ds_write_b128 v121,v[116:119] offset:20480\n"
AR "v72,a124\n"
AR "v73,a125\n"
AR "v74,a126\n"
MF "a[148:151],v[164:167],v[100:103],a[148:151]\n"
AR "v75,a127\n"
AR "v76,a144\n"
AR "v77,a145\n"
AR "v78,a146\n"
AR "v79,a147\n"
"v_pk_fma_f32 v[72:73],v[72:73],v[58:59],v[52:53] op_sel:[0,0,1] op_sel_hi:[1,0,1]\n"
"v_pk_fma_f32 v[74:75],v[74:75],v[58:59],v[52:53] op_sel:[0,0,1] op_sel_hi:[1,0,1]\n"
"v_pk_fma_f32 v[76:77],v[76:77],v[58:59],v[52:53] op_sel:[0,0,1] op_sel_hi:[1,0,1]\n"
"v_pk_fma_f32 v[78:79],v[78:79],v[58:59],v[52:53] op_sel:[0,0,1] op_sel_hi:[1,0,1]\n"
CV "v112,v72,v73\n"
CV "v113,v74,v75\n"
CV "v114,v76,v77\n"
CV "v115,v78,v79\n"
MF "a[132:135],v[160:163],v[104:107],a[132:135]\n"
"ds_write_b128 v120,v[112:115]\n"
AR "v80,a128\n"
AR "v81,a129\n"
AR "v82,a130\n"
MF "a[152:155],v[164:167],v[104:107],a[152:155]\n"
AR "v83,a131\n"
AR "v84,a148\n"
AR "v85,a149\n"
AR "v86,a150\n"
AR "v87,a151\n"
"v_pk_fma_f32 v[80:81],v[80:81],v[58:59],v[54:55] op_sel:[0,1,0] op_sel_hi:[1,1,0]\n"
"v_pk_fma_f32 v[82:83],v[82:83],v[58:59],v[54:55] op_sel:[0,1,0] op_sel_hi:[1,1,0]\n"
"v_pk_fma_f32 v[84:85],v[84:85],v[58:59],v[54:55] op_sel:[0,1,0] op_sel_hi:[1,1,0]\n"
"v_pk_fma_f32 v[86:87],v[86:87],v[58:59],v[54:55] op_sel:[0,1,0] op_sel_hi:[1,1,0]\n"
CV "v116,v80,v81\n"
CV "v117,v82,v83\n"
CV "v118,v84,v85\n"
CV "v119,v86,v87\n"
MF "a[136:139],v[160:163],v[108:111],a[136:139]\n"
"ds_write_b128 v120,v[116:119] offset:4096\n"
AR "v72,a132\n"
AR "v73,a133\n"
AR "v74,a134\n"
MF "a[156:159],v[164:167],v[108:111],a[156:159]\n"
AR "v75,a135\n"
AR "v76,a152\n"
AR "v77,a153\n"
AR "v78,a154\n"
AR "v79,a155\n"
"v_pk_fma_f32 v[72:73],v[72:73],v[60:61],v[54:55] op_sel:[0,0,1] op_sel_hi:[1,0,1]\n"
"v_pk_fma_f32 v[74:75],v[74:75],v[60:61],v[54:55] op_sel:[0,0,1] op_sel_hi:[1,0,1]\n"
"v_pk_fma_f32 v[76:77],v[76:77],v[60:61],v[54:55] op_sel:[0,0,1] op_sel_hi:[1,0,1]\n"
"v_pk_fma_f32 v[78:79],v[78:79],v[60:61],v[54:55] op_sel:[0,0,1] op_sel_hi:[1,0,1]\n"
CV "v112,v72,v73\n"
CV "v113,v74,v75\n"
CV "v114,v76,v77\n"
CV "v115,v78,v79\n"
MF "a[160:163],v[168:171],v[92:95],a[160:163]\n"
"ds_write_b128 v120,v[112:115] offset:8192\n"
AR "v80,a136\n"
AR "v81,a137\n"
AR "v82,a138\n"
MF "a[180:183],v[172:175],v[92:95],a[180:183]\n"
AR "v83,a139\n"
AR "v84,a156\n"
AR "v85,a157\n"
AR "v86,a158\n"
AR "v87,a159\n"
"v_pk_fma_f32 v[80:81],v[80:81],v[60:61],v[56:57] op_sel:[0,1,0] op_sel_hi:[1,1,0]\n"
"v_pk_fma_f32 v[82:83],v[82:83],v[60:61],v[56:57] op_sel:[0,1,0] op_sel_hi:[1,1,0]\n"
"v_pk_fma_f32 v[84:85],v[84:85],v[60:61],v[56:57] op_sel:[0,1,0] op_sel_hi:[1,1,0]\n"
"v_pk_fma_f32 v[86:87],v[86:87],v[60:61],v[56:57] op_sel:[0,1,0] op_sel_hi:[1,1,0]\n"
CV "v116,v80,v81\n"
CV "v117,v82,v83\n"
CV "v118,v84,v85\n"
CV "v119,v86,v87\n"
MF "a[164:167],v[168:171],v[96:99],a[164:167]\n"
"ds_write_b128 v120,v[116:119] offset:12288\n"
AR "v72,a160\n"
AR "v73,a161\n"
AR "v74,a162\n"
MF "a[184:187],v[172:175],v[96:99],a[184:187]\n"
AR "v75,a163\n"
AR "v76,a180\n"
AR "v77,a181\n"
AR "v78,a182\n"
AR "v79,a183\n"
"v_pk_fma_f32 v[72:73],v[72:73],v[56:57],v[52:53] op_sel:[0,1,0] op_sel_hi:[1,1,0]\n"
"v_pk_fma_f32 v[74:75],v[74:75],v[56:57],v[52:53] op_sel:[0,1,0] op_sel_hi:[1,1,0]\n"
"v_pk_fma_f32 v[76:77],v[76:77],v[56:57],v[52:53] op_sel:[0,1,0] op_sel_hi:[1,1,0]\n"
"v_pk_fma_f32 v[78:79],v[78:79],v[56:57],v[52:53] op_sel:[0,1,0] op_sel_hi:[1,1,0]\n"
CV "v112,v72,v73\n"
CV "v113,v74,v75\n"
CV "v114,v76,v77\n"
CV "v115,v78,v79\n"
MF "a[168:171],v[168:171],v[100:103],a[168:171]\n"
"ds_write_b128 v120,v[112:115] offset:16384\n"
AR "v80,a164\n"
AR "v81,a165\n"
AR "v82,a166\n"
MF "a[188:191],v[172:175],v[100:103],a[188:191]\n"
AR "v83,a167\n"
AR "v84,a184\n"
AR "v85,a185\n"
AR "v86,a186\n"
AR "v87,a187\n"
"v_pk_fma_f32 v[80:81],v[80:81],v[58:59],v[52:53] op_sel:[0,0,1] op_sel_hi:[1,0,1]\n"
"v_pk_fma_f32 v[82:83],v[82:83],v[58:59],v[52:53] op_sel:[0,0,1] op_sel_hi:[1,0,1]\n"
"v_pk_fma_f32 v[84:85],v[84:85],v[58:59],v[52:53] op_sel:[0,0,1] op_sel_hi:[1,0,1]\n"
"v_pk_fma_f32 v[86:87],v[86:87],v[58:59],v[52:53] op_sel:[0,0,1] op_sel_hi:[1,0,1]\n"
CV "v116,v80,v81\n"
CV "v117,v82,v83\n"
CV "v118,v84,v85\n"
CV "v119,v86,v87\n"
MF "a[172:175],v[168:171],v[104:107],a[172:175]\n"
"ds_write_b128 v120,v[116:119] offset:20480\n"
AR "v72,a168\n"
AR "v73,a169\n"
AR "v74,a170\n"
MF "a[192:195],v[172:175],v[104:107],a[192:195]\n"
AR "v75,a171\n"
AR "v76,a188\n"
AR "v77,a189\n"
AR "v78,a190\n"
AR "v79,a191\n"
"v_pk_fma_f32 v[72:73],v[72:73],v[58:59],v[54:55] op_sel:[0,1,0] op_sel_hi:[1,1,0]\n"
"v_pk_fma_f32 v[74:75],v[74:75],v[58:59],v[54:55] op_sel:[0,1,0] op_sel_hi:[1,1,0]\n"
"v_pk_fma_f32 v[76:77],v[76:77],v[58:59],v[54:55] op_sel:[0,1,0] op_sel_hi:[1,1,0]\n"
"v_pk_fma_f32 v[78:79],v[78:79],v[58:59],v[54:55] op_sel:[0,1,0] op_sel_hi:[1,1,0]\n"
CV "v112,v72,v73\n"
CV "v113,v74,v75\n"
CV "v114,v76,v77\n"
CV "v115,v78,v79\n"
MF "a[176:179],v[168:171],v[108:111],a[176:179]\n"
AR "v152,a240\n"
AW "a240,v112\n"
AR "v153,a241\n"
AW "a241,v113\n"
AR "v154,a242\n"
AW "a242,v114\n"
AR "v155,a243\n"
AW "a243,v115\n"
AR "v80,a172\n"
AR "v81,a173\n"
AR "v82,a174\n"
MF "a[196:199],v[172:175],v[108:111],a[196:199]\n"
AR "v83,a175\n"
AR "v84,a192\n"
AR "v85,a193\n"
AR "v86,a194\n"
AR "v87,a195\n"
"v_pk_fma_f32 v[80:81],v[80:81],v[60:61],v[54:55] op_sel:[0,0,1] op_sel_hi:[1,0,1]\n"
"v_pk_fma_f32 v[82:83],v[82:83],v[60:61],v[54:55] op_sel:[0,0,1] op_sel_hi:[1,0,1]\n"
"v_pk_fma_f32 v[84:85],v[84:85],v[60:61],v[54:55] op_sel:[0,0,1] op_sel_hi:[1,0,1]\n"
"v_pk_fma_f32 v[86:87],v[86:87],v[60:61],v[54:55] op_sel:[0,0,1] op_sel_hi:[1,0,1]\n"
CV "v116,v80,v81\n"
CV "v117,v82,v83\n"
CV "v118,v84,v85\n"
CV "v119,v86,v87\n"
MF "a[200:203],v[176:179],v[92:95],a[200:203]\n"
AR "v156,a244\n"
AW "a244,v116\n"
AR "v157,a245\n"
AW "a245,v117\n"
AR "v158,a246\n"
AW "a246,v118\n"
AR "v159,a247\n"
AW "a247,v119\n"
AR "v72,a176\n"
AR "v73,a177\n"
AR "v74,a178\n"
MF "a[220:223],v[180:183],v[92:95],a[220:223]\n"
AR "v75,a179\n"
AR "v76,a196\n"
AR "v77,a197\n"
AR "v78,a198\n"
AR "v79,a199\n"
"v_pk_fma_f32 v[72:73],v[72:73],v[60:61],v[56:57] op_sel:[0,1,0] op_sel_hi:[1,1,0]\n"
"v_pk_fma_f32 v[74:75],v[74:75],v[60:61],v[56:57] op_sel:[0,1,0] op_sel_hi:[1,1,0]\n"
"v_pk_fma_f32 v[76:77],v[76:77],v[60:61],v[56:57] op_sel:[0,1,0] op_sel_hi:[1,1,0]\n"
"v_pk_fma_f32 v[78:79],v[78:79],v[60:61],v[56:57] op_sel:[0,1,0] op_sel_hi:[1,1,0]\n"
CV "v112,v72,v73\n"
CV "v113,v74,v75\n"
CV "v114,v76,v77\n"
CV "v115,v78,v79\n"
MF "a[204:207],v[176:179],v[96:99],a[204:207]\n"
AR "v160,a248\n"
AW "a248,v112\n"
AR "v161,a249\n"
AW "a249,v113\n"
AR "v162,a250\n"
AW "a250,v114\n"
AR "v163,a251\n"
AW "a251,v115\n"
AR "v80,a200\n"
AR "v81,a201\n"
AR "v82,a202\n"
MF "a[224:227],v[180:183],v[96:99],a[224:227]\n"
AR "v83,a203\n"
AR "v84,a220\n"
AR "v85,a221\n"
AR "v86,a222\n"
AR "v87,a223\n"
"v_pk_fma_f32 v[80:81],v[80:81],v[56:57],v[52:53] op_sel:[0,1,0] op_sel_hi:[1,1,0]\n"
"v_pk_fma_f32 v[82:83],v[82:83],v[56:57],v[52:53] op_sel:[0,1,0] op_sel_hi:[1,1,0]\n"
"v_pk_fma_f32 v[84:85],v[84:85],v[56:57],v[52:53] op_sel:[0,1,0] op_sel_hi:[1,1,0]\n"
"v_pk_fma_f32 v[86:87],v[86:87],v[56:57],v[52:53] op_sel:[0,1,0] op_sel_hi:[1,1,0]\n"
CV "v116,v80,v81\n"
CV "v117,v82,v83\n"
CV "v118,v84,v85\n"
CV "v119,v86,v87\n"
MF "a[208:211],v[176:179],v[100:103],a[208:211]\n"
"v_mov_b32 v164,v62\n"
"v_mov_b32 v62,v116\n"
"v_mov_b32 v165,v63\n"
"v_mov_b32 v63,v117\n"
"v_mov_b32 v166,v64\n"
"v_mov_b32 v64,v118\n"
"v_mov_b32 v167,v65\n"
"v_mov_b32 v65,v119\n"
AR "v72,a204\n"
AR "v73,a205\n"
AR "v74,a206\n"
MF "a[228:231],v[180:183],v[100:103],a[228:231]\n"
AR "v75,a207\n"
AR "v76,a224\n"
AR "v77,a225\n"
AR "v78,a226\n"
AR "v79,a227\n"
"v_pk_fma_f32 v[72:73],v[72:73],v[58:59],v[52:53] op_sel:[0,0,1] op_sel_hi:[1,0,1]\n"
"v_pk_fma_f32 v[74:75],v[74:75],v[58:59],v[52:53] op_sel:[0,0,1] op_sel_hi:[1,0,1]\n"
"v_pk_fma_f32 v[76:77],v[76:77],v[58:59],v[52:53] op_sel:[0,0,1] op_sel_hi:[1,0,1]\n"
"v_pk_fma_f32 v[78:79],v[78:79],v[58:59],v[52:53] op_sel:[0,0,1] op_sel_hi:[1,0,1]\n"
CV "v112,v72,v73\n"
CV "v113,v74,v75\n"
CV "v114,v76,v77\n"
CV "v115,v78,v79\n"
MF "a[212:215],v[176:179],v[104:107],a[212:215]\n"
"v_mov_b32 v168,v66\n"
"v_mov_b32 v66,v112\n"
"v_mov_b32 v169,v67\n"
"v_mov_b32 v67,v113\n"
"v_mov_b32 v170,v68\n"
"v_mov_b32 v68,v114\n"
"v_mov_b32 v171,v69\n"
"v_mov_b32 v69,v115\n"
AR "v80,a208\n"
AR "v81,a209\n"
AR "v82,a210\n"
MF "a[232:235],v[180:183],v[104:107],a[232:235]\n"
AR "v83,a211\n"
AR "v84,a228\n"
AR "v85,a229\n"
AR "v86,a230\n"
AR "v87,a231\n"
"v_pk_fma_f32 v[80:81],v[80:81],v[58:59],v[54:55] op_sel:[0,1,0] op_sel_hi:[1,1,0]\n"
"v_pk_fma_f32 v[82:83],v[82:83],v[58:59],v[54:55] op_sel:[0,1,0] op_sel_hi:[1,1,0]\n"
"v_pk_fma_f32 v[84:85],v[84:85],v[58:59],v[54:55] op_sel:[0,1,0] op_sel_hi:[1,1,0]\n"
"v_pk_fma_f32 v[86:87],v[86:87],v[58:59],v[54:55] op_sel:[0,1,0] op_sel_hi:[1,1,0]\n"
CV "v116,v80,v81\n"
CV "v117,v82,v83\n"
CV "v118,v84,v85\n"
CV "v119,v86,v87\n"
MF "a[216:219],v[176:179],v[108:111],a[216:219]\n"
"v_mov_b32 v172,v70\n"
"v_mov_b32 v70,v116\n"
"v_mov_b32 v173,v71\n"
"v_mov_b32 v71,v117\n"
"v_mov_b32 v174,v132\n"
"v_mov_b32 v132,v118\n"
"v_mov_b32 v175,v133\n"
"v_mov_b32 v133,v119\n"
AR "v72,a212\n"
AR "v73,a213\n"
AR "v74,a214\n"
MF "a[236:239],v[180:183],v[108:111],a[236:239]\n"
AR "v75,a215\n"
AR "v76,a232\n"
AR "v77,a233\n"
AR "v78,a234\n"
AR "v79,a235\n"
"v_pk_fma_f32 v[72:73],v[72:73],v[60:61],v[54:55] op_sel:[0,0,1] op_sel_hi:[1,0,1]\n"
"v_pk_fma_f32 v[74:75],v[74:75],v[60:61],v[54:55] op_sel:[0,0,1] op_sel_hi:[1,0,1]\n"
"v_pk_fma_f32 v[76:77],v[76:77],v[60:61],v[54:55] op_sel:[0,0,1] op_sel_hi:[1,0,1]\n"
"v_pk_fma_f32 v[78:79],v[78:79],v[60:61],v[54:55] op_sel:[0,0,1] op_sel_hi:[1,0,1]\n"
CV "v112,v72,v73\n"
CV "v113,v74,v75\n"
CV "v114,v76,v77\n"
CV "v115,v78,v79\n"
"s_waitcnt vmcnt(0) lgkmcnt(0)\n"
"s_barrier\n"
"ds_read_b128 v[92:95],v0\n"
"ds_read_b128 v[96:99],v1\n"
"ds_read_b128 v[100:103],v0 offset:1024\n"
"v_mov_b32 v176,v134\n"
"v_mov_b32 v134,v112\n"
"v_mov_b32 v177,v135\n"
"v_mov_b32 v135,v113\n"
"v_mov_b32 v178,v6\n"
"v_mov_b32 v6,v114\n"
"v_mov_b32 v179,v13\n"
"v_mov_b32 v13,v115\n"
AR "v80,a216\n"
AR "v81,a217\n"
AR "v82,a218\n"
AR "v83,a219\n"
AR "v84,a236\n"
AR "v85,a237\n"
AR "v86,a238\n"
AR "v87,a239\n"
"v_pk_fma_f32 v[80:81],v[80:81],v[60:61],v[56:57] op_sel:[0,1,0] op_sel_hi:[1,1,0]\n"
"v_pk_fma_f32 v[82:83],v[82:83],v[60:61],v[56:57] op_sel:[0,1,0] op_sel_hi:[1,1,0]\n"
"v_pk_fma_f32 v[84:85],v[84:85],v[60:61],v[56:57] op_sel:[0,1,0] op_sel_hi:[1,1,0]\n"
"v_pk_fma_f32 v[86:87],v[86:87],v[60:61],v[56:57] op_sel:[0,1,0] op_sel_hi:[1,1,0]\n"
CV "v116,v80,v81\n"
CV "v117,v82,v83\n"
CV "v118,v84,v85\n"
CV "v119,v86,v87\n"
"v_mov_b32 v180,v14\n"
"v_mov_b32 v14,v116\n"
"v_mov_b32 v181,v15\n"
"v_mov_b32 v15,v117\n"
AR "v182,a254\n"
AW "a254,v118\n"
AR "v183,a255\n"
AW "a255,v119\n"
"s_mov_b32 s13,s11\n"
"v_subrev_u32 v2,49152,v2\n"
"v_subrev_u32 v3,49152,v3\n"
"v_subrev_u32 v4,49152,v4\n"
"v_subrev_u32 v5,49152,v5\n"
"s_mov_b32 s57,2\n"
"s_mov_b32 s69,1\n"
"s_mov_b32 s61,3\n"
"s_mov_b32 s97,0\n"
"s_mov_b32 s95,0x47800000\n"
"s_mov_b32 s77,0x47800000\n"
"s_branch .Latt2\n"
".LpgattB_f:\n"
"s_cbranch_execz .Lisr3\n"
".LattB:\n"
AR "v2,a245\n"
"v_xor_b32 v3,32,v2\n"
"v_xor_b32 v4,64,v2\n"
"v_xor_b32 v5,96,v2\n"
"s_mov_b32 s61,3\n"
".Latt2:\n"
"s_cmp_eq_u32 s57,1\n"
"s_cbranch_scc1 .Lt0k\n"
".Lt0n:\n"
"s_waitcnt lgkmcnt(2)\n"
MF "v[72:75],v[92:95],v[136:139],0\n"
"ds_read_b128 v[104:107],v1 offset:1024\n"
MF "v[76:79],v[92:95],v[140:143],0\n"
MF "v[80:83],v[92:95],v[144:147],0\n"
MF "v[84:87],v[92:95],v[148:151],0\n"
MF "v[88:91],v[92:95],v[152:155],0\n"
"s_waitcnt lgkmcnt(2)\n"
"ds_read_b128 v[92:95],v0 offset:2048\n"
MF "v[72:75],v[96:99],v[156:159],v[72:75]\n"
MF "v[76:79],v[96:99],v[160:163],v[76:79]\n"
MF "v[80:83],v[96:99],v[164:167],v[80:83]\n"
MF "v[84:87],v[96:99],v[168:171],v[84:87]\n"
MF "v[88:91],v[96:99],v[172:175],v[88:91]\n"
"s_waitcnt lgkmcnt(2)\n"
"ds_read_b128 v[96:99],v1 offset:2048\n"
MF "v[72:75],v[100:103],v[176:179],v[72:75]\n"
MF "v[76:79],v[100:103],v[180:183],v[76:79]\n"
MF "v[80:83],v[100:103],v[184:187],v[80:83]\n"
MF "v[84:87],v[100:103],v[188:191],v[84:87]\n"
MF "v[88:91],v[100:103],v[192:195],v[88:91]\n"
"s_waitcnt lgkmcnt(2)\n"
"ds_read_b128 v[100:103],v0 offset:6144\n"
MF "v[72:75],v[104:107],v[196:199],v[72:75]\n"
MF "v[76:79],v[104:107],v[200:203],v[76:79]\n"
MF "v[80:83],v[104:107],v[204:207],v[80:83]\n"
MF "v[84:87],v[104:107],v[208:211],v[84:87]\n"
MF "v[88:91],v[104:107],v[212:215],v[88:91]\n"
"s_waitcnt lgkmcnt(2)\n"
"ds_read_b128 v[104:107],v1 offset:6144\n"
MF "v[72:75],v[92:95],v[216:219],v[72:75]\n"
MF "v[76:79],v[92:95],v[220:223],v[76:79]\n"
MF "v[80:83],v[92:95],v[224:227],v[80:83]\n"
MF "v[84:87],v[92:95],v[228:231],v[84:87]\n"
MF "v[88:91],v[92:95],v[232:235],v[88:91]\n"
"s_waitcnt lgkmcnt(2)\n"
"ds_read_b128 v[92:95],v0 offset:7168\n"
MF "v[72:75],v[96:99],v[236:239],v[72:75]\n"
MF "v[76:79],v[96:99],v[240:243],v[76:79]\n"
MF "v[80:83],v[96:99],v[244:247],v[80:83]\n"
MF "v[84:87],v[96:99],v[248:251],v[84:87]\n"
MF "v[88:91],v[96:99],v[252:255],v[88:91]\n"
"s_waitcnt lgkmcnt(2)\n"
"ds_read_b128 v[96:99],v1 offset:7168\n"
MF "v[112:115],v[100:103],v[136:139],0\n"
MF "v[116:119],v[100:103],v[140:143],0\n"
MF "v[120:123],v[100:103],v[144:147],0\n"
MF "v[124:127],v[100:103],v[148:151],0\n"
MF "v[128:131],v[100:103],v[152:155],0\n"
"s_waitcnt lgkmcnt(2)\n"
"ds_read_b128 v[100:103],v0 offset:8192\n"
MF "v[112:115],v[104:107],v[156:159],v[112:115]\n"
"v_max3_f32 v52,v72,v73,v74\n"
"v_max_f32 v52,v52,v75\n"
MF "v[116:119],v[104:107],v[160:163],v[116:119]\n"
"v_max3_f32 v53,v76,v77,v78\n"
"v_max_f32 v53,v53,v79\n"
MF "v[120:123],v[104:107],v[164:167],v[120:123]\n"
"v_max3_f32 v54,v80,v81,v82\n"
"v_max_f32 v54,v54,v83\n"
MF "v[124:127],v[104:107],v[168:171],v[124:127]\n"
"v_max3_f32 v55,v84,v85,v86\n"
"v_max_f32 v55,v55,v87\n"
MF "v[128:131],v[104:107],v[172:175],v[128:131]\n"
"s_waitcnt lgkmcnt(2)\n"
"ds_read_b128 v[104:107],v1 offset:8192\n"
"v_max3_f32 v56,v88,v89,v90\n"
MF "v[112:115],v[92:95],v[176:179],v[112:115]\n"
"v_max_f32 v56,v56,v91\n"
MF "v[116:119],v[92:95],v[180:183],v[116:119]\n"
MF "v[120:123],v[92:95],v[184:187],v[120:123]\n"
MF "v[124:127],v[92:95],v[188:191],v[124:127]\n"
MF "v[128:131],v[92:95],v[192:195],v[128:131]\n"
"s_waitcnt lgkmcnt(2)\n"
MF "v[112:115],v[96:99],v[196:199],v[112:115]\n"
MF "v[116:119],v[96:99],v[200:203],v[116:119]\n"
"s_waitcnt lgkmcnt(0)\n"
"s_barrier\n"
MF "v[120:123],v[96:99],v[204:207],v[120:123]\n"
"s_add_u32 s72,s72,6144\n"
MF "v[124:127],v[96:99],v[208:211],v[124:127]\n"
"s_cmp_eq_u32 s72,s17\n"
"s_cselect_b32 s72,s16,s72\n"
"s_add_u32 m0,s64,0\n"
MF "v[128:131],v[96:99],v[212:215],v[128:131]\n"
LD "v7,s[32:35],s72 offen nt lds\n"
MF "v[112:115],v[100:103],v[216:219],v[112:115]\n"
"s_add_u32 m0,s64,896\n"
MF "v[116:119],v[100:103],v[220:223],v[116:119]\n"
LD "v7,s[32:35],s72 offen offset:128 nt lds\n"
MF "v[120:123],v[100:103],v[224:227],v[120:123]\n"
"s_add_u32 m0,s64,1792\n"
MF "v[124:127],v[100:103],v[228:231],v[124:127]\n"
LD "v7,s[32:35],s72 offen offset:256 nt lds\n"
MF "v[128:131],v[100:103],v[232:235],v[128:131]\n"
"s_add_u32 s72,s72,6144\n"
"s_add_u32 m0,s64,6144\n"
MF "v[112:115],v[104:107],v[236:239],v[112:115]\n"
LD "v7,s[32:35],s72 offen nt lds\n"
MF "v[116:119],v[104:107],v[240:243],v[116:119]\n"
"s_add_u32 m0,s64,7040\n"
MF "v[120:123],v[104:107],v[244:247],v[120:123]\n"
LD "v7,s[32:35],s72 offen offset:128 nt lds\n"
MF "v[124:127],v[104:107],v[248:251],v[124:127]\n"
"s_add_u32 m0,s64,7936\n"
MF "v[128:131],v[104:107],v[252:255],v[128:131]\n"
LD "v7,s[32:35],s72 offen offset:256 nt lds\n"
"s_branch .Lt0e\n"
".Lpgt0k_f:\n"
"s_cbranch_execz .Lisr3\n"
".Lt0k:\n"
"s_lshl_b32 s58,s56,10\n"
"s_add_u32 s58,s58,73728\n"
"v_add_u32 v32,s58,v9\n"
"v_add_u32 v33,65536,v32\n"
"s_waitcnt lgkmcnt(2)\n"
MF "v[72:75],v[92:95],v[136:139],0\n"
"ds_read_b128 v[104:107],v1 offset:1024\n"
AR "v62,a20\n"
MF "v[76:79],v[92:95],v[140:143],0\n"
"ds_write_b128 v32,a[0:3] offset:24576\n"
MF "v[80:83],v[92:95],v[144:147],0\n"
"ds_write_b128 v32,a[4:7] offset:28672\n"
MF "v[84:87],v[92:95],v[148:151],0\n"
AR "v63,a21\n"
AR "v64,a22\n"
MF "v[88:91],v[92:95],v[152:155],0\n"
"s_waitcnt lgkmcnt(4)\n"
"ds_read_b128 v[92:95],v0 offset:2048\n"
AR "v65,a23\n"
MF "v[72:75],v[96:99],v[156:159],v[72:75]\n"
"ds_write_b128 v32,a[8:11] offset:32768\n"
MF "v[76:79],v[96:99],v[160:163],v[76:79]\n"
AR "v66,a24\n"
AR "v67,a25\n"
MF "v[80:83],v[96:99],v[164:167],v[80:83]\n"
".Lpg16f:\n"
"s_cbranch_execz .Lisr3\n"
"ds_write_b128 v32,a[12:15] offset:36864\n"
MF "v[84:87],v[96:99],v[168:171],v[84:87]\n"
AR "v68,a26\n"
AR "v69,a27\n"
MF "v[88:91],v[96:99],v[172:175],v[88:91]\n"
"s_waitcnt lgkmcnt(6)\n"
"ds_read_b128 v[96:99],v1 offset:2048\n"
AR "v70,a28\n"
MF "v[72:75],v[100:103],v[176:179],v[72:75]\n"
"ds_write_b128 v32,a[16:19] offset:40960\n"
MF "v[76:79],v[100:103],v[180:183],v[76:79]\n"
AR "v71,a29\n"
AR "v132,a30\n"
MF "v[80:83],v[100:103],v[184:187],v[80:83]\n"
AR "v133,a31\n"
AR "v134,a32\n"
MF "v[84:87],v[100:103],v[188:191],v[84:87]\n"
"ds_write_b128 v32,a[40:43] offset:45056\n"
MF "v[88:91],v[100:103],v[192:195],v[88:91]\n"
"s_waitcnt lgkmcnt(8)\n"
"ds_read_b128 v[100:103],v0 offset:6144\n"
AR "v135,a33\n"
MF "v[72:75],v[104:107],v[196:199],v[72:75]\n"
"ds_write_b128 v32,a[44:47] offset:49152\n"
MF "v[76:79],v[104:107],v[200:203],v[76:79]\n"
AR "v6,a34\n"
AR "v13,a35\n"
MF "v[80:83],v[104:107],v[204:207],v[80:83]\n"
AR "v14,a36\n"
AR "v15,a37\n"
MF "v[84:87],v[104:107],v[208:211],v[84:87]\n"
"ds_write_b128 v32,a[48:51] offset:53248\n"
MF "v[88:91],v[104:107],v[212:215],v[88:91]\n"
"s_waitcnt lgkmcnt(8)\n"
"ds_read_b128 v[104:107],v1 offset:6144\n"
"v_accvgpr_mov_b32 a254,a38\n"
MF "v[72:75],v[92:95],v[216:219],v[72:75]\n"
"v_accvgpr_mov_b32 a255,a39\n"
"v_accvgpr_mov_b32 a240,a168\n"
MF "v[76:79],v[92:95],v[220:223],v[76:79]\n"
"ds_write_b128 v32,a[52:55] offset:57344\n"
MF "v[80:83],v[92:95],v[224:227],v[80:83]\n"
"v_accvgpr_mov_b32 a241,a169\n"
"v_accvgpr_mov_b32 a242,a170\n"
MF "v[84:87],v[92:95],v[228:231],v[84:87]\n"
"ds_write_b128 v32,a[56:59] offset:61440\n"
MF "v[88:91],v[92:95],v[232:235],v[88:91]\n"
"s_waitcnt lgkmcnt(8)\n"
"ds_read_b128 v[92:95],v0 offset:7168\n"
"v_accvgpr_mov_b32 a243,a171\n"
MF "v[72:75],v[96:99],v[236:239],v[72:75]\n"
"v_accvgpr_mov_b32 a244,a172\n"
"v_accvgpr_mov_b32 a245,a173\n"
MF "v[76:79],v[96:99],v[240:243],v[76:79]\n"
"ds_write_b128 v33,a[80:83]\n"
MF "v[80:83],v[96:99],v[244:247],v[80:83]\n"
"v_accvgpr_mov_b32 a246,a174\n"
"v_accvgpr_mov_b32 a247,a175\n"
MF "v[84:87],v[96:99],v[248:251],v[84:87]\n"
"v_accvgpr_mov_b32 a248,a176\n"
"v_accvgpr_mov_b32 a249,a177\n"
MF "v[88:91],v[96:99],v[252:255],v[88:91]\n"
"s_waitcnt lgkmcnt(7)\n"
"ds_read_b128 v[96:99],v1 offset:7168\n"
"v_accvgpr_mov_b32 a250,a178\n"
MF "v[112:115],v[100:103],v[136:139],0\n"
"ds_write_b128 v33,a[84:87] offset:4096\n"
MF "v[116:119],v[100:103],v[140:143],0\n"
"ds_write_b128 v33,a[88:91] offset:8192\n"
MF "v[120:123],v[100:103],v[144:147],0\n"
"v_accvgpr_mov_b32 a251,a179\n"
MF "v[124:127],v[100:103],v[148:151],0\n"
MF "v[128:131],v[100:103],v[152:155],0\n"
"s_waitcnt lgkmcnt(7)\n"
"ds_read_b128 v[100:103],v0 offset:8192\n"
MF "v[112:115],v[104:107],v[156:159],v[112:115]\n"
"v_max3_f32 v52,v72,v73,v74\n"
"v_max_f32 v52,v52,v75\n"
MF "v[116:119],v[104:107],v[160:163],v[116:119]\n"
"v_max3_f32 v53,v76,v77,v78\n"
"v_max_f32 v53,v53,v79\n"
MF "v[120:123],v[104:107],v[164:167],v[120:123]\n"
"v_max3_f32 v54,v80,v81,v82\n"
"v_max_f32 v54,v54,v83\n"
MF "v[124:127],v[104:107],v[168:171],v[124:127]\n"
"v_max3_f32 v55,v84,v85,v86\n"
"v_max_f32 v55,v55,v87\n"
MF "v[128:131],v[104:107],v[172:175],v[128:131]\n"
"s_waitcnt lgkmcnt(5)\n"
"ds_read_b128 v[104:107],v1 offset:8192\n"
"v_max3_f32 v56,v88,v89,v90\n"
MF "v[112:115],v[92:95],v[176:179],v[112:115]\n"
"v_max_f32 v56,v56,v91\n"
MF "v[116:119],v[92:95],v[180:183],v[116:119]\n"
"ds_write_b128 v33,a[92:95] offset:12288\n"
MF "v[120:123],v[92:95],v[184:187],v[120:123]\n"
"ds_write_b128 v33,a[96:99] offset:16384\n"
MF "v[124:127],v[92:95],v[188:191],v[124:127]\n"
"ds_write_b128 v33,a[120:123] offset:20480\n"
MF "v[128:131],v[92:95],v[192:195],v[128:131]\n"
"s_waitcnt lgkmcnt(7)\n"
"ds_write_b128 v32,a[124:127]\n"
MF "v[112:115],v[96:99],v[196:199],v[112:115]\n"
"ds_write_b128 v32,a[128:131] offset:4096\n"
MF "v[116:119],v[96:99],v[200:203],v[116:119]\n"
"s_waitcnt lgkmcnt(0)\n"
"s_barrier\n"
MF "v[120:123],v[96:99],v[204:207],v[120:123]\n"
"s_add_u32 s72,s72,6144\n"
"ds_write_b128 v32,a[132:135] offset:8192\n"
MF "v[124:127],v[96:99],v[208:211],v[124:127]\n"
"s_cmp_eq_u32 s72,s17\n"
"s_cselect_b32 s72,s16,s72\n"
"s_add_u32 m0,s64,0\n"
MF "v[128:131],v[96:99],v[212:215],v[128:131]\n"
LD "v7,s[32:35],s72 offen nt lds\n"
MF "v[112:115],v[100:103],v[216:219],v[112:115]\n"
"s_add_u32 m0,s64,896\n"
"ds_write_b128 v32,a[136:139] offset:12288\n"
MF "v[116:119],v[100:103],v[220:223],v[116:119]\n"
LD "v7,s[32:35],s72 offen offset:128 nt lds\n"
MF "v[120:123],v[100:103],v[224:227],v[120:123]\n"
"s_add_u32 m0,s64,1792\n"
"ds_write_b128 v32,a[160:163] offset:16384\n"
MF "v[124:127],v[100:103],v[228:231],v[124:127]\n"
LD "v7,s[32:35],s72 offen offset:256 nt lds\n"
MF "v[128:131],v[100:103],v[232:235],v[128:131]\n"
"s_add_u32 s72,s72,6144\n"
"s_add_u32 m0,s64,6144\n"
MF "v[112:115],v[104:107],v[236:239],v[112:115]\n"
LD "v7,s[32:35],s72 offen nt lds\n"
MF "v[116:119],v[104:107],v[240:243],v[116:119]\n"
"s_add_u32 m0,s64,7040\n"
"ds_write_b128 v32,a[164:167] offset:20480\n"
MF "v[120:123],v[104:107],v[244:247],v[120:123]\n"
LD "v7,s[32:35],s72 offen offset:128 nt lds\n"
MF "v[124:127],v[104:107],v[248:251],v[124:127]\n"
"s_add_u32 m0,s64,7936\n"
MF "v[128:131],v[104:107],v[252:255],v[128:131]\n"
LD "v7,s[32:35],s72 offen offset:256 nt lds\n"
".Lpgt0e_g:\n"
"s_cbranch_execz .Lisr3\n"
".Lt0e:\n"
"s_nop 1\n"
"v_max3_f32 v52,v52,v112,v113\n"
"v_max3_f32 v53,v53,v116,v117\n"
"v_max3_f32 v54,v54,v120,v121\n"
"s_nop 0\n"
"v_max3_f32 v55,v55,v124,v125\n"
"s_nop 0\n"
"v_max3_f32 v56,v56,v128,v129\n"
"v_max3_f32 v52,v52,v114,v115\n"
"v_max3_f32 v53,v53,v118,v119\n"
"v_max3_f32 v54,v54,v122,v123\n"
"v_max3_f32 v55,v55,v126,v127\n"
"v_max3_f32 v56,v56,v130,v131\n"
"v_mov_b32 v57,v52\n"
"v_mov_b32 v58,v53\n"
"v_mov_b32 v59,v54\n"
"v_mov_b32 v60,v55\n"
"v_mov_b32 v61,v56\n"
"v_permlane16_swap_b32 v52,v57\n"
"v_permlane16_swap_b32 v53,v58\n"
"v_permlane16_swap_b32 v54,v59\n"
"v_permlane16_swap_b32 v55,v60\n"
"v_permlane16_swap_b32 v56,v61\n"
"v_max_f32 v52,v52,v57\n"
"v_max_f32 v53,v53,v58\n"
"v_max_f32 v54,v54,v59\n"
"v_max_f32 v55,v55,v60\n"
"v_max_f32 v56,v56,v61\n"
"v_mov_b32 v57,v52\n"
"v_mov_b32 v58,v53\n"
"v_mov_b32 v59,v54\n"
"v_mov_b32 v60,v55\n"
"v_mov_b32 v61,v56\n"
"v_permlane32_swap_b32 v52,v57\n"
"v_permlane32_swap_b32 v53,v58\n"
"v_permlane32_swap_b32 v54,v59\n"
"v_permlane32_swap_b32 v55,v60\n"
"v_permlane32_swap_b32 v56,v61\n"
"v_max_f32 v52,v52,v57\n"
"v_max_f32 v53,v53,v58\n"
"v_max_f32 v54,v54,v59\n"
"v_max_f32 v55,v55,v60\n"
"v_max_f32_e64 v56,v56,v61\n"
"v_sub_f32 v32,0xc1300000,v52\n"
"v_sub_f32 v36,0xc1300000,v53\n"
"v_sub_f32 v40,0xc1300000,v54\n"
"v_sub_f32 v44,0xc1300000,v55\n"
"v_sub_f32 v48,0xc1300000,v56\n"
"v_mov_b32_e64 v33,v32\n"
"v_pk_mov_b32 v[34:35],v[32:33],v[32:33]\n"
"v_mov_b32 v37,v36\n"
"v_pk_mov_b32 v[38:39],v[36:37],v[36:37]\n"
"v_mov_b32 v41,v40\n"
"v_pk_mov_b32 v[42:43],v[40:41],v[40:41]\n"
"v_mov_b32 v45,v44\n"
"v_pk_mov_b32 v[46:47],v[44:45],v[44:45]\n"
"v_mov_b32 v49,v48\n"
"v_pk_mov_b32 v[50:51],v[48:49],v[48:49]\n"
"v_pk_add_f32 v[72:73],v[72:73],v[32:33]\n"
"v_pk_add_f32 v[74:75],v[74:75],v[32:33]\n"
"v_pk_add_f32 v[112:113],v[112:113],v[32:33]\n"
"v_pk_add_f32 v[114:115],v[114:115],v[32:33]\n"
"v_exp_f32 v72,v72\n"
"v_exp_f32 v73,v73\n"
"v_exp_f32 v74,v74\n"
"v_exp_f32 v75,v75\n"
"v_exp_f32 v112,v112\n"
"v_exp_f32 v113,v113\n"
"v_exp_f32 v114,v114\n"
"v_exp_f32 v115,v115\n"
"v_pk_add_f32 v[10:11],v[72:73],v[74:75]\n"
"v_pk_add_f32 v[28:29],v[112:113],v[114:115]\n"
"v_pk_add_f32 v[10:11],v[10:11],v[28:29]\n"
"v_add_f32_e64 v52,v10,v11\n"
CV "v72,v72,v73\n"
CV "v73,v74,v75\n"
CV "v74,v112,v113\n"
CV "v75,v114,v115\n"
"v_pk_add_f32 v[76:77],v[76:77],v[36:37]\n"
"v_pk_add_f32 v[78:79],v[78:79],v[36:37]\n"
"v_pk_add_f32 v[116:117],v[116:117],v[36:37]\n"
"v_pk_add_f32 v[118:119],v[118:119],v[36:37]\n"
"v_exp_f32 v76,v76\n"
"v_exp_f32 v77,v77\n"
"v_exp_f32 v78,v78\n"
"v_exp_f32 v79,v79\n"
"v_exp_f32 v116,v116\n"
"v_exp_f32 v117,v117\n"
"v_exp_f32 v118,v118\n"
"v_exp_f32 v119,v119\n"
"v_pk_add_f32 v[10:11],v[76:77],v[78:79]\n"
"v_pk_add_f32 v[28:29],v[116:117],v[118:119]\n"
"v_pk_add_f32 v[10:11],v[10:11],v[28:29]\n"
"v_add_f32_e64 v53,v10,v11\n"
CV "v76,v76,v77\n"
CV "v77,v78,v79\n"
CV "v78,v116,v117\n"
CV "v79,v118,v119\n"
"v_pk_add_f32 v[80:81],v[80:81],v[40:41]\n"
"v_pk_add_f32 v[82:83],v[82:83],v[40:41]\n"
"v_pk_add_f32 v[120:121],v[120:121],v[40:41]\n"
"v_pk_add_f32 v[122:123],v[122:123],v[40:41]\n"
"v_exp_f32 v80,v80\n"
"v_exp_f32 v81,v81\n"
"v_exp_f32 v82,v82\n"
"v_exp_f32 v83,v83\n"
"v_exp_f32 v120,v120\n"
"v_exp_f32 v121,v121\n"
"v_exp_f32 v122,v122\n"
"v_exp_f32 v123,v123\n"
"v_pk_add_f32 v[10:11],v[80:81],v[82:83]\n"
"v_pk_add_f32 v[28:29],v[120:121],v[122:123]\n"
"v_pk_add_f32 v[10:11],v[10:11],v[28:29]\n"
"v_add_f32_e64 v54,v10,v11\n"
CV "v80,v80,v81\n"
CV "v81,v82,v83\n"
CV "v82,v120,v121\n"
CV "v83,v122,v123\n"
"v_pk_add_f32 v[84:85],v[84:85],v[44:45]\n"
"v_pk_add_f32 v[86:87],v[86:87],v[44:45]\n"
"v_pk_add_f32 v[124:125],v[124:125],v[44:45]\n"
"v_pk_add_f32 v[126:127],v[126:127],v[44:45]\n"
"v_mov_b32_e64 v55,0\n"
"v_pk_add_f32 v[88:89],v[88:89],v[48:49]\n"
"v_pk_add_f32 v[90:91],v[90:91],v[48:49]\n"
"v_pk_add_f32 v[128:129],v[128:129],v[48:49]\n"
"v_pk_add_f32 v[130:131],v[130:131],v[48:49]\n"
"v_mov_b32 v56,0\n"
"s_waitcnt vmcnt(12) lgkmcnt(0)\n"
"s_barrier\n"
"ds_read_b128 v[16:19],v0 offset:24576\n"
"ds_read_b128 v[20:23],v1 offset:24576\n"
AW "a220,s13\n"
AW "a221,s13\n"
AW "a222,s13\n"
AW "a223,s13\n"
"s_waitcnt lgkmcnt(1)\n"
MF "v[92:95],v[16:19],v[136:139],v[32:35]\n"
"s_nop 0\n"
"ds_read_b128 v[24:27],v0 offset:25600\n"
"ds_read_b128 v[28:31],v1 offset:25600\n"
"s_waitcnt lgkmcnt(2)\n"
MF "v[92:95],v[20:23],v[156:159],v[92:95]\n"
"v_exp_f32 v84,v84\n"
MF "v[96:99],v[20:23],v[160:163],v[36:39]\n"
"v_exp_f32 v85,v85\n"
MF "v[96:99],v[16:19],v[140:143],v[96:99]\n"
"v_exp_f32 v86,v86\n"
MF "v[100:103],v[16:19],v[144:147],v[40:43]\n"
"v_exp_f32 v87,v87\n"
MF "v[100:103],v[20:23],v[164:167],v[100:103]\n"
"v_add_f32 v55,v55,v84\n"
"v_add_f32 v55,v55,v85\n"
MF "v[104:107],v[20:23],v[168:171],v[44:47]\n"
CV "v84,v84,v85\n"
"v_add_f32 v55,v55,v86\n"
MF "v[104:107],v[16:19],v[148:151],v[104:107]\n"
"v_add_f32 v55,v55,v87\n"
CV "v85,v86,v87\n"
MF "v[108:111],v[16:19],v[152:155],v[48:51]\n"
"v_exp_f32 v124,v124\n"
MF "v[108:111],v[20:23],v[172:175],v[108:111]\n"
"s_waitcnt lgkmcnt(0)\n"
"ds_read_b128 v[16:19],v0 offset:26624\n"
"ds_read_b128 v[20:23],v1 offset:26624\n"
MF "v[108:111],v[24:27],v[192:195],v[108:111]\n"
"v_exp_f32 v125,v125\n"
MF "v[108:111],v[28:31],v[212:215],v[108:111]\n"
"v_exp_f32 v126,v126\n"
MF "v[104:107],v[28:31],v[208:211],v[104:107]\n"
"v_exp_f32 v127,v127\n"
MF "v[104:107],v[24:27],v[188:191],v[104:107]\n"
"v_add_f32 v55,v55,v124\n"
"v_add_f32 v55,v55,v125\n"
MF "v[100:103],v[24:27],v[184:187],v[100:103]\n"
CV "v86,v124,v125\n"
"v_add_f32 v55,v55,v126\n"
MF "v[100:103],v[28:31],v[204:207],v[100:103]\n"
"v_add_f32 v55,v55,v127\n"
CV "v87,v126,v127\n"
MF "v[96:99],v[28:31],v[200:203],v[96:99]\n"
"v_exp_f32 v88,v88\n"
MF "v[96:99],v[24:27],v[180:183],v[96:99]\n"
"v_exp_f32 v89,v89\n"
MF "v[92:95],v[24:27],v[176:179],v[92:95]\n"
"v_exp_f32 v90,v90\n"
MF "v[92:95],v[28:31],v[196:199],v[92:95]\n"
"s_waitcnt lgkmcnt(0)\n"
"ds_read_b128 v[24:27],v0 offset:30720\n"
"ds_read_b128 v[28:31],v1 offset:30720\n"
MF "v[92:95],v[16:19],v[216:219],v[92:95]\n"
"v_exp_f32 v91,v91\n"
MF "v[92:95],v[20:23],v[236:239],v[92:95]\n"
"v_add_f32 v56,v56,v88\n"
"v_add_f32 v56,v56,v89\n"
MF "v[96:99],v[20:23],v[240:243],v[96:99]\n"
CV "v88,v88,v89\n"
"v_add_f32 v56,v56,v90\n"
MF "v[96:99],v[16:19],v[220:223],v[96:99]\n"
"v_add_f32 v56,v56,v91\n"
CV "v89,v90,v91\n"
MF "v[100:103],v[16:19],v[224:227],v[100:103]\n"
"v_exp_f32 v128,v128\n"
MF "v[100:103],v[20:23],v[244:247],v[100:103]\n"
"v_exp_f32 v129,v129\n"
MF "v[104:107],v[20:23],v[248:251],v[104:107]\n"
"v_exp_f32 v130,v130\n"
MF "v[104:107],v[16:19],v[228:231],v[104:107]\n"
"v_exp_f32 v131,v131\n"
MF "v[108:111],v[16:19],v[232:235],v[108:111]\n"
"v_add_f32 v56,v56,v128\n"
"v_add_f32 v56,v56,v129\n"
MF "v[108:111],v[20:23],v[252:255],v[108:111]\n"
"s_waitcnt lgkmcnt(0)\n"
"ds_read_b128 v[16:19],v0 offset:31744\n"
"ds_read_b128 v[20:23],v1 offset:31744\n"
MF "v[112:115],v[24:27],v[136:139],v[32:35]\n"
CV "v90,v128,v129\n"
"v_add_f32 v56,v56,v130\n"
MF "v[112:115],v[28:31],v[156:159],v[112:115]\n"
"v_add_f32 v56,v56,v131\n"
CV "v91,v130,v131\n"
MF "v[116:119],v[28:31],v[160:163],v[36:39]\n"
"v_exp_f32 v92,v92\n"
MF "v[116:119],v[24:27],v[140:143],v[116:119]\n"
"v_exp_f32 v93,v93\n"
MF "v[120:123],v[24:27],v[144:147],v[40:43]\n"
"v_exp_f32 v94,v94\n"
MF "v[120:123],v[28:31],v[164:167],v[120:123]\n"
"v_exp_f32 v95,v95\n"
MF "v[124:127],v[28:31],v[168:171],v[44:47]\n"
"v_add_f32 v57,v52,v92\n"
"v_add_f32 v57,v57,v93\n"
MF "v[124:127],v[24:27],v[148:151],v[124:127]\n"
CV "v92,v92,v93\n"
"v_add_f32 v57,v57,v94\n"
MF "v[128:131],v[24:27],v[152:155],v[48:51]\n"
"v_add_f32 v57,v57,v95\n"
CV "v93,v94,v95\n"
MF "v[128:131],v[28:31],v[172:175],v[128:131]\n"
"s_waitcnt lgkmcnt(0)\n"
"ds_read_b128 v[24:27],v0 offset:32768\n"
"ds_read_b128 v[28:31],v1 offset:32768\n"
MF "v[128:131],v[16:19],v[192:195],v[128:131]\n"
"v_exp_f32 v96,v96\n"
MF "v[128:131],v[20:23],v[212:215],v[128:131]\n"
"v_exp_f32 v97,v97\n"
MF "v[124:127],v[20:23],v[208:211],v[124:127]\n"
"v_exp_f32 v98,v98\n"
MF "v[124:127],v[16:19],v[188:191],v[124:127]\n"
"v_exp_f32 v99,v99\n"
MF "v[120:123],v[16:19],v[184:187],v[120:123]\n"
"v_add_f32 v58,v53,v96\n"
"v_add_f32 v58,v58,v97\n"
MF "v[120:123],v[20:23],v[204:207],v[120:123]\n"
CV "v96,v96,v97\n"
"v_add_f32 v58,v58,v98\n"
MF "v[116:119],v[20:23],v[200:203],v[116:119]\n"
"v_add_f32 v58,v58,v99\n"
CV "v97,v98,v99\n"
MF "v[116:119],v[16:19],v[180:183],v[116:119]\n"
"s_waitcnt vmcnt(6) lgkmcnt(0)\n"
"s_barrier\n"
MF "v[112:115],v[16:19],v[176:179],v[112:115]\n"
"v_exp_f32 v100,v100\n"
MF "v[112:115],v[20:23],v[196:199],v[112:115]\n"
"s_nop 0\n"
TR "v[16:17],v2 offset:49152\n"
TR "v[18:19],v2 offset:55296\n"
MF "v[112:115],v[24:27],v[216:219],v[112:115]\n"
"s_add_u32 s73,s73,12288\n"
TR "v[20:21],v3 offset:49152\n"
TR "v[22:23],v3 offset:55296\n"
MF "v[112:115],v[28:31],v[236:239],v[112:115]\n"
"s_cmp_eq_u32 s73,s19\n"
"s_cselect_b32 s73,s18,s73\n"
"s_add_u32 m0,s65,24576\n"
MF "v[116:119],v[28:31],v[240:243],v[116:119]\n"
LD "v8,s[36:39],s73 offen nt lds\n"
MF "v[116:119],v[24:27],v[220:223],v[116:119]\n"
"s_add_u32 m0,s65,25472\n"
"v_exp_f32 v101,v101\n"
MF "v[120:123],v[24:27],v[224:227],v[120:123]\n"
LD "v8,s[36:39],s73 offen offset:128 nt lds\n"
MF "v[120:123],v[28:31],v[244:247],v[120:123]\n"
"v_exp_f32 v102,v102\n"
MF "v[124:127],v[28:31],v[248:251],v[124:127]\n"
"s_add_u32 m0,s65,26368\n"
"v_exp_f32 v103,v103\n"
MF "v[124:127],v[24:27],v[228:231],v[124:127]\n"
LD "v8,s[36:39],s73 offen offset:256 nt lds\n"
MF "v[128:131],v[24:27],v[232:235],v[128:131]\n"
"v_add_f32 v59,v54,v100\n"
"v_add_f32 v59,v59,v101\n"
MF "v[128:131],v[28:31],v[252:255],v[128:131]\n"
"s_waitcnt lgkmcnt(0)\n"
TR "v[24:25],v4 offset:49152\n"
TR "v[26:27],v4 offset:55296\n"
MF "a[0:3],v[16:19],v[72:75],a[220:223]\n"
"s_add_u32 m0,s65,24576\n"
TR "v[28:29],v5 offset:49152\n"
TR "v[30:31],v5 offset:55296\n"
MF "a[20:23],v[20:23],v[72:75],a[220:223]\n"
LD "v8,s[36:39],s73 offen offset:3072 nt lds\n"
MF "a[24:27],v[20:23],v[76:79],a[220:223]\n"
CV "v100,v100,v101\n"
"v_add_f32 v59,v59,v102\n"
MF "a[4:7],v[16:19],v[76:79],a[220:223]\n"
"s_add_u32 m0,s65,25472\n"
"v_add_f32 v59,v59,v103\n"
CV "v101,v102,v103\n"
MF "a[8:11],v[16:19],v[80:83],a[220:223]\n"
LD "v8,s[36:39],s73 offen offset:3200 nt lds\n"
MF "a[28:31],v[20:23],v[80:83],a[220:223]\n"
"v_exp_f32 v104,v104\n"
MF "a[32:35],v[20:23],v[84:87],a[220:223]\n"
"s_add_u32 m0,s65,26368\n"
"v_exp_f32 v105,v105\n"
MF "a[12:15],v[16:19],v[84:87],a[220:223]\n"
LD "v8,s[36:39],s73 offen offset:3328 nt lds\n"
MF "a[16:19],v[16:19],v[88:91],a[220:223]\n"
"v_exp_f32 v106,v106\n"
MF "a[36:39],v[20:23],v[88:91],a[220:223]\n"
"s_waitcnt lgkmcnt(0)\n"
TR "v[16:17],v2 offset:50176\n"
TR "v[18:19],v2 offset:56320\n"
MF "a[56:59],v[24:27],v[88:91],a[220:223]\n"
"s_nop 0\n"
TR "v[20:21],v3 offset:50176\n"
TR "v[22:23],v3 offset:56320\n"
MF "a[76:79],v[28:31],v[88:91],a[220:223]\n"
"v_exp_f32 v107,v107\n"
MF "a[72:75],v[28:31],v[84:87],a[220:223]\n"
"v_add_f32 v60,v55,v104\n"
"v_add_f32 v60,v60,v105\n"
MF "a[52:55],v[24:27],v[84:87],a[220:223]\n"
CV "v104,v104,v105\n"
"v_add_f32 v60,v60,v106\n"
MF "a[48:51],v[24:27],v[80:83],a[220:223]\n"
"v_add_f32 v60,v60,v107\n"
CV "v105,v106,v107\n"
MF "a[68:71],v[28:31],v[80:83],a[220:223]\n"
"v_exp_f32 v108,v108\n"
MF "a[64:67],v[28:31],v[76:79],a[220:223]\n"
"v_exp_f32 v109,v109\n"
MF "a[44:47],v[24:27],v[76:79],a[220:223]\n"
"v_exp_f32 v110,v110\n"
MF "a[40:43],v[24:27],v[72:75],a[220:223]\n"
"v_exp_f32 v111,v111\n"
MF "a[60:63],v[28:31],v[72:75],a[220:223]\n"
"s_waitcnt lgkmcnt(0)\n"
TR "v[24:25],v4 offset:50176\n"
TR "v[26:27],v4 offset:56320\n"
MF "a[80:83],v[16:19],v[72:75],a[220:223]\n"
"s_nop 0\n"
TR "v[28:29],v5 offset:50176\n"
TR "v[30:31],v5 offset:56320\n"
MF "a[100:103],v[20:23],v[72:75],a[220:223]\n"
"v_add_f32 v61,v56,v108\n"
"v_add_f32 v61,v61,v109\n"
MF "a[104:107],v[20:23],v[76:79],a[220:223]\n"
CV "v108,v108,v109\n"
"v_add_f32 v61,v61,v110\n"
MF "a[84:87],v[16:19],v[76:79],a[220:223]\n"
"v_add_f32 v61,v61,v111\n"
CV "v109,v110,v111\n"
MF "a[88:91],v[16:19],v[80:83],a[220:223]\n"
"v_exp_f32 v112,v112\n"
MF "a[108:111],v[20:23],v[80:83],a[220:223]\n"
"v_exp_f32 v113,v113\n"
MF "a[112:115],v[20:23],v[84:87],a[220:223]\n"
"v_exp_f32 v114,v114\n"
MF "a[92:95],v[16:19],v[84:87],a[220:223]\n"
"v_exp_f32 v115,v115\n"
MF "a[96:99],v[16:19],v[88:91],a[220:223]\n"
"v_add_f32 v57,v57,v112\n"
"v_add_f32 v57,v57,v113\n"
MF "a[116:119],v[20:23],v[88:91],a[220:223]\n"
"s_waitcnt lgkmcnt(0)\n"
TR "v[16:17],v2 offset:51200\n"
TR "v[18:19],v2 offset:57344\n"
MF "a[136:139],v[24:27],v[88:91],a[220:223]\n"
"s_nop 0\n"
TR "v[20:21],v3 offset:51200\n"
TR "v[22:23],v3 offset:57344\n"
MF "a[156:159],v[28:31],v[88:91],a[220:223]\n"
CV "v94,v112,v113\n"
"v_add_f32 v57,v57,v114\n"
MF "a[152:155],v[28:31],v[84:87],a[220:223]\n"
"v_add_f32 v57,v57,v115\n"
CV "v95,v114,v115\n"
MF "a[132:135],v[24:27],v[84:87],a[220:223]\n"
"v_exp_f32 v116,v116\n"
MF "a[128:131],v[24:27],v[80:83],a[220:223]\n"
"v_exp_f32 v117,v117\n"
MF "a[148:151],v[28:31],v[80:83],a[220:223]\n"
"v_exp_f32 v118,v118\n"
MF "a[144:147],v[28:31],v[76:79],a[220:223]\n"
"v_exp_f32 v119,v119\n"
MF "a[124:127],v[24:27],v[76:79],a[220:223]\n"
"v_add_f32 v58,v58,v116\n"
"v_add_f32 v58,v58,v117\n"
MF "a[120:123],v[24:27],v[72:75],a[220:223]\n"
CV "v98,v116,v117\n"
"v_add_f32 v58,v58,v118\n"
MF "a[140:143],v[28:31],v[72:75],a[220:223]\n"
"s_waitcnt lgkmcnt(0)\n"
TR "v[24:25],v4 offset:51200\n"
TR "v[26:27],v4 offset:57344\n"
MF "a[160:163],v[16:19],v[72:75],a[220:223]\n"
"s_nop 0\n"
TR "v[28:29],v5 offset:51200\n"
TR "v[30:31],v5 offset:57344\n"
MF "a[180:183],v[20:23],v[72:75],a[220:223]\n"
"v_add_f32 v58,v58,v119\n"
CV "v99,v118,v119\n"
MF "a[184:187],v[20:23],v[76:79],a[220:223]\n"
"v_exp_f32 v120,v120\n"
MF "a[164:167],v[16:19],v[76:79],a[220:223]\n"
"v_exp_f32 v121,v121\n"
MF "a[168:171],v[16:19],v[80:83],a[220:223]\n"
"v_exp_f32 v122,v122\n"
MF "a[188:191],v[20:23],v[80:83],a[220:223]\n"
"v_exp_f32 v123,v123\n"
MF "a[192:195],v[20:23],v[84:87],a[220:223]\n"
"v_add_f32 v59,v59,v120\n"
"v_add_f32 v59,v59,v121\n"
MF "a[172:175],v[16:19],v[84:87],a[220:223]\n"
CV "v102,v120,v121\n"
"v_add_f32 v59,v59,v122\n"
MF "a[176:179],v[16:19],v[88:91],a[220:223]\n"
"s_waitcnt vmcnt(6) lgkmcnt(0)\n"
"s_barrier\n"
MF "a[196:199],v[20:23],v[88:91],a[220:223]\n"
"s_add_u32 s72,s72,6144\n"
"ds_read_b128 v[16:19],v0\n"
"ds_read_b128 v[20:23],v1\n"
MF "a[216:219],v[24:27],v[88:91],a[220:223]\n"
"s_cmp_eq_u32 s72,s17\n"
"s_cselect_b32 s72,s16,s72\n"
"s_add_u32 m0,s64,49152\n"
MF "a[236:239],v[28:31],v[88:91],a[220:223]\n"
LD "v7,s[32:35],s72 offen nt lds\n"
MF "a[232:235],v[28:31],v[84:87],a[220:223]\n"
"s_add_u32 m0,s64,50048\n"
"v_add_f32 v59,v59,v123\n"
CV "v103,v122,v123\n"
MF "a[212:215],v[24:27],v[84:87],a[220:223]\n"
LD "v7,s[32:35],s72 offen offset:128 nt lds\n"
MF "a[208:211],v[24:27],v[80:83],a[220:223]\n"
"s_add_u32 m0,s64,50944\n"
MF "a[228:231],v[28:31],v[80:83],a[220:223]\n"
LD "v7,s[32:35],s72 offen offset:256 nt lds\n"
MF "a[224:227],v[28:31],v[76:79],a[220:223]\n"
MF "a[204:207],v[24:27],v[76:79],a[220:223]\n"
MF "a[200:203],v[24:27],v[72:75],a[220:223]\n"
MF "a[220:223],v[28:31],v[72:75],a[220:223]\n"
"s_waitcnt lgkmcnt(0)\n"
"s_branch .Lmid\n"
".Laloop:\n"
MF "v[72:75],v[16:19],v[136:139],v[32:35]\n"
"s_nop 0\n"
"ds_read_b128 v[24:27],v0 offset:25600\n"
"ds_read_b128 v[28:31],v1 offset:25600\n"
MF "v[72:75],v[20:23],v[156:159],v[72:75]\n"
MF "v[76:79],v[20:23],v[160:163],v[36:39]\n"
MF "v[76:79],v[16:19],v[140:143],v[76:79]\n"
MF "v[80:83],v[16:19],v[144:147],v[40:43]\n"
MF "v[80:83],v[20:23],v[164:167],v[80:83]\n"
MF "v[84:87],v[20:23],v[168:171],v[44:47]\n"
MF "v[84:87],v[16:19],v[148:151],v[84:87]\n"
"s_add_u32 s72,s72,6144\n"
"s_add_u32 m0,s64,6144\n"
MF "v[88:91],v[16:19],v[152:155],v[48:51]\n"
LD "v7,s[32:35],s72 offen nt lds\n"
MF "v[88:91],v[20:23],v[172:175],v[88:91]\n"
"s_waitcnt lgkmcnt(0)\n"
"ds_read_b128 v[16:19],v0 offset:26624\n"
"ds_read_b128 v[20:23],v1 offset:26624\n"
MF "v[88:91],v[24:27],v[192:195],v[88:91]\n"
"v_exp_f32 v124,v124\n"
MF "v[88:91],v[28:31],v[212:215],v[88:91]\n"
"s_add_u32 m0,s64,7040\n"
"v_exp_f32 v125,v125\n"
MF "v[84:87],v[28:31],v[208:211],v[84:87]\n"
LD "v7,s[32:35],s72 offen offset:128 nt lds\n"
MF "v[84:87],v[24:27],v[188:191],v[84:87]\n"
"v_exp_f32 v126,v126\n"
MF "v[80:83],v[24:27],v[184:187],v[80:83]\n"
"v_exp_f32 v127,v127\n"
MF "v[80:83],v[28:31],v[204:207],v[80:83]\n"
"s_add_u32 m0,s64,7936\n"
"v_add_f32 v60,v60,v124\n"
"v_add_f32 v60,v60,v125\n"
MF "v[76:79],v[28:31],v[200:203],v[76:79]\n"
LD "v7,s[32:35],s72 offen offset:256 nt lds\n"
MF "v[76:79],v[24:27],v[180:183],v[76:79]\n"
CV "v106,v124,v125\n"
"v_add_f32 v60,v60,v126\n"
MF "v[72:75],v[24:27],v[176:179],v[72:75]\n"
"v_add_f32 v60,v60,v127\n"
CV "v107,v126,v127\n"
MF "v[72:75],v[28:31],v[196:199],v[72:75]\n"
"s_waitcnt lgkmcnt(0)\n"
"ds_read_b128 v[24:27],v0 offset:30720\n"
"ds_read_b128 v[28:31],v1 offset:30720\n"
MF "v[72:75],v[16:19],v[216:219],v[72:75]\n"
"v_exp_f32 v128,v128\n"
MF "v[72:75],v[20:23],v[236:239],v[72:75]\n"
"v_exp_f32 v129,v129\n"
MF "v[76:79],v[20:23],v[240:243],v[76:79]\n"
"v_exp_f32 v130,v130\n"
MF "v[76:79],v[16:19],v[220:223],v[76:79]\n"
"v_exp_f32 v131,v131\n"
MF "v[80:83],v[16:19],v[224:227],v[80:83]\n"
"v_add_f32 v61,v61,v128\n"
"v_add_f32 v61,v61,v129\n"
MF "v[80:83],v[20:23],v[244:247],v[80:83]\n"
CV "v110,v128,v129\n"
"v_add_f32 v61,v61,v130\n"
MF "v[84:87],v[20:23],v[248:251],v[84:87]\n"
"v_add_f32 v61,v61,v131\n"
CV "v111,v130,v131\n"
MF "v[84:87],v[16:19],v[228:231],v[84:87]\n"
"v_max3_f32 v10,v57,v58,v59\n"
"v_max3_f32 v10,v10,v60,v61\n"
MF "v[88:91],v[16:19],v[232:235],v[88:91]\n"
"v_cmp_ngt_f32 vcc,s77,v10\n"
MF "v[88:91],v[20:23],v[252:255],v[88:91]\n"
"s_waitcnt lgkmcnt(0)\n"
"s_cbranch_vccnz .Lstub_11\n"
".Lback_11:\n"
MF "v[112:115],v[24:27],v[136:139],v[32:35]\n"
"s_add_u32 s74,s74,12288\n"
"ds_read_b128 v[16:19],v0 offset:31744\n"
"ds_read_b128 v[20:23],v1 offset:31744\n"
MF "v[112:115],v[28:31],v[156:159],v[112:115]\n"
"s_cmp_eq_u32 s74,s20\n"
"s_cselect_b32 s74,s70,s74\n"
MF "v[116:119],v[28:31],v[160:163],v[36:39]\n"
"v_exp_f32 v72,v72\n"
MF "v[116:119],v[24:27],v[140:143],v[116:119]\n"
"v_exp_f32 v73,v73\n"
MF "v[120:123],v[24:27],v[144:147],v[40:43]\n"
"v_exp_f32 v74,v74\n"
MF "v[120:123],v[28:31],v[164:167],v[120:123]\n"
"v_exp_f32 v75,v75\n"
MF "v[124:127],v[28:31],v[168:171],v[44:47]\n"
"v_add_f32 v52,v57,v72\n"
"v_add_f32 v52,v52,v73\n"
MF "v[124:127],v[24:27],v[148:151],v[124:127]\n"
CV "v72,v72,v73\n"
"v_add_f32 v52,v52,v74\n"
MF "v[128:131],v[24:27],v[152:155],v[48:51]\n"
"v_add_f32 v52,v52,v75\n"
CV "v73,v74,v75\n"
MF "v[128:131],v[28:31],v[172:175],v[128:131]\n"
"s_waitcnt lgkmcnt(0)\n"
"ds_read_b128 v[24:27],v0 offset:32768\n"
"ds_read_b128 v[28:31],v1 offset:32768\n"
MF "v[128:131],v[16:19],v[192:195],v[128:131]\n"
"v_exp_f32 v76,v76\n"
MF "v[128:131],v[20:23],v[212:215],v[128:131]\n"
"v_exp_f32 v77,v77\n"
MF "v[124:127],v[20:23],v[208:211],v[124:127]\n"
"v_exp_f32 v78,v78\n"
MF "v[124:127],v[16:19],v[188:191],v[124:127]\n"
"v_exp_f32 v79,v79\n"
MF "v[120:123],v[16:19],v[184:187],v[120:123]\n"
"v_add_f32 v53,v58,v76\n"
"v_add_f32 v53,v53,v77\n"
MF "v[120:123],v[20:23],v[204:207],v[120:123]\n"
CV "v76,v76,v77\n"
"v_add_f32 v53,v53,v78\n"
MF "v[116:119],v[20:23],v[200:203],v[116:119]\n"
"v_add_f32 v53,v53,v79\n"
CV "v77,v78,v79\n"
MF "v[116:119],v[16:19],v[180:183],v[116:119]\n"
"s_waitcnt vmcnt(6) lgkmcnt(0)\n"
"s_barrier\n"
MF "v[112:115],v[16:19],v[176:179],v[112:115]\n"
"v_exp_f32 v80,v80\n"
MF "v[112:115],v[20:23],v[196:199],v[112:115]\n"
"s_nop 0\n"
TR "v[16:17],v2 offset:49152\n"
TR "v[18:19],v2 offset:55296\n"
MF "v[112:115],v[24:27],v[216:219],v[112:115]\n"
"s_add_u32 s73,s73,12288\n"
TR "v[20:21],v3 offset:49152\n"
TR "v[22:23],v3 offset:55296\n"
MF "v[112:115],v[28:31],v[236:239],v[112:115]\n"
"s_cmp_eq_u32 s73,s19\n"
"s_cselect_b32 s73,s18,s73\n"
"s_add_u32 m0,s65,24576\n"
MF "v[116:119],v[28:31],v[240:243],v[116:119]\n"
LD "v8,s[36:39],s73 offen nt lds\n"
MF "v[116:119],v[24:27],v[220:223],v[116:119]\n"
"s_add_u32 m0,s65,25472\n"
"v_exp_f32 v81,v81\n"
MF "v[120:123],v[24:27],v[224:227],v[120:123]\n"
LD "v8,s[36:39],s73 offen offset:128 nt lds\n"
MF "v[120:123],v[28:31],v[244:247],v[120:123]\n"
"v_exp_f32 v82,v82\n"
MF "v[124:127],v[28:31],v[248:251],v[124:127]\n"
"s_add_u32 m0,s65,26368\n"
"v_exp_f32 v83,v83\n"
MF "v[124:127],v[24:27],v[228:231],v[124:127]\n"
LD "v8,s[36:39],s73 offen offset:256 nt lds\n"
MF "v[128:131],v[24:27],v[232:235],v[128:131]\n"
"v_add_f32 v54,v59,v80\n"
"v_add_f32 v54,v54,v81\n"
MF "v[128:131],v[28:31],v[252:255],v[128:131]\n"
"s_waitcnt lgkmcnt(0)\n"
TR "v[24:25],v4 offset:49152\n"
TR "v[26:27],v4 offset:55296\n"
MF "a[0:3],v[16:19],v[92:95],a[0:3]\n"
"s_add_u32 m0,s65,24576\n"
TR "v[28:29],v5 offset:49152\n"
TR "v[30:31],v5 offset:55296\n"
MF "a[20:23],v[20:23],v[92:95],a[20:23]\n"
LD "v8,s[36:39],s73 offen offset:3072 nt lds\n"
MF "a[24:27],v[20:23],v[96:99],a[24:27]\n"
CV "v80,v80,v81\n"
"v_add_f32 v54,v54,v82\n"
MF "a[4:7],v[16:19],v[96:99],a[4:7]\n"
"s_add_u32 m0,s65,25472\n"
"v_add_f32 v54,v54,v83\n"
CV "v81,v82,v83\n"
MF "a[8:11],v[16:19],v[100:103],a[8:11]\n"
LD "v8,s[36:39],s73 offen offset:3200 nt lds\n"
MF "a[28:31],v[20:23],v[100:103],a[28:31]\n"
"v_exp_f32 v84,v84\n"
MF "a[32:35],v[20:23],v[104:107],a[32:35]\n"
"s_add_u32 m0,s65,26368\n"
"v_exp_f32 v85,v85\n"
MF "a[12:15],v[16:19],v[104:107],a[12:15]\n"
LD "v8,s[36:39],s73 offen offset:3328 nt lds\n"
MF "a[16:19],v[16:19],v[108:111],a[16:19]\n"
"v_exp_f32 v86,v86\n"
MF "a[36:39],v[20:23],v[108:111],a[36:39]\n"
"s_waitcnt lgkmcnt(0)\n"
TR "v[16:17],v2 offset:50176\n"
TR "v[18:19],v2 offset:56320\n"
MF "a[56:59],v[24:27],v[108:111],a[56:59]\n"
"s_nop 0\n"
TR "v[20:21],v3 offset:50176\n"
TR "v[22:23],v3 offset:56320\n"
MF "a[76:79],v[28:31],v[108:111],a[76:79]\n"
"v_exp_f32 v87,v87\n"
MF "a[72:75],v[28:31],v[104:107],a[72:75]\n"
"v_add_f32 v55,v60,v84\n"
"v_add_f32 v55,v55,v85\n"
MF "a[52:55],v[24:27],v[104:107],a[52:55]\n"
CV "v84,v84,v85\n"
"v_add_f32 v55,v55,v86\n"
MF "a[48:51],v[24:27],v[100:103],a[48:51]\n"
"v_add_f32 v55,v55,v87\n"
CV "v85,v86,v87\n"
MF "a[68:71],v[28:31],v[100:103],a[68:71]\n"
"v_exp_f32 v88,v88\n"
MF "a[64:67],v[28:31],v[96:99],a[64:67]\n"
"v_exp_f32 v89,v89\n"
MF "a[44:47],v[24:27],v[96:99],a[44:47]\n"
"v_exp_f32 v90,v90\n"
MF "a[40:43],v[24:27],v[92:95],a[40:43]\n"
"v_exp_f32 v91,v91\n"
MF "a[60:63],v[28:31],v[92:95],a[60:63]\n"
"s_waitcnt lgkmcnt(0)\n"
TR "v[24:25],v4 offset:50176\n"
TR "v[26:27],v4 offset:56320\n"
MF "a[80:83],v[16:19],v[92:95],a[80:83]\n"
"s_nop 0\n"
TR "v[28:29],v5 offset:50176\n"
TR "v[30:31],v5 offset:56320\n"
MF "a[100:103],v[20:23],v[92:95],a[100:103]\n"
"v_add_f32 v56,v61,v88\n"
"v_add_f32 v56,v56,v89\n"
MF "a[104:107],v[20:23],v[96:99],a[104:107]\n"
CV "v88,v88,v89\n"
"v_add_f32 v56,v56,v90\n"
MF "a[84:87],v[16:19],v[96:99],a[84:87]\n"
"v_add_f32 v56,v56,v91\n"
CV "v89,v90,v91\n"
MF "a[88:91],v[16:19],v[100:103],a[88:91]\n"
"v_exp_f32 v112,v112\n"
MF "a[108:111],v[20:23],v[100:103],a[108:111]\n"
"v_exp_f32 v113,v113\n"
MF "a[112:115],v[20:23],v[104:107],a[112:115]\n"
"v_exp_f32 v114,v114\n"
MF "a[92:95],v[16:19],v[104:107],a[92:95]\n"
"v_exp_f32 v115,v115\n"
MF "a[96:99],v[16:19],v[108:111],a[96:99]\n"
"v_add_f32 v52,v52,v112\n"
"v_add_f32 v52,v52,v113\n"
MF "a[116:119],v[20:23],v[108:111],a[116:119]\n"
"s_waitcnt lgkmcnt(0)\n"
TR "v[16:17],v2 offset:51200\n"
TR "v[18:19],v2 offset:57344\n"
MF "a[136:139],v[24:27],v[108:111],a[136:139]\n"
"s_nop 0\n"
TR "v[20:21],v3 offset:51200\n"
TR "v[22:23],v3 offset:57344\n"
MF "a[156:159],v[28:31],v[108:111],a[156:159]\n"
CV "v74,v112,v113\n"
"v_add_f32 v52,v52,v114\n"
MF "a[152:155],v[28:31],v[104:107],a[152:155]\n"
"v_add_f32 v52,v52,v115\n"
CV "v75,v114,v115\n"
MF "a[132:135],v[24:27],v[104:107],a[132:135]\n"
"v_exp_f32 v116,v116\n"
MF "a[128:131],v[24:27],v[100:103],a[128:131]\n"
"v_exp_f32 v117,v117\n"
MF "a[148:151],v[28:31],v[100:103],a[148:151]\n"
"v_exp_f32 v118,v118\n"
MF "a[144:147],v[28:31],v[96:99],a[144:147]\n"
"v_exp_f32 v119,v119\n"
MF "a[124:127],v[24:27],v[96:99],a[124:127]\n"
"v_add_f32 v53,v53,v116\n"
"v_add_f32 v53,v53,v117\n"
MF "a[120:123],v[24:27],v[92:95],a[120:123]\n"
CV "v78,v116,v117\n"
"v_add_f32 v53,v53,v118\n"
MF "a[140:143],v[28:31],v[92:95],a[140:143]\n"
"s_waitcnt lgkmcnt(0)\n"
TR "v[24:25],v4 offset:51200\n"
TR "v[26:27],v4 offset:57344\n"
MF "a[160:163],v[16:19],v[92:95],a[160:163]\n"
"s_nop 0\n"
TR "v[28:29],v5 offset:51200\n"
TR "v[30:31],v5 offset:57344\n"
MF "a[180:183],v[20:23],v[92:95],a[180:183]\n"
"v_add_f32 v53,v53,v119\n"
CV "v79,v118,v119\n"
MF "a[184:187],v[20:23],v[96:99],a[184:187]\n"
"v_exp_f32 v120,v120\n"
MF "a[164:167],v[16:19],v[96:99],a[164:167]\n"
"v_exp_f32 v121,v121\n"
MF "a[168:171],v[16:19],v[100:103],a[168:171]\n"
"v_exp_f32 v122,v122\n"
MF "a[188:191],v[20:23],v[100:103],a[188:191]\n"
"v_exp_f32 v123,v123\n"
MF "a[192:195],v[20:23],v[104:107],a[192:195]\n"
"v_add_f32 v54,v54,v120\n"
"v_add_f32 v54,v54,v121\n"
MF "a[172:175],v[16:19],v[104:107],a[172:175]\n"
CV "v82,v120,v121\n"
"v_add_f32 v54,v54,v122\n"
MF "a[176:179],v[16:19],v[108:111],a[176:179]\n"
"s_waitcnt vmcnt(6) lgkmcnt(0)\n"
"s_barrier\n"
MF "a[196:199],v[20:23],v[108:111],a[196:199]\n"
"s_add_u32 s72,s72,6144\n"
"ds_read_b128 v[16:19],v0\n"
"ds_read_b128 v[20:23],v1\n"
MF "a[216:219],v[24:27],v[108:111],a[216:219]\n"
"s_cmp_eq_u32 s72,s17\n"
"s_cselect_b32 s72,s16,s72\n"
"s_add_u32 m0,s64,49152\n"
MF "a[236:239],v[28:31],v[108:111],a[236:239]\n"
LD "v7,s[32:35],s72 offen nt lds\n"
MF "a[232:235],v[28:31],v[104:107],a[232:235]\n"
"s_add_u32 m0,s64,50048\n"
"v_add_f32 v54,v54,v123\n"
CV "v83,v122,v123\n"
MF "a[212:215],v[24:27],v[104:107],a[212:215]\n"
LD "v7,s[32:35],s72 offen offset:128 nt lds\n"
MF "a[208:211],v[24:27],v[100:103],a[208:211]\n"
"s_add_u32 m0,s64,50944\n"
MF "a[228:231],v[28:31],v[100:103],a[228:231]\n"
LD "v7,s[32:35],s72 offen offset:256 nt lds\n"
MF "a[224:227],v[28:31],v[96:99],a[224:227]\n"
MF "a[204:207],v[24:27],v[96:99],a[204:207]\n"
MF "a[200:203],v[24:27],v[92:95],a[200:203]\n"
MF "a[220:223],v[28:31],v[92:95],a[220:223]\n"
"s_waitcnt lgkmcnt(0)\n"
MF "v[92:95],v[16:19],v[136:139],v[32:35]\n"
"s_add_u32 s72,s72,6144\n"
"ds_read_b128 v[24:27],v0 offset:1024\n"
"ds_read_b128 v[28:31],v1 offset:1024\n"
MF "v[92:95],v[20:23],v[156:159],v[92:95]\n"
"s_add_u32 m0,s64,55296\n"
"v_exp_f32 v124,v124\n"
MF "v[96:99],v[20:23],v[160:163],v[36:39]\n"
LD "v7,s[32:35],s72 offen nt lds\n"
MF "v[96:99],v[16:19],v[140:143],v[96:99]\n"
"v_exp_f32 v125,v125\n"
MF "v[100:103],v[16:19],v[144:147],v[40:43]\n"
"s_add_u32 m0,s64,56192\n"
"v_exp_f32 v126,v126\n"
MF "v[100:103],v[20:23],v[164:167],v[100:103]\n"
LD "v7,s[32:35],s72 offen offset:128 nt lds\n"
MF "v[104:107],v[20:23],v[168:171],v[44:47]\n"
"v_exp_f32 v127,v127\n"
MF "v[104:107],v[16:19],v[148:151],v[104:107]\n"
"s_add_u32 m0,s64,57088\n"
"v_add_f32 v55,v55,v124\n"
"v_add_f32 v55,v55,v125\n"
MF "v[108:111],v[16:19],v[152:155],v[48:51]\n"
LD "v7,s[32:35],s72 offen offset:256 nt lds\n"
MF "v[108:111],v[20:23],v[172:175],v[108:111]\n"
"s_waitcnt lgkmcnt(0)\n"
"ds_read_b128 v[16:19],v0 offset:2048\n"
"ds_read_b128 v[20:23],v1 offset:2048\n"
MF "v[108:111],v[24:27],v[192:195],v[108:111]\n"
CV "v86,v124,v125\n"
"v_add_f32 v55,v55,v126\n"
MF "v[108:111],v[28:31],v[212:215],v[108:111]\n"
"v_add_f32 v55,v55,v127\n"
CV "v87,v126,v127\n"
MF "v[104:107],v[28:31],v[208:211],v[104:107]\n"
"v_exp_f32 v128,v128\n"
MF "v[104:107],v[24:27],v[188:191],v[104:107]\n"
"v_exp_f32 v129,v129\n"
MF "v[100:103],v[24:27],v[184:187],v[100:103]\n"
"v_exp_f32 v130,v130\n"
MF "v[100:103],v[28:31],v[204:207],v[100:103]\n"
"v_exp_f32 v131,v131\n"
MF "v[96:99],v[28:31],v[200:203],v[96:99]\n"
"v_add_f32 v56,v56,v128\n"
"v_add_f32 v56,v56,v129\n"
MF "v[96:99],v[24:27],v[180:183],v[96:99]\n"
CV "v90,v128,v129\n"
"v_add_f32 v56,v56,v130\n"
MF "v[92:95],v[24:27],v[176:179],v[92:95]\n"
"v_add_f32 v56,v56,v131\n"
CV "v91,v130,v131\n"
MF "v[92:95],v[28:31],v[196:199],v[92:95]\n"
"s_waitcnt lgkmcnt(0)\n"
"ds_read_b128 v[24:27],v0 offset:6144\n"
"ds_read_b128 v[28:31],v1 offset:6144\n"
MF "v[92:95],v[16:19],v[216:219],v[92:95]\n"
MF "v[92:95],v[20:23],v[236:239],v[92:95]\n"
MF "v[96:99],v[20:23],v[240:243],v[96:99]\n"
MF "v[96:99],v[16:19],v[220:223],v[96:99]\n"
MF "v[100:103],v[16:19],v[224:227],v[100:103]\n"
MF "v[100:103],v[20:23],v[244:247],v[100:103]\n"
MF "v[104:107],v[20:23],v[248:251],v[104:107]\n"
MF "v[104:107],v[16:19],v[228:231],v[104:107]\n"
"v_max3_f32 v10,v52,v53,v54\n"
"v_max3_f32 v10,v10,v55,v56\n"
MF "v[108:111],v[16:19],v[232:235],v[108:111]\n"
"v_cmp_ngt_f32 vcc,s77,v10\n"
MF "v[108:111],v[20:23],v[252:255],v[108:111]\n"
"s_waitcnt lgkmcnt(0)\n"
"s_cbranch_vccnz .Lstub_12\n"
".Lback_12:\n"
MF "v[112:115],v[24:27],v[136:139],v[32:35]\n"
"s_add_u32 s74,s74,12288\n"
"ds_read_b128 v[16:19],v0 offset:7168\n"
"ds_read_b128 v[20:23],v1 offset:7168\n"
MF "v[112:115],v[28:31],v[156:159],v[112:115]\n"
"s_cmp_eq_u32 s74,s20\n"
"s_cselect_b32 s74,s70,s74\n"
MF "v[116:119],v[28:31],v[160:163],v[36:39]\n"
"v_exp_f32 v92,v92\n"
MF "v[116:119],v[24:27],v[140:143],v[116:119]\n"
"v_exp_f32 v93,v93\n"
MF "v[120:123],v[24:27],v[144:147],v[40:43]\n"
"v_exp_f32 v94,v94\n"
MF "v[120:123],v[28:31],v[164:167],v[120:123]\n"
"v_exp_f32 v95,v95\n"
MF "v[124:127],v[28:31],v[168:171],v[44:47]\n"
"v_add_f32 v57,v52,v92\n"
"v_add_f32 v57,v57,v93\n"
MF "v[124:127],v[24:27],v[148:151],v[124:127]\n"
CV "v92,v92,v93\n"
"v_add_f32 v57,v57,v94\n"
MF "v[128:131],v[24:27],v[152:155],v[48:51]\n"
"v_add_f32 v57,v57,v95\n"
CV "v93,v94,v95\n"
MF "v[128:131],v[28:31],v[172:175],v[128:131]\n"
"s_waitcnt lgkmcnt(0)\n"
"ds_read_b128 v[24:27],v0 offset:8192\n"
"ds_read_b128 v[28:31],v1 offset:8192\n"
MF "v[128:131],v[16:19],v[192:195],v[128:131]\n"
"v_exp_f32 v96,v96\n"
MF "v[128:131],v[20:23],v[212:215],v[128:131]\n"
"v_exp_f32 v97,v97\n"
MF "v[124:127],v[20:23],v[208:211],v[124:127]\n"
"v_exp_f32 v98,v98\n"
MF "v[124:127],v[16:19],v[188:191],v[124:127]\n"
"v_exp_f32 v99,v99\n"
MF "v[120:123],v[16:19],v[184:187],v[120:123]\n"
"v_add_f32 v58,v53,v96\n"
"v_add_f32 v58,v58,v97\n"
MF "v[120:123],v[20:23],v[204:207],v[120:123]\n"
CV "v96,v96,v97\n"
"v_add_f32 v58,v58,v98\n"
MF "v[116:119],v[20:23],v[200:203],v[116:119]\n"
"v_add_f32 v58,v58,v99\n"
CV "v97,v98,v99\n"
MF "v[116:119],v[16:19],v[180:183],v[116:119]\n"
"s_waitcnt vmcnt(6) lgkmcnt(0)\n"
"s_barrier\n"
MF "v[112:115],v[16:19],v[176:179],v[112:115]\n"
"v_exp_f32 v100,v100\n"
MF "v[112:115],v[20:23],v[196:199],v[112:115]\n"
"s_nop 0\n"
TR "v[16:17],v2 offset:24576\n"
TR "v[18:19],v2 offset:30720\n"
MF "v[112:115],v[24:27],v[216:219],v[112:115]\n"
"s_add_u32 s73,s73,12288\n"
TR "v[20:21],v3 offset:24576\n"
TR "v[22:23],v3 offset:30720\n"
MF "v[112:115],v[28:31],v[236:239],v[112:115]\n"
"s_cmp_eq_u32 s73,s19\n"
"s_cselect_b32 s73,s18,s73\n"
"s_add_u32 m0,s65,0\n"
MF "v[116:119],v[28:31],v[240:243],v[116:119]\n"
LD "v8,s[36:39],s73 offen nt lds\n"
MF "v[116:119],v[24:27],v[220:223],v[116:119]\n"
"s_add_u32 m0,s65,896\n"
"v_exp_f32 v101,v101\n"
MF "v[120:123],v[24:27],v[224:227],v[120:123]\n"
LD "v8,s[36:39],s73 offen offset:128 nt lds\n"
MF "v[120:123],v[28:31],v[244:247],v[120:123]\n"
"v_exp_f32 v102,v102\n"
MF "v[124:127],v[28:31],v[248:251],v[124:127]\n"
"s_add_u32 m0,s65,1792\n"
"v_exp_f32 v103,v103\n"
MF "v[124:127],v[24:27],v[228:231],v[124:127]\n"
LD "v8,s[36:39],s73 offen offset:256 nt lds\n"
MF "v[128:131],v[24:27],v[232:235],v[128:131]\n"
"v_add_f32 v59,v54,v100\n"
"v_add_f32 v59,v59,v101\n"
MF "v[128:131],v[28:31],v[252:255],v[128:131]\n"
"s_waitcnt lgkmcnt(0)\n"
TR "v[24:25],v4 offset:24576\n"
TR "v[26:27],v4 offset:30720\n"
MF "a[0:3],v[16:19],v[72:75],a[0:3]\n"
"s_add_u32 m0,s65,0\n"
TR "v[28:29],v5 offset:24576\n"
TR "v[30:31],v5 offset:30720\n"
MF "a[20:23],v[20:23],v[72:75],a[20:23]\n"
LD "v8,s[36:39],s73 offen offset:3072 nt lds\n"
MF "a[24:27],v[20:23],v[76:79],a[24:27]\n"
CV "v100,v100,v101\n"
"v_add_f32 v59,v59,v102\n"
MF "a[4:7],v[16:19],v[76:79],a[4:7]\n"
"s_add_u32 m0,s65,896\n"
"v_add_f32 v59,v59,v103\n"
CV "v101,v102,v103\n"
MF "a[8:11],v[16:19],v[80:83],a[8:11]\n"
LD "v8,s[36:39],s73 offen offset:3200 nt lds\n"
MF "a[28:31],v[20:23],v[80:83],a[28:31]\n"
"v_exp_f32 v104,v104\n"
MF "a[32:35],v[20:23],v[84:87],a[32:35]\n"
"s_add_u32 m0,s65,1792\n"
"v_exp_f32 v105,v105\n"
MF "a[12:15],v[16:19],v[84:87],a[12:15]\n"
LD "v8,s[36:39],s73 offen offset:3328 nt lds\n"
MF "a[16:19],v[16:19],v[88:91],a[16:19]\n"
"v_exp_f32 v106,v106\n"
MF "a[36:39],v[20:23],v[88:91],a[36:39]\n"
"s_waitcnt lgkmcnt(0)\n"
TR "v[16:17],v2 offset:25600\n"
TR "v[18:19],v2 offset:31744\n"
MF "a[56:59],v[24:27],v[88:91],a[56:59]\n"
"s_nop 0\n"
TR "v[20:21],v3 offset:25600\n"
TR "v[22:23],v3 offset:31744\n"
MF "a[76:79],v[28:31],v[88:91],a[76:79]\n"
"v_exp_f32 v107,v107\n"
MF "a[72:75],v[28:31],v[84:87],a[72:75]\n"
"v_add_f32 v60,v55,v104\n"
"v_add_f32 v60,v60,v105\n"
MF "a[52:55],v[24:27],v[84:87],a[52:55]\n"
CV "v104,v104,v105\n"
"v_add_f32 v60,v60,v106\n"
MF "a[48:51],v[24:27],v[80:83],a[48:51]\n"
"v_add_f32 v60,v60,v107\n"
CV "v105,v106,v107\n"
MF "a[68:71],v[28:31],v[80:83],a[68:71]\n"
"v_exp_f32 v108,v108\n"
MF "a[64:67],v[28:31],v[76:79],a[64:67]\n"
"v_exp_f32 v109,v109\n"
MF "a[44:47],v[24:27],v[76:79],a[44:47]\n"
"v_exp_f32 v110,v110\n"
MF "a[40:43],v[24:27],v[72:75],a[40:43]\n"
"v_exp_f32 v111,v111\n"
MF "a[60:63],v[28:31],v[72:75],a[60:63]\n"
"s_waitcnt lgkmcnt(0)\n"
TR "v[24:25],v4 offset:25600\n"
TR "v[26:27],v4 offset:31744\n"
MF "a[80:83],v[16:19],v[72:75],a[80:83]\n"
"s_nop 0\n"
TR "v[28:29],v5 offset:25600\n"
TR "v[30:31],v5 offset:31744\n"
MF "a[100:103],v[20:23],v[72:75],a[100:103]\n"
"v_add_f32 v61,v56,v108\n"
"v_add_f32 v61,v61,v109\n"
MF "a[104:107],v[20:23],v[76:79],a[104:107]\n"
CV "v108,v108,v109\n"
"v_add_f32 v61,v61,v110\n"
MF "a[84:87],v[16:19],v[76:79],a[84:87]\n"
"v_add_f32 v61,v61,v111\n"
CV "v109,v110,v111\n"
MF "a[88:91],v[16:19],v[80:83],a[88:91]\n"
"v_exp_f32 v112,v112\n"
MF "a[108:111],v[20:23],v[80:83],a[108:111]\n"
"v_exp_f32 v113,v113\n"
MF "a[112:115],v[20:23],v[84:87],a[112:115]\n"
"v_exp_f32 v114,v114\n"
MF "a[92:95],v[16:19],v[84:87],a[92:95]\n"
"v_exp_f32 v115,v115\n"
MF "a[96:99],v[16:19],v[88:91],a[96:99]\n"
"v_add_f32 v57,v57,v112\n"
"v_add_f32 v57,v57,v113\n"
MF "a[116:119],v[20:23],v[88:91],a[116:119]\n"
"s_waitcnt lgkmcnt(0)\n"
TR "v[16:17],v2 offset:26624\n"
TR "v[18:19],v2 offset:32768\n"
MF "a[136:139],v[24:27],v[88:91],a[136:139]\n"
"s_nop 0\n"
TR "v[20:21],v3 offset:26624\n"
TR "v[22:23],v3 offset:32768\n"
MF "a[156:159],v[28:31],v[88:91],a[156:159]\n"
CV "v94,v112,v113\n"
"v_add_f32 v57,v57,v114\n"
MF "a[152:155],v[28:31],v[84:87],a[152:155]\n"
"v_add_f32 v57,v57,v115\n"
CV "v95,v114,v115\n"
MF "a[132:135],v[24:27],v[84:87],a[132:135]\n"
"v_exp_f32 v116,v116\n"
MF "a[128:131],v[24:27],v[80:83],a[128:131]\n"
"v_exp_f32 v117,v117\n"
MF "a[148:151],v[28:31],v[80:83],a[148:151]\n"
"v_exp_f32 v118,v118\n"
MF "a[144:147],v[28:31],v[76:79],a[144:147]\n"
"v_exp_f32 v119,v119\n"
MF "a[124:127],v[24:27],v[76:79],a[124:127]\n"
"v_add_f32 v58,v58,v116\n"
"v_add_f32 v58,v58,v117\n"
MF "a[120:123],v[24:27],v[72:75],a[120:123]\n"
CV "v98,v116,v117\n"
"v_add_f32 v58,v58,v118\n"
MF "a[140:143],v[28:31],v[72:75],a[140:143]\n"
"s_waitcnt lgkmcnt(0)\n"
TR "v[24:25],v4 offset:26624\n"
TR "v[26:27],v4 offset:32768\n"
MF "a[160:163],v[16:19],v[72:75],a[160:163]\n"
"s_nop 0\n"
TR "v[28:29],v5 offset:26624\n"
TR "v[30:31],v5 offset:32768\n"
MF "a[180:183],v[20:23],v[72:75],a[180:183]\n"
"v_add_f32 v58,v58,v119\n"
CV "v99,v118,v119\n"
MF "a[184:187],v[20:23],v[76:79],a[184:187]\n"
"v_exp_f32 v120,v120\n"
MF "a[164:167],v[16:19],v[76:79],a[164:167]\n"
"v_exp_f32 v121,v121\n"
MF "a[168:171],v[16:19],v[80:83],a[168:171]\n"
"v_exp_f32 v122,v122\n"
MF "a[188:191],v[20:23],v[80:83],a[188:191]\n"
"v_exp_f32 v123,v123\n"
MF "a[192:195],v[20:23],v[84:87],a[192:195]\n"
"v_add_f32 v59,v59,v120\n"
"v_add_f32 v59,v59,v121\n"
MF "a[172:175],v[16:19],v[84:87],a[172:175]\n"
CV "v102,v120,v121\n"
"v_add_f32 v59,v59,v122\n"
MF "a[176:179],v[16:19],v[88:91],a[176:179]\n"
"s_waitcnt vmcnt(6) lgkmcnt(0)\n"
"s_barrier\n"
MF "a[196:199],v[20:23],v[88:91],a[196:199]\n"
"s_add_u32 s72,s72,6144\n"
"ds_read_b128 v[16:19],v0 offset:49152\n"
"ds_read_b128 v[20:23],v1 offset:49152\n"
MF "a[216:219],v[24:27],v[88:91],a[216:219]\n"
"s_cmp_eq_u32 s72,s17\n"
"s_cselect_b32 s72,s16,s72\n"
"s_add_u32 m0,s64,24576\n"
MF "a[236:239],v[28:31],v[88:91],a[236:239]\n"
LD "v7,s[32:35],s72 offen nt lds\n"
MF "a[232:235],v[28:31],v[84:87],a[232:235]\n"
"s_add_u32 m0,s64,25472\n"
"v_add_f32 v59,v59,v123\n"
CV "v103,v122,v123\n"
MF "a[212:215],v[24:27],v[84:87],a[212:215]\n"
LD "v7,s[32:35],s72 offen offset:128 nt lds\n"
MF "a[208:211],v[24:27],v[80:83],a[208:211]\n"
"s_add_u32 m0,s64,26368\n"
MF "a[228:231],v[28:31],v[80:83],a[228:231]\n"
LD "v7,s[32:35],s72 offen offset:256 nt lds\n"
MF "a[224:227],v[28:31],v[76:79],a[224:227]\n"
MF "a[204:207],v[24:27],v[76:79],a[204:207]\n"
MF "a[200:203],v[24:27],v[72:75],a[200:203]\n"
MF "a[220:223],v[28:31],v[72:75],a[220:223]\n"
"s_waitcnt lgkmcnt(0)\n"
MF "v[72:75],v[16:19],v[136:139],v[32:35]\n"
"s_add_u32 s72,s72,6144\n"
"ds_read_b128 v[24:27],v0 offset:50176\n"
"ds_read_b128 v[28:31],v1 offset:50176\n"
MF "v[72:75],v[20:23],v[156:159],v[72:75]\n"
"s_add_u32 m0,s64,30720\n"
"v_exp_f32 v124,v124\n"
MF "v[76:79],v[20:23],v[160:163],v[36:39]\n"
LD "v7,s[32:35],s72 offen nt lds\n"
MF "v[76:79],v[16:19],v[140:143],v[76:79]\n"
"v_exp_f32 v125,v125\n"
MF "v[80:83],v[16:19],v[144:147],v[40:43]\n"
"s_add_u32 m0,s64,31616\n"
"v_exp_f32 v126,v126\n"
MF "v[80:83],v[20:23],v[164:167],v[80:83]\n"
LD "v7,s[32:35],s72 offen offset:128 nt lds\n"
MF "v[84:87],v[20:23],v[168:171],v[44:47]\n"
"v_exp_f32 v127,v127\n"
MF "v[84:87],v[16:19],v[148:151],v[84:87]\n"
"s_add_u32 m0,s64,32512\n"
"v_add_f32 v60,v60,v124\n"
"v_add_f32 v60,v60,v125\n"
MF "v[88:91],v[16:19],v[152:155],v[48:51]\n"
LD "v7,s[32:35],s72 offen offset:256 nt lds\n"
MF "v[88:91],v[20:23],v[172:175],v[88:91]\n"
"s_waitcnt lgkmcnt(0)\n"
"ds_read_b128 v[16:19],v0 offset:51200\n"
"ds_read_b128 v[20:23],v1 offset:51200\n"
MF "v[88:91],v[24:27],v[192:195],v[88:91]\n"
CV "v106,v124,v125\n"
"v_add_f32 v60,v60,v126\n"
MF "v[88:91],v[28:31],v[212:215],v[88:91]\n"
"v_add_f32 v60,v60,v127\n"
CV "v107,v126,v127\n"
MF "v[84:87],v[28:31],v[208:211],v[84:87]\n"
"v_exp_f32 v128,v128\n"
MF "v[84:87],v[24:27],v[188:191],v[84:87]\n"
"v_exp_f32 v129,v129\n"
MF "v[80:83],v[24:27],v[184:187],v[80:83]\n"
"v_exp_f32 v130,v130\n"
MF "v[80:83],v[28:31],v[204:207],v[80:83]\n"
"v_exp_f32 v131,v131\n"
MF "v[76:79],v[28:31],v[200:203],v[76:79]\n"
"v_add_f32 v61,v61,v128\n"
"v_add_f32 v61,v61,v129\n"
MF "v[76:79],v[24:27],v[180:183],v[76:79]\n"
CV "v110,v128,v129\n"
"v_add_f32 v61,v61,v130\n"
MF "v[72:75],v[24:27],v[176:179],v[72:75]\n"
"v_add_f32 v61,v61,v131\n"
CV "v111,v130,v131\n"
MF "v[72:75],v[28:31],v[196:199],v[72:75]\n"
"s_waitcnt lgkmcnt(0)\n"
"ds_read_b128 v[24:27],v0 offset:55296\n"
"ds_read_b128 v[28:31],v1 offset:55296\n"
MF "v[72:75],v[16:19],v[216:219],v[72:75]\n"
MF "v[72:75],v[20:23],v[236:239],v[72:75]\n"
MF "v[76:79],v[20:23],v[240:243],v[76:79]\n"
MF "v[76:79],v[16:19],v[220:223],v[76:79]\n"
MF "v[80:83],v[16:19],v[224:227],v[80:83]\n"
MF "v[80:83],v[20:23],v[244:247],v[80:83]\n"
MF "v[84:87],v[20:23],v[248:251],v[84:87]\n"
MF "v[84:87],v[16:19],v[228:231],v[84:87]\n"
"v_max3_f32 v10,v57,v58,v59\n"
"v_max3_f32 v10,v10,v60,v61\n"
MF "v[88:91],v[16:19],v[232:235],v[88:91]\n"
"v_cmp_ngt_f32 vcc,s95,v10\n"
MF "v[88:91],v[20:23],v[252:255],v[88:91]\n"
"s_waitcnt lgkmcnt(0)\n"
"s_cbranch_vccnz .Lstub_13\n"
".Lback_13:\n"
MF "v[112:115],v[24:27],v[136:139],v[32:35]\n"
"s_add_u32 s74,s74,12288\n"
"ds_read_b128 v[16:19],v0 offset:56320\n"
"ds_read_b128 v[20:23],v1 offset:56320\n"
MF "v[112:115],v[28:31],v[156:159],v[112:115]\n"
"s_cmp_eq_u32 s74,s20\n"
"s_cselect_b32 s74,s70,s74\n"
MF "v[116:119],v[28:31],v[160:163],v[36:39]\n"
"v_exp_f32 v72,v72\n"
MF "v[116:119],v[24:27],v[140:143],v[116:119]\n"
"v_exp_f32 v73,v73\n"
MF "v[120:123],v[24:27],v[144:147],v[40:43]\n"
"v_exp_f32 v74,v74\n"
MF "v[120:123],v[28:31],v[164:167],v[120:123]\n"
"v_exp_f32 v75,v75\n"
MF "v[124:127],v[28:31],v[168:171],v[44:47]\n"
"v_add_f32 v52,v57,v72\n"
"v_add_f32 v52,v52,v73\n"
MF "v[124:127],v[24:27],v[148:151],v[124:127]\n"
CV "v72,v72,v73\n"
"v_add_f32 v52,v52,v74\n"
MF "v[128:131],v[24:27],v[152:155],v[48:51]\n"
"v_add_f32 v52,v52,v75\n"
CV "v73,v74,v75\n"
MF "v[128:131],v[28:31],v[172:175],v[128:131]\n"
"s_waitcnt lgkmcnt(0)\n"
"ds_read_b128 v[24:27],v0 offset:57344\n"
"ds_read_b128 v[28:31],v1 offset:57344\n"
MF "v[128:131],v[16:19],v[192:195],v[128:131]\n"
"v_exp_f32 v76,v76\n"
MF "v[128:131],v[20:23],v[212:215],v[128:131]\n"
"v_exp_f32 v77,v77\n"
MF "v[124:127],v[20:23],v[208:211],v[124:127]\n"
"v_exp_f32 v78,v78\n"
MF "v[124:127],v[16:19],v[188:191],v[124:127]\n"
"v_exp_f32 v79,v79\n"
MF "v[120:123],v[16:19],v[184:187],v[120:123]\n"
"v_add_f32 v53,v58,v76\n"
"v_add_f32 v53,v53,v77\n"
MF "v[120:123],v[20:23],v[204:207],v[120:123]\n"
CV "v76,v76,v77\n"
"v_add_f32 v53,v53,v78\n"
MF "v[116:119],v[20:23],v[200:203],v[116:119]\n"
"v_add_f32 v53,v53,v79\n"
CV "v77,v78,v79\n"
MF "v[116:119],v[16:19],v[180:183],v[116:119]\n"
"s_waitcnt vmcnt(6) lgkmcnt(0)\n"
"s_barrier\n"
MF "v[112:115],v[16:19],v[176:179],v[112:115]\n"
"v_exp_f32 v80,v80\n"
MF "v[112:115],v[20:23],v[196:199],v[112:115]\n"
"s_nop 0\n"
TR "v[16:17],v2\n"
TR "v[18:19],v2 offset:6144\n"
MF "v[112:115],v[24:27],v[216:219],v[112:115]\n"
"s_add_u32 s73,s73,12288\n"
TR "v[20:21],v3\n"
TR "v[22:23],v3 offset:6144\n"
MF "v[112:115],v[28:31],v[236:239],v[112:115]\n"
"s_cmp_eq_u32 s73,s19\n"
"s_cselect_b32 s73,s18,s73\n"
"s_add_u32 m0,s65,49152\n"
MF "v[116:119],v[28:31],v[240:243],v[116:119]\n"
LD "v8,s[36:39],s73 offen nt lds\n"
MF "v[116:119],v[24:27],v[220:223],v[116:119]\n"
"s_add_u32 m0,s65,50048\n"
"v_exp_f32 v81,v81\n"
MF "v[120:123],v[24:27],v[224:227],v[120:123]\n"
LD "v8,s[36:39],s73 offen offset:128 nt lds\n"
MF "v[120:123],v[28:31],v[244:247],v[120:123]\n"
"v_exp_f32 v82,v82\n"
MF "v[124:127],v[28:31],v[248:251],v[124:127]\n"
"s_add_u32 m0,s65,50944\n"
"v_exp_f32 v83,v83\n"
MF "v[124:127],v[24:27],v[228:231],v[124:127]\n"
LD "v8,s[36:39],s73 offen offset:256 nt lds\n"
MF "v[128:131],v[24:27],v[232:235],v[128:131]\n"
"v_add_f32 v54,v59,v80\n"
"v_add_f32 v54,v54,v81\n"
MF "v[128:131],v[28:31],v[252:255],v[128:131]\n"
"s_waitcnt lgkmcnt(0)\n"
TR "v[24:25],v4\n"
TR "v[26:27],v4 offset:6144\n"
MF "a[0:3],v[16:19],v[92:95],a[0:3]\n"
"s_add_u32 m0,s65,49152\n"
TR "v[28:29],v5\n"
TR "v[30:31],v5 offset:6144\n"
MF "a[20:23],v[20:23],v[92:95],a[20:23]\n"
LD "v8,s[36:39],s73 offen offset:3072 nt lds\n"
MF "a[24:27],v[20:23],v[96:99],a[24:27]\n"
CV "v80,v80,v81\n"
"v_add_f32 v54,v54,v82\n"
MF "a[4:7],v[16:19],v[96:99],a[4:7]\n"
"s_add_u32 m0,s65,50048\n"
"v_add_f32 v54,v54,v83\n"
CV "v81,v82,v83\n"
MF "a[8:11],v[16:19],v[100:103],a[8:11]\n"
LD "v8,s[36:39],s73 offen offset:3200 nt lds\n"
MF "a[28:31],v[20:23],v[100:103],a[28:31]\n"
"v_exp_f32 v84,v84\n"
MF "a[32:35],v[20:23],v[104:107],a[32:35]\n"
"s_add_u32 m0,s65,50944\n"
"v_exp_f32 v85,v85\n"
MF "a[12:15],v[16:19],v[104:107],a[12:15]\n"
LD "v8,s[36:39],s73 offen offset:3328 nt lds\n"
MF "a[16:19],v[16:19],v[108:111],a[16:19]\n"
"v_exp_f32 v86,v86\n"
MF "a[36:39],v[20:23],v[108:111],a[36:39]\n"
"s_waitcnt lgkmcnt(0)\n"
TR "v[16:17],v2 offset:1024\n"
TR "v[18:19],v2 offset:7168\n"
MF "a[56:59],v[24:27],v[108:111],a[56:59]\n"
"s_nop 0\n"
TR "v[20:21],v3 offset:1024\n"
TR "v[22:23],v3 offset:7168\n"
MF "a[76:79],v[28:31],v[108:111],a[76:79]\n"
"v_exp_f32 v87,v87\n"
MF "a[72:75],v[28:31],v[104:107],a[72:75]\n"
"v_add_f32 v55,v60,v84\n"
"v_add_f32 v55,v55,v85\n"
MF "a[52:55],v[24:27],v[104:107],a[52:55]\n"
CV "v84,v84,v85\n"
"v_add_f32 v55,v55,v86\n"
MF "a[48:51],v[24:27],v[100:103],a[48:51]\n"
"v_add_f32 v55,v55,v87\n"
CV "v85,v86,v87\n"
MF "a[68:71],v[28:31],v[100:103],a[68:71]\n"
"v_exp_f32 v88,v88\n"
MF "a[64:67],v[28:31],v[96:99],a[64:67]\n"
"v_exp_f32 v89,v89\n"
MF "a[44:47],v[24:27],v[96:99],a[44:47]\n"
"v_exp_f32 v90,v90\n"
MF "a[40:43],v[24:27],v[92:95],a[40:43]\n"
"v_exp_f32 v91,v91\n"
MF "a[60:63],v[28:31],v[92:95],a[60:63]\n"
"s_waitcnt lgkmcnt(0)\n"
TR "v[24:25],v4 offset:1024\n"
TR "v[26:27],v4 offset:7168\n"
MF "a[80:83],v[16:19],v[92:95],a[80:83]\n"
"s_nop 0\n"
TR "v[28:29],v5 offset:1024\n"
TR "v[30:31],v5 offset:7168\n"
MF "a[100:103],v[20:23],v[92:95],a[100:103]\n"
"v_add_f32 v56,v61,v88\n"
"v_add_f32 v56,v56,v89\n"
MF "a[104:107],v[20:23],v[96:99],a[104:107]\n"
CV "v88,v88,v89\n"
"v_add_f32 v56,v56,v90\n"
MF "a[84:87],v[16:19],v[96:99],a[84:87]\n"
"v_add_f32 v56,v56,v91\n"
CV "v89,v90,v91\n"
MF "a[88:91],v[16:19],v[100:103],a[88:91]\n"
"v_exp_f32 v112,v112\n"
MF "a[108:111],v[20:23],v[100:103],a[108:111]\n"
"v_exp_f32 v113,v113\n"
MF "a[112:115],v[20:23],v[104:107],a[112:115]\n"
"v_exp_f32 v114,v114\n"
MF "a[92:95],v[16:19],v[104:107],a[92:95]\n"
"v_exp_f32 v115,v115\n"
MF "a[96:99],v[16:19],v[108:111],a[96:99]\n"
"v_add_f32 v52,v52,v112\n"
"v_add_f32 v52,v52,v113\n"
MF "a[116:119],v[20:23],v[108:111],a[116:119]\n"
"s_waitcnt lgkmcnt(0)\n"
TR "v[16:17],v2 offset:2048\n"
TR "v[18:19],v2 offset:8192\n"
MF "a[136:139],v[24:27],v[108:111],a[136:139]\n"
"s_nop 0\n"
TR "v[20:21],v3 offset:2048\n"
TR "v[22:23],v3 offset:8192\n"
MF "a[156:159],v[28:31],v[108:111],a[156:159]\n"
CV "v74,v112,v113\n"
"v_add_f32 v52,v52,v114\n"
MF "a[152:155],v[28:31],v[104:107],a[152:155]\n"
"v_add_f32 v52,v52,v115\n"
CV "v75,v114,v115\n"
MF "a[132:135],v[24:27],v[104:107],a[132:135]\n"
"v_exp_f32 v116,v116\n"
MF "a[128:131],v[24:27],v[100:103],a[128:131]\n"
"v_exp_f32 v117,v117\n"
MF "a[148:151],v[28:31],v[100:103],a[148:151]\n"
"v_exp_f32 v118,v118\n"
MF "a[144:147],v[28:31],v[96:99],a[144:147]\n"
"v_exp_f32 v119,v119\n"
MF "a[124:127],v[24:27],v[96:99],a[124:127]\n"
"v_add_f32 v53,v53,v116\n"
"v_add_f32 v53,v53,v117\n"
MF "a[120:123],v[24:27],v[92:95],a[120:123]\n"
CV "v78,v116,v117\n"
"v_add_f32 v53,v53,v118\n"
MF "a[140:143],v[28:31],v[92:95],a[140:143]\n"
"s_waitcnt lgkmcnt(0)\n"
TR "v[24:25],v4 offset:2048\n"
TR "v[26:27],v4 offset:8192\n"
MF "a[160:163],v[16:19],v[92:95],a[160:163]\n"
"s_nop 0\n"
TR "v[28:29],v5 offset:2048\n"
TR "v[30:31],v5 offset:8192\n"
MF "a[180:183],v[20:23],v[92:95],a[180:183]\n"
"v_add_f32 v53,v53,v119\n"
CV "v79,v118,v119\n"
MF "a[184:187],v[20:23],v[96:99],a[184:187]\n"
"v_exp_f32 v120,v120\n"
MF "a[164:167],v[16:19],v[96:99],a[164:167]\n"
"v_exp_f32 v121,v121\n"
MF "a[168:171],v[16:19],v[100:103],a[168:171]\n"
"v_exp_f32 v122,v122\n"
MF "a[188:191],v[20:23],v[100:103],a[188:191]\n"
"v_exp_f32 v123,v123\n"
MF "a[192:195],v[20:23],v[104:107],a[192:195]\n"
"v_add_f32 v54,v54,v120\n"
"v_add_f32 v54,v54,v121\n"
MF "a[172:175],v[16:19],v[104:107],a[172:175]\n"
CV "v82,v120,v121\n"
"v_add_f32 v54,v54,v122\n"
MF "a[176:179],v[16:19],v[108:111],a[176:179]\n"
"s_waitcnt vmcnt(6) lgkmcnt(0)\n"
"s_barrier\n"
MF "a[196:199],v[20:23],v[108:111],a[196:199]\n"
"s_add_u32 s72,s72,6144\n"
"ds_read_b128 v[16:19],v0 offset:24576\n"
"ds_read_b128 v[20:23],v1 offset:24576\n"
MF "a[216:219],v[24:27],v[108:111],a[216:219]\n"
"s_cmp_eq_u32 s72,s17\n"
"s_cselect_b32 s72,s16,s72\n"
"s_add_u32 m0,s64,0\n"
MF "a[236:239],v[28:31],v[108:111],a[236:239]\n"
LD "v7,s[32:35],s72 offen nt lds\n"
MF "a[232:235],v[28:31],v[104:107],a[232:235]\n"
"s_add_u32 m0,s64,896\n"
"v_add_f32 v54,v54,v123\n"
CV "v83,v122,v123\n"
MF "a[212:215],v[24:27],v[104:107],a[212:215]\n"
LD "v7,s[32:35],s72 offen offset:128 nt lds\n"
MF "a[208:211],v[24:27],v[100:103],a[208:211]\n"
"s_add_u32 m0,s64,1792\n"
MF "a[228:231],v[28:31],v[100:103],a[228:231]\n"
LD "v7,s[32:35],s72 offen offset:256 nt lds\n"
MF "a[224:227],v[28:31],v[96:99],a[224:227]\n"
MF "a[204:207],v[24:27],v[96:99],a[204:207]\n"
MF "a[200:203],v[24:27],v[92:95],a[200:203]\n"
MF "a[220:223],v[28:31],v[92:95],a[220:223]\n"
"s_waitcnt lgkmcnt(0)\n"
MF "v[92:95],v[16:19],v[136:139],v[32:35]\n"
"s_add_u32 s72,s72,6144\n"
"ds_read_b128 v[24:27],v0 offset:25600\n"
"ds_read_b128 v[28:31],v1 offset:25600\n"
MF "v[92:95],v[20:23],v[156:159],v[92:95]\n"
"s_add_u32 m0,s64,6144\n"
"v_exp_f32 v124,v124\n"
MF "v[96:99],v[20:23],v[160:163],v[36:39]\n"
LD "v7,s[32:35],s72 offen nt lds\n"
MF "v[96:99],v[16:19],v[140:143],v[96:99]\n"
"v_exp_f32 v125,v125\n"
MF "v[100:103],v[16:19],v[144:147],v[40:43]\n"
"s_add_u32 m0,s64,7040\n"
"v_exp_f32 v126,v126\n"
MF "v[100:103],v[20:23],v[164:167],v[100:103]\n"
LD "v7,s[32:35],s72 offen offset:128 nt lds\n"
MF "v[104:107],v[20:23],v[168:171],v[44:47]\n"
"v_exp_f32 v127,v127\n"
MF "v[104:107],v[16:19],v[148:151],v[104:107]\n"
"s_add_u32 m0,s64,7936\n"
"v_add_f32 v55,v55,v124\n"
"v_add_f32 v55,v55,v125\n"
MF "v[108:111],v[16:19],v[152:155],v[48:51]\n"
LD "v7,s[32:35],s72 offen offset:256 nt lds\n"
MF "v[108:111],v[20:23],v[172:175],v[108:111]\n"
"s_waitcnt lgkmcnt(0)\n"
"ds_read_b128 v[16:19],v0 offset:26624\n"
"ds_read_b128 v[20:23],v1 offset:26624\n"
MF "v[108:111],v[24:27],v[192:195],v[108:111]\n"
CV "v86,v124,v125\n"
"v_add_f32 v55,v55,v126\n"
MF "v[108:111],v[28:31],v[212:215],v[108:111]\n"
"v_add_f32 v55,v55,v127\n"
CV "v87,v126,v127\n"
MF "v[104:107],v[28:31],v[208:211],v[104:107]\n"
"v_exp_f32 v128,v128\n"
MF "v[104:107],v[24:27],v[188:191],v[104:107]\n"
"v_exp_f32 v129,v129\n"
MF "v[100:103],v[24:27],v[184:187],v[100:103]\n"
"v_exp_f32 v130,v130\n"
MF "v[100:103],v[28:31],v[204:207],v[100:103]\n"
"v_exp_f32 v131,v131\n"
MF "v[96:99],v[28:31],v[200:203],v[96:99]\n"
"v_add_f32 v56,v56,v128\n"
"v_add_f32 v56,v56,v129\n"
MF "v[96:99],v[24:27],v[180:183],v[96:99]\n"
CV "v90,v128,v129\n"
"v_add_f32 v56,v56,v130\n"
MF "v[92:95],v[24:27],v[176:179],v[92:95]\n"
"v_add_f32 v56,v56,v131\n"
CV "v91,v130,v131\n"
MF "v[92:95],v[28:31],v[196:199],v[92:95]\n"
"s_waitcnt lgkmcnt(0)\n"
"ds_read_b128 v[24:27],v0 offset:30720\n"
"ds_read_b128 v[28:31],v1 offset:30720\n"
MF "v[92:95],v[16:19],v[216:219],v[92:95]\n"
MF "v[92:95],v[20:23],v[236:239],v[92:95]\n"
MF "v[96:99],v[20:23],v[240:243],v[96:99]\n"
MF "v[96:99],v[16:19],v[220:223],v[96:99]\n"
MF "v[100:103],v[16:19],v[224:227],v[100:103]\n"
MF "v[100:103],v[20:23],v[244:247],v[100:103]\n"
MF "v[104:107],v[20:23],v[248:251],v[104:107]\n"
MF "v[104:107],v[16:19],v[228:231],v[104:107]\n"
"v_max3_f32 v10,v52,v53,v54\n"
"v_max3_f32 v10,v10,v55,v56\n"
MF "v[108:111],v[16:19],v[232:235],v[108:111]\n"
"v_cmp_ngt_f32 vcc,s77,v10\n"
MF "v[108:111],v[20:23],v[252:255],v[108:111]\n"
"s_waitcnt lgkmcnt(0)\n"
"s_cbranch_vccnz .Lstub_14\n"
".Lback_14:\n"
MF "v[112:115],v[24:27],v[136:139],v[32:35]\n"
"s_add_u32 s74,s74,12288\n"
"ds_read_b128 v[16:19],v0 offset:31744\n"
"ds_read_b128 v[20:23],v1 offset:31744\n"
MF "v[112:115],v[28:31],v[156:159],v[112:115]\n"
"s_cmp_eq_u32 s74,s20\n"
"s_cselect_b32 s74,s70,s74\n"
MF "v[116:119],v[28:31],v[160:163],v[36:39]\n"
"v_exp_f32 v92,v92\n"
MF "v[116:119],v[24:27],v[140:143],v[116:119]\n"
"v_exp_f32 v93,v93\n"
MF "v[120:123],v[24:27],v[144:147],v[40:43]\n"
"v_exp_f32 v94,v94\n"
MF "v[120:123],v[28:31],v[164:167],v[120:123]\n"
"v_exp_f32 v95,v95\n"
MF "v[124:127],v[28:31],v[168:171],v[44:47]\n"
"v_add_f32 v57,v52,v92\n"
"v_add_f32 v57,v57,v93\n"
MF "v[124:127],v[24:27],v[148:151],v[124:127]\n"
CV "v92,v92,v93\n"
"v_add_f32 v57,v57,v94\n"
MF "v[128:131],v[24:27],v[152:155],v[48:51]\n"
"v_add_f32 v57,v57,v95\n"
CV "v93,v94,v95\n"
MF "v[128:131],v[28:31],v[172:175],v[128:131]\n"
"s_waitcnt lgkmcnt(0)\n"
"ds_read_b128 v[24:27],v0 offset:32768\n"
"ds_read_b128 v[28:31],v1 offset:32768\n"
MF "v[128:131],v[16:19],v[192:195],v[128:131]\n"
"v_exp_f32 v96,v96\n"
MF "v[128:131],v[20:23],v[212:215],v[128:131]\n"
"v_exp_f32 v97,v97\n"
MF "v[124:127],v[20:23],v[208:211],v[124:127]\n"
"v_exp_f32 v98,v98\n"
MF "v[124:127],v[16:19],v[188:191],v[124:127]\n"
"v_exp_f32 v99,v99\n"
MF "v[120:123],v[16:19],v[184:187],v[120:123]\n"
"v_add_f32 v58,v53,v96\n"
"v_add_f32 v58,v58,v97\n"
MF "v[120:123],v[20:23],v[204:207],v[120:123]\n"
CV "v96,v96,v97\n"
"v_add_f32 v58,v58,v98\n"
MF "v[116:119],v[20:23],v[200:203],v[116:119]\n"
"v_add_f32 v58,v58,v99\n"
CV "v97,v98,v99\n"
MF "v[116:119],v[16:19],v[180:183],v[116:119]\n"
"s_waitcnt vmcnt(6) lgkmcnt(0)\n"
"s_barrier\n"
MF "v[112:115],v[16:19],v[176:179],v[112:115]\n"
"v_exp_f32 v100,v100\n"
MF "v[112:115],v[20:23],v[196:199],v[112:115]\n"
"s_nop 0\n"
TR "v[16:17],v2 offset:49152\n"
TR "v[18:19],v2 offset:55296\n"
MF "v[112:115],v[24:27],v[216:219],v[112:115]\n"
"s_add_u32 s73,s73,12288\n"
TR "v[20:21],v3 offset:49152\n"
TR "v[22:23],v3 offset:55296\n"
MF "v[112:115],v[28:31],v[236:239],v[112:115]\n"
"s_cmp_eq_u32 s73,s19\n"
"s_cselect_b32 s73,s18,s73\n"
"s_add_u32 m0,s65,24576\n"
MF "v[116:119],v[28:31],v[240:243],v[116:119]\n"
LD "v8,s[36:39],s73 offen nt lds\n"
MF "v[116:119],v[24:27],v[220:223],v[116:119]\n"
"s_add_u32 m0,s65,25472\n"
"v_exp_f32 v101,v101\n"
MF "v[120:123],v[24:27],v[224:227],v[120:123]\n"
LD "v8,s[36:39],s73 offen offset:128 nt lds\n"
MF "v[120:123],v[28:31],v[244:247],v[120:123]\n"
"v_exp_f32 v102,v102\n"
MF "v[124:127],v[28:31],v[248:251],v[124:127]\n"
"s_add_u32 m0,s65,26368\n"
"v_exp_f32 v103,v103\n"
MF "v[124:127],v[24:27],v[228:231],v[124:127]\n"
LD "v8,s[36:39],s73 offen offset:256 nt lds\n"
MF "v[128:131],v[24:27],v[232:235],v[128:131]\n"
"v_add_f32 v59,v54,v100\n"
"v_add_f32 v59,v59,v101\n"
MF "v[128:131],v[28:31],v[252:255],v[128:131]\n"
"s_waitcnt lgkmcnt(0)\n"
TR "v[24:25],v4 offset:49152\n"
TR "v[26:27],v4 offset:55296\n"
MF "a[0:3],v[16:19],v[72:75],a[0:3]\n"
"s_add_u32 m0,s65,24576\n"
TR "v[28:29],v5 offset:49152\n"
TR "v[30:31],v5 offset:55296\n"
MF "a[20:23],v[20:23],v[72:75],a[20:23]\n"
LD "v8,s[36:39],s73 offen offset:3072 nt lds\n"
MF "a[24:27],v[20:23],v[76:79],a[24:27]\n"
CV "v100,v100,v101\n"
"v_add_f32 v59,v59,v102\n"
MF "a[4:7],v[16:19],v[76:79],a[4:7]\n"
"s_add_u32 m0,s65,25472\n"
"v_add_f32 v59,v59,v103\n"
CV "v101,v102,v103\n"
MF "a[8:11],v[16:19],v[80:83],a[8:11]\n"
LD "v8,s[36:39],s73 offen offset:3200 nt lds\n"
MF "a[28:31],v[20:23],v[80:83],a[28:31]\n"
"v_exp_f32 v104,v104\n"
MF "a[32:35],v[20:23],v[84:87],a[32:35]\n"
"s_add_u32 m0,s65,26368\n"
"v_exp_f32 v105,v105\n"
MF "a[12:15],v[16:19],v[84:87],a[12:15]\n"
LD "v8,s[36:39],s73 offen offset:3328 nt lds\n"
MF "a[16:19],v[16:19],v[88:91],a[16:19]\n"
"v_exp_f32 v106,v106\n"
MF "a[36:39],v[20:23],v[88:91],a[36:39]\n"
"s_waitcnt lgkmcnt(0)\n"
TR "v[16:17],v2 offset:50176\n"
TR "v[18:19],v2 offset:56320\n"
MF "a[56:59],v[24:27],v[88:91],a[56:59]\n"
"s_nop 0\n"
TR "v[20:21],v3 offset:50176\n"
TR "v[22:23],v3 offset:56320\n"
MF "a[76:79],v[28:31],v[88:91],a[76:79]\n"
"v_exp_f32 v107,v107\n"
MF "a[72:75],v[28:31],v[84:87],a[72:75]\n"
"v_add_f32 v60,v55,v104\n"
"v_add_f32 v60,v60,v105\n"
MF "a[52:55],v[24:27],v[84:87],a[52:55]\n"
CV "v104,v104,v105\n"
"v_add_f32 v60,v60,v106\n"
MF "a[48:51],v[24:27],v[80:83],a[48:51]\n"
"v_add_f32 v60,v60,v107\n"
CV "v105,v106,v107\n"
MF "a[68:71],v[28:31],v[80:83],a[68:71]\n"
"v_exp_f32 v108,v108\n"
MF "a[64:67],v[28:31],v[76:79],a[64:67]\n"
"v_exp_f32 v109,v109\n"
MF "a[44:47],v[24:27],v[76:79],a[44:47]\n"
"v_exp_f32 v110,v110\n"
MF "a[40:43],v[24:27],v[72:75],a[40:43]\n"
"v_exp_f32 v111,v111\n"
MF "a[60:63],v[28:31],v[72:75],a[60:63]\n"
"s_waitcnt lgkmcnt(0)\n"
TR "v[24:25],v4 offset:50176\n"
TR "v[26:27],v4 offset:56320\n"
MF "a[80:83],v[16:19],v[72:75],a[80:83]\n"
"s_nop 0\n"
TR "v[28:29],v5 offset:50176\n"
TR "v[30:31],v5 offset:56320\n"
MF "a[100:103],v[20:23],v[72:75],a[100:103]\n"
"v_add_f32 v61,v56,v108\n"
"v_add_f32 v61,v61,v109\n"
MF "a[104:107],v[20:23],v[76:79],a[104:107]\n"
CV "v108,v108,v109\n"
"v_add_f32 v61,v61,v110\n"
MF "a[84:87],v[16:19],v[76:79],a[84:87]\n"
"v_add_f32 v61,v61,v111\n"
CV "v109,v110,v111\n"
MF "a[88:91],v[16:19],v[80:83],a[88:91]\n"
"v_exp_f32 v112,v112\n"
MF "a[108:111],v[20:23],v[80:83],a[108:111]\n"
"v_exp_f32 v113,v113\n"
MF "a[112:115],v[20:23],v[84:87],a[112:115]\n"
"v_exp_f32 v114,v114\n"
MF "a[92:95],v[16:19],v[84:87],a[92:95]\n"
"v_exp_f32 v115,v115\n"
MF "a[96:99],v[16:19],v[88:91],a[96:99]\n"
"v_add_f32 v57,v57,v112\n"
"v_add_f32 v57,v57,v113\n"
MF "a[116:119],v[20:23],v[88:91],a[116:119]\n"
"s_waitcnt lgkmcnt(0)\n"
TR "v[16:17],v2 offset:51200\n"
TR "v[18:19],v2 offset:57344\n"
MF "a[136:139],v[24:27],v[88:91],a[136:139]\n"
"s_nop 0\n"
TR "v[20:21],v3 offset:51200\n"
TR "v[22:23],v3 offset:57344\n"
MF "a[156:159],v[28:31],v[88:91],a[156:159]\n"
CV "v94,v112,v113\n"
"v_add_f32 v57,v57,v114\n"
MF "a[152:155],v[28:31],v[84:87],a[152:155]\n"
"v_add_f32 v57,v57,v115\n"
CV "v95,v114,v115\n"
MF "a[132:135],v[24:27],v[84:87],a[132:135]\n"
"v_exp_f32 v116,v116\n"
MF "a[128:131],v[24:27],v[80:83],a[128:131]\n"
"v_exp_f32 v117,v117\n"
MF "a[148:151],v[28:31],v[80:83],a[148:151]\n"
"v_exp_f32 v118,v118\n"
MF "a[144:147],v[28:31],v[76:79],a[144:147]\n"
"v_exp_f32 v119,v119\n"
MF "a[124:127],v[24:27],v[76:79],a[124:127]\n"
"v_add_f32 v58,v58,v116\n"
"v_add_f32 v58,v58,v117\n"
MF "a[120:123],v[24:27],v[72:75],a[120:123]\n"
CV "v98,v116,v117\n"
"v_add_f32 v58,v58,v118\n"
MF "a[140:143],v[28:31],v[72:75],a[140:143]\n"
"s_waitcnt lgkmcnt(0)\n"
TR "v[24:25],v4 offset:51200\n"
TR "v[26:27],v4 offset:57344\n"
MF "a[160:163],v[16:19],v[72:75],a[160:163]\n"
"s_nop 0\n"
TR "v[28:29],v5 offset:51200\n"
TR "v[30:31],v5 offset:57344\n"
MF "a[180:183],v[20:23],v[72:75],a[180:183]\n"
"v_add_f32 v58,v58,v119\n"
CV "v99,v118,v119\n"
MF "a[184:187],v[20:23],v[76:79],a[184:187]\n"
"v_exp_f32 v120,v120\n"
MF "a[164:167],v[16:19],v[76:79],a[164:167]\n"
"v_exp_f32 v121,v121\n"
MF "a[168:171],v[16:19],v[80:83],a[168:171]\n"
"v_exp_f32 v122,v122\n"
MF "a[188:191],v[20:23],v[80:83],a[188:191]\n"
"v_exp_f32 v123,v123\n"
MF "a[192:195],v[20:23],v[84:87],a[192:195]\n"
"v_add_f32 v59,v59,v120\n"
"v_add_f32 v59,v59,v121\n"
MF "a[172:175],v[16:19],v[84:87],a[172:175]\n"
CV "v102,v120,v121\n"
"v_add_f32 v59,v59,v122\n"
MF "a[176:179],v[16:19],v[88:91],a[176:179]\n"
"s_waitcnt vmcnt(6) lgkmcnt(0)\n"
"s_barrier\n"
MF "a[196:199],v[20:23],v[88:91],a[196:199]\n"
"s_add_u32 s72,s72,6144\n"
"ds_read_b128 v[16:19],v0\n"
"ds_read_b128 v[20:23],v1\n"
MF "a[216:219],v[24:27],v[88:91],a[216:219]\n"
"s_cmp_eq_u32 s72,s17\n"
"s_cselect_b32 s72,s16,s72\n"
"s_add_u32 m0,s64,49152\n"
MF "a[236:239],v[28:31],v[88:91],a[236:239]\n"
LD "v7,s[32:35],s72 offen nt lds\n"
MF "a[232:235],v[28:31],v[84:87],a[232:235]\n"
"s_add_u32 m0,s64,50048\n"
"v_add_f32 v59,v59,v123\n"
CV "v103,v122,v123\n"
MF "a[212:215],v[24:27],v[84:87],a[212:215]\n"
LD "v7,s[32:35],s72 offen offset:128 nt lds\n"
MF "a[208:211],v[24:27],v[80:83],a[208:211]\n"
"s_add_u32 m0,s64,50944\n"
MF "a[228:231],v[28:31],v[80:83],a[228:231]\n"
LD "v7,s[32:35],s72 offen offset:256 nt lds\n"
MF "a[224:227],v[28:31],v[76:79],a[224:227]\n"
MF "a[204:207],v[24:27],v[76:79],a[204:207]\n"
MF "a[200:203],v[24:27],v[72:75],a[200:203]\n"
MF "a[220:223],v[28:31],v[72:75],a[220:223]\n"
"s_waitcnt lgkmcnt(0)\n"
".Lmid:\n"
MF "v[72:75],v[16:19],v[136:139],v[32:35]\n"
"s_add_u32 s72,s72,6144\n"
"ds_read_b128 v[24:27],v0 offset:1024\n"
"ds_read_b128 v[28:31],v1 offset:1024\n"
MF "v[72:75],v[20:23],v[156:159],v[72:75]\n"
"s_add_u32 m0,s64,55296\n"
"v_exp_f32 v124,v124\n"
MF "v[76:79],v[20:23],v[160:163],v[36:39]\n"
LD "v7,s[32:35],s72 offen nt lds\n"
MF "v[76:79],v[16:19],v[140:143],v[76:79]\n"
"v_exp_f32 v125,v125\n"
MF "v[80:83],v[16:19],v[144:147],v[40:43]\n"
"s_add_u32 m0,s64,56192\n"
"v_exp_f32 v126,v126\n"
MF "v[80:83],v[20:23],v[164:167],v[80:83]\n"
LD "v7,s[32:35],s72 offen offset:128 nt lds\n"
MF "v[84:87],v[20:23],v[168:171],v[44:47]\n"
"v_exp_f32 v127,v127\n"
MF "v[84:87],v[16:19],v[148:151],v[84:87]\n"
"s_add_u32 m0,s64,57088\n"
"v_add_f32 v60,v60,v124\n"
"v_add_f32 v60,v60,v125\n"
MF "v[88:91],v[16:19],v[152:155],v[48:51]\n"
LD "v7,s[32:35],s72 offen offset:256 nt lds\n"
MF "v[88:91],v[20:23],v[172:175],v[88:91]\n"
"s_waitcnt lgkmcnt(0)\n"
"ds_read_b128 v[16:19],v0 offset:2048\n"
"ds_read_b128 v[20:23],v1 offset:2048\n"
MF "v[88:91],v[24:27],v[192:195],v[88:91]\n"
CV "v106,v124,v125\n"
"v_add_f32 v60,v60,v126\n"
MF "v[88:91],v[28:31],v[212:215],v[88:91]\n"
"v_add_f32 v60,v60,v127\n"
CV "v107,v126,v127\n"
MF "v[84:87],v[28:31],v[208:211],v[84:87]\n"
"v_exp_f32 v128,v128\n"
MF "v[84:87],v[24:27],v[188:191],v[84:87]\n"
"v_exp_f32 v129,v129\n"
MF "v[80:83],v[24:27],v[184:187],v[80:83]\n"
"v_exp_f32 v130,v130\n"
MF "v[80:83],v[28:31],v[204:207],v[80:83]\n"
"v_exp_f32 v131,v131\n"
MF "v[76:79],v[28:31],v[200:203],v[76:79]\n"
"v_add_f32 v61,v61,v128\n"
"v_add_f32 v61,v61,v129\n"
MF "v[76:79],v[24:27],v[180:183],v[76:79]\n"
CV "v110,v128,v129\n"
"v_add_f32 v61,v61,v130\n"
MF "v[72:75],v[24:27],v[176:179],v[72:75]\n"
"v_add_f32 v61,v61,v131\n"
CV "v111,v130,v131\n"
MF "v[72:75],v[28:31],v[196:199],v[72:75]\n"
"s_waitcnt lgkmcnt(0)\n"
"ds_read_b128 v[24:27],v0 offset:6144\n"
"ds_read_b128 v[28:31],v1 offset:6144\n"
MF "v[72:75],v[16:19],v[216:219],v[72:75]\n"
MF "v[72:75],v[20:23],v[236:239],v[72:75]\n"
MF "v[76:79],v[20:23],v[240:243],v[76:79]\n"
MF "v[76:79],v[16:19],v[220:223],v[76:79]\n"
MF "v[80:83],v[16:19],v[224:227],v[80:83]\n"
MF "v[80:83],v[20:23],v[244:247],v[80:83]\n"
MF "v[84:87],v[20:23],v[248:251],v[84:87]\n"
MF "v[84:87],v[16:19],v[228:231],v[84:87]\n"
"v_max3_f32 v10,v57,v58,v59\n"
"v_max3_f32 v10,v10,v60,v61\n"
MF "v[88:91],v[16:19],v[232:235],v[88:91]\n"
"v_cmp_ngt_f32 vcc,s95,v10\n"
MF "v[88:91],v[20:23],v[252:255],v[88:91]\n"
"s_waitcnt lgkmcnt(0)\n"
"s_cbranch_vccnz .Lstub_15\n"
".Lback_15:\n"
MF "v[112:115],v[24:27],v[136:139],v[32:35]\n"
"s_add_u32 s74,s74,12288\n"
"ds_read_b128 v[16:19],v0 offset:7168\n"
"ds_read_b128 v[20:23],v1 offset:7168\n"
MF "v[112:115],v[28:31],v[156:159],v[112:115]\n"
"s_cmp_eq_u32 s74,s20\n"
"s_cselect_b32 s74,s70,s74\n"
MF "v[116:119],v[28:31],v[160:163],v[36:39]\n"
"v_exp_f32 v72,v72\n"
MF "v[116:119],v[24:27],v[140:143],v[116:119]\n"
"v_exp_f32 v73,v73\n"
MF "v[120:123],v[24:27],v[144:147],v[40:43]\n"
"v_exp_f32 v74,v74\n"
MF "v[120:123],v[28:31],v[164:167],v[120:123]\n"
"v_exp_f32 v75,v75\n"
MF "v[124:127],v[28:31],v[168:171],v[44:47]\n"
"v_add_f32 v52,v57,v72\n"
"v_add_f32 v52,v52,v73\n"
MF "v[124:127],v[24:27],v[148:151],v[124:127]\n"
CV "v72,v72,v73\n"
"v_add_f32 v52,v52,v74\n"
MF "v[128:131],v[24:27],v[152:155],v[48:51]\n"
"v_add_f32 v52,v52,v75\n"
CV "v73,v74,v75\n"
MF "v[128:131],v[28:31],v[172:175],v[128:131]\n"
"s_waitcnt lgkmcnt(0)\n"
"ds_read_b128 v[24:27],v0 offset:8192\n"
"ds_read_b128 v[28:31],v1 offset:8192\n"
MF "v[128:131],v[16:19],v[192:195],v[128:131]\n"
"v_exp_f32 v76,v76\n"
MF "v[128:131],v[20:23],v[212:215],v[128:131]\n"
"v_exp_f32 v77,v77\n"
MF "v[124:127],v[20:23],v[208:211],v[124:127]\n"
"v_exp_f32 v78,v78\n"
MF "v[124:127],v[16:19],v[188:191],v[124:127]\n"
"v_exp_f32 v79,v79\n"
MF "v[120:123],v[16:19],v[184:187],v[120:123]\n"
"v_add_f32 v53,v58,v76\n"
"v_add_f32 v53,v53,v77\n"
MF "v[120:123],v[20:23],v[204:207],v[120:123]\n"
CV "v76,v76,v77\n"
"v_add_f32 v53,v53,v78\n"
MF "v[116:119],v[20:23],v[200:203],v[116:119]\n"
"v_add_f32 v53,v53,v79\n"
CV "v77,v78,v79\n"
MF "v[116:119],v[16:19],v[180:183],v[116:119]\n"
"s_waitcnt vmcnt(6) lgkmcnt(0)\n"
"s_barrier\n"
MF "v[112:115],v[16:19],v[176:179],v[112:115]\n"
"v_exp_f32 v80,v80\n"
MF "v[112:115],v[20:23],v[196:199],v[112:115]\n"
"s_nop 0\n"
TR "v[16:17],v2 offset:24576\n"
TR "v[18:19],v2 offset:30720\n"
MF "v[112:115],v[24:27],v[216:219],v[112:115]\n"
"s_add_u32 s73,s73,12288\n"
TR "v[20:21],v3 offset:24576\n"
TR "v[22:23],v3 offset:30720\n"
MF "v[112:115],v[28:31],v[236:239],v[112:115]\n"
"s_cmp_eq_u32 s73,s19\n"
"s_cselect_b32 s73,s18,s73\n"
"s_add_u32 m0,s65,0\n"
MF "v[116:119],v[28:31],v[240:243],v[116:119]\n"
LD "v8,s[36:39],s73 offen nt lds\n"
MF "v[116:119],v[24:27],v[220:223],v[116:119]\n"
"s_add_u32 m0,s65,896\n"
"v_exp_f32 v81,v81\n"
MF "v[120:123],v[24:27],v[224:227],v[120:123]\n"
LD "v8,s[36:39],s73 offen offset:128 nt lds\n"
MF "v[120:123],v[28:31],v[244:247],v[120:123]\n"
"v_exp_f32 v82,v82\n"
MF "v[124:127],v[28:31],v[248:251],v[124:127]\n"
"s_add_u32 m0,s65,1792\n"
"v_exp_f32 v83,v83\n"
MF "v[124:127],v[24:27],v[228:231],v[124:127]\n"
LD "v8,s[36:39],s73 offen offset:256 nt lds\n"
MF "v[128:131],v[24:27],v[232:235],v[128:131]\n"
"v_add_f32 v54,v59,v80\n"
"v_add_f32 v54,v54,v81\n"
MF "v[128:131],v[28:31],v[252:255],v[128:131]\n"
"s_waitcnt lgkmcnt(0)\n"
TR "v[24:25],v4 offset:24576\n"
TR "v[26:27],v4 offset:30720\n"
MF "a[0:3],v[16:19],v[92:95],a[0:3]\n"
"s_add_u32 m0,s65,0\n"
TR "v[28:29],v5 offset:24576\n"
TR "v[30:31],v5 offset:30720\n"
MF "a[20:23],v[20:23],v[92:95],a[20:23]\n"
LD "v8,s[36:39],s73 offen offset:3072 nt lds\n"
MF "a[24:27],v[20:23],v[96:99],a[24:27]\n"
CV "v80,v80,v81\n"
"v_add_f32 v54,v54,v82\n"
MF "a[4:7],v[16:19],v[96:99],a[4:7]\n"
"s_add_u32 m0,s65,896\n"
"v_add_f32 v54,v54,v83\n"
CV "v81,v82,v83\n"
MF "a[8:11],v[16:19],v[100:103],a[8:11]\n"
LD "v8,s[36:39],s73 offen offset:3200 nt lds\n"
MF "a[28:31],v[20:23],v[100:103],a[28:31]\n"
"v_exp_f32 v84,v84\n"
MF "a[32:35],v[20:23],v[104:107],a[32:35]\n"
"s_add_u32 m0,s65,1792\n"
"v_exp_f32 v85,v85\n"
MF "a[12:15],v[16:19],v[104:107],a[12:15]\n"
LD "v8,s[36:39],s73 offen offset:3328 nt lds\n"
MF "a[16:19],v[16:19],v[108:111],a[16:19]\n"
"v_exp_f32 v86,v86\n"
MF "a[36:39],v[20:23],v[108:111],a[36:39]\n"
"s_waitcnt lgkmcnt(0)\n"
TR "v[16:17],v2 offset:25600\n"
TR "v[18:19],v2 offset:31744\n"
MF "a[56:59],v[24:27],v[108:111],a[56:59]\n"
"s_nop 0\n"
TR "v[20:21],v3 offset:25600\n"
TR "v[22:23],v3 offset:31744\n"
MF "a[76:79],v[28:31],v[108:111],a[76:79]\n"
"v_exp_f32 v87,v87\n"
MF "a[72:75],v[28:31],v[104:107],a[72:75]\n"
"v_add_f32 v55,v60,v84\n"
"v_add_f32 v55,v55,v85\n"
MF "a[52:55],v[24:27],v[104:107],a[52:55]\n"
CV "v84,v84,v85\n"
"v_add_f32 v55,v55,v86\n"
MF "a[48:51],v[24:27],v[100:103],a[48:51]\n"
"v_add_f32 v55,v55,v87\n"
CV "v85,v86,v87\n"
MF "a[68:71],v[28:31],v[100:103],a[68:71]\n"
"v_exp_f32 v88,v88\n"
MF "a[64:67],v[28:31],v[96:99],a[64:67]\n"
"v_exp_f32 v89,v89\n"
MF "a[44:47],v[24:27],v[96:99],a[44:47]\n"
"v_exp_f32 v90,v90\n"
MF "a[40:43],v[24:27],v[92:95],a[40:43]\n"
"v_exp_f32 v91,v91\n"
MF "a[60:63],v[28:31],v[92:95],a[60:63]\n"
"s_waitcnt lgkmcnt(0)\n"
TR "v[24:25],v4 offset:25600\n"
TR "v[26:27],v4 offset:31744\n"
MF "a[80:83],v[16:19],v[92:95],a[80:83]\n"
"s_nop 0\n"
TR "v[28:29],v5 offset:25600\n"
TR "v[30:31],v5 offset:31744\n"
MF "a[100:103],v[20:23],v[92:95],a[100:103]\n"
"v_add_f32 v56,v61,v88\n"
"v_add_f32 v56,v56,v89\n"
MF "a[104:107],v[20:23],v[96:99],a[104:107]\n"
CV "v88,v88,v89\n"
"v_add_f32 v56,v56,v90\n"
MF "a[84:87],v[16:19],v[96:99],a[84:87]\n"
"v_add_f32 v56,v56,v91\n"
CV "v89,v90,v91\n"
MF "a[88:91],v[16:19],v[100:103],a[88:91]\n"
"v_exp_f32 v112,v112\n"
MF "a[108:111],v[20:23],v[100:103],a[108:111]\n"
"v_exp_f32 v113,v113\n"
MF "a[112:115],v[20:23],v[104:107],a[112:115]\n"
"v_exp_f32 v114,v114\n"
MF "a[92:95],v[16:19],v[104:107],a[92:95]\n"
"v_exp_f32 v115,v115\n"
MF "a[96:99],v[16:19],v[108:111],a[96:99]\n"
"v_add_f32 v52,v52,v112\n"
"v_add_f32 v52,v52,v113\n"
MF "a[116:119],v[20:23],v[108:111],a[116:119]\n"
"s_waitcnt lgkmcnt(0)\n"
TR "v[16:17],v2 offset:26624\n"
TR "v[18:19],v2 offset:32768\n"
MF "a[136:139],v[24:27],v[108:111],a[136:139]\n"
"s_nop 0\n"
TR "v[20:21],v3 offset:26624\n"
TR "v[22:23],v3 offset:32768\n"
MF "a[156:159],v[28:31],v[108:111],a[156:159]\n"
CV "v74,v112,v113\n"
"v_add_f32 v52,v52,v114\n"
MF "a[152:155],v[28:31],v[104:107],a[152:155]\n"
"v_add_f32 v52,v52,v115\n"
CV "v75,v114,v115\n"
MF "a[132:135],v[24:27],v[104:107],a[132:135]\n"
"v_exp_f32 v116,v116\n"
MF "a[128:131],v[24:27],v[100:103],a[128:131]\n"
"v_exp_f32 v117,v117\n"
MF "a[148:151],v[28:31],v[100:103],a[148:151]\n"
"v_exp_f32 v118,v118\n"
MF "a[144:147],v[28:31],v[96:99],a[144:147]\n"
"v_exp_f32 v119,v119\n"
MF "a[124:127],v[24:27],v[96:99],a[124:127]\n"
"v_add_f32 v53,v53,v116\n"
"v_add_f32 v53,v53,v117\n"
MF "a[120:123],v[24:27],v[92:95],a[120:123]\n"
CV "v78,v116,v117\n"
"v_add_f32 v53,v53,v118\n"
MF "a[140:143],v[28:31],v[92:95],a[140:143]\n"
"s_waitcnt lgkmcnt(0)\n"
TR "v[24:25],v4 offset:26624\n"
TR "v[26:27],v4 offset:32768\n"
MF "a[160:163],v[16:19],v[92:95],a[160:163]\n"
"s_nop 0\n"
TR "v[28:29],v5 offset:26624\n"
TR "v[30:31],v5 offset:32768\n"
MF "a[180:183],v[20:23],v[92:95],a[180:183]\n"
"v_add_f32 v53,v53,v119\n"
CV "v79,v118,v119\n"
MF "a[184:187],v[20:23],v[96:99],a[184:187]\n"
"v_exp_f32 v120,v120\n"
MF "a[164:167],v[16:19],v[96:99],a[164:167]\n"
"v_exp_f32 v121,v121\n"
MF "a[168:171],v[16:19],v[100:103],a[168:171]\n"
"v_exp_f32 v122,v122\n"
MF "a[188:191],v[20:23],v[100:103],a[188:191]\n"
"v_exp_f32 v123,v123\n"
MF "a[192:195],v[20:23],v[104:107],a[192:195]\n"
"v_add_f32 v54,v54,v120\n"
"v_add_f32 v54,v54,v121\n"
MF "a[172:175],v[16:19],v[104:107],a[172:175]\n"
CV "v82,v120,v121\n"
"v_add_f32 v54,v54,v122\n"
MF "a[176:179],v[16:19],v[108:111],a[176:179]\n"
"s_waitcnt vmcnt(6) lgkmcnt(0)\n"
"s_barrier\n"
MF "a[196:199],v[20:23],v[108:111],a[196:199]\n"
"s_add_u32 s72,s72,6144\n"
"ds_read_b128 v[16:19],v0 offset:49152\n"
"ds_read_b128 v[20:23],v1 offset:49152\n"
MF "a[216:219],v[24:27],v[108:111],a[216:219]\n"
"s_cmp_eq_u32 s72,s17\n"
"s_cselect_b32 s72,s16,s72\n"
"s_add_u32 m0,s64,24576\n"
MF "a[236:239],v[28:31],v[108:111],a[236:239]\n"
LD "v7,s[32:35],s72 offen nt lds\n"
MF "a[232:235],v[28:31],v[104:107],a[232:235]\n"
"s_add_u32 m0,s64,25472\n"
"v_add_f32 v54,v54,v123\n"
CV "v83,v122,v123\n"
MF "a[212:215],v[24:27],v[104:107],a[212:215]\n"
LD "v7,s[32:35],s72 offen offset:128 nt lds\n"
MF "a[208:211],v[24:27],v[100:103],a[208:211]\n"
"s_add_u32 m0,s64,26368\n"
MF "a[228:231],v[28:31],v[100:103],a[228:231]\n"
LD "v7,s[32:35],s72 offen offset:256 nt lds\n"
MF "a[224:227],v[28:31],v[96:99],a[224:227]\n"
MF "a[204:207],v[24:27],v[96:99],a[204:207]\n"
MF "a[200:203],v[24:27],v[92:95],a[200:203]\n"
MF "a[220:223],v[28:31],v[92:95],a[220:223]\n"
"s_waitcnt lgkmcnt(0)\n"
MF "v[92:95],v[16:19],v[136:139],v[32:35]\n"
"s_add_u32 s72,s72,6144\n"
"ds_read_b128 v[24:27],v0 offset:50176\n"
"ds_read_b128 v[28:31],v1 offset:50176\n"
MF "v[92:95],v[20:23],v[156:159],v[92:95]\n"
"s_add_u32 m0,s64,30720\n"
"v_exp_f32 v124,v124\n"
MF "v[96:99],v[20:23],v[160:163],v[36:39]\n"
LD "v7,s[32:35],s72 offen nt lds\n"
MF "v[96:99],v[16:19],v[140:143],v[96:99]\n"
"v_exp_f32 v125,v125\n"
MF "v[100:103],v[16:19],v[144:147],v[40:43]\n"
"s_add_u32 m0,s64,31616\n"
"v_exp_f32 v126,v126\n"
MF "v[100:103],v[20:23],v[164:167],v[100:103]\n"
LD "v7,s[32:35],s72 offen offset:128 nt lds\n"
MF "v[104:107],v[20:23],v[168:171],v[44:47]\n"
"v_exp_f32 v127,v127\n"
MF "v[104:107],v[16:19],v[148:151],v[104:107]\n"
"s_add_u32 m0,s64,32512\n"
"v_add_f32 v55,v55,v124\n"
"v_add_f32 v55,v55,v125\n"
MF "v[108:111],v[16:19],v[152:155],v[48:51]\n"
LD "v7,s[32:35],s72 offen offset:256 nt lds\n"
MF "v[108:111],v[20:23],v[172:175],v[108:111]\n"
"s_waitcnt lgkmcnt(0)\n"
"ds_read_b128 v[16:19],v0 offset:51200\n"
"ds_read_b128 v[20:23],v1 offset:51200\n"
MF "v[108:111],v[24:27],v[192:195],v[108:111]\n"
CV "v86,v124,v125\n"
"v_add_f32 v55,v55,v126\n"
MF "v[108:111],v[28:31],v[212:215],v[108:111]\n"
"v_add_f32 v55,v55,v127\n"
CV "v87,v126,v127\n"
MF "v[104:107],v[28:31],v[208:211],v[104:107]\n"
"v_exp_f32 v128,v128\n"
MF "v[104:107],v[24:27],v[188:191],v[104:107]\n"
"v_exp_f32 v129,v129\n"
MF "v[100:103],v[24:27],v[184:187],v[100:103]\n"
"v_exp_f32 v130,v130\n"
MF "v[100:103],v[28:31],v[204:207],v[100:103]\n"
"v_exp_f32 v131,v131\n"
MF "v[96:99],v[28:31],v[200:203],v[96:99]\n"
"v_add_f32 v56,v56,v128\n"
"v_add_f32 v56,v56,v129\n"
MF "v[96:99],v[24:27],v[180:183],v[96:99]\n"
CV "v90,v128,v129\n"
"v_add_f32 v56,v56,v130\n"
MF "v[92:95],v[24:27],v[176:179],v[92:95]\n"
"v_add_f32 v56,v56,v131\n"
CV "v91,v130,v131\n"
MF "v[92:95],v[28:31],v[196:199],v[92:95]\n"
"s_waitcnt lgkmcnt(0)\n"
"ds_read_b128 v[24:27],v0 offset:55296\n"
"ds_read_b128 v[28:31],v1 offset:55296\n"
MF "v[92:95],v[16:19],v[216:219],v[92:95]\n"
MF "v[92:95],v[20:23],v[236:239],v[92:95]\n"
MF "v[96:99],v[20:23],v[240:243],v[96:99]\n"
MF "v[96:99],v[16:19],v[220:223],v[96:99]\n"
MF "v[100:103],v[16:19],v[224:227],v[100:103]\n"
MF "v[100:103],v[20:23],v[244:247],v[100:103]\n"
MF "v[104:107],v[20:23],v[248:251],v[104:107]\n"
MF "v[104:107],v[16:19],v[228:231],v[104:107]\n"
"v_max3_f32 v10,v52,v53,v54\n"
"v_max3_f32 v10,v10,v55,v56\n"
MF "v[108:111],v[16:19],v[232:235],v[108:111]\n"
"v_cmp_ngt_f32 vcc,s77,v10\n"
MF "v[108:111],v[20:23],v[252:255],v[108:111]\n"
"s_waitcnt lgkmcnt(0)\n"
"s_cbranch_vccnz .Lstub_16\n"
".Lback_16:\n"
MF "v[112:115],v[24:27],v[136:139],v[32:35]\n"
"s_add_u32 s74,s74,12288\n"
"ds_read_b128 v[16:19],v0 offset:56320\n"
"ds_read_b128 v[20:23],v1 offset:56320\n"
MF "v[112:115],v[28:31],v[156:159],v[112:115]\n"
"s_cmp_eq_u32 s74,s20\n"
"s_cselect_b32 s74,s70,s74\n"
MF "v[116:119],v[28:31],v[160:163],v[36:39]\n"
"v_exp_f32 v92,v92\n"
MF "v[116:119],v[24:27],v[140:143],v[116:119]\n"
"v_exp_f32 v93,v93\n"
MF "v[120:123],v[24:27],v[144:147],v[40:43]\n"
"v_exp_f32 v94,v94\n"
MF "v[120:123],v[28:31],v[164:167],v[120:123]\n"
"v_exp_f32 v95,v95\n"
MF "v[124:127],v[28:31],v[168:171],v[44:47]\n"
"v_add_f32 v57,v52,v92\n"
"v_add_f32 v57,v57,v93\n"
MF "v[124:127],v[24:27],v[148:151],v[124:127]\n"
CV "v92,v92,v93\n"
"v_add_f32 v57,v57,v94\n"
MF "v[128:131],v[24:27],v[152:155],v[48:51]\n"
"v_add_f32 v57,v57,v95\n"
CV "v93,v94,v95\n"
MF "v[128:131],v[28:31],v[172:175],v[128:131]\n"
"s_waitcnt lgkmcnt(0)\n"
"ds_read_b128 v[24:27],v0 offset:57344\n"
"ds_read_b128 v[28:31],v1 offset:57344\n"
MF "v[128:131],v[16:19],v[192:195],v[128:131]\n"
"s_sub_u32 s61,s61,1\n"
"v_exp_f32 v96,v96\n"
MF "v[128:131],v[20:23],v[212:215],v[128:131]\n"
"s_cmp_eq_u32 s61,1\n"
"s_cselect_b32 s95,0,s95\n"
"v_add_f32 v58,v53,v96\n"
MF "v[124:127],v[20:23],v[208:211],v[124:127]\n"
"v_exp_f32 v97,v97\n"
MF "v[124:127],v[16:19],v[188:191],v[124:127]\n"
"v_exp_f32 v98,v98\n"
MF "v[120:123],v[16:19],v[184:187],v[120:123]\n"
"v_exp_f32 v99,v99\n"
MF "v[120:123],v[20:23],v[204:207],v[120:123]\n"
"v_add_f32 v58,v58,v97\n"
CV "v96,v96,v97\n"
MF "v[116:119],v[20:23],v[200:203],v[116:119]\n"
"v_add_f32 v58,v58,v98\n"
"v_add_f32 v58,v58,v99\n"
MF "v[116:119],v[16:19],v[180:183],v[116:119]\n"
"s_waitcnt vmcnt(6) lgkmcnt(0)\n"
"s_barrier\n"
MF "v[112:115],v[16:19],v[176:179],v[112:115]\n"
CV "v97,v98,v99\n"
MF "v[112:115],v[20:23],v[196:199],v[112:115]\n"
"s_nop 0\n"
TR "v[16:17],v2\n"
TR "v[18:19],v2 offset:6144\n"
MF "v[112:115],v[24:27],v[216:219],v[112:115]\n"
"s_add_u32 s73,s73,12288\n"
TR "v[20:21],v3\n"
TR "v[22:23],v3 offset:6144\n"
MF "v[112:115],v[28:31],v[236:239],v[112:115]\n"
"s_cmp_eq_u32 s73,s19\n"
"s_cselect_b32 s73,s18,s73\n"
"s_add_u32 m0,s65,49152\n"
MF "v[116:119],v[28:31],v[240:243],v[116:119]\n"
LD "v8,s[36:39],s73 offen nt lds\n"
MF "v[116:119],v[24:27],v[220:223],v[116:119]\n"
"s_add_u32 m0,s65,50048\n"
"v_exp_f32 v100,v100\n"
MF "v[120:123],v[24:27],v[224:227],v[120:123]\n"
LD "v8,s[36:39],s73 offen offset:128 nt lds\n"
MF "v[120:123],v[28:31],v[244:247],v[120:123]\n"
"v_exp_f32 v101,v101\n"
MF "v[124:127],v[28:31],v[248:251],v[124:127]\n"
"s_add_u32 m0,s65,50944\n"
"v_exp_f32 v102,v102\n"
MF "v[124:127],v[24:27],v[228:231],v[124:127]\n"
LD "v8,s[36:39],s73 offen offset:256 nt lds\n"
MF "v[128:131],v[24:27],v[232:235],v[128:131]\n"
"v_exp_f32 v103,v103\n"
MF "v[128:131],v[28:31],v[252:255],v[128:131]\n"
"s_waitcnt lgkmcnt(0)\n"
TR "v[24:25],v4\n"
TR "v[26:27],v4 offset:6144\n"
MF "a[0:3],v[16:19],v[72:75],a[0:3]\n"
"s_add_u32 m0,s65,49152\n"
TR "v[28:29],v5\n"
TR "v[30:31],v5 offset:6144\n"
MF "a[20:23],v[20:23],v[72:75],a[20:23]\n"
LD "v8,s[36:39],s73 offen offset:3072 nt lds\n"
MF "a[24:27],v[20:23],v[76:79],a[24:27]\n"
"v_add_f32 v59,v54,v100\n"
"v_add_f32 v59,v59,v101\n"
MF "a[4:7],v[16:19],v[76:79],a[4:7]\n"
"s_add_u32 m0,s65,50048\n"
CV "v100,v100,v101\n"
"v_add_f32 v59,v59,v102\n"
MF "a[8:11],v[16:19],v[80:83],a[8:11]\n"
LD "v8,s[36:39],s73 offen offset:3200 nt lds\n"
MF "a[28:31],v[20:23],v[80:83],a[28:31]\n"
"v_add_f32 v59,v59,v103\n"
CV "v101,v102,v103\n"
MF "a[32:35],v[20:23],v[84:87],a[32:35]\n"
"s_add_u32 m0,s65,50944\n"
"v_exp_f32 v104,v104\n"
MF "a[12:15],v[16:19],v[84:87],a[12:15]\n"
LD "v8,s[36:39],s73 offen offset:3328 nt lds\n"
MF "a[16:19],v[16:19],v[88:91],a[16:19]\n"
"v_exp_f32 v105,v105\n"
MF "a[36:39],v[20:23],v[88:91],a[36:39]\n"
"s_waitcnt lgkmcnt(0)\n"
TR "v[16:17],v2 offset:1024\n"
TR "v[18:19],v2 offset:7168\n"
MF "a[56:59],v[24:27],v[88:91],a[56:59]\n"
"s_nop 0\n"
TR "v[20:21],v3 offset:1024\n"
TR "v[22:23],v3 offset:7168\n"
MF "a[76:79],v[28:31],v[88:91],a[76:79]\n"
"v_exp_f32 v106,v106\n"
MF "a[72:75],v[28:31],v[84:87],a[72:75]\n"
"v_exp_f32 v107,v107\n"
MF "a[52:55],v[24:27],v[84:87],a[52:55]\n"
"v_add_f32 v60,v55,v104\n"
"v_add_f32 v60,v60,v105\n"
MF "a[48:51],v[24:27],v[80:83],a[48:51]\n"
CV "v104,v104,v105\n"
"v_add_f32 v60,v60,v106\n"
MF "a[68:71],v[28:31],v[80:83],a[68:71]\n"
"v_add_f32 v60,v60,v107\n"
CV "v105,v106,v107\n"
MF "a[64:67],v[28:31],v[76:79],a[64:67]\n"
"v_exp_f32 v108,v108\n"
MF "a[44:47],v[24:27],v[76:79],a[44:47]\n"
"v_exp_f32 v109,v109\n"
MF "a[40:43],v[24:27],v[72:75],a[40:43]\n"
"v_exp_f32 v110,v110\n"
MF "a[60:63],v[28:31],v[72:75],a[60:63]\n"
"s_waitcnt lgkmcnt(0)\n"
TR "v[24:25],v4 offset:1024\n"
TR "v[26:27],v4 offset:7168\n"
MF "a[80:83],v[16:19],v[72:75],a[80:83]\n"
"s_nop 0\n"
TR "v[28:29],v5 offset:1024\n"
TR "v[30:31],v5 offset:7168\n"
MF "a[100:103],v[20:23],v[72:75],a[100:103]\n"
"v_exp_f32 v111,v111\n"
MF "a[104:107],v[20:23],v[76:79],a[104:107]\n"
"v_add_f32 v61,v56,v108\n"
"v_add_f32 v61,v61,v109\n"
MF "a[84:87],v[16:19],v[76:79],a[84:87]\n"
CV "v108,v108,v109\n"
"v_add_f32 v61,v61,v110\n"
MF "a[88:91],v[16:19],v[80:83],a[88:91]\n"
"v_add_f32 v61,v61,v111\n"
CV "v109,v110,v111\n"
MF "a[108:111],v[20:23],v[80:83],a[108:111]\n"
"v_exp_f32 v112,v112\n"
MF "a[112:115],v[20:23],v[84:87],a[112:115]\n"
"v_exp_f32 v113,v113\n"
MF "a[92:95],v[16:19],v[84:87],a[92:95]\n"
"v_exp_f32 v114,v114\n"
MF "a[96:99],v[16:19],v[88:91],a[96:99]\n"
"v_exp_f32 v115,v115\n"
MF "a[116:119],v[20:23],v[88:91],a[116:119]\n"
"s_waitcnt lgkmcnt(0)\n"
TR "v[16:17],v2 offset:2048\n"
TR "v[18:19],v2 offset:8192\n"
MF "a[136:139],v[24:27],v[88:91],a[136:139]\n"
"s_nop 0\n"
TR "v[20:21],v3 offset:2048\n"
TR "v[22:23],v3 offset:8192\n"
MF "a[156:159],v[28:31],v[88:91],a[156:159]\n"
"v_add_f32 v57,v57,v112\n"
"v_add_f32 v57,v57,v113\n"
MF "a[152:155],v[28:31],v[84:87],a[152:155]\n"
CV "v94,v112,v113\n"
"v_add_f32 v57,v57,v114\n"
MF "a[132:135],v[24:27],v[84:87],a[132:135]\n"
"v_add_f32 v57,v57,v115\n"
CV "v95,v114,v115\n"
MF "a[128:131],v[24:27],v[80:83],a[128:131]\n"
"v_exp_f32 v116,v116\n"
MF "a[148:151],v[28:31],v[80:83],a[148:151]\n"
"v_exp_f32 v117,v117\n"
MF "a[144:147],v[28:31],v[76:79],a[144:147]\n"
"v_exp_f32 v118,v118\n"
MF "a[124:127],v[24:27],v[76:79],a[124:127]\n"
"v_exp_f32 v119,v119\n"
MF "a[120:123],v[24:27],v[72:75],a[120:123]\n"
"v_add_f32 v58,v58,v116\n"
"v_add_f32 v58,v58,v117\n"
MF "a[140:143],v[28:31],v[72:75],a[140:143]\n"
"s_waitcnt lgkmcnt(0)\n"
TR "v[24:25],v4 offset:2048\n"
TR "v[26:27],v4 offset:8192\n"
MF "a[160:163],v[16:19],v[72:75],a[160:163]\n"
"s_nop 0\n"
TR "v[28:29],v5 offset:2048\n"
TR "v[30:31],v5 offset:8192\n"
MF "a[180:183],v[20:23],v[72:75],a[180:183]\n"
CV "v98,v116,v117\n"
"v_add_f32 v58,v58,v118\n"
MF "a[184:187],v[20:23],v[76:79],a[184:187]\n"
"v_add_f32 v58,v58,v119\n"
CV "v99,v118,v119\n"
MF "a[164:167],v[16:19],v[76:79],a[164:167]\n"
"v_exp_f32 v120,v120\n"
MF "a[168:171],v[16:19],v[80:83],a[168:171]\n"
"v_exp_f32 v121,v121\n"
MF "a[188:191],v[20:23],v[80:83],a[188:191]\n"
"v_exp_f32 v122,v122\n"
MF "a[192:195],v[20:23],v[84:87],a[192:195]\n"
"v_exp_f32 v123,v123\n"
MF "a[172:175],v[16:19],v[84:87],a[172:175]\n"
"v_add_f32 v59,v59,v120\n"
"v_add_f32 v59,v59,v121\n"
MF "a[176:179],v[16:19],v[88:91],a[176:179]\n"
"s_waitcnt vmcnt(6) lgkmcnt(0)\n"
"s_barrier\n"
MF "a[196:199],v[20:23],v[88:91],a[196:199]\n"
"s_add_u32 s72,s72,6144\n"
"ds_read_b128 v[16:19],v0 offset:24576\n"
"ds_read_b128 v[20:23],v1 offset:24576\n"
MF "a[216:219],v[24:27],v[88:91],a[216:219]\n"
"s_cmp_eq_u32 s72,s17\n"
"s_cselect_b32 s72,s16,s72\n"
"s_add_u32 m0,s64,0\n"
MF "a[236:239],v[28:31],v[88:91],a[236:239]\n"
LD "v7,s[32:35],s72 offen nt lds\n"
MF "a[232:235],v[28:31],v[84:87],a[232:235]\n"
"s_add_u32 m0,s64,896\n"
CV "v102,v120,v121\n"
"v_add_f32 v59,v59,v122\n"
MF "a[212:215],v[24:27],v[84:87],a[212:215]\n"
LD "v7,s[32:35],s72 offen offset:128 nt lds\n"
MF "a[208:211],v[24:27],v[80:83],a[208:211]\n"
"s_add_u32 m0,s64,1792\n"
"v_add_f32 v59,v59,v123\n"
CV "v103,v122,v123\n"
MF "a[228:231],v[28:31],v[80:83],a[228:231]\n"
LD "v7,s[32:35],s72 offen offset:256 nt lds\n"
MF "a[224:227],v[28:31],v[76:79],a[224:227]\n"
MF "a[204:207],v[24:27],v[76:79],a[204:207]\n"
"s_waitcnt lgkmcnt(0)\n"
MF "a[200:203],v[24:27],v[72:75],a[200:203]\n"
"s_cmp_lg_u32 s61,0\n"
MF "a[220:223],v[28:31],v[72:75],a[220:223]\n"
"s_cbranch_scc1 .Laloop\n"
".Lepi2:\n"
"v_exp_f32 v124,v124\n"
"v_exp_f32 v125,v125\n"
"v_exp_f32 v126,v126\n"
"v_exp_f32 v127,v127\n"
"v_exp_f32 v128,v128\n"
"v_exp_f32 v129,v129\n"
"v_exp_f32 v130,v130\n"
"v_exp_f32 v131,v131\n"
"v_add_f32 v60,v60,v124\n"
"v_add_f32 v61,v61,v128\n"
"v_add_f32 v60,v60,v125\n"
"v_add_f32 v61,v61,v129\n"
"v_add_f32 v60,v60,v126\n"
"v_add_f32 v61,v61,v130\n"
"v_add_f32 v60,v60,v127\n"
"v_add_f32 v61,v61,v131\n"
CV "v106,v124,v125\n"
CV "v107,v126,v127\n"
CV "v110,v128,v129\n"
CV "v111,v130,v131\n"
"v_max3_f32 v10,v57,v58,v59\n"
"v_max3_f32 v10,v10,v60,v61\n"
"v_cmp_le_f32 vcc,0x47800000,v10\n"
"s_cbranch_vccnz .Lstub_17\n"
".Lback_17:\n"
"s_waitcnt vmcnt(3) lgkmcnt(0)\n"
"s_barrier\n"
"s_and_b32 s58,s56,1\n"
"s_mul_i32 s79,s58,12288\n"
"s_lshr_b32 s58,s56,1\n"
"s_mul_i32 s72,s58,61440\n"
"s_mul_i32 s73,s58,6144\n"
"s_add_u32 s73,s73,s79\n"
"v_lshrrev_b32_e64 v192,7,v9\n"
"v_mul_u32_u24 v192,896,v192\n"
"v_add_u32_e64 v192,v192,v8\n"
"s_add_u32 s88,s88,1024\n"
"s_add_u32 s89,s89,1024\n"
"s_add_u32 s72,s72,122880\n"
"s_lshl_b32 s58,s56,10\n"
"s_add_u32 s58,s58,73728\n"
"v_add_u32 v236,s58,v9\n"
"v_add_u32 v237,65536,v236\n"
"v_mov_b32 v10,1.0\n"
TR "v[160:161],v4 offset:50176\n"
TR "v[162:163],v4 offset:56320\n"
"v_mfma_f32_16x16x4_f32 v[16:19],v10,v57,0\n"
TR "v[164:165],v5 offset:50176\n"
TR "v[166:167],v5 offset:56320\n"
TR "v[168:169],v2 offset:51200\n"
TR "v[170:171],v2 offset:57344\n"
TR "v[172:173],v3 offset:51200\n"
TR "v[174:175],v3 offset:57344\n"
"v_mfma_f32_16x16x4_f32 v[20:23],v10,v58,0\n"
TR "v[176:177],v4 offset:51200\n"
TR "v[178:179],v4 offset:57344\n"
TR "v[180:181],v5 offset:51200\n"
TR "v[182:183],v5 offset:57344\n"
TR "v[136:137],v2 offset:49152\n"
TR "v[138:139],v2 offset:55296\n"
"v_mfma_f32_16x16x4_f32 v[24:27],v10,v59,0\n"
TR "v[140:141],v3 offset:49152\n"
TR "v[142:143],v3 offset:55296\n"
TR "v[144:145],v4 offset:49152\n"
TR "v[146:147],v4 offset:55296\n"
TR "v[148:149],v5 offset:49152\n"
TR "v[150:151],v5 offset:55296\n"
"v_mfma_f32_16x16x4_f32 v[28:31],v10,v60,0\n"
TR "v[152:153],v2 offset:50176\n"
TR "v[154:155],v2 offset:56320\n"
TR "v[156:157],v3 offset:50176\n"
TR "v[158:159],v3 offset:56320\n"
"v_mfma_f32_16x16x4_f32 v[32:35],v10,v61,0\n"
"s_add_u32 m0,s73,24576\n"
"s_add_u32 s80,s89,s72\n"
LD "v192,s[48:51],s80 offen lds\n"
"v_add_u32 v193,9216,v192\n"
LD "v193,s[48:51],s80 offen offset:1024 lds\n"
"s_waitcnt lgkmcnt(15)\n"
MF "a[120:123],v[160:163],v[92:95],a[120:123]\n"
MF "a[140:143],v[164:167],v[92:95],a[140:143]\n"
"v_add_u32 v194,18432,v192\n"
LD "v194,s[48:51],s80 offen offset:2048 lds\n"
"v_add_u32 v195,27648,v192\n"
LD "v195,s[48:51],s80 offen offset:3072 lds\n"
"v_rcp_f32 v208,v16\n"
"v_rcp_f32 v209,v20\n"
"v_rcp_f32 v210,v24\n"
"v_rcp_f32 v211,v28\n"
"v_rcp_f32 v212,v32\n"
MF "a[124:127],v[160:163],v[96:99],a[124:127]\n"
"v_mul_f32_e64 v214,-s13,v208\n"
"v_mul_f32_e64 v215,-s13,v209\n"
"v_mul_f32_e64 v216,-s13,v210\n"
"v_mul_f32_e64 v217,-s13,v211\n"
"v_mul_f32_e64 v218,-s13,v212\n"
"s_add_u32 m0,s73,28672\n"
"v_add_u32 v196,40960,v192\n"
LD "v196,s[48:51],s80 offen lds\n"
AR "v72,a120\n"
AR "v73,a121\n"
AR "v74,a122\n"
MF "a[144:147],v[164:167],v[96:99],a[144:147]\n"
AR "v75,a123\n"
AR "v76,a140\n"
AR "v77,a141\n"
AR "v78,a142\n"
AR "v79,a143\n"
"v_pk_fma_f32 v[72:73],v[72:73],v[208:209],v[214:215] op_sel:[0,0,0] op_sel_hi:[1,0,0]\n"
"v_pk_fma_f32 v[74:75],v[74:75],v[208:209],v[214:215] op_sel:[0,0,0] op_sel_hi:[1,0,0]\n"
"v_pk_fma_f32 v[76:77],v[76:77],v[208:209],v[214:215] op_sel:[0,0,0] op_sel_hi:[1,0,0]\n"
"v_pk_fma_f32 v[78:79],v[78:79],v[208:209],v[214:215] op_sel:[0,0,0] op_sel_hi:[1,0,0]\n"
CV "v112,v72,v73\n"
CV "v113,v74,v75\n"
CV "v114,v76,v77\n"
CV "v115,v78,v79\n"
MF "a[128:131],v[160:163],v[100:103],a[128:131]\n"
"ds_read_b128 a[140:143],v237 offset:20480\n"
AW "a120,v112\n"
AW "a121,v113\n"
AW "a122,v114\n"
AW "a123,v115\n"
"s_waitcnt lgkmcnt(7)\n"
MF "a[44:47],v[144:147],v[96:99],a[44:47]\n"
"v_add_u32 v197,50176,v192\n"
LD "v197,s[48:51],s80 offen offset:1024 lds\n"
AR "v80,a124\n"
AR "v81,a125\n"
AR "v82,a126\n"
MF "a[148:151],v[164:167],v[100:103],a[148:151]\n"
AR "v83,a127\n"
AR "v84,a144\n"
AR "v85,a145\n"
"s_nop 0\n"
AR "v86,a146\n"
AR "v87,a147\n"
"v_pk_fma_f32 v[80:81],v[80:81],v[208:209],v[214:215] op_sel:[0,1,1] op_sel_hi:[1,1,1]\n"
"v_pk_fma_f32 v[82:83],v[82:83],v[208:209],v[214:215] op_sel:[0,1,1] op_sel_hi:[1,1,1]\n"
"v_pk_fma_f32 v[84:85],v[84:85],v[208:209],v[214:215] op_sel:[0,1,1] op_sel_hi:[1,1,1]\n"
"v_pk_fma_f32 v[86:87],v[86:87],v[208:209],v[214:215] op_sel:[0,1,1] op_sel_hi:[1,1,1]\n"
CV "v116,v80,v81\n"
CV "v117,v82,v83\n"
CV "v118,v84,v85\n"
CV "v119,v86,v87\n"
MF "a[132:135],v[160:163],v[104:107],a[132:135]\n"
"ds_read_b128 a[144:147],v236\n"
AW "a124,v116\n"
AW "a125,v117\n"
AW "a126,v118\n"
AW "a127,v119\n"
"s_waitcnt lgkmcnt(6)\n"
MF "a[64:67],v[148:151],v[96:99],a[64:67]\n"
"s_add_u32 m0,s73,0\n"
"s_add_u32 s80,s88,s72\n"
"s_sub_u32 s72,s72,122880\n"
"s_nop 0\n"
LD "v192,s[48:51],s80 offen lds\n"
AR "v72,a128\n"
AR "v73,a129\n"
AR "v74,a130\n"
MF "a[152:155],v[164:167],v[104:107],a[152:155]\n"
AR "v75,a131\n"
AR "v76,a148\n"
AR "v77,a149\n"
AR "v78,a150\n"
AR "v79,a151\n"
"v_pk_fma_f32 v[72:73],v[72:73],v[210:211],v[216:217] op_sel:[0,0,0] op_sel_hi:[1,0,0]\n"
"v_pk_fma_f32 v[74:75],v[74:75],v[210:211],v[216:217] op_sel:[0,0,0] op_sel_hi:[1,0,0]\n"
"v_pk_fma_f32 v[76:77],v[76:77],v[210:211],v[216:217] op_sel:[0,0,0] op_sel_hi:[1,0,0]\n"
"v_pk_fma_f32 v[78:79],v[78:79],v[210:211],v[216:217] op_sel:[0,0,0] op_sel_hi:[1,0,0]\n"
LD "v193,s[48:51],s80 offen offset:1024 lds\n"
CV "v112,v72,v73\n"
CV "v113,v74,v75\n"
CV "v114,v76,v77\n"
CV "v115,v78,v79\n"
MF "a[136:139],v[160:163],v[108:111],a[136:139]\n"
"ds_read_b128 a[148:151],v236 offset:4096\n"
AW "a128,v112\n"
AW "a129,v113\n"
AW "a130,v114\n"
AW "a131,v115\n"
"s_waitcnt lgkmcnt(5)\n"
MF "a[84:87],v[152:155],v[96:99],a[84:87]\n"
LD "v194,s[48:51],s80 offen offset:2048 lds\n"
AR "v80,a132\n"
AR "v81,a133\n"
AR "v82,a134\n"
MF "a[156:159],v[164:167],v[108:111],a[156:159]\n"
AR "v83,a135\n"
AR "v84,a152\n"
AR "v85,a153\n"
"s_nop 0\n"
AR "v86,a154\n"
AR "v87,a155\n"
"v_pk_fma_f32 v[80:81],v[80:81],v[210:211],v[216:217] op_sel:[0,1,1] op_sel_hi:[1,1,1]\n"
"v_pk_fma_f32 v[82:83],v[82:83],v[210:211],v[216:217] op_sel:[0,1,1] op_sel_hi:[1,1,1]\n"
"v_pk_fma_f32 v[84:85],v[84:85],v[210:211],v[216:217] op_sel:[0,1,1] op_sel_hi:[1,1,1]\n"
"v_pk_fma_f32 v[86:87],v[86:87],v[210:211],v[216:217] op_sel:[0,1,1] op_sel_hi:[1,1,1]\n"
CV "v116,v80,v81\n"
CV "v117,v82,v83\n"
CV "v118,v84,v85\n"
CV "v119,v86,v87\n"
MF "a[160:163],v[168:171],v[92:95],a[160:163]\n"
"ds_read_b128 a[152:155],v236 offset:8192\n"
AW "a132,v116\n"
AW "a133,v117\n"
AW "a134,v118\n"
AW "a135,v119\n"
"s_waitcnt lgkmcnt(4)\n"
MF "a[104:107],v[156:159],v[96:99],a[104:107]\n"
LD "v195,s[48:51],s80 offen offset:3072 lds\n"
AR "v72,a136\n"
AR "v73,a137\n"
AR "v74,a138\n"
MF "a[180:183],v[172:175],v[92:95],a[180:183]\n"
AR "v75,a139\n"
AR "v76,a156\n"
AR "v77,a157\n"
"s_nop 0\n"
AR "v78,a158\n"
AR "v79,a159\n"
"v_pk_fma_f32 v[72:73],v[72:73],v[212:213],v[218:219] op_sel:[0,0,0] op_sel_hi:[1,0,0]\n"
"v_pk_fma_f32 v[74:75],v[74:75],v[212:213],v[218:219] op_sel:[0,0,0] op_sel_hi:[1,0,0]\n"
"v_pk_fma_f32 v[76:77],v[76:77],v[212:213],v[218:219] op_sel:[0,0,0] op_sel_hi:[1,0,0]\n"
"v_pk_fma_f32 v[78:79],v[78:79],v[212:213],v[218:219] op_sel:[0,0,0] op_sel_hi:[1,0,0]\n"
CV "v112,v72,v73\n"
CV "v113,v74,v75\n"
CV "v114,v76,v77\n"
CV "v115,v78,v79\n"
MF "a[164:167],v[168:171],v[96:99],a[164:167]\n"
"ds_read_b128 a[156:159],v236 offset:12288\n"
AW "a136,v112\n"
AW "a137,v113\n"
AW "a138,v114\n"
AW "a139,v115\n"
"v_max3_f32 v10,|v72|,|v73|,|v74|\n"
"s_lshl_b32 s58,s56,10\n"
"s_add_u32 s58,s58,159744\n"
"v_max3_f32 v10,v10,|v75|,|v76|\n"
"v_max3_f32 v10,v10,|v77|,|v78|\n"
"v_max3_f32 v10,v10,|v78|,|v79|\n"
"v_mul_f32 v10,s10,v10\n"
"v_lshrrev_b32_e64 v11,2,v9\n"
"v_readlane_b32 s12,v10,3\n"
"v_readlane_b32 s6,v10,22\n"
"v_readlane_b32 s7,v10,41\n"
"v_readlane_b32 s8,v10,60\n"
"v_add_u32 v11,s58,v11\n"
"s_min_u32 s12,s12,s6\n"
"s_min_u32 s7,s7,s8\n"
"s_min_u32 s12,s12,s7\n"
"s_and_b32 s12,s12,0x7f800000\n"
"s_sub_u32 s12,s12,0x14000000\n"
"s_cmp_lt_u32 s12,0x50000000\n"
"s_cselect_b32 s12,s12,0xe7400000\n"
"s_add_u32 s12,s12,0x18c00000\n"
"v_mov_b32 v10,s12\n"
"v_or_b32 v12,0x80000000,v10\n"
"ds_write2st64_b32 v11,v10,v12 offset1:1\n"
MF "a[8:11],v[136:139],v[100:103],a[8:11]\n"
"s_add_u32 m0,s73,4096\n"
"s_nop 0\n"
LD "v196,s[48:51],s80 offen lds\n"
AR "v80,a160\n"
AR "v81,a161\n"
AR "v82,a162\n"
MF "a[184:187],v[172:175],v[96:99],a[184:187]\n"
AR "v83,a163\n"
AR "v84,a180\n"
AR "v85,a181\n"
AR "v86,a182\n"
AR "v87,a183\n"
"v_pk_fma_f32 v[80:81],v[80:81],v[208:209],v[214:215] op_sel:[0,0,0] op_sel_hi:[1,0,0]\n"
"v_pk_fma_f32 v[82:83],v[82:83],v[208:209],v[214:215] op_sel:[0,0,0] op_sel_hi:[1,0,0]\n"
"v_pk_fma_f32 v[84:85],v[84:85],v[208:209],v[214:215] op_sel:[0,0,0] op_sel_hi:[1,0,0]\n"
"v_pk_fma_f32 v[86:87],v[86:87],v[208:209],v[214:215] op_sel:[0,0,0] op_sel_hi:[1,0,0]\n"
LD "v197,s[48:51],s80 offen offset:1024 lds\n"
CV "v116,v80,v81\n"
CV "v117,v82,v83\n"
CV "v118,v84,v85\n"
CV "v119,v86,v87\n"
MF "a[168:171],v[168:171],v[100:103],a[168:171]\n"
"ds_read_b128 a[180:183],v236 offset:16384\n"
AW "a160,v116\n"
AW "a161,v117\n"
AW "a162,v118\n"
AW "a163,v119\n"
MF "a[28:31],v[140:143],v[100:103],a[28:31]\n"
AR "v72,a164\n"
AR "v73,a165\n"
AR "v74,a166\n"
MF "a[188:191],v[172:175],v[100:103],a[188:191]\n"
AR "v75,a167\n"
AR "v76,a184\n"
AR "v77,a185\n"
AR "v78,a186\n"
AR "v79,a187\n"
"v_pk_fma_f32 v[72:73],v[72:73],v[208:209],v[214:215] op_sel:[0,1,1] op_sel_hi:[1,1,1]\n"
"v_pk_fma_f32 v[74:75],v[74:75],v[208:209],v[214:215] op_sel:[0,1,1] op_sel_hi:[1,1,1]\n"
"v_pk_fma_f32 v[76:77],v[76:77],v[208:209],v[214:215] op_sel:[0,1,1] op_sel_hi:[1,1,1]\n"
"v_pk_fma_f32 v[78:79],v[78:79],v[208:209],v[214:215] op_sel:[0,1,1] op_sel_hi:[1,1,1]\n"
CV "v112,v72,v73\n"
CV "v113,v74,v75\n"
CV "v114,v76,v77\n"
CV "v115,v78,v79\n"
MF "a[172:175],v[168:171],v[104:107],a[172:175]\n"
"ds_read_b128 a[184:187],v236 offset:20480\n"
AW "a164,v112\n"
AW "a165,v113\n"
AW "a166,v114\n"
AW "a167,v115\n"
MF "a[48:51],v[144:147],v[100:103],a[48:51]\n"
"s_waitcnt lgkmcnt(0)\n"
"s_barrier\n"
"s_add_u32 m0,s73,49152\n"
"s_add_u32 s80,s89,s72\n"
"s_nop 0\n"
LD "v192,s[48:51],s80 offen lds\n"
AR "v80,a168\n"
AR "v81,a169\n"
AR "v82,a170\n"
MF "a[192:195],v[172:175],v[104:107],a[192:195]\n"
AR "v83,a171\n"
AR "v84,a188\n"
AR "v85,a189\n"
AR "v86,a190\n"
AR "v87,a191\n"
"v_pk_fma_f32 v[80:81],v[80:81],v[210:211],v[216:217] op_sel:[0,0,0] op_sel_hi:[1,0,0]\n"
"v_pk_fma_f32 v[82:83],v[82:83],v[210:211],v[216:217] op_sel:[0,0,0] op_sel_hi:[1,0,0]\n"
"v_pk_fma_f32 v[84:85],v[84:85],v[210:211],v[216:217] op_sel:[0,0,0] op_sel_hi:[1,0,0]\n"
"v_pk_fma_f32 v[86:87],v[86:87],v[210:211],v[216:217] op_sel:[0,0,0] op_sel_hi:[1,0,0]\n"
LD "v193,s[48:51],s80 offen offset:1024 lds\n"
CV "v116,v80,v81\n"
CV "v117,v82,v83\n"
CV "v118,v84,v85\n"
CV "v119,v86,v87\n"
MF "a[176:179],v[168:171],v[108:111],a[176:179]\n"
"v_accvgpr_mov_b32 a188,a240\n"
"v_accvgpr_mov_b32 a189,a241\n"
"v_accvgpr_mov_b32 a190,a242\n"
"v_accvgpr_mov_b32 a191,a243\n"
AW "a168,v116\n"
AW "a169,v117\n"
AW "a170,v118\n"
AW "a171,v119\n"
MF "a[68:71],v[148:151],v[100:103],a[68:71]\n"
LD "v194,s[48:51],s80 offen offset:2048 lds\n"
AR "v72,a172\n"
AR "v73,a173\n"
AR "v74,a174\n"
MF "a[196:199],v[172:175],v[108:111],a[196:199]\n"
AR "v75,a175\n"
AR "v76,a192\n"
AR "v77,a193\n"
AR "v78,a194\n"
AR "v79,a195\n"
"v_pk_fma_f32 v[72:73],v[72:73],v[210:211],v[216:217] op_sel:[0,1,1] op_sel_hi:[1,1,1]\n"
"v_pk_fma_f32 v[74:75],v[74:75],v[210:211],v[216:217] op_sel:[0,1,1] op_sel_hi:[1,1,1]\n"
"v_pk_fma_f32 v[76:77],v[76:77],v[210:211],v[216:217] op_sel:[0,1,1] op_sel_hi:[1,1,1]\n"
"v_pk_fma_f32 v[78:79],v[78:79],v[210:211],v[216:217] op_sel:[0,1,1] op_sel_hi:[1,1,1]\n"
LD "v195,s[48:51],s80 offen offset:3072 lds\n"
CV "v112,v72,v73\n"
CV "v113,v74,v75\n"
CV "v114,v76,v77\n"
CV "v115,v78,v79\n"
MF "a[200:203],v[176:179],v[92:95],a[200:203]\n"
"v_accvgpr_mov_b32 a192,a244\n"
"v_accvgpr_mov_b32 a193,a245\n"
"v_accvgpr_mov_b32 a194,a246\n"
"v_accvgpr_mov_b32 a195,a247\n"
AW "a172,v112\n"
AW "a173,v113\n"
AW "a174,v114\n"
AW "a175,v115\n"
MF "a[88:91],v[152:155],v[100:103],a[88:91]\n"
"s_add_u32 m0,s73,53248\n"
"s_nop 0\n"
LD "v196,s[48:51],s80 offen lds\n"
AR "v80,a176\n"
AR "v81,a177\n"
AR "v82,a178\n"
MF "a[220:223],v[180:183],v[92:95],a[220:223]\n"
AR "v83,a179\n"
AR "v84,a196\n"
AR "v85,a197\n"
"s_nop 0\n"
AR "v86,a198\n"
AR "v87,a199\n"
"v_pk_fma_f32 v[80:81],v[80:81],v[212:213],v[218:219] op_sel:[0,0,0] op_sel_hi:[1,0,0]\n"
"v_pk_fma_f32 v[82:83],v[82:83],v[212:213],v[218:219] op_sel:[0,0,0] op_sel_hi:[1,0,0]\n"
"v_pk_fma_f32 v[84:85],v[84:85],v[212:213],v[218:219] op_sel:[0,0,0] op_sel_hi:[1,0,0]\n"
"v_pk_fma_f32 v[86:87],v[86:87],v[212:213],v[218:219] op_sel:[0,0,0] op_sel_hi:[1,0,0]\n"
CV "v116,v80,v81\n"
CV "v117,v82,v83\n"
CV "v118,v84,v85\n"
CV "v119,v86,v87\n"
MF "a[204:207],v[176:179],v[96:99],a[204:207]\n"
"v_accvgpr_mov_b32 a196,a248\n"
"v_accvgpr_mov_b32 a197,a249\n"
"v_accvgpr_mov_b32 a198,a250\n"
"v_accvgpr_mov_b32 a199,a251\n"
AW "a176,v116\n"
AW "a177,v117\n"
AW "a178,v118\n"
AW "a179,v119\n"
MF "a[108:111],v[156:159],v[100:103],a[108:111]\n"
LD "v197,s[48:51],s80 offen offset:1024 lds\n"
AR "v72,a200\n"
AR "v73,a201\n"
AR "v74,a202\n"
MF "a[224:227],v[180:183],v[96:99],a[224:227]\n"
AR "v75,a203\n"
AR "v76,a220\n"
AR "v77,a221\n"
AR "v78,a222\n"
AR "v79,a223\n"
"v_pk_fma_f32 v[72:73],v[72:73],v[208:209],v[214:215] op_sel:[0,0,0] op_sel_hi:[1,0,0]\n"
"v_pk_fma_f32 v[74:75],v[74:75],v[208:209],v[214:215] op_sel:[0,0,0] op_sel_hi:[1,0,0]\n"
"v_pk_fma_f32 v[76:77],v[76:77],v[208:209],v[214:215] op_sel:[0,0,0] op_sel_hi:[1,0,0]\n"
"v_pk_fma_f32 v[78:79],v[78:79],v[208:209],v[214:215] op_sel:[0,0,0] op_sel_hi:[1,0,0]\n"
CV "v112,v72,v73\n"
CV "v113,v74,v75\n"
CV "v114,v76,v77\n"
CV "v115,v78,v79\n"
MF "a[208:211],v[176:179],v[100:103],a[208:211]\n"
AW "a220,v62\n"
AW "a221,v63\n"
AW "a222,v64\n"
AW "a223,v65\n"
AW "a200,v112\n"
AW "a201,v113\n"
AW "a202,v114\n"
AW "a203,v115\n"
MF "a[12:15],v[136:139],v[104:107],a[12:15]\n"
"s_add_u32 m0,s73,73728\n"
"s_add_u32 s80,s88,s72\n"
LD "v192,s[48:51],s80 offen lds\n"
AR "v80,a204\n"
AR "v81,a205\n"
AR "v82,a206\n"
MF "a[228:231],v[180:183],v[100:103],a[228:231]\n"
AR "v83,a207\n"
AR "v84,a224\n"
AR "v85,a225\n"
"s_nop 0\n"
AR "v86,a226\n"
AR "v87,a227\n"
"v_pk_fma_f32 v[80:81],v[80:81],v[208:209],v[214:215] op_sel:[0,1,1] op_sel_hi:[1,1,1]\n"
"v_pk_fma_f32 v[82:83],v[82:83],v[208:209],v[214:215] op_sel:[0,1,1] op_sel_hi:[1,1,1]\n"
"v_pk_fma_f32 v[84:85],v[84:85],v[208:209],v[214:215] op_sel:[0,1,1] op_sel_hi:[1,1,1]\n"
"v_pk_fma_f32 v[86:87],v[86:87],v[208:209],v[214:215] op_sel:[0,1,1] op_sel_hi:[1,1,1]\n"
LD "v193,s[48:51],s80 offen offset:1024 lds\n"
CV "v116,v80,v81\n"
CV "v117,v82,v83\n"
CV "v118,v84,v85\n"
CV "v119,v86,v87\n"
MF "a[212:215],v[176:179],v[104:107],a[212:215]\n"
AW "a224,v66\n"
AW "a225,v67\n"
AW "a226,v68\n"
AW "a227,v69\n"
AW "a204,v116\n"
AW "a205,v117\n"
AW "a206,v118\n"
AW "a207,v119\n"
MF "a[32:35],v[140:143],v[104:107],a[32:35]\n"
LD "v194,s[48:51],s80 offen offset:2048 lds\n"
AR "v72,a208\n"
AR "v73,a209\n"
AR "v74,a210\n"
MF "a[232:235],v[180:183],v[104:107],a[232:235]\n"
AR "v75,a211\n"
AR "v76,a228\n"
AR "v77,a229\n"
AR "v78,a230\n"
AR "v79,a231\n"
"v_pk_fma_f32 v[72:73],v[72:73],v[210:211],v[216:217] op_sel:[0,0,0] op_sel_hi:[1,0,0]\n"
"v_pk_fma_f32 v[74:75],v[74:75],v[210:211],v[216:217] op_sel:[0,0,0] op_sel_hi:[1,0,0]\n"
"v_pk_fma_f32 v[76:77],v[76:77],v[210:211],v[216:217] op_sel:[0,0,0] op_sel_hi:[1,0,0]\n"
"v_pk_fma_f32 v[78:79],v[78:79],v[210:211],v[216:217] op_sel:[0,0,0] op_sel_hi:[1,0,0]\n"
CV "v112,v72,v73\n"
CV "v113,v74,v75\n"
CV "v114,v76,v77\n"
CV "v115,v78,v79\n"
MF "a[216:219],v[176:179],v[108:111],a[216:219]\n"
AW "a228,v70\n"
AW "a229,v71\n"
AW "a230,v132\n"
AW "a231,v133\n"
AW "a208,v112\n"
AW "a209,v113\n"
AW "a210,v114\n"
AW "a211,v115\n"
MF "a[52:55],v[144:147],v[104:107],a[52:55]\n"
LD "v195,s[48:51],s80 offen offset:3072 lds\n"
AR "v80,a212\n"
AR "v81,a213\n"
AR "v82,a214\n"
MF "a[236:239],v[180:183],v[108:111],a[236:239]\n"
AR "v83,a215\n"
AR "v84,a232\n"
AR "v85,a233\n"
AR "v86,a234\n"
AR "v87,a235\n"
"v_pk_fma_f32 v[80:81],v[80:81],v[210:211],v[216:217] op_sel:[0,1,1] op_sel_hi:[1,1,1]\n"
"v_pk_fma_f32 v[82:83],v[82:83],v[210:211],v[216:217] op_sel:[0,1,1] op_sel_hi:[1,1,1]\n"
"v_pk_fma_f32 v[84:85],v[84:85],v[210:211],v[216:217] op_sel:[0,1,1] op_sel_hi:[1,1,1]\n"
"v_pk_fma_f32 v[86:87],v[86:87],v[210:211],v[216:217] op_sel:[0,1,1] op_sel_hi:[1,1,1]\n"
CV "v116,v80,v81\n"
CV "v117,v82,v83\n"
CV "v118,v84,v85\n"
CV "v119,v86,v87\n"
MF "a[0:3],v[136:139],v[92:95],a[0:3]\n"
AW "a232,v134\n"
AW "a233,v135\n"
AW "a234,v6\n"
AW "a235,v13\n"
AW "a212,v116\n"
AW "a213,v117\n"
AW "a214,v118\n"
AW "a215,v119\n"
MF "a[72:75],v[148:151],v[104:107],a[72:75]\n"
"s_add_u32 m0,s73,77824\n"
"s_nop 0\n"
LD "v196,s[48:51],s80 offen lds\n"
AR "v72,a216\n"
AR "v73,a217\n"
AR "v74,a218\n"
MF "a[20:23],v[140:143],v[92:95],a[20:23]\n"
AR "v75,a219\n"
AR "v76,a236\n"
AR "v77,a237\n"
"s_nop 0\n"
AR "v78,a238\n"
AR "v79,a239\n"
"v_pk_fma_f32 v[72:73],v[72:73],v[212:213],v[218:219] op_sel:[0,0,0] op_sel_hi:[1,0,0]\n"
"v_pk_fma_f32 v[74:75],v[74:75],v[212:213],v[218:219] op_sel:[0,0,0] op_sel_hi:[1,0,0]\n"
"v_pk_fma_f32 v[76:77],v[76:77],v[212:213],v[218:219] op_sel:[0,0,0] op_sel_hi:[1,0,0]\n"
"v_pk_fma_f32 v[78:79],v[78:79],v[212:213],v[218:219] op_sel:[0,0,0] op_sel_hi:[1,0,0]\n"
LD "v197,s[48:51],s80 offen offset:1024 lds\n"
CV "v112,v72,v73\n"
CV "v113,v74,v75\n"
CV "v114,v76,v77\n"
CV "v115,v78,v79\n"
MF "a[40:43],v[144:147],v[92:95],a[40:43]\n"
AW "a236,v14\n"
AW "a237,v15\n"
"v_accvgpr_mov_b32 a238,a254\n"
".Lpg21h:\n"
"s_cbranch_execz .Lpg25h\n"
"s_nop 0\n"
"v_accvgpr_mov_b32 a239,a255\n"
AW "a216,v112\n"
AW "a217,v113\n"
AW "a218,v114\n"
AW "a219,v115\n"
MF "a[92:95],v[152:155],v[104:107],a[92:95]\n"
AR "v80,a0\n"
AR "v81,a1\n"
AR "v82,a2\n"
MF "a[60:63],v[148:151],v[92:95],a[60:63]\n"
AR "v83,a3\n"
AR "v84,a20\n"
AR "v85,a21\n"
AR "v86,a22\n"
AR "v87,a23\n"
"v_pk_fma_f32 v[80:81],v[80:81],v[208:209],v[214:215] op_sel:[0,0,0] op_sel_hi:[1,0,0]\n"
"v_pk_fma_f32 v[82:83],v[82:83],v[208:209],v[214:215] op_sel:[0,0,0] op_sel_hi:[1,0,0]\n"
"v_pk_fma_f32 v[84:85],v[84:85],v[208:209],v[214:215] op_sel:[0,0,0] op_sel_hi:[1,0,0]\n"
"v_pk_fma_f32 v[86:87],v[86:87],v[208:209],v[214:215] op_sel:[0,0,0] op_sel_hi:[1,0,0]\n"
CV "v116,v80,v81\n"
CV "v117,v82,v83\n"
CV "v118,v84,v85\n"
CV "v119,v86,v87\n"
MF "a[80:83],v[152:155],v[92:95],a[80:83]\n"
"ds_read_b128 a[20:23],v236 offset:24576\n"
AW "a0,v116\n"
AW "a1,v117\n"
AW "a2,v118\n"
AW "a3,v119\n"
MF "a[112:115],v[156:159],v[104:107],a[112:115]\n"
AR "v72,a40\n"
AR "v73,a41\n"
AR "v74,a42\n"
MF "a[100:103],v[156:159],v[92:95],a[100:103]\n"
AR "v75,a43\n"
AR "v76,a60\n"
AR "v77,a61\n"
AR "v78,a62\n"
AR "v79,a63\n"
"v_pk_fma_f32 v[72:73],v[72:73],v[208:209],v[214:215] op_sel:[0,0,0] op_sel_hi:[1,0,0]\n"
"v_pk_fma_f32 v[74:75],v[74:75],v[208:209],v[214:215] op_sel:[0,0,0] op_sel_hi:[1,0,0]\n"
"v_pk_fma_f32 v[76:77],v[76:77],v[208:209],v[214:215] op_sel:[0,0,0] op_sel_hi:[1,0,0]\n"
"v_pk_fma_f32 v[78:79],v[78:79],v[208:209],v[214:215] op_sel:[0,0,0] op_sel_hi:[1,0,0]\n"
CV "v112,v72,v73\n"
CV "v113,v74,v75\n"
CV "v114,v76,v77\n"
CV "v115,v78,v79\n"
MF "a[4:7],v[136:139],v[96:99],a[4:7]\n"
"ds_read_b128 a[60:63],v236 offset:45056\n"
AW "a40,v112\n"
AW "a41,v113\n"
AW "a42,v114\n"
AW "a43,v115\n"
MF "a[16:19],v[136:139],v[108:111],a[16:19]\n"
AR "v80,a80\n"
AR "v81,a81\n"
AR "v82,a82\n"
MF "a[24:27],v[140:143],v[96:99],a[24:27]\n"
AR "v83,a83\n"
AR "v84,a100\n"
AR "v85,a101\n"
AR "v86,a102\n"
AR "v87,a103\n"
"v_pk_fma_f32 v[80:81],v[80:81],v[208:209],v[214:215] op_sel:[0,0,0] op_sel_hi:[1,0,0]\n"
"v_pk_fma_f32 v[82:83],v[82:83],v[208:209],v[214:215] op_sel:[0,0,0] op_sel_hi:[1,0,0]\n"
"v_pk_fma_f32 v[84:85],v[84:85],v[208:209],v[214:215] op_sel:[0,0,0] op_sel_hi:[1,0,0]\n"
"v_pk_fma_f32 v[86:87],v[86:87],v[208:209],v[214:215] op_sel:[0,0,0] op_sel_hi:[1,0,0]\n"
CV "v116,v80,v81\n"
CV "v117,v82,v83\n"
CV "v118,v84,v85\n"
CV "v119,v86,v87\n"
MF "a[36:39],v[140:143],v[108:111],a[36:39]\n"
"ds_read_b128 a[100:103],v237\n"
AW "a80,v116\n"
AW "a81,v117\n"
AW "a82,v118\n"
AW "a83,v119\n"
MF "a[56:59],v[144:147],v[108:111],a[56:59]\n"
AR "v72,a4\n"
AR "v73,a5\n"
AR "v74,a6\n"
MF "a[76:79],v[148:151],v[108:111],a[76:79]\n"
AR "v75,a7\n"
AR "v76,a24\n"
AR "v77,a25\n"
AR "v78,a26\n"
AR "v79,a27\n"
"v_pk_fma_f32 v[72:73],v[72:73],v[208:209],v[214:215] op_sel:[0,1,1] op_sel_hi:[1,1,1]\n"
"v_pk_fma_f32 v[74:75],v[74:75],v[208:209],v[214:215] op_sel:[0,1,1] op_sel_hi:[1,1,1]\n"
"v_pk_fma_f32 v[76:77],v[76:77],v[208:209],v[214:215] op_sel:[0,1,1] op_sel_hi:[1,1,1]\n"
"v_pk_fma_f32 v[78:79],v[78:79],v[208:209],v[214:215] op_sel:[0,1,1] op_sel_hi:[1,1,1]\n"
CV "v112,v72,v73\n"
CV "v113,v74,v75\n"
CV "v114,v76,v77\n"
CV "v115,v78,v79\n"
MF "a[96:99],v[152:155],v[108:111],a[96:99]\n"
"ds_read_b128 a[24:27],v236 offset:28672\n"
AW "a4,v112\n"
AW "a5,v113\n"
AW "a6,v114\n"
AW "a7,v115\n"
MF "a[116:119],v[156:159],v[108:111],a[116:119]\n"
"v_and_b32 v0,512,v9\n"
"v_lshlrev_b32 v0,2,v0\n"
"v_sub_u32 v0,v2,v0\n"
"s_bfe_u32 s58,s22,0x10014\n"
"s_lshl_b32 s58,s58,5\n"
"v_xor_b32 v0,s58,v0\n"
"s_and_b32 s58,s56,1\n"
"s_lshl_b32 s58,s58,10\n"
"s_lshr_b32 s59,s56,1\n"
"s_lshl_b32 s59,s59,8\n"
"s_add_u32 s58,s58,s59\n"
"s_add_u32 s58,s58,159744\n"
"v_mov_b32 v252,s58\n"
"v_xor_b32 v1,32,v0\n"
"v_xor_b32 v2,64,v0\n"
"v_xor_b32 v3,96,v0\n"
"v_add_u32 v4,73728,v0\n"
"v_add_u32 v5,73728,v1\n"
"v_add_u32 v6,73728,v2\n"
"v_add_u32 v7,73728,v3\n"
"s_waitcnt vmcnt(12) lgkmcnt(0)\n"
"s_barrier\n"
"ds_read_b128 v[252:255],v252\n"
TR "v[176:177],v0 offset:24576\n"
TR "v[178:179],v0 offset:26624\n"
TR "v[180:181],v0 offset:28672\n"
TR "v[182:183],v0 offset:30720\n"
TR "v[184:185],v0 offset:32768\n"
TR "v[186:187],v0 offset:34816\n"
"s_waitcnt lgkmcnt(0)\n"
"s_cmp_ge_u32 s56,2\n"
"s_cbranch_scc1 .Lrole1\n"
MF "v[16:19],a[120:123],v[176:179],v[252:255]\n"
"s_mov_b32 s65,128\n"
TR "v[188:189],v1 offset:24576\n"
AR "v220,a44\n"
MF "v[16:19],a[160:163],v[180:183],v[16:19]\n"
"s_mov_b32 s9,0x3f80\n"
TR "v[190:191],v1 offset:26624\n"
AR "v221,a45\n"
MF "v[16:19],a[200:203],v[184:187],v[16:19]\n"
"s_add_u32 s89,s89,s65\n"
AR "v222,a46\n"
AR "v223,a47\n"
MF "v[24:27],a[204:207],v[184:187],v[252:255]\n"
"s_bfe_u32 s58,s22,0x30014\n"
TR "a[240:241],v1 offset:28672\n"
AR "v224,a64\n"
MF "v[24:27],a[164:167],v[180:183],v[24:27]\n"
"s_add_u32 s61,s58,1\n"
TR "a[242:243],v1 offset:30720\n"
AR "v225,a65\n"
MF "v[24:27],a[124:127],v[176:179],v[24:27]\n"
"s_and_b32 s61,s61,7\n"
AR "v226,a66\n"
AR "v227,a67\n"
MF "v[32:35],a[128:131],v[176:179],v[252:255]\n"
"s_mov_b32 s64,9\n"
TR "a[244:245],v1 offset:32768\n"
"ds_read_b128 a[64:67],v236 offset:49152\n"
MF "v[32:35],a[168:171],v[180:183],v[32:35]\n"
"s_xor_b32 s67,s78,8\n"
TR "a[246:247],v1 offset:34816\n"
"v_fma_f32 v220,v220,v209,v215\n"
MF "v[32:35],a[208:211],v[184:187],v[32:35]\n"
"s_mov_b32 s71,204800\n"
"v_fma_f32 v221,v221,v209,v215\n"
"v_fma_f32 v222,v222,v209,v215\n"
MF "v[40:43],a[212:215],v[184:187],v[252:255]\n"
"s_mov_b32 s66,204800\n"
"v_fma_f32 v223,v223,v209,v215\n"
"v_fma_f32 v224,v224,v209,v215\n"
MF "v[40:43],a[172:175],v[180:183],v[40:43]\n"
"s_and_b32 s73,s56,1\n"
"v_fma_f32 v225,v225,v209,v215\n"
"v_fma_f32 v226,v226,v209,v215\n"
MF "v[40:43],a[132:135],v[176:179],v[40:43]\n"
"s_mul_i32 s73,s73,5120\n"
"v_fma_f32 v227,v227,v209,v215\n"
CV "v220,v220,v221\n"
MF "v[48:51],a[136:139],v[176:179],v[252:255]\n"
"s_add_u32 s73,s73,s93\n"
CV "v221,v222,v223\n"
CV "v222,v224,v225\n"
MF "v[48:51],a[176:179],v[180:183],v[48:51]\n"
"s_xor_b32 s58,s58,4\n"
CV "v223,v226,v227\n"
AW "a44,v220\n"
MF "v[48:51],a[216:219],v[184:187],v[48:51]\n"
"s_waitcnt lgkmcnt(0)\n"
AW "a45,v221\n"
AW "a46,v222\n"
MF "v[52:55],a[216:219],a[244:247],v[252:255]\n"
"s_mul_i32 s70,s58,204800\n"
TR "v[176:177],v2 offset:24576\n"
AW "a47,v223\n"
MF "v[52:55],a[176:179],a[240:243],v[52:55]\n"
"s_add_u32 s70,s70,s73\n"
TR "v[178:179],v2 offset:26624\n"
AR "v228,a84\n"
MF "v[52:55],a[136:139],v[188:191],v[52:55]\n"
"s_add_u32 s70,s70,0xa000\n"
AR "v229,a85\n"
AR "v230,a86\n"
MF "v[44:47],a[132:135],v[188:191],v[252:255]\n"
"s_mov_b32 s85,1\n"
TR "v[180:181],v2 offset:28672\n"
AR "v231,a87\n"
MF "v[44:47],a[172:175],a[240:243],v[44:47]\n"
"s_mov_b32 s83,0xf0000000\n"
TR "v[182:183],v2 offset:30720\n"
AR "v232,a104\n"
MF "v[44:47],a[212:215],a[244:247],v[44:47]\n"
"s_mov_b32 s82,0xf0000000\n"
AR "v233,a105\n"
AR "v234,a106\n"
MF "v[36:39],a[208:211],a[244:247],v[252:255]\n"
"s_add_u32 s58,s79,73728\n"
TR "v[184:185],v2 offset:32768\n"
AR "v235,a107\n"
MF "v[36:39],a[168:171],a[240:243],v[36:39]\n"
"s_mul_i32 s95,s92,3277\n"
TR "v[186:187],v2 offset:34816\n"
"ds_read_b128 a[104:107],v237 offset:4096\n"
MF "v[36:39],a[128:131],v[188:191],v[36:39]\n"
"s_lshr_b32 s95,s95,16\n"
"v_fma_f32 v228,v228,v209,v215\n"
"v_fma_f32 v229,v229,v209,v215\n"
MF "v[28:31],a[124:127],v[188:191],v[252:255]\n"
"s_mul_i32 s59,s95,20\n"
"v_fma_f32 v230,v230,v209,v215\n"
"v_fma_f32 v231,v231,v209,v215\n"
MF "v[28:31],a[164:167],a[240:243],v[28:31]\n"
"s_sub_u32 s59,s92,s59\n"
"v_fma_f32 v232,v232,v209,v215\n"
"v_fma_f32 v233,v233,v209,v215\n"
MF "v[28:31],a[204:207],a[244:247],v[28:31]\n"
"s_cmp_ge_u32 s59,10\n"
"s_cselect_b32 s96,4294557760,0\n"
"v_fma_f32 v234,v234,v209,v215\n"
MF "v[20:23],a[200:203],a[244:247],v[252:255]\n"
"s_add_u32 s96,s96,1024\n"
"v_fma_f32 v235,v235,v209,v215\n"
CV "v228,v228,v229\n"
MF "v[20:23],a[160:163],a[240:243],v[20:23]\n"
"s_mul_i32 s59,s59,40960\n"
CV "v229,v230,v231\n"
CV "v230,v232,v233\n"
MF "v[20:23],a[120:123],v[188:191],v[20:23]\n"
"s_waitcnt lgkmcnt(1)\n"
CV "v231,v234,v235\n"
AW "a84,v228\n"
MF "v[56:59],a[120:123],v[176:179],v[252:255]\n"
"s_add_u32 s96,s96,s59\n"
TR "v[188:189],v3 offset:24576\n"
AW "a85,v229\n"
MF "v[56:59],a[160:163],v[180:183],v[56:59]\n"
"s_lshl_b32 s59,s95,7\n"
TR "v[190:191],v3 offset:26624\n"
AW "a86,v230\n"
MF "v[56:59],a[200:203],v[184:187],v[56:59]\n"
"s_add_u32 s96,s96,s59\n"
AW "a87,v231\n"
AR "v220,a8\n"
MF "v[64:67],a[204:207],v[184:187],v[252:255]\n"
"s_lshl_b32 s2,s92,10\n"
TR "a[240:241],v3 offset:28672\n"
AR "v221,a9\n"
MF "v[64:67],a[164:167],v[180:183],v[64:67]\n"
"s_add_u32 s2,s2,s93\n"
TR "a[242:243],v3 offset:30720\n"
AR "v222,a10\n"
MF "v[64:67],a[124:127],v[176:179],v[64:67]\n"
"s_mov_b32 s0,s96\n"
AR "v223,a11\n"
AR "v224,a28\n"
MF "v[72:75],a[128:131],v[176:179],v[252:255]\n"
"s_xor_b32 s92,s92,32\n"
TR "a[244:245],v3 offset:32768\n"
AR "v225,a29\n"
MF "v[72:75],a[168:171],v[180:183],v[72:75]\n"
"s_mul_i32 s95,s92,3277\n"
TR "a[246:247],v3 offset:34816\n"
AR "v226,a30\n"
MF "v[72:75],a[208:211],v[184:187],v[72:75]\n"
"s_lshr_b32 s95,s95,16\n"
AR "v227,a31\n"
"v_fma_f32 v220,v220,v210,v216\n"
MF "v[80:83],a[212:215],v[184:187],v[252:255]\n"
"s_mul_i32 s59,s95,20\n"
"ds_read_b128 a[28:31],v236 offset:32768\n"
"v_fma_f32 v221,v221,v210,v216\n"
MF "v[80:83],a[172:175],v[180:183],v[80:83]\n"
"s_sub_u32 s59,s92,s59\n"
"v_fma_f32 v222,v222,v210,v216\n"
"v_fma_f32 v223,v223,v210,v216\n"
MF "v[80:83],a[132:135],v[176:179],v[80:83]\n"
"s_cmp_ge_u32 s59,10\n"
"s_cselect_b32 s96,4294557760,0\n"
"v_fma_f32 v224,v224,v210,v216\n"
MF "v[88:91],a[136:139],v[176:179],v[252:255]\n"
"s_add_u32 s96,s96,1024\n"
"v_fma_f32 v225,v225,v210,v216\n"
"v_fma_f32 v226,v226,v210,v216\n"
MF "v[88:91],a[176:179],v[180:183],v[88:91]\n"
"s_mul_i32 s59,s59,40960\n"
"v_fma_f32 v227,v227,v210,v216\n"
CV "v220,v220,v221\n"
MF "v[88:91],a[216:219],v[184:187],v[88:91]\n"
"s_waitcnt vmcnt(0) lgkmcnt(0)\n"
"s_barrier\n"
MF "v[92:95],a[216:219],a[244:247],v[252:255]\n"
"s_add_u32 s96,s96,s59\n"
TR "v[176:177],v0\n"
CV "v221,v222,v223\n"
MF "v[92:95],a[176:179],a[240:243],v[92:95]\n"
"s_lshl_b32 s59,s95,7\n"
TR "v[178:179],v0 offset:2048\n"
CV "v222,v224,v225\n"
MF "v[92:95],a[136:139],v[188:191],v[92:95]\n"
"s_add_u32 s96,s96,s59\n"
TR "v[96:97],v0 offset:49152\n"
CV "v223,v226,v227\n"
MF "v[84:87],a[132:135],v[188:191],v[252:255]\n"
"s_lshl_b32 s3,s92,10\n"
TR "v[180:181],v0 offset:4096\n"
AW "a8,v220\n"
MF "v[84:87],a[172:175],a[240:243],v[84:87]\n"
"s_add_u32 s3,s3,s93\n"
TR "v[182:183],v0 offset:6144\n"
AW "a9,v221\n"
MF "v[84:87],a[212:215],a[244:247],v[84:87]\n"
"s_cmp_lt_u32 s92,40\n"
"s_cselect_b32 s3,s3,0xf0000000\n"
TR "v[98:99],v0 offset:51200\n"
MF "v[76:79],a[208:211],a[244:247],v[252:255]\n"
"s_cmp_lt_u32 s92,40\n"
"s_cselect_b32 s1,s96,0xf0000000\n"
TR "v[184:185],v0 offset:8192\n"
MF "v[76:79],a[168:171],a[240:243],v[76:79]\n"
"s_xor_b32 s92,s92,32\n"
TR "v[186:187],v0 offset:10240\n"
AW "a10,v222\n"
MF "v[76:79],a[128:131],v[188:191],v[76:79]\n"
TR "v[100:101],v0 offset:53248\n"
AW "a11,v223\n"
MF "v[68:71],a[124:127],v[188:191],v[252:255]\n"
TR "v[102:103],v0 offset:55296\n"
AR "v228,a48\n"
MF "v[68:71],a[164:167],a[240:243],v[68:71]\n"
TR "v[104:105],v0 offset:57344\n"
AR "v229,a49\n"
MF "v[68:71],a[204:207],a[244:247],v[68:71]\n"
TR "v[106:107],v0 offset:59392\n"
AR "v230,a50\n"
MF "v[60:63],a[200:203],a[244:247],v[252:255]\n"
TR "v[108:109],v4\n"
AR "v231,a51\n"
MF "v[60:63],a[160:163],a[240:243],v[60:63]\n"
TR "v[110:111],v4 offset:2048\n"
AR "v232,a68\n"
MF "v[60:63],a[120:123],v[188:191],v[60:63]\n"
"s_waitcnt lgkmcnt(6)\n"
TR "v[112:113],v4 offset:4096\n"
AR "v233,a69\n"
MF "v[16:19],a[140:143],v[176:179],v[16:19]\n"
TR "v[188:189],v1\n"
AR "v234,a70\n"
MF "v[16:19],a[180:183],v[180:183],v[16:19]\n"
TR "v[190:191],v1 offset:2048\n"
AR "v235,a71\n"
MF "v[16:19],a[220:223],v[184:187],v[16:19]\n"
"s_nop 0\n"
TR "v[114:115],v4 offset:6144\n"
"ds_read_b128 a[68:71],v236 offset:53248\n"
MF "v[24:27],a[224:227],v[184:187],v[24:27]\n"
TR "a[240:241],v1 offset:4096\n"
"v_fma_f32 v228,v228,v210,v216\n"
MF "v[24:27],a[184:187],v[180:183],v[24:27]\n"
TR "a[242:243],v1 offset:6144\n"
"v_fma_f32 v229,v229,v210,v216\n"
MF "v[24:27],a[144:147],v[176:179],v[24:27]\n"
TR "v[116:117],v4 offset:8192\n"
"v_fma_f32 v230,v230,v210,v216\n"
MF "v[32:35],a[148:151],v[176:179],v[32:35]\n"
TR "a[244:245],v1 offset:8192\n"
"v_fma_f32 v231,v231,v210,v216\n"
MF "v[32:35],a[188:191],v[180:183],v[32:35]\n"
TR "a[246:247],v1 offset:10240\n"
"v_fma_f32 v232,v232,v210,v216\n"
MF "v[32:35],a[228:231],v[184:187],v[32:35]\n"
TR "v[118:119],v4 offset:10240\n"
"v_fma_f32 v233,v233,v210,v216\n"
MF "v[40:43],a[232:235],v[184:187],v[40:43]\n"
TR "v[120:121],v1 offset:49152\n"
"v_fma_f32 v234,v234,v210,v216\n"
MF "v[40:43],a[192:195],v[180:183],v[40:43]\n"
TR "v[122:123],v1 offset:51200\n"
"v_fma_f32 v235,v235,v210,v216\n"
MF "v[40:43],a[152:155],v[176:179],v[40:43]\n"
TR "v[124:125],v1 offset:53248\n"
CV "v228,v228,v229\n"
MF "v[48:51],a[156:159],v[176:179],v[48:51]\n"
TR "v[126:127],v1 offset:55296\n"
CV "v229,v230,v231\n"
MF "v[48:51],a[196:199],v[180:183],v[48:51]\n"
TR "v[128:129],v1 offset:57344\n"
CV "v230,v232,v233\n"
MF "v[48:51],a[236:239],v[184:187],v[48:51]\n"
"s_waitcnt lgkmcnt(6)\n"
TR "v[130:131],v1 offset:59392\n"
CV "v231,v234,v235\n"
MF "v[52:55],a[236:239],a[244:247],v[52:55]\n"
TR "v[176:177],v2\n"
AW "a48,v228\n"
MF "v[52:55],a[196:199],a[240:243],v[52:55]\n"
TR "v[178:179],v2 offset:2048\n"
AW "a49,v229\n"
MF "v[52:55],a[156:159],v[188:191],v[52:55]\n"
TR "v[132:133],v5\n"
AW "a50,v230\n"
MF "v[44:47],a[152:155],v[188:191],v[44:47]\n"
TR "v[180:181],v2 offset:4096\n"
AW "a51,v231\n"
MF "v[44:47],a[192:195],a[240:243],v[44:47]\n"
TR "v[182:183],v2 offset:6144\n"
AR "v220,a88\n"
MF "v[44:47],a[232:235],a[244:247],v[44:47]\n"
TR "v[134:135],v5 offset:2048\n"
AR "v221,a89\n"
MF "v[36:39],a[228:231],a[244:247],v[36:39]\n"
TR "v[184:185],v2 offset:8192\n"
AR "v222,a90\n"
MF "v[36:39],a[188:191],a[240:243],v[36:39]\n"
TR "v[186:187],v2 offset:10240\n"
AR "v223,a91\n"
MF "v[36:39],a[148:151],v[188:191],v[36:39]\n"
TR "v[136:137],v5 offset:4096\n"
AR "v224,a108\n"
MF "v[28:31],a[144:147],v[188:191],v[28:31]\n"
TR "v[138:139],v5 offset:6144\n"
AR "v225,a109\n"
MF "v[28:31],a[184:187],a[240:243],v[28:31]\n"
TR "v[140:141],v5 offset:8192\n"
AR "v226,a110\n"
MF "v[28:31],a[224:227],a[244:247],v[28:31]\n"
TR "v[142:143],v5 offset:10240\n"
AR "v227,a111\n"
MF "v[20:23],a[220:223],a[244:247],v[20:23]\n"
"s_nop 0\n"
TR "v[144:145],v2 offset:49152\n"
"ds_read_b128 a[108:111],v237 offset:8192\n"
MF "v[20:23],a[180:183],a[240:243],v[20:23]\n"
TR "v[146:147],v2 offset:51200\n"
"v_fma_f32 v220,v220,v210,v216\n"
MF "v[20:23],a[140:143],v[188:191],v[20:23]\n"
"s_waitcnt lgkmcnt(7)\n"
TR "v[148:149],v2 offset:53248\n"
"v_fma_f32 v221,v221,v210,v216\n"
MF "v[56:59],a[140:143],v[176:179],v[56:59]\n"
TR "v[188:189],v3\n"
"v_add_u32 v198,59392,v192\n"
MF "v[56:59],a[180:183],v[180:183],v[56:59]\n"
TR "v[190:191],v3 offset:2048\n"
"v_add_u32 v199,68608,v192\n"
MF "v[56:59],a[220:223],v[184:187],v[56:59]\n"
TR "v[150:151],v2 offset:55296\n"
"v_add_u32 v200,81920,v192\n"
"v_add_u32 v201,91136,v192\n"
MF "v[64:67],a[224:227],v[184:187],v[64:67]\n"
TR "a[240:241],v3 offset:4096\n"
"v_add_u32 v202,100352,v192\n"
MF "v[64:67],a[184:187],v[180:183],v[64:67]\n"
TR "a[242:243],v3 offset:6144\n"
"v_add_u32 v203,109568,v192\n"
MF "v[64:67],a[144:147],v[176:179],v[64:67]\n"
TR "v[152:153],v2 offset:57344\n"
"v_fma_f32 v222,v222,v210,v216\n"
MF "v[72:75],a[148:151],v[176:179],v[72:75]\n"
TR "a[244:245],v3 offset:8192\n"
"v_fma_f32 v223,v223,v210,v216\n"
MF "v[72:75],a[188:191],v[180:183],v[72:75]\n"
TR "a[246:247],v3 offset:10240\n"
"v_fma_f32 v224,v224,v210,v216\n"
MF "v[72:75],a[228:231],v[184:187],v[72:75]\n"
TR "v[154:155],v2 offset:59392\n"
"v_fma_f32 v225,v225,v210,v216\n"
MF "v[80:83],a[232:235],v[184:187],v[80:83]\n"
TR "v[156:157],v6\n"
"v_fma_f32 v226,v226,v210,v216\n"
MF "v[80:83],a[192:195],v[180:183],v[80:83]\n"
TR "v[158:159],v6 offset:2048\n"
"v_fma_f32 v227,v227,v210,v216\n"
MF "v[80:83],a[152:155],v[176:179],v[80:83]\n"
TR "v[160:161],v6 offset:4096\n"
CV "v220,v220,v221\n"
MF "v[88:91],a[156:159],v[176:179],v[88:91]\n"
TR "v[162:163],v6 offset:6144\n"
CV "v221,v222,v223\n"
MF "v[88:91],a[196:199],v[180:183],v[88:91]\n"
TR "v[164:165],v6 offset:8192\n"
CV "v222,v224,v225\n"
MF "v[88:91],a[236:239],v[184:187],v[88:91]\n"
"s_waitcnt lgkmcnt(0)\n"
"s_barrier\n"
MF "v[92:95],a[236:239],a[244:247],v[92:95]\n"
"s_add_u32 s80,s89,122880\n"
"s_add_u32 m0,s79,0\n"
TR "v[166:167],v6 offset:10240\n"
MF "v[92:95],a[196:199],a[240:243],v[92:95]\n"
LD "v192,s[48:51],s80 offen lds\n"
MF "v[92:95],a[156:159],v[188:191],v[92:95]\n"
TR "v[168:169],v3 offset:49152\n"
CV "v223,v226,v227\n"
MF "v[84:87],a[152:155],v[188:191],v[84:87]\n"
TR "v[170:171],v3 offset:51200\n"
AW "a88,v220\n"
MF "v[84:87],a[192:195],a[240:243],v[84:87]\n"
TR "v[172:173],v3 offset:53248\n"
AW "a89,v221\n"
MF "v[84:87],a[232:235],a[244:247],v[84:87]\n"
LD "v193,s[48:51],s80 offen offset:1024 lds\n"
MF "v[76:79],a[228:231],a[244:247],v[76:79]\n"
TR "v[174:175],v3 offset:55296\n"
AW "a90,v222\n"
MF "v[76:79],a[188:191],a[240:243],v[76:79]\n"
TR "v[204:205],v3 offset:57344\n"
AW "a91,v223\n"
MF "v[76:79],a[148:151],v[188:191],v[76:79]\n"
TR "v[206:207],v3 offset:59392\n"
AR "v228,a12\n"
MF "v[68:71],a[144:147],v[188:191],v[68:71]\n"
TR "v[244:245],v7\n"
AR "v229,a13\n"
MF "v[68:71],a[184:187],a[240:243],v[68:71]\n"
LD "v194,s[48:51],s80 offen offset:2048 lds\n"
MF "v[68:71],a[224:227],a[244:247],v[68:71]\n"
TR "v[246:247],v7 offset:2048\n"
AR "v230,a14\n"
MF "v[60:63],a[220:223],a[244:247],v[60:63]\n"
".Lpg22h:\n"
"s_cbranch_execz .Lpg26h\n"
TR "v[248:249],v7 offset:4096\n"
AR "v231,a15\n"
MF "v[60:63],a[180:183],a[240:243],v[60:63]\n"
TR "v[250:251],v7 offset:6144\n"
AR "v232,a32\n"
MF "v[60:63],a[140:143],v[188:191],v[60:63]\n"
LD "v195,s[48:51],s80 offen offset:3072 lds\n"
MF "v[16:19],a[0:3],v[96:99],v[16:19]\n"
TR "v[238:239],v7 offset:8192\n"
AR "v233,a33\n"
MF "v[16:19],a[40:43],v[100:103],v[16:19]\n"
TR "v[240:241],v7 offset:10240\n"
AR "v234,a34\n"
MF "v[16:19],a[80:83],v[104:107],v[16:19]\n"
AR "v235,a35\n"
"v_fma_f32 v228,v228,v211,v217\n"
MF "v[16:19],a[20:23],v[108:111],v[16:19]\n"
"s_add_u32 m0,s79,4096\n"
"ds_read_b128 a[32:35],v236 offset:36864\n"
"v_fma_f32 v229,v229,v211,v217\n"
MF "v[16:19],a[60:63],v[112:115],v[16:19]\n"
LD "v196,s[48:51],s80 offen lds\n"
MF "v[16:19],a[100:103],v[116:119],v[16:19]\n"
"v_fma_f32 v230,v230,v211,v217\n"
"v_fma_f32 v231,v231,v211,v217\n"
MF "v[20:23],a[100:103],v[140:143],v[20:23]\n"
"v_fma_f32 v232,v232,v211,v217\n"
"v_fma_f32 v233,v233,v211,v217\n"
MF "v[20:23],a[60:63],v[136:139],v[20:23]\n"
"v_fma_f32 v234,v234,v211,v217\n"
"v_fma_f32 v235,v235,v211,v217\n"
MF "v[20:23],a[20:23],v[132:135],v[20:23]\n"
LD "v197,s[48:51],s80 offen offset:1024 lds\n"
MF "v[20:23],a[80:83],v[128:131],v[20:23]\n"
CV "v228,v228,v229\n"
CV "v229,v230,v231\n"
MF "v[20:23],a[40:43],v[124:127],v[20:23]\n"
CV "v230,v232,v233\n"
CV "v231,v234,v235\n"
MF "v[20:23],a[0:3],v[120:123],v[20:23]\n"
"s_waitcnt lgkmcnt(13)\n"
AW "a12,v228\n"
AW "a13,v229\n"
MF "v[56:59],a[0:3],v[144:147],v[56:59]\n"
AW "a14,v230\n"
AW "a15,v231\n"
MF "v[56:59],a[40:43],v[148:151],v[56:59]\n"
LD "v198,s[48:51],s80 offen offset:2048 lds\n"
MF "v[56:59],a[80:83],v[152:155],v[56:59]\n"
AR "v220,a52\n"
AR "v221,a53\n"
MF "v[56:59],a[20:23],v[156:159],v[56:59]\n"
AR "v222,a54\n"
AR "v223,a55\n"
MF "v[56:59],a[60:63],v[160:163],v[56:59]\n"
AR "v224,a72\n"
AR "v225,a73\n"
MF "v[56:59],a[100:103],v[164:167],v[56:59]\n"
"s_waitcnt lgkmcnt(1)\n"
AR "v226,a74\n"
AR "v227,a75\n"
MF "v[60:63],a[100:103],v[238:241],v[60:63]\n"
LD "v199,s[48:51],s80 offen offset:3072 lds\n"
MF "v[60:63],a[60:63],v[248:251],v[60:63]\n"
"ds_read_b128 a[72:75],v236 offset:57344\n"
"v_fma_f32 v220,v220,v211,v217\n"
MF "v[60:63],a[20:23],v[244:247],v[60:63]\n"
"v_fma_f32 v221,v221,v211,v217\n"
"v_fma_f32 v222,v222,v211,v217\n"
MF "v[60:63],a[80:83],v[204:207],v[60:63]\n"
"s_add_u32 m0,s79,8192\n"
"v_fma_f32 v223,v223,v211,v217\n"
"v_fma_f32 v224,v224,v211,v217\n"
MF "v[60:63],a[40:43],v[172:175],v[60:63]\n"
LD "v200,s[48:51],s80 offen lds\n"
MF "v[60:63],a[0:3],v[168:171],v[60:63]\n"
"v_fma_f32 v225,v225,v211,v217\n"
"v_fma_f32 v226,v226,v211,v217\n"
MF "v[68:71],a[4:7],v[168:171],v[68:71]\n"
"v_fma_f32 v227,v227,v211,v217\n"
CV "v220,v220,v221\n"
MF "v[68:71],a[44:47],v[172:175],v[68:71]\n"
CV "v221,v222,v223\n"
CV "v222,v224,v225\n"
MF "v[68:71],a[84:87],v[204:207],v[68:71]\n"
CV "v223,v226,v227\n"
AW "a52,v220\n"
MF "v[68:71],a[24:27],v[244:247],v[68:71]\n"
LD "v201,s[48:51],s80 offen offset:1024 lds\n"
MF "v[68:71],a[64:67],v[248:251],v[68:71]\n"
AW "a53,v221\n"
AW "a54,v222\n"
MF "v[68:71],a[104:107],v[238:241],v[68:71]\n"
AW "a55,v223\n"
AR "v228,a92\n"
MF "v[64:67],a[104:107],v[164:167],v[64:67]\n"
"s_waitcnt lgkmcnt(0)\n"
"s_barrier\n"
MF "v[64:67],a[64:67],v[160:163],v[64:67]\n"
LD "v202,s[48:51],s80 offen offset:2048 lds\n"
MF "v[64:67],a[24:27],v[156:159],v[64:67]\n"
AR "v229,a93\n"
AR "v230,a94\n"
MF "v[64:67],a[84:87],v[152:155],v[64:67]\n"
AR "v231,a95\n"
AR "v232,a112\n"
MF "v[64:67],a[44:47],v[148:151],v[64:67]\n"
AR "v233,a113\n"
AR "v234,a114\n"
MF "v[64:67],a[4:7],v[144:147],v[64:67]\n"
AR "v235,a115\n"
"v_fma_f32 v228,v228,v211,v217\n"
MF "v[28:31],a[4:7],v[120:123],v[28:31]\n"
LD "v203,s[48:51],s80 offen offset:3072 lds\n"
MF "v[28:31],a[44:47],v[124:127],v[28:31]\n"
"ds_read_b128 a[112:115],v237 offset:12288\n"
"v_fma_f32 v229,v229,v211,v217\n"
MF "v[28:31],a[84:87],v[128:131],v[28:31]\n"
"v_fma_f32 v230,v230,v211,v217\n"
"v_fma_f32 v231,v231,v211,v217\n"
MF "v[28:31],a[24:27],v[132:135],v[28:31]\n"
"v_fma_f32 v232,v232,v211,v217\n"
"v_fma_f32 v233,v233,v211,v217\n"
MF "v[28:31],a[64:67],v[136:139],v[28:31]\n"
"v_fma_f32 v234,v234,v211,v217\n"
"v_fma_f32 v235,v235,v211,v217\n"
MF "v[28:31],a[104:107],v[140:143],v[28:31]\n"
"s_add_u32 s88,s88,s65\n"
"s_add_u32 m0,s79,49152\n"
CV "v228,v228,v229\n"
MF "v[24:27],a[104:107],v[116:119],v[24:27]\n"
LD "v192,s[48:51],s88 offen lds\n"
MF "v[24:27],a[64:67],v[112:115],v[24:27]\n"
CV "v229,v230,v231\n"
CV "v230,v232,v233\n"
MF "v[24:27],a[24:27],v[108:111],v[24:27]\n"
CV "v231,v234,v235\n"
AW "a92,v228\n"
MF "v[24:27],a[84:87],v[104:107],v[24:27]\n"
AW "a93,v229\n"
AW "a94,v230\n"
MF "v[24:27],a[44:47],v[100:103],v[24:27]\n"
LD "v193,s[48:51],s88 offen offset:1024 lds\n"
MF "v[24:27],a[4:7],v[96:99],v[24:27]\n"
AW "a95,v231\n"
AR "v220,a16\n"
MF "v[32:35],a[8:11],v[96:99],v[32:35]\n"
AR "v221,a17\n"
AR "v222,a18\n"
MF "v[32:35],a[48:51],v[100:103],v[32:35]\n"
AR "v223,a19\n"
AR "v224,a36\n"
MF "v[32:35],a[88:91],v[104:107],v[32:35]\n"
AR "v225,a37\n"
AR "v226,a38\n"
MF "v[32:35],a[28:31],v[108:111],v[32:35]\n"
LD "v194,s[48:51],s88 offen offset:2048 lds\n"
MF "v[32:35],a[68:71],v[112:115],v[32:35]\n"
AR "v227,a39\n"
"v_fma_f32 v220,v220,v212,v218\n"
MF "v[32:35],a[108:111],v[116:119],v[32:35]\n"
"ds_read_b128 a[36:39],v236 offset:40960\n"
"v_fma_f32 v221,v221,v212,v218\n"
MF "v[36:39],a[108:111],v[140:143],v[36:39]\n"
"v_fma_f32 v222,v222,v212,v218\n"
"v_fma_f32 v223,v223,v212,v218\n"
MF "v[36:39],a[68:71],v[136:139],v[36:39]\n"
"v_fma_f32 v224,v224,v212,v218\n"
"v_fma_f32 v225,v225,v212,v218\n"
MF "v[36:39],a[28:31],v[132:135],v[36:39]\n"
LD "v195,s[48:51],s88 offen offset:3072 lds\n"
MF "v[36:39],a[88:91],v[128:131],v[36:39]\n"
"v_fma_f32 v226,v226,v212,v218\n"
"v_fma_f32 v227,v227,v212,v218\n"
MF "v[36:39],a[48:51],v[124:127],v[36:39]\n"
CV "v220,v220,v221\n"
CV "v221,v222,v223\n"
MF "v[36:39],a[8:11],v[120:123],v[36:39]\n"
CV "v222,v224,v225\n"
CV "v223,v226,v227\n"
MF "v[72:75],a[8:11],v[144:147],v[72:75]\n"
"s_add_u32 m0,s79,53248\n"
AW "a16,v220\n"
AW "a17,v221\n"
MF "v[72:75],a[48:51],v[148:151],v[72:75]\n"
LD "v196,s[48:51],s88 offen lds\n"
MF "v[72:75],a[88:91],v[152:155],v[72:75]\n"
AW "a18,v222\n"
AW "a19,v223\n"
MF "v[72:75],a[28:31],v[156:159],v[72:75]\n"
AR "v228,a56\n"
AR "v229,a57\n"
MF "v[72:75],a[68:71],v[160:163],v[72:75]\n"
AR "v230,a58\n"
AR "v231,a59\n"
MF "v[72:75],a[108:111],v[164:167],v[72:75]\n"
AR "v232,a76\n"
AR "v233,a77\n"
MF "v[76:79],a[108:111],v[238:241],v[76:79]\n"
LD "v197,s[48:51],s88 offen offset:1024 lds\n"
MF "v[76:79],a[68:71],v[248:251],v[76:79]\n"
AR "v234,a78\n"
AR "v235,a79\n"
MF "v[76:79],a[28:31],v[244:247],v[76:79]\n"
"ds_read_b128 a[76:79],v236 offset:61440\n"
"v_fma_f32 v228,v228,v212,v218\n"
MF "v[76:79],a[88:91],v[204:207],v[76:79]\n"
"v_fma_f32 v229,v229,v212,v218\n"
"v_fma_f32 v230,v230,v212,v218\n"
MF "v[76:79],a[48:51],v[172:175],v[76:79]\n"
"v_fma_f32 v231,v231,v212,v218\n"
"v_fma_f32 v232,v232,v212,v218\n"
MF "v[76:79],a[8:11],v[168:171],v[76:79]\n"
"s_waitcnt lgkmcnt(2)\n"
LD "v198,s[48:51],s88 offen offset:2048 lds\n"
MF "v[84:87],a[12:15],v[168:171],v[84:87]\n"
"v_fma_f32 v233,v233,v212,v218\n"
"v_fma_f32 v234,v234,v212,v218\n"
MF "v[84:87],a[52:55],v[172:175],v[84:87]\n"
"v_fma_f32 v235,v235,v212,v218\n"
CV "v228,v228,v229\n"
MF "v[84:87],a[92:95],v[204:207],v[84:87]\n"
CV "v229,v230,v231\n"
CV "v230,v232,v233\n"
MF "v[84:87],a[32:35],v[244:247],v[84:87]\n"
CV "v231,v234,v235\n"
AW "a56,v228\n"
MF "v[84:87],a[72:75],v[248:251],v[84:87]\n"
LD "v199,s[48:51],s88 offen offset:3072 lds\n"
MF "v[84:87],a[112:115],v[238:241],v[84:87]\n"
AW "a57,v229\n"
AW "a58,v230\n"
MF "v[80:83],a[112:115],v[164:167],v[80:83]\n"
AW "a59,v231\n"
AR "v220,a96\n"
MF "v[80:83],a[72:75],v[160:163],v[80:83]\n"
AR "v221,a97\n"
AR "v222,a98\n"
MF "v[80:83],a[32:35],v[156:159],v[80:83]\n"
"s_add_u32 m0,s79,57344\n"
AR "v223,a99\n"
AR "v224,a116\n"
MF "v[80:83],a[92:95],v[152:155],v[80:83]\n"
LD "v200,s[48:51],s88 offen lds\n"
MF "v[80:83],a[52:55],v[148:151],v[80:83]\n"
AR "v225,a117\n"
AR "v226,a118\n"
MF "v[80:83],a[12:15],v[144:147],v[80:83]\n"
AR "v227,a119\n"
"v_fma_f32 v220,v220,v212,v218\n"
MF "v[44:47],a[12:15],v[120:123],v[44:47]\n"
"ds_read_b128 a[116:119],v237 offset:16384\n"
"v_fma_f32 v221,v221,v212,v218\n"
MF "v[44:47],a[52:55],v[124:127],v[44:47]\n"
"v_fma_f32 v222,v222,v212,v218\n"
"v_fma_f32 v223,v223,v212,v218\n"
MF "v[44:47],a[92:95],v[128:131],v[44:47]\n"
LD "v201,s[48:51],s88 offen offset:1024 lds\n"
MF "v[44:47],a[32:35],v[132:135],v[44:47]\n"
"v_fma_f32 v224,v224,v212,v218\n"
"v_fma_f32 v225,v225,v212,v218\n"
MF "v[44:47],a[72:75],v[136:139],v[44:47]\n"
"v_fma_f32 v226,v226,v212,v218\n"
"v_fma_f32 v227,v227,v212,v218\n"
MF "v[44:47],a[112:115],v[140:143],v[44:47]\n"
CV "v220,v220,v221\n"
CV "v221,v222,v223\n"
MF "v[40:43],a[112:115],v[116:119],v[40:43]\n"
CV "v222,v224,v225\n"
CV "v223,v226,v227\n"
MF "v[40:43],a[72:75],v[112:115],v[40:43]\n"
LD "v202,s[48:51],s88 offen offset:2048 lds\n"
MF "v[40:43],a[32:35],v[108:111],v[40:43]\n"
AW "a96,v220\n"
AW "a97,v221\n"
MF "v[40:43],a[92:95],v[104:107],v[40:43]\n"
AW "a98,v222\n"
AW "a99,v223\n"
MF "v[40:43],a[52:55],v[100:103],v[40:43]\n"
AR "v8,a252\n"
AR "v11,a253\n"
MF "v[40:43],a[12:15],v[96:99],v[40:43]\n"
"s_waitcnt lgkmcnt(0)\n"
"v_bfe_u32 v224,v9,5,3\n"
"v_bfe_u32 v225,v9,8,2\n"
MF "v[48:51],a[16:19],v[96:99],v[48:51]\n"
LD "v203,s[48:51],s88 offen offset:3072 lds\n"
MF "v[48:51],a[56:59],v[100:103],v[48:51]\n"
"v_lshlrev_b32 v225,1,v225\n"
"v_and_b32 v226,16,v9\n"
MF "v[48:51],a[96:99],v[104:107],v[48:51]\n"
"v_lshlrev_b32_e64 v226,v226,s9\n"
"v_sub_u32 v224,v224,v225\n"
MF "v[48:51],a[36:39],v[108:111],v[48:51]\n"
"v_cmp_eq_u32 vcc,0,v224\n"
MF "v[48:51],a[76:79],v[112:115],v[48:51]\n"
"s_nop 0\n"
"v_cndmask_b32 v220,0,v226,vcc\n"
"v_cmp_eq_u32 vcc,1,v224\n"
MF "v[48:51],a[116:119],v[116:119],v[48:51]\n"
"s_nop 0\n"
"v_cndmask_b32 v221,0,v226,vcc\n"
AW "a248,0\n"
MF "v[52:55],a[116:119],v[140:143],v[52:55]\n"
AW "a249,0\n"
AW "a250,v220\n"
MF "v[52:55],a[76:79],v[136:139],v[52:55]\n"
AW "a251,v221\n"
AW "a252,v220\n"
MF "v[52:55],a[36:39],v[132:135],v[52:55]\n"
"s_waitcnt lgkmcnt(0)\n"
"s_barrier\n"
MF "v[52:55],a[96:99],v[128:131],v[52:55]\n"
"s_add_u32 s80,s88,122880\n"
TR "v[176:177],v0 offset:24576\n"
AW "a253,v221\n"
MF "v[52:55],a[56:59],v[124:127],v[52:55]\n"
"s_add_u32 m0,s79,98304\n"
TR "v[178:179],v0 offset:26624\n"
AW "a254,0\n"
MF "v[52:55],a[16:19],v[120:123],v[52:55]\n"
LD "v192,s[48:51],s80 offen lds\n"
MF "v[88:91],a[16:19],v[144:147],v[88:91]\n"
"s_nop 0\n"
TR "v[180:181],v0 offset:28672\n"
"ds_write_addtid_b32 v16 offset:24576\n"
MF "v[88:91],a[56:59],v[148:151],v[88:91]\n"
"s_nop 0\n"
TR "v[182:183],v0 offset:30720\n"
"ds_write_addtid_b32 v17 offset:24832\n"
MF "v[88:91],a[96:99],v[152:155],v[88:91]\n"
LD "v193,s[48:51],s80 offen offset:1024 lds\n"
MF "v[88:91],a[36:39],v[156:159],v[88:91]\n"
"s_nop 0\n"
TR "v[184:185],v0 offset:32768\n"
"ds_write_addtid_b32 v18 offset:25088\n"
MF "v[88:91],a[76:79],v[160:163],v[88:91]\n"
"s_nop 0\n"
TR "v[186:187],v0 offset:34816\n"
"ds_write_addtid_b32 v19 offset:25344\n"
MF "v[88:91],a[116:119],v[164:167],v[88:91]\n"
"s_nop 0\n"
"ds_write_addtid_b32 v20 offset:25600\n"
"ds_write_addtid_b32 v21 offset:25856\n"
MF "v[92:95],a[116:119],v[238:241],v[92:95]\n"
LD "v194,s[48:51],s80 offen offset:2048 lds\n"
MF "v[92:95],a[76:79],v[248:251],v[92:95]\n"
"s_nop 0\n"
"ds_write_addtid_b32 v22 offset:26112\n"
"ds_write_addtid_b32 v23 offset:26368\n"
MF "v[92:95],a[36:39],v[244:247],v[92:95]\n"
"s_nop 0\n"
"ds_write_addtid_b32 v24 offset:26624\n"
"ds_write_addtid_b32 v25 offset:26880\n"
MF "v[92:95],a[96:99],v[204:207],v[92:95]\n"
"s_nop 0\n"
"ds_write_addtid_b32 v26 offset:27136\n"
"ds_write_addtid_b32 v27 offset:27392\n"
MF "v[92:95],a[56:59],v[172:175],v[92:95]\n"
AW "a255,0\n"
"v_bfe_u32 v12,v9,4,4\n"
MF "v[92:95],a[16:19],v[168:171],v[92:95]\n"
"s_waitcnt lgkmcnt(9)\n"
"v_lshlrev_b32 v12,2,v12\n"
"v_lshrrev_b32 v13,2,v9\n"
"v_add_u32_e64 v13,s58,v13\n"
"v_add_u32 v4,24576,v4\n"
"v_add_u32 v5,24576,v5\n"
"v_add_u32 v6,24576,v6\n"
"v_add_u32 v7,24576,v7\n"
".Lpl05:\n"
MF "v[96:99],a[0:3],v[176:179],v[252:255]\n"
"s_nop 0\n"
TR "v[188:189],v1 offset:24576\n"
"ds_write_addtid_b32 v28 offset:27648\n"
MF "v[96:99],a[40:43],v[180:183],v[96:99]\n"
"s_nop 0\n"
TR "v[190:191],v1 offset:26624\n"
"ds_write_addtid_b32 v29 offset:27904\n"
MF "v[96:99],a[80:83],v[184:187],v[96:99]\n"
LD "v195,s[48:51],s80 offen offset:3072 lds\n"
MF "v[104:107],a[84:87],v[184:187],v[252:255]\n"
"s_add_u32 s60,s64,1\n"
TR "a[240:241],v1 offset:28672\n"
"ds_write_addtid_b32 v30 offset:28160\n"
MF "v[104:107],a[44:47],v[180:183],v[104:107]\n"
"s_add_u32 m0,s79,102400\n"
TR "a[242:243],v1 offset:30720\n"
"ds_write_addtid_b32 v31 offset:24320\n"
MF "v[104:107],a[4:7],v[176:179],v[104:107]\n"
LD "v196,s[48:51],s80 offen lds\n"
MF "v[112:115],a[8:11],v[176:179],v[252:255]\n"
"s_cmp_eq_u32 s60,8\n"
"s_cselect_b32 s60,0,s60\n"
TR "a[244:245],v1 offset:32768\n"
MF "v[112:115],a[48:51],v[180:183],v[112:115]\n"
"s_cmp_eq_u32 s60,10\n"
"s_cselect_b32 s60,s61,s60\n"
TR "a[246:247],v1 offset:34816\n"
MF "v[112:115],a[88:91],v[184:187],v[112:115]\n"
"s_sub_u32 s65,s60,s64\n"
"ds_write_addtid_b32 v32 offset:24576\n"
"ds_write_addtid_b32 v33 offset:24832\n"
MF "v[120:123],a[92:95],v[184:187],v[252:255]\n"
"s_lshl_b32 s65,s65,7\n"
"ds_write_addtid_b32 v34 offset:25088\n"
"ds_write_addtid_b32 v35 offset:25344\n"
MF "v[120:123],a[52:55],v[180:183],v[120:123]\n"
LD "v197,s[48:51],s80 offen offset:1024 lds\n"
MF "v[120:123],a[12:15],v[176:179],v[120:123]\n"
"s_mov_b32 s64,s60\n"
"ds_write_addtid_b32 v36 offset:25600\n"
"ds_write_addtid_b32 v37 offset:25856\n"
MF "v[128:131],a[16:19],v[176:179],v[252:255]\n"
"s_nop 0\n"
"ds_write_addtid_b32 v38 offset:26112\n"
"ds_write_addtid_b32 v39 offset:26368\n"
MF "v[128:131],a[56:59],v[180:183],v[128:131]\n"
"s_nop 0\n"
"ds_write_addtid_b32 v40 offset:26624\n"
"ds_write_addtid_b32 v41 offset:26880\n"
MF "v[128:131],a[96:99],v[184:187],v[128:131]\n"
"s_waitcnt lgkmcnt(10)\n"
LD "v198,s[48:51],s80 offen offset:2048 lds\n"
MF "v[132:135],a[96:99],a[244:247],v[252:255]\n"
"s_nop 0\n"
TR "v[176:177],v2 offset:24576\n"
"ds_write_addtid_b32 v42 offset:27136\n"
MF "v[132:135],a[56:59],a[240:243],v[132:135]\n"
"s_nop 0\n"
TR "v[178:179],v2 offset:26624\n"
"ds_write_addtid_b32 v43 offset:27392\n"
MF "v[132:135],a[16:19],v[188:191],v[132:135]\n"
"s_nop 0\n"
"ds_write_addtid_b32 v44 offset:27648\n"
"ds_write_addtid_b32 v45 offset:27904\n"
MF "v[124:127],a[12:15],v[188:191],v[252:255]\n"
"s_nop 0\n"
TR "v[180:181],v2 offset:28672\n"
"ds_write_addtid_b32 v46 offset:28160\n"
MF "v[124:127],a[52:55],a[240:243],v[124:127]\n"
"s_nop 0\n"
TR "v[182:183],v2 offset:30720\n"
"ds_write_addtid_b32 v47 offset:28416\n"
MF "v[124:127],a[92:95],a[244:247],v[124:127]\n"
LD "v199,s[48:51],s80 offen offset:3072 lds\n"
MF "v[116:119],a[88:91],a[244:247],v[252:255]\n"
"s_nop 0\n"
TR "v[184:185],v2 offset:32768\n"
"ds_write_addtid_b32 v48 offset:28672\n"
MF "v[116:119],a[48:51],a[240:243],v[116:119]\n"
"s_add_u32 m0,s79,106496\n"
TR "v[186:187],v2 offset:34816\n"
"ds_write_addtid_b32 v49 offset:24832\n"
MF "v[116:119],a[8:11],v[188:191],v[116:119]\n"
LD "v200,s[48:51],s80 offen lds\n"
MF "v[108:111],a[4:7],v[188:191],v[252:255]\n"
"s_nop 0\n"
"ds_write_addtid_b32 v50 offset:25088\n"
"ds_write_addtid_b32 v51 offset:25344\n"
MF "v[108:111],a[44:47],a[240:243],v[108:111]\n"
"s_nop 0\n"
"ds_write_addtid_b32 v52 offset:25600\n"
"ds_write_addtid_b32 v53 offset:25856\n"
MF "v[108:111],a[84:87],a[244:247],v[108:111]\n"
"s_nop 0\n"
"ds_write_addtid_b32 v54 offset:26112\n"
"ds_write_addtid_b32 v55 offset:26368\n"
MF "v[100:103],a[80:83],a[244:247],v[252:255]\n"
MF "v[100:103],a[40:43],a[240:243],v[100:103]\n"
LD "v201,s[48:51],s80 offen offset:1024 lds\n"
MF "v[100:103],a[0:3],v[188:191],v[100:103]\n"
"s_waitcnt lgkmcnt(7)\n"
MF "v[136:139],a[0:3],v[176:179],v[252:255]\n"
TR "v[188:189],v3 offset:24576\n"
MF "v[136:139],a[40:43],v[180:183],v[136:139]\n"
TR "v[190:191],v3 offset:26624\n"
MF "v[136:139],a[80:83],v[184:187],v[136:139]\n"
LD "v202,s[48:51],s80 offen offset:2048 lds\n"
MF "v[144:147],a[84:87],v[184:187],v[252:255]\n"
TR "a[240:241],v3 offset:28672\n"
MF "v[144:147],a[44:47],v[180:183],v[144:147]\n"
TR "a[242:243],v3 offset:30720\n"
MF "v[144:147],a[4:7],v[176:179],v[144:147]\n"
LD "v203,s[48:51],s80 offen offset:3072 lds\n"
MF "v[152:155],a[8:11],v[176:179],v[252:255]\n"
TR "a[244:245],v3 offset:32768\n"
MF "v[152:155],a[48:51],v[180:183],v[152:155]\n"
TR "a[246:247],v3 offset:34816\n"
MF "v[152:155],a[88:91],v[184:187],v[152:155]\n"
MF "v[160:163],a[92:95],v[184:187],v[252:255]\n"
MF "v[160:163],a[52:55],v[180:183],v[160:163]\n"
MF "v[160:163],a[12:15],v[176:179],v[160:163]\n"
MF "v[168:171],a[16:19],v[176:179],v[252:255]\n"
MF "v[168:171],a[56:59],v[180:183],v[168:171]\n"
MF "v[168:171],a[96:99],v[184:187],v[168:171]\n"
"s_waitcnt vmcnt(24) lgkmcnt(0)\n"
"s_barrier\n"
MF "v[172:175],a[96:99],a[244:247],v[252:255]\n"
"s_add_u32 s89,s89,s65\n"
TR "v[176:177],v0\n"
MF "v[172:175],a[56:59],a[240:243],v[172:175]\n"
"s_add_u32 m0,s79,24576\n"
TR "v[178:179],v0 offset:2048\n"
MF "v[172:175],a[16:19],v[188:191],v[172:175]\n"
LD "v192,s[48:51],s89 offen lds\n"
MF "v[164:167],a[12:15],v[188:191],v[252:255]\n"
TR "v[180:181],v0 offset:4096\n"
MF "v[164:167],a[52:55],a[240:243],v[164:167]\n"
TR "v[182:183],v0 offset:6144\n"
MF "v[164:167],a[92:95],a[244:247],v[164:167]\n"
LD "v193,s[48:51],s89 offen offset:1024 lds\n"
MF "v[156:159],a[88:91],a[244:247],v[252:255]\n"
TR "v[184:185],v0 offset:8192\n"
MF "v[156:159],a[48:51],a[240:243],v[156:159]\n"
"s_nop 0\n"
TR "v[186:187],v0 offset:10240\n"
"ds_write_addtid_b32 v56 offset:49152\n"
MF "v[156:159],a[8:11],v[188:191],v[156:159]\n"
"s_nop 0\n"
"ds_write_addtid_b32 v57 offset:49408\n"
"ds_write_addtid_b32 v58 offset:49664\n"
MF "v[148:151],a[4:7],v[188:191],v[252:255]\n"
"s_nop 0\n"
"ds_write_addtid_b32 v59 offset:49920\n"
"ds_write_addtid_b32 v60 offset:50176\n"
MF "v[148:151],a[44:47],a[240:243],v[148:151]\n"
LD "v194,s[48:51],s89 offen offset:2048 lds\n"
MF "v[148:151],a[84:87],a[244:247],v[148:151]\n"
"s_nop 0\n"
"ds_write_addtid_b32 v61 offset:50432\n"
"ds_write_addtid_b32 v62 offset:50688\n"
MF "v[140:143],a[80:83],a[244:247],v[252:255]\n"
"s_nop 0\n"
"ds_write_addtid_b32 v63 offset:50944\n"
"ds_write_addtid_b32 v64 offset:51200\n"
MF "v[140:143],a[40:43],a[240:243],v[140:143]\n"
"s_nop 0\n"
"ds_write_addtid_b32 v65 offset:51456\n"
"ds_write_addtid_b32 v66 offset:51712\n"
MF "v[140:143],a[0:3],v[188:191],v[140:143]\n"
"s_waitcnt lgkmcnt(11)\n"
LD "v195,s[48:51],s89 offen offset:3072 lds\n"
MF "v[96:99],a[120:123],v[176:179],v[96:99]\n"
".Lpg23h:\n"
"s_cbranch_execz .Lpg27h\n"
"s_nop 0\n"
TR "v[188:189],v1\n"
"ds_write_addtid_b32 v67 offset:51968\n"
MF "v[96:99],a[160:163],v[180:183],v[96:99]\n"
"s_nop 0\n"
TR "v[190:191],v1 offset:2048\n"
"ds_write_addtid_b32 v68 offset:52224\n"
MF "v[96:99],a[200:203],v[184:187],v[96:99]\n"
"s_nop 0\n"
"ds_write_addtid_b32 v69 offset:52480\n"
"ds_write_addtid_b32 v70 offset:52736\n"
MF "v[104:107],a[204:207],v[184:187],v[104:107]\n"
"s_nop 0\n"
TR "a[240:241],v1 offset:4096\n"
"ds_write_addtid_b32 v71 offset:52992\n"
MF "v[104:107],a[164:167],v[180:183],v[104:107]\n"
"s_add_u32 m0,s79,28672\n"
TR "a[242:243],v1 offset:6144\n"
"ds_write_addtid_b32 v72 offset:49152\n"
MF "v[104:107],a[124:127],v[176:179],v[104:107]\n"
LD "v196,s[48:51],s89 offen lds\n"
MF "v[112:115],a[128:131],v[176:179],v[112:115]\n"
"s_nop 0\n"
TR "a[244:245],v1 offset:8192\n"
"ds_write_addtid_b32 v73 offset:49408\n"
MF "v[112:115],a[168:171],v[180:183],v[112:115]\n"
"s_nop 0\n"
TR "a[246:247],v1 offset:10240\n"
"ds_write_addtid_b32 v74 offset:49664\n"
MF "v[112:115],a[208:211],v[184:187],v[112:115]\n"
LD "v197,s[48:51],s89 offen offset:1024 lds\n"
MF "v[120:123],a[212:215],v[184:187],v[120:123]\n"
"s_nop 0\n"
"ds_write_addtid_b32 v75 offset:49920\n"
"ds_write_addtid_b32 v76 offset:50176\n"
MF "v[120:123],a[172:175],v[180:183],v[120:123]\n"
"s_nop 0\n"
"ds_write_addtid_b32 v77 offset:50432\n"
"ds_write_addtid_b32 v78 offset:50688\n"
MF "v[120:123],a[132:135],v[176:179],v[120:123]\n"
"s_nop 0\n"
"ds_write_addtid_b32 v79 offset:50944\n"
"ds_write_addtid_b32 v80 offset:51200\n"
MF "v[128:131],a[136:139],v[176:179],v[128:131]\n"
"s_nop 0\n"
"ds_write_addtid_b32 v81 offset:51456\n"
"ds_write_addtid_b32 v82 offset:51712\n"
MF "v[128:131],a[176:179],v[180:183],v[128:131]\n"
LD "v198,s[48:51],s89 offen offset:2048 lds\n"
MF "v[128:131],a[216:219],v[184:187],v[128:131]\n"
"s_waitcnt lgkmcnt(9)\n"
"ds_write_addtid_b32 v83 offset:51968\n"
"ds_write_addtid_b32 v84 offset:52224\n"
MF "v[132:135],a[216:219],a[244:247],v[132:135]\n"
"s_nop 0\n"
TR "v[176:177],v2\n"
"ds_write_addtid_b32 v85 offset:52480\n"
MF "v[132:135],a[176:179],a[240:243],v[132:135]\n"
"s_nop 0\n"
TR "v[178:179],v2 offset:2048\n"
"ds_write_addtid_b32 v86 offset:52736\n"
MF "v[132:135],a[136:139],v[188:191],v[132:135]\n"
LD "v199,s[48:51],s89 offen offset:3072 lds\n"
MF "v[124:127],a[132:135],v[188:191],v[124:127]\n"
"s_nop 0\n"
TR "v[180:181],v2 offset:4096\n"
"ds_write_addtid_b32 v87 offset:52992\n"
MF "v[124:127],a[172:175],a[240:243],v[124:127]\n"
"s_nop 0\n"
TR "v[182:183],v2 offset:6144\n"
"ds_write_addtid_b32 v88 offset:53248\n"
MF "v[124:127],a[212:215],a[244:247],v[124:127]\n"
"s_nop 0\n"
"ds_write_addtid_b32 v89 offset:53504\n"
"ds_write_addtid_b32 v90 offset:53760\n"
MF "v[116:119],a[208:211],a[244:247],v[116:119]\n"
"s_nop 0\n"
TR "v[184:185],v2 offset:8192\n"
"ds_write_addtid_b32 v91 offset:54016\n"
MF "v[116:119],a[168:171],a[240:243],v[116:119]\n"
"s_add_u32 m0,s79,32768\n"
TR "v[186:187],v2 offset:10240\n"
"ds_write_addtid_b32 v92 offset:50176\n"
MF "v[116:119],a[128:131],v[188:191],v[116:119]\n"
LD "v200,s[48:51],s89 offen lds\n"
MF "v[108:111],a[124:127],v[188:191],v[108:111]\n"
"s_nop 0\n"
"ds_write_addtid_b32 v93 offset:50432\n"
"ds_write_addtid_b32 v94 offset:50688\n"
MF "v[108:111],a[164:167],a[240:243],v[108:111]\n"
"ds_write_addtid_b32 v95 offset:50944\n"
MF "v[108:111],a[204:207],a[244:247],v[108:111]\n"
MF "v[100:103],a[200:203],a[244:247],v[100:103]\n"
LD "v201,s[48:51],s89 offen offset:1024 lds\n"
MF "v[100:103],a[160:163],a[240:243],v[100:103]\n"
MF "v[100:103],a[120:123],v[188:191],v[100:103]\n"
"s_waitcnt lgkmcnt(4)\n"
MF "v[136:139],a[120:123],v[176:179],v[136:139]\n"
TR "v[188:189],v3\n"
MF "v[136:139],a[160:163],v[180:183],v[136:139]\n"
TR "v[190:191],v3 offset:2048\n"
MF "v[136:139],a[200:203],v[184:187],v[136:139]\n"
LD "v202,s[48:51],s89 offen offset:2048 lds\n"
MF "v[144:147],a[204:207],v[184:187],v[144:147]\n"
TR "a[240:241],v3 offset:4096\n"
MF "v[144:147],a[164:167],v[180:183],v[144:147]\n"
TR "a[242:243],v3 offset:6144\n"
MF "v[144:147],a[124:127],v[176:179],v[144:147]\n"
LD "v203,s[48:51],s89 offen offset:3072 lds\n"
MF "v[152:155],a[128:131],v[176:179],v[152:155]\n"
TR "a[244:245],v3 offset:8192\n"
MF "v[152:155],a[168:171],v[180:183],v[152:155]\n"
TR "a[246:247],v3 offset:10240\n"
MF "v[152:155],a[208:211],v[184:187],v[152:155]\n"
MF "v[160:163],a[212:215],v[184:187],v[160:163]\n"
MF "v[160:163],a[172:175],v[180:183],v[160:163]\n"
MF "v[160:163],a[132:135],v[176:179],v[160:163]\n"
MF "v[168:171],a[136:139],v[176:179],v[168:171]\n"
MF "v[168:171],a[176:179],v[180:183],v[168:171]\n"
MF "v[168:171],a[216:219],v[184:187],v[168:171]\n"
"s_waitcnt vmcnt(24) lgkmcnt(0)\n"
"s_barrier\n"
MF "v[172:175],a[216:219],a[244:247],v[172:175]\n"
"s_add_u32 s80,s89,122880\n"
TR "v[176:177],v0 offset:49152\n"
MF "v[172:175],a[176:179],a[240:243],v[172:175]\n"
"s_add_u32 m0,s79,0\n"
TR "v[178:179],v0 offset:51200\n"
MF "v[172:175],a[136:139],v[188:191],v[172:175]\n"
LD "v192,s[48:51],s80 offen lds\n"
MF "v[164:167],a[132:135],v[188:191],v[164:167]\n"
TR "v[180:181],v0 offset:53248\n"
MF "v[164:167],a[172:175],a[240:243],v[164:167]\n"
TR "v[182:183],v0 offset:55296\n"
MF "v[164:167],a[212:215],a[244:247],v[164:167]\n"
LD "v193,s[48:51],s80 offen offset:1024 lds\n"
MF "v[156:159],a[208:211],a[244:247],v[156:159]\n"
TR "v[184:185],v0 offset:57344\n"
MF "v[156:159],a[168:171],a[240:243],v[156:159]\n"
TR "v[186:187],v0 offset:59392\n"
MF "v[156:159],a[128:131],v[188:191],v[156:159]\n"
MF "v[148:151],a[124:127],v[188:191],v[148:151]\n"
MF "v[148:151],a[164:167],a[240:243],v[148:151]\n"
LD "v194,s[48:51],s80 offen offset:2048 lds\n"
MF "v[148:151],a[204:207],a[244:247],v[148:151]\n"
MF "v[140:143],a[200:203],a[244:247],v[140:143]\n"
MF "v[140:143],a[160:163],a[240:243],v[140:143]\n"
MF "v[140:143],a[120:123],v[188:191],v[140:143]\n"
"s_waitcnt lgkmcnt(0)\n"
LD "v195,s[48:51],s80 offen offset:3072 lds\n"
MF "v[96:99],a[20:23],v[176:179],v[96:99]\n"
TR "v[188:189],v1 offset:49152\n"
MF "v[96:99],a[60:63],v[180:183],v[96:99]\n"
TR "v[190:191],v1 offset:51200\n"
MF "v[96:99],a[100:103],v[184:187],v[96:99]\n"
MF "v[104:107],a[104:107],v[184:187],v[104:107]\n"
TR "a[240:241],v1 offset:53248\n"
MF "v[104:107],a[64:67],v[180:183],v[104:107]\n"
"s_add_u32 m0,s79,4096\n"
TR "a[242:243],v1 offset:55296\n"
MF "v[104:107],a[24:27],v[176:179],v[104:107]\n"
LD "v196,s[48:51],s80 offen lds\n"
MF "v[112:115],a[28:31],v[176:179],v[112:115]\n"
TR "a[244:245],v1 offset:57344\n"
MF "v[112:115],a[68:71],v[180:183],v[112:115]\n"
TR "a[246:247],v1 offset:59392\n"
MF "v[112:115],a[108:111],v[184:187],v[112:115]\n"
LD "v197,s[48:51],s80 offen offset:1024 lds\n"
MF "v[120:123],a[112:115],v[184:187],v[120:123]\n"
MF "v[120:123],a[72:75],v[180:183],v[120:123]\n"
MF "v[120:123],a[32:35],v[176:179],v[120:123]\n"
MF "v[128:131],a[36:39],v[176:179],v[128:131]\n"
MF "v[128:131],a[76:79],v[180:183],v[128:131]\n"
LD "v198,s[48:51],s80 offen offset:2048 lds\n"
MF "v[128:131],a[116:119],v[184:187],v[128:131]\n"
"s_waitcnt lgkmcnt(0)\n"
MF "v[132:135],a[116:119],a[244:247],v[132:135]\n"
TR "v[176:177],v2 offset:49152\n"
MF "v[132:135],a[76:79],a[240:243],v[132:135]\n"
TR "v[178:179],v2 offset:51200\n"
MF "v[132:135],a[36:39],v[188:191],v[132:135]\n"
LD "v199,s[48:51],s80 offen offset:3072 lds\n"
MF "v[124:127],a[32:35],v[188:191],v[124:127]\n"
TR "v[180:181],v2 offset:53248\n"
MF "v[124:127],a[72:75],a[240:243],v[124:127]\n"
TR "v[182:183],v2 offset:55296\n"
MF "v[124:127],a[112:115],a[244:247],v[124:127]\n"
MF "v[116:119],a[108:111],a[244:247],v[116:119]\n"
TR "v[184:185],v2 offset:57344\n"
MF "v[116:119],a[68:71],a[240:243],v[116:119]\n"
"s_add_u32 m0,s79,8192\n"
TR "v[186:187],v2 offset:59392\n"
MF "v[116:119],a[28:31],v[188:191],v[116:119]\n"
LD "v200,s[48:51],s80 offen lds\n"
MF "v[108:111],a[24:27],v[188:191],v[108:111]\n"
MF "v[108:111],a[64:67],a[240:243],v[108:111]\n"
MF "v[108:111],a[104:107],a[244:247],v[108:111]\n"
MF "v[100:103],a[100:103],a[244:247],v[100:103]\n"
LD "v201,s[48:51],s80 offen offset:1024 lds\n"
MF "v[100:103],a[60:63],a[240:243],v[100:103]\n"
MF "v[100:103],a[20:23],v[188:191],v[100:103]\n"
"s_waitcnt lgkmcnt(0)\n"
MF "v[136:139],a[20:23],v[176:179],v[136:139]\n"
TR "v[188:189],v3 offset:49152\n"
MF "v[136:139],a[60:63],v[180:183],v[136:139]\n"
TR "v[190:191],v3 offset:51200\n"
MF "v[136:139],a[100:103],v[184:187],v[136:139]\n"
LD "v202,s[48:51],s80 offen offset:2048 lds\n"
MF "v[144:147],a[104:107],v[184:187],v[144:147]\n"
TR "a[240:241],v3 offset:53248\n"
MF "v[144:147],a[64:67],v[180:183],v[144:147]\n"
TR "a[242:243],v3 offset:55296\n"
MF "v[144:147],a[24:27],v[176:179],v[144:147]\n"
LD "v203,s[48:51],s80 offen offset:3072 lds\n"
MF "v[152:155],a[28:31],v[176:179],v[152:155]\n"
TR "a[244:245],v3 offset:57344\n"
MF "v[152:155],a[68:71],v[180:183],v[152:155]\n"
TR "a[246:247],v3 offset:59392\n"
MF "v[152:155],a[108:111],v[184:187],v[152:155]\n"
MF "v[160:163],a[112:115],v[184:187],v[160:163]\n"
MF "v[160:163],a[72:75],v[180:183],v[160:163]\n"
MF "v[160:163],a[32:35],v[176:179],v[160:163]\n"
MF "v[168:171],a[36:39],v[176:179],v[168:171]\n"
MF "v[168:171],a[76:79],v[180:183],v[168:171]\n"
MF "v[168:171],a[116:119],v[184:187],v[168:171]\n"
"s_waitcnt vmcnt(24) lgkmcnt(0)\n"
"s_barrier\n"
MF "v[172:175],a[116:119],a[244:247],v[172:175]\n"
"s_add_u32 s88,s88,s65\n"
TR "v[176:177],v4\n"
MF "v[172:175],a[76:79],a[240:243],v[172:175]\n"
"s_add_u32 m0,s79,49152\n"
TR "v[178:179],v4 offset:2048\n"
MF "v[172:175],a[36:39],v[188:191],v[172:175]\n"
LD "v192,s[48:51],s88 offen lds\n"
MF "v[164:167],a[32:35],v[188:191],v[164:167]\n"
TR "v[180:181],v4 offset:4096\n"
MF "v[164:167],a[72:75],a[240:243],v[164:167]\n"
TR "v[182:183],v4 offset:6144\n"
MF "v[164:167],a[112:115],a[244:247],v[164:167]\n"
LD "v193,s[48:51],s88 offen offset:1024 lds\n"
MF "v[156:159],a[108:111],a[244:247],v[156:159]\n"
TR "v[184:185],v4 offset:8192\n"
MF "v[156:159],a[68:71],a[240:243],v[156:159]\n"
TR "v[186:187],v4 offset:10240\n"
MF "v[156:159],a[28:31],v[188:191],v[156:159]\n"
MF "v[148:151],a[24:27],v[188:191],v[148:151]\n"
MF "v[148:151],a[64:67],a[240:243],v[148:151]\n"
LD "v194,s[48:51],s88 offen offset:2048 lds\n"
MF "v[148:151],a[104:107],a[244:247],v[148:151]\n"
MF "v[140:143],a[100:103],a[244:247],v[140:143]\n"
MF "v[140:143],a[60:63],a[240:243],v[140:143]\n"
MF "v[140:143],a[20:23],v[188:191],v[140:143]\n"
"s_waitcnt lgkmcnt(0)\n"
LD "v195,s[48:51],s88 offen offset:3072 lds\n"
MF "v[96:99],a[140:143],v[176:179],v[96:99]\n"
TR "v[188:189],v5\n"
MF "v[96:99],a[180:183],v[180:183],v[96:99]\n"
TR "v[190:191],v5 offset:2048\n"
MF "v[96:99],a[220:223],v[184:187],v[96:99]\n"
MF "v[104:107],a[224:227],v[184:187],v[104:107]\n"
TR "a[240:241],v5 offset:4096\n"
MF "v[104:107],a[184:187],v[180:183],v[104:107]\n"
"s_add_u32 m0,s79,53248\n"
TR "a[242:243],v5 offset:6144\n"
MF "v[104:107],a[144:147],v[176:179],v[104:107]\n"
LD "v196,s[48:51],s88 offen lds\n"
MF "v[112:115],a[148:151],v[176:179],v[112:115]\n"
TR "a[244:245],v5 offset:8192\n"
MF "v[112:115],a[188:191],v[180:183],v[112:115]\n"
TR "a[246:247],v5 offset:10240\n"
MF "v[112:115],a[228:231],v[184:187],v[112:115]\n"
LD "v197,s[48:51],s88 offen offset:1024 lds\n"
MF "v[120:123],a[232:235],v[184:187],v[120:123]\n"
MF "v[120:123],a[192:195],v[180:183],v[120:123]\n"
MF "v[120:123],a[152:155],v[176:179],v[120:123]\n"
MF "v[128:131],a[156:159],v[176:179],v[128:131]\n"
MF "v[128:131],a[196:199],v[180:183],v[128:131]\n"
LD "v198,s[48:51],s88 offen offset:2048 lds\n"
MF "v[128:131],a[236:239],v[184:187],v[128:131]\n"
"s_waitcnt lgkmcnt(0)\n"
MF "v[132:135],a[236:239],a[244:247],v[132:135]\n"
TR "v[176:177],v6\n"
MF "v[132:135],a[196:199],a[240:243],v[132:135]\n"
TR "v[178:179],v6 offset:2048\n"
MF "v[132:135],a[156:159],v[188:191],v[132:135]\n"
LD "v199,s[48:51],s88 offen offset:3072 lds\n"
MF "v[124:127],a[152:155],v[188:191],v[124:127]\n"
TR "v[180:181],v6 offset:4096\n"
MF "v[124:127],a[192:195],a[240:243],v[124:127]\n"
TR "v[182:183],v6 offset:6144\n"
MF "v[124:127],a[232:235],a[244:247],v[124:127]\n"
MF "v[116:119],a[228:231],a[244:247],v[116:119]\n"
TR "v[184:185],v6 offset:8192\n"
MF "v[116:119],a[188:191],a[240:243],v[116:119]\n"
"s_add_u32 m0,s79,57344\n"
TR "v[186:187],v6 offset:10240\n"
MF "v[116:119],a[148:151],v[188:191],v[116:119]\n"
LD "v200,s[48:51],s88 offen lds\n"
MF "v[108:111],a[144:147],v[188:191],v[108:111]\n"
MF "v[108:111],a[184:187],a[240:243],v[108:111]\n"
MF "v[108:111],a[224:227],a[244:247],v[108:111]\n"
MF "v[100:103],a[220:223],a[244:247],v[100:103]\n"
LD "v201,s[48:51],s88 offen offset:1024 lds\n"
MF "v[100:103],a[180:183],a[240:243],v[100:103]\n"
MF "v[100:103],a[140:143],v[188:191],v[100:103]\n"
"s_waitcnt lgkmcnt(0)\n"
MF "v[136:139],a[140:143],v[176:179],v[136:139]\n"
TR "v[188:189],v7\n"
MF "v[136:139],a[180:183],v[180:183],v[136:139]\n"
TR "v[190:191],v7 offset:2048\n"
MF "v[136:139],a[220:223],v[184:187],v[136:139]\n"
LD "v202,s[48:51],s88 offen offset:2048 lds\n"
MF "v[144:147],a[224:227],v[184:187],v[144:147]\n"
TR "a[240:241],v7 offset:4096\n"
MF "v[144:147],a[184:187],v[180:183],v[144:147]\n"
TR "a[242:243],v7 offset:6144\n"
MF "v[144:147],a[144:147],v[176:179],v[144:147]\n"
LD "v203,s[48:51],s88 offen offset:3072 lds\n"
MF "v[152:155],a[148:151],v[176:179],v[152:155]\n"
TR "a[244:245],v7 offset:8192\n"
MF "v[152:155],a[188:191],v[180:183],v[152:155]\n"
TR "a[246:247],v7 offset:10240\n"
MF "v[152:155],a[228:231],v[184:187],v[152:155]\n"
MF "v[160:163],a[232:235],v[184:187],v[160:163]\n"
MF "v[160:163],a[192:195],v[180:183],v[160:163]\n"
MF "v[160:163],a[152:155],v[176:179],v[160:163]\n"
MF "v[168:171],a[156:159],v[176:179],v[168:171]\n"
MF "v[168:171],a[196:199],v[180:183],v[168:171]\n"
MF "v[168:171],a[236:239],v[184:187],v[168:171]\n"
"s_waitcnt vmcnt(24) lgkmcnt(0)\n"
"s_barrier\n"
MF "v[172:175],a[236:239],a[244:247],v[172:175]\n"
"s_add_u32 s80,s88,122880\n"
TR "v[176:177],v0 offset:24576\n"
MF "v[172:175],a[196:199],a[240:243],v[172:175]\n"
"s_add_u32 m0,s79,98304\n"
"s_add_u32 s85,s85,1\n"
TR "v[178:179],v0 offset:26624\n"
MF "v[172:175],a[156:159],v[188:191],v[172:175]\n"
LD "v192,s[48:51],s80 offen lds\n"
MF "v[164:167],a[152:155],v[188:191],v[164:167]\n"
"s_nop 0\n"
TR "v[180:181],v0 offset:28672\n"
"ds_write_addtid_b32 v96 offset:24576\n"
MF "v[164:167],a[192:195],a[240:243],v[164:167]\n"
"s_nop 0\n"
TR "v[182:183],v0 offset:30720\n"
"ds_write_addtid_b32 v97 offset:24832\n"
MF "v[164:167],a[232:235],a[244:247],v[164:167]\n"
LD "v193,s[48:51],s80 offen offset:1024 lds\n"
MF "v[156:159],a[228:231],a[244:247],v[156:159]\n"
"s_nop 0\n"
TR "v[184:185],v0 offset:32768\n"
"ds_write_addtid_b32 v98 offset:25088\n"
MF "v[156:159],a[188:191],a[240:243],v[156:159]\n"
"s_nop 0\n"
TR "v[186:187],v0 offset:34816\n"
"ds_write_addtid_b32 v99 offset:25344\n"
MF "v[156:159],a[148:151],v[188:191],v[156:159]\n"
"s_nop 0\n"
"ds_write_addtid_b32 v100 offset:25600\n"
"ds_write_addtid_b32 v101 offset:25856\n"
MF "v[148:151],a[144:147],v[188:191],v[148:151]\n"
LD "v194,s[48:51],s80 offen offset:2048 lds\n"
MF "v[148:151],a[184:187],a[240:243],v[148:151]\n"
"s_nop 0\n"
"ds_write_addtid_b32 v102 offset:26112\n"
"ds_write_addtid_b32 v103 offset:26368\n"
MF "v[148:151],a[224:227],a[244:247],v[148:151]\n"
"s_nop 0\n"
"ds_write_addtid_b32 v104 offset:26624\n"
"ds_write_addtid_b32 v105 offset:26880\n"
MF "v[140:143],a[220:223],a[244:247],v[140:143]\n"
"s_nop 0\n"
"ds_write_addtid_b32 v106 offset:27136\n"
"ds_write_addtid_b32 v107 offset:27392\n"
MF "v[140:143],a[180:183],a[240:243],v[140:143]\n"
MF "v[140:143],a[140:143],v[188:191],v[140:143]\n"
"s_waitcnt lgkmcnt(9)\n"
MF "v[16:19],a[0:3],v[176:179],v[252:255]\n"
"s_nop 0\n"
TR "v[188:189],v1 offset:24576\n"
"ds_write_addtid_b32 v108 offset:27648\n"
MF "v[16:19],a[40:43],v[180:183],v[16:19]\n"
"s_nop 0\n"
TR "v[190:191],v1 offset:26624\n"
"ds_write_addtid_b32 v109 offset:27904\n"
MF "v[16:19],a[80:83],v[184:187],v[16:19]\n"
LD "v195,s[48:51],s80 offen offset:3072 lds\n"
MF "v[24:27],a[84:87],v[184:187],v[252:255]\n"
"s_add_u32 s60,s64,1\n"
TR "a[240:241],v1 offset:28672\n"
"ds_write_addtid_b32 v110 offset:28160\n"
MF "v[24:27],a[44:47],v[180:183],v[24:27]\n"
"s_add_u32 m0,s79,102400\n"
TR "a[242:243],v1 offset:30720\n"
"ds_write_addtid_b32 v111 offset:24320\n"
MF "v[24:27],a[4:7],v[176:179],v[24:27]\n"
LD "v196,s[48:51],s80 offen lds\n"
MF "v[32:35],a[8:11],v[176:179],v[252:255]\n"
"s_cmp_eq_u32 s60,8\n"
"s_cselect_b32 s60,0,s60\n"
TR "a[244:245],v1 offset:32768\n"
MF "v[32:35],a[48:51],v[180:183],v[32:35]\n"
"s_cmp_eq_u32 s60,10\n"
"s_cselect_b32 s60,s61,s60\n"
TR "a[246:247],v1 offset:34816\n"
MF "v[32:35],a[88:91],v[184:187],v[32:35]\n"
"s_sub_u32 s65,s60,s64\n"
"ds_write_addtid_b32 v112 offset:24576\n"
"ds_write_addtid_b32 v113 offset:24832\n"
MF "v[40:43],a[92:95],v[184:187],v[252:255]\n"
"s_lshl_b32 s65,s65,7\n"
"ds_write_addtid_b32 v114 offset:25088\n"
"ds_write_addtid_b32 v115 offset:25344\n"
MF "v[40:43],a[52:55],v[180:183],v[40:43]\n"
LD "v197,s[48:51],s80 offen offset:1024 lds\n"
MF "v[40:43],a[12:15],v[176:179],v[40:43]\n"
"s_mov_b32 s64,s60\n"
"ds_write_addtid_b32 v116 offset:25600\n"
"ds_write_addtid_b32 v117 offset:25856\n"
MF "v[48:51],a[16:19],v[176:179],v[252:255]\n"
"s_nop 0\n"
"ds_write_addtid_b32 v118 offset:26112\n"
"ds_write_addtid_b32 v119 offset:26368\n"
MF "v[48:51],a[56:59],v[180:183],v[48:51]\n"
"s_nop 0\n"
"ds_write_addtid_b32 v120 offset:26624\n"
"ds_write_addtid_b32 v121 offset:26880\n"
MF "v[48:51],a[96:99],v[184:187],v[48:51]\n"
"s_waitcnt lgkmcnt(10)\n"
LD "v198,s[48:51],s80 offen offset:2048 lds\n"
MF "v[52:55],a[96:99],a[244:247],v[252:255]\n"
"s_nop 0\n"
TR "v[176:177],v2 offset:24576\n"
"ds_write_addtid_b32 v122 offset:27136\n"
MF "v[52:55],a[56:59],a[240:243],v[52:55]\n"
"s_nop 0\n"
TR "v[178:179],v2 offset:26624\n"
"ds_write_addtid_b32 v123 offset:27392\n"
MF "v[52:55],a[16:19],v[188:191],v[52:55]\n"
"s_nop 0\n"
"ds_write_addtid_b32 v124 offset:27648\n"
"ds_write_addtid_b32 v125 offset:27904\n"
MF "v[44:47],a[12:15],v[188:191],v[252:255]\n"
"s_nop 0\n"
TR "v[180:181],v2 offset:28672\n"
"ds_write_addtid_b32 v126 offset:28160\n"
MF "v[44:47],a[52:55],a[240:243],v[44:47]\n"
"s_nop 0\n"
TR "v[182:183],v2 offset:30720\n"
"ds_write_addtid_b32 v127 offset:28416\n"
MF "v[44:47],a[92:95],a[244:247],v[44:47]\n"
LD "v199,s[48:51],s80 offen offset:3072 lds\n"
MF "v[36:39],a[88:91],a[244:247],v[252:255]\n"
"s_nop 0\n"
TR "v[184:185],v2 offset:32768\n"
"ds_write_addtid_b32 v128 offset:28672\n"
MF "v[36:39],a[48:51],a[240:243],v[36:39]\n"
"s_add_u32 m0,s79,106496\n"
TR "v[186:187],v2 offset:34816\n"
"ds_write_addtid_b32 v129 offset:24832\n"
MF "v[36:39],a[8:11],v[188:191],v[36:39]\n"
LD "v200,s[48:51],s80 offen lds\n"
MF "v[28:31],a[4:7],v[188:191],v[252:255]\n"
"s_nop 0\n"
"ds_write_addtid_b32 v130 offset:25088\n"
"ds_write_addtid_b32 v131 offset:25344\n"
MF "v[28:31],a[44:47],a[240:243],v[28:31]\n"
"s_nop 0\n"
"ds_write_addtid_b32 v132 offset:25600\n"
"ds_write_addtid_b32 v133 offset:25856\n"
MF "v[28:31],a[84:87],a[244:247],v[28:31]\n"
"s_nop 0\n"
"ds_write_addtid_b32 v134 offset:26112\n"
"ds_write_addtid_b32 v135 offset:26368\n"
MF "v[20:23],a[80:83],a[244:247],v[252:255]\n"
MF "v[20:23],a[40:43],a[240:243],v[20:23]\n"
LD "v201,s[48:51],s80 offen offset:1024 lds\n"
MF "v[20:23],a[0:3],v[188:191],v[20:23]\n"
"s_waitcnt lgkmcnt(7)\n"
MF "v[56:59],a[0:3],v[176:179],v[252:255]\n"
TR "v[188:189],v3 offset:24576\n"
MF "v[56:59],a[40:43],v[180:183],v[56:59]\n"
TR "v[190:191],v3 offset:26624\n"
MF "v[56:59],a[80:83],v[184:187],v[56:59]\n"
LD "v202,s[48:51],s80 offen offset:2048 lds\n"
MF "v[64:67],a[84:87],v[184:187],v[252:255]\n"
TR "a[240:241],v3 offset:28672\n"
MF "v[64:67],a[44:47],v[180:183],v[64:67]\n"
TR "a[242:243],v3 offset:30720\n"
MF "v[64:67],a[4:7],v[176:179],v[64:67]\n"
LD "v203,s[48:51],s80 offen offset:3072 lds\n"
MF "v[72:75],a[8:11],v[176:179],v[252:255]\n"
TR "a[244:245],v3 offset:32768\n"
MF "v[72:75],a[48:51],v[180:183],v[72:75]\n"
TR "a[246:247],v3 offset:34816\n"
MF "v[72:75],a[88:91],v[184:187],v[72:75]\n"
MF "v[80:83],a[92:95],v[184:187],v[252:255]\n"
MF "v[80:83],a[52:55],v[180:183],v[80:83]\n"
MF "v[80:83],a[12:15],v[176:179],v[80:83]\n"
MF "v[88:91],a[16:19],v[176:179],v[252:255]\n"
MF "v[88:91],a[56:59],v[180:183],v[88:91]\n"
MF "v[88:91],a[96:99],v[184:187],v[88:91]\n"
"s_waitcnt vmcnt(24) lgkmcnt(0)\n"
"s_barrier\n"
MF "v[92:95],a[96:99],a[244:247],v[252:255]\n"
"s_add_u32 s89,s89,s65\n"
TR "v[176:177],v0\n"
MF "v[92:95],a[56:59],a[240:243],v[92:95]\n"
"s_add_u32 m0,s79,24576\n"
TR "v[178:179],v0 offset:2048\n"
MF "v[92:95],a[16:19],v[188:191],v[92:95]\n"
LD "v192,s[48:51],s89 offen lds\n"
MF "v[84:87],a[12:15],v[188:191],v[252:255]\n"
TR "v[180:181],v0 offset:4096\n"
MF "v[84:87],a[52:55],a[240:243],v[84:87]\n"
TR "v[182:183],v0 offset:6144\n"
MF "v[84:87],a[92:95],a[244:247],v[84:87]\n"
LD "v193,s[48:51],s89 offen offset:1024 lds\n"
MF "v[76:79],a[88:91],a[244:247],v[252:255]\n"
TR "v[184:185],v0 offset:8192\n"
MF "v[76:79],a[48:51],a[240:243],v[76:79]\n"
"s_nop 0\n"
TR "v[186:187],v0 offset:10240\n"
"ds_write_addtid_b32 v136 offset:49152\n"
MF "v[76:79],a[8:11],v[188:191],v[76:79]\n"
"s_nop 0\n"
"ds_write_addtid_b32 v137 offset:49408\n"
"ds_write_addtid_b32 v138 offset:49664\n"
MF "v[68:71],a[4:7],v[188:191],v[252:255]\n"
"s_nop 0\n"
"ds_write_addtid_b32 v139 offset:49920\n"
"ds_write_addtid_b32 v140 offset:50176\n"
MF "v[68:71],a[44:47],a[240:243],v[68:71]\n"
LD "v194,s[48:51],s89 offen offset:2048 lds\n"
MF "v[68:71],a[84:87],a[244:247],v[68:71]\n"
"s_nop 0\n"
"ds_write_addtid_b32 v141 offset:50432\n"
"ds_write_addtid_b32 v142 offset:50688\n"
MF "v[60:63],a[80:83],a[244:247],v[252:255]\n"
"s_nop 0\n"
"ds_write_addtid_b32 v143 offset:50944\n"
"ds_write_addtid_b32 v144 offset:51200\n"
MF "v[60:63],a[40:43],a[240:243],v[60:63]\n"
"s_nop 0\n"
"ds_write_addtid_b32 v145 offset:51456\n"
"ds_write_addtid_b32 v146 offset:51712\n"
MF "v[60:63],a[0:3],v[188:191],v[60:63]\n"
".Lpg24h:\n"
"s_cbranch_execz .Lpg28h\n"
"s_waitcnt lgkmcnt(11)\n"
LD "v195,s[48:51],s89 offen offset:3072 lds\n"
MF "v[16:19],a[120:123],v[176:179],v[16:19]\n"
"s_nop 0\n"
TR "v[188:189],v1\n"
"ds_write_addtid_b32 v147 offset:51968\n"
MF "v[16:19],a[160:163],v[180:183],v[16:19]\n"
"s_nop 0\n"
TR "v[190:191],v1 offset:2048\n"
"ds_write_addtid_b32 v148 offset:52224\n"
MF "v[16:19],a[200:203],v[184:187],v[16:19]\n"
"s_nop 0\n"
"ds_write_addtid_b32 v149 offset:52480\n"
"ds_write_addtid_b32 v150 offset:52736\n"
MF "v[24:27],a[204:207],v[184:187],v[24:27]\n"
"s_nop 0\n"
TR "a[240:241],v1 offset:4096\n"
"ds_write_addtid_b32 v151 offset:52992\n"
MF "v[24:27],a[164:167],v[180:183],v[24:27]\n"
"s_add_u32 m0,s79,28672\n"
TR "a[242:243],v1 offset:6144\n"
"ds_write_addtid_b32 v152 offset:49152\n"
MF "v[24:27],a[124:127],v[176:179],v[24:27]\n"
LD "v196,s[48:51],s89 offen lds\n"
MF "v[32:35],a[128:131],v[176:179],v[32:35]\n"
"s_nop 0\n"
TR "a[244:245],v1 offset:8192\n"
"ds_write_addtid_b32 v153 offset:49408\n"
MF "v[32:35],a[168:171],v[180:183],v[32:35]\n"
"s_nop 0\n"
TR "a[246:247],v1 offset:10240\n"
"ds_write_addtid_b32 v154 offset:49664\n"
MF "v[32:35],a[208:211],v[184:187],v[32:35]\n"
LD "v197,s[48:51],s89 offen offset:1024 lds\n"
MF "v[40:43],a[212:215],v[184:187],v[40:43]\n"
"s_nop 0\n"
"ds_write_addtid_b32 v155 offset:49920\n"
"ds_write_addtid_b32 v156 offset:50176\n"
MF "v[40:43],a[172:175],v[180:183],v[40:43]\n"
"s_nop 0\n"
"ds_write_addtid_b32 v157 offset:50432\n"
"ds_write_addtid_b32 v158 offset:50688\n"
MF "v[40:43],a[132:135],v[176:179],v[40:43]\n"
"s_nop 0\n"
"ds_write_addtid_b32 v159 offset:50944\n"
"ds_write_addtid_b32 v160 offset:51200\n"
MF "v[48:51],a[136:139],v[176:179],v[48:51]\n"
"s_nop 0\n"
"ds_write_addtid_b32 v161 offset:51456\n"
"ds_write_addtid_b32 v162 offset:51712\n"
MF "v[48:51],a[176:179],v[180:183],v[48:51]\n"
LD "v198,s[48:51],s89 offen offset:2048 lds\n"
MF "v[48:51],a[216:219],v[184:187],v[48:51]\n"
"s_waitcnt lgkmcnt(9)\n"
"ds_write_addtid_b32 v163 offset:51968\n"
"ds_write_addtid_b32 v164 offset:52224\n"
MF "v[52:55],a[216:219],a[244:247],v[52:55]\n"
"s_nop 0\n"
TR "v[176:177],v2\n"
"ds_write_addtid_b32 v165 offset:52480\n"
MF "v[52:55],a[176:179],a[240:243],v[52:55]\n"
"s_nop 0\n"
TR "v[178:179],v2 offset:2048\n"
"ds_write_addtid_b32 v166 offset:52736\n"
MF "v[52:55],a[136:139],v[188:191],v[52:55]\n"
LD "v199,s[48:51],s89 offen offset:3072 lds\n"
MF "v[44:47],a[132:135],v[188:191],v[44:47]\n"
"s_nop 0\n"
TR "v[180:181],v2 offset:4096\n"
"ds_write_addtid_b32 v167 offset:52992\n"
MF "v[44:47],a[172:175],a[240:243],v[44:47]\n"
"s_nop 0\n"
TR "v[182:183],v2 offset:6144\n"
"ds_write_addtid_b32 v168 offset:53248\n"
MF "v[44:47],a[212:215],a[244:247],v[44:47]\n"
"s_nop 0\n"
"ds_write_addtid_b32 v169 offset:53504\n"
"ds_write_addtid_b32 v170 offset:53760\n"
MF "v[36:39],a[208:211],a[244:247],v[36:39]\n"
"s_nop 0\n"
TR "v[184:185],v2 offset:8192\n"
"ds_write_addtid_b32 v171 offset:54016\n"
MF "v[36:39],a[168:171],a[240:243],v[36:39]\n"
"s_add_u32 m0,s79,32768\n"
TR "v[186:187],v2 offset:10240\n"
"ds_write_addtid_b32 v172 offset:50176\n"
MF "v[36:39],a[128:131],v[188:191],v[36:39]\n"
LD "v200,s[48:51],s89 offen lds\n"
MF "v[28:31],a[124:127],v[188:191],v[28:31]\n"
"s_nop 0\n"
"ds_write_addtid_b32 v173 offset:50432\n"
"ds_write_addtid_b32 v174 offset:50688\n"
MF "v[28:31],a[164:167],a[240:243],v[28:31]\n"
"ds_write_addtid_b32 v175 offset:50944\n"
MF "v[28:31],a[204:207],a[244:247],v[28:31]\n"
MF "v[20:23],a[200:203],a[244:247],v[20:23]\n"
LD "v201,s[48:51],s89 offen offset:1024 lds\n"
MF "v[20:23],a[160:163],a[240:243],v[20:23]\n"
MF "v[20:23],a[120:123],v[188:191],v[20:23]\n"
"s_waitcnt lgkmcnt(4)\n"
MF "v[56:59],a[120:123],v[176:179],v[56:59]\n"
TR "v[188:189],v3\n"
MF "v[56:59],a[160:163],v[180:183],v[56:59]\n"
TR "v[190:191],v3 offset:2048\n"
MF "v[56:59],a[200:203],v[184:187],v[56:59]\n"
LD "v202,s[48:51],s89 offen offset:2048 lds\n"
MF "v[64:67],a[204:207],v[184:187],v[64:67]\n"
TR "a[240:241],v3 offset:4096\n"
MF "v[64:67],a[164:167],v[180:183],v[64:67]\n"
TR "a[242:243],v3 offset:6144\n"
MF "v[64:67],a[124:127],v[176:179],v[64:67]\n"
LD "v203,s[48:51],s89 offen offset:3072 lds\n"
MF "v[72:75],a[128:131],v[176:179],v[72:75]\n"
TR "a[244:245],v3 offset:8192\n"
MF "v[72:75],a[168:171],v[180:183],v[72:75]\n"
TR "a[246:247],v3 offset:10240\n"
MF "v[72:75],a[208:211],v[184:187],v[72:75]\n"
MF "v[80:83],a[212:215],v[184:187],v[80:83]\n"
MF "v[80:83],a[172:175],v[180:183],v[80:83]\n"
MF "v[80:83],a[132:135],v[176:179],v[80:83]\n"
MF "v[88:91],a[136:139],v[176:179],v[88:91]\n"
MF "v[88:91],a[176:179],v[180:183],v[88:91]\n"
MF "v[88:91],a[216:219],v[184:187],v[88:91]\n"
"s_waitcnt vmcnt(24) lgkmcnt(0)\n"
"s_barrier\n"
MF "v[92:95],a[216:219],a[244:247],v[92:95]\n"
"s_add_u32 s80,s89,122880\n"
TR "v[176:177],v0 offset:49152\n"
MF "v[92:95],a[176:179],a[240:243],v[92:95]\n"
"s_add_u32 m0,s79,0\n"
TR "v[178:179],v0 offset:51200\n"
MF "v[92:95],a[136:139],v[188:191],v[92:95]\n"
LD "v192,s[48:51],s80 offen lds\n"
MF "v[84:87],a[132:135],v[188:191],v[84:87]\n"
TR "v[180:181],v0 offset:53248\n"
MF "v[84:87],a[172:175],a[240:243],v[84:87]\n"
TR "v[182:183],v0 offset:55296\n"
MF "v[84:87],a[212:215],a[244:247],v[84:87]\n"
LD "v193,s[48:51],s80 offen offset:1024 lds\n"
MF "v[76:79],a[208:211],a[244:247],v[76:79]\n"
TR "v[184:185],v0 offset:57344\n"
MF "v[76:79],a[168:171],a[240:243],v[76:79]\n"
TR "v[186:187],v0 offset:59392\n"
MF "v[76:79],a[128:131],v[188:191],v[76:79]\n"
MF "v[68:71],a[124:127],v[188:191],v[68:71]\n"
MF "v[68:71],a[164:167],a[240:243],v[68:71]\n"
LD "v194,s[48:51],s80 offen offset:2048 lds\n"
MF "v[68:71],a[204:207],a[244:247],v[68:71]\n"
MF "v[60:63],a[200:203],a[244:247],v[60:63]\n"
MF "v[60:63],a[160:163],a[240:243],v[60:63]\n"
MF "v[60:63],a[120:123],v[188:191],v[60:63]\n"
"s_waitcnt lgkmcnt(0)\n"
LD "v195,s[48:51],s80 offen offset:3072 lds\n"
MF "v[16:19],a[20:23],v[176:179],v[16:19]\n"
TR "v[188:189],v1 offset:49152\n"
MF "v[16:19],a[60:63],v[180:183],v[16:19]\n"
TR "v[190:191],v1 offset:51200\n"
MF "v[16:19],a[100:103],v[184:187],v[16:19]\n"
MF "v[24:27],a[104:107],v[184:187],v[24:27]\n"
TR "a[240:241],v1 offset:53248\n"
MF "v[24:27],a[64:67],v[180:183],v[24:27]\n"
"s_add_u32 m0,s79,4096\n"
TR "a[242:243],v1 offset:55296\n"
MF "v[24:27],a[24:27],v[176:179],v[24:27]\n"
LD "v196,s[48:51],s80 offen lds\n"
MF "v[32:35],a[28:31],v[176:179],v[32:35]\n"
TR "a[244:245],v1 offset:57344\n"
MF "v[32:35],a[68:71],v[180:183],v[32:35]\n"
TR "a[246:247],v1 offset:59392\n"
MF "v[32:35],a[108:111],v[184:187],v[32:35]\n"
LD "v197,s[48:51],s80 offen offset:1024 lds\n"
MF "v[40:43],a[112:115],v[184:187],v[40:43]\n"
MF "v[40:43],a[72:75],v[180:183],v[40:43]\n"
MF "v[40:43],a[32:35],v[176:179],v[40:43]\n"
MF "v[48:51],a[36:39],v[176:179],v[48:51]\n"
MF "v[48:51],a[76:79],v[180:183],v[48:51]\n"
LD "v198,s[48:51],s80 offen offset:2048 lds\n"
MF "v[48:51],a[116:119],v[184:187],v[48:51]\n"
"s_waitcnt lgkmcnt(0)\n"
MF "v[52:55],a[116:119],a[244:247],v[52:55]\n"
TR "v[176:177],v2 offset:49152\n"
MF "v[52:55],a[76:79],a[240:243],v[52:55]\n"
TR "v[178:179],v2 offset:51200\n"
MF "v[52:55],a[36:39],v[188:191],v[52:55]\n"
LD "v199,s[48:51],s80 offen offset:3072 lds\n"
MF "v[44:47],a[32:35],v[188:191],v[44:47]\n"
TR "v[180:181],v2 offset:53248\n"
MF "v[44:47],a[72:75],a[240:243],v[44:47]\n"
TR "v[182:183],v2 offset:55296\n"
MF "v[44:47],a[112:115],a[244:247],v[44:47]\n"
MF "v[36:39],a[108:111],a[244:247],v[36:39]\n"
TR "v[184:185],v2 offset:57344\n"
MF "v[36:39],a[68:71],a[240:243],v[36:39]\n"
"s_add_u32 m0,s79,8192\n"
TR "v[186:187],v2 offset:59392\n"
MF "v[36:39],a[28:31],v[188:191],v[36:39]\n"
LD "v200,s[48:51],s80 offen lds\n"
MF "v[28:31],a[24:27],v[188:191],v[28:31]\n"
MF "v[28:31],a[64:67],a[240:243],v[28:31]\n"
MF "v[28:31],a[104:107],a[244:247],v[28:31]\n"
MF "v[20:23],a[100:103],a[244:247],v[20:23]\n"
LD "v201,s[48:51],s80 offen offset:1024 lds\n"
MF "v[20:23],a[60:63],a[240:243],v[20:23]\n"
MF "v[20:23],a[20:23],v[188:191],v[20:23]\n"
"s_waitcnt lgkmcnt(0)\n"
MF "v[56:59],a[20:23],v[176:179],v[56:59]\n"
TR "v[188:189],v3 offset:49152\n"
MF "v[56:59],a[60:63],v[180:183],v[56:59]\n"
TR "v[190:191],v3 offset:51200\n"
MF "v[56:59],a[100:103],v[184:187],v[56:59]\n"
LD "v202,s[48:51],s80 offen offset:2048 lds\n"
MF "v[64:67],a[104:107],v[184:187],v[64:67]\n"
TR "a[240:241],v3 offset:53248\n"
MF "v[64:67],a[64:67],v[180:183],v[64:67]\n"
TR "a[242:243],v3 offset:55296\n"
MF "v[64:67],a[24:27],v[176:179],v[64:67]\n"
LD "v203,s[48:51],s80 offen offset:3072 lds\n"
MF "v[72:75],a[28:31],v[176:179],v[72:75]\n"
TR "a[244:245],v3 offset:57344\n"
MF "v[72:75],a[68:71],v[180:183],v[72:75]\n"
TR "a[246:247],v3 offset:59392\n"
MF "v[72:75],a[108:111],v[184:187],v[72:75]\n"
MF "v[80:83],a[112:115],v[184:187],v[80:83]\n"
MF "v[80:83],a[72:75],v[180:183],v[80:83]\n"
MF "v[80:83],a[32:35],v[176:179],v[80:83]\n"
MF "v[88:91],a[36:39],v[176:179],v[88:91]\n"
MF "v[88:91],a[76:79],v[180:183],v[88:91]\n"
MF "v[88:91],a[116:119],v[184:187],v[88:91]\n"
"s_waitcnt vmcnt(24) lgkmcnt(0)\n"
"s_barrier\n"
MF "v[92:95],a[116:119],a[244:247],v[92:95]\n"
"s_add_u32 s88,s88,s65\n"
TR "v[176:177],v4\n"
MF "v[92:95],a[76:79],a[240:243],v[92:95]\n"
"s_add_u32 m0,s79,49152\n"
TR "v[178:179],v4 offset:2048\n"
MF "v[92:95],a[36:39],v[188:191],v[92:95]\n"
LD "v192,s[48:51],s88 offen lds\n"
MF "v[84:87],a[32:35],v[188:191],v[84:87]\n"
TR "v[180:181],v4 offset:4096\n"
MF "v[84:87],a[72:75],a[240:243],v[84:87]\n"
TR "v[182:183],v4 offset:6144\n"
MF "v[84:87],a[112:115],a[244:247],v[84:87]\n"
LD "v193,s[48:51],s88 offen offset:1024 lds\n"
MF "v[76:79],a[108:111],a[244:247],v[76:79]\n"
TR "v[184:185],v4 offset:8192\n"
MF "v[76:79],a[68:71],a[240:243],v[76:79]\n"
TR "v[186:187],v4 offset:10240\n"
MF "v[76:79],a[28:31],v[188:191],v[76:79]\n"
MF "v[68:71],a[24:27],v[188:191],v[68:71]\n"
MF "v[68:71],a[64:67],a[240:243],v[68:71]\n"
LD "v194,s[48:51],s88 offen offset:2048 lds\n"
MF "v[68:71],a[104:107],a[244:247],v[68:71]\n"
MF "v[60:63],a[100:103],a[244:247],v[60:63]\n"
MF "v[60:63],a[60:63],a[240:243],v[60:63]\n"
MF "v[60:63],a[20:23],v[188:191],v[60:63]\n"
"s_waitcnt lgkmcnt(0)\n"
LD "v195,s[48:51],s88 offen offset:3072 lds\n"
MF "v[16:19],a[140:143],v[176:179],v[16:19]\n"
TR "v[188:189],v5\n"
MF "v[16:19],a[180:183],v[180:183],v[16:19]\n"
TR "v[190:191],v5 offset:2048\n"
MF "v[16:19],a[220:223],v[184:187],v[16:19]\n"
MF "v[24:27],a[224:227],v[184:187],v[24:27]\n"
TR "a[240:241],v5 offset:4096\n"
MF "v[24:27],a[184:187],v[180:183],v[24:27]\n"
"s_add_u32 m0,s79,53248\n"
TR "a[242:243],v5 offset:6144\n"
MF "v[24:27],a[144:147],v[176:179],v[24:27]\n"
LD "v196,s[48:51],s88 offen lds\n"
MF "v[32:35],a[148:151],v[176:179],v[32:35]\n"
TR "a[244:245],v5 offset:8192\n"
MF "v[32:35],a[188:191],v[180:183],v[32:35]\n"
TR "a[246:247],v5 offset:10240\n"
MF "v[32:35],a[228:231],v[184:187],v[32:35]\n"
LD "v197,s[48:51],s88 offen offset:1024 lds\n"
MF "v[40:43],a[232:235],v[184:187],v[40:43]\n"
MF "v[40:43],a[192:195],v[180:183],v[40:43]\n"
MF "v[40:43],a[152:155],v[176:179],v[40:43]\n"
MF "v[48:51],a[156:159],v[176:179],v[48:51]\n"
MF "v[48:51],a[196:199],v[180:183],v[48:51]\n"
LD "v198,s[48:51],s88 offen offset:2048 lds\n"
MF "v[48:51],a[236:239],v[184:187],v[48:51]\n"
"s_waitcnt lgkmcnt(0)\n"
MF "v[52:55],a[236:239],a[244:247],v[52:55]\n"
TR "v[176:177],v6\n"
MF "v[52:55],a[196:199],a[240:243],v[52:55]\n"
TR "v[178:179],v6 offset:2048\n"
MF "v[52:55],a[156:159],v[188:191],v[52:55]\n"
LD "v199,s[48:51],s88 offen offset:3072 lds\n"
MF "v[44:47],a[152:155],v[188:191],v[44:47]\n"
TR "v[180:181],v6 offset:4096\n"
MF "v[44:47],a[192:195],a[240:243],v[44:47]\n"
TR "v[182:183],v6 offset:6144\n"
MF "v[44:47],a[232:235],a[244:247],v[44:47]\n"
MF "v[36:39],a[228:231],a[244:247],v[36:39]\n"
TR "v[184:185],v6 offset:8192\n"
MF "v[36:39],a[188:191],a[240:243],v[36:39]\n"
"s_add_u32 m0,s79,57344\n"
TR "v[186:187],v6 offset:10240\n"
MF "v[36:39],a[148:151],v[188:191],v[36:39]\n"
LD "v200,s[48:51],s88 offen lds\n"
MF "v[28:31],a[144:147],v[188:191],v[28:31]\n"
MF "v[28:31],a[184:187],a[240:243],v[28:31]\n"
MF "v[28:31],a[224:227],a[244:247],v[28:31]\n"
MF "v[20:23],a[220:223],a[244:247],v[20:23]\n"
LD "v201,s[48:51],s88 offen offset:1024 lds\n"
MF "v[20:23],a[180:183],a[240:243],v[20:23]\n"
MF "v[20:23],a[140:143],v[188:191],v[20:23]\n"
"s_waitcnt lgkmcnt(0)\n"
MF "v[56:59],a[140:143],v[176:179],v[56:59]\n"
TR "v[188:189],v7\n"
MF "v[56:59],a[180:183],v[180:183],v[56:59]\n"
TR "v[190:191],v7 offset:2048\n"
MF "v[56:59],a[220:223],v[184:187],v[56:59]\n"
LD "v202,s[48:51],s88 offen offset:2048 lds\n"
MF "v[64:67],a[224:227],v[184:187],v[64:67]\n"
TR "a[240:241],v7 offset:4096\n"
MF "v[64:67],a[184:187],v[180:183],v[64:67]\n"
TR "a[242:243],v7 offset:6144\n"
MF "v[64:67],a[144:147],v[176:179],v[64:67]\n"
LD "v203,s[48:51],s88 offen offset:3072 lds\n"
MF "v[72:75],a[148:151],v[176:179],v[72:75]\n"
TR "a[244:245],v7 offset:8192\n"
MF "v[72:75],a[188:191],v[180:183],v[72:75]\n"
TR "a[246:247],v7 offset:10240\n"
MF "v[72:75],a[228:231],v[184:187],v[72:75]\n"
MF "v[80:83],a[232:235],v[184:187],v[80:83]\n"
MF "v[80:83],a[192:195],v[180:183],v[80:83]\n"
MF "v[80:83],a[152:155],v[176:179],v[80:83]\n"
MF "v[88:91],a[156:159],v[176:179],v[88:91]\n"
MF "v[88:91],a[196:199],v[180:183],v[88:91]\n"
MF "v[88:91],a[236:239],v[184:187],v[88:91]\n"
"s_waitcnt vmcnt(24) lgkmcnt(0)\n"
"s_barrier\n"
MF "v[92:95],a[236:239],a[244:247],v[92:95]\n"
"s_add_u32 s80,s88,122880\n"
TR "v[176:177],v0 offset:24576\n"
MF "v[92:95],a[196:199],a[240:243],v[92:95]\n"
"s_add_u32 m0,s79,98304\n"
"s_add_u32 s85,s85,1\n"
TR "v[178:179],v0 offset:26624\n"
MF "v[92:95],a[156:159],v[188:191],v[92:95]\n"
LD "v192,s[48:51],s80 offen lds\n"
MF "v[84:87],a[152:155],v[188:191],v[84:87]\n"
"s_nop 0\n"
TR "v[180:181],v0 offset:28672\n"
"ds_write_addtid_b32 v16 offset:24576\n"
MF "v[84:87],a[192:195],a[240:243],v[84:87]\n"
"s_nop 0\n"
TR "v[182:183],v0 offset:30720\n"
"ds_write_addtid_b32 v17 offset:24832\n"
MF "v[84:87],a[232:235],a[244:247],v[84:87]\n"
LD "v193,s[48:51],s80 offen offset:1024 lds\n"
MF "v[76:79],a[228:231],a[244:247],v[76:79]\n"
"s_nop 0\n"
TR "v[184:185],v0 offset:32768\n"
"ds_write_addtid_b32 v18 offset:25088\n"
MF "v[76:79],a[188:191],a[240:243],v[76:79]\n"
"s_nop 0\n"
TR "v[186:187],v0 offset:34816\n"
"ds_write_addtid_b32 v19 offset:25344\n"
MF "v[76:79],a[148:151],v[188:191],v[76:79]\n"
"s_nop 0\n"
"ds_write_addtid_b32 v20 offset:25600\n"
"ds_write_addtid_b32 v21 offset:25856\n"
MF "v[68:71],a[144:147],v[188:191],v[68:71]\n"
LD "v194,s[48:51],s80 offen offset:2048 lds\n"
MF "v[68:71],a[184:187],a[240:243],v[68:71]\n"
"s_nop 0\n"
"ds_write_addtid_b32 v22 offset:26112\n"
"ds_write_addtid_b32 v23 offset:26368\n"
MF "v[68:71],a[224:227],a[244:247],v[68:71]\n"
"s_nop 0\n"
"ds_write_addtid_b32 v24 offset:26624\n"
"ds_write_addtid_b32 v25 offset:26880\n"
MF "v[60:63],a[220:223],a[244:247],v[60:63]\n"
"s_nop 0\n"
"ds_write_addtid_b32 v26 offset:27136\n"
"ds_write_addtid_b32 v27 offset:27392\n"
MF "v[60:63],a[180:183],a[240:243],v[60:63]\n"
MF "v[60:63],a[140:143],v[188:191],v[60:63]\n"
"s_waitcnt lgkmcnt(9)\n"
"s_cmp_lg_u32 s85,5\n"
"s_cbranch_scc1 .Lpl05\n"
".Lpl09:\n"
MF "v[96:99],a[0:3],v[176:179],v[252:255]\n"
"s_nop 0\n"
TR "v[188:189],v1 offset:24576\n"
"ds_write_addtid_b32 v28 offset:27648\n"
MF "v[96:99],a[40:43],v[180:183],v[96:99]\n"
"s_nop 0\n"
TR "v[190:191],v1 offset:26624\n"
"ds_write_addtid_b32 v29 offset:27904\n"
MF "v[96:99],a[80:83],v[184:187],v[96:99]\n"
LD "v195,s[48:51],s80 offen offset:3072 lds\n"
MF "v[104:107],a[84:87],v[184:187],v[252:255]\n"
"s_add_u32 s60,s64,1\n"
TR "a[240:241],v1 offset:28672\n"
"ds_write_addtid_b32 v30 offset:28160\n"
MF "v[104:107],a[44:47],v[180:183],v[104:107]\n"
"s_add_u32 m0,s79,102400\n"
TR "a[242:243],v1 offset:30720\n"
"ds_write_addtid_b32 v31 offset:24320\n"
MF "v[104:107],a[4:7],v[176:179],v[104:107]\n"
LD "v196,s[48:51],s80 offen lds\n"
MF "v[112:115],a[8:11],v[176:179],v[252:255]\n"
"s_cmp_eq_u32 s60,8\n"
"s_cselect_b32 s60,0,s60\n"
TR "a[244:245],v1 offset:32768\n"
MF "v[112:115],a[48:51],v[180:183],v[112:115]\n"
"s_cmp_eq_u32 s60,10\n"
"s_cselect_b32 s60,s61,s60\n"
TR "a[246:247],v1 offset:34816\n"
MF "v[112:115],a[88:91],v[184:187],v[112:115]\n"
"s_sub_u32 s65,s60,s64\n"
"ds_write_addtid_b32 v32 offset:24576\n"
"ds_write_addtid_b32 v33 offset:24832\n"
MF "v[120:123],a[92:95],v[184:187],v[252:255]\n"
"s_lshl_b32 s65,s65,7\n"
"ds_write_addtid_b32 v34 offset:25088\n"
"ds_write_addtid_b32 v35 offset:25344\n"
MF "v[120:123],a[52:55],v[180:183],v[120:123]\n"
LD "v197,s[48:51],s80 offen offset:1024 lds\n"
MF "v[120:123],a[12:15],v[176:179],v[120:123]\n"
"s_mov_b32 s64,s60\n"
"ds_write_addtid_b32 v36 offset:25600\n"
"ds_write_addtid_b32 v37 offset:25856\n"
MF "v[128:131],a[16:19],v[176:179],v[252:255]\n"
"s_nop 0\n"
"ds_write_addtid_b32 v38 offset:26112\n"
"ds_write_addtid_b32 v39 offset:26368\n"
MF "v[128:131],a[56:59],v[180:183],v[128:131]\n"
"s_nop 0\n"
"ds_write_addtid_b32 v40 offset:26624\n"
"ds_write_addtid_b32 v41 offset:26880\n"
MF "v[128:131],a[96:99],v[184:187],v[128:131]\n"
"s_waitcnt lgkmcnt(10)\n"
LD "v198,s[48:51],s80 offen offset:2048 lds\n"
MF "v[132:135],a[96:99],a[244:247],v[252:255]\n"
"s_nop 0\n"
TR "v[176:177],v2 offset:24576\n"
"ds_write_addtid_b32 v42 offset:27136\n"
MF "v[132:135],a[56:59],a[240:243],v[132:135]\n"
"s_nop 0\n"
TR "v[178:179],v2 offset:26624\n"
"ds_write_addtid_b32 v43 offset:27392\n"
MF "v[132:135],a[16:19],v[188:191],v[132:135]\n"
"s_nop 0\n"
"ds_write_addtid_b32 v44 offset:27648\n"
"ds_write_addtid_b32 v45 offset:27904\n"
MF "v[124:127],a[12:15],v[188:191],v[252:255]\n"
"s_nop 0\n"
TR "v[180:181],v2 offset:28672\n"
"ds_write_addtid_b32 v46 offset:28160\n"
MF "v[124:127],a[52:55],a[240:243],v[124:127]\n"
"s_nop 0\n"
TR "v[182:183],v2 offset:30720\n"
"ds_write_addtid_b32 v47 offset:28416\n"
MF "v[124:127],a[92:95],a[244:247],v[124:127]\n"
LD "v199,s[48:51],s80 offen offset:3072 lds\n"
MF "v[116:119],a[88:91],a[244:247],v[252:255]\n"
"s_nop 0\n"
TR "v[184:185],v2 offset:32768\n"
"ds_write_addtid_b32 v48 offset:28672\n"
MF "v[116:119],a[48:51],a[240:243],v[116:119]\n"
"s_add_u32 m0,s79,106496\n"
TR "v[186:187],v2 offset:34816\n"
"ds_write_addtid_b32 v49 offset:24832\n"
MF "v[116:119],a[8:11],v[188:191],v[116:119]\n"
LD "v200,s[48:51],s80 offen lds\n"
MF "v[108:111],a[4:7],v[188:191],v[252:255]\n"
"s_nop 0\n"
"ds_write_addtid_b32 v50 offset:25088\n"
"ds_write_addtid_b32 v51 offset:25344\n"
MF "v[108:111],a[44:47],a[240:243],v[108:111]\n"
"s_nop 0\n"
"ds_write_addtid_b32 v52 offset:25600\n"
"ds_write_addtid_b32 v53 offset:25856\n"
MF "v[108:111],a[84:87],a[244:247],v[108:111]\n"
"s_nop 0\n"
"ds_write_addtid_b32 v54 offset:26112\n"
"ds_write_addtid_b32 v55 offset:26368\n"
MF "v[100:103],a[80:83],a[244:247],v[252:255]\n"
MF "v[100:103],a[40:43],a[240:243],v[100:103]\n"
LD "v201,s[48:51],s80 offen offset:1024 lds\n"
MF "v[100:103],a[0:3],v[188:191],v[100:103]\n"
"s_waitcnt lgkmcnt(7)\n"
MF "v[136:139],a[0:3],v[176:179],v[252:255]\n"
TR "v[188:189],v3 offset:24576\n"
MF "v[136:139],a[40:43],v[180:183],v[136:139]\n"
TR "v[190:191],v3 offset:26624\n"
MF "v[136:139],a[80:83],v[184:187],v[136:139]\n"
LD "v202,s[48:51],s80 offen offset:2048 lds\n"
MF "v[144:147],a[84:87],v[184:187],v[252:255]\n"
TR "a[240:241],v3 offset:28672\n"
MF "v[144:147],a[44:47],v[180:183],v[144:147]\n"
TR "a[242:243],v3 offset:30720\n"
MF "v[144:147],a[4:7],v[176:179],v[144:147]\n"
LD "v203,s[48:51],s80 offen offset:3072 lds\n"
MF "v[152:155],a[8:11],v[176:179],v[252:255]\n"
TR "a[244:245],v3 offset:32768\n"
MF "v[152:155],a[48:51],v[180:183],v[152:155]\n"
TR "a[246:247],v3 offset:34816\n"
MF "v[152:155],a[88:91],v[184:187],v[152:155]\n"
MF "v[160:163],a[92:95],v[184:187],v[252:255]\n"
MF "v[160:163],a[52:55],v[180:183],v[160:163]\n"
MF "v[160:163],a[12:15],v[176:179],v[160:163]\n"
MF "v[168:171],a[16:19],v[176:179],v[252:255]\n"
MF "v[168:171],a[56:59],v[180:183],v[168:171]\n"
MF "v[168:171],a[96:99],v[184:187],v[168:171]\n"
"s_waitcnt vmcnt(24) lgkmcnt(0)\n"
"s_barrier\n"
MF "v[172:175],a[96:99],a[244:247],v[252:255]\n"
"s_add_u32 s89,s89,s65\n"
TR "v[176:177],v0\n"
MF "v[172:175],a[56:59],a[240:243],v[172:175]\n"
"s_add_u32 m0,s79,24576\n"
TR "v[178:179],v0 offset:2048\n"
MF "v[172:175],a[16:19],v[188:191],v[172:175]\n"
LD "v192,s[48:51],s89 offen lds\n"
MF "v[164:167],a[12:15],v[188:191],v[252:255]\n"
TR "v[180:181],v0 offset:4096\n"
MF "v[164:167],a[52:55],a[240:243],v[164:167]\n"
TR "v[182:183],v0 offset:6144\n"
MF "v[164:167],a[92:95],a[244:247],v[164:167]\n"
LD "v193,s[48:51],s89 offen offset:1024 lds\n"
MF "v[156:159],a[88:91],a[244:247],v[252:255]\n"
TR "v[184:185],v0 offset:8192\n"
MF "v[156:159],a[48:51],a[240:243],v[156:159]\n"
"s_nop 0\n"
TR "v[186:187],v0 offset:10240\n"
"ds_write_addtid_b32 v56 offset:49152\n"
MF "v[156:159],a[8:11],v[188:191],v[156:159]\n"
"s_nop 0\n"
"ds_write_addtid_b32 v57 offset:49408\n"
"ds_write_addtid_b32 v58 offset:49664\n"
MF "v[148:151],a[4:7],v[188:191],v[252:255]\n"
LD "v194,s[48:51],s89 offen offset:2048 lds\n"
MF "v[148:151],a[44:47],a[240:243],v[148:151]\n"
"buffer_load_dword v10,v12,s[40:43],s86 offen sc1\n"
MF "v[148:151],a[84:87],a[244:247],v[148:151]\n"
"s_nop 0\n"
"ds_write_addtid_b32 v59 offset:49920\n"
"ds_write_addtid_b32 v60 offset:50176\n"
MF "v[140:143],a[80:83],a[244:247],v[252:255]\n"
"s_nop 0\n"
"ds_write_addtid_b32 v61 offset:50432\n"
"ds_write_addtid_b32 v62 offset:50688\n"
MF "v[140:143],a[40:43],a[240:243],v[140:143]\n"
".Lpg25h:\n"
"s_cbranch_execz .Lpgrole1_h\n"
"s_nop 0\n"
"ds_write_addtid_b32 v63 offset:50944\n"
"ds_write_addtid_b32 v64 offset:51200\n"
MF "v[140:143],a[0:3],v[188:191],v[140:143]\n"
"s_waitcnt lgkmcnt(9)\n"
LD "v195,s[48:51],s89 offen offset:3072 lds\n"
MF "v[96:99],a[120:123],v[176:179],v[96:99]\n"
"s_nop 0\n"
TR "v[188:189],v1\n"
"ds_write_addtid_b32 v65 offset:51456\n"
MF "v[96:99],a[160:163],v[180:183],v[96:99]\n"
"s_nop 0\n"
TR "v[190:191],v1 offset:2048\n"
"ds_write_addtid_b32 v66 offset:51712\n"
MF "v[96:99],a[200:203],v[184:187],v[96:99]\n"
"s_nop 0\n"
"ds_write_addtid_b32 v67 offset:51968\n"
"ds_write_addtid_b32 v68 offset:52224\n"
MF "v[104:107],a[204:207],v[184:187],v[104:107]\n"
"s_nop 0\n"
TR "a[240:241],v1 offset:4096\n"
"ds_write_addtid_b32 v69 offset:52480\n"
MF "v[104:107],a[164:167],v[180:183],v[104:107]\n"
"s_add_u32 m0,s79,28672\n"
TR "a[242:243],v1 offset:6144\n"
"ds_write_addtid_b32 v70 offset:48640\n"
MF "v[104:107],a[124:127],v[176:179],v[104:107]\n"
LD "v196,s[48:51],s89 offen lds\n"
MF "v[112:115],a[128:131],v[176:179],v[112:115]\n"
"s_nop 0\n"
TR "a[244:245],v1 offset:8192\n"
"ds_write_addtid_b32 v71 offset:48896\n"
MF "v[112:115],a[168:171],v[180:183],v[112:115]\n"
"s_nop 0\n"
TR "a[246:247],v1 offset:10240\n"
"ds_write_addtid_b32 v72 offset:49152\n"
MF "v[112:115],a[208:211],v[184:187],v[112:115]\n"
LD "v197,s[48:51],s89 offen offset:1024 lds\n"
MF "v[120:123],a[212:215],v[184:187],v[120:123]\n"
"s_nop 0\n"
"ds_write_addtid_b32 v73 offset:49408\n"
"ds_write_addtid_b32 v74 offset:49664\n"
MF "v[120:123],a[172:175],v[180:183],v[120:123]\n"
"s_nop 0\n"
"ds_write_addtid_b32 v75 offset:49920\n"
"ds_write_addtid_b32 v76 offset:50176\n"
MF "v[120:123],a[132:135],v[176:179],v[120:123]\n"
"s_nop 0\n"
"ds_write_addtid_b32 v77 offset:50432\n"
"ds_write_addtid_b32 v78 offset:50688\n"
MF "v[128:131],a[136:139],v[176:179],v[128:131]\n"
"s_nop 0\n"
"ds_write_addtid_b32 v79 offset:50944\n"
"ds_write_addtid_b32 v80 offset:51200\n"
MF "v[128:131],a[176:179],v[180:183],v[128:131]\n"
LD "v198,s[48:51],s89 offen offset:2048 lds\n"
MF "v[128:131],a[216:219],v[184:187],v[128:131]\n"
"s_waitcnt lgkmcnt(9)\n"
"ds_write_addtid_b32 v81 offset:51456\n"
"ds_write_addtid_b32 v82 offset:51712\n"
MF "v[132:135],a[216:219],a[244:247],v[132:135]\n"
"s_nop 0\n"
TR "v[176:177],v2\n"
"ds_write_addtid_b32 v83 offset:51968\n"
MF "v[132:135],a[176:179],a[240:243],v[132:135]\n"
"s_nop 0\n"
TR "v[178:179],v2 offset:2048\n"
"ds_write_addtid_b32 v84 offset:52224\n"
MF "v[132:135],a[136:139],v[188:191],v[132:135]\n"
LD "v199,s[48:51],s89 offen offset:3072 lds\n"
MF "v[124:127],a[132:135],v[188:191],v[124:127]\n"
"s_nop 0\n"
TR "v[180:181],v2 offset:4096\n"
"ds_write_addtid_b32 v85 offset:52480\n"
MF "v[124:127],a[172:175],a[240:243],v[124:127]\n"
"s_nop 0\n"
TR "v[182:183],v2 offset:6144\n"
"ds_write_addtid_b32 v86 offset:52736\n"
MF "v[124:127],a[212:215],a[244:247],v[124:127]\n"
"s_nop 0\n"
"ds_write_addtid_b32 v87 offset:52992\n"
"ds_write_addtid_b32 v88 offset:53248\n"
MF "v[116:119],a[208:211],a[244:247],v[116:119]\n"
"s_nop 0\n"
TR "v[184:185],v2 offset:8192\n"
"ds_write_addtid_b32 v89 offset:53504\n"
MF "v[116:119],a[168:171],a[240:243],v[116:119]\n"
"s_add_u32 m0,s79,32768\n"
TR "v[186:187],v2 offset:10240\n"
"ds_write_addtid_b32 v90 offset:49664\n"
MF "v[116:119],a[128:131],v[188:191],v[116:119]\n"
LD "v200,s[48:51],s89 offen lds\n"
MF "v[108:111],a[124:127],v[188:191],v[108:111]\n"
"s_nop 0\n"
"ds_write_addtid_b32 v91 offset:49920\n"
"ds_write_addtid_b32 v92 offset:50176\n"
MF "v[108:111],a[164:167],a[240:243],v[108:111]\n"
"s_nop 0\n"
"ds_write_addtid_b32 v93 offset:50432\n"
"ds_write_addtid_b32 v94 offset:50688\n"
MF "v[108:111],a[204:207],a[244:247],v[108:111]\n"
"ds_write_addtid_b32 v95 offset:50944\n"
MF "v[100:103],a[200:203],a[244:247],v[100:103]\n"
LD "v201,s[48:51],s89 offen offset:1024 lds\n"
MF "v[100:103],a[160:163],a[240:243],v[100:103]\n"
MF "v[100:103],a[120:123],v[188:191],v[100:103]\n"
"s_waitcnt lgkmcnt(6)\n"
MF "v[136:139],a[120:123],v[176:179],v[136:139]\n"
TR "v[188:189],v3\n"
MF "v[136:139],a[160:163],v[180:183],v[136:139]\n"
TR "v[190:191],v3 offset:2048\n"
MF "v[136:139],a[200:203],v[184:187],v[136:139]\n"
LD "v202,s[48:51],s89 offen offset:2048 lds\n"
MF "v[144:147],a[204:207],v[184:187],v[144:147]\n"
TR "a[240:241],v3 offset:4096\n"
MF "v[144:147],a[164:167],v[180:183],v[144:147]\n"
TR "a[242:243],v3 offset:6144\n"
MF "v[144:147],a[124:127],v[176:179],v[144:147]\n"
LD "v203,s[48:51],s89 offen offset:3072 lds\n"
MF "v[152:155],a[128:131],v[176:179],v[152:155]\n"
TR "a[244:245],v3 offset:8192\n"
MF "v[152:155],a[168:171],v[180:183],v[152:155]\n"
TR "a[246:247],v3 offset:10240\n"
MF "v[152:155],a[208:211],v[184:187],v[152:155]\n"
MF "v[160:163],a[212:215],v[184:187],v[160:163]\n"
MF "v[160:163],a[172:175],v[180:183],v[160:163]\n"
MF "v[160:163],a[132:135],v[176:179],v[160:163]\n"
MF "v[168:171],a[136:139],v[176:179],v[168:171]\n"
MF "v[168:171],a[176:179],v[180:183],v[168:171]\n"
MF "v[168:171],a[216:219],v[184:187],v[168:171]\n"
"s_waitcnt vmcnt(25) lgkmcnt(0)\n"
"s_barrier\n"
MF "v[172:175],a[216:219],a[244:247],v[172:175]\n"
"s_add_u32 s80,s89,122880\n"
TR "v[176:177],v0 offset:49152\n"
MF "v[172:175],a[176:179],a[240:243],v[172:175]\n"
"s_add_u32 m0,s79,0\n"
"s_mov_b32 s82,s70\n"
TR "v[178:179],v0 offset:51200\n"
MF "v[172:175],a[136:139],v[188:191],v[172:175]\n"
LD "v192,s[48:51],s80 offen lds\n"
MF "v[164:167],a[132:135],v[188:191],v[164:167]\n"
TR "v[180:181],v0 offset:53248\n"
MF "v[164:167],a[172:175],a[240:243],v[164:167]\n"
TR "v[182:183],v0 offset:55296\n"
MF "v[164:167],a[212:215],a[244:247],v[164:167]\n"
LD "v193,s[48:51],s80 offen offset:1024 lds\n"
MF "v[156:159],a[208:211],a[244:247],v[156:159]\n"
TR "v[184:185],v0 offset:57344\n"
MF "v[156:159],a[168:171],a[240:243],v[156:159]\n"
TR "v[186:187],v0 offset:59392\n"
MF "v[156:159],a[128:131],v[188:191],v[156:159]\n"
MF "v[148:151],a[124:127],v[188:191],v[148:151]\n"
MF "v[148:151],a[164:167],a[240:243],v[148:151]\n"
LD "v194,s[48:51],s80 offen offset:2048 lds\n"
MF "v[148:151],a[204:207],a[244:247],v[148:151]\n"
MF "v[140:143],a[200:203],a[244:247],v[140:143]\n"
MF "v[140:143],a[160:163],a[240:243],v[140:143]\n"
MF "v[140:143],a[120:123],v[188:191],v[140:143]\n"
"s_waitcnt lgkmcnt(0)\n"
LD "v195,s[48:51],s80 offen offset:3072 lds\n"
MF "v[96:99],a[20:23],v[176:179],v[96:99]\n"
TR "v[188:189],v1 offset:49152\n"
MF "v[96:99],a[60:63],v[180:183],v[96:99]\n"
TR "v[190:191],v1 offset:51200\n"
MF "v[96:99],a[100:103],v[184:187],v[96:99]\n"
MF "v[104:107],a[104:107],v[184:187],v[104:107]\n"
TR "a[240:241],v1 offset:53248\n"
MF "v[104:107],a[64:67],v[180:183],v[104:107]\n"
"s_add_u32 m0,s79,4096\n"
TR "a[242:243],v1 offset:55296\n"
MF "v[104:107],a[24:27],v[176:179],v[104:107]\n"
LD "v196,s[48:51],s80 offen lds\n"
MF "v[112:115],a[28:31],v[176:179],v[112:115]\n"
TR "a[244:245],v1 offset:57344\n"
MF "v[112:115],a[68:71],v[180:183],v[112:115]\n"
TR "a[246:247],v1 offset:59392\n"
MF "v[112:115],a[108:111],v[184:187],v[112:115]\n"
LD "v197,s[48:51],s80 offen offset:1024 lds\n"
MF "v[120:123],a[112:115],v[184:187],v[120:123]\n"
MF "v[120:123],a[72:75],v[180:183],v[120:123]\n"
MF "v[120:123],a[32:35],v[176:179],v[120:123]\n"
MF "v[128:131],a[36:39],v[176:179],v[128:131]\n"
MF "v[128:131],a[76:79],v[180:183],v[128:131]\n"
LD "v198,s[48:51],s80 offen offset:2048 lds\n"
MF "v[128:131],a[116:119],v[184:187],v[128:131]\n"
"s_waitcnt lgkmcnt(0)\n"
MF "v[132:135],a[116:119],a[244:247],v[132:135]\n"
TR "v[176:177],v2 offset:49152\n"
MF "v[132:135],a[76:79],a[240:243],v[132:135]\n"
TR "v[178:179],v2 offset:51200\n"
MF "v[132:135],a[36:39],v[188:191],v[132:135]\n"
LD "v199,s[48:51],s80 offen offset:3072 lds\n"
MF "v[124:127],a[32:35],v[188:191],v[124:127]\n"
TR "v[180:181],v2 offset:53248\n"
MF "v[124:127],a[72:75],a[240:243],v[124:127]\n"
TR "v[182:183],v2 offset:55296\n"
MF "v[124:127],a[112:115],a[244:247],v[124:127]\n"
MF "v[116:119],a[108:111],a[244:247],v[116:119]\n"
TR "v[184:185],v2 offset:57344\n"
MF "v[116:119],a[68:71],a[240:243],v[116:119]\n"
"s_add_u32 m0,s79,8192\n"
TR "v[186:187],v2 offset:59392\n"
MF "v[116:119],a[28:31],v[188:191],v[116:119]\n"
LD "v200,s[48:51],s80 offen lds\n"
MF "v[108:111],a[24:27],v[188:191],v[108:111]\n"
MF "v[108:111],a[64:67],a[240:243],v[108:111]\n"
MF "v[108:111],a[104:107],a[244:247],v[108:111]\n"
MF "v[100:103],a[100:103],a[244:247],v[100:103]\n"
LD "v201,s[48:51],s80 offen offset:1024 lds\n"
MF "v[100:103],a[60:63],a[240:243],v[100:103]\n"
"s_waitcnt vmcnt(19)\n"
"s_cmp_lt_u32 s85,6\n"
MF "v[100:103],a[20:23],v[188:191],v[100:103]\n"
"s_cbranch_scc1 .Ljs4\n"
MF "v[96:99],a[252:255],v[208:211],v[96:99]\n"
MF "v[104:107],a[252:255],v[212:215],v[104:107]\n"
MF "v[112:115],a[252:255],v[216:219],v[112:115]\n"
MF "v[120:123],a[252:255],v[220:223],v[120:123]\n"
MF "v[128:131],a[252:255],v[224:227],v[128:131]\n"
MF "v[100:103],a[248:251],v[208:211],v[100:103]\n"
MF "v[108:111],a[248:251],v[212:215],v[108:111]\n"
MF "v[116:119],a[248:251],v[216:219],v[116:119]\n"
MF "v[124:127],a[248:251],v[220:223],v[124:127]\n"
MF "v[132:135],a[248:251],v[224:227],v[132:135]\n"
".Ljs4:\n"
"s_waitcnt lgkmcnt(0)\n"
MF "v[136:139],a[20:23],v[176:179],v[136:139]\n"
TR "v[188:189],v3 offset:49152\n"
"v_sub_u32 v10,v10,v11\n"
MF "v[136:139],a[60:63],v[180:183],v[136:139]\n"
TR "v[190:191],v3 offset:51200\n"
MF "v[136:139],a[100:103],v[184:187],v[136:139]\n"
LD "v202,s[48:51],s80 offen offset:2048 lds\n"
MF "v[144:147],a[104:107],v[184:187],v[144:147]\n"
TR "a[240:241],v3 offset:53248\n"
MF "v[144:147],a[64:67],v[180:183],v[144:147]\n"
TR "a[242:243],v3 offset:55296\n"
MF "v[144:147],a[24:27],v[176:179],v[144:147]\n"
LD "v203,s[48:51],s80 offen offset:3072 lds\n"
MF "v[152:155],a[28:31],v[176:179],v[152:155]\n"
TR "a[244:245],v3 offset:57344\n"
MF "v[152:155],a[68:71],v[180:183],v[152:155]\n"
TR "a[246:247],v3 offset:59392\n"
MF "v[152:155],a[108:111],v[184:187],v[152:155]\n"
".Lnf0:\n"
"v_readlane_b32 s58,v10,s67\n"
"s_cmp_lt_u32 s58,s85\n"
"s_cbranch_scc1 .Lsp0\n"
".Lnb0:\n"
MF "v[160:163],a[112:115],v[184:187],v[160:163]\n"
"s_add_u32 s70,s70,20480\n"
MF "v[160:163],a[72:75],v[180:183],v[160:163]\n"
MF "v[160:163],a[32:35],v[176:179],v[160:163]\n"
MF "v[168:171],a[36:39],v[176:179],v[168:171]\n"
MF "v[168:171],a[76:79],v[180:183],v[168:171]\n"
MF "v[168:171],a[116:119],v[184:187],v[168:171]\n"
"s_waitcnt vmcnt(25) lgkmcnt(0)\n"
"s_barrier\n"
MF "v[172:175],a[116:119],a[244:247],v[172:175]\n"
"s_add_u32 s88,s88,s65\n"
TR "v[176:177],v4\n"
MF "v[172:175],a[76:79],a[240:243],v[172:175]\n"
"s_add_u32 m0,s79,49152\n"
TR "v[178:179],v4 offset:2048\n"
MF "v[172:175],a[36:39],v[188:191],v[172:175]\n"
LD "v192,s[48:51],s88 offen lds\n"
MF "v[164:167],a[32:35],v[188:191],v[164:167]\n"
TR "v[180:181],v4 offset:4096\n"
MF "v[164:167],a[72:75],a[240:243],v[164:167]\n"
TR "v[182:183],v4 offset:6144\n"
MF "v[164:167],a[112:115],a[244:247],v[164:167]\n"
LD "v193,s[48:51],s88 offen offset:1024 lds\n"
MF "v[156:159],a[108:111],a[244:247],v[156:159]\n"
TR "v[184:185],v4 offset:8192\n"
MF "v[156:159],a[68:71],a[240:243],v[156:159]\n"
TR "v[186:187],v4 offset:10240\n"
MF "v[156:159],a[28:31],v[188:191],v[156:159]\n"
LD "v[208:211],v9,s[40:43],s82 offen sc1\n"
MF "v[148:151],a[24:27],v[188:191],v[148:151]\n"
MF "v[148:151],a[64:67],a[240:243],v[148:151]\n"
LD "v194,s[48:51],s88 offen offset:2048 lds\n"
MF "v[148:151],a[104:107],a[244:247],v[148:151]\n"
MF "v[140:143],a[100:103],a[244:247],v[140:143]\n"
MF "v[140:143],a[60:63],a[240:243],v[140:143]\n"
MF "v[140:143],a[20:23],v[188:191],v[140:143]\n"
"s_waitcnt lgkmcnt(0)\n"
LD "v195,s[48:51],s88 offen offset:3072 lds\n"
MF "v[96:99],a[140:143],v[176:179],v[96:99]\n"
TR "v[188:189],v5\n"
MF "v[96:99],a[180:183],v[180:183],v[96:99]\n"
TR "v[190:191],v5 offset:2048\n"
MF "v[96:99],a[220:223],v[184:187],v[96:99]\n"
MF "v[104:107],a[224:227],v[184:187],v[104:107]\n"
TR "a[240:241],v5 offset:4096\n"
MF "v[104:107],a[184:187],v[180:183],v[104:107]\n"
"s_add_u32 m0,s79,53248\n"
TR "a[242:243],v5 offset:6144\n"
MF "v[104:107],a[144:147],v[176:179],v[104:107]\n"
LD "v196,s[48:51],s88 offen lds\n"
MF "v[112:115],a[148:151],v[176:179],v[112:115]\n"
TR "a[244:245],v5 offset:8192\n"
MF "v[112:115],a[188:191],v[180:183],v[112:115]\n"
TR "a[246:247],v5 offset:10240\n"
MF "v[112:115],a[228:231],v[184:187],v[112:115]\n"
LD "v197,s[48:51],s88 offen offset:1024 lds\n"
MF "v[120:123],a[232:235],v[184:187],v[120:123]\n"
LD "v[212:215],v9,s[40:43],s82 offen offset:1024 sc1\n"
MF "v[120:123],a[192:195],v[180:183],v[120:123]\n"
MF "v[120:123],a[152:155],v[176:179],v[120:123]\n"
MF "v[128:131],a[156:159],v[176:179],v[128:131]\n"
MF "v[128:131],a[196:199],v[180:183],v[128:131]\n"
LD "v198,s[48:51],s88 offen offset:2048 lds\n"
MF "v[128:131],a[236:239],v[184:187],v[128:131]\n"
"s_waitcnt lgkmcnt(0)\n"
MF "v[132:135],a[236:239],a[244:247],v[132:135]\n"
TR "v[176:177],v6\n"
MF "v[132:135],a[196:199],a[240:243],v[132:135]\n"
TR "v[178:179],v6 offset:2048\n"
MF "v[132:135],a[156:159],v[188:191],v[132:135]\n"
LD "v199,s[48:51],s88 offen offset:3072 lds\n"
MF "v[124:127],a[152:155],v[188:191],v[124:127]\n"
TR "v[180:181],v6 offset:4096\n"
MF "v[124:127],a[192:195],a[240:243],v[124:127]\n"
TR "v[182:183],v6 offset:6144\n"
MF "v[124:127],a[232:235],a[244:247],v[124:127]\n"
LD "v[216:219],v9,s[40:43],s82 offen offset:2048 sc1\n"
MF "v[116:119],a[228:231],a[244:247],v[116:119]\n"
TR "v[184:185],v6 offset:8192\n"
MF "v[116:119],a[188:191],a[240:243],v[116:119]\n"
"s_add_u32 m0,s79,57344\n"
TR "v[186:187],v6 offset:10240\n"
MF "v[116:119],a[148:151],v[188:191],v[116:119]\n"
LD "v200,s[48:51],s88 offen lds\n"
MF "v[108:111],a[144:147],v[188:191],v[108:111]\n"
MF "v[108:111],a[184:187],a[240:243],v[108:111]\n"
MF "v[108:111],a[224:227],a[244:247],v[108:111]\n"
MF "v[100:103],a[220:223],a[244:247],v[100:103]\n"
LD "v201,s[48:51],s88 offen offset:1024 lds\n"
MF "v[100:103],a[180:183],a[240:243],v[100:103]\n"
MF "v[100:103],a[140:143],v[188:191],v[100:103]\n"
"s_waitcnt lgkmcnt(0)\n"
LD "v[220:223],v9,s[40:43],s82 offen offset:3072 sc1\n"
MF "v[136:139],a[140:143],v[176:179],v[136:139]\n"
TR "v[188:189],v7\n"
MF "v[136:139],a[180:183],v[180:183],v[136:139]\n"
TR "v[190:191],v7 offset:2048\n"
MF "v[136:139],a[220:223],v[184:187],v[136:139]\n"
LD "v202,s[48:51],s88 offen offset:2048 lds\n"
MF "v[144:147],a[224:227],v[184:187],v[144:147]\n"
TR "a[240:241],v7 offset:4096\n"
MF "v[144:147],a[184:187],v[180:183],v[144:147]\n"
TR "a[242:243],v7 offset:6144\n"
MF "v[144:147],a[144:147],v[176:179],v[144:147]\n"
LD "v203,s[48:51],s88 offen offset:3072 lds\n"
MF "v[152:155],a[148:151],v[176:179],v[152:155]\n"
TR "a[244:245],v7 offset:8192\n"
MF "v[152:155],a[188:191],v[180:183],v[152:155]\n"
TR "a[246:247],v7 offset:10240\n"
MF "v[152:155],a[228:231],v[184:187],v[152:155]\n"
MF "v[160:163],a[232:235],v[184:187],v[160:163]\n"
MF "v[160:163],a[192:195],v[180:183],v[160:163]\n"
MF "v[160:163],a[152:155],v[176:179],v[160:163]\n"
MF "v[168:171],a[156:159],v[176:179],v[168:171]\n"
"s_add_u32 s82,s82,4096\n"
MF "v[168:171],a[196:199],v[180:183],v[168:171]\n"
LD "v[224:227],v9,s[40:43],s82 offen sc1\n"
MF "v[168:171],a[236:239],v[184:187],v[168:171]\n"
"s_waitcnt vmcnt(29) lgkmcnt(0)\n"
"s_barrier\n"
MF "v[172:175],a[236:239],a[244:247],v[172:175]\n"
"s_add_u32 s80,s88,122880\n"
TR "v[176:177],v0 offset:24576\n"
MF "v[172:175],a[196:199],a[240:243],v[172:175]\n"
"s_add_u32 m0,s79,98304\n"
"s_add_u32 s85,s85,1\n"
TR "v[178:179],v0 offset:26624\n"
MF "v[172:175],a[156:159],v[188:191],v[172:175]\n"
LD "v192,s[48:51],s80 offen lds\n"
MF "v[164:167],a[152:155],v[188:191],v[164:167]\n"
"s_nop 0\n"
TR "v[180:181],v0 offset:28672\n"
"ds_write_addtid_b32 v96 offset:24576\n"
MF "v[164:167],a[192:195],a[240:243],v[164:167]\n"
"s_nop 0\n"
TR "v[182:183],v0 offset:30720\n"
"ds_write_addtid_b32 v97 offset:24832\n"
MF "v[164:167],a[232:235],a[244:247],v[164:167]\n"
LD "v193,s[48:51],s80 offen offset:1024 lds\n"
MF "v[156:159],a[228:231],a[244:247],v[156:159]\n"
"s_nop 0\n"
TR "v[184:185],v0 offset:32768\n"
"ds_write_addtid_b32 v98 offset:25088\n"
MF "v[156:159],a[188:191],a[240:243],v[156:159]\n"
"s_nop 0\n"
TR "v[186:187],v0 offset:34816\n"
"ds_write_addtid_b32 v99 offset:25344\n"
MF "v[156:159],a[148:151],v[188:191],v[156:159]\n"
"s_nop 0\n"
"ds_write_addtid_b32 v100 offset:25600\n"
"ds_write_addtid_b32 v101 offset:25856\n"
MF "v[148:151],a[144:147],v[188:191],v[148:151]\n"
LD "v194,s[48:51],s80 offen offset:2048 lds\n"
MF "v[148:151],a[184:187],a[240:243],v[148:151]\n"
"s_nop 0\n"
"ds_write_addtid_b32 v102 offset:26112\n"
"ds_write_addtid_b32 v103 offset:26368\n"
MF "v[148:151],a[224:227],a[244:247],v[148:151]\n"
"s_nop 0\n"
"ds_write_addtid_b32 v104 offset:26624\n"
"ds_write_addtid_b32 v105 offset:26880\n"
MF "v[140:143],a[220:223],a[244:247],v[140:143]\n"
"s_nop 0\n"
"ds_write_addtid_b32 v106 offset:27136\n"
"ds_write_addtid_b32 v107 offset:27392\n"
MF "v[140:143],a[180:183],a[240:243],v[140:143]\n"
MF "v[140:143],a[140:143],v[188:191],v[140:143]\n"
"s_waitcnt lgkmcnt(9)\n"
MF "v[16:19],a[0:3],v[176:179],v[252:255]\n"
"s_nop 0\n"
TR "v[188:189],v1 offset:24576\n"
"ds_write_addtid_b32 v108 offset:27648\n"
MF "v[16:19],a[40:43],v[180:183],v[16:19]\n"
"s_nop 0\n"
TR "v[190:191],v1 offset:26624\n"
"ds_write_addtid_b32 v109 offset:27904\n"
MF "v[16:19],a[80:83],v[184:187],v[16:19]\n"
LD "v195,s[48:51],s80 offen offset:3072 lds\n"
MF "v[24:27],a[84:87],v[184:187],v[252:255]\n"
"s_add_u32 s60,s64,1\n"
TR "a[240:241],v1 offset:28672\n"
"ds_write_addtid_b32 v110 offset:28160\n"
MF "v[24:27],a[44:47],v[180:183],v[24:27]\n"
"s_add_u32 m0,s79,102400\n"
TR "a[242:243],v1 offset:30720\n"
"ds_write_addtid_b32 v111 offset:24320\n"
MF "v[24:27],a[4:7],v[176:179],v[24:27]\n"
LD "v196,s[48:51],s80 offen lds\n"
MF "v[32:35],a[8:11],v[176:179],v[252:255]\n"
"s_cmp_eq_u32 s60,8\n"
"s_cselect_b32 s60,0,s60\n"
TR "a[244:245],v1 offset:32768\n"
MF "v[32:35],a[48:51],v[180:183],v[32:35]\n"
"s_cmp_eq_u32 s60,10\n"
"s_cselect_b32 s60,s61,s60\n"
TR "a[246:247],v1 offset:34816\n"
MF "v[32:35],a[88:91],v[184:187],v[32:35]\n"
"s_sub_u32 s65,s60,s64\n"
"ds_write_addtid_b32 v112 offset:24576\n"
"ds_write_addtid_b32 v113 offset:24832\n"
MF "v[40:43],a[92:95],v[184:187],v[252:255]\n"
"s_lshl_b32 s65,s65,7\n"
"ds_write_addtid_b32 v114 offset:25088\n"
"ds_write_addtid_b32 v115 offset:25344\n"
MF "v[40:43],a[52:55],v[180:183],v[40:43]\n"
LD "v197,s[48:51],s80 offen offset:1024 lds\n"
MF "v[40:43],a[12:15],v[176:179],v[40:43]\n"
"s_mov_b32 s64,s60\n"
"ds_write_addtid_b32 v116 offset:25600\n"
"ds_write_addtid_b32 v117 offset:25856\n"
MF "v[48:51],a[16:19],v[176:179],v[252:255]\n"
"s_nop 0\n"
"ds_write_addtid_b32 v118 offset:26112\n"
"ds_write_addtid_b32 v119 offset:26368\n"
MF "v[48:51],a[56:59],v[180:183],v[48:51]\n"
"s_nop 0\n"
"ds_write_addtid_b32 v120 offset:26624\n"
"ds_write_addtid_b32 v121 offset:26880\n"
MF "v[48:51],a[96:99],v[184:187],v[48:51]\n"
"s_waitcnt lgkmcnt(10)\n"
LD "v198,s[48:51],s80 offen offset:2048 lds\n"
MF "v[52:55],a[96:99],a[244:247],v[252:255]\n"
"s_nop 0\n"
TR "v[176:177],v2 offset:24576\n"
"ds_write_addtid_b32 v122 offset:27136\n"
MF "v[52:55],a[56:59],a[240:243],v[52:55]\n"
"s_nop 0\n"
TR "v[178:179],v2 offset:26624\n"
"ds_write_addtid_b32 v123 offset:27392\n"
MF "v[52:55],a[16:19],v[188:191],v[52:55]\n"
"s_nop 0\n"
"ds_write_addtid_b32 v124 offset:27648\n"
"ds_write_addtid_b32 v125 offset:27904\n"
MF "v[44:47],a[12:15],v[188:191],v[252:255]\n"
"s_nop 0\n"
TR "v[180:181],v2 offset:28672\n"
"ds_write_addtid_b32 v126 offset:28160\n"
MF "v[44:47],a[52:55],a[240:243],v[44:47]\n"
"s_nop 0\n"
TR "v[182:183],v2 offset:30720\n"
"ds_write_addtid_b32 v127 offset:28416\n"
MF "v[44:47],a[92:95],a[244:247],v[44:47]\n"
LD "v199,s[48:51],s80 offen offset:3072 lds\n"
MF "v[36:39],a[88:91],a[244:247],v[252:255]\n"
"s_nop 0\n"
TR "v[184:185],v2 offset:32768\n"
"ds_write_addtid_b32 v128 offset:28672\n"
MF "v[36:39],a[48:51],a[240:243],v[36:39]\n"
"s_add_u32 m0,s79,106496\n"
TR "v[186:187],v2 offset:34816\n"
"ds_write_addtid_b32 v129 offset:24832\n"
MF "v[36:39],a[8:11],v[188:191],v[36:39]\n"
LD "v200,s[48:51],s80 offen lds\n"
MF "v[28:31],a[4:7],v[188:191],v[252:255]\n"
"s_nop 0\n"
"ds_write_addtid_b32 v130 offset:25088\n"
"ds_write_addtid_b32 v131 offset:25344\n"
MF "v[28:31],a[44:47],a[240:243],v[28:31]\n"
"s_nop 0\n"
"ds_write_addtid_b32 v132 offset:25600\n"
"ds_write_addtid_b32 v133 offset:25856\n"
MF "v[28:31],a[84:87],a[244:247],v[28:31]\n"
"s_nop 0\n"
"ds_write_addtid_b32 v134 offset:26112\n"
"ds_write_addtid_b32 v135 offset:26368\n"
MF "v[20:23],a[80:83],a[244:247],v[252:255]\n"
MF "v[20:23],a[40:43],a[240:243],v[20:23]\n"
LD "v201,s[48:51],s80 offen offset:1024 lds\n"
MF "v[20:23],a[0:3],v[188:191],v[20:23]\n"
"s_waitcnt lgkmcnt(7)\n"
MF "v[56:59],a[0:3],v[176:179],v[252:255]\n"
TR "v[188:189],v3 offset:24576\n"
MF "v[56:59],a[40:43],v[180:183],v[56:59]\n"
TR "v[190:191],v3 offset:26624\n"
MF "v[56:59],a[80:83],v[184:187],v[56:59]\n"
LD "v202,s[48:51],s80 offen offset:2048 lds\n"
MF "v[64:67],a[84:87],v[184:187],v[252:255]\n"
TR "a[240:241],v3 offset:28672\n"
MF "v[64:67],a[44:47],v[180:183],v[64:67]\n"
TR "a[242:243],v3 offset:30720\n"
MF "v[64:67],a[4:7],v[176:179],v[64:67]\n"
LD "v203,s[48:51],s80 offen offset:3072 lds\n"
MF "v[72:75],a[8:11],v[176:179],v[252:255]\n"
TR "a[244:245],v3 offset:32768\n"
MF "v[72:75],a[48:51],v[180:183],v[72:75]\n"
TR "a[246:247],v3 offset:34816\n"
MF "v[72:75],a[88:91],v[184:187],v[72:75]\n"
MF "v[80:83],a[92:95],v[184:187],v[252:255]\n"
MF "v[80:83],a[52:55],v[180:183],v[80:83]\n"
MF "v[80:83],a[12:15],v[176:179],v[80:83]\n"
MF "v[88:91],a[16:19],v[176:179],v[252:255]\n"
MF "v[88:91],a[56:59],v[180:183],v[88:91]\n"
MF "v[88:91],a[96:99],v[184:187],v[88:91]\n"
"s_waitcnt vmcnt(29) lgkmcnt(0)\n"
"s_barrier\n"
MF "v[92:95],a[96:99],a[244:247],v[252:255]\n"
"s_add_u32 s89,s89,s65\n"
TR "v[176:177],v0\n"
MF "v[92:95],a[56:59],a[240:243],v[92:95]\n"
"s_add_u32 m0,s79,24576\n"
TR "v[178:179],v0 offset:2048\n"
MF "v[92:95],a[16:19],v[188:191],v[92:95]\n"
LD "v192,s[48:51],s89 offen lds\n"
MF "v[84:87],a[12:15],v[188:191],v[252:255]\n"
".Lpg26h:\n"
"s_cbranch_execz .Lpg29h\n"
TR "v[180:181],v0 offset:4096\n"
MF "v[84:87],a[52:55],a[240:243],v[84:87]\n"
TR "v[182:183],v0 offset:6144\n"
MF "v[84:87],a[92:95],a[244:247],v[84:87]\n"
LD "v193,s[48:51],s89 offen offset:1024 lds\n"
MF "v[76:79],a[88:91],a[244:247],v[252:255]\n"
TR "v[184:185],v0 offset:8192\n"
MF "v[76:79],a[48:51],a[240:243],v[76:79]\n"
"s_nop 0\n"
TR "v[186:187],v0 offset:10240\n"
"ds_write_addtid_b32 v136 offset:49152\n"
MF "v[76:79],a[8:11],v[188:191],v[76:79]\n"
"s_nop 0\n"
"ds_write_addtid_b32 v137 offset:49408\n"
"ds_write_addtid_b32 v138 offset:49664\n"
MF "v[68:71],a[4:7],v[188:191],v[252:255]\n"
LD "v194,s[48:51],s89 offen offset:2048 lds\n"
MF "v[68:71],a[44:47],a[240:243],v[68:71]\n"
"buffer_load_dword v10,v12,s[40:43],s86 offen sc1\n"
MF "v[68:71],a[84:87],a[244:247],v[68:71]\n"
"s_nop 0\n"
"ds_write_addtid_b32 v139 offset:49920\n"
"ds_write_addtid_b32 v140 offset:50176\n"
MF "v[60:63],a[80:83],a[244:247],v[252:255]\n"
"s_nop 0\n"
"ds_write_addtid_b32 v141 offset:50432\n"
"ds_write_addtid_b32 v142 offset:50688\n"
MF "v[60:63],a[40:43],a[240:243],v[60:63]\n"
"s_nop 0\n"
"ds_write_addtid_b32 v143 offset:50944\n"
"ds_write_addtid_b32 v144 offset:51200\n"
MF "v[60:63],a[0:3],v[188:191],v[60:63]\n"
"s_waitcnt lgkmcnt(9)\n"
LD "v195,s[48:51],s89 offen offset:3072 lds\n"
MF "v[16:19],a[120:123],v[176:179],v[16:19]\n"
"s_nop 0\n"
TR "v[188:189],v1\n"
"ds_write_addtid_b32 v145 offset:51456\n"
MF "v[16:19],a[160:163],v[180:183],v[16:19]\n"
"s_nop 0\n"
TR "v[190:191],v1 offset:2048\n"
"ds_write_addtid_b32 v146 offset:51712\n"
MF "v[16:19],a[200:203],v[184:187],v[16:19]\n"
"s_nop 0\n"
"ds_write_addtid_b32 v147 offset:51968\n"
"ds_write_addtid_b32 v148 offset:52224\n"
MF "v[24:27],a[204:207],v[184:187],v[24:27]\n"
"s_nop 0\n"
TR "a[240:241],v1 offset:4096\n"
"ds_write_addtid_b32 v149 offset:52480\n"
MF "v[24:27],a[164:167],v[180:183],v[24:27]\n"
"s_add_u32 m0,s79,28672\n"
TR "a[242:243],v1 offset:6144\n"
"ds_write_addtid_b32 v150 offset:48640\n"
MF "v[24:27],a[124:127],v[176:179],v[24:27]\n"
LD "v196,s[48:51],s89 offen lds\n"
MF "v[32:35],a[128:131],v[176:179],v[32:35]\n"
"s_nop 0\n"
TR "a[244:245],v1 offset:8192\n"
"ds_write_addtid_b32 v151 offset:48896\n"
MF "v[32:35],a[168:171],v[180:183],v[32:35]\n"
"s_nop 0\n"
TR "a[246:247],v1 offset:10240\n"
"ds_write_addtid_b32 v152 offset:49152\n"
MF "v[32:35],a[208:211],v[184:187],v[32:35]\n"
LD "v197,s[48:51],s89 offen offset:1024 lds\n"
MF "v[40:43],a[212:215],v[184:187],v[40:43]\n"
"s_nop 0\n"
"ds_write_addtid_b32 v153 offset:49408\n"
"ds_write_addtid_b32 v154 offset:49664\n"
MF "v[40:43],a[172:175],v[180:183],v[40:43]\n"
"s_nop 0\n"
"ds_write_addtid_b32 v155 offset:49920\n"
"ds_write_addtid_b32 v156 offset:50176\n"
MF "v[40:43],a[132:135],v[176:179],v[40:43]\n"
"s_nop 0\n"
"ds_write_addtid_b32 v157 offset:50432\n"
"ds_write_addtid_b32 v158 offset:50688\n"
MF "v[48:51],a[136:139],v[176:179],v[48:51]\n"
"s_nop 0\n"
"ds_write_addtid_b32 v159 offset:50944\n"
"ds_write_addtid_b32 v160 offset:51200\n"
MF "v[48:51],a[176:179],v[180:183],v[48:51]\n"
LD "v198,s[48:51],s89 offen offset:2048 lds\n"
MF "v[48:51],a[216:219],v[184:187],v[48:51]\n"
"s_waitcnt lgkmcnt(9)\n"
"ds_write_addtid_b32 v161 offset:51456\n"
"ds_write_addtid_b32 v162 offset:51712\n"
MF "v[52:55],a[216:219],a[244:247],v[52:55]\n"
"s_nop 0\n"
TR "v[176:177],v2\n"
"ds_write_addtid_b32 v163 offset:51968\n"
MF "v[52:55],a[176:179],a[240:243],v[52:55]\n"
"s_nop 0\n"
TR "v[178:179],v2 offset:2048\n"
"ds_write_addtid_b32 v164 offset:52224\n"
MF "v[52:55],a[136:139],v[188:191],v[52:55]\n"
LD "v199,s[48:51],s89 offen offset:3072 lds\n"
MF "v[44:47],a[132:135],v[188:191],v[44:47]\n"
"s_nop 0\n"
TR "v[180:181],v2 offset:4096\n"
"ds_write_addtid_b32 v165 offset:52480\n"
MF "v[44:47],a[172:175],a[240:243],v[44:47]\n"
"s_nop 0\n"
TR "v[182:183],v2 offset:6144\n"
"ds_write_addtid_b32 v166 offset:52736\n"
MF "v[44:47],a[212:215],a[244:247],v[44:47]\n"
"s_nop 0\n"
"ds_write_addtid_b32 v167 offset:52992\n"
"ds_write_addtid_b32 v168 offset:53248\n"
MF "v[36:39],a[208:211],a[244:247],v[36:39]\n"
"s_nop 0\n"
TR "v[184:185],v2 offset:8192\n"
"ds_write_addtid_b32 v169 offset:53504\n"
MF "v[36:39],a[168:171],a[240:243],v[36:39]\n"
"s_add_u32 m0,s79,32768\n"
TR "v[186:187],v2 offset:10240\n"
"ds_write_addtid_b32 v170 offset:49664\n"
MF "v[36:39],a[128:131],v[188:191],v[36:39]\n"
LD "v200,s[48:51],s89 offen lds\n"
MF "v[28:31],a[124:127],v[188:191],v[28:31]\n"
"s_nop 0\n"
"ds_write_addtid_b32 v171 offset:49920\n"
"ds_write_addtid_b32 v172 offset:50176\n"
MF "v[28:31],a[164:167],a[240:243],v[28:31]\n"
"s_nop 0\n"
"ds_write_addtid_b32 v173 offset:50432\n"
"ds_write_addtid_b32 v174 offset:50688\n"
MF "v[28:31],a[204:207],a[244:247],v[28:31]\n"
"ds_write_addtid_b32 v175 offset:50944\n"
MF "v[20:23],a[200:203],a[244:247],v[20:23]\n"
LD "v201,s[48:51],s89 offen offset:1024 lds\n"
MF "v[20:23],a[160:163],a[240:243],v[20:23]\n"
MF "v[20:23],a[120:123],v[188:191],v[20:23]\n"
"s_waitcnt lgkmcnt(6)\n"
MF "v[56:59],a[120:123],v[176:179],v[56:59]\n"
TR "v[188:189],v3\n"
MF "v[56:59],a[160:163],v[180:183],v[56:59]\n"
TR "v[190:191],v3 offset:2048\n"
MF "v[56:59],a[200:203],v[184:187],v[56:59]\n"
LD "v202,s[48:51],s89 offen offset:2048 lds\n"
MF "v[64:67],a[204:207],v[184:187],v[64:67]\n"
TR "a[240:241],v3 offset:4096\n"
MF "v[64:67],a[164:167],v[180:183],v[64:67]\n"
TR "a[242:243],v3 offset:6144\n"
MF "v[64:67],a[124:127],v[176:179],v[64:67]\n"
LD "v203,s[48:51],s89 offen offset:3072 lds\n"
MF "v[72:75],a[128:131],v[176:179],v[72:75]\n"
TR "a[244:245],v3 offset:8192\n"
MF "v[72:75],a[168:171],v[180:183],v[72:75]\n"
TR "a[246:247],v3 offset:10240\n"
MF "v[72:75],a[208:211],v[184:187],v[72:75]\n"
MF "v[80:83],a[212:215],v[184:187],v[80:83]\n"
MF "v[80:83],a[172:175],v[180:183],v[80:83]\n"
MF "v[80:83],a[132:135],v[176:179],v[80:83]\n"
MF "v[88:91],a[136:139],v[176:179],v[88:91]\n"
MF "v[88:91],a[176:179],v[180:183],v[88:91]\n"
MF "v[88:91],a[216:219],v[184:187],v[88:91]\n"
"s_waitcnt vmcnt(26) lgkmcnt(0)\n"
"s_barrier\n"
MF "v[92:95],a[216:219],a[244:247],v[92:95]\n"
"s_add_u32 s80,s89,122880\n"
TR "v[176:177],v0 offset:49152\n"
MF "v[92:95],a[176:179],a[240:243],v[92:95]\n"
"s_add_u32 m0,s79,0\n"
"s_mov_b32 s82,s70\n"
TR "v[178:179],v0 offset:51200\n"
MF "v[92:95],a[136:139],v[188:191],v[92:95]\n"
LD "v192,s[48:51],s80 offen lds\n"
MF "v[84:87],a[132:135],v[188:191],v[84:87]\n"
TR "v[180:181],v0 offset:53248\n"
MF "v[84:87],a[172:175],a[240:243],v[84:87]\n"
TR "v[182:183],v0 offset:55296\n"
MF "v[84:87],a[212:215],a[244:247],v[84:87]\n"
LD "v193,s[48:51],s80 offen offset:1024 lds\n"
MF "v[76:79],a[208:211],a[244:247],v[76:79]\n"
TR "v[184:185],v0 offset:57344\n"
MF "v[76:79],a[168:171],a[240:243],v[76:79]\n"
TR "v[186:187],v0 offset:59392\n"
MF "v[76:79],a[128:131],v[188:191],v[76:79]\n"
MF "v[68:71],a[124:127],v[188:191],v[68:71]\n"
MF "v[68:71],a[164:167],a[240:243],v[68:71]\n"
LD "v194,s[48:51],s80 offen offset:2048 lds\n"
MF "v[68:71],a[204:207],a[244:247],v[68:71]\n"
MF "v[60:63],a[200:203],a[244:247],v[60:63]\n"
MF "v[60:63],a[160:163],a[240:243],v[60:63]\n"
MF "v[60:63],a[120:123],v[188:191],v[60:63]\n"
"s_waitcnt lgkmcnt(0)\n"
LD "v195,s[48:51],s80 offen offset:3072 lds\n"
MF "v[16:19],a[20:23],v[176:179],v[16:19]\n"
TR "v[188:189],v1 offset:49152\n"
MF "v[16:19],a[60:63],v[180:183],v[16:19]\n"
TR "v[190:191],v1 offset:51200\n"
MF "v[16:19],a[100:103],v[184:187],v[16:19]\n"
MF "v[24:27],a[104:107],v[184:187],v[24:27]\n"
TR "a[240:241],v1 offset:53248\n"
MF "v[24:27],a[64:67],v[180:183],v[24:27]\n"
"s_add_u32 m0,s79,4096\n"
TR "a[242:243],v1 offset:55296\n"
MF "v[24:27],a[24:27],v[176:179],v[24:27]\n"
LD "v196,s[48:51],s80 offen lds\n"
MF "v[32:35],a[28:31],v[176:179],v[32:35]\n"
TR "a[244:245],v1 offset:57344\n"
MF "v[32:35],a[68:71],v[180:183],v[32:35]\n"
TR "a[246:247],v1 offset:59392\n"
MF "v[32:35],a[108:111],v[184:187],v[32:35]\n"
LD "v197,s[48:51],s80 offen offset:1024 lds\n"
MF "v[40:43],a[112:115],v[184:187],v[40:43]\n"
MF "v[40:43],a[72:75],v[180:183],v[40:43]\n"
MF "v[40:43],a[32:35],v[176:179],v[40:43]\n"
MF "v[48:51],a[36:39],v[176:179],v[48:51]\n"
MF "v[48:51],a[76:79],v[180:183],v[48:51]\n"
LD "v198,s[48:51],s80 offen offset:2048 lds\n"
MF "v[48:51],a[116:119],v[184:187],v[48:51]\n"
"s_waitcnt lgkmcnt(0)\n"
MF "v[52:55],a[116:119],a[244:247],v[52:55]\n"
TR "v[176:177],v2 offset:49152\n"
MF "v[52:55],a[76:79],a[240:243],v[52:55]\n"
TR "v[178:179],v2 offset:51200\n"
MF "v[52:55],a[36:39],v[188:191],v[52:55]\n"
LD "v199,s[48:51],s80 offen offset:3072 lds\n"
MF "v[44:47],a[32:35],v[188:191],v[44:47]\n"
TR "v[180:181],v2 offset:53248\n"
MF "v[44:47],a[72:75],a[240:243],v[44:47]\n"
TR "v[182:183],v2 offset:55296\n"
MF "v[44:47],a[112:115],a[244:247],v[44:47]\n"
MF "v[36:39],a[108:111],a[244:247],v[36:39]\n"
TR "v[184:185],v2 offset:57344\n"
MF "v[36:39],a[68:71],a[240:243],v[36:39]\n"
"s_add_u32 m0,s79,8192\n"
TR "v[186:187],v2 offset:59392\n"
MF "v[36:39],a[28:31],v[188:191],v[36:39]\n"
LD "v200,s[48:51],s80 offen lds\n"
MF "v[28:31],a[24:27],v[188:191],v[28:31]\n"
MF "v[28:31],a[64:67],a[240:243],v[28:31]\n"
MF "v[28:31],a[104:107],a[244:247],v[28:31]\n"
MF "v[20:23],a[100:103],a[244:247],v[20:23]\n"
LD "v201,s[48:51],s80 offen offset:1024 lds\n"
MF "v[20:23],a[60:63],a[240:243],v[20:23]\n"
"s_waitcnt vmcnt(19)\n"
MF "v[20:23],a[20:23],v[188:191],v[20:23]\n"
MF "v[16:19],a[252:255],v[208:211],v[16:19]\n"
MF "v[24:27],a[252:255],v[212:215],v[24:27]\n"
MF "v[32:35],a[252:255],v[216:219],v[32:35]\n"
MF "v[40:43],a[252:255],v[220:223],v[40:43]\n"
MF "v[48:51],a[252:255],v[224:227],v[48:51]\n"
MF "v[20:23],a[248:251],v[208:211],v[20:23]\n"
MF "v[28:31],a[248:251],v[212:215],v[28:31]\n"
MF "v[36:39],a[248:251],v[216:219],v[36:39]\n"
MF "v[44:47],a[248:251],v[220:223],v[44:47]\n"
MF "v[52:55],a[248:251],v[224:227],v[52:55]\n"
"s_waitcnt lgkmcnt(0)\n"
MF "v[56:59],a[20:23],v[176:179],v[56:59]\n"
TR "v[188:189],v3 offset:49152\n"
"v_sub_u32 v10,v10,v11\n"
MF "v[56:59],a[60:63],v[180:183],v[56:59]\n"
TR "v[190:191],v3 offset:51200\n"
MF "v[56:59],a[100:103],v[184:187],v[56:59]\n"
LD "v202,s[48:51],s80 offen offset:2048 lds\n"
MF "v[64:67],a[104:107],v[184:187],v[64:67]\n"
TR "a[240:241],v3 offset:53248\n"
MF "v[64:67],a[64:67],v[180:183],v[64:67]\n"
TR "a[242:243],v3 offset:55296\n"
MF "v[64:67],a[24:27],v[176:179],v[64:67]\n"
LD "v203,s[48:51],s80 offen offset:3072 lds\n"
MF "v[72:75],a[28:31],v[176:179],v[72:75]\n"
TR "a[244:245],v3 offset:57344\n"
MF "v[72:75],a[68:71],v[180:183],v[72:75]\n"
TR "a[246:247],v3 offset:59392\n"
MF "v[72:75],a[108:111],v[184:187],v[72:75]\n"
".Lnf1:\n"
"v_readlane_b32 s58,v10,s67\n"
"s_cmp_lt_u32 s58,s85\n"
"s_cbranch_scc1 .Lsp1\n"
".Lnb1:\n"
MF "v[80:83],a[112:115],v[184:187],v[80:83]\n"
"s_add_u32 s70,s70,20480\n"
MF "v[80:83],a[72:75],v[180:183],v[80:83]\n"
MF "v[80:83],a[32:35],v[176:179],v[80:83]\n"
MF "v[88:91],a[36:39],v[176:179],v[88:91]\n"
MF "v[88:91],a[76:79],v[180:183],v[88:91]\n"
MF "v[88:91],a[116:119],v[184:187],v[88:91]\n"
"s_waitcnt vmcnt(25) lgkmcnt(0)\n"
"s_barrier\n"
MF "v[92:95],a[116:119],a[244:247],v[92:95]\n"
"s_add_u32 s88,s88,s65\n"
TR "v[176:177],v4\n"
MF "v[92:95],a[76:79],a[240:243],v[92:95]\n"
"s_add_u32 m0,s79,49152\n"
TR "v[178:179],v4 offset:2048\n"
MF "v[92:95],a[36:39],v[188:191],v[92:95]\n"
LD "v192,s[48:51],s88 offen lds\n"
MF "v[84:87],a[32:35],v[188:191],v[84:87]\n"
TR "v[180:181],v4 offset:4096\n"
MF "v[84:87],a[72:75],a[240:243],v[84:87]\n"
TR "v[182:183],v4 offset:6144\n"
MF "v[84:87],a[112:115],a[244:247],v[84:87]\n"
LD "v193,s[48:51],s88 offen offset:1024 lds\n"
MF "v[76:79],a[108:111],a[244:247],v[76:79]\n"
TR "v[184:185],v4 offset:8192\n"
MF "v[76:79],a[68:71],a[240:243],v[76:79]\n"
TR "v[186:187],v4 offset:10240\n"
MF "v[76:79],a[28:31],v[188:191],v[76:79]\n"
LD "v[208:211],v9,s[40:43],s82 offen sc1\n"
MF "v[68:71],a[24:27],v[188:191],v[68:71]\n"
MF "v[68:71],a[64:67],a[240:243],v[68:71]\n"
LD "v194,s[48:51],s88 offen offset:2048 lds\n"
MF "v[68:71],a[104:107],a[244:247],v[68:71]\n"
MF "v[60:63],a[100:103],a[244:247],v[60:63]\n"
MF "v[60:63],a[60:63],a[240:243],v[60:63]\n"
MF "v[60:63],a[20:23],v[188:191],v[60:63]\n"
"s_waitcnt lgkmcnt(0)\n"
LD "v195,s[48:51],s88 offen offset:3072 lds\n"
MF "v[16:19],a[140:143],v[176:179],v[16:19]\n"
TR "v[188:189],v5\n"
MF "v[16:19],a[180:183],v[180:183],v[16:19]\n"
TR "v[190:191],v5 offset:2048\n"
MF "v[16:19],a[220:223],v[184:187],v[16:19]\n"
MF "v[24:27],a[224:227],v[184:187],v[24:27]\n"
TR "a[240:241],v5 offset:4096\n"
MF "v[24:27],a[184:187],v[180:183],v[24:27]\n"
"s_add_u32 m0,s79,53248\n"
TR "a[242:243],v5 offset:6144\n"
MF "v[24:27],a[144:147],v[176:179],v[24:27]\n"
LD "v196,s[48:51],s88 offen lds\n"
MF "v[32:35],a[148:151],v[176:179],v[32:35]\n"
TR "a[244:245],v5 offset:8192\n"
MF "v[32:35],a[188:191],v[180:183],v[32:35]\n"
TR "a[246:247],v5 offset:10240\n"
MF "v[32:35],a[228:231],v[184:187],v[32:35]\n"
LD "v197,s[48:51],s88 offen offset:1024 lds\n"
MF "v[40:43],a[232:235],v[184:187],v[40:43]\n"
LD "v[212:215],v9,s[40:43],s82 offen offset:1024 sc1\n"
MF "v[40:43],a[192:195],v[180:183],v[40:43]\n"
MF "v[40:43],a[152:155],v[176:179],v[40:43]\n"
MF "v[48:51],a[156:159],v[176:179],v[48:51]\n"
MF "v[48:51],a[196:199],v[180:183],v[48:51]\n"
LD "v198,s[48:51],s88 offen offset:2048 lds\n"
MF "v[48:51],a[236:239],v[184:187],v[48:51]\n"
"s_waitcnt lgkmcnt(0)\n"
MF "v[52:55],a[236:239],a[244:247],v[52:55]\n"
TR "v[176:177],v6\n"
MF "v[52:55],a[196:199],a[240:243],v[52:55]\n"
TR "v[178:179],v6 offset:2048\n"
MF "v[52:55],a[156:159],v[188:191],v[52:55]\n"
LD "v199,s[48:51],s88 offen offset:3072 lds\n"
MF "v[44:47],a[152:155],v[188:191],v[44:47]\n"
TR "v[180:181],v6 offset:4096\n"
MF "v[44:47],a[192:195],a[240:243],v[44:47]\n"
TR "v[182:183],v6 offset:6144\n"
MF "v[44:47],a[232:235],a[244:247],v[44:47]\n"
LD "v[216:219],v9,s[40:43],s82 offen offset:2048 sc1\n"
MF "v[36:39],a[228:231],a[244:247],v[36:39]\n"
TR "v[184:185],v6 offset:8192\n"
MF "v[36:39],a[188:191],a[240:243],v[36:39]\n"
"s_add_u32 m0,s79,57344\n"
TR "v[186:187],v6 offset:10240\n"
MF "v[36:39],a[148:151],v[188:191],v[36:39]\n"
LD "v200,s[48:51],s88 offen lds\n"
MF "v[28:31],a[144:147],v[188:191],v[28:31]\n"
MF "v[28:31],a[184:187],a[240:243],v[28:31]\n"
MF "v[28:31],a[224:227],a[244:247],v[28:31]\n"
MF "v[20:23],a[220:223],a[244:247],v[20:23]\n"
LD "v201,s[48:51],s88 offen offset:1024 lds\n"
MF "v[20:23],a[180:183],a[240:243],v[20:23]\n"
MF "v[20:23],a[140:143],v[188:191],v[20:23]\n"
"s_waitcnt lgkmcnt(0)\n"
LD "v[220:223],v9,s[40:43],s82 offen offset:3072 sc1\n"
MF "v[56:59],a[140:143],v[176:179],v[56:59]\n"
TR "v[188:189],v7\n"
MF "v[56:59],a[180:183],v[180:183],v[56:59]\n"
TR "v[190:191],v7 offset:2048\n"
MF "v[56:59],a[220:223],v[184:187],v[56:59]\n"
LD "v202,s[48:51],s88 offen offset:2048 lds\n"
MF "v[64:67],a[224:227],v[184:187],v[64:67]\n"
TR "a[240:241],v7 offset:4096\n"
MF "v[64:67],a[184:187],v[180:183],v[64:67]\n"
TR "a[242:243],v7 offset:6144\n"
MF "v[64:67],a[144:147],v[176:179],v[64:67]\n"
LD "v203,s[48:51],s88 offen offset:3072 lds\n"
MF "v[72:75],a[148:151],v[176:179],v[72:75]\n"
TR "a[244:245],v7 offset:8192\n"
MF "v[72:75],a[188:191],v[180:183],v[72:75]\n"
TR "a[246:247],v7 offset:10240\n"
MF "v[72:75],a[228:231],v[184:187],v[72:75]\n"
MF "v[80:83],a[232:235],v[184:187],v[80:83]\n"
MF "v[80:83],a[192:195],v[180:183],v[80:83]\n"
MF "v[80:83],a[152:155],v[176:179],v[80:83]\n"
MF "v[88:91],a[156:159],v[176:179],v[88:91]\n"
"s_add_u32 s82,s82,4096\n"
MF "v[88:91],a[196:199],v[180:183],v[88:91]\n"
LD "v[224:227],v9,s[40:43],s82 offen sc1\n"
MF "v[88:91],a[236:239],v[184:187],v[88:91]\n"
"s_waitcnt vmcnt(29) lgkmcnt(0)\n"
"s_barrier\n"
MF "v[92:95],a[236:239],a[244:247],v[92:95]\n"
"s_add_u32 s80,s88,122880\n"
TR "v[176:177],v0 offset:24576\n"
MF "v[92:95],a[196:199],a[240:243],v[92:95]\n"
"s_add_u32 m0,s79,98304\n"
"s_add_u32 s85,s85,1\n"
TR "v[178:179],v0 offset:26624\n"
MF "v[92:95],a[156:159],v[188:191],v[92:95]\n"
LD "v192,s[48:51],s80 offen lds\n"
MF "v[84:87],a[152:155],v[188:191],v[84:87]\n"
"s_nop 0\n"
TR "v[180:181],v0 offset:28672\n"
"ds_write_addtid_b32 v16 offset:24576\n"
MF "v[84:87],a[192:195],a[240:243],v[84:87]\n"
"s_nop 0\n"
TR "v[182:183],v0 offset:30720\n"
"ds_write_addtid_b32 v17 offset:24832\n"
MF "v[84:87],a[232:235],a[244:247],v[84:87]\n"
LD "v193,s[48:51],s80 offen offset:1024 lds\n"
MF "v[76:79],a[228:231],a[244:247],v[76:79]\n"
"s_nop 0\n"
TR "v[184:185],v0 offset:32768\n"
"ds_write_addtid_b32 v18 offset:25088\n"
MF "v[76:79],a[188:191],a[240:243],v[76:79]\n"
"s_nop 0\n"
TR "v[186:187],v0 offset:34816\n"
"ds_write_addtid_b32 v19 offset:25344\n"
MF "v[76:79],a[148:151],v[188:191],v[76:79]\n"
"s_nop 0\n"
"ds_write_addtid_b32 v20 offset:25600\n"
"ds_write_addtid_b32 v21 offset:25856\n"
MF "v[68:71],a[144:147],v[188:191],v[68:71]\n"
LD "v194,s[48:51],s80 offen offset:2048 lds\n"
MF "v[68:71],a[184:187],a[240:243],v[68:71]\n"
"s_nop 0\n"
"ds_write_addtid_b32 v22 offset:26112\n"
"ds_write_addtid_b32 v23 offset:26368\n"
MF "v[68:71],a[224:227],a[244:247],v[68:71]\n"
"s_nop 0\n"
"ds_write_addtid_b32 v24 offset:26624\n"
"ds_write_addtid_b32 v25 offset:26880\n"
MF "v[60:63],a[220:223],a[244:247],v[60:63]\n"
"s_nop 0\n"
"ds_write_addtid_b32 v26 offset:27136\n"
"ds_write_addtid_b32 v27 offset:27392\n"
MF "v[60:63],a[180:183],a[240:243],v[60:63]\n"
MF "v[60:63],a[140:143],v[188:191],v[60:63]\n"
"s_waitcnt lgkmcnt(9)\n"
"s_cmp_lg_u32 s85,9\n"
"s_cbranch_scc1 .Lpl09\n"
".Lplast0:\n"
MF "v[96:99],v[176:179],a[0:3],v[252:255]\n"
"s_nop 0\n"
TR "v[188:189],v1 offset:24576\n"
TR "v[190:191],v1 offset:26624\n"
MF "v[96:99],v[180:183],a[40:43],v[96:99]\n"
"s_nop 0\n"
TR "a[240:241],v1 offset:28672\n"
TR "a[242:243],v1 offset:30720\n"
MF "v[96:99],v[184:187],a[80:83],v[96:99]\n"
"s_add_u32 s90,s91,163840\n"
TR "a[244:245],v1 offset:32768\n"
TR "a[246:247],v1 offset:34816\n"
MF "v[104:107],v[184:187],a[84:87],v[252:255]\n"
LD "v195,s[48:51],s80 offen offset:3072 lds\n"
MF "v[104:107],v[180:183],a[44:47],v[104:107]\n"
"s_nop 0\n"
"ds_write_addtid_b32 v28 offset:27648\n"
"ds_write_addtid_b32 v29 offset:27904\n"
MF "v[104:107],v[176:179],a[4:7],v[104:107]\n"
"s_nop 0\n"
"ds_write_addtid_b32 v30 offset:28160\n"
"ds_write_addtid_b32 v31 offset:28416\n"
MF "v[112:115],v[176:179],a[8:11],v[252:255]\n"
"s_nop 0\n"
"ds_write_addtid_b32 v32 offset:28672\n"
"ds_write_addtid_b32 v33 offset:28928\n"
MF "v[112:115],v[180:183],a[48:51],v[112:115]\n"
"s_add_u32 m0,s79,102400\n"
"s_nop 0\n"
"ds_write_addtid_b32 v34 offset:25088\n"
"ds_write_addtid_b32 v35 offset:25344\n"
MF "v[112:115],v[184:187],a[88:91],v[112:115]\n"
LD "v196,s[48:51],s80 offen lds\n"
MF "v[120:123],v[184:187],a[92:95],v[252:255]\n"
"s_nop 0\n"
"ds_write_addtid_b32 v36 offset:25600\n"
"ds_write_addtid_b32 v37 offset:25856\n"
MF "v[120:123],v[180:183],a[52:55],v[120:123]\n"
"ds_write_addtid_b32 v38 offset:26112\n"
MF "v[120:123],v[176:179],a[12:15],v[120:123]\n"
"ds_write_addtid_b32 v39 offset:26368\n"
MF "v[128:131],v[176:179],a[16:19],v[252:255]\n"
"ds_write_addtid_b32 v40 offset:26624\n"
MF "v[128:131],v[180:183],a[56:59],v[128:131]\n"
"ds_write_addtid_b32 v41 offset:26880\n"
MF "v[128:131],v[184:187],a[96:99],v[128:131]\n"
"s_waitcnt lgkmcnt(14)\n"
LD "v197,s[48:51],s80 offen offset:1024 lds\n"
MF "v[132:135],a[244:247],a[96:99],v[252:255]\n"
"s_nop 0\n"
TR "v[176:177],v2 offset:24576\n"
TR "v[178:179],v2 offset:26624\n"
MF "v[132:135],a[240:243],a[56:59],v[132:135]\n"
"s_nop 0\n"
TR "v[180:181],v2 offset:28672\n"
TR "v[182:183],v2 offset:30720\n"
MF "v[132:135],v[188:191],a[16:19],v[132:135]\n"
"s_nop 0\n"
TR "v[184:185],v2 offset:32768\n"
TR "v[186:187],v2 offset:34816\n"
MF "v[124:127],v[188:191],a[12:15],v[252:255]\n"
"s_nop 0\n"
"ds_write_addtid_b32 v42 offset:27136\n"
"ds_write_addtid_b32 v43 offset:27392\n"
MF "v[124:127],a[240:243],a[52:55],v[124:127]\n"
"s_nop 0\n"
"ds_write_addtid_b32 v44 offset:27648\n"
"ds_write_addtid_b32 v45 offset:27904\n"
MF "v[124:127],a[244:247],a[92:95],v[124:127]\n"
"s_nop 0\n"
"ds_write_addtid_b32 v46 offset:28160\n"
"ds_write_addtid_b32 v47 offset:28416\n"
MF "v[116:119],a[244:247],a[88:91],v[252:255]\n"
LD "v198,s[48:51],s80 offen offset:2048 lds\n"
MF "v[116:119],a[240:243],a[48:51],v[116:119]\n"
"s_nop 0\n"
"ds_write_addtid_b32 v48 offset:28672\n"
"ds_write_addtid_b32 v49 offset:28928\n"
MF "v[116:119],v[188:191],a[8:11],v[116:119]\n"
"s_nop 0\n"
"ds_write_addtid_b32 v50 offset:29184\n"
"ds_write_addtid_b32 v51 offset:29440\n"
MF "v[108:111],v[188:191],a[4:7],v[252:255]\n"
"ds_write_addtid_b32 v52 offset:29696\n"
MF "v[108:111],a[240:243],a[44:47],v[108:111]\n"
"ds_write_addtid_b32 v53 offset:29952\n"
MF "v[108:111],a[244:247],a[84:87],v[108:111]\n"
"ds_write_addtid_b32 v54 offset:30208\n"
MF "v[100:103],a[244:247],a[80:83],v[252:255]\n"
"ds_write_addtid_b32 v55 offset:30464\n"
MF "v[100:103],a[240:243],a[40:43],v[100:103]\n"
LD "v199,s[48:51],s80 offen offset:3072 lds\n"
MF "v[100:103],v[188:191],a[0:3],v[100:103]\n"
"s_waitcnt lgkmcnt(14)\n"
MF "v[136:139],v[176:179],a[0:3],v[252:255]\n"
"s_nop 0\n"
TR "v[188:189],v3 offset:24576\n"
TR "v[190:191],v3 offset:26624\n"
MF "v[136:139],v[180:183],a[40:43],v[136:139]\n"
"s_nop 0\n"
TR "a[240:241],v3 offset:28672\n"
TR "a[242:243],v3 offset:30720\n"
MF "v[136:139],v[184:187],a[80:83],v[136:139]\n"
"s_add_u32 m0,s79,106496\n"
TR "a[244:245],v3 offset:32768\n"
TR "a[246:247],v3 offset:34816\n"
MF "v[144:147],v[184:187],a[84:87],v[252:255]\n"
".Lpg27h:\n"
"s_cbranch_execz .Lpg30h\n"
"s_waitcnt lgkmcnt(0)\n"
"s_barrier\n"
MF "v[144:147],v[180:183],a[44:47],v[144:147]\n"
LD "v200,s[48:51],s80 offen lds\n"
MF "v[144:147],v[176:179],a[4:7],v[144:147]\n"
"ds_read2st64_b32 v[16:17],v13 offset0:0 offset1:1\n"
MF "v[152:155],v[176:179],a[8:11],v[252:255]\n"
"ds_read2st64_b32 v[18:19],v13 offset0:2 offset1:3\n"
MF "v[152:155],v[180:183],a[48:51],v[152:155]\n"
"ds_read2st64_b32 v[20:21],v13 offset0:4 offset1:5\n"
MF "v[152:155],v[184:187],a[88:91],v[152:155]\n"
"ds_read2st64_b32 v[22:23],v13 offset0:6 offset1:7\n"
MF "v[160:163],v[184:187],a[92:95],v[252:255]\n"
"ds_read2st64_b32 v[24:25],v13 offset0:8 offset1:9\n"
MF "v[160:163],v[180:183],a[52:55],v[160:163]\n"
"ds_read2st64_b32 v[26:27],v13 offset0:10 offset1:11\n"
MF "v[160:163],v[176:179],a[12:15],v[160:163]\n"
LD "v201,s[48:51],s80 offen offset:1024 lds\n"
MF "v[168:171],v[176:179],a[16:19],v[252:255]\n"
"ds_read2st64_b32 v[28:29],v13 offset0:12 offset1:13\n"
MF "v[168:171],v[180:183],a[56:59],v[168:171]\n"
"s_waitcnt lgkmcnt(3)\n"
"ds_read2st64_b32 v[30:31],v13 offset0:14 offset1:15\n"
"v_add_f32 v56,v56,v16\n"
MF "v[168:171],v[184:187],a[96:99],v[168:171]\n"
"s_waitcnt vmcnt(27) lgkmcnt(0)\n"
"s_barrier\n"
MF "v[172:175],a[244:247],a[96:99],v[252:255]\n"
"s_nop 0\n"
TR "v[176:177],v0\n"
TR "v[178:179],v0 offset:2048\n"
MF "v[172:175],a[240:243],a[56:59],v[172:175]\n"
"s_nop 0\n"
TR "v[180:181],v0 offset:4096\n"
TR "v[182:183],v0 offset:6144\n"
MF "v[172:175],v[188:191],a[16:19],v[172:175]\n"
"s_nop 0\n"
TR "v[184:185],v0 offset:8192\n"
TR "v[186:187],v0 offset:10240\n"
MF "v[164:167],v[188:191],a[12:15],v[252:255]\n"
LD "v202,s[48:51],s80 offen offset:2048 lds\n"
MF "v[164:167],a[240:243],a[52:55],v[164:167]\n"
"ds_read2st64_b32 v[32:33],v13 offset0:16 offset1:17\n"
"v_add_f32 v57,v57,v17\n"
MF "v[164:167],a[244:247],a[92:95],v[164:167]\n"
"ds_read2st64_b32 v[34:35],v13 offset0:18 offset1:19\n"
"v_add_f32 v58,v58,v18\n"
MF "v[156:159],a[244:247],a[88:91],v[252:255]\n"
"ds_read2st64_b32 v[36:37],v13 offset0:20 offset1:21\n"
"v_add_f32 v59,v59,v19\n"
MF "v[156:159],a[240:243],a[48:51],v[156:159]\n"
"ds_read2st64_b32 v[38:39],v13 offset0:22 offset1:23\n"
"v_add_f32 v60,v60,v20\n"
MF "v[156:159],v[188:191],a[8:11],v[156:159]\n"
"ds_read2st64_b32 v[40:41],v13 offset0:24 offset1:25\n"
"v_add_f32 v61,v61,v21\n"
MF "v[148:151],v[188:191],a[4:7],v[252:255]\n"
LD "v203,s[48:51],s80 offen offset:3072 lds\n"
MF "v[148:151],a[240:243],a[44:47],v[148:151]\n"
"ds_read2st64_b32 v[42:43],v13 offset0:26 offset1:27\n"
"v_add_f32 v62,v62,v22\n"
MF "v[148:151],a[244:247],a[84:87],v[148:151]\n"
"ds_read2st64_b32 v[44:45],v13 offset0:28 offset1:29\n"
"v_add_f32 v63,v63,v23\n"
MF "v[140:143],a[244:247],a[80:83],v[252:255]\n"
"ds_read2st64_b32 v[46:47],v13 offset0:30 offset1:31\n"
CV "v56,v56,v57\n"
MF "v[140:143],a[240:243],a[40:43],v[140:143]\n"
"ds_read2st64_b32 v[48:49],v13 offset0:32 offset1:33\n"
CV "v57,v58,v59\n"
MF "v[140:143],v[188:191],a[0:3],v[140:143]\n"
"s_waitcnt lgkmcnt(9)\n"
"ds_read2st64_b32 v[50:51],v13 offset0:34 offset1:35\n"
CV "v58,v60,v61\n"
MF "v[96:99],v[176:179],a[120:123],v[96:99]\n"
"s_nop 0\n"
TR "v[188:189],v1\n"
TR "v[190:191],v1 offset:2048\n"
MF "v[96:99],v[180:183],a[160:163],v[96:99]\n"
"s_nop 0\n"
TR "a[240:241],v1 offset:4096\n"
TR "a[242:243],v1 offset:6144\n"
MF "v[96:99],v[184:187],a[200:203],v[96:99]\n"
"s_nop 0\n"
TR "a[244:245],v1 offset:8192\n"
TR "a[246:247],v1 offset:10240\n"
MF "v[104:107],v[184:187],a[204:207],v[104:107]\n"
"ds_read2st64_b32 v[52:53],v13 offset0:36 offset1:37\n"
CV "v59,v62,v63\n"
MF "v[104:107],v[180:183],a[164:167],v[104:107]\n"
"s_add_u32 s87,s90,10240\n"
"ds_read2st64_b32 v[54:55],v13 offset0:38 offset1:39\n"
"v_add_f32 v64,v64,v24\n"
MF "v[104:107],v[176:179],a[124:127],v[104:107]\n"
"buffer_store_dwordx4 v[56:59],v9,s[40:43],s87 offen sc1\n"
MF "v[112:115],v[176:179],a[128:131],v[112:115]\n"
"v_add_f32 v65,v65,v25\n"
"v_add_f32 v66,v66,v26\n"
MF "v[112:115],v[180:183],a[168:171],v[112:115]\n"
"v_add_f32 v67,v67,v27\n"
"v_add_f32 v68,v68,v28\n"
MF "v[112:115],v[184:187],a[208:211],v[112:115]\n"
"v_add_f32 v69,v69,v29\n"
"v_add_f32 v70,v70,v30\n"
MF "v[120:123],v[184:187],a[212:215],v[120:123]\n"
"v_add_f32 v71,v71,v31\n"
CV "v64,v64,v65\n"
MF "v[120:123],v[180:183],a[172:175],v[120:123]\n"
CV "v65,v66,v67\n"
CV "v66,v68,v69\n"
MF "v[120:123],v[176:179],a[132:135],v[120:123]\n"
"s_add_u32 s87,s90,11264\n"
"s_waitcnt lgkmcnt(14)\n"
CV "v67,v70,v71\n"
MF "v[128:131],v[176:179],a[136:139],v[128:131]\n"
"buffer_store_dwordx4 v[64:67],v9,s[40:43],s87 offen sc1\n"
MF "v[128:131],v[180:183],a[176:179],v[128:131]\n"
"v_add_f32 v72,v72,v32\n"
"v_add_f32 v73,v73,v33\n"
MF "v[128:131],v[184:187],a[216:219],v[128:131]\n"
"s_waitcnt lgkmcnt(2)\n"
"v_add_f32 v74,v74,v34\n"
"v_add_f32 v75,v75,v35\n"
MF "v[132:135],a[244:247],a[216:219],v[132:135]\n"
"s_nop 0\n"
TR "v[176:177],v2\n"
TR "v[178:179],v2 offset:2048\n"
MF "v[132:135],a[240:243],a[176:179],v[132:135]\n"
"s_nop 0\n"
TR "v[180:181],v2 offset:4096\n"
TR "v[182:183],v2 offset:6144\n"
MF "v[132:135],v[188:191],a[136:139],v[132:135]\n"
"s_nop 0\n"
TR "v[184:185],v2 offset:8192\n"
TR "v[186:187],v2 offset:10240\n"
MF "v[124:127],v[188:191],a[132:135],v[124:127]\n"
"v_add_f32 v76,v76,v36\n"
"v_add_f32 v77,v77,v37\n"
MF "v[124:127],a[240:243],a[172:175],v[124:127]\n"
"v_add_f32 v78,v78,v38\n"
"v_add_f32 v79,v79,v39\n"
MF "v[124:127],a[244:247],a[212:215],v[124:127]\n"
CV "v72,v72,v73\n"
CV "v73,v74,v75\n"
MF "v[116:119],a[244:247],a[208:211],v[116:119]\n"
"s_add_u32 s87,s90,12288\n"
CV "v74,v76,v77\n"
CV "v75,v78,v79\n"
MF "v[116:119],a[240:243],a[168:171],v[116:119]\n"
"buffer_store_dwordx4 v[72:75],v9,s[40:43],s87 offen sc1\n"
MF "v[116:119],v[188:191],a[128:131],v[116:119]\n"
"v_add_f32 v80,v80,v40\n"
"v_add_f32 v81,v81,v41\n"
MF "v[108:111],v[188:191],a[124:127],v[108:111]\n"
"v_add_f32 v82,v82,v42\n"
"v_add_f32 v83,v83,v43\n"
MF "v[108:111],a[240:243],a[164:167],v[108:111]\n"
"v_add_f32 v84,v84,v44\n"
"v_add_f32 v85,v85,v45\n"
MF "v[108:111],a[244:247],a[204:207],v[108:111]\n"
"v_add_f32 v86,v86,v46\n"
"v_add_f32 v87,v87,v47\n"
MF "v[100:103],a[244:247],a[200:203],v[100:103]\n"
CV "v80,v80,v81\n"
CV "v81,v82,v83\n"
MF "v[100:103],a[240:243],a[160:163],v[100:103]\n"
"s_add_u32 s87,s90,13312\n"
CV "v82,v84,v85\n"
CV "v83,v86,v87\n"
MF "v[100:103],v[188:191],a[120:123],v[100:103]\n"
"s_waitcnt lgkmcnt(0)\n"
"buffer_store_dwordx4 v[80:83],v9,s[40:43],s87 offen sc1\n"
MF "v[136:139],v[176:179],a[120:123],v[136:139]\n"
"s_nop 0\n"
TR "v[188:189],v3\n"
TR "v[190:191],v3 offset:2048\n"
MF "v[136:139],v[180:183],a[160:163],v[136:139]\n"
"s_nop 0\n"
TR "a[240:241],v3 offset:4096\n"
TR "a[242:243],v3 offset:6144\n"
MF "v[136:139],v[184:187],a[200:203],v[136:139]\n"
"s_nop 0\n"
TR "a[244:245],v3 offset:8192\n"
TR "a[246:247],v3 offset:10240\n"
MF "v[144:147],v[184:187],a[204:207],v[144:147]\n"
"v_add_f32 v88,v88,v48\n"
"v_add_f32 v89,v89,v49\n"
MF "v[144:147],v[180:183],a[164:167],v[144:147]\n"
"v_add_f32 v90,v90,v50\n"
"v_add_f32 v91,v91,v51\n"
MF "v[144:147],v[176:179],a[124:127],v[144:147]\n"
"v_add_f32 v92,v92,v52\n"
"v_add_f32 v93,v93,v53\n"
MF "v[152:155],v[176:179],a[128:131],v[152:155]\n"
"v_add_f32 v94,v94,v54\n"
"v_add_f32 v95,v95,v55\n"
MF "v[152:155],v[180:183],a[168:171],v[152:155]\n"
CV "v88,v88,v89\n"
CV "v89,v90,v91\n"
MF "v[152:155],v[184:187],a[208:211],v[152:155]\n"
"s_add_u32 s87,s90,14336\n"
CV "v90,v92,v93\n"
CV "v91,v94,v95\n"
MF "v[160:163],v[184:187],a[212:215],v[160:163]\n"
"buffer_store_dwordx4 v[88:91],v9,s[40:43],s87 offen sc1\n"
MF "v[160:163],v[180:183],a[172:175],v[160:163]\n"
"v_cmp_gt_u32 vcc,4,v10\n"
MF "v[160:163],v[176:179],a[132:135],v[160:163]\n"
"s_cmp_eq_u64 vcc,0\n"
"s_cselect_b32 s2,s2,0xf0000000\n"
MF "v[168:171],v[176:179],a[136:139],v[168:171]\n"
"s_cmp_eq_u64 vcc,0\n"
"s_cselect_b32 s3,s3,0xf0000000\n"
MF "v[168:171],v[180:183],a[176:179],v[168:171]\n"
"s_cmp_eq_u64 vcc,0\n"
"s_cselect_b32 s0,s0,0xf0000000\n"
MF "v[168:171],v[184:187],a[216:219],v[168:171]\n"
"s_waitcnt vmcnt(18) lgkmcnt(0)\n"
"s_barrier\n"
MF "v[172:175],a[244:247],a[216:219],v[172:175]\n"
"s_nop 0\n"
TR "v[176:177],v0 offset:49152\n"
TR "v[178:179],v0 offset:51200\n"
MF "v[172:175],a[240:243],a[176:179],v[172:175]\n"
"s_nop 0\n"
TR "v[180:181],v0 offset:53248\n"
TR "v[182:183],v0 offset:55296\n"
MF "v[172:175],v[188:191],a[136:139],v[172:175]\n"
"s_nop 0\n"
TR "v[184:185],v0 offset:57344\n"
TR "v[186:187],v0 offset:59392\n"
MF "v[164:167],v[188:191],a[132:135],v[164:167]\n"
"buffer_load_dword v10,v12,s[40:43],s86 offen sc1\n"
MF "v[164:167],a[240:243],a[172:175],v[164:167]\n"
"s_cmp_eq_u64 vcc,0\n"
"s_cselect_b32 s1,s1,0xf0000000\n"
MF "v[164:167],a[244:247],a[212:215],v[164:167]\n"
"s_cmp_eq_u64 vcc,0\n"
"s_cselect_b32 s92,64,s92\n"
MF "v[156:159],a[244:247],a[208:211],v[156:159]\n"
MF "v[156:159],a[240:243],a[168:171],v[156:159]\n"
MF "v[156:159],v[188:191],a[128:131],v[156:159]\n"
MF "v[148:151],v[188:191],a[124:127],v[148:151]\n"
MF "v[148:151],a[240:243],a[164:167],v[148:151]\n"
MF "v[148:151],a[244:247],a[204:207],v[148:151]\n"
MF "v[140:143],a[244:247],a[200:203],v[140:143]\n"
MF "v[140:143],a[240:243],a[160:163],v[140:143]\n"
MF "v[140:143],v[188:191],a[120:123],v[140:143]\n"
"s_waitcnt lgkmcnt(0)\n"
MF "v[96:99],v[176:179],a[20:23],v[96:99]\n"
"s_nop 0\n"
TR "v[188:189],v1 offset:49152\n"
TR "v[190:191],v1 offset:51200\n"
MF "v[96:99],v[180:183],a[60:63],v[96:99]\n"
"s_nop 0\n"
TR "a[240:241],v1 offset:53248\n"
TR "a[242:243],v1 offset:55296\n"
MF "v[96:99],v[184:187],a[100:103],v[96:99]\n"
"s_nop 0\n"
TR "a[244:245],v1 offset:57344\n"
TR "a[246:247],v1 offset:59392\n"
MF "v[104:107],v[184:187],a[104:107],v[104:107]\n"
MF "v[104:107],v[180:183],a[64:67],v[104:107]\n"
MF "v[104:107],v[176:179],a[24:27],v[104:107]\n"
"s_waitcnt vmcnt(18)\n"
MF "v[96:99],v[208:211],a[252:255],v[96:99]\n"
MF "v[104:107],v[212:215],a[252:255],v[104:107]\n"
MF "v[112:115],v[216:219],a[252:255],v[112:115]\n"
MF "v[120:123],v[220:223],a[252:255],v[120:123]\n"
MF "v[128:131],v[224:227],a[252:255],v[128:131]\n"
MF "v[100:103],v[208:211],a[248:251],v[100:103]\n"
MF "v[108:111],v[212:215],a[248:251],v[108:111]\n"
MF "v[116:119],v[216:219],a[248:251],v[116:119]\n"
MF "v[124:127],v[220:223],a[248:251],v[124:127]\n"
MF "v[132:135],v[224:227],a[248:251],v[132:135]\n"
MF "v[112:115],v[176:179],a[28:31],v[112:115]\n"
MF "v[112:115],v[180:183],a[68:71],v[112:115]\n"
MF "v[112:115],v[184:187],a[108:111],v[112:115]\n"
MF "v[120:123],v[184:187],a[112:115],v[120:123]\n"
MF "v[120:123],v[180:183],a[72:75],v[120:123]\n"
"s_bfe_u32 s87,s22,0x30014\n"
MF "v[120:123],v[176:179],a[32:35],v[120:123]\n"
"s_lshl_b32 s87,s87,7\n"
MF "v[128:131],v[176:179],a[36:39],v[128:131]\n"
"s_and_b32 s60,s56,1\n"
MF "v[128:131],v[180:183],a[76:79],v[128:131]\n"
"s_mul_i32 s60,s60,204800\n"
MF "v[128:131],v[184:187],a[116:119],v[128:131]\n"
"s_waitcnt lgkmcnt(0)\n"
MF "v[132:135],a[244:247],a[116:119],v[132:135]\n"
"s_add_u32 s87,s87,s60\n"
TR "v[176:177],v2 offset:49152\n"
TR "v[178:179],v2 offset:51200\n"
MF "v[132:135],a[240:243],a[76:79],v[132:135]\n"
"s_mov_b32 s71,204800\n"
TR "v[180:181],v2 offset:53248\n"
TR "v[182:183],v2 offset:55296\n"
MF "v[132:135],v[188:191],a[36:39],v[132:135]\n"
"s_mov_b32 s66,204800\n"
TR "v[184:185],v2 offset:57344\n"
TR "v[186:187],v2 offset:59392\n"
MF "v[124:127],v[188:191],a[32:35],v[124:127]\n"
LD "a[0:3],v9,s[40:43],s2 offen sc1\n"
MF "v[124:127],a[240:243],a[72:75],v[124:127]\n"
"s_add_u32 s2,s2,204800\n"
MF "v[124:127],a[244:247],a[112:115],v[124:127]\n"
LD "a[4:7],v9,s[40:43],s2 offen sc1\n"
MF "v[116:119],a[244:247],a[108:111],v[116:119]\n"
"s_add_u32 s72,s78,2\n"
MF "v[116:119],a[240:243],a[68:71],v[116:119]\n"
"s_add_u32 s2,s2,204800\n"
MF "v[116:119],v[188:191],a[28:31],v[116:119]\n"
LD "a[8:11],v9,s[40:43],s2 offen sc1\n"
MF "v[108:111],v[188:191],a[24:27],v[108:111]\n"
"s_add_u32 s16,s78,6\n"
MF "v[108:111],a[240:243],a[64:67],v[108:111]\n"
"s_add_u32 s2,s2,204800\n"
MF "v[108:111],a[244:247],a[104:107],v[108:111]\n"
LD "a[12:15],v9,s[40:43],s2 offen sc1\n"
MF "v[100:103],a[244:247],a[100:103],v[100:103]\n"
"s_and_b32 s16,s16,15\n"
MF "v[100:103],a[240:243],a[60:63],v[100:103]\n"
"s_add_u32 s2,s2,204800\n"
MF "v[100:103],v[188:191],a[20:23],v[100:103]\n"
"s_waitcnt lgkmcnt(0)\n"
LD "a[40:43],v9,s[40:43],s2 offen sc1\n"
MF "v[136:139],v[176:179],a[20:23],v[136:139]\n"
"s_add_u32 s17,s78,4\n"
TR "v[188:189],v3 offset:49152\n"
TR "v[190:191],v3 offset:51200\n"
MF "v[136:139],v[180:183],a[60:63],v[136:139]\n"
"s_and_b32 s17,s17,15\n"
TR "a[240:241],v3 offset:53248\n"
TR "a[242:243],v3 offset:55296\n"
MF "v[136:139],v[184:187],a[100:103],v[136:139]\n"
"s_add_u32 s2,s2,204800\n"
TR "a[244:245],v3 offset:57344\n"
TR "a[246:247],v3 offset:59392\n"
MF "v[144:147],v[184:187],a[104:107],v[144:147]\n"
LD "a[44:47],v9,s[40:43],s2 offen sc1\n"
MF "v[144:147],v[180:183],a[64:67],v[144:147]\n"
"s_add_u32 s2,s2,204800\n"
MF "v[144:147],v[176:179],a[24:27],v[144:147]\n"
LD "a[48:51],v9,s[40:43],s2 offen sc1\n"
MF "v[152:155],v[176:179],a[28:31],v[152:155]\n"
"s_and_b32 s72,s72,15\n"
MF "v[152:155],v[180:183],a[68:71],v[152:155]\n"
"s_add_u32 s2,s2,204800\n"
MF "v[152:155],v[184:187],a[108:111],v[152:155]\n"
LD "a[52:55],v9,s[40:43],s2 offen sc1\n"
MF "v[160:163],v[184:187],a[112:115],v[160:163]\n"
"s_bfe_u32 s60,s22,0x10014\n"
MF "v[160:163],v[180:183],a[72:75],v[160:163]\n"
"s_lshl_b32 s60,s60,5\n"
MF "v[160:163],v[176:179],a[32:35],v[160:163]\n"
"v_xor_b32 v242,s60,v8\n"
MF "v[168:171],v[176:179],a[36:39],v[168:171]\n"
MF "v[168:171],v[180:183],a[76:79],v[168:171]\n"
MF "v[168:171],v[184:187],a[116:119],v[168:171]\n"
"s_waitcnt vmcnt(8) lgkmcnt(0)\n"
"s_barrier\n"
MF "v[172:175],a[244:247],a[116:119],v[172:175]\n"
"v_sub_u32 v10,v10,v11\n"
MF "v[172:175],a[240:243],a[76:79],v[172:175]\n"
"s_nop 0\n"
TR "v[176:177],v4\n"
TR "v[178:179],v4 offset:2048\n"
MF "v[172:175],v[188:191],a[36:39],v[172:175]\n"
"s_nop 0\n"
TR "v[180:181],v4 offset:4096\n"
TR "v[182:183],v4 offset:6144\n"
MF "v[164:167],v[188:191],a[32:35],v[164:167]\n"
"s_nop 0\n"
TR "v[184:185],v4 offset:8192\n"
TR "v[186:187],v4 offset:10240\n"
MF "v[164:167],a[240:243],a[72:75],v[164:167]\n"
"v_readlane_b32 s16,v10,s16\n"
MF "v[164:167],a[244:247],a[112:115],v[164:167]\n"
"s_cmp_lt_u32 s16,9\n"
"s_cselect_b32 s16,0xf0000000,0\n"
MF "v[156:159],a[244:247],a[108:111],v[156:159]\n"
"v_readlane_b32 s17,v10,s17\n"
MF "v[156:159],a[240:243],a[68:71],v[156:159]\n"
"s_cmp_lt_u32 s17,10\n"
"s_cselect_b32 s17,0xf0000000,0\n"
MF "v[156:159],v[188:191],a[28:31],v[156:159]\n"
"s_bfe_u32 s62,s22,0x30014\n"
MF "v[148:151],v[188:191],a[24:27],v[148:151]\n"
"s_add_u32 s62,s62,3\n"
"s_and_b32 s62,s62,7\n"
"s_mul_i32 s62,s62,204800\n"
MF "v[148:151],a[240:243],a[64:67],v[148:151]\n"
"s_add_u32 s62,s62,s73\n"
"s_add_u32 s62,s62,133120\n"
"s_or_b32 s62,s62,s16\n"
MF "v[148:151],a[244:247],a[104:107],v[148:151]\n"
LD "v[16:19],v9,s[40:43],s62 offen sc1\n"
MF "v[140:143],a[244:247],a[100:103],v[140:143]\n"
LD "v[24:27],v9,s[40:43],s62 offen offset:1024 sc1\n"
MF "v[140:143],a[240:243],a[60:63],v[140:143]\n"
MF "v[140:143],v[188:191],a[20:23],v[140:143]\n"
"s_waitcnt lgkmcnt(0)\n"
LD "v[32:35],v9,s[40:43],s62 offen offset:2048 sc1\n"
MF "v[96:99],v[176:179],a[140:143],v[96:99]\n"
"s_nop 0\n"
TR "v[188:189],v5\n"
TR "v[190:191],v5 offset:2048\n"
MF "v[96:99],v[180:183],a[180:183],v[96:99]\n"
"s_nop 0\n"
TR "a[240:241],v5 offset:4096\n"
TR "a[242:243],v5 offset:6144\n"
MF "v[96:99],v[184:187],a[220:223],v[96:99]\n"
"s_nop 0\n"
TR "a[244:245],v5 offset:8192\n"
TR "a[246:247],v5 offset:10240\n"
MF "v[104:107],v[184:187],a[224:227],v[104:107]\n"
LD "v[40:43],v9,s[40:43],s62 offen offset:3072 sc1\n"
MF "v[104:107],v[180:183],a[184:187],v[104:107]\n"
MF "v[104:107],v[176:179],a[144:147],v[104:107]\n"
"s_add_u32 s62,s62,4096\n"
MF "v[112:115],v[176:179],a[148:151],v[112:115]\n"
LD "v[48:51],v9,s[40:43],s62 offen sc1\n"
MF "v[112:115],v[180:183],a[188:191],v[112:115]\n"
"s_bfe_u32 s62,s22,0x30014\n"
"ds_write_addtid_b32 v96 offset:16384\n"
"ds_write_addtid_b32 v97 offset:16640\n"
MF "v[112:115],v[184:187],a[228:231],v[112:115]\n"
"s_add_u32 s62,s62,2\n"
"s_and_b32 s62,s62,7\n"
"s_mul_i32 s62,s62,204800\n"
MF "v[120:123],v[184:187],a[232:235],v[120:123]\n"
"s_add_u32 s62,s62,s73\n"
"s_add_u32 s62,s62,153600\n"
"s_or_b32 s62,s62,s17\n"
MF "v[120:123],v[180:183],a[192:195],v[120:123]\n"
LD "v[20:23],v9,s[40:43],s62 offen sc1\n"
MF "v[120:123],v[176:179],a[152:155],v[120:123]\n"
"s_nop 0\n"
"ds_write_addtid_b32 v98 offset:16896\n"
"ds_write_addtid_b32 v99 offset:17152\n"
MF "v[128:131],v[176:179],a[156:159],v[128:131]\n"
LD "v[28:31],v9,s[40:43],s62 offen offset:1024 sc1\n"
MF "v[128:131],v[180:183],a[196:199],v[128:131]\n"
"s_nop 0\n"
"ds_write_addtid_b32 v104 offset:18432\n"
"ds_write_addtid_b32 v105 offset:18688\n"
MF "v[128:131],v[184:187],a[236:239],v[128:131]\n"
"s_waitcnt lgkmcnt(6)\n"
LD "v[36:39],v9,s[40:43],s62 offen offset:2048 sc1\n"
MF "v[100:103],v[188:191],a[140:143],v[100:103]\n"
"s_nop 0\n"
TR "v[176:177],v6\n"
TR "v[178:179],v6 offset:2048\n"
MF "v[100:103],a[240:243],a[180:183],v[100:103]\n"
"s_nop 0\n"
TR "v[180:181],v6 offset:4096\n"
TR "v[182:183],v6 offset:6144\n"
MF "v[100:103],a[244:247],a[220:223],v[100:103]\n"
"s_nop 0\n"
TR "v[184:185],v6 offset:8192\n"
TR "v[186:187],v6 offset:10240\n"
MF "v[108:111],a[244:247],a[224:227],v[108:111]\n"
LD "v[44:47],v9,s[40:43],s62 offen offset:3072 sc1\n"
MF "v[108:111],a[240:243],a[184:187],v[108:111]\n"
"s_nop 0\n"
"ds_write_addtid_b32 v106 offset:18944\n"
"ds_write_addtid_b32 v107 offset:19200\n"
MF "v[108:111],v[188:191],a[144:147],v[108:111]\n"
"s_add_u32 s62,s62,4096\n"
"ds_write_addtid_b32 v112 offset:20480\n"
"ds_write_addtid_b32 v113 offset:20736\n"
MF "v[116:119],v[188:191],a[148:151],v[116:119]\n"
LD "v[52:55],v9,s[40:43],s62 offen sc1\n"
MF "v[116:119],a[240:243],a[188:191],v[116:119]\n"
"s_nop 0\n"
"ds_write_addtid_b32 v114 offset:20992\n"
"ds_write_addtid_b32 v115 offset:21248\n"
MF "v[116:119],a[244:247],a[228:231],v[116:119]\n"
"s_nop 0\n"
"ds_write_addtid_b32 v120 offset:22528\n"
"ds_write_addtid_b32 v121 offset:22784\n"
MF "v[124:127],a[244:247],a[232:235],v[124:127]\n"
"s_nop 0\n"
"ds_write_addtid_b32 v122 offset:23040\n"
"ds_write_addtid_b32 v123 offset:23296\n"
MF "v[124:127],a[240:243],a[192:195],v[124:127]\n"
"s_nop 0\n"
"ds_write_addtid_b32 v128 offset:24576\n"
"ds_write_addtid_b32 v129 offset:24832\n"
MF "v[124:127],v[188:191],a[152:155],v[124:127]\n"
"buffer_load_dword v10,v12,s[40:43],s86 offen sc1\n"
MF "v[132:135],v[188:191],a[156:159],v[132:135]\n"
"s_nop 0\n"
"ds_write_addtid_b32 v130 offset:25088\n"
"ds_write_addtid_b32 v131 offset:25344\n"
MF "v[132:135],a[240:243],a[196:199],v[132:135]\n"
"s_nop 0\n"
"ds_write_addtid_b32 v100 offset:17408\n"
"ds_write_addtid_b32 v101 offset:17664\n"
MF "v[132:135],a[244:247],a[236:239],v[132:135]\n"
"s_waitcnt lgkmcnt(15)\n"
"ds_write_addtid_b32 v102 offset:17920\n"
"ds_write_addtid_b32 v103 offset:18176\n"
MF "v[136:139],v[176:179],a[140:143],v[136:139]\n"
"s_nop 0\n"
TR "v[188:189],v7\n"
TR "v[190:191],v7 offset:2048\n"
MF "v[136:139],v[180:183],a[180:183],v[136:139]\n"
"s_nop 0\n"
TR "a[240:241],v7 offset:4096\n"
TR "a[242:243],v7 offset:6144\n"
MF "v[136:139],v[184:187],a[220:223],v[136:139]\n"
"s_nop 0\n"
TR "a[244:245],v7 offset:8192\n"
TR "a[246:247],v7 offset:10240\n"
MF "v[144:147],v[184:187],a[224:227],v[144:147]\n"
"s_nop 0\n"
"ds_write_addtid_b32 v108 offset:19456\n"
"ds_write_addtid_b32 v109 offset:19712\n"
MF "v[144:147],v[180:183],a[184:187],v[144:147]\n"
"s_nop 0\n"
"ds_write_addtid_b32 v110 offset:19968\n"
"ds_write_addtid_b32 v111 offset:20224\n"
MF "v[144:147],v[176:179],a[144:147],v[144:147]\n"
"s_nop 0\n"
"ds_write_addtid_b32 v116 offset:21504\n"
"ds_write_addtid_b32 v117 offset:21760\n"
MF "v[152:155],v[176:179],a[148:151],v[152:155]\n"
"s_nop 0\n"
"ds_write_addtid_b32 v118 offset:22016\n"
"ds_write_addtid_b32 v119 offset:22272\n"
MF "v[152:155],v[180:183],a[188:191],v[152:155]\n"
"ds_write_addtid_b32 v124 offset:23552\n"
MF "v[152:155],v[184:187],a[228:231],v[152:155]\n"
"ds_write_addtid_b32 v125 offset:23808\n"
MF "v[160:163],v[184:187],a[232:235],v[160:163]\n"
"ds_write_addtid_b32 v126 offset:24064\n"
MF "v[160:163],v[180:183],a[192:195],v[160:163]\n"
"ds_write_addtid_b32 v127 offset:24320\n"
MF "v[160:163],v[176:179],a[152:155],v[160:163]\n"
"ds_write_addtid_b32 v132 offset:25600\n"
MF "v[168:171],v[176:179],a[156:159],v[168:171]\n"
"ds_write_addtid_b32 v133 offset:25856\n"
MF "v[168:171],v[180:183],a[196:199],v[168:171]\n"
"ds_write_addtid_b32 v134 offset:26112\n"
MF "v[168:171],v[184:187],a[236:239],v[168:171]\n"
"s_waitcnt lgkmcnt(15)\n"
"ds_write_addtid_b32 v135 offset:26368\n"
MF "v[140:143],v[188:191],a[140:143],v[140:143]\n"
MF "v[140:143],a[240:243],a[180:183],v[140:143]\n"
MF "v[140:143],a[244:247],a[220:223],v[140:143]\n"
MF "v[148:151],a[244:247],a[224:227],v[148:151]\n"
MF "v[148:151],a[240:243],a[184:187],v[148:151]\n"
"s_waitcnt lgkmcnt(0)\n"
"s_barrier\n"
MF "v[148:151],v[188:191],a[144:147],v[148:151]\n"
"ds_read2st64_b32 v[56:57],v13 offset0:0 offset1:1\n"
MF "v[156:159],v[188:191],a[148:151],v[156:159]\n"
"ds_read2st64_b32 v[58:59],v13 offset0:2 offset1:3\n"
MF "v[156:159],a[240:243],a[188:191],v[156:159]\n"
"ds_read2st64_b32 v[60:61],v13 offset0:4 offset1:5\n"
MF "v[156:159],a[244:247],a[228:231],v[156:159]\n"
"ds_read2st64_b32 v[62:63],v13 offset0:6 offset1:7\n"
MF "v[164:167],a[244:247],a[232:235],v[164:167]\n"
"ds_read2st64_b32 v[64:65],v13 offset0:8 offset1:9\n"
MF "v[164:167],a[240:243],a[192:195],v[164:167]\n"
"ds_read2st64_b32 v[66:67],v13 offset0:10 offset1:11\n"
MF "v[164:167],v[188:191],a[152:155],v[164:167]\n"
"ds_read2st64_b32 v[68:69],v13 offset0:12 offset1:13\n"
MF "v[172:175],v[188:191],a[156:159],v[172:175]\n"
"ds_read2st64_b32 v[70:71],v13 offset0:14 offset1:15\n"
MF "v[172:175],a[240:243],a[196:199],v[172:175]\n"
"ds_read2st64_b32 v[72:73],v13 offset0:16 offset1:17\n"
MF "v[172:175],a[244:247],a[236:239],v[172:175]\n"
"ds_read2st64_b32 v[74:75],v13 offset0:18 offset1:19\n"
"ds_read2st64_b32 v[76:77],v13 offset0:20 offset1:21\n"
"ds_read2st64_b32 v[78:79],v13 offset0:22 offset1:23\n"
"s_nop 0\n"
"ds_read2st64_b32 v[80:81],v13 offset0:24 offset1:25\n"
".Lpg28h:\n"
"s_cbranch_execz .Lpg31h\n"
"s_nop 0\n"
"ds_read2st64_b32 v[82:83],v13 offset0:26 offset1:27\n"
"ds_read2st64_b32 v[84:85],v13 offset0:28 offset1:29\n"
"ds_read2st64_b32 v[86:87],v13 offset0:30 offset1:31\n"
"ds_read2st64_b32 v[88:89],v13 offset0:32 offset1:33\n"
"ds_read2st64_b32 v[90:91],v13 offset0:34 offset1:35\n"
"ds_read2st64_b32 v[92:93],v13 offset0:36 offset1:37\n"
"ds_read2st64_b32 v[94:95],v13 offset0:38 offset1:39\n"
"s_waitcnt vmcnt(0)\n"
"v_sub_u32 v10,v10,v11\n"
"s_cmp_eq_u32 s16,0\n"
"s_cbranch_scc1 .Lhave03\n"
"s_add_u32 s59,s78,6\n"
"s_and_b32 s59,s59,15\n"
"s_nop 3\n"
".Lnf2:\n"
"v_readlane_b32 s58,v10,s59\n"
"s_cmp_lt_u32 s58,9\n"
"s_cbranch_scc1 .Lsp2\n"
".Lnb2:\n"
"s_bfe_u32 s62,s22,0x30014\n"
"s_add_u32 s62,s62,3\n"
"s_and_b32 s62,s62,7\n"
"s_mul_i32 s62,s62,204800\n"
"s_add_u32 s62,s62,s73\n"
"s_add_u32 s62,s62,133120\n"
LD "v[16:19],v9,s[40:43],s62 offen sc1\n"
LD "v[24:27],v9,s[40:43],s62 offen offset:1024 sc1\n"
LD "v[32:35],v9,s[40:43],s62 offen offset:2048 sc1\n"
LD "v[40:43],v9,s[40:43],s62 offen offset:3072 sc1\n"
"s_add_u32 s62,s62,4096\n"
LD "v[48:51],v9,s[40:43],s62 offen sc1\n"
".Lhave03:\n"
"s_cmp_eq_u32 s17,0\n"
"s_cbranch_scc1 .Lhave02\n"
"s_add_u32 s59,s78,4\n"
"s_and_b32 s59,s59,15\n"
"s_nop 3\n"
".Lnf3:\n"
"v_readlane_b32 s58,v10,s59\n"
"s_cmp_lt_u32 s58,10\n"
"s_cbranch_scc1 .Lsp3\n"
".Lnb3:\n"
"s_bfe_u32 s62,s22,0x30014\n"
"s_add_u32 s62,s62,2\n"
"s_and_b32 s62,s62,7\n"
"s_mul_i32 s62,s62,204800\n"
"s_add_u32 s62,s62,s73\n"
"s_add_u32 s62,s62,153600\n"
LD "v[20:23],v9,s[40:43],s62 offen sc1\n"
LD "v[28:31],v9,s[40:43],s62 offen offset:1024 sc1\n"
LD "v[36:39],v9,s[40:43],s62 offen offset:2048 sc1\n"
LD "v[44:47],v9,s[40:43],s62 offen offset:3072 sc1\n"
"s_add_u32 s62,s62,4096\n"
LD "v[52:55],v9,s[40:43],s62 offen sc1\n"
".Lhave02:\n"
".Lnf4:\n"
"v_readlane_b32 s58,v10,s72\n"
"s_cmp_lt_u32 s58,11\n"
"s_cbranch_scc1 .Lsp4\n"
".Lnb4:\n"
"s_bfe_u32 s62,s22,0x30014\n"
"s_add_u32 s62,s62,1\n"
"s_and_b32 s62,s62,7\n"
"s_mul_i32 s62,s62,204800\n"
"s_add_u32 s62,s62,s73\n"
"s_nop 0\n"
"s_add_u32 s62,s62,174080\n"
LD "v[208:211],v9,s[40:43],s62 offen sc1\n"
LD "v[212:215],v9,s[40:43],s62 offen offset:1024 sc1\n"
LD "v[216:219],v9,s[40:43],s62 offen offset:2048 sc1\n"
LD "v[220:223],v9,s[40:43],s62 offen offset:3072 sc1\n"
"s_add_u32 s62,s62,4096\n"
LD "v[224:227],v9,s[40:43],s62 offen sc1\n"
"s_cmp_eq_u32 s1,0xf0000000\n"
"s_cbranch_scc1 .Ljy0\n"
"s_nop 0\n"
LD "a[80:83],v9,s[40:43],s3 offen sc1\n"
"s_add_u32 s3,s3,204800\n"
LD "a[84:87],v9,s[40:43],s3 offen sc1\n"
"s_add_u32 s3,s3,204800\n"
LD "a[88:91],v9,s[40:43],s3 offen sc1\n"
"s_add_u32 s3,s3,204800\n"
LD "a[92:95],v9,s[40:43],s3 offen sc1\n"
"s_add_u32 s3,s3,204800\n"
LD "a[120:123],v9,s[40:43],s3 offen sc1\n"
"s_add_u32 s3,s3,204800\n"
LD "a[124:127],v9,s[40:43],s3 offen sc1\n"
"s_add_u32 s3,s3,204800\n"
LD "a[128:131],v9,s[40:43],s3 offen sc1\n"
"s_add_u32 s3,s3,204800\n"
LD "a[132:135],v9,s[40:43],s3 offen sc1\n"
".Ljy0:\n"
"s_waitcnt vmcnt(26)\n"
"v_accvgpr_mov_b32 a16,a0\n"
"v_accvgpr_mov_b32 a17,a1\n"
"v_accvgpr_mov_b32 a56,a40\n"
"v_accvgpr_mov_b32 a57,a41\n"
MF "v[248:251],a[2:5],a[250:253],0\n"
MF "v[244:247],a[6:9],a[250:253],0\n"
MF "v[248:251],a[10:13],a[250:253],v[248:251]\n"
MF "v[244:247],a[14:17],a[250:253],v[244:247]\n"
MF "v[248:251],a[42:45],a[250:253],v[248:251]\n"
MF "v[244:247],a[46:49],a[250:253],v[244:247]\n"
MF "v[248:251],a[50:53],a[250:253],v[248:251]\n"
MF "v[244:247],a[54:57],a[250:253],v[244:247]\n"
"s_waitcnt lgkmcnt(15)\n"
"v_pk_add_f32 v[136:137],v[136:137],v[56:57]\n"
"v_pk_add_f32 v[138:139],v[138:139],v[58:59]\n"
"v_pk_add_f32 v[140:141],v[140:141],v[60:61]\n"
"v_pk_add_f32 v[142:143],v[142:143],v[62:63]\n"
"v_pk_add_f32 v[144:145],v[144:145],v[64:65]\n"
"s_waitcnt lgkmcnt(14)\n"
"v_pk_add_f32 v[146:147],v[146:147],v[66:67]\n"
"s_waitcnt lgkmcnt(13)\n"
"v_pk_add_f32 v[148:149],v[148:149],v[68:69]\n"
"s_waitcnt lgkmcnt(12)\n"
"v_pk_add_f32 v[150:151],v[150:151],v[70:71]\n"
"s_waitcnt lgkmcnt(11)\n"
"v_pk_add_f32 v[152:153],v[152:153],v[72:73]\n"
"s_waitcnt lgkmcnt(10)\n"
"v_pk_add_f32 v[154:155],v[154:155],v[74:75]\n"
"s_waitcnt lgkmcnt(9)\n"
"v_pk_add_f32 v[156:157],v[156:157],v[76:77]\n"
"s_waitcnt lgkmcnt(8)\n"
"v_pk_add_f32 v[158:159],v[158:159],v[78:79]\n"
"s_waitcnt lgkmcnt(7)\n"
"v_pk_add_f32 v[160:161],v[160:161],v[80:81]\n"
"s_waitcnt lgkmcnt(6)\n"
"v_pk_add_f32 v[162:163],v[162:163],v[82:83]\n"
"s_waitcnt lgkmcnt(5)\n"
"v_pk_add_f32 v[164:165],v[164:165],v[84:85]\n"
"s_waitcnt lgkmcnt(4)\n"
"v_pk_add_f32 v[166:167],v[166:167],v[86:87]\n"
"s_waitcnt lgkmcnt(3)\n"
"v_pk_add_f32 v[168:169],v[168:169],v[88:89]\n"
"s_waitcnt lgkmcnt(2)\n"
"v_pk_add_f32 v[170:171],v[170:171],v[90:91]\n"
"s_waitcnt lgkmcnt(1)\n"
"v_pk_add_f32 v[172:173],v[172:173],v[92:93]\n"
"s_waitcnt lgkmcnt(0)\n"
"v_pk_add_f32 v[174:175],v[174:175],v[94:95]\n"
CV "v244,v244,v245\n"
CV "v245,v246,v247\n"
CV "v246,v248,v249\n"
CV "v247,v250,v251\n"
"s_nop 0\n"
"v_permlane16_swap_b32 v244,v246\n"
"v_permlane16_swap_b32 v245,v247\n"
"buffer_store_dwordx4 v[244:247],v8,s[44:47],s0 offen sc1\n"
"s_waitcnt vmcnt(6)\n"
MF "v[140:143],v[16:19],a[252:255],v[140:143]\n"
MF "v[148:151],v[24:27],a[252:255],v[148:151]\n"
MF "v[156:159],v[32:35],a[252:255],v[156:159]\n"
MF "v[164:167],v[40:43],a[252:255],v[164:167]\n"
MF "v[172:175],v[48:51],a[252:255],v[172:175]\n"
MF "v[136:139],v[16:19],a[248:251],v[136:139]\n"
MF "v[144:147],v[24:27],a[248:251],v[144:147]\n"
MF "v[152:155],v[32:35],a[248:251],v[152:155]\n"
MF "v[160:163],v[40:43],a[248:251],v[160:163]\n"
MF "v[168:171],v[48:51],a[248:251],v[168:171]\n"
MF "v[136:139],v[20:23],a[252:255],v[136:139]\n"
MF "v[144:147],v[28:31],a[252:255],v[144:147]\n"
MF "v[152:155],v[36:39],a[252:255],v[152:155]\n"
MF "v[160:163],v[44:47],a[252:255],v[160:163]\n"
MF "v[168:171],v[52:55],a[252:255],v[168:171]\n"
MF "v[140:143],v[20:23],a[248:251],v[140:143]\n"
MF "v[148:151],v[28:31],a[248:251],v[148:151]\n"
MF "v[156:159],v[36:39],a[248:251],v[156:159]\n"
MF "v[164:167],v[44:47],a[248:251],v[164:167]\n"
MF "v[172:175],v[52:55],a[248:251],v[172:175]\n"
"s_waitcnt vmcnt(1)\n"
MF "v[140:143],v[208:211],a[252:255],v[140:143]\n"
MF "v[136:139],v[208:211],a[248:251],v[136:139]\n"
MF "v[148:151],v[212:215],a[252:255],v[148:151]\n"
MF "v[144:147],v[212:215],a[248:251],v[144:147]\n"
MF "v[156:159],v[216:219],a[252:255],v[156:159]\n"
"s_nop 4\n"
CV "v136,v136,v137\n"
CV "v137,v138,v139\n"
MF "v[152:155],v[216:219],a[248:251],v[152:155]\n"
CV "v138,v140,v141\n"
CV "v139,v142,v143\n"
MF "v[164:167],v[220:223],a[252:255],v[164:167]\n"
CV "v144,v144,v145\n"
CV "v145,v146,v147\n"
MF "v[160:163],v[220:223],a[248:251],v[160:163]\n"
CV "v146,v148,v149\n"
CV "v147,v150,v151\n"
"v_permlane16_swap_b32 v136,v138\n"
"v_permlane16_swap_b32 v137,v139\n"
"buffer_store_dwordx4 v[136:139],v242,s[44:47],s87 offen offset:64 sc1\n"
"s_add_u32 s87,s87,40960\n"
MF "v[172:175],v[224:227],a[252:255],v[172:175]\n"
CV "v152,v152,v153\n"
CV "v153,v154,v155\n"
MF "v[168:171],v[224:227],a[248:251],v[168:171]\n"
CV "v154,v156,v157\n"
CV "v155,v158,v159\n"
"v_permlane16_swap_b32 v144,v146\n"
"v_permlane16_swap_b32 v145,v147\n"
"buffer_store_dwordx4 v[144:147],v242,s[44:47],s87 offen offset:64 sc1\n"
"s_add_u32 s87,s87,40960\n"
CV "v160,v160,v161\n"
CV "v161,v162,v163\n"
CV "v162,v164,v165\n"
CV "v163,v166,v167\n"
"v_permlane16_swap_b32 v152,v154\n"
"v_permlane16_swap_b32 v153,v155\n"
"buffer_store_dwordx4 v[152:155],v242,s[44:47],s87 offen offset:64 sc1\n"
"s_add_u32 s87,s87,40960\n"
CV "v168,v168,v169\n"
CV "v169,v170,v171\n"
CV "v170,v172,v173\n"
CV "v171,v174,v175\n"
"v_permlane16_swap_b32 v160,v162\n"
"v_permlane16_swap_b32 v161,v163\n"
"buffer_store_dwordx4 v[160:163],v242,s[44:47],s87 offen offset:64 sc1\n"
"s_add_u32 s87,s87,40960\n"
"v_permlane16_swap_b32 v168,v170\n"
"v_permlane16_swap_b32 v169,v171\n"
"buffer_store_dwordx4 v[168:171],v242,s[44:47],s87 offen offset:64 sc1\n"
"s_add_u32 s87,s87,40960\n"
"s_cmp_eq_u32 s1,0xf0000000\n"
"s_cbranch_scc1 .Ljx0\n"
"s_waitcnt vmcnt(5)\n"
"v_accvgpr_mov_b32 a96,a80\n"
"v_accvgpr_mov_b32 a97,a81\n"
"v_accvgpr_mov_b32 a136,a120\n"
"v_accvgpr_mov_b32 a137,a121\n"
MF "v[196:199],a[82:85],a[250:253],0\n"
MF "v[192:195],a[86:89],a[250:253],0\n"
MF "v[196:199],a[90:93],a[250:253],v[196:199]\n"
MF "v[192:195],a[94:97],a[250:253],v[192:195]\n"
MF "v[196:199],a[122:125],a[250:253],v[196:199]\n"
MF "v[192:195],a[126:129],a[250:253],v[192:195]\n"
MF "v[196:199],a[130:133],a[250:253],v[196:199]\n"
MF "v[192:195],a[134:137],a[250:253],v[192:195]\n"
"s_nop 7\n"
CV "v192,v192,v193\n"
CV "v193,v194,v195\n"
CV "v194,v196,v197\n"
CV "v195,v198,v199\n"
"s_nop 0\n"
"v_permlane16_swap_b32 v192,v194\n"
"v_permlane16_swap_b32 v193,v195\n"
"buffer_store_dwordx4 v[192:195],v8,s[44:47],s1 offen sc1\n"
".Ljx0:\n"
"s_cmp_lt_u32 s92,40\n"
"s_cbranch_scc1 .Ljobs\n"
"s_endpgm\n"
".Lpgrole1_h:\n"
"s_cbranch_execz .Lpg32h\n"
".Lrole1:\n"
MF "v[16:19],a[120:123],v[176:179],v[252:255]\n"
"s_mov_b32 s65,128\n"
TR "v[188:189],v1 offset:24576\n"
AR "v220,a44\n"
MF "v[16:19],a[160:163],v[180:183],v[16:19]\n"
"s_mov_b32 s9,0x3f80\n"
TR "v[190:191],v1 offset:26624\n"
AR "v221,a45\n"
MF "v[16:19],a[200:203],v[184:187],v[16:19]\n"
AR "v222,a46\n"
AR "v223,a47\n"
MF "v[24:27],a[204:207],v[184:187],v[252:255]\n"
"s_bfe_u32 s58,s22,0x30014\n"
TR "a[240:241],v1 offset:28672\n"
AR "v224,a64\n"
MF "v[24:27],a[164:167],v[180:183],v[24:27]\n"
"s_add_u32 s61,s58,1\n"
TR "a[242:243],v1 offset:30720\n"
AR "v225,a65\n"
MF "v[24:27],a[124:127],v[176:179],v[24:27]\n"
"s_and_b32 s61,s61,7\n"
AR "v226,a66\n"
AR "v227,a67\n"
MF "v[32:35],a[128:131],v[176:179],v[252:255]\n"
"s_mov_b32 s64,9\n"
TR "a[244:245],v1 offset:32768\n"
"ds_read_b128 a[64:67],v236 offset:49152\n"
MF "v[32:35],a[168:171],v[180:183],v[32:35]\n"
"s_xor_b32 s67,s78,8\n"
TR "a[246:247],v1 offset:34816\n"
"v_fma_f32 v220,v220,v209,v215\n"
MF "v[32:35],a[208:211],v[184:187],v[32:35]\n"
"s_mov_b32 s71,204800\n"
"v_fma_f32 v221,v221,v209,v215\n"
"v_fma_f32 v222,v222,v209,v215\n"
MF "v[40:43],a[212:215],v[184:187],v[252:255]\n"
"s_mov_b32 s66,204800\n"
"v_fma_f32 v223,v223,v209,v215\n"
"v_fma_f32 v224,v224,v209,v215\n"
MF "v[40:43],a[172:175],v[180:183],v[40:43]\n"
"s_and_b32 s73,s56,1\n"
"v_fma_f32 v225,v225,v209,v215\n"
"v_fma_f32 v226,v226,v209,v215\n"
MF "v[40:43],a[132:135],v[176:179],v[40:43]\n"
"s_mul_i32 s73,s73,5120\n"
"v_fma_f32 v227,v227,v209,v215\n"
CV "v220,v220,v221\n"
MF "v[48:51],a[136:139],v[176:179],v[252:255]\n"
"s_add_u32 s73,s73,s93\n"
CV "v221,v222,v223\n"
CV "v222,v224,v225\n"
MF "v[48:51],a[176:179],v[180:183],v[48:51]\n"
"s_xor_b32 s58,s58,4\n"
CV "v223,v226,v227\n"
AW "a44,v220\n"
MF "v[48:51],a[216:219],v[184:187],v[48:51]\n"
"s_waitcnt lgkmcnt(0)\n"
AW "a45,v221\n"
AW "a46,v222\n"
MF "v[52:55],a[216:219],a[244:247],v[252:255]\n"
"s_mul_i32 s70,s58,204800\n"
TR "v[176:177],v2 offset:24576\n"
AW "a47,v223\n"
MF "v[52:55],a[176:179],a[240:243],v[52:55]\n"
"s_add_u32 s70,s70,s73\n"
TR "v[178:179],v2 offset:26624\n"
AR "v228,a84\n"
MF "v[52:55],a[136:139],v[188:191],v[52:55]\n"
"s_add_u32 s70,s70,0xc800\n"
AR "v229,a85\n"
AR "v230,a86\n"
MF "v[44:47],a[132:135],v[188:191],v[252:255]\n"
"s_mov_b32 s85,1\n"
TR "v[180:181],v2 offset:28672\n"
AR "v231,a87\n"
MF "v[44:47],a[172:175],a[240:243],v[44:47]\n"
"s_mov_b32 s83,0xf0000000\n"
TR "v[182:183],v2 offset:30720\n"
AR "v232,a104\n"
MF "v[44:47],a[212:215],a[244:247],v[44:47]\n"
"s_mov_b32 s82,0xf0000000\n"
AR "v233,a105\n"
AR "v234,a106\n"
MF "v[36:39],a[208:211],a[244:247],v[252:255]\n"
"s_add_u32 s58,s79,73728\n"
TR "v[184:185],v2 offset:32768\n"
AR "v235,a107\n"
MF "v[36:39],a[168:171],a[240:243],v[36:39]\n"
"s_mov_b32 s90,s91\n"
TR "v[186:187],v2 offset:34816\n"
"ds_read_b128 a[104:107],v237 offset:4096\n"
MF "v[36:39],a[128:131],v[188:191],v[36:39]\n"
"s_add_u32 s91,s91,20480\n"
"v_fma_f32 v228,v228,v209,v215\n"
"v_fma_f32 v229,v229,v209,v215\n"
MF "v[28:31],a[124:127],v[188:191],v[252:255]\n"
"s_bfe_u32 s74,s23,0x70000\n"
"v_fma_f32 v230,v230,v209,v215\n"
"v_fma_f32 v231,v231,v209,v215\n"
MF "v[28:31],a[164:167],a[240:243],v[28:31]\n"
"s_lshl_b32 s74,s74,1\n"
"v_fma_f32 v232,v232,v209,v215\n"
"v_fma_f32 v233,v233,v209,v215\n"
MF "v[28:31],a[204:207],a[244:247],v[28:31]\n"
"s_add_u32 s74,s74,s56\n"
"v_fma_f32 v234,v234,v209,v215\n"
"v_fma_f32 v235,v235,v209,v215\n"
MF "v[20:23],a[200:203],a[244:247],v[252:255]\n"
"s_sub_u32 s74,s74,2\n"
CV "v228,v228,v229\n"
CV "v229,v230,v231\n"
MF "v[20:23],a[160:163],a[240:243],v[20:23]\n"
"s_mul_i32 s74,s74,30720\n"
CV "v230,v232,v233\n"
CV "v231,v234,v235\n"
MF "v[20:23],a[120:123],v[188:191],v[20:23]\n"
"s_waitcnt lgkmcnt(1)\n"
AW "a84,v228\n"
AW "a85,v229\n"
MF "v[56:59],a[120:123],v[176:179],v[252:255]\n"
"s_bfe_u32 s68,s23,0x30003\n"
TR "v[188:189],v3 offset:24576\n"
"v_add_u32 v198,59392,v192\n"
MF "v[56:59],a[160:163],v[180:183],v[56:59]\n"
"s_add_u32 s68,s68,7\n"
TR "v[190:191],v3 offset:26624\n"
"v_add_u32 v199,68608,v192\n"
MF "v[56:59],a[200:203],v[184:187],v[56:59]\n"
"s_lshl_b32 s68,s68,7\n"
"v_add_u32 v200,81920,v192\n"
"v_add_u32 v201,91136,v192\n"
MF "v[64:67],a[204:207],v[184:187],v[252:255]\n"
"s_and_b32 s68,s68,768\n"
TR "a[240:241],v3 offset:28672\n"
"v_add_u32 v202,100352,v192\n"
MF "v[64:67],a[164:167],v[180:183],v[64:67]\n"
"s_mul_i32 s95,s92,3277\n"
TR "a[242:243],v3 offset:30720\n"
"v_add_u32 v203,109568,v192\n"
MF "v[64:67],a[124:127],v[176:179],v[64:67]\n"
"s_lshr_b32 s95,s95,16\n"
AW "a86,v230\n"
AW "a87,v231\n"
MF "v[72:75],a[128:131],v[176:179],v[252:255]\n"
"s_mul_i32 s59,s95,20\n"
TR "a[244:245],v3 offset:32768\n"
AR "v220,a8\n"
MF "v[72:75],a[168:171],v[180:183],v[72:75]\n"
"s_sub_u32 s59,s92,s59\n"
TR "a[246:247],v3 offset:34816\n"
AR "v221,a9\n"
MF "v[72:75],a[208:211],v[184:187],v[72:75]\n"
"s_cmp_ge_u32 s59,10\n"
"s_cselect_b32 s96,4294557760,0\n"
AR "v222,a10\n"
MF "v[80:83],a[212:215],v[184:187],v[252:255]\n"
"s_add_u32 s96,s96,1024\n"
AR "v223,a11\n"
AR "v224,a28\n"
MF "v[80:83],a[172:175],v[180:183],v[80:83]\n"
"s_mul_i32 s59,s59,40960\n"
AR "v225,a29\n"
AR "v226,a30\n"
MF "v[80:83],a[132:135],v[176:179],v[80:83]\n"
"s_add_u32 s96,s96,s59\n"
AR "v227,a31\n"
"v_fma_f32 v220,v220,v210,v216\n"
MF "v[88:91],a[136:139],v[176:179],v[252:255]\n"
"s_lshl_b32 s59,s95,7\n"
"ds_read_b128 a[28:31],v236 offset:32768\n"
"v_fma_f32 v221,v221,v210,v216\n"
MF "v[88:91],a[176:179],v[180:183],v[88:91]\n"
"s_add_u32 s96,s96,s59\n"
"v_fma_f32 v222,v222,v210,v216\n"
"v_fma_f32 v223,v223,v210,v216\n"
MF "v[88:91],a[216:219],v[184:187],v[88:91]\n"
"s_waitcnt vmcnt(0) lgkmcnt(0)\n"
"s_barrier\n"
MF "v[92:95],a[216:219],a[244:247],v[252:255]\n"
"s_add_u32 s89,s89,s65\n"
TR "v[176:177],v0\n"
"v_fma_f32 v224,v224,v210,v216\n"
MF "v[92:95],a[176:179],a[240:243],v[92:95]\n"
"s_add_u32 m0,s79,24576\n"
TR "v[178:179],v0 offset:2048\n"
"v_fma_f32 v225,v225,v210,v216\n"
MF "v[92:95],a[136:139],v[188:191],v[92:95]\n"
LD "v192,s[48:51],s89 offen lds\n"
MF "v[84:87],a[132:135],v[188:191],v[252:255]\n"
"s_lshl_b32 s2,s92,10\n"
TR "v[180:181],v0 offset:4096\n"
"v_fma_f32 v226,v226,v210,v216\n"
MF "v[84:87],a[172:175],a[240:243],v[84:87]\n"
"s_add_u32 s2,s2,s93\n"
TR "v[182:183],v0 offset:6144\n"
"v_fma_f32 v227,v227,v210,v216\n"
MF "v[84:87],a[212:215],a[244:247],v[84:87]\n"
LD "v193,s[48:51],s89 offen offset:1024 lds\n"
MF "v[76:79],a[208:211],a[244:247],v[252:255]\n"
"s_mov_b32 s0,s96\n"
TR "v[184:185],v0 offset:8192\n"
CV "v220,v220,v221\n"
MF "v[76:79],a[168:171],a[240:243],v[76:79]\n"
"s_xor_b32 s92,s92,32\n"
TR "v[186:187],v0 offset:10240\n"
CV "v221,v222,v223\n"
MF "v[76:79],a[128:131],v[188:191],v[76:79]\n"
"s_mul_i32 s95,s92,3277\n"
TR "v[96:97],v0 offset:49152\n"
CV "v222,v224,v225\n"
MF "v[68:71],a[124:127],v[188:191],v[252:255]\n"
"s_lshr_b32 s95,s95,16\n"
TR "v[98:99],v0 offset:51200\n"
CV "v223,v226,v227\n"
MF "v[68:71],a[164:167],a[240:243],v[68:71]\n"
LD "v194,s[48:51],s89 offen offset:2048 lds\n"
MF "v[68:71],a[204:207],a[244:247],v[68:71]\n"
"s_mul_i32 s59,s95,20\n"
TR "v[100:101],v0 offset:53248\n"
AW "a8,v220\n"
MF "v[60:63],a[200:203],a[244:247],v[252:255]\n"
"s_sub_u32 s59,s92,s59\n"
TR "v[102:103],v0 offset:55296\n"
AW "a9,v221\n"
MF "v[60:63],a[160:163],a[240:243],v[60:63]\n"
"s_cmp_ge_u32 s59,10\n"
"s_cselect_b32 s96,4294557760,0\n"
TR "v[104:105],v0 offset:57344\n"
MF "v[60:63],a[120:123],v[188:191],v[60:63]\n"
"s_waitcnt lgkmcnt(5)\n"
LD "v195,s[48:51],s89 offen offset:3072 lds\n"
MF "v[16:19],a[140:143],v[176:179],v[16:19]\n"
"s_add_u32 s96,s96,1024\n"
TR "v[188:189],v1\n"
AW "a10,v222\n"
MF "v[16:19],a[180:183],v[180:183],v[16:19]\n"
"s_mul_i32 s59,s59,40960\n"
TR "v[190:191],v1 offset:2048\n"
AW "a11,v223\n"
MF "v[16:19],a[220:223],v[184:187],v[16:19]\n"
"s_add_u32 s96,s96,s59\n"
TR "v[106:107],v0 offset:59392\n"
AR "v228,a48\n"
MF "v[24:27],a[224:227],v[184:187],v[24:27]\n"
"s_lshl_b32 s59,s95,7\n"
TR "a[240:241],v1 offset:4096\n"
AR "v229,a49\n"
MF "v[24:27],a[184:187],v[180:183],v[24:27]\n"
"s_add_u32 m0,s79,28672\n"
TR "a[242:243],v1 offset:6144\n"
AR "v230,a50\n"
MF "v[24:27],a[144:147],v[176:179],v[24:27]\n"
LD "v196,s[48:51],s89 offen lds\n"
MF "v[32:35],a[148:151],v[176:179],v[32:35]\n"
"s_add_u32 s96,s96,s59\n"
TR "a[244:245],v1 offset:8192\n"
AR "v231,a51\n"
MF "v[32:35],a[188:191],v[180:183],v[32:35]\n"
"s_lshl_b32 s3,s92,10\n"
TR "a[246:247],v1 offset:10240\n"
AR "v232,a68\n"
MF "v[32:35],a[228:231],v[184:187],v[32:35]\n"
LD "v197,s[48:51],s89 offen offset:1024 lds\n"
MF "v[40:43],a[232:235],v[184:187],v[40:43]\n"
"s_add_u32 s3,s3,s93\n"
TR "v[108:109],v4\n"
AR "v233,a69\n"
MF "v[40:43],a[192:195],v[180:183],v[40:43]\n"
"s_cmp_lt_u32 s92,40\n"
"s_cselect_b32 s3,s3,0xf0000000\n"
TR "v[110:111],v4 offset:2048\n"
MF "v[40:43],a[152:155],v[176:179],v[40:43]\n"
"s_cmp_lt_u32 s92,40\n"
"s_cselect_b32 s1,s96,0xf0000000\n"
TR "v[112:113],v4 offset:4096\n"
MF "v[48:51],a[156:159],v[176:179],v[48:51]\n"
"s_xor_b32 s92,s92,32\n"
TR "v[114:115],v4 offset:6144\n"
AR "v234,a70\n"
MF "v[48:51],a[196:199],v[180:183],v[48:51]\n"
LD "v198,s[48:51],s89 offen offset:2048 lds\n"
MF "v[48:51],a[236:239],v[184:187],v[48:51]\n"
"s_waitcnt lgkmcnt(4)\n"
TR "v[116:117],v4 offset:8192\n"
AR "v235,a71\n"
MF "v[52:55],a[236:239],a[244:247],v[52:55]\n"
"s_nop 0\n"
TR "v[176:177],v2\n"
"ds_read_b128 a[68:71],v236 offset:53248\n"
MF "v[52:55],a[196:199],a[240:243],v[52:55]\n"
TR "v[178:179],v2 offset:2048\n"
"v_fma_f32 v228,v228,v210,v216\n"
MF "v[52:55],a[156:159],v[188:191],v[52:55]\n"
LD "v199,s[48:51],s89 offen offset:3072 lds\n"
MF "v[44:47],a[152:155],v[188:191],v[44:47]\n"
TR "v[180:181],v2 offset:4096\n"
"v_fma_f32 v229,v229,v210,v216\n"
MF "v[44:47],a[192:195],a[240:243],v[44:47]\n"
TR "v[182:183],v2 offset:6144\n"
"v_fma_f32 v230,v230,v210,v216\n"
MF "v[44:47],a[232:235],a[244:247],v[44:47]\n"
TR "v[118:119],v4 offset:10240\n"
"v_fma_f32 v231,v231,v210,v216\n"
MF "v[36:39],a[228:231],a[244:247],v[36:39]\n"
TR "v[184:185],v2 offset:8192\n"
"v_fma_f32 v232,v232,v210,v216\n"
MF "v[36:39],a[188:191],a[240:243],v[36:39]\n"
"s_add_u32 m0,s79,32768\n"
TR "v[186:187],v2 offset:10240\n"
"v_fma_f32 v233,v233,v210,v216\n"
MF "v[36:39],a[148:151],v[188:191],v[36:39]\n"
LD "v200,s[48:51],s89 offen lds\n"
MF "v[28:31],a[144:147],v[188:191],v[28:31]\n"
TR "v[120:121],v1 offset:49152\n"
"v_fma_f32 v234,v234,v210,v216\n"
MF "v[28:31],a[184:187],a[240:243],v[28:31]\n"
TR "v[122:123],v1 offset:51200\n"
"v_fma_f32 v235,v235,v210,v216\n"
MF "v[28:31],a[224:227],a[244:247],v[28:31]\n"
TR "v[124:125],v1 offset:53248\n"
CV "v228,v228,v229\n"
MF "v[20:23],a[220:223],a[244:247],v[20:23]\n"
LD "v201,s[48:51],s89 offen offset:1024 lds\n"
MF "v[20:23],a[180:183],a[240:243],v[20:23]\n"
TR "v[126:127],v1 offset:55296\n"
CV "v229,v230,v231\n"
MF "v[20:23],a[140:143],v[188:191],v[20:23]\n"
"s_waitcnt lgkmcnt(4)\n"
TR "v[128:129],v1 offset:57344\n"
CV "v230,v232,v233\n"
MF "v[56:59],a[140:143],v[176:179],v[56:59]\n"
TR "v[188:189],v3\n"
CV "v231,v234,v235\n"
MF "v[56:59],a[180:183],v[180:183],v[56:59]\n"
".Lpg29h:\n"
"s_cbranch_execz .Lpg33h\n"
TR "v[190:191],v3 offset:2048\n"
AW "a48,v228\n"
MF "v[56:59],a[220:223],v[184:187],v[56:59]\n"
LD "v202,s[48:51],s89 offen offset:2048 lds\n"
MF "v[64:67],a[224:227],v[184:187],v[64:67]\n"
TR "a[240:241],v3 offset:4096\n"
AW "a49,v229\n"
MF "v[64:67],a[184:187],v[180:183],v[64:67]\n"
TR "a[242:243],v3 offset:6144\n"
AW "a50,v230\n"
MF "v[64:67],a[144:147],v[176:179],v[64:67]\n"
LD "v203,s[48:51],s89 offen offset:3072 lds\n"
MF "v[72:75],a[148:151],v[176:179],v[72:75]\n"
TR "a[244:245],v3 offset:8192\n"
AW "a51,v231\n"
MF "v[72:75],a[188:191],v[180:183],v[72:75]\n"
TR "a[246:247],v3 offset:10240\n"
AR "v220,a88\n"
MF "v[72:75],a[228:231],v[184:187],v[72:75]\n"
TR "v[130:131],v1 offset:59392\n"
AR "v221,a89\n"
MF "v[80:83],a[232:235],v[184:187],v[80:83]\n"
TR "v[132:133],v5\n"
AR "v222,a90\n"
MF "v[80:83],a[192:195],v[180:183],v[80:83]\n"
TR "v[134:135],v5 offset:2048\n"
AR "v223,a91\n"
MF "v[80:83],a[152:155],v[176:179],v[80:83]\n"
TR "v[136:137],v5 offset:4096\n"
AR "v224,a108\n"
MF "v[88:91],a[156:159],v[176:179],v[88:91]\n"
TR "v[138:139],v5 offset:6144\n"
AR "v225,a109\n"
MF "v[88:91],a[196:199],v[180:183],v[88:91]\n"
TR "v[140:141],v5 offset:8192\n"
AR "v226,a110\n"
MF "v[88:91],a[236:239],v[184:187],v[88:91]\n"
"s_waitcnt lgkmcnt(0)\n"
"s_barrier\n"
MF "v[92:95],a[236:239],a[244:247],v[92:95]\n"
TR "v[142:143],v5 offset:10240\n"
AR "v227,a111\n"
MF "v[92:95],a[196:199],a[240:243],v[92:95]\n"
"s_nop 0\n"
TR "v[144:145],v2 offset:49152\n"
"ds_read_b128 a[108:111],v237 offset:8192\n"
MF "v[92:95],a[156:159],v[188:191],v[92:95]\n"
TR "v[146:147],v2 offset:51200\n"
"v_fma_f32 v220,v220,v210,v216\n"
MF "v[84:87],a[152:155],v[188:191],v[84:87]\n"
TR "v[148:149],v2 offset:53248\n"
"v_fma_f32 v221,v221,v210,v216\n"
MF "v[84:87],a[192:195],a[240:243],v[84:87]\n"
TR "v[150:151],v2 offset:55296\n"
"v_fma_f32 v222,v222,v210,v216\n"
MF "v[84:87],a[232:235],a[244:247],v[84:87]\n"
TR "v[152:153],v2 offset:57344\n"
"v_fma_f32 v223,v223,v210,v216\n"
MF "v[76:79],a[228:231],a[244:247],v[76:79]\n"
TR "v[154:155],v2 offset:59392\n"
"v_fma_f32 v224,v224,v210,v216\n"
MF "v[76:79],a[188:191],a[240:243],v[76:79]\n"
TR "v[156:157],v6\n"
"v_fma_f32 v225,v225,v210,v216\n"
MF "v[76:79],a[148:151],v[188:191],v[76:79]\n"
TR "v[158:159],v6 offset:2048\n"
"v_fma_f32 v226,v226,v210,v216\n"
MF "v[68:71],a[144:147],v[188:191],v[68:71]\n"
TR "v[160:161],v6 offset:4096\n"
"v_fma_f32 v227,v227,v210,v216\n"
MF "v[68:71],a[184:187],a[240:243],v[68:71]\n"
TR "v[162:163],v6 offset:6144\n"
CV "v220,v220,v221\n"
MF "v[68:71],a[224:227],a[244:247],v[68:71]\n"
TR "v[164:165],v6 offset:8192\n"
CV "v221,v222,v223\n"
MF "v[60:63],a[220:223],a[244:247],v[60:63]\n"
TR "v[166:167],v6 offset:10240\n"
CV "v222,v224,v225\n"
MF "v[60:63],a[180:183],a[240:243],v[60:63]\n"
TR "v[168:169],v3 offset:49152\n"
CV "v223,v226,v227\n"
MF "v[60:63],a[140:143],v[188:191],v[60:63]\n"
TR "v[170:171],v3 offset:51200\n"
AW "a88,v220\n"
MF "v[16:19],a[0:3],v[96:99],v[16:19]\n"
TR "v[172:173],v3 offset:53248\n"
AW "a89,v221\n"
MF "v[16:19],a[40:43],v[100:103],v[16:19]\n"
TR "v[174:175],v3 offset:55296\n"
AW "a90,v222\n"
MF "v[16:19],a[80:83],v[104:107],v[16:19]\n"
TR "v[204:205],v3 offset:57344\n"
AW "a91,v223\n"
MF "v[16:19],a[20:23],v[108:111],v[16:19]\n"
TR "v[206:207],v3 offset:59392\n"
AR "v228,a12\n"
MF "v[16:19],a[60:63],v[112:115],v[16:19]\n"
TR "v[244:245],v7\n"
AR "v229,a13\n"
MF "v[16:19],a[100:103],v[116:119],v[16:19]\n"
"s_waitcnt lgkmcnt(15)\n"
TR "v[246:247],v7 offset:2048\n"
AR "v230,a14\n"
MF "v[20:23],a[100:103],v[140:143],v[20:23]\n"
TR "v[248:249],v7 offset:4096\n"
AR "v231,a15\n"
MF "v[20:23],a[60:63],v[136:139],v[20:23]\n"
TR "v[250:251],v7 offset:6144\n"
AR "v232,a32\n"
MF "v[20:23],a[20:23],v[132:135],v[20:23]\n"
TR "v[238:239],v7 offset:8192\n"
AR "v233,a33\n"
MF "v[20:23],a[80:83],v[128:131],v[20:23]\n"
TR "v[240:241],v7 offset:10240\n"
AR "v234,a34\n"
MF "v[20:23],a[40:43],v[124:127],v[20:23]\n"
AR "v235,a35\n"
"v_fma_f32 v228,v228,v211,v217\n"
MF "v[20:23],a[0:3],v[120:123],v[20:23]\n"
"s_waitcnt lgkmcnt(12)\n"
"ds_read_b128 a[32:35],v236 offset:36864\n"
"v_fma_f32 v229,v229,v211,v217\n"
MF "v[56:59],a[0:3],v[144:147],v[56:59]\n"
"v_fma_f32 v230,v230,v211,v217\n"
"v_fma_f32 v231,v231,v211,v217\n"
MF "v[56:59],a[40:43],v[148:151],v[56:59]\n"
"v_fma_f32 v232,v232,v211,v217\n"
"v_fma_f32 v233,v233,v211,v217\n"
MF "v[56:59],a[80:83],v[152:155],v[56:59]\n"
"v_fma_f32 v234,v234,v211,v217\n"
"v_fma_f32 v235,v235,v211,v217\n"
MF "v[56:59],a[20:23],v[156:159],v[56:59]\n"
CV "v228,v228,v229\n"
CV "v229,v230,v231\n"
MF "v[56:59],a[60:63],v[160:163],v[56:59]\n"
CV "v230,v232,v233\n"
CV "v231,v234,v235\n"
MF "v[56:59],a[100:103],v[164:167],v[56:59]\n"
"s_waitcnt lgkmcnt(1)\n"
AW "a12,v228\n"
AW "a13,v229\n"
MF "v[60:63],a[100:103],v[238:241],v[60:63]\n"
AW "a14,v230\n"
AW "a15,v231\n"
MF "v[60:63],a[60:63],v[248:251],v[60:63]\n"
AR "v220,a52\n"
AR "v221,a53\n"
MF "v[60:63],a[20:23],v[244:247],v[60:63]\n"
AR "v222,a54\n"
AR "v223,a55\n"
MF "v[60:63],a[80:83],v[204:207],v[60:63]\n"
AR "v224,a72\n"
AR "v225,a73\n"
MF "v[60:63],a[40:43],v[172:175],v[60:63]\n"
AR "v226,a74\n"
AR "v227,a75\n"
MF "v[60:63],a[0:3],v[168:171],v[60:63]\n"
"ds_read_b128 a[72:75],v236 offset:57344\n"
"v_fma_f32 v220,v220,v211,v217\n"
MF "v[68:71],a[4:7],v[168:171],v[68:71]\n"
"v_fma_f32 v221,v221,v211,v217\n"
"v_fma_f32 v222,v222,v211,v217\n"
MF "v[68:71],a[44:47],v[172:175],v[68:71]\n"
"v_fma_f32 v223,v223,v211,v217\n"
"v_fma_f32 v224,v224,v211,v217\n"
MF "v[68:71],a[84:87],v[204:207],v[68:71]\n"
"v_fma_f32 v225,v225,v211,v217\n"
"v_fma_f32 v226,v226,v211,v217\n"
MF "v[68:71],a[24:27],v[244:247],v[68:71]\n"
"v_fma_f32 v227,v227,v211,v217\n"
CV "v220,v220,v221\n"
MF "v[68:71],a[64:67],v[248:251],v[68:71]\n"
CV "v221,v222,v223\n"
CV "v222,v224,v225\n"
MF "v[68:71],a[104:107],v[238:241],v[68:71]\n"
CV "v223,v226,v227\n"
AW "a52,v220\n"
MF "v[64:67],a[104:107],v[164:167],v[64:67]\n"
"s_waitcnt lgkmcnt(0)\n"
"s_barrier\n"
MF "v[64:67],a[64:67],v[160:163],v[64:67]\n"
AW "a53,v221\n"
AW "a54,v222\n"
MF "v[64:67],a[24:27],v[156:159],v[64:67]\n"
AW "a55,v223\n"
AR "v228,a92\n"
MF "v[64:67],a[84:87],v[152:155],v[64:67]\n"
AR "v229,a93\n"
AR "v230,a94\n"
MF "v[64:67],a[44:47],v[148:151],v[64:67]\n"
AR "v231,a95\n"
AR "v232,a112\n"
MF "v[64:67],a[4:7],v[144:147],v[64:67]\n"
AR "v233,a113\n"
AR "v234,a114\n"
MF "v[28:31],a[4:7],v[120:123],v[28:31]\n"
AR "v235,a115\n"
"v_fma_f32 v228,v228,v211,v217\n"
MF "v[28:31],a[44:47],v[124:127],v[28:31]\n"
"ds_read_b128 a[112:115],v237 offset:12288\n"
"v_fma_f32 v229,v229,v211,v217\n"
MF "v[28:31],a[84:87],v[128:131],v[28:31]\n"
"v_fma_f32 v230,v230,v211,v217\n"
"v_fma_f32 v231,v231,v211,v217\n"
MF "v[28:31],a[24:27],v[132:135],v[28:31]\n"
"v_fma_f32 v232,v232,v211,v217\n"
"v_fma_f32 v233,v233,v211,v217\n"
MF "v[28:31],a[64:67],v[136:139],v[28:31]\n"
"v_fma_f32 v234,v234,v211,v217\n"
"v_fma_f32 v235,v235,v211,v217\n"
MF "v[28:31],a[104:107],v[140:143],v[28:31]\n"
CV "v228,v228,v229\n"
CV "v229,v230,v231\n"
MF "v[24:27],a[104:107],v[116:119],v[24:27]\n"
CV "v230,v232,v233\n"
CV "v231,v234,v235\n"
MF "v[24:27],a[64:67],v[112:115],v[24:27]\n"
AW "a92,v228\n"
AW "a93,v229\n"
MF "v[24:27],a[24:27],v[108:111],v[24:27]\n"
AW "a94,v230\n"
AW "a95,v231\n"
MF "v[24:27],a[84:87],v[104:107],v[24:27]\n"
AR "v220,a16\n"
AR "v221,a17\n"
MF "v[24:27],a[44:47],v[100:103],v[24:27]\n"
AR "v222,a18\n"
AR "v223,a19\n"
MF "v[24:27],a[4:7],v[96:99],v[24:27]\n"
AR "v224,a36\n"
AR "v225,a37\n"
MF "v[32:35],a[8:11],v[96:99],v[32:35]\n"
AR "v226,a38\n"
AR "v227,a39\n"
MF "v[32:35],a[48:51],v[100:103],v[32:35]\n"
"ds_read_b128 a[36:39],v236 offset:40960\n"
"v_fma_f32 v220,v220,v212,v218\n"
MF "v[32:35],a[88:91],v[104:107],v[32:35]\n"
"v_fma_f32 v221,v221,v212,v218\n"
"v_fma_f32 v222,v222,v212,v218\n"
MF "v[32:35],a[28:31],v[108:111],v[32:35]\n"
"v_fma_f32 v223,v223,v212,v218\n"
"v_fma_f32 v224,v224,v212,v218\n"
MF "v[32:35],a[68:71],v[112:115],v[32:35]\n"
"v_fma_f32 v225,v225,v212,v218\n"
"v_fma_f32 v226,v226,v212,v218\n"
MF "v[32:35],a[108:111],v[116:119],v[32:35]\n"
"v_fma_f32 v227,v227,v212,v218\n"
CV "v220,v220,v221\n"
MF "v[36:39],a[108:111],v[140:143],v[36:39]\n"
CV "v221,v222,v223\n"
CV "v222,v224,v225\n"
MF "v[36:39],a[68:71],v[136:139],v[36:39]\n"
CV "v223,v226,v227\n"
AW "a16,v220\n"
MF "v[36:39],a[28:31],v[132:135],v[36:39]\n"
AW "a17,v221\n"
AW "a18,v222\n"
MF "v[36:39],a[88:91],v[128:131],v[36:39]\n"
AW "a19,v223\n"
AR "v228,a56\n"
MF "v[36:39],a[48:51],v[124:127],v[36:39]\n"
AR "v229,a57\n"
AR "v230,a58\n"
MF "v[36:39],a[8:11],v[120:123],v[36:39]\n"
AR "v231,a59\n"
AR "v232,a76\n"
MF "v[72:75],a[8:11],v[144:147],v[72:75]\n"
AR "v233,a77\n"
AR "v234,a78\n"
MF "v[72:75],a[48:51],v[148:151],v[72:75]\n"
AR "v235,a79\n"
"v_fma_f32 v228,v228,v212,v218\n"
MF "v[72:75],a[88:91],v[152:155],v[72:75]\n"
"ds_read_b128 a[76:79],v236 offset:61440\n"
"v_fma_f32 v229,v229,v212,v218\n"
MF "v[72:75],a[28:31],v[156:159],v[72:75]\n"
"v_fma_f32 v230,v230,v212,v218\n"
"v_fma_f32 v231,v231,v212,v218\n"
MF "v[72:75],a[68:71],v[160:163],v[72:75]\n"
"v_fma_f32 v232,v232,v212,v218\n"
"v_fma_f32 v233,v233,v212,v218\n"
MF "v[72:75],a[108:111],v[164:167],v[72:75]\n"
"v_fma_f32 v234,v234,v212,v218\n"
"v_fma_f32 v235,v235,v212,v218\n"
MF "v[76:79],a[108:111],v[238:241],v[76:79]\n"
CV "v228,v228,v229\n"
CV "v229,v230,v231\n"
MF "v[76:79],a[68:71],v[248:251],v[76:79]\n"
CV "v230,v232,v233\n"
CV "v231,v234,v235\n"
MF "v[76:79],a[28:31],v[244:247],v[76:79]\n"
AW "a56,v228\n"
AW "a57,v229\n"
MF "v[76:79],a[88:91],v[204:207],v[76:79]\n"
AW "a58,v230\n"
AW "a59,v231\n"
MF "v[76:79],a[48:51],v[172:175],v[76:79]\n"
AR "v220,a96\n"
AR "v221,a97\n"
MF "v[76:79],a[8:11],v[168:171],v[76:79]\n"
"s_waitcnt lgkmcnt(2)\n"
AR "v222,a98\n"
AR "v223,a99\n"
MF "v[84:87],a[12:15],v[168:171],v[84:87]\n"
AR "v224,a116\n"
AR "v225,a117\n"
MF "v[84:87],a[52:55],v[172:175],v[84:87]\n"
AR "v226,a118\n"
AR "v227,a119\n"
MF "v[84:87],a[92:95],v[204:207],v[84:87]\n"
"ds_read_b128 a[116:119],v237 offset:16384\n"
"v_fma_f32 v220,v220,v212,v218\n"
MF "v[84:87],a[32:35],v[244:247],v[84:87]\n"
"v_fma_f32 v221,v221,v212,v218\n"
"v_fma_f32 v222,v222,v212,v218\n"
MF "v[84:87],a[72:75],v[248:251],v[84:87]\n"
"v_fma_f32 v223,v223,v212,v218\n"
"v_fma_f32 v224,v224,v212,v218\n"
MF "v[84:87],a[112:115],v[238:241],v[84:87]\n"
"v_fma_f32 v225,v225,v212,v218\n"
"v_fma_f32 v226,v226,v212,v218\n"
MF "v[80:83],a[112:115],v[164:167],v[80:83]\n"
"v_fma_f32 v227,v227,v212,v218\n"
CV "v220,v220,v221\n"
MF "v[80:83],a[72:75],v[160:163],v[80:83]\n"
CV "v221,v222,v223\n"
CV "v222,v224,v225\n"
MF "v[80:83],a[32:35],v[156:159],v[80:83]\n"
CV "v223,v226,v227\n"
AW "a96,v220\n"
MF "v[80:83],a[92:95],v[152:155],v[80:83]\n"
AW "a97,v221\n"
AW "a98,v222\n"
MF "v[80:83],a[52:55],v[148:151],v[80:83]\n"
AW "a99,v223\n"
"v_mov_b32 v15,0\n"
MF "v[80:83],a[12:15],v[144:147],v[80:83]\n"
"v_lshrrev_b32 v242,5,v9\n"
"v_min_u32 v242,23,v242\n"
MF "v[44:47],a[12:15],v[120:123],v[44:47]\n"
"v_mul_u32_u24 v242,160,v242\n"
"v_and_or_b32 v242,v9,16,v242\n"
MF "v[44:47],a[52:55],v[124:127],v[44:47]\n"
"v_lshlrev_b32 v242,3,v242\n"
AR "v8,a252\n"
MF "v[44:47],a[92:95],v[128:131],v[44:47]\n"
AR "v11,a253\n"
"v_bfe_u32 v224,v9,5,3\n"
MF "v[44:47],a[32:35],v[132:135],v[44:47]\n"
"s_add_u32 s62,s74,s68\n"
"s_add_u32 s68,s68,256\n"
"s_and_b32 s68,s68,768\n"
MF "v[44:47],a[72:75],v[136:139],v[44:47]\n"
"buffer_load_dword v243,v242,s[48:51],s62 offen\n"
MF "v[44:47],a[112:115],v[140:143],v[44:47]\n"
"v_bfe_u32 v225,v9,8,2\n"
"v_lshlrev_b32 v225,1,v225\n"
MF "v[40:43],a[112:115],v[116:119],v[40:43]\n"
"v_mov_b32 v14,s76\n"
"s_add_u32 s76,s76,1\n"
MF "v[40:43],a[72:75],v[112:115],v[40:43]\n"
"buffer_store_dword v14,v15,s[40:43],s94 offen sc1\n"
MF "v[40:43],a[32:35],v[108:111],v[40:43]\n"
"v_and_b32 v226,16,v9\n"
"v_lshlrev_b32_e64 v226,v226,s9\n"
MF "v[40:43],a[92:95],v[104:107],v[40:43]\n"
"v_sub_u32 v224,v224,v225\n"
"v_cmp_eq_u32 vcc,0,v224\n"
MF "v[40:43],a[52:55],v[100:103],v[40:43]\n"
"s_nop 0\n"
"v_cndmask_b32 v220,0,v226,vcc\n"
"v_cmp_eq_u32 vcc,1,v224\n"
MF "v[40:43],a[12:15],v[96:99],v[40:43]\n"
"s_waitcnt lgkmcnt(0)\n"
"v_cndmask_b32 v221,0,v226,vcc\n"
AW "a248,0\n"
MF "v[48:51],a[16:19],v[96:99],v[48:51]\n"
AW "a249,0\n"
AW "a250,v220\n"
MF "v[48:51],a[56:59],v[100:103],v[48:51]\n"
AW "a251,v221\n"
AW "a252,v220\n"
MF "v[48:51],a[96:99],v[104:107],v[48:51]\n"
AW "a253,v221\n"
AW "a254,0\n"
MF "v[48:51],a[36:39],v[108:111],v[48:51]\n"
AW "a255,0\n"
"v_bfe_u32 v12,v9,4,4\n"
MF "v[48:51],a[76:79],v[112:115],v[48:51]\n"
"v_lshlrev_b32 v12,2,v12\n"
"v_lshrrev_b32 v13,2,v9\n"
MF "v[48:51],a[116:119],v[116:119],v[48:51]\n"
"v_add_u32 v13,s58,v13\n"
"v_add_u32 v4,24576,v4\n"
MF "v[52:55],a[116:119],v[140:143],v[52:55]\n"
"v_add_u32 v5,24576,v5\n"
"v_add_u32 v6,24576,v6\n"
MF "v[52:55],a[76:79],v[136:139],v[52:55]\n"
"v_add_u32 v7,24576,v7\n"
MF "v[52:55],a[36:39],v[132:135],v[52:55]\n"
"s_waitcnt vmcnt(2) lgkmcnt(0)\n"
"s_barrier\n"
MF "v[52:55],a[96:99],v[128:131],v[52:55]\n"
TR "v[176:177],v0 offset:24576\n"
MF "v[52:55],a[56:59],v[124:127],v[52:55]\n"
TR "v[178:179],v0 offset:26624\n"
MF "v[52:55],a[16:19],v[120:123],v[52:55]\n"
MF "v[88:91],a[16:19],v[144:147],v[88:91]\n"
TR "v[180:181],v0 offset:28672\n"
MF "v[88:91],a[56:59],v[148:151],v[88:91]\n"
TR "v[182:183],v0 offset:30720\n"
MF "v[88:91],a[96:99],v[152:155],v[88:91]\n"
MF "v[88:91],a[36:39],v[156:159],v[88:91]\n"
TR "v[184:185],v0 offset:32768\n"
MF "v[88:91],a[76:79],v[160:163],v[88:91]\n"
TR "v[186:187],v0 offset:34816\n"
MF "v[88:91],a[116:119],v[164:167],v[88:91]\n"
MF "v[92:95],a[116:119],v[238:241],v[92:95]\n"
MF "v[92:95],a[76:79],v[248:251],v[92:95]\n"
MF "v[92:95],a[36:39],v[244:247],v[92:95]\n"
MF "v[92:95],a[96:99],v[204:207],v[92:95]\n"
MF "v[92:95],a[56:59],v[172:175],v[92:95]\n"
MF "v[92:95],a[16:19],v[168:171],v[92:95]\n"
"s_waitcnt lgkmcnt(0)\n"
".Lpl15:\n"
MF "v[96:99],a[0:3],v[176:179],v[252:255]\n"
TR "v[188:189],v1 offset:24576\n"
MF "v[96:99],a[40:43],v[180:183],v[96:99]\n"
TR "v[190:191],v1 offset:26624\n"
MF "v[96:99],a[80:83],v[184:187],v[96:99]\n"
MF "v[104:107],a[84:87],v[184:187],v[252:255]\n"
TR "a[240:241],v1 offset:28672\n"
MF "v[104:107],a[44:47],v[180:183],v[104:107]\n"
TR "a[242:243],v1 offset:30720\n"
MF "v[104:107],a[4:7],v[176:179],v[104:107]\n"
MF "v[112:115],a[8:11],v[176:179],v[252:255]\n"
TR "a[244:245],v1 offset:32768\n"
MF "v[112:115],a[48:51],v[180:183],v[112:115]\n"
TR "a[246:247],v1 offset:34816\n"
MF "v[112:115],a[88:91],v[184:187],v[112:115]\n"
MF "v[120:123],a[92:95],v[184:187],v[252:255]\n"
MF "v[120:123],a[52:55],v[180:183],v[120:123]\n"
MF "v[120:123],a[12:15],v[176:179],v[120:123]\n"
MF "v[128:131],a[16:19],v[176:179],v[252:255]\n"
MF "v[128:131],a[56:59],v[180:183],v[128:131]\n"
MF "v[128:131],a[96:99],v[184:187],v[128:131]\n"
"s_waitcnt lgkmcnt(0)\n"
MF "v[132:135],a[96:99],a[244:247],v[252:255]\n"
TR "v[176:177],v2 offset:24576\n"
MF "v[132:135],a[56:59],a[240:243],v[132:135]\n"
TR "v[178:179],v2 offset:26624\n"
MF "v[132:135],a[16:19],v[188:191],v[132:135]\n"
MF "v[124:127],a[12:15],v[188:191],v[252:255]\n"
TR "v[180:181],v2 offset:28672\n"
MF "v[124:127],a[52:55],a[240:243],v[124:127]\n"
TR "v[182:183],v2 offset:30720\n"
MF "v[124:127],a[92:95],a[244:247],v[124:127]\n"
MF "v[116:119],a[88:91],a[244:247],v[252:255]\n"
TR "v[184:185],v2 offset:32768\n"
MF "v[116:119],a[48:51],a[240:243],v[116:119]\n"
TR "v[186:187],v2 offset:34816\n"
MF "v[116:119],a[8:11],v[188:191],v[116:119]\n"
MF "v[108:111],a[4:7],v[188:191],v[252:255]\n"
MF "v[108:111],a[44:47],a[240:243],v[108:111]\n"
MF "v[108:111],a[84:87],a[244:247],v[108:111]\n"
MF "v[100:103],a[80:83],a[244:247],v[252:255]\n"
MF "v[100:103],a[40:43],a[240:243],v[100:103]\n"
MF "v[100:103],a[0:3],v[188:191],v[100:103]\n"
"s_waitcnt lgkmcnt(0)\n"
MF "v[136:139],a[0:3],v[176:179],v[252:255]\n"
TR "v[188:189],v3 offset:24576\n"
MF "v[136:139],a[40:43],v[180:183],v[136:139]\n"
TR "v[190:191],v3 offset:26624\n"
MF "v[136:139],a[80:83],v[184:187],v[136:139]\n"
MF "v[144:147],a[84:87],v[184:187],v[252:255]\n"
TR "a[240:241],v3 offset:28672\n"
MF "v[144:147],a[44:47],v[180:183],v[144:147]\n"
TR "a[242:243],v3 offset:30720\n"
MF "v[144:147],a[4:7],v[176:179],v[144:147]\n"
MF "v[152:155],a[8:11],v[176:179],v[252:255]\n"
TR "a[244:245],v3 offset:32768\n"
MF "v[152:155],a[48:51],v[180:183],v[152:155]\n"
TR "a[246:247],v3 offset:34816\n"
MF "v[152:155],a[88:91],v[184:187],v[152:155]\n"
MF "v[160:163],a[92:95],v[184:187],v[252:255]\n"
MF "v[160:163],a[52:55],v[180:183],v[160:163]\n"
MF "v[160:163],a[12:15],v[176:179],v[160:163]\n"
MF "v[168:171],a[16:19],v[176:179],v[252:255]\n"
MF "v[168:171],a[56:59],v[180:183],v[168:171]\n"
MF "v[168:171],a[96:99],v[184:187],v[168:171]\n"
"s_waitcnt lgkmcnt(0)\n"
"s_barrier\n"
MF "v[172:175],a[96:99],a[244:247],v[252:255]\n"
TR "v[176:177],v0\n"
MF "v[172:175],a[56:59],a[240:243],v[172:175]\n"
TR "v[178:179],v0 offset:2048\n"
MF "v[172:175],a[16:19],v[188:191],v[172:175]\n"
"ds_read2st64_b32 v[192:193],v13 offset0:192 offset1:193\n"
MF "v[164:167],a[12:15],v[188:191],v[252:255]\n"
TR "v[180:181],v0 offset:4096\n"
MF "v[164:167],a[52:55],a[240:243],v[164:167]\n"
TR "v[182:183],v0 offset:6144\n"
MF "v[164:167],a[92:95],a[244:247],v[164:167]\n"
"ds_read2st64_b32 v[194:195],v13 offset0:194 offset1:195\n"
MF "v[156:159],a[88:91],a[244:247],v[252:255]\n"
TR "v[184:185],v0 offset:8192\n"
MF "v[156:159],a[48:51],a[240:243],v[156:159]\n"
TR "v[186:187],v0 offset:10240\n"
MF "v[156:159],a[8:11],v[188:191],v[156:159]\n"
"ds_read2st64_b32 v[196:197],v13 offset0:196 offset1:197\n"
MF "v[148:151],a[4:7],v[188:191],v[252:255]\n"
"ds_read2st64_b32 v[198:199],v13 offset0:198 offset1:199\n"
MF "v[148:151],a[44:47],a[240:243],v[148:151]\n"
MF "v[148:151],a[84:87],a[244:247],v[148:151]\n"
MF "v[140:143],a[80:83],a[244:247],v[252:255]\n"
MF "v[140:143],a[40:43],a[240:243],v[140:143]\n"
MF "v[140:143],a[0:3],v[188:191],v[140:143]\n"
"s_waitcnt lgkmcnt(2)\n"
MF "v[96:99],a[120:123],v[176:179],v[96:99]\n"
"s_waitcnt lgkmcnt(0)\n"
TR "v[188:189],v1\n"
"v_add_f32 v16,v16,v192\n"
MF "v[96:99],a[160:163],v[180:183],v[96:99]\n"
TR "v[190:191],v1 offset:2048\n"
"v_add_f32 v17,v17,v193\n"
MF "v[96:99],a[200:203],v[184:187],v[96:99]\n"
"ds_read2st64_b32 v[200:201],v13 offset0:200 offset1:201\n"
"v_add_f32 v18,v18,v194\n"
"s_waitcnt vmcnt(0)\n"
"v_mov_b32 v14,s76\n"
"s_add_u32 s76,s76,1\n"
MF "v[104:107],a[204:207],v[184:187],v[104:107]\n"
".Lpg30h:\n"
"s_cbranch_execz .Lpg34h\n"
TR "a[240:241],v1 offset:4096\n"
"v_add_f32 v19,v19,v195\n"
"buffer_store_dword v14,v15,s[40:43],s94 offen sc1\n"
MF "v[104:107],a[164:167],v[180:183],v[104:107]\n"
TR "a[242:243],v1 offset:6144\n"
"v_add_f32 v20,v20,v196\n"
MF "v[104:107],a[124:127],v[176:179],v[104:107]\n"
"ds_read2st64_b32 v[202:203],v13 offset0:202 offset1:203\n"
"v_add_f32 v21,v21,v197\n"
MF "v[112:115],a[128:131],v[176:179],v[112:115]\n"
TR "a[244:245],v1 offset:8192\n"
"v_add_f32 v22,v22,v198\n"
MF "v[112:115],a[168:171],v[180:183],v[112:115]\n"
TR "a[246:247],v1 offset:10240\n"
"v_add_f32 v23,v23,v199\n"
MF "v[112:115],a[208:211],v[184:187],v[112:115]\n"
"ds_read2st64_b32 v[204:205],v13 offset0:204 offset1:205\n"
CV "v16,v16,v17\n"
MF "v[120:123],a[212:215],v[184:187],v[120:123]\n"
"ds_read2st64_b32 v[206:207],v13 offset0:206 offset1:207\n"
CV "v17,v18,v19\n"
MF "v[120:123],a[172:175],v[180:183],v[120:123]\n"
"s_add_u32 s87,s90,0\n"
CV "v18,v20,v21\n"
CV "v19,v22,v23\n"
MF "v[120:123],a[132:135],v[176:179],v[120:123]\n"
MF "v[128:131],a[136:139],v[176:179],v[128:131]\n"
"buffer_store_dwordx4 v[16:19],v9,s[40:43],s87 offen sc1\n"
MF "v[128:131],a[176:179],v[180:183],v[128:131]\n"
MF "v[128:131],a[216:219],v[184:187],v[128:131]\n"
"s_waitcnt lgkmcnt(2)\n"
MF "v[132:135],a[216:219],a[244:247],v[132:135]\n"
"s_waitcnt lgkmcnt(0)\n"
TR "v[176:177],v2\n"
"v_add_f32 v24,v24,v200\n"
MF "v[132:135],a[176:179],a[240:243],v[132:135]\n"
TR "v[178:179],v2 offset:2048\n"
"v_add_f32 v25,v25,v201\n"
MF "v[132:135],a[136:139],v[188:191],v[132:135]\n"
"ds_read2st64_b32 v[192:193],v13 offset0:208 offset1:209\n"
"v_add_f32 v26,v26,v202\n"
MF "v[124:127],a[132:135],v[188:191],v[124:127]\n"
TR "v[180:181],v2 offset:4096\n"
"v_add_f32 v27,v27,v203\n"
MF "v[124:127],a[172:175],a[240:243],v[124:127]\n"
TR "v[182:183],v2 offset:6144\n"
"v_add_f32 v28,v28,v204\n"
MF "v[124:127],a[212:215],a[244:247],v[124:127]\n"
"ds_read2st64_b32 v[194:195],v13 offset0:210 offset1:211\n"
"v_add_f32 v29,v29,v205\n"
MF "v[116:119],a[208:211],a[244:247],v[116:119]\n"
TR "v[184:185],v2 offset:8192\n"
"v_add_f32 v30,v30,v206\n"
MF "v[116:119],a[168:171],a[240:243],v[116:119]\n"
TR "v[186:187],v2 offset:10240\n"
"v_add_f32 v31,v31,v207\n"
MF "v[116:119],a[128:131],v[188:191],v[116:119]\n"
"ds_read2st64_b32 v[196:197],v13 offset0:212 offset1:213\n"
CV "v24,v24,v25\n"
MF "v[108:111],a[124:127],v[188:191],v[108:111]\n"
"ds_read2st64_b32 v[198:199],v13 offset0:214 offset1:215\n"
CV "v25,v26,v27\n"
MF "v[108:111],a[164:167],a[240:243],v[108:111]\n"
"s_add_u32 s87,s90,1024\n"
CV "v26,v28,v29\n"
CV "v27,v30,v31\n"
MF "v[108:111],a[204:207],a[244:247],v[108:111]\n"
MF "v[100:103],a[200:203],a[244:247],v[100:103]\n"
"buffer_store_dwordx4 v[24:27],v9,s[40:43],s87 offen sc1\n"
MF "v[100:103],a[160:163],a[240:243],v[100:103]\n"
MF "v[100:103],a[120:123],v[188:191],v[100:103]\n"
"s_waitcnt lgkmcnt(2)\n"
MF "v[136:139],a[120:123],v[176:179],v[136:139]\n"
"s_waitcnt lgkmcnt(0)\n"
TR "v[188:189],v3\n"
"v_add_f32 v32,v32,v192\n"
MF "v[136:139],a[160:163],v[180:183],v[136:139]\n"
TR "v[190:191],v3 offset:2048\n"
"v_add_f32 v33,v33,v193\n"
MF "v[136:139],a[200:203],v[184:187],v[136:139]\n"
"ds_read2st64_b32 v[200:201],v13 offset0:216 offset1:217\n"
"v_add_f32 v34,v34,v194\n"
MF "v[144:147],a[204:207],v[184:187],v[144:147]\n"
TR "a[240:241],v3 offset:4096\n"
"v_add_f32 v35,v35,v195\n"
MF "v[144:147],a[164:167],v[180:183],v[144:147]\n"
TR "a[242:243],v3 offset:6144\n"
"v_add_f32 v36,v36,v196\n"
MF "v[144:147],a[124:127],v[176:179],v[144:147]\n"
"ds_read2st64_b32 v[202:203],v13 offset0:218 offset1:219\n"
"v_add_f32 v37,v37,v197\n"
MF "v[152:155],a[128:131],v[176:179],v[152:155]\n"
TR "a[244:245],v3 offset:8192\n"
"v_add_f32 v38,v38,v198\n"
MF "v[152:155],a[168:171],v[180:183],v[152:155]\n"
TR "a[246:247],v3 offset:10240\n"
"v_add_f32 v39,v39,v199\n"
MF "v[152:155],a[208:211],v[184:187],v[152:155]\n"
"ds_read2st64_b32 v[204:205],v13 offset0:220 offset1:221\n"
CV "v32,v32,v33\n"
MF "v[160:163],a[212:215],v[184:187],v[160:163]\n"
"ds_read2st64_b32 v[206:207],v13 offset0:222 offset1:223\n"
CV "v33,v34,v35\n"
MF "v[160:163],a[172:175],v[180:183],v[160:163]\n"
"s_add_u32 s87,s90,2048\n"
CV "v34,v36,v37\n"
CV "v35,v38,v39\n"
MF "v[160:163],a[132:135],v[176:179],v[160:163]\n"
MF "v[168:171],a[136:139],v[176:179],v[168:171]\n"
"buffer_store_dwordx4 v[32:35],v9,s[40:43],s87 offen sc1\n"
MF "v[168:171],a[176:179],v[180:183],v[168:171]\n"
MF "v[168:171],a[216:219],v[184:187],v[168:171]\n"
"s_waitcnt lgkmcnt(0)\n"
"s_barrier\n"
MF "v[172:175],a[216:219],a[244:247],v[172:175]\n"
TR "v[176:177],v0 offset:49152\n"
"v_add_f32 v40,v40,v200\n"
MF "v[172:175],a[176:179],a[240:243],v[172:175]\n"
TR "v[178:179],v0 offset:51200\n"
"v_add_f32 v41,v41,v201\n"
MF "v[172:175],a[136:139],v[188:191],v[172:175]\n"
"ds_read2st64_b32 v[192:193],v13 offset0:224 offset1:225\n"
"v_add_f32 v42,v42,v202\n"
MF "v[164:167],a[132:135],v[188:191],v[164:167]\n"
TR "v[180:181],v0 offset:53248\n"
"v_add_f32 v43,v43,v203\n"
MF "v[164:167],a[172:175],a[240:243],v[164:167]\n"
TR "v[182:183],v0 offset:55296\n"
"v_add_f32 v44,v44,v204\n"
MF "v[164:167],a[212:215],a[244:247],v[164:167]\n"
"ds_read2st64_b32 v[194:195],v13 offset0:226 offset1:227\n"
"v_add_f32 v45,v45,v205\n"
MF "v[156:159],a[208:211],a[244:247],v[156:159]\n"
TR "v[184:185],v0 offset:57344\n"
"v_add_f32 v46,v46,v206\n"
MF "v[156:159],a[168:171],a[240:243],v[156:159]\n"
TR "v[186:187],v0 offset:59392\n"
"v_add_f32 v47,v47,v207\n"
MF "v[156:159],a[128:131],v[188:191],v[156:159]\n"
"ds_read2st64_b32 v[196:197],v13 offset0:228 offset1:229\n"
CV "v40,v40,v41\n"
MF "v[148:151],a[124:127],v[188:191],v[148:151]\n"
"ds_read2st64_b32 v[198:199],v13 offset0:230 offset1:231\n"
CV "v41,v42,v43\n"
MF "v[148:151],a[164:167],a[240:243],v[148:151]\n"
"s_add_u32 s87,s90,3072\n"
CV "v42,v44,v45\n"
CV "v43,v46,v47\n"
MF "v[148:151],a[204:207],a[244:247],v[148:151]\n"
MF "v[140:143],a[200:203],a[244:247],v[140:143]\n"
"buffer_store_dwordx4 v[40:43],v9,s[40:43],s87 offen sc1\n"
MF "v[140:143],a[160:163],a[240:243],v[140:143]\n"
MF "v[140:143],a[120:123],v[188:191],v[140:143]\n"
"s_waitcnt lgkmcnt(2)\n"
MF "v[96:99],a[20:23],v[176:179],v[96:99]\n"
"s_waitcnt lgkmcnt(0)\n"
TR "v[188:189],v1 offset:49152\n"
"v_add_f32 v48,v48,v192\n"
MF "v[96:99],a[60:63],v[180:183],v[96:99]\n"
TR "v[190:191],v1 offset:51200\n"
"v_add_f32 v49,v49,v193\n"
MF "v[96:99],a[100:103],v[184:187],v[96:99]\n"
"ds_read2st64_b32 v[200:201],v13 offset0:0 offset1:1\n"
"v_add_f32 v50,v50,v194\n"
MF "v[104:107],a[104:107],v[184:187],v[104:107]\n"
TR "a[240:241],v1 offset:53248\n"
"v_add_f32 v51,v51,v195\n"
MF "v[104:107],a[64:67],v[180:183],v[104:107]\n"
TR "a[242:243],v1 offset:55296\n"
"v_add_f32 v52,v52,v196\n"
MF "v[104:107],a[24:27],v[176:179],v[104:107]\n"
"ds_read2st64_b32 v[202:203],v13 offset0:2 offset1:3\n"
"v_add_f32 v53,v53,v197\n"
MF "v[112:115],a[28:31],v[176:179],v[112:115]\n"
TR "a[244:245],v1 offset:57344\n"
"v_add_f32 v54,v54,v198\n"
MF "v[112:115],a[68:71],v[180:183],v[112:115]\n"
TR "a[246:247],v1 offset:59392\n"
"v_add_f32 v55,v55,v199\n"
MF "v[112:115],a[108:111],v[184:187],v[112:115]\n"
"ds_read2st64_b32 v[204:205],v13 offset0:4 offset1:5\n"
CV "v48,v48,v49\n"
MF "v[120:123],a[112:115],v[184:187],v[120:123]\n"
"ds_read2st64_b32 v[206:207],v13 offset0:6 offset1:7\n"
CV "v49,v50,v51\n"
MF "v[120:123],a[72:75],v[180:183],v[120:123]\n"
"s_add_u32 s87,s90,4096\n"
CV "v50,v52,v53\n"
CV "v51,v54,v55\n"
MF "v[120:123],a[32:35],v[176:179],v[120:123]\n"
MF "v[128:131],a[36:39],v[176:179],v[128:131]\n"
"buffer_store_dwordx4 v[48:51],v9,s[40:43],s87 offen sc1\n"
MF "v[128:131],a[76:79],v[180:183],v[128:131]\n"
MF "v[128:131],a[116:119],v[184:187],v[128:131]\n"
"s_waitcnt lgkmcnt(2)\n"
MF "v[132:135],a[116:119],a[244:247],v[132:135]\n"
"s_waitcnt lgkmcnt(0)\n"
TR "v[176:177],v2 offset:49152\n"
"v_add_f32 v56,v56,v200\n"
MF "v[132:135],a[76:79],a[240:243],v[132:135]\n"
TR "v[178:179],v2 offset:51200\n"
"v_add_f32 v57,v57,v201\n"
MF "v[132:135],a[36:39],v[188:191],v[132:135]\n"
"ds_read2st64_b32 v[192:193],v13 offset0:8 offset1:9\n"
"v_add_f32 v58,v58,v202\n"
MF "v[124:127],a[32:35],v[188:191],v[124:127]\n"
TR "v[180:181],v2 offset:53248\n"
"v_add_f32 v59,v59,v203\n"
MF "v[124:127],a[72:75],a[240:243],v[124:127]\n"
TR "v[182:183],v2 offset:55296\n"
"v_add_f32 v60,v60,v204\n"
MF "v[124:127],a[112:115],a[244:247],v[124:127]\n"
"ds_read2st64_b32 v[194:195],v13 offset0:10 offset1:11\n"
"v_add_f32 v61,v61,v205\n"
MF "v[116:119],a[108:111],a[244:247],v[116:119]\n"
TR "v[184:185],v2 offset:57344\n"
"v_add_f32 v62,v62,v206\n"
MF "v[116:119],a[68:71],a[240:243],v[116:119]\n"
TR "v[186:187],v2 offset:59392\n"
"v_add_f32 v63,v63,v207\n"
MF "v[116:119],a[28:31],v[188:191],v[116:119]\n"
"ds_read2st64_b32 v[196:197],v13 offset0:12 offset1:13\n"
CV "v56,v56,v57\n"
MF "v[108:111],a[24:27],v[188:191],v[108:111]\n"
"ds_read2st64_b32 v[198:199],v13 offset0:14 offset1:15\n"
CV "v57,v58,v59\n"
MF "v[108:111],a[64:67],a[240:243],v[108:111]\n"
"s_add_u32 s87,s90,10240\n"
CV "v58,v60,v61\n"
CV "v59,v62,v63\n"
MF "v[108:111],a[104:107],a[244:247],v[108:111]\n"
MF "v[100:103],a[100:103],a[244:247],v[100:103]\n"
"buffer_store_dwordx4 v[56:59],v9,s[40:43],s87 offen sc1\n"
MF "v[100:103],a[60:63],a[240:243],v[100:103]\n"
MF "v[100:103],a[20:23],v[188:191],v[100:103]\n"
"s_waitcnt lgkmcnt(2)\n"
MF "v[136:139],a[20:23],v[176:179],v[136:139]\n"
"s_waitcnt lgkmcnt(0)\n"
TR "v[188:189],v3 offset:49152\n"
"v_add_f32 v64,v64,v192\n"
MF "v[136:139],a[60:63],v[180:183],v[136:139]\n"
TR "v[190:191],v3 offset:51200\n"
"v_add_f32 v65,v65,v193\n"
MF "v[136:139],a[100:103],v[184:187],v[136:139]\n"
"ds_read2st64_b32 v[200:201],v13 offset0:16 offset1:17\n"
"v_add_f32 v66,v66,v194\n"
MF "v[144:147],a[104:107],v[184:187],v[144:147]\n"
TR "a[240:241],v3 offset:53248\n"
"v_add_f32 v67,v67,v195\n"
MF "v[144:147],a[64:67],v[180:183],v[144:147]\n"
TR "a[242:243],v3 offset:55296\n"
"v_add_f32 v68,v68,v196\n"
MF "v[144:147],a[24:27],v[176:179],v[144:147]\n"
"ds_read2st64_b32 v[202:203],v13 offset0:18 offset1:19\n"
"v_add_f32 v69,v69,v197\n"
MF "v[152:155],a[28:31],v[176:179],v[152:155]\n"
TR "a[244:245],v3 offset:57344\n"
"v_add_f32 v70,v70,v198\n"
MF "v[152:155],a[68:71],v[180:183],v[152:155]\n"
TR "a[246:247],v3 offset:59392\n"
"v_add_f32 v71,v71,v199\n"
MF "v[152:155],a[108:111],v[184:187],v[152:155]\n"
"ds_read2st64_b32 v[204:205],v13 offset0:20 offset1:21\n"
CV "v64,v64,v65\n"
MF "v[160:163],a[112:115],v[184:187],v[160:163]\n"
"ds_read2st64_b32 v[206:207],v13 offset0:22 offset1:23\n"
CV "v65,v66,v67\n"
MF "v[160:163],a[72:75],v[180:183],v[160:163]\n"
"s_add_u32 s87,s90,11264\n"
CV "v66,v68,v69\n"
CV "v67,v70,v71\n"
MF "v[160:163],a[32:35],v[176:179],v[160:163]\n"
MF "v[168:171],a[36:39],v[176:179],v[168:171]\n"
"buffer_store_dwordx4 v[64:67],v9,s[40:43],s87 offen sc1\n"
MF "v[168:171],a[76:79],v[180:183],v[168:171]\n"
MF "v[168:171],a[116:119],v[184:187],v[168:171]\n"
"s_waitcnt lgkmcnt(0)\n"
"s_barrier\n"
MF "v[172:175],a[116:119],a[244:247],v[172:175]\n"
TR "v[176:177],v4\n"
"v_add_f32 v72,v72,v200\n"
MF "v[172:175],a[76:79],a[240:243],v[172:175]\n"
TR "v[178:179],v4 offset:2048\n"
"v_add_f32 v73,v73,v201\n"
MF "v[172:175],a[36:39],v[188:191],v[172:175]\n"
"ds_read2st64_b32 v[192:193],v13 offset0:24 offset1:25\n"
"v_add_f32 v74,v74,v202\n"
MF "v[164:167],a[32:35],v[188:191],v[164:167]\n"
TR "v[180:181],v4 offset:4096\n"
"v_add_f32 v75,v75,v203\n"
MF "v[164:167],a[72:75],a[240:243],v[164:167]\n"
TR "v[182:183],v4 offset:6144\n"
"v_add_f32 v76,v76,v204\n"
MF "v[164:167],a[112:115],a[244:247],v[164:167]\n"
"ds_read2st64_b32 v[194:195],v13 offset0:26 offset1:27\n"
"v_add_f32 v77,v77,v205\n"
MF "v[156:159],a[108:111],a[244:247],v[156:159]\n"
TR "v[184:185],v4 offset:8192\n"
"v_add_f32 v78,v78,v206\n"
MF "v[156:159],a[68:71],a[240:243],v[156:159]\n"
TR "v[186:187],v4 offset:10240\n"
"v_add_f32 v79,v79,v207\n"
MF "v[156:159],a[28:31],v[188:191],v[156:159]\n"
"ds_read2st64_b32 v[196:197],v13 offset0:28 offset1:29\n"
CV "v72,v72,v73\n"
MF "v[148:151],a[24:27],v[188:191],v[148:151]\n"
"ds_read2st64_b32 v[198:199],v13 offset0:30 offset1:31\n"
CV "v73,v74,v75\n"
MF "v[148:151],a[64:67],a[240:243],v[148:151]\n"
"s_add_u32 s87,s90,12288\n"
CV "v74,v76,v77\n"
CV "v75,v78,v79\n"
MF "v[148:151],a[104:107],a[244:247],v[148:151]\n"
MF "v[140:143],a[100:103],a[244:247],v[140:143]\n"
"buffer_store_dwordx4 v[72:75],v9,s[40:43],s87 offen sc1\n"
MF "v[140:143],a[60:63],a[240:243],v[140:143]\n"
MF "v[140:143],a[20:23],v[188:191],v[140:143]\n"
"s_waitcnt lgkmcnt(2)\n"
MF "v[96:99],a[140:143],v[176:179],v[96:99]\n"
"s_waitcnt lgkmcnt(0)\n"
TR "v[188:189],v5\n"
"v_add_f32 v80,v80,v192\n"
MF "v[96:99],a[180:183],v[180:183],v[96:99]\n"
TR "v[190:191],v5 offset:2048\n"
"v_add_f32 v81,v81,v193\n"
MF "v[96:99],a[220:223],v[184:187],v[96:99]\n"
"ds_read2st64_b32 v[200:201],v13 offset0:32 offset1:33\n"
"v_add_f32 v82,v82,v194\n"
MF "v[104:107],a[224:227],v[184:187],v[104:107]\n"
TR "a[240:241],v5 offset:4096\n"
"v_add_f32 v83,v83,v195\n"
MF "v[104:107],a[184:187],v[180:183],v[104:107]\n"
TR "a[242:243],v5 offset:6144\n"
"v_add_f32 v84,v84,v196\n"
MF "v[104:107],a[144:147],v[176:179],v[104:107]\n"
"ds_read2st64_b32 v[202:203],v13 offset0:34 offset1:35\n"
"v_add_f32 v85,v85,v197\n"
MF "v[112:115],a[148:151],v[176:179],v[112:115]\n"
TR "a[244:245],v5 offset:8192\n"
"v_add_f32 v86,v86,v198\n"
MF "v[112:115],a[188:191],v[180:183],v[112:115]\n"
TR "a[246:247],v5 offset:10240\n"
"v_add_f32 v87,v87,v199\n"
MF "v[112:115],a[228:231],v[184:187],v[112:115]\n"
"ds_read2st64_b32 v[204:205],v13 offset0:36 offset1:37\n"
CV "v80,v80,v81\n"
MF "v[120:123],a[232:235],v[184:187],v[120:123]\n"
"ds_read2st64_b32 v[206:207],v13 offset0:38 offset1:39\n"
CV "v81,v82,v83\n"
MF "v[120:123],a[192:195],v[180:183],v[120:123]\n"
"s_add_u32 s87,s90,13312\n"
CV "v82,v84,v85\n"
CV "v83,v86,v87\n"
MF "v[120:123],a[152:155],v[176:179],v[120:123]\n"
MF "v[128:131],a[156:159],v[176:179],v[128:131]\n"
"buffer_store_dwordx4 v[80:83],v9,s[40:43],s87 offen sc1\n"
MF "v[128:131],a[196:199],v[180:183],v[128:131]\n"
MF "v[128:131],a[236:239],v[184:187],v[128:131]\n"
"s_waitcnt lgkmcnt(2)\n"
MF "v[132:135],a[236:239],a[244:247],v[132:135]\n"
"s_waitcnt lgkmcnt(0)\n"
TR "v[176:177],v6\n"
"v_add_f32 v88,v88,v200\n"
MF "v[132:135],a[196:199],a[240:243],v[132:135]\n"
TR "v[178:179],v6 offset:2048\n"
"v_add_f32 v89,v89,v201\n"
MF "v[132:135],a[156:159],v[188:191],v[132:135]\n"
"v_add_f32 v90,v90,v202\n"
"v_add_f32 v91,v91,v203\n"
MF "v[124:127],a[152:155],v[188:191],v[124:127]\n"
TR "v[180:181],v6 offset:4096\n"
"v_add_f32 v92,v92,v204\n"
MF "v[124:127],a[192:195],a[240:243],v[124:127]\n"
TR "v[182:183],v6 offset:6144\n"
"v_add_f32 v93,v93,v205\n"
MF "v[124:127],a[232:235],a[244:247],v[124:127]\n"
"v_add_f32 v94,v94,v206\n"
"v_add_f32 v95,v95,v207\n"
MF "v[116:119],a[228:231],a[244:247],v[116:119]\n"
TR "v[184:185],v6 offset:8192\n"
CV "v88,v88,v89\n"
MF "v[116:119],a[188:191],a[240:243],v[116:119]\n"
TR "v[186:187],v6 offset:10240\n"
CV "v89,v90,v91\n"
MF "v[116:119],a[148:151],v[188:191],v[116:119]\n"
"s_add_u32 s87,s90,14336\n"
CV "v90,v92,v93\n"
CV "v91,v94,v95\n"
MF "v[108:111],a[144:147],v[188:191],v[108:111]\n"
MF "v[108:111],a[184:187],a[240:243],v[108:111]\n"
MF "v[108:111],a[224:227],a[244:247],v[108:111]\n"
MF "v[100:103],a[220:223],a[244:247],v[100:103]\n"
"buffer_store_dwordx4 v[88:91],v9,s[40:43],s87 offen sc1\n"
MF "v[100:103],a[180:183],a[240:243],v[100:103]\n"
"s_mov_b32 s90,s91\n"
"s_add_u32 s91,s91,20480\n"
MF "v[100:103],a[140:143],v[188:191],v[100:103]\n"
"s_waitcnt lgkmcnt(0)\n"
MF "v[136:139],a[140:143],v[176:179],v[136:139]\n"
TR "v[188:189],v7\n"
MF "v[136:139],a[180:183],v[180:183],v[136:139]\n"
TR "v[190:191],v7 offset:2048\n"
MF "v[136:139],a[220:223],v[184:187],v[136:139]\n"
MF "v[144:147],a[224:227],v[184:187],v[144:147]\n"
TR "a[240:241],v7 offset:4096\n"
MF "v[144:147],a[184:187],v[180:183],v[144:147]\n"
TR "a[242:243],v7 offset:6144\n"
MF "v[144:147],a[144:147],v[176:179],v[144:147]\n"
MF "v[152:155],a[148:151],v[176:179],v[152:155]\n"
TR "a[244:245],v7 offset:8192\n"
MF "v[152:155],a[188:191],v[180:183],v[152:155]\n"
TR "a[246:247],v7 offset:10240\n"
MF "v[152:155],a[228:231],v[184:187],v[152:155]\n"
MF "v[160:163],a[232:235],v[184:187],v[160:163]\n"
MF "v[160:163],a[192:195],v[180:183],v[160:163]\n"
MF "v[160:163],a[152:155],v[176:179],v[160:163]\n"
MF "v[168:171],a[156:159],v[176:179],v[168:171]\n"
MF "v[168:171],a[196:199],v[180:183],v[168:171]\n"
MF "v[168:171],a[236:239],v[184:187],v[168:171]\n"
"s_waitcnt lgkmcnt(0)\n"
"s_barrier\n"
MF "v[172:175],a[236:239],a[244:247],v[172:175]\n"
TR "v[176:177],v0 offset:24576\n"
MF "v[172:175],a[196:199],a[240:243],v[172:175]\n"
"s_add_u32 s85,s85,1\n"
TR "v[178:179],v0 offset:26624\n"
MF "v[172:175],a[156:159],v[188:191],v[172:175]\n"
MF "v[164:167],a[152:155],v[188:191],v[164:167]\n"
TR "v[180:181],v0 offset:28672\n"
MF "v[164:167],a[192:195],a[240:243],v[164:167]\n"
TR "v[182:183],v0 offset:30720\n"
MF "v[164:167],a[232:235],a[244:247],v[164:167]\n"
MF "v[156:159],a[228:231],a[244:247],v[156:159]\n"
TR "v[184:185],v0 offset:32768\n"
MF "v[156:159],a[188:191],a[240:243],v[156:159]\n"
TR "v[186:187],v0 offset:34816\n"
MF "v[156:159],a[148:151],v[188:191],v[156:159]\n"
MF "v[148:151],a[144:147],v[188:191],v[148:151]\n"
MF "v[148:151],a[184:187],a[240:243],v[148:151]\n"
MF "v[148:151],a[224:227],a[244:247],v[148:151]\n"
MF "v[140:143],a[220:223],a[244:247],v[140:143]\n"
MF "v[140:143],a[180:183],a[240:243],v[140:143]\n"
MF "v[140:143],a[140:143],v[188:191],v[140:143]\n"
"s_waitcnt lgkmcnt(0)\n"
MF "v[16:19],a[0:3],v[176:179],v[252:255]\n"
TR "v[188:189],v1 offset:24576\n"
MF "v[16:19],a[40:43],v[180:183],v[16:19]\n"
TR "v[190:191],v1 offset:26624\n"
MF "v[16:19],a[80:83],v[184:187],v[16:19]\n"
MF "v[24:27],a[84:87],v[184:187],v[252:255]\n"
TR "a[240:241],v1 offset:28672\n"
MF "v[24:27],a[44:47],v[180:183],v[24:27]\n"
TR "a[242:243],v1 offset:30720\n"
MF "v[24:27],a[4:7],v[176:179],v[24:27]\n"
MF "v[32:35],a[8:11],v[176:179],v[252:255]\n"
TR "a[244:245],v1 offset:32768\n"
MF "v[32:35],a[48:51],v[180:183],v[32:35]\n"
TR "a[246:247],v1 offset:34816\n"
MF "v[32:35],a[88:91],v[184:187],v[32:35]\n"
MF "v[40:43],a[92:95],v[184:187],v[252:255]\n"
MF "v[40:43],a[52:55],v[180:183],v[40:43]\n"
MF "v[40:43],a[12:15],v[176:179],v[40:43]\n"
MF "v[48:51],a[16:19],v[176:179],v[252:255]\n"
MF "v[48:51],a[56:59],v[180:183],v[48:51]\n"
MF "v[48:51],a[96:99],v[184:187],v[48:51]\n"
"s_waitcnt lgkmcnt(0)\n"
MF "v[52:55],a[96:99],a[244:247],v[252:255]\n"
TR "v[176:177],v2 offset:24576\n"
MF "v[52:55],a[56:59],a[240:243],v[52:55]\n"
TR "v[178:179],v2 offset:26624\n"
MF "v[52:55],a[16:19],v[188:191],v[52:55]\n"
MF "v[44:47],a[12:15],v[188:191],v[252:255]\n"
TR "v[180:181],v2 offset:28672\n"
MF "v[44:47],a[52:55],a[240:243],v[44:47]\n"
TR "v[182:183],v2 offset:30720\n"
MF "v[44:47],a[92:95],a[244:247],v[44:47]\n"
MF "v[36:39],a[88:91],a[244:247],v[252:255]\n"
TR "v[184:185],v2 offset:32768\n"
MF "v[36:39],a[48:51],a[240:243],v[36:39]\n"
TR "v[186:187],v2 offset:34816\n"
MF "v[36:39],a[8:11],v[188:191],v[36:39]\n"
MF "v[28:31],a[4:7],v[188:191],v[252:255]\n"
MF "v[28:31],a[44:47],a[240:243],v[28:31]\n"
MF "v[28:31],a[84:87],a[244:247],v[28:31]\n"
MF "v[20:23],a[80:83],a[244:247],v[252:255]\n"
MF "v[20:23],a[40:43],a[240:243],v[20:23]\n"
MF "v[20:23],a[0:3],v[188:191],v[20:23]\n"
"s_waitcnt lgkmcnt(0)\n"
MF "v[56:59],a[0:3],v[176:179],v[252:255]\n"
TR "v[188:189],v3 offset:24576\n"
MF "v[56:59],a[40:43],v[180:183],v[56:59]\n"
TR "v[190:191],v3 offset:26624\n"
MF "v[56:59],a[80:83],v[184:187],v[56:59]\n"
MF "v[64:67],a[84:87],v[184:187],v[252:255]\n"
TR "a[240:241],v3 offset:28672\n"
MF "v[64:67],a[44:47],v[180:183],v[64:67]\n"
TR "a[242:243],v3 offset:30720\n"
MF "v[64:67],a[4:7],v[176:179],v[64:67]\n"
MF "v[72:75],a[8:11],v[176:179],v[252:255]\n"
TR "a[244:245],v3 offset:32768\n"
MF "v[72:75],a[48:51],v[180:183],v[72:75]\n"
TR "a[246:247],v3 offset:34816\n"
MF "v[72:75],a[88:91],v[184:187],v[72:75]\n"
MF "v[80:83],a[92:95],v[184:187],v[252:255]\n"
MF "v[80:83],a[52:55],v[180:183],v[80:83]\n"
MF "v[80:83],a[12:15],v[176:179],v[80:83]\n"
MF "v[88:91],a[16:19],v[176:179],v[252:255]\n"
MF "v[88:91],a[56:59],v[180:183],v[88:91]\n"
MF "v[88:91],a[96:99],v[184:187],v[88:91]\n"
"s_waitcnt lgkmcnt(0)\n"
"s_barrier\n"
MF "v[92:95],a[96:99],a[244:247],v[252:255]\n"
TR "v[176:177],v0\n"
MF "v[92:95],a[56:59],a[240:243],v[92:95]\n"
TR "v[178:179],v0 offset:2048\n"
MF "v[92:95],a[16:19],v[188:191],v[92:95]\n"
"ds_read2st64_b32 v[192:193],v13 offset0:192 offset1:193\n"
MF "v[84:87],a[12:15],v[188:191],v[252:255]\n"
TR "v[180:181],v0 offset:4096\n"
MF "v[84:87],a[52:55],a[240:243],v[84:87]\n"
TR "v[182:183],v0 offset:6144\n"
MF "v[84:87],a[92:95],a[244:247],v[84:87]\n"
"ds_read2st64_b32 v[194:195],v13 offset0:194 offset1:195\n"
MF "v[76:79],a[88:91],a[244:247],v[252:255]\n"
TR "v[184:185],v0 offset:8192\n"
MF "v[76:79],a[48:51],a[240:243],v[76:79]\n"
TR "v[186:187],v0 offset:10240\n"
MF "v[76:79],a[8:11],v[188:191],v[76:79]\n"
"ds_read2st64_b32 v[196:197],v13 offset0:196 offset1:197\n"
MF "v[68:71],a[4:7],v[188:191],v[252:255]\n"
"ds_read2st64_b32 v[198:199],v13 offset0:198 offset1:199\n"
MF "v[68:71],a[44:47],a[240:243],v[68:71]\n"
MF "v[68:71],a[84:87],a[244:247],v[68:71]\n"
MF "v[60:63],a[80:83],a[244:247],v[252:255]\n"
MF "v[60:63],a[40:43],a[240:243],v[60:63]\n"
MF "v[60:63],a[0:3],v[188:191],v[60:63]\n"
"s_waitcnt lgkmcnt(2)\n"
MF "v[16:19],a[120:123],v[176:179],v[16:19]\n"
"s_waitcnt lgkmcnt(0)\n"
TR "v[188:189],v1\n"
"v_add_f32 v96,v96,v192\n"
MF "v[16:19],a[160:163],v[180:183],v[16:19]\n"
TR "v[190:191],v1 offset:2048\n"
"v_add_f32 v97,v97,v193\n"
MF "v[16:19],a[200:203],v[184:187],v[16:19]\n"
"ds_read2st64_b32 v[200:201],v13 offset0:200 offset1:201\n"
"v_add_f32 v98,v98,v194\n"
"s_waitcnt vmcnt(0)\n"
"v_mov_b32 v14,s76\n"
"s_add_u32 s76,s76,1\n"
MF "v[24:27],a[204:207],v[184:187],v[24:27]\n"
TR "a[240:241],v1 offset:4096\n"
"v_add_f32 v99,v99,v195\n"
"buffer_store_dword v14,v15,s[40:43],s94 offen sc1\n"
MF "v[24:27],a[164:167],v[180:183],v[24:27]\n"
TR "a[242:243],v1 offset:6144\n"
"v_add_f32 v100,v100,v196\n"
MF "v[24:27],a[124:127],v[176:179],v[24:27]\n"
"ds_read2st64_b32 v[202:203],v13 offset0:202 offset1:203\n"
"v_add_f32 v101,v101,v197\n"
MF "v[32:35],a[128:131],v[176:179],v[32:35]\n"
TR "a[244:245],v1 offset:8192\n"
"v_add_f32 v102,v102,v198\n"
MF "v[32:35],a[168:171],v[180:183],v[32:35]\n"
TR "a[246:247],v1 offset:10240\n"
"v_add_f32 v103,v103,v199\n"
MF "v[32:35],a[208:211],v[184:187],v[32:35]\n"
"ds_read2st64_b32 v[204:205],v13 offset0:204 offset1:205\n"
CV "v96,v96,v97\n"
MF "v[40:43],a[212:215],v[184:187],v[40:43]\n"
".Lpg31h:\n"
"s_cbranch_execz .Lpg35h\n"
"ds_read2st64_b32 v[206:207],v13 offset0:206 offset1:207\n"
CV "v97,v98,v99\n"
MF "v[40:43],a[172:175],v[180:183],v[40:43]\n"
"s_add_u32 s87,s90,0\n"
CV "v98,v100,v101\n"
CV "v99,v102,v103\n"
MF "v[40:43],a[132:135],v[176:179],v[40:43]\n"
MF "v[48:51],a[136:139],v[176:179],v[48:51]\n"
"buffer_store_dwordx4 v[96:99],v9,s[40:43],s87 offen sc1\n"
MF "v[48:51],a[176:179],v[180:183],v[48:51]\n"
MF "v[48:51],a[216:219],v[184:187],v[48:51]\n"
"s_waitcnt lgkmcnt(2)\n"
MF "v[52:55],a[216:219],a[244:247],v[52:55]\n"
"s_waitcnt lgkmcnt(0)\n"
TR "v[176:177],v2\n"
"v_add_f32 v104,v104,v200\n"
MF "v[52:55],a[176:179],a[240:243],v[52:55]\n"
TR "v[178:179],v2 offset:2048\n"
"v_add_f32 v105,v105,v201\n"
MF "v[52:55],a[136:139],v[188:191],v[52:55]\n"
"ds_read2st64_b32 v[192:193],v13 offset0:208 offset1:209\n"
"v_add_f32 v106,v106,v202\n"
MF "v[44:47],a[132:135],v[188:191],v[44:47]\n"
TR "v[180:181],v2 offset:4096\n"
"v_add_f32 v107,v107,v203\n"
MF "v[44:47],a[172:175],a[240:243],v[44:47]\n"
TR "v[182:183],v2 offset:6144\n"
"v_add_f32 v108,v108,v204\n"
MF "v[44:47],a[212:215],a[244:247],v[44:47]\n"
"ds_read2st64_b32 v[194:195],v13 offset0:210 offset1:211\n"
"v_add_f32 v109,v109,v205\n"
MF "v[36:39],a[208:211],a[244:247],v[36:39]\n"
TR "v[184:185],v2 offset:8192\n"
"v_add_f32 v110,v110,v206\n"
MF "v[36:39],a[168:171],a[240:243],v[36:39]\n"
TR "v[186:187],v2 offset:10240\n"
"v_add_f32 v111,v111,v207\n"
MF "v[36:39],a[128:131],v[188:191],v[36:39]\n"
"ds_read2st64_b32 v[196:197],v13 offset0:212 offset1:213\n"
CV "v104,v104,v105\n"
MF "v[28:31],a[124:127],v[188:191],v[28:31]\n"
"ds_read2st64_b32 v[198:199],v13 offset0:214 offset1:215\n"
CV "v105,v106,v107\n"
MF "v[28:31],a[164:167],a[240:243],v[28:31]\n"
"s_add_u32 s87,s90,1024\n"
CV "v106,v108,v109\n"
CV "v107,v110,v111\n"
MF "v[28:31],a[204:207],a[244:247],v[28:31]\n"
MF "v[20:23],a[200:203],a[244:247],v[20:23]\n"
"buffer_store_dwordx4 v[104:107],v9,s[40:43],s87 offen sc1\n"
MF "v[20:23],a[160:163],a[240:243],v[20:23]\n"
MF "v[20:23],a[120:123],v[188:191],v[20:23]\n"
"s_waitcnt lgkmcnt(2)\n"
MF "v[56:59],a[120:123],v[176:179],v[56:59]\n"
"s_waitcnt lgkmcnt(0)\n"
TR "v[188:189],v3\n"
"v_add_f32 v112,v112,v192\n"
MF "v[56:59],a[160:163],v[180:183],v[56:59]\n"
TR "v[190:191],v3 offset:2048\n"
"v_add_f32 v113,v113,v193\n"
MF "v[56:59],a[200:203],v[184:187],v[56:59]\n"
"ds_read2st64_b32 v[200:201],v13 offset0:216 offset1:217\n"
"v_add_f32 v114,v114,v194\n"
MF "v[64:67],a[204:207],v[184:187],v[64:67]\n"
TR "a[240:241],v3 offset:4096\n"
"v_add_f32 v115,v115,v195\n"
MF "v[64:67],a[164:167],v[180:183],v[64:67]\n"
TR "a[242:243],v3 offset:6144\n"
"v_add_f32 v116,v116,v196\n"
MF "v[64:67],a[124:127],v[176:179],v[64:67]\n"
"ds_read2st64_b32 v[202:203],v13 offset0:218 offset1:219\n"
"v_add_f32 v117,v117,v197\n"
MF "v[72:75],a[128:131],v[176:179],v[72:75]\n"
TR "a[244:245],v3 offset:8192\n"
"v_add_f32 v118,v118,v198\n"
MF "v[72:75],a[168:171],v[180:183],v[72:75]\n"
TR "a[246:247],v3 offset:10240\n"
"v_add_f32 v119,v119,v199\n"
MF "v[72:75],a[208:211],v[184:187],v[72:75]\n"
"ds_read2st64_b32 v[204:205],v13 offset0:220 offset1:221\n"
CV "v112,v112,v113\n"
MF "v[80:83],a[212:215],v[184:187],v[80:83]\n"
"ds_read2st64_b32 v[206:207],v13 offset0:222 offset1:223\n"
CV "v113,v114,v115\n"
MF "v[80:83],a[172:175],v[180:183],v[80:83]\n"
"s_add_u32 s87,s90,2048\n"
CV "v114,v116,v117\n"
CV "v115,v118,v119\n"
MF "v[80:83],a[132:135],v[176:179],v[80:83]\n"
MF "v[88:91],a[136:139],v[176:179],v[88:91]\n"
"buffer_store_dwordx4 v[112:115],v9,s[40:43],s87 offen sc1\n"
MF "v[88:91],a[176:179],v[180:183],v[88:91]\n"
MF "v[88:91],a[216:219],v[184:187],v[88:91]\n"
"s_waitcnt lgkmcnt(0)\n"
"s_barrier\n"
MF "v[92:95],a[216:219],a[244:247],v[92:95]\n"
TR "v[176:177],v0 offset:49152\n"
"v_add_f32 v120,v120,v200\n"
MF "v[92:95],a[176:179],a[240:243],v[92:95]\n"
TR "v[178:179],v0 offset:51200\n"
"v_add_f32 v121,v121,v201\n"
MF "v[92:95],a[136:139],v[188:191],v[92:95]\n"
"ds_read2st64_b32 v[192:193],v13 offset0:224 offset1:225\n"
"v_add_f32 v122,v122,v202\n"
MF "v[84:87],a[132:135],v[188:191],v[84:87]\n"
TR "v[180:181],v0 offset:53248\n"
"v_add_f32 v123,v123,v203\n"
MF "v[84:87],a[172:175],a[240:243],v[84:87]\n"
TR "v[182:183],v0 offset:55296\n"
"v_add_f32 v124,v124,v204\n"
MF "v[84:87],a[212:215],a[244:247],v[84:87]\n"
"ds_read2st64_b32 v[194:195],v13 offset0:226 offset1:227\n"
"v_add_f32 v125,v125,v205\n"
MF "v[76:79],a[208:211],a[244:247],v[76:79]\n"
TR "v[184:185],v0 offset:57344\n"
"v_add_f32 v126,v126,v206\n"
MF "v[76:79],a[168:171],a[240:243],v[76:79]\n"
TR "v[186:187],v0 offset:59392\n"
"v_add_f32 v127,v127,v207\n"
MF "v[76:79],a[128:131],v[188:191],v[76:79]\n"
"ds_read2st64_b32 v[196:197],v13 offset0:228 offset1:229\n"
CV "v120,v120,v121\n"
MF "v[68:71],a[124:127],v[188:191],v[68:71]\n"
"ds_read2st64_b32 v[198:199],v13 offset0:230 offset1:231\n"
CV "v121,v122,v123\n"
MF "v[68:71],a[164:167],a[240:243],v[68:71]\n"
"s_add_u32 s87,s90,3072\n"
CV "v122,v124,v125\n"
CV "v123,v126,v127\n"
MF "v[68:71],a[204:207],a[244:247],v[68:71]\n"
MF "v[60:63],a[200:203],a[244:247],v[60:63]\n"
"buffer_store_dwordx4 v[120:123],v9,s[40:43],s87 offen sc1\n"
MF "v[60:63],a[160:163],a[240:243],v[60:63]\n"
MF "v[60:63],a[120:123],v[188:191],v[60:63]\n"
"s_waitcnt lgkmcnt(2)\n"
MF "v[16:19],a[20:23],v[176:179],v[16:19]\n"
"s_waitcnt lgkmcnt(0)\n"
TR "v[188:189],v1 offset:49152\n"
"v_add_f32 v128,v128,v192\n"
MF "v[16:19],a[60:63],v[180:183],v[16:19]\n"
TR "v[190:191],v1 offset:51200\n"
"v_add_f32 v129,v129,v193\n"
MF "v[16:19],a[100:103],v[184:187],v[16:19]\n"
"ds_read2st64_b32 v[200:201],v13 offset0:0 offset1:1\n"
"v_add_f32 v130,v130,v194\n"
MF "v[24:27],a[104:107],v[184:187],v[24:27]\n"
TR "a[240:241],v1 offset:53248\n"
"v_add_f32 v131,v131,v195\n"
MF "v[24:27],a[64:67],v[180:183],v[24:27]\n"
TR "a[242:243],v1 offset:55296\n"
"v_add_f32 v132,v132,v196\n"
MF "v[24:27],a[24:27],v[176:179],v[24:27]\n"
"ds_read2st64_b32 v[202:203],v13 offset0:2 offset1:3\n"
"v_add_f32 v133,v133,v197\n"
MF "v[32:35],a[28:31],v[176:179],v[32:35]\n"
TR "a[244:245],v1 offset:57344\n"
"v_add_f32 v134,v134,v198\n"
MF "v[32:35],a[68:71],v[180:183],v[32:35]\n"
TR "a[246:247],v1 offset:59392\n"
"v_add_f32 v135,v135,v199\n"
MF "v[32:35],a[108:111],v[184:187],v[32:35]\n"
"ds_read2st64_b32 v[204:205],v13 offset0:4 offset1:5\n"
CV "v128,v128,v129\n"
MF "v[40:43],a[112:115],v[184:187],v[40:43]\n"
"ds_read2st64_b32 v[206:207],v13 offset0:6 offset1:7\n"
CV "v129,v130,v131\n"
MF "v[40:43],a[72:75],v[180:183],v[40:43]\n"
"s_add_u32 s87,s90,4096\n"
CV "v130,v132,v133\n"
CV "v131,v134,v135\n"
MF "v[40:43],a[32:35],v[176:179],v[40:43]\n"
MF "v[48:51],a[36:39],v[176:179],v[48:51]\n"
"buffer_store_dwordx4 v[128:131],v9,s[40:43],s87 offen sc1\n"
MF "v[48:51],a[76:79],v[180:183],v[48:51]\n"
MF "v[48:51],a[116:119],v[184:187],v[48:51]\n"
"s_waitcnt lgkmcnt(2)\n"
MF "v[52:55],a[116:119],a[244:247],v[52:55]\n"
"s_waitcnt lgkmcnt(0)\n"
TR "v[176:177],v2 offset:49152\n"
"v_add_f32 v136,v136,v200\n"
MF "v[52:55],a[76:79],a[240:243],v[52:55]\n"
TR "v[178:179],v2 offset:51200\n"
"v_add_f32 v137,v137,v201\n"
MF "v[52:55],a[36:39],v[188:191],v[52:55]\n"
"ds_read2st64_b32 v[192:193],v13 offset0:8 offset1:9\n"
"v_add_f32 v138,v138,v202\n"
MF "v[44:47],a[32:35],v[188:191],v[44:47]\n"
TR "v[180:181],v2 offset:53248\n"
"v_add_f32 v139,v139,v203\n"
MF "v[44:47],a[72:75],a[240:243],v[44:47]\n"
TR "v[182:183],v2 offset:55296\n"
"v_add_f32 v140,v140,v204\n"
MF "v[44:47],a[112:115],a[244:247],v[44:47]\n"
"ds_read2st64_b32 v[194:195],v13 offset0:10 offset1:11\n"
"v_add_f32 v141,v141,v205\n"
MF "v[36:39],a[108:111],a[244:247],v[36:39]\n"
TR "v[184:185],v2 offset:57344\n"
"v_add_f32 v142,v142,v206\n"
MF "v[36:39],a[68:71],a[240:243],v[36:39]\n"
TR "v[186:187],v2 offset:59392\n"
"v_add_f32 v143,v143,v207\n"
MF "v[36:39],a[28:31],v[188:191],v[36:39]\n"
"ds_read2st64_b32 v[196:197],v13 offset0:12 offset1:13\n"
CV "v136,v136,v137\n"
MF "v[28:31],a[24:27],v[188:191],v[28:31]\n"
"ds_read2st64_b32 v[198:199],v13 offset0:14 offset1:15\n"
CV "v137,v138,v139\n"
MF "v[28:31],a[64:67],a[240:243],v[28:31]\n"
"s_add_u32 s87,s90,10240\n"
CV "v138,v140,v141\n"
CV "v139,v142,v143\n"
MF "v[28:31],a[104:107],a[244:247],v[28:31]\n"
MF "v[20:23],a[100:103],a[244:247],v[20:23]\n"
"buffer_store_dwordx4 v[136:139],v9,s[40:43],s87 offen sc1\n"
MF "v[20:23],a[60:63],a[240:243],v[20:23]\n"
MF "v[20:23],a[20:23],v[188:191],v[20:23]\n"
"s_waitcnt lgkmcnt(2)\n"
MF "v[56:59],a[20:23],v[176:179],v[56:59]\n"
"s_waitcnt lgkmcnt(0)\n"
TR "v[188:189],v3 offset:49152\n"
"v_add_f32 v144,v144,v192\n"
MF "v[56:59],a[60:63],v[180:183],v[56:59]\n"
TR "v[190:191],v3 offset:51200\n"
"v_add_f32 v145,v145,v193\n"
MF "v[56:59],a[100:103],v[184:187],v[56:59]\n"
"ds_read2st64_b32 v[200:201],v13 offset0:16 offset1:17\n"
"v_add_f32 v146,v146,v194\n"
MF "v[64:67],a[104:107],v[184:187],v[64:67]\n"
TR "a[240:241],v3 offset:53248\n"
"v_add_f32 v147,v147,v195\n"
MF "v[64:67],a[64:67],v[180:183],v[64:67]\n"
TR "a[242:243],v3 offset:55296\n"
"v_add_f32 v148,v148,v196\n"
MF "v[64:67],a[24:27],v[176:179],v[64:67]\n"
"ds_read2st64_b32 v[202:203],v13 offset0:18 offset1:19\n"
"v_add_f32 v149,v149,v197\n"
MF "v[72:75],a[28:31],v[176:179],v[72:75]\n"
TR "a[244:245],v3 offset:57344\n"
"v_add_f32 v150,v150,v198\n"
MF "v[72:75],a[68:71],v[180:183],v[72:75]\n"
TR "a[246:247],v3 offset:59392\n"
"v_add_f32 v151,v151,v199\n"
MF "v[72:75],a[108:111],v[184:187],v[72:75]\n"
"ds_read2st64_b32 v[204:205],v13 offset0:20 offset1:21\n"
CV "v144,v144,v145\n"
MF "v[80:83],a[112:115],v[184:187],v[80:83]\n"
"ds_read2st64_b32 v[206:207],v13 offset0:22 offset1:23\n"
CV "v145,v146,v147\n"
MF "v[80:83],a[72:75],v[180:183],v[80:83]\n"
"s_add_u32 s87,s90,11264\n"
CV "v146,v148,v149\n"
CV "v147,v150,v151\n"
MF "v[80:83],a[32:35],v[176:179],v[80:83]\n"
MF "v[88:91],a[36:39],v[176:179],v[88:91]\n"
"buffer_store_dwordx4 v[144:147],v9,s[40:43],s87 offen sc1\n"
MF "v[88:91],a[76:79],v[180:183],v[88:91]\n"
MF "v[88:91],a[116:119],v[184:187],v[88:91]\n"
"s_waitcnt lgkmcnt(0)\n"
"s_barrier\n"
MF "v[92:95],a[116:119],a[244:247],v[92:95]\n"
TR "v[176:177],v4\n"
"v_add_f32 v152,v152,v200\n"
MF "v[92:95],a[76:79],a[240:243],v[92:95]\n"
TR "v[178:179],v4 offset:2048\n"
"v_add_f32 v153,v153,v201\n"
MF "v[92:95],a[36:39],v[188:191],v[92:95]\n"
"ds_read2st64_b32 v[192:193],v13 offset0:24 offset1:25\n"
"v_add_f32 v154,v154,v202\n"
MF "v[84:87],a[32:35],v[188:191],v[84:87]\n"
TR "v[180:181],v4 offset:4096\n"
"v_add_f32 v155,v155,v203\n"
MF "v[84:87],a[72:75],a[240:243],v[84:87]\n"
TR "v[182:183],v4 offset:6144\n"
"v_add_f32 v156,v156,v204\n"
MF "v[84:87],a[112:115],a[244:247],v[84:87]\n"
"ds_read2st64_b32 v[194:195],v13 offset0:26 offset1:27\n"
"v_add_f32 v157,v157,v205\n"
MF "v[76:79],a[108:111],a[244:247],v[76:79]\n"
TR "v[184:185],v4 offset:8192\n"
"v_add_f32 v158,v158,v206\n"
MF "v[76:79],a[68:71],a[240:243],v[76:79]\n"
TR "v[186:187],v4 offset:10240\n"
"v_add_f32 v159,v159,v207\n"
MF "v[76:79],a[28:31],v[188:191],v[76:79]\n"
"ds_read2st64_b32 v[196:197],v13 offset0:28 offset1:29\n"
CV "v152,v152,v153\n"
MF "v[68:71],a[24:27],v[188:191],v[68:71]\n"
"ds_read2st64_b32 v[198:199],v13 offset0:30 offset1:31\n"
CV "v153,v154,v155\n"
MF "v[68:71],a[64:67],a[240:243],v[68:71]\n"
"s_add_u32 s87,s90,12288\n"
CV "v154,v156,v157\n"
CV "v155,v158,v159\n"
MF "v[68:71],a[104:107],a[244:247],v[68:71]\n"
MF "v[60:63],a[100:103],a[244:247],v[60:63]\n"
"buffer_store_dwordx4 v[152:155],v9,s[40:43],s87 offen sc1\n"
MF "v[60:63],a[60:63],a[240:243],v[60:63]\n"
MF "v[60:63],a[20:23],v[188:191],v[60:63]\n"
"s_waitcnt lgkmcnt(2)\n"
MF "v[16:19],a[140:143],v[176:179],v[16:19]\n"
"s_waitcnt lgkmcnt(0)\n"
TR "v[188:189],v5\n"
"v_add_f32 v160,v160,v192\n"
MF "v[16:19],a[180:183],v[180:183],v[16:19]\n"
TR "v[190:191],v5 offset:2048\n"
"v_add_f32 v161,v161,v193\n"
MF "v[16:19],a[220:223],v[184:187],v[16:19]\n"
"ds_read2st64_b32 v[200:201],v13 offset0:32 offset1:33\n"
"v_add_f32 v162,v162,v194\n"
MF "v[24:27],a[224:227],v[184:187],v[24:27]\n"
TR "a[240:241],v5 offset:4096\n"
"v_add_f32 v163,v163,v195\n"
MF "v[24:27],a[184:187],v[180:183],v[24:27]\n"
TR "a[242:243],v5 offset:6144\n"
"v_add_f32 v164,v164,v196\n"
MF "v[24:27],a[144:147],v[176:179],v[24:27]\n"
"ds_read2st64_b32 v[202:203],v13 offset0:34 offset1:35\n"
"v_add_f32 v165,v165,v197\n"
MF "v[32:35],a[148:151],v[176:179],v[32:35]\n"
TR "a[244:245],v5 offset:8192\n"
"v_add_f32 v166,v166,v198\n"
MF "v[32:35],a[188:191],v[180:183],v[32:35]\n"
TR "a[246:247],v5 offset:10240\n"
"v_add_f32 v167,v167,v199\n"
MF "v[32:35],a[228:231],v[184:187],v[32:35]\n"
"ds_read2st64_b32 v[204:205],v13 offset0:36 offset1:37\n"
CV "v160,v160,v161\n"
MF "v[40:43],a[232:235],v[184:187],v[40:43]\n"
"ds_read2st64_b32 v[206:207],v13 offset0:38 offset1:39\n"
CV "v161,v162,v163\n"
MF "v[40:43],a[192:195],v[180:183],v[40:43]\n"
"s_add_u32 s87,s90,13312\n"
CV "v162,v164,v165\n"
CV "v163,v166,v167\n"
MF "v[40:43],a[152:155],v[176:179],v[40:43]\n"
MF "v[48:51],a[156:159],v[176:179],v[48:51]\n"
"buffer_store_dwordx4 v[160:163],v9,s[40:43],s87 offen sc1\n"
MF "v[48:51],a[196:199],v[180:183],v[48:51]\n"
MF "v[48:51],a[236:239],v[184:187],v[48:51]\n"
"s_waitcnt lgkmcnt(2)\n"
MF "v[52:55],a[236:239],a[244:247],v[52:55]\n"
"s_waitcnt lgkmcnt(0)\n"
TR "v[176:177],v6\n"
"v_add_f32 v168,v168,v200\n"
MF "v[52:55],a[196:199],a[240:243],v[52:55]\n"
TR "v[178:179],v6 offset:2048\n"
"v_add_f32 v169,v169,v201\n"
MF "v[52:55],a[156:159],v[188:191],v[52:55]\n"
"v_add_f32 v170,v170,v202\n"
"v_add_f32 v171,v171,v203\n"
MF "v[44:47],a[152:155],v[188:191],v[44:47]\n"
TR "v[180:181],v6 offset:4096\n"
"v_add_f32 v172,v172,v204\n"
MF "v[44:47],a[192:195],a[240:243],v[44:47]\n"
TR "v[182:183],v6 offset:6144\n"
"v_add_f32 v173,v173,v205\n"
MF "v[44:47],a[232:235],a[244:247],v[44:47]\n"
"v_add_f32 v174,v174,v206\n"
"v_add_f32 v175,v175,v207\n"
MF "v[36:39],a[228:231],a[244:247],v[36:39]\n"
TR "v[184:185],v6 offset:8192\n"
CV "v168,v168,v169\n"
MF "v[36:39],a[188:191],a[240:243],v[36:39]\n"
TR "v[186:187],v6 offset:10240\n"
CV "v169,v170,v171\n"
MF "v[36:39],a[148:151],v[188:191],v[36:39]\n"
"s_add_u32 s87,s90,14336\n"
CV "v170,v172,v173\n"
CV "v171,v174,v175\n"
MF "v[28:31],a[144:147],v[188:191],v[28:31]\n"
MF "v[28:31],a[184:187],a[240:243],v[28:31]\n"
MF "v[28:31],a[224:227],a[244:247],v[28:31]\n"
MF "v[20:23],a[220:223],a[244:247],v[20:23]\n"
"buffer_store_dwordx4 v[168:171],v9,s[40:43],s87 offen sc1\n"
MF "v[20:23],a[180:183],a[240:243],v[20:23]\n"
"s_mov_b32 s90,s91\n"
"s_add_u32 s91,s91,20480\n"
MF "v[20:23],a[140:143],v[188:191],v[20:23]\n"
"s_waitcnt lgkmcnt(0)\n"
MF "v[56:59],a[140:143],v[176:179],v[56:59]\n"
TR "v[188:189],v7\n"
MF "v[56:59],a[180:183],v[180:183],v[56:59]\n"
TR "v[190:191],v7 offset:2048\n"
MF "v[56:59],a[220:223],v[184:187],v[56:59]\n"
MF "v[64:67],a[224:227],v[184:187],v[64:67]\n"
TR "a[240:241],v7 offset:4096\n"
MF "v[64:67],a[184:187],v[180:183],v[64:67]\n"
TR "a[242:243],v7 offset:6144\n"
MF "v[64:67],a[144:147],v[176:179],v[64:67]\n"
MF "v[72:75],a[148:151],v[176:179],v[72:75]\n"
TR "a[244:245],v7 offset:8192\n"
MF "v[72:75],a[188:191],v[180:183],v[72:75]\n"
TR "a[246:247],v7 offset:10240\n"
MF "v[72:75],a[228:231],v[184:187],v[72:75]\n"
MF "v[80:83],a[232:235],v[184:187],v[80:83]\n"
MF "v[80:83],a[192:195],v[180:183],v[80:83]\n"
MF "v[80:83],a[152:155],v[176:179],v[80:83]\n"
MF "v[88:91],a[156:159],v[176:179],v[88:91]\n"
MF "v[88:91],a[196:199],v[180:183],v[88:91]\n"
MF "v[88:91],a[236:239],v[184:187],v[88:91]\n"
"s_waitcnt lgkmcnt(0)\n"
"s_barrier\n"
MF "v[92:95],a[236:239],a[244:247],v[92:95]\n"
TR "v[176:177],v0 offset:24576\n"
MF "v[92:95],a[196:199],a[240:243],v[92:95]\n"
"s_add_u32 s85,s85,1\n"
TR "v[178:179],v0 offset:26624\n"
MF "v[92:95],a[156:159],v[188:191],v[92:95]\n"
MF "v[84:87],a[152:155],v[188:191],v[84:87]\n"
TR "v[180:181],v0 offset:28672\n"
MF "v[84:87],a[192:195],a[240:243],v[84:87]\n"
TR "v[182:183],v0 offset:30720\n"
MF "v[84:87],a[232:235],a[244:247],v[84:87]\n"
MF "v[76:79],a[228:231],a[244:247],v[76:79]\n"
TR "v[184:185],v0 offset:32768\n"
MF "v[76:79],a[188:191],a[240:243],v[76:79]\n"
TR "v[186:187],v0 offset:34816\n"
MF "v[76:79],a[148:151],v[188:191],v[76:79]\n"
MF "v[68:71],a[144:147],v[188:191],v[68:71]\n"
MF "v[68:71],a[184:187],a[240:243],v[68:71]\n"
MF "v[68:71],a[224:227],a[244:247],v[68:71]\n"
MF "v[60:63],a[220:223],a[244:247],v[60:63]\n"
MF "v[60:63],a[180:183],a[240:243],v[60:63]\n"
MF "v[60:63],a[140:143],v[188:191],v[60:63]\n"
"s_waitcnt lgkmcnt(0)\n"
"s_cmp_lg_u32 s85,5\n"
"s_cbranch_scc1 .Lpl15\n"
".Lpl19:\n"
MF "v[96:99],a[0:3],v[176:179],v[252:255]\n"
TR "v[188:189],v1 offset:24576\n"
MF "v[96:99],a[40:43],v[180:183],v[96:99]\n"
"s_add_u32 s82,s82,4096\n"
TR "v[190:191],v1 offset:26624\n"
MF "v[96:99],a[80:83],v[184:187],v[96:99]\n"
LD "v[224:227],v9,s[40:43],s82 offen sc1\n"
MF "v[104:107],a[84:87],v[184:187],v[252:255]\n"
TR "a[240:241],v1 offset:28672\n"
MF "v[104:107],a[44:47],v[180:183],v[104:107]\n"
TR "a[242:243],v1 offset:30720\n"
MF "v[104:107],a[4:7],v[176:179],v[104:107]\n"
MF "v[112:115],a[8:11],v[176:179],v[252:255]\n"
TR "a[244:245],v1 offset:32768\n"
MF "v[112:115],a[48:51],v[180:183],v[112:115]\n"
TR "a[246:247],v1 offset:34816\n"
MF "v[112:115],a[88:91],v[184:187],v[112:115]\n"
MF "v[120:123],a[92:95],v[184:187],v[252:255]\n"
MF "v[120:123],a[52:55],v[180:183],v[120:123]\n"
MF "v[120:123],a[12:15],v[176:179],v[120:123]\n"
MF "v[128:131],a[16:19],v[176:179],v[252:255]\n"
MF "v[128:131],a[56:59],v[180:183],v[128:131]\n"
MF "v[128:131],a[96:99],v[184:187],v[128:131]\n"
"s_waitcnt lgkmcnt(0)\n"
MF "v[132:135],a[96:99],a[244:247],v[252:255]\n"
TR "v[176:177],v2 offset:24576\n"
MF "v[132:135],a[56:59],a[240:243],v[132:135]\n"
TR "v[178:179],v2 offset:26624\n"
MF "v[132:135],a[16:19],v[188:191],v[132:135]\n"
MF "v[124:127],a[12:15],v[188:191],v[252:255]\n"
TR "v[180:181],v2 offset:28672\n"
MF "v[124:127],a[52:55],a[240:243],v[124:127]\n"
TR "v[182:183],v2 offset:30720\n"
MF "v[124:127],a[92:95],a[244:247],v[124:127]\n"
MF "v[116:119],a[88:91],a[244:247],v[252:255]\n"
TR "v[184:185],v2 offset:32768\n"
MF "v[116:119],a[48:51],a[240:243],v[116:119]\n"
TR "v[186:187],v2 offset:34816\n"
MF "v[116:119],a[8:11],v[188:191],v[116:119]\n"
MF "v[108:111],a[4:7],v[188:191],v[252:255]\n"
MF "v[108:111],a[44:47],a[240:243],v[108:111]\n"
MF "v[108:111],a[84:87],a[244:247],v[108:111]\n"
MF "v[100:103],a[80:83],a[244:247],v[252:255]\n"
MF "v[100:103],a[40:43],a[240:243],v[100:103]\n"
MF "v[100:103],a[0:3],v[188:191],v[100:103]\n"
"s_waitcnt lgkmcnt(0)\n"
MF "v[136:139],a[0:3],v[176:179],v[252:255]\n"
TR "v[188:189],v3 offset:24576\n"
MF "v[136:139],a[40:43],v[180:183],v[136:139]\n"
TR "v[190:191],v3 offset:26624\n"
MF "v[136:139],a[80:83],v[184:187],v[136:139]\n"
MF "v[144:147],a[84:87],v[184:187],v[252:255]\n"
TR "a[240:241],v3 offset:28672\n"
MF "v[144:147],a[44:47],v[180:183],v[144:147]\n"
TR "a[242:243],v3 offset:30720\n"
MF "v[144:147],a[4:7],v[176:179],v[144:147]\n"
MF "v[152:155],a[8:11],v[176:179],v[252:255]\n"
TR "a[244:245],v3 offset:32768\n"
MF "v[152:155],a[48:51],v[180:183],v[152:155]\n"
TR "a[246:247],v3 offset:34816\n"
MF "v[152:155],a[88:91],v[184:187],v[152:155]\n"
MF "v[160:163],a[92:95],v[184:187],v[252:255]\n"
MF "v[160:163],a[52:55],v[180:183],v[160:163]\n"
MF "v[160:163],a[12:15],v[176:179],v[160:163]\n"
MF "v[168:171],a[16:19],v[176:179],v[252:255]\n"
MF "v[168:171],a[56:59],v[180:183],v[168:171]\n"
MF "v[168:171],a[96:99],v[184:187],v[168:171]\n"
"s_waitcnt lgkmcnt(0)\n"
"s_barrier\n"
MF "v[172:175],a[96:99],a[244:247],v[252:255]\n"
TR "v[176:177],v0\n"
MF "v[172:175],a[56:59],a[240:243],v[172:175]\n"
TR "v[178:179],v0 offset:2048\n"
MF "v[172:175],a[16:19],v[188:191],v[172:175]\n"
"ds_read2st64_b32 v[192:193],v13 offset0:192 offset1:193\n"
MF "v[164:167],a[12:15],v[188:191],v[252:255]\n"
TR "v[180:181],v0 offset:4096\n"
MF "v[164:167],a[52:55],a[240:243],v[164:167]\n"
TR "v[182:183],v0 offset:6144\n"
MF "v[164:167],a[92:95],a[244:247],v[164:167]\n"
"ds_read2st64_b32 v[194:195],v13 offset0:194 offset1:195\n"
MF "v[156:159],a[88:91],a[244:247],v[252:255]\n"
TR "v[184:185],v0 offset:8192\n"
MF "v[156:159],a[48:51],a[240:243],v[156:159]\n"
TR "v[186:187],v0 offset:10240\n"
MF "v[156:159],a[8:11],v[188:191],v[156:159]\n"
"ds_read2st64_b32 v[196:197],v13 offset0:196 offset1:197\n"
MF "v[148:151],a[4:7],v[188:191],v[252:255]\n"
"ds_read2st64_b32 v[198:199],v13 offset0:198 offset1:199\n"
MF "v[148:151],a[44:47],a[240:243],v[148:151]\n"
"buffer_load_dword v10,v12,s[40:43],s86 offen sc1\n"
MF "v[148:151],a[84:87],a[244:247],v[148:151]\n"
MF "v[140:143],a[80:83],a[244:247],v[252:255]\n"
MF "v[140:143],a[40:43],a[240:243],v[140:143]\n"
MF "v[140:143],a[0:3],v[188:191],v[140:143]\n"
"s_waitcnt lgkmcnt(2)\n"
MF "v[96:99],a[120:123],v[176:179],v[96:99]\n"
"s_waitcnt lgkmcnt(0)\n"
TR "v[188:189],v1\n"
"v_add_f32 v16,v16,v192\n"
MF "v[96:99],a[160:163],v[180:183],v[96:99]\n"
TR "v[190:191],v1 offset:2048\n"
"v_add_f32 v17,v17,v193\n"
MF "v[96:99],a[200:203],v[184:187],v[96:99]\n"
"ds_read2st64_b32 v[200:201],v13 offset0:200 offset1:201\n"
"v_add_f32 v18,v18,v194\n"
"s_waitcnt vmcnt(2)\n"
"v_mov_b32 v14,s76\n"
"s_add_u32 s76,s76,1\n"
MF "v[104:107],a[204:207],v[184:187],v[104:107]\n"
TR "a[240:241],v1 offset:4096\n"
"v_add_f32 v19,v19,v195\n"
"buffer_store_dword v14,v15,s[40:43],s94 offen sc1\n"
MF "v[104:107],a[164:167],v[180:183],v[104:107]\n"
TR "a[242:243],v1 offset:6144\n"
"v_add_f32 v20,v20,v196\n"
MF "v[104:107],a[124:127],v[176:179],v[104:107]\n"
"ds_read2st64_b32 v[202:203],v13 offset0:202 offset1:203\n"
"v_add_f32 v21,v21,v197\n"
MF "v[112:115],a[128:131],v[176:179],v[112:115]\n"
TR "a[244:245],v1 offset:8192\n"
"v_add_f32 v22,v22,v198\n"
MF "v[112:115],a[168:171],v[180:183],v[112:115]\n"
TR "a[246:247],v1 offset:10240\n"
"v_add_f32 v23,v23,v199\n"
MF "v[112:115],a[208:211],v[184:187],v[112:115]\n"
"ds_read2st64_b32 v[204:205],v13 offset0:204 offset1:205\n"
CV "v16,v16,v17\n"
MF "v[120:123],a[212:215],v[184:187],v[120:123]\n"
"ds_read2st64_b32 v[206:207],v13 offset0:206 offset1:207\n"
CV "v17,v18,v19\n"
MF "v[120:123],a[172:175],v[180:183],v[120:123]\n"
"s_add_u32 s87,s90,0\n"
CV "v18,v20,v21\n"
CV "v19,v22,v23\n"
MF "v[120:123],a[132:135],v[176:179],v[120:123]\n"
MF "v[128:131],a[136:139],v[176:179],v[128:131]\n"
"buffer_store_dwordx4 v[16:19],v9,s[40:43],s87 offen sc1\n"
MF "v[128:131],a[176:179],v[180:183],v[128:131]\n"
MF "v[128:131],a[216:219],v[184:187],v[128:131]\n"
"s_waitcnt lgkmcnt(2)\n"
MF "v[132:135],a[216:219],a[244:247],v[132:135]\n"
"s_waitcnt lgkmcnt(0)\n"
TR "v[176:177],v2\n"
"v_add_f32 v24,v24,v200\n"
MF "v[132:135],a[176:179],a[240:243],v[132:135]\n"
".Lpg32h:\n"
"s_cbranch_execz .Lisr4\n"
TR "v[178:179],v2 offset:2048\n"
"v_add_f32 v25,v25,v201\n"
MF "v[132:135],a[136:139],v[188:191],v[132:135]\n"
"ds_read2st64_b32 v[192:193],v13 offset0:208 offset1:209\n"
"v_add_f32 v26,v26,v202\n"
MF "v[124:127],a[132:135],v[188:191],v[124:127]\n"
TR "v[180:181],v2 offset:4096\n"
"v_add_f32 v27,v27,v203\n"
MF "v[124:127],a[172:175],a[240:243],v[124:127]\n"
TR "v[182:183],v2 offset:6144\n"
"v_add_f32 v28,v28,v204\n"
MF "v[124:127],a[212:215],a[244:247],v[124:127]\n"
"ds_read2st64_b32 v[194:195],v13 offset0:210 offset1:211\n"
"v_add_f32 v29,v29,v205\n"
MF "v[116:119],a[208:211],a[244:247],v[116:119]\n"
TR "v[184:185],v2 offset:8192\n"
"v_add_f32 v30,v30,v206\n"
MF "v[116:119],a[168:171],a[240:243],v[116:119]\n"
TR "v[186:187],v2 offset:10240\n"
"v_add_f32 v31,v31,v207\n"
MF "v[116:119],a[128:131],v[188:191],v[116:119]\n"
"ds_read2st64_b32 v[196:197],v13 offset0:212 offset1:213\n"
CV "v24,v24,v25\n"
MF "v[108:111],a[124:127],v[188:191],v[108:111]\n"
"ds_read2st64_b32 v[198:199],v13 offset0:214 offset1:215\n"
CV "v25,v26,v27\n"
MF "v[108:111],a[164:167],a[240:243],v[108:111]\n"
"s_add_u32 s87,s90,1024\n"
CV "v26,v28,v29\n"
CV "v27,v30,v31\n"
MF "v[108:111],a[204:207],a[244:247],v[108:111]\n"
MF "v[100:103],a[200:203],a[244:247],v[100:103]\n"
"buffer_store_dwordx4 v[24:27],v9,s[40:43],s87 offen sc1\n"
MF "v[100:103],a[160:163],a[240:243],v[100:103]\n"
MF "v[100:103],a[120:123],v[188:191],v[100:103]\n"
"s_waitcnt lgkmcnt(2)\n"
MF "v[136:139],a[120:123],v[176:179],v[136:139]\n"
"s_waitcnt lgkmcnt(0)\n"
TR "v[188:189],v3\n"
"v_add_f32 v32,v32,v192\n"
MF "v[136:139],a[160:163],v[180:183],v[136:139]\n"
TR "v[190:191],v3 offset:2048\n"
"v_add_f32 v33,v33,v193\n"
MF "v[136:139],a[200:203],v[184:187],v[136:139]\n"
"ds_read2st64_b32 v[200:201],v13 offset0:216 offset1:217\n"
"v_add_f32 v34,v34,v194\n"
MF "v[144:147],a[204:207],v[184:187],v[144:147]\n"
TR "a[240:241],v3 offset:4096\n"
"v_add_f32 v35,v35,v195\n"
MF "v[144:147],a[164:167],v[180:183],v[144:147]\n"
TR "a[242:243],v3 offset:6144\n"
"v_add_f32 v36,v36,v196\n"
MF "v[144:147],a[124:127],v[176:179],v[144:147]\n"
"ds_read2st64_b32 v[202:203],v13 offset0:218 offset1:219\n"
"v_add_f32 v37,v37,v197\n"
MF "v[152:155],a[128:131],v[176:179],v[152:155]\n"
TR "a[244:245],v3 offset:8192\n"
"v_add_f32 v38,v38,v198\n"
MF "v[152:155],a[168:171],v[180:183],v[152:155]\n"
TR "a[246:247],v3 offset:10240\n"
"v_add_f32 v39,v39,v199\n"
MF "v[152:155],a[208:211],v[184:187],v[152:155]\n"
"ds_read2st64_b32 v[204:205],v13 offset0:220 offset1:221\n"
CV "v32,v32,v33\n"
MF "v[160:163],a[212:215],v[184:187],v[160:163]\n"
"ds_read2st64_b32 v[206:207],v13 offset0:222 offset1:223\n"
CV "v33,v34,v35\n"
MF "v[160:163],a[172:175],v[180:183],v[160:163]\n"
"s_add_u32 s87,s90,2048\n"
CV "v34,v36,v37\n"
CV "v35,v38,v39\n"
MF "v[160:163],a[132:135],v[176:179],v[160:163]\n"
MF "v[168:171],a[136:139],v[176:179],v[168:171]\n"
"buffer_store_dwordx4 v[32:35],v9,s[40:43],s87 offen sc1\n"
MF "v[168:171],a[176:179],v[180:183],v[168:171]\n"
MF "v[168:171],a[216:219],v[184:187],v[168:171]\n"
"s_waitcnt lgkmcnt(0)\n"
"s_barrier\n"
MF "v[172:175],a[216:219],a[244:247],v[172:175]\n"
TR "v[176:177],v0 offset:49152\n"
"v_add_f32 v40,v40,v200\n"
MF "v[172:175],a[176:179],a[240:243],v[172:175]\n"
"s_mov_b32 s82,s70\n"
TR "v[178:179],v0 offset:51200\n"
"v_add_f32 v41,v41,v201\n"
MF "v[172:175],a[136:139],v[188:191],v[172:175]\n"
"ds_read2st64_b32 v[192:193],v13 offset0:224 offset1:225\n"
"v_add_f32 v42,v42,v202\n"
MF "v[164:167],a[132:135],v[188:191],v[164:167]\n"
TR "v[180:181],v0 offset:53248\n"
"v_add_f32 v43,v43,v203\n"
MF "v[164:167],a[172:175],a[240:243],v[164:167]\n"
TR "v[182:183],v0 offset:55296\n"
"v_add_f32 v44,v44,v204\n"
MF "v[164:167],a[212:215],a[244:247],v[164:167]\n"
"ds_read2st64_b32 v[194:195],v13 offset0:226 offset1:227\n"
"v_add_f32 v45,v45,v205\n"
MF "v[156:159],a[208:211],a[244:247],v[156:159]\n"
TR "v[184:185],v0 offset:57344\n"
"v_add_f32 v46,v46,v206\n"
MF "v[156:159],a[168:171],a[240:243],v[156:159]\n"
TR "v[186:187],v0 offset:59392\n"
"v_add_f32 v47,v47,v207\n"
MF "v[156:159],a[128:131],v[188:191],v[156:159]\n"
"ds_read2st64_b32 v[196:197],v13 offset0:228 offset1:229\n"
CV "v40,v40,v41\n"
MF "v[148:151],a[124:127],v[188:191],v[148:151]\n"
"ds_read2st64_b32 v[198:199],v13 offset0:230 offset1:231\n"
CV "v41,v42,v43\n"
MF "v[148:151],a[164:167],a[240:243],v[148:151]\n"
"s_add_u32 s87,s90,3072\n"
CV "v42,v44,v45\n"
CV "v43,v46,v47\n"
MF "v[148:151],a[204:207],a[244:247],v[148:151]\n"
MF "v[140:143],a[200:203],a[244:247],v[140:143]\n"
"buffer_store_dwordx4 v[40:43],v9,s[40:43],s87 offen sc1\n"
MF "v[140:143],a[160:163],a[240:243],v[140:143]\n"
MF "v[140:143],a[120:123],v[188:191],v[140:143]\n"
"s_waitcnt lgkmcnt(2)\n"
MF "v[96:99],a[20:23],v[176:179],v[96:99]\n"
"s_waitcnt lgkmcnt(0)\n"
TR "v[188:189],v1 offset:49152\n"
"v_add_f32 v48,v48,v192\n"
MF "v[96:99],a[60:63],v[180:183],v[96:99]\n"
TR "v[190:191],v1 offset:51200\n"
"v_add_f32 v49,v49,v193\n"
MF "v[96:99],a[100:103],v[184:187],v[96:99]\n"
"ds_read2st64_b32 v[200:201],v13 offset0:0 offset1:1\n"
"v_add_f32 v50,v50,v194\n"
MF "v[104:107],a[104:107],v[184:187],v[104:107]\n"
TR "a[240:241],v1 offset:53248\n"
"v_add_f32 v51,v51,v195\n"
MF "v[104:107],a[64:67],v[180:183],v[104:107]\n"
TR "a[242:243],v1 offset:55296\n"
"v_add_f32 v52,v52,v196\n"
MF "v[104:107],a[24:27],v[176:179],v[104:107]\n"
"ds_read2st64_b32 v[202:203],v13 offset0:2 offset1:3\n"
"v_add_f32 v53,v53,v197\n"
MF "v[112:115],a[28:31],v[176:179],v[112:115]\n"
TR "a[244:245],v1 offset:57344\n"
"v_add_f32 v54,v54,v198\n"
MF "v[112:115],a[68:71],v[180:183],v[112:115]\n"
TR "a[246:247],v1 offset:59392\n"
"v_add_f32 v55,v55,v199\n"
MF "v[112:115],a[108:111],v[184:187],v[112:115]\n"
"ds_read2st64_b32 v[204:205],v13 offset0:4 offset1:5\n"
CV "v48,v48,v49\n"
MF "v[120:123],a[112:115],v[184:187],v[120:123]\n"
"ds_read2st64_b32 v[206:207],v13 offset0:6 offset1:7\n"
CV "v49,v50,v51\n"
MF "v[120:123],a[72:75],v[180:183],v[120:123]\n"
"s_add_u32 s87,s90,4096\n"
CV "v50,v52,v53\n"
CV "v51,v54,v55\n"
MF "v[120:123],a[32:35],v[176:179],v[120:123]\n"
MF "v[128:131],a[36:39],v[176:179],v[128:131]\n"
"buffer_store_dwordx4 v[48:51],v9,s[40:43],s87 offen sc1\n"
MF "v[128:131],a[76:79],v[180:183],v[128:131]\n"
MF "v[128:131],a[116:119],v[184:187],v[128:131]\n"
"s_waitcnt lgkmcnt(2)\n"
MF "v[132:135],a[116:119],a[244:247],v[132:135]\n"
"s_waitcnt lgkmcnt(0)\n"
TR "v[176:177],v2 offset:49152\n"
"v_add_f32 v56,v56,v200\n"
MF "v[132:135],a[76:79],a[240:243],v[132:135]\n"
TR "v[178:179],v2 offset:51200\n"
"v_add_f32 v57,v57,v201\n"
MF "v[132:135],a[36:39],v[188:191],v[132:135]\n"
"ds_read2st64_b32 v[192:193],v13 offset0:8 offset1:9\n"
"v_add_f32 v58,v58,v202\n"
MF "v[124:127],a[32:35],v[188:191],v[124:127]\n"
TR "v[180:181],v2 offset:53248\n"
"v_add_f32 v59,v59,v203\n"
MF "v[124:127],a[72:75],a[240:243],v[124:127]\n"
TR "v[182:183],v2 offset:55296\n"
"v_add_f32 v60,v60,v204\n"
MF "v[124:127],a[112:115],a[244:247],v[124:127]\n"
"ds_read2st64_b32 v[194:195],v13 offset0:10 offset1:11\n"
"v_add_f32 v61,v61,v205\n"
MF "v[116:119],a[108:111],a[244:247],v[116:119]\n"
TR "v[184:185],v2 offset:57344\n"
"v_add_f32 v62,v62,v206\n"
MF "v[116:119],a[68:71],a[240:243],v[116:119]\n"
TR "v[186:187],v2 offset:59392\n"
"v_add_f32 v63,v63,v207\n"
MF "v[116:119],a[28:31],v[188:191],v[116:119]\n"
"ds_read2st64_b32 v[196:197],v13 offset0:12 offset1:13\n"
CV "v56,v56,v57\n"
MF "v[108:111],a[24:27],v[188:191],v[108:111]\n"
"ds_read2st64_b32 v[198:199],v13 offset0:14 offset1:15\n"
CV "v57,v58,v59\n"
MF "v[108:111],a[64:67],a[240:243],v[108:111]\n"
"s_add_u32 s87,s90,10240\n"
CV "v58,v60,v61\n"
CV "v59,v62,v63\n"
MF "v[108:111],a[104:107],a[244:247],v[108:111]\n"
MF "v[100:103],a[100:103],a[244:247],v[100:103]\n"
"buffer_store_dwordx4 v[56:59],v9,s[40:43],s87 offen sc1\n"
MF "v[100:103],a[60:63],a[240:243],v[100:103]\n"
"s_waitcnt vmcnt(7)\n"
"s_cmp_lt_u32 s85,6\n"
MF "v[100:103],a[20:23],v[188:191],v[100:103]\n"
"s_cbranch_scc1 .Ljs10\n"
MF "v[136:139],a[252:255],v[208:211],v[136:139]\n"
MF "v[144:147],a[252:255],v[212:215],v[144:147]\n"
MF "v[152:155],a[252:255],v[216:219],v[152:155]\n"
MF "v[160:163],a[252:255],v[220:223],v[160:163]\n"
MF "v[168:171],a[252:255],v[224:227],v[168:171]\n"
MF "v[140:143],a[248:251],v[208:211],v[140:143]\n"
MF "v[148:151],a[248:251],v[212:215],v[148:151]\n"
MF "v[156:159],a[248:251],v[216:219],v[156:159]\n"
MF "v[164:167],a[248:251],v[220:223],v[164:167]\n"
MF "v[172:175],a[248:251],v[224:227],v[172:175]\n"
".Ljs10:\n"
"s_waitcnt lgkmcnt(2)\n"
MF "v[136:139],a[20:23],v[176:179],v[136:139]\n"
"s_waitcnt lgkmcnt(0)\n"
TR "v[188:189],v3 offset:49152\n"
"v_add_f32 v64,v64,v192\n"
MF "v[136:139],a[60:63],v[180:183],v[136:139]\n"
TR "v[190:191],v3 offset:51200\n"
"v_add_f32 v65,v65,v193\n"
MF "v[136:139],a[100:103],v[184:187],v[136:139]\n"
"ds_read2st64_b32 v[200:201],v13 offset0:16 offset1:17\n"
"v_add_f32 v66,v66,v194\n"
MF "v[144:147],a[104:107],v[184:187],v[144:147]\n"
TR "a[240:241],v3 offset:53248\n"
"v_add_f32 v67,v67,v195\n"
MF "v[144:147],a[64:67],v[180:183],v[144:147]\n"
TR "a[242:243],v3 offset:55296\n"
"v_add_f32 v68,v68,v196\n"
MF "v[144:147],a[24:27],v[176:179],v[144:147]\n"
"ds_read2st64_b32 v[202:203],v13 offset0:18 offset1:19\n"
"v_add_f32 v69,v69,v197\n"
MF "v[152:155],a[28:31],v[176:179],v[152:155]\n"
TR "a[244:245],v3 offset:57344\n"
"v_add_f32 v70,v70,v198\n"
MF "v[152:155],a[68:71],v[180:183],v[152:155]\n"
TR "a[246:247],v3 offset:59392\n"
"v_add_f32 v71,v71,v199\n"
MF "v[152:155],a[108:111],v[184:187],v[152:155]\n"
"ds_read2st64_b32 v[204:205],v13 offset0:20 offset1:21\n"
CV "v64,v64,v65\n"
MF "v[160:163],a[112:115],v[184:187],v[160:163]\n"
"ds_read2st64_b32 v[206:207],v13 offset0:22 offset1:23\n"
CV "v65,v66,v67\n"
MF "v[160:163],a[72:75],v[180:183],v[160:163]\n"
"s_add_u32 s87,s90,11264\n"
CV "v66,v68,v69\n"
CV "v67,v70,v71\n"
MF "v[160:163],a[32:35],v[176:179],v[160:163]\n"
"v_sub_u32 v10,v10,v11\n"
MF "v[168:171],a[36:39],v[176:179],v[168:171]\n"
"buffer_store_dwordx4 v[64:67],v9,s[40:43],s87 offen sc1\n"
MF "v[168:171],a[76:79],v[180:183],v[168:171]\n"
".Lnf5:\n"
"v_readlane_b32 s58,v10,s67\n"
"s_cmp_lt_u32 s58,s85\n"
"s_cbranch_scc1 .Lsp5\n"
".Lnb5:\n"
MF "v[168:171],a[116:119],v[184:187],v[168:171]\n"
"s_waitcnt lgkmcnt(0)\n"
"s_barrier\n"
MF "v[172:175],a[116:119],a[244:247],v[172:175]\n"
TR "v[176:177],v4\n"
"v_add_f32 v72,v72,v200\n"
MF "v[172:175],a[76:79],a[240:243],v[172:175]\n"
"s_add_u32 s70,s70,20480\n"
TR "v[178:179],v4 offset:2048\n"
"v_add_f32 v73,v73,v201\n"
MF "v[172:175],a[36:39],v[188:191],v[172:175]\n"
"ds_read2st64_b32 v[192:193],v13 offset0:24 offset1:25\n"
"v_add_f32 v74,v74,v202\n"
MF "v[164:167],a[32:35],v[188:191],v[164:167]\n"
TR "v[180:181],v4 offset:4096\n"
"v_add_f32 v75,v75,v203\n"
MF "v[164:167],a[72:75],a[240:243],v[164:167]\n"
TR "v[182:183],v4 offset:6144\n"
"v_add_f32 v76,v76,v204\n"
MF "v[164:167],a[112:115],a[244:247],v[164:167]\n"
"ds_read2st64_b32 v[194:195],v13 offset0:26 offset1:27\n"
"v_add_f32 v77,v77,v205\n"
MF "v[156:159],a[108:111],a[244:247],v[156:159]\n"
TR "v[184:185],v4 offset:8192\n"
"v_add_f32 v78,v78,v206\n"
MF "v[156:159],a[68:71],a[240:243],v[156:159]\n"
TR "v[186:187],v4 offset:10240\n"
"v_add_f32 v79,v79,v207\n"
MF "v[156:159],a[28:31],v[188:191],v[156:159]\n"
"ds_read2st64_b32 v[196:197],v13 offset0:28 offset1:29\n"
CV "v72,v72,v73\n"
MF "v[148:151],a[24:27],v[188:191],v[148:151]\n"
"ds_read2st64_b32 v[198:199],v13 offset0:30 offset1:31\n"
CV "v73,v74,v75\n"
MF "v[148:151],a[64:67],a[240:243],v[148:151]\n"
"s_add_u32 s87,s90,12288\n"
CV "v74,v76,v77\n"
CV "v75,v78,v79\n"
MF "v[148:151],a[104:107],a[244:247],v[148:151]\n"
MF "v[140:143],a[100:103],a[244:247],v[140:143]\n"
"buffer_store_dwordx4 v[72:75],v9,s[40:43],s87 offen sc1\n"
MF "v[140:143],a[60:63],a[240:243],v[140:143]\n"
LD "v[208:211],v9,s[40:43],s82 offen sc1\n"
MF "v[140:143],a[20:23],v[188:191],v[140:143]\n"
"s_waitcnt lgkmcnt(2)\n"
MF "v[96:99],a[140:143],v[176:179],v[96:99]\n"
"s_waitcnt lgkmcnt(0)\n"
TR "v[188:189],v5\n"
"v_add_f32 v80,v80,v192\n"
MF "v[96:99],a[180:183],v[180:183],v[96:99]\n"
TR "v[190:191],v5 offset:2048\n"
"v_add_f32 v81,v81,v193\n"
MF "v[96:99],a[220:223],v[184:187],v[96:99]\n"
"ds_read2st64_b32 v[200:201],v13 offset0:32 offset1:33\n"
"v_add_f32 v82,v82,v194\n"
MF "v[104:107],a[224:227],v[184:187],v[104:107]\n"
TR "a[240:241],v5 offset:4096\n"
"v_add_f32 v83,v83,v195\n"
MF "v[104:107],a[184:187],v[180:183],v[104:107]\n"
TR "a[242:243],v5 offset:6144\n"
"v_add_f32 v84,v84,v196\n"
MF "v[104:107],a[144:147],v[176:179],v[104:107]\n"
"ds_read2st64_b32 v[202:203],v13 offset0:34 offset1:35\n"
"v_add_f32 v85,v85,v197\n"
MF "v[112:115],a[148:151],v[176:179],v[112:115]\n"
TR "a[244:245],v5 offset:8192\n"
"v_add_f32 v86,v86,v198\n"
MF "v[112:115],a[188:191],v[180:183],v[112:115]\n"
TR "a[246:247],v5 offset:10240\n"
"v_add_f32 v87,v87,v199\n"
MF "v[112:115],a[228:231],v[184:187],v[112:115]\n"
"ds_read2st64_b32 v[204:205],v13 offset0:36 offset1:37\n"
CV "v80,v80,v81\n"
MF "v[120:123],a[232:235],v[184:187],v[120:123]\n"
"ds_read2st64_b32 v[206:207],v13 offset0:38 offset1:39\n"
CV "v81,v82,v83\n"
MF "v[120:123],a[192:195],v[180:183],v[120:123]\n"
"s_add_u32 s87,s90,13312\n"
CV "v82,v84,v85\n"
CV "v83,v86,v87\n"
MF "v[120:123],a[152:155],v[176:179],v[120:123]\n"
MF "v[128:131],a[156:159],v[176:179],v[128:131]\n"
"buffer_store_dwordx4 v[80:83],v9,s[40:43],s87 offen sc1\n"
MF "v[128:131],a[196:199],v[180:183],v[128:131]\n"
LD "v[212:215],v9,s[40:43],s82 offen offset:1024 sc1\n"
MF "v[128:131],a[236:239],v[184:187],v[128:131]\n"
"s_waitcnt lgkmcnt(2)\n"
MF "v[132:135],a[236:239],a[244:247],v[132:135]\n"
"s_waitcnt lgkmcnt(0)\n"
TR "v[176:177],v6\n"
"v_add_f32 v88,v88,v200\n"
MF "v[132:135],a[196:199],a[240:243],v[132:135]\n"
TR "v[178:179],v6 offset:2048\n"
"v_add_f32 v89,v89,v201\n"
MF "v[132:135],a[156:159],v[188:191],v[132:135]\n"
"v_add_f32 v90,v90,v202\n"
"v_add_f32 v91,v91,v203\n"
MF "v[124:127],a[152:155],v[188:191],v[124:127]\n"
TR "v[180:181],v6 offset:4096\n"
"v_add_f32 v92,v92,v204\n"
MF "v[124:127],a[192:195],a[240:243],v[124:127]\n"
TR "v[182:183],v6 offset:6144\n"
"v_add_f32 v93,v93,v205\n"
MF "v[124:127],a[232:235],a[244:247],v[124:127]\n"
"v_add_f32 v94,v94,v206\n"
"v_add_f32 v95,v95,v207\n"
MF "v[116:119],a[228:231],a[244:247],v[116:119]\n"
TR "v[184:185],v6 offset:8192\n"
CV "v88,v88,v89\n"
MF "v[116:119],a[188:191],a[240:243],v[116:119]\n"
TR "v[186:187],v6 offset:10240\n"
CV "v89,v90,v91\n"
MF "v[116:119],a[148:151],v[188:191],v[116:119]\n"
"s_add_u32 s87,s90,14336\n"
CV "v90,v92,v93\n"
CV "v91,v94,v95\n"
MF "v[108:111],a[144:147],v[188:191],v[108:111]\n"
MF "v[108:111],a[184:187],a[240:243],v[108:111]\n"
MF "v[108:111],a[224:227],a[244:247],v[108:111]\n"
MF "v[100:103],a[220:223],a[244:247],v[100:103]\n"
"buffer_store_dwordx4 v[88:91],v9,s[40:43],s87 offen sc1\n"
MF "v[100:103],a[180:183],a[240:243],v[100:103]\n"
"s_mov_b32 s90,s91\n"
"s_add_u32 s91,s91,20480\n"
MF "v[100:103],a[140:143],v[188:191],v[100:103]\n"
"s_waitcnt lgkmcnt(0)\n"
LD "v[216:219],v9,s[40:43],s82 offen offset:2048 sc1\n"
MF "v[136:139],a[140:143],v[176:179],v[136:139]\n"
TR "v[188:189],v7\n"
MF "v[136:139],a[180:183],v[180:183],v[136:139]\n"
TR "v[190:191],v7 offset:2048\n"
MF "v[136:139],a[220:223],v[184:187],v[136:139]\n"
MF "v[144:147],a[224:227],v[184:187],v[144:147]\n"
TR "a[240:241],v7 offset:4096\n"
MF "v[144:147],a[184:187],v[180:183],v[144:147]\n"
TR "a[242:243],v7 offset:6144\n"
MF "v[144:147],a[144:147],v[176:179],v[144:147]\n"
MF "v[152:155],a[148:151],v[176:179],v[152:155]\n"
TR "a[244:245],v7 offset:8192\n"
MF "v[152:155],a[188:191],v[180:183],v[152:155]\n"
TR "a[246:247],v7 offset:10240\n"
MF "v[152:155],a[228:231],v[184:187],v[152:155]\n"
MF "v[160:163],a[232:235],v[184:187],v[160:163]\n"
MF "v[160:163],a[192:195],v[180:183],v[160:163]\n"
MF "v[160:163],a[152:155],v[176:179],v[160:163]\n"
MF "v[168:171],a[156:159],v[176:179],v[168:171]\n"
MF "v[168:171],a[196:199],v[180:183],v[168:171]\n"
LD "v[220:223],v9,s[40:43],s82 offen offset:3072 sc1\n"
MF "v[168:171],a[236:239],v[184:187],v[168:171]\n"
"s_waitcnt lgkmcnt(0)\n"
"s_barrier\n"
MF "v[172:175],a[236:239],a[244:247],v[172:175]\n"
TR "v[176:177],v0 offset:24576\n"
MF "v[172:175],a[196:199],a[240:243],v[172:175]\n"
"s_add_u32 s85,s85,1\n"
TR "v[178:179],v0 offset:26624\n"
MF "v[172:175],a[156:159],v[188:191],v[172:175]\n"
MF "v[164:167],a[152:155],v[188:191],v[164:167]\n"
TR "v[180:181],v0 offset:28672\n"
MF "v[164:167],a[192:195],a[240:243],v[164:167]\n"
TR "v[182:183],v0 offset:30720\n"
MF "v[164:167],a[232:235],a[244:247],v[164:167]\n"
MF "v[156:159],a[228:231],a[244:247],v[156:159]\n"
TR "v[184:185],v0 offset:32768\n"
MF "v[156:159],a[188:191],a[240:243],v[156:159]\n"
TR "v[186:187],v0 offset:34816\n"
MF "v[156:159],a[148:151],v[188:191],v[156:159]\n"
MF "v[148:151],a[144:147],v[188:191],v[148:151]\n"
MF "v[148:151],a[184:187],a[240:243],v[148:151]\n"
MF "v[148:151],a[224:227],a[244:247],v[148:151]\n"
MF "v[140:143],a[220:223],a[244:247],v[140:143]\n"
MF "v[140:143],a[180:183],a[240:243],v[140:143]\n"
MF "v[140:143],a[140:143],v[188:191],v[140:143]\n"
"s_waitcnt lgkmcnt(0)\n"
MF "v[16:19],a[0:3],v[176:179],v[252:255]\n"
TR "v[188:189],v1 offset:24576\n"
MF "v[16:19],a[40:43],v[180:183],v[16:19]\n"
"s_add_u32 s82,s82,4096\n"
TR "v[190:191],v1 offset:26624\n"
MF "v[16:19],a[80:83],v[184:187],v[16:19]\n"
LD "v[224:227],v9,s[40:43],s82 offen sc1\n"
MF "v[24:27],a[84:87],v[184:187],v[252:255]\n"
TR "a[240:241],v1 offset:28672\n"
MF "v[24:27],a[44:47],v[180:183],v[24:27]\n"
TR "a[242:243],v1 offset:30720\n"
MF "v[24:27],a[4:7],v[176:179],v[24:27]\n"
MF "v[32:35],a[8:11],v[176:179],v[252:255]\n"
TR "a[244:245],v1 offset:32768\n"
MF "v[32:35],a[48:51],v[180:183],v[32:35]\n"
TR "a[246:247],v1 offset:34816\n"
MF "v[32:35],a[88:91],v[184:187],v[32:35]\n"
MF "v[40:43],a[92:95],v[184:187],v[252:255]\n"
MF "v[40:43],a[52:55],v[180:183],v[40:43]\n"
MF "v[40:43],a[12:15],v[176:179],v[40:43]\n"
MF "v[48:51],a[16:19],v[176:179],v[252:255]\n"
MF "v[48:51],a[56:59],v[180:183],v[48:51]\n"
MF "v[48:51],a[96:99],v[184:187],v[48:51]\n"
"s_waitcnt lgkmcnt(0)\n"
MF "v[52:55],a[96:99],a[244:247],v[252:255]\n"
TR "v[176:177],v2 offset:24576\n"
MF "v[52:55],a[56:59],a[240:243],v[52:55]\n"
TR "v[178:179],v2 offset:26624\n"
MF "v[52:55],a[16:19],v[188:191],v[52:55]\n"
MF "v[44:47],a[12:15],v[188:191],v[252:255]\n"
TR "v[180:181],v2 offset:28672\n"
MF "v[44:47],a[52:55],a[240:243],v[44:47]\n"
TR "v[182:183],v2 offset:30720\n"
MF "v[44:47],a[92:95],a[244:247],v[44:47]\n"
MF "v[36:39],a[88:91],a[244:247],v[252:255]\n"
TR "v[184:185],v2 offset:32768\n"
MF "v[36:39],a[48:51],a[240:243],v[36:39]\n"
TR "v[186:187],v2 offset:34816\n"
MF "v[36:39],a[8:11],v[188:191],v[36:39]\n"
MF "v[28:31],a[4:7],v[188:191],v[252:255]\n"
MF "v[28:31],a[44:47],a[240:243],v[28:31]\n"
MF "v[28:31],a[84:87],a[244:247],v[28:31]\n"
MF "v[20:23],a[80:83],a[244:247],v[252:255]\n"
MF "v[20:23],a[40:43],a[240:243],v[20:23]\n"
MF "v[20:23],a[0:3],v[188:191],v[20:23]\n"
"s_waitcnt lgkmcnt(0)\n"
MF "v[56:59],a[0:3],v[176:179],v[252:255]\n"
TR "v[188:189],v3 offset:24576\n"
MF "v[56:59],a[40:43],v[180:183],v[56:59]\n"
TR "v[190:191],v3 offset:26624\n"
MF "v[56:59],a[80:83],v[184:187],v[56:59]\n"
MF "v[64:67],a[84:87],v[184:187],v[252:255]\n"
TR "a[240:241],v3 offset:28672\n"
MF "v[64:67],a[44:47],v[180:183],v[64:67]\n"
TR "a[242:243],v3 offset:30720\n"
MF "v[64:67],a[4:7],v[176:179],v[64:67]\n"
MF "v[72:75],a[8:11],v[176:179],v[252:255]\n"
TR "a[244:245],v3 offset:32768\n"
MF "v[72:75],a[48:51],v[180:183],v[72:75]\n"
TR "a[246:247],v3 offset:34816\n"
MF "v[72:75],a[88:91],v[184:187],v[72:75]\n"
MF "v[80:83],a[92:95],v[184:187],v[252:255]\n"
MF "v[80:83],a[52:55],v[180:183],v[80:83]\n"
MF "v[80:83],a[12:15],v[176:179],v[80:83]\n"
MF "v[88:91],a[16:19],v[176:179],v[252:255]\n"
MF "v[88:91],a[56:59],v[180:183],v[88:91]\n"
MF "v[88:91],a[96:99],v[184:187],v[88:91]\n"
"s_waitcnt lgkmcnt(0)\n"
"s_barrier\n"
MF "v[92:95],a[96:99],a[244:247],v[252:255]\n"
TR "v[176:177],v0\n"
MF "v[92:95],a[56:59],a[240:243],v[92:95]\n"
TR "v[178:179],v0 offset:2048\n"
MF "v[92:95],a[16:19],v[188:191],v[92:95]\n"
"ds_read2st64_b32 v[192:193],v13 offset0:192 offset1:193\n"
MF "v[84:87],a[12:15],v[188:191],v[252:255]\n"
TR "v[180:181],v0 offset:4096\n"
MF "v[84:87],a[52:55],a[240:243],v[84:87]\n"
TR "v[182:183],v0 offset:6144\n"
MF "v[84:87],a[92:95],a[244:247],v[84:87]\n"
"ds_read2st64_b32 v[194:195],v13 offset0:194 offset1:195\n"
MF "v[76:79],a[88:91],a[244:247],v[252:255]\n"
TR "v[184:185],v0 offset:8192\n"
MF "v[76:79],a[48:51],a[240:243],v[76:79]\n"
TR "v[186:187],v0 offset:10240\n"
MF "v[76:79],a[8:11],v[188:191],v[76:79]\n"
"ds_read2st64_b32 v[196:197],v13 offset0:196 offset1:197\n"
MF "v[68:71],a[4:7],v[188:191],v[252:255]\n"
"ds_read2st64_b32 v[198:199],v13 offset0:198 offset1:199\n"
MF "v[68:71],a[44:47],a[240:243],v[68:71]\n"
"buffer_load_dword v10,v12,s[40:43],s86 offen sc1\n"
MF "v[68:71],a[84:87],a[244:247],v[68:71]\n"
MF "v[60:63],a[80:83],a[244:247],v[252:255]\n"
MF "v[60:63],a[40:43],a[240:243],v[60:63]\n"
MF "v[60:63],a[0:3],v[188:191],v[60:63]\n"
"s_waitcnt lgkmcnt(2)\n"
MF "v[16:19],a[120:123],v[176:179],v[16:19]\n"
"s_waitcnt lgkmcnt(0)\n"
TR "v[188:189],v1\n"
"v_add_f32 v96,v96,v192\n"
MF "v[16:19],a[160:163],v[180:183],v[16:19]\n"
TR "v[190:191],v1 offset:2048\n"
"v_add_f32 v97,v97,v193\n"
MF "v[16:19],a[200:203],v[184:187],v[16:19]\n"
"ds_read2st64_b32 v[200:201],v13 offset0:200 offset1:201\n"
"v_add_f32 v98,v98,v194\n"
"s_waitcnt vmcnt(4)\n"
"v_mov_b32 v14,s76\n"
"s_add_u32 s76,s76,1\n"
MF "v[24:27],a[204:207],v[184:187],v[24:27]\n"
TR "a[240:241],v1 offset:4096\n"
"v_add_f32 v99,v99,v195\n"
"buffer_store_dword v14,v15,s[40:43],s94 offen sc1\n"
MF "v[24:27],a[164:167],v[180:183],v[24:27]\n"
TR "a[242:243],v1 offset:6144\n"
"v_add_f32 v100,v100,v196\n"
MF "v[24:27],a[124:127],v[176:179],v[24:27]\n"
"ds_read2st64_b32 v[202:203],v13 offset0:202 offset1:203\n"
"v_add_f32 v101,v101,v197\n"
MF "v[32:35],a[128:131],v[176:179],v[32:35]\n"
TR "a[244:245],v1 offset:8192\n"
"v_add_f32 v102,v102,v198\n"
MF "v[32:35],a[168:171],v[180:183],v[32:35]\n"
TR "a[246:247],v1 offset:10240\n"
"v_add_f32 v103,v103,v199\n"
MF "v[32:35],a[208:211],v[184:187],v[32:35]\n"
"ds_read2st64_b32 v[204:205],v13 offset0:204 offset1:205\n"
CV "v96,v96,v97\n"
MF "v[40:43],a[212:215],v[184:187],v[40:43]\n"
"ds_read2st64_b32 v[206:207],v13 offset0:206 offset1:207\n"
CV "v97,v98,v99\n"
MF "v[40:43],a[172:175],v[180:183],v[40:43]\n"
"s_add_u32 s87,s90,0\n"
CV "v98,v100,v101\n"
CV "v99,v102,v103\n"
MF "v[40:43],a[132:135],v[176:179],v[40:43]\n"
MF "v[48:51],a[136:139],v[176:179],v[48:51]\n"
"buffer_store_dwordx4 v[96:99],v9,s[40:43],s87 offen sc1\n"
MF "v[48:51],a[176:179],v[180:183],v[48:51]\n"
".Lpg33h:\n"
"s_cbranch_execz .Lisr4\n"
MF "v[48:51],a[216:219],v[184:187],v[48:51]\n"
"s_waitcnt lgkmcnt(2)\n"
MF "v[52:55],a[216:219],a[244:247],v[52:55]\n"
"s_waitcnt lgkmcnt(0)\n"
TR "v[176:177],v2\n"
"v_add_f32 v104,v104,v200\n"
MF "v[52:55],a[176:179],a[240:243],v[52:55]\n"
TR "v[178:179],v2 offset:2048\n"
"v_add_f32 v105,v105,v201\n"
MF "v[52:55],a[136:139],v[188:191],v[52:55]\n"
"ds_read2st64_b32 v[192:193],v13 offset0:208 offset1:209\n"
"v_add_f32 v106,v106,v202\n"
MF "v[44:47],a[132:135],v[188:191],v[44:47]\n"
TR "v[180:181],v2 offset:4096\n"
"v_add_f32 v107,v107,v203\n"
MF "v[44:47],a[172:175],a[240:243],v[44:47]\n"
TR "v[182:183],v2 offset:6144\n"
"v_add_f32 v108,v108,v204\n"
MF "v[44:47],a[212:215],a[244:247],v[44:47]\n"
"ds_read2st64_b32 v[194:195],v13 offset0:210 offset1:211\n"
"v_add_f32 v109,v109,v205\n"
MF "v[36:39],a[208:211],a[244:247],v[36:39]\n"
TR "v[184:185],v2 offset:8192\n"
"v_add_f32 v110,v110,v206\n"
MF "v[36:39],a[168:171],a[240:243],v[36:39]\n"
TR "v[186:187],v2 offset:10240\n"
"v_add_f32 v111,v111,v207\n"
MF "v[36:39],a[128:131],v[188:191],v[36:39]\n"
"ds_read2st64_b32 v[196:197],v13 offset0:212 offset1:213\n"
CV "v104,v104,v105\n"
MF "v[28:31],a[124:127],v[188:191],v[28:31]\n"
"ds_read2st64_b32 v[198:199],v13 offset0:214 offset1:215\n"
CV "v105,v106,v107\n"
MF "v[28:31],a[164:167],a[240:243],v[28:31]\n"
"s_add_u32 s87,s90,1024\n"
CV "v106,v108,v109\n"
CV "v107,v110,v111\n"
MF "v[28:31],a[204:207],a[244:247],v[28:31]\n"
MF "v[20:23],a[200:203],a[244:247],v[20:23]\n"
"buffer_store_dwordx4 v[104:107],v9,s[40:43],s87 offen sc1\n"
MF "v[20:23],a[160:163],a[240:243],v[20:23]\n"
MF "v[20:23],a[120:123],v[188:191],v[20:23]\n"
"s_waitcnt lgkmcnt(2)\n"
MF "v[56:59],a[120:123],v[176:179],v[56:59]\n"
"s_waitcnt lgkmcnt(0)\n"
TR "v[188:189],v3\n"
"v_add_f32 v112,v112,v192\n"
MF "v[56:59],a[160:163],v[180:183],v[56:59]\n"
TR "v[190:191],v3 offset:2048\n"
"v_add_f32 v113,v113,v193\n"
MF "v[56:59],a[200:203],v[184:187],v[56:59]\n"
"ds_read2st64_b32 v[200:201],v13 offset0:216 offset1:217\n"
"v_add_f32 v114,v114,v194\n"
MF "v[64:67],a[204:207],v[184:187],v[64:67]\n"
TR "a[240:241],v3 offset:4096\n"
"v_add_f32 v115,v115,v195\n"
MF "v[64:67],a[164:167],v[180:183],v[64:67]\n"
TR "a[242:243],v3 offset:6144\n"
"v_add_f32 v116,v116,v196\n"
MF "v[64:67],a[124:127],v[176:179],v[64:67]\n"
"ds_read2st64_b32 v[202:203],v13 offset0:218 offset1:219\n"
"v_add_f32 v117,v117,v197\n"
MF "v[72:75],a[128:131],v[176:179],v[72:75]\n"
TR "a[244:245],v3 offset:8192\n"
"v_add_f32 v118,v118,v198\n"
MF "v[72:75],a[168:171],v[180:183],v[72:75]\n"
TR "a[246:247],v3 offset:10240\n"
"v_add_f32 v119,v119,v199\n"
MF "v[72:75],a[208:211],v[184:187],v[72:75]\n"
"ds_read2st64_b32 v[204:205],v13 offset0:220 offset1:221\n"
CV "v112,v112,v113\n"
MF "v[80:83],a[212:215],v[184:187],v[80:83]\n"
"ds_read2st64_b32 v[206:207],v13 offset0:222 offset1:223\n"
CV "v113,v114,v115\n"
MF "v[80:83],a[172:175],v[180:183],v[80:83]\n"
"s_add_u32 s87,s90,2048\n"
CV "v114,v116,v117\n"
CV "v115,v118,v119\n"
MF "v[80:83],a[132:135],v[176:179],v[80:83]\n"
MF "v[88:91],a[136:139],v[176:179],v[88:91]\n"
"buffer_store_dwordx4 v[112:115],v9,s[40:43],s87 offen sc1\n"
MF "v[88:91],a[176:179],v[180:183],v[88:91]\n"
MF "v[88:91],a[216:219],v[184:187],v[88:91]\n"
"s_waitcnt lgkmcnt(0)\n"
"s_barrier\n"
MF "v[92:95],a[216:219],a[244:247],v[92:95]\n"
TR "v[176:177],v0 offset:49152\n"
"v_add_f32 v120,v120,v200\n"
MF "v[92:95],a[176:179],a[240:243],v[92:95]\n"
"s_mov_b32 s82,s70\n"
TR "v[178:179],v0 offset:51200\n"
"v_add_f32 v121,v121,v201\n"
MF "v[92:95],a[136:139],v[188:191],v[92:95]\n"
"ds_read2st64_b32 v[192:193],v13 offset0:224 offset1:225\n"
"v_add_f32 v122,v122,v202\n"
MF "v[84:87],a[132:135],v[188:191],v[84:87]\n"
TR "v[180:181],v0 offset:53248\n"
"v_add_f32 v123,v123,v203\n"
MF "v[84:87],a[172:175],a[240:243],v[84:87]\n"
TR "v[182:183],v0 offset:55296\n"
"v_add_f32 v124,v124,v204\n"
MF "v[84:87],a[212:215],a[244:247],v[84:87]\n"
"ds_read2st64_b32 v[194:195],v13 offset0:226 offset1:227\n"
"v_add_f32 v125,v125,v205\n"
MF "v[76:79],a[208:211],a[244:247],v[76:79]\n"
TR "v[184:185],v0 offset:57344\n"
"v_add_f32 v126,v126,v206\n"
MF "v[76:79],a[168:171],a[240:243],v[76:79]\n"
TR "v[186:187],v0 offset:59392\n"
"v_add_f32 v127,v127,v207\n"
MF "v[76:79],a[128:131],v[188:191],v[76:79]\n"
"ds_read2st64_b32 v[196:197],v13 offset0:228 offset1:229\n"
CV "v120,v120,v121\n"
MF "v[68:71],a[124:127],v[188:191],v[68:71]\n"
"ds_read2st64_b32 v[198:199],v13 offset0:230 offset1:231\n"
CV "v121,v122,v123\n"
MF "v[68:71],a[164:167],a[240:243],v[68:71]\n"
"s_add_u32 s87,s90,3072\n"
CV "v122,v124,v125\n"
CV "v123,v126,v127\n"
MF "v[68:71],a[204:207],a[244:247],v[68:71]\n"
MF "v[60:63],a[200:203],a[244:247],v[60:63]\n"
"buffer_store_dwordx4 v[120:123],v9,s[40:43],s87 offen sc1\n"
MF "v[60:63],a[160:163],a[240:243],v[60:63]\n"
MF "v[60:63],a[120:123],v[188:191],v[60:63]\n"
"s_waitcnt lgkmcnt(2)\n"
MF "v[16:19],a[20:23],v[176:179],v[16:19]\n"
"s_waitcnt lgkmcnt(0)\n"
TR "v[188:189],v1 offset:49152\n"
"v_add_f32 v128,v128,v192\n"
MF "v[16:19],a[60:63],v[180:183],v[16:19]\n"
TR "v[190:191],v1 offset:51200\n"
"v_add_f32 v129,v129,v193\n"
MF "v[16:19],a[100:103],v[184:187],v[16:19]\n"
"ds_read2st64_b32 v[200:201],v13 offset0:0 offset1:1\n"
"v_add_f32 v130,v130,v194\n"
MF "v[24:27],a[104:107],v[184:187],v[24:27]\n"
TR "a[240:241],v1 offset:53248\n"
"v_add_f32 v131,v131,v195\n"
MF "v[24:27],a[64:67],v[180:183],v[24:27]\n"
TR "a[242:243],v1 offset:55296\n"
"v_add_f32 v132,v132,v196\n"
MF "v[24:27],a[24:27],v[176:179],v[24:27]\n"
"ds_read2st64_b32 v[202:203],v13 offset0:2 offset1:3\n"
"v_add_f32 v133,v133,v197\n"
MF "v[32:35],a[28:31],v[176:179],v[32:35]\n"
TR "a[244:245],v1 offset:57344\n"
"v_add_f32 v134,v134,v198\n"
MF "v[32:35],a[68:71],v[180:183],v[32:35]\n"
TR "a[246:247],v1 offset:59392\n"
"v_add_f32 v135,v135,v199\n"
MF "v[32:35],a[108:111],v[184:187],v[32:35]\n"
"ds_read2st64_b32 v[204:205],v13 offset0:4 offset1:5\n"
CV "v128,v128,v129\n"
MF "v[40:43],a[112:115],v[184:187],v[40:43]\n"
"ds_read2st64_b32 v[206:207],v13 offset0:6 offset1:7\n"
CV "v129,v130,v131\n"
MF "v[40:43],a[72:75],v[180:183],v[40:43]\n"
"s_add_u32 s87,s90,4096\n"
CV "v130,v132,v133\n"
CV "v131,v134,v135\n"
MF "v[40:43],a[32:35],v[176:179],v[40:43]\n"
MF "v[48:51],a[36:39],v[176:179],v[48:51]\n"
"buffer_store_dwordx4 v[128:131],v9,s[40:43],s87 offen sc1\n"
MF "v[48:51],a[76:79],v[180:183],v[48:51]\n"
MF "v[48:51],a[116:119],v[184:187],v[48:51]\n"
"s_waitcnt lgkmcnt(2)\n"
MF "v[52:55],a[116:119],a[244:247],v[52:55]\n"
"s_waitcnt lgkmcnt(0)\n"
TR "v[176:177],v2 offset:49152\n"
"v_add_f32 v136,v136,v200\n"
MF "v[52:55],a[76:79],a[240:243],v[52:55]\n"
TR "v[178:179],v2 offset:51200\n"
"v_add_f32 v137,v137,v201\n"
MF "v[52:55],a[36:39],v[188:191],v[52:55]\n"
"ds_read2st64_b32 v[192:193],v13 offset0:8 offset1:9\n"
"v_add_f32 v138,v138,v202\n"
MF "v[44:47],a[32:35],v[188:191],v[44:47]\n"
TR "v[180:181],v2 offset:53248\n"
"v_add_f32 v139,v139,v203\n"
MF "v[44:47],a[72:75],a[240:243],v[44:47]\n"
TR "v[182:183],v2 offset:55296\n"
"v_add_f32 v140,v140,v204\n"
MF "v[44:47],a[112:115],a[244:247],v[44:47]\n"
"ds_read2st64_b32 v[194:195],v13 offset0:10 offset1:11\n"
"v_add_f32 v141,v141,v205\n"
MF "v[36:39],a[108:111],a[244:247],v[36:39]\n"
TR "v[184:185],v2 offset:57344\n"
"v_add_f32 v142,v142,v206\n"
MF "v[36:39],a[68:71],a[240:243],v[36:39]\n"
TR "v[186:187],v2 offset:59392\n"
"v_add_f32 v143,v143,v207\n"
MF "v[36:39],a[28:31],v[188:191],v[36:39]\n"
"ds_read2st64_b32 v[196:197],v13 offset0:12 offset1:13\n"
CV "v136,v136,v137\n"
MF "v[28:31],a[24:27],v[188:191],v[28:31]\n"
"ds_read2st64_b32 v[198:199],v13 offset0:14 offset1:15\n"
CV "v137,v138,v139\n"
MF "v[28:31],a[64:67],a[240:243],v[28:31]\n"
"s_add_u32 s87,s90,10240\n"
CV "v138,v140,v141\n"
CV "v139,v142,v143\n"
MF "v[28:31],a[104:107],a[244:247],v[28:31]\n"
MF "v[20:23],a[100:103],a[244:247],v[20:23]\n"
"buffer_store_dwordx4 v[136:139],v9,s[40:43],s87 offen sc1\n"
MF "v[20:23],a[60:63],a[240:243],v[20:23]\n"
"s_waitcnt vmcnt(7)\n"
MF "v[20:23],a[20:23],v[188:191],v[20:23]\n"
MF "v[56:59],a[252:255],v[208:211],v[56:59]\n"
MF "v[64:67],a[252:255],v[212:215],v[64:67]\n"
MF "v[72:75],a[252:255],v[216:219],v[72:75]\n"
MF "v[80:83],a[252:255],v[220:223],v[80:83]\n"
MF "v[88:91],a[252:255],v[224:227],v[88:91]\n"
MF "v[60:63],a[248:251],v[208:211],v[60:63]\n"
MF "v[68:71],a[248:251],v[212:215],v[68:71]\n"
MF "v[76:79],a[248:251],v[216:219],v[76:79]\n"
MF "v[84:87],a[248:251],v[220:223],v[84:87]\n"
MF "v[92:95],a[248:251],v[224:227],v[92:95]\n"
"s_waitcnt lgkmcnt(2)\n"
MF "v[56:59],a[20:23],v[176:179],v[56:59]\n"
"s_waitcnt lgkmcnt(0)\n"
TR "v[188:189],v3 offset:49152\n"
"v_add_f32 v144,v144,v192\n"
MF "v[56:59],a[60:63],v[180:183],v[56:59]\n"
TR "v[190:191],v3 offset:51200\n"
"v_add_f32 v145,v145,v193\n"
MF "v[56:59],a[100:103],v[184:187],v[56:59]\n"
"ds_read2st64_b32 v[200:201],v13 offset0:16 offset1:17\n"
"v_add_f32 v146,v146,v194\n"
MF "v[64:67],a[104:107],v[184:187],v[64:67]\n"
TR "a[240:241],v3 offset:53248\n"
"v_add_f32 v147,v147,v195\n"
MF "v[64:67],a[64:67],v[180:183],v[64:67]\n"
TR "a[242:243],v3 offset:55296\n"
"v_add_f32 v148,v148,v196\n"
MF "v[64:67],a[24:27],v[176:179],v[64:67]\n"
"ds_read2st64_b32 v[202:203],v13 offset0:18 offset1:19\n"
"v_add_f32 v149,v149,v197\n"
MF "v[72:75],a[28:31],v[176:179],v[72:75]\n"
TR "a[244:245],v3 offset:57344\n"
"v_add_f32 v150,v150,v198\n"
MF "v[72:75],a[68:71],v[180:183],v[72:75]\n"
TR "a[246:247],v3 offset:59392\n"
"v_add_f32 v151,v151,v199\n"
MF "v[72:75],a[108:111],v[184:187],v[72:75]\n"
"ds_read2st64_b32 v[204:205],v13 offset0:20 offset1:21\n"
CV "v144,v144,v145\n"
MF "v[80:83],a[112:115],v[184:187],v[80:83]\n"
"ds_read2st64_b32 v[206:207],v13 offset0:22 offset1:23\n"
CV "v145,v146,v147\n"
MF "v[80:83],a[72:75],v[180:183],v[80:83]\n"
"s_add_u32 s87,s90,11264\n"
CV "v146,v148,v149\n"
CV "v147,v150,v151\n"
MF "v[80:83],a[32:35],v[176:179],v[80:83]\n"
"v_sub_u32 v10,v10,v11\n"
MF "v[88:91],a[36:39],v[176:179],v[88:91]\n"
"buffer_store_dwordx4 v[144:147],v9,s[40:43],s87 offen sc1\n"
MF "v[88:91],a[76:79],v[180:183],v[88:91]\n"
".Lnf6:\n"
"v_readlane_b32 s58,v10,s67\n"
"s_cmp_lt_u32 s58,s85\n"
"s_cbranch_scc1 .Lsp6\n"
".Lnb6:\n"
MF "v[88:91],a[116:119],v[184:187],v[88:91]\n"
"s_waitcnt lgkmcnt(0)\n"
"s_barrier\n"
MF "v[92:95],a[116:119],a[244:247],v[92:95]\n"
TR "v[176:177],v4\n"
"v_add_f32 v152,v152,v200\n"
MF "v[92:95],a[76:79],a[240:243],v[92:95]\n"
"s_add_u32 s70,s70,20480\n"
TR "v[178:179],v4 offset:2048\n"
"v_add_f32 v153,v153,v201\n"
MF "v[92:95],a[36:39],v[188:191],v[92:95]\n"
"ds_read2st64_b32 v[192:193],v13 offset0:24 offset1:25\n"
"v_add_f32 v154,v154,v202\n"
MF "v[84:87],a[32:35],v[188:191],v[84:87]\n"
TR "v[180:181],v4 offset:4096\n"
"v_add_f32 v155,v155,v203\n"
MF "v[84:87],a[72:75],a[240:243],v[84:87]\n"
TR "v[182:183],v4 offset:6144\n"
"v_add_f32 v156,v156,v204\n"
MF "v[84:87],a[112:115],a[244:247],v[84:87]\n"
"ds_read2st64_b32 v[194:195],v13 offset0:26 offset1:27\n"
"v_add_f32 v157,v157,v205\n"
MF "v[76:79],a[108:111],a[244:247],v[76:79]\n"
TR "v[184:185],v4 offset:8192\n"
"v_add_f32 v158,v158,v206\n"
MF "v[76:79],a[68:71],a[240:243],v[76:79]\n"
TR "v[186:187],v4 offset:10240\n"
"v_add_f32 v159,v159,v207\n"
MF "v[76:79],a[28:31],v[188:191],v[76:79]\n"
"ds_read2st64_b32 v[196:197],v13 offset0:28 offset1:29\n"
CV "v152,v152,v153\n"
MF "v[68:71],a[24:27],v[188:191],v[68:71]\n"
"ds_read2st64_b32 v[198:199],v13 offset0:30 offset1:31\n"
CV "v153,v154,v155\n"
MF "v[68:71],a[64:67],a[240:243],v[68:71]\n"
"s_add_u32 s87,s90,12288\n"
CV "v154,v156,v157\n"
CV "v155,v158,v159\n"
MF "v[68:71],a[104:107],a[244:247],v[68:71]\n"
MF "v[60:63],a[100:103],a[244:247],v[60:63]\n"
"buffer_store_dwordx4 v[152:155],v9,s[40:43],s87 offen sc1\n"
MF "v[60:63],a[60:63],a[240:243],v[60:63]\n"
LD "v[208:211],v9,s[40:43],s82 offen sc1\n"
MF "v[60:63],a[20:23],v[188:191],v[60:63]\n"
"s_waitcnt lgkmcnt(2)\n"
MF "v[16:19],a[140:143],v[176:179],v[16:19]\n"
"s_waitcnt lgkmcnt(0)\n"
TR "v[188:189],v5\n"
"v_add_f32 v160,v160,v192\n"
MF "v[16:19],a[180:183],v[180:183],v[16:19]\n"
TR "v[190:191],v5 offset:2048\n"
"v_add_f32 v161,v161,v193\n"
MF "v[16:19],a[220:223],v[184:187],v[16:19]\n"
"ds_read2st64_b32 v[200:201],v13 offset0:32 offset1:33\n"
"v_add_f32 v162,v162,v194\n"
MF "v[24:27],a[224:227],v[184:187],v[24:27]\n"
TR "a[240:241],v5 offset:4096\n"
"v_add_f32 v163,v163,v195\n"
MF "v[24:27],a[184:187],v[180:183],v[24:27]\n"
TR "a[242:243],v5 offset:6144\n"
"v_add_f32 v164,v164,v196\n"
MF "v[24:27],a[144:147],v[176:179],v[24:27]\n"
"ds_read2st64_b32 v[202:203],v13 offset0:34 offset1:35\n"
"v_add_f32 v165,v165,v197\n"
MF "v[32:35],a[148:151],v[176:179],v[32:35]\n"
TR "a[244:245],v5 offset:8192\n"
"v_add_f32 v166,v166,v198\n"
MF "v[32:35],a[188:191],v[180:183],v[32:35]\n"
TR "a[246:247],v5 offset:10240\n"
"v_add_f32 v167,v167,v199\n"
MF "v[32:35],a[228:231],v[184:187],v[32:35]\n"
"ds_read2st64_b32 v[204:205],v13 offset0:36 offset1:37\n"
CV "v160,v160,v161\n"
MF "v[40:43],a[232:235],v[184:187],v[40:43]\n"
"ds_read2st64_b32 v[206:207],v13 offset0:38 offset1:39\n"
CV "v161,v162,v163\n"
MF "v[40:43],a[192:195],v[180:183],v[40:43]\n"
"s_add_u32 s87,s90,13312\n"
CV "v162,v164,v165\n"
CV "v163,v166,v167\n"
MF "v[40:43],a[152:155],v[176:179],v[40:43]\n"
MF "v[48:51],a[156:159],v[176:179],v[48:51]\n"
"buffer_store_dwordx4 v[160:163],v9,s[40:43],s87 offen sc1\n"
MF "v[48:51],a[196:199],v[180:183],v[48:51]\n"
LD "v[212:215],v9,s[40:43],s82 offen offset:1024 sc1\n"
MF "v[48:51],a[236:239],v[184:187],v[48:51]\n"
"s_waitcnt lgkmcnt(2)\n"
MF "v[52:55],a[236:239],a[244:247],v[52:55]\n"
"s_waitcnt lgkmcnt(0)\n"
TR "v[176:177],v6\n"
"v_add_f32 v168,v168,v200\n"
MF "v[52:55],a[196:199],a[240:243],v[52:55]\n"
TR "v[178:179],v6 offset:2048\n"
"v_add_f32 v169,v169,v201\n"
MF "v[52:55],a[156:159],v[188:191],v[52:55]\n"
"v_add_f32 v170,v170,v202\n"
"v_add_f32 v171,v171,v203\n"
MF "v[44:47],a[152:155],v[188:191],v[44:47]\n"
TR "v[180:181],v6 offset:4096\n"
"v_add_f32 v172,v172,v204\n"
MF "v[44:47],a[192:195],a[240:243],v[44:47]\n"
TR "v[182:183],v6 offset:6144\n"
"v_add_f32 v173,v173,v205\n"
MF "v[44:47],a[232:235],a[244:247],v[44:47]\n"
"v_add_f32 v174,v174,v206\n"
"v_add_f32 v175,v175,v207\n"
MF "v[36:39],a[228:231],a[244:247],v[36:39]\n"
TR "v[184:185],v6 offset:8192\n"
CV "v168,v168,v169\n"
MF "v[36:39],a[188:191],a[240:243],v[36:39]\n"
TR "v[186:187],v6 offset:10240\n"
CV "v169,v170,v171\n"
MF "v[36:39],a[148:151],v[188:191],v[36:39]\n"
"s_add_u32 s87,s90,14336\n"
CV "v170,v172,v173\n"
CV "v171,v174,v175\n"
MF "v[28:31],a[144:147],v[188:191],v[28:31]\n"
MF "v[28:31],a[184:187],a[240:243],v[28:31]\n"
MF "v[28:31],a[224:227],a[244:247],v[28:31]\n"
MF "v[20:23],a[220:223],a[244:247],v[20:23]\n"
"buffer_store_dwordx4 v[168:171],v9,s[40:43],s87 offen sc1\n"
MF "v[20:23],a[180:183],a[240:243],v[20:23]\n"
"s_mov_b32 s90,s91\n"
"s_add_u32 s91,s91,20480\n"
MF "v[20:23],a[140:143],v[188:191],v[20:23]\n"
"s_waitcnt lgkmcnt(0)\n"
LD "v[216:219],v9,s[40:43],s82 offen offset:2048 sc1\n"
MF "v[56:59],a[140:143],v[176:179],v[56:59]\n"
TR "v[188:189],v7\n"
MF "v[56:59],a[180:183],v[180:183],v[56:59]\n"
TR "v[190:191],v7 offset:2048\n"
MF "v[56:59],a[220:223],v[184:187],v[56:59]\n"
MF "v[64:67],a[224:227],v[184:187],v[64:67]\n"
TR "a[240:241],v7 offset:4096\n"
MF "v[64:67],a[184:187],v[180:183],v[64:67]\n"
TR "a[242:243],v7 offset:6144\n"
MF "v[64:67],a[144:147],v[176:179],v[64:67]\n"
MF "v[72:75],a[148:151],v[176:179],v[72:75]\n"
TR "a[244:245],v7 offset:8192\n"
MF "v[72:75],a[188:191],v[180:183],v[72:75]\n"
TR "a[246:247],v7 offset:10240\n"
MF "v[72:75],a[228:231],v[184:187],v[72:75]\n"
MF "v[80:83],a[232:235],v[184:187],v[80:83]\n"
MF "v[80:83],a[192:195],v[180:183],v[80:83]\n"
MF "v[80:83],a[152:155],v[176:179],v[80:83]\n"
MF "v[88:91],a[156:159],v[176:179],v[88:91]\n"
MF "v[88:91],a[196:199],v[180:183],v[88:91]\n"
LD "v[220:223],v9,s[40:43],s82 offen offset:3072 sc1\n"
MF "v[88:91],a[236:239],v[184:187],v[88:91]\n"
"s_waitcnt lgkmcnt(0)\n"
"s_barrier\n"
MF "v[92:95],a[236:239],a[244:247],v[92:95]\n"
TR "v[176:177],v0 offset:24576\n"
MF "v[92:95],a[196:199],a[240:243],v[92:95]\n"
"s_add_u32 s85,s85,1\n"
TR "v[178:179],v0 offset:26624\n"
MF "v[92:95],a[156:159],v[188:191],v[92:95]\n"
MF "v[84:87],a[152:155],v[188:191],v[84:87]\n"
TR "v[180:181],v0 offset:28672\n"
MF "v[84:87],a[192:195],a[240:243],v[84:87]\n"
TR "v[182:183],v0 offset:30720\n"
MF "v[84:87],a[232:235],a[244:247],v[84:87]\n"
MF "v[76:79],a[228:231],a[244:247],v[76:79]\n"
TR "v[184:185],v0 offset:32768\n"
MF "v[76:79],a[188:191],a[240:243],v[76:79]\n"
TR "v[186:187],v0 offset:34816\n"
MF "v[76:79],a[148:151],v[188:191],v[76:79]\n"
MF "v[68:71],a[144:147],v[188:191],v[68:71]\n"
MF "v[68:71],a[184:187],a[240:243],v[68:71]\n"
MF "v[68:71],a[224:227],a[244:247],v[68:71]\n"
MF "v[60:63],a[220:223],a[244:247],v[60:63]\n"
MF "v[60:63],a[180:183],a[240:243],v[60:63]\n"
MF "v[60:63],a[140:143],v[188:191],v[60:63]\n"
"s_waitcnt lgkmcnt(0)\n"
"s_cmp_lg_u32 s85,9\n"
"s_cbranch_scc1 .Lpl19\n"
".Lplast1:\n"
MF "v[96:99],v[176:179],a[0:3],v[252:255]\n"
"s_add_u32 m0,s79,73728\n"
TR "v[188:189],v1 offset:24576\n"
TR "v[190:191],v1 offset:26624\n"
MF "v[96:99],v[180:183],a[40:43],v[96:99]\n"
"s_nop 0\n"
TR "a[240:241],v1 offset:28672\n"
TR "a[242:243],v1 offset:30720\n"
MF "v[96:99],v[184:187],a[80:83],v[96:99]\n"
"s_add_u32 s82,s82,4096\n"
TR "a[244:245],v1 offset:32768\n"
TR "a[246:247],v1 offset:34816\n"
MF "v[104:107],v[184:187],a[84:87],v[252:255]\n"
LD "v[224:227],v9,s[40:43],s82 offen sc1\n"
MF "v[104:107],v[180:183],a[44:47],v[104:107]\n"
"s_nop 0\n"
"ds_write_addtid_b32 v56\n"
"ds_write_addtid_b32 v57 offset:256\n"
MF "v[104:107],v[176:179],a[4:7],v[104:107]\n"
"s_nop 0\n"
"ds_write_addtid_b32 v58 offset:512\n"
"ds_write_addtid_b32 v59 offset:768\n"
MF "v[112:115],v[176:179],a[8:11],v[252:255]\n"
"s_nop 0\n"
"ds_write_addtid_b32 v60 offset:1024\n"
"ds_write_addtid_b32 v61 offset:1280\n"
MF "v[112:115],v[180:183],a[48:51],v[112:115]\n"
"s_nop 0\n"
"ds_write_addtid_b32 v62 offset:1536\n"
"ds_write_addtid_b32 v63 offset:1792\n"
MF "v[112:115],v[184:187],a[88:91],v[112:115]\n"
"s_nop 0\n"
"ds_write_addtid_b32 v64 offset:2048\n"
"ds_write_addtid_b32 v65 offset:2304\n"
MF "v[120:123],v[184:187],a[92:95],v[252:255]\n"
"s_nop 0\n"
"ds_write_addtid_b32 v66 offset:2560\n"
"ds_write_addtid_b32 v67 offset:2816\n"
MF "v[120:123],v[180:183],a[52:55],v[120:123]\n"
"s_nop 0\n"
"ds_write_addtid_b32 v68 offset:3072\n"
"ds_write_addtid_b32 v69 offset:3328\n"
MF "v[120:123],v[176:179],a[12:15],v[120:123]\n"
"s_nop 0\n"
"ds_write_addtid_b32 v70 offset:3584\n"
"ds_write_addtid_b32 v71 offset:3840\n"
MF "v[128:131],v[176:179],a[16:19],v[252:255]\n"
"s_nop 0\n"
"ds_write_addtid_b32 v72 offset:4096\n"
"ds_write_addtid_b32 v73 offset:4352\n"
MF "v[128:131],v[180:183],a[56:59],v[128:131]\n"
"s_nop 0\n"
"ds_write_addtid_b32 v74 offset:4608\n"
"ds_write_addtid_b32 v75 offset:4864\n"
MF "v[128:131],v[184:187],a[96:99],v[128:131]\n"
"s_waitcnt lgkmcnt(15)\n"
"ds_write_addtid_b32 v76 offset:5120\n"
MF "v[132:135],a[244:247],a[96:99],v[252:255]\n"
"s_nop 0\n"
TR "v[176:177],v2 offset:24576\n"
TR "v[178:179],v2 offset:26624\n"
MF "v[132:135],a[240:243],a[56:59],v[132:135]\n"
"s_nop 0\n"
TR "v[180:181],v2 offset:28672\n"
TR "v[182:183],v2 offset:30720\n"
MF "v[132:135],v[188:191],a[16:19],v[132:135]\n"
"s_nop 0\n"
TR "v[184:185],v2 offset:32768\n"
TR "v[186:187],v2 offset:34816\n"
MF "v[124:127],v[188:191],a[12:15],v[252:255]\n"
"s_nop 0\n"
"ds_write_addtid_b32 v77 offset:5376\n"
"ds_write_addtid_b32 v78 offset:5632\n"
MF "v[124:127],a[240:243],a[52:55],v[124:127]\n"
"s_nop 0\n"
"ds_write_addtid_b32 v79 offset:5888\n"
"ds_write_addtid_b32 v80 offset:6144\n"
MF "v[124:127],a[244:247],a[92:95],v[124:127]\n"
"s_nop 0\n"
"ds_write_addtid_b32 v81 offset:6400\n"
"ds_write_addtid_b32 v82 offset:6656\n"
MF "v[116:119],a[244:247],a[88:91],v[252:255]\n"
"s_nop 0\n"
"ds_write_addtid_b32 v83 offset:6912\n"
"ds_write_addtid_b32 v84 offset:7168\n"
MF "v[116:119],a[240:243],a[48:51],v[116:119]\n"
"s_nop 0\n"
"ds_write_addtid_b32 v85 offset:7424\n"
"ds_write_addtid_b32 v86 offset:7680\n"
MF "v[116:119],v[188:191],a[8:11],v[116:119]\n"
"s_nop 0\n"
"ds_write_addtid_b32 v87 offset:7936\n"
"ds_write_addtid_b32 v88 offset:8192\n"
MF "v[108:111],v[188:191],a[4:7],v[252:255]\n"
"s_nop 0\n"
"ds_write_addtid_b32 v89 offset:8448\n"
"ds_write_addtid_b32 v90 offset:8704\n"
MF "v[108:111],a[240:243],a[44:47],v[108:111]\n"
"s_nop 0\n"
"ds_write_addtid_b32 v91 offset:8960\n"
"ds_write_addtid_b32 v92 offset:9216\n"
MF "v[108:111],a[244:247],a[84:87],v[108:111]\n"
"s_nop 0\n"
"ds_write_addtid_b32 v93 offset:9472\n"
"ds_write_addtid_b32 v94 offset:9728\n"
MF "v[100:103],a[244:247],a[80:83],v[252:255]\n"
"ds_write_addtid_b32 v95 offset:9984\n"
MF "v[100:103],a[240:243],a[40:43],v[100:103]\n"
MF "v[100:103],v[188:191],a[0:3],v[100:103]\n"
"s_waitcnt lgkmcnt(15)\n"
MF "v[136:139],v[176:179],a[0:3],v[252:255]\n"
"s_nop 0\n"
TR "v[188:189],v3 offset:24576\n"
TR "v[190:191],v3 offset:26624\n"
MF "v[136:139],v[180:183],a[40:43],v[136:139]\n"
"s_nop 0\n"
TR "a[240:241],v3 offset:28672\n"
TR "a[242:243],v3 offset:30720\n"
MF "v[136:139],v[184:187],a[80:83],v[136:139]\n"
"s_nop 0\n"
TR "a[244:245],v3 offset:32768\n"
TR "a[246:247],v3 offset:34816\n"
MF "v[144:147],v[184:187],a[84:87],v[252:255]\n"
"s_waitcnt lgkmcnt(0)\n"
"s_barrier\n"
MF "v[144:147],v[180:183],a[44:47],v[144:147]\n"
"ds_read2st64_b32 v[56:57],v13 offset0:192 offset1:193\n"
MF "v[144:147],v[176:179],a[4:7],v[144:147]\n"
"ds_read2st64_b32 v[58:59],v13 offset0:194 offset1:195\n"
MF "v[152:155],v[176:179],a[8:11],v[252:255]\n"
"ds_read2st64_b32 v[60:61],v13 offset0:196 offset1:197\n"
MF "v[152:155],v[180:183],a[48:51],v[152:155]\n"
"ds_read2st64_b32 v[62:63],v13 offset0:198 offset1:199\n"
MF "v[152:155],v[184:187],a[88:91],v[152:155]\n"
"ds_read2st64_b32 v[64:65],v13 offset0:200 offset1:201\n"
MF "v[160:163],v[184:187],a[92:95],v[252:255]\n"
"ds_read2st64_b32 v[66:67],v13 offset0:202 offset1:203\n"
MF "v[160:163],v[180:183],a[52:55],v[160:163]\n"
"ds_read2st64_b32 v[68:69],v13 offset0:204 offset1:205\n"
MF "v[160:163],v[176:179],a[12:15],v[160:163]\n"
".Lpg34h:\n"
"s_cbranch_execz .Lisr4\n"
"ds_read2st64_b32 v[70:71],v13 offset0:206 offset1:207\n"
MF "v[168:171],v[176:179],a[16:19],v[252:255]\n"
"s_waitcnt lgkmcnt(4)\n"
"ds_read2st64_b32 v[72:73],v13 offset0:208 offset1:209\n"
"v_add_f32 v16,v16,v56\n"
MF "v[168:171],v[180:183],a[56:59],v[168:171]\n"
"ds_read2st64_b32 v[74:75],v13 offset0:210 offset1:211\n"
"v_add_f32 v17,v17,v57\n"
MF "v[168:171],v[184:187],a[96:99],v[168:171]\n"
"s_waitcnt lgkmcnt(0)\n"
"s_barrier\n"
MF "v[172:175],a[244:247],a[96:99],v[252:255]\n"
"s_nop 0\n"
TR "v[176:177],v0\n"
TR "v[178:179],v0 offset:2048\n"
MF "v[172:175],a[240:243],a[56:59],v[172:175]\n"
"s_nop 0\n"
TR "v[180:181],v0 offset:4096\n"
TR "v[182:183],v0 offset:6144\n"
MF "v[172:175],v[188:191],a[16:19],v[172:175]\n"
"s_nop 0\n"
TR "v[184:185],v0 offset:8192\n"
TR "v[186:187],v0 offset:10240\n"
MF "v[164:167],v[188:191],a[12:15],v[252:255]\n"
"ds_read2st64_b32 v[76:77],v13 offset0:212 offset1:213\n"
"v_add_f32 v18,v18,v58\n"
MF "v[164:167],a[240:243],a[52:55],v[164:167]\n"
"ds_read2st64_b32 v[78:79],v13 offset0:214 offset1:215\n"
"v_add_f32 v19,v19,v59\n"
MF "v[164:167],a[244:247],a[92:95],v[164:167]\n"
"ds_read2st64_b32 v[80:81],v13 offset0:216 offset1:217\n"
"v_add_f32 v20,v20,v60\n"
MF "v[156:159],a[244:247],a[88:91],v[252:255]\n"
"ds_read2st64_b32 v[82:83],v13 offset0:218 offset1:219\n"
"v_add_f32 v21,v21,v61\n"
MF "v[156:159],a[240:243],a[48:51],v[156:159]\n"
"ds_read2st64_b32 v[84:85],v13 offset0:220 offset1:221\n"
"v_add_f32 v22,v22,v62\n"
MF "v[156:159],v[188:191],a[8:11],v[156:159]\n"
"ds_read2st64_b32 v[86:87],v13 offset0:222 offset1:223\n"
"v_add_f32 v23,v23,v63\n"
MF "v[148:151],v[188:191],a[4:7],v[252:255]\n"
"s_waitcnt vmcnt(3)\n"
"v_mov_b32 v14,s76\n"
"s_add_u32 s76,s76,1\n"
MF "v[148:151],a[240:243],a[44:47],v[148:151]\n"
"buffer_store_dword v14,v15,s[40:43],s94 offen sc1\n"
MF "v[148:151],a[244:247],a[84:87],v[148:151]\n"
"ds_read2st64_b32 v[88:89],v13 offset0:224 offset1:225\n"
CV "v16,v16,v17\n"
MF "v[140:143],a[244:247],a[80:83],v[252:255]\n"
"ds_read2st64_b32 v[90:91],v13 offset0:226 offset1:227\n"
CV "v17,v18,v19\n"
MF "v[140:143],a[240:243],a[40:43],v[140:143]\n"
"ds_read2st64_b32 v[92:93],v13 offset0:228 offset1:229\n"
CV "v18,v20,v21\n"
MF "v[140:143],v[188:191],a[0:3],v[140:143]\n"
"s_waitcnt lgkmcnt(9)\n"
"ds_read2st64_b32 v[94:95],v13 offset0:230 offset1:231\n"
CV "v19,v22,v23\n"
MF "v[96:99],v[176:179],a[120:123],v[96:99]\n"
"s_nop 0\n"
TR "v[188:189],v1\n"
TR "v[190:191],v1 offset:2048\n"
MF "v[96:99],v[180:183],a[160:163],v[96:99]\n"
"s_nop 0\n"
TR "a[240:241],v1 offset:4096\n"
TR "a[242:243],v1 offset:6144\n"
MF "v[96:99],v[184:187],a[200:203],v[96:99]\n"
"s_add_u32 s87,s90,0\n"
TR "a[244:245],v1 offset:8192\n"
TR "a[246:247],v1 offset:10240\n"
MF "v[104:107],v[184:187],a[204:207],v[104:107]\n"
"buffer_store_dwordx4 v[16:19],v9,s[40:43],s87 offen sc1\n"
MF "v[104:107],v[180:183],a[164:167],v[104:107]\n"
"v_add_f32 v24,v24,v64\n"
"v_add_f32 v25,v25,v65\n"
MF "v[104:107],v[176:179],a[124:127],v[104:107]\n"
"v_add_f32 v26,v26,v66\n"
"v_add_f32 v27,v27,v67\n"
MF "v[112:115],v[176:179],a[128:131],v[112:115]\n"
"v_add_f32 v28,v28,v68\n"
"v_add_f32 v29,v29,v69\n"
MF "v[112:115],v[180:183],a[168:171],v[112:115]\n"
"v_add_f32 v30,v30,v70\n"
"v_add_f32 v31,v31,v71\n"
MF "v[112:115],v[184:187],a[208:211],v[112:115]\n"
CV "v24,v24,v25\n"
CV "v25,v26,v27\n"
MF "v[120:123],v[184:187],a[212:215],v[120:123]\n"
"s_add_u32 s87,s90,1024\n"
CV "v26,v28,v29\n"
CV "v27,v30,v31\n"
MF "v[120:123],v[180:183],a[172:175],v[120:123]\n"
"s_waitcnt lgkmcnt(14)\n"
"buffer_store_dwordx4 v[24:27],v9,s[40:43],s87 offen sc1\n"
MF "v[120:123],v[176:179],a[132:135],v[120:123]\n"
"v_add_f32 v32,v32,v72\n"
"v_add_f32 v33,v33,v73\n"
MF "v[128:131],v[176:179],a[136:139],v[128:131]\n"
"v_add_f32 v34,v34,v74\n"
"v_add_f32 v35,v35,v75\n"
MF "v[128:131],v[180:183],a[176:179],v[128:131]\n"
"v_add_f32 v36,v36,v76\n"
"v_add_f32 v37,v37,v77\n"
MF "v[128:131],v[184:187],a[216:219],v[128:131]\n"
"s_waitcnt lgkmcnt(0)\n"
"v_add_f32 v38,v38,v78\n"
"v_add_f32 v39,v39,v79\n"
MF "v[132:135],a[244:247],a[216:219],v[132:135]\n"
"s_nop 0\n"
TR "v[176:177],v2\n"
TR "v[178:179],v2 offset:2048\n"
MF "v[132:135],a[240:243],a[176:179],v[132:135]\n"
"s_nop 0\n"
TR "v[180:181],v2 offset:4096\n"
TR "v[182:183],v2 offset:6144\n"
MF "v[132:135],v[188:191],a[136:139],v[132:135]\n"
"s_nop 0\n"
TR "v[184:185],v2 offset:8192\n"
TR "v[186:187],v2 offset:10240\n"
MF "v[124:127],v[188:191],a[132:135],v[124:127]\n"
CV "v32,v32,v33\n"
CV "v33,v34,v35\n"
MF "v[124:127],a[240:243],a[172:175],v[124:127]\n"
"s_add_u32 s87,s90,2048\n"
CV "v34,v36,v37\n"
CV "v35,v38,v39\n"
MF "v[124:127],a[244:247],a[212:215],v[124:127]\n"
"buffer_store_dwordx4 v[32:35],v9,s[40:43],s87 offen sc1\n"
MF "v[116:119],a[244:247],a[208:211],v[116:119]\n"
"v_add_f32 v40,v40,v80\n"
"v_add_f32 v41,v41,v81\n"
MF "v[116:119],a[240:243],a[168:171],v[116:119]\n"
"v_add_f32 v42,v42,v82\n"
"v_add_f32 v43,v43,v83\n"
MF "v[116:119],v[188:191],a[128:131],v[116:119]\n"
"v_add_f32 v44,v44,v84\n"
"v_add_f32 v45,v45,v85\n"
MF "v[108:111],v[188:191],a[124:127],v[108:111]\n"
"v_add_f32 v46,v46,v86\n"
"v_add_f32 v47,v47,v87\n"
MF "v[108:111],a[240:243],a[164:167],v[108:111]\n"
CV "v40,v40,v41\n"
CV "v41,v42,v43\n"
MF "v[108:111],a[244:247],a[204:207],v[108:111]\n"
"s_add_u32 s87,s90,3072\n"
CV "v42,v44,v45\n"
CV "v43,v46,v47\n"
MF "v[100:103],a[244:247],a[200:203],v[100:103]\n"
"buffer_store_dwordx4 v[40:43],v9,s[40:43],s87 offen sc1\n"
MF "v[100:103],a[240:243],a[160:163],v[100:103]\n"
"v_add_f32 v48,v48,v88\n"
"v_add_f32 v49,v49,v89\n"
MF "v[100:103],v[188:191],a[120:123],v[100:103]\n"
"s_waitcnt lgkmcnt(0)\n"
"v_add_f32 v50,v50,v90\n"
"v_add_f32 v51,v51,v91\n"
MF "v[136:139],v[176:179],a[120:123],v[136:139]\n"
"s_nop 0\n"
TR "v[188:189],v3\n"
TR "v[190:191],v3 offset:2048\n"
MF "v[136:139],v[180:183],a[160:163],v[136:139]\n"
"s_nop 0\n"
TR "a[240:241],v3 offset:4096\n"
TR "a[242:243],v3 offset:6144\n"
MF "v[136:139],v[184:187],a[200:203],v[136:139]\n"
"s_nop 0\n"
TR "a[244:245],v3 offset:8192\n"
TR "a[246:247],v3 offset:10240\n"
MF "v[144:147],v[184:187],a[204:207],v[144:147]\n"
"v_add_f32 v52,v52,v92\n"
"v_add_f32 v53,v53,v93\n"
MF "v[144:147],v[180:183],a[164:167],v[144:147]\n"
"v_add_f32 v54,v54,v94\n"
"v_add_f32 v55,v55,v95\n"
MF "v[144:147],v[176:179],a[124:127],v[144:147]\n"
CV "v48,v48,v49\n"
CV "v49,v50,v51\n"
MF "v[152:155],v[176:179],a[128:131],v[152:155]\n"
"s_add_u32 s87,s90,4096\n"
CV "v50,v52,v53\n"
CV "v51,v54,v55\n"
MF "v[152:155],v[180:183],a[168:171],v[152:155]\n"
"buffer_store_dwordx4 v[48:51],v9,s[40:43],s87 offen sc1\n"
MF "v[152:155],v[184:187],a[208:211],v[152:155]\n"
"v_cmp_gt_u32 vcc,4,v10\n"
MF "v[160:163],v[184:187],a[212:215],v[160:163]\n"
"s_cmp_eq_u64 vcc,0\n"
"s_cselect_b32 s2,s2,0xf0000000\n"
MF "v[160:163],v[180:183],a[172:175],v[160:163]\n"
"s_cmp_eq_u64 vcc,0\n"
"s_cselect_b32 s3,s3,0xf0000000\n"
MF "v[160:163],v[176:179],a[132:135],v[160:163]\n"
"s_cmp_eq_u64 vcc,0\n"
"s_cselect_b32 s0,s0,0xf0000000\n"
MF "v[168:171],v[176:179],a[136:139],v[168:171]\n"
"s_cmp_eq_u64 vcc,0\n"
"s_cselect_b32 s1,s1,0xf0000000\n"
MF "v[168:171],v[180:183],a[176:179],v[168:171]\n"
"s_cmp_eq_u64 vcc,0\n"
"s_cselect_b32 s92,64,s92\n"
MF "v[168:171],v[184:187],a[216:219],v[168:171]\n"
"s_waitcnt lgkmcnt(0)\n"
"s_barrier\n"
MF "v[172:175],a[244:247],a[216:219],v[172:175]\n"
"s_nop 0\n"
TR "v[176:177],v0 offset:49152\n"
TR "v[178:179],v0 offset:51200\n"
MF "v[172:175],a[240:243],a[176:179],v[172:175]\n"
"s_nop 0\n"
TR "v[180:181],v0 offset:53248\n"
TR "v[182:183],v0 offset:55296\n"
MF "v[172:175],v[188:191],a[136:139],v[172:175]\n"
"s_nop 0\n"
TR "v[184:185],v0 offset:57344\n"
TR "v[186:187],v0 offset:59392\n"
MF "v[164:167],v[188:191],a[132:135],v[164:167]\n"
"buffer_load_dword v10,v12,s[40:43],s86 offen sc1\n"
MF "v[164:167],a[240:243],a[172:175],v[164:167]\n"
MF "v[164:167],a[244:247],a[212:215],v[164:167]\n"
MF "v[156:159],a[244:247],a[208:211],v[156:159]\n"
MF "v[156:159],a[240:243],a[168:171],v[156:159]\n"
MF "v[156:159],v[188:191],a[128:131],v[156:159]\n"
MF "v[148:151],v[188:191],a[124:127],v[148:151]\n"
MF "v[148:151],a[240:243],a[164:167],v[148:151]\n"
MF "v[148:151],a[244:247],a[204:207],v[148:151]\n"
MF "v[140:143],a[244:247],a[200:203],v[140:143]\n"
MF "v[140:143],a[240:243],a[160:163],v[140:143]\n"
MF "v[140:143],v[188:191],a[120:123],v[140:143]\n"
"s_waitcnt lgkmcnt(0)\n"
MF "v[96:99],v[176:179],a[20:23],v[96:99]\n"
"s_nop 0\n"
TR "v[188:189],v1 offset:49152\n"
TR "v[190:191],v1 offset:51200\n"
MF "v[96:99],v[180:183],a[60:63],v[96:99]\n"
"s_nop 0\n"
TR "a[240:241],v1 offset:53248\n"
TR "a[242:243],v1 offset:55296\n"
MF "v[96:99],v[184:187],a[100:103],v[96:99]\n"
"s_nop 0\n"
TR "a[244:245],v1 offset:57344\n"
TR "a[246:247],v1 offset:59392\n"
MF "v[104:107],v[184:187],a[104:107],v[104:107]\n"
MF "v[104:107],v[180:183],a[64:67],v[104:107]\n"
MF "v[104:107],v[176:179],a[24:27],v[104:107]\n"
"s_waitcnt vmcnt(7)\n"
MF "v[136:139],v[208:211],a[252:255],v[136:139]\n"
MF "v[144:147],v[212:215],a[252:255],v[144:147]\n"
MF "v[152:155],v[216:219],a[252:255],v[152:155]\n"
MF "v[160:163],v[220:223],a[252:255],v[160:163]\n"
MF "v[168:171],v[224:227],a[252:255],v[168:171]\n"
MF "v[140:143],v[208:211],a[248:251],v[140:143]\n"
MF "v[148:151],v[212:215],a[248:251],v[148:151]\n"
MF "v[156:159],v[216:219],a[248:251],v[156:159]\n"
MF "v[164:167],v[220:223],a[248:251],v[164:167]\n"
MF "v[172:175],v[224:227],a[248:251],v[172:175]\n"
MF "v[112:115],v[176:179],a[28:31],v[112:115]\n"
MF "v[112:115],v[180:183],a[68:71],v[112:115]\n"
MF "v[112:115],v[184:187],a[108:111],v[112:115]\n"
MF "v[120:123],v[184:187],a[112:115],v[120:123]\n"
MF "v[120:123],v[180:183],a[72:75],v[120:123]\n"
"s_bfe_u32 s87,s22,0x30014\n"
MF "v[120:123],v[176:179],a[32:35],v[120:123]\n"
"s_lshl_b32 s87,s87,7\n"
MF "v[128:131],v[176:179],a[36:39],v[128:131]\n"
"s_and_b32 s60,s56,1\n"
MF "v[128:131],v[180:183],a[76:79],v[128:131]\n"
"s_mul_i32 s60,s60,204800\n"
MF "v[128:131],v[184:187],a[116:119],v[128:131]\n"
"s_waitcnt lgkmcnt(0)\n"
MF "v[132:135],a[244:247],a[116:119],v[132:135]\n"
"s_add_u32 s87,s87,s60\n"
TR "v[176:177],v2 offset:49152\n"
TR "v[178:179],v2 offset:51200\n"
MF "v[132:135],a[240:243],a[76:79],v[132:135]\n"
"s_mov_b32 s71,204800\n"
TR "v[180:181],v2 offset:53248\n"
TR "v[182:183],v2 offset:55296\n"
MF "v[132:135],v[188:191],a[36:39],v[132:135]\n"
"s_mov_b32 s66,204800\n"
TR "v[184:185],v2 offset:57344\n"
TR "v[186:187],v2 offset:59392\n"
MF "v[124:127],v[188:191],a[32:35],v[124:127]\n"
LD "a[0:3],v9,s[40:43],s2 offen sc1\n"
MF "v[124:127],a[240:243],a[72:75],v[124:127]\n"
"s_add_u32 s2,s2,204800\n"
MF "v[124:127],a[244:247],a[112:115],v[124:127]\n"
LD "a[4:7],v9,s[40:43],s2 offen sc1\n"
MF "v[116:119],a[244:247],a[108:111],v[116:119]\n"
"s_add_u32 s72,s78,2\n"
MF "v[116:119],a[240:243],a[68:71],v[116:119]\n"
"s_add_u32 s2,s2,204800\n"
MF "v[116:119],v[188:191],a[28:31],v[116:119]\n"
LD "a[8:11],v9,s[40:43],s2 offen sc1\n"
MF "v[108:111],v[188:191],a[24:27],v[108:111]\n"
"s_add_u32 s16,s78,6\n"
MF "v[108:111],a[240:243],a[64:67],v[108:111]\n"
"s_add_u32 s2,s2,204800\n"
MF "v[108:111],a[244:247],a[104:107],v[108:111]\n"
LD "a[12:15],v9,s[40:43],s2 offen sc1\n"
MF "v[100:103],a[244:247],a[100:103],v[100:103]\n"
"s_and_b32 s16,s16,15\n"
MF "v[100:103],a[240:243],a[60:63],v[100:103]\n"
"s_add_u32 s2,s2,204800\n"
MF "v[100:103],v[188:191],a[20:23],v[100:103]\n"
"s_waitcnt lgkmcnt(0)\n"
LD "a[40:43],v9,s[40:43],s2 offen sc1\n"
MF "v[136:139],v[176:179],a[20:23],v[136:139]\n"
"s_add_u32 s17,s78,4\n"
TR "v[188:189],v3 offset:49152\n"
TR "v[190:191],v3 offset:51200\n"
MF "v[136:139],v[180:183],a[60:63],v[136:139]\n"
"s_and_b32 s17,s17,15\n"
TR "a[240:241],v3 offset:53248\n"
TR "a[242:243],v3 offset:55296\n"
MF "v[136:139],v[184:187],a[100:103],v[136:139]\n"
"s_add_u32 s2,s2,204800\n"
TR "a[244:245],v3 offset:57344\n"
TR "a[246:247],v3 offset:59392\n"
MF "v[144:147],v[184:187],a[104:107],v[144:147]\n"
LD "a[44:47],v9,s[40:43],s2 offen sc1\n"
MF "v[144:147],v[180:183],a[64:67],v[144:147]\n"
"s_add_u32 s2,s2,204800\n"
"v_mov_b32 v14,s76\n"
"s_add_u32 s76,s76,1\n"
MF "v[144:147],v[176:179],a[24:27],v[144:147]\n"
LD "a[48:51],v9,s[40:43],s2 offen sc1\n"
MF "v[152:155],v[176:179],a[28:31],v[152:155]\n"
"s_and_b32 s72,s72,15\n"
MF "v[152:155],v[180:183],a[68:71],v[152:155]\n"
"s_add_u32 s2,s2,204800\n"
MF "v[152:155],v[184:187],a[108:111],v[152:155]\n"
LD "a[52:55],v9,s[40:43],s2 offen sc1\n"
MF "v[160:163],v[184:187],a[112:115],v[160:163]\n"
"s_bfe_u32 s60,s22,0x10014\n"
MF "v[160:163],v[180:183],a[72:75],v[160:163]\n"
"s_lshl_b32 s60,s60,5\n"
MF "v[160:163],v[176:179],a[32:35],v[160:163]\n"
"v_xor_b32 v242,s60,v8\n"
MF "v[168:171],v[176:179],a[36:39],v[168:171]\n"
MF "v[168:171],v[180:183],a[76:79],v[168:171]\n"
MF "v[168:171],v[184:187],a[116:119],v[168:171]\n"
"s_waitcnt vmcnt(8) lgkmcnt(0)\n"
"s_barrier\n"
MF "v[172:175],a[244:247],a[116:119],v[172:175]\n"
"buffer_store_dword v14,v15,s[40:43],s94 offen sc1\n"
MF "v[172:175],a[240:243],a[76:79],v[172:175]\n"
"s_nop 0\n"
TR "v[176:177],v6\n"
TR "v[178:179],v6 offset:2048\n"
MF "v[172:175],v[188:191],a[36:39],v[172:175]\n"
"s_nop 0\n"
TR "v[180:181],v6 offset:4096\n"
TR "v[182:183],v6 offset:6144\n"
MF "v[164:167],v[188:191],a[32:35],v[164:167]\n"
"s_nop 0\n"
TR "v[184:185],v6 offset:8192\n"
TR "v[186:187],v6 offset:10240\n"
MF "v[164:167],a[240:243],a[72:75],v[164:167]\n"
"v_sub_u32 v10,v10,v11\n"
MF "v[164:167],a[244:247],a[112:115],v[164:167]\n"
"v_readlane_b32 s16,v10,s16\n"
MF "v[156:159],a[244:247],a[108:111],v[156:159]\n"
"s_cmp_lt_u32 s16,9\n"
"s_cselect_b32 s16,0xf0000000,0\n"
MF "v[156:159],a[240:243],a[68:71],v[156:159]\n"
"v_readlane_b32 s17,v10,s17\n"
MF "v[156:159],v[188:191],a[28:31],v[156:159]\n"
"s_cmp_lt_u32 s17,10\n"
"s_cselect_b32 s17,0xf0000000,0\n"
MF "v[148:151],v[188:191],a[24:27],v[148:151]\n"
"s_bfe_u32 s62,s22,0x30014\n"
MF "v[148:151],a[240:243],a[64:67],v[148:151]\n"
"s_add_u32 s62,s62,3\n"
"s_and_b32 s62,s62,7\n"
"s_mul_i32 s62,s62,204800\n"
MF "v[148:151],a[244:247],a[104:107],v[148:151]\n"
"s_add_u32 s62,s62,s73\n"
"s_add_u32 s62,s62,122880\n"
"s_or_b32 s62,s62,s16\n"
MF "v[140:143],a[244:247],a[100:103],v[140:143]\n"
LD "v[56:59],v9,s[40:43],s62 offen sc1\n"
MF "v[140:143],a[240:243],a[60:63],v[140:143]\n"
LD "v[64:67],v9,s[40:43],s62 offen offset:1024 sc1\n"
MF "v[140:143],v[188:191],a[20:23],v[140:143]\n"
"s_waitcnt lgkmcnt(0)\n"
LD "v[72:75],v9,s[40:43],s62 offen offset:2048 sc1\n"
MF "v[136:139],v[176:179],a[140:143],v[136:139]\n"
"s_nop 0\n"
TR "v[188:189],v7\n"
TR "v[190:191],v7 offset:2048\n"
MF "v[136:139],v[180:183],a[180:183],v[136:139]\n"
"s_nop 0\n"
TR "a[240:241],v7 offset:4096\n"
TR "a[242:243],v7 offset:6144\n"
MF "v[136:139],v[184:187],a[220:223],v[136:139]\n"
"s_nop 0\n"
TR "a[244:245],v7 offset:8192\n"
TR "a[246:247],v7 offset:10240\n"
MF "v[144:147],v[184:187],a[224:227],v[144:147]\n"
LD "v[80:83],v9,s[40:43],s62 offen offset:3072 sc1\n"
MF "v[144:147],v[180:183],a[184:187],v[144:147]\n"
MF "v[144:147],v[176:179],a[144:147],v[144:147]\n"
"s_add_u32 s62,s62,4096\n"
MF "v[152:155],v[176:179],a[148:151],v[152:155]\n"
LD "v[88:91],v9,s[40:43],s62 offen sc1\n"
MF "v[152:155],v[180:183],a[188:191],v[152:155]\n"
"s_bfe_u32 s62,s22,0x30014\n"
"ds_write_addtid_b32 v136\n"
"ds_write_addtid_b32 v137 offset:256\n"
MF "v[152:155],v[184:187],a[228:231],v[152:155]\n"
"s_add_u32 s62,s62,2\n"
"s_and_b32 s62,s62,7\n"
"s_mul_i32 s62,s62,204800\n"
MF "v[160:163],v[184:187],a[232:235],v[160:163]\n"
"s_add_u32 s62,s62,s73\n"
"s_add_u32 s62,s62,143360\n"
"s_or_b32 s62,s62,s17\n"
MF "v[160:163],v[180:183],a[192:195],v[160:163]\n"
LD "v[60:63],v9,s[40:43],s62 offen sc1\n"
MF "v[160:163],v[176:179],a[152:155],v[160:163]\n"
"s_nop 0\n"
"ds_write_addtid_b32 v138 offset:512\n"
"ds_write_addtid_b32 v139 offset:768\n"
MF "v[168:171],v[176:179],a[156:159],v[168:171]\n"
LD "v[68:71],v9,s[40:43],s62 offen offset:1024 sc1\n"
MF "v[168:171],v[180:183],a[196:199],v[168:171]\n"
"s_nop 0\n"
"ds_write_addtid_b32 v144 offset:2048\n"
"ds_write_addtid_b32 v145 offset:2304\n"
MF "v[168:171],v[184:187],a[236:239],v[168:171]\n"
"s_waitcnt lgkmcnt(6)\n"
LD "v[76:79],v9,s[40:43],s62 offen offset:2048 sc1\n"
MF "v[140:143],v[188:191],a[140:143],v[140:143]\n"
"s_nop 0\n"
TR "v[176:177],v4\n"
TR "v[178:179],v4 offset:2048\n"
MF "v[140:143],a[240:243],a[180:183],v[140:143]\n"
"s_nop 0\n"
TR "v[180:181],v4 offset:4096\n"
TR "v[182:183],v4 offset:6144\n"
MF "v[140:143],a[244:247],a[220:223],v[140:143]\n"
"s_nop 0\n"
TR "v[184:185],v4 offset:8192\n"
TR "v[186:187],v4 offset:10240\n"
MF "v[148:151],a[244:247],a[224:227],v[148:151]\n"
LD "v[84:87],v9,s[40:43],s62 offen offset:3072 sc1\n"
MF "v[148:151],a[240:243],a[184:187],v[148:151]\n"
"s_nop 0\n"
"ds_write_addtid_b32 v146 offset:2560\n"
"ds_write_addtid_b32 v147 offset:2816\n"
MF "v[148:151],v[188:191],a[144:147],v[148:151]\n"
"s_add_u32 s62,s62,4096\n"
"ds_write_addtid_b32 v152 offset:4096\n"
"ds_write_addtid_b32 v153 offset:4352\n"
MF "v[156:159],v[188:191],a[148:151],v[156:159]\n"
LD "v[92:95],v9,s[40:43],s62 offen sc1\n"
MF "v[156:159],a[240:243],a[188:191],v[156:159]\n"
"s_nop 0\n"
"ds_write_addtid_b32 v154 offset:4608\n"
"ds_write_addtid_b32 v155 offset:4864\n"
MF "v[156:159],a[244:247],a[228:231],v[156:159]\n"
"s_nop 0\n"
"ds_write_addtid_b32 v160 offset:6144\n"
"ds_write_addtid_b32 v161 offset:6400\n"
MF "v[164:167],a[244:247],a[232:235],v[164:167]\n"
"s_nop 0\n"
"ds_write_addtid_b32 v162 offset:6656\n"
"ds_write_addtid_b32 v163 offset:6912\n"
MF "v[164:167],a[240:243],a[192:195],v[164:167]\n"
"s_nop 0\n"
"ds_write_addtid_b32 v168 offset:8192\n"
"ds_write_addtid_b32 v169 offset:8448\n"
MF "v[164:167],v[188:191],a[152:155],v[164:167]\n"
"buffer_load_dword v10,v12,s[40:43],s86 offen sc1\n"
MF "v[172:175],v[188:191],a[156:159],v[172:175]\n"
"s_nop 0\n"
"ds_write_addtid_b32 v170 offset:8704\n"
"ds_write_addtid_b32 v171 offset:8960\n"
MF "v[172:175],a[240:243],a[196:199],v[172:175]\n"
"s_nop 0\n"
"ds_write_addtid_b32 v140 offset:1024\n"
"ds_write_addtid_b32 v141 offset:1280\n"
MF "v[172:175],a[244:247],a[236:239],v[172:175]\n"
"s_waitcnt lgkmcnt(15)\n"
"ds_write_addtid_b32 v142 offset:1536\n"
"ds_write_addtid_b32 v143 offset:1792\n"
MF "v[96:99],v[176:179],a[140:143],v[96:99]\n"
"s_nop 0\n"
TR "v[188:189],v5\n"
TR "v[190:191],v5 offset:2048\n"
MF "v[96:99],v[180:183],a[180:183],v[96:99]\n"
"s_nop 0\n"
TR "a[240:241],v5 offset:4096\n"
TR "a[242:243],v5 offset:6144\n"
MF "v[96:99],v[184:187],a[220:223],v[96:99]\n"
"s_nop 0\n"
TR "a[244:245],v5 offset:8192\n"
TR "a[246:247],v5 offset:10240\n"
MF "v[104:107],v[184:187],a[224:227],v[104:107]\n"
"s_nop 0\n"
"ds_write_addtid_b32 v148 offset:3072\n"
"ds_write_addtid_b32 v149 offset:3328\n"
MF "v[104:107],v[180:183],a[184:187],v[104:107]\n"
"s_nop 0\n"
"ds_write_addtid_b32 v150 offset:3584\n"
"ds_write_addtid_b32 v151 offset:3840\n"
MF "v[104:107],v[176:179],a[144:147],v[104:107]\n"
"s_nop 0\n"
"ds_write_addtid_b32 v156 offset:5120\n"
"ds_write_addtid_b32 v157 offset:5376\n"
MF "v[112:115],v[176:179],a[148:151],v[112:115]\n"
"s_nop 0\n"
"ds_write_addtid_b32 v158 offset:5632\n"
"ds_write_addtid_b32 v159 offset:5888\n"
MF "v[112:115],v[180:183],a[188:191],v[112:115]\n"
"ds_write_addtid_b32 v164 offset:7168\n"
MF "v[112:115],v[184:187],a[228:231],v[112:115]\n"
"ds_write_addtid_b32 v165 offset:7424\n"
MF "v[120:123],v[184:187],a[232:235],v[120:123]\n"
"ds_write_addtid_b32 v166 offset:7680\n"
MF "v[120:123],v[180:183],a[192:195],v[120:123]\n"
"ds_write_addtid_b32 v167 offset:7936\n"
MF "v[120:123],v[176:179],a[152:155],v[120:123]\n"
"ds_write_addtid_b32 v172 offset:9216\n"
MF "v[128:131],v[176:179],a[156:159],v[128:131]\n"
"ds_write_addtid_b32 v173 offset:9472\n"
MF "v[128:131],v[180:183],a[196:199],v[128:131]\n"
"ds_write_addtid_b32 v174 offset:9728\n"
MF "v[128:131],v[184:187],a[236:239],v[128:131]\n"
"s_waitcnt lgkmcnt(15)\n"
"ds_write_addtid_b32 v175 offset:9984\n"
MF "v[100:103],v[188:191],a[140:143],v[100:103]\n"
MF "v[100:103],a[240:243],a[180:183],v[100:103]\n"
MF "v[100:103],a[244:247],a[220:223],v[100:103]\n"
MF "v[108:111],a[244:247],a[224:227],v[108:111]\n"
MF "v[108:111],a[240:243],a[184:187],v[108:111]\n"
"s_waitcnt lgkmcnt(0)\n"
"s_barrier\n"
MF "v[108:111],v[188:191],a[144:147],v[108:111]\n"
"ds_read2st64_b32 v[16:17],v13 offset0:192 offset1:193\n"
MF "v[116:119],v[188:191],a[148:151],v[116:119]\n"
"ds_read2st64_b32 v[18:19],v13 offset0:194 offset1:195\n"
MF "v[116:119],a[240:243],a[188:191],v[116:119]\n"
"ds_read2st64_b32 v[20:21],v13 offset0:196 offset1:197\n"
MF "v[116:119],a[244:247],a[228:231],v[116:119]\n"
"ds_read2st64_b32 v[22:23],v13 offset0:198 offset1:199\n"
MF "v[124:127],a[244:247],a[232:235],v[124:127]\n"
"ds_read2st64_b32 v[24:25],v13 offset0:200 offset1:201\n"
MF "v[124:127],a[240:243],a[192:195],v[124:127]\n"
"ds_read2st64_b32 v[26:27],v13 offset0:202 offset1:203\n"
MF "v[124:127],v[188:191],a[152:155],v[124:127]\n"
"ds_read2st64_b32 v[28:29],v13 offset0:204 offset1:205\n"
MF "v[132:135],v[188:191],a[156:159],v[132:135]\n"
"ds_read2st64_b32 v[30:31],v13 offset0:206 offset1:207\n"
MF "v[132:135],a[240:243],a[196:199],v[132:135]\n"
"ds_read2st64_b32 v[32:33],v13 offset0:208 offset1:209\n"
MF "v[132:135],a[244:247],a[236:239],v[132:135]\n"
"ds_read2st64_b32 v[34:35],v13 offset0:210 offset1:211\n"
"ds_read2st64_b32 v[36:37],v13 offset0:212 offset1:213\n"
"ds_read2st64_b32 v[38:39],v13 offset0:214 offset1:215\n"
"s_nop 0\n"
"ds_read2st64_b32 v[40:41],v13 offset0:216 offset1:217\n"
"ds_read2st64_b32 v[42:43],v13 offset0:218 offset1:219\n"
"ds_read2st64_b32 v[44:45],v13 offset0:220 offset1:221\n"
"ds_read2st64_b32 v[46:47],v13 offset0:222 offset1:223\n"
"ds_read2st64_b32 v[48:49],v13 offset0:224 offset1:225\n"
"ds_read2st64_b32 v[50:51],v13 offset0:226 offset1:227\n"
"ds_read2st64_b32 v[52:53],v13 offset0:228 offset1:229\n"
"ds_read2st64_b32 v[54:55],v13 offset0:230 offset1:231\n"
"s_waitcnt vmcnt(0)\n"
"v_sub_u32 v10,v10,v11\n"
"s_cmp_eq_u32 s16,0\n"
"s_cbranch_scc1 .Lhave13\n"
"s_add_u32 s59,s78,6\n"
"s_and_b32 s59,s59,15\n"
"s_nop 3\n"
".Lnf7:\n"
"v_readlane_b32 s58,v10,s59\n"
"s_cmp_lt_u32 s58,9\n"
"s_cbranch_scc1 .Lsp7\n"
".Lnb7:\n"
"s_bfe_u32 s62,s22,0x30014\n"
"s_add_u32 s62,s62,3\n"
"s_and_b32 s62,s62,7\n"
"s_mul_i32 s62,s62,204800\n"
"s_add_u32 s62,s62,s73\n"
".Lpg35h:\n"
"s_cbranch_execz .Lisr4\n"
"s_nop 0\n"
"s_add_u32 s62,s62,122880\n"
LD "v[56:59],v9,s[40:43],s62 offen sc1\n"
LD "v[64:67],v9,s[40:43],s62 offen offset:1024 sc1\n"
LD "v[72:75],v9,s[40:43],s62 offen offset:2048 sc1\n"
LD "v[80:83],v9,s[40:43],s62 offen offset:3072 sc1\n"
"s_add_u32 s62,s62,4096\n"
LD "v[88:91],v9,s[40:43],s62 offen sc1\n"
".Lhave13:\n"
"s_cmp_eq_u32 s17,0\n"
"s_cbranch_scc1 .Lhave12\n"
"s_add_u32 s59,s78,4\n"
"s_and_b32 s59,s59,15\n"
"s_nop 3\n"
".Lnf8:\n"
"v_readlane_b32 s58,v10,s59\n"
"s_cmp_lt_u32 s58,10\n"
"s_cbranch_scc1 .Lsp8\n"
".Lnb8:\n"
"s_bfe_u32 s62,s22,0x30014\n"
"s_add_u32 s62,s62,2\n"
"s_and_b32 s62,s62,7\n"
"s_mul_i32 s62,s62,204800\n"
"s_add_u32 s62,s62,s73\n"
"s_add_u32 s62,s62,143360\n"
LD "v[60:63],v9,s[40:43],s62 offen sc1\n"
LD "v[68:71],v9,s[40:43],s62 offen offset:1024 sc1\n"
LD "v[76:79],v9,s[40:43],s62 offen offset:2048 sc1\n"
LD "v[84:87],v9,s[40:43],s62 offen offset:3072 sc1\n"
"s_add_u32 s62,s62,4096\n"
LD "v[92:95],v9,s[40:43],s62 offen sc1\n"
".Lhave12:\n"
".Lnf9:\n"
"v_readlane_b32 s58,v10,s72\n"
"s_cmp_lt_u32 s58,11\n"
"s_cbranch_scc1 .Lsp9\n"
".Lnb9:\n"
"s_bfe_u32 s62,s22,0x30014\n"
"s_add_u32 s62,s62,1\n"
"s_and_b32 s62,s62,7\n"
"s_mul_i32 s62,s62,204800\n"
"s_add_u32 s62,s62,s73\n"
"s_nop 0\n"
"s_add_u32 s62,s62,163840\n"
LD "v[208:211],v9,s[40:43],s62 offen sc1\n"
LD "v[212:215],v9,s[40:43],s62 offen offset:1024 sc1\n"
LD "v[216:219],v9,s[40:43],s62 offen offset:2048 sc1\n"
LD "v[220:223],v9,s[40:43],s62 offen offset:3072 sc1\n"
"s_add_u32 s62,s62,4096\n"
LD "v[224:227],v9,s[40:43],s62 offen sc1\n"
"s_cmp_eq_u32 s1,0xf0000000\n"
"s_cbranch_scc1 .Ljy1\n"
"s_nop 0\n"
LD "a[80:83],v9,s[40:43],s3 offen sc1\n"
"s_add_u32 s3,s3,204800\n"
LD "a[84:87],v9,s[40:43],s3 offen sc1\n"
"s_add_u32 s3,s3,204800\n"
LD "a[88:91],v9,s[40:43],s3 offen sc1\n"
"s_add_u32 s3,s3,204800\n"
LD "a[92:95],v9,s[40:43],s3 offen sc1\n"
"s_add_u32 s3,s3,204800\n"
LD "a[120:123],v9,s[40:43],s3 offen sc1\n"
"s_add_u32 s3,s3,204800\n"
LD "a[124:127],v9,s[40:43],s3 offen sc1\n"
"s_add_u32 s3,s3,204800\n"
LD "a[128:131],v9,s[40:43],s3 offen sc1\n"
"s_add_u32 s3,s3,204800\n"
LD "a[132:135],v9,s[40:43],s3 offen sc1\n"
".Ljy1:\n"
"s_waitcnt vmcnt(27)\n"
"v_accvgpr_mov_b32 a16,a0\n"
"v_accvgpr_mov_b32 a17,a1\n"
"v_accvgpr_mov_b32 a56,a40\n"
"v_accvgpr_mov_b32 a57,a41\n"
MF "v[248:251],a[2:5],a[250:253],0\n"
MF "v[244:247],a[6:9],a[250:253],0\n"
MF "v[248:251],a[10:13],a[250:253],v[248:251]\n"
MF "v[244:247],a[14:17],a[250:253],v[244:247]\n"
MF "v[248:251],a[42:45],a[250:253],v[248:251]\n"
MF "v[244:247],a[46:49],a[250:253],v[244:247]\n"
MF "v[248:251],a[50:53],a[250:253],v[248:251]\n"
MF "v[244:247],a[54:57],a[250:253],v[244:247]\n"
"s_waitcnt lgkmcnt(15)\n"
"v_pk_add_f32 v[96:97],v[96:97],v[16:17]\n"
"v_pk_add_f32 v[98:99],v[98:99],v[18:19]\n"
"v_pk_add_f32 v[100:101],v[100:101],v[20:21]\n"
"v_pk_add_f32 v[102:103],v[102:103],v[22:23]\n"
"v_pk_add_f32 v[104:105],v[104:105],v[24:25]\n"
"s_waitcnt lgkmcnt(14)\n"
"v_pk_add_f32 v[106:107],v[106:107],v[26:27]\n"
"s_waitcnt lgkmcnt(13)\n"
"v_pk_add_f32 v[108:109],v[108:109],v[28:29]\n"
"s_waitcnt lgkmcnt(12)\n"
"v_pk_add_f32 v[110:111],v[110:111],v[30:31]\n"
"s_waitcnt lgkmcnt(11)\n"
"v_pk_add_f32 v[112:113],v[112:113],v[32:33]\n"
"s_waitcnt lgkmcnt(10)\n"
"v_pk_add_f32 v[114:115],v[114:115],v[34:35]\n"
"s_waitcnt lgkmcnt(9)\n"
"v_pk_add_f32 v[116:117],v[116:117],v[36:37]\n"
"s_waitcnt lgkmcnt(8)\n"
"v_pk_add_f32 v[118:119],v[118:119],v[38:39]\n"
"s_waitcnt lgkmcnt(7)\n"
"v_pk_add_f32 v[120:121],v[120:121],v[40:41]\n"
"s_waitcnt lgkmcnt(6)\n"
"v_pk_add_f32 v[122:123],v[122:123],v[42:43]\n"
"s_waitcnt lgkmcnt(5)\n"
"v_pk_add_f32 v[124:125],v[124:125],v[44:45]\n"
"s_waitcnt lgkmcnt(4)\n"
"v_pk_add_f32 v[126:127],v[126:127],v[46:47]\n"
"s_waitcnt lgkmcnt(3)\n"
"v_pk_add_f32 v[128:129],v[128:129],v[48:49]\n"
"s_waitcnt lgkmcnt(2)\n"
"v_pk_add_f32 v[130:131],v[130:131],v[50:51]\n"
"s_waitcnt lgkmcnt(1)\n"
"v_pk_add_f32 v[132:133],v[132:133],v[52:53]\n"
"s_waitcnt lgkmcnt(0)\n"
"v_pk_add_f32 v[134:135],v[134:135],v[54:55]\n"
CV "v244,v244,v245\n"
CV "v245,v246,v247\n"
CV "v246,v248,v249\n"
CV "v247,v250,v251\n"
"s_nop 0\n"
"v_permlane16_swap_b32 v244,v246\n"
"v_permlane16_swap_b32 v245,v247\n"
"buffer_store_dwordx4 v[244:247],v8,s[44:47],s0 offen sc1\n"
"s_waitcnt vmcnt(6)\n"
MF "v[100:103],v[56:59],a[252:255],v[100:103]\n"
MF "v[108:111],v[64:67],a[252:255],v[108:111]\n"
MF "v[116:119],v[72:75],a[252:255],v[116:119]\n"
MF "v[124:127],v[80:83],a[252:255],v[124:127]\n"
MF "v[132:135],v[88:91],a[252:255],v[132:135]\n"
MF "v[96:99],v[56:59],a[248:251],v[96:99]\n"
MF "v[104:107],v[64:67],a[248:251],v[104:107]\n"
MF "v[112:115],v[72:75],a[248:251],v[112:115]\n"
MF "v[120:123],v[80:83],a[248:251],v[120:123]\n"
MF "v[128:131],v[88:91],a[248:251],v[128:131]\n"
MF "v[96:99],v[60:63],a[252:255],v[96:99]\n"
MF "v[104:107],v[68:71],a[252:255],v[104:107]\n"
MF "v[112:115],v[76:79],a[252:255],v[112:115]\n"
MF "v[120:123],v[84:87],a[252:255],v[120:123]\n"
MF "v[128:131],v[92:95],a[252:255],v[128:131]\n"
MF "v[100:103],v[60:63],a[248:251],v[100:103]\n"
MF "v[108:111],v[68:71],a[248:251],v[108:111]\n"
MF "v[116:119],v[76:79],a[248:251],v[116:119]\n"
MF "v[124:127],v[84:87],a[248:251],v[124:127]\n"
MF "v[132:135],v[92:95],a[248:251],v[132:135]\n"
"s_waitcnt vmcnt(1)\n"
MF "v[100:103],v[208:211],a[252:255],v[100:103]\n"
MF "v[96:99],v[208:211],a[248:251],v[96:99]\n"
MF "v[108:111],v[212:215],a[252:255],v[108:111]\n"
MF "v[104:107],v[212:215],a[248:251],v[104:107]\n"
MF "v[116:119],v[216:219],a[252:255],v[116:119]\n"
"s_nop 4\n"
CV "v96,v96,v97\n"
CV "v97,v98,v99\n"
MF "v[112:115],v[216:219],a[248:251],v[112:115]\n"
CV "v98,v100,v101\n"
CV "v99,v102,v103\n"
MF "v[124:127],v[220:223],a[252:255],v[124:127]\n"
CV "v104,v104,v105\n"
CV "v105,v106,v107\n"
MF "v[120:123],v[220:223],a[248:251],v[120:123]\n"
CV "v106,v108,v109\n"
CV "v107,v110,v111\n"
"v_permlane16_swap_b32 v96,v98\n"
"v_permlane16_swap_b32 v97,v99\n"
"buffer_store_dwordx4 v[96:99],v242,s[44:47],s87 offen sc1\n"
"s_add_u32 s87,s87,40960\n"
MF "v[132:135],v[224:227],a[252:255],v[132:135]\n"
CV "v112,v112,v113\n"
CV "v113,v114,v115\n"
MF "v[128:131],v[224:227],a[248:251],v[128:131]\n"
CV "v114,v116,v117\n"
CV "v115,v118,v119\n"
"v_permlane16_swap_b32 v104,v106\n"
"v_permlane16_swap_b32 v105,v107\n"
"buffer_store_dwordx4 v[104:107],v242,s[44:47],s87 offen sc1\n"
"s_add_u32 s87,s87,40960\n"
CV "v120,v120,v121\n"
CV "v121,v122,v123\n"
CV "v122,v124,v125\n"
CV "v123,v126,v127\n"
"v_permlane16_swap_b32 v112,v114\n"
"v_permlane16_swap_b32 v113,v115\n"
"buffer_store_dwordx4 v[112:115],v242,s[44:47],s87 offen sc1\n"
"s_add_u32 s87,s87,40960\n"
CV "v128,v128,v129\n"
CV "v129,v130,v131\n"
CV "v130,v132,v133\n"
CV "v131,v134,v135\n"
"v_permlane16_swap_b32 v120,v122\n"
"v_permlane16_swap_b32 v121,v123\n"
"buffer_store_dwordx4 v[120:123],v242,s[44:47],s87 offen sc1\n"
"s_add_u32 s87,s87,40960\n"
"v_permlane16_swap_b32 v128,v130\n"
"v_permlane16_swap_b32 v129,v131\n"
"buffer_store_dwordx4 v[128:131],v242,s[44:47],s87 offen sc1\n"
"s_add_u32 s87,s87,40960\n"
"s_cmp_eq_u32 s1,0xf0000000\n"
"s_cbranch_scc1 .Ljx1\n"
"s_waitcnt vmcnt(5)\n"
"v_accvgpr_mov_b32 a96,a80\n"
"v_accvgpr_mov_b32 a97,a81\n"
"v_accvgpr_mov_b32 a136,a120\n"
"v_accvgpr_mov_b32 a137,a121\n"
MF "v[196:199],a[82:85],a[250:253],0\n"
MF "v[192:195],a[86:89],a[250:253],0\n"
MF "v[196:199],a[90:93],a[250:253],v[196:199]\n"
MF "v[192:195],a[94:97],a[250:253],v[192:195]\n"
MF "v[196:199],a[122:125],a[250:253],v[196:199]\n"
MF "v[192:195],a[126:129],a[250:253],v[192:195]\n"
MF "v[196:199],a[130:133],a[250:253],v[196:199]\n"
MF "v[192:195],a[134:137],a[250:253],v[192:195]\n"
"s_nop 7\n"
CV "v192,v192,v193\n"
CV "v193,v194,v195\n"
CV "v194,v196,v197\n"
CV "v195,v198,v199\n"
"s_nop 0\n"
"v_permlane16_swap_b32 v192,v194\n"
"v_permlane16_swap_b32 v193,v195\n"
"buffer_store_dwordx4 v[192:195],v8,s[44:47],s1 offen sc1\n"
".Ljx1:\n"
"s_cmp_lt_u32 s92,40\n"
"s_cbranch_scc1 .Ljobs\n"
"s_endpgm\n"
".Ljobs:\n"
"s_cmp_ge_u32 s92,40\n"
"s_cbranch_scc1 .Ldone\n"
"buffer_load_dword v10,v12,s[40:43],s86 offen sc1\n"
"s_mul_i32 s95,s92,3277\n"
"s_lshr_b32 s95,s95,16\n"
"s_mul_i32 s59,s95,20\n"
"s_sub_u32 s59,s92,s59\n"
"s_cmp_ge_u32 s59,10\n"
"s_cselect_b32 s96,4294557760,0\n"
"s_add_u32 s96,s96,1024\n"
"s_mul_i32 s59,s59,40960\n"
"s_add_u32 s96,s96,s59\n"
"s_lshl_b32 s59,s95,7\n"
"s_add_u32 s96,s96,s59\n"
"s_add_u32 s95,s95,3\n"
"s_waitcnt vmcnt(0)\n"
".Ljtry:\n"
"s_cmp_eq_u32 s98,0\n"
"s_cselect_b32 s95,0,s95\n"
"v_sub_u32 v10,v10,v11\n"
"v_cmp_gt_u32 vcc,s95,v10\n"
"s_lshl_b32 s82,s92,10\n"
"s_add_u32 s82,s82,s93\n"
"s_cmp_eq_u64 vcc,0\n"
"s_cselect_b32 s82,s82,0xf0000000\n"
"s_cselect_b32 s84,s96,0xf0000000\n"
"s_cselect_b32 s59,32,0\n"
"s_add_u32 s92,s92,s59\n"
"s_cmp_eq_u32 s59,0\n"
"s_cbranch_scc0 .Ljgo\n"
"s_sub_u32 s98,s98,1\n"
"s_sleep 2\n"
"s_branch .Ljobs\n"
".Ljgo:\n"
"s_mov_b32 s83,s84\n"
LD "v[208:211],v9,s[40:43],s82 offen sc1\n"
"s_add_u32 s82,s82,s71\n"
LD "v[212:215],v9,s[40:43],s82 offen sc1\n"
"s_add_u32 s82,s82,s71\n"
LD "v[216:219],v9,s[40:43],s82 offen sc1\n"
"s_add_u32 s82,s82,s71\n"
LD "v[220:223],v9,s[40:43],s82 offen sc1\n"
"s_add_u32 s82,s82,s71\n"
LD "v[224:227],v9,s[40:43],s82 offen sc1\n"
"s_add_u32 s82,s82,s66\n"
LD "v[228:231],v9,s[40:43],s82 offen sc1\n"
"s_add_u32 s82,s82,s71\n"
LD "v[232:235],v9,s[40:43],s82 offen sc1\n"
"s_add_u32 s82,s82,s71\n"
LD "v[236:239],v9,s[40:43],s82 offen sc1\n"
"buffer_load_dword v10,v12,s[40:43],s86 offen sc1\n"
"s_waitcnt vmcnt(0)\n"
".Ljfin:\n"
"s_nop 1\n"
"v_mov_b32 v240,v208\n"
"v_mov_b32 v241,v209\n"
MF "v[248:251],v[210:213],a[250:253],0\n"
MF "v[244:247],v[214:217],a[250:253],0\n"
MF "v[248:251],v[218:221],a[250:253],v[248:251]\n"
MF "v[244:247],v[222:225],a[250:253],v[244:247]\n"
MF "v[248:251],v[226:229],a[250:253],v[248:251]\n"
MF "v[244:247],v[230:233],a[250:253],v[244:247]\n"
MF "v[248:251],v[234:237],a[250:253],v[248:251]\n"
MF "v[244:247],v[238:241],a[250:253],v[244:247]\n"
"s_nop 7\n"
CV "v244,v244,v245\n"
CV "v245,v246,v247\n"
CV "v246,v248,v249\n"
CV "v247,v250,v251\n"
"s_nop 0\n"
"v_permlane16_swap_b32 v244,v246\n"
"v_permlane16_swap_b32 v245,v247\n"
"buffer_store_dwordx4 v[244:247],v8,s[44:47],s83 offen sc1\n"
"s_cmp_ge_u32 s92,40\n"
"s_cbranch_scc1 .Ldone\n"
"s_mul_i32 s95,s92,3277\n"
"s_lshr_b32 s95,s95,16\n"
"s_mul_i32 s59,s95,20\n"
"s_sub_u32 s59,s92,s59\n"
"s_cmp_ge_u32 s59,10\n"
"s_cselect_b32 s96,4294557760,0\n"
"s_add_u32 s96,s96,1024\n"
"s_mul_i32 s59,s59,40960\n"
"s_add_u32 s96,s96,s59\n"
"s_lshl_b32 s59,s95,7\n"
"s_add_u32 s96,s96,s59\n"
"s_add_u32 s95,s95,3\n"
"s_branch .Ljtry\n"
".Lool:\n"
".Lsp0:\n"
"s_cmp_eq_u32 s98,0\n"
"s_cbranch_scc1 .Lnb0\n"
"s_sub_u32 s98,s98,1\n"
"s_sleep 2\n"
"buffer_load_dword v10,v12,s[40:43],s86 offen sc1\n"
"s_waitcnt vmcnt(0)\n"
"v_sub_u32 v10,v10,v11\n"
"s_nop 1\n"
"s_branch .Lnf0\n"
".Lsp1:\n"
"s_cmp_eq_u32 s98,0\n"
"s_cbranch_scc1 .Lnb1\n"
"s_sub_u32 s98,s98,1\n"
"s_sleep 2\n"
"buffer_load_dword v10,v12,s[40:43],s86 offen sc1\n"
"s_waitcnt vmcnt(0)\n"
"v_sub_u32 v10,v10,v11\n"
"s_nop 1\n"
"s_branch .Lnf1\n"
".Lsp2:\n"
"s_cmp_eq_u32 s98,0\n"
"s_cbranch_scc1 .Lnb2\n"
"s_sub_u32 s98,s98,1\n"
"s_sleep 2\n"
"buffer_load_dword v10,v12,s[40:43],s86 offen sc1\n"
"s_waitcnt vmcnt(0)\n"
"v_sub_u32 v10,v10,v11\n"
"s_nop 1\n"
"s_branch .Lnf2\n"
".Lsp3:\n"
"s_cmp_eq_u32 s98,0\n"
"s_cbranch_scc1 .Lnb3\n"
"s_sub_u32 s98,s98,1\n"
"s_sleep 2\n"
"buffer_load_dword v10,v12,s[40:43],s86 offen sc1\n"
"s_waitcnt vmcnt(0)\n"
"v_sub_u32 v10,v10,v11\n"
"s_nop 1\n"
"s_branch .Lnf3\n"
".Lsp4:\n"
"s_cmp_eq_u32 s98,0\n"
"s_cbranch_scc1 .Lnb4\n"
"s_sub_u32 s98,s98,1\n"
"s_sleep 2\n"
"buffer_load_dword v10,v12,s[40:43],s86 offen sc1\n"
"s_waitcnt vmcnt(0)\n"
"v_sub_u32 v10,v10,v11\n"
"s_nop 1\n"
"s_branch .Lnf4\n"
".Lsp5:\n"
"s_cmp_eq_u32 s98,0\n"
"s_cbranch_scc1 .Lnb5\n"
"s_sub_u32 s98,s98,1\n"
"s_sleep 2\n"
"buffer_load_dword v10,v12,s[40:43],s86 offen sc1\n"
"s_waitcnt vmcnt(0)\n"
"v_sub_u32 v10,v10,v11\n"
"s_nop 1\n"
"s_branch .Lnf5\n"
".Lsp6:\n"
"s_cmp_eq_u32 s98,0\n"
"s_cbranch_scc1 .Lnb6\n"
"s_sub_u32 s98,s98,1\n"
"s_sleep 2\n"
"buffer_load_dword v10,v12,s[40:43],s86 offen sc1\n"
"s_waitcnt vmcnt(0)\n"
"v_sub_u32 v10,v10,v11\n"
"s_nop 1\n"
"s_branch .Lnf6\n"
".Lsp7:\n"
"s_cmp_eq_u32 s98,0\n"
"s_cbranch_scc1 .Lnb7\n"
"s_sub_u32 s98,s98,1\n"
"s_sleep 2\n"
"buffer_load_dword v10,v12,s[40:43],s86 offen sc1\n"
"s_waitcnt vmcnt(0)\n"
"v_sub_u32 v10,v10,v11\n"
"s_nop 1\n"
"s_branch .Lnf7\n"
".Lsp8:\n"
"s_cmp_eq_u32 s98,0\n"
"s_cbranch_scc1 .Lnb8\n"
"s_sub_u32 s98,s98,1\n"
"s_sleep 2\n"
"buffer_load_dword v10,v12,s[40:43],s86 offen sc1\n"
"s_waitcnt vmcnt(0)\n"
"v_sub_u32 v10,v10,v11\n"
"s_nop 1\n"
"s_branch .Lnf8\n"
".Lsp9:\n"
"s_cmp_eq_u32 s98,0\n"
"s_cbranch_scc1 .Lnb9\n"
"s_sub_u32 s98,s98,1\n"
"s_sleep 2\n"
"buffer_load_dword v10,v12,s[40:43],s86 offen sc1\n"
"s_waitcnt vmcnt(0)\n"
"v_sub_u32 v10,v10,v11\n"
"s_nop 1\n"
"s_branch .Lnf9\n"
".Lcode1:\n"
".Lslow_0:\n"
"s_nop 7\n"
"s_nop 7\n"
"v_and_b32 v10,240,v9\n"
"v_mul_u32_u24 v10,24,v10\n"
"v_lshrrev_b32 v11,4,v9\n"
"v_and_or_b32 v10,v11,48,v10\n"
"s_mov_b32 s59,s34\n"
"s_mov_b32 s34,s20\n"
"s_add_u32 s58,s74,0\n"
"s_nop 3\n"
LD "v[52:55],v10,s[32:35],s58 offen\n"
"s_waitcnt vmcnt(0)\n"
MF "v[72:75],v[52:55],v[136:139],v[32:35]\n"
MF "v[76:79],v[52:55],v[140:143],v[36:39]\n"
MF "v[80:83],v[52:55],v[144:147],v[40:43]\n"
MF "v[84:87],v[52:55],v[148:151],v[44:47]\n"
MF "v[88:91],v[52:55],v[152:155],v[48:51]\n"
"s_add_u32 s58,s74,0\n"
"s_nop 3\n"
LD "v[52:55],v10,s[32:35],s58 offen offset:64\n"
"s_waitcnt vmcnt(0)\n"
MF "v[72:75],v[52:55],v[156:159],v[72:75]\n"
MF "v[76:79],v[52:55],v[160:163],v[76:79]\n"
MF "v[80:83],v[52:55],v[164:167],v[80:83]\n"
MF "v[84:87],v[52:55],v[168:171],v[84:87]\n"
MF "v[88:91],v[52:55],v[172:175],v[88:91]\n"
"s_add_u32 s58,s74,0\n"
"s_nop 3\n"
LD "v[52:55],v10,s[32:35],s58 offen offset:128\n"
"s_waitcnt vmcnt(0)\n"
MF "v[72:75],v[52:55],v[176:179],v[72:75]\n"
MF "v[76:79],v[52:55],v[180:183],v[76:79]\n"
MF "v[80:83],v[52:55],v[184:187],v[80:83]\n"
MF "v[84:87],v[52:55],v[188:191],v[84:87]\n"
MF "v[88:91],v[52:55],v[192:195],v[88:91]\n"
"s_add_u32 s58,s74,0\n"
"s_nop 3\n"
LD "v[52:55],v10,s[32:35],s58 offen offset:192\n"
"s_waitcnt vmcnt(0)\n"
MF "v[72:75],v[52:55],v[196:199],v[72:75]\n"
MF "v[76:79],v[52:55],v[200:203],v[76:79]\n"
MF "v[80:83],v[52:55],v[204:207],v[80:83]\n"
MF "v[84:87],v[52:55],v[208:211],v[84:87]\n"
MF "v[88:91],v[52:55],v[212:215],v[88:91]\n"
"s_add_u32 s58,s74,0\n"
"s_nop 3\n"
LD "v[52:55],v10,s[32:35],s58 offen offset:256\n"
"s_waitcnt vmcnt(0)\n"
MF "v[72:75],v[52:55],v[216:219],v[72:75]\n"
MF "v[76:79],v[52:55],v[220:223],v[76:79]\n"
MF "v[80:83],v[52:55],v[224:227],v[80:83]\n"
MF "v[84:87],v[52:55],v[228:231],v[84:87]\n"
MF "v[88:91],v[52:55],v[232:235],v[88:91]\n"
"s_add_u32 s58,s74,0\n"
"s_nop 3\n"
LD "v[52:55],v10,s[32:35],s58 offen offset:320\n"
"s_waitcnt vmcnt(0)\n"
MF "v[72:75],v[52:55],v[236:239],v[72:75]\n"
MF "v[76:79],v[52:55],v[240:243],v[76:79]\n"
MF "v[80:83],v[52:55],v[244:247],v[80:83]\n"
MF "v[84:87],v[52:55],v[248:251],v[84:87]\n"
MF "v[88:91],v[52:55],v[252:255],v[88:91]\n"
"s_add_u32 s58,s74,6144\n"
"s_nop 3\n"
LD "v[52:55],v10,s[32:35],s58 offen\n"
"s_waitcnt vmcnt(0)\n"
MF "v[112:115],v[52:55],v[136:139],v[32:35]\n"
MF "v[116:119],v[52:55],v[140:143],v[36:39]\n"
MF "v[120:123],v[52:55],v[144:147],v[40:43]\n"
MF "v[124:127],v[52:55],v[148:151],v[44:47]\n"
MF "v[128:131],v[52:55],v[152:155],v[48:51]\n"
"s_add_u32 s58,s74,6144\n"
"s_nop 3\n"
LD "v[52:55],v10,s[32:35],s58 offen offset:64\n"
"s_waitcnt vmcnt(0)\n"
MF "v[112:115],v[52:55],v[156:159],v[112:115]\n"
MF "v[116:119],v[52:55],v[160:163],v[116:119]\n"
MF "v[120:123],v[52:55],v[164:167],v[120:123]\n"
MF "v[124:127],v[52:55],v[168:171],v[124:127]\n"
MF "v[128:131],v[52:55],v[172:175],v[128:131]\n"
"s_add_u32 s58,s74,6144\n"
"s_nop 3\n"
LD "v[52:55],v10,s[32:35],s58 offen offset:128\n"
"s_waitcnt vmcnt(0)\n"
MF "v[112:115],v[52:55],v[176:179],v[112:115]\n"
MF "v[116:119],v[52:55],v[180:183],v[116:119]\n"
MF "v[120:123],v[52:55],v[184:187],v[120:123]\n"
MF "v[124:127],v[52:55],v[188:191],v[124:127]\n"
MF "v[128:131],v[52:55],v[192:195],v[128:131]\n"
"s_add_u32 s58,s74,6144\n"
"s_nop 3\n"
LD "v[52:55],v10,s[32:35],s58 offen offset:192\n"
"s_waitcnt vmcnt(0)\n"
MF "v[112:115],v[52:55],v[196:199],v[112:115]\n"
MF "v[116:119],v[52:55],v[200:203],v[116:119]\n"
MF "v[120:123],v[52:55],v[204:207],v[120:123]\n"
MF "v[124:127],v[52:55],v[208:211],v[124:127]\n"
MF "v[128:131],v[52:55],v[212:215],v[128:131]\n"
"s_add_u32 s58,s74,6144\n"
"s_nop 3\n"
LD "v[52:55],v10,s[32:35],s58 offen offset:256\n"
"s_waitcnt vmcnt(0)\n"
MF "v[112:115],v[52:55],v[216:219],v[112:115]\n"
MF "v[116:119],v[52:55],v[220:223],v[116:119]\n"
MF "v[120:123],v[52:55],v[224:227],v[120:123]\n"
MF "v[124:127],v[52:55],v[228:231],v[124:127]\n"
MF "v[128:131],v[52:55],v[232:235],v[128:131]\n"
"s_add_u32 s58,s74,6144\n"
"s_nop 3\n"
LD "v[52:55],v10,s[32:35],s58 offen offset:320\n"
"s_waitcnt vmcnt(0)\n"
MF "v[112:115],v[52:55],v[236:239],v[112:115]\n"
MF "v[116:119],v[52:55],v[240:243],v[116:119]\n"
MF "v[120:123],v[52:55],v[244:247],v[120:123]\n"
MF "v[124:127],v[52:55],v[248:251],v[124:127]\n"
MF "v[128:131],v[52:55],v[252:255],v[128:131]\n"
"s_nop 7\n"
"s_mov_b32 s34,s59\n"
"v_max3_f32 v10,v72,v73,v74\n"
"s_nop 0\n"
"v_max3_f32 v11,v75,v112,v113\n"
"v_max3_f32 v10,v10,v114,v115\n"
"v_max_f32 v10,v10,v11\n"
"v_mov_b32 v11,v10\n"
"s_nop 1\n"
"v_permlane16_swap_b32 v10,v11\n"
"v_max_f32 v10,v10,v11\n"
"v_mov_b32 v11,v10\n"
"s_nop 1\n"
"v_permlane32_swap_b32 v10,v11\n"
"v_max_f32 v10,v10,v11\n"
"v_add_f32 v10,0x41300000,v10\n"
"v_max_f32_e64 v10,0,v10\n"
"v_exp_f32 v11,-v10\n"
AR "v12,a0\n"
"v_subrev_f32 v12,s13,v12\n"
"v_fma_f32 v12,v12,v11,s13\n"
AW "a0,v12\n"
AR "v12,a20\n"
"v_subrev_f32 v12,s13,v12\n"
"v_fma_f32 v12,v12,v11,s13\n"
AW "a20,v12\n"
AR "v12,a40\n"
"v_subrev_f32 v12,s13,v12\n"
"v_fma_f32 v12,v12,v11,s13\n"
AW "a40,v12\n"
AR "v12,a60\n"
"v_subrev_f32 v12,s13,v12\n"
"v_fma_f32 v12,v12,v11,s13\n"
AW "a60,v12\n"
AR "v12,a80\n"
"v_subrev_f32 v12,s13,v12\n"
"v_fma_f32 v12,v12,v11,s13\n"
AW "a80,v12\n"
AR "v12,a100\n"
"v_subrev_f32 v12,s13,v12\n"
"v_fma_f32 v12,v12,v11,s13\n"
AW "a100,v12\n"
AR "v12,a120\n"
"v_subrev_f32 v12,s13,v12\n"
"v_fma_f32 v12,v12,v11,s13\n"
AW "a120,v12\n"
AR "v12,a140\n"
"v_subrev_f32 v12,s13,v12\n"
"v_fma_f32 v12,v12,v11,s13\n"
AW "a140,v12\n"
AR "v12,a160\n"
"v_subrev_f32 v12,s13,v12\n"
"v_fma_f32 v12,v12,v11,s13\n"
AW "a160,v12\n"
AR "v12,a180\n"
"v_subrev_f32 v12,s13,v12\n"
"v_fma_f32 v12,v12,v11,s13\n"
AW "a180,v12\n"
AR "v12,a200\n"
"v_subrev_f32 v12,s13,v12\n"
"v_fma_f32 v12,v12,v11,s13\n"
AW "a200,v12\n"
AR "v12,a220\n"
"v_subrev_f32 v12,s13,v12\n"
"v_fma_f32 v12,v12,v11,s13\n"
AW "a220,v12\n"
AR "v12,a1\n"
"v_subrev_f32 v12,s13,v12\n"
"v_fma_f32 v12,v12,v11,s13\n"
AW "a1,v12\n"
AR "v12,a21\n"
"v_subrev_f32 v12,s13,v12\n"
"v_fma_f32 v12,v12,v11,s13\n"
AW "a21,v12\n"
AR "v12,a41\n"
"v_subrev_f32 v12,s13,v12\n"
"v_fma_f32 v12,v12,v11,s13\n"
AW "a41,v12\n"
AR "v12,a61\n"
"v_subrev_f32 v12,s13,v12\n"
"v_fma_f32 v12,v12,v11,s13\n"
AW "a61,v12\n"
AR "v12,a81\n"
"v_subrev_f32 v12,s13,v12\n"
"v_fma_f32 v12,v12,v11,s13\n"
AW "a81,v12\n"
AR "v12,a101\n"
"v_subrev_f32 v12,s13,v12\n"
"v_fma_f32 v12,v12,v11,s13\n"
AW "a101,v12\n"
AR "v12,a121\n"
"v_subrev_f32 v12,s13,v12\n"
"v_fma_f32 v12,v12,v11,s13\n"
AW "a121,v12\n"
AR "v12,a141\n"
"v_subrev_f32 v12,s13,v12\n"
"v_fma_f32 v12,v12,v11,s13\n"
AW "a141,v12\n"
AR "v12,a161\n"
"v_subrev_f32 v12,s13,v12\n"
"v_fma_f32 v12,v12,v11,s13\n"
AW "a161,v12\n"
AR "v12,a181\n"
"v_subrev_f32 v12,s13,v12\n"
"v_fma_f32 v12,v12,v11,s13\n"
AW "a181,v12\n"
AR "v12,a201\n"
"v_subrev_f32 v12,s13,v12\n"
"v_fma_f32 v12,v12,v11,s13\n"
AW "a201,v12\n"
AR "v12,a221\n"
"v_subrev_f32 v12,s13,v12\n"
"v_fma_f32 v12,v12,v11,s13\n"
AW "a221,v12\n"
AR "v12,a2\n"
"v_subrev_f32 v12,s13,v12\n"
"v_fma_f32 v12,v12,v11,s13\n"
AW "a2,v12\n"
AR "v12,a22\n"
"v_subrev_f32 v12,s13,v12\n"
"v_fma_f32 v12,v12,v11,s13\n"
AW "a22,v12\n"
AR "v12,a42\n"
"v_subrev_f32 v12,s13,v12\n"
"v_fma_f32 v12,v12,v11,s13\n"
AW "a42,v12\n"
AR "v12,a62\n"
"v_subrev_f32 v12,s13,v12\n"
"v_fma_f32 v12,v12,v11,s13\n"
AW "a62,v12\n"
AR "v12,a82\n"
"v_subrev_f32 v12,s13,v12\n"
"v_fma_f32 v12,v12,v11,s13\n"
AW "a82,v12\n"
AR "v12,a102\n"
"v_subrev_f32 v12,s13,v12\n"
"v_fma_f32 v12,v12,v11,s13\n"
AW "a102,v12\n"
AR "v12,a122\n"
"v_subrev_f32 v12,s13,v12\n"
"v_fma_f32 v12,v12,v11,s13\n"
AW "a122,v12\n"
AR "v12,a142\n"
"v_subrev_f32 v12,s13,v12\n"
"v_fma_f32 v12,v12,v11,s13\n"
AW "a142,v12\n"
AR "v12,a162\n"
"v_subrev_f32 v12,s13,v12\n"
"v_fma_f32 v12,v12,v11,s13\n"
AW "a162,v12\n"
AR "v12,a182\n"
"v_subrev_f32 v12,s13,v12\n"
"v_fma_f32 v12,v12,v11,s13\n"
AW "a182,v12\n"
AR "v12,a202\n"
"v_subrev_f32 v12,s13,v12\n"
"v_fma_f32 v12,v12,v11,s13\n"
AW "a202,v12\n"
AR "v12,a222\n"
"v_subrev_f32 v12,s13,v12\n"
"v_fma_f32 v12,v12,v11,s13\n"
AW "a222,v12\n"
AR "v12,a3\n"
"v_subrev_f32 v12,s13,v12\n"
"v_fma_f32 v12,v12,v11,s13\n"
AW "a3,v12\n"
AR "v12,a23\n"
"v_subrev_f32 v12,s13,v12\n"
"v_fma_f32 v12,v12,v11,s13\n"
AW "a23,v12\n"
AR "v12,a43\n"
"v_subrev_f32 v12,s13,v12\n"
"v_fma_f32 v12,v12,v11,s13\n"
AW "a43,v12\n"
AR "v12,a63\n"
"v_subrev_f32 v12,s13,v12\n"
"v_fma_f32 v12,v12,v11,s13\n"
AW "a63,v12\n"
AR "v12,a83\n"
"v_subrev_f32 v12,s13,v12\n"
"v_fma_f32 v12,v12,v11,s13\n"
AW "a83,v12\n"
AR "v12,a103\n"
"v_subrev_f32 v12,s13,v12\n"
"v_fma_f32 v12,v12,v11,s13\n"
AW "a103,v12\n"
AR "v12,a123\n"
"v_subrev_f32 v12,s13,v12\n"
"v_fma_f32 v12,v12,v11,s13\n"
AW "a123,v12\n"
AR "v12,a143\n"
"v_subrev_f32 v12,s13,v12\n"
"v_fma_f32 v12,v12,v11,s13\n"
AW "a143,v12\n"
AR "v12,a163\n"
"v_subrev_f32 v12,s13,v12\n"
"v_fma_f32 v12,v12,v11,s13\n"
AW "a163,v12\n"
AR "v12,a183\n"
"v_subrev_f32 v12,s13,v12\n"
"v_fma_f32 v12,v12,v11,s13\n"
AW "a183,v12\n"
AR "v12,a203\n"
"v_subrev_f32 v12,s13,v12\n"
"v_fma_f32 v12,v12,v11,s13\n"
AW "a203,v12\n"
AR "v12,a223\n"
"v_subrev_f32 v12,s13,v12\n"
"v_fma_f32 v12,v12,v11,s13\n"
AW "a223,v12\n"
"v_mul_f32 v52,v57,v11\n"
"v_sub_f32 v32,v32,v10\n"
"v_sub_f32 v33,v33,v10\n"
"v_sub_f32 v34,v34,v10\n"
"v_sub_f32 v35,v35,v10\n"
"v_sub_f32 v92,v92,v10\n"
"v_sub_f32 v93,v93,v10\n"
"v_sub_f32 v94,v94,v10\n"
"v_sub_f32 v95,v95,v10\n"
"v_sub_f32 v72,v72,v10\n"
"v_sub_f32 v73,v73,v10\n"
"v_sub_f32 v74,v74,v10\n"
"v_sub_f32 v75,v75,v10\n"
"v_sub_f32 v112,v112,v10\n"
"v_sub_f32 v113,v113,v10\n"
"v_sub_f32 v114,v114,v10\n"
"v_sub_f32 v115,v115,v10\n"
"v_exp_f32 v72,v72\n"
"v_exp_f32 v73,v73\n"
"v_exp_f32 v74,v74\n"
"v_exp_f32 v75,v75\n"
"v_exp_f32 v112,v112\n"
"v_exp_f32 v113,v113\n"
"v_exp_f32 v114,v114\n"
"v_exp_f32 v115,v115\n"
"v_add_f32 v52,v52,v72\n"
"v_add_f32 v52,v52,v73\n"
"v_add_f32 v52,v52,v74\n"
"v_add_f32 v52,v52,v75\n"
"v_add_f32 v52,v52,v112\n"
"v_add_f32 v52,v52,v113\n"
"v_add_f32 v52,v52,v114\n"
"v_add_f32_e64 v52,v52,v115\n"
CV "v72,v72,v73\n"
CV "v73,v74,v75\n"
CV "v74,v112,v113\n"
CV "v75,v114,v115\n"
"v_max3_f32 v10,v76,v77,v78\n"
"v_max3_f32 v11,v79,v116,v117\n"
"v_max3_f32 v10,v10,v118,v119\n"
"v_max_f32 v10,v10,v11\n"
"v_mov_b32 v11,v10\n"
"s_nop 1\n"
"v_permlane16_swap_b32 v10,v11\n"
"v_max_f32 v10,v10,v11\n"
"v_mov_b32 v11,v10\n"
"s_nop 1\n"
"v_permlane32_swap_b32 v10,v11\n"
"v_max_f32 v10,v10,v11\n"
"v_add_f32 v10,0x41300000,v10\n"
"v_max_f32 v10,0,v10\n"
"v_exp_f32 v11,-v10\n"
AR "v12,a4\n"
"v_subrev_f32 v12,s13,v12\n"
"v_fma_f32 v12,v12,v11,s13\n"
AW "a4,v12\n"
AR "v12,a24\n"
"v_subrev_f32 v12,s13,v12\n"
"v_fma_f32 v12,v12,v11,s13\n"
AW "a24,v12\n"
AR "v12,a44\n"
"v_subrev_f32 v12,s13,v12\n"
"v_fma_f32 v12,v12,v11,s13\n"
AW "a44,v12\n"
AR "v12,a64\n"
"v_subrev_f32 v12,s13,v12\n"
"v_fma_f32 v12,v12,v11,s13\n"
AW "a64,v12\n"
AR "v12,a84\n"
"v_subrev_f32 v12,s13,v12\n"
"v_fma_f32 v12,v12,v11,s13\n"
AW "a84,v12\n"
AR "v12,a104\n"
"v_subrev_f32 v12,s13,v12\n"
"v_fma_f32 v12,v12,v11,s13\n"
AW "a104,v12\n"
AR "v12,a124\n"
"v_subrev_f32 v12,s13,v12\n"
"v_fma_f32 v12,v12,v11,s13\n"
AW "a124,v12\n"
AR "v12,a144\n"
"v_subrev_f32 v12,s13,v12\n"
"v_fma_f32 v12,v12,v11,s13\n"
AW "a144,v12\n"
AR "v12,a164\n"
"v_subrev_f32 v12,s13,v12\n"
"v_fma_f32 v12,v12,v11,s13\n"
AW "a164,v12\n"
AR "v12,a184\n"
"v_subrev_f32 v12,s13,v12\n"
"v_fma_f32 v12,v12,v11,s13\n"
AW "a184,v12\n"
AR "v12,a204\n"
"v_subrev_f32 v12,s13,v12\n"
"v_fma_f32 v12,v12,v11,s13\n"
AW "a204,v12\n"
AR "v12,a224\n"
"v_subrev_f32 v12,s13,v12\n"
"v_fma_f32 v12,v12,v11,s13\n"
AW "a224,v12\n"
AR "v12,a5\n"
"v_subrev_f32 v12,s13,v12\n"
"v_fma_f32 v12,v12,v11,s13\n"
AW "a5,v12\n"
AR "v12,a25\n"
"v_subrev_f32 v12,s13,v12\n"
"v_fma_f32 v12,v12,v11,s13\n"
AW "a25,v12\n"
AR "v12,a45\n"
"v_subrev_f32 v12,s13,v12\n"
"v_fma_f32 v12,v12,v11,s13\n"
AW "a45,v12\n"
AR "v12,a65\n"
"v_subrev_f32 v12,s13,v12\n"
"v_fma_f32 v12,v12,v11,s13\n"
AW "a65,v12\n"
AR "v12,a85\n"
"v_subrev_f32 v12,s13,v12\n"
"v_fma_f32 v12,v12,v11,s13\n"
AW "a85,v12\n"
AR "v12,a105\n"
"v_subrev_f32 v12,s13,v12\n"
"v_fma_f32 v12,v12,v11,s13\n"
AW "a105,v12\n"
AR "v12,a125\n"
"v_subrev_f32 v12,s13,v12\n"
"v_fma_f32 v12,v12,v11,s13\n"
AW "a125,v12\n"
AR "v12,a145\n"
"v_subrev_f32 v12,s13,v12\n"
"v_fma_f32 v12,v12,v11,s13\n"
AW "a145,v12\n"
AR "v12,a165\n"
"v_subrev_f32 v12,s13,v12\n"
"v_fma_f32 v12,v12,v11,s13\n"
AW "a165,v12\n"
AR "v12,a185\n"
"v_subrev_f32 v12,s13,v12\n"
"v_fma_f32 v12,v12,v11,s13\n"
AW "a185,v12\n"
AR "v12,a205\n"
"v_subrev_f32 v12,s13,v12\n"
"v_fma_f32 v12,v12,v11,s13\n"
AW "a205,v12\n"
AR "v12,a225\n"
"v_subrev_f32 v12,s13,v12\n"
"v_fma_f32 v12,v12,v11,s13\n"
AW "a225,v12\n"
AR "v12,a6\n"
"v_subrev_f32 v12,s13,v12\n"
"v_fma_f32 v12,v12,v11,s13\n"
AW "a6,v12\n"
AR "v12,a26\n"
"v_subrev_f32 v12,s13,v12\n"
"v_fma_f32 v12,v12,v11,s13\n"
AW "a26,v12\n"
AR "v12,a46\n"
"v_subrev_f32 v12,s13,v12\n"
"v_fma_f32 v12,v12,v11,s13\n"
AW "a46,v12\n"
AR "v12,a66\n"
"v_subrev_f32 v12,s13,v12\n"
"v_fma_f32 v12,v12,v11,s13\n"
AW "a66,v12\n"
AR "v12,a86\n"
"v_subrev_f32 v12,s13,v12\n"
"v_fma_f32 v12,v12,v11,s13\n"
AW "a86,v12\n"
AR "v12,a106\n"
"v_subrev_f32 v12,s13,v12\n"
"v_fma_f32 v12,v12,v11,s13\n"
AW "a106,v12\n"
AR "v12,a126\n"
"v_subrev_f32 v12,s13,v12\n"
"v_fma_f32 v12,v12,v11,s13\n"
AW "a126,v12\n"
AR "v12,a146\n"
"v_subrev_f32 v12,s13,v12\n"
"v_fma_f32 v12,v12,v11,s13\n"
AW "a146,v12\n"
AR "v12,a166\n"
"v_subrev_f32 v12,s13,v12\n"
"v_fma_f32 v12,v12,v11,s13\n"
AW "a166,v12\n"
AR "v12,a186\n"
"v_subrev_f32 v12,s13,v12\n"
"v_fma_f32 v12,v12,v11,s13\n"
AW "a186,v12\n"
AR "v12,a206\n"
"v_subrev_f32 v12,s13,v12\n"
"v_fma_f32 v12,v12,v11,s13\n"
AW "a206,v12\n"
AR "v12,a226\n"
"v_subrev_f32 v12,s13,v12\n"
"v_fma_f32 v12,v12,v11,s13\n"
AW "a226,v12\n"
AR "v12,a7\n"
"v_subrev_f32 v12,s13,v12\n"
"v_fma_f32 v12,v12,v11,s13\n"
AW "a7,v12\n"
AR "v12,a27\n"
"v_subrev_f32 v12,s13,v12\n"
"v_fma_f32 v12,v12,v11,s13\n"
AW "a27,v12\n"
AR "v12,a47\n"
"v_subrev_f32 v12,s13,v12\n"
"v_fma_f32 v12,v12,v11,s13\n"
AW "a47,v12\n"
AR "v12,a67\n"
"v_subrev_f32 v12,s13,v12\n"
"v_fma_f32 v12,v12,v11,s13\n"
AW "a67,v12\n"
AR "v12,a87\n"
"v_subrev_f32 v12,s13,v12\n"
"v_fma_f32 v12,v12,v11,s13\n"
AW "a87,v12\n"
AR "v12,a107\n"
"v_subrev_f32 v12,s13,v12\n"
"v_fma_f32 v12,v12,v11,s13\n"
AW "a107,v12\n"
AR "v12,a127\n"
"v_subrev_f32 v12,s13,v12\n"
"v_fma_f32 v12,v12,v11,s13\n"
AW "a127,v12\n"
AR "v12,a147\n"
"v_subrev_f32 v12,s13,v12\n"
"v_fma_f32 v12,v12,v11,s13\n"
AW "a147,v12\n"
AR "v12,a167\n"
"v_subrev_f32 v12,s13,v12\n"
"v_fma_f32 v12,v12,v11,s13\n"
AW "a167,v12\n"
AR "v12,a187\n"
"v_subrev_f32 v12,s13,v12\n"
"v_fma_f32 v12,v12,v11,s13\n"
AW "a187,v12\n"
AR "v12,a207\n"
"v_subrev_f32 v12,s13,v12\n"
"v_fma_f32 v12,v12,v11,s13\n"
AW "a207,v12\n"
AR "v12,a227\n"
"v_subrev_f32 v12,s13,v12\n"
"v_fma_f32 v12,v12,v11,s13\n"
AW "a227,v12\n"
"v_mul_f32 v53,v58,v11\n"
"v_sub_f32 v36,v36,v10\n"
"v_sub_f32 v37,v37,v10\n"
"v_sub_f32 v38,v38,v10\n"
"v_sub_f32 v39,v39,v10\n"
"v_sub_f32 v96,v96,v10\n"
"v_sub_f32 v97,v97,v10\n"
"v_sub_f32 v98,v98,v10\n"
"v_sub_f32 v99,v99,v10\n"
"v_sub_f32 v76,v76,v10\n"
"v_sub_f32 v77,v77,v10\n"
"v_sub_f32 v78,v78,v10\n"
"v_sub_f32 v79,v79,v10\n"
"v_sub_f32 v116,v116,v10\n"
"v_sub_f32 v117,v117,v10\n"
"v_sub_f32 v118,v118,v10\n"
"v_sub_f32 v119,v119,v10\n"
"v_exp_f32 v76,v76\n"
"v_exp_f32 v77,v77\n"
"v_exp_f32 v78,v78\n"
"v_exp_f32 v79,v79\n"
"v_exp_f32 v116,v116\n"
"v_exp_f32 v117,v117\n"
"v_exp_f32 v118,v118\n"
"v_exp_f32 v119,v119\n"
"v_add_f32 v53,v53,v76\n"
"v_add_f32 v53,v53,v77\n"
"v_add_f32 v53,v53,v78\n"
"v_add_f32 v53,v53,v79\n"
"v_add_f32 v53,v53,v116\n"
"v_add_f32 v53,v53,v117\n"
"v_add_f32 v53,v53,v118\n"
"v_add_f32_e64 v53,v53,v119\n"
CV "v76,v76,v77\n"
CV "v77,v78,v79\n"
CV "v78,v116,v117\n"
CV "v79,v118,v119\n"
"v_max3_f32 v10,v80,v81,v82\n"
"v_max3_f32 v11,v83,v120,v121\n"
"v_max3_f32 v10,v10,v122,v123\n"
"v_max_f32 v10,v10,v11\n"
"v_mov_b32 v11,v10\n"
"s_nop 1\n"
"v_permlane16_swap_b32 v10,v11\n"
"v_max_f32 v10,v10,v11\n"
"v_mov_b32 v11,v10\n"
"s_nop 1\n"
"v_permlane32_swap_b32 v10,v11\n"
"v_max_f32_e64 v10,v10,v11\n"
"v_add_f32 v10,0x41300000,v10\n"
"v_max_f32 v10,0,v10\n"
"v_exp_f32 v11,-v10\n"
AR "v12,a8\n"
"v_subrev_f32 v12,s13,v12\n"
"v_fma_f32 v12,v12,v11,s13\n"
AW "a8,v12\n"
AR "v12,a28\n"
"v_subrev_f32 v12,s13,v12\n"
"v_fma_f32 v12,v12,v11,s13\n"
AW "a28,v12\n"
AR "v12,a48\n"
"v_subrev_f32 v12,s13,v12\n"
"v_fma_f32 v12,v12,v11,s13\n"
AW "a48,v12\n"
AR "v12,a68\n"
"v_subrev_f32 v12,s13,v12\n"
"v_fma_f32 v12,v12,v11,s13\n"
AW "a68,v12\n"
AR "v12,a88\n"
"v_subrev_f32 v12,s13,v12\n"
"v_fma_f32 v12,v12,v11,s13\n"
AW "a88,v12\n"
AR "v12,a108\n"
"v_subrev_f32 v12,s13,v12\n"
"v_fma_f32 v12,v12,v11,s13\n"
AW "a108,v12\n"
AR "v12,a128\n"
"v_subrev_f32 v12,s13,v12\n"
"v_fma_f32 v12,v12,v11,s13\n"
AW "a128,v12\n"
AR "v12,a148\n"
"v_subrev_f32 v12,s13,v12\n"
"v_fma_f32 v12,v12,v11,s13\n"
AW "a148,v12\n"
AR "v12,a168\n"
"v_subrev_f32 v12,s13,v12\n"
"v_fma_f32 v12,v12,v11,s13\n"
AW "a168,v12\n"
AR "v12,a188\n"
"v_subrev_f32 v12,s13,v12\n"
"v_fma_f32 v12,v12,v11,s13\n"
AW "a188,v12\n"
AR "v12,a208\n"
"v_subrev_f32 v12,s13,v12\n"
"v_fma_f32 v12,v12,v11,s13\n"
AW "a208,v12\n"
AR "v12,a228\n"
"v_subrev_f32 v12,s13,v12\n"
"v_fma_f32 v12,v12,v11,s13\n"
AW "a228,v12\n"
AR "v12,a9\n"
"v_subrev_f32 v12,s13,v12\n"
"v_fma_f32 v12,v12,v11,s13\n"
AW "a9,v12\n"
AR "v12,a29\n"
"v_subrev_f32 v12,s13,v12\n"
"v_fma_f32 v12,v12,v11,s13\n"
AW "a29,v12\n"
AR "v12,a49\n"
"v_subrev_f32 v12,s13,v12\n"
"v_fma_f32 v12,v12,v11,s13\n"
AW "a49,v12\n"
AR "v12,a69\n"
"v_subrev_f32 v12,s13,v12\n"
"v_fma_f32 v12,v12,v11,s13\n"
AW "a69,v12\n"
AR "v12,a89\n"
"v_subrev_f32 v12,s13,v12\n"
"v_fma_f32 v12,v12,v11,s13\n"
AW "a89,v12\n"
AR "v12,a109\n"
"v_subrev_f32 v12,s13,v12\n"
"v_fma_f32 v12,v12,v11,s13\n"
AW "a109,v12\n"
AR "v12,a129\n"
"v_subrev_f32 v12,s13,v12\n"
"v_fma_f32 v12,v12,v11,s13\n"
AW "a129,v12\n"
AR "v12,a149\n"
"v_subrev_f32 v12,s13,v12\n"
"v_fma_f32 v12,v12,v11,s13\n"
AW "a149,v12\n"
AR "v12,a169\n"
"v_subrev_f32 v12,s13,v12\n"
"v_fma_f32 v12,v12,v11,s13\n"
AW "a169,v12\n"
AR "v12,a189\n"
"v_subrev_f32 v12,s13,v12\n"
"v_fma_f32 v12,v12,v11,s13\n"
AW "a189,v12\n"
AR "v12,a209\n"
"v_subrev_f32 v12,s13,v12\n"
"v_fma_f32 v12,v12,v11,s13\n"
AW "a209,v12\n"
AR "v12,a229\n"
"v_subrev_f32 v12,s13,v12\n"
"v_fma_f32 v12,v12,v11,s13\n"
AW "a229,v12\n"
AR "v12,a10\n"
"v_subrev_f32 v12,s13,v12\n"
"v_fma_f32 v12,v12,v11,s13\n"
AW "a10,v12\n"
AR "v12,a30\n"
"v_subrev_f32 v12,s13,v12\n"
"v_fma_f32 v12,v12,v11,s13\n"
AW "a30,v12\n"
AR "v12,a50\n"
"v_subrev_f32 v12,s13,v12\n"
"v_fma_f32 v12,v12,v11,s13\n"
AW "a50,v12\n"
AR "v12,a70\n"
"v_subrev_f32 v12,s13,v12\n"
"v_fma_f32 v12,v12,v11,s13\n"
AW "a70,v12\n"
AR "v12,a90\n"
"v_subrev_f32 v12,s13,v12\n"
"v_fma_f32 v12,v12,v11,s13\n"
AW "a90,v12\n"
AR "v12,a110\n"
"v_subrev_f32 v12,s13,v12\n"
"v_fma_f32 v12,v12,v11,s13\n"
AW "a110,v12\n"
AR "v12,a130\n"
"v_subrev_f32 v12,s13,v12\n"
"v_fma_f32 v12,v12,v11,s13\n"
AW "a130,v12\n"
AR "v12,a150\n"
"v_subrev_f32 v12,s13,v12\n"
"v_fma_f32 v12,v12,v11,s13\n"
AW "a150,v12\n"
AR "v12,a170\n"
"v_subrev_f32 v12,s13,v12\n"
"v_fma_f32 v12,v12,v11,s13\n"
AW "a170,v12\n"
AR "v12,a190\n"
"v_subrev_f32 v12,s13,v12\n"
"v_fma_f32 v12,v12,v11,s13\n"
AW "a190,v12\n"
AR "v12,a210\n"
"v_subrev_f32 v12,s13,v12\n"
"v_fma_f32 v12,v12,v11,s13\n"
AW "a210,v12\n"
AR "v12,a230\n"
"v_subrev_f32 v12,s13,v12\n"
"v_fma_f32 v12,v12,v11,s13\n"
AW "a230,v12\n"
AR "v12,a11\n"
"v_subrev_f32 v12,s13,v12\n"
"v_fma_f32 v12,v12,v11,s13\n"
AW "a11,v12\n"
AR "v12,a31\n"
"v_subrev_f32 v12,s13,v12\n"
"v_fma_f32 v12,v12,v11,s13\n"
AW "a31,v12\n"
AR "v12,a51\n"
"v_subrev_f32 v12,s13,v12\n"
"v_fma_f32 v12,v12,v11,s13\n"
AW "a51,v12\n"
AR "v12,a71\n"
"v_subrev_f32 v12,s13,v12\n"
"v_fma_f32 v12,v12,v11,s13\n"
AW "a71,v12\n"
AR "v12,a91\n"
"v_subrev_f32 v12,s13,v12\n"
"v_fma_f32 v12,v12,v11,s13\n"
AW "a91,v12\n"
AR "v12,a111\n"
"v_subrev_f32 v12,s13,v12\n"
"v_fma_f32 v12,v12,v11,s13\n"
AW "a111,v12\n"
AR "v12,a131\n"
"v_subrev_f32 v12,s13,v12\n"
"v_fma_f32 v12,v12,v11,s13\n"
AW "a131,v12\n"
AR "v12,a151\n"
"v_subrev_f32 v12,s13,v12\n"
"v_fma_f32 v12,v12,v11,s13\n"
AW "a151,v12\n"
AR "v12,a171\n"
"v_subrev_f32 v12,s13,v12\n"
"v_fma_f32 v12,v12,v11,s13\n"
AW "a171,v12\n"
AR "v12,a191\n"
"v_subrev_f32 v12,s13,v12\n"
"v_fma_f32 v12,v12,v11,s13\n"
AW "a191,v12\n"
AR "v12,a211\n"
"v_subrev_f32 v12,s13,v12\n"
"v_fma_f32 v12,v12,v11,s13\n"
AW "a211,v12\n"
AR "v12,a231\n"
"v_subrev_f32 v12,s13,v12\n"
"v_fma_f32 v12,v12,v11,s13\n"
AW "a231,v12\n"
"v_mul_f32 v54,v59,v11\n"
"v_sub_f32 v40,v40,v10\n"
"v_sub_f32 v41,v41,v10\n"
"v_sub_f32 v42,v42,v10\n"
"v_sub_f32 v43,v43,v10\n"
"v_sub_f32 v100,v100,v10\n"
"v_sub_f32 v101,v101,v10\n"
"v_sub_f32 v102,v102,v10\n"
"v_sub_f32 v103,v103,v10\n"
"v_sub_f32 v80,v80,v10\n"
"v_sub_f32 v81,v81,v10\n"
"v_sub_f32 v82,v82,v10\n"
"v_sub_f32 v83,v83,v10\n"
"v_sub_f32 v120,v120,v10\n"
"v_sub_f32 v121,v121,v10\n"
"v_sub_f32 v122,v122,v10\n"
"v_sub_f32 v123,v123,v10\n"
"v_exp_f32 v80,v80\n"
"v_exp_f32 v81,v81\n"
"v_exp_f32 v82,v82\n"
"v_exp_f32 v83,v83\n"
"v_exp_f32 v120,v120\n"
"v_exp_f32 v121,v121\n"
"v_exp_f32 v122,v122\n"
"v_exp_f32 v123,v123\n"
"v_add_f32 v54,v54,v80\n"
"v_add_f32 v54,v54,v81\n"
"v_add_f32 v54,v54,v82\n"
"v_add_f32 v54,v54,v83\n"
"v_add_f32 v54,v54,v120\n"
"v_add_f32 v54,v54,v121\n"
"v_add_f32 v54,v54,v122\n"
"v_add_f32 v54,v54,v123\n"
CV "v80,v80,v81\n"
CV "v81,v82,v83\n"
CV "v82,v120,v121\n"
CV "v83,v122,v123\n"
"v_max3_f32 v10,v84,v85,v86\n"
"v_max3_f32 v11,v87,v124,v125\n"
"v_max3_f32 v10,v10,v126,v127\n"
"v_max_f32 v10,v10,v11\n"
"v_mov_b32 v11,v10\n"
"s_nop 1\n"
"v_permlane16_swap_b32 v10,v11\n"
"v_max_f32 v10,v10,v11\n"
"v_mov_b32 v11,v10\n"
"s_nop 1\n"
"v_permlane32_swap_b32 v10,v11\n"
"v_max_f32 v10,v10,v11\n"
"v_add_f32 v10,0x41300000,v10\n"
"v_max_f32 v10,0,v10\n"
"v_exp_f32 v11,-v10\n"
AR "v12,a12\n"
"v_subrev_f32 v12,s13,v12\n"
"v_fma_f32 v12,v12,v11,s13\n"
AW "a12,v12\n"
AR "v12,a32\n"
"v_subrev_f32 v12,s13,v12\n"
"v_fma_f32 v12,v12,v11,s13\n"
AW "a32,v12\n"
AR "v12,a52\n"
"v_subrev_f32 v12,s13,v12\n"
"v_fma_f32 v12,v12,v11,s13\n"
AW "a52,v12\n"
AR "v12,a72\n"
"v_subrev_f32 v12,s13,v12\n"
"v_fma_f32 v12,v12,v11,s13\n"
AW "a72,v12\n"
AR "v12,a92\n"
"v_subrev_f32 v12,s13,v12\n"
"v_fma_f32 v12,v12,v11,s13\n"
AW "a92,v12\n"
AR "v12,a112\n"
"v_subrev_f32 v12,s13,v12\n"
"v_fma_f32 v12,v12,v11,s13\n"
AW "a112,v12\n"
AR "v12,a132\n"
"v_subrev_f32 v12,s13,v12\n"
"v_fma_f32 v12,v12,v11,s13\n"
AW "a132,v12\n"
AR "v12,a152\n"
"v_subrev_f32 v12,s13,v12\n"
"v_fma_f32 v12,v12,v11,s13\n"
AW "a152,v12\n"
AR "v12,a172\n"
"v_subrev_f32 v12,s13,v12\n"
"v_fma_f32 v12,v12,v11,s13\n"
AW "a172,v12\n"
AR "v12,a192\n"
"v_subrev_f32 v12,s13,v12\n"
"v_fma_f32 v12,v12,v11,s13\n"
AW "a192,v12\n"
AR "v12,a212\n"
"v_subrev_f32 v12,s13,v12\n"
"v_fma_f32 v12,v12,v11,s13\n"
AW "a212,v12\n"
AR "v12,a232\n"
"v_subrev_f32 v12,s13,v12\n"
"v_fma_f32 v12,v12,v11,s13\n"
AW "a232,v12\n"
AR "v12,a13\n"
"v_subrev_f32 v12,s13,v12\n"
"v_fma_f32 v12,v12,v11,s13\n"
AW "a13,v12\n"
AR "v12,a33\n"
"v_subrev_f32 v12,s13,v12\n"
"v_fma_f32 v12,v12,v11,s13\n"
AW "a33,v12\n"
AR "v12,a53\n"
"v_subrev_f32 v12,s13,v12\n"
"v_fma_f32 v12,v12,v11,s13\n"
AW "a53,v12\n"
AR "v12,a73\n"
"v_subrev_f32 v12,s13,v12\n"
"v_fma_f32 v12,v12,v11,s13\n"
AW "a73,v12\n"
AR "v12,a93\n"
"v_subrev_f32 v12,s13,v12\n"
"v_fma_f32 v12,v12,v11,s13\n"
AW "a93,v12\n"
AR "v12,a113\n"
"v_subrev_f32 v12,s13,v12\n"
"v_fma_f32 v12,v12,v11,s13\n"
AW "a113,v12\n"
AR "v12,a133\n"
"v_subrev_f32 v12,s13,v12\n"
"v_fma_f32 v12,v12,v11,s13\n"
AW "a133,v12\n"
AR "v12,a153\n"
"v_subrev_f32 v12,s13,v12\n"
"v_fma_f32 v12,v12,v11,s13\n"
AW "a153,v12\n"
AR "v12,a173\n"
"v_subrev_f32 v12,s13,v12\n"
"v_fma_f32 v12,v12,v11,s13\n"
AW "a173,v12\n"
AR "v12,a193\n"
"v_subrev_f32 v12,s13,v12\n"
"v_fma_f32 v12,v12,v11,s13\n"
AW "a193,v12\n"
AR "v12,a213\n"
"v_subrev_f32 v12,s13,v12\n"
"v_fma_f32 v12,v12,v11,s13\n"
AW "a213,v12\n"
AR "v12,a233\n"
"v_subrev_f32 v12,s13,v12\n"
"v_fma_f32 v12,v12,v11,s13\n"
AW "a233,v12\n"
AR "v12,a14\n"
"v_subrev_f32 v12,s13,v12\n"
"v_fma_f32 v12,v12,v11,s13\n"
AW "a14,v12\n"
AR "v12,a34\n"
"v_subrev_f32 v12,s13,v12\n"
"v_fma_f32 v12,v12,v11,s13\n"
AW "a34,v12\n"
AR "v12,a54\n"
"v_subrev_f32 v12,s13,v12\n"
"v_fma_f32 v12,v12,v11,s13\n"
AW "a54,v12\n"
AR "v12,a74\n"
"v_subrev_f32 v12,s13,v12\n"
"v_fma_f32 v12,v12,v11,s13\n"
AW "a74,v12\n"
AR "v12,a94\n"
"v_subrev_f32 v12,s13,v12\n"
"v_fma_f32 v12,v12,v11,s13\n"
AW "a94,v12\n"
AR "v12,a114\n"
"v_subrev_f32 v12,s13,v12\n"
"v_fma_f32 v12,v12,v11,s13\n"
AW "a114,v12\n"
AR "v12,a134\n"
"v_subrev_f32 v12,s13,v12\n"
"v_fma_f32 v12,v12,v11,s13\n"
AW "a134,v12\n"
AR "v12,a154\n"
"v_subrev_f32 v12,s13,v12\n"
"v_fma_f32 v12,v12,v11,s13\n"
AW "a154,v12\n"
AR "v12,a174\n"
"v_subrev_f32 v12,s13,v12\n"
"v_fma_f32 v12,v12,v11,s13\n"
AW "a174,v12\n"
AR "v12,a194\n"
"v_subrev_f32 v12,s13,v12\n"
"v_fma_f32 v12,v12,v11,s13\n"
AW "a194,v12\n"
AR "v12,a214\n"
"v_subrev_f32 v12,s13,v12\n"
"v_fma_f32 v12,v12,v11,s13\n"
AW "a214,v12\n"
AR "v12,a234\n"
"v_subrev_f32 v12,s13,v12\n"
"v_fma_f32 v12,v12,v11,s13\n"
AW "a234,v12\n"
AR "v12,a15\n"
"v_subrev_f32 v12,s13,v12\n"
"v_fma_f32 v12,v12,v11,s13\n"
AW "a15,v12\n"
AR "v12,a35\n"
"v_subrev_f32 v12,s13,v12\n"
"v_fma_f32 v12,v12,v11,s13\n"
AW "a35,v12\n"
AR "v12,a55\n"
"v_subrev_f32 v12,s13,v12\n"
"v_fma_f32 v12,v12,v11,s13\n"
AW "a55,v12\n"
AR "v12,a75\n"
"v_subrev_f32 v12,s13,v12\n"
"v_fma_f32 v12,v12,v11,s13\n"
AW "a75,v12\n"
AR "v12,a95\n"
"v_subrev_f32 v12,s13,v12\n"
"v_fma_f32 v12,v12,v11,s13\n"
AW "a95,v12\n"
AR "v12,a115\n"
"v_subrev_f32 v12,s13,v12\n"
"v_fma_f32 v12,v12,v11,s13\n"
AW "a115,v12\n"
AR "v12,a135\n"
"v_subrev_f32 v12,s13,v12\n"
"v_fma_f32 v12,v12,v11,s13\n"
AW "a135,v12\n"
AR "v12,a155\n"
"v_subrev_f32 v12,s13,v12\n"
"v_fma_f32 v12,v12,v11,s13\n"
AW "a155,v12\n"
AR "v12,a175\n"
"v_subrev_f32 v12,s13,v12\n"
"v_fma_f32 v12,v12,v11,s13\n"
AW "a175,v12\n"
AR "v12,a195\n"
"v_subrev_f32 v12,s13,v12\n"
"v_fma_f32 v12,v12,v11,s13\n"
AW "a195,v12\n"
AR "v12,a215\n"
"v_subrev_f32 v12,s13,v12\n"
"v_fma_f32 v12,v12,v11,s13\n"
AW "a215,v12\n"
AR "v12,a235\n"
"v_subrev_f32 v12,s13,v12\n"
"v_fma_f32 v12,v12,v11,s13\n"
AW "a235,v12\n"
"v_mul_f32 v55,v60,v11\n"
"v_sub_f32 v44,v44,v10\n"
"v_sub_f32 v45,v45,v10\n"
"v_sub_f32 v46,v46,v10\n"
"v_sub_f32 v47,v47,v10\n"
"v_sub_f32 v104,v104,v10\n"
"v_sub_f32 v105,v105,v10\n"
"v_sub_f32 v106,v106,v10\n"
"v_sub_f32 v107,v107,v10\n"
"v_sub_f32 v84,v84,v10\n"
"v_sub_f32 v85,v85,v10\n"
"v_sub_f32 v86,v86,v10\n"
"v_sub_f32 v87,v87,v10\n"
"v_sub_f32 v124,v124,v10\n"
"v_sub_f32 v125,v125,v10\n"
"v_sub_f32 v126,v126,v10\n"
"v_sub_f32 v127,v127,v10\n"
"v_exp_f32 v84,v84\n"
"v_exp_f32 v85,v85\n"
"v_exp_f32 v86,v86\n"
"v_exp_f32 v87,v87\n"
"v_exp_f32 v124,v124\n"
"v_exp_f32 v125,v125\n"
"v_exp_f32 v126,v126\n"
"v_exp_f32 v127,v127\n"
"v_add_f32 v55,v55,v84\n"
"v_add_f32 v55,v55,v85\n"
"v_add_f32 v55,v55,v86\n"
"v_add_f32 v55,v55,v87\n"
"v_add_f32 v55,v55,v124\n"
"v_add_f32 v55,v55,v125\n"
"v_add_f32 v55,v55,v126\n"
"v_add_f32_e64 v55,v55,v127\n"
CV "v84,v84,v85\n"
CV "v85,v86,v87\n"
CV "v86,v124,v125\n"
CV "v87,v126,v127\n"
"v_max3_f32 v10,v88,v89,v90\n"
"v_max3_f32 v11,v91,v128,v129\n"
"v_max3_f32 v10,v10,v130,v131\n"
"v_max_f32 v10,v10,v11\n"
"v_mov_b32 v11,v10\n"
"s_nop 1\n"
"v_permlane16_swap_b32 v10,v11\n"
"v_max_f32 v10,v10,v11\n"
"v_mov_b32 v11,v10\n"
"s_nop 1\n"
"v_permlane32_swap_b32 v10,v11\n"
"v_max_f32 v10,v10,v11\n"
"v_add_f32 v10,0x41300000,v10\n"
"v_max_f32 v10,0,v10\n"
"v_exp_f32 v11,-v10\n"
AR "v12,a16\n"
"v_subrev_f32 v12,s13,v12\n"
"v_fma_f32 v12,v12,v11,s13\n"
AW "a16,v12\n"
AR "v12,a36\n"
"v_subrev_f32 v12,s13,v12\n"
"v_fma_f32 v12,v12,v11,s13\n"
AW "a36,v12\n"
AR "v12,a56\n"
"v_subrev_f32 v12,s13,v12\n"
"v_fma_f32 v12,v12,v11,s13\n"
AW "a56,v12\n"
AR "v12,a76\n"
"v_subrev_f32 v12,s13,v12\n"
"v_fma_f32 v12,v12,v11,s13\n"
AW "a76,v12\n"
AR "v12,a96\n"
"v_subrev_f32 v12,s13,v12\n"
"v_fma_f32 v12,v12,v11,s13\n"
AW "a96,v12\n"
AR "v12,a116\n"
"v_subrev_f32 v12,s13,v12\n"
"v_fma_f32 v12,v12,v11,s13\n"
AW "a116,v12\n"
AR "v12,a136\n"
"v_subrev_f32 v12,s13,v12\n"
"v_fma_f32 v12,v12,v11,s13\n"
AW "a136,v12\n"
AR "v12,a156\n"
"v_subrev_f32 v12,s13,v12\n"
"v_fma_f32 v12,v12,v11,s13\n"
AW "a156,v12\n"
AR "v12,a176\n"
"v_subrev_f32 v12,s13,v12\n"
"v_fma_f32 v12,v12,v11,s13\n"
AW "a176,v12\n"
AR "v12,a196\n"
"v_subrev_f32 v12,s13,v12\n"
"v_fma_f32 v12,v12,v11,s13\n"
AW "a196,v12\n"
AR "v12,a216\n"
"v_subrev_f32 v12,s13,v12\n"
"v_fma_f32 v12,v12,v11,s13\n"
AW "a216,v12\n"
AR "v12,a236\n"
"v_subrev_f32 v12,s13,v12\n"
"v_fma_f32 v12,v12,v11,s13\n"
AW "a236,v12\n"
AR "v12,a17\n"
"v_subrev_f32 v12,s13,v12\n"
"v_fma_f32 v12,v12,v11,s13\n"
AW "a17,v12\n"
AR "v12,a37\n"
"v_subrev_f32 v12,s13,v12\n"
"v_fma_f32 v12,v12,v11,s13\n"
AW "a37,v12\n"
AR "v12,a57\n"
"v_subrev_f32 v12,s13,v12\n"
"v_fma_f32 v12,v12,v11,s13\n"
AW "a57,v12\n"
AR "v12,a77\n"
"v_subrev_f32 v12,s13,v12\n"
"v_fma_f32 v12,v12,v11,s13\n"
AW "a77,v12\n"
AR "v12,a97\n"
"v_subrev_f32 v12,s13,v12\n"
"v_fma_f32 v12,v12,v11,s13\n"
AW "a97,v12\n"
AR "v12,a117\n"
"v_subrev_f32 v12,s13,v12\n"
"v_fma_f32 v12,v12,v11,s13\n"
AW "a117,v12\n"
AR "v12,a137\n"
"v_subrev_f32 v12,s13,v12\n"
"v_fma_f32 v12,v12,v11,s13\n"
AW "a137,v12\n"
AR "v12,a157\n"
"v_subrev_f32 v12,s13,v12\n"
"v_fma_f32 v12,v12,v11,s13\n"
AW "a157,v12\n"
AR "v12,a177\n"
"v_subrev_f32 v12,s13,v12\n"
"v_fma_f32 v12,v12,v11,s13\n"
AW "a177,v12\n"
AR "v12,a197\n"
"v_subrev_f32 v12,s13,v12\n"
"v_fma_f32 v12,v12,v11,s13\n"
AW "a197,v12\n"
AR "v12,a217\n"
"v_subrev_f32 v12,s13,v12\n"
"v_fma_f32 v12,v12,v11,s13\n"
AW "a217,v12\n"
AR "v12,a237\n"
"v_subrev_f32 v12,s13,v12\n"
"v_fma_f32 v12,v12,v11,s13\n"
AW "a237,v12\n"
AR "v12,a18\n"
"v_subrev_f32 v12,s13,v12\n"
"v_fma_f32 v12,v12,v11,s13\n"
AW "a18,v12\n"
AR "v12,a38\n"
"v_subrev_f32 v12,s13,v12\n"
"v_fma_f32 v12,v12,v11,s13\n"
AW "a38,v12\n"
AR "v12,a58\n"
"v_subrev_f32 v12,s13,v12\n"
"v_fma_f32 v12,v12,v11,s13\n"
AW "a58,v12\n"
AR "v12,a78\n"
"v_subrev_f32 v12,s13,v12\n"
"v_fma_f32 v12,v12,v11,s13\n"
AW "a78,v12\n"
AR "v12,a98\n"
"v_subrev_f32 v12,s13,v12\n"
"v_fma_f32 v12,v12,v11,s13\n"
AW "a98,v12\n"
AR "v12,a118\n"
"v_subrev_f32 v12,s13,v12\n"
"v_fma_f32 v12,v12,v11,s13\n"
AW "a118,v12\n"
AR "v12,a138\n"
"v_subrev_f32 v12,s13,v12\n"
"v_fma_f32 v12,v12,v11,s13\n"
AW "a138,v12\n"
AR "v12,a158\n"
"v_subrev_f32 v12,s13,v12\n"
"v_fma_f32 v12,v12,v11,s13\n"
AW "a158,v12\n"
AR "v12,a178\n"
"v_subrev_f32 v12,s13,v12\n"
"v_fma_f32 v12,v12,v11,s13\n"
AW "a178,v12\n"
AR "v12,a198\n"
"v_subrev_f32 v12,s13,v12\n"
"v_fma_f32 v12,v12,v11,s13\n"
AW "a198,v12\n"
AR "v12,a218\n"
"v_subrev_f32 v12,s13,v12\n"
"v_fma_f32 v12,v12,v11,s13\n"
AW "a218,v12\n"
AR "v12,a238\n"
"v_subrev_f32 v12,s13,v12\n"
"v_fma_f32 v12,v12,v11,s13\n"
AW "a238,v12\n"
AR "v12,a19\n"
"v_subrev_f32 v12,s13,v12\n"
"v_fma_f32 v12,v12,v11,s13\n"
AW "a19,v12\n"
AR "v12,a39\n"
"v_subrev_f32 v12,s13,v12\n"
"v_fma_f32 v12,v12,v11,s13\n"
AW "a39,v12\n"
AR "v12,a59\n"
"v_subrev_f32 v12,s13,v12\n"
"v_fma_f32 v12,v12,v11,s13\n"
AW "a59,v12\n"
AR "v12,a79\n"
"v_subrev_f32 v12,s13,v12\n"
"v_fma_f32 v12,v12,v11,s13\n"
AW "a79,v12\n"
AR "v12,a99\n"
"v_subrev_f32 v12,s13,v12\n"
"v_fma_f32 v12,v12,v11,s13\n"
AW "a99,v12\n"
AR "v12,a119\n"
"v_subrev_f32 v12,s13,v12\n"
"v_fma_f32 v12,v12,v11,s13\n"
AW "a119,v12\n"
AR "v12,a139\n"
"v_subrev_f32 v12,s13,v12\n"
"v_fma_f32 v12,v12,v11,s13\n"
AW "a139,v12\n"
AR "v12,a159\n"
"v_subrev_f32 v12,s13,v12\n"
"v_fma_f32 v12,v12,v11,s13\n"
AW "a159,v12\n"
AR "v12,a179\n"
"v_subrev_f32 v12,s13,v12\n"
"v_fma_f32 v12,v12,v11,s13\n"
AW "a179,v12\n"
AR "v12,a199\n"
"v_subrev_f32 v12,s13,v12\n"
"v_fma_f32 v12,v12,v11,s13\n"
AW "a199,v12\n"
AR "v12,a219\n"
"v_subrev_f32 v12,s13,v12\n"
"v_fma_f32 v12,v12,v11,s13\n"
AW "a219,v12\n"
AR "v12,a239\n"
"v_subrev_f32 v12,s13,v12\n"
"v_fma_f32 v12,v12,v11,s13\n"
AW "a239,v12\n"
"v_mul_f32 v56,v61,v11\n"
"v_sub_f32 v48,v48,v10\n"
"v_sub_f32 v49,v49,v10\n"
"v_sub_f32 v50,v50,v10\n"
"v_sub_f32 v51,v51,v10\n"
"v_sub_f32 v108,v108,v10\n"
"v_sub_f32 v109,v109,v10\n"
"v_sub_f32 v110,v110,v10\n"
"v_sub_f32 v111,v111,v10\n"
"v_sub_f32 v88,v88,v10\n"
"v_sub_f32 v89,v89,v10\n"
"v_sub_f32 v90,v90,v10\n"
"v_sub_f32 v91,v91,v10\n"
"v_sub_f32 v128,v128,v10\n"
"v_sub_f32 v129,v129,v10\n"
"v_sub_f32 v130,v130,v10\n"
"v_sub_f32 v131,v131,v10\n"
"v_exp_f32 v88,v88\n"
"v_exp_f32 v89,v89\n"
"v_exp_f32 v90,v90\n"
"v_exp_f32 v91,v91\n"
"v_exp_f32 v128,v128\n"
"v_exp_f32 v129,v129\n"
"v_exp_f32 v130,v130\n"
"v_exp_f32 v131,v131\n"
"v_add_f32 v56,v56,v88\n"
"v_add_f32 v56,v56,v89\n"
"v_add_f32 v56,v56,v90\n"
"v_add_f32 v56,v56,v91\n"
"v_add_f32 v56,v56,v128\n"
"v_add_f32 v56,v56,v129\n"
"v_add_f32 v56,v56,v130\n"
"v_add_f32_e64 v56,v56,v131\n"
CV "v88,v88,v89\n"
CV "v89,v90,v91\n"
CV "v90,v128,v129\n"
CV "v91,v130,v131\n"
"s_nop 7\n"
"s_setpc_b64 s[52:53]\n"
".Lslow_1:\n"
"s_nop 7\n"
"s_nop 7\n"
"v_and_b32 v10,240,v9\n"
"v_mul_u32_u24 v10,24,v10\n"
"v_lshrrev_b32 v11,4,v9\n"
"v_and_or_b32 v10,v11,48,v10\n"
"s_mov_b32 s59,s34\n"
"s_mov_b32 s34,s20\n"
"s_add_u32 s58,s74,0\n"
"s_nop 3\n"
LD "v[58:61],v10,s[32:35],s58 offen\n"
"s_waitcnt vmcnt(0)\n"
MF "v[92:95],v[58:61],v[136:139],v[32:35]\n"
MF "v[96:99],v[58:61],v[140:143],v[36:39]\n"
MF "v[100:103],v[58:61],v[144:147],v[40:43]\n"
MF "v[104:107],v[58:61],v[148:151],v[44:47]\n"
MF "v[108:111],v[58:61],v[152:155],v[48:51]\n"
"s_add_u32 s58,s74,0\n"
"s_nop 3\n"
LD "v[58:61],v10,s[32:35],s58 offen offset:64\n"
"s_waitcnt vmcnt(0)\n"
MF "v[92:95],v[58:61],v[156:159],v[92:95]\n"
MF "v[96:99],v[58:61],v[160:163],v[96:99]\n"
MF "v[100:103],v[58:61],v[164:167],v[100:103]\n"
MF "v[104:107],v[58:61],v[168:171],v[104:107]\n"
MF "v[108:111],v[58:61],v[172:175],v[108:111]\n"
"s_add_u32 s58,s74,0\n"
"s_nop 3\n"
LD "v[58:61],v10,s[32:35],s58 offen offset:128\n"
"s_waitcnt vmcnt(0)\n"
MF "v[92:95],v[58:61],v[176:179],v[92:95]\n"
MF "v[96:99],v[58:61],v[180:183],v[96:99]\n"
MF "v[100:103],v[58:61],v[184:187],v[100:103]\n"
MF "v[104:107],v[58:61],v[188:191],v[104:107]\n"
MF "v[108:111],v[58:61],v[192:195],v[108:111]\n"
"s_add_u32 s58,s74,0\n"
"s_nop 3\n"
LD "v[58:61],v10,s[32:35],s58 offen offset:192\n"
"s_waitcnt vmcnt(0)\n"
MF "v[92:95],v[58:61],v[196:199],v[92:95]\n"
MF "v[96:99],v[58:61],v[200:203],v[96:99]\n"
MF "v[100:103],v[58:61],v[204:207],v[100:103]\n"
MF "v[104:107],v[58:61],v[208:211],v[104:107]\n"
MF "v[108:111],v[58:61],v[212:215],v[108:111]\n"
"s_add_u32 s58,s74,0\n"
"s_nop 3\n"
LD "v[58:61],v10,s[32:35],s58 offen offset:256\n"
"s_waitcnt vmcnt(0)\n"
MF "v[92:95],v[58:61],v[216:219],v[92:95]\n"
MF "v[96:99],v[58:61],v[220:223],v[96:99]\n"
MF "v[100:103],v[58:61],v[224:227],v[100:103]\n"
MF "v[104:107],v[58:61],v[228:231],v[104:107]\n"
MF "v[108:111],v[58:61],v[232:235],v[108:111]\n"
"s_add_u32 s58,s74,0\n"
"s_nop 3\n"
LD "v[58:61],v10,s[32:35],s58 offen offset:320\n"
"s_waitcnt vmcnt(0)\n"
MF "v[92:95],v[58:61],v[236:239],v[92:95]\n"
MF "v[96:99],v[58:61],v[240:243],v[96:99]\n"
MF "v[100:103],v[58:61],v[244:247],v[100:103]\n"
MF "v[104:107],v[58:61],v[248:251],v[104:107]\n"
MF "v[108:111],v[58:61],v[252:255],v[108:111]\n"
"s_add_u32 s58,s74,6144\n"
"s_nop 3\n"
LD "v[58:61],v10,s[32:35],s58 offen\n"
"s_waitcnt vmcnt(0)\n"
MF "v[112:115],v[58:61],v[136:139],v[32:35]\n"
MF "v[116:119],v[58:61],v[140:143],v[36:39]\n"
MF "v[120:123],v[58:61],v[144:147],v[40:43]\n"
MF "v[124:127],v[58:61],v[148:151],v[44:47]\n"
MF "v[128:131],v[58:61],v[152:155],v[48:51]\n"
"s_add_u32 s58,s74,6144\n"
"s_nop 3\n"
LD "v[58:61],v10,s[32:35],s58 offen offset:64\n"
"s_waitcnt vmcnt(0)\n"
MF "v[112:115],v[58:61],v[156:159],v[112:115]\n"
MF "v[116:119],v[58:61],v[160:163],v[116:119]\n"
MF "v[120:123],v[58:61],v[164:167],v[120:123]\n"
MF "v[124:127],v[58:61],v[168:171],v[124:127]\n"
MF "v[128:131],v[58:61],v[172:175],v[128:131]\n"
"s_add_u32 s58,s74,6144\n"
"s_nop 3\n"
LD "v[58:61],v10,s[32:35],s58 offen offset:128\n"
"s_waitcnt vmcnt(0)\n"
MF "v[112:115],v[58:61],v[176:179],v[112:115]\n"
MF "v[116:119],v[58:61],v[180:183],v[116:119]\n"
MF "v[120:123],v[58:61],v[184:187],v[120:123]\n"
MF "v[124:127],v[58:61],v[188:191],v[124:127]\n"
MF "v[128:131],v[58:61],v[192:195],v[128:131]\n"
"s_add_u32 s58,s74,6144\n"
"s_nop 3\n"
LD "v[58:61],v10,s[32:35],s58 offen offset:192\n"
"s_waitcnt vmcnt(0)\n"
MF "v[112:115],v[58:61],v[196:199],v[112:115]\n"
MF "v[116:119],v[58:61],v[200:203],v[116:119]\n"
MF "v[120:123],v[58:61],v[204:207],v[120:123]\n"
MF "v[124:127],v[58:61],v[208:211],v[124:127]\n"
MF "v[128:131],v[58:61],v[212:215],v[128:131]\n"
"s_add_u32 s58,s74,6144\n"
"s_nop 3\n"
LD "v[58:61],v10,s[32:35],s58 offen offset:256\n"
"s_waitcnt vmcnt(0)\n"
MF "v[112:115],v[58:61],v[216:219],v[112:115]\n"
MF "v[116:119],v[58:61],v[220:223],v[116:119]\n"
MF "v[120:123],v[58:61],v[224:227],v[120:123]\n"
MF "v[124:127],v[58:61],v[228:231],v[124:127]\n"
MF "v[128:131],v[58:61],v[232:235],v[128:131]\n"
"s_add_u32 s58,s74,6144\n"
"s_nop 3\n"
LD "v[58:61],v10,s[32:35],s58 offen offset:320\n"
"s_waitcnt vmcnt(0)\n"
MF "v[112:115],v[58:61],v[236:239],v[112:115]\n"
MF "v[116:119],v[58:61],v[240:243],v[116:119]\n"
MF "v[120:123],v[58:61],v[244:247],v[120:123]\n"
MF "v[124:127],v[58:61],v[248:251],v[124:127]\n"
MF "v[128:131],v[58:61],v[252:255],v[128:131]\n"
"s_nop 7\n"
"s_mov_b32 s34,s59\n"
"v_max3_f32 v10,v92,v93,v94\n"
"s_nop 0\n"
"v_max3_f32 v11,v95,v112,v113\n"
"v_max3_f32 v10,v10,v114,v115\n"
"v_max_f32 v10,v10,v11\n"
"v_mov_b32 v11,v10\n"
"s_nop 1\n"
"v_permlane16_swap_b32 v10,v11\n"
"v_max_f32 v10,v10,v11\n"
"v_mov_b32 v11,v10\n"
"s_nop 1\n"
"v_permlane32_swap_b32 v10,v11\n"
"v_max_f32 v10,v10,v11\n"
"v_add_f32 v10,0x41300000,v10\n"
"v_max_f32_e64 v10,0,v10\n"
"v_exp_f32 v11,-v10\n"
AR "v12,a0\n"
"v_subrev_f32 v12,s13,v12\n"
"v_fma_f32 v12,v12,v11,s13\n"
AW "a0,v12\n"
AR "v12,a20\n"
"v_subrev_f32 v12,s13,v12\n"
"v_fma_f32 v12,v12,v11,s13\n"
AW "a20,v12\n"
AR "v12,a40\n"
"v_subrev_f32 v12,s13,v12\n"
"v_fma_f32 v12,v12,v11,s13\n"
AW "a40,v12\n"
AR "v12,a60\n"
"v_subrev_f32 v12,s13,v12\n"
"v_fma_f32 v12,v12,v11,s13\n"
AW "a60,v12\n"
AR "v12,a80\n"
"v_subrev_f32 v12,s13,v12\n"
"v_fma_f32 v12,v12,v11,s13\n"
AW "a80,v12\n"
AR "v12,a100\n"
"v_subrev_f32 v12,s13,v12\n"
"v_fma_f32 v12,v12,v11,s13\n"
AW "a100,v12\n"
AR "v12,a120\n"
"v_subrev_f32 v12,s13,v12\n"
"v_fma_f32 v12,v12,v11,s13\n"
AW "a120,v12\n"
AR "v12,a140\n"
"v_subrev_f32 v12,s13,v12\n"
"v_fma_f32 v12,v12,v11,s13\n"
AW "a140,v12\n"
AR "v12,a160\n"
"v_subrev_f32 v12,s13,v12\n"
"v_fma_f32 v12,v12,v11,s13\n"
AW "a160,v12\n"
AR "v12,a180\n"
"v_subrev_f32 v12,s13,v12\n"
"v_fma_f32 v12,v12,v11,s13\n"
AW "a180,v12\n"
AR "v12,a200\n"
"v_subrev_f32 v12,s13,v12\n"
"v_fma_f32 v12,v12,v11,s13\n"
AW "a200,v12\n"
AR "v12,a220\n"
"v_subrev_f32 v12,s13,v12\n"
"v_fma_f32 v12,v12,v11,s13\n"
AW "a220,v12\n"
AR "v12,a1\n"
"v_subrev_f32 v12,s13,v12\n"
"v_fma_f32 v12,v12,v11,s13\n"
AW "a1,v12\n"
AR "v12,a21\n"
"v_subrev_f32 v12,s13,v12\n"
"v_fma_f32 v12,v12,v11,s13\n"
AW "a21,v12\n"
AR "v12,a41\n"
"v_subrev_f32 v12,s13,v12\n"
"v_fma_f32 v12,v12,v11,s13\n"
AW "a41,v12\n"
AR "v12,a61\n"
"v_subrev_f32 v12,s13,v12\n"
"v_fma_f32 v12,v12,v11,s13\n"
AW "a61,v12\n"
AR "v12,a81\n"
"v_subrev_f32 v12,s13,v12\n"
"v_fma_f32 v12,v12,v11,s13\n"
AW "a81,v12\n"
AR "v12,a101\n"
"v_subrev_f32 v12,s13,v12\n"
"v_fma_f32 v12,v12,v11,s13\n"
AW "a101,v12\n"
AR "v12,a121\n"
"v_subrev_f32 v12,s13,v12\n"
"v_fma_f32 v12,v12,v11,s13\n"
AW "a121,v12\n"
AR "v12,a141\n"
"v_subrev_f32 v12,s13,v12\n"
"v_fma_f32 v12,v12,v11,s13\n"
AW "a141,v12\n"
AR "v12,a161\n"
"v_subrev_f32 v12,s13,v12\n"
"v_fma_f32 v12,v12,v11,s13\n"
AW "a161,v12\n"
AR "v12,a181\n"
"v_subrev_f32 v12,s13,v12\n"
"v_fma_f32 v12,v12,v11,s13\n"
AW "a181,v12\n"
AR "v12,a201\n"
"v_subrev_f32 v12,s13,v12\n"
"v_fma_f32 v12,v12,v11,s13\n"
AW "a201,v12\n"
AR "v12,a221\n"
"v_subrev_f32 v12,s13,v12\n"
"v_fma_f32 v12,v12,v11,s13\n"
AW "a221,v12\n"
AR "v12,a2\n"
"v_subrev_f32 v12,s13,v12\n"
"v_fma_f32 v12,v12,v11,s13\n"
AW "a2,v12\n"
AR "v12,a22\n"
"v_subrev_f32 v12,s13,v12\n"
"v_fma_f32 v12,v12,v11,s13\n"
AW "a22,v12\n"
AR "v12,a42\n"
"v_subrev_f32 v12,s13,v12\n"
"v_fma_f32 v12,v12,v11,s13\n"
AW "a42,v12\n"
AR "v12,a62\n"
"v_subrev_f32 v12,s13,v12\n"
"v_fma_f32 v12,v12,v11,s13\n"
AW "a62,v12\n"
AR "v12,a82\n"
"v_subrev_f32 v12,s13,v12\n"
"v_fma_f32 v12,v12,v11,s13\n"
AW "a82,v12\n"
AR "v12,a102\n"
"v_subrev_f32 v12,s13,v12\n"
"v_fma_f32 v12,v12,v11,s13\n"
AW "a102,v12\n"
AR "v12,a122\n"
"v_subrev_f32 v12,s13,v12\n"
"v_fma_f32 v12,v12,v11,s13\n"
AW "a122,v12\n"
AR "v12,a142\n"
"v_subrev_f32 v12,s13,v12\n"
"v_fma_f32 v12,v12,v11,s13\n"
AW "a142,v12\n"
AR "v12,a162\n"
"v_subrev_f32 v12,s13,v12\n"
"v_fma_f32 v12,v12,v11,s13\n"
AW "a162,v12\n"
AR "v12,a182\n"
"v_subrev_f32 v12,s13,v12\n"
"v_fma_f32 v12,v12,v11,s13\n"
AW "a182,v12\n"
AR "v12,a202\n"
"v_subrev_f32 v12,s13,v12\n"
"v_fma_f32 v12,v12,v11,s13\n"
AW "a202,v12\n"
AR "v12,a222\n"
"v_subrev_f32 v12,s13,v12\n"
"v_fma_f32 v12,v12,v11,s13\n"
AW "a222,v12\n"
AR "v12,a3\n"
"v_subrev_f32 v12,s13,v12\n"
"v_fma_f32 v12,v12,v11,s13\n"
AW "a3,v12\n"
AR "v12,a23\n"
"v_subrev_f32 v12,s13,v12\n"
"v_fma_f32 v12,v12,v11,s13\n"
AW "a23,v12\n"
AR "v12,a43\n"
"v_subrev_f32 v12,s13,v12\n"
"v_fma_f32 v12,v12,v11,s13\n"
AW "a43,v12\n"
AR "v12,a63\n"
"v_subrev_f32 v12,s13,v12\n"
"v_fma_f32 v12,v12,v11,s13\n"
AW "a63,v12\n"
AR "v12,a83\n"
"v_subrev_f32 v12,s13,v12\n"
"v_fma_f32 v12,v12,v11,s13\n"
AW "a83,v12\n"
AR "v12,a103\n"
"v_subrev_f32 v12,s13,v12\n"
"v_fma_f32 v12,v12,v11,s13\n"
AW "a103,v12\n"
AR "v12,a123\n"
"v_subrev_f32 v12,s13,v12\n"
"v_fma_f32 v12,v12,v11,s13\n"
AW "a123,v12\n"
AR "v12,a143\n"
"v_subrev_f32 v12,s13,v12\n"
"v_fma_f32 v12,v12,v11,s13\n"
AW "a143,v12\n"
AR "v12,a163\n"
"v_subrev_f32 v12,s13,v12\n"
"v_fma_f32 v12,v12,v11,s13\n"
AW "a163,v12\n"
AR "v12,a183\n"
"v_subrev_f32 v12,s13,v12\n"
"v_fma_f32 v12,v12,v11,s13\n"
AW "a183,v12\n"
AR "v12,a203\n"
"v_subrev_f32 v12,s13,v12\n"
"v_fma_f32 v12,v12,v11,s13\n"
AW "a203,v12\n"
AR "v12,a223\n"
"v_subrev_f32 v12,s13,v12\n"
"v_fma_f32 v12,v12,v11,s13\n"
AW "a223,v12\n"
"v_mul_f32 v57,v52,v11\n"
"v_sub_f32 v32,v32,v10\n"
"v_sub_f32 v33,v33,v10\n"
"v_sub_f32 v34,v34,v10\n"
"v_sub_f32 v35,v35,v10\n"
"v_sub_f32 v72,v72,v10\n"
"v_sub_f32 v73,v73,v10\n"
"v_sub_f32 v74,v74,v10\n"
"v_sub_f32 v75,v75,v10\n"
"v_sub_f32 v92,v92,v10\n"
"v_sub_f32 v93,v93,v10\n"
"v_sub_f32 v94,v94,v10\n"
"v_sub_f32 v95,v95,v10\n"
"v_sub_f32 v112,v112,v10\n"
"v_sub_f32 v113,v113,v10\n"
"v_sub_f32 v114,v114,v10\n"
"v_sub_f32 v115,v115,v10\n"
"v_exp_f32 v92,v92\n"
"v_exp_f32 v93,v93\n"
"v_exp_f32 v94,v94\n"
"v_exp_f32 v95,v95\n"
"v_exp_f32 v112,v112\n"
"v_exp_f32 v113,v113\n"
"v_exp_f32 v114,v114\n"
"v_exp_f32 v115,v115\n"
"v_add_f32 v57,v57,v92\n"
"v_add_f32 v57,v57,v93\n"
"v_add_f32 v57,v57,v94\n"
"v_add_f32 v57,v57,v95\n"
"v_add_f32 v57,v57,v112\n"
"v_add_f32 v57,v57,v113\n"
"v_add_f32 v57,v57,v114\n"
"v_add_f32_e64 v57,v57,v115\n"
CV "v92,v92,v93\n"
CV "v93,v94,v95\n"
CV "v94,v112,v113\n"
CV "v95,v114,v115\n"
"v_max3_f32 v10,v96,v97,v98\n"
"v_max3_f32 v11,v99,v116,v117\n"
"v_max3_f32 v10,v10,v118,v119\n"
"v_max_f32 v10,v10,v11\n"
"v_mov_b32 v11,v10\n"
"s_nop 1\n"
"v_permlane16_swap_b32 v10,v11\n"
"v_max_f32 v10,v10,v11\n"
"v_mov_b32 v11,v10\n"
"s_nop 1\n"
"v_permlane32_swap_b32 v10,v11\n"
"v_max_f32 v10,v10,v11\n"
"v_add_f32 v10,0x41300000,v10\n"
"v_max_f32 v10,0,v10\n"
"v_exp_f32 v11,-v10\n"
AR "v12,a4\n"
"v_subrev_f32 v12,s13,v12\n"
"v_fma_f32 v12,v12,v11,s13\n"
AW "a4,v12\n"
AR "v12,a24\n"
"v_subrev_f32 v12,s13,v12\n"
"v_fma_f32 v12,v12,v11,s13\n"
AW "a24,v12\n"
AR "v12,a44\n"
"v_subrev_f32 v12,s13,v12\n"
"v_fma_f32 v12,v12,v11,s13\n"
AW "a44,v12\n"
AR "v12,a64\n"
"v_subrev_f32 v12,s13,v12\n"
"v_fma_f32 v12,v12,v11,s13\n"
AW "a64,v12\n"
AR "v12,a84\n"
"v_subrev_f32 v12,s13,v12\n"
"v_fma_f32 v12,v12,v11,s13\n"
AW "a84,v12\n"
AR "v12,a104\n"
"v_subrev_f32 v12,s13,v12\n"
"v_fma_f32 v12,v12,v11,s13\n"
AW "a104,v12\n"
AR "v12,a124\n"
"v_subrev_f32 v12,s13,v12\n"
"v_fma_f32 v12,v12,v11,s13\n"
AW "a124,v12\n"
AR "v12,a144\n"
"v_subrev_f32 v12,s13,v12\n"
"v_fma_f32 v12,v12,v11,s13\n"
AW "a144,v12\n"
AR "v12,a164\n"
"v_subrev_f32 v12,s13,v12\n"
"v_fma_f32 v12,v12,v11,s13\n"
AW "a164,v12\n"
AR "v12,a184\n"
"v_subrev_f32 v12,s13,v12\n"
"v_fma_f32 v12,v12,v11,s13\n"
AW "a184,v12\n"
AR "v12,a204\n"
"v_subrev_f32 v12,s13,v12\n"
"v_fma_f32 v12,v12,v11,s13\n"
AW "a204,v12\n"
AR "v12,a224\n"
"v_subrev_f32 v12,s13,v12\n"
"v_fma_f32 v12,v12,v11,s13\n"
AW "a224,v12\n"
AR "v12,a5\n"
"v_subrev_f32 v12,s13,v12\n"
"v_fma_f32 v12,v12,v11,s13\n"
AW "a5,v12\n"
AR "v12,a25\n"
"v_subrev_f32 v12,s13,v12\n"
"v_fma_f32 v12,v12,v11,s13\n"
AW "a25,v12\n"
AR "v12,a45\n"
"v_subrev_f32 v12,s13,v12\n"
"v_fma_f32 v12,v12,v11,s13\n"
AW "a45,v12\n"
AR "v12,a65\n"
"v_subrev_f32 v12,s13,v12\n"
"v_fma_f32 v12,v12,v11,s13\n"
AW "a65,v12\n"
AR "v12,a85\n"
"v_subrev_f32 v12,s13,v12\n"
"v_fma_f32 v12,v12,v11,s13\n"
AW "a85,v12\n"
AR "v12,a105\n"
"v_subrev_f32 v12,s13,v12\n"
"v_fma_f32 v12,v12,v11,s13\n"
AW "a105,v12\n"
AR "v12,a125\n"
"v_subrev_f32 v12,s13,v12\n"
"v_fma_f32 v12,v12,v11,s13\n"
AW "a125,v12\n"
AR "v12,a145\n"
"v_subrev_f32 v12,s13,v12\n"
"v_fma_f32 v12,v12,v11,s13\n"
AW "a145,v12\n"
AR "v12,a165\n"
"v_subrev_f32 v12,s13,v12\n"
"v_fma_f32 v12,v12,v11,s13\n"
AW "a165,v12\n"
AR "v12,a185\n"
"v_subrev_f32 v12,s13,v12\n"
"v_fma_f32 v12,v12,v11,s13\n"
AW "a185,v12\n"
AR "v12,a205\n"
"v_subrev_f32 v12,s13,v12\n"
"v_fma_f32 v12,v12,v11,s13\n"
AW "a205,v12\n"
AR "v12,a225\n"
"v_subrev_f32 v12,s13,v12\n"
"v_fma_f32 v12,v12,v11,s13\n"
AW "a225,v12\n"
AR "v12,a6\n"
"v_subrev_f32 v12,s13,v12\n"
"v_fma_f32 v12,v12,v11,s13\n"
AW "a6,v12\n"
AR "v12,a26\n"
"v_subrev_f32 v12,s13,v12\n"
"v_fma_f32 v12,v12,v11,s13\n"
AW "a26,v12\n"
AR "v12,a46\n"
"v_subrev_f32 v12,s13,v12\n"
"v_fma_f32 v12,v12,v11,s13\n"
AW "a46,v12\n"
AR "v12,a66\n"
"v_subrev_f32 v12,s13,v12\n"
"v_fma_f32 v12,v12,v11,s13\n"
AW "a66,v12\n"
AR "v12,a86\n"
"v_subrev_f32 v12,s13,v12\n"
"v_fma_f32 v12,v12,v11,s13\n"
AW "a86,v12\n"
AR "v12,a106\n"
"v_subrev_f32 v12,s13,v12\n"
"v_fma_f32 v12,v12,v11,s13\n"
AW "a106,v12\n"
AR "v12,a126\n"
"v_subrev_f32 v12,s13,v12\n"
"v_fma_f32 v12,v12,v11,s13\n"
AW "a126,v12\n"
AR "v12,a146\n"
"v_subrev_f32 v12,s13,v12\n"
"v_fma_f32 v12,v12,v11,s13\n"
AW "a146,v12\n"
AR "v12,a166\n"
"v_subrev_f32 v12,s13,v12\n"
"v_fma_f32 v12,v12,v11,s13\n"
AW "a166,v12\n"
AR "v12,a186\n"
"v_subrev_f32 v12,s13,v12\n"
"v_fma_f32 v12,v12,v11,s13\n"
AW "a186,v12\n"
AR "v12,a206\n"
"v_subrev_f32 v12,s13,v12\n"
"v_fma_f32 v12,v12,v11,s13\n"
AW "a206,v12\n"
AR "v12,a226\n"
"v_subrev_f32 v12,s13,v12\n"
"v_fma_f32 v12,v12,v11,s13\n"
AW "a226,v12\n"
AR "v12,a7\n"
"v_subrev_f32 v12,s13,v12\n"
"v_fma_f32 v12,v12,v11,s13\n"
AW "a7,v12\n"
AR "v12,a27\n"
"v_subrev_f32 v12,s13,v12\n"
"v_fma_f32 v12,v12,v11,s13\n"
AW "a27,v12\n"
AR "v12,a47\n"
"v_subrev_f32 v12,s13,v12\n"
"v_fma_f32 v12,v12,v11,s13\n"
AW "a47,v12\n"
AR "v12,a67\n"
"v_subrev_f32 v12,s13,v12\n"
"v_fma_f32 v12,v12,v11,s13\n"
AW "a67,v12\n"
AR "v12,a87\n"
"v_subrev_f32 v12,s13,v12\n"
"v_fma_f32 v12,v12,v11,s13\n"
AW "a87,v12\n"
AR "v12,a107\n"
"v_subrev_f32 v12,s13,v12\n"
"v_fma_f32 v12,v12,v11,s13\n"
AW "a107,v12\n"
AR "v12,a127\n"
"v_subrev_f32 v12,s13,v12\n"
"v_fma_f32 v12,v12,v11,s13\n"
AW "a127,v12\n"
AR "v12,a147\n"
"v_subrev_f32 v12,s13,v12\n"
"v_fma_f32 v12,v12,v11,s13\n"
AW "a147,v12\n"
AR "v12,a167\n"
"v_subrev_f32 v12,s13,v12\n"
"v_fma_f32 v12,v12,v11,s13\n"
AW "a167,v12\n"
AR "v12,a187\n"
"v_subrev_f32 v12,s13,v12\n"
"v_fma_f32 v12,v12,v11,s13\n"
AW "a187,v12\n"
AR "v12,a207\n"
"v_subrev_f32 v12,s13,v12\n"
"v_fma_f32 v12,v12,v11,s13\n"
AW "a207,v12\n"
AR "v12,a227\n"
"v_subrev_f32 v12,s13,v12\n"
"v_fma_f32 v12,v12,v11,s13\n"
AW "a227,v12\n"
"v_mul_f32 v58,v53,v11\n"
"v_sub_f32 v36,v36,v10\n"
"v_sub_f32 v37,v37,v10\n"
"v_sub_f32 v38,v38,v10\n"
"v_sub_f32 v39,v39,v10\n"
"v_sub_f32 v76,v76,v10\n"
"v_sub_f32 v77,v77,v10\n"
"v_sub_f32 v78,v78,v10\n"
"v_sub_f32 v79,v79,v10\n"
"v_sub_f32 v96,v96,v10\n"
"v_sub_f32 v97,v97,v10\n"
"v_sub_f32 v98,v98,v10\n"
"v_sub_f32 v99,v99,v10\n"
"v_sub_f32 v116,v116,v10\n"
"v_sub_f32 v117,v117,v10\n"
"v_sub_f32 v118,v118,v10\n"
"v_sub_f32 v119,v119,v10\n"
"v_exp_f32 v96,v96\n"
"v_exp_f32 v97,v97\n"
"v_exp_f32 v98,v98\n"
"v_exp_f32 v99,v99\n"
"v_exp_f32 v116,v116\n"
"v_exp_f32 v117,v117\n"
"v_exp_f32 v118,v118\n"
"v_exp_f32 v119,v119\n"
"v_add_f32 v58,v58,v96\n"
"v_add_f32 v58,v58,v97\n"
"v_add_f32 v58,v58,v98\n"
"v_add_f32 v58,v58,v99\n"
"v_add_f32 v58,v58,v116\n"
"v_add_f32 v58,v58,v117\n"
"v_add_f32 v58,v58,v118\n"
"v_add_f32_e64 v58,v58,v119\n"
CV "v96,v96,v97\n"
CV "v97,v98,v99\n"
CV "v98,v116,v117\n"
CV "v99,v118,v119\n"
"v_max3_f32 v10,v100,v101,v102\n"
"v_max3_f32 v11,v103,v120,v121\n"
"v_max3_f32 v10,v10,v122,v123\n"
"v_max_f32 v10,v10,v11\n"
"v_mov_b32 v11,v10\n"
"s_nop 1\n"
"v_permlane16_swap_b32 v10,v11\n"
"v_max_f32 v10,v10,v11\n"
"v_mov_b32 v11,v10\n"
"s_nop 1\n"
"v_permlane32_swap_b32 v10,v11\n"
"v_max_f32_e64 v10,v10,v11\n"
"v_add_f32 v10,0x41300000,v10\n"
"v_max_f32 v10,0,v10\n"
"v_exp_f32 v11,-v10\n"
AR "v12,a8\n"
"v_subrev_f32 v12,s13,v12\n"
"v_fma_f32 v12,v12,v11,s13\n"
AW "a8,v12\n"
AR "v12,a28\n"
"v_subrev_f32 v12,s13,v12\n"
"v_fma_f32 v12,v12,v11,s13\n"
AW "a28,v12\n"
AR "v12,a48\n"
"v_subrev_f32 v12,s13,v12\n"
"v_fma_f32 v12,v12,v11,s13\n"
AW "a48,v12\n"
AR "v12,a68\n"
"v_subrev_f32 v12,s13,v12\n"
"v_fma_f32 v12,v12,v11,s13\n"
AW "a68,v12\n"
AR "v12,a88\n"
"v_subrev_f32 v12,s13,v12\n"
"v_fma_f32 v12,v12,v11,s13\n"
AW "a88,v12\n"
AR "v12,a108\n"
"v_subrev_f32 v12,s13,v12\n"
"v_fma_f32 v12,v12,v11,s13\n"
AW "a108,v12\n"
AR "v12,a128\n"
"v_subrev_f32 v12,s13,v12\n"
"v_fma_f32 v12,v12,v11,s13\n"
AW "a128,v12\n"
AR "v12,a148\n"
"v_subrev_f32 v12,s13,v12\n"
"v_fma_f32 v12,v12,v11,s13\n"
AW "a148,v12\n"
AR "v12,a168\n"
"v_subrev_f32 v12,s13,v12\n"
"v_fma_f32 v12,v12,v11,s13\n"
AW "a168,v12\n"
AR "v12,a188\n"
"v_subrev_f32 v12,s13,v12\n"
"v_fma_f32 v12,v12,v11,s13\n"
AW "a188,v12\n"
AR "v12,a208\n"
"v_subrev_f32 v12,s13,v12\n"
"v_fma_f32 v12,v12,v11,s13\n"
AW "a208,v12\n"
AR "v12,a228\n"
"v_subrev_f32 v12,s13,v12\n"
"v_fma_f32 v12,v12,v11,s13\n"
AW "a228,v12\n"
AR "v12,a9\n"
"v_subrev_f32 v12,s13,v12\n"
"v_fma_f32 v12,v12,v11,s13\n"
AW "a9,v12\n"
AR "v12,a29\n"
"v_subrev_f32 v12,s13,v12\n"
"v_fma_f32 v12,v12,v11,s13\n"
AW "a29,v12\n"
AR "v12,a49\n"
"v_subrev_f32 v12,s13,v12\n"
"v_fma_f32 v12,v12,v11,s13\n"
AW "a49,v12\n"
AR "v12,a69\n"
"v_subrev_f32 v12,s13,v12\n"
"v_fma_f32 v12,v12,v11,s13\n"
AW "a69,v12\n"
AR "v12,a89\n"
"v_subrev_f32 v12,s13,v12\n"
"v_fma_f32 v12,v12,v11,s13\n"
AW "a89,v12\n"
AR "v12,a109\n"
"v_subrev_f32 v12,s13,v12\n"
"v_fma_f32 v12,v12,v11,s13\n"
AW "a109,v12\n"
AR "v12,a129\n"
"v_subrev_f32 v12,s13,v12\n"
"v_fma_f32 v12,v12,v11,s13\n"
AW "a129,v12\n"
AR "v12,a149\n"
"v_subrev_f32 v12,s13,v12\n"
"v_fma_f32 v12,v12,v11,s13\n"
AW "a149,v12\n"
AR "v12,a169\n"
"v_subrev_f32 v12,s13,v12\n"
"v_fma_f32 v12,v12,v11,s13\n"
AW "a169,v12\n"
AR "v12,a189\n"
"v_subrev_f32 v12,s13,v12\n"
"v_fma_f32 v12,v12,v11,s13\n"
AW "a189,v12\n"
AR "v12,a209\n"
"v_subrev_f32 v12,s13,v12\n"
"v_fma_f32 v12,v12,v11,s13\n"
AW "a209,v12\n"
AR "v12,a229\n"
"v_subrev_f32 v12,s13,v12\n"
"v_fma_f32 v12,v12,v11,s13\n"
AW "a229,v12\n"
AR "v12,a10\n"
"v_subrev_f32 v12,s13,v12\n"
"v_fma_f32 v12,v12,v11,s13\n"
AW "a10,v12\n"
AR "v12,a30\n"
"v_subrev_f32 v12,s13,v12\n"
"v_fma_f32 v12,v12,v11,s13\n"
AW "a30,v12\n"
AR "v12,a50\n"
"v_subrev_f32 v12,s13,v12\n"
"v_fma_f32 v12,v12,v11,s13\n"
AW "a50,v12\n"
AR "v12,a70\n"
"v_subrev_f32 v12,s13,v12\n"
"v_fma_f32 v12,v12,v11,s13\n"
AW "a70,v12\n"
AR "v12,a90\n"
"v_subrev_f32 v12,s13,v12\n"
"v_fma_f32 v12,v12,v11,s13\n"
AW "a90,v12\n"
AR "v12,a110\n"
"v_subrev_f32 v12,s13,v12\n"
"v_fma_f32 v12,v12,v11,s13\n"
AW "a110,v12\n"
AR "v12,a130\n"
"v_subrev_f32 v12,s13,v12\n"
"v_fma_f32 v12,v12,v11,s13\n"
AW "a130,v12\n"
AR "v12,a150\n"
"v_subrev_f32 v12,s13,v12\n"
"v_fma_f32 v12,v12,v11,s13\n"
AW "a150,v12\n"
AR "v12,a170\n"
"v_subrev_f32 v12,s13,v12\n"
"v_fma_f32 v12,v12,v11,s13\n"
AW "a170,v12\n"
AR "v12,a190\n"
"v_subrev_f32 v12,s13,v12\n"
"v_fma_f32 v12,v12,v11,s13\n"
AW "a190,v12\n"
AR "v12,a210\n"
"v_subrev_f32 v12,s13,v12\n"
"v_fma_f32 v12,v12,v11,s13\n"
AW "a210,v12\n"
AR "v12,a230\n"
"v_subrev_f32 v12,s13,v12\n"
"v_fma_f32 v12,v12,v11,s13\n"
AW "a230,v12\n"
AR "v12,a11\n"
"v_subrev_f32 v12,s13,v12\n"
"v_fma_f32 v12,v12,v11,s13\n"
AW "a11,v12\n"
AR "v12,a31\n"
"v_subrev_f32 v12,s13,v12\n"
"v_fma_f32 v12,v12,v11,s13\n"
AW "a31,v12\n"
AR "v12,a51\n"
"v_subrev_f32 v12,s13,v12\n"
"v_fma_f32 v12,v12,v11,s13\n"
AW "a51,v12\n"
AR "v12,a71\n"
"v_subrev_f32 v12,s13,v12\n"
"v_fma_f32 v12,v12,v11,s13\n"
AW "a71,v12\n"
AR "v12,a91\n"
"v_subrev_f32 v12,s13,v12\n"
"v_fma_f32 v12,v12,v11,s13\n"
AW "a91,v12\n"
AR "v12,a111\n"
"v_subrev_f32 v12,s13,v12\n"
"v_fma_f32 v12,v12,v11,s13\n"
AW "a111,v12\n"
AR "v12,a131\n"
"v_subrev_f32 v12,s13,v12\n"
"v_fma_f32 v12,v12,v11,s13\n"
AW "a131,v12\n"
AR "v12,a151\n"
"v_subrev_f32 v12,s13,v12\n"
"v_fma_f32 v12,v12,v11,s13\n"
AW "a151,v12\n"
AR "v12,a171\n"
"v_subrev_f32 v12,s13,v12\n"
"v_fma_f32 v12,v12,v11,s13\n"
AW "a171,v12\n"
AR "v12,a191\n"
"v_subrev_f32 v12,s13,v12\n"
"v_fma_f32 v12,v12,v11,s13\n"
AW "a191,v12\n"
AR "v12,a211\n"
"v_subrev_f32 v12,s13,v12\n"
"v_fma_f32 v12,v12,v11,s13\n"
AW "a211,v12\n"
AR "v12,a231\n"
"v_subrev_f32 v12,s13,v12\n"
"v_fma_f32 v12,v12,v11,s13\n"
AW "a231,v12\n"
"v_mul_f32 v59,v54,v11\n"
"v_sub_f32 v40,v40,v10\n"
"v_sub_f32 v41,v41,v10\n"
"v_sub_f32 v42,v42,v10\n"
"v_sub_f32 v43,v43,v10\n"
"v_sub_f32 v80,v80,v10\n"
"v_sub_f32 v81,v81,v10\n"
"v_sub_f32 v82,v82,v10\n"
"v_sub_f32 v83,v83,v10\n"
"v_sub_f32 v100,v100,v10\n"
"v_sub_f32 v101,v101,v10\n"
"v_sub_f32 v102,v102,v10\n"
"v_sub_f32 v103,v103,v10\n"
"v_sub_f32 v120,v120,v10\n"
"v_sub_f32 v121,v121,v10\n"
"v_sub_f32 v122,v122,v10\n"
"v_sub_f32 v123,v123,v10\n"
"v_exp_f32 v100,v100\n"
"v_exp_f32 v101,v101\n"
"v_exp_f32 v102,v102\n"
"v_exp_f32 v103,v103\n"
"v_exp_f32 v120,v120\n"
"v_exp_f32 v121,v121\n"
"v_exp_f32 v122,v122\n"
"v_exp_f32 v123,v123\n"
"v_add_f32 v59,v59,v100\n"
"v_add_f32 v59,v59,v101\n"
"v_add_f32 v59,v59,v102\n"
"v_add_f32 v59,v59,v103\n"
"v_add_f32 v59,v59,v120\n"
"v_add_f32 v59,v59,v121\n"
"v_add_f32 v59,v59,v122\n"
"v_add_f32 v59,v59,v123\n"
CV "v100,v100,v101\n"
CV "v101,v102,v103\n"
CV "v102,v120,v121\n"
CV "v103,v122,v123\n"
"v_max3_f32 v10,v104,v105,v106\n"
"v_max3_f32 v11,v107,v124,v125\n"
"v_max3_f32 v10,v10,v126,v127\n"
"v_max_f32 v10,v10,v11\n"
"v_mov_b32 v11,v10\n"
"s_nop 1\n"
"v_permlane16_swap_b32 v10,v11\n"
"v_max_f32 v10,v10,v11\n"
"v_mov_b32 v11,v10\n"
"s_nop 1\n"
"v_permlane32_swap_b32 v10,v11\n"
"v_max_f32 v10,v10,v11\n"
"v_add_f32 v10,0x41300000,v10\n"
"v_max_f32 v10,0,v10\n"
"v_exp_f32 v11,-v10\n"
AR "v12,a12\n"
"v_subrev_f32 v12,s13,v12\n"
"v_fma_f32 v12,v12,v11,s13\n"
AW "a12,v12\n"
AR "v12,a32\n"
"v_subrev_f32 v12,s13,v12\n"
"v_fma_f32 v12,v12,v11,s13\n"
AW "a32,v12\n"
AR "v12,a52\n"
"v_subrev_f32 v12,s13,v12\n"
"v_fma_f32 v12,v12,v11,s13\n"
AW "a52,v12\n"
AR "v12,a72\n"
"v_subrev_f32 v12,s13,v12\n"
"v_fma_f32 v12,v12,v11,s13\n"
AW "a72,v12\n"
AR "v12,a92\n"
"v_subrev_f32 v12,s13,v12\n"
"v_fma_f32 v12,v12,v11,s13\n"
AW "a92,v12\n"
AR "v12,a112\n"
"v_subrev_f32 v12,s13,v12\n"
"v_fma_f32 v12,v12,v11,s13\n"
AW "a112,v12\n"
AR "v12,a132\n"
"v_subrev_f32 v12,s13,v12\n"
"v_fma_f32 v12,v12,v11,s13\n"
AW "a132,v12\n"
AR "v12,a152\n"
"v_subrev_f32 v12,s13,v12\n"
"v_fma_f32 v12,v12,v11,s13\n"
AW "a152,v12\n"
AR "v12,a172\n"
"v_subrev_f32 v12,s13,v12\n"
"v_fma_f32 v12,v12,v11,s13\n"
AW "a172,v12\n"
AR "v12,a192\n"
"v_subrev_f32 v12,s13,v12\n"
"v_fma_f32 v12,v12,v11,s13\n"
AW "a192,v12\n"
AR "v12,a212\n"
"v_subrev_f32 v12,s13,v12\n"
"v_fma_f32 v12,v12,v11,s13\n"
AW "a212,v12\n"
AR "v12,a232\n"
"v_subrev_f32 v12,s13,v12\n"
"v_fma_f32 v12,v12,v11,s13\n"
AW "a232,v12\n"
AR "v12,a13\n"
"v_subrev_f32 v12,s13,v12\n"
"v_fma_f32 v12,v12,v11,s13\n"
AW "a13,v12\n"
AR "v12,a33\n"
"v_subrev_f32 v12,s13,v12\n"
"v_fma_f32 v12,v12,v11,s13\n"
AW "a33,v12\n"
AR "v12,a53\n"
"v_subrev_f32 v12,s13,v12\n"
"v_fma_f32 v12,v12,v11,s13\n"
AW "a53,v12\n"
AR "v12,a73\n"
"v_subrev_f32 v12,s13,v12\n"
"v_fma_f32 v12,v12,v11,s13\n"
AW "a73,v12\n"
AR "v12,a93\n"
"v_subrev_f32 v12,s13,v12\n"
"v_fma_f32 v12,v12,v11,s13\n"
AW "a93,v12\n"
AR "v12,a113\n"
"v_subrev_f32 v12,s13,v12\n"
"v_fma_f32 v12,v12,v11,s13\n"
AW "a113,v12\n"
AR "v12,a133\n"
"v_subrev_f32 v12,s13,v12\n"
"v_fma_f32 v12,v12,v11,s13\n"
AW "a133,v12\n"
AR "v12,a153\n"
"v_subrev_f32 v12,s13,v12\n"
"v_fma_f32 v12,v12,v11,s13\n"
AW "a153,v12\n"
AR "v12,a173\n"
"v_subrev_f32 v12,s13,v12\n"
"v_fma_f32 v12,v12,v11,s13\n"
AW "a173,v12\n"
AR "v12,a193\n"
"v_subrev_f32 v12,s13,v12\n"
"v_fma_f32 v12,v12,v11,s13\n"
AW "a193,v12\n"
AR "v12,a213\n"
"v_subrev_f32 v12,s13,v12\n"
"v_fma_f32 v12,v12,v11,s13\n"
AW "a213,v12\n"
AR "v12,a233\n"
"v_subrev_f32 v12,s13,v12\n"
"v_fma_f32 v12,v12,v11,s13\n"
AW "a233,v12\n"
AR "v12,a14\n"
"v_subrev_f32 v12,s13,v12\n"
"v_fma_f32 v12,v12,v11,s13\n"
AW "a14,v12\n"
AR "v12,a34\n"
"v_subrev_f32 v12,s13,v12\n"
"v_fma_f32 v12,v12,v11,s13\n"
AW "a34,v12\n"
AR "v12,a54\n"
"v_subrev_f32 v12,s13,v12\n"
"v_fma_f32 v12,v12,v11,s13\n"
AW "a54,v12\n"
AR "v12,a74\n"
"v_subrev_f32 v12,s13,v12\n"
"v_fma_f32 v12,v12,v11,s13\n"
AW "a74,v12\n"
AR "v12,a94\n"
"v_subrev_f32 v12,s13,v12\n"
"v_fma_f32 v12,v12,v11,s13\n"
AW "a94,v12\n"
AR "v12,a114\n"
"v_subrev_f32 v12,s13,v12\n"
"v_fma_f32 v12,v12,v11,s13\n"
AW "a114,v12\n"
AR "v12,a134\n"
"v_subrev_f32 v12,s13,v12\n"
"v_fma_f32 v12,v12,v11,s13\n"
AW "a134,v12\n"
AR "v12,a154\n"
"v_subrev_f32 v12,s13,v12\n"
"v_fma_f32 v12,v12,v11,s13\n"
AW "a154,v12\n"
AR "v12,a174\n"
"v_subrev_f32 v12,s13,v12\n"
"v_fma_f32 v12,v12,v11,s13\n"
AW "a174,v12\n"
AR "v12,a194\n"
"v_subrev_f32 v12,s13,v12\n"
"v_fma_f32 v12,v12,v11,s13\n"
AW "a194,v12\n"
AR "v12,a214\n"
"v_subrev_f32 v12,s13,v12\n"
"v_fma_f32 v12,v12,v11,s13\n"
AW "a214,v12\n"
AR "v12,a234\n"
"v_subrev_f32 v12,s13,v12\n"
"v_fma_f32 v12,v12,v11,s13\n"
AW "a234,v12\n"
AR "v12,a15\n"
"v_subrev_f32 v12,s13,v12\n"
"v_fma_f32 v12,v12,v11,s13\n"
AW "a15,v12\n"
AR "v12,a35\n"
"v_subrev_f32 v12,s13,v12\n"
"v_fma_f32 v12,v12,v11,s13\n"
AW "a35,v12\n"
AR "v12,a55\n"
"v_subrev_f32 v12,s13,v12\n"
"v_fma_f32 v12,v12,v11,s13\n"
AW "a55,v12\n"
AR "v12,a75\n"
"v_subrev_f32 v12,s13,v12\n"
"v_fma_f32 v12,v12,v11,s13\n"
AW "a75,v12\n"
AR "v12,a95\n"
"v_subrev_f32 v12,s13,v12\n"
"v_fma_f32 v12,v12,v11,s13\n"
AW "a95,v12\n"
AR "v12,a115\n"
"v_subrev_f32 v12,s13,v12\n"
"v_fma_f32 v12,v12,v11,s13\n"
AW "a115,v12\n"
AR "v12,a135\n"
"v_subrev_f32 v12,s13,v12\n"
"v_fma_f32 v12,v12,v11,s13\n"
AW "a135,v12\n"
AR "v12,a155\n"
"v_subrev_f32 v12,s13,v12\n"
"v_fma_f32 v12,v12,v11,s13\n"
AW "a155,v12\n"
AR "v12,a175\n"
"v_subrev_f32 v12,s13,v12\n"
"v_fma_f32 v12,v12,v11,s13\n"
AW "a175,v12\n"
AR "v12,a195\n"
"v_subrev_f32 v12,s13,v12\n"
"v_fma_f32 v12,v12,v11,s13\n"
AW "a195,v12\n"
AR "v12,a215\n"
"v_subrev_f32 v12,s13,v12\n"
"v_fma_f32 v12,v12,v11,s13\n"
AW "a215,v12\n"
AR "v12,a235\n"
"v_subrev_f32 v12,s13,v12\n"
"v_fma_f32 v12,v12,v11,s13\n"
AW "a235,v12\n"
"v_mul_f32 v60,v55,v11\n"
"v_sub_f32 v44,v44,v10\n"
"v_sub_f32 v45,v45,v10\n"
"v_sub_f32 v46,v46,v10\n"
"v_sub_f32 v47,v47,v10\n"
"v_sub_f32 v84,v84,v10\n"
"v_sub_f32 v85,v85,v10\n"
"v_sub_f32 v86,v86,v10\n"
"v_sub_f32 v87,v87,v10\n"
"v_sub_f32 v104,v104,v10\n"
"v_sub_f32 v105,v105,v10\n"
"v_sub_f32 v106,v106,v10\n"
"v_sub_f32 v107,v107,v10\n"
"v_sub_f32 v124,v124,v10\n"
"v_sub_f32 v125,v125,v10\n"
"v_sub_f32 v126,v126,v10\n"
"v_sub_f32 v127,v127,v10\n"
"v_exp_f32 v104,v104\n"
"v_exp_f32 v105,v105\n"
"v_exp_f32 v106,v106\n"
"v_exp_f32 v107,v107\n"
"v_exp_f32 v124,v124\n"
"v_exp_f32 v125,v125\n"
"v_exp_f32 v126,v126\n"
"v_exp_f32 v127,v127\n"
"v_add_f32 v60,v60,v104\n"
"v_add_f32 v60,v60,v105\n"
"v_add_f32 v60,v60,v106\n"
"v_add_f32 v60,v60,v107\n"
"v_add_f32 v60,v60,v124\n"
"v_add_f32 v60,v60,v125\n"
"v_add_f32 v60,v60,v126\n"
"v_add_f32_e64 v60,v60,v127\n"
CV "v104,v104,v105\n"
CV "v105,v106,v107\n"
CV "v106,v124,v125\n"
CV "v107,v126,v127\n"
"v_max3_f32 v10,v108,v109,v110\n"
"v_max3_f32 v11,v111,v128,v129\n"
"v_max3_f32 v10,v10,v130,v131\n"
"v_max_f32 v10,v10,v11\n"
"v_mov_b32 v11,v10\n"
"s_nop 1\n"
"v_permlane16_swap_b32 v10,v11\n"
"v_max_f32 v10,v10,v11\n"
"v_mov_b32 v11,v10\n"
"s_nop 1\n"
"v_permlane32_swap_b32 v10,v11\n"
"v_max_f32 v10,v10,v11\n"
"v_add_f32 v10,0x41300000,v10\n"
"v_max_f32 v10,0,v10\n"
"v_exp_f32 v11,-v10\n"
AR "v12,a16\n"
"v_subrev_f32 v12,s13,v12\n"
"v_fma_f32 v12,v12,v11,s13\n"
AW "a16,v12\n"
AR "v12,a36\n"
"v_subrev_f32 v12,s13,v12\n"
"v_fma_f32 v12,v12,v11,s13\n"
AW "a36,v12\n"
AR "v12,a56\n"
"v_subrev_f32 v12,s13,v12\n"
"v_fma_f32 v12,v12,v11,s13\n"
AW "a56,v12\n"
AR "v12,a76\n"
"v_subrev_f32 v12,s13,v12\n"
"v_fma_f32 v12,v12,v11,s13\n"
AW "a76,v12\n"
AR "v12,a96\n"
"v_subrev_f32 v12,s13,v12\n"
"v_fma_f32 v12,v12,v11,s13\n"
AW "a96,v12\n"
AR "v12,a116\n"
"v_subrev_f32 v12,s13,v12\n"
"v_fma_f32 v12,v12,v11,s13\n"
AW "a116,v12\n"
AR "v12,a136\n"
"v_subrev_f32 v12,s13,v12\n"
"v_fma_f32 v12,v12,v11,s13\n"
AW "a136,v12\n"
AR "v12,a156\n"
"v_subrev_f32 v12,s13,v12\n"
"v_fma_f32 v12,v12,v11,s13\n"
AW "a156,v12\n"
AR "v12,a176\n"
"v_subrev_f32 v12,s13,v12\n"
"v_fma_f32 v12,v12,v11,s13\n"
AW "a176,v12\n"
AR "v12,a196\n"
"v_subrev_f32 v12,s13,v12\n"
"v_fma_f32 v12,v12,v11,s13\n"
AW "a196,v12\n"
AR "v12,a216\n"
"v_subrev_f32 v12,s13,v12\n"
"v_fma_f32 v12,v12,v11,s13\n"
AW "a216,v12\n"
AR "v12,a236\n"
"v_subrev_f32 v12,s13,v12\n"
"v_fma_f32 v12,v12,v11,s13\n"
AW "a236,v12\n"
AR "v12,a17\n"
"v_subrev_f32 v12,s13,v12\n"
"v_fma_f32 v12,v12,v11,s13\n"
AW "a17,v12\n"
AR "v12,a37\n"
"v_subrev_f32 v12,s13,v12\n"
"v_fma_f32 v12,v12,v11,s13\n"
AW "a37,v12\n"
AR "v12,a57\n"
"v_subrev_f32 v12,s13,v12\n"
"v_fma_f32 v12,v12,v11,s13\n"
AW "a57,v12\n"
AR "v12,a77\n"
"v_subrev_f32 v12,s13,v12\n"
"v_fma_f32 v12,v12,v11,s13\n"
AW "a77,v12\n"
AR "v12,a97\n"
"v_subrev_f32 v12,s13,v12\n"
"v_fma_f32 v12,v12,v11,s13\n"
AW "a97,v12\n"
AR "v12,a117\n"
"v_subrev_f32 v12,s13,v12\n"
"v_fma_f32 v12,v12,v11,s13\n"
AW "a117,v12\n"
AR "v12,a137\n"
"v_subrev_f32 v12,s13,v12\n"
"v_fma_f32 v12,v12,v11,s13\n"
AW "a137,v12\n"
AR "v12,a157\n"
"v_subrev_f32 v12,s13,v12\n"
"v_fma_f32 v12,v12,v11,s13\n"
AW "a157,v12\n"
AR "v12,a177\n"
"v_subrev_f32 v12,s13,v12\n"
"v_fma_f32 v12,v12,v11,s13\n"
AW "a177,v12\n"
AR "v12,a197\n"
"v_subrev_f32 v12,s13,v12\n"
"v_fma_f32 v12,v12,v11,s13\n"
AW "a197,v12\n"
AR "v12,a217\n"
"v_subrev_f32 v12,s13,v12\n"
"v_fma_f32 v12,v12,v11,s13\n"
AW "a217,v12\n"
AR "v12,a237\n"
"v_subrev_f32 v12,s13,v12\n"
"v_fma_f32 v12,v12,v11,s13\n"
AW "a237,v12\n"
AR "v12,a18\n"
"v_subrev_f32 v12,s13,v12\n"
"v_fma_f32 v12,v12,v11,s13\n"
AW "a18,v12\n"
AR "v12,a38\n"
"v_subrev_f32 v12,s13,v12\n"
"v_fma_f32 v12,v12,v11,s13\n"
AW "a38,v12\n"
AR "v12,a58\n"
"v_subrev_f32 v12,s13,v12\n"
"v_fma_f32 v12,v12,v11,s13\n"
AW "a58,v12\n"
AR "v12,a78\n"
"v_subrev_f32 v12,s13,v12\n"
"v_fma_f32 v12,v12,v11,s13\n"
AW "a78,v12\n"
AR "v12,a98\n"
"v_subrev_f32 v12,s13,v12\n"
"v_fma_f32 v12,v12,v11,s13\n"
AW "a98,v12\n"
AR "v12,a118\n"
"v_subrev_f32 v12,s13,v12\n"
"v_fma_f32 v12,v12,v11,s13\n"
AW "a118,v12\n"
AR "v12,a138\n"
"v_subrev_f32 v12,s13,v12\n"
"v_fma_f32 v12,v12,v11,s13\n"
AW "a138,v12\n"
AR "v12,a158\n"
"v_subrev_f32 v12,s13,v12\n"
"v_fma_f32 v12,v12,v11,s13\n"
AW "a158,v12\n"
AR "v12,a178\n"
"v_subrev_f32 v12,s13,v12\n"
"v_fma_f32 v12,v12,v11,s13\n"
AW "a178,v12\n"
AR "v12,a198\n"
"v_subrev_f32 v12,s13,v12\n"
"v_fma_f32 v12,v12,v11,s13\n"
AW "a198,v12\n"
AR "v12,a218\n"
"v_subrev_f32 v12,s13,v12\n"
"v_fma_f32 v12,v12,v11,s13\n"
AW "a218,v12\n"
AR "v12,a238\n"
"v_subrev_f32 v12,s13,v12\n"
"v_fma_f32 v12,v12,v11,s13\n"
AW "a238,v12\n"
AR "v12,a19\n"
"v_subrev_f32 v12,s13,v12\n"
"v_fma_f32 v12,v12,v11,s13\n"
AW "a19,v12\n"
AR "v12,a39\n"
"v_subrev_f32 v12,s13,v12\n"
"v_fma_f32 v12,v12,v11,s13\n"
AW "a39,v12\n"
AR "v12,a59\n"
"v_subrev_f32 v12,s13,v12\n"
"v_fma_f32 v12,v12,v11,s13\n"
AW "a59,v12\n"
AR "v12,a79\n"
"v_subrev_f32 v12,s13,v12\n"
"v_fma_f32 v12,v12,v11,s13\n"
AW "a79,v12\n"
AR "v12,a99\n"
"v_subrev_f32 v12,s13,v12\n"
"v_fma_f32 v12,v12,v11,s13\n"
AW "a99,v12\n"
AR "v12,a119\n"
"v_subrev_f32 v12,s13,v12\n"
"v_fma_f32 v12,v12,v11,s13\n"
AW "a119,v12\n"
AR "v12,a139\n"
"v_subrev_f32 v12,s13,v12\n"
"v_fma_f32 v12,v12,v11,s13\n"
AW "a139,v12\n"
AR "v12,a159\n"
"v_subrev_f32 v12,s13,v12\n"
"v_fma_f32 v12,v12,v11,s13\n"
AW "a159,v12\n"
AR "v12,a179\n"
"v_subrev_f32 v12,s13,v12\n"
"v_fma_f32 v12,v12,v11,s13\n"
AW "a179,v12\n"
AR "v12,a199\n"
"v_subrev_f32 v12,s13,v12\n"
"v_fma_f32 v12,v12,v11,s13\n"
AW "a199,v12\n"
AR "v12,a219\n"
"v_subrev_f32 v12,s13,v12\n"
"v_fma_f32 v12,v12,v11,s13\n"
AW "a219,v12\n"
AR "v12,a239\n"
"v_subrev_f32 v12,s13,v12\n"
"v_fma_f32 v12,v12,v11,s13\n"
AW "a239,v12\n"
"v_mul_f32 v61,v56,v11\n"
"v_sub_f32 v48,v48,v10\n"
"v_sub_f32 v49,v49,v10\n"
"v_sub_f32 v50,v50,v10\n"
"v_sub_f32 v51,v51,v10\n"
"v_sub_f32 v88,v88,v10\n"
"v_sub_f32 v89,v89,v10\n"
"v_sub_f32 v90,v90,v10\n"
"v_sub_f32 v91,v91,v10\n"
"v_sub_f32 v108,v108,v10\n"
"v_sub_f32 v109,v109,v10\n"
"v_sub_f32 v110,v110,v10\n"
"v_sub_f32 v111,v111,v10\n"
"v_sub_f32 v128,v128,v10\n"
"v_sub_f32 v129,v129,v10\n"
"v_sub_f32 v130,v130,v10\n"
"v_sub_f32 v131,v131,v10\n"
"v_exp_f32 v108,v108\n"
"v_exp_f32 v109,v109\n"
"v_exp_f32 v110,v110\n"
"v_exp_f32 v111,v111\n"
"v_exp_f32 v128,v128\n"
"v_exp_f32 v129,v129\n"
"v_exp_f32 v130,v130\n"
"v_exp_f32 v131,v131\n"
"v_add_f32 v61,v61,v108\n"
"v_add_f32 v61,v61,v109\n"
"v_add_f32 v61,v61,v110\n"
"v_add_f32 v61,v61,v111\n"
"v_add_f32 v61,v61,v128\n"
"v_add_f32 v61,v61,v129\n"
"v_add_f32 v61,v61,v130\n"
"v_add_f32_e64 v61,v61,v131\n"
CV "v108,v108,v109\n"
CV "v109,v110,v111\n"
CV "v110,v128,v129\n"
CV "v111,v130,v131\n"
"s_nop 7\n"
"s_setpc_b64 s[52:53]\n"
".Ldone:\n"
    : "+{v0}"(tid), "+{s23}"(bid), "+{s[20:21]}"(ka)
    :
    : "v1", "v2", "v3", "v4", "v5", "v6", "v7", "v8", "v9", "v10", "v11", "v12", "v13", "v14", "v15", "v16", "v17", "v18", "v19", "v20", "v21", "v22", "v23", "v24", "v25", "v26", "v27", "v28", "v29", "v30", "v31", "v32", "v33", "v34", "v35", "v36", "v37", "v38", "v39", "v40", "v41", "v42", "v43", "v44", "v45", "v46", "v47", "v48", "v49", "v50", "v51", "v52", "v53", "v54", "v55", "v56", "v57", "v58", "v59", "v60", "v61", "v62", "v63", "v64", "v65", "v66", "v67", "v68", "v69", "v70", "v71", "v72", "v73", "v74", "v75", "v76", "v77", "v78", "v79", "v80", "v81", "v82", "v83", "v84", "v85", "v86", "v87", "v88", "v89", "v90", "v91", "v92", "v93", "v94", "v95", "v96", "v97", "v98", "v99", "v100", "v101", "v102", "v103", "v104", "v105", "v106", "v107", "v108", "v109", "v110", "v111", "v112", "v113", "v114", "v115", "v116", "v117", "v118", "v119", "v120", "v121", "v122", "v123", "v124", "v125", "v126", "v127", "v128", "v129", "v130", "v131", "v132", "v133", "v134", "v135", "v136", "v137", "v138", "v139", "v140", "v141", "v142", "v143", "v144", "v145", "v146", "v147", "v148", "v149", "v150", "v151", "v152", "v153", "v154", "v155", "v156", "v157", "v158", "v159", "v160", "v161", "v162", "v163", "v164", "v165", "v166", "v167", "v168", "v169", "v170", "v171", "v172", "v173", "v174", "v175", "v176", "v177", "v178", "v179", "v180", "v181", "v182", "v183", "v184", "v185", "v186", "v187", "v188", "v189", "v190", "v191", "v192", "v193", "v194", "v195", "v196", "v197", "v198", "v199", "v200", "v201", "v202", "v203", "v204", "v205", "v206", "v207", "v208", "v209", "v210", "v211", "v212", "v213", "v214", "v215", "v216", "v217", "v218", "v219", "v220", "v221", "v222", "v223", "v224", "v225", "v226", "v227", "v228", "v229", "v230", "v231", "v232", "v233", "v234", "v235", "v236", "v237", "v238", "v239", "v240", "v241", "v242", "v243", "v244", "v245", "v246", "v247", "v248", "v249", "v250", "v251", "v252", "v253", "v254", "v255", "a0", "a1", "a2", "a3", "a4", "a5", "a6", "a7", "a8", "a9", "a10", "a11", "a12", "a13", "a14", "a15", "a16", "a17", "a18", "a19", "a20", "a21", "a22", "a23", "a24", "a25", "a26", "a27", "a28", "a29", "a30", "a31", "a32", "a33", "a34", "a35", "a36", "a37", "a38", "a39", "a40", "a41", "a42", "a43", "a44", "a45", "a46", "a47", "a48", "a49", "a50", "a51", "a52", "a53", "a54", "a55", "a56", "a57", "a58", "a59", "a60", "a61", "a62", "a63", "a64", "a65", "a66", "a67", "a68", "a69", "a70", "a71", "a72", "a73", "a74", "a75", "a76", "a77", "a78", "a79", "a80", "a81", "a82", "a83", "a84", "a85", "a86", "a87", "a88", "a89", "a90", "a91", "a92", "a93", "a94", "a95", "a96", "a97", "a98", "a99", "a100", "a101", "a102", "a103", "a104", "a105", "a106", "a107", "a108", "a109", "a110", "a111", "a112", "a113", "a114", "a115", "a116", "a117", "a118", "a119", "a120", "a121", "a122", "a123", "a124", "a125", "a126", "a127", "a128", "a129", "a130", "a131", "a132", "a133", "a134", "a135", "a136", "a137", "a138", "a139", "a140", "a141", "a142", "a143", "a144", "a145", "a146", "a147", "a148", "a149", "a150", "a151", "a152", "a153", "a154", "a155", "a156", "a157", "a158", "a159", "a160", "a161", "a162", "a163", "a164", "a165", "a166", "a167", "a168", "a169", "a170", "a171", "a172", "a173", "a174", "a175", "a176", "a177", "a178", "a179", "a180", "a181", "a182", "a183", "a184", "a185", "a186", "a187", "a188", "a189", "a190", "a191", "a192", "a193", "a194", "a195", "a196", "a197", "a198", "a199", "a200", "a201", "a202", "a203", "a204", "a205", "a206", "a207", "a208", "a209", "a210", "a211", "a212", "a213", "a214", "a215", "a216", "a217", "a218", "a219", "a220", "a221", "a222", "a223", "a224", "a225", "a226", "a227", "a228", "a229", "a230", "a231", "a232", "a233", "a234", "a235", "a236", "a237", "a238", "a239", "a240", "a241", "a242", "a243", "a244", "a245", "a246", "a247", "a248", "a249", "a250", "a251", "a252", "a253", "a254", "a255", "s0", "s1", "s2", "s3", "s4", "s5", "s6", "s7", "s8", "s9", "s10", "s11", "s12", "s13", "s14", "s15", "s16", "s17", "s18", "s19", "s22", "s24", "s25", "s26", "s27", "s28", "s29", "s30", "s31", "s32", "s33", "s34", "s35", "s36", "s37", "s38", "s39", "s40", "s41", "s42", "s43", "s44", "s45", "s46", "s47", "s48", "s49", "s50", "s51", "s52", "s53", "s54", "s55", "s56", "s57", "s58", "s59", "s60", "s61", "s62", "s63", "s64", "s65", "s66", "s67", "s68", "s69", "s70", "s71", "s72", "s73", "s74", "s75", "s76", "s77", "s78", "s79", "s80", "s81", "s82", "s83", "s84", "s85", "s86", "s87", "s88", "s89", "s90", "s91", "s92", "s93", "s94", "s95", "s96", "s97", "s98", "s99", "s100", "s101", "vcc", "scc", "memory");
}

extern "C" void kernel_launch(void* const* d_in, const int* in_sizes, int n_in, void* d_out, int out_size, void* d_ws, size_t ws_size, hipStream_t stream) {
  Ptrs P;
  for (int br = 0; br < 2; ++br) { P.x[br] = d_in[5 * br]; P.k[br] = d_in[5 * br + 1]; P.v[br] = d_in[5 * br + 2]; P.wq[br] = d_in[5 * br + 3]; P.wo[br] = d_in[5 * br + 4]; }
  (void)hipFuncSetAttribute((const void*)fused_kernel, hipFuncAttributeMaxDynamicSharedMemorySize, LDS1);
  fused_kernel<<<256, 256, LDS1, stream>>>(P, (char*)d_ws, (char*)d_out);
}
```

```cpp
#include <hip/hip_runtime.h>
#include <hip/hip_bf16.h>

#define MF "v_mfma_f32_16x16x32_bf16 "
#define TR "ds_read_b64_tr_b16 "
#define AR "v_accvgpr_read_b32 "
#define AW "v_accvgpr_write_b32 "
#define CV "v_cvt_pk_bf16_f32 "
#define LD "buffer_load_dwordx4 "
constexpr unsigned LDS1 = 160 * 1024;

struct Ptrs { const void* x[2]; const void* k[2]; const void* v[2]; const void* wq[2]; const void* wo[2]; };

__global__ __launch_bounds__(256, 1) __attribute__((aligned(4096))) void fused_kernel(Ptrs P, char* __restrict__ ws, char* __restrict__ out) {
  unsigned tid = threadIdx.x, bid = blockIdx.x;
  unsigned long long ka = (unsigned long long)__builtin_amdgcn_kernarg_segment_ptr();
  asm volatile(
".p2align 5\n"
".Lcode0:\n"
"s_and_b32 s58,s23,4\n"
"s_lshl_b32 s58,s58,1\n"
"s_load_dwordx2 s[28:29],s[20:21],s58 offset:48\n"
"s_load_dwordx2 s[24:25],s[20:21],s58\n"
"s_load_dwordx2 s[32:33],s[20:21],s58 offset:16\n"
"s_load_dwordx2 s[36:37],s[20:21],s58 offset:32\n"
"s_load_dwordx2 s[48:49],s[20:21],s58 offset:64\n"
"s_memtime s[100:101]\n"
"s_load_dwordx2 s[40:41],s[20:21],0x50\n"
"s_load_dwordx2 s[44:45],s[20:21],0x58\n"
"v_readfirstlane_b32 s22,v0\n"
"v_and_b32 v9,63,v0\n"
"v_bfe_u32 v10,v0,3,3\n"
"v_and_b32 v11,7,v0\n"
"s_lshr_b32 s22,s22,6\n"
"s_or_b32 s22,s22,s58\n"
"s_and_b32 s58,s23,1\n"
"s_lshl_b32 s58,s58,3\n"
"s_lshr_b32 s59,s23,5\n"
"s_add_u32 s59,s59,s58\n"
"s_mul_i32 s59,s59,4112\n"
"s_or_b32 s22,s22,s59\n"
"s_and_b32 s59,s23,2\n"
"s_lshl_b32 s59,s59,1\n"
"s_bfe_u32 s60,s23,0x20003\n"
"s_or_b32 s59,s59,s60\n"
"s_lshl_b32 s60,s59,20\n"
"s_or_b32 s22,s22,s60\n"
"s_or_b32 s59,s59,s58\n"
"s_mul_i32 s59,s59,65792\n"
"s_xor_b32 s59,s59,524288\n"
"s_or_b32 s22,s22,s59\n"
"v_lshrrev_b32 v6,2,v0\n"
"v_and_b32 v6,4,v6\n"
"v_xor_b32 v6,v6,v11\n"
"v_lshlrev_b32 v6,4,v6\n"
"s_mov_b32 s58,12288\n"
"v_mad_u32_u24 v6,v10,s58,v6\n"
"v_bfe_u32 v4,v0,4,3\n"
"v_xor_b32 v4,v4,v11\n"
"v_lshlrev_b32 v4,4,v4\n"
"s_mov_b32 s58,1280\n"
"v_mad_u32_u24 v4,v10,s58,v4\n"
"v_lshlrev_b32 v9,4,v9\n"
"s_mov_b32 s26,3276800\n"
"s_mov_b32 s27,0x00020000\n"
"s_mov_b32 s30,7864320\n"
"s_mov_b32 s31,0x00020000\n"
"s_mov_b32 s34,0\n"
"s_mov_b32 s35,0x00020000\n"
"s_mov_b32 s38,0\n"
"s_mov_b32 s39,0x00020000\n"
"s_mov_b32 s42,268435456\n"
"s_mov_b32 s43,0x00020000\n"
"s_mov_b32 s46,6553600\n"
"s_mov_b32 s47,0x00020000\n"
"s_mov_b32 s50,7864320\n"
"s_mov_b32 s51,0x00020000\n"
"s_mov_b32 s98,20000\n"
"s_bfe_u32 s56,s22,0x20000\n"
"s_bfe_u32 s72,s22,0x10003\n"
"s_lshr_b32 s73,s56,1\n"
"s_and_b32 s74,s56,1\n"
"s_mul_i32 s62,s56,4096\n"
"s_mul_i32 s63,s56,5120\n"
"s_add_u32 s63,s63,98304\n"
"s_bfe_u32 s58,s22,0x40004\n"
"s_bfe_u32 s59,s22,0x40008\n"
"s_mul_i32 s68,s58,204800\n"
"s_mul_i32 s60,s56,51200\n"
"s_add_u32 s68,s68,s60\n"
"s_lshl_b32 s59,s59,1\n"
"s_add_u32 s59,s59,s73\n"
"s_mul_i32 s66,s59,384\n"
"s_mul_i32 s60,s74,393216\n"
"s_add_u32 s66,s66,s60\n"
"s_lshr_b32 s58,s22,6\n"
"s_xor_b32 s58,s58,s22\n"
"s_bfe_u32 s58,s58,0x10004\n"
"s_mul_i32 s81,s58,640\n"
"s_mul_i32 s60,s81,6144\n"
"s_add_u32 s66,s66,s60\n"
"v_xor_b32 v19,64,v4\n"
"v_add_u32 v16,97280,v6\n"
"v_add_u32 v17,194560,v6\n"
"v_add_u32 v18,291840,v6\n"
"s_waitcnt lgkmcnt(0)\n"
"s_mov_b32 s80,s100\n"
"s_mov_b32 s87,2\n"
"s_add_u32 m0,s62,0\n"
"s_add_u32 s82,s66,0\n"
"s_sub_u32 s15,s82,7864320\n"
"s_min_u32 s82,s82,s15\n"
LD "v6,s[28:31],s82 offen lds\n"
LD "v16,s[28:31],s82 offen offset:1024 lds\n"
LD "v17,s[28:31],s82 offen offset:2048 lds\n"
LD "v18,s[28:31],s82 offen offset:3072 lds\n"
"s_add_u32 s14,s81,0\n"
"s_sub_u32 s15,s14,1280\n"
"s_min_u32 s14,s14,s15\n"
"s_add_u32 s14,s14,s68\n"
"s_add_u32 s84,s14,0\n"
"s_add_u32 m0,s63,0\n"
"s_nop 0\n"
LD "v4,s[24:27],s84 offen lds\n"
"s_add_u32 s84,s14,9216\n"
LD "v19,s[24:27],s84 offen offset:1024 lds\n"
"s_add_u32 s84,s14,18432\n"
LD "v4,s[24:27],s84 offen offset:2048 lds\n"
"s_add_u32 s84,s14,27648\n"
LD "v19,s[24:27],s84 offen offset:3072 lds\n"
"s_add_u32 s84,s14,40960\n"
"s_add_u32 m0,s63,4096\n"
"s_nop 0\n"
LD "v4,s[24:27],s84 offen lds\n"
"s_lshr_b32 s58,s23,3\n"
"s_mul_i32 s59,s58,7\n"
"s_and_b32 s60,s23,7\n"
"s_mul_i32 s60,s60,3\n"
"s_add_u32 s59,s59,s60\n"
"s_and_b32 s59,s59,15\n"
"s_lshl_b32 s59,s59,8\n"
"s_or_b32 s58,s58,s59\n"
"s_and_b32 s59,s23,3\n"
"s_lshl_b32 s59,s59,5\n"
"s_or_b32 s58,s58,s59\n"
"s_bfe_u32 s59,s23,0x10001\n"
"s_lshl_b32 s59,s59,12\n"
"s_or_b32 s58,s58,s59\n"
"s_lshl_b32 s23,s23,16\n"
"s_or_b32 s23,s23,s58\n"
"v_bfe_u32 v20,v0,4,2\n"
"v_mul_u32_u24 v21,384,v10\n"
"v_xor_b32 v22,v11,v20\n"
"s_add_u32 m0,s62,16384\n"
"s_add_u32 s82,s66,128\n"
"s_sub_u32 s15,s82,7864320\n"
"s_min_u32 s82,s82,s15\n"
LD "v6,s[28:31],s82 offen lds\n"
"v_lshl_add_u32 v7,v22,4,v21\n"
"s_lshl_b32 s58,s74,6\n"
"v_xor_b32 v7,s58,v7\n"
"v_lshlrev_b32 v22,1,v20\n"
"v_xor_b32 v22,v11,v22\n"
"v_lshl_add_u32 v8,v22,4,v21\n"
"v_bfe_u32 v21,v0,1,3\n"
"v_xor_b32 v21,v21,v20\n"
"v_lshlrev_b32 v21,4,v21\n"
"v_lshl_add_u32 v21,v11,7,v21\n"
"v_bfe_u32 v22,v0,3,1\n"
"s_mul_i32 s58,s73,12288\n"
"v_mul_u32_u24 v23,3072,v22\n"
"v_add3_u32 v1,v23,v21,s58\n"
"s_mul_i32 s59,s74,10240\n"
"s_add_u32 s59,s59,98304\n"
"v_lshl_add_u32 v2,v22,10,v21\n"
"v_add_u32_e64 v2,s59,v2\n"
"v_bfe_u32 v21,v0,2,3\n"
LD "v16,s[28:31],s82 offen offset:1024 lds\n"
"v_lshlrev_b32_e64 v21,7,v21\n"
"v_bfe_u32 v23,v0,3,2\n"
"v_lshl_add_u32 v21,v23,5,v21\n"
"v_and_b32_e64 v23,3,v0\n"
"v_lshl_add_u32 v21,v23,3,v21\n"
"v_bfe_u32 v23,v0,5,1\n"
"v_mul_u32_u24 v3,3072,v23\n"
"v_add3_u32 v3,v3,v21,s58\n"
"s_bfe_u32 s58,s22,0x40004\n"
"s_mul_i32 s58,s58,409600\n"
"s_mul_i32 s59,s72,1280\n"
"s_add_u32 s58,s58,s59\n"
"v_and_b32 v21,15,v0\n"
"v_mul_u32_u24 v21,2560,v21\n"
"v_lshl_add_u32 v21,v23,4,v21\n"
"v_bfe_u32 v23,v0,4,1\n"
"v_lshl_add_u32 v21,v23,5,v21\n"
"v_add_u32_e64 v12,s58,v21\n"
"v_bfe_u32 v21,v0,2,2\n"
LD "v17,s[28:31],s82 offen offset:2048 lds\n"
"v_lshlrev_b32 v21,7,v21\n"
"v_lshl_add_u32 v21,v20,10,v21\n"
"v_lshl_add_u32 v21,v22,6,v21\n"
"v_and_b32 v23,3,v0\n"
"v_lshl_add_u32 v21,v23,4,v21\n"
"s_lshl_b32 s58,s73,13\n"
"v_add_u32 v0,s58,v21\n"
"s_getpc_b64 s[52:53]\n"
".Lcts:\n"
"s_sub_u32 s52,s52,.Lcts-.Lcode0\n"
"s_subb_u32 s53,s53,0\n"
"s_bfe_u32 s58,s23,0x30000\n"
"s_lshl_b32 s58,s58,2\n"
"s_add_u32 s58,s58,s56\n"
"s_lshl_b32 s58,s58,13\n"
"v_lshlrev_b32 v15,3,v9\n"
"v_add_u32_e64 v15,s58,v15\n"
"v_min_u32 v15,.Lcode1-.Lcode0,v15\n"
LD "v18,s[28:31],s82 offen offset:3072 lds\n"
"s_bfe_u32 s58,s22,0x4000c\n"
"s_bfe_u32 s59,s22,0x40010\n"
"s_mul_i32 s69,s58,204800\n"
"s_mul_i32 s60,s56,51200\n"
"s_add_u32 s69,s69,s60\n"
"s_lshl_b32 s59,s59,1\n"
"s_add_u32 s59,s59,s73\n"
"s_mul_i32 s67,s59,384\n"
"s_mul_i32 s60,s74,393216\n"
"s_add_u32 s67,s67,s60\n"
"s_mul_i32 s60,s81,6144\n"
"s_add_u32 s67,s67,s60\n"
"s_bfe_u32 s58,s22,0x40004\n"
"s_bfe_u32 s59,s22,0x40008\n"
"s_lshl_b32 s59,s59,1\n"
"s_add_u32 s88,s59,s74\n"
"s_mul_i32 s88,s88,245760\n"
"s_add_u32 s59,s59,s73\n"
"s_lshl_b32 s70,s58,5\n"
"s_add_u32 m0,s62,32768\n"
"s_add_u32 s82,s66,256\n"
"s_sub_u32 s15,s82,7864320\n"
"s_min_u32 s82,s82,s15\n"
LD "v6,s[28:31],s82 offen lds\n"
"s_add_u32 s70,s70,s59\n"
"s_mul_i32 s70,s70,196608\n"
"s_bfe_u32 s58,s22,0x4000c\n"
"s_bfe_u32 s59,s22,0x40010\n"
"s_lshl_b32 s59,s59,1\n"
"s_add_u32 s89,s59,s74\n"
"s_mul_i32 s89,s89,245760\n"
"s_add_u32 s59,s59,s73\n"
"s_lshl_b32 s71,s58,5\n"
"s_add_u32 s71,s71,s59\n"
"s_mul_i32 s71,s71,196608\n"
"s_bitcmp1_b32 s23,12\n"
"s_cselect_b32 s58,s71,s70\n"
"s_cselect_b32 s71,s70,s71\n"
"s_mov_b32 s70,s58\n"
"s_cselect_b32 s58,s89,s88\n"
"s_cselect_b32 s89,s88,s89\n"
"s_mov_b32 s88,s58\n"
"s_cselect_b32 s69,98,1\n"
LD "v16,s[28:31],s82 offen offset:1024 lds\n"
"s_mov_b32 s54,1037384597\n"
"s_mov_b32 s55,1037384597\n"
"s_bfe_u32 s58,s22,0x40004\n"
"s_lshl_b32 s59,s72,4\n"
"s_add_u32 s59,s59,s58\n"
"s_lshl_b32 s86,s59,12\n"
"s_add_u32 s86,s86,100663296\n"
"s_mul_i32 s93,s59,1638400\n"
"s_add_u32 s93,s93,33554432\n"
"s_bfe_u32 s58,s22,0x30014\n"
"s_lshl_b32 s78,s58,1\n"
"s_add_u32 s78,s78,s74\n"
"s_lshl_b32 s94,s78,2\n"
"s_add_u32 s94,s94,s86\n"
"s_lshl_b32 s92,s58,2\n"
"s_add_u32 s92,s92,s56\n"
"s_mul_i32 s91,s58,204800\n"
"s_add_u32 s91,s91,s93\n"
"s_mul_i32 s75,s56,30720\n"
LD "v17,s[28:31],s82 offen offset:2048 lds\n"
"s_add_u32 s75,s75,s91\n"
"s_add_u32 s75,s75,0\n"
"s_mul_i32 s58,s74,5120\n"
"s_add_u32 s91,s91,s58\n"
"s_mov_b32 s90,0xf0000000\n"
"s_mov_b32 s77,0xf0000000\n"
"s_mov_b32 s85,0xf0000000\n"
AW "a240,v0\n"
AW "a244,v1\n"
AW "a241,v2\n"
AW "a245,v3\n"
AW "a242,v4\n"
AW "a243,v6\n"
AW "a252,v12\n"
"s_mov_b32 s57,0\n"
"s_mov_b32 s83,0\n"
LD "v18,s[28:31],s82 offen offset:3072 lds\n"
"global_load_dword a249,v15,s[52:53]\n"
AR "v0,a240\n"
AR "v2,a241\n"
AR "v4,a242\n"
AR "v6,a243\n"
"v_xor_b32 v1,64,v0\n"
"v_xor_b32 v3,64,v2\n"
"v_xor_b32_e64 v5,64,v4\n"
"v_add_u32 v11,97280,v6\n"
"v_add_u32 v12,194560,v6\n"
"v_add_u32 v13,291840,v6\n"
"s_mov_b64 vcc,0\n"
"s_waitcnt vmcnt(9) lgkmcnt(0)\n"
"s_barrier\n"
"s_nop 0\n"
TR "a[200:201],v0\n"
TR "a[202:203],v0 offset:512\n"
TR "a[204:205],v0 offset:4096\n"
TR "a[206:207],v0 offset:4608\n"
"ds_read_b128 a[60:63],v2\n"
"ds_read_b128 a[100:103],v3\n"
"ds_read_b128 a[64:67],v2 offset:2048\n"
"ds_read_b128 a[104:107],v3 offset:2048\n"
"ds_read_b128 a[68:71],v2 offset:4096\n"
"ds_read_b128 a[108:111],v3 offset:4096\n"
"ds_read_b128 a[72:75],v2 offset:6144\n"
"ds_read_b128 a[112:115],v3 offset:6144\n"
"ds_read_b128 a[76:79],v2 offset:8192\n"
"ds_read_b128 a[116:119],v3 offset:8192\n"
TR "a[208:209],v0 offset:8\n"
TR "a[210:211],v0 offset:520\n"
TR "a[212:213],v0 offset:4104\n"
TR "a[214:215],v0 offset:4616\n"
"s_waitcnt lgkmcnt(0)\n"
MF "v[16:19],a[200:203],a[60:63],0\n"
TR "a[216:217],v1\n"
MF "v[16:19],a[204:207],a[100:103],v[16:19]\n"
"s_add_u32 s14,s81,128\n"
TR "a[218:219],v1 offset:512\n"
MF "v[24:27],a[204:207],a[104:107],0\n"
"s_sub_u32 s15,s14,1280\n"
"v_lshrrev_b32 v15,5,v9\n"
"v_min_u32 v15,11,v15\n"
MF "v[24:27],a[200:203],a[64:67],v[24:27]\n"
"s_min_u32 s14,s14,s15\n"
TR "a[220:221],v1 offset:4096\n"
"v_mul_u32_u24 v15,160,v15\n"
MF "v[32:35],a[200:203],a[68:71],0\n"
"s_add_u32 s14,s14,s68\n"
TR "a[222:223],v1 offset:4608\n"
"v_and_or_b32 v15,v9,16,v15\n"
MF "v[32:35],a[204:207],a[108:111],v[32:35]\n"
"s_add_u32 s84,s14,0\n"
"v_max3_f32 v220,|v16|,|v17|,|v18|\n"
"v_max_f32_e64 v220,v220,|v19|\n"
MF "v[40:43],a[204:207],a[112:115],0\n"
"s_add_u32 m0,s63,20480\n"
"v_readlane_b32 s12,v220,3\n"
"v_readlane_b32 s6,v220,22\n"
MF "v[40:43],a[200:203],a[72:75],v[40:43]\n"
"s_min_u32 s12,s12,s6\n"
"v_readlane_b32 s7,v220,41\n"
"v_readlane_b32 s8,v220,60\n"
MF "v[48:51],a[200:203],a[76:79],0\n"
"s_min_u32 s7,s7,s8\n"
"v_lshlrev_b32 v15,3,v15\n"
MF "v[48:51],a[204:207],a[116:119],v[48:51]\n"
LD "v4,s[24:27],s84 offen lds\n"
MF "v[52:55],a[212:215],a[116:119],0\n"
"s_min_u32 s12,s12,s7\n"
TR "a[200:201],v1 offset:8\n"
MF "v[52:55],a[208:211],a[76:79],v[52:55]\n"
"s_and_b32 s12,s12,0x7f800000\n"
TR "a[202:203],v1 offset:520\n"
MF "v[44:47],a[208:211],a[72:75],0\n"
"s_sub_u32 s12,s12,0x14000000\n"
MF "v[44:47],a[212:215],a[112:115],v[44:47]\n"
"s_cmp_lt_u32 s12,0x50000000\n"
"s_cselect_b32 s12,s12,0xe8400000\n"
TR "a[204:205],v1 offset:4104\n"
MF "v[36:39],a[212:215],a[108:111],0\n"
"s_add_u32 s12,s12,0x17c00000\n"
TR "a[206:207],v1 offset:4616\n"
MF "v[36:39],a[208:211],a[68:71],v[36:39]\n"
"s_bfe_u32 s60,s23,0x70000\n"
"v_mov_b32 v220,s12\n"
"v_mov_b32 v221,s12\n"
MF "v[28:31],a[208:211],a[64:67],0\n"
"s_add_u32 s84,s14,9216\n"
"v_mov_b32 v222,s12\n"
"v_mov_b32 v223,s12\n"
MF "v[28:31],a[212:215],a[104:107],v[28:31]\n"
LD "v5,s[24:27],s84 offen offset:1024 lds\n"
MF "v[20:23],a[212:215],a[100:103],0\n"
"s_waitcnt vmcnt(7) lgkmcnt(0)\n"
MF "v[20:23],a[208:211],a[60:63],v[20:23]\n"
"s_barrier\n"
MF "v[56:59],a[216:219],a[60:63],v[220:223]\n"
"s_lshl_b32 s60,s60,2\n"
TR "a[208:209],v0 offset:16384\n"
MF "v[56:59],a[220:223],a[100:103],v[56:59]\n"
"s_add_u32 s84,s14,18432\n"
TR "a[210:211],v0 offset:16896\n"
MF "v[64:67],a[220:223],a[104:107],v[220:223]\n"
LD "v4,s[24:27],s84 offen offset:2048 lds\n"
MF "v[64:67],a[216:219],a[64:67],v[64:67]\n"
"s_add_u32 s60,s60,s56\n"
TR "a[212:213],v0 offset:20480\n"
MF "v[72:75],a[216:219],a[68:71],v[220:223]\n"
"s_mul_i32 s60,s60,15360\n"
TR "a[214:215],v0 offset:20992\n"
MF "v[72:75],a[220:223],a[108:111],v[72:75]\n"
MF "v[80:83],a[220:223],a[112:115],v[220:223]\n"
"s_add_u32 s84,s14,27648\n"
MF "v[80:83],a[216:219],a[72:75],v[80:83]\n"
LD "v5,s[24:27],s84 offen offset:3072 lds\n"
MF "v[88:91],a[216:219],a[76:79],v[220:223]\n"
MF "v[88:91],a[220:223],a[116:119],v[88:91]\n"
MF "v[92:95],a[204:207],a[116:119],v[220:223]\n"
"s_add_u32 s84,s14,40960\n"
TR "a[216:217],v0 offset:16392\n"
MF "v[92:95],a[200:203],a[76:79],v[92:95]\n"
"s_add_u32 m0,s63,24576\n"
TR "a[218:219],v0 offset:16904\n"
MF "v[84:87],a[200:203],a[72:75],v[220:223]\n"
LD "v4,s[24:27],s84 offen lds\n"
MF "v[84:87],a[204:207],a[112:115],v[84:87]\n"
"s_add_u32 m0,s62,49152\n"
TR "a[220:221],v0 offset:20488\n"
MF "v[76:79],a[204:207],a[108:111],v[220:223]\n"
"s_add_u32 s82,s66,786432\n"
TR "a[222:223],v0 offset:21000\n"
MF "v[76:79],a[200:203],a[68:71],v[76:79]\n"
"s_sub_u32 s15,s82,7864320\n"
MF "v[68:71],a[200:203],a[64:67],v[220:223]\n"
"s_min_u32 s82,s82,s15\n"
MF "v[68:71],a[204:207],a[104:107],v[68:71]\n"
LD "v6,s[28:31],s82 offen lds\n"
MF "v[60:63],a[204:207],a[100:103],v[220:223]\n"
MF "v[60:63],a[200:203],a[60:63],v[60:63]\n"
"s_waitcnt lgkmcnt(4)\n"
MF "v[96:99],a[208:211],a[60:63],v[220:223]\n"
TR "a[200:201],v1 offset:16384\n"
MF "v[96:99],a[212:215],a[100:103],v[96:99]\n"
TR "a[202:203],v1 offset:16896\n"
MF "v[104:107],a[212:215],a[104:107],v[220:223]\n"
LD "v11,s[28:31],s82 offen offset:1024 lds\n"
MF "v[104:107],a[208:211],a[64:67],v[104:107]\n"
TR "a[204:205],v1 offset:20480\n"
MF "v[112:115],a[208:211],a[68:71],v[220:223]\n"
TR "a[206:207],v1 offset:20992\n"
MF "v[112:115],a[212:215],a[108:111],v[112:115]\n"
MF "v[120:123],a[212:215],a[112:115],v[220:223]\n"
MF "v[120:123],a[208:211],a[72:75],v[120:123]\n"
LD "v12,s[28:31],s82 offen offset:2048 lds\n"
MF "v[128:131],a[208:211],a[76:79],v[220:223]\n"
MF "v[128:131],a[212:215],a[116:119],v[128:131]\n"
"s_waitcnt lgkmcnt(4)\n"
MF "v[132:135],a[220:223],a[116:119],v[220:223]\n"
TR "a[208:209],v1 offset:16392\n"
MF "v[132:135],a[216:219],a[76:79],v[132:135]\n"
TR "a[210:211],v1 offset:16904\n"
MF "v[124:127],a[216:219],a[72:75],v[220:223]\n"
LD "v13,s[28:31],s82 offen offset:3072 lds\n"
MF "v[124:127],a[220:223],a[112:115],v[124:127]\n"
"s_add_u32 m0,s62,65536\n"
TR "a[212:213],v1 offset:20488\n"
MF "v[116:119],a[220:223],a[108:111],v[220:223]\n"
"s_add_u32 s82,s66,786560\n"
TR "a[214:215],v1 offset:21000\n"
MF "v[116:119],a[216:219],a[68:71],v[116:119]\n"
"s_sub_u32 s15,s82,7864320\n"
MF "v[108:111],a[216:219],a[64:67],v[220:223]\n"
"s_min_u32 s82,s82,s15\n"
MF "v[108:111],a[220:223],a[104:107],v[108:111]\n"
LD "v6,s[28:31],s82 offen lds\n"
MF "v[100:103],a[220:223],a[100:103],v[220:223]\n"
"s_waitcnt vmcnt(5) lgkmcnt(0)\n"
MF "v[100:103],a[216:219],a[60:63],v[100:103]\n"
"s_barrier\n"
MF "v[136:139],a[200:203],a[60:63],v[220:223]\n"
TR "a[216:217],v0 offset:32768\n"
MF "v[136:139],a[204:207],a[100:103],v[136:139]\n"
TR "a[218:219],v0 offset:33280\n"
MF "v[144:147],a[204:207],a[104:107],v[220:223]\n"
"ds_read_b128 a[140:143],v2 offset:20480\n"
MF "v[144:147],a[200:203],a[64:67],v[144:147]\n"
TR "a[220:221],v0 offset:36864\n"
MF "v[152:155],a[200:203],a[68:71],v[220:223]\n"
TR "a[222:223],v0 offset:37376\n"
MF "v[152:155],a[204:207],a[108:111],v[152:155]\n"
LD "v11,s[28:31],s82 offen offset:1024 lds\n"
MF "v[160:163],a[204:207],a[112:115],v[220:223]\n"
"ds_read_b128 a[180:183],v3 offset:20480\n"
MF "v[160:163],a[200:203],a[72:75],v[160:163]\n"
MF "v[168:171],a[200:203],a[76:79],v[220:223]\n"
MF "v[168:171],a[204:207],a[116:119],v[168:171]\n"
LD "v12,s[28:31],s82 offen offset:2048 lds\n"
MF "v[172:175],a[212:215],a[116:119],v[220:223]\n"
TR "a[200:201],v0 offset:32776\n"
MF "v[172:175],a[208:211],a[76:79],v[172:175]\n"
TR "a[202:203],v0 offset:33288\n"
MF "v[164:167],a[208:211],a[72:75],v[220:223]\n"
"ds_read_b128 a[144:147],v2 offset:22528\n"
MF "v[164:167],a[212:215],a[112:115],v[164:167]\n"
TR "a[204:205],v0 offset:36872\n"
MF "v[156:159],a[212:215],a[108:111],v[220:223]\n"
TR "a[206:207],v0 offset:37384\n"
MF "v[156:159],a[208:211],a[68:71],v[156:159]\n"
LD "v13,s[28:31],s82 offen offset:3072 lds\n"
MF "v[148:151],a[208:211],a[64:67],v[220:223]\n"
"ds_read_b128 a[184:187],v3 offset:22528\n"
MF "v[148:151],a[212:215],a[104:107],v[148:151]\n"
"s_add_u32 m0,s62,81920\n"
MF "v[140:143],a[212:215],a[100:103],v[220:223]\n"
"s_add_u32 s82,s66,786688\n"
"s_sub_u32 s15,s82,7864320\n"
"s_min_u32 s82,s82,s15\n"
MF "v[140:143],a[208:211],a[60:63],v[140:143]\n"
"s_waitcnt lgkmcnt(7)\n"
LD "v6,s[28:31],s82 offen lds\n"
MF "v[176:179],a[216:219],a[60:63],v[220:223]\n"
TR "a[208:209],v1 offset:32768\n"
MF "v[176:179],a[220:223],a[100:103],v[176:179]\n"
TR "a[210:211],v1 offset:33280\n"
MF "v[184:187],a[220:223],a[104:107],v[220:223]\n"
"ds_read_b128 a[148:151],v2 offset:24576\n"
MF "v[184:187],a[216:219],a[64:67],v[184:187]\n"
TR "a[212:213],v1 offset:36864\n"
MF "v[192:195],a[216:219],a[68:71],v[220:223]\n"
TR "a[214:215],v1 offset:37376\n"
MF "v[192:195],a[220:223],a[108:111],v[192:195]\n"
LD "v11,s[28:31],s82 offen offset:1024 lds\n"
MF "v[200:203],a[220:223],a[112:115],v[220:223]\n"
"ds_read_b128 a[188:191],v3 offset:24576\n"
MF "v[200:203],a[216:219],a[72:75],v[200:203]\n"
MF "v[208:211],a[216:219],a[76:79],v[220:223]\n"
MF "v[208:211],a[220:223],a[116:119],v[208:211]\n"
"s_waitcnt lgkmcnt(7)\n"
LD "v12,s[28:31],s82 offen offset:2048 lds\n"
MF "v[212:215],a[204:207],a[116:119],v[220:223]\n"
TR "a[216:217],v1 offset:32776\n"
MF "v[212:215],a[200:203],a[76:79],v[212:215]\n"
TR "a[218:219],v1 offset:33288\n"
MF "v[204:207],a[200:203],a[72:75],v[220:223]\n"
"ds_read_b128 a[152:155],v2 offset:26624\n"
MF "v[204:207],a[204:207],a[112:115],v[204:207]\n"
TR "a[220:221],v1 offset:36872\n"
MF "v[196:199],a[204:207],a[108:111],v[220:223]\n"
TR "a[222:223],v1 offset:37384\n"
MF "v[196:199],a[200:203],a[68:71],v[196:199]\n"
LD "v13,s[28:31],s82 offen offset:3072 lds\n"
MF "v[188:191],a[200:203],a[64:67],v[220:223]\n"
"v_add_u32 v0,0xc000,v0\n"
MF "v[188:191],a[204:207],a[104:107],v[188:191]\n"
"s_add_u32 s14,s81,256\n"
"v_add_u32 v1,0xc000,v1\n"
MF "v[180:183],a[204:207],a[100:103],v[220:223]\n"
"s_waitcnt vmcnt(8) lgkmcnt(0)\n"
"s_mov_b64 exec,0\n"
"s_bitcmp1_b32 s23,12\n"
"s_cbranch_scc1 .Lisy1\n"
"s_cmp_eq_u32 s56,0\n"
"s_cbranch_scc1 .Lpg8b\n"
"s_cmp_eq_u32 s56,1\n"
"s_cbranch_scc1 .Lpg9b\n"
"s_cmp_eq_u32 s56,2\n"
"s_cbranch_scc1 .Lpgt0ex_b\n"
"s_branch .Lpg10b\n"
".Lisy1:\n"
"s_cmp_eq_u32 s56,0\n"
"s_cbranch_scc1 .Lpgq0_c\n"
"s_cmp_eq_u32 s56,1\n"
"s_cbranch_scc1 .Lpg4c\n"
"s_cmp_eq_u32 s56,2\n"
"s_cbranch_scc1 .Lpg5c\n"
"s_branch .Lpg6c\n"
".Lisr1:\n"
"s_mov_b64 exec,-1\n"
".Lisx1:\n"
MF "v[180:183],a[200:203],a[60:63],v[180:183]\n"
"s_barrier\n"
MF "v[216:219],a[208:211],a[60:63],v[220:223]\n"
"s_sub_u32 s15,s14,1280\n"
TR "a[200:201],v0\n"
MF "v[216:219],a[212:215],a[100:103],v[216:219]\n"
"s_min_u32 s14,s14,s15\n"
TR "a[202:203],v0 offset:512\n"
MF "v[224:227],a[212:215],a[104:107],v[220:223]\n"
"s_add_u32 s14,s14,s68\n"
"ds_read_b128 a[192:195],v3 offset:26624\n"
MF "v[224:227],a[208:211],a[64:67],v[224:227]\n"
"s_add_u32 s84,s14,0\n"
TR "a[204:205],v0 offset:4096\n"
MF "v[232:235],a[208:211],a[68:71],v[220:223]\n"
"s_add_u32 m0,s63,40960\n"
TR "a[206:207],v0 offset:4608\n"
MF "v[232:235],a[212:215],a[108:111],v[232:235]\n"
LD "v4,s[24:27],s84 offen lds\n"
MF "v[240:243],a[212:215],a[112:115],v[220:223]\n"
"ds_read_b128 a[156:159],v2 offset:28672\n"
MF "v[240:243],a[208:211],a[72:75],v[240:243]\n"
"ds_read_b128 a[196:199],v3 offset:28672\n"
MF "v[248:251],a[208:211],a[76:79],v[220:223]\n"
"s_add_u32 s84,s14,9216\n"
MF "v[248:251],a[212:215],a[116:119],v[248:251]\n"
LD "v5,s[24:27],s84 offen offset:1024 lds\n"
MF "v[252:255],a[220:223],a[116:119],v[220:223]\n"
TR "a[208:209],v0 offset:8\n"
MF "v[252:255],a[216:219],a[76:79],v[252:255]\n"
TR "a[210:211],v0 offset:520\n"
MF "v[244:247],a[216:219],a[72:75],v[220:223]\n"
MF "v[244:247],a[220:223],a[112:115],v[244:247]\n"
TR "a[212:213],v0 offset:4104\n"
MF "v[236:239],a[220:223],a[108:111],v[220:223]\n"
"s_add_u32 s84,s14,18432\n"
TR "a[214:215],v0 offset:4616\n"
MF "v[236:239],a[216:219],a[68:71],v[236:239]\n"
LD "v4,s[24:27],s84 offen offset:2048 lds\n"
MF "v[228:231],a[216:219],a[64:67],v[220:223]\n"
MF "v[228:231],a[220:223],a[104:107],v[228:231]\n"
MF "v[220:223],a[220:223],a[100:103],v[220:223]\n"
"s_add_u32 s84,s14,27648\n"
MF "v[220:223],a[216:219],a[60:63],v[220:223]\n"
".Lpg1a:\n"
"s_cbranch_execz .Lpg7a\n"
"s_waitcnt lgkmcnt(4)\n"
LD "v5,s[24:27],s84 offen offset:3072 lds\n"
MF "v[16:19],a[200:203],a[140:143],v[16:19]\n"
TR "a[216:217],v1\n"
MF "v[16:19],a[204:207],a[180:183],v[16:19]\n"
TR "a[218:219],v1 offset:512\n"
MF "v[24:27],a[204:207],a[184:187],v[24:27]\n"
MF "v[24:27],a[200:203],a[144:147],v[24:27]\n"
"s_add_u32 s84,s14,40960\n"
TR "a[220:221],v1 offset:4096\n"
MF "v[32:35],a[200:203],a[148:151],v[32:35]\n"
"s_add_u32 m0,s63,45056\n"
TR "a[222:223],v1 offset:4608\n"
MF "v[32:35],a[204:207],a[188:191],v[32:35]\n"
LD "v4,s[24:27],s84 offen lds\n"
MF "v[40:43],a[204:207],a[192:195],v[40:43]\n"
MF "v[40:43],a[200:203],a[152:155],v[40:43]\n"
"s_add_u32 m0,s62,0\n"
MF "v[48:51],a[200:203],a[156:159],v[48:51]\n"
"s_add_u32 s82,s66,1572864\n"
"s_sub_u32 s15,s82,7864320\n"
"s_min_u32 s82,s82,s15\n"
MF "v[48:51],a[204:207],a[196:199],v[48:51]\n"
"s_waitcnt lgkmcnt(4)\n"
LD "v6,s[28:31],s82 offen lds\n"
MF "v[52:55],a[212:215],a[196:199],v[52:55]\n"
TR "a[200:201],v1 offset:8\n"
MF "v[52:55],a[208:211],a[156:159],v[52:55]\n"
TR "a[202:203],v1 offset:520\n"
MF "v[44:47],a[208:211],a[152:155],v[44:47]\n"
MF "v[44:47],a[212:215],a[192:195],v[44:47]\n"
TR "a[204:205],v1 offset:4104\n"
MF "v[36:39],a[212:215],a[188:191],v[36:39]\n"
TR "a[206:207],v1 offset:4616\n"
MF "v[36:39],a[208:211],a[148:151],v[36:39]\n"
LD "v11,s[28:31],s82 offen offset:1024 lds\n"
MF "v[28:31],a[208:211],a[144:147],v[28:31]\n"
MF "v[28:31],a[212:215],a[184:187],v[28:31]\n"
MF "v[20:23],a[212:215],a[180:183],v[20:23]\n"
"s_waitcnt vmcnt(11) lgkmcnt(0)\n"
LD "v12,s[28:31],s82 offen offset:2048 lds\n"
MF "v[20:23],a[208:211],a[140:143],v[20:23]\n"
"s_barrier\n"
"v_bfe_u32 v14,v9,4,4\n"
"v_lshlrev_b32 v14,2,v14\n"
"buffer_load_dword v10,v14,s[40:43],s86 offen sc1\n"
".Lhs2:\n"
MF "v[56:59],a[216:219],a[140:143],v[56:59]\n"
TR "a[208:209],v0 offset:16384\n"
MF "v[56:59],a[220:223],a[180:183],v[56:59]\n"
TR "a[210:211],v0 offset:16896\n"
MF "v[64:67],a[220:223],a[184:187],v[64:67]\n"
LD "v13,s[28:31],s82 offen offset:3072 lds\n"
MF "v[64:67],a[216:219],a[144:147],v[64:67]\n"
TR "a[212:213],v0 offset:20480\n"
MF "v[72:75],a[216:219],a[148:151],v[72:75]\n"
TR "a[214:215],v0 offset:20992\n"
MF "v[72:75],a[220:223],a[188:191],v[72:75]\n"
"s_add_u32 s14,s81,384\n"
"s_sub_u32 s15,s14,1280\n"
"s_min_u32 s14,s14,s15\n"
MF "v[80:83],a[220:223],a[192:195],v[80:83]\n"
"s_add_u32 s14,s14,s68\n"
"s_add_u32 s84,s14,0\n"
"s_add_u32 m0,s63,0\n"
MF "v[80:83],a[216:219],a[152:155],v[80:83]\n"
LD "v4,s[24:27],s84 offen lds\n"
MF "v[88:91],a[216:219],a[156:159],v[88:91]\n"
MF "v[88:91],a[220:223],a[196:199],v[88:91]\n"
MF "v[92:95],a[204:207],a[196:199],v[92:95]\n"
TR "a[216:217],v0 offset:16392\n"
MF "v[92:95],a[200:203],a[156:159],v[92:95]\n"
"s_add_u32 s84,s14,9216\n"
TR "a[218:219],v0 offset:16904\n"
MF "v[84:87],a[200:203],a[152:155],v[84:87]\n"
LD "v5,s[24:27],s84 offen offset:1024 lds\n"
MF "v[84:87],a[204:207],a[192:195],v[84:87]\n"
"s_add_u32 m0,s62,16384\n"
TR "a[220:221],v0 offset:20488\n"
MF "v[76:79],a[204:207],a[188:191],v[76:79]\n"
"s_add_u32 s82,s66,1572992\n"
TR "a[222:223],v0 offset:21000\n"
MF "v[76:79],a[200:203],a[148:151],v[76:79]\n"
"s_sub_u32 s15,s82,7864320\n"
MF "v[68:71],a[200:203],a[144:147],v[68:71]\n"
"s_min_u32 s82,s82,s15\n"
MF "v[68:71],a[204:207],a[184:187],v[68:71]\n"
LD "v6,s[28:31],s82 offen lds\n"
MF "v[60:63],a[204:207],a[180:183],v[60:63]\n"
MF "v[60:63],a[200:203],a[140:143],v[60:63]\n"
"s_waitcnt lgkmcnt(4)\n"
MF "v[96:99],a[208:211],a[140:143],v[96:99]\n"
TR "a[200:201],v1 offset:16384\n"
MF "v[96:99],a[212:215],a[180:183],v[96:99]\n"
TR "a[202:203],v1 offset:16896\n"
MF "v[104:107],a[212:215],a[184:187],v[104:107]\n"
LD "v11,s[28:31],s82 offen offset:1024 lds\n"
MF "v[104:107],a[208:211],a[144:147],v[104:107]\n"
TR "a[204:205],v1 offset:20480\n"
MF "v[112:115],a[208:211],a[148:151],v[112:115]\n"
TR "a[206:207],v1 offset:20992\n"
MF "v[112:115],a[212:215],a[188:191],v[112:115]\n"
MF "v[120:123],a[212:215],a[192:195],v[120:123]\n"
MF "v[120:123],a[208:211],a[152:155],v[120:123]\n"
LD "v12,s[28:31],s82 offen offset:2048 lds\n"
MF "v[128:131],a[208:211],a[156:159],v[128:131]\n"
MF "v[128:131],a[212:215],a[196:199],v[128:131]\n"
"s_waitcnt lgkmcnt(4)\n"
MF "v[132:135],a[220:223],a[196:199],v[132:135]\n"
TR "a[208:209],v1 offset:16392\n"
MF "v[132:135],a[216:219],a[156:159],v[132:135]\n"
TR "a[210:211],v1 offset:16904\n"
MF "v[124:127],a[216:219],a[152:155],v[124:127]\n"
LD "v13,s[28:31],s82 offen offset:3072 lds\n"
MF "v[124:127],a[220:223],a[192:195],v[124:127]\n"
TR "a[212:213],v1 offset:20488\n"
MF "v[116:119],a[220:223],a[188:191],v[116:119]\n"
TR "a[214:215],v1 offset:21000\n"
MF "v[116:119],a[216:219],a[148:151],v[116:119]\n"
"s_add_u32 s84,s14,20480\n"
MF "v[108:111],a[216:219],a[144:147],v[108:111]\n"
"s_add_u32 m0,s63,2048\n"
MF "v[108:111],a[220:223],a[184:187],v[108:111]\n"
LD "v4,s[24:27],s84 offen lds\n"
MF "v[100:103],a[220:223],a[180:183],v[100:103]\n"
"s_waitcnt vmcnt(11) lgkmcnt(0)\n"
MF "v[100:103],a[216:219],a[140:143],v[100:103]\n"
"s_barrier\n"
MF "v[136:139],a[200:203],a[140:143],v[136:139]\n"
TR "a[216:217],v0 offset:32768\n"
MF "v[136:139],a[204:207],a[180:183],v[136:139]\n"
TR "a[218:219],v0 offset:33280\n"
MF "v[144:147],a[204:207],a[184:187],v[144:147]\n"
"ds_read_b128 a[60:63],v2 offset:40960\n"
MF "v[144:147],a[200:203],a[144:147],v[144:147]\n"
TR "a[220:221],v0 offset:36864\n"
MF "v[152:155],a[200:203],a[148:151],v[152:155]\n"
"s_add_u32 s84,s14,29696\n"
TR "a[222:223],v0 offset:37376\n"
MF "v[152:155],a[204:207],a[188:191],v[152:155]\n"
LD "v5,s[24:27],s84 offen offset:1024 lds\n"
MF "v[160:163],a[204:207],a[192:195],v[160:163]\n"
"ds_read_b128 a[100:103],v3 offset:40960\n"
MF "v[160:163],a[200:203],a[152:155],v[160:163]\n"
"s_add_u32 m0,s62,32768\n"
MF "v[168:171],a[200:203],a[156:159],v[168:171]\n"
"s_add_u32 s82,s66,1573120\n"
"s_sub_u32 s15,s82,7864320\n"
"s_min_u32 s82,s82,s15\n"
MF "v[168:171],a[204:207],a[196:199],v[168:171]\n"
LD "v6,s[28:31],s82 offen lds\n"
MF "v[172:175],a[212:215],a[196:199],v[172:175]\n"
TR "a[200:201],v0 offset:32776\n"
MF "v[172:175],a[208:211],a[156:159],v[172:175]\n"
TR "a[202:203],v0 offset:33288\n"
MF "v[164:167],a[208:211],a[152:155],v[164:167]\n"
"ds_read_b128 a[64:67],v2 offset:43008\n"
MF "v[164:167],a[212:215],a[192:195],v[164:167]\n"
TR "a[204:205],v0 offset:36872\n"
MF "v[156:159],a[212:215],a[188:191],v[156:159]\n"
TR "a[206:207],v0 offset:37384\n"
MF "v[156:159],a[208:211],a[148:151],v[156:159]\n"
LD "v11,s[28:31],s82 offen offset:1024 lds\n"
MF "v[148:151],a[208:211],a[144:147],v[148:151]\n"
"ds_read_b128 a[104:107],v3 offset:43008\n"
MF "v[148:151],a[212:215],a[184:187],v[148:151]\n"
MF "v[140:143],a[212:215],a[180:183],v[140:143]\n"
MF "v[140:143],a[208:211],a[140:143],v[140:143]\n"
"s_waitcnt lgkmcnt(7)\n"
LD "v12,s[28:31],s82 offen offset:2048 lds\n"
MF "v[176:179],a[216:219],a[140:143],v[176:179]\n"
TR "a[208:209],v1 offset:32768\n"
MF "v[176:179],a[220:223],a[180:183],v[176:179]\n"
TR "a[210:211],v1 offset:33280\n"
MF "v[184:187],a[220:223],a[184:187],v[184:187]\n"
"ds_read_b128 a[68:71],v2 offset:45056\n"
MF "v[184:187],a[216:219],a[144:147],v[184:187]\n"
TR "a[212:213],v1 offset:36864\n"
MF "v[192:195],a[216:219],a[148:151],v[192:195]\n"
TR "a[214:215],v1 offset:37376\n"
MF "v[192:195],a[220:223],a[188:191],v[192:195]\n"
LD "v13,s[28:31],s82 offen offset:3072 lds\n"
MF "v[200:203],a[220:223],a[192:195],v[200:203]\n"
"ds_read_b128 a[108:111],v3 offset:45056\n"
MF "v[200:203],a[216:219],a[152:155],v[200:203]\n"
"s_add_u32 s84,s14,40960\n"
MF "v[208:211],a[216:219],a[156:159],v[208:211]\n"
"s_add_u32 m0,s63,4096\n"
MF "v[208:211],a[220:223],a[196:199],v[208:211]\n"
"s_waitcnt lgkmcnt(7)\n"
LD "v4,s[24:27],s84 offen lds\n"
MF "v[212:215],a[204:207],a[196:199],v[212:215]\n"
"s_sub_u32 s66,s66,s83\n"
TR "a[216:217],v1 offset:32776\n"
MF "v[212:215],a[200:203],a[156:159],v[212:215]\n"
"s_add_u32 m0,s62,49152\n"
TR "a[218:219],v1 offset:33288\n"
MF "v[204:207],a[200:203],a[152:155],v[204:207]\n"
"s_add_u32 s82,s66,2359296\n"
"ds_read_b128 a[72:75],v2 offset:47104\n"
MF "v[204:207],a[204:207],a[192:195],v[204:207]\n"
"s_sub_u32 s15,s82,7864320\n"
TR "a[220:221],v1 offset:36872\n"
MF "v[196:199],a[204:207],a[188:191],v[196:199]\n"
"s_min_u32 s82,s82,s15\n"
TR "a[222:223],v1 offset:37384\n"
MF "v[196:199],a[200:203],a[148:151],v[196:199]\n"
LD "v6,s[28:31],s82 offen lds\n"
MF "v[188:191],a[200:203],a[144:147],v[188:191]\n"
"v_add_u32 v0,0xffff4000,v0\n"
MF "v[188:191],a[204:207],a[184:187],v[188:191]\n"
"v_add_u32 v1,0xffff4000,v1\n"
MF "v[180:183],a[204:207],a[180:183],v[180:183]\n"
"s_waitcnt vmcnt(14) lgkmcnt(0)\n"
LD "v11,s[28:31],s82 offen offset:1024 lds\n"
MF "v[180:183],a[200:203],a[140:143],v[180:183]\n"
"s_barrier\n"
MF "v[216:219],a[208:211],a[140:143],v[216:219]\n"
TR "a[200:201],v0\n"
MF "v[216:219],a[212:215],a[180:183],v[216:219]\n"
TR "a[202:203],v0 offset:512\n"
MF "v[224:227],a[212:215],a[184:187],v[224:227]\n"
"ds_read_b128 a[112:115],v3 offset:47104\n"
MF "v[224:227],a[208:211],a[144:147],v[224:227]\n"
TR "a[204:205],v0 offset:4096\n"
MF "v[232:235],a[208:211],a[148:151],v[232:235]\n"
TR "a[206:207],v0 offset:4608\n"
MF "v[232:235],a[212:215],a[188:191],v[232:235]\n"
LD "v12,s[28:31],s82 offen offset:2048 lds\n"
MF "v[240:243],a[212:215],a[192:195],v[240:243]\n"
"ds_read_b128 a[76:79],v2 offset:49152\n"
MF "v[240:243],a[208:211],a[152:155],v[240:243]\n"
"ds_read_b128 a[116:119],v3 offset:49152\n"
MF "v[248:251],a[208:211],a[156:159],v[248:251]\n"
MF "v[248:251],a[212:215],a[196:199],v[248:251]\n"
LD "v13,s[28:31],s82 offen offset:3072 lds\n"
MF "v[252:255],a[220:223],a[196:199],v[252:255]\n"
TR "a[208:209],v0 offset:8\n"
MF "v[252:255],a[216:219],a[156:159],v[252:255]\n"
TR "a[210:211],v0 offset:520\n"
MF "v[244:247],a[216:219],a[152:155],v[244:247]\n"
"s_add_u32 s14,s81,512\n"
"s_sub_u32 s15,s14,1280\n"
MF "v[244:247],a[220:223],a[192:195],v[244:247]\n"
"s_min_u32 s14,s14,s15\n"
"s_add_u32 s14,s14,s68\n"
TR "a[212:213],v0 offset:4104\n"
MF "v[236:239],a[220:223],a[188:191],v[236:239]\n"
"s_add_u32 s84,s14,0\n"
"s_add_u32 m0,s63,20480\n"
TR "a[214:215],v0 offset:4616\n"
MF "v[236:239],a[216:219],a[148:151],v[236:239]\n"
LD "v4,s[24:27],s84 offen lds\n"
MF "v[228:231],a[216:219],a[144:147],v[228:231]\n"
MF "v[228:231],a[220:223],a[184:187],v[228:231]\n"
MF "v[220:223],a[220:223],a[180:183],v[220:223]\n"
"s_add_u32 s84,s14,9216\n"
MF "v[220:223],a[216:219],a[140:143],v[220:223]\n"
"s_waitcnt lgkmcnt(4)\n"
LD "v5,s[24:27],s84 offen offset:1024 lds\n"
MF "v[16:19],a[200:203],a[60:63],v[16:19]\n"
TR "a[216:217],v1\n"
MF "v[16:19],a[204:207],a[100:103],v[16:19]\n"
"s_add_u32 m0,s62,65536\n"
TR "a[218:219],v1 offset:512\n"
MF "v[24:27],a[204:207],a[104:107],v[24:27]\n"
"s_add_u32 s82,s66,2359424\n"
MF "v[24:27],a[200:203],a[64:67],v[24:27]\n"
"s_sub_u32 s15,s82,7864320\n"
TR "a[220:221],v1 offset:4096\n"
MF "v[32:35],a[200:203],a[68:71],v[32:35]\n"
"s_min_u32 s82,s82,s15\n"
TR "a[222:223],v1 offset:4608\n"
MF "v[32:35],a[204:207],a[108:111],v[32:35]\n"
LD "v6,s[28:31],s82 offen lds\n"
MF "v[40:43],a[204:207],a[112:115],v[40:43]\n"
MF "v[40:43],a[200:203],a[72:75],v[40:43]\n"
MF "v[48:51],a[200:203],a[76:79],v[48:51]\n"
MF "v[48:51],a[204:207],a[116:119],v[48:51]\n"
"s_waitcnt lgkmcnt(4)\n"
LD "v11,s[28:31],s82 offen offset:1024 lds\n"
MF "v[52:55],a[212:215],a[116:119],v[52:55]\n"
TR "a[200:201],v1 offset:8\n"
MF "v[52:55],a[208:211],a[76:79],v[52:55]\n"
TR "a[202:203],v1 offset:520\n"
MF "v[44:47],a[208:211],a[72:75],v[44:47]\n"
MF "v[44:47],a[212:215],a[112:115],v[44:47]\n"
TR "a[204:205],v1 offset:4104\n"
MF "v[36:39],a[212:215],a[108:111],v[36:39]\n"
TR "a[206:207],v1 offset:4616\n"
MF "v[36:39],a[208:211],a[68:71],v[36:39]\n"
LD "v12,s[28:31],s82 offen offset:2048 lds\n"
MF "v[28:31],a[208:211],a[64:67],v[28:31]\n"
MF "v[28:31],a[212:215],a[104:107],v[28:31]\n"
MF "v[20:23],a[212:215],a[100:103],v[20:23]\n"
"s_waitcnt vmcnt(16) lgkmcnt(0)\n"
LD "v13,s[28:31],s82 offen offset:3072 lds\n"
MF "v[20:23],a[208:211],a[60:63],v[20:23]\n"
"s_barrier\n"
MF "v[56:59],a[216:219],a[60:63],v[56:59]\n"
"s_add_u32 s84,s14,20480\n"
TR "a[208:209],v0 offset:16384\n"
MF "v[56:59],a[220:223],a[100:103],v[56:59]\n"
"s_add_u32 m0,s63,22528\n"
TR "a[210:211],v0 offset:16896\n"
MF "v[64:67],a[220:223],a[104:107],v[64:67]\n"
LD "v4,s[24:27],s84 offen lds\n"
MF "v[64:67],a[216:219],a[64:67],v[64:67]\n"
TR "a[212:213],v0 offset:20480\n"
MF "v[72:75],a[216:219],a[68:71],v[72:75]\n"
TR "a[214:215],v0 offset:20992\n"
MF "v[72:75],a[220:223],a[108:111],v[72:75]\n"
MF "v[80:83],a[220:223],a[112:115],v[80:83]\n"
"s_add_u32 s84,s14,29696\n"
MF "v[80:83],a[216:219],a[72:75],v[80:83]\n"
LD "v5,s[24:27],s84 offen offset:1024 lds\n"
MF "v[88:91],a[216:219],a[76:79],v[88:91]\n"
"s_add_u32 m0,s62,81920\n"
MF "v[88:91],a[220:223],a[116:119],v[88:91]\n"
"s_add_u32 s82,s66,2359552\n"
MF "v[92:95],a[204:207],a[116:119],v[92:95]\n"
"s_sub_u32 s15,s82,7864320\n"
TR "a[216:217],v0 offset:16392\n"
MF "v[92:95],a[200:203],a[76:79],v[92:95]\n"
"s_min_u32 s82,s82,s15\n"
TR "a[218:219],v0 offset:16904\n"
MF "v[84:87],a[200:203],a[72:75],v[84:87]\n"
LD "v6,s[28:31],s82 offen lds\n"
MF "v[84:87],a[204:207],a[112:115],v[84:87]\n"
TR "a[220:221],v0 offset:20488\n"
MF "v[76:79],a[204:207],a[108:111],v[76:79]\n"
TR "a[222:223],v0 offset:21000\n"
MF "v[76:79],a[200:203],a[68:71],v[76:79]\n"
MF "v[68:71],a[200:203],a[64:67],v[68:71]\n"
MF "v[68:71],a[204:207],a[104:107],v[68:71]\n"
LD "v11,s[28:31],s82 offen offset:1024 lds\n"
MF "v[60:63],a[204:207],a[100:103],v[60:63]\n"
MF "v[60:63],a[200:203],a[60:63],v[60:63]\n"
"s_waitcnt lgkmcnt(4)\n"
MF "v[96:99],a[208:211],a[60:63],v[96:99]\n"
TR "a[200:201],v1 offset:16384\n"
MF "v[96:99],a[212:215],a[100:103],v[96:99]\n"
TR "a[202:203],v1 offset:16896\n"
MF "v[104:107],a[212:215],a[104:107],v[104:107]\n"
LD "v12,s[28:31],s82 offen offset:2048 lds\n"
MF "v[104:107],a[208:211],a[64:67],v[104:107]\n"
TR "a[204:205],v1 offset:20480\n"
MF "v[112:115],a[208:211],a[68:71],v[112:115]\n"
TR "a[206:207],v1 offset:20992\n"
MF "v[112:115],a[212:215],a[108:111],v[112:115]\n"
MF "v[120:123],a[212:215],a[112:115],v[120:123]\n"
MF "v[120:123],a[208:211],a[72:75],v[120:123]\n"
LD "v13,s[28:31],s82 offen offset:3072 lds\n"
MF "v[128:131],a[208:211],a[76:79],v[128:131]\n"
MF "v[128:131],a[212:215],a[116:119],v[128:131]\n"
"s_waitcnt lgkmcnt(4)\n"
MF "v[132:135],a[220:223],a[116:119],v[132:135]\n"
"s_add_u32 s84,s14,40960\n"
TR "a[208:209],v1 offset:16392\n"
MF "v[132:135],a[216:219],a[76:79],v[132:135]\n"
"s_add_u32 m0,s63,24576\n"
TR "a[210:211],v1 offset:16904\n"
MF "v[124:127],a[216:219],a[72:75],v[124:127]\n"
LD "v4,s[24:27],s84 offen lds\n"
MF "v[124:127],a[220:223],a[112:115],v[124:127]\n"
"s_add_u32 m0,s62,0\n"
TR "a[212:213],v1 offset:20488\n"
MF "v[116:119],a[220:223],a[108:111],v[116:119]\n"
"s_add_u32 s82,s66,3145728\n"
TR "a[214:215],v1 offset:21000\n"
MF "v[116:119],a[216:219],a[68:71],v[116:119]\n"
"s_sub_u32 s15,s82,7864320\n"
MF "v[108:111],a[216:219],a[64:67],v[108:111]\n"
"s_min_u32 s82,s82,s15\n"
MF "v[108:111],a[220:223],a[104:107],v[108:111]\n"
LD "v6,s[28:31],s82 offen lds\n"
MF "v[100:103],a[220:223],a[100:103],v[100:103]\n"
"s_waitcnt vmcnt(18) lgkmcnt(0)\n"
MF "v[100:103],a[216:219],a[60:63],v[100:103]\n"
"s_barrier\n"
MF "v[136:139],a[200:203],a[60:63],v[136:139]\n"
TR "a[216:217],v0 offset:32768\n"
MF "v[136:139],a[204:207],a[100:103],v[136:139]\n"
TR "a[218:219],v0 offset:33280\n"
MF "v[144:147],a[204:207],a[104:107],v[144:147]\n"
"ds_read_b128 a[140:143],v2\n"
MF "v[144:147],a[200:203],a[64:67],v[144:147]\n"
TR "a[220:221],v0 offset:36864\n"
MF "v[152:155],a[200:203],a[68:71],v[152:155]\n"
TR "a[222:223],v0 offset:37376\n"
MF "v[152:155],a[204:207],a[108:111],v[152:155]\n"
LD "v11,s[28:31],s82 offen offset:1024 lds\n"
MF "v[160:163],a[204:207],a[112:115],v[160:163]\n"
"ds_read_b128 a[180:183],v3\n"
MF "v[160:163],a[200:203],a[72:75],v[160:163]\n"
MF "v[168:171],a[200:203],a[76:79],v[168:171]\n"
MF "v[168:171],a[204:207],a[116:119],v[168:171]\n"
LD "v12,s[28:31],s82 offen offset:2048 lds\n"
MF "v[172:175],a[212:215],a[116:119],v[172:175]\n"
TR "a[200:201],v0 offset:32776\n"
MF "v[172:175],a[208:211],a[76:79],v[172:175]\n"
TR "a[202:203],v0 offset:33288\n"
MF "v[164:167],a[208:211],a[72:75],v[164:167]\n"
"ds_read_b128 a[144:147],v2 offset:2048\n"
MF "v[164:167],a[212:215],a[112:115],v[164:167]\n"
TR "a[204:205],v0 offset:36872\n"
MF "v[156:159],a[212:215],a[108:111],v[156:159]\n"
TR "a[206:207],v0 offset:37384\n"
MF "v[156:159],a[208:211],a[68:71],v[156:159]\n"
LD "v13,s[28:31],s82 offen offset:3072 lds\n"
MF "v[148:151],a[208:211],a[64:67],v[148:151]\n"
"ds_read_b128 a[184:187],v3 offset:2048\n"
MF "v[148:151],a[212:215],a[104:107],v[148:151]\n"
"s_add_u32 s14,s81,640\n"
"s_sub_u32 s15,s14,1280\n"
"s_min_u32 s14,s14,s15\n"
MF "v[140:143],a[212:215],a[100:103],v[140:143]\n"
"s_add_u32 s14,s14,s68\n"
"s_add_u32 s84,s14,0\n"
"s_add_u32 m0,s63,40960\n"
MF "v[140:143],a[208:211],a[60:63],v[140:143]\n"
"s_waitcnt lgkmcnt(7)\n"
LD "v4,s[24:27],s84 offen lds\n"
MF "v[176:179],a[216:219],a[60:63],v[176:179]\n"
TR "a[208:209],v1 offset:32768\n"
MF "v[176:179],a[220:223],a[100:103],v[176:179]\n"
TR "a[210:211],v1 offset:33280\n"
MF "v[184:187],a[220:223],a[104:107],v[184:187]\n"
"ds_read_b128 a[148:151],v2 offset:4096\n"
MF "v[184:187],a[216:219],a[64:67],v[184:187]\n"
TR "a[212:213],v1 offset:36864\n"
MF "v[192:195],a[216:219],a[68:71],v[192:195]\n"
"s_add_u32 s84,s14,9216\n"
TR "a[214:215],v1 offset:37376\n"
MF "v[192:195],a[220:223],a[108:111],v[192:195]\n"
LD "v5,s[24:27],s84 offen offset:1024 lds\n"
MF "v[200:203],a[220:223],a[112:115],v[200:203]\n"
"ds_read_b128 a[188:191],v3 offset:4096\n"
MF "v[200:203],a[216:219],a[72:75],v[200:203]\n"
"s_add_u32 m0,s62,16384\n"
MF "v[208:211],a[216:219],a[76:79],v[208:211]\n"
"s_add_u32 s82,s66,3145856\n"
"s_sub_u32 s15,s82,7864320\n"
"s_min_u32 s82,s82,s15\n"
MF "v[208:211],a[220:223],a[116:119],v[208:211]\n"
"s_waitcnt lgkmcnt(7)\n"
LD "v6,s[28:31],s82 offen lds\n"
MF "v[212:215],a[204:207],a[116:119],v[212:215]\n"
TR "a[216:217],v1 offset:32776\n"
MF "v[212:215],a[200:203],a[76:79],v[212:215]\n"
TR "a[218:219],v1 offset:33288\n"
MF "v[204:207],a[200:203],a[72:75],v[204:207]\n"
"ds_read_b128 a[152:155],v2 offset:6144\n"
MF "v[204:207],a[204:207],a[112:115],v[204:207]\n"
TR "a[220:221],v1 offset:36872\n"
MF "v[196:199],a[204:207],a[108:111],v[196:199]\n"
TR "a[222:223],v1 offset:37384\n"
MF "v[196:199],a[200:203],a[68:71],v[196:199]\n"
LD "v11,s[28:31],s82 offen offset:1024 lds\n"
MF "v[188:191],a[200:203],a[64:67],v[188:191]\n"
"v_add_u32 v0,0xc000,v0\n"
MF "v[188:191],a[204:207],a[104:107],v[188:191]\n"
"v_add_u32 v1,0xc000,v1\n"
MF "v[180:183],a[204:207],a[100:103],v[180:183]\n"
"s_waitcnt vmcnt(21) lgkmcnt(0)\n"
LD "v12,s[28:31],s82 offen offset:2048 lds\n"
MF "v[180:183],a[200:203],a[60:63],v[180:183]\n"
"s_barrier\n"
MF "v[216:219],a[208:211],a[60:63],v[216:219]\n"
TR "a[200:201],v0\n"
MF "v[216:219],a[212:215],a[100:103],v[216:219]\n"
TR "a[202:203],v0 offset:512\n"
MF "v[224:227],a[212:215],a[104:107],v[224:227]\n"
"ds_read_b128 a[192:195],v3 offset:6144\n"
MF "v[224:227],a[208:211],a[64:67],v[224:227]\n"
TR "a[204:205],v0 offset:4096\n"
MF "v[232:235],a[208:211],a[68:71],v[232:235]\n"
TR "a[206:207],v0 offset:4608\n"
MF "v[232:235],a[212:215],a[108:111],v[232:235]\n"
LD "v13,s[28:31],s82 offen offset:3072 lds\n"
MF "v[240:243],a[212:215],a[112:115],v[240:243]\n"
"ds_read_b128 a[156:159],v2 offset:8192\n"
MF "v[240:243],a[208:211],a[72:75],v[240:243]\n"
"s_add_u32 s84,s14,20480\n"
"ds_read_b128 a[196:199],v3 offset:8192\n"
MF "v[248:251],a[208:211],a[76:79],v[248:251]\n"
"s_add_u32 m0,s63,43008\n"
MF "v[248:251],a[212:215],a[116:119],v[248:251]\n"
LD "v4,s[24:27],s84 offen lds\n"
MF "v[252:255],a[220:223],a[116:119],v[252:255]\n"
TR "a[208:209],v0 offset:8\n"
MF "v[252:255],a[216:219],a[76:79],v[252:255]\n"
TR "a[210:211],v0 offset:520\n"
MF "v[244:247],a[216:219],a[72:75],v[244:247]\n"
MF "v[244:247],a[220:223],a[112:115],v[244:247]\n"
TR "a[212:213],v0 offset:4104\n"
MF "v[236:239],a[220:223],a[108:111],v[236:239]\n"
"s_add_u32 s84,s14,29696\n"
TR "a[214:215],v0 offset:4616\n"
MF "v[236:239],a[216:219],a[68:71],v[236:239]\n"
LD "v5,s[24:27],s84 offen offset:1024 lds\n"
MF "v[228:231],a[216:219],a[64:67],v[228:231]\n"
MF "v[228:231],a[220:223],a[104:107],v[228:231]\n"
"s_add_u32 m0,s62,32768\n"
MF "v[220:223],a[220:223],a[100:103],v[220:223]\n"
"s_add_u32 s82,s66,3145984\n"
"s_sub_u32 s15,s82,7864320\n"
"s_min_u32 s82,s82,s15\n"
MF "v[220:223],a[216:219],a[60:63],v[220:223]\n"
"s_waitcnt lgkmcnt(4)\n"
LD "v6,s[28:31],s82 offen lds\n"
MF "v[16:19],a[200:203],a[140:143],v[16:19]\n"
TR "a[216:217],v1\n"
MF "v[16:19],a[204:207],a[180:183],v[16:19]\n"
TR "a[218:219],v1 offset:512\n"
MF "v[24:27],a[204:207],a[184:187],v[24:27]\n"
MF "v[24:27],a[200:203],a[144:147],v[24:27]\n"
TR "a[220:221],v1 offset:4096\n"
MF "v[32:35],a[200:203],a[148:151],v[32:35]\n"
TR "a[222:223],v1 offset:4608\n"
MF "v[32:35],a[204:207],a[188:191],v[32:35]\n"
LD "v11,s[28:31],s82 offen offset:1024 lds\n"
MF "v[40:43],a[204:207],a[192:195],v[40:43]\n"
MF "v[40:43],a[200:203],a[152:155],v[40:43]\n"
MF "v[48:51],a[200:203],a[156:159],v[48:51]\n"
MF "v[48:51],a[204:207],a[196:199],v[48:51]\n"
"s_waitcnt lgkmcnt(4)\n"
LD "v12,s[28:31],s82 offen offset:2048 lds\n"
MF "v[52:55],a[212:215],a[196:199],v[52:55]\n"
TR "a[200:201],v1 offset:8\n"
MF "v[52:55],a[208:211],a[156:159],v[52:55]\n"
TR "a[202:203],v1 offset:520\n"
MF "v[44:47],a[208:211],a[152:155],v[44:47]\n"
MF "v[44:47],a[212:215],a[192:195],v[44:47]\n"
TR "a[204:205],v1 offset:4104\n"
MF "v[36:39],a[212:215],a[188:191],v[36:39]\n"
".Lpg2a:\n"
"s_cbranch_execz .Lisr1\n"
TR "a[206:207],v1 offset:4616\n"
MF "v[36:39],a[208:211],a[148:151],v[36:39]\n"
LD "v13,s[28:31],s82 offen offset:3072 lds\n"
MF "v[28:31],a[208:211],a[144:147],v[28:31]\n"
"s_add_u32 s84,s14,40960\n"
MF "v[28:31],a[212:215],a[184:187],v[28:31]\n"
"s_add_u32 m0,s63,45056\n"
MF "v[20:23],a[212:215],a[180:183],v[20:23]\n"
"s_waitcnt vmcnt(23) lgkmcnt(0)\n"
LD "v4,s[24:27],s84 offen lds\n"
MF "v[20:23],a[208:211],a[140:143],v[20:23]\n"
"s_barrier\n"
AW "a253,v10\n"
"v_add_u32 v10,1,v10\n"
"s_bfe_u32 s58,s22,0x30014\n"
"s_lshl_b32 s58,s58,2\n"
"s_add_u32 s58,s58,s56\n"
"s_lshl_b32 s58,s58,6\n"
"s_add_u32 s58,s58,s86\n"
"v_readlane_b32 s76,v10,s78\n"
"buffer_store_dword v10,v14,s[40:43],s58 offen offset:128 sc1\n"
".Lhs5:\n"
MF "v[56:59],a[216:219],a[140:143],v[56:59]\n"
"s_add_u32 m0,s62,49152\n"
"s_add_u32 s82,s66,3932160\n"
TR "a[208:209],v0 offset:16384\n"
MF "v[56:59],a[220:223],a[180:183],v[56:59]\n"
"s_sub_u32 s15,s82,7864320\n"
"s_min_u32 s82,s82,s15\n"
TR "a[210:211],v0 offset:16896\n"
MF "v[64:67],a[220:223],a[184:187],v[64:67]\n"
LD "v6,s[28:31],s82 offen lds\n"
MF "v[64:67],a[216:219],a[144:147],v[64:67]\n"
TR "a[212:213],v0 offset:20480\n"
MF "v[72:75],a[216:219],a[148:151],v[72:75]\n"
TR "a[214:215],v0 offset:20992\n"
MF "v[72:75],a[220:223],a[188:191],v[72:75]\n"
"buffer_load_dword a249,v15,s[48:51],s60 offen offset:1024\n"
MF "v[80:83],a[220:223],a[192:195],v[80:83]\n"
"s_bfe_u32 s59,s23,0x30003\n"
MF "v[80:83],a[216:219],a[152:155],v[80:83]\n"
LD "v11,s[28:31],s82 offen offset:1024 lds\n"
MF "v[88:91],a[216:219],a[156:159],v[88:91]\n"
"s_add_u32 s59,s59,1\n"
MF "v[88:91],a[220:223],a[196:199],v[88:91]\n"
"s_lshl_b32 s59,s59,7\n"
MF "v[92:95],a[204:207],a[196:199],v[92:95]\n"
"s_and_b32 s59,s59,768\n"
TR "a[216:217],v0 offset:16392\n"
MF "v[92:95],a[200:203],a[156:159],v[92:95]\n"
"s_add_u32 s59,s59,s60\n"
TR "a[218:219],v0 offset:16904\n"
MF "v[84:87],a[200:203],a[152:155],v[84:87]\n"
LD "v12,s[28:31],s82 offen offset:2048 lds\n"
MF "v[84:87],a[204:207],a[192:195],v[84:87]\n"
TR "a[220:221],v0 offset:20488\n"
MF "v[76:79],a[204:207],a[188:191],v[76:79]\n"
TR "a[222:223],v0 offset:21000\n"
MF "v[76:79],a[200:203],a[148:151],v[76:79]\n"
MF "v[68:71],a[200:203],a[144:147],v[68:71]\n"
MF "v[68:71],a[204:207],a[184:187],v[68:71]\n"
LD "v13,s[28:31],s82 offen offset:3072 lds\n"
MF "v[60:63],a[204:207],a[180:183],v[60:63]\n"
MF "v[60:63],a[200:203],a[140:143],v[60:63]\n"
"s_add_u32 s14,s81,768\n"
"s_sub_u32 s15,s14,1280\n"
"s_waitcnt lgkmcnt(4)\n"
MF "v[96:99],a[208:211],a[140:143],v[96:99]\n"
"s_min_u32 s14,s14,s15\n"
"s_add_u32 s14,s14,s68\n"
TR "a[200:201],v1 offset:16384\n"
MF "v[96:99],a[212:215],a[180:183],v[96:99]\n"
"s_add_u32 s84,s14,0\n"
"s_add_u32 m0,s63,0\n"
TR "a[202:203],v1 offset:16896\n"
MF "v[104:107],a[212:215],a[184:187],v[104:107]\n"
LD "v4,s[24:27],s84 offen lds\n"
MF "v[104:107],a[208:211],a[144:147],v[104:107]\n"
TR "a[204:205],v1 offset:20480\n"
MF "v[112:115],a[208:211],a[148:151],v[112:115]\n"
TR "a[206:207],v1 offset:20992\n"
MF "v[112:115],a[212:215],a[188:191],v[112:115]\n"
MF "v[120:123],a[212:215],a[192:195],v[120:123]\n"
"s_add_u32 s84,s14,9216\n"
MF "v[120:123],a[208:211],a[152:155],v[120:123]\n"
LD "v5,s[24:27],s84 offen offset:1024 lds\n"
MF "v[128:131],a[208:211],a[156:159],v[128:131]\n"
MF "v[128:131],a[212:215],a[196:199],v[128:131]\n"
"s_waitcnt lgkmcnt(4)\n"
MF "v[132:135],a[220:223],a[196:199],v[132:135]\n"
TR "a[208:209],v1 offset:16392\n"
MF "v[132:135],a[216:219],a[156:159],v[132:135]\n"
TR "a[210:211],v1 offset:16904\n"
MF "v[124:127],a[216:219],a[152:155],v[124:127]\n"
MF "v[124:127],a[220:223],a[192:195],v[124:127]\n"
TR "a[212:213],v1 offset:20488\n"
MF "v[116:119],a[220:223],a[188:191],v[116:119]\n"
TR "a[214:215],v1 offset:21000\n"
MF "v[116:119],a[216:219],a[148:151],v[116:119]\n"
MF "v[108:111],a[216:219],a[144:147],v[108:111]\n"
MF "v[108:111],a[220:223],a[184:187],v[108:111]\n"
MF "v[100:103],a[220:223],a[180:183],v[100:103]\n"
"s_waitcnt vmcnt(24) lgkmcnt(0)\n"
MF "v[100:103],a[216:219],a[140:143],v[100:103]\n"
"s_barrier\n"
MF "v[136:139],a[200:203],a[140:143],v[136:139]\n"
TR "a[216:217],v0 offset:32768\n"
MF "v[136:139],a[204:207],a[180:183],v[136:139]\n"
"s_add_u32 m0,s62,65536\n"
TR "a[218:219],v0 offset:33280\n"
MF "v[144:147],a[204:207],a[184:187],v[144:147]\n"
"s_add_u32 s82,s66,3932288\n"
"ds_read_b128 a[60:63],v2 offset:20480\n"
MF "v[144:147],a[200:203],a[144:147],v[144:147]\n"
"s_sub_u32 s15,s82,7864320\n"
TR "a[220:221],v0 offset:36864\n"
MF "v[152:155],a[200:203],a[148:151],v[152:155]\n"
"s_min_u32 s82,s82,s15\n"
TR "a[222:223],v0 offset:37376\n"
MF "v[152:155],a[204:207],a[188:191],v[152:155]\n"
LD "v6,s[28:31],s82 offen lds\n"
MF "v[160:163],a[204:207],a[192:195],v[160:163]\n"
"ds_read_b128 a[100:103],v3 offset:20480\n"
MF "v[160:163],a[200:203],a[152:155],v[160:163]\n"
MF "v[168:171],a[200:203],a[156:159],v[168:171]\n"
MF "v[168:171],a[204:207],a[196:199],v[168:171]\n"
LD "v11,s[28:31],s82 offen offset:1024 lds\n"
MF "v[172:175],a[212:215],a[196:199],v[172:175]\n"
TR "a[200:201],v0 offset:32776\n"
MF "v[172:175],a[208:211],a[156:159],v[172:175]\n"
TR "a[202:203],v0 offset:33288\n"
MF "v[164:167],a[208:211],a[152:155],v[164:167]\n"
"ds_read_b128 a[64:67],v2 offset:22528\n"
MF "v[164:167],a[212:215],a[192:195],v[164:167]\n"
TR "a[204:205],v0 offset:36872\n"
MF "v[156:159],a[212:215],a[188:191],v[156:159]\n"
TR "a[206:207],v0 offset:37384\n"
MF "v[156:159],a[208:211],a[148:151],v[156:159]\n"
LD "v12,s[28:31],s82 offen offset:2048 lds\n"
MF "v[148:151],a[208:211],a[144:147],v[148:151]\n"
"ds_read_b128 a[104:107],v3 offset:22528\n"
MF "v[148:151],a[212:215],a[184:187],v[148:151]\n"
MF "v[140:143],a[212:215],a[180:183],v[140:143]\n"
MF "v[140:143],a[208:211],a[140:143],v[140:143]\n"
"s_waitcnt lgkmcnt(7)\n"
LD "v13,s[28:31],s82 offen offset:3072 lds\n"
MF "v[176:179],a[216:219],a[140:143],v[176:179]\n"
TR "a[208:209],v1 offset:32768\n"
MF "v[176:179],a[220:223],a[180:183],v[176:179]\n"
TR "a[210:211],v1 offset:33280\n"
MF "v[184:187],a[220:223],a[184:187],v[184:187]\n"
"ds_read_b128 a[68:71],v2 offset:24576\n"
MF "v[184:187],a[216:219],a[144:147],v[184:187]\n"
"s_add_u32 s84,s14,20480\n"
TR "a[212:213],v1 offset:36864\n"
MF "v[192:195],a[216:219],a[148:151],v[192:195]\n"
"s_add_u32 m0,s63,2048\n"
TR "a[214:215],v1 offset:37376\n"
MF "v[192:195],a[220:223],a[188:191],v[192:195]\n"
LD "v4,s[24:27],s84 offen lds\n"
MF "v[200:203],a[220:223],a[192:195],v[200:203]\n"
"ds_read_b128 a[108:111],v3 offset:24576\n"
MF "v[200:203],a[216:219],a[152:155],v[200:203]\n"
MF "v[208:211],a[216:219],a[156:159],v[208:211]\n"
"s_add_u32 s84,s14,29696\n"
MF "v[208:211],a[220:223],a[196:199],v[208:211]\n"
"s_waitcnt lgkmcnt(7)\n"
LD "v5,s[24:27],s84 offen offset:1024 lds\n"
MF "v[212:215],a[204:207],a[196:199],v[212:215]\n"
TR "a[216:217],v1 offset:32776\n"
MF "v[212:215],a[200:203],a[156:159],v[212:215]\n"
TR "a[218:219],v1 offset:33288\n"
MF "v[204:207],a[200:203],a[152:155],v[204:207]\n"
"ds_read_b128 a[72:75],v2 offset:26624\n"
MF "v[204:207],a[204:207],a[192:195],v[204:207]\n"
TR "a[220:221],v1 offset:36872\n"
MF "v[196:199],a[204:207],a[188:191],v[196:199]\n"
TR "a[222:223],v1 offset:37384\n"
MF "v[196:199],a[200:203],a[148:151],v[196:199]\n"
MF "v[188:191],a[200:203],a[144:147],v[188:191]\n"
"v_add_u32 v0,0xffff4000,v0\n"
MF "v[188:191],a[204:207],a[184:187],v[188:191]\n"
"v_add_u32 v1,0xffff4000,v1\n"
MF "v[180:183],a[204:207],a[180:183],v[180:183]\n"
"s_waitcnt vmcnt(26) lgkmcnt(0)\n"
MF "v[180:183],a[200:203],a[140:143],v[180:183]\n"
"s_barrier\n"
MF "v[216:219],a[208:211],a[140:143],v[216:219]\n"
TR "a[200:201],v0\n"
MF "v[216:219],a[212:215],a[180:183],v[216:219]\n"
"s_add_u32 m0,s62,81920\n"
TR "a[202:203],v0 offset:512\n"
MF "v[224:227],a[212:215],a[184:187],v[224:227]\n"
"s_add_u32 s82,s66,3932416\n"
"ds_read_b128 a[112:115],v3 offset:26624\n"
MF "v[224:227],a[208:211],a[144:147],v[224:227]\n"
"s_sub_u32 s15,s82,7864320\n"
TR "a[204:205],v0 offset:4096\n"
MF "v[232:235],a[208:211],a[148:151],v[232:235]\n"
"s_min_u32 s82,s82,s15\n"
TR "a[206:207],v0 offset:4608\n"
MF "v[232:235],a[212:215],a[188:191],v[232:235]\n"
LD "v6,s[28:31],s82 offen lds\n"
MF "v[240:243],a[212:215],a[192:195],v[240:243]\n"
"ds_read_b128 a[76:79],v2 offset:28672\n"
MF "v[240:243],a[208:211],a[152:155],v[240:243]\n"
"ds_read_b128 a[116:119],v3 offset:28672\n"
MF "v[248:251],a[208:211],a[156:159],v[248:251]\n"
MF "v[248:251],a[212:215],a[196:199],v[248:251]\n"
LD "v11,s[28:31],s82 offen offset:1024 lds\n"
MF "v[252:255],a[220:223],a[196:199],v[252:255]\n"
TR "a[208:209],v0 offset:8\n"
MF "v[252:255],a[216:219],a[156:159],v[252:255]\n"
TR "a[210:211],v0 offset:520\n"
MF "v[244:247],a[216:219],a[152:155],v[244:247]\n"
MF "v[244:247],a[220:223],a[192:195],v[244:247]\n"
TR "a[212:213],v0 offset:4104\n"
MF "v[236:239],a[220:223],a[188:191],v[236:239]\n"
TR "a[214:215],v0 offset:4616\n"
MF "v[236:239],a[216:219],a[148:151],v[236:239]\n"
LD "v12,s[28:31],s82 offen offset:2048 lds\n"
MF "v[228:231],a[216:219],a[144:147],v[228:231]\n"
MF "v[228:231],a[220:223],a[184:187],v[228:231]\n"
MF "v[220:223],a[220:223],a[180:183],v[220:223]\n"
MF "v[220:223],a[216:219],a[140:143],v[220:223]\n"
"s_waitcnt lgkmcnt(4)\n"
LD "v13,s[28:31],s82 offen offset:3072 lds\n"
MF "v[16:19],a[200:203],a[60:63],v[16:19]\n"
TR "a[216:217],v1\n"
MF "v[16:19],a[204:207],a[100:103],v[16:19]\n"
TR "a[218:219],v1 offset:512\n"
MF "v[24:27],a[204:207],a[104:107],v[24:27]\n"
MF "v[24:27],a[200:203],a[64:67],v[24:27]\n"
"s_add_u32 s84,s14,40960\n"
TR "a[220:221],v1 offset:4096\n"
MF "v[32:35],a[200:203],a[68:71],v[32:35]\n"
"s_add_u32 m0,s63,4096\n"
TR "a[222:223],v1 offset:4608\n"
MF "v[32:35],a[204:207],a[108:111],v[32:35]\n"
LD "v4,s[24:27],s84 offen lds\n"
MF "v[40:43],a[204:207],a[112:115],v[40:43]\n"
MF "v[40:43],a[200:203],a[72:75],v[40:43]\n"
MF "v[48:51],a[200:203],a[76:79],v[48:51]\n"
MF "v[48:51],a[204:207],a[116:119],v[48:51]\n"
"s_waitcnt lgkmcnt(4)\n"
MF "v[52:55],a[212:215],a[116:119],v[52:55]\n"
TR "a[200:201],v1 offset:8\n"
MF "v[52:55],a[208:211],a[76:79],v[52:55]\n"
TR "a[202:203],v1 offset:520\n"
MF "v[44:47],a[208:211],a[72:75],v[44:47]\n"
MF "v[44:47],a[212:215],a[112:115],v[44:47]\n"
TR "a[204:205],v1 offset:4104\n"
MF "v[36:39],a[212:215],a[108:111],v[36:39]\n"
TR "a[206:207],v1 offset:4616\n"
MF "v[36:39],a[208:211],a[68:71],v[36:39]\n"
MF "v[28:31],a[208:211],a[64:67],v[28:31]\n"
MF "v[28:31],a[212:215],a[104:107],v[28:31]\n"
MF "v[20:23],a[212:215],a[100:103],v[20:23]\n"
"s_waitcnt vmcnt(25) lgkmcnt(0)\n"
MF "v[20:23],a[208:211],a[60:63],v[20:23]\n"
"s_barrier\n"
MF "v[56:59],a[216:219],a[60:63],v[56:59]\n"
"s_add_u32 m0,s62,0\n"
"s_add_u32 s82,s66,4718592\n"
TR "a[208:209],v0 offset:16384\n"
MF "v[56:59],a[220:223],a[100:103],v[56:59]\n"
"s_sub_u32 s15,s82,7864320\n"
"s_min_u32 s82,s82,s15\n"
TR "a[210:211],v0 offset:16896\n"
MF "v[64:67],a[220:223],a[104:107],v[64:67]\n"
LD "v6,s[28:31],s82 offen lds\n"
MF "v[64:67],a[216:219],a[64:67],v[64:67]\n"
TR "a[212:213],v0 offset:20480\n"
MF "v[72:75],a[216:219],a[68:71],v[72:75]\n"
TR "a[214:215],v0 offset:20992\n"
MF "v[72:75],a[220:223],a[108:111],v[72:75]\n"
"buffer_load_dword a249,v15,s[48:51],s59 offen\n"
MF "v[80:83],a[220:223],a[112:115],v[80:83]\n"
"s_bfe_u32 s59,s23,0x30003\n"
MF "v[80:83],a[216:219],a[72:75],v[80:83]\n"
LD "v11,s[28:31],s82 offen offset:1024 lds\n"
MF "v[88:91],a[216:219],a[76:79],v[88:91]\n"
"s_add_u32 s59,s59,3\n"
MF "v[88:91],a[220:223],a[116:119],v[88:91]\n"
"s_lshl_b32 s59,s59,7\n"
MF "v[92:95],a[204:207],a[116:119],v[92:95]\n"
"s_and_b32 s59,s59,768\n"
TR "a[216:217],v0 offset:16392\n"
MF "v[92:95],a[200:203],a[76:79],v[92:95]\n"
"s_add_u32 s59,s59,s60\n"
TR "a[218:219],v0 offset:16904\n"
MF "v[84:87],a[200:203],a[72:75],v[84:87]\n"
LD "v12,s[28:31],s82 offen offset:2048 lds\n"
MF "v[84:87],a[204:207],a[112:115],v[84:87]\n"
TR "a[220:221],v0 offset:20488\n"
MF "v[76:79],a[204:207],a[108:111],v[76:79]\n"
TR "a[222:223],v0 offset:21000\n"
MF "v[76:79],a[200:203],a[68:71],v[76:79]\n"
MF "v[68:71],a[200:203],a[64:67],v[68:71]\n"
MF "v[68:71],a[204:207],a[104:107],v[68:71]\n"
LD "v13,s[28:31],s82 offen offset:3072 lds\n"
MF "v[60:63],a[204:207],a[100:103],v[60:63]\n"
MF "v[60:63],a[200:203],a[60:63],v[60:63]\n"
"s_add_u32 s14,s81,896\n"
"s_sub_u32 s15,s14,1280\n"
"s_waitcnt lgkmcnt(4)\n"
MF "v[96:99],a[208:211],a[60:63],v[96:99]\n"
"s_min_u32 s14,s14,s15\n"
"s_add_u32 s14,s14,s68\n"
TR "a[200:201],v1 offset:16384\n"
MF "v[96:99],a[212:215],a[100:103],v[96:99]\n"
"s_add_u32 s84,s14,0\n"
"s_add_u32 m0,s63,20480\n"
TR "a[202:203],v1 offset:16896\n"
MF "v[104:107],a[212:215],a[104:107],v[104:107]\n"
LD "v4,s[24:27],s84 offen lds\n"
MF "v[104:107],a[208:211],a[64:67],v[104:107]\n"
TR "a[204:205],v1 offset:20480\n"
MF "v[112:115],a[208:211],a[68:71],v[112:115]\n"
TR "a[206:207],v1 offset:20992\n"
MF "v[112:115],a[212:215],a[108:111],v[112:115]\n"
MF "v[120:123],a[212:215],a[112:115],v[120:123]\n"
"s_add_u32 s84,s14,9216\n"
MF "v[120:123],a[208:211],a[72:75],v[120:123]\n"
LD "v5,s[24:27],s84 offen offset:1024 lds\n"
MF "v[128:131],a[208:211],a[76:79],v[128:131]\n"
MF "v[128:131],a[212:215],a[116:119],v[128:131]\n"
"s_waitcnt lgkmcnt(4)\n"
MF "v[132:135],a[220:223],a[116:119],v[132:135]\n"
TR "a[208:209],v1 offset:16392\n"
MF "v[132:135],a[216:219],a[76:79],v[132:135]\n"
TR "a[210:211],v1 offset:16904\n"
MF "v[124:127],a[216:219],a[72:75],v[124:127]\n"
MF "v[124:127],a[220:223],a[112:115],v[124:127]\n"
TR "a[212:213],v1 offset:20488\n"
MF "v[116:119],a[220:223],a[108:111],v[116:119]\n"
TR "a[214:215],v1 offset:21000\n"
MF "v[116:119],a[216:219],a[68:71],v[116:119]\n"
MF "v[108:111],a[216:219],a[64:67],v[108:111]\n"
MF "v[108:111],a[220:223],a[104:107],v[108:111]\n"
MF "v[100:103],a[220:223],a[100:103],v[100:103]\n"
"s_waitcnt vmcnt(25) lgkmcnt(0)\n"
MF "v[100:103],a[216:219],a[60:63],v[100:103]\n"
"s_barrier\n"
MF "v[136:139],a[200:203],a[60:63],v[136:139]\n"
TR "a[216:217],v0 offset:32768\n"
MF "v[136:139],a[204:207],a[100:103],v[136:139]\n"
"s_add_u32 m0,s62,16384\n"
TR "a[218:219],v0 offset:33280\n"
MF "v[144:147],a[204:207],a[104:107],v[144:147]\n"
"s_add_u32 s82,s66,4718720\n"
"ds_read_b128 a[140:143],v2 offset:40960\n"
MF "v[144:147],a[200:203],a[64:67],v[144:147]\n"
"s_sub_u32 s15,s82,7864320\n"
TR "a[220:221],v0 offset:36864\n"
MF "v[152:155],a[200:203],a[68:71],v[152:155]\n"
"s_min_u32 s82,s82,s15\n"
TR "a[222:223],v0 offset:37376\n"
MF "v[152:155],a[204:207],a[108:111],v[152:155]\n"
LD "v6,s[28:31],s82 offen lds\n"
MF "v[160:163],a[204:207],a[112:115],v[160:163]\n"
"ds_read_b128 a[180:183],v3 offset:40960\n"
MF "v[160:163],a[200:203],a[72:75],v[160:163]\n"
MF "v[168:171],a[200:203],a[76:79],v[168:171]\n"
MF "v[168:171],a[204:207],a[116:119],v[168:171]\n"
LD "v11,s[28:31],s82 offen offset:1024 lds\n"
MF "v[172:175],a[212:215],a[116:119],v[172:175]\n"
TR "a[200:201],v0 offset:32776\n"
MF "v[172:175],a[208:211],a[76:79],v[172:175]\n"
TR "a[202:203],v0 offset:33288\n"
MF "v[164:167],a[208:211],a[72:75],v[164:167]\n"
"ds_read_b128 a[144:147],v2 offset:43008\n"
MF "v[164:167],a[212:215],a[112:115],v[164:167]\n"
TR "a[204:205],v0 offset:36872\n"
MF "v[156:159],a[212:215],a[108:111],v[156:159]\n"
TR "a[206:207],v0 offset:37384\n"
MF "v[156:159],a[208:211],a[68:71],v[156:159]\n"
LD "v12,s[28:31],s82 offen offset:2048 lds\n"
MF "v[148:151],a[208:211],a[64:67],v[148:151]\n"
"ds_read_b128 a[184:187],v3 offset:43008\n"
MF "v[148:151],a[212:215],a[104:107],v[148:151]\n"
MF "v[140:143],a[212:215],a[100:103],v[140:143]\n"
MF "v[140:143],a[208:211],a[60:63],v[140:143]\n"
"s_waitcnt lgkmcnt(7)\n"
LD "v13,s[28:31],s82 offen offset:3072 lds\n"
MF "v[176:179],a[216:219],a[60:63],v[176:179]\n"
TR "a[208:209],v1 offset:32768\n"
MF "v[176:179],a[220:223],a[100:103],v[176:179]\n"
TR "a[210:211],v1 offset:33280\n"
MF "v[184:187],a[220:223],a[104:107],v[184:187]\n"
"ds_read_b128 a[148:151],v2 offset:45056\n"
MF "v[184:187],a[216:219],a[64:67],v[184:187]\n"
"s_add_u32 s84,s14,20480\n"
TR "a[212:213],v1 offset:36864\n"
MF "v[192:195],a[216:219],a[68:71],v[192:195]\n"
"s_add_u32 m0,s63,22528\n"
TR "a[214:215],v1 offset:37376\n"
MF "v[192:195],a[220:223],a[108:111],v[192:195]\n"
LD "v4,s[24:27],s84 offen lds\n"
MF "v[200:203],a[220:223],a[112:115],v[200:203]\n"
"ds_read_b128 a[188:191],v3 offset:45056\n"
MF "v[200:203],a[216:219],a[72:75],v[200:203]\n"
MF "v[208:211],a[216:219],a[76:79],v[208:211]\n"
"s_add_u32 s84,s14,29696\n"
MF "v[208:211],a[220:223],a[116:119],v[208:211]\n"
"s_waitcnt lgkmcnt(7)\n"
LD "v5,s[24:27],s84 offen offset:1024 lds\n"
MF "v[212:215],a[204:207],a[116:119],v[212:215]\n"
TR "a[216:217],v1 offset:32776\n"
MF "v[212:215],a[200:203],a[76:79],v[212:215]\n"
TR "a[218:219],v1 offset:33288\n"
MF "v[204:207],a[200:203],a[72:75],v[204:207]\n"
"ds_read_b128 a[152:155],v2 offset:47104\n"
MF "v[204:207],a[204:207],a[112:115],v[204:207]\n"
TR "a[220:221],v1 offset:36872\n"
MF "v[196:199],a[204:207],a[108:111],v[196:199]\n"
TR "a[222:223],v1 offset:37384\n"
MF "v[196:199],a[200:203],a[68:71],v[196:199]\n"
MF "v[188:191],a[200:203],a[64:67],v[188:191]\n"
"v_add_u32 v0,0xc000,v0\n"
MF "v[188:191],a[204:207],a[104:107],v[188:191]\n"
"v_add_u32 v1,0xc000,v1\n"
MF "v[180:183],a[204:207],a[100:103],v[180:183]\n"
"s_waitcnt vmcnt(26) lgkmcnt(0)\n"
MF "v[180:183],a[200:203],a[60:63],v[180:183]\n"
"s_barrier\n"
MF "v[216:219],a[208:211],a[60:63],v[216:219]\n"
TR "a[200:201],v0\n"
MF "v[216:219],a[212:215],a[100:103],v[216:219]\n"
"s_add_u32 m0,s62,32768\n"
TR "a[202:203],v0 offset:512\n"
MF "v[224:227],a[212:215],a[104:107],v[224:227]\n"
"s_add_u32 s82,s66,4718848\n"
"ds_read_b128 a[192:195],v3 offset:47104\n"
MF "v[224:227],a[208:211],a[64:67],v[224:227]\n"
"s_sub_u32 s15,s82,7864320\n"
TR "a[204:205],v0 offset:4096\n"
MF "v[232:235],a[208:211],a[68:71],v[232:235]\n"
"s_min_u32 s82,s82,s15\n"
TR "a[206:207],v0 offset:4608\n"
MF "v[232:235],a[212:215],a[108:111],v[232:235]\n"
LD "v6,s[28:31],s82 offen lds\n"
MF "v[240:243],a[212:215],a[112:115],v[240:243]\n"
"ds_read_b128 a[156:159],v2 offset:49152\n"
MF "v[240:243],a[208:211],a[72:75],v[240:243]\n"
"ds_read_b128 a[196:199],v3 offset:49152\n"
MF "v[248:251],a[208:211],a[76:79],v[248:251]\n"
MF "v[248:251],a[212:215],a[116:119],v[248:251]\n"
LD "v11,s[28:31],s82 offen offset:1024 lds\n"
MF "v[252:255],a[220:223],a[116:119],v[252:255]\n"
TR "a[208:209],v0 offset:8\n"
MF "v[252:255],a[216:219],a[76:79],v[252:255]\n"
TR "a[210:211],v0 offset:520\n"
MF "v[244:247],a[216:219],a[72:75],v[244:247]\n"
MF "v[244:247],a[220:223],a[112:115],v[244:247]\n"
TR "a[212:213],v0 offset:4104\n"
MF "v[236:239],a[220:223],a[108:111],v[236:239]\n"
TR "a[214:215],v0 offset:4616\n"
MF "v[236:239],a[216:219],a[68:71],v[236:239]\n"
LD "v12,s[28:31],s82 offen offset:2048 lds\n"
MF "v[228:231],a[216:219],a[64:67],v[228:231]\n"
MF "v[228:231],a[220:223],a[104:107],v[228:231]\n"
MF "v[220:223],a[220:223],a[100:103],v[220:223]\n"
MF "v[220:223],a[216:219],a[60:63],v[220:223]\n"
"s_waitcnt lgkmcnt(4)\n"
LD "v13,s[28:31],s82 offen offset:3072 lds\n"
MF "v[16:19],a[200:203],a[140:143],v[16:19]\n"
TR "a[216:217],v1\n"
MF "v[16:19],a[204:207],a[180:183],v[16:19]\n"
TR "a[218:219],v1 offset:512\n"
MF "v[24:27],a[204:207],a[184:187],v[24:27]\n"
MF "v[24:27],a[200:203],a[144:147],v[24:27]\n"
"s_add_u32 s84,s14,40960\n"
TR "a[220:221],v1 offset:4096\n"
MF "v[32:35],a[200:203],a[148:151],v[32:35]\n"
"s_add_u32 m0,s63,24576\n"
TR "a[222:223],v1 offset:4608\n"
MF "v[32:35],a[204:207],a[188:191],v[32:35]\n"
LD "v4,s[24:27],s84 offen lds\n"
MF "v[40:43],a[204:207],a[192:195],v[40:43]\n"
MF "v[40:43],a[200:203],a[152:155],v[40:43]\n"
MF "v[48:51],a[200:203],a[156:159],v[48:51]\n"
MF "v[48:51],a[204:207],a[196:199],v[48:51]\n"
"s_waitcnt lgkmcnt(4)\n"
MF "v[52:55],a[212:215],a[196:199],v[52:55]\n"
TR "a[200:201],v1 offset:8\n"
MF "v[52:55],a[208:211],a[156:159],v[52:55]\n"
TR "a[202:203],v1 offset:520\n"
MF "v[44:47],a[208:211],a[152:155],v[44:47]\n"
MF "v[44:47],a[212:215],a[192:195],v[44:47]\n"
TR "a[204:205],v1 offset:4104\n"
MF "v[36:39],a[212:215],a[188:191],v[36:39]\n"
TR "a[206:207],v1 offset:4616\n"
MF "v[36:39],a[208:211],a[148:151],v[36:39]\n"
MF "v[28:31],a[208:211],a[144:147],v[28:31]\n"
MF "v[28:31],a[212:215],a[184:187],v[28:31]\n"
MF "v[20:23],a[212:215],a[180:183],v[20:23]\n"
"s_waitcnt vmcnt(25) lgkmcnt(0)\n"
MF "v[20:23],a[208:211],a[140:143],v[20:23]\n"
"s_barrier\n"
MF "v[56:59],a[216:219],a[140:143],v[56:59]\n"
"s_add_u32 m0,s62,49152\n"
"s_add_u32 s82,s66,5505024\n"
TR "a[208:209],v0 offset:16384\n"
MF "v[56:59],a[220:223],a[180:183],v[56:59]\n"
"s_sub_u32 s15,s82,7864320\n"
"s_min_u32 s82,s82,s15\n"
TR "a[210:211],v0 offset:16896\n"
MF "v[64:67],a[220:223],a[184:187],v[64:67]\n"
LD "v6,s[28:31],s82 offen lds\n"
MF "v[64:67],a[216:219],a[144:147],v[64:67]\n"
TR "a[212:213],v0 offset:20480\n"
MF "v[72:75],a[216:219],a[148:151],v[72:75]\n"
TR "a[214:215],v0 offset:20992\n"
MF "v[72:75],a[220:223],a[188:191],v[72:75]\n"
"buffer_load_dword a249,v15,s[48:51],s59 offen\n"
MF "v[80:83],a[220:223],a[192:195],v[80:83]\n"
"s_bfe_u32 s59,s23,0x30003\n"
MF "v[80:83],a[216:219],a[152:155],v[80:83]\n"
LD "v11,s[28:31],s82 offen offset:1024 lds\n"
MF "v[88:91],a[216:219],a[156:159],v[88:91]\n"
"s_add_u32 s59,s59,5\n"
MF "v[88:91],a[220:223],a[196:199],v[88:91]\n"
"s_lshl_b32 s59,s59,7\n"
MF "v[92:95],a[204:207],a[196:199],v[92:95]\n"
"s_and_b32 s59,s59,768\n"
TR "a[216:217],v0 offset:16392\n"
MF "v[92:95],a[200:203],a[156:159],v[92:95]\n"
"s_add_u32 s59,s59,s60\n"
TR "a[218:219],v0 offset:16904\n"
MF "v[84:87],a[200:203],a[152:155],v[84:87]\n"
LD "v12,s[28:31],s82 offen offset:2048 lds\n"
MF "v[84:87],a[204:207],a[192:195],v[84:87]\n"
TR "a[220:221],v0 offset:20488\n"
MF "v[76:79],a[204:207],a[188:191],v[76:79]\n"
TR "a[222:223],v0 offset:21000\n"
MF "v[76:79],a[200:203],a[148:151],v[76:79]\n"
MF "v[68:71],a[200:203],a[144:147],v[68:71]\n"
MF "v[68:71],a[204:207],a[184:187],v[68:71]\n"
LD "v13,s[28:31],s82 offen offset:3072 lds\n"
MF "v[60:63],a[204:207],a[180:183],v[60:63]\n"
".Lpg3a:\n"
"s_cbranch_execz .Lisr1\n"
MF "v[60:63],a[200:203],a[140:143],v[60:63]\n"
"s_add_u32 s14,s81,1024\n"
"s_sub_u32 s15,s14,1280\n"
"s_waitcnt lgkmcnt(4)\n"
MF "v[96:99],a[208:211],a[140:143],v[96:99]\n"
"s_min_u32 s14,s14,s15\n"
"s_add_u32 s14,s14,s68\n"
TR "a[200:201],v1 offset:16384\n"
MF "v[96:99],a[212:215],a[180:183],v[96:99]\n"
"s_add_u32 s84,s14,0\n"
"s_add_u32 m0,s63,40960\n"
TR "a[202:203],v1 offset:16896\n"
MF "v[104:107],a[212:215],a[184:187],v[104:107]\n"
LD "v4,s[24:27],s84 offen lds\n"
MF "v[104:107],a[208:211],a[144:147],v[104:107]\n"
TR "a[204:205],v1 offset:20480\n"
MF "v[112:115],a[208:211],a[148:151],v[112:115]\n"
TR "a[206:207],v1 offset:20992\n"
MF "v[112:115],a[212:215],a[188:191],v[112:115]\n"
MF "v[120:123],a[212:215],a[192:195],v[120:123]\n"
"s_add_u32 s84,s14,9216\n"
MF "v[120:123],a[208:211],a[152:155],v[120:123]\n"
LD "v5,s[24:27],s84 offen offset:1024 lds\n"
MF "v[128:131],a[208:211],a[156:159],v[128:131]\n"
MF "v[128:131],a[212:215],a[196:199],v[128:131]\n"
"s_waitcnt lgkmcnt(4)\n"
MF "v[132:135],a[220:223],a[196:199],v[132:135]\n"
TR "a[208:209],v1 offset:16392\n"
MF "v[132:135],a[216:219],a[156:159],v[132:135]\n"
TR "a[210:211],v1 offset:16904\n"
MF "v[124:127],a[216:219],a[152:155],v[124:127]\n"
MF "v[124:127],a[220:223],a[192:195],v[124:127]\n"
TR "a[212:213],v1 offset:20488\n"
MF "v[116:119],a[220:223],a[188:191],v[116:119]\n"
TR "a[214:215],v1 offset:21000\n"
MF "v[116:119],a[216:219],a[148:151],v[116:119]\n"
MF "v[108:111],a[216:219],a[144:147],v[108:111]\n"
MF "v[108:111],a[220:223],a[184:187],v[108:111]\n"
MF "v[100:103],a[220:223],a[180:183],v[100:103]\n"
"s_waitcnt vmcnt(25) lgkmcnt(0)\n"
MF "v[100:103],a[216:219],a[140:143],v[100:103]\n"
"s_barrier\n"
MF "v[136:139],a[200:203],a[140:143],v[136:139]\n"
TR "a[216:217],v0 offset:32768\n"
MF "v[136:139],a[204:207],a[180:183],v[136:139]\n"
"s_add_u32 m0,s62,65536\n"
TR "a[218:219],v0 offset:33280\n"
MF "v[144:147],a[204:207],a[184:187],v[144:147]\n"
"s_add_u32 s82,s66,5505152\n"
"ds_read_b128 a[60:63],v2\n"
MF "v[144:147],a[200:203],a[144:147],v[144:147]\n"
"s_sub_u32 s15,s82,7864320\n"
TR "a[220:221],v0 offset:36864\n"
MF "v[152:155],a[200:203],a[148:151],v[152:155]\n"
"s_min_u32 s82,s82,s15\n"
TR "a[222:223],v0 offset:37376\n"
MF "v[152:155],a[204:207],a[188:191],v[152:155]\n"
LD "v6,s[28:31],s82 offen lds\n"
MF "v[160:163],a[204:207],a[192:195],v[160:163]\n"
"ds_read_b128 a[100:103],v3\n"
MF "v[160:163],a[200:203],a[152:155],v[160:163]\n"
MF "v[168:171],a[200:203],a[156:159],v[168:171]\n"
MF "v[168:171],a[204:207],a[196:199],v[168:171]\n"
LD "v11,s[28:31],s82 offen offset:1024 lds\n"
MF "v[172:175],a[212:215],a[196:199],v[172:175]\n"
TR "a[200:201],v0 offset:32776\n"
MF "v[172:175],a[208:211],a[156:159],v[172:175]\n"
TR "a[202:203],v0 offset:33288\n"
MF "v[164:167],a[208:211],a[152:155],v[164:167]\n"
"ds_read_b128 a[64:67],v2 offset:2048\n"
MF "v[164:167],a[212:215],a[192:195],v[164:167]\n"
TR "a[204:205],v0 offset:36872\n"
MF "v[156:159],a[212:215],a[188:191],v[156:159]\n"
TR "a[206:207],v0 offset:37384\n"
MF "v[156:159],a[208:211],a[148:151],v[156:159]\n"
LD "v12,s[28:31],s82 offen offset:2048 lds\n"
MF "v[148:151],a[208:211],a[144:147],v[148:151]\n"
"ds_read_b128 a[104:107],v3 offset:2048\n"
MF "v[148:151],a[212:215],a[184:187],v[148:151]\n"
MF "v[140:143],a[212:215],a[180:183],v[140:143]\n"
MF "v[140:143],a[208:211],a[140:143],v[140:143]\n"
"s_waitcnt lgkmcnt(7)\n"
LD "v13,s[28:31],s82 offen offset:3072 lds\n"
MF "v[176:179],a[216:219],a[140:143],v[176:179]\n"
TR "a[208:209],v1 offset:32768\n"
MF "v[176:179],a[220:223],a[180:183],v[176:179]\n"
TR "a[210:211],v1 offset:33280\n"
MF "v[184:187],a[220:223],a[184:187],v[184:187]\n"
"ds_read_b128 a[68:71],v2 offset:4096\n"
MF "v[184:187],a[216:219],a[144:147],v[184:187]\n"
"s_add_u32 s84,s14,20480\n"
TR "a[212:213],v1 offset:36864\n"
MF "v[192:195],a[216:219],a[148:151],v[192:195]\n"
"s_add_u32 m0,s63,43008\n"
TR "a[214:215],v1 offset:37376\n"
MF "v[192:195],a[220:223],a[188:191],v[192:195]\n"
LD "v4,s[24:27],s84 offen lds\n"
MF "v[200:203],a[220:223],a[192:195],v[200:203]\n"
"ds_read_b128 a[108:111],v3 offset:4096\n"
MF "v[200:203],a[216:219],a[152:155],v[200:203]\n"
MF "v[208:211],a[216:219],a[156:159],v[208:211]\n"
"s_add_u32 s84,s14,29696\n"
MF "v[208:211],a[220:223],a[196:199],v[208:211]\n"
"s_waitcnt lgkmcnt(7)\n"
LD "v5,s[24:27],s84 offen offset:1024 lds\n"
MF "v[212:215],a[204:207],a[196:199],v[212:215]\n"
TR "a[216:217],v1 offset:32776\n"
MF "v[212:215],a[200:203],a[156:159],v[212:215]\n"
TR "a[218:219],v1 offset:33288\n"
MF "v[204:207],a[200:203],a[152:155],v[204:207]\n"
"ds_read_b128 a[72:75],v2 offset:6144\n"
MF "v[204:207],a[204:207],a[192:195],v[204:207]\n"
TR "a[220:221],v1 offset:36872\n"
MF "v[196:199],a[204:207],a[188:191],v[196:199]\n"
TR "a[222:223],v1 offset:37384\n"
MF "v[196:199],a[200:203],a[148:151],v[196:199]\n"
MF "v[188:191],a[200:203],a[144:147],v[188:191]\n"
"v_add_u32 v0,0xffff4000,v0\n"
MF "v[188:191],a[204:207],a[184:187],v[188:191]\n"
"v_add_u32 v1,0xffff4000,v1\n"
MF "v[180:183],a[204:207],a[180:183],v[180:183]\n"
"s_waitcnt vmcnt(26) lgkmcnt(0)\n"
MF "v[180:183],a[200:203],a[140:143],v[180:183]\n"
"s_barrier\n"
MF "v[216:219],a[208:211],a[140:143],v[216:219]\n"
TR "a[200:201],v0\n"
MF "v[216:219],a[212:215],a[180:183],v[216:219]\n"
"s_add_u32 m0,s62,81920\n"
TR "a[202:203],v0 offset:512\n"
MF "v[224:227],a[212:215],a[184:187],v[224:227]\n"
"s_add_u32 s82,s66,5505280\n"
"ds_read_b128 a[112:115],v3 offset:6144\n"
MF "v[224:227],a[208:211],a[144:147],v[224:227]\n"
"s_sub_u32 s15,s82,7864320\n"
TR "a[204:205],v0 offset:4096\n"
MF "v[232:235],a[208:211],a[148:151],v[232:235]\n"
"s_min_u32 s82,s82,s15\n"
TR "a[206:207],v0 offset:4608\n"
MF "v[232:235],a[212:215],a[188:191],v[232:235]\n"
LD "v6,s[28:31],s82 offen lds\n"
MF "v[240:243],a[212:215],a[192:195],v[240:243]\n"
"ds_read_b128 a[76:79],v2 offset:8192\n"
MF "v[240:243],a[208:211],a[152:155],v[240:243]\n"
"ds_read_b128 a[116:119],v3 offset:8192\n"
MF "v[248:251],a[208:211],a[156:159],v[248:251]\n"
MF "v[248:251],a[212:215],a[196:199],v[248:251]\n"
LD "v11,s[28:31],s82 offen offset:1024 lds\n"
MF "v[252:255],a[220:223],a[196:199],v[252:255]\n"
TR "a[208:209],v0 offset:8\n"
MF "v[252:255],a[216:219],a[156:159],v[252:255]\n"
TR "a[210:211],v0 offset:520\n"
MF "v[244:247],a[216:219],a[152:155],v[244:247]\n"
MF "v[244:247],a[220:223],a[192:195],v[244:247]\n"
TR "a[212:213],v0 offset:4104\n"
MF "v[236:239],a[220:223],a[188:191],v[236:239]\n"
TR "a[214:215],v0 offset:4616\n"
MF "v[236:239],a[216:219],a[148:151],v[236:239]\n"
LD "v12,s[28:31],s82 offen offset:2048 lds\n"
MF "v[228:231],a[216:219],a[144:147],v[228:231]\n"
MF "v[228:231],a[220:223],a[184:187],v[228:231]\n"
MF "v[220:223],a[220:223],a[180:183],v[220:223]\n"
MF "v[220:223],a[216:219],a[140:143],v[220:223]\n"
"s_waitcnt lgkmcnt(4)\n"
LD "v13,s[28:31],s82 offen offset:3072 lds\n"
MF "v[16:19],a[200:203],a[60:63],v[16:19]\n"
TR "a[216:217],v1\n"
MF "v[16:19],a[204:207],a[100:103],v[16:19]\n"
TR "a[218:219],v1 offset:512\n"
MF "v[24:27],a[204:207],a[104:107],v[24:27]\n"
MF "v[24:27],a[200:203],a[64:67],v[24:27]\n"
"s_add_u32 s84,s14,40960\n"
TR "a[220:221],v1 offset:4096\n"
MF "v[32:35],a[200:203],a[68:71],v[32:35]\n"
"s_add_u32 m0,s63,45056\n"
TR "a[222:223],v1 offset:4608\n"
MF "v[32:35],a[204:207],a[108:111],v[32:35]\n"
LD "v4,s[24:27],s84 offen lds\n"
MF "v[40:43],a[204:207],a[112:115],v[40:43]\n"
MF "v[40:43],a[200:203],a[72:75],v[40:43]\n"
MF "v[48:51],a[200:203],a[76:79],v[48:51]\n"
MF "v[48:51],a[204:207],a[116:119],v[48:51]\n"
"s_waitcnt lgkmcnt(4)\n"
MF "v[52:55],a[212:215],a[116:119],v[52:55]\n"
TR "a[200:201],v1 offset:8\n"
MF "v[52:55],a[208:211],a[76:79],v[52:55]\n"
TR "a[202:203],v1 offset:520\n"
MF "v[44:47],a[208:211],a[72:75],v[44:47]\n"
MF "v[44:47],a[212:215],a[112:115],v[44:47]\n"
TR "a[204:205],v1 offset:4104\n"
MF "v[36:39],a[212:215],a[108:111],v[36:39]\n"
TR "a[206:207],v1 offset:4616\n"
MF "v[36:39],a[208:211],a[68:71],v[36:39]\n"
MF "v[28:31],a[208:211],a[64:67],v[28:31]\n"
MF "v[28:31],a[212:215],a[104:107],v[28:31]\n"
MF "v[20:23],a[212:215],a[100:103],v[20:23]\n"
"s_waitcnt vmcnt(25) lgkmcnt(0)\n"
MF "v[20:23],a[208:211],a[60:63],v[20:23]\n"
"s_barrier\n"
MF "v[56:59],a[216:219],a[60:63],v[56:59]\n"
"s_add_u32 m0,s62,0\n"
"s_add_u32 s82,s66,6291712\n"
TR "a[208:209],v0 offset:16384\n"
MF "v[56:59],a[220:223],a[100:103],v[56:59]\n"
"s_sub_u32 s15,s82,7864320\n"
"s_min_u32 s82,s82,s15\n"
TR "a[210:211],v0 offset:16896\n"
MF "v[64:67],a[220:223],a[104:107],v[64:67]\n"
LD "v6,s[28:31],s82 offen lds\n"
MF "v[64:67],a[216:219],a[64:67],v[64:67]\n"
TR "a[212:213],v0 offset:20480\n"
MF "v[72:75],a[216:219],a[68:71],v[72:75]\n"
TR "a[214:215],v0 offset:20992\n"
MF "v[72:75],a[220:223],a[108:111],v[72:75]\n"
"buffer_load_dword a249,v15,s[48:51],s59 offen\n"
MF "v[80:83],a[220:223],a[112:115],v[80:83]\n"
MF "v[80:83],a[216:219],a[72:75],v[80:83]\n"
LD "v11,s[28:31],s82 offen offset:1024 lds\n"
MF "v[88:91],a[216:219],a[76:79],v[88:91]\n"
MF "v[88:91],a[220:223],a[116:119],v[88:91]\n"
MF "v[92:95],a[204:207],a[116:119],v[92:95]\n"
TR "a[216:217],v0 offset:16392\n"
MF "v[92:95],a[200:203],a[76:79],v[92:95]\n"
TR "a[218:219],v0 offset:16904\n"
MF "v[84:87],a[200:203],a[72:75],v[84:87]\n"
LD "v12,s[28:31],s82 offen offset:2048 lds\n"
MF "v[84:87],a[204:207],a[112:115],v[84:87]\n"
TR "a[220:221],v0 offset:20488\n"
MF "v[76:79],a[204:207],a[108:111],v[76:79]\n"
TR "a[222:223],v0 offset:21000\n"
MF "v[76:79],a[200:203],a[68:71],v[76:79]\n"
MF "v[68:71],a[200:203],a[64:67],v[68:71]\n"
MF "v[68:71],a[204:207],a[104:107],v[68:71]\n"
LD "v13,s[28:31],s82 offen offset:3072 lds\n"
MF "v[60:63],a[204:207],a[100:103],v[60:63]\n"
MF "v[60:63],a[200:203],a[60:63],v[60:63]\n"
"s_add_u32 s14,s81,1152\n"
"s_sub_u32 s15,s14,1280\n"
"s_waitcnt lgkmcnt(4)\n"
MF "v[96:99],a[208:211],a[60:63],v[96:99]\n"
"s_min_u32 s14,s14,s15\n"
"s_add_u32 s14,s14,s68\n"
TR "a[200:201],v1 offset:16384\n"
MF "v[96:99],a[212:215],a[100:103],v[96:99]\n"
"s_add_u32 s84,s14,0\n"
"s_add_u32 m0,s63,0\n"
TR "a[202:203],v1 offset:16896\n"
MF "v[104:107],a[212:215],a[104:107],v[104:107]\n"
LD "v4,s[24:27],s84 offen lds\n"
MF "v[104:107],a[208:211],a[64:67],v[104:107]\n"
TR "a[204:205],v1 offset:20480\n"
MF "v[112:115],a[208:211],a[68:71],v[112:115]\n"
TR "a[206:207],v1 offset:20992\n"
MF "v[112:115],a[212:215],a[108:111],v[112:115]\n"
MF "v[120:123],a[212:215],a[112:115],v[120:123]\n"
"s_add_u32 s84,s14,9216\n"
MF "v[120:123],a[208:211],a[72:75],v[120:123]\n"
LD "v5,s[24:27],s84 offen offset:1024 lds\n"
MF "v[128:131],a[208:211],a[76:79],v[128:131]\n"
MF "v[128:131],a[212:215],a[116:119],v[128:131]\n"
"s_waitcnt lgkmcnt(4)\n"
MF "v[132:135],a[220:223],a[116:119],v[132:135]\n"
TR "a[208:209],v1 offset:16392\n"
MF "v[132:135],a[216:219],a[76:79],v[132:135]\n"
"s_add_u32 s84,s14,18432\n"
TR "a[210:211],v1 offset:16904\n"
MF "v[124:127],a[216:219],a[72:75],v[124:127]\n"
LD "v4,s[24:27],s84 offen offset:2048 lds\n"
MF "v[124:127],a[220:223],a[112:115],v[124:127]\n"
TR "a[212:213],v1 offset:20488\n"
MF "v[116:119],a[220:223],a[108:111],v[116:119]\n"
TR "a[214:215],v1 offset:21000\n"
MF "v[116:119],a[216:219],a[68:71],v[116:119]\n"
MF "v[108:111],a[216:219],a[64:67],v[108:111]\n"
MF "v[108:111],a[220:223],a[104:107],v[108:111]\n"
MF "v[100:103],a[220:223],a[100:103],v[100:103]\n"
"s_waitcnt vmcnt(26) lgkmcnt(0)\n"
MF "v[100:103],a[216:219],a[60:63],v[100:103]\n"
"s_barrier\n"
MF "v[136:139],a[200:203],a[60:63],v[136:139]\n"
TR "a[216:217],v0 offset:32768\n"
MF "v[136:139],a[204:207],a[100:103],v[136:139]\n"
"s_add_u32 m0,s62,16384\n"
TR "a[218:219],v0 offset:33280\n"
MF "v[144:147],a[204:207],a[104:107],v[144:147]\n"
"s_add_u32 s82,s66,7078144\n"
"ds_read_b128 a[140:143],v2 offset:20480\n"
MF "v[144:147],a[200:203],a[64:67],v[144:147]\n"
"s_sub_u32 s15,s82,7864320\n"
TR "a[220:221],v0 offset:36864\n"
MF "v[152:155],a[200:203],a[68:71],v[152:155]\n"
"s_min_u32 s82,s82,s15\n"
TR "a[222:223],v0 offset:37376\n"
MF "v[152:155],a[204:207],a[108:111],v[152:155]\n"
LD "v6,s[28:31],s82 offen lds\n"
MF "v[160:163],a[204:207],a[112:115],v[160:163]\n"
"ds_read_b128 a[180:183],v3 offset:20480\n"
MF "v[160:163],a[200:203],a[72:75],v[160:163]\n"
MF "v[168:171],a[200:203],a[76:79],v[168:171]\n"
MF "v[168:171],a[204:207],a[116:119],v[168:171]\n"
LD "v11,s[28:31],s82 offen offset:1024 lds\n"
MF "v[172:175],a[212:215],a[116:119],v[172:175]\n"
TR "a[200:201],v0 offset:32776\n"
MF "v[172:175],a[208:211],a[76:79],v[172:175]\n"
TR "a[202:203],v0 offset:33288\n"
MF "v[164:167],a[208:211],a[72:75],v[164:167]\n"
"ds_read_b128 a[144:147],v2 offset:22528\n"
MF "v[164:167],a[212:215],a[112:115],v[164:167]\n"
TR "a[204:205],v0 offset:36872\n"
MF "v[156:159],a[212:215],a[108:111],v[156:159]\n"
TR "a[206:207],v0 offset:37384\n"
MF "v[156:159],a[208:211],a[68:71],v[156:159]\n"
LD "v12,s[28:31],s82 offen offset:2048 lds\n"
MF "v[148:151],a[208:211],a[64:67],v[148:151]\n"
"ds_read_b128 a[184:187],v3 offset:22528\n"
MF "v[148:151],a[212:215],a[104:107],v[148:151]\n"
MF "v[140:143],a[212:215],a[100:103],v[140:143]\n"
MF "v[140:143],a[208:211],a[60:63],v[140:143]\n"
"s_waitcnt lgkmcnt(7)\n"
LD "v13,s[28:31],s82 offen offset:3072 lds\n"
MF "v[176:179],a[216:219],a[60:63],v[176:179]\n"
TR "a[208:209],v1 offset:32768\n"
MF "v[176:179],a[220:223],a[100:103],v[176:179]\n"
TR "a[210:211],v1 offset:33280\n"
MF "v[184:187],a[220:223],a[104:107],v[184:187]\n"
"ds_read_b128 a[148:151],v2 offset:24576\n"
MF "v[184:187],a[216:219],a[64:67],v[184:187]\n"
"s_add_u32 s84,s14,30720\n"
TR "a[212:213],v1 offset:36864\n"
MF "v[192:195],a[216:219],a[68:71],v[192:195]\n"
"s_add_u32 m0,s63,3072\n"
TR "a[214:215],v1 offset:37376\n"
MF "v[192:195],a[220:223],a[108:111],v[192:195]\n"
LD "v5,s[24:27],s84 offen lds\n"
MF "v[200:203],a[220:223],a[112:115],v[200:203]\n"
"ds_read_b128 a[188:191],v3 offset:24576\n"
MF "v[200:203],a[216:219],a[72:75],v[200:203]\n"
MF "v[208:211],a[216:219],a[76:79],v[208:211]\n"
"s_add_u32 s84,s14,39936\n"
MF "v[208:211],a[220:223],a[116:119],v[208:211]\n"
"s_waitcnt lgkmcnt(7)\n"
LD "v4,s[24:27],s84 offen offset:1024 lds\n"
MF "v[212:215],a[204:207],a[116:119],v[212:215]\n"
TR "a[216:217],v1 offset:32776\n"
MF "v[212:215],a[200:203],a[76:79],v[212:215]\n"
TR "a[218:219],v1 offset:33288\n"
MF "v[204:207],a[200:203],a[72:75],v[204:207]\n"
"ds_read_b128 a[152:155],v2 offset:26624\n"
MF "v[204:207],a[204:207],a[112:115],v[204:207]\n"
TR "a[220:221],v1 offset:36872\n"
MF "v[196:199],a[204:207],a[108:111],v[196:199]\n"
TR "a[222:223],v1 offset:37384\n"
MF "v[196:199],a[200:203],a[68:71],v[196:199]\n"
MF "v[188:191],a[200:203],a[64:67],v[188:191]\n"
"v_add_u32 v0,0xc000,v0\n"
MF "v[188:191],a[204:207],a[104:107],v[188:191]\n"
"v_add_u32 v1,0xc000,v1\n"
MF "v[180:183],a[204:207],a[100:103],v[180:183]\n"
"s_waitcnt vmcnt(27) lgkmcnt(0)\n"
"s_branch .Lqj\n"
".Lpgq0_c:\n"
"s_cbranch_execz .Lpgqy_c\n"
".Lpgq0_f:\n"
"s_cbranch_execz .Lpg12f\n"
".Lq0:\n"
MF "v[16:19],a[200:203],a[60:63],0\n"
"s_add_u32 m0,s62,49152\n"
"s_add_u32 s82,s66,786432\n"
TR "a[216:217],v1\n"
MF "v[16:19],a[204:207],a[100:103],v[16:19]\n"
"s_sub_u32 s15,s82,7864320\n"
"s_min_u32 s82,s82,s15\n"
TR "a[218:219],v1 offset:512\n"
MF "v[24:27],a[204:207],a[104:107],0\n"
LD "v6,s[28:31],s82 offen lds\n"
MF "v[24:27],a[200:203],a[64:67],v[24:27]\n"
TR "a[220:221],v1 offset:4096\n"
MF "v[32:35],a[200:203],a[68:71],0\n"
TR "a[222:223],v1 offset:4608\n"
MF "v[32:35],a[204:207],a[108:111],v[32:35]\n"
"v_max3_f32 v220,|v16|,|v17|,|v18|\n"
"v_max_f32_e64 v220,v220,|v19|\n"
MF "v[40:43],a[204:207],a[112:115],0\n"
"v_readlane_b32 s12,v220,3\n"
"v_readlane_b32 s6,v220,22\n"
MF "v[40:43],a[200:203],a[72:75],v[40:43]\n"
"s_min_u32 s12,s12,s6\n"
"v_readlane_b32 s7,v220,41\n"
"v_readlane_b32 s8,v220,60\n"
MF "v[48:51],a[200:203],a[76:79],0\n"
"s_min_u32 s7,s7,s8\n"
MF "v[48:51],a[204:207],a[116:119],v[48:51]\n"
LD "v11,s[28:31],s82 offen offset:1024 lds\n"
MF "v[52:55],a[212:215],a[116:119],0\n"
"s_min_u32 s12,s12,s7\n"
TR "a[200:201],v1 offset:8\n"
MF "v[52:55],a[208:211],a[76:79],v[52:55]\n"
"s_and_b32 s12,s12,0x7f800000\n"
TR "a[202:203],v1 offset:520\n"
MF "v[44:47],a[208:211],a[72:75],0\n"
"s_sub_u32 s12,s12,0x14000000\n"
MF "v[44:47],a[212:215],a[112:115],v[44:47]\n"
"s_cmp_lt_u32 s12,0x50000000\n"
"s_cselect_b32 s12,s12,0xe8400000\n"
TR "a[204:205],v1 offset:4104\n"
MF "v[36:39],a[212:215],a[108:111],0\n"
"s_add_u32 s12,s12,0x17c00000\n"
TR "a[206:207],v1 offset:4616\n"
MF "v[36:39],a[208:211],a[68:71],v[36:39]\n"
"v_mov_b32 v220,s12\n"
"v_mov_b32 v221,s12\n"
MF "v[28:31],a[208:211],a[64:67],0\n"
"v_mov_b32 v222,s12\n"
"v_mov_b32 v223,s12\n"
MF "v[28:31],a[212:215],a[104:107],v[28:31]\n"
LD "v12,s[28:31],s82 offen offset:2048 lds\n"
MF "v[20:23],a[212:215],a[100:103],0\n"
"s_waitcnt vmcnt(7) lgkmcnt(0)\n"
MF "v[20:23],a[208:211],a[60:63],v[20:23]\n"
"s_barrier\n"
MF "v[56:59],a[216:219],a[60:63],v[220:223]\n"
TR "a[208:209],v0 offset:16384\n"
MF "v[56:59],a[220:223],a[100:103],v[56:59]\n"
TR "a[210:211],v0 offset:16896\n"
MF "v[64:67],a[220:223],a[104:107],v[220:223]\n"
LD "v13,s[28:31],s82 offen offset:3072 lds\n"
MF "v[64:67],a[216:219],a[64:67],v[64:67]\n"
"s_add_u32 m0,s62,65536\n"
TR "a[212:213],v0 offset:20480\n"
MF "v[72:75],a[216:219],a[68:71],v[220:223]\n"
"s_add_u32 s82,s66,786560\n"
TR "a[214:215],v0 offset:20992\n"
MF "v[72:75],a[220:223],a[108:111],v[72:75]\n"
"s_sub_u32 s15,s82,7864320\n"
MF "v[80:83],a[220:223],a[112:115],v[220:223]\n"
"s_min_u32 s82,s82,s15\n"
MF "v[80:83],a[216:219],a[72:75],v[80:83]\n"
LD "v6,s[28:31],s82 offen lds\n"
MF "v[88:91],a[216:219],a[76:79],v[220:223]\n"
MF "v[88:91],a[220:223],a[116:119],v[88:91]\n"
MF "v[92:95],a[204:207],a[116:119],v[220:223]\n"
TR "a[216:217],v0 offset:16392\n"
MF "v[92:95],a[200:203],a[76:79],v[92:95]\n"
TR "a[218:219],v0 offset:16904\n"
MF "v[84:87],a[200:203],a[72:75],v[220:223]\n"
LD "v11,s[28:31],s82 offen offset:1024 lds\n"
MF "v[84:87],a[204:207],a[112:115],v[84:87]\n"
TR "a[220:221],v0 offset:20488\n"
MF "v[76:79],a[204:207],a[108:111],v[220:223]\n"
TR "a[222:223],v0 offset:21000\n"
MF "v[76:79],a[200:203],a[68:71],v[76:79]\n"
MF "v[68:71],a[200:203],a[64:67],v[220:223]\n"
MF "v[68:71],a[204:207],a[104:107],v[68:71]\n"
LD "v12,s[28:31],s82 offen offset:2048 lds\n"
MF "v[60:63],a[204:207],a[100:103],v[220:223]\n"
MF "v[60:63],a[200:203],a[60:63],v[60:63]\n"
"s_waitcnt lgkmcnt(4)\n"
MF "v[96:99],a[208:211],a[60:63],v[220:223]\n"
TR "a[200:201],v1 offset:16384\n"
MF "v[96:99],a[212:215],a[100:103],v[96:99]\n"
TR "a[202:203],v1 offset:16896\n"
MF "v[104:107],a[212:215],a[104:107],v[220:223]\n"
LD "v13,s[28:31],s82 offen offset:3072 lds\n"
MF "v[104:107],a[208:211],a[64:67],v[104:107]\n"
"s_add_u32 m0,s62,81920\n"
TR "a[204:205],v1 offset:20480\n"
MF "v[112:115],a[208:211],a[68:71],v[220:223]\n"
"s_add_u32 s82,s66,786688\n"
TR "a[206:207],v1 offset:20992\n"
MF "v[112:115],a[212:215],a[108:111],v[112:115]\n"
"s_sub_u32 s15,s82,7864320\n"
MF "v[120:123],a[212:215],a[112:115],v[220:223]\n"
"s_min_u32 s82,s82,s15\n"
MF "v[120:123],a[208:211],a[72:75],v[120:123]\n"
LD "v6,s[28:31],s82 offen lds\n"
MF "v[128:131],a[208:211],a[76:79],v[220:223]\n"
MF "v[128:131],a[212:215],a[116:119],v[128:131]\n"
"s_waitcnt lgkmcnt(4)\n"
MF "v[132:135],a[220:223],a[116:119],v[220:223]\n"
TR "a[208:209],v1 offset:16392\n"
MF "v[132:135],a[216:219],a[76:79],v[132:135]\n"
TR "a[210:211],v1 offset:16904\n"
MF "v[124:127],a[216:219],a[72:75],v[220:223]\n"
LD "v11,s[28:31],s82 offen offset:1024 lds\n"
MF "v[124:127],a[220:223],a[112:115],v[124:127]\n"
TR "a[212:213],v1 offset:20488\n"
MF "v[116:119],a[220:223],a[108:111],v[220:223]\n"
TR "a[214:215],v1 offset:21000\n"
MF "v[116:119],a[216:219],a[68:71],v[116:119]\n"
MF "v[108:111],a[216:219],a[64:67],v[220:223]\n"
MF "v[108:111],a[220:223],a[104:107],v[108:111]\n"
LD "v12,s[28:31],s82 offen offset:2048 lds\n"
MF "v[100:103],a[220:223],a[100:103],v[220:223]\n"
"s_waitcnt vmcnt(11) lgkmcnt(0)\n"
MF "v[100:103],a[216:219],a[60:63],v[100:103]\n"
"s_barrier\n"
MF "v[136:139],a[200:203],a[60:63],v[220:223]\n"
TR "a[216:217],v0 offset:32768\n"
MF "v[136:139],a[204:207],a[100:103],v[136:139]\n"
TR "a[218:219],v0 offset:33280\n"
MF "v[144:147],a[204:207],a[104:107],v[220:223]\n"
"ds_read_b128 a[140:143],v2 offset:20480\n"
MF "v[144:147],a[200:203],a[64:67],v[144:147]\n"
TR "a[220:221],v0 offset:36864\n"
MF "v[152:155],a[200:203],a[68:71],v[220:223]\n"
TR "a[222:223],v0 offset:37376\n"
MF "v[152:155],a[204:207],a[108:111],v[152:155]\n"
LD "v13,s[28:31],s82 offen offset:3072 lds\n"
MF "v[160:163],a[204:207],a[112:115],v[220:223]\n"
"ds_read_b128 a[180:183],v3 offset:20480\n"
MF "v[160:163],a[200:203],a[72:75],v[160:163]\n"
"s_add_u32 m0,s62,0\n"
MF "v[168:171],a[200:203],a[76:79],v[220:223]\n"
"s_add_u32 s82,s66,1572864\n"
"s_sub_u32 s15,s82,7864320\n"
"s_min_u32 s82,s82,s15\n"
MF "v[168:171],a[204:207],a[116:119],v[168:171]\n"
LD "v6,s[28:31],s82 offen lds\n"
MF "v[172:175],a[212:215],a[116:119],v[220:223]\n"
TR "a[200:201],v0 offset:32776\n"
MF "v[172:175],a[208:211],a[76:79],v[172:175]\n"
TR "a[202:203],v0 offset:33288\n"
MF "v[164:167],a[208:211],a[72:75],v[220:223]\n"
"ds_read_b128 a[144:147],v2 offset:22528\n"
MF "v[164:167],a[212:215],a[112:115],v[164:167]\n"
TR "a[204:205],v0 offset:36872\n"
MF "v[156:159],a[212:215],a[108:111],v[220:223]\n"
TR "a[206:207],v0 offset:37384\n"
MF "v[156:159],a[208:211],a[68:71],v[156:159]\n"
LD "v11,s[28:31],s82 offen offset:1024 lds\n"
MF "v[148:151],a[208:211],a[64:67],v[220:223]\n"
"ds_read_b128 a[184:187],v3 offset:22528\n"
MF "v[148:151],a[212:215],a[104:107],v[148:151]\n"
MF "v[140:143],a[212:215],a[100:103],v[220:223]\n"
MF "v[140:143],a[208:211],a[60:63],v[140:143]\n"
"s_waitcnt lgkmcnt(7)\n"
LD "v12,s[28:31],s82 offen offset:2048 lds\n"
MF "v[176:179],a[216:219],a[60:63],v[220:223]\n"
TR "a[208:209],v1 offset:32768\n"
MF "v[176:179],a[220:223],a[100:103],v[176:179]\n"
TR "a[210:211],v1 offset:33280\n"
MF "v[184:187],a[220:223],a[104:107],v[220:223]\n"
".Lpg4c:\n"
"s_cbranch_execz .Lpg13c\n"
".Lpg4f:\n"
"s_cbranch_execz .LpgattB_f\n"
"ds_read_b128 a[148:151],v2 offset:24576\n"
MF "v[184:187],a[216:219],a[64:67],v[184:187]\n"
TR "a[212:213],v1 offset:36864\n"
MF "v[192:195],a[216:219],a[68:71],v[220:223]\n"
TR "a[214:215],v1 offset:37376\n"
MF "v[192:195],a[220:223],a[108:111],v[192:195]\n"
LD "v13,s[28:31],s82 offen offset:3072 lds\n"
MF "v[200:203],a[220:223],a[112:115],v[220:223]\n"
"ds_read_b128 a[188:191],v3 offset:24576\n"
MF "v[200:203],a[216:219],a[72:75],v[200:203]\n"
"s_add_u32 s14,s81,384\n"
"s_sub_u32 s15,s14,1280\n"
"s_min_u32 s14,s14,s15\n"
MF "v[208:211],a[216:219],a[76:79],v[220:223]\n"
"s_add_u32 s14,s14,s68\n"
"s_add_u32 s84,s14,0\n"
"s_add_u32 m0,s63,0\n"
MF "v[208:211],a[220:223],a[116:119],v[208:211]\n"
"s_waitcnt lgkmcnt(7)\n"
LD "v4,s[24:27],s84 offen lds\n"
MF "v[212:215],a[204:207],a[116:119],v[220:223]\n"
TR "a[216:217],v1 offset:32776\n"
MF "v[212:215],a[200:203],a[76:79],v[212:215]\n"
TR "a[218:219],v1 offset:33288\n"
MF "v[204:207],a[200:203],a[72:75],v[220:223]\n"
"ds_read_b128 a[152:155],v2 offset:26624\n"
MF "v[204:207],a[204:207],a[112:115],v[204:207]\n"
TR "a[220:221],v1 offset:36872\n"
MF "v[196:199],a[204:207],a[108:111],v[220:223]\n"
"s_add_u32 s84,s14,9216\n"
TR "a[222:223],v1 offset:37384\n"
MF "v[196:199],a[200:203],a[68:71],v[196:199]\n"
LD "v5,s[24:27],s84 offen offset:1024 lds\n"
MF "v[188:191],a[200:203],a[64:67],v[220:223]\n"
"s_add_u32 m0,s62,16384\n"
"s_add_u32 s82,s66,1572992\n"
"v_add_u32 v0,0xc000,v0\n"
MF "v[188:191],a[204:207],a[104:107],v[188:191]\n"
"s_sub_u32 s15,s82,7864320\n"
"s_min_u32 s82,s82,s15\n"
"v_add_u32 v1,0xc000,v1\n"
MF "v[180:183],a[204:207],a[100:103],v[220:223]\n"
"s_waitcnt vmcnt(14) lgkmcnt(0)\n"
LD "v6,s[28:31],s82 offen lds\n"
MF "v[180:183],a[200:203],a[60:63],v[180:183]\n"
"s_barrier\n"
MF "v[216:219],a[208:211],a[60:63],v[220:223]\n"
TR "a[200:201],v0\n"
MF "v[216:219],a[212:215],a[100:103],v[216:219]\n"
TR "a[202:203],v0 offset:512\n"
MF "v[224:227],a[212:215],a[104:107],v[220:223]\n"
"ds_read_b128 a[192:195],v3 offset:26624\n"
MF "v[224:227],a[208:211],a[64:67],v[224:227]\n"
TR "a[204:205],v0 offset:4096\n"
MF "v[232:235],a[208:211],a[68:71],v[220:223]\n"
TR "a[206:207],v0 offset:4608\n"
MF "v[232:235],a[212:215],a[108:111],v[232:235]\n"
LD "v11,s[28:31],s82 offen offset:1024 lds\n"
MF "v[240:243],a[212:215],a[112:115],v[220:223]\n"
"ds_read_b128 a[156:159],v2 offset:28672\n"
MF "v[240:243],a[208:211],a[72:75],v[240:243]\n"
"ds_read_b128 a[196:199],v3 offset:28672\n"
MF "v[248:251],a[208:211],a[76:79],v[220:223]\n"
MF "v[248:251],a[212:215],a[116:119],v[248:251]\n"
LD "v12,s[28:31],s82 offen offset:2048 lds\n"
MF "v[252:255],a[220:223],a[116:119],v[220:223]\n"
TR "a[208:209],v0 offset:8\n"
MF "v[252:255],a[216:219],a[76:79],v[252:255]\n"
TR "a[210:211],v0 offset:520\n"
MF "v[244:247],a[216:219],a[72:75],v[220:223]\n"
MF "v[244:247],a[220:223],a[112:115],v[244:247]\n"
TR "a[212:213],v0 offset:4104\n"
MF "v[236:239],a[220:223],a[108:111],v[220:223]\n"
TR "a[214:215],v0 offset:4616\n"
MF "v[236:239],a[216:219],a[68:71],v[236:239]\n"
LD "v13,s[28:31],s82 offen offset:3072 lds\n"
MF "v[228:231],a[216:219],a[64:67],v[220:223]\n"
MF "v[228:231],a[220:223],a[104:107],v[228:231]\n"
"s_add_u32 s84,s14,20480\n"
MF "v[220:223],a[220:223],a[100:103],v[220:223]\n"
"s_add_u32 m0,s63,2048\n"
MF "v[220:223],a[216:219],a[60:63],v[220:223]\n"
"s_waitcnt lgkmcnt(4)\n"
LD "v4,s[24:27],s84 offen lds\n"
MF "v[16:19],a[200:203],a[140:143],v[16:19]\n"
TR "a[216:217],v1\n"
MF "v[16:19],a[204:207],a[180:183],v[16:19]\n"
TR "a[218:219],v1 offset:512\n"
MF "v[24:27],a[204:207],a[184:187],v[24:27]\n"
MF "v[24:27],a[200:203],a[144:147],v[24:27]\n"
TR "a[220:221],v1 offset:4096\n"
MF "v[32:35],a[200:203],a[148:151],v[32:35]\n"
"s_add_u32 s84,s14,29696\n"
TR "a[222:223],v1 offset:4608\n"
MF "v[32:35],a[204:207],a[188:191],v[32:35]\n"
LD "v5,s[24:27],s84 offen offset:1024 lds\n"
MF "v[40:43],a[204:207],a[192:195],v[40:43]\n"
MF "v[40:43],a[200:203],a[152:155],v[40:43]\n"
"s_add_u32 m0,s62,32768\n"
MF "v[48:51],a[200:203],a[156:159],v[48:51]\n"
"s_add_u32 s82,s66,1573120\n"
"s_sub_u32 s15,s82,7864320\n"
"s_min_u32 s82,s82,s15\n"
MF "v[48:51],a[204:207],a[196:199],v[48:51]\n"
"s_waitcnt lgkmcnt(4)\n"
LD "v6,s[28:31],s82 offen lds\n"
MF "v[52:55],a[212:215],a[196:199],v[52:55]\n"
TR "a[200:201],v1 offset:8\n"
MF "v[52:55],a[208:211],a[156:159],v[52:55]\n"
TR "a[202:203],v1 offset:520\n"
MF "v[44:47],a[208:211],a[152:155],v[44:47]\n"
MF "v[44:47],a[212:215],a[192:195],v[44:47]\n"
TR "a[204:205],v1 offset:4104\n"
MF "v[36:39],a[212:215],a[188:191],v[36:39]\n"
TR "a[206:207],v1 offset:4616\n"
MF "v[36:39],a[208:211],a[148:151],v[36:39]\n"
LD "v11,s[28:31],s82 offen offset:1024 lds\n"
MF "v[28:31],a[208:211],a[144:147],v[28:31]\n"
MF "v[28:31],a[212:215],a[184:187],v[28:31]\n"
MF "v[20:23],a[212:215],a[180:183],v[20:23]\n"
"s_waitcnt vmcnt(18) lgkmcnt(0)\n"
LD "v12,s[28:31],s82 offen offset:2048 lds\n"
"s_bitcmp0_b32 s23,12\n"
"s_cbranch_scc1 .Lisx2\n"
"s_mov_b64 exec,0\n"
"s_cmp_eq_u32 s56,0\n"
"s_cbranch_scc1 .Lpg11d\n"
"s_cmp_eq_u32 s56,1\n"
"s_cbranch_scc1 .Lpg10d\n"
"s_cmp_eq_u32 s56,2\n"
"s_cbranch_scc1 .Lpgt0kx_d\n"
"s_branch .Lpg8d\n"
".Lisr2:\n"
"s_mov_b64 exec,-1\n"
".Lisx2:\n"
MF "v[20:23],a[208:211],a[140:143],v[20:23]\n"
"s_barrier\n"
MF "v[56:59],a[216:219],a[140:143],v[56:59]\n"
TR "a[208:209],v0 offset:16384\n"
MF "v[56:59],a[220:223],a[180:183],v[56:59]\n"
TR "a[210:211],v0 offset:16896\n"
MF "v[64:67],a[220:223],a[184:187],v[64:67]\n"
LD "v13,s[28:31],s82 offen offset:3072 lds\n"
MF "v[64:67],a[216:219],a[144:147],v[64:67]\n"
TR "a[212:213],v0 offset:20480\n"
MF "v[72:75],a[216:219],a[148:151],v[72:75]\n"
TR "a[214:215],v0 offset:20992\n"
MF "v[72:75],a[220:223],a[188:191],v[72:75]\n"
"s_add_u32 s84,s14,40960\n"
MF "v[80:83],a[220:223],a[192:195],v[80:83]\n"
"s_add_u32 m0,s63,4096\n"
MF "v[80:83],a[216:219],a[152:155],v[80:83]\n"
LD "v4,s[24:27],s84 offen lds\n"
MF "v[88:91],a[216:219],a[156:159],v[88:91]\n"
MF "v[88:91],a[220:223],a[196:199],v[88:91]\n"
"s_sub_u32 s66,s66,s83\n"
MF "v[92:95],a[204:207],a[196:199],v[92:95]\n"
"s_add_u32 m0,s62,49152\n"
"s_add_u32 s82,s66,2359296\n"
TR "a[216:217],v0 offset:16392\n"
MF "v[92:95],a[200:203],a[156:159],v[92:95]\n"
"s_sub_u32 s15,s82,7864320\n"
"s_min_u32 s82,s82,s15\n"
TR "a[218:219],v0 offset:16904\n"
MF "v[84:87],a[200:203],a[152:155],v[84:87]\n"
LD "v6,s[28:31],s82 offen lds\n"
MF "v[84:87],a[204:207],a[192:195],v[84:87]\n"
TR "a[220:221],v0 offset:20488\n"
MF "v[76:79],a[204:207],a[188:191],v[76:79]\n"
TR "a[222:223],v0 offset:21000\n"
MF "v[76:79],a[200:203],a[148:151],v[76:79]\n"
MF "v[68:71],a[200:203],a[144:147],v[68:71]\n"
MF "v[68:71],a[204:207],a[184:187],v[68:71]\n"
LD "v11,s[28:31],s82 offen offset:1024 lds\n"
MF "v[60:63],a[204:207],a[180:183],v[60:63]\n"
MF "v[60:63],a[200:203],a[140:143],v[60:63]\n"
"s_waitcnt lgkmcnt(4)\n"
MF "v[96:99],a[208:211],a[140:143],v[96:99]\n"
TR "a[200:201],v1 offset:16384\n"
MF "v[96:99],a[212:215],a[180:183],v[96:99]\n"
TR "a[202:203],v1 offset:16896\n"
MF "v[104:107],a[212:215],a[184:187],v[104:107]\n"
LD "v12,s[28:31],s82 offen offset:2048 lds\n"
MF "v[104:107],a[208:211],a[144:147],v[104:107]\n"
TR "a[204:205],v1 offset:20480\n"
MF "v[112:115],a[208:211],a[148:151],v[112:115]\n"
TR "a[206:207],v1 offset:20992\n"
MF "v[112:115],a[212:215],a[188:191],v[112:115]\n"
MF "v[120:123],a[212:215],a[192:195],v[120:123]\n"
MF "v[120:123],a[208:211],a[152:155],v[120:123]\n"
LD "v13,s[28:31],s82 offen offset:3072 lds\n"
MF "v[128:131],a[208:211],a[156:159],v[128:131]\n"
MF "v[128:131],a[212:215],a[196:199],v[128:131]\n"
"s_add_u32 s14,s81,512\n"
"s_sub_u32 s15,s14,1280\n"
"s_waitcnt lgkmcnt(4)\n"
MF "v[132:135],a[220:223],a[196:199],v[132:135]\n"
"s_min_u32 s14,s14,s15\n"
"s_add_u32 s14,s14,s68\n"
TR "a[208:209],v1 offset:16392\n"
MF "v[132:135],a[216:219],a[156:159],v[132:135]\n"
"s_add_u32 s84,s14,0\n"
"s_add_u32 m0,s63,20480\n"
TR "a[210:211],v1 offset:16904\n"
MF "v[124:127],a[216:219],a[152:155],v[124:127]\n"
LD "v4,s[24:27],s84 offen lds\n"
MF "v[124:127],a[220:223],a[192:195],v[124:127]\n"
TR "a[212:213],v1 offset:20488\n"
MF "v[116:119],a[220:223],a[188:191],v[116:119]\n"
TR "a[214:215],v1 offset:21000\n"
MF "v[116:119],a[216:219],a[148:151],v[116:119]\n"
MF "v[108:111],a[216:219],a[144:147],v[108:111]\n"
"s_add_u32 s84,s14,9216\n"
MF "v[108:111],a[220:223],a[184:187],v[108:111]\n"
LD "v5,s[24:27],s84 offen offset:1024 lds\n"
MF "v[100:103],a[220:223],a[180:183],v[100:103]\n"
"s_waitcnt vmcnt(23) lgkmcnt(0)\n"
MF "v[100:103],a[216:219],a[140:143],v[100:103]\n"
"s_barrier\n"
MF "v[136:139],a[200:203],a[140:143],v[136:139]\n"
TR "a[216:217],v0 offset:32768\n"
MF "v[136:139],a[204:207],a[180:183],v[136:139]\n"
"s_add_u32 m0,s62,65536\n"
TR "a[218:219],v0 offset:33280\n"
MF "v[144:147],a[204:207],a[184:187],v[144:147]\n"
"s_add_u32 s82,s66,2359424\n"
"ds_read_b128 a[60:63],v2 offset:40960\n"
MF "v[144:147],a[200:203],a[144:147],v[144:147]\n"
"s_sub_u32 s15,s82,7864320\n"
TR "a[220:221],v0 offset:36864\n"
MF "v[152:155],a[200:203],a[148:151],v[152:155]\n"
"s_min_u32 s82,s82,s15\n"
TR "a[222:223],v0 offset:37376\n"
MF "v[152:155],a[204:207],a[188:191],v[152:155]\n"
LD "v6,s[28:31],s82 offen lds\n"
MF "v[160:163],a[204:207],a[192:195],v[160:163]\n"
"ds_read_b128 a[100:103],v3 offset:40960\n"
MF "v[160:163],a[200:203],a[152:155],v[160:163]\n"
LD "v11,s[28:31],s82 offen offset:1024 lds\n"
MF "v[168:171],a[200:203],a[156:159],v[168:171]\n"
MF "v[168:171],a[204:207],a[196:199],v[168:171]\n"
LD "v12,s[28:31],s82 offen offset:2048 lds\n"
MF "v[172:175],a[212:215],a[196:199],v[172:175]\n"
TR "a[200:201],v0 offset:32776\n"
MF "v[172:175],a[208:211],a[156:159],v[172:175]\n"
TR "a[202:203],v0 offset:33288\n"
MF "v[164:167],a[208:211],a[152:155],v[164:167]\n"
"ds_read_b128 a[64:67],v2 offset:43008\n"
MF "v[164:167],a[212:215],a[192:195],v[164:167]\n"
TR "a[204:205],v0 offset:36872\n"
MF "v[156:159],a[212:215],a[188:191],v[156:159]\n"
TR "a[206:207],v0 offset:37384\n"
MF "v[156:159],a[208:211],a[148:151],v[156:159]\n"
MF "v[148:151],a[208:211],a[144:147],v[148:151]\n"
"ds_read_b128 a[104:107],v3 offset:43008\n"
MF "v[148:151],a[212:215],a[184:187],v[148:151]\n"
MF "v[140:143],a[212:215],a[180:183],v[140:143]\n"
LD "v13,s[28:31],s82 offen offset:3072 lds\n"
MF "v[140:143],a[208:211],a[140:143],v[140:143]\n"
"s_waitcnt lgkmcnt(7)\n"
MF "v[176:179],a[216:219],a[140:143],v[176:179]\n"
TR "a[208:209],v1 offset:32768\n"
MF "v[176:179],a[220:223],a[180:183],v[176:179]\n"
TR "a[210:211],v1 offset:33280\n"
MF "v[184:187],a[220:223],a[184:187],v[184:187]\n"
"ds_read_b128 a[68:71],v2 offset:45056\n"
MF "v[184:187],a[216:219],a[144:147],v[184:187]\n"
"s_add_u32 s84,s14,20480\n"
TR "a[212:213],v1 offset:36864\n"
MF "v[192:195],a[216:219],a[148:151],v[192:195]\n"
"s_add_u32 m0,s63,22528\n"
TR "a[214:215],v1 offset:37376\n"
MF "v[192:195],a[220:223],a[188:191],v[192:195]\n"
LD "v4,s[24:27],s84 offen lds\n"
MF "v[200:203],a[220:223],a[192:195],v[200:203]\n"
"ds_read_b128 a[108:111],v3 offset:45056\n"
MF "v[200:203],a[216:219],a[152:155],v[200:203]\n"
MF "v[208:211],a[216:219],a[156:159],v[208:211]\n"
"s_add_u32 s84,s14,29696\n"
MF "v[208:211],a[220:223],a[196:199],v[208:211]\n"
"s_waitcnt lgkmcnt(7)\n"
LD "v5,s[24:27],s84 offen offset:1024 lds\n"
MF "v[212:215],a[204:207],a[196:199],v[212:215]\n"
TR "a[216:217],v1 offset:32776\n"
MF "v[212:215],a[200:203],a[156:159],v[212:215]\n"
TR "a[218:219],v1 offset:33288\n"
MF "v[204:207],a[200:203],a[152:155],v[204:207]\n"
"ds_read_b128 a[72:75],v2 offset:47104\n"
MF "v[204:207],a[204:207],a[192:195],v[204:207]\n"
TR "a[220:221],v1 offset:36872\n"
MF "v[196:199],a[204:207],a[188:191],v[196:199]\n"
TR "a[222:223],v1 offset:37384\n"
MF "v[196:199],a[200:203],a[148:151],v[196:199]\n"
MF "v[188:191],a[200:203],a[144:147],v[188:191]\n"
"v_add_u32 v0,0xffff4000,v0\n"
MF "v[188:191],a[204:207],a[184:187],v[188:191]\n"
"v_add_u32 v1,0xffff4000,v1\n"
MF "v[180:183],a[204:207],a[180:183],v[180:183]\n"
"s_waitcnt vmcnt(25) lgkmcnt(0)\n"
MF "v[180:183],a[200:203],a[140:143],v[180:183]\n"
"s_barrier\n"
MF "v[216:219],a[208:211],a[140:143],v[216:219]\n"
TR "a[200:201],v0\n"
MF "v[216:219],a[212:215],a[180:183],v[216:219]\n"
"s_add_u32 m0,s62,81920\n"
TR "a[202:203],v0 offset:512\n"
MF "v[224:227],a[212:215],a[184:187],v[224:227]\n"
"s_add_u32 s82,s66,2359552\n"
"ds_read_b128 a[112:115],v3 offset:47104\n"
MF "v[224:227],a[208:211],a[144:147],v[224:227]\n"
"s_sub_u32 s15,s82,7864320\n"
TR "a[204:205],v0 offset:4096\n"
MF "v[232:235],a[208:211],a[148:151],v[232:235]\n"
"s_min_u32 s82,s82,s15\n"
TR "a[206:207],v0 offset:4608\n"
MF "v[232:235],a[212:215],a[188:191],v[232:235]\n"
LD "v6,s[28:31],s82 offen lds\n"
MF "v[240:243],a[212:215],a[192:195],v[240:243]\n"
"ds_read_b128 a[76:79],v2 offset:49152\n"
MF "v[240:243],a[208:211],a[152:155],v[240:243]\n"
"ds_read_b128 a[116:119],v3 offset:49152\n"
MF "v[248:251],a[208:211],a[156:159],v[248:251]\n"
LD "v11,s[28:31],s82 offen offset:1024 lds\n"
MF "v[248:251],a[212:215],a[196:199],v[248:251]\n"
MF "v[252:255],a[220:223],a[196:199],v[252:255]\n"
TR "a[208:209],v0 offset:8\n"
MF "v[252:255],a[216:219],a[156:159],v[252:255]\n"
TR "a[210:211],v0 offset:520\n"
MF "v[244:247],a[216:219],a[152:155],v[244:247]\n"
MF "v[244:247],a[220:223],a[192:195],v[244:247]\n"
TR "a[212:213],v0 offset:4104\n"
MF "v[236:239],a[220:223],a[188:191],v[236:239]\n"
TR "a[214:215],v0 offset:4616\n"
MF "v[236:239],a[216:219],a[148:151],v[236:239]\n"
LD "v12,s[28:31],s82 offen offset:2048 lds\n"
MF "v[228:231],a[216:219],a[144:147],v[228:231]\n"
MF "v[228:231],a[220:223],a[184:187],v[228:231]\n"
MF "v[220:223],a[220:223],a[180:183],v[220:223]\n"
MF "v[220:223],a[216:219],a[140:143],v[220:223]\n"
"s_waitcnt lgkmcnt(4)\n"
MF "v[16:19],a[200:203],a[60:63],v[16:19]\n"
TR "a[216:217],v1\n"
MF "v[16:19],a[204:207],a[100:103],v[16:19]\n"
TR "a[218:219],v1 offset:512\n"
MF "v[24:27],a[204:207],a[104:107],v[24:27]\n"
LD "v13,s[28:31],s82 offen offset:3072 lds\n"
MF "v[24:27],a[200:203],a[64:67],v[24:27]\n"
TR "a[220:221],v1 offset:4096\n"
MF "v[32:35],a[200:203],a[68:71],v[32:35]\n"
TR "a[222:223],v1 offset:4608\n"
MF "v[32:35],a[204:207],a[108:111],v[32:35]\n"
MF "v[40:43],a[204:207],a[112:115],v[40:43]\n"
MF "v[40:43],a[200:203],a[72:75],v[40:43]\n"
"s_add_u32 s84,s14,40960\n"
MF "v[48:51],a[200:203],a[76:79],v[48:51]\n"
"s_add_u32 m0,s63,24576\n"
MF "v[48:51],a[204:207],a[116:119],v[48:51]\n"
"s_waitcnt lgkmcnt(4)\n"
LD "v4,s[24:27],s84 offen lds\n"
MF "v[52:55],a[212:215],a[116:119],v[52:55]\n"
TR "a[200:201],v1 offset:8\n"
MF "v[52:55],a[208:211],a[76:79],v[52:55]\n"
TR "a[202:203],v1 offset:520\n"
MF "v[44:47],a[208:211],a[72:75],v[44:47]\n"
MF "v[44:47],a[212:215],a[112:115],v[44:47]\n"
TR "a[204:205],v1 offset:4104\n"
MF "v[36:39],a[212:215],a[108:111],v[36:39]\n"
TR "a[206:207],v1 offset:4616\n"
MF "v[36:39],a[208:211],a[68:71],v[36:39]\n"
MF "v[28:31],a[208:211],a[64:67],v[28:31]\n"
MF "v[28:31],a[212:215],a[104:107],v[28:31]\n"
MF "v[20:23],a[212:215],a[100:103],v[20:23]\n"
"s_waitcnt vmcnt(24) lgkmcnt(0)\n"
MF "v[20:23],a[208:211],a[60:63],v[20:23]\n"
"s_barrier\n"
MF "v[56:59],a[216:219],a[60:63],v[56:59]\n"
"s_add_u32 m0,s62,0\n"
"s_add_u32 s82,s66,3145728\n"
TR "a[208:209],v0 offset:16384\n"
MF "v[56:59],a[220:223],a[100:103],v[56:59]\n"
"s_sub_u32 s15,s82,7864320\n"
"s_min_u32 s82,s82,s15\n"
TR "a[210:211],v0 offset:16896\n"
MF "v[64:67],a[220:223],a[104:107],v[64:67]\n"
LD "v6,s[28:31],s82 offen lds\n"
MF "v[64:67],a[216:219],a[64:67],v[64:67]\n"
TR "a[212:213],v0 offset:20480\n"
MF "v[72:75],a[216:219],a[68:71],v[72:75]\n"
TR "a[214:215],v0 offset:20992\n"
MF "v[72:75],a[220:223],a[108:111],v[72:75]\n"
MF "v[80:83],a[220:223],a[112:115],v[80:83]\n"
LD "v11,s[28:31],s82 offen offset:1024 lds\n"
MF "v[80:83],a[216:219],a[72:75],v[80:83]\n"
MF "v[88:91],a[216:219],a[76:79],v[88:91]\n"
MF "v[88:91],a[220:223],a[116:119],v[88:91]\n"
MF "v[92:95],a[204:207],a[116:119],v[92:95]\n"
TR "a[216:217],v0 offset:16392\n"
MF "v[92:95],a[200:203],a[76:79],v[92:95]\n"
TR "a[218:219],v0 offset:16904\n"
MF "v[84:87],a[200:203],a[72:75],v[84:87]\n"
LD "v12,s[28:31],s82 offen offset:2048 lds\n"
MF "v[84:87],a[204:207],a[112:115],v[84:87]\n"
TR "a[220:221],v0 offset:20488\n"
MF "v[76:79],a[204:207],a[108:111],v[76:79]\n"
TR "a[222:223],v0 offset:21000\n"
MF "v[76:79],a[200:203],a[68:71],v[76:79]\n"
MF "v[68:71],a[200:203],a[64:67],v[68:71]\n"
MF "v[68:71],a[204:207],a[104:107],v[68:71]\n"
MF "v[60:63],a[204:207],a[100:103],v[60:63]\n"
LD "v13,s[28:31],s82 offen offset:3072 lds\n"
MF "v[60:63],a[200:203],a[60:63],v[60:63]\n"
"s_waitcnt lgkmcnt(4)\n"
MF "v[96:99],a[208:211],a[60:63],v[96:99]\n"
TR "a[200:201],v1 offset:16384\n"
MF "v[96:99],a[212:215],a[100:103],v[96:99]\n"
TR "a[202:203],v1 offset:16896\n"
MF "v[104:107],a[212:215],a[104:107],v[104:107]\n"
"s_add_u32 s14,s81,640\n"
"s_sub_u32 s15,s14,1280\n"
MF "v[104:107],a[208:211],a[64:67],v[104:107]\n"
"s_min_u32 s14,s14,s15\n"
"s_add_u32 s14,s14,s68\n"
TR "a[204:205],v1 offset:20480\n"
MF "v[112:115],a[208:211],a[68:71],v[112:115]\n"
"s_add_u32 s84,s14,0\n"
"s_add_u32 m0,s63,40960\n"
TR "a[206:207],v1 offset:20992\n"
MF "v[112:115],a[212:215],a[108:111],v[112:115]\n"
LD "v4,s[24:27],s84 offen lds\n"
MF "v[120:123],a[212:215],a[112:115],v[120:123]\n"
MF "v[120:123],a[208:211],a[72:75],v[120:123]\n"
MF "v[128:131],a[208:211],a[76:79],v[128:131]\n"
"s_add_u32 s84,s14,9216\n"
MF "v[128:131],a[212:215],a[116:119],v[128:131]\n"
"s_waitcnt lgkmcnt(4)\n"
LD "v5,s[24:27],s84 offen offset:1024 lds\n"
MF "v[132:135],a[220:223],a[116:119],v[132:135]\n"
TR "a[208:209],v1 offset:16392\n"
MF "v[132:135],a[216:219],a[76:79],v[132:135]\n"
TR "a[210:211],v1 offset:16904\n"
MF "v[124:127],a[216:219],a[72:75],v[124:127]\n"
MF "v[124:127],a[220:223],a[112:115],v[124:127]\n"
TR "a[212:213],v1 offset:20488\n"
MF "v[116:119],a[220:223],a[108:111],v[116:119]\n"
TR "a[214:215],v1 offset:21000\n"
MF "v[116:119],a[216:219],a[68:71],v[116:119]\n"
MF "v[108:111],a[216:219],a[64:67],v[108:111]\n"
MF "v[108:111],a[220:223],a[104:107],v[108:111]\n"
MF "v[100:103],a[220:223],a[100:103],v[100:103]\n"
"s_waitcnt vmcnt(23) lgkmcnt(0)\n"
MF "v[100:103],a[216:219],a[60:63],v[100:103]\n"
"s_barrier\n"
MF "v[136:139],a[200:203],a[60:63],v[136:139]\n"
TR "a[216:217],v0 offset:32768\n"
MF "v[136:139],a[204:207],a[100:103],v[136:139]\n"
"s_add_u32 m0,s62,16384\n"
TR "a[218:219],v0 offset:33280\n"
MF "v[144:147],a[204:207],a[104:107],v[144:147]\n"
"s_add_u32 s82,s66,3145856\n"
"ds_read_b128 a[140:143],v2\n"
MF "v[144:147],a[200:203],a[64:67],v[144:147]\n"
"s_sub_u32 s15,s82,7864320\n"
TR "a[220:221],v0 offset:36864\n"
MF "v[152:155],a[200:203],a[68:71],v[152:155]\n"
"s_min_u32 s82,s82,s15\n"
TR "a[222:223],v0 offset:37376\n"
MF "v[152:155],a[204:207],a[108:111],v[152:155]\n"
LD "v6,s[28:31],s82 offen lds\n"
MF "v[160:163],a[204:207],a[112:115],v[160:163]\n"
"ds_read_b128 a[180:183],v3\n"
MF "v[160:163],a[200:203],a[72:75],v[160:163]\n"
LD "v11,s[28:31],s82 offen offset:1024 lds\n"
MF "v[168:171],a[200:203],a[76:79],v[168:171]\n"
MF "v[168:171],a[204:207],a[116:119],v[168:171]\n"
LD "v12,s[28:31],s82 offen offset:2048 lds\n"
MF "v[172:175],a[212:215],a[116:119],v[172:175]\n"
TR "a[200:201],v0 offset:32776\n"
MF "v[172:175],a[208:211],a[76:79],v[172:175]\n"
TR "a[202:203],v0 offset:33288\n"
MF "v[164:167],a[208:211],a[72:75],v[164:167]\n"
"ds_read_b128 a[144:147],v2 offset:2048\n"
MF "v[164:167],a[212:215],a[112:115],v[164:167]\n"
TR "a[204:205],v0 offset:36872\n"
MF "v[156:159],a[212:215],a[108:111],v[156:159]\n"
TR "a[206:207],v0 offset:37384\n"
MF "v[156:159],a[208:211],a[68:71],v[156:159]\n"
MF "v[148:151],a[208:211],a[64:67],v[148:151]\n"
"ds_read_b128 a[184:187],v3 offset:2048\n"
MF "v[148:151],a[212:215],a[104:107],v[148:151]\n"
MF "v[140:143],a[212:215],a[100:103],v[140:143]\n"
LD "v13,s[28:31],s82 offen offset:3072 lds\n"
MF "v[140:143],a[208:211],a[60:63],v[140:143]\n"
"s_waitcnt lgkmcnt(7)\n"
MF "v[176:179],a[216:219],a[60:63],v[176:179]\n"
TR "a[208:209],v1 offset:32768\n"
MF "v[176:179],a[220:223],a[100:103],v[176:179]\n"
TR "a[210:211],v1 offset:33280\n"
MF "v[184:187],a[220:223],a[104:107],v[184:187]\n"
"ds_read_b128 a[148:151],v2 offset:4096\n"
MF "v[184:187],a[216:219],a[64:67],v[184:187]\n"
"s_add_u32 s84,s14,20480\n"
TR "a[212:213],v1 offset:36864\n"
MF "v[192:195],a[216:219],a[68:71],v[192:195]\n"
"s_add_u32 m0,s63,43008\n"
TR "a[214:215],v1 offset:37376\n"
MF "v[192:195],a[220:223],a[108:111],v[192:195]\n"
LD "v4,s[24:27],s84 offen lds\n"
MF "v[200:203],a[220:223],a[112:115],v[200:203]\n"
"ds_read_b128 a[188:191],v3 offset:4096\n"
MF "v[200:203],a[216:219],a[72:75],v[200:203]\n"
MF "v[208:211],a[216:219],a[76:79],v[208:211]\n"
"s_add_u32 s84,s14,29696\n"
MF "v[208:211],a[220:223],a[116:119],v[208:211]\n"
"s_waitcnt lgkmcnt(7)\n"
LD "v5,s[24:27],s84 offen offset:1024 lds\n"
MF "v[212:215],a[204:207],a[116:119],v[212:215]\n"
TR "a[216:217],v1 offset:32776\n"
MF "v[212:215],a[200:203],a[76:79],v[212:215]\n"
TR "a[218:219],v1 offset:33288\n"
MF "v[204:207],a[200:203],a[72:75],v[204:207]\n"
"ds_read_b128 a[152:155],v2 offset:6144\n"
MF "v[204:207],a[204:207],a[112:115],v[204:207]\n"
TR "a[220:221],v1 offset:36872\n"
MF "v[196:199],a[204:207],a[108:111],v[196:199]\n"
TR "a[222:223],v1 offset:37384\n"
MF "v[196:199],a[200:203],a[68:71],v[196:199]\n"
MF "v[188:191],a[200:203],a[64:67],v[188:191]\n"
"v_add_u32 v0,0xc000,v0\n"
MF "v[188:191],a[204:207],a[104:107],v[188:191]\n"
"v_add_u32 v1,0xc000,v1\n"
MF "v[180:183],a[204:207],a[100:103],v[180:183]\n"
"s_waitcnt vmcnt(25) lgkmcnt(0)\n"
MF "v[180:183],a[200:203],a[60:63],v[180:183]\n"
"s_barrier\n"
MF "v[216:219],a[208:211],a[60:63],v[216:219]\n"
TR "a[200:201],v0\n"
MF "v[216:219],a[212:215],a[100:103],v[216:219]\n"
"s_add_u32 m0,s62,32768\n"
TR "a[202:203],v0 offset:512\n"
MF "v[224:227],a[212:215],a[104:107],v[224:227]\n"
"s_add_u32 s82,s66,3145984\n"
"ds_read_b128 a[192:195],v3 offset:6144\n"
MF "v[224:227],a[208:211],a[64:67],v[224:227]\n"
".Lpg5c:\n"
"s_cbranch_execz .Lpg14c\n"
".Lpg5f:\n"
"s_cbranch_execz .Lpgt0k_f\n"
"s_sub_u32 s15,s82,7864320\n"
TR "a[204:205],v0 offset:4096\n"
MF "v[232:235],a[208:211],a[68:71],v[232:235]\n"
"s_min_u32 s82,s82,s15\n"
TR "a[206:207],v0 offset:4608\n"
MF "v[232:235],a[212:215],a[108:111],v[232:235]\n"
LD "v6,s[28:31],s82 offen lds\n"
MF "v[240:243],a[212:215],a[112:115],v[240:243]\n"
"ds_read_b128 a[156:159],v2 offset:8192\n"
MF "v[240:243],a[208:211],a[72:75],v[240:243]\n"
"ds_read_b128 a[196:199],v3 offset:8192\n"
MF "v[248:251],a[208:211],a[76:79],v[248:251]\n"
LD "v11,s[28:31],s82 offen offset:1024 lds\n"
MF "v[248:251],a[212:215],a[116:119],v[248:251]\n"
MF "v[252:255],a[220:223],a[116:119],v[252:255]\n"
TR "a[208:209],v0 offset:8\n"
MF "v[252:255],a[216:219],a[76:79],v[252:255]\n"
TR "a[210:211],v0 offset:520\n"
MF "v[244:247],a[216:219],a[72:75],v[244:247]\n"
MF "v[244:247],a[220:223],a[112:115],v[244:247]\n"
TR "a[212:213],v0 offset:4104\n"
MF "v[236:239],a[220:223],a[108:111],v[236:239]\n"
TR "a[214:215],v0 offset:4616\n"
MF "v[236:239],a[216:219],a[68:71],v[236:239]\n"
LD "v12,s[28:31],s82 offen offset:2048 lds\n"
MF "v[228:231],a[216:219],a[64:67],v[228:231]\n"
MF "v[228:231],a[220:223],a[104:107],v[228:231]\n"
MF "v[220:223],a[220:223],a[100:103],v[220:223]\n"
MF "v[220:223],a[216:219],a[60:63],v[220:223]\n"
"s_waitcnt lgkmcnt(4)\n"
MF "v[16:19],a[200:203],a[140:143],v[16:19]\n"
TR "a[216:217],v1\n"
MF "v[16:19],a[204:207],a[180:183],v[16:19]\n"
TR "a[218:219],v1 offset:512\n"
MF "v[24:27],a[204:207],a[184:187],v[24:27]\n"
LD "v13,s[28:31],s82 offen offset:3072 lds\n"
MF "v[24:27],a[200:203],a[144:147],v[24:27]\n"
TR "a[220:221],v1 offset:4096\n"
MF "v[32:35],a[200:203],a[148:151],v[32:35]\n"
TR "a[222:223],v1 offset:4608\n"
MF "v[32:35],a[204:207],a[188:191],v[32:35]\n"
MF "v[40:43],a[204:207],a[192:195],v[40:43]\n"
MF "v[40:43],a[200:203],a[152:155],v[40:43]\n"
"s_add_u32 s84,s14,40960\n"
MF "v[48:51],a[200:203],a[156:159],v[48:51]\n"
"s_add_u32 m0,s63,45056\n"
MF "v[48:51],a[204:207],a[196:199],v[48:51]\n"
"s_waitcnt lgkmcnt(4)\n"
LD "v4,s[24:27],s84 offen lds\n"
MF "v[52:55],a[212:215],a[196:199],v[52:55]\n"
TR "a[200:201],v1 offset:8\n"
MF "v[52:55],a[208:211],a[156:159],v[52:55]\n"
TR "a[202:203],v1 offset:520\n"
MF "v[44:47],a[208:211],a[152:155],v[44:47]\n"
MF "v[44:47],a[212:215],a[192:195],v[44:47]\n"
TR "a[204:205],v1 offset:4104\n"
MF "v[36:39],a[212:215],a[188:191],v[36:39]\n"
TR "a[206:207],v1 offset:4616\n"
MF "v[36:39],a[208:211],a[148:151],v[36:39]\n"
MF "v[28:31],a[208:211],a[144:147],v[28:31]\n"
MF "v[28:31],a[212:215],a[184:187],v[28:31]\n"
MF "v[20:23],a[212:215],a[180:183],v[20:23]\n"
"s_waitcnt vmcnt(24) lgkmcnt(0)\n"
MF "v[20:23],a[208:211],a[140:143],v[20:23]\n"
"s_barrier\n"
MF "v[56:59],a[216:219],a[140:143],v[56:59]\n"
"s_add_u32 m0,s62,49152\n"
"s_add_u32 s82,s66,3932160\n"
TR "a[208:209],v0 offset:16384\n"
MF "v[56:59],a[220:223],a[180:183],v[56:59]\n"
"s_sub_u32 s15,s82,7864320\n"
"s_min_u32 s82,s82,s15\n"
TR "a[210:211],v0 offset:16896\n"
MF "v[64:67],a[220:223],a[184:187],v[64:67]\n"
LD "v6,s[28:31],s82 offen lds\n"
MF "v[64:67],a[216:219],a[144:147],v[64:67]\n"
TR "a[212:213],v0 offset:20480\n"
MF "v[72:75],a[216:219],a[148:151],v[72:75]\n"
TR "a[214:215],v0 offset:20992\n"
MF "v[72:75],a[220:223],a[188:191],v[72:75]\n"
MF "v[80:83],a[220:223],a[192:195],v[80:83]\n"
LD "v11,s[28:31],s82 offen offset:1024 lds\n"
MF "v[80:83],a[216:219],a[152:155],v[80:83]\n"
MF "v[88:91],a[216:219],a[156:159],v[88:91]\n"
MF "v[88:91],a[220:223],a[196:199],v[88:91]\n"
MF "v[92:95],a[204:207],a[196:199],v[92:95]\n"
TR "a[216:217],v0 offset:16392\n"
MF "v[92:95],a[200:203],a[156:159],v[92:95]\n"
TR "a[218:219],v0 offset:16904\n"
MF "v[84:87],a[200:203],a[152:155],v[84:87]\n"
LD "v12,s[28:31],s82 offen offset:2048 lds\n"
MF "v[84:87],a[204:207],a[192:195],v[84:87]\n"
TR "a[220:221],v0 offset:20488\n"
MF "v[76:79],a[204:207],a[188:191],v[76:79]\n"
TR "a[222:223],v0 offset:21000\n"
MF "v[76:79],a[200:203],a[148:151],v[76:79]\n"
MF "v[68:71],a[200:203],a[144:147],v[68:71]\n"
MF "v[68:71],a[204:207],a[184:187],v[68:71]\n"
MF "v[60:63],a[204:207],a[180:183],v[60:63]\n"
LD "v13,s[28:31],s82 offen offset:3072 lds\n"
MF "v[60:63],a[200:203],a[140:143],v[60:63]\n"
"s_waitcnt lgkmcnt(4)\n"
MF "v[96:99],a[208:211],a[140:143],v[96:99]\n"
TR "a[200:201],v1 offset:16384\n"
MF "v[96:99],a[212:215],a[180:183],v[96:99]\n"
TR "a[202:203],v1 offset:16896\n"
MF "v[104:107],a[212:215],a[184:187],v[104:107]\n"
"s_add_u32 s14,s81,768\n"
"s_sub_u32 s15,s14,1280\n"
MF "v[104:107],a[208:211],a[144:147],v[104:107]\n"
"s_min_u32 s14,s14,s15\n"
"s_add_u32 s14,s14,s68\n"
TR "a[204:205],v1 offset:20480\n"
MF "v[112:115],a[208:211],a[148:151],v[112:115]\n"
"s_add_u32 s84,s14,0\n"
"s_add_u32 m0,s63,0\n"
TR "a[206:207],v1 offset:20992\n"
MF "v[112:115],a[212:215],a[188:191],v[112:115]\n"
LD "v4,s[24:27],s84 offen lds\n"
MF "v[120:123],a[212:215],a[192:195],v[120:123]\n"
MF "v[120:123],a[208:211],a[152:155],v[120:123]\n"
MF "v[128:131],a[208:211],a[156:159],v[128:131]\n"
"s_add_u32 s84,s14,9216\n"
MF "v[128:131],a[212:215],a[196:199],v[128:131]\n"
"s_waitcnt lgkmcnt(4)\n"
LD "v5,s[24:27],s84 offen offset:1024 lds\n"
MF "v[132:135],a[220:223],a[196:199],v[132:135]\n"
TR "a[208:209],v1 offset:16392\n"
MF "v[132:135],a[216:219],a[156:159],v[132:135]\n"
TR "a[210:211],v1 offset:16904\n"
MF "v[124:127],a[216:219],a[152:155],v[124:127]\n"
MF "v[124:127],a[220:223],a[192:195],v[124:127]\n"
TR "a[212:213],v1 offset:20488\n"
MF "v[116:119],a[220:223],a[188:191],v[116:119]\n"
TR "a[214:215],v1 offset:21000\n"
MF "v[116:119],a[216:219],a[148:151],v[116:119]\n"
MF "v[108:111],a[216:219],a[144:147],v[108:111]\n"
MF "v[108:111],a[220:223],a[184:187],v[108:111]\n"
MF "v[100:103],a[220:223],a[180:183],v[100:103]\n"
"s_waitcnt vmcnt(23) lgkmcnt(0)\n"
MF "v[100:103],a[216:219],a[140:143],v[100:103]\n"
"s_barrier\n"
MF "v[136:139],a[200:203],a[140:143],v[136:139]\n"
TR "a[216:217],v0 offset:32768\n"
MF "v[136:139],a[204:207],a[180:183],v[136:139]\n"
"s_add_u32 m0,s62,65536\n"
TR "a[218:219],v0 offset:33280\n"
MF "v[144:147],a[204:207],a[184:187],v[144:147]\n"
"s_add_u32 s82,s66,3932288\n"
"ds_read_b128 a[60:63],v2 offset:20480\n"
MF "v[144:147],a[200:203],a[144:147],v[144:147]\n"
"s_sub_u32 s15,s82,7864320\n"
TR "a[220:221],v0 offset:36864\n"
MF "v[152:155],a[200:203],a[148:151],v[152:155]\n"
"s_min_u32 s82,s82,s15\n"
TR "a[222:223],v0 offset:37376\n"
MF "v[152:155],a[204:207],a[188:191],v[152:155]\n"
LD "v6,s[28:31],s82 offen lds\n"
MF "v[160:163],a[204:207],a[192:195],v[160:163]\n"
"ds_read_b128 a[100:103],v3 offset:20480\n"
MF "v[160:163],a[200:203],a[152:155],v[160:163]\n"
LD "v11,s[28:31],s82 offen offset:1024 lds\n"
MF "v[168:171],a[200:203],a[156:159],v[168:171]\n"
MF "v[168:171],a[204:207],a[196:199],v[168:171]\n"
LD "v12,s[28:31],s82 offen offset:2048 lds\n"
MF "v[172:175],a[212:215],a[196:199],v[172:175]\n"
TR "a[200:201],v0 offset:32776\n"
MF "v[172:175],a[208:211],a[156:159],v[172:175]\n"
TR "a[202:203],v0 offset:33288\n"
MF "v[164:167],a[208:211],a[152:155],v[164:167]\n"
"ds_read_b128 a[64:67],v2 offset:22528\n"
MF "v[164:167],a[212:215],a[192:195],v[164:167]\n"
TR "a[204:205],v0 offset:36872\n"
MF "v[156:159],a[212:215],a[188:191],v[156:159]\n"
TR "a[206:207],v0 offset:37384\n"
MF "v[156:159],a[208:211],a[148:151],v[156:159]\n"
MF "v[148:151],a[208:211],a[144:147],v[148:151]\n"
"ds_read_b128 a[104:107],v3 offset:22528\n"
MF "v[148:151],a[212:215],a[184:187],v[148:151]\n"
MF "v[140:143],a[212:215],a[180:183],v[140:143]\n"
LD "v13,s[28:31],s82 offen offset:3072 lds\n"
MF "v[140:143],a[208:211],a[140:143],v[140:143]\n"
"s_waitcnt lgkmcnt(7)\n"
MF "v[176:179],a[216:219],a[140:143],v[176:179]\n"
TR "a[208:209],v1 offset:32768\n"
MF "v[176:179],a[220:223],a[180:183],v[176:179]\n"
TR "a[210:211],v1 offset:33280\n"
MF "v[184:187],a[220:223],a[184:187],v[184:187]\n"
"ds_read_b128 a[68:71],v2 offset:24576\n"
MF "v[184:187],a[216:219],a[144:147],v[184:187]\n"
"s_add_u32 s84,s14,20480\n"
TR "a[212:213],v1 offset:36864\n"
MF "v[192:195],a[216:219],a[148:151],v[192:195]\n"
"s_add_u32 m0,s63,2048\n"
TR "a[214:215],v1 offset:37376\n"
MF "v[192:195],a[220:223],a[188:191],v[192:195]\n"
LD "v4,s[24:27],s84 offen lds\n"
MF "v[200:203],a[220:223],a[192:195],v[200:203]\n"
"ds_read_b128 a[108:111],v3 offset:24576\n"
MF "v[200:203],a[216:219],a[152:155],v[200:203]\n"
MF "v[208:211],a[216:219],a[156:159],v[208:211]\n"
"s_add_u32 s84,s14,29696\n"
MF "v[208:211],a[220:223],a[196:199],v[208:211]\n"
"s_waitcnt lgkmcnt(7)\n"
LD "v5,s[24:27],s84 offen offset:1024 lds\n"
MF "v[212:215],a[204:207],a[196:199],v[212:215]\n"
TR "a[216:217],v1 offset:32776\n"
MF "v[212:215],a[200:203],a[156:159],v[212:215]\n"
TR "a[218:219],v1 offset:33288\n"
MF "v[204:207],a[200:203],a[152:155],v[204:207]\n"
"ds_read_b128 a[72:75],v2 offset:26624\n"
MF "v[204:207],a[204:207],a[192:195],v[204:207]\n"
TR "a[220:221],v1 offset:36872\n"
MF "v[196:199],a[204:207],a[188:191],v[196:199]\n"
TR "a[222:223],v1 offset:37384\n"
MF "v[196:199],a[200:203],a[148:151],v[196:199]\n"
MF "v[188:191],a[200:203],a[144:147],v[188:191]\n"
"v_add_u32 v0,0xffff4000,v0\n"
MF "v[188:191],a[204:207],a[184:187],v[188:191]\n"
"v_add_u32 v1,0xffff4000,v1\n"
MF "v[180:183],a[204:207],a[180:183],v[180:183]\n"
"s_waitcnt vmcnt(25) lgkmcnt(0)\n"
MF "v[180:183],a[200:203],a[140:143],v[180:183]\n"
"s_barrier\n"
MF "v[216:219],a[208:211],a[140:143],v[216:219]\n"
TR "a[200:201],v0\n"
MF "v[216:219],a[212:215],a[180:183],v[216:219]\n"
"s_add_u32 m0,s62,81920\n"
TR "a[202:203],v0 offset:512\n"
MF "v[224:227],a[212:215],a[184:187],v[224:227]\n"
"s_add_u32 s82,s66,3932416\n"
"ds_read_b128 a[112:115],v3 offset:26624\n"
MF "v[224:227],a[208:211],a[144:147],v[224:227]\n"
"s_sub_u32 s15,s82,7864320\n"
TR "a[204:205],v0 offset:4096\n"
MF "v[232:235],a[208:211],a[148:151],v[232:235]\n"
"s_min_u32 s82,s82,s15\n"
TR "a[206:207],v0 offset:4608\n"
MF "v[232:235],a[212:215],a[188:191],v[232:235]\n"
LD "v6,s[28:31],s82 offen lds\n"
MF "v[240:243],a[212:215],a[192:195],v[240:243]\n"
"ds_read_b128 a[76:79],v2 offset:28672\n"
MF "v[240:243],a[208:211],a[152:155],v[240:243]\n"
"ds_read_b128 a[116:119],v3 offset:28672\n"
MF "v[248:251],a[208:211],a[156:159],v[248:251]\n"
LD "v11,s[28:31],s82 offen offset:1024 lds\n"
MF "v[248:251],a[212:215],a[196:199],v[248:251]\n"
MF "v[252:255],a[220:223],a[196:199],v[252:255]\n"
TR "a[208:209],v0 offset:8\n"
MF "v[252:255],a[216:219],a[156:159],v[252:255]\n"
TR "a[210:211],v0 offset:520\n"
MF "v[244:247],a[216:219],a[152:155],v[244:247]\n"
MF "v[244:247],a[220:223],a[192:195],v[244:247]\n"
TR "a[212:213],v0 offset:4104\n"
MF "v[236:239],a[220:223],a[188:191],v[236:239]\n"
TR "a[214:215],v0 offset:4616\n"
MF "v[236:239],a[216:219],a[148:151],v[236:239]\n"
LD "v12,s[28:31],s82 offen offset:2048 lds\n"
MF "v[228:231],a[216:219],a[144:147],v[228:231]\n"
MF "v[228:231],a[220:223],a[184:187],v[228:231]\n"
MF "v[220:223],a[220:223],a[180:183],v[220:223]\n"
MF "v[220:223],a[216:219],a[140:143],v[220:223]\n"
"s_waitcnt lgkmcnt(4)\n"
MF "v[16:19],a[200:203],a[60:63],v[16:19]\n"
TR "a[216:217],v1\n"
MF "v[16:19],a[204:207],a[100:103],v[16:19]\n"
TR "a[218:219],v1 offset:512\n"
MF "v[24:27],a[204:207],a[104:107],v[24:27]\n"
LD "v13,s[28:31],s82 offen offset:3072 lds\n"
MF "v[24:27],a[200:203],a[64:67],v[24:27]\n"
TR "a[220:221],v1 offset:4096\n"
MF "v[32:35],a[200:203],a[68:71],v[32:35]\n"
TR "a[222:223],v1 offset:4608\n"
MF "v[32:35],a[204:207],a[108:111],v[32:35]\n"
MF "v[40:43],a[204:207],a[112:115],v[40:43]\n"
MF "v[40:43],a[200:203],a[72:75],v[40:43]\n"
"s_add_u32 s84,s14,40960\n"
MF "v[48:51],a[200:203],a[76:79],v[48:51]\n"
"s_add_u32 m0,s63,4096\n"
MF "v[48:51],a[204:207],a[116:119],v[48:51]\n"
"s_waitcnt lgkmcnt(4)\n"
LD "v4,s[24:27],s84 offen lds\n"
MF "v[52:55],a[212:215],a[116:119],v[52:55]\n"
TR "a[200:201],v1 offset:8\n"
MF "v[52:55],a[208:211],a[76:79],v[52:55]\n"
TR "a[202:203],v1 offset:520\n"
MF "v[44:47],a[208:211],a[72:75],v[44:47]\n"
MF "v[44:47],a[212:215],a[112:115],v[44:47]\n"
TR "a[204:205],v1 offset:4104\n"
MF "v[36:39],a[212:215],a[108:111],v[36:39]\n"
TR "a[206:207],v1 offset:4616\n"
MF "v[36:39],a[208:211],a[68:71],v[36:39]\n"
MF "v[28:31],a[208:211],a[64:67],v[28:31]\n"
MF "v[28:31],a[212:215],a[104:107],v[28:31]\n"
MF "v[20:23],a[212:215],a[100:103],v[20:23]\n"
"s_waitcnt vmcnt(24) lgkmcnt(0)\n"
MF "v[20:23],a[208:211],a[60:63],v[20:23]\n"
"s_barrier\n"
MF "v[56:59],a[216:219],a[60:63],v[56:59]\n"
"s_add_u32 m0,s62,0\n"
"s_add_u32 s82,s66,4718592\n"
TR "a[208:209],v0 offset:16384\n"
MF "v[56:59],a[220:223],a[100:103],v[56:59]\n"
"s_sub_u32 s15,s82,7864320\n"
"s_min_u32 s82,s82,s15\n"
TR "a[210:211],v0 offset:16896\n"
MF "v[64:67],a[220:223],a[104:107],v[64:67]\n"
LD "v6,s[28:31],s82 offen lds\n"
MF "v[64:67],a[216:219],a[64:67],v[64:67]\n"
TR "a[212:213],v0 offset:20480\n"
MF "v[72:75],a[216:219],a[68:71],v[72:75]\n"
TR "a[214:215],v0 offset:20992\n"
MF "v[72:75],a[220:223],a[108:111],v[72:75]\n"
MF "v[80:83],a[220:223],a[112:115],v[80:83]\n"
LD "v11,s[28:31],s82 offen offset:1024 lds\n"
MF "v[80:83],a[216:219],a[72:75],v[80:83]\n"
MF "v[88:91],a[216:219],a[76:79],v[88:91]\n"
MF "v[88:91],a[220:223],a[116:119],v[88:91]\n"
MF "v[92:95],a[204:207],a[116:119],v[92:95]\n"
TR "a[216:217],v0 offset:16392\n"
MF "v[92:95],a[200:203],a[76:79],v[92:95]\n"
TR "a[218:219],v0 offset:16904\n"
MF "v[84:87],a[200:203],a[72:75],v[84:87]\n"
LD "v12,s[28:31],s82 offen offset:2048 lds\n"
MF "v[84:87],a[204:207],a[112:115],v[84:87]\n"
TR "a[220:221],v0 offset:20488\n"
MF "v[76:79],a[204:207],a[108:111],v[76:79]\n"
TR "a[222:223],v0 offset:21000\n"
MF "v[76:79],a[200:203],a[68:71],v[76:79]\n"
MF "v[68:71],a[200:203],a[64:67],v[68:71]\n"
MF "v[68:71],a[204:207],a[104:107],v[68:71]\n"
MF "v[60:63],a[204:207],a[100:103],v[60:63]\n"
LD "v13,s[28:31],s82 offen offset:3072 lds\n"
MF "v[60:63],a[200:203],a[60:63],v[60:63]\n"
"s_waitcnt lgkmcnt(4)\n"
MF "v[96:99],a[208:211],a[60:63],v[96:99]\n"
TR "a[200:201],v1 offset:16384\n"
MF "v[96:99],a[212:215],a[100:103],v[96:99]\n"
TR "a[202:203],v1 offset:16896\n"
MF "v[104:107],a[212:215],a[104:107],v[104:107]\n"
"s_add_u32 s14,s81,896\n"
"s_sub_u32 s15,s14,1280\n"
MF "v[104:107],a[208:211],a[64:67],v[104:107]\n"
"s_min_u32 s14,s14,s15\n"
"s_add_u32 s14,s14,s68\n"
TR "a[204:205],v1 offset:20480\n"
MF "v[112:115],a[208:211],a[68:71],v[112:115]\n"
"s_add_u32 s84,s14,0\n"
"s_add_u32 m0,s63,20480\n"
TR "a[206:207],v1 offset:20992\n"
MF "v[112:115],a[212:215],a[108:111],v[112:115]\n"
LD "v4,s[24:27],s84 offen lds\n"
MF "v[120:123],a[212:215],a[112:115],v[120:123]\n"
MF "v[120:123],a[208:211],a[72:75],v[120:123]\n"
MF "v[128:131],a[208:211],a[76:79],v[128:131]\n"
"s_add_u32 s84,s14,9216\n"
MF "v[128:131],a[212:215],a[116:119],v[128:131]\n"
"s_waitcnt lgkmcnt(4)\n"
LD "v5,s[24:27],s84 offen offset:1024 lds\n"
MF "v[132:135],a[220:223],a[116:119],v[132:135]\n"
TR "a[208:209],v1 offset:16392\n"
MF "v[132:135],a[216:219],a[76:79],v[132:135]\n"
TR "a[210:211],v1 offset:16904\n"
MF "v[124:127],a[216:219],a[72:75],v[124:127]\n"
MF "v[124:127],a[220:223],a[112:115],v[124:127]\n"
TR "a[212:213],v1 offset:20488\n"
MF "v[116:119],a[220:223],a[108:111],v[116:119]\n"
TR "a[214:215],v1 offset:21000\n"
MF "v[116:119],a[216:219],a[68:71],v[116:119]\n"
MF "v[108:111],a[216:219],a[64:67],v[108:111]\n"
MF "v[108:111],a[220:223],a[104:107],v[108:111]\n"
MF "v[100:103],a[220:223],a[100:103],v[100:103]\n"
"s_waitcnt vmcnt(23) lgkmcnt(0)\n"
MF "v[100:103],a[216:219],a[60:63],v[100:103]\n"
"s_barrier\n"
MF "v[136:139],a[200:203],a[60:63],v[136:139]\n"
TR "a[216:217],v0 offset:32768\n"
MF "v[136:139],a[204:207],a[100:103],v[136:139]\n"
"s_add_u32 m0,s62,16384\n"
TR "a[218:219],v0 offset:33280\n"
MF "v[144:147],a[204:207],a[104:107],v[144:147]\n"
"s_add_u32 s82,s66,4718720\n"
"ds_read_b128 a[140:143],v2 offset:40960\n"
MF "v[144:147],a[200:203],a[64:67],v[144:147]\n"
"s_sub_u32 s15,s82,7864320\n"
TR "a[220:221],v0 offset:36864\n"
MF "v[152:155],a[200:203],a[68:71],v[152:155]\n"
"s_min_u32 s82,s82,s15\n"
TR "a[222:223],v0 offset:37376\n"
MF "v[152:155],a[204:207],a[108:111],v[152:155]\n"
LD "v6,s[28:31],s82 offen lds\n"
MF "v[160:163],a[204:207],a[112:115],v[160:163]\n"
"ds_read_b128 a[180:183],v3 offset:40960\n"
MF "v[160:163],a[200:203],a[72:75],v[160:163]\n"
LD "v11,s[28:31],s82 offen offset:1024 lds\n"
MF "v[168:171],a[200:203],a[76:79],v[168:171]\n"
MF "v[168:171],a[204:207],a[116:119],v[168:171]\n"
LD "v12,s[28:31],s82 offen offset:2048 lds\n"
MF "v[172:175],a[212:215],a[116:119],v[172:175]\n"
TR "a[200:201],v0 offset:32776\n"
MF "v[172:175],a[208:211],a[76:79],v[172:175]\n"
TR "a[202:203],v0 offset:33288\n"
MF "v[164:167],a[208:211],a[72:75],v[164:167]\n"
"ds_read_b128 a[144:147],v2 offset:43008\n"
MF "v[164:167],a[212:215],a[112:115],v[164:167]\n"
TR "a[204:205],v0 offset:36872\n"
MF "v[156:159],a[212:215],a[108:111],v[156:159]\n"
TR "a[206:207],v0 offset:37384\n"
MF "v[156:159],a[208:211],a[68:71],v[156:159]\n"
MF "v[148:151],a[208:211],a[64:67],v[148:151]\n"
"ds_read_b128 a[184:187],v3 offset:43008\n"
MF "v[148:151],a[212:215],a[104:107],v[148:151]\n"
MF "v[140:143],a[212:215],a[100:103],v[140:143]\n"
LD "v13,s[28:31],s82 offen offset:3072 lds\n"
MF "v[140:143],a[208:211],a[60:63],v[140:143]\n"
"s_waitcnt lgkmcnt(7)\n"
MF "v[176:179],a[216:219],a[60:63],v[176:179]\n"
TR "a[208:209],v1 offset:32768\n"
MF "v[176:179],a[220:223],a[100:103],v[176:179]\n"
TR "a[210:211],v1 offset:33280\n"
MF "v[184:187],a[220:223],a[104:107],v[184:187]\n"
"ds_read_b128 a[148:151],v2 offset:45056\n"
MF "v[184:187],a[216:219],a[64:67],v[184:187]\n"
"s_add_u32 s84,s14,20480\n"
TR "a[212:213],v1 offset:36864\n"
MF "v[192:195],a[216:219],a[68:71],v[192:195]\n"
"s_add_u32 m0,s63,22528\n"
TR "a[214:215],v1 offset:37376\n"
MF "v[192:195],a[220:223],a[108:111],v[192:195]\n"
LD "v4,s[24:27],s84 offen lds\n"
MF "v[200:203],a[220:223],a[112:115],v[200:203]\n"
"ds_read_b128 a[188:191],v3 offset:45056\n"
MF "v[200:203],a[216:219],a[72:75],v[200:203]\n"
MF "v[208:211],a[216:219],a[76:79],v[208:211]\n"
"s_add_u32 s84,s14,29696\n"
MF "v[208:211],a[220:223],a[116:119],v[208:211]\n"
"s_waitcnt lgkmcnt(7)\n"
LD "v5,s[24:27],s84 offen offset:1024 lds\n"
MF "v[212:215],a[204:207],a[116:119],v[212:215]\n"
TR "a[216:217],v1 offset:32776\n"
MF "v[212:215],a[200:203],a[76:79],v[212:215]\n"
TR "a[218:219],v1 offset:33288\n"
MF "v[204:207],a[200:203],a[72:75],v[204:207]\n"
"ds_read_b128 a[152:155],v2 offset:47104\n"
MF "v[204:207],a[204:207],a[112:115],v[204:207]\n"
TR "a[220:221],v1 offset:36872\n"
MF "v[196:199],a[204:207],a[108:111],v[196:199]\n"
TR "a[222:223],v1 offset:37384\n"
MF "v[196:199],a[200:203],a[68:71],v[196:199]\n"
MF "v[188:191],a[200:203],a[64:67],v[188:191]\n"
"v_add_u32 v0,0xc000,v0\n"
MF "v[188:191],a[204:207],a[104:107],v[188:191]\n"
"v_add_u32 v1,0xc000,v1\n"
MF "v[180:183],a[204:207],a[100:103],v[180:183]\n"
"s_waitcnt vmcnt(25) lgkmcnt(0)\n"
MF "v[180:183],a[200:203],a[60:63],v[180:183]\n"
"s_barrier\n"
MF "v[216:219],a[208:211],a[60:63],v[216:219]\n"
TR "a[200:201],v0\n"
MF "v[216:219],a[212:215],a[100:103],v[216:219]\n"
"s_add_u32 m0,s62,32768\n"
TR "a[202:203],v0 offset:512\n"
MF "v[224:227],a[212:215],a[104:107],v[224:227]\n"
"s_add_u32 s82,s66,4718848\n"
"ds_read_b128 a[192:195],v3 offset:47104\n"
MF "v[224:227],a[208:211],a[64:67],v[224:227]\n"
"s_sub_u32 s15,s82,7864320\n"
TR "a[204:205],v0 offset:4096\n"
MF "v[232:235],a[208:211],a[68:71],v[232:235]\n"
"s_min_u32 s82,s82,s15\n"
TR "a[206:207],v0 offset:4608\n"
MF "v[232:235],a[212:215],a[108:111],v[232:235]\n"
LD "v6,s[28:31],s82 offen lds\n"
MF "v[240:243],a[212:215],a[112:115],v[240:243]\n"
"ds_read_b128 a[156:159],v2 offset:49152\n"
MF "v[240:243],a[208:211],a[72:75],v[240:243]\n"
"ds_read_b128 a[196:199],v3 offset:49152\n"
MF "v[248:251],a[208:211],a[76:79],v[248:251]\n"
LD "v11,s[28:31],s82 offen offset:1024 lds\n"
MF "v[248:251],a[212:215],a[116:119],v[248:251]\n"
MF "v[252:255],a[220:223],a[116:119],v[252:255]\n"
TR "a[208:209],v0 offset:8\n"
MF "v[252:255],a[216:219],a[76:79],v[252:255]\n"
TR "a[210:211],v0 offset:520\n"
MF "v[244:247],a[216:219],a[72:75],v[244:247]\n"
MF "v[244:247],a[220:223],a[112:115],v[244:247]\n"
TR "a[212:213],v0 offset:4104\n"
MF "v[236:239],a[220:223],a[108:111],v[236:239]\n"
TR "a[214:215],v0 offset:4616\n"
MF "v[236:239],a[216:219],a[68:71],v[236:239]\n"
LD "v12,s[28:31],s82 offen offset:2048 lds\n"
MF "v[228:231],a[216:219],a[64:67],v[228:231]\n"
MF "v[228:231],a[220:223],a[104:107],v[228:231]\n"
MF "v[220:223],a[220:223],a[100:103],v[220:223]\n"
MF "v[220:223],a[216:219],a[60:63],v[220:223]\n"
"s_waitcnt lgkmcnt(4)\n"
MF "v[16:19],a[200:203],a[140:143],v[16:19]\n"
TR "a[216:217],v1\n"
MF "v[16:19],a[204:207],a[180:183],v[16:19]\n"
TR "a[218:219],v1 offset:512\n"
MF "v[24:27],a[204:207],a[184:187],v[24:27]\n"
LD "v13,s[28:31],s82 offen offset:3072 lds\n"
MF "v[24:27],a[200:203],a[144:147],v[24:27]\n"
TR "a[220:221],v1 offset:4096\n"
MF "v[32:35],a[200:203],a[148:151],v[32:35]\n"
TR "a[222:223],v1 offset:4608\n"
MF "v[32:35],a[204:207],a[188:191],v[32:35]\n"
MF "v[40:43],a[204:207],a[192:195],v[40:43]\n"
MF "v[40:43],a[200:203],a[152:155],v[40:43]\n"
"s_add_u32 s84,s14,40960\n"
MF "v[48:51],a[200:203],a[156:159],v[48:51]\n"
"s_add_u32 m0,s63,24576\n"
MF "v[48:51],a[204:207],a[196:199],v[48:51]\n"
"s_waitcnt lgkmcnt(4)\n"
LD "v4,s[24:27],s84 offen lds\n"
MF "v[52:55],a[212:215],a[196:199],v[52:55]\n"
TR "a[200:201],v1 offset:8\n"
MF "v[52:55],a[208:211],a[156:159],v[52:55]\n"
TR "a[202:203],v1 offset:520\n"
MF "v[44:47],a[208:211],a[152:155],v[44:47]\n"
MF "v[44:47],a[212:215],a[192:195],v[44:47]\n"
TR "a[204:205],v1 offset:4104\n"
MF "v[36:39],a[212:215],a[188:191],v[36:39]\n"
TR "a[206:207],v1 offset:4616\n"
MF "v[36:39],a[208:211],a[148:151],v[36:39]\n"
MF "v[28:31],a[208:211],a[144:147],v[28:31]\n"
MF "v[28:31],a[212:215],a[184:187],v[28:31]\n"
MF "v[20:23],a[212:215],a[180:183],v[20:23]\n"
"s_waitcnt vmcnt(24) lgkmcnt(0)\n"
MF "v[20:23],a[208:211],a[140:143],v[20:23]\n"
".Lpg6c:\n"
"s_cbranch_execz .Lpg1a\n"
".Lpg6f:\n"
"s_cbranch_execz .Lpg16f\n"
"s_barrier\n"
MF "v[56:59],a[216:219],a[140:143],v[56:59]\n"
"s_add_u32 m0,s62,49152\n"
"s_add_u32 s82,s66,5505024\n"
TR "a[208:209],v0 offset:16384\n"
MF "v[56:59],a[220:223],a[180:183],v[56:59]\n"
"s_sub_u32 s15,s82,7864320\n"
"s_min_u32 s82,s82,s15\n"
TR "a[210:211],v0 offset:16896\n"
MF "v[64:67],a[220:223],a[184:187],v[64:67]\n"
LD "v6,s[28:31],s82 offen lds\n"
MF "v[64:67],a[216:219],a[144:147],v[64:67]\n"
TR "a[212:213],v0 offset:20480\n"
MF "v[72:75],a[216:219],a[148:151],v[72:75]\n"
TR "a[214:215],v0 offset:20992\n"
MF "v[72:75],a[220:223],a[188:191],v[72:75]\n"
MF "v[80:83],a[220:223],a[192:195],v[80:83]\n"
LD "v11,s[28:31],s82 offen offset:1024 lds\n"
MF "v[80:83],a[216:219],a[152:155],v[80:83]\n"
MF "v[88:91],a[216:219],a[156:159],v[88:91]\n"
MF "v[88:91],a[220:223],a[196:199],v[88:91]\n"
MF "v[92:95],a[204:207],a[196:199],v[92:95]\n"
TR "a[216:217],v0 offset:16392\n"
MF "v[92:95],a[200:203],a[156:159],v[92:95]\n"
TR "a[218:219],v0 offset:16904\n"
MF "v[84:87],a[200:203],a[152:155],v[84:87]\n"
LD "v12,s[28:31],s82 offen offset:2048 lds\n"
MF "v[84:87],a[204:207],a[192:195],v[84:87]\n"
TR "a[220:221],v0 offset:20488\n"
MF "v[76:79],a[204:207],a[188:191],v[76:79]\n"
TR "a[222:223],v0 offset:21000\n"
MF "v[76:79],a[200:203],a[148:151],v[76:79]\n"
MF "v[68:71],a[200:203],a[144:147],v[68:71]\n"
MF "v[68:71],a[204:207],a[184:187],v[68:71]\n"
MF "v[60:63],a[204:207],a[180:183],v[60:63]\n"
LD "v13,s[28:31],s82 offen offset:3072 lds\n"
MF "v[60:63],a[200:203],a[140:143],v[60:63]\n"
"s_waitcnt lgkmcnt(4)\n"
MF "v[96:99],a[208:211],a[140:143],v[96:99]\n"
TR "a[200:201],v1 offset:16384\n"
MF "v[96:99],a[212:215],a[180:183],v[96:99]\n"
TR "a[202:203],v1 offset:16896\n"
MF "v[104:107],a[212:215],a[184:187],v[104:107]\n"
"s_add_u32 s14,s81,1024\n"
"s_sub_u32 s15,s14,1280\n"
MF "v[104:107],a[208:211],a[144:147],v[104:107]\n"
"s_min_u32 s14,s14,s15\n"
"s_add_u32 s14,s14,s68\n"
TR "a[204:205],v1 offset:20480\n"
MF "v[112:115],a[208:211],a[148:151],v[112:115]\n"
"s_add_u32 s84,s14,0\n"
"s_add_u32 m0,s63,40960\n"
TR "a[206:207],v1 offset:20992\n"
MF "v[112:115],a[212:215],a[188:191],v[112:115]\n"
LD "v4,s[24:27],s84 offen lds\n"
MF "v[120:123],a[212:215],a[192:195],v[120:123]\n"
MF "v[120:123],a[208:211],a[152:155],v[120:123]\n"
MF "v[128:131],a[208:211],a[156:159],v[128:131]\n"
"s_add_u32 s84,s14,9216\n"
MF "v[128:131],a[212:215],a[196:199],v[128:131]\n"
"s_waitcnt lgkmcnt(4)\n"
LD "v5,s[24:27],s84 offen offset:1024 lds\n"
MF "v[132:135],a[220:223],a[196:199],v[132:135]\n"
TR "a[208:209],v1 offset:16392\n"
MF "v[132:135],a[216:219],a[156:159],v[132:135]\n"
TR "a[210:211],v1 offset:16904\n"
MF "v[124:127],a[216:219],a[152:155],v[124:127]\n"
MF "v[124:127],a[220:223],a[192:195],v[124:127]\n"
TR "a[212:213],v1 offset:20488\n"
MF "v[116:119],a[220:223],a[188:191],v[116:119]\n"
TR "a[214:215],v1 offset:21000\n"
MF "v[116:119],a[216:219],a[148:151],v[116:119]\n"
MF "v[108:111],a[216:219],a[144:147],v[108:111]\n"
MF "v[108:111],a[220:223],a[184:187],v[108:111]\n"
MF "v[100:103],a[220:223],a[180:183],v[100:103]\n"
"s_waitcnt vmcnt(23) lgkmcnt(0)\n"
MF "v[100:103],a[216:219],a[140:143],v[100:103]\n"
"s_barrier\n"
"s_cmp_eq_u32 s57,1\n"
"s_cbranch_scc0 .Lhs12\n"
"v_bfe_u32 v14,v9,4,5\n"
"v_lshlrev_b32 v14,6,v14\n"
"s_lshl_b32 s58,s78,2\n"
"s_add_u32 s58,s58,s86\n"
"buffer_load_dword v10,v14,s[40:43],s58 offen offset:128 sc1\n"
".Lhs12:\n"
MF "v[136:139],a[200:203],a[140:143],v[136:139]\n"
TR "a[216:217],v0 offset:32768\n"
MF "v[136:139],a[204:207],a[180:183],v[136:139]\n"
"s_add_u32 m0,s62,65536\n"
TR "a[218:219],v0 offset:33280\n"
MF "v[144:147],a[204:207],a[184:187],v[144:147]\n"
"s_add_u32 s82,s66,5505152\n"
"ds_read_b128 a[60:63],v2\n"
MF "v[144:147],a[200:203],a[144:147],v[144:147]\n"
"s_sub_u32 s15,s82,7864320\n"
TR "a[220:221],v0 offset:36864\n"
MF "v[152:155],a[200:203],a[148:151],v[152:155]\n"
"s_min_u32 s82,s82,s15\n"
TR "a[222:223],v0 offset:37376\n"
MF "v[152:155],a[204:207],a[188:191],v[152:155]\n"
LD "v6,s[28:31],s82 offen lds\n"
MF "v[160:163],a[204:207],a[192:195],v[160:163]\n"
"ds_read_b128 a[100:103],v3\n"
MF "v[160:163],a[200:203],a[152:155],v[160:163]\n"
LD "v11,s[28:31],s82 offen offset:1024 lds\n"
MF "v[168:171],a[200:203],a[156:159],v[168:171]\n"
MF "v[168:171],a[204:207],a[196:199],v[168:171]\n"
LD "v12,s[28:31],s82 offen offset:2048 lds\n"
MF "v[172:175],a[212:215],a[196:199],v[172:175]\n"
TR "a[200:201],v0 offset:32776\n"
MF "v[172:175],a[208:211],a[156:159],v[172:175]\n"
TR "a[202:203],v0 offset:33288\n"
MF "v[164:167],a[208:211],a[152:155],v[164:167]\n"
"ds_read_b128 a[64:67],v2 offset:2048\n"
MF "v[164:167],a[212:215],a[192:195],v[164:167]\n"
TR "a[204:205],v0 offset:36872\n"
MF "v[156:159],a[212:215],a[188:191],v[156:159]\n"
TR "a[206:207],v0 offset:37384\n"
MF "v[156:159],a[208:211],a[148:151],v[156:159]\n"
MF "v[148:151],a[208:211],a[144:147],v[148:151]\n"
"ds_read_b128 a[104:107],v3 offset:2048\n"
MF "v[148:151],a[212:215],a[184:187],v[148:151]\n"
MF "v[140:143],a[212:215],a[180:183],v[140:143]\n"
LD "v13,s[28:31],s82 offen offset:3072 lds\n"
MF "v[140:143],a[208:211],a[140:143],v[140:143]\n"
"s_waitcnt lgkmcnt(7)\n"
MF "v[176:179],a[216:219],a[140:143],v[176:179]\n"
TR "a[208:209],v1 offset:32768\n"
MF "v[176:179],a[220:223],a[180:183],v[176:179]\n"
TR "a[210:211],v1 offset:33280\n"
MF "v[184:187],a[220:223],a[184:187],v[184:187]\n"
"ds_read_b128 a[68:71],v2 offset:4096\n"
MF "v[184:187],a[216:219],a[144:147],v[184:187]\n"
"s_add_u32 s84,s14,20480\n"
TR "a[212:213],v1 offset:36864\n"
MF "v[192:195],a[216:219],a[148:151],v[192:195]\n"
"s_add_u32 m0,s63,43008\n"
TR "a[214:215],v1 offset:37376\n"
MF "v[192:195],a[220:223],a[188:191],v[192:195]\n"
LD "v4,s[24:27],s84 offen lds\n"
MF "v[200:203],a[220:223],a[192:195],v[200:203]\n"
"ds_read_b128 a[108:111],v3 offset:4096\n"
MF "v[200:203],a[216:219],a[152:155],v[200:203]\n"
MF "v[208:211],a[216:219],a[156:159],v[208:211]\n"
"s_add_u32 s84,s14,29696\n"
MF "v[208:211],a[220:223],a[196:199],v[208:211]\n"
"s_waitcnt lgkmcnt(7)\n"
LD "v5,s[24:27],s84 offen offset:1024 lds\n"
MF "v[212:215],a[204:207],a[196:199],v[212:215]\n"
TR "a[216:217],v1 offset:32776\n"
MF "v[212:215],a[200:203],a[156:159],v[212:215]\n"
TR "a[218:219],v1 offset:33288\n"
MF "v[204:207],a[200:203],a[152:155],v[204:207]\n"
"ds_read_b128 a[72:75],v2 offset:6144\n"
MF "v[204:207],a[204:207],a[192:195],v[204:207]\n"
TR "a[220:221],v1 offset:36872\n"
MF "v[196:199],a[204:207],a[188:191],v[196:199]\n"
TR "a[222:223],v1 offset:37384\n"
MF "v[196:199],a[200:203],a[148:151],v[196:199]\n"
MF "v[188:191],a[200:203],a[144:147],v[188:191]\n"
"v_add_u32 v0,0xffff4000,v0\n"
MF "v[188:191],a[204:207],a[184:187],v[188:191]\n"
"v_add_u32 v1,0xffff4000,v1\n"
MF "v[180:183],a[204:207],a[180:183],v[180:183]\n"
"s_waitcnt vmcnt(25) lgkmcnt(0)\n"
MF "v[180:183],a[200:203],a[140:143],v[180:183]\n"
"s_barrier\n"
MF "v[216:219],a[208:211],a[140:143],v[216:219]\n"
TR "a[200:201],v0\n"
MF "v[216:219],a[212:215],a[180:183],v[216:219]\n"
"s_add_u32 m0,s62,81920\n"
TR "a[202:203],v0 offset:512\n"
MF "v[224:227],a[212:215],a[184:187],v[224:227]\n"
"s_add_u32 s82,s66,5505280\n"
"ds_read_b128 a[112:115],v3 offset:6144\n"
MF "v[224:227],a[208:211],a[144:147],v[224:227]\n"
"s_sub_u32 s15,s82,7864320\n"
TR "a[204:205],v0 offset:4096\n"
MF "v[232:235],a[208:211],a[148:151],v[232:235]\n"
"s_min_u32 s82,s82,s15\n"
TR "a[206:207],v0 offset:4608\n"
MF "v[232:235],a[212:215],a[188:191],v[232:235]\n"
LD "v6,s[28:31],s82 offen lds\n"
MF "v[240:243],a[212:215],a[192:195],v[240:243]\n"
"ds_read_b128 a[76:79],v2 offset:8192\n"
MF "v[240:243],a[208:211],a[152:155],v[240:243]\n"
"ds_read_b128 a[116:119],v3 offset:8192\n"
MF "v[248:251],a[208:211],a[156:159],v[248:251]\n"
LD "v11,s[28:31],s82 offen offset:1024 lds\n"
MF "v[248:251],a[212:215],a[196:199],v[248:251]\n"
MF "v[252:255],a[220:223],a[196:199],v[252:255]\n"
TR "a[208:209],v0 offset:8\n"
MF "v[252:255],a[216:219],a[156:159],v[252:255]\n"
TR "a[210:211],v0 offset:520\n"
MF "v[244:247],a[216:219],a[152:155],v[244:247]\n"
MF "v[244:247],a[220:223],a[192:195],v[244:247]\n"
TR "a[212:213],v0 offset:4104\n"
MF "v[236:239],a[220:223],a[188:191],v[236:239]\n"
TR "a[214:215],v0 offset:4616\n"
MF "v[236:239],a[216:219],a[148:151],v[236:239]\n"
LD "v12,s[28:31],s82 offen offset:2048 lds\n"
MF "v[228:231],a[216:219],a[144:147],v[228:231]\n"
MF "v[228:231],a[220:223],a[184:187],v[228:231]\n"
MF "v[220:223],a[220:223],a[180:183],v[220:223]\n"
MF "v[220:223],a[216:219],a[140:143],v[220:223]\n"
"s_waitcnt lgkmcnt(4)\n"
MF "v[16:19],a[200:203],a[60:63],v[16:19]\n"
TR "a[216:217],v1\n"
MF "v[16:19],a[204:207],a[100:103],v[16:19]\n"
TR "a[218:219],v1 offset:512\n"
MF "v[24:27],a[204:207],a[104:107],v[24:27]\n"
LD "v13,s[28:31],s82 offen offset:3072 lds\n"
MF "v[24:27],a[200:203],a[64:67],v[24:27]\n"
TR "a[220:221],v1 offset:4096\n"
MF "v[32:35],a[200:203],a[68:71],v[32:35]\n"
TR "a[222:223],v1 offset:4608\n"
MF "v[32:35],a[204:207],a[108:111],v[32:35]\n"
MF "v[40:43],a[204:207],a[112:115],v[40:43]\n"
MF "v[40:43],a[200:203],a[72:75],v[40:43]\n"
"s_add_u32 s84,s14,40960\n"
MF "v[48:51],a[200:203],a[76:79],v[48:51]\n"
"s_add_u32 m0,s63,45056\n"
MF "v[48:51],a[204:207],a[116:119],v[48:51]\n"
"s_waitcnt lgkmcnt(4)\n"
LD "v4,s[24:27],s84 offen lds\n"
MF "v[52:55],a[212:215],a[116:119],v[52:55]\n"
TR "a[200:201],v1 offset:8\n"
MF "v[52:55],a[208:211],a[76:79],v[52:55]\n"
TR "a[202:203],v1 offset:520\n"
MF "v[44:47],a[208:211],a[72:75],v[44:47]\n"
MF "v[44:47],a[212:215],a[112:115],v[44:47]\n"
TR "a[204:205],v1 offset:4104\n"
MF "v[36:39],a[212:215],a[108:111],v[36:39]\n"
TR "a[206:207],v1 offset:4616\n"
MF "v[36:39],a[208:211],a[68:71],v[36:39]\n"
MF "v[28:31],a[208:211],a[64:67],v[28:31]\n"
MF "v[28:31],a[212:215],a[104:107],v[28:31]\n"
MF "v[20:23],a[212:215],a[100:103],v[20:23]\n"
"s_waitcnt vmcnt(24) lgkmcnt(0)\n"
MF "v[20:23],a[208:211],a[60:63],v[20:23]\n"
"s_barrier\n"
MF "v[56:59],a[216:219],a[60:63],v[56:59]\n"
"s_add_u32 m0,s62,0\n"
"s_add_u32 s82,s66,6291712\n"
TR "a[208:209],v0 offset:16384\n"
MF "v[56:59],a[220:223],a[100:103],v[56:59]\n"
"s_sub_u32 s15,s82,7864320\n"
"s_min_u32 s82,s82,s15\n"
TR "a[210:211],v0 offset:16896\n"
MF "v[64:67],a[220:223],a[104:107],v[64:67]\n"
LD "v6,s[28:31],s82 offen lds\n"
MF "v[64:67],a[216:219],a[64:67],v[64:67]\n"
TR "a[212:213],v0 offset:20480\n"
MF "v[72:75],a[216:219],a[68:71],v[72:75]\n"
TR "a[214:215],v0 offset:20992\n"
MF "v[72:75],a[220:223],a[108:111],v[72:75]\n"
MF "v[80:83],a[220:223],a[112:115],v[80:83]\n"
LD "v11,s[28:31],s82 offen offset:1024 lds\n"
MF "v[80:83],a[216:219],a[72:75],v[80:83]\n"
MF "v[88:91],a[216:219],a[76:79],v[88:91]\n"
MF "v[88:91],a[220:223],a[116:119],v[88:91]\n"
MF "v[92:95],a[204:207],a[116:119],v[92:95]\n"
TR "a[216:217],v0 offset:16392\n"
MF "v[92:95],a[200:203],a[76:79],v[92:95]\n"
TR "a[218:219],v0 offset:16904\n"
MF "v[84:87],a[200:203],a[72:75],v[84:87]\n"
LD "v12,s[28:31],s82 offen offset:2048 lds\n"
MF "v[84:87],a[204:207],a[112:115],v[84:87]\n"
TR "a[220:221],v0 offset:20488\n"
MF "v[76:79],a[204:207],a[108:111],v[76:79]\n"
TR "a[222:223],v0 offset:21000\n"
MF "v[76:79],a[200:203],a[68:71],v[76:79]\n"
MF "v[68:71],a[200:203],a[64:67],v[68:71]\n"
LD "v13,s[28:31],s82 offen offset:3072 lds\n"
MF "v[68:71],a[204:207],a[104:107],v[68:71]\n"
MF "v[60:63],a[204:207],a[100:103],v[60:63]\n"
MF "v[60:63],a[200:203],a[60:63],v[60:63]\n"
"s_add_u32 s14,s81,1152\n"
"s_sub_u32 s15,s14,1280\n"
"s_waitcnt lgkmcnt(4)\n"
MF "v[96:99],a[208:211],a[60:63],v[96:99]\n"
"s_min_u32 s14,s14,s15\n"
"s_add_u32 s14,s14,s68\n"
TR "a[200:201],v1 offset:16384\n"
MF "v[96:99],a[212:215],a[100:103],v[96:99]\n"
"s_add_u32 s84,s14,0\n"
"s_add_u32 m0,s63,0\n"
TR "a[202:203],v1 offset:16896\n"
MF "v[104:107],a[212:215],a[104:107],v[104:107]\n"
LD "v4,s[24:27],s84 offen lds\n"
MF "v[104:107],a[208:211],a[64:67],v[104:107]\n"
TR "a[204:205],v1 offset:20480\n"
MF "v[112:115],a[208:211],a[68:71],v[112:115]\n"
TR "a[206:207],v1 offset:20992\n"
MF "v[112:115],a[212:215],a[108:111],v[112:115]\n"
"s_add_u32 s84,s14,9216\n"
MF "v[120:123],a[212:215],a[112:115],v[120:123]\n"
LD "v5,s[24:27],s84 offen offset:1024 lds\n"
MF "v[120:123],a[208:211],a[72:75],v[120:123]\n"
MF "v[128:131],a[208:211],a[76:79],v[128:131]\n"
MF "v[128:131],a[212:215],a[116:119],v[128:131]\n"
"s_waitcnt lgkmcnt(4)\n"
MF "v[132:135],a[220:223],a[116:119],v[132:135]\n"
TR "a[208:209],v1 offset:16392\n"
MF "v[132:135],a[216:219],a[76:79],v[132:135]\n"
"s_add_u32 s84,s14,18432\n"
TR "a[210:211],v1 offset:16904\n"
MF "v[124:127],a[216:219],a[72:75],v[124:127]\n"
LD "v4,s[24:27],s84 offen offset:2048 lds\n"
MF "v[124:127],a[220:223],a[112:115],v[124:127]\n"
TR "a[212:213],v1 offset:20488\n"
MF "v[116:119],a[220:223],a[108:111],v[116:119]\n"
TR "a[214:215],v1 offset:21000\n"
MF "v[116:119],a[216:219],a[68:71],v[116:119]\n"
MF "v[108:111],a[216:219],a[64:67],v[108:111]\n"
MF "v[108:111],a[220:223],a[104:107],v[108:111]\n"
MF "v[100:103],a[220:223],a[100:103],v[100:103]\n"
"s_waitcnt vmcnt(24) lgkmcnt(0)\n"
MF "v[100:103],a[216:219],a[60:63],v[100:103]\n"
"s_barrier\n"
MF "v[136:139],a[200:203],a[60:63],v[136:139]\n"
TR "a[216:217],v0 offset:32768\n"
MF "v[136:139],a[204:207],a[100:103],v[136:139]\n"
"s_add_u32 m0,s62,16384\n"
TR "a[218:219],v0 offset:33280\n"
MF "v[144:147],a[204:207],a[104:107],v[144:147]\n"
"s_add_u32 s82,s66,7078144\n"
"ds_read_b128 a[140:143],v2 offset:20480\n"
MF "v[144:147],a[200:203],a[64:67],v[144:147]\n"
"s_sub_u32 s15,s82,7864320\n"
TR "a[220:221],v0 offset:36864\n"
MF "v[152:155],a[200:203],a[68:71],v[152:155]\n"
"s_min_u32 s82,s82,s15\n"
TR "a[222:223],v0 offset:37376\n"
MF "v[152:155],a[204:207],a[108:111],v[152:155]\n"
LD "v6,s[28:31],s82 offen lds\n"
MF "v[160:163],a[204:207],a[112:115],v[160:163]\n"
"ds_read_b128 a[180:183],v3 offset:20480\n"
MF "v[160:163],a[200:203],a[72:75],v[160:163]\n"
LD "v11,s[28:31],s82 offen offset:1024 lds\n"
MF "v[168:171],a[200:203],a[76:79],v[168:171]\n"
MF "v[168:171],a[204:207],a[116:119],v[168:171]\n"
LD "v12,s[28:31],s82 offen offset:2048 lds\n"
MF "v[172:175],a[212:215],a[116:119],v[172:175]\n"
TR "a[200:201],v0 offset:32776\n"
MF "v[172:175],a[208:211],a[76:79],v[172:175]\n"
TR "a[202:203],v0 offset:33288\n"
MF "v[164:167],a[208:211],a[72:75],v[164:167]\n"
"ds_read_b128 a[144:147],v2 offset:22528\n"
MF "v[164:167],a[212:215],a[112:115],v[164:167]\n"
TR "a[204:205],v0 offset:36872\n"
MF "v[156:159],a[212:215],a[108:111],v[156:159]\n"
TR "a[206:207],v0 offset:37384\n"
MF "v[156:159],a[208:211],a[68:71],v[156:159]\n"
MF "v[148:151],a[208:211],a[64:67],v[148:151]\n"
"ds_read_b128 a[184:187],v3 offset:22528\n"
MF "v[148:151],a[212:215],a[104:107],v[148:151]\n"
MF "v[140:143],a[212:215],a[100:103],v[140:143]\n"
LD "v13,s[28:31],s82 offen offset:3072 lds\n"
MF "v[140:143],a[208:211],a[60:63],v[140:143]\n"
"s_waitcnt lgkmcnt(7)\n"
MF "v[176:179],a[216:219],a[60:63],v[176:179]\n"
TR "a[208:209],v1 offset:32768\n"
MF "v[176:179],a[220:223],a[100:103],v[176:179]\n"
TR "a[210:211],v1 offset:33280\n"
MF "v[184:187],a[220:223],a[104:107],v[184:187]\n"
"ds_read_b128 a[148:151],v2 offset:24576\n"
MF "v[184:187],a[216:219],a[64:67],v[184:187]\n"
"s_add_u32 s84,s14,30720\n"
TR "a[212:213],v1 offset:36864\n"
MF "v[192:195],a[216:219],a[68:71],v[192:195]\n"
"s_add_u32 m0,s63,3072\n"
TR "a[214:215],v1 offset:37376\n"
MF "v[192:195],a[220:223],a[108:111],v[192:195]\n"
LD "v5,s[24:27],s84 offen lds\n"
MF "v[200:203],a[220:223],a[112:115],v[200:203]\n"
"ds_read_b128 a[188:191],v3 offset:24576\n"
MF "v[200:203],a[216:219],a[72:75],v[200:203]\n"
MF "v[208:211],a[216:219],a[76:79],v[208:211]\n"
"s_add_u32 s84,s14,39936\n"
MF "v[208:211],a[220:223],a[116:119],v[208:211]\n"
"s_waitcnt lgkmcnt(7)\n"
LD "v4,s[24:27],s84 offen offset:1024 lds\n"
MF "v[212:215],a[204:207],a[116:119],v[212:215]\n"
TR "a[216:217],v1 offset:32776\n"
MF "v[212:215],a[200:203],a[76:79],v[212:215]\n"
TR "a[218:219],v1 offset:33288\n"
MF "v[204:207],a[200:203],a[72:75],v[204:207]\n"
"ds_read_b128 a[152:155],v2 offset:26624\n"
MF "v[204:207],a[204:207],a[112:115],v[204:207]\n"
TR "a[220:221],v1 offset:36872\n"
MF "v[196:199],a[204:207],a[108:111],v[196:199]\n"
TR "a[222:223],v1 offset:37384\n"
MF "v[196:199],a[200:203],a[68:71],v[196:199]\n"
MF "v[188:191],a[200:203],a[64:67],v[188:191]\n"
"v_add_u32 v0,0xc000,v0\n"
MF "v[188:191],a[204:207],a[104:107],v[188:191]\n"
"v_add_u32 v1,0xc000,v1\n"
MF "v[180:183],a[204:207],a[100:103],v[180:183]\n"
"s_waitcnt vmcnt(26) lgkmcnt(0)\n"
".Lpgqj_a:\n"
"s_cbranch_execz .Lisr1\n"
".Lqj:\n"
MF "v[180:183],a[200:203],a[60:63],v[180:183]\n"
"s_barrier\n"
"s_getpc_b64 s[52:53]\n"
".Lctq:\n"
"s_sub_u32 s52,s52,.Lctq-.Lcode0\n"
"s_subb_u32 s53,s53,0\n"
"s_bfe_u32 s58,s23,0x30000\n"
"s_lshl_b32 s58,s58,2\n"
"s_add_u32 s58,s58,s56\n"
"s_lshl_b32 s58,s58,13\n"
"v_lshlrev_b32 v15,3,v9\n"
"v_add_u32 v15,s58,v15\n"
"v_min_u32 v15,.Lcode1-.Lcode0,v15\n"
"global_load_dword a249,v15,s[52:53]\n"
MF "v[216:219],a[208:211],a[60:63],v[216:219]\n"
TR "a[200:201],v0\n"
MF "v[216:219],a[212:215],a[100:103],v[216:219]\n"
"s_add_u32 m0,s62,32768\n"
TR "a[202:203],v0 offset:512\n"
MF "v[224:227],a[212:215],a[104:107],v[224:227]\n"
"s_add_u32 s82,s66,6291584\n"
"ds_read_b128 a[192:195],v3 offset:26624\n"
MF "v[224:227],a[208:211],a[64:67],v[224:227]\n"
"s_sub_u32 s15,s82,7864320\n"
TR "a[204:205],v0 offset:4096\n"
MF "v[232:235],a[208:211],a[68:71],v[232:235]\n"
"s_min_u32 s82,s82,s15\n"
TR "a[206:207],v0 offset:4608\n"
MF "v[232:235],a[212:215],a[108:111],v[232:235]\n"
LD "v6,s[28:31],s82 offen lds\n"
MF "v[240:243],a[212:215],a[112:115],v[240:243]\n"
"ds_read_b128 a[156:159],v2 offset:28672\n"
MF "v[240:243],a[208:211],a[72:75],v[240:243]\n"
"ds_read_b128 a[196:199],v3 offset:28672\n"
MF "v[248:251],a[208:211],a[76:79],v[248:251]\n"
MF "v[248:251],a[212:215],a[116:119],v[248:251]\n"
LD "v11,s[28:31],s82 offen offset:1024 lds\n"
MF "v[252:255],a[220:223],a[116:119],v[252:255]\n"
TR "a[208:209],v0 offset:8\n"
MF "v[252:255],a[216:219],a[76:79],v[252:255]\n"
TR "a[210:211],v0 offset:520\n"
MF "v[244:247],a[216:219],a[72:75],v[244:247]\n"
MF "v[244:247],a[220:223],a[112:115],v[244:247]\n"
TR "a[212:213],v0 offset:4104\n"
MF "v[236:239],a[220:223],a[108:111],v[236:239]\n"
TR "a[214:215],v0 offset:4616\n"
MF "v[236:239],a[216:219],a[68:71],v[236:239]\n"
MF "v[228:231],a[216:219],a[64:67],v[228:231]\n"
MF "v[228:231],a[220:223],a[104:107],v[228:231]\n"
MF "v[220:223],a[220:223],a[100:103],v[220:223]\n"
LD "v12,s[28:31],s82 offen offset:2048 lds\n"
MF "v[220:223],a[216:219],a[60:63],v[220:223]\n"
"s_waitcnt lgkmcnt(4)\n"
MF "v[16:19],a[200:203],a[140:143],v[16:19]\n"
TR "a[216:217],v1\n"
MF "v[16:19],a[204:207],a[180:183],v[16:19]\n"
TR "a[218:219],v1 offset:512\n"
MF "v[24:27],a[204:207],a[184:187],v[24:27]\n"
MF "v[24:27],a[200:203],a[144:147],v[24:27]\n"
TR "a[220:221],v1 offset:4096\n"
MF "v[32:35],a[200:203],a[148:151],v[32:35]\n"
TR "a[222:223],v1 offset:4608\n"
MF "v[32:35],a[204:207],a[188:191],v[32:35]\n"
MF "v[40:43],a[204:207],a[192:195],v[40:43]\n"
MF "v[40:43],a[200:203],a[152:155],v[40:43]\n"
LD "v13,s[28:31],s82 offen offset:3072 lds\n"
MF "v[48:51],a[200:203],a[156:159],v[48:51]\n"
"s_memtime s[52:53]\n"
"s_memrealtime s[100:101]\n"
MF "v[48:51],a[204:207],a[196:199],v[48:51]\n"
"s_waitcnt lgkmcnt(4)\n"
MF "v[52:55],a[212:215],a[196:199],v[52:55]\n"
TR "a[200:201],v1 offset:8\n"
MF "v[52:55],a[208:211],a[156:159],v[52:55]\n"
TR "a[202:203],v1 offset:520\n"
MF "v[44:47],a[208:211],a[152:155],v[44:47]\n"
MF "v[44:47],a[212:215],a[192:195],v[44:47]\n"
TR "a[204:205],v1 offset:4104\n"
MF "v[36:39],a[212:215],a[188:191],v[36:39]\n"
TR "a[206:207],v1 offset:4616\n"
MF "v[36:39],a[208:211],a[148:151],v[36:39]\n"
MF "v[28:31],a[208:211],a[144:147],v[28:31]\n"
MF "v[28:31],a[212:215],a[184:187],v[28:31]\n"
MF "v[20:23],a[212:215],a[180:183],v[20:23]\n"
"s_waitcnt vmcnt(25) lgkmcnt(0)\n"
MF "v[20:23],a[208:211],a[140:143],v[20:23]\n"
"s_barrier\n"
MF "v[56:59],a[216:219],a[140:143],v[56:59]\n"
"s_add_u32 m0,s62,49152\n"
"s_add_u32 s82,s66,7078016\n"
TR "a[208:209],v0 offset:16384\n"
MF "v[56:59],a[220:223],a[180:183],v[56:59]\n"
"s_sub_u32 s15,s82,7864320\n"
"s_min_u32 s82,s82,s15\n"
TR "a[210:211],v0 offset:16896\n"
MF "v[64:67],a[220:223],a[184:187],v[64:67]\n"
LD "v6,s[28:31],s82 offen lds\n"
MF "v[64:67],a[216:219],a[144:147],v[64:67]\n"
"s_sub_u32 s53,s52,s80\n"
TR "a[212:213],v0 offset:20480\n"
"v_lshrrev_b32 v15,8,v9\n"
MF "v[72:75],a[216:219],a[148:151],v[72:75]\n"
TR "a[214:215],v0 offset:20992\n"
"v_mul_u32_u24 v15,48960,v15\n"
MF "v[72:75],a[220:223],a[188:191],v[72:75]\n"
"s_lshr_b32 s7,s56,1\n"
"v_add_u32 v15,v15,v9\n"
MF "v[80:83],a[220:223],a[192:195],v[80:83]\n"
"s_and_b32 s8,s56,1\n"
MF "v[80:83],a[216:219],a[152:155],v[80:83]\n"
"s_sub_u32 s7,s7,s8\n"
MF "v[88:91],a[216:219],a[156:159],v[88:91]\n"
"s_mul_i32 s7,s7,245760\n"
MF "v[88:91],a[220:223],a[196:199],v[88:91]\n"
LD "v11,s[28:31],s82 offen offset:1024 lds\n"
MF "v[92:95],a[204:207],a[196:199],v[92:95]\n"
"s_add_u32 s7,s7,s89\n"
TR "a[216:217],v0 offset:16392\n"
MF "v[92:95],a[200:203],a[156:159],v[92:95]\n"
"s_cmp_gt_u32 s53,24100\n"
"s_cselect_b32 s101,1,0\n"
TR "a[218:219],v0 offset:16904\n"
MF "v[84:87],a[200:203],a[152:155],v[84:87]\n"
"s_cmp_eq_u32 s87,2\n"
"s_cselect_b32 s87,s101,s87\n"
MF "v[84:87],a[204:207],a[192:195],v[84:87]\n"
TR "a[220:221],v0 offset:20488\n"
MF "v[76:79],a[204:207],a[188:191],v[76:79]\n"
TR "a[222:223],v0 offset:21000\n"
MF "v[76:79],a[200:203],a[148:151],v[76:79]\n"
"s_cmp_gt_u32 s87,s57\n"
"s_cselect_b32 s97,130,0\n"
"s_cselect_b32 s95,0,0x47800000\n"
MF "v[68:71],a[200:203],a[144:147],v[68:71]\n"
"s_mov_b32 s21,s100\n"
MF "v[68:71],a[204:207],a[184:187],v[68:71]\n"
"s_mov_b32 s77,s95\n"
MF "v[60:63],a[204:207],a[180:183],v[60:63]\n"
LD "v12,s[28:31],s82 offen offset:2048 lds\n"
MF "v[60:63],a[200:203],a[140:143],v[60:63]\n"
"s_waitcnt lgkmcnt(4)\n"
MF "v[96:99],a[208:211],a[140:143],v[96:99]\n"
TR "a[200:201],v1 offset:16384\n"
MF "v[96:99],a[212:215],a[180:183],v[96:99]\n"
".Lpg7a:\n"
"s_cbranch_execz .Lisr1\n"
TR "a[202:203],v1 offset:16896\n"
MF "v[104:107],a[212:215],a[184:187],v[104:107]\n"
MF "v[104:107],a[208:211],a[144:147],v[104:107]\n"
TR "a[204:205],v1 offset:20480\n"
MF "v[112:115],a[208:211],a[148:151],v[112:115]\n"
TR "a[206:207],v1 offset:20992\n"
MF "v[112:115],a[212:215],a[188:191],v[112:115]\n"
MF "v[120:123],a[212:215],a[192:195],v[120:123]\n"
MF "v[120:123],a[208:211],a[152:155],v[120:123]\n"
LD "v13,s[28:31],s82 offen offset:3072 lds\n"
MF "v[128:131],a[208:211],a[156:159],v[128:131]\n"
MF "v[128:131],a[212:215],a[196:199],v[128:131]\n"
"s_waitcnt lgkmcnt(4)\n"
MF "v[132:135],a[220:223],a[196:199],v[132:135]\n"
TR "a[208:209],v1 offset:16392\n"
MF "v[132:135],a[216:219],a[156:159],v[132:135]\n"
TR "a[210:211],v1 offset:16904\n"
MF "v[124:127],a[216:219],a[152:155],v[124:127]\n"
MF "v[124:127],a[220:223],a[192:195],v[124:127]\n"
TR "a[212:213],v1 offset:20488\n"
MF "v[116:119],a[220:223],a[188:191],v[116:119]\n"
TR "a[214:215],v1 offset:21000\n"
MF "v[116:119],a[216:219],a[148:151],v[116:119]\n"
MF "v[108:111],a[216:219],a[144:147],v[108:111]\n"
MF "v[108:111],a[220:223],a[184:187],v[108:111]\n"
MF "v[100:103],a[220:223],a[180:183],v[100:103]\n"
"s_waitcnt vmcnt(22) lgkmcnt(0)\n"
MF "v[100:103],a[216:219],a[140:143],v[100:103]\n"
"s_barrier\n"
"s_cmp_eq_u32 s57,1\n"
"s_cbranch_scc0 .Lhs15\n"
".Lack_q:\n"
"v_cmp_ne_u32 vcc,s76,v10\n"
"s_cbranch_vccnz .Lackspin_q\n"
".Lackok_q:\n"
".Lhs15:\n"
MF "v[136:139],a[200:203],a[140:143],v[136:139]\n"
"s_and_b32 s58,s56,1\n"
TR "a[216:217],v0 offset:32768\n"
MF "v[136:139],a[204:207],a[180:183],v[136:139]\n"
"s_add_u32 m0,s62,65536\n"
TR "a[218:219],v0 offset:33280\n"
MF "v[144:147],a[204:207],a[184:187],v[144:147]\n"
"s_add_u32 s82,s66,6291456\n"
"ds_read_b128 a[60:63],v2 offset:40960\n"
MF "v[144:147],a[200:203],a[144:147],v[144:147]\n"
"s_sub_u32 s15,s82,7864320\n"
TR "a[220:221],v0 offset:36864\n"
MF "v[152:155],a[200:203],a[148:151],v[152:155]\n"
"s_min_u32 s82,s82,s15\n"
TR "a[222:223],v0 offset:37376\n"
MF "v[152:155],a[204:207],a[188:191],v[152:155]\n"
LD "v6,s[28:31],s82 offen lds\n"
MF "v[160:163],a[204:207],a[192:195],v[160:163]\n"
"s_mul_i32 s59,s58,3072\n"
"ds_read_b128 a[100:103],v3 offset:40960\n"
MF "v[160:163],a[200:203],a[152:155],v[160:163]\n"
"s_add_u32 s16,s70,s59\n"
MF "v[168:171],a[200:203],a[156:159],v[168:171]\n"
"s_mul_i32 s59,s58,6144\n"
MF "v[168:171],a[204:207],a[196:199],v[168:171]\n"
LD "v11,s[28:31],s82 offen offset:1024 lds\n"
MF "v[172:175],a[212:215],a[196:199],v[172:175]\n"
"s_add_u32 s18,s70,s59\n"
TR "a[200:201],v0 offset:32776\n"
MF "v[172:175],a[208:211],a[156:159],v[172:175]\n"
"s_add_u32 s17,s16,196608\n"
TR "a[202:203],v0 offset:33288\n"
MF "v[164:167],a[208:211],a[152:155],v[164:167]\n"
"s_add_u32 s19,s18,196608\n"
"ds_read_b128 a[64:67],v2 offset:43008\n"
MF "v[164:167],a[212:215],a[192:195],v[164:167]\n"
"s_add_u32 s20,s70,196608\n"
TR "a[204:205],v0 offset:36872\n"
MF "v[156:159],a[212:215],a[188:191],v[156:159]\n"
"s_bfe_u32 s58,s23,0x40008\n"
TR "a[206:207],v0 offset:37384\n"
MF "v[156:159],a[208:211],a[148:151],v[156:159]\n"
"s_lshr_b32 s59,s56,1\n"
MF "v[148:151],a[208:211],a[144:147],v[148:151]\n"
"s_mul_i32 s59,s59,5\n"
"ds_read_b128 a[104:107],v3 offset:43008\n"
MF "v[148:151],a[212:215],a[184:187],v[148:151]\n"
"s_add_u32 s58,s58,s59\n"
MF "v[140:143],a[212:215],a[180:183],v[140:143]\n"
LD "v12,s[28:31],s82 offen offset:2048 lds\n"
MF "v[140:143],a[208:211],a[140:143],v[140:143]\n"
"s_waitcnt lgkmcnt(7)\n"
MF "v[176:179],a[216:219],a[140:143],v[176:179]\n"
"s_and_b32 s58,s58,15\n"
TR "a[208:209],v1 offset:32768\n"
MF "v[176:179],a[220:223],a[180:183],v[176:179]\n"
"s_mul_i32 s58,s58,12288\n"
TR "a[210:211],v1 offset:33280\n"
MF "v[184:187],a[220:223],a[184:187],v[184:187]\n"
"s_add_u32 s72,s16,s58\n"
"ds_read_b128 a[68:71],v2 offset:45056\n"
MF "v[184:187],a[216:219],a[144:147],v[184:187]\n"
"s_add_u32 s73,s18,s58\n"
TR "a[212:213],v1 offset:36864\n"
MF "v[192:195],a[216:219],a[148:151],v[192:195]\n"
"s_add_u32 s74,s70,s58\n"
TR "a[214:215],v1 offset:37376\n"
MF "v[192:195],a[220:223],a[188:191],v[192:195]\n"
"s_add_u32 s74,s74,12288\n"
MF "v[200:203],a[220:223],a[192:195],v[200:203]\n"
"s_cmp_eq_u32 s74,s20\n"
"s_cselect_b32 s74,s70,s74\n"
"ds_read_b128 a[108:111],v3 offset:45056\n"
MF "v[200:203],a[216:219],a[152:155],v[200:203]\n"
LD "v13,s[28:31],s82 offen offset:3072 lds\n"
MF "v[208:211],a[216:219],a[156:159],v[208:211]\n"
"s_lshr_b32 s58,s56,1\n"
MF "v[208:211],a[220:223],a[196:199],v[208:211]\n"
"s_waitcnt lgkmcnt(7)\n"
MF "v[212:215],a[204:207],a[196:199],v[212:215]\n"
"s_mul_i32 s58,s58,6144\n"
TR "a[216:217],v1 offset:32776\n"
MF "v[212:215],a[200:203],a[156:159],v[212:215]\n"
"s_mul_i32 s64,s56,3072\n"
TR "a[218:219],v1 offset:33288\n"
MF "v[204:207],a[200:203],a[152:155],v[204:207]\n"
"s_add_u32 s64,s64,s58\n"
"ds_read_b128 a[72:75],v2 offset:47104\n"
MF "v[204:207],a[204:207],a[192:195],v[204:207]\n"
"s_mul_i32 s65,s56,6144\n"
TR "a[220:221],v1 offset:36872\n"
MF "v[196:199],a[204:207],a[188:191],v[196:199]\n"
"s_sub_u32 s58,s71,s70\n"
TR "a[222:223],v1 offset:37384\n"
MF "v[196:199],a[200:203],a[148:151],v[196:199]\n"
"s_add_u32 s96,s74,s58\n"
MF "v[188:191],a[200:203],a[144:147],v[188:191]\n"
"s_sub_u32 s58,s16,s70\n"
"v_add_u32 v0,0xffff4000,v0\n"
MF "v[188:191],a[204:207],a[184:187],v[188:191]\n"
"s_add_u32 s96,s96,s58\n"
"v_add_u32 v1,0xffff4000,v1\n"
MF "v[180:183],a[204:207],a[180:183],v[180:183]\n"
"s_waitcnt vmcnt(13) lgkmcnt(0)\n"
MF "v[180:183],a[200:203],a[140:143],v[180:183]\n"
"s_barrier\n"
MF "v[216:219],a[208:211],a[140:143],v[216:219]\n"
"s_max_u32 s60,s70,s71\n"
TR "a[200:201],v1 offset:8\n"
MF "v[216:219],a[212:215],a[180:183],v[216:219]\n"
"s_add_u32 m0,s62,81920\n"
TR "a[202:203],v1 offset:520\n"
"v_mov_b32 v14,s12\n"
MF "v[224:227],a[212:215],a[184:187],v[224:227]\n"
"s_add_u32 s82,s66,7077888\n"
"ds_read_b128 a[112:115],v3 offset:47104\n"
"v_mul_f32 v14,0xbdd53b95,v14\n"
MF "v[224:227],a[208:211],a[144:147],v[224:227]\n"
"s_sub_u32 s15,s82,7864320\n"
TR "a[204:205],v1 offset:4104\n"
MF "v[232:235],a[208:211],a[148:151],v[232:235]\n"
"s_min_u32 s82,s82,s15\n"
TR "a[206:207],v1 offset:4616\n"
MF "v[232:235],a[212:215],a[188:191],v[232:235]\n"
LD "v6,s[28:31],s82 offen lds\n"
MF "v[240:243],a[212:215],a[192:195],v[240:243]\n"
"s_add_u32 s34,s60,196608\n"
"ds_read_b128 a[76:79],v2 offset:49152\n"
MF "v[240:243],a[208:211],a[152:155],v[240:243]\n"
"s_add_u32 s38,s60,196608\n"
"ds_read_b128 a[116:119],v3 offset:49152\n"
MF "v[248:251],a[208:211],a[156:159],v[248:251]\n"
MF "v[248:251],a[212:215],a[196:199],v[248:251]\n"
LD "v11,s[28:31],s82 offen offset:1024 lds\n"
MF "v[220:223],a[216:219],a[140:143],v[220:223]\n"
TR "a[208:209],v1 offset:16392\n"
MF "v[220:223],a[220:223],a[180:183],v[220:223]\n"
TR "a[210:211],v1 offset:16904\n"
MF "v[228:231],a[220:223],a[184:187],v[228:231]\n"
"ds_read_b128 a[140:143],v2\n"
MF "v[228:231],a[216:219],a[144:147],v[228:231]\n"
TR "a[212:213],v1 offset:20488\n"
MF "v[236:239],a[216:219],a[148:151],v[236:239]\n"
TR "a[214:215],v1 offset:21000\n"
MF "v[236:239],a[220:223],a[188:191],v[236:239]\n"
"ds_read_b128 a[180:183],v3\n"
MF "v[244:247],a[220:223],a[192:195],v[244:247]\n"
"ds_read_b128 a[144:147],v2 offset:2048\n"
MF "v[244:247],a[216:219],a[152:155],v[244:247]\n"
"ds_read_b128 a[184:187],v3 offset:2048\n"
MF "v[252:255],a[216:219],a[156:159],v[252:255]\n"
"ds_read_b128 a[148:151],v2 offset:4096\n"
MF "v[252:255],a[220:223],a[196:199],v[252:255]\n"
"s_waitcnt lgkmcnt(9)\n"
"ds_read_b128 a[188:191],v3 offset:4096\n"
MF "v[220:223],a[200:203],a[60:63],v[220:223]\n"
TR "a[216:217],v1\n"
MF "v[220:223],a[204:207],a[100:103],v[220:223]\n"
TR "a[218:219],v1 offset:512\n"
MF "v[228:231],a[204:207],a[104:107],v[228:231]\n"
"ds_read_b128 a[152:155],v2 offset:6144\n"
MF "v[228:231],a[200:203],a[64:67],v[228:231]\n"
TR "a[220:221],v1 offset:4096\n"
MF "v[236:239],a[200:203],a[68:71],v[236:239]\n"
TR "a[222:223],v1 offset:4608\n"
MF "v[236:239],a[204:207],a[108:111],v[236:239]\n"
"ds_read_b128 a[192:195],v3 offset:6144\n"
MF "v[244:247],a[204:207],a[112:115],v[244:247]\n"
"ds_read_b128 a[156:159],v2 offset:8192\n"
MF "v[244:247],a[200:203],a[72:75],v[244:247]\n"
"ds_read_b128 a[196:199],v3 offset:8192\n"
MF "v[252:255],a[200:203],a[76:79],v[252:255]\n"
LD "v12,s[28:31],s82 offen offset:2048 lds\n"
MF "v[252:255],a[204:207],a[116:119],v[252:255]\n"
"s_waitcnt lgkmcnt(8)\n"
LD "v13,s[28:31],s82 offen offset:3072 lds\n"
MF "v[220:223],a[208:211],a[140:143],v[220:223]\n"
TR "a[200:201],v1 offset:16384\n"
MF "v[220:223],a[212:215],a[180:183],v[220:223]\n"
TR "a[202:203],v1 offset:16896\n"
MF "v[228:231],a[212:215],a[184:187],v[228:231]\n"
MF "v[228:231],a[208:211],a[144:147],v[228:231]\n"
TR "a[204:205],v1 offset:20480\n"
MF "v[236:239],a[208:211],a[148:151],v[236:239]\n"
TR "a[206:207],v1 offset:20992\n"
MF "v[236:239],a[212:215],a[188:191],v[236:239]\n"
"s_waitcnt lgkmcnt(4)\n"
MF "v[244:247],a[212:215],a[192:195],v[244:247]\n"
MF "v[244:247],a[208:211],a[152:155],v[244:247]\n"
MF "v[252:255],a[208:211],a[156:159],v[252:255]\n"
MF "v[252:255],a[212:215],a[196:199],v[252:255]\n"
"s_waitcnt lgkmcnt(0)\n"
"s_cmp_eq_u32 s69,98\n"
"s_cbranch_scc1 .Lqy\n"
".Lqf:\n"
MF "v[216:219],a[216:219],a[60:63],v[216:219]\n"
TR "a[208:209],v0 offset:8\n"
MF "v[216:219],a[220:223],a[100:103],v[216:219]\n"
TR "a[210:211],v0 offset:520\n"
MF "v[224:227],a[220:223],a[104:107],v[224:227]\n"
"buffer_load_dword a224,v15,s[36:39],s70 offen offset:2688 nt\n"
MF "v[224:227],a[216:219],a[64:67],v[224:227]\n"
TR "a[212:213],v0 offset:4104\n"
MF "v[232:235],a[216:219],a[68:71],v[232:235]\n"
TR "a[214:215],v0 offset:4616\n"
MF "v[232:235],a[220:223],a[108:111],v[232:235]\n"
"buffer_load_dword a225,v15,s[36:39],s71 offen offset:2688 nt\n"
MF "v[240:243],a[220:223],a[112:115],v[240:243]\n"
MF "v[240:243],a[216:219],a[72:75],v[240:243]\n"
"buffer_load_dword a226,v15,s[48:51],s7 offen offset:2688\n"
MF "v[248:251],a[216:219],a[76:79],v[248:251]\n"
MF "v[248:251],a[220:223],a[116:119],v[248:251]\n"
MF "v[248:251],a[200:203],a[156:159],v[248:251]\n"
TR "a[216:217],v0 offset:16392\n"
MF "v[248:251],a[204:207],a[196:199],v[248:251]\n"
TR "a[218:219],v0 offset:16904\n"
MF "v[240:243],a[204:207],a[192:195],v[240:243]\n"
MF "v[240:243],a[200:203],a[152:155],v[240:243]\n"
TR "a[220:221],v0 offset:20488\n"
MF "v[232:235],a[200:203],a[148:151],v[232:235]\n"
TR "a[222:223],v0 offset:21000\n"
MF "v[232:235],a[204:207],a[188:191],v[232:235]\n"
MF "v[224:227],a[204:207],a[184:187],v[224:227]\n"
MF "v[224:227],a[200:203],a[144:147],v[224:227]\n"
"v_fma_f32 v248,v248,s54,v14\n"
"v_fma_f32 v249,v249,s54,v14\n"
MF "v[216:219],a[200:203],a[140:143],v[216:219]\n"
"v_fma_f32 v250,v250,s54,v14\n"
"v_fma_f32 v251,v251,s54,v14\n"
MF "v[216:219],a[204:207],a[180:183],v[216:219]\n"
"s_waitcnt lgkmcnt(4)\n"
"v_fma_f32 v252,v252,s54,v14\n"
"v_fma_f32 v253,v253,s54,v14\n"
MF "v[180:183],a[208:211],a[60:63],v[180:183]\n"
TR "a[200:201],v0\n"
"v_fma_f32 v254,v254,s54,v14\n"
MF "v[180:183],a[212:215],a[100:103],v[180:183]\n"
TR "a[202:203],v0 offset:512\n"
"v_fma_f32 v255,v255,s54,v14\n"
MF "v[188:191],a[212:215],a[104:107],v[188:191]\n"
CV "v255,v254,v255\n"
CV "v254,v252,v253\n"
MF "v[188:191],a[208:211],a[64:67],v[188:191]\n"
TR "a[204:205],v0 offset:4096\n"
CV "v253,v250,v251\n"
MF "v[196:199],a[208:211],a[68:71],v[196:199]\n"
TR "a[206:207],v0 offset:4608\n"
CV "v252,v248,v249\n"
MF "v[196:199],a[212:215],a[108:111],v[196:199]\n"
"v_fma_f32 v240,v240,s54,v14\n"
"v_fma_f32 v241,v241,s54,v14\n"
MF "v[204:207],a[212:215],a[112:115],v[204:207]\n"
"v_fma_f32 v242,v242,s54,v14\n"
"v_fma_f32 v243,v243,s54,v14\n"
MF "v[204:207],a[208:211],a[72:75],v[204:207]\n"
"v_fma_f32 v244,v244,s54,v14\n"
"v_fma_f32 v245,v245,s54,v14\n"
MF "v[212:215],a[208:211],a[76:79],v[212:215]\n"
"v_fma_f32 v246,v246,s54,v14\n"
"v_fma_f32 v247,v247,s54,v14\n"
MF "v[212:215],a[212:215],a[116:119],v[212:215]\n"
"s_waitcnt lgkmcnt(4)\n"
CV "v251,v246,v247\n"
CV "v250,v244,v245\n"
MF "v[180:183],a[216:219],a[140:143],v[180:183]\n"
TR "a[208:209],v0 offset:16384\n"
CV "v249,v242,v243\n"
MF "v[180:183],a[220:223],a[180:183],v[180:183]\n"
TR "a[210:211],v0 offset:16896\n"
CV "v248,v240,v241\n"
MF "v[188:191],a[220:223],a[184:187],v[188:191]\n"
"v_fma_f32 v232,v232,s54,v14\n"
"v_fma_f32 v233,v233,s54,v14\n"
MF "v[188:191],a[216:219],a[144:147],v[188:191]\n"
TR "a[212:213],v0 offset:20480\n"
"v_fma_f32 v234,v234,s54,v14\n"
MF "v[196:199],a[216:219],a[148:151],v[196:199]\n"
TR "a[214:215],v0 offset:20992\n"
"v_fma_f32 v235,v235,s54,v14\n"
MF "v[196:199],a[220:223],a[188:191],v[196:199]\n"
"v_fma_f32 v236,v236,s54,v14\n"
"v_fma_f32 v237,v237,s54,v14\n"
MF "v[204:207],a[220:223],a[192:195],v[204:207]\n"
"v_fma_f32 v238,v238,s54,v14\n"
"v_fma_f32 v239,v239,s54,v14\n"
MF "v[204:207],a[216:219],a[152:155],v[204:207]\n"
CV "v247,v238,v239\n"
CV "v246,v236,v237\n"
MF "v[212:215],a[216:219],a[156:159],v[212:215]\n"
"s_waitcnt vmcnt(11) lgkmcnt(0)\n"
CV "v245,v234,v235\n"
CV "v244,v232,v233\n"
MF "v[212:215],a[220:223],a[196:199],v[212:215]\n"
"s_barrier\n"
MF "v[176:179],a[200:203],a[60:63],v[176:179]\n"
TR "a[216:217],v1 offset:32776\n"
"v_fma_f32 v224,v224,s54,v14\n"
MF "v[176:179],a[204:207],a[100:103],v[176:179]\n"
"s_add_u32 m0,s64,0\n"
TR "a[218:219],v1 offset:33288\n"
"v_fma_f32 v225,v225,s54,v14\n"
MF "v[184:187],a[204:207],a[104:107],v[184:187]\n"
LD "v7,s[32:35],s72 offen nt lds\n"
MF "v[184:187],a[200:203],a[64:67],v[184:187]\n"
TR "a[220:221],v1 offset:36872\n"
"v_fma_f32 v226,v226,s54,v14\n"
MF "v[192:195],a[200:203],a[68:71],v[192:195]\n"
TR "a[222:223],v1 offset:37384\n"
"v_fma_f32 v227,v227,s54,v14\n"
MF "v[192:195],a[204:207],a[108:111],v[192:195]\n"
"s_add_u32 m0,s64,896\n"
"v_fma_f32 v228,v228,s54,v14\n"
"v_fma_f32 v229,v229,s54,v14\n"
MF "v[200:203],a[204:207],a[112:115],v[200:203]\n"
LD "v7,s[32:35],s72 offen offset:128 nt lds\n"
MF "v[200:203],a[200:203],a[72:75],v[200:203]\n"
"v_fma_f32 v230,v230,s54,v14\n"
"v_fma_f32 v231,v231,s54,v14\n"
MF "v[208:211],a[200:203],a[76:79],v[208:211]\n"
CV "v243,v230,v231\n"
CV "v242,v228,v229\n"
MF "v[208:211],a[204:207],a[116:119],v[208:211]\n"
CV "v241,v226,v227\n"
CV "v240,v224,v225\n"
MF "v[208:211],a[208:211],a[156:159],v[208:211]\n"
TR "a[200:201],v1 offset:49160\n"
"v_fma_f32 v216,v216,s54,v14\n"
MF "v[208:211],a[212:215],a[196:199],v[208:211]\n"
"s_add_u32 m0,s64,1792\n"
TR "a[202:203],v1 offset:49672\n"
"v_fma_f32 v217,v217,s54,v14\n"
MF "v[200:203],a[212:215],a[192:195],v[200:203]\n"
LD "v7,s[32:35],s72 offen offset:256 nt lds\n"
MF "v[200:203],a[208:211],a[152:155],v[200:203]\n"
TR "a[204:205],v1 offset:53256\n"
"v_fma_f32 v218,v218,s54,v14\n"
MF "v[192:195],a[208:211],a[148:151],v[192:195]\n"
TR "a[206:207],v1 offset:53768\n"
"v_fma_f32 v219,v219,s54,v14\n"
MF "v[192:195],a[212:215],a[188:191],v[192:195]\n"
"v_fma_f32 v220,v220,s54,v14\n"
"v_fma_f32 v221,v221,s54,v14\n"
MF "v[184:187],a[212:215],a[184:187],v[184:187]\n"
"s_add_u32 s72,s72,6144\n"
"v_fma_f32 v222,v222,s54,v14\n"
"v_fma_f32 v223,v223,s54,v14\n"
MF "v[184:187],a[208:211],a[144:147],v[184:187]\n"
"s_add_u32 m0,s64,6144\n"
CV "v239,v222,v223\n"
CV "v238,v220,v221\n"
MF "v[176:179],a[208:211],a[140:143],v[176:179]\n"
LD "v7,s[32:35],s72 offen nt lds\n"
MF "v[176:179],a[212:215],a[180:183],v[176:179]\n"
"s_waitcnt lgkmcnt(4)\n"
CV "v237,v218,v219\n"
CV "v236,v216,v217\n"
MF "v[140:143],a[216:219],a[60:63],v[140:143]\n"
TR "a[208:209],v1 offset:32768\n"
"v_fma_f32 v208,v208,s54,v14\n"
MF "v[140:143],a[220:223],a[100:103],v[140:143]\n"
TR "a[210:211],v1 offset:33280\n"
"v_fma_f32 v209,v209,s54,v14\n"
MF "v[148:151],a[220:223],a[104:107],v[148:151]\n"
"v_fma_f32 v210,v210,s54,v14\n"
"v_fma_f32 v211,v211,s54,v14\n"
MF "v[148:151],a[216:219],a[64:67],v[148:151]\n"
TR "a[212:213],v1 offset:36864\n"
"v_fma_f32 v212,v212,s54,v14\n"
MF "v[156:159],a[216:219],a[68:71],v[156:159]\n"
"s_add_u32 m0,s64,7040\n"
TR "a[214:215],v1 offset:37376\n"
"v_fma_f32 v213,v213,s54,v14\n"
MF "v[156:159],a[220:223],a[108:111],v[156:159]\n"
LD "v7,s[32:35],s72 offen offset:128 nt lds\n"
MF "v[164:167],a[220:223],a[112:115],v[164:167]\n"
"v_fma_f32 v214,v214,s54,v14\n"
"v_fma_f32 v215,v215,s54,v14\n"
MF "v[164:167],a[216:219],a[72:75],v[164:167]\n"
CV "v235,v214,v215\n"
CV "v234,v212,v213\n"
MF "v[172:175],a[216:219],a[76:79],v[172:175]\n"
"s_add_u32 m0,s64,7936\n"
CV "v233,v210,v211\n"
CV "v232,v208,v209\n"
MF "v[172:175],a[220:223],a[116:119],v[172:175]\n"
"s_waitcnt lgkmcnt(4)\n"
LD "v7,s[32:35],s72 offen offset:256 nt lds\n"
MF "v[140:143],a[200:203],a[140:143],v[140:143]\n"
TR "a[216:217],v1 offset:49152\n"
"v_fma_f32 v200,v200,s54,v14\n"
MF "v[140:143],a[204:207],a[180:183],v[140:143]\n"
TR "a[218:219],v1 offset:49664\n"
"v_fma_f32 v201,v201,s54,v14\n"
MF "v[148:151],a[204:207],a[184:187],v[148:151]\n"
"v_fma_f32 v202,v202,s54,v14\n"
"v_fma_f32 v203,v203,s54,v14\n"
MF "v[148:151],a[200:203],a[144:147],v[148:151]\n"
TR "a[220:221],v1 offset:53248\n"
"v_fma_f32 v204,v204,s54,v14\n"
MF "v[156:159],a[200:203],a[148:151],v[156:159]\n"
TR "a[222:223],v1 offset:53760\n"
"v_fma_f32 v205,v205,s54,v14\n"
MF "v[156:159],a[204:207],a[188:191],v[156:159]\n"
"v_fma_f32 v206,v206,s54,v14\n"
"v_fma_f32 v207,v207,s54,v14\n"
MF "v[164:167],a[204:207],a[192:195],v[164:167]\n"
CV "v231,v206,v207\n"
CV "v230,v204,v205\n"
MF "v[164:167],a[200:203],a[152:155],v[164:167]\n"
CV "v229,v202,v203\n"
CV "v228,v200,v201\n"
MF "v[172:175],a[200:203],a[156:159],v[172:175]\n"
"v_fma_f32 v192,v192,s54,v14\n"
"v_fma_f32 v193,v193,s54,v14\n"
MF "v[172:175],a[204:207],a[196:199],v[172:175]\n"
"s_waitcnt lgkmcnt(0)\n"
"v_fma_f32 v194,v194,s54,v14\n"
"v_fma_f32 v195,v195,s54,v14\n"
MF "v[136:139],a[208:211],a[60:63],v[136:139]\n"
TR "a[200:201],v0 offset:32776\n"
"v_fma_f32 v196,v196,s54,v14\n"
MF "v[136:139],a[212:215],a[100:103],v[136:139]\n"
TR "a[202:203],v0 offset:33288\n"
"v_fma_f32 v197,v197,s54,v14\n"
MF "v[144:147],a[212:215],a[104:107],v[144:147]\n"
"v_fma_f32 v198,v198,s54,v14\n"
"v_fma_f32 v199,v199,s54,v14\n"
MF "v[144:147],a[208:211],a[64:67],v[144:147]\n"
TR "a[204:205],v0 offset:36872\n"
CV "v227,v198,v199\n"
MF "v[152:155],a[208:211],a[68:71],v[152:155]\n"
TR "a[206:207],v0 offset:37384\n"
CV "v226,v196,v197\n"
MF "v[152:155],a[212:215],a[108:111],v[152:155]\n"
CV "v225,v194,v195\n"
CV "v224,v192,v193\n"
MF "v[160:163],a[212:215],a[112:115],v[160:163]\n"
"v_fma_f32 v184,v184,s54,v14\n"
"v_fma_f32 v185,v185,s54,v14\n"
MF "v[160:163],a[208:211],a[72:75],v[160:163]\n"
"v_fma_f32 v186,v186,s54,v14\n"
"v_fma_f32 v187,v187,s54,v14\n"
MF "v[168:171],a[208:211],a[76:79],v[168:171]\n"
"v_fma_f32 v188,v188,s54,v14\n"
"v_fma_f32 v189,v189,s54,v14\n"
MF "v[168:171],a[212:215],a[116:119],v[168:171]\n"
"v_fma_f32 v190,v190,s54,v14\n"
"v_fma_f32 v191,v191,s54,v14\n"
MF "v[168:171],a[216:219],a[156:159],v[168:171]\n"
TR "a[208:209],v0 offset:49160\n"
CV "v223,v190,v191\n"
MF "v[168:171],a[220:223],a[196:199],v[168:171]\n"
TR "a[210:211],v0 offset:49672\n"
CV "v222,v188,v189\n"
MF "v[160:163],a[220:223],a[192:195],v[160:163]\n"
CV "v221,v186,v187\n"
CV "v220,v184,v185\n"
MF "v[160:163],a[216:219],a[152:155],v[160:163]\n"
TR "a[212:213],v0 offset:53256\n"
"v_fma_f32 v176,v176,s54,v14\n"
MF "v[152:155],a[216:219],a[148:151],v[152:155]\n"
TR "a[214:215],v0 offset:53768\n"
"v_fma_f32 v177,v177,s54,v14\n"
MF "v[152:155],a[220:223],a[188:191],v[152:155]\n"
"v_fma_f32 v178,v178,s54,v14\n"
"v_fma_f32 v179,v179,s54,v14\n"
MF "v[144:147],a[220:223],a[184:187],v[144:147]\n"
"v_fma_f32 v180,v180,s54,v14\n"
"v_fma_f32 v181,v181,s54,v14\n"
MF "v[144:147],a[216:219],a[144:147],v[144:147]\n"
"v_fma_f32 v182,v182,s54,v14\n"
"v_fma_f32 v183,v183,s54,v14\n"
MF "v[136:139],a[216:219],a[140:143],v[136:139]\n"
CV "v219,v182,v183\n"
CV "v218,v180,v181\n"
MF "v[136:139],a[220:223],a[180:183],v[136:139]\n"
"s_waitcnt lgkmcnt(4)\n"
CV "v217,v178,v179\n"
CV "v216,v176,v177\n"
MF "v[100:103],a[200:203],a[60:63],v[100:103]\n"
TR "a[216:217],v0 offset:32768\n"
"v_fma_f32 v168,v168,s54,v14\n"
MF "v[100:103],a[204:207],a[100:103],v[100:103]\n"
TR "a[218:219],v0 offset:33280\n"
"v_fma_f32 v169,v169,s54,v14\n"
MF "v[108:111],a[204:207],a[104:107],v[108:111]\n"
"v_fma_f32 v170,v170,s54,v14\n"
"v_fma_f32 v171,v171,s54,v14\n"
MF "v[108:111],a[200:203],a[64:67],v[108:111]\n"
TR "a[220:221],v0 offset:36864\n"
"v_fma_f32 v172,v172,s54,v14\n"
MF "v[116:119],a[200:203],a[68:71],v[116:119]\n"
TR "a[222:223],v0 offset:37376\n"
"v_fma_f32 v173,v173,s54,v14\n"
MF "v[116:119],a[204:207],a[108:111],v[116:119]\n"
"v_fma_f32 v174,v174,s54,v14\n"
"v_fma_f32 v175,v175,s54,v14\n"
MF "v[124:127],a[204:207],a[112:115],v[124:127]\n"
CV "v215,v174,v175\n"
CV "v214,v172,v173\n"
MF "v[124:127],a[200:203],a[72:75],v[124:127]\n"
CV "v213,v170,v171\n"
CV "v212,v168,v169\n"
MF "v[132:135],a[200:203],a[76:79],v[132:135]\n"
".Lpg8b:\n"
"s_cbranch_execz .Lpg11b\n"
".Lpg8d:\n"
"s_cbranch_execz .Lpgpsl_d\n"
"v_fma_f32 v160,v160,s54,v14\n"
"v_fma_f32 v161,v161,s54,v14\n"
MF "v[132:135],a[204:207],a[116:119],v[132:135]\n"
"s_waitcnt lgkmcnt(4)\n"
"v_fma_f32 v162,v162,s54,v14\n"
"v_fma_f32 v163,v163,s54,v14\n"
MF "v[100:103],a[208:211],a[140:143],v[100:103]\n"
TR "a[200:201],v0 offset:49152\n"
"v_fma_f32 v164,v164,s54,v14\n"
MF "v[100:103],a[212:215],a[180:183],v[100:103]\n"
TR "a[202:203],v0 offset:49664\n"
"v_fma_f32 v165,v165,s54,v14\n"
MF "v[108:111],a[212:215],a[184:187],v[108:111]\n"
"v_fma_f32 v166,v166,s54,v14\n"
"v_fma_f32 v167,v167,s54,v14\n"
MF "v[108:111],a[208:211],a[144:147],v[108:111]\n"
TR "a[204:205],v0 offset:53248\n"
CV "v211,v166,v167\n"
MF "v[116:119],a[208:211],a[148:151],v[116:119]\n"
TR "a[206:207],v0 offset:53760\n"
CV "v210,v164,v165\n"
MF "v[116:119],a[212:215],a[188:191],v[116:119]\n"
CV "v209,v162,v163\n"
CV "v208,v160,v161\n"
MF "v[124:127],a[212:215],a[192:195],v[124:127]\n"
"v_add_u32 v0,0xc000,v0\n"
"v_fma_f32 v152,v152,s54,v14\n"
MF "v[124:127],a[208:211],a[152:155],v[124:127]\n"
"v_add_u32 v1,0xc000,v1\n"
"v_fma_f32 v153,v153,s54,v14\n"
MF "v[132:135],a[208:211],a[156:159],v[132:135]\n"
"s_waitcnt vmcnt(9) lgkmcnt(0)\n"
"v_fma_f32 v154,v154,s54,v14\n"
"v_fma_f32 v155,v155,s54,v14\n"
MF "v[132:135],a[212:215],a[196:199],v[132:135]\n"
"s_barrier\n"
MF "v[96:99],a[216:219],a[60:63],v[96:99]\n"
TR "a[208:209],v1 offset:16392\n"
"v_fma_f32 v156,v156,s54,v14\n"
MF "v[96:99],a[220:223],a[100:103],v[96:99]\n"
TR "a[210:211],v1 offset:16904\n"
"v_fma_f32 v157,v157,s54,v14\n"
MF "v[104:107],a[220:223],a[104:107],v[104:107]\n"
"s_add_u32 s72,s72,6144\n"
"v_fma_f32 v158,v158,s54,v14\n"
"v_fma_f32 v159,v159,s54,v14\n"
MF "v[104:107],a[216:219],a[64:67],v[104:107]\n"
"s_cmp_eq_u32 s72,s17\n"
"s_cselect_b32 s72,s16,s72\n"
TR "a[212:213],v1 offset:20488\n"
MF "v[112:115],a[216:219],a[68:71],v[112:115]\n"
"s_add_u32 m0,s64,24576\n"
TR "a[214:215],v1 offset:21000\n"
CV "v207,v158,v159\n"
MF "v[112:115],a[220:223],a[108:111],v[112:115]\n"
LD "v7,s[32:35],s72 offen nt lds\n"
MF "v[120:123],a[220:223],a[112:115],v[120:123]\n"
"s_add_u32 m0,s64,25472\n"
CV "v206,v156,v157\n"
CV "v205,v154,v155\n"
MF "v[120:123],a[216:219],a[72:75],v[120:123]\n"
LD "v7,s[32:35],s72 offen offset:128 nt lds\n"
MF "v[128:131],a[216:219],a[76:79],v[128:131]\n"
CV "v204,v152,v153\n"
"v_fma_f32 v144,v144,s54,v14\n"
MF "v[128:131],a[220:223],a[116:119],v[128:131]\n"
"v_fma_f32 v145,v145,s54,v14\n"
"v_fma_f32 v146,v146,s54,v14\n"
MF "v[128:131],a[200:203],a[156:159],v[128:131]\n"
TR "a[216:217],v1 offset:32776\n"
"v_fma_f32 v147,v147,s54,v14\n"
MF "v[128:131],a[204:207],a[196:199],v[128:131]\n"
"s_add_u32 m0,s64,26368\n"
TR "a[218:219],v1 offset:33288\n"
"v_fma_f32 v148,v148,s54,v14\n"
MF "v[120:123],a[204:207],a[192:195],v[120:123]\n"
LD "v7,s[32:35],s72 offen offset:256 nt lds\n"
MF "v[120:123],a[200:203],a[152:155],v[120:123]\n"
TR "a[220:221],v1 offset:36872\n"
"v_fma_f32 v149,v149,s54,v14\n"
MF "v[112:115],a[200:203],a[148:151],v[112:115]\n"
TR "a[222:223],v1 offset:37384\n"
"v_fma_f32 v150,v150,s54,v14\n"
MF "v[112:115],a[204:207],a[188:191],v[112:115]\n"
"v_fma_f32 v151,v151,s54,v14\n"
CV "v203,v150,v151\n"
MF "v[104:107],a[204:207],a[184:187],v[104:107]\n"
"s_add_u32 s72,s72,6144\n"
CV "v202,v148,v149\n"
CV "v201,v146,v147\n"
MF "v[104:107],a[200:203],a[144:147],v[104:107]\n"
"s_add_u32 m0,s64,30720\n"
CV "v200,v144,v145\n"
"v_fma_f32 v136,v136,s54,v14\n"
MF "v[96:99],a[200:203],a[140:143],v[96:99]\n"
LD "v7,s[32:35],s72 offen nt lds\n"
MF "v[96:99],a[204:207],a[180:183],v[96:99]\n"
"s_waitcnt lgkmcnt(4)\n"
"v_fma_f32 v137,v137,s54,v14\n"
"v_fma_f32 v138,v138,s54,v14\n"
MF "v[60:63],a[208:211],a[60:63],v[60:63]\n"
TR "a[200:201],v1 offset:16384\n"
"v_fma_f32 v139,v139,s54,v14\n"
MF "v[60:63],a[212:215],a[100:103],v[60:63]\n"
TR "a[202:203],v1 offset:16896\n"
"v_fma_f32 v140,v140,s54,v14\n"
MF "v[68:71],a[212:215],a[104:107],v[68:71]\n"
"v_fma_f32 v141,v141,s54,v14\n"
"v_fma_f32 v142,v142,s54,v14\n"
MF "v[68:71],a[208:211],a[64:67],v[68:71]\n"
TR "a[204:205],v1 offset:20480\n"
"v_fma_f32 v143,v143,s54,v14\n"
MF "v[76:79],a[208:211],a[68:71],v[76:79]\n"
"s_add_u32 m0,s64,31616\n"
TR "a[206:207],v1 offset:20992\n"
CV "v199,v142,v143\n"
MF "v[76:79],a[212:215],a[108:111],v[76:79]\n"
LD "v7,s[32:35],s72 offen offset:128 nt lds\n"
MF "v[84:87],a[212:215],a[112:115],v[84:87]\n"
CV "v198,v140,v141\n"
CV "v197,v138,v139\n"
MF "v[84:87],a[208:211],a[72:75],v[84:87]\n"
CV "v196,v136,v137\n"
"v_fma_f32 v128,v128,s54,v14\n"
MF "v[92:95],a[208:211],a[76:79],v[92:95]\n"
"s_add_u32 m0,s64,32512\n"
"v_fma_f32 v129,v129,s54,v14\n"
"v_fma_f32 v130,v130,s54,v14\n"
MF "v[92:95],a[212:215],a[116:119],v[92:95]\n"
"s_waitcnt lgkmcnt(4)\n"
LD "v7,s[32:35],s72 offen offset:256 nt lds\n"
MF "v[60:63],a[216:219],a[140:143],v[60:63]\n"
TR "a[208:209],v1 offset:32768\n"
"v_fma_f32 v131,v131,s54,v14\n"
MF "v[60:63],a[220:223],a[180:183],v[60:63]\n"
TR "a[210:211],v1 offset:33280\n"
"v_fma_f32 v132,v132,s54,v14\n"
MF "v[68:71],a[220:223],a[184:187],v[68:71]\n"
"v_fma_f32 v133,v133,s54,v14\n"
"v_fma_f32 v134,v134,s54,v14\n"
MF "v[68:71],a[216:219],a[144:147],v[68:71]\n"
TR "a[212:213],v1 offset:36864\n"
"v_fma_f32 v135,v135,s54,v14\n"
MF "v[76:79],a[216:219],a[148:151],v[76:79]\n"
TR "a[214:215],v1 offset:37376\n"
CV "v195,v134,v135\n"
MF "v[76:79],a[220:223],a[188:191],v[76:79]\n"
CV "v194,v132,v133\n"
CV "v193,v130,v131\n"
MF "v[84:87],a[220:223],a[192:195],v[84:87]\n"
CV "v192,v128,v129\n"
"v_fma_f32 v120,v120,s54,v14\n"
MF "v[84:87],a[216:219],a[152:155],v[84:87]\n"
"v_fma_f32 v121,v121,s54,v14\n"
"v_fma_f32 v122,v122,s54,v14\n"
MF "v[92:95],a[216:219],a[156:159],v[92:95]\n"
"v_fma_f32 v123,v123,s54,v14\n"
"v_fma_f32 v124,v124,s54,v14\n"
MF "v[92:95],a[220:223],a[196:199],v[92:95]\n"
"s_waitcnt lgkmcnt(0)\n"
"v_fma_f32 v125,v125,s54,v14\n"
"v_fma_f32 v126,v126,s54,v14\n"
MF "v[56:59],a[200:203],a[60:63],v[56:59]\n"
TR "a[216:217],v0 offset:16392\n"
"v_fma_f32 v127,v127,s54,v14\n"
MF "v[56:59],a[204:207],a[100:103],v[56:59]\n"
TR "a[218:219],v0 offset:16904\n"
CV "v191,v126,v127\n"
MF "v[64:67],a[204:207],a[104:107],v[64:67]\n"
CV "v190,v124,v125\n"
CV "v189,v122,v123\n"
MF "v[64:67],a[200:203],a[64:67],v[64:67]\n"
TR "a[220:221],v0 offset:20488\n"
CV "v188,v120,v121\n"
MF "v[72:75],a[200:203],a[68:71],v[72:75]\n"
TR "a[222:223],v0 offset:21000\n"
"v_fma_f32 v112,v112,s54,v14\n"
MF "v[72:75],a[204:207],a[108:111],v[72:75]\n"
"v_fma_f32 v113,v113,s54,v14\n"
"v_fma_f32 v114,v114,s54,v14\n"
MF "v[80:83],a[204:207],a[112:115],v[80:83]\n"
"v_fma_f32 v115,v115,s54,v14\n"
"v_fma_f32 v116,v116,s54,v14\n"
MF "v[80:83],a[200:203],a[72:75],v[80:83]\n"
"v_fma_f32 v117,v117,s54,v14\n"
"v_fma_f32 v118,v118,s54,v14\n"
MF "v[88:91],a[200:203],a[76:79],v[88:91]\n"
"v_fma_f32 v119,v119,s54,v14\n"
CV "v187,v118,v119\n"
MF "v[88:91],a[204:207],a[116:119],v[88:91]\n"
CV "v186,v116,v117\n"
CV "v185,v114,v115\n"
MF "v[88:91],a[208:211],a[156:159],v[88:91]\n"
TR "a[200:201],v0 offset:32776\n"
CV "v184,v112,v113\n"
MF "v[88:91],a[212:215],a[196:199],v[88:91]\n"
TR "a[202:203],v0 offset:33288\n"
"v_fma_f32 v104,v104,s54,v14\n"
MF "v[80:83],a[212:215],a[192:195],v[80:83]\n"
"v_fma_f32 v105,v105,s54,v14\n"
"v_fma_f32 v106,v106,s54,v14\n"
MF "v[80:83],a[208:211],a[152:155],v[80:83]\n"
TR "a[204:205],v0 offset:36872\n"
"v_fma_f32 v107,v107,s54,v14\n"
MF "v[72:75],a[208:211],a[148:151],v[72:75]\n"
TR "a[206:207],v0 offset:37384\n"
"v_fma_f32 v108,v108,s54,v14\n"
MF "v[72:75],a[212:215],a[188:191],v[72:75]\n"
"v_fma_f32 v109,v109,s54,v14\n"
"v_fma_f32 v110,v110,s54,v14\n"
MF "v[64:67],a[212:215],a[184:187],v[64:67]\n"
"v_fma_f32 v111,v111,s54,v14\n"
CV "v183,v110,v111\n"
MF "v[64:67],a[208:211],a[144:147],v[64:67]\n"
CV "v182,v108,v109\n"
CV "v181,v106,v107\n"
MF "v[56:59],a[208:211],a[140:143],v[56:59]\n"
CV "v180,v104,v105\n"
"v_fma_f32 v96,v96,s54,v14\n"
MF "v[56:59],a[212:215],a[180:183],v[56:59]\n"
"s_waitcnt lgkmcnt(4)\n"
"v_fma_f32 v97,v97,s54,v14\n"
"v_fma_f32 v98,v98,s54,v14\n"
MF "v[20:23],a[216:219],a[60:63],v[20:23]\n"
TR "a[208:209],v0 offset:16384\n"
"v_fma_f32 v99,v99,s54,v14\n"
MF "v[20:23],a[220:223],a[100:103],v[20:23]\n"
TR "a[210:211],v0 offset:16896\n"
"v_fma_f32 v100,v100,s54,v14\n"
MF "v[28:31],a[220:223],a[104:107],v[28:31]\n"
"v_fma_f32 v101,v101,s54,v14\n"
"v_fma_f32 v102,v102,s54,v14\n"
MF "v[28:31],a[216:219],a[64:67],v[28:31]\n"
TR "a[212:213],v0 offset:20480\n"
"v_fma_f32 v103,v103,s54,v14\n"
MF "v[36:39],a[216:219],a[68:71],v[36:39]\n"
TR "a[214:215],v0 offset:20992\n"
CV "v179,v102,v103\n"
MF "v[36:39],a[220:223],a[108:111],v[36:39]\n"
CV "v178,v100,v101\n"
CV "v177,v98,v99\n"
MF "v[44:47],a[220:223],a[112:115],v[44:47]\n"
CV "v176,v96,v97\n"
"v_fma_f32 v88,v88,s54,v14\n"
MF "v[44:47],a[216:219],a[72:75],v[44:47]\n"
"v_fma_f32 v89,v89,s54,v14\n"
"v_fma_f32 v90,v90,s54,v14\n"
MF "v[52:55],a[216:219],a[76:79],v[52:55]\n"
"v_fma_f32 v91,v91,s54,v14\n"
"v_fma_f32 v92,v92,s54,v14\n"
MF "v[52:55],a[220:223],a[116:119],v[52:55]\n"
"s_waitcnt lgkmcnt(4)\n"
"v_fma_f32 v93,v93,s54,v14\n"
"v_fma_f32 v94,v94,s54,v14\n"
MF "v[20:23],a[200:203],a[140:143],v[20:23]\n"
TR "a[216:217],v0 offset:32768\n"
"v_fma_f32 v95,v95,s54,v14\n"
MF "v[20:23],a[204:207],a[180:183],v[20:23]\n"
TR "a[218:219],v0 offset:33280\n"
CV "v175,v94,v95\n"
MF "v[28:31],a[204:207],a[184:187],v[28:31]\n"
CV "v174,v92,v93\n"
CV "v173,v90,v91\n"
MF "v[28:31],a[200:203],a[144:147],v[28:31]\n"
TR "a[220:221],v0 offset:36864\n"
CV "v172,v88,v89\n"
MF "v[36:39],a[200:203],a[148:151],v[36:39]\n"
TR "a[222:223],v0 offset:37376\n"
"v_fma_f32 v80,v80,s54,v14\n"
MF "v[36:39],a[204:207],a[188:191],v[36:39]\n"
"v_fma_f32 v81,v81,s54,v14\n"
"v_fma_f32 v82,v82,s54,v14\n"
MF "v[44:47],a[204:207],a[192:195],v[44:47]\n"
"v_fma_f32 v83,v83,s54,v14\n"
"v_fma_f32 v84,v84,s54,v14\n"
MF "v[44:47],a[200:203],a[152:155],v[44:47]\n"
"v_fma_f32 v85,v85,s54,v14\n"
"v_fma_f32 v86,v86,s54,v14\n"
MF "v[52:55],a[200:203],a[156:159],v[52:55]\n"
"s_waitcnt lgkmcnt(0)\n"
"v_fma_f32 v87,v87,s54,v14\n"
CV "v171,v86,v87\n"
MF "v[52:55],a[204:207],a[196:199],v[52:55]\n"
"s_barrier\n"
MF "v[16:19],a[208:211],a[60:63],v[16:19]\n"
"s_add_u32 m0,s65,49152\n"
CV "v170,v84,v85\n"
CV "v169,v82,v83\n"
MF "v[16:19],a[212:215],a[100:103],v[16:19]\n"
LD "v8,s[36:39],s73 offen nt lds\n"
MF "v[24:27],a[212:215],a[104:107],v[24:27]\n"
"s_add_u32 m0,s65,50048\n"
CV "v168,v80,v81\n"
"v_fma_f32 v72,v72,s54,v14\n"
MF "v[24:27],a[208:211],a[64:67],v[24:27]\n"
LD "v8,s[36:39],s73 offen offset:128 nt lds\n"
MF "v[32:35],a[208:211],a[68:71],v[32:35]\n"
"v_fma_f32 v73,v73,s54,v14\n"
"v_fma_f32 v74,v74,s54,v14\n"
MF "v[32:35],a[212:215],a[108:111],v[32:35]\n"
"s_add_u32 m0,s65,50944\n"
"v_fma_f32 v75,v75,s54,v14\n"
"v_fma_f32 v76,v76,s54,v14\n"
MF "v[40:43],a[212:215],a[112:115],v[40:43]\n"
LD "v8,s[36:39],s73 offen offset:256 nt lds\n"
MF "v[40:43],a[208:211],a[72:75],v[40:43]\n"
"v_fma_f32 v77,v77,s54,v14\n"
"v_fma_f32 v78,v78,s54,v14\n"
MF "v[48:51],a[208:211],a[76:79],v[48:51]\n"
"s_add_u32 m0,s65,49152\n"
"v_fma_f32 v79,v79,s54,v14\n"
CV "v167,v78,v79\n"
MF "v[48:51],a[212:215],a[116:119],v[48:51]\n"
LD "v8,s[36:39],s73 offen offset:3072 nt lds\n"
MF "v[48:51],a[216:219],a[156:159],v[48:51]\n"
"s_add_u32 m0,s65,50048\n"
CV "v166,v76,v77\n"
CV "v165,v74,v75\n"
MF "v[48:51],a[220:223],a[196:199],v[48:51]\n"
LD "v8,s[36:39],s73 offen offset:3200 nt lds\n"
MF "v[40:43],a[220:223],a[192:195],v[40:43]\n"
CV "v164,v72,v73\n"
"v_fma_f32 v64,v64,s54,v14\n"
MF "v[40:43],a[216:219],a[152:155],v[40:43]\n"
"s_add_u32 m0,s65,50944\n"
"v_fma_f32 v65,v65,s54,v14\n"
"v_fma_f32 v66,v66,s54,v14\n"
MF "v[32:35],a[216:219],a[148:151],v[32:35]\n"
LD "v8,s[36:39],s73 offen offset:3328 nt lds\n"
MF "v[32:35],a[220:223],a[188:191],v[32:35]\n"
"v_fma_f32 v67,v67,s54,v14\n"
"v_fma_f32 v68,v68,s54,v14\n"
MF "v[24:27],a[220:223],a[184:187],v[24:27]\n"
"v_fma_f32 v69,v69,s54,v14\n"
"v_fma_f32 v70,v70,s54,v14\n"
MF "v[24:27],a[216:219],a[144:147],v[24:27]\n"
"v_fma_f32 v71,v71,s54,v14\n"
CV "v163,v70,v71\n"
MF "v[16:19],a[216:219],a[140:143],v[16:19]\n"
CV "v162,v68,v69\n"
CV "v161,v66,v67\n"
MF "v[16:19],a[220:223],a[180:183],v[16:19]\n"
CV "v160,v64,v65\n"
AR "v0,a244\n"
"v_xor_b32 v1,64,v0\n"
"s_waitcnt vmcnt(12) lgkmcnt(0)\n"
"s_barrier\n"
"s_nop 0\n"
"ds_read_b128 v[92:95],v0\n"
"ds_read_b128 v[96:99],v1\n"
"ds_read_b128 v[100:103],v0 offset:1024\n"
"v_pk_fma_f32 v[56:57],v[56:57],s[54:55],v[14:15] op_sel_hi:[1,1,0]\n"
"v_pk_fma_f32 v[58:59],v[58:59],s[54:55],v[14:15] op_sel_hi:[1,1,0]\n"
"v_pk_fma_f32 v[60:61],v[60:61],s[54:55],v[14:15] op_sel_hi:[1,1,0]\n"
"v_pk_fma_f32 v[62:63],v[62:63],s[54:55],v[14:15] op_sel_hi:[1,1,0]\n"
CV "v159,v62,v63\n"
CV "v158,v60,v61\n"
CV "v157,v58,v59\n"
CV "v156,v56,v57\n"
"v_pk_mul_f32 v[48:49],v[48:49],s[54:55]\n"
"v_pk_mul_f32 v[50:51],v[50:51],s[54:55]\n"
"v_pk_mul_f32 v[52:53],v[52:53],s[54:55]\n"
"v_pk_mul_f32 v[54:55],v[54:55],s[54:55]\n"
CV "v155,v54,v55\n"
CV "v154,v52,v53\n"
CV "v153,v50,v51\n"
CV "v152,v48,v49\n"
"v_pk_mul_f32 v[40:41],v[40:41],s[54:55]\n"
"v_pk_mul_f32 v[42:43],v[42:43],s[54:55]\n"
"v_pk_mul_f32 v[44:45],v[44:45],s[54:55]\n"
"v_pk_mul_f32 v[46:47],v[46:47],s[54:55]\n"
CV "v151,v46,v47\n"
CV "v150,v44,v45\n"
CV "v149,v42,v43\n"
CV "v148,v40,v41\n"
"v_pk_mul_f32 v[32:33],v[32:33],s[54:55]\n"
"v_pk_mul_f32 v[34:35],v[34:35],s[54:55]\n"
"v_pk_mul_f32 v[36:37],v[36:37],s[54:55]\n"
"v_pk_mul_f32 v[38:39],v[38:39],s[54:55]\n"
CV "v147,v38,v39\n"
CV "v146,v36,v37\n"
CV "v145,v34,v35\n"
CV "v144,v32,v33\n"
"v_pk_mul_f32 v[24:25],v[24:25],s[54:55]\n"
"v_pk_mul_f32 v[26:27],v[26:27],s[54:55]\n"
"v_pk_mul_f32 v[28:29],v[28:29],s[54:55]\n"
"v_pk_mul_f32 v[30:31],v[30:31],s[54:55]\n"
CV "v143,v30,v31\n"
CV "v142,v28,v29\n"
CV "v141,v26,v27\n"
CV "v140,v24,v25\n"
"v_pk_mul_f32 v[16:17],v[16:17],s[54:55]\n"
"v_pk_mul_f32 v[18:19],v[18:19],s[54:55]\n"
"v_pk_mul_f32 v[20:21],v[20:21],s[54:55]\n"
"v_pk_mul_f32 v[22:23],v[22:23],s[54:55]\n"
CV "v139,v22,v23\n"
CV "v138,v20,v21\n"
CV "v137,v18,v19\n"
CV "v136,v16,v17\n"
"s_cmp_eq_u32 s57,s69\n"
"s_cbranch_scc1 .LattB\n"
AR "v2,a245\n"
"v_add_u32 v2,49152,v2\n"
"v_xor_b32 v3,32,v2\n"
"v_xor_b32 v4,64,v2\n"
"v_xor_b32 v5,96,v2\n"
"s_mov_b32 s61,7\n"
".Latt2x:\n"
"s_cmp_eq_u32 s57,1\n"
"s_cbranch_scc1 .Lt0kx\n"
".Lt0nx:\n"
"s_waitcnt lgkmcnt(2)\n"
MF "v[72:75],v[92:95],v[136:139],0\n"
"s_add_u32 s73,s73,12288\n"
"ds_read_b128 v[104:107],v1 offset:1024\n"
AR "v10,a224\n"
MF "v[76:79],v[92:95],v[140:143],0\n"
"s_cmp_eq_u32 s73,s19\n"
"s_cselect_b32 s73,s18,s73\n"
"s_add_u32 m0,s65,73728\n"
MF "v[80:83],v[92:95],v[144:147],0\n"
LD "v8,s[36:39],s73 offen nt lds\n"
MF "v[84:87],v[92:95],v[148:151],0\n"
"v_max_f32_dpp v10,|v10|,|v10| quad_perm:[1,0,3,2] row_mask:0xf bank_mask:0xf\n"
AR "v11,a225\n"
MF "v[88:91],v[92:95],v[152:155],0\n"
"s_waitcnt lgkmcnt(2)\n"
"ds_read_b128 v[92:95],v0 offset:2048\n"
AR "v12,a226\n"
MF "v[72:75],v[96:99],v[156:159],v[72:75]\n"
"s_add_u32 m0,s65,74624\n"
"v_max_f32_dpp v10,v10,v10 quad_perm:[2,3,0,1] row_mask:0xf bank_mask:0xf\n"
"v_max_f32_dpp v11,|v11|,|v11| quad_perm:[1,0,3,2] row_mask:0xf bank_mask:0xf\n"
MF "v[76:79],v[96:99],v[160:163],v[76:79]\n"
LD "v8,s[36:39],s73 offen offset:128 nt lds\n"
MF "v[80:83],v[96:99],v[164:167],v[80:83]\n"
"v_max_f32_dpp v10,v10,v10 row_half_mirror row_mask:0xf bank_mask:0xf\n"
"v_max_f32_dpp v11,v11,v11 quad_perm:[2,3,0,1] row_mask:0xf bank_mask:0xf\n"
MF "v[84:87],v[96:99],v[168:171],v[84:87]\n"
"v_readlane_b32 s13,v10,0\n"
"v_readlane_b32 s0,v10,24\n"
MF "v[88:91],v[96:99],v[172:175],v[88:91]\n"
"s_waitcnt lgkmcnt(2)\n"
"ds_read_b128 v[96:99],v1 offset:2048\n"
"v_readlane_b32 s1,v10,32\n"
MF "v[72:75],v[100:103],v[176:179],v[72:75]\n"
"s_add_u32 m0,s65,75520\n"
"v_readlane_b32 s2,v10,56\n"
"v_max_f32_dpp v11,v11,v11 row_half_mirror row_mask:0xf bank_mask:0xf\n"
MF "v[76:79],v[100:103],v[180:183],v[76:79]\n"
LD "v8,s[36:39],s73 offen offset:256 nt lds\n"
MF "v[80:83],v[100:103],v[184:187],v[80:83]\n"
"s_min_u32 s13,s13,s0\n"
"v_readlane_b32 s11,v11,0\n"
"v_readlane_b32 s3,v11,24\n"
MF "v[84:87],v[100:103],v[188:191],v[84:87]\n"
"s_min_u32 s1,s1,s2\n"
"v_readlane_b32 s4,v11,32\n"
"v_readlane_b32 s5,v11,56\n"
MF "v[88:91],v[100:103],v[192:195],v[88:91]\n"
"s_waitcnt lgkmcnt(2)\n"
"ds_read_b128 v[100:103],v0 offset:6144\n"
"v_max_f32_dpp v12,|v12|,|v12| quad_perm:[1,0,3,2] row_mask:0xf bank_mask:0xf\n"
MF "v[72:75],v[104:107],v[196:199],v[72:75]\n"
"s_add_u32 m0,s65,73728\n"
MF "v[76:79],v[104:107],v[200:203],v[76:79]\n"
LD "v8,s[36:39],s73 offen offset:3072 nt lds\n"
MF "v[80:83],v[104:107],v[204:207],v[80:83]\n"
"s_min_u32 s13,s13,s1\n"
"v_max_f32_dpp v12,v12,v12 quad_perm:[2,3,0,1] row_mask:0xf bank_mask:0xf\n"
MF "v[84:87],v[104:107],v[208:211],v[84:87]\n"
"s_and_b32 s13,s13,0x7f800000\n"
MF "v[88:91],v[104:107],v[212:215],v[88:91]\n"
"s_waitcnt lgkmcnt(2)\n"
"ds_read_b128 v[104:107],v1 offset:6144\n"
"v_max_f32_dpp v12,v12,v12 row_half_mirror row_mask:0xf bank_mask:0xf\n"
MF "v[72:75],v[92:95],v[216:219],v[72:75]\n"
"s_add_u32 m0,s65,74624\n"
"v_readlane_b32 s10,v12,0\n"
"v_readlane_b32 s6,v12,24\n"
MF "v[76:79],v[92:95],v[220:223],v[76:79]\n"
LD "v8,s[36:39],s73 offen offset:3200 nt lds\n"
MF "v[80:83],v[92:95],v[224:227],v[80:83]\n"
"s_sub_u32 s13,s13,0x14000000\n"
"v_readlane_b32 s7,v12,32\n"
"v_readlane_b32 s8,v12,56\n"
MF "v[84:87],v[92:95],v[228:231],v[84:87]\n"
"s_cmp_lt_u32 s13,0x50000000\n"
"s_cselect_b32 s13,s13,0xed400000\n"
MF "v[88:91],v[92:95],v[232:235],v[88:91]\n"
"s_waitcnt lgkmcnt(2)\n"
"ds_read_b128 v[92:95],v0 offset:7168\n"
MF "v[72:75],v[96:99],v[236:239],v[72:75]\n"
"s_add_u32 m0,s65,75520\n"
MF "v[76:79],v[96:99],v[240:243],v[76:79]\n"
LD "v8,s[36:39],s73 offen offset:3328 nt lds\n"
MF "v[80:83],v[96:99],v[244:247],v[80:83]\n"
"s_add_u32 s13,s13,0x12c00000\n"
MF "v[84:87],v[96:99],v[248:251],v[84:87]\n"
"s_min_u32 s11,s11,s3\n"
MF "v[88:91],v[96:99],v[252:255],v[88:91]\n"
"s_waitcnt lgkmcnt(2)\n"
"ds_read_b128 v[96:99],v1 offset:7168\n"
MF "v[112:115],v[100:103],v[136:139],0\n"
"s_min_u32 s4,s4,s5\n"
MF "v[116:119],v[100:103],v[140:143],0\n"
"s_min_u32 s11,s11,s4\n"
MF "v[120:123],v[100:103],v[144:147],0\n"
"s_and_b32 s11,s11,0x7f800000\n"
MF "v[124:127],v[100:103],v[148:151],0\n"
"s_sub_u32 s11,s11,0x14000000\n"
MF "v[128:131],v[100:103],v[152:155],0\n"
"s_waitcnt lgkmcnt(2)\n"
"ds_read_b128 v[100:103],v0 offset:8192\n"
MF "v[112:115],v[104:107],v[156:159],v[112:115]\n"
"s_min_u32 s10,s10,s6\n"
"v_max3_f32 v52,v72,v73,v74\n"
"v_max_f32 v52,v52,v75\n"
MF "v[116:119],v[104:107],v[160:163],v[116:119]\n"
"s_min_u32 s7,s7,s8\n"
"v_max3_f32 v53,v76,v77,v78\n"
"v_max_f32 v53,v53,v79\n"
MF "v[120:123],v[104:107],v[164:167],v[120:123]\n"
"s_min_u32 s10,s10,s7\n"
"v_max3_f32 v54,v80,v81,v82\n"
"v_max_f32 v54,v54,v83\n"
MF "v[124:127],v[104:107],v[168:171],v[124:127]\n"
"v_max3_f32 v55,v84,v85,v86\n"
"v_max_f32 v55,v55,v87\n"
MF "v[128:131],v[104:107],v[172:175],v[128:131]\n"
"s_waitcnt lgkmcnt(2)\n"
"ds_read_b128 v[104:107],v1 offset:8192\n"
"v_max3_f32 v56,v88,v89,v90\n"
MF "v[112:115],v[92:95],v[176:179],v[112:115]\n"
"s_cmp_lt_u32 s11,0x50000000\n"
"s_cselect_b32 s11,s11,0xed400000\n"
"v_max_f32 v56,v56,v91\n"
MF "v[116:119],v[92:95],v[180:183],v[116:119]\n"
".Lpg9b:\n"
"s_cbranch_execz .Lpgpsl_b\n"
"s_add_u32 s11,s11,0x12c00000\n"
MF "v[120:123],v[92:95],v[184:187],v[120:123]\n"
MF "v[124:127],v[92:95],v[188:191],v[124:127]\n"
MF "v[128:131],v[92:95],v[192:195],v[128:131]\n"
"s_waitcnt lgkmcnt(2)\n"
MF "v[112:115],v[96:99],v[196:199],v[112:115]\n"
"s_waitcnt lgkmcnt(0)\n"
MF "v[116:119],v[96:99],v[200:203],v[116:119]\n"
"s_barrier\n"
MF "v[120:123],v[96:99],v[204:207],v[120:123]\n"
"s_add_u32 s72,s72,6144\n"
MF "v[124:127],v[96:99],v[208:211],v[124:127]\n"
"s_cmp_eq_u32 s72,s17\n"
"s_cselect_b32 s72,s16,s72\n"
"s_add_u32 m0,s64,0\n"
MF "v[128:131],v[96:99],v[212:215],v[128:131]\n"
LD "v7,s[32:35],s72 offen nt lds\n"
MF "v[112:115],v[100:103],v[216:219],v[112:115]\n"
"s_add_u32 m0,s64,896\n"
MF "v[116:119],v[100:103],v[220:223],v[116:119]\n"
LD "v7,s[32:35],s72 offen offset:128 nt lds\n"
MF "v[120:123],v[100:103],v[224:227],v[120:123]\n"
"s_add_u32 m0,s64,1792\n"
MF "v[124:127],v[100:103],v[228:231],v[124:127]\n"
LD "v7,s[32:35],s72 offen offset:256 nt lds\n"
MF "v[128:131],v[100:103],v[232:235],v[128:131]\n"
"s_add_u32 s72,s72,6144\n"
"s_add_u32 m0,s64,6144\n"
MF "v[112:115],v[104:107],v[236:239],v[112:115]\n"
LD "v7,s[32:35],s72 offen nt lds\n"
MF "v[116:119],v[104:107],v[240:243],v[116:119]\n"
"s_add_u32 m0,s64,7040\n"
MF "v[120:123],v[104:107],v[244:247],v[120:123]\n"
LD "v7,s[32:35],s72 offen offset:128 nt lds\n"
MF "v[124:127],v[104:107],v[248:251],v[124:127]\n"
"s_add_u32 m0,s64,7936\n"
MF "v[128:131],v[104:107],v[252:255],v[128:131]\n"
LD "v7,s[32:35],s72 offen offset:256 nt lds\n"
"s_branch .Lt0ex\n"
".Lpgt0kx_d:\n"
"s_cbranch_execz .Lisr2\n"
".Lt0kx:\n"
"s_lshl_b32 s58,s56,10\n"
"s_add_u32 s58,s58,73728\n"
"v_add_u32 v32,s58,v9\n"
"v_add_u32 v33,65536,v32\n"
"s_waitcnt lgkmcnt(2)\n"
MF "v[72:75],v[92:95],v[136:139],0\n"
"s_add_u32 s73,s73,12288\n"
"ds_read_b128 v[104:107],v1 offset:1024\n"
AR "v10,a224\n"
MF "v[76:79],v[92:95],v[140:143],0\n"
"s_cmp_eq_u32 s73,s19\n"
"s_cselect_b32 s73,s18,s73\n"
"s_add_u32 m0,s65,73728\n"
MF "v[80:83],v[92:95],v[144:147],0\n"
LD "v8,s[36:39],s73 offen nt lds\n"
MF "v[84:87],v[92:95],v[148:151],0\n"
"v_max_f32_dpp v10,|v10|,|v10| quad_perm:[1,0,3,2] row_mask:0xf bank_mask:0xf\n"
AR "v11,a225\n"
MF "v[88:91],v[92:95],v[152:155],0\n"
"s_waitcnt lgkmcnt(2)\n"
"ds_read_b128 v[92:95],v0 offset:2048\n"
AR "v12,a226\n"
MF "v[72:75],v[96:99],v[156:159],v[72:75]\n"
"s_add_u32 m0,s65,74624\n"
"v_max_f32_dpp v10,v10,v10 quad_perm:[2,3,0,1] row_mask:0xf bank_mask:0xf\n"
"v_max_f32_dpp v11,|v11|,|v11| quad_perm:[1,0,3,2] row_mask:0xf bank_mask:0xf\n"
MF "v[76:79],v[96:99],v[160:163],v[76:79]\n"
LD "v8,s[36:39],s73 offen offset:128 nt lds\n"
MF "v[80:83],v[96:99],v[164:167],v[80:83]\n"
"v_max_f32_dpp v10,v10,v10 row_half_mirror row_mask:0xf bank_mask:0xf\n"
"v_max_f32_dpp v11,v11,v11 quad_perm:[2,3,0,1] row_mask:0xf bank_mask:0xf\n"
MF "v[84:87],v[96:99],v[168:171],v[84:87]\n"
"v_readlane_b32 s13,v10,0\n"
"v_readlane_b32 s0,v10,24\n"
MF "v[88:91],v[96:99],v[172:175],v[88:91]\n"
"s_waitcnt lgkmcnt(2)\n"
"ds_read_b128 v[96:99],v1 offset:2048\n"
"v_readlane_b32 s1,v10,32\n"
MF "v[72:75],v[100:103],v[176:179],v[72:75]\n"
"s_add_u32 m0,s65,75520\n"
"v_readlane_b32 s2,v10,56\n"
"v_max_f32_dpp v11,v11,v11 row_half_mirror row_mask:0xf bank_mask:0xf\n"
MF "v[76:79],v[100:103],v[180:183],v[76:79]\n"
LD "v8,s[36:39],s73 offen offset:256 nt lds\n"
MF "v[80:83],v[100:103],v[184:187],v[80:83]\n"
"s_min_u32 s13,s13,s0\n"
"v_readlane_b32 s11,v11,0\n"
"v_readlane_b32 s3,v11,24\n"
MF "v[84:87],v[100:103],v[188:191],v[84:87]\n"
"s_min_u32 s1,s1,s2\n"
"v_readlane_b32 s4,v11,32\n"
"v_readlane_b32 s5,v11,56\n"
MF "v[88:91],v[100:103],v[192:195],v[88:91]\n"
"s_waitcnt lgkmcnt(2)\n"
"ds_read_b128 v[100:103],v0 offset:6144\n"
"v_max_f32_dpp v12,|v12|,|v12| quad_perm:[1,0,3,2] row_mask:0xf bank_mask:0xf\n"
MF "v[72:75],v[104:107],v[196:199],v[72:75]\n"
"s_add_u32 m0,s65,73728\n"
"ds_write_b128 v32,a[0:3] offset:24576\n"
MF "v[76:79],v[104:107],v[200:203],v[76:79]\n"
LD "v8,s[36:39],s73 offen offset:3072 nt lds\n"
MF "v[80:83],v[104:107],v[204:207],v[80:83]\n"
"s_min_u32 s13,s13,s1\n"
"v_max_f32_dpp v12,v12,v12 quad_perm:[2,3,0,1] row_mask:0xf bank_mask:0xf\n"
AR "v62,a20\n"
MF "v[84:87],v[104:107],v[208:211],v[84:87]\n"
"s_and_b32 s13,s13,0x7f800000\n"
"ds_write_b128 v32,a[4:7] offset:28672\n"
MF "v[88:91],v[104:107],v[212:215],v[88:91]\n"
"s_waitcnt lgkmcnt(4)\n"
"ds_read_b128 v[104:107],v1 offset:6144\n"
"v_max_f32_dpp v12,v12,v12 row_half_mirror row_mask:0xf bank_mask:0xf\n"
MF "v[72:75],v[92:95],v[216:219],v[72:75]\n"
"s_add_u32 m0,s65,74624\n"
"v_readlane_b32 s10,v12,0\n"
"v_readlane_b32 s6,v12,24\n"
MF "v[76:79],v[92:95],v[220:223],v[76:79]\n"
LD "v8,s[36:39],s73 offen offset:3200 nt lds\n"
MF "v[80:83],v[92:95],v[224:227],v[80:83]\n"
"s_sub_u32 s13,s13,0x14000000\n"
"v_readlane_b32 s7,v12,32\n"
"v_readlane_b32 s8,v12,56\n"
MF "v[84:87],v[92:95],v[228:231],v[84:87]\n"
"s_cmp_lt_u32 s13,0x50000000\n"
"s_cselect_b32 s13,s13,0xed400000\n"
AR "v63,a21\n"
MF "v[88:91],v[92:95],v[232:235],v[88:91]\n"
"s_waitcnt lgkmcnt(4)\n"
"ds_read_b128 v[92:95],v0 offset:7168\n"
AR "v64,a22\n"
MF "v[72:75],v[96:99],v[236:239],v[72:75]\n"
"s_add_u32 m0,s65,75520\n"
"ds_write_b128 v32,a[8:11] offset:32768\n"
MF "v[76:79],v[96:99],v[240:243],v[76:79]\n"
LD "v8,s[36:39],s73 offen offset:3328 nt lds\n"
MF "v[80:83],v[96:99],v[244:247],v[80:83]\n"
"s_add_u32 s13,s13,0x12c00000\n"
"ds_write_b128 v32,a[12:15] offset:36864\n"
MF "v[84:87],v[96:99],v[248:251],v[84:87]\n"
"s_min_u32 s11,s11,s3\n"
"ds_write_b128 v32,a[16:19] offset:40960\n"
MF "v[88:91],v[96:99],v[252:255],v[88:91]\n"
"s_waitcnt lgkmcnt(7)\n"
"ds_read_b128 v[96:99],v1 offset:7168\n"
AR "v65,a23\n"
MF "v[112:115],v[100:103],v[136:139],0\n"
"s_min_u32 s4,s4,s5\n"
"ds_write_b128 v32,a[40:43] offset:45056\n"
MF "v[116:119],v[100:103],v[140:143],0\n"
"s_min_u32 s11,s11,s4\n"
"ds_write_b128 v32,a[44:47] offset:49152\n"
MF "v[120:123],v[100:103],v[144:147],0\n"
"s_and_b32 s11,s11,0x7f800000\n"
"ds_write_b128 v32,a[48:51] offset:53248\n"
MF "v[124:127],v[100:103],v[148:151],0\n"
"s_sub_u32 s11,s11,0x14000000\n"
"ds_write_b128 v32,a[52:55] offset:57344\n"
MF "v[128:131],v[100:103],v[152:155],0\n"
"s_waitcnt lgkmcnt(9)\n"
"ds_read_b128 v[100:103],v0 offset:8192\n"
AR "v66,a24\n"
MF "v[112:115],v[104:107],v[156:159],v[112:115]\n"
"s_min_u32 s10,s10,s6\n"
"v_max3_f32 v52,v72,v73,v74\n"
"v_max_f32 v52,v52,v75\n"
MF "v[116:119],v[104:107],v[160:163],v[116:119]\n"
"s_min_u32 s7,s7,s8\n"
"v_max3_f32 v53,v76,v77,v78\n"
"v_max_f32 v53,v53,v79\n"
MF "v[120:123],v[104:107],v[164:167],v[120:123]\n"
"s_min_u32 s10,s10,s7\n"
"v_max3_f32 v54,v80,v81,v82\n"
"v_max_f32 v54,v54,v83\n"
MF "v[124:127],v[104:107],v[168:171],v[124:127]\n"
"v_max3_f32 v55,v84,v85,v86\n"
"v_max_f32 v55,v55,v87\n"
MF "v[128:131],v[104:107],v[172:175],v[128:131]\n"
"s_waitcnt lgkmcnt(9)\n"
"ds_read_b128 v[104:107],v1 offset:8192\n"
"v_max3_f32 v56,v88,v89,v90\n"
MF "v[112:115],v[92:95],v[176:179],v[112:115]\n"
"s_cmp_lt_u32 s11,0x50000000\n"
"s_cselect_b32 s11,s11,0xed400000\n"
"v_max_f32 v56,v56,v91\n"
MF "v[116:119],v[92:95],v[180:183],v[116:119]\n"
"s_add_u32 s11,s11,0x12c00000\n"
"ds_write_b128 v32,a[56:59] offset:61440\n"
MF "v[120:123],v[92:95],v[184:187],v[120:123]\n"
"ds_write_b128 v33,a[80:83]\n"
MF "v[124:127],v[92:95],v[188:191],v[124:127]\n"
"ds_write_b128 v33,a[84:87] offset:4096\n"
MF "v[128:131],v[92:95],v[192:195],v[128:131]\n"
"s_waitcnt lgkmcnt(9)\n"
"ds_write_b128 v33,a[88:91] offset:8192\n"
MF "v[112:115],v[96:99],v[196:199],v[112:115]\n"
"s_waitcnt lgkmcnt(0)\n"
AR "v67,a25\n"
AR "v68,a26\n"
MF "v[116:119],v[96:99],v[200:203],v[116:119]\n"
"s_barrier\n"
MF "v[120:123],v[96:99],v[204:207],v[120:123]\n"
"s_add_u32 s72,s72,6144\n"
"ds_write_b128 v33,a[92:95] offset:12288\n"
MF "v[124:127],v[96:99],v[208:211],v[124:127]\n"
"s_cmp_eq_u32 s72,s17\n"
"s_cselect_b32 s72,s16,s72\n"
"s_add_u32 m0,s64,0\n"
MF "v[128:131],v[96:99],v[212:215],v[128:131]\n"
LD "v7,s[32:35],s72 offen nt lds\n"
MF "v[112:115],v[100:103],v[216:219],v[112:115]\n"
"s_add_u32 m0,s64,896\n"
"ds_write_b128 v33,a[96:99] offset:16384\n"
MF "v[116:119],v[100:103],v[220:223],v[116:119]\n"
LD "v7,s[32:35],s72 offen offset:128 nt lds\n"
MF "v[120:123],v[100:103],v[224:227],v[120:123]\n"
"s_add_u32 m0,s64,1792\n"
AR "v69,a27\n"
AR "v70,a28\n"
MF "v[124:127],v[100:103],v[228:231],v[124:127]\n"
LD "v7,s[32:35],s72 offen offset:256 nt lds\n"
MF "v[128:131],v[100:103],v[232:235],v[128:131]\n"
"s_add_u32 s72,s72,6144\n"
"s_add_u32 m0,s64,6144\n"
AR "v71,a29\n"
MF "v[112:115],v[104:107],v[236:239],v[112:115]\n"
LD "v7,s[32:35],s72 offen nt lds\n"
MF "v[116:119],v[104:107],v[240:243],v[116:119]\n"
"s_add_u32 m0,s64,7040\n"
"ds_write_b128 v33,a[120:123] offset:20480\n"
MF "v[120:123],v[104:107],v[244:247],v[120:123]\n"
LD "v7,s[32:35],s72 offen offset:128 nt lds\n"
MF "v[124:127],v[104:107],v[248:251],v[124:127]\n"
"s_add_u32 m0,s64,7936\n"
AR "v132,a30\n"
AR "v133,a31\n"
MF "v[128:131],v[104:107],v[252:255],v[128:131]\n"
LD "v7,s[32:35],s72 offen offset:256 nt lds\n"
AR "v134,a32\n"
AR "v135,a33\n"
AR "v6,a34\n"
AR "v13,a35\n"
AR "v14,a36\n"
AR "v15,a37\n"
"v_accvgpr_mov_b32 a254,a38\n"
"v_accvgpr_mov_b32 a255,a39\n"
"v_accvgpr_mov_b32 a240,a168\n"
"v_accvgpr_mov_b32 a241,a169\n"
"v_accvgpr_mov_b32 a242,a170\n"
"v_accvgpr_mov_b32 a243,a171\n"
"v_accvgpr_mov_b32 a244,a172\n"
"v_accvgpr_mov_b32 a245,a173\n"
"v_accvgpr_mov_b32 a246,a174\n"
"v_accvgpr_mov_b32 a247,a175\n"
"v_accvgpr_mov_b32 a248,a176\n"
"v_accvgpr_mov_b32 a249,a177\n"
"v_accvgpr_mov_b32 a250,a178\n"
"v_accvgpr_mov_b32 a251,a179\n"
".Lpgt0ex_b:\n"
"s_cbranch_execz .Lpg2a\n"
".Lt0ex:\n"
"s_nop 1\n"
"v_max3_f32 v52,v52,v112,v113\n"
"v_max3_f32 v53,v53,v116,v117\n"
"v_max3_f32 v54,v54,v120,v121\n"
"v_max3_f32 v55,v55,v124,v125\n"
"v_max3_f32 v56,v56,v128,v129\n"
"v_max3_f32 v52,v52,v114,v115\n"
"v_max3_f32 v53,v53,v118,v119\n"
"v_max3_f32 v54,v54,v122,v123\n"
"v_max3_f32 v55,v55,v126,v127\n"
"v_max3_f32 v56,v56,v130,v131\n"
"v_mov_b32 v57,v52\n"
"v_mov_b32 v58,v53\n"
"v_mov_b32 v59,v54\n"
"v_mov_b32 v60,v55\n"
"v_mov_b32 v61,v56\n"
"v_permlane16_swap_b32 v52,v57\n"
"v_permlane16_swap_b32 v53,v58\n"
"v_permlane16_swap_b32 v54,v59\n"
"v_permlane16_swap_b32 v55,v60\n"
"v_permlane16_swap_b32 v56,v61\n"
"v_max_f32 v52,v52,v57\n"
"v_max_f32 v53,v53,v58\n"
"v_max_f32 v54,v54,v59\n"
"v_max_f32 v55,v55,v60\n"
"v_max_f32 v56,v56,v61\n"
"v_mov_b32 v57,v52\n"
"v_mov_b32 v58,v53\n"
"v_mov_b32 v59,v54\n"
"v_mov_b32 v60,v55\n"
"v_mov_b32 v61,v56\n"
"v_permlane32_swap_b32 v52,v57\n"
"v_permlane32_swap_b32 v53,v58\n"
"v_permlane32_swap_b32 v54,v59\n"
"v_permlane32_swap_b32 v55,v60\n"
"v_permlane32_swap_b32 v56,v61\n"
"v_max_f32 v52,v52,v57\n"
"v_max_f32 v53,v53,v58\n"
"v_max_f32 v54,v54,v59\n"
"v_max_f32 v55,v55,v60\n"
"v_max_f32 v56,v56,v61\n"
"v_sub_f32 v32,0xc1300000,v52\n"
"v_sub_f32 v36,0xc1300000,v53\n"
"v_sub_f32 v40,0xc1300000,v54\n"
"v_sub_f32 v44,0xc1300000,v55\n"
"v_sub_f32 v48,0xc1300000,v56\n"
"v_mov_b32_e64 v33,v32\n"
"v_pk_mov_b32 v[34:35],v[32:33],v[32:33]\n"
"v_mov_b32 v37,v36\n"
"v_pk_mov_b32 v[38:39],v[36:37],v[36:37]\n"
"v_mov_b32 v41,v40\n"
"v_pk_mov_b32 v[42:43],v[40:41],v[40:41]\n"
"v_mov_b32 v45,v44\n"
"v_pk_mov_b32 v[46:47],v[44:45],v[44:45]\n"
"v_mov_b32 v49,v48\n"
"v_pk_mov_b32 v[50:51],v[48:49],v[48:49]\n"
"v_pk_add_f32 v[72:73],v[72:73],v[32:33]\n"
"v_pk_add_f32 v[74:75],v[74:75],v[32:33]\n"
"v_pk_add_f32 v[112:113],v[112:113],v[32:33]\n"
"v_pk_add_f32 v[114:115],v[114:115],v[32:33]\n"
"v_exp_f32 v72,v72\n"
"v_exp_f32 v73,v73\n"
"v_exp_f32 v74,v74\n"
"v_exp_f32 v75,v75\n"
"v_exp_f32 v112,v112\n"
"v_exp_f32 v113,v113\n"
"v_exp_f32 v114,v114\n"
"v_exp_f32 v115,v115\n"
"v_pk_add_f32 v[10:11],v[72:73],v[74:75]\n"
"v_pk_add_f32 v[28:29],v[112:113],v[114:115]\n"
"v_pk_add_f32 v[10:11],v[10:11],v[28:29]\n"
"v_add_f32_e64 v52,v10,v11\n"
CV "v72,v72,v73\n"
CV "v73,v74,v75\n"
CV "v74,v112,v113\n"
CV "v75,v114,v115\n"
"v_pk_add_f32 v[76:77],v[76:77],v[36:37]\n"
"v_pk_add_f32 v[78:79],v[78:79],v[36:37]\n"
"v_pk_add_f32 v[116:117],v[116:117],v[36:37]\n"
"v_pk_add_f32 v[118:119],v[118:119],v[36:37]\n"
"v_exp_f32 v76,v76\n"
"v_exp_f32 v77,v77\n"
"v_exp_f32 v78,v78\n"
"v_exp_f32 v79,v79\n"
"v_exp_f32 v116,v116\n"
"v_exp_f32 v117,v117\n"
"v_exp_f32 v118,v118\n"
"v_exp_f32 v119,v119\n"
"v_pk_add_f32 v[10:11],v[76:77],v[78:79]\n"
"v_pk_add_f32 v[28:29],v[116:117],v[118:119]\n"
"v_pk_add_f32 v[10:11],v[10:11],v[28:29]\n"
"v_add_f32_e64 v53,v10,v11\n"
CV "v76,v76,v77\n"
CV "v77,v78,v79\n"
CV "v78,v116,v117\n"
CV "v79,v118,v119\n"
"v_pk_add_f32 v[80:81],v[80:81],v[40:41]\n"
"v_pk_add_f32 v[82:83],v[82:83],v[40:41]\n"
"v_pk_add_f32 v[120:121],v[120:121],v[40:41]\n"
"v_pk_add_f32 v[122:123],v[122:123],v[40:41]\n"
"v_exp_f32 v80,v80\n"
"v_exp_f32 v81,v81\n"
"v_exp_f32 v82,v82\n"
"v_exp_f32 v83,v83\n"
"v_exp_f32 v120,v120\n"
"v_exp_f32 v121,v121\n"
"v_exp_f32 v122,v122\n"
"v_exp_f32 v123,v123\n"
"v_pk_add_f32 v[10:11],v[80:81],v[82:83]\n"
"v_pk_add_f32 v[28:29],v[120:121],v[122:123]\n"
"v_pk_add_f32 v[10:11],v[10:11],v[28:29]\n"
"v_add_f32_e64 v54,v10,v11\n"
CV "v80,v80,v81\n"
CV "v81,v82,v83\n"
CV "v82,v120,v121\n"
CV "v83,v122,v123\n"
"v_pk_add_f32 v[84:85],v[84:85],v[44:45]\n"
"v_pk_add_f32 v[86:87],v[86:87],v[44:45]\n"
"v_pk_add_f32 v[124:125],v[124:125],v[44:45]\n"
"v_pk_add_f32 v[126:127],v[126:127],v[44:45]\n"
"v_mov_b32_e64 v55,0\n"
"v_pk_add_f32 v[88:89],v[88:89],v[48:49]\n"
"v_pk_add_f32 v[90:91],v[90:91],v[48:49]\n"
"v_pk_add_f32 v[128:129],v[128:129],v[48:49]\n"
"v_pk_add_f32 v[130:131],v[130:131],v[48:49]\n"
"v_mov_b32 v56,0\n"
"s_waitcnt vmcnt(18) lgkmcnt(0)\n"
"s_barrier\n"
"ds_read_b128 v[16:19],v0 offset:24576\n"
"ds_read_b128 v[20:23],v1 offset:24576\n"
AW "a220,s13\n"
AW "a221,s13\n"
AW "a222,s13\n"
AW "a223,s13\n"
"s_waitcnt lgkmcnt(1)\n"
MF "v[92:95],v[16:19],v[136:139],v[32:35]\n"
"s_nop 0\n"
"ds_read_b128 v[24:27],v0 offset:25600\n"
"ds_read_b128 v[28:31],v1 offset:25600\n"
"s_waitcnt lgkmcnt(2)\n"
MF "v[92:95],v[20:23],v[156:159],v[92:95]\n"
"v_exp_f32 v84,v84\n"
MF "v[96:99],v[20:23],v[160:163],v[36:39]\n"
"v_exp_f32 v85,v85\n"
MF "v[96:99],v[16:19],v[140:143],v[96:99]\n"
"v_exp_f32 v86,v86\n"
MF "v[100:103],v[16:19],v[144:147],v[40:43]\n"
"v_exp_f32 v87,v87\n"
MF "v[100:103],v[20:23],v[164:167],v[100:103]\n"
"v_add_f32 v55,v55,v84\n"
"v_add_f32 v55,v55,v85\n"
MF "v[104:107],v[20:23],v[168:171],v[44:47]\n"
CV "v84,v84,v85\n"
"v_add_f32 v55,v55,v86\n"
MF "v[104:107],v[16:19],v[148:151],v[104:107]\n"
"v_add_f32 v55,v55,v87\n"
CV "v85,v86,v87\n"
MF "v[108:111],v[16:19],v[152:155],v[48:51]\n"
"v_exp_f32 v124,v124\n"
MF "v[108:111],v[20:23],v[172:175],v[108:111]\n"
"s_waitcnt lgkmcnt(0)\n"
"ds_read_b128 v[16:19],v0 offset:26624\n"
"ds_read_b128 v[20:23],v1 offset:26624\n"
MF "v[108:111],v[24:27],v[192:195],v[108:111]\n"
"v_exp_f32 v125,v125\n"
MF "v[108:111],v[28:31],v[212:215],v[108:111]\n"
"v_exp_f32 v126,v126\n"
MF "v[104:107],v[28:31],v[208:211],v[104:107]\n"
"v_exp_f32 v127,v127\n"
MF "v[104:107],v[24:27],v[188:191],v[104:107]\n"
"v_add_f32 v55,v55,v124\n"
"v_add_f32 v55,v55,v125\n"
MF "v[100:103],v[24:27],v[184:187],v[100:103]\n"
CV "v86,v124,v125\n"
"v_add_f32 v55,v55,v126\n"
MF "v[100:103],v[28:31],v[204:207],v[100:103]\n"
"v_add_f32 v55,v55,v127\n"
CV "v87,v126,v127\n"
MF "v[96:99],v[28:31],v[200:203],v[96:99]\n"
"v_exp_f32 v88,v88\n"
MF "v[96:99],v[24:27],v[180:183],v[96:99]\n"
"v_exp_f32 v89,v89\n"
MF "v[92:95],v[24:27],v[176:179],v[92:95]\n"
"v_exp_f32 v90,v90\n"
MF "v[92:95],v[28:31],v[196:199],v[92:95]\n"
"s_waitcnt lgkmcnt(0)\n"
"ds_read_b128 v[24:27],v0 offset:30720\n"
"ds_read_b128 v[28:31],v1 offset:30720\n"
MF "v[92:95],v[16:19],v[216:219],v[92:95]\n"
"v_exp_f32 v91,v91\n"
MF "v[92:95],v[20:23],v[236:239],v[92:95]\n"
"v_add_f32 v56,v56,v88\n"
"v_add_f32 v56,v56,v89\n"
MF "v[96:99],v[20:23],v[240:243],v[96:99]\n"
CV "v88,v88,v89\n"
"v_add_f32 v56,v56,v90\n"
MF "v[96:99],v[16:19],v[220:223],v[96:99]\n"
"v_add_f32 v56,v56,v91\n"
CV "v89,v90,v91\n"
MF "v[100:103],v[16:19],v[224:227],v[100:103]\n"
"v_exp_f32 v128,v128\n"
MF "v[100:103],v[20:23],v[244:247],v[100:103]\n"
"v_exp_f32 v129,v129\n"
MF "v[104:107],v[20:23],v[248:251],v[104:107]\n"
"v_exp_f32 v130,v130\n"
MF "v[104:107],v[16:19],v[228:231],v[104:107]\n"
"v_exp_f32 v131,v131\n"
MF "v[108:111],v[16:19],v[232:235],v[108:111]\n"
"v_add_f32 v56,v56,v128\n"
"v_add_f32 v56,v56,v129\n"
MF "v[108:111],v[20:23],v[252:255],v[108:111]\n"
"s_waitcnt lgkmcnt(0)\n"
"ds_read_b128 v[16:19],v0 offset:31744\n"
"ds_read_b128 v[20:23],v1 offset:31744\n"
MF "v[112:115],v[24:27],v[136:139],v[32:35]\n"
CV "v90,v128,v129\n"
"v_add_f32 v56,v56,v130\n"
MF "v[112:115],v[28:31],v[156:159],v[112:115]\n"
"v_add_f32 v56,v56,v131\n"
CV "v91,v130,v131\n"
MF "v[116:119],v[28:31],v[160:163],v[36:39]\n"
"v_exp_f32 v92,v92\n"
MF "v[116:119],v[24:27],v[140:143],v[116:119]\n"
"v_exp_f32 v93,v93\n"
MF "v[120:123],v[24:27],v[144:147],v[40:43]\n"
"v_exp_f32 v94,v94\n"
MF "v[120:123],v[28:31],v[164:167],v[120:123]\n"
"v_exp_f32 v95,v95\n"
MF "v[124:127],v[28:31],v[168:171],v[44:47]\n"
"v_add_f32 v57,v52,v92\n"
"v_add_f32 v57,v57,v93\n"
MF "v[124:127],v[24:27],v[148:151],v[124:127]\n"
CV "v92,v92,v93\n"
"v_add_f32 v57,v57,v94\n"
MF "v[128:131],v[24:27],v[152:155],v[48:51]\n"
"v_add_f32 v57,v57,v95\n"
CV "v93,v94,v95\n"
MF "v[128:131],v[28:31],v[172:175],v[128:131]\n"
"s_waitcnt lgkmcnt(0)\n"
"ds_read_b128 v[24:27],v0 offset:32768\n"
"ds_read_b128 v[28:31],v1 offset:32768\n"
MF "v[128:131],v[16:19],v[192:195],v[128:131]\n"
"v_exp_f32 v96,v96\n"
MF "v[128:131],v[20:23],v[212:215],v[128:131]\n"
"v_exp_f32 v97,v97\n"
MF "v[124:127],v[20:23],v[208:211],v[124:127]\n"
"v_exp_f32 v98,v98\n"
MF "v[124:127],v[16:19],v[188:191],v[124:127]\n"
"v_exp_f32 v99,v99\n"
MF "v[120:123],v[16:19],v[184:187],v[120:123]\n"
"v_add_f32 v58,v53,v96\n"
"v_add_f32 v58,v58,v97\n"
MF "v[120:123],v[20:23],v[204:207],v[120:123]\n"
CV "v96,v96,v97\n"
"v_add_f32 v58,v58,v98\n"
MF "v[116:119],v[20:23],v[200:203],v[116:119]\n"
"s_waitcnt vmcnt(12) lgkmcnt(0)\n"
"v_add_f32 v58,v58,v99\n"
CV "v97,v98,v99\n"
MF "v[116:119],v[16:19],v[180:183],v[116:119]\n"
"s_barrier\n"
MF "v[112:115],v[16:19],v[176:179],v[112:115]\n"
"v_exp_f32 v100,v100\n"
MF "v[112:115],v[20:23],v[196:199],v[112:115]\n"
"s_nop 0\n"
TR "v[16:17],v2\n"
TR "v[18:19],v2 offset:6144\n"
MF "v[112:115],v[24:27],v[216:219],v[112:115]\n"
"s_add_u32 s72,s72,6144\n"
TR "v[20:21],v3\n"
TR "v[22:23],v3 offset:6144\n"
MF "v[112:115],v[28:31],v[236:239],v[112:115]\n"
"s_cmp_eq_u32 s72,s17\n"
"s_cselect_b32 s72,s16,s72\n"
"s_add_u32 m0,s64,24576\n"
MF "v[116:119],v[28:31],v[240:243],v[116:119]\n"
LD "v7,s[32:35],s72 offen nt lds\n"
MF "v[116:119],v[24:27],v[220:223],v[116:119]\n"
"s_add_u32 m0,s64,25472\n"
"v_exp_f32 v101,v101\n"
MF "v[120:123],v[24:27],v[224:227],v[120:123]\n"
LD "v7,s[32:35],s72 offen offset:128 nt lds\n"
MF "v[120:123],v[28:31],v[244:247],v[120:123]\n"
"v_exp_f32 v102,v102\n"
MF "v[124:127],v[28:31],v[248:251],v[124:127]\n"
"s_add_u32 m0,s64,26368\n"
"v_exp_f32 v103,v103\n"
MF "v[124:127],v[24:27],v[228:231],v[124:127]\n"
LD "v7,s[32:35],s72 offen offset:256 nt lds\n"
MF "v[128:131],v[24:27],v[232:235],v[128:131]\n"
"s_add_u32 s72,s72,6144\n"
"v_add_f32 v59,v54,v100\n"
"v_add_f32 v59,v59,v101\n"
MF "v[128:131],v[28:31],v[252:255],v[128:131]\n"
"s_waitcnt lgkmcnt(0)\n"
TR "v[24:25],v4\n"
TR "v[26:27],v4 offset:6144\n"
MF "a[0:3],v[16:19],v[72:75],a[220:223]\n"
"s_add_u32 m0,s64,30720\n"
TR "v[28:29],v5\n"
TR "v[30:31],v5 offset:6144\n"
MF "a[20:23],v[20:23],v[72:75],a[220:223]\n"
LD "v7,s[32:35],s72 offen nt lds\n"
MF "a[24:27],v[20:23],v[76:79],a[220:223]\n"
CV "v100,v100,v101\n"
"v_add_f32 v59,v59,v102\n"
MF "a[4:7],v[16:19],v[76:79],a[220:223]\n"
"s_add_u32 m0,s64,31616\n"
"v_add_f32 v59,v59,v103\n"
CV "v101,v102,v103\n"
MF "a[8:11],v[16:19],v[80:83],a[220:223]\n"
LD "v7,s[32:35],s72 offen offset:128 nt lds\n"
MF "a[28:31],v[20:23],v[80:83],a[220:223]\n"
"v_exp_f32 v104,v104\n"
MF "a[32:35],v[20:23],v[84:87],a[220:223]\n"
"s_add_u32 m0,s64,32512\n"
"v_exp_f32 v105,v105\n"
MF "a[12:15],v[16:19],v[84:87],a[220:223]\n"
LD "v7,s[32:35],s72 offen offset:256 nt lds\n"
MF "a[16:19],v[16:19],v[88:91],a[220:223]\n"
"v_exp_f32 v106,v106\n"
MF "a[36:39],v[20:23],v[88:91],a[220:223]\n"
"s_waitcnt lgkmcnt(0)\n"
TR "v[16:17],v2 offset:1024\n"
TR "v[18:19],v2 offset:7168\n"
MF "a[56:59],v[24:27],v[88:91],a[220:223]\n"
"s_nop 0\n"
TR "v[20:21],v3 offset:1024\n"
TR "v[22:23],v3 offset:7168\n"
MF "a[76:79],v[28:31],v[88:91],a[220:223]\n"
"v_exp_f32 v107,v107\n"
MF "a[72:75],v[28:31],v[84:87],a[220:223]\n"
"v_add_f32 v60,v55,v104\n"
"v_add_f32 v60,v60,v105\n"
MF "a[52:55],v[24:27],v[84:87],a[220:223]\n"
CV "v104,v104,v105\n"
"v_add_f32 v60,v60,v106\n"
MF "a[48:51],v[24:27],v[80:83],a[220:223]\n"
"v_add_f32 v60,v60,v107\n"
CV "v105,v106,v107\n"
MF "a[68:71],v[28:31],v[80:83],a[220:223]\n"
"v_exp_f32 v108,v108\n"
MF "a[64:67],v[28:31],v[76:79],a[220:223]\n"
"v_exp_f32 v109,v109\n"
MF "a[44:47],v[24:27],v[76:79],a[220:223]\n"
"v_exp_f32 v110,v110\n"
MF "a[40:43],v[24:27],v[72:75],a[220:223]\n"
"v_exp_f32 v111,v111\n"
MF "a[60:63],v[28:31],v[72:75],a[220:223]\n"
"s_waitcnt lgkmcnt(0)\n"
TR "v[24:25],v4 offset:1024\n"
TR "v[26:27],v4 offset:7168\n"
MF "a[80:83],v[16:19],v[72:75],a[220:223]\n"
".Lpg10b:\n"
"s_cbranch_execz .Lpg1a\n"
".Lpg10d:\n"
"s_cbranch_execz .Lisr2\n"
"s_nop 0\n"
TR "v[28:29],v5 offset:1024\n"
TR "v[30:31],v5 offset:7168\n"
MF "a[100:103],v[20:23],v[72:75],a[220:223]\n"
"v_add_f32 v61,v56,v108\n"
"v_add_f32 v61,v61,v109\n"
MF "a[104:107],v[20:23],v[76:79],a[220:223]\n"
CV "v108,v108,v109\n"
"v_add_f32 v61,v61,v110\n"
MF "a[84:87],v[16:19],v[76:79],a[220:223]\n"
"v_add_f32 v61,v61,v111\n"
CV "v109,v110,v111\n"
MF "a[88:91],v[16:19],v[80:83],a[220:223]\n"
"v_exp_f32 v112,v112\n"
MF "a[108:111],v[20:23],v[80:83],a[220:223]\n"
"v_exp_f32 v113,v113\n"
MF "a[112:115],v[20:23],v[84:87],a[220:223]\n"
"v_exp_f32 v114,v114\n"
MF "a[92:95],v[16:19],v[84:87],a[220:223]\n"
"v_exp_f32 v115,v115\n"
MF "a[96:99],v[16:19],v[88:91],a[220:223]\n"
"v_add_f32 v57,v57,v112\n"
"v_add_f32 v57,v57,v113\n"
MF "a[116:119],v[20:23],v[88:91],a[220:223]\n"
"s_waitcnt lgkmcnt(0)\n"
TR "v[16:17],v2 offset:2048\n"
TR "v[18:19],v2 offset:8192\n"
MF "a[136:139],v[24:27],v[88:91],a[220:223]\n"
"s_nop 0\n"
TR "v[20:21],v3 offset:2048\n"
TR "v[22:23],v3 offset:8192\n"
MF "a[156:159],v[28:31],v[88:91],a[220:223]\n"
CV "v94,v112,v113\n"
"v_add_f32 v57,v57,v114\n"
MF "a[152:155],v[28:31],v[84:87],a[220:223]\n"
"v_add_f32 v57,v57,v115\n"
CV "v95,v114,v115\n"
MF "a[132:135],v[24:27],v[84:87],a[220:223]\n"
"v_exp_f32 v116,v116\n"
MF "a[128:131],v[24:27],v[80:83],a[220:223]\n"
"v_exp_f32 v117,v117\n"
MF "a[148:151],v[28:31],v[80:83],a[220:223]\n"
"v_exp_f32 v118,v118\n"
MF "a[144:147],v[28:31],v[76:79],a[220:223]\n"
"v_exp_f32 v119,v119\n"
MF "a[124:127],v[24:27],v[76:79],a[220:223]\n"
"v_add_f32 v58,v58,v116\n"
"v_add_f32 v58,v58,v117\n"
MF "a[120:123],v[24:27],v[72:75],a[220:223]\n"
CV "v98,v116,v117\n"
"v_add_f32 v58,v58,v118\n"
MF "a[140:143],v[28:31],v[72:75],a[220:223]\n"
"s_waitcnt lgkmcnt(0)\n"
TR "v[24:25],v4 offset:2048\n"
TR "v[26:27],v4 offset:8192\n"
MF "a[160:163],v[16:19],v[72:75],a[220:223]\n"
"s_nop 0\n"
TR "v[28:29],v5 offset:2048\n"
TR "v[30:31],v5 offset:8192\n"
MF "a[180:183],v[20:23],v[72:75],a[220:223]\n"
"v_add_f32 v58,v58,v119\n"
CV "v99,v118,v119\n"
MF "a[184:187],v[20:23],v[76:79],a[220:223]\n"
"v_exp_f32 v120,v120\n"
MF "a[164:167],v[16:19],v[76:79],a[220:223]\n"
"v_exp_f32 v121,v121\n"
MF "a[168:171],v[16:19],v[80:83],a[220:223]\n"
"v_exp_f32 v122,v122\n"
MF "a[188:191],v[20:23],v[80:83],a[220:223]\n"
"v_exp_f32 v123,v123\n"
MF "a[192:195],v[20:23],v[84:87],a[220:223]\n"
"v_add_f32 v59,v59,v120\n"
"v_add_f32 v59,v59,v121\n"
MF "a[172:175],v[16:19],v[84:87],a[220:223]\n"
"s_waitcnt vmcnt(6) lgkmcnt(0)\n"
CV "v102,v120,v121\n"
"v_add_f32 v59,v59,v122\n"
MF "a[176:179],v[16:19],v[88:91],a[220:223]\n"
"s_barrier\n"
MF "a[196:199],v[20:23],v[88:91],a[220:223]\n"
"s_add_u32 s73,s73,12288\n"
"ds_read_b128 v[16:19],v0\n"
"ds_read_b128 v[20:23],v1\n"
MF "a[216:219],v[24:27],v[88:91],a[220:223]\n"
"s_cmp_eq_u32 s73,s19\n"
"s_cselect_b32 s73,s18,s73\n"
"s_add_u32 m0,s65,49152\n"
MF "a[236:239],v[28:31],v[88:91],a[220:223]\n"
LD "v8,s[36:39],s73 offen nt lds\n"
MF "a[232:235],v[28:31],v[84:87],a[220:223]\n"
"s_add_u32 m0,s65,50048\n"
"v_add_f32 v59,v59,v123\n"
CV "v103,v122,v123\n"
MF "a[212:215],v[24:27],v[84:87],a[220:223]\n"
LD "v8,s[36:39],s73 offen offset:128 nt lds\n"
MF "a[208:211],v[24:27],v[80:83],a[220:223]\n"
"s_add_u32 m0,s65,50944\n"
MF "a[228:231],v[28:31],v[80:83],a[220:223]\n"
LD "v8,s[36:39],s73 offen offset:256 nt lds\n"
MF "a[224:227],v[28:31],v[76:79],a[220:223]\n"
MF "a[204:207],v[24:27],v[76:79],a[220:223]\n"
MF "a[200:203],v[24:27],v[72:75],a[220:223]\n"
MF "a[220:223],v[28:31],v[72:75],a[220:223]\n"
"s_waitcnt lgkmcnt(0)\n"
".Laloopx:\n"
MF "v[72:75],v[16:19],v[136:139],v[32:35]\n"
"s_nop 0\n"
"ds_read_b128 v[24:27],v0 offset:1024\n"
"ds_read_b128 v[28:31],v1 offset:1024\n"
MF "v[72:75],v[20:23],v[156:159],v[72:75]\n"
MF "v[76:79],v[20:23],v[160:163],v[36:39]\n"
MF "v[76:79],v[16:19],v[140:143],v[76:79]\n"
MF "v[80:83],v[16:19],v[144:147],v[40:43]\n"
MF "v[80:83],v[20:23],v[164:167],v[80:83]\n"
MF "v[84:87],v[20:23],v[168:171],v[44:47]\n"
MF "v[84:87],v[16:19],v[148:151],v[84:87]\n"
"s_add_u32 m0,s65,49152\n"
MF "v[88:91],v[16:19],v[152:155],v[48:51]\n"
LD "v8,s[36:39],s73 offen offset:3072 nt lds\n"
MF "v[88:91],v[20:23],v[172:175],v[88:91]\n"
"s_waitcnt lgkmcnt(0)\n"
"ds_read_b128 v[16:19],v0 offset:2048\n"
"ds_read_b128 v[20:23],v1 offset:2048\n"
MF "v[88:91],v[24:27],v[192:195],v[88:91]\n"
"v_exp_f32 v124,v124\n"
MF "v[88:91],v[28:31],v[212:215],v[88:91]\n"
"s_add_u32 m0,s65,50048\n"
"v_exp_f32 v125,v125\n"
MF "v[84:87],v[28:31],v[208:211],v[84:87]\n"
LD "v8,s[36:39],s73 offen offset:3200 nt lds\n"
MF "v[84:87],v[24:27],v[188:191],v[84:87]\n"
"v_exp_f32 v126,v126\n"
MF "v[80:83],v[24:27],v[184:187],v[80:83]\n"
"v_exp_f32 v127,v127\n"
MF "v[80:83],v[28:31],v[204:207],v[80:83]\n"
"s_add_u32 m0,s65,50944\n"
"v_add_f32 v60,v60,v124\n"
"v_add_f32 v60,v60,v125\n"
MF "v[76:79],v[28:31],v[200:203],v[76:79]\n"
LD "v8,s[36:39],s73 offen offset:3328 nt lds\n"
MF "v[76:79],v[24:27],v[180:183],v[76:79]\n"
CV "v106,v124,v125\n"
"v_add_f32 v60,v60,v126\n"
MF "v[72:75],v[24:27],v[176:179],v[72:75]\n"
"v_add_f32 v60,v60,v127\n"
CV "v107,v126,v127\n"
MF "v[72:75],v[28:31],v[196:199],v[72:75]\n"
"s_waitcnt lgkmcnt(0)\n"
"ds_read_b128 v[24:27],v0 offset:6144\n"
"ds_read_b128 v[28:31],v1 offset:6144\n"
MF "v[72:75],v[16:19],v[216:219],v[72:75]\n"
"v_exp_f32 v128,v128\n"
MF "v[72:75],v[20:23],v[236:239],v[72:75]\n"
"v_exp_f32 v129,v129\n"
MF "v[76:79],v[20:23],v[240:243],v[76:79]\n"
"v_exp_f32 v130,v130\n"
MF "v[76:79],v[16:19],v[220:223],v[76:79]\n"
"v_exp_f32 v131,v131\n"
MF "v[80:83],v[16:19],v[224:227],v[80:83]\n"
"v_add_f32 v61,v61,v128\n"
"v_add_f32 v61,v61,v129\n"
MF "v[80:83],v[20:23],v[244:247],v[80:83]\n"
CV "v110,v128,v129\n"
"v_add_f32 v61,v61,v130\n"
MF "v[84:87],v[20:23],v[248:251],v[84:87]\n"
"v_add_f32 v61,v61,v131\n"
CV "v111,v130,v131\n"
MF "v[84:87],v[16:19],v[228:231],v[84:87]\n"
"v_max3_f32 v10,v57,v58,v59\n"
"v_max3_f32 v10,v10,v60,v61\n"
MF "v[88:91],v[16:19],v[232:235],v[88:91]\n"
"v_cmp_ngt_f32 vcc,s95,v10\n"
MF "v[88:91],v[20:23],v[252:255],v[88:91]\n"
"s_waitcnt lgkmcnt(0)\n"
"s_cbranch_vccnz .Lstub_6\n"
".Lback_6:\n"
MF "v[112:115],v[24:27],v[136:139],v[32:35]\n"
"s_add_u32 s74,s74,12288\n"
"ds_read_b128 v[16:19],v0 offset:7168\n"
"ds_read_b128 v[20:23],v1 offset:7168\n"
MF "v[112:115],v[28:31],v[156:159],v[112:115]\n"
"s_cmp_eq_u32 s74,s20\n"
"s_cselect_b32 s74,s70,s74\n"
MF "v[116:119],v[28:31],v[160:163],v[36:39]\n"
"v_exp_f32 v72,v72\n"
MF "v[116:119],v[24:27],v[140:143],v[116:119]\n"
"v_exp_f32 v73,v73\n"
MF "v[120:123],v[24:27],v[144:147],v[40:43]\n"
"v_exp_f32 v74,v74\n"
MF "v[120:123],v[28:31],v[164:167],v[120:123]\n"
"v_exp_f32 v75,v75\n"
MF "v[124:127],v[28:31],v[168:171],v[44:47]\n"
"v_add_f32 v52,v57,v72\n"
"v_add_f32 v52,v52,v73\n"
MF "v[124:127],v[24:27],v[148:151],v[124:127]\n"
CV "v72,v72,v73\n"
"v_add_f32 v52,v52,v74\n"
MF "v[128:131],v[24:27],v[152:155],v[48:51]\n"
"v_add_f32 v52,v52,v75\n"
CV "v73,v74,v75\n"
MF "v[128:131],v[28:31],v[172:175],v[128:131]\n"
"s_waitcnt lgkmcnt(0)\n"
"ds_read_b128 v[24:27],v0 offset:8192\n"
"ds_read_b128 v[28:31],v1 offset:8192\n"
MF "v[128:131],v[16:19],v[192:195],v[128:131]\n"
"v_exp_f32 v76,v76\n"
MF "v[128:131],v[20:23],v[212:215],v[128:131]\n"
"v_exp_f32 v77,v77\n"
MF "v[124:127],v[20:23],v[208:211],v[124:127]\n"
"v_exp_f32 v78,v78\n"
MF "v[124:127],v[16:19],v[188:191],v[124:127]\n"
"v_exp_f32 v79,v79\n"
MF "v[120:123],v[16:19],v[184:187],v[120:123]\n"
"v_add_f32 v53,v58,v76\n"
"v_add_f32 v53,v53,v77\n"
MF "v[120:123],v[20:23],v[204:207],v[120:123]\n"
CV "v76,v76,v77\n"
"v_add_f32 v53,v53,v78\n"
MF "v[116:119],v[20:23],v[200:203],v[116:119]\n"
"s_waitcnt vmcnt(12) lgkmcnt(0)\n"
"v_add_f32 v53,v53,v79\n"
CV "v77,v78,v79\n"
MF "v[116:119],v[16:19],v[180:183],v[116:119]\n"
"s_barrier\n"
MF "v[112:115],v[16:19],v[176:179],v[112:115]\n"
"v_exp_f32 v80,v80\n"
MF "v[112:115],v[20:23],v[196:199],v[112:115]\n"
"s_nop 0\n"
TR "v[16:17],v2 offset:24576\n"
TR "v[18:19],v2 offset:30720\n"
MF "v[112:115],v[24:27],v[216:219],v[112:115]\n"
"s_add_u32 s72,s72,6144\n"
TR "v[20:21],v3 offset:24576\n"
TR "v[22:23],v3 offset:30720\n"
MF "v[112:115],v[28:31],v[236:239],v[112:115]\n"
"s_cmp_eq_u32 s72,s17\n"
"s_cselect_b32 s72,s16,s72\n"
"s_add_u32 m0,s64,0\n"
MF "v[116:119],v[28:31],v[240:243],v[116:119]\n"
LD "v7,s[32:35],s72 offen nt lds\n"
MF "v[116:119],v[24:27],v[220:223],v[116:119]\n"
"s_add_u32 m0,s64,896\n"
"v_exp_f32 v81,v81\n"
MF "v[120:123],v[24:27],v[224:227],v[120:123]\n"
LD "v7,s[32:35],s72 offen offset:128 nt lds\n"
MF "v[120:123],v[28:31],v[244:247],v[120:123]\n"
"v_exp_f32 v82,v82\n"
MF "v[124:127],v[28:31],v[248:251],v[124:127]\n"
"s_add_u32 m0,s64,1792\n"
"v_exp_f32 v83,v83\n"
MF "v[124:127],v[24:27],v[228:231],v[124:127]\n"
LD "v7,s[32:35],s72 offen offset:256 nt lds\n"
MF "v[128:131],v[24:27],v[232:235],v[128:131]\n"
"s_add_u32 s72,s72,6144\n"
"v_add_f32 v54,v59,v80\n"
"v_add_f32 v54,v54,v81\n"
MF "v[128:131],v[28:31],v[252:255],v[128:131]\n"
"s_waitcnt lgkmcnt(0)\n"
TR "v[24:25],v4 offset:24576\n"
TR "v[26:27],v4 offset:30720\n"
MF "a[0:3],v[16:19],v[92:95],a[0:3]\n"
"s_add_u32 m0,s64,6144\n"
TR "v[28:29],v5 offset:24576\n"
TR "v[30:31],v5 offset:30720\n"
MF "a[20:23],v[20:23],v[92:95],a[20:23]\n"
LD "v7,s[32:35],s72 offen nt lds\n"
MF "a[24:27],v[20:23],v[96:99],a[24:27]\n"
CV "v80,v80,v81\n"
"v_add_f32 v54,v54,v82\n"
MF "a[4:7],v[16:19],v[96:99],a[4:7]\n"
"s_add_u32 m0,s64,7040\n"
"v_add_f32 v54,v54,v83\n"
CV "v81,v82,v83\n"
MF "a[8:11],v[16:19],v[100:103],a[8:11]\n"
LD "v7,s[32:35],s72 offen offset:128 nt lds\n"
MF "a[28:31],v[20:23],v[100:103],a[28:31]\n"
"v_exp_f32 v84,v84\n"
MF "a[32:35],v[20:23],v[104:107],a[32:35]\n"
"s_add_u32 m0,s64,7936\n"
"v_exp_f32 v85,v85\n"
MF "a[12:15],v[16:19],v[104:107],a[12:15]\n"
LD "v7,s[32:35],s72 offen offset:256 nt lds\n"
MF "a[16:19],v[16:19],v[108:111],a[16:19]\n"
"v_exp_f32 v86,v86\n"
MF "a[36:39],v[20:23],v[108:111],a[36:39]\n"
"s_waitcnt lgkmcnt(0)\n"
TR "v[16:17],v2 offset:25600\n"
TR "v[18:19],v2 offset:31744\n"
MF "a[56:59],v[24:27],v[108:111],a[56:59]\n"
"s_nop 0\n"
TR "v[20:21],v3 offset:25600\n"
TR "v[22:23],v3 offset:31744\n"
MF "a[76:79],v[28:31],v[108:111],a[76:79]\n"
"v_exp_f32 v87,v87\n"
MF "a[72:75],v[28:31],v[104:107],a[72:75]\n"
"v_add_f32 v55,v60,v84\n"
"v_add_f32 v55,v55,v85\n"
MF "a[52:55],v[24:27],v[104:107],a[52:55]\n"
CV "v84,v84,v85\n"
"v_add_f32 v55,v55,v86\n"
MF "a[48:51],v[24:27],v[100:103],a[48:51]\n"
"v_add_f32 v55,v55,v87\n"
CV "v85,v86,v87\n"
MF "a[68:71],v[28:31],v[100:103],a[68:71]\n"
"v_exp_f32 v88,v88\n"
MF "a[64:67],v[28:31],v[96:99],a[64:67]\n"
"v_exp_f32 v89,v89\n"
MF "a[44:47],v[24:27],v[96:99],a[44:47]\n"
"v_exp_f32 v90,v90\n"
MF "a[40:43],v[24:27],v[92:95],a[40:43]\n"
"v_exp_f32 v91,v91\n"
MF "a[60:63],v[28:31],v[92:95],a[60:63]\n"
"s_waitcnt lgkmcnt(0)\n"
TR "v[24:25],v4 offset:25600\n"
TR "v[26:27],v4 offset:31744\n"
MF "a[80:83],v[16:19],v[92:95],a[80:83]\n"
"s_nop 0\n"
TR "v[28:29],v5 offset:25600\n"
TR "v[30:31],v5 offset:31744\n"
MF "a[100:103],v[20:23],v[92:95],a[100:103]\n"
"v_add_f32 v56,v61,v88\n"
"v_add_f32 v56,v56,v89\n"
MF "a[104:107],v[20:23],v[96:99],a[104:107]\n"
CV "v88,v88,v89\n"
"v_add_f32 v56,v56,v90\n"
MF "a[84:87],v[16:19],v[96:99],a[84:87]\n"
"v_add_f32 v56,v56,v91\n"
CV "v89,v90,v91\n"
MF "a[88:91],v[16:19],v[100:103],a[88:91]\n"
"v_exp_f32 v112,v112\n"
MF "a[108:111],v[20:23],v[100:103],a[108:111]\n"
"v_exp_f32 v113,v113\n"
MF "a[112:115],v[20:23],v[104:107],a[112:115]\n"
"v_exp_f32 v114,v114\n"
MF "a[92:95],v[16:19],v[104:107],a[92:95]\n"
"v_exp_f32 v115,v115\n"
MF "a[96:99],v[16:19],v[108:111],a[96:99]\n"
"v_add_f32 v52,v52,v112\n"
"v_add_f32 v52,v52,v113\n"
MF "a[116:119],v[20:23],v[108:111],a[116:119]\n"
"s_waitcnt lgkmcnt(0)\n"
TR "v[16:17],v2 offset:26624\n"
TR "v[18:19],v2 offset:32768\n"
MF "a[136:139],v[24:27],v[108:111],a[136:139]\n"
"s_nop 0\n"
TR "v[20:21],v3 offset:26624\n"
TR "v[22:23],v3 offset:32768\n"
MF "a[156:159],v[28:31],v[108:111],a[156:159]\n"
CV "v74,v112,v113\n"
"v_add_f32 v52,v52,v114\n"
MF "a[152:155],v[28:31],v[104:107],a[152:155]\n"
"v_add_f32 v52,v52,v115\n"
CV "v75,v114,v115\n"
MF "a[132:135],v[24:27],v[104:107],a[132:135]\n"
"v_exp_f32 v116,v116\n"
MF "a[128:131],v[24:27],v[100:103],a[128:131]\n"
"v_exp_f32 v117,v117\n"
MF "a[148:151],v[28:31],v[100:103],a[148:151]\n"
"v_exp_f32 v118,v118\n"
MF "a[144:147],v[28:31],v[96:99],a[144:147]\n"
"v_exp_f32 v119,v119\n"
MF "a[124:127],v[24:27],v[96:99],a[124:127]\n"
"v_add_f32 v53,v53,v116\n"
"v_add_f32 v53,v53,v117\n"
MF "a[120:123],v[24:27],v[92:95],a[120:123]\n"
CV "v78,v116,v117\n"
"v_add_f32 v53,v53,v118\n"
MF "a[140:143],v[28:31],v[92:95],a[140:143]\n"
"s_waitcnt lgkmcnt(0)\n"
TR "v[24:25],v4 offset:26624\n"
TR "v[26:27],v4 offset:32768\n"
MF "a[160:163],v[16:19],v[92:95],a[160:163]\n"
"s_nop 0\n"
TR "v[28:29],v5 offset:26624\n"
TR "v[30:31],v5 offset:32768\n"
MF "a[180:183],v[20:23],v[92:95],a[180:183]\n"
"v_add_f32 v53,v53,v119\n"
CV "v79,v118,v119\n"
MF "a[184:187],v[20:23],v[96:99],a[184:187]\n"
"v_exp_f32 v120,v120\n"
MF "a[164:167],v[16:19],v[96:99],a[164:167]\n"
"v_exp_f32 v121,v121\n"
MF "a[168:171],v[16:19],v[100:103],a[168:171]\n"
"v_exp_f32 v122,v122\n"
MF "a[188:191],v[20:23],v[100:103],a[188:191]\n"
"v_exp_f32 v123,v123\n"
MF "a[192:195],v[20:23],v[104:107],a[192:195]\n"
"v_add_f32 v54,v54,v120\n"
"v_add_f32 v54,v54,v121\n"
MF "a[172:175],v[16:19],v[104:107],a[172:175]\n"
"s_waitcnt vmcnt(12) lgkmcnt(0)\n"
CV "v82,v120,v121\n"
"v_add_f32 v54,v54,v122\n"
MF "a[176:179],v[16:19],v[108:111],a[176:179]\n"
"s_barrier\n"
MF "a[196:199],v[20:23],v[108:111],a[196:199]\n"
"s_add_u32 s73,s73,12288\n"
"ds_read_b128 v[16:19],v0 offset:24576\n"
"ds_read_b128 v[20:23],v1 offset:24576\n"
MF "a[216:219],v[24:27],v[108:111],a[216:219]\n"
"s_cmp_eq_u32 s73,s19\n"
"s_cselect_b32 s73,s18,s73\n"
"s_add_u32 m0,s65,73728\n"
MF "a[236:239],v[28:31],v[108:111],a[236:239]\n"
LD "v8,s[36:39],s73 offen nt lds\n"
MF "a[232:235],v[28:31],v[104:107],a[232:235]\n"
"s_add_u32 m0,s65,74624\n"
"v_add_f32 v54,v54,v123\n"
CV "v83,v122,v123\n"
MF "a[212:215],v[24:27],v[104:107],a[212:215]\n"
LD "v8,s[36:39],s73 offen offset:128 nt lds\n"
MF "a[208:211],v[24:27],v[100:103],a[208:211]\n"
"s_add_u32 m0,s65,75520\n"
MF "a[228:231],v[28:31],v[100:103],a[228:231]\n"
LD "v8,s[36:39],s73 offen offset:256 nt lds\n"
MF "a[224:227],v[28:31],v[96:99],a[224:227]\n"
MF "a[204:207],v[24:27],v[96:99],a[204:207]\n"
MF "a[200:203],v[24:27],v[92:95],a[200:203]\n"
MF "a[220:223],v[28:31],v[92:95],a[220:223]\n"
"s_waitcnt lgkmcnt(0)\n"
MF "v[92:95],v[16:19],v[136:139],v[32:35]\n"
"s_add_u32 m0,s65,73728\n"
"ds_read_b128 v[24:27],v0 offset:25600\n"
"ds_read_b128 v[28:31],v1 offset:25600\n"
MF "v[92:95],v[20:23],v[156:159],v[92:95]\n"
LD "v8,s[36:39],s73 offen offset:3072 nt lds\n"
MF "v[96:99],v[20:23],v[160:163],v[36:39]\n"
"v_exp_f32 v124,v124\n"
MF "v[96:99],v[16:19],v[140:143],v[96:99]\n"
"v_exp_f32 v125,v125\n"
MF "v[100:103],v[16:19],v[144:147],v[40:43]\n"
"s_add_u32 m0,s65,74624\n"
"v_exp_f32 v126,v126\n"
MF "v[100:103],v[20:23],v[164:167],v[100:103]\n"
LD "v8,s[36:39],s73 offen offset:3200 nt lds\n"
MF "v[104:107],v[20:23],v[168:171],v[44:47]\n"
"v_exp_f32 v127,v127\n"
MF "v[104:107],v[16:19],v[148:151],v[104:107]\n"
"s_add_u32 m0,s65,75520\n"
"v_add_f32 v55,v55,v124\n"
"v_add_f32 v55,v55,v125\n"
MF "v[108:111],v[16:19],v[152:155],v[48:51]\n"
LD "v8,s[36:39],s73 offen offset:3328 nt lds\n"
MF "v[108:111],v[20:23],v[172:175],v[108:111]\n"
"s_waitcnt lgkmcnt(0)\n"
"ds_read_b128 v[16:19],v0 offset:26624\n"
"ds_read_b128 v[20:23],v1 offset:26624\n"
MF "v[108:111],v[24:27],v[192:195],v[108:111]\n"
CV "v86,v124,v125\n"
"v_add_f32 v55,v55,v126\n"
MF "v[108:111],v[28:31],v[212:215],v[108:111]\n"
"v_add_f32 v55,v55,v127\n"
CV "v87,v126,v127\n"
MF "v[104:107],v[28:31],v[208:211],v[104:107]\n"
"v_exp_f32 v128,v128\n"
MF "v[104:107],v[24:27],v[188:191],v[104:107]\n"
"v_exp_f32 v129,v129\n"
MF "v[100:103],v[24:27],v[184:187],v[100:103]\n"
"v_exp_f32 v130,v130\n"
MF "v[100:103],v[28:31],v[204:207],v[100:103]\n"
"v_exp_f32 v131,v131\n"
MF "v[96:99],v[28:31],v[200:203],v[96:99]\n"
"v_add_f32 v56,v56,v128\n"
"v_add_f32 v56,v56,v129\n"
MF "v[96:99],v[24:27],v[180:183],v[96:99]\n"
CV "v90,v128,v129\n"
"v_add_f32 v56,v56,v130\n"
MF "v[92:95],v[24:27],v[176:179],v[92:95]\n"
"v_add_f32 v56,v56,v131\n"
CV "v91,v130,v131\n"
MF "v[92:95],v[28:31],v[196:199],v[92:95]\n"
"s_waitcnt lgkmcnt(0)\n"
"ds_read_b128 v[24:27],v0 offset:30720\n"
"ds_read_b128 v[28:31],v1 offset:30720\n"
MF "v[92:95],v[16:19],v[216:219],v[92:95]\n"
MF "v[92:95],v[20:23],v[236:239],v[92:95]\n"
MF "v[96:99],v[20:23],v[240:243],v[96:99]\n"
MF "v[96:99],v[16:19],v[220:223],v[96:99]\n"
MF "v[100:103],v[16:19],v[224:227],v[100:103]\n"
MF "v[100:103],v[20:23],v[244:247],v[100:103]\n"
MF "v[104:107],v[20:23],v[248:251],v[104:107]\n"
MF "v[104:107],v[16:19],v[228:231],v[104:107]\n"
"v_max3_f32 v10,v52,v53,v54\n"
"v_max3_f32 v10,v10,v55,v56\n"
MF "v[108:111],v[16:19],v[232:235],v[108:111]\n"
"v_cmp_ngt_f32 vcc,s95,v10\n"
MF "v[108:111],v[20:23],v[252:255],v[108:111]\n"
"s_waitcnt lgkmcnt(0)\n"
"s_cbranch_vccnz .Lstub_7\n"
".Lback_7:\n"
MF "v[112:115],v[24:27],v[136:139],v[32:35]\n"
"s_add_u32 s74,s74,12288\n"
"ds_read_b128 v[16:19],v0 offset:31744\n"
"ds_read_b128 v[20:23],v1 offset:31744\n"
MF "v[112:115],v[28:31],v[156:159],v[112:115]\n"
"s_cmp_eq_u32 s74,s20\n"
"s_cselect_b32 s74,s70,s74\n"
MF "v[116:119],v[28:31],v[160:163],v[36:39]\n"
"v_exp_f32 v92,v92\n"
MF "v[116:119],v[24:27],v[140:143],v[116:119]\n"
"v_exp_f32 v93,v93\n"
MF "v[120:123],v[24:27],v[144:147],v[40:43]\n"
"v_exp_f32 v94,v94\n"
MF "v[120:123],v[28:31],v[164:167],v[120:123]\n"
"v_exp_f32 v95,v95\n"
MF "v[124:127],v[28:31],v[168:171],v[44:47]\n"
"v_add_f32 v57,v52,v92\n"
"v_add_f32 v57,v57,v93\n"
MF "v[124:127],v[24:27],v[148:151],v[124:127]\n"
CV "v92,v92,v93\n"
"v_add_f32 v57,v57,v94\n"
MF "v[128:131],v[24:27],v[152:155],v[48:51]\n"
"v_add_f32 v57,v57,v95\n"
CV "v93,v94,v95\n"
MF "v[128:131],v[28:31],v[172:175],v[128:131]\n"
"s_waitcnt lgkmcnt(0)\n"
"ds_read_b128 v[24:27],v0 offset:32768\n"
"ds_read_b128 v[28:31],v1 offset:32768\n"
MF "v[128:131],v[16:19],v[192:195],v[128:131]\n"
"s_sub_u32 s61,s61,1\n"
"v_exp_f32 v96,v96\n"
MF "v[128:131],v[20:23],v[212:215],v[128:131]\n"
"s_cmp_eq_u32 s61,1\n"
"s_cselect_b32 s95,0,s95\n"
"v_add_f32 v58,v53,v96\n"
MF "v[124:127],v[20:23],v[208:211],v[124:127]\n"
"v_exp_f32 v97,v97\n"
MF "v[124:127],v[16:19],v[188:191],v[124:127]\n"
"v_exp_f32 v98,v98\n"
MF "v[120:123],v[16:19],v[184:187],v[120:123]\n"
"v_exp_f32 v99,v99\n"
MF "v[120:123],v[20:23],v[204:207],v[120:123]\n"
"v_add_f32 v58,v58,v97\n"
CV "v96,v96,v97\n"
MF "v[116:119],v[20:23],v[200:203],v[116:119]\n"
"s_waitcnt vmcnt(12) lgkmcnt(0)\n"
"v_add_f32 v58,v58,v98\n"
"v_add_f32 v58,v58,v99\n"
MF "v[116:119],v[16:19],v[180:183],v[116:119]\n"
"s_barrier\n"
MF "v[112:115],v[16:19],v[176:179],v[112:115]\n"
CV "v97,v98,v99\n"
MF "v[112:115],v[20:23],v[196:199],v[112:115]\n"
"s_nop 0\n"
TR "v[16:17],v2\n"
TR "v[18:19],v2 offset:6144\n"
MF "v[112:115],v[24:27],v[216:219],v[112:115]\n"
"s_add_u32 s72,s72,6144\n"
TR "v[20:21],v3\n"
TR "v[22:23],v3 offset:6144\n"
MF "v[112:115],v[28:31],v[236:239],v[112:115]\n"
"s_cmp_eq_u32 s72,s17\n"
"s_cselect_b32 s72,s16,s72\n"
"s_add_u32 m0,s64,24576\n"
MF "v[116:119],v[28:31],v[240:243],v[116:119]\n"
LD "v7,s[32:35],s72 offen nt lds\n"
MF "v[116:119],v[24:27],v[220:223],v[116:119]\n"
"s_add_u32 m0,s64,25472\n"
"v_exp_f32 v100,v100\n"
MF "v[120:123],v[24:27],v[224:227],v[120:123]\n"
LD "v7,s[32:35],s72 offen offset:128 nt lds\n"
MF "v[120:123],v[28:31],v[244:247],v[120:123]\n"
"v_exp_f32 v101,v101\n"
MF "v[124:127],v[28:31],v[248:251],v[124:127]\n"
"s_add_u32 m0,s64,26368\n"
"v_exp_f32 v102,v102\n"
MF "v[124:127],v[24:27],v[228:231],v[124:127]\n"
LD "v7,s[32:35],s72 offen offset:256 nt lds\n"
MF "v[128:131],v[24:27],v[232:235],v[128:131]\n"
"s_add_u32 s72,s72,6144\n"
"v_exp_f32 v103,v103\n"
MF "v[128:131],v[28:31],v[252:255],v[128:131]\n"
"s_waitcnt lgkmcnt(0)\n"
TR "v[24:25],v4\n"
TR "v[26:27],v4 offset:6144\n"
MF "a[0:3],v[16:19],v[72:75],a[0:3]\n"
"s_add_u32 m0,s64,30720\n"
TR "v[28:29],v5\n"
TR "v[30:31],v5 offset:6144\n"
MF "a[20:23],v[20:23],v[72:75],a[20:23]\n"
LD "v7,s[32:35],s72 offen nt lds\n"
MF "a[24:27],v[20:23],v[76:79],a[24:27]\n"
"v_add_f32 v59,v54,v100\n"
"v_add_f32 v59,v59,v101\n"
MF "a[4:7],v[16:19],v[76:79],a[4:7]\n"
"s_add_u32 m0,s64,31616\n"
CV "v100,v100,v101\n"
"v_add_f32 v59,v59,v102\n"
MF "a[8:11],v[16:19],v[80:83],a[8:11]\n"
LD "v7,s[32:35],s72 offen offset:128 nt lds\n"
MF "a[28:31],v[20:23],v[80:83],a[28:31]\n"
"v_add_f32 v59,v59,v103\n"
CV "v101,v102,v103\n"
MF "a[32:35],v[20:23],v[84:87],a[32:35]\n"
"s_add_u32 m0,s64,32512\n"
"v_exp_f32 v104,v104\n"
MF "a[12:15],v[16:19],v[84:87],a[12:15]\n"
LD "v7,s[32:35],s72 offen offset:256 nt lds\n"
MF "a[16:19],v[16:19],v[88:91],a[16:19]\n"
"v_exp_f32 v105,v105\n"
MF "a[36:39],v[20:23],v[88:91],a[36:39]\n"
"s_waitcnt lgkmcnt(0)\n"
TR "v[16:17],v2 offset:1024\n"
TR "v[18:19],v2 offset:7168\n"
MF "a[56:59],v[24:27],v[88:91],a[56:59]\n"
"s_nop 0\n"
TR "v[20:21],v3 offset:1024\n"
TR "v[22:23],v3 offset:7168\n"
MF "a[76:79],v[28:31],v[88:91],a[76:79]\n"
"v_exp_f32 v106,v106\n"
MF "a[72:75],v[28:31],v[84:87],a[72:75]\n"
"v_exp_f32 v107,v107\n"
MF "a[52:55],v[24:27],v[84:87],a[52:55]\n"
"v_add_f32 v60,v55,v104\n"
"v_add_f32 v60,v60,v105\n"
MF "a[48:51],v[24:27],v[80:83],a[48:51]\n"
CV "v104,v104,v105\n"
"v_add_f32 v60,v60,v106\n"
MF "a[68:71],v[28:31],v[80:83],a[68:71]\n"
"v_add_f32 v60,v60,v107\n"
CV "v105,v106,v107\n"
MF "a[64:67],v[28:31],v[76:79],a[64:67]\n"
".Lpg11b:\n"
"s_cbranch_execz .Lpgqj_a\n"
".Lpg11d:\n"
"s_cbranch_execz .Lisr2\n"
"v_exp_f32 v108,v108\n"
MF "a[44:47],v[24:27],v[76:79],a[44:47]\n"
"v_exp_f32 v109,v109\n"
MF "a[40:43],v[24:27],v[72:75],a[40:43]\n"
"v_exp_f32 v110,v110\n"
MF "a[60:63],v[28:31],v[72:75],a[60:63]\n"
"s_waitcnt lgkmcnt(0)\n"
TR "v[24:25],v4 offset:1024\n"
TR "v[26:27],v4 offset:7168\n"
MF "a[80:83],v[16:19],v[72:75],a[80:83]\n"
"s_nop 0\n"
TR "v[28:29],v5 offset:1024\n"
TR "v[30:31],v5 offset:7168\n"
MF "a[100:103],v[20:23],v[72:75],a[100:103]\n"
"v_exp_f32 v111,v111\n"
MF "a[104:107],v[20:23],v[76:79],a[104:107]\n"
"v_add_f32 v61,v56,v108\n"
"v_add_f32 v61,v61,v109\n"
MF "a[84:87],v[16:19],v[76:79],a[84:87]\n"
CV "v108,v108,v109\n"
"v_add_f32 v61,v61,v110\n"
MF "a[88:91],v[16:19],v[80:83],a[88:91]\n"
"v_add_f32 v61,v61,v111\n"
CV "v109,v110,v111\n"
MF "a[108:111],v[20:23],v[80:83],a[108:111]\n"
"v_exp_f32 v112,v112\n"
MF "a[112:115],v[20:23],v[84:87],a[112:115]\n"
"v_exp_f32 v113,v113\n"
MF "a[92:95],v[16:19],v[84:87],a[92:95]\n"
"v_exp_f32 v114,v114\n"
MF "a[96:99],v[16:19],v[88:91],a[96:99]\n"
"v_exp_f32 v115,v115\n"
MF "a[116:119],v[20:23],v[88:91],a[116:119]\n"
"s_waitcnt lgkmcnt(0)\n"
TR "v[16:17],v2 offset:2048\n"
TR "v[18:19],v2 offset:8192\n"
MF "a[136:139],v[24:27],v[88:91],a[136:139]\n"
"s_nop 0\n"
TR "v[20:21],v3 offset:2048\n"
TR "v[22:23],v3 offset:8192\n"
MF "a[156:159],v[28:31],v[88:91],a[156:159]\n"
"v_add_f32 v57,v57,v112\n"
"v_add_f32 v57,v57,v113\n"
MF "a[152:155],v[28:31],v[84:87],a[152:155]\n"
CV "v94,v112,v113\n"
"v_add_f32 v57,v57,v114\n"
MF "a[132:135],v[24:27],v[84:87],a[132:135]\n"
"v_add_f32 v57,v57,v115\n"
CV "v95,v114,v115\n"
MF "a[128:131],v[24:27],v[80:83],a[128:131]\n"
"v_exp_f32 v116,v116\n"
MF "a[148:151],v[28:31],v[80:83],a[148:151]\n"
"v_exp_f32 v117,v117\n"
MF "a[144:147],v[28:31],v[76:79],a[144:147]\n"
"v_exp_f32 v118,v118\n"
MF "a[124:127],v[24:27],v[76:79],a[124:127]\n"
"v_exp_f32 v119,v119\n"
MF "a[120:123],v[24:27],v[72:75],a[120:123]\n"
"v_add_f32 v58,v58,v116\n"
"v_add_f32 v58,v58,v117\n"
MF "a[140:143],v[28:31],v[72:75],a[140:143]\n"
"s_waitcnt lgkmcnt(0)\n"
TR "v[24:25],v4 offset:2048\n"
TR "v[26:27],v4 offset:8192\n"
MF "a[160:163],v[16:19],v[72:75],a[160:163]\n"
"s_nop 0\n"
TR "v[28:29],v5 offset:2048\n"
TR "v[30:31],v5 offset:8192\n"
MF "a[180:183],v[20:23],v[72:75],a[180:183]\n"
CV "v98,v116,v117\n"
"v_add_f32 v58,v58,v118\n"
MF "a[184:187],v[20:23],v[76:79],a[184:187]\n"
"v_add_f32 v58,v58,v119\n"
CV "v99,v118,v119\n"
MF "a[164:167],v[16:19],v[76:79],a[164:167]\n"
"v_exp_f32 v120,v120\n"
MF "a[168:171],v[16:19],v[80:83],a[168:171]\n"
"v_exp_f32 v121,v121\n"
MF "a[188:191],v[20:23],v[80:83],a[188:191]\n"
"v_exp_f32 v122,v122\n"
MF "a[192:195],v[20:23],v[84:87],a[192:195]\n"
"v_exp_f32 v123,v123\n"
MF "a[172:175],v[16:19],v[84:87],a[172:175]\n"
"s_waitcnt vmcnt(12) lgkmcnt(0)\n"
"v_add_f32 v59,v59,v120\n"
"v_add_f32 v59,v59,v121\n"
MF "a[176:179],v[16:19],v[88:91],a[176:179]\n"
"s_barrier\n"
MF "a[196:199],v[20:23],v[88:91],a[196:199]\n"
"s_add_u32 s73,s73,12288\n"
"ds_read_b128 v[16:19],v0\n"
"ds_read_b128 v[20:23],v1\n"
MF "a[216:219],v[24:27],v[88:91],a[216:219]\n"
"s_cmp_eq_u32 s73,s19\n"
"s_cselect_b32 s73,s18,s73\n"
"s_add_u32 m0,s65,49152\n"
MF "a[236:239],v[28:31],v[88:91],a[236:239]\n"
LD "v8,s[36:39],s73 offen nt lds\n"
MF "a[232:235],v[28:31],v[84:87],a[232:235]\n"
"s_add_u32 m0,s65,50048\n"
CV "v102,v120,v121\n"
"v_add_f32 v59,v59,v122\n"
MF "a[212:215],v[24:27],v[84:87],a[212:215]\n"
LD "v8,s[36:39],s73 offen offset:128 nt lds\n"
MF "a[208:211],v[24:27],v[80:83],a[208:211]\n"
"s_add_u32 m0,s65,50944\n"
"v_add_f32 v59,v59,v123\n"
CV "v103,v122,v123\n"
MF "a[228:231],v[28:31],v[80:83],a[228:231]\n"
LD "v8,s[36:39],s73 offen offset:256 nt lds\n"
MF "a[224:227],v[28:31],v[76:79],a[224:227]\n"
MF "a[204:207],v[24:27],v[76:79],a[204:207]\n"
"s_waitcnt lgkmcnt(0)\n"
MF "a[200:203],v[24:27],v[72:75],a[200:203]\n"
"s_cmp_lg_u32 s61,0\n"
MF "a[220:223],v[28:31],v[72:75],a[220:223]\n"
"s_cbranch_scc1 .Laloopx\n"
".Lepi1:\n"
"v_exp_f32 v124,v124\n"
"v_exp_f32 v125,v125\n"
"v_exp_f32 v126,v126\n"
"v_exp_f32 v127,v127\n"
"v_exp_f32 v128,v128\n"
"v_exp_f32 v129,v129\n"
"v_exp_f32 v130,v130\n"
"v_exp_f32 v131,v131\n"
"v_add_f32 v60,v60,v124\n"
"v_add_f32 v61,v61,v128\n"
"v_add_f32 v60,v60,v125\n"
"v_add_f32 v61,v61,v129\n"
"v_add_f32 v60,v60,v126\n"
"v_add_f32 v61,v61,v130\n"
"v_add_f32 v60,v60,v127\n"
"v_add_f32 v61,v61,v131\n"
CV "v106,v124,v125\n"
CV "v107,v126,v127\n"
CV "v110,v128,v129\n"
CV "v111,v130,v131\n"
"v_max3_f32 v10,v57,v58,v59\n"
"v_max3_f32 v10,v10,v60,v61\n"
"v_cmp_le_f32 vcc,0x47800000,v10\n"
"s_cbranch_vccnz .Lstub_8\n"
".Lback_8:\n"
"s_waitcnt vmcnt(9) lgkmcnt(0)\n"
"s_barrier\n"
"s_cmp_eq_u32 s57,0\n"
"s_cbranch_scc0 .Lep\n"
".Lepx:\n"
"s_add_u32 s66,s67,4718592\n"
"s_sub_u32 s81,s81,384\n"
"s_mov_b32 s83,7077888\n"
AR "v122,a243\n"
"v_add_u32 v123,97280,v122\n"
"v_add_u32 v124,194560,v122\n"
"v_add_u32 v125,291840,v122\n"
"v_mov_b32 v10,1.0\n"
TR "v[136:137],v2 offset:24576\n"
TR "v[138:139],v2 offset:30720\n"
"v_mfma_f32_16x16x4_f32 v[16:19],v10,v57,0\n"
TR "v[140:141],v3 offset:24576\n"
TR "v[142:143],v3 offset:30720\n"
TR "v[144:145],v4 offset:24576\n"
TR "v[146:147],v4 offset:30720\n"
TR "v[148:149],v5 offset:24576\n"
TR "v[150:151],v5 offset:30720\n"
"v_mfma_f32_16x16x4_f32 v[20:23],v10,v58,0\n"
TR "v[152:153],v2 offset:25600\n"
TR "v[154:155],v2 offset:31744\n"
TR "v[156:157],v3 offset:25600\n"
TR "v[158:159],v3 offset:31744\n"
TR "v[160:161],v4 offset:25600\n"
TR "v[162:163],v4 offset:31744\n"
"v_mfma_f32_16x16x4_f32 v[24:27],v10,v59,0\n"
TR "v[164:165],v5 offset:25600\n"
TR "v[166:167],v5 offset:31744\n"
TR "v[168:169],v2 offset:26624\n"
TR "v[170:171],v2 offset:32768\n"
TR "v[172:173],v3 offset:26624\n"
TR "v[174:175],v3 offset:32768\n"
"v_mfma_f32_16x16x4_f32 v[28:31],v10,v60,0\n"
TR "v[176:177],v4 offset:26624\n"
TR "v[178:179],v4 offset:32768\n"
TR "v[180:181],v5 offset:26624\n"
TR "v[182:183],v5 offset:32768\n"
"v_mfma_f32_16x16x4_f32 v[32:35],v10,v61,0\n"
"s_add_u32 m0,s62,0\n"
"s_add_u32 s82,s66,2359296\n"
"s_sub_u32 s15,s82,7864320\n"
"s_min_u32 s82,s82,s15\n"
LD "v122,s[28:31],s82 offen lds\n"
LD "v123,s[28:31],s82 offen offset:1024 lds\n"
"s_waitcnt lgkmcnt(15)\n"
MF "a[0:3],v[136:139],v[92:95],a[0:3]\n"
MF "a[20:23],v[140:143],v[92:95],a[20:23]\n"
LD "v124,s[28:31],s82 offen offset:2048 lds\n"
LD "v125,s[28:31],s82 offen offset:3072 lds\n"
"v_rcp_f32 v57,v16\n"
"v_rcp_f32 v58,v20\n"
"v_rcp_f32 v59,v24\n"
"v_rcp_f32 v60,v28\n"
"v_rcp_f32 v61,v32\n"
MF "a[4:7],v[136:139],v[96:99],a[4:7]\n"
"v_mul_f32_e64 v52,-s13,v57\n"
"v_mul_f32_e64 v53,-s13,v58\n"
"v_mul_f32_e64 v54,-s13,v59\n"
"v_mul_f32_e64 v55,-s13,v60\n"
"v_mul_f32_e64 v56,-s13,v61\n"
AR "v72,a0\n"
AR "v73,a1\n"
AR "v74,a2\n"
MF "a[24:27],v[140:143],v[96:99],a[24:27]\n"
AR "v75,a3\n"
AR "v76,a20\n"
AR "v77,a21\n"
AR "v78,a22\n"
AR "v79,a23\n"
"v_pk_fma_f32 v[72:73],v[72:73],v[56:57],v[52:53] op_sel:[0,1,0] op_sel_hi:[1,1,0]\n"
"v_pk_fma_f32 v[74:75],v[74:75],v[56:57],v[52:53] op_sel:[0,1,0] op_sel_hi:[1,1,0]\n"
"v_pk_fma_f32 v[76:77],v[76:77],v[56:57],v[52:53] op_sel:[0,1,0] op_sel_hi:[1,1,0]\n"
"v_pk_fma_f32 v[78:79],v[78:79],v[56:57],v[52:53] op_sel:[0,1,0] op_sel_hi:[1,1,0]\n"
CV "v112,v72,v73\n"
CV "v113,v74,v75\n"
CV "v114,v76,v77\n"
CV "v115,v78,v79\n"
MF "a[8:11],v[136:139],v[100:103],a[8:11]\n"
AW "a0,v112\n"
AW "a1,v113\n"
AW "a2,v114\n"
AW "a3,v115\n"
"s_add_u32 m0,s62,16384\n"
"s_add_u32 s82,s66,2359424\n"
"s_sub_u32 s15,s82,7864320\n"
"s_min_u32 s82,s82,s15\n"
"s_nop 0\n"
LD "v122,s[28:31],s82 offen lds\n"
AR "v80,a4\n"
AR "v81,a5\n"
AR "v82,a6\n"
MF "a[28:31],v[140:143],v[100:103],a[28:31]\n"
AR "v83,a7\n"
AR "v84,a24\n"
AR "v85,a25\n"
AR "v86,a26\n"
AR "v87,a27\n"
"v_pk_fma_f32 v[80:81],v[80:81],v[58:59],v[52:53] op_sel:[0,0,1] op_sel_hi:[1,0,1]\n"
"v_pk_fma_f32 v[82:83],v[82:83],v[58:59],v[52:53] op_sel:[0,0,1] op_sel_hi:[1,0,1]\n"
"v_pk_fma_f32 v[84:85],v[84:85],v[58:59],v[52:53] op_sel:[0,0,1] op_sel_hi:[1,0,1]\n"
"v_pk_fma_f32 v[86:87],v[86:87],v[58:59],v[52:53] op_sel:[0,0,1] op_sel_hi:[1,0,1]\n"
CV "v116,v80,v81\n"
CV "v117,v82,v83\n"
CV "v118,v84,v85\n"
CV "v119,v86,v87\n"
MF "a[12:15],v[136:139],v[104:107],a[12:15]\n"
AW "a4,v116\n"
AW "a5,v117\n"
AW "a6,v118\n"
AW "a7,v119\n"
LD "v123,s[28:31],s82 offen offset:1024 lds\n"
AR "v72,a8\n"
AR "v73,a9\n"
AR "v74,a10\n"
MF "a[32:35],v[140:143],v[104:107],a[32:35]\n"
AR "v75,a11\n"
AR "v76,a28\n"
AR "v77,a29\n"
AR "v78,a30\n"
AR "v79,a31\n"
"v_pk_fma_f32 v[72:73],v[72:73],v[58:59],v[54:55] op_sel:[0,1,0] op_sel_hi:[1,1,0]\n"
"v_pk_fma_f32 v[74:75],v[74:75],v[58:59],v[54:55] op_sel:[0,1,0] op_sel_hi:[1,1,0]\n"
"v_pk_fma_f32 v[76:77],v[76:77],v[58:59],v[54:55] op_sel:[0,1,0] op_sel_hi:[1,1,0]\n"
"v_pk_fma_f32 v[78:79],v[78:79],v[58:59],v[54:55] op_sel:[0,1,0] op_sel_hi:[1,1,0]\n"
CV "v112,v72,v73\n"
CV "v113,v74,v75\n"
CV "v114,v76,v77\n"
CV "v115,v78,v79\n"
MF "a[16:19],v[136:139],v[108:111],a[16:19]\n"
AW "a8,v112\n"
AW "a9,v113\n"
AW "a10,v114\n"
AW "a11,v115\n"
"s_waitcnt lgkmcnt(0)\n"
"s_barrier\n"
AR "v80,a12\n"
AR "v81,a13\n"
AR "v82,a14\n"
MF "a[36:39],v[140:143],v[108:111],a[36:39]\n"
AR "v83,a15\n"
AR "v84,a32\n"
AR "v85,a33\n"
AR "v86,a34\n"
AR "v87,a35\n"
"v_pk_fma_f32 v[80:81],v[80:81],v[60:61],v[54:55] op_sel:[0,0,1] op_sel_hi:[1,0,1]\n"
"v_pk_fma_f32 v[82:83],v[82:83],v[60:61],v[54:55] op_sel:[0,0,1] op_sel_hi:[1,0,1]\n"
"v_pk_fma_f32 v[84:85],v[84:85],v[60:61],v[54:55] op_sel:[0,0,1] op_sel_hi:[1,0,1]\n"
"v_pk_fma_f32 v[86:87],v[86:87],v[60:61],v[54:55] op_sel:[0,0,1] op_sel_hi:[1,0,1]\n"
CV "v116,v80,v81\n"
CV "v117,v82,v83\n"
CV "v118,v84,v85\n"
CV "v119,v86,v87\n"
MF "a[40:43],v[144:147],v[92:95],a[40:43]\n"
AW "a12,v116\n"
AW "a13,v117\n"
AW "a14,v118\n"
AW "a15,v119\n"
LD "v124,s[28:31],s82 offen offset:2048 lds\n"
AR "v72,a16\n"
AR "v73,a17\n"
AR "v74,a18\n"
MF "a[60:63],v[148:151],v[92:95],a[60:63]\n"
AR "v75,a19\n"
AR "v76,a36\n"
AR "v77,a37\n"
AR "v78,a38\n"
AR "v79,a39\n"
"v_pk_fma_f32 v[72:73],v[72:73],v[60:61],v[56:57] op_sel:[0,1,0] op_sel_hi:[1,1,0]\n"
"v_pk_fma_f32 v[74:75],v[74:75],v[60:61],v[56:57] op_sel:[0,1,0] op_sel_hi:[1,1,0]\n"
"v_pk_fma_f32 v[76:77],v[76:77],v[60:61],v[56:57] op_sel:[0,1,0] op_sel_hi:[1,1,0]\n"
"v_pk_fma_f32 v[78:79],v[78:79],v[60:61],v[56:57] op_sel:[0,1,0] op_sel_hi:[1,1,0]\n"
CV "v112,v72,v73\n"
CV "v113,v74,v75\n"
CV "v114,v76,v77\n"
CV "v115,v78,v79\n"
MF "a[44:47],v[144:147],v[96:99],a[44:47]\n"
AW "a16,v112\n"
AW "a17,v113\n"
AW "a18,v114\n"
AW "a19,v115\n"
LD "v125,s[28:31],s82 offen offset:3072 lds\n"
AR "v80,a40\n"
AR "v81,a41\n"
AR "v82,a42\n"
MF "a[64:67],v[148:151],v[96:99],a[64:67]\n"
AR "v83,a43\n"
AR "v84,a60\n"
AR "v85,a61\n"
AR "v86,a62\n"
AR "v87,a63\n"
"v_pk_fma_f32 v[80:81],v[80:81],v[56:57],v[52:53] op_sel:[0,1,0] op_sel_hi:[1,1,0]\n"
"v_pk_fma_f32 v[82:83],v[82:83],v[56:57],v[52:53] op_sel:[0,1,0] op_sel_hi:[1,1,0]\n"
"v_pk_fma_f32 v[84:85],v[84:85],v[56:57],v[52:53] op_sel:[0,1,0] op_sel_hi:[1,1,0]\n"
"v_pk_fma_f32 v[86:87],v[86:87],v[56:57],v[52:53] op_sel:[0,1,0] op_sel_hi:[1,1,0]\n"
CV "v116,v80,v81\n"
CV "v117,v82,v83\n"
CV "v118,v84,v85\n"
CV "v119,v86,v87\n"
MF "a[48:51],v[144:147],v[100:103],a[48:51]\n"
AW "a40,v116\n"
AW "a41,v117\n"
AW "a42,v118\n"
AW "a43,v119\n"
AR "v72,a44\n"
AR "v73,a45\n"
AR "v74,a46\n"
MF "a[68:71],v[148:151],v[100:103],a[68:71]\n"
AR "v75,a47\n"
AR "v76,a64\n"
AR "v77,a65\n"
AR "v78,a66\n"
AR "v79,a67\n"
"v_pk_fma_f32 v[72:73],v[72:73],v[58:59],v[52:53] op_sel:[0,0,1] op_sel_hi:[1,0,1]\n"
"v_pk_fma_f32 v[74:75],v[74:75],v[58:59],v[52:53] op_sel:[0,0,1] op_sel_hi:[1,0,1]\n"
"v_pk_fma_f32 v[76:77],v[76:77],v[58:59],v[52:53] op_sel:[0,0,1] op_sel_hi:[1,0,1]\n"
"v_pk_fma_f32 v[78:79],v[78:79],v[58:59],v[52:53] op_sel:[0,0,1] op_sel_hi:[1,0,1]\n"
CV "v112,v72,v73\n"
CV "v113,v74,v75\n"
CV "v114,v76,v77\n"
CV "v115,v78,v79\n"
MF "a[52:55],v[144:147],v[104:107],a[52:55]\n"
AW "a44,v112\n"
AW "a45,v113\n"
AW "a46,v114\n"
AW "a47,v115\n"
"s_add_u32 m0,s62,32768\n"
"s_add_u32 s82,s66,2359552\n"
"s_sub_u32 s15,s82,7864320\n"
"s_min_u32 s82,s82,s15\n"
"s_nop 0\n"
LD "v122,s[28:31],s82 offen lds\n"
AR "v80,a48\n"
AR "v81,a49\n"
AR "v82,a50\n"
MF "a[72:75],v[148:151],v[104:107],a[72:75]\n"
AR "v83,a51\n"
AR "v84,a68\n"
AR "v85,a69\n"
AR "v86,a70\n"
AR "v87,a71\n"
"v_pk_fma_f32 v[80:81],v[80:81],v[58:59],v[54:55] op_sel:[0,1,0] op_sel_hi:[1,1,0]\n"
"v_pk_fma_f32 v[82:83],v[82:83],v[58:59],v[54:55] op_sel:[0,1,0] op_sel_hi:[1,1,0]\n"
"v_pk_fma_f32 v[84:85],v[84:85],v[58:59],v[54:55] op_sel:[0,1,0] op_sel_hi:[1,1,0]\n"
"v_pk_fma_f32 v[86:87],v[86:87],v[58:59],v[54:55] op_sel:[0,1,0] op_sel_hi:[1,1,0]\n"
CV "v116,v80,v81\n"
CV "v117,v82,v83\n"
CV "v118,v84,v85\n"
CV "v119,v86,v87\n"
MF "a[56:59],v[144:147],v[108:111],a[56:59]\n"
AW "a48,v116\n"
AW "a49,v117\n"
AW "a50,v118\n"
AW "a51,v119\n"
LD "v123,s[28:31],s82 offen offset:1024 lds\n"
AR "v72,a52\n"
AR "v73,a53\n"
AR "v74,a54\n"
MF "a[76:79],v[148:151],v[108:111],a[76:79]\n"
AR "v75,a55\n"
AR "v76,a72\n"
AR "v77,a73\n"
AR "v78,a74\n"
AR "v79,a75\n"
"v_pk_fma_f32 v[72:73],v[72:73],v[60:61],v[54:55] op_sel:[0,0,1] op_sel_hi:[1,0,1]\n"
"v_pk_fma_f32 v[74:75],v[74:75],v[60:61],v[54:55] op_sel:[0,0,1] op_sel_hi:[1,0,1]\n"
"v_pk_fma_f32 v[76:77],v[76:77],v[60:61],v[54:55] op_sel:[0,0,1] op_sel_hi:[1,0,1]\n"
"v_pk_fma_f32 v[78:79],v[78:79],v[60:61],v[54:55] op_sel:[0,0,1] op_sel_hi:[1,0,1]\n"
CV "v112,v72,v73\n"
CV "v113,v74,v75\n"
CV "v114,v76,v77\n"
CV "v115,v78,v79\n"
MF "a[80:83],v[152:155],v[92:95],a[80:83]\n"
AW "a52,v112\n"
AW "a53,v113\n"
AW "a54,v114\n"
AW "a55,v115\n"
AR "v80,a56\n"
AR "v81,a57\n"
AR "v82,a58\n"
MF "a[100:103],v[156:159],v[92:95],a[100:103]\n"
AR "v83,a59\n"
AR "v84,a76\n"
AR "v85,a77\n"
AR "v86,a78\n"
AR "v87,a79\n"
"v_pk_fma_f32 v[80:81],v[80:81],v[60:61],v[56:57] op_sel:[0,1,0] op_sel_hi:[1,1,0]\n"
"v_pk_fma_f32 v[82:83],v[82:83],v[60:61],v[56:57] op_sel:[0,1,0] op_sel_hi:[1,1,0]\n"
"v_pk_fma_f32 v[84:85],v[84:85],v[60:61],v[56:57] op_sel:[0,1,0] op_sel_hi:[1,1,0]\n"
"v_pk_fma_f32 v[86:87],v[86:87],v[60:61],v[56:57] op_sel:[0,1,0] op_sel_hi:[1,1,0]\n"
CV "v116,v80,v81\n"
CV "v117,v82,v83\n"
CV "v118,v84,v85\n"
CV "v119,v86,v87\n"
MF "a[84:87],v[152:155],v[96:99],a[84:87]\n"
AW "a56,v116\n"
AW "a57,v117\n"
AW "a58,v118\n"
AW "a59,v119\n"
LD "v124,s[28:31],s82 offen offset:2048 lds\n"
AR "v72,a80\n"
AR "v73,a81\n"
AR "v74,a82\n"
MF "a[104:107],v[156:159],v[96:99],a[104:107]\n"
AR "v75,a83\n"
AR "v76,a100\n"
AR "v77,a101\n"
AR "v78,a102\n"
AR "v79,a103\n"
"v_pk_fma_f32 v[72:73],v[72:73],v[56:57],v[52:53] op_sel:[0,1,0] op_sel_hi:[1,1,0]\n"
"v_pk_fma_f32 v[74:75],v[74:75],v[56:57],v[52:53] op_sel:[0,1,0] op_sel_hi:[1,1,0]\n"
"v_pk_fma_f32 v[76:77],v[76:77],v[56:57],v[52:53] op_sel:[0,1,0] op_sel_hi:[1,1,0]\n"
"v_pk_fma_f32 v[78:79],v[78:79],v[56:57],v[52:53] op_sel:[0,1,0] op_sel_hi:[1,1,0]\n"
CV "v112,v72,v73\n"
CV "v113,v74,v75\n"
CV "v114,v76,v77\n"
CV "v115,v78,v79\n"
MF "a[88:91],v[152:155],v[100:103],a[88:91]\n"
AW "a80,v112\n"
AW "a81,v113\n"
AW "a82,v114\n"
AW "a83,v115\n"
LD "v125,s[28:31],s82 offen offset:3072 lds\n"
AR "v80,a84\n"
AR "v81,a85\n"
AR "v82,a86\n"
MF "a[108:111],v[156:159],v[100:103],a[108:111]\n"
AR "v83,a87\n"
AR "v84,a104\n"
AR "v85,a105\n"
AR "v86,a106\n"
AR "v87,a107\n"
"v_pk_fma_f32 v[80:81],v[80:81],v[58:59],v[52:53] op_sel:[0,0,1] op_sel_hi:[1,0,1]\n"
"v_pk_fma_f32 v[82:83],v[82:83],v[58:59],v[52:53] op_sel:[0,0,1] op_sel_hi:[1,0,1]\n"
"v_pk_fma_f32 v[84:85],v[84:85],v[58:59],v[52:53] op_sel:[0,0,1] op_sel_hi:[1,0,1]\n"
"v_pk_fma_f32 v[86:87],v[86:87],v[58:59],v[52:53] op_sel:[0,0,1] op_sel_hi:[1,0,1]\n"
CV "v116,v80,v81\n"
CV "v117,v82,v83\n"
CV "v118,v84,v85\n"
CV "v119,v86,v87\n"
MF "a[92:95],v[152:155],v[104:107],a[92:95]\n"
AW "a84,v116\n"
AW "a85,v117\n"
AW "a86,v118\n"
AW "a87,v119\n"
AR "v72,a88\n"
AR "v73,a89\n"
AR "v74,a90\n"
MF "a[112:115],v[156:159],v[104:107],a[112:115]\n"
AR "v75,a91\n"
AR "v76,a108\n"
AR "v77,a109\n"
AR "v78,a110\n"
AR "v79,a111\n"
"v_pk_fma_f32 v[72:73],v[72:73],v[58:59],v[54:55] op_sel:[0,1,0] op_sel_hi:[1,1,0]\n"
"v_pk_fma_f32 v[74:75],v[74:75],v[58:59],v[54:55] op_sel:[0,1,0] op_sel_hi:[1,1,0]\n"
"v_pk_fma_f32 v[76:77],v[76:77],v[58:59],v[54:55] op_sel:[0,1,0] op_sel_hi:[1,1,0]\n"
"v_pk_fma_f32 v[78:79],v[78:79],v[58:59],v[54:55] op_sel:[0,1,0] op_sel_hi:[1,1,0]\n"
CV "v112,v72,v73\n"
CV "v113,v74,v75\n"
CV "v114,v76,v77\n"
CV "v115,v78,v79\n"
MF "a[96:99],v[152:155],v[108:111],a[96:99]\n"
AW "a88,v112\n"
AW "a89,v113\n"
AW "a90,v114\n"
AW "a91,v115\n"
AR "v80,a92\n"
AR "v81,a93\n"
AR "v82,a94\n"
MF "a[116:119],v[156:159],v[108:111],a[116:119]\n"
AR "v83,a95\n"
AR "v84,a112\n"
AR "v85,a113\n"
AR "v86,a114\n"
AR "v87,a115\n"
"v_pk_fma_f32 v[80:81],v[80:81],v[60:61],v[54:55] op_sel:[0,0,1] op_sel_hi:[1,0,1]\n"
"v_pk_fma_f32 v[82:83],v[82:83],v[60:61],v[54:55] op_sel:[0,0,1] op_sel_hi:[1,0,1]\n"
"v_pk_fma_f32 v[84:85],v[84:85],v[60:61],v[54:55] op_sel:[0,0,1] op_sel_hi:[1,0,1]\n"
"v_pk_fma_f32 v[86:87],v[86:87],v[60:61],v[54:55] op_sel:[0,0,1] op_sel_hi:[1,0,1]\n"
CV "v116,v80,v81\n"
CV "v117,v82,v83\n"
CV "v118,v84,v85\n"
CV "v119,v86,v87\n"
MF "a[120:123],v[160:163],v[92:95],a[120:123]\n"
AW "a92,v116\n"
AW "a93,v117\n"
AW "a94,v118\n"
AW "a95,v119\n"
AR "v72,a96\n"
AR "v73,a97\n"
AR "v74,a98\n"
MF "a[140:143],v[164:167],v[92:95],a[140:143]\n"
AR "v75,a99\n"
AR "v76,a116\n"
AR "v77,a117\n"
AR "v78,a118\n"
AR "v79,a119\n"
"v_pk_fma_f32 v[72:73],v[72:73],v[60:61],v[56:57] op_sel:[0,1,0] op_sel_hi:[1,1,0]\n"
"v_pk_fma_f32 v[74:75],v[74:75],v[60:61],v[56:57] op_sel:[0,1,0] op_sel_hi:[1,1,0]\n"
"v_pk_fma_f32 v[76:77],v[76:77],v[60:61],v[56:57] op_sel:[0,1,0] op_sel_hi:[1,1,0]\n"
"v_pk_fma_f32 v[78:79],v[78:79],v[60:61],v[56:57] op_sel:[0,1,0] op_sel_hi:[1,1,0]\n"
CV "v112,v72,v73\n"
CV "v113,v74,v75\n"
CV "v114,v76,v77\n"
CV "v115,v78,v79\n"
MF "a[124:127],v[160:163],v[96:99],a[124:127]\n"
".Lpg12f:\n"
"s_cbranch_execz .Lisr3\n"
AW "a96,v112\n"
AW "a97,v113\n"
AW "a98,v114\n"
"s_nop 0\n"
AW "a99,v115\n"
AR "v80,a120\n"
AR "v81,a121\n"
AR "v82,a122\n"
MF "a[144:147],v[164:167],v[96:99],a[144:147]\n"
AR "v83,a123\n"
AR "v84,a140\n"
AR "v85,a141\n"
AR "v86,a142\n"
AR "v87,a143\n"
"v_pk_fma_f32 v[80:81],v[80:81],v[56:57],v[52:53] op_sel:[0,1,0] op_sel_hi:[1,1,0]\n"
"v_pk_fma_f32 v[82:83],v[82:83],v[56:57],v[52:53] op_sel:[0,1,0] op_sel_hi:[1,1,0]\n"
"v_pk_fma_f32 v[84:85],v[84:85],v[56:57],v[52:53] op_sel:[0,1,0] op_sel_hi:[1,1,0]\n"
"v_pk_fma_f32 v[86:87],v[86:87],v[56:57],v[52:53] op_sel:[0,1,0] op_sel_hi:[1,1,0]\n"
CV "v116,v80,v81\n"
CV "v117,v82,v83\n"
CV "v118,v84,v85\n"
CV "v119,v86,v87\n"
MF "a[128:131],v[160:163],v[100:103],a[128:131]\n"
AW "a120,v116\n"
AW "a121,v117\n"
AW "a122,v118\n"
AW "a123,v119\n"
AR "v72,a124\n"
AR "v73,a125\n"
AR "v74,a126\n"
MF "a[148:151],v[164:167],v[100:103],a[148:151]\n"
AR "v75,a127\n"
AR "v76,a144\n"
AR "v77,a145\n"
AR "v78,a146\n"
AR "v79,a147\n"
"v_pk_fma_f32 v[72:73],v[72:73],v[58:59],v[52:53] op_sel:[0,0,1] op_sel_hi:[1,0,1]\n"
"v_pk_fma_f32 v[74:75],v[74:75],v[58:59],v[52:53] op_sel:[0,0,1] op_sel_hi:[1,0,1]\n"
"v_pk_fma_f32 v[76:77],v[76:77],v[58:59],v[52:53] op_sel:[0,0,1] op_sel_hi:[1,0,1]\n"
"v_pk_fma_f32 v[78:79],v[78:79],v[58:59],v[52:53] op_sel:[0,0,1] op_sel_hi:[1,0,1]\n"
CV "v112,v72,v73\n"
CV "v113,v74,v75\n"
CV "v114,v76,v77\n"
CV "v115,v78,v79\n"
MF "a[132:135],v[160:163],v[104:107],a[132:135]\n"
AW "a124,v112\n"
AW "a125,v113\n"
AW "a126,v114\n"
AW "a127,v115\n"
AR "v80,a128\n"
AR "v81,a129\n"
AR "v82,a130\n"
MF "a[152:155],v[164:167],v[104:107],a[152:155]\n"
AR "v83,a131\n"
AR "v84,a148\n"
AR "v85,a149\n"
AR "v86,a150\n"
AR "v87,a151\n"
"v_pk_fma_f32 v[80:81],v[80:81],v[58:59],v[54:55] op_sel:[0,1,0] op_sel_hi:[1,1,0]\n"
"v_pk_fma_f32 v[82:83],v[82:83],v[58:59],v[54:55] op_sel:[0,1,0] op_sel_hi:[1,1,0]\n"
"v_pk_fma_f32 v[84:85],v[84:85],v[58:59],v[54:55] op_sel:[0,1,0] op_sel_hi:[1,1,0]\n"
"v_pk_fma_f32 v[86:87],v[86:87],v[58:59],v[54:55] op_sel:[0,1,0] op_sel_hi:[1,1,0]\n"
CV "v116,v80,v81\n"
CV "v117,v82,v83\n"
CV "v118,v84,v85\n"
CV "v119,v86,v87\n"
MF "a[136:139],v[160:163],v[108:111],a[136:139]\n"
AW "a128,v116\n"
AW "a129,v117\n"
AW "a130,v118\n"
AW "a131,v119\n"
AR "v72,a132\n"
AR "v73,a133\n"
AR "v74,a134\n"
MF "a[156:159],v[164:167],v[108:111],a[156:159]\n"
AR "v75,a135\n"
AR "v76,a152\n"
AR "v77,a153\n"
AR "v78,a154\n"
AR "v79,a155\n"
"v_pk_fma_f32 v[72:73],v[72:73],v[60:61],v[54:55] op_sel:[0,0,1] op_sel_hi:[1,0,1]\n"
"v_pk_fma_f32 v[74:75],v[74:75],v[60:61],v[54:55] op_sel:[0,0,1] op_sel_hi:[1,0,1]\n"
"v_pk_fma_f32 v[76:77],v[76:77],v[60:61],v[54:55] op_sel:[0,0,1] op_sel_hi:[1,0,1]\n"
"v_pk_fma_f32 v[78:79],v[78:79],v[60:61],v[54:55] op_sel:[0,0,1] op_sel_hi:[1,0,1]\n"
CV "v112,v72,v73\n"
CV "v113,v74,v75\n"
CV "v114,v76,v77\n"
CV "v115,v78,v79\n"
MF "a[160:163],v[168:171],v[92:95],a[160:163]\n"
AW "a132,v112\n"
AW "a133,v113\n"
AW "a134,v114\n"
AW "a135,v115\n"
AR "v80,a136\n"
AR "v81,a137\n"
AR "v82,a138\n"
MF "a[180:183],v[172:175],v[92:95],a[180:183]\n"
AR "v83,a139\n"
AR "v84,a156\n"
AR "v85,a157\n"
AR "v86,a158\n"
AR "v87,a159\n"
"v_pk_fma_f32 v[80:81],v[80:81],v[60:61],v[56:57] op_sel:[0,1,0] op_sel_hi:[1,1,0]\n"
"v_pk_fma_f32 v[82:83],v[82:83],v[60:61],v[56:57] op_sel:[0,1,0] op_sel_hi:[1,1,0]\n"
"v_pk_fma_f32 v[84:85],v[84:85],v[60:61],v[56:57] op_sel:[0,1,0] op_sel_hi:[1,1,0]\n"
"v_pk_fma_f32 v[86:87],v[86:87],v[60:61],v[56:57] op_sel:[0,1,0] op_sel_hi:[1,1,0]\n"
CV "v116,v80,v81\n"
CV "v117,v82,v83\n"
CV "v118,v84,v85\n"
CV "v119,v86,v87\n"
MF "a[164:167],v[168:171],v[96:99],a[164:167]\n"
AW "a136,v116\n"
AW "a137,v117\n"
AW "a138,v118\n"
AW "a139,v119\n"
AR "v72,a160\n"
AR "v73,a161\n"
AR "v74,a162\n"
MF "a[184:187],v[172:175],v[96:99],a[184:187]\n"
AR "v75,a163\n"
AR "v76,a180\n"
AR "v77,a181\n"
AR "v78,a182\n"
AR "v79,a183\n"
"v_pk_fma_f32 v[72:73],v[72:73],v[56:57],v[52:53] op_sel:[0,1,0] op_sel_hi:[1,1,0]\n"
"v_pk_fma_f32 v[74:75],v[74:75],v[56:57],v[52:53] op_sel:[0,1,0] op_sel_hi:[1,1,0]\n"
"v_pk_fma_f32 v[76:77],v[76:77],v[56:57],v[52:53] op_sel:[0,1,0] op_sel_hi:[1,1,0]\n"
"v_pk_fma_f32 v[78:79],v[78:79],v[56:57],v[52:53] op_sel:[0,1,0] op_sel_hi:[1,1,0]\n"
CV "v112,v72,v73\n"
CV "v113,v74,v75\n"
CV "v114,v76,v77\n"
CV "v115,v78,v79\n"
MF "a[168:171],v[168:171],v[100:103],a[168:171]\n"
AW "a160,v112\n"
AW "a161,v113\n"
AW "a162,v114\n"
AW "a163,v115\n"
AR "v80,a164\n"
AR "v81,a165\n"
AR "v82,a166\n"
MF "a[188:191],v[172:175],v[100:103],a[188:191]\n"
AR "v83,a167\n"
AR "v84,a184\n"
AR "v85,a185\n"
AR "v86,a186\n"
AR "v87,a187\n"
"v_pk_fma_f32 v[80:81],v[80:81],v[58:59],v[52:53] op_sel:[0,0,1] op_sel_hi:[1,0,1]\n"
"v_pk_fma_f32 v[82:83],v[82:83],v[58:59],v[52:53] op_sel:[0,0,1] op_sel_hi:[1,0,1]\n"
"v_pk_fma_f32 v[84:85],v[84:85],v[58:59],v[52:53] op_sel:[0,0,1] op_sel_hi:[1,0,1]\n"
"v_pk_fma_f32 v[86:87],v[86:87],v[58:59],v[52:53] op_sel:[0,0,1] op_sel_hi:[1,0,1]\n"
CV "v116,v80,v81\n"
CV "v117,v82,v83\n"
CV "v118,v84,v85\n"
CV "v119,v86,v87\n"
MF "a[172:175],v[168:171],v[104:107],a[172:175]\n"
AW "a164,v116\n"
AW "a165,v117\n"
AW "a166,v118\n"
AW "a167,v119\n"
AR "v72,a168\n"
AR "v73,a169\n"
AR "v74,a170\n"
MF "a[192:195],v[172:175],v[104:107],a[192:195]\n"
AR "v75,a171\n"
AR "v76,a188\n"
AR "v77,a189\n"
AR "v78,a190\n"
AR "v79,a191\n"
"v_pk_fma_f32 v[72:73],v[72:73],v[58:59],v[54:55] op_sel:[0,1,0] op_sel_hi:[1,1,0]\n"
"v_pk_fma_f32 v[74:75],v[74:75],v[58:59],v[54:55] op_sel:[0,1,0] op_sel_hi:[1,1,0]\n"
"v_pk_fma_f32 v[76:77],v[76:77],v[58:59],v[54:55] op_sel:[0,1,0] op_sel_hi:[1,1,0]\n"
"v_pk_fma_f32 v[78:79],v[78:79],v[58:59],v[54:55] op_sel:[0,1,0] op_sel_hi:[1,1,0]\n"
CV "v112,v72,v73\n"
CV "v113,v74,v75\n"
CV "v114,v76,v77\n"
CV "v115,v78,v79\n"
MF "a[176:179],v[168:171],v[108:111],a[176:179]\n"
AW "a168,v112\n"
AW "a169,v113\n"
AW "a170,v114\n"
AW "a171,v115\n"
AR "v80,a172\n"
AR "v81,a173\n"
AR "v82,a174\n"
MF "a[196:199],v[172:175],v[108:111],a[196:199]\n"
AR "v83,a175\n"
AR "v84,a192\n"
AR "v85,a193\n"
AR "v86,a194\n"
AR "v87,a195\n"
"v_pk_fma_f32 v[80:81],v[80:81],v[60:61],v[54:55] op_sel:[0,0,1] op_sel_hi:[1,0,1]\n"
"v_pk_fma_f32 v[82:83],v[82:83],v[60:61],v[54:55] op_sel:[0,0,1] op_sel_hi:[1,0,1]\n"
"v_pk_fma_f32 v[84:85],v[84:85],v[60:61],v[54:55] op_sel:[0,0,1] op_sel_hi:[1,0,1]\n"
"v_pk_fma_f32 v[86:87],v[86:87],v[60:61],v[54:55] op_sel:[0,0,1] op_sel_hi:[1,0,1]\n"
CV "v116,v80,v81\n"
CV "v117,v82,v83\n"
CV "v118,v84,v85\n"
CV "v119,v86,v87\n"
MF "a[200:203],v[176:179],v[92:95],a[200:203]\n"
AW "a172,v116\n"
AW "a173,v117\n"
AW "a174,v118\n"
AW "a175,v119\n"
AR "v72,a176\n"
AR "v73,a177\n"
AR "v74,a178\n"
MF "a[220:223],v[180:183],v[92:95],a[220:223]\n"
AR "v75,a179\n"
AR "v76,a196\n"
AR "v77,a197\n"
AR "v78,a198\n"
AR "v79,a199\n"
"v_pk_fma_f32 v[72:73],v[72:73],v[60:61],v[56:57] op_sel:[0,1,0] op_sel_hi:[1,1,0]\n"
"v_pk_fma_f32 v[74:75],v[74:75],v[60:61],v[56:57] op_sel:[0,1,0] op_sel_hi:[1,1,0]\n"
"v_pk_fma_f32 v[76:77],v[76:77],v[60:61],v[56:57] op_sel:[0,1,0] op_sel_hi:[1,1,0]\n"
"v_pk_fma_f32 v[78:79],v[78:79],v[60:61],v[56:57] op_sel:[0,1,0] op_sel_hi:[1,1,0]\n"
CV "v112,v72,v73\n"
CV "v113,v74,v75\n"
CV "v114,v76,v77\n"
CV "v115,v78,v79\n"
MF "a[204:207],v[176:179],v[96:99],a[204:207]\n"
AW "a176,v112\n"
AW "a177,v113\n"
AW "a178,v114\n"
AW "a179,v115\n"
AR "v80,a200\n"
AR "v81,a201\n"
AR "v82,a202\n"
MF "a[224:227],v[180:183],v[96:99],a[224:227]\n"
AR "v83,a203\n"
AR "v84,a220\n"
AR "v85,a221\n"
AR "v86,a222\n"
AR "v87,a223\n"
"v_pk_fma_f32 v[80:81],v[80:81],v[56:57],v[52:53] op_sel:[0,1,0] op_sel_hi:[1,1,0]\n"
"v_pk_fma_f32 v[82:83],v[82:83],v[56:57],v[52:53] op_sel:[0,1,0] op_sel_hi:[1,1,0]\n"
"v_pk_fma_f32 v[84:85],v[84:85],v[56:57],v[52:53] op_sel:[0,1,0] op_sel_hi:[1,1,0]\n"
"v_pk_fma_f32 v[86:87],v[86:87],v[56:57],v[52:53] op_sel:[0,1,0] op_sel_hi:[1,1,0]\n"
CV "v116,v80,v81\n"
CV "v117,v82,v83\n"
CV "v118,v84,v85\n"
CV "v119,v86,v87\n"
MF "a[208:211],v[176:179],v[100:103],a[208:211]\n"
AW "a20,v116\n"
AW "a21,v117\n"
AW "a22,v118\n"
AW "a23,v119\n"
AR "v72,a204\n"
AR "v73,a205\n"
AR "v74,a206\n"
MF "a[228:231],v[180:183],v[100:103],a[228:231]\n"
AR "v75,a207\n"
AR "v76,a224\n"
AR "v77,a225\n"
AR "v78,a226\n"
AR "v79,a227\n"
"v_pk_fma_f32 v[72:73],v[72:73],v[58:59],v[52:53] op_sel:[0,0,1] op_sel_hi:[1,0,1]\n"
"v_pk_fma_f32 v[74:75],v[74:75],v[58:59],v[52:53] op_sel:[0,0,1] op_sel_hi:[1,0,1]\n"
"v_pk_fma_f32 v[76:77],v[76:77],v[58:59],v[52:53] op_sel:[0,0,1] op_sel_hi:[1,0,1]\n"
"v_pk_fma_f32 v[78:79],v[78:79],v[58:59],v[52:53] op_sel:[0,0,1] op_sel_hi:[1,0,1]\n"
CV "v112,v72,v73\n"
CV "v113,v74,v75\n"
CV "v114,v76,v77\n"
CV "v115,v78,v79\n"
MF "a[212:215],v[176:179],v[104:107],a[212:215]\n"
AW "a24,v112\n"
AW "a25,v113\n"
AW "a26,v114\n"
AW "a27,v115\n"
AR "v80,a208\n"
AR "v81,a209\n"
AR "v82,a210\n"
MF "a[232:235],v[180:183],v[104:107],a[232:235]\n"
AR "v83,a211\n"
AR "v84,a228\n"
AR "v85,a229\n"
AR "v86,a230\n"
AR "v87,a231\n"
"v_pk_fma_f32 v[80:81],v[80:81],v[58:59],v[54:55] op_sel:[0,1,0] op_sel_hi:[1,1,0]\n"
"v_pk_fma_f32 v[82:83],v[82:83],v[58:59],v[54:55] op_sel:[0,1,0] op_sel_hi:[1,1,0]\n"
"v_pk_fma_f32 v[84:85],v[84:85],v[58:59],v[54:55] op_sel:[0,1,0] op_sel_hi:[1,1,0]\n"
"v_pk_fma_f32 v[86:87],v[86:87],v[58:59],v[54:55] op_sel:[0,1,0] op_sel_hi:[1,1,0]\n"
CV "v116,v80,v81\n"
CV "v117,v82,v83\n"
CV "v118,v84,v85\n"
CV "v119,v86,v87\n"
MF "a[216:219],v[176:179],v[108:111],a[216:219]\n"
AW "a28,v116\n"
AW "a29,v117\n"
AW "a30,v118\n"
AW "a31,v119\n"
AR "v72,a212\n"
AR "v73,a213\n"
AR "v74,a214\n"
MF "a[236:239],v[180:183],v[108:111],a[236:239]\n"
AR "v75,a215\n"
AR "v76,a232\n"
AR "v77,a233\n"
AR "v78,a234\n"
AR "v79,a235\n"
"v_pk_fma_f32 v[72:73],v[72:73],v[60:61],v[54:55] op_sel:[0,0,1] op_sel_hi:[1,0,1]\n"
"v_pk_fma_f32 v[74:75],v[74:75],v[60:61],v[54:55] op_sel:[0,0,1] op_sel_hi:[1,0,1]\n"
"v_pk_fma_f32 v[76:77],v[76:77],v[60:61],v[54:55] op_sel:[0,0,1] op_sel_hi:[1,0,1]\n"
"v_pk_fma_f32 v[78:79],v[78:79],v[60:61],v[54:55] op_sel:[0,0,1] op_sel_hi:[1,0,1]\n"
CV "v112,v72,v73\n"
CV "v113,v74,v75\n"
CV "v114,v76,v77\n"
CV "v115,v78,v79\n"
AW "a32,v112\n"
AW "a33,v113\n"
AW "a34,v114\n"
AW "a35,v115\n"
AR "v80,a216\n"
AR "v81,a217\n"
AR "v82,a218\n"
AR "v83,a219\n"
AR "v84,a236\n"
AR "v85,a237\n"
AR "v86,a238\n"
AR "v87,a239\n"
"v_pk_fma_f32 v[80:81],v[80:81],v[60:61],v[56:57] op_sel:[0,1,0] op_sel_hi:[1,1,0]\n"
"v_pk_fma_f32 v[82:83],v[82:83],v[60:61],v[56:57] op_sel:[0,1,0] op_sel_hi:[1,1,0]\n"
"v_pk_fma_f32 v[84:85],v[84:85],v[60:61],v[56:57] op_sel:[0,1,0] op_sel_hi:[1,1,0]\n"
"v_pk_fma_f32 v[86:87],v[86:87],v[60:61],v[56:57] op_sel:[0,1,0] op_sel_hi:[1,1,0]\n"
CV "v116,v80,v81\n"
CV "v117,v82,v83\n"
CV "v118,v84,v85\n"
CV "v119,v86,v87\n"
AW "a36,v116\n"
AW "a37,v117\n"
AW "a38,v118\n"
AW "a39,v119\n"
"s_mov_b32 s13,s11\n"
"s_mov_b32 s70,s71\n"
"s_mov_b32 s57,1\n"
AR "v0,a240\n"
AR "v2,a241\n"
AR "v4,a242\n"
AR "v6,a243\n"
"v_xor_b32 v1,64,v0\n"
"v_xor_b32 v3,64,v2\n"
"v_xor_b32 v5,64,v4\n"
"v_add_u32 v11,97280,v6\n"
"v_add_u32 v12,194560,v6\n"
"v_add_u32 v13,291840,v6\n"
"s_mov_b64 vcc,-1\n"
"s_waitcnt vmcnt(8) lgkmcnt(0)\n"
"s_barrier\n"
"s_nop 0\n"
TR "a[200:201],v0\n"
TR "a[202:203],v0 offset:512\n"
TR "a[204:205],v0 offset:4096\n"
TR "a[206:207],v0 offset:4608\n"
"ds_read_b128 a[60:63],v2\n"
"ds_read_b128 a[100:103],v3\n"
"ds_read_b128 a[64:67],v2 offset:2048\n"
"ds_read_b128 a[104:107],v3 offset:2048\n"
"ds_read_b128 a[68:71],v2 offset:4096\n"
"ds_read_b128 a[108:111],v3 offset:4096\n"
"ds_read_b128 a[72:75],v2 offset:6144\n"
"ds_read_b128 a[112:115],v3 offset:6144\n"
"ds_read_b128 a[76:79],v2 offset:8192\n"
"ds_read_b128 a[116:119],v3 offset:8192\n"
TR "a[208:209],v0 offset:8\n"
TR "a[210:211],v0 offset:520\n"
TR "a[212:213],v0 offset:4104\n"
TR "a[214:215],v0 offset:4616\n"
"s_waitcnt lgkmcnt(0)\n"
"s_branch .Lq0\n"
".Lpgqy_c:\n"
"s_cbranch_execz .Lpgqj_a\n"
".Lqy:\n"
MF "v[216:219],a[216:219],a[60:63],v[216:219]\n"
TR "a[208:209],v0 offset:8\n"
MF "v[216:219],a[220:223],a[100:103],v[216:219]\n"
TR "a[210:211],v0 offset:520\n"
MF "v[224:227],a[220:223],a[104:107],v[224:227]\n"
MF "v[224:227],a[216:219],a[64:67],v[224:227]\n"
TR "a[212:213],v0 offset:4104\n"
MF "v[232:235],a[216:219],a[68:71],v[232:235]\n"
TR "a[214:215],v0 offset:4616\n"
MF "v[232:235],a[220:223],a[108:111],v[232:235]\n"
MF "v[240:243],a[220:223],a[112:115],v[240:243]\n"
MF "v[240:243],a[216:219],a[72:75],v[240:243]\n"
MF "v[248:251],a[216:219],a[76:79],v[248:251]\n"
MF "v[248:251],a[220:223],a[116:119],v[248:251]\n"
MF "v[248:251],a[200:203],a[156:159],v[248:251]\n"
TR "a[216:217],v0 offset:16392\n"
MF "v[248:251],a[204:207],a[196:199],v[248:251]\n"
TR "a[218:219],v0 offset:16904\n"
MF "v[240:243],a[204:207],a[192:195],v[240:243]\n"
MF "v[240:243],a[200:203],a[152:155],v[240:243]\n"
TR "a[220:221],v0 offset:20488\n"
MF "v[232:235],a[200:203],a[148:151],v[232:235]\n"
TR "a[222:223],v0 offset:21000\n"
MF "v[232:235],a[204:207],a[188:191],v[232:235]\n"
MF "v[224:227],a[204:207],a[184:187],v[224:227]\n"
MF "v[224:227],a[200:203],a[144:147],v[224:227]\n"
"v_fma_f32 v248,v248,s54,v14\n"
"v_fma_f32 v249,v249,s54,v14\n"
MF "v[216:219],a[200:203],a[140:143],v[216:219]\n"
"v_fma_f32 v250,v250,s54,v14\n"
"v_fma_f32 v251,v251,s54,v14\n"
MF "v[216:219],a[204:207],a[180:183],v[216:219]\n"
"s_waitcnt lgkmcnt(4)\n"
"v_fma_f32 v252,v252,s54,v14\n"
"v_fma_f32 v253,v253,s54,v14\n"
MF "v[180:183],a[208:211],a[60:63],v[180:183]\n"
TR "a[200:201],v0\n"
"v_fma_f32 v254,v254,s54,v14\n"
MF "v[180:183],a[212:215],a[100:103],v[180:183]\n"
TR "a[202:203],v0 offset:512\n"
"v_fma_f32 v255,v255,s54,v14\n"
MF "v[188:191],a[212:215],a[104:107],v[188:191]\n"
CV "v255,v254,v255\n"
CV "v254,v252,v253\n"
MF "v[188:191],a[208:211],a[64:67],v[188:191]\n"
TR "a[204:205],v0 offset:4096\n"
CV "v253,v250,v251\n"
MF "v[196:199],a[208:211],a[68:71],v[196:199]\n"
TR "a[206:207],v0 offset:4608\n"
CV "v252,v248,v249\n"
MF "v[196:199],a[212:215],a[108:111],v[196:199]\n"
"s_add_u32 s58,s75,4096\n"
"v_fma_f32 v240,v240,s54,v14\n"
"v_fma_f32 v241,v241,s54,v14\n"
MF "v[204:207],a[212:215],a[112:115],v[204:207]\n"
"buffer_store_dwordx4 v[252:255],v9,s[40:43],s58 offen offset:1024 sc1\n"
MF "v[204:207],a[208:211],a[72:75],v[204:207]\n"
"v_fma_f32 v242,v242,s54,v14\n"
"v_fma_f32 v243,v243,s54,v14\n"
MF "v[212:215],a[208:211],a[76:79],v[212:215]\n"
"v_fma_f32 v244,v244,s54,v14\n"
"v_fma_f32 v245,v245,s54,v14\n"
MF "v[212:215],a[212:215],a[116:119],v[212:215]\n"
"s_waitcnt lgkmcnt(4)\n"
"v_fma_f32 v246,v246,s54,v14\n"
"v_fma_f32 v247,v247,s54,v14\n"
MF "v[180:183],a[216:219],a[140:143],v[180:183]\n"
TR "a[208:209],v0 offset:16384\n"
CV "v251,v246,v247\n"
MF "v[180:183],a[220:223],a[180:183],v[180:183]\n"
TR "a[210:211],v0 offset:16896\n"
CV "v250,v244,v245\n"
MF "v[188:191],a[220:223],a[184:187],v[188:191]\n"
CV "v249,v242,v243\n"
CV "v248,v240,v241\n"
MF "v[188:191],a[216:219],a[144:147],v[188:191]\n"
TR "a[212:213],v0 offset:20480\n"
"v_fma_f32 v232,v232,s54,v14\n"
MF "v[196:199],a[216:219],a[148:151],v[196:199]\n"
TR "a[214:215],v0 offset:20992\n"
"v_fma_f32 v233,v233,s54,v14\n"
MF "v[196:199],a[220:223],a[188:191],v[196:199]\n"
"buffer_store_dwordx4 v[248:251],v9,s[40:43],s58 offen sc1\n"
MF "v[204:207],a[220:223],a[192:195],v[204:207]\n"
"v_fma_f32 v234,v234,s54,v14\n"
"v_fma_f32 v235,v235,s54,v14\n"
MF "v[204:207],a[216:219],a[152:155],v[204:207]\n"
"v_fma_f32 v236,v236,s54,v14\n"
"v_fma_f32 v237,v237,s54,v14\n"
MF "v[212:215],a[216:219],a[156:159],v[212:215]\n"
"s_waitcnt vmcnt(10) lgkmcnt(0)\n"
"v_fma_f32 v238,v238,s54,v14\n"
"v_fma_f32 v239,v239,s54,v14\n"
MF "v[212:215],a[220:223],a[196:199],v[212:215]\n"
"s_barrier\n"
MF "v[176:179],a[200:203],a[60:63],v[176:179]\n"
TR "a[216:217],v1 offset:32776\n"
CV "v247,v238,v239\n"
MF "v[176:179],a[204:207],a[100:103],v[176:179]\n"
TR "a[218:219],v1 offset:33288\n"
CV "v246,v236,v237\n"
MF "v[184:187],a[204:207],a[104:107],v[184:187]\n"
"s_add_u32 s66,s67,4718592\n"
"s_sub_u32 s81,s81,384\n"
"s_mov_b32 s83,7077888\n"
MF "v[184:187],a[200:203],a[64:67],v[184:187]\n"
"s_add_u32 m0,s62,0\n"
"s_add_u32 s82,s66,2359296\n"
TR "a[220:221],v1 offset:36872\n"
MF "v[192:195],a[200:203],a[68:71],v[192:195]\n"
"s_sub_u32 s15,s82,7864320\n"
"s_min_u32 s82,s82,s15\n"
TR "a[222:223],v1 offset:37384\n"
MF "v[192:195],a[204:207],a[108:111],v[192:195]\n"
LD "v6,s[28:31],s82 offen lds\n"
MF "v[200:203],a[204:207],a[112:115],v[200:203]\n"
CV "v245,v234,v235\n"
CV "v244,v232,v233\n"
MF "v[200:203],a[200:203],a[72:75],v[200:203]\n"
"v_fma_f32 v224,v224,s54,v14\n"
"v_fma_f32 v225,v225,s54,v14\n"
MF "v[208:211],a[200:203],a[76:79],v[208:211]\n"
"buffer_store_dwordx4 v[244:247],v9,s[40:43],s75 offen offset:3072 sc1\n"
MF "v[208:211],a[204:207],a[116:119],v[208:211]\n"
LD "v11,s[28:31],s82 offen offset:1024 lds\n"
MF "v[208:211],a[208:211],a[156:159],v[208:211]\n"
TR "a[200:201],v1 offset:49160\n"
"v_fma_f32 v226,v226,s54,v14\n"
MF "v[208:211],a[212:215],a[196:199],v[208:211]\n"
TR "a[202:203],v1 offset:49672\n"
"v_fma_f32 v227,v227,s54,v14\n"
MF "v[200:203],a[212:215],a[192:195],v[200:203]\n"
"v_fma_f32 v228,v228,s54,v14\n"
"v_fma_f32 v229,v229,s54,v14\n"
MF "v[200:203],a[208:211],a[152:155],v[200:203]\n"
TR "a[204:205],v1 offset:53256\n"
"v_fma_f32 v230,v230,s54,v14\n"
MF "v[192:195],a[208:211],a[148:151],v[192:195]\n"
TR "a[206:207],v1 offset:53768\n"
"v_fma_f32 v231,v231,s54,v14\n"
MF "v[192:195],a[212:215],a[188:191],v[192:195]\n"
CV "v243,v230,v231\n"
CV "v242,v228,v229\n"
MF "v[184:187],a[212:215],a[184:187],v[184:187]\n"
CV "v241,v226,v227\n"
CV "v240,v224,v225\n"
MF "v[184:187],a[208:211],a[144:147],v[184:187]\n"
"buffer_store_dwordx4 v[240:243],v9,s[40:43],s75 offen offset:2048 sc1\n"
MF "v[176:179],a[208:211],a[140:143],v[176:179]\n"
LD "v12,s[28:31],s82 offen offset:2048 lds\n"
MF "v[176:179],a[212:215],a[180:183],v[176:179]\n"
"s_waitcnt lgkmcnt(4)\n"
"v_fma_f32 v216,v216,s54,v14\n"
"v_fma_f32 v217,v217,s54,v14\n"
MF "v[140:143],a[216:219],a[60:63],v[140:143]\n"
".Lpg13c:\n"
"s_cbranch_execz .Lpg3a\n"
TR "a[208:209],v1 offset:32768\n"
"v_fma_f32 v218,v218,s54,v14\n"
MF "v[140:143],a[220:223],a[100:103],v[140:143]\n"
TR "a[210:211],v1 offset:33280\n"
"v_fma_f32 v219,v219,s54,v14\n"
MF "v[148:151],a[220:223],a[104:107],v[148:151]\n"
"v_fma_f32 v220,v220,s54,v14\n"
"v_fma_f32 v221,v221,s54,v14\n"
MF "v[148:151],a[216:219],a[64:67],v[148:151]\n"
TR "a[212:213],v1 offset:36864\n"
"v_fma_f32 v222,v222,s54,v14\n"
MF "v[156:159],a[216:219],a[68:71],v[156:159]\n"
TR "a[214:215],v1 offset:37376\n"
"v_fma_f32 v223,v223,s54,v14\n"
MF "v[156:159],a[220:223],a[108:111],v[156:159]\n"
CV "v239,v222,v223\n"
CV "v238,v220,v221\n"
MF "v[164:167],a[220:223],a[112:115],v[164:167]\n"
CV "v237,v218,v219\n"
CV "v236,v216,v217\n"
MF "v[164:167],a[216:219],a[72:75],v[164:167]\n"
LD "v13,s[28:31],s82 offen offset:3072 lds\n"
MF "v[172:175],a[216:219],a[76:79],v[172:175]\n"
"buffer_store_dwordx4 v[236:239],v9,s[40:43],s75 offen offset:1024 sc1\n"
MF "v[172:175],a[220:223],a[116:119],v[172:175]\n"
"s_waitcnt lgkmcnt(4)\n"
"v_fma_f32 v208,v208,s54,v14\n"
"v_fma_f32 v209,v209,s54,v14\n"
MF "v[140:143],a[200:203],a[140:143],v[140:143]\n"
TR "a[216:217],v1 offset:49152\n"
"v_fma_f32 v210,v210,s54,v14\n"
MF "v[140:143],a[204:207],a[180:183],v[140:143]\n"
TR "a[218:219],v1 offset:49664\n"
"v_fma_f32 v211,v211,s54,v14\n"
MF "v[148:151],a[204:207],a[184:187],v[148:151]\n"
"v_fma_f32 v212,v212,s54,v14\n"
"v_fma_f32 v213,v213,s54,v14\n"
MF "v[148:151],a[200:203],a[144:147],v[148:151]\n"
TR "a[220:221],v1 offset:53248\n"
"v_fma_f32 v214,v214,s54,v14\n"
MF "v[156:159],a[200:203],a[148:151],v[156:159]\n"
TR "a[222:223],v1 offset:53760\n"
"v_fma_f32 v215,v215,s54,v14\n"
MF "v[156:159],a[204:207],a[188:191],v[156:159]\n"
CV "v235,v214,v215\n"
CV "v234,v212,v213\n"
MF "v[164:167],a[204:207],a[192:195],v[164:167]\n"
CV "v233,v210,v211\n"
CV "v232,v208,v209\n"
MF "v[164:167],a[200:203],a[152:155],v[164:167]\n"
"v_fma_f32 v200,v200,s54,v14\n"
"v_fma_f32 v201,v201,s54,v14\n"
MF "v[172:175],a[200:203],a[156:159],v[172:175]\n"
"buffer_store_dwordx4 v[232:235],v9,s[40:43],s75 offen sc1\n"
MF "v[172:175],a[204:207],a[196:199],v[172:175]\n"
"s_waitcnt lgkmcnt(0)\n"
"v_fma_f32 v202,v202,s54,v14\n"
"v_fma_f32 v203,v203,s54,v14\n"
MF "v[136:139],a[208:211],a[60:63],v[136:139]\n"
"s_add_u32 m0,s62,16384\n"
"s_add_u32 s82,s66,2359424\n"
TR "a[200:201],v0 offset:32776\n"
MF "v[136:139],a[212:215],a[100:103],v[136:139]\n"
"s_sub_u32 s15,s82,7864320\n"
"s_min_u32 s82,s82,s15\n"
TR "a[202:203],v0 offset:33288\n"
MF "v[144:147],a[212:215],a[104:107],v[144:147]\n"
LD "v6,s[28:31],s82 offen lds\n"
MF "v[144:147],a[208:211],a[64:67],v[144:147]\n"
TR "a[204:205],v0 offset:36872\n"
"v_fma_f32 v204,v204,s54,v14\n"
MF "v[152:155],a[208:211],a[68:71],v[152:155]\n"
TR "a[206:207],v0 offset:37384\n"
"v_fma_f32 v205,v205,s54,v14\n"
MF "v[152:155],a[212:215],a[108:111],v[152:155]\n"
"v_fma_f32 v206,v206,s54,v14\n"
"v_fma_f32 v207,v207,s54,v14\n"
MF "v[160:163],a[212:215],a[112:115],v[160:163]\n"
CV "v231,v206,v207\n"
CV "v230,v204,v205\n"
MF "v[160:163],a[208:211],a[72:75],v[160:163]\n"
CV "v229,v202,v203\n"
CV "v228,v200,v201\n"
MF "v[168:171],a[208:211],a[76:79],v[168:171]\n"
AW "a84,v228\n"
AW "a85,v229\n"
MF "v[168:171],a[212:215],a[116:119],v[168:171]\n"
LD "v11,s[28:31],s82 offen offset:1024 lds\n"
MF "v[168:171],a[216:219],a[156:159],v[168:171]\n"
TR "a[208:209],v0 offset:49160\n"
AW "a86,v230\n"
MF "v[168:171],a[220:223],a[196:199],v[168:171]\n"
TR "a[210:211],v0 offset:49672\n"
AW "a87,v231\n"
MF "v[160:163],a[220:223],a[192:195],v[160:163]\n"
"v_fma_f32 v192,v192,s54,v14\n"
"v_fma_f32 v193,v193,s54,v14\n"
MF "v[160:163],a[216:219],a[152:155],v[160:163]\n"
TR "a[212:213],v0 offset:53256\n"
"v_fma_f32 v194,v194,s54,v14\n"
MF "v[152:155],a[216:219],a[148:151],v[152:155]\n"
TR "a[214:215],v0 offset:53768\n"
"v_fma_f32 v195,v195,s54,v14\n"
MF "v[152:155],a[220:223],a[188:191],v[152:155]\n"
"v_fma_f32 v196,v196,s54,v14\n"
"v_fma_f32 v197,v197,s54,v14\n"
MF "v[144:147],a[220:223],a[184:187],v[144:147]\n"
"v_fma_f32 v198,v198,s54,v14\n"
"v_fma_f32 v199,v199,s54,v14\n"
MF "v[144:147],a[216:219],a[144:147],v[144:147]\n"
CV "v227,v198,v199\n"
CV "v226,v196,v197\n"
MF "v[136:139],a[216:219],a[140:143],v[136:139]\n"
LD "v12,s[28:31],s82 offen offset:2048 lds\n"
MF "v[136:139],a[220:223],a[180:183],v[136:139]\n"
"s_waitcnt lgkmcnt(4)\n"
CV "v225,v194,v195\n"
CV "v224,v192,v193\n"
MF "v[100:103],a[200:203],a[60:63],v[100:103]\n"
TR "a[216:217],v0 offset:32768\n"
AW "a80,v224\n"
MF "v[100:103],a[204:207],a[100:103],v[100:103]\n"
TR "a[218:219],v0 offset:33280\n"
AW "a81,v225\n"
MF "v[108:111],a[204:207],a[104:107],v[108:111]\n"
AW "a82,v226\n"
AW "a83,v227\n"
MF "v[108:111],a[200:203],a[64:67],v[108:111]\n"
TR "a[220:221],v0 offset:36864\n"
"v_fma_f32 v184,v184,s54,v14\n"
MF "v[116:119],a[200:203],a[68:71],v[116:119]\n"
TR "a[222:223],v0 offset:37376\n"
"v_fma_f32 v185,v185,s54,v14\n"
MF "v[116:119],a[204:207],a[108:111],v[116:119]\n"
"v_fma_f32 v186,v186,s54,v14\n"
"v_fma_f32 v187,v187,s54,v14\n"
MF "v[124:127],a[204:207],a[112:115],v[124:127]\n"
"v_fma_f32 v188,v188,s54,v14\n"
"v_fma_f32 v189,v189,s54,v14\n"
MF "v[124:127],a[200:203],a[72:75],v[124:127]\n"
LD "v13,s[28:31],s82 offen offset:3072 lds\n"
MF "v[132:135],a[200:203],a[76:79],v[132:135]\n"
"v_fma_f32 v190,v190,s54,v14\n"
"v_fma_f32 v191,v191,s54,v14\n"
MF "v[132:135],a[204:207],a[116:119],v[132:135]\n"
"s_waitcnt lgkmcnt(4)\n"
CV "v223,v190,v191\n"
CV "v222,v188,v189\n"
MF "v[100:103],a[208:211],a[140:143],v[100:103]\n"
TR "a[200:201],v0 offset:49152\n"
CV "v221,v186,v187\n"
MF "v[100:103],a[212:215],a[180:183],v[100:103]\n"
TR "a[202:203],v0 offset:49664\n"
CV "v220,v184,v185\n"
MF "v[108:111],a[212:215],a[184:187],v[108:111]\n"
AW "a56,v220\n"
AW "a57,v221\n"
MF "v[108:111],a[208:211],a[144:147],v[108:111]\n"
TR "a[204:205],v0 offset:53248\n"
AW "a58,v222\n"
MF "v[116:119],a[208:211],a[148:151],v[116:119]\n"
TR "a[206:207],v0 offset:53760\n"
AW "a59,v223\n"
MF "v[116:119],a[212:215],a[188:191],v[116:119]\n"
"v_fma_f32 v176,v176,s54,v14\n"
"v_fma_f32 v177,v177,s54,v14\n"
MF "v[124:127],a[212:215],a[192:195],v[124:127]\n"
"v_add_u32 v0,0xc000,v0\n"
"v_fma_f32 v178,v178,s54,v14\n"
MF "v[124:127],a[208:211],a[152:155],v[124:127]\n"
"v_add_u32 v1,0xc000,v1\n"
"v_fma_f32 v179,v179,s54,v14\n"
MF "v[132:135],a[208:211],a[156:159],v[132:135]\n"
"s_waitcnt vmcnt(14) lgkmcnt(0)\n"
"v_fma_f32 v180,v180,s54,v14\n"
"v_fma_f32 v181,v181,s54,v14\n"
MF "v[132:135],a[212:215],a[196:199],v[132:135]\n"
"s_barrier\n"
MF "v[96:99],a[216:219],a[60:63],v[96:99]\n"
"s_add_u32 m0,s62,32768\n"
"s_add_u32 s82,s66,2359552\n"
TR "a[208:209],v1 offset:16392\n"
MF "v[96:99],a[220:223],a[100:103],v[96:99]\n"
"s_sub_u32 s15,s82,7864320\n"
"s_min_u32 s82,s82,s15\n"
TR "a[210:211],v1 offset:16904\n"
MF "v[104:107],a[220:223],a[104:107],v[104:107]\n"
LD "v6,s[28:31],s82 offen lds\n"
MF "v[104:107],a[216:219],a[64:67],v[104:107]\n"
TR "a[212:213],v1 offset:20488\n"
"v_fma_f32 v182,v182,s54,v14\n"
MF "v[112:115],a[216:219],a[68:71],v[112:115]\n"
TR "a[214:215],v1 offset:21000\n"
"v_fma_f32 v183,v183,s54,v14\n"
MF "v[112:115],a[220:223],a[108:111],v[112:115]\n"
CV "v219,v182,v183\n"
CV "v218,v180,v181\n"
MF "v[120:123],a[220:223],a[112:115],v[120:123]\n"
CV "v217,v178,v179\n"
CV "v216,v176,v177\n"
MF "v[120:123],a[216:219],a[72:75],v[120:123]\n"
AW "a52,v216\n"
AW "a53,v217\n"
MF "v[128:131],a[216:219],a[76:79],v[128:131]\n"
AW "a54,v218\n"
AW "a55,v219\n"
MF "v[128:131],a[220:223],a[116:119],v[128:131]\n"
LD "v11,s[28:31],s82 offen offset:1024 lds\n"
MF "v[128:131],a[200:203],a[156:159],v[128:131]\n"
TR "a[216:217],v1 offset:32776\n"
"v_fma_f32 v168,v168,s54,v14\n"
MF "v[128:131],a[204:207],a[196:199],v[128:131]\n"
TR "a[218:219],v1 offset:33288\n"
"v_fma_f32 v169,v169,s54,v14\n"
MF "v[120:123],a[204:207],a[192:195],v[120:123]\n"
"v_fma_f32 v170,v170,s54,v14\n"
"v_fma_f32 v171,v171,s54,v14\n"
MF "v[120:123],a[200:203],a[152:155],v[120:123]\n"
TR "a[220:221],v1 offset:36872\n"
"v_fma_f32 v172,v172,s54,v14\n"
MF "v[112:115],a[200:203],a[148:151],v[112:115]\n"
TR "a[222:223],v1 offset:37384\n"
"v_fma_f32 v173,v173,s54,v14\n"
MF "v[112:115],a[204:207],a[188:191],v[112:115]\n"
"v_fma_f32 v174,v174,s54,v14\n"
"v_fma_f32 v175,v175,s54,v14\n"
MF "v[104:107],a[204:207],a[184:187],v[104:107]\n"
CV "v215,v174,v175\n"
CV "v214,v172,v173\n"
MF "v[104:107],a[200:203],a[144:147],v[104:107]\n"
CV "v213,v170,v171\n"
CV "v212,v168,v169\n"
MF "v[96:99],a[200:203],a[140:143],v[96:99]\n"
LD "v12,s[28:31],s82 offen offset:2048 lds\n"
MF "v[96:99],a[204:207],a[180:183],v[96:99]\n"
"s_waitcnt lgkmcnt(4)\n"
AW "a48,v212\n"
AW "a49,v213\n"
MF "v[60:63],a[208:211],a[60:63],v[60:63]\n"
TR "a[200:201],v1 offset:16384\n"
AW "a50,v214\n"
MF "v[60:63],a[212:215],a[100:103],v[60:63]\n"
TR "a[202:203],v1 offset:16896\n"
AW "a51,v215\n"
MF "v[68:71],a[212:215],a[104:107],v[68:71]\n"
"v_fma_f32 v160,v160,s54,v14\n"
"v_fma_f32 v161,v161,s54,v14\n"
MF "v[68:71],a[208:211],a[64:67],v[68:71]\n"
TR "a[204:205],v1 offset:20480\n"
"v_fma_f32 v162,v162,s54,v14\n"
MF "v[76:79],a[208:211],a[68:71],v[76:79]\n"
TR "a[206:207],v1 offset:20992\n"
"v_fma_f32 v163,v163,s54,v14\n"
MF "v[76:79],a[212:215],a[108:111],v[76:79]\n"
"v_fma_f32 v164,v164,s54,v14\n"
"v_fma_f32 v165,v165,s54,v14\n"
MF "v[84:87],a[212:215],a[112:115],v[84:87]\n"
"v_fma_f32 v166,v166,s54,v14\n"
"v_fma_f32 v167,v167,s54,v14\n"
MF "v[84:87],a[208:211],a[72:75],v[84:87]\n"
LD "v13,s[28:31],s82 offen offset:3072 lds\n"
MF "v[92:95],a[208:211],a[76:79],v[92:95]\n"
CV "v211,v166,v167\n"
CV "v210,v164,v165\n"
MF "v[92:95],a[212:215],a[116:119],v[92:95]\n"
"s_waitcnt lgkmcnt(4)\n"
CV "v209,v162,v163\n"
CV "v208,v160,v161\n"
MF "v[60:63],a[216:219],a[140:143],v[60:63]\n"
TR "a[208:209],v1 offset:32768\n"
AW "a44,v208\n"
MF "v[60:63],a[220:223],a[180:183],v[60:63]\n"
TR "a[210:211],v1 offset:33280\n"
AW "a45,v209\n"
MF "v[68:71],a[220:223],a[184:187],v[68:71]\n"
AW "a46,v210\n"
AW "a47,v211\n"
MF "v[68:71],a[216:219],a[144:147],v[68:71]\n"
TR "a[212:213],v1 offset:36864\n"
"v_fma_f32 v152,v152,s54,v14\n"
MF "v[76:79],a[216:219],a[148:151],v[76:79]\n"
TR "a[214:215],v1 offset:37376\n"
"v_fma_f32 v153,v153,s54,v14\n"
MF "v[76:79],a[220:223],a[188:191],v[76:79]\n"
"v_fma_f32 v154,v154,s54,v14\n"
"v_fma_f32 v155,v155,s54,v14\n"
MF "v[84:87],a[220:223],a[192:195],v[84:87]\n"
"v_fma_f32 v156,v156,s54,v14\n"
"v_fma_f32 v157,v157,s54,v14\n"
MF "v[84:87],a[216:219],a[152:155],v[84:87]\n"
"v_fma_f32 v158,v158,s54,v14\n"
"v_fma_f32 v159,v159,s54,v14\n"
MF "v[92:95],a[216:219],a[156:159],v[92:95]\n"
CV "v207,v158,v159\n"
CV "v206,v156,v157\n"
MF "v[92:95],a[220:223],a[196:199],v[92:95]\n"
"s_waitcnt lgkmcnt(0)\n"
CV "v205,v154,v155\n"
CV "v204,v152,v153\n"
MF "v[56:59],a[200:203],a[60:63],v[56:59]\n"
TR "a[216:217],v0 offset:16392\n"
AW "a40,v204\n"
MF "v[56:59],a[204:207],a[100:103],v[56:59]\n"
TR "a[218:219],v0 offset:16904\n"
AW "a41,v205\n"
MF "v[64:67],a[204:207],a[104:107],v[64:67]\n"
AW "a42,v206\n"
AW "a43,v207\n"
MF "v[64:67],a[200:203],a[64:67],v[64:67]\n"
TR "a[220:221],v0 offset:20488\n"
"v_fma_f32 v144,v144,s54,v14\n"
MF "v[72:75],a[200:203],a[68:71],v[72:75]\n"
TR "a[222:223],v0 offset:21000\n"
"v_fma_f32 v145,v145,s54,v14\n"
MF "v[72:75],a[204:207],a[108:111],v[72:75]\n"
"v_fma_f32 v146,v146,s54,v14\n"
"v_fma_f32 v147,v147,s54,v14\n"
MF "v[80:83],a[204:207],a[112:115],v[80:83]\n"
"v_fma_f32 v148,v148,s54,v14\n"
"v_fma_f32 v149,v149,s54,v14\n"
MF "v[80:83],a[200:203],a[72:75],v[80:83]\n"
"v_fma_f32 v150,v150,s54,v14\n"
"v_fma_f32 v151,v151,s54,v14\n"
MF "v[88:91],a[200:203],a[76:79],v[88:91]\n"
CV "v203,v150,v151\n"
CV "v202,v148,v149\n"
MF "v[88:91],a[204:207],a[116:119],v[88:91]\n"
CV "v201,v146,v147\n"
CV "v200,v144,v145\n"
MF "v[88:91],a[208:211],a[156:159],v[88:91]\n"
TR "a[200:201],v0 offset:32776\n"
AW "a16,v200\n"
MF "v[88:91],a[212:215],a[196:199],v[88:91]\n"
TR "a[202:203],v0 offset:33288\n"
AW "a17,v201\n"
MF "v[80:83],a[212:215],a[192:195],v[80:83]\n"
AW "a18,v202\n"
AW "a19,v203\n"
MF "v[80:83],a[208:211],a[152:155],v[80:83]\n"
TR "a[204:205],v0 offset:36872\n"
"v_fma_f32 v136,v136,s54,v14\n"
MF "v[72:75],a[208:211],a[148:151],v[72:75]\n"
TR "a[206:207],v0 offset:37384\n"
"v_fma_f32 v137,v137,s54,v14\n"
MF "v[72:75],a[212:215],a[188:191],v[72:75]\n"
"v_fma_f32 v138,v138,s54,v14\n"
"v_fma_f32 v139,v139,s54,v14\n"
MF "v[64:67],a[212:215],a[184:187],v[64:67]\n"
"v_fma_f32 v140,v140,s54,v14\n"
"v_fma_f32 v141,v141,s54,v14\n"
MF "v[64:67],a[208:211],a[144:147],v[64:67]\n"
"v_fma_f32 v142,v142,s54,v14\n"
"v_fma_f32 v143,v143,s54,v14\n"
MF "v[56:59],a[208:211],a[140:143],v[56:59]\n"
CV "v199,v142,v143\n"
CV "v198,v140,v141\n"
MF "v[56:59],a[212:215],a[180:183],v[56:59]\n"
"s_waitcnt lgkmcnt(4)\n"
CV "v197,v138,v139\n"
CV "v196,v136,v137\n"
MF "v[20:23],a[216:219],a[60:63],v[20:23]\n"
TR "a[208:209],v0 offset:16384\n"
AW "a12,v196\n"
MF "v[20:23],a[220:223],a[100:103],v[20:23]\n"
TR "a[210:211],v0 offset:16896\n"
AW "a13,v197\n"
MF "v[28:31],a[220:223],a[104:107],v[28:31]\n"
AW "a14,v198\n"
AW "a15,v199\n"
MF "v[28:31],a[216:219],a[64:67],v[28:31]\n"
TR "a[212:213],v0 offset:20480\n"
"v_fma_f32 v128,v128,s54,v14\n"
MF "v[36:39],a[216:219],a[68:71],v[36:39]\n"
TR "a[214:215],v0 offset:20992\n"
"v_fma_f32 v129,v129,s54,v14\n"
MF "v[36:39],a[220:223],a[108:111],v[36:39]\n"
"v_fma_f32 v130,v130,s54,v14\n"
"v_fma_f32 v131,v131,s54,v14\n"
MF "v[44:47],a[220:223],a[112:115],v[44:47]\n"
"v_fma_f32 v132,v132,s54,v14\n"
"v_fma_f32 v133,v133,s54,v14\n"
MF "v[44:47],a[216:219],a[72:75],v[44:47]\n"
"v_fma_f32 v134,v134,s54,v14\n"
"v_fma_f32 v135,v135,s54,v14\n"
MF "v[52:55],a[216:219],a[76:79],v[52:55]\n"
CV "v195,v134,v135\n"
CV "v194,v132,v133\n"
MF "v[52:55],a[220:223],a[116:119],v[52:55]\n"
"s_waitcnt lgkmcnt(4)\n"
CV "v193,v130,v131\n"
CV "v192,v128,v129\n"
MF "v[20:23],a[200:203],a[140:143],v[20:23]\n"
TR "a[216:217],v0 offset:32768\n"
AW "a8,v192\n"
MF "v[20:23],a[204:207],a[180:183],v[20:23]\n"
TR "a[218:219],v0 offset:33280\n"
AW "a9,v193\n"
MF "v[28:31],a[204:207],a[184:187],v[28:31]\n"
AW "a10,v194\n"
AW "a11,v195\n"
MF "v[28:31],a[200:203],a[144:147],v[28:31]\n"
TR "a[220:221],v0 offset:36864\n"
"v_fma_f32 v120,v120,s54,v14\n"
MF "v[36:39],a[200:203],a[148:151],v[36:39]\n"
TR "a[222:223],v0 offset:37376\n"
"v_fma_f32 v121,v121,s54,v14\n"
MF "v[36:39],a[204:207],a[188:191],v[36:39]\n"
"v_fma_f32 v122,v122,s54,v14\n"
"v_fma_f32 v123,v123,s54,v14\n"
MF "v[44:47],a[204:207],a[192:195],v[44:47]\n"
"v_fma_f32 v124,v124,s54,v14\n"
"v_fma_f32 v125,v125,s54,v14\n"
MF "v[44:47],a[200:203],a[152:155],v[44:47]\n"
"v_fma_f32 v126,v126,s54,v14\n"
"v_fma_f32 v127,v127,s54,v14\n"
MF "v[52:55],a[200:203],a[156:159],v[52:55]\n"
"s_waitcnt lgkmcnt(0)\n"
CV "v191,v126,v127\n"
CV "v190,v124,v125\n"
MF "v[52:55],a[204:207],a[196:199],v[52:55]\n"
"s_barrier\n"
MF "v[16:19],a[208:211],a[60:63],v[16:19]\n"
CV "v189,v122,v123\n"
CV "v188,v120,v121\n"
MF "v[16:19],a[212:215],a[100:103],v[16:19]\n"
AW "a4,v188\n"
AW "a5,v189\n"
MF "v[24:27],a[212:215],a[104:107],v[24:27]\n"
AW "a6,v190\n"
AW "a7,v191\n"
MF "v[24:27],a[208:211],a[64:67],v[24:27]\n"
"v_fma_f32 v112,v112,s54,v14\n"
"v_fma_f32 v113,v113,s54,v14\n"
MF "v[32:35],a[208:211],a[68:71],v[32:35]\n"
"v_fma_f32 v114,v114,s54,v14\n"
"v_fma_f32 v115,v115,s54,v14\n"
MF "v[32:35],a[212:215],a[108:111],v[32:35]\n"
"v_fma_f32 v116,v116,s54,v14\n"
"v_fma_f32 v117,v117,s54,v14\n"
MF "v[40:43],a[212:215],a[112:115],v[40:43]\n"
"v_fma_f32 v118,v118,s54,v14\n"
"v_fma_f32 v119,v119,s54,v14\n"
MF "v[40:43],a[208:211],a[72:75],v[40:43]\n"
CV "v187,v118,v119\n"
CV "v186,v116,v117\n"
MF "v[48:51],a[208:211],a[76:79],v[48:51]\n"
CV "v185,v114,v115\n"
CV "v184,v112,v113\n"
MF "v[48:51],a[212:215],a[116:119],v[48:51]\n"
AW "a0,v184\n"
AW "a1,v185\n"
MF "v[48:51],a[216:219],a[156:159],v[48:51]\n"
AW "a2,v186\n"
AW "a3,v187\n"
MF "v[48:51],a[220:223],a[196:199],v[48:51]\n"
"v_fma_f32 v104,v104,s54,v14\n"
"v_fma_f32 v105,v105,s54,v14\n"
MF "v[40:43],a[220:223],a[192:195],v[40:43]\n"
"v_fma_f32 v106,v106,s54,v14\n"
"v_fma_f32 v107,v107,s54,v14\n"
MF "v[40:43],a[216:219],a[152:155],v[40:43]\n"
"v_fma_f32 v108,v108,s54,v14\n"
"v_fma_f32 v109,v109,s54,v14\n"
MF "v[32:35],a[216:219],a[148:151],v[32:35]\n"
"v_fma_f32 v110,v110,s54,v14\n"
"v_fma_f32 v111,v111,s54,v14\n"
MF "v[32:35],a[220:223],a[188:191],v[32:35]\n"
CV "v183,v110,v111\n"
CV "v182,v108,v109\n"
MF "v[24:27],a[220:223],a[184:187],v[24:27]\n"
CV "v181,v106,v107\n"
CV "v180,v104,v105\n"
MF "v[24:27],a[216:219],a[144:147],v[24:27]\n"
AW "a36,v180\n"
AW "a37,v181\n"
MF "v[16:19],a[216:219],a[140:143],v[16:19]\n"
AW "a38,v182\n"
AW "a39,v183\n"
MF "v[16:19],a[220:223],a[180:183],v[16:19]\n"
"s_mov_b32 s57,1\n"
"s_mov_b32 s69,99\n"
AR "v0,a240\n"
AR "v2,a241\n"
AR "v4,a242\n"
AR "v6,a243\n"
"v_xor_b32 v1,64,v0\n"
"v_xor_b32 v3,64,v2\n"
"v_xor_b32 v5,64,v4\n"
"v_add_u32 v11,97280,v6\n"
"v_add_u32 v12,194560,v6\n"
"v_add_u32 v13,291840,v6\n"
"s_mov_b64 vcc,-1\n"
"s_waitcnt vmcnt(10) lgkmcnt(0)\n"
"s_barrier\n"
"s_nop 0\n"
TR "a[200:201],v0\n"
TR "a[202:203],v0 offset:512\n"
TR "a[204:205],v0 offset:4096\n"
TR "a[206:207],v0 offset:4608\n"
"ds_read_b128 a[60:63],v2\n"
"ds_read_b128 a[100:103],v3\n"
"ds_read_b128 a[64:67],v2 offset:2048\n"
"ds_read_b128 a[104:107],v3 offset:2048\n"
"ds_read_b128 a[68:71],v2 offset:4096\n"
"ds_read_b128 a[108:111],v3 offset:4096\n"
"ds_read_b128 a[72:75],v2 offset:6144\n"
"ds_read_b128 a[112:115],v3 offset:6144\n"
"ds_read_b128 a[76:79],v2 offset:8192\n"
"ds_read_b128 a[116:119],v3 offset:8192\n"
TR "a[208:209],v0 offset:8\n"
TR "a[210:211],v0 offset:520\n"
TR "a[212:213],v0 offset:4104\n"
TR "a[214:215],v0 offset:4616\n"
"v_pk_fma_f32 v[96:97],v[96:97],s[54:55],v[14:15] op_sel_hi:[1,1,0]\n"
"v_pk_fma_f32 v[98:99],v[98:99],s[54:55],v[14:15] op_sel_hi:[1,1,0]\n"
"v_pk_fma_f32 v[100:101],v[100:101],s[54:55],v[14:15] op_sel_hi:[1,1,0]\n"
"v_pk_fma_f32 v[102:103],v[102:103],s[54:55],v[14:15] op_sel_hi:[1,1,0]\n"
CV "v179,v102,v103\n"
CV "v178,v100,v101\n"
CV "v177,v98,v99\n"
".Lpg14c:\n"
"s_cbranch_execz .Lpg2a\n"
"s_nop 0\n"
CV "v176,v96,v97\n"
AW "a32,v176\n"
AW "a33,v177\n"
AW "a34,v178\n"
AW "a35,v179\n"
"v_pk_fma_f32 v[88:89],v[88:89],s[54:55],v[14:15] op_sel_hi:[1,1,0]\n"
"v_pk_fma_f32 v[90:91],v[90:91],s[54:55],v[14:15] op_sel_hi:[1,1,0]\n"
"v_pk_fma_f32 v[92:93],v[92:93],s[54:55],v[14:15] op_sel_hi:[1,1,0]\n"
"v_pk_fma_f32 v[94:95],v[94:95],s[54:55],v[14:15] op_sel_hi:[1,1,0]\n"
CV "v175,v94,v95\n"
CV "v174,v92,v93\n"
CV "v173,v90,v91\n"
CV "v172,v88,v89\n"
AW "a28,v172\n"
AW "a29,v173\n"
AW "a30,v174\n"
AW "a31,v175\n"
"v_pk_fma_f32 v[80:81],v[80:81],s[54:55],v[14:15] op_sel_hi:[1,1,0]\n"
"v_pk_fma_f32 v[82:83],v[82:83],s[54:55],v[14:15] op_sel_hi:[1,1,0]\n"
"v_pk_fma_f32 v[84:85],v[84:85],s[54:55],v[14:15] op_sel_hi:[1,1,0]\n"
"v_pk_fma_f32 v[86:87],v[86:87],s[54:55],v[14:15] op_sel_hi:[1,1,0]\n"
CV "v171,v86,v87\n"
CV "v170,v84,v85\n"
CV "v169,v82,v83\n"
CV "v168,v80,v81\n"
AW "a24,v168\n"
AW "a25,v169\n"
AW "a26,v170\n"
AW "a27,v171\n"
"v_pk_fma_f32 v[72:73],v[72:73],s[54:55],v[14:15] op_sel_hi:[1,1,0]\n"
"v_pk_fma_f32 v[74:75],v[74:75],s[54:55],v[14:15] op_sel_hi:[1,1,0]\n"
"v_pk_fma_f32 v[76:77],v[76:77],s[54:55],v[14:15] op_sel_hi:[1,1,0]\n"
"v_pk_fma_f32 v[78:79],v[78:79],s[54:55],v[14:15] op_sel_hi:[1,1,0]\n"
CV "v167,v78,v79\n"
CV "v166,v76,v77\n"
CV "v165,v74,v75\n"
CV "v164,v72,v73\n"
AW "a20,v164\n"
AW "a21,v165\n"
AW "a22,v166\n"
AW "a23,v167\n"
"v_pk_fma_f32 v[64:65],v[64:65],s[54:55],v[14:15] op_sel_hi:[1,1,0]\n"
"v_pk_fma_f32 v[66:67],v[66:67],s[54:55],v[14:15] op_sel_hi:[1,1,0]\n"
"v_pk_fma_f32 v[68:69],v[68:69],s[54:55],v[14:15] op_sel_hi:[1,1,0]\n"
"v_pk_fma_f32 v[70:71],v[70:71],s[54:55],v[14:15] op_sel_hi:[1,1,0]\n"
CV "v163,v70,v71\n"
CV "v162,v68,v69\n"
CV "v161,v66,v67\n"
CV "v160,v64,v65\n"
AW "a176,v160\n"
AW "a177,v161\n"
AW "a178,v162\n"
AW "a179,v163\n"
"v_pk_fma_f32 v[56:57],v[56:57],s[54:55],v[14:15] op_sel_hi:[1,1,0]\n"
"v_pk_fma_f32 v[58:59],v[58:59],s[54:55],v[14:15] op_sel_hi:[1,1,0]\n"
"v_pk_fma_f32 v[60:61],v[60:61],s[54:55],v[14:15] op_sel_hi:[1,1,0]\n"
"v_pk_fma_f32 v[62:63],v[62:63],s[54:55],v[14:15] op_sel_hi:[1,1,0]\n"
CV "v159,v62,v63\n"
CV "v158,v60,v61\n"
CV "v157,v58,v59\n"
CV "v156,v56,v57\n"
AW "a172,v156\n"
AW "a173,v157\n"
AW "a174,v158\n"
AW "a175,v159\n"
"v_pk_mul_f32 v[48:49],v[48:49],s[54:55]\n"
"v_pk_mul_f32 v[50:51],v[50:51],s[54:55]\n"
"v_pk_mul_f32 v[52:53],v[52:53],s[54:55]\n"
"v_pk_mul_f32 v[54:55],v[54:55],s[54:55]\n"
CV "v155,v54,v55\n"
CV "v154,v52,v53\n"
CV "v153,v50,v51\n"
CV "v152,v48,v49\n"
AW "a168,v152\n"
AW "a169,v153\n"
AW "a170,v154\n"
AW "a171,v155\n"
"v_pk_mul_f32 v[40:41],v[40:41],s[54:55]\n"
"v_pk_mul_f32 v[42:43],v[42:43],s[54:55]\n"
"v_pk_mul_f32 v[44:45],v[44:45],s[54:55]\n"
"v_pk_mul_f32 v[46:47],v[46:47],s[54:55]\n"
CV "v151,v46,v47\n"
CV "v150,v44,v45\n"
CV "v149,v42,v43\n"
CV "v148,v40,v41\n"
AW "a120,v148\n"
AW "a121,v149\n"
AW "a122,v150\n"
AW "a123,v151\n"
"v_pk_mul_f32 v[32:33],v[32:33],s[54:55]\n"
"v_pk_mul_f32 v[34:35],v[34:35],s[54:55]\n"
"v_pk_mul_f32 v[36:37],v[36:37],s[54:55]\n"
"v_pk_mul_f32 v[38:39],v[38:39],s[54:55]\n"
CV "v147,v38,v39\n"
CV "v146,v36,v37\n"
CV "v145,v34,v35\n"
CV "v144,v32,v33\n"
AW "a96,v144\n"
AW "a97,v145\n"
AW "a98,v146\n"
AW "a99,v147\n"
"v_pk_mul_f32 v[24:25],v[24:25],s[54:55]\n"
"v_pk_mul_f32 v[26:27],v[26:27],s[54:55]\n"
"v_pk_mul_f32 v[28:29],v[28:29],s[54:55]\n"
"v_pk_mul_f32 v[30:31],v[30:31],s[54:55]\n"
CV "v143,v30,v31\n"
CV "v142,v28,v29\n"
CV "v141,v26,v27\n"
CV "v140,v24,v25\n"
AW "a92,v140\n"
AW "a93,v141\n"
AW "a94,v142\n"
AW "a95,v143\n"
"v_pk_mul_f32 v[16:17],v[16:17],s[54:55]\n"
"v_pk_mul_f32 v[18:19],v[18:19],s[54:55]\n"
"v_pk_mul_f32 v[20:21],v[20:21],s[54:55]\n"
"v_pk_mul_f32 v[22:23],v[22:23],s[54:55]\n"
CV "v139,v22,v23\n"
CV "v138,v20,v21\n"
CV "v137,v18,v19\n"
CV "v136,v16,v17\n"
AW "a88,v136\n"
AW "a89,v137\n"
AW "a90,v138\n"
AW "a91,v139\n"
"s_waitcnt lgkmcnt(0)\n"
"s_branch .Lq0\n"
".Lpgpsl_b:\n"
"s_cbranch_execz .Lpg3a\n"
".Lpgpsl_d:\n"
"s_cbranch_execz .Lisr2\n"
".Lpsl:\n"
"s_sleep 1\n"
".Lpsub:\n"
"s_memrealtime s[100:101]\n"
"s_waitcnt lgkmcnt(0)\n"
"s_sub_u32 s101,s21,s100\n"
"s_cmp_gt_i32 s101,0\n"
"s_cbranch_scc1 .Lpsl\n"
"s_add_u32 s21,s100,s97\n"
"s_setpc_b64 s[52:53]\n"
".Lstub_6:\n"
"v_cmp_le_f32 vcc,0x47800000,v10\n"
"s_cmp_eq_u32 s61,1\n"
"s_cbranch_scc0 .Lhook_6\n"
"s_sub_u32 s58,s71,s70\n"
"s_add_u32 s72,s72,s58\n"
"s_add_u32 s16,s16,s58\n"
"s_add_u32 s17,s17,s58\n"
"s_cmp_eq_u32 s57,0\n"
"s_cselect_b32 s34,0,s34\n"
"s_cselect_b32 s64,163840,s64\n"
"s_mov_b64 exec,0\n"
"s_bitcmp1_b32 s23,12\n"
"s_cbranch_scc1 .Lisy3\n"
"s_cmp_eq_u32 s56,0\n"
"s_cbranch_scc1 .Lpgq0_f\n"
"s_cmp_eq_u32 s56,1\n"
"s_cbranch_scc1 .Lpg4f\n"
"s_cmp_eq_u32 s56,2\n"
"s_cbranch_scc1 .Lpg5f\n"
"s_branch .Lpg6f\n"
".Lisy3:\n"
"s_cmp_eq_u32 s56,0\n"
"s_cbranch_scc1 .Lpgep_g\n"
"s_cmp_eq_u32 s56,1\n"
"s_cbranch_scc1 .Lpg15g\n"
"s_cmp_eq_u32 s56,2\n"
"s_cbranch_scc1 .Lpgt0e_g\n"
"s_branch .Lisr3\n"
".Lisr3:\n"
"s_mov_b64 exec,-1\n"
".Lisx3:\n"
".Lhook_6:\n"
"s_cmp_eq_u32 s77,0\n"
"s_cbranch_scc0 .Lnop_6\n"
"s_call_b64 s[52:53],.Lpsub\n"
".Lnop_6:\n"
"s_cbranch_vccz .Lback_6\n"
"s_call_b64 s[52:53],.Lslow_1\n"
"s_branch .Lback_6\n"
".Lstub_7:\n"
"v_cmp_le_f32 vcc,0x47800000,v10\n"
"s_cmp_eq_u32 s61,1\n"
"s_cbranch_scc0 .Lhook_7\n"
"s_sub_u32 s58,s71,s70\n"
"s_add_u32 s73,s73,s58\n"
"s_add_u32 s18,s18,s58\n"
"s_add_u32 s19,s19,s58\n"
"s_cmp_eq_u32 s57,0\n"
"s_cselect_b32 s38,0,s38\n"
"s_cselect_b32 s65,163840,s65\n"
".Lhook_7:\n"
"s_cmp_eq_u32 s77,0\n"
"s_cbranch_scc0 .Lnop_7\n"
"s_call_b64 s[52:53],.Lpsub\n"
"s_cmp_eq_u32 s61,4\n"
"s_cselect_b32 s77,0x47800000,s77\n"
"s_cselect_b32 s95,0x47800000,s95\n"
".Lnop_7:\n"
"s_cbranch_vccz .Lback_7\n"
"s_call_b64 s[52:53],.Lslow_0\n"
"s_branch .Lback_7\n"
".Lstub_8:\n"
"s_call_b64 s[52:53],.Lslow_1\n"
"s_branch .Lback_8\n"
".Lstub_11:\n"
"v_cmp_le_f32 vcc,0x47800000,v10\n"
"s_cmp_eq_u32 s77,0\n"
"s_cbranch_scc0 .Lnop_11\n"
"s_call_b64 s[52:53],.Lpsub\n"
".Lnop_11:\n"
"s_cbranch_vccz .Lback_11\n"
"s_call_b64 s[52:53],.Lslow_1\n"
"s_branch .Lback_11\n"
".Lstub_12:\n"
"v_cmp_le_f32 vcc,0x47800000,v10\n"
"s_cmp_eq_u32 s77,0\n"
"s_cbranch_scc0 .Lnop_12\n"
"s_call_b64 s[52:53],.Lpsub\n"
".Lnop_12:\n"
"s_cbranch_vccz .Lback_12\n"
"s_call_b64 s[52:53],.Lslow_0\n"
"s_branch .Lback_12\n"
".Lstub_13:\n"
"v_cmp_le_f32 vcc,0x47800000,v10\n"
"s_cmp_eq_u32 s61,1\n"
"s_cbranch_scc0 .Lhook_13\n"
"s_mov_b64 exec,0\n"
"s_cmp_eq_u32 s56,0\n"
"s_cbranch_scc1 .Lpg24h\n"
"s_cmp_eq_u32 s56,1\n"
"s_cbranch_scc1 .Lpg23h\n"
"s_cmp_eq_u32 s56,2\n"
"s_cbranch_scc1 .Lpg22h\n"
"s_branch .Lpg21h\n"
".Lisr4:\n"
"s_mov_b64 exec,-1\n"
".Lisx4:\n"
"s_mov_b32 s97,0\n"
".Lhook_13:\n"
"s_cmp_eq_u32 s77,0\n"
"s_cbranch_scc0 .Lnop_13\n"
"s_call_b64 s[52:53],.Lpsub\n"
".Lnop_13:\n"
"s_cbranch_vccz .Lback_13\n"
"s_call_b64 s[52:53],.Lslow_1\n"
"s_branch .Lback_13\n"
".Lstub_14:\n"
"v_cmp_le_f32 vcc,0x47800000,v10\n"
"s_cmp_eq_u32 s77,0\n"
"s_cbranch_scc0 .Lnop_14\n"
"s_call_b64 s[52:53],.Lpsub\n"
".Lnop_14:\n"
"s_cbranch_vccz .Lback_14\n"
"s_call_b64 s[52:53],.Lslow_0\n"
"s_branch .Lback_14\n"
".Lstub_15:\n"
"v_cmp_le_f32 vcc,0x47800000,v10\n"
"s_cmp_eq_u32 s61,1\n"
"s_cbranch_scc0 .Lhook_15\n"
"s_mov_b32 s34,0\n"
"s_mov_b32 s64,163840\n"
".Lhook_15:\n"
"s_cmp_eq_u32 s77,0\n"
"s_cbranch_scc0 .Lnop_15\n"
"s_call_b64 s[52:53],.Lpsub\n"
".Lnop_15:\n"
"s_cbranch_vccz .Lback_15\n"
"s_call_b64 s[52:53],.Lslow_1\n"
"s_branch .Lback_15\n"
".Lstub_16:\n"
"v_cmp_le_f32 vcc,0x47800000,v10\n"
"s_cmp_eq_u32 s77,0\n"
"s_cbranch_scc0 .Lnop_16\n"
"s_call_b64 s[52:53],.Lpsub\n"
".Lnop_16:\n"
"s_cbranch_vccz .Lback_16\n"
"s_call_b64 s[52:53],.Lslow_0\n"
"s_branch .Lback_16\n"
".Lstub_17:\n"
"s_call_b64 s[52:53],.Lslow_1\n"
"s_branch .Lback_17\n"
".Lackspin_q:\n"
"s_cmp_eq_u32 s98,0\n"
"s_cbranch_scc1 .Lackok_q\n"
"s_sub_u32 s98,s98,1\n"
"s_sleep 8\n"
"v_bfe_u32 v14,v9,4,5\n"
"v_lshlrev_b32 v14,6,v14\n"
"s_lshl_b32 s58,s78,2\n"
"s_add_u32 s58,s58,s86\n"
"buffer_load_dword v10,v14,s[40:43],s58 offen offset:128 sc1\n"
"s_waitcnt vmcnt(0)\n"
"s_branch .Lack_q\n"
".Lpgep_g:\n"
"s_cbranch_execz .Lisr3\n"
".Lep:\n"
"s_lshl_b32 s58,s56,10\n"
"s_add_u32 s58,s58,73728\n"
"v_add_u32 v120,s58,v9\n"
"v_add_u32 v121,65536,v120\n"
"s_sub_u32 s59,s71,s70\n"
"v_mov_b32 v10,1.0\n"
TR "v[136:137],v2 offset:24576\n"
TR "v[138:139],v2 offset:30720\n"
"v_mfma_f32_16x16x4_f32 v[16:19],v10,v57,0\n"
TR "v[140:141],v3 offset:24576\n"
TR "v[142:143],v3 offset:30720\n"
TR "v[144:145],v4 offset:24576\n"
TR "v[146:147],v4 offset:30720\n"
TR "v[148:149],v5 offset:24576\n"
TR "v[150:151],v5 offset:30720\n"
"v_mfma_f32_16x16x4_f32 v[20:23],v10,v58,0\n"
TR "v[152:153],v2 offset:25600\n"
TR "v[154:155],v2 offset:31744\n"
TR "v[156:157],v3 offset:25600\n"
TR "v[158:159],v3 offset:31744\n"
TR "v[160:161],v4 offset:25600\n"
TR "v[162:163],v4 offset:31744\n"
"v_mfma_f32_16x16x4_f32 v[24:27],v10,v59,0\n"
TR "v[164:165],v5 offset:25600\n"
TR "v[166:167],v5 offset:31744\n"
TR "v[168:169],v2 offset:26624\n"
TR "v[170:171],v2 offset:32768\n"
TR "v[172:173],v3 offset:26624\n"
TR "v[174:175],v3 offset:32768\n"
"v_mfma_f32_16x16x4_f32 v[28:31],v10,v60,0\n"
TR "v[176:177],v4 offset:26624\n"
TR "v[178:179],v4 offset:32768\n"
TR "v[180:181],v5 offset:26624\n"
TR "v[182:183],v5 offset:32768\n"
"v_mfma_f32_16x16x4_f32 v[32:35],v10,v61,0\n"
"s_add_u32 m0,s65,49152\n"
"s_nop 0\n"
LD "v8,s[36:39],s73 offen offset:3072 nt lds\n"
"s_add_u32 m0,s65,50048\n"
"s_nop 0\n"
LD "v8,s[36:39],s73 offen offset:3200 nt lds\n"
"s_waitcnt lgkmcnt(15)\n"
MF "a[0:3],v[136:139],v[92:95],a[0:3]\n"
MF "a[20:23],v[140:143],v[92:95],a[20:23]\n"
"s_add_u32 m0,s65,50944\n"
"s_nop 0\n"
LD "v8,s[36:39],s73 offen offset:3328 nt lds\n"
"s_sub_u32 s74,s96,s16\n"
"s_add_u32 s74,s74,s71\n"
"s_add_u32 s20,s20,s59\n"
"s_mov_b32 s70,s71\n"
LD "v[232:235],v9,s[40:43],s75 offen sc1\n"
"v_rcp_f32 v57,v16\n"
"v_rcp_f32 v58,v20\n"
"v_rcp_f32 v59,v24\n"
"v_rcp_f32 v60,v28\n"
"v_rcp_f32 v61,v32\n"
MF "a[4:7],v[136:139],v[96:99],a[4:7]\n"
"v_mul_f32_e64 v52,-s13,v57\n"
"v_mul_f32_e64 v53,-s13,v58\n"
"v_mul_f32_e64 v54,-s13,v59\n"
"v_mul_f32_e64 v55,-s13,v60\n"
"v_mul_f32_e64 v56,-s13,v61\n"
AR "v72,a0\n"
AR "v73,a1\n"
AR "v74,a2\n"
MF "a[24:27],v[140:143],v[96:99],a[24:27]\n"
AR "v75,a3\n"
AR "v76,a20\n"
AR "v77,a21\n"
AR "v78,a22\n"
AR "v79,a23\n"
"v_pk_fma_f32 v[72:73],v[72:73],v[56:57],v[52:53] op_sel:[0,1,0] op_sel_hi:[1,1,0]\n"
"v_pk_fma_f32 v[74:75],v[74:75],v[56:57],v[52:53] op_sel:[0,1,0] op_sel_hi:[1,1,0]\n"
"v_pk_fma_f32 v[76:77],v[76:77],v[56:57],v[52:53] op_sel:[0,1,0] op_sel_hi:[1,1,0]\n"
"v_pk_fma_f32 v[78:79],v[78:79],v[56:57],v[52:53] op_sel:[0,1,0] op_sel_hi:[1,1,0]\n"
CV "v112,v72,v73\n"
CV "v113,v74,v75\n"
CV "v114,v76,v77\n"
CV "v115,v78,v79\n"
MF "a[8:11],v[136:139],v[100:103],a[8:11]\n"
"ds_read_b128 v[184:187],v120 offset:24576\n"
"ds_write_b128 v120,v[112:115] offset:24576\n"
LD "v[236:239],v9,s[40:43],s75 offen offset:1024 sc1\n"
AR "v80,a4\n"
AR "v81,a5\n"
AR "v82,a6\n"
MF "a[28:31],v[140:143],v[100:103],a[28:31]\n"
AR "v83,a7\n"
AR "v84,a24\n"
AR "v85,a25\n"
AR "v86,a26\n"
AR "v87,a27\n"
"v_pk_fma_f32 v[80:81],v[80:81],v[58:59],v[52:53] op_sel:[0,0,1] op_sel_hi:[1,0,1]\n"
"v_pk_fma_f32 v[82:83],v[82:83],v[58:59],v[52:53] op_sel:[0,0,1] op_sel_hi:[1,0,1]\n"
"v_pk_fma_f32 v[84:85],v[84:85],v[58:59],v[52:53] op_sel:[0,0,1] op_sel_hi:[1,0,1]\n"
"v_pk_fma_f32 v[86:87],v[86:87],v[58:59],v[52:53] op_sel:[0,0,1] op_sel_hi:[1,0,1]\n"
CV "v116,v80,v81\n"
CV "v117,v82,v83\n"
CV "v118,v84,v85\n"
CV "v119,v86,v87\n"
MF "a[12:15],v[136:139],v[104:107],a[12:15]\n"
"ds_read_b128 v[188:191],v120 offset:28672\n"
"ds_write_b128 v120,v[116:119] offset:28672\n"
AR "v72,a8\n"
AR "v73,a9\n"
AR "v74,a10\n"
MF "a[32:35],v[140:143],v[104:107],a[32:35]\n"
AR "v75,a11\n"
AR "v76,a28\n"
AR "v77,a29\n"
AR "v78,a30\n"
AR "v79,a31\n"
"v_pk_fma_f32 v[72:73],v[72:73],v[58:59],v[54:55] op_sel:[0,1,0] op_sel_hi:[1,1,0]\n"
"v_pk_fma_f32 v[74:75],v[74:75],v[58:59],v[54:55] op_sel:[0,1,0] op_sel_hi:[1,1,0]\n"
"v_pk_fma_f32 v[76:77],v[76:77],v[58:59],v[54:55] op_sel:[0,1,0] op_sel_hi:[1,1,0]\n"
"v_pk_fma_f32 v[78:79],v[78:79],v[58:59],v[54:55] op_sel:[0,1,0] op_sel_hi:[1,1,0]\n"
CV "v112,v72,v73\n"
CV "v113,v74,v75\n"
CV "v114,v76,v77\n"
CV "v115,v78,v79\n"
MF "a[16:19],v[136:139],v[108:111],a[16:19]\n"
"ds_read_b128 v[192:195],v120 offset:32768\n"
"ds_write_b128 v120,v[112:115] offset:32768\n"
"s_waitcnt lgkmcnt(0)\n"
"s_barrier\n"
LD "v[240:243],v9,s[40:43],s75 offen offset:2048 sc1\n"
AR "v80,a12\n"
AR "v81,a13\n"
AR "v82,a14\n"
MF "a[36:39],v[140:143],v[108:111],a[36:39]\n"
AR "v83,a15\n"
AR "v84,a32\n"
AR "v85,a33\n"
AR "v86,a34\n"
AR "v87,a35\n"
"v_pk_fma_f32 v[80:81],v[80:81],v[60:61],v[54:55] op_sel:[0,0,1] op_sel_hi:[1,0,1]\n"
"v_pk_fma_f32 v[82:83],v[82:83],v[60:61],v[54:55] op_sel:[0,0,1] op_sel_hi:[1,0,1]\n"
"v_pk_fma_f32 v[84:85],v[84:85],v[60:61],v[54:55] op_sel:[0,0,1] op_sel_hi:[1,0,1]\n"
"v_pk_fma_f32 v[86:87],v[86:87],v[60:61],v[54:55] op_sel:[0,0,1] op_sel_hi:[1,0,1]\n"
CV "v116,v80,v81\n"
CV "v117,v82,v83\n"
CV "v118,v84,v85\n"
CV "v119,v86,v87\n"
MF "a[40:43],v[144:147],v[92:95],a[40:43]\n"
"ds_read_b128 v[196:199],v120 offset:36864\n"
"ds_write_b128 v120,v[116:119] offset:36864\n"
LD "v[244:247],v9,s[40:43],s75 offen offset:3072 sc1\n"
AR "v72,a16\n"
AR "v73,a17\n"
AR "v74,a18\n"
MF "a[60:63],v[148:151],v[92:95],a[60:63]\n"
AR "v75,a19\n"
AR "v76,a36\n"
AR "v77,a37\n"
AR "v78,a38\n"
AR "v79,a39\n"
"v_pk_fma_f32 v[72:73],v[72:73],v[60:61],v[56:57] op_sel:[0,1,0] op_sel_hi:[1,1,0]\n"
"v_pk_fma_f32 v[74:75],v[74:75],v[60:61],v[56:57] op_sel:[0,1,0] op_sel_hi:[1,1,0]\n"
"v_pk_fma_f32 v[76:77],v[76:77],v[60:61],v[56:57] op_sel:[0,1,0] op_sel_hi:[1,1,0]\n"
"v_pk_fma_f32 v[78:79],v[78:79],v[60:61],v[56:57] op_sel:[0,1,0] op_sel_hi:[1,1,0]\n"
CV "v112,v72,v73\n"
CV "v113,v74,v75\n"
CV "v114,v76,v77\n"
CV "v115,v78,v79\n"
MF "a[44:47],v[144:147],v[96:99],a[44:47]\n"
"ds_read_b128 v[200:203],v120 offset:40960\n"
"ds_write_b128 v120,v[112:115] offset:40960\n"
AR "v80,a40\n"
AR "v81,a41\n"
AR "v82,a42\n"
MF "a[64:67],v[148:151],v[96:99],a[64:67]\n"
AR "v83,a43\n"
AR "v84,a60\n"
AR "v85,a61\n"
AR "v86,a62\n"
AR "v87,a63\n"
"v_pk_fma_f32 v[80:81],v[80:81],v[56:57],v[52:53] op_sel:[0,1,0] op_sel_hi:[1,1,0]\n"
"v_pk_fma_f32 v[82:83],v[82:83],v[56:57],v[52:53] op_sel:[0,1,0] op_sel_hi:[1,1,0]\n"
"v_pk_fma_f32 v[84:85],v[84:85],v[56:57],v[52:53] op_sel:[0,1,0] op_sel_hi:[1,1,0]\n"
"v_pk_fma_f32 v[86:87],v[86:87],v[56:57],v[52:53] op_sel:[0,1,0] op_sel_hi:[1,1,0]\n"
CV "v116,v80,v81\n"
CV "v117,v82,v83\n"
CV "v118,v84,v85\n"
CV "v119,v86,v87\n"
MF "a[48:51],v[144:147],v[100:103],a[48:51]\n"
"ds_read_b128 v[204:207],v120 offset:45056\n"
"ds_write_b128 v120,v[116:119] offset:45056\n"
"s_add_u32 s58,s75,4096\n"
LD "v[248:251],v9,s[40:43],s58 offen sc1\n"
AR "v72,a44\n"
AR "v73,a45\n"
AR "v74,a46\n"
MF "a[68:71],v[148:151],v[100:103],a[68:71]\n"
AR "v75,a47\n"
AR "v76,a64\n"
AR "v77,a65\n"
AR "v78,a66\n"
AR "v79,a67\n"
"v_pk_fma_f32 v[72:73],v[72:73],v[58:59],v[52:53] op_sel:[0,0,1] op_sel_hi:[1,0,1]\n"
"v_pk_fma_f32 v[74:75],v[74:75],v[58:59],v[52:53] op_sel:[0,0,1] op_sel_hi:[1,0,1]\n"
"v_pk_fma_f32 v[76:77],v[76:77],v[58:59],v[52:53] op_sel:[0,0,1] op_sel_hi:[1,0,1]\n"
"v_pk_fma_f32 v[78:79],v[78:79],v[58:59],v[52:53] op_sel:[0,0,1] op_sel_hi:[1,0,1]\n"
CV "v112,v72,v73\n"
CV "v113,v74,v75\n"
CV "v114,v76,v77\n"
CV "v115,v78,v79\n"
MF "a[52:55],v[144:147],v[104:107],a[52:55]\n"
"ds_read_b128 v[208:211],v120 offset:49152\n"
"ds_write_b128 v120,v[112:115] offset:49152\n"
LD "v[252:255],v9,s[40:43],s58 offen offset:1024 sc1\n"
AR "v80,a48\n"
AR "v81,a49\n"
AR "v82,a50\n"
MF "a[72:75],v[148:151],v[104:107],a[72:75]\n"
AR "v83,a51\n"
AR "v84,a68\n"
AR "v85,a69\n"
AR "v86,a70\n"
AR "v87,a71\n"
"v_pk_fma_f32 v[80:81],v[80:81],v[58:59],v[54:55] op_sel:[0,1,0] op_sel_hi:[1,1,0]\n"
"v_pk_fma_f32 v[82:83],v[82:83],v[58:59],v[54:55] op_sel:[0,1,0] op_sel_hi:[1,1,0]\n"
"v_pk_fma_f32 v[84:85],v[84:85],v[58:59],v[54:55] op_sel:[0,1,0] op_sel_hi:[1,1,0]\n"
"v_pk_fma_f32 v[86:87],v[86:87],v[58:59],v[54:55] op_sel:[0,1,0] op_sel_hi:[1,1,0]\n"
CV "v116,v80,v81\n"
CV "v117,v82,v83\n"
CV "v118,v84,v85\n"
CV "v119,v86,v87\n"
MF "a[56:59],v[144:147],v[108:111],a[56:59]\n"
"ds_read_b128 v[212:215],v120 offset:53248\n"
"ds_write_b128 v120,v[116:119] offset:53248\n"
AR "v72,a52\n"
AR "v73,a53\n"
AR "v74,a54\n"
MF "a[76:79],v[148:151],v[108:111],a[76:79]\n"
AR "v75,a55\n"
AR "v76,a72\n"
AR "v77,a73\n"
AR "v78,a74\n"
AR "v79,a75\n"
"v_pk_fma_f32 v[72:73],v[72:73],v[60:61],v[54:55] op_sel:[0,0,1] op_sel_hi:[1,0,1]\n"
"v_pk_fma_f32 v[74:75],v[74:75],v[60:61],v[54:55] op_sel:[0,0,1] op_sel_hi:[1,0,1]\n"
"v_pk_fma_f32 v[76:77],v[76:77],v[60:61],v[54:55] op_sel:[0,0,1] op_sel_hi:[1,0,1]\n"
"v_pk_fma_f32 v[78:79],v[78:79],v[60:61],v[54:55] op_sel:[0,0,1] op_sel_hi:[1,0,1]\n"
CV "v112,v72,v73\n"
CV "v113,v74,v75\n"
CV "v114,v76,v77\n"
CV "v115,v78,v79\n"
MF "a[80:83],v[152:155],v[92:95],a[80:83]\n"
"ds_read_b128 v[216:219],v120 offset:57344\n"
"ds_write_b128 v120,v[112:115] offset:57344\n"
AR "v80,a56\n"
AR "v81,a57\n"
AR "v82,a58\n"
MF "a[100:103],v[156:159],v[92:95],a[100:103]\n"
AR "v83,a59\n"
AR "v84,a76\n"
AR "v85,a77\n"
AR "v86,a78\n"
AR "v87,a79\n"
"v_pk_fma_f32 v[80:81],v[80:81],v[60:61],v[56:57] op_sel:[0,1,0] op_sel_hi:[1,1,0]\n"
"v_pk_fma_f32 v[82:83],v[82:83],v[60:61],v[56:57] op_sel:[0,1,0] op_sel_hi:[1,1,0]\n"
"v_pk_fma_f32 v[84:85],v[84:85],v[60:61],v[56:57] op_sel:[0,1,0] op_sel_hi:[1,1,0]\n"
"v_pk_fma_f32 v[86:87],v[86:87],v[60:61],v[56:57] op_sel:[0,1,0] op_sel_hi:[1,1,0]\n"
CV "v116,v80,v81\n"
CV "v117,v82,v83\n"
CV "v118,v84,v85\n"
CV "v119,v86,v87\n"
MF "a[84:87],v[152:155],v[96:99],a[84:87]\n"
"ds_read_b128 v[220:223],v120 offset:61440\n"
"ds_write_b128 v120,v[116:119] offset:61440\n"
AR "v72,a80\n"
AR "v73,a81\n"
AR "v74,a82\n"
MF "a[104:107],v[156:159],v[96:99],a[104:107]\n"
AR "v75,a83\n"
AR "v76,a100\n"
AR "v77,a101\n"
AR "v78,a102\n"
AR "v79,a103\n"
"v_pk_fma_f32 v[72:73],v[72:73],v[56:57],v[52:53] op_sel:[0,1,0] op_sel_hi:[1,1,0]\n"
"v_pk_fma_f32 v[74:75],v[74:75],v[56:57],v[52:53] op_sel:[0,1,0] op_sel_hi:[1,1,0]\n"
"v_pk_fma_f32 v[76:77],v[76:77],v[56:57],v[52:53] op_sel:[0,1,0] op_sel_hi:[1,1,0]\n"
"v_pk_fma_f32 v[78:79],v[78:79],v[56:57],v[52:53] op_sel:[0,1,0] op_sel_hi:[1,1,0]\n"
CV "v112,v72,v73\n"
CV "v113,v74,v75\n"
CV "v114,v76,v77\n"
CV "v115,v78,v79\n"
MF "a[88:91],v[152:155],v[100:103],a[88:91]\n"
"ds_read_b128 v[224:227],v121\n"
"ds_write_b128 v121,v[112:115]\n"
AR "v80,a84\n"
AR "v81,a85\n"
AR "v82,a86\n"
MF "a[108:111],v[156:159],v[100:103],a[108:111]\n"
AR "v83,a87\n"
AR "v84,a104\n"
AR "v85,a105\n"
AR "v86,a106\n"
AR "v87,a107\n"
"v_pk_fma_f32 v[80:81],v[80:81],v[58:59],v[52:53] op_sel:[0,0,1] op_sel_hi:[1,0,1]\n"
"v_pk_fma_f32 v[82:83],v[82:83],v[58:59],v[52:53] op_sel:[0,0,1] op_sel_hi:[1,0,1]\n"
"v_pk_fma_f32 v[84:85],v[84:85],v[58:59],v[52:53] op_sel:[0,0,1] op_sel_hi:[1,0,1]\n"
"v_pk_fma_f32 v[86:87],v[86:87],v[58:59],v[52:53] op_sel:[0,0,1] op_sel_hi:[1,0,1]\n"
CV "v116,v80,v81\n"
CV "v117,v82,v83\n"
CV "v118,v84,v85\n"
CV "v119,v86,v87\n"
MF "a[92:95],v[152:155],v[104:107],a[92:95]\n"
"ds_read_b128 v[228:231],v121 offset:4096\n"
"ds_write_b128 v121,v[116:119] offset:4096\n"
AR "v72,a88\n"
AR "v73,a89\n"
AR "v74,a90\n"
MF "a[112:115],v[156:159],v[104:107],a[112:115]\n"
AR "v75,a91\n"
AR "v76,a108\n"
AR "v77,a109\n"
".Lpg15g:\n"
"s_cbranch_execz .Lisr3\n"
"s_nop 0\n"
AR "v78,a110\n"
AR "v79,a111\n"
"v_pk_fma_f32 v[72:73],v[72:73],v[58:59],v[54:55] op_sel:[0,1,0] op_sel_hi:[1,1,0]\n"
"v_pk_fma_f32 v[74:75],v[74:75],v[58:59],v[54:55] op_sel:[0,1,0] op_sel_hi:[1,1,0]\n"
"v_pk_fma_f32 v[76:77],v[76:77],v[58:59],v[54:55] op_sel:[0,1,0] op_sel_hi:[1,1,0]\n"
"v_pk_fma_f32 v[78:79],v[78:79],v[58:59],v[54:55] op_sel:[0,1,0] op_sel_hi:[1,1,0]\n"
CV "v112,v72,v73\n"
CV "v113,v74,v75\n"
CV "v114,v76,v77\n"
CV "v115,v78,v79\n"
MF "a[96:99],v[152:155],v[108:111],a[96:99]\n"
"ds_read_b128 v[136:139],v121 offset:8192\n"
"ds_write_b128 v121,v[112:115] offset:8192\n"
AR "v80,a92\n"
AR "v81,a93\n"
AR "v82,a94\n"
MF "a[116:119],v[156:159],v[108:111],a[116:119]\n"
AR "v83,a95\n"
AR "v84,a112\n"
AR "v85,a113\n"
AR "v86,a114\n"
AR "v87,a115\n"
"v_pk_fma_f32 v[80:81],v[80:81],v[60:61],v[54:55] op_sel:[0,0,1] op_sel_hi:[1,0,1]\n"
"v_pk_fma_f32 v[82:83],v[82:83],v[60:61],v[54:55] op_sel:[0,0,1] op_sel_hi:[1,0,1]\n"
"v_pk_fma_f32 v[84:85],v[84:85],v[60:61],v[54:55] op_sel:[0,0,1] op_sel_hi:[1,0,1]\n"
"v_pk_fma_f32 v[86:87],v[86:87],v[60:61],v[54:55] op_sel:[0,0,1] op_sel_hi:[1,0,1]\n"
CV "v116,v80,v81\n"
CV "v117,v82,v83\n"
CV "v118,v84,v85\n"
CV "v119,v86,v87\n"
MF "a[120:123],v[160:163],v[92:95],a[120:123]\n"
"ds_read_b128 v[140:143],v121 offset:12288\n"
"ds_write_b128 v121,v[116:119] offset:12288\n"
AR "v72,a96\n"
AR "v73,a97\n"
AR "v74,a98\n"
MF "a[140:143],v[164:167],v[92:95],a[140:143]\n"
AR "v75,a99\n"
AR "v76,a116\n"
AR "v77,a117\n"
AR "v78,a118\n"
AR "v79,a119\n"
"v_pk_fma_f32 v[72:73],v[72:73],v[60:61],v[56:57] op_sel:[0,1,0] op_sel_hi:[1,1,0]\n"
"v_pk_fma_f32 v[74:75],v[74:75],v[60:61],v[56:57] op_sel:[0,1,0] op_sel_hi:[1,1,0]\n"
"v_pk_fma_f32 v[76:77],v[76:77],v[60:61],v[56:57] op_sel:[0,1,0] op_sel_hi:[1,1,0]\n"
"v_pk_fma_f32 v[78:79],v[78:79],v[60:61],v[56:57] op_sel:[0,1,0] op_sel_hi:[1,1,0]\n"
CV "v112,v72,v73\n"
CV "v113,v74,v75\n"
CV "v114,v76,v77\n"
CV "v115,v78,v79\n"
MF "a[124:127],v[160:163],v[96:99],a[124:127]\n"
"ds_read_b128 v[144:147],v121 offset:16384\n"
"ds_write_b128 v121,v[112:115] offset:16384\n"
AR "v80,a120\n"
AR "v81,a121\n"
AR "v82,a122\n"
MF "a[144:147],v[164:167],v[96:99],a[144:147]\n"
AR "v83,a123\n"
AR "v84,a140\n"
AR "v85,a141\n"
AR "v86,a142\n"
AR "v87,a143\n"
"v_pk_fma_f32 v[80:81],v[80:81],v[56:57],v[52:53] op_sel:[0,1,0] op_sel_hi:[1,1,0]\n"
"v_pk_fma_f32 v[82:83],v[82:83],v[56:57],v[52:53] op_sel:[0,1,0] op_sel_hi:[1,1,0]\n"
"v_pk_fma_f32 v[84:85],v[84:85],v[56:57],v[52:53] op_sel:[0,1,0] op_sel_hi:[1,1,0]\n"
"v_pk_fma_f32 v[86:87],v[86:87],v[56:57],v[52:53] op_sel:[0,1,0] op_sel_hi:[1,1,0]\n"
CV "v116,v80,v81\n"
CV "v117,v82,v83\n"
CV "v118,v84,v85\n"
CV "v119,v86,v87\n"
MF "a[128:131],v[160:163],v[100:103],a[128:131]\n"
"ds_read_b128 v[148:151],v121 offset:20480\n"
"ds_write_b128 v121,v[116:119] offset:20480\n"
AR "v72,a124\n"
AR "v73,a125\n"
AR "v74,a126\n"
MF "a[148:151],v[164:167],v[100:103],a[148:151]\n"
AR "v75,a127\n"
AR "v76,a144\n"
AR "v77,a145\n"
AR "v78,a146\n"
AR "v79,a147\n"
"v_pk_fma_f32 v[72:73],v[72:73],v[58:59],v[52:53] op_sel:[0,0,1] op_sel_hi:[1,0,1]\n"
"v_pk_fma_f32 v[74:75],v[74:75],v[58:59],v[52:53] op_sel:[0,0,1] op_sel_hi:[1,0,1]\n"
"v_pk_fma_f32 v[76:77],v[76:77],v[58:59],v[52:53] op_sel:[0,0,1] op_sel_hi:[1,0,1]\n"
"v_pk_fma_f32 v[78:79],v[78:79],v[58:59],v[52:53] op_sel:[0,0,1] op_sel_hi:[1,0,1]\n"
CV "v112,v72,v73\n"
CV "v113,v74,v75\n"
CV "v114,v76,v77\n"
CV "v115,v78,v79\n"
MF "a[132:135],v[160:163],v[104:107],a[132:135]\n"
"ds_write_b128 v120,v[112:115]\n"
AR "v80,a128\n"
AR "v81,a129\n"
AR "v82,a130\n"
MF "a[152:155],v[164:167],v[104:107],a[152:155]\n"
AR "v83,a131\n"
AR "v84,a148\n"
AR "v85,a149\n"
AR "v86,a150\n"
AR "v87,a151\n"
"v_pk_fma_f32 v[80:81],v[80:81],v[58:59],v[54:55] op_sel:[0,1,0] op_sel_hi:[1,1,0]\n"
"v_pk_fma_f32 v[82:83],v[82:83],v[58:59],v[54:55] op_sel:[0,1,0] op_sel_hi:[1,1,0]\n"
"v_pk_fma_f32 v[84:85],v[84:85],v[58:59],v[54:55] op_sel:[0,1,0] op_sel_hi:[1,1,0]\n"
"v_pk_fma_f32 v[86:87],v[86:87],v[58:59],v[54:55] op_sel:[0,1,0] op_sel_hi:[1,1,0]\n"
CV "v116,v80,v81\n"
CV "v117,v82,v83\n"
CV "v118,v84,v85\n"
CV "v119,v86,v87\n"
MF "a[136:139],v[160:163],v[108:111],a[136:139]\n"
"ds_write_b128 v120,v[116:119] offset:4096\n"
AR "v72,a132\n"
AR "v73,a133\n"
AR "v74,a134\n"
MF "a[156:159],v[164:167],v[108:111],a[156:159]\n"
AR "v75,a135\n"
AR "v76,a152\n"
AR "v77,a153\n"
AR "v78,a154\n"
AR "v79,a155\n"
"v_pk_fma_f32 v[72:73],v[72:73],v[60:61],v[54:55] op_sel:[0,0,1] op_sel_hi:[1,0,1]\n"
"v_pk_fma_f32 v[74:75],v[74:75],v[60:61],v[54:55] op_sel:[0,0,1] op_sel_hi:[1,0,1]\n"
"v_pk_fma_f32 v[76:77],v[76:77],v[60:61],v[54:55] op_sel:[0,0,1] op_sel_hi:[1,0,1]\n"
"v_pk_fma_f32 v[78:79],v[78:79],v[60:61],v[54:55] op_sel:[0,0,1] op_sel_hi:[1,0,1]\n"
CV "v112,v72,v73\n"
CV "v113,v74,v75\n"
CV "v114,v76,v77\n"
CV "v115,v78,v79\n"
MF "a[160:163],v[168:171],v[92:95],a[160:163]\n"
"ds_write_b128 v120,v[112:115] offset:8192\n"
AR "v80,a136\n"
AR "v81,a137\n"
AR "v82,a138\n"
MF "a[180:183],v[172:175],v[92:95],a[180:183]\n"
AR "v83,a139\n"
AR "v84,a156\n"
AR "v85,a157\n"
AR "v86,a158\n"
AR "v87,a159\n"
"v_pk_fma_f32 v[80:81],v[80:81],v[60:61],v[56:57] op_sel:[0,1,0] op_sel_hi:[1,1,0]\n"
"v_pk_fma_f32 v[82:83],v[82:83],v[60:61],v[56:57] op_sel:[0,1,0] op_sel_hi:[1,1,0]\n"
"v_pk_fma_f32 v[84:85],v[84:85],v[60:61],v[56:57] op_sel:[0,1,0] op_sel_hi:[1,1,0]\n"
"v_pk_fma_f32 v[86:87],v[86:87],v[60:61],v[56:57] op_sel:[0,1,0] op_sel_hi:[1,1,0]\n"
CV "v116,v80,v81\n"
CV "v117,v82,v83\n"
CV "v118,v84,v85\n"
CV "v119,v86,v87\n"
MF "a[164:167],v[168:171],v[96:99],a[164:167]\n"
"ds_write_b128 v120,v[116:119] offset:12288\n"
AR "v72,a160\n"
AR "v73,a161\n"
AR "v74,a162\n"
MF "a[184:187],v[172:175],v[96:99],a[184:187]\n"
AR "v75,a163\n"
AR "v76,a180\n"
AR "v77,a181\n"
AR "v78,a182\n"
AR "v79,a183\n"
"v_pk_fma_f32 v[72:73],v[72:73],v[56:57],v[52:53] op_sel:[0,1,0] op_sel_hi:[1,1,0]\n"
"v_pk_fma_f32 v[74:75],v[74:75],v[56:57],v[52:53] op_sel:[0,1,0] op_sel_hi:[1,1,0]\n"
"v_pk_fma_f32 v[76:77],v[76:77],v[56:57],v[52:53] op_sel:[0,1,0] op_sel_hi:[1,1,0]\n"
"v_pk_fma_f32 v[78:79],v[78:79],v[56:57],v[52:53] op_sel:[0,1,0] op_sel_hi:[1,1,0]\n"
CV "v112,v72,v73\n"
CV "v113,v74,v75\n"
CV "v114,v76,v77\n"
CV "v115,v78,v79\n"
MF "a[168:171],v[168:171],v[100:103],a[168:171]\n"
"ds_write_b128 v120,v[112:115] offset:16384\n"
AR "v80,a164\n"
AR "v81,a165\n"
AR "v82,a166\n"
MF "a[188:191],v[172:175],v[100:103],a[188:191]\n"
AR "v83,a167\n"
AR "v84,a184\n"
AR "v85,a185\n"
AR "v86,a186\n"
AR "v87,a187\n"
"v_pk_fma_f32 v[80:81],v[80:81],v[58:59],v[52:53] op_sel:[0,0,1] op_sel_hi:[1,0,1]\n"
"v_pk_fma_f32 v[82:83],v[82:83],v[58:59],v[52:53] op_sel:[0,0,1] op_sel_hi:[1,0,1]\n"
"v_pk_fma_f32 v[84:85],v[84:85],v[58:59],v[52:53] op_sel:[0,0,1] op_sel_hi:[1,0,1]\n"
"v_pk_fma_f32 v[86:87],v[86:87],v[58:59],v[52:53] op_sel:[0,0,1] op_sel_hi:[1,0,1]\n"
CV "v116,v80,v81\n"
CV "v117,v82,v83\n"
CV "v118,v84,v85\n"
CV "v119,v86,v87\n"
MF "a[172:175],v[168:171],v[104:107],a[172:175]\n"
"ds_write_b128 v120,v[116:119] offset:20480\n"
AR "v72,a168\n"
AR "v73,a169\n"
AR "v74,a170\n"
MF "a[192:195],v[172:175],v[104:107],a[192:195]\n"
AR "v75,a171\n"
AR "v76,a188\n"
AR "v77,a189\n"
AR "v78,a190\n"
AR "v79,a191\n"
"v_pk_fma_f32 v[72:73],v[72:73],v[58:59],v[54:55] op_sel:[0,1,0] op_sel_hi:[1,1,0]\n"
"v_pk_fma_f32 v[74:75],v[74:75],v[58:59],v[54:55] op_sel:[0,1,0] op_sel_hi:[1,1,0]\n"
"v_pk_fma_f32 v[76:77],v[76:77],v[58:59],v[54:55] op_sel:[0,1,0] op_sel_hi:[1,1,0]\n"
"v_pk_fma_f32 v[78:79],v[78:79],v[58:59],v[54:55] op_sel:[0,1,0] op_sel_hi:[1,1,0]\n"
CV "v112,v72,v73\n"
CV "v113,v74,v75\n"
CV "v114,v76,v77\n"
CV "v115,v78,v79\n"
MF "a[176:179],v[168:171],v[108:111],a[176:179]\n"
AR "v152,a240\n"
AW "a240,v112\n"
AR "v153,a241\n"
AW "a241,v113\n"
AR "v154,a242\n"
AW "a242,v114\n"
AR "v155,a243\n"
AW "a243,v115\n"
AR "v80,a172\n"
AR "v81,a173\n"
AR "v82,a174\n"
MF "a[196:199],v[172:175],v[108:111],a[196:199]\n"
AR "v83,a175\n"
AR "v84,a192\n"
AR "v85,a193\n"
AR "v86,a194\n"
AR "v87,a195\n"
"v_pk_fma_f32 v[80:81],v[80:81],v[60:61],v[54:55] op_sel:[0,0,1] op_sel_hi:[1,0,1]\n"
"v_pk_fma_f32 v[82:83],v[82:83],v[60:61],v[54:55] op_sel:[0,0,1] op_sel_hi:[1,0,1]\n"
"v_pk_fma_f32 v[84:85],v[84:85],v[60:61],v[54:55] op_sel:[0,0,1] op_sel_hi:[1,0,1]\n"
"v_pk_fma_f32 v[86:87],v[86:87],v[60:61],v[54:55] op_sel:[0,0,1] op_sel_hi:[1,0,1]\n"
CV "v116,v80,v81\n"
CV "v117,v82,v83\n"
CV "v118,v84,v85\n"
CV "v119,v86,v87\n"
MF "a[200:203],v[176:179],v[92:95],a[200:203]\n"
AR "v156,a244\n"
AW "a244,v116\n"
AR "v157,a245\n"
AW "a245,v117\n"
AR "v158,a246\n"
AW "a246,v118\n"
AR "v159,a247\n"
AW "a247,v119\n"
AR "v72,a176\n"
AR "v73,a177\n"
AR "v74,a178\n"
MF "a[220:223],v[180:183],v[92:95],a[220:223]\n"
AR "v75,a179\n"
AR "v76,a196\n"
AR "v77,a197\n"
AR "v78,a198\n"
AR "v79,a199\n"
"v_pk_fma_f32 v[72:73],v[72:73],v[60:61],v[56:57] op_sel:[0,1,0] op_sel_hi:[1,1,0]\n"
"v_pk_fma_f32 v[74:75],v[74:75],v[60:61],v[56:57] op_sel:[0,1,0] op_sel_hi:[1,1,0]\n"
"v_pk_fma_f32 v[76:77],v[76:77],v[60:61],v[56:57] op_sel:[0,1,0] op_sel_hi:[1,1,0]\n"
"v_pk_fma_f32 v[78:79],v[78:79],v[60:61],v[56:57] op_sel:[0,1,0] op_sel_hi:[1,1,0]\n"
CV "v112,v72,v73\n"
CV "v113,v74,v75\n"
CV "v114,v76,v77\n"
CV "v115,v78,v79\n"
MF "a[204:207],v[176:179],v[96:99],a[204:207]\n"
AR "v160,a248\n"
AW "a248,v112\n"
AR "v161,a249\n"
AW "a249,v113\n"
AR "v162,a250\n"
AW "a250,v114\n"
AR "v163,a251\n"
AW "a251,v115\n"
AR "v80,a200\n"
AR "v81,a201\n"
AR "v82,a202\n"
MF "a[224:227],v[180:183],v[96:99],a[224:227]\n"
AR "v83,a203\n"
AR "v84,a220\n"
AR "v85,a221\n"
AR "v86,a222\n"
AR "v87,a223\n"
"v_pk_fma_f32 v[80:81],v[80:81],v[56:57],v[52:53] op_sel:[0,1,0] op_sel_hi:[1,1,0]\n"
"v_pk_fma_f32 v[82:83],v[82:83],v[56:57],v[52:53] op_sel:[0,1,0] op_sel_hi:[1,1,0]\n"
"v_pk_fma_f32 v[84:85],v[84:85],v[56:57],v[52:53] op_sel:[0,1,0] op_sel_hi:[1,1,0]\n"
"v_pk_fma_f32 v[86:87],v[86:87],v[56:57],v[52:53] op_sel:[0,1,0] op_sel_hi:[1,1,0]\n"
CV "v116,v80,v81\n"
CV "v117,v82,v83\n"
CV "v118,v84,v85\n"
CV "v119,v86,v87\n"
MF "a[208:211],v[176:179],v[100:103],a[208:211]\n"
"v_mov_b32 v164,v62\n"
"v_mov_b32 v62,v116\n"
"v_mov_b32 v165,v63\n"
"v_mov_b32 v63,v117\n"
"v_mov_b32 v166,v64\n"
"v_mov_b32 v64,v118\n"
"v_mov_b32 v167,v65\n"
"v_mov_b32 v65,v119\n"
AR "v72,a204\n"
AR "v73,a205\n"
AR "v74,a206\n"
MF "a[228:231],v[180:183],v[100:103],a[228:231]\n"
AR "v75,a207\n"
AR "v76,a224\n"
AR "v77,a225\n"
AR "v78,a226\n"
AR "v79,a227\n"
"v_pk_fma_f32 v[72:73],v[72:73],v[58:59],v[52:53] op_sel:[0,0,1] op_sel_hi:[1,0,1]\n"
"v_pk_fma_f32 v[74:75],v[74:75],v[58:59],v[52:53] op_sel:[0,0,1] op_sel_hi:[1,0,1]\n"
"v_pk_fma_f32 v[76:77],v[76:77],v[58:59],v[52:53] op_sel:[0,0,1] op_sel_hi:[1,0,1]\n"
"v_pk_fma_f32 v[78:79],v[78:79],v[58:59],v[52:53] op_sel:[0,0,1] op_sel_hi:[1,0,1]\n"
CV "v112,v72,v73\n"
CV "v113,v74,v75\n"
CV "v114,v76,v77\n"
CV "v115,v78,v79\n"
MF "a[212:215],v[176:179],v[104:107],a[212:215]\n"
"v_mov_b32 v168,v66\n"
"v_mov_b32 v66,v112\n"
"v_mov_b32 v169,v67\n"
"v_mov_b32 v67,v113\n"
"v_mov_b32 v170,v68\n"
"v_mov_b32 v68,v114\n"
"v_mov_b32 v171,v69\n"
"v_mov_b32 v69,v115\n"
AR "v80,a208\n"
AR "v81,a209\n"
AR "v82,a210\n"
MF "a[232:235],v[180:183],v[104:107],a[232:235]\n"
AR "v83,a211\n"
AR "v84,a228\n"
AR "v85,a229\n"
AR "v86,a230\n"
AR "v87,a231\n"
"v_pk_fma_f32 v[80:81],v[80:81],v[58:59],v[54:55] op_sel:[0,1,0] op_sel_hi:[1,1,0]\n"
"v_pk_fma_f32 v[82:83],v[82:83],v[58:59],v[54:55] op_sel:[0,1,0] op_sel_hi:[1,1,0]\n"
"v_pk_fma_f32 v[84:85],v[84:85],v[58:59],v[54:55] op_sel:[0,1,0] op_sel_hi:[1,1,0]\n"
"v_pk_fma_f32 v[86:87],v[86:87],v[58:59],v[54:55] op_sel:[0,1,0] op_sel_hi:[1,1,0]\n"
CV "v116,v80,v81\n"
CV "v117,v82,v83\n"
CV "v118,v84,v85\n"
CV "v119,v86,v87\n"
MF "a[216:219],v[176:179],v[108:111],a[216:219]\n"
"v_mov_b32 v172,v70\n"
"v_mov_b32 v70,v116\n"
"v_mov_b32 v173,v71\n"
"v_mov_b32 v71,v117\n"
"v_mov_b32 v174,v132\n"
"v_mov_b32 v132,v118\n"
"v_mov_b32 v175,v133\n"
"v_mov_b32 v133,v119\n"
AR "v72,a212\n"
AR "v73,a213\n"
AR "v74,a214\n"
MF "a[236:239],v[180:183],v[108:111],a[236:239]\n"
AR "v75,a215\n"
AR "v76,a232\n"
AR "v77,a233\n"
AR "v78,a234\n"
AR "v79,a235\n"
"v_pk_fma_f32 v[72:73],v[72:73],v[60:61],v[54:55] op_sel:[0,0,1] op_sel_hi:[1,0,1]\n"
"v_pk_fma_f32 v[74:75],v[74:75],v[60:61],v[54:55] op_sel:[0,0,1] op_sel_hi:[1,0,1]\n"
"v_pk_fma_f32 v[76:77],v[76:77],v[60:61],v[54:55] op_sel:[0,0,1] op_sel_hi:[1,0,1]\n"
"v_pk_fma_f32 v[78:79],v[78:79],v[60:61],v[54:55] op_sel:[0,0,1] op_sel_hi:[1,0,1]\n"
CV "v112,v72,v73\n"
CV "v113,v74,v75\n"
CV "v114,v76,v77\n"
CV "v115,v78,v79\n"
"s_waitcnt vmcnt(0) lgkmcnt(0)\n"
"s_barrier\n"
"ds_read_b128 v[92:95],v0\n"
"ds_read_b128 v[96:99],v1\n"
"ds_read_b128 v[100:103],v0 offset:1024\n"
"v_mov_b32 v176,v134\n"
"v_mov_b32 v134,v112\n"
"v_mov_b32 v177,v135\n"
"v_mov_b32 v135,v113\n"
"v_mov_b32 v178,v6\n"
"v_mov_b32 v6,v114\n"
"v_mov_b32 v179,v13\n"
"v_mov_b32 v13,v115\n"
AR "v80,a216\n"
AR "v81,a217\n"
AR "v82,a218\n"
AR "v83,a219\n"
AR "v84,a236\n"
AR "v85,a237\n"
AR "v86,a238\n"
AR "v87,a239\n"
"v_pk_fma_f32 v[80:81],v[80:81],v[60:61],v[56:57] op_sel:[0,1,0] op_sel_hi:[1,1,0]\n"
"v_pk_fma_f32 v[82:83],v[82:83],v[60:61],v[56:57] op_sel:[0,1,0] op_sel_hi:[1,1,0]\n"
"v_pk_fma_f32 v[84:85],v[84:85],v[60:61],v[56:57] op_sel:[0,1,0] op_sel_hi:[1,1,0]\n"
"v_pk_fma_f32 v[86:87],v[86:87],v[60:61],v[56:57] op_sel:[0,1,0] op_sel_hi:[1,1,0]\n"
CV "v116,v80,v81\n"
CV "v117,v82,v83\n"
CV "v118,v84,v85\n"
CV "v119,v86,v87\n"
"v_mov_b32 v180,v14\n"
"v_mov_b32 v14,v116\n"
"v_mov_b32 v181,v15\n"
"v_mov_b32 v15,v117\n"
AR "v182,a254\n"
AW "a254,v118\n"
AR "v183,a255\n"
AW "a255,v119\n"
"s_mov_b32 s13,s11\n"
"v_subrev_u32 v2,49152,v2\n"
"v_subrev_u32 v3,49152,v3\n"
"v_subrev_u32 v4,49152,v4\n"
"v_subrev_u32 v5,49152,v5\n"
"s_mov_b32 s57,2\n"
"s_mov_b32 s69,1\n"
"s_mov_b32 s61,3\n"
"s_mov_b32 s97,0\n"
"s_mov_b32 s95,0x47800000\n"
"s_mov_b32 s77,0x47800000\n"
"s_branch .Latt2\n"
".LpgattB_f:\n"
"s_cbranch_execz .Lisr3\n"
".LattB:\n"
AR "v2,a245\n"
"v_xor_b32 v3,32,v2\n"
"v_xor_b32 v4,64,v2\n"
"v_xor_b32 v5,96,v2\n"
"s_mov_b32 s61,3\n"
".Latt2:\n"
"s_cmp_eq_u32 s57,1\n"
"s_cbranch_scc1 .Lt0k\n"
".Lt0n:\n"
"s_waitcnt lgkmcnt(2)\n"
MF "v[72:75],v[92:95],v[136:139],0\n"
"ds_read_b128 v[104:107],v1 offset:1024\n"
MF "v[76:79],v[92:95],v[140:143],0\n"
MF "v[80:83],v[92:95],v[144:147],0\n"
MF "v[84:87],v[92:95],v[148:151],0\n"
MF "v[88:91],v[92:95],v[152:155],0\n"
"s_waitcnt lgkmcnt(2)\n"
"ds_read_b128 v[92:95],v0 offset:2048\n"
MF "v[72:75],v[96:99],v[156:159],v[72:75]\n"
MF "v[76:79],v[96:99],v[160:163],v[76:79]\n"
MF "v[80:83],v[96:99],v[164:167],v[80:83]\n"
MF "v[84:87],v[96:99],v[168:171],v[84:87]\n"
MF "v[88:91],v[96:99],v[172:175],v[88:91]\n"
"s_waitcnt lgkmcnt(2)\n"
"ds_read_b128 v[96:99],v1 offset:2048\n"
MF "v[72:75],v[100:103],v[176:179],v[72:75]\n"
MF "v[76:79],v[100:103],v[180:183],v[76:79]\n"
MF "v[80:83],v[100:103],v[184:187],v[80:83]\n"
MF "v[84:87],v[100:103],v[188:191],v[84:87]\n"
MF "v[88:91],v[100:103],v[192:195],v[88:91]\n"
"s_waitcnt lgkmcnt(2)\n"
"ds_read_b128 v[100:103],v0 offset:6144\n"
MF "v[72:75],v[104:107],v[196:199],v[72:75]\n"
MF "v[76:79],v[104:107],v[200:203],v[76:79]\n"
MF "v[80:83],v[104:107],v[204:207],v[80:83]\n"
MF "v[84:87],v[104:107],v[208:211],v[84:87]\n"
MF "v[88:91],v[104:107],v[212:215],v[88:91]\n"
"s_waitcnt lgkmcnt(2)\n"
"ds_read_b128 v[104:107],v1 offset:6144\n"
MF "v[72:75],v[92:95],v[216:219],v[72:75]\n"
MF "v[76:79],v[92:95],v[220:223],v[76:79]\n"
MF "v[80:83],v[92:95],v[224:227],v[80:83]\n"
MF "v[84:87],v[92:95],v[228:231],v[84:87]\n"
MF "v[88:91],v[92:95],v[232:235],v[88:91]\n"
"s_waitcnt lgkmcnt(2)\n"
"ds_read_b128 v[92:95],v0 offset:7168\n"
MF "v[72:75],v[96:99],v[236:239],v[72:75]\n"
MF "v[76:79],v[96:99],v[240:243],v[76:79]\n"
MF "v[80:83],v[96:99],v[244:247],v[80:83]\n"
MF "v[84:87],v[96:99],v[248:251],v[84:87]\n"
MF "v[88:91],v[96:99],v[252:255],v[88:91]\n"
"s_waitcnt lgkmcnt(2)\n"
"ds_read_b128 v[96:99],v1 offset:7168\n"
MF "v[112:115],v[100:103],v[136:139],0\n"
MF "v[116:119],v[100:103],v[140:143],0\n"
MF "v[120:123],v[100:103],v[144:147],0\n"
MF "v[124:127],v[100:103],v[148:151],0\n"
MF "v[128:131],v[100:103],v[152:155],0\n"
"s_waitcnt lgkmcnt(2)\n"
"ds_read_b128 v[100:103],v0 offset:8192\n"
MF "v[112:115],v[104:107],v[156:159],v[112:115]\n"
"v_max3_f32 v52,v72,v73,v74\n"
"v_max_f32 v52,v52,v75\n"
MF "v[116:119],v[104:107],v[160:163],v[116:119]\n"
"v_max3_f32 v53,v76,v77,v78\n"
"v_max_f32 v53,v53,v79\n"
MF "v[120:123],v[104:107],v[164:167],v[120:123]\n"
"v_max3_f32 v54,v80,v81,v82\n"
"v_max_f32 v54,v54,v83\n"
MF "v[124:127],v[104:107],v[168:171],v[124:127]\n"
"v_max3_f32 v55,v84,v85,v86\n"
"v_max_f32 v55,v55,v87\n"
MF "v[128:131],v[104:107],v[172:175],v[128:131]\n"
"s_waitcnt lgkmcnt(2)\n"
"ds_read_b128 v[104:107],v1 offset:8192\n"
"v_max3_f32 v56,v88,v89,v90\n"
MF "v[112:115],v[92:95],v[176:179],v[112:115]\n"
"v_max_f32 v56,v56,v91\n"
MF "v[116:119],v[92:95],v[180:183],v[116:119]\n"
MF "v[120:123],v[92:95],v[184:187],v[120:123]\n"
MF "v[124:127],v[92:95],v[188:191],v[124:127]\n"
MF "v[128:131],v[92:95],v[192:195],v[128:131]\n"
"s_waitcnt lgkmcnt(2)\n"
MF "v[112:115],v[96:99],v[196:199],v[112:115]\n"
"s_waitcnt lgkmcnt(0)\n"
MF "v[116:119],v[96:99],v[200:203],v[116:119]\n"
"s_barrier\n"
MF "v[120:123],v[96:99],v[204:207],v[120:123]\n"
"s_add_u32 s72,s72,6144\n"
MF "v[124:127],v[96:99],v[208:211],v[124:127]\n"
"s_cmp_eq_u32 s72,s17\n"
"s_cselect_b32 s72,s16,s72\n"
"s_add_u32 m0,s64,0\n"
MF "v[128:131],v[96:99],v[212:215],v[128:131]\n"
LD "v7,s[32:35],s72 offen nt lds\n"
MF "v[112:115],v[100:103],v[216:219],v[112:115]\n"
"s_add_u32 m0,s64,896\n"
MF "v[116:119],v[100:103],v[220:223],v[116:119]\n"
LD "v7,s[32:35],s72 offen offset:128 nt lds\n"
MF "v[120:123],v[100:103],v[224:227],v[120:123]\n"
"s_add_u32 m0,s64,1792\n"
MF "v[124:127],v[100:103],v[228:231],v[124:127]\n"
LD "v7,s[32:35],s72 offen offset:256 nt lds\n"
MF "v[128:131],v[100:103],v[232:235],v[128:131]\n"
"s_add_u32 s72,s72,6144\n"
"s_add_u32 m0,s64,6144\n"
MF "v[112:115],v[104:107],v[236:239],v[112:115]\n"
LD "v7,s[32:35],s72 offen nt lds\n"
MF "v[116:119],v[104:107],v[240:243],v[116:119]\n"
"s_add_u32 m0,s64,7040\n"
MF "v[120:123],v[104:107],v[244:247],v[120:123]\n"
LD "v7,s[32:35],s72 offen offset:128 nt lds\n"
MF "v[124:127],v[104:107],v[248:251],v[124:127]\n"
"s_add_u32 m0,s64,7936\n"
MF "v[128:131],v[104:107],v[252:255],v[128:131]\n"
LD "v7,s[32:35],s72 offen offset:256 nt lds\n"
"s_branch .Lt0e\n"
".Lpgt0k_f:\n"
"s_cbranch_execz .Lisr3\n"
".Lt0k:\n"
"s_lshl_b32 s58,s56,10\n"
"s_add_u32 s58,s58,73728\n"
"v_add_u32 v32,s58,v9\n"
"v_add_u32 v33,65536,v32\n"
"s_waitcnt lgkmcnt(2)\n"
MF "v[72:75],v[92:95],v[136:139],0\n"
"ds_read_b128 v[104:107],v1 offset:1024\n"
AR "v62,a20\n"
MF "v[76:79],v[92:95],v[140:143],0\n"
"ds_write_b128 v32,a[0:3] offset:24576\n"
MF "v[80:83],v[92:95],v[144:147],0\n"
"ds_write_b128 v32,a[4:7] offset:28672\n"
MF "v[84:87],v[92:95],v[148:151],0\n"
AR "v63,a21\n"
AR "v64,a22\n"
MF "v[88:91],v[92:95],v[152:155],0\n"
"s_waitcnt lgkmcnt(4)\n"
"ds_read_b128 v[92:95],v0 offset:2048\n"
AR "v65,a23\n"
MF "v[72:75],v[96:99],v[156:159],v[72:75]\n"
"ds_write_b128 v32,a[8:11] offset:32768\n"
MF "v[76:79],v[96:99],v[160:163],v[76:79]\n"
AR "v66,a24\n"
AR "v67,a25\n"
MF "v[80:83],v[96:99],v[164:167],v[80:83]\n"
".Lpg16f:\n"
"s_cbranch_execz .Lisr3\n"
"ds_write_b128 v32,a[12:15] offset:36864\n"
MF "v[84:87],v[96:99],v[168:171],v[84:87]\n"
AR "v68,a26\n"
AR "v69,a27\n"
MF "v[88:91],v[96:99],v[172:175],v[88:91]\n"
"s_waitcnt lgkmcnt(6)\n"
"ds_read_b128 v[96:99],v1 offset:2048\n"
AR "v70,a28\n"
MF "v[72:75],v[100:103],v[176:179],v[72:75]\n"
"ds_write_b128 v32,a[16:19] offset:40960\n"
MF "v[76:79],v[100:103],v[180:183],v[76:79]\n"
AR "v71,a29\n"
AR "v132,a30\n"
MF "v[80:83],v[100:103],v[184:187],v[80:83]\n"
AR "v133,a31\n"
AR "v134,a32\n"
MF "v[84:87],v[100:103],v[188:191],v[84:87]\n"
"ds_write_b128 v32,a[40:43] offset:45056\n"
MF "v[88:91],v[100:103],v[192:195],v[88:91]\n"
"s_waitcnt lgkmcnt(8)\n"
"ds_read_b128 v[100:103],v0 offset:6144\n"
AR "v135,a33\n"
MF "v[72:75],v[104:107],v[196:199],v[72:75]\n"
"ds_write_b128 v32,a[44:47] offset:49152\n"
MF "v[76:79],v[104:107],v[200:203],v[76:79]\n"
AR "v6,a34\n"
AR "v13,a35\n"
MF "v[80:83],v[104:107],v[204:207],v[80:83]\n"
AR "v14,a36\n"
AR "v15,a37\n"
MF "v[84:87],v[104:107],v[208:211],v[84:87]\n"
"ds_write_b128 v32,a[48:51] offset:53248\n"
MF "v[88:91],v[104:107],v[212:215],v[88:91]\n"
"s_waitcnt lgkmcnt(8)\n"
"ds_read_b128 v[104:107],v1 offset:6144\n"
"v_accvgpr_mov_b32 a254,a38\n"
MF "v[72:75],v[92:95],v[216:219],v[72:75]\n"
"v_accvgpr_mov_b32 a255,a39\n"
"v_accvgpr_mov_b32 a240,a168\n"
MF "v[76:79],v[92:95],v[220:223],v[76:79]\n"
"ds_write_b128 v32,a[52:55] offset:57344\n"
MF "v[80:83],v[92:95],v[224:227],v[80:83]\n"
"v_accvgpr_mov_b32 a241,a169\n"
"v_accvgpr_mov_b32 a242,a170\n"
MF "v[84:87],v[92:95],v[228:231],v[84:87]\n"
"ds_write_b128 v32,a[56:59] offset:61440\n"
MF "v[88:91],v[92:95],v[232:235],v[88:91]\n"
"s_waitcnt lgkmcnt(8)\n"
"ds_read_b128 v[92:95],v0 offset:7168\n"
"v_accvgpr_mov_b32 a243,a171\n"
MF "v[72:75],v[96:99],v[236:239],v[72:75]\n"
"v_accvgpr_mov_b32 a244,a172\n"
"v_accvgpr_mov_b32 a245,a173\n"
MF "v[76:79],v[96:99],v[240:243],v[76:79]\n"
"ds_write_b128 v33,a[80:83]\n"
MF "v[80:83],v[96:99],v[244:247],v[80:83]\n"
"v_accvgpr_mov_b32 a246,a174\n"
"v_accvgpr_mov_b32 a247,a175\n"
MF "v[84:87],v[96:99],v[248:251],v[84:87]\n"
"v_accvgpr_mov_b32 a248,a176\n"
"v_accvgpr_mov_b32 a249,a177\n"
MF "v[88:91],v[96:99],v[252:255],v[88:91]\n"
"s_waitcnt lgkmcnt(7)\n"
"ds_read_b128 v[96:99],v1 offset:7168\n"
"v_accvgpr_mov_b32 a250,a178\n"
MF "v[112:115],v[100:103],v[136:139],0\n"
"ds_write_b128 v33,a[84:87] offset:4096\n"
MF "v[116:119],v[100:103],v[140:143],0\n"
"ds_write_b128 v33,a[88:91] offset:8192\n"
MF "v[120:123],v[100:103],v[144:147],0\n"
"v_accvgpr_mov_b32 a251,a179\n"
MF "v[124:127],v[100:103],v[148:151],0\n"
MF "v[128:131],v[100:103],v[152:155],0\n"
"s_waitcnt lgkmcnt(7)\n"
"ds_read_b128 v[100:103],v0 offset:8192\n"
MF "v[112:115],v[104:107],v[156:159],v[112:115]\n"
"v_max3_f32 v52,v72,v73,v74\n"
"v_max_f32 v52,v52,v75\n"
MF "v[116:119],v[104:107],v[160:163],v[116:119]\n"
"v_max3_f32 v53,v76,v77,v78\n"
"v_max_f32 v53,v53,v79\n"
MF "v[120:123],v[104:107],v[164:167],v[120:123]\n"
"v_max3_f32 v54,v80,v81,v82\n"
"v_max_f32 v54,v54,v83\n"
MF "v[124:127],v[104:107],v[168:171],v[124:127]\n"
"v_max3_f32 v55,v84,v85,v86\n"
"v_max_f32 v55,v55,v87\n"
MF "v[128:131],v[104:107],v[172:175],v[128:131]\n"
"s_waitcnt lgkmcnt(5)\n"
"ds_read_b128 v[104:107],v1 offset:8192\n"
"v_max3_f32 v56,v88,v89,v90\n"
MF "v[112:115],v[92:95],v[176:179],v[112:115]\n"
"v_max_f32 v56,v56,v91\n"
MF "v[116:119],v[92:95],v[180:183],v[116:119]\n"
"ds_write_b128 v33,a[92:95] offset:12288\n"
MF "v[120:123],v[92:95],v[184:187],v[120:123]\n"
"ds_write_b128 v33,a[96:99] offset:16384\n"
MF "v[124:127],v[92:95],v[188:191],v[124:127]\n"
"ds_write_b128 v33,a[120:123] offset:20480\n"
MF "v[128:131],v[92:95],v[192:195],v[128:131]\n"
"s_waitcnt lgkmcnt(7)\n"
"ds_write_b128 v32,a[124:127]\n"
MF "v[112:115],v[96:99],v[196:199],v[112:115]\n"
"s_waitcnt lgkmcnt(0)\n"
MF "v[116:119],v[96:99],v[200:203],v[116:119]\n"
"s_barrier\n"
MF "v[120:123],v[96:99],v[204:207],v[120:123]\n"
"s_add_u32 s72,s72,6144\n"
"ds_write_b128 v32,a[128:131] offset:4096\n"
MF "v[124:127],v[96:99],v[208:211],v[124:127]\n"
"s_cmp_eq_u32 s72,s17\n"
"s_cselect_b32 s72,s16,s72\n"
"s_add_u32 m0,s64,0\n"
MF "v[128:131],v[96:99],v[212:215],v[128:131]\n"
LD "v7,s[32:35],s72 offen nt lds\n"
MF "v[112:115],v[100:103],v[216:219],v[112:115]\n"
"s_add_u32 m0,s64,896\n"
"ds_write_b128 v32,a[132:135] offset:8192\n"
MF "v[116:119],v[100:103],v[220:223],v[116:119]\n"
LD "v7,s[32:35],s72 offen offset:128 nt lds\n"
MF "v[120:123],v[100:103],v[224:227],v[120:123]\n"
"s_add_u32 m0,s64,1792\n"
"ds_write_b128 v32,a[136:139] offset:12288\n"
MF "v[124:127],v[100:103],v[228:231],v[124:127]\n"
LD "v7,s[32:35],s72 offen offset:256 nt lds\n"
MF "v[128:131],v[100:103],v[232:235],v[128:131]\n"
"s_add_u32 s72,s72,6144\n"
"s_add_u32 m0,s64,6144\n"
MF "v[112:115],v[104:107],v[236:239],v[112:115]\n"
LD "v7,s[32:35],s72 offen nt lds\n"
MF "v[116:119],v[104:107],v[240:243],v[116:119]\n"
"s_add_u32 m0,s64,7040\n"
"ds_write_b128 v32,a[160:163] offset:16384\n"
MF "v[120:123],v[104:107],v[244:247],v[120:123]\n"
LD "v7,s[32:35],s72 offen offset:128 nt lds\n"
MF "v[124:127],v[104:107],v[248:251],v[124:127]\n"
"s_add_u32 m0,s64,7936\n"
"ds_write_b128 v32,a[164:167] offset:20480\n"
MF "v[128:131],v[104:107],v[252:255],v[128:131]\n"
LD "v7,s[32:35],s72 offen offset:256 nt lds\n"
".Lpgt0e_g:\n"
"s_cbranch_execz .Lisr3\n"
".Lt0e:\n"
"s_nop 1\n"
"v_max3_f32 v52,v52,v112,v113\n"
"v_max3_f32 v53,v53,v116,v117\n"
"v_max3_f32 v54,v54,v120,v121\n"
"v_max3_f32 v55,v55,v124,v125\n"
"s_nop 1\n"
"v_max3_f32 v56,v56,v128,v129\n"
"v_max3_f32 v52,v52,v114,v115\n"
"v_max3_f32 v53,v53,v118,v119\n"
"v_max3_f32 v54,v54,v122,v123\n"
"v_max3_f32 v55,v55,v126,v127\n"
"v_max3_f32 v56,v56,v130,v131\n"
"v_mov_b32 v57,v52\n"
"v_mov_b32 v58,v53\n"
"v_mov_b32 v59,v54\n"
"v_mov_b32 v60,v55\n"
"v_mov_b32 v61,v56\n"
"v_permlane16_swap_b32 v52,v57\n"
"v_permlane16_swap_b32 v53,v58\n"
"v_permlane16_swap_b32 v54,v59\n"
"v_permlane16_swap_b32 v55,v60\n"
"v_permlane16_swap_b32 v56,v61\n"
"v_max_f32 v52,v52,v57\n"
"v_max_f32 v53,v53,v58\n"
"v_max_f32 v54,v54,v59\n"
"v_max_f32 v55,v55,v60\n"
"v_max_f32 v56,v56,v61\n"
"v_mov_b32 v57,v52\n"
"v_mov_b32 v58,v53\n"
"v_mov_b32 v59,v54\n"
"v_mov_b32 v60,v55\n"
"v_mov_b32 v61,v56\n"
"v_permlane32_swap_b32 v52,v57\n"
"v_permlane32_swap_b32 v53,v58\n"
"v_permlane32_swap_b32 v54,v59\n"
"v_permlane32_swap_b32 v55,v60\n"
"v_permlane32_swap_b32 v56,v61\n"
"v_max_f32 v52,v52,v57\n"
"v_max_f32 v53,v53,v58\n"
"v_max_f32 v54,v54,v59\n"
"v_max_f32 v55,v55,v60\n"
"v_max_f32 v56,v56,v61\n"
"v_sub_f32 v32,0xc1300000,v52\n"
"v_sub_f32 v36,0xc1300000,v53\n"
"v_sub_f32 v40,0xc1300000,v54\n"
"v_sub_f32 v44,0xc1300000,v55\n"
"v_sub_f32 v48,0xc1300000,v56\n"
"v_mov_b32 v33,v32\n"
"v_pk_mov_b32 v[34:35],v[32:33],v[32:33]\n"
"v_mov_b32 v37,v36\n"
"v_pk_mov_b32 v[38:39],v[36:37],v[36:37]\n"
"v_mov_b32 v41,v40\n"
"v_pk_mov_b32 v[42:43],v[40:41],v[40:41]\n"
"v_mov_b32 v45,v44\n"
"v_pk_mov_b32 v[46:47],v[44:45],v[44:45]\n"
"v_mov_b32_e64 v49,v48\n"
"v_pk_mov_b32 v[50:51],v[48:49],v[48:49]\n"
"v_pk_add_f32 v[72:73],v[72:73],v[32:33]\n"
"v_pk_add_f32 v[74:75],v[74:75],v[32:33]\n"
"v_pk_add_f32 v[112:113],v[112:113],v[32:33]\n"
"v_pk_add_f32 v[114:115],v[114:115],v[32:33]\n"
"v_exp_f32 v72,v72\n"
"v_exp_f32 v73,v73\n"
"v_exp_f32 v74,v74\n"
"v_exp_f32 v75,v75\n"
"v_exp_f32 v112,v112\n"
"v_exp_f32 v113,v113\n"
"v_exp_f32 v114,v114\n"
"v_exp_f32 v115,v115\n"
"v_pk_add_f32 v[10:11],v[72:73],v[74:75]\n"
"v_pk_add_f32 v[28:29],v[112:113],v[114:115]\n"
"v_pk_add_f32 v[10:11],v[10:11],v[28:29]\n"
"v_add_f32_e64 v52,v10,v11\n"
CV "v72,v72,v73\n"
CV "v73,v74,v75\n"
CV "v74,v112,v113\n"
CV "v75,v114,v115\n"
"v_pk_add_f32 v[76:77],v[76:77],v[36:37]\n"
"v_pk_add_f32 v[78:79],v[78:79],v[36:37]\n"
"v_pk_add_f32 v[116:117],v[116:117],v[36:37]\n"
"v_pk_add_f32 v[118:119],v[118:119],v[36:37]\n"
"v_exp_f32 v76,v76\n"
"v_exp_f32 v77,v77\n"
"v_exp_f32 v78,v78\n"
"v_exp_f32 v79,v79\n"
"v_exp_f32 v116,v116\n"
"v_exp_f32 v117,v117\n"
"v_exp_f32 v118,v118\n"
"v_exp_f32 v119,v119\n"
"v_pk_add_f32 v[10:11],v[76:77],v[78:79]\n"
"v_pk_add_f32 v[28:29],v[116:117],v[118:119]\n"
"v_pk_add_f32 v[10:11],v[10:11],v[28:29]\n"
"v_add_f32_e64 v53,v10,v11\n"
CV "v76,v76,v77\n"
CV "v77,v78,v79\n"
CV "v78,v116,v117\n"
CV "v79,v118,v119\n"
"v_pk_add_f32 v[80:81],v[80:81],v[40:41]\n"
"v_pk_add_f32 v[82:83],v[82:83],v[40:41]\n"
"v_pk_add_f32 v[120:121],v[120:121],v[40:41]\n"
"v_pk_add_f32 v[122:123],v[122:123],v[40:41]\n"
"v_exp_f32 v80,v80\n"
"v_exp_f32 v81,v81\n"
"v_exp_f32 v82,v82\n"
"v_exp_f32 v83,v83\n"
"v_exp_f32 v120,v120\n"
"v_exp_f32 v121,v121\n"
"v_exp_f32 v122,v122\n"
"v_exp_f32 v123,v123\n"
"v_pk_add_f32 v[10:11],v[80:81],v[82:83]\n"
"v_pk_add_f32 v[28:29],v[120:121],v[122:123]\n"
"v_pk_add_f32 v[10:11],v[10:11],v[28:29]\n"
"v_add_f32_e64 v54,v10,v11\n"
CV "v80,v80,v81\n"
CV "v81,v82,v83\n"
CV "v82,v120,v121\n"
CV "v83,v122,v123\n"
"v_pk_add_f32 v[84:85],v[84:85],v[44:45]\n"
"v_pk_add_f32 v[86:87],v[86:87],v[44:45]\n"
"v_pk_add_f32 v[124:125],v[124:125],v[44:45]\n"
"v_pk_add_f32 v[126:127],v[126:127],v[44:45]\n"
"v_mov_b32_e64 v55,0\n"
"v_pk_add_f32 v[88:89],v[88:89],v[48:49]\n"
"v_pk_add_f32 v[90:91],v[90:91],v[48:49]\n"
"v_pk_add_f32 v[128:129],v[128:129],v[48:49]\n"
"v_pk_add_f32 v[130:131],v[130:131],v[48:49]\n"
"v_mov_b32 v56,0\n"
"s_waitcnt vmcnt(12) lgkmcnt(0)\n"
"s_barrier\n"
"ds_read_b128 v[16:19],v0 offset:24576\n"
"ds_read_b128 v[20:23],v1 offset:24576\n"
AW "a220,s13\n"
AW "a221,s13\n"
AW "a222,s13\n"
AW "a223,s13\n"
"s_waitcnt lgkmcnt(1)\n"
MF "v[92:95],v[16:19],v[136:139],v[32:35]\n"
"s_nop 0\n"
"ds_read_b128 v[24:27],v0 offset:25600\n"
"ds_read_b128 v[28:31],v1 offset:25600\n"
"s_waitcnt lgkmcnt(2)\n"
MF "v[92:95],v[20:23],v[156:159],v[92:95]\n"
"v_exp_f32 v84,v84\n"
MF "v[96:99],v[20:23],v[160:163],v[36:39]\n"
"v_exp_f32 v85,v85\n"
MF "v[96:99],v[16:19],v[140:143],v[96:99]\n"
"v_exp_f32 v86,v86\n"
MF "v[100:103],v[16:19],v[144:147],v[40:43]\n"
"v_exp_f32 v87,v87\n"
MF "v[100:103],v[20:23],v[164:167],v[100:103]\n"
"v_add_f32 v55,v55,v84\n"
"v_add_f32 v55,v55,v85\n"
MF "v[104:107],v[20:23],v[168:171],v[44:47]\n"
CV "v84,v84,v85\n"
"v_add_f32 v55,v55,v86\n"
MF "v[104:107],v[16:19],v[148:151],v[104:107]\n"
"v_add_f32 v55,v55,v87\n"
CV "v85,v86,v87\n"
MF "v[108:111],v[16:19],v[152:155],v[48:51]\n"
"v_exp_f32 v124,v124\n"
MF "v[108:111],v[20:23],v[172:175],v[108:111]\n"
"s_waitcnt lgkmcnt(0)\n"
"ds_read_b128 v[16:19],v0 offset:26624\n"
"ds_read_b128 v[20:23],v1 offset:26624\n"
MF "v[108:111],v[24:27],v[192:195],v[108:111]\n"
"v_exp_f32 v125,v125\n"
MF "v[108:111],v[28:31],v[212:215],v[108:111]\n"
"v_exp_f32 v126,v126\n"
MF "v[104:107],v[28:31],v[208:211],v[104:107]\n"
"v_exp_f32 v127,v127\n"
MF "v[104:107],v[24:27],v[188:191],v[104:107]\n"
"v_add_f32 v55,v55,v124\n"
"v_add_f32 v55,v55,v125\n"
MF "v[100:103],v[24:27],v[184:187],v[100:103]\n"
CV "v86,v124,v125\n"
"v_add_f32 v55,v55,v126\n"
MF "v[100:103],v[28:31],v[204:207],v[100:103]\n"
"v_add_f32 v55,v55,v127\n"
CV "v87,v126,v127\n"
MF "v[96:99],v[28:31],v[200:203],v[96:99]\n"
"v_exp_f32 v88,v88\n"
MF "v[96:99],v[24:27],v[180:183],v[96:99]\n"
"v_exp_f32 v89,v89\n"
MF "v[92:95],v[24:27],v[176:179],v[92:95]\n"
"v_exp_f32 v90,v90\n"
MF "v[92:95],v[28:31],v[196:199],v[92:95]\n"
"s_waitcnt lgkmcnt(0)\n"
"ds_read_b128 v[24:27],v0 offset:30720\n"
"ds_read_b128 v[28:31],v1 offset:30720\n"
MF "v[92:95],v[16:19],v[216:219],v[92:95]\n"
"v_exp_f32 v91,v91\n"
MF "v[92:95],v[20:23],v[236:239],v[92:95]\n"
"v_add_f32 v56,v56,v88\n"
"v_add_f32 v56,v56,v89\n"
MF "v[96:99],v[20:23],v[240:243],v[96:99]\n"
CV "v88,v88,v89\n"
"v_add_f32 v56,v56,v90\n"
MF "v[96:99],v[16:19],v[220:223],v[96:99]\n"
"v_add_f32 v56,v56,v91\n"
CV "v89,v90,v91\n"
MF "v[100:103],v[16:19],v[224:227],v[100:103]\n"
"v_exp_f32 v128,v128\n"
MF "v[100:103],v[20:23],v[244:247],v[100:103]\n"
"v_exp_f32 v129,v129\n"
MF "v[104:107],v[20:23],v[248:251],v[104:107]\n"
"v_exp_f32 v130,v130\n"
MF "v[104:107],v[16:19],v[228:231],v[104:107]\n"
"v_exp_f32 v131,v131\n"
MF "v[108:111],v[16:19],v[232:235],v[108:111]\n"
"v_add_f32 v56,v56,v128\n"
"v_add_f32 v56,v56,v129\n"
MF "v[108:111],v[20:23],v[252:255],v[108:111]\n"
"s_waitcnt lgkmcnt(0)\n"
"ds_read_b128 v[16:19],v0 offset:31744\n"
"ds_read_b128 v[20:23],v1 offset:31744\n"
MF "v[112:115],v[24:27],v[136:139],v[32:35]\n"
CV "v90,v128,v129\n"
"v_add_f32 v56,v56,v130\n"
MF "v[112:115],v[28:31],v[156:159],v[112:115]\n"
"v_add_f32 v56,v56,v131\n"
CV "v91,v130,v131\n"
MF "v[116:119],v[28:31],v[160:163],v[36:39]\n"
"v_exp_f32 v92,v92\n"
MF "v[116:119],v[24:27],v[140:143],v[116:119]\n"
"v_exp_f32 v93,v93\n"
MF "v[120:123],v[24:27],v[144:147],v[40:43]\n"
"v_exp_f32 v94,v94\n"
MF "v[120:123],v[28:31],v[164:167],v[120:123]\n"
"v_exp_f32 v95,v95\n"
MF "v[124:127],v[28:31],v[168:171],v[44:47]\n"
"v_add_f32 v57,v52,v92\n"
"v_add_f32 v57,v57,v93\n"
MF "v[124:127],v[24:27],v[148:151],v[124:127]\n"
CV "v92,v92,v93\n"
"v_add_f32 v57,v57,v94\n"
MF "v[128:131],v[24:27],v[152:155],v[48:51]\n"
"v_add_f32 v57,v57,v95\n"
CV "v93,v94,v95\n"
MF "v[128:131],v[28:31],v[172:175],v[128:131]\n"
"s_waitcnt lgkmcnt(0)\n"
"ds_read_b128 v[24:27],v0 offset:32768\n"
"ds_read_b128 v[28:31],v1 offset:32768\n"
MF "v[128:131],v[16:19],v[192:195],v[128:131]\n"
"v_exp_f32 v96,v96\n"
MF "v[128:131],v[20:23],v[212:215],v[128:131]\n"
"v_exp_f32 v97,v97\n"
MF "v[124:127],v[20:23],v[208:211],v[124:127]\n"
"v_exp_f32 v98,v98\n"
MF "v[124:127],v[16:19],v[188:191],v[124:127]\n"
"v_exp_f32 v99,v99\n"
MF "v[120:123],v[16:19],v[184:187],v[120:123]\n"
"v_add_f32 v58,v53,v96\n"
"v_add_f32 v58,v58,v97\n"
MF "v[120:123],v[20:23],v[204:207],v[120:123]\n"
CV "v96,v96,v97\n"
"v_add_f32 v58,v58,v98\n"
MF "v[116:119],v[20:23],v[200:203],v[116:119]\n"
"s_waitcnt vmcnt(6) lgkmcnt(0)\n"
"v_add_f32 v58,v58,v99\n"
CV "v97,v98,v99\n"
MF "v[116:119],v[16:19],v[180:183],v[116:119]\n"
"s_barrier\n"
MF "v[112:115],v[16:19],v[176:179],v[112:115]\n"
"v_exp_f32 v100,v100\n"
MF "v[112:115],v[20:23],v[196:199],v[112:115]\n"
"s_nop 0\n"
TR "v[16:17],v2 offset:49152\n"
TR "v[18:19],v2 offset:55296\n"
MF "v[112:115],v[24:27],v[216:219],v[112:115]\n"
"s_add_u32 s73,s73,12288\n"
TR "v[20:21],v3 offset:49152\n"
TR "v[22:23],v3 offset:55296\n"
MF "v[112:115],v[28:31],v[236:239],v[112:115]\n"
"s_cmp_eq_u32 s73,s19\n"
"s_cselect_b32 s73,s18,s73\n"
"s_add_u32 m0,s65,24576\n"
MF "v[116:119],v[28:31],v[240:243],v[116:119]\n"
LD "v8,s[36:39],s73 offen nt lds\n"
MF "v[116:119],v[24:27],v[220:223],v[116:119]\n"
"s_add_u32 m0,s65,25472\n"
"v_exp_f32 v101,v101\n"
MF "v[120:123],v[24:27],v[224:227],v[120:123]\n"
LD "v8,s[36:39],s73 offen offset:128 nt lds\n"
MF "v[120:123],v[28:31],v[244:247],v[120:123]\n"
"v_exp_f32 v102,v102\n"
MF "v[124:127],v[28:31],v[248:251],v[124:127]\n"
"s_add_u32 m0,s65,26368\n"
"v_exp_f32 v103,v103\n"
MF "v[124:127],v[24:27],v[228:231],v[124:127]\n"
LD "v8,s[36:39],s73 offen offset:256 nt lds\n"
MF "v[128:131],v[24:27],v[232:235],v[128:131]\n"
"v_add_f32 v59,v54,v100\n"
"v_add_f32 v59,v59,v101\n"
MF "v[128:131],v[28:31],v[252:255],v[128:131]\n"
"s_waitcnt lgkmcnt(0)\n"
TR "v[24:25],v4 offset:49152\n"
TR "v[26:27],v4 offset:55296\n"
MF "a[0:3],v[16:19],v[72:75],a[220:223]\n"
"s_add_u32 m0,s65,24576\n"
TR "v[28:29],v5 offset:49152\n"
TR "v[30:31],v5 offset:55296\n"
MF "a[20:23],v[20:23],v[72:75],a[220:223]\n"
LD "v8,s[36:39],s73 offen offset:3072 nt lds\n"
MF "a[24:27],v[20:23],v[76:79],a[220:223]\n"
CV "v100,v100,v101\n"
"v_add_f32 v59,v59,v102\n"
MF "a[4:7],v[16:19],v[76:79],a[220:223]\n"
"s_add_u32 m0,s65,25472\n"
"v_add_f32 v59,v59,v103\n"
CV "v101,v102,v103\n"
MF "a[8:11],v[16:19],v[80:83],a[220:223]\n"
LD "v8,s[36:39],s73 offen offset:3200 nt lds\n"
MF "a[28:31],v[20:23],v[80:83],a[220:223]\n"
"v_exp_f32 v104,v104\n"
MF "a[32:35],v[20:23],v[84:87],a[220:223]\n"
"s_add_u32 m0,s65,26368\n"
"v_exp_f32 v105,v105\n"
MF "a[12:15],v[16:19],v[84:87],a[220:223]\n"
LD "v8,s[36:39],s73 offen offset:3328 nt lds\n"
MF "a[16:19],v[16:19],v[88:91],a[220:223]\n"
"v_exp_f32 v106,v106\n"
MF "a[36:39],v[20:23],v[88:91],a[220:223]\n"
"s_waitcnt lgkmcnt(0)\n"
TR "v[16:17],v2 offset:50176\n"
TR "v[18:19],v2 offset:56320\n"
MF "a[56:59],v[24:27],v[88:91],a[220:223]\n"
"s_nop 0\n"
TR "v[20:21],v3 offset:50176\n"
TR "v[22:23],v3 offset:56320\n"
MF "a[76:79],v[28:31],v[88:91],a[220:223]\n"
"v_exp_f32 v107,v107\n"
MF "a[72:75],v[28:31],v[84:87],a[220:223]\n"
"v_add_f32 v60,v55,v104\n"
"v_add_f32 v60,v60,v105\n"
MF "a[52:55],v[24:27],v[84:87],a[220:223]\n"
CV "v104,v104,v105\n"
"v_add_f32 v60,v60,v106\n"
MF "a[48:51],v[24:27],v[80:83],a[220:223]\n"
"v_add_f32 v60,v60,v107\n"
CV "v105,v106,v107\n"
MF "a[68:71],v[28:31],v[80:83],a[220:223]\n"
"v_exp_f32 v108,v108\n"
MF "a[64:67],v[28:31],v[76:79],a[220:223]\n"
"v_exp_f32 v109,v109\n"
MF "a[44:47],v[24:27],v[76:79],a[220:223]\n"
"v_exp_f32 v110,v110\n"
MF "a[40:43],v[24:27],v[72:75],a[220:223]\n"
"v_exp_f32 v111,v111\n"
MF "a[60:63],v[28:31],v[72:75],a[220:223]\n"
"s_waitcnt lgkmcnt(0)\n"
TR "v[24:25],v4 offset:50176\n"
TR "v[26:27],v4 offset:56320\n"
MF "a[80:83],v[16:19],v[72:75],a[220:223]\n"
"s_nop 0\n"
TR "v[28:29],v5 offset:50176\n"
TR "v[30:31],v5 offset:56320\n"
MF "a[100:103],v[20:23],v[72:75],a[220:223]\n"
"v_add_f32 v61,v56,v108\n"
"v_add_f32 v61,v61,v109\n"
MF "a[104:107],v[20:23],v[76:79],a[220:223]\n"
CV "v108,v108,v109\n"
"v_add_f32 v61,v61,v110\n"
MF "a[84:87],v[16:19],v[76:79],a[220:223]\n"
"v_add_f32 v61,v61,v111\n"
CV "v109,v110,v111\n"
MF "a[88:91],v[16:19],v[80:83],a[220:223]\n"
"v_exp_f32 v112,v112\n"
MF "a[108:111],v[20:23],v[80:83],a[220:223]\n"
"v_exp_f32 v113,v113\n"
MF "a[112:115],v[20:23],v[84:87],a[220:223]\n"
"v_exp_f32 v114,v114\n"
MF "a[92:95],v[16:19],v[84:87],a[220:223]\n"
"v_exp_f32 v115,v115\n"
MF "a[96:99],v[16:19],v[88:91],a[220:223]\n"
"v_add_f32 v57,v57,v112\n"
"v_add_f32 v57,v57,v113\n"
MF "a[116:119],v[20:23],v[88:91],a[220:223]\n"
"s_waitcnt lgkmcnt(0)\n"
TR "v[16:17],v2 offset:51200\n"
TR "v[18:19],v2 offset:57344\n"
MF "a[136:139],v[24:27],v[88:91],a[220:223]\n"
"s_nop 0\n"
TR "v[20:21],v3 offset:51200\n"
TR "v[22:23],v3 offset:57344\n"
MF "a[156:159],v[28:31],v[88:91],a[220:223]\n"
CV "v94,v112,v113\n"
"v_add_f32 v57,v57,v114\n"
MF "a[152:155],v[28:31],v[84:87],a[220:223]\n"
"v_add_f32 v57,v57,v115\n"
CV "v95,v114,v115\n"
MF "a[132:135],v[24:27],v[84:87],a[220:223]\n"
"v_exp_f32 v116,v116\n"
MF "a[128:131],v[24:27],v[80:83],a[220:223]\n"
"v_exp_f32 v117,v117\n"
MF "a[148:151],v[28:31],v[80:83],a[220:223]\n"
"v_exp_f32 v118,v118\n"
MF "a[144:147],v[28:31],v[76:79],a[220:223]\n"
"v_exp_f32 v119,v119\n"
MF "a[124:127],v[24:27],v[76:79],a[220:223]\n"
"v_add_f32 v58,v58,v116\n"
"v_add_f32 v58,v58,v117\n"
MF "a[120:123],v[24:27],v[72:75],a[220:223]\n"
CV "v98,v116,v117\n"
"v_add_f32 v58,v58,v118\n"
MF "a[140:143],v[28:31],v[72:75],a[220:223]\n"
"s_waitcnt lgkmcnt(0)\n"
TR "v[24:25],v4 offset:51200\n"
TR "v[26:27],v4 offset:57344\n"
MF "a[160:163],v[16:19],v[72:75],a[220:223]\n"
"s_nop 0\n"
TR "v[28:29],v5 offset:51200\n"
TR "v[30:31],v5 offset:57344\n"
MF "a[180:183],v[20:23],v[72:75],a[220:223]\n"
"v_add_f32 v58,v58,v119\n"
CV "v99,v118,v119\n"
MF "a[184:187],v[20:23],v[76:79],a[220:223]\n"
"v_exp_f32 v120,v120\n"
MF "a[164:167],v[16:19],v[76:79],a[220:223]\n"
"v_exp_f32 v121,v121\n"
MF "a[168:171],v[16:19],v[80:83],a[220:223]\n"
"v_exp_f32 v122,v122\n"
MF "a[188:191],v[20:23],v[80:83],a[220:223]\n"
"v_exp_f32 v123,v123\n"
MF "a[192:195],v[20:23],v[84:87],a[220:223]\n"
"v_add_f32 v59,v59,v120\n"
"v_add_f32 v59,v59,v121\n"
MF "a[172:175],v[16:19],v[84:87],a[220:223]\n"
"s_waitcnt vmcnt(6) lgkmcnt(0)\n"
CV "v102,v120,v121\n"
"v_add_f32 v59,v59,v122\n"
MF "a[176:179],v[16:19],v[88:91],a[220:223]\n"
"s_barrier\n"
MF "a[196:199],v[20:23],v[88:91],a[220:223]\n"
"s_add_u32 s72,s72,6144\n"
"ds_read_b128 v[16:19],v0\n"
"ds_read_b128 v[20:23],v1\n"
MF "a[216:219],v[24:27],v[88:91],a[220:223]\n"
"s_cmp_eq_u32 s72,s17\n"
"s_cselect_b32 s72,s16,s72\n"
"s_add_u32 m0,s64,49152\n"
MF "a[236:239],v[28:31],v[88:91],a[220:223]\n"
LD "v7,s[32:35],s72 offen nt lds\n"
MF "a[232:235],v[28:31],v[84:87],a[220:223]\n"
"s_add_u32 m0,s64,50048\n"
"v_add_f32 v59,v59,v123\n"
CV "v103,v122,v123\n"
MF "a[212:215],v[24:27],v[84:87],a[220:223]\n"
LD "v7,s[32:35],s72 offen offset:128 nt lds\n"
MF "a[208:211],v[24:27],v[80:83],a[220:223]\n"
"s_add_u32 m0,s64,50944\n"
MF "a[228:231],v[28:31],v[80:83],a[220:223]\n"
LD "v7,s[32:35],s72 offen offset:256 nt lds\n"
MF "a[224:227],v[28:31],v[76:79],a[220:223]\n"
MF "a[204:207],v[24:27],v[76:79],a[220:223]\n"
MF "a[200:203],v[24:27],v[72:75],a[220:223]\n"
MF "a[220:223],v[28:31],v[72:75],a[220:223]\n"
"s_waitcnt lgkmcnt(0)\n"
"s_branch .Lmid\n"
".Laloop:\n"
MF "v[72:75],v[16:19],v[136:139],v[32:35]\n"
"s_nop 0\n"
"ds_read_b128 v[24:27],v0 offset:25600\n"
"ds_read_b128 v[28:31],v1 offset:25600\n"
MF "v[72:75],v[20:23],v[156:159],v[72:75]\n"
MF "v[76:79],v[20:23],v[160:163],v[36:39]\n"
MF "v[76:79],v[16:19],v[140:143],v[76:79]\n"
MF "v[80:83],v[16:19],v[144:147],v[40:43]\n"
MF "v[80:83],v[20:23],v[164:167],v[80:83]\n"
MF "v[84:87],v[20:23],v[168:171],v[44:47]\n"
MF "v[84:87],v[16:19],v[148:151],v[84:87]\n"
"s_add_u32 s72,s72,6144\n"
"s_add_u32 m0,s64,6144\n"
MF "v[88:91],v[16:19],v[152:155],v[48:51]\n"
LD "v7,s[32:35],s72 offen nt lds\n"
MF "v[88:91],v[20:23],v[172:175],v[88:91]\n"
"s_waitcnt lgkmcnt(0)\n"
"ds_read_b128 v[16:19],v0 offset:26624\n"
"ds_read_b128 v[20:23],v1 offset:26624\n"
MF "v[88:91],v[24:27],v[192:195],v[88:91]\n"
"v_exp_f32 v124,v124\n"
MF "v[88:91],v[28:31],v[212:215],v[88:91]\n"
"s_add_u32 m0,s64,7040\n"
"v_exp_f32 v125,v125\n"
MF "v[84:87],v[28:31],v[208:211],v[84:87]\n"
LD "v7,s[32:35],s72 offen offset:128 nt lds\n"
MF "v[84:87],v[24:27],v[188:191],v[84:87]\n"
"v_exp_f32 v126,v126\n"
MF "v[80:83],v[24:27],v[184:187],v[80:83]\n"
"v_exp_f32 v127,v127\n"
MF "v[80:83],v[28:31],v[204:207],v[80:83]\n"
"s_add_u32 m0,s64,7936\n"
"v_add_f32 v60,v60,v124\n"
"v_add_f32 v60,v60,v125\n"
MF "v[76:79],v[28:31],v[200:203],v[76:79]\n"
LD "v7,s[32:35],s72 offen offset:256 nt lds\n"
MF "v[76:79],v[24:27],v[180:183],v[76:79]\n"
CV "v106,v124,v125\n"
"v_add_f32 v60,v60,v126\n"
MF "v[72:75],v[24:27],v[176:179],v[72:75]\n"
"v_add_f32 v60,v60,v127\n"
CV "v107,v126,v127\n"
MF "v[72:75],v[28:31],v[196:199],v[72:75]\n"
"s_waitcnt lgkmcnt(0)\n"
"ds_read_b128 v[24:27],v0 offset:30720\n"
"ds_read_b128 v[28:31],v1 offset:30720\n"
MF "v[72:75],v[16:19],v[216:219],v[72:75]\n"
"v_exp_f32 v128,v128\n"
MF "v[72:75],v[20:23],v[236:239],v[72:75]\n"
"v_exp_f32 v129,v129\n"
MF "v[76:79],v[20:23],v[240:243],v[76:79]\n"
"v_exp_f32 v130,v130\n"
MF "v[76:79],v[16:19],v[220:223],v[76:79]\n"
"v_exp_f32 v131,v131\n"
MF "v[80:83],v[16:19],v[224:227],v[80:83]\n"
"v_add_f32 v61,v61,v128\n"
"v_add_f32 v61,v61,v129\n"
MF "v[80:83],v[20:23],v[244:247],v[80:83]\n"
CV "v110,v128,v129\n"
"v_add_f32 v61,v61,v130\n"
MF "v[84:87],v[20:23],v[248:251],v[84:87]\n"
"v_add_f32 v61,v61,v131\n"
CV "v111,v130,v131\n"
MF "v[84:87],v[16:19],v[228:231],v[84:87]\n"
"v_max3_f32 v10,v57,v58,v59\n"
"v_max3_f32 v10,v10,v60,v61\n"
MF "v[88:91],v[16:19],v[232:235],v[88:91]\n"
"v_cmp_ngt_f32 vcc,s77,v10\n"
MF "v[88:91],v[20:23],v[252:255],v[88:91]\n"
"s_waitcnt lgkmcnt(0)\n"
"s_cbranch_vccnz .Lstub_11\n"
".Lback_11:\n"
MF "v[112:115],v[24:27],v[136:139],v[32:35]\n"
"s_add_u32 s74,s74,12288\n"
"ds_read_b128 v[16:19],v0 offset:31744\n"
"ds_read_b128 v[20:23],v1 offset:31744\n"
MF "v[112:115],v[28:31],v[156:159],v[112:115]\n"
"s_cmp_eq_u32 s74,s20\n"
"s_cselect_b32 s74,s70,s74\n"
MF "v[116:119],v[28:31],v[160:163],v[36:39]\n"
"v_exp_f32 v72,v72\n"
MF "v[116:119],v[24:27],v[140:143],v[116:119]\n"
"v_exp_f32 v73,v73\n"
MF "v[120:123],v[24:27],v[144:147],v[40:43]\n"
"v_exp_f32 v74,v74\n"
MF "v[120:123],v[28:31],v[164:167],v[120:123]\n"
"v_exp_f32 v75,v75\n"
MF "v[124:127],v[28:31],v[168:171],v[44:47]\n"
"v_add_f32 v52,v57,v72\n"
"v_add_f32 v52,v52,v73\n"
MF "v[124:127],v[24:27],v[148:151],v[124:127]\n"
CV "v72,v72,v73\n"
"v_add_f32 v52,v52,v74\n"
MF "v[128:131],v[24:27],v[152:155],v[48:51]\n"
"v_add_f32 v52,v52,v75\n"
CV "v73,v74,v75\n"
MF "v[128:131],v[28:31],v[172:175],v[128:131]\n"
"s_waitcnt lgkmcnt(0)\n"
"ds_read_b128 v[24:27],v0 offset:32768\n"
"ds_read_b128 v[28:31],v1 offset:32768\n"
MF "v[128:131],v[16:19],v[192:195],v[128:131]\n"
"v_exp_f32 v76,v76\n"
MF "v[128:131],v[20:23],v[212:215],v[128:131]\n"
"v_exp_f32 v77,v77\n"
MF "v[124:127],v[20:23],v[208:211],v[124:127]\n"
"v_exp_f32 v78,v78\n"
MF "v[124:127],v[16:19],v[188:191],v[124:127]\n"
"v_exp_f32 v79,v79\n"
MF "v[120:123],v[16:19],v[184:187],v[120:123]\n"
"v_add_f32 v53,v58,v76\n"
"v_add_f32 v53,v53,v77\n"
MF "v[120:123],v[20:23],v[204:207],v[120:123]\n"
CV "v76,v76,v77\n"
"v_add_f32 v53,v53,v78\n"
MF "v[116:119],v[20:23],v[200:203],v[116:119]\n"
"s_waitcnt vmcnt(6) lgkmcnt(0)\n"
"v_add_f32 v53,v53,v79\n"
CV "v77,v78,v79\n"
MF "v[116:119],v[16:19],v[180:183],v[116:119]\n"
"s_barrier\n"
MF "v[112:115],v[16:19],v[176:179],v[112:115]\n"
"v_exp_f32 v80,v80\n"
MF "v[112:115],v[20:23],v[196:199],v[112:115]\n"
"s_nop 0\n"
TR "v[16:17],v2 offset:49152\n"
TR "v[18:19],v2 offset:55296\n"
MF "v[112:115],v[24:27],v[216:219],v[112:115]\n"
"s_add_u32 s73,s73,12288\n"
TR "v[20:21],v3 offset:49152\n"
TR "v[22:23],v3 offset:55296\n"
MF "v[112:115],v[28:31],v[236:239],v[112:115]\n"
"s_cmp_eq_u32 s73,s19\n"
"s_cselect_b32 s73,s18,s73\n"
"s_add_u32 m0,s65,24576\n"
MF "v[116:119],v[28:31],v[240:243],v[116:119]\n"
LD "v8,s[36:39],s73 offen nt lds\n"
MF "v[116:119],v[24:27],v[220:223],v[116:119]\n"
"s_add_u32 m0,s65,25472\n"
"v_exp_f32 v81,v81\n"
MF "v[120:123],v[24:27],v[224:227],v[120:123]\n"
LD "v8,s[36:39],s73 offen offset:128 nt lds\n"
MF "v[120:123],v[28:31],v[244:247],v[120:123]\n"
"v_exp_f32 v82,v82\n"
MF "v[124:127],v[28:31],v[248:251],v[124:127]\n"
"s_add_u32 m0,s65,26368\n"
"v_exp_f32 v83,v83\n"
MF "v[124:127],v[24:27],v[228:231],v[124:127]\n"
LD "v8,s[36:39],s73 offen offset:256 nt lds\n"
MF "v[128:131],v[24:27],v[232:235],v[128:131]\n"
"v_add_f32 v54,v59,v80\n"
"v_add_f32 v54,v54,v81\n"
MF "v[128:131],v[28:31],v[252:255],v[128:131]\n"
"s_waitcnt lgkmcnt(0)\n"
TR "v[24:25],v4 offset:49152\n"
TR "v[26:27],v4 offset:55296\n"
MF "a[0:3],v[16:19],v[92:95],a[0:3]\n"
"s_add_u32 m0,s65,24576\n"
TR "v[28:29],v5 offset:49152\n"
TR "v[30:31],v5 offset:55296\n"
MF "a[20:23],v[20:23],v[92:95],a[20:23]\n"
LD "v8,s[36:39],s73 offen offset:3072 nt lds\n"
MF "a[24:27],v[20:23],v[96:99],a[24:27]\n"
CV "v80,v80,v81\n"
"v_add_f32 v54,v54,v82\n"
MF "a[4:7],v[16:19],v[96:99],a[4:7]\n"
"s_add_u32 m0,s65,25472\n"
"v_add_f32 v54,v54,v83\n"
CV "v81,v82,v83\n"
MF "a[8:11],v[16:19],v[100:103],a[8:11]\n"
LD "v8,s[36:39],s73 offen offset:3200 nt lds\n"
MF "a[28:31],v[20:23],v[100:103],a[28:31]\n"
"v_exp_f32 v84,v84\n"
MF "a[32:35],v[20:23],v[104:107],a[32:35]\n"
"s_add_u32 m0,s65,26368\n"
"v_exp_f32 v85,v85\n"
MF "a[12:15],v[16:19],v[104:107],a[12:15]\n"
LD "v8,s[36:39],s73 offen offset:3328 nt lds\n"
MF "a[16:19],v[16:19],v[108:111],a[16:19]\n"
"v_exp_f32 v86,v86\n"
MF "a[36:39],v[20:23],v[108:111],a[36:39]\n"
"s_waitcnt lgkmcnt(0)\n"
TR "v[16:17],v2 offset:50176\n"
TR "v[18:19],v2 offset:56320\n"
MF "a[56:59],v[24:27],v[108:111],a[56:59]\n"
"s_nop 0\n"
TR "v[20:21],v3 offset:50176\n"
TR "v[22:23],v3 offset:56320\n"
MF "a[76:79],v[28:31],v[108:111],a[76:79]\n"
"v_exp_f32 v87,v87\n"
MF "a[72:75],v[28:31],v[104:107],a[72:75]\n"
"v_add_f32 v55,v60,v84\n"
"v_add_f32 v55,v55,v85\n"
MF "a[52:55],v[24:27],v[104:107],a[52:55]\n"
CV "v84,v84,v85\n"
"v_add_f32 v55,v55,v86\n"
MF "a[48:51],v[24:27],v[100:103],a[48:51]\n"
"v_add_f32 v55,v55,v87\n"
CV "v85,v86,v87\n"
MF "a[68:71],v[28:31],v[100:103],a[68:71]\n"
"v_exp_f32 v88,v88\n"
MF "a[64:67],v[28:31],v[96:99],a[64:67]\n"
"v_exp_f32 v89,v89\n"
MF "a[44:47],v[24:27],v[96:99],a[44:47]\n"
"v_exp_f32 v90,v90\n"
MF "a[40:43],v[24:27],v[92:95],a[40:43]\n"
"v_exp_f32 v91,v91\n"
MF "a[60:63],v[28:31],v[92:95],a[60:63]\n"
"s_waitcnt lgkmcnt(0)\n"
TR "v[24:25],v4 offset:50176\n"
TR "v[26:27],v4 offset:56320\n"
MF "a[80:83],v[16:19],v[92:95],a[80:83]\n"
"s_nop 0\n"
TR "v[28:29],v5 offset:50176\n"
TR "v[30:31],v5 offset:56320\n"
MF "a[100:103],v[20:23],v[92:95],a[100:103]\n"
"v_add_f32 v56,v61,v88\n"
"v_add_f32 v56,v56,v89\n"
MF "a[104:107],v[20:23],v[96:99],a[104:107]\n"
CV "v88,v88,v89\n"
"v_add_f32 v56,v56,v90\n"
MF "a[84:87],v[16:19],v[96:99],a[84:87]\n"
"v_add_f32 v56,v56,v91\n"
CV "v89,v90,v91\n"
MF "a[88:91],v[16:19],v[100:103],a[88:91]\n"
"v_exp_f32 v112,v112\n"
MF "a[108:111],v[20:23],v[100:103],a[108:111]\n"
"v_exp_f32 v113,v113\n"
MF "a[112:115],v[20:23],v[104:107],a[112:115]\n"
"v_exp_f32 v114,v114\n"
MF "a[92:95],v[16:19],v[104:107],a[92:95]\n"
"v_exp_f32 v115,v115\n"
MF "a[96:99],v[16:19],v[108:111],a[96:99]\n"
"v_add_f32 v52,v52,v112\n"
"v_add_f32 v52,v52,v113\n"
MF "a[116:119],v[20:23],v[108:111],a[116:119]\n"
"s_waitcnt lgkmcnt(0)\n"
TR "v[16:17],v2 offset:51200\n"
TR "v[18:19],v2 offset:57344\n"
MF "a[136:139],v[24:27],v[108:111],a[136:139]\n"
"s_nop 0\n"
TR "v[20:21],v3 offset:51200\n"
TR "v[22:23],v3 offset:57344\n"
MF "a[156:159],v[28:31],v[108:111],a[156:159]\n"
CV "v74,v112,v113\n"
"v_add_f32 v52,v52,v114\n"
MF "a[152:155],v[28:31],v[104:107],a[152:155]\n"
"v_add_f32 v52,v52,v115\n"
CV "v75,v114,v115\n"
MF "a[132:135],v[24:27],v[104:107],a[132:135]\n"
"v_exp_f32 v116,v116\n"
MF "a[128:131],v[24:27],v[100:103],a[128:131]\n"
"v_exp_f32 v117,v117\n"
MF "a[148:151],v[28:31],v[100:103],a[148:151]\n"
"v_exp_f32 v118,v118\n"
MF "a[144:147],v[28:31],v[96:99],a[144:147]\n"
"v_exp_f32 v119,v119\n"
MF "a[124:127],v[24:27],v[96:99],a[124:127]\n"
"v_add_f32 v53,v53,v116\n"
"v_add_f32 v53,v53,v117\n"
MF "a[120:123],v[24:27],v[92:95],a[120:123]\n"
CV "v78,v116,v117\n"
"v_add_f32 v53,v53,v118\n"
MF "a[140:143],v[28:31],v[92:95],a[140:143]\n"
"s_waitcnt lgkmcnt(0)\n"
TR "v[24:25],v4 offset:51200\n"
TR "v[26:27],v4 offset:57344\n"
MF "a[160:163],v[16:19],v[92:95],a[160:163]\n"
"s_nop 0\n"
TR "v[28:29],v5 offset:51200\n"
TR "v[30:31],v5 offset:57344\n"
MF "a[180:183],v[20:23],v[92:95],a[180:183]\n"
"v_add_f32 v53,v53,v119\n"
CV "v79,v118,v119\n"
MF "a[184:187],v[20:23],v[96:99],a[184:187]\n"
"v_exp_f32 v120,v120\n"
MF "a[164:167],v[16:19],v[96:99],a[164:167]\n"
"v_exp_f32 v121,v121\n"
MF "a[168:171],v[16:19],v[100:103],a[168:171]\n"
"v_exp_f32 v122,v122\n"
MF "a[188:191],v[20:23],v[100:103],a[188:191]\n"
"v_exp_f32 v123,v123\n"
MF "a[192:195],v[20:23],v[104:107],a[192:195]\n"
"v_add_f32 v54,v54,v120\n"
"v_add_f32 v54,v54,v121\n"
MF "a[172:175],v[16:19],v[104:107],a[172:175]\n"
"s_waitcnt vmcnt(6) lgkmcnt(0)\n"
CV "v82,v120,v121\n"
"v_add_f32 v54,v54,v122\n"
MF "a[176:179],v[16:19],v[108:111],a[176:179]\n"
"s_barrier\n"
MF "a[196:199],v[20:23],v[108:111],a[196:199]\n"
"s_add_u32 s72,s72,6144\n"
"ds_read_b128 v[16:19],v0\n"
"ds_read_b128 v[20:23],v1\n"
MF "a[216:219],v[24:27],v[108:111],a[216:219]\n"
"s_cmp_eq_u32 s72,s17\n"
"s_cselect_b32 s72,s16,s72\n"
"s_add_u32 m0,s64,49152\n"
MF "a[236:239],v[28:31],v[108:111],a[236:239]\n"
LD "v7,s[32:35],s72 offen nt lds\n"
MF "a[232:235],v[28:31],v[104:107],a[232:235]\n"
"s_add_u32 m0,s64,50048\n"
"v_add_f32 v54,v54,v123\n"
CV "v83,v122,v123\n"
MF "a[212:215],v[24:27],v[104:107],a[212:215]\n"
LD "v7,s[32:35],s72 offen offset:128 nt lds\n"
MF "a[208:211],v[24:27],v[100:103],a[208:211]\n"
"s_add_u32 m0,s64,50944\n"
MF "a[228:231],v[28:31],v[100:103],a[228:231]\n"
LD "v7,s[32:35],s72 offen offset:256 nt lds\n"
MF "a[224:227],v[28:31],v[96:99],a[224:227]\n"
MF "a[204:207],v[24:27],v[96:99],a[204:207]\n"
MF "a[200:203],v[24:27],v[92:95],a[200:203]\n"
MF "a[220:223],v[28:31],v[92:95],a[220:223]\n"
"s_waitcnt lgkmcnt(0)\n"
MF "v[92:95],v[16:19],v[136:139],v[32:35]\n"
"s_add_u32 s72,s72,6144\n"
"ds_read_b128 v[24:27],v0 offset:1024\n"
"ds_read_b128 v[28:31],v1 offset:1024\n"
MF "v[92:95],v[20:23],v[156:159],v[92:95]\n"
"s_add_u32 m0,s64,55296\n"
"v_exp_f32 v124,v124\n"
MF "v[96:99],v[20:23],v[160:163],v[36:39]\n"
LD "v7,s[32:35],s72 offen nt lds\n"
MF "v[96:99],v[16:19],v[140:143],v[96:99]\n"
"v_exp_f32 v125,v125\n"
MF "v[100:103],v[16:19],v[144:147],v[40:43]\n"
"s_add_u32 m0,s64,56192\n"
"v_exp_f32 v126,v126\n"
MF "v[100:103],v[20:23],v[164:167],v[100:103]\n"
LD "v7,s[32:35],s72 offen offset:128 nt lds\n"
MF "v[104:107],v[20:23],v[168:171],v[44:47]\n"
"v_exp_f32 v127,v127\n"
MF "v[104:107],v[16:19],v[148:151],v[104:107]\n"
"s_add_u32 m0,s64,57088\n"
"v_add_f32 v55,v55,v124\n"
"v_add_f32 v55,v55,v125\n"
MF "v[108:111],v[16:19],v[152:155],v[48:51]\n"
LD "v7,s[32:35],s72 offen offset:256 nt lds\n"
MF "v[108:111],v[20:23],v[172:175],v[108:111]\n"
"s_waitcnt lgkmcnt(0)\n"
"ds_read_b128 v[16:19],v0 offset:2048\n"
"ds_read_b128 v[20:23],v1 offset:2048\n"
MF "v[108:111],v[24:27],v[192:195],v[108:111]\n"
CV "v86,v124,v125\n"
"v_add_f32 v55,v55,v126\n"
MF "v[108:111],v[28:31],v[212:215],v[108:111]\n"
"v_add_f32 v55,v55,v127\n"
CV "v87,v126,v127\n"
MF "v[104:107],v[28:31],v[208:211],v[104:107]\n"
"v_exp_f32 v128,v128\n"
MF "v[104:107],v[24:27],v[188:191],v[104:107]\n"
"v_exp_f32 v129,v129\n"
MF "v[100:103],v[24:27],v[184:187],v[100:103]\n"
"v_exp_f32 v130,v130\n"
MF "v[100:103],v[28:31],v[204:207],v[100:103]\n"
"v_exp_f32 v131,v131\n"
MF "v[96:99],v[28:31],v[200:203],v[96:99]\n"
"v_add_f32 v56,v56,v128\n"
"v_add_f32 v56,v56,v129\n"
MF "v[96:99],v[24:27],v[180:183],v[96:99]\n"
CV "v90,v128,v129\n"
"v_add_f32 v56,v56,v130\n"
MF "v[92:95],v[24:27],v[176:179],v[92:95]\n"
"v_add_f32 v56,v56,v131\n"
CV "v91,v130,v131\n"
MF "v[92:95],v[28:31],v[196:199],v[92:95]\n"
"s_waitcnt lgkmcnt(0)\n"
"ds_read_b128 v[24:27],v0 offset:6144\n"
"ds_read_b128 v[28:31],v1 offset:6144\n"
MF "v[92:95],v[16:19],v[216:219],v[92:95]\n"
MF "v[92:95],v[20:23],v[236:239],v[92:95]\n"
MF "v[96:99],v[20:23],v[240:243],v[96:99]\n"
MF "v[96:99],v[16:19],v[220:223],v[96:99]\n"
MF "v[100:103],v[16:19],v[224:227],v[100:103]\n"
MF "v[100:103],v[20:23],v[244:247],v[100:103]\n"
MF "v[104:107],v[20:23],v[248:251],v[104:107]\n"
MF "v[104:107],v[16:19],v[228:231],v[104:107]\n"
"v_max3_f32 v10,v52,v53,v54\n"
"v_max3_f32 v10,v10,v55,v56\n"
MF "v[108:111],v[16:19],v[232:235],v[108:111]\n"
"v_cmp_ngt_f32 vcc,s77,v10\n"
MF "v[108:111],v[20:23],v[252:255],v[108:111]\n"
"s_waitcnt lgkmcnt(0)\n"
"s_cbranch_vccnz .Lstub_12\n"
".Lback_12:\n"
MF "v[112:115],v[24:27],v[136:139],v[32:35]\n"
"s_add_u32 s74,s74,12288\n"
"ds_read_b128 v[16:19],v0 offset:7168\n"
"ds_read_b128 v[20:23],v1 offset:7168\n"
MF "v[112:115],v[28:31],v[156:159],v[112:115]\n"
"s_cmp_eq_u32 s74,s20\n"
"s_cselect_b32 s74,s70,s74\n"
MF "v[116:119],v[28:31],v[160:163],v[36:39]\n"
"v_exp_f32 v92,v92\n"
MF "v[116:119],v[24:27],v[140:143],v[116:119]\n"
"v_exp_f32 v93,v93\n"
MF "v[120:123],v[24:27],v[144:147],v[40:43]\n"
"v_exp_f32 v94,v94\n"
MF "v[120:123],v[28:31],v[164:167],v[120:123]\n"
"v_exp_f32 v95,v95\n"
MF "v[124:127],v[28:31],v[168:171],v[44:47]\n"
"v_add_f32 v57,v52,v92\n"
"v_add_f32 v57,v57,v93\n"
MF "v[124:127],v[24:27],v[148:151],v[124:127]\n"
CV "v92,v92,v93\n"
"v_add_f32 v57,v57,v94\n"
MF "v[128:131],v[24:27],v[152:155],v[48:51]\n"
"v_add_f32 v57,v57,v95\n"
CV "v93,v94,v95\n"
MF "v[128:131],v[28:31],v[172:175],v[128:131]\n"
"s_waitcnt lgkmcnt(0)\n"
"ds_read_b128 v[24:27],v0 offset:8192\n"
"ds_read_b128 v[28:31],v1 offset:8192\n"
MF "v[128:131],v[16:19],v[192:195],v[128:131]\n"
"v_exp_f32 v96,v96\n"
MF "v[128:131],v[20:23],v[212:215],v[128:131]\n"
"v_exp_f32 v97,v97\n"
MF "v[124:127],v[20:23],v[208:211],v[124:127]\n"
"v_exp_f32 v98,v98\n"
MF "v[124:127],v[16:19],v[188:191],v[124:127]\n"
"v_exp_f32 v99,v99\n"
MF "v[120:123],v[16:19],v[184:187],v[120:123]\n"
"v_add_f32 v58,v53,v96\n"
"v_add_f32 v58,v58,v97\n"
MF "v[120:123],v[20:23],v[204:207],v[120:123]\n"
CV "v96,v96,v97\n"
"v_add_f32 v58,v58,v98\n"
MF "v[116:119],v[20:23],v[200:203],v[116:119]\n"
"s_waitcnt vmcnt(6) lgkmcnt(0)\n"
"v_add_f32 v58,v58,v99\n"
CV "v97,v98,v99\n"
MF "v[116:119],v[16:19],v[180:183],v[116:119]\n"
"s_barrier\n"
MF "v[112:115],v[16:19],v[176:179],v[112:115]\n"
"v_exp_f32 v100,v100\n"
MF "v[112:115],v[20:23],v[196:199],v[112:115]\n"
"s_nop 0\n"
TR "v[16:17],v2 offset:24576\n"
TR "v[18:19],v2 offset:30720\n"
MF "v[112:115],v[24:27],v[216:219],v[112:115]\n"
"s_add_u32 s73,s73,12288\n"
TR "v[20:21],v3 offset:24576\n"
TR "v[22:23],v3 offset:30720\n"
MF "v[112:115],v[28:31],v[236:239],v[112:115]\n"
"s_cmp_eq_u32 s73,s19\n"
"s_cselect_b32 s73,s18,s73\n"
"s_add_u32 m0,s65,0\n"
MF "v[116:119],v[28:31],v[240:243],v[116:119]\n"
LD "v8,s[36:39],s73 offen nt lds\n"
MF "v[116:119],v[24:27],v[220:223],v[116:119]\n"
"s_add_u32 m0,s65,896\n"
"v_exp_f32 v101,v101\n"
MF "v[120:123],v[24:27],v[224:227],v[120:123]\n"
LD "v8,s[36:39],s73 offen offset:128 nt lds\n"
MF "v[120:123],v[28:31],v[244:247],v[120:123]\n"
"v_exp_f32 v102,v102\n"
MF "v[124:127],v[28:31],v[248:251],v[124:127]\n"
"s_add_u32 m0,s65,1792\n"
"v_exp_f32 v103,v103\n"
MF "v[124:127],v[24:27],v[228:231],v[124:127]\n"
LD "v8,s[36:39],s73 offen offset:256 nt lds\n"
MF "v[128:131],v[24:27],v[232:235],v[128:131]\n"
"v_add_f32 v59,v54,v100\n"
"v_add_f32 v59,v59,v101\n"
MF "v[128:131],v[28:31],v[252:255],v[128:131]\n"
"s_waitcnt lgkmcnt(0)\n"
TR "v[24:25],v4 offset:24576\n"
TR "v[26:27],v4 offset:30720\n"
MF "a[0:3],v[16:19],v[72:75],a[0:3]\n"
"s_add_u32 m0,s65,0\n"
TR "v[28:29],v5 offset:24576\n"
TR "v[30:31],v5 offset:30720\n"
MF "a[20:23],v[20:23],v[72:75],a[20:23]\n"
LD "v8,s[36:39],s73 offen offset:3072 nt lds\n"
MF "a[24:27],v[20:23],v[76:79],a[24:27]\n"
CV "v100,v100,v101\n"
"v_add_f32 v59,v59,v102\n"
MF "a[4:7],v[16:19],v[76:79],a[4:7]\n"
"s_add_u32 m0,s65,896\n"
"v_add_f32 v59,v59,v103\n"
CV "v101,v102,v103\n"
MF "a[8:11],v[16:19],v[80:83],a[8:11]\n"
LD "v8,s[36:39],s73 offen offset:3200 nt lds\n"
MF "a[28:31],v[20:23],v[80:83],a[28:31]\n"
"v_exp_f32 v104,v104\n"
MF "a[32:35],v[20:23],v[84:87],a[32:35]\n"
"s_add_u32 m0,s65,1792\n"
"v_exp_f32 v105,v105\n"
MF "a[12:15],v[16:19],v[84:87],a[12:15]\n"
LD "v8,s[36:39],s73 offen offset:3328 nt lds\n"
MF "a[16:19],v[16:19],v[88:91],a[16:19]\n"
"v_exp_f32 v106,v106\n"
MF "a[36:39],v[20:23],v[88:91],a[36:39]\n"
"s_waitcnt lgkmcnt(0)\n"
TR "v[16:17],v2 offset:25600\n"
TR "v[18:19],v2 offset:31744\n"
MF "a[56:59],v[24:27],v[88:91],a[56:59]\n"
"s_nop 0\n"
TR "v[20:21],v3 offset:25600\n"
TR "v[22:23],v3 offset:31744\n"
MF "a[76:79],v[28:31],v[88:91],a[76:79]\n"
"v_exp_f32 v107,v107\n"
MF "a[72:75],v[28:31],v[84:87],a[72:75]\n"
"v_add_f32 v60,v55,v104\n"
"v_add_f32 v60,v60,v105\n"
MF "a[52:55],v[24:27],v[84:87],a[52:55]\n"
CV "v104,v104,v105\n"
"v_add_f32 v60,v60,v106\n"
MF "a[48:51],v[24:27],v[80:83],a[48:51]\n"
"v_add_f32 v60,v60,v107\n"
CV "v105,v106,v107\n"
MF "a[68:71],v[28:31],v[80:83],a[68:71]\n"
"v_exp_f32 v108,v108\n"
MF "a[64:67],v[28:31],v[76:79],a[64:67]\n"
"v_exp_f32 v109,v109\n"
MF "a[44:47],v[24:27],v[76:79],a[44:47]\n"
"v_exp_f32 v110,v110\n"
MF "a[40:43],v[24:27],v[72:75],a[40:43]\n"
"v_exp_f32 v111,v111\n"
MF "a[60:63],v[28:31],v[72:75],a[60:63]\n"
"s_waitcnt lgkmcnt(0)\n"
TR "v[24:25],v4 offset:25600\n"
TR "v[26:27],v4 offset:31744\n"
MF "a[80:83],v[16:19],v[72:75],a[80:83]\n"
"s_nop 0\n"
TR "v[28:29],v5 offset:25600\n"
TR "v[30:31],v5 offset:31744\n"
MF "a[100:103],v[20:23],v[72:75],a[100:103]\n"
"v_add_f32 v61,v56,v108\n"
"v_add_f32 v61,v61,v109\n"
MF "a[104:107],v[20:23],v[76:79],a[104:107]\n"
CV "v108,v108,v109\n"
"v_add_f32 v61,v61,v110\n"
MF "a[84:87],v[16:19],v[76:79],a[84:87]\n"
"v_add_f32 v61,v61,v111\n"
CV "v109,v110,v111\n"
MF "a[88:91],v[16:19],v[80:83],a[88:91]\n"
"v_exp_f32 v112,v112\n"
MF "a[108:111],v[20:23],v[80:83],a[108:111]\n"
"v_exp_f32 v113,v113\n"
MF "a[112:115],v[20:23],v[84:87],a[112:115]\n"
"v_exp_f32 v114,v114\n"
MF "a[92:95],v[16:19],v[84:87],a[92:95]\n"
"v_exp_f32 v115,v115\n"
MF "a[96:99],v[16:19],v[88:91],a[96:99]\n"
"v_add_f32 v57,v57,v112\n"
"v_add_f32 v57,v57,v113\n"
MF "a[116:119],v[20:23],v[88:91],a[116:119]\n"
"s_waitcnt lgkmcnt(0)\n"
TR "v[16:17],v2 offset:26624\n"
TR "v[18:19],v2 offset:32768\n"
MF "a[136:139],v[24:27],v[88:91],a[136:139]\n"
"s_nop 0\n"
TR "v[20:21],v3 offset:26624\n"
TR "v[22:23],v3 offset:32768\n"
MF "a[156:159],v[28:31],v[88:91],a[156:159]\n"
CV "v94,v112,v113\n"
"v_add_f32 v57,v57,v114\n"
MF "a[152:155],v[28:31],v[84:87],a[152:155]\n"
"v_add_f32 v57,v57,v115\n"
CV "v95,v114,v115\n"
MF "a[132:135],v[24:27],v[84:87],a[132:135]\n"
"v_exp_f32 v116,v116\n"
MF "a[128:131],v[24:27],v[80:83],a[128:131]\n"
"v_exp_f32 v117,v117\n"
MF "a[148:151],v[28:31],v[80:83],a[148:151]\n"
"v_exp_f32 v118,v118\n"
MF "a[144:147],v[28:31],v[76:79],a[144:147]\n"
"v_exp_f32 v119,v119\n"
MF "a[124:127],v[24:27],v[76:79],a[124:127]\n"
"v_add_f32 v58,v58,v116\n"
"v_add_f32 v58,v58,v117\n"
MF "a[120:123],v[24:27],v[72:75],a[120:123]\n"
CV "v98,v116,v117\n"
"v_add_f32 v58,v58,v118\n"
MF "a[140:143],v[28:31],v[72:75],a[140:143]\n"
"s_waitcnt lgkmcnt(0)\n"
TR "v[24:25],v4 offset:26624\n"
TR "v[26:27],v4 offset:32768\n"
MF "a[160:163],v[16:19],v[72:75],a[160:163]\n"
"s_nop 0\n"
TR "v[28:29],v5 offset:26624\n"
TR "v[30:31],v5 offset:32768\n"
MF "a[180:183],v[20:23],v[72:75],a[180:183]\n"
"v_add_f32 v58,v58,v119\n"
CV "v99,v118,v119\n"
MF "a[184:187],v[20:23],v[76:79],a[184:187]\n"
"v_exp_f32 v120,v120\n"
MF "a[164:167],v[16:19],v[76:79],a[164:167]\n"
"v_exp_f32 v121,v121\n"
MF "a[168:171],v[16:19],v[80:83],a[168:171]\n"
"v_exp_f32 v122,v122\n"
MF "a[188:191],v[20:23],v[80:83],a[188:191]\n"
"v_exp_f32 v123,v123\n"
MF "a[192:195],v[20:23],v[84:87],a[192:195]\n"
"v_add_f32 v59,v59,v120\n"
"v_add_f32 v59,v59,v121\n"
MF "a[172:175],v[16:19],v[84:87],a[172:175]\n"
"s_waitcnt vmcnt(6) lgkmcnt(0)\n"
CV "v102,v120,v121\n"
"v_add_f32 v59,v59,v122\n"
MF "a[176:179],v[16:19],v[88:91],a[176:179]\n"
"s_barrier\n"
MF "a[196:199],v[20:23],v[88:91],a[196:199]\n"
"s_add_u32 s72,s72,6144\n"
"ds_read_b128 v[16:19],v0 offset:49152\n"
"ds_read_b128 v[20:23],v1 offset:49152\n"
MF "a[216:219],v[24:27],v[88:91],a[216:219]\n"
"s_cmp_eq_u32 s72,s17\n"
"s_cselect_b32 s72,s16,s72\n"
"s_add_u32 m0,s64,24576\n"
MF "a[236:239],v[28:31],v[88:91],a[236:239]\n"
LD "v7,s[32:35],s72 offen nt lds\n"
MF "a[232:235],v[28:31],v[84:87],a[232:235]\n"
"s_add_u32 m0,s64,25472\n"
"v_add_f32 v59,v59,v123\n"
CV "v103,v122,v123\n"
MF "a[212:215],v[24:27],v[84:87],a[212:215]\n"
LD "v7,s[32:35],s72 offen offset:128 nt lds\n"
MF "a[208:211],v[24:27],v[80:83],a[208:211]\n"
"s_add_u32 m0,s64,26368\n"
MF "a[228:231],v[28:31],v[80:83],a[228:231]\n"
LD "v7,s[32:35],s72 offen offset:256 nt lds\n"
MF "a[224:227],v[28:31],v[76:79],a[224:227]\n"
MF "a[204:207],v[24:27],v[76:79],a[204:207]\n"
MF "a[200:203],v[24:27],v[72:75],a[200:203]\n"
MF "a[220:223],v[28:31],v[72:75],a[220:223]\n"
"s_waitcnt lgkmcnt(0)\n"
MF "v[72:75],v[16:19],v[136:139],v[32:35]\n"
"s_add_u32 s72,s72,6144\n"
"ds_read_b128 v[24:27],v0 offset:50176\n"
"ds_read_b128 v[28:31],v1 offset:50176\n"
MF "v[72:75],v[20:23],v[156:159],v[72:75]\n"
"s_add_u32 m0,s64,30720\n"
"v_exp_f32 v124,v124\n"
MF "v[76:79],v[20:23],v[160:163],v[36:39]\n"
LD "v7,s[32:35],s72 offen nt lds\n"
MF "v[76:79],v[16:19],v[140:143],v[76:79]\n"
"v_exp_f32 v125,v125\n"
MF "v[80:83],v[16:19],v[144:147],v[40:43]\n"
"s_add_u32 m0,s64,31616\n"
"v_exp_f32 v126,v126\n"
MF "v[80:83],v[20:23],v[164:167],v[80:83]\n"
LD "v7,s[32:35],s72 offen offset:128 nt lds\n"
MF "v[84:87],v[20:23],v[168:171],v[44:47]\n"
"v_exp_f32 v127,v127\n"
MF "v[84:87],v[16:19],v[148:151],v[84:87]\n"
"s_add_u32 m0,s64,32512\n"
"v_add_f32 v60,v60,v124\n"
"v_add_f32 v60,v60,v125\n"
MF "v[88:91],v[16:19],v[152:155],v[48:51]\n"
LD "v7,s[32:35],s72 offen offset:256 nt lds\n"
MF "v[88:91],v[20:23],v[172:175],v[88:91]\n"
"s_waitcnt lgkmcnt(0)\n"
"ds_read_b128 v[16:19],v0 offset:51200\n"
"ds_read_b128 v[20:23],v1 offset:51200\n"
MF "v[88:91],v[24:27],v[192:195],v[88:91]\n"
CV "v106,v124,v125\n"
"v_add_f32 v60,v60,v126\n"
MF "v[88:91],v[28:31],v[212:215],v[88:91]\n"
"v_add_f32 v60,v60,v127\n"
CV "v107,v126,v127\n"
MF "v[84:87],v[28:31],v[208:211],v[84:87]\n"
"v_exp_f32 v128,v128\n"
MF "v[84:87],v[24:27],v[188:191],v[84:87]\n"
"v_exp_f32 v129,v129\n"
MF "v[80:83],v[24:27],v[184:187],v[80:83]\n"
"v_exp_f32 v130,v130\n"
MF "v[80:83],v[28:31],v[204:207],v[80:83]\n"
"v_exp_f32 v131,v131\n"
MF "v[76:79],v[28:31],v[200:203],v[76:79]\n"
"v_add_f32 v61,v61,v128\n"
"v_add_f32 v61,v61,v129\n"
MF "v[76:79],v[24:27],v[180:183],v[76:79]\n"
CV "v110,v128,v129\n"
"v_add_f32 v61,v61,v130\n"
MF "v[72:75],v[24:27],v[176:179],v[72:75]\n"
"v_add_f32 v61,v61,v131\n"
CV "v111,v130,v131\n"
MF "v[72:75],v[28:31],v[196:199],v[72:75]\n"
"s_waitcnt lgkmcnt(0)\n"
"ds_read_b128 v[24:27],v0 offset:55296\n"
"ds_read_b128 v[28:31],v1 offset:55296\n"
MF "v[72:75],v[16:19],v[216:219],v[72:75]\n"
MF "v[72:75],v[20:23],v[236:239],v[72:75]\n"
MF "v[76:79],v[20:23],v[240:243],v[76:79]\n"
MF "v[76:79],v[16:19],v[220:223],v[76:79]\n"
MF "v[80:83],v[16:19],v[224:227],v[80:83]\n"
MF "v[80:83],v[20:23],v[244:247],v[80:83]\n"
MF "v[84:87],v[20:23],v[248:251],v[84:87]\n"
MF "v[84:87],v[16:19],v[228:231],v[84:87]\n"
"v_max3_f32 v10,v57,v58,v59\n"
"v_max3_f32 v10,v10,v60,v61\n"
MF "v[88:91],v[16:19],v[232:235],v[88:91]\n"
"v_cmp_ngt_f32 vcc,s95,v10\n"
MF "v[88:91],v[20:23],v[252:255],v[88:91]\n"
"s_waitcnt lgkmcnt(0)\n"
"s_cbranch_vccnz .Lstub_13\n"
".Lback_13:\n"
MF "v[112:115],v[24:27],v[136:139],v[32:35]\n"
"s_add_u32 s74,s74,12288\n"
"ds_read_b128 v[16:19],v0 offset:56320\n"
"ds_read_b128 v[20:23],v1 offset:56320\n"
MF "v[112:115],v[28:31],v[156:159],v[112:115]\n"
"s_cmp_eq_u32 s74,s20\n"
"s_cselect_b32 s74,s70,s74\n"
MF "v[116:119],v[28:31],v[160:163],v[36:39]\n"
"v_exp_f32 v72,v72\n"
MF "v[116:119],v[24:27],v[140:143],v[116:119]\n"
"v_exp_f32 v73,v73\n"
MF "v[120:123],v[24:27],v[144:147],v[40:43]\n"
"v_exp_f32 v74,v74\n"
MF "v[120:123],v[28:31],v[164:167],v[120:123]\n"
"v_exp_f32 v75,v75\n"
MF "v[124:127],v[28:31],v[168:171],v[44:47]\n"
"v_add_f32 v52,v57,v72\n"
"v_add_f32 v52,v52,v73\n"
MF "v[124:127],v[24:27],v[148:151],v[124:127]\n"
CV "v72,v72,v73\n"
"v_add_f32 v52,v52,v74\n"
MF "v[128:131],v[24:27],v[152:155],v[48:51]\n"
"v_add_f32 v52,v52,v75\n"
CV "v73,v74,v75\n"
MF "v[128:131],v[28:31],v[172:175],v[128:131]\n"
"s_waitcnt lgkmcnt(0)\n"
"ds_read_b128 v[24:27],v0 offset:57344\n"
"ds_read_b128 v[28:31],v1 offset:57344\n"
MF "v[128:131],v[16:19],v[192:195],v[128:131]\n"
"v_exp_f32 v76,v76\n"
MF "v[128:131],v[20:23],v[212:215],v[128:131]\n"
"v_exp_f32 v77,v77\n"
MF "v[124:127],v[20:23],v[208:211],v[124:127]\n"
"v_exp_f32 v78,v78\n"
MF "v[124:127],v[16:19],v[188:191],v[124:127]\n"
"v_exp_f32 v79,v79\n"
MF "v[120:123],v[16:19],v[184:187],v[120:123]\n"
"v_add_f32 v53,v58,v76\n"
"v_add_f32 v53,v53,v77\n"
MF "v[120:123],v[20:23],v[204:207],v[120:123]\n"
CV "v76,v76,v77\n"
"v_add_f32 v53,v53,v78\n"
MF "v[116:119],v[20:23],v[200:203],v[116:119]\n"
"s_waitcnt vmcnt(6) lgkmcnt(0)\n"
"v_add_f32 v53,v53,v79\n"
CV "v77,v78,v79\n"
MF "v[116:119],v[16:19],v[180:183],v[116:119]\n"
"s_barrier\n"
MF "v[112:115],v[16:19],v[176:179],v[112:115]\n"
"v_exp_f32 v80,v80\n"
MF "v[112:115],v[20:23],v[196:199],v[112:115]\n"
"s_nop 0\n"
TR "v[16:17],v2\n"
TR "v[18:19],v2 offset:6144\n"
MF "v[112:115],v[24:27],v[216:219],v[112:115]\n"
"s_add_u32 s73,s73,12288\n"
TR "v[20:21],v3\n"
TR "v[22:23],v3 offset:6144\n"
MF "v[112:115],v[28:31],v[236:239],v[112:115]\n"
"s_cmp_eq_u32 s73,s19\n"
"s_cselect_b32 s73,s18,s73\n"
"s_add_u32 m0,s65,49152\n"
MF "v[116:119],v[28:31],v[240:243],v[116:119]\n"
LD "v8,s[36:39],s73 offen nt lds\n"
MF "v[116:119],v[24:27],v[220:223],v[116:119]\n"
"s_add_u32 m0,s65,50048\n"
"v_exp_f32 v81,v81\n"
MF "v[120:123],v[24:27],v[224:227],v[120:123]\n"
LD "v8,s[36:39],s73 offen offset:128 nt lds\n"
MF "v[120:123],v[28:31],v[244:247],v[120:123]\n"
"v_exp_f32 v82,v82\n"
MF "v[124:127],v[28:31],v[248:251],v[124:127]\n"
"s_add_u32 m0,s65,50944\n"
"v_exp_f32 v83,v83\n"
MF "v[124:127],v[24:27],v[228:231],v[124:127]\n"
LD "v8,s[36:39],s73 offen offset:256 nt lds\n"
MF "v[128:131],v[24:27],v[232:235],v[128:131]\n"
"v_add_f32 v54,v59,v80\n"
"v_add_f32 v54,v54,v81\n"
MF "v[128:131],v[28:31],v[252:255],v[128:131]\n"
"s_waitcnt lgkmcnt(0)\n"
TR "v[24:25],v4\n"
TR "v[26:27],v4 offset:6144\n"
MF "a[0:3],v[16:19],v[92:95],a[0:3]\n"
"s_add_u32 m0,s65,49152\n"
TR "v[28:29],v5\n"
TR "v[30:31],v5 offset:6144\n"
MF "a[20:23],v[20:23],v[92:95],a[20:23]\n"
LD "v8,s[36:39],s73 offen offset:3072 nt lds\n"
MF "a[24:27],v[20:23],v[96:99],a[24:27]\n"
CV "v80,v80,v81\n"
"v_add_f32 v54,v54,v82\n"
MF "a[4:7],v[16:19],v[96:99],a[4:7]\n"
"s_add_u32 m0,s65,50048\n"
"v_add_f32 v54,v54,v83\n"
CV "v81,v82,v83\n"
MF "a[8:11],v[16:19],v[100:103],a[8:11]\n"
LD "v8,s[36:39],s73 offen offset:3200 nt lds\n"
MF "a[28:31],v[20:23],v[100:103],a[28:31]\n"
"v_exp_f32 v84,v84\n"
MF "a[32:35],v[20:23],v[104:107],a[32:35]\n"
"s_add_u32 m0,s65,50944\n"
"v_exp_f32 v85,v85\n"
MF "a[12:15],v[16:19],v[104:107],a[12:15]\n"
LD "v8,s[36:39],s73 offen offset:3328 nt lds\n"
MF "a[16:19],v[16:19],v[108:111],a[16:19]\n"
"v_exp_f32 v86,v86\n"
MF "a[36:39],v[20:23],v[108:111],a[36:39]\n"
"s_waitcnt lgkmcnt(0)\n"
TR "v[16:17],v2 offset:1024\n"
TR "v[18:19],v2 offset:7168\n"
MF "a[56:59],v[24:27],v[108:111],a[56:59]\n"
"s_nop 0\n"
TR "v[20:21],v3 offset:1024\n"
TR "v[22:23],v3 offset:7168\n"
MF "a[76:79],v[28:31],v[108:111],a[76:79]\n"
"v_exp_f32 v87,v87\n"
MF "a[72:75],v[28:31],v[104:107],a[72:75]\n"
"v_add_f32 v55,v60,v84\n"
"v_add_f32 v55,v55,v85\n"
MF "a[52:55],v[24:27],v[104:107],a[52:55]\n"
CV "v84,v84,v85\n"
"v_add_f32 v55,v55,v86\n"
MF "a[48:51],v[24:27],v[100:103],a[48:51]\n"
"v_add_f32 v55,v55,v87\n"
CV "v85,v86,v87\n"
MF "a[68:71],v[28:31],v[100:103],a[68:71]\n"
"v_exp_f32 v88,v88\n"
MF "a[64:67],v[28:31],v[96:99],a[64:67]\n"
"v_exp_f32 v89,v89\n"
MF "a[44:47],v[24:27],v[96:99],a[44:47]\n"
"v_exp_f32 v90,v90\n"
MF "a[40:43],v[24:27],v[92:95],a[40:43]\n"
"v_exp_f32 v91,v91\n"
MF "a[60:63],v[28:31],v[92:95],a[60:63]\n"
"s_waitcnt lgkmcnt(0)\n"
TR "v[24:25],v4 offset:1024\n"
TR "v[26:27],v4 offset:7168\n"
MF "a[80:83],v[16:19],v[92:95],a[80:83]\n"
"s_nop 0\n"
TR "v[28:29],v5 offset:1024\n"
TR "v[30:31],v5 offset:7168\n"
MF "a[100:103],v[20:23],v[92:95],a[100:103]\n"
"v_add_f32 v56,v61,v88\n"
"v_add_f32 v56,v56,v89\n"
MF "a[104:107],v[20:23],v[96:99],a[104:107]\n"
CV "v88,v88,v89\n"
"v_add_f32 v56,v56,v90\n"
MF "a[84:87],v[16:19],v[96:99],a[84:87]\n"
"v_add_f32 v56,v56,v91\n"
CV "v89,v90,v91\n"
MF "a[88:91],v[16:19],v[100:103],a[88:91]\n"
"v_exp_f32 v112,v112\n"
MF "a[108:111],v[20:23],v[100:103],a[108:111]\n"
"v_exp_f32 v113,v113\n"
MF "a[112:115],v[20:23],v[104:107],a[112:115]\n"
"v_exp_f32 v114,v114\n"
MF "a[92:95],v[16:19],v[104:107],a[92:95]\n"
"v_exp_f32 v115,v115\n"
MF "a[96:99],v[16:19],v[108:111],a[96:99]\n"
"v_add_f32 v52,v52,v112\n"
"v_add_f32 v52,v52,v113\n"
MF "a[116:119],v[20:23],v[108:111],a[116:119]\n"
"s_waitcnt lgkmcnt(0)\n"
TR "v[16:17],v2 offset:2048\n"
TR "v[18:19],v2 offset:8192\n"
MF "a[136:139],v[24:27],v[108:111],a[136:139]\n"
"s_nop 0\n"
TR "v[20:21],v3 offset:2048\n"
TR "v[22:23],v3 offset:8192\n"
MF "a[156:159],v[28:31],v[108:111],a[156:159]\n"
CV "v74,v112,v113\n"
"v_add_f32 v52,v52,v114\n"
MF "a[152:155],v[28:31],v[104:107],a[152:155]\n"
"v_add_f32 v52,v52,v115\n"
CV "v75,v114,v115\n"
MF "a[132:135],v[24:27],v[104:107],a[132:135]\n"
"v_exp_f32 v116,v116\n"
MF "a[128:131],v[24:27],v[100:103],a[128:131]\n"
"v_exp_f32 v117,v117\n"
MF "a[148:151],v[28:31],v[100:103],a[148:151]\n"
"v_exp_f32 v118,v118\n"
MF "a[144:147],v[28:31],v[96:99],a[144:147]\n"
"v_exp_f32 v119,v119\n"
MF "a[124:127],v[24:27],v[96:99],a[124:127]\n"
"v_add_f32 v53,v53,v116\n"
"v_add_f32 v53,v53,v117\n"
MF "a[120:123],v[24:27],v[92:95],a[120:123]\n"
CV "v78,v116,v117\n"
"v_add_f32 v53,v53,v118\n"
MF "a[140:143],v[28:31],v[92:95],a[140:143]\n"
"s_waitcnt lgkmcnt(0)\n"
TR "v[24:25],v4 offset:2048\n"
TR "v[26:27],v4 offset:8192\n"
MF "a[160:163],v[16:19],v[92:95],a[160:163]\n"
"s_nop 0\n"
TR "v[28:29],v5 offset:2048\n"
TR "v[30:31],v5 offset:8192\n"
MF "a[180:183],v[20:23],v[92:95],a[180:183]\n"
"v_add_f32 v53,v53,v119\n"
CV "v79,v118,v119\n"
MF "a[184:187],v[20:23],v[96:99],a[184:187]\n"
"v_exp_f32 v120,v120\n"
MF "a[164:167],v[16:19],v[96:99],a[164:167]\n"
"v_exp_f32 v121,v121\n"
MF "a[168:171],v[16:19],v[100:103],a[168:171]\n"
"v_exp_f32 v122,v122\n"
MF "a[188:191],v[20:23],v[100:103],a[188:191]\n"
"v_exp_f32 v123,v123\n"
MF "a[192:195],v[20:23],v[104:107],a[192:195]\n"
"v_add_f32 v54,v54,v120\n"
"v_add_f32 v54,v54,v121\n"
MF "a[172:175],v[16:19],v[104:107],a[172:175]\n"
"s_waitcnt vmcnt(6) lgkmcnt(0)\n"
CV "v82,v120,v121\n"
"v_add_f32 v54,v54,v122\n"
MF "a[176:179],v[16:19],v[108:111],a[176:179]\n"
"s_barrier\n"
MF "a[196:199],v[20:23],v[108:111],a[196:199]\n"
"s_add_u32 s72,s72,6144\n"
"ds_read_b128 v[16:19],v0 offset:24576\n"
"ds_read_b128 v[20:23],v1 offset:24576\n"
MF "a[216:219],v[24:27],v[108:111],a[216:219]\n"
"s_cmp_eq_u32 s72,s17\n"
"s_cselect_b32 s72,s16,s72\n"
"s_add_u32 m0,s64,0\n"
MF "a[236:239],v[28:31],v[108:111],a[236:239]\n"
LD "v7,s[32:35],s72 offen nt lds\n"
MF "a[232:235],v[28:31],v[104:107],a[232:235]\n"
"s_add_u32 m0,s64,896\n"
"v_add_f32 v54,v54,v123\n"
CV "v83,v122,v123\n"
MF "a[212:215],v[24:27],v[104:107],a[212:215]\n"
LD "v7,s[32:35],s72 offen offset:128 nt lds\n"
MF "a[208:211],v[24:27],v[100:103],a[208:211]\n"
"s_add_u32 m0,s64,1792\n"
MF "a[228:231],v[28:31],v[100:103],a[228:231]\n"
LD "v7,s[32:35],s72 offen offset:256 nt lds\n"
MF "a[224:227],v[28:31],v[96:99],a[224:227]\n"
MF "a[204:207],v[24:27],v[96:99],a[204:207]\n"
MF "a[200:203],v[24:27],v[92:95],a[200:203]\n"
MF "a[220:223],v[28:31],v[92:95],a[220:223]\n"
"s_waitcnt lgkmcnt(0)\n"
MF "v[92:95],v[16:19],v[136:139],v[32:35]\n"
"s_add_u32 s72,s72,6144\n"
"ds_read_b128 v[24:27],v0 offset:25600\n"
"ds_read_b128 v[28:31],v1 offset:25600\n"
MF "v[92:95],v[20:23],v[156:159],v[92:95]\n"
"s_add_u32 m0,s64,6144\n"
"v_exp_f32 v124,v124\n"
MF "v[96:99],v[20:23],v[160:163],v[36:39]\n"
LD "v7,s[32:35],s72 offen nt lds\n"
MF "v[96:99],v[16:19],v[140:143],v[96:99]\n"
"v_exp_f32 v125,v125\n"
MF "v[100:103],v[16:19],v[144:147],v[40:43]\n"
"s_add_u32 m0,s64,7040\n"
"v_exp_f32 v126,v126\n"
MF "v[100:103],v[20:23],v[164:167],v[100:103]\n"
LD "v7,s[32:35],s72 offen offset:128 nt lds\n"
MF "v[104:107],v[20:23],v[168:171],v[44:47]\n"
"v_exp_f32 v127,v127\n"
MF "v[104:107],v[16:19],v[148:151],v[104:107]\n"
"s_add_u32 m0,s64,7936\n"
"v_add_f32 v55,v55,v124\n"
"v_add_f32 v55,v55,v125\n"
MF "v[108:111],v[16:19],v[152:155],v[48:51]\n"
LD "v7,s[32:35],s72 offen offset:256 nt lds\n"
MF "v[108:111],v[20:23],v[172:175],v[108:111]\n"
"s_waitcnt lgkmcnt(0)\n"
"ds_read_b128 v[16:19],v0 offset:26624\n"
"ds_read_b128 v[20:23],v1 offset:26624\n"
MF "v[108:111],v[24:27],v[192:195],v[108:111]\n"
CV "v86,v124,v125\n"
"v_add_f32 v55,v55,v126\n"
MF "v[108:111],v[28:31],v[212:215],v[108:111]\n"
"v_add_f32 v55,v55,v127\n"
CV "v87,v126,v127\n"
MF "v[104:107],v[28:31],v[208:211],v[104:107]\n"
"v_exp_f32 v128,v128\n"
MF "v[104:107],v[24:27],v[188:191],v[104:107]\n"
"v_exp_f32 v129,v129\n"
MF "v[100:103],v[24:27],v[184:187],v[100:103]\n"
"v_exp_f32 v130,v130\n"
MF "v[100:103],v[28:31],v[204:207],v[100:103]\n"
"v_exp_f32 v131,v131\n"
MF "v[96:99],v[28:31],v[200:203],v[96:99]\n"
"v_add_f32 v56,v56,v128\n"
"v_add_f32 v56,v56,v129\n"
MF "v[96:99],v[24:27],v[180:183],v[96:99]\n"
CV "v90,v128,v129\n"
"v_add_f32 v56,v56,v130\n"
MF "v[92:95],v[24:27],v[176:179],v[92:95]\n"
"v_add_f32 v56,v56,v131\n"
CV "v91,v130,v131\n"
MF "v[92:95],v[28:31],v[196:199],v[92:95]\n"
"s_waitcnt lgkmcnt(0)\n"
"ds_read_b128 v[24:27],v0 offset:30720\n"
"ds_read_b128 v[28:31],v1 offset:30720\n"
MF "v[92:95],v[16:19],v[216:219],v[92:95]\n"
MF "v[92:95],v[20:23],v[236:239],v[92:95]\n"
MF "v[96:99],v[20:23],v[240:243],v[96:99]\n"
MF "v[96:99],v[16:19],v[220:223],v[96:99]\n"
MF "v[100:103],v[16:19],v[224:227],v[100:103]\n"
MF "v[100:103],v[20:23],v[244:247],v[100:103]\n"
MF "v[104:107],v[20:23],v[248:251],v[104:107]\n"
MF "v[104:107],v[16:19],v[228:231],v[104:107]\n"
"v_max3_f32 v10,v52,v53,v54\n"
"v_max3_f32 v10,v10,v55,v56\n"
MF "v[108:111],v[16:19],v[232:235],v[108:111]\n"
"v_cmp_ngt_f32 vcc,s77,v10\n"
MF "v[108:111],v[20:23],v[252:255],v[108:111]\n"
"s_waitcnt lgkmcnt(0)\n"
"s_cbranch_vccnz .Lstub_14\n"
".Lback_14:\n"
MF "v[112:115],v[24:27],v[136:139],v[32:35]\n"
"s_add_u32 s74,s74,12288\n"
"ds_read_b128 v[16:19],v0 offset:31744\n"
"ds_read_b128 v[20:23],v1 offset:31744\n"
MF "v[112:115],v[28:31],v[156:159],v[112:115]\n"
"s_cmp_eq_u32 s74,s20\n"
"s_cselect_b32 s74,s70,s74\n"
MF "v[116:119],v[28:31],v[160:163],v[36:39]\n"
"v_exp_f32 v92,v92\n"
MF "v[116:119],v[24:27],v[140:143],v[116:119]\n"
"v_exp_f32 v93,v93\n"
MF "v[120:123],v[24:27],v[144:147],v[40:43]\n"
"v_exp_f32 v94,v94\n"
MF "v[120:123],v[28:31],v[164:167],v[120:123]\n"
"v_exp_f32 v95,v95\n"
MF "v[124:127],v[28:31],v[168:171],v[44:47]\n"
"v_add_f32 v57,v52,v92\n"
"v_add_f32 v57,v57,v93\n"
MF "v[124:127],v[24:27],v[148:151],v[124:127]\n"
CV "v92,v92,v93\n"
"v_add_f32 v57,v57,v94\n"
MF "v[128:131],v[24:27],v[152:155],v[48:51]\n"
"v_add_f32 v57,v57,v95\n"
CV "v93,v94,v95\n"
MF "v[128:131],v[28:31],v[172:175],v[128:131]\n"
"s_waitcnt lgkmcnt(0)\n"
"ds_read_b128 v[24:27],v0 offset:32768\n"
"ds_read_b128 v[28:31],v1 offset:32768\n"
MF "v[128:131],v[16:19],v[192:195],v[128:131]\n"
"v_exp_f32 v96,v96\n"
MF "v[128:131],v[20:23],v[212:215],v[128:131]\n"
"v_exp_f32 v97,v97\n"
MF "v[124:127],v[20:23],v[208:211],v[124:127]\n"
"v_exp_f32 v98,v98\n"
MF "v[124:127],v[16:19],v[188:191],v[124:127]\n"
"v_exp_f32 v99,v99\n"
MF "v[120:123],v[16:19],v[184:187],v[120:123]\n"
"v_add_f32 v58,v53,v96\n"
"v_add_f32 v58,v58,v97\n"
MF "v[120:123],v[20:23],v[204:207],v[120:123]\n"
CV "v96,v96,v97\n"
"v_add_f32 v58,v58,v98\n"
MF "v[116:119],v[20:23],v[200:203],v[116:119]\n"
"s_waitcnt vmcnt(6) lgkmcnt(0)\n"
"v_add_f32 v58,v58,v99\n"
CV "v97,v98,v99\n"
MF "v[116:119],v[16:19],v[180:183],v[116:119]\n"
"s_barrier\n"
MF "v[112:115],v[16:19],v[176:179],v[112:115]\n"
"v_exp_f32 v100,v100\n"
MF "v[112:115],v[20:23],v[196:199],v[112:115]\n"
"s_nop 0\n"
TR "v[16:17],v2 offset:49152\n"
TR "v[18:19],v2 offset:55296\n"
MF "v[112:115],v[24:27],v[216:219],v[112:115]\n"
"s_add_u32 s73,s73,12288\n"
TR "v[20:21],v3 offset:49152\n"
TR "v[22:23],v3 offset:55296\n"
MF "v[112:115],v[28:31],v[236:239],v[112:115]\n"
"s_cmp_eq_u32 s73,s19\n"
"s_cselect_b32 s73,s18,s73\n"
"s_add_u32 m0,s65,24576\n"
MF "v[116:119],v[28:31],v[240:243],v[116:119]\n"
LD "v8,s[36:39],s73 offen nt lds\n"
MF "v[116:119],v[24:27],v[220:223],v[116:119]\n"
"s_add_u32 m0,s65,25472\n"
"v_exp_f32 v101,v101\n"
MF "v[120:123],v[24:27],v[224:227],v[120:123]\n"
LD "v8,s[36:39],s73 offen offset:128 nt lds\n"
MF "v[120:123],v[28:31],v[244:247],v[120:123]\n"
"v_exp_f32 v102,v102\n"
MF "v[124:127],v[28:31],v[248:251],v[124:127]\n"
"s_add_u32 m0,s65,26368\n"
"v_exp_f32 v103,v103\n"
MF "v[124:127],v[24:27],v[228:231],v[124:127]\n"
LD "v8,s[36:39],s73 offen offset:256 nt lds\n"
MF "v[128:131],v[24:27],v[232:235],v[128:131]\n"
"v_add_f32 v59,v54,v100\n"
"v_add_f32 v59,v59,v101\n"
MF "v[128:131],v[28:31],v[252:255],v[128:131]\n"
"s_waitcnt lgkmcnt(0)\n"
TR "v[24:25],v4 offset:49152\n"
TR "v[26:27],v4 offset:55296\n"
MF "a[0:3],v[16:19],v[72:75],a[0:3]\n"
"s_add_u32 m0,s65,24576\n"
TR "v[28:29],v5 offset:49152\n"
TR "v[30:31],v5 offset:55296\n"
MF "a[20:23],v[20:23],v[72:75],a[20:23]\n"
LD "v8,s[36:39],s73 offen offset:3072 nt lds\n"
MF "a[24:27],v[20:23],v[76:79],a[24:27]\n"
CV "v100,v100,v101\n"
"v_add_f32 v59,v59,v102\n"
MF "a[4:7],v[16:19],v[76:79],a[4:7]\n"
"s_add_u32 m0,s65,25472\n"
"v_add_f32 v59,v59,v103\n"
CV "v101,v102,v103\n"
MF "a[8:11],v[16:19],v[80:83],a[8:11]\n"
LD "v8,s[36:39],s73 offen offset:3200 nt lds\n"
MF "a[28:31],v[20:23],v[80:83],a[28:31]\n"
"v_exp_f32 v104,v104\n"
MF "a[32:35],v[20:23],v[84:87],a[32:35]\n"
"s_add_u32 m0,s65,26368\n"
"v_exp_f32 v105,v105\n"
MF "a[12:15],v[16:19],v[84:87],a[12:15]\n"
LD "v8,s[36:39],s73 offen offset:3328 nt lds\n"
MF "a[16:19],v[16:19],v[88:91],a[16:19]\n"
"v_exp_f32 v106,v106\n"
MF "a[36:39],v[20:23],v[88:91],a[36:39]\n"
"s_waitcnt lgkmcnt(0)\n"
TR "v[16:17],v2 offset:50176\n"
TR "v[18:19],v2 offset:56320\n"
MF "a[56:59],v[24:27],v[88:91],a[56:59]\n"
"s_nop 0\n"
TR "v[20:21],v3 offset:50176\n"
TR "v[22:23],v3 offset:56320\n"
MF "a[76:79],v[28:31],v[88:91],a[76:79]\n"
"v_exp_f32 v107,v107\n"
MF "a[72:75],v[28:31],v[84:87],a[72:75]\n"
"v_add_f32 v60,v55,v104\n"
"v_add_f32 v60,v60,v105\n"
MF "a[52:55],v[24:27],v[84:87],a[52:55]\n"
CV "v104,v104,v105\n"
"v_add_f32 v60,v60,v106\n"
MF "a[48:51],v[24:27],v[80:83],a[48:51]\n"
"v_add_f32 v60,v60,v107\n"
CV "v105,v106,v107\n"
MF "a[68:71],v[28:31],v[80:83],a[68:71]\n"
"v_exp_f32 v108,v108\n"
MF "a[64:67],v[28:31],v[76:79],a[64:67]\n"
"v_exp_f32 v109,v109\n"
MF "a[44:47],v[24:27],v[76:79],a[44:47]\n"
"v_exp_f32 v110,v110\n"
MF "a[40:43],v[24:27],v[72:75],a[40:43]\n"
"v_exp_f32 v111,v111\n"
MF "a[60:63],v[28:31],v[72:75],a[60:63]\n"
"s_waitcnt lgkmcnt(0)\n"
TR "v[24:25],v4 offset:50176\n"
TR "v[26:27],v4 offset:56320\n"
MF "a[80:83],v[16:19],v[72:75],a[80:83]\n"
"s_nop 0\n"
TR "v[28:29],v5 offset:50176\n"
TR "v[30:31],v5 offset:56320\n"
MF "a[100:103],v[20:23],v[72:75],a[100:103]\n"
"v_add_f32 v61,v56,v108\n"
"v_add_f32 v61,v61,v109\n"
MF "a[104:107],v[20:23],v[76:79],a[104:107]\n"
CV "v108,v108,v109\n"
"v_add_f32 v61,v61,v110\n"
MF "a[84:87],v[16:19],v[76:79],a[84:87]\n"
"v_add_f32 v61,v61,v111\n"
CV "v109,v110,v111\n"
MF "a[88:91],v[16:19],v[80:83],a[88:91]\n"
"v_exp_f32 v112,v112\n"
MF "a[108:111],v[20:23],v[80:83],a[108:111]\n"
"v_exp_f32 v113,v113\n"
MF "a[112:115],v[20:23],v[84:87],a[112:115]\n"
"v_exp_f32 v114,v114\n"
MF "a[92:95],v[16:19],v[84:87],a[92:95]\n"
"v_exp_f32 v115,v115\n"
MF "a[96:99],v[16:19],v[88:91],a[96:99]\n"
"v_add_f32 v57,v57,v112\n"
"v_add_f32 v57,v57,v113\n"
MF "a[116:119],v[20:23],v[88:91],a[116:119]\n"
"s_waitcnt lgkmcnt(0)\n"
TR "v[16:17],v2 offset:51200\n"
TR "v[18:19],v2 offset:57344\n"
MF "a[136:139],v[24:27],v[88:91],a[136:139]\n"
"s_nop 0\n"
TR "v[20:21],v3 offset:51200\n"
TR "v[22:23],v3 offset:57344\n"
MF "a[156:159],v[28:31],v[88:91],a[156:159]\n"
CV "v94,v112,v113\n"
"v_add_f32 v57,v57,v114\n"
MF "a[152:155],v[28:31],v[84:87],a[152:155]\n"
"v_add_f32 v57,v57,v115\n"
CV "v95,v114,v115\n"
MF "a[132:135],v[24:27],v[84:87],a[132:135]\n"
"v_exp_f32 v116,v116\n"
MF "a[128:131],v[24:27],v[80:83],a[128:131]\n"
"v_exp_f32 v117,v117\n"
MF "a[148:151],v[28:31],v[80:83],a[148:151]\n"
"v_exp_f32 v118,v118\n"
MF "a[144:147],v[28:31],v[76:79],a[144:147]\n"
"v_exp_f32 v119,v119\n"
MF "a[124:127],v[24:27],v[76:79],a[124:127]\n"
"v_add_f32 v58,v58,v116\n"
"v_add_f32 v58,v58,v117\n"
MF "a[120:123],v[24:27],v[72:75],a[120:123]\n"
CV "v98,v116,v117\n"
"v_add_f32 v58,v58,v118\n"
MF "a[140:143],v[28:31],v[72:75],a[140:143]\n"
"s_waitcnt lgkmcnt(0)\n"
TR "v[24:25],v4 offset:51200\n"
TR "v[26:27],v4 offset:57344\n"
MF "a[160:163],v[16:19],v[72:75],a[160:163]\n"
"s_nop 0\n"
TR "v[28:29],v5 offset:51200\n"
TR "v[30:31],v5 offset:57344\n"
MF "a[180:183],v[20:23],v[72:75],a[180:183]\n"
"v_add_f32 v58,v58,v119\n"
CV "v99,v118,v119\n"
MF "a[184:187],v[20:23],v[76:79],a[184:187]\n"
"v_exp_f32 v120,v120\n"
MF "a[164:167],v[16:19],v[76:79],a[164:167]\n"
"v_exp_f32 v121,v121\n"
MF "a[168:171],v[16:19],v[80:83],a[168:171]\n"
"v_exp_f32 v122,v122\n"
MF "a[188:191],v[20:23],v[80:83],a[188:191]\n"
"v_exp_f32 v123,v123\n"
MF "a[192:195],v[20:23],v[84:87],a[192:195]\n"
"v_add_f32 v59,v59,v120\n"
"v_add_f32 v59,v59,v121\n"
MF "a[172:175],v[16:19],v[84:87],a[172:175]\n"
"s_waitcnt vmcnt(6) lgkmcnt(0)\n"
CV "v102,v120,v121\n"
"v_add_f32 v59,v59,v122\n"
MF "a[176:179],v[16:19],v[88:91],a[176:179]\n"
"s_barrier\n"
MF "a[196:199],v[20:23],v[88:91],a[196:199]\n"
"s_add_u32 s72,s72,6144\n"
"ds_read_b128 v[16:19],v0\n"
"ds_read_b128 v[20:23],v1\n"
MF "a[216:219],v[24:27],v[88:91],a[216:219]\n"
"s_cmp_eq_u32 s72,s17\n"
"s_cselect_b32 s72,s16,s72\n"
"s_add_u32 m0,s64,49152\n"
MF "a[236:239],v[28:31],v[88:91],a[236:239]\n"
LD "v7,s[32:35],s72 offen nt lds\n"
MF "a[232:235],v[28:31],v[84:87],a[232:235]\n"
"s_add_u32 m0,s64,50048\n"
"v_add_f32 v59,v59,v123\n"
CV "v103,v122,v123\n"
MF "a[212:215],v[24:27],v[84:87],a[212:215]\n"
LD "v7,s[32:35],s72 offen offset:128 nt lds\n"
MF "a[208:211],v[24:27],v[80:83],a[208:211]\n"
"s_add_u32 m0,s64,50944\n"
MF "a[228:231],v[28:31],v[80:83],a[228:231]\n"
LD "v7,s[32:35],s72 offen offset:256 nt lds\n"
MF "a[224:227],v[28:31],v[76:79],a[224:227]\n"
MF "a[204:207],v[24:27],v[76:79],a[204:207]\n"
MF "a[200:203],v[24:27],v[72:75],a[200:203]\n"
MF "a[220:223],v[28:31],v[72:75],a[220:223]\n"
"s_waitcnt lgkmcnt(0)\n"
".Lmid:\n"
MF "v[72:75],v[16:19],v[136:139],v[32:35]\n"
"s_add_u32 s72,s72,6144\n"
"ds_read_b128 v[24:27],v0 offset:1024\n"
"ds_read_b128 v[28:31],v1 offset:1024\n"
MF "v[72:75],v[20:23],v[156:159],v[72:75]\n"
"s_add_u32 m0,s64,55296\n"
"v_exp_f32 v124,v124\n"
MF "v[76:79],v[20:23],v[160:163],v[36:39]\n"
LD "v7,s[32:35],s72 offen nt lds\n"
MF "v[76:79],v[16:19],v[140:143],v[76:79]\n"
"v_exp_f32 v125,v125\n"
MF "v[80:83],v[16:19],v[144:147],v[40:43]\n"
"s_add_u32 m0,s64,56192\n"
"v_exp_f32 v126,v126\n"
MF "v[80:83],v[20:23],v[164:167],v[80:83]\n"
LD "v7,s[32:35],s72 offen offset:128 nt lds\n"
MF "v[84:87],v[20:23],v[168:171],v[44:47]\n"
"v_exp_f32 v127,v127\n"
MF "v[84:87],v[16:19],v[148:151],v[84:87]\n"
"s_add_u32 m0,s64,57088\n"
"v_add_f32 v60,v60,v124\n"
"v_add_f32 v60,v60,v125\n"
MF "v[88:91],v[16:19],v[152:155],v[48:51]\n"
LD "v7,s[32:35],s72 offen offset:256 nt lds\n"
MF "v[88:91],v[20:23],v[172:175],v[88:91]\n"
"s_waitcnt lgkmcnt(0)\n"
"ds_read_b128 v[16:19],v0 offset:2048\n"
"ds_read_b128 v[20:23],v1 offset:2048\n"
MF "v[88:91],v[24:27],v[192:195],v[88:91]\n"
CV "v106,v124,v125\n"
"v_add_f32 v60,v60,v126\n"
MF "v[88:91],v[28:31],v[212:215],v[88:91]\n"
"v_add_f32 v60,v60,v127\n"
CV "v107,v126,v127\n"
MF "v[84:87],v[28:31],v[208:211],v[84:87]\n"
"v_exp_f32 v128,v128\n"
MF "v[84:87],v[24:27],v[188:191],v[84:87]\n"
"v_exp_f32 v129,v129\n"
MF "v[80:83],v[24:27],v[184:187],v[80:83]\n"
"v_exp_f32 v130,v130\n"
MF "v[80:83],v[28:31],v[204:207],v[80:83]\n"
"v_exp_f32 v131,v131\n"
MF "v[76:79],v[28:31],v[200:203],v[76:79]\n"
"v_add_f32 v61,v61,v128\n"
"v_add_f32 v61,v61,v129\n"
MF "v[76:79],v[24:27],v[180:183],v[76:79]\n"
CV "v110,v128,v129\n"
"v_add_f32 v61,v61,v130\n"
MF "v[72:75],v[24:27],v[176:179],v[72:75]\n"
"v_add_f32 v61,v61,v131\n"
CV "v111,v130,v131\n"
MF "v[72:75],v[28:31],v[196:199],v[72:75]\n"
"s_waitcnt lgkmcnt(0)\n"
"ds_read_b128 v[24:27],v0 offset:6144\n"
"ds_read_b128 v[28:31],v1 offset:6144\n"
MF "v[72:75],v[16:19],v[216:219],v[72:75]\n"
MF "v[72:75],v[20:23],v[236:239],v[72:75]\n"
MF "v[76:79],v[20:23],v[240:243],v[76:79]\n"
MF "v[76:79],v[16:19],v[220:223],v[76:79]\n"
MF "v[80:83],v[16:19],v[224:227],v[80:83]\n"
MF "v[80:83],v[20:23],v[244:247],v[80:83]\n"
MF "v[84:87],v[20:23],v[248:251],v[84:87]\n"
MF "v[84:87],v[16:19],v[228:231],v[84:87]\n"
"v_max3_f32 v10,v57,v58,v59\n"
"v_max3_f32 v10,v10,v60,v61\n"
MF "v[88:91],v[16:19],v[232:235],v[88:91]\n"
"v_cmp_ngt_f32 vcc,s95,v10\n"
MF "v[88:91],v[20:23],v[252:255],v[88:91]\n"
"s_waitcnt lgkmcnt(0)\n"
"s_cbranch_vccnz .Lstub_15\n"
".Lback_15:\n"
MF "v[112:115],v[24:27],v[136:139],v[32:35]\n"
"s_add_u32 s74,s74,12288\n"
"ds_read_b128 v[16:19],v0 offset:7168\n"
"ds_read_b128 v[20:23],v1 offset:7168\n"
MF "v[112:115],v[28:31],v[156:159],v[112:115]\n"
"s_cmp_eq_u32 s74,s20\n"
"s_cselect_b32 s74,s70,s74\n"
MF "v[116:119],v[28:31],v[160:163],v[36:39]\n"
"v_exp_f32 v72,v72\n"
MF "v[116:119],v[24:27],v[140:143],v[116:119]\n"
"v_exp_f32 v73,v73\n"
MF "v[120:123],v[24:27],v[144:147],v[40:43]\n"
"v_exp_f32 v74,v74\n"
MF "v[120:123],v[28:31],v[164:167],v[120:123]\n"
"v_exp_f32 v75,v75\n"
MF "v[124:127],v[28:31],v[168:171],v[44:47]\n"
"v_add_f32 v52,v57,v72\n"
"v_add_f32 v52,v52,v73\n"
MF "v[124:127],v[24:27],v[148:151],v[124:127]\n"
CV "v72,v72,v73\n"
"v_add_f32 v52,v52,v74\n"
MF "v[128:131],v[24:27],v[152:155],v[48:51]\n"
"v_add_f32 v52,v52,v75\n"
CV "v73,v74,v75\n"
MF "v[128:131],v[28:31],v[172:175],v[128:131]\n"
"s_waitcnt lgkmcnt(0)\n"
"ds_read_b128 v[24:27],v0 offset:8192\n"
"ds_read_b128 v[28:31],v1 offset:8192\n"
MF "v[128:131],v[16:19],v[192:195],v[128:131]\n"
"v_exp_f32 v76,v76\n"
MF "v[128:131],v[20:23],v[212:215],v[128:131]\n"
"v_exp_f32 v77,v77\n"
MF "v[124:127],v[20:23],v[208:211],v[124:127]\n"
"v_exp_f32 v78,v78\n"
MF "v[124:127],v[16:19],v[188:191],v[124:127]\n"
"v_exp_f32 v79,v79\n"
MF "v[120:123],v[16:19],v[184:187],v[120:123]\n"
"v_add_f32 v53,v58,v76\n"
"v_add_f32 v53,v53,v77\n"
MF "v[120:123],v[20:23],v[204:207],v[120:123]\n"
CV "v76,v76,v77\n"
"v_add_f32 v53,v53,v78\n"
MF "v[116:119],v[20:23],v[200:203],v[116:119]\n"
"s_waitcnt vmcnt(6) lgkmcnt(0)\n"
"v_add_f32 v53,v53,v79\n"
CV "v77,v78,v79\n"
MF "v[116:119],v[16:19],v[180:183],v[116:119]\n"
"s_barrier\n"
MF "v[112:115],v[16:19],v[176:179],v[112:115]\n"
"v_exp_f32 v80,v80\n"
MF "v[112:115],v[20:23],v[196:199],v[112:115]\n"
"s_nop 0\n"
TR "v[16:17],v2 offset:24576\n"
TR "v[18:19],v2 offset:30720\n"
MF "v[112:115],v[24:27],v[216:219],v[112:115]\n"
"s_add_u32 s73,s73,12288\n"
TR "v[20:21],v3 offset:24576\n"
TR "v[22:23],v3 offset:30720\n"
MF "v[112:115],v[28:31],v[236:239],v[112:115]\n"
"s_cmp_eq_u32 s73,s19\n"
"s_cselect_b32 s73,s18,s73\n"
"s_add_u32 m0,s65,0\n"
MF "v[116:119],v[28:31],v[240:243],v[116:119]\n"
LD "v8,s[36:39],s73 offen nt lds\n"
MF "v[116:119],v[24:27],v[220:223],v[116:119]\n"
"s_add_u32 m0,s65,896\n"
"v_exp_f32 v81,v81\n"
MF "v[120:123],v[24:27],v[224:227],v[120:123]\n"
LD "v8,s[36:39],s73 offen offset:128 nt lds\n"
MF "v[120:123],v[28:31],v[244:247],v[120:123]\n"
"v_exp_f32 v82,v82\n"
MF "v[124:127],v[28:31],v[248:251],v[124:127]\n"
"s_add_u32 m0,s65,1792\n"
"v_exp_f32 v83,v83\n"
MF "v[124:127],v[24:27],v[228:231],v[124:127]\n"
LD "v8,s[36:39],s73 offen offset:256 nt lds\n"
MF "v[128:131],v[24:27],v[232:235],v[128:131]\n"
"v_add_f32 v54,v59,v80\n"
"v_add_f32 v54,v54,v81\n"
MF "v[128:131],v[28:31],v[252:255],v[128:131]\n"
"s_waitcnt lgkmcnt(0)\n"
TR "v[24:25],v4 offset:24576\n"
TR "v[26:27],v4 offset:30720\n"
MF "a[0:3],v[16:19],v[92:95],a[0:3]\n"
"s_add_u32 m0,s65,0\n"
TR "v[28:29],v5 offset:24576\n"
TR "v[30:31],v5 offset:30720\n"
MF "a[20:23],v[20:23],v[92:95],a[20:23]\n"
LD "v8,s[36:39],s73 offen offset:3072 nt lds\n"
MF "a[24:27],v[20:23],v[96:99],a[24:27]\n"
CV "v80,v80,v81\n"
"v_add_f32 v54,v54,v82\n"
MF "a[4:7],v[16:19],v[96:99],a[4:7]\n"
"s_add_u32 m0,s65,896\n"
"v_add_f32 v54,v54,v83\n"
CV "v81,v82,v83\n"
MF "a[8:11],v[16:19],v[100:103],a[8:11]\n"
LD "v8,s[36:39],s73 offen offset:3200 nt lds\n"
MF "a[28:31],v[20:23],v[100:103],a[28:31]\n"
"v_exp_f32 v84,v84\n"
MF "a[32:35],v[20:23],v[104:107],a[32:35]\n"
"s_add_u32 m0,s65,1792\n"
"v_exp_f32 v85,v85\n"
MF "a[12:15],v[16:19],v[104:107],a[12:15]\n"
LD "v8,s[36:39],s73 offen offset:3328 nt lds\n"
MF "a[16:19],v[16:19],v[108:111],a[16:19]\n"
"v_exp_f32 v86,v86\n"
MF "a[36:39],v[20:23],v[108:111],a[36:39]\n"
"s_waitcnt lgkmcnt(0)\n"
TR "v[16:17],v2 offset:25600\n"
TR "v[18:19],v2 offset:31744\n"
MF "a[56:59],v[24:27],v[108:111],a[56:59]\n"
"s_nop 0\n"
TR "v[20:21],v3 offset:25600\n"
TR "v[22:23],v3 offset:31744\n"
MF "a[76:79],v[28:31],v[108:111],a[76:79]\n"
"v_exp_f32 v87,v87\n"
MF "a[72:75],v[28:31],v[104:107],a[72:75]\n"
"v_add_f32 v55,v60,v84\n"
"v_add_f32 v55,v55,v85\n"
MF "a[52:55],v[24:27],v[104:107],a[52:55]\n"
CV "v84,v84,v85\n"
"v_add_f32 v55,v55,v86\n"
MF "a[48:51],v[24:27],v[100:103],a[48:51]\n"
"v_add_f32 v55,v55,v87\n"
CV "v85,v86,v87\n"
MF "a[68:71],v[28:31],v[100:103],a[68:71]\n"
"v_exp_f32 v88,v88\n"
MF "a[64:67],v[28:31],v[96:99],a[64:67]\n"
"v_exp_f32 v89,v89\n"
MF "a[44:47],v[24:27],v[96:99],a[44:47]\n"
"v_exp_f32 v90,v90\n"
MF "a[40:43],v[24:27],v[92:95],a[40:43]\n"
"v_exp_f32 v91,v91\n"
MF "a[60:63],v[28:31],v[92:95],a[60:63]\n"
"s_waitcnt lgkmcnt(0)\n"
TR "v[24:25],v4 offset:25600\n"
TR "v[26:27],v4 offset:31744\n"
MF "a[80:83],v[16:19],v[92:95],a[80:83]\n"
"s_nop 0\n"
TR "v[28:29],v5 offset:25600\n"
TR "v[30:31],v5 offset:31744\n"
MF "a[100:103],v[20:23],v[92:95],a[100:103]\n"
"v_add_f32 v56,v61,v88\n"
"v_add_f32 v56,v56,v89\n"
MF "a[104:107],v[20:23],v[96:99],a[104:107]\n"
CV "v88,v88,v89\n"
"v_add_f32 v56,v56,v90\n"
MF "a[84:87],v[16:19],v[96:99],a[84:87]\n"
"v_add_f32 v56,v56,v91\n"
CV "v89,v90,v91\n"
MF "a[88:91],v[16:19],v[100:103],a[88:91]\n"
"v_exp_f32 v112,v112\n"
MF "a[108:111],v[20:23],v[100:103],a[108:111]\n"
"v_exp_f32 v113,v113\n"
MF "a[112:115],v[20:23],v[104:107],a[112:115]\n"
"v_exp_f32 v114,v114\n"
MF "a[92:95],v[16:19],v[104:107],a[92:95]\n"
"v_exp_f32 v115,v115\n"
MF "a[96:99],v[16:19],v[108:111],a[96:99]\n"
"v_add_f32 v52,v52,v112\n"
"v_add_f32 v52,v52,v113\n"
MF "a[116:119],v[20:23],v[108:111],a[116:119]\n"
"s_waitcnt lgkmcnt(0)\n"
TR "v[16:17],v2 offset:26624\n"
TR "v[18:19],v2 offset:32768\n"
MF "a[136:139],v[24:27],v[108:111],a[136:139]\n"
"s_nop 0\n"
TR "v[20:21],v3 offset:26624\n"
TR "v[22:23],v3 offset:32768\n"
MF "a[156:159],v[28:31],v[108:111],a[156:159]\n"
CV "v74,v112,v113\n"
"v_add_f32 v52,v52,v114\n"
MF "a[152:155],v[28:31],v[104:107],a[152:155]\n"
"v_add_f32 v52,v52,v115\n"
CV "v75,v114,v115\n"
MF "a[132:135],v[24:27],v[104:107],a[132:135]\n"
"v_exp_f32 v116,v116\n"
MF "a[128:131],v[24:27],v[100:103],a[128:131]\n"
"v_exp_f32 v117,v117\n"
MF "a[148:151],v[28:31],v[100:103],a[148:151]\n"
"v_exp_f32 v118,v118\n"
MF "a[144:147],v[28:31],v[96:99],a[144:147]\n"
"v_exp_f32 v119,v119\n"
MF "a[124:127],v[24:27],v[96:99],a[124:127]\n"
"v_add_f32 v53,v53,v116\n"
"v_add_f32 v53,v53,v117\n"
MF "a[120:123],v[24:27],v[92:95],a[120:123]\n"
CV "v78,v116,v117\n"
"v_add_f32 v53,v53,v118\n"
MF "a[140:143],v[28:31],v[92:95],a[140:143]\n"
"s_waitcnt lgkmcnt(0)\n"
TR "v[24:25],v4 offset:26624\n"
TR "v[26:27],v4 offset:32768\n"
MF "a[160:163],v[16:19],v[92:95],a[160:163]\n"
"s_nop 0\n"
TR "v[28:29],v5 offset:26624\n"
TR "v[30:31],v5 offset:32768\n"
MF "a[180:183],v[20:23],v[92:95],a[180:183]\n"
"v_add_f32 v53,v53,v119\n"
CV "v79,v118,v119\n"
MF "a[184:187],v[20:23],v[96:99],a[184:187]\n"
"v_exp_f32 v120,v120\n"
MF "a[164:167],v[16:19],v[96:99],a[164:167]\n"
"v_exp_f32 v121,v121\n"
MF "a[168:171],v[16:19],v[100:103],a[168:171]\n"
"v_exp_f32 v122,v122\n"
MF "a[188:191],v[20:23],v[100:103],a[188:191]\n"
"v_exp_f32 v123,v123\n"
MF "a[192:195],v[20:23],v[104:107],a[192:195]\n"
"v_add_f32 v54,v54,v120\n"
"v_add_f32 v54,v54,v121\n"
MF "a[172:175],v[16:19],v[104:107],a[172:175]\n"
"s_waitcnt vmcnt(6) lgkmcnt(0)\n"
CV "v82,v120,v121\n"
"v_add_f32 v54,v54,v122\n"
MF "a[176:179],v[16:19],v[108:111],a[176:179]\n"
"s_barrier\n"
MF "a[196:199],v[20:23],v[108:111],a[196:199]\n"
"s_add_u32 s72,s72,6144\n"
"ds_read_b128 v[16:19],v0 offset:49152\n"
"ds_read_b128 v[20:23],v1 offset:49152\n"
MF "a[216:219],v[24:27],v[108:111],a[216:219]\n"
"s_cmp_eq_u32 s72,s17\n"
"s_cselect_b32 s72,s16,s72\n"
"s_add_u32 m0,s64,24576\n"
MF "a[236:239],v[28:31],v[108:111],a[236:239]\n"
LD "v7,s[32:35],s72 offen nt lds\n"
MF "a[232:235],v[28:31],v[104:107],a[232:235]\n"
"s_add_u32 m0,s64,25472\n"
"v_add_f32 v54,v54,v123\n"
CV "v83,v122,v123\n"
MF "a[212:215],v[24:27],v[104:107],a[212:215]\n"
LD "v7,s[32:35],s72 offen offset:128 nt lds\n"
MF "a[208:211],v[24:27],v[100:103],a[208:211]\n"
"s_add_u32 m0,s64,26368\n"
MF "a[228:231],v[28:31],v[100:103],a[228:231]\n"
LD "v7,s[32:35],s72 offen offset:256 nt lds\n"
MF "a[224:227],v[28:31],v[96:99],a[224:227]\n"
MF "a[204:207],v[24:27],v[96:99],a[204:207]\n"
MF "a[200:203],v[24:27],v[92:95],a[200:203]\n"
MF "a[220:223],v[28:31],v[92:95],a[220:223]\n"
"s_waitcnt lgkmcnt(0)\n"
MF "v[92:95],v[16:19],v[136:139],v[32:35]\n"
"s_add_u32 s72,s72,6144\n"
"ds_read_b128 v[24:27],v0 offset:50176\n"
"ds_read_b128 v[28:31],v1 offset:50176\n"
MF "v[92:95],v[20:23],v[156:159],v[92:95]\n"
"s_add_u32 m0,s64,30720\n"
"v_exp_f32 v124,v124\n"
MF "v[96:99],v[20:23],v[160:163],v[36:39]\n"
LD "v7,s[32:35],s72 offen nt lds\n"
MF "v[96:99],v[16:19],v[140:143],v[96:99]\n"
"v_exp_f32 v125,v125\n"
MF "v[100:103],v[16:19],v[144:147],v[40:43]\n"
"s_add_u32 m0,s64,31616\n"
"v_exp_f32 v126,v126\n"
MF "v[100:103],v[20:23],v[164:167],v[100:103]\n"
LD "v7,s[32:35],s72 offen offset:128 nt lds\n"
MF "v[104:107],v[20:23],v[168:171],v[44:47]\n"
"v_exp_f32 v127,v127\n"
MF "v[104:107],v[16:19],v[148:151],v[104:107]\n"
"s_add_u32 m0,s64,32512\n"
"v_add_f32 v55,v55,v124\n"
"v_add_f32 v55,v55,v125\n"
MF "v[108:111],v[16:19],v[152:155],v[48:51]\n"
LD "v7,s[32:35],s72 offen offset:256 nt lds\n"
MF "v[108:111],v[20:23],v[172:175],v[108:111]\n"
"s_waitcnt lgkmcnt(0)\n"
"ds_read_b128 v[16:19],v0 offset:51200\n"
"ds_read_b128 v[20:23],v1 offset:51200\n"
MF "v[108:111],v[24:27],v[192:195],v[108:111]\n"
CV "v86,v124,v125\n"
"v_add_f32 v55,v55,v126\n"
MF "v[108:111],v[28:31],v[212:215],v[108:111]\n"
"v_add_f32 v55,v55,v127\n"
CV "v87,v126,v127\n"
MF "v[104:107],v[28:31],v[208:211],v[104:107]\n"
"v_exp_f32 v128,v128\n"
MF "v[104:107],v[24:27],v[188:191],v[104:107]\n"
"v_exp_f32 v129,v129\n"
MF "v[100:103],v[24:27],v[184:187],v[100:103]\n"
"v_exp_f32 v130,v130\n"
MF "v[100:103],v[28:31],v[204:207],v[100:103]\n"
"v_exp_f32 v131,v131\n"
MF "v[96:99],v[28:31],v[200:203],v[96:99]\n"
"v_add_f32 v56,v56,v128\n"
"v_add_f32 v56,v56,v129\n"
MF "v[96:99],v[24:27],v[180:183],v[96:99]\n"
CV "v90,v128,v129\n"
"v_add_f32 v56,v56,v130\n"
MF "v[92:95],v[24:27],v[176:179],v[92:95]\n"
"v_add_f32 v56,v56,v131\n"
CV "v91,v130,v131\n"
MF "v[92:95],v[28:31],v[196:199],v[92:95]\n"
"s_waitcnt lgkmcnt(0)\n"
"ds_read_b128 v[24:27],v0 offset:55296\n"
"ds_read_b128 v[28:31],v1 offset:55296\n"
MF "v[92:95],v[16:19],v[216:219],v[92:95]\n"
MF "v[92:95],v[20:23],v[236:239],v[92:95]\n"
MF "v[96:99],v[20:23],v[240:243],v[96:99]\n"
MF "v[96:99],v[16:19],v[220:223],v[96:99]\n"
MF "v[100:103],v[16:19],v[224:227],v[100:103]\n"
MF "v[100:103],v[20:23],v[244:247],v[100:103]\n"
MF "v[104:107],v[20:23],v[248:251],v[104:107]\n"
MF "v[104:107],v[16:19],v[228:231],v[104:107]\n"
"v_max3_f32 v10,v52,v53,v54\n"
"v_max3_f32 v10,v10,v55,v56\n"
MF "v[108:111],v[16:19],v[232:235],v[108:111]\n"
"v_cmp_ngt_f32 vcc,s77,v10\n"
MF "v[108:111],v[20:23],v[252:255],v[108:111]\n"
"s_waitcnt lgkmcnt(0)\n"
"s_cbranch_vccnz .Lstub_16\n"
".Lback_16:\n"
MF "v[112:115],v[24:27],v[136:139],v[32:35]\n"
"s_add_u32 s74,s74,12288\n"
"ds_read_b128 v[16:19],v0 offset:56320\n"
"ds_read_b128 v[20:23],v1 offset:56320\n"
MF "v[112:115],v[28:31],v[156:159],v[112:115]\n"
"s_cmp_eq_u32 s74,s20\n"
"s_cselect_b32 s74,s70,s74\n"
MF "v[116:119],v[28:31],v[160:163],v[36:39]\n"
"v_exp_f32 v92,v92\n"
MF "v[116:119],v[24:27],v[140:143],v[116:119]\n"
"v_exp_f32 v93,v93\n"
MF "v[120:123],v[24:27],v[144:147],v[40:43]\n"
"v_exp_f32 v94,v94\n"
MF "v[120:123],v[28:31],v[164:167],v[120:123]\n"
"v_exp_f32 v95,v95\n"
MF "v[124:127],v[28:31],v[168:171],v[44:47]\n"
"v_add_f32 v57,v52,v92\n"
"v_add_f32 v57,v57,v93\n"
MF "v[124:127],v[24:27],v[148:151],v[124:127]\n"
CV "v92,v92,v93\n"
"v_add_f32 v57,v57,v94\n"
MF "v[128:131],v[24:27],v[152:155],v[48:51]\n"
"v_add_f32 v57,v57,v95\n"
CV "v93,v94,v95\n"
MF "v[128:131],v[28:31],v[172:175],v[128:131]\n"
"s_waitcnt lgkmcnt(0)\n"
"ds_read_b128 v[24:27],v0 offset:57344\n"
"ds_read_b128 v[28:31],v1 offset:57344\n"
MF "v[128:131],v[16:19],v[192:195],v[128:131]\n"
"s_sub_u32 s61,s61,1\n"
"v_exp_f32 v96,v96\n"
MF "v[128:131],v[20:23],v[212:215],v[128:131]\n"
"s_cmp_eq_u32 s61,1\n"
"s_cselect_b32 s95,0,s95\n"
"v_add_f32 v58,v53,v96\n"
MF "v[124:127],v[20:23],v[208:211],v[124:127]\n"
"v_exp_f32 v97,v97\n"
MF "v[124:127],v[16:19],v[188:191],v[124:127]\n"
"v_exp_f32 v98,v98\n"
MF "v[120:123],v[16:19],v[184:187],v[120:123]\n"
"v_exp_f32 v99,v99\n"
MF "v[120:123],v[20:23],v[204:207],v[120:123]\n"
"v_add_f32 v58,v58,v97\n"
CV "v96,v96,v97\n"
MF "v[116:119],v[20:23],v[200:203],v[116:119]\n"
"s_waitcnt vmcnt(6) lgkmcnt(0)\n"
"v_add_f32 v58,v58,v98\n"
"v_add_f32 v58,v58,v99\n"
MF "v[116:119],v[16:19],v[180:183],v[116:119]\n"
"s_barrier\n"
MF "v[112:115],v[16:19],v[176:179],v[112:115]\n"
CV "v97,v98,v99\n"
MF "v[112:115],v[20:23],v[196:199],v[112:115]\n"
"s_nop 0\n"
TR "v[16:17],v2\n"
TR "v[18:19],v2 offset:6144\n"
MF "v[112:115],v[24:27],v[216:219],v[112:115]\n"
"s_add_u32 s73,s73,12288\n"
TR "v[20:21],v3\n"
TR "v[22:23],v3 offset:6144\n"
MF "v[112:115],v[28:31],v[236:239],v[112:115]\n"
"s_cmp_eq_u32 s73,s19\n"
"s_cselect_b32 s73,s18,s73\n"
"s_add_u32 m0,s65,49152\n"
MF "v[116:119],v[28:31],v[240:243],v[116:119]\n"
LD "v8,s[36:39],s73 offen nt lds\n"
MF "v[116:119],v[24:27],v[220:223],v[116:119]\n"
"s_add_u32 m0,s65,50048\n"
"v_exp_f32 v100,v100\n"
MF "v[120:123],v[24:27],v[224:227],v[120:123]\n"
LD "v8,s[36:39],s73 offen offset:128 nt lds\n"
MF "v[120:123],v[28:31],v[244:247],v[120:123]\n"
"v_exp_f32 v101,v101\n"
MF "v[124:127],v[28:31],v[248:251],v[124:127]\n"
"s_add_u32 m0,s65,50944\n"
"v_exp_f32 v102,v102\n"
MF "v[124:127],v[24:27],v[228:231],v[124:127]\n"
LD "v8,s[36:39],s73 offen offset:256 nt lds\n"
MF "v[128:131],v[24:27],v[232:235],v[128:131]\n"
"v_exp_f32 v103,v103\n"
MF "v[128:131],v[28:31],v[252:255],v[128:131]\n"
"s_waitcnt lgkmcnt(0)\n"
TR "v[24:25],v4\n"
TR "v[26:27],v4 offset:6144\n"
MF "a[0:3],v[16:19],v[72:75],a[0:3]\n"
"s_add_u32 m0,s65,49152\n"
TR "v[28:29],v5\n"
TR "v[30:31],v5 offset:6144\n"
MF "a[20:23],v[20:23],v[72:75],a[20:23]\n"
LD "v8,s[36:39],s73 offen offset:3072 nt lds\n"
MF "a[24:27],v[20:23],v[76:79],a[24:27]\n"
"v_add_f32 v59,v54,v100\n"
"v_add_f32 v59,v59,v101\n"
MF "a[4:7],v[16:19],v[76:79],a[4:7]\n"
"s_add_u32 m0,s65,50048\n"
CV "v100,v100,v101\n"
"v_add_f32 v59,v59,v102\n"
MF "a[8:11],v[16:19],v[80:83],a[8:11]\n"
LD "v8,s[36:39],s73 offen offset:3200 nt lds\n"
MF "a[28:31],v[20:23],v[80:83],a[28:31]\n"
"v_add_f32 v59,v59,v103\n"
CV "v101,v102,v103\n"
MF "a[32:35],v[20:23],v[84:87],a[32:35]\n"
"s_add_u32 m0,s65,50944\n"
"v_exp_f32 v104,v104\n"
MF "a[12:15],v[16:19],v[84:87],a[12:15]\n"
LD "v8,s[36:39],s73 offen offset:3328 nt lds\n"
MF "a[16:19],v[16:19],v[88:91],a[16:19]\n"
"v_exp_f32 v105,v105\n"
MF "a[36:39],v[20:23],v[88:91],a[36:39]\n"
"s_waitcnt lgkmcnt(0)\n"
TR "v[16:17],v2 offset:1024\n"
TR "v[18:19],v2 offset:7168\n"
MF "a[56:59],v[24:27],v[88:91],a[56:59]\n"
"s_nop 0\n"
TR "v[20:21],v3 offset:1024\n"
TR "v[22:23],v3 offset:7168\n"
MF "a[76:79],v[28:31],v[88:91],a[76:79]\n"
"v_exp_f32 v106,v106\n"
MF "a[72:75],v[28:31],v[84:87],a[72:75]\n"
"v_exp_f32 v107,v107\n"
MF "a[52:55],v[24:27],v[84:87],a[52:55]\n"
"v_add_f32 v60,v55,v104\n"
"v_add_f32 v60,v60,v105\n"
MF "a[48:51],v[24:27],v[80:83],a[48:51]\n"
CV "v104,v104,v105\n"
"v_add_f32 v60,v60,v106\n"
MF "a[68:71],v[28:31],v[80:83],a[68:71]\n"
"v_add_f32 v60,v60,v107\n"
CV "v105,v106,v107\n"
MF "a[64:67],v[28:31],v[76:79],a[64:67]\n"
"v_exp_f32 v108,v108\n"
MF "a[44:47],v[24:27],v[76:79],a[44:47]\n"
"v_exp_f32 v109,v109\n"
MF "a[40:43],v[24:27],v[72:75],a[40:43]\n"
"v_exp_f32 v110,v110\n"
MF "a[60:63],v[28:31],v[72:75],a[60:63]\n"
"s_waitcnt lgkmcnt(0)\n"
TR "v[24:25],v4 offset:1024\n"
TR "v[26:27],v4 offset:7168\n"
MF "a[80:83],v[16:19],v[72:75],a[80:83]\n"
"s_nop 0\n"
TR "v[28:29],v5 offset:1024\n"
TR "v[30:31],v5 offset:7168\n"
MF "a[100:103],v[20:23],v[72:75],a[100:103]\n"
"v_exp_f32 v111,v111\n"
MF "a[104:107],v[20:23],v[76:79],a[104:107]\n"
"v_add_f32 v61,v56,v108\n"
"v_add_f32 v61,v61,v109\n"
MF "a[84:87],v[16:19],v[76:79],a[84:87]\n"
CV "v108,v108,v109\n"
"v_add_f32 v61,v61,v110\n"
MF "a[88:91],v[16:19],v[80:83],a[88:91]\n"
"v_add_f32 v61,v61,v111\n"
CV "v109,v110,v111\n"
MF "a[108:111],v[20:23],v[80:83],a[108:111]\n"
"v_exp_f32 v112,v112\n"
MF "a[112:115],v[20:23],v[84:87],a[112:115]\n"
"v_exp_f32 v113,v113\n"
MF "a[92:95],v[16:19],v[84:87],a[92:95]\n"
"v_exp_f32 v114,v114\n"
MF "a[96:99],v[16:19],v[88:91],a[96:99]\n"
"v_exp_f32 v115,v115\n"
MF "a[116:119],v[20:23],v[88:91],a[116:119]\n"
"s_waitcnt lgkmcnt(0)\n"
TR "v[16:17],v2 offset:2048\n"
TR "v[18:19],v2 offset:8192\n"
MF "a[136:139],v[24:27],v[88:91],a[136:139]\n"
"s_nop 0\n"
TR "v[20:21],v3 offset:2048\n"
TR "v[22:23],v3 offset:8192\n"
MF "a[156:159],v[28:31],v[88:91],a[156:159]\n"
"v_add_f32 v57,v57,v112\n"
"v_add_f32 v57,v57,v113\n"
MF "a[152:155],v[28:31],v[84:87],a[152:155]\n"
CV "v94,v112,v113\n"
"v_add_f32 v57,v57,v114\n"
MF "a[132:135],v[24:27],v[84:87],a[132:135]\n"
"v_add_f32 v57,v57,v115\n"
CV "v95,v114,v115\n"
MF "a[128:131],v[24:27],v[80:83],a[128:131]\n"
"v_exp_f32 v116,v116\n"
MF "a[148:151],v[28:31],v[80:83],a[148:151]\n"
"v_exp_f32 v117,v117\n"
MF "a[144:147],v[28:31],v[76:79],a[144:147]\n"
"v_exp_f32 v118,v118\n"
MF "a[124:127],v[24:27],v[76:79],a[124:127]\n"
"v_exp_f32 v119,v119\n"
MF "a[120:123],v[24:27],v[72:75],a[120:123]\n"
"v_add_f32 v58,v58,v116\n"
"v_add_f32 v58,v58,v117\n"
MF "a[140:143],v[28:31],v[72:75],a[140:143]\n"
"s_waitcnt lgkmcnt(0)\n"
TR "v[24:25],v4 offset:2048\n"
TR "v[26:27],v4 offset:8192\n"
MF "a[160:163],v[16:19],v[72:75],a[160:163]\n"
"s_nop 0\n"
TR "v[28:29],v5 offset:2048\n"
TR "v[30:31],v5 offset:8192\n"
MF "a[180:183],v[20:23],v[72:75],a[180:183]\n"
CV "v98,v116,v117\n"
"v_add_f32 v58,v58,v118\n"
MF "a[184:187],v[20:23],v[76:79],a[184:187]\n"
"v_add_f32 v58,v58,v119\n"
CV "v99,v118,v119\n"
MF "a[164:167],v[16:19],v[76:79],a[164:167]\n"
"v_exp_f32 v120,v120\n"
MF "a[168:171],v[16:19],v[80:83],a[168:171]\n"
"v_exp_f32 v121,v121\n"
MF "a[188:191],v[20:23],v[80:83],a[188:191]\n"
"v_exp_f32 v122,v122\n"
MF "a[192:195],v[20:23],v[84:87],a[192:195]\n"
"v_exp_f32 v123,v123\n"
MF "a[172:175],v[16:19],v[84:87],a[172:175]\n"
"s_waitcnt vmcnt(6) lgkmcnt(0)\n"
"v_add_f32 v59,v59,v120\n"
"v_add_f32 v59,v59,v121\n"
MF "a[176:179],v[16:19],v[88:91],a[176:179]\n"
"s_barrier\n"
MF "a[196:199],v[20:23],v[88:91],a[196:199]\n"
"s_add_u32 s72,s72,6144\n"
"ds_read_b128 v[16:19],v0 offset:24576\n"
"ds_read_b128 v[20:23],v1 offset:24576\n"
MF "a[216:219],v[24:27],v[88:91],a[216:219]\n"
"s_cmp_eq_u32 s72,s17\n"
"s_cselect_b32 s72,s16,s72\n"
"s_add_u32 m0,s64,0\n"
MF "a[236:239],v[28:31],v[88:91],a[236:239]\n"
LD "v7,s[32:35],s72 offen nt lds\n"
MF "a[232:235],v[28:31],v[84:87],a[232:235]\n"
"s_add_u32 m0,s64,896\n"
CV "v102,v120,v121\n"
"v_add_f32 v59,v59,v122\n"
MF "a[212:215],v[24:27],v[84:87],a[212:215]\n"
LD "v7,s[32:35],s72 offen offset:128 nt lds\n"
MF "a[208:211],v[24:27],v[80:83],a[208:211]\n"
"s_add_u32 m0,s64,1792\n"
"v_add_f32 v59,v59,v123\n"
CV "v103,v122,v123\n"
MF "a[228:231],v[28:31],v[80:83],a[228:231]\n"
LD "v7,s[32:35],s72 offen offset:256 nt lds\n"
MF "a[224:227],v[28:31],v[76:79],a[224:227]\n"
MF "a[204:207],v[24:27],v[76:79],a[204:207]\n"
"s_waitcnt lgkmcnt(0)\n"
MF "a[200:203],v[24:27],v[72:75],a[200:203]\n"
"s_cmp_lg_u32 s61,0\n"
MF "a[220:223],v[28:31],v[72:75],a[220:223]\n"
"s_cbranch_scc1 .Laloop\n"
".Lepi2:\n"
"v_exp_f32 v124,v124\n"
"v_exp_f32 v125,v125\n"
"v_exp_f32 v126,v126\n"
"v_exp_f32 v127,v127\n"
"v_exp_f32 v128,v128\n"
"v_exp_f32 v129,v129\n"
"v_exp_f32 v130,v130\n"
"v_exp_f32 v131,v131\n"
"v_add_f32 v60,v60,v124\n"
"v_add_f32 v61,v61,v128\n"
"v_add_f32 v60,v60,v125\n"
"v_add_f32 v61,v61,v129\n"
"v_add_f32 v60,v60,v126\n"
"v_add_f32 v61,v61,v130\n"
"v_add_f32 v60,v60,v127\n"
"v_add_f32 v61,v61,v131\n"
CV "v106,v124,v125\n"
CV "v107,v126,v127\n"
CV "v110,v128,v129\n"
CV "v111,v130,v131\n"
"v_max3_f32 v10,v57,v58,v59\n"
"v_max3_f32 v10,v10,v60,v61\n"
"v_cmp_le_f32 vcc,0x47800000,v10\n"
"s_cbranch_vccnz .Lstub_17\n"
".Lback_17:\n"
"s_waitcnt vmcnt(3) lgkmcnt(0)\n"
"s_barrier\n"
"s_and_b32 s58,s56,1\n"
"s_mul_i32 s79,s58,12288\n"
"s_lshr_b32 s58,s56,1\n"
"s_mul_i32 s72,s58,61440\n"
"s_mul_i32 s73,s58,6144\n"
"s_add_u32 s73,s73,s79\n"
"v_lshrrev_b32 v192,7,v9\n"
"v_mul_u32_u24 v192,896,v192\n"
"v_add_u32 v192,v192,v8\n"
"s_add_u32 s88,s88,1024\n"
"s_add_u32 s89,s89,1024\n"
"s_add_u32 s72,s72,122880\n"
"s_lshl_b32 s58,s56,10\n"
"s_add_u32 s58,s58,73728\n"
"v_add_u32 v236,s58,v9\n"
"v_add_u32 v237,65536,v236\n"
"v_mov_b32 v10,1.0\n"
TR "v[160:161],v4 offset:50176\n"
TR "v[162:163],v4 offset:56320\n"
"v_mfma_f32_16x16x4_f32 v[16:19],v10,v57,0\n"
TR "v[164:165],v5 offset:50176\n"
TR "v[166:167],v5 offset:56320\n"
TR "v[168:169],v2 offset:51200\n"
TR "v[170:171],v2 offset:57344\n"
TR "v[172:173],v3 offset:51200\n"
TR "v[174:175],v3 offset:57344\n"
"v_mfma_f32_16x16x4_f32 v[20:23],v10,v58,0\n"
TR "v[176:177],v4 offset:51200\n"
TR "v[178:179],v4 offset:57344\n"
TR "v[180:181],v5 offset:51200\n"
TR "v[182:183],v5 offset:57344\n"
TR "v[136:137],v2 offset:49152\n"
TR "v[138:139],v2 offset:55296\n"
"v_mfma_f32_16x16x4_f32 v[24:27],v10,v59,0\n"
TR "v[140:141],v3 offset:49152\n"
TR "v[142:143],v3 offset:55296\n"
TR "v[144:145],v4 offset:49152\n"
TR "v[146:147],v4 offset:55296\n"
TR "v[148:149],v5 offset:49152\n"
TR "v[150:151],v5 offset:55296\n"
"v_mfma_f32_16x16x4_f32 v[28:31],v10,v60,0\n"
TR "v[152:153],v2 offset:50176\n"
TR "v[154:155],v2 offset:56320\n"
TR "v[156:157],v3 offset:50176\n"
TR "v[158:159],v3 offset:56320\n"
"v_mfma_f32_16x16x4_f32 v[32:35],v10,v61,0\n"
"s_add_u32 m0,s73,24576\n"
"s_add_u32 s80,s89,s72\n"
LD "v192,s[48:51],s80 offen lds\n"
"v_add_u32 v193,9216,v192\n"
LD "v193,s[48:51],s80 offen offset:1024 lds\n"
"s_waitcnt lgkmcnt(15)\n"
MF "a[120:123],v[160:163],v[92:95],a[120:123]\n"
MF "a[140:143],v[164:167],v[92:95],a[140:143]\n"
"v_add_u32 v194,18432,v192\n"
LD "v194,s[48:51],s80 offen offset:2048 lds\n"
"v_add_u32 v195,27648,v192\n"
LD "v195,s[48:51],s80 offen offset:3072 lds\n"
"v_rcp_f32 v208,v16\n"
"v_rcp_f32 v209,v20\n"
"v_rcp_f32 v210,v24\n"
"v_rcp_f32 v211,v28\n"
"v_rcp_f32 v212,v32\n"
MF "a[124:127],v[160:163],v[96:99],a[124:127]\n"
"v_mul_f32_e64 v214,-s13,v208\n"
"v_mul_f32_e64 v215,-s13,v209\n"
"v_mul_f32_e64 v216,-s13,v210\n"
"v_mul_f32_e64 v217,-s13,v211\n"
"v_mul_f32_e64 v218,-s13,v212\n"
"s_add_u32 m0,s73,28672\n"
"v_add_u32 v196,40960,v192\n"
LD "v196,s[48:51],s80 offen lds\n"
AR "v72,a120\n"
AR "v73,a121\n"
AR "v74,a122\n"
MF "a[144:147],v[164:167],v[96:99],a[144:147]\n"
AR "v75,a123\n"
AR "v76,a140\n"
AR "v77,a141\n"
AR "v78,a142\n"
AR "v79,a143\n"
"v_pk_fma_f32 v[72:73],v[72:73],v[208:209],v[214:215] op_sel:[0,0,0] op_sel_hi:[1,0,0]\n"
"v_pk_fma_f32 v[74:75],v[74:75],v[208:209],v[214:215] op_sel:[0,0,0] op_sel_hi:[1,0,0]\n"
"v_pk_fma_f32 v[76:77],v[76:77],v[208:209],v[214:215] op_sel:[0,0,0] op_sel_hi:[1,0,0]\n"
"v_pk_fma_f32 v[78:79],v[78:79],v[208:209],v[214:215] op_sel:[0,0,0] op_sel_hi:[1,0,0]\n"
CV "v112,v72,v73\n"
CV "v113,v74,v75\n"
CV "v114,v76,v77\n"
CV "v115,v78,v79\n"
MF "a[128:131],v[160:163],v[100:103],a[128:131]\n"
"ds_read_b128 a[140:143],v237 offset:20480\n"
AW "a120,v112\n"
AW "a121,v113\n"
AW "a122,v114\n"
AW "a123,v115\n"
"s_waitcnt lgkmcnt(7)\n"
MF "a[44:47],v[144:147],v[96:99],a[44:47]\n"
"v_add_u32 v197,50176,v192\n"
LD "v197,s[48:51],s80 offen offset:1024 lds\n"
AR "v80,a124\n"
AR "v81,a125\n"
AR "v82,a126\n"
MF "a[148:151],v[164:167],v[100:103],a[148:151]\n"
AR "v83,a127\n"
AR "v84,a144\n"
AR "v85,a145\n"
"s_nop 0\n"
AR "v86,a146\n"
AR "v87,a147\n"
"v_pk_fma_f32 v[80:81],v[80:81],v[208:209],v[214:215] op_sel:[0,1,1] op_sel_hi:[1,1,1]\n"
"v_pk_fma_f32 v[82:83],v[82:83],v[208:209],v[214:215] op_sel:[0,1,1] op_sel_hi:[1,1,1]\n"
"v_pk_fma_f32 v[84:85],v[84:85],v[208:209],v[214:215] op_sel:[0,1,1] op_sel_hi:[1,1,1]\n"
"v_pk_fma_f32 v[86:87],v[86:87],v[208:209],v[214:215] op_sel:[0,1,1] op_sel_hi:[1,1,1]\n"
CV "v116,v80,v81\n"
CV "v117,v82,v83\n"
CV "v118,v84,v85\n"
CV "v119,v86,v87\n"
MF "a[132:135],v[160:163],v[104:107],a[132:135]\n"
"ds_read_b128 a[144:147],v236\n"
AW "a124,v116\n"
AW "a125,v117\n"
AW "a126,v118\n"
AW "a127,v119\n"
"s_waitcnt lgkmcnt(6)\n"
MF "a[64:67],v[148:151],v[96:99],a[64:67]\n"
"s_add_u32 m0,s73,0\n"
"s_add_u32 s80,s88,s72\n"
"s_sub_u32 s72,s72,122880\n"
"s_nop 0\n"
LD "v192,s[48:51],s80 offen lds\n"
AR "v72,a128\n"
AR "v73,a129\n"
AR "v74,a130\n"
MF "a[152:155],v[164:167],v[104:107],a[152:155]\n"
AR "v75,a131\n"
AR "v76,a148\n"
AR "v77,a149\n"
AR "v78,a150\n"
AR "v79,a151\n"
"v_pk_fma_f32 v[72:73],v[72:73],v[210:211],v[216:217] op_sel:[0,0,0] op_sel_hi:[1,0,0]\n"
"v_pk_fma_f32 v[74:75],v[74:75],v[210:211],v[216:217] op_sel:[0,0,0] op_sel_hi:[1,0,0]\n"
"v_pk_fma_f32 v[76:77],v[76:77],v[210:211],v[216:217] op_sel:[0,0,0] op_sel_hi:[1,0,0]\n"
"v_pk_fma_f32 v[78:79],v[78:79],v[210:211],v[216:217] op_sel:[0,0,0] op_sel_hi:[1,0,0]\n"
LD "v193,s[48:51],s80 offen offset:1024 lds\n"
CV "v112,v72,v73\n"
CV "v113,v74,v75\n"
CV "v114,v76,v77\n"
CV "v115,v78,v79\n"
MF "a[136:139],v[160:163],v[108:111],a[136:139]\n"
"ds_read_b128 a[148:151],v236 offset:4096\n"
AW "a128,v112\n"
AW "a129,v113\n"
AW "a130,v114\n"
AW "a131,v115\n"
"s_waitcnt lgkmcnt(5)\n"
MF "a[84:87],v[152:155],v[96:99],a[84:87]\n"
LD "v194,s[48:51],s80 offen offset:2048 lds\n"
AR "v80,a132\n"
AR "v81,a133\n"
AR "v82,a134\n"
MF "a[156:159],v[164:167],v[108:111],a[156:159]\n"
AR "v83,a135\n"
AR "v84,a152\n"
AR "v85,a153\n"
"s_nop 0\n"
AR "v86,a154\n"
AR "v87,a155\n"
"v_pk_fma_f32 v[80:81],v[80:81],v[210:211],v[216:217] op_sel:[0,1,1] op_sel_hi:[1,1,1]\n"
"v_pk_fma_f32 v[82:83],v[82:83],v[210:211],v[216:217] op_sel:[0,1,1] op_sel_hi:[1,1,1]\n"
"v_pk_fma_f32 v[84:85],v[84:85],v[210:211],v[216:217] op_sel:[0,1,1] op_sel_hi:[1,1,1]\n"
"v_pk_fma_f32 v[86:87],v[86:87],v[210:211],v[216:217] op_sel:[0,1,1] op_sel_hi:[1,1,1]\n"
CV "v116,v80,v81\n"
CV "v117,v82,v83\n"
CV "v118,v84,v85\n"
CV "v119,v86,v87\n"
MF "a[160:163],v[168:171],v[92:95],a[160:163]\n"
"ds_read_b128 a[152:155],v236 offset:8192\n"
AW "a132,v116\n"
AW "a133,v117\n"
AW "a134,v118\n"
AW "a135,v119\n"
"s_waitcnt lgkmcnt(4)\n"
MF "a[104:107],v[156:159],v[96:99],a[104:107]\n"
LD "v195,s[48:51],s80 offen offset:3072 lds\n"
AR "v72,a136\n"
AR "v73,a137\n"
AR "v74,a138\n"
MF "a[180:183],v[172:175],v[92:95],a[180:183]\n"
AR "v75,a139\n"
AR "v76,a156\n"
AR "v77,a157\n"
"s_nop 0\n"
AR "v78,a158\n"
AR "v79,a159\n"
"v_pk_fma_f32 v[72:73],v[72:73],v[212:213],v[218:219] op_sel:[0,0,0] op_sel_hi:[1,0,0]\n"
"v_pk_fma_f32 v[74:75],v[74:75],v[212:213],v[218:219] op_sel:[0,0,0] op_sel_hi:[1,0,0]\n"
"v_pk_fma_f32 v[76:77],v[76:77],v[212:213],v[218:219] op_sel:[0,0,0] op_sel_hi:[1,0,0]\n"
"v_pk_fma_f32 v[78:79],v[78:79],v[212:213],v[218:219] op_sel:[0,0,0] op_sel_hi:[1,0,0]\n"
CV "v112,v72,v73\n"
CV "v113,v74,v75\n"
CV "v114,v76,v77\n"
CV "v115,v78,v79\n"
MF "a[164:167],v[168:171],v[96:99],a[164:167]\n"
"ds_read_b128 a[156:159],v236 offset:12288\n"
AW "a136,v112\n"
AW "a137,v113\n"
AW "a138,v114\n"
AW "a139,v115\n"
"v_max3_f32 v10,|v72|,|v73|,|v74|\n"
"s_lshl_b32 s58,s56,10\n"
"s_add_u32 s58,s58,159744\n"
"v_max3_f32 v10,v10,|v75|,|v76|\n"
"v_max3_f32 v10,v10,|v77|,|v78|\n"
"v_max3_f32 v10,v10,|v78|,|v79|\n"
"v_mul_f32 v10,s10,v10\n"
"v_lshrrev_b32_e64 v11,2,v9\n"
"v_readlane_b32 s12,v10,3\n"
"v_readlane_b32 s6,v10,22\n"
"v_readlane_b32 s7,v10,41\n"
"v_readlane_b32 s8,v10,60\n"
"v_add_u32 v11,s58,v11\n"
"s_min_u32 s12,s12,s6\n"
"s_min_u32 s7,s7,s8\n"
"s_min_u32 s12,s12,s7\n"
"s_and_b32 s12,s12,0x7f800000\n"
"s_sub_u32 s12,s12,0x14000000\n"
"s_cmp_lt_u32 s12,0x50000000\n"
"s_cselect_b32 s12,s12,0xe7400000\n"
"s_add_u32 s12,s12,0x18c00000\n"
"v_mov_b32 v10,s12\n"
"v_or_b32 v12,0x80000000,v10\n"
"ds_write2st64_b32 v11,v10,v12 offset1:1\n"
MF "a[8:11],v[136:139],v[100:103],a[8:11]\n"
"s_add_u32 m0,s73,4096\n"
"s_nop 0\n"
LD "v196,s[48:51],s80 offen lds\n"
AR "v80,a160\n"
AR "v81,a161\n"
AR "v82,a162\n"
MF "a[184:187],v[172:175],v[96:99],a[184:187]\n"
AR "v83,a163\n"
AR "v84,a180\n"
AR "v85,a181\n"
AR "v86,a182\n"
AR "v87,a183\n"
"v_pk_fma_f32 v[80:81],v[80:81],v[208:209],v[214:215] op_sel:[0,0,0] op_sel_hi:[1,0,0]\n"
"v_pk_fma_f32 v[82:83],v[82:83],v[208:209],v[214:215] op_sel:[0,0,0] op_sel_hi:[1,0,0]\n"
"v_pk_fma_f32 v[84:85],v[84:85],v[208:209],v[214:215] op_sel:[0,0,0] op_sel_hi:[1,0,0]\n"
"v_pk_fma_f32 v[86:87],v[86:87],v[208:209],v[214:215] op_sel:[0,0,0] op_sel_hi:[1,0,0]\n"
LD "v197,s[48:51],s80 offen offset:1024 lds\n"
CV "v116,v80,v81\n"
CV "v117,v82,v83\n"
CV "v118,v84,v85\n"
CV "v119,v86,v87\n"
MF "a[168:171],v[168:171],v[100:103],a[168:171]\n"
"ds_read_b128 a[180:183],v236 offset:16384\n"
AW "a160,v116\n"
AW "a161,v117\n"
AW "a162,v118\n"
AW "a163,v119\n"
MF "a[28:31],v[140:143],v[100:103],a[28:31]\n"
AR "v72,a164\n"
AR "v73,a165\n"
AR "v74,a166\n"
MF "a[188:191],v[172:175],v[100:103],a[188:191]\n"
AR "v75,a167\n"
AR "v76,a184\n"
AR "v77,a185\n"
AR "v78,a186\n"
AR "v79,a187\n"
"v_pk_fma_f32 v[72:73],v[72:73],v[208:209],v[214:215] op_sel:[0,1,1] op_sel_hi:[1,1,1]\n"
"v_pk_fma_f32 v[74:75],v[74:75],v[208:209],v[214:215] op_sel:[0,1,1] op_sel_hi:[1,1,1]\n"
"v_pk_fma_f32 v[76:77],v[76:77],v[208:209],v[214:215] op_sel:[0,1,1] op_sel_hi:[1,1,1]\n"
"v_pk_fma_f32 v[78:79],v[78:79],v[208:209],v[214:215] op_sel:[0,1,1] op_sel_hi:[1,1,1]\n"
CV "v112,v72,v73\n"
CV "v113,v74,v75\n"
CV "v114,v76,v77\n"
CV "v115,v78,v79\n"
MF "a[172:175],v[168:171],v[104:107],a[172:175]\n"
"ds_read_b128 a[184:187],v236 offset:20480\n"
AW "a164,v112\n"
AW "a165,v113\n"
AW "a166,v114\n"
AW "a167,v115\n"
MF "a[48:51],v[144:147],v[100:103],a[48:51]\n"
"s_waitcnt lgkmcnt(0)\n"
"s_barrier\n"
"s_add_u32 m0,s73,49152\n"
"s_add_u32 s80,s89,s72\n"
"s_nop 0\n"
LD "v192,s[48:51],s80 offen lds\n"
AR "v80,a168\n"
AR "v81,a169\n"
AR "v82,a170\n"
MF "a[192:195],v[172:175],v[104:107],a[192:195]\n"
AR "v83,a171\n"
AR "v84,a188\n"
AR "v85,a189\n"
AR "v86,a190\n"
AR "v87,a191\n"
"v_pk_fma_f32 v[80:81],v[80:81],v[210:211],v[216:217] op_sel:[0,0,0] op_sel_hi:[1,0,0]\n"
"v_pk_fma_f32 v[82:83],v[82:83],v[210:211],v[216:217] op_sel:[0,0,0] op_sel_hi:[1,0,0]\n"
"v_pk_fma_f32 v[84:85],v[84:85],v[210:211],v[216:217] op_sel:[0,0,0] op_sel_hi:[1,0,0]\n"
"v_pk_fma_f32 v[86:87],v[86:87],v[210:211],v[216:217] op_sel:[0,0,0] op_sel_hi:[1,0,0]\n"
LD "v193,s[48:51],s80 offen offset:1024 lds\n"
CV "v116,v80,v81\n"
CV "v117,v82,v83\n"
CV "v118,v84,v85\n"
CV "v119,v86,v87\n"
MF "a[176:179],v[168:171],v[108:111],a[176:179]\n"
"v_accvgpr_mov_b32 a188,a240\n"
"v_accvgpr_mov_b32 a189,a241\n"
"v_accvgpr_mov_b32 a190,a242\n"
"v_accvgpr_mov_b32 a191,a243\n"
AW "a168,v116\n"
AW "a169,v117\n"
AW "a170,v118\n"
AW "a171,v119\n"
MF "a[68:71],v[148:151],v[100:103],a[68:71]\n"
LD "v194,s[48:51],s80 offen offset:2048 lds\n"
AR "v72,a172\n"
AR "v73,a173\n"
AR "v74,a174\n"
MF "a[196:199],v[172:175],v[108:111],a[196:199]\n"
AR "v75,a175\n"
AR "v76,a192\n"
AR "v77,a193\n"
AR "v78,a194\n"
AR "v79,a195\n"
"v_pk_fma_f32 v[72:73],v[72:73],v[210:211],v[216:217] op_sel:[0,1,1] op_sel_hi:[1,1,1]\n"
"v_pk_fma_f32 v[74:75],v[74:75],v[210:211],v[216:217] op_sel:[0,1,1] op_sel_hi:[1,1,1]\n"
"v_pk_fma_f32 v[76:77],v[76:77],v[210:211],v[216:217] op_sel:[0,1,1] op_sel_hi:[1,1,1]\n"
"v_pk_fma_f32 v[78:79],v[78:79],v[210:211],v[216:217] op_sel:[0,1,1] op_sel_hi:[1,1,1]\n"
LD "v195,s[48:51],s80 offen offset:3072 lds\n"
CV "v112,v72,v73\n"
CV "v113,v74,v75\n"
CV "v114,v76,v77\n"
CV "v115,v78,v79\n"
MF "a[200:203],v[176:179],v[92:95],a[200:203]\n"
"v_accvgpr_mov_b32 a192,a244\n"
"v_accvgpr_mov_b32 a193,a245\n"
"v_accvgpr_mov_b32 a194,a246\n"
"v_accvgpr_mov_b32 a195,a247\n"
AW "a172,v112\n"
AW "a173,v113\n"
AW "a174,v114\n"
AW "a175,v115\n"
MF "a[88:91],v[152:155],v[100:103],a[88:91]\n"
"s_add_u32 m0,s73,53248\n"
"s_nop 0\n"
LD "v196,s[48:51],s80 offen lds\n"
AR "v80,a176\n"
AR "v81,a177\n"
AR "v82,a178\n"
MF "a[220:223],v[180:183],v[92:95],a[220:223]\n"
AR "v83,a179\n"
AR "v84,a196\n"
AR "v85,a197\n"
"s_nop 0\n"
AR "v86,a198\n"
AR "v87,a199\n"
"v_pk_fma_f32 v[80:81],v[80:81],v[212:213],v[218:219] op_sel:[0,0,0] op_sel_hi:[1,0,0]\n"
"v_pk_fma_f32 v[82:83],v[82:83],v[212:213],v[218:219] op_sel:[0,0,0] op_sel_hi:[1,0,0]\n"
"v_pk_fma_f32 v[84:85],v[84:85],v[212:213],v[218:219] op_sel:[0,0,0] op_sel_hi:[1,0,0]\n"
"v_pk_fma_f32 v[86:87],v[86:87],v[212:213],v[218:219] op_sel:[0,0,0] op_sel_hi:[1,0,0]\n"
CV "v116,v80,v81\n"
CV "v117,v82,v83\n"
CV "v118,v84,v85\n"
CV "v119,v86,v87\n"
MF "a[204:207],v[176:179],v[96:99],a[204:207]\n"
"v_accvgpr_mov_b32 a196,a248\n"
"v_accvgpr_mov_b32 a197,a249\n"
"v_accvgpr_mov_b32 a198,a250\n"
"v_accvgpr_mov_b32 a199,a251\n"
AW "a176,v116\n"
AW "a177,v117\n"
AW "a178,v118\n"
AW "a179,v119\n"
MF "a[108:111],v[156:159],v[100:103],a[108:111]\n"
LD "v197,s[48:51],s80 offen offset:1024 lds\n"
AR "v72,a200\n"
AR "v73,a201\n"
AR "v74,a202\n"
MF "a[224:227],v[180:183],v[96:99],a[224:227]\n"
AR "v75,a203\n"
AR "v76,a220\n"
AR "v77,a221\n"
AR "v78,a222\n"
AR "v79,a223\n"
"v_pk_fma_f32 v[72:73],v[72:73],v[208:209],v[214:215] op_sel:[0,0,0] op_sel_hi:[1,0,0]\n"
"v_pk_fma_f32 v[74:75],v[74:75],v[208:209],v[214:215] op_sel:[0,0,0] op_sel_hi:[1,0,0]\n"
"v_pk_fma_f32 v[76:77],v[76:77],v[208:209],v[214:215] op_sel:[0,0,0] op_sel_hi:[1,0,0]\n"
"v_pk_fma_f32 v[78:79],v[78:79],v[208:209],v[214:215] op_sel:[0,0,0] op_sel_hi:[1,0,0]\n"
CV "v112,v72,v73\n"
CV "v113,v74,v75\n"
CV "v114,v76,v77\n"
CV "v115,v78,v79\n"
MF "a[208:211],v[176:179],v[100:103],a[208:211]\n"
AW "a220,v62\n"
AW "a221,v63\n"
AW "a222,v64\n"
AW "a223,v65\n"
AW "a200,v112\n"
AW "a201,v113\n"
AW "a202,v114\n"
AW "a203,v115\n"
MF "a[12:15],v[136:139],v[104:107],a[12:15]\n"
"s_add_u32 m0,s73,73728\n"
"s_add_u32 s80,s88,s72\n"
LD "v192,s[48:51],s80 offen lds\n"
AR "v80,a204\n"
AR "v81,a205\n"
AR "v82,a206\n"
MF "a[228:231],v[180:183],v[100:103],a[228:231]\n"
AR "v83,a207\n"
AR "v84,a224\n"
AR "v85,a225\n"
"s_nop 0\n"
AR "v86,a226\n"
AR "v87,a227\n"
"v_pk_fma_f32 v[80:81],v[80:81],v[208:209],v[214:215] op_sel:[0,1,1] op_sel_hi:[1,1,1]\n"
"v_pk_fma_f32 v[82:83],v[82:83],v[208:209],v[214:215] op_sel:[0,1,1] op_sel_hi:[1,1,1]\n"
"v_pk_fma_f32 v[84:85],v[84:85],v[208:209],v[214:215] op_sel:[0,1,1] op_sel_hi:[1,1,1]\n"
"v_pk_fma_f32 v[86:87],v[86:87],v[208:209],v[214:215] op_sel:[0,1,1] op_sel_hi:[1,1,1]\n"
LD "v193,s[48:51],s80 offen offset:1024 lds\n"
CV "v116,v80,v81\n"
CV "v117,v82,v83\n"
CV "v118,v84,v85\n"
CV "v119,v86,v87\n"
MF "a[212:215],v[176:179],v[104:107],a[212:215]\n"
AW "a224,v66\n"
AW "a225,v67\n"
AW "a226,v68\n"
AW "a227,v69\n"
AW "a204,v116\n"
AW "a205,v117\n"
AW "a206,v118\n"
AW "a207,v119\n"
MF "a[32:35],v[140:143],v[104:107],a[32:35]\n"
LD "v194,s[48:51],s80 offen offset:2048 lds\n"
AR "v72,a208\n"
AR "v73,a209\n"
AR "v74,a210\n"
MF "a[232:235],v[180:183],v[104:107],a[232:235]\n"
AR "v75,a211\n"
AR "v76,a228\n"
AR "v77,a229\n"
AR "v78,a230\n"
AR "v79,a231\n"
"v_pk_fma_f32 v[72:73],v[72:73],v[210:211],v[216:217] op_sel:[0,0,0] op_sel_hi:[1,0,0]\n"
"v_pk_fma_f32 v[74:75],v[74:75],v[210:211],v[216:217] op_sel:[0,0,0] op_sel_hi:[1,0,0]\n"
"v_pk_fma_f32 v[76:77],v[76:77],v[210:211],v[216:217] op_sel:[0,0,0] op_sel_hi:[1,0,0]\n"
"v_pk_fma_f32 v[78:79],v[78:79],v[210:211],v[216:217] op_sel:[0,0,0] op_sel_hi:[1,0,0]\n"
CV "v112,v72,v73\n"
CV "v113,v74,v75\n"
CV "v114,v76,v77\n"
CV "v115,v78,v79\n"
MF "a[216:219],v[176:179],v[108:111],a[216:219]\n"
AW "a228,v70\n"
AW "a229,v71\n"
AW "a230,v132\n"
AW "a231,v133\n"
AW "a208,v112\n"
AW "a209,v113\n"
AW "a210,v114\n"
AW "a211,v115\n"
MF "a[52:55],v[144:147],v[104:107],a[52:55]\n"
LD "v195,s[48:51],s80 offen offset:3072 lds\n"
AR "v80,a212\n"
AR "v81,a213\n"
AR "v82,a214\n"
MF "a[236:239],v[180:183],v[108:111],a[236:239]\n"
AR "v83,a215\n"
AR "v84,a232\n"
AR "v85,a233\n"
AR "v86,a234\n"
AR "v87,a235\n"
"v_pk_fma_f32 v[80:81],v[80:81],v[210:211],v[216:217] op_sel:[0,1,1] op_sel_hi:[1,1,1]\n"
"v_pk_fma_f32 v[82:83],v[82:83],v[210:211],v[216:217] op_sel:[0,1,1] op_sel_hi:[1,1,1]\n"
"v_pk_fma_f32 v[84:85],v[84:85],v[210:211],v[216:217] op_sel:[0,1,1] op_sel_hi:[1,1,1]\n"
"v_pk_fma_f32 v[86:87],v[86:87],v[210:211],v[216:217] op_sel:[0,1,1] op_sel_hi:[1,1,1]\n"
CV "v116,v80,v81\n"
CV "v117,v82,v83\n"
CV "v118,v84,v85\n"
CV "v119,v86,v87\n"
MF "a[0:3],v[136:139],v[92:95],a[0:3]\n"
AW "a232,v134\n"
AW "a233,v135\n"
AW "a234,v6\n"
AW "a235,v13\n"
AW "a212,v116\n"
AW "a213,v117\n"
AW "a214,v118\n"
AW "a215,v119\n"
MF "a[72:75],v[148:151],v[104:107],a[72:75]\n"
"s_add_u32 m0,s73,77824\n"
"s_nop 0\n"
LD "v196,s[48:51],s80 offen lds\n"
AR "v72,a216\n"
AR "v73,a217\n"
AR "v74,a218\n"
MF "a[20:23],v[140:143],v[92:95],a[20:23]\n"
AR "v75,a219\n"
AR "v76,a236\n"
AR "v77,a237\n"
"s_nop 0\n"
AR "v78,a238\n"
AR "v79,a239\n"
"v_pk_fma_f32 v[72:73],v[72:73],v[212:213],v[218:219] op_sel:[0,0,0] op_sel_hi:[1,0,0]\n"
"v_pk_fma_f32 v[74:75],v[74:75],v[212:213],v[218:219] op_sel:[0,0,0] op_sel_hi:[1,0,0]\n"
"v_pk_fma_f32 v[76:77],v[76:77],v[212:213],v[218:219] op_sel:[0,0,0] op_sel_hi:[1,0,0]\n"
"v_pk_fma_f32 v[78:79],v[78:79],v[212:213],v[218:219] op_sel:[0,0,0] op_sel_hi:[1,0,0]\n"
LD "v197,s[48:51],s80 offen offset:1024 lds\n"
CV "v112,v72,v73\n"
CV "v113,v74,v75\n"
CV "v114,v76,v77\n"
CV "v115,v78,v79\n"
MF "a[40:43],v[144:147],v[92:95],a[40:43]\n"
AW "a236,v14\n"
AW "a237,v15\n"
"v_accvgpr_mov_b32 a238,a254\n"
"v_accvgpr_mov_b32 a239,a255\n"
AW "a216,v112\n"
AW "a217,v113\n"
AW "a218,v114\n"
AW "a219,v115\n"
MF "a[92:95],v[152:155],v[104:107],a[92:95]\n"
".Lpg21h:\n"
"s_cbranch_execz .Lpg25h\n"
AR "v80,a0\n"
AR "v81,a1\n"
AR "v82,a2\n"
MF "a[60:63],v[148:151],v[92:95],a[60:63]\n"
AR "v83,a3\n"
AR "v84,a20\n"
AR "v85,a21\n"
"s_nop 0\n"
AR "v86,a22\n"
AR "v87,a23\n"
"v_pk_fma_f32 v[80:81],v[80:81],v[208:209],v[214:215] op_sel:[0,0,0] op_sel_hi:[1,0,0]\n"
"v_pk_fma_f32 v[82:83],v[82:83],v[208:209],v[214:215] op_sel:[0,0,0] op_sel_hi:[1,0,0]\n"
"v_pk_fma_f32 v[84:85],v[84:85],v[208:209],v[214:215] op_sel:[0,0,0] op_sel_hi:[1,0,0]\n"
"v_pk_fma_f32 v[86:87],v[86:87],v[208:209],v[214:215] op_sel:[0,0,0] op_sel_hi:[1,0,0]\n"
CV "v116,v80,v81\n"
CV "v117,v82,v83\n"
CV "v118,v84,v85\n"
CV "v119,v86,v87\n"
MF "a[80:83],v[152:155],v[92:95],a[80:83]\n"
"ds_read_b128 a[20:23],v236 offset:24576\n"
AW "a0,v116\n"
AW "a1,v117\n"
AW "a2,v118\n"
AW "a3,v119\n"
MF "a[112:115],v[156:159],v[104:107],a[112:115]\n"
AR "v72,a40\n"
AR "v73,a41\n"
AR "v74,a42\n"
MF "a[100:103],v[156:159],v[92:95],a[100:103]\n"
AR "v75,a43\n"
AR "v76,a60\n"
AR "v77,a61\n"
AR "v78,a62\n"
AR "v79,a63\n"
"v_pk_fma_f32 v[72:73],v[72:73],v[208:209],v[214:215] op_sel:[0,0,0] op_sel_hi:[1,0,0]\n"
"v_pk_fma_f32 v[74:75],v[74:75],v[208:209],v[214:215] op_sel:[0,0,0] op_sel_hi:[1,0,0]\n"
"v_pk_fma_f32 v[76:77],v[76:77],v[208:209],v[214:215] op_sel:[0,0,0] op_sel_hi:[1,0,0]\n"
"v_pk_fma_f32 v[78:79],v[78:79],v[208:209],v[214:215] op_sel:[0,0,0] op_sel_hi:[1,0,0]\n"
CV "v112,v72,v73\n"
CV "v113,v74,v75\n"
CV "v114,v76,v77\n"
CV "v115,v78,v79\n"
MF "a[4:7],v[136:139],v[96:99],a[4:7]\n"
"ds_read_b128 a[60:63],v236 offset:45056\n"
AW "a40,v112\n"
AW "a41,v113\n"
AW "a42,v114\n"
AW "a43,v115\n"
MF "a[16:19],v[136:139],v[108:111],a[16:19]\n"
AR "v80,a80\n"
AR "v81,a81\n"
AR "v82,a82\n"
MF "a[24:27],v[140:143],v[96:99],a[24:27]\n"
AR "v83,a83\n"
AR "v84,a100\n"
AR "v85,a101\n"
AR "v86,a102\n"
AR "v87,a103\n"
"v_pk_fma_f32 v[80:81],v[80:81],v[208:209],v[214:215] op_sel:[0,0,0] op_sel_hi:[1,0,0]\n"
"v_pk_fma_f32 v[82:83],v[82:83],v[208:209],v[214:215] op_sel:[0,0,0] op_sel_hi:[1,0,0]\n"
"v_pk_fma_f32 v[84:85],v[84:85],v[208:209],v[214:215] op_sel:[0,0,0] op_sel_hi:[1,0,0]\n"
"v_pk_fma_f32 v[86:87],v[86:87],v[208:209],v[214:215] op_sel:[0,0,0] op_sel_hi:[1,0,0]\n"
CV "v116,v80,v81\n"
CV "v117,v82,v83\n"
CV "v118,v84,v85\n"
CV "v119,v86,v87\n"
MF "a[36:39],v[140:143],v[108:111],a[36:39]\n"
"ds_read_b128 a[100:103],v237\n"
AW "a80,v116\n"
AW "a81,v117\n"
AW "a82,v118\n"
AW "a83,v119\n"
MF "a[56:59],v[144:147],v[108:111],a[56:59]\n"
AR "v72,a4\n"
AR "v73,a5\n"
AR "v74,a6\n"
MF "a[76:79],v[148:151],v[108:111],a[76:79]\n"
AR "v75,a7\n"
AR "v76,a24\n"
AR "v77,a25\n"
AR "v78,a26\n"
AR "v79,a27\n"
"v_pk_fma_f32 v[72:73],v[72:73],v[208:209],v[214:215] op_sel:[0,1,1] op_sel_hi:[1,1,1]\n"
"v_pk_fma_f32 v[74:75],v[74:75],v[208:209],v[214:215] op_sel:[0,1,1] op_sel_hi:[1,1,1]\n"
"v_pk_fma_f32 v[76:77],v[76:77],v[208:209],v[214:215] op_sel:[0,1,1] op_sel_hi:[1,1,1]\n"
"v_pk_fma_f32 v[78:79],v[78:79],v[208:209],v[214:215] op_sel:[0,1,1] op_sel_hi:[1,1,1]\n"
CV "v112,v72,v73\n"
CV "v113,v74,v75\n"
CV "v114,v76,v77\n"
CV "v115,v78,v79\n"
MF "a[96:99],v[152:155],v[108:111],a[96:99]\n"
"ds_read_b128 a[24:27],v236 offset:28672\n"
AW "a4,v112\n"
AW "a5,v113\n"
AW "a6,v114\n"
AW "a7,v115\n"
MF "a[116:119],v[156:159],v[108:111],a[116:119]\n"
"v_and_b32 v0,512,v9\n"
"v_lshlrev_b32 v0,2,v0\n"
"v_sub_u32 v0,v2,v0\n"
"s_bfe_u32 s58,s22,0x10014\n"
"s_lshl_b32 s58,s58,5\n"
"v_xor_b32 v0,s58,v0\n"
"s_and_b32 s58,s56,1\n"
"s_lshl_b32 s58,s58,10\n"
"s_lshr_b32 s59,s56,1\n"
"s_lshl_b32 s59,s59,8\n"
"s_add_u32 s58,s58,s59\n"
"s_add_u32 s58,s58,159744\n"
"v_mov_b32 v252,s58\n"
"v_xor_b32 v1,32,v0\n"
"v_xor_b32 v2,64,v0\n"
"v_xor_b32 v3,96,v0\n"
"v_add_u32 v4,73728,v0\n"
"v_add_u32 v5,73728,v1\n"
"v_add_u32 v6,73728,v2\n"
"v_add_u32 v7,73728,v3\n"
"s_waitcnt vmcnt(12) lgkmcnt(0)\n"
"s_barrier\n"
"ds_read_b128 v[252:255],v252\n"
TR "v[176:177],v0 offset:24576\n"
TR "v[178:179],v0 offset:26624\n"
TR "v[180:181],v0 offset:28672\n"
TR "v[182:183],v0 offset:30720\n"
TR "v[184:185],v0 offset:32768\n"
TR "v[186:187],v0 offset:34816\n"
"s_waitcnt lgkmcnt(0)\n"
"s_cmp_ge_u32 s56,2\n"
"s_cbranch_scc1 .Lrole1\n"
MF "v[16:19],a[120:123],v[176:179],v[252:255]\n"
"s_mov_b32 s65,128\n"
TR "v[188:189],v1 offset:24576\n"
AR "v220,a44\n"
MF "v[16:19],a[160:163],v[180:183],v[16:19]\n"
"s_mov_b32 s9,0x3f80\n"
TR "v[190:191],v1 offset:26624\n"
AR "v221,a45\n"
MF "v[16:19],a[200:203],v[184:187],v[16:19]\n"
"s_add_u32 s89,s89,s65\n"
AR "v222,a46\n"
AR "v223,a47\n"
MF "v[24:27],a[204:207],v[184:187],v[252:255]\n"
"s_bfe_u32 s58,s22,0x30014\n"
TR "a[240:241],v1 offset:28672\n"
AR "v224,a64\n"
MF "v[24:27],a[164:167],v[180:183],v[24:27]\n"
"s_add_u32 s61,s58,1\n"
TR "a[242:243],v1 offset:30720\n"
AR "v225,a65\n"
MF "v[24:27],a[124:127],v[176:179],v[24:27]\n"
"s_and_b32 s61,s61,7\n"
AR "v226,a66\n"
AR "v227,a67\n"
MF "v[32:35],a[128:131],v[176:179],v[252:255]\n"
"s_mov_b32 s64,9\n"
TR "a[244:245],v1 offset:32768\n"
"ds_read_b128 a[64:67],v236 offset:49152\n"
MF "v[32:35],a[168:171],v[180:183],v[32:35]\n"
"s_xor_b32 s67,s78,8\n"
TR "a[246:247],v1 offset:34816\n"
"v_fma_f32 v220,v220,v209,v215\n"
MF "v[32:35],a[208:211],v[184:187],v[32:35]\n"
"s_mov_b32 s71,204800\n"
"v_fma_f32 v221,v221,v209,v215\n"
"v_fma_f32 v222,v222,v209,v215\n"
MF "v[40:43],a[212:215],v[184:187],v[252:255]\n"
"s_mov_b32 s66,204800\n"
"v_fma_f32 v223,v223,v209,v215\n"
"v_fma_f32 v224,v224,v209,v215\n"
MF "v[40:43],a[172:175],v[180:183],v[40:43]\n"
"s_and_b32 s73,s56,1\n"
"v_fma_f32 v225,v225,v209,v215\n"
"v_fma_f32 v226,v226,v209,v215\n"
MF "v[40:43],a[132:135],v[176:179],v[40:43]\n"
"s_mul_i32 s73,s73,5120\n"
"v_fma_f32 v227,v227,v209,v215\n"
CV "v220,v220,v221\n"
MF "v[48:51],a[136:139],v[176:179],v[252:255]\n"
"s_add_u32 s73,s73,s93\n"
CV "v221,v222,v223\n"
CV "v222,v224,v225\n"
MF "v[48:51],a[176:179],v[180:183],v[48:51]\n"
"s_xor_b32 s58,s58,4\n"
CV "v223,v226,v227\n"
AW "a44,v220\n"
MF "v[48:51],a[216:219],v[184:187],v[48:51]\n"
"s_waitcnt lgkmcnt(0)\n"
AW "a45,v221\n"
AW "a46,v222\n"
MF "v[52:55],a[216:219],a[244:247],v[252:255]\n"
"s_mul_i32 s70,s58,204800\n"
TR "v[176:177],v2 offset:24576\n"
AW "a47,v223\n"
MF "v[52:55],a[176:179],a[240:243],v[52:55]\n"
"s_add_u32 s70,s70,s73\n"
TR "v[178:179],v2 offset:26624\n"
AR "v228,a84\n"
MF "v[52:55],a[136:139],v[188:191],v[52:55]\n"
"s_add_u32 s70,s70,0xa000\n"
AR "v229,a85\n"
AR "v230,a86\n"
MF "v[44:47],a[132:135],v[188:191],v[252:255]\n"
"s_mov_b32 s85,1\n"
TR "v[180:181],v2 offset:28672\n"
AR "v231,a87\n"
MF "v[44:47],a[172:175],a[240:243],v[44:47]\n"
"s_mov_b32 s83,0xf0000000\n"
TR "v[182:183],v2 offset:30720\n"
AR "v232,a104\n"
MF "v[44:47],a[212:215],a[244:247],v[44:47]\n"
"s_mov_b32 s82,0xf0000000\n"
AR "v233,a105\n"
AR "v234,a106\n"
MF "v[36:39],a[208:211],a[244:247],v[252:255]\n"
"s_add_u32 s58,s79,73728\n"
TR "v[184:185],v2 offset:32768\n"
AR "v235,a107\n"
MF "v[36:39],a[168:171],a[240:243],v[36:39]\n"
"s_mov_b32 s4,64\n"
TR "v[186:187],v2 offset:34816\n"
"ds_read_b128 a[104:107],v237 offset:4096\n"
MF "v[36:39],a[128:131],v[188:191],v[36:39]\n"
"s_mul_i32 s95,s92,3277\n"
"v_fma_f32 v228,v228,v209,v215\n"
"v_fma_f32 v229,v229,v209,v215\n"
MF "v[28:31],a[124:127],v[188:191],v[252:255]\n"
"s_lshr_b32 s95,s95,16\n"
"v_fma_f32 v230,v230,v209,v215\n"
"v_fma_f32 v231,v231,v209,v215\n"
MF "v[28:31],a[164:167],a[240:243],v[28:31]\n"
"s_mul_i32 s59,s95,20\n"
"v_fma_f32 v232,v232,v209,v215\n"
"v_fma_f32 v233,v233,v209,v215\n"
MF "v[28:31],a[204:207],a[244:247],v[28:31]\n"
"s_sub_u32 s59,s92,s59\n"
"v_fma_f32 v234,v234,v209,v215\n"
"v_fma_f32 v235,v235,v209,v215\n"
MF "v[20:23],a[200:203],a[244:247],v[252:255]\n"
"s_cmp_ge_u32 s59,10\n"
"s_cselect_b32 s96,4294557760,0\n"
CV "v228,v228,v229\n"
MF "v[20:23],a[160:163],a[240:243],v[20:23]\n"
"s_add_u32 s96,s96,1024\n"
CV "v229,v230,v231\n"
CV "v230,v232,v233\n"
MF "v[20:23],a[120:123],v[188:191],v[20:23]\n"
"s_waitcnt lgkmcnt(1)\n"
CV "v231,v234,v235\n"
AW "a84,v228\n"
MF "v[56:59],a[120:123],v[176:179],v[252:255]\n"
"s_mul_i32 s59,s59,40960\n"
TR "v[188:189],v3 offset:24576\n"
AW "a85,v229\n"
MF "v[56:59],a[160:163],v[180:183],v[56:59]\n"
"s_add_u32 s96,s96,s59\n"
TR "v[190:191],v3 offset:26624\n"
AW "a86,v230\n"
MF "v[56:59],a[200:203],v[184:187],v[56:59]\n"
"s_lshl_b32 s59,s95,7\n"
AW "a87,v231\n"
AR "v220,a8\n"
MF "v[64:67],a[204:207],v[184:187],v[252:255]\n"
"s_add_u32 s96,s96,s59\n"
TR "a[240:241],v3 offset:28672\n"
AR "v221,a9\n"
MF "v[64:67],a[164:167],v[180:183],v[64:67]\n"
"s_lshl_b32 s2,s92,10\n"
TR "a[242:243],v3 offset:30720\n"
AR "v222,a10\n"
MF "v[64:67],a[124:127],v[176:179],v[64:67]\n"
"s_add_u32 s2,s2,s93\n"
AR "v223,a11\n"
AR "v224,a28\n"
MF "v[72:75],a[128:131],v[176:179],v[252:255]\n"
"s_mov_b32 s0,s96\n"
TR "a[244:245],v3 offset:32768\n"
AR "v225,a29\n"
MF "v[72:75],a[168:171],v[180:183],v[72:75]\n"
"s_sub_u32 s2,s2,204800\n"
TR "a[246:247],v3 offset:34816\n"
AR "v226,a30\n"
MF "v[72:75],a[208:211],v[184:187],v[72:75]\n"
"s_mov_b32 s92,64\n"
AR "v227,a31\n"
"v_fma_f32 v220,v220,v210,v216\n"
MF "v[80:83],a[212:215],v[184:187],v[252:255]\n"
"ds_read_b128 a[28:31],v236 offset:32768\n"
"v_fma_f32 v221,v221,v210,v216\n"
MF "v[80:83],a[172:175],v[180:183],v[80:83]\n"
"v_fma_f32 v222,v222,v210,v216\n"
"v_fma_f32 v223,v223,v210,v216\n"
MF "v[80:83],a[132:135],v[176:179],v[80:83]\n"
"v_fma_f32 v224,v224,v210,v216\n"
"v_fma_f32 v225,v225,v210,v216\n"
MF "v[88:91],a[136:139],v[176:179],v[252:255]\n"
"v_fma_f32 v226,v226,v210,v216\n"
"v_fma_f32 v227,v227,v210,v216\n"
MF "v[88:91],a[176:179],v[180:183],v[88:91]\n"
"s_waitcnt vmcnt(0) lgkmcnt(0)\n"
CV "v220,v220,v221\n"
CV "v221,v222,v223\n"
MF "v[88:91],a[216:219],v[184:187],v[88:91]\n"
"s_barrier\n"
MF "v[92:95],a[216:219],a[244:247],v[252:255]\n"
TR "v[176:177],v0\n"
CV "v222,v224,v225\n"
MF "v[92:95],a[176:179],a[240:243],v[92:95]\n"
TR "v[178:179],v0 offset:2048\n"
CV "v223,v226,v227\n"
MF "v[92:95],a[136:139],v[188:191],v[92:95]\n"
TR "v[96:97],v0 offset:49152\n"
AW "a8,v220\n"
MF "v[84:87],a[132:135],v[188:191],v[252:255]\n"
TR "v[180:181],v0 offset:4096\n"
AW "a9,v221\n"
MF "v[84:87],a[172:175],a[240:243],v[84:87]\n"
TR "v[182:183],v0 offset:6144\n"
AW "a10,v222\n"
MF "v[84:87],a[212:215],a[244:247],v[84:87]\n"
TR "v[98:99],v0 offset:51200\n"
AW "a11,v223\n"
MF "v[76:79],a[208:211],a[244:247],v[252:255]\n"
TR "v[184:185],v0 offset:8192\n"
AR "v228,a48\n"
MF "v[76:79],a[168:171],a[240:243],v[76:79]\n"
TR "v[186:187],v0 offset:10240\n"
AR "v229,a49\n"
MF "v[76:79],a[128:131],v[188:191],v[76:79]\n"
TR "v[100:101],v0 offset:53248\n"
AR "v230,a50\n"
MF "v[68:71],a[124:127],v[188:191],v[252:255]\n"
TR "v[102:103],v0 offset:55296\n"
AR "v231,a51\n"
MF "v[68:71],a[164:167],a[240:243],v[68:71]\n"
TR "v[104:105],v0 offset:57344\n"
AR "v232,a68\n"
MF "v[68:71],a[204:207],a[244:247],v[68:71]\n"
TR "v[106:107],v0 offset:59392\n"
AR "v233,a69\n"
MF "v[60:63],a[200:203],a[244:247],v[252:255]\n"
TR "v[108:109],v4\n"
AR "v234,a70\n"
MF "v[60:63],a[160:163],a[240:243],v[60:63]\n"
TR "v[110:111],v4 offset:2048\n"
AR "v235,a71\n"
MF "v[60:63],a[120:123],v[188:191],v[60:63]\n"
"s_waitcnt lgkmcnt(6)\n"
TR "v[112:113],v4 offset:4096\n"
"ds_read_b128 a[68:71],v236 offset:53248\n"
MF "v[16:19],a[140:143],v[176:179],v[16:19]\n"
TR "v[188:189],v1\n"
"v_fma_f32 v228,v228,v210,v216\n"
MF "v[16:19],a[180:183],v[180:183],v[16:19]\n"
TR "v[190:191],v1 offset:2048\n"
"v_fma_f32 v229,v229,v210,v216\n"
MF "v[16:19],a[220:223],v[184:187],v[16:19]\n"
TR "v[114:115],v4 offset:6144\n"
"v_fma_f32 v230,v230,v210,v216\n"
MF "v[24:27],a[224:227],v[184:187],v[24:27]\n"
TR "a[240:241],v1 offset:4096\n"
"v_fma_f32 v231,v231,v210,v216\n"
MF "v[24:27],a[184:187],v[180:183],v[24:27]\n"
TR "a[242:243],v1 offset:6144\n"
"v_fma_f32 v232,v232,v210,v216\n"
MF "v[24:27],a[144:147],v[176:179],v[24:27]\n"
TR "v[116:117],v4 offset:8192\n"
"v_fma_f32 v233,v233,v210,v216\n"
MF "v[32:35],a[148:151],v[176:179],v[32:35]\n"
TR "a[244:245],v1 offset:8192\n"
"v_fma_f32 v234,v234,v210,v216\n"
MF "v[32:35],a[188:191],v[180:183],v[32:35]\n"
TR "a[246:247],v1 offset:10240\n"
"v_fma_f32 v235,v235,v210,v216\n"
MF "v[32:35],a[228:231],v[184:187],v[32:35]\n"
TR "v[118:119],v4 offset:10240\n"
CV "v228,v228,v229\n"
MF "v[40:43],a[232:235],v[184:187],v[40:43]\n"
TR "v[120:121],v1 offset:49152\n"
CV "v229,v230,v231\n"
MF "v[40:43],a[192:195],v[180:183],v[40:43]\n"
TR "v[122:123],v1 offset:51200\n"
CV "v230,v232,v233\n"
MF "v[40:43],a[152:155],v[176:179],v[40:43]\n"
TR "v[124:125],v1 offset:53248\n"
CV "v231,v234,v235\n"
MF "v[48:51],a[156:159],v[176:179],v[48:51]\n"
TR "v[126:127],v1 offset:55296\n"
AW "a48,v228\n"
MF "v[48:51],a[196:199],v[180:183],v[48:51]\n"
TR "v[128:129],v1 offset:57344\n"
AW "a49,v229\n"
MF "v[48:51],a[236:239],v[184:187],v[48:51]\n"
"s_waitcnt lgkmcnt(6)\n"
TR "v[130:131],v1 offset:59392\n"
AW "a50,v230\n"
MF "v[52:55],a[236:239],a[244:247],v[52:55]\n"
TR "v[176:177],v2\n"
AW "a51,v231\n"
MF "v[52:55],a[196:199],a[240:243],v[52:55]\n"
TR "v[178:179],v2 offset:2048\n"
AR "v220,a88\n"
MF "v[52:55],a[156:159],v[188:191],v[52:55]\n"
TR "v[132:133],v5\n"
AR "v221,a89\n"
MF "v[44:47],a[152:155],v[188:191],v[44:47]\n"
TR "v[180:181],v2 offset:4096\n"
AR "v222,a90\n"
MF "v[44:47],a[192:195],a[240:243],v[44:47]\n"
TR "v[182:183],v2 offset:6144\n"
AR "v223,a91\n"
MF "v[44:47],a[232:235],a[244:247],v[44:47]\n"
TR "v[134:135],v5 offset:2048\n"
AR "v224,a108\n"
MF "v[36:39],a[228:231],a[244:247],v[36:39]\n"
TR "v[184:185],v2 offset:8192\n"
AR "v225,a109\n"
MF "v[36:39],a[188:191],a[240:243],v[36:39]\n"
TR "v[186:187],v2 offset:10240\n"
AR "v226,a110\n"
MF "v[36:39],a[148:151],v[188:191],v[36:39]\n"
TR "v[136:137],v5 offset:4096\n"
AR "v227,a111\n"
MF "v[28:31],a[144:147],v[188:191],v[28:31]\n"
"s_nop 0\n"
TR "v[138:139],v5 offset:6144\n"
"ds_read_b128 a[108:111],v237 offset:8192\n"
MF "v[28:31],a[184:187],a[240:243],v[28:31]\n"
TR "v[140:141],v5 offset:8192\n"
"v_fma_f32 v220,v220,v210,v216\n"
MF "v[28:31],a[224:227],a[244:247],v[28:31]\n"
TR "v[142:143],v5 offset:10240\n"
"v_fma_f32 v221,v221,v210,v216\n"
MF "v[20:23],a[220:223],a[244:247],v[20:23]\n"
TR "v[144:145],v2 offset:49152\n"
"v_fma_f32 v222,v222,v210,v216\n"
MF "v[20:23],a[180:183],a[240:243],v[20:23]\n"
TR "v[146:147],v2 offset:51200\n"
"v_fma_f32 v223,v223,v210,v216\n"
MF "v[20:23],a[140:143],v[188:191],v[20:23]\n"
"s_waitcnt lgkmcnt(7)\n"
TR "v[148:149],v2 offset:53248\n"
"v_fma_f32 v224,v224,v210,v216\n"
MF "v[56:59],a[140:143],v[176:179],v[56:59]\n"
TR "v[188:189],v3\n"
"v_add_u32 v198,59392,v192\n"
MF "v[56:59],a[180:183],v[180:183],v[56:59]\n"
TR "v[190:191],v3 offset:2048\n"
"v_add_u32 v199,68608,v192\n"
MF "v[56:59],a[220:223],v[184:187],v[56:59]\n"
TR "v[150:151],v2 offset:55296\n"
"v_add_u32 v200,81920,v192\n"
"v_add_u32 v201,91136,v192\n"
MF "v[64:67],a[224:227],v[184:187],v[64:67]\n"
TR "a[240:241],v3 offset:4096\n"
"v_add_u32 v202,100352,v192\n"
MF "v[64:67],a[184:187],v[180:183],v[64:67]\n"
TR "a[242:243],v3 offset:6144\n"
"v_add_u32 v203,109568,v192\n"
MF "v[64:67],a[144:147],v[176:179],v[64:67]\n"
TR "v[152:153],v2 offset:57344\n"
"v_fma_f32 v225,v225,v210,v216\n"
MF "v[72:75],a[148:151],v[176:179],v[72:75]\n"
TR "a[244:245],v3 offset:8192\n"
"v_fma_f32 v226,v226,v210,v216\n"
MF "v[72:75],a[188:191],v[180:183],v[72:75]\n"
TR "a[246:247],v3 offset:10240\n"
"v_fma_f32 v227,v227,v210,v216\n"
MF "v[72:75],a[228:231],v[184:187],v[72:75]\n"
TR "v[154:155],v2 offset:59392\n"
CV "v220,v220,v221\n"
MF "v[80:83],a[232:235],v[184:187],v[80:83]\n"
TR "v[156:157],v6\n"
CV "v221,v222,v223\n"
MF "v[80:83],a[192:195],v[180:183],v[80:83]\n"
TR "v[158:159],v6 offset:2048\n"
CV "v222,v224,v225\n"
MF "v[80:83],a[152:155],v[176:179],v[80:83]\n"
TR "v[160:161],v6 offset:4096\n"
CV "v223,v226,v227\n"
MF "v[88:91],a[156:159],v[176:179],v[88:91]\n"
TR "v[162:163],v6 offset:6144\n"
AW "a88,v220\n"
MF "v[88:91],a[196:199],v[180:183],v[88:91]\n"
TR "v[164:165],v6 offset:8192\n"
"s_waitcnt lgkmcnt(0)\n"
MF "v[88:91],a[236:239],v[184:187],v[88:91]\n"
"s_barrier\n"
MF "v[92:95],a[236:239],a[244:247],v[92:95]\n"
"s_add_u32 s80,s89,122880\n"
"s_add_u32 m0,s79,0\n"
TR "v[166:167],v6 offset:10240\n"
MF "v[92:95],a[196:199],a[240:243],v[92:95]\n"
LD "v192,s[48:51],s80 offen lds\n"
MF "v[92:95],a[156:159],v[188:191],v[92:95]\n"
TR "v[168:169],v3 offset:49152\n"
AW "a89,v221\n"
MF "v[84:87],a[152:155],v[188:191],v[84:87]\n"
TR "v[170:171],v3 offset:51200\n"
AW "a90,v222\n"
MF "v[84:87],a[192:195],a[240:243],v[84:87]\n"
TR "v[172:173],v3 offset:53248\n"
AW "a91,v223\n"
MF "v[84:87],a[232:235],a[244:247],v[84:87]\n"
LD "v193,s[48:51],s80 offen offset:1024 lds\n"
MF "v[76:79],a[228:231],a[244:247],v[76:79]\n"
TR "v[174:175],v3 offset:55296\n"
AR "v228,a12\n"
MF "v[76:79],a[188:191],a[240:243],v[76:79]\n"
TR "v[204:205],v3 offset:57344\n"
AR "v229,a13\n"
MF "v[76:79],a[148:151],v[188:191],v[76:79]\n"
TR "v[206:207],v3 offset:59392\n"
AR "v230,a14\n"
MF "v[68:71],a[144:147],v[188:191],v[68:71]\n"
TR "v[244:245],v7\n"
AR "v231,a15\n"
MF "v[68:71],a[184:187],a[240:243],v[68:71]\n"
LD "v194,s[48:51],s80 offen offset:2048 lds\n"
MF "v[68:71],a[224:227],a[244:247],v[68:71]\n"
TR "v[246:247],v7 offset:2048\n"
AR "v232,a32\n"
MF "v[60:63],a[220:223],a[244:247],v[60:63]\n"
TR "v[248:249],v7 offset:4096\n"
AR "v233,a33\n"
MF "v[60:63],a[180:183],a[240:243],v[60:63]\n"
TR "v[250:251],v7 offset:6144\n"
AR "v234,a34\n"
MF "v[60:63],a[140:143],v[188:191],v[60:63]\n"
LD "v195,s[48:51],s80 offen offset:3072 lds\n"
MF "v[16:19],a[0:3],v[96:99],v[16:19]\n"
TR "v[238:239],v7 offset:8192\n"
AR "v235,a35\n"
MF "v[16:19],a[40:43],v[100:103],v[16:19]\n"
".Lpg22h:\n"
"s_cbranch_execz .Lpg26h\n"
"s_nop 0\n"
TR "v[240:241],v7 offset:10240\n"
"ds_read_b128 a[32:35],v236 offset:36864\n"
MF "v[16:19],a[80:83],v[104:107],v[16:19]\n"
"v_fma_f32 v228,v228,v211,v217\n"
"v_fma_f32 v229,v229,v211,v217\n"
MF "v[16:19],a[20:23],v[108:111],v[16:19]\n"
"s_add_u32 m0,s79,4096\n"
"v_fma_f32 v230,v230,v211,v217\n"
"v_fma_f32 v231,v231,v211,v217\n"
MF "v[16:19],a[60:63],v[112:115],v[16:19]\n"
LD "v196,s[48:51],s80 offen lds\n"
MF "v[16:19],a[100:103],v[116:119],v[16:19]\n"
"v_fma_f32 v232,v232,v211,v217\n"
"v_fma_f32 v233,v233,v211,v217\n"
MF "v[20:23],a[100:103],v[140:143],v[20:23]\n"
"v_fma_f32 v234,v234,v211,v217\n"
"v_fma_f32 v235,v235,v211,v217\n"
MF "v[20:23],a[60:63],v[136:139],v[20:23]\n"
CV "v228,v228,v229\n"
CV "v229,v230,v231\n"
MF "v[20:23],a[20:23],v[132:135],v[20:23]\n"
LD "v197,s[48:51],s80 offen offset:1024 lds\n"
MF "v[20:23],a[80:83],v[128:131],v[20:23]\n"
CV "v230,v232,v233\n"
CV "v231,v234,v235\n"
MF "v[20:23],a[40:43],v[124:127],v[20:23]\n"
AW "a12,v228\n"
AW "a13,v229\n"
MF "v[20:23],a[0:3],v[120:123],v[20:23]\n"
"s_waitcnt lgkmcnt(13)\n"
AW "a14,v230\n"
AW "a15,v231\n"
MF "v[56:59],a[0:3],v[144:147],v[56:59]\n"
AR "v220,a52\n"
AR "v221,a53\n"
MF "v[56:59],a[40:43],v[148:151],v[56:59]\n"
LD "v198,s[48:51],s80 offen offset:2048 lds\n"
MF "v[56:59],a[80:83],v[152:155],v[56:59]\n"
AR "v222,a54\n"
AR "v223,a55\n"
MF "v[56:59],a[20:23],v[156:159],v[56:59]\n"
AR "v224,a72\n"
AR "v225,a73\n"
MF "v[56:59],a[60:63],v[160:163],v[56:59]\n"
AR "v226,a74\n"
AR "v227,a75\n"
MF "v[56:59],a[100:103],v[164:167],v[56:59]\n"
"s_waitcnt lgkmcnt(1)\n"
"ds_read_b128 a[72:75],v236 offset:57344\n"
"v_fma_f32 v220,v220,v211,v217\n"
MF "v[60:63],a[100:103],v[238:241],v[60:63]\n"
LD "v199,s[48:51],s80 offen offset:3072 lds\n"
MF "v[60:63],a[60:63],v[248:251],v[60:63]\n"
"v_fma_f32 v221,v221,v211,v217\n"
"v_fma_f32 v222,v222,v211,v217\n"
MF "v[60:63],a[20:23],v[244:247],v[60:63]\n"
"v_fma_f32 v223,v223,v211,v217\n"
"v_fma_f32 v224,v224,v211,v217\n"
MF "v[60:63],a[80:83],v[204:207],v[60:63]\n"
"s_add_u32 m0,s79,8192\n"
"v_fma_f32 v225,v225,v211,v217\n"
"v_fma_f32 v226,v226,v211,v217\n"
MF "v[60:63],a[40:43],v[172:175],v[60:63]\n"
LD "v200,s[48:51],s80 offen lds\n"
MF "v[60:63],a[0:3],v[168:171],v[60:63]\n"
"v_fma_f32 v227,v227,v211,v217\n"
CV "v220,v220,v221\n"
MF "v[68:71],a[4:7],v[168:171],v[68:71]\n"
CV "v221,v222,v223\n"
CV "v222,v224,v225\n"
MF "v[68:71],a[44:47],v[172:175],v[68:71]\n"
CV "v223,v226,v227\n"
AW "a52,v220\n"
MF "v[68:71],a[84:87],v[204:207],v[68:71]\n"
AW "a53,v221\n"
AW "a54,v222\n"
MF "v[68:71],a[24:27],v[244:247],v[68:71]\n"
LD "v201,s[48:51],s80 offen offset:1024 lds\n"
MF "v[68:71],a[64:67],v[248:251],v[68:71]\n"
AW "a55,v223\n"
AR "v228,a92\n"
MF "v[68:71],a[104:107],v[238:241],v[68:71]\n"
"s_waitcnt lgkmcnt(0)\n"
AR "v229,a93\n"
AR "v230,a94\n"
MF "v[64:67],a[104:107],v[164:167],v[64:67]\n"
"s_barrier\n"
MF "v[64:67],a[64:67],v[160:163],v[64:67]\n"
LD "v202,s[48:51],s80 offen offset:2048 lds\n"
MF "v[64:67],a[24:27],v[156:159],v[64:67]\n"
AR "v231,a95\n"
AR "v232,a112\n"
MF "v[64:67],a[84:87],v[152:155],v[64:67]\n"
AR "v233,a113\n"
AR "v234,a114\n"
MF "v[64:67],a[44:47],v[148:151],v[64:67]\n"
AR "v235,a115\n"
"v_fma_f32 v228,v228,v211,v217\n"
MF "v[64:67],a[4:7],v[144:147],v[64:67]\n"
"ds_read_b128 a[112:115],v237 offset:12288\n"
"v_fma_f32 v229,v229,v211,v217\n"
MF "v[28:31],a[4:7],v[120:123],v[28:31]\n"
LD "v203,s[48:51],s80 offen offset:3072 lds\n"
MF "v[28:31],a[44:47],v[124:127],v[28:31]\n"
"v_fma_f32 v230,v230,v211,v217\n"
"v_fma_f32 v231,v231,v211,v217\n"
MF "v[28:31],a[84:87],v[128:131],v[28:31]\n"
"v_fma_f32 v232,v232,v211,v217\n"
"v_fma_f32 v233,v233,v211,v217\n"
MF "v[28:31],a[24:27],v[132:135],v[28:31]\n"
"v_fma_f32 v234,v234,v211,v217\n"
"v_fma_f32 v235,v235,v211,v217\n"
MF "v[28:31],a[64:67],v[136:139],v[28:31]\n"
CV "v228,v228,v229\n"
CV "v229,v230,v231\n"
MF "v[28:31],a[104:107],v[140:143],v[28:31]\n"
"s_add_u32 s88,s88,s65\n"
"s_add_u32 m0,s79,49152\n"
CV "v230,v232,v233\n"
MF "v[24:27],a[104:107],v[116:119],v[24:27]\n"
LD "v192,s[48:51],s88 offen lds\n"
MF "v[24:27],a[64:67],v[112:115],v[24:27]\n"
CV "v231,v234,v235\n"
AW "a92,v228\n"
MF "v[24:27],a[24:27],v[108:111],v[24:27]\n"
AW "a93,v229\n"
AW "a94,v230\n"
MF "v[24:27],a[84:87],v[104:107],v[24:27]\n"
AW "a95,v231\n"
AR "v220,a16\n"
MF "v[24:27],a[44:47],v[100:103],v[24:27]\n"
LD "v193,s[48:51],s88 offen offset:1024 lds\n"
MF "v[24:27],a[4:7],v[96:99],v[24:27]\n"
AR "v221,a17\n"
AR "v222,a18\n"
MF "v[32:35],a[8:11],v[96:99],v[32:35]\n"
AR "v223,a19\n"
AR "v224,a36\n"
MF "v[32:35],a[48:51],v[100:103],v[32:35]\n"
AR "v225,a37\n"
AR "v226,a38\n"
MF "v[32:35],a[88:91],v[104:107],v[32:35]\n"
AR "v227,a39\n"
"v_fma_f32 v220,v220,v212,v218\n"
MF "v[32:35],a[28:31],v[108:111],v[32:35]\n"
LD "v194,s[48:51],s88 offen offset:2048 lds\n"
MF "v[32:35],a[68:71],v[112:115],v[32:35]\n"
"ds_read_b128 a[36:39],v236 offset:40960\n"
"v_fma_f32 v221,v221,v212,v218\n"
MF "v[32:35],a[108:111],v[116:119],v[32:35]\n"
"v_fma_f32 v222,v222,v212,v218\n"
"v_fma_f32 v223,v223,v212,v218\n"
MF "v[36:39],a[108:111],v[140:143],v[36:39]\n"
"v_fma_f32 v224,v224,v212,v218\n"
"v_fma_f32 v225,v225,v212,v218\n"
MF "v[36:39],a[68:71],v[136:139],v[36:39]\n"
"v_fma_f32 v226,v226,v212,v218\n"
"v_fma_f32 v227,v227,v212,v218\n"
MF "v[36:39],a[28:31],v[132:135],v[36:39]\n"
LD "v195,s[48:51],s88 offen offset:3072 lds\n"
MF "v[36:39],a[88:91],v[128:131],v[36:39]\n"
CV "v220,v220,v221\n"
CV "v221,v222,v223\n"
MF "v[36:39],a[48:51],v[124:127],v[36:39]\n"
CV "v222,v224,v225\n"
CV "v223,v226,v227\n"
MF "v[36:39],a[8:11],v[120:123],v[36:39]\n"
AW "a16,v220\n"
AW "a17,v221\n"
MF "v[72:75],a[8:11],v[144:147],v[72:75]\n"
"s_add_u32 m0,s79,53248\n"
AW "a18,v222\n"
AW "a19,v223\n"
MF "v[72:75],a[48:51],v[148:151],v[72:75]\n"
LD "v196,s[48:51],s88 offen lds\n"
MF "v[72:75],a[88:91],v[152:155],v[72:75]\n"
AR "v228,a56\n"
AR "v229,a57\n"
MF "v[72:75],a[28:31],v[156:159],v[72:75]\n"
AR "v230,a58\n"
AR "v231,a59\n"
MF "v[72:75],a[68:71],v[160:163],v[72:75]\n"
AR "v232,a76\n"
AR "v233,a77\n"
MF "v[72:75],a[108:111],v[164:167],v[72:75]\n"
AR "v234,a78\n"
AR "v235,a79\n"
MF "v[76:79],a[108:111],v[238:241],v[76:79]\n"
LD "v197,s[48:51],s88 offen offset:1024 lds\n"
MF "v[76:79],a[68:71],v[248:251],v[76:79]\n"
"ds_read_b128 a[76:79],v236 offset:61440\n"
"v_fma_f32 v228,v228,v212,v218\n"
MF "v[76:79],a[28:31],v[244:247],v[76:79]\n"
"v_fma_f32 v229,v229,v212,v218\n"
"v_fma_f32 v230,v230,v212,v218\n"
MF "v[76:79],a[88:91],v[204:207],v[76:79]\n"
"v_fma_f32 v231,v231,v212,v218\n"
"v_fma_f32 v232,v232,v212,v218\n"
MF "v[76:79],a[48:51],v[172:175],v[76:79]\n"
"v_fma_f32 v233,v233,v212,v218\n"
"v_fma_f32 v234,v234,v212,v218\n"
MF "v[76:79],a[8:11],v[168:171],v[76:79]\n"
"s_waitcnt lgkmcnt(2)\n"
LD "v198,s[48:51],s88 offen offset:2048 lds\n"
MF "v[84:87],a[12:15],v[168:171],v[84:87]\n"
"v_fma_f32 v235,v235,v212,v218\n"
CV "v228,v228,v229\n"
MF "v[84:87],a[52:55],v[172:175],v[84:87]\n"
CV "v229,v230,v231\n"
CV "v230,v232,v233\n"
MF "v[84:87],a[92:95],v[204:207],v[84:87]\n"
CV "v231,v234,v235\n"
AW "a56,v228\n"
MF "v[84:87],a[32:35],v[244:247],v[84:87]\n"
AW "a57,v229\n"
AW "a58,v230\n"
MF "v[84:87],a[72:75],v[248:251],v[84:87]\n"
LD "v199,s[48:51],s88 offen offset:3072 lds\n"
MF "v[84:87],a[112:115],v[238:241],v[84:87]\n"
AW "a59,v231\n"
AR "v220,a96\n"
MF "v[80:83],a[112:115],v[164:167],v[80:83]\n"
AR "v221,a97\n"
AR "v222,a98\n"
MF "v[80:83],a[72:75],v[160:163],v[80:83]\n"
AR "v223,a99\n"
AR "v224,a116\n"
MF "v[80:83],a[32:35],v[156:159],v[80:83]\n"
"s_add_u32 m0,s79,57344\n"
AR "v225,a117\n"
AR "v226,a118\n"
MF "v[80:83],a[92:95],v[152:155],v[80:83]\n"
LD "v200,s[48:51],s88 offen lds\n"
MF "v[80:83],a[52:55],v[148:151],v[80:83]\n"
AR "v227,a119\n"
"v_fma_f32 v220,v220,v212,v218\n"
MF "v[80:83],a[12:15],v[144:147],v[80:83]\n"
"ds_read_b128 a[116:119],v237 offset:16384\n"
"v_fma_f32 v221,v221,v212,v218\n"
MF "v[44:47],a[12:15],v[120:123],v[44:47]\n"
"v_fma_f32 v222,v222,v212,v218\n"
"v_fma_f32 v223,v223,v212,v218\n"
MF "v[44:47],a[52:55],v[124:127],v[44:47]\n"
"v_fma_f32 v224,v224,v212,v218\n"
"v_fma_f32 v225,v225,v212,v218\n"
MF "v[44:47],a[92:95],v[128:131],v[44:47]\n"
LD "v201,s[48:51],s88 offen offset:1024 lds\n"
MF "v[44:47],a[32:35],v[132:135],v[44:47]\n"
"v_fma_f32 v226,v226,v212,v218\n"
"v_fma_f32 v227,v227,v212,v218\n"
MF "v[44:47],a[72:75],v[136:139],v[44:47]\n"
CV "v220,v220,v221\n"
CV "v221,v222,v223\n"
MF "v[44:47],a[112:115],v[140:143],v[44:47]\n"
CV "v222,v224,v225\n"
CV "v223,v226,v227\n"
MF "v[40:43],a[112:115],v[116:119],v[40:43]\n"
AW "a96,v220\n"
AW "a97,v221\n"
MF "v[40:43],a[72:75],v[112:115],v[40:43]\n"
LD "v202,s[48:51],s88 offen offset:2048 lds\n"
MF "v[40:43],a[32:35],v[108:111],v[40:43]\n"
AW "a98,v222\n"
AW "a99,v223\n"
MF "v[40:43],a[92:95],v[104:107],v[40:43]\n"
AR "v8,a252\n"
AR "v11,a253\n"
MF "v[40:43],a[52:55],v[100:103],v[40:43]\n"
"v_bfe_u32 v224,v9,5,3\n"
"v_bfe_u32 v225,v9,8,2\n"
MF "v[40:43],a[12:15],v[96:99],v[40:43]\n"
"s_waitcnt lgkmcnt(0)\n"
"v_lshlrev_b32 v225,1,v225\n"
"v_and_b32 v226,16,v9\n"
MF "v[48:51],a[16:19],v[96:99],v[48:51]\n"
LD "v203,s[48:51],s88 offen offset:3072 lds\n"
MF "v[48:51],a[56:59],v[100:103],v[48:51]\n"
"v_lshlrev_b32_e64 v226,v226,s9\n"
"v_sub_u32 v224,v224,v225\n"
MF "v[48:51],a[96:99],v[104:107],v[48:51]\n"
"v_cmp_eq_u32 vcc,0,v224\n"
MF "v[48:51],a[36:39],v[108:111],v[48:51]\n"
"s_nop 0\n"
"v_cndmask_b32 v220,0,v226,vcc\n"
"v_cmp_eq_u32 vcc,1,v224\n"
MF "v[48:51],a[76:79],v[112:115],v[48:51]\n"
"s_nop 0\n"
"v_cndmask_b32 v221,0,v226,vcc\n"
AW "a248,0\n"
MF "v[48:51],a[116:119],v[116:119],v[48:51]\n"
AW "a249,0\n"
AW "a250,v220\n"
MF "v[52:55],a[116:119],v[140:143],v[52:55]\n"
AW "a251,v221\n"
AW "a252,v220\n"
MF "v[52:55],a[76:79],v[136:139],v[52:55]\n"
"s_waitcnt lgkmcnt(0)\n"
AW "a253,v221\n"
AW "a254,0\n"
MF "v[52:55],a[36:39],v[132:135],v[52:55]\n"
"s_barrier\n"
MF "v[52:55],a[96:99],v[128:131],v[52:55]\n"
"s_add_u32 s80,s88,122880\n"
TR "v[176:177],v0 offset:24576\n"
AW "a255,0\n"
MF "v[52:55],a[56:59],v[124:127],v[52:55]\n"
"s_add_u32 m0,s79,98304\n"
TR "v[178:179],v0 offset:26624\n"
"v_bfe_u32 v12,v9,4,4\n"
MF "v[52:55],a[16:19],v[120:123],v[52:55]\n"
LD "v192,s[48:51],s80 offen lds\n"
MF "v[88:91],a[16:19],v[144:147],v[88:91]\n"
"s_nop 0\n"
TR "v[180:181],v0 offset:28672\n"
"ds_write_addtid_b32 v16 offset:24576\n"
MF "v[88:91],a[56:59],v[148:151],v[88:91]\n"
"s_nop 0\n"
TR "v[182:183],v0 offset:30720\n"
"ds_write_addtid_b32 v17 offset:24832\n"
MF "v[88:91],a[96:99],v[152:155],v[88:91]\n"
LD "v193,s[48:51],s80 offen offset:1024 lds\n"
MF "v[88:91],a[36:39],v[156:159],v[88:91]\n"
"s_nop 0\n"
TR "v[184:185],v0 offset:32768\n"
"ds_write_addtid_b32 v18 offset:25088\n"
MF "v[88:91],a[76:79],v[160:163],v[88:91]\n"
"s_nop 0\n"
TR "v[186:187],v0 offset:34816\n"
"ds_write_addtid_b32 v19 offset:25344\n"
MF "v[88:91],a[116:119],v[164:167],v[88:91]\n"
"s_nop 0\n"
"ds_write_addtid_b32 v20 offset:25600\n"
"ds_write_addtid_b32 v21 offset:25856\n"
MF "v[92:95],a[116:119],v[238:241],v[92:95]\n"
LD "v194,s[48:51],s80 offen offset:2048 lds\n"
MF "v[92:95],a[76:79],v[248:251],v[92:95]\n"
"s_nop 0\n"
"ds_write_addtid_b32 v22 offset:26112\n"
"ds_write_addtid_b32 v23 offset:26368\n"
MF "v[92:95],a[36:39],v[244:247],v[92:95]\n"
"s_nop 0\n"
"ds_write_addtid_b32 v24 offset:26624\n"
"ds_write_addtid_b32 v25 offset:26880\n"
MF "v[92:95],a[96:99],v[204:207],v[92:95]\n"
"s_nop 0\n"
"ds_write_addtid_b32 v26 offset:27136\n"
"ds_write_addtid_b32 v27 offset:27392\n"
MF "v[92:95],a[56:59],v[172:175],v[92:95]\n"
"v_lshlrev_b32 v12,2,v12\n"
"v_lshrrev_b32 v13,2,v9\n"
MF "v[92:95],a[16:19],v[168:171],v[92:95]\n"
"s_waitcnt lgkmcnt(9)\n"
"v_add_u32 v13,s58,v13\n"
"v_add_u32 v4,24576,v4\n"
"v_add_u32 v5,24576,v5\n"
"v_add_u32 v6,24576,v6\n"
"v_add_u32 v7,24576,v7\n"
".Lpl05:\n"
MF "v[96:99],a[0:3],v[176:179],v[252:255]\n"
"s_nop 0\n"
TR "v[188:189],v1 offset:24576\n"
"ds_write_addtid_b32 v28 offset:27648\n"
MF "v[96:99],a[40:43],v[180:183],v[96:99]\n"
"s_nop 0\n"
TR "v[190:191],v1 offset:26624\n"
"ds_write_addtid_b32 v29 offset:27904\n"
MF "v[96:99],a[80:83],v[184:187],v[96:99]\n"
LD "v195,s[48:51],s80 offen offset:3072 lds\n"
MF "v[104:107],a[84:87],v[184:187],v[252:255]\n"
"s_add_u32 s60,s64,1\n"
TR "a[240:241],v1 offset:28672\n"
"ds_write_addtid_b32 v30 offset:28160\n"
MF "v[104:107],a[44:47],v[180:183],v[104:107]\n"
"s_add_u32 m0,s79,102400\n"
TR "a[242:243],v1 offset:30720\n"
"ds_write_addtid_b32 v31 offset:24320\n"
MF "v[104:107],a[4:7],v[176:179],v[104:107]\n"
LD "v196,s[48:51],s80 offen lds\n"
MF "v[112:115],a[8:11],v[176:179],v[252:255]\n"
"s_cmp_eq_u32 s60,8\n"
"s_cselect_b32 s60,0,s60\n"
TR "a[244:245],v1 offset:32768\n"
MF "v[112:115],a[48:51],v[180:183],v[112:115]\n"
"s_cmp_eq_u32 s60,10\n"
"s_cselect_b32 s60,s61,s60\n"
TR "a[246:247],v1 offset:34816\n"
MF "v[112:115],a[88:91],v[184:187],v[112:115]\n"
"s_sub_u32 s65,s60,s64\n"
"ds_write_addtid_b32 v32 offset:24576\n"
"ds_write_addtid_b32 v33 offset:24832\n"
MF "v[120:123],a[92:95],v[184:187],v[252:255]\n"
"s_lshl_b32 s65,s65,7\n"
"ds_write_addtid_b32 v34 offset:25088\n"
"ds_write_addtid_b32 v35 offset:25344\n"
MF "v[120:123],a[52:55],v[180:183],v[120:123]\n"
LD "v197,s[48:51],s80 offen offset:1024 lds\n"
MF "v[120:123],a[12:15],v[176:179],v[120:123]\n"
"s_mov_b32 s64,s60\n"
"ds_write_addtid_b32 v36 offset:25600\n"
"ds_write_addtid_b32 v37 offset:25856\n"
MF "v[128:131],a[16:19],v[176:179],v[252:255]\n"
"s_nop 0\n"
"ds_write_addtid_b32 v38 offset:26112\n"
"ds_write_addtid_b32 v39 offset:26368\n"
MF "v[128:131],a[56:59],v[180:183],v[128:131]\n"
"s_nop 0\n"
"ds_write_addtid_b32 v40 offset:26624\n"
"ds_write_addtid_b32 v41 offset:26880\n"
MF "v[128:131],a[96:99],v[184:187],v[128:131]\n"
"s_waitcnt lgkmcnt(10)\n"
LD "v198,s[48:51],s80 offen offset:2048 lds\n"
MF "v[132:135],a[96:99],a[244:247],v[252:255]\n"
"s_nop 0\n"
TR "v[176:177],v2 offset:24576\n"
"ds_write_addtid_b32 v42 offset:27136\n"
MF "v[132:135],a[56:59],a[240:243],v[132:135]\n"
"s_nop 0\n"
TR "v[178:179],v2 offset:26624\n"
"ds_write_addtid_b32 v43 offset:27392\n"
MF "v[132:135],a[16:19],v[188:191],v[132:135]\n"
"s_nop 0\n"
"ds_write_addtid_b32 v44 offset:27648\n"
"ds_write_addtid_b32 v45 offset:27904\n"
MF "v[124:127],a[12:15],v[188:191],v[252:255]\n"
"s_nop 0\n"
TR "v[180:181],v2 offset:28672\n"
"ds_write_addtid_b32 v46 offset:28160\n"
MF "v[124:127],a[52:55],a[240:243],v[124:127]\n"
"s_nop 0\n"
TR "v[182:183],v2 offset:30720\n"
"ds_write_addtid_b32 v47 offset:28416\n"
MF "v[124:127],a[92:95],a[244:247],v[124:127]\n"
LD "v199,s[48:51],s80 offen offset:3072 lds\n"
MF "v[116:119],a[88:91],a[244:247],v[252:255]\n"
"s_nop 0\n"
TR "v[184:185],v2 offset:32768\n"
"ds_write_addtid_b32 v48 offset:28672\n"
MF "v[116:119],a[48:51],a[240:243],v[116:119]\n"
"s_add_u32 m0,s79,106496\n"
TR "v[186:187],v2 offset:34816\n"
"ds_write_addtid_b32 v49 offset:24832\n"
MF "v[116:119],a[8:11],v[188:191],v[116:119]\n"
LD "v200,s[48:51],s80 offen lds\n"
MF "v[108:111],a[4:7],v[188:191],v[252:255]\n"
"s_nop 0\n"
"ds_write_addtid_b32 v50 offset:25088\n"
"ds_write_addtid_b32 v51 offset:25344\n"
MF "v[108:111],a[44:47],a[240:243],v[108:111]\n"
"s_nop 0\n"
"ds_write_addtid_b32 v52 offset:25600\n"
"ds_write_addtid_b32 v53 offset:25856\n"
MF "v[108:111],a[84:87],a[244:247],v[108:111]\n"
"s_nop 0\n"
"ds_write_addtid_b32 v54 offset:26112\n"
"ds_write_addtid_b32 v55 offset:26368\n"
MF "v[100:103],a[80:83],a[244:247],v[252:255]\n"
MF "v[100:103],a[40:43],a[240:243],v[100:103]\n"
LD "v201,s[48:51],s80 offen offset:1024 lds\n"
MF "v[100:103],a[0:3],v[188:191],v[100:103]\n"
"s_waitcnt lgkmcnt(7)\n"
MF "v[136:139],a[0:3],v[176:179],v[252:255]\n"
TR "v[188:189],v3 offset:24576\n"
MF "v[136:139],a[40:43],v[180:183],v[136:139]\n"
TR "v[190:191],v3 offset:26624\n"
MF "v[136:139],a[80:83],v[184:187],v[136:139]\n"
LD "v202,s[48:51],s80 offen offset:2048 lds\n"
MF "v[144:147],a[84:87],v[184:187],v[252:255]\n"
TR "a[240:241],v3 offset:28672\n"
MF "v[144:147],a[44:47],v[180:183],v[144:147]\n"
TR "a[242:243],v3 offset:30720\n"
MF "v[144:147],a[4:7],v[176:179],v[144:147]\n"
LD "v203,s[48:51],s80 offen offset:3072 lds\n"
MF "v[152:155],a[8:11],v[176:179],v[252:255]\n"
TR "a[244:245],v3 offset:32768\n"
MF "v[152:155],a[48:51],v[180:183],v[152:155]\n"
TR "a[246:247],v3 offset:34816\n"
MF "v[152:155],a[88:91],v[184:187],v[152:155]\n"
MF "v[160:163],a[92:95],v[184:187],v[252:255]\n"
MF "v[160:163],a[52:55],v[180:183],v[160:163]\n"
MF "v[160:163],a[12:15],v[176:179],v[160:163]\n"
MF "v[168:171],a[16:19],v[176:179],v[252:255]\n"
MF "v[168:171],a[56:59],v[180:183],v[168:171]\n"
"s_waitcnt vmcnt(24) lgkmcnt(0)\n"
MF "v[168:171],a[96:99],v[184:187],v[168:171]\n"
"s_barrier\n"
MF "v[172:175],a[96:99],a[244:247],v[252:255]\n"
"s_add_u32 s89,s89,s65\n"
TR "v[176:177],v0\n"
MF "v[172:175],a[56:59],a[240:243],v[172:175]\n"
"s_add_u32 m0,s79,24576\n"
TR "v[178:179],v0 offset:2048\n"
MF "v[172:175],a[16:19],v[188:191],v[172:175]\n"
LD "v192,s[48:51],s89 offen lds\n"
MF "v[164:167],a[12:15],v[188:191],v[252:255]\n"
TR "v[180:181],v0 offset:4096\n"
MF "v[164:167],a[52:55],a[240:243],v[164:167]\n"
TR "v[182:183],v0 offset:6144\n"
MF "v[164:167],a[92:95],a[244:247],v[164:167]\n"
LD "v193,s[48:51],s89 offen offset:1024 lds\n"
MF "v[156:159],a[88:91],a[244:247],v[252:255]\n"
TR "v[184:185],v0 offset:8192\n"
MF "v[156:159],a[48:51],a[240:243],v[156:159]\n"
"s_nop 0\n"
TR "v[186:187],v0 offset:10240\n"
"ds_write_addtid_b32 v56 offset:49152\n"
MF "v[156:159],a[8:11],v[188:191],v[156:159]\n"
"s_nop 0\n"
"ds_write_addtid_b32 v57 offset:49408\n"
"ds_write_addtid_b32 v58 offset:49664\n"
MF "v[148:151],a[4:7],v[188:191],v[252:255]\n"
"s_nop 0\n"
"ds_write_addtid_b32 v59 offset:49920\n"
"ds_write_addtid_b32 v60 offset:50176\n"
MF "v[148:151],a[44:47],a[240:243],v[148:151]\n"
LD "v194,s[48:51],s89 offen offset:2048 lds\n"
MF "v[148:151],a[84:87],a[244:247],v[148:151]\n"
"s_nop 0\n"
"ds_write_addtid_b32 v61 offset:50432\n"
"ds_write_addtid_b32 v62 offset:50688\n"
MF "v[140:143],a[80:83],a[244:247],v[252:255]\n"
"s_nop 0\n"
"ds_write_addtid_b32 v63 offset:50944\n"
"ds_write_addtid_b32 v64 offset:51200\n"
MF "v[140:143],a[40:43],a[240:243],v[140:143]\n"
"s_nop 0\n"
"ds_write_addtid_b32 v65 offset:51456\n"
"ds_write_addtid_b32 v66 offset:51712\n"
MF "v[140:143],a[0:3],v[188:191],v[140:143]\n"
"s_waitcnt lgkmcnt(11)\n"
LD "v195,s[48:51],s89 offen offset:3072 lds\n"
MF "v[96:99],a[120:123],v[176:179],v[96:99]\n"
"s_nop 0\n"
TR "v[188:189],v1\n"
"ds_write_addtid_b32 v67 offset:51968\n"
MF "v[96:99],a[160:163],v[180:183],v[96:99]\n"
"s_nop 0\n"
TR "v[190:191],v1 offset:2048\n"
"ds_write_addtid_b32 v68 offset:52224\n"
MF "v[96:99],a[200:203],v[184:187],v[96:99]\n"
"s_nop 0\n"
"ds_write_addtid_b32 v69 offset:52480\n"
"ds_write_addtid_b32 v70 offset:52736\n"
MF "v[104:107],a[204:207],v[184:187],v[104:107]\n"
"s_nop 0\n"
TR "a[240:241],v1 offset:4096\n"
"ds_write_addtid_b32 v71 offset:52992\n"
MF "v[104:107],a[164:167],v[180:183],v[104:107]\n"
".Lpg23h:\n"
"s_cbranch_execz .Lpg27h\n"
"s_add_u32 m0,s79,28672\n"
TR "a[242:243],v1 offset:6144\n"
"ds_write_addtid_b32 v72 offset:49152\n"
MF "v[104:107],a[124:127],v[176:179],v[104:107]\n"
LD "v196,s[48:51],s89 offen lds\n"
MF "v[112:115],a[128:131],v[176:179],v[112:115]\n"
"s_nop 0\n"
TR "a[244:245],v1 offset:8192\n"
"ds_write_addtid_b32 v73 offset:49408\n"
MF "v[112:115],a[168:171],v[180:183],v[112:115]\n"
"s_nop 0\n"
TR "a[246:247],v1 offset:10240\n"
"ds_write_addtid_b32 v74 offset:49664\n"
MF "v[112:115],a[208:211],v[184:187],v[112:115]\n"
LD "v197,s[48:51],s89 offen offset:1024 lds\n"
MF "v[120:123],a[212:215],v[184:187],v[120:123]\n"
"s_nop 0\n"
"ds_write_addtid_b32 v75 offset:49920\n"
"ds_write_addtid_b32 v76 offset:50176\n"
MF "v[120:123],a[172:175],v[180:183],v[120:123]\n"
"s_nop 0\n"
"ds_write_addtid_b32 v77 offset:50432\n"
"ds_write_addtid_b32 v78 offset:50688\n"
MF "v[120:123],a[132:135],v[176:179],v[120:123]\n"
"s_nop 0\n"
"ds_write_addtid_b32 v79 offset:50944\n"
"ds_write_addtid_b32 v80 offset:51200\n"
MF "v[128:131],a[136:139],v[176:179],v[128:131]\n"
"s_nop 0\n"
"ds_write_addtid_b32 v81 offset:51456\n"
"ds_write_addtid_b32 v82 offset:51712\n"
MF "v[128:131],a[176:179],v[180:183],v[128:131]\n"
LD "v198,s[48:51],s89 offen offset:2048 lds\n"
MF "v[128:131],a[216:219],v[184:187],v[128:131]\n"
"s_waitcnt lgkmcnt(9)\n"
"ds_write_addtid_b32 v83 offset:51968\n"
"ds_write_addtid_b32 v84 offset:52224\n"
MF "v[132:135],a[216:219],a[244:247],v[132:135]\n"
"s_nop 0\n"
TR "v[176:177],v2\n"
"ds_write_addtid_b32 v85 offset:52480\n"
MF "v[132:135],a[176:179],a[240:243],v[132:135]\n"
"s_nop 0\n"
TR "v[178:179],v2 offset:2048\n"
"ds_write_addtid_b32 v86 offset:52736\n"
MF "v[132:135],a[136:139],v[188:191],v[132:135]\n"
LD "v199,s[48:51],s89 offen offset:3072 lds\n"
MF "v[124:127],a[132:135],v[188:191],v[124:127]\n"
"s_nop 0\n"
TR "v[180:181],v2 offset:4096\n"
"ds_write_addtid_b32 v87 offset:52992\n"
MF "v[124:127],a[172:175],a[240:243],v[124:127]\n"
"s_nop 0\n"
TR "v[182:183],v2 offset:6144\n"
"ds_write_addtid_b32 v88 offset:53248\n"
MF "v[124:127],a[212:215],a[244:247],v[124:127]\n"
"s_nop 0\n"
"ds_write_addtid_b32 v89 offset:53504\n"
"ds_write_addtid_b32 v90 offset:53760\n"
MF "v[116:119],a[208:211],a[244:247],v[116:119]\n"
"s_nop 0\n"
TR "v[184:185],v2 offset:8192\n"
"ds_write_addtid_b32 v91 offset:54016\n"
MF "v[116:119],a[168:171],a[240:243],v[116:119]\n"
"s_add_u32 m0,s79,32768\n"
TR "v[186:187],v2 offset:10240\n"
"ds_write_addtid_b32 v92 offset:50176\n"
MF "v[116:119],a[128:131],v[188:191],v[116:119]\n"
LD "v200,s[48:51],s89 offen lds\n"
MF "v[108:111],a[124:127],v[188:191],v[108:111]\n"
"s_nop 0\n"
"ds_write_addtid_b32 v93 offset:50432\n"
"ds_write_addtid_b32 v94 offset:50688\n"
MF "v[108:111],a[164:167],a[240:243],v[108:111]\n"
"ds_write_addtid_b32 v95 offset:50944\n"
MF "v[108:111],a[204:207],a[244:247],v[108:111]\n"
MF "v[100:103],a[200:203],a[244:247],v[100:103]\n"
LD "v201,s[48:51],s89 offen offset:1024 lds\n"
MF "v[100:103],a[160:163],a[240:243],v[100:103]\n"
MF "v[100:103],a[120:123],v[188:191],v[100:103]\n"
"s_waitcnt lgkmcnt(4)\n"
MF "v[136:139],a[120:123],v[176:179],v[136:139]\n"
TR "v[188:189],v3\n"
MF "v[136:139],a[160:163],v[180:183],v[136:139]\n"
TR "v[190:191],v3 offset:2048\n"
MF "v[136:139],a[200:203],v[184:187],v[136:139]\n"
LD "v202,s[48:51],s89 offen offset:2048 lds\n"
MF "v[144:147],a[204:207],v[184:187],v[144:147]\n"
TR "a[240:241],v3 offset:4096\n"
MF "v[144:147],a[164:167],v[180:183],v[144:147]\n"
TR "a[242:243],v3 offset:6144\n"
MF "v[144:147],a[124:127],v[176:179],v[144:147]\n"
LD "v203,s[48:51],s89 offen offset:3072 lds\n"
MF "v[152:155],a[128:131],v[176:179],v[152:155]\n"
TR "a[244:245],v3 offset:8192\n"
MF "v[152:155],a[168:171],v[180:183],v[152:155]\n"
TR "a[246:247],v3 offset:10240\n"
MF "v[152:155],a[208:211],v[184:187],v[152:155]\n"
MF "v[160:163],a[212:215],v[184:187],v[160:163]\n"
MF "v[160:163],a[172:175],v[180:183],v[160:163]\n"
MF "v[160:163],a[132:135],v[176:179],v[160:163]\n"
MF "v[168:171],a[136:139],v[176:179],v[168:171]\n"
MF "v[168:171],a[176:179],v[180:183],v[168:171]\n"
"s_waitcnt vmcnt(24) lgkmcnt(0)\n"
MF "v[168:171],a[216:219],v[184:187],v[168:171]\n"
"s_barrier\n"
MF "v[172:175],a[216:219],a[244:247],v[172:175]\n"
"s_add_u32 s80,s89,122880\n"
TR "v[176:177],v0 offset:49152\n"
MF "v[172:175],a[176:179],a[240:243],v[172:175]\n"
"s_add_u32 m0,s79,0\n"
TR "v[178:179],v0 offset:51200\n"
MF "v[172:175],a[136:139],v[188:191],v[172:175]\n"
LD "v192,s[48:51],s80 offen lds\n"
MF "v[164:167],a[132:135],v[188:191],v[164:167]\n"
TR "v[180:181],v0 offset:53248\n"
MF "v[164:167],a[172:175],a[240:243],v[164:167]\n"
TR "v[182:183],v0 offset:55296\n"
MF "v[164:167],a[212:215],a[244:247],v[164:167]\n"
LD "v193,s[48:51],s80 offen offset:1024 lds\n"
MF "v[156:159],a[208:211],a[244:247],v[156:159]\n"
TR "v[184:185],v0 offset:57344\n"
MF "v[156:159],a[168:171],a[240:243],v[156:159]\n"
TR "v[186:187],v0 offset:59392\n"
MF "v[156:159],a[128:131],v[188:191],v[156:159]\n"
MF "v[148:151],a[124:127],v[188:191],v[148:151]\n"
MF "v[148:151],a[164:167],a[240:243],v[148:151]\n"
LD "v194,s[48:51],s80 offen offset:2048 lds\n"
MF "v[148:151],a[204:207],a[244:247],v[148:151]\n"
MF "v[140:143],a[200:203],a[244:247],v[140:143]\n"
MF "v[140:143],a[160:163],a[240:243],v[140:143]\n"
MF "v[140:143],a[120:123],v[188:191],v[140:143]\n"
"s_waitcnt lgkmcnt(0)\n"
LD "v195,s[48:51],s80 offen offset:3072 lds\n"
MF "v[96:99],a[20:23],v[176:179],v[96:99]\n"
TR "v[188:189],v1 offset:49152\n"
MF "v[96:99],a[60:63],v[180:183],v[96:99]\n"
TR "v[190:191],v1 offset:51200\n"
MF "v[96:99],a[100:103],v[184:187],v[96:99]\n"
MF "v[104:107],a[104:107],v[184:187],v[104:107]\n"
TR "a[240:241],v1 offset:53248\n"
MF "v[104:107],a[64:67],v[180:183],v[104:107]\n"
"s_add_u32 m0,s79,4096\n"
TR "a[242:243],v1 offset:55296\n"
MF "v[104:107],a[24:27],v[176:179],v[104:107]\n"
LD "v196,s[48:51],s80 offen lds\n"
MF "v[112:115],a[28:31],v[176:179],v[112:115]\n"
TR "a[244:245],v1 offset:57344\n"
MF "v[112:115],a[68:71],v[180:183],v[112:115]\n"
TR "a[246:247],v1 offset:59392\n"
MF "v[112:115],a[108:111],v[184:187],v[112:115]\n"
LD "v197,s[48:51],s80 offen offset:1024 lds\n"
MF "v[120:123],a[112:115],v[184:187],v[120:123]\n"
MF "v[120:123],a[72:75],v[180:183],v[120:123]\n"
MF "v[120:123],a[32:35],v[176:179],v[120:123]\n"
MF "v[128:131],a[36:39],v[176:179],v[128:131]\n"
MF "v[128:131],a[76:79],v[180:183],v[128:131]\n"
LD "v198,s[48:51],s80 offen offset:2048 lds\n"
MF "v[128:131],a[116:119],v[184:187],v[128:131]\n"
"s_waitcnt lgkmcnt(0)\n"
MF "v[132:135],a[116:119],a[244:247],v[132:135]\n"
TR "v[176:177],v2 offset:49152\n"
MF "v[132:135],a[76:79],a[240:243],v[132:135]\n"
TR "v[178:179],v2 offset:51200\n"
MF "v[132:135],a[36:39],v[188:191],v[132:135]\n"
LD "v199,s[48:51],s80 offen offset:3072 lds\n"
MF "v[124:127],a[32:35],v[188:191],v[124:127]\n"
TR "v[180:181],v2 offset:53248\n"
MF "v[124:127],a[72:75],a[240:243],v[124:127]\n"
TR "v[182:183],v2 offset:55296\n"
MF "v[124:127],a[112:115],a[244:247],v[124:127]\n"
MF "v[116:119],a[108:111],a[244:247],v[116:119]\n"
TR "v[184:185],v2 offset:57344\n"
MF "v[116:119],a[68:71],a[240:243],v[116:119]\n"
"s_add_u32 m0,s79,8192\n"
TR "v[186:187],v2 offset:59392\n"
MF "v[116:119],a[28:31],v[188:191],v[116:119]\n"
LD "v200,s[48:51],s80 offen lds\n"
MF "v[108:111],a[24:27],v[188:191],v[108:111]\n"
MF "v[108:111],a[64:67],a[240:243],v[108:111]\n"
MF "v[108:111],a[104:107],a[244:247],v[108:111]\n"
MF "v[100:103],a[100:103],a[244:247],v[100:103]\n"
LD "v201,s[48:51],s80 offen offset:1024 lds\n"
MF "v[100:103],a[60:63],a[240:243],v[100:103]\n"
MF "v[100:103],a[20:23],v[188:191],v[100:103]\n"
"s_waitcnt lgkmcnt(0)\n"
MF "v[136:139],a[20:23],v[176:179],v[136:139]\n"
TR "v[188:189],v3 offset:49152\n"
MF "v[136:139],a[60:63],v[180:183],v[136:139]\n"
TR "v[190:191],v3 offset:51200\n"
MF "v[136:139],a[100:103],v[184:187],v[136:139]\n"
LD "v202,s[48:51],s80 offen offset:2048 lds\n"
MF "v[144:147],a[104:107],v[184:187],v[144:147]\n"
TR "a[240:241],v3 offset:53248\n"
MF "v[144:147],a[64:67],v[180:183],v[144:147]\n"
TR "a[242:243],v3 offset:55296\n"
MF "v[144:147],a[24:27],v[176:179],v[144:147]\n"
LD "v203,s[48:51],s80 offen offset:3072 lds\n"
MF "v[152:155],a[28:31],v[176:179],v[152:155]\n"
TR "a[244:245],v3 offset:57344\n"
MF "v[152:155],a[68:71],v[180:183],v[152:155]\n"
TR "a[246:247],v3 offset:59392\n"
MF "v[152:155],a[108:111],v[184:187],v[152:155]\n"
MF "v[160:163],a[112:115],v[184:187],v[160:163]\n"
MF "v[160:163],a[72:75],v[180:183],v[160:163]\n"
MF "v[160:163],a[32:35],v[176:179],v[160:163]\n"
MF "v[168:171],a[36:39],v[176:179],v[168:171]\n"
MF "v[168:171],a[76:79],v[180:183],v[168:171]\n"
"s_waitcnt vmcnt(24) lgkmcnt(0)\n"
MF "v[168:171],a[116:119],v[184:187],v[168:171]\n"
"s_barrier\n"
MF "v[172:175],a[116:119],a[244:247],v[172:175]\n"
"s_add_u32 s88,s88,s65\n"
TR "v[176:177],v4\n"
MF "v[172:175],a[76:79],a[240:243],v[172:175]\n"
"s_add_u32 m0,s79,49152\n"
TR "v[178:179],v4 offset:2048\n"
MF "v[172:175],a[36:39],v[188:191],v[172:175]\n"
LD "v192,s[48:51],s88 offen lds\n"
MF "v[164:167],a[32:35],v[188:191],v[164:167]\n"
TR "v[180:181],v4 offset:4096\n"
MF "v[164:167],a[72:75],a[240:243],v[164:167]\n"
TR "v[182:183],v4 offset:6144\n"
MF "v[164:167],a[112:115],a[244:247],v[164:167]\n"
LD "v193,s[48:51],s88 offen offset:1024 lds\n"
MF "v[156:159],a[108:111],a[244:247],v[156:159]\n"
TR "v[184:185],v4 offset:8192\n"
MF "v[156:159],a[68:71],a[240:243],v[156:159]\n"
TR "v[186:187],v4 offset:10240\n"
MF "v[156:159],a[28:31],v[188:191],v[156:159]\n"
MF "v[148:151],a[24:27],v[188:191],v[148:151]\n"
MF "v[148:151],a[64:67],a[240:243],v[148:151]\n"
LD "v194,s[48:51],s88 offen offset:2048 lds\n"
MF "v[148:151],a[104:107],a[244:247],v[148:151]\n"
MF "v[140:143],a[100:103],a[244:247],v[140:143]\n"
MF "v[140:143],a[60:63],a[240:243],v[140:143]\n"
MF "v[140:143],a[20:23],v[188:191],v[140:143]\n"
"s_waitcnt lgkmcnt(0)\n"
LD "v195,s[48:51],s88 offen offset:3072 lds\n"
MF "v[96:99],a[140:143],v[176:179],v[96:99]\n"
TR "v[188:189],v5\n"
MF "v[96:99],a[180:183],v[180:183],v[96:99]\n"
TR "v[190:191],v5 offset:2048\n"
MF "v[96:99],a[220:223],v[184:187],v[96:99]\n"
MF "v[104:107],a[224:227],v[184:187],v[104:107]\n"
TR "a[240:241],v5 offset:4096\n"
MF "v[104:107],a[184:187],v[180:183],v[104:107]\n"
"s_add_u32 m0,s79,53248\n"
TR "a[242:243],v5 offset:6144\n"
MF "v[104:107],a[144:147],v[176:179],v[104:107]\n"
LD "v196,s[48:51],s88 offen lds\n"
MF "v[112:115],a[148:151],v[176:179],v[112:115]\n"
TR "a[244:245],v5 offset:8192\n"
MF "v[112:115],a[188:191],v[180:183],v[112:115]\n"
TR "a[246:247],v5 offset:10240\n"
MF "v[112:115],a[228:231],v[184:187],v[112:115]\n"
LD "v197,s[48:51],s88 offen offset:1024 lds\n"
MF "v[120:123],a[232:235],v[184:187],v[120:123]\n"
MF "v[120:123],a[192:195],v[180:183],v[120:123]\n"
MF "v[120:123],a[152:155],v[176:179],v[120:123]\n"
MF "v[128:131],a[156:159],v[176:179],v[128:131]\n"
MF "v[128:131],a[196:199],v[180:183],v[128:131]\n"
LD "v198,s[48:51],s88 offen offset:2048 lds\n"
MF "v[128:131],a[236:239],v[184:187],v[128:131]\n"
"s_waitcnt lgkmcnt(0)\n"
MF "v[132:135],a[236:239],a[244:247],v[132:135]\n"
TR "v[176:177],v6\n"
MF "v[132:135],a[196:199],a[240:243],v[132:135]\n"
TR "v[178:179],v6 offset:2048\n"
MF "v[132:135],a[156:159],v[188:191],v[132:135]\n"
LD "v199,s[48:51],s88 offen offset:3072 lds\n"
MF "v[124:127],a[152:155],v[188:191],v[124:127]\n"
TR "v[180:181],v6 offset:4096\n"
MF "v[124:127],a[192:195],a[240:243],v[124:127]\n"
TR "v[182:183],v6 offset:6144\n"
MF "v[124:127],a[232:235],a[244:247],v[124:127]\n"
MF "v[116:119],a[228:231],a[244:247],v[116:119]\n"
TR "v[184:185],v6 offset:8192\n"
MF "v[116:119],a[188:191],a[240:243],v[116:119]\n"
"s_add_u32 m0,s79,57344\n"
TR "v[186:187],v6 offset:10240\n"
MF "v[116:119],a[148:151],v[188:191],v[116:119]\n"
LD "v200,s[48:51],s88 offen lds\n"
MF "v[108:111],a[144:147],v[188:191],v[108:111]\n"
MF "v[108:111],a[184:187],a[240:243],v[108:111]\n"
MF "v[108:111],a[224:227],a[244:247],v[108:111]\n"
MF "v[100:103],a[220:223],a[244:247],v[100:103]\n"
LD "v201,s[48:51],s88 offen offset:1024 lds\n"
MF "v[100:103],a[180:183],a[240:243],v[100:103]\n"
MF "v[100:103],a[140:143],v[188:191],v[100:103]\n"
"s_waitcnt lgkmcnt(0)\n"
MF "v[136:139],a[140:143],v[176:179],v[136:139]\n"
TR "v[188:189],v7\n"
MF "v[136:139],a[180:183],v[180:183],v[136:139]\n"
TR "v[190:191],v7 offset:2048\n"
MF "v[136:139],a[220:223],v[184:187],v[136:139]\n"
LD "v202,s[48:51],s88 offen offset:2048 lds\n"
MF "v[144:147],a[224:227],v[184:187],v[144:147]\n"
TR "a[240:241],v7 offset:4096\n"
MF "v[144:147],a[184:187],v[180:183],v[144:147]\n"
TR "a[242:243],v7 offset:6144\n"
MF "v[144:147],a[144:147],v[176:179],v[144:147]\n"
LD "v203,s[48:51],s88 offen offset:3072 lds\n"
MF "v[152:155],a[148:151],v[176:179],v[152:155]\n"
TR "a[244:245],v7 offset:8192\n"
MF "v[152:155],a[188:191],v[180:183],v[152:155]\n"
TR "a[246:247],v7 offset:10240\n"
MF "v[152:155],a[228:231],v[184:187],v[152:155]\n"
MF "v[160:163],a[232:235],v[184:187],v[160:163]\n"
MF "v[160:163],a[192:195],v[180:183],v[160:163]\n"
MF "v[160:163],a[152:155],v[176:179],v[160:163]\n"
MF "v[168:171],a[156:159],v[176:179],v[168:171]\n"
MF "v[168:171],a[196:199],v[180:183],v[168:171]\n"
"s_waitcnt vmcnt(24) lgkmcnt(0)\n"
MF "v[168:171],a[236:239],v[184:187],v[168:171]\n"
"s_barrier\n"
MF "v[172:175],a[236:239],a[244:247],v[172:175]\n"
"s_add_u32 s80,s88,122880\n"
TR "v[176:177],v0 offset:24576\n"
MF "v[172:175],a[196:199],a[240:243],v[172:175]\n"
"s_add_u32 m0,s79,98304\n"
"s_add_u32 s85,s85,1\n"
TR "v[178:179],v0 offset:26624\n"
MF "v[172:175],a[156:159],v[188:191],v[172:175]\n"
LD "v192,s[48:51],s80 offen lds\n"
MF "v[164:167],a[152:155],v[188:191],v[164:167]\n"
"s_nop 0\n"
TR "v[180:181],v0 offset:28672\n"
"ds_write_addtid_b32 v96 offset:24576\n"
MF "v[164:167],a[192:195],a[240:243],v[164:167]\n"
"s_nop 0\n"
TR "v[182:183],v0 offset:30720\n"
"ds_write_addtid_b32 v97 offset:24832\n"
MF "v[164:167],a[232:235],a[244:247],v[164:167]\n"
LD "v193,s[48:51],s80 offen offset:1024 lds\n"
MF "v[156:159],a[228:231],a[244:247],v[156:159]\n"
"s_nop 0\n"
TR "v[184:185],v0 offset:32768\n"
"ds_write_addtid_b32 v98 offset:25088\n"
MF "v[156:159],a[188:191],a[240:243],v[156:159]\n"
"s_nop 0\n"
TR "v[186:187],v0 offset:34816\n"
"ds_write_addtid_b32 v99 offset:25344\n"
MF "v[156:159],a[148:151],v[188:191],v[156:159]\n"
"s_nop 0\n"
"ds_write_addtid_b32 v100 offset:25600\n"
"ds_write_addtid_b32 v101 offset:25856\n"
MF "v[148:151],a[144:147],v[188:191],v[148:151]\n"
LD "v194,s[48:51],s80 offen offset:2048 lds\n"
MF "v[148:151],a[184:187],a[240:243],v[148:151]\n"
"s_nop 0\n"
"ds_write_addtid_b32 v102 offset:26112\n"
"ds_write_addtid_b32 v103 offset:26368\n"
MF "v[148:151],a[224:227],a[244:247],v[148:151]\n"
"s_nop 0\n"
"ds_write_addtid_b32 v104 offset:26624\n"
"ds_write_addtid_b32 v105 offset:26880\n"
MF "v[140:143],a[220:223],a[244:247],v[140:143]\n"
"s_nop 0\n"
"ds_write_addtid_b32 v106 offset:27136\n"
"ds_write_addtid_b32 v107 offset:27392\n"
MF "v[140:143],a[180:183],a[240:243],v[140:143]\n"
MF "v[140:143],a[140:143],v[188:191],v[140:143]\n"
"s_waitcnt lgkmcnt(9)\n"
MF "v[16:19],a[0:3],v[176:179],v[252:255]\n"
"s_nop 0\n"
TR "v[188:189],v1 offset:24576\n"
"ds_write_addtid_b32 v108 offset:27648\n"
MF "v[16:19],a[40:43],v[180:183],v[16:19]\n"
"s_nop 0\n"
TR "v[190:191],v1 offset:26624\n"
"ds_write_addtid_b32 v109 offset:27904\n"
MF "v[16:19],a[80:83],v[184:187],v[16:19]\n"
LD "v195,s[48:51],s80 offen offset:3072 lds\n"
MF "v[24:27],a[84:87],v[184:187],v[252:255]\n"
"s_add_u32 s60,s64,1\n"
TR "a[240:241],v1 offset:28672\n"
"ds_write_addtid_b32 v110 offset:28160\n"
MF "v[24:27],a[44:47],v[180:183],v[24:27]\n"
"s_add_u32 m0,s79,102400\n"
TR "a[242:243],v1 offset:30720\n"
"ds_write_addtid_b32 v111 offset:24320\n"
MF "v[24:27],a[4:7],v[176:179],v[24:27]\n"
LD "v196,s[48:51],s80 offen lds\n"
MF "v[32:35],a[8:11],v[176:179],v[252:255]\n"
"s_cmp_eq_u32 s60,8\n"
"s_cselect_b32 s60,0,s60\n"
TR "a[244:245],v1 offset:32768\n"
MF "v[32:35],a[48:51],v[180:183],v[32:35]\n"
"s_cmp_eq_u32 s60,10\n"
"s_cselect_b32 s60,s61,s60\n"
TR "a[246:247],v1 offset:34816\n"
MF "v[32:35],a[88:91],v[184:187],v[32:35]\n"
"s_sub_u32 s65,s60,s64\n"
"ds_write_addtid_b32 v112 offset:24576\n"
"ds_write_addtid_b32 v113 offset:24832\n"
MF "v[40:43],a[92:95],v[184:187],v[252:255]\n"
"s_lshl_b32 s65,s65,7\n"
"ds_write_addtid_b32 v114 offset:25088\n"
"ds_write_addtid_b32 v115 offset:25344\n"
MF "v[40:43],a[52:55],v[180:183],v[40:43]\n"
LD "v197,s[48:51],s80 offen offset:1024 lds\n"
MF "v[40:43],a[12:15],v[176:179],v[40:43]\n"
"s_mov_b32 s64,s60\n"
"ds_write_addtid_b32 v116 offset:25600\n"
"ds_write_addtid_b32 v117 offset:25856\n"
MF "v[48:51],a[16:19],v[176:179],v[252:255]\n"
"s_nop 0\n"
"ds_write_addtid_b32 v118 offset:26112\n"
"ds_write_addtid_b32 v119 offset:26368\n"
MF "v[48:51],a[56:59],v[180:183],v[48:51]\n"
"s_nop 0\n"
"ds_write_addtid_b32 v120 offset:26624\n"
"ds_write_addtid_b32 v121 offset:26880\n"
MF "v[48:51],a[96:99],v[184:187],v[48:51]\n"
"s_waitcnt lgkmcnt(10)\n"
LD "v198,s[48:51],s80 offen offset:2048 lds\n"
MF "v[52:55],a[96:99],a[244:247],v[252:255]\n"
"s_nop 0\n"
TR "v[176:177],v2 offset:24576\n"
"ds_write_addtid_b32 v122 offset:27136\n"
MF "v[52:55],a[56:59],a[240:243],v[52:55]\n"
"s_nop 0\n"
TR "v[178:179],v2 offset:26624\n"
"ds_write_addtid_b32 v123 offset:27392\n"
MF "v[52:55],a[16:19],v[188:191],v[52:55]\n"
"s_nop 0\n"
"ds_write_addtid_b32 v124 offset:27648\n"
"ds_write_addtid_b32 v125 offset:27904\n"
MF "v[44:47],a[12:15],v[188:191],v[252:255]\n"
"s_nop 0\n"
TR "v[180:181],v2 offset:28672\n"
"ds_write_addtid_b32 v126 offset:28160\n"
MF "v[44:47],a[52:55],a[240:243],v[44:47]\n"
"s_nop 0\n"
TR "v[182:183],v2 offset:30720\n"
"ds_write_addtid_b32 v127 offset:28416\n"
MF "v[44:47],a[92:95],a[244:247],v[44:47]\n"
LD "v199,s[48:51],s80 offen offset:3072 lds\n"
MF "v[36:39],a[88:91],a[244:247],v[252:255]\n"
"s_nop 0\n"
TR "v[184:185],v2 offset:32768\n"
"ds_write_addtid_b32 v128 offset:28672\n"
MF "v[36:39],a[48:51],a[240:243],v[36:39]\n"
"s_add_u32 m0,s79,106496\n"
TR "v[186:187],v2 offset:34816\n"
"ds_write_addtid_b32 v129 offset:24832\n"
MF "v[36:39],a[8:11],v[188:191],v[36:39]\n"
LD "v200,s[48:51],s80 offen lds\n"
MF "v[28:31],a[4:7],v[188:191],v[252:255]\n"
"s_nop 0\n"
"ds_write_addtid_b32 v130 offset:25088\n"
"ds_write_addtid_b32 v131 offset:25344\n"
MF "v[28:31],a[44:47],a[240:243],v[28:31]\n"
"s_nop 0\n"
"ds_write_addtid_b32 v132 offset:25600\n"
"ds_write_addtid_b32 v133 offset:25856\n"
MF "v[28:31],a[84:87],a[244:247],v[28:31]\n"
"s_nop 0\n"
"ds_write_addtid_b32 v134 offset:26112\n"
"ds_write_addtid_b32 v135 offset:26368\n"
MF "v[20:23],a[80:83],a[244:247],v[252:255]\n"
MF "v[20:23],a[40:43],a[240:243],v[20:23]\n"
LD "v201,s[48:51],s80 offen offset:1024 lds\n"
MF "v[20:23],a[0:3],v[188:191],v[20:23]\n"
"s_waitcnt lgkmcnt(7)\n"
MF "v[56:59],a[0:3],v[176:179],v[252:255]\n"
TR "v[188:189],v3 offset:24576\n"
MF "v[56:59],a[40:43],v[180:183],v[56:59]\n"
TR "v[190:191],v3 offset:26624\n"
MF "v[56:59],a[80:83],v[184:187],v[56:59]\n"
LD "v202,s[48:51],s80 offen offset:2048 lds\n"
MF "v[64:67],a[84:87],v[184:187],v[252:255]\n"
TR "a[240:241],v3 offset:28672\n"
MF "v[64:67],a[44:47],v[180:183],v[64:67]\n"
TR "a[242:243],v3 offset:30720\n"
MF "v[64:67],a[4:7],v[176:179],v[64:67]\n"
LD "v203,s[48:51],s80 offen offset:3072 lds\n"
MF "v[72:75],a[8:11],v[176:179],v[252:255]\n"
TR "a[244:245],v3 offset:32768\n"
MF "v[72:75],a[48:51],v[180:183],v[72:75]\n"
TR "a[246:247],v3 offset:34816\n"
MF "v[72:75],a[88:91],v[184:187],v[72:75]\n"
MF "v[80:83],a[92:95],v[184:187],v[252:255]\n"
MF "v[80:83],a[52:55],v[180:183],v[80:83]\n"
MF "v[80:83],a[12:15],v[176:179],v[80:83]\n"
MF "v[88:91],a[16:19],v[176:179],v[252:255]\n"
MF "v[88:91],a[56:59],v[180:183],v[88:91]\n"
"s_waitcnt vmcnt(24) lgkmcnt(0)\n"
MF "v[88:91],a[96:99],v[184:187],v[88:91]\n"
"s_barrier\n"
MF "v[92:95],a[96:99],a[244:247],v[252:255]\n"
"s_add_u32 s89,s89,s65\n"
TR "v[176:177],v0\n"
MF "v[92:95],a[56:59],a[240:243],v[92:95]\n"
"s_add_u32 m0,s79,24576\n"
TR "v[178:179],v0 offset:2048\n"
MF "v[92:95],a[16:19],v[188:191],v[92:95]\n"
LD "v192,s[48:51],s89 offen lds\n"
MF "v[84:87],a[12:15],v[188:191],v[252:255]\n"
TR "v[180:181],v0 offset:4096\n"
MF "v[84:87],a[52:55],a[240:243],v[84:87]\n"
TR "v[182:183],v0 offset:6144\n"
MF "v[84:87],a[92:95],a[244:247],v[84:87]\n"
LD "v193,s[48:51],s89 offen offset:1024 lds\n"
MF "v[76:79],a[88:91],a[244:247],v[252:255]\n"
TR "v[184:185],v0 offset:8192\n"
MF "v[76:79],a[48:51],a[240:243],v[76:79]\n"
"s_nop 0\n"
TR "v[186:187],v0 offset:10240\n"
"ds_write_addtid_b32 v136 offset:49152\n"
MF "v[76:79],a[8:11],v[188:191],v[76:79]\n"
"s_nop 0\n"
"ds_write_addtid_b32 v137 offset:49408\n"
"ds_write_addtid_b32 v138 offset:49664\n"
MF "v[68:71],a[4:7],v[188:191],v[252:255]\n"
"s_nop 0\n"
"ds_write_addtid_b32 v139 offset:49920\n"
"ds_write_addtid_b32 v140 offset:50176\n"
MF "v[68:71],a[44:47],a[240:243],v[68:71]\n"
LD "v194,s[48:51],s89 offen offset:2048 lds\n"
MF "v[68:71],a[84:87],a[244:247],v[68:71]\n"
"s_nop 0\n"
"ds_write_addtid_b32 v141 offset:50432\n"
"ds_write_addtid_b32 v142 offset:50688\n"
MF "v[60:63],a[80:83],a[244:247],v[252:255]\n"
"s_nop 0\n"
"ds_write_addtid_b32 v143 offset:50944\n"
"ds_write_addtid_b32 v144 offset:51200\n"
MF "v[60:63],a[40:43],a[240:243],v[60:63]\n"
"s_nop 0\n"
"ds_write_addtid_b32 v145 offset:51456\n"
"ds_write_addtid_b32 v146 offset:51712\n"
MF "v[60:63],a[0:3],v[188:191],v[60:63]\n"
"s_waitcnt lgkmcnt(11)\n"
LD "v195,s[48:51],s89 offen offset:3072 lds\n"
MF "v[16:19],a[120:123],v[176:179],v[16:19]\n"
"s_nop 0\n"
TR "v[188:189],v1\n"
"ds_write_addtid_b32 v147 offset:51968\n"
MF "v[16:19],a[160:163],v[180:183],v[16:19]\n"
"s_nop 0\n"
TR "v[190:191],v1 offset:2048\n"
"ds_write_addtid_b32 v148 offset:52224\n"
MF "v[16:19],a[200:203],v[184:187],v[16:19]\n"
"s_nop 0\n"
"ds_write_addtid_b32 v149 offset:52480\n"
"ds_write_addtid_b32 v150 offset:52736\n"
MF "v[24:27],a[204:207],v[184:187],v[24:27]\n"
".Lpg24h:\n"
"s_cbranch_execz .Lpg28h\n"
"s_nop 0\n"
TR "a[240:241],v1 offset:4096\n"
"ds_write_addtid_b32 v151 offset:52992\n"
MF "v[24:27],a[164:167],v[180:183],v[24:27]\n"
"s_add_u32 m0,s79,28672\n"
TR "a[242:243],v1 offset:6144\n"
"ds_write_addtid_b32 v152 offset:49152\n"
MF "v[24:27],a[124:127],v[176:179],v[24:27]\n"
LD "v196,s[48:51],s89 offen lds\n"
MF "v[32:35],a[128:131],v[176:179],v[32:35]\n"
"s_nop 0\n"
TR "a[244:245],v1 offset:8192\n"
"ds_write_addtid_b32 v153 offset:49408\n"
MF "v[32:35],a[168:171],v[180:183],v[32:35]\n"
"s_nop 0\n"
TR "a[246:247],v1 offset:10240\n"
"ds_write_addtid_b32 v154 offset:49664\n"
MF "v[32:35],a[208:211],v[184:187],v[32:35]\n"
LD "v197,s[48:51],s89 offen offset:1024 lds\n"
MF "v[40:43],a[212:215],v[184:187],v[40:43]\n"
"s_nop 0\n"
"ds_write_addtid_b32 v155 offset:49920\n"
"ds_write_addtid_b32 v156 offset:50176\n"
MF "v[40:43],a[172:175],v[180:183],v[40:43]\n"
"s_nop 0\n"
"ds_write_addtid_b32 v157 offset:50432\n"
"ds_write_addtid_b32 v158 offset:50688\n"
MF "v[40:43],a[132:135],v[176:179],v[40:43]\n"
"s_nop 0\n"
"ds_write_addtid_b32 v159 offset:50944\n"
"ds_write_addtid_b32 v160 offset:51200\n"
MF "v[48:51],a[136:139],v[176:179],v[48:51]\n"
"s_nop 0\n"
"ds_write_addtid_b32 v161 offset:51456\n"
"ds_write_addtid_b32 v162 offset:51712\n"
MF "v[48:51],a[176:179],v[180:183],v[48:51]\n"
LD "v198,s[48:51],s89 offen offset:2048 lds\n"
MF "v[48:51],a[216:219],v[184:187],v[48:51]\n"
"s_waitcnt lgkmcnt(9)\n"
"ds_write_addtid_b32 v163 offset:51968\n"
"ds_write_addtid_b32 v164 offset:52224\n"
MF "v[52:55],a[216:219],a[244:247],v[52:55]\n"
"s_nop 0\n"
TR "v[176:177],v2\n"
"ds_write_addtid_b32 v165 offset:52480\n"
MF "v[52:55],a[176:179],a[240:243],v[52:55]\n"
"s_nop 0\n"
TR "v[178:179],v2 offset:2048\n"
"ds_write_addtid_b32 v166 offset:52736\n"
MF "v[52:55],a[136:139],v[188:191],v[52:55]\n"
LD "v199,s[48:51],s89 offen offset:3072 lds\n"
MF "v[44:47],a[132:135],v[188:191],v[44:47]\n"
"s_nop 0\n"
TR "v[180:181],v2 offset:4096\n"
"ds_write_addtid_b32 v167 offset:52992\n"
MF "v[44:47],a[172:175],a[240:243],v[44:47]\n"
"s_nop 0\n"
TR "v[182:183],v2 offset:6144\n"
"ds_write_addtid_b32 v168 offset:53248\n"
MF "v[44:47],a[212:215],a[244:247],v[44:47]\n"
"s_nop 0\n"
"ds_write_addtid_b32 v169 offset:53504\n"
"ds_write_addtid_b32 v170 offset:53760\n"
MF "v[36:39],a[208:211],a[244:247],v[36:39]\n"
"s_nop 0\n"
TR "v[184:185],v2 offset:8192\n"
"ds_write_addtid_b32 v171 offset:54016\n"
MF "v[36:39],a[168:171],a[240:243],v[36:39]\n"
"s_add_u32 m0,s79,32768\n"
TR "v[186:187],v2 offset:10240\n"
"ds_write_addtid_b32 v172 offset:50176\n"
MF "v[36:39],a[128:131],v[188:191],v[36:39]\n"
LD "v200,s[48:51],s89 offen lds\n"
MF "v[28:31],a[124:127],v[188:191],v[28:31]\n"
"s_nop 0\n"
"ds_write_addtid_b32 v173 offset:50432\n"
"ds_write_addtid_b32 v174 offset:50688\n"
MF "v[28:31],a[164:167],a[240:243],v[28:31]\n"
"ds_write_addtid_b32 v175 offset:50944\n"
MF "v[28:31],a[204:207],a[244:247],v[28:31]\n"
MF "v[20:23],a[200:203],a[244:247],v[20:23]\n"
LD "v201,s[48:51],s89 offen offset:1024 lds\n"
MF "v[20:23],a[160:163],a[240:243],v[20:23]\n"
MF "v[20:23],a[120:123],v[188:191],v[20:23]\n"
"s_waitcnt lgkmcnt(4)\n"
MF "v[56:59],a[120:123],v[176:179],v[56:59]\n"
TR "v[188:189],v3\n"
MF "v[56:59],a[160:163],v[180:183],v[56:59]\n"
TR "v[190:191],v3 offset:2048\n"
MF "v[56:59],a[200:203],v[184:187],v[56:59]\n"
LD "v202,s[48:51],s89 offen offset:2048 lds\n"
MF "v[64:67],a[204:207],v[184:187],v[64:67]\n"
TR "a[240:241],v3 offset:4096\n"
MF "v[64:67],a[164:167],v[180:183],v[64:67]\n"
TR "a[242:243],v3 offset:6144\n"
MF "v[64:67],a[124:127],v[176:179],v[64:67]\n"
LD "v203,s[48:51],s89 offen offset:3072 lds\n"
MF "v[72:75],a[128:131],v[176:179],v[72:75]\n"
TR "a[244:245],v3 offset:8192\n"
MF "v[72:75],a[168:171],v[180:183],v[72:75]\n"
TR "a[246:247],v3 offset:10240\n"
MF "v[72:75],a[208:211],v[184:187],v[72:75]\n"
MF "v[80:83],a[212:215],v[184:187],v[80:83]\n"
MF "v[80:83],a[172:175],v[180:183],v[80:83]\n"
MF "v[80:83],a[132:135],v[176:179],v[80:83]\n"
MF "v[88:91],a[136:139],v[176:179],v[88:91]\n"
MF "v[88:91],a[176:179],v[180:183],v[88:91]\n"
"s_waitcnt vmcnt(24) lgkmcnt(0)\n"
MF "v[88:91],a[216:219],v[184:187],v[88:91]\n"
"s_barrier\n"
MF "v[92:95],a[216:219],a[244:247],v[92:95]\n"
"s_add_u32 s80,s89,122880\n"
TR "v[176:177],v0 offset:49152\n"
MF "v[92:95],a[176:179],a[240:243],v[92:95]\n"
"s_add_u32 m0,s79,0\n"
TR "v[178:179],v0 offset:51200\n"
MF "v[92:95],a[136:139],v[188:191],v[92:95]\n"
LD "v192,s[48:51],s80 offen lds\n"
MF "v[84:87],a[132:135],v[188:191],v[84:87]\n"
TR "v[180:181],v0 offset:53248\n"
MF "v[84:87],a[172:175],a[240:243],v[84:87]\n"
TR "v[182:183],v0 offset:55296\n"
MF "v[84:87],a[212:215],a[244:247],v[84:87]\n"
LD "v193,s[48:51],s80 offen offset:1024 lds\n"
MF "v[76:79],a[208:211],a[244:247],v[76:79]\n"
TR "v[184:185],v0 offset:57344\n"
MF "v[76:79],a[168:171],a[240:243],v[76:79]\n"
TR "v[186:187],v0 offset:59392\n"
MF "v[76:79],a[128:131],v[188:191],v[76:79]\n"
MF "v[68:71],a[124:127],v[188:191],v[68:71]\n"
MF "v[68:71],a[164:167],a[240:243],v[68:71]\n"
LD "v194,s[48:51],s80 offen offset:2048 lds\n"
MF "v[68:71],a[204:207],a[244:247],v[68:71]\n"
MF "v[60:63],a[200:203],a[244:247],v[60:63]\n"
MF "v[60:63],a[160:163],a[240:243],v[60:63]\n"
MF "v[60:63],a[120:123],v[188:191],v[60:63]\n"
"s_waitcnt lgkmcnt(0)\n"
LD "v195,s[48:51],s80 offen offset:3072 lds\n"
MF "v[16:19],a[20:23],v[176:179],v[16:19]\n"
TR "v[188:189],v1 offset:49152\n"
MF "v[16:19],a[60:63],v[180:183],v[16:19]\n"
TR "v[190:191],v1 offset:51200\n"
MF "v[16:19],a[100:103],v[184:187],v[16:19]\n"
MF "v[24:27],a[104:107],v[184:187],v[24:27]\n"
TR "a[240:241],v1 offset:53248\n"
MF "v[24:27],a[64:67],v[180:183],v[24:27]\n"
"s_add_u32 m0,s79,4096\n"
TR "a[242:243],v1 offset:55296\n"
MF "v[24:27],a[24:27],v[176:179],v[24:27]\n"
LD "v196,s[48:51],s80 offen lds\n"
MF "v[32:35],a[28:31],v[176:179],v[32:35]\n"
TR "a[244:245],v1 offset:57344\n"
MF "v[32:35],a[68:71],v[180:183],v[32:35]\n"
TR "a[246:247],v1 offset:59392\n"
MF "v[32:35],a[108:111],v[184:187],v[32:35]\n"
LD "v197,s[48:51],s80 offen offset:1024 lds\n"
MF "v[40:43],a[112:115],v[184:187],v[40:43]\n"
MF "v[40:43],a[72:75],v[180:183],v[40:43]\n"
MF "v[40:43],a[32:35],v[176:179],v[40:43]\n"
MF "v[48:51],a[36:39],v[176:179],v[48:51]\n"
MF "v[48:51],a[76:79],v[180:183],v[48:51]\n"
LD "v198,s[48:51],s80 offen offset:2048 lds\n"
MF "v[48:51],a[116:119],v[184:187],v[48:51]\n"
"s_waitcnt lgkmcnt(0)\n"
MF "v[52:55],a[116:119],a[244:247],v[52:55]\n"
TR "v[176:177],v2 offset:49152\n"
MF "v[52:55],a[76:79],a[240:243],v[52:55]\n"
TR "v[178:179],v2 offset:51200\n"
MF "v[52:55],a[36:39],v[188:191],v[52:55]\n"
LD "v199,s[48:51],s80 offen offset:3072 lds\n"
MF "v[44:47],a[32:35],v[188:191],v[44:47]\n"
TR "v[180:181],v2 offset:53248\n"
MF "v[44:47],a[72:75],a[240:243],v[44:47]\n"
TR "v[182:183],v2 offset:55296\n"
MF "v[44:47],a[112:115],a[244:247],v[44:47]\n"
MF "v[36:39],a[108:111],a[244:247],v[36:39]\n"
TR "v[184:185],v2 offset:57344\n"
MF "v[36:39],a[68:71],a[240:243],v[36:39]\n"
"s_add_u32 m0,s79,8192\n"
TR "v[186:187],v2 offset:59392\n"
MF "v[36:39],a[28:31],v[188:191],v[36:39]\n"
LD "v200,s[48:51],s80 offen lds\n"
MF "v[28:31],a[24:27],v[188:191],v[28:31]\n"
MF "v[28:31],a[64:67],a[240:243],v[28:31]\n"
MF "v[28:31],a[104:107],a[244:247],v[28:31]\n"
MF "v[20:23],a[100:103],a[244:247],v[20:23]\n"
LD "v201,s[48:51],s80 offen offset:1024 lds\n"
MF "v[20:23],a[60:63],a[240:243],v[20:23]\n"
MF "v[20:23],a[20:23],v[188:191],v[20:23]\n"
"s_waitcnt lgkmcnt(0)\n"
MF "v[56:59],a[20:23],v[176:179],v[56:59]\n"
TR "v[188:189],v3 offset:49152\n"
MF "v[56:59],a[60:63],v[180:183],v[56:59]\n"
TR "v[190:191],v3 offset:51200\n"
MF "v[56:59],a[100:103],v[184:187],v[56:59]\n"
LD "v202,s[48:51],s80 offen offset:2048 lds\n"
MF "v[64:67],a[104:107],v[184:187],v[64:67]\n"
TR "a[240:241],v3 offset:53248\n"
MF "v[64:67],a[64:67],v[180:183],v[64:67]\n"
TR "a[242:243],v3 offset:55296\n"
MF "v[64:67],a[24:27],v[176:179],v[64:67]\n"
LD "v203,s[48:51],s80 offen offset:3072 lds\n"
MF "v[72:75],a[28:31],v[176:179],v[72:75]\n"
TR "a[244:245],v3 offset:57344\n"
MF "v[72:75],a[68:71],v[180:183],v[72:75]\n"
TR "a[246:247],v3 offset:59392\n"
MF "v[72:75],a[108:111],v[184:187],v[72:75]\n"
MF "v[80:83],a[112:115],v[184:187],v[80:83]\n"
MF "v[80:83],a[72:75],v[180:183],v[80:83]\n"
MF "v[80:83],a[32:35],v[176:179],v[80:83]\n"
MF "v[88:91],a[36:39],v[176:179],v[88:91]\n"
MF "v[88:91],a[76:79],v[180:183],v[88:91]\n"
"s_waitcnt vmcnt(24) lgkmcnt(0)\n"
MF "v[88:91],a[116:119],v[184:187],v[88:91]\n"
"s_barrier\n"
MF "v[92:95],a[116:119],a[244:247],v[92:95]\n"
"s_add_u32 s88,s88,s65\n"
TR "v[176:177],v4\n"
MF "v[92:95],a[76:79],a[240:243],v[92:95]\n"
"s_add_u32 m0,s79,49152\n"
TR "v[178:179],v4 offset:2048\n"
MF "v[92:95],a[36:39],v[188:191],v[92:95]\n"
LD "v192,s[48:51],s88 offen lds\n"
MF "v[84:87],a[32:35],v[188:191],v[84:87]\n"
TR "v[180:181],v4 offset:4096\n"
MF "v[84:87],a[72:75],a[240:243],v[84:87]\n"
TR "v[182:183],v4 offset:6144\n"
MF "v[84:87],a[112:115],a[244:247],v[84:87]\n"
LD "v193,s[48:51],s88 offen offset:1024 lds\n"
MF "v[76:79],a[108:111],a[244:247],v[76:79]\n"
TR "v[184:185],v4 offset:8192\n"
MF "v[76:79],a[68:71],a[240:243],v[76:79]\n"
TR "v[186:187],v4 offset:10240\n"
MF "v[76:79],a[28:31],v[188:191],v[76:79]\n"
MF "v[68:71],a[24:27],v[188:191],v[68:71]\n"
MF "v[68:71],a[64:67],a[240:243],v[68:71]\n"
LD "v194,s[48:51],s88 offen offset:2048 lds\n"
MF "v[68:71],a[104:107],a[244:247],v[68:71]\n"
MF "v[60:63],a[100:103],a[244:247],v[60:63]\n"
MF "v[60:63],a[60:63],a[240:243],v[60:63]\n"
MF "v[60:63],a[20:23],v[188:191],v[60:63]\n"
"s_waitcnt lgkmcnt(0)\n"
LD "v195,s[48:51],s88 offen offset:3072 lds\n"
MF "v[16:19],a[140:143],v[176:179],v[16:19]\n"
TR "v[188:189],v5\n"
MF "v[16:19],a[180:183],v[180:183],v[16:19]\n"
TR "v[190:191],v5 offset:2048\n"
MF "v[16:19],a[220:223],v[184:187],v[16:19]\n"
MF "v[24:27],a[224:227],v[184:187],v[24:27]\n"
TR "a[240:241],v5 offset:4096\n"
MF "v[24:27],a[184:187],v[180:183],v[24:27]\n"
"s_add_u32 m0,s79,53248\n"
TR "a[242:243],v5 offset:6144\n"
MF "v[24:27],a[144:147],v[176:179],v[24:27]\n"
LD "v196,s[48:51],s88 offen lds\n"
MF "v[32:35],a[148:151],v[176:179],v[32:35]\n"
TR "a[244:245],v5 offset:8192\n"
MF "v[32:35],a[188:191],v[180:183],v[32:35]\n"
TR "a[246:247],v5 offset:10240\n"
MF "v[32:35],a[228:231],v[184:187],v[32:35]\n"
LD "v197,s[48:51],s88 offen offset:1024 lds\n"
MF "v[40:43],a[232:235],v[184:187],v[40:43]\n"
MF "v[40:43],a[192:195],v[180:183],v[40:43]\n"
MF "v[40:43],a[152:155],v[176:179],v[40:43]\n"
MF "v[48:51],a[156:159],v[176:179],v[48:51]\n"
MF "v[48:51],a[196:199],v[180:183],v[48:51]\n"
LD "v198,s[48:51],s88 offen offset:2048 lds\n"
MF "v[48:51],a[236:239],v[184:187],v[48:51]\n"
"s_waitcnt lgkmcnt(0)\n"
MF "v[52:55],a[236:239],a[244:247],v[52:55]\n"
TR "v[176:177],v6\n"
MF "v[52:55],a[196:199],a[240:243],v[52:55]\n"
TR "v[178:179],v6 offset:2048\n"
MF "v[52:55],a[156:159],v[188:191],v[52:55]\n"
LD "v199,s[48:51],s88 offen offset:3072 lds\n"
MF "v[44:47],a[152:155],v[188:191],v[44:47]\n"
TR "v[180:181],v6 offset:4096\n"
MF "v[44:47],a[192:195],a[240:243],v[44:47]\n"
TR "v[182:183],v6 offset:6144\n"
MF "v[44:47],a[232:235],a[244:247],v[44:47]\n"
MF "v[36:39],a[228:231],a[244:247],v[36:39]\n"
TR "v[184:185],v6 offset:8192\n"
MF "v[36:39],a[188:191],a[240:243],v[36:39]\n"
"s_add_u32 m0,s79,57344\n"
TR "v[186:187],v6 offset:10240\n"
MF "v[36:39],a[148:151],v[188:191],v[36:39]\n"
LD "v200,s[48:51],s88 offen lds\n"
MF "v[28:31],a[144:147],v[188:191],v[28:31]\n"
MF "v[28:31],a[184:187],a[240:243],v[28:31]\n"
MF "v[28:31],a[224:227],a[244:247],v[28:31]\n"
MF "v[20:23],a[220:223],a[244:247],v[20:23]\n"
LD "v201,s[48:51],s88 offen offset:1024 lds\n"
MF "v[20:23],a[180:183],a[240:243],v[20:23]\n"
MF "v[20:23],a[140:143],v[188:191],v[20:23]\n"
"s_waitcnt lgkmcnt(0)\n"
MF "v[56:59],a[140:143],v[176:179],v[56:59]\n"
TR "v[188:189],v7\n"
MF "v[56:59],a[180:183],v[180:183],v[56:59]\n"
TR "v[190:191],v7 offset:2048\n"
MF "v[56:59],a[220:223],v[184:187],v[56:59]\n"
LD "v202,s[48:51],s88 offen offset:2048 lds\n"
MF "v[64:67],a[224:227],v[184:187],v[64:67]\n"
TR "a[240:241],v7 offset:4096\n"
"v_mov_b32 v244,0\n"
MF "v[64:67],a[184:187],v[180:183],v[64:67]\n"
TR "a[242:243],v7 offset:6144\n"
"v_mov_b32 v245,0\n"
MF "v[64:67],a[144:147],v[176:179],v[64:67]\n"
LD "v203,s[48:51],s88 offen offset:3072 lds\n"
MF "v[72:75],a[148:151],v[176:179],v[72:75]\n"
TR "a[244:245],v7 offset:8192\n"
"v_mov_b32 v246,0\n"
MF "v[72:75],a[188:191],v[180:183],v[72:75]\n"
TR "a[246:247],v7 offset:10240\n"
"v_mov_b32 v247,0\n"
MF "v[72:75],a[228:231],v[184:187],v[72:75]\n"
"v_mov_b32 v248,0\n"
"v_mov_b32 v249,0\n"
MF "v[80:83],a[232:235],v[184:187],v[80:83]\n"
"v_mov_b32 v250,0\n"
"v_mov_b32 v251,0\n"
MF "v[80:83],a[192:195],v[180:183],v[80:83]\n"
MF "v[80:83],a[152:155],v[176:179],v[80:83]\n"
MF "v[88:91],a[156:159],v[176:179],v[88:91]\n"
MF "v[88:91],a[196:199],v[180:183],v[88:91]\n"
"s_waitcnt vmcnt(24) lgkmcnt(0)\n"
MF "v[88:91],a[236:239],v[184:187],v[88:91]\n"
"s_barrier\n"
MF "v[92:95],a[236:239],a[244:247],v[92:95]\n"
"s_add_u32 s80,s88,122880\n"
TR "v[176:177],v0 offset:24576\n"
MF "v[92:95],a[196:199],a[240:243],v[92:95]\n"
"s_add_u32 m0,s79,98304\n"
"s_add_u32 s85,s85,1\n"
TR "v[178:179],v0 offset:26624\n"
MF "v[92:95],a[156:159],v[188:191],v[92:95]\n"
LD "v192,s[48:51],s80 offen lds\n"
MF "v[84:87],a[152:155],v[188:191],v[84:87]\n"
"s_nop 0\n"
TR "v[180:181],v0 offset:28672\n"
"ds_write_addtid_b32 v16 offset:24576\n"
MF "v[84:87],a[192:195],a[240:243],v[84:87]\n"
"s_nop 0\n"
TR "v[182:183],v0 offset:30720\n"
"ds_write_addtid_b32 v17 offset:24832\n"
MF "v[84:87],a[232:235],a[244:247],v[84:87]\n"
LD "v193,s[48:51],s80 offen offset:1024 lds\n"
MF "v[76:79],a[228:231],a[244:247],v[76:79]\n"
"s_nop 0\n"
TR "v[184:185],v0 offset:32768\n"
"ds_write_addtid_b32 v18 offset:25088\n"
MF "v[76:79],a[188:191],a[240:243],v[76:79]\n"
"s_nop 0\n"
TR "v[186:187],v0 offset:34816\n"
"ds_write_addtid_b32 v19 offset:25344\n"
MF "v[76:79],a[148:151],v[188:191],v[76:79]\n"
"s_nop 0\n"
"ds_write_addtid_b32 v20 offset:25600\n"
"ds_write_addtid_b32 v21 offset:25856\n"
MF "v[68:71],a[144:147],v[188:191],v[68:71]\n"
LD "v194,s[48:51],s80 offen offset:2048 lds\n"
MF "v[68:71],a[184:187],a[240:243],v[68:71]\n"
"s_nop 0\n"
"ds_write_addtid_b32 v22 offset:26112\n"
"ds_write_addtid_b32 v23 offset:26368\n"
MF "v[68:71],a[224:227],a[244:247],v[68:71]\n"
"s_nop 0\n"
"ds_write_addtid_b32 v24 offset:26624\n"
"ds_write_addtid_b32 v25 offset:26880\n"
MF "v[60:63],a[220:223],a[244:247],v[60:63]\n"
"s_nop 0\n"
"ds_write_addtid_b32 v26 offset:27136\n"
"ds_write_addtid_b32 v27 offset:27392\n"
MF "v[60:63],a[180:183],a[240:243],v[60:63]\n"
MF "v[60:63],a[140:143],v[188:191],v[60:63]\n"
"s_waitcnt lgkmcnt(9)\n"
"s_cmp_lg_u32 s85,5\n"
"s_cbranch_scc1 .Lpl05\n"
".Lpl09:\n"
MF "v[96:99],a[0:3],v[176:179],v[252:255]\n"
"s_nop 0\n"
TR "v[188:189],v1 offset:24576\n"
"ds_write_addtid_b32 v28 offset:27648\n"
MF "v[96:99],a[40:43],v[180:183],v[96:99]\n"
"s_nop 0\n"
TR "v[190:191],v1 offset:26624\n"
"ds_write_addtid_b32 v29 offset:27904\n"
MF "v[96:99],a[80:83],v[184:187],v[96:99]\n"
LD "v195,s[48:51],s80 offen offset:3072 lds\n"
MF "v[104:107],a[84:87],v[184:187],v[252:255]\n"
"s_add_u32 s60,s64,1\n"
TR "a[240:241],v1 offset:28672\n"
"ds_write_addtid_b32 v30 offset:28160\n"
MF "v[104:107],a[44:47],v[180:183],v[104:107]\n"
"s_add_u32 m0,s79,102400\n"
TR "a[242:243],v1 offset:30720\n"
"ds_write_addtid_b32 v31 offset:24320\n"
MF "v[104:107],a[4:7],v[176:179],v[104:107]\n"
LD "v196,s[48:51],s80 offen lds\n"
MF "v[112:115],a[8:11],v[176:179],v[252:255]\n"
"s_cmp_eq_u32 s60,8\n"
"s_cselect_b32 s60,0,s60\n"
TR "a[244:245],v1 offset:32768\n"
MF "v[112:115],a[48:51],v[180:183],v[112:115]\n"
"s_cmp_eq_u32 s60,10\n"
"s_cselect_b32 s60,s61,s60\n"
TR "a[246:247],v1 offset:34816\n"
MF "v[112:115],a[88:91],v[184:187],v[112:115]\n"
"s_sub_u32 s65,s60,s64\n"
"ds_write_addtid_b32 v32 offset:24576\n"
"ds_write_addtid_b32 v33 offset:24832\n"
MF "v[120:123],a[92:95],v[184:187],v[252:255]\n"
"s_lshl_b32 s65,s65,7\n"
"ds_write_addtid_b32 v34 offset:25088\n"
"ds_write_addtid_b32 v35 offset:25344\n"
MF "v[120:123],a[52:55],v[180:183],v[120:123]\n"
LD "v197,s[48:51],s80 offen offset:1024 lds\n"
MF "v[120:123],a[12:15],v[176:179],v[120:123]\n"
"s_mov_b32 s64,s60\n"
"ds_write_addtid_b32 v36 offset:25600\n"
"ds_write_addtid_b32 v37 offset:25856\n"
MF "v[128:131],a[16:19],v[176:179],v[252:255]\n"
"s_nop 0\n"
"ds_write_addtid_b32 v38 offset:26112\n"
"ds_write_addtid_b32 v39 offset:26368\n"
MF "v[128:131],a[56:59],v[180:183],v[128:131]\n"
"s_nop 0\n"
"ds_write_addtid_b32 v40 offset:26624\n"
"ds_write_addtid_b32 v41 offset:26880\n"
MF "v[128:131],a[96:99],v[184:187],v[128:131]\n"
"s_waitcnt lgkmcnt(10)\n"
LD "v198,s[48:51],s80 offen offset:2048 lds\n"
MF "v[132:135],a[96:99],a[244:247],v[252:255]\n"
"s_nop 0\n"
TR "v[176:177],v2 offset:24576\n"
"ds_write_addtid_b32 v42 offset:27136\n"
MF "v[132:135],a[56:59],a[240:243],v[132:135]\n"
"s_nop 0\n"
TR "v[178:179],v2 offset:26624\n"
"ds_write_addtid_b32 v43 offset:27392\n"
MF "v[132:135],a[16:19],v[188:191],v[132:135]\n"
"s_nop 0\n"
"ds_write_addtid_b32 v44 offset:27648\n"
"ds_write_addtid_b32 v45 offset:27904\n"
MF "v[124:127],a[12:15],v[188:191],v[252:255]\n"
"s_nop 0\n"
TR "v[180:181],v2 offset:28672\n"
"ds_write_addtid_b32 v46 offset:28160\n"
MF "v[124:127],a[52:55],a[240:243],v[124:127]\n"
"s_nop 0\n"
TR "v[182:183],v2 offset:30720\n"
"ds_write_addtid_b32 v47 offset:28416\n"
MF "v[124:127],a[92:95],a[244:247],v[124:127]\n"
LD "v199,s[48:51],s80 offen offset:3072 lds\n"
MF "v[116:119],a[88:91],a[244:247],v[252:255]\n"
"s_nop 0\n"
TR "v[184:185],v2 offset:32768\n"
"ds_write_addtid_b32 v48 offset:28672\n"
MF "v[116:119],a[48:51],a[240:243],v[116:119]\n"
"s_add_u32 m0,s79,106496\n"
TR "v[186:187],v2 offset:34816\n"
"ds_write_addtid_b32 v49 offset:24832\n"
MF "v[116:119],a[8:11],v[188:191],v[116:119]\n"
LD "v200,s[48:51],s80 offen lds\n"
MF "v[108:111],a[4:7],v[188:191],v[252:255]\n"
"s_nop 0\n"
"ds_write_addtid_b32 v50 offset:25088\n"
"ds_write_addtid_b32 v51 offset:25344\n"
MF "v[108:111],a[44:47],a[240:243],v[108:111]\n"
"s_nop 0\n"
"ds_write_addtid_b32 v52 offset:25600\n"
"ds_write_addtid_b32 v53 offset:25856\n"
MF "v[108:111],a[84:87],a[244:247],v[108:111]\n"
"s_nop 0\n"
"ds_write_addtid_b32 v54 offset:26112\n"
"ds_write_addtid_b32 v55 offset:26368\n"
MF "v[100:103],a[80:83],a[244:247],v[252:255]\n"
MF "v[100:103],a[40:43],a[240:243],v[100:103]\n"
LD "v201,s[48:51],s80 offen offset:1024 lds\n"
MF "v[100:103],a[0:3],v[188:191],v[100:103]\n"
"s_waitcnt lgkmcnt(7)\n"
MF "v[136:139],a[0:3],v[176:179],v[252:255]\n"
TR "v[188:189],v3 offset:24576\n"
MF "v[136:139],a[40:43],v[180:183],v[136:139]\n"
TR "v[190:191],v3 offset:26624\n"
MF "v[136:139],a[80:83],v[184:187],v[136:139]\n"
LD "v202,s[48:51],s80 offen offset:2048 lds\n"
MF "v[144:147],a[84:87],v[184:187],v[252:255]\n"
TR "a[240:241],v3 offset:28672\n"
MF "v[144:147],a[44:47],v[180:183],v[144:147]\n"
TR "a[242:243],v3 offset:30720\n"
MF "v[144:147],a[4:7],v[176:179],v[144:147]\n"
LD "v203,s[48:51],s80 offen offset:3072 lds\n"
MF "v[152:155],a[8:11],v[176:179],v[252:255]\n"
TR "a[244:245],v3 offset:32768\n"
MF "v[152:155],a[48:51],v[180:183],v[152:155]\n"
TR "a[246:247],v3 offset:34816\n"
MF "v[152:155],a[88:91],v[184:187],v[152:155]\n"
MF "v[160:163],a[92:95],v[184:187],v[252:255]\n"
MF "v[160:163],a[52:55],v[180:183],v[160:163]\n"
MF "v[160:163],a[12:15],v[176:179],v[160:163]\n"
MF "v[168:171],a[16:19],v[176:179],v[252:255]\n"
MF "v[168:171],a[56:59],v[180:183],v[168:171]\n"
"s_waitcnt vmcnt(24) lgkmcnt(0)\n"
MF "v[168:171],a[96:99],v[184:187],v[168:171]\n"
"s_barrier\n"
MF "v[172:175],a[96:99],a[244:247],v[252:255]\n"
"s_add_u32 s89,s89,s65\n"
TR "v[176:177],v0\n"
MF "v[172:175],a[56:59],a[240:243],v[172:175]\n"
"s_add_u32 m0,s79,24576\n"
TR "v[178:179],v0 offset:2048\n"
MF "v[172:175],a[16:19],v[188:191],v[172:175]\n"
LD "v192,s[48:51],s89 offen lds\n"
MF "v[164:167],a[12:15],v[188:191],v[252:255]\n"
TR "v[180:181],v0 offset:4096\n"
MF "v[164:167],a[52:55],a[240:243],v[164:167]\n"
TR "v[182:183],v0 offset:6144\n"
MF "v[164:167],a[92:95],a[244:247],v[164:167]\n"
LD "v193,s[48:51],s89 offen offset:1024 lds\n"
MF "v[156:159],a[88:91],a[244:247],v[252:255]\n"
TR "v[184:185],v0 offset:8192\n"
MF "v[156:159],a[48:51],a[240:243],v[156:159]\n"
"s_nop 0\n"
TR "v[186:187],v0 offset:10240\n"
"ds_write_addtid_b32 v56 offset:49152\n"
MF "v[156:159],a[8:11],v[188:191],v[156:159]\n"
"s_nop 0\n"
"ds_write_addtid_b32 v57 offset:49408\n"
"ds_write_addtid_b32 v58 offset:49664\n"
MF "v[148:151],a[4:7],v[188:191],v[252:255]\n"
LD "v194,s[48:51],s89 offen offset:2048 lds\n"
MF "v[148:151],a[44:47],a[240:243],v[148:151]\n"
"buffer_load_dword v10,v12,s[40:43],s86 offen sc1\n"
MF "v[148:151],a[84:87],a[244:247],v[148:151]\n"
"s_nop 0\n"
"ds_write_addtid_b32 v59 offset:49920\n"
"ds_write_addtid_b32 v60 offset:50176\n"
MF "v[140:143],a[80:83],a[244:247],v[252:255]\n"
"s_nop 0\n"
"ds_write_addtid_b32 v61 offset:50432\n"
"ds_write_addtid_b32 v62 offset:50688\n"
MF "v[140:143],a[40:43],a[240:243],v[140:143]\n"
"s_nop 0\n"
"ds_write_addtid_b32 v63 offset:50944\n"
"ds_write_addtid_b32 v64 offset:51200\n"
MF "v[140:143],a[0:3],v[188:191],v[140:143]\n"
"s_waitcnt lgkmcnt(9)\n"
LD "v195,s[48:51],s89 offen offset:3072 lds\n"
MF "v[96:99],a[120:123],v[176:179],v[96:99]\n"
"s_nop 0\n"
TR "v[188:189],v1\n"
"ds_write_addtid_b32 v65 offset:51456\n"
MF "v[96:99],a[160:163],v[180:183],v[96:99]\n"
".Lpg25h:\n"
"s_cbranch_execz .Lpgrole1_h\n"
"s_nop 0\n"
TR "v[190:191],v1 offset:2048\n"
"ds_write_addtid_b32 v66 offset:51712\n"
MF "v[96:99],a[200:203],v[184:187],v[96:99]\n"
"s_nop 0\n"
"ds_write_addtid_b32 v67 offset:51968\n"
"ds_write_addtid_b32 v68 offset:52224\n"
MF "v[104:107],a[204:207],v[184:187],v[104:107]\n"
"s_nop 0\n"
TR "a[240:241],v1 offset:4096\n"
"ds_write_addtid_b32 v69 offset:52480\n"
MF "v[104:107],a[164:167],v[180:183],v[104:107]\n"
"s_add_u32 m0,s79,28672\n"
TR "a[242:243],v1 offset:6144\n"
"ds_write_addtid_b32 v70 offset:48640\n"
MF "v[104:107],a[124:127],v[176:179],v[104:107]\n"
LD "v196,s[48:51],s89 offen lds\n"
MF "v[112:115],a[128:131],v[176:179],v[112:115]\n"
"s_nop 0\n"
TR "a[244:245],v1 offset:8192\n"
"ds_write_addtid_b32 v71 offset:48896\n"
MF "v[112:115],a[168:171],v[180:183],v[112:115]\n"
"s_nop 0\n"
TR "a[246:247],v1 offset:10240\n"
"ds_write_addtid_b32 v72 offset:49152\n"
MF "v[112:115],a[208:211],v[184:187],v[112:115]\n"
LD "v197,s[48:51],s89 offen offset:1024 lds\n"
MF "v[120:123],a[212:215],v[184:187],v[120:123]\n"
"s_nop 0\n"
"ds_write_addtid_b32 v73 offset:49408\n"
"ds_write_addtid_b32 v74 offset:49664\n"
MF "v[120:123],a[172:175],v[180:183],v[120:123]\n"
"s_nop 0\n"
"ds_write_addtid_b32 v75 offset:49920\n"
"ds_write_addtid_b32 v76 offset:50176\n"
MF "v[120:123],a[132:135],v[176:179],v[120:123]\n"
"s_nop 0\n"
"ds_write_addtid_b32 v77 offset:50432\n"
"ds_write_addtid_b32 v78 offset:50688\n"
MF "v[128:131],a[136:139],v[176:179],v[128:131]\n"
"s_nop 0\n"
"ds_write_addtid_b32 v79 offset:50944\n"
"ds_write_addtid_b32 v80 offset:51200\n"
MF "v[128:131],a[176:179],v[180:183],v[128:131]\n"
LD "v198,s[48:51],s89 offen offset:2048 lds\n"
MF "v[128:131],a[216:219],v[184:187],v[128:131]\n"
"s_waitcnt lgkmcnt(9)\n"
"ds_write_addtid_b32 v81 offset:51456\n"
"ds_write_addtid_b32 v82 offset:51712\n"
MF "v[132:135],a[216:219],a[244:247],v[132:135]\n"
"s_nop 0\n"
TR "v[176:177],v2\n"
"ds_write_addtid_b32 v83 offset:51968\n"
MF "v[132:135],a[176:179],a[240:243],v[132:135]\n"
"s_nop 0\n"
TR "v[178:179],v2 offset:2048\n"
"ds_write_addtid_b32 v84 offset:52224\n"
MF "v[132:135],a[136:139],v[188:191],v[132:135]\n"
LD "v199,s[48:51],s89 offen offset:3072 lds\n"
MF "v[124:127],a[132:135],v[188:191],v[124:127]\n"
"s_nop 0\n"
TR "v[180:181],v2 offset:4096\n"
"ds_write_addtid_b32 v85 offset:52480\n"
MF "v[124:127],a[172:175],a[240:243],v[124:127]\n"
"s_nop 0\n"
TR "v[182:183],v2 offset:6144\n"
"ds_write_addtid_b32 v86 offset:52736\n"
MF "v[124:127],a[212:215],a[244:247],v[124:127]\n"
"s_nop 0\n"
"ds_write_addtid_b32 v87 offset:52992\n"
"ds_write_addtid_b32 v88 offset:53248\n"
MF "v[116:119],a[208:211],a[244:247],v[116:119]\n"
"s_nop 0\n"
TR "v[184:185],v2 offset:8192\n"
"ds_write_addtid_b32 v89 offset:53504\n"
MF "v[116:119],a[168:171],a[240:243],v[116:119]\n"
"s_add_u32 m0,s79,32768\n"
TR "v[186:187],v2 offset:10240\n"
"ds_write_addtid_b32 v90 offset:49664\n"
MF "v[116:119],a[128:131],v[188:191],v[116:119]\n"
LD "v200,s[48:51],s89 offen lds\n"
MF "v[108:111],a[124:127],v[188:191],v[108:111]\n"
"s_nop 0\n"
"ds_write_addtid_b32 v91 offset:49920\n"
"ds_write_addtid_b32 v92 offset:50176\n"
MF "v[108:111],a[164:167],a[240:243],v[108:111]\n"
"s_nop 0\n"
"ds_write_addtid_b32 v93 offset:50432\n"
"ds_write_addtid_b32 v94 offset:50688\n"
MF "v[108:111],a[204:207],a[244:247],v[108:111]\n"
"ds_write_addtid_b32 v95 offset:50944\n"
MF "v[100:103],a[200:203],a[244:247],v[100:103]\n"
LD "v201,s[48:51],s89 offen offset:1024 lds\n"
MF "v[100:103],a[160:163],a[240:243],v[100:103]\n"
MF "v[100:103],a[120:123],v[188:191],v[100:103]\n"
"s_waitcnt lgkmcnt(6)\n"
MF "v[136:139],a[120:123],v[176:179],v[136:139]\n"
TR "v[188:189],v3\n"
MF "v[136:139],a[160:163],v[180:183],v[136:139]\n"
TR "v[190:191],v3 offset:2048\n"
MF "v[136:139],a[200:203],v[184:187],v[136:139]\n"
LD "v202,s[48:51],s89 offen offset:2048 lds\n"
MF "v[144:147],a[204:207],v[184:187],v[144:147]\n"
TR "a[240:241],v3 offset:4096\n"
MF "v[144:147],a[164:167],v[180:183],v[144:147]\n"
TR "a[242:243],v3 offset:6144\n"
MF "v[144:147],a[124:127],v[176:179],v[144:147]\n"
LD "v203,s[48:51],s89 offen offset:3072 lds\n"
MF "v[152:155],a[128:131],v[176:179],v[152:155]\n"
TR "a[244:245],v3 offset:8192\n"
MF "v[152:155],a[168:171],v[180:183],v[152:155]\n"
TR "a[246:247],v3 offset:10240\n"
MF "v[152:155],a[208:211],v[184:187],v[152:155]\n"
MF "v[160:163],a[212:215],v[184:187],v[160:163]\n"
MF "v[160:163],a[172:175],v[180:183],v[160:163]\n"
MF "v[160:163],a[132:135],v[176:179],v[160:163]\n"
MF "v[168:171],a[136:139],v[176:179],v[168:171]\n"
MF "v[168:171],a[176:179],v[180:183],v[168:171]\n"
"s_waitcnt vmcnt(25) lgkmcnt(0)\n"
MF "v[168:171],a[216:219],v[184:187],v[168:171]\n"
"s_barrier\n"
MF "v[172:175],a[216:219],a[244:247],v[172:175]\n"
"s_add_u32 s80,s89,122880\n"
TR "v[176:177],v0 offset:49152\n"
MF "v[172:175],a[176:179],a[240:243],v[172:175]\n"
"s_add_u32 m0,s79,0\n"
"s_mov_b32 s82,s70\n"
TR "v[178:179],v0 offset:51200\n"
MF "v[172:175],a[136:139],v[188:191],v[172:175]\n"
LD "v192,s[48:51],s80 offen lds\n"
MF "v[164:167],a[132:135],v[188:191],v[164:167]\n"
TR "v[180:181],v0 offset:53248\n"
MF "v[164:167],a[172:175],a[240:243],v[164:167]\n"
TR "v[182:183],v0 offset:55296\n"
MF "v[164:167],a[212:215],a[244:247],v[164:167]\n"
LD "v193,s[48:51],s80 offen offset:1024 lds\n"
MF "v[156:159],a[208:211],a[244:247],v[156:159]\n"
TR "v[184:185],v0 offset:57344\n"
MF "v[156:159],a[168:171],a[240:243],v[156:159]\n"
TR "v[186:187],v0 offset:59392\n"
MF "v[156:159],a[128:131],v[188:191],v[156:159]\n"
MF "v[148:151],a[124:127],v[188:191],v[148:151]\n"
MF "v[148:151],a[164:167],a[240:243],v[148:151]\n"
LD "v194,s[48:51],s80 offen offset:2048 lds\n"
MF "v[148:151],a[204:207],a[244:247],v[148:151]\n"
MF "v[140:143],a[200:203],a[244:247],v[140:143]\n"
MF "v[140:143],a[160:163],a[240:243],v[140:143]\n"
MF "v[140:143],a[120:123],v[188:191],v[140:143]\n"
"s_waitcnt lgkmcnt(0)\n"
LD "v195,s[48:51],s80 offen offset:3072 lds\n"
MF "v[96:99],a[20:23],v[176:179],v[96:99]\n"
TR "v[188:189],v1 offset:49152\n"
MF "v[96:99],a[60:63],v[180:183],v[96:99]\n"
TR "v[190:191],v1 offset:51200\n"
MF "v[96:99],a[100:103],v[184:187],v[96:99]\n"
MF "v[104:107],a[104:107],v[184:187],v[104:107]\n"
TR "a[240:241],v1 offset:53248\n"
MF "v[104:107],a[64:67],v[180:183],v[104:107]\n"
"s_add_u32 m0,s79,4096\n"
TR "a[242:243],v1 offset:55296\n"
MF "v[104:107],a[24:27],v[176:179],v[104:107]\n"
LD "v196,s[48:51],s80 offen lds\n"
MF "v[112:115],a[28:31],v[176:179],v[112:115]\n"
TR "a[244:245],v1 offset:57344\n"
MF "v[112:115],a[68:71],v[180:183],v[112:115]\n"
TR "a[246:247],v1 offset:59392\n"
MF "v[112:115],a[108:111],v[184:187],v[112:115]\n"
LD "v197,s[48:51],s80 offen offset:1024 lds\n"
MF "v[120:123],a[112:115],v[184:187],v[120:123]\n"
MF "v[120:123],a[72:75],v[180:183],v[120:123]\n"
MF "v[120:123],a[32:35],v[176:179],v[120:123]\n"
MF "v[128:131],a[36:39],v[176:179],v[128:131]\n"
MF "v[128:131],a[76:79],v[180:183],v[128:131]\n"
LD "v198,s[48:51],s80 offen offset:2048 lds\n"
MF "v[128:131],a[116:119],v[184:187],v[128:131]\n"
"s_waitcnt lgkmcnt(0)\n"
MF "v[132:135],a[116:119],a[244:247],v[132:135]\n"
TR "v[176:177],v2 offset:49152\n"
MF "v[132:135],a[76:79],a[240:243],v[132:135]\n"
TR "v[178:179],v2 offset:51200\n"
MF "v[132:135],a[36:39],v[188:191],v[132:135]\n"
LD "v199,s[48:51],s80 offen offset:3072 lds\n"
MF "v[124:127],a[32:35],v[188:191],v[124:127]\n"
TR "v[180:181],v2 offset:53248\n"
MF "v[124:127],a[72:75],a[240:243],v[124:127]\n"
TR "v[182:183],v2 offset:55296\n"
MF "v[124:127],a[112:115],a[244:247],v[124:127]\n"
MF "v[116:119],a[108:111],a[244:247],v[116:119]\n"
TR "v[184:185],v2 offset:57344\n"
MF "v[116:119],a[68:71],a[240:243],v[116:119]\n"
"s_add_u32 m0,s79,8192\n"
TR "v[186:187],v2 offset:59392\n"
MF "v[116:119],a[28:31],v[188:191],v[116:119]\n"
LD "v200,s[48:51],s80 offen lds\n"
MF "v[108:111],a[24:27],v[188:191],v[108:111]\n"
MF "v[108:111],a[64:67],a[240:243],v[108:111]\n"
MF "v[108:111],a[104:107],a[244:247],v[108:111]\n"
MF "v[100:103],a[100:103],a[244:247],v[100:103]\n"
LD "v201,s[48:51],s80 offen offset:1024 lds\n"
MF "v[100:103],a[60:63],a[240:243],v[100:103]\n"
"s_waitcnt vmcnt(19)\n"
"s_cmp_lt_u32 s85,6\n"
MF "v[100:103],a[20:23],v[188:191],v[100:103]\n"
"s_cbranch_scc1 .Ljs4\n"
"v_mov_b32 v236,v228\n"
"v_mov_b32 v237,v229\n"
MF "v[96:99],a[252:255],v[208:211],v[96:99]\n"
MF "v[104:107],a[252:255],v[212:215],v[104:107]\n"
MF "v[112:115],a[252:255],v[216:219],v[112:115]\n"
MF "v[120:123],a[252:255],v[220:223],v[120:123]\n"
MF "v[128:131],a[252:255],v[224:227],v[128:131]\n"
MF "v[100:103],a[248:251],v[208:211],v[100:103]\n"
MF "v[108:111],a[248:251],v[212:215],v[108:111]\n"
MF "v[116:119],a[248:251],v[216:219],v[116:119]\n"
MF "v[124:127],a[248:251],v[220:223],v[124:127]\n"
MF "v[132:135],a[248:251],v[224:227],v[132:135]\n"
MF "v[248:251],v[230:233],a[250:253],v[248:251]\n"
MF "v[244:247],v[234:237],a[250:253],v[244:247]\n"
".Ljs4:\n"
"s_waitcnt lgkmcnt(0)\n"
MF "v[136:139],a[20:23],v[176:179],v[136:139]\n"
TR "v[188:189],v3 offset:49152\n"
"v_sub_u32 v10,v10,v11\n"
MF "v[136:139],a[60:63],v[180:183],v[136:139]\n"
TR "v[190:191],v3 offset:51200\n"
MF "v[136:139],a[100:103],v[184:187],v[136:139]\n"
LD "v202,s[48:51],s80 offen offset:2048 lds\n"
MF "v[144:147],a[104:107],v[184:187],v[144:147]\n"
TR "a[240:241],v3 offset:53248\n"
MF "v[144:147],a[64:67],v[180:183],v[144:147]\n"
TR "a[242:243],v3 offset:55296\n"
MF "v[144:147],a[24:27],v[176:179],v[144:147]\n"
LD "v203,s[48:51],s80 offen offset:3072 lds\n"
MF "v[152:155],a[28:31],v[176:179],v[152:155]\n"
TR "a[244:245],v3 offset:57344\n"
MF "v[152:155],a[68:71],v[180:183],v[152:155]\n"
TR "a[246:247],v3 offset:59392\n"
MF "v[152:155],a[108:111],v[184:187],v[152:155]\n"
".Lnf0:\n"
"v_readlane_b32 s58,v10,s67\n"
"s_cmp_lt_u32 s58,s85\n"
"s_cbranch_scc1 .Lsp0\n"
".Lnb0:\n"
MF "v[160:163],a[112:115],v[184:187],v[160:163]\n"
"s_add_u32 s70,s70,20480\n"
".Lnf1:\n"
"v_cmp_gt_u32 vcc,4,v10\n"
"s_cbranch_vccnz .Lsp1\n"
".Lnb1:\n"
MF "v[160:163],a[72:75],v[180:183],v[160:163]\n"
MF "v[160:163],a[32:35],v[176:179],v[160:163]\n"
MF "v[168:171],a[36:39],v[176:179],v[168:171]\n"
MF "v[168:171],a[76:79],v[180:183],v[168:171]\n"
"s_waitcnt vmcnt(25) lgkmcnt(0)\n"
MF "v[168:171],a[116:119],v[184:187],v[168:171]\n"
"s_barrier\n"
MF "v[172:175],a[116:119],a[244:247],v[172:175]\n"
"s_add_u32 s88,s88,s65\n"
TR "v[176:177],v4\n"
MF "v[172:175],a[76:79],a[240:243],v[172:175]\n"
"s_add_u32 m0,s79,49152\n"
TR "v[178:179],v4 offset:2048\n"
MF "v[172:175],a[36:39],v[188:191],v[172:175]\n"
LD "v192,s[48:51],s88 offen lds\n"
MF "v[164:167],a[32:35],v[188:191],v[164:167]\n"
TR "v[180:181],v4 offset:4096\n"
MF "v[164:167],a[72:75],a[240:243],v[164:167]\n"
TR "v[182:183],v4 offset:6144\n"
MF "v[164:167],a[112:115],a[244:247],v[164:167]\n"
LD "v193,s[48:51],s88 offen offset:1024 lds\n"
MF "v[156:159],a[108:111],a[244:247],v[156:159]\n"
TR "v[184:185],v4 offset:8192\n"
MF "v[156:159],a[68:71],a[240:243],v[156:159]\n"
"s_add_u32 s2,s2,204800\n"
TR "v[186:187],v4 offset:10240\n"
MF "v[156:159],a[28:31],v[188:191],v[156:159]\n"
LD "v[228:231],v9,s[40:43],s2 offen sc1\n"
MF "v[148:151],a[24:27],v[188:191],v[148:151]\n"
MF "v[148:151],a[64:67],a[240:243],v[148:151]\n"
LD "v194,s[48:51],s88 offen offset:2048 lds\n"
MF "v[148:151],a[104:107],a[244:247],v[148:151]\n"
MF "v[140:143],a[100:103],a[244:247],v[140:143]\n"
MF "v[140:143],a[60:63],a[240:243],v[140:143]\n"
MF "v[140:143],a[20:23],v[188:191],v[140:143]\n"
"s_waitcnt lgkmcnt(0)\n"
LD "v195,s[48:51],s88 offen offset:3072 lds\n"
MF "v[96:99],a[140:143],v[176:179],v[96:99]\n"
TR "v[188:189],v5\n"
MF "v[96:99],a[180:183],v[180:183],v[96:99]\n"
"s_add_u32 s2,s2,204800\n"
TR "v[190:191],v5 offset:2048\n"
MF "v[96:99],a[220:223],v[184:187],v[96:99]\n"
LD "v[232:235],v9,s[40:43],s2 offen sc1\n"
MF "v[104:107],a[224:227],v[184:187],v[104:107]\n"
TR "a[240:241],v5 offset:4096\n"
MF "v[104:107],a[184:187],v[180:183],v[104:107]\n"
"s_add_u32 m0,s79,53248\n"
TR "a[242:243],v5 offset:6144\n"
MF "v[104:107],a[144:147],v[176:179],v[104:107]\n"
LD "v196,s[48:51],s88 offen lds\n"
MF "v[112:115],a[148:151],v[176:179],v[112:115]\n"
TR "a[244:245],v5 offset:8192\n"
MF "v[112:115],a[188:191],v[180:183],v[112:115]\n"
TR "a[246:247],v5 offset:10240\n"
MF "v[112:115],a[228:231],v[184:187],v[112:115]\n"
LD "v197,s[48:51],s88 offen offset:1024 lds\n"
MF "v[120:123],a[232:235],v[184:187],v[120:123]\n"
MF "v[120:123],a[192:195],v[180:183],v[120:123]\n"
MF "v[120:123],a[152:155],v[176:179],v[120:123]\n"
LD "v[208:211],v9,s[40:43],s82 offen sc1\n"
MF "v[128:131],a[156:159],v[176:179],v[128:131]\n"
MF "v[128:131],a[196:199],v[180:183],v[128:131]\n"
LD "v198,s[48:51],s88 offen offset:2048 lds\n"
MF "v[128:131],a[236:239],v[184:187],v[128:131]\n"
"s_waitcnt lgkmcnt(0)\n"
MF "v[132:135],a[236:239],a[244:247],v[132:135]\n"
TR "v[176:177],v6\n"
MF "v[132:135],a[196:199],a[240:243],v[132:135]\n"
TR "v[178:179],v6 offset:2048\n"
MF "v[132:135],a[156:159],v[188:191],v[132:135]\n"
LD "v199,s[48:51],s88 offen offset:3072 lds\n"
MF "v[124:127],a[152:155],v[188:191],v[124:127]\n"
TR "v[180:181],v6 offset:4096\n"
MF "v[124:127],a[192:195],a[240:243],v[124:127]\n"
TR "v[182:183],v6 offset:6144\n"
MF "v[124:127],a[232:235],a[244:247],v[124:127]\n"
LD "v[212:215],v9,s[40:43],s82 offen offset:1024 sc1\n"
MF "v[116:119],a[228:231],a[244:247],v[116:119]\n"
TR "v[184:185],v6 offset:8192\n"
MF "v[116:119],a[188:191],a[240:243],v[116:119]\n"
"s_add_u32 m0,s79,57344\n"
TR "v[186:187],v6 offset:10240\n"
MF "v[116:119],a[148:151],v[188:191],v[116:119]\n"
LD "v200,s[48:51],s88 offen lds\n"
MF "v[108:111],a[144:147],v[188:191],v[108:111]\n"
MF "v[108:111],a[184:187],a[240:243],v[108:111]\n"
MF "v[108:111],a[224:227],a[244:247],v[108:111]\n"
MF "v[100:103],a[220:223],a[244:247],v[100:103]\n"
LD "v201,s[48:51],s88 offen offset:1024 lds\n"
MF "v[100:103],a[180:183],a[240:243],v[100:103]\n"
MF "v[100:103],a[140:143],v[188:191],v[100:103]\n"
"s_waitcnt lgkmcnt(0)\n"
LD "v[216:219],v9,s[40:43],s82 offen offset:2048 sc1\n"
MF "v[136:139],a[140:143],v[176:179],v[136:139]\n"
TR "v[188:189],v7\n"
MF "v[136:139],a[180:183],v[180:183],v[136:139]\n"
TR "v[190:191],v7 offset:2048\n"
MF "v[136:139],a[220:223],v[184:187],v[136:139]\n"
LD "v202,s[48:51],s88 offen offset:2048 lds\n"
MF "v[144:147],a[224:227],v[184:187],v[144:147]\n"
TR "a[240:241],v7 offset:4096\n"
MF "v[144:147],a[184:187],v[180:183],v[144:147]\n"
TR "a[242:243],v7 offset:6144\n"
MF "v[144:147],a[144:147],v[176:179],v[144:147]\n"
LD "v203,s[48:51],s88 offen offset:3072 lds\n"
MF "v[152:155],a[148:151],v[176:179],v[152:155]\n"
TR "a[244:245],v7 offset:8192\n"
MF "v[152:155],a[188:191],v[180:183],v[152:155]\n"
TR "a[246:247],v7 offset:10240\n"
MF "v[152:155],a[228:231],v[184:187],v[152:155]\n"
LD "v[220:223],v9,s[40:43],s82 offen offset:3072 sc1\n"
MF "v[160:163],a[232:235],v[184:187],v[160:163]\n"
MF "v[160:163],a[192:195],v[180:183],v[160:163]\n"
MF "v[160:163],a[152:155],v[176:179],v[160:163]\n"
MF "v[168:171],a[156:159],v[176:179],v[168:171]\n"
"s_add_u32 s82,s82,4096\n"
MF "v[168:171],a[196:199],v[180:183],v[168:171]\n"
"s_waitcnt vmcnt(30) lgkmcnt(0)\n"
LD "v[224:227],v9,s[40:43],s82 offen sc1\n"
MF "v[168:171],a[236:239],v[184:187],v[168:171]\n"
"s_barrier\n"
MF "v[172:175],a[236:239],a[244:247],v[172:175]\n"
"s_add_u32 s80,s88,122880\n"
TR "v[176:177],v0 offset:24576\n"
MF "v[172:175],a[196:199],a[240:243],v[172:175]\n"
"s_add_u32 m0,s79,98304\n"
"s_add_u32 s85,s85,1\n"
TR "v[178:179],v0 offset:26624\n"
MF "v[172:175],a[156:159],v[188:191],v[172:175]\n"
LD "v192,s[48:51],s80 offen lds\n"
MF "v[164:167],a[152:155],v[188:191],v[164:167]\n"
"s_nop 0\n"
TR "v[180:181],v0 offset:28672\n"
"ds_write_addtid_b32 v96 offset:24576\n"
MF "v[164:167],a[192:195],a[240:243],v[164:167]\n"
"s_nop 0\n"
TR "v[182:183],v0 offset:30720\n"
"ds_write_addtid_b32 v97 offset:24832\n"
MF "v[164:167],a[232:235],a[244:247],v[164:167]\n"
LD "v193,s[48:51],s80 offen offset:1024 lds\n"
MF "v[156:159],a[228:231],a[244:247],v[156:159]\n"
"s_nop 0\n"
TR "v[184:185],v0 offset:32768\n"
"ds_write_addtid_b32 v98 offset:25088\n"
MF "v[156:159],a[188:191],a[240:243],v[156:159]\n"
"s_nop 0\n"
TR "v[186:187],v0 offset:34816\n"
"ds_write_addtid_b32 v99 offset:25344\n"
MF "v[156:159],a[148:151],v[188:191],v[156:159]\n"
"s_nop 0\n"
"ds_write_addtid_b32 v100 offset:25600\n"
"ds_write_addtid_b32 v101 offset:25856\n"
MF "v[148:151],a[144:147],v[188:191],v[148:151]\n"
LD "v194,s[48:51],s80 offen offset:2048 lds\n"
MF "v[148:151],a[184:187],a[240:243],v[148:151]\n"
"s_nop 0\n"
"ds_write_addtid_b32 v102 offset:26112\n"
"ds_write_addtid_b32 v103 offset:26368\n"
MF "v[148:151],a[224:227],a[244:247],v[148:151]\n"
"s_nop 0\n"
"ds_write_addtid_b32 v104 offset:26624\n"
"ds_write_addtid_b32 v105 offset:26880\n"
MF "v[140:143],a[220:223],a[244:247],v[140:143]\n"
"s_nop 0\n"
"ds_write_addtid_b32 v106 offset:27136\n"
"ds_write_addtid_b32 v107 offset:27392\n"
MF "v[140:143],a[180:183],a[240:243],v[140:143]\n"
MF "v[140:143],a[140:143],v[188:191],v[140:143]\n"
"s_waitcnt lgkmcnt(9)\n"
MF "v[16:19],a[0:3],v[176:179],v[252:255]\n"
"s_nop 0\n"
TR "v[188:189],v1 offset:24576\n"
"ds_write_addtid_b32 v108 offset:27648\n"
MF "v[16:19],a[40:43],v[180:183],v[16:19]\n"
"s_nop 0\n"
TR "v[190:191],v1 offset:26624\n"
"ds_write_addtid_b32 v109 offset:27904\n"
MF "v[16:19],a[80:83],v[184:187],v[16:19]\n"
LD "v195,s[48:51],s80 offen offset:3072 lds\n"
MF "v[24:27],a[84:87],v[184:187],v[252:255]\n"
"s_add_u32 s60,s64,1\n"
TR "a[240:241],v1 offset:28672\n"
"ds_write_addtid_b32 v110 offset:28160\n"
MF "v[24:27],a[44:47],v[180:183],v[24:27]\n"
"s_add_u32 m0,s79,102400\n"
TR "a[242:243],v1 offset:30720\n"
"ds_write_addtid_b32 v111 offset:24320\n"
MF "v[24:27],a[4:7],v[176:179],v[24:27]\n"
LD "v196,s[48:51],s80 offen lds\n"
MF "v[32:35],a[8:11],v[176:179],v[252:255]\n"
"s_cmp_eq_u32 s60,8\n"
"s_cselect_b32 s60,0,s60\n"
TR "a[244:245],v1 offset:32768\n"
MF "v[32:35],a[48:51],v[180:183],v[32:35]\n"
"s_cmp_eq_u32 s60,10\n"
"s_cselect_b32 s60,s61,s60\n"
TR "a[246:247],v1 offset:34816\n"
MF "v[32:35],a[88:91],v[184:187],v[32:35]\n"
"s_sub_u32 s65,s60,s64\n"
"ds_write_addtid_b32 v112 offset:24576\n"
"ds_write_addtid_b32 v113 offset:24832\n"
MF "v[40:43],a[92:95],v[184:187],v[252:255]\n"
"s_lshl_b32 s65,s65,7\n"
"ds_write_addtid_b32 v114 offset:25088\n"
"ds_write_addtid_b32 v115 offset:25344\n"
MF "v[40:43],a[52:55],v[180:183],v[40:43]\n"
LD "v197,s[48:51],s80 offen offset:1024 lds\n"
MF "v[40:43],a[12:15],v[176:179],v[40:43]\n"
"s_mov_b32 s64,s60\n"
"ds_write_addtid_b32 v116 offset:25600\n"
"ds_write_addtid_b32 v117 offset:25856\n"
MF "v[48:51],a[16:19],v[176:179],v[252:255]\n"
"s_nop 0\n"
"ds_write_addtid_b32 v118 offset:26112\n"
"ds_write_addtid_b32 v119 offset:26368\n"
MF "v[48:51],a[56:59],v[180:183],v[48:51]\n"
"s_nop 0\n"
"ds_write_addtid_b32 v120 offset:26624\n"
"ds_write_addtid_b32 v121 offset:26880\n"
MF "v[48:51],a[96:99],v[184:187],v[48:51]\n"
"s_waitcnt lgkmcnt(10)\n"
LD "v198,s[48:51],s80 offen offset:2048 lds\n"
MF "v[52:55],a[96:99],a[244:247],v[252:255]\n"
"s_nop 0\n"
TR "v[176:177],v2 offset:24576\n"
"ds_write_addtid_b32 v122 offset:27136\n"
MF "v[52:55],a[56:59],a[240:243],v[52:55]\n"
"s_nop 0\n"
TR "v[178:179],v2 offset:26624\n"
"ds_write_addtid_b32 v123 offset:27392\n"
MF "v[52:55],a[16:19],v[188:191],v[52:55]\n"
"s_nop 0\n"
"ds_write_addtid_b32 v124 offset:27648\n"
"ds_write_addtid_b32 v125 offset:27904\n"
MF "v[44:47],a[12:15],v[188:191],v[252:255]\n"
"s_nop 0\n"
TR "v[180:181],v2 offset:28672\n"
"ds_write_addtid_b32 v126 offset:28160\n"
MF "v[44:47],a[52:55],a[240:243],v[44:47]\n"
"s_nop 0\n"
TR "v[182:183],v2 offset:30720\n"
"ds_write_addtid_b32 v127 offset:28416\n"
MF "v[44:47],a[92:95],a[244:247],v[44:47]\n"
LD "v199,s[48:51],s80 offen offset:3072 lds\n"
MF "v[36:39],a[88:91],a[244:247],v[252:255]\n"
"s_nop 0\n"
TR "v[184:185],v2 offset:32768\n"
"ds_write_addtid_b32 v128 offset:28672\n"
MF "v[36:39],a[48:51],a[240:243],v[36:39]\n"
"s_add_u32 m0,s79,106496\n"
TR "v[186:187],v2 offset:34816\n"
"ds_write_addtid_b32 v129 offset:24832\n"
MF "v[36:39],a[8:11],v[188:191],v[36:39]\n"
LD "v200,s[48:51],s80 offen lds\n"
MF "v[28:31],a[4:7],v[188:191],v[252:255]\n"
"s_nop 0\n"
"ds_write_addtid_b32 v130 offset:25088\n"
"ds_write_addtid_b32 v131 offset:25344\n"
MF "v[28:31],a[44:47],a[240:243],v[28:31]\n"
"s_nop 0\n"
"ds_write_addtid_b32 v132 offset:25600\n"
"ds_write_addtid_b32 v133 offset:25856\n"
MF "v[28:31],a[84:87],a[244:247],v[28:31]\n"
"s_nop 0\n"
"ds_write_addtid_b32 v134 offset:26112\n"
"ds_write_addtid_b32 v135 offset:26368\n"
MF "v[20:23],a[80:83],a[244:247],v[252:255]\n"
MF "v[20:23],a[40:43],a[240:243],v[20:23]\n"
LD "v201,s[48:51],s80 offen offset:1024 lds\n"
MF "v[20:23],a[0:3],v[188:191],v[20:23]\n"
"s_waitcnt lgkmcnt(7)\n"
MF "v[56:59],a[0:3],v[176:179],v[252:255]\n"
TR "v[188:189],v3 offset:24576\n"
MF "v[56:59],a[40:43],v[180:183],v[56:59]\n"
TR "v[190:191],v3 offset:26624\n"
MF "v[56:59],a[80:83],v[184:187],v[56:59]\n"
LD "v202,s[48:51],s80 offen offset:2048 lds\n"
MF "v[64:67],a[84:87],v[184:187],v[252:255]\n"
TR "a[240:241],v3 offset:28672\n"
MF "v[64:67],a[44:47],v[180:183],v[64:67]\n"
TR "a[242:243],v3 offset:30720\n"
MF "v[64:67],a[4:7],v[176:179],v[64:67]\n"
LD "v203,s[48:51],s80 offen offset:3072 lds\n"
MF "v[72:75],a[8:11],v[176:179],v[252:255]\n"
TR "a[244:245],v3 offset:32768\n"
MF "v[72:75],a[48:51],v[180:183],v[72:75]\n"
TR "a[246:247],v3 offset:34816\n"
MF "v[72:75],a[88:91],v[184:187],v[72:75]\n"
MF "v[80:83],a[92:95],v[184:187],v[252:255]\n"
MF "v[80:83],a[52:55],v[180:183],v[80:83]\n"
MF "v[80:83],a[12:15],v[176:179],v[80:83]\n"
MF "v[88:91],a[16:19],v[176:179],v[252:255]\n"
MF "v[88:91],a[56:59],v[180:183],v[88:91]\n"
"s_waitcnt vmcnt(31) lgkmcnt(0)\n"
MF "v[88:91],a[96:99],v[184:187],v[88:91]\n"
"s_barrier\n"
MF "v[92:95],a[96:99],a[244:247],v[252:255]\n"
"s_add_u32 s89,s89,s65\n"
TR "v[176:177],v0\n"
MF "v[92:95],a[56:59],a[240:243],v[92:95]\n"
"s_add_u32 m0,s79,24576\n"
TR "v[178:179],v0 offset:2048\n"
MF "v[92:95],a[16:19],v[188:191],v[92:95]\n"
LD "v192,s[48:51],s89 offen lds\n"
MF "v[84:87],a[12:15],v[188:191],v[252:255]\n"
".Lpg26h:\n"
"s_cbranch_execz .Lpg29h\n"
TR "v[180:181],v0 offset:4096\n"
MF "v[84:87],a[52:55],a[240:243],v[84:87]\n"
TR "v[182:183],v0 offset:6144\n"
MF "v[84:87],a[92:95],a[244:247],v[84:87]\n"
LD "v193,s[48:51],s89 offen offset:1024 lds\n"
MF "v[76:79],a[88:91],a[244:247],v[252:255]\n"
TR "v[184:185],v0 offset:8192\n"
MF "v[76:79],a[48:51],a[240:243],v[76:79]\n"
"s_nop 0\n"
TR "v[186:187],v0 offset:10240\n"
"ds_write_addtid_b32 v136 offset:49152\n"
MF "v[76:79],a[8:11],v[188:191],v[76:79]\n"
"s_nop 0\n"
"ds_write_addtid_b32 v137 offset:49408\n"
"ds_write_addtid_b32 v138 offset:49664\n"
MF "v[68:71],a[4:7],v[188:191],v[252:255]\n"
LD "v194,s[48:51],s89 offen offset:2048 lds\n"
MF "v[68:71],a[44:47],a[240:243],v[68:71]\n"
"buffer_load_dword v10,v12,s[40:43],s86 offen sc1\n"
MF "v[68:71],a[84:87],a[244:247],v[68:71]\n"
"s_nop 0\n"
"ds_write_addtid_b32 v139 offset:49920\n"
"ds_write_addtid_b32 v140 offset:50176\n"
MF "v[60:63],a[80:83],a[244:247],v[252:255]\n"
"s_nop 0\n"
"ds_write_addtid_b32 v141 offset:50432\n"
"ds_write_addtid_b32 v142 offset:50688\n"
MF "v[60:63],a[40:43],a[240:243],v[60:63]\n"
"s_nop 0\n"
"ds_write_addtid_b32 v143 offset:50944\n"
"ds_write_addtid_b32 v144 offset:51200\n"
MF "v[60:63],a[0:3],v[188:191],v[60:63]\n"
"s_waitcnt lgkmcnt(9)\n"
LD "v195,s[48:51],s89 offen offset:3072 lds\n"
MF "v[16:19],a[120:123],v[176:179],v[16:19]\n"
"s_nop 0\n"
TR "v[188:189],v1\n"
"ds_write_addtid_b32 v145 offset:51456\n"
MF "v[16:19],a[160:163],v[180:183],v[16:19]\n"
"s_nop 0\n"
TR "v[190:191],v1 offset:2048\n"
"ds_write_addtid_b32 v146 offset:51712\n"
MF "v[16:19],a[200:203],v[184:187],v[16:19]\n"
"s_nop 0\n"
"ds_write_addtid_b32 v147 offset:51968\n"
"ds_write_addtid_b32 v148 offset:52224\n"
MF "v[24:27],a[204:207],v[184:187],v[24:27]\n"
"s_nop 0\n"
TR "a[240:241],v1 offset:4096\n"
"ds_write_addtid_b32 v149 offset:52480\n"
MF "v[24:27],a[164:167],v[180:183],v[24:27]\n"
"s_add_u32 m0,s79,28672\n"
TR "a[242:243],v1 offset:6144\n"
"ds_write_addtid_b32 v150 offset:48640\n"
MF "v[24:27],a[124:127],v[176:179],v[24:27]\n"
LD "v196,s[48:51],s89 offen lds\n"
MF "v[32:35],a[128:131],v[176:179],v[32:35]\n"
"s_nop 0\n"
TR "a[244:245],v1 offset:8192\n"
"ds_write_addtid_b32 v151 offset:48896\n"
MF "v[32:35],a[168:171],v[180:183],v[32:35]\n"
"s_nop 0\n"
TR "a[246:247],v1 offset:10240\n"
"ds_write_addtid_b32 v152 offset:49152\n"
MF "v[32:35],a[208:211],v[184:187],v[32:35]\n"
LD "v197,s[48:51],s89 offen offset:1024 lds\n"
MF "v[40:43],a[212:215],v[184:187],v[40:43]\n"
"s_nop 0\n"
"ds_write_addtid_b32 v153 offset:49408\n"
"ds_write_addtid_b32 v154 offset:49664\n"
MF "v[40:43],a[172:175],v[180:183],v[40:43]\n"
"s_nop 0\n"
"ds_write_addtid_b32 v155 offset:49920\n"
"ds_write_addtid_b32 v156 offset:50176\n"
MF "v[40:43],a[132:135],v[176:179],v[40:43]\n"
"s_nop 0\n"
"ds_write_addtid_b32 v157 offset:50432\n"
"ds_write_addtid_b32 v158 offset:50688\n"
MF "v[48:51],a[136:139],v[176:179],v[48:51]\n"
"s_nop 0\n"
"ds_write_addtid_b32 v159 offset:50944\n"
"ds_write_addtid_b32 v160 offset:51200\n"
MF "v[48:51],a[176:179],v[180:183],v[48:51]\n"
LD "v198,s[48:51],s89 offen offset:2048 lds\n"
MF "v[48:51],a[216:219],v[184:187],v[48:51]\n"
"s_waitcnt lgkmcnt(9)\n"
"ds_write_addtid_b32 v161 offset:51456\n"
"ds_write_addtid_b32 v162 offset:51712\n"
MF "v[52:55],a[216:219],a[244:247],v[52:55]\n"
"s_nop 0\n"
TR "v[176:177],v2\n"
"ds_write_addtid_b32 v163 offset:51968\n"
MF "v[52:55],a[176:179],a[240:243],v[52:55]\n"
"s_nop 0\n"
TR "v[178:179],v2 offset:2048\n"
"ds_write_addtid_b32 v164 offset:52224\n"
MF "v[52:55],a[136:139],v[188:191],v[52:55]\n"
LD "v199,s[48:51],s89 offen offset:3072 lds\n"
MF "v[44:47],a[132:135],v[188:191],v[44:47]\n"
"s_nop 0\n"
TR "v[180:181],v2 offset:4096\n"
"ds_write_addtid_b32 v165 offset:52480\n"
MF "v[44:47],a[172:175],a[240:243],v[44:47]\n"
"s_nop 0\n"
TR "v[182:183],v2 offset:6144\n"
"ds_write_addtid_b32 v166 offset:52736\n"
MF "v[44:47],a[212:215],a[244:247],v[44:47]\n"
"s_nop 0\n"
"ds_write_addtid_b32 v167 offset:52992\n"
"ds_write_addtid_b32 v168 offset:53248\n"
MF "v[36:39],a[208:211],a[244:247],v[36:39]\n"
"s_nop 0\n"
TR "v[184:185],v2 offset:8192\n"
"ds_write_addtid_b32 v169 offset:53504\n"
MF "v[36:39],a[168:171],a[240:243],v[36:39]\n"
"s_add_u32 m0,s79,32768\n"
TR "v[186:187],v2 offset:10240\n"
"ds_write_addtid_b32 v170 offset:49664\n"
MF "v[36:39],a[128:131],v[188:191],v[36:39]\n"
LD "v200,s[48:51],s89 offen lds\n"
MF "v[28:31],a[124:127],v[188:191],v[28:31]\n"
"s_nop 0\n"
"ds_write_addtid_b32 v171 offset:49920\n"
"ds_write_addtid_b32 v172 offset:50176\n"
MF "v[28:31],a[164:167],a[240:243],v[28:31]\n"
"s_nop 0\n"
"ds_write_addtid_b32 v173 offset:50432\n"
"ds_write_addtid_b32 v174 offset:50688\n"
MF "v[28:31],a[204:207],a[244:247],v[28:31]\n"
"ds_write_addtid_b32 v175 offset:50944\n"
MF "v[20:23],a[200:203],a[244:247],v[20:23]\n"
LD "v201,s[48:51],s89 offen offset:1024 lds\n"
MF "v[20:23],a[160:163],a[240:243],v[20:23]\n"
MF "v[20:23],a[120:123],v[188:191],v[20:23]\n"
"s_waitcnt lgkmcnt(6)\n"
MF "v[56:59],a[120:123],v[176:179],v[56:59]\n"
TR "v[188:189],v3\n"
MF "v[56:59],a[160:163],v[180:183],v[56:59]\n"
TR "v[190:191],v3 offset:2048\n"
MF "v[56:59],a[200:203],v[184:187],v[56:59]\n"
LD "v202,s[48:51],s89 offen offset:2048 lds\n"
MF "v[64:67],a[204:207],v[184:187],v[64:67]\n"
TR "a[240:241],v3 offset:4096\n"
MF "v[64:67],a[164:167],v[180:183],v[64:67]\n"
TR "a[242:243],v3 offset:6144\n"
MF "v[64:67],a[124:127],v[176:179],v[64:67]\n"
LD "v203,s[48:51],s89 offen offset:3072 lds\n"
MF "v[72:75],a[128:131],v[176:179],v[72:75]\n"
TR "a[244:245],v3 offset:8192\n"
MF "v[72:75],a[168:171],v[180:183],v[72:75]\n"
TR "a[246:247],v3 offset:10240\n"
MF "v[72:75],a[208:211],v[184:187],v[72:75]\n"
MF "v[80:83],a[212:215],v[184:187],v[80:83]\n"
MF "v[80:83],a[172:175],v[180:183],v[80:83]\n"
MF "v[80:83],a[132:135],v[176:179],v[80:83]\n"
MF "v[88:91],a[136:139],v[176:179],v[88:91]\n"
MF "v[88:91],a[176:179],v[180:183],v[88:91]\n"
"s_waitcnt vmcnt(27) lgkmcnt(0)\n"
MF "v[88:91],a[216:219],v[184:187],v[88:91]\n"
"s_barrier\n"
MF "v[92:95],a[216:219],a[244:247],v[92:95]\n"
"s_add_u32 s80,s89,122880\n"
TR "v[176:177],v0 offset:49152\n"
MF "v[92:95],a[176:179],a[240:243],v[92:95]\n"
"s_add_u32 m0,s79,0\n"
"s_mov_b32 s82,s70\n"
TR "v[178:179],v0 offset:51200\n"
MF "v[92:95],a[136:139],v[188:191],v[92:95]\n"
LD "v192,s[48:51],s80 offen lds\n"
MF "v[84:87],a[132:135],v[188:191],v[84:87]\n"
TR "v[180:181],v0 offset:53248\n"
MF "v[84:87],a[172:175],a[240:243],v[84:87]\n"
TR "v[182:183],v0 offset:55296\n"
MF "v[84:87],a[212:215],a[244:247],v[84:87]\n"
LD "v193,s[48:51],s80 offen offset:1024 lds\n"
MF "v[76:79],a[208:211],a[244:247],v[76:79]\n"
TR "v[184:185],v0 offset:57344\n"
MF "v[76:79],a[168:171],a[240:243],v[76:79]\n"
TR "v[186:187],v0 offset:59392\n"
MF "v[76:79],a[128:131],v[188:191],v[76:79]\n"
MF "v[68:71],a[124:127],v[188:191],v[68:71]\n"
MF "v[68:71],a[164:167],a[240:243],v[68:71]\n"
LD "v194,s[48:51],s80 offen offset:2048 lds\n"
MF "v[68:71],a[204:207],a[244:247],v[68:71]\n"
MF "v[60:63],a[200:203],a[244:247],v[60:63]\n"
MF "v[60:63],a[160:163],a[240:243],v[60:63]\n"
MF "v[60:63],a[120:123],v[188:191],v[60:63]\n"
"s_waitcnt lgkmcnt(0)\n"
LD "v195,s[48:51],s80 offen offset:3072 lds\n"
MF "v[16:19],a[20:23],v[176:179],v[16:19]\n"
TR "v[188:189],v1 offset:49152\n"
MF "v[16:19],a[60:63],v[180:183],v[16:19]\n"
TR "v[190:191],v1 offset:51200\n"
MF "v[16:19],a[100:103],v[184:187],v[16:19]\n"
MF "v[24:27],a[104:107],v[184:187],v[24:27]\n"
TR "a[240:241],v1 offset:53248\n"
MF "v[24:27],a[64:67],v[180:183],v[24:27]\n"
"s_add_u32 m0,s79,4096\n"
TR "a[242:243],v1 offset:55296\n"
MF "v[24:27],a[24:27],v[176:179],v[24:27]\n"
LD "v196,s[48:51],s80 offen lds\n"
MF "v[32:35],a[28:31],v[176:179],v[32:35]\n"
TR "a[244:245],v1 offset:57344\n"
MF "v[32:35],a[68:71],v[180:183],v[32:35]\n"
TR "a[246:247],v1 offset:59392\n"
MF "v[32:35],a[108:111],v[184:187],v[32:35]\n"
LD "v197,s[48:51],s80 offen offset:1024 lds\n"
MF "v[40:43],a[112:115],v[184:187],v[40:43]\n"
MF "v[40:43],a[72:75],v[180:183],v[40:43]\n"
MF "v[40:43],a[32:35],v[176:179],v[40:43]\n"
MF "v[48:51],a[36:39],v[176:179],v[48:51]\n"
MF "v[48:51],a[76:79],v[180:183],v[48:51]\n"
LD "v198,s[48:51],s80 offen offset:2048 lds\n"
MF "v[48:51],a[116:119],v[184:187],v[48:51]\n"
"s_waitcnt lgkmcnt(0)\n"
MF "v[52:55],a[116:119],a[244:247],v[52:55]\n"
TR "v[176:177],v2 offset:49152\n"
MF "v[52:55],a[76:79],a[240:243],v[52:55]\n"
TR "v[178:179],v2 offset:51200\n"
MF "v[52:55],a[36:39],v[188:191],v[52:55]\n"
LD "v199,s[48:51],s80 offen offset:3072 lds\n"
MF "v[44:47],a[32:35],v[188:191],v[44:47]\n"
TR "v[180:181],v2 offset:53248\n"
MF "v[44:47],a[72:75],a[240:243],v[44:47]\n"
TR "v[182:183],v2 offset:55296\n"
MF "v[44:47],a[112:115],a[244:247],v[44:47]\n"
MF "v[36:39],a[108:111],a[244:247],v[36:39]\n"
TR "v[184:185],v2 offset:57344\n"
MF "v[36:39],a[68:71],a[240:243],v[36:39]\n"
"s_add_u32 m0,s79,8192\n"
TR "v[186:187],v2 offset:59392\n"
MF "v[36:39],a[28:31],v[188:191],v[36:39]\n"
LD "v200,s[48:51],s80 offen lds\n"
MF "v[28:31],a[24:27],v[188:191],v[28:31]\n"
MF "v[28:31],a[64:67],a[240:243],v[28:31]\n"
MF "v[28:31],a[104:107],a[244:247],v[28:31]\n"
MF "v[20:23],a[100:103],a[244:247],v[20:23]\n"
LD "v201,s[48:51],s80 offen offset:1024 lds\n"
MF "v[20:23],a[60:63],a[240:243],v[20:23]\n"
"s_waitcnt vmcnt(19)\n"
MF "v[20:23],a[20:23],v[188:191],v[20:23]\n"
"v_mov_b32 v236,v228\n"
"v_mov_b32 v237,v229\n"
MF "v[16:19],a[252:255],v[208:211],v[16:19]\n"
MF "v[24:27],a[252:255],v[212:215],v[24:27]\n"
MF "v[32:35],a[252:255],v[216:219],v[32:35]\n"
MF "v[40:43],a[252:255],v[220:223],v[40:43]\n"
MF "v[48:51],a[252:255],v[224:227],v[48:51]\n"
MF "v[20:23],a[248:251],v[208:211],v[20:23]\n"
MF "v[28:31],a[248:251],v[212:215],v[28:31]\n"
MF "v[36:39],a[248:251],v[216:219],v[36:39]\n"
MF "v[44:47],a[248:251],v[220:223],v[44:47]\n"
MF "v[52:55],a[248:251],v[224:227],v[52:55]\n"
MF "v[248:251],v[230:233],a[250:253],v[248:251]\n"
MF "v[244:247],v[234:237],a[250:253],v[244:247]\n"
"s_waitcnt lgkmcnt(0)\n"
MF "v[56:59],a[20:23],v[176:179],v[56:59]\n"
TR "v[188:189],v3 offset:49152\n"
"v_sub_u32 v10,v10,v11\n"
MF "v[56:59],a[60:63],v[180:183],v[56:59]\n"
TR "v[190:191],v3 offset:51200\n"
MF "v[56:59],a[100:103],v[184:187],v[56:59]\n"
LD "v202,s[48:51],s80 offen offset:2048 lds\n"
MF "v[64:67],a[104:107],v[184:187],v[64:67]\n"
TR "a[240:241],v3 offset:53248\n"
MF "v[64:67],a[64:67],v[180:183],v[64:67]\n"
TR "a[242:243],v3 offset:55296\n"
MF "v[64:67],a[24:27],v[176:179],v[64:67]\n"
LD "v203,s[48:51],s80 offen offset:3072 lds\n"
MF "v[72:75],a[28:31],v[176:179],v[72:75]\n"
TR "a[244:245],v3 offset:57344\n"
MF "v[72:75],a[68:71],v[180:183],v[72:75]\n"
TR "a[246:247],v3 offset:59392\n"
MF "v[72:75],a[108:111],v[184:187],v[72:75]\n"
".Lnf2:\n"
"v_readlane_b32 s58,v10,s67\n"
"s_cmp_lt_u32 s58,s85\n"
"s_cbranch_scc1 .Lsp2\n"
".Lnb2:\n"
MF "v[80:83],a[112:115],v[184:187],v[80:83]\n"
"s_add_u32 s70,s70,20480\n"
MF "v[80:83],a[72:75],v[180:183],v[80:83]\n"
MF "v[80:83],a[32:35],v[176:179],v[80:83]\n"
MF "v[88:91],a[36:39],v[176:179],v[88:91]\n"
"s_add_u32 s2,s2,204800\n"
MF "v[88:91],a[76:79],v[180:183],v[88:91]\n"
"s_waitcnt vmcnt(25) lgkmcnt(0)\n"
LD "v[228:231],v9,s[40:43],s2 offen sc1\n"
MF "v[88:91],a[116:119],v[184:187],v[88:91]\n"
"s_barrier\n"
MF "v[92:95],a[116:119],a[244:247],v[92:95]\n"
"s_add_u32 s88,s88,s65\n"
TR "v[176:177],v4\n"
MF "v[92:95],a[76:79],a[240:243],v[92:95]\n"
"s_add_u32 m0,s79,49152\n"
TR "v[178:179],v4 offset:2048\n"
MF "v[92:95],a[36:39],v[188:191],v[92:95]\n"
LD "v192,s[48:51],s88 offen lds\n"
MF "v[84:87],a[32:35],v[188:191],v[84:87]\n"
TR "v[180:181],v4 offset:4096\n"
MF "v[84:87],a[72:75],a[240:243],v[84:87]\n"
TR "v[182:183],v4 offset:6144\n"
MF "v[84:87],a[112:115],a[244:247],v[84:87]\n"
LD "v193,s[48:51],s88 offen offset:1024 lds\n"
MF "v[76:79],a[108:111],a[244:247],v[76:79]\n"
TR "v[184:185],v4 offset:8192\n"
MF "v[76:79],a[68:71],a[240:243],v[76:79]\n"
"s_add_u32 s2,s2,204800\n"
TR "v[186:187],v4 offset:10240\n"
MF "v[76:79],a[28:31],v[188:191],v[76:79]\n"
LD "v[232:235],v9,s[40:43],s2 offen sc1\n"
MF "v[68:71],a[24:27],v[188:191],v[68:71]\n"
MF "v[68:71],a[64:67],a[240:243],v[68:71]\n"
LD "v194,s[48:51],s88 offen offset:2048 lds\n"
MF "v[68:71],a[104:107],a[244:247],v[68:71]\n"
MF "v[60:63],a[100:103],a[244:247],v[60:63]\n"
MF "v[60:63],a[60:63],a[240:243],v[60:63]\n"
MF "v[60:63],a[20:23],v[188:191],v[60:63]\n"
"s_waitcnt lgkmcnt(0)\n"
LD "v195,s[48:51],s88 offen offset:3072 lds\n"
MF "v[16:19],a[140:143],v[176:179],v[16:19]\n"
TR "v[188:189],v5\n"
MF "v[16:19],a[180:183],v[180:183],v[16:19]\n"
TR "v[190:191],v5 offset:2048\n"
MF "v[16:19],a[220:223],v[184:187],v[16:19]\n"
LD "v[208:211],v9,s[40:43],s82 offen sc1\n"
MF "v[24:27],a[224:227],v[184:187],v[24:27]\n"
TR "a[240:241],v5 offset:4096\n"
MF "v[24:27],a[184:187],v[180:183],v[24:27]\n"
"s_add_u32 m0,s79,53248\n"
TR "a[242:243],v5 offset:6144\n"
MF "v[24:27],a[144:147],v[176:179],v[24:27]\n"
LD "v196,s[48:51],s88 offen lds\n"
MF "v[32:35],a[148:151],v[176:179],v[32:35]\n"
TR "a[244:245],v5 offset:8192\n"
MF "v[32:35],a[188:191],v[180:183],v[32:35]\n"
TR "a[246:247],v5 offset:10240\n"
MF "v[32:35],a[228:231],v[184:187],v[32:35]\n"
LD "v197,s[48:51],s88 offen offset:1024 lds\n"
MF "v[40:43],a[232:235],v[184:187],v[40:43]\n"
MF "v[40:43],a[192:195],v[180:183],v[40:43]\n"
MF "v[40:43],a[152:155],v[176:179],v[40:43]\n"
MF "v[48:51],a[156:159],v[176:179],v[48:51]\n"
LD "v[212:215],v9,s[40:43],s82 offen offset:1024 sc1\n"
MF "v[48:51],a[196:199],v[180:183],v[48:51]\n"
LD "v198,s[48:51],s88 offen offset:2048 lds\n"
MF "v[48:51],a[236:239],v[184:187],v[48:51]\n"
"s_waitcnt lgkmcnt(0)\n"
MF "v[52:55],a[236:239],a[244:247],v[52:55]\n"
TR "v[176:177],v6\n"
MF "v[52:55],a[196:199],a[240:243],v[52:55]\n"
TR "v[178:179],v6 offset:2048\n"
MF "v[52:55],a[156:159],v[188:191],v[52:55]\n"
LD "v199,s[48:51],s88 offen offset:3072 lds\n"
MF "v[44:47],a[152:155],v[188:191],v[44:47]\n"
TR "v[180:181],v6 offset:4096\n"
MF "v[44:47],a[192:195],a[240:243],v[44:47]\n"
TR "v[182:183],v6 offset:6144\n"
MF "v[44:47],a[232:235],a[244:247],v[44:47]\n"
MF "v[36:39],a[228:231],a[244:247],v[36:39]\n"
TR "v[184:185],v6 offset:8192\n"
MF "v[36:39],a[188:191],a[240:243],v[36:39]\n"
"s_add_u32 m0,s79,57344\n"
TR "v[186:187],v6 offset:10240\n"
MF "v[36:39],a[148:151],v[188:191],v[36:39]\n"
LD "v200,s[48:51],s88 offen lds\n"
MF "v[28:31],a[144:147],v[188:191],v[28:31]\n"
LD "v[216:219],v9,s[40:43],s82 offen offset:2048 sc1\n"
MF "v[28:31],a[184:187],a[240:243],v[28:31]\n"
MF "v[28:31],a[224:227],a[244:247],v[28:31]\n"
MF "v[20:23],a[220:223],a[244:247],v[20:23]\n"
LD "v201,s[48:51],s88 offen offset:1024 lds\n"
MF "v[20:23],a[180:183],a[240:243],v[20:23]\n"
MF "v[20:23],a[140:143],v[188:191],v[20:23]\n"
"s_waitcnt lgkmcnt(0)\n"
MF "v[56:59],a[140:143],v[176:179],v[56:59]\n"
TR "v[188:189],v7\n"
MF "v[56:59],a[180:183],v[180:183],v[56:59]\n"
TR "v[190:191],v7 offset:2048\n"
MF "v[56:59],a[220:223],v[184:187],v[56:59]\n"
LD "v202,s[48:51],s88 offen offset:2048 lds\n"
MF "v[64:67],a[224:227],v[184:187],v[64:67]\n"
TR "a[240:241],v7 offset:4096\n"
MF "v[64:67],a[184:187],v[180:183],v[64:67]\n"
TR "a[242:243],v7 offset:6144\n"
MF "v[64:67],a[144:147],v[176:179],v[64:67]\n"
LD "v203,s[48:51],s88 offen offset:3072 lds\n"
MF "v[72:75],a[148:151],v[176:179],v[72:75]\n"
TR "a[244:245],v7 offset:8192\n"
MF "v[72:75],a[188:191],v[180:183],v[72:75]\n"
TR "a[246:247],v7 offset:10240\n"
MF "v[72:75],a[228:231],v[184:187],v[72:75]\n"
LD "v[220:223],v9,s[40:43],s82 offen offset:3072 sc1\n"
MF "v[80:83],a[232:235],v[184:187],v[80:83]\n"
MF "v[80:83],a[192:195],v[180:183],v[80:83]\n"
MF "v[80:83],a[152:155],v[176:179],v[80:83]\n"
MF "v[88:91],a[156:159],v[176:179],v[88:91]\n"
"s_add_u32 s82,s82,4096\n"
MF "v[88:91],a[196:199],v[180:183],v[88:91]\n"
"s_waitcnt vmcnt(30) lgkmcnt(0)\n"
LD "v[224:227],v9,s[40:43],s82 offen sc1\n"
MF "v[88:91],a[236:239],v[184:187],v[88:91]\n"
"s_barrier\n"
MF "v[92:95],a[236:239],a[244:247],v[92:95]\n"
"s_add_u32 s80,s88,122880\n"
TR "v[176:177],v0 offset:24576\n"
MF "v[92:95],a[196:199],a[240:243],v[92:95]\n"
"s_add_u32 m0,s79,98304\n"
"s_add_u32 s85,s85,1\n"
TR "v[178:179],v0 offset:26624\n"
MF "v[92:95],a[156:159],v[188:191],v[92:95]\n"
LD "v192,s[48:51],s80 offen lds\n"
MF "v[84:87],a[152:155],v[188:191],v[84:87]\n"
"s_nop 0\n"
TR "v[180:181],v0 offset:28672\n"
"ds_write_addtid_b32 v16 offset:24576\n"
MF "v[84:87],a[192:195],a[240:243],v[84:87]\n"
"s_nop 0\n"
TR "v[182:183],v0 offset:30720\n"
"ds_write_addtid_b32 v17 offset:24832\n"
MF "v[84:87],a[232:235],a[244:247],v[84:87]\n"
LD "v193,s[48:51],s80 offen offset:1024 lds\n"
MF "v[76:79],a[228:231],a[244:247],v[76:79]\n"
"s_nop 0\n"
TR "v[184:185],v0 offset:32768\n"
"ds_write_addtid_b32 v18 offset:25088\n"
MF "v[76:79],a[188:191],a[240:243],v[76:79]\n"
"s_nop 0\n"
TR "v[186:187],v0 offset:34816\n"
"ds_write_addtid_b32 v19 offset:25344\n"
MF "v[76:79],a[148:151],v[188:191],v[76:79]\n"
"s_nop 0\n"
"ds_write_addtid_b32 v20 offset:25600\n"
"ds_write_addtid_b32 v21 offset:25856\n"
MF "v[68:71],a[144:147],v[188:191],v[68:71]\n"
LD "v194,s[48:51],s80 offen offset:2048 lds\n"
MF "v[68:71],a[184:187],a[240:243],v[68:71]\n"
"s_nop 0\n"
"ds_write_addtid_b32 v22 offset:26112\n"
"ds_write_addtid_b32 v23 offset:26368\n"
MF "v[68:71],a[224:227],a[244:247],v[68:71]\n"
"s_nop 0\n"
"ds_write_addtid_b32 v24 offset:26624\n"
"ds_write_addtid_b32 v25 offset:26880\n"
MF "v[60:63],a[220:223],a[244:247],v[60:63]\n"
"s_nop 0\n"
"ds_write_addtid_b32 v26 offset:27136\n"
"ds_write_addtid_b32 v27 offset:27392\n"
MF "v[60:63],a[180:183],a[240:243],v[60:63]\n"
MF "v[60:63],a[140:143],v[188:191],v[60:63]\n"
"s_waitcnt lgkmcnt(9)\n"
"s_cmp_lg_u32 s85,9\n"
"s_cbranch_scc1 .Lpl09\n"
".Lplast0:\n"
MF "v[96:99],v[176:179],a[0:3],v[252:255]\n"
"s_nop 0\n"
TR "v[188:189],v1 offset:24576\n"
TR "v[190:191],v1 offset:26624\n"
MF "v[96:99],v[180:183],a[40:43],v[96:99]\n"
"s_nop 0\n"
TR "a[240:241],v1 offset:28672\n"
TR "a[242:243],v1 offset:30720\n"
MF "v[96:99],v[184:187],a[80:83],v[96:99]\n"
"s_add_u32 s90,s91,163840\n"
TR "a[244:245],v1 offset:32768\n"
TR "a[246:247],v1 offset:34816\n"
MF "v[104:107],v[184:187],a[84:87],v[252:255]\n"
LD "v195,s[48:51],s80 offen offset:3072 lds\n"
MF "v[104:107],v[180:183],a[44:47],v[104:107]\n"
"s_nop 0\n"
"ds_write_addtid_b32 v28 offset:27648\n"
"ds_write_addtid_b32 v29 offset:27904\n"
MF "v[104:107],v[176:179],a[4:7],v[104:107]\n"
"s_nop 0\n"
"ds_write_addtid_b32 v30 offset:28160\n"
"ds_write_addtid_b32 v31 offset:28416\n"
MF "v[112:115],v[176:179],a[8:11],v[252:255]\n"
"s_nop 0\n"
"ds_write_addtid_b32 v32 offset:28672\n"
"ds_write_addtid_b32 v33 offset:28928\n"
MF "v[112:115],v[180:183],a[48:51],v[112:115]\n"
"s_add_u32 m0,s79,102400\n"
"s_nop 0\n"
"ds_write_addtid_b32 v34 offset:25088\n"
"ds_write_addtid_b32 v35 offset:25344\n"
MF "v[112:115],v[184:187],a[88:91],v[112:115]\n"
LD "v196,s[48:51],s80 offen lds\n"
MF "v[120:123],v[184:187],a[92:95],v[252:255]\n"
"s_nop 0\n"
"ds_write_addtid_b32 v36 offset:25600\n"
"ds_write_addtid_b32 v37 offset:25856\n"
MF "v[120:123],v[180:183],a[52:55],v[120:123]\n"
"ds_write_addtid_b32 v38 offset:26112\n"
MF "v[120:123],v[176:179],a[12:15],v[120:123]\n"
"ds_write_addtid_b32 v39 offset:26368\n"
MF "v[128:131],v[176:179],a[16:19],v[252:255]\n"
"ds_write_addtid_b32 v40 offset:26624\n"
MF "v[128:131],v[180:183],a[56:59],v[128:131]\n"
"ds_write_addtid_b32 v41 offset:26880\n"
MF "v[128:131],v[184:187],a[96:99],v[128:131]\n"
"s_waitcnt lgkmcnt(14)\n"
LD "v197,s[48:51],s80 offen offset:1024 lds\n"
MF "v[132:135],a[244:247],a[96:99],v[252:255]\n"
"s_nop 0\n"
TR "v[176:177],v2 offset:24576\n"
TR "v[178:179],v2 offset:26624\n"
MF "v[132:135],a[240:243],a[56:59],v[132:135]\n"
"s_nop 0\n"
TR "v[180:181],v2 offset:28672\n"
TR "v[182:183],v2 offset:30720\n"
MF "v[132:135],v[188:191],a[16:19],v[132:135]\n"
"s_nop 0\n"
TR "v[184:185],v2 offset:32768\n"
TR "v[186:187],v2 offset:34816\n"
MF "v[124:127],v[188:191],a[12:15],v[252:255]\n"
"s_nop 0\n"
"ds_write_addtid_b32 v42 offset:27136\n"
"ds_write_addtid_b32 v43 offset:27392\n"
MF "v[124:127],a[240:243],a[52:55],v[124:127]\n"
"s_nop 0\n"
"ds_write_addtid_b32 v44 offset:27648\n"
"ds_write_addtid_b32 v45 offset:27904\n"
MF "v[124:127],a[244:247],a[92:95],v[124:127]\n"
"s_nop 0\n"
"ds_write_addtid_b32 v46 offset:28160\n"
"ds_write_addtid_b32 v47 offset:28416\n"
MF "v[116:119],a[244:247],a[88:91],v[252:255]\n"
LD "v198,s[48:51],s80 offen offset:2048 lds\n"
MF "v[116:119],a[240:243],a[48:51],v[116:119]\n"
"s_nop 0\n"
"ds_write_addtid_b32 v48 offset:28672\n"
"ds_write_addtid_b32 v49 offset:28928\n"
MF "v[116:119],v[188:191],a[8:11],v[116:119]\n"
"s_nop 0\n"
"ds_write_addtid_b32 v50 offset:29184\n"
"ds_write_addtid_b32 v51 offset:29440\n"
MF "v[108:111],v[188:191],a[4:7],v[252:255]\n"
"ds_write_addtid_b32 v52 offset:29696\n"
MF "v[108:111],a[240:243],a[44:47],v[108:111]\n"
"ds_write_addtid_b32 v53 offset:29952\n"
MF "v[108:111],a[244:247],a[84:87],v[108:111]\n"
"ds_write_addtid_b32 v54 offset:30208\n"
MF "v[100:103],a[244:247],a[80:83],v[252:255]\n"
"ds_write_addtid_b32 v55 offset:30464\n"
MF "v[100:103],a[240:243],a[40:43],v[100:103]\n"
LD "v199,s[48:51],s80 offen offset:3072 lds\n"
MF "v[100:103],v[188:191],a[0:3],v[100:103]\n"
"s_waitcnt lgkmcnt(14)\n"
MF "v[136:139],v[176:179],a[0:3],v[252:255]\n"
"s_nop 0\n"
TR "v[188:189],v3 offset:24576\n"
TR "v[190:191],v3 offset:26624\n"
MF "v[136:139],v[180:183],a[40:43],v[136:139]\n"
".Lpg27h:\n"
"s_cbranch_execz .Lpg30h\n"
"s_add_u32 m0,s79,106496\n"
TR "a[240:241],v3 offset:28672\n"
TR "a[242:243],v3 offset:30720\n"
MF "v[136:139],v[184:187],a[80:83],v[136:139]\n"
"s_nop 0\n"
TR "a[244:245],v3 offset:32768\n"
TR "a[246:247],v3 offset:34816\n"
"s_waitcnt lgkmcnt(0)\n"
MF "v[144:147],v[184:187],a[84:87],v[252:255]\n"
"s_barrier\n"
MF "v[144:147],v[180:183],a[44:47],v[144:147]\n"
LD "v200,s[48:51],s80 offen lds\n"
MF "v[144:147],v[176:179],a[4:7],v[144:147]\n"
"ds_read2st64_b32 v[16:17],v13 offset0:0 offset1:1\n"
MF "v[152:155],v[176:179],a[8:11],v[252:255]\n"
"ds_read2st64_b32 v[18:19],v13 offset0:2 offset1:3\n"
MF "v[152:155],v[180:183],a[48:51],v[152:155]\n"
"ds_read2st64_b32 v[20:21],v13 offset0:4 offset1:5\n"
MF "v[152:155],v[184:187],a[88:91],v[152:155]\n"
"ds_read2st64_b32 v[22:23],v13 offset0:6 offset1:7\n"
MF "v[160:163],v[184:187],a[92:95],v[252:255]\n"
"ds_read2st64_b32 v[24:25],v13 offset0:8 offset1:9\n"
MF "v[160:163],v[180:183],a[52:55],v[160:163]\n"
"ds_read2st64_b32 v[26:27],v13 offset0:10 offset1:11\n"
MF "v[160:163],v[176:179],a[12:15],v[160:163]\n"
LD "v201,s[48:51],s80 offen offset:1024 lds\n"
MF "v[168:171],v[176:179],a[16:19],v[252:255]\n"
"ds_read2st64_b32 v[28:29],v13 offset0:12 offset1:13\n"
MF "v[168:171],v[180:183],a[56:59],v[168:171]\n"
"ds_read2st64_b32 v[30:31],v13 offset0:14 offset1:15\n"
"s_waitcnt vmcnt(29) lgkmcnt(0)\n"
MF "v[168:171],v[184:187],a[96:99],v[168:171]\n"
"s_barrier\n"
MF "v[172:175],a[244:247],a[96:99],v[252:255]\n"
"s_nop 0\n"
TR "v[176:177],v0\n"
TR "v[178:179],v0 offset:2048\n"
MF "v[172:175],a[240:243],a[56:59],v[172:175]\n"
"s_nop 0\n"
TR "v[180:181],v0 offset:4096\n"
TR "v[182:183],v0 offset:6144\n"
MF "v[172:175],v[188:191],a[16:19],v[172:175]\n"
"s_nop 0\n"
TR "v[184:185],v0 offset:8192\n"
TR "v[186:187],v0 offset:10240\n"
MF "v[164:167],v[188:191],a[12:15],v[252:255]\n"
LD "v202,s[48:51],s80 offen offset:2048 lds\n"
MF "v[164:167],a[240:243],a[52:55],v[164:167]\n"
"ds_read2st64_b32 v[32:33],v13 offset0:16 offset1:17\n"
"v_add_f32 v56,v56,v16\n"
MF "v[164:167],a[244:247],a[92:95],v[164:167]\n"
"ds_read2st64_b32 v[34:35],v13 offset0:18 offset1:19\n"
"v_add_f32 v57,v57,v17\n"
MF "v[156:159],a[244:247],a[88:91],v[252:255]\n"
"ds_read2st64_b32 v[36:37],v13 offset0:20 offset1:21\n"
"v_add_f32 v58,v58,v18\n"
MF "v[156:159],a[240:243],a[48:51],v[156:159]\n"
"ds_read2st64_b32 v[38:39],v13 offset0:22 offset1:23\n"
"v_add_f32 v59,v59,v19\n"
MF "v[156:159],v[188:191],a[8:11],v[156:159]\n"
"ds_read2st64_b32 v[40:41],v13 offset0:24 offset1:25\n"
"v_add_f32 v60,v60,v20\n"
MF "v[148:151],v[188:191],a[4:7],v[252:255]\n"
LD "v203,s[48:51],s80 offen offset:3072 lds\n"
MF "v[148:151],a[240:243],a[44:47],v[148:151]\n"
"ds_read2st64_b32 v[42:43],v13 offset0:26 offset1:27\n"
"v_add_f32 v61,v61,v21\n"
MF "v[148:151],a[244:247],a[84:87],v[148:151]\n"
"ds_read2st64_b32 v[44:45],v13 offset0:28 offset1:29\n"
"v_add_f32 v62,v62,v22\n"
MF "v[140:143],a[244:247],a[80:83],v[252:255]\n"
"ds_read2st64_b32 v[46:47],v13 offset0:30 offset1:31\n"
"v_add_f32 v63,v63,v23\n"
MF "v[140:143],a[240:243],a[40:43],v[140:143]\n"
"ds_read2st64_b32 v[48:49],v13 offset0:32 offset1:33\n"
CV "v56,v56,v57\n"
MF "v[140:143],v[188:191],a[0:3],v[140:143]\n"
"s_waitcnt lgkmcnt(9)\n"
"ds_read2st64_b32 v[50:51],v13 offset0:34 offset1:35\n"
CV "v57,v58,v59\n"
MF "v[96:99],v[176:179],a[120:123],v[96:99]\n"
"s_nop 0\n"
TR "v[188:189],v1\n"
TR "v[190:191],v1 offset:2048\n"
MF "v[96:99],v[180:183],a[160:163],v[96:99]\n"
"s_nop 0\n"
TR "a[240:241],v1 offset:4096\n"
TR "a[242:243],v1 offset:6144\n"
MF "v[96:99],v[184:187],a[200:203],v[96:99]\n"
"s_nop 0\n"
TR "a[244:245],v1 offset:8192\n"
TR "a[246:247],v1 offset:10240\n"
MF "v[104:107],v[184:187],a[204:207],v[104:107]\n"
"ds_read2st64_b32 v[52:53],v13 offset0:36 offset1:37\n"
CV "v58,v60,v61\n"
MF "v[104:107],v[180:183],a[164:167],v[104:107]\n"
"s_add_u32 s87,s90,10240\n"
"ds_read2st64_b32 v[54:55],v13 offset0:38 offset1:39\n"
CV "v59,v62,v63\n"
MF "v[104:107],v[176:179],a[124:127],v[104:107]\n"
"buffer_store_dwordx4 v[56:59],v9,s[40:43],s87 offen sc1\n"
MF "v[112:115],v[176:179],a[128:131],v[112:115]\n"
"v_add_f32 v64,v64,v24\n"
"v_add_f32 v65,v65,v25\n"
MF "v[112:115],v[180:183],a[168:171],v[112:115]\n"
"v_add_f32 v66,v66,v26\n"
"v_add_f32 v67,v67,v27\n"
MF "v[112:115],v[184:187],a[208:211],v[112:115]\n"
"v_add_f32 v68,v68,v28\n"
"v_add_f32 v69,v69,v29\n"
MF "v[120:123],v[184:187],a[212:215],v[120:123]\n"
"v_add_f32 v70,v70,v30\n"
"v_add_f32 v71,v71,v31\n"
MF "v[120:123],v[180:183],a[172:175],v[120:123]\n"
CV "v64,v64,v65\n"
CV "v65,v66,v67\n"
MF "v[120:123],v[176:179],a[132:135],v[120:123]\n"
"s_add_u32 s87,s90,11264\n"
CV "v66,v68,v69\n"
CV "v67,v70,v71\n"
MF "v[128:131],v[176:179],a[136:139],v[128:131]\n"
"s_waitcnt lgkmcnt(14)\n"
"buffer_store_dwordx4 v[64:67],v9,s[40:43],s87 offen sc1\n"
MF "v[128:131],v[180:183],a[176:179],v[128:131]\n"
"v_add_f32 v72,v72,v32\n"
"v_add_f32 v73,v73,v33\n"
MF "v[128:131],v[184:187],a[216:219],v[128:131]\n"
"s_waitcnt lgkmcnt(2)\n"
"v_add_f32 v74,v74,v34\n"
"v_add_f32 v75,v75,v35\n"
MF "v[132:135],a[244:247],a[216:219],v[132:135]\n"
"s_nop 0\n"
TR "v[176:177],v2\n"
TR "v[178:179],v2 offset:2048\n"
MF "v[132:135],a[240:243],a[176:179],v[132:135]\n"
"s_nop 0\n"
TR "v[180:181],v2 offset:4096\n"
TR "v[182:183],v2 offset:6144\n"
MF "v[132:135],v[188:191],a[136:139],v[132:135]\n"
"s_nop 0\n"
TR "v[184:185],v2 offset:8192\n"
TR "v[186:187],v2 offset:10240\n"
MF "v[124:127],v[188:191],a[132:135],v[124:127]\n"
"v_add_f32 v76,v76,v36\n"
"v_add_f32 v77,v77,v37\n"
MF "v[124:127],a[240:243],a[172:175],v[124:127]\n"
"v_add_f32 v78,v78,v38\n"
"v_add_f32 v79,v79,v39\n"
MF "v[124:127],a[244:247],a[212:215],v[124:127]\n"
CV "v72,v72,v73\n"
CV "v73,v74,v75\n"
MF "v[116:119],a[244:247],a[208:211],v[116:119]\n"
"s_add_u32 s87,s90,12288\n"
CV "v74,v76,v77\n"
CV "v75,v78,v79\n"
MF "v[116:119],a[240:243],a[168:171],v[116:119]\n"
"buffer_store_dwordx4 v[72:75],v9,s[40:43],s87 offen sc1\n"
MF "v[116:119],v[188:191],a[128:131],v[116:119]\n"
"v_add_f32 v80,v80,v40\n"
"v_add_f32 v81,v81,v41\n"
MF "v[108:111],v[188:191],a[124:127],v[108:111]\n"
"v_add_f32 v82,v82,v42\n"
"v_add_f32 v83,v83,v43\n"
MF "v[108:111],a[240:243],a[164:167],v[108:111]\n"
"v_add_f32 v84,v84,v44\n"
"v_add_f32 v85,v85,v45\n"
MF "v[108:111],a[244:247],a[204:207],v[108:111]\n"
"v_add_f32 v86,v86,v46\n"
"v_add_f32 v87,v87,v47\n"
MF "v[100:103],a[244:247],a[200:203],v[100:103]\n"
CV "v80,v80,v81\n"
CV "v81,v82,v83\n"
MF "v[100:103],a[240:243],a[160:163],v[100:103]\n"
"s_add_u32 s87,s90,13312\n"
CV "v82,v84,v85\n"
CV "v83,v86,v87\n"
MF "v[100:103],v[188:191],a[120:123],v[100:103]\n"
"s_waitcnt lgkmcnt(0)\n"
"buffer_store_dwordx4 v[80:83],v9,s[40:43],s87 offen sc1\n"
MF "v[136:139],v[176:179],a[120:123],v[136:139]\n"
"s_nop 0\n"
TR "v[188:189],v3\n"
TR "v[190:191],v3 offset:2048\n"
MF "v[136:139],v[180:183],a[160:163],v[136:139]\n"
"s_nop 0\n"
TR "a[240:241],v3 offset:4096\n"
TR "a[242:243],v3 offset:6144\n"
MF "v[136:139],v[184:187],a[200:203],v[136:139]\n"
"s_nop 0\n"
TR "a[244:245],v3 offset:8192\n"
TR "a[246:247],v3 offset:10240\n"
MF "v[144:147],v[184:187],a[204:207],v[144:147]\n"
"v_add_f32 v88,v88,v48\n"
"v_add_f32 v89,v89,v49\n"
MF "v[144:147],v[180:183],a[164:167],v[144:147]\n"
"v_add_f32 v90,v90,v50\n"
"v_add_f32 v91,v91,v51\n"
MF "v[144:147],v[176:179],a[124:127],v[144:147]\n"
"v_add_f32 v92,v92,v52\n"
"v_add_f32 v93,v93,v53\n"
MF "v[152:155],v[176:179],a[128:131],v[152:155]\n"
"v_add_f32 v94,v94,v54\n"
"v_add_f32 v95,v95,v55\n"
MF "v[152:155],v[180:183],a[168:171],v[152:155]\n"
CV "v88,v88,v89\n"
CV "v89,v90,v91\n"
MF "v[152:155],v[184:187],a[208:211],v[152:155]\n"
"s_add_u32 s87,s90,14336\n"
CV "v90,v92,v93\n"
CV "v91,v94,v95\n"
MF "v[160:163],v[184:187],a[212:215],v[160:163]\n"
"buffer_store_dwordx4 v[88:91],v9,s[40:43],s87 offen sc1\n"
MF "v[160:163],v[180:183],a[172:175],v[160:163]\n"
MF "v[160:163],v[176:179],a[132:135],v[160:163]\n"
MF "v[168:171],v[176:179],a[136:139],v[168:171]\n"
MF "v[168:171],v[180:183],a[176:179],v[168:171]\n"
"s_waitcnt vmcnt(19) lgkmcnt(0)\n"
MF "v[168:171],v[184:187],a[216:219],v[168:171]\n"
"s_barrier\n"
MF "v[172:175],a[244:247],a[216:219],v[172:175]\n"
"s_nop 0\n"
TR "v[176:177],v0 offset:49152\n"
TR "v[178:179],v0 offset:51200\n"
MF "v[172:175],a[240:243],a[176:179],v[172:175]\n"
"s_nop 0\n"
TR "v[180:181],v0 offset:53248\n"
TR "v[182:183],v0 offset:55296\n"
MF "v[172:175],v[188:191],a[136:139],v[172:175]\n"
"s_nop 0\n"
TR "v[184:185],v0 offset:57344\n"
TR "v[186:187],v0 offset:59392\n"
MF "v[164:167],v[188:191],a[132:135],v[164:167]\n"
"buffer_load_dword v10,v12,s[40:43],s86 offen sc1\n"
MF "v[164:167],a[240:243],a[172:175],v[164:167]\n"
MF "v[164:167],a[244:247],a[212:215],v[164:167]\n"
MF "v[156:159],a[244:247],a[208:211],v[156:159]\n"
MF "v[156:159],a[240:243],a[168:171],v[156:159]\n"
MF "v[156:159],v[188:191],a[128:131],v[156:159]\n"
MF "v[148:151],v[188:191],a[124:127],v[148:151]\n"
MF "v[148:151],a[240:243],a[164:167],v[148:151]\n"
MF "v[148:151],a[244:247],a[204:207],v[148:151]\n"
MF "v[140:143],a[244:247],a[200:203],v[140:143]\n"
MF "v[140:143],a[240:243],a[160:163],v[140:143]\n"
MF "v[140:143],v[188:191],a[120:123],v[140:143]\n"
"s_waitcnt lgkmcnt(0)\n"
MF "v[96:99],v[176:179],a[20:23],v[96:99]\n"
"s_nop 0\n"
TR "v[188:189],v1 offset:49152\n"
TR "v[190:191],v1 offset:51200\n"
MF "v[96:99],v[180:183],a[60:63],v[96:99]\n"
"s_nop 0\n"
TR "a[240:241],v1 offset:53248\n"
TR "a[242:243],v1 offset:55296\n"
MF "v[96:99],v[184:187],a[100:103],v[96:99]\n"
"s_nop 0\n"
TR "a[244:245],v1 offset:57344\n"
TR "a[246:247],v1 offset:59392\n"
MF "v[104:107],v[184:187],a[104:107],v[104:107]\n"
MF "v[104:107],v[180:183],a[64:67],v[104:107]\n"
MF "v[104:107],v[176:179],a[24:27],v[104:107]\n"
"s_waitcnt vmcnt(18)\n"
"v_mov_b32 v236,v228\n"
"v_mov_b32 v237,v229\n"
MF "v[96:99],v[208:211],a[252:255],v[96:99]\n"
MF "v[104:107],v[212:215],a[252:255],v[104:107]\n"
MF "v[112:115],v[216:219],a[252:255],v[112:115]\n"
MF "v[120:123],v[220:223],a[252:255],v[120:123]\n"
MF "v[128:131],v[224:227],a[252:255],v[128:131]\n"
MF "v[100:103],v[208:211],a[248:251],v[100:103]\n"
MF "v[108:111],v[212:215],a[248:251],v[108:111]\n"
MF "v[116:119],v[216:219],a[248:251],v[116:119]\n"
MF "v[124:127],v[220:223],a[248:251],v[124:127]\n"
MF "v[132:135],v[224:227],a[248:251],v[132:135]\n"
MF "v[248:251],v[230:233],a[250:253],v[248:251]\n"
MF "v[244:247],v[234:237],a[250:253],v[244:247]\n"
MF "v[112:115],v[176:179],a[28:31],v[112:115]\n"
MF "v[112:115],v[180:183],a[68:71],v[112:115]\n"
MF "v[112:115],v[184:187],a[108:111],v[112:115]\n"
MF "v[120:123],v[184:187],a[112:115],v[120:123]\n"
"s_nop 3\n"
CV "v244,v244,v245\n"
CV "v245,v246,v247\n"
MF "v[120:123],v[180:183],a[72:75],v[120:123]\n"
"s_bfe_u32 s87,s22,0x30014\n"
CV "v246,v248,v249\n"
CV "v247,v250,v251\n"
MF "v[120:123],v[176:179],a[32:35],v[120:123]\n"
"s_lshl_b32 s87,s87,7\n"
"v_permlane16_swap_b32 v244,v246\n"
"v_permlane16_swap_b32 v245,v247\n"
MF "v[128:131],v[176:179],a[36:39],v[128:131]\n"
"buffer_store_dwordx4 v[244:247],v8,s[44:47],s0 offen sc1\n"
MF "v[128:131],v[180:183],a[76:79],v[128:131]\n"
"s_and_b32 s60,s56,1\n"
MF "v[128:131],v[184:187],a[116:119],v[128:131]\n"
"s_waitcnt lgkmcnt(0)\n"
MF "v[132:135],a[244:247],a[116:119],v[132:135]\n"
"s_mul_i32 s60,s60,204800\n"
TR "v[176:177],v2 offset:49152\n"
TR "v[178:179],v2 offset:51200\n"
MF "v[132:135],a[240:243],a[76:79],v[132:135]\n"
"s_add_u32 s87,s87,s60\n"
TR "v[180:181],v2 offset:53248\n"
TR "v[182:183],v2 offset:55296\n"
MF "v[132:135],v[188:191],a[36:39],v[132:135]\n"
"s_mov_b32 s71,204800\n"
TR "v[184:185],v2 offset:57344\n"
TR "v[186:187],v2 offset:59392\n"
MF "v[124:127],v[188:191],a[32:35],v[124:127]\n"
"s_mov_b32 s66,204800\n"
MF "v[124:127],a[240:243],a[72:75],v[124:127]\n"
"s_add_u32 s72,s78,2\n"
MF "v[124:127],a[244:247],a[112:115],v[124:127]\n"
"s_and_b32 s72,s72,15\n"
MF "v[116:119],a[244:247],a[108:111],v[116:119]\n"
"s_bfe_u32 s60,s22,0x10014\n"
MF "v[116:119],a[240:243],a[68:71],v[116:119]\n"
"s_lshl_b32 s60,s60,5\n"
MF "v[116:119],v[188:191],a[28:31],v[116:119]\n"
"v_xor_b32 v242,s60,v8\n"
MF "v[108:111],v[188:191],a[24:27],v[108:111]\n"
"s_add_u32 s16,s78,6\n"
MF "v[108:111],a[240:243],a[64:67],v[108:111]\n"
"s_and_b32 s16,s16,15\n"
MF "v[108:111],a[244:247],a[104:107],v[108:111]\n"
"s_add_u32 s17,s78,4\n"
MF "v[100:103],a[244:247],a[100:103],v[100:103]\n"
"s_and_b32 s17,s17,15\n"
MF "v[100:103],a[240:243],a[60:63],v[100:103]\n"
MF "v[100:103],v[188:191],a[20:23],v[100:103]\n"
"s_waitcnt lgkmcnt(0)\n"
MF "v[136:139],v[176:179],a[20:23],v[136:139]\n"
"s_nop 0\n"
TR "v[188:189],v3 offset:49152\n"
TR "v[190:191],v3 offset:51200\n"
MF "v[136:139],v[180:183],a[60:63],v[136:139]\n"
"s_nop 0\n"
TR "a[240:241],v3 offset:53248\n"
TR "a[242:243],v3 offset:55296\n"
MF "v[136:139],v[184:187],a[100:103],v[136:139]\n"
"s_nop 0\n"
TR "a[244:245],v3 offset:57344\n"
TR "a[246:247],v3 offset:59392\n"
MF "v[144:147],v[184:187],a[104:107],v[144:147]\n"
MF "v[144:147],v[180:183],a[64:67],v[144:147]\n"
MF "v[144:147],v[176:179],a[24:27],v[144:147]\n"
MF "v[152:155],v[176:179],a[28:31],v[152:155]\n"
MF "v[152:155],v[180:183],a[68:71],v[152:155]\n"
MF "v[152:155],v[184:187],a[108:111],v[152:155]\n"
MF "v[160:163],v[184:187],a[112:115],v[160:163]\n"
MF "v[160:163],v[180:183],a[72:75],v[160:163]\n"
MF "v[160:163],v[176:179],a[32:35],v[160:163]\n"
MF "v[168:171],v[176:179],a[36:39],v[168:171]\n"
MF "v[168:171],v[180:183],a[76:79],v[168:171]\n"
"s_waitcnt vmcnt(1) lgkmcnt(0)\n"
MF "v[168:171],v[184:187],a[116:119],v[168:171]\n"
"s_barrier\n"
MF "v[172:175],a[244:247],a[116:119],v[172:175]\n"
"v_sub_u32 v10,v10,v11\n"
MF "v[172:175],a[240:243],a[76:79],v[172:175]\n"
"s_nop 0\n"
TR "v[176:177],v4\n"
TR "v[178:179],v4 offset:2048\n"
MF "v[172:175],v[188:191],a[36:39],v[172:175]\n"
"s_nop 0\n"
TR "v[180:181],v4 offset:4096\n"
TR "v[182:183],v4 offset:6144\n"
MF "v[164:167],v[188:191],a[32:35],v[164:167]\n"
"s_nop 0\n"
TR "v[184:185],v4 offset:8192\n"
TR "v[186:187],v4 offset:10240\n"
MF "v[164:167],a[240:243],a[72:75],v[164:167]\n"
"v_readlane_b32 s16,v10,s16\n"
MF "v[164:167],a[244:247],a[112:115],v[164:167]\n"
"s_cmp_lt_u32 s16,9\n"
"s_cselect_b32 s16,0xf0000000,0\n"
MF "v[156:159],a[244:247],a[108:111],v[156:159]\n"
"v_readlane_b32 s17,v10,s17\n"
MF "v[156:159],a[240:243],a[68:71],v[156:159]\n"
"s_cmp_lt_u32 s17,10\n"
"s_cselect_b32 s17,0xf0000000,0\n"
MF "v[156:159],v[188:191],a[28:31],v[156:159]\n"
"s_bfe_u32 s62,s22,0x30014\n"
MF "v[148:151],v[188:191],a[24:27],v[148:151]\n"
"s_add_u32 s62,s62,3\n"
"s_and_b32 s62,s62,7\n"
"s_mul_i32 s62,s62,204800\n"
MF "v[148:151],a[240:243],a[64:67],v[148:151]\n"
"s_add_u32 s62,s62,s73\n"
"s_add_u32 s62,s62,133120\n"
"s_or_b32 s62,s62,s16\n"
MF "v[148:151],a[244:247],a[104:107],v[148:151]\n"
LD "v[16:19],v9,s[40:43],s62 offen sc1\n"
MF "v[140:143],a[244:247],a[100:103],v[140:143]\n"
LD "v[24:27],v9,s[40:43],s62 offen offset:1024 sc1\n"
MF "v[140:143],a[240:243],a[60:63],v[140:143]\n"
MF "v[140:143],v[188:191],a[20:23],v[140:143]\n"
"s_waitcnt lgkmcnt(0)\n"
LD "v[32:35],v9,s[40:43],s62 offen offset:2048 sc1\n"
MF "v[96:99],v[176:179],a[140:143],v[96:99]\n"
"s_nop 0\n"
TR "v[188:189],v5\n"
TR "v[190:191],v5 offset:2048\n"
MF "v[96:99],v[180:183],a[180:183],v[96:99]\n"
"s_nop 0\n"
TR "a[240:241],v5 offset:4096\n"
TR "a[242:243],v5 offset:6144\n"
MF "v[96:99],v[184:187],a[220:223],v[96:99]\n"
"s_nop 0\n"
TR "a[244:245],v5 offset:8192\n"
TR "a[246:247],v5 offset:10240\n"
MF "v[104:107],v[184:187],a[224:227],v[104:107]\n"
LD "v[40:43],v9,s[40:43],s62 offen offset:3072 sc1\n"
MF "v[104:107],v[180:183],a[184:187],v[104:107]\n"
MF "v[104:107],v[176:179],a[144:147],v[104:107]\n"
"s_add_u32 s62,s62,4096\n"
MF "v[112:115],v[176:179],a[148:151],v[112:115]\n"
LD "v[48:51],v9,s[40:43],s62 offen sc1\n"
MF "v[112:115],v[180:183],a[188:191],v[112:115]\n"
"s_bfe_u32 s62,s22,0x30014\n"
"ds_write_addtid_b32 v96 offset:16384\n"
"ds_write_addtid_b32 v97 offset:16640\n"
MF "v[112:115],v[184:187],a[228:231],v[112:115]\n"
"s_add_u32 s62,s62,2\n"
"s_and_b32 s62,s62,7\n"
"s_mul_i32 s62,s62,204800\n"
MF "v[120:123],v[184:187],a[232:235],v[120:123]\n"
"s_add_u32 s62,s62,s73\n"
"s_add_u32 s62,s62,153600\n"
"s_or_b32 s62,s62,s17\n"
MF "v[120:123],v[180:183],a[192:195],v[120:123]\n"
LD "v[20:23],v9,s[40:43],s62 offen sc1\n"
MF "v[120:123],v[176:179],a[152:155],v[120:123]\n"
"s_nop 0\n"
"ds_write_addtid_b32 v98 offset:16896\n"
"ds_write_addtid_b32 v99 offset:17152\n"
MF "v[128:131],v[176:179],a[156:159],v[128:131]\n"
LD "v[28:31],v9,s[40:43],s62 offen offset:1024 sc1\n"
MF "v[128:131],v[180:183],a[196:199],v[128:131]\n"
"s_nop 0\n"
"ds_write_addtid_b32 v104 offset:18432\n"
"ds_write_addtid_b32 v105 offset:18688\n"
MF "v[128:131],v[184:187],a[236:239],v[128:131]\n"
"s_waitcnt lgkmcnt(6)\n"
LD "v[36:39],v9,s[40:43],s62 offen offset:2048 sc1\n"
MF "v[100:103],v[188:191],a[140:143],v[100:103]\n"
"s_nop 0\n"
TR "v[176:177],v6\n"
TR "v[178:179],v6 offset:2048\n"
MF "v[100:103],a[240:243],a[180:183],v[100:103]\n"
"s_nop 0\n"
TR "v[180:181],v6 offset:4096\n"
TR "v[182:183],v6 offset:6144\n"
MF "v[100:103],a[244:247],a[220:223],v[100:103]\n"
"s_nop 0\n"
TR "v[184:185],v6 offset:8192\n"
TR "v[186:187],v6 offset:10240\n"
MF "v[108:111],a[244:247],a[224:227],v[108:111]\n"
LD "v[44:47],v9,s[40:43],s62 offen offset:3072 sc1\n"
MF "v[108:111],a[240:243],a[184:187],v[108:111]\n"
"s_nop 0\n"
"ds_write_addtid_b32 v106 offset:18944\n"
"ds_write_addtid_b32 v107 offset:19200\n"
MF "v[108:111],v[188:191],a[144:147],v[108:111]\n"
"s_add_u32 s62,s62,4096\n"
"ds_write_addtid_b32 v112 offset:20480\n"
"ds_write_addtid_b32 v113 offset:20736\n"
MF "v[116:119],v[188:191],a[148:151],v[116:119]\n"
LD "v[52:55],v9,s[40:43],s62 offen sc1\n"
MF "v[116:119],a[240:243],a[188:191],v[116:119]\n"
"s_nop 0\n"
"ds_write_addtid_b32 v114 offset:20992\n"
"ds_write_addtid_b32 v115 offset:21248\n"
MF "v[116:119],a[244:247],a[228:231],v[116:119]\n"
"s_nop 0\n"
"ds_write_addtid_b32 v120 offset:22528\n"
"ds_write_addtid_b32 v121 offset:22784\n"
MF "v[124:127],a[244:247],a[232:235],v[124:127]\n"
"s_nop 0\n"
"ds_write_addtid_b32 v122 offset:23040\n"
"ds_write_addtid_b32 v123 offset:23296\n"
MF "v[124:127],a[240:243],a[192:195],v[124:127]\n"
"s_nop 0\n"
"ds_write_addtid_b32 v128 offset:24576\n"
"ds_write_addtid_b32 v129 offset:24832\n"
MF "v[124:127],v[188:191],a[152:155],v[124:127]\n"
"buffer_load_dword v10,v12,s[40:43],s86 offen sc1\n"
MF "v[132:135],v[188:191],a[156:159],v[132:135]\n"
"s_nop 0\n"
"ds_write_addtid_b32 v130 offset:25088\n"
"ds_write_addtid_b32 v131 offset:25344\n"
MF "v[132:135],a[240:243],a[196:199],v[132:135]\n"
"s_nop 0\n"
"ds_write_addtid_b32 v100 offset:17408\n"
"ds_write_addtid_b32 v101 offset:17664\n"
MF "v[132:135],a[244:247],a[236:239],v[132:135]\n"
"s_waitcnt lgkmcnt(15)\n"
"ds_write_addtid_b32 v102 offset:17920\n"
"ds_write_addtid_b32 v103 offset:18176\n"
MF "v[136:139],v[176:179],a[140:143],v[136:139]\n"
"s_nop 0\n"
TR "v[188:189],v7\n"
TR "v[190:191],v7 offset:2048\n"
MF "v[136:139],v[180:183],a[180:183],v[136:139]\n"
"s_nop 0\n"
TR "a[240:241],v7 offset:4096\n"
TR "a[242:243],v7 offset:6144\n"
MF "v[136:139],v[184:187],a[220:223],v[136:139]\n"
"s_nop 0\n"
TR "a[244:245],v7 offset:8192\n"
TR "a[246:247],v7 offset:10240\n"
MF "v[144:147],v[184:187],a[224:227],v[144:147]\n"
"s_nop 0\n"
"ds_write_addtid_b32 v108 offset:19456\n"
"ds_write_addtid_b32 v109 offset:19712\n"
MF "v[144:147],v[180:183],a[184:187],v[144:147]\n"
"s_nop 0\n"
"ds_write_addtid_b32 v110 offset:19968\n"
"ds_write_addtid_b32 v111 offset:20224\n"
MF "v[144:147],v[176:179],a[144:147],v[144:147]\n"
"s_nop 0\n"
"ds_write_addtid_b32 v116 offset:21504\n"
"ds_write_addtid_b32 v117 offset:21760\n"
MF "v[152:155],v[176:179],a[148:151],v[152:155]\n"
"s_nop 0\n"
"ds_write_addtid_b32 v118 offset:22016\n"
"ds_write_addtid_b32 v119 offset:22272\n"
MF "v[152:155],v[180:183],a[188:191],v[152:155]\n"
"ds_write_addtid_b32 v124 offset:23552\n"
MF "v[152:155],v[184:187],a[228:231],v[152:155]\n"
"ds_write_addtid_b32 v125 offset:23808\n"
MF "v[160:163],v[184:187],a[232:235],v[160:163]\n"
"ds_write_addtid_b32 v126 offset:24064\n"
MF "v[160:163],v[180:183],a[192:195],v[160:163]\n"
"ds_write_addtid_b32 v127 offset:24320\n"
MF "v[160:163],v[176:179],a[152:155],v[160:163]\n"
"ds_write_addtid_b32 v132 offset:25600\n"
MF "v[168:171],v[176:179],a[156:159],v[168:171]\n"
"ds_write_addtid_b32 v133 offset:25856\n"
MF "v[168:171],v[180:183],a[196:199],v[168:171]\n"
"ds_write_addtid_b32 v134 offset:26112\n"
MF "v[168:171],v[184:187],a[236:239],v[168:171]\n"
"s_waitcnt lgkmcnt(15)\n"
"ds_write_addtid_b32 v135 offset:26368\n"
MF "v[140:143],v[188:191],a[140:143],v[140:143]\n"
MF "v[140:143],a[240:243],a[180:183],v[140:143]\n"
MF "v[140:143],a[244:247],a[220:223],v[140:143]\n"
MF "v[148:151],a[244:247],a[224:227],v[148:151]\n"
"s_waitcnt lgkmcnt(0)\n"
MF "v[148:151],a[240:243],a[184:187],v[148:151]\n"
"s_barrier\n"
MF "v[148:151],v[188:191],a[144:147],v[148:151]\n"
"ds_read2st64_b32 v[56:57],v13 offset0:0 offset1:1\n"
MF "v[156:159],v[188:191],a[148:151],v[156:159]\n"
"ds_read2st64_b32 v[58:59],v13 offset0:2 offset1:3\n"
MF "v[156:159],a[240:243],a[188:191],v[156:159]\n"
"ds_read2st64_b32 v[60:61],v13 offset0:4 offset1:5\n"
MF "v[156:159],a[244:247],a[228:231],v[156:159]\n"
"ds_read2st64_b32 v[62:63],v13 offset0:6 offset1:7\n"
MF "v[164:167],a[244:247],a[232:235],v[164:167]\n"
"ds_read2st64_b32 v[64:65],v13 offset0:8 offset1:9\n"
MF "v[164:167],a[240:243],a[192:195],v[164:167]\n"
"ds_read2st64_b32 v[66:67],v13 offset0:10 offset1:11\n"
MF "v[164:167],v[188:191],a[152:155],v[164:167]\n"
"ds_read2st64_b32 v[68:69],v13 offset0:12 offset1:13\n"
MF "v[172:175],v[188:191],a[156:159],v[172:175]\n"
"ds_read2st64_b32 v[70:71],v13 offset0:14 offset1:15\n"
MF "v[172:175],a[240:243],a[196:199],v[172:175]\n"
"ds_read2st64_b32 v[72:73],v13 offset0:16 offset1:17\n"
MF "v[172:175],a[244:247],a[236:239],v[172:175]\n"
"ds_read2st64_b32 v[74:75],v13 offset0:18 offset1:19\n"
"ds_read2st64_b32 v[76:77],v13 offset0:20 offset1:21\n"
"ds_read2st64_b32 v[78:79],v13 offset0:22 offset1:23\n"
"s_nop 0\n"
"ds_read2st64_b32 v[80:81],v13 offset0:24 offset1:25\n"
"ds_read2st64_b32 v[82:83],v13 offset0:26 offset1:27\n"
"ds_read2st64_b32 v[84:85],v13 offset0:28 offset1:29\n"
"ds_read2st64_b32 v[86:87],v13 offset0:30 offset1:31\n"
"ds_read2st64_b32 v[88:89],v13 offset0:32 offset1:33\n"
"ds_read2st64_b32 v[90:91],v13 offset0:34 offset1:35\n"
".Lpg28h:\n"
"s_cbranch_execz .Lpg31h\n"
"s_nop 0\n"
"ds_read2st64_b32 v[92:93],v13 offset0:36 offset1:37\n"
"ds_read2st64_b32 v[94:95],v13 offset0:38 offset1:39\n"
"s_waitcnt vmcnt(0)\n"
"v_sub_u32 v10,v10,v11\n"
"s_cmp_eq_u32 s16,0\n"
"s_cbranch_scc1 .Lhave03\n"
"s_add_u32 s59,s78,6\n"
"s_and_b32 s59,s59,15\n"
"s_nop 3\n"
".Lnf3:\n"
"v_readlane_b32 s58,v10,s59\n"
"s_cmp_lt_u32 s58,9\n"
"s_cbranch_scc1 .Lsp3\n"
".Lnb3:\n"
"s_bfe_u32 s62,s22,0x30014\n"
"s_add_u32 s62,s62,3\n"
"s_and_b32 s62,s62,7\n"
"s_mul_i32 s62,s62,204800\n"
"s_add_u32 s62,s62,s73\n"
"s_add_u32 s62,s62,133120\n"
LD "v[16:19],v9,s[40:43],s62 offen sc1\n"
LD "v[24:27],v9,s[40:43],s62 offen offset:1024 sc1\n"
LD "v[32:35],v9,s[40:43],s62 offen offset:2048 sc1\n"
LD "v[40:43],v9,s[40:43],s62 offen offset:3072 sc1\n"
"s_add_u32 s62,s62,4096\n"
LD "v[48:51],v9,s[40:43],s62 offen sc1\n"
".Lhave03:\n"
"s_cmp_eq_u32 s17,0\n"
"s_cbranch_scc1 .Lhave02\n"
"s_add_u32 s59,s78,4\n"
"s_and_b32 s59,s59,15\n"
"s_nop 3\n"
".Lnf4:\n"
"v_readlane_b32 s58,v10,s59\n"
"s_cmp_lt_u32 s58,10\n"
"s_cbranch_scc1 .Lsp4\n"
".Lnb4:\n"
"s_bfe_u32 s62,s22,0x30014\n"
"s_add_u32 s62,s62,2\n"
"s_and_b32 s62,s62,7\n"
"s_mul_i32 s62,s62,204800\n"
"s_add_u32 s62,s62,s73\n"
"s_add_u32 s62,s62,153600\n"
LD "v[20:23],v9,s[40:43],s62 offen sc1\n"
LD "v[28:31],v9,s[40:43],s62 offen offset:1024 sc1\n"
LD "v[36:39],v9,s[40:43],s62 offen offset:2048 sc1\n"
LD "v[44:47],v9,s[40:43],s62 offen offset:3072 sc1\n"
"s_add_u32 s62,s62,4096\n"
LD "v[52:55],v9,s[40:43],s62 offen sc1\n"
".Lhave02:\n"
".Lnf5:\n"
"v_readlane_b32 s58,v10,s72\n"
"s_cmp_lt_u32 s58,11\n"
"s_cbranch_scc1 .Lsp5\n"
".Lnb5:\n"
"s_bfe_u32 s62,s22,0x30014\n"
"s_add_u32 s62,s62,1\n"
"s_and_b32 s62,s62,7\n"
"s_mul_i32 s62,s62,204800\n"
"s_add_u32 s62,s62,s73\n"
"s_add_u32 s62,s62,174080\n"
LD "v[208:211],v9,s[40:43],s62 offen sc1\n"
LD "v[212:215],v9,s[40:43],s62 offen offset:1024 sc1\n"
LD "v[216:219],v9,s[40:43],s62 offen offset:2048 sc1\n"
LD "v[220:223],v9,s[40:43],s62 offen offset:3072 sc1\n"
"s_add_u32 s62,s62,4096\n"
LD "v[224:227],v9,s[40:43],s62 offen sc1\n"
"s_waitcnt lgkmcnt(15)\n"
"v_pk_add_f32 v[136:137],v[136:137],v[56:57]\n"
"v_pk_add_f32 v[138:139],v[138:139],v[58:59]\n"
"v_pk_add_f32 v[140:141],v[140:141],v[60:61]\n"
"v_pk_add_f32 v[142:143],v[142:143],v[62:63]\n"
"v_pk_add_f32 v[144:145],v[144:145],v[64:65]\n"
"s_waitcnt lgkmcnt(14)\n"
"v_pk_add_f32 v[146:147],v[146:147],v[66:67]\n"
"s_waitcnt lgkmcnt(13)\n"
"v_pk_add_f32 v[148:149],v[148:149],v[68:69]\n"
"s_waitcnt lgkmcnt(12)\n"
"v_pk_add_f32 v[150:151],v[150:151],v[70:71]\n"
"s_waitcnt lgkmcnt(11)\n"
"v_pk_add_f32 v[152:153],v[152:153],v[72:73]\n"
"s_waitcnt lgkmcnt(10)\n"
"v_pk_add_f32 v[154:155],v[154:155],v[74:75]\n"
"s_waitcnt lgkmcnt(9)\n"
"v_pk_add_f32 v[156:157],v[156:157],v[76:77]\n"
"s_waitcnt lgkmcnt(8)\n"
"v_pk_add_f32 v[158:159],v[158:159],v[78:79]\n"
"s_waitcnt lgkmcnt(7)\n"
"v_pk_add_f32 v[160:161],v[160:161],v[80:81]\n"
"s_waitcnt lgkmcnt(6)\n"
"v_pk_add_f32 v[162:163],v[162:163],v[82:83]\n"
"s_waitcnt lgkmcnt(5)\n"
"v_pk_add_f32 v[164:165],v[164:165],v[84:85]\n"
"s_waitcnt lgkmcnt(4)\n"
"v_pk_add_f32 v[166:167],v[166:167],v[86:87]\n"
"s_waitcnt lgkmcnt(3)\n"
"v_pk_add_f32 v[168:169],v[168:169],v[88:89]\n"
"s_waitcnt lgkmcnt(2)\n"
"v_pk_add_f32 v[170:171],v[170:171],v[90:91]\n"
"s_waitcnt lgkmcnt(1)\n"
"v_pk_add_f32 v[172:173],v[172:173],v[92:93]\n"
"s_waitcnt lgkmcnt(0)\n"
"v_pk_add_f32 v[174:175],v[174:175],v[94:95]\n"
"s_waitcnt vmcnt(5)\n"
"s_nop 0\n"
MF "v[140:143],v[16:19],a[252:255],v[140:143]\n"
MF "v[148:151],v[24:27],a[252:255],v[148:151]\n"
MF "v[156:159],v[32:35],a[252:255],v[156:159]\n"
MF "v[164:167],v[40:43],a[252:255],v[164:167]\n"
MF "v[172:175],v[48:51],a[252:255],v[172:175]\n"
MF "v[136:139],v[16:19],a[248:251],v[136:139]\n"
MF "v[144:147],v[24:27],a[248:251],v[144:147]\n"
MF "v[152:155],v[32:35],a[248:251],v[152:155]\n"
MF "v[160:163],v[40:43],a[248:251],v[160:163]\n"
MF "v[168:171],v[48:51],a[248:251],v[168:171]\n"
MF "v[136:139],v[20:23],a[252:255],v[136:139]\n"
MF "v[144:147],v[28:31],a[252:255],v[144:147]\n"
MF "v[152:155],v[36:39],a[252:255],v[152:155]\n"
MF "v[160:163],v[44:47],a[252:255],v[160:163]\n"
MF "v[168:171],v[52:55],a[252:255],v[168:171]\n"
MF "v[140:143],v[20:23],a[248:251],v[140:143]\n"
MF "v[148:151],v[28:31],a[248:251],v[148:151]\n"
MF "v[156:159],v[36:39],a[248:251],v[156:159]\n"
MF "v[164:167],v[44:47],a[248:251],v[164:167]\n"
MF "v[172:175],v[52:55],a[248:251],v[172:175]\n"
"s_waitcnt vmcnt(0)\n"
MF "v[140:143],v[208:211],a[252:255],v[140:143]\n"
MF "v[136:139],v[208:211],a[248:251],v[136:139]\n"
MF "v[148:151],v[212:215],a[252:255],v[148:151]\n"
MF "v[144:147],v[212:215],a[248:251],v[144:147]\n"
MF "v[156:159],v[216:219],a[252:255],v[156:159]\n"
"s_nop 4\n"
CV "v136,v136,v137\n"
CV "v137,v138,v139\n"
MF "v[152:155],v[216:219],a[248:251],v[152:155]\n"
CV "v138,v140,v141\n"
CV "v139,v142,v143\n"
MF "v[164:167],v[220:223],a[252:255],v[164:167]\n"
CV "v144,v144,v145\n"
CV "v145,v146,v147\n"
MF "v[160:163],v[220:223],a[248:251],v[160:163]\n"
CV "v146,v148,v149\n"
CV "v147,v150,v151\n"
"v_permlane16_swap_b32 v136,v138\n"
"v_permlane16_swap_b32 v137,v139\n"
"buffer_store_dwordx4 v[136:139],v242,s[44:47],s87 offen offset:64 sc1\n"
"s_add_u32 s87,s87,40960\n"
MF "v[172:175],v[224:227],a[252:255],v[172:175]\n"
CV "v152,v152,v153\n"
CV "v153,v154,v155\n"
MF "v[168:171],v[224:227],a[248:251],v[168:171]\n"
CV "v154,v156,v157\n"
CV "v155,v158,v159\n"
"v_permlane16_swap_b32 v144,v146\n"
"v_permlane16_swap_b32 v145,v147\n"
"buffer_store_dwordx4 v[144:147],v242,s[44:47],s87 offen offset:64 sc1\n"
"s_add_u32 s87,s87,40960\n"
CV "v160,v160,v161\n"
CV "v161,v162,v163\n"
CV "v162,v164,v165\n"
CV "v163,v166,v167\n"
"v_permlane16_swap_b32 v152,v154\n"
"v_permlane16_swap_b32 v153,v155\n"
"buffer_store_dwordx4 v[152:155],v242,s[44:47],s87 offen offset:64 sc1\n"
"s_add_u32 s87,s87,40960\n"
CV "v168,v168,v169\n"
CV "v169,v170,v171\n"
CV "v170,v172,v173\n"
CV "v171,v174,v175\n"
"v_permlane16_swap_b32 v160,v162\n"
"v_permlane16_swap_b32 v161,v163\n"
"buffer_store_dwordx4 v[160:163],v242,s[44:47],s87 offen offset:64 sc1\n"
"s_add_u32 s87,s87,40960\n"
"v_permlane16_swap_b32 v168,v170\n"
"v_permlane16_swap_b32 v169,v171\n"
"buffer_store_dwordx4 v[168:171],v242,s[44:47],s87 offen offset:64 sc1\n"
"s_add_u32 s87,s87,40960\n"
"s_endpgm\n"
".Lpgrole1_h:\n"
"s_cbranch_execz .Lpg32h\n"
".Lrole1:\n"
MF "v[16:19],a[120:123],v[176:179],v[252:255]\n"
"s_mov_b32 s65,128\n"
TR "v[188:189],v1 offset:24576\n"
AR "v220,a44\n"
MF "v[16:19],a[160:163],v[180:183],v[16:19]\n"
"s_mov_b32 s9,0x3f80\n"
TR "v[190:191],v1 offset:26624\n"
AR "v221,a45\n"
MF "v[16:19],a[200:203],v[184:187],v[16:19]\n"
AR "v222,a46\n"
AR "v223,a47\n"
MF "v[24:27],a[204:207],v[184:187],v[252:255]\n"
"s_bfe_u32 s58,s22,0x30014\n"
TR "a[240:241],v1 offset:28672\n"
AR "v224,a64\n"
MF "v[24:27],a[164:167],v[180:183],v[24:27]\n"
"s_add_u32 s61,s58,1\n"
TR "a[242:243],v1 offset:30720\n"
AR "v225,a65\n"
MF "v[24:27],a[124:127],v[176:179],v[24:27]\n"
"s_and_b32 s61,s61,7\n"
AR "v226,a66\n"
AR "v227,a67\n"
MF "v[32:35],a[128:131],v[176:179],v[252:255]\n"
"s_mov_b32 s64,9\n"
TR "a[244:245],v1 offset:32768\n"
"ds_read_b128 a[64:67],v236 offset:49152\n"
MF "v[32:35],a[168:171],v[180:183],v[32:35]\n"
"s_xor_b32 s67,s78,8\n"
TR "a[246:247],v1 offset:34816\n"
"v_fma_f32 v220,v220,v209,v215\n"
MF "v[32:35],a[208:211],v[184:187],v[32:35]\n"
"s_mov_b32 s71,204800\n"
"v_fma_f32 v221,v221,v209,v215\n"
"v_fma_f32 v222,v222,v209,v215\n"
MF "v[40:43],a[212:215],v[184:187],v[252:255]\n"
"s_mov_b32 s66,204800\n"
"v_fma_f32 v223,v223,v209,v215\n"
"v_fma_f32 v224,v224,v209,v215\n"
MF "v[40:43],a[172:175],v[180:183],v[40:43]\n"
"s_and_b32 s73,s56,1\n"
"v_fma_f32 v225,v225,v209,v215\n"
"v_fma_f32 v226,v226,v209,v215\n"
MF "v[40:43],a[132:135],v[176:179],v[40:43]\n"
"s_mul_i32 s73,s73,5120\n"
"v_fma_f32 v227,v227,v209,v215\n"
CV "v220,v220,v221\n"
MF "v[48:51],a[136:139],v[176:179],v[252:255]\n"
"s_add_u32 s73,s73,s93\n"
CV "v221,v222,v223\n"
CV "v222,v224,v225\n"
MF "v[48:51],a[176:179],v[180:183],v[48:51]\n"
"s_xor_b32 s58,s58,4\n"
CV "v223,v226,v227\n"
AW "a44,v220\n"
MF "v[48:51],a[216:219],v[184:187],v[48:51]\n"
"s_waitcnt lgkmcnt(0)\n"
AW "a45,v221\n"
AW "a46,v222\n"
MF "v[52:55],a[216:219],a[244:247],v[252:255]\n"
"s_mul_i32 s70,s58,204800\n"
TR "v[176:177],v2 offset:24576\n"
AW "a47,v223\n"
MF "v[52:55],a[176:179],a[240:243],v[52:55]\n"
"s_add_u32 s70,s70,s73\n"
TR "v[178:179],v2 offset:26624\n"
AR "v228,a84\n"
MF "v[52:55],a[136:139],v[188:191],v[52:55]\n"
"s_add_u32 s70,s70,0xc800\n"
AR "v229,a85\n"
AR "v230,a86\n"
MF "v[44:47],a[132:135],v[188:191],v[252:255]\n"
"s_mov_b32 s85,1\n"
TR "v[180:181],v2 offset:28672\n"
AR "v231,a87\n"
MF "v[44:47],a[172:175],a[240:243],v[44:47]\n"
"s_mov_b32 s83,0xf0000000\n"
TR "v[182:183],v2 offset:30720\n"
AR "v232,a104\n"
MF "v[44:47],a[212:215],a[244:247],v[44:47]\n"
"s_mov_b32 s82,0xf0000000\n"
AR "v233,a105\n"
AR "v234,a106\n"
MF "v[36:39],a[208:211],a[244:247],v[252:255]\n"
"s_add_u32 s58,s79,73728\n"
TR "v[184:185],v2 offset:32768\n"
AR "v235,a107\n"
MF "v[36:39],a[168:171],a[240:243],v[36:39]\n"
"s_mov_b32 s90,s91\n"
TR "v[186:187],v2 offset:34816\n"
"ds_read_b128 a[104:107],v237 offset:4096\n"
MF "v[36:39],a[128:131],v[188:191],v[36:39]\n"
"s_add_u32 s91,s91,20480\n"
"v_fma_f32 v228,v228,v209,v215\n"
"v_fma_f32 v229,v229,v209,v215\n"
MF "v[28:31],a[124:127],v[188:191],v[252:255]\n"
"s_bfe_u32 s74,s23,0x70000\n"
"v_fma_f32 v230,v230,v209,v215\n"
"v_fma_f32 v231,v231,v209,v215\n"
MF "v[28:31],a[164:167],a[240:243],v[28:31]\n"
"s_lshl_b32 s74,s74,1\n"
"v_fma_f32 v232,v232,v209,v215\n"
"v_fma_f32 v233,v233,v209,v215\n"
MF "v[28:31],a[204:207],a[244:247],v[28:31]\n"
"s_add_u32 s74,s74,s56\n"
"v_fma_f32 v234,v234,v209,v215\n"
"v_fma_f32 v235,v235,v209,v215\n"
MF "v[20:23],a[200:203],a[244:247],v[252:255]\n"
"s_sub_u32 s74,s74,2\n"
CV "v228,v228,v229\n"
CV "v229,v230,v231\n"
MF "v[20:23],a[160:163],a[240:243],v[20:23]\n"
"s_mul_i32 s74,s74,30720\n"
CV "v230,v232,v233\n"
CV "v231,v234,v235\n"
MF "v[20:23],a[120:123],v[188:191],v[20:23]\n"
"s_waitcnt lgkmcnt(1)\n"
AW "a84,v228\n"
AW "a85,v229\n"
MF "v[56:59],a[120:123],v[176:179],v[252:255]\n"
"s_bfe_u32 s68,s23,0x30003\n"
TR "v[188:189],v3 offset:24576\n"
"v_add_u32 v198,59392,v192\n"
MF "v[56:59],a[160:163],v[180:183],v[56:59]\n"
"s_add_u32 s68,s68,7\n"
TR "v[190:191],v3 offset:26624\n"
"v_add_u32 v199,68608,v192\n"
MF "v[56:59],a[200:203],v[184:187],v[56:59]\n"
"s_lshl_b32 s68,s68,7\n"
"v_add_u32 v200,81920,v192\n"
"v_add_u32 v201,91136,v192\n"
MF "v[64:67],a[204:207],v[184:187],v[252:255]\n"
"s_and_b32 s68,s68,768\n"
TR "a[240:241],v3 offset:28672\n"
"v_add_u32 v202,100352,v192\n"
MF "v[64:67],a[164:167],v[180:183],v[64:67]\n"
"s_bfe_u32 s4,s22,0x30014\n"
TR "a[242:243],v3 offset:30720\n"
"v_add_u32 v203,109568,v192\n"
MF "v[64:67],a[124:127],v[176:179],v[64:67]\n"
"s_add_u32 s4,s4,32\n"
AW "a86,v230\n"
AW "a87,v231\n"
MF "v[72:75],a[128:131],v[176:179],v[252:255]\n"
"s_sub_u32 s4,s4,s92\n"
TR "a[244:245],v3 offset:32768\n"
AR "v220,a8\n"
MF "v[72:75],a[168:171],v[180:183],v[72:75]\n"
"s_cmp_eq_u32 s56,2\n"
"s_cselect_b32 s4,s4,64\n"
TR "a[246:247],v3 offset:34816\n"
MF "v[72:75],a[208:211],v[184:187],v[72:75]\n"
"s_mul_i32 s95,s92,3277\n"
AR "v221,a9\n"
AR "v222,a10\n"
MF "v[80:83],a[212:215],v[184:187],v[252:255]\n"
"s_lshr_b32 s95,s95,16\n"
AR "v223,a11\n"
AR "v224,a28\n"
MF "v[80:83],a[172:175],v[180:183],v[80:83]\n"
"s_mul_i32 s59,s95,20\n"
AR "v225,a29\n"
AR "v226,a30\n"
MF "v[80:83],a[132:135],v[176:179],v[80:83]\n"
"s_sub_u32 s59,s92,s59\n"
AR "v227,a31\n"
"v_fma_f32 v220,v220,v210,v216\n"
MF "v[88:91],a[136:139],v[176:179],v[252:255]\n"
"s_cmp_ge_u32 s59,10\n"
"s_cselect_b32 s96,4294557760,0\n"
"ds_read_b128 a[28:31],v236 offset:32768\n"
MF "v[88:91],a[176:179],v[180:183],v[88:91]\n"
"s_waitcnt vmcnt(0) lgkmcnt(0)\n"
"v_fma_f32 v221,v221,v210,v216\n"
"v_fma_f32 v222,v222,v210,v216\n"
MF "v[88:91],a[216:219],v[184:187],v[88:91]\n"
"s_barrier\n"
MF "v[92:95],a[216:219],a[244:247],v[252:255]\n"
"s_add_u32 s89,s89,s65\n"
TR "v[176:177],v0\n"
"v_fma_f32 v223,v223,v210,v216\n"
MF "v[92:95],a[176:179],a[240:243],v[92:95]\n"
"s_add_u32 m0,s79,24576\n"
TR "v[178:179],v0 offset:2048\n"
"v_fma_f32 v224,v224,v210,v216\n"
MF "v[92:95],a[136:139],v[188:191],v[92:95]\n"
LD "v192,s[48:51],s89 offen lds\n"
MF "v[84:87],a[132:135],v[188:191],v[252:255]\n"
"s_add_u32 s96,s96,1024\n"
TR "v[180:181],v0 offset:4096\n"
"v_fma_f32 v225,v225,v210,v216\n"
MF "v[84:87],a[172:175],a[240:243],v[84:87]\n"
"s_mul_i32 s59,s59,40960\n"
TR "v[182:183],v0 offset:6144\n"
"v_fma_f32 v226,v226,v210,v216\n"
MF "v[84:87],a[212:215],a[244:247],v[84:87]\n"
LD "v193,s[48:51],s89 offen offset:1024 lds\n"
MF "v[76:79],a[208:211],a[244:247],v[252:255]\n"
"s_add_u32 s96,s96,s59\n"
TR "v[184:185],v0 offset:8192\n"
"v_fma_f32 v227,v227,v210,v216\n"
MF "v[76:79],a[168:171],a[240:243],v[76:79]\n"
"s_lshl_b32 s59,s95,7\n"
TR "v[186:187],v0 offset:10240\n"
CV "v220,v220,v221\n"
MF "v[76:79],a[128:131],v[188:191],v[76:79]\n"
"s_add_u32 s96,s96,s59\n"
TR "v[96:97],v0 offset:49152\n"
CV "v221,v222,v223\n"
MF "v[68:71],a[124:127],v[188:191],v[252:255]\n"
"s_lshl_b32 s2,s92,10\n"
TR "v[98:99],v0 offset:51200\n"
CV "v222,v224,v225\n"
MF "v[68:71],a[164:167],a[240:243],v[68:71]\n"
LD "v194,s[48:51],s89 offen offset:2048 lds\n"
MF "v[68:71],a[204:207],a[244:247],v[68:71]\n"
"s_add_u32 s2,s2,s93\n"
TR "v[100:101],v0 offset:53248\n"
CV "v223,v226,v227\n"
MF "v[60:63],a[200:203],a[244:247],v[252:255]\n"
"s_mov_b32 s0,s96\n"
TR "v[102:103],v0 offset:55296\n"
AW "a8,v220\n"
MF "v[60:63],a[160:163],a[240:243],v[60:63]\n"
"s_add_u32 s92,s92,s4\n"
TR "v[104:105],v0 offset:57344\n"
AW "a9,v221\n"
MF "v[60:63],a[120:123],v[188:191],v[60:63]\n"
"s_waitcnt lgkmcnt(5)\n"
LD "v195,s[48:51],s89 offen offset:3072 lds\n"
MF "v[16:19],a[140:143],v[176:179],v[16:19]\n"
"s_mul_i32 s95,s92,3277\n"
TR "v[188:189],v1\n"
AW "a10,v222\n"
MF "v[16:19],a[180:183],v[180:183],v[16:19]\n"
"s_lshr_b32 s95,s95,16\n"
TR "v[190:191],v1 offset:2048\n"
AW "a11,v223\n"
MF "v[16:19],a[220:223],v[184:187],v[16:19]\n"
"s_mul_i32 s59,s95,20\n"
TR "v[106:107],v0 offset:59392\n"
AR "v228,a48\n"
MF "v[24:27],a[224:227],v[184:187],v[24:27]\n"
"s_sub_u32 s59,s92,s59\n"
TR "a[240:241],v1 offset:4096\n"
AR "v229,a49\n"
MF "v[24:27],a[184:187],v[180:183],v[24:27]\n"
"s_add_u32 m0,s79,28672\n"
TR "a[242:243],v1 offset:6144\n"
AR "v230,a50\n"
MF "v[24:27],a[144:147],v[176:179],v[24:27]\n"
LD "v196,s[48:51],s89 offen lds\n"
MF "v[32:35],a[148:151],v[176:179],v[32:35]\n"
"s_cmp_ge_u32 s59,10\n"
"s_cselect_b32 s96,4294557760,0\n"
TR "a[244:245],v1 offset:8192\n"
MF "v[32:35],a[188:191],v[180:183],v[32:35]\n"
"s_add_u32 s96,s96,1024\n"
TR "a[246:247],v1 offset:10240\n"
AR "v231,a51\n"
MF "v[32:35],a[228:231],v[184:187],v[32:35]\n"
LD "v197,s[48:51],s89 offen offset:1024 lds\n"
MF "v[40:43],a[232:235],v[184:187],v[40:43]\n"
"s_mul_i32 s59,s59,40960\n"
TR "v[108:109],v4\n"
AR "v232,a68\n"
MF "v[40:43],a[192:195],v[180:183],v[40:43]\n"
"s_add_u32 s96,s96,s59\n"
TR "v[110:111],v4 offset:2048\n"
AR "v233,a69\n"
MF "v[40:43],a[152:155],v[176:179],v[40:43]\n"
"s_lshl_b32 s59,s95,7\n"
TR "v[112:113],v4 offset:4096\n"
AR "v234,a70\n"
MF "v[48:51],a[156:159],v[176:179],v[48:51]\n"
"s_add_u32 s96,s96,s59\n"
TR "v[114:115],v4 offset:6144\n"
AR "v235,a71\n"
MF "v[48:51],a[196:199],v[180:183],v[48:51]\n"
LD "v198,s[48:51],s89 offen offset:2048 lds\n"
MF "v[48:51],a[236:239],v[184:187],v[48:51]\n"
"s_waitcnt lgkmcnt(4)\n"
TR "v[116:117],v4 offset:8192\n"
"ds_read_b128 a[68:71],v236 offset:53248\n"
MF "v[52:55],a[236:239],a[244:247],v[52:55]\n"
"s_lshl_b32 s3,s92,10\n"
TR "v[176:177],v2\n"
"v_fma_f32 v228,v228,v210,v216\n"
MF "v[52:55],a[196:199],a[240:243],v[52:55]\n"
"s_add_u32 s3,s3,s93\n"
TR "v[178:179],v2 offset:2048\n"
"v_fma_f32 v229,v229,v210,v216\n"
MF "v[52:55],a[156:159],v[188:191],v[52:55]\n"
LD "v199,s[48:51],s89 offen offset:3072 lds\n"
MF "v[44:47],a[152:155],v[188:191],v[44:47]\n"
"s_cmp_lt_u32 s92,40\n"
"s_cselect_b32 s3,s3,0xf0000000\n"
TR "v[180:181],v2 offset:4096\n"
MF "v[44:47],a[192:195],a[240:243],v[44:47]\n"
"s_cmp_lt_u32 s92,40\n"
"s_cselect_b32 s1,s96,0xf0000000\n"
TR "v[182:183],v2 offset:6144\n"
MF "v[44:47],a[232:235],a[244:247],v[44:47]\n"
"s_sub_u32 s92,s92,s4\n"
TR "v[118:119],v4 offset:10240\n"
"v_fma_f32 v230,v230,v210,v216\n"
MF "v[36:39],a[228:231],a[244:247],v[36:39]\n"
"s_sub_u32 s2,s2,204800\n"
TR "v[184:185],v2 offset:8192\n"
"v_fma_f32 v231,v231,v210,v216\n"
MF "v[36:39],a[188:191],a[240:243],v[36:39]\n"
"s_add_u32 m0,s79,32768\n"
TR "v[186:187],v2 offset:10240\n"
"v_fma_f32 v232,v232,v210,v216\n"
MF "v[36:39],a[148:151],v[188:191],v[36:39]\n"
LD "v200,s[48:51],s89 offen lds\n"
MF "v[28:31],a[144:147],v[188:191],v[28:31]\n"
"s_mov_b32 s92,64\n"
TR "v[120:121],v1 offset:49152\n"
"v_fma_f32 v233,v233,v210,v216\n"
MF "v[28:31],a[184:187],a[240:243],v[28:31]\n"
TR "v[122:123],v1 offset:51200\n"
"v_fma_f32 v234,v234,v210,v216\n"
MF "v[28:31],a[224:227],a[244:247],v[28:31]\n"
TR "v[124:125],v1 offset:53248\n"
"v_fma_f32 v235,v235,v210,v216\n"
MF "v[20:23],a[220:223],a[244:247],v[20:23]\n"
LD "v201,s[48:51],s89 offen offset:1024 lds\n"
MF "v[20:23],a[180:183],a[240:243],v[20:23]\n"
TR "v[126:127],v1 offset:55296\n"
CV "v228,v228,v229\n"
MF "v[20:23],a[140:143],v[188:191],v[20:23]\n"
"s_waitcnt lgkmcnt(4)\n"
TR "v[128:129],v1 offset:57344\n"
CV "v229,v230,v231\n"
MF "v[56:59],a[140:143],v[176:179],v[56:59]\n"
TR "v[188:189],v3\n"
CV "v230,v232,v233\n"
MF "v[56:59],a[180:183],v[180:183],v[56:59]\n"
TR "v[190:191],v3 offset:2048\n"
CV "v231,v234,v235\n"
MF "v[56:59],a[220:223],v[184:187],v[56:59]\n"
LD "v202,s[48:51],s89 offen offset:2048 lds\n"
MF "v[64:67],a[224:227],v[184:187],v[64:67]\n"
TR "a[240:241],v3 offset:4096\n"
AW "a48,v228\n"
MF "v[64:67],a[184:187],v[180:183],v[64:67]\n"
TR "a[242:243],v3 offset:6144\n"
AW "a49,v229\n"
MF "v[64:67],a[144:147],v[176:179],v[64:67]\n"
LD "v203,s[48:51],s89 offen offset:3072 lds\n"
MF "v[72:75],a[148:151],v[176:179],v[72:75]\n"
TR "a[244:245],v3 offset:8192\n"
AW "a50,v230\n"
MF "v[72:75],a[188:191],v[180:183],v[72:75]\n"
TR "a[246:247],v3 offset:10240\n"
AW "a51,v231\n"
MF "v[72:75],a[228:231],v[184:187],v[72:75]\n"
TR "v[130:131],v1 offset:59392\n"
AR "v220,a88\n"
MF "v[80:83],a[232:235],v[184:187],v[80:83]\n"
TR "v[132:133],v5\n"
AR "v221,a89\n"
MF "v[80:83],a[192:195],v[180:183],v[80:83]\n"
TR "v[134:135],v5 offset:2048\n"
AR "v222,a90\n"
MF "v[80:83],a[152:155],v[176:179],v[80:83]\n"
TR "v[136:137],v5 offset:4096\n"
AR "v223,a91\n"
MF "v[88:91],a[156:159],v[176:179],v[88:91]\n"
TR "v[138:139],v5 offset:6144\n"
AR "v224,a108\n"
MF "v[88:91],a[196:199],v[180:183],v[88:91]\n"
TR "v[140:141],v5 offset:8192\n"
"s_waitcnt lgkmcnt(0)\n"
MF "v[88:91],a[236:239],v[184:187],v[88:91]\n"
"s_barrier\n"
MF "v[92:95],a[236:239],a[244:247],v[92:95]\n"
TR "v[142:143],v5 offset:10240\n"
AR "v225,a109\n"
MF "v[92:95],a[196:199],a[240:243],v[92:95]\n"
TR "v[144:145],v2 offset:49152\n"
AR "v226,a110\n"
MF "v[92:95],a[156:159],v[188:191],v[92:95]\n"
TR "v[146:147],v2 offset:51200\n"
AR "v227,a111\n"
MF "v[84:87],a[152:155],v[188:191],v[84:87]\n"
"s_nop 0\n"
TR "v[148:149],v2 offset:53248\n"
"ds_read_b128 a[108:111],v237 offset:8192\n"
MF "v[84:87],a[192:195],a[240:243],v[84:87]\n"
TR "v[150:151],v2 offset:55296\n"
"v_fma_f32 v220,v220,v210,v216\n"
MF "v[84:87],a[232:235],a[244:247],v[84:87]\n"
TR "v[152:153],v2 offset:57344\n"
"v_fma_f32 v221,v221,v210,v216\n"
MF "v[76:79],a[228:231],a[244:247],v[76:79]\n"
".Lpg29h:\n"
"s_cbranch_execz .Lpg33h\n"
TR "v[154:155],v2 offset:59392\n"
"v_fma_f32 v222,v222,v210,v216\n"
MF "v[76:79],a[188:191],a[240:243],v[76:79]\n"
TR "v[156:157],v6\n"
"v_fma_f32 v223,v223,v210,v216\n"
MF "v[76:79],a[148:151],v[188:191],v[76:79]\n"
TR "v[158:159],v6 offset:2048\n"
"v_fma_f32 v224,v224,v210,v216\n"
MF "v[68:71],a[144:147],v[188:191],v[68:71]\n"
TR "v[160:161],v6 offset:4096\n"
"v_fma_f32 v225,v225,v210,v216\n"
MF "v[68:71],a[184:187],a[240:243],v[68:71]\n"
TR "v[162:163],v6 offset:6144\n"
"v_fma_f32 v226,v226,v210,v216\n"
MF "v[68:71],a[224:227],a[244:247],v[68:71]\n"
TR "v[164:165],v6 offset:8192\n"
"v_fma_f32 v227,v227,v210,v216\n"
MF "v[60:63],a[220:223],a[244:247],v[60:63]\n"
TR "v[166:167],v6 offset:10240\n"
CV "v220,v220,v221\n"
MF "v[60:63],a[180:183],a[240:243],v[60:63]\n"
TR "v[168:169],v3 offset:49152\n"
CV "v221,v222,v223\n"
MF "v[60:63],a[140:143],v[188:191],v[60:63]\n"
TR "v[170:171],v3 offset:51200\n"
CV "v222,v224,v225\n"
MF "v[16:19],a[0:3],v[96:99],v[16:19]\n"
TR "v[172:173],v3 offset:53248\n"
CV "v223,v226,v227\n"
MF "v[16:19],a[40:43],v[100:103],v[16:19]\n"
TR "v[174:175],v3 offset:55296\n"
AW "a88,v220\n"
MF "v[16:19],a[80:83],v[104:107],v[16:19]\n"
TR "v[204:205],v3 offset:57344\n"
AW "a89,v221\n"
MF "v[16:19],a[20:23],v[108:111],v[16:19]\n"
TR "v[206:207],v3 offset:59392\n"
AW "a90,v222\n"
MF "v[16:19],a[60:63],v[112:115],v[16:19]\n"
TR "v[244:245],v7\n"
AW "a91,v223\n"
MF "v[16:19],a[100:103],v[116:119],v[16:19]\n"
"s_waitcnt lgkmcnt(15)\n"
TR "v[246:247],v7 offset:2048\n"
AR "v228,a12\n"
MF "v[20:23],a[100:103],v[140:143],v[20:23]\n"
TR "v[248:249],v7 offset:4096\n"
AR "v229,a13\n"
MF "v[20:23],a[60:63],v[136:139],v[20:23]\n"
TR "v[250:251],v7 offset:6144\n"
AR "v230,a14\n"
MF "v[20:23],a[20:23],v[132:135],v[20:23]\n"
TR "v[238:239],v7 offset:8192\n"
AR "v231,a15\n"
MF "v[20:23],a[80:83],v[128:131],v[20:23]\n"
TR "v[240:241],v7 offset:10240\n"
AR "v232,a32\n"
MF "v[20:23],a[40:43],v[124:127],v[20:23]\n"
AR "v233,a33\n"
AR "v234,a34\n"
MF "v[20:23],a[0:3],v[120:123],v[20:23]\n"
"s_waitcnt lgkmcnt(12)\n"
AR "v235,a35\n"
"v_fma_f32 v228,v228,v211,v217\n"
MF "v[56:59],a[0:3],v[144:147],v[56:59]\n"
"ds_read_b128 a[32:35],v236 offset:36864\n"
"v_fma_f32 v229,v229,v211,v217\n"
MF "v[56:59],a[40:43],v[148:151],v[56:59]\n"
"v_fma_f32 v230,v230,v211,v217\n"
"v_fma_f32 v231,v231,v211,v217\n"
MF "v[56:59],a[80:83],v[152:155],v[56:59]\n"
"v_fma_f32 v232,v232,v211,v217\n"
"v_fma_f32 v233,v233,v211,v217\n"
MF "v[56:59],a[20:23],v[156:159],v[56:59]\n"
"v_fma_f32 v234,v234,v211,v217\n"
"v_fma_f32 v235,v235,v211,v217\n"
MF "v[56:59],a[60:63],v[160:163],v[56:59]\n"
CV "v228,v228,v229\n"
CV "v229,v230,v231\n"
MF "v[56:59],a[100:103],v[164:167],v[56:59]\n"
"s_waitcnt lgkmcnt(1)\n"
CV "v230,v232,v233\n"
CV "v231,v234,v235\n"
MF "v[60:63],a[100:103],v[238:241],v[60:63]\n"
AW "a12,v228\n"
AW "a13,v229\n"
MF "v[60:63],a[60:63],v[248:251],v[60:63]\n"
AW "a14,v230\n"
AW "a15,v231\n"
MF "v[60:63],a[20:23],v[244:247],v[60:63]\n"
AR "v220,a52\n"
AR "v221,a53\n"
MF "v[60:63],a[80:83],v[204:207],v[60:63]\n"
AR "v222,a54\n"
AR "v223,a55\n"
MF "v[60:63],a[40:43],v[172:175],v[60:63]\n"
AR "v224,a72\n"
AR "v225,a73\n"
MF "v[60:63],a[0:3],v[168:171],v[60:63]\n"
AR "v226,a74\n"
AR "v227,a75\n"
MF "v[68:71],a[4:7],v[168:171],v[68:71]\n"
"ds_read_b128 a[72:75],v236 offset:57344\n"
"v_fma_f32 v220,v220,v211,v217\n"
MF "v[68:71],a[44:47],v[172:175],v[68:71]\n"
"v_fma_f32 v221,v221,v211,v217\n"
"v_fma_f32 v222,v222,v211,v217\n"
MF "v[68:71],a[84:87],v[204:207],v[68:71]\n"
"v_fma_f32 v223,v223,v211,v217\n"
"v_fma_f32 v224,v224,v211,v217\n"
MF "v[68:71],a[24:27],v[244:247],v[68:71]\n"
"v_fma_f32 v225,v225,v211,v217\n"
"v_fma_f32 v226,v226,v211,v217\n"
MF "v[68:71],a[64:67],v[248:251],v[68:71]\n"
"v_fma_f32 v227,v227,v211,v217\n"
CV "v220,v220,v221\n"
MF "v[68:71],a[104:107],v[238:241],v[68:71]\n"
"s_waitcnt lgkmcnt(0)\n"
CV "v221,v222,v223\n"
CV "v222,v224,v225\n"
MF "v[64:67],a[104:107],v[164:167],v[64:67]\n"
"s_barrier\n"
MF "v[64:67],a[64:67],v[160:163],v[64:67]\n"
CV "v223,v226,v227\n"
AW "a52,v220\n"
MF "v[64:67],a[24:27],v[156:159],v[64:67]\n"
AW "a53,v221\n"
AW "a54,v222\n"
MF "v[64:67],a[84:87],v[152:155],v[64:67]\n"
AW "a55,v223\n"
AR "v228,a92\n"
MF "v[64:67],a[44:47],v[148:151],v[64:67]\n"
AR "v229,a93\n"
AR "v230,a94\n"
MF "v[64:67],a[4:7],v[144:147],v[64:67]\n"
AR "v231,a95\n"
AR "v232,a112\n"
MF "v[28:31],a[4:7],v[120:123],v[28:31]\n"
AR "v233,a113\n"
AR "v234,a114\n"
MF "v[28:31],a[44:47],v[124:127],v[28:31]\n"
AR "v235,a115\n"
"v_fma_f32 v228,v228,v211,v217\n"
MF "v[28:31],a[84:87],v[128:131],v[28:31]\n"
"ds_read_b128 a[112:115],v237 offset:12288\n"
"v_fma_f32 v229,v229,v211,v217\n"
MF "v[28:31],a[24:27],v[132:135],v[28:31]\n"
"v_fma_f32 v230,v230,v211,v217\n"
"v_fma_f32 v231,v231,v211,v217\n"
MF "v[28:31],a[64:67],v[136:139],v[28:31]\n"
"v_fma_f32 v232,v232,v211,v217\n"
"v_fma_f32 v233,v233,v211,v217\n"
MF "v[28:31],a[104:107],v[140:143],v[28:31]\n"
"v_fma_f32 v234,v234,v211,v217\n"
"v_fma_f32 v235,v235,v211,v217\n"
MF "v[24:27],a[104:107],v[116:119],v[24:27]\n"
CV "v228,v228,v229\n"
CV "v229,v230,v231\n"
MF "v[24:27],a[64:67],v[112:115],v[24:27]\n"
CV "v230,v232,v233\n"
CV "v231,v234,v235\n"
MF "v[24:27],a[24:27],v[108:111],v[24:27]\n"
AW "a92,v228\n"
AW "a93,v229\n"
MF "v[24:27],a[84:87],v[104:107],v[24:27]\n"
AW "a94,v230\n"
AW "a95,v231\n"
MF "v[24:27],a[44:47],v[100:103],v[24:27]\n"
AR "v220,a16\n"
AR "v221,a17\n"
MF "v[24:27],a[4:7],v[96:99],v[24:27]\n"
AR "v222,a18\n"
AR "v223,a19\n"
MF "v[32:35],a[8:11],v[96:99],v[32:35]\n"
AR "v224,a36\n"
AR "v225,a37\n"
MF "v[32:35],a[48:51],v[100:103],v[32:35]\n"
AR "v226,a38\n"
AR "v227,a39\n"
MF "v[32:35],a[88:91],v[104:107],v[32:35]\n"
"ds_read_b128 a[36:39],v236 offset:40960\n"
"v_fma_f32 v220,v220,v212,v218\n"
MF "v[32:35],a[28:31],v[108:111],v[32:35]\n"
"v_fma_f32 v221,v221,v212,v218\n"
"v_fma_f32 v222,v222,v212,v218\n"
MF "v[32:35],a[68:71],v[112:115],v[32:35]\n"
"v_fma_f32 v223,v223,v212,v218\n"
"v_fma_f32 v224,v224,v212,v218\n"
MF "v[32:35],a[108:111],v[116:119],v[32:35]\n"
"v_fma_f32 v225,v225,v212,v218\n"
"v_fma_f32 v226,v226,v212,v218\n"
MF "v[36:39],a[108:111],v[140:143],v[36:39]\n"
"v_fma_f32 v227,v227,v212,v218\n"
CV "v220,v220,v221\n"
MF "v[36:39],a[68:71],v[136:139],v[36:39]\n"
CV "v221,v222,v223\n"
CV "v222,v224,v225\n"
MF "v[36:39],a[28:31],v[132:135],v[36:39]\n"
CV "v223,v226,v227\n"
AW "a16,v220\n"
MF "v[36:39],a[88:91],v[128:131],v[36:39]\n"
AW "a17,v221\n"
AW "a18,v222\n"
MF "v[36:39],a[48:51],v[124:127],v[36:39]\n"
AW "a19,v223\n"
AR "v228,a56\n"
MF "v[36:39],a[8:11],v[120:123],v[36:39]\n"
AR "v229,a57\n"
AR "v230,a58\n"
MF "v[72:75],a[8:11],v[144:147],v[72:75]\n"
AR "v231,a59\n"
AR "v232,a76\n"
MF "v[72:75],a[48:51],v[148:151],v[72:75]\n"
AR "v233,a77\n"
AR "v234,a78\n"
MF "v[72:75],a[88:91],v[152:155],v[72:75]\n"
AR "v235,a79\n"
"v_fma_f32 v228,v228,v212,v218\n"
MF "v[72:75],a[28:31],v[156:159],v[72:75]\n"
"ds_read_b128 a[76:79],v236 offset:61440\n"
"v_fma_f32 v229,v229,v212,v218\n"
MF "v[72:75],a[68:71],v[160:163],v[72:75]\n"
"v_fma_f32 v230,v230,v212,v218\n"
"v_fma_f32 v231,v231,v212,v218\n"
MF "v[72:75],a[108:111],v[164:167],v[72:75]\n"
"v_fma_f32 v232,v232,v212,v218\n"
"v_fma_f32 v233,v233,v212,v218\n"
MF "v[76:79],a[108:111],v[238:241],v[76:79]\n"
"v_fma_f32 v234,v234,v212,v218\n"
"v_fma_f32 v235,v235,v212,v218\n"
MF "v[76:79],a[68:71],v[248:251],v[76:79]\n"
CV "v228,v228,v229\n"
CV "v229,v230,v231\n"
MF "v[76:79],a[28:31],v[244:247],v[76:79]\n"
CV "v230,v232,v233\n"
CV "v231,v234,v235\n"
MF "v[76:79],a[88:91],v[204:207],v[76:79]\n"
AW "a56,v228\n"
AW "a57,v229\n"
MF "v[76:79],a[48:51],v[172:175],v[76:79]\n"
AW "a58,v230\n"
AW "a59,v231\n"
MF "v[76:79],a[8:11],v[168:171],v[76:79]\n"
"s_waitcnt lgkmcnt(2)\n"
AR "v220,a96\n"
AR "v221,a97\n"
MF "v[84:87],a[12:15],v[168:171],v[84:87]\n"
AR "v222,a98\n"
AR "v223,a99\n"
MF "v[84:87],a[52:55],v[172:175],v[84:87]\n"
AR "v224,a116\n"
AR "v225,a117\n"
MF "v[84:87],a[92:95],v[204:207],v[84:87]\n"
AR "v226,a118\n"
AR "v227,a119\n"
MF "v[84:87],a[32:35],v[244:247],v[84:87]\n"
"ds_read_b128 a[116:119],v237 offset:16384\n"
"v_fma_f32 v220,v220,v212,v218\n"
MF "v[84:87],a[72:75],v[248:251],v[84:87]\n"
"v_fma_f32 v221,v221,v212,v218\n"
"v_fma_f32 v222,v222,v212,v218\n"
MF "v[84:87],a[112:115],v[238:241],v[84:87]\n"
"v_fma_f32 v223,v223,v212,v218\n"
"v_fma_f32 v224,v224,v212,v218\n"
MF "v[80:83],a[112:115],v[164:167],v[80:83]\n"
"v_fma_f32 v225,v225,v212,v218\n"
"v_fma_f32 v226,v226,v212,v218\n"
MF "v[80:83],a[72:75],v[160:163],v[80:83]\n"
"v_fma_f32 v227,v227,v212,v218\n"
CV "v220,v220,v221\n"
MF "v[80:83],a[32:35],v[156:159],v[80:83]\n"
CV "v221,v222,v223\n"
CV "v222,v224,v225\n"
MF "v[80:83],a[92:95],v[152:155],v[80:83]\n"
CV "v223,v226,v227\n"
AW "a96,v220\n"
MF "v[80:83],a[52:55],v[148:151],v[80:83]\n"
AW "a97,v221\n"
AW "a98,v222\n"
MF "v[80:83],a[12:15],v[144:147],v[80:83]\n"
AW "a99,v223\n"
"v_mov_b32 v15,0\n"
MF "v[44:47],a[12:15],v[120:123],v[44:47]\n"
"v_lshrrev_b32 v242,5,v9\n"
"v_min_u32 v242,23,v242\n"
MF "v[44:47],a[52:55],v[124:127],v[44:47]\n"
"v_mul_u32_u24 v242,160,v242\n"
"v_and_or_b32 v242,v9,16,v242\n"
MF "v[44:47],a[92:95],v[128:131],v[44:47]\n"
"v_lshlrev_b32 v242,3,v242\n"
AR "v8,a252\n"
MF "v[44:47],a[32:35],v[132:135],v[44:47]\n"
AR "v11,a253\n"
"v_bfe_u32 v224,v9,5,3\n"
MF "v[44:47],a[72:75],v[136:139],v[44:47]\n"
"s_add_u32 s62,s74,s68\n"
"s_add_u32 s68,s68,256\n"
"s_and_b32 s68,s68,768\n"
MF "v[44:47],a[112:115],v[140:143],v[44:47]\n"
"buffer_load_dword v243,v242,s[48:51],s62 offen\n"
MF "v[40:43],a[112:115],v[116:119],v[40:43]\n"
"v_bfe_u32 v225,v9,8,2\n"
"v_lshlrev_b32 v225,1,v225\n"
MF "v[40:43],a[72:75],v[112:115],v[40:43]\n"
"v_mov_b32 v14,s76\n"
"s_add_u32 s76,s76,1\n"
MF "v[40:43],a[32:35],v[108:111],v[40:43]\n"
"buffer_store_dword v14,v15,s[40:43],s94 offen sc1\n"
MF "v[40:43],a[92:95],v[104:107],v[40:43]\n"
"v_and_b32 v226,16,v9\n"
"v_lshlrev_b32_e64 v226,v226,s9\n"
MF "v[40:43],a[52:55],v[100:103],v[40:43]\n"
"v_sub_u32 v224,v224,v225\n"
"v_cmp_eq_u32 vcc,0,v224\n"
MF "v[40:43],a[12:15],v[96:99],v[40:43]\n"
"s_waitcnt lgkmcnt(0)\n"
"v_cndmask_b32 v220,0,v226,vcc\n"
"v_cmp_eq_u32 vcc,1,v224\n"
MF "v[48:51],a[16:19],v[96:99],v[48:51]\n"
"s_nop 0\n"
"v_cndmask_b32 v221,0,v226,vcc\n"
AW "a248,0\n"
MF "v[48:51],a[56:59],v[100:103],v[48:51]\n"
AW "a249,0\n"
AW "a250,v220\n"
MF "v[48:51],a[96:99],v[104:107],v[48:51]\n"
AW "a251,v221\n"
AW "a252,v220\n"
MF "v[48:51],a[36:39],v[108:111],v[48:51]\n"
AW "a253,v221\n"
AW "a254,0\n"
MF "v[48:51],a[76:79],v[112:115],v[48:51]\n"
AW "a255,0\n"
"v_bfe_u32 v12,v9,4,4\n"
MF "v[48:51],a[116:119],v[116:119],v[48:51]\n"
"v_lshlrev_b32 v12,2,v12\n"
"v_lshrrev_b32 v13,2,v9\n"
MF "v[52:55],a[116:119],v[140:143],v[52:55]\n"
"v_add_u32 v13,s58,v13\n"
"v_add_u32 v4,24576,v4\n"
MF "v[52:55],a[76:79],v[136:139],v[52:55]\n"
"s_waitcnt vmcnt(2) lgkmcnt(0)\n"
"v_add_u32 v5,24576,v5\n"
"v_add_u32 v6,24576,v6\n"
MF "v[52:55],a[36:39],v[132:135],v[52:55]\n"
"s_barrier\n"
MF "v[52:55],a[96:99],v[128:131],v[52:55]\n"
TR "v[176:177],v0 offset:24576\n"
"v_add_u32 v7,24576,v7\n"
MF "v[52:55],a[56:59],v[124:127],v[52:55]\n"
TR "v[178:179],v0 offset:26624\n"
MF "v[52:55],a[16:19],v[120:123],v[52:55]\n"
MF "v[88:91],a[16:19],v[144:147],v[88:91]\n"
TR "v[180:181],v0 offset:28672\n"
MF "v[88:91],a[56:59],v[148:151],v[88:91]\n"
TR "v[182:183],v0 offset:30720\n"
MF "v[88:91],a[96:99],v[152:155],v[88:91]\n"
MF "v[88:91],a[36:39],v[156:159],v[88:91]\n"
TR "v[184:185],v0 offset:32768\n"
MF "v[88:91],a[76:79],v[160:163],v[88:91]\n"
TR "v[186:187],v0 offset:34816\n"
MF "v[88:91],a[116:119],v[164:167],v[88:91]\n"
MF "v[92:95],a[116:119],v[238:241],v[92:95]\n"
MF "v[92:95],a[76:79],v[248:251],v[92:95]\n"
MF "v[92:95],a[36:39],v[244:247],v[92:95]\n"
MF "v[92:95],a[96:99],v[204:207],v[92:95]\n"
MF "v[92:95],a[56:59],v[172:175],v[92:95]\n"
MF "v[92:95],a[16:19],v[168:171],v[92:95]\n"
"s_waitcnt lgkmcnt(0)\n"
".Lpl15:\n"
MF "v[96:99],a[0:3],v[176:179],v[252:255]\n"
TR "v[188:189],v1 offset:24576\n"
MF "v[96:99],a[40:43],v[180:183],v[96:99]\n"
TR "v[190:191],v1 offset:26624\n"
MF "v[96:99],a[80:83],v[184:187],v[96:99]\n"
MF "v[104:107],a[84:87],v[184:187],v[252:255]\n"
TR "a[240:241],v1 offset:28672\n"
MF "v[104:107],a[44:47],v[180:183],v[104:107]\n"
TR "a[242:243],v1 offset:30720\n"
MF "v[104:107],a[4:7],v[176:179],v[104:107]\n"
MF "v[112:115],a[8:11],v[176:179],v[252:255]\n"
TR "a[244:245],v1 offset:32768\n"
MF "v[112:115],a[48:51],v[180:183],v[112:115]\n"
TR "a[246:247],v1 offset:34816\n"
MF "v[112:115],a[88:91],v[184:187],v[112:115]\n"
MF "v[120:123],a[92:95],v[184:187],v[252:255]\n"
MF "v[120:123],a[52:55],v[180:183],v[120:123]\n"
MF "v[120:123],a[12:15],v[176:179],v[120:123]\n"
MF "v[128:131],a[16:19],v[176:179],v[252:255]\n"
MF "v[128:131],a[56:59],v[180:183],v[128:131]\n"
MF "v[128:131],a[96:99],v[184:187],v[128:131]\n"
"s_waitcnt lgkmcnt(0)\n"
MF "v[132:135],a[96:99],a[244:247],v[252:255]\n"
TR "v[176:177],v2 offset:24576\n"
MF "v[132:135],a[56:59],a[240:243],v[132:135]\n"
TR "v[178:179],v2 offset:26624\n"
MF "v[132:135],a[16:19],v[188:191],v[132:135]\n"
MF "v[124:127],a[12:15],v[188:191],v[252:255]\n"
TR "v[180:181],v2 offset:28672\n"
MF "v[124:127],a[52:55],a[240:243],v[124:127]\n"
TR "v[182:183],v2 offset:30720\n"
MF "v[124:127],a[92:95],a[244:247],v[124:127]\n"
MF "v[116:119],a[88:91],a[244:247],v[252:255]\n"
TR "v[184:185],v2 offset:32768\n"
MF "v[116:119],a[48:51],a[240:243],v[116:119]\n"
TR "v[186:187],v2 offset:34816\n"
MF "v[116:119],a[8:11],v[188:191],v[116:119]\n"
MF "v[108:111],a[4:7],v[188:191],v[252:255]\n"
MF "v[108:111],a[44:47],a[240:243],v[108:111]\n"
MF "v[108:111],a[84:87],a[244:247],v[108:111]\n"
MF "v[100:103],a[80:83],a[244:247],v[252:255]\n"
MF "v[100:103],a[40:43],a[240:243],v[100:103]\n"
MF "v[100:103],a[0:3],v[188:191],v[100:103]\n"
"s_waitcnt lgkmcnt(0)\n"
MF "v[136:139],a[0:3],v[176:179],v[252:255]\n"
TR "v[188:189],v3 offset:24576\n"
MF "v[136:139],a[40:43],v[180:183],v[136:139]\n"
TR "v[190:191],v3 offset:26624\n"
MF "v[136:139],a[80:83],v[184:187],v[136:139]\n"
MF "v[144:147],a[84:87],v[184:187],v[252:255]\n"
TR "a[240:241],v3 offset:28672\n"
MF "v[144:147],a[44:47],v[180:183],v[144:147]\n"
TR "a[242:243],v3 offset:30720\n"
MF "v[144:147],a[4:7],v[176:179],v[144:147]\n"
MF "v[152:155],a[8:11],v[176:179],v[252:255]\n"
TR "a[244:245],v3 offset:32768\n"
MF "v[152:155],a[48:51],v[180:183],v[152:155]\n"
TR "a[246:247],v3 offset:34816\n"
MF "v[152:155],a[88:91],v[184:187],v[152:155]\n"
MF "v[160:163],a[92:95],v[184:187],v[252:255]\n"
MF "v[160:163],a[52:55],v[180:183],v[160:163]\n"
MF "v[160:163],a[12:15],v[176:179],v[160:163]\n"
MF "v[168:171],a[16:19],v[176:179],v[252:255]\n"
MF "v[168:171],a[56:59],v[180:183],v[168:171]\n"
"s_waitcnt lgkmcnt(0)\n"
MF "v[168:171],a[96:99],v[184:187],v[168:171]\n"
"s_barrier\n"
MF "v[172:175],a[96:99],a[244:247],v[252:255]\n"
TR "v[176:177],v0\n"
MF "v[172:175],a[56:59],a[240:243],v[172:175]\n"
TR "v[178:179],v0 offset:2048\n"
MF "v[172:175],a[16:19],v[188:191],v[172:175]\n"
"ds_read2st64_b32 v[192:193],v13 offset0:192 offset1:193\n"
MF "v[164:167],a[12:15],v[188:191],v[252:255]\n"
TR "v[180:181],v0 offset:4096\n"
MF "v[164:167],a[52:55],a[240:243],v[164:167]\n"
TR "v[182:183],v0 offset:6144\n"
MF "v[164:167],a[92:95],a[244:247],v[164:167]\n"
"ds_read2st64_b32 v[194:195],v13 offset0:194 offset1:195\n"
MF "v[156:159],a[88:91],a[244:247],v[252:255]\n"
TR "v[184:185],v0 offset:8192\n"
MF "v[156:159],a[48:51],a[240:243],v[156:159]\n"
TR "v[186:187],v0 offset:10240\n"
MF "v[156:159],a[8:11],v[188:191],v[156:159]\n"
"ds_read2st64_b32 v[196:197],v13 offset0:196 offset1:197\n"
MF "v[148:151],a[4:7],v[188:191],v[252:255]\n"
"ds_read2st64_b32 v[198:199],v13 offset0:198 offset1:199\n"
MF "v[148:151],a[44:47],a[240:243],v[148:151]\n"
MF "v[148:151],a[84:87],a[244:247],v[148:151]\n"
MF "v[140:143],a[80:83],a[244:247],v[252:255]\n"
MF "v[140:143],a[40:43],a[240:243],v[140:143]\n"
MF "v[140:143],a[0:3],v[188:191],v[140:143]\n"
"s_waitcnt lgkmcnt(2)\n"
MF "v[96:99],a[120:123],v[176:179],v[96:99]\n"
"s_waitcnt lgkmcnt(0)\n"
TR "v[188:189],v1\n"
"v_add_f32 v16,v16,v192\n"
MF "v[96:99],a[160:163],v[180:183],v[96:99]\n"
TR "v[190:191],v1 offset:2048\n"
"v_add_f32 v17,v17,v193\n"
MF "v[96:99],a[200:203],v[184:187],v[96:99]\n"
"ds_read2st64_b32 v[200:201],v13 offset0:200 offset1:201\n"
"v_add_f32 v18,v18,v194\n"
"s_waitcnt vmcnt(0)\n"
"v_mov_b32 v14,s76\n"
"s_add_u32 s76,s76,1\n"
MF "v[104:107],a[204:207],v[184:187],v[104:107]\n"
TR "a[240:241],v1 offset:4096\n"
"v_add_f32 v19,v19,v195\n"
"buffer_store_dword v14,v15,s[40:43],s94 offen sc1\n"
MF "v[104:107],a[164:167],v[180:183],v[104:107]\n"
TR "a[242:243],v1 offset:6144\n"
"v_add_f32 v20,v20,v196\n"
MF "v[104:107],a[124:127],v[176:179],v[104:107]\n"
"ds_read2st64_b32 v[202:203],v13 offset0:202 offset1:203\n"
"v_add_f32 v21,v21,v197\n"
MF "v[112:115],a[128:131],v[176:179],v[112:115]\n"
TR "a[244:245],v1 offset:8192\n"
"v_add_f32 v22,v22,v198\n"
MF "v[112:115],a[168:171],v[180:183],v[112:115]\n"
TR "a[246:247],v1 offset:10240\n"
"v_add_f32 v23,v23,v199\n"
MF "v[112:115],a[208:211],v[184:187],v[112:115]\n"
"ds_read2st64_b32 v[204:205],v13 offset0:204 offset1:205\n"
CV "v16,v16,v17\n"
MF "v[120:123],a[212:215],v[184:187],v[120:123]\n"
"ds_read2st64_b32 v[206:207],v13 offset0:206 offset1:207\n"
CV "v17,v18,v19\n"
MF "v[120:123],a[172:175],v[180:183],v[120:123]\n"
"s_add_u32 s87,s90,0\n"
CV "v18,v20,v21\n"
CV "v19,v22,v23\n"
MF "v[120:123],a[132:135],v[176:179],v[120:123]\n"
MF "v[128:131],a[136:139],v[176:179],v[128:131]\n"
"buffer_store_dwordx4 v[16:19],v9,s[40:43],s87 offen sc1\n"
MF "v[128:131],a[176:179],v[180:183],v[128:131]\n"
MF "v[128:131],a[216:219],v[184:187],v[128:131]\n"
"s_waitcnt lgkmcnt(2)\n"
MF "v[132:135],a[216:219],a[244:247],v[132:135]\n"
"s_waitcnt lgkmcnt(0)\n"
TR "v[176:177],v2\n"
"v_add_f32 v24,v24,v200\n"
MF "v[132:135],a[176:179],a[240:243],v[132:135]\n"
TR "v[178:179],v2 offset:2048\n"
"v_add_f32 v25,v25,v201\n"
MF "v[132:135],a[136:139],v[188:191],v[132:135]\n"
"ds_read2st64_b32 v[192:193],v13 offset0:208 offset1:209\n"
"v_add_f32 v26,v26,v202\n"
MF "v[124:127],a[132:135],v[188:191],v[124:127]\n"
TR "v[180:181],v2 offset:4096\n"
"v_add_f32 v27,v27,v203\n"
MF "v[124:127],a[172:175],a[240:243],v[124:127]\n"
TR "v[182:183],v2 offset:6144\n"
"v_add_f32 v28,v28,v204\n"
MF "v[124:127],a[212:215],a[244:247],v[124:127]\n"
"ds_read2st64_b32 v[194:195],v13 offset0:210 offset1:211\n"
"v_add_f32 v29,v29,v205\n"
MF "v[116:119],a[208:211],a[244:247],v[116:119]\n"
TR "v[184:185],v2 offset:8192\n"
"v_add_f32 v30,v30,v206\n"
MF "v[116:119],a[168:171],a[240:243],v[116:119]\n"
TR "v[186:187],v2 offset:10240\n"
"v_add_f32 v31,v31,v207\n"
MF "v[116:119],a[128:131],v[188:191],v[116:119]\n"
"ds_read2st64_b32 v[196:197],v13 offset0:212 offset1:213\n"
CV "v24,v24,v25\n"
MF "v[108:111],a[124:127],v[188:191],v[108:111]\n"
"ds_read2st64_b32 v[198:199],v13 offset0:214 offset1:215\n"
CV "v25,v26,v27\n"
MF "v[108:111],a[164:167],a[240:243],v[108:111]\n"
".Lpg30h:\n"
"s_cbranch_execz .Lpg34h\n"
"s_add_u32 s87,s90,1024\n"
CV "v26,v28,v29\n"
CV "v27,v30,v31\n"
MF "v[108:111],a[204:207],a[244:247],v[108:111]\n"
MF "v[100:103],a[200:203],a[244:247],v[100:103]\n"
"buffer_store_dwordx4 v[24:27],v9,s[40:43],s87 offen sc1\n"
MF "v[100:103],a[160:163],a[240:243],v[100:103]\n"
MF "v[100:103],a[120:123],v[188:191],v[100:103]\n"
"s_waitcnt lgkmcnt(2)\n"
MF "v[136:139],a[120:123],v[176:179],v[136:139]\n"
"s_waitcnt lgkmcnt(0)\n"
TR "v[188:189],v3\n"
"v_add_f32 v32,v32,v192\n"
MF "v[136:139],a[160:163],v[180:183],v[136:139]\n"
TR "v[190:191],v3 offset:2048\n"
"v_add_f32 v33,v33,v193\n"
MF "v[136:139],a[200:203],v[184:187],v[136:139]\n"
"ds_read2st64_b32 v[200:201],v13 offset0:216 offset1:217\n"
"v_add_f32 v34,v34,v194\n"
MF "v[144:147],a[204:207],v[184:187],v[144:147]\n"
TR "a[240:241],v3 offset:4096\n"
"v_add_f32 v35,v35,v195\n"
MF "v[144:147],a[164:167],v[180:183],v[144:147]\n"
TR "a[242:243],v3 offset:6144\n"
"v_add_f32 v36,v36,v196\n"
MF "v[144:147],a[124:127],v[176:179],v[144:147]\n"
"ds_read2st64_b32 v[202:203],v13 offset0:218 offset1:219\n"
"v_add_f32 v37,v37,v197\n"
MF "v[152:155],a[128:131],v[176:179],v[152:155]\n"
TR "a[244:245],v3 offset:8192\n"
"v_add_f32 v38,v38,v198\n"
MF "v[152:155],a[168:171],v[180:183],v[152:155]\n"
TR "a[246:247],v3 offset:10240\n"
"v_add_f32 v39,v39,v199\n"
MF "v[152:155],a[208:211],v[184:187],v[152:155]\n"
"ds_read2st64_b32 v[204:205],v13 offset0:220 offset1:221\n"
CV "v32,v32,v33\n"
MF "v[160:163],a[212:215],v[184:187],v[160:163]\n"
"ds_read2st64_b32 v[206:207],v13 offset0:222 offset1:223\n"
CV "v33,v34,v35\n"
MF "v[160:163],a[172:175],v[180:183],v[160:163]\n"
"s_add_u32 s87,s90,2048\n"
CV "v34,v36,v37\n"
CV "v35,v38,v39\n"
MF "v[160:163],a[132:135],v[176:179],v[160:163]\n"
MF "v[168:171],a[136:139],v[176:179],v[168:171]\n"
"buffer_store_dwordx4 v[32:35],v9,s[40:43],s87 offen sc1\n"
MF "v[168:171],a[176:179],v[180:183],v[168:171]\n"
"s_waitcnt lgkmcnt(0)\n"
MF "v[168:171],a[216:219],v[184:187],v[168:171]\n"
"s_barrier\n"
MF "v[172:175],a[216:219],a[244:247],v[172:175]\n"
TR "v[176:177],v0 offset:49152\n"
"v_add_f32 v40,v40,v200\n"
MF "v[172:175],a[176:179],a[240:243],v[172:175]\n"
TR "v[178:179],v0 offset:51200\n"
"v_add_f32 v41,v41,v201\n"
MF "v[172:175],a[136:139],v[188:191],v[172:175]\n"
"ds_read2st64_b32 v[192:193],v13 offset0:224 offset1:225\n"
"v_add_f32 v42,v42,v202\n"
MF "v[164:167],a[132:135],v[188:191],v[164:167]\n"
TR "v[180:181],v0 offset:53248\n"
"v_add_f32 v43,v43,v203\n"
MF "v[164:167],a[172:175],a[240:243],v[164:167]\n"
TR "v[182:183],v0 offset:55296\n"
"v_add_f32 v44,v44,v204\n"
MF "v[164:167],a[212:215],a[244:247],v[164:167]\n"
"ds_read2st64_b32 v[194:195],v13 offset0:226 offset1:227\n"
"v_add_f32 v45,v45,v205\n"
MF "v[156:159],a[208:211],a[244:247],v[156:159]\n"
TR "v[184:185],v0 offset:57344\n"
"v_add_f32 v46,v46,v206\n"
MF "v[156:159],a[168:171],a[240:243],v[156:159]\n"
TR "v[186:187],v0 offset:59392\n"
"v_add_f32 v47,v47,v207\n"
MF "v[156:159],a[128:131],v[188:191],v[156:159]\n"
"ds_read2st64_b32 v[196:197],v13 offset0:228 offset1:229\n"
CV "v40,v40,v41\n"
MF "v[148:151],a[124:127],v[188:191],v[148:151]\n"
"ds_read2st64_b32 v[198:199],v13 offset0:230 offset1:231\n"
CV "v41,v42,v43\n"
MF "v[148:151],a[164:167],a[240:243],v[148:151]\n"
"s_add_u32 s87,s90,3072\n"
CV "v42,v44,v45\n"
CV "v43,v46,v47\n"
MF "v[148:151],a[204:207],a[244:247],v[148:151]\n"
MF "v[140:143],a[200:203],a[244:247],v[140:143]\n"
"buffer_store_dwordx4 v[40:43],v9,s[40:43],s87 offen sc1\n"
MF "v[140:143],a[160:163],a[240:243],v[140:143]\n"
MF "v[140:143],a[120:123],v[188:191],v[140:143]\n"
"s_waitcnt lgkmcnt(2)\n"
MF "v[96:99],a[20:23],v[176:179],v[96:99]\n"
"s_waitcnt lgkmcnt(0)\n"
TR "v[188:189],v1 offset:49152\n"
"v_add_f32 v48,v48,v192\n"
MF "v[96:99],a[60:63],v[180:183],v[96:99]\n"
TR "v[190:191],v1 offset:51200\n"
"v_add_f32 v49,v49,v193\n"
MF "v[96:99],a[100:103],v[184:187],v[96:99]\n"
"ds_read2st64_b32 v[200:201],v13 offset0:0 offset1:1\n"
"v_add_f32 v50,v50,v194\n"
MF "v[104:107],a[104:107],v[184:187],v[104:107]\n"
TR "a[240:241],v1 offset:53248\n"
"v_add_f32 v51,v51,v195\n"
MF "v[104:107],a[64:67],v[180:183],v[104:107]\n"
TR "a[242:243],v1 offset:55296\n"
"v_add_f32 v52,v52,v196\n"
MF "v[104:107],a[24:27],v[176:179],v[104:107]\n"
"ds_read2st64_b32 v[202:203],v13 offset0:2 offset1:3\n"
"v_add_f32 v53,v53,v197\n"
MF "v[112:115],a[28:31],v[176:179],v[112:115]\n"
TR "a[244:245],v1 offset:57344\n"
"v_add_f32 v54,v54,v198\n"
MF "v[112:115],a[68:71],v[180:183],v[112:115]\n"
TR "a[246:247],v1 offset:59392\n"
"v_add_f32 v55,v55,v199\n"
MF "v[112:115],a[108:111],v[184:187],v[112:115]\n"
"ds_read2st64_b32 v[204:205],v13 offset0:4 offset1:5\n"
CV "v48,v48,v49\n"
MF "v[120:123],a[112:115],v[184:187],v[120:123]\n"
"ds_read2st64_b32 v[206:207],v13 offset0:6 offset1:7\n"
CV "v49,v50,v51\n"
MF "v[120:123],a[72:75],v[180:183],v[120:123]\n"
"s_add_u32 s87,s90,4096\n"
CV "v50,v52,v53\n"
CV "v51,v54,v55\n"
MF "v[120:123],a[32:35],v[176:179],v[120:123]\n"
MF "v[128:131],a[36:39],v[176:179],v[128:131]\n"
"buffer_store_dwordx4 v[48:51],v9,s[40:43],s87 offen sc1\n"
MF "v[128:131],a[76:79],v[180:183],v[128:131]\n"
MF "v[128:131],a[116:119],v[184:187],v[128:131]\n"
"s_waitcnt lgkmcnt(2)\n"
MF "v[132:135],a[116:119],a[244:247],v[132:135]\n"
"s_waitcnt lgkmcnt(0)\n"
TR "v[176:177],v2 offset:49152\n"
"v_add_f32 v56,v56,v200\n"
MF "v[132:135],a[76:79],a[240:243],v[132:135]\n"
TR "v[178:179],v2 offset:51200\n"
"v_add_f32 v57,v57,v201\n"
MF "v[132:135],a[36:39],v[188:191],v[132:135]\n"
"ds_read2st64_b32 v[192:193],v13 offset0:8 offset1:9\n"
"v_add_f32 v58,v58,v202\n"
MF "v[124:127],a[32:35],v[188:191],v[124:127]\n"
TR "v[180:181],v2 offset:53248\n"
"v_add_f32 v59,v59,v203\n"
MF "v[124:127],a[72:75],a[240:243],v[124:127]\n"
TR "v[182:183],v2 offset:55296\n"
"v_add_f32 v60,v60,v204\n"
MF "v[124:127],a[112:115],a[244:247],v[124:127]\n"
"ds_read2st64_b32 v[194:195],v13 offset0:10 offset1:11\n"
"v_add_f32 v61,v61,v205\n"
MF "v[116:119],a[108:111],a[244:247],v[116:119]\n"
TR "v[184:185],v2 offset:57344\n"
"v_add_f32 v62,v62,v206\n"
MF "v[116:119],a[68:71],a[240:243],v[116:119]\n"
TR "v[186:187],v2 offset:59392\n"
"v_add_f32 v63,v63,v207\n"
MF "v[116:119],a[28:31],v[188:191],v[116:119]\n"
"ds_read2st64_b32 v[196:197],v13 offset0:12 offset1:13\n"
CV "v56,v56,v57\n"
MF "v[108:111],a[24:27],v[188:191],v[108:111]\n"
"ds_read2st64_b32 v[198:199],v13 offset0:14 offset1:15\n"
CV "v57,v58,v59\n"
MF "v[108:111],a[64:67],a[240:243],v[108:111]\n"
"s_add_u32 s87,s90,10240\n"
CV "v58,v60,v61\n"
CV "v59,v62,v63\n"
MF "v[108:111],a[104:107],a[244:247],v[108:111]\n"
MF "v[100:103],a[100:103],a[244:247],v[100:103]\n"
"buffer_store_dwordx4 v[56:59],v9,s[40:43],s87 offen sc1\n"
MF "v[100:103],a[60:63],a[240:243],v[100:103]\n"
MF "v[100:103],a[20:23],v[188:191],v[100:103]\n"
"s_waitcnt lgkmcnt(2)\n"
MF "v[136:139],a[20:23],v[176:179],v[136:139]\n"
"s_waitcnt lgkmcnt(0)\n"
TR "v[188:189],v3 offset:49152\n"
"v_add_f32 v64,v64,v192\n"
MF "v[136:139],a[60:63],v[180:183],v[136:139]\n"
TR "v[190:191],v3 offset:51200\n"
"v_add_f32 v65,v65,v193\n"
MF "v[136:139],a[100:103],v[184:187],v[136:139]\n"
"ds_read2st64_b32 v[200:201],v13 offset0:16 offset1:17\n"
"v_add_f32 v66,v66,v194\n"
MF "v[144:147],a[104:107],v[184:187],v[144:147]\n"
TR "a[240:241],v3 offset:53248\n"
"v_add_f32 v67,v67,v195\n"
MF "v[144:147],a[64:67],v[180:183],v[144:147]\n"
TR "a[242:243],v3 offset:55296\n"
"v_add_f32 v68,v68,v196\n"
MF "v[144:147],a[24:27],v[176:179],v[144:147]\n"
"ds_read2st64_b32 v[202:203],v13 offset0:18 offset1:19\n"
"v_add_f32 v69,v69,v197\n"
MF "v[152:155],a[28:31],v[176:179],v[152:155]\n"
TR "a[244:245],v3 offset:57344\n"
"v_add_f32 v70,v70,v198\n"
MF "v[152:155],a[68:71],v[180:183],v[152:155]\n"
TR "a[246:247],v3 offset:59392\n"
"v_add_f32 v71,v71,v199\n"
MF "v[152:155],a[108:111],v[184:187],v[152:155]\n"
"ds_read2st64_b32 v[204:205],v13 offset0:20 offset1:21\n"
CV "v64,v64,v65\n"
MF "v[160:163],a[112:115],v[184:187],v[160:163]\n"
"ds_read2st64_b32 v[206:207],v13 offset0:22 offset1:23\n"
CV "v65,v66,v67\n"
MF "v[160:163],a[72:75],v[180:183],v[160:163]\n"
"s_add_u32 s87,s90,11264\n"
CV "v66,v68,v69\n"
CV "v67,v70,v71\n"
MF "v[160:163],a[32:35],v[176:179],v[160:163]\n"
MF "v[168:171],a[36:39],v[176:179],v[168:171]\n"
"buffer_store_dwordx4 v[64:67],v9,s[40:43],s87 offen sc1\n"
MF "v[168:171],a[76:79],v[180:183],v[168:171]\n"
"s_waitcnt lgkmcnt(0)\n"
MF "v[168:171],a[116:119],v[184:187],v[168:171]\n"
"s_barrier\n"
MF "v[172:175],a[116:119],a[244:247],v[172:175]\n"
TR "v[176:177],v4\n"
"v_add_f32 v72,v72,v200\n"
MF "v[172:175],a[76:79],a[240:243],v[172:175]\n"
TR "v[178:179],v4 offset:2048\n"
"v_add_f32 v73,v73,v201\n"
MF "v[172:175],a[36:39],v[188:191],v[172:175]\n"
"ds_read2st64_b32 v[192:193],v13 offset0:24 offset1:25\n"
"v_add_f32 v74,v74,v202\n"
MF "v[164:167],a[32:35],v[188:191],v[164:167]\n"
TR "v[180:181],v4 offset:4096\n"
"v_add_f32 v75,v75,v203\n"
MF "v[164:167],a[72:75],a[240:243],v[164:167]\n"
TR "v[182:183],v4 offset:6144\n"
"v_add_f32 v76,v76,v204\n"
MF "v[164:167],a[112:115],a[244:247],v[164:167]\n"
"ds_read2st64_b32 v[194:195],v13 offset0:26 offset1:27\n"
"v_add_f32 v77,v77,v205\n"
MF "v[156:159],a[108:111],a[244:247],v[156:159]\n"
TR "v[184:185],v4 offset:8192\n"
"v_add_f32 v78,v78,v206\n"
MF "v[156:159],a[68:71],a[240:243],v[156:159]\n"
TR "v[186:187],v4 offset:10240\n"
"v_add_f32 v79,v79,v207\n"
MF "v[156:159],a[28:31],v[188:191],v[156:159]\n"
"ds_read2st64_b32 v[196:197],v13 offset0:28 offset1:29\n"
CV "v72,v72,v73\n"
MF "v[148:151],a[24:27],v[188:191],v[148:151]\n"
"ds_read2st64_b32 v[198:199],v13 offset0:30 offset1:31\n"
CV "v73,v74,v75\n"
MF "v[148:151],a[64:67],a[240:243],v[148:151]\n"
"s_add_u32 s87,s90,12288\n"
CV "v74,v76,v77\n"
CV "v75,v78,v79\n"
MF "v[148:151],a[104:107],a[244:247],v[148:151]\n"
MF "v[140:143],a[100:103],a[244:247],v[140:143]\n"
"buffer_store_dwordx4 v[72:75],v9,s[40:43],s87 offen sc1\n"
MF "v[140:143],a[60:63],a[240:243],v[140:143]\n"
MF "v[140:143],a[20:23],v[188:191],v[140:143]\n"
"s_waitcnt lgkmcnt(2)\n"
MF "v[96:99],a[140:143],v[176:179],v[96:99]\n"
"s_waitcnt lgkmcnt(0)\n"
TR "v[188:189],v5\n"
"v_add_f32 v80,v80,v192\n"
MF "v[96:99],a[180:183],v[180:183],v[96:99]\n"
TR "v[190:191],v5 offset:2048\n"
"v_add_f32 v81,v81,v193\n"
MF "v[96:99],a[220:223],v[184:187],v[96:99]\n"
"ds_read2st64_b32 v[200:201],v13 offset0:32 offset1:33\n"
"v_add_f32 v82,v82,v194\n"
MF "v[104:107],a[224:227],v[184:187],v[104:107]\n"
TR "a[240:241],v5 offset:4096\n"
"v_add_f32 v83,v83,v195\n"
MF "v[104:107],a[184:187],v[180:183],v[104:107]\n"
TR "a[242:243],v5 offset:6144\n"
"v_add_f32 v84,v84,v196\n"
MF "v[104:107],a[144:147],v[176:179],v[104:107]\n"
"ds_read2st64_b32 v[202:203],v13 offset0:34 offset1:35\n"
"v_add_f32 v85,v85,v197\n"
MF "v[112:115],a[148:151],v[176:179],v[112:115]\n"
TR "a[244:245],v5 offset:8192\n"
"v_add_f32 v86,v86,v198\n"
MF "v[112:115],a[188:191],v[180:183],v[112:115]\n"
TR "a[246:247],v5 offset:10240\n"
"v_add_f32 v87,v87,v199\n"
MF "v[112:115],a[228:231],v[184:187],v[112:115]\n"
"ds_read2st64_b32 v[204:205],v13 offset0:36 offset1:37\n"
CV "v80,v80,v81\n"
MF "v[120:123],a[232:235],v[184:187],v[120:123]\n"
"ds_read2st64_b32 v[206:207],v13 offset0:38 offset1:39\n"
CV "v81,v82,v83\n"
MF "v[120:123],a[192:195],v[180:183],v[120:123]\n"
"s_add_u32 s87,s90,13312\n"
CV "v82,v84,v85\n"
CV "v83,v86,v87\n"
MF "v[120:123],a[152:155],v[176:179],v[120:123]\n"
MF "v[128:131],a[156:159],v[176:179],v[128:131]\n"
"buffer_store_dwordx4 v[80:83],v9,s[40:43],s87 offen sc1\n"
MF "v[128:131],a[196:199],v[180:183],v[128:131]\n"
MF "v[128:131],a[236:239],v[184:187],v[128:131]\n"
"s_waitcnt lgkmcnt(2)\n"
MF "v[132:135],a[236:239],a[244:247],v[132:135]\n"
"s_waitcnt lgkmcnt(0)\n"
TR "v[176:177],v6\n"
"v_add_f32 v88,v88,v200\n"
MF "v[132:135],a[196:199],a[240:243],v[132:135]\n"
TR "v[178:179],v6 offset:2048\n"
"v_add_f32 v89,v89,v201\n"
MF "v[132:135],a[156:159],v[188:191],v[132:135]\n"
"v_add_f32 v90,v90,v202\n"
"v_add_f32 v91,v91,v203\n"
MF "v[124:127],a[152:155],v[188:191],v[124:127]\n"
TR "v[180:181],v6 offset:4096\n"
"v_add_f32 v92,v92,v204\n"
MF "v[124:127],a[192:195],a[240:243],v[124:127]\n"
TR "v[182:183],v6 offset:6144\n"
"v_add_f32 v93,v93,v205\n"
MF "v[124:127],a[232:235],a[244:247],v[124:127]\n"
"v_add_f32 v94,v94,v206\n"
"v_add_f32 v95,v95,v207\n"
MF "v[116:119],a[228:231],a[244:247],v[116:119]\n"
TR "v[184:185],v6 offset:8192\n"
CV "v88,v88,v89\n"
MF "v[116:119],a[188:191],a[240:243],v[116:119]\n"
TR "v[186:187],v6 offset:10240\n"
CV "v89,v90,v91\n"
MF "v[116:119],a[148:151],v[188:191],v[116:119]\n"
"s_add_u32 s87,s90,14336\n"
CV "v90,v92,v93\n"
CV "v91,v94,v95\n"
MF "v[108:111],a[144:147],v[188:191],v[108:111]\n"
MF "v[108:111],a[184:187],a[240:243],v[108:111]\n"
MF "v[108:111],a[224:227],a[244:247],v[108:111]\n"
MF "v[100:103],a[220:223],a[244:247],v[100:103]\n"
"buffer_store_dwordx4 v[88:91],v9,s[40:43],s87 offen sc1\n"
MF "v[100:103],a[180:183],a[240:243],v[100:103]\n"
"s_mov_b32 s90,s91\n"
"s_add_u32 s91,s91,20480\n"
MF "v[100:103],a[140:143],v[188:191],v[100:103]\n"
"s_waitcnt lgkmcnt(0)\n"
MF "v[136:139],a[140:143],v[176:179],v[136:139]\n"
TR "v[188:189],v7\n"
MF "v[136:139],a[180:183],v[180:183],v[136:139]\n"
TR "v[190:191],v7 offset:2048\n"
MF "v[136:139],a[220:223],v[184:187],v[136:139]\n"
MF "v[144:147],a[224:227],v[184:187],v[144:147]\n"
TR "a[240:241],v7 offset:4096\n"
MF "v[144:147],a[184:187],v[180:183],v[144:147]\n"
TR "a[242:243],v7 offset:6144\n"
MF "v[144:147],a[144:147],v[176:179],v[144:147]\n"
MF "v[152:155],a[148:151],v[176:179],v[152:155]\n"
TR "a[244:245],v7 offset:8192\n"
MF "v[152:155],a[188:191],v[180:183],v[152:155]\n"
TR "a[246:247],v7 offset:10240\n"
MF "v[152:155],a[228:231],v[184:187],v[152:155]\n"
MF "v[160:163],a[232:235],v[184:187],v[160:163]\n"
MF "v[160:163],a[192:195],v[180:183],v[160:163]\n"
MF "v[160:163],a[152:155],v[176:179],v[160:163]\n"
MF "v[168:171],a[156:159],v[176:179],v[168:171]\n"
MF "v[168:171],a[196:199],v[180:183],v[168:171]\n"
"s_waitcnt lgkmcnt(0)\n"
MF "v[168:171],a[236:239],v[184:187],v[168:171]\n"
"s_barrier\n"
MF "v[172:175],a[236:239],a[244:247],v[172:175]\n"
TR "v[176:177],v0 offset:24576\n"
MF "v[172:175],a[196:199],a[240:243],v[172:175]\n"
"s_add_u32 s85,s85,1\n"
TR "v[178:179],v0 offset:26624\n"
MF "v[172:175],a[156:159],v[188:191],v[172:175]\n"
MF "v[164:167],a[152:155],v[188:191],v[164:167]\n"
TR "v[180:181],v0 offset:28672\n"
MF "v[164:167],a[192:195],a[240:243],v[164:167]\n"
TR "v[182:183],v0 offset:30720\n"
MF "v[164:167],a[232:235],a[244:247],v[164:167]\n"
MF "v[156:159],a[228:231],a[244:247],v[156:159]\n"
TR "v[184:185],v0 offset:32768\n"
MF "v[156:159],a[188:191],a[240:243],v[156:159]\n"
TR "v[186:187],v0 offset:34816\n"
MF "v[156:159],a[148:151],v[188:191],v[156:159]\n"
MF "v[148:151],a[144:147],v[188:191],v[148:151]\n"
MF "v[148:151],a[184:187],a[240:243],v[148:151]\n"
MF "v[148:151],a[224:227],a[244:247],v[148:151]\n"
MF "v[140:143],a[220:223],a[244:247],v[140:143]\n"
MF "v[140:143],a[180:183],a[240:243],v[140:143]\n"
MF "v[140:143],a[140:143],v[188:191],v[140:143]\n"
"s_waitcnt lgkmcnt(0)\n"
MF "v[16:19],a[0:3],v[176:179],v[252:255]\n"
TR "v[188:189],v1 offset:24576\n"
MF "v[16:19],a[40:43],v[180:183],v[16:19]\n"
TR "v[190:191],v1 offset:26624\n"
MF "v[16:19],a[80:83],v[184:187],v[16:19]\n"
MF "v[24:27],a[84:87],v[184:187],v[252:255]\n"
TR "a[240:241],v1 offset:28672\n"
MF "v[24:27],a[44:47],v[180:183],v[24:27]\n"
TR "a[242:243],v1 offset:30720\n"
MF "v[24:27],a[4:7],v[176:179],v[24:27]\n"
MF "v[32:35],a[8:11],v[176:179],v[252:255]\n"
TR "a[244:245],v1 offset:32768\n"
MF "v[32:35],a[48:51],v[180:183],v[32:35]\n"
TR "a[246:247],v1 offset:34816\n"
MF "v[32:35],a[88:91],v[184:187],v[32:35]\n"
MF "v[40:43],a[92:95],v[184:187],v[252:255]\n"
MF "v[40:43],a[52:55],v[180:183],v[40:43]\n"
MF "v[40:43],a[12:15],v[176:179],v[40:43]\n"
MF "v[48:51],a[16:19],v[176:179],v[252:255]\n"
MF "v[48:51],a[56:59],v[180:183],v[48:51]\n"
MF "v[48:51],a[96:99],v[184:187],v[48:51]\n"
"s_waitcnt lgkmcnt(0)\n"
MF "v[52:55],a[96:99],a[244:247],v[252:255]\n"
TR "v[176:177],v2 offset:24576\n"
MF "v[52:55],a[56:59],a[240:243],v[52:55]\n"
TR "v[178:179],v2 offset:26624\n"
MF "v[52:55],a[16:19],v[188:191],v[52:55]\n"
MF "v[44:47],a[12:15],v[188:191],v[252:255]\n"
TR "v[180:181],v2 offset:28672\n"
MF "v[44:47],a[52:55],a[240:243],v[44:47]\n"
TR "v[182:183],v2 offset:30720\n"
MF "v[44:47],a[92:95],a[244:247],v[44:47]\n"
MF "v[36:39],a[88:91],a[244:247],v[252:255]\n"
TR "v[184:185],v2 offset:32768\n"
MF "v[36:39],a[48:51],a[240:243],v[36:39]\n"
TR "v[186:187],v2 offset:34816\n"
MF "v[36:39],a[8:11],v[188:191],v[36:39]\n"
MF "v[28:31],a[4:7],v[188:191],v[252:255]\n"
MF "v[28:31],a[44:47],a[240:243],v[28:31]\n"
MF "v[28:31],a[84:87],a[244:247],v[28:31]\n"
MF "v[20:23],a[80:83],a[244:247],v[252:255]\n"
MF "v[20:23],a[40:43],a[240:243],v[20:23]\n"
MF "v[20:23],a[0:3],v[188:191],v[20:23]\n"
"s_waitcnt lgkmcnt(0)\n"
MF "v[56:59],a[0:3],v[176:179],v[252:255]\n"
TR "v[188:189],v3 offset:24576\n"
MF "v[56:59],a[40:43],v[180:183],v[56:59]\n"
TR "v[190:191],v3 offset:26624\n"
MF "v[56:59],a[80:83],v[184:187],v[56:59]\n"
MF "v[64:67],a[84:87],v[184:187],v[252:255]\n"
TR "a[240:241],v3 offset:28672\n"
MF "v[64:67],a[44:47],v[180:183],v[64:67]\n"
TR "a[242:243],v3 offset:30720\n"
MF "v[64:67],a[4:7],v[176:179],v[64:67]\n"
MF "v[72:75],a[8:11],v[176:179],v[252:255]\n"
TR "a[244:245],v3 offset:32768\n"
MF "v[72:75],a[48:51],v[180:183],v[72:75]\n"
TR "a[246:247],v3 offset:34816\n"
MF "v[72:75],a[88:91],v[184:187],v[72:75]\n"
MF "v[80:83],a[92:95],v[184:187],v[252:255]\n"
MF "v[80:83],a[52:55],v[180:183],v[80:83]\n"
MF "v[80:83],a[12:15],v[176:179],v[80:83]\n"
MF "v[88:91],a[16:19],v[176:179],v[252:255]\n"
MF "v[88:91],a[56:59],v[180:183],v[88:91]\n"
"s_waitcnt lgkmcnt(0)\n"
MF "v[88:91],a[96:99],v[184:187],v[88:91]\n"
"s_barrier\n"
MF "v[92:95],a[96:99],a[244:247],v[252:255]\n"
TR "v[176:177],v0\n"
MF "v[92:95],a[56:59],a[240:243],v[92:95]\n"
TR "v[178:179],v0 offset:2048\n"
MF "v[92:95],a[16:19],v[188:191],v[92:95]\n"
"ds_read2st64_b32 v[192:193],v13 offset0:192 offset1:193\n"
MF "v[84:87],a[12:15],v[188:191],v[252:255]\n"
TR "v[180:181],v0 offset:4096\n"
MF "v[84:87],a[52:55],a[240:243],v[84:87]\n"
TR "v[182:183],v0 offset:6144\n"
MF "v[84:87],a[92:95],a[244:247],v[84:87]\n"
"ds_read2st64_b32 v[194:195],v13 offset0:194 offset1:195\n"
MF "v[76:79],a[88:91],a[244:247],v[252:255]\n"
TR "v[184:185],v0 offset:8192\n"
MF "v[76:79],a[48:51],a[240:243],v[76:79]\n"
TR "v[186:187],v0 offset:10240\n"
MF "v[76:79],a[8:11],v[188:191],v[76:79]\n"
"ds_read2st64_b32 v[196:197],v13 offset0:196 offset1:197\n"
MF "v[68:71],a[4:7],v[188:191],v[252:255]\n"
"ds_read2st64_b32 v[198:199],v13 offset0:198 offset1:199\n"
MF "v[68:71],a[44:47],a[240:243],v[68:71]\n"
MF "v[68:71],a[84:87],a[244:247],v[68:71]\n"
MF "v[60:63],a[80:83],a[244:247],v[252:255]\n"
MF "v[60:63],a[40:43],a[240:243],v[60:63]\n"
MF "v[60:63],a[0:3],v[188:191],v[60:63]\n"
"s_waitcnt lgkmcnt(2)\n"
MF "v[16:19],a[120:123],v[176:179],v[16:19]\n"
"s_waitcnt lgkmcnt(0)\n"
TR "v[188:189],v1\n"
"v_add_f32 v96,v96,v192\n"
MF "v[16:19],a[160:163],v[180:183],v[16:19]\n"
TR "v[190:191],v1 offset:2048\n"
"v_add_f32 v97,v97,v193\n"
MF "v[16:19],a[200:203],v[184:187],v[16:19]\n"
"ds_read2st64_b32 v[200:201],v13 offset0:200 offset1:201\n"
"v_add_f32 v98,v98,v194\n"
"s_waitcnt vmcnt(0)\n"
"v_mov_b32 v14,s76\n"
"s_add_u32 s76,s76,1\n"
MF "v[24:27],a[204:207],v[184:187],v[24:27]\n"
TR "a[240:241],v1 offset:4096\n"
"v_add_f32 v99,v99,v195\n"
"buffer_store_dword v14,v15,s[40:43],s94 offen sc1\n"
MF "v[24:27],a[164:167],v[180:183],v[24:27]\n"
TR "a[242:243],v1 offset:6144\n"
"v_add_f32 v100,v100,v196\n"
MF "v[24:27],a[124:127],v[176:179],v[24:27]\n"
"ds_read2st64_b32 v[202:203],v13 offset0:202 offset1:203\n"
"v_add_f32 v101,v101,v197\n"
MF "v[32:35],a[128:131],v[176:179],v[32:35]\n"
TR "a[244:245],v1 offset:8192\n"
"v_add_f32 v102,v102,v198\n"
MF "v[32:35],a[168:171],v[180:183],v[32:35]\n"
TR "a[246:247],v1 offset:10240\n"
"v_add_f32 v103,v103,v199\n"
MF "v[32:35],a[208:211],v[184:187],v[32:35]\n"
"ds_read2st64_b32 v[204:205],v13 offset0:204 offset1:205\n"
CV "v96,v96,v97\n"
MF "v[40:43],a[212:215],v[184:187],v[40:43]\n"
"ds_read2st64_b32 v[206:207],v13 offset0:206 offset1:207\n"
CV "v97,v98,v99\n"
MF "v[40:43],a[172:175],v[180:183],v[40:43]\n"
"s_add_u32 s87,s90,0\n"
CV "v98,v100,v101\n"
CV "v99,v102,v103\n"
MF "v[40:43],a[132:135],v[176:179],v[40:43]\n"
MF "v[48:51],a[136:139],v[176:179],v[48:51]\n"
"buffer_store_dwordx4 v[96:99],v9,s[40:43],s87 offen sc1\n"
MF "v[48:51],a[176:179],v[180:183],v[48:51]\n"
MF "v[48:51],a[216:219],v[184:187],v[48:51]\n"
"s_waitcnt lgkmcnt(2)\n"
MF "v[52:55],a[216:219],a[244:247],v[52:55]\n"
"s_waitcnt lgkmcnt(0)\n"
TR "v[176:177],v2\n"
"v_add_f32 v104,v104,v200\n"
MF "v[52:55],a[176:179],a[240:243],v[52:55]\n"
TR "v[178:179],v2 offset:2048\n"
"v_add_f32 v105,v105,v201\n"
MF "v[52:55],a[136:139],v[188:191],v[52:55]\n"
"ds_read2st64_b32 v[192:193],v13 offset0:208 offset1:209\n"
"v_add_f32 v106,v106,v202\n"
MF "v[44:47],a[132:135],v[188:191],v[44:47]\n"
TR "v[180:181],v2 offset:4096\n"
"v_add_f32 v107,v107,v203\n"
MF "v[44:47],a[172:175],a[240:243],v[44:47]\n"
TR "v[182:183],v2 offset:6144\n"
"v_add_f32 v108,v108,v204\n"
MF "v[44:47],a[212:215],a[244:247],v[44:47]\n"
"ds_read2st64_b32 v[194:195],v13 offset0:210 offset1:211\n"
"v_add_f32 v109,v109,v205\n"
MF "v[36:39],a[208:211],a[244:247],v[36:39]\n"
TR "v[184:185],v2 offset:8192\n"
"v_add_f32 v110,v110,v206\n"
MF "v[36:39],a[168:171],a[240:243],v[36:39]\n"
TR "v[186:187],v2 offset:10240\n"
"v_add_f32 v111,v111,v207\n"
MF "v[36:39],a[128:131],v[188:191],v[36:39]\n"
"ds_read2st64_b32 v[196:197],v13 offset0:212 offset1:213\n"
CV "v104,v104,v105\n"
MF "v[28:31],a[124:127],v[188:191],v[28:31]\n"
"ds_read2st64_b32 v[198:199],v13 offset0:214 offset1:215\n"
CV "v105,v106,v107\n"
MF "v[28:31],a[164:167],a[240:243],v[28:31]\n"
"s_add_u32 s87,s90,1024\n"
CV "v106,v108,v109\n"
CV "v107,v110,v111\n"
MF "v[28:31],a[204:207],a[244:247],v[28:31]\n"
MF "v[20:23],a[200:203],a[244:247],v[20:23]\n"
"buffer_store_dwordx4 v[104:107],v9,s[40:43],s87 offen sc1\n"
MF "v[20:23],a[160:163],a[240:243],v[20:23]\n"
MF "v[20:23],a[120:123],v[188:191],v[20:23]\n"
"s_waitcnt lgkmcnt(2)\n"
MF "v[56:59],a[120:123],v[176:179],v[56:59]\n"
"s_waitcnt lgkmcnt(0)\n"
TR "v[188:189],v3\n"
"v_add_f32 v112,v112,v192\n"
MF "v[56:59],a[160:163],v[180:183],v[56:59]\n"
TR "v[190:191],v3 offset:2048\n"
"v_add_f32 v113,v113,v193\n"
MF "v[56:59],a[200:203],v[184:187],v[56:59]\n"
"ds_read2st64_b32 v[200:201],v13 offset0:216 offset1:217\n"
"v_add_f32 v114,v114,v194\n"
MF "v[64:67],a[204:207],v[184:187],v[64:67]\n"
".Lpg31h:\n"
"s_cbranch_execz .Lpg35h\n"
TR "a[240:241],v3 offset:4096\n"
"v_add_f32 v115,v115,v195\n"
MF "v[64:67],a[164:167],v[180:183],v[64:67]\n"
TR "a[242:243],v3 offset:6144\n"
"v_add_f32 v116,v116,v196\n"
MF "v[64:67],a[124:127],v[176:179],v[64:67]\n"
"ds_read2st64_b32 v[202:203],v13 offset0:218 offset1:219\n"
"v_add_f32 v117,v117,v197\n"
MF "v[72:75],a[128:131],v[176:179],v[72:75]\n"
TR "a[244:245],v3 offset:8192\n"
"v_add_f32 v118,v118,v198\n"
MF "v[72:75],a[168:171],v[180:183],v[72:75]\n"
TR "a[246:247],v3 offset:10240\n"
"v_add_f32 v119,v119,v199\n"
MF "v[72:75],a[208:211],v[184:187],v[72:75]\n"
"ds_read2st64_b32 v[204:205],v13 offset0:220 offset1:221\n"
CV "v112,v112,v113\n"
MF "v[80:83],a[212:215],v[184:187],v[80:83]\n"
"ds_read2st64_b32 v[206:207],v13 offset0:222 offset1:223\n"
CV "v113,v114,v115\n"
MF "v[80:83],a[172:175],v[180:183],v[80:83]\n"
"s_add_u32 s87,s90,2048\n"
CV "v114,v116,v117\n"
CV "v115,v118,v119\n"
MF "v[80:83],a[132:135],v[176:179],v[80:83]\n"
MF "v[88:91],a[136:139],v[176:179],v[88:91]\n"
"buffer_store_dwordx4 v[112:115],v9,s[40:43],s87 offen sc1\n"
MF "v[88:91],a[176:179],v[180:183],v[88:91]\n"
"s_waitcnt lgkmcnt(0)\n"
MF "v[88:91],a[216:219],v[184:187],v[88:91]\n"
"s_barrier\n"
MF "v[92:95],a[216:219],a[244:247],v[92:95]\n"
TR "v[176:177],v0 offset:49152\n"
"v_add_f32 v120,v120,v200\n"
MF "v[92:95],a[176:179],a[240:243],v[92:95]\n"
TR "v[178:179],v0 offset:51200\n"
"v_add_f32 v121,v121,v201\n"
MF "v[92:95],a[136:139],v[188:191],v[92:95]\n"
"ds_read2st64_b32 v[192:193],v13 offset0:224 offset1:225\n"
"v_add_f32 v122,v122,v202\n"
MF "v[84:87],a[132:135],v[188:191],v[84:87]\n"
TR "v[180:181],v0 offset:53248\n"
"v_add_f32 v123,v123,v203\n"
MF "v[84:87],a[172:175],a[240:243],v[84:87]\n"
TR "v[182:183],v0 offset:55296\n"
"v_add_f32 v124,v124,v204\n"
MF "v[84:87],a[212:215],a[244:247],v[84:87]\n"
"ds_read2st64_b32 v[194:195],v13 offset0:226 offset1:227\n"
"v_add_f32 v125,v125,v205\n"
MF "v[76:79],a[208:211],a[244:247],v[76:79]\n"
TR "v[184:185],v0 offset:57344\n"
"v_add_f32 v126,v126,v206\n"
MF "v[76:79],a[168:171],a[240:243],v[76:79]\n"
TR "v[186:187],v0 offset:59392\n"
"v_add_f32 v127,v127,v207\n"
MF "v[76:79],a[128:131],v[188:191],v[76:79]\n"
"ds_read2st64_b32 v[196:197],v13 offset0:228 offset1:229\n"
CV "v120,v120,v121\n"
MF "v[68:71],a[124:127],v[188:191],v[68:71]\n"
"ds_read2st64_b32 v[198:199],v13 offset0:230 offset1:231\n"
CV "v121,v122,v123\n"
MF "v[68:71],a[164:167],a[240:243],v[68:71]\n"
"s_add_u32 s87,s90,3072\n"
CV "v122,v124,v125\n"
CV "v123,v126,v127\n"
MF "v[68:71],a[204:207],a[244:247],v[68:71]\n"
MF "v[60:63],a[200:203],a[244:247],v[60:63]\n"
"buffer_store_dwordx4 v[120:123],v9,s[40:43],s87 offen sc1\n"
MF "v[60:63],a[160:163],a[240:243],v[60:63]\n"
MF "v[60:63],a[120:123],v[188:191],v[60:63]\n"
"s_waitcnt lgkmcnt(2)\n"
MF "v[16:19],a[20:23],v[176:179],v[16:19]\n"
"s_waitcnt lgkmcnt(0)\n"
TR "v[188:189],v1 offset:49152\n"
"v_add_f32 v128,v128,v192\n"
MF "v[16:19],a[60:63],v[180:183],v[16:19]\n"
TR "v[190:191],v1 offset:51200\n"
"v_add_f32 v129,v129,v193\n"
MF "v[16:19],a[100:103],v[184:187],v[16:19]\n"
"ds_read2st64_b32 v[200:201],v13 offset0:0 offset1:1\n"
"v_add_f32 v130,v130,v194\n"
MF "v[24:27],a[104:107],v[184:187],v[24:27]\n"
TR "a[240:241],v1 offset:53248\n"
"v_add_f32 v131,v131,v195\n"
MF "v[24:27],a[64:67],v[180:183],v[24:27]\n"
TR "a[242:243],v1 offset:55296\n"
"v_add_f32 v132,v132,v196\n"
MF "v[24:27],a[24:27],v[176:179],v[24:27]\n"
"ds_read2st64_b32 v[202:203],v13 offset0:2 offset1:3\n"
"v_add_f32 v133,v133,v197\n"
MF "v[32:35],a[28:31],v[176:179],v[32:35]\n"
TR "a[244:245],v1 offset:57344\n"
"v_add_f32 v134,v134,v198\n"
MF "v[32:35],a[68:71],v[180:183],v[32:35]\n"
TR "a[246:247],v1 offset:59392\n"
"v_add_f32 v135,v135,v199\n"
MF "v[32:35],a[108:111],v[184:187],v[32:35]\n"
"ds_read2st64_b32 v[204:205],v13 offset0:4 offset1:5\n"
CV "v128,v128,v129\n"
MF "v[40:43],a[112:115],v[184:187],v[40:43]\n"
"ds_read2st64_b32 v[206:207],v13 offset0:6 offset1:7\n"
CV "v129,v130,v131\n"
MF "v[40:43],a[72:75],v[180:183],v[40:43]\n"
"s_add_u32 s87,s90,4096\n"
CV "v130,v132,v133\n"
CV "v131,v134,v135\n"
MF "v[40:43],a[32:35],v[176:179],v[40:43]\n"
MF "v[48:51],a[36:39],v[176:179],v[48:51]\n"
"buffer_store_dwordx4 v[128:131],v9,s[40:43],s87 offen sc1\n"
MF "v[48:51],a[76:79],v[180:183],v[48:51]\n"
MF "v[48:51],a[116:119],v[184:187],v[48:51]\n"
"s_waitcnt lgkmcnt(2)\n"
MF "v[52:55],a[116:119],a[244:247],v[52:55]\n"
"s_waitcnt lgkmcnt(0)\n"
TR "v[176:177],v2 offset:49152\n"
"v_add_f32 v136,v136,v200\n"
MF "v[52:55],a[76:79],a[240:243],v[52:55]\n"
TR "v[178:179],v2 offset:51200\n"
"v_add_f32 v137,v137,v201\n"
MF "v[52:55],a[36:39],v[188:191],v[52:55]\n"
"ds_read2st64_b32 v[192:193],v13 offset0:8 offset1:9\n"
"v_add_f32 v138,v138,v202\n"
MF "v[44:47],a[32:35],v[188:191],v[44:47]\n"
TR "v[180:181],v2 offset:53248\n"
"v_add_f32 v139,v139,v203\n"
MF "v[44:47],a[72:75],a[240:243],v[44:47]\n"
TR "v[182:183],v2 offset:55296\n"
"v_add_f32 v140,v140,v204\n"
MF "v[44:47],a[112:115],a[244:247],v[44:47]\n"
"ds_read2st64_b32 v[194:195],v13 offset0:10 offset1:11\n"
"v_add_f32 v141,v141,v205\n"
MF "v[36:39],a[108:111],a[244:247],v[36:39]\n"
TR "v[184:185],v2 offset:57344\n"
"v_add_f32 v142,v142,v206\n"
MF "v[36:39],a[68:71],a[240:243],v[36:39]\n"
TR "v[186:187],v2 offset:59392\n"
"v_add_f32 v143,v143,v207\n"
MF "v[36:39],a[28:31],v[188:191],v[36:39]\n"
"ds_read2st64_b32 v[196:197],v13 offset0:12 offset1:13\n"
CV "v136,v136,v137\n"
MF "v[28:31],a[24:27],v[188:191],v[28:31]\n"
"ds_read2st64_b32 v[198:199],v13 offset0:14 offset1:15\n"
CV "v137,v138,v139\n"
MF "v[28:31],a[64:67],a[240:243],v[28:31]\n"
"s_add_u32 s87,s90,10240\n"
CV "v138,v140,v141\n"
CV "v139,v142,v143\n"
MF "v[28:31],a[104:107],a[244:247],v[28:31]\n"
MF "v[20:23],a[100:103],a[244:247],v[20:23]\n"
"buffer_store_dwordx4 v[136:139],v9,s[40:43],s87 offen sc1\n"
MF "v[20:23],a[60:63],a[240:243],v[20:23]\n"
MF "v[20:23],a[20:23],v[188:191],v[20:23]\n"
"s_waitcnt lgkmcnt(2)\n"
MF "v[56:59],a[20:23],v[176:179],v[56:59]\n"
"s_waitcnt lgkmcnt(0)\n"
TR "v[188:189],v3 offset:49152\n"
"v_add_f32 v144,v144,v192\n"
MF "v[56:59],a[60:63],v[180:183],v[56:59]\n"
TR "v[190:191],v3 offset:51200\n"
"v_add_f32 v145,v145,v193\n"
MF "v[56:59],a[100:103],v[184:187],v[56:59]\n"
"ds_read2st64_b32 v[200:201],v13 offset0:16 offset1:17\n"
"v_add_f32 v146,v146,v194\n"
MF "v[64:67],a[104:107],v[184:187],v[64:67]\n"
TR "a[240:241],v3 offset:53248\n"
"v_add_f32 v147,v147,v195\n"
MF "v[64:67],a[64:67],v[180:183],v[64:67]\n"
TR "a[242:243],v3 offset:55296\n"
"v_add_f32 v148,v148,v196\n"
MF "v[64:67],a[24:27],v[176:179],v[64:67]\n"
"ds_read2st64_b32 v[202:203],v13 offset0:18 offset1:19\n"
"v_add_f32 v149,v149,v197\n"
MF "v[72:75],a[28:31],v[176:179],v[72:75]\n"
TR "a[244:245],v3 offset:57344\n"
"v_add_f32 v150,v150,v198\n"
MF "v[72:75],a[68:71],v[180:183],v[72:75]\n"
TR "a[246:247],v3 offset:59392\n"
"v_add_f32 v151,v151,v199\n"
MF "v[72:75],a[108:111],v[184:187],v[72:75]\n"
"ds_read2st64_b32 v[204:205],v13 offset0:20 offset1:21\n"
CV "v144,v144,v145\n"
MF "v[80:83],a[112:115],v[184:187],v[80:83]\n"
"ds_read2st64_b32 v[206:207],v13 offset0:22 offset1:23\n"
CV "v145,v146,v147\n"
MF "v[80:83],a[72:75],v[180:183],v[80:83]\n"
"s_add_u32 s87,s90,11264\n"
CV "v146,v148,v149\n"
CV "v147,v150,v151\n"
MF "v[80:83],a[32:35],v[176:179],v[80:83]\n"
MF "v[88:91],a[36:39],v[176:179],v[88:91]\n"
"buffer_store_dwordx4 v[144:147],v9,s[40:43],s87 offen sc1\n"
MF "v[88:91],a[76:79],v[180:183],v[88:91]\n"
"s_waitcnt lgkmcnt(0)\n"
MF "v[88:91],a[116:119],v[184:187],v[88:91]\n"
"s_barrier\n"
MF "v[92:95],a[116:119],a[244:247],v[92:95]\n"
TR "v[176:177],v4\n"
"v_add_f32 v152,v152,v200\n"
MF "v[92:95],a[76:79],a[240:243],v[92:95]\n"
TR "v[178:179],v4 offset:2048\n"
"v_add_f32 v153,v153,v201\n"
MF "v[92:95],a[36:39],v[188:191],v[92:95]\n"
"ds_read2st64_b32 v[192:193],v13 offset0:24 offset1:25\n"
"v_add_f32 v154,v154,v202\n"
MF "v[84:87],a[32:35],v[188:191],v[84:87]\n"
TR "v[180:181],v4 offset:4096\n"
"v_add_f32 v155,v155,v203\n"
MF "v[84:87],a[72:75],a[240:243],v[84:87]\n"
TR "v[182:183],v4 offset:6144\n"
"v_add_f32 v156,v156,v204\n"
MF "v[84:87],a[112:115],a[244:247],v[84:87]\n"
"ds_read2st64_b32 v[194:195],v13 offset0:26 offset1:27\n"
"v_add_f32 v157,v157,v205\n"
MF "v[76:79],a[108:111],a[244:247],v[76:79]\n"
TR "v[184:185],v4 offset:8192\n"
"v_add_f32 v158,v158,v206\n"
MF "v[76:79],a[68:71],a[240:243],v[76:79]\n"
TR "v[186:187],v4 offset:10240\n"
"v_add_f32 v159,v159,v207\n"
MF "v[76:79],a[28:31],v[188:191],v[76:79]\n"
"ds_read2st64_b32 v[196:197],v13 offset0:28 offset1:29\n"
CV "v152,v152,v153\n"
MF "v[68:71],a[24:27],v[188:191],v[68:71]\n"
"ds_read2st64_b32 v[198:199],v13 offset0:30 offset1:31\n"
CV "v153,v154,v155\n"
MF "v[68:71],a[64:67],a[240:243],v[68:71]\n"
"s_add_u32 s87,s90,12288\n"
CV "v154,v156,v157\n"
CV "v155,v158,v159\n"
MF "v[68:71],a[104:107],a[244:247],v[68:71]\n"
MF "v[60:63],a[100:103],a[244:247],v[60:63]\n"
"buffer_store_dwordx4 v[152:155],v9,s[40:43],s87 offen sc1\n"
MF "v[60:63],a[60:63],a[240:243],v[60:63]\n"
MF "v[60:63],a[20:23],v[188:191],v[60:63]\n"
"s_waitcnt lgkmcnt(2)\n"
MF "v[16:19],a[140:143],v[176:179],v[16:19]\n"
"s_waitcnt lgkmcnt(0)\n"
TR "v[188:189],v5\n"
"v_add_f32 v160,v160,v192\n"
MF "v[16:19],a[180:183],v[180:183],v[16:19]\n"
TR "v[190:191],v5 offset:2048\n"
"v_add_f32 v161,v161,v193\n"
MF "v[16:19],a[220:223],v[184:187],v[16:19]\n"
"ds_read2st64_b32 v[200:201],v13 offset0:32 offset1:33\n"
"v_add_f32 v162,v162,v194\n"
MF "v[24:27],a[224:227],v[184:187],v[24:27]\n"
TR "a[240:241],v5 offset:4096\n"
"v_add_f32 v163,v163,v195\n"
MF "v[24:27],a[184:187],v[180:183],v[24:27]\n"
TR "a[242:243],v5 offset:6144\n"
"v_add_f32 v164,v164,v196\n"
MF "v[24:27],a[144:147],v[176:179],v[24:27]\n"
"ds_read2st64_b32 v[202:203],v13 offset0:34 offset1:35\n"
"v_add_f32 v165,v165,v197\n"
MF "v[32:35],a[148:151],v[176:179],v[32:35]\n"
TR "a[244:245],v5 offset:8192\n"
"v_add_f32 v166,v166,v198\n"
MF "v[32:35],a[188:191],v[180:183],v[32:35]\n"
TR "a[246:247],v5 offset:10240\n"
"v_add_f32 v167,v167,v199\n"
MF "v[32:35],a[228:231],v[184:187],v[32:35]\n"
"ds_read2st64_b32 v[204:205],v13 offset0:36 offset1:37\n"
CV "v160,v160,v161\n"
MF "v[40:43],a[232:235],v[184:187],v[40:43]\n"
"ds_read2st64_b32 v[206:207],v13 offset0:38 offset1:39\n"
CV "v161,v162,v163\n"
MF "v[40:43],a[192:195],v[180:183],v[40:43]\n"
"s_add_u32 s87,s90,13312\n"
CV "v162,v164,v165\n"
CV "v163,v166,v167\n"
MF "v[40:43],a[152:155],v[176:179],v[40:43]\n"
MF "v[48:51],a[156:159],v[176:179],v[48:51]\n"
"buffer_store_dwordx4 v[160:163],v9,s[40:43],s87 offen sc1\n"
MF "v[48:51],a[196:199],v[180:183],v[48:51]\n"
MF "v[48:51],a[236:239],v[184:187],v[48:51]\n"
"s_waitcnt lgkmcnt(2)\n"
MF "v[52:55],a[236:239],a[244:247],v[52:55]\n"
"s_waitcnt lgkmcnt(0)\n"
TR "v[176:177],v6\n"
"v_add_f32 v168,v168,v200\n"
MF "v[52:55],a[196:199],a[240:243],v[52:55]\n"
TR "v[178:179],v6 offset:2048\n"
"v_add_f32 v169,v169,v201\n"
MF "v[52:55],a[156:159],v[188:191],v[52:55]\n"
"v_add_f32 v170,v170,v202\n"
"v_add_f32 v171,v171,v203\n"
MF "v[44:47],a[152:155],v[188:191],v[44:47]\n"
TR "v[180:181],v6 offset:4096\n"
"v_add_f32 v172,v172,v204\n"
MF "v[44:47],a[192:195],a[240:243],v[44:47]\n"
TR "v[182:183],v6 offset:6144\n"
"v_add_f32 v173,v173,v205\n"
MF "v[44:47],a[232:235],a[244:247],v[44:47]\n"
"v_add_f32 v174,v174,v206\n"
"v_add_f32 v175,v175,v207\n"
MF "v[36:39],a[228:231],a[244:247],v[36:39]\n"
TR "v[184:185],v6 offset:8192\n"
CV "v168,v168,v169\n"
MF "v[36:39],a[188:191],a[240:243],v[36:39]\n"
TR "v[186:187],v6 offset:10240\n"
CV "v169,v170,v171\n"
MF "v[36:39],a[148:151],v[188:191],v[36:39]\n"
"s_add_u32 s87,s90,14336\n"
CV "v170,v172,v173\n"
CV "v171,v174,v175\n"
MF "v[28:31],a[144:147],v[188:191],v[28:31]\n"
MF "v[28:31],a[184:187],a[240:243],v[28:31]\n"
MF "v[28:31],a[224:227],a[244:247],v[28:31]\n"
MF "v[20:23],a[220:223],a[244:247],v[20:23]\n"
"buffer_store_dwordx4 v[168:171],v9,s[40:43],s87 offen sc1\n"
MF "v[20:23],a[180:183],a[240:243],v[20:23]\n"
"s_mov_b32 s90,s91\n"
"s_add_u32 s91,s91,20480\n"
MF "v[20:23],a[140:143],v[188:191],v[20:23]\n"
"s_waitcnt lgkmcnt(0)\n"
MF "v[56:59],a[140:143],v[176:179],v[56:59]\n"
TR "v[188:189],v7\n"
MF "v[56:59],a[180:183],v[180:183],v[56:59]\n"
TR "v[190:191],v7 offset:2048\n"
MF "v[56:59],a[220:223],v[184:187],v[56:59]\n"
"v_mov_b32 v244,0\n"
"v_mov_b32 v245,0\n"
MF "v[64:67],a[224:227],v[184:187],v[64:67]\n"
TR "a[240:241],v7 offset:4096\n"
"v_mov_b32 v246,0\n"
MF "v[64:67],a[184:187],v[180:183],v[64:67]\n"
TR "a[242:243],v7 offset:6144\n"
"v_mov_b32 v247,0\n"
MF "v[64:67],a[144:147],v[176:179],v[64:67]\n"
"v_mov_b32 v248,0\n"
"v_mov_b32 v249,0\n"
MF "v[72:75],a[148:151],v[176:179],v[72:75]\n"
TR "a[244:245],v7 offset:8192\n"
"v_mov_b32 v250,0\n"
MF "v[72:75],a[188:191],v[180:183],v[72:75]\n"
TR "a[246:247],v7 offset:10240\n"
"v_mov_b32 v251,0\n"
MF "v[72:75],a[228:231],v[184:187],v[72:75]\n"
MF "v[80:83],a[232:235],v[184:187],v[80:83]\n"
MF "v[80:83],a[192:195],v[180:183],v[80:83]\n"
MF "v[80:83],a[152:155],v[176:179],v[80:83]\n"
MF "v[88:91],a[156:159],v[176:179],v[88:91]\n"
MF "v[88:91],a[196:199],v[180:183],v[88:91]\n"
"s_waitcnt lgkmcnt(0)\n"
MF "v[88:91],a[236:239],v[184:187],v[88:91]\n"
"s_barrier\n"
MF "v[92:95],a[236:239],a[244:247],v[92:95]\n"
TR "v[176:177],v0 offset:24576\n"
MF "v[92:95],a[196:199],a[240:243],v[92:95]\n"
"s_add_u32 s85,s85,1\n"
TR "v[178:179],v0 offset:26624\n"
MF "v[92:95],a[156:159],v[188:191],v[92:95]\n"
MF "v[84:87],a[152:155],v[188:191],v[84:87]\n"
TR "v[180:181],v0 offset:28672\n"
MF "v[84:87],a[192:195],a[240:243],v[84:87]\n"
TR "v[182:183],v0 offset:30720\n"
MF "v[84:87],a[232:235],a[244:247],v[84:87]\n"
MF "v[76:79],a[228:231],a[244:247],v[76:79]\n"
TR "v[184:185],v0 offset:32768\n"
MF "v[76:79],a[188:191],a[240:243],v[76:79]\n"
TR "v[186:187],v0 offset:34816\n"
MF "v[76:79],a[148:151],v[188:191],v[76:79]\n"
MF "v[68:71],a[144:147],v[188:191],v[68:71]\n"
MF "v[68:71],a[184:187],a[240:243],v[68:71]\n"
MF "v[68:71],a[224:227],a[244:247],v[68:71]\n"
MF "v[60:63],a[220:223],a[244:247],v[60:63]\n"
MF "v[60:63],a[180:183],a[240:243],v[60:63]\n"
MF "v[60:63],a[140:143],v[188:191],v[60:63]\n"
"s_waitcnt lgkmcnt(0)\n"
"s_cmp_lg_u32 s85,5\n"
"s_cbranch_scc1 .Lpl15\n"
".Lpl19:\n"
MF "v[96:99],a[0:3],v[176:179],v[252:255]\n"
TR "v[188:189],v1 offset:24576\n"
MF "v[96:99],a[40:43],v[180:183],v[96:99]\n"
"s_add_u32 s82,s82,4096\n"
TR "v[190:191],v1 offset:26624\n"
MF "v[96:99],a[80:83],v[184:187],v[96:99]\n"
LD "v[224:227],v9,s[40:43],s82 offen sc1\n"
MF "v[104:107],a[84:87],v[184:187],v[252:255]\n"
TR "a[240:241],v1 offset:28672\n"
MF "v[104:107],a[44:47],v[180:183],v[104:107]\n"
TR "a[242:243],v1 offset:30720\n"
MF "v[104:107],a[4:7],v[176:179],v[104:107]\n"
MF "v[112:115],a[8:11],v[176:179],v[252:255]\n"
TR "a[244:245],v1 offset:32768\n"
MF "v[112:115],a[48:51],v[180:183],v[112:115]\n"
TR "a[246:247],v1 offset:34816\n"
MF "v[112:115],a[88:91],v[184:187],v[112:115]\n"
MF "v[120:123],a[92:95],v[184:187],v[252:255]\n"
MF "v[120:123],a[52:55],v[180:183],v[120:123]\n"
MF "v[120:123],a[12:15],v[176:179],v[120:123]\n"
MF "v[128:131],a[16:19],v[176:179],v[252:255]\n"
MF "v[128:131],a[56:59],v[180:183],v[128:131]\n"
MF "v[128:131],a[96:99],v[184:187],v[128:131]\n"
"s_waitcnt lgkmcnt(0)\n"
MF "v[132:135],a[96:99],a[244:247],v[252:255]\n"
TR "v[176:177],v2 offset:24576\n"
MF "v[132:135],a[56:59],a[240:243],v[132:135]\n"
TR "v[178:179],v2 offset:26624\n"
MF "v[132:135],a[16:19],v[188:191],v[132:135]\n"
MF "v[124:127],a[12:15],v[188:191],v[252:255]\n"
TR "v[180:181],v2 offset:28672\n"
MF "v[124:127],a[52:55],a[240:243],v[124:127]\n"
TR "v[182:183],v2 offset:30720\n"
MF "v[124:127],a[92:95],a[244:247],v[124:127]\n"
MF "v[116:119],a[88:91],a[244:247],v[252:255]\n"
TR "v[184:185],v2 offset:32768\n"
MF "v[116:119],a[48:51],a[240:243],v[116:119]\n"
TR "v[186:187],v2 offset:34816\n"
MF "v[116:119],a[8:11],v[188:191],v[116:119]\n"
MF "v[108:111],a[4:7],v[188:191],v[252:255]\n"
MF "v[108:111],a[44:47],a[240:243],v[108:111]\n"
MF "v[108:111],a[84:87],a[244:247],v[108:111]\n"
MF "v[100:103],a[80:83],a[244:247],v[252:255]\n"
MF "v[100:103],a[40:43],a[240:243],v[100:103]\n"
MF "v[100:103],a[0:3],v[188:191],v[100:103]\n"
"s_waitcnt lgkmcnt(0)\n"
MF "v[136:139],a[0:3],v[176:179],v[252:255]\n"
TR "v[188:189],v3 offset:24576\n"
MF "v[136:139],a[40:43],v[180:183],v[136:139]\n"
TR "v[190:191],v3 offset:26624\n"
MF "v[136:139],a[80:83],v[184:187],v[136:139]\n"
MF "v[144:147],a[84:87],v[184:187],v[252:255]\n"
TR "a[240:241],v3 offset:28672\n"
MF "v[144:147],a[44:47],v[180:183],v[144:147]\n"
TR "a[242:243],v3 offset:30720\n"
MF "v[144:147],a[4:7],v[176:179],v[144:147]\n"
MF "v[152:155],a[8:11],v[176:179],v[252:255]\n"
TR "a[244:245],v3 offset:32768\n"
MF "v[152:155],a[48:51],v[180:183],v[152:155]\n"
TR "a[246:247],v3 offset:34816\n"
MF "v[152:155],a[88:91],v[184:187],v[152:155]\n"
MF "v[160:163],a[92:95],v[184:187],v[252:255]\n"
MF "v[160:163],a[52:55],v[180:183],v[160:163]\n"
MF "v[160:163],a[12:15],v[176:179],v[160:163]\n"
MF "v[168:171],a[16:19],v[176:179],v[252:255]\n"
MF "v[168:171],a[56:59],v[180:183],v[168:171]\n"
"s_waitcnt lgkmcnt(0)\n"
MF "v[168:171],a[96:99],v[184:187],v[168:171]\n"
"s_barrier\n"
MF "v[172:175],a[96:99],a[244:247],v[252:255]\n"
TR "v[176:177],v0\n"
MF "v[172:175],a[56:59],a[240:243],v[172:175]\n"
TR "v[178:179],v0 offset:2048\n"
MF "v[172:175],a[16:19],v[188:191],v[172:175]\n"
"ds_read2st64_b32 v[192:193],v13 offset0:192 offset1:193\n"
MF "v[164:167],a[12:15],v[188:191],v[252:255]\n"
TR "v[180:181],v0 offset:4096\n"
MF "v[164:167],a[52:55],a[240:243],v[164:167]\n"
TR "v[182:183],v0 offset:6144\n"
MF "v[164:167],a[92:95],a[244:247],v[164:167]\n"
"ds_read2st64_b32 v[194:195],v13 offset0:194 offset1:195\n"
MF "v[156:159],a[88:91],a[244:247],v[252:255]\n"
TR "v[184:185],v0 offset:8192\n"
MF "v[156:159],a[48:51],a[240:243],v[156:159]\n"
TR "v[186:187],v0 offset:10240\n"
MF "v[156:159],a[8:11],v[188:191],v[156:159]\n"
"ds_read2st64_b32 v[196:197],v13 offset0:196 offset1:197\n"
MF "v[148:151],a[4:7],v[188:191],v[252:255]\n"
"ds_read2st64_b32 v[198:199],v13 offset0:198 offset1:199\n"
MF "v[148:151],a[44:47],a[240:243],v[148:151]\n"
"buffer_load_dword v10,v12,s[40:43],s86 offen sc1\n"
MF "v[148:151],a[84:87],a[244:247],v[148:151]\n"
MF "v[140:143],a[80:83],a[244:247],v[252:255]\n"
MF "v[140:143],a[40:43],a[240:243],v[140:143]\n"
MF "v[140:143],a[0:3],v[188:191],v[140:143]\n"
"s_waitcnt lgkmcnt(2)\n"
MF "v[96:99],a[120:123],v[176:179],v[96:99]\n"
"s_waitcnt lgkmcnt(0)\n"
TR "v[188:189],v1\n"
"v_add_f32 v16,v16,v192\n"
MF "v[96:99],a[160:163],v[180:183],v[96:99]\n"
TR "v[190:191],v1 offset:2048\n"
"v_add_f32 v17,v17,v193\n"
MF "v[96:99],a[200:203],v[184:187],v[96:99]\n"
"ds_read2st64_b32 v[200:201],v13 offset0:200 offset1:201\n"
"v_add_f32 v18,v18,v194\n"
"s_waitcnt vmcnt(2)\n"
"v_mov_b32 v14,s76\n"
"s_add_u32 s76,s76,1\n"
MF "v[104:107],a[204:207],v[184:187],v[104:107]\n"
TR "a[240:241],v1 offset:4096\n"
"v_add_f32 v19,v19,v195\n"
"buffer_store_dword v14,v15,s[40:43],s94 offen sc1\n"
MF "v[104:107],a[164:167],v[180:183],v[104:107]\n"
TR "a[242:243],v1 offset:6144\n"
"v_add_f32 v20,v20,v196\n"
MF "v[104:107],a[124:127],v[176:179],v[104:107]\n"
"ds_read2st64_b32 v[202:203],v13 offset0:202 offset1:203\n"
"v_add_f32 v21,v21,v197\n"
MF "v[112:115],a[128:131],v[176:179],v[112:115]\n"
TR "a[244:245],v1 offset:8192\n"
"v_add_f32 v22,v22,v198\n"
MF "v[112:115],a[168:171],v[180:183],v[112:115]\n"
TR "a[246:247],v1 offset:10240\n"
"v_add_f32 v23,v23,v199\n"
MF "v[112:115],a[208:211],v[184:187],v[112:115]\n"
"ds_read2st64_b32 v[204:205],v13 offset0:204 offset1:205\n"
CV "v16,v16,v17\n"
MF "v[120:123],a[212:215],v[184:187],v[120:123]\n"
"ds_read2st64_b32 v[206:207],v13 offset0:206 offset1:207\n"
CV "v17,v18,v19\n"
MF "v[120:123],a[172:175],v[180:183],v[120:123]\n"
"s_add_u32 s87,s90,0\n"
CV "v18,v20,v21\n"
CV "v19,v22,v23\n"
MF "v[120:123],a[132:135],v[176:179],v[120:123]\n"
MF "v[128:131],a[136:139],v[176:179],v[128:131]\n"
"buffer_store_dwordx4 v[16:19],v9,s[40:43],s87 offen sc1\n"
MF "v[128:131],a[176:179],v[180:183],v[128:131]\n"
MF "v[128:131],a[216:219],v[184:187],v[128:131]\n"
"s_waitcnt lgkmcnt(2)\n"
MF "v[132:135],a[216:219],a[244:247],v[132:135]\n"
"s_waitcnt lgkmcnt(0)\n"
TR "v[176:177],v2\n"
"v_add_f32 v24,v24,v200\n"
MF "v[132:135],a[176:179],a[240:243],v[132:135]\n"
TR "v[178:179],v2 offset:2048\n"
"v_add_f32 v25,v25,v201\n"
MF "v[132:135],a[136:139],v[188:191],v[132:135]\n"
"ds_read2st64_b32 v[192:193],v13 offset0:208 offset1:209\n"
"v_add_f32 v26,v26,v202\n"
MF "v[124:127],a[132:135],v[188:191],v[124:127]\n"
TR "v[180:181],v2 offset:4096\n"
"v_add_f32 v27,v27,v203\n"
MF "v[124:127],a[172:175],a[240:243],v[124:127]\n"
TR "v[182:183],v2 offset:6144\n"
"v_add_f32 v28,v28,v204\n"
MF "v[124:127],a[212:215],a[244:247],v[124:127]\n"
"ds_read2st64_b32 v[194:195],v13 offset0:210 offset1:211\n"
"v_add_f32 v29,v29,v205\n"
MF "v[116:119],a[208:211],a[244:247],v[116:119]\n"
TR "v[184:185],v2 offset:8192\n"
"v_add_f32 v30,v30,v206\n"
MF "v[116:119],a[168:171],a[240:243],v[116:119]\n"
TR "v[186:187],v2 offset:10240\n"
"v_add_f32 v31,v31,v207\n"
MF "v[116:119],a[128:131],v[188:191],v[116:119]\n"
"ds_read2st64_b32 v[196:197],v13 offset0:212 offset1:213\n"
CV "v24,v24,v25\n"
MF "v[108:111],a[124:127],v[188:191],v[108:111]\n"
"ds_read2st64_b32 v[198:199],v13 offset0:214 offset1:215\n"
CV "v25,v26,v27\n"
MF "v[108:111],a[164:167],a[240:243],v[108:111]\n"
"s_add_u32 s87,s90,1024\n"
CV "v26,v28,v29\n"
CV "v27,v30,v31\n"
MF "v[108:111],a[204:207],a[244:247],v[108:111]\n"
MF "v[100:103],a[200:203],a[244:247],v[100:103]\n"
"buffer_store_dwordx4 v[24:27],v9,s[40:43],s87 offen sc1\n"
MF "v[100:103],a[160:163],a[240:243],v[100:103]\n"
MF "v[100:103],a[120:123],v[188:191],v[100:103]\n"
"s_waitcnt lgkmcnt(2)\n"
MF "v[136:139],a[120:123],v[176:179],v[136:139]\n"
"s_waitcnt lgkmcnt(0)\n"
TR "v[188:189],v3\n"
"v_add_f32 v32,v32,v192\n"
MF "v[136:139],a[160:163],v[180:183],v[136:139]\n"
TR "v[190:191],v3 offset:2048\n"
"v_add_f32 v33,v33,v193\n"
MF "v[136:139],a[200:203],v[184:187],v[136:139]\n"
"ds_read2st64_b32 v[200:201],v13 offset0:216 offset1:217\n"
"v_add_f32 v34,v34,v194\n"
MF "v[144:147],a[204:207],v[184:187],v[144:147]\n"
TR "a[240:241],v3 offset:4096\n"
"v_add_f32 v35,v35,v195\n"
MF "v[144:147],a[164:167],v[180:183],v[144:147]\n"
TR "a[242:243],v3 offset:6144\n"
"v_add_f32 v36,v36,v196\n"
MF "v[144:147],a[124:127],v[176:179],v[144:147]\n"
"ds_read2st64_b32 v[202:203],v13 offset0:218 offset1:219\n"
"v_add_f32 v37,v37,v197\n"
MF "v[152:155],a[128:131],v[176:179],v[152:155]\n"
TR "a[244:245],v3 offset:8192\n"
"v_add_f32 v38,v38,v198\n"
MF "v[152:155],a[168:171],v[180:183],v[152:155]\n"
".Lpg32h:\n"
"s_cbranch_execz .Lisr4\n"
TR "a[246:247],v3 offset:10240\n"
"v_add_f32 v39,v39,v199\n"
MF "v[152:155],a[208:211],v[184:187],v[152:155]\n"
"ds_read2st64_b32 v[204:205],v13 offset0:220 offset1:221\n"
CV "v32,v32,v33\n"
MF "v[160:163],a[212:215],v[184:187],v[160:163]\n"
"ds_read2st64_b32 v[206:207],v13 offset0:222 offset1:223\n"
CV "v33,v34,v35\n"
MF "v[160:163],a[172:175],v[180:183],v[160:163]\n"
"s_add_u32 s87,s90,2048\n"
CV "v34,v36,v37\n"
CV "v35,v38,v39\n"
MF "v[160:163],a[132:135],v[176:179],v[160:163]\n"
MF "v[168:171],a[136:139],v[176:179],v[168:171]\n"
"buffer_store_dwordx4 v[32:35],v9,s[40:43],s87 offen sc1\n"
MF "v[168:171],a[176:179],v[180:183],v[168:171]\n"
"s_waitcnt lgkmcnt(0)\n"
MF "v[168:171],a[216:219],v[184:187],v[168:171]\n"
"s_barrier\n"
MF "v[172:175],a[216:219],a[244:247],v[172:175]\n"
TR "v[176:177],v0 offset:49152\n"
"v_add_f32 v40,v40,v200\n"
MF "v[172:175],a[176:179],a[240:243],v[172:175]\n"
"s_mov_b32 s82,s70\n"
TR "v[178:179],v0 offset:51200\n"
"v_add_f32 v41,v41,v201\n"
MF "v[172:175],a[136:139],v[188:191],v[172:175]\n"
"ds_read2st64_b32 v[192:193],v13 offset0:224 offset1:225\n"
"v_add_f32 v42,v42,v202\n"
MF "v[164:167],a[132:135],v[188:191],v[164:167]\n"
TR "v[180:181],v0 offset:53248\n"
"v_add_f32 v43,v43,v203\n"
MF "v[164:167],a[172:175],a[240:243],v[164:167]\n"
TR "v[182:183],v0 offset:55296\n"
"v_add_f32 v44,v44,v204\n"
MF "v[164:167],a[212:215],a[244:247],v[164:167]\n"
"ds_read2st64_b32 v[194:195],v13 offset0:226 offset1:227\n"
"v_add_f32 v45,v45,v205\n"
MF "v[156:159],a[208:211],a[244:247],v[156:159]\n"
TR "v[184:185],v0 offset:57344\n"
"v_add_f32 v46,v46,v206\n"
MF "v[156:159],a[168:171],a[240:243],v[156:159]\n"
TR "v[186:187],v0 offset:59392\n"
"v_add_f32 v47,v47,v207\n"
MF "v[156:159],a[128:131],v[188:191],v[156:159]\n"
"ds_read2st64_b32 v[196:197],v13 offset0:228 offset1:229\n"
CV "v40,v40,v41\n"
MF "v[148:151],a[124:127],v[188:191],v[148:151]\n"
"ds_read2st64_b32 v[198:199],v13 offset0:230 offset1:231\n"
CV "v41,v42,v43\n"
MF "v[148:151],a[164:167],a[240:243],v[148:151]\n"
"s_add_u32 s87,s90,3072\n"
CV "v42,v44,v45\n"
CV "v43,v46,v47\n"
MF "v[148:151],a[204:207],a[244:247],v[148:151]\n"
MF "v[140:143],a[200:203],a[244:247],v[140:143]\n"
"buffer_store_dwordx4 v[40:43],v9,s[40:43],s87 offen sc1\n"
MF "v[140:143],a[160:163],a[240:243],v[140:143]\n"
MF "v[140:143],a[120:123],v[188:191],v[140:143]\n"
"s_waitcnt lgkmcnt(2)\n"
MF "v[96:99],a[20:23],v[176:179],v[96:99]\n"
"s_waitcnt lgkmcnt(0)\n"
TR "v[188:189],v1 offset:49152\n"
"v_add_f32 v48,v48,v192\n"
MF "v[96:99],a[60:63],v[180:183],v[96:99]\n"
TR "v[190:191],v1 offset:51200\n"
"v_add_f32 v49,v49,v193\n"
MF "v[96:99],a[100:103],v[184:187],v[96:99]\n"
"ds_read2st64_b32 v[200:201],v13 offset0:0 offset1:1\n"
"v_add_f32 v50,v50,v194\n"
MF "v[104:107],a[104:107],v[184:187],v[104:107]\n"
TR "a[240:241],v1 offset:53248\n"
"v_add_f32 v51,v51,v195\n"
MF "v[104:107],a[64:67],v[180:183],v[104:107]\n"
TR "a[242:243],v1 offset:55296\n"
"v_add_f32 v52,v52,v196\n"
MF "v[104:107],a[24:27],v[176:179],v[104:107]\n"
"ds_read2st64_b32 v[202:203],v13 offset0:2 offset1:3\n"
"v_add_f32 v53,v53,v197\n"
MF "v[112:115],a[28:31],v[176:179],v[112:115]\n"
TR "a[244:245],v1 offset:57344\n"
"v_add_f32 v54,v54,v198\n"
MF "v[112:115],a[68:71],v[180:183],v[112:115]\n"
TR "a[246:247],v1 offset:59392\n"
"v_add_f32 v55,v55,v199\n"
MF "v[112:115],a[108:111],v[184:187],v[112:115]\n"
"ds_read2st64_b32 v[204:205],v13 offset0:4 offset1:5\n"
CV "v48,v48,v49\n"
MF "v[120:123],a[112:115],v[184:187],v[120:123]\n"
"ds_read2st64_b32 v[206:207],v13 offset0:6 offset1:7\n"
CV "v49,v50,v51\n"
MF "v[120:123],a[72:75],v[180:183],v[120:123]\n"
"s_add_u32 s87,s90,4096\n"
CV "v50,v52,v53\n"
CV "v51,v54,v55\n"
MF "v[120:123],a[32:35],v[176:179],v[120:123]\n"
MF "v[128:131],a[36:39],v[176:179],v[128:131]\n"
"buffer_store_dwordx4 v[48:51],v9,s[40:43],s87 offen sc1\n"
MF "v[128:131],a[76:79],v[180:183],v[128:131]\n"
MF "v[128:131],a[116:119],v[184:187],v[128:131]\n"
"s_waitcnt lgkmcnt(2)\n"
MF "v[132:135],a[116:119],a[244:247],v[132:135]\n"
"s_waitcnt lgkmcnt(0)\n"
TR "v[176:177],v2 offset:49152\n"
"v_add_f32 v56,v56,v200\n"
MF "v[132:135],a[76:79],a[240:243],v[132:135]\n"
TR "v[178:179],v2 offset:51200\n"
"v_add_f32 v57,v57,v201\n"
MF "v[132:135],a[36:39],v[188:191],v[132:135]\n"
"ds_read2st64_b32 v[192:193],v13 offset0:8 offset1:9\n"
"v_add_f32 v58,v58,v202\n"
MF "v[124:127],a[32:35],v[188:191],v[124:127]\n"
TR "v[180:181],v2 offset:53248\n"
"v_add_f32 v59,v59,v203\n"
MF "v[124:127],a[72:75],a[240:243],v[124:127]\n"
TR "v[182:183],v2 offset:55296\n"
"v_add_f32 v60,v60,v204\n"
MF "v[124:127],a[112:115],a[244:247],v[124:127]\n"
"ds_read2st64_b32 v[194:195],v13 offset0:10 offset1:11\n"
"v_add_f32 v61,v61,v205\n"
MF "v[116:119],a[108:111],a[244:247],v[116:119]\n"
TR "v[184:185],v2 offset:57344\n"
"v_add_f32 v62,v62,v206\n"
MF "v[116:119],a[68:71],a[240:243],v[116:119]\n"
TR "v[186:187],v2 offset:59392\n"
"v_add_f32 v63,v63,v207\n"
MF "v[116:119],a[28:31],v[188:191],v[116:119]\n"
"ds_read2st64_b32 v[196:197],v13 offset0:12 offset1:13\n"
CV "v56,v56,v57\n"
MF "v[108:111],a[24:27],v[188:191],v[108:111]\n"
"ds_read2st64_b32 v[198:199],v13 offset0:14 offset1:15\n"
CV "v57,v58,v59\n"
MF "v[108:111],a[64:67],a[240:243],v[108:111]\n"
"s_add_u32 s87,s90,10240\n"
CV "v58,v60,v61\n"
CV "v59,v62,v63\n"
MF "v[108:111],a[104:107],a[244:247],v[108:111]\n"
MF "v[100:103],a[100:103],a[244:247],v[100:103]\n"
"buffer_store_dwordx4 v[56:59],v9,s[40:43],s87 offen sc1\n"
MF "v[100:103],a[60:63],a[240:243],v[100:103]\n"
"s_waitcnt vmcnt(7)\n"
"s_cmp_lt_u32 s85,6\n"
MF "v[100:103],a[20:23],v[188:191],v[100:103]\n"
"s_cbranch_scc1 .Ljs10\n"
"v_mov_b32 v236,v228\n"
"v_mov_b32 v237,v229\n"
MF "v[136:139],a[252:255],v[208:211],v[136:139]\n"
MF "v[144:147],a[252:255],v[212:215],v[144:147]\n"
MF "v[152:155],a[252:255],v[216:219],v[152:155]\n"
MF "v[160:163],a[252:255],v[220:223],v[160:163]\n"
MF "v[168:171],a[252:255],v[224:227],v[168:171]\n"
MF "v[140:143],a[248:251],v[208:211],v[140:143]\n"
MF "v[148:151],a[248:251],v[212:215],v[148:151]\n"
MF "v[156:159],a[248:251],v[216:219],v[156:159]\n"
MF "v[164:167],a[248:251],v[220:223],v[164:167]\n"
MF "v[172:175],a[248:251],v[224:227],v[172:175]\n"
MF "v[248:251],v[230:233],a[250:253],v[248:251]\n"
MF "v[244:247],v[234:237],a[250:253],v[244:247]\n"
".Ljs10:\n"
"s_waitcnt lgkmcnt(2)\n"
MF "v[136:139],a[20:23],v[176:179],v[136:139]\n"
"s_waitcnt lgkmcnt(0)\n"
TR "v[188:189],v3 offset:49152\n"
"v_add_f32 v64,v64,v192\n"
MF "v[136:139],a[60:63],v[180:183],v[136:139]\n"
TR "v[190:191],v3 offset:51200\n"
"v_add_f32 v65,v65,v193\n"
MF "v[136:139],a[100:103],v[184:187],v[136:139]\n"
"ds_read2st64_b32 v[200:201],v13 offset0:16 offset1:17\n"
"v_add_f32 v66,v66,v194\n"
MF "v[144:147],a[104:107],v[184:187],v[144:147]\n"
TR "a[240:241],v3 offset:53248\n"
"v_add_f32 v67,v67,v195\n"
MF "v[144:147],a[64:67],v[180:183],v[144:147]\n"
TR "a[242:243],v3 offset:55296\n"
"v_add_f32 v68,v68,v196\n"
MF "v[144:147],a[24:27],v[176:179],v[144:147]\n"
"ds_read2st64_b32 v[202:203],v13 offset0:18 offset1:19\n"
"v_add_f32 v69,v69,v197\n"
MF "v[152:155],a[28:31],v[176:179],v[152:155]\n"
TR "a[244:245],v3 offset:57344\n"
"v_add_f32 v70,v70,v198\n"
MF "v[152:155],a[68:71],v[180:183],v[152:155]\n"
TR "a[246:247],v3 offset:59392\n"
"v_add_f32 v71,v71,v199\n"
MF "v[152:155],a[108:111],v[184:187],v[152:155]\n"
"ds_read2st64_b32 v[204:205],v13 offset0:20 offset1:21\n"
CV "v64,v64,v65\n"
MF "v[160:163],a[112:115],v[184:187],v[160:163]\n"
"ds_read2st64_b32 v[206:207],v13 offset0:22 offset1:23\n"
CV "v65,v66,v67\n"
MF "v[160:163],a[72:75],v[180:183],v[160:163]\n"
"s_add_u32 s87,s90,11264\n"
CV "v66,v68,v69\n"
CV "v67,v70,v71\n"
MF "v[160:163],a[32:35],v[176:179],v[160:163]\n"
"v_sub_u32 v10,v10,v11\n"
MF "v[168:171],a[36:39],v[176:179],v[168:171]\n"
"buffer_store_dwordx4 v[64:67],v9,s[40:43],s87 offen sc1\n"
MF "v[168:171],a[76:79],v[180:183],v[168:171]\n"
"s_waitcnt lgkmcnt(0)\n"
MF "v[168:171],a[116:119],v[184:187],v[168:171]\n"
"s_barrier\n"
MF "v[172:175],a[116:119],a[244:247],v[172:175]\n"
TR "v[176:177],v4\n"
"v_add_f32 v72,v72,v200\n"
MF "v[172:175],a[76:79],a[240:243],v[172:175]\n"
TR "v[178:179],v4 offset:2048\n"
"v_add_f32 v73,v73,v201\n"
MF "v[172:175],a[36:39],v[188:191],v[172:175]\n"
"ds_read2st64_b32 v[192:193],v13 offset0:24 offset1:25\n"
"v_add_f32 v74,v74,v202\n"
MF "v[164:167],a[32:35],v[188:191],v[164:167]\n"
TR "v[180:181],v4 offset:4096\n"
"v_add_f32 v75,v75,v203\n"
MF "v[164:167],a[72:75],a[240:243],v[164:167]\n"
TR "v[182:183],v4 offset:6144\n"
"v_add_f32 v76,v76,v204\n"
MF "v[164:167],a[112:115],a[244:247],v[164:167]\n"
"ds_read2st64_b32 v[194:195],v13 offset0:26 offset1:27\n"
"v_add_f32 v77,v77,v205\n"
MF "v[156:159],a[108:111],a[244:247],v[156:159]\n"
TR "v[184:185],v4 offset:8192\n"
"v_add_f32 v78,v78,v206\n"
MF "v[156:159],a[68:71],a[240:243],v[156:159]\n"
TR "v[186:187],v4 offset:10240\n"
"v_add_f32 v79,v79,v207\n"
MF "v[156:159],a[28:31],v[188:191],v[156:159]\n"
"ds_read2st64_b32 v[196:197],v13 offset0:28 offset1:29\n"
CV "v72,v72,v73\n"
MF "v[148:151],a[24:27],v[188:191],v[148:151]\n"
"ds_read2st64_b32 v[198:199],v13 offset0:30 offset1:31\n"
CV "v73,v74,v75\n"
MF "v[148:151],a[64:67],a[240:243],v[148:151]\n"
"s_add_u32 s87,s90,12288\n"
CV "v74,v76,v77\n"
CV "v75,v78,v79\n"
MF "v[148:151],a[104:107],a[244:247],v[148:151]\n"
".Lnf6:\n"
"v_readlane_b32 s58,v10,s67\n"
"s_cmp_lt_u32 s58,s85\n"
"s_cbranch_scc1 .Lsp6\n"
".Lnb6:\n"
MF "v[140:143],a[100:103],a[244:247],v[140:143]\n"
"buffer_store_dwordx4 v[72:75],v9,s[40:43],s87 offen sc1\n"
MF "v[140:143],a[60:63],a[240:243],v[140:143]\n"
"s_add_u32 s70,s70,20480\n"
".Lnf7:\n"
"v_cmp_gt_u32 vcc,4,v10\n"
"s_cbranch_vccnz .Lsp7\n"
".Lnb7:\n"
MF "v[140:143],a[20:23],v[188:191],v[140:143]\n"
"s_waitcnt lgkmcnt(2)\n"
MF "v[96:99],a[140:143],v[176:179],v[96:99]\n"
"s_waitcnt lgkmcnt(0)\n"
TR "v[188:189],v5\n"
"v_add_f32 v80,v80,v192\n"
MF "v[96:99],a[180:183],v[180:183],v[96:99]\n"
TR "v[190:191],v5 offset:2048\n"
"v_add_f32 v81,v81,v193\n"
MF "v[96:99],a[220:223],v[184:187],v[96:99]\n"
"ds_read2st64_b32 v[200:201],v13 offset0:32 offset1:33\n"
"v_add_f32 v82,v82,v194\n"
MF "v[104:107],a[224:227],v[184:187],v[104:107]\n"
TR "a[240:241],v5 offset:4096\n"
"v_add_f32 v83,v83,v195\n"
MF "v[104:107],a[184:187],v[180:183],v[104:107]\n"
TR "a[242:243],v5 offset:6144\n"
"v_add_f32 v84,v84,v196\n"
MF "v[104:107],a[144:147],v[176:179],v[104:107]\n"
"ds_read2st64_b32 v[202:203],v13 offset0:34 offset1:35\n"
"v_add_f32 v85,v85,v197\n"
MF "v[112:115],a[148:151],v[176:179],v[112:115]\n"
TR "a[244:245],v5 offset:8192\n"
"v_add_f32 v86,v86,v198\n"
MF "v[112:115],a[188:191],v[180:183],v[112:115]\n"
TR "a[246:247],v5 offset:10240\n"
"v_add_f32 v87,v87,v199\n"
MF "v[112:115],a[228:231],v[184:187],v[112:115]\n"
"ds_read2st64_b32 v[204:205],v13 offset0:36 offset1:37\n"
CV "v80,v80,v81\n"
MF "v[120:123],a[232:235],v[184:187],v[120:123]\n"
"s_add_u32 s2,s2,204800\n"
"ds_read2st64_b32 v[206:207],v13 offset0:38 offset1:39\n"
CV "v81,v82,v83\n"
MF "v[120:123],a[192:195],v[180:183],v[120:123]\n"
"s_add_u32 s87,s90,13312\n"
CV "v82,v84,v85\n"
CV "v83,v86,v87\n"
MF "v[120:123],a[152:155],v[176:179],v[120:123]\n"
LD "v[228:231],v9,s[40:43],s2 offen sc1\n"
MF "v[128:131],a[156:159],v[176:179],v[128:131]\n"
"buffer_store_dwordx4 v[80:83],v9,s[40:43],s87 offen sc1\n"
MF "v[128:131],a[196:199],v[180:183],v[128:131]\n"
"s_add_u32 s2,s2,204800\n"
MF "v[128:131],a[236:239],v[184:187],v[128:131]\n"
"s_waitcnt lgkmcnt(2)\n"
LD "v[232:235],v9,s[40:43],s2 offen sc1\n"
MF "v[132:135],a[236:239],a[244:247],v[132:135]\n"
"s_waitcnt lgkmcnt(0)\n"
TR "v[176:177],v6\n"
"v_add_f32 v88,v88,v200\n"
MF "v[132:135],a[196:199],a[240:243],v[132:135]\n"
TR "v[178:179],v6 offset:2048\n"
"v_add_f32 v89,v89,v201\n"
MF "v[132:135],a[156:159],v[188:191],v[132:135]\n"
"v_add_f32 v90,v90,v202\n"
"v_add_f32 v91,v91,v203\n"
MF "v[124:127],a[152:155],v[188:191],v[124:127]\n"
TR "v[180:181],v6 offset:4096\n"
"v_add_f32 v92,v92,v204\n"
MF "v[124:127],a[192:195],a[240:243],v[124:127]\n"
TR "v[182:183],v6 offset:6144\n"
"v_add_f32 v93,v93,v205\n"
MF "v[124:127],a[232:235],a[244:247],v[124:127]\n"
"v_add_f32 v94,v94,v206\n"
"v_add_f32 v95,v95,v207\n"
MF "v[116:119],a[228:231],a[244:247],v[116:119]\n"
TR "v[184:185],v6 offset:8192\n"
CV "v88,v88,v89\n"
MF "v[116:119],a[188:191],a[240:243],v[116:119]\n"
TR "v[186:187],v6 offset:10240\n"
CV "v89,v90,v91\n"
MF "v[116:119],a[148:151],v[188:191],v[116:119]\n"
"s_add_u32 s87,s90,14336\n"
CV "v90,v92,v93\n"
CV "v91,v94,v95\n"
MF "v[108:111],a[144:147],v[188:191],v[108:111]\n"
LD "v[208:211],v9,s[40:43],s82 offen sc1\n"
MF "v[108:111],a[184:187],a[240:243],v[108:111]\n"
MF "v[108:111],a[224:227],a[244:247],v[108:111]\n"
MF "v[100:103],a[220:223],a[244:247],v[100:103]\n"
"buffer_store_dwordx4 v[88:91],v9,s[40:43],s87 offen sc1\n"
MF "v[100:103],a[180:183],a[240:243],v[100:103]\n"
"s_mov_b32 s90,s91\n"
"s_add_u32 s91,s91,20480\n"
MF "v[100:103],a[140:143],v[188:191],v[100:103]\n"
"s_waitcnt lgkmcnt(0)\n"
MF "v[136:139],a[140:143],v[176:179],v[136:139]\n"
TR "v[188:189],v7\n"
MF "v[136:139],a[180:183],v[180:183],v[136:139]\n"
TR "v[190:191],v7 offset:2048\n"
MF "v[136:139],a[220:223],v[184:187],v[136:139]\n"
LD "v[212:215],v9,s[40:43],s82 offen offset:1024 sc1\n"
MF "v[144:147],a[224:227],v[184:187],v[144:147]\n"
TR "a[240:241],v7 offset:4096\n"
MF "v[144:147],a[184:187],v[180:183],v[144:147]\n"
TR "a[242:243],v7 offset:6144\n"
MF "v[144:147],a[144:147],v[176:179],v[144:147]\n"
MF "v[152:155],a[148:151],v[176:179],v[152:155]\n"
TR "a[244:245],v7 offset:8192\n"
MF "v[152:155],a[188:191],v[180:183],v[152:155]\n"
TR "a[246:247],v7 offset:10240\n"
MF "v[152:155],a[228:231],v[184:187],v[152:155]\n"
MF "v[160:163],a[232:235],v[184:187],v[160:163]\n"
MF "v[160:163],a[192:195],v[180:183],v[160:163]\n"
LD "v[216:219],v9,s[40:43],s82 offen offset:2048 sc1\n"
MF "v[160:163],a[152:155],v[176:179],v[160:163]\n"
MF "v[168:171],a[156:159],v[176:179],v[168:171]\n"
MF "v[168:171],a[196:199],v[180:183],v[168:171]\n"
"s_waitcnt lgkmcnt(0)\n"
MF "v[168:171],a[236:239],v[184:187],v[168:171]\n"
"s_barrier\n"
MF "v[172:175],a[236:239],a[244:247],v[172:175]\n"
TR "v[176:177],v0 offset:24576\n"
MF "v[172:175],a[196:199],a[240:243],v[172:175]\n"
"s_add_u32 s85,s85,1\n"
TR "v[178:179],v0 offset:26624\n"
MF "v[172:175],a[156:159],v[188:191],v[172:175]\n"
MF "v[164:167],a[152:155],v[188:191],v[164:167]\n"
TR "v[180:181],v0 offset:28672\n"
MF "v[164:167],a[192:195],a[240:243],v[164:167]\n"
TR "v[182:183],v0 offset:30720\n"
MF "v[164:167],a[232:235],a[244:247],v[164:167]\n"
LD "v[220:223],v9,s[40:43],s82 offen offset:3072 sc1\n"
MF "v[156:159],a[228:231],a[244:247],v[156:159]\n"
TR "v[184:185],v0 offset:32768\n"
MF "v[156:159],a[188:191],a[240:243],v[156:159]\n"
TR "v[186:187],v0 offset:34816\n"
MF "v[156:159],a[148:151],v[188:191],v[156:159]\n"
MF "v[148:151],a[144:147],v[188:191],v[148:151]\n"
MF "v[148:151],a[184:187],a[240:243],v[148:151]\n"
MF "v[148:151],a[224:227],a[244:247],v[148:151]\n"
MF "v[140:143],a[220:223],a[244:247],v[140:143]\n"
MF "v[140:143],a[180:183],a[240:243],v[140:143]\n"
MF "v[140:143],a[140:143],v[188:191],v[140:143]\n"
"s_waitcnt lgkmcnt(0)\n"
MF "v[16:19],a[0:3],v[176:179],v[252:255]\n"
TR "v[188:189],v1 offset:24576\n"
MF "v[16:19],a[40:43],v[180:183],v[16:19]\n"
"s_add_u32 s82,s82,4096\n"
TR "v[190:191],v1 offset:26624\n"
MF "v[16:19],a[80:83],v[184:187],v[16:19]\n"
LD "v[224:227],v9,s[40:43],s82 offen sc1\n"
MF "v[24:27],a[84:87],v[184:187],v[252:255]\n"
TR "a[240:241],v1 offset:28672\n"
MF "v[24:27],a[44:47],v[180:183],v[24:27]\n"
TR "a[242:243],v1 offset:30720\n"
MF "v[24:27],a[4:7],v[176:179],v[24:27]\n"
MF "v[32:35],a[8:11],v[176:179],v[252:255]\n"
TR "a[244:245],v1 offset:32768\n"
MF "v[32:35],a[48:51],v[180:183],v[32:35]\n"
TR "a[246:247],v1 offset:34816\n"
MF "v[32:35],a[88:91],v[184:187],v[32:35]\n"
MF "v[40:43],a[92:95],v[184:187],v[252:255]\n"
MF "v[40:43],a[52:55],v[180:183],v[40:43]\n"
MF "v[40:43],a[12:15],v[176:179],v[40:43]\n"
MF "v[48:51],a[16:19],v[176:179],v[252:255]\n"
MF "v[48:51],a[56:59],v[180:183],v[48:51]\n"
MF "v[48:51],a[96:99],v[184:187],v[48:51]\n"
"s_waitcnt lgkmcnt(0)\n"
MF "v[52:55],a[96:99],a[244:247],v[252:255]\n"
TR "v[176:177],v2 offset:24576\n"
MF "v[52:55],a[56:59],a[240:243],v[52:55]\n"
TR "v[178:179],v2 offset:26624\n"
MF "v[52:55],a[16:19],v[188:191],v[52:55]\n"
MF "v[44:47],a[12:15],v[188:191],v[252:255]\n"
TR "v[180:181],v2 offset:28672\n"
MF "v[44:47],a[52:55],a[240:243],v[44:47]\n"
TR "v[182:183],v2 offset:30720\n"
MF "v[44:47],a[92:95],a[244:247],v[44:47]\n"
MF "v[36:39],a[88:91],a[244:247],v[252:255]\n"
TR "v[184:185],v2 offset:32768\n"
MF "v[36:39],a[48:51],a[240:243],v[36:39]\n"
TR "v[186:187],v2 offset:34816\n"
MF "v[36:39],a[8:11],v[188:191],v[36:39]\n"
MF "v[28:31],a[4:7],v[188:191],v[252:255]\n"
MF "v[28:31],a[44:47],a[240:243],v[28:31]\n"
MF "v[28:31],a[84:87],a[244:247],v[28:31]\n"
MF "v[20:23],a[80:83],a[244:247],v[252:255]\n"
MF "v[20:23],a[40:43],a[240:243],v[20:23]\n"
MF "v[20:23],a[0:3],v[188:191],v[20:23]\n"
"s_waitcnt lgkmcnt(0)\n"
MF "v[56:59],a[0:3],v[176:179],v[252:255]\n"
TR "v[188:189],v3 offset:24576\n"
MF "v[56:59],a[40:43],v[180:183],v[56:59]\n"
TR "v[190:191],v3 offset:26624\n"
MF "v[56:59],a[80:83],v[184:187],v[56:59]\n"
MF "v[64:67],a[84:87],v[184:187],v[252:255]\n"
TR "a[240:241],v3 offset:28672\n"
MF "v[64:67],a[44:47],v[180:183],v[64:67]\n"
TR "a[242:243],v3 offset:30720\n"
MF "v[64:67],a[4:7],v[176:179],v[64:67]\n"
MF "v[72:75],a[8:11],v[176:179],v[252:255]\n"
TR "a[244:245],v3 offset:32768\n"
MF "v[72:75],a[48:51],v[180:183],v[72:75]\n"
TR "a[246:247],v3 offset:34816\n"
MF "v[72:75],a[88:91],v[184:187],v[72:75]\n"
MF "v[80:83],a[92:95],v[184:187],v[252:255]\n"
MF "v[80:83],a[52:55],v[180:183],v[80:83]\n"
MF "v[80:83],a[12:15],v[176:179],v[80:83]\n"
MF "v[88:91],a[16:19],v[176:179],v[252:255]\n"
MF "v[88:91],a[56:59],v[180:183],v[88:91]\n"
"s_waitcnt lgkmcnt(0)\n"
MF "v[88:91],a[96:99],v[184:187],v[88:91]\n"
"s_barrier\n"
MF "v[92:95],a[96:99],a[244:247],v[252:255]\n"
TR "v[176:177],v0\n"
MF "v[92:95],a[56:59],a[240:243],v[92:95]\n"
TR "v[178:179],v0 offset:2048\n"
MF "v[92:95],a[16:19],v[188:191],v[92:95]\n"
"ds_read2st64_b32 v[192:193],v13 offset0:192 offset1:193\n"
MF "v[84:87],a[12:15],v[188:191],v[252:255]\n"
TR "v[180:181],v0 offset:4096\n"
MF "v[84:87],a[52:55],a[240:243],v[84:87]\n"
TR "v[182:183],v0 offset:6144\n"
MF "v[84:87],a[92:95],a[244:247],v[84:87]\n"
"ds_read2st64_b32 v[194:195],v13 offset0:194 offset1:195\n"
MF "v[76:79],a[88:91],a[244:247],v[252:255]\n"
TR "v[184:185],v0 offset:8192\n"
MF "v[76:79],a[48:51],a[240:243],v[76:79]\n"
TR "v[186:187],v0 offset:10240\n"
MF "v[76:79],a[8:11],v[188:191],v[76:79]\n"
"ds_read2st64_b32 v[196:197],v13 offset0:196 offset1:197\n"
MF "v[68:71],a[4:7],v[188:191],v[252:255]\n"
"ds_read2st64_b32 v[198:199],v13 offset0:198 offset1:199\n"
MF "v[68:71],a[44:47],a[240:243],v[68:71]\n"
"buffer_load_dword v10,v12,s[40:43],s86 offen sc1\n"
MF "v[68:71],a[84:87],a[244:247],v[68:71]\n"
MF "v[60:63],a[80:83],a[244:247],v[252:255]\n"
MF "v[60:63],a[40:43],a[240:243],v[60:63]\n"
MF "v[60:63],a[0:3],v[188:191],v[60:63]\n"
"s_waitcnt lgkmcnt(2)\n"
MF "v[16:19],a[120:123],v[176:179],v[16:19]\n"
"s_waitcnt lgkmcnt(0)\n"
TR "v[188:189],v1\n"
"v_add_f32 v96,v96,v192\n"
MF "v[16:19],a[160:163],v[180:183],v[16:19]\n"
TR "v[190:191],v1 offset:2048\n"
"v_add_f32 v97,v97,v193\n"
MF "v[16:19],a[200:203],v[184:187],v[16:19]\n"
"ds_read2st64_b32 v[200:201],v13 offset0:200 offset1:201\n"
"v_add_f32 v98,v98,v194\n"
"s_waitcnt vmcnt(5)\n"
"v_mov_b32 v14,s76\n"
"s_add_u32 s76,s76,1\n"
MF "v[24:27],a[204:207],v[184:187],v[24:27]\n"
TR "a[240:241],v1 offset:4096\n"
"v_add_f32 v99,v99,v195\n"
"buffer_store_dword v14,v15,s[40:43],s94 offen sc1\n"
MF "v[24:27],a[164:167],v[180:183],v[24:27]\n"
TR "a[242:243],v1 offset:6144\n"
"v_add_f32 v100,v100,v196\n"
MF "v[24:27],a[124:127],v[176:179],v[24:27]\n"
"ds_read2st64_b32 v[202:203],v13 offset0:202 offset1:203\n"
"v_add_f32 v101,v101,v197\n"
MF "v[32:35],a[128:131],v[176:179],v[32:35]\n"
TR "a[244:245],v1 offset:8192\n"
"v_add_f32 v102,v102,v198\n"
MF "v[32:35],a[168:171],v[180:183],v[32:35]\n"
TR "a[246:247],v1 offset:10240\n"
"v_add_f32 v103,v103,v199\n"
MF "v[32:35],a[208:211],v[184:187],v[32:35]\n"
"ds_read2st64_b32 v[204:205],v13 offset0:204 offset1:205\n"
CV "v96,v96,v97\n"
MF "v[40:43],a[212:215],v[184:187],v[40:43]\n"
"ds_read2st64_b32 v[206:207],v13 offset0:206 offset1:207\n"
CV "v97,v98,v99\n"
MF "v[40:43],a[172:175],v[180:183],v[40:43]\n"
"s_add_u32 s87,s90,0\n"
CV "v98,v100,v101\n"
CV "v99,v102,v103\n"
MF "v[40:43],a[132:135],v[176:179],v[40:43]\n"
MF "v[48:51],a[136:139],v[176:179],v[48:51]\n"
"buffer_store_dwordx4 v[96:99],v9,s[40:43],s87 offen sc1\n"
MF "v[48:51],a[176:179],v[180:183],v[48:51]\n"
MF "v[48:51],a[216:219],v[184:187],v[48:51]\n"
"s_waitcnt lgkmcnt(2)\n"
MF "v[52:55],a[216:219],a[244:247],v[52:55]\n"
"s_waitcnt lgkmcnt(0)\n"
TR "v[176:177],v2\n"
"v_add_f32 v104,v104,v200\n"
MF "v[52:55],a[176:179],a[240:243],v[52:55]\n"
TR "v[178:179],v2 offset:2048\n"
"v_add_f32 v105,v105,v201\n"
MF "v[52:55],a[136:139],v[188:191],v[52:55]\n"
"ds_read2st64_b32 v[192:193],v13 offset0:208 offset1:209\n"
"v_add_f32 v106,v106,v202\n"
MF "v[44:47],a[132:135],v[188:191],v[44:47]\n"
TR "v[180:181],v2 offset:4096\n"
"v_add_f32 v107,v107,v203\n"
MF "v[44:47],a[172:175],a[240:243],v[44:47]\n"
TR "v[182:183],v2 offset:6144\n"
"v_add_f32 v108,v108,v204\n"
MF "v[44:47],a[212:215],a[244:247],v[44:47]\n"
"ds_read2st64_b32 v[194:195],v13 offset0:210 offset1:211\n"
"v_add_f32 v109,v109,v205\n"
MF "v[36:39],a[208:211],a[244:247],v[36:39]\n"
TR "v[184:185],v2 offset:8192\n"
"v_add_f32 v110,v110,v206\n"
MF "v[36:39],a[168:171],a[240:243],v[36:39]\n"
TR "v[186:187],v2 offset:10240\n"
"v_add_f32 v111,v111,v207\n"
MF "v[36:39],a[128:131],v[188:191],v[36:39]\n"
"ds_read2st64_b32 v[196:197],v13 offset0:212 offset1:213\n"
CV "v104,v104,v105\n"
MF "v[28:31],a[124:127],v[188:191],v[28:31]\n"
"ds_read2st64_b32 v[198:199],v13 offset0:214 offset1:215\n"
CV "v105,v106,v107\n"
MF "v[28:31],a[164:167],a[240:243],v[28:31]\n"
"s_add_u32 s87,s90,1024\n"
CV "v106,v108,v109\n"
CV "v107,v110,v111\n"
MF "v[28:31],a[204:207],a[244:247],v[28:31]\n"
MF "v[20:23],a[200:203],a[244:247],v[20:23]\n"
"buffer_store_dwordx4 v[104:107],v9,s[40:43],s87 offen sc1\n"
MF "v[20:23],a[160:163],a[240:243],v[20:23]\n"
MF "v[20:23],a[120:123],v[188:191],v[20:23]\n"
"s_waitcnt lgkmcnt(2)\n"
MF "v[56:59],a[120:123],v[176:179],v[56:59]\n"
"s_waitcnt lgkmcnt(0)\n"
TR "v[188:189],v3\n"
"v_add_f32 v112,v112,v192\n"
MF "v[56:59],a[160:163],v[180:183],v[56:59]\n"
TR "v[190:191],v3 offset:2048\n"
"v_add_f32 v113,v113,v193\n"
MF "v[56:59],a[200:203],v[184:187],v[56:59]\n"
".Lpg33h:\n"
"s_cbranch_execz .Lisr4\n"
"ds_read2st64_b32 v[200:201],v13 offset0:216 offset1:217\n"
"v_add_f32 v114,v114,v194\n"
MF "v[64:67],a[204:207],v[184:187],v[64:67]\n"
TR "a[240:241],v3 offset:4096\n"
"v_add_f32 v115,v115,v195\n"
MF "v[64:67],a[164:167],v[180:183],v[64:67]\n"
TR "a[242:243],v3 offset:6144\n"
"v_add_f32 v116,v116,v196\n"
MF "v[64:67],a[124:127],v[176:179],v[64:67]\n"
"ds_read2st64_b32 v[202:203],v13 offset0:218 offset1:219\n"
"v_add_f32 v117,v117,v197\n"
MF "v[72:75],a[128:131],v[176:179],v[72:75]\n"
TR "a[244:245],v3 offset:8192\n"
"v_add_f32 v118,v118,v198\n"
MF "v[72:75],a[168:171],v[180:183],v[72:75]\n"
TR "a[246:247],v3 offset:10240\n"
"v_add_f32 v119,v119,v199\n"
MF "v[72:75],a[208:211],v[184:187],v[72:75]\n"
"ds_read2st64_b32 v[204:205],v13 offset0:220 offset1:221\n"
CV "v112,v112,v113\n"
MF "v[80:83],a[212:215],v[184:187],v[80:83]\n"
"ds_read2st64_b32 v[206:207],v13 offset0:222 offset1:223\n"
CV "v113,v114,v115\n"
MF "v[80:83],a[172:175],v[180:183],v[80:83]\n"
"s_add_u32 s87,s90,2048\n"
CV "v114,v116,v117\n"
CV "v115,v118,v119\n"
MF "v[80:83],a[132:135],v[176:179],v[80:83]\n"
MF "v[88:91],a[136:139],v[176:179],v[88:91]\n"
"buffer_store_dwordx4 v[112:115],v9,s[40:43],s87 offen sc1\n"
MF "v[88:91],a[176:179],v[180:183],v[88:91]\n"
"s_waitcnt lgkmcnt(0)\n"
MF "v[88:91],a[216:219],v[184:187],v[88:91]\n"
"s_barrier\n"
MF "v[92:95],a[216:219],a[244:247],v[92:95]\n"
TR "v[176:177],v0 offset:49152\n"
"v_add_f32 v120,v120,v200\n"
MF "v[92:95],a[176:179],a[240:243],v[92:95]\n"
"s_mov_b32 s82,s70\n"
TR "v[178:179],v0 offset:51200\n"
"v_add_f32 v121,v121,v201\n"
MF "v[92:95],a[136:139],v[188:191],v[92:95]\n"
"ds_read2st64_b32 v[192:193],v13 offset0:224 offset1:225\n"
"v_add_f32 v122,v122,v202\n"
MF "v[84:87],a[132:135],v[188:191],v[84:87]\n"
TR "v[180:181],v0 offset:53248\n"
"v_add_f32 v123,v123,v203\n"
MF "v[84:87],a[172:175],a[240:243],v[84:87]\n"
TR "v[182:183],v0 offset:55296\n"
"v_add_f32 v124,v124,v204\n"
MF "v[84:87],a[212:215],a[244:247],v[84:87]\n"
"ds_read2st64_b32 v[194:195],v13 offset0:226 offset1:227\n"
"v_add_f32 v125,v125,v205\n"
MF "v[76:79],a[208:211],a[244:247],v[76:79]\n"
TR "v[184:185],v0 offset:57344\n"
"v_add_f32 v126,v126,v206\n"
MF "v[76:79],a[168:171],a[240:243],v[76:79]\n"
TR "v[186:187],v0 offset:59392\n"
"v_add_f32 v127,v127,v207\n"
MF "v[76:79],a[128:131],v[188:191],v[76:79]\n"
"ds_read2st64_b32 v[196:197],v13 offset0:228 offset1:229\n"
CV "v120,v120,v121\n"
MF "v[68:71],a[124:127],v[188:191],v[68:71]\n"
"ds_read2st64_b32 v[198:199],v13 offset0:230 offset1:231\n"
CV "v121,v122,v123\n"
MF "v[68:71],a[164:167],a[240:243],v[68:71]\n"
"s_add_u32 s87,s90,3072\n"
CV "v122,v124,v125\n"
CV "v123,v126,v127\n"
MF "v[68:71],a[204:207],a[244:247],v[68:71]\n"
MF "v[60:63],a[200:203],a[244:247],v[60:63]\n"
"buffer_store_dwordx4 v[120:123],v9,s[40:43],s87 offen sc1\n"
MF "v[60:63],a[160:163],a[240:243],v[60:63]\n"
MF "v[60:63],a[120:123],v[188:191],v[60:63]\n"
"s_waitcnt lgkmcnt(2)\n"
MF "v[16:19],a[20:23],v[176:179],v[16:19]\n"
"s_waitcnt lgkmcnt(0)\n"
TR "v[188:189],v1 offset:49152\n"
"v_add_f32 v128,v128,v192\n"
MF "v[16:19],a[60:63],v[180:183],v[16:19]\n"
TR "v[190:191],v1 offset:51200\n"
"v_add_f32 v129,v129,v193\n"
MF "v[16:19],a[100:103],v[184:187],v[16:19]\n"
"ds_read2st64_b32 v[200:201],v13 offset0:0 offset1:1\n"
"v_add_f32 v130,v130,v194\n"
MF "v[24:27],a[104:107],v[184:187],v[24:27]\n"
TR "a[240:241],v1 offset:53248\n"
"v_add_f32 v131,v131,v195\n"
MF "v[24:27],a[64:67],v[180:183],v[24:27]\n"
TR "a[242:243],v1 offset:55296\n"
"v_add_f32 v132,v132,v196\n"
MF "v[24:27],a[24:27],v[176:179],v[24:27]\n"
"ds_read2st64_b32 v[202:203],v13 offset0:2 offset1:3\n"
"v_add_f32 v133,v133,v197\n"
MF "v[32:35],a[28:31],v[176:179],v[32:35]\n"
TR "a[244:245],v1 offset:57344\n"
"v_add_f32 v134,v134,v198\n"
MF "v[32:35],a[68:71],v[180:183],v[32:35]\n"
TR "a[246:247],v1 offset:59392\n"
"v_add_f32 v135,v135,v199\n"
MF "v[32:35],a[108:111],v[184:187],v[32:35]\n"
"ds_read2st64_b32 v[204:205],v13 offset0:4 offset1:5\n"
CV "v128,v128,v129\n"
MF "v[40:43],a[112:115],v[184:187],v[40:43]\n"
"ds_read2st64_b32 v[206:207],v13 offset0:6 offset1:7\n"
CV "v129,v130,v131\n"
MF "v[40:43],a[72:75],v[180:183],v[40:43]\n"
"s_add_u32 s87,s90,4096\n"
CV "v130,v132,v133\n"
CV "v131,v134,v135\n"
MF "v[40:43],a[32:35],v[176:179],v[40:43]\n"
MF "v[48:51],a[36:39],v[176:179],v[48:51]\n"
"buffer_store_dwordx4 v[128:131],v9,s[40:43],s87 offen sc1\n"
MF "v[48:51],a[76:79],v[180:183],v[48:51]\n"
MF "v[48:51],a[116:119],v[184:187],v[48:51]\n"
"s_waitcnt lgkmcnt(2)\n"
MF "v[52:55],a[116:119],a[244:247],v[52:55]\n"
"s_waitcnt lgkmcnt(0)\n"
TR "v[176:177],v2 offset:49152\n"
"v_add_f32 v136,v136,v200\n"
MF "v[52:55],a[76:79],a[240:243],v[52:55]\n"
TR "v[178:179],v2 offset:51200\n"
"v_add_f32 v137,v137,v201\n"
MF "v[52:55],a[36:39],v[188:191],v[52:55]\n"
"ds_read2st64_b32 v[192:193],v13 offset0:8 offset1:9\n"
"v_add_f32 v138,v138,v202\n"
MF "v[44:47],a[32:35],v[188:191],v[44:47]\n"
TR "v[180:181],v2 offset:53248\n"
"v_add_f32 v139,v139,v203\n"
MF "v[44:47],a[72:75],a[240:243],v[44:47]\n"
TR "v[182:183],v2 offset:55296\n"
"v_add_f32 v140,v140,v204\n"
MF "v[44:47],a[112:115],a[244:247],v[44:47]\n"
"ds_read2st64_b32 v[194:195],v13 offset0:10 offset1:11\n"
"v_add_f32 v141,v141,v205\n"
MF "v[36:39],a[108:111],a[244:247],v[36:39]\n"
TR "v[184:185],v2 offset:57344\n"
"v_add_f32 v142,v142,v206\n"
MF "v[36:39],a[68:71],a[240:243],v[36:39]\n"
TR "v[186:187],v2 offset:59392\n"
"v_add_f32 v143,v143,v207\n"
MF "v[36:39],a[28:31],v[188:191],v[36:39]\n"
"ds_read2st64_b32 v[196:197],v13 offset0:12 offset1:13\n"
CV "v136,v136,v137\n"
MF "v[28:31],a[24:27],v[188:191],v[28:31]\n"
"ds_read2st64_b32 v[198:199],v13 offset0:14 offset1:15\n"
CV "v137,v138,v139\n"
MF "v[28:31],a[64:67],a[240:243],v[28:31]\n"
"s_add_u32 s87,s90,10240\n"
CV "v138,v140,v141\n"
CV "v139,v142,v143\n"
MF "v[28:31],a[104:107],a[244:247],v[28:31]\n"
MF "v[20:23],a[100:103],a[244:247],v[20:23]\n"
"buffer_store_dwordx4 v[136:139],v9,s[40:43],s87 offen sc1\n"
MF "v[20:23],a[60:63],a[240:243],v[20:23]\n"
"s_waitcnt vmcnt(7)\n"
MF "v[20:23],a[20:23],v[188:191],v[20:23]\n"
"v_mov_b32 v236,v228\n"
"v_mov_b32 v237,v229\n"
MF "v[56:59],a[252:255],v[208:211],v[56:59]\n"
MF "v[64:67],a[252:255],v[212:215],v[64:67]\n"
MF "v[72:75],a[252:255],v[216:219],v[72:75]\n"
MF "v[80:83],a[252:255],v[220:223],v[80:83]\n"
MF "v[88:91],a[252:255],v[224:227],v[88:91]\n"
MF "v[60:63],a[248:251],v[208:211],v[60:63]\n"
MF "v[68:71],a[248:251],v[212:215],v[68:71]\n"
MF "v[76:79],a[248:251],v[216:219],v[76:79]\n"
MF "v[84:87],a[248:251],v[220:223],v[84:87]\n"
MF "v[92:95],a[248:251],v[224:227],v[92:95]\n"
MF "v[248:251],v[230:233],a[250:253],v[248:251]\n"
MF "v[244:247],v[234:237],a[250:253],v[244:247]\n"
"s_waitcnt lgkmcnt(2)\n"
MF "v[56:59],a[20:23],v[176:179],v[56:59]\n"
"s_waitcnt lgkmcnt(0)\n"
TR "v[188:189],v3 offset:49152\n"
"v_add_f32 v144,v144,v192\n"
MF "v[56:59],a[60:63],v[180:183],v[56:59]\n"
TR "v[190:191],v3 offset:51200\n"
"v_add_f32 v145,v145,v193\n"
MF "v[56:59],a[100:103],v[184:187],v[56:59]\n"
"ds_read2st64_b32 v[200:201],v13 offset0:16 offset1:17\n"
"v_add_f32 v146,v146,v194\n"
MF "v[64:67],a[104:107],v[184:187],v[64:67]\n"
TR "a[240:241],v3 offset:53248\n"
"v_add_f32 v147,v147,v195\n"
MF "v[64:67],a[64:67],v[180:183],v[64:67]\n"
TR "a[242:243],v3 offset:55296\n"
"v_add_f32 v148,v148,v196\n"
MF "v[64:67],a[24:27],v[176:179],v[64:67]\n"
"ds_read2st64_b32 v[202:203],v13 offset0:18 offset1:19\n"
"v_add_f32 v149,v149,v197\n"
MF "v[72:75],a[28:31],v[176:179],v[72:75]\n"
TR "a[244:245],v3 offset:57344\n"
"v_add_f32 v150,v150,v198\n"
MF "v[72:75],a[68:71],v[180:183],v[72:75]\n"
TR "a[246:247],v3 offset:59392\n"
"v_add_f32 v151,v151,v199\n"
MF "v[72:75],a[108:111],v[184:187],v[72:75]\n"
"ds_read2st64_b32 v[204:205],v13 offset0:20 offset1:21\n"
CV "v144,v144,v145\n"
MF "v[80:83],a[112:115],v[184:187],v[80:83]\n"
"ds_read2st64_b32 v[206:207],v13 offset0:22 offset1:23\n"
CV "v145,v146,v147\n"
MF "v[80:83],a[72:75],v[180:183],v[80:83]\n"
"s_add_u32 s87,s90,11264\n"
CV "v146,v148,v149\n"
CV "v147,v150,v151\n"
MF "v[80:83],a[32:35],v[176:179],v[80:83]\n"
"v_sub_u32 v10,v10,v11\n"
MF "v[88:91],a[36:39],v[176:179],v[88:91]\n"
"buffer_store_dwordx4 v[144:147],v9,s[40:43],s87 offen sc1\n"
MF "v[88:91],a[76:79],v[180:183],v[88:91]\n"
"s_waitcnt lgkmcnt(0)\n"
MF "v[88:91],a[116:119],v[184:187],v[88:91]\n"
"s_barrier\n"
MF "v[92:95],a[116:119],a[244:247],v[92:95]\n"
TR "v[176:177],v4\n"
"v_add_f32 v152,v152,v200\n"
MF "v[92:95],a[76:79],a[240:243],v[92:95]\n"
TR "v[178:179],v4 offset:2048\n"
"v_add_f32 v153,v153,v201\n"
MF "v[92:95],a[36:39],v[188:191],v[92:95]\n"
"ds_read2st64_b32 v[192:193],v13 offset0:24 offset1:25\n"
"v_add_f32 v154,v154,v202\n"
MF "v[84:87],a[32:35],v[188:191],v[84:87]\n"
TR "v[180:181],v4 offset:4096\n"
"v_add_f32 v155,v155,v203\n"
MF "v[84:87],a[72:75],a[240:243],v[84:87]\n"
TR "v[182:183],v4 offset:6144\n"
"v_add_f32 v156,v156,v204\n"
MF "v[84:87],a[112:115],a[244:247],v[84:87]\n"
"ds_read2st64_b32 v[194:195],v13 offset0:26 offset1:27\n"
"v_add_f32 v157,v157,v205\n"
MF "v[76:79],a[108:111],a[244:247],v[76:79]\n"
TR "v[184:185],v4 offset:8192\n"
"v_add_f32 v158,v158,v206\n"
MF "v[76:79],a[68:71],a[240:243],v[76:79]\n"
TR "v[186:187],v4 offset:10240\n"
"v_add_f32 v159,v159,v207\n"
MF "v[76:79],a[28:31],v[188:191],v[76:79]\n"
"ds_read2st64_b32 v[196:197],v13 offset0:28 offset1:29\n"
CV "v152,v152,v153\n"
MF "v[68:71],a[24:27],v[188:191],v[68:71]\n"
"ds_read2st64_b32 v[198:199],v13 offset0:30 offset1:31\n"
CV "v153,v154,v155\n"
MF "v[68:71],a[64:67],a[240:243],v[68:71]\n"
"s_add_u32 s87,s90,12288\n"
CV "v154,v156,v157\n"
CV "v155,v158,v159\n"
MF "v[68:71],a[104:107],a[244:247],v[68:71]\n"
".Lnf8:\n"
"v_readlane_b32 s58,v10,s67\n"
"s_cmp_lt_u32 s58,s85\n"
"s_cbranch_scc1 .Lsp8\n"
".Lnb8:\n"
MF "v[60:63],a[100:103],a[244:247],v[60:63]\n"
"buffer_store_dwordx4 v[152:155],v9,s[40:43],s87 offen sc1\n"
MF "v[60:63],a[60:63],a[240:243],v[60:63]\n"
"s_add_u32 s70,s70,20480\n"
"s_add_u32 s2,s2,204800\n"
MF "v[60:63],a[20:23],v[188:191],v[60:63]\n"
"s_waitcnt lgkmcnt(2)\n"
LD "v[228:231],v9,s[40:43],s2 offen sc1\n"
MF "v[16:19],a[140:143],v[176:179],v[16:19]\n"
"s_waitcnt lgkmcnt(0)\n"
TR "v[188:189],v5\n"
"v_add_f32 v160,v160,v192\n"
MF "v[16:19],a[180:183],v[180:183],v[16:19]\n"
TR "v[190:191],v5 offset:2048\n"
"v_add_f32 v161,v161,v193\n"
MF "v[16:19],a[220:223],v[184:187],v[16:19]\n"
"ds_read2st64_b32 v[200:201],v13 offset0:32 offset1:33\n"
"v_add_f32 v162,v162,v194\n"
MF "v[24:27],a[224:227],v[184:187],v[24:27]\n"
TR "a[240:241],v5 offset:4096\n"
"v_add_f32 v163,v163,v195\n"
MF "v[24:27],a[184:187],v[180:183],v[24:27]\n"
TR "a[242:243],v5 offset:6144\n"
"v_add_f32 v164,v164,v196\n"
MF "v[24:27],a[144:147],v[176:179],v[24:27]\n"
"ds_read2st64_b32 v[202:203],v13 offset0:34 offset1:35\n"
"v_add_f32 v165,v165,v197\n"
MF "v[32:35],a[148:151],v[176:179],v[32:35]\n"
TR "a[244:245],v5 offset:8192\n"
"v_add_f32 v166,v166,v198\n"
MF "v[32:35],a[188:191],v[180:183],v[32:35]\n"
TR "a[246:247],v5 offset:10240\n"
"v_add_f32 v167,v167,v199\n"
MF "v[32:35],a[228:231],v[184:187],v[32:35]\n"
"ds_read2st64_b32 v[204:205],v13 offset0:36 offset1:37\n"
CV "v160,v160,v161\n"
MF "v[40:43],a[232:235],v[184:187],v[40:43]\n"
"s_add_u32 s2,s2,204800\n"
"ds_read2st64_b32 v[206:207],v13 offset0:38 offset1:39\n"
CV "v161,v162,v163\n"
MF "v[40:43],a[192:195],v[180:183],v[40:43]\n"
"s_add_u32 s87,s90,13312\n"
CV "v162,v164,v165\n"
CV "v163,v166,v167\n"
MF "v[40:43],a[152:155],v[176:179],v[40:43]\n"
LD "v[232:235],v9,s[40:43],s2 offen sc1\n"
MF "v[48:51],a[156:159],v[176:179],v[48:51]\n"
"buffer_store_dwordx4 v[160:163],v9,s[40:43],s87 offen sc1\n"
MF "v[48:51],a[196:199],v[180:183],v[48:51]\n"
MF "v[48:51],a[236:239],v[184:187],v[48:51]\n"
"s_waitcnt lgkmcnt(2)\n"
MF "v[52:55],a[236:239],a[244:247],v[52:55]\n"
"s_waitcnt lgkmcnt(0)\n"
TR "v[176:177],v6\n"
"v_add_f32 v168,v168,v200\n"
MF "v[52:55],a[196:199],a[240:243],v[52:55]\n"
TR "v[178:179],v6 offset:2048\n"
"v_add_f32 v169,v169,v201\n"
MF "v[52:55],a[156:159],v[188:191],v[52:55]\n"
"v_add_f32 v170,v170,v202\n"
"v_add_f32 v171,v171,v203\n"
MF "v[44:47],a[152:155],v[188:191],v[44:47]\n"
TR "v[180:181],v6 offset:4096\n"
"v_add_f32 v172,v172,v204\n"
MF "v[44:47],a[192:195],a[240:243],v[44:47]\n"
TR "v[182:183],v6 offset:6144\n"
"v_add_f32 v173,v173,v205\n"
MF "v[44:47],a[232:235],a[244:247],v[44:47]\n"
"v_add_f32 v174,v174,v206\n"
"v_add_f32 v175,v175,v207\n"
MF "v[36:39],a[228:231],a[244:247],v[36:39]\n"
TR "v[184:185],v6 offset:8192\n"
CV "v168,v168,v169\n"
MF "v[36:39],a[188:191],a[240:243],v[36:39]\n"
TR "v[186:187],v6 offset:10240\n"
CV "v169,v170,v171\n"
MF "v[36:39],a[148:151],v[188:191],v[36:39]\n"
"s_add_u32 s87,s90,14336\n"
CV "v170,v172,v173\n"
CV "v171,v174,v175\n"
MF "v[28:31],a[144:147],v[188:191],v[28:31]\n"
LD "v[208:211],v9,s[40:43],s82 offen sc1\n"
MF "v[28:31],a[184:187],a[240:243],v[28:31]\n"
MF "v[28:31],a[224:227],a[244:247],v[28:31]\n"
MF "v[20:23],a[220:223],a[244:247],v[20:23]\n"
"buffer_store_dwordx4 v[168:171],v9,s[40:43],s87 offen sc1\n"
MF "v[20:23],a[180:183],a[240:243],v[20:23]\n"
"s_mov_b32 s90,s91\n"
"s_add_u32 s91,s91,20480\n"
MF "v[20:23],a[140:143],v[188:191],v[20:23]\n"
"s_waitcnt lgkmcnt(0)\n"
MF "v[56:59],a[140:143],v[176:179],v[56:59]\n"
TR "v[188:189],v7\n"
MF "v[56:59],a[180:183],v[180:183],v[56:59]\n"
TR "v[190:191],v7 offset:2048\n"
MF "v[56:59],a[220:223],v[184:187],v[56:59]\n"
LD "v[212:215],v9,s[40:43],s82 offen offset:1024 sc1\n"
MF "v[64:67],a[224:227],v[184:187],v[64:67]\n"
TR "a[240:241],v7 offset:4096\n"
MF "v[64:67],a[184:187],v[180:183],v[64:67]\n"
TR "a[242:243],v7 offset:6144\n"
MF "v[64:67],a[144:147],v[176:179],v[64:67]\n"
MF "v[72:75],a[148:151],v[176:179],v[72:75]\n"
TR "a[244:245],v7 offset:8192\n"
MF "v[72:75],a[188:191],v[180:183],v[72:75]\n"
TR "a[246:247],v7 offset:10240\n"
MF "v[72:75],a[228:231],v[184:187],v[72:75]\n"
MF "v[80:83],a[232:235],v[184:187],v[80:83]\n"
MF "v[80:83],a[192:195],v[180:183],v[80:83]\n"
LD "v[216:219],v9,s[40:43],s82 offen offset:2048 sc1\n"
MF "v[80:83],a[152:155],v[176:179],v[80:83]\n"
MF "v[88:91],a[156:159],v[176:179],v[88:91]\n"
MF "v[88:91],a[196:199],v[180:183],v[88:91]\n"
"s_waitcnt lgkmcnt(0)\n"
MF "v[88:91],a[236:239],v[184:187],v[88:91]\n"
"s_barrier\n"
MF "v[92:95],a[236:239],a[244:247],v[92:95]\n"
TR "v[176:177],v0 offset:24576\n"
MF "v[92:95],a[196:199],a[240:243],v[92:95]\n"
"s_add_u32 s85,s85,1\n"
TR "v[178:179],v0 offset:26624\n"
MF "v[92:95],a[156:159],v[188:191],v[92:95]\n"
MF "v[84:87],a[152:155],v[188:191],v[84:87]\n"
TR "v[180:181],v0 offset:28672\n"
MF "v[84:87],a[192:195],a[240:243],v[84:87]\n"
TR "v[182:183],v0 offset:30720\n"
MF "v[84:87],a[232:235],a[244:247],v[84:87]\n"
LD "v[220:223],v9,s[40:43],s82 offen offset:3072 sc1\n"
MF "v[76:79],a[228:231],a[244:247],v[76:79]\n"
TR "v[184:185],v0 offset:32768\n"
MF "v[76:79],a[188:191],a[240:243],v[76:79]\n"
TR "v[186:187],v0 offset:34816\n"
MF "v[76:79],a[148:151],v[188:191],v[76:79]\n"
MF "v[68:71],a[144:147],v[188:191],v[68:71]\n"
MF "v[68:71],a[184:187],a[240:243],v[68:71]\n"
MF "v[68:71],a[224:227],a[244:247],v[68:71]\n"
MF "v[60:63],a[220:223],a[244:247],v[60:63]\n"
MF "v[60:63],a[180:183],a[240:243],v[60:63]\n"
MF "v[60:63],a[140:143],v[188:191],v[60:63]\n"
"s_waitcnt lgkmcnt(0)\n"
"s_cmp_lg_u32 s85,9\n"
"s_cbranch_scc1 .Lpl19\n"
".Lplast1:\n"
MF "v[96:99],v[176:179],a[0:3],v[252:255]\n"
"s_add_u32 m0,s79,73728\n"
TR "v[188:189],v1 offset:24576\n"
TR "v[190:191],v1 offset:26624\n"
MF "v[96:99],v[180:183],a[40:43],v[96:99]\n"
"s_nop 0\n"
TR "a[240:241],v1 offset:28672\n"
TR "a[242:243],v1 offset:30720\n"
MF "v[96:99],v[184:187],a[80:83],v[96:99]\n"
"s_add_u32 s82,s82,4096\n"
TR "a[244:245],v1 offset:32768\n"
TR "a[246:247],v1 offset:34816\n"
MF "v[104:107],v[184:187],a[84:87],v[252:255]\n"
LD "v[224:227],v9,s[40:43],s82 offen sc1\n"
MF "v[104:107],v[180:183],a[44:47],v[104:107]\n"
"s_nop 0\n"
"ds_write_addtid_b32 v56\n"
"ds_write_addtid_b32 v57 offset:256\n"
MF "v[104:107],v[176:179],a[4:7],v[104:107]\n"
"s_nop 0\n"
"ds_write_addtid_b32 v58 offset:512\n"
"ds_write_addtid_b32 v59 offset:768\n"
MF "v[112:115],v[176:179],a[8:11],v[252:255]\n"
"s_nop 0\n"
"ds_write_addtid_b32 v60 offset:1024\n"
"ds_write_addtid_b32 v61 offset:1280\n"
MF "v[112:115],v[180:183],a[48:51],v[112:115]\n"
"s_nop 0\n"
"ds_write_addtid_b32 v62 offset:1536\n"
"ds_write_addtid_b32 v63 offset:1792\n"
MF "v[112:115],v[184:187],a[88:91],v[112:115]\n"
"s_nop 0\n"
"ds_write_addtid_b32 v64 offset:2048\n"
"ds_write_addtid_b32 v65 offset:2304\n"
MF "v[120:123],v[184:187],a[92:95],v[252:255]\n"
"s_nop 0\n"
"ds_write_addtid_b32 v66 offset:2560\n"
"ds_write_addtid_b32 v67 offset:2816\n"
MF "v[120:123],v[180:183],a[52:55],v[120:123]\n"
"s_nop 0\n"
"ds_write_addtid_b32 v68 offset:3072\n"
"ds_write_addtid_b32 v69 offset:3328\n"
MF "v[120:123],v[176:179],a[12:15],v[120:123]\n"
"s_nop 0\n"
"ds_write_addtid_b32 v70 offset:3584\n"
"ds_write_addtid_b32 v71 offset:3840\n"
MF "v[128:131],v[176:179],a[16:19],v[252:255]\n"
"s_nop 0\n"
"ds_write_addtid_b32 v72 offset:4096\n"
"ds_write_addtid_b32 v73 offset:4352\n"
MF "v[128:131],v[180:183],a[56:59],v[128:131]\n"
"s_nop 0\n"
"ds_write_addtid_b32 v74 offset:4608\n"
"ds_write_addtid_b32 v75 offset:4864\n"
MF "v[128:131],v[184:187],a[96:99],v[128:131]\n"
"s_waitcnt lgkmcnt(15)\n"
"ds_write_addtid_b32 v76 offset:5120\n"
MF "v[132:135],a[244:247],a[96:99],v[252:255]\n"
"s_nop 0\n"
TR "v[176:177],v2 offset:24576\n"
TR "v[178:179],v2 offset:26624\n"
MF "v[132:135],a[240:243],a[56:59],v[132:135]\n"
"s_nop 0\n"
TR "v[180:181],v2 offset:28672\n"
TR "v[182:183],v2 offset:30720\n"
MF "v[132:135],v[188:191],a[16:19],v[132:135]\n"
"s_nop 0\n"
TR "v[184:185],v2 offset:32768\n"
TR "v[186:187],v2 offset:34816\n"
MF "v[124:127],v[188:191],a[12:15],v[252:255]\n"
"s_nop 0\n"
"ds_write_addtid_b32 v77 offset:5376\n"
"ds_write_addtid_b32 v78 offset:5632\n"
MF "v[124:127],a[240:243],a[52:55],v[124:127]\n"
"s_nop 0\n"
"ds_write_addtid_b32 v79 offset:5888\n"
"ds_write_addtid_b32 v80 offset:6144\n"
MF "v[124:127],a[244:247],a[92:95],v[124:127]\n"
"s_nop 0\n"
"ds_write_addtid_b32 v81 offset:6400\n"
"ds_write_addtid_b32 v82 offset:6656\n"
MF "v[116:119],a[244:247],a[88:91],v[252:255]\n"
"s_nop 0\n"
"ds_write_addtid_b32 v83 offset:6912\n"
"ds_write_addtid_b32 v84 offset:7168\n"
MF "v[116:119],a[240:243],a[48:51],v[116:119]\n"
"s_nop 0\n"
"ds_write_addtid_b32 v85 offset:7424\n"
"ds_write_addtid_b32 v86 offset:7680\n"
MF "v[116:119],v[188:191],a[8:11],v[116:119]\n"
"s_nop 0\n"
"ds_write_addtid_b32 v87 offset:7936\n"
"ds_write_addtid_b32 v88 offset:8192\n"
MF "v[108:111],v[188:191],a[4:7],v[252:255]\n"
"s_nop 0\n"
"ds_write_addtid_b32 v89 offset:8448\n"
"ds_write_addtid_b32 v90 offset:8704\n"
MF "v[108:111],a[240:243],a[44:47],v[108:111]\n"
"s_nop 0\n"
"ds_write_addtid_b32 v91 offset:8960\n"
"ds_write_addtid_b32 v92 offset:9216\n"
MF "v[108:111],a[244:247],a[84:87],v[108:111]\n"
"s_nop 0\n"
"ds_write_addtid_b32 v93 offset:9472\n"
"ds_write_addtid_b32 v94 offset:9728\n"
MF "v[100:103],a[244:247],a[80:83],v[252:255]\n"
"ds_write_addtid_b32 v95 offset:9984\n"
MF "v[100:103],a[240:243],a[40:43],v[100:103]\n"
MF "v[100:103],v[188:191],a[0:3],v[100:103]\n"
"s_waitcnt lgkmcnt(15)\n"
MF "v[136:139],v[176:179],a[0:3],v[252:255]\n"
"s_nop 0\n"
TR "v[188:189],v3 offset:24576\n"
TR "v[190:191],v3 offset:26624\n"
MF "v[136:139],v[180:183],a[40:43],v[136:139]\n"
"s_nop 0\n"
TR "a[240:241],v3 offset:28672\n"
TR "a[242:243],v3 offset:30720\n"
MF "v[136:139],v[184:187],a[80:83],v[136:139]\n"
"s_nop 0\n"
TR "a[244:245],v3 offset:32768\n"
TR "a[246:247],v3 offset:34816\n"
"s_waitcnt lgkmcnt(0)\n"
MF "v[144:147],v[184:187],a[84:87],v[252:255]\n"
"s_barrier\n"
MF "v[144:147],v[180:183],a[44:47],v[144:147]\n"
"ds_read2st64_b32 v[56:57],v13 offset0:192 offset1:193\n"
MF "v[144:147],v[176:179],a[4:7],v[144:147]\n"
"ds_read2st64_b32 v[58:59],v13 offset0:194 offset1:195\n"
MF "v[152:155],v[176:179],a[8:11],v[252:255]\n"
"ds_read2st64_b32 v[60:61],v13 offset0:196 offset1:197\n"
MF "v[152:155],v[180:183],a[48:51],v[152:155]\n"
"ds_read2st64_b32 v[62:63],v13 offset0:198 offset1:199\n"
MF "v[152:155],v[184:187],a[88:91],v[152:155]\n"
"ds_read2st64_b32 v[64:65],v13 offset0:200 offset1:201\n"
MF "v[160:163],v[184:187],a[92:95],v[252:255]\n"
"ds_read2st64_b32 v[66:67],v13 offset0:202 offset1:203\n"
MF "v[160:163],v[180:183],a[52:55],v[160:163]\n"
"ds_read2st64_b32 v[68:69],v13 offset0:204 offset1:205\n"
MF "v[160:163],v[176:179],a[12:15],v[160:163]\n"
"ds_read2st64_b32 v[70:71],v13 offset0:206 offset1:207\n"
MF "v[168:171],v[176:179],a[16:19],v[252:255]\n"
"s_waitcnt lgkmcnt(4)\n"
"ds_read2st64_b32 v[72:73],v13 offset0:208 offset1:209\n"
"v_add_f32 v16,v16,v56\n"
MF "v[168:171],v[180:183],a[56:59],v[168:171]\n"
"ds_read2st64_b32 v[74:75],v13 offset0:210 offset1:211\n"
"s_waitcnt lgkmcnt(0)\n"
MF "v[168:171],v[184:187],a[96:99],v[168:171]\n"
"s_barrier\n"
MF "v[172:175],a[244:247],a[96:99],v[252:255]\n"
"s_nop 0\n"
TR "v[176:177],v0\n"
TR "v[178:179],v0 offset:2048\n"
MF "v[172:175],a[240:243],a[56:59],v[172:175]\n"
"s_nop 0\n"
TR "v[180:181],v0 offset:4096\n"
TR "v[182:183],v0 offset:6144\n"
MF "v[172:175],v[188:191],a[16:19],v[172:175]\n"
"s_nop 0\n"
TR "v[184:185],v0 offset:8192\n"
TR "v[186:187],v0 offset:10240\n"
MF "v[164:167],v[188:191],a[12:15],v[252:255]\n"
"ds_read2st64_b32 v[76:77],v13 offset0:212 offset1:213\n"
"v_add_f32 v17,v17,v57\n"
MF "v[164:167],a[240:243],a[52:55],v[164:167]\n"
"ds_read2st64_b32 v[78:79],v13 offset0:214 offset1:215\n"
"v_add_f32 v18,v18,v58\n"
MF "v[164:167],a[244:247],a[92:95],v[164:167]\n"
"ds_read2st64_b32 v[80:81],v13 offset0:216 offset1:217\n"
"v_add_f32 v19,v19,v59\n"
MF "v[156:159],a[244:247],a[88:91],v[252:255]\n"
"ds_read2st64_b32 v[82:83],v13 offset0:218 offset1:219\n"
"v_add_f32 v20,v20,v60\n"
MF "v[156:159],a[240:243],a[48:51],v[156:159]\n"
"ds_read2st64_b32 v[84:85],v13 offset0:220 offset1:221\n"
"v_add_f32 v21,v21,v61\n"
MF "v[156:159],v[188:191],a[8:11],v[156:159]\n"
"ds_read2st64_b32 v[86:87],v13 offset0:222 offset1:223\n"
"v_add_f32 v22,v22,v62\n"
MF "v[148:151],v[188:191],a[4:7],v[252:255]\n"
"s_waitcnt vmcnt(4)\n"
"v_mov_b32 v14,s76\n"
"s_add_u32 s76,s76,1\n"
MF "v[148:151],a[240:243],a[44:47],v[148:151]\n"
".Lpg34h:\n"
"s_cbranch_execz .Lisr4\n"
"buffer_store_dword v14,v15,s[40:43],s94 offen sc1\n"
MF "v[148:151],a[244:247],a[84:87],v[148:151]\n"
"ds_read2st64_b32 v[88:89],v13 offset0:224 offset1:225\n"
"v_add_f32 v23,v23,v63\n"
MF "v[140:143],a[244:247],a[80:83],v[252:255]\n"
"ds_read2st64_b32 v[90:91],v13 offset0:226 offset1:227\n"
CV "v16,v16,v17\n"
MF "v[140:143],a[240:243],a[40:43],v[140:143]\n"
"ds_read2st64_b32 v[92:93],v13 offset0:228 offset1:229\n"
CV "v17,v18,v19\n"
MF "v[140:143],v[188:191],a[0:3],v[140:143]\n"
"s_waitcnt lgkmcnt(9)\n"
"ds_read2st64_b32 v[94:95],v13 offset0:230 offset1:231\n"
CV "v18,v20,v21\n"
MF "v[96:99],v[176:179],a[120:123],v[96:99]\n"
"s_nop 0\n"
TR "v[188:189],v1\n"
TR "v[190:191],v1 offset:2048\n"
MF "v[96:99],v[180:183],a[160:163],v[96:99]\n"
"s_nop 0\n"
TR "a[240:241],v1 offset:4096\n"
TR "a[242:243],v1 offset:6144\n"
MF "v[96:99],v[184:187],a[200:203],v[96:99]\n"
"s_nop 0\n"
TR "a[244:245],v1 offset:8192\n"
TR "a[246:247],v1 offset:10240\n"
MF "v[104:107],v[184:187],a[204:207],v[104:107]\n"
"s_add_u32 s87,s90,0\n"
CV "v19,v22,v23\n"
MF "v[104:107],v[180:183],a[164:167],v[104:107]\n"
"buffer_store_dwordx4 v[16:19],v9,s[40:43],s87 offen sc1\n"
MF "v[104:107],v[176:179],a[124:127],v[104:107]\n"
"v_add_f32 v24,v24,v64\n"
"v_add_f32 v25,v25,v65\n"
MF "v[112:115],v[176:179],a[128:131],v[112:115]\n"
"v_add_f32 v26,v26,v66\n"
"v_add_f32 v27,v27,v67\n"
MF "v[112:115],v[180:183],a[168:171],v[112:115]\n"
"v_add_f32 v28,v28,v68\n"
"v_add_f32 v29,v29,v69\n"
MF "v[112:115],v[184:187],a[208:211],v[112:115]\n"
"v_add_f32 v30,v30,v70\n"
"v_add_f32 v31,v31,v71\n"
MF "v[120:123],v[184:187],a[212:215],v[120:123]\n"
CV "v24,v24,v25\n"
CV "v25,v26,v27\n"
MF "v[120:123],v[180:183],a[172:175],v[120:123]\n"
"s_add_u32 s87,s90,1024\n"
CV "v26,v28,v29\n"
CV "v27,v30,v31\n"
MF "v[120:123],v[176:179],a[132:135],v[120:123]\n"
"s_waitcnt lgkmcnt(14)\n"
"buffer_store_dwordx4 v[24:27],v9,s[40:43],s87 offen sc1\n"
MF "v[128:131],v[176:179],a[136:139],v[128:131]\n"
"v_add_f32 v32,v32,v72\n"
"v_add_f32 v33,v33,v73\n"
MF "v[128:131],v[180:183],a[176:179],v[128:131]\n"
"v_add_f32 v34,v34,v74\n"
"v_add_f32 v35,v35,v75\n"
MF "v[128:131],v[184:187],a[216:219],v[128:131]\n"
"s_waitcnt lgkmcnt(0)\n"
"v_add_f32 v36,v36,v76\n"
"v_add_f32 v37,v37,v77\n"
MF "v[132:135],a[244:247],a[216:219],v[132:135]\n"
"s_nop 0\n"
TR "v[176:177],v2\n"
TR "v[178:179],v2 offset:2048\n"
MF "v[132:135],a[240:243],a[176:179],v[132:135]\n"
"s_nop 0\n"
TR "v[180:181],v2 offset:4096\n"
TR "v[182:183],v2 offset:6144\n"
MF "v[132:135],v[188:191],a[136:139],v[132:135]\n"
"s_nop 0\n"
TR "v[184:185],v2 offset:8192\n"
TR "v[186:187],v2 offset:10240\n"
MF "v[124:127],v[188:191],a[132:135],v[124:127]\n"
"v_add_f32 v38,v38,v78\n"
"v_add_f32 v39,v39,v79\n"
MF "v[124:127],a[240:243],a[172:175],v[124:127]\n"
CV "v32,v32,v33\n"
CV "v33,v34,v35\n"
MF "v[124:127],a[244:247],a[212:215],v[124:127]\n"
"s_add_u32 s87,s90,2048\n"
CV "v34,v36,v37\n"
CV "v35,v38,v39\n"
MF "v[116:119],a[244:247],a[208:211],v[116:119]\n"
"buffer_store_dwordx4 v[32:35],v9,s[40:43],s87 offen sc1\n"
MF "v[116:119],a[240:243],a[168:171],v[116:119]\n"
"v_add_f32 v40,v40,v80\n"
"v_add_f32 v41,v41,v81\n"
MF "v[116:119],v[188:191],a[128:131],v[116:119]\n"
"v_add_f32 v42,v42,v82\n"
"v_add_f32 v43,v43,v83\n"
MF "v[108:111],v[188:191],a[124:127],v[108:111]\n"
"v_add_f32 v44,v44,v84\n"
"v_add_f32 v45,v45,v85\n"
MF "v[108:111],a[240:243],a[164:167],v[108:111]\n"
"v_add_f32 v46,v46,v86\n"
"v_add_f32 v47,v47,v87\n"
MF "v[108:111],a[244:247],a[204:207],v[108:111]\n"
CV "v40,v40,v41\n"
CV "v41,v42,v43\n"
MF "v[100:103],a[244:247],a[200:203],v[100:103]\n"
"s_add_u32 s87,s90,3072\n"
CV "v42,v44,v45\n"
CV "v43,v46,v47\n"
MF "v[100:103],a[240:243],a[160:163],v[100:103]\n"
"buffer_store_dwordx4 v[40:43],v9,s[40:43],s87 offen sc1\n"
MF "v[100:103],v[188:191],a[120:123],v[100:103]\n"
"s_waitcnt lgkmcnt(0)\n"
"v_add_f32 v48,v48,v88\n"
"v_add_f32 v49,v49,v89\n"
MF "v[136:139],v[176:179],a[120:123],v[136:139]\n"
"s_nop 0\n"
TR "v[188:189],v3\n"
TR "v[190:191],v3 offset:2048\n"
MF "v[136:139],v[180:183],a[160:163],v[136:139]\n"
"s_nop 0\n"
TR "a[240:241],v3 offset:4096\n"
TR "a[242:243],v3 offset:6144\n"
MF "v[136:139],v[184:187],a[200:203],v[136:139]\n"
"s_nop 0\n"
TR "a[244:245],v3 offset:8192\n"
TR "a[246:247],v3 offset:10240\n"
MF "v[144:147],v[184:187],a[204:207],v[144:147]\n"
"v_add_f32 v50,v50,v90\n"
"v_add_f32 v51,v51,v91\n"
MF "v[144:147],v[180:183],a[164:167],v[144:147]\n"
"v_add_f32 v52,v52,v92\n"
"v_add_f32 v53,v53,v93\n"
MF "v[144:147],v[176:179],a[124:127],v[144:147]\n"
"v_add_f32 v54,v54,v94\n"
"v_add_f32 v55,v55,v95\n"
MF "v[152:155],v[176:179],a[128:131],v[152:155]\n"
CV "v48,v48,v49\n"
CV "v49,v50,v51\n"
MF "v[152:155],v[180:183],a[168:171],v[152:155]\n"
"s_add_u32 s87,s90,4096\n"
CV "v50,v52,v53\n"
CV "v51,v54,v55\n"
MF "v[152:155],v[184:187],a[208:211],v[152:155]\n"
"buffer_store_dwordx4 v[48:51],v9,s[40:43],s87 offen sc1\n"
MF "v[160:163],v[184:187],a[212:215],v[160:163]\n"
MF "v[160:163],v[180:183],a[172:175],v[160:163]\n"
MF "v[160:163],v[176:179],a[132:135],v[160:163]\n"
MF "v[168:171],v[176:179],a[136:139],v[168:171]\n"
MF "v[168:171],v[180:183],a[176:179],v[168:171]\n"
"s_waitcnt lgkmcnt(0)\n"
MF "v[168:171],v[184:187],a[216:219],v[168:171]\n"
"s_barrier\n"
MF "v[172:175],a[244:247],a[216:219],v[172:175]\n"
"s_nop 0\n"
TR "v[176:177],v0 offset:49152\n"
TR "v[178:179],v0 offset:51200\n"
MF "v[172:175],a[240:243],a[176:179],v[172:175]\n"
"s_nop 0\n"
TR "v[180:181],v0 offset:53248\n"
TR "v[182:183],v0 offset:55296\n"
MF "v[172:175],v[188:191],a[136:139],v[172:175]\n"
"s_nop 0\n"
TR "v[184:185],v0 offset:57344\n"
TR "v[186:187],v0 offset:59392\n"
MF "v[164:167],v[188:191],a[132:135],v[164:167]\n"
"buffer_load_dword v10,v12,s[40:43],s86 offen sc1\n"
MF "v[164:167],a[240:243],a[172:175],v[164:167]\n"
MF "v[164:167],a[244:247],a[212:215],v[164:167]\n"
LD "a[0:3],v9,s[40:43],s3 offen sc1\n"
MF "v[156:159],a[244:247],a[208:211],v[156:159]\n"
MF "v[156:159],a[240:243],a[168:171],v[156:159]\n"
MF "v[156:159],v[188:191],a[128:131],v[156:159]\n"
MF "v[148:151],v[188:191],a[124:127],v[148:151]\n"
MF "v[148:151],a[240:243],a[164:167],v[148:151]\n"
"s_add_u32 s3,s3,204800\n"
MF "v[148:151],a[244:247],a[204:207],v[148:151]\n"
LD "a[4:7],v9,s[40:43],s3 offen sc1\n"
MF "v[140:143],a[244:247],a[200:203],v[140:143]\n"
MF "v[140:143],a[240:243],a[160:163],v[140:143]\n"
MF "v[140:143],v[188:191],a[120:123],v[140:143]\n"
"s_waitcnt lgkmcnt(0)\n"
MF "v[96:99],v[176:179],a[20:23],v[96:99]\n"
"s_nop 0\n"
TR "v[188:189],v1 offset:49152\n"
TR "v[190:191],v1 offset:51200\n"
MF "v[96:99],v[180:183],a[60:63],v[96:99]\n"
"s_nop 0\n"
TR "a[240:241],v1 offset:53248\n"
TR "a[242:243],v1 offset:55296\n"
MF "v[96:99],v[184:187],a[100:103],v[96:99]\n"
"s_add_u32 s3,s3,204800\n"
TR "a[244:245],v1 offset:57344\n"
TR "a[246:247],v1 offset:59392\n"
MF "v[104:107],v[184:187],a[104:107],v[104:107]\n"
LD "a[8:11],v9,s[40:43],s3 offen sc1\n"
MF "v[104:107],v[180:183],a[64:67],v[104:107]\n"
MF "v[104:107],v[176:179],a[24:27],v[104:107]\n"
"s_waitcnt vmcnt(10)\n"
"v_mov_b32 v236,v228\n"
"v_mov_b32 v237,v229\n"
MF "v[136:139],v[208:211],a[252:255],v[136:139]\n"
MF "v[144:147],v[212:215],a[252:255],v[144:147]\n"
MF "v[152:155],v[216:219],a[252:255],v[152:155]\n"
MF "v[160:163],v[220:223],a[252:255],v[160:163]\n"
MF "v[168:171],v[224:227],a[252:255],v[168:171]\n"
MF "v[140:143],v[208:211],a[248:251],v[140:143]\n"
MF "v[148:151],v[212:215],a[248:251],v[148:151]\n"
MF "v[156:159],v[216:219],a[248:251],v[156:159]\n"
MF "v[164:167],v[220:223],a[248:251],v[164:167]\n"
MF "v[172:175],v[224:227],a[248:251],v[172:175]\n"
MF "v[248:251],v[230:233],a[250:253],v[248:251]\n"
MF "v[244:247],v[234:237],a[250:253],v[244:247]\n"
MF "v[112:115],v[176:179],a[28:31],v[112:115]\n"
MF "v[112:115],v[180:183],a[68:71],v[112:115]\n"
"s_add_u32 s3,s3,204800\n"
MF "v[112:115],v[184:187],a[108:111],v[112:115]\n"
LD "a[12:15],v9,s[40:43],s3 offen sc1\n"
MF "v[120:123],v[184:187],a[112:115],v[120:123]\n"
"s_nop 1\n"
CV "v244,v244,v245\n"
CV "v245,v246,v247\n"
MF "v[120:123],v[180:183],a[72:75],v[120:123]\n"
"s_bfe_u32 s87,s22,0x30014\n"
CV "v246,v248,v249\n"
CV "v247,v250,v251\n"
MF "v[120:123],v[176:179],a[32:35],v[120:123]\n"
"s_lshl_b32 s87,s87,7\n"
"v_permlane16_swap_b32 v244,v246\n"
"v_permlane16_swap_b32 v245,v247\n"
MF "v[128:131],v[176:179],a[36:39],v[128:131]\n"
"buffer_store_dwordx4 v[244:247],v8,s[44:47],s0 offen sc1\n"
MF "v[128:131],v[180:183],a[76:79],v[128:131]\n"
"s_add_u32 s3,s3,204800\n"
MF "v[128:131],v[184:187],a[116:119],v[128:131]\n"
"s_waitcnt lgkmcnt(0)\n"
LD "a[40:43],v9,s[40:43],s3 offen sc1\n"
MF "v[132:135],a[244:247],a[116:119],v[132:135]\n"
"s_and_b32 s60,s56,1\n"
TR "v[176:177],v2 offset:49152\n"
TR "v[178:179],v2 offset:51200\n"
MF "v[132:135],a[240:243],a[76:79],v[132:135]\n"
"s_mul_i32 s60,s60,204800\n"
TR "v[180:181],v2 offset:53248\n"
TR "v[182:183],v2 offset:55296\n"
MF "v[132:135],v[188:191],a[36:39],v[132:135]\n"
"s_add_u32 s87,s87,s60\n"
TR "v[184:185],v2 offset:57344\n"
TR "v[186:187],v2 offset:59392\n"
MF "v[124:127],v[188:191],a[32:35],v[124:127]\n"
"s_mov_b32 s71,204800\n"
MF "v[124:127],a[240:243],a[72:75],v[124:127]\n"
"s_mov_b32 s66,204800\n"
MF "v[124:127],a[244:247],a[112:115],v[124:127]\n"
"s_add_u32 s72,s78,2\n"
MF "v[116:119],a[244:247],a[108:111],v[116:119]\n"
"s_add_u32 s3,s3,204800\n"
MF "v[116:119],a[240:243],a[68:71],v[116:119]\n"
LD "a[44:47],v9,s[40:43],s3 offen sc1\n"
MF "v[116:119],v[188:191],a[28:31],v[116:119]\n"
"s_and_b32 s72,s72,15\n"
MF "v[108:111],v[188:191],a[24:27],v[108:111]\n"
"s_add_u32 s16,s78,6\n"
MF "v[108:111],a[240:243],a[64:67],v[108:111]\n"
"s_and_b32 s16,s16,15\n"
MF "v[108:111],a[244:247],a[104:107],v[108:111]\n"
"s_add_u32 s17,s78,4\n"
MF "v[100:103],a[244:247],a[100:103],v[100:103]\n"
"s_and_b32 s17,s17,15\n"
MF "v[100:103],a[240:243],a[60:63],v[100:103]\n"
"s_add_u32 s3,s3,204800\n"
MF "v[100:103],v[188:191],a[20:23],v[100:103]\n"
"s_waitcnt lgkmcnt(0)\n"
LD "a[48:51],v9,s[40:43],s3 offen sc1\n"
MF "v[136:139],v[176:179],a[20:23],v[136:139]\n"
"s_bfe_u32 s60,s22,0x10014\n"
TR "v[188:189],v3 offset:49152\n"
TR "v[190:191],v3 offset:51200\n"
MF "v[136:139],v[180:183],a[60:63],v[136:139]\n"
"s_lshl_b32 s60,s60,5\n"
TR "a[240:241],v3 offset:53248\n"
TR "a[242:243],v3 offset:55296\n"
MF "v[136:139],v[184:187],a[100:103],v[136:139]\n"
"s_nop 0\n"
TR "a[244:245],v3 offset:57344\n"
TR "a[246:247],v3 offset:59392\n"
MF "v[144:147],v[184:187],a[104:107],v[144:147]\n"
"v_xor_b32 v242,s60,v8\n"
MF "v[144:147],v[180:183],a[64:67],v[144:147]\n"
"s_add_u32 s3,s3,204800\n"
"v_mov_b32 v14,s76\n"
"s_add_u32 s76,s76,1\n"
MF "v[144:147],v[176:179],a[24:27],v[144:147]\n"
LD "a[52:55],v9,s[40:43],s3 offen sc1\n"
MF "v[152:155],v[176:179],a[28:31],v[152:155]\n"
MF "v[152:155],v[180:183],a[68:71],v[152:155]\n"
MF "v[152:155],v[184:187],a[108:111],v[152:155]\n"
MF "v[160:163],v[184:187],a[112:115],v[160:163]\n"
MF "v[160:163],v[180:183],a[72:75],v[160:163]\n"
MF "v[160:163],v[176:179],a[32:35],v[160:163]\n"
MF "v[168:171],v[176:179],a[36:39],v[168:171]\n"
MF "v[168:171],v[180:183],a[76:79],v[168:171]\n"
"s_waitcnt vmcnt(9) lgkmcnt(0)\n"
MF "v[168:171],v[184:187],a[116:119],v[168:171]\n"
"s_barrier\n"
MF "v[172:175],a[244:247],a[116:119],v[172:175]\n"
"buffer_store_dword v14,v15,s[40:43],s94 offen sc1\n"
MF "v[172:175],a[240:243],a[76:79],v[172:175]\n"
"s_nop 0\n"
TR "v[176:177],v6\n"
TR "v[178:179],v6 offset:2048\n"
MF "v[172:175],v[188:191],a[36:39],v[172:175]\n"
"s_nop 0\n"
TR "v[180:181],v6 offset:4096\n"
TR "v[182:183],v6 offset:6144\n"
MF "v[164:167],v[188:191],a[32:35],v[164:167]\n"
"s_nop 0\n"
TR "v[184:185],v6 offset:8192\n"
TR "v[186:187],v6 offset:10240\n"
MF "v[164:167],a[240:243],a[72:75],v[164:167]\n"
"v_sub_u32 v10,v10,v11\n"
MF "v[164:167],a[244:247],a[112:115],v[164:167]\n"
"v_readlane_b32 s16,v10,s16\n"
MF "v[156:159],a[244:247],a[108:111],v[156:159]\n"
"s_cmp_lt_u32 s16,9\n"
"s_cselect_b32 s16,0xf0000000,0\n"
MF "v[156:159],a[240:243],a[68:71],v[156:159]\n"
"v_readlane_b32 s17,v10,s17\n"
MF "v[156:159],v[188:191],a[28:31],v[156:159]\n"
"s_cmp_lt_u32 s17,10\n"
"s_cselect_b32 s17,0xf0000000,0\n"
MF "v[148:151],v[188:191],a[24:27],v[148:151]\n"
"s_bfe_u32 s62,s22,0x30014\n"
MF "v[148:151],a[240:243],a[64:67],v[148:151]\n"
"s_add_u32 s62,s62,3\n"
"s_and_b32 s62,s62,7\n"
"s_mul_i32 s62,s62,204800\n"
MF "v[148:151],a[244:247],a[104:107],v[148:151]\n"
"s_add_u32 s62,s62,s73\n"
"s_add_u32 s62,s62,122880\n"
"s_or_b32 s62,s62,s16\n"
MF "v[140:143],a[244:247],a[100:103],v[140:143]\n"
LD "v[56:59],v9,s[40:43],s62 offen sc1\n"
MF "v[140:143],a[240:243],a[60:63],v[140:143]\n"
LD "v[64:67],v9,s[40:43],s62 offen offset:1024 sc1\n"
MF "v[140:143],v[188:191],a[20:23],v[140:143]\n"
"s_waitcnt lgkmcnt(0)\n"
LD "v[72:75],v9,s[40:43],s62 offen offset:2048 sc1\n"
MF "v[136:139],v[176:179],a[140:143],v[136:139]\n"
"s_nop 0\n"
TR "v[188:189],v7\n"
TR "v[190:191],v7 offset:2048\n"
MF "v[136:139],v[180:183],a[180:183],v[136:139]\n"
"s_nop 0\n"
TR "a[240:241],v7 offset:4096\n"
TR "a[242:243],v7 offset:6144\n"
MF "v[136:139],v[184:187],a[220:223],v[136:139]\n"
"s_nop 0\n"
TR "a[244:245],v7 offset:8192\n"
TR "a[246:247],v7 offset:10240\n"
MF "v[144:147],v[184:187],a[224:227],v[144:147]\n"
LD "v[80:83],v9,s[40:43],s62 offen offset:3072 sc1\n"
MF "v[144:147],v[180:183],a[184:187],v[144:147]\n"
MF "v[144:147],v[176:179],a[144:147],v[144:147]\n"
"s_add_u32 s62,s62,4096\n"
MF "v[152:155],v[176:179],a[148:151],v[152:155]\n"
LD "v[88:91],v9,s[40:43],s62 offen sc1\n"
MF "v[152:155],v[180:183],a[188:191],v[152:155]\n"
"s_bfe_u32 s62,s22,0x30014\n"
"ds_write_addtid_b32 v136\n"
"ds_write_addtid_b32 v137 offset:256\n"
MF "v[152:155],v[184:187],a[228:231],v[152:155]\n"
"s_add_u32 s62,s62,2\n"
"s_and_b32 s62,s62,7\n"
"s_mul_i32 s62,s62,204800\n"
MF "v[160:163],v[184:187],a[232:235],v[160:163]\n"
"s_add_u32 s62,s62,s73\n"
"s_add_u32 s62,s62,143360\n"
"s_or_b32 s62,s62,s17\n"
MF "v[160:163],v[180:183],a[192:195],v[160:163]\n"
LD "v[60:63],v9,s[40:43],s62 offen sc1\n"
MF "v[160:163],v[176:179],a[152:155],v[160:163]\n"
"s_nop 0\n"
"ds_write_addtid_b32 v138 offset:512\n"
"ds_write_addtid_b32 v139 offset:768\n"
MF "v[168:171],v[176:179],a[156:159],v[168:171]\n"
LD "v[68:71],v9,s[40:43],s62 offen offset:1024 sc1\n"
MF "v[168:171],v[180:183],a[196:199],v[168:171]\n"
"s_nop 0\n"
"ds_write_addtid_b32 v144 offset:2048\n"
"ds_write_addtid_b32 v145 offset:2304\n"
MF "v[168:171],v[184:187],a[236:239],v[168:171]\n"
"s_waitcnt lgkmcnt(6)\n"
LD "v[76:79],v9,s[40:43],s62 offen offset:2048 sc1\n"
MF "v[140:143],v[188:191],a[140:143],v[140:143]\n"
"s_nop 0\n"
TR "v[176:177],v4\n"
TR "v[178:179],v4 offset:2048\n"
MF "v[140:143],a[240:243],a[180:183],v[140:143]\n"
"s_nop 0\n"
TR "v[180:181],v4 offset:4096\n"
TR "v[182:183],v4 offset:6144\n"
MF "v[140:143],a[244:247],a[220:223],v[140:143]\n"
"s_nop 0\n"
TR "v[184:185],v4 offset:8192\n"
TR "v[186:187],v4 offset:10240\n"
MF "v[148:151],a[244:247],a[224:227],v[148:151]\n"
LD "v[84:87],v9,s[40:43],s62 offen offset:3072 sc1\n"
MF "v[148:151],a[240:243],a[184:187],v[148:151]\n"
"s_nop 0\n"
"ds_write_addtid_b32 v146 offset:2560\n"
"ds_write_addtid_b32 v147 offset:2816\n"
MF "v[148:151],v[188:191],a[144:147],v[148:151]\n"
"s_add_u32 s62,s62,4096\n"
"ds_write_addtid_b32 v152 offset:4096\n"
"ds_write_addtid_b32 v153 offset:4352\n"
MF "v[156:159],v[188:191],a[148:151],v[156:159]\n"
LD "v[92:95],v9,s[40:43],s62 offen sc1\n"
MF "v[156:159],a[240:243],a[188:191],v[156:159]\n"
"s_nop 0\n"
"ds_write_addtid_b32 v154 offset:4608\n"
"ds_write_addtid_b32 v155 offset:4864\n"
MF "v[156:159],a[244:247],a[228:231],v[156:159]\n"
"s_nop 0\n"
"ds_write_addtid_b32 v160 offset:6144\n"
"ds_write_addtid_b32 v161 offset:6400\n"
MF "v[164:167],a[244:247],a[232:235],v[164:167]\n"
"s_nop 0\n"
"ds_write_addtid_b32 v162 offset:6656\n"
"ds_write_addtid_b32 v163 offset:6912\n"
MF "v[164:167],a[240:243],a[192:195],v[164:167]\n"
"s_nop 0\n"
"ds_write_addtid_b32 v168 offset:8192\n"
"ds_write_addtid_b32 v169 offset:8448\n"
MF "v[164:167],v[188:191],a[152:155],v[164:167]\n"
"buffer_load_dword v10,v12,s[40:43],s86 offen sc1\n"
MF "v[172:175],v[188:191],a[156:159],v[172:175]\n"
"s_nop 0\n"
"ds_write_addtid_b32 v170 offset:8704\n"
"ds_write_addtid_b32 v171 offset:8960\n"
MF "v[172:175],a[240:243],a[196:199],v[172:175]\n"
"s_nop 0\n"
"ds_write_addtid_b32 v140 offset:1024\n"
"ds_write_addtid_b32 v141 offset:1280\n"
MF "v[172:175],a[244:247],a[236:239],v[172:175]\n"
"s_waitcnt lgkmcnt(15)\n"
"ds_write_addtid_b32 v142 offset:1536\n"
"ds_write_addtid_b32 v143 offset:1792\n"
MF "v[96:99],v[176:179],a[140:143],v[96:99]\n"
"s_nop 0\n"
TR "v[188:189],v5\n"
TR "v[190:191],v5 offset:2048\n"
MF "v[96:99],v[180:183],a[180:183],v[96:99]\n"
"s_nop 0\n"
TR "a[240:241],v5 offset:4096\n"
TR "a[242:243],v5 offset:6144\n"
MF "v[96:99],v[184:187],a[220:223],v[96:99]\n"
"s_nop 0\n"
TR "a[244:245],v5 offset:8192\n"
TR "a[246:247],v5 offset:10240\n"
MF "v[104:107],v[184:187],a[224:227],v[104:107]\n"
"s_nop 0\n"
"ds_write_addtid_b32 v148 offset:3072\n"
"ds_write_addtid_b32 v149 offset:3328\n"
MF "v[104:107],v[180:183],a[184:187],v[104:107]\n"
"s_nop 0\n"
"ds_write_addtid_b32 v150 offset:3584\n"
"ds_write_addtid_b32 v151 offset:3840\n"
MF "v[104:107],v[176:179],a[144:147],v[104:107]\n"
"s_nop 0\n"
"ds_write_addtid_b32 v156 offset:5120\n"
"ds_write_addtid_b32 v157 offset:5376\n"
MF "v[112:115],v[176:179],a[148:151],v[112:115]\n"
"s_nop 0\n"
"ds_write_addtid_b32 v158 offset:5632\n"
"ds_write_addtid_b32 v159 offset:5888\n"
MF "v[112:115],v[180:183],a[188:191],v[112:115]\n"
"ds_write_addtid_b32 v164 offset:7168\n"
MF "v[112:115],v[184:187],a[228:231],v[112:115]\n"
"ds_write_addtid_b32 v165 offset:7424\n"
MF "v[120:123],v[184:187],a[232:235],v[120:123]\n"
"ds_write_addtid_b32 v166 offset:7680\n"
MF "v[120:123],v[180:183],a[192:195],v[120:123]\n"
"ds_write_addtid_b32 v167 offset:7936\n"
MF "v[120:123],v[176:179],a[152:155],v[120:123]\n"
"ds_write_addtid_b32 v172 offset:9216\n"
MF "v[128:131],v[176:179],a[156:159],v[128:131]\n"
"ds_write_addtid_b32 v173 offset:9472\n"
MF "v[128:131],v[180:183],a[196:199],v[128:131]\n"
"ds_write_addtid_b32 v174 offset:9728\n"
MF "v[128:131],v[184:187],a[236:239],v[128:131]\n"
"s_waitcnt lgkmcnt(15)\n"
"ds_write_addtid_b32 v175 offset:9984\n"
MF "v[100:103],v[188:191],a[140:143],v[100:103]\n"
MF "v[100:103],a[240:243],a[180:183],v[100:103]\n"
MF "v[100:103],a[244:247],a[220:223],v[100:103]\n"
MF "v[108:111],a[244:247],a[224:227],v[108:111]\n"
"s_waitcnt lgkmcnt(0)\n"
MF "v[108:111],a[240:243],a[184:187],v[108:111]\n"
"s_barrier\n"
MF "v[108:111],v[188:191],a[144:147],v[108:111]\n"
"ds_read2st64_b32 v[16:17],v13 offset0:192 offset1:193\n"
MF "v[116:119],v[188:191],a[148:151],v[116:119]\n"
"ds_read2st64_b32 v[18:19],v13 offset0:194 offset1:195\n"
MF "v[116:119],a[240:243],a[188:191],v[116:119]\n"
"ds_read2st64_b32 v[20:21],v13 offset0:196 offset1:197\n"
MF "v[116:119],a[244:247],a[228:231],v[116:119]\n"
"ds_read2st64_b32 v[22:23],v13 offset0:198 offset1:199\n"
MF "v[124:127],a[244:247],a[232:235],v[124:127]\n"
"ds_read2st64_b32 v[24:25],v13 offset0:200 offset1:201\n"
MF "v[124:127],a[240:243],a[192:195],v[124:127]\n"
"ds_read2st64_b32 v[26:27],v13 offset0:202 offset1:203\n"
MF "v[124:127],v[188:191],a[152:155],v[124:127]\n"
"ds_read2st64_b32 v[28:29],v13 offset0:204 offset1:205\n"
MF "v[132:135],v[188:191],a[156:159],v[132:135]\n"
"ds_read2st64_b32 v[30:31],v13 offset0:206 offset1:207\n"
MF "v[132:135],a[240:243],a[196:199],v[132:135]\n"
"ds_read2st64_b32 v[32:33],v13 offset0:208 offset1:209\n"
MF "v[132:135],a[244:247],a[236:239],v[132:135]\n"
"ds_read2st64_b32 v[34:35],v13 offset0:210 offset1:211\n"
"ds_read2st64_b32 v[36:37],v13 offset0:212 offset1:213\n"
"ds_read2st64_b32 v[38:39],v13 offset0:214 offset1:215\n"
"ds_read2st64_b32 v[40:41],v13 offset0:216 offset1:217\n"
"ds_read2st64_b32 v[42:43],v13 offset0:218 offset1:219\n"
"ds_read2st64_b32 v[44:45],v13 offset0:220 offset1:221\n"
"ds_read2st64_b32 v[46:47],v13 offset0:222 offset1:223\n"
"ds_read2st64_b32 v[48:49],v13 offset0:224 offset1:225\n"
"ds_read2st64_b32 v[50:51],v13 offset0:226 offset1:227\n"
"ds_read2st64_b32 v[52:53],v13 offset0:228 offset1:229\n"
"ds_read2st64_b32 v[54:55],v13 offset0:230 offset1:231\n"
"s_waitcnt vmcnt(0)\n"
"v_sub_u32 v10,v10,v11\n"
"s_cmp_eq_u32 s16,0\n"
"s_cbranch_scc1 .Lhave13\n"
"s_add_u32 s59,s78,6\n"
"s_and_b32 s59,s59,15\n"
"s_nop 3\n"
".Lnf9:\n"
"v_readlane_b32 s58,v10,s59\n"
"s_cmp_lt_u32 s58,9\n"
"s_cbranch_scc1 .Lsp9\n"
".Lnb9:\n"
"s_bfe_u32 s62,s22,0x30014\n"
"s_add_u32 s62,s62,3\n"
"s_and_b32 s62,s62,7\n"
"s_mul_i32 s62,s62,204800\n"
"s_add_u32 s62,s62,s73\n"
"s_add_u32 s62,s62,122880\n"
LD "v[56:59],v9,s[40:43],s62 offen sc1\n"
LD "v[64:67],v9,s[40:43],s62 offen offset:1024 sc1\n"
LD "v[72:75],v9,s[40:43],s62 offen offset:2048 sc1\n"
LD "v[80:83],v9,s[40:43],s62 offen offset:3072 sc1\n"
"s_add_u32 s62,s62,4096\n"
LD "v[88:91],v9,s[40:43],s62 offen sc1\n"
".Lhave13:\n"
"s_cmp_eq_u32 s17,0\n"
"s_cbranch_scc1 .Lhave12\n"
"s_add_u32 s59,s78,4\n"
"s_and_b32 s59,s59,15\n"
"s_nop 3\n"
".Lnf10:\n"
"v_readlane_b32 s58,v10,s59\n"
"s_cmp_lt_u32 s58,10\n"
"s_cbranch_scc1 .Lsp10\n"
".Lnb10:\n"
"s_bfe_u32 s62,s22,0x30014\n"
"s_add_u32 s62,s62,2\n"
"s_and_b32 s62,s62,7\n"
"s_mul_i32 s62,s62,204800\n"
"s_add_u32 s62,s62,s73\n"
"s_add_u32 s62,s62,143360\n"
LD "v[60:63],v9,s[40:43],s62 offen sc1\n"
LD "v[68:71],v9,s[40:43],s62 offen offset:1024 sc1\n"
LD "v[76:79],v9,s[40:43],s62 offen offset:2048 sc1\n"
LD "v[84:87],v9,s[40:43],s62 offen offset:3072 sc1\n"
"s_add_u32 s62,s62,4096\n"
LD "v[92:95],v9,s[40:43],s62 offen sc1\n"
".Lhave12:\n"
".Lnf11:\n"
"v_readlane_b32 s58,v10,s72\n"
"s_cmp_lt_u32 s58,11\n"
"s_cbranch_scc1 .Lsp11\n"
".Lnb11:\n"
"s_bfe_u32 s62,s22,0x30014\n"
"s_add_u32 s62,s62,1\n"
"s_and_b32 s62,s62,7\n"
"s_mul_i32 s62,s62,204800\n"
"s_add_u32 s62,s62,s73\n"
"s_nop 0\n"
"s_add_u32 s62,s62,163840\n"
LD "v[208:211],v9,s[40:43],s62 offen sc1\n"
LD "v[212:215],v9,s[40:43],s62 offen offset:1024 sc1\n"
LD "v[216:219],v9,s[40:43],s62 offen offset:2048 sc1\n"
LD "v[220:223],v9,s[40:43],s62 offen offset:3072 sc1\n"
"s_add_u32 s62,s62,4096\n"
LD "v[224:227],v9,s[40:43],s62 offen sc1\n"
".Lpg35h:\n"
"s_cbranch_execz .Lisr4\n"
"s_nop 0\n"
"s_cmp_eq_u32 s1,0xf0000000\n"
"s_cbranch_scc1 .Ljx1\n"
"v_accvgpr_mov_b32 a16,a0\n"
"v_accvgpr_mov_b32 a17,a1\n"
"v_accvgpr_mov_b32 a56,a40\n"
"v_accvgpr_mov_b32 a57,a41\n"
MF "v[196:199],a[2:5],a[250:253],0\n"
MF "v[192:195],a[6:9],a[250:253],0\n"
MF "v[196:199],a[10:13],a[250:253],v[196:199]\n"
MF "v[192:195],a[14:17],a[250:253],v[192:195]\n"
MF "v[196:199],a[42:45],a[250:253],v[196:199]\n"
MF "v[192:195],a[46:49],a[250:253],v[192:195]\n"
MF "v[196:199],a[50:53],a[250:253],v[196:199]\n"
MF "v[192:195],a[54:57],a[250:253],v[192:195]\n"
"s_nop 7\n"
CV "v192,v192,v193\n"
CV "v193,v194,v195\n"
CV "v194,v196,v197\n"
CV "v195,v198,v199\n"
"s_nop 0\n"
"v_permlane16_swap_b32 v192,v194\n"
"v_permlane16_swap_b32 v193,v195\n"
"buffer_store_dwordx4 v[192:195],v8,s[44:47],s1 offen sc1\n"
".Ljx1:\n"
"s_waitcnt lgkmcnt(15)\n"
"v_pk_add_f32 v[96:97],v[96:97],v[16:17]\n"
"v_pk_add_f32 v[98:99],v[98:99],v[18:19]\n"
"v_pk_add_f32 v[100:101],v[100:101],v[20:21]\n"
"v_pk_add_f32 v[102:103],v[102:103],v[22:23]\n"
"v_pk_add_f32 v[104:105],v[104:105],v[24:25]\n"
"s_waitcnt lgkmcnt(14)\n"
"v_pk_add_f32 v[106:107],v[106:107],v[26:27]\n"
"s_waitcnt lgkmcnt(13)\n"
"v_pk_add_f32 v[108:109],v[108:109],v[28:29]\n"
"s_waitcnt lgkmcnt(12)\n"
"v_pk_add_f32 v[110:111],v[110:111],v[30:31]\n"
"s_waitcnt lgkmcnt(11)\n"
"v_pk_add_f32 v[112:113],v[112:113],v[32:33]\n"
"s_waitcnt lgkmcnt(10)\n"
"v_pk_add_f32 v[114:115],v[114:115],v[34:35]\n"
"s_waitcnt lgkmcnt(9)\n"
"v_pk_add_f32 v[116:117],v[116:117],v[36:37]\n"
"s_waitcnt lgkmcnt(8)\n"
"v_pk_add_f32 v[118:119],v[118:119],v[38:39]\n"
"s_waitcnt lgkmcnt(7)\n"
"v_pk_add_f32 v[120:121],v[120:121],v[40:41]\n"
"s_waitcnt lgkmcnt(6)\n"
"v_pk_add_f32 v[122:123],v[122:123],v[42:43]\n"
"s_waitcnt lgkmcnt(5)\n"
"v_pk_add_f32 v[124:125],v[124:125],v[44:45]\n"
"s_waitcnt lgkmcnt(4)\n"
"v_pk_add_f32 v[126:127],v[126:127],v[46:47]\n"
"s_waitcnt lgkmcnt(3)\n"
"v_pk_add_f32 v[128:129],v[128:129],v[48:49]\n"
"s_waitcnt lgkmcnt(2)\n"
"v_pk_add_f32 v[130:131],v[130:131],v[50:51]\n"
"s_waitcnt lgkmcnt(1)\n"
"v_pk_add_f32 v[132:133],v[132:133],v[52:53]\n"
"s_waitcnt lgkmcnt(0)\n"
"v_pk_add_f32 v[134:135],v[134:135],v[54:55]\n"
"s_waitcnt vmcnt(5)\n"
"s_nop 0\n"
MF "v[100:103],v[56:59],a[252:255],v[100:103]\n"
MF "v[108:111],v[64:67],a[252:255],v[108:111]\n"
MF "v[116:119],v[72:75],a[252:255],v[116:119]\n"
MF "v[124:127],v[80:83],a[252:255],v[124:127]\n"
MF "v[132:135],v[88:91],a[252:255],v[132:135]\n"
MF "v[96:99],v[56:59],a[248:251],v[96:99]\n"
MF "v[104:107],v[64:67],a[248:251],v[104:107]\n"
MF "v[112:115],v[72:75],a[248:251],v[112:115]\n"
MF "v[120:123],v[80:83],a[248:251],v[120:123]\n"
MF "v[128:131],v[88:91],a[248:251],v[128:131]\n"
MF "v[96:99],v[60:63],a[252:255],v[96:99]\n"
MF "v[104:107],v[68:71],a[252:255],v[104:107]\n"
MF "v[112:115],v[76:79],a[252:255],v[112:115]\n"
MF "v[120:123],v[84:87],a[252:255],v[120:123]\n"
MF "v[128:131],v[92:95],a[252:255],v[128:131]\n"
MF "v[100:103],v[60:63],a[248:251],v[100:103]\n"
MF "v[108:111],v[68:71],a[248:251],v[108:111]\n"
MF "v[116:119],v[76:79],a[248:251],v[116:119]\n"
MF "v[124:127],v[84:87],a[248:251],v[124:127]\n"
MF "v[132:135],v[92:95],a[248:251],v[132:135]\n"
"s_waitcnt vmcnt(0)\n"
MF "v[100:103],v[208:211],a[252:255],v[100:103]\n"
MF "v[96:99],v[208:211],a[248:251],v[96:99]\n"
MF "v[108:111],v[212:215],a[252:255],v[108:111]\n"
MF "v[104:107],v[212:215],a[248:251],v[104:107]\n"
MF "v[116:119],v[216:219],a[252:255],v[116:119]\n"
"s_nop 4\n"
CV "v96,v96,v97\n"
CV "v97,v98,v99\n"
MF "v[112:115],v[216:219],a[248:251],v[112:115]\n"
CV "v98,v100,v101\n"
CV "v99,v102,v103\n"
MF "v[124:127],v[220:223],a[252:255],v[124:127]\n"
CV "v104,v104,v105\n"
CV "v105,v106,v107\n"
MF "v[120:123],v[220:223],a[248:251],v[120:123]\n"
CV "v106,v108,v109\n"
CV "v107,v110,v111\n"
"v_permlane16_swap_b32 v96,v98\n"
"v_permlane16_swap_b32 v97,v99\n"
"buffer_store_dwordx4 v[96:99],v242,s[44:47],s87 offen sc1\n"
"s_add_u32 s87,s87,40960\n"
MF "v[132:135],v[224:227],a[252:255],v[132:135]\n"
CV "v112,v112,v113\n"
CV "v113,v114,v115\n"
MF "v[128:131],v[224:227],a[248:251],v[128:131]\n"
CV "v114,v116,v117\n"
CV "v115,v118,v119\n"
"v_permlane16_swap_b32 v104,v106\n"
"v_permlane16_swap_b32 v105,v107\n"
"buffer_store_dwordx4 v[104:107],v242,s[44:47],s87 offen sc1\n"
"s_add_u32 s87,s87,40960\n"
CV "v120,v120,v121\n"
CV "v121,v122,v123\n"
CV "v122,v124,v125\n"
CV "v123,v126,v127\n"
"v_permlane16_swap_b32 v112,v114\n"
"v_permlane16_swap_b32 v113,v115\n"
"buffer_store_dwordx4 v[112:115],v242,s[44:47],s87 offen sc1\n"
"s_add_u32 s87,s87,40960\n"
CV "v128,v128,v129\n"
CV "v129,v130,v131\n"
CV "v130,v132,v133\n"
CV "v131,v134,v135\n"
"v_permlane16_swap_b32 v120,v122\n"
"v_permlane16_swap_b32 v121,v123\n"
"buffer_store_dwordx4 v[120:123],v242,s[44:47],s87 offen sc1\n"
"s_add_u32 s87,s87,40960\n"
"v_permlane16_swap_b32 v128,v130\n"
"v_permlane16_swap_b32 v129,v131\n"
"buffer_store_dwordx4 v[128:131],v242,s[44:47],s87 offen sc1\n"
"s_add_u32 s87,s87,40960\n"
"s_endpgm\n"
".Ljobs:\n"
"s_cmp_ge_u32 s92,40\n"
"s_cbranch_scc1 .Ldone\n"
"buffer_load_dword v10,v12,s[40:43],s86 offen sc1\n"
"s_mul_i32 s95,s92,3277\n"
"s_lshr_b32 s95,s95,16\n"
"s_mul_i32 s59,s95,20\n"
"s_sub_u32 s59,s92,s59\n"
"s_cmp_ge_u32 s59,10\n"
"s_cselect_b32 s96,4294557760,0\n"
"s_add_u32 s96,s96,1024\n"
"s_mul_i32 s59,s59,40960\n"
"s_add_u32 s96,s96,s59\n"
"s_lshl_b32 s59,s95,7\n"
"s_add_u32 s96,s96,s59\n"
"s_add_u32 s95,s95,3\n"
"s_waitcnt vmcnt(0)\n"
".Ljtry:\n"
"s_cmp_eq_u32 s98,0\n"
"s_cselect_b32 s95,0,s95\n"
"v_sub_u32 v10,v10,v11\n"
"v_cmp_gt_u32 vcc,s95,v10\n"
"s_lshl_b32 s82,s92,10\n"
"s_add_u32 s82,s82,s93\n"
"s_cmp_eq_u64 vcc,0\n"
"s_cselect_b32 s82,s82,0xf0000000\n"
"s_cselect_b32 s84,s96,0xf0000000\n"
"s_cselect_b32 s59,s4,0\n"
"s_add_u32 s92,s92,s59\n"
"s_cmp_eq_u32 s59,0\n"
"s_cbranch_scc0 .Ljgo\n"
"s_sub_u32 s98,s98,1\n"
"s_sleep 2\n"
"s_branch .Ljobs\n"
".Ljgo:\n"
"s_mov_b32 s83,s84\n"
LD "v[208:211],v9,s[40:43],s82 offen sc1\n"
"s_add_u32 s82,s82,s71\n"
LD "v[212:215],v9,s[40:43],s82 offen sc1\n"
"s_add_u32 s82,s82,s71\n"
LD "v[216:219],v9,s[40:43],s82 offen sc1\n"
"s_add_u32 s82,s82,s71\n"
LD "v[220:223],v9,s[40:43],s82 offen sc1\n"
"s_add_u32 s82,s82,s71\n"
LD "v[224:227],v9,s[40:43],s82 offen sc1\n"
"s_add_u32 s82,s82,s66\n"
LD "v[228:231],v9,s[40:43],s82 offen sc1\n"
"s_add_u32 s82,s82,s71\n"
LD "v[232:235],v9,s[40:43],s82 offen sc1\n"
"s_add_u32 s82,s82,s71\n"
LD "v[236:239],v9,s[40:43],s82 offen sc1\n"
"buffer_load_dword v10,v12,s[40:43],s86 offen sc1\n"
"s_waitcnt vmcnt(0)\n"
".Ljfin:\n"
"s_nop 1\n"
"v_mov_b32 v240,v208\n"
"v_mov_b32 v241,v209\n"
MF "v[248:251],v[210:213],a[250:253],0\n"
MF "v[244:247],v[214:217],a[250:253],0\n"
MF "v[248:251],v[218:221],a[250:253],v[248:251]\n"
MF "v[244:247],v[222:225],a[250:253],v[244:247]\n"
MF "v[248:251],v[226:229],a[250:253],v[248:251]\n"
MF "v[244:247],v[230:233],a[250:253],v[244:247]\n"
MF "v[248:251],v[234:237],a[250:253],v[248:251]\n"
MF "v[244:247],v[238:241],a[250:253],v[244:247]\n"
"s_nop 7\n"
CV "v244,v244,v245\n"
CV "v245,v246,v247\n"
CV "v246,v248,v249\n"
CV "v247,v250,v251\n"
"s_nop 0\n"
"v_permlane16_swap_b32 v244,v246\n"
"v_permlane16_swap_b32 v245,v247\n"
"buffer_store_dwordx4 v[244:247],v8,s[44:47],s83 offen sc1\n"
"s_cmp_ge_u32 s92,40\n"
"s_cbranch_scc1 .Ldone\n"
"s_mul_i32 s95,s92,3277\n"
"s_lshr_b32 s95,s95,16\n"
"s_mul_i32 s59,s95,20\n"
"s_sub_u32 s59,s92,s59\n"
"s_cmp_ge_u32 s59,10\n"
"s_cselect_b32 s96,4294557760,0\n"
"s_add_u32 s96,s96,1024\n"
"s_mul_i32 s59,s59,40960\n"
"s_add_u32 s96,s96,s59\n"
"s_lshl_b32 s59,s95,7\n"
"s_add_u32 s96,s96,s59\n"
"s_add_u32 s95,s95,3\n"
"s_branch .Ljtry\n"
".Lool:\n"
".Lsp0:\n"
"s_cmp_eq_u32 s98,0\n"
"s_cbranch_scc1 .Lnb0\n"
"s_sub_u32 s98,s98,1\n"
"s_sleep 2\n"
"buffer_load_dword v10,v12,s[40:43],s86 offen sc1\n"
"s_waitcnt vmcnt(0)\n"
"v_sub_u32 v10,v10,v11\n"
"s_nop 1\n"
"s_branch .Lnf0\n"
".Lsp1:\n"
"s_cmp_eq_u32 s98,0\n"
"s_cbranch_scc1 .Lnb1\n"
"s_sub_u32 s98,s98,1\n"
"s_sleep 2\n"
"buffer_load_dword v10,v12,s[40:43],s86 offen sc1\n"
"s_waitcnt vmcnt(0)\n"
"v_sub_u32 v10,v10,v11\n"
"s_nop 1\n"
"s_branch .Lnf1\n"
".Lsp2:\n"
"s_cmp_eq_u32 s98,0\n"
"s_cbranch_scc1 .Lnb2\n"
"s_sub_u32 s98,s98,1\n"
"s_sleep 2\n"
"buffer_load_dword v10,v12,s[40:43],s86 offen sc1\n"
"s_waitcnt vmcnt(0)\n"
"v_sub_u32 v10,v10,v11\n"
"s_nop 1\n"
"s_branch .Lnf2\n"
".Lsp3:\n"
"s_cmp_eq_u32 s98,0\n"
"s_cbranch_scc1 .Lnb3\n"
"s_sub_u32 s98,s98,1\n"
"s_sleep 2\n"
"buffer_load_dword v10,v12,s[40:43],s86 offen sc1\n"
"s_waitcnt vmcnt(0)\n"
"v_sub_u32 v10,v10,v11\n"
"s_nop 1\n"
"s_branch .Lnf3\n"
".Lsp4:\n"
"s_cmp_eq_u32 s98,0\n"
"s_cbranch_scc1 .Lnb4\n"
"s_sub_u32 s98,s98,1\n"
"s_sleep 2\n"
"buffer_load_dword v10,v12,s[40:43],s86 offen sc1\n"
"s_waitcnt vmcnt(0)\n"
"v_sub_u32 v10,v10,v11\n"
"s_nop 1\n"
"s_branch .Lnf4\n"
".Lsp5:\n"
"s_cmp_eq_u32 s98,0\n"
"s_cbranch_scc1 .Lnb5\n"
"s_sub_u32 s98,s98,1\n"
"s_sleep 2\n"
"buffer_load_dword v10,v12,s[40:43],s86 offen sc1\n"
"s_waitcnt vmcnt(0)\n"
"v_sub_u32 v10,v10,v11\n"
"s_nop 1\n"
"s_branch .Lnf5\n"
".Lsp6:\n"
"s_cmp_eq_u32 s98,0\n"
"s_cbranch_scc1 .Lnb6\n"
"s_sub_u32 s98,s98,1\n"
"s_sleep 2\n"
"buffer_load_dword v10,v12,s[40:43],s86 offen sc1\n"
"s_waitcnt vmcnt(0)\n"
"v_sub_u32 v10,v10,v11\n"
"s_nop 1\n"
"s_branch .Lnf6\n"
".Lsp7:\n"
"s_cmp_eq_u32 s98,0\n"
"s_cbranch_scc1 .Lnb7\n"
"s_sub_u32 s98,s98,1\n"
"s_sleep 2\n"
"buffer_load_dword v10,v12,s[40:43],s86 offen sc1\n"
"s_waitcnt vmcnt(0)\n"
"v_sub_u32 v10,v10,v11\n"
"s_nop 1\n"
"s_branch .Lnf7\n"
".Lsp8:\n"
"s_cmp_eq_u32 s98,0\n"
"s_cbranch_scc1 .Lnb8\n"
"s_sub_u32 s98,s98,1\n"
"s_sleep 2\n"
"buffer_load_dword v10,v12,s[40:43],s86 offen sc1\n"
"s_waitcnt vmcnt(0)\n"
"v_sub_u32 v10,v10,v11\n"
"s_nop 1\n"
"s_branch .Lnf8\n"
".Lsp9:\n"
"s_cmp_eq_u32 s98,0\n"
"s_cbranch_scc1 .Lnb9\n"
"s_sub_u32 s98,s98,1\n"
"s_sleep 2\n"
"buffer_load_dword v10,v12,s[40:43],s86 offen sc1\n"
"s_waitcnt vmcnt(0)\n"
"v_sub_u32 v10,v10,v11\n"
"s_nop 1\n"
"s_branch .Lnf9\n"
".Lsp10:\n"
"s_cmp_eq_u32 s98,0\n"
"s_cbranch_scc1 .Lnb10\n"
"s_sub_u32 s98,s98,1\n"
"s_sleep 2\n"
"buffer_load_dword v10,v12,s[40:43],s86 offen sc1\n"
"s_waitcnt vmcnt(0)\n"
"v_sub_u32 v10,v10,v11\n"
"s_nop 1\n"
"s_branch .Lnf10\n"
".Lsp11:\n"
"s_cmp_eq_u32 s98,0\n"
"s_cbranch_scc1 .Lnb11\n"
"s_sub_u32 s98,s98,1\n"
"s_sleep 2\n"
"buffer_load_dword v10,v12,s[40:43],s86 offen sc1\n"
"s_waitcnt vmcnt(0)\n"
"v_sub_u32 v10,v10,v11\n"
"s_nop 1\n"
"s_branch .Lnf11\n"
".Lcode1:\n"
".Lslow_0:\n"
"s_nop 7\n"
"s_nop 7\n"
"v_and_b32 v10,240,v9\n"
"v_mul_u32_u24 v10,24,v10\n"
"v_lshrrev_b32 v11,4,v9\n"
"v_and_or_b32 v10,v11,48,v10\n"
"s_mov_b32 s59,s34\n"
"s_mov_b32 s34,s20\n"
"s_add_u32 s58,s74,0\n"
"s_nop 3\n"
LD "v[52:55],v10,s[32:35],s58 offen\n"
"s_waitcnt vmcnt(0)\n"
MF "v[72:75],v[52:55],v[136:139],v[32:35]\n"
MF "v[76:79],v[52:55],v[140:143],v[36:39]\n"
MF "v[80:83],v[52:55],v[144:147],v[40:43]\n"
MF "v[84:87],v[52:55],v[148:151],v[44:47]\n"
MF "v[88:91],v[52:55],v[152:155],v[48:51]\n"
"s_add_u32 s58,s74,0\n"
"s_nop 3\n"
LD "v[52:55],v10,s[32:35],s58 offen offset:64\n"
"s_waitcnt vmcnt(0)\n"
MF "v[72:75],v[52:55],v[156:159],v[72:75]\n"
MF "v[76:79],v[52:55],v[160:163],v[76:79]\n"
MF "v[80:83],v[52:55],v[164:167],v[80:83]\n"
MF "v[84:87],v[52:55],v[168:171],v[84:87]\n"
MF "v[88:91],v[52:55],v[172:175],v[88:91]\n"
"s_add_u32 s58,s74,0\n"
"s_nop 3\n"
LD "v[52:55],v10,s[32:35],s58 offen offset:128\n"
"s_waitcnt vmcnt(0)\n"
MF "v[72:75],v[52:55],v[176:179],v[72:75]\n"
MF "v[76:79],v[52:55],v[180:183],v[76:79]\n"
MF "v[80:83],v[52:55],v[184:187],v[80:83]\n"
MF "v[84:87],v[52:55],v[188:191],v[84:87]\n"
MF "v[88:91],v[52:55],v[192:195],v[88:91]\n"
"s_add_u32 s58,s74,0\n"
"s_nop 3\n"
LD "v[52:55],v10,s[32:35],s58 offen offset:192\n"
"s_waitcnt vmcnt(0)\n"
MF "v[72:75],v[52:55],v[196:199],v[72:75]\n"
MF "v[76:79],v[52:55],v[200:203],v[76:79]\n"
MF "v[80:83],v[52:55],v[204:207],v[80:83]\n"
MF "v[84:87],v[52:55],v[208:211],v[84:87]\n"
MF "v[88:91],v[52:55],v[212:215],v[88:91]\n"
"s_add_u32 s58,s74,0\n"
"s_nop 3\n"
LD "v[52:55],v10,s[32:35],s58 offen offset:256\n"
"s_waitcnt vmcnt(0)\n"
MF "v[72:75],v[52:55],v[216:219],v[72:75]\n"
MF "v[76:79],v[52:55],v[220:223],v[76:79]\n"
MF "v[80:83],v[52:55],v[224:227],v[80:83]\n"
MF "v[84:87],v[52:55],v[228:231],v[84:87]\n"
MF "v[88:91],v[52:55],v[232:235],v[88:91]\n"
"s_add_u32 s58,s74,0\n"
"s_nop 3\n"
LD "v[52:55],v10,s[32:35],s58 offen offset:320\n"
"s_waitcnt vmcnt(0)\n"
MF "v[72:75],v[52:55],v[236:239],v[72:75]\n"
MF "v[76:79],v[52:55],v[240:243],v[76:79]\n"
MF "v[80:83],v[52:55],v[244:247],v[80:83]\n"
MF "v[84:87],v[52:55],v[248:251],v[84:87]\n"
MF "v[88:91],v[52:55],v[252:255],v[88:91]\n"
"s_add_u32 s58,s74,6144\n"
"s_nop 3\n"
LD "v[52:55],v10,s[32:35],s58 offen\n"
"s_waitcnt vmcnt(0)\n"
MF "v[112:115],v[52:55],v[136:139],v[32:35]\n"
MF "v[116:119],v[52:55],v[140:143],v[36:39]\n"
MF "v[120:123],v[52:55],v[144:147],v[40:43]\n"
MF "v[124:127],v[52:55],v[148:151],v[44:47]\n"
MF "v[128:131],v[52:55],v[152:155],v[48:51]\n"
"s_add_u32 s58,s74,6144\n"
"s_nop 3\n"
LD "v[52:55],v10,s[32:35],s58 offen offset:64\n"
"s_waitcnt vmcnt(0)\n"
MF "v[112:115],v[52:55],v[156:159],v[112:115]\n"
MF "v[116:119],v[52:55],v[160:163],v[116:119]\n"
MF "v[120:123],v[52:55],v[164:167],v[120:123]\n"
MF "v[124:127],v[52:55],v[168:171],v[124:127]\n"
MF "v[128:131],v[52:55],v[172:175],v[128:131]\n"
"s_add_u32 s58,s74,6144\n"
"s_nop 3\n"
LD "v[52:55],v10,s[32:35],s58 offen offset:128\n"
"s_waitcnt vmcnt(0)\n"
MF "v[112:115],v[52:55],v[176:179],v[112:115]\n"
MF "v[116:119],v[52:55],v[180:183],v[116:119]\n"
MF "v[120:123],v[52:55],v[184:187],v[120:123]\n"
MF "v[124:127],v[52:55],v[188:191],v[124:127]\n"
MF "v[128:131],v[52:55],v[192:195],v[128:131]\n"
"s_add_u32 s58,s74,6144\n"
"s_nop 3\n"
LD "v[52:55],v10,s[32:35],s58 offen offset:192\n"
"s_waitcnt vmcnt(0)\n"
MF "v[112:115],v[52:55],v[196:199],v[112:115]\n"
MF "v[116:119],v[52:55],v[200:203],v[116:119]\n"
MF "v[120:123],v[52:55],v[204:207],v[120:123]\n"
MF "v[124:127],v[52:55],v[208:211],v[124:127]\n"
MF "v[128:131],v[52:55],v[212:215],v[128:131]\n"
"s_add_u32 s58,s74,6144\n"
"s_nop 3\n"
LD "v[52:55],v10,s[32:35],s58 offen offset:256\n"
"s_waitcnt vmcnt(0)\n"
MF "v[112:115],v[52:55],v[216:219],v[112:115]\n"
MF "v[116:119],v[52:55],v[220:223],v[116:119]\n"
MF "v[120:123],v[52:55],v[224:227],v[120:123]\n"
MF "v[124:127],v[52:55],v[228:231],v[124:127]\n"
MF "v[128:131],v[52:55],v[232:235],v[128:131]\n"
"s_add_u32 s58,s74,6144\n"
"s_nop 3\n"
LD "v[52:55],v10,s[32:35],s58 offen offset:320\n"
"s_waitcnt vmcnt(0)\n"
MF "v[112:115],v[52:55],v[236:239],v[112:115]\n"
MF "v[116:119],v[52:55],v[240:243],v[116:119]\n"
MF "v[120:123],v[52:55],v[244:247],v[120:123]\n"
MF "v[124:127],v[52:55],v[248:251],v[124:127]\n"
MF "v[128:131],v[52:55],v[252:255],v[128:131]\n"
"s_nop 7\n"
"s_mov_b32 s34,s59\n"
"v_max3_f32 v10,v72,v73,v74\n"
"s_nop 0\n"
"v_max3_f32 v11,v75,v112,v113\n"
"v_max3_f32 v10,v10,v114,v115\n"
"v_max_f32 v10,v10,v11\n"
"v_mov_b32 v11,v10\n"
"s_nop 1\n"
"v_permlane16_swap_b32 v10,v11\n"
"v_max_f32 v10,v10,v11\n"
"v_mov_b32 v11,v10\n"
"s_nop 1\n"
"v_permlane32_swap_b32 v10,v11\n"
"v_max_f32 v10,v10,v11\n"
"v_add_f32 v10,0x41300000,v10\n"
"v_max_f32_e64 v10,0,v10\n"
"v_exp_f32 v11,-v10\n"
AR "v12,a0\n"
"v_subrev_f32 v12,s13,v12\n"
"v_fma_f32 v12,v12,v11,s13\n"
AW "a0,v12\n"
AR "v12,a20\n"
"v_subrev_f32 v12,s13,v12\n"
"v_fma_f32 v12,v12,v11,s13\n"
AW "a20,v12\n"
AR "v12,a40\n"
"v_subrev_f32 v12,s13,v12\n"
"v_fma_f32 v12,v12,v11,s13\n"
AW "a40,v12\n"
AR "v12,a60\n"
"v_subrev_f32 v12,s13,v12\n"
"v_fma_f32 v12,v12,v11,s13\n"
AW "a60,v12\n"
AR "v12,a80\n"
"v_subrev_f32 v12,s13,v12\n"
"v_fma_f32 v12,v12,v11,s13\n"
AW "a80,v12\n"
AR "v12,a100\n"
"v_subrev_f32 v12,s13,v12\n"
"v_fma_f32 v12,v12,v11,s13\n"
AW "a100,v12\n"
AR "v12,a120\n"
"v_subrev_f32 v12,s13,v12\n"
"v_fma_f32 v12,v12,v11,s13\n"
AW "a120,v12\n"
AR "v12,a140\n"
"v_subrev_f32 v12,s13,v12\n"
"v_fma_f32 v12,v12,v11,s13\n"
AW "a140,v12\n"
AR "v12,a160\n"
"v_subrev_f32 v12,s13,v12\n"
"v_fma_f32 v12,v12,v11,s13\n"
AW "a160,v12\n"
AR "v12,a180\n"
"v_subrev_f32 v12,s13,v12\n"
"v_fma_f32 v12,v12,v11,s13\n"
AW "a180,v12\n"
AR "v12,a200\n"
"v_subrev_f32 v12,s13,v12\n"
"v_fma_f32 v12,v12,v11,s13\n"
AW "a200,v12\n"
AR "v12,a220\n"
"v_subrev_f32 v12,s13,v12\n"
"v_fma_f32 v12,v12,v11,s13\n"
AW "a220,v12\n"
AR "v12,a1\n"
"v_subrev_f32 v12,s13,v12\n"
"v_fma_f32 v12,v12,v11,s13\n"
AW "a1,v12\n"
AR "v12,a21\n"
"v_subrev_f32 v12,s13,v12\n"
"v_fma_f32 v12,v12,v11,s13\n"
AW "a21,v12\n"
AR "v12,a41\n"
"v_subrev_f32 v12,s13,v12\n"
"v_fma_f32 v12,v12,v11,s13\n"
AW "a41,v12\n"
AR "v12,a61\n"
"v_subrev_f32 v12,s13,v12\n"
"v_fma_f32 v12,v12,v11,s13\n"
AW "a61,v12\n"
AR "v12,a81\n"
"v_subrev_f32 v12,s13,v12\n"
"v_fma_f32 v12,v12,v11,s13\n"
AW "a81,v12\n"
AR "v12,a101\n"
"v_subrev_f32 v12,s13,v12\n"
"v_fma_f32 v12,v12,v11,s13\n"
AW "a101,v12\n"
AR "v12,a121\n"
"v_subrev_f32 v12,s13,v12\n"
"v_fma_f32 v12,v12,v11,s13\n"
AW "a121,v12\n"
AR "v12,a141\n"
"v_subrev_f32 v12,s13,v12\n"
"v_fma_f32 v12,v12,v11,s13\n"
AW "a141,v12\n"
AR "v12,a161\n"
"v_subrev_f32 v12,s13,v12\n"
"v_fma_f32 v12,v12,v11,s13\n"
AW "a161,v12\n"
AR "v12,a181\n"
"v_subrev_f32 v12,s13,v12\n"
"v_fma_f32 v12,v12,v11,s13\n"
AW "a181,v12\n"
AR "v12,a201\n"
"v_subrev_f32 v12,s13,v12\n"
"v_fma_f32 v12,v12,v11,s13\n"
AW "a201,v12\n"
AR "v12,a221\n"
"v_subrev_f32 v12,s13,v12\n"
"v_fma_f32 v12,v12,v11,s13\n"
AW "a221,v12\n"
AR "v12,a2\n"
"v_subrev_f32 v12,s13,v12\n"
"v_fma_f32 v12,v12,v11,s13\n"
AW "a2,v12\n"
AR "v12,a22\n"
"v_subrev_f32 v12,s13,v12\n"
"v_fma_f32 v12,v12,v11,s13\n"
AW "a22,v12\n"
AR "v12,a42\n"
"v_subrev_f32 v12,s13,v12\n"
"v_fma_f32 v12,v12,v11,s13\n"
AW "a42,v12\n"
AR "v12,a62\n"
"v_subrev_f32 v12,s13,v12\n"
"v_fma_f32 v12,v12,v11,s13\n"
AW "a62,v12\n"
AR "v12,a82\n"
"v_subrev_f32 v12,s13,v12\n"
"v_fma_f32 v12,v12,v11,s13\n"
AW "a82,v12\n"
AR "v12,a102\n"
"v_subrev_f32 v12,s13,v12\n"
"v_fma_f32 v12,v12,v11,s13\n"
AW "a102,v12\n"
AR "v12,a122\n"
"v_subrev_f32 v12,s13,v12\n"
"v_fma_f32 v12,v12,v11,s13\n"
AW "a122,v12\n"
AR "v12,a142\n"
"v_subrev_f32 v12,s13,v12\n"
"v_fma_f32 v12,v12,v11,s13\n"
AW "a142,v12\n"
AR "v12,a162\n"
"v_subrev_f32 v12,s13,v12\n"
"v_fma_f32 v12,v12,v11,s13\n"
AW "a162,v12\n"
AR "v12,a182\n"
"v_subrev_f32 v12,s13,v12\n"
"v_fma_f32 v12,v12,v11,s13\n"
AW "a182,v12\n"
AR "v12,a202\n"
"v_subrev_f32 v12,s13,v12\n"
"v_fma_f32 v12,v12,v11,s13\n"
AW "a202,v12\n"
AR "v12,a222\n"
"v_subrev_f32 v12,s13,v12\n"
"v_fma_f32 v12,v12,v11,s13\n"
AW "a222,v12\n"
AR "v12,a3\n"
"v_subrev_f32 v12,s13,v12\n"
"v_fma_f32 v12,v12,v11,s13\n"
AW "a3,v12\n"
AR "v12,a23\n"
"v_subrev_f32 v12,s13,v12\n"
"v_fma_f32 v12,v12,v11,s13\n"
AW "a23,v12\n"
AR "v12,a43\n"
"v_subrev_f32 v12,s13,v12\n"
"v_fma_f32 v12,v12,v11,s13\n"
AW "a43,v12\n"
AR "v12,a63\n"
"v_subrev_f32 v12,s13,v12\n"
"v_fma_f32 v12,v12,v11,s13\n"
AW "a63,v12\n"
AR "v12,a83\n"
"v_subrev_f32 v12,s13,v12\n"
"v_fma_f32 v12,v12,v11,s13\n"
AW "a83,v12\n"
AR "v12,a103\n"
"v_subrev_f32 v12,s13,v12\n"
"v_fma_f32 v12,v12,v11,s13\n"
AW "a103,v12\n"
AR "v12,a123\n"
"v_subrev_f32 v12,s13,v12\n"
"v_fma_f32 v12,v12,v11,s13\n"
AW "a123,v12\n"
AR "v12,a143\n"
"v_subrev_f32 v12,s13,v12\n"
"v_fma_f32 v12,v12,v11,s13\n"
AW "a143,v12\n"
AR "v12,a163\n"
"v_subrev_f32 v12,s13,v12\n"
"v_fma_f32 v12,v12,v11,s13\n"
AW "a163,v12\n"
AR "v12,a183\n"
"v_subrev_f32 v12,s13,v12\n"
"v_fma_f32 v12,v12,v11,s13\n"
AW "a183,v12\n"
AR "v12,a203\n"
"v_subrev_f32 v12,s13,v12\n"
"v_fma_f32 v12,v12,v11,s13\n"
AW "a203,v12\n"
AR "v12,a223\n"
"v_subrev_f32 v12,s13,v12\n"
"v_fma_f32 v12,v12,v11,s13\n"
AW "a223,v12\n"
"v_mul_f32 v52,v57,v11\n"
"v_sub_f32 v32,v32,v10\n"
"v_sub_f32 v33,v33,v10\n"
"v_sub_f32 v34,v34,v10\n"
"v_sub_f32 v35,v35,v10\n"
"v_sub_f32 v92,v92,v10\n"
"v_sub_f32 v93,v93,v10\n"
"v_sub_f32 v94,v94,v10\n"
"v_sub_f32 v95,v95,v10\n"
"v_sub_f32 v72,v72,v10\n"
"v_sub_f32 v73,v73,v10\n"
"v_sub_f32 v74,v74,v10\n"
"v_sub_f32 v75,v75,v10\n"
"v_sub_f32 v112,v112,v10\n"
"v_sub_f32 v113,v113,v10\n"
"v_sub_f32 v114,v114,v10\n"
"v_sub_f32 v115,v115,v10\n"
"v_exp_f32 v72,v72\n"
"v_exp_f32 v73,v73\n"
"v_exp_f32 v74,v74\n"
"v_exp_f32 v75,v75\n"
"v_exp_f32 v112,v112\n"
"v_exp_f32 v113,v113\n"
"v_exp_f32 v114,v114\n"
"v_exp_f32 v115,v115\n"
"v_add_f32 v52,v52,v72\n"
"v_add_f32 v52,v52,v73\n"
"v_add_f32 v52,v52,v74\n"
"v_add_f32 v52,v52,v75\n"
"v_add_f32 v52,v52,v112\n"
"v_add_f32 v52,v52,v113\n"
"v_add_f32 v52,v52,v114\n"
"v_add_f32_e64 v52,v52,v115\n"
CV "v72,v72,v73\n"
CV "v73,v74,v75\n"
CV "v74,v112,v113\n"
CV "v75,v114,v115\n"
"v_max3_f32 v10,v76,v77,v78\n"
"v_max3_f32 v11,v79,v116,v117\n"
"v_max3_f32 v10,v10,v118,v119\n"
"v_max_f32 v10,v10,v11\n"
"v_mov_b32 v11,v10\n"
"s_nop 1\n"
"v_permlane16_swap_b32 v10,v11\n"
"v_max_f32 v10,v10,v11\n"
"v_mov_b32 v11,v10\n"
"s_nop 1\n"
"v_permlane32_swap_b32 v10,v11\n"
"v_max_f32 v10,v10,v11\n"
"v_add_f32 v10,0x41300000,v10\n"
"v_max_f32 v10,0,v10\n"
"v_exp_f32 v11,-v10\n"
AR "v12,a4\n"
"v_subrev_f32 v12,s13,v12\n"
"v_fma_f32 v12,v12,v11,s13\n"
AW "a4,v12\n"
AR "v12,a24\n"
"v_subrev_f32 v12,s13,v12\n"
"v_fma_f32 v12,v12,v11,s13\n"
AW "a24,v12\n"
AR "v12,a44\n"
"v_subrev_f32 v12,s13,v12\n"
"v_fma_f32 v12,v12,v11,s13\n"
AW "a44,v12\n"
AR "v12,a64\n"
"v_subrev_f32 v12,s13,v12\n"
"v_fma_f32 v12,v12,v11,s13\n"
AW "a64,v12\n"
AR "v12,a84\n"
"v_subrev_f32 v12,s13,v12\n"
"v_fma_f32 v12,v12,v11,s13\n"
AW "a84,v12\n"
AR "v12,a104\n"
"v_subrev_f32 v12,s13,v12\n"
"v_fma_f32 v12,v12,v11,s13\n"
AW "a104,v12\n"
AR "v12,a124\n"
"v_subrev_f32 v12,s13,v12\n"
"v_fma_f32 v12,v12,v11,s13\n"
AW "a124,v12\n"
AR "v12,a144\n"
"v_subrev_f32 v12,s13,v12\n"
"v_fma_f32 v12,v12,v11,s13\n"
AW "a144,v12\n"
AR "v12,a164\n"
"v_subrev_f32 v12,s13,v12\n"
"v_fma_f32 v12,v12,v11,s13\n"
AW "a164,v12\n"
AR "v12,a184\n"
"v_subrev_f32 v12,s13,v12\n"
"v_fma_f32 v12,v12,v11,s13\n"
AW "a184,v12\n"
AR "v12,a204\n"
"v_subrev_f32 v12,s13,v12\n"
"v_fma_f32 v12,v12,v11,s13\n"
AW "a204,v12\n"
AR "v12,a224\n"
"v_subrev_f32 v12,s13,v12\n"
"v_fma_f32 v12,v12,v11,s13\n"
AW "a224,v12\n"
AR "v12,a5\n"
"v_subrev_f32 v12,s13,v12\n"
"v_fma_f32 v12,v12,v11,s13\n"
AW "a5,v12\n"
AR "v12,a25\n"
"v_subrev_f32 v12,s13,v12\n"
"v_fma_f32 v12,v12,v11,s13\n"
AW "a25,v12\n"
AR "v12,a45\n"
"v_subrev_f32 v12,s13,v12\n"
"v_fma_f32 v12,v12,v11,s13\n"
AW "a45,v12\n"
AR "v12,a65\n"
"v_subrev_f32 v12,s13,v12\n"
"v_fma_f32 v12,v12,v11,s13\n"
AW "a65,v12\n"
AR "v12,a85\n"
"v_subrev_f32 v12,s13,v12\n"
"v_fma_f32 v12,v12,v11,s13\n"
AW "a85,v12\n"
AR "v12,a105\n"
"v_subrev_f32 v12,s13,v12\n"
"v_fma_f32 v12,v12,v11,s13\n"
AW "a105,v12\n"
AR "v12,a125\n"
"v_subrev_f32 v12,s13,v12\n"
"v_fma_f32 v12,v12,v11,s13\n"
AW "a125,v12\n"
AR "v12,a145\n"
"v_subrev_f32 v12,s13,v12\n"
"v_fma_f32 v12,v12,v11,s13\n"
AW "a145,v12\n"
AR "v12,a165\n"
"v_subrev_f32 v12,s13,v12\n"
"v_fma_f32 v12,v12,v11,s13\n"
AW "a165,v12\n"
AR "v12,a185\n"
"v_subrev_f32 v12,s13,v12\n"
"v_fma_f32 v12,v12,v11,s13\n"
AW "a185,v12\n"
AR "v12,a205\n"
"v_subrev_f32 v12,s13,v12\n"
"v_fma_f32 v12,v12,v11,s13\n"
AW "a205,v12\n"
AR "v12,a225\n"
"v_subrev_f32 v12,s13,v12\n"
"v_fma_f32 v12,v12,v11,s13\n"
AW "a225,v12\n"
AR "v12,a6\n"
"v_subrev_f32 v12,s13,v12\n"
"v_fma_f32 v12,v12,v11,s13\n"
AW "a6,v12\n"
AR "v12,a26\n"
"v_subrev_f32 v12,s13,v12\n"
"v_fma_f32 v12,v12,v11,s13\n"
AW "a26,v12\n"
AR "v12,a46\n"
"v_subrev_f32 v12,s13,v12\n"
"v_fma_f32 v12,v12,v11,s13\n"
AW "a46,v12\n"
AR "v12,a66\n"
"v_subrev_f32 v12,s13,v12\n"
"v_fma_f32 v12,v12,v11,s13\n"
AW "a66,v12\n"
AR "v12,a86\n"
"v_subrev_f32 v12,s13,v12\n"
"v_fma_f32 v12,v12,v11,s13\n"
AW "a86,v12\n"
AR "v12,a106\n"
"v_subrev_f32 v12,s13,v12\n"
"v_fma_f32 v12,v12,v11,s13\n"
AW "a106,v12\n"
AR "v12,a126\n"
"v_subrev_f32 v12,s13,v12\n"
"v_fma_f32 v12,v12,v11,s13\n"
AW "a126,v12\n"
AR "v12,a146\n"
"v_subrev_f32 v12,s13,v12\n"
"v_fma_f32 v12,v12,v11,s13\n"
AW "a146,v12\n"
AR "v12,a166\n"
"v_subrev_f32 v12,s13,v12\n"
"v_fma_f32 v12,v12,v11,s13\n"
AW "a166,v12\n"
AR "v12,a186\n"
"v_subrev_f32 v12,s13,v12\n"
"v_fma_f32 v12,v12,v11,s13\n"
AW "a186,v12\n"
AR "v12,a206\n"
"v_subrev_f32 v12,s13,v12\n"
"v_fma_f32 v12,v12,v11,s13\n"
AW "a206,v12\n"
AR "v12,a226\n"
"v_subrev_f32 v12,s13,v12\n"
"v_fma_f32 v12,v12,v11,s13\n"
AW "a226,v12\n"
AR "v12,a7\n"
"v_subrev_f32 v12,s13,v12\n"
"v_fma_f32 v12,v12,v11,s13\n"
AW "a7,v12\n"
AR "v12,a27\n"
"v_subrev_f32 v12,s13,v12\n"
"v_fma_f32 v12,v12,v11,s13\n"
AW "a27,v12\n"
AR "v12,a47\n"
"v_subrev_f32 v12,s13,v12\n"
"v_fma_f32 v12,v12,v11,s13\n"
AW "a47,v12\n"
AR "v12,a67\n"
"v_subrev_f32 v12,s13,v12\n"
"v_fma_f32 v12,v12,v11,s13\n"
AW "a67,v12\n"
AR "v12,a87\n"
"v_subrev_f32 v12,s13,v12\n"
"v_fma_f32 v12,v12,v11,s13\n"
AW "a87,v12\n"
AR "v12,a107\n"
"v_subrev_f32 v12,s13,v12\n"
"v_fma_f32 v12,v12,v11,s13\n"
AW "a107,v12\n"
AR "v12,a127\n"
"v_subrev_f32 v12,s13,v12\n"
"v_fma_f32 v12,v12,v11,s13\n"
AW "a127,v12\n"
AR "v12,a147\n"
"v_subrev_f32 v12,s13,v12\n"
"v_fma_f32 v12,v12,v11,s13\n"
AW "a147,v12\n"
AR "v12,a167\n"
"v_subrev_f32 v12,s13,v12\n"
"v_fma_f32 v12,v12,v11,s13\n"
AW "a167,v12\n"
AR "v12,a187\n"
"v_subrev_f32 v12,s13,v12\n"
"v_fma_f32 v12,v12,v11,s13\n"
AW "a187,v12\n"
AR "v12,a207\n"
"v_subrev_f32 v12,s13,v12\n"
"v_fma_f32 v12,v12,v11,s13\n"
AW "a207,v12\n"
AR "v12,a227\n"
"v_subrev_f32 v12,s13,v12\n"
"v_fma_f32 v12,v12,v11,s13\n"
AW "a227,v12\n"
"v_mul_f32 v53,v58,v11\n"
"v_sub_f32 v36,v36,v10\n"
"v_sub_f32 v37,v37,v10\n"
"v_sub_f32 v38,v38,v10\n"
"v_sub_f32 v39,v39,v10\n"
"v_sub_f32 v96,v96,v10\n"
"v_sub_f32 v97,v97,v10\n"
"v_sub_f32 v98,v98,v10\n"
"v_sub_f32 v99,v99,v10\n"
"v_sub_f32 v76,v76,v10\n"
"v_sub_f32 v77,v77,v10\n"
"v_sub_f32 v78,v78,v10\n"
"v_sub_f32 v79,v79,v10\n"
"v_sub_f32 v116,v116,v10\n"
"v_sub_f32 v117,v117,v10\n"
"v_sub_f32 v118,v118,v10\n"
"v_sub_f32 v119,v119,v10\n"
"v_exp_f32 v76,v76\n"
"v_exp_f32 v77,v77\n"
"v_exp_f32 v78,v78\n"
"v_exp_f32 v79,v79\n"
"v_exp_f32 v116,v116\n"
"v_exp_f32 v117,v117\n"
"v_exp_f32 v118,v118\n"
"v_exp_f32 v119,v119\n"
"v_add_f32 v53,v53,v76\n"
"v_add_f32 v53,v53,v77\n"
"v_add_f32 v53,v53,v78\n"
"v_add_f32 v53,v53,v79\n"
"v_add_f32 v53,v53,v116\n"
"v_add_f32 v53,v53,v117\n"
"v_add_f32 v53,v53,v118\n"
"v_add_f32_e64 v53,v53,v119\n"
CV "v76,v76,v77\n"
CV "v77,v78,v79\n"
CV "v78,v116,v117\n"
CV "v79,v118,v119\n"
"v_max3_f32 v10,v80,v81,v82\n"
"v_max3_f32 v11,v83,v120,v121\n"
"v_max3_f32 v10,v10,v122,v123\n"
"v_max_f32 v10,v10,v11\n"
"v_mov_b32 v11,v10\n"
"s_nop 1\n"
"v_permlane16_swap_b32 v10,v11\n"
"v_max_f32 v10,v10,v11\n"
"v_mov_b32 v11,v10\n"
"s_nop 1\n"
"v_permlane32_swap_b32 v10,v11\n"
"v_max_f32_e64 v10,v10,v11\n"
"v_add_f32 v10,0x41300000,v10\n"
"v_max_f32 v10,0,v10\n"
"v_exp_f32 v11,-v10\n"
AR "v12,a8\n"
"v_subrev_f32 v12,s13,v12\n"
"v_fma_f32 v12,v12,v11,s13\n"
AW "a8,v12\n"
AR "v12,a28\n"
"v_subrev_f32 v12,s13,v12\n"
"v_fma_f32 v12,v12,v11,s13\n"
AW "a28,v12\n"
AR "v12,a48\n"
"v_subrev_f32 v12,s13,v12\n"
"v_fma_f32 v12,v12,v11,s13\n"
AW "a48,v12\n"
AR "v12,a68\n"
"v_subrev_f32 v12,s13,v12\n"
"v_fma_f32 v12,v12,v11,s13\n"
AW "a68,v12\n"
AR "v12,a88\n"
"v_subrev_f32 v12,s13,v12\n"
"v_fma_f32 v12,v12,v11,s13\n"
AW "a88,v12\n"
AR "v12,a108\n"
"v_subrev_f32 v12,s13,v12\n"
"v_fma_f32 v12,v12,v11,s13\n"
AW "a108,v12\n"
AR "v12,a128\n"
"v_subrev_f32 v12,s13,v12\n"
"v_fma_f32 v12,v12,v11,s13\n"
AW "a128,v12\n"
AR "v12,a148\n"
"v_subrev_f32 v12,s13,v12\n"
"v_fma_f32 v12,v12,v11,s13\n"
AW "a148,v12\n"
AR "v12,a168\n"
"v_subrev_f32 v12,s13,v12\n"
"v_fma_f32 v12,v12,v11,s13\n"
AW "a168,v12\n"
AR "v12,a188\n"
"v_subrev_f32 v12,s13,v12\n"
"v_fma_f32 v12,v12,v11,s13\n"
AW "a188,v12\n"
AR "v12,a208\n"
"v_subrev_f32 v12,s13,v12\n"
"v_fma_f32 v12,v12,v11,s13\n"
AW "a208,v12\n"
AR "v12,a228\n"
"v_subrev_f32 v12,s13,v12\n"
"v_fma_f32 v12,v12,v11,s13\n"
AW "a228,v12\n"
AR "v12,a9\n"
"v_subrev_f32 v12,s13,v12\n"
"v_fma_f32 v12,v12,v11,s13\n"
AW "a9,v12\n"
AR "v12,a29\n"
"v_subrev_f32 v12,s13,v12\n"
"v_fma_f32 v12,v12,v11,s13\n"
AW "a29,v12\n"
AR "v12,a49\n"
"v_subrev_f32 v12,s13,v12\n"
"v_fma_f32 v12,v12,v11,s13\n"
AW "a49,v12\n"
AR "v12,a69\n"
"v_subrev_f32 v12,s13,v12\n"
"v_fma_f32 v12,v12,v11,s13\n"
AW "a69,v12\n"
AR "v12,a89\n"
"v_subrev_f32 v12,s13,v12\n"
"v_fma_f32 v12,v12,v11,s13\n"
AW "a89,v12\n"
AR "v12,a109\n"
"v_subrev_f32 v12,s13,v12\n"
"v_fma_f32 v12,v12,v11,s13\n"
AW "a109,v12\n"
AR "v12,a129\n"
"v_subrev_f32 v12,s13,v12\n"
"v_fma_f32 v12,v12,v11,s13\n"
AW "a129,v12\n"
AR "v12,a149\n"
"v_subrev_f32 v12,s13,v12\n"
"v_fma_f32 v12,v12,v11,s13\n"
AW "a149,v12\n"
AR "v12,a169\n"
"v_subrev_f32 v12,s13,v12\n"
"v_fma_f32 v12,v12,v11,s13\n"
AW "a169,v12\n"
AR "v12,a189\n"
"v_subrev_f32 v12,s13,v12\n"
"v_fma_f32 v12,v12,v11,s13\n"
AW "a189,v12\n"
AR "v12,a209\n"
"v_subrev_f32 v12,s13,v12\n"
"v_fma_f32 v12,v12,v11,s13\n"
AW "a209,v12\n"
AR "v12,a229\n"
"v_subrev_f32 v12,s13,v12\n"
"v_fma_f32 v12,v12,v11,s13\n"
AW "a229,v12\n"
AR "v12,a10\n"
"v_subrev_f32 v12,s13,v12\n"
"v_fma_f32 v12,v12,v11,s13\n"
AW "a10,v12\n"
AR "v12,a30\n"
"v_subrev_f32 v12,s13,v12\n"
"v_fma_f32 v12,v12,v11,s13\n"
AW "a30,v12\n"
AR "v12,a50\n"
"v_subrev_f32 v12,s13,v12\n"
"v_fma_f32 v12,v12,v11,s13\n"
AW "a50,v12\n"
AR "v12,a70\n"
"v_subrev_f32 v12,s13,v12\n"
"v_fma_f32 v12,v12,v11,s13\n"
AW "a70,v12\n"
AR "v12,a90\n"
"v_subrev_f32 v12,s13,v12\n"
"v_fma_f32 v12,v12,v11,s13\n"
AW "a90,v12\n"
AR "v12,a110\n"
"v_subrev_f32 v12,s13,v12\n"
"v_fma_f32 v12,v12,v11,s13\n"
AW "a110,v12\n"
AR "v12,a130\n"
"v_subrev_f32 v12,s13,v12\n"
"v_fma_f32 v12,v12,v11,s13\n"
AW "a130,v12\n"
AR "v12,a150\n"
"v_subrev_f32 v12,s13,v12\n"
"v_fma_f32 v12,v12,v11,s13\n"
AW "a150,v12\n"
AR "v12,a170\n"
"v_subrev_f32 v12,s13,v12\n"
"v_fma_f32 v12,v12,v11,s13\n"
AW "a170,v12\n"
AR "v12,a190\n"
"v_subrev_f32 v12,s13,v12\n"
"v_fma_f32 v12,v12,v11,s13\n"
AW "a190,v12\n"
AR "v12,a210\n"
"v_subrev_f32 v12,s13,v12\n"
"v_fma_f32 v12,v12,v11,s13\n"
AW "a210,v12\n"
AR "v12,a230\n"
"v_subrev_f32 v12,s13,v12\n"
"v_fma_f32 v12,v12,v11,s13\n"
AW "a230,v12\n"
AR "v12,a11\n"
"v_subrev_f32 v12,s13,v12\n"
"v_fma_f32 v12,v12,v11,s13\n"
AW "a11,v12\n"
AR "v12,a31\n"
"v_subrev_f32 v12,s13,v12\n"
"v_fma_f32 v12,v12,v11,s13\n"
AW "a31,v12\n"
AR "v12,a51\n"
"v_subrev_f32 v12,s13,v12\n"
"v_fma_f32 v12,v12,v11,s13\n"
AW "a51,v12\n"
AR "v12,a71\n"
"v_subrev_f32 v12,s13,v12\n"
"v_fma_f32 v12,v12,v11,s13\n"
AW "a71,v12\n"
AR "v12,a91\n"
"v_subrev_f32 v12,s13,v12\n"
"v_fma_f32 v12,v12,v11,s13\n"
AW "a91,v12\n"
AR "v12,a111\n"
"v_subrev_f32 v12,s13,v12\n"
"v_fma_f32 v12,v12,v11,s13\n"
AW "a111,v12\n"
AR "v12,a131\n"
"v_subrev_f32 v12,s13,v12\n"
"v_fma_f32 v12,v12,v11,s13\n"
AW "a131,v12\n"
AR "v12,a151\n"
"v_subrev_f32 v12,s13,v12\n"
"v_fma_f32 v12,v12,v11,s13\n"
AW "a151,v12\n"
AR "v12,a171\n"
"v_subrev_f32 v12,s13,v12\n"
"v_fma_f32 v12,v12,v11,s13\n"
AW "a171,v12\n"
AR "v12,a191\n"
"v_subrev_f32 v12,s13,v12\n"
"v_fma_f32 v12,v12,v11,s13\n"
AW "a191,v12\n"
AR "v12,a211\n"
"v_subrev_f32 v12,s13,v12\n"
"v_fma_f32 v12,v12,v11,s13\n"
AW "a211,v12\n"
AR "v12,a231\n"
"v_subrev_f32 v12,s13,v12\n"
"v_fma_f32 v12,v12,v11,s13\n"
AW "a231,v12\n"
"v_mul_f32 v54,v59,v11\n"
"v_sub_f32 v40,v40,v10\n"
"v_sub_f32 v41,v41,v10\n"
"v_sub_f32 v42,v42,v10\n"
"v_sub_f32 v43,v43,v10\n"
"v_sub_f32 v100,v100,v10\n"
"v_sub_f32 v101,v101,v10\n"
"v_sub_f32 v102,v102,v10\n"
"v_sub_f32 v103,v103,v10\n"
"v_sub_f32 v80,v80,v10\n"
"v_sub_f32 v81,v81,v10\n"
"v_sub_f32 v82,v82,v10\n"
"v_sub_f32 v83,v83,v10\n"
"v_sub_f32 v120,v120,v10\n"
"v_sub_f32 v121,v121,v10\n"
"v_sub_f32 v122,v122,v10\n"
"v_sub_f32 v123,v123,v10\n"
"v_exp_f32 v80,v80\n"
"v_exp_f32 v81,v81\n"
"v_exp_f32 v82,v82\n"
"v_exp_f32 v83,v83\n"
"v_exp_f32 v120,v120\n"
"v_exp_f32 v121,v121\n"
"v_exp_f32 v122,v122\n"
"v_exp_f32 v123,v123\n"
"v_add_f32 v54,v54,v80\n"
"v_add_f32 v54,v54,v81\n"
"v_add_f32 v54,v54,v82\n"
"v_add_f32 v54,v54,v83\n"
"v_add_f32 v54,v54,v120\n"
"v_add_f32 v54,v54,v121\n"
"v_add_f32 v54,v54,v122\n"
"v_add_f32 v54,v54,v123\n"
CV "v80,v80,v81\n"
CV "v81,v82,v83\n"
CV "v82,v120,v121\n"
CV "v83,v122,v123\n"
"v_max3_f32 v10,v84,v85,v86\n"
"v_max3_f32 v11,v87,v124,v125\n"
"v_max3_f32 v10,v10,v126,v127\n"
"v_max_f32 v10,v10,v11\n"
"v_mov_b32 v11,v10\n"
"s_nop 1\n"
"v_permlane16_swap_b32 v10,v11\n"
"v_max_f32 v10,v10,v11\n"
"v_mov_b32 v11,v10\n"
"s_nop 1\n"
"v_permlane32_swap_b32 v10,v11\n"
"v_max_f32 v10,v10,v11\n"
"v_add_f32 v10,0x41300000,v10\n"
"v_max_f32 v10,0,v10\n"
"v_exp_f32 v11,-v10\n"
AR "v12,a12\n"
"v_subrev_f32 v12,s13,v12\n"
"v_fma_f32 v12,v12,v11,s13\n"
AW "a12,v12\n"
AR "v12,a32\n"
"v_subrev_f32 v12,s13,v12\n"
"v_fma_f32 v12,v12,v11,s13\n"
AW "a32,v12\n"
AR "v12,a52\n"
"v_subrev_f32 v12,s13,v12\n"
"v_fma_f32 v12,v12,v11,s13\n"
AW "a52,v12\n"
AR "v12,a72\n"
"v_subrev_f32 v12,s13,v12\n"
"v_fma_f32 v12,v12,v11,s13\n"
AW "a72,v12\n"
AR "v12,a92\n"
"v_subrev_f32 v12,s13,v12\n"
"v_fma_f32 v12,v12,v11,s13\n"
AW "a92,v12\n"
AR "v12,a112\n"
"v_subrev_f32 v12,s13,v12\n"
"v_fma_f32 v12,v12,v11,s13\n"
AW "a112,v12\n"
AR "v12,a132\n"
"v_subrev_f32 v12,s13,v12\n"
"v_fma_f32 v12,v12,v11,s13\n"
AW "a132,v12\n"
AR "v12,a152\n"
"v_subrev_f32 v12,s13,v12\n"
"v_fma_f32 v12,v12,v11,s13\n"
AW "a152,v12\n"
AR "v12,a172\n"
"v_subrev_f32 v12,s13,v12\n"
"v_fma_f32 v12,v12,v11,s13\n"
AW "a172,v12\n"
AR "v12,a192\n"
"v_subrev_f32 v12,s13,v12\n"
"v_fma_f32 v12,v12,v11,s13\n"
AW "a192,v12\n"
AR "v12,a212\n"
"v_subrev_f32 v12,s13,v12\n"
"v_fma_f32 v12,v12,v11,s13\n"
AW "a212,v12\n"
AR "v12,a232\n"
"v_subrev_f32 v12,s13,v12\n"
"v_fma_f32 v12,v12,v11,s13\n"
AW "a232,v12\n"
AR "v12,a13\n"
"v_subrev_f32 v12,s13,v12\n"
"v_fma_f32 v12,v12,v11,s13\n"
AW "a13,v12\n"
AR "v12,a33\n"
"v_subrev_f32 v12,s13,v12\n"
"v_fma_f32 v12,v12,v11,s13\n"
AW "a33,v12\n"
AR "v12,a53\n"
"v_subrev_f32 v12,s13,v12\n"
"v_fma_f32 v12,v12,v11,s13\n"
AW "a53,v12\n"
AR "v12,a73\n"
"v_subrev_f32 v12,s13,v12\n"
"v_fma_f32 v12,v12,v11,s13\n"
AW "a73,v12\n"
AR "v12,a93\n"
"v_subrev_f32 v12,s13,v12\n"
"v_fma_f32 v12,v12,v11,s13\n"
AW "a93,v12\n"
AR "v12,a113\n"
"v_subrev_f32 v12,s13,v12\n"
"v_fma_f32 v12,v12,v11,s13\n"
AW "a113,v12\n"
AR "v12,a133\n"
"v_subrev_f32 v12,s13,v12\n"
"v_fma_f32 v12,v12,v11,s13\n"
AW "a133,v12\n"
AR "v12,a153\n"
"v_subrev_f32 v12,s13,v12\n"
"v_fma_f32 v12,v12,v11,s13\n"
AW "a153,v12\n"
AR "v12,a173\n"
"v_subrev_f32 v12,s13,v12\n"
"v_fma_f32 v12,v12,v11,s13\n"
AW "a173,v12\n"
AR "v12,a193\n"
"v_subrev_f32 v12,s13,v12\n"
"v_fma_f32 v12,v12,v11,s13\n"
AW "a193,v12\n"
AR "v12,a213\n"
"v_subrev_f32 v12,s13,v12\n"
"v_fma_f32 v12,v12,v11,s13\n"
AW "a213,v12\n"
AR "v12,a233\n"
"v_subrev_f32 v12,s13,v12\n"
"v_fma_f32 v12,v12,v11,s13\n"
AW "a233,v12\n"
AR "v12,a14\n"
"v_subrev_f32 v12,s13,v12\n"
"v_fma_f32 v12,v12,v11,s13\n"
AW "a14,v12\n"
AR "v12,a34\n"
"v_subrev_f32 v12,s13,v12\n"
"v_fma_f32 v12,v12,v11,s13\n"
AW "a34,v12\n"
AR "v12,a54\n"
"v_subrev_f32 v12,s13,v12\n"
"v_fma_f32 v12,v12,v11,s13\n"
AW "a54,v12\n"
AR "v12,a74\n"
"v_subrev_f32 v12,s13,v12\n"
"v_fma_f32 v12,v12,v11,s13\n"
AW "a74,v12\n"
AR "v12,a94\n"
"v_subrev_f32 v12,s13,v12\n"
"v_fma_f32 v12,v12,v11,s13\n"
AW "a94,v12\n"
AR "v12,a114\n"
"v_subrev_f32 v12,s13,v12\n"
"v_fma_f32 v12,v12,v11,s13\n"
AW "a114,v12\n"
AR "v12,a134\n"
"v_subrev_f32 v12,s13,v12\n"
"v_fma_f32 v12,v12,v11,s13\n"
AW "a134,v12\n"
AR "v12,a154\n"
"v_subrev_f32 v12,s13,v12\n"
"v_fma_f32 v12,v12,v11,s13\n"
AW "a154,v12\n"
AR "v12,a174\n"
"v_subrev_f32 v12,s13,v12\n"
"v_fma_f32 v12,v12,v11,s13\n"
AW "a174,v12\n"
AR "v12,a194\n"
"v_subrev_f32 v12,s13,v12\n"
"v_fma_f32 v12,v12,v11,s13\n"
AW "a194,v12\n"
AR "v12,a214\n"
"v_subrev_f32 v12,s13,v12\n"
"v_fma_f32 v12,v12,v11,s13\n"
AW "a214,v12\n"
AR "v12,a234\n"
"v_subrev_f32 v12,s13,v12\n"
"v_fma_f32 v12,v12,v11,s13\n"
AW "a234,v12\n"
AR "v12,a15\n"
"v_subrev_f32 v12,s13,v12\n"
"v_fma_f32 v12,v12,v11,s13\n"
AW "a15,v12\n"
AR "v12,a35\n"
"v_subrev_f32 v12,s13,v12\n"
"v_fma_f32 v12,v12,v11,s13\n"
AW "a35,v12\n"
AR "v12,a55\n"
"v_subrev_f32 v12,s13,v12\n"
"v_fma_f32 v12,v12,v11,s13\n"
AW "a55,v12\n"
AR "v12,a75\n"
"v_subrev_f32 v12,s13,v12\n"
"v_fma_f32 v12,v12,v11,s13\n"
AW "a75,v12\n"
AR "v12,a95\n"
"v_subrev_f32 v12,s13,v12\n"
"v_fma_f32 v12,v12,v11,s13\n"
AW "a95,v12\n"
AR "v12,a115\n"
"v_subrev_f32 v12,s13,v12\n"
"v_fma_f32 v12,v12,v11,s13\n"
AW "a115,v12\n"
AR "v12,a135\n"
"v_subrev_f32 v12,s13,v12\n"
"v_fma_f32 v12,v12,v11,s13\n"
AW "a135,v12\n"
AR "v12,a155\n"
"v_subrev_f32 v12,s13,v12\n"
"v_fma_f32 v12,v12,v11,s13\n"
AW "a155,v12\n"
AR "v12,a175\n"
"v_subrev_f32 v12,s13,v12\n"
"v_fma_f32 v12,v12,v11,s13\n"
AW "a175,v12\n"
AR "v12,a195\n"
"v_subrev_f32 v12,s13,v12\n"
"v_fma_f32 v12,v12,v11,s13\n"
AW "a195,v12\n"
AR "v12,a215\n"
"v_subrev_f32 v12,s13,v12\n"
"v_fma_f32 v12,v12,v11,s13\n"
AW "a215,v12\n"
AR "v12,a235\n"
"v_subrev_f32 v12,s13,v12\n"
"v_fma_f32 v12,v12,v11,s13\n"
AW "a235,v12\n"
"v_mul_f32 v55,v60,v11\n"
"v_sub_f32 v44,v44,v10\n"
"v_sub_f32 v45,v45,v10\n"
"v_sub_f32 v46,v46,v10\n"
"v_sub_f32 v47,v47,v10\n"
"v_sub_f32 v104,v104,v10\n"
"v_sub_f32 v105,v105,v10\n"
"v_sub_f32 v106,v106,v10\n"
"v_sub_f32 v107,v107,v10\n"
"v_sub_f32 v84,v84,v10\n"
"v_sub_f32 v85,v85,v10\n"
"v_sub_f32 v86,v86,v10\n"
"v_sub_f32 v87,v87,v10\n"
"v_sub_f32 v124,v124,v10\n"
"v_sub_f32 v125,v125,v10\n"
"v_sub_f32 v126,v126,v10\n"
"v_sub_f32 v127,v127,v10\n"
"v_exp_f32 v84,v84\n"
"v_exp_f32 v85,v85\n"
"v_exp_f32 v86,v86\n"
"v_exp_f32 v87,v87\n"
"v_exp_f32 v124,v124\n"
"v_exp_f32 v125,v125\n"
"v_exp_f32 v126,v126\n"
"v_exp_f32 v127,v127\n"
"v_add_f32 v55,v55,v84\n"
"v_add_f32 v55,v55,v85\n"
"v_add_f32 v55,v55,v86\n"
"v_add_f32 v55,v55,v87\n"
"v_add_f32 v55,v55,v124\n"
"v_add_f32 v55,v55,v125\n"
"v_add_f32 v55,v55,v126\n"
"v_add_f32_e64 v55,v55,v127\n"
CV "v84,v84,v85\n"
CV "v85,v86,v87\n"
CV "v86,v124,v125\n"
CV "v87,v126,v127\n"
"v_max3_f32 v10,v88,v89,v90\n"
"v_max3_f32 v11,v91,v128,v129\n"
"v_max3_f32 v10,v10,v130,v131\n"
"v_max_f32 v10,v10,v11\n"
"v_mov_b32 v11,v10\n"
"s_nop 1\n"
"v_permlane16_swap_b32 v10,v11\n"
"v_max_f32 v10,v10,v11\n"
"v_mov_b32 v11,v10\n"
"s_nop 1\n"
"v_permlane32_swap_b32 v10,v11\n"
"v_max_f32 v10,v10,v11\n"
"v_add_f32 v10,0x41300000,v10\n"
"v_max_f32 v10,0,v10\n"
"v_exp_f32 v11,-v10\n"
AR "v12,a16\n"
"v_subrev_f32 v12,s13,v12\n"
"v_fma_f32 v12,v12,v11,s13\n"
AW "a16,v12\n"
AR "v12,a36\n"
"v_subrev_f32 v12,s13,v12\n"
"v_fma_f32 v12,v12,v11,s13\n"
AW "a36,v12\n"
AR "v12,a56\n"
"v_subrev_f32 v12,s13,v12\n"
"v_fma_f32 v12,v12,v11,s13\n"
AW "a56,v12\n"
AR "v12,a76\n"
"v_subrev_f32 v12,s13,v12\n"
"v_fma_f32 v12,v12,v11,s13\n"
AW "a76,v12\n"
AR "v12,a96\n"
"v_subrev_f32 v12,s13,v12\n"
"v_fma_f32 v12,v12,v11,s13\n"
AW "a96,v12\n"
AR "v12,a116\n"
"v_subrev_f32 v12,s13,v12\n"
"v_fma_f32 v12,v12,v11,s13\n"
AW "a116,v12\n"
AR "v12,a136\n"
"v_subrev_f32 v12,s13,v12\n"
"v_fma_f32 v12,v12,v11,s13\n"
AW "a136,v12\n"
AR "v12,a156\n"
"v_subrev_f32 v12,s13,v12\n"
"v_fma_f32 v12,v12,v11,s13\n"
AW "a156,v12\n"
AR "v12,a176\n"
"v_subrev_f32 v12,s13,v12\n"
"v_fma_f32 v12,v12,v11,s13\n"
AW "a176,v12\n"
AR "v12,a196\n"
"v_subrev_f32 v12,s13,v12\n"
"v_fma_f32 v12,v12,v11,s13\n"
AW "a196,v12\n"
AR "v12,a216\n"
"v_subrev_f32 v12,s13,v12\n"
"v_fma_f32 v12,v12,v11,s13\n"
AW "a216,v12\n"
AR "v12,a236\n"
"v_subrev_f32 v12,s13,v12\n"
"v_fma_f32 v12,v12,v11,s13\n"
AW "a236,v12\n"
AR "v12,a17\n"
"v_subrev_f32 v12,s13,v12\n"
"v_fma_f32 v12,v12,v11,s13\n"
AW "a17,v12\n"
AR "v12,a37\n"
"v_subrev_f32 v12,s13,v12\n"
"v_fma_f32 v12,v12,v11,s13\n"
AW "a37,v12\n"
AR "v12,a57\n"
"v_subrev_f32 v12,s13,v12\n"
"v_fma_f32 v12,v12,v11,s13\n"
AW "a57,v12\n"
AR "v12,a77\n"
"v_subrev_f32 v12,s13,v12\n"
"v_fma_f32 v12,v12,v11,s13\n"
AW "a77,v12\n"
AR "v12,a97\n"
"v_subrev_f32 v12,s13,v12\n"
"v_fma_f32 v12,v12,v11,s13\n"
AW "a97,v12\n"
AR "v12,a117\n"
"v_subrev_f32 v12,s13,v12\n"
"v_fma_f32 v12,v12,v11,s13\n"
AW "a117,v12\n"
AR "v12,a137\n"
"v_subrev_f32 v12,s13,v12\n"
"v_fma_f32 v12,v12,v11,s13\n"
AW "a137,v12\n"
AR "v12,a157\n"
"v_subrev_f32 v12,s13,v12\n"
"v_fma_f32 v12,v12,v11,s13\n"
AW "a157,v12\n"
AR "v12,a177\n"
"v_subrev_f32 v12,s13,v12\n"
"v_fma_f32 v12,v12,v11,s13\n"
AW "a177,v12\n"
AR "v12,a197\n"
"v_subrev_f32 v12,s13,v12\n"
"v_fma_f32 v12,v12,v11,s13\n"
AW "a197,v12\n"
AR "v12,a217\n"
"v_subrev_f32 v12,s13,v12\n"
"v_fma_f32 v12,v12,v11,s13\n"
AW "a217,v12\n"
AR "v12,a237\n"
"v_subrev_f32 v12,s13,v12\n"
"v_fma_f32 v12,v12,v11,s13\n"
AW "a237,v12\n"
AR "v12,a18\n"
"v_subrev_f32 v12,s13,v12\n"
"v_fma_f32 v12,v12,v11,s13\n"
AW "a18,v12\n"
AR "v12,a38\n"
"v_subrev_f32 v12,s13,v12\n"
"v_fma_f32 v12,v12,v11,s13\n"
AW "a38,v12\n"
AR "v12,a58\n"
"v_subrev_f32 v12,s13,v12\n"
"v_fma_f32 v12,v12,v11,s13\n"
AW "a58,v12\n"
AR "v12,a78\n"
"v_subrev_f32 v12,s13,v12\n"
"v_fma_f32 v12,v12,v11,s13\n"
AW "a78,v12\n"
AR "v12,a98\n"
"v_subrev_f32 v12,s13,v12\n"
"v_fma_f32 v12,v12,v11,s13\n"
AW "a98,v12\n"
AR "v12,a118\n"
"v_subrev_f32 v12,s13,v12\n"
"v_fma_f32 v12,v12,v11,s13\n"
AW "a118,v12\n"
AR "v12,a138\n"
"v_subrev_f32 v12,s13,v12\n"
"v_fma_f32 v12,v12,v11,s13\n"
AW "a138,v12\n"
AR "v12,a158\n"
"v_subrev_f32 v12,s13,v12\n"
"v_fma_f32 v12,v12,v11,s13\n"
AW "a158,v12\n"
AR "v12,a178\n"
"v_subrev_f32 v12,s13,v12\n"
"v_fma_f32 v12,v12,v11,s13\n"
AW "a178,v12\n"
AR "v12,a198\n"
"v_subrev_f32 v12,s13,v12\n"
"v_fma_f32 v12,v12,v11,s13\n"
AW "a198,v12\n"
AR "v12,a218\n"
"v_subrev_f32 v12,s13,v12\n"
"v_fma_f32 v12,v12,v11,s13\n"
AW "a218,v12\n"
AR "v12,a238\n"
"v_subrev_f32 v12,s13,v12\n"
"v_fma_f32 v12,v12,v11,s13\n"
AW "a238,v12\n"
AR "v12,a19\n"
"v_subrev_f32 v12,s13,v12\n"
"v_fma_f32 v12,v12,v11,s13\n"
AW "a19,v12\n"
AR "v12,a39\n"
"v_subrev_f32 v12,s13,v12\n"
"v_fma_f32 v12,v12,v11,s13\n"
AW "a39,v12\n"
AR "v12,a59\n"
"v_subrev_f32 v12,s13,v12\n"
"v_fma_f32 v12,v12,v11,s13\n"
AW "a59,v12\n"
AR "v12,a79\n"
"v_subrev_f32 v12,s13,v12\n"
"v_fma_f32 v12,v12,v11,s13\n"
AW "a79,v12\n"
AR "v12,a99\n"
"v_subrev_f32 v12,s13,v12\n"
"v_fma_f32 v12,v12,v11,s13\n"
AW "a99,v12\n"
AR "v12,a119\n"
"v_subrev_f32 v12,s13,v12\n"
"v_fma_f32 v12,v12,v11,s13\n"
AW "a119,v12\n"
AR "v12,a139\n"
"v_subrev_f32 v12,s13,v12\n"
"v_fma_f32 v12,v12,v11,s13\n"
AW "a139,v12\n"
AR "v12,a159\n"
"v_subrev_f32 v12,s13,v12\n"
"v_fma_f32 v12,v12,v11,s13\n"
AW "a159,v12\n"
AR "v12,a179\n"
"v_subrev_f32 v12,s13,v12\n"
"v_fma_f32 v12,v12,v11,s13\n"
AW "a179,v12\n"
AR "v12,a199\n"
"v_subrev_f32 v12,s13,v12\n"
"v_fma_f32 v12,v12,v11,s13\n"
AW "a199,v12\n"
AR "v12,a219\n"
"v_subrev_f32 v12,s13,v12\n"
"v_fma_f32 v12,v12,v11,s13\n"
AW "a219,v12\n"
AR "v12,a239\n"
"v_subrev_f32 v12,s13,v12\n"
"v_fma_f32 v12,v12,v11,s13\n"
AW "a239,v12\n"
"v_mul_f32 v56,v61,v11\n"
"v_sub_f32 v48,v48,v10\n"
"v_sub_f32 v49,v49,v10\n"
"v_sub_f32 v50,v50,v10\n"
"v_sub_f32 v51,v51,v10\n"
"v_sub_f32 v108,v108,v10\n"
"v_sub_f32 v109,v109,v10\n"
"v_sub_f32 v110,v110,v10\n"
"v_sub_f32 v111,v111,v10\n"
"v_sub_f32 v88,v88,v10\n"
"v_sub_f32 v89,v89,v10\n"
"v_sub_f32 v90,v90,v10\n"
"v_sub_f32 v91,v91,v10\n"
"v_sub_f32 v128,v128,v10\n"
"v_sub_f32 v129,v129,v10\n"
"v_sub_f32 v130,v130,v10\n"
"v_sub_f32 v131,v131,v10\n"
"v_exp_f32 v88,v88\n"
"v_exp_f32 v89,v89\n"
"v_exp_f32 v90,v90\n"
"v_exp_f32 v91,v91\n"
"v_exp_f32 v128,v128\n"
"v_exp_f32 v129,v129\n"
"v_exp_f32 v130,v130\n"
"v_exp_f32 v131,v131\n"
"v_add_f32 v56,v56,v88\n"
"v_add_f32 v56,v56,v89\n"
"v_add_f32 v56,v56,v90\n"
"v_add_f32 v56,v56,v91\n"
"v_add_f32 v56,v56,v128\n"
"v_add_f32 v56,v56,v129\n"
"v_add_f32 v56,v56,v130\n"
"v_add_f32_e64 v56,v56,v131\n"
CV "v88,v88,v89\n"
CV "v89,v90,v91\n"
CV "v90,v128,v129\n"
CV "v91,v130,v131\n"
"s_nop 7\n"
"s_setpc_b64 s[52:53]\n"
".Lslow_1:\n"
"s_nop 7\n"
"s_nop 7\n"
"v_and_b32 v10,240,v9\n"
"v_mul_u32_u24 v10,24,v10\n"
"v_lshrrev_b32 v11,4,v9\n"
"v_and_or_b32 v10,v11,48,v10\n"
"s_mov_b32 s59,s34\n"
"s_mov_b32 s34,s20\n"
"s_add_u32 s58,s74,0\n"
"s_nop 3\n"
LD "v[58:61],v10,s[32:35],s58 offen\n"
"s_waitcnt vmcnt(0)\n"
MF "v[92:95],v[58:61],v[136:139],v[32:35]\n"
MF "v[96:99],v[58:61],v[140:143],v[36:39]\n"
MF "v[100:103],v[58:61],v[144:147],v[40:43]\n"
MF "v[104:107],v[58:61],v[148:151],v[44:47]\n"
MF "v[108:111],v[58:61],v[152:155],v[48:51]\n"
"s_add_u32 s58,s74,0\n"
"s_nop 3\n"
LD "v[58:61],v10,s[32:35],s58 offen offset:64\n"
"s_waitcnt vmcnt(0)\n"
MF "v[92:95],v[58:61],v[156:159],v[92:95]\n"
MF "v[96:99],v[58:61],v[160:163],v[96:99]\n"
MF "v[100:103],v[58:61],v[164:167],v[100:103]\n"
MF "v[104:107],v[58:61],v[168:171],v[104:107]\n"
MF "v[108:111],v[58:61],v[172:175],v[108:111]\n"
"s_add_u32 s58,s74,0\n"
"s_nop 3\n"
LD "v[58:61],v10,s[32:35],s58 offen offset:128\n"
"s_waitcnt vmcnt(0)\n"
MF "v[92:95],v[58:61],v[176:179],v[92:95]\n"
MF "v[96:99],v[58:61],v[180:183],v[96:99]\n"
MF "v[100:103],v[58:61],v[184:187],v[100:103]\n"
MF "v[104:107],v[58:61],v[188:191],v[104:107]\n"
MF "v[108:111],v[58:61],v[192:195],v[108:111]\n"
"s_add_u32 s58,s74,0\n"
"s_nop 3\n"
LD "v[58:61],v10,s[32:35],s58 offen offset:192\n"
"s_waitcnt vmcnt(0)\n"
MF "v[92:95],v[58:61],v[196:199],v[92:95]\n"
MF "v[96:99],v[58:61],v[200:203],v[96:99]\n"
MF "v[100:103],v[58:61],v[204:207],v[100:103]\n"
MF "v[104:107],v[58:61],v[208:211],v[104:107]\n"
MF "v[108:111],v[58:61],v[212:215],v[108:111]\n"
"s_add_u32 s58,s74,0\n"
"s_nop 3\n"
LD "v[58:61],v10,s[32:35],s58 offen offset:256\n"
"s_waitcnt vmcnt(0)\n"
MF "v[92:95],v[58:61],v[216:219],v[92:95]\n"
MF "v[96:99],v[58:61],v[220:223],v[96:99]\n"
MF "v[100:103],v[58:61],v[224:227],v[100:103]\n"
MF "v[104:107],v[58:61],v[228:231],v[104:107]\n"
MF "v[108:111],v[58:61],v[232:235],v[108:111]\n"
"s_add_u32 s58,s74,0\n"
"s_nop 3\n"
LD "v[58:61],v10,s[32:35],s58 offen offset:320\n"
"s_waitcnt vmcnt(0)\n"
MF "v[92:95],v[58:61],v[236:239],v[92:95]\n"
MF "v[96:99],v[58:61],v[240:243],v[96:99]\n"
MF "v[100:103],v[58:61],v[244:247],v[100:103]\n"
MF "v[104:107],v[58:61],v[248:251],v[104:107]\n"
MF "v[108:111],v[58:61],v[252:255],v[108:111]\n"
"s_add_u32 s58,s74,6144\n"
"s_nop 3\n"
LD "v[58:61],v10,s[32:35],s58 offen\n"
"s_waitcnt vmcnt(0)\n"
MF "v[112:115],v[58:61],v[136:139],v[32:35]\n"
MF "v[116:119],v[58:61],v[140:143],v[36:39]\n"
MF "v[120:123],v[58:61],v[144:147],v[40:43]\n"
MF "v[124:127],v[58:61],v[148:151],v[44:47]\n"
MF "v[128:131],v[58:61],v[152:155],v[48:51]\n"
"s_add_u32 s58,s74,6144\n"
"s_nop 3\n"
LD "v[58:61],v10,s[32:35],s58 offen offset:64\n"
"s_waitcnt vmcnt(0)\n"
MF "v[112:115],v[58:61],v[156:159],v[112:115]\n"
MF "v[116:119],v[58:61],v[160:163],v[116:119]\n"
MF "v[120:123],v[58:61],v[164:167],v[120:123]\n"
MF "v[124:127],v[58:61],v[168:171],v[124:127]\n"
MF "v[128:131],v[58:61],v[172:175],v[128:131]\n"
"s_add_u32 s58,s74,6144\n"
"s_nop 3\n"
LD "v[58:61],v10,s[32:35],s58 offen offset:128\n"
"s_waitcnt vmcnt(0)\n"
MF "v[112:115],v[58:61],v[176:179],v[112:115]\n"
MF "v[116:119],v[58:61],v[180:183],v[116:119]\n"
MF "v[120:123],v[58:61],v[184:187],v[120:123]\n"
MF "v[124:127],v[58:61],v[188:191],v[124:127]\n"
MF "v[128:131],v[58:61],v[192:195],v[128:131]\n"
"s_add_u32 s58,s74,6144\n"
"s_nop 3\n"
LD "v[58:61],v10,s[32:35],s58 offen offset:192\n"
"s_waitcnt vmcnt(0)\n"
MF "v[112:115],v[58:61],v[196:199],v[112:115]\n"
MF "v[116:119],v[58:61],v[200:203],v[116:119]\n"
MF "v[120:123],v[58:61],v[204:207],v[120:123]\n"
MF "v[124:127],v[58:61],v[208:211],v[124:127]\n"
MF "v[128:131],v[58:61],v[212:215],v[128:131]\n"
"s_add_u32 s58,s74,6144\n"
"s_nop 3\n"
LD "v[58:61],v10,s[32:35],s58 offen offset:256\n"
"s_waitcnt vmcnt(0)\n"
MF "v[112:115],v[58:61],v[216:219],v[112:115]\n"
MF "v[116:119],v[58:61],v[220:223],v[116:119]\n"
MF "v[120:123],v[58:61],v[224:227],v[120:123]\n"
MF "v[124:127],v[58:61],v[228:231],v[124:127]\n"
MF "v[128:131],v[58:61],v[232:235],v[128:131]\n"
"s_add_u32 s58,s74,6144\n"
"s_nop 3\n"
LD "v[58:61],v10,s[32:35],s58 offen offset:320\n"
"s_waitcnt vmcnt(0)\n"
MF "v[112:115],v[58:61],v[236:239],v[112:115]\n"
MF "v[116:119],v[58:61],v[240:243],v[116:119]\n"
MF "v[120:123],v[58:61],v[244:247],v[120:123]\n"
MF "v[124:127],v[58:61],v[248:251],v[124:127]\n"
MF "v[128:131],v[58:61],v[252:255],v[128:131]\n"
"s_nop 7\n"
"s_mov_b32 s34,s59\n"
"v_max3_f32 v10,v92,v93,v94\n"
"s_nop 0\n"
"v_max3_f32 v11,v95,v112,v113\n"
"v_max3_f32 v10,v10,v114,v115\n"
"v_max_f32 v10,v10,v11\n"
"v_mov_b32 v11,v10\n"
"s_nop 1\n"
"v_permlane16_swap_b32 v10,v11\n"
"v_max_f32 v10,v10,v11\n"
"v_mov_b32 v11,v10\n"
"s_nop 1\n"
"v_permlane32_swap_b32 v10,v11\n"
"v_max_f32 v10,v10,v11\n"
"v_add_f32 v10,0x41300000,v10\n"
"v_max_f32_e64 v10,0,v10\n"
"v_exp_f32 v11,-v10\n"
AR "v12,a0\n"
"v_subrev_f32 v12,s13,v12\n"
"v_fma_f32 v12,v12,v11,s13\n"
AW "a0,v12\n"
AR "v12,a20\n"
"v_subrev_f32 v12,s13,v12\n"
"v_fma_f32 v12,v12,v11,s13\n"
AW "a20,v12\n"
AR "v12,a40\n"
"v_subrev_f32 v12,s13,v12\n"
"v_fma_f32 v12,v12,v11,s13\n"
AW "a40,v12\n"
AR "v12,a60\n"
"v_subrev_f32 v12,s13,v12\n"
"v_fma_f32 v12,v12,v11,s13\n"
AW "a60,v12\n"
AR "v12,a80\n"
"v_subrev_f32 v12,s13,v12\n"
"v_fma_f32 v12,v12,v11,s13\n"
AW "a80,v12\n"
AR "v12,a100\n"
"v_subrev_f32 v12,s13,v12\n"
"v_fma_f32 v12,v12,v11,s13\n"
AW "a100,v12\n"
AR "v12,a120\n"
"v_subrev_f32 v12,s13,v12\n"
"v_fma_f32 v12,v12,v11,s13\n"
AW "a120,v12\n"
AR "v12,a140\n"
"v_subrev_f32 v12,s13,v12\n"
"v_fma_f32 v12,v12,v11,s13\n"
AW "a140,v12\n"
AR "v12,a160\n"
"v_subrev_f32 v12,s13,v12\n"
"v_fma_f32 v12,v12,v11,s13\n"
AW "a160,v12\n"
AR "v12,a180\n"
"v_subrev_f32 v12,s13,v12\n"
"v_fma_f32 v12,v12,v11,s13\n"
AW "a180,v12\n"
AR "v12,a200\n"
"v_subrev_f32 v12,s13,v12\n"
"v_fma_f32 v12,v12,v11,s13\n"
AW "a200,v12\n"
AR "v12,a220\n"
"v_subrev_f32 v12,s13,v12\n"
"v_fma_f32 v12,v12,v11,s13\n"
AW "a220,v12\n"
AR "v12,a1\n"
"v_subrev_f32 v12,s13,v12\n"
"v_fma_f32 v12,v12,v11,s13\n"
AW "a1,v12\n"
AR "v12,a21\n"
"v_subrev_f32 v12,s13,v12\n"
"v_fma_f32 v12,v12,v11,s13\n"
AW "a21,v12\n"
AR "v12,a41\n"
"v_subrev_f32 v12,s13,v12\n"
"v_fma_f32 v12,v12,v11,s13\n"
AW "a41,v12\n"
AR "v12,a61\n"
"v_subrev_f32 v12,s13,v12\n"
"v_fma_f32 v12,v12,v11,s13\n"
AW "a61,v12\n"
AR "v12,a81\n"
"v_subrev_f32 v12,s13,v12\n"
"v_fma_f32 v12,v12,v11,s13\n"
AW "a81,v12\n"
AR "v12,a101\n"
"v_subrev_f32 v12,s13,v12\n"
"v_fma_f32 v12,v12,v11,s13\n"
AW "a101,v12\n"
AR "v12,a121\n"
"v_subrev_f32 v12,s13,v12\n"
"v_fma_f32 v12,v12,v11,s13\n"
AW "a121,v12\n"
AR "v12,a141\n"
"v_subrev_f32 v12,s13,v12\n"
"v_fma_f32 v12,v12,v11,s13\n"
AW "a141,v12\n"
AR "v12,a161\n"
"v_subrev_f32 v12,s13,v12\n"
"v_fma_f32 v12,v12,v11,s13\n"
AW "a161,v12\n"
AR "v12,a181\n"
"v_subrev_f32 v12,s13,v12\n"
"v_fma_f32 v12,v12,v11,s13\n"
AW "a181,v12\n"
AR "v12,a201\n"
"v_subrev_f32 v12,s13,v12\n"
"v_fma_f32 v12,v12,v11,s13\n"
AW "a201,v12\n"
AR "v12,a221\n"
"v_subrev_f32 v12,s13,v12\n"
"v_fma_f32 v12,v12,v11,s13\n"
AW "a221,v12\n"
AR "v12,a2\n"
"v_subrev_f32 v12,s13,v12\n"
"v_fma_f32 v12,v12,v11,s13\n"
AW "a2,v12\n"
AR "v12,a22\n"
"v_subrev_f32 v12,s13,v12\n"
"v_fma_f32 v12,v12,v11,s13\n"
AW "a22,v12\n"
AR "v12,a42\n"
"v_subrev_f32 v12,s13,v12\n"
"v_fma_f32 v12,v12,v11,s13\n"
AW "a42,v12\n"
AR "v12,a62\n"
"v_subrev_f32 v12,s13,v12\n"
"v_fma_f32 v12,v12,v11,s13\n"
AW "a62,v12\n"
AR "v12,a82\n"
"v_subrev_f32 v12,s13,v12\n"
"v_fma_f32 v12,v12,v11,s13\n"
AW "a82,v12\n"
AR "v12,a102\n"
"v_subrev_f32 v12,s13,v12\n"
"v_fma_f32 v12,v12,v11,s13\n"
AW "a102,v12\n"
AR "v12,a122\n"
"v_subrev_f32 v12,s13,v12\n"
"v_fma_f32 v12,v12,v11,s13\n"
AW "a122,v12\n"
AR "v12,a142\n"
"v_subrev_f32 v12,s13,v12\n"
"v_fma_f32 v12,v12,v11,s13\n"
AW "a142,v12\n"
AR "v12,a162\n"
"v_subrev_f32 v12,s13,v12\n"
"v_fma_f32 v12,v12,v11,s13\n"
AW "a162,v12\n"
AR "v12,a182\n"
"v_subrev_f32 v12,s13,v12\n"
"v_fma_f32 v12,v12,v11,s13\n"
AW "a182,v12\n"
AR "v12,a202\n"
"v_subrev_f32 v12,s13,v12\n"
"v_fma_f32 v12,v12,v11,s13\n"
AW "a202,v12\n"
AR "v12,a222\n"
"v_subrev_f32 v12,s13,v12\n"
"v_fma_f32 v12,v12,v11,s13\n"
AW "a222,v12\n"
AR "v12,a3\n"
"v_subrev_f32 v12,s13,v12\n"
"v_fma_f32 v12,v12,v11,s13\n"
AW "a3,v12\n"
AR "v12,a23\n"
"v_subrev_f32 v12,s13,v12\n"
"v_fma_f32 v12,v12,v11,s13\n"
AW "a23,v12\n"
AR "v12,a43\n"
"v_subrev_f32 v12,s13,v12\n"
"v_fma_f32 v12,v12,v11,s13\n"
AW "a43,v12\n"
AR "v12,a63\n"
"v_subrev_f32 v12,s13,v12\n"
"v_fma_f32 v12,v12,v11,s13\n"
AW "a63,v12\n"
AR "v12,a83\n"
"v_subrev_f32 v12,s13,v12\n"
"v_fma_f32 v12,v12,v11,s13\n"
AW "a83,v12\n"
AR "v12,a103\n"
"v_subrev_f32 v12,s13,v12\n"
"v_fma_f32 v12,v12,v11,s13\n"
AW "a103,v12\n"
AR "v12,a123\n"
"v_subrev_f32 v12,s13,v12\n"
"v_fma_f32 v12,v12,v11,s13\n"
AW "a123,v12\n"
AR "v12,a143\n"
"v_subrev_f32 v12,s13,v12\n"
"v_fma_f32 v12,v12,v11,s13\n"
AW "a143,v12\n"
AR "v12,a163\n"
"v_subrev_f32 v12,s13,v12\n"
"v_fma_f32 v12,v12,v11,s13\n"
AW "a163,v12\n"
AR "v12,a183\n"
"v_subrev_f32 v12,s13,v12\n"
"v_fma_f32 v12,v12,v11,s13\n"
AW "a183,v12\n"
AR "v12,a203\n"
"v_subrev_f32 v12,s13,v12\n"
"v_fma_f32 v12,v12,v11,s13\n"
AW "a203,v12\n"
AR "v12,a223\n"
"v_subrev_f32 v12,s13,v12\n"
"v_fma_f32 v12,v12,v11,s13\n"
AW "a223,v12\n"
"v_mul_f32 v57,v52,v11\n"
"v_sub_f32 v32,v32,v10\n"
"v_sub_f32 v33,v33,v10\n"
"v_sub_f32 v34,v34,v10\n"
"v_sub_f32 v35,v35,v10\n"
"v_sub_f32 v72,v72,v10\n"
"v_sub_f32 v73,v73,v10\n"
"v_sub_f32 v74,v74,v10\n"
"v_sub_f32 v75,v75,v10\n"
"v_sub_f32 v92,v92,v10\n"
"v_sub_f32 v93,v93,v10\n"
"v_sub_f32 v94,v94,v10\n"
"v_sub_f32 v95,v95,v10\n"
"v_sub_f32 v112,v112,v10\n"
"v_sub_f32 v113,v113,v10\n"
"v_sub_f32 v114,v114,v10\n"
"v_sub_f32 v115,v115,v10\n"
"v_exp_f32 v92,v92\n"
"v_exp_f32 v93,v93\n"
"v_exp_f32 v94,v94\n"
"v_exp_f32 v95,v95\n"
"v_exp_f32 v112,v112\n"
"v_exp_f32 v113,v113\n"
"v_exp_f32 v114,v114\n"
"v_exp_f32 v115,v115\n"
"v_add_f32 v57,v57,v92\n"
"v_add_f32 v57,v57,v93\n"
"v_add_f32 v57,v57,v94\n"
"v_add_f32 v57,v57,v95\n"
"v_add_f32 v57,v57,v112\n"
"v_add_f32 v57,v57,v113\n"
"v_add_f32 v57,v57,v114\n"
"v_add_f32_e64 v57,v57,v115\n"
CV "v92,v92,v93\n"
CV "v93,v94,v95\n"
CV "v94,v112,v113\n"
CV "v95,v114,v115\n"
"v_max3_f32 v10,v96,v97,v98\n"
"v_max3_f32 v11,v99,v116,v117\n"
"v_max3_f32 v10,v10,v118,v119\n"
"v_max_f32 v10,v10,v11\n"
"v_mov_b32 v11,v10\n"
"s_nop 1\n"
"v_permlane16_swap_b32 v10,v11\n"
"v_max_f32 v10,v10,v11\n"
"v_mov_b32 v11,v10\n"
"s_nop 1\n"
"v_permlane32_swap_b32 v10,v11\n"
"v_max_f32 v10,v10,v11\n"
"v_add_f32 v10,0x41300000,v10\n"
"v_max_f32 v10,0,v10\n"
"v_exp_f32 v11,-v10\n"
AR "v12,a4\n"
"v_subrev_f32 v12,s13,v12\n"
"v_fma_f32 v12,v12,v11,s13\n"
AW "a4,v12\n"
AR "v12,a24\n"
"v_subrev_f32 v12,s13,v12\n"
"v_fma_f32 v12,v12,v11,s13\n"
AW "a24,v12\n"
AR "v12,a44\n"
"v_subrev_f32 v12,s13,v12\n"
"v_fma_f32 v12,v12,v11,s13\n"
AW "a44,v12\n"
AR "v12,a64\n"
"v_subrev_f32 v12,s13,v12\n"
"v_fma_f32 v12,v12,v11,s13\n"
AW "a64,v12\n"
AR "v12,a84\n"
"v_subrev_f32 v12,s13,v12\n"
"v_fma_f32 v12,v12,v11,s13\n"
AW "a84,v12\n"
AR "v12,a104\n"
"v_subrev_f32 v12,s13,v12\n"
"v_fma_f32 v12,v12,v11,s13\n"
AW "a104,v12\n"
AR "v12,a124\n"
"v_subrev_f32 v12,s13,v12\n"
"v_fma_f32 v12,v12,v11,s13\n"
AW "a124,v12\n"
AR "v12,a144\n"
"v_subrev_f32 v12,s13,v12\n"
"v_fma_f32 v12,v12,v11,s13\n"
AW "a144,v12\n"
AR "v12,a164\n"
"v_subrev_f32 v12,s13,v12\n"
"v_fma_f32 v12,v12,v11,s13\n"
AW "a164,v12\n"
AR "v12,a184\n"
"v_subrev_f32 v12,s13,v12\n"
"v_fma_f32 v12,v12,v11,s13\n"
AW "a184,v12\n"
AR "v12,a204\n"
"v_subrev_f32 v12,s13,v12\n"
"v_fma_f32 v12,v12,v11,s13\n"
AW "a204,v12\n"
AR "v12,a224\n"
"v_subrev_f32 v12,s13,v12\n"
"v_fma_f32 v12,v12,v11,s13\n"
AW "a224,v12\n"
AR "v12,a5\n"
"v_subrev_f32 v12,s13,v12\n"
"v_fma_f32 v12,v12,v11,s13\n"
AW "a5,v12\n"
AR "v12,a25\n"
"v_subrev_f32 v12,s13,v12\n"
"v_fma_f32 v12,v12,v11,s13\n"
AW "a25,v12\n"
AR "v12,a45\n"
"v_subrev_f32 v12,s13,v12\n"
"v_fma_f32 v12,v12,v11,s13\n"
AW "a45,v12\n"
AR "v12,a65\n"
"v_subrev_f32 v12,s13,v12\n"
"v_fma_f32 v12,v12,v11,s13\n"
AW "a65,v12\n"
AR "v12,a85\n"
"v_subrev_f32 v12,s13,v12\n"
"v_fma_f32 v12,v12,v11,s13\n"
AW "a85,v12\n"
AR "v12,a105\n"
"v_subrev_f32 v12,s13,v12\n"
"v_fma_f32 v12,v12,v11,s13\n"
AW "a105,v12\n"
AR "v12,a125\n"
"v_subrev_f32 v12,s13,v12\n"
"v_fma_f32 v12,v12,v11,s13\n"
AW "a125,v12\n"
AR "v12,a145\n"
"v_subrev_f32 v12,s13,v12\n"
"v_fma_f32 v12,v12,v11,s13\n"
AW "a145,v12\n"
AR "v12,a165\n"
"v_subrev_f32 v12,s13,v12\n"
"v_fma_f32 v12,v12,v11,s13\n"
AW "a165,v12\n"
AR "v12,a185\n"
"v_subrev_f32 v12,s13,v12\n"
"v_fma_f32 v12,v12,v11,s13\n"
AW "a185,v12\n"
AR "v12,a205\n"
"v_subrev_f32 v12,s13,v12\n"
"v_fma_f32 v12,v12,v11,s13\n"
AW "a205,v12\n"
AR "v12,a225\n"
"v_subrev_f32 v12,s13,v12\n"
"v_fma_f32 v12,v12,v11,s13\n"
AW "a225,v12\n"
AR "v12,a6\n"
"v_subrev_f32 v12,s13,v12\n"
"v_fma_f32 v12,v12,v11,s13\n"
AW "a6,v12\n"
AR "v12,a26\n"
"v_subrev_f32 v12,s13,v12\n"
"v_fma_f32 v12,v12,v11,s13\n"
AW "a26,v12\n"
AR "v12,a46\n"
"v_subrev_f32 v12,s13,v12\n"
"v_fma_f32 v12,v12,v11,s13\n"
AW "a46,v12\n"
AR "v12,a66\n"
"v_subrev_f32 v12,s13,v12\n"
"v_fma_f32 v12,v12,v11,s13\n"
AW "a66,v12\n"
AR "v12,a86\n"
"v_subrev_f32 v12,s13,v12\n"
"v_fma_f32 v12,v12,v11,s13\n"
AW "a86,v12\n"
AR "v12,a106\n"
"v_subrev_f32 v12,s13,v12\n"
"v_fma_f32 v12,v12,v11,s13\n"
AW "a106,v12\n"
AR "v12,a126\n"
"v_subrev_f32 v12,s13,v12\n"
"v_fma_f32 v12,v12,v11,s13\n"
AW "a126,v12\n"
AR "v12,a146\n"
"v_subrev_f32 v12,s13,v12\n"
"v_fma_f32 v12,v12,v11,s13\n"
AW "a146,v12\n"
AR "v12,a166\n"
"v_subrev_f32 v12,s13,v12\n"
"v_fma_f32 v12,v12,v11,s13\n"
AW "a166,v12\n"
AR "v12,a186\n"
"v_subrev_f32 v12,s13,v12\n"
"v_fma_f32 v12,v12,v11,s13\n"
AW "a186,v12\n"
AR "v12,a206\n"
"v_subrev_f32 v12,s13,v12\n"
"v_fma_f32 v12,v12,v11,s13\n"
AW "a206,v12\n"
AR "v12,a226\n"
"v_subrev_f32 v12,s13,v12\n"
"v_fma_f32 v12,v12,v11,s13\n"
AW "a226,v12\n"
AR "v12,a7\n"
"v_subrev_f32 v12,s13,v12\n"
"v_fma_f32 v12,v12,v11,s13\n"
AW "a7,v12\n"
AR "v12,a27\n"
"v_subrev_f32 v12,s13,v12\n"
"v_fma_f32 v12,v12,v11,s13\n"
AW "a27,v12\n"
AR "v12,a47\n"
"v_subrev_f32 v12,s13,v12\n"
"v_fma_f32 v12,v12,v11,s13\n"
AW "a47,v12\n"
AR "v12,a67\n"
"v_subrev_f32 v12,s13,v12\n"
"v_fma_f32 v12,v12,v11,s13\n"
AW "a67,v12\n"
AR "v12,a87\n"
"v_subrev_f32 v12,s13,v12\n"
"v_fma_f32 v12,v12,v11,s13\n"
AW "a87,v12\n"
AR "v12,a107\n"
"v_subrev_f32 v12,s13,v12\n"
"v_fma_f32 v12,v12,v11,s13\n"
AW "a107,v12\n"
AR "v12,a127\n"
"v_subrev_f32 v12,s13,v12\n"
"v_fma_f32 v12,v12,v11,s13\n"
AW "a127,v12\n"
AR "v12,a147\n"
"v_subrev_f32 v12,s13,v12\n"
"v_fma_f32 v12,v12,v11,s13\n"
AW "a147,v12\n"
AR "v12,a167\n"
"v_subrev_f32 v12,s13,v12\n"
"v_fma_f32 v12,v12,v11,s13\n"
AW "a167,v12\n"
AR "v12,a187\n"
"v_subrev_f32 v12,s13,v12\n"
"v_fma_f32 v12,v12,v11,s13\n"
AW "a187,v12\n"
AR "v12,a207\n"
"v_subrev_f32 v12,s13,v12\n"
"v_fma_f32 v12,v12,v11,s13\n"
AW "a207,v12\n"
AR "v12,a227\n"
"v_subrev_f32 v12,s13,v12\n"
"v_fma_f32 v12,v12,v11,s13\n"
AW "a227,v12\n"
"v_mul_f32 v58,v53,v11\n"
"v_sub_f32 v36,v36,v10\n"
"v_sub_f32 v37,v37,v10\n"
"v_sub_f32 v38,v38,v10\n"
"v_sub_f32 v39,v39,v10\n"
"v_sub_f32 v76,v76,v10\n"
"v_sub_f32 v77,v77,v10\n"
"v_sub_f32 v78,v78,v10\n"
"v_sub_f32 v79,v79,v10\n"
"v_sub_f32 v96,v96,v10\n"
"v_sub_f32 v97,v97,v10\n"
"v_sub_f32 v98,v98,v10\n"
"v_sub_f32 v99,v99,v10\n"
"v_sub_f32 v116,v116,v10\n"
"v_sub_f32 v117,v117,v10\n"
"v_sub_f32 v118,v118,v10\n"
"v_sub_f32 v119,v119,v10\n"
"v_exp_f32 v96,v96\n"
"v_exp_f32 v97,v97\n"
"v_exp_f32 v98,v98\n"
"v_exp_f32 v99,v99\n"
"v_exp_f32 v116,v116\n"
"v_exp_f32 v117,v117\n"
"v_exp_f32 v118,v118\n"
"v_exp_f32 v119,v119\n"
"v_add_f32 v58,v58,v96\n"
"v_add_f32 v58,v58,v97\n"
"v_add_f32 v58,v58,v98\n"
"v_add_f32 v58,v58,v99\n"
"v_add_f32 v58,v58,v116\n"
"v_add_f32 v58,v58,v117\n"
"v_add_f32 v58,v58,v118\n"
"v_add_f32_e64 v58,v58,v119\n"
CV "v96,v96,v97\n"
CV "v97,v98,v99\n"
CV "v98,v116,v117\n"
CV "v99,v118,v119\n"
"v_max3_f32 v10,v100,v101,v102\n"
"v_max3_f32 v11,v103,v120,v121\n"
"v_max3_f32 v10,v10,v122,v123\n"
"v_max_f32 v10,v10,v11\n"
"v_mov_b32 v11,v10\n"
"s_nop 1\n"
"v_permlane16_swap_b32 v10,v11\n"
"v_max_f32 v10,v10,v11\n"
"v_mov_b32 v11,v10\n"
"s_nop 1\n"
"v_permlane32_swap_b32 v10,v11\n"
"v_max_f32_e64 v10,v10,v11\n"
"v_add_f32 v10,0x41300000,v10\n"
"v_max_f32 v10,0,v10\n"
"v_exp_f32 v11,-v10\n"
AR "v12,a8\n"
"v_subrev_f32 v12,s13,v12\n"
"v_fma_f32 v12,v12,v11,s13\n"
AW "a8,v12\n"
AR "v12,a28\n"
"v_subrev_f32 v12,s13,v12\n"
"v_fma_f32 v12,v12,v11,s13\n"
AW "a28,v12\n"
AR "v12,a48\n"
"v_subrev_f32 v12,s13,v12\n"
"v_fma_f32 v12,v12,v11,s13\n"
AW "a48,v12\n"
AR "v12,a68\n"
"v_subrev_f32 v12,s13,v12\n"
"v_fma_f32 v12,v12,v11,s13\n"
AW "a68,v12\n"
AR "v12,a88\n"
"v_subrev_f32 v12,s13,v12\n"
"v_fma_f32 v12,v12,v11,s13\n"
AW "a88,v12\n"
AR "v12,a108\n"
"v_subrev_f32 v12,s13,v12\n"
"v_fma_f32 v12,v12,v11,s13\n"
AW "a108,v12\n"
AR "v12,a128\n"
"v_subrev_f32 v12,s13,v12\n"
"v_fma_f32 v12,v12,v11,s13\n"
AW "a128,v12\n"
AR "v12,a148\n"
"v_subrev_f32 v12,s13,v12\n"
"v_fma_f32 v12,v12,v11,s13\n"
AW "a148,v12\n"
AR "v12,a168\n"
"v_subrev_f32 v12,s13,v12\n"
"v_fma_f32 v12,v12,v11,s13\n"
AW "a168,v12\n"
AR "v12,a188\n"
"v_subrev_f32 v12,s13,v12\n"
"v_fma_f32 v12,v12,v11,s13\n"
AW "a188,v12\n"
AR "v12,a208\n"
"v_subrev_f32 v12,s13,v12\n"
"v_fma_f32 v12,v12,v11,s13\n"
AW "a208,v12\n"
AR "v12,a228\n"
"v_subrev_f32 v12,s13,v12\n"
"v_fma_f32 v12,v12,v11,s13\n"
AW "a228,v12\n"
AR "v12,a9\n"
"v_subrev_f32 v12,s13,v12\n"
"v_fma_f32 v12,v12,v11,s13\n"
AW "a9,v12\n"
AR "v12,a29\n"
"v_subrev_f32 v12,s13,v12\n"
"v_fma_f32 v12,v12,v11,s13\n"
AW "a29,v12\n"
AR "v12,a49\n"
"v_subrev_f32 v12,s13,v12\n"
"v_fma_f32 v12,v12,v11,s13\n"
AW "a49,v12\n"
AR "v12,a69\n"
"v_subrev_f32 v12,s13,v12\n"
"v_fma_f32 v12,v12,v11,s13\n"
AW "a69,v12\n"
AR "v12,a89\n"
"v_subrev_f32 v12,s13,v12\n"
"v_fma_f32 v12,v12,v11,s13\n"
AW "a89,v12\n"
AR "v12,a109\n"
"v_subrev_f32 v12,s13,v12\n"
"v_fma_f32 v12,v12,v11,s13\n"
AW "a109,v12\n"
AR "v12,a129\n"
"v_subrev_f32 v12,s13,v12\n"
"v_fma_f32 v12,v12,v11,s13\n"
AW "a129,v12\n"
AR "v12,a149\n"
"v_subrev_f32 v12,s13,v12\n"
"v_fma_f32 v12,v12,v11,s13\n"
AW "a149,v12\n"
AR "v12,a169\n"
"v_subrev_f32 v12,s13,v12\n"
"v_fma_f32 v12,v12,v11,s13\n"
AW "a169,v12\n"
AR "v12,a189\n"
"v_subrev_f32 v12,s13,v12\n"
"v_fma_f32 v12,v12,v11,s13\n"
AW "a189,v12\n"
AR "v12,a209\n"
"v_subrev_f32 v12,s13,v12\n"
"v_fma_f32 v12,v12,v11,s13\n"
AW "a209,v12\n"
AR "v12,a229\n"
"v_subrev_f32 v12,s13,v12\n"
"v_fma_f32 v12,v12,v11,s13\n"
AW "a229,v12\n"
AR "v12,a10\n"
"v_subrev_f32 v12,s13,v12\n"
"v_fma_f32 v12,v12,v11,s13\n"
AW "a10,v12\n"
AR "v12,a30\n"
"v_subrev_f32 v12,s13,v12\n"
"v_fma_f32 v12,v12,v11,s13\n"
AW "a30,v12\n"
AR "v12,a50\n"
"v_subrev_f32 v12,s13,v12\n"
"v_fma_f32 v12,v12,v11,s13\n"
AW "a50,v12\n"
AR "v12,a70\n"
"v_subrev_f32 v12,s13,v12\n"
"v_fma_f32 v12,v12,v11,s13\n"
AW "a70,v12\n"
AR "v12,a90\n"
"v_subrev_f32 v12,s13,v12\n"
"v_fma_f32 v12,v12,v11,s13\n"
AW "a90,v12\n"
AR "v12,a110\n"
"v_subrev_f32 v12,s13,v12\n"
"v_fma_f32 v12,v12,v11,s13\n"
AW "a110,v12\n"
AR "v12,a130\n"
"v_subrev_f32 v12,s13,v12\n"
"v_fma_f32 v12,v12,v11,s13\n"
AW "a130,v12\n"
AR "v12,a150\n"
"v_subrev_f32 v12,s13,v12\n"
"v_fma_f32 v12,v12,v11,s13\n"
AW "a150,v12\n"
AR "v12,a170\n"
"v_subrev_f32 v12,s13,v12\n"
"v_fma_f32 v12,v12,v11,s13\n"
AW "a170,v12\n"
AR "v12,a190\n"
"v_subrev_f32 v12,s13,v12\n"
"v_fma_f32 v12,v12,v11,s13\n"
AW "a190,v12\n"
AR "v12,a210\n"
"v_subrev_f32 v12,s13,v12\n"
"v_fma_f32 v12,v12,v11,s13\n"
AW "a210,v12\n"
AR "v12,a230\n"
"v_subrev_f32 v12,s13,v12\n"
"v_fma_f32 v12,v12,v11,s13\n"
AW "a230,v12\n"
AR "v12,a11\n"
"v_subrev_f32 v12,s13,v12\n"
"v_fma_f32 v12,v12,v11,s13\n"
AW "a11,v12\n"
AR "v12,a31\n"
"v_subrev_f32 v12,s13,v12\n"
"v_fma_f32 v12,v12,v11,s13\n"
AW "a31,v12\n"
AR "v12,a51\n"
"v_subrev_f32 v12,s13,v12\n"
"v_fma_f32 v12,v12,v11,s13\n"
AW "a51,v12\n"
AR "v12,a71\n"
"v_subrev_f32 v12,s13,v12\n"
"v_fma_f32 v12,v12,v11,s13\n"
AW "a71,v12\n"
AR "v12,a91\n"
"v_subrev_f32 v12,s13,v12\n"
"v_fma_f32 v12,v12,v11,s13\n"
AW "a91,v12\n"
AR "v12,a111\n"
"v_subrev_f32 v12,s13,v12\n"
"v_fma_f32 v12,v12,v11,s13\n"
AW "a111,v12\n"
AR "v12,a131\n"
"v_subrev_f32 v12,s13,v12\n"
"v_fma_f32 v12,v12,v11,s13\n"
AW "a131,v12\n"
AR "v12,a151\n"
"v_subrev_f32 v12,s13,v12\n"
"v_fma_f32 v12,v12,v11,s13\n"
AW "a151,v12\n"
AR "v12,a171\n"
"v_subrev_f32 v12,s13,v12\n"
"v_fma_f32 v12,v12,v11,s13\n"
AW "a171,v12\n"
AR "v12,a191\n"
"v_subrev_f32 v12,s13,v12\n"
"v_fma_f32 v12,v12,v11,s13\n"
AW "a191,v12\n"
AR "v12,a211\n"
"v_subrev_f32 v12,s13,v12\n"
"v_fma_f32 v12,v12,v11,s13\n"
AW "a211,v12\n"
AR "v12,a231\n"
"v_subrev_f32 v12,s13,v12\n"
"v_fma_f32 v12,v12,v11,s13\n"
AW "a231,v12\n"
"v_mul_f32 v59,v54,v11\n"
"v_sub_f32 v40,v40,v10\n"
"v_sub_f32 v41,v41,v10\n"
"v_sub_f32 v42,v42,v10\n"
"v_sub_f32 v43,v43,v10\n"
"v_sub_f32 v80,v80,v10\n"
"v_sub_f32 v81,v81,v10\n"
"v_sub_f32 v82,v82,v10\n"
"v_sub_f32 v83,v83,v10\n"
"v_sub_f32 v100,v100,v10\n"
"v_sub_f32 v101,v101,v10\n"
"v_sub_f32 v102,v102,v10\n"
"v_sub_f32 v103,v103,v10\n"
"v_sub_f32 v120,v120,v10\n"
"v_sub_f32 v121,v121,v10\n"
"v_sub_f32 v122,v122,v10\n"
"v_sub_f32 v123,v123,v10\n"
"v_exp_f32 v100,v100\n"
"v_exp_f32 v101,v101\n"
"v_exp_f32 v102,v102\n"
"v_exp_f32 v103,v103\n"
"v_exp_f32 v120,v120\n"
"v_exp_f32 v121,v121\n"
"v_exp_f32 v122,v122\n"
"v_exp_f32 v123,v123\n"
"v_add_f32 v59,v59,v100\n"
"v_add_f32 v59,v59,v101\n"
"v_add_f32 v59,v59,v102\n"
"v_add_f32 v59,v59,v103\n"
"v_add_f32 v59,v59,v120\n"
"v_add_f32 v59,v59,v121\n"
"v_add_f32 v59,v59,v122\n"
"v_add_f32 v59,v59,v123\n"
CV "v100,v100,v101\n"
CV "v101,v102,v103\n"
CV "v102,v120,v121\n"
CV "v103,v122,v123\n"
"v_max3_f32 v10,v104,v105,v106\n"
"v_max3_f32 v11,v107,v124,v125\n"
"v_max3_f32 v10,v10,v126,v127\n"
"v_max_f32 v10,v10,v11\n"
"v_mov_b32 v11,v10\n"
"s_nop 1\n"
"v_permlane16_swap_b32 v10,v11\n"
"v_max_f32 v10,v10,v11\n"
"v_mov_b32 v11,v10\n"
"s_nop 1\n"
"v_permlane32_swap_b32 v10,v11\n"
"v_max_f32 v10,v10,v11\n"
"v_add_f32 v10,0x41300000,v10\n"
"v_max_f32 v10,0,v10\n"
"v_exp_f32 v11,-v10\n"
AR "v12,a12\n"
"v_subrev_f32 v12,s13,v12\n"
"v_fma_f32 v12,v12,v11,s13\n"
AW "a12,v12\n"
AR "v12,a32\n"
"v_subrev_f32 v12,s13,v12\n"
"v_fma_f32 v12,v12,v11,s13\n"
AW "a32,v12\n"
AR "v12,a52\n"
"v_subrev_f32 v12,s13,v12\n"
"v_fma_f32 v12,v12,v11,s13\n"
AW "a52,v12\n"
AR "v12,a72\n"
"v_subrev_f32 v12,s13,v12\n"
"v_fma_f32 v12,v12,v11,s13\n"
AW "a72,v12\n"
AR "v12,a92\n"
"v_subrev_f32 v12,s13,v12\n"
"v_fma_f32 v12,v12,v11,s13\n"
AW "a92,v12\n"
AR "v12,a112\n"
"v_subrev_f32 v12,s13,v12\n"
"v_fma_f32 v12,v12,v11,s13\n"
AW "a112,v12\n"
AR "v12,a132\n"
"v_subrev_f32 v12,s13,v12\n"
"v_fma_f32 v12,v12,v11,s13\n"
AW "a132,v12\n"
AR "v12,a152\n"
"v_subrev_f32 v12,s13,v12\n"
"v_fma_f32 v12,v12,v11,s13\n"
AW "a152,v12\n"
AR "v12,a172\n"
"v_subrev_f32 v12,s13,v12\n"
"v_fma_f32 v12,v12,v11,s13\n"
AW "a172,v12\n"
AR "v12,a192\n"
"v_subrev_f32 v12,s13,v12\n"
"v_fma_f32 v12,v12,v11,s13\n"
AW "a192,v12\n"
AR "v12,a212\n"
"v_subrev_f32 v12,s13,v12\n"
"v_fma_f32 v12,v12,v11,s13\n"
AW "a212,v12\n"
AR "v12,a232\n"
"v_subrev_f32 v12,s13,v12\n"
"v_fma_f32 v12,v12,v11,s13\n"
AW "a232,v12\n"
AR "v12,a13\n"
"v_subrev_f32 v12,s13,v12\n"
"v_fma_f32 v12,v12,v11,s13\n"
AW "a13,v12\n"
AR "v12,a33\n"
"v_subrev_f32 v12,s13,v12\n"
"v_fma_f32 v12,v12,v11,s13\n"
AW "a33,v12\n"
AR "v12,a53\n"
"v_subrev_f32 v12,s13,v12\n"
"v_fma_f32 v12,v12,v11,s13\n"
AW "a53,v12\n"
AR "v12,a73\n"
"v_subrev_f32 v12,s13,v12\n"
"v_fma_f32 v12,v12,v11,s13\n"
AW "a73,v12\n"
AR "v12,a93\n"
"v_subrev_f32 v12,s13,v12\n"
"v_fma_f32 v12,v12,v11,s13\n"
AW "a93,v12\n"
AR "v12,a113\n"
"v_subrev_f32 v12,s13,v12\n"
"v_fma_f32 v12,v12,v11,s13\n"
AW "a113,v12\n"
AR "v12,a133\n"
"v_subrev_f32 v12,s13,v12\n"
"v_fma_f32 v12,v12,v11,s13\n"
AW "a133,v12\n"
AR "v12,a153\n"
"v_subrev_f32 v12,s13,v12\n"
"v_fma_f32 v12,v12,v11,s13\n"
AW "a153,v12\n"
AR "v12,a173\n"
"v_subrev_f32 v12,s13,v12\n"
"v_fma_f32 v12,v12,v11,s13\n"
AW "a173,v12\n"
AR "v12,a193\n"
"v_subrev_f32 v12,s13,v12\n"
"v_fma_f32 v12,v12,v11,s13\n"
AW "a193,v12\n"
AR "v12,a213\n"
"v_subrev_f32 v12,s13,v12\n"
"v_fma_f32 v12,v12,v11,s13\n"
AW "a213,v12\n"
AR "v12,a233\n"
"v_subrev_f32 v12,s13,v12\n"
"v_fma_f32 v12,v12,v11,s13\n"
AW "a233,v12\n"
AR "v12,a14\n"
"v_subrev_f32 v12,s13,v12\n"
"v_fma_f32 v12,v12,v11,s13\n"
AW "a14,v12\n"
AR "v12,a34\n"
"v_subrev_f32 v12,s13,v12\n"
"v_fma_f32 v12,v12,v11,s13\n"
AW "a34,v12\n"
AR "v12,a54\n"
"v_subrev_f32 v12,s13,v12\n"
"v_fma_f32 v12,v12,v11,s13\n"
AW "a54,v12\n"
AR "v12,a74\n"
"v_subrev_f32 v12,s13,v12\n"
"v_fma_f32 v12,v12,v11,s13\n"
AW "a74,v12\n"
AR "v12,a94\n"
"v_subrev_f32 v12,s13,v12\n"
"v_fma_f32 v12,v12,v11,s13\n"
AW "a94,v12\n"
AR "v12,a114\n"
"v_subrev_f32 v12,s13,v12\n"
"v_fma_f32 v12,v12,v11,s13\n"
AW "a114,v12\n"
AR "v12,a134\n"
"v_subrev_f32 v12,s13,v12\n"
"v_fma_f32 v12,v12,v11,s13\n"
AW "a134,v12\n"
AR "v12,a154\n"
"v_subrev_f32 v12,s13,v12\n"
"v_fma_f32 v12,v12,v11,s13\n"
AW "a154,v12\n"
AR "v12,a174\n"
"v_subrev_f32 v12,s13,v12\n"
"v_fma_f32 v12,v12,v11,s13\n"
AW "a174,v12\n"
AR "v12,a194\n"
"v_subrev_f32 v12,s13,v12\n"
"v_fma_f32 v12,v12,v11,s13\n"
AW "a194,v12\n"
AR "v12,a214\n"
"v_subrev_f32 v12,s13,v12\n"
"v_fma_f32 v12,v12,v11,s13\n"
AW "a214,v12\n"
AR "v12,a234\n"
"v_subrev_f32 v12,s13,v12\n"
"v_fma_f32 v12,v12,v11,s13\n"
AW "a234,v12\n"
AR "v12,a15\n"
"v_subrev_f32 v12,s13,v12\n"
"v_fma_f32 v12,v12,v11,s13\n"
AW "a15,v12\n"
AR "v12,a35\n"
"v_subrev_f32 v12,s13,v12\n"
"v_fma_f32 v12,v12,v11,s13\n"
AW "a35,v12\n"
AR "v12,a55\n"
"v_subrev_f32 v12,s13,v12\n"
"v_fma_f32 v12,v12,v11,s13\n"
AW "a55,v12\n"
AR "v12,a75\n"
"v_subrev_f32 v12,s13,v12\n"
"v_fma_f32 v12,v12,v11,s13\n"
AW "a75,v12\n"
AR "v12,a95\n"
"v_subrev_f32 v12,s13,v12\n"
"v_fma_f32 v12,v12,v11,s13\n"
AW "a95,v12\n"
AR "v12,a115\n"
"v_subrev_f32 v12,s13,v12\n"
"v_fma_f32 v12,v12,v11,s13\n"
AW "a115,v12\n"
AR "v12,a135\n"
"v_subrev_f32 v12,s13,v12\n"
"v_fma_f32 v12,v12,v11,s13\n"
AW "a135,v12\n"
AR "v12,a155\n"
"v_subrev_f32 v12,s13,v12\n"
"v_fma_f32 v12,v12,v11,s13\n"
AW "a155,v12\n"
AR "v12,a175\n"
"v_subrev_f32 v12,s13,v12\n"
"v_fma_f32 v12,v12,v11,s13\n"
AW "a175,v12\n"
AR "v12,a195\n"
"v_subrev_f32 v12,s13,v12\n"
"v_fma_f32 v12,v12,v11,s13\n"
AW "a195,v12\n"
AR "v12,a215\n"
"v_subrev_f32 v12,s13,v12\n"
"v_fma_f32 v12,v12,v11,s13\n"
AW "a215,v12\n"
AR "v12,a235\n"
"v_subrev_f32 v12,s13,v12\n"
"v_fma_f32 v12,v12,v11,s13\n"
AW "a235,v12\n"
"v_mul_f32 v60,v55,v11\n"
"v_sub_f32 v44,v44,v10\n"
"v_sub_f32 v45,v45,v10\n"
"v_sub_f32 v46,v46,v10\n"
"v_sub_f32 v47,v47,v10\n"
"v_sub_f32 v84,v84,v10\n"
"v_sub_f32 v85,v85,v10\n"
"v_sub_f32 v86,v86,v10\n"
"v_sub_f32 v87,v87,v10\n"
"v_sub_f32 v104,v104,v10\n"
"v_sub_f32 v105,v105,v10\n"
"v_sub_f32 v106,v106,v10\n"
"v_sub_f32 v107,v107,v10\n"
"v_sub_f32 v124,v124,v10\n"
"v_sub_f32 v125,v125,v10\n"
"v_sub_f32 v126,v126,v10\n"
"v_sub_f32 v127,v127,v10\n"
"v_exp_f32 v104,v104\n"
"v_exp_f32 v105,v105\n"
"v_exp_f32 v106,v106\n"
"v_exp_f32 v107,v107\n"
"v_exp_f32 v124,v124\n"
"v_exp_f32 v125,v125\n"
"v_exp_f32 v126,v126\n"
"v_exp_f32 v127,v127\n"
"v_add_f32 v60,v60,v104\n"
"v_add_f32 v60,v60,v105\n"
"v_add_f32 v60,v60,v106\n"
"v_add_f32 v60,v60,v107\n"
"v_add_f32 v60,v60,v124\n"
"v_add_f32 v60,v60,v125\n"
"v_add_f32 v60,v60,v126\n"
"v_add_f32_e64 v60,v60,v127\n"
CV "v104,v104,v105\n"
CV "v105,v106,v107\n"
CV "v106,v124,v125\n"
CV "v107,v126,v127\n"
"v_max3_f32 v10,v108,v109,v110\n"
"v_max3_f32 v11,v111,v128,v129\n"
"v_max3_f32 v10,v10,v130,v131\n"
"v_max_f32 v10,v10,v11\n"
"v_mov_b32 v11,v10\n"
"s_nop 1\n"
"v_permlane16_swap_b32 v10,v11\n"
"v_max_f32 v10,v10,v11\n"
"v_mov_b32 v11,v10\n"
"s_nop 1\n"
"v_permlane32_swap_b32 v10,v11\n"
"v_max_f32 v10,v10,v11\n"
"v_add_f32 v10,0x41300000,v10\n"
"v_max_f32 v10,0,v10\n"
"v_exp_f32 v11,-v10\n"
AR "v12,a16\n"
"v_subrev_f32 v12,s13,v12\n"
"v_fma_f32 v12,v12,v11,s13\n"
AW "a16,v12\n"
AR "v12,a36\n"
"v_subrev_f32 v12,s13,v12\n"
"v_fma_f32 v12,v12,v11,s13\n"
AW "a36,v12\n"
AR "v12,a56\n"
"v_subrev_f32 v12,s13,v12\n"
"v_fma_f32 v12,v12,v11,s13\n"
AW "a56,v12\n"
AR "v12,a76\n"
"v_subrev_f32 v12,s13,v12\n"
"v_fma_f32 v12,v12,v11,s13\n"
AW "a76,v12\n"
AR "v12,a96\n"
"v_subrev_f32 v12,s13,v12\n"
"v_fma_f32 v12,v12,v11,s13\n"
AW "a96,v12\n"
AR "v12,a116\n"
"v_subrev_f32 v12,s13,v12\n"
"v_fma_f32 v12,v12,v11,s13\n"
AW "a116,v12\n"
AR "v12,a136\n"
"v_subrev_f32 v12,s13,v12\n"
"v_fma_f32 v12,v12,v11,s13\n"
AW "a136,v12\n"
AR "v12,a156\n"
"v_subrev_f32 v12,s13,v12\n"
"v_fma_f32 v12,v12,v11,s13\n"
AW "a156,v12\n"
AR "v12,a176\n"
"v_subrev_f32 v12,s13,v12\n"
"v_fma_f32 v12,v12,v11,s13\n"
AW "a176,v12\n"
AR "v12,a196\n"
"v_subrev_f32 v12,s13,v12\n"
"v_fma_f32 v12,v12,v11,s13\n"
AW "a196,v12\n"
AR "v12,a216\n"
"v_subrev_f32 v12,s13,v12\n"
"v_fma_f32 v12,v12,v11,s13\n"
AW "a216,v12\n"
AR "v12,a236\n"
"v_subrev_f32 v12,s13,v12\n"
"v_fma_f32 v12,v12,v11,s13\n"
AW "a236,v12\n"
AR "v12,a17\n"
"v_subrev_f32 v12,s13,v12\n"
"v_fma_f32 v12,v12,v11,s13\n"
AW "a17,v12\n"
AR "v12,a37\n"
"v_subrev_f32 v12,s13,v12\n"
"v_fma_f32 v12,v12,v11,s13\n"
AW "a37,v12\n"
AR "v12,a57\n"
"v_subrev_f32 v12,s13,v12\n"
"v_fma_f32 v12,v12,v11,s13\n"
AW "a57,v12\n"
AR "v12,a77\n"
"v_subrev_f32 v12,s13,v12\n"
"v_fma_f32 v12,v12,v11,s13\n"
AW "a77,v12\n"
AR "v12,a97\n"
"v_subrev_f32 v12,s13,v12\n"
"v_fma_f32 v12,v12,v11,s13\n"
AW "a97,v12\n"
AR "v12,a117\n"
"v_subrev_f32 v12,s13,v12\n"
"v_fma_f32 v12,v12,v11,s13\n"
AW "a117,v12\n"
AR "v12,a137\n"
"v_subrev_f32 v12,s13,v12\n"
"v_fma_f32 v12,v12,v11,s13\n"
AW "a137,v12\n"
AR "v12,a157\n"
"v_subrev_f32 v12,s13,v12\n"
"v_fma_f32 v12,v12,v11,s13\n"
AW "a157,v12\n"
AR "v12,a177\n"
"v_subrev_f32 v12,s13,v12\n"
"v_fma_f32 v12,v12,v11,s13\n"
AW "a177,v12\n"
AR "v12,a197\n"
"v_subrev_f32 v12,s13,v12\n"
"v_fma_f32 v12,v12,v11,s13\n"
AW "a197,v12\n"
AR "v12,a217\n"
"v_subrev_f32 v12,s13,v12\n"
"v_fma_f32 v12,v12,v11,s13\n"
AW "a217,v12\n"
AR "v12,a237\n"
"v_subrev_f32 v12,s13,v12\n"
"v_fma_f32 v12,v12,v11,s13\n"
AW "a237,v12\n"
AR "v12,a18\n"
"v_subrev_f32 v12,s13,v12\n"
"v_fma_f32 v12,v12,v11,s13\n"
AW "a18,v12\n"
AR "v12,a38\n"
"v_subrev_f32 v12,s13,v12\n"
"v_fma_f32 v12,v12,v11,s13\n"
AW "a38,v12\n"
AR "v12,a58\n"
"v_subrev_f32 v12,s13,v12\n"
"v_fma_f32 v12,v12,v11,s13\n"
AW "a58,v12\n"
AR "v12,a78\n"
"v_subrev_f32 v12,s13,v12\n"
"v_fma_f32 v12,v12,v11,s13\n"
AW "a78,v12\n"
AR "v12,a98\n"
"v_subrev_f32 v12,s13,v12\n"
"v_fma_f32 v12,v12,v11,s13\n"
AW "a98,v12\n"
AR "v12,a118\n"
"v_subrev_f32 v12,s13,v12\n"
"v_fma_f32 v12,v12,v11,s13\n"
AW "a118,v12\n"
AR "v12,a138\n"
"v_subrev_f32 v12,s13,v12\n"
"v_fma_f32 v12,v12,v11,s13\n"
AW "a138,v12\n"
AR "v12,a158\n"
"v_subrev_f32 v12,s13,v12\n"
"v_fma_f32 v12,v12,v11,s13\n"
AW "a158,v12\n"
AR "v12,a178\n"
"v_subrev_f32 v12,s13,v12\n"
"v_fma_f32 v12,v12,v11,s13\n"
AW "a178,v12\n"
AR "v12,a198\n"
"v_subrev_f32 v12,s13,v12\n"
"v_fma_f32 v12,v12,v11,s13\n"
AW "a198,v12\n"
AR "v12,a218\n"
"v_subrev_f32 v12,s13,v12\n"
"v_fma_f32 v12,v12,v11,s13\n"
AW "a218,v12\n"
AR "v12,a238\n"
"v_subrev_f32 v12,s13,v12\n"
"v_fma_f32 v12,v12,v11,s13\n"
AW "a238,v12\n"
AR "v12,a19\n"
"v_subrev_f32 v12,s13,v12\n"
"v_fma_f32 v12,v12,v11,s13\n"
AW "a19,v12\n"
AR "v12,a39\n"
"v_subrev_f32 v12,s13,v12\n"
"v_fma_f32 v12,v12,v11,s13\n"
AW "a39,v12\n"
AR "v12,a59\n"
"v_subrev_f32 v12,s13,v12\n"
"v_fma_f32 v12,v12,v11,s13\n"
AW "a59,v12\n"
AR "v12,a79\n"
"v_subrev_f32 v12,s13,v12\n"
"v_fma_f32 v12,v12,v11,s13\n"
AW "a79,v12\n"
AR "v12,a99\n"
"v_subrev_f32 v12,s13,v12\n"
"v_fma_f32 v12,v12,v11,s13\n"
AW "a99,v12\n"
AR "v12,a119\n"
"v_subrev_f32 v12,s13,v12\n"
"v_fma_f32 v12,v12,v11,s13\n"
AW "a119,v12\n"
AR "v12,a139\n"
"v_subrev_f32 v12,s13,v12\n"
"v_fma_f32 v12,v12,v11,s13\n"
AW "a139,v12\n"
AR "v12,a159\n"
"v_subrev_f32 v12,s13,v12\n"
"v_fma_f32 v12,v12,v11,s13\n"
AW "a159,v12\n"
AR "v12,a179\n"
"v_subrev_f32 v12,s13,v12\n"
"v_fma_f32 v12,v12,v11,s13\n"
AW "a179,v12\n"
AR "v12,a199\n"
"v_subrev_f32 v12,s13,v12\n"
"v_fma_f32 v12,v12,v11,s13\n"
AW "a199,v12\n"
AR "v12,a219\n"
"v_subrev_f32 v12,s13,v12\n"
"v_fma_f32 v12,v12,v11,s13\n"
AW "a219,v12\n"
AR "v12,a239\n"
"v_subrev_f32 v12,s13,v12\n"
"v_fma_f32 v12,v12,v11,s13\n"
AW "a239,v12\n"
"v_mul_f32 v61,v56,v11\n"
"v_sub_f32 v48,v48,v10\n"
"v_sub_f32 v49,v49,v10\n"
"v_sub_f32 v50,v50,v10\n"
"v_sub_f32 v51,v51,v10\n"
"v_sub_f32 v88,v88,v10\n"
"v_sub_f32 v89,v89,v10\n"
"v_sub_f32 v90,v90,v10\n"
"v_sub_f32 v91,v91,v10\n"
"v_sub_f32 v108,v108,v10\n"
"v_sub_f32 v109,v109,v10\n"
"v_sub_f32 v110,v110,v10\n"
"v_sub_f32 v111,v111,v10\n"
"v_sub_f32 v128,v128,v10\n"
"v_sub_f32 v129,v129,v10\n"
"v_sub_f32 v130,v130,v10\n"
"v_sub_f32 v131,v131,v10\n"
"v_exp_f32 v108,v108\n"
"v_exp_f32 v109,v109\n"
"v_exp_f32 v110,v110\n"
"v_exp_f32 v111,v111\n"
"v_exp_f32 v128,v128\n"
"v_exp_f32 v129,v129\n"
"v_exp_f32 v130,v130\n"
"v_exp_f32 v131,v131\n"
"v_add_f32 v61,v61,v108\n"
"v_add_f32 v61,v61,v109\n"
"v_add_f32 v61,v61,v110\n"
"v_add_f32 v61,v61,v111\n"
"v_add_f32 v61,v61,v128\n"
"v_add_f32 v61,v61,v129\n"
"v_add_f32 v61,v61,v130\n"
"v_add_f32_e64 v61,v61,v131\n"
CV "v108,v108,v109\n"
CV "v109,v110,v111\n"
CV "v110,v128,v129\n"
CV "v111,v130,v131\n"
"s_nop 7\n"
"s_setpc_b64 s[52:53]\n"
".Ldone:\n"
    : "+{v0}"(tid), "+{s23}"(bid), "+{s[20:21]}"(ka)
    :
    : "v1", "v2", "v3", "v4", "v5", "v6", "v7", "v8", "v9", "v10", "v11", "v12", "v13", "v14", "v15", "v16", "v17", "v18", "v19", "v20", "v21", "v22", "v23", "v24", "v25", "v26", "v27", "v28", "v29", "v30", "v31", "v32", "v33", "v34", "v35", "v36", "v37", "v38", "v39", "v40", "v41", "v42", "v43", "v44", "v45", "v46", "v47", "v48", "v49", "v50", "v51", "v52", "v53", "v54", "v55", "v56", "v57", "v58", "v59", "v60", "v61", "v62", "v63", "v64", "v65", "v66", "v67", "v68", "v69", "v70", "v71", "v72", "v73", "v74", "v75", "v76", "v77", "v78", "v79", "v80", "v81", "v82", "v83", "v84", "v85", "v86", "v87", "v88", "v89", "v90", "v91", "v92", "v93", "v94", "v95", "v96", "v97", "v98", "v99", "v100", "v101", "v102", "v103", "v104", "v105", "v106", "v107", "v108", "v109", "v110", "v111", "v112", "v113", "v114", "v115", "v116", "v117", "v118", "v119", "v120", "v121", "v122", "v123", "v124", "v125", "v126", "v127", "v128", "v129", "v130", "v131", "v132", "v133", "v134", "v135", "v136", "v137", "v138", "v139", "v140", "v141", "v142", "v143", "v144", "v145", "v146", "v147", "v148", "v149", "v150", "v151", "v152", "v153", "v154", "v155", "v156", "v157", "v158", "v159", "v160", "v161", "v162", "v163", "v164", "v165", "v166", "v167", "v168", "v169", "v170", "v171", "v172", "v173", "v174", "v175", "v176", "v177", "v178", "v179", "v180", "v181", "v182", "v183", "v184", "v185", "v186", "v187", "v188", "v189", "v190", "v191", "v192", "v193", "v194", "v195", "v196", "v197", "v198", "v199", "v200", "v201", "v202", "v203", "v204", "v205", "v206", "v207", "v208", "v209", "v210", "v211", "v212", "v213", "v214", "v215", "v216", "v217", "v218", "v219", "v220", "v221", "v222", "v223", "v224", "v225", "v226", "v227", "v228", "v229", "v230", "v231", "v232", "v233", "v234", "v235", "v236", "v237", "v238", "v239", "v240", "v241", "v242", "v243", "v244", "v245", "v246", "v247", "v248", "v249", "v250", "v251", "v252", "v253", "v254", "v255", "a0", "a1", "a2", "a3", "a4", "a5", "a6", "a7", "a8", "a9", "a10", "a11", "a12", "a13", "a14", "a15", "a16", "a17", "a18", "a19", "a20", "a21", "a22", "a23", "a24", "a25", "a26", "a27", "a28", "a29", "a30", "a31", "a32", "a33", "a34", "a35", "a36", "a37", "a38", "a39", "a40", "a41", "a42", "a43", "a44", "a45", "a46", "a47", "a48", "a49", "a50", "a51", "a52", "a53", "a54", "a55", "a56", "a57", "a58", "a59", "a60", "a61", "a62", "a63", "a64", "a65", "a66", "a67", "a68", "a69", "a70", "a71", "a72", "a73", "a74", "a75", "a76", "a77", "a78", "a79", "a80", "a81", "a82", "a83", "a84", "a85", "a86", "a87", "a88", "a89", "a90", "a91", "a92", "a93", "a94", "a95", "a96", "a97", "a98", "a99", "a100", "a101", "a102", "a103", "a104", "a105", "a106", "a107", "a108", "a109", "a110", "a111", "a112", "a113", "a114", "a115", "a116", "a117", "a118", "a119", "a120", "a121", "a122", "a123", "a124", "a125", "a126", "a127", "a128", "a129", "a130", "a131", "a132", "a133", "a134", "a135", "a136", "a137", "a138", "a139", "a140", "a141", "a142", "a143", "a144", "a145", "a146", "a147", "a148", "a149", "a150", "a151", "a152", "a153", "a154", "a155", "a156", "a157", "a158", "a159", "a160", "a161", "a162", "a163", "a164", "a165", "a166", "a167", "a168", "a169", "a170", "a171", "a172", "a173", "a174", "a175", "a176", "a177", "a178", "a179", "a180", "a181", "a182", "a183", "a184", "a185", "a186", "a187", "a188", "a189", "a190", "a191", "a192", "a193", "a194", "a195", "a196", "a197", "a198", "a199", "a200", "a201", "a202", "a203", "a204", "a205", "a206", "a207", "a208", "a209", "a210", "a211", "a212", "a213", "a214", "a215", "a216", "a217", "a218", "a219", "a220", "a221", "a222", "a223", "a224", "a225", "a226", "a227", "a228", "a229", "a230", "a231", "a232", "a233", "a234", "a235", "a236", "a237", "a238", "a239", "a240", "a241", "a242", "a243", "a244", "a245", "a246", "a247", "a248", "a249", "a250", "a251", "a252", "a253", "a254", "a255", "s0", "s1", "s2", "s3", "s4", "s5", "s6", "s7", "s8", "s9", "s10", "s11", "s12", "s13", "s14", "s15", "s16", "s17", "s18", "s19", "s22", "s24", "s25", "s26", "s27", "s28", "s29", "s30", "s31", "s32", "s33", "s34", "s35", "s36", "s37", "s38", "s39", "s40", "s41", "s42", "s43", "s44", "s45", "s46", "s47", "s48", "s49", "s50", "s51", "s52", "s53", "s54", "s55", "s56", "s57", "s58", "s59", "s60", "s61", "s62", "s63", "s64", "s65", "s66", "s67", "s68", "s69", "s70", "s71", "s72", "s73", "s74", "s75", "s76", "s77", "s78", "s79", "s80", "s81", "s82", "s83", "s84", "s85", "s86", "s87", "s88", "s89", "s90", "s91", "s92", "s93", "s94", "s95", "s96", "s97", "s98", "s99", "s100", "s101", "vcc", "scc", "memory");
}

extern "C" void kernel_launch(void* const* d_in, const int* in_sizes, int n_in, void* d_out, int out_size, void* d_ws, size_t ws_size, hipStream_t stream) {
  Ptrs P;
  for (int br = 0; br < 2; ++br) { P.x[br] = d_in[5 * br]; P.k[br] = d_in[5 * br + 1]; P.v[br] = d_in[5 * br + 2]; P.wq[br] = d_in[5 * br + 3]; P.wo[br] = d_in[5 * br + 4]; }
  (void)hipFuncSetAttribute((const void*)fused_kernel, hipFuncAttributeMaxDynamicSharedMemorySize, LDS1);
  fused_kernel<<<256, 256, LDS1, stream>>>(P, (char*)d_ws, (char*)d_out);
}
```

```cpp
#include <hip/hip_runtime.h>
#include <hip/hip_bf16.h>

#define MF "v_mfma_f32_16x16x32_bf16 "
#define TR "ds_read_b64_tr_b16 "
#define AR "v_accvgpr_read_b32 "
#define AW "v_accvgpr_write_b32 "
#define CV "v_cvt_pk_bf16_f32 "
#define LD "buffer_load_dwordx4 "
constexpr unsigned LDS1 = 160 * 1024;

struct Ptrs { const void* x[2]; const void* k[2]; const void* v[2]; const void* wq[2]; const void* wo[2]; };

__global__ __launch_bounds__(256, 1) __attribute__((aligned(4096))) void fused_kernel(Ptrs P, char* __restrict__ ws, char* __restrict__ out) {
  unsigned tid = threadIdx.x, bid = blockIdx.x;
  unsigned long long ka = (unsigned long long)__builtin_amdgcn_kernarg_segment_ptr();
  asm volatile(
".p2align 5\n"
".Lcode0:\n"
"s_and_b32 s58,s23,4\n"
"s_lshl_b32 s58,s58,1\n"
"s_load_dwordx2 s[28:29],s[20:21],s58 offset:48\n"
"s_load_dwordx2 s[24:25],s[20:21],s58\n"
"s_load_dwordx2 s[32:33],s[20:21],s58 offset:16\n"
"s_load_dwordx2 s[36:37],s[20:21],s58 offset:32\n"
"s_load_dwordx2 s[48:49],s[20:21],s58 offset:64\n"
"s_memtime s[100:101]\n"
"s_load_dwordx2 s[40:41],s[20:21],0x50\n"
"s_load_dwordx2 s[44:45],s[20:21],0x58\n"
"v_readfirstlane_b32 s22,v0\n"
"v_and_b32 v9,63,v0\n"
"v_bfe_u32 v10,v0,3,3\n"
"v_and_b32 v11,7,v0\n"
"s_lshr_b32 s22,s22,6\n"
"s_or_b32 s22,s22,s58\n"
"s_and_b32 s58,s23,1\n"
"s_lshl_b32 s58,s58,3\n"
"s_lshr_b32 s59,s23,5\n"
"s_add_u32 s59,s59,s58\n"
"s_mul_i32 s59,s59,4112\n"
"s_or_b32 s22,s22,s59\n"
"s_and_b32 s59,s23,2\n"
"s_lshl_b32 s59,s59,1\n"
"s_bfe_u32 s60,s23,0x20003\n"
"s_or_b32 s59,s59,s60\n"
"s_lshl_b32 s60,s59,20\n"
"s_or_b32 s22,s22,s60\n"
"s_or_b32 s59,s59,s58\n"
"s_mul_i32 s59,s59,65792\n"
"s_xor_b32 s59,s59,524288\n"
"s_or_b32 s22,s22,s59\n"
"v_lshrrev_b32 v6,2,v0\n"
"v_and_b32 v6,4,v6\n"
"v_xor_b32 v6,v6,v11\n"
"v_lshlrev_b32 v6,4,v6\n"
"s_mov_b32 s58,12288\n"
"v_mad_u32_u24 v6,v10,s58,v6\n"
"v_bfe_u32 v4,v0,4,3\n"
"v_xor_b32 v4,v4,v11\n"
"v_lshlrev_b32 v4,4,v4\n"
"s_mov_b32 s58,1280\n"
"v_mad_u32_u24 v4,v10,s58,v4\n"
"v_lshlrev_b32 v9,4,v9\n"
"s_mov_b32 s26,3276800\n"
"s_mov_b32 s27,0x00020000\n"
"s_mov_b32 s30,7864320\n"
"s_mov_b32 s31,0x00020000\n"
"s_mov_b32 s34,0\n"
"s_mov_b32 s35,0x00020000\n"
"s_mov_b32 s38,0\n"
"s_mov_b32 s39,0x00020000\n"
"s_mov_b32 s42,268435456\n"
"s_mov_b32 s43,0x00020000\n"
"s_mov_b32 s46,6553600\n"
"s_mov_b32 s47,0x00020000\n"
"s_mov_b32 s50,7864320\n"
"s_mov_b32 s51,0x00020000\n"
"s_mov_b32 s98,20000\n"
"s_bfe_u32 s56,s22,0x20000\n"
"s_bfe_u32 s72,s22,0x10003\n"
"s_lshr_b32 s73,s56,1\n"
"s_and_b32 s74,s56,1\n"
"s_mul_i32 s62,s56,4096\n"
"s_mul_i32 s63,s56,5120\n"
"s_add_u32 s63,s63,98304\n"
"s_bfe_u32 s58,s22,0x40004\n"
"s_bfe_u32 s59,s22,0x40008\n"
"s_mul_i32 s68,s58,204800\n"
"s_mul_i32 s60,s56,51200\n"
"s_add_u32 s68,s68,s60\n"
"s_lshl_b32 s59,s59,1\n"
"s_add_u32 s59,s59,s73\n"
"s_mul_i32 s66,s59,384\n"
"s_mul_i32 s60,s74,393216\n"
"s_add_u32 s66,s66,s60\n"
"s_lshr_b32 s58,s22,6\n"
"s_xor_b32 s58,s58,s22\n"
"s_bfe_u32 s58,s58,0x10004\n"
"s_mul_i32 s81,s58,640\n"
"s_mul_i32 s60,s81,6144\n"
"s_add_u32 s66,s66,s60\n"
"v_xor_b32 v19,64,v4\n"
"v_add_u32 v16,97280,v6\n"
"v_add_u32 v17,194560,v6\n"
"v_add_u32 v18,291840,v6\n"
"s_waitcnt lgkmcnt(0)\n"
"s_mov_b32 s80,s100\n"
"s_mov_b32 s87,2\n"
"s_add_u32 m0,s62,0\n"
"s_add_u32 s82,s66,0\n"
"s_sub_u32 s15,s82,7864320\n"
"s_min_u32 s82,s82,s15\n"
LD "v6,s[28:31],s82 offen lds\n"
LD "v16,s[28:31],s82 offen offset:1024 lds\n"
LD "v17,s[28:31],s82 offen offset:2048 lds\n"
LD "v18,s[28:31],s82 offen offset:3072 lds\n"
"s_add_u32 s14,s81,0\n"
"s_sub_u32 s15,s14,1280\n"
"s_min_u32 s14,s14,s15\n"
"s_add_u32 s14,s14,s68\n"
"s_add_u32 s84,s14,0\n"
"s_add_u32 m0,s63,0\n"
"s_nop 0\n"
LD "v4,s[24:27],s84 offen lds\n"
"s_add_u32 s84,s14,9216\n"
LD "v19,s[24:27],s84 offen offset:1024 lds\n"
"s_add_u32 s84,s14,18432\n"
LD "v4,s[24:27],s84 offen offset:2048 lds\n"
"s_add_u32 s84,s14,27648\n"
LD "v19,s[24:27],s84 offen offset:3072 lds\n"
"s_add_u32 s84,s14,40960\n"
"s_add_u32 m0,s63,4096\n"
"s_nop 0\n"
LD "v4,s[24:27],s84 offen lds\n"
"s_lshr_b32 s58,s23,3\n"
"s_mul_i32 s59,s58,7\n"
"s_and_b32 s60,s23,7\n"
"s_mul_i32 s60,s60,3\n"
"s_add_u32 s59,s59,s60\n"
"s_and_b32 s59,s59,15\n"
"s_lshl_b32 s59,s59,8\n"
"s_or_b32 s58,s58,s59\n"
"s_and_b32 s59,s23,3\n"
"s_lshl_b32 s59,s59,5\n"
"s_or_b32 s58,s58,s59\n"
"s_bfe_u32 s59,s23,0x10001\n"
"s_lshl_b32 s59,s59,12\n"
"s_or_b32 s58,s58,s59\n"
"s_lshl_b32 s23,s23,16\n"
"s_or_b32 s23,s23,s58\n"
"v_bfe_u32 v20,v0,4,2\n"
"v_mul_u32_u24 v21,384,v10\n"
"v_xor_b32 v22,v11,v20\n"
"s_add_u32 m0,s62,16384\n"
"s_add_u32 s82,s66,128\n"
"s_sub_u32 s15,s82,7864320\n"
"s_min_u32 s82,s82,s15\n"
LD "v6,s[28:31],s82 offen lds\n"
"v_lshl_add_u32 v7,v22,4,v21\n"
"s_lshl_b32 s58,s74,6\n"
"v_xor_b32 v7,s58,v7\n"
"v_lshlrev_b32 v22,1,v20\n"
"v_xor_b32 v22,v11,v22\n"
"v_lshl_add_u32 v8,v22,4,v21\n"
"v_bfe_u32 v21,v0,1,3\n"
"v_xor_b32 v21,v21,v20\n"
"v_lshlrev_b32 v21,4,v21\n"
"v_lshl_add_u32 v21,v11,7,v21\n"
"v_bfe_u32 v22,v0,3,1\n"
"s_mul_i32 s58,s73,12288\n"
"v_mul_u32_u24 v23,3072,v22\n"
"v_add3_u32 v1,v23,v21,s58\n"
"s_mul_i32 s59,s74,10240\n"
"s_add_u32 s59,s59,98304\n"
"v_lshl_add_u32 v2,v22,10,v21\n"
"v_add_u32_e64 v2,s59,v2\n"
"v_bfe_u32 v21,v0,2,3\n"
LD "v16,s[28:31],s82 offen offset:1024 lds\n"
"v_lshlrev_b32_e64 v21,7,v21\n"
"v_bfe_u32 v23,v0,3,2\n"
"v_lshl_add_u32 v21,v23,5,v21\n"
"v_and_b32_e64 v23,3,v0\n"
"v_lshl_add_u32 v21,v23,3,v21\n"
"v_bfe_u32 v23,v0,5,1\n"
"v_mul_u32_u24 v3,3072,v23\n"
"v_add3_u32 v3,v3,v21,s58\n"
"s_bfe_u32 s58,s22,0x40004\n"
"s_mul_i32 s58,s58,409600\n"
"s_mul_i32 s59,s72,1280\n"
"s_add_u32 s58,s58,s59\n"
"v_and_b32 v21,15,v0\n"
"v_mul_u32_u24 v21,2560,v21\n"
"v_lshl_add_u32 v21,v23,4,v21\n"
"v_bfe_u32 v23,v0,4,1\n"
"v_lshl_add_u32 v21,v23,5,v21\n"
"v_add_u32_e64 v12,s58,v21\n"
"v_bfe_u32 v21,v0,2,2\n"
LD "v17,s[28:31],s82 offen offset:2048 lds\n"
"v_lshlrev_b32 v21,7,v21\n"
"v_lshl_add_u32 v21,v20,10,v21\n"
"v_lshl_add_u32 v21,v22,6,v21\n"
"v_and_b32 v23,3,v0\n"
"v_lshl_add_u32 v21,v23,4,v21\n"
"s_lshl_b32 s58,s73,13\n"
"v_add_u32 v0,s58,v21\n"
"s_getpc_b64 s[52:53]\n"
".Lcts:\n"
"s_sub_u32 s52,s52,.Lcts-.Lcode0\n"
"s_subb_u32 s53,s53,0\n"
"s_bfe_u32 s58,s23,0x50000\n"
"s_lshl_b32 s58,s58,2\n"
"s_add_u32 s58,s58,s56\n"
"s_mul_i32 s58,s58,1280\n"
"v_min_u32 v15,144,v9\n"
"v_lshl_add_u32 v15,v15,3,s58\n"
"v_min_u32 v15,.Lcode1-.Lcode0,v15\n"
LD "v18,s[28:31],s82 offen offset:3072 lds\n"
"s_bfe_u32 s58,s22,0x4000c\n"
"s_bfe_u32 s59,s22,0x40010\n"
"s_mul_i32 s69,s58,204800\n"
"s_mul_i32 s60,s56,51200\n"
"s_add_u32 s69,s69,s60\n"
"s_lshl_b32 s59,s59,1\n"
"s_add_u32 s59,s59,s73\n"
"s_mul_i32 s67,s59,384\n"
"s_mul_i32 s60,s74,393216\n"
"s_add_u32 s67,s67,s60\n"
"s_mul_i32 s60,s81,6144\n"
"s_add_u32 s67,s67,s60\n"
"s_bfe_u32 s58,s22,0x40004\n"
"s_bfe_u32 s59,s22,0x40008\n"
"s_lshl_b32 s59,s59,1\n"
"s_add_u32 s88,s59,s74\n"
"s_mul_i32 s88,s88,245760\n"
"s_add_u32 s59,s59,s73\n"
"s_lshl_b32 s70,s58,5\n"
"s_add_u32 m0,s62,32768\n"
"s_add_u32 s82,s66,256\n"
"s_sub_u32 s15,s82,7864320\n"
"s_min_u32 s82,s82,s15\n"
LD "v6,s[28:31],s82 offen lds\n"
"s_add_u32 s70,s70,s59\n"
"s_mul_i32 s70,s70,196608\n"
"s_bfe_u32 s58,s22,0x4000c\n"
"s_bfe_u32 s59,s22,0x40010\n"
"s_lshl_b32 s59,s59,1\n"
"s_add_u32 s89,s59,s74\n"
"s_mul_i32 s89,s89,245760\n"
"s_add_u32 s59,s59,s73\n"
"s_lshl_b32 s71,s58,5\n"
"s_add_u32 s71,s71,s59\n"
"s_mul_i32 s71,s71,196608\n"
"s_bitcmp1_b32 s23,12\n"
"s_cselect_b32 s58,s71,s70\n"
"s_cselect_b32 s71,s70,s71\n"
"s_mov_b32 s70,s58\n"
"s_cselect_b32 s58,s89,s88\n"
"s_cselect_b32 s89,s88,s89\n"
"s_mov_b32 s88,s58\n"
"s_cselect_b32 s69,98,1\n"
LD "v16,s[28:31],s82 offen offset:1024 lds\n"
"s_mov_b32 s54,1037384597\n"
"s_mov_b32 s55,1037384597\n"
"s_bfe_u32 s58,s22,0x40004\n"
"s_lshl_b32 s59,s72,4\n"
"s_add_u32 s59,s59,s58\n"
"s_lshl_b32 s86,s59,12\n"
"s_add_u32 s86,s86,100663296\n"
"s_mul_i32 s93,s59,1638400\n"
"s_add_u32 s93,s93,33554432\n"
"s_bfe_u32 s58,s22,0x30014\n"
"s_lshl_b32 s78,s58,1\n"
"s_add_u32 s78,s78,s74\n"
"s_lshl_b32 s94,s78,2\n"
"s_add_u32 s94,s94,s86\n"
"s_lshl_b32 s92,s58,2\n"
"s_add_u32 s92,s92,s56\n"
"s_mul_i32 s91,s58,204800\n"
"s_add_u32 s91,s91,s93\n"
"s_mul_i32 s75,s56,30720\n"
LD "v17,s[28:31],s82 offen offset:2048 lds\n"
"s_add_u32 s75,s75,s91\n"
"s_add_u32 s75,s75,0\n"
"s_mul_i32 s58,s74,5120\n"
"s_add_u32 s91,s91,s58\n"
"s_mov_b32 s90,0xf0000000\n"
"s_mov_b32 s77,0xf0000000\n"
"s_mov_b32 s85,0xf0000000\n"
AW "a240,v0\n"
AW "a244,v1\n"
AW "a241,v2\n"
AW "a245,v3\n"
AW "a242,v4\n"
AW "a243,v6\n"
AW "a252,v12\n"
"s_mov_b32 s57,0\n"
"s_mov_b32 s83,0\n"
LD "v18,s[28:31],s82 offen offset:3072 lds\n"
"global_load_dword a249,v15,s[52:53]\n"
AR "v0,a240\n"
AR "v2,a241\n"
AR "v4,a242\n"
AR "v6,a243\n"
"v_xor_b32 v1,64,v0\n"
"v_xor_b32 v3,64,v2\n"
"v_xor_b32_e64 v5,64,v4\n"
"v_add_u32 v11,97280,v6\n"
"v_add_u32 v12,194560,v6\n"
"v_add_u32 v13,291840,v6\n"
"s_mov_b64 vcc,0\n"
"s_waitcnt vmcnt(9) lgkmcnt(0)\n"
"s_barrier\n"
"s_nop 0\n"
TR "a[200:201],v0\n"
TR "a[202:203],v0 offset:512\n"
TR "a[204:205],v0 offset:4096\n"
TR "a[206:207],v0 offset:4608\n"
"ds_read_b128 a[60:63],v2\n"
"ds_read_b128 a[100:103],v3\n"
"ds_read_b128 a[64:67],v2 offset:2048\n"
"ds_read_b128 a[104:107],v3 offset:2048\n"
"ds_read_b128 a[68:71],v2 offset:4096\n"
"ds_read_b128 a[108:111],v3 offset:4096\n"
"ds_read_b128 a[72:75],v2 offset:6144\n"
"ds_read_b128 a[112:115],v3 offset:6144\n"
"ds_read_b128 a[76:79],v2 offset:8192\n"
"ds_read_b128 a[116:119],v3 offset:8192\n"
TR "a[208:209],v0 offset:8\n"
TR "a[210:211],v0 offset:520\n"
TR "a[212:213],v0 offset:4104\n"
TR "a[214:215],v0 offset:4616\n"
"s_waitcnt lgkmcnt(0)\n"
MF "v[16:19],a[200:203],a[60:63],0\n"
TR "a[216:217],v1\n"
MF "v[16:19],a[204:207],a[100:103],v[16:19]\n"
"s_add_u32 s14,s81,128\n"
TR "a[218:219],v1 offset:512\n"
MF "v[24:27],a[204:207],a[104:107],0\n"
"s_sub_u32 s15,s14,1280\n"
"v_lshrrev_b32 v15,5,v9\n"
"v_min_u32 v15,11,v15\n"
MF "v[24:27],a[200:203],a[64:67],v[24:27]\n"
"s_min_u32 s14,s14,s15\n"
TR "a[220:221],v1 offset:4096\n"
"v_mul_u32_u24 v15,160,v15\n"
MF "v[32:35],a[200:203],a[68:71],0\n"
"s_add_u32 s14,s14,s68\n"
TR "a[222:223],v1 offset:4608\n"
"v_and_or_b32 v15,v9,16,v15\n"
MF "v[32:35],a[204:207],a[108:111],v[32:35]\n"
"s_add_u32 s84,s14,0\n"
"v_max3_f32 v220,|v16|,|v17|,|v18|\n"
"v_max_f32_e64 v220,v220,|v19|\n"
MF "v[40:43],a[204:207],a[112:115],0\n"
"s_add_u32 m0,s63,20480\n"
"v_readlane_b32 s12,v220,3\n"
"v_readlane_b32 s6,v220,22\n"
MF "v[40:43],a[200:203],a[72:75],v[40:43]\n"
"s_min_u32 s12,s12,s6\n"
"v_readlane_b32 s7,v220,41\n"
"v_readlane_b32 s8,v220,60\n"
MF "v[48:51],a[200:203],a[76:79],0\n"
"s_min_u32 s7,s7,s8\n"
"v_lshlrev_b32 v15,3,v15\n"
MF "v[48:51],a[204:207],a[116:119],v[48:51]\n"
LD "v4,s[24:27],s84 offen lds\n"
MF "v[52:55],a[212:215],a[116:119],0\n"
"s_min_u32 s12,s12,s7\n"
TR "a[200:201],v1 offset:8\n"
MF "v[52:55],a[208:211],a[76:79],v[52:55]\n"
"s_and_b32 s12,s12,0x7f800000\n"
TR "a[202:203],v1 offset:520\n"
MF "v[44:47],a[208:211],a[72:75],0\n"
"s_sub_u32 s12,s12,0x14000000\n"
MF "v[44:47],a[212:215],a[112:115],v[44:47]\n"
"s_cmp_lt_u32 s12,0x50000000\n"
"s_cselect_b32 s12,s12,0xe8400000\n"
TR "a[204:205],v1 offset:4104\n"
MF "v[36:39],a[212:215],a[108:111],0\n"
"s_add_u32 s12,s12,0x17c00000\n"
TR "a[206:207],v1 offset:4616\n"
MF "v[36:39],a[208:211],a[68:71],v[36:39]\n"
"s_bfe_u32 s60,s23,0x70000\n"
"v_mov_b32 v220,s12\n"
"v_mov_b32 v221,s12\n"
MF "v[28:31],a[208:211],a[64:67],0\n"
"s_add_u32 s84,s14,9216\n"
"v_mov_b32 v222,s12\n"
"v_mov_b32 v223,s12\n"
MF "v[28:31],a[212:215],a[104:107],v[28:31]\n"
LD "v5,s[24:27],s84 offen offset:1024 lds\n"
MF "v[20:23],a[212:215],a[100:103],0\n"
"s_waitcnt vmcnt(7) lgkmcnt(0)\n"
MF "v[20:23],a[208:211],a[60:63],v[20:23]\n"
"s_barrier\n"
MF "v[56:59],a[216:219],a[60:63],v[220:223]\n"
"s_lshl_b32 s60,s60,2\n"
TR "a[208:209],v0 offset:16384\n"
MF "v[56:59],a[220:223],a[100:103],v[56:59]\n"
"s_add_u32 s84,s14,18432\n"
TR "a[210:211],v0 offset:16896\n"
MF "v[64:67],a[220:223],a[104:107],v[220:223]\n"
LD "v4,s[24:27],s84 offen offset:2048 lds\n"
MF "v[64:67],a[216:219],a[64:67],v[64:67]\n"
"s_add_u32 s60,s60,s56\n"
TR "a[212:213],v0 offset:20480\n"
MF "v[72:75],a[216:219],a[68:71],v[220:223]\n"
"s_mul_i32 s60,s60,15360\n"
TR "a[214:215],v0 offset:20992\n"
MF "v[72:75],a[220:223],a[108:111],v[72:75]\n"
MF "v[80:83],a[220:223],a[112:115],v[220:223]\n"
"s_add_u32 s84,s14,27648\n"
MF "v[80:83],a[216:219],a[72:75],v[80:83]\n"
LD "v5,s[24:27],s84 offen offset:3072 lds\n"
MF "v[88:91],a[216:219],a[76:79],v[220:223]\n"
MF "v[88:91],a[220:223],a[116:119],v[88:91]\n"
MF "v[92:95],a[204:207],a[116:119],v[220:223]\n"
"s_add_u32 s84,s14,40960\n"
TR "a[216:217],v0 offset:16392\n"
MF "v[92:95],a[200:203],a[76:79],v[92:95]\n"
"s_add_u32 m0,s63,24576\n"
TR "a[218:219],v0 offset:16904\n"
MF "v[84:87],a[200:203],a[72:75],v[220:223]\n"
LD "v4,s[24:27],s84 offen lds\n"
MF "v[84:87],a[204:207],a[112:115],v[84:87]\n"
"s_add_u32 m0,s62,49152\n"
TR "a[220:221],v0 offset:20488\n"
MF "v[76:79],a[204:207],a[108:111],v[220:223]\n"
"s_add_u32 s82,s66,786432\n"
TR "a[222:223],v0 offset:21000\n"
MF "v[76:79],a[200:203],a[68:71],v[76:79]\n"
"s_sub_u32 s15,s82,7864320\n"
MF "v[68:71],a[200:203],a[64:67],v[220:223]\n"
"s_min_u32 s82,s82,s15\n"
MF "v[68:71],a[204:207],a[104:107],v[68:71]\n"
LD "v6,s[28:31],s82 offen lds\n"
MF "v[60:63],a[204:207],a[100:103],v[220:223]\n"
MF "v[60:63],a[200:203],a[60:63],v[60:63]\n"
"s_waitcnt lgkmcnt(4)\n"
MF "v[96:99],a[208:211],a[60:63],v[220:223]\n"
TR "a[200:201],v1 offset:16384\n"
MF "v[96:99],a[212:215],a[100:103],v[96:99]\n"
TR "a[202:203],v1 offset:16896\n"
MF "v[104:107],a[212:215],a[104:107],v[220:223]\n"
LD "v11,s[28:31],s82 offen offset:1024 lds\n"
MF "v[104:107],a[208:211],a[64:67],v[104:107]\n"
TR "a[204:205],v1 offset:20480\n"
MF "v[112:115],a[208:211],a[68:71],v[220:223]\n"
TR "a[206:207],v1 offset:20992\n"
MF "v[112:115],a[212:215],a[108:111],v[112:115]\n"
MF "v[120:123],a[212:215],a[112:115],v[220:223]\n"
MF "v[120:123],a[208:211],a[72:75],v[120:123]\n"
LD "v12,s[28:31],s82 offen offset:2048 lds\n"
MF "v[128:131],a[208:211],a[76:79],v[220:223]\n"
MF "v[128:131],a[212:215],a[116:119],v[128:131]\n"
"s_waitcnt lgkmcnt(4)\n"
MF "v[132:135],a[220:223],a[116:119],v[220:223]\n"
TR "a[208:209],v1 offset:16392\n"
MF "v[132:135],a[216:219],a[76:79],v[132:135]\n"
TR "a[210:211],v1 offset:16904\n"
MF "v[124:127],a[216:219],a[72:75],v[220:223]\n"
LD "v13,s[28:31],s82 offen offset:3072 lds\n"
MF "v[124:127],a[220:223],a[112:115],v[124:127]\n"
"s_add_u32 m0,s62,65536\n"
TR "a[212:213],v1 offset:20488\n"
MF "v[116:119],a[220:223],a[108:111],v[220:223]\n"
"s_add_u32 s82,s66,786560\n"
TR "a[214:215],v1 offset:21000\n"
MF "v[116:119],a[216:219],a[68:71],v[116:119]\n"
"s_sub_u32 s15,s82,7864320\n"
MF "v[108:111],a[216:219],a[64:67],v[220:223]\n"
"s_min_u32 s82,s82,s15\n"
MF "v[108:111],a[220:223],a[104:107],v[108:111]\n"
LD "v6,s[28:31],s82 offen lds\n"
MF "v[100:103],a[220:223],a[100:103],v[220:223]\n"
"s_waitcnt vmcnt(5) lgkmcnt(0)\n"
MF "v[100:103],a[216:219],a[60:63],v[100:103]\n"
"s_barrier\n"
MF "v[136:139],a[200:203],a[60:63],v[220:223]\n"
TR "a[216:217],v0 offset:32768\n"
MF "v[136:139],a[204:207],a[100:103],v[136:139]\n"
TR "a[218:219],v0 offset:33280\n"
MF "v[144:147],a[204:207],a[104:107],v[220:223]\n"
"ds_read_b128 a[140:143],v2 offset:20480\n"
MF "v[144:147],a[200:203],a[64:67],v[144:147]\n"
TR "a[220:221],v0 offset:36864\n"
MF "v[152:155],a[200:203],a[68:71],v[220:223]\n"
TR "a[222:223],v0 offset:37376\n"
MF "v[152:155],a[204:207],a[108:111],v[152:155]\n"
LD "v11,s[28:31],s82 offen offset:1024 lds\n"
MF "v[160:163],a[204:207],a[112:115],v[220:223]\n"
"ds_read_b128 a[180:183],v3 offset:20480\n"
MF "v[160:163],a[200:203],a[72:75],v[160:163]\n"
MF "v[168:171],a[200:203],a[76:79],v[220:223]\n"
MF "v[168:171],a[204:207],a[116:119],v[168:171]\n"
LD "v12,s[28:31],s82 offen offset:2048 lds\n"
MF "v[172:175],a[212:215],a[116:119],v[220:223]\n"
TR "a[200:201],v0 offset:32776\n"
MF "v[172:175],a[208:211],a[76:79],v[172:175]\n"
TR "a[202:203],v0 offset:33288\n"
MF "v[164:167],a[208:211],a[72:75],v[220:223]\n"
"ds_read_b128 a[144:147],v2 offset:22528\n"
MF "v[164:167],a[212:215],a[112:115],v[164:167]\n"
TR "a[204:205],v0 offset:36872\n"
MF "v[156:159],a[212:215],a[108:111],v[220:223]\n"
TR "a[206:207],v0 offset:37384\n"
MF "v[156:159],a[208:211],a[68:71],v[156:159]\n"
LD "v13,s[28:31],s82 offen offset:3072 lds\n"
MF "v[148:151],a[208:211],a[64:67],v[220:223]\n"
"ds_read_b128 a[184:187],v3 offset:22528\n"
MF "v[148:151],a[212:215],a[104:107],v[148:151]\n"
"s_add_u32 m0,s62,81920\n"
MF "v[140:143],a[212:215],a[100:103],v[220:223]\n"
"s_add_u32 s82,s66,786688\n"
"s_sub_u32 s15,s82,7864320\n"
"s_min_u32 s82,s82,s15\n"
MF "v[140:143],a[208:211],a[60:63],v[140:143]\n"
"s_waitcnt lgkmcnt(7)\n"
LD "v6,s[28:31],s82 offen lds\n"
MF "v[176:179],a[216:219],a[60:63],v[220:223]\n"
TR "a[208:209],v1 offset:32768\n"
MF "v[176:179],a[220:223],a[100:103],v[176:179]\n"
TR "a[210:211],v1 offset:33280\n"
MF "v[184:187],a[220:223],a[104:107],v[220:223]\n"
"ds_read_b128 a[148:151],v2 offset:24576\n"
MF "v[184:187],a[216:219],a[64:67],v[184:187]\n"
TR "a[212:213],v1 offset:36864\n"
MF "v[192:195],a[216:219],a[68:71],v[220:223]\n"
TR "a[214:215],v1 offset:37376\n"
MF "v[192:195],a[220:223],a[108:111],v[192:195]\n"
LD "v11,s[28:31],s82 offen offset:1024 lds\n"
MF "v[200:203],a[220:223],a[112:115],v[220:223]\n"
"ds_read_b128 a[188:191],v3 offset:24576\n"
MF "v[200:203],a[216:219],a[72:75],v[200:203]\n"
MF "v[208:211],a[216:219],a[76:79],v[220:223]\n"
MF "v[208:211],a[220:223],a[116:119],v[208:211]\n"
"s_waitcnt lgkmcnt(7)\n"
LD "v12,s[28:31],s82 offen offset:2048 lds\n"
MF "v[212:215],a[204:207],a[116:119],v[220:223]\n"
TR "a[216:217],v1 offset:32776\n"
MF "v[212:215],a[200:203],a[76:79],v[212:215]\n"
TR "a[218:219],v1 offset:33288\n"
MF "v[204:207],a[200:203],a[72:75],v[220:223]\n"
"ds_read_b128 a[152:155],v2 offset:26624\n"
MF "v[204:207],a[204:207],a[112:115],v[204:207]\n"
TR "a[220:221],v1 offset:36872\n"
MF "v[196:199],a[204:207],a[108:111],v[220:223]\n"
TR "a[222:223],v1 offset:37384\n"
MF "v[196:199],a[200:203],a[68:71],v[196:199]\n"
LD "v13,s[28:31],s82 offen offset:3072 lds\n"
MF "v[188:191],a[200:203],a[64:67],v[220:223]\n"
"v_add_u32 v0,0xc000,v0\n"
MF "v[188:191],a[204:207],a[104:107],v[188:191]\n"
"s_add_u32 s14,s81,256\n"
"v_add_u32 v1,0xc000,v1\n"
MF "v[180:183],a[204:207],a[100:103],v[220:223]\n"
"s_waitcnt vmcnt(8) lgkmcnt(0)\n"
"s_mov_b64 exec,0\n"
"s_bitcmp1_b32 s23,12\n"
"s_cbranch_scc1 .Lisy1\n"
"s_cmp_eq_u32 s56,0\n"
"s_cbranch_scc1 .Lpg8b\n"
"s_cmp_eq_u32 s56,1\n"
"s_cbranch_scc1 .Lpg9b\n"
"s_cmp_eq_u32 s56,2\n"
"s_cbranch_scc1 .Lpgt0ex_b\n"
"s_branch .Lpg10b\n"
".Lisy1:\n"
"s_cmp_eq_u32 s56,0\n"
"s_cbranch_scc1 .Lpgq0_c\n"
"s_cmp_eq_u32 s56,1\n"
"s_cbranch_scc1 .Lpg4c\n"
"s_cmp_eq_u32 s56,2\n"
"s_cbranch_scc1 .Lpg5c\n"
"s_branch .Lpg6c\n"
".Lisr1:\n"
"s_mov_b64 exec,-1\n"
".Lisx1:\n"
MF "v[180:183],a[200:203],a[60:63],v[180:183]\n"
"s_barrier\n"
MF "v[216:219],a[208:211],a[60:63],v[220:223]\n"
"s_sub_u32 s15,s14,1280\n"
TR "a[200:201],v0\n"
MF "v[216:219],a[212:215],a[100:103],v[216:219]\n"
"s_min_u32 s14,s14,s15\n"
TR "a[202:203],v0 offset:512\n"
MF "v[224:227],a[212:215],a[104:107],v[220:223]\n"
"s_add_u32 s14,s14,s68\n"
"ds_read_b128 a[192:195],v3 offset:26624\n"
MF "v[224:227],a[208:211],a[64:67],v[224:227]\n"
"s_add_u32 s84,s14,0\n"
TR "a[204:205],v0 offset:4096\n"
MF "v[232:235],a[208:211],a[68:71],v[220:223]\n"
"s_add_u32 m0,s63,40960\n"
TR "a[206:207],v0 offset:4608\n"
MF "v[232:235],a[212:215],a[108:111],v[232:235]\n"
LD "v4,s[24:27],s84 offen lds\n"
MF "v[240:243],a[212:215],a[112:115],v[220:223]\n"
"ds_read_b128 a[156:159],v2 offset:28672\n"
MF "v[240:243],a[208:211],a[72:75],v[240:243]\n"
"ds_read_b128 a[196:199],v3 offset:28672\n"
MF "v[248:251],a[208:211],a[76:79],v[220:223]\n"
"s_add_u32 s84,s14,9216\n"
MF "v[248:251],a[212:215],a[116:119],v[248:251]\n"
LD "v5,s[24:27],s84 offen offset:1024 lds\n"
MF "v[252:255],a[220:223],a[116:119],v[220:223]\n"
TR "a[208:209],v0 offset:8\n"
MF "v[252:255],a[216:219],a[76:79],v[252:255]\n"
TR "a[210:211],v0 offset:520\n"
MF "v[244:247],a[216:219],a[72:75],v[220:223]\n"
MF "v[244:247],a[220:223],a[112:115],v[244:247]\n"
TR "a[212:213],v0 offset:4104\n"
MF "v[236:239],a[220:223],a[108:111],v[220:223]\n"
"s_add_u32 s84,s14,18432\n"
TR "a[214:215],v0 offset:4616\n"
MF "v[236:239],a[216:219],a[68:71],v[236:239]\n"
LD "v4,s[24:27],s84 offen offset:2048 lds\n"
MF "v[228:231],a[216:219],a[64:67],v[220:223]\n"
MF "v[228:231],a[220:223],a[104:107],v[228:231]\n"
MF "v[220:223],a[220:223],a[100:103],v[220:223]\n"
".Lpg1a:\n"
"s_cbranch_execz .Lpg7a\n"
"s_add_u32 s84,s14,27648\n"
MF "v[220:223],a[216:219],a[60:63],v[220:223]\n"
"s_waitcnt lgkmcnt(4)\n"
LD "v5,s[24:27],s84 offen offset:3072 lds\n"
MF "v[16:19],a[200:203],a[140:143],v[16:19]\n"
TR "a[216:217],v1\n"
MF "v[16:19],a[204:207],a[180:183],v[16:19]\n"
TR "a[218:219],v1 offset:512\n"
MF "v[24:27],a[204:207],a[184:187],v[24:27]\n"
MF "v[24:27],a[200:203],a[144:147],v[24:27]\n"
"s_add_u32 s84,s14,40960\n"
TR "a[220:221],v1 offset:4096\n"
MF "v[32:35],a[200:203],a[148:151],v[32:35]\n"
"s_add_u32 m0,s63,45056\n"
TR "a[222:223],v1 offset:4608\n"
MF "v[32:35],a[204:207],a[188:191],v[32:35]\n"
LD "v4,s[24:27],s84 offen lds\n"
MF "v[40:43],a[204:207],a[192:195],v[40:43]\n"
MF "v[40:43],a[200:203],a[152:155],v[40:43]\n"
"s_add_u32 m0,s62,0\n"
MF "v[48:51],a[200:203],a[156:159],v[48:51]\n"
"s_add_u32 s82,s66,1572864\n"
"s_sub_u32 s15,s82,7864320\n"
"s_min_u32 s82,s82,s15\n"
MF "v[48:51],a[204:207],a[196:199],v[48:51]\n"
"s_waitcnt lgkmcnt(4)\n"
LD "v6,s[28:31],s82 offen lds\n"
MF "v[52:55],a[212:215],a[196:199],v[52:55]\n"
TR "a[200:201],v1 offset:8\n"
MF "v[52:55],a[208:211],a[156:159],v[52:55]\n"
TR "a[202:203],v1 offset:520\n"
MF "v[44:47],a[208:211],a[152:155],v[44:47]\n"
MF "v[44:47],a[212:215],a[192:195],v[44:47]\n"
TR "a[204:205],v1 offset:4104\n"
MF "v[36:39],a[212:215],a[188:191],v[36:39]\n"
TR "a[206:207],v1 offset:4616\n"
MF "v[36:39],a[208:211],a[148:151],v[36:39]\n"
LD "v11,s[28:31],s82 offen offset:1024 lds\n"
MF "v[28:31],a[208:211],a[144:147],v[28:31]\n"
MF "v[28:31],a[212:215],a[184:187],v[28:31]\n"
MF "v[20:23],a[212:215],a[180:183],v[20:23]\n"
"s_waitcnt vmcnt(11) lgkmcnt(0)\n"
LD "v12,s[28:31],s82 offen offset:2048 lds\n"
MF "v[20:23],a[208:211],a[140:143],v[20:23]\n"
"s_barrier\n"
"v_bfe_u32 v14,v9,4,4\n"
"v_lshlrev_b32 v14,2,v14\n"
"buffer_load_dword v10,v14,s[40:43],s86 offen sc1\n"
".Lhs2:\n"
MF "v[56:59],a[216:219],a[140:143],v[56:59]\n"
TR "a[208:209],v0 offset:16384\n"
MF "v[56:59],a[220:223],a[180:183],v[56:59]\n"
TR "a[210:211],v0 offset:16896\n"
MF "v[64:67],a[220:223],a[184:187],v[64:67]\n"
LD "v13,s[28:31],s82 offen offset:3072 lds\n"
MF "v[64:67],a[216:219],a[144:147],v[64:67]\n"
TR "a[212:213],v0 offset:20480\n"
MF "v[72:75],a[216:219],a[148:151],v[72:75]\n"
TR "a[214:215],v0 offset:20992\n"
MF "v[72:75],a[220:223],a[188:191],v[72:75]\n"
"s_add_u32 s14,s81,384\n"
"s_sub_u32 s15,s14,1280\n"
"s_min_u32 s14,s14,s15\n"
MF "v[80:83],a[220:223],a[192:195],v[80:83]\n"
"s_add_u32 s14,s14,s68\n"
"s_add_u32 s84,s14,0\n"
"s_add_u32 m0,s63,0\n"
MF "v[80:83],a[216:219],a[152:155],v[80:83]\n"
LD "v4,s[24:27],s84 offen lds\n"
MF "v[88:91],a[216:219],a[156:159],v[88:91]\n"
MF "v[88:91],a[220:223],a[196:199],v[88:91]\n"
MF "v[92:95],a[204:207],a[196:199],v[92:95]\n"
TR "a[216:217],v0 offset:16392\n"
MF "v[92:95],a[200:203],a[156:159],v[92:95]\n"
"s_add_u32 s84,s14,9216\n"
TR "a[218:219],v0 offset:16904\n"
MF "v[84:87],a[200:203],a[152:155],v[84:87]\n"
LD "v5,s[24:27],s84 offen offset:1024 lds\n"
MF "v[84:87],a[204:207],a[192:195],v[84:87]\n"
"s_add_u32 m0,s62,16384\n"
TR "a[220:221],v0 offset:20488\n"
MF "v[76:79],a[204:207],a[188:191],v[76:79]\n"
"s_add_u32 s82,s66,1572992\n"
TR "a[222:223],v0 offset:21000\n"
MF "v[76:79],a[200:203],a[148:151],v[76:79]\n"
"s_sub_u32 s15,s82,7864320\n"
MF "v[68:71],a[200:203],a[144:147],v[68:71]\n"
"s_min_u32 s82,s82,s15\n"
MF "v[68:71],a[204:207],a[184:187],v[68:71]\n"
LD "v6,s[28:31],s82 offen lds\n"
MF "v[60:63],a[204:207],a[180:183],v[60:63]\n"
MF "v[60:63],a[200:203],a[140:143],v[60:63]\n"
"s_waitcnt lgkmcnt(4)\n"
MF "v[96:99],a[208:211],a[140:143],v[96:99]\n"
TR "a[200:201],v1 offset:16384\n"
MF "v[96:99],a[212:215],a[180:183],v[96:99]\n"
TR "a[202:203],v1 offset:16896\n"
MF "v[104:107],a[212:215],a[184:187],v[104:107]\n"
LD "v11,s[28:31],s82 offen offset:1024 lds\n"
MF "v[104:107],a[208:211],a[144:147],v[104:107]\n"
TR "a[204:205],v1 offset:20480\n"
MF "v[112:115],a[208:211],a[148:151],v[112:115]\n"
TR "a[206:207],v1 offset:20992\n"
MF "v[112:115],a[212:215],a[188:191],v[112:115]\n"
MF "v[120:123],a[212:215],a[192:195],v[120:123]\n"
MF "v[120:123],a[208:211],a[152:155],v[120:123]\n"
LD "v12,s[28:31],s82 offen offset:2048 lds\n"
MF "v[128:131],a[208:211],a[156:159],v[128:131]\n"
MF "v[128:131],a[212:215],a[196:199],v[128:131]\n"
"s_waitcnt lgkmcnt(4)\n"
MF "v[132:135],a[220:223],a[196:199],v[132:135]\n"
TR "a[208:209],v1 offset:16392\n"
MF "v[132:135],a[216:219],a[156:159],v[132:135]\n"
TR "a[210:211],v1 offset:16904\n"
MF "v[124:127],a[216:219],a[152:155],v[124:127]\n"
LD "v13,s[28:31],s82 offen offset:3072 lds\n"
MF "v[124:127],a[220:223],a[192:195],v[124:127]\n"
TR "a[212:213],v1 offset:20488\n"
MF "v[116:119],a[220:223],a[188:191],v[116:119]\n"
TR "a[214:215],v1 offset:21000\n"
MF "v[116:119],a[216:219],a[148:151],v[116:119]\n"
"s_add_u32 s84,s14,20480\n"
MF "v[108:111],a[216:219],a[144:147],v[108:111]\n"
"s_add_u32 m0,s63,2048\n"
MF "v[108:111],a[220:223],a[184:187],v[108:111]\n"
LD "v4,s[24:27],s84 offen lds\n"
MF "v[100:103],a[220:223],a[180:183],v[100:103]\n"
"s_waitcnt vmcnt(11) lgkmcnt(0)\n"
MF "v[100:103],a[216:219],a[140:143],v[100:103]\n"
"s_barrier\n"
MF "v[136:139],a[200:203],a[140:143],v[136:139]\n"
TR "a[216:217],v0 offset:32768\n"
MF "v[136:139],a[204:207],a[180:183],v[136:139]\n"
TR "a[218:219],v0 offset:33280\n"
MF "v[144:147],a[204:207],a[184:187],v[144:147]\n"
"ds_read_b128 a[60:63],v2 offset:40960\n"
MF "v[144:147],a[200:203],a[144:147],v[144:147]\n"
TR "a[220:221],v0 offset:36864\n"
MF "v[152:155],a[200:203],a[148:151],v[152:155]\n"
"s_add_u32 s84,s14,29696\n"
TR "a[222:223],v0 offset:37376\n"
MF "v[152:155],a[204:207],a[188:191],v[152:155]\n"
LD "v5,s[24:27],s84 offen offset:1024 lds\n"
MF "v[160:163],a[204:207],a[192:195],v[160:163]\n"
"ds_read_b128 a[100:103],v3 offset:40960\n"
MF "v[160:163],a[200:203],a[152:155],v[160:163]\n"
"s_add_u32 m0,s62,32768\n"
MF "v[168:171],a[200:203],a[156:159],v[168:171]\n"
"s_add_u32 s82,s66,1573120\n"
"s_sub_u32 s15,s82,7864320\n"
"s_min_u32 s82,s82,s15\n"
MF "v[168:171],a[204:207],a[196:199],v[168:171]\n"
LD "v6,s[28:31],s82 offen lds\n"
MF "v[172:175],a[212:215],a[196:199],v[172:175]\n"
TR "a[200:201],v0 offset:32776\n"
MF "v[172:175],a[208:211],a[156:159],v[172:175]\n"
TR "a[202:203],v0 offset:33288\n"
MF "v[164:167],a[208:211],a[152:155],v[164:167]\n"
"ds_read_b128 a[64:67],v2 offset:43008\n"
MF "v[164:167],a[212:215],a[192:195],v[164:167]\n"
TR "a[204:205],v0 offset:36872\n"
MF "v[156:159],a[212:215],a[188:191],v[156:159]\n"
TR "a[206:207],v0 offset:37384\n"
MF "v[156:159],a[208:211],a[148:151],v[156:159]\n"
LD "v11,s[28:31],s82 offen offset:1024 lds\n"
MF "v[148:151],a[208:211],a[144:147],v[148:151]\n"
"ds_read_b128 a[104:107],v3 offset:43008\n"
MF "v[148:151],a[212:215],a[184:187],v[148:151]\n"
MF "v[140:143],a[212:215],a[180:183],v[140:143]\n"
MF "v[140:143],a[208:211],a[140:143],v[140:143]\n"
"s_waitcnt lgkmcnt(7)\n"
LD "v12,s[28:31],s82 offen offset:2048 lds\n"
MF "v[176:179],a[216:219],a[140:143],v[176:179]\n"
TR "a[208:209],v1 offset:32768\n"
MF "v[176:179],a[220:223],a[180:183],v[176:179]\n"
TR "a[210:211],v1 offset:33280\n"
MF "v[184:187],a[220:223],a[184:187],v[184:187]\n"
"ds_read_b128 a[68:71],v2 offset:45056\n"
MF "v[184:187],a[216:219],a[144:147],v[184:187]\n"
TR "a[212:213],v1 offset:36864\n"
MF "v[192:195],a[216:219],a[148:151],v[192:195]\n"
TR "a[214:215],v1 offset:37376\n"
MF "v[192:195],a[220:223],a[188:191],v[192:195]\n"
LD "v13,s[28:31],s82 offen offset:3072 lds\n"
MF "v[200:203],a[220:223],a[192:195],v[200:203]\n"
"ds_read_b128 a[108:111],v3 offset:45056\n"
MF "v[200:203],a[216:219],a[152:155],v[200:203]\n"
"s_add_u32 s84,s14,40960\n"
MF "v[208:211],a[216:219],a[156:159],v[208:211]\n"
"s_add_u32 m0,s63,4096\n"
MF "v[208:211],a[220:223],a[196:199],v[208:211]\n"
"s_waitcnt lgkmcnt(7)\n"
LD "v4,s[24:27],s84 offen lds\n"
MF "v[212:215],a[204:207],a[196:199],v[212:215]\n"
"s_sub_u32 s66,s66,s83\n"
TR "a[216:217],v1 offset:32776\n"
MF "v[212:215],a[200:203],a[156:159],v[212:215]\n"
"s_add_u32 m0,s62,49152\n"
TR "a[218:219],v1 offset:33288\n"
MF "v[204:207],a[200:203],a[152:155],v[204:207]\n"
"s_add_u32 s82,s66,2359296\n"
"ds_read_b128 a[72:75],v2 offset:47104\n"
MF "v[204:207],a[204:207],a[192:195],v[204:207]\n"
"s_sub_u32 s15,s82,7864320\n"
TR "a[220:221],v1 offset:36872\n"
MF "v[196:199],a[204:207],a[188:191],v[196:199]\n"
"s_min_u32 s82,s82,s15\n"
TR "a[222:223],v1 offset:37384\n"
MF "v[196:199],a[200:203],a[148:151],v[196:199]\n"
LD "v6,s[28:31],s82 offen lds\n"
MF "v[188:191],a[200:203],a[144:147],v[188:191]\n"
"v_add_u32 v0,0xffff4000,v0\n"
MF "v[188:191],a[204:207],a[184:187],v[188:191]\n"
"v_add_u32 v1,0xffff4000,v1\n"
MF "v[180:183],a[204:207],a[180:183],v[180:183]\n"
"s_waitcnt vmcnt(14) lgkmcnt(0)\n"
LD "v11,s[28:31],s82 offen offset:1024 lds\n"
MF "v[180:183],a[200:203],a[140:143],v[180:183]\n"
"s_barrier\n"
MF "v[216:219],a[208:211],a[140:143],v[216:219]\n"
TR "a[200:201],v0\n"
MF "v[216:219],a[212:215],a[180:183],v[216:219]\n"
TR "a[202:203],v0 offset:512\n"
MF "v[224:227],a[212:215],a[184:187],v[224:227]\n"
"ds_read_b128 a[112:115],v3 offset:47104\n"
MF "v[224:227],a[208:211],a[144:147],v[224:227]\n"
TR "a[204:205],v0 offset:4096\n"
MF "v[232:235],a[208:211],a[148:151],v[232:235]\n"
TR "a[206:207],v0 offset:4608\n"
MF "v[232:235],a[212:215],a[188:191],v[232:235]\n"
LD "v12,s[28:31],s82 offen offset:2048 lds\n"
MF "v[240:243],a[212:215],a[192:195],v[240:243]\n"
"ds_read_b128 a[76:79],v2 offset:49152\n"
MF "v[240:243],a[208:211],a[152:155],v[240:243]\n"
"ds_read_b128 a[116:119],v3 offset:49152\n"
MF "v[248:251],a[208:211],a[156:159],v[248:251]\n"
MF "v[248:251],a[212:215],a[196:199],v[248:251]\n"
LD "v13,s[28:31],s82 offen offset:3072 lds\n"
MF "v[252:255],a[220:223],a[196:199],v[252:255]\n"
TR "a[208:209],v0 offset:8\n"
MF "v[252:255],a[216:219],a[156:159],v[252:255]\n"
TR "a[210:211],v0 offset:520\n"
MF "v[244:247],a[216:219],a[152:155],v[244:247]\n"
"s_add_u32 s14,s81,512\n"
"s_sub_u32 s15,s14,1280\n"
MF "v[244:247],a[220:223],a[192:195],v[244:247]\n"
"s_min_u32 s14,s14,s15\n"
"s_add_u32 s14,s14,s68\n"
TR "a[212:213],v0 offset:4104\n"
MF "v[236:239],a[220:223],a[188:191],v[236:239]\n"
"s_add_u32 s84,s14,0\n"
"s_add_u32 m0,s63,20480\n"
TR "a[214:215],v0 offset:4616\n"
MF "v[236:239],a[216:219],a[148:151],v[236:239]\n"
LD "v4,s[24:27],s84 offen lds\n"
MF "v[228:231],a[216:219],a[144:147],v[228:231]\n"
MF "v[228:231],a[220:223],a[184:187],v[228:231]\n"
MF "v[220:223],a[220:223],a[180:183],v[220:223]\n"
"s_add_u32 s84,s14,9216\n"
MF "v[220:223],a[216:219],a[140:143],v[220:223]\n"
"s_waitcnt lgkmcnt(4)\n"
LD "v5,s[24:27],s84 offen offset:1024 lds\n"
MF "v[16:19],a[200:203],a[60:63],v[16:19]\n"
TR "a[216:217],v1\n"
MF "v[16:19],a[204:207],a[100:103],v[16:19]\n"
"s_add_u32 m0,s62,65536\n"
TR "a[218:219],v1 offset:512\n"
MF "v[24:27],a[204:207],a[104:107],v[24:27]\n"
"s_add_u32 s82,s66,2359424\n"
MF "v[24:27],a[200:203],a[64:67],v[24:27]\n"
"s_sub_u32 s15,s82,7864320\n"
TR "a[220:221],v1 offset:4096\n"
MF "v[32:35],a[200:203],a[68:71],v[32:35]\n"
"s_min_u32 s82,s82,s15\n"
TR "a[222:223],v1 offset:4608\n"
MF "v[32:35],a[204:207],a[108:111],v[32:35]\n"
LD "v6,s[28:31],s82 offen lds\n"
MF "v[40:43],a[204:207],a[112:115],v[40:43]\n"
MF "v[40:43],a[200:203],a[72:75],v[40:43]\n"
MF "v[48:51],a[200:203],a[76:79],v[48:51]\n"
MF "v[48:51],a[204:207],a[116:119],v[48:51]\n"
"s_waitcnt lgkmcnt(4)\n"
LD "v11,s[28:31],s82 offen offset:1024 lds\n"
MF "v[52:55],a[212:215],a[116:119],v[52:55]\n"
TR "a[200:201],v1 offset:8\n"
MF "v[52:55],a[208:211],a[76:79],v[52:55]\n"
TR "a[202:203],v1 offset:520\n"
MF "v[44:47],a[208:211],a[72:75],v[44:47]\n"
MF "v[44:47],a[212:215],a[112:115],v[44:47]\n"
TR "a[204:205],v1 offset:4104\n"
MF "v[36:39],a[212:215],a[108:111],v[36:39]\n"
TR "a[206:207],v1 offset:4616\n"
MF "v[36:39],a[208:211],a[68:71],v[36:39]\n"
LD "v12,s[28:31],s82 offen offset:2048 lds\n"
MF "v[28:31],a[208:211],a[64:67],v[28:31]\n"
MF "v[28:31],a[212:215],a[104:107],v[28:31]\n"
MF "v[20:23],a[212:215],a[100:103],v[20:23]\n"
"s_waitcnt vmcnt(16) lgkmcnt(0)\n"
LD "v13,s[28:31],s82 offen offset:3072 lds\n"
MF "v[20:23],a[208:211],a[60:63],v[20:23]\n"
"s_barrier\n"
MF "v[56:59],a[216:219],a[60:63],v[56:59]\n"
"s_add_u32 s84,s14,20480\n"
TR "a[208:209],v0 offset:16384\n"
MF "v[56:59],a[220:223],a[100:103],v[56:59]\n"
"s_add_u32 m0,s63,22528\n"
TR "a[210:211],v0 offset:16896\n"
MF "v[64:67],a[220:223],a[104:107],v[64:67]\n"
LD "v4,s[24:27],s84 offen lds\n"
MF "v[64:67],a[216:219],a[64:67],v[64:67]\n"
TR "a[212:213],v0 offset:20480\n"
MF "v[72:75],a[216:219],a[68:71],v[72:75]\n"
TR "a[214:215],v0 offset:20992\n"
MF "v[72:75],a[220:223],a[108:111],v[72:75]\n"
MF "v[80:83],a[220:223],a[112:115],v[80:83]\n"
"s_add_u32 s84,s14,29696\n"
MF "v[80:83],a[216:219],a[72:75],v[80:83]\n"
LD "v5,s[24:27],s84 offen offset:1024 lds\n"
MF "v[88:91],a[216:219],a[76:79],v[88:91]\n"
"s_add_u32 m0,s62,81920\n"
MF "v[88:91],a[220:223],a[116:119],v[88:91]\n"
"s_add_u32 s82,s66,2359552\n"
MF "v[92:95],a[204:207],a[116:119],v[92:95]\n"
"s_sub_u32 s15,s82,7864320\n"
TR "a[216:217],v0 offset:16392\n"
MF "v[92:95],a[200:203],a[76:79],v[92:95]\n"
"s_min_u32 s82,s82,s15\n"
TR "a[218:219],v0 offset:16904\n"
MF "v[84:87],a[200:203],a[72:75],v[84:87]\n"
LD "v6,s[28:31],s82 offen lds\n"
MF "v[84:87],a[204:207],a[112:115],v[84:87]\n"
TR "a[220:221],v0 offset:20488\n"
MF "v[76:79],a[204:207],a[108:111],v[76:79]\n"
TR "a[222:223],v0 offset:21000\n"
MF "v[76:79],a[200:203],a[68:71],v[76:79]\n"
MF "v[68:71],a[200:203],a[64:67],v[68:71]\n"
MF "v[68:71],a[204:207],a[104:107],v[68:71]\n"
LD "v11,s[28:31],s82 offen offset:1024 lds\n"
MF "v[60:63],a[204:207],a[100:103],v[60:63]\n"
MF "v[60:63],a[200:203],a[60:63],v[60:63]\n"
"s_waitcnt lgkmcnt(4)\n"
MF "v[96:99],a[208:211],a[60:63],v[96:99]\n"
TR "a[200:201],v1 offset:16384\n"
MF "v[96:99],a[212:215],a[100:103],v[96:99]\n"
TR "a[202:203],v1 offset:16896\n"
MF "v[104:107],a[212:215],a[104:107],v[104:107]\n"
LD "v12,s[28:31],s82 offen offset:2048 lds\n"
MF "v[104:107],a[208:211],a[64:67],v[104:107]\n"
TR "a[204:205],v1 offset:20480\n"
MF "v[112:115],a[208:211],a[68:71],v[112:115]\n"
TR "a[206:207],v1 offset:20992\n"
MF "v[112:115],a[212:215],a[108:111],v[112:115]\n"
MF "v[120:123],a[212:215],a[112:115],v[120:123]\n"
MF "v[120:123],a[208:211],a[72:75],v[120:123]\n"
LD "v13,s[28:31],s82 offen offset:3072 lds\n"
MF "v[128:131],a[208:211],a[76:79],v[128:131]\n"
MF "v[128:131],a[212:215],a[116:119],v[128:131]\n"
"s_waitcnt lgkmcnt(4)\n"
MF "v[132:135],a[220:223],a[116:119],v[132:135]\n"
"s_add_u32 s84,s14,40960\n"
TR "a[208:209],v1 offset:16392\n"
MF "v[132:135],a[216:219],a[76:79],v[132:135]\n"
"s_add_u32 m0,s63,24576\n"
TR "a[210:211],v1 offset:16904\n"
MF "v[124:127],a[216:219],a[72:75],v[124:127]\n"
LD "v4,s[24:27],s84 offen lds\n"
MF "v[124:127],a[220:223],a[112:115],v[124:127]\n"
"s_add_u32 m0,s62,0\n"
TR "a[212:213],v1 offset:20488\n"
MF "v[116:119],a[220:223],a[108:111],v[116:119]\n"
"s_add_u32 s82,s66,3145728\n"
TR "a[214:215],v1 offset:21000\n"
MF "v[116:119],a[216:219],a[68:71],v[116:119]\n"
"s_sub_u32 s15,s82,7864320\n"
MF "v[108:111],a[216:219],a[64:67],v[108:111]\n"
"s_min_u32 s82,s82,s15\n"
MF "v[108:111],a[220:223],a[104:107],v[108:111]\n"
LD "v6,s[28:31],s82 offen lds\n"
MF "v[100:103],a[220:223],a[100:103],v[100:103]\n"
"s_waitcnt vmcnt(18) lgkmcnt(0)\n"
MF "v[100:103],a[216:219],a[60:63],v[100:103]\n"
"s_barrier\n"
MF "v[136:139],a[200:203],a[60:63],v[136:139]\n"
TR "a[216:217],v0 offset:32768\n"
MF "v[136:139],a[204:207],a[100:103],v[136:139]\n"
TR "a[218:219],v0 offset:33280\n"
MF "v[144:147],a[204:207],a[104:107],v[144:147]\n"
"ds_read_b128 a[140:143],v2\n"
MF "v[144:147],a[200:203],a[64:67],v[144:147]\n"
TR "a[220:221],v0 offset:36864\n"
MF "v[152:155],a[200:203],a[68:71],v[152:155]\n"
TR "a[222:223],v0 offset:37376\n"
MF "v[152:155],a[204:207],a[108:111],v[152:155]\n"
LD "v11,s[28:31],s82 offen offset:1024 lds\n"
MF "v[160:163],a[204:207],a[112:115],v[160:163]\n"
"ds_read_b128 a[180:183],v3\n"
MF "v[160:163],a[200:203],a[72:75],v[160:163]\n"
MF "v[168:171],a[200:203],a[76:79],v[168:171]\n"
MF "v[168:171],a[204:207],a[116:119],v[168:171]\n"
LD "v12,s[28:31],s82 offen offset:2048 lds\n"
MF "v[172:175],a[212:215],a[116:119],v[172:175]\n"
TR "a[200:201],v0 offset:32776\n"
MF "v[172:175],a[208:211],a[76:79],v[172:175]\n"
TR "a[202:203],v0 offset:33288\n"
MF "v[164:167],a[208:211],a[72:75],v[164:167]\n"
"ds_read_b128 a[144:147],v2 offset:2048\n"
MF "v[164:167],a[212:215],a[112:115],v[164:167]\n"
TR "a[204:205],v0 offset:36872\n"
MF "v[156:159],a[212:215],a[108:111],v[156:159]\n"
TR "a[206:207],v0 offset:37384\n"
MF "v[156:159],a[208:211],a[68:71],v[156:159]\n"
LD "v13,s[28:31],s82 offen offset:3072 lds\n"
MF "v[148:151],a[208:211],a[64:67],v[148:151]\n"
"ds_read_b128 a[184:187],v3 offset:2048\n"
MF "v[148:151],a[212:215],a[104:107],v[148:151]\n"
"s_add_u32 s14,s81,640\n"
"s_sub_u32 s15,s14,1280\n"
"s_min_u32 s14,s14,s15\n"
MF "v[140:143],a[212:215],a[100:103],v[140:143]\n"
"s_add_u32 s14,s14,s68\n"
"s_add_u32 s84,s14,0\n"
"s_add_u32 m0,s63,40960\n"
MF "v[140:143],a[208:211],a[60:63],v[140:143]\n"
"s_waitcnt lgkmcnt(7)\n"
LD "v4,s[24:27],s84 offen lds\n"
MF "v[176:179],a[216:219],a[60:63],v[176:179]\n"
TR "a[208:209],v1 offset:32768\n"
MF "v[176:179],a[220:223],a[100:103],v[176:179]\n"
TR "a[210:211],v1 offset:33280\n"
MF "v[184:187],a[220:223],a[104:107],v[184:187]\n"
"ds_read_b128 a[148:151],v2 offset:4096\n"
MF "v[184:187],a[216:219],a[64:67],v[184:187]\n"
TR "a[212:213],v1 offset:36864\n"
MF "v[192:195],a[216:219],a[68:71],v[192:195]\n"
"s_add_u32 s84,s14,9216\n"
TR "a[214:215],v1 offset:37376\n"
MF "v[192:195],a[220:223],a[108:111],v[192:195]\n"
LD "v5,s[24:27],s84 offen offset:1024 lds\n"
MF "v[200:203],a[220:223],a[112:115],v[200:203]\n"
"ds_read_b128 a[188:191],v3 offset:4096\n"
MF "v[200:203],a[216:219],a[72:75],v[200:203]\n"
"s_add_u32 m0,s62,16384\n"
MF "v[208:211],a[216:219],a[76:79],v[208:211]\n"
"s_add_u32 s82,s66,3145856\n"
"s_sub_u32 s15,s82,7864320\n"
"s_min_u32 s82,s82,s15\n"
MF "v[208:211],a[220:223],a[116:119],v[208:211]\n"
"s_waitcnt lgkmcnt(7)\n"
LD "v6,s[28:31],s82 offen lds\n"
MF "v[212:215],a[204:207],a[116:119],v[212:215]\n"
TR "a[216:217],v1 offset:32776\n"
MF "v[212:215],a[200:203],a[76:79],v[212:215]\n"
TR "a[218:219],v1 offset:33288\n"
MF "v[204:207],a[200:203],a[72:75],v[204:207]\n"
"ds_read_b128 a[152:155],v2 offset:6144\n"
MF "v[204:207],a[204:207],a[112:115],v[204:207]\n"
TR "a[220:221],v1 offset:36872\n"
MF "v[196:199],a[204:207],a[108:111],v[196:199]\n"
TR "a[222:223],v1 offset:37384\n"
MF "v[196:199],a[200:203],a[68:71],v[196:199]\n"
LD "v11,s[28:31],s82 offen offset:1024 lds\n"
MF "v[188:191],a[200:203],a[64:67],v[188:191]\n"
"v_add_u32 v0,0xc000,v0\n"
MF "v[188:191],a[204:207],a[104:107],v[188:191]\n"
"v_add_u32 v1,0xc000,v1\n"
MF "v[180:183],a[204:207],a[100:103],v[180:183]\n"
"s_waitcnt vmcnt(21) lgkmcnt(0)\n"
LD "v12,s[28:31],s82 offen offset:2048 lds\n"
MF "v[180:183],a[200:203],a[60:63],v[180:183]\n"
"s_barrier\n"
MF "v[216:219],a[208:211],a[60:63],v[216:219]\n"
TR "a[200:201],v0\n"
MF "v[216:219],a[212:215],a[100:103],v[216:219]\n"
TR "a[202:203],v0 offset:512\n"
MF "v[224:227],a[212:215],a[104:107],v[224:227]\n"
"ds_read_b128 a[192:195],v3 offset:6144\n"
MF "v[224:227],a[208:211],a[64:67],v[224:227]\n"
TR "a[204:205],v0 offset:4096\n"
MF "v[232:235],a[208:211],a[68:71],v[232:235]\n"
TR "a[206:207],v0 offset:4608\n"
MF "v[232:235],a[212:215],a[108:111],v[232:235]\n"
LD "v13,s[28:31],s82 offen offset:3072 lds\n"
MF "v[240:243],a[212:215],a[112:115],v[240:243]\n"
"ds_read_b128 a[156:159],v2 offset:8192\n"
MF "v[240:243],a[208:211],a[72:75],v[240:243]\n"
"s_add_u32 s84,s14,20480\n"
"ds_read_b128 a[196:199],v3 offset:8192\n"
MF "v[248:251],a[208:211],a[76:79],v[248:251]\n"
"s_add_u32 m0,s63,43008\n"
MF "v[248:251],a[212:215],a[116:119],v[248:251]\n"
LD "v4,s[24:27],s84 offen lds\n"
MF "v[252:255],a[220:223],a[116:119],v[252:255]\n"
TR "a[208:209],v0 offset:8\n"
MF "v[252:255],a[216:219],a[76:79],v[252:255]\n"
TR "a[210:211],v0 offset:520\n"
MF "v[244:247],a[216:219],a[72:75],v[244:247]\n"
MF "v[244:247],a[220:223],a[112:115],v[244:247]\n"
TR "a[212:213],v0 offset:4104\n"
MF "v[236:239],a[220:223],a[108:111],v[236:239]\n"
"s_add_u32 s84,s14,29696\n"
TR "a[214:215],v0 offset:4616\n"
MF "v[236:239],a[216:219],a[68:71],v[236:239]\n"
LD "v5,s[24:27],s84 offen offset:1024 lds\n"
MF "v[228:231],a[216:219],a[64:67],v[228:231]\n"
MF "v[228:231],a[220:223],a[104:107],v[228:231]\n"
"s_add_u32 m0,s62,32768\n"
MF "v[220:223],a[220:223],a[100:103],v[220:223]\n"
"s_add_u32 s82,s66,3145984\n"
"s_sub_u32 s15,s82,7864320\n"
"s_min_u32 s82,s82,s15\n"
MF "v[220:223],a[216:219],a[60:63],v[220:223]\n"
"s_waitcnt lgkmcnt(4)\n"
LD "v6,s[28:31],s82 offen lds\n"
MF "v[16:19],a[200:203],a[140:143],v[16:19]\n"
TR "a[216:217],v1\n"
MF "v[16:19],a[204:207],a[180:183],v[16:19]\n"
TR "a[218:219],v1 offset:512\n"
MF "v[24:27],a[204:207],a[184:187],v[24:27]\n"
MF "v[24:27],a[200:203],a[144:147],v[24:27]\n"
TR "a[220:221],v1 offset:4096\n"
MF "v[32:35],a[200:203],a[148:151],v[32:35]\n"
TR "a[222:223],v1 offset:4608\n"
MF "v[32:35],a[204:207],a[188:191],v[32:35]\n"
LD "v11,s[28:31],s82 offen offset:1024 lds\n"
MF "v[40:43],a[204:207],a[192:195],v[40:43]\n"
MF "v[40:43],a[200:203],a[152:155],v[40:43]\n"
MF "v[48:51],a[200:203],a[156:159],v[48:51]\n"
MF "v[48:51],a[204:207],a[196:199],v[48:51]\n"
"s_waitcnt lgkmcnt(4)\n"
LD "v12,s[28:31],s82 offen offset:2048 lds\n"
MF "v[52:55],a[212:215],a[196:199],v[52:55]\n"
TR "a[200:201],v1 offset:8\n"
MF "v[52:55],a[208:211],a[156:159],v[52:55]\n"
TR "a[202:203],v1 offset:520\n"
MF "v[44:47],a[208:211],a[152:155],v[44:47]\n"
MF "v[44:47],a[212:215],a[192:195],v[44:47]\n"
TR "a[204:205],v1 offset:4104\n"
MF "v[36:39],a[212:215],a[188:191],v[36:39]\n"
".Lpg2a:\n"
"s_cbranch_execz .Lisr1\n"
TR "a[206:207],v1 offset:4616\n"
MF "v[36:39],a[208:211],a[148:151],v[36:39]\n"
LD "v13,s[28:31],s82 offen offset:3072 lds\n"
MF "v[28:31],a[208:211],a[144:147],v[28:31]\n"
"s_add_u32 s84,s14,40960\n"
MF "v[28:31],a[212:215],a[184:187],v[28:31]\n"
"s_add_u32 m0,s63,45056\n"
MF "v[20:23],a[212:215],a[180:183],v[20:23]\n"
"s_waitcnt vmcnt(23) lgkmcnt(0)\n"
LD "v4,s[24:27],s84 offen lds\n"
MF "v[20:23],a[208:211],a[140:143],v[20:23]\n"
"s_barrier\n"
AW "a253,v10\n"
"v_add_u32 v10,1,v10\n"
"s_bfe_u32 s58,s22,0x30014\n"
"s_lshl_b32 s58,s58,2\n"
"s_add_u32 s58,s58,s56\n"
"s_lshl_b32 s58,s58,6\n"
"s_add_u32 s58,s58,s86\n"
"v_readlane_b32 s76,v10,s78\n"
"buffer_store_dword v10,v14,s[40:43],s58 offen offset:128 sc1\n"
".Lhs5:\n"
MF "v[56:59],a[216:219],a[140:143],v[56:59]\n"
"s_add_u32 m0,s62,49152\n"
"s_add_u32 s82,s66,3932160\n"
TR "a[208:209],v0 offset:16384\n"
MF "v[56:59],a[220:223],a[180:183],v[56:59]\n"
"s_sub_u32 s15,s82,7864320\n"
"s_min_u32 s82,s82,s15\n"
TR "a[210:211],v0 offset:16896\n"
MF "v[64:67],a[220:223],a[184:187],v[64:67]\n"
LD "v6,s[28:31],s82 offen lds\n"
MF "v[64:67],a[216:219],a[144:147],v[64:67]\n"
TR "a[212:213],v0 offset:20480\n"
MF "v[72:75],a[216:219],a[148:151],v[72:75]\n"
TR "a[214:215],v0 offset:20992\n"
MF "v[72:75],a[220:223],a[188:191],v[72:75]\n"
"buffer_load_dword a249,v15,s[48:51],s60 offen offset:1024\n"
MF "v[80:83],a[220:223],a[192:195],v[80:83]\n"
"s_bfe_u32 s59,s23,0x30003\n"
MF "v[80:83],a[216:219],a[152:155],v[80:83]\n"
LD "v11,s[28:31],s82 offen offset:1024 lds\n"
MF "v[88:91],a[216:219],a[156:159],v[88:91]\n"
"s_add_u32 s59,s59,1\n"
MF "v[88:91],a[220:223],a[196:199],v[88:91]\n"
"s_lshl_b32 s59,s59,7\n"
MF "v[92:95],a[204:207],a[196:199],v[92:95]\n"
"s_and_b32 s59,s59,768\n"
TR "a[216:217],v0 offset:16392\n"
MF "v[92:95],a[200:203],a[156:159],v[92:95]\n"
"s_add_u32 s59,s59,s60\n"
TR "a[218:219],v0 offset:16904\n"
MF "v[84:87],a[200:203],a[152:155],v[84:87]\n"
LD "v12,s[28:31],s82 offen offset:2048 lds\n"
MF "v[84:87],a[204:207],a[192:195],v[84:87]\n"
TR "a[220:221],v0 offset:20488\n"
MF "v[76:79],a[204:207],a[188:191],v[76:79]\n"
TR "a[222:223],v0 offset:21000\n"
MF "v[76:79],a[200:203],a[148:151],v[76:79]\n"
MF "v[68:71],a[200:203],a[144:147],v[68:71]\n"
MF "v[68:71],a[204:207],a[184:187],v[68:71]\n"
LD "v13,s[28:31],s82 offen offset:3072 lds\n"
MF "v[60:63],a[204:207],a[180:183],v[60:63]\n"
MF "v[60:63],a[200:203],a[140:143],v[60:63]\n"
"s_add_u32 s14,s81,768\n"
"s_sub_u32 s15,s14,1280\n"
"s_waitcnt lgkmcnt(4)\n"
MF "v[96:99],a[208:211],a[140:143],v[96:99]\n"
"s_min_u32 s14,s14,s15\n"
"s_add_u32 s14,s14,s68\n"
TR "a[200:201],v1 offset:16384\n"
MF "v[96:99],a[212:215],a[180:183],v[96:99]\n"
"s_add_u32 s84,s14,0\n"
"s_add_u32 m0,s63,0\n"
TR "a[202:203],v1 offset:16896\n"
MF "v[104:107],a[212:215],a[184:187],v[104:107]\n"
LD "v4,s[24:27],s84 offen lds\n"
MF "v[104:107],a[208:211],a[144:147],v[104:107]\n"
TR "a[204:205],v1 offset:20480\n"
MF "v[112:115],a[208:211],a[148:151],v[112:115]\n"
TR "a[206:207],v1 offset:20992\n"
MF "v[112:115],a[212:215],a[188:191],v[112:115]\n"
MF "v[120:123],a[212:215],a[192:195],v[120:123]\n"
"s_add_u32 s84,s14,9216\n"
MF "v[120:123],a[208:211],a[152:155],v[120:123]\n"
LD "v5,s[24:27],s84 offen offset:1024 lds\n"
MF "v[128:131],a[208:211],a[156:159],v[128:131]\n"
MF "v[128:131],a[212:215],a[196:199],v[128:131]\n"
"s_waitcnt lgkmcnt(4)\n"
MF "v[132:135],a[220:223],a[196:199],v[132:135]\n"
TR "a[208:209],v1 offset:16392\n"
MF "v[132:135],a[216:219],a[156:159],v[132:135]\n"
TR "a[210:211],v1 offset:16904\n"
MF "v[124:127],a[216:219],a[152:155],v[124:127]\n"
MF "v[124:127],a[220:223],a[192:195],v[124:127]\n"
TR "a[212:213],v1 offset:20488\n"
MF "v[116:119],a[220:223],a[188:191],v[116:119]\n"
TR "a[214:215],v1 offset:21000\n"
MF "v[116:119],a[216:219],a[148:151],v[116:119]\n"
MF "v[108:111],a[216:219],a[144:147],v[108:111]\n"
MF "v[108:111],a[220:223],a[184:187],v[108:111]\n"
MF "v[100:103],a[220:223],a[180:183],v[100:103]\n"
"s_waitcnt vmcnt(24) lgkmcnt(0)\n"
MF "v[100:103],a[216:219],a[140:143],v[100:103]\n"
"s_barrier\n"
MF "v[136:139],a[200:203],a[140:143],v[136:139]\n"
TR "a[216:217],v0 offset:32768\n"
MF "v[136:139],a[204:207],a[180:183],v[136:139]\n"
"s_add_u32 m0,s62,65536\n"
TR "a[218:219],v0 offset:33280\n"
MF "v[144:147],a[204:207],a[184:187],v[144:147]\n"
"s_add_u32 s82,s66,3932288\n"
"ds_read_b128 a[60:63],v2 offset:20480\n"
MF "v[144:147],a[200:203],a[144:147],v[144:147]\n"
"s_sub_u32 s15,s82,7864320\n"
TR "a[220:221],v0 offset:36864\n"
MF "v[152:155],a[200:203],a[148:151],v[152:155]\n"
"s_min_u32 s82,s82,s15\n"
TR "a[222:223],v0 offset:37376\n"
MF "v[152:155],a[204:207],a[188:191],v[152:155]\n"
LD "v6,s[28:31],s82 offen lds\n"
MF "v[160:163],a[204:207],a[192:195],v[160:163]\n"
"ds_read_b128 a[100:103],v3 offset:20480\n"
MF "v[160:163],a[200:203],a[152:155],v[160:163]\n"
MF "v[168:171],a[200:203],a[156:159],v[168:171]\n"
MF "v[168:171],a[204:207],a[196:199],v[168:171]\n"
LD "v11,s[28:31],s82 offen offset:1024 lds\n"
MF "v[172:175],a[212:215],a[196:199],v[172:175]\n"
TR "a[200:201],v0 offset:32776\n"
MF "v[172:175],a[208:211],a[156:159],v[172:175]\n"
TR "a[202:203],v0 offset:33288\n"
MF "v[164:167],a[208:211],a[152:155],v[164:167]\n"
"ds_read_b128 a[64:67],v2 offset:22528\n"
MF "v[164:167],a[212:215],a[192:195],v[164:167]\n"
TR "a[204:205],v0 offset:36872\n"
MF "v[156:159],a[212:215],a[188:191],v[156:159]\n"
TR "a[206:207],v0 offset:37384\n"
MF "v[156:159],a[208:211],a[148:151],v[156:159]\n"
LD "v12,s[28:31],s82 offen offset:2048 lds\n"
MF "v[148:151],a[208:211],a[144:147],v[148:151]\n"
"ds_read_b128 a[104:107],v3 offset:22528\n"
MF "v[148:151],a[212:215],a[184:187],v[148:151]\n"
MF "v[140:143],a[212:215],a[180:183],v[140:143]\n"
MF "v[140:143],a[208:211],a[140:143],v[140:143]\n"
"s_waitcnt lgkmcnt(7)\n"
LD "v13,s[28:31],s82 offen offset:3072 lds\n"
MF "v[176:179],a[216:219],a[140:143],v[176:179]\n"
TR "a[208:209],v1 offset:32768\n"
MF "v[176:179],a[220:223],a[180:183],v[176:179]\n"
TR "a[210:211],v1 offset:33280\n"
MF "v[184:187],a[220:223],a[184:187],v[184:187]\n"
"ds_read_b128 a[68:71],v2 offset:24576\n"
MF "v[184:187],a[216:219],a[144:147],v[184:187]\n"
"s_add_u32 s84,s14,20480\n"
TR "a[212:213],v1 offset:36864\n"
MF "v[192:195],a[216:219],a[148:151],v[192:195]\n"
"s_add_u32 m0,s63,2048\n"
TR "a[214:215],v1 offset:37376\n"
MF "v[192:195],a[220:223],a[188:191],v[192:195]\n"
LD "v4,s[24:27],s84 offen lds\n"
MF "v[200:203],a[220:223],a[192:195],v[200:203]\n"
"ds_read_b128 a[108:111],v3 offset:24576\n"
MF "v[200:203],a[216:219],a[152:155],v[200:203]\n"
MF "v[208:211],a[216:219],a[156:159],v[208:211]\n"
"s_add_u32 s84,s14,29696\n"
MF "v[208:211],a[220:223],a[196:199],v[208:211]\n"
"s_waitcnt lgkmcnt(7)\n"
LD "v5,s[24:27],s84 offen offset:1024 lds\n"
MF "v[212:215],a[204:207],a[196:199],v[212:215]\n"
TR "a[216:217],v1 offset:32776\n"
MF "v[212:215],a[200:203],a[156:159],v[212:215]\n"
TR "a[218:219],v1 offset:33288\n"
MF "v[204:207],a[200:203],a[152:155],v[204:207]\n"
"ds_read_b128 a[72:75],v2 offset:26624\n"
MF "v[204:207],a[204:207],a[192:195],v[204:207]\n"
TR "a[220:221],v1 offset:36872\n"
MF "v[196:199],a[204:207],a[188:191],v[196:199]\n"
TR "a[222:223],v1 offset:37384\n"
MF "v[196:199],a[200:203],a[148:151],v[196:199]\n"
MF "v[188:191],a[200:203],a[144:147],v[188:191]\n"
"v_add_u32 v0,0xffff4000,v0\n"
MF "v[188:191],a[204:207],a[184:187],v[188:191]\n"
"v_add_u32 v1,0xffff4000,v1\n"
MF "v[180:183],a[204:207],a[180:183],v[180:183]\n"
"s_waitcnt vmcnt(26) lgkmcnt(0)\n"
MF "v[180:183],a[200:203],a[140:143],v[180:183]\n"
"s_barrier\n"
MF "v[216:219],a[208:211],a[140:143],v[216:219]\n"
TR "a[200:201],v0\n"
MF "v[216:219],a[212:215],a[180:183],v[216:219]\n"
"s_add_u32 m0,s62,81920\n"
TR "a[202:203],v0 offset:512\n"
MF "v[224:227],a[212:215],a[184:187],v[224:227]\n"
"s_add_u32 s82,s66,3932416\n"
"ds_read_b128 a[112:115],v3 offset:26624\n"
MF "v[224:227],a[208:211],a[144:147],v[224:227]\n"
"s_sub_u32 s15,s82,7864320\n"
TR "a[204:205],v0 offset:4096\n"
MF "v[232:235],a[208:211],a[148:151],v[232:235]\n"
"s_min_u32 s82,s82,s15\n"
TR "a[206:207],v0 offset:4608\n"
MF "v[232:235],a[212:215],a[188:191],v[232:235]\n"
LD "v6,s[28:31],s82 offen lds\n"
MF "v[240:243],a[212:215],a[192:195],v[240:243]\n"
"ds_read_b128 a[76:79],v2 offset:28672\n"
MF "v[240:243],a[208:211],a[152:155],v[240:243]\n"
"ds_read_b128 a[116:119],v3 offset:28672\n"
MF "v[248:251],a[208:211],a[156:159],v[248:251]\n"
MF "v[248:251],a[212:215],a[196:199],v[248:251]\n"
LD "v11,s[28:31],s82 offen offset:1024 lds\n"
MF "v[252:255],a[220:223],a[196:199],v[252:255]\n"
TR "a[208:209],v0 offset:8\n"
MF "v[252:255],a[216:219],a[156:159],v[252:255]\n"
TR "a[210:211],v0 offset:520\n"
MF "v[244:247],a[216:219],a[152:155],v[244:247]\n"
MF "v[244:247],a[220:223],a[192:195],v[244:247]\n"
TR "a[212:213],v0 offset:4104\n"
MF "v[236:239],a[220:223],a[188:191],v[236:239]\n"
TR "a[214:215],v0 offset:4616\n"
MF "v[236:239],a[216:219],a[148:151],v[236:239]\n"
LD "v12,s[28:31],s82 offen offset:2048 lds\n"
MF "v[228:231],a[216:219],a[144:147],v[228:231]\n"
MF "v[228:231],a[220:223],a[184:187],v[228:231]\n"
MF "v[220:223],a[220:223],a[180:183],v[220:223]\n"
MF "v[220:223],a[216:219],a[140:143],v[220:223]\n"
"s_waitcnt lgkmcnt(4)\n"
LD "v13,s[28:31],s82 offen offset:3072 lds\n"
MF "v[16:19],a[200:203],a[60:63],v[16:19]\n"
TR "a[216:217],v1\n"
MF "v[16:19],a[204:207],a[100:103],v[16:19]\n"
TR "a[218:219],v1 offset:512\n"
MF "v[24:27],a[204:207],a[104:107],v[24:27]\n"
MF "v[24:27],a[200:203],a[64:67],v[24:27]\n"
"s_add_u32 s84,s14,40960\n"
TR "a[220:221],v1 offset:4096\n"
MF "v[32:35],a[200:203],a[68:71],v[32:35]\n"
"s_add_u32 m0,s63,4096\n"
TR "a[222:223],v1 offset:4608\n"
MF "v[32:35],a[204:207],a[108:111],v[32:35]\n"
LD "v4,s[24:27],s84 offen lds\n"
MF "v[40:43],a[204:207],a[112:115],v[40:43]\n"
MF "v[40:43],a[200:203],a[72:75],v[40:43]\n"
MF "v[48:51],a[200:203],a[76:79],v[48:51]\n"
MF "v[48:51],a[204:207],a[116:119],v[48:51]\n"
"s_waitcnt lgkmcnt(4)\n"
MF "v[52:55],a[212:215],a[116:119],v[52:55]\n"
TR "a[200:201],v1 offset:8\n"
MF "v[52:55],a[208:211],a[76:79],v[52:55]\n"
TR "a[202:203],v1 offset:520\n"
MF "v[44:47],a[208:211],a[72:75],v[44:47]\n"
MF "v[44:47],a[212:215],a[112:115],v[44:47]\n"
TR "a[204:205],v1 offset:4104\n"
MF "v[36:39],a[212:215],a[108:111],v[36:39]\n"
TR "a[206:207],v1 offset:4616\n"
MF "v[36:39],a[208:211],a[68:71],v[36:39]\n"
MF "v[28:31],a[208:211],a[64:67],v[28:31]\n"
MF "v[28:31],a[212:215],a[104:107],v[28:31]\n"
MF "v[20:23],a[212:215],a[100:103],v[20:23]\n"
"s_waitcnt vmcnt(25) lgkmcnt(0)\n"
MF "v[20:23],a[208:211],a[60:63],v[20:23]\n"
"s_barrier\n"
MF "v[56:59],a[216:219],a[60:63],v[56:59]\n"
"s_add_u32 m0,s62,0\n"
"s_add_u32 s82,s66,4718592\n"
TR "a[208:209],v0 offset:16384\n"
MF "v[56:59],a[220:223],a[100:103],v[56:59]\n"
"s_sub_u32 s15,s82,7864320\n"
"s_min_u32 s82,s82,s15\n"
TR "a[210:211],v0 offset:16896\n"
MF "v[64:67],a[220:223],a[104:107],v[64:67]\n"
LD "v6,s[28:31],s82 offen lds\n"
MF "v[64:67],a[216:219],a[64:67],v[64:67]\n"
TR "a[212:213],v0 offset:20480\n"
MF "v[72:75],a[216:219],a[68:71],v[72:75]\n"
TR "a[214:215],v0 offset:20992\n"
MF "v[72:75],a[220:223],a[108:111],v[72:75]\n"
"buffer_load_dword a249,v15,s[48:51],s59 offen\n"
MF "v[80:83],a[220:223],a[112:115],v[80:83]\n"
"s_bfe_u32 s59,s23,0x30003\n"
MF "v[80:83],a[216:219],a[72:75],v[80:83]\n"
LD "v11,s[28:31],s82 offen offset:1024 lds\n"
MF "v[88:91],a[216:219],a[76:79],v[88:91]\n"
"s_add_u32 s59,s59,3\n"
MF "v[88:91],a[220:223],a[116:119],v[88:91]\n"
"s_lshl_b32 s59,s59,7\n"
MF "v[92:95],a[204:207],a[116:119],v[92:95]\n"
"s_and_b32 s59,s59,768\n"
TR "a[216:217],v0 offset:16392\n"
MF "v[92:95],a[200:203],a[76:79],v[92:95]\n"
"s_add_u32 s59,s59,s60\n"
TR "a[218:219],v0 offset:16904\n"
MF "v[84:87],a[200:203],a[72:75],v[84:87]\n"
LD "v12,s[28:31],s82 offen offset:2048 lds\n"
MF "v[84:87],a[204:207],a[112:115],v[84:87]\n"
TR "a[220:221],v0 offset:20488\n"
MF "v[76:79],a[204:207],a[108:111],v[76:79]\n"
TR "a[222:223],v0 offset:21000\n"
MF "v[76:79],a[200:203],a[68:71],v[76:79]\n"
MF "v[68:71],a[200:203],a[64:67],v[68:71]\n"
MF "v[68:71],a[204:207],a[104:107],v[68:71]\n"
LD "v13,s[28:31],s82 offen offset:3072 lds\n"
MF "v[60:63],a[204:207],a[100:103],v[60:63]\n"
MF "v[60:63],a[200:203],a[60:63],v[60:63]\n"
"s_add_u32 s14,s81,896\n"
"s_sub_u32 s15,s14,1280\n"
"s_waitcnt lgkmcnt(4)\n"
MF "v[96:99],a[208:211],a[60:63],v[96:99]\n"
"s_min_u32 s14,s14,s15\n"
"s_add_u32 s14,s14,s68\n"
TR "a[200:201],v1 offset:16384\n"
MF "v[96:99],a[212:215],a[100:103],v[96:99]\n"
"s_add_u32 s84,s14,0\n"
"s_add_u32 m0,s63,20480\n"
TR "a[202:203],v1 offset:16896\n"
MF "v[104:107],a[212:215],a[104:107],v[104:107]\n"
LD "v4,s[24:27],s84 offen lds\n"
MF "v[104:107],a[208:211],a[64:67],v[104:107]\n"
TR "a[204:205],v1 offset:20480\n"
MF "v[112:115],a[208:211],a[68:71],v[112:115]\n"
TR "a[206:207],v1 offset:20992\n"
MF "v[112:115],a[212:215],a[108:111],v[112:115]\n"
MF "v[120:123],a[212:215],a[112:115],v[120:123]\n"
"s_add_u32 s84,s14,9216\n"
MF "v[120:123],a[208:211],a[72:75],v[120:123]\n"
LD "v5,s[24:27],s84 offen offset:1024 lds\n"
MF "v[128:131],a[208:211],a[76:79],v[128:131]\n"
MF "v[128:131],a[212:215],a[116:119],v[128:131]\n"
"s_waitcnt lgkmcnt(4)\n"
MF "v[132:135],a[220:223],a[116:119],v[132:135]\n"
TR "a[208:209],v1 offset:16392\n"
MF "v[132:135],a[216:219],a[76:79],v[132:135]\n"
TR "a[210:211],v1 offset:16904\n"
MF "v[124:127],a[216:219],a[72:75],v[124:127]\n"
MF "v[124:127],a[220:223],a[112:115],v[124:127]\n"
TR "a[212:213],v1 offset:20488\n"
MF "v[116:119],a[220:223],a[108:111],v[116:119]\n"
TR "a[214:215],v1 offset:21000\n"
MF "v[116:119],a[216:219],a[68:71],v[116:119]\n"
MF "v[108:111],a[216:219],a[64:67],v[108:111]\n"
MF "v[108:111],a[220:223],a[104:107],v[108:111]\n"
MF "v[100:103],a[220:223],a[100:103],v[100:103]\n"
"s_waitcnt vmcnt(25) lgkmcnt(0)\n"
MF "v[100:103],a[216:219],a[60:63],v[100:103]\n"
"s_barrier\n"
MF "v[136:139],a[200:203],a[60:63],v[136:139]\n"
TR "a[216:217],v0 offset:32768\n"
MF "v[136:139],a[204:207],a[100:103],v[136:139]\n"
"s_add_u32 m0,s62,16384\n"
TR "a[218:219],v0 offset:33280\n"
MF "v[144:147],a[204:207],a[104:107],v[144:147]\n"
"s_add_u32 s82,s66,4718720\n"
"ds_read_b128 a[140:143],v2 offset:40960\n"
MF "v[144:147],a[200:203],a[64:67],v[144:147]\n"
"s_sub_u32 s15,s82,7864320\n"
TR "a[220:221],v0 offset:36864\n"
MF "v[152:155],a[200:203],a[68:71],v[152:155]\n"
"s_min_u32 s82,s82,s15\n"
TR "a[222:223],v0 offset:37376\n"
MF "v[152:155],a[204:207],a[108:111],v[152:155]\n"
LD "v6,s[28:31],s82 offen lds\n"
MF "v[160:163],a[204:207],a[112:115],v[160:163]\n"
"ds_read_b128 a[180:183],v3 offset:40960\n"
MF "v[160:163],a[200:203],a[72:75],v[160:163]\n"
MF "v[168:171],a[200:203],a[76:79],v[168:171]\n"
MF "v[168:171],a[204:207],a[116:119],v[168:171]\n"
LD "v11,s[28:31],s82 offen offset:1024 lds\n"
MF "v[172:175],a[212:215],a[116:119],v[172:175]\n"
TR "a[200:201],v0 offset:32776\n"
MF "v[172:175],a[208:211],a[76:79],v[172:175]\n"
TR "a[202:203],v0 offset:33288\n"
MF "v[164:167],a[208:211],a[72:75],v[164:167]\n"
"ds_read_b128 a[144:147],v2 offset:43008\n"
MF "v[164:167],a[212:215],a[112:115],v[164:167]\n"
TR "a[204:205],v0 offset:36872\n"
MF "v[156:159],a[212:215],a[108:111],v[156:159]\n"
TR "a[206:207],v0 offset:37384\n"
MF "v[156:159],a[208:211],a[68:71],v[156:159]\n"
LD "v12,s[28:31],s82 offen offset:2048 lds\n"
MF "v[148:151],a[208:211],a[64:67],v[148:151]\n"
"ds_read_b128 a[184:187],v3 offset:43008\n"
MF "v[148:151],a[212:215],a[104:107],v[148:151]\n"
MF "v[140:143],a[212:215],a[100:103],v[140:143]\n"
MF "v[140:143],a[208:211],a[60:63],v[140:143]\n"
"s_waitcnt lgkmcnt(7)\n"
LD "v13,s[28:31],s82 offen offset:3072 lds\n"
MF "v[176:179],a[216:219],a[60:63],v[176:179]\n"
TR "a[208:209],v1 offset:32768\n"
MF "v[176:179],a[220:223],a[100:103],v[176:179]\n"
TR "a[210:211],v1 offset:33280\n"
MF "v[184:187],a[220:223],a[104:107],v[184:187]\n"
"ds_read_b128 a[148:151],v2 offset:45056\n"
MF "v[184:187],a[216:219],a[64:67],v[184:187]\n"
"s_add_u32 s84,s14,20480\n"
TR "a[212:213],v1 offset:36864\n"
MF "v[192:195],a[216:219],a[68:71],v[192:195]\n"
"s_add_u32 m0,s63,22528\n"
TR "a[214:215],v1 offset:37376\n"
MF "v[192:195],a[220:223],a[108:111],v[192:195]\n"
LD "v4,s[24:27],s84 offen lds\n"
MF "v[200:203],a[220:223],a[112:115],v[200:203]\n"
"ds_read_b128 a[188:191],v3 offset:45056\n"
MF "v[200:203],a[216:219],a[72:75],v[200:203]\n"
MF "v[208:211],a[216:219],a[76:79],v[208:211]\n"
"s_add_u32 s84,s14,29696\n"
MF "v[208:211],a[220:223],a[116:119],v[208:211]\n"
"s_waitcnt lgkmcnt(7)\n"
LD "v5,s[24:27],s84 offen offset:1024 lds\n"
MF "v[212:215],a[204:207],a[116:119],v[212:215]\n"
TR "a[216:217],v1 offset:32776\n"
MF "v[212:215],a[200:203],a[76:79],v[212:215]\n"
TR "a[218:219],v1 offset:33288\n"
MF "v[204:207],a[200:203],a[72:75],v[204:207]\n"
"ds_read_b128 a[152:155],v2 offset:47104\n"
MF "v[204:207],a[204:207],a[112:115],v[204:207]\n"
TR "a[220:221],v1 offset:36872\n"
MF "v[196:199],a[204:207],a[108:111],v[196:199]\n"
TR "a[222:223],v1 offset:37384\n"
MF "v[196:199],a[200:203],a[68:71],v[196:199]\n"
MF "v[188:191],a[200:203],a[64:67],v[188:191]\n"
"v_add_u32 v0,0xc000,v0\n"
MF "v[188:191],a[204:207],a[104:107],v[188:191]\n"
"v_add_u32 v1,0xc000,v1\n"
MF "v[180:183],a[204:207],a[100:103],v[180:183]\n"
"s_waitcnt vmcnt(26) lgkmcnt(0)\n"
MF "v[180:183],a[200:203],a[60:63],v[180:183]\n"
"s_barrier\n"
MF "v[216:219],a[208:211],a[60:63],v[216:219]\n"
TR "a[200:201],v0\n"
MF "v[216:219],a[212:215],a[100:103],v[216:219]\n"
"s_add_u32 m0,s62,32768\n"
TR "a[202:203],v0 offset:512\n"
MF "v[224:227],a[212:215],a[104:107],v[224:227]\n"
"s_add_u32 s82,s66,4718848\n"
"ds_read_b128 a[192:195],v3 offset:47104\n"
MF "v[224:227],a[208:211],a[64:67],v[224:227]\n"
"s_sub_u32 s15,s82,7864320\n"
TR "a[204:205],v0 offset:4096\n"
MF "v[232:235],a[208:211],a[68:71],v[232:235]\n"
"s_min_u32 s82,s82,s15\n"
TR "a[206:207],v0 offset:4608\n"
MF "v[232:235],a[212:215],a[108:111],v[232:235]\n"
LD "v6,s[28:31],s82 offen lds\n"
MF "v[240:243],a[212:215],a[112:115],v[240:243]\n"
"ds_read_b128 a[156:159],v2 offset:49152\n"
MF "v[240:243],a[208:211],a[72:75],v[240:243]\n"
"ds_read_b128 a[196:199],v3 offset:49152\n"
MF "v[248:251],a[208:211],a[76:79],v[248:251]\n"
MF "v[248:251],a[212:215],a[116:119],v[248:251]\n"
LD "v11,s[28:31],s82 offen offset:1024 lds\n"
MF "v[252:255],a[220:223],a[116:119],v[252:255]\n"
TR "a[208:209],v0 offset:8\n"
MF "v[252:255],a[216:219],a[76:79],v[252:255]\n"
TR "a[210:211],v0 offset:520\n"
MF "v[244:247],a[216:219],a[72:75],v[244:247]\n"
MF "v[244:247],a[220:223],a[112:115],v[244:247]\n"
TR "a[212:213],v0 offset:4104\n"
MF "v[236:239],a[220:223],a[108:111],v[236:239]\n"
TR "a[214:215],v0 offset:4616\n"
MF "v[236:239],a[216:219],a[68:71],v[236:239]\n"
LD "v12,s[28:31],s82 offen offset:2048 lds\n"
MF "v[228:231],a[216:219],a[64:67],v[228:231]\n"
MF "v[228:231],a[220:223],a[104:107],v[228:231]\n"
MF "v[220:223],a[220:223],a[100:103],v[220:223]\n"
MF "v[220:223],a[216:219],a[60:63],v[220:223]\n"
"s_waitcnt lgkmcnt(4)\n"
LD "v13,s[28:31],s82 offen offset:3072 lds\n"
MF "v[16:19],a[200:203],a[140:143],v[16:19]\n"
TR "a[216:217],v1\n"
MF "v[16:19],a[204:207],a[180:183],v[16:19]\n"
TR "a[218:219],v1 offset:512\n"
MF "v[24:27],a[204:207],a[184:187],v[24:27]\n"
MF "v[24:27],a[200:203],a[144:147],v[24:27]\n"
"s_add_u32 s84,s14,40960\n"
TR "a[220:221],v1 offset:4096\n"
MF "v[32:35],a[200:203],a[148:151],v[32:35]\n"
"s_add_u32 m0,s63,24576\n"
TR "a[222:223],v1 offset:4608\n"
MF "v[32:35],a[204:207],a[188:191],v[32:35]\n"
LD "v4,s[24:27],s84 offen lds\n"
MF "v[40:43],a[204:207],a[192:195],v[40:43]\n"
MF "v[40:43],a[200:203],a[152:155],v[40:43]\n"
MF "v[48:51],a[200:203],a[156:159],v[48:51]\n"
MF "v[48:51],a[204:207],a[196:199],v[48:51]\n"
"s_waitcnt lgkmcnt(4)\n"
MF "v[52:55],a[212:215],a[196:199],v[52:55]\n"
TR "a[200:201],v1 offset:8\n"
MF "v[52:55],a[208:211],a[156:159],v[52:55]\n"
TR "a[202:203],v1 offset:520\n"
MF "v[44:47],a[208:211],a[152:155],v[44:47]\n"
MF "v[44:47],a[212:215],a[192:195],v[44:47]\n"
TR "a[204:205],v1 offset:4104\n"
MF "v[36:39],a[212:215],a[188:191],v[36:39]\n"
TR "a[206:207],v1 offset:4616\n"
MF "v[36:39],a[208:211],a[148:151],v[36:39]\n"
MF "v[28:31],a[208:211],a[144:147],v[28:31]\n"
MF "v[28:31],a[212:215],a[184:187],v[28:31]\n"
MF "v[20:23],a[212:215],a[180:183],v[20:23]\n"
"s_waitcnt vmcnt(25) lgkmcnt(0)\n"
MF "v[20:23],a[208:211],a[140:143],v[20:23]\n"
"s_barrier\n"
MF "v[56:59],a[216:219],a[140:143],v[56:59]\n"
"s_add_u32 m0,s62,49152\n"
"s_add_u32 s82,s66,5505024\n"
TR "a[208:209],v0 offset:16384\n"
MF "v[56:59],a[220:223],a[180:183],v[56:59]\n"
"s_sub_u32 s15,s82,7864320\n"
"s_min_u32 s82,s82,s15\n"
TR "a[210:211],v0 offset:16896\n"
MF "v[64:67],a[220:223],a[184:187],v[64:67]\n"
LD "v6,s[28:31],s82 offen lds\n"
MF "v[64:67],a[216:219],a[144:147],v[64:67]\n"
TR "a[212:213],v0 offset:20480\n"
MF "v[72:75],a[216:219],a[148:151],v[72:75]\n"
TR "a[214:215],v0 offset:20992\n"
MF "v[72:75],a[220:223],a[188:191],v[72:75]\n"
"buffer_load_dword a249,v15,s[48:51],s59 offen\n"
MF "v[80:83],a[220:223],a[192:195],v[80:83]\n"
"s_bfe_u32 s59,s23,0x30003\n"
MF "v[80:83],a[216:219],a[152:155],v[80:83]\n"
LD "v11,s[28:31],s82 offen offset:1024 lds\n"
MF "v[88:91],a[216:219],a[156:159],v[88:91]\n"
"s_add_u32 s59,s59,5\n"
MF "v[88:91],a[220:223],a[196:199],v[88:91]\n"
"s_lshl_b32 s59,s59,7\n"
MF "v[92:95],a[204:207],a[196:199],v[92:95]\n"
"s_and_b32 s59,s59,768\n"
TR "a[216:217],v0 offset:16392\n"
MF "v[92:95],a[200:203],a[156:159],v[92:95]\n"
"s_add_u32 s59,s59,s60\n"
TR "a[218:219],v0 offset:16904\n"
MF "v[84:87],a[200:203],a[152:155],v[84:87]\n"
LD "v12,s[28:31],s82 offen offset:2048 lds\n"
MF "v[84:87],a[204:207],a[192:195],v[84:87]\n"
TR "a[220:221],v0 offset:20488\n"
MF "v[76:79],a[204:207],a[188:191],v[76:79]\n"
TR "a[222:223],v0 offset:21000\n"
MF "v[76:79],a[200:203],a[148:151],v[76:79]\n"
MF "v[68:71],a[200:203],a[144:147],v[68:71]\n"
MF "v[68:71],a[204:207],a[184:187],v[68:71]\n"
".Lpg3a:\n"
"s_cbranch_execz .Lisr1\n"
LD "v13,s[28:31],s82 offen offset:3072 lds\n"
MF "v[60:63],a[204:207],a[180:183],v[60:63]\n"
MF "v[60:63],a[200:203],a[140:143],v[60:63]\n"
"s_add_u32 s14,s81,1024\n"
"s_sub_u32 s15,s14,1280\n"
"s_waitcnt lgkmcnt(4)\n"
MF "v[96:99],a[208:211],a[140:143],v[96:99]\n"
"s_min_u32 s14,s14,s15\n"
"s_add_u32 s14,s14,s68\n"
TR "a[200:201],v1 offset:16384\n"
MF "v[96:99],a[212:215],a[180:183],v[96:99]\n"
"s_add_u32 s84,s14,0\n"
"s_add_u32 m0,s63,40960\n"
TR "a[202:203],v1 offset:16896\n"
MF "v[104:107],a[212:215],a[184:187],v[104:107]\n"
LD "v4,s[24:27],s84 offen lds\n"
MF "v[104:107],a[208:211],a[144:147],v[104:107]\n"
TR "a[204:205],v1 offset:20480\n"
MF "v[112:115],a[208:211],a[148:151],v[112:115]\n"
TR "a[206:207],v1 offset:20992\n"
MF "v[112:115],a[212:215],a[188:191],v[112:115]\n"
MF "v[120:123],a[212:215],a[192:195],v[120:123]\n"
"s_add_u32 s84,s14,9216\n"
MF "v[120:123],a[208:211],a[152:155],v[120:123]\n"
LD "v5,s[24:27],s84 offen offset:1024 lds\n"
MF "v[128:131],a[208:211],a[156:159],v[128:131]\n"
MF "v[128:131],a[212:215],a[196:199],v[128:131]\n"
"s_waitcnt lgkmcnt(4)\n"
MF "v[132:135],a[220:223],a[196:199],v[132:135]\n"
TR "a[208:209],v1 offset:16392\n"
MF "v[132:135],a[216:219],a[156:159],v[132:135]\n"
TR "a[210:211],v1 offset:16904\n"
MF "v[124:127],a[216:219],a[152:155],v[124:127]\n"
MF "v[124:127],a[220:223],a[192:195],v[124:127]\n"
TR "a[212:213],v1 offset:20488\n"
MF "v[116:119],a[220:223],a[188:191],v[116:119]\n"
TR "a[214:215],v1 offset:21000\n"
MF "v[116:119],a[216:219],a[148:151],v[116:119]\n"
MF "v[108:111],a[216:219],a[144:147],v[108:111]\n"
MF "v[108:111],a[220:223],a[184:187],v[108:111]\n"
MF "v[100:103],a[220:223],a[180:183],v[100:103]\n"
"s_waitcnt vmcnt(25) lgkmcnt(0)\n"
MF "v[100:103],a[216:219],a[140:143],v[100:103]\n"
"s_barrier\n"
MF "v[136:139],a[200:203],a[140:143],v[136:139]\n"
TR "a[216:217],v0 offset:32768\n"
MF "v[136:139],a[204:207],a[180:183],v[136:139]\n"
"s_add_u32 m0,s62,65536\n"
TR "a[218:219],v0 offset:33280\n"
MF "v[144:147],a[204:207],a[184:187],v[144:147]\n"
"s_add_u32 s82,s66,5505152\n"
"ds_read_b128 a[60:63],v2\n"
MF "v[144:147],a[200:203],a[144:147],v[144:147]\n"
"s_sub_u32 s15,s82,7864320\n"
TR "a[220:221],v0 offset:36864\n"
MF "v[152:155],a[200:203],a[148:151],v[152:155]\n"
"s_min_u32 s82,s82,s15\n"
TR "a[222:223],v0 offset:37376\n"
MF "v[152:155],a[204:207],a[188:191],v[152:155]\n"
LD "v6,s[28:31],s82 offen lds\n"
MF "v[160:163],a[204:207],a[192:195],v[160:163]\n"
"ds_read_b128 a[100:103],v3\n"
MF "v[160:163],a[200:203],a[152:155],v[160:163]\n"
MF "v[168:171],a[200:203],a[156:159],v[168:171]\n"
MF "v[168:171],a[204:207],a[196:199],v[168:171]\n"
LD "v11,s[28:31],s82 offen offset:1024 lds\n"
MF "v[172:175],a[212:215],a[196:199],v[172:175]\n"
TR "a[200:201],v0 offset:32776\n"
MF "v[172:175],a[208:211],a[156:159],v[172:175]\n"
TR "a[202:203],v0 offset:33288\n"
MF "v[164:167],a[208:211],a[152:155],v[164:167]\n"
"ds_read_b128 a[64:67],v2 offset:2048\n"
MF "v[164:167],a[212:215],a[192:195],v[164:167]\n"
TR "a[204:205],v0 offset:36872\n"
MF "v[156:159],a[212:215],a[188:191],v[156:159]\n"
TR "a[206:207],v0 offset:37384\n"
MF "v[156:159],a[208:211],a[148:151],v[156:159]\n"
LD "v12,s[28:31],s82 offen offset:2048 lds\n"
MF "v[148:151],a[208:211],a[144:147],v[148:151]\n"
"ds_read_b128 a[104:107],v3 offset:2048\n"
MF "v[148:151],a[212:215],a[184:187],v[148:151]\n"
MF "v[140:143],a[212:215],a[180:183],v[140:143]\n"
MF "v[140:143],a[208:211],a[140:143],v[140:143]\n"
"s_waitcnt lgkmcnt(7)\n"
LD "v13,s[28:31],s82 offen offset:3072 lds\n"
MF "v[176:179],a[216:219],a[140:143],v[176:179]\n"
TR "a[208:209],v1 offset:32768\n"
MF "v[176:179],a[220:223],a[180:183],v[176:179]\n"
TR "a[210:211],v1 offset:33280\n"
MF "v[184:187],a[220:223],a[184:187],v[184:187]\n"
"ds_read_b128 a[68:71],v2 offset:4096\n"
MF "v[184:187],a[216:219],a[144:147],v[184:187]\n"
"s_add_u32 s84,s14,20480\n"
TR "a[212:213],v1 offset:36864\n"
MF "v[192:195],a[216:219],a[148:151],v[192:195]\n"
"s_add_u32 m0,s63,43008\n"
TR "a[214:215],v1 offset:37376\n"
MF "v[192:195],a[220:223],a[188:191],v[192:195]\n"
LD "v4,s[24:27],s84 offen lds\n"
MF "v[200:203],a[220:223],a[192:195],v[200:203]\n"
"ds_read_b128 a[108:111],v3 offset:4096\n"
MF "v[200:203],a[216:219],a[152:155],v[200:203]\n"
MF "v[208:211],a[216:219],a[156:159],v[208:211]\n"
"s_add_u32 s84,s14,29696\n"
MF "v[208:211],a[220:223],a[196:199],v[208:211]\n"
"s_waitcnt lgkmcnt(7)\n"
LD "v5,s[24:27],s84 offen offset:1024 lds\n"
MF "v[212:215],a[204:207],a[196:199],v[212:215]\n"
TR "a[216:217],v1 offset:32776\n"
MF "v[212:215],a[200:203],a[156:159],v[212:215]\n"
TR "a[218:219],v1 offset:33288\n"
MF "v[204:207],a[200:203],a[152:155],v[204:207]\n"
"ds_read_b128 a[72:75],v2 offset:6144\n"
MF "v[204:207],a[204:207],a[192:195],v[204:207]\n"
TR "a[220:221],v1 offset:36872\n"
MF "v[196:199],a[204:207],a[188:191],v[196:199]\n"
TR "a[222:223],v1 offset:37384\n"
MF "v[196:199],a[200:203],a[148:151],v[196:199]\n"
MF "v[188:191],a[200:203],a[144:147],v[188:191]\n"
"v_add_u32 v0,0xffff4000,v0\n"
MF "v[188:191],a[204:207],a[184:187],v[188:191]\n"
"v_add_u32 v1,0xffff4000,v1\n"
MF "v[180:183],a[204:207],a[180:183],v[180:183]\n"
"s_waitcnt vmcnt(26) lgkmcnt(0)\n"
MF "v[180:183],a[200:203],a[140:143],v[180:183]\n"
"s_barrier\n"
MF "v[216:219],a[208:211],a[140:143],v[216:219]\n"
TR "a[200:201],v0\n"
MF "v[216:219],a[212:215],a[180:183],v[216:219]\n"
"s_add_u32 m0,s62,81920\n"
TR "a[202:203],v0 offset:512\n"
MF "v[224:227],a[212:215],a[184:187],v[224:227]\n"
"s_add_u32 s82,s66,5505280\n"
"ds_read_b128 a[112:115],v3 offset:6144\n"
MF "v[224:227],a[208:211],a[144:147],v[224:227]\n"
"s_sub_u32 s15,s82,7864320\n"
TR "a[204:205],v0 offset:4096\n"
MF "v[232:235],a[208:211],a[148:151],v[232:235]\n"
"s_min_u32 s82,s82,s15\n"
TR "a[206:207],v0 offset:4608\n"
MF "v[232:235],a[212:215],a[188:191],v[232:235]\n"
LD "v6,s[28:31],s82 offen lds\n"
MF "v[240:243],a[212:215],a[192:195],v[240:243]\n"
"ds_read_b128 a[76:79],v2 offset:8192\n"
MF "v[240:243],a[208:211],a[152:155],v[240:243]\n"
"ds_read_b128 a[116:119],v3 offset:8192\n"
MF "v[248:251],a[208:211],a[156:159],v[248:251]\n"
MF "v[248:251],a[212:215],a[196:199],v[248:251]\n"
LD "v11,s[28:31],s82 offen offset:1024 lds\n"
MF "v[252:255],a[220:223],a[196:199],v[252:255]\n"
TR "a[208:209],v0 offset:8\n"
MF "v[252:255],a[216:219],a[156:159],v[252:255]\n"
TR "a[210:211],v0 offset:520\n"
MF "v[244:247],a[216:219],a[152:155],v[244:247]\n"
MF "v[244:247],a[220:223],a[192:195],v[244:247]\n"
TR "a[212:213],v0 offset:4104\n"
MF "v[236:239],a[220:223],a[188:191],v[236:239]\n"
TR "a[214:215],v0 offset:4616\n"
MF "v[236:239],a[216:219],a[148:151],v[236:239]\n"
LD "v12,s[28:31],s82 offen offset:2048 lds\n"
MF "v[228:231],a[216:219],a[144:147],v[228:231]\n"
MF "v[228:231],a[220:223],a[184:187],v[228:231]\n"
MF "v[220:223],a[220:223],a[180:183],v[220:223]\n"
MF "v[220:223],a[216:219],a[140:143],v[220:223]\n"
"s_waitcnt lgkmcnt(4)\n"
LD "v13,s[28:31],s82 offen offset:3072 lds\n"
MF "v[16:19],a[200:203],a[60:63],v[16:19]\n"
TR "a[216:217],v1\n"
MF "v[16:19],a[204:207],a[100:103],v[16:19]\n"
TR "a[218:219],v1 offset:512\n"
MF "v[24:27],a[204:207],a[104:107],v[24:27]\n"
MF "v[24:27],a[200:203],a[64:67],v[24:27]\n"
"s_add_u32 s84,s14,40960\n"
TR "a[220:221],v1 offset:4096\n"
MF "v[32:35],a[200:203],a[68:71],v[32:35]\n"
"s_add_u32 m0,s63,45056\n"
TR "a[222:223],v1 offset:4608\n"
MF "v[32:35],a[204:207],a[108:111],v[32:35]\n"
LD "v4,s[24:27],s84 offen lds\n"
MF "v[40:43],a[204:207],a[112:115],v[40:43]\n"
MF "v[40:43],a[200:203],a[72:75],v[40:43]\n"
MF "v[48:51],a[200:203],a[76:79],v[48:51]\n"
MF "v[48:51],a[204:207],a[116:119],v[48:51]\n"
"s_waitcnt lgkmcnt(4)\n"
MF "v[52:55],a[212:215],a[116:119],v[52:55]\n"
TR "a[200:201],v1 offset:8\n"
MF "v[52:55],a[208:211],a[76:79],v[52:55]\n"
TR "a[202:203],v1 offset:520\n"
MF "v[44:47],a[208:211],a[72:75],v[44:47]\n"
MF "v[44:47],a[212:215],a[112:115],v[44:47]\n"
TR "a[204:205],v1 offset:4104\n"
MF "v[36:39],a[212:215],a[108:111],v[36:39]\n"
TR "a[206:207],v1 offset:4616\n"
MF "v[36:39],a[208:211],a[68:71],v[36:39]\n"
MF "v[28:31],a[208:211],a[64:67],v[28:31]\n"
MF "v[28:31],a[212:215],a[104:107],v[28:31]\n"
MF "v[20:23],a[212:215],a[100:103],v[20:23]\n"
"s_waitcnt vmcnt(25) lgkmcnt(0)\n"
MF "v[20:23],a[208:211],a[60:63],v[20:23]\n"
"s_barrier\n"
MF "v[56:59],a[216:219],a[60:63],v[56:59]\n"
"s_add_u32 m0,s62,0\n"
"s_add_u32 s82,s66,6291712\n"
TR "a[208:209],v0 offset:16384\n"
MF "v[56:59],a[220:223],a[100:103],v[56:59]\n"
"s_sub_u32 s15,s82,7864320\n"
"s_min_u32 s82,s82,s15\n"
TR "a[210:211],v0 offset:16896\n"
MF "v[64:67],a[220:223],a[104:107],v[64:67]\n"
LD "v6,s[28:31],s82 offen lds\n"
MF "v[64:67],a[216:219],a[64:67],v[64:67]\n"
TR "a[212:213],v0 offset:20480\n"
MF "v[72:75],a[216:219],a[68:71],v[72:75]\n"
TR "a[214:215],v0 offset:20992\n"
MF "v[72:75],a[220:223],a[108:111],v[72:75]\n"
"buffer_load_dword a249,v15,s[48:51],s59 offen\n"
MF "v[80:83],a[220:223],a[112:115],v[80:83]\n"
MF "v[80:83],a[216:219],a[72:75],v[80:83]\n"
LD "v11,s[28:31],s82 offen offset:1024 lds\n"
MF "v[88:91],a[216:219],a[76:79],v[88:91]\n"
MF "v[88:91],a[220:223],a[116:119],v[88:91]\n"
MF "v[92:95],a[204:207],a[116:119],v[92:95]\n"
TR "a[216:217],v0 offset:16392\n"
MF "v[92:95],a[200:203],a[76:79],v[92:95]\n"
TR "a[218:219],v0 offset:16904\n"
MF "v[84:87],a[200:203],a[72:75],v[84:87]\n"
LD "v12,s[28:31],s82 offen offset:2048 lds\n"
MF "v[84:87],a[204:207],a[112:115],v[84:87]\n"
TR "a[220:221],v0 offset:20488\n"
MF "v[76:79],a[204:207],a[108:111],v[76:79]\n"
TR "a[222:223],v0 offset:21000\n"
MF "v[76:79],a[200:203],a[68:71],v[76:79]\n"
MF "v[68:71],a[200:203],a[64:67],v[68:71]\n"
MF "v[68:71],a[204:207],a[104:107],v[68:71]\n"
LD "v13,s[28:31],s82 offen offset:3072 lds\n"
MF "v[60:63],a[204:207],a[100:103],v[60:63]\n"
MF "v[60:63],a[200:203],a[60:63],v[60:63]\n"
"s_add_u32 s14,s81,1152\n"
"s_sub_u32 s15,s14,1280\n"
"s_waitcnt lgkmcnt(4)\n"
MF "v[96:99],a[208:211],a[60:63],v[96:99]\n"
"s_min_u32 s14,s14,s15\n"
"s_add_u32 s14,s14,s68\n"
TR "a[200:201],v1 offset:16384\n"
MF "v[96:99],a[212:215],a[100:103],v[96:99]\n"
"s_add_u32 s84,s14,0\n"
"s_add_u32 m0,s63,0\n"
TR "a[202:203],v1 offset:16896\n"
MF "v[104:107],a[212:215],a[104:107],v[104:107]\n"
LD "v4,s[24:27],s84 offen lds\n"
MF "v[104:107],a[208:211],a[64:67],v[104:107]\n"
TR "a[204:205],v1 offset:20480\n"
MF "v[112:115],a[208:211],a[68:71],v[112:115]\n"
TR "a[206:207],v1 offset:20992\n"
MF "v[112:115],a[212:215],a[108:111],v[112:115]\n"
MF "v[120:123],a[212:215],a[112:115],v[120:123]\n"
"s_add_u32 s84,s14,9216\n"
MF "v[120:123],a[208:211],a[72:75],v[120:123]\n"
LD "v5,s[24:27],s84 offen offset:1024 lds\n"
MF "v[128:131],a[208:211],a[76:79],v[128:131]\n"
MF "v[128:131],a[212:215],a[116:119],v[128:131]\n"
"s_waitcnt lgkmcnt(4)\n"
MF "v[132:135],a[220:223],a[116:119],v[132:135]\n"
TR "a[208:209],v1 offset:16392\n"
MF "v[132:135],a[216:219],a[76:79],v[132:135]\n"
"s_add_u32 s84,s14,18432\n"
TR "a[210:211],v1 offset:16904\n"
MF "v[124:127],a[216:219],a[72:75],v[124:127]\n"
LD "v4,s[24:27],s84 offen offset:2048 lds\n"
MF "v[124:127],a[220:223],a[112:115],v[124:127]\n"
TR "a[212:213],v1 offset:20488\n"
MF "v[116:119],a[220:223],a[108:111],v[116:119]\n"
TR "a[214:215],v1 offset:21000\n"
MF "v[116:119],a[216:219],a[68:71],v[116:119]\n"
MF "v[108:111],a[216:219],a[64:67],v[108:111]\n"
MF "v[108:111],a[220:223],a[104:107],v[108:111]\n"
MF "v[100:103],a[220:223],a[100:103],v[100:103]\n"
"s_waitcnt vmcnt(26) lgkmcnt(0)\n"
MF "v[100:103],a[216:219],a[60:63],v[100:103]\n"
"s_barrier\n"
MF "v[136:139],a[200:203],a[60:63],v[136:139]\n"
TR "a[216:217],v0 offset:32768\n"
MF "v[136:139],a[204:207],a[100:103],v[136:139]\n"
"s_add_u32 m0,s62,16384\n"
TR "a[218:219],v0 offset:33280\n"
MF "v[144:147],a[204:207],a[104:107],v[144:147]\n"
"s_add_u32 s82,s66,7078144\n"
"ds_read_b128 a[140:143],v2 offset:20480\n"
MF "v[144:147],a[200:203],a[64:67],v[144:147]\n"
"s_sub_u32 s15,s82,7864320\n"
TR "a[220:221],v0 offset:36864\n"
MF "v[152:155],a[200:203],a[68:71],v[152:155]\n"
"s_min_u32 s82,s82,s15\n"
TR "a[222:223],v0 offset:37376\n"
MF "v[152:155],a[204:207],a[108:111],v[152:155]\n"
LD "v6,s[28:31],s82 offen lds\n"
MF "v[160:163],a[204:207],a[112:115],v[160:163]\n"
"ds_read_b128 a[180:183],v3 offset:20480\n"
MF "v[160:163],a[200:203],a[72:75],v[160:163]\n"
MF "v[168:171],a[200:203],a[76:79],v[168:171]\n"
MF "v[168:171],a[204:207],a[116:119],v[168:171]\n"
LD "v11,s[28:31],s82 offen offset:1024 lds\n"
MF "v[172:175],a[212:215],a[116:119],v[172:175]\n"
TR "a[200:201],v0 offset:32776\n"
MF "v[172:175],a[208:211],a[76:79],v[172:175]\n"
TR "a[202:203],v0 offset:33288\n"
MF "v[164:167],a[208:211],a[72:75],v[164:167]\n"
"ds_read_b128 a[144:147],v2 offset:22528\n"
MF "v[164:167],a[212:215],a[112:115],v[164:167]\n"
TR "a[204:205],v0 offset:36872\n"
MF "v[156:159],a[212:215],a[108:111],v[156:159]\n"
TR "a[206:207],v0 offset:37384\n"
MF "v[156:159],a[208:211],a[68:71],v[156:159]\n"
LD "v12,s[28:31],s82 offen offset:2048 lds\n"
MF "v[148:151],a[208:211],a[64:67],v[148:151]\n"
"ds_read_b128 a[184:187],v3 offset:22528\n"
MF "v[148:151],a[212:215],a[104:107],v[148:151]\n"
MF "v[140:143],a[212:215],a[100:103],v[140:143]\n"
MF "v[140:143],a[208:211],a[60:63],v[140:143]\n"
"s_waitcnt lgkmcnt(7)\n"
LD "v13,s[28:31],s82 offen offset:3072 lds\n"
MF "v[176:179],a[216:219],a[60:63],v[176:179]\n"
TR "a[208:209],v1 offset:32768\n"
MF "v[176:179],a[220:223],a[100:103],v[176:179]\n"
TR "a[210:211],v1 offset:33280\n"
MF "v[184:187],a[220:223],a[104:107],v[184:187]\n"
"ds_read_b128 a[148:151],v2 offset:24576\n"
MF "v[184:187],a[216:219],a[64:67],v[184:187]\n"
"s_add_u32 s84,s14,30720\n"
TR "a[212:213],v1 offset:36864\n"
MF "v[192:195],a[216:219],a[68:71],v[192:195]\n"
"s_add_u32 m0,s63,3072\n"
TR "a[214:215],v1 offset:37376\n"
MF "v[192:195],a[220:223],a[108:111],v[192:195]\n"
LD "v5,s[24:27],s84 offen lds\n"
MF "v[200:203],a[220:223],a[112:115],v[200:203]\n"
"ds_read_b128 a[188:191],v3 offset:24576\n"
MF "v[200:203],a[216:219],a[72:75],v[200:203]\n"
MF "v[208:211],a[216:219],a[76:79],v[208:211]\n"
"s_add_u32 s84,s14,39936\n"
MF "v[208:211],a[220:223],a[116:119],v[208:211]\n"
"s_waitcnt lgkmcnt(7)\n"
LD "v4,s[24:27],s84 offen offset:1024 lds\n"
MF "v[212:215],a[204:207],a[116:119],v[212:215]\n"
TR "a[216:217],v1 offset:32776\n"
MF "v[212:215],a[200:203],a[76:79],v[212:215]\n"
TR "a[218:219],v1 offset:33288\n"
MF "v[204:207],a[200:203],a[72:75],v[204:207]\n"
"ds_read_b128 a[152:155],v2 offset:26624\n"
MF "v[204:207],a[204:207],a[112:115],v[204:207]\n"
TR "a[220:221],v1 offset:36872\n"
MF "v[196:199],a[204:207],a[108:111],v[196:199]\n"
TR "a[222:223],v1 offset:37384\n"
MF "v[196:199],a[200:203],a[68:71],v[196:199]\n"
MF "v[188:191],a[200:203],a[64:67],v[188:191]\n"
"v_add_u32 v0,0xc000,v0\n"
MF "v[188:191],a[204:207],a[104:107],v[188:191]\n"
"v_add_u32 v1,0xc000,v1\n"
MF "v[180:183],a[204:207],a[100:103],v[180:183]\n"
"s_waitcnt vmcnt(27) lgkmcnt(0)\n"
"s_branch .Lqj\n"
".Lpgq0_c:\n"
"s_cbranch_execz .Lpgqy_c\n"
".Lpgq0_f:\n"
"s_cbranch_execz .Lpg12f\n"
".Lq0:\n"
MF "v[16:19],a[200:203],a[60:63],0\n"
"s_add_u32 m0,s62,49152\n"
"s_add_u32 s82,s66,786432\n"
TR "a[216:217],v1\n"
MF "v[16:19],a[204:207],a[100:103],v[16:19]\n"
"s_sub_u32 s15,s82,7864320\n"
"s_min_u32 s82,s82,s15\n"
TR "a[218:219],v1 offset:512\n"
MF "v[24:27],a[204:207],a[104:107],0\n"
LD "v6,s[28:31],s82 offen lds\n"
MF "v[24:27],a[200:203],a[64:67],v[24:27]\n"
TR "a[220:221],v1 offset:4096\n"
MF "v[32:35],a[200:203],a[68:71],0\n"
TR "a[222:223],v1 offset:4608\n"
MF "v[32:35],a[204:207],a[108:111],v[32:35]\n"
"v_max3_f32 v220,|v16|,|v17|,|v18|\n"
"v_max_f32_e64 v220,v220,|v19|\n"
MF "v[40:43],a[204:207],a[112:115],0\n"
"v_readlane_b32 s12,v220,3\n"
"v_readlane_b32 s6,v220,22\n"
MF "v[40:43],a[200:203],a[72:75],v[40:43]\n"
"s_min_u32 s12,s12,s6\n"
"v_readlane_b32 s7,v220,41\n"
"v_readlane_b32 s8,v220,60\n"
MF "v[48:51],a[200:203],a[76:79],0\n"
"s_min_u32 s7,s7,s8\n"
MF "v[48:51],a[204:207],a[116:119],v[48:51]\n"
LD "v11,s[28:31],s82 offen offset:1024 lds\n"
MF "v[52:55],a[212:215],a[116:119],0\n"
"s_min_u32 s12,s12,s7\n"
TR "a[200:201],v1 offset:8\n"
MF "v[52:55],a[208:211],a[76:79],v[52:55]\n"
"s_and_b32 s12,s12,0x7f800000\n"
TR "a[202:203],v1 offset:520\n"
MF "v[44:47],a[208:211],a[72:75],0\n"
"s_sub_u32 s12,s12,0x14000000\n"
MF "v[44:47],a[212:215],a[112:115],v[44:47]\n"
"s_cmp_lt_u32 s12,0x50000000\n"
"s_cselect_b32 s12,s12,0xe8400000\n"
TR "a[204:205],v1 offset:4104\n"
MF "v[36:39],a[212:215],a[108:111],0\n"
"s_add_u32 s12,s12,0x17c00000\n"
TR "a[206:207],v1 offset:4616\n"
MF "v[36:39],a[208:211],a[68:71],v[36:39]\n"
"v_mov_b32 v220,s12\n"
"v_mov_b32 v221,s12\n"
MF "v[28:31],a[208:211],a[64:67],0\n"
"v_mov_b32 v222,s12\n"
"v_mov_b32 v223,s12\n"
MF "v[28:31],a[212:215],a[104:107],v[28:31]\n"
LD "v12,s[28:31],s82 offen offset:2048 lds\n"
MF "v[20:23],a[212:215],a[100:103],0\n"
"s_waitcnt vmcnt(7) lgkmcnt(0)\n"
MF "v[20:23],a[208:211],a[60:63],v[20:23]\n"
"s_barrier\n"
MF "v[56:59],a[216:219],a[60:63],v[220:223]\n"
TR "a[208:209],v0 offset:16384\n"
MF "v[56:59],a[220:223],a[100:103],v[56:59]\n"
TR "a[210:211],v0 offset:16896\n"
MF "v[64:67],a[220:223],a[104:107],v[220:223]\n"
LD "v13,s[28:31],s82 offen offset:3072 lds\n"
MF "v[64:67],a[216:219],a[64:67],v[64:67]\n"
"s_add_u32 m0,s62,65536\n"
TR "a[212:213],v0 offset:20480\n"
MF "v[72:75],a[216:219],a[68:71],v[220:223]\n"
"s_add_u32 s82,s66,786560\n"
TR "a[214:215],v0 offset:20992\n"
MF "v[72:75],a[220:223],a[108:111],v[72:75]\n"
"s_sub_u32 s15,s82,7864320\n"
MF "v[80:83],a[220:223],a[112:115],v[220:223]\n"
"s_min_u32 s82,s82,s15\n"
MF "v[80:83],a[216:219],a[72:75],v[80:83]\n"
LD "v6,s[28:31],s82 offen lds\n"
MF "v[88:91],a[216:219],a[76:79],v[220:223]\n"
MF "v[88:91],a[220:223],a[116:119],v[88:91]\n"
MF "v[92:95],a[204:207],a[116:119],v[220:223]\n"
TR "a[216:217],v0 offset:16392\n"
MF "v[92:95],a[200:203],a[76:79],v[92:95]\n"
TR "a[218:219],v0 offset:16904\n"
MF "v[84:87],a[200:203],a[72:75],v[220:223]\n"
LD "v11,s[28:31],s82 offen offset:1024 lds\n"
MF "v[84:87],a[204:207],a[112:115],v[84:87]\n"
TR "a[220:221],v0 offset:20488\n"
MF "v[76:79],a[204:207],a[108:111],v[220:223]\n"
TR "a[222:223],v0 offset:21000\n"
MF "v[76:79],a[200:203],a[68:71],v[76:79]\n"
MF "v[68:71],a[200:203],a[64:67],v[220:223]\n"
MF "v[68:71],a[204:207],a[104:107],v[68:71]\n"
LD "v12,s[28:31],s82 offen offset:2048 lds\n"
MF "v[60:63],a[204:207],a[100:103],v[220:223]\n"
MF "v[60:63],a[200:203],a[60:63],v[60:63]\n"
"s_waitcnt lgkmcnt(4)\n"
MF "v[96:99],a[208:211],a[60:63],v[220:223]\n"
TR "a[200:201],v1 offset:16384\n"
MF "v[96:99],a[212:215],a[100:103],v[96:99]\n"
TR "a[202:203],v1 offset:16896\n"
MF "v[104:107],a[212:215],a[104:107],v[220:223]\n"
LD "v13,s[28:31],s82 offen offset:3072 lds\n"
MF "v[104:107],a[208:211],a[64:67],v[104:107]\n"
"s_add_u32 m0,s62,81920\n"
TR "a[204:205],v1 offset:20480\n"
MF "v[112:115],a[208:211],a[68:71],v[220:223]\n"
"s_add_u32 s82,s66,786688\n"
TR "a[206:207],v1 offset:20992\n"
MF "v[112:115],a[212:215],a[108:111],v[112:115]\n"
"s_sub_u32 s15,s82,7864320\n"
MF "v[120:123],a[212:215],a[112:115],v[220:223]\n"
"s_min_u32 s82,s82,s15\n"
MF "v[120:123],a[208:211],a[72:75],v[120:123]\n"
LD "v6,s[28:31],s82 offen lds\n"
MF "v[128:131],a[208:211],a[76:79],v[220:223]\n"
MF "v[128:131],a[212:215],a[116:119],v[128:131]\n"
"s_waitcnt lgkmcnt(4)\n"
MF "v[132:135],a[220:223],a[116:119],v[220:223]\n"
TR "a[208:209],v1 offset:16392\n"
MF "v[132:135],a[216:219],a[76:79],v[132:135]\n"
TR "a[210:211],v1 offset:16904\n"
MF "v[124:127],a[216:219],a[72:75],v[220:223]\n"
LD "v11,s[28:31],s82 offen offset:1024 lds\n"
MF "v[124:127],a[220:223],a[112:115],v[124:127]\n"
TR "a[212:213],v1 offset:20488\n"
MF "v[116:119],a[220:223],a[108:111],v[220:223]\n"
TR "a[214:215],v1 offset:21000\n"
MF "v[116:119],a[216:219],a[68:71],v[116:119]\n"
MF "v[108:111],a[216:219],a[64:67],v[220:223]\n"
MF "v[108:111],a[220:223],a[104:107],v[108:111]\n"
LD "v12,s[28:31],s82 offen offset:2048 lds\n"
MF "v[100:103],a[220:223],a[100:103],v[220:223]\n"
"s_waitcnt vmcnt(11) lgkmcnt(0)\n"
MF "v[100:103],a[216:219],a[60:63],v[100:103]\n"
"s_barrier\n"
MF "v[136:139],a[200:203],a[60:63],v[220:223]\n"
TR "a[216:217],v0 offset:32768\n"
MF "v[136:139],a[204:207],a[100:103],v[136:139]\n"
TR "a[218:219],v0 offset:33280\n"
MF "v[144:147],a[204:207],a[104:107],v[220:223]\n"
"ds_read_b128 a[140:143],v2 offset:20480\n"
MF "v[144:147],a[200:203],a[64:67],v[144:147]\n"
TR "a[220:221],v0 offset:36864\n"
MF "v[152:155],a[200:203],a[68:71],v[220:223]\n"
TR "a[222:223],v0 offset:37376\n"
MF "v[152:155],a[204:207],a[108:111],v[152:155]\n"
LD "v13,s[28:31],s82 offen offset:3072 lds\n"
MF "v[160:163],a[204:207],a[112:115],v[220:223]\n"
"ds_read_b128 a[180:183],v3 offset:20480\n"
MF "v[160:163],a[200:203],a[72:75],v[160:163]\n"
"s_add_u32 m0,s62,0\n"
MF "v[168:171],a[200:203],a[76:79],v[220:223]\n"
"s_add_u32 s82,s66,1572864\n"
"s_sub_u32 s15,s82,7864320\n"
"s_min_u32 s82,s82,s15\n"
MF "v[168:171],a[204:207],a[116:119],v[168:171]\n"
LD "v6,s[28:31],s82 offen lds\n"
MF "v[172:175],a[212:215],a[116:119],v[220:223]\n"
TR "a[200:201],v0 offset:32776\n"
MF "v[172:175],a[208:211],a[76:79],v[172:175]\n"
TR "a[202:203],v0 offset:33288\n"
MF "v[164:167],a[208:211],a[72:75],v[220:223]\n"
"ds_read_b128 a[144:147],v2 offset:22528\n"
MF "v[164:167],a[212:215],a[112:115],v[164:167]\n"
TR "a[204:205],v0 offset:36872\n"
MF "v[156:159],a[212:215],a[108:111],v[220:223]\n"
TR "a[206:207],v0 offset:37384\n"
MF "v[156:159],a[208:211],a[68:71],v[156:159]\n"
LD "v11,s[28:31],s82 offen offset:1024 lds\n"
MF "v[148:151],a[208:211],a[64:67],v[220:223]\n"
"ds_read_b128 a[184:187],v3 offset:22528\n"
MF "v[148:151],a[212:215],a[104:107],v[148:151]\n"
MF "v[140:143],a[212:215],a[100:103],v[220:223]\n"
MF "v[140:143],a[208:211],a[60:63],v[140:143]\n"
"s_waitcnt lgkmcnt(7)\n"
LD "v12,s[28:31],s82 offen offset:2048 lds\n"
MF "v[176:179],a[216:219],a[60:63],v[220:223]\n"
TR "a[208:209],v1 offset:32768\n"
MF "v[176:179],a[220:223],a[100:103],v[176:179]\n"
".Lpg4c:\n"
"s_cbranch_execz .Lpg13c\n"
".Lpg4f:\n"
"s_cbranch_execz .LpgattB_f\n"
TR "a[210:211],v1 offset:33280\n"
MF "v[184:187],a[220:223],a[104:107],v[220:223]\n"
"ds_read_b128 a[148:151],v2 offset:24576\n"
MF "v[184:187],a[216:219],a[64:67],v[184:187]\n"
TR "a[212:213],v1 offset:36864\n"
MF "v[192:195],a[216:219],a[68:71],v[220:223]\n"
TR "a[214:215],v1 offset:37376\n"
MF "v[192:195],a[220:223],a[108:111],v[192:195]\n"
LD "v13,s[28:31],s82 offen offset:3072 lds\n"
MF "v[200:203],a[220:223],a[112:115],v[220:223]\n"
"ds_read_b128 a[188:191],v3 offset:24576\n"
MF "v[200:203],a[216:219],a[72:75],v[200:203]\n"
"s_add_u32 s14,s81,384\n"
"s_sub_u32 s15,s14,1280\n"
"s_min_u32 s14,s14,s15\n"
MF "v[208:211],a[216:219],a[76:79],v[220:223]\n"
"s_add_u32 s14,s14,s68\n"
"s_add_u32 s84,s14,0\n"
"s_add_u32 m0,s63,0\n"
MF "v[208:211],a[220:223],a[116:119],v[208:211]\n"
"s_waitcnt lgkmcnt(7)\n"
LD "v4,s[24:27],s84 offen lds\n"
MF "v[212:215],a[204:207],a[116:119],v[220:223]\n"
TR "a[216:217],v1 offset:32776\n"
MF "v[212:215],a[200:203],a[76:79],v[212:215]\n"
TR "a[218:219],v1 offset:33288\n"
MF "v[204:207],a[200:203],a[72:75],v[220:223]\n"
"ds_read_b128 a[152:155],v2 offset:26624\n"
MF "v[204:207],a[204:207],a[112:115],v[204:207]\n"
TR "a[220:221],v1 offset:36872\n"
MF "v[196:199],a[204:207],a[108:111],v[220:223]\n"
"s_add_u32 s84,s14,9216\n"
TR "a[222:223],v1 offset:37384\n"
MF "v[196:199],a[200:203],a[68:71],v[196:199]\n"
LD "v5,s[24:27],s84 offen offset:1024 lds\n"
MF "v[188:191],a[200:203],a[64:67],v[220:223]\n"
"s_add_u32 m0,s62,16384\n"
"s_add_u32 s82,s66,1572992\n"
"v_add_u32 v0,0xc000,v0\n"
MF "v[188:191],a[204:207],a[104:107],v[188:191]\n"
"s_sub_u32 s15,s82,7864320\n"
"s_min_u32 s82,s82,s15\n"
"v_add_u32 v1,0xc000,v1\n"
MF "v[180:183],a[204:207],a[100:103],v[220:223]\n"
"s_waitcnt vmcnt(14) lgkmcnt(0)\n"
LD "v6,s[28:31],s82 offen lds\n"
MF "v[180:183],a[200:203],a[60:63],v[180:183]\n"
"s_barrier\n"
MF "v[216:219],a[208:211],a[60:63],v[220:223]\n"
TR "a[200:201],v0\n"
MF "v[216:219],a[212:215],a[100:103],v[216:219]\n"
TR "a[202:203],v0 offset:512\n"
MF "v[224:227],a[212:215],a[104:107],v[220:223]\n"
"ds_read_b128 a[192:195],v3 offset:26624\n"
MF "v[224:227],a[208:211],a[64:67],v[224:227]\n"
TR "a[204:205],v0 offset:4096\n"
MF "v[232:235],a[208:211],a[68:71],v[220:223]\n"
TR "a[206:207],v0 offset:4608\n"
MF "v[232:235],a[212:215],a[108:111],v[232:235]\n"
LD "v11,s[28:31],s82 offen offset:1024 lds\n"
MF "v[240:243],a[212:215],a[112:115],v[220:223]\n"
"ds_read_b128 a[156:159],v2 offset:28672\n"
MF "v[240:243],a[208:211],a[72:75],v[240:243]\n"
"ds_read_b128 a[196:199],v3 offset:28672\n"
MF "v[248:251],a[208:211],a[76:79],v[220:223]\n"
MF "v[248:251],a[212:215],a[116:119],v[248:251]\n"
LD "v12,s[28:31],s82 offen offset:2048 lds\n"
MF "v[252:255],a[220:223],a[116:119],v[220:223]\n"
TR "a[208:209],v0 offset:8\n"
MF "v[252:255],a[216:219],a[76:79],v[252:255]\n"
TR "a[210:211],v0 offset:520\n"
MF "v[244:247],a[216:219],a[72:75],v[220:223]\n"
MF "v[244:247],a[220:223],a[112:115],v[244:247]\n"
TR "a[212:213],v0 offset:4104\n"
MF "v[236:239],a[220:223],a[108:111],v[220:223]\n"
TR "a[214:215],v0 offset:4616\n"
MF "v[236:239],a[216:219],a[68:71],v[236:239]\n"
LD "v13,s[28:31],s82 offen offset:3072 lds\n"
MF "v[228:231],a[216:219],a[64:67],v[220:223]\n"
MF "v[228:231],a[220:223],a[104:107],v[228:231]\n"
"s_add_u32 s84,s14,20480\n"
MF "v[220:223],a[220:223],a[100:103],v[220:223]\n"
"s_add_u32 m0,s63,2048\n"
MF "v[220:223],a[216:219],a[60:63],v[220:223]\n"
"s_waitcnt lgkmcnt(4)\n"
LD "v4,s[24:27],s84 offen lds\n"
MF "v[16:19],a[200:203],a[140:143],v[16:19]\n"
TR "a[216:217],v1\n"
MF "v[16:19],a[204:207],a[180:183],v[16:19]\n"
TR "a[218:219],v1 offset:512\n"
MF "v[24:27],a[204:207],a[184:187],v[24:27]\n"
MF "v[24:27],a[200:203],a[144:147],v[24:27]\n"
TR "a[220:221],v1 offset:4096\n"
MF "v[32:35],a[200:203],a[148:151],v[32:35]\n"
"s_add_u32 s84,s14,29696\n"
TR "a[222:223],v1 offset:4608\n"
MF "v[32:35],a[204:207],a[188:191],v[32:35]\n"
LD "v5,s[24:27],s84 offen offset:1024 lds\n"
MF "v[40:43],a[204:207],a[192:195],v[40:43]\n"
MF "v[40:43],a[200:203],a[152:155],v[40:43]\n"
"s_add_u32 m0,s62,32768\n"
MF "v[48:51],a[200:203],a[156:159],v[48:51]\n"
"s_add_u32 s82,s66,1573120\n"
"s_sub_u32 s15,s82,7864320\n"
"s_min_u32 s82,s82,s15\n"
MF "v[48:51],a[204:207],a[196:199],v[48:51]\n"
"s_waitcnt lgkmcnt(4)\n"
LD "v6,s[28:31],s82 offen lds\n"
MF "v[52:55],a[212:215],a[196:199],v[52:55]\n"
TR "a[200:201],v1 offset:8\n"
MF "v[52:55],a[208:211],a[156:159],v[52:55]\n"
TR "a[202:203],v1 offset:520\n"
MF "v[44:47],a[208:211],a[152:155],v[44:47]\n"
MF "v[44:47],a[212:215],a[192:195],v[44:47]\n"
TR "a[204:205],v1 offset:4104\n"
MF "v[36:39],a[212:215],a[188:191],v[36:39]\n"
TR "a[206:207],v1 offset:4616\n"
MF "v[36:39],a[208:211],a[148:151],v[36:39]\n"
LD "v11,s[28:31],s82 offen offset:1024 lds\n"
MF "v[28:31],a[208:211],a[144:147],v[28:31]\n"
MF "v[28:31],a[212:215],a[184:187],v[28:31]\n"
MF "v[20:23],a[212:215],a[180:183],v[20:23]\n"
"s_waitcnt vmcnt(18) lgkmcnt(0)\n"
LD "v12,s[28:31],s82 offen offset:2048 lds\n"
"s_bitcmp0_b32 s23,12\n"
"s_cbranch_scc1 .Lisx2\n"
"s_mov_b64 exec,0\n"
"s_cmp_eq_u32 s56,0\n"
"s_cbranch_scc1 .Lpg11d\n"
"s_cmp_eq_u32 s56,1\n"
"s_cbranch_scc1 .Lpg10d\n"
"s_cmp_eq_u32 s56,2\n"
"s_cbranch_scc1 .Lpgt0kx_d\n"
"s_branch .Lpg8d\n"
".Lisr2:\n"
"s_mov_b64 exec,-1\n"
".Lisx2:\n"
MF "v[20:23],a[208:211],a[140:143],v[20:23]\n"
"s_barrier\n"
MF "v[56:59],a[216:219],a[140:143],v[56:59]\n"
TR "a[208:209],v0 offset:16384\n"
MF "v[56:59],a[220:223],a[180:183],v[56:59]\n"
TR "a[210:211],v0 offset:16896\n"
MF "v[64:67],a[220:223],a[184:187],v[64:67]\n"
LD "v13,s[28:31],s82 offen offset:3072 lds\n"
MF "v[64:67],a[216:219],a[144:147],v[64:67]\n"
TR "a[212:213],v0 offset:20480\n"
MF "v[72:75],a[216:219],a[148:151],v[72:75]\n"
TR "a[214:215],v0 offset:20992\n"
MF "v[72:75],a[220:223],a[188:191],v[72:75]\n"
"s_add_u32 s84,s14,40960\n"
MF "v[80:83],a[220:223],a[192:195],v[80:83]\n"
"s_add_u32 m0,s63,4096\n"
MF "v[80:83],a[216:219],a[152:155],v[80:83]\n"
LD "v4,s[24:27],s84 offen lds\n"
MF "v[88:91],a[216:219],a[156:159],v[88:91]\n"
MF "v[88:91],a[220:223],a[196:199],v[88:91]\n"
"s_sub_u32 s66,s66,s83\n"
MF "v[92:95],a[204:207],a[196:199],v[92:95]\n"
"s_add_u32 m0,s62,49152\n"
"s_add_u32 s82,s66,2359296\n"
TR "a[216:217],v0 offset:16392\n"
MF "v[92:95],a[200:203],a[156:159],v[92:95]\n"
"s_sub_u32 s15,s82,7864320\n"
"s_min_u32 s82,s82,s15\n"
TR "a[218:219],v0 offset:16904\n"
MF "v[84:87],a[200:203],a[152:155],v[84:87]\n"
LD "v6,s[28:31],s82 offen lds\n"
MF "v[84:87],a[204:207],a[192:195],v[84:87]\n"
TR "a[220:221],v0 offset:20488\n"
MF "v[76:79],a[204:207],a[188:191],v[76:79]\n"
TR "a[222:223],v0 offset:21000\n"
MF "v[76:79],a[200:203],a[148:151],v[76:79]\n"
MF "v[68:71],a[200:203],a[144:147],v[68:71]\n"
MF "v[68:71],a[204:207],a[184:187],v[68:71]\n"
LD "v11,s[28:31],s82 offen offset:1024 lds\n"
MF "v[60:63],a[204:207],a[180:183],v[60:63]\n"
MF "v[60:63],a[200:203],a[140:143],v[60:63]\n"
"s_waitcnt lgkmcnt(4)\n"
MF "v[96:99],a[208:211],a[140:143],v[96:99]\n"
TR "a[200:201],v1 offset:16384\n"
MF "v[96:99],a[212:215],a[180:183],v[96:99]\n"
TR "a[202:203],v1 offset:16896\n"
MF "v[104:107],a[212:215],a[184:187],v[104:107]\n"
LD "v12,s[28:31],s82 offen offset:2048 lds\n"
MF "v[104:107],a[208:211],a[144:147],v[104:107]\n"
TR "a[204:205],v1 offset:20480\n"
MF "v[112:115],a[208:211],a[148:151],v[112:115]\n"
TR "a[206:207],v1 offset:20992\n"
MF "v[112:115],a[212:215],a[188:191],v[112:115]\n"
MF "v[120:123],a[212:215],a[192:195],v[120:123]\n"
MF "v[120:123],a[208:211],a[152:155],v[120:123]\n"
LD "v13,s[28:31],s82 offen offset:3072 lds\n"
MF "v[128:131],a[208:211],a[156:159],v[128:131]\n"
MF "v[128:131],a[212:215],a[196:199],v[128:131]\n"
"s_add_u32 s14,s81,512\n"
"s_sub_u32 s15,s14,1280\n"
"s_waitcnt lgkmcnt(4)\n"
MF "v[132:135],a[220:223],a[196:199],v[132:135]\n"
"s_min_u32 s14,s14,s15\n"
"s_add_u32 s14,s14,s68\n"
TR "a[208:209],v1 offset:16392\n"
MF "v[132:135],a[216:219],a[156:159],v[132:135]\n"
"s_add_u32 s84,s14,0\n"
"s_add_u32 m0,s63,20480\n"
TR "a[210:211],v1 offset:16904\n"
MF "v[124:127],a[216:219],a[152:155],v[124:127]\n"
LD "v4,s[24:27],s84 offen lds\n"
MF "v[124:127],a[220:223],a[192:195],v[124:127]\n"
TR "a[212:213],v1 offset:20488\n"
MF "v[116:119],a[220:223],a[188:191],v[116:119]\n"
TR "a[214:215],v1 offset:21000\n"
MF "v[116:119],a[216:219],a[148:151],v[116:119]\n"
MF "v[108:111],a[216:219],a[144:147],v[108:111]\n"
"s_add_u32 s84,s14,9216\n"
MF "v[108:111],a[220:223],a[184:187],v[108:111]\n"
LD "v5,s[24:27],s84 offen offset:1024 lds\n"
MF "v[100:103],a[220:223],a[180:183],v[100:103]\n"
"s_waitcnt vmcnt(23) lgkmcnt(0)\n"
MF "v[100:103],a[216:219],a[140:143],v[100:103]\n"
"s_barrier\n"
MF "v[136:139],a[200:203],a[140:143],v[136:139]\n"
TR "a[216:217],v0 offset:32768\n"
MF "v[136:139],a[204:207],a[180:183],v[136:139]\n"
"s_add_u32 m0,s62,65536\n"
TR "a[218:219],v0 offset:33280\n"
MF "v[144:147],a[204:207],a[184:187],v[144:147]\n"
"s_add_u32 s82,s66,2359424\n"
"ds_read_b128 a[60:63],v2 offset:40960\n"
MF "v[144:147],a[200:203],a[144:147],v[144:147]\n"
"s_sub_u32 s15,s82,7864320\n"
TR "a[220:221],v0 offset:36864\n"
MF "v[152:155],a[200:203],a[148:151],v[152:155]\n"
"s_min_u32 s82,s82,s15\n"
TR "a[222:223],v0 offset:37376\n"
MF "v[152:155],a[204:207],a[188:191],v[152:155]\n"
LD "v6,s[28:31],s82 offen lds\n"
MF "v[160:163],a[204:207],a[192:195],v[160:163]\n"
"ds_read_b128 a[100:103],v3 offset:40960\n"
MF "v[160:163],a[200:203],a[152:155],v[160:163]\n"
LD "v11,s[28:31],s82 offen offset:1024 lds\n"
MF "v[168:171],a[200:203],a[156:159],v[168:171]\n"
MF "v[168:171],a[204:207],a[196:199],v[168:171]\n"
LD "v12,s[28:31],s82 offen offset:2048 lds\n"
MF "v[172:175],a[212:215],a[196:199],v[172:175]\n"
TR "a[200:201],v0 offset:32776\n"
MF "v[172:175],a[208:211],a[156:159],v[172:175]\n"
TR "a[202:203],v0 offset:33288\n"
MF "v[164:167],a[208:211],a[152:155],v[164:167]\n"
"ds_read_b128 a[64:67],v2 offset:43008\n"
MF "v[164:167],a[212:215],a[192:195],v[164:167]\n"
TR "a[204:205],v0 offset:36872\n"
MF "v[156:159],a[212:215],a[188:191],v[156:159]\n"
TR "a[206:207],v0 offset:37384\n"
MF "v[156:159],a[208:211],a[148:151],v[156:159]\n"
MF "v[148:151],a[208:211],a[144:147],v[148:151]\n"
"ds_read_b128 a[104:107],v3 offset:43008\n"
MF "v[148:151],a[212:215],a[184:187],v[148:151]\n"
MF "v[140:143],a[212:215],a[180:183],v[140:143]\n"
LD "v13,s[28:31],s82 offen offset:3072 lds\n"
MF "v[140:143],a[208:211],a[140:143],v[140:143]\n"
"s_waitcnt lgkmcnt(7)\n"
MF "v[176:179],a[216:219],a[140:143],v[176:179]\n"
TR "a[208:209],v1 offset:32768\n"
MF "v[176:179],a[220:223],a[180:183],v[176:179]\n"
TR "a[210:211],v1 offset:33280\n"
MF "v[184:187],a[220:223],a[184:187],v[184:187]\n"
"ds_read_b128 a[68:71],v2 offset:45056\n"
MF "v[184:187],a[216:219],a[144:147],v[184:187]\n"
"s_add_u32 s84,s14,20480\n"
TR "a[212:213],v1 offset:36864\n"
MF "v[192:195],a[216:219],a[148:151],v[192:195]\n"
"s_add_u32 m0,s63,22528\n"
TR "a[214:215],v1 offset:37376\n"
MF "v[192:195],a[220:223],a[188:191],v[192:195]\n"
LD "v4,s[24:27],s84 offen lds\n"
MF "v[200:203],a[220:223],a[192:195],v[200:203]\n"
"ds_read_b128 a[108:111],v3 offset:45056\n"
MF "v[200:203],a[216:219],a[152:155],v[200:203]\n"
MF "v[208:211],a[216:219],a[156:159],v[208:211]\n"
"s_add_u32 s84,s14,29696\n"
MF "v[208:211],a[220:223],a[196:199],v[208:211]\n"
"s_waitcnt lgkmcnt(7)\n"
LD "v5,s[24:27],s84 offen offset:1024 lds\n"
MF "v[212:215],a[204:207],a[196:199],v[212:215]\n"
TR "a[216:217],v1 offset:32776\n"
MF "v[212:215],a[200:203],a[156:159],v[212:215]\n"
TR "a[218:219],v1 offset:33288\n"
MF "v[204:207],a[200:203],a[152:155],v[204:207]\n"
"ds_read_b128 a[72:75],v2 offset:47104\n"
MF "v[204:207],a[204:207],a[192:195],v[204:207]\n"
TR "a[220:221],v1 offset:36872\n"
MF "v[196:199],a[204:207],a[188:191],v[196:199]\n"
TR "a[222:223],v1 offset:37384\n"
MF "v[196:199],a[200:203],a[148:151],v[196:199]\n"
MF "v[188:191],a[200:203],a[144:147],v[188:191]\n"
"v_add_u32 v0,0xffff4000,v0\n"
MF "v[188:191],a[204:207],a[184:187],v[188:191]\n"
"v_add_u32 v1,0xffff4000,v1\n"
MF "v[180:183],a[204:207],a[180:183],v[180:183]\n"
"s_waitcnt vmcnt(25) lgkmcnt(0)\n"
MF "v[180:183],a[200:203],a[140:143],v[180:183]\n"
"s_barrier\n"
MF "v[216:219],a[208:211],a[140:143],v[216:219]\n"
TR "a[200:201],v0\n"
MF "v[216:219],a[212:215],a[180:183],v[216:219]\n"
"s_add_u32 m0,s62,81920\n"
TR "a[202:203],v0 offset:512\n"
MF "v[224:227],a[212:215],a[184:187],v[224:227]\n"
"s_add_u32 s82,s66,2359552\n"
"ds_read_b128 a[112:115],v3 offset:47104\n"
MF "v[224:227],a[208:211],a[144:147],v[224:227]\n"
"s_sub_u32 s15,s82,7864320\n"
TR "a[204:205],v0 offset:4096\n"
MF "v[232:235],a[208:211],a[148:151],v[232:235]\n"
"s_min_u32 s82,s82,s15\n"
TR "a[206:207],v0 offset:4608\n"
MF "v[232:235],a[212:215],a[188:191],v[232:235]\n"
LD "v6,s[28:31],s82 offen lds\n"
MF "v[240:243],a[212:215],a[192:195],v[240:243]\n"
"ds_read_b128 a[76:79],v2 offset:49152\n"
MF "v[240:243],a[208:211],a[152:155],v[240:243]\n"
"ds_read_b128 a[116:119],v3 offset:49152\n"
MF "v[248:251],a[208:211],a[156:159],v[248:251]\n"
LD "v11,s[28:31],s82 offen offset:1024 lds\n"
MF "v[248:251],a[212:215],a[196:199],v[248:251]\n"
MF "v[252:255],a[220:223],a[196:199],v[252:255]\n"
TR "a[208:209],v0 offset:8\n"
MF "v[252:255],a[216:219],a[156:159],v[252:255]\n"
TR "a[210:211],v0 offset:520\n"
MF "v[244:247],a[216:219],a[152:155],v[244:247]\n"
MF "v[244:247],a[220:223],a[192:195],v[244:247]\n"
TR "a[212:213],v0 offset:4104\n"
MF "v[236:239],a[220:223],a[188:191],v[236:239]\n"
TR "a[214:215],v0 offset:4616\n"
MF "v[236:239],a[216:219],a[148:151],v[236:239]\n"
LD "v12,s[28:31],s82 offen offset:2048 lds\n"
MF "v[228:231],a[216:219],a[144:147],v[228:231]\n"
MF "v[228:231],a[220:223],a[184:187],v[228:231]\n"
MF "v[220:223],a[220:223],a[180:183],v[220:223]\n"
MF "v[220:223],a[216:219],a[140:143],v[220:223]\n"
"s_waitcnt lgkmcnt(4)\n"
MF "v[16:19],a[200:203],a[60:63],v[16:19]\n"
TR "a[216:217],v1\n"
MF "v[16:19],a[204:207],a[100:103],v[16:19]\n"
TR "a[218:219],v1 offset:512\n"
MF "v[24:27],a[204:207],a[104:107],v[24:27]\n"
LD "v13,s[28:31],s82 offen offset:3072 lds\n"
MF "v[24:27],a[200:203],a[64:67],v[24:27]\n"
TR "a[220:221],v1 offset:4096\n"
MF "v[32:35],a[200:203],a[68:71],v[32:35]\n"
TR "a[222:223],v1 offset:4608\n"
MF "v[32:35],a[204:207],a[108:111],v[32:35]\n"
MF "v[40:43],a[204:207],a[112:115],v[40:43]\n"
MF "v[40:43],a[200:203],a[72:75],v[40:43]\n"
"s_add_u32 s84,s14,40960\n"
MF "v[48:51],a[200:203],a[76:79],v[48:51]\n"
"s_add_u32 m0,s63,24576\n"
MF "v[48:51],a[204:207],a[116:119],v[48:51]\n"
"s_waitcnt lgkmcnt(4)\n"
LD "v4,s[24:27],s84 offen lds\n"
MF "v[52:55],a[212:215],a[116:119],v[52:55]\n"
TR "a[200:201],v1 offset:8\n"
MF "v[52:55],a[208:211],a[76:79],v[52:55]\n"
TR "a[202:203],v1 offset:520\n"
MF "v[44:47],a[208:211],a[72:75],v[44:47]\n"
MF "v[44:47],a[212:215],a[112:115],v[44:47]\n"
TR "a[204:205],v1 offset:4104\n"
MF "v[36:39],a[212:215],a[108:111],v[36:39]\n"
TR "a[206:207],v1 offset:4616\n"
MF "v[36:39],a[208:211],a[68:71],v[36:39]\n"
MF "v[28:31],a[208:211],a[64:67],v[28:31]\n"
MF "v[28:31],a[212:215],a[104:107],v[28:31]\n"
MF "v[20:23],a[212:215],a[100:103],v[20:23]\n"
"s_waitcnt vmcnt(24) lgkmcnt(0)\n"
MF "v[20:23],a[208:211],a[60:63],v[20:23]\n"
"s_barrier\n"
MF "v[56:59],a[216:219],a[60:63],v[56:59]\n"
"s_add_u32 m0,s62,0\n"
"s_add_u32 s82,s66,3145728\n"
TR "a[208:209],v0 offset:16384\n"
MF "v[56:59],a[220:223],a[100:103],v[56:59]\n"
"s_sub_u32 s15,s82,7864320\n"
"s_min_u32 s82,s82,s15\n"
TR "a[210:211],v0 offset:16896\n"
MF "v[64:67],a[220:223],a[104:107],v[64:67]\n"
LD "v6,s[28:31],s82 offen lds\n"
MF "v[64:67],a[216:219],a[64:67],v[64:67]\n"
TR "a[212:213],v0 offset:20480\n"
MF "v[72:75],a[216:219],a[68:71],v[72:75]\n"
TR "a[214:215],v0 offset:20992\n"
MF "v[72:75],a[220:223],a[108:111],v[72:75]\n"
MF "v[80:83],a[220:223],a[112:115],v[80:83]\n"
LD "v11,s[28:31],s82 offen offset:1024 lds\n"
MF "v[80:83],a[216:219],a[72:75],v[80:83]\n"
MF "v[88:91],a[216:219],a[76:79],v[88:91]\n"
MF "v[88:91],a[220:223],a[116:119],v[88:91]\n"
MF "v[92:95],a[204:207],a[116:119],v[92:95]\n"
TR "a[216:217],v0 offset:16392\n"
MF "v[92:95],a[200:203],a[76:79],v[92:95]\n"
TR "a[218:219],v0 offset:16904\n"
MF "v[84:87],a[200:203],a[72:75],v[84:87]\n"
LD "v12,s[28:31],s82 offen offset:2048 lds\n"
MF "v[84:87],a[204:207],a[112:115],v[84:87]\n"
TR "a[220:221],v0 offset:20488\n"
MF "v[76:79],a[204:207],a[108:111],v[76:79]\n"
TR "a[222:223],v0 offset:21000\n"
MF "v[76:79],a[200:203],a[68:71],v[76:79]\n"
MF "v[68:71],a[200:203],a[64:67],v[68:71]\n"
MF "v[68:71],a[204:207],a[104:107],v[68:71]\n"
MF "v[60:63],a[204:207],a[100:103],v[60:63]\n"
LD "v13,s[28:31],s82 offen offset:3072 lds\n"
MF "v[60:63],a[200:203],a[60:63],v[60:63]\n"
"s_waitcnt lgkmcnt(4)\n"
MF "v[96:99],a[208:211],a[60:63],v[96:99]\n"
TR "a[200:201],v1 offset:16384\n"
MF "v[96:99],a[212:215],a[100:103],v[96:99]\n"
TR "a[202:203],v1 offset:16896\n"
MF "v[104:107],a[212:215],a[104:107],v[104:107]\n"
"s_add_u32 s14,s81,640\n"
"s_sub_u32 s15,s14,1280\n"
MF "v[104:107],a[208:211],a[64:67],v[104:107]\n"
"s_min_u32 s14,s14,s15\n"
"s_add_u32 s14,s14,s68\n"
TR "a[204:205],v1 offset:20480\n"
MF "v[112:115],a[208:211],a[68:71],v[112:115]\n"
"s_add_u32 s84,s14,0\n"
"s_add_u32 m0,s63,40960\n"
TR "a[206:207],v1 offset:20992\n"
MF "v[112:115],a[212:215],a[108:111],v[112:115]\n"
LD "v4,s[24:27],s84 offen lds\n"
MF "v[120:123],a[212:215],a[112:115],v[120:123]\n"
MF "v[120:123],a[208:211],a[72:75],v[120:123]\n"
MF "v[128:131],a[208:211],a[76:79],v[128:131]\n"
"s_add_u32 s84,s14,9216\n"
MF "v[128:131],a[212:215],a[116:119],v[128:131]\n"
"s_waitcnt lgkmcnt(4)\n"
LD "v5,s[24:27],s84 offen offset:1024 lds\n"
MF "v[132:135],a[220:223],a[116:119],v[132:135]\n"
TR "a[208:209],v1 offset:16392\n"
MF "v[132:135],a[216:219],a[76:79],v[132:135]\n"
TR "a[210:211],v1 offset:16904\n"
MF "v[124:127],a[216:219],a[72:75],v[124:127]\n"
MF "v[124:127],a[220:223],a[112:115],v[124:127]\n"
TR "a[212:213],v1 offset:20488\n"
MF "v[116:119],a[220:223],a[108:111],v[116:119]\n"
TR "a[214:215],v1 offset:21000\n"
MF "v[116:119],a[216:219],a[68:71],v[116:119]\n"
MF "v[108:111],a[216:219],a[64:67],v[108:111]\n"
MF "v[108:111],a[220:223],a[104:107],v[108:111]\n"
MF "v[100:103],a[220:223],a[100:103],v[100:103]\n"
"s_waitcnt vmcnt(23) lgkmcnt(0)\n"
MF "v[100:103],a[216:219],a[60:63],v[100:103]\n"
"s_barrier\n"
MF "v[136:139],a[200:203],a[60:63],v[136:139]\n"
TR "a[216:217],v0 offset:32768\n"
MF "v[136:139],a[204:207],a[100:103],v[136:139]\n"
"s_add_u32 m0,s62,16384\n"
TR "a[218:219],v0 offset:33280\n"
MF "v[144:147],a[204:207],a[104:107],v[144:147]\n"
"s_add_u32 s82,s66,3145856\n"
"ds_read_b128 a[140:143],v2\n"
MF "v[144:147],a[200:203],a[64:67],v[144:147]\n"
"s_sub_u32 s15,s82,7864320\n"
TR "a[220:221],v0 offset:36864\n"
MF "v[152:155],a[200:203],a[68:71],v[152:155]\n"
"s_min_u32 s82,s82,s15\n"
TR "a[222:223],v0 offset:37376\n"
MF "v[152:155],a[204:207],a[108:111],v[152:155]\n"
LD "v6,s[28:31],s82 offen lds\n"
MF "v[160:163],a[204:207],a[112:115],v[160:163]\n"
"ds_read_b128 a[180:183],v3\n"
MF "v[160:163],a[200:203],a[72:75],v[160:163]\n"
LD "v11,s[28:31],s82 offen offset:1024 lds\n"
MF "v[168:171],a[200:203],a[76:79],v[168:171]\n"
MF "v[168:171],a[204:207],a[116:119],v[168:171]\n"
LD "v12,s[28:31],s82 offen offset:2048 lds\n"
MF "v[172:175],a[212:215],a[116:119],v[172:175]\n"
TR "a[200:201],v0 offset:32776\n"
MF "v[172:175],a[208:211],a[76:79],v[172:175]\n"
TR "a[202:203],v0 offset:33288\n"
MF "v[164:167],a[208:211],a[72:75],v[164:167]\n"
"ds_read_b128 a[144:147],v2 offset:2048\n"
MF "v[164:167],a[212:215],a[112:115],v[164:167]\n"
TR "a[204:205],v0 offset:36872\n"
MF "v[156:159],a[212:215],a[108:111],v[156:159]\n"
TR "a[206:207],v0 offset:37384\n"
MF "v[156:159],a[208:211],a[68:71],v[156:159]\n"
MF "v[148:151],a[208:211],a[64:67],v[148:151]\n"
"ds_read_b128 a[184:187],v3 offset:2048\n"
MF "v[148:151],a[212:215],a[104:107],v[148:151]\n"
MF "v[140:143],a[212:215],a[100:103],v[140:143]\n"
LD "v13,s[28:31],s82 offen offset:3072 lds\n"
MF "v[140:143],a[208:211],a[60:63],v[140:143]\n"
"s_waitcnt lgkmcnt(7)\n"
MF "v[176:179],a[216:219],a[60:63],v[176:179]\n"
TR "a[208:209],v1 offset:32768\n"
MF "v[176:179],a[220:223],a[100:103],v[176:179]\n"
TR "a[210:211],v1 offset:33280\n"
MF "v[184:187],a[220:223],a[104:107],v[184:187]\n"
"ds_read_b128 a[148:151],v2 offset:4096\n"
MF "v[184:187],a[216:219],a[64:67],v[184:187]\n"
"s_add_u32 s84,s14,20480\n"
TR "a[212:213],v1 offset:36864\n"
MF "v[192:195],a[216:219],a[68:71],v[192:195]\n"
"s_add_u32 m0,s63,43008\n"
TR "a[214:215],v1 offset:37376\n"
MF "v[192:195],a[220:223],a[108:111],v[192:195]\n"
LD "v4,s[24:27],s84 offen lds\n"
MF "v[200:203],a[220:223],a[112:115],v[200:203]\n"
"ds_read_b128 a[188:191],v3 offset:4096\n"
MF "v[200:203],a[216:219],a[72:75],v[200:203]\n"
MF "v[208:211],a[216:219],a[76:79],v[208:211]\n"
"s_add_u32 s84,s14,29696\n"
MF "v[208:211],a[220:223],a[116:119],v[208:211]\n"
"s_waitcnt lgkmcnt(7)\n"
LD "v5,s[24:27],s84 offen offset:1024 lds\n"
MF "v[212:215],a[204:207],a[116:119],v[212:215]\n"
TR "a[216:217],v1 offset:32776\n"
MF "v[212:215],a[200:203],a[76:79],v[212:215]\n"
TR "a[218:219],v1 offset:33288\n"
MF "v[204:207],a[200:203],a[72:75],v[204:207]\n"
"ds_read_b128 a[152:155],v2 offset:6144\n"
MF "v[204:207],a[204:207],a[112:115],v[204:207]\n"
TR "a[220:221],v1 offset:36872\n"
MF "v[196:199],a[204:207],a[108:111],v[196:199]\n"
TR "a[222:223],v1 offset:37384\n"
MF "v[196:199],a[200:203],a[68:71],v[196:199]\n"
MF "v[188:191],a[200:203],a[64:67],v[188:191]\n"
"v_add_u32 v0,0xc000,v0\n"
MF "v[188:191],a[204:207],a[104:107],v[188:191]\n"
"v_add_u32 v1,0xc000,v1\n"
MF "v[180:183],a[204:207],a[100:103],v[180:183]\n"
"s_waitcnt vmcnt(25) lgkmcnt(0)\n"
MF "v[180:183],a[200:203],a[60:63],v[180:183]\n"
"s_barrier\n"
MF "v[216:219],a[208:211],a[60:63],v[216:219]\n"
TR "a[200:201],v0\n"
MF "v[216:219],a[212:215],a[100:103],v[216:219]\n"
"s_add_u32 m0,s62,32768\n"
TR "a[202:203],v0 offset:512\n"
MF "v[224:227],a[212:215],a[104:107],v[224:227]\n"
".Lpg5c:\n"
"s_cbranch_execz .Lpg14c\n"
".Lpg5f:\n"
"s_cbranch_execz .Lpgt0k_f\n"
"s_add_u32 s82,s66,3145984\n"
"ds_read_b128 a[192:195],v3 offset:6144\n"
MF "v[224:227],a[208:211],a[64:67],v[224:227]\n"
"s_sub_u32 s15,s82,7864320\n"
TR "a[204:205],v0 offset:4096\n"
MF "v[232:235],a[208:211],a[68:71],v[232:235]\n"
"s_min_u32 s82,s82,s15\n"
TR "a[206:207],v0 offset:4608\n"
MF "v[232:235],a[212:215],a[108:111],v[232:235]\n"
LD "v6,s[28:31],s82 offen lds\n"
MF "v[240:243],a[212:215],a[112:115],v[240:243]\n"
"ds_read_b128 a[156:159],v2 offset:8192\n"
MF "v[240:243],a[208:211],a[72:75],v[240:243]\n"
"ds_read_b128 a[196:199],v3 offset:8192\n"
MF "v[248:251],a[208:211],a[76:79],v[248:251]\n"
LD "v11,s[28:31],s82 offen offset:1024 lds\n"
MF "v[248:251],a[212:215],a[116:119],v[248:251]\n"
MF "v[252:255],a[220:223],a[116:119],v[252:255]\n"
TR "a[208:209],v0 offset:8\n"
MF "v[252:255],a[216:219],a[76:79],v[252:255]\n"
TR "a[210:211],v0 offset:520\n"
MF "v[244:247],a[216:219],a[72:75],v[244:247]\n"
MF "v[244:247],a[220:223],a[112:115],v[244:247]\n"
TR "a[212:213],v0 offset:4104\n"
MF "v[236:239],a[220:223],a[108:111],v[236:239]\n"
TR "a[214:215],v0 offset:4616\n"
MF "v[236:239],a[216:219],a[68:71],v[236:239]\n"
LD "v12,s[28:31],s82 offen offset:2048 lds\n"
MF "v[228:231],a[216:219],a[64:67],v[228:231]\n"
MF "v[228:231],a[220:223],a[104:107],v[228:231]\n"
MF "v[220:223],a[220:223],a[100:103],v[220:223]\n"
MF "v[220:223],a[216:219],a[60:63],v[220:223]\n"
"s_waitcnt lgkmcnt(4)\n"
MF "v[16:19],a[200:203],a[140:143],v[16:19]\n"
TR "a[216:217],v1\n"
MF "v[16:19],a[204:207],a[180:183],v[16:19]\n"
TR "a[218:219],v1 offset:512\n"
MF "v[24:27],a[204:207],a[184:187],v[24:27]\n"
LD "v13,s[28:31],s82 offen offset:3072 lds\n"
MF "v[24:27],a[200:203],a[144:147],v[24:27]\n"
TR "a[220:221],v1 offset:4096\n"
MF "v[32:35],a[200:203],a[148:151],v[32:35]\n"
TR "a[222:223],v1 offset:4608\n"
MF "v[32:35],a[204:207],a[188:191],v[32:35]\n"
MF "v[40:43],a[204:207],a[192:195],v[40:43]\n"
MF "v[40:43],a[200:203],a[152:155],v[40:43]\n"
"s_add_u32 s84,s14,40960\n"
MF "v[48:51],a[200:203],a[156:159],v[48:51]\n"
"s_add_u32 m0,s63,45056\n"
MF "v[48:51],a[204:207],a[196:199],v[48:51]\n"
"s_waitcnt lgkmcnt(4)\n"
LD "v4,s[24:27],s84 offen lds\n"
MF "v[52:55],a[212:215],a[196:199],v[52:55]\n"
TR "a[200:201],v1 offset:8\n"
MF "v[52:55],a[208:211],a[156:159],v[52:55]\n"
TR "a[202:203],v1 offset:520\n"
MF "v[44:47],a[208:211],a[152:155],v[44:47]\n"
MF "v[44:47],a[212:215],a[192:195],v[44:47]\n"
TR "a[204:205],v1 offset:4104\n"
MF "v[36:39],a[212:215],a[188:191],v[36:39]\n"
TR "a[206:207],v1 offset:4616\n"
MF "v[36:39],a[208:211],a[148:151],v[36:39]\n"
MF "v[28:31],a[208:211],a[144:147],v[28:31]\n"
MF "v[28:31],a[212:215],a[184:187],v[28:31]\n"
MF "v[20:23],a[212:215],a[180:183],v[20:23]\n"
"s_waitcnt vmcnt(24) lgkmcnt(0)\n"
MF "v[20:23],a[208:211],a[140:143],v[20:23]\n"
"s_barrier\n"
MF "v[56:59],a[216:219],a[140:143],v[56:59]\n"
"s_add_u32 m0,s62,49152\n"
"s_add_u32 s82,s66,3932160\n"
TR "a[208:209],v0 offset:16384\n"
MF "v[56:59],a[220:223],a[180:183],v[56:59]\n"
"s_sub_u32 s15,s82,7864320\n"
"s_min_u32 s82,s82,s15\n"
TR "a[210:211],v0 offset:16896\n"
MF "v[64:67],a[220:223],a[184:187],v[64:67]\n"
LD "v6,s[28:31],s82 offen lds\n"
MF "v[64:67],a[216:219],a[144:147],v[64:67]\n"
TR "a[212:213],v0 offset:20480\n"
MF "v[72:75],a[216:219],a[148:151],v[72:75]\n"
TR "a[214:215],v0 offset:20992\n"
MF "v[72:75],a[220:223],a[188:191],v[72:75]\n"
MF "v[80:83],a[220:223],a[192:195],v[80:83]\n"
LD "v11,s[28:31],s82 offen offset:1024 lds\n"
MF "v[80:83],a[216:219],a[152:155],v[80:83]\n"
MF "v[88:91],a[216:219],a[156:159],v[88:91]\n"
MF "v[88:91],a[220:223],a[196:199],v[88:91]\n"
MF "v[92:95],a[204:207],a[196:199],v[92:95]\n"
TR "a[216:217],v0 offset:16392\n"
MF "v[92:95],a[200:203],a[156:159],v[92:95]\n"
TR "a[218:219],v0 offset:16904\n"
MF "v[84:87],a[200:203],a[152:155],v[84:87]\n"
LD "v12,s[28:31],s82 offen offset:2048 lds\n"
MF "v[84:87],a[204:207],a[192:195],v[84:87]\n"
TR "a[220:221],v0 offset:20488\n"
MF "v[76:79],a[204:207],a[188:191],v[76:79]\n"
TR "a[222:223],v0 offset:21000\n"
MF "v[76:79],a[200:203],a[148:151],v[76:79]\n"
MF "v[68:71],a[200:203],a[144:147],v[68:71]\n"
MF "v[68:71],a[204:207],a[184:187],v[68:71]\n"
MF "v[60:63],a[204:207],a[180:183],v[60:63]\n"
LD "v13,s[28:31],s82 offen offset:3072 lds\n"
MF "v[60:63],a[200:203],a[140:143],v[60:63]\n"
"s_waitcnt lgkmcnt(4)\n"
MF "v[96:99],a[208:211],a[140:143],v[96:99]\n"
TR "a[200:201],v1 offset:16384\n"
MF "v[96:99],a[212:215],a[180:183],v[96:99]\n"
TR "a[202:203],v1 offset:16896\n"
MF "v[104:107],a[212:215],a[184:187],v[104:107]\n"
"s_add_u32 s14,s81,768\n"
"s_sub_u32 s15,s14,1280\n"
MF "v[104:107],a[208:211],a[144:147],v[104:107]\n"
"s_min_u32 s14,s14,s15\n"
"s_add_u32 s14,s14,s68\n"
TR "a[204:205],v1 offset:20480\n"
MF "v[112:115],a[208:211],a[148:151],v[112:115]\n"
"s_add_u32 s84,s14,0\n"
"s_add_u32 m0,s63,0\n"
TR "a[206:207],v1 offset:20992\n"
MF "v[112:115],a[212:215],a[188:191],v[112:115]\n"
LD "v4,s[24:27],s84 offen lds\n"
MF "v[120:123],a[212:215],a[192:195],v[120:123]\n"
MF "v[120:123],a[208:211],a[152:155],v[120:123]\n"
MF "v[128:131],a[208:211],a[156:159],v[128:131]\n"
"s_add_u32 s84,s14,9216\n"
MF "v[128:131],a[212:215],a[196:199],v[128:131]\n"
"s_waitcnt lgkmcnt(4)\n"
LD "v5,s[24:27],s84 offen offset:1024 lds\n"
MF "v[132:135],a[220:223],a[196:199],v[132:135]\n"
TR "a[208:209],v1 offset:16392\n"
MF "v[132:135],a[216:219],a[156:159],v[132:135]\n"
TR "a[210:211],v1 offset:16904\n"
MF "v[124:127],a[216:219],a[152:155],v[124:127]\n"
MF "v[124:127],a[220:223],a[192:195],v[124:127]\n"
TR "a[212:213],v1 offset:20488\n"
MF "v[116:119],a[220:223],a[188:191],v[116:119]\n"
TR "a[214:215],v1 offset:21000\n"
MF "v[116:119],a[216:219],a[148:151],v[116:119]\n"
MF "v[108:111],a[216:219],a[144:147],v[108:111]\n"
MF "v[108:111],a[220:223],a[184:187],v[108:111]\n"
MF "v[100:103],a[220:223],a[180:183],v[100:103]\n"
"s_waitcnt vmcnt(23) lgkmcnt(0)\n"
MF "v[100:103],a[216:219],a[140:143],v[100:103]\n"
"s_barrier\n"
MF "v[136:139],a[200:203],a[140:143],v[136:139]\n"
TR "a[216:217],v0 offset:32768\n"
MF "v[136:139],a[204:207],a[180:183],v[136:139]\n"
"s_add_u32 m0,s62,65536\n"
TR "a[218:219],v0 offset:33280\n"
MF "v[144:147],a[204:207],a[184:187],v[144:147]\n"
"s_add_u32 s82,s66,3932288\n"
"ds_read_b128 a[60:63],v2 offset:20480\n"
MF "v[144:147],a[200:203],a[144:147],v[144:147]\n"
"s_sub_u32 s15,s82,7864320\n"
TR "a[220:221],v0 offset:36864\n"
MF "v[152:155],a[200:203],a[148:151],v[152:155]\n"
"s_min_u32 s82,s82,s15\n"
TR "a[222:223],v0 offset:37376\n"
MF "v[152:155],a[204:207],a[188:191],v[152:155]\n"
LD "v6,s[28:31],s82 offen lds\n"
MF "v[160:163],a[204:207],a[192:195],v[160:163]\n"
"ds_read_b128 a[100:103],v3 offset:20480\n"
MF "v[160:163],a[200:203],a[152:155],v[160:163]\n"
LD "v11,s[28:31],s82 offen offset:1024 lds\n"
MF "v[168:171],a[200:203],a[156:159],v[168:171]\n"
MF "v[168:171],a[204:207],a[196:199],v[168:171]\n"
LD "v12,s[28:31],s82 offen offset:2048 lds\n"
MF "v[172:175],a[212:215],a[196:199],v[172:175]\n"
TR "a[200:201],v0 offset:32776\n"
MF "v[172:175],a[208:211],a[156:159],v[172:175]\n"
TR "a[202:203],v0 offset:33288\n"
MF "v[164:167],a[208:211],a[152:155],v[164:167]\n"
"ds_read_b128 a[64:67],v2 offset:22528\n"
MF "v[164:167],a[212:215],a[192:195],v[164:167]\n"
TR "a[204:205],v0 offset:36872\n"
MF "v[156:159],a[212:215],a[188:191],v[156:159]\n"
TR "a[206:207],v0 offset:37384\n"
MF "v[156:159],a[208:211],a[148:151],v[156:159]\n"
MF "v[148:151],a[208:211],a[144:147],v[148:151]\n"
"ds_read_b128 a[104:107],v3 offset:22528\n"
MF "v[148:151],a[212:215],a[184:187],v[148:151]\n"
MF "v[140:143],a[212:215],a[180:183],v[140:143]\n"
LD "v13,s[28:31],s82 offen offset:3072 lds\n"
MF "v[140:143],a[208:211],a[140:143],v[140:143]\n"
"s_waitcnt lgkmcnt(7)\n"
MF "v[176:179],a[216:219],a[140:143],v[176:179]\n"
TR "a[208:209],v1 offset:32768\n"
MF "v[176:179],a[220:223],a[180:183],v[176:179]\n"
TR "a[210:211],v1 offset:33280\n"
MF "v[184:187],a[220:223],a[184:187],v[184:187]\n"
"ds_read_b128 a[68:71],v2 offset:24576\n"
MF "v[184:187],a[216:219],a[144:147],v[184:187]\n"
"s_add_u32 s84,s14,20480\n"
TR "a[212:213],v1 offset:36864\n"
MF "v[192:195],a[216:219],a[148:151],v[192:195]\n"
"s_add_u32 m0,s63,2048\n"
TR "a[214:215],v1 offset:37376\n"
MF "v[192:195],a[220:223],a[188:191],v[192:195]\n"
LD "v4,s[24:27],s84 offen lds\n"
MF "v[200:203],a[220:223],a[192:195],v[200:203]\n"
"ds_read_b128 a[108:111],v3 offset:24576\n"
MF "v[200:203],a[216:219],a[152:155],v[200:203]\n"
MF "v[208:211],a[216:219],a[156:159],v[208:211]\n"
"s_add_u32 s84,s14,29696\n"
MF "v[208:211],a[220:223],a[196:199],v[208:211]\n"
"s_waitcnt lgkmcnt(7)\n"
LD "v5,s[24:27],s84 offen offset:1024 lds\n"
MF "v[212:215],a[204:207],a[196:199],v[212:215]\n"
TR "a[216:217],v1 offset:32776\n"
MF "v[212:215],a[200:203],a[156:159],v[212:215]\n"
TR "a[218:219],v1 offset:33288\n"
MF "v[204:207],a[200:203],a[152:155],v[204:207]\n"
"ds_read_b128 a[72:75],v2 offset:26624\n"
MF "v[204:207],a[204:207],a[192:195],v[204:207]\n"
TR "a[220:221],v1 offset:36872\n"
MF "v[196:199],a[204:207],a[188:191],v[196:199]\n"
TR "a[222:223],v1 offset:37384\n"
MF "v[196:199],a[200:203],a[148:151],v[196:199]\n"
MF "v[188:191],a[200:203],a[144:147],v[188:191]\n"
"v_add_u32 v0,0xffff4000,v0\n"
MF "v[188:191],a[204:207],a[184:187],v[188:191]\n"
"v_add_u32 v1,0xffff4000,v1\n"
MF "v[180:183],a[204:207],a[180:183],v[180:183]\n"
"s_waitcnt vmcnt(25) lgkmcnt(0)\n"
MF "v[180:183],a[200:203],a[140:143],v[180:183]\n"
"s_barrier\n"
MF "v[216:219],a[208:211],a[140:143],v[216:219]\n"
TR "a[200:201],v0\n"
MF "v[216:219],a[212:215],a[180:183],v[216:219]\n"
"s_add_u32 m0,s62,81920\n"
TR "a[202:203],v0 offset:512\n"
MF "v[224:227],a[212:215],a[184:187],v[224:227]\n"
"s_add_u32 s82,s66,3932416\n"
"ds_read_b128 a[112:115],v3 offset:26624\n"
MF "v[224:227],a[208:211],a[144:147],v[224:227]\n"
"s_sub_u32 s15,s82,7864320\n"
TR "a[204:205],v0 offset:4096\n"
MF "v[232:235],a[208:211],a[148:151],v[232:235]\n"
"s_min_u32 s82,s82,s15\n"
TR "a[206:207],v0 offset:4608\n"
MF "v[232:235],a[212:215],a[188:191],v[232:235]\n"
LD "v6,s[28:31],s82 offen lds\n"
MF "v[240:243],a[212:215],a[192:195],v[240:243]\n"
"ds_read_b128 a[76:79],v2 offset:28672\n"
MF "v[240:243],a[208:211],a[152:155],v[240:243]\n"
"ds_read_b128 a[116:119],v3 offset:28672\n"
MF "v[248:251],a[208:211],a[156:159],v[248:251]\n"
LD "v11,s[28:31],s82 offen offset:1024 lds\n"
MF "v[248:251],a[212:215],a[196:199],v[248:251]\n"
MF "v[252:255],a[220:223],a[196:199],v[252:255]\n"
TR "a[208:209],v0 offset:8\n"
MF "v[252:255],a[216:219],a[156:159],v[252:255]\n"
TR "a[210:211],v0 offset:520\n"
MF "v[244:247],a[216:219],a[152:155],v[244:247]\n"
MF "v[244:247],a[220:223],a[192:195],v[244:247]\n"
TR "a[212:213],v0 offset:4104\n"
MF "v[236:239],a[220:223],a[188:191],v[236:239]\n"
TR "a[214:215],v0 offset:4616\n"
MF "v[236:239],a[216:219],a[148:151],v[236:239]\n"
LD "v12,s[28:31],s82 offen offset:2048 lds\n"
MF "v[228:231],a[216:219],a[144:147],v[228:231]\n"
MF "v[228:231],a[220:223],a[184:187],v[228:231]\n"
MF "v[220:223],a[220:223],a[180:183],v[220:223]\n"
MF "v[220:223],a[216:219],a[140:143],v[220:223]\n"
"s_waitcnt lgkmcnt(4)\n"
MF "v[16:19],a[200:203],a[60:63],v[16:19]\n"
TR "a[216:217],v1\n"
MF "v[16:19],a[204:207],a[100:103],v[16:19]\n"
TR "a[218:219],v1 offset:512\n"
MF "v[24:27],a[204:207],a[104:107],v[24:27]\n"
LD "v13,s[28:31],s82 offen offset:3072 lds\n"
MF "v[24:27],a[200:203],a[64:67],v[24:27]\n"
TR "a[220:221],v1 offset:4096\n"
MF "v[32:35],a[200:203],a[68:71],v[32:35]\n"
TR "a[222:223],v1 offset:4608\n"
MF "v[32:35],a[204:207],a[108:111],v[32:35]\n"
MF "v[40:43],a[204:207],a[112:115],v[40:43]\n"
MF "v[40:43],a[200:203],a[72:75],v[40:43]\n"
"s_add_u32 s84,s14,40960\n"
MF "v[48:51],a[200:203],a[76:79],v[48:51]\n"
"s_add_u32 m0,s63,4096\n"
MF "v[48:51],a[204:207],a[116:119],v[48:51]\n"
"s_waitcnt lgkmcnt(4)\n"
LD "v4,s[24:27],s84 offen lds\n"
MF "v[52:55],a[212:215],a[116:119],v[52:55]\n"
TR "a[200:201],v1 offset:8\n"
MF "v[52:55],a[208:211],a[76:79],v[52:55]\n"
TR "a[202:203],v1 offset:520\n"
MF "v[44:47],a[208:211],a[72:75],v[44:47]\n"
MF "v[44:47],a[212:215],a[112:115],v[44:47]\n"
TR "a[204:205],v1 offset:4104\n"
MF "v[36:39],a[212:215],a[108:111],v[36:39]\n"
TR "a[206:207],v1 offset:4616\n"
MF "v[36:39],a[208:211],a[68:71],v[36:39]\n"
MF "v[28:31],a[208:211],a[64:67],v[28:31]\n"
MF "v[28:31],a[212:215],a[104:107],v[28:31]\n"
MF "v[20:23],a[212:215],a[100:103],v[20:23]\n"
"s_waitcnt vmcnt(24) lgkmcnt(0)\n"
MF "v[20:23],a[208:211],a[60:63],v[20:23]\n"
"s_barrier\n"
MF "v[56:59],a[216:219],a[60:63],v[56:59]\n"
"s_add_u32 m0,s62,0\n"
"s_add_u32 s82,s66,4718592\n"
TR "a[208:209],v0 offset:16384\n"
MF "v[56:59],a[220:223],a[100:103],v[56:59]\n"
"s_sub_u32 s15,s82,7864320\n"
"s_min_u32 s82,s82,s15\n"
TR "a[210:211],v0 offset:16896\n"
MF "v[64:67],a[220:223],a[104:107],v[64:67]\n"
LD "v6,s[28:31],s82 offen lds\n"
MF "v[64:67],a[216:219],a[64:67],v[64:67]\n"
TR "a[212:213],v0 offset:20480\n"
MF "v[72:75],a[216:219],a[68:71],v[72:75]\n"
TR "a[214:215],v0 offset:20992\n"
MF "v[72:75],a[220:223],a[108:111],v[72:75]\n"
MF "v[80:83],a[220:223],a[112:115],v[80:83]\n"
LD "v11,s[28:31],s82 offen offset:1024 lds\n"
MF "v[80:83],a[216:219],a[72:75],v[80:83]\n"
MF "v[88:91],a[216:219],a[76:79],v[88:91]\n"
MF "v[88:91],a[220:223],a[116:119],v[88:91]\n"
MF "v[92:95],a[204:207],a[116:119],v[92:95]\n"
TR "a[216:217],v0 offset:16392\n"
MF "v[92:95],a[200:203],a[76:79],v[92:95]\n"
TR "a[218:219],v0 offset:16904\n"
MF "v[84:87],a[200:203],a[72:75],v[84:87]\n"
LD "v12,s[28:31],s82 offen offset:2048 lds\n"
MF "v[84:87],a[204:207],a[112:115],v[84:87]\n"
TR "a[220:221],v0 offset:20488\n"
MF "v[76:79],a[204:207],a[108:111],v[76:79]\n"
TR "a[222:223],v0 offset:21000\n"
MF "v[76:79],a[200:203],a[68:71],v[76:79]\n"
MF "v[68:71],a[200:203],a[64:67],v[68:71]\n"
MF "v[68:71],a[204:207],a[104:107],v[68:71]\n"
MF "v[60:63],a[204:207],a[100:103],v[60:63]\n"
LD "v13,s[28:31],s82 offen offset:3072 lds\n"
MF "v[60:63],a[200:203],a[60:63],v[60:63]\n"
"s_waitcnt lgkmcnt(4)\n"
MF "v[96:99],a[208:211],a[60:63],v[96:99]\n"
TR "a[200:201],v1 offset:16384\n"
MF "v[96:99],a[212:215],a[100:103],v[96:99]\n"
TR "a[202:203],v1 offset:16896\n"
MF "v[104:107],a[212:215],a[104:107],v[104:107]\n"
"s_add_u32 s14,s81,896\n"
"s_sub_u32 s15,s14,1280\n"
MF "v[104:107],a[208:211],a[64:67],v[104:107]\n"
"s_min_u32 s14,s14,s15\n"
"s_add_u32 s14,s14,s68\n"
TR "a[204:205],v1 offset:20480\n"
MF "v[112:115],a[208:211],a[68:71],v[112:115]\n"
"s_add_u32 s84,s14,0\n"
"s_add_u32 m0,s63,20480\n"
TR "a[206:207],v1 offset:20992\n"
MF "v[112:115],a[212:215],a[108:111],v[112:115]\n"
LD "v4,s[24:27],s84 offen lds\n"
MF "v[120:123],a[212:215],a[112:115],v[120:123]\n"
MF "v[120:123],a[208:211],a[72:75],v[120:123]\n"
MF "v[128:131],a[208:211],a[76:79],v[128:131]\n"
"s_add_u32 s84,s14,9216\n"
MF "v[128:131],a[212:215],a[116:119],v[128:131]\n"
"s_waitcnt lgkmcnt(4)\n"
LD "v5,s[24:27],s84 offen offset:1024 lds\n"
MF "v[132:135],a[220:223],a[116:119],v[132:135]\n"
TR "a[208:209],v1 offset:16392\n"
MF "v[132:135],a[216:219],a[76:79],v[132:135]\n"
TR "a[210:211],v1 offset:16904\n"
MF "v[124:127],a[216:219],a[72:75],v[124:127]\n"
MF "v[124:127],a[220:223],a[112:115],v[124:127]\n"
TR "a[212:213],v1 offset:20488\n"
MF "v[116:119],a[220:223],a[108:111],v[116:119]\n"
TR "a[214:215],v1 offset:21000\n"
MF "v[116:119],a[216:219],a[68:71],v[116:119]\n"
MF "v[108:111],a[216:219],a[64:67],v[108:111]\n"
MF "v[108:111],a[220:223],a[104:107],v[108:111]\n"
MF "v[100:103],a[220:223],a[100:103],v[100:103]\n"
"s_waitcnt vmcnt(23) lgkmcnt(0)\n"
MF "v[100:103],a[216:219],a[60:63],v[100:103]\n"
"s_barrier\n"
MF "v[136:139],a[200:203],a[60:63],v[136:139]\n"
TR "a[216:217],v0 offset:32768\n"
MF "v[136:139],a[204:207],a[100:103],v[136:139]\n"
"s_add_u32 m0,s62,16384\n"
TR "a[218:219],v0 offset:33280\n"
MF "v[144:147],a[204:207],a[104:107],v[144:147]\n"
"s_add_u32 s82,s66,4718720\n"
"ds_read_b128 a[140:143],v2 offset:40960\n"
MF "v[144:147],a[200:203],a[64:67],v[144:147]\n"
"s_sub_u32 s15,s82,7864320\n"
TR "a[220:221],v0 offset:36864\n"
MF "v[152:155],a[200:203],a[68:71],v[152:155]\n"
"s_min_u32 s82,s82,s15\n"
TR "a[222:223],v0 offset:37376\n"
MF "v[152:155],a[204:207],a[108:111],v[152:155]\n"
LD "v6,s[28:31],s82 offen lds\n"
MF "v[160:163],a[204:207],a[112:115],v[160:163]\n"
"ds_read_b128 a[180:183],v3 offset:40960\n"
MF "v[160:163],a[200:203],a[72:75],v[160:163]\n"
LD "v11,s[28:31],s82 offen offset:1024 lds\n"
MF "v[168:171],a[200:203],a[76:79],v[168:171]\n"
MF "v[168:171],a[204:207],a[116:119],v[168:171]\n"
LD "v12,s[28:31],s82 offen offset:2048 lds\n"
MF "v[172:175],a[212:215],a[116:119],v[172:175]\n"
TR "a[200:201],v0 offset:32776\n"
MF "v[172:175],a[208:211],a[76:79],v[172:175]\n"
TR "a[202:203],v0 offset:33288\n"
MF "v[164:167],a[208:211],a[72:75],v[164:167]\n"
"ds_read_b128 a[144:147],v2 offset:43008\n"
MF "v[164:167],a[212:215],a[112:115],v[164:167]\n"
TR "a[204:205],v0 offset:36872\n"
MF "v[156:159],a[212:215],a[108:111],v[156:159]\n"
TR "a[206:207],v0 offset:37384\n"
MF "v[156:159],a[208:211],a[68:71],v[156:159]\n"
MF "v[148:151],a[208:211],a[64:67],v[148:151]\n"
"ds_read_b128 a[184:187],v3 offset:43008\n"
MF "v[148:151],a[212:215],a[104:107],v[148:151]\n"
MF "v[140:143],a[212:215],a[100:103],v[140:143]\n"
LD "v13,s[28:31],s82 offen offset:3072 lds\n"
MF "v[140:143],a[208:211],a[60:63],v[140:143]\n"
"s_waitcnt lgkmcnt(7)\n"
MF "v[176:179],a[216:219],a[60:63],v[176:179]\n"
TR "a[208:209],v1 offset:32768\n"
MF "v[176:179],a[220:223],a[100:103],v[176:179]\n"
TR "a[210:211],v1 offset:33280\n"
MF "v[184:187],a[220:223],a[104:107],v[184:187]\n"
"ds_read_b128 a[148:151],v2 offset:45056\n"
MF "v[184:187],a[216:219],a[64:67],v[184:187]\n"
"s_add_u32 s84,s14,20480\n"
TR "a[212:213],v1 offset:36864\n"
MF "v[192:195],a[216:219],a[68:71],v[192:195]\n"
"s_add_u32 m0,s63,22528\n"
TR "a[214:215],v1 offset:37376\n"
MF "v[192:195],a[220:223],a[108:111],v[192:195]\n"
LD "v4,s[24:27],s84 offen lds\n"
MF "v[200:203],a[220:223],a[112:115],v[200:203]\n"
"ds_read_b128 a[188:191],v3 offset:45056\n"
MF "v[200:203],a[216:219],a[72:75],v[200:203]\n"
MF "v[208:211],a[216:219],a[76:79],v[208:211]\n"
"s_add_u32 s84,s14,29696\n"
MF "v[208:211],a[220:223],a[116:119],v[208:211]\n"
"s_waitcnt lgkmcnt(7)\n"
LD "v5,s[24:27],s84 offen offset:1024 lds\n"
MF "v[212:215],a[204:207],a[116:119],v[212:215]\n"
TR "a[216:217],v1 offset:32776\n"
MF "v[212:215],a[200:203],a[76:79],v[212:215]\n"
TR "a[218:219],v1 offset:33288\n"
MF "v[204:207],a[200:203],a[72:75],v[204:207]\n"
"ds_read_b128 a[152:155],v2 offset:47104\n"
MF "v[204:207],a[204:207],a[112:115],v[204:207]\n"
TR "a[220:221],v1 offset:36872\n"
MF "v[196:199],a[204:207],a[108:111],v[196:199]\n"
TR "a[222:223],v1 offset:37384\n"
MF "v[196:199],a[200:203],a[68:71],v[196:199]\n"
MF "v[188:191],a[200:203],a[64:67],v[188:191]\n"
"v_add_u32 v0,0xc000,v0\n"
MF "v[188:191],a[204:207],a[104:107],v[188:191]\n"
"v_add_u32 v1,0xc000,v1\n"
MF "v[180:183],a[204:207],a[100:103],v[180:183]\n"
"s_waitcnt vmcnt(25) lgkmcnt(0)\n"
MF "v[180:183],a[200:203],a[60:63],v[180:183]\n"
"s_barrier\n"
MF "v[216:219],a[208:211],a[60:63],v[216:219]\n"
TR "a[200:201],v0\n"
MF "v[216:219],a[212:215],a[100:103],v[216:219]\n"
"s_add_u32 m0,s62,32768\n"
TR "a[202:203],v0 offset:512\n"
MF "v[224:227],a[212:215],a[104:107],v[224:227]\n"
"s_add_u32 s82,s66,4718848\n"
"ds_read_b128 a[192:195],v3 offset:47104\n"
MF "v[224:227],a[208:211],a[64:67],v[224:227]\n"
"s_sub_u32 s15,s82,7864320\n"
TR "a[204:205],v0 offset:4096\n"
MF "v[232:235],a[208:211],a[68:71],v[232:235]\n"
"s_min_u32 s82,s82,s15\n"
TR "a[206:207],v0 offset:4608\n"
MF "v[232:235],a[212:215],a[108:111],v[232:235]\n"
LD "v6,s[28:31],s82 offen lds\n"
MF "v[240:243],a[212:215],a[112:115],v[240:243]\n"
"ds_read_b128 a[156:159],v2 offset:49152\n"
MF "v[240:243],a[208:211],a[72:75],v[240:243]\n"
"ds_read_b128 a[196:199],v3 offset:49152\n"
MF "v[248:251],a[208:211],a[76:79],v[248:251]\n"
LD "v11,s[28:31],s82 offen offset:1024 lds\n"
MF "v[248:251],a[212:215],a[116:119],v[248:251]\n"
MF "v[252:255],a[220:223],a[116:119],v[252:255]\n"
TR "a[208:209],v0 offset:8\n"
MF "v[252:255],a[216:219],a[76:79],v[252:255]\n"
TR "a[210:211],v0 offset:520\n"
MF "v[244:247],a[216:219],a[72:75],v[244:247]\n"
MF "v[244:247],a[220:223],a[112:115],v[244:247]\n"
TR "a[212:213],v0 offset:4104\n"
MF "v[236:239],a[220:223],a[108:111],v[236:239]\n"
TR "a[214:215],v0 offset:4616\n"
MF "v[236:239],a[216:219],a[68:71],v[236:239]\n"
LD "v12,s[28:31],s82 offen offset:2048 lds\n"
MF "v[228:231],a[216:219],a[64:67],v[228:231]\n"
MF "v[228:231],a[220:223],a[104:107],v[228:231]\n"
MF "v[220:223],a[220:223],a[100:103],v[220:223]\n"
MF "v[220:223],a[216:219],a[60:63],v[220:223]\n"
"s_waitcnt lgkmcnt(4)\n"
MF "v[16:19],a[200:203],a[140:143],v[16:19]\n"
TR "a[216:217],v1\n"
MF "v[16:19],a[204:207],a[180:183],v[16:19]\n"
TR "a[218:219],v1 offset:512\n"
MF "v[24:27],a[204:207],a[184:187],v[24:27]\n"
LD "v13,s[28:31],s82 offen offset:3072 lds\n"
MF "v[24:27],a[200:203],a[144:147],v[24:27]\n"
TR "a[220:221],v1 offset:4096\n"
MF "v[32:35],a[200:203],a[148:151],v[32:35]\n"
TR "a[222:223],v1 offset:4608\n"
MF "v[32:35],a[204:207],a[188:191],v[32:35]\n"
MF "v[40:43],a[204:207],a[192:195],v[40:43]\n"
MF "v[40:43],a[200:203],a[152:155],v[40:43]\n"
"s_add_u32 s84,s14,40960\n"
MF "v[48:51],a[200:203],a[156:159],v[48:51]\n"
"s_add_u32 m0,s63,24576\n"
MF "v[48:51],a[204:207],a[196:199],v[48:51]\n"
"s_waitcnt lgkmcnt(4)\n"
LD "v4,s[24:27],s84 offen lds\n"
MF "v[52:55],a[212:215],a[196:199],v[52:55]\n"
TR "a[200:201],v1 offset:8\n"
MF "v[52:55],a[208:211],a[156:159],v[52:55]\n"
TR "a[202:203],v1 offset:520\n"
MF "v[44:47],a[208:211],a[152:155],v[44:47]\n"
MF "v[44:47],a[212:215],a[192:195],v[44:47]\n"
TR "a[204:205],v1 offset:4104\n"
MF "v[36:39],a[212:215],a[188:191],v[36:39]\n"
TR "a[206:207],v1 offset:4616\n"
MF "v[36:39],a[208:211],a[148:151],v[36:39]\n"
MF "v[28:31],a[208:211],a[144:147],v[28:31]\n"
MF "v[28:31],a[212:215],a[184:187],v[28:31]\n"
MF "v[20:23],a[212:215],a[180:183],v[20:23]\n"
".Lpg6c:\n"
"s_cbranch_execz .Lpg1a\n"
".Lpg6f:\n"
"s_cbranch_execz .Lpg16f\n"
"s_waitcnt vmcnt(24) lgkmcnt(0)\n"
MF "v[20:23],a[208:211],a[140:143],v[20:23]\n"
"s_barrier\n"
MF "v[56:59],a[216:219],a[140:143],v[56:59]\n"
"s_add_u32 m0,s62,49152\n"
"s_add_u32 s82,s66,5505024\n"
TR "a[208:209],v0 offset:16384\n"
MF "v[56:59],a[220:223],a[180:183],v[56:59]\n"
"s_sub_u32 s15,s82,7864320\n"
"s_min_u32 s82,s82,s15\n"
TR "a[210:211],v0 offset:16896\n"
MF "v[64:67],a[220:223],a[184:187],v[64:67]\n"
LD "v6,s[28:31],s82 offen lds\n"
MF "v[64:67],a[216:219],a[144:147],v[64:67]\n"
TR "a[212:213],v0 offset:20480\n"
MF "v[72:75],a[216:219],a[148:151],v[72:75]\n"
TR "a[214:215],v0 offset:20992\n"
MF "v[72:75],a[220:223],a[188:191],v[72:75]\n"
MF "v[80:83],a[220:223],a[192:195],v[80:83]\n"
LD "v11,s[28:31],s82 offen offset:1024 lds\n"
MF "v[80:83],a[216:219],a[152:155],v[80:83]\n"
MF "v[88:91],a[216:219],a[156:159],v[88:91]\n"
MF "v[88:91],a[220:223],a[196:199],v[88:91]\n"
MF "v[92:95],a[204:207],a[196:199],v[92:95]\n"
TR "a[216:217],v0 offset:16392\n"
MF "v[92:95],a[200:203],a[156:159],v[92:95]\n"
TR "a[218:219],v0 offset:16904\n"
MF "v[84:87],a[200:203],a[152:155],v[84:87]\n"
LD "v12,s[28:31],s82 offen offset:2048 lds\n"
MF "v[84:87],a[204:207],a[192:195],v[84:87]\n"
TR "a[220:221],v0 offset:20488\n"
MF "v[76:79],a[204:207],a[188:191],v[76:79]\n"
TR "a[222:223],v0 offset:21000\n"
MF "v[76:79],a[200:203],a[148:151],v[76:79]\n"
MF "v[68:71],a[200:203],a[144:147],v[68:71]\n"
MF "v[68:71],a[204:207],a[184:187],v[68:71]\n"
MF "v[60:63],a[204:207],a[180:183],v[60:63]\n"
LD "v13,s[28:31],s82 offen offset:3072 lds\n"
MF "v[60:63],a[200:203],a[140:143],v[60:63]\n"
"s_waitcnt lgkmcnt(4)\n"
MF "v[96:99],a[208:211],a[140:143],v[96:99]\n"
TR "a[200:201],v1 offset:16384\n"
MF "v[96:99],a[212:215],a[180:183],v[96:99]\n"
TR "a[202:203],v1 offset:16896\n"
MF "v[104:107],a[212:215],a[184:187],v[104:107]\n"
"s_add_u32 s14,s81,1024\n"
"s_sub_u32 s15,s14,1280\n"
MF "v[104:107],a[208:211],a[144:147],v[104:107]\n"
"s_min_u32 s14,s14,s15\n"
"s_add_u32 s14,s14,s68\n"
TR "a[204:205],v1 offset:20480\n"
MF "v[112:115],a[208:211],a[148:151],v[112:115]\n"
"s_add_u32 s84,s14,0\n"
"s_add_u32 m0,s63,40960\n"
TR "a[206:207],v1 offset:20992\n"
MF "v[112:115],a[212:215],a[188:191],v[112:115]\n"
LD "v4,s[24:27],s84 offen lds\n"
MF "v[120:123],a[212:215],a[192:195],v[120:123]\n"
MF "v[120:123],a[208:211],a[152:155],v[120:123]\n"
MF "v[128:131],a[208:211],a[156:159],v[128:131]\n"
"s_add_u32 s84,s14,9216\n"
MF "v[128:131],a[212:215],a[196:199],v[128:131]\n"
"s_waitcnt lgkmcnt(4)\n"
LD "v5,s[24:27],s84 offen offset:1024 lds\n"
MF "v[132:135],a[220:223],a[196:199],v[132:135]\n"
TR "a[208:209],v1 offset:16392\n"
MF "v[132:135],a[216:219],a[156:159],v[132:135]\n"
TR "a[210:211],v1 offset:16904\n"
MF "v[124:127],a[216:219],a[152:155],v[124:127]\n"
MF "v[124:127],a[220:223],a[192:195],v[124:127]\n"
TR "a[212:213],v1 offset:20488\n"
MF "v[116:119],a[220:223],a[188:191],v[116:119]\n"
TR "a[214:215],v1 offset:21000\n"
MF "v[116:119],a[216:219],a[148:151],v[116:119]\n"
MF "v[108:111],a[216:219],a[144:147],v[108:111]\n"
MF "v[108:111],a[220:223],a[184:187],v[108:111]\n"
MF "v[100:103],a[220:223],a[180:183],v[100:103]\n"
"s_waitcnt vmcnt(23) lgkmcnt(0)\n"
MF "v[100:103],a[216:219],a[140:143],v[100:103]\n"
"s_barrier\n"
"s_cmp_eq_u32 s57,1\n"
"s_cbranch_scc0 .Lhs12\n"
"v_bfe_u32 v14,v9,4,5\n"
"v_lshlrev_b32 v14,6,v14\n"
"s_lshl_b32 s58,s78,2\n"
"s_add_u32 s58,s58,s86\n"
"buffer_load_dword v10,v14,s[40:43],s58 offen offset:128 sc1\n"
".Lhs12:\n"
MF "v[136:139],a[200:203],a[140:143],v[136:139]\n"
TR "a[216:217],v0 offset:32768\n"
MF "v[136:139],a[204:207],a[180:183],v[136:139]\n"
"s_add_u32 m0,s62,65536\n"
TR "a[218:219],v0 offset:33280\n"
MF "v[144:147],a[204:207],a[184:187],v[144:147]\n"
"s_add_u32 s82,s66,5505152\n"
"ds_read_b128 a[60:63],v2\n"
MF "v[144:147],a[200:203],a[144:147],v[144:147]\n"
"s_sub_u32 s15,s82,7864320\n"
TR "a[220:221],v0 offset:36864\n"
MF "v[152:155],a[200:203],a[148:151],v[152:155]\n"
"s_min_u32 s82,s82,s15\n"
TR "a[222:223],v0 offset:37376\n"
MF "v[152:155],a[204:207],a[188:191],v[152:155]\n"
LD "v6,s[28:31],s82 offen lds\n"
MF "v[160:163],a[204:207],a[192:195],v[160:163]\n"
"ds_read_b128 a[100:103],v3\n"
MF "v[160:163],a[200:203],a[152:155],v[160:163]\n"
LD "v11,s[28:31],s82 offen offset:1024 lds\n"
MF "v[168:171],a[200:203],a[156:159],v[168:171]\n"
MF "v[168:171],a[204:207],a[196:199],v[168:171]\n"
LD "v12,s[28:31],s82 offen offset:2048 lds\n"
MF "v[172:175],a[212:215],a[196:199],v[172:175]\n"
TR "a[200:201],v0 offset:32776\n"
MF "v[172:175],a[208:211],a[156:159],v[172:175]\n"
TR "a[202:203],v0 offset:33288\n"
MF "v[164:167],a[208:211],a[152:155],v[164:167]\n"
"ds_read_b128 a[64:67],v2 offset:2048\n"
MF "v[164:167],a[212:215],a[192:195],v[164:167]\n"
TR "a[204:205],v0 offset:36872\n"
MF "v[156:159],a[212:215],a[188:191],v[156:159]\n"
TR "a[206:207],v0 offset:37384\n"
MF "v[156:159],a[208:211],a[148:151],v[156:159]\n"
MF "v[148:151],a[208:211],a[144:147],v[148:151]\n"
"ds_read_b128 a[104:107],v3 offset:2048\n"
MF "v[148:151],a[212:215],a[184:187],v[148:151]\n"
MF "v[140:143],a[212:215],a[180:183],v[140:143]\n"
LD "v13,s[28:31],s82 offen offset:3072 lds\n"
MF "v[140:143],a[208:211],a[140:143],v[140:143]\n"
"s_waitcnt lgkmcnt(7)\n"
MF "v[176:179],a[216:219],a[140:143],v[176:179]\n"
TR "a[208:209],v1 offset:32768\n"
MF "v[176:179],a[220:223],a[180:183],v[176:179]\n"
TR "a[210:211],v1 offset:33280\n"
MF "v[184:187],a[220:223],a[184:187],v[184:187]\n"
"ds_read_b128 a[68:71],v2 offset:4096\n"
MF "v[184:187],a[216:219],a[144:147],v[184:187]\n"
"s_add_u32 s84,s14,20480\n"
TR "a[212:213],v1 offset:36864\n"
MF "v[192:195],a[216:219],a[148:151],v[192:195]\n"
"s_add_u32 m0,s63,43008\n"
TR "a[214:215],v1 offset:37376\n"
MF "v[192:195],a[220:223],a[188:191],v[192:195]\n"
LD "v4,s[24:27],s84 offen lds\n"
MF "v[200:203],a[220:223],a[192:195],v[200:203]\n"
"ds_read_b128 a[108:111],v3 offset:4096\n"
MF "v[200:203],a[216:219],a[152:155],v[200:203]\n"
MF "v[208:211],a[216:219],a[156:159],v[208:211]\n"
"s_add_u32 s84,s14,29696\n"
MF "v[208:211],a[220:223],a[196:199],v[208:211]\n"
"s_waitcnt lgkmcnt(7)\n"
LD "v5,s[24:27],s84 offen offset:1024 lds\n"
MF "v[212:215],a[204:207],a[196:199],v[212:215]\n"
TR "a[216:217],v1 offset:32776\n"
MF "v[212:215],a[200:203],a[156:159],v[212:215]\n"
TR "a[218:219],v1 offset:33288\n"
MF "v[204:207],a[200:203],a[152:155],v[204:207]\n"
"ds_read_b128 a[72:75],v2 offset:6144\n"
MF "v[204:207],a[204:207],a[192:195],v[204:207]\n"
TR "a[220:221],v1 offset:36872\n"
MF "v[196:199],a[204:207],a[188:191],v[196:199]\n"
TR "a[222:223],v1 offset:37384\n"
MF "v[196:199],a[200:203],a[148:151],v[196:199]\n"
MF "v[188:191],a[200:203],a[144:147],v[188:191]\n"
"v_add_u32 v0,0xffff4000,v0\n"
MF "v[188:191],a[204:207],a[184:187],v[188:191]\n"
"v_add_u32 v1,0xffff4000,v1\n"
MF "v[180:183],a[204:207],a[180:183],v[180:183]\n"
"s_waitcnt vmcnt(25) lgkmcnt(0)\n"
MF "v[180:183],a[200:203],a[140:143],v[180:183]\n"
"s_barrier\n"
MF "v[216:219],a[208:211],a[140:143],v[216:219]\n"
TR "a[200:201],v0\n"
MF "v[216:219],a[212:215],a[180:183],v[216:219]\n"
"s_add_u32 m0,s62,81920\n"
TR "a[202:203],v0 offset:512\n"
MF "v[224:227],a[212:215],a[184:187],v[224:227]\n"
"s_add_u32 s82,s66,5505280\n"
"ds_read_b128 a[112:115],v3 offset:6144\n"
MF "v[224:227],a[208:211],a[144:147],v[224:227]\n"
"s_sub_u32 s15,s82,7864320\n"
TR "a[204:205],v0 offset:4096\n"
MF "v[232:235],a[208:211],a[148:151],v[232:235]\n"
"s_min_u32 s82,s82,s15\n"
TR "a[206:207],v0 offset:4608\n"
MF "v[232:235],a[212:215],a[188:191],v[232:235]\n"
LD "v6,s[28:31],s82 offen lds\n"
MF "v[240:243],a[212:215],a[192:195],v[240:243]\n"
"ds_read_b128 a[76:79],v2 offset:8192\n"
MF "v[240:243],a[208:211],a[152:155],v[240:243]\n"
"ds_read_b128 a[116:119],v3 offset:8192\n"
MF "v[248:251],a[208:211],a[156:159],v[248:251]\n"
LD "v11,s[28:31],s82 offen offset:1024 lds\n"
MF "v[248:251],a[212:215],a[196:199],v[248:251]\n"
MF "v[252:255],a[220:223],a[196:199],v[252:255]\n"
TR "a[208:209],v0 offset:8\n"
MF "v[252:255],a[216:219],a[156:159],v[252:255]\n"
TR "a[210:211],v0 offset:520\n"
MF "v[244:247],a[216:219],a[152:155],v[244:247]\n"
MF "v[244:247],a[220:223],a[192:195],v[244:247]\n"
TR "a[212:213],v0 offset:4104\n"
MF "v[236:239],a[220:223],a[188:191],v[236:239]\n"
TR "a[214:215],v0 offset:4616\n"
MF "v[236:239],a[216:219],a[148:151],v[236:239]\n"
LD "v12,s[28:31],s82 offen offset:2048 lds\n"
MF "v[228:231],a[216:219],a[144:147],v[228:231]\n"
MF "v[228:231],a[220:223],a[184:187],v[228:231]\n"
MF "v[220:223],a[220:223],a[180:183],v[220:223]\n"
MF "v[220:223],a[216:219],a[140:143],v[220:223]\n"
"s_waitcnt lgkmcnt(4)\n"
MF "v[16:19],a[200:203],a[60:63],v[16:19]\n"
TR "a[216:217],v1\n"
MF "v[16:19],a[204:207],a[100:103],v[16:19]\n"
TR "a[218:219],v1 offset:512\n"
MF "v[24:27],a[204:207],a[104:107],v[24:27]\n"
LD "v13,s[28:31],s82 offen offset:3072 lds\n"
MF "v[24:27],a[200:203],a[64:67],v[24:27]\n"
TR "a[220:221],v1 offset:4096\n"
MF "v[32:35],a[200:203],a[68:71],v[32:35]\n"
TR "a[222:223],v1 offset:4608\n"
MF "v[32:35],a[204:207],a[108:111],v[32:35]\n"
MF "v[40:43],a[204:207],a[112:115],v[40:43]\n"
MF "v[40:43],a[200:203],a[72:75],v[40:43]\n"
"s_add_u32 s84,s14,40960\n"
MF "v[48:51],a[200:203],a[76:79],v[48:51]\n"
"s_add_u32 m0,s63,45056\n"
MF "v[48:51],a[204:207],a[116:119],v[48:51]\n"
"s_waitcnt lgkmcnt(4)\n"
LD "v4,s[24:27],s84 offen lds\n"
MF "v[52:55],a[212:215],a[116:119],v[52:55]\n"
TR "a[200:201],v1 offset:8\n"
MF "v[52:55],a[208:211],a[76:79],v[52:55]\n"
TR "a[202:203],v1 offset:520\n"
MF "v[44:47],a[208:211],a[72:75],v[44:47]\n"
MF "v[44:47],a[212:215],a[112:115],v[44:47]\n"
TR "a[204:205],v1 offset:4104\n"
MF "v[36:39],a[212:215],a[108:111],v[36:39]\n"
TR "a[206:207],v1 offset:4616\n"
MF "v[36:39],a[208:211],a[68:71],v[36:39]\n"
MF "v[28:31],a[208:211],a[64:67],v[28:31]\n"
MF "v[28:31],a[212:215],a[104:107],v[28:31]\n"
MF "v[20:23],a[212:215],a[100:103],v[20:23]\n"
"s_waitcnt vmcnt(24) lgkmcnt(0)\n"
MF "v[20:23],a[208:211],a[60:63],v[20:23]\n"
"s_barrier\n"
MF "v[56:59],a[216:219],a[60:63],v[56:59]\n"
"s_add_u32 m0,s62,0\n"
"s_add_u32 s82,s66,6291712\n"
TR "a[208:209],v0 offset:16384\n"
MF "v[56:59],a[220:223],a[100:103],v[56:59]\n"
"s_sub_u32 s15,s82,7864320\n"
"s_min_u32 s82,s82,s15\n"
TR "a[210:211],v0 offset:16896\n"
MF "v[64:67],a[220:223],a[104:107],v[64:67]\n"
LD "v6,s[28:31],s82 offen lds\n"
MF "v[64:67],a[216:219],a[64:67],v[64:67]\n"
TR "a[212:213],v0 offset:20480\n"
MF "v[72:75],a[216:219],a[68:71],v[72:75]\n"
TR "a[214:215],v0 offset:20992\n"
MF "v[72:75],a[220:223],a[108:111],v[72:75]\n"
MF "v[80:83],a[220:223],a[112:115],v[80:83]\n"
LD "v11,s[28:31],s82 offen offset:1024 lds\n"
MF "v[80:83],a[216:219],a[72:75],v[80:83]\n"
MF "v[88:91],a[216:219],a[76:79],v[88:91]\n"
MF "v[88:91],a[220:223],a[116:119],v[88:91]\n"
MF "v[92:95],a[204:207],a[116:119],v[92:95]\n"
TR "a[216:217],v0 offset:16392\n"
MF "v[92:95],a[200:203],a[76:79],v[92:95]\n"
TR "a[218:219],v0 offset:16904\n"
MF "v[84:87],a[200:203],a[72:75],v[84:87]\n"
LD "v12,s[28:31],s82 offen offset:2048 lds\n"
MF "v[84:87],a[204:207],a[112:115],v[84:87]\n"
TR "a[220:221],v0 offset:20488\n"
MF "v[76:79],a[204:207],a[108:111],v[76:79]\n"
TR "a[222:223],v0 offset:21000\n"
MF "v[76:79],a[200:203],a[68:71],v[76:79]\n"
MF "v[68:71],a[200:203],a[64:67],v[68:71]\n"
LD "v13,s[28:31],s82 offen offset:3072 lds\n"
MF "v[68:71],a[204:207],a[104:107],v[68:71]\n"
MF "v[60:63],a[204:207],a[100:103],v[60:63]\n"
MF "v[60:63],a[200:203],a[60:63],v[60:63]\n"
"s_add_u32 s14,s81,1152\n"
"s_sub_u32 s15,s14,1280\n"
"s_waitcnt lgkmcnt(4)\n"
MF "v[96:99],a[208:211],a[60:63],v[96:99]\n"
"s_min_u32 s14,s14,s15\n"
"s_add_u32 s14,s14,s68\n"
TR "a[200:201],v1 offset:16384\n"
MF "v[96:99],a[212:215],a[100:103],v[96:99]\n"
"s_add_u32 s84,s14,0\n"
"s_add_u32 m0,s63,0\n"
TR "a[202:203],v1 offset:16896\n"
MF "v[104:107],a[212:215],a[104:107],v[104:107]\n"
LD "v4,s[24:27],s84 offen lds\n"
MF "v[104:107],a[208:211],a[64:67],v[104:107]\n"
TR "a[204:205],v1 offset:20480\n"
MF "v[112:115],a[208:211],a[68:71],v[112:115]\n"
TR "a[206:207],v1 offset:20992\n"
MF "v[112:115],a[212:215],a[108:111],v[112:115]\n"
"s_add_u32 s84,s14,9216\n"
MF "v[120:123],a[212:215],a[112:115],v[120:123]\n"
LD "v5,s[24:27],s84 offen offset:1024 lds\n"
MF "v[120:123],a[208:211],a[72:75],v[120:123]\n"
MF "v[128:131],a[208:211],a[76:79],v[128:131]\n"
MF "v[128:131],a[212:215],a[116:119],v[128:131]\n"
"s_waitcnt lgkmcnt(4)\n"
MF "v[132:135],a[220:223],a[116:119],v[132:135]\n"
TR "a[208:209],v1 offset:16392\n"
MF "v[132:135],a[216:219],a[76:79],v[132:135]\n"
"s_add_u32 s84,s14,18432\n"
TR "a[210:211],v1 offset:16904\n"
MF "v[124:127],a[216:219],a[72:75],v[124:127]\n"
LD "v4,s[24:27],s84 offen offset:2048 lds\n"
MF "v[124:127],a[220:223],a[112:115],v[124:127]\n"
TR "a[212:213],v1 offset:20488\n"
MF "v[116:119],a[220:223],a[108:111],v[116:119]\n"
TR "a[214:215],v1 offset:21000\n"
MF "v[116:119],a[216:219],a[68:71],v[116:119]\n"
MF "v[108:111],a[216:219],a[64:67],v[108:111]\n"
MF "v[108:111],a[220:223],a[104:107],v[108:111]\n"
MF "v[100:103],a[220:223],a[100:103],v[100:103]\n"
"s_waitcnt vmcnt(24) lgkmcnt(0)\n"
MF "v[100:103],a[216:219],a[60:63],v[100:103]\n"
"s_barrier\n"
MF "v[136:139],a[200:203],a[60:63],v[136:139]\n"
TR "a[216:217],v0 offset:32768\n"
MF "v[136:139],a[204:207],a[100:103],v[136:139]\n"
"s_add_u32 m0,s62,16384\n"
TR "a[218:219],v0 offset:33280\n"
MF "v[144:147],a[204:207],a[104:107],v[144:147]\n"
"s_add_u32 s82,s66,7078144\n"
"ds_read_b128 a[140:143],v2 offset:20480\n"
MF "v[144:147],a[200:203],a[64:67],v[144:147]\n"
"s_sub_u32 s15,s82,7864320\n"
TR "a[220:221],v0 offset:36864\n"
MF "v[152:155],a[200:203],a[68:71],v[152:155]\n"
"s_min_u32 s82,s82,s15\n"
TR "a[222:223],v0 offset:37376\n"
MF "v[152:155],a[204:207],a[108:111],v[152:155]\n"
LD "v6,s[28:31],s82 offen lds\n"
MF "v[160:163],a[204:207],a[112:115],v[160:163]\n"
"ds_read_b128 a[180:183],v3 offset:20480\n"
MF "v[160:163],a[200:203],a[72:75],v[160:163]\n"
LD "v11,s[28:31],s82 offen offset:1024 lds\n"
MF "v[168:171],a[200:203],a[76:79],v[168:171]\n"
MF "v[168:171],a[204:207],a[116:119],v[168:171]\n"
LD "v12,s[28:31],s82 offen offset:2048 lds\n"
MF "v[172:175],a[212:215],a[116:119],v[172:175]\n"
TR "a[200:201],v0 offset:32776\n"
MF "v[172:175],a[208:211],a[76:79],v[172:175]\n"
TR "a[202:203],v0 offset:33288\n"
MF "v[164:167],a[208:211],a[72:75],v[164:167]\n"
"ds_read_b128 a[144:147],v2 offset:22528\n"
MF "v[164:167],a[212:215],a[112:115],v[164:167]\n"
TR "a[204:205],v0 offset:36872\n"
MF "v[156:159],a[212:215],a[108:111],v[156:159]\n"
TR "a[206:207],v0 offset:37384\n"
MF "v[156:159],a[208:211],a[68:71],v[156:159]\n"
MF "v[148:151],a[208:211],a[64:67],v[148:151]\n"
"ds_read_b128 a[184:187],v3 offset:22528\n"
MF "v[148:151],a[212:215],a[104:107],v[148:151]\n"
MF "v[140:143],a[212:215],a[100:103],v[140:143]\n"
LD "v13,s[28:31],s82 offen offset:3072 lds\n"
MF "v[140:143],a[208:211],a[60:63],v[140:143]\n"
"s_waitcnt lgkmcnt(7)\n"
MF "v[176:179],a[216:219],a[60:63],v[176:179]\n"
TR "a[208:209],v1 offset:32768\n"
MF "v[176:179],a[220:223],a[100:103],v[176:179]\n"
TR "a[210:211],v1 offset:33280\n"
MF "v[184:187],a[220:223],a[104:107],v[184:187]\n"
"ds_read_b128 a[148:151],v2 offset:24576\n"
MF "v[184:187],a[216:219],a[64:67],v[184:187]\n"
"s_add_u32 s84,s14,30720\n"
TR "a[212:213],v1 offset:36864\n"
MF "v[192:195],a[216:219],a[68:71],v[192:195]\n"
"s_add_u32 m0,s63,3072\n"
TR "a[214:215],v1 offset:37376\n"
MF "v[192:195],a[220:223],a[108:111],v[192:195]\n"
LD "v5,s[24:27],s84 offen lds\n"
MF "v[200:203],a[220:223],a[112:115],v[200:203]\n"
"ds_read_b128 a[188:191],v3 offset:24576\n"
MF "v[200:203],a[216:219],a[72:75],v[200:203]\n"
MF "v[208:211],a[216:219],a[76:79],v[208:211]\n"
"s_add_u32 s84,s14,39936\n"
MF "v[208:211],a[220:223],a[116:119],v[208:211]\n"
"s_waitcnt lgkmcnt(7)\n"
LD "v4,s[24:27],s84 offen offset:1024 lds\n"
MF "v[212:215],a[204:207],a[116:119],v[212:215]\n"
TR "a[216:217],v1 offset:32776\n"
MF "v[212:215],a[200:203],a[76:79],v[212:215]\n"
TR "a[218:219],v1 offset:33288\n"
MF "v[204:207],a[200:203],a[72:75],v[204:207]\n"
"ds_read_b128 a[152:155],v2 offset:26624\n"
MF "v[204:207],a[204:207],a[112:115],v[204:207]\n"
TR "a[220:221],v1 offset:36872\n"
MF "v[196:199],a[204:207],a[108:111],v[196:199]\n"
TR "a[222:223],v1 offset:37384\n"
MF "v[196:199],a[200:203],a[68:71],v[196:199]\n"
MF "v[188:191],a[200:203],a[64:67],v[188:191]\n"
"v_add_u32 v0,0xc000,v0\n"
MF "v[188:191],a[204:207],a[104:107],v[188:191]\n"
"v_add_u32 v1,0xc000,v1\n"
MF "v[180:183],a[204:207],a[100:103],v[180:183]\n"
"s_waitcnt vmcnt(26) lgkmcnt(0)\n"
".Lpgqj_a:\n"
"s_cbranch_execz .Lisr1\n"
".Lqj:\n"
MF "v[180:183],a[200:203],a[60:63],v[180:183]\n"
"s_barrier\n"
"s_getpc_b64 s[52:53]\n"
".Lctq:\n"
"s_sub_u32 s52,s52,.Lctq-.Lcode0\n"
"s_subb_u32 s53,s53,0\n"
"s_bfe_u32 s58,s23,0x50000\n"
"s_lshl_b32 s58,s58,2\n"
"s_add_u32 s58,s58,s56\n"
"s_nop 0\n"
"s_mul_i32 s58,s58,1280\n"
"v_min_u32 v15,144,v9\n"
"v_lshl_add_u32 v15,v15,3,s58\n"
"v_min_u32 v15,.Lcode1-.Lcode0,v15\n"
"global_load_dword a249,v15,s[52:53]\n"
MF "v[216:219],a[208:211],a[60:63],v[216:219]\n"
TR "a[200:201],v0\n"
MF "v[216:219],a[212:215],a[100:103],v[216:219]\n"
"s_add_u32 m0,s62,32768\n"
TR "a[202:203],v0 offset:512\n"
MF "v[224:227],a[212:215],a[104:107],v[224:227]\n"
"s_add_u32 s82,s66,6291584\n"
"ds_read_b128 a[192:195],v3 offset:26624\n"
MF "v[224:227],a[208:211],a[64:67],v[224:227]\n"
"s_sub_u32 s15,s82,7864320\n"
TR "a[204:205],v0 offset:4096\n"
MF "v[232:235],a[208:211],a[68:71],v[232:235]\n"
"s_min_u32 s82,s82,s15\n"
TR "a[206:207],v0 offset:4608\n"
MF "v[232:235],a[212:215],a[108:111],v[232:235]\n"
LD "v6,s[28:31],s82 offen lds\n"
MF "v[240:243],a[212:215],a[112:115],v[240:243]\n"
"ds_read_b128 a[156:159],v2 offset:28672\n"
MF "v[240:243],a[208:211],a[72:75],v[240:243]\n"
"ds_read_b128 a[196:199],v3 offset:28672\n"
MF "v[248:251],a[208:211],a[76:79],v[248:251]\n"
MF "v[248:251],a[212:215],a[116:119],v[248:251]\n"
LD "v11,s[28:31],s82 offen offset:1024 lds\n"
MF "v[252:255],a[220:223],a[116:119],v[252:255]\n"
TR "a[208:209],v0 offset:8\n"
MF "v[252:255],a[216:219],a[76:79],v[252:255]\n"
TR "a[210:211],v0 offset:520\n"
MF "v[244:247],a[216:219],a[72:75],v[244:247]\n"
MF "v[244:247],a[220:223],a[112:115],v[244:247]\n"
TR "a[212:213],v0 offset:4104\n"
MF "v[236:239],a[220:223],a[108:111],v[236:239]\n"
TR "a[214:215],v0 offset:4616\n"
MF "v[236:239],a[216:219],a[68:71],v[236:239]\n"
MF "v[228:231],a[216:219],a[64:67],v[228:231]\n"
MF "v[228:231],a[220:223],a[104:107],v[228:231]\n"
MF "v[220:223],a[220:223],a[100:103],v[220:223]\n"
LD "v12,s[28:31],s82 offen offset:2048 lds\n"
MF "v[220:223],a[216:219],a[60:63],v[220:223]\n"
"s_waitcnt lgkmcnt(4)\n"
MF "v[16:19],a[200:203],a[140:143],v[16:19]\n"
TR "a[216:217],v1\n"
MF "v[16:19],a[204:207],a[180:183],v[16:19]\n"
TR "a[218:219],v1 offset:512\n"
MF "v[24:27],a[204:207],a[184:187],v[24:27]\n"
MF "v[24:27],a[200:203],a[144:147],v[24:27]\n"
TR "a[220:221],v1 offset:4096\n"
MF "v[32:35],a[200:203],a[148:151],v[32:35]\n"
TR "a[222:223],v1 offset:4608\n"
MF "v[32:35],a[204:207],a[188:191],v[32:35]\n"
MF "v[40:43],a[204:207],a[192:195],v[40:43]\n"
MF "v[40:43],a[200:203],a[152:155],v[40:43]\n"
LD "v13,s[28:31],s82 offen offset:3072 lds\n"
MF "v[48:51],a[200:203],a[156:159],v[48:51]\n"
"s_memtime s[52:53]\n"
"s_memrealtime s[100:101]\n"
MF "v[48:51],a[204:207],a[196:199],v[48:51]\n"
"s_waitcnt lgkmcnt(4)\n"
MF "v[52:55],a[212:215],a[196:199],v[52:55]\n"
TR "a[200:201],v1 offset:8\n"
MF "v[52:55],a[208:211],a[156:159],v[52:55]\n"
TR "a[202:203],v1 offset:520\n"
MF "v[44:47],a[208:211],a[152:155],v[44:47]\n"
MF "v[44:47],a[212:215],a[192:195],v[44:47]\n"
TR "a[204:205],v1 offset:4104\n"
MF "v[36:39],a[212:215],a[188:191],v[36:39]\n"
TR "a[206:207],v1 offset:4616\n"
MF "v[36:39],a[208:211],a[148:151],v[36:39]\n"
MF "v[28:31],a[208:211],a[144:147],v[28:31]\n"
MF "v[28:31],a[212:215],a[184:187],v[28:31]\n"
MF "v[20:23],a[212:215],a[180:183],v[20:23]\n"
"s_waitcnt vmcnt(25) lgkmcnt(0)\n"
MF "v[20:23],a[208:211],a[140:143],v[20:23]\n"
"s_barrier\n"
MF "v[56:59],a[216:219],a[140:143],v[56:59]\n"
"s_add_u32 m0,s62,49152\n"
"s_add_u32 s82,s66,7078016\n"
TR "a[208:209],v0 offset:16384\n"
MF "v[56:59],a[220:223],a[180:183],v[56:59]\n"
"s_sub_u32 s15,s82,7864320\n"
"s_min_u32 s82,s82,s15\n"
TR "a[210:211],v0 offset:16896\n"
MF "v[64:67],a[220:223],a[184:187],v[64:67]\n"
LD "v6,s[28:31],s82 offen lds\n"
MF "v[64:67],a[216:219],a[144:147],v[64:67]\n"
"s_sub_u32 s53,s52,s80\n"
TR "a[212:213],v0 offset:20480\n"
"v_lshrrev_b32 v15,8,v9\n"
MF "v[72:75],a[216:219],a[148:151],v[72:75]\n"
TR "a[214:215],v0 offset:20992\n"
"v_mul_u32_u24 v15,48960,v15\n"
MF "v[72:75],a[220:223],a[188:191],v[72:75]\n"
"s_lshr_b32 s7,s56,1\n"
"v_add_u32 v15,v15,v9\n"
MF "v[80:83],a[220:223],a[192:195],v[80:83]\n"
"s_and_b32 s8,s56,1\n"
MF "v[80:83],a[216:219],a[152:155],v[80:83]\n"
"s_sub_u32 s7,s7,s8\n"
MF "v[88:91],a[216:219],a[156:159],v[88:91]\n"
"s_mul_i32 s7,s7,245760\n"
MF "v[88:91],a[220:223],a[196:199],v[88:91]\n"
LD "v11,s[28:31],s82 offen offset:1024 lds\n"
MF "v[92:95],a[204:207],a[196:199],v[92:95]\n"
"s_add_u32 s7,s7,s89\n"
TR "a[216:217],v0 offset:16392\n"
MF "v[92:95],a[200:203],a[156:159],v[92:95]\n"
"s_cmp_gt_u32 s53,24100\n"
"s_cselect_b32 s101,1,0\n"
TR "a[218:219],v0 offset:16904\n"
MF "v[84:87],a[200:203],a[152:155],v[84:87]\n"
"s_cmp_eq_u32 s87,2\n"
"s_cselect_b32 s87,s101,s87\n"
MF "v[84:87],a[204:207],a[192:195],v[84:87]\n"
TR "a[220:221],v0 offset:20488\n"
MF "v[76:79],a[204:207],a[188:191],v[76:79]\n"
TR "a[222:223],v0 offset:21000\n"
MF "v[76:79],a[200:203],a[148:151],v[76:79]\n"
"s_cmp_gt_u32 s87,s57\n"
"s_cselect_b32 s97,130,0\n"
"s_cselect_b32 s95,0,0x47800000\n"
MF "v[68:71],a[200:203],a[144:147],v[68:71]\n"
"s_mov_b32 s21,s100\n"
MF "v[68:71],a[204:207],a[184:187],v[68:71]\n"
"s_mov_b32 s77,s95\n"
MF "v[60:63],a[204:207],a[180:183],v[60:63]\n"
LD "v12,s[28:31],s82 offen offset:2048 lds\n"
MF "v[60:63],a[200:203],a[140:143],v[60:63]\n"
"s_waitcnt lgkmcnt(4)\n"
MF "v[96:99],a[208:211],a[140:143],v[96:99]\n"
".Lpg7a:\n"
"s_cbranch_execz .Lisr1\n"
TR "a[200:201],v1 offset:16384\n"
MF "v[96:99],a[212:215],a[180:183],v[96:99]\n"
TR "a[202:203],v1 offset:16896\n"
MF "v[104:107],a[212:215],a[184:187],v[104:107]\n"
MF "v[104:107],a[208:211],a[144:147],v[104:107]\n"
TR "a[204:205],v1 offset:20480\n"
MF "v[112:115],a[208:211],a[148:151],v[112:115]\n"
TR "a[206:207],v1 offset:20992\n"
MF "v[112:115],a[212:215],a[188:191],v[112:115]\n"
MF "v[120:123],a[212:215],a[192:195],v[120:123]\n"
MF "v[120:123],a[208:211],a[152:155],v[120:123]\n"
LD "v13,s[28:31],s82 offen offset:3072 lds\n"
MF "v[128:131],a[208:211],a[156:159],v[128:131]\n"
MF "v[128:131],a[212:215],a[196:199],v[128:131]\n"
"s_waitcnt lgkmcnt(4)\n"
MF "v[132:135],a[220:223],a[196:199],v[132:135]\n"
TR "a[208:209],v1 offset:16392\n"
MF "v[132:135],a[216:219],a[156:159],v[132:135]\n"
TR "a[210:211],v1 offset:16904\n"
MF "v[124:127],a[216:219],a[152:155],v[124:127]\n"
MF "v[124:127],a[220:223],a[192:195],v[124:127]\n"
TR "a[212:213],v1 offset:20488\n"
MF "v[116:119],a[220:223],a[188:191],v[116:119]\n"
TR "a[214:215],v1 offset:21000\n"
MF "v[116:119],a[216:219],a[148:151],v[116:119]\n"
MF "v[108:111],a[216:219],a[144:147],v[108:111]\n"
MF "v[108:111],a[220:223],a[184:187],v[108:111]\n"
MF "v[100:103],a[220:223],a[180:183],v[100:103]\n"
"s_waitcnt vmcnt(22) lgkmcnt(0)\n"
MF "v[100:103],a[216:219],a[140:143],v[100:103]\n"
"s_barrier\n"
"s_cmp_eq_u32 s57,1\n"
"s_cbranch_scc0 .Lhs15\n"
".Lack_q:\n"
"v_cmp_ne_u32 vcc,s76,v10\n"
"s_cbranch_vccnz .Lackspin_q\n"
".Lackok_q:\n"
".Lhs15:\n"
MF "v[136:139],a[200:203],a[140:143],v[136:139]\n"
"s_and_b32 s58,s56,1\n"
TR "a[216:217],v0 offset:32768\n"
MF "v[136:139],a[204:207],a[180:183],v[136:139]\n"
"s_add_u32 m0,s62,65536\n"
TR "a[218:219],v0 offset:33280\n"
MF "v[144:147],a[204:207],a[184:187],v[144:147]\n"
"s_add_u32 s82,s66,6291456\n"
"ds_read_b128 a[60:63],v2 offset:40960\n"
MF "v[144:147],a[200:203],a[144:147],v[144:147]\n"
"s_sub_u32 s15,s82,7864320\n"
TR "a[220:221],v0 offset:36864\n"
MF "v[152:155],a[200:203],a[148:151],v[152:155]\n"
"s_min_u32 s82,s82,s15\n"
TR "a[222:223],v0 offset:37376\n"
MF "v[152:155],a[204:207],a[188:191],v[152:155]\n"
LD "v6,s[28:31],s82 offen lds\n"
MF "v[160:163],a[204:207],a[192:195],v[160:163]\n"
"s_mul_i32 s59,s58,3072\n"
"ds_read_b128 a[100:103],v3 offset:40960\n"
MF "v[160:163],a[200:203],a[152:155],v[160:163]\n"
"s_add_u32 s16,s70,s59\n"
MF "v[168:171],a[200:203],a[156:159],v[168:171]\n"
"s_mul_i32 s59,s58,6144\n"
MF "v[168:171],a[204:207],a[196:199],v[168:171]\n"
LD "v11,s[28:31],s82 offen offset:1024 lds\n"
MF "v[172:175],a[212:215],a[196:199],v[172:175]\n"
"s_add_u32 s18,s70,s59\n"
TR "a[200:201],v0 offset:32776\n"
MF "v[172:175],a[208:211],a[156:159],v[172:175]\n"
"s_add_u32 s17,s16,196608\n"
TR "a[202:203],v0 offset:33288\n"
MF "v[164:167],a[208:211],a[152:155],v[164:167]\n"
"s_add_u32 s19,s18,196608\n"
"ds_read_b128 a[64:67],v2 offset:43008\n"
MF "v[164:167],a[212:215],a[192:195],v[164:167]\n"
"s_add_u32 s20,s70,196608\n"
TR "a[204:205],v0 offset:36872\n"
MF "v[156:159],a[212:215],a[188:191],v[156:159]\n"
"s_bfe_u32 s58,s23,0x40008\n"
TR "a[206:207],v0 offset:37384\n"
MF "v[156:159],a[208:211],a[148:151],v[156:159]\n"
"s_lshr_b32 s59,s56,1\n"
MF "v[148:151],a[208:211],a[144:147],v[148:151]\n"
"s_mul_i32 s59,s59,5\n"
"ds_read_b128 a[104:107],v3 offset:43008\n"
MF "v[148:151],a[212:215],a[184:187],v[148:151]\n"
"s_add_u32 s58,s58,s59\n"
MF "v[140:143],a[212:215],a[180:183],v[140:143]\n"
LD "v12,s[28:31],s82 offen offset:2048 lds\n"
MF "v[140:143],a[208:211],a[140:143],v[140:143]\n"
"s_waitcnt lgkmcnt(7)\n"
MF "v[176:179],a[216:219],a[140:143],v[176:179]\n"
"s_and_b32 s58,s58,15\n"
TR "a[208:209],v1 offset:32768\n"
MF "v[176:179],a[220:223],a[180:183],v[176:179]\n"
"s_mul_i32 s58,s58,12288\n"
TR "a[210:211],v1 offset:33280\n"
MF "v[184:187],a[220:223],a[184:187],v[184:187]\n"
"s_add_u32 s72,s16,s58\n"
"ds_read_b128 a[68:71],v2 offset:45056\n"
MF "v[184:187],a[216:219],a[144:147],v[184:187]\n"
"s_add_u32 s73,s18,s58\n"
TR "a[212:213],v1 offset:36864\n"
MF "v[192:195],a[216:219],a[148:151],v[192:195]\n"
"s_add_u32 s74,s70,s58\n"
TR "a[214:215],v1 offset:37376\n"
MF "v[192:195],a[220:223],a[188:191],v[192:195]\n"
"s_add_u32 s74,s74,12288\n"
MF "v[200:203],a[220:223],a[192:195],v[200:203]\n"
"s_cmp_eq_u32 s74,s20\n"
"s_cselect_b32 s74,s70,s74\n"
"ds_read_b128 a[108:111],v3 offset:45056\n"
MF "v[200:203],a[216:219],a[152:155],v[200:203]\n"
LD "v13,s[28:31],s82 offen offset:3072 lds\n"
MF "v[208:211],a[216:219],a[156:159],v[208:211]\n"
"s_lshr_b32 s58,s56,1\n"
MF "v[208:211],a[220:223],a[196:199],v[208:211]\n"
"s_waitcnt lgkmcnt(7)\n"
MF "v[212:215],a[204:207],a[196:199],v[212:215]\n"
"s_mul_i32 s58,s58,6144\n"
TR "a[216:217],v1 offset:32776\n"
MF "v[212:215],a[200:203],a[156:159],v[212:215]\n"
"s_mul_i32 s64,s56,3072\n"
TR "a[218:219],v1 offset:33288\n"
MF "v[204:207],a[200:203],a[152:155],v[204:207]\n"
"s_add_u32 s64,s64,s58\n"
"ds_read_b128 a[72:75],v2 offset:47104\n"
MF "v[204:207],a[204:207],a[192:195],v[204:207]\n"
"s_mul_i32 s65,s56,6144\n"
TR "a[220:221],v1 offset:36872\n"
MF "v[196:199],a[204:207],a[188:191],v[196:199]\n"
"s_sub_u32 s58,s71,s70\n"
TR "a[222:223],v1 offset:37384\n"
MF "v[196:199],a[200:203],a[148:151],v[196:199]\n"
"s_add_u32 s96,s74,s58\n"
MF "v[188:191],a[200:203],a[144:147],v[188:191]\n"
"s_sub_u32 s58,s16,s70\n"
"v_add_u32 v0,0xffff4000,v0\n"
MF "v[188:191],a[204:207],a[184:187],v[188:191]\n"
"s_add_u32 s96,s96,s58\n"
"v_add_u32 v1,0xffff4000,v1\n"
MF "v[180:183],a[204:207],a[180:183],v[180:183]\n"
"s_waitcnt vmcnt(13) lgkmcnt(0)\n"
MF "v[180:183],a[200:203],a[140:143],v[180:183]\n"
"s_barrier\n"
MF "v[216:219],a[208:211],a[140:143],v[216:219]\n"
"s_max_u32 s60,s70,s71\n"
TR "a[200:201],v1 offset:8\n"
MF "v[216:219],a[212:215],a[180:183],v[216:219]\n"
"s_add_u32 m0,s62,81920\n"
TR "a[202:203],v1 offset:520\n"
"v_mov_b32 v14,s12\n"
MF "v[224:227],a[212:215],a[184:187],v[224:227]\n"
"s_add_u32 s82,s66,7077888\n"
"ds_read_b128 a[112:115],v3 offset:47104\n"
"v_mul_f32 v14,0xbdd53b95,v14\n"
MF "v[224:227],a[208:211],a[144:147],v[224:227]\n"
"s_sub_u32 s15,s82,7864320\n"
TR "a[204:205],v1 offset:4104\n"
MF "v[232:235],a[208:211],a[148:151],v[232:235]\n"
"s_min_u32 s82,s82,s15\n"
TR "a[206:207],v1 offset:4616\n"
MF "v[232:235],a[212:215],a[188:191],v[232:235]\n"
LD "v6,s[28:31],s82 offen lds\n"
MF "v[240:243],a[212:215],a[192:195],v[240:243]\n"
"s_add_u32 s34,s60,196608\n"
"ds_read_b128 a[76:79],v2 offset:49152\n"
MF "v[240:243],a[208:211],a[152:155],v[240:243]\n"
"s_add_u32 s38,s60,196608\n"
"ds_read_b128 a[116:119],v3 offset:49152\n"
MF "v[248:251],a[208:211],a[156:159],v[248:251]\n"
MF "v[248:251],a[212:215],a[196:199],v[248:251]\n"
LD "v11,s[28:31],s82 offen offset:1024 lds\n"
MF "v[220:223],a[216:219],a[140:143],v[220:223]\n"
TR "a[208:209],v1 offset:16392\n"
MF "v[220:223],a[220:223],a[180:183],v[220:223]\n"
TR "a[210:211],v1 offset:16904\n"
MF "v[228:231],a[220:223],a[184:187],v[228:231]\n"
"ds_read_b128 a[140:143],v2\n"
MF "v[228:231],a[216:219],a[144:147],v[228:231]\n"
TR "a[212:213],v1 offset:20488\n"
MF "v[236:239],a[216:219],a[148:151],v[236:239]\n"
TR "a[214:215],v1 offset:21000\n"
MF "v[236:239],a[220:223],a[188:191],v[236:239]\n"
"ds_read_b128 a[180:183],v3\n"
MF "v[244:247],a[220:223],a[192:195],v[244:247]\n"
"ds_read_b128 a[144:147],v2 offset:2048\n"
MF "v[244:247],a[216:219],a[152:155],v[244:247]\n"
"ds_read_b128 a[184:187],v3 offset:2048\n"
MF "v[252:255],a[216:219],a[156:159],v[252:255]\n"
"ds_read_b128 a[148:151],v2 offset:4096\n"
MF "v[252:255],a[220:223],a[196:199],v[252:255]\n"
"s_waitcnt lgkmcnt(9)\n"
"ds_read_b128 a[188:191],v3 offset:4096\n"
MF "v[220:223],a[200:203],a[60:63],v[220:223]\n"
TR "a[216:217],v1\n"
MF "v[220:223],a[204:207],a[100:103],v[220:223]\n"
TR "a[218:219],v1 offset:512\n"
MF "v[228:231],a[204:207],a[104:107],v[228:231]\n"
"ds_read_b128 a[152:155],v2 offset:6144\n"
MF "v[228:231],a[200:203],a[64:67],v[228:231]\n"
TR "a[220:221],v1 offset:4096\n"
MF "v[236:239],a[200:203],a[68:71],v[236:239]\n"
TR "a[222:223],v1 offset:4608\n"
MF "v[236:239],a[204:207],a[108:111],v[236:239]\n"
"ds_read_b128 a[192:195],v3 offset:6144\n"
MF "v[244:247],a[204:207],a[112:115],v[244:247]\n"
"ds_read_b128 a[156:159],v2 offset:8192\n"
MF "v[244:247],a[200:203],a[72:75],v[244:247]\n"
"ds_read_b128 a[196:199],v3 offset:8192\n"
MF "v[252:255],a[200:203],a[76:79],v[252:255]\n"
LD "v12,s[28:31],s82 offen offset:2048 lds\n"
MF "v[252:255],a[204:207],a[116:119],v[252:255]\n"
"s_waitcnt lgkmcnt(8)\n"
LD "v13,s[28:31],s82 offen offset:3072 lds\n"
MF "v[220:223],a[208:211],a[140:143],v[220:223]\n"
TR "a[200:201],v1 offset:16384\n"
MF "v[220:223],a[212:215],a[180:183],v[220:223]\n"
TR "a[202:203],v1 offset:16896\n"
MF "v[228:231],a[212:215],a[184:187],v[228:231]\n"
MF "v[228:231],a[208:211],a[144:147],v[228:231]\n"
TR "a[204:205],v1 offset:20480\n"
MF "v[236:239],a[208:211],a[148:151],v[236:239]\n"
TR "a[206:207],v1 offset:20992\n"
MF "v[236:239],a[212:215],a[188:191],v[236:239]\n"
"s_waitcnt lgkmcnt(4)\n"
MF "v[244:247],a[212:215],a[192:195],v[244:247]\n"
MF "v[244:247],a[208:211],a[152:155],v[244:247]\n"
MF "v[252:255],a[208:211],a[156:159],v[252:255]\n"
MF "v[252:255],a[212:215],a[196:199],v[252:255]\n"
"s_waitcnt lgkmcnt(0)\n"
"s_cmp_eq_u32 s69,98\n"
"s_cbranch_scc1 .Lqy\n"
".Lqf:\n"
MF "v[216:219],a[216:219],a[60:63],v[216:219]\n"
TR "a[208:209],v0 offset:8\n"
MF "v[216:219],a[220:223],a[100:103],v[216:219]\n"
TR "a[210:211],v0 offset:520\n"
MF "v[224:227],a[220:223],a[104:107],v[224:227]\n"
"buffer_load_dword a224,v15,s[36:39],s70 offen offset:2688 nt\n"
MF "v[224:227],a[216:219],a[64:67],v[224:227]\n"
TR "a[212:213],v0 offset:4104\n"
MF "v[232:235],a[216:219],a[68:71],v[232:235]\n"
TR "a[214:215],v0 offset:4616\n"
MF "v[232:235],a[220:223],a[108:111],v[232:235]\n"
"buffer_load_dword a225,v15,s[36:39],s71 offen offset:2688 nt\n"
MF "v[240:243],a[220:223],a[112:115],v[240:243]\n"
MF "v[240:243],a[216:219],a[72:75],v[240:243]\n"
"buffer_load_dword a226,v15,s[48:51],s7 offen offset:2688\n"
MF "v[248:251],a[216:219],a[76:79],v[248:251]\n"
MF "v[248:251],a[220:223],a[116:119],v[248:251]\n"
MF "v[248:251],a[200:203],a[156:159],v[248:251]\n"
TR "a[216:217],v0 offset:16392\n"
MF "v[248:251],a[204:207],a[196:199],v[248:251]\n"
TR "a[218:219],v0 offset:16904\n"
MF "v[240:243],a[204:207],a[192:195],v[240:243]\n"
MF "v[240:243],a[200:203],a[152:155],v[240:243]\n"
TR "a[220:221],v0 offset:20488\n"
MF "v[232:235],a[200:203],a[148:151],v[232:235]\n"
TR "a[222:223],v0 offset:21000\n"
MF "v[232:235],a[204:207],a[188:191],v[232:235]\n"
MF "v[224:227],a[204:207],a[184:187],v[224:227]\n"
MF "v[224:227],a[200:203],a[144:147],v[224:227]\n"
"v_fma_f32 v248,v248,s54,v14\n"
"v_fma_f32 v249,v249,s54,v14\n"
MF "v[216:219],a[200:203],a[140:143],v[216:219]\n"
"v_fma_f32 v250,v250,s54,v14\n"
"v_fma_f32 v251,v251,s54,v14\n"
MF "v[216:219],a[204:207],a[180:183],v[216:219]\n"
"s_waitcnt lgkmcnt(4)\n"
"v_fma_f32 v252,v252,s54,v14\n"
"v_fma_f32 v253,v253,s54,v14\n"
MF "v[180:183],a[208:211],a[60:63],v[180:183]\n"
TR "a[200:201],v0\n"
"v_fma_f32 v254,v254,s54,v14\n"
MF "v[180:183],a[212:215],a[100:103],v[180:183]\n"
TR "a[202:203],v0 offset:512\n"
"v_fma_f32 v255,v255,s54,v14\n"
MF "v[188:191],a[212:215],a[104:107],v[188:191]\n"
CV "v255,v254,v255\n"
CV "v254,v252,v253\n"
MF "v[188:191],a[208:211],a[64:67],v[188:191]\n"
TR "a[204:205],v0 offset:4096\n"
CV "v253,v250,v251\n"
MF "v[196:199],a[208:211],a[68:71],v[196:199]\n"
TR "a[206:207],v0 offset:4608\n"
CV "v252,v248,v249\n"
MF "v[196:199],a[212:215],a[108:111],v[196:199]\n"
"v_fma_f32 v240,v240,s54,v14\n"
"v_fma_f32 v241,v241,s54,v14\n"
MF "v[204:207],a[212:215],a[112:115],v[204:207]\n"
"v_fma_f32 v242,v242,s54,v14\n"
"v_fma_f32 v243,v243,s54,v14\n"
MF "v[204:207],a[208:211],a[72:75],v[204:207]\n"
"v_fma_f32 v244,v244,s54,v14\n"
"v_fma_f32 v245,v245,s54,v14\n"
MF "v[212:215],a[208:211],a[76:79],v[212:215]\n"
"v_fma_f32 v246,v246,s54,v14\n"
"v_fma_f32 v247,v247,s54,v14\n"
MF "v[212:215],a[212:215],a[116:119],v[212:215]\n"
"s_waitcnt lgkmcnt(4)\n"
CV "v251,v246,v247\n"
CV "v250,v244,v245\n"
MF "v[180:183],a[216:219],a[140:143],v[180:183]\n"
TR "a[208:209],v0 offset:16384\n"
CV "v249,v242,v243\n"
MF "v[180:183],a[220:223],a[180:183],v[180:183]\n"
TR "a[210:211],v0 offset:16896\n"
CV "v248,v240,v241\n"
MF "v[188:191],a[220:223],a[184:187],v[188:191]\n"
"v_fma_f32 v232,v232,s54,v14\n"
"v_fma_f32 v233,v233,s54,v14\n"
MF "v[188:191],a[216:219],a[144:147],v[188:191]\n"
TR "a[212:213],v0 offset:20480\n"
"v_fma_f32 v234,v234,s54,v14\n"
MF "v[196:199],a[216:219],a[148:151],v[196:199]\n"
TR "a[214:215],v0 offset:20992\n"
"v_fma_f32 v235,v235,s54,v14\n"
MF "v[196:199],a[220:223],a[188:191],v[196:199]\n"
"v_fma_f32 v236,v236,s54,v14\n"
"v_fma_f32 v237,v237,s54,v14\n"
MF "v[204:207],a[220:223],a[192:195],v[204:207]\n"
"v_fma_f32 v238,v238,s54,v14\n"
"v_fma_f32 v239,v239,s54,v14\n"
MF "v[204:207],a[216:219],a[152:155],v[204:207]\n"
CV "v247,v238,v239\n"
CV "v246,v236,v237\n"
MF "v[212:215],a[216:219],a[156:159],v[212:215]\n"
"s_waitcnt vmcnt(11) lgkmcnt(0)\n"
CV "v245,v234,v235\n"
CV "v244,v232,v233\n"
MF "v[212:215],a[220:223],a[196:199],v[212:215]\n"
"s_barrier\n"
MF "v[176:179],a[200:203],a[60:63],v[176:179]\n"
TR "a[216:217],v1 offset:32776\n"
"v_fma_f32 v224,v224,s54,v14\n"
MF "v[176:179],a[204:207],a[100:103],v[176:179]\n"
"s_add_u32 m0,s64,0\n"
TR "a[218:219],v1 offset:33288\n"
"v_fma_f32 v225,v225,s54,v14\n"
MF "v[184:187],a[204:207],a[104:107],v[184:187]\n"
LD "v7,s[32:35],s72 offen nt lds\n"
MF "v[184:187],a[200:203],a[64:67],v[184:187]\n"
TR "a[220:221],v1 offset:36872\n"
"v_fma_f32 v226,v226,s54,v14\n"
MF "v[192:195],a[200:203],a[68:71],v[192:195]\n"
TR "a[222:223],v1 offset:37384\n"
"v_fma_f32 v227,v227,s54,v14\n"
MF "v[192:195],a[204:207],a[108:111],v[192:195]\n"
"s_add_u32 m0,s64,896\n"
"v_fma_f32 v228,v228,s54,v14\n"
"v_fma_f32 v229,v229,s54,v14\n"
MF "v[200:203],a[204:207],a[112:115],v[200:203]\n"
LD "v7,s[32:35],s72 offen offset:128 nt lds\n"
MF "v[200:203],a[200:203],a[72:75],v[200:203]\n"
"v_fma_f32 v230,v230,s54,v14\n"
"v_fma_f32 v231,v231,s54,v14\n"
MF "v[208:211],a[200:203],a[76:79],v[208:211]\n"
CV "v243,v230,v231\n"
CV "v242,v228,v229\n"
MF "v[208:211],a[204:207],a[116:119],v[208:211]\n"
CV "v241,v226,v227\n"
CV "v240,v224,v225\n"
MF "v[208:211],a[208:211],a[156:159],v[208:211]\n"
TR "a[200:201],v1 offset:49160\n"
"v_fma_f32 v216,v216,s54,v14\n"
MF "v[208:211],a[212:215],a[196:199],v[208:211]\n"
"s_add_u32 m0,s64,1792\n"
TR "a[202:203],v1 offset:49672\n"
"v_fma_f32 v217,v217,s54,v14\n"
MF "v[200:203],a[212:215],a[192:195],v[200:203]\n"
LD "v7,s[32:35],s72 offen offset:256 nt lds\n"
MF "v[200:203],a[208:211],a[152:155],v[200:203]\n"
TR "a[204:205],v1 offset:53256\n"
"v_fma_f32 v218,v218,s54,v14\n"
MF "v[192:195],a[208:211],a[148:151],v[192:195]\n"
TR "a[206:207],v1 offset:53768\n"
"v_fma_f32 v219,v219,s54,v14\n"
MF "v[192:195],a[212:215],a[188:191],v[192:195]\n"
"v_fma_f32 v220,v220,s54,v14\n"
"v_fma_f32 v221,v221,s54,v14\n"
MF "v[184:187],a[212:215],a[184:187],v[184:187]\n"
"s_add_u32 s72,s72,6144\n"
"v_fma_f32 v222,v222,s54,v14\n"
"v_fma_f32 v223,v223,s54,v14\n"
MF "v[184:187],a[208:211],a[144:147],v[184:187]\n"
"s_add_u32 m0,s64,6144\n"
CV "v239,v222,v223\n"
CV "v238,v220,v221\n"
MF "v[176:179],a[208:211],a[140:143],v[176:179]\n"
LD "v7,s[32:35],s72 offen nt lds\n"
MF "v[176:179],a[212:215],a[180:183],v[176:179]\n"
"s_waitcnt lgkmcnt(4)\n"
CV "v237,v218,v219\n"
CV "v236,v216,v217\n"
MF "v[140:143],a[216:219],a[60:63],v[140:143]\n"
TR "a[208:209],v1 offset:32768\n"
"v_fma_f32 v208,v208,s54,v14\n"
MF "v[140:143],a[220:223],a[100:103],v[140:143]\n"
TR "a[210:211],v1 offset:33280\n"
"v_fma_f32 v209,v209,s54,v14\n"
MF "v[148:151],a[220:223],a[104:107],v[148:151]\n"
"v_fma_f32 v210,v210,s54,v14\n"
"v_fma_f32 v211,v211,s54,v14\n"
MF "v[148:151],a[216:219],a[64:67],v[148:151]\n"
TR "a[212:213],v1 offset:36864\n"
"v_fma_f32 v212,v212,s54,v14\n"
MF "v[156:159],a[216:219],a[68:71],v[156:159]\n"
"s_add_u32 m0,s64,7040\n"
TR "a[214:215],v1 offset:37376\n"
"v_fma_f32 v213,v213,s54,v14\n"
MF "v[156:159],a[220:223],a[108:111],v[156:159]\n"
LD "v7,s[32:35],s72 offen offset:128 nt lds\n"
MF "v[164:167],a[220:223],a[112:115],v[164:167]\n"
"v_fma_f32 v214,v214,s54,v14\n"
"v_fma_f32 v215,v215,s54,v14\n"
MF "v[164:167],a[216:219],a[72:75],v[164:167]\n"
CV "v235,v214,v215\n"
CV "v234,v212,v213\n"
MF "v[172:175],a[216:219],a[76:79],v[172:175]\n"
"s_add_u32 m0,s64,7936\n"
CV "v233,v210,v211\n"
CV "v232,v208,v209\n"
MF "v[172:175],a[220:223],a[116:119],v[172:175]\n"
"s_waitcnt lgkmcnt(4)\n"
LD "v7,s[32:35],s72 offen offset:256 nt lds\n"
MF "v[140:143],a[200:203],a[140:143],v[140:143]\n"
TR "a[216:217],v1 offset:49152\n"
"v_fma_f32 v200,v200,s54,v14\n"
MF "v[140:143],a[204:207],a[180:183],v[140:143]\n"
TR "a[218:219],v1 offset:49664\n"
"v_fma_f32 v201,v201,s54,v14\n"
MF "v[148:151],a[204:207],a[184:187],v[148:151]\n"
"v_fma_f32 v202,v202,s54,v14\n"
"v_fma_f32 v203,v203,s54,v14\n"
MF "v[148:151],a[200:203],a[144:147],v[148:151]\n"
TR "a[220:221],v1 offset:53248\n"
"v_fma_f32 v204,v204,s54,v14\n"
MF "v[156:159],a[200:203],a[148:151],v[156:159]\n"
TR "a[222:223],v1 offset:53760\n"
"v_fma_f32 v205,v205,s54,v14\n"
MF "v[156:159],a[204:207],a[188:191],v[156:159]\n"
"v_fma_f32 v206,v206,s54,v14\n"
"v_fma_f32 v207,v207,s54,v14\n"
MF "v[164:167],a[204:207],a[192:195],v[164:167]\n"
CV "v231,v206,v207\n"
CV "v230,v204,v205\n"
MF "v[164:167],a[200:203],a[152:155],v[164:167]\n"
CV "v229,v202,v203\n"
CV "v228,v200,v201\n"
MF "v[172:175],a[200:203],a[156:159],v[172:175]\n"
"v_fma_f32 v192,v192,s54,v14\n"
"v_fma_f32 v193,v193,s54,v14\n"
MF "v[172:175],a[204:207],a[196:199],v[172:175]\n"
"s_waitcnt lgkmcnt(0)\n"
"v_fma_f32 v194,v194,s54,v14\n"
"v_fma_f32 v195,v195,s54,v14\n"
MF "v[136:139],a[208:211],a[60:63],v[136:139]\n"
TR "a[200:201],v0 offset:32776\n"
"v_fma_f32 v196,v196,s54,v14\n"
MF "v[136:139],a[212:215],a[100:103],v[136:139]\n"
TR "a[202:203],v0 offset:33288\n"
"v_fma_f32 v197,v197,s54,v14\n"
MF "v[144:147],a[212:215],a[104:107],v[144:147]\n"
"v_fma_f32 v198,v198,s54,v14\n"
"v_fma_f32 v199,v199,s54,v14\n"
MF "v[144:147],a[208:211],a[64:67],v[144:147]\n"
TR "a[204:205],v0 offset:36872\n"
CV "v227,v198,v199\n"
MF "v[152:155],a[208:211],a[68:71],v[152:155]\n"
TR "a[206:207],v0 offset:37384\n"
CV "v226,v196,v197\n"
MF "v[152:155],a[212:215],a[108:111],v[152:155]\n"
CV "v225,v194,v195\n"
CV "v224,v192,v193\n"
MF "v[160:163],a[212:215],a[112:115],v[160:163]\n"
"v_fma_f32 v184,v184,s54,v14\n"
"v_fma_f32 v185,v185,s54,v14\n"
MF "v[160:163],a[208:211],a[72:75],v[160:163]\n"
"v_fma_f32 v186,v186,s54,v14\n"
"v_fma_f32 v187,v187,s54,v14\n"
MF "v[168:171],a[208:211],a[76:79],v[168:171]\n"
"v_fma_f32 v188,v188,s54,v14\n"
"v_fma_f32 v189,v189,s54,v14\n"
MF "v[168:171],a[212:215],a[116:119],v[168:171]\n"
"v_fma_f32 v190,v190,s54,v14\n"
"v_fma_f32 v191,v191,s54,v14\n"
MF "v[168:171],a[216:219],a[156:159],v[168:171]\n"
TR "a[208:209],v0 offset:49160\n"
CV "v223,v190,v191\n"
MF "v[168:171],a[220:223],a[196:199],v[168:171]\n"
TR "a[210:211],v0 offset:49672\n"
CV "v222,v188,v189\n"
MF "v[160:163],a[220:223],a[192:195],v[160:163]\n"
CV "v221,v186,v187\n"
CV "v220,v184,v185\n"
MF "v[160:163],a[216:219],a[152:155],v[160:163]\n"
TR "a[212:213],v0 offset:53256\n"
"v_fma_f32 v176,v176,s54,v14\n"
MF "v[152:155],a[216:219],a[148:151],v[152:155]\n"
TR "a[214:215],v0 offset:53768\n"
"v_fma_f32 v177,v177,s54,v14\n"
MF "v[152:155],a[220:223],a[188:191],v[152:155]\n"
"v_fma_f32 v178,v178,s54,v14\n"
"v_fma_f32 v179,v179,s54,v14\n"
MF "v[144:147],a[220:223],a[184:187],v[144:147]\n"
"v_fma_f32 v180,v180,s54,v14\n"
"v_fma_f32 v181,v181,s54,v14\n"
MF "v[144:147],a[216:219],a[144:147],v[144:147]\n"
"v_fma_f32 v182,v182,s54,v14\n"
"v_fma_f32 v183,v183,s54,v14\n"
MF "v[136:139],a[216:219],a[140:143],v[136:139]\n"
CV "v219,v182,v183\n"
CV "v218,v180,v181\n"
MF "v[136:139],a[220:223],a[180:183],v[136:139]\n"
"s_waitcnt lgkmcnt(4)\n"
CV "v217,v178,v179\n"
CV "v216,v176,v177\n"
MF "v[100:103],a[200:203],a[60:63],v[100:103]\n"
TR "a[216:217],v0 offset:32768\n"
"v_fma_f32 v168,v168,s54,v14\n"
MF "v[100:103],a[204:207],a[100:103],v[100:103]\n"
TR "a[218:219],v0 offset:33280\n"
"v_fma_f32 v169,v169,s54,v14\n"
MF "v[108:111],a[204:207],a[104:107],v[108:111]\n"
"v_fma_f32 v170,v170,s54,v14\n"
"v_fma_f32 v171,v171,s54,v14\n"
MF "v[108:111],a[200:203],a[64:67],v[108:111]\n"
TR "a[220:221],v0 offset:36864\n"
"v_fma_f32 v172,v172,s54,v14\n"
MF "v[116:119],a[200:203],a[68:71],v[116:119]\n"
TR "a[222:223],v0 offset:37376\n"
"v_fma_f32 v173,v173,s54,v14\n"
MF "v[116:119],a[204:207],a[108:111],v[116:119]\n"
"v_fma_f32 v174,v174,s54,v14\n"
"v_fma_f32 v175,v175,s54,v14\n"
MF "v[124:127],a[204:207],a[112:115],v[124:127]\n"
CV "v215,v174,v175\n"
CV "v214,v172,v173\n"
MF "v[124:127],a[200:203],a[72:75],v[124:127]\n"
".Lpg8b:\n"
"s_cbranch_execz .Lpg11b\n"
".Lpg8d:\n"
"s_cbranch_execz .Lpgpsl_d\n"
CV "v213,v170,v171\n"
CV "v212,v168,v169\n"
MF "v[132:135],a[200:203],a[76:79],v[132:135]\n"
"v_fma_f32 v160,v160,s54,v14\n"
"v_fma_f32 v161,v161,s54,v14\n"
MF "v[132:135],a[204:207],a[116:119],v[132:135]\n"
"s_waitcnt lgkmcnt(4)\n"
"v_fma_f32 v162,v162,s54,v14\n"
"v_fma_f32 v163,v163,s54,v14\n"
MF "v[100:103],a[208:211],a[140:143],v[100:103]\n"
TR "a[200:201],v0 offset:49152\n"
"v_fma_f32 v164,v164,s54,v14\n"
MF "v[100:103],a[212:215],a[180:183],v[100:103]\n"
TR "a[202:203],v0 offset:49664\n"
"v_fma_f32 v165,v165,s54,v14\n"
MF "v[108:111],a[212:215],a[184:187],v[108:111]\n"
"v_fma_f32 v166,v166,s54,v14\n"
"v_fma_f32 v167,v167,s54,v14\n"
MF "v[108:111],a[208:211],a[144:147],v[108:111]\n"
TR "a[204:205],v0 offset:53248\n"
CV "v211,v166,v167\n"
MF "v[116:119],a[208:211],a[148:151],v[116:119]\n"
TR "a[206:207],v0 offset:53760\n"
CV "v210,v164,v165\n"
MF "v[116:119],a[212:215],a[188:191],v[116:119]\n"
CV "v209,v162,v163\n"
CV "v208,v160,v161\n"
MF "v[124:127],a[212:215],a[192:195],v[124:127]\n"
"v_add_u32 v0,0xc000,v0\n"
"v_fma_f32 v152,v152,s54,v14\n"
MF "v[124:127],a[208:211],a[152:155],v[124:127]\n"
"v_add_u32 v1,0xc000,v1\n"
"v_fma_f32 v153,v153,s54,v14\n"
MF "v[132:135],a[208:211],a[156:159],v[132:135]\n"
"s_waitcnt vmcnt(9) lgkmcnt(0)\n"
"v_fma_f32 v154,v154,s54,v14\n"
"v_fma_f32 v155,v155,s54,v14\n"
MF "v[132:135],a[212:215],a[196:199],v[132:135]\n"
"s_barrier\n"
MF "v[96:99],a[216:219],a[60:63],v[96:99]\n"
TR "a[208:209],v1 offset:16392\n"
"v_fma_f32 v156,v156,s54,v14\n"
MF "v[96:99],a[220:223],a[100:103],v[96:99]\n"
TR "a[210:211],v1 offset:16904\n"
"v_fma_f32 v157,v157,s54,v14\n"
MF "v[104:107],a[220:223],a[104:107],v[104:107]\n"
"s_add_u32 s72,s72,6144\n"
"v_fma_f32 v158,v158,s54,v14\n"
"v_fma_f32 v159,v159,s54,v14\n"
MF "v[104:107],a[216:219],a[64:67],v[104:107]\n"
"s_cmp_eq_u32 s72,s17\n"
"s_cselect_b32 s72,s16,s72\n"
TR "a[212:213],v1 offset:20488\n"
MF "v[112:115],a[216:219],a[68:71],v[112:115]\n"
"s_add_u32 m0,s64,24576\n"
TR "a[214:215],v1 offset:21000\n"
CV "v207,v158,v159\n"
MF "v[112:115],a[220:223],a[108:111],v[112:115]\n"
LD "v7,s[32:35],s72 offen nt lds\n"
MF "v[120:123],a[220:223],a[112:115],v[120:123]\n"
"s_add_u32 m0,s64,25472\n"
CV "v206,v156,v157\n"
CV "v205,v154,v155\n"
MF "v[120:123],a[216:219],a[72:75],v[120:123]\n"
LD "v7,s[32:35],s72 offen offset:128 nt lds\n"
MF "v[128:131],a[216:219],a[76:79],v[128:131]\n"
CV "v204,v152,v153\n"
"v_fma_f32 v144,v144,s54,v14\n"
MF "v[128:131],a[220:223],a[116:119],v[128:131]\n"
"v_fma_f32 v145,v145,s54,v14\n"
"v_fma_f32 v146,v146,s54,v14\n"
MF "v[128:131],a[200:203],a[156:159],v[128:131]\n"
TR "a[216:217],v1 offset:32776\n"
"v_fma_f32 v147,v147,s54,v14\n"
MF "v[128:131],a[204:207],a[196:199],v[128:131]\n"
"s_add_u32 m0,s64,26368\n"
TR "a[218:219],v1 offset:33288\n"
"v_fma_f32 v148,v148,s54,v14\n"
MF "v[120:123],a[204:207],a[192:195],v[120:123]\n"
LD "v7,s[32:35],s72 offen offset:256 nt lds\n"
MF "v[120:123],a[200:203],a[152:155],v[120:123]\n"
TR "a[220:221],v1 offset:36872\n"
"v_fma_f32 v149,v149,s54,v14\n"
MF "v[112:115],a[200:203],a[148:151],v[112:115]\n"
TR "a[222:223],v1 offset:37384\n"
"v_fma_f32 v150,v150,s54,v14\n"
MF "v[112:115],a[204:207],a[188:191],v[112:115]\n"
"v_fma_f32 v151,v151,s54,v14\n"
CV "v203,v150,v151\n"
MF "v[104:107],a[204:207],a[184:187],v[104:107]\n"
"s_add_u32 s72,s72,6144\n"
CV "v202,v148,v149\n"
CV "v201,v146,v147\n"
MF "v[104:107],a[200:203],a[144:147],v[104:107]\n"
"s_add_u32 m0,s64,30720\n"
CV "v200,v144,v145\n"
"v_fma_f32 v136,v136,s54,v14\n"
MF "v[96:99],a[200:203],a[140:143],v[96:99]\n"
LD "v7,s[32:35],s72 offen nt lds\n"
MF "v[96:99],a[204:207],a[180:183],v[96:99]\n"
"s_waitcnt lgkmcnt(4)\n"
"v_fma_f32 v137,v137,s54,v14\n"
"v_fma_f32 v138,v138,s54,v14\n"
MF "v[60:63],a[208:211],a[60:63],v[60:63]\n"
TR "a[200:201],v1 offset:16384\n"
"v_fma_f32 v139,v139,s54,v14\n"
MF "v[60:63],a[212:215],a[100:103],v[60:63]\n"
TR "a[202:203],v1 offset:16896\n"
"v_fma_f32 v140,v140,s54,v14\n"
MF "v[68:71],a[212:215],a[104:107],v[68:71]\n"
"v_fma_f32 v141,v141,s54,v14\n"
"v_fma_f32 v142,v142,s54,v14\n"
MF "v[68:71],a[208:211],a[64:67],v[68:71]\n"
TR "a[204:205],v1 offset:20480\n"
"v_fma_f32 v143,v143,s54,v14\n"
MF "v[76:79],a[208:211],a[68:71],v[76:79]\n"
"s_add_u32 m0,s64,31616\n"
TR "a[206:207],v1 offset:20992\n"
CV "v199,v142,v143\n"
MF "v[76:79],a[212:215],a[108:111],v[76:79]\n"
LD "v7,s[32:35],s72 offen offset:128 nt lds\n"
MF "v[84:87],a[212:215],a[112:115],v[84:87]\n"
CV "v198,v140,v141\n"
CV "v197,v138,v139\n"
MF "v[84:87],a[208:211],a[72:75],v[84:87]\n"
CV "v196,v136,v137\n"
"v_fma_f32 v128,v128,s54,v14\n"
MF "v[92:95],a[208:211],a[76:79],v[92:95]\n"
"s_add_u32 m0,s64,32512\n"
"v_fma_f32 v129,v129,s54,v14\n"
"v_fma_f32 v130,v130,s54,v14\n"
MF "v[92:95],a[212:215],a[116:119],v[92:95]\n"
"s_waitcnt lgkmcnt(4)\n"
LD "v7,s[32:35],s72 offen offset:256 nt lds\n"
MF "v[60:63],a[216:219],a[140:143],v[60:63]\n"
TR "a[208:209],v1 offset:32768\n"
"v_fma_f32 v131,v131,s54,v14\n"
MF "v[60:63],a[220:223],a[180:183],v[60:63]\n"
TR "a[210:211],v1 offset:33280\n"
"v_fma_f32 v132,v132,s54,v14\n"
MF "v[68:71],a[220:223],a[184:187],v[68:71]\n"
"v_fma_f32 v133,v133,s54,v14\n"
"v_fma_f32 v134,v134,s54,v14\n"
MF "v[68:71],a[216:219],a[144:147],v[68:71]\n"
TR "a[212:213],v1 offset:36864\n"
"v_fma_f32 v135,v135,s54,v14\n"
MF "v[76:79],a[216:219],a[148:151],v[76:79]\n"
TR "a[214:215],v1 offset:37376\n"
CV "v195,v134,v135\n"
MF "v[76:79],a[220:223],a[188:191],v[76:79]\n"
CV "v194,v132,v133\n"
CV "v193,v130,v131\n"
MF "v[84:87],a[220:223],a[192:195],v[84:87]\n"
CV "v192,v128,v129\n"
"v_fma_f32 v120,v120,s54,v14\n"
MF "v[84:87],a[216:219],a[152:155],v[84:87]\n"
"v_fma_f32 v121,v121,s54,v14\n"
"v_fma_f32 v122,v122,s54,v14\n"
MF "v[92:95],a[216:219],a[156:159],v[92:95]\n"
"v_fma_f32 v123,v123,s54,v14\n"
"v_fma_f32 v124,v124,s54,v14\n"
MF "v[92:95],a[220:223],a[196:199],v[92:95]\n"
"s_waitcnt lgkmcnt(0)\n"
"v_fma_f32 v125,v125,s54,v14\n"
"v_fma_f32 v126,v126,s54,v14\n"
MF "v[56:59],a[200:203],a[60:63],v[56:59]\n"
TR "a[216:217],v0 offset:16392\n"
"v_fma_f32 v127,v127,s54,v14\n"
MF "v[56:59],a[204:207],a[100:103],v[56:59]\n"
TR "a[218:219],v0 offset:16904\n"
CV "v191,v126,v127\n"
MF "v[64:67],a[204:207],a[104:107],v[64:67]\n"
CV "v190,v124,v125\n"
CV "v189,v122,v123\n"
MF "v[64:67],a[200:203],a[64:67],v[64:67]\n"
TR "a[220:221],v0 offset:20488\n"
CV "v188,v120,v121\n"
MF "v[72:75],a[200:203],a[68:71],v[72:75]\n"
TR "a[222:223],v0 offset:21000\n"
"v_fma_f32 v112,v112,s54,v14\n"
MF "v[72:75],a[204:207],a[108:111],v[72:75]\n"
"v_fma_f32 v113,v113,s54,v14\n"
"v_fma_f32 v114,v114,s54,v14\n"
MF "v[80:83],a[204:207],a[112:115],v[80:83]\n"
"v_fma_f32 v115,v115,s54,v14\n"
"v_fma_f32 v116,v116,s54,v14\n"
MF "v[80:83],a[200:203],a[72:75],v[80:83]\n"
"v_fma_f32 v117,v117,s54,v14\n"
"v_fma_f32 v118,v118,s54,v14\n"
MF "v[88:91],a[200:203],a[76:79],v[88:91]\n"
"v_fma_f32 v119,v119,s54,v14\n"
CV "v187,v118,v119\n"
MF "v[88:91],a[204:207],a[116:119],v[88:91]\n"
CV "v186,v116,v117\n"
CV "v185,v114,v115\n"
MF "v[88:91],a[208:211],a[156:159],v[88:91]\n"
TR "a[200:201],v0 offset:32776\n"
CV "v184,v112,v113\n"
MF "v[88:91],a[212:215],a[196:199],v[88:91]\n"
TR "a[202:203],v0 offset:33288\n"
"v_fma_f32 v104,v104,s54,v14\n"
MF "v[80:83],a[212:215],a[192:195],v[80:83]\n"
"v_fma_f32 v105,v105,s54,v14\n"
"v_fma_f32 v106,v106,s54,v14\n"
MF "v[80:83],a[208:211],a[152:155],v[80:83]\n"
TR "a[204:205],v0 offset:36872\n"
"v_fma_f32 v107,v107,s54,v14\n"
MF "v[72:75],a[208:211],a[148:151],v[72:75]\n"
TR "a[206:207],v0 offset:37384\n"
"v_fma_f32 v108,v108,s54,v14\n"
MF "v[72:75],a[212:215],a[188:191],v[72:75]\n"
"v_fma_f32 v109,v109,s54,v14\n"
"v_fma_f32 v110,v110,s54,v14\n"
MF "v[64:67],a[212:215],a[184:187],v[64:67]\n"
"v_fma_f32 v111,v111,s54,v14\n"
CV "v183,v110,v111\n"
MF "v[64:67],a[208:211],a[144:147],v[64:67]\n"
CV "v182,v108,v109\n"
CV "v181,v106,v107\n"
MF "v[56:59],a[208:211],a[140:143],v[56:59]\n"
CV "v180,v104,v105\n"
"v_fma_f32 v96,v96,s54,v14\n"
MF "v[56:59],a[212:215],a[180:183],v[56:59]\n"
"s_waitcnt lgkmcnt(4)\n"
"v_fma_f32 v97,v97,s54,v14\n"
"v_fma_f32 v98,v98,s54,v14\n"
MF "v[20:23],a[216:219],a[60:63],v[20:23]\n"
TR "a[208:209],v0 offset:16384\n"
"v_fma_f32 v99,v99,s54,v14\n"
MF "v[20:23],a[220:223],a[100:103],v[20:23]\n"
TR "a[210:211],v0 offset:16896\n"
"v_fma_f32 v100,v100,s54,v14\n"
MF "v[28:31],a[220:223],a[104:107],v[28:31]\n"
"v_fma_f32 v101,v101,s54,v14\n"
"v_fma_f32 v102,v102,s54,v14\n"
MF "v[28:31],a[216:219],a[64:67],v[28:31]\n"
TR "a[212:213],v0 offset:20480\n"
"v_fma_f32 v103,v103,s54,v14\n"
MF "v[36:39],a[216:219],a[68:71],v[36:39]\n"
TR "a[214:215],v0 offset:20992\n"
CV "v179,v102,v103\n"
MF "v[36:39],a[220:223],a[108:111],v[36:39]\n"
CV "v178,v100,v101\n"
CV "v177,v98,v99\n"
MF "v[44:47],a[220:223],a[112:115],v[44:47]\n"
CV "v176,v96,v97\n"
"v_fma_f32 v88,v88,s54,v14\n"
MF "v[44:47],a[216:219],a[72:75],v[44:47]\n"
"v_fma_f32 v89,v89,s54,v14\n"
"v_fma_f32 v90,v90,s54,v14\n"
MF "v[52:55],a[216:219],a[76:79],v[52:55]\n"
"v_fma_f32 v91,v91,s54,v14\n"
"v_fma_f32 v92,v92,s54,v14\n"
MF "v[52:55],a[220:223],a[116:119],v[52:55]\n"
"s_waitcnt lgkmcnt(4)\n"
"v_fma_f32 v93,v93,s54,v14\n"
"v_fma_f32 v94,v94,s54,v14\n"
MF "v[20:23],a[200:203],a[140:143],v[20:23]\n"
TR "a[216:217],v0 offset:32768\n"
"v_fma_f32 v95,v95,s54,v14\n"
MF "v[20:23],a[204:207],a[180:183],v[20:23]\n"
TR "a[218:219],v0 offset:33280\n"
CV "v175,v94,v95\n"
MF "v[28:31],a[204:207],a[184:187],v[28:31]\n"
CV "v174,v92,v93\n"
CV "v173,v90,v91\n"
MF "v[28:31],a[200:203],a[144:147],v[28:31]\n"
TR "a[220:221],v0 offset:36864\n"
CV "v172,v88,v89\n"
MF "v[36:39],a[200:203],a[148:151],v[36:39]\n"
TR "a[222:223],v0 offset:37376\n"
"v_fma_f32 v80,v80,s54,v14\n"
MF "v[36:39],a[204:207],a[188:191],v[36:39]\n"
"v_fma_f32 v81,v81,s54,v14\n"
"v_fma_f32 v82,v82,s54,v14\n"
MF "v[44:47],a[204:207],a[192:195],v[44:47]\n"
"v_fma_f32 v83,v83,s54,v14\n"
"v_fma_f32 v84,v84,s54,v14\n"
MF "v[44:47],a[200:203],a[152:155],v[44:47]\n"
"v_fma_f32 v85,v85,s54,v14\n"
"v_fma_f32 v86,v86,s54,v14\n"
MF "v[52:55],a[200:203],a[156:159],v[52:55]\n"
"s_waitcnt lgkmcnt(0)\n"
"v_fma_f32 v87,v87,s54,v14\n"
CV "v171,v86,v87\n"
MF "v[52:55],a[204:207],a[196:199],v[52:55]\n"
"s_barrier\n"
MF "v[16:19],a[208:211],a[60:63],v[16:19]\n"
"s_add_u32 m0,s65,49152\n"
CV "v170,v84,v85\n"
CV "v169,v82,v83\n"
MF "v[16:19],a[212:215],a[100:103],v[16:19]\n"
LD "v8,s[36:39],s73 offen nt lds\n"
MF "v[24:27],a[212:215],a[104:107],v[24:27]\n"
"s_add_u32 m0,s65,50048\n"
CV "v168,v80,v81\n"
"v_fma_f32 v72,v72,s54,v14\n"
MF "v[24:27],a[208:211],a[64:67],v[24:27]\n"
LD "v8,s[36:39],s73 offen offset:128 nt lds\n"
MF "v[32:35],a[208:211],a[68:71],v[32:35]\n"
"v_fma_f32 v73,v73,s54,v14\n"
"v_fma_f32 v74,v74,s54,v14\n"
MF "v[32:35],a[212:215],a[108:111],v[32:35]\n"
"s_add_u32 m0,s65,50944\n"
"v_fma_f32 v75,v75,s54,v14\n"
"v_fma_f32 v76,v76,s54,v14\n"
MF "v[40:43],a[212:215],a[112:115],v[40:43]\n"
LD "v8,s[36:39],s73 offen offset:256 nt lds\n"
MF "v[40:43],a[208:211],a[72:75],v[40:43]\n"
"v_fma_f32 v77,v77,s54,v14\n"
"v_fma_f32 v78,v78,s54,v14\n"
MF "v[48:51],a[208:211],a[76:79],v[48:51]\n"
"s_add_u32 m0,s65,49152\n"
"v_fma_f32 v79,v79,s54,v14\n"
CV "v167,v78,v79\n"
MF "v[48:51],a[212:215],a[116:119],v[48:51]\n"
LD "v8,s[36:39],s73 offen offset:3072 nt lds\n"
MF "v[48:51],a[216:219],a[156:159],v[48:51]\n"
"s_add_u32 m0,s65,50048\n"
CV "v166,v76,v77\n"
CV "v165,v74,v75\n"
MF "v[48:51],a[220:223],a[196:199],v[48:51]\n"
LD "v8,s[36:39],s73 offen offset:3200 nt lds\n"
MF "v[40:43],a[220:223],a[192:195],v[40:43]\n"
CV "v164,v72,v73\n"
"v_fma_f32 v64,v64,s54,v14\n"
MF "v[40:43],a[216:219],a[152:155],v[40:43]\n"
"s_add_u32 m0,s65,50944\n"
"v_fma_f32 v65,v65,s54,v14\n"
"v_fma_f32 v66,v66,s54,v14\n"
MF "v[32:35],a[216:219],a[148:151],v[32:35]\n"
LD "v8,s[36:39],s73 offen offset:3328 nt lds\n"
MF "v[32:35],a[220:223],a[188:191],v[32:35]\n"
"v_fma_f32 v67,v67,s54,v14\n"
"v_fma_f32 v68,v68,s54,v14\n"
MF "v[24:27],a[220:223],a[184:187],v[24:27]\n"
"v_fma_f32 v69,v69,s54,v14\n"
"v_fma_f32 v70,v70,s54,v14\n"
MF "v[24:27],a[216:219],a[144:147],v[24:27]\n"
"v_fma_f32 v71,v71,s54,v14\n"
CV "v163,v70,v71\n"
MF "v[16:19],a[216:219],a[140:143],v[16:19]\n"
CV "v162,v68,v69\n"
CV "v161,v66,v67\n"
MF "v[16:19],a[220:223],a[180:183],v[16:19]\n"
CV "v160,v64,v65\n"
AR "v0,a244\n"
"v_xor_b32 v1,64,v0\n"
"s_waitcnt vmcnt(12) lgkmcnt(0)\n"
"s_barrier\n"
"s_nop 0\n"
"ds_read_b128 v[92:95],v0\n"
"ds_read_b128 v[96:99],v1\n"
"ds_read_b128 v[100:103],v0 offset:1024\n"
"v_pk_fma_f32 v[56:57],v[56:57],s[54:55],v[14:15] op_sel_hi:[1,1,0]\n"
"v_pk_fma_f32 v[58:59],v[58:59],s[54:55],v[14:15] op_sel_hi:[1,1,0]\n"
"v_pk_fma_f32 v[60:61],v[60:61],s[54:55],v[14:15] op_sel_hi:[1,1,0]\n"
"v_pk_fma_f32 v[62:63],v[62:63],s[54:55],v[14:15] op_sel_hi:[1,1,0]\n"
CV "v159,v62,v63\n"
CV "v158,v60,v61\n"
CV "v157,v58,v59\n"
CV "v156,v56,v57\n"
"v_pk_mul_f32 v[48:49],v[48:49],s[54:55]\n"
"v_pk_mul_f32 v[50:51],v[50:51],s[54:55]\n"
"v_pk_mul_f32 v[52:53],v[52:53],s[54:55]\n"
"v_pk_mul_f32 v[54:55],v[54:55],s[54:55]\n"
CV "v155,v54,v55\n"
CV "v154,v52,v53\n"
CV "v153,v50,v51\n"
CV "v152,v48,v49\n"
"v_pk_mul_f32 v[40:41],v[40:41],s[54:55]\n"
"v_pk_mul_f32 v[42:43],v[42:43],s[54:55]\n"
"v_pk_mul_f32 v[44:45],v[44:45],s[54:55]\n"
"v_pk_mul_f32 v[46:47],v[46:47],s[54:55]\n"
CV "v151,v46,v47\n"
CV "v150,v44,v45\n"
CV "v149,v42,v43\n"
CV "v148,v40,v41\n"
"v_pk_mul_f32 v[32:33],v[32:33],s[54:55]\n"
"v_pk_mul_f32 v[34:35],v[34:35],s[54:55]\n"
"v_pk_mul_f32 v[36:37],v[36:37],s[54:55]\n"
"v_pk_mul_f32 v[38:39],v[38:39],s[54:55]\n"
CV "v147,v38,v39\n"
CV "v146,v36,v37\n"
CV "v145,v34,v35\n"
CV "v144,v32,v33\n"
"v_pk_mul_f32 v[24:25],v[24:25],s[54:55]\n"
"v_pk_mul_f32 v[26:27],v[26:27],s[54:55]\n"
"v_pk_mul_f32 v[28:29],v[28:29],s[54:55]\n"
"v_pk_mul_f32 v[30:31],v[30:31],s[54:55]\n"
CV "v143,v30,v31\n"
CV "v142,v28,v29\n"
CV "v141,v26,v27\n"
CV "v140,v24,v25\n"
"v_pk_mul_f32 v[16:17],v[16:17],s[54:55]\n"
"v_pk_mul_f32 v[18:19],v[18:19],s[54:55]\n"
"v_pk_mul_f32 v[20:21],v[20:21],s[54:55]\n"
"v_pk_mul_f32 v[22:23],v[22:23],s[54:55]\n"
CV "v139,v22,v23\n"
CV "v138,v20,v21\n"
CV "v137,v18,v19\n"
CV "v136,v16,v17\n"
"s_cmp_eq_u32 s57,s69\n"
"s_cbranch_scc1 .LattB\n"
AR "v2,a245\n"
"v_add_u32 v2,49152,v2\n"
"v_xor_b32 v3,32,v2\n"
"v_xor_b32 v4,64,v2\n"
"v_xor_b32 v5,96,v2\n"
"s_mov_b32 s61,7\n"
".Latt2x:\n"
"s_cmp_eq_u32 s57,1\n"
"s_cbranch_scc1 .Lt0kx\n"
".Lt0nx:\n"
"s_waitcnt lgkmcnt(2)\n"
MF "v[72:75],v[92:95],v[136:139],0\n"
"s_add_u32 s73,s73,12288\n"
"ds_read_b128 v[104:107],v1 offset:1024\n"
AR "v10,a224\n"
MF "v[76:79],v[92:95],v[140:143],0\n"
"s_cmp_eq_u32 s73,s19\n"
"s_cselect_b32 s73,s18,s73\n"
"s_add_u32 m0,s65,73728\n"
MF "v[80:83],v[92:95],v[144:147],0\n"
LD "v8,s[36:39],s73 offen nt lds\n"
MF "v[84:87],v[92:95],v[148:151],0\n"
"v_max_f32_dpp v10,|v10|,|v10| quad_perm:[1,0,3,2] row_mask:0xf bank_mask:0xf\n"
AR "v11,a225\n"
MF "v[88:91],v[92:95],v[152:155],0\n"
"s_waitcnt lgkmcnt(2)\n"
"ds_read_b128 v[92:95],v0 offset:2048\n"
AR "v12,a226\n"
MF "v[72:75],v[96:99],v[156:159],v[72:75]\n"
"s_add_u32 m0,s65,74624\n"
"v_max_f32_dpp v10,v10,v10 quad_perm:[2,3,0,1] row_mask:0xf bank_mask:0xf\n"
"v_max_f32_dpp v11,|v11|,|v11| quad_perm:[1,0,3,2] row_mask:0xf bank_mask:0xf\n"
MF "v[76:79],v[96:99],v[160:163],v[76:79]\n"
LD "v8,s[36:39],s73 offen offset:128 nt lds\n"
MF "v[80:83],v[96:99],v[164:167],v[80:83]\n"
"v_max_f32_dpp v10,v10,v10 row_half_mirror row_mask:0xf bank_mask:0xf\n"
"v_max_f32_dpp v11,v11,v11 quad_perm:[2,3,0,1] row_mask:0xf bank_mask:0xf\n"
MF "v[84:87],v[96:99],v[168:171],v[84:87]\n"
"v_readlane_b32 s13,v10,0\n"
"v_readlane_b32 s0,v10,24\n"
MF "v[88:91],v[96:99],v[172:175],v[88:91]\n"
"s_waitcnt lgkmcnt(2)\n"
"ds_read_b128 v[96:99],v1 offset:2048\n"
"v_readlane_b32 s1,v10,32\n"
MF "v[72:75],v[100:103],v[176:179],v[72:75]\n"
"s_add_u32 m0,s65,75520\n"
"v_readlane_b32 s2,v10,56\n"
"v_max_f32_dpp v11,v11,v11 row_half_mirror row_mask:0xf bank_mask:0xf\n"
MF "v[76:79],v[100:103],v[180:183],v[76:79]\n"
LD "v8,s[36:39],s73 offen offset:256 nt lds\n"
MF "v[80:83],v[100:103],v[184:187],v[80:83]\n"
"s_min_u32 s13,s13,s0\n"
"v_readlane_b32 s11,v11,0\n"
"v_readlane_b32 s3,v11,24\n"
MF "v[84:87],v[100:103],v[188:191],v[84:87]\n"
"s_min_u32 s1,s1,s2\n"
"v_readlane_b32 s4,v11,32\n"
"v_readlane_b32 s5,v11,56\n"
MF "v[88:91],v[100:103],v[192:195],v[88:91]\n"
"s_waitcnt lgkmcnt(2)\n"
"ds_read_b128 v[100:103],v0 offset:6144\n"
"v_max_f32_dpp v12,|v12|,|v12| quad_perm:[1,0,3,2] row_mask:0xf bank_mask:0xf\n"
MF "v[72:75],v[104:107],v[196:199],v[72:75]\n"
"s_add_u32 m0,s65,73728\n"
MF "v[76:79],v[104:107],v[200:203],v[76:79]\n"
LD "v8,s[36:39],s73 offen offset:3072 nt lds\n"
MF "v[80:83],v[104:107],v[204:207],v[80:83]\n"
"s_min_u32 s13,s13,s1\n"
"v_max_f32_dpp v12,v12,v12 quad_perm:[2,3,0,1] row_mask:0xf bank_mask:0xf\n"
MF "v[84:87],v[104:107],v[208:211],v[84:87]\n"
"s_and_b32 s13,s13,0x7f800000\n"
MF "v[88:91],v[104:107],v[212:215],v[88:91]\n"
"s_waitcnt lgkmcnt(2)\n"
"ds_read_b128 v[104:107],v1 offset:6144\n"
"v_max_f32_dpp v12,v12,v12 row_half_mirror row_mask:0xf bank_mask:0xf\n"
MF "v[72:75],v[92:95],v[216:219],v[72:75]\n"
"s_add_u32 m0,s65,74624\n"
"v_readlane_b32 s10,v12,0\n"
"v_readlane_b32 s6,v12,24\n"
MF "v[76:79],v[92:95],v[220:223],v[76:79]\n"
LD "v8,s[36:39],s73 offen offset:3200 nt lds\n"
MF "v[80:83],v[92:95],v[224:227],v[80:83]\n"
"s_sub_u32 s13,s13,0x14000000\n"
"v_readlane_b32 s7,v12,32\n"
"v_readlane_b32 s8,v12,56\n"
MF "v[84:87],v[92:95],v[228:231],v[84:87]\n"
"s_cmp_lt_u32 s13,0x50000000\n"
"s_cselect_b32 s13,s13,0xed400000\n"
MF "v[88:91],v[92:95],v[232:235],v[88:91]\n"
"s_waitcnt lgkmcnt(2)\n"
"ds_read_b128 v[92:95],v0 offset:7168\n"
MF "v[72:75],v[96:99],v[236:239],v[72:75]\n"
"s_add_u32 m0,s65,75520\n"
MF "v[76:79],v[96:99],v[240:243],v[76:79]\n"
LD "v8,s[36:39],s73 offen offset:3328 nt lds\n"
MF "v[80:83],v[96:99],v[244:247],v[80:83]\n"
"s_add_u32 s13,s13,0x12c00000\n"
MF "v[84:87],v[96:99],v[248:251],v[84:87]\n"
"s_min_u32 s11,s11,s3\n"
MF "v[88:91],v[96:99],v[252:255],v[88:91]\n"
"s_waitcnt lgkmcnt(2)\n"
"ds_read_b128 v[96:99],v1 offset:7168\n"
MF "v[112:115],v[100:103],v[136:139],0\n"
"s_min_u32 s4,s4,s5\n"
MF "v[116:119],v[100:103],v[140:143],0\n"
"s_min_u32 s11,s11,s4\n"
MF "v[120:123],v[100:103],v[144:147],0\n"
"s_and_b32 s11,s11,0x7f800000\n"
MF "v[124:127],v[100:103],v[148:151],0\n"
"s_sub_u32 s11,s11,0x14000000\n"
MF "v[128:131],v[100:103],v[152:155],0\n"
"s_waitcnt lgkmcnt(2)\n"
"ds_read_b128 v[100:103],v0 offset:8192\n"
MF "v[112:115],v[104:107],v[156:159],v[112:115]\n"
"s_min_u32 s10,s10,s6\n"
"v_max3_f32 v52,v72,v73,v74\n"
"v_max_f32 v52,v52,v75\n"
MF "v[116:119],v[104:107],v[160:163],v[116:119]\n"
"s_min_u32 s7,s7,s8\n"
"v_max3_f32 v53,v76,v77,v78\n"
"v_max_f32 v53,v53,v79\n"
MF "v[120:123],v[104:107],v[164:167],v[120:123]\n"
"s_min_u32 s10,s10,s7\n"
"v_max3_f32 v54,v80,v81,v82\n"
"v_max_f32 v54,v54,v83\n"
MF "v[124:127],v[104:107],v[168:171],v[124:127]\n"
"v_max3_f32 v55,v84,v85,v86\n"
"v_max_f32 v55,v55,v87\n"
MF "v[128:131],v[104:107],v[172:175],v[128:131]\n"
"s_waitcnt lgkmcnt(2)\n"
"ds_read_b128 v[104:107],v1 offset:8192\n"
"v_max3_f32 v56,v88,v89,v90\n"
MF "v[112:115],v[92:95],v[176:179],v[112:115]\n"
".Lpg9b:\n"
"s_cbranch_execz .Lpgpsl_b\n"
"s_cmp_lt_u32 s11,0x50000000\n"
"s_cselect_b32 s11,s11,0xed400000\n"
"v_max_f32 v56,v56,v91\n"
MF "v[116:119],v[92:95],v[180:183],v[116:119]\n"
"s_add_u32 s11,s11,0x12c00000\n"
MF "v[120:123],v[92:95],v[184:187],v[120:123]\n"
MF "v[124:127],v[92:95],v[188:191],v[124:127]\n"
MF "v[128:131],v[92:95],v[192:195],v[128:131]\n"
"s_waitcnt lgkmcnt(2)\n"
MF "v[112:115],v[96:99],v[196:199],v[112:115]\n"
"s_waitcnt lgkmcnt(0)\n"
MF "v[116:119],v[96:99],v[200:203],v[116:119]\n"
"s_barrier\n"
MF "v[120:123],v[96:99],v[204:207],v[120:123]\n"
"s_add_u32 s72,s72,6144\n"
MF "v[124:127],v[96:99],v[208:211],v[124:127]\n"
"s_cmp_eq_u32 s72,s17\n"
"s_cselect_b32 s72,s16,s72\n"
"s_add_u32 m0,s64,0\n"
MF "v[128:131],v[96:99],v[212:215],v[128:131]\n"
LD "v7,s[32:35],s72 offen nt lds\n"
MF "v[112:115],v[100:103],v[216:219],v[112:115]\n"
"s_add_u32 m0,s64,896\n"
MF "v[116:119],v[100:103],v[220:223],v[116:119]\n"
LD "v7,s[32:35],s72 offen offset:128 nt lds\n"
MF "v[120:123],v[100:103],v[224:227],v[120:123]\n"
"s_add_u32 m0,s64,1792\n"
MF "v[124:127],v[100:103],v[228:231],v[124:127]\n"
LD "v7,s[32:35],s72 offen offset:256 nt lds\n"
MF "v[128:131],v[100:103],v[232:235],v[128:131]\n"
"s_add_u32 s72,s72,6144\n"
"s_add_u32 m0,s64,6144\n"
MF "v[112:115],v[104:107],v[236:239],v[112:115]\n"
LD "v7,s[32:35],s72 offen nt lds\n"
MF "v[116:119],v[104:107],v[240:243],v[116:119]\n"
"s_add_u32 m0,s64,7040\n"
MF "v[120:123],v[104:107],v[244:247],v[120:123]\n"
LD "v7,s[32:35],s72 offen offset:128 nt lds\n"
MF "v[124:127],v[104:107],v[248:251],v[124:127]\n"
"s_add_u32 m0,s64,7936\n"
MF "v[128:131],v[104:107],v[252:255],v[128:131]\n"
LD "v7,s[32:35],s72 offen offset:256 nt lds\n"
"s_branch .Lt0ex\n"
".Lpgt0kx_d:\n"
"s_cbranch_execz .Lisr2\n"
".Lt0kx:\n"
"s_lshl_b32 s58,s56,10\n"
"s_add_u32 s58,s58,73728\n"
"v_add_u32 v32,s58,v9\n"
"v_add_u32 v33,65536,v32\n"
"s_waitcnt lgkmcnt(2)\n"
MF "v[72:75],v[92:95],v[136:139],0\n"
"s_add_u32 s73,s73,12288\n"
"ds_read_b128 v[104:107],v1 offset:1024\n"
AR "v10,a224\n"
MF "v[76:79],v[92:95],v[140:143],0\n"
"s_cmp_eq_u32 s73,s19\n"
"s_cselect_b32 s73,s18,s73\n"
"s_add_u32 m0,s65,73728\n"
MF "v[80:83],v[92:95],v[144:147],0\n"
LD "v8,s[36:39],s73 offen nt lds\n"
MF "v[84:87],v[92:95],v[148:151],0\n"
"v_max_f32_dpp v10,|v10|,|v10| quad_perm:[1,0,3,2] row_mask:0xf bank_mask:0xf\n"
AR "v11,a225\n"
MF "v[88:91],v[92:95],v[152:155],0\n"
"s_waitcnt lgkmcnt(2)\n"
"ds_read_b128 v[92:95],v0 offset:2048\n"
AR "v12,a226\n"
MF "v[72:75],v[96:99],v[156:159],v[72:75]\n"
"s_add_u32 m0,s65,74624\n"
"v_max_f32_dpp v10,v10,v10 quad_perm:[2,3,0,1] row_mask:0xf bank_mask:0xf\n"
"v_max_f32_dpp v11,|v11|,|v11| quad_perm:[1,0,3,2] row_mask:0xf bank_mask:0xf\n"
MF "v[76:79],v[96:99],v[160:163],v[76:79]\n"
LD "v8,s[36:39],s73 offen offset:128 nt lds\n"
MF "v[80:83],v[96:99],v[164:167],v[80:83]\n"
"v_max_f32_dpp v10,v10,v10 row_half_mirror row_mask:0xf bank_mask:0xf\n"
"v_max_f32_dpp v11,v11,v11 quad_perm:[2,3,0,1] row_mask:0xf bank_mask:0xf\n"
MF "v[84:87],v[96:99],v[168:171],v[84:87]\n"
"v_readlane_b32 s13,v10,0\n"
"v_readlane_b32 s0,v10,24\n"
MF "v[88:91],v[96:99],v[172:175],v[88:91]\n"
"s_waitcnt lgkmcnt(2)\n"
"ds_read_b128 v[96:99],v1 offset:2048\n"
"v_readlane_b32 s1,v10,32\n"
MF "v[72:75],v[100:103],v[176:179],v[72:75]\n"
"s_add_u32 m0,s65,75520\n"
"v_readlane_b32 s2,v10,56\n"
"v_max_f32_dpp v11,v11,v11 row_half_mirror row_mask:0xf bank_mask:0xf\n"
MF "v[76:79],v[100:103],v[180:183],v[76:79]\n"
LD "v8,s[36:39],s73 offen offset:256 nt lds\n"
MF "v[80:83],v[100:103],v[184:187],v[80:83]\n"
"s_min_u32 s13,s13,s0\n"
"v_readlane_b32 s11,v11,0\n"
"v_readlane_b32 s3,v11,24\n"
MF "v[84:87],v[100:103],v[188:191],v[84:87]\n"
"s_min_u32 s1,s1,s2\n"
"v_readlane_b32 s4,v11,32\n"
"v_readlane_b32 s5,v11,56\n"
MF "v[88:91],v[100:103],v[192:195],v[88:91]\n"
"s_waitcnt lgkmcnt(2)\n"
"ds_read_b128 v[100:103],v0 offset:6144\n"
"v_max_f32_dpp v12,|v12|,|v12| quad_perm:[1,0,3,2] row_mask:0xf bank_mask:0xf\n"
MF "v[72:75],v[104:107],v[196:199],v[72:75]\n"
"s_add_u32 m0,s65,73728\n"
"ds_write_b128 v32,a[0:3] offset:24576\n"
MF "v[76:79],v[104:107],v[200:203],v[76:79]\n"
LD "v8,s[36:39],s73 offen offset:3072 nt lds\n"
MF "v[80:83],v[104:107],v[204:207],v[80:83]\n"
"s_min_u32 s13,s13,s1\n"
"v_max_f32_dpp v12,v12,v12 quad_perm:[2,3,0,1] row_mask:0xf bank_mask:0xf\n"
AR "v62,a20\n"
MF "v[84:87],v[104:107],v[208:211],v[84:87]\n"
"s_and_b32 s13,s13,0x7f800000\n"
"ds_write_b128 v32,a[4:7] offset:28672\n"
MF "v[88:91],v[104:107],v[212:215],v[88:91]\n"
"s_waitcnt lgkmcnt(4)\n"
"ds_read_b128 v[104:107],v1 offset:6144\n"
"v_max_f32_dpp v12,v12,v12 row_half_mirror row_mask:0xf bank_mask:0xf\n"
MF "v[72:75],v[92:95],v[216:219],v[72:75]\n"
"s_add_u32 m0,s65,74624\n"
"v_readlane_b32 s10,v12,0\n"
"v_readlane_b32 s6,v12,24\n"
MF "v[76:79],v[92:95],v[220:223],v[76:79]\n"
LD "v8,s[36:39],s73 offen offset:3200 nt lds\n"
MF "v[80:83],v[92:95],v[224:227],v[80:83]\n"
"s_sub_u32 s13,s13,0x14000000\n"
"v_readlane_b32 s7,v12,32\n"
"v_readlane_b32 s8,v12,56\n"
MF "v[84:87],v[92:95],v[228:231],v[84:87]\n"
"s_cmp_lt_u32 s13,0x50000000\n"
"s_cselect_b32 s13,s13,0xed400000\n"
AR "v63,a21\n"
MF "v[88:91],v[92:95],v[232:235],v[88:91]\n"
"s_waitcnt lgkmcnt(4)\n"
"ds_read_b128 v[92:95],v0 offset:7168\n"
AR "v64,a22\n"
MF "v[72:75],v[96:99],v[236:239],v[72:75]\n"
"s_add_u32 m0,s65,75520\n"
"ds_write_b128 v32,a[8:11] offset:32768\n"
MF "v[76:79],v[96:99],v[240:243],v[76:79]\n"
LD "v8,s[36:39],s73 offen offset:3328 nt lds\n"
MF "v[80:83],v[96:99],v[244:247],v[80:83]\n"
"s_add_u32 s13,s13,0x12c00000\n"
"ds_write_b128 v32,a[12:15] offset:36864\n"
MF "v[84:87],v[96:99],v[248:251],v[84:87]\n"
"s_min_u32 s11,s11,s3\n"
"ds_write_b128 v32,a[16:19] offset:40960\n"
MF "v[88:91],v[96:99],v[252:255],v[88:91]\n"
"s_waitcnt lgkmcnt(7)\n"
"ds_read_b128 v[96:99],v1 offset:7168\n"
AR "v65,a23\n"
MF "v[112:115],v[100:103],v[136:139],0\n"
"s_min_u32 s4,s4,s5\n"
"ds_write_b128 v32,a[40:43] offset:45056\n"
MF "v[116:119],v[100:103],v[140:143],0\n"
"s_min_u32 s11,s11,s4\n"
"ds_write_b128 v32,a[44:47] offset:49152\n"
MF "v[120:123],v[100:103],v[144:147],0\n"
"s_and_b32 s11,s11,0x7f800000\n"
"ds_write_b128 v32,a[48:51] offset:53248\n"
MF "v[124:127],v[100:103],v[148:151],0\n"
"s_sub_u32 s11,s11,0x14000000\n"
"ds_write_b128 v32,a[52:55] offset:57344\n"
MF "v[128:131],v[100:103],v[152:155],0\n"
"s_waitcnt lgkmcnt(9)\n"
"ds_read_b128 v[100:103],v0 offset:8192\n"
AR "v66,a24\n"
MF "v[112:115],v[104:107],v[156:159],v[112:115]\n"
"s_min_u32 s10,s10,s6\n"
"v_max3_f32 v52,v72,v73,v74\n"
"v_max_f32 v52,v52,v75\n"
MF "v[116:119],v[104:107],v[160:163],v[116:119]\n"
"s_min_u32 s7,s7,s8\n"
"v_max3_f32 v53,v76,v77,v78\n"
"v_max_f32 v53,v53,v79\n"
MF "v[120:123],v[104:107],v[164:167],v[120:123]\n"
"s_min_u32 s10,s10,s7\n"
"v_max3_f32 v54,v80,v81,v82\n"
"v_max_f32 v54,v54,v83\n"
MF "v[124:127],v[104:107],v[168:171],v[124:127]\n"
"v_max3_f32 v55,v84,v85,v86\n"
"v_max_f32 v55,v55,v87\n"
MF "v[128:131],v[104:107],v[172:175],v[128:131]\n"
"s_waitcnt lgkmcnt(9)\n"
"ds_read_b128 v[104:107],v1 offset:8192\n"
"v_max3_f32 v56,v88,v89,v90\n"
MF "v[112:115],v[92:95],v[176:179],v[112:115]\n"
"s_cmp_lt_u32 s11,0x50000000\n"
"s_cselect_b32 s11,s11,0xed400000\n"
"v_max_f32 v56,v56,v91\n"
MF "v[116:119],v[92:95],v[180:183],v[116:119]\n"
"s_add_u32 s11,s11,0x12c00000\n"
"ds_write_b128 v32,a[56:59] offset:61440\n"
MF "v[120:123],v[92:95],v[184:187],v[120:123]\n"
"ds_write_b128 v33,a[80:83]\n"
MF "v[124:127],v[92:95],v[188:191],v[124:127]\n"
"ds_write_b128 v33,a[84:87] offset:4096\n"
MF "v[128:131],v[92:95],v[192:195],v[128:131]\n"
"s_waitcnt lgkmcnt(9)\n"
"ds_write_b128 v33,a[88:91] offset:8192\n"
MF "v[112:115],v[96:99],v[196:199],v[112:115]\n"
"s_waitcnt lgkmcnt(0)\n"
AR "v67,a25\n"
AR "v68,a26\n"
MF "v[116:119],v[96:99],v[200:203],v[116:119]\n"
"s_barrier\n"
MF "v[120:123],v[96:99],v[204:207],v[120:123]\n"
"s_add_u32 s72,s72,6144\n"
"ds_write_b128 v33,a[92:95] offset:12288\n"
MF "v[124:127],v[96:99],v[208:211],v[124:127]\n"
"s_cmp_eq_u32 s72,s17\n"
"s_cselect_b32 s72,s16,s72\n"
"s_add_u32 m0,s64,0\n"
MF "v[128:131],v[96:99],v[212:215],v[128:131]\n"
LD "v7,s[32:35],s72 offen nt lds\n"
MF "v[112:115],v[100:103],v[216:219],v[112:115]\n"
"s_add_u32 m0,s64,896\n"
"ds_write_b128 v33,a[96:99] offset:16384\n"
MF "v[116:119],v[100:103],v[220:223],v[116:119]\n"
LD "v7,s[32:35],s72 offen offset:128 nt lds\n"
MF "v[120:123],v[100:103],v[224:227],v[120:123]\n"
"s_add_u32 m0,s64,1792\n"
AR "v69,a27\n"
AR "v70,a28\n"
MF "v[124:127],v[100:103],v[228:231],v[124:127]\n"
LD "v7,s[32:35],s72 offen offset:256 nt lds\n"
MF "v[128:131],v[100:103],v[232:235],v[128:131]\n"
"s_add_u32 s72,s72,6144\n"
"s_add_u32 m0,s64,6144\n"
AR "v71,a29\n"
MF "v[112:115],v[104:107],v[236:239],v[112:115]\n"
LD "v7,s[32:35],s72 offen nt lds\n"
MF "v[116:119],v[104:107],v[240:243],v[116:119]\n"
"s_add_u32 m0,s64,7040\n"
"ds_write_b128 v33,a[120:123] offset:20480\n"
MF "v[120:123],v[104:107],v[244:247],v[120:123]\n"
LD "v7,s[32:35],s72 offen offset:128 nt lds\n"
MF "v[124:127],v[104:107],v[248:251],v[124:127]\n"
"s_add_u32 m0,s64,7936\n"
AR "v132,a30\n"
AR "v133,a31\n"
MF "v[128:131],v[104:107],v[252:255],v[128:131]\n"
LD "v7,s[32:35],s72 offen offset:256 nt lds\n"
AR "v134,a32\n"
AR "v135,a33\n"
AR "v6,a34\n"
AR "v13,a35\n"
AR "v14,a36\n"
AR "v15,a37\n"
"v_accvgpr_mov_b32 a254,a38\n"
"v_accvgpr_mov_b32 a255,a39\n"
"v_accvgpr_mov_b32 a240,a168\n"
"v_accvgpr_mov_b32 a241,a169\n"
"v_accvgpr_mov_b32 a242,a170\n"
"v_accvgpr_mov_b32 a243,a171\n"
"v_accvgpr_mov_b32 a244,a172\n"
"v_accvgpr_mov_b32 a245,a173\n"
"v_accvgpr_mov_b32 a246,a174\n"
"v_accvgpr_mov_b32 a247,a175\n"
"v_accvgpr_mov_b32 a248,a176\n"
"v_accvgpr_mov_b32 a249,a177\n"
"v_accvgpr_mov_b32 a250,a178\n"
"v_accvgpr_mov_b32 a251,a179\n"
".Lpgt0ex_b:\n"
"s_cbranch_execz .Lpg2a\n"
".Lt0ex:\n"
"s_nop 1\n"
"v_max3_f32 v52,v52,v112,v113\n"
"v_max3_f32 v53,v53,v116,v117\n"
"v_max3_f32 v54,v54,v120,v121\n"
"v_max3_f32 v55,v55,v124,v125\n"
"v_max3_f32 v56,v56,v128,v129\n"
"v_max3_f32 v52,v52,v114,v115\n"
"v_max3_f32 v53,v53,v118,v119\n"
"v_max3_f32 v54,v54,v122,v123\n"
"v_max3_f32 v55,v55,v126,v127\n"
"v_max3_f32 v56,v56,v130,v131\n"
"v_mov_b32 v57,v52\n"
"v_mov_b32 v58,v53\n"
"v_mov_b32 v59,v54\n"
"v_mov_b32 v60,v55\n"
"v_mov_b32 v61,v56\n"
"v_permlane16_swap_b32 v52,v57\n"
"v_permlane16_swap_b32 v53,v58\n"
"v_permlane16_swap_b32 v54,v59\n"
"v_permlane16_swap_b32 v55,v60\n"
"v_permlane16_swap_b32 v56,v61\n"
"v_max_f32 v52,v52,v57\n"
"v_max_f32 v53,v53,v58\n"
"v_max_f32 v54,v54,v59\n"
"v_max_f32 v55,v55,v60\n"
"v_max_f32 v56,v56,v61\n"
"v_mov_b32 v57,v52\n"
"v_mov_b32 v58,v53\n"
"v_mov_b32 v59,v54\n"
"v_mov_b32 v60,v55\n"
"v_mov_b32 v61,v56\n"
"v_permlane32_swap_b32 v52,v57\n"
"v_permlane32_swap_b32 v53,v58\n"
"v_permlane32_swap_b32 v54,v59\n"
"v_permlane32_swap_b32 v55,v60\n"
"v_permlane32_swap_b32 v56,v61\n"
"v_max_f32 v52,v52,v57\n"
"v_max_f32 v53,v53,v58\n"
"v_max_f32 v54,v54,v59\n"
"v_max_f32 v55,v55,v60\n"
"v_max_f32 v56,v56,v61\n"
"v_sub_f32 v32,0xc1300000,v52\n"
"v_sub_f32 v36,0xc1300000,v53\n"
"v_sub_f32 v40,0xc1300000,v54\n"
"v_sub_f32 v44,0xc1300000,v55\n"
"v_sub_f32 v48,0xc1300000,v56\n"
"v_mov_b32 v33,v32\n"
"v_pk_mov_b32 v[34:35],v[32:33],v[32:33]\n"
"v_mov_b32 v37,v36\n"
"v_pk_mov_b32 v[38:39],v[36:37],v[36:37]\n"
"v_mov_b32 v41,v40\n"
"v_pk_mov_b32 v[42:43],v[40:41],v[40:41]\n"
"v_mov_b32 v45,v44\n"
"v_pk_mov_b32 v[46:47],v[44:45],v[44:45]\n"
"v_mov_b32_e64 v49,v48\n"
"v_pk_mov_b32 v[50:51],v[48:49],v[48:49]\n"
"v_pk_add_f32 v[72:73],v[72:73],v[32:33]\n"
"v_pk_add_f32 v[74:75],v[74:75],v[32:33]\n"
"v_pk_add_f32 v[112:113],v[112:113],v[32:33]\n"
"v_pk_add_f32 v[114:115],v[114:115],v[32:33]\n"
"v_exp_f32 v72,v72\n"
"v_exp_f32 v73,v73\n"
"v_exp_f32 v74,v74\n"
"v_exp_f32 v75,v75\n"
"v_exp_f32 v112,v112\n"
"v_exp_f32 v113,v113\n"
"v_exp_f32 v114,v114\n"
"v_exp_f32 v115,v115\n"
"v_pk_add_f32 v[10:11],v[72:73],v[74:75]\n"
"v_pk_add_f32 v[28:29],v[112:113],v[114:115]\n"
"v_pk_add_f32 v[10:11],v[10:11],v[28:29]\n"
"v_add_f32_e64 v52,v10,v11\n"
CV "v72,v72,v73\n"
CV "v73,v74,v75\n"
CV "v74,v112,v113\n"
CV "v75,v114,v115\n"
"v_pk_add_f32 v[76:77],v[76:77],v[36:37]\n"
"v_pk_add_f32 v[78:79],v[78:79],v[36:37]\n"
"v_pk_add_f32 v[116:117],v[116:117],v[36:37]\n"
"v_pk_add_f32 v[118:119],v[118:119],v[36:37]\n"
"v_exp_f32 v76,v76\n"
"v_exp_f32 v77,v77\n"
"v_exp_f32 v78,v78\n"
"v_exp_f32 v79,v79\n"
"v_exp_f32 v116,v116\n"
"v_exp_f32 v117,v117\n"
"v_exp_f32 v118,v118\n"
"v_exp_f32 v119,v119\n"
"v_pk_add_f32 v[10:11],v[76:77],v[78:79]\n"
"v_pk_add_f32 v[28:29],v[116:117],v[118:119]\n"
"v_pk_add_f32 v[10:11],v[10:11],v[28:29]\n"
"v_add_f32_e64 v53,v10,v11\n"
CV "v76,v76,v77\n"
CV "v77,v78,v79\n"
CV "v78,v116,v117\n"
CV "v79,v118,v119\n"
"v_pk_add_f32 v[80:81],v[80:81],v[40:41]\n"
"v_pk_add_f32 v[82:83],v[82:83],v[40:41]\n"
"v_pk_add_f32 v[120:121],v[120:121],v[40:41]\n"
"v_pk_add_f32 v[122:123],v[122:123],v[40:41]\n"
"v_exp_f32 v80,v80\n"
"v_exp_f32 v81,v81\n"
"v_exp_f32 v82,v82\n"
"v_exp_f32 v83,v83\n"
"v_exp_f32 v120,v120\n"
"v_exp_f32 v121,v121\n"
"v_exp_f32 v122,v122\n"
"v_exp_f32 v123,v123\n"
"v_pk_add_f32 v[10:11],v[80:81],v[82:83]\n"
"v_pk_add_f32 v[28:29],v[120:121],v[122:123]\n"
"v_pk_add_f32 v[10:11],v[10:11],v[28:29]\n"
"v_add_f32_e64 v54,v10,v11\n"
CV "v80,v80,v81\n"
CV "v81,v82,v83\n"
CV "v82,v120,v121\n"
CV "v83,v122,v123\n"
"v_pk_add_f32 v[84:85],v[84:85],v[44:45]\n"
"v_pk_add_f32 v[86:87],v[86:87],v[44:45]\n"
"v_pk_add_f32 v[124:125],v[124:125],v[44:45]\n"
"v_pk_add_f32 v[126:127],v[126:127],v[44:45]\n"
"v_mov_b32_e64 v55,0\n"
"v_pk_add_f32 v[88:89],v[88:89],v[48:49]\n"
"v_pk_add_f32 v[90:91],v[90:91],v[48:49]\n"
"v_pk_add_f32 v[128:129],v[128:129],v[48:49]\n"
"v_pk_add_f32 v[130:131],v[130:131],v[48:49]\n"
"v_mov_b32 v56,0\n"
"s_waitcnt vmcnt(18) lgkmcnt(0)\n"
"s_barrier\n"
"ds_read_b128 v[16:19],v0 offset:24576\n"
"ds_read_b128 v[20:23],v1 offset:24576\n"
AW "a220,s13\n"
AW "a221,s13\n"
AW "a222,s13\n"
AW "a223,s13\n"
"s_waitcnt lgkmcnt(1)\n"
MF "v[92:95],v[16:19],v[136:139],v[32:35]\n"
"s_nop 0\n"
"ds_read_b128 v[24:27],v0 offset:25600\n"
"ds_read_b128 v[28:31],v1 offset:25600\n"
"s_waitcnt lgkmcnt(2)\n"
MF "v[92:95],v[20:23],v[156:159],v[92:95]\n"
"v_exp_f32 v84,v84\n"
MF "v[96:99],v[20:23],v[160:163],v[36:39]\n"
"v_exp_f32 v85,v85\n"
MF "v[96:99],v[16:19],v[140:143],v[96:99]\n"
"v_exp_f32 v86,v86\n"
MF "v[100:103],v[16:19],v[144:147],v[40:43]\n"
"v_exp_f32 v87,v87\n"
MF "v[100:103],v[20:23],v[164:167],v[100:103]\n"
"v_add_f32 v55,v55,v84\n"
"v_add_f32 v55,v55,v85\n"
MF "v[104:107],v[20:23],v[168:171],v[44:47]\n"
CV "v84,v84,v85\n"
"v_add_f32 v55,v55,v86\n"
MF "v[104:107],v[16:19],v[148:151],v[104:107]\n"
"v_add_f32 v55,v55,v87\n"
CV "v85,v86,v87\n"
MF "v[108:111],v[16:19],v[152:155],v[48:51]\n"
"v_exp_f32 v124,v124\n"
MF "v[108:111],v[20:23],v[172:175],v[108:111]\n"
"s_waitcnt lgkmcnt(0)\n"
"ds_read_b128 v[16:19],v0 offset:26624\n"
"ds_read_b128 v[20:23],v1 offset:26624\n"
MF "v[108:111],v[24:27],v[192:195],v[108:111]\n"
"v_exp_f32 v125,v125\n"
MF "v[108:111],v[28:31],v[212:215],v[108:111]\n"
"v_exp_f32 v126,v126\n"
MF "v[104:107],v[28:31],v[208:211],v[104:107]\n"
"v_exp_f32 v127,v127\n"
MF "v[104:107],v[24:27],v[188:191],v[104:107]\n"
"v_add_f32 v55,v55,v124\n"
"v_add_f32 v55,v55,v125\n"
MF "v[100:103],v[24:27],v[184:187],v[100:103]\n"
CV "v86,v124,v125\n"
"v_add_f32 v55,v55,v126\n"
MF "v[100:103],v[28:31],v[204:207],v[100:103]\n"
"v_add_f32 v55,v55,v127\n"
CV "v87,v126,v127\n"
MF "v[96:99],v[28:31],v[200:203],v[96:99]\n"
"v_exp_f32 v88,v88\n"
MF "v[96:99],v[24:27],v[180:183],v[96:99]\n"
"v_exp_f32 v89,v89\n"
MF "v[92:95],v[24:27],v[176:179],v[92:95]\n"
"v_exp_f32 v90,v90\n"
MF "v[92:95],v[28:31],v[196:199],v[92:95]\n"
"s_waitcnt lgkmcnt(0)\n"
"ds_read_b128 v[24:27],v0 offset:30720\n"
"ds_read_b128 v[28:31],v1 offset:30720\n"
MF "v[92:95],v[16:19],v[216:219],v[92:95]\n"
"v_exp_f32 v91,v91\n"
MF "v[92:95],v[20:23],v[236:239],v[92:95]\n"
"v_add_f32 v56,v56,v88\n"
"v_add_f32 v56,v56,v89\n"
MF "v[96:99],v[20:23],v[240:243],v[96:99]\n"
CV "v88,v88,v89\n"
"v_add_f32 v56,v56,v90\n"
MF "v[96:99],v[16:19],v[220:223],v[96:99]\n"
"v_add_f32 v56,v56,v91\n"
CV "v89,v90,v91\n"
MF "v[100:103],v[16:19],v[224:227],v[100:103]\n"
"v_exp_f32 v128,v128\n"
MF "v[100:103],v[20:23],v[244:247],v[100:103]\n"
"v_exp_f32 v129,v129\n"
MF "v[104:107],v[20:23],v[248:251],v[104:107]\n"
"v_exp_f32 v130,v130\n"
MF "v[104:107],v[16:19],v[228:231],v[104:107]\n"
"v_exp_f32 v131,v131\n"
MF "v[108:111],v[16:19],v[232:235],v[108:111]\n"
"v_add_f32 v56,v56,v128\n"
"v_add_f32 v56,v56,v129\n"
MF "v[108:111],v[20:23],v[252:255],v[108:111]\n"
"s_waitcnt lgkmcnt(0)\n"
"ds_read_b128 v[16:19],v0 offset:31744\n"
"ds_read_b128 v[20:23],v1 offset:31744\n"
MF "v[112:115],v[24:27],v[136:139],v[32:35]\n"
CV "v90,v128,v129\n"
"v_add_f32 v56,v56,v130\n"
MF "v[112:115],v[28:31],v[156:159],v[112:115]\n"
"v_add_f32 v56,v56,v131\n"
CV "v91,v130,v131\n"
MF "v[116:119],v[28:31],v[160:163],v[36:39]\n"
"v_exp_f32 v92,v92\n"
MF "v[116:119],v[24:27],v[140:143],v[116:119]\n"
"v_exp_f32 v93,v93\n"
MF "v[120:123],v[24:27],v[144:147],v[40:43]\n"
"v_exp_f32 v94,v94\n"
MF "v[120:123],v[28:31],v[164:167],v[120:123]\n"
"v_exp_f32 v95,v95\n"
MF "v[124:127],v[28:31],v[168:171],v[44:47]\n"
"v_add_f32 v57,v52,v92\n"
"v_add_f32 v57,v57,v93\n"
MF "v[124:127],v[24:27],v[148:151],v[124:127]\n"
CV "v92,v92,v93\n"
"v_add_f32 v57,v57,v94\n"
MF "v[128:131],v[24:27],v[152:155],v[48:51]\n"
"v_add_f32 v57,v57,v95\n"
CV "v93,v94,v95\n"
MF "v[128:131],v[28:31],v[172:175],v[128:131]\n"
"s_waitcnt lgkmcnt(0)\n"
"ds_read_b128 v[24:27],v0 offset:32768\n"
"ds_read_b128 v[28:31],v1 offset:32768\n"
MF "v[128:131],v[16:19],v[192:195],v[128:131]\n"
"v_exp_f32 v96,v96\n"
MF "v[128:131],v[20:23],v[212:215],v[128:131]\n"
"v_exp_f32 v97,v97\n"
MF "v[124:127],v[20:23],v[208:211],v[124:127]\n"
"v_exp_f32 v98,v98\n"
MF "v[124:127],v[16:19],v[188:191],v[124:127]\n"
"v_exp_f32 v99,v99\n"
MF "v[120:123],v[16:19],v[184:187],v[120:123]\n"
"v_add_f32 v58,v53,v96\n"
"v_add_f32 v58,v58,v97\n"
MF "v[120:123],v[20:23],v[204:207],v[120:123]\n"
CV "v96,v96,v97\n"
"v_add_f32 v58,v58,v98\n"
MF "v[116:119],v[20:23],v[200:203],v[116:119]\n"
"s_waitcnt vmcnt(12) lgkmcnt(0)\n"
"v_add_f32 v58,v58,v99\n"
CV "v97,v98,v99\n"
MF "v[116:119],v[16:19],v[180:183],v[116:119]\n"
"s_barrier\n"
MF "v[112:115],v[16:19],v[176:179],v[112:115]\n"
"v_exp_f32 v100,v100\n"
MF "v[112:115],v[20:23],v[196:199],v[112:115]\n"
"s_nop 0\n"
TR "v[16:17],v2\n"
TR "v[18:19],v2 offset:6144\n"
MF "v[112:115],v[24:27],v[216:219],v[112:115]\n"
"s_add_u32 s72,s72,6144\n"
TR "v[20:21],v3\n"
TR "v[22:23],v3 offset:6144\n"
MF "v[112:115],v[28:31],v[236:239],v[112:115]\n"
"s_cmp_eq_u32 s72,s17\n"
"s_cselect_b32 s72,s16,s72\n"
"s_add_u32 m0,s64,24576\n"
MF "v[116:119],v[28:31],v[240:243],v[116:119]\n"
LD "v7,s[32:35],s72 offen nt lds\n"
MF "v[116:119],v[24:27],v[220:223],v[116:119]\n"
"s_add_u32 m0,s64,25472\n"
"v_exp_f32 v101,v101\n"
MF "v[120:123],v[24:27],v[224:227],v[120:123]\n"
LD "v7,s[32:35],s72 offen offset:128 nt lds\n"
MF "v[120:123],v[28:31],v[244:247],v[120:123]\n"
"v_exp_f32 v102,v102\n"
MF "v[124:127],v[28:31],v[248:251],v[124:127]\n"
"s_add_u32 m0,s64,26368\n"
"v_exp_f32 v103,v103\n"
MF "v[124:127],v[24:27],v[228:231],v[124:127]\n"
LD "v7,s[32:35],s72 offen offset:256 nt lds\n"
MF "v[128:131],v[24:27],v[232:235],v[128:131]\n"
"s_add_u32 s72,s72,6144\n"
"v_add_f32 v59,v54,v100\n"
"v_add_f32 v59,v59,v101\n"
MF "v[128:131],v[28:31],v[252:255],v[128:131]\n"
"s_waitcnt lgkmcnt(0)\n"
TR "v[24:25],v4\n"
TR "v[26:27],v4 offset:6144\n"
MF "a[0:3],v[16:19],v[72:75],a[220:223]\n"
"s_add_u32 m0,s64,30720\n"
TR "v[28:29],v5\n"
TR "v[30:31],v5 offset:6144\n"
MF "a[20:23],v[20:23],v[72:75],a[220:223]\n"
LD "v7,s[32:35],s72 offen nt lds\n"
MF "a[24:27],v[20:23],v[76:79],a[220:223]\n"
CV "v100,v100,v101\n"
"v_add_f32 v59,v59,v102\n"
MF "a[4:7],v[16:19],v[76:79],a[220:223]\n"
"s_add_u32 m0,s64,31616\n"
"v_add_f32 v59,v59,v103\n"
CV "v101,v102,v103\n"
MF "a[8:11],v[16:19],v[80:83],a[220:223]\n"
LD "v7,s[32:35],s72 offen offset:128 nt lds\n"
MF "a[28:31],v[20:23],v[80:83],a[220:223]\n"
"v_exp_f32 v104,v104\n"
MF "a[32:35],v[20:23],v[84:87],a[220:223]\n"
"s_add_u32 m0,s64,32512\n"
"v_exp_f32 v105,v105\n"
MF "a[12:15],v[16:19],v[84:87],a[220:223]\n"
LD "v7,s[32:35],s72 offen offset:256 nt lds\n"
MF "a[16:19],v[16:19],v[88:91],a[220:223]\n"
"v_exp_f32 v106,v106\n"
MF "a[36:39],v[20:23],v[88:91],a[220:223]\n"
"s_waitcnt lgkmcnt(0)\n"
TR "v[16:17],v2 offset:1024\n"
TR "v[18:19],v2 offset:7168\n"
MF "a[56:59],v[24:27],v[88:91],a[220:223]\n"
"s_nop 0\n"
TR "v[20:21],v3 offset:1024\n"
TR "v[22:23],v3 offset:7168\n"
MF "a[76:79],v[28:31],v[88:91],a[220:223]\n"
"v_exp_f32 v107,v107\n"
MF "a[72:75],v[28:31],v[84:87],a[220:223]\n"
"v_add_f32 v60,v55,v104\n"
"v_add_f32 v60,v60,v105\n"
MF "a[52:55],v[24:27],v[84:87],a[220:223]\n"
CV "v104,v104,v105\n"
"v_add_f32 v60,v60,v106\n"
MF "a[48:51],v[24:27],v[80:83],a[220:223]\n"
"v_add_f32 v60,v60,v107\n"
CV "v105,v106,v107\n"
MF "a[68:71],v[28:31],v[80:83],a[220:223]\n"
"v_exp_f32 v108,v108\n"
MF "a[64:67],v[28:31],v[76:79],a[220:223]\n"
"v_exp_f32 v109,v109\n"
MF "a[44:47],v[24:27],v[76:79],a[220:223]\n"
"v_exp_f32 v110,v110\n"
MF "a[40:43],v[24:27],v[72:75],a[220:223]\n"
"v_exp_f32 v111,v111\n"
MF "a[60:63],v[28:31],v[72:75],a[220:223]\n"
".Lpg10b:\n"
"s_cbranch_execz .Lpg1a\n"
".Lpg10d:\n"
"s_cbranch_execz .Lisr2\n"
"s_waitcnt lgkmcnt(0)\n"
TR "v[24:25],v4 offset:1024\n"
TR "v[26:27],v4 offset:7168\n"
MF "a[80:83],v[16:19],v[72:75],a[220:223]\n"
"s_nop 0\n"
TR "v[28:29],v5 offset:1024\n"
TR "v[30:31],v5 offset:7168\n"
MF "a[100:103],v[20:23],v[72:75],a[220:223]\n"
"v_add_f32 v61,v56,v108\n"
"v_add_f32 v61,v61,v109\n"
MF "a[104:107],v[20:23],v[76:79],a[220:223]\n"
CV "v108,v108,v109\n"
"v_add_f32 v61,v61,v110\n"
MF "a[84:87],v[16:19],v[76:79],a[220:223]\n"
"v_add_f32 v61,v61,v111\n"
CV "v109,v110,v111\n"
MF "a[88:91],v[16:19],v[80:83],a[220:223]\n"
"v_exp_f32 v112,v112\n"
MF "a[108:111],v[20:23],v[80:83],a[220:223]\n"
"v_exp_f32 v113,v113\n"
MF "a[112:115],v[20:23],v[84:87],a[220:223]\n"
"v_exp_f32 v114,v114\n"
MF "a[92:95],v[16:19],v[84:87],a[220:223]\n"
"v_exp_f32 v115,v115\n"
MF "a[96:99],v[16:19],v[88:91],a[220:223]\n"
"v_add_f32 v57,v57,v112\n"
"v_add_f32 v57,v57,v113\n"
MF "a[116:119],v[20:23],v[88:91],a[220:223]\n"
"s_waitcnt lgkmcnt(0)\n"
TR "v[16:17],v2 offset:2048\n"
TR "v[18:19],v2 offset:8192\n"
MF "a[136:139],v[24:27],v[88:91],a[220:223]\n"
"s_nop 0\n"
TR "v[20:21],v3 offset:2048\n"
TR "v[22:23],v3 offset:8192\n"
MF "a[156:159],v[28:31],v[88:91],a[220:223]\n"
CV "v94,v112,v113\n"
"v_add_f32 v57,v57,v114\n"
MF "a[152:155],v[28:31],v[84:87],a[220:223]\n"
"v_add_f32 v57,v57,v115\n"
CV "v95,v114,v115\n"
MF "a[132:135],v[24:27],v[84:87],a[220:223]\n"
"v_exp_f32 v116,v116\n"
MF "a[128:131],v[24:27],v[80:83],a[220:223]\n"
"v_exp_f32 v117,v117\n"
MF "a[148:151],v[28:31],v[80:83],a[220:223]\n"
"v_exp_f32 v118,v118\n"
MF "a[144:147],v[28:31],v[76:79],a[220:223]\n"
"v_exp_f32 v119,v119\n"
MF "a[124:127],v[24:27],v[76:79],a[220:223]\n"
"v_add_f32 v58,v58,v116\n"
"v_add_f32 v58,v58,v117\n"
MF "a[120:123],v[24:27],v[72:75],a[220:223]\n"
CV "v98,v116,v117\n"
"v_add_f32 v58,v58,v118\n"
MF "a[140:143],v[28:31],v[72:75],a[220:223]\n"
"s_waitcnt lgkmcnt(0)\n"
TR "v[24:25],v4 offset:2048\n"
TR "v[26:27],v4 offset:8192\n"
MF "a[160:163],v[16:19],v[72:75],a[220:223]\n"
"s_nop 0\n"
TR "v[28:29],v5 offset:2048\n"
TR "v[30:31],v5 offset:8192\n"
MF "a[180:183],v[20:23],v[72:75],a[220:223]\n"
"v_add_f32 v58,v58,v119\n"
CV "v99,v118,v119\n"
MF "a[184:187],v[20:23],v[76:79],a[220:223]\n"
"v_exp_f32 v120,v120\n"
MF "a[164:167],v[16:19],v[76:79],a[220:223]\n"
"v_exp_f32 v121,v121\n"
MF "a[168:171],v[16:19],v[80:83],a[220:223]\n"
"v_exp_f32 v122,v122\n"
MF "a[188:191],v[20:23],v[80:83],a[220:223]\n"
"v_exp_f32 v123,v123\n"
MF "a[192:195],v[20:23],v[84:87],a[220:223]\n"
"v_add_f32 v59,v59,v120\n"
"v_add_f32 v59,v59,v121\n"
MF "a[172:175],v[16:19],v[84:87],a[220:223]\n"
"s_waitcnt vmcnt(6) lgkmcnt(0)\n"
CV "v102,v120,v121\n"
"v_add_f32 v59,v59,v122\n"
MF "a[176:179],v[16:19],v[88:91],a[220:223]\n"
"s_barrier\n"
MF "a[196:199],v[20:23],v[88:91],a[220:223]\n"
"s_add_u32 s73,s73,12288\n"
"ds_read_b128 v[16:19],v0\n"
"ds_read_b128 v[20:23],v1\n"
MF "a[216:219],v[24:27],v[88:91],a[220:223]\n"
"s_cmp_eq_u32 s73,s19\n"
"s_cselect_b32 s73,s18,s73\n"
"s_add_u32 m0,s65,49152\n"
MF "a[236:239],v[28:31],v[88:91],a[220:223]\n"
LD "v8,s[36:39],s73 offen nt lds\n"
MF "a[232:235],v[28:31],v[84:87],a[220:223]\n"
"s_add_u32 m0,s65,50048\n"
"v_add_f32 v59,v59,v123\n"
CV "v103,v122,v123\n"
MF "a[212:215],v[24:27],v[84:87],a[220:223]\n"
LD "v8,s[36:39],s73 offen offset:128 nt lds\n"
MF "a[208:211],v[24:27],v[80:83],a[220:223]\n"
"s_add_u32 m0,s65,50944\n"
MF "a[228:231],v[28:31],v[80:83],a[220:223]\n"
LD "v8,s[36:39],s73 offen offset:256 nt lds\n"
MF "a[224:227],v[28:31],v[76:79],a[220:223]\n"
MF "a[204:207],v[24:27],v[76:79],a[220:223]\n"
MF "a[200:203],v[24:27],v[72:75],a[220:223]\n"
MF "a[220:223],v[28:31],v[72:75],a[220:223]\n"
"s_waitcnt lgkmcnt(0)\n"
".Laloopx:\n"
MF "v[72:75],v[16:19],v[136:139],v[32:35]\n"
"s_nop 0\n"
"ds_read_b128 v[24:27],v0 offset:1024\n"
"ds_read_b128 v[28:31],v1 offset:1024\n"
MF "v[72:75],v[20:23],v[156:159],v[72:75]\n"
MF "v[76:79],v[20:23],v[160:163],v[36:39]\n"
MF "v[76:79],v[16:19],v[140:143],v[76:79]\n"
MF "v[80:83],v[16:19],v[144:147],v[40:43]\n"
MF "v[80:83],v[20:23],v[164:167],v[80:83]\n"
MF "v[84:87],v[20:23],v[168:171],v[44:47]\n"
MF "v[84:87],v[16:19],v[148:151],v[84:87]\n"
"s_add_u32 m0,s65,49152\n"
MF "v[88:91],v[16:19],v[152:155],v[48:51]\n"
LD "v8,s[36:39],s73 offen offset:3072 nt lds\n"
MF "v[88:91],v[20:23],v[172:175],v[88:91]\n"
"s_waitcnt lgkmcnt(0)\n"
"ds_read_b128 v[16:19],v0 offset:2048\n"
"ds_read_b128 v[20:23],v1 offset:2048\n"
MF "v[88:91],v[24:27],v[192:195],v[88:91]\n"
"v_exp_f32 v124,v124\n"
MF "v[88:91],v[28:31],v[212:215],v[88:91]\n"
"s_add_u32 m0,s65,50048\n"
"v_exp_f32 v125,v125\n"
MF "v[84:87],v[28:31],v[208:211],v[84:87]\n"
LD "v8,s[36:39],s73 offen offset:3200 nt lds\n"
MF "v[84:87],v[24:27],v[188:191],v[84:87]\n"
"v_exp_f32 v126,v126\n"
MF "v[80:83],v[24:27],v[184:187],v[80:83]\n"
"v_exp_f32 v127,v127\n"
MF "v[80:83],v[28:31],v[204:207],v[80:83]\n"
"s_add_u32 m0,s65,50944\n"
"v_add_f32 v60,v60,v124\n"
"v_add_f32 v60,v60,v125\n"
MF "v[76:79],v[28:31],v[200:203],v[76:79]\n"
LD "v8,s[36:39],s73 offen offset:3328 nt lds\n"
MF "v[76:79],v[24:27],v[180:183],v[76:79]\n"
CV "v106,v124,v125\n"
"v_add_f32 v60,v60,v126\n"
MF "v[72:75],v[24:27],v[176:179],v[72:75]\n"
"v_add_f32 v60,v60,v127\n"
CV "v107,v126,v127\n"
MF "v[72:75],v[28:31],v[196:199],v[72:75]\n"
"s_waitcnt lgkmcnt(0)\n"
"ds_read_b128 v[24:27],v0 offset:6144\n"
"ds_read_b128 v[28:31],v1 offset:6144\n"
MF "v[72:75],v[16:19],v[216:219],v[72:75]\n"
"v_exp_f32 v128,v128\n"
MF "v[72:75],v[20:23],v[236:239],v[72:75]\n"
"v_exp_f32 v129,v129\n"
MF "v[76:79],v[20:23],v[240:243],v[76:79]\n"
"v_exp_f32 v130,v130\n"
MF "v[76:79],v[16:19],v[220:223],v[76:79]\n"
"v_exp_f32 v131,v131\n"
MF "v[80:83],v[16:19],v[224:227],v[80:83]\n"
"v_add_f32 v61,v61,v128\n"
"v_add_f32 v61,v61,v129\n"
MF "v[80:83],v[20:23],v[244:247],v[80:83]\n"
CV "v110,v128,v129\n"
"v_add_f32 v61,v61,v130\n"
MF "v[84:87],v[20:23],v[248:251],v[84:87]\n"
"v_add_f32 v61,v61,v131\n"
CV "v111,v130,v131\n"
MF "v[84:87],v[16:19],v[228:231],v[84:87]\n"
"v_max3_f32 v10,v57,v58,v59\n"
"v_max3_f32 v10,v10,v60,v61\n"
MF "v[88:91],v[16:19],v[232:235],v[88:91]\n"
"v_cmp_ngt_f32 vcc,s95,v10\n"
MF "v[88:91],v[20:23],v[252:255],v[88:91]\n"
"s_waitcnt lgkmcnt(0)\n"
"s_cbranch_vccnz .Lstub_6\n"
".Lback_6:\n"
MF "v[112:115],v[24:27],v[136:139],v[32:35]\n"
"s_add_u32 s74,s74,12288\n"
"ds_read_b128 v[16:19],v0 offset:7168\n"
"ds_read_b128 v[20:23],v1 offset:7168\n"
MF "v[112:115],v[28:31],v[156:159],v[112:115]\n"
"s_cmp_eq_u32 s74,s20\n"
"s_cselect_b32 s74,s70,s74\n"
MF "v[116:119],v[28:31],v[160:163],v[36:39]\n"
"v_exp_f32 v72,v72\n"
MF "v[116:119],v[24:27],v[140:143],v[116:119]\n"
"v_exp_f32 v73,v73\n"
MF "v[120:123],v[24:27],v[144:147],v[40:43]\n"
"v_exp_f32 v74,v74\n"
MF "v[120:123],v[28:31],v[164:167],v[120:123]\n"
"v_exp_f32 v75,v75\n"
MF "v[124:127],v[28:31],v[168:171],v[44:47]\n"
"v_add_f32 v52,v57,v72\n"
"v_add_f32 v52,v52,v73\n"
MF "v[124:127],v[24:27],v[148:151],v[124:127]\n"
CV "v72,v72,v73\n"
"v_add_f32 v52,v52,v74\n"
MF "v[128:131],v[24:27],v[152:155],v[48:51]\n"
"v_add_f32 v52,v52,v75\n"
CV "v73,v74,v75\n"
MF "v[128:131],v[28:31],v[172:175],v[128:131]\n"
"s_waitcnt lgkmcnt(0)\n"
"ds_read_b128 v[24:27],v0 offset:8192\n"
"ds_read_b128 v[28:31],v1 offset:8192\n"
MF "v[128:131],v[16:19],v[192:195],v[128:131]\n"
"v_exp_f32 v76,v76\n"
MF "v[128:131],v[20:23],v[212:215],v[128:131]\n"
"v_exp_f32 v77,v77\n"
MF "v[124:127],v[20:23],v[208:211],v[124:127]\n"
"v_exp_f32 v78,v78\n"
MF "v[124:127],v[16:19],v[188:191],v[124:127]\n"
"v_exp_f32 v79,v79\n"
MF "v[120:123],v[16:19],v[184:187],v[120:123]\n"
"v_add_f32 v53,v58,v76\n"
"v_add_f32 v53,v53,v77\n"
MF "v[120:123],v[20:23],v[204:207],v[120:123]\n"
CV "v76,v76,v77\n"
"v_add_f32 v53,v53,v78\n"
MF "v[116:119],v[20:23],v[200:203],v[116:119]\n"
"s_waitcnt vmcnt(12) lgkmcnt(0)\n"
"v_add_f32 v53,v53,v79\n"
CV "v77,v78,v79\n"
MF "v[116:119],v[16:19],v[180:183],v[116:119]\n"
"s_barrier\n"
MF "v[112:115],v[16:19],v[176:179],v[112:115]\n"
"v_exp_f32 v80,v80\n"
MF "v[112:115],v[20:23],v[196:199],v[112:115]\n"
"s_nop 0\n"
TR "v[16:17],v2 offset:24576\n"
TR "v[18:19],v2 offset:30720\n"
MF "v[112:115],v[24:27],v[216:219],v[112:115]\n"
"s_add_u32 s72,s72,6144\n"
TR "v[20:21],v3 offset:24576\n"
TR "v[22:23],v3 offset:30720\n"
MF "v[112:115],v[28:31],v[236:239],v[112:115]\n"
"s_cmp_eq_u32 s72,s17\n"
"s_cselect_b32 s72,s16,s72\n"
"s_add_u32 m0,s64,0\n"
MF "v[116:119],v[28:31],v[240:243],v[116:119]\n"
LD "v7,s[32:35],s72 offen nt lds\n"
MF "v[116:119],v[24:27],v[220:223],v[116:119]\n"
"s_add_u32 m0,s64,896\n"
"v_exp_f32 v81,v81\n"
MF "v[120:123],v[24:27],v[224:227],v[120:123]\n"
LD "v7,s[32:35],s72 offen offset:128 nt lds\n"
MF "v[120:123],v[28:31],v[244:247],v[120:123]\n"
"v_exp_f32 v82,v82\n"
MF "v[124:127],v[28:31],v[248:251],v[124:127]\n"
"s_add_u32 m0,s64,1792\n"
"v_exp_f32 v83,v83\n"
MF "v[124:127],v[24:27],v[228:231],v[124:127]\n"
LD "v7,s[32:35],s72 offen offset:256 nt lds\n"
MF "v[128:131],v[24:27],v[232:235],v[128:131]\n"
"s_add_u32 s72,s72,6144\n"
"v_add_f32 v54,v59,v80\n"
"v_add_f32 v54,v54,v81\n"
MF "v[128:131],v[28:31],v[252:255],v[128:131]\n"
"s_waitcnt lgkmcnt(0)\n"
TR "v[24:25],v4 offset:24576\n"
TR "v[26:27],v4 offset:30720\n"
MF "a[0:3],v[16:19],v[92:95],a[0:3]\n"
"s_add_u32 m0,s64,6144\n"
TR "v[28:29],v5 offset:24576\n"
TR "v[30:31],v5 offset:30720\n"
MF "a[20:23],v[20:23],v[92:95],a[20:23]\n"
LD "v7,s[32:35],s72 offen nt lds\n"
MF "a[24:27],v[20:23],v[96:99],a[24:27]\n"
CV "v80,v80,v81\n"
"v_add_f32 v54,v54,v82\n"
MF "a[4:7],v[16:19],v[96:99],a[4:7]\n"
"s_add_u32 m0,s64,7040\n"
"v_add_f32 v54,v54,v83\n"
CV "v81,v82,v83\n"
MF "a[8:11],v[16:19],v[100:103],a[8:11]\n"
LD "v7,s[32:35],s72 offen offset:128 nt lds\n"
MF "a[28:31],v[20:23],v[100:103],a[28:31]\n"
"v_exp_f32 v84,v84\n"
MF "a[32:35],v[20:23],v[104:107],a[32:35]\n"
"s_add_u32 m0,s64,7936\n"
"v_exp_f32 v85,v85\n"
MF "a[12:15],v[16:19],v[104:107],a[12:15]\n"
LD "v7,s[32:35],s72 offen offset:256 nt lds\n"
MF "a[16:19],v[16:19],v[108:111],a[16:19]\n"
"v_exp_f32 v86,v86\n"
MF "a[36:39],v[20:23],v[108:111],a[36:39]\n"
"s_waitcnt lgkmcnt(0)\n"
TR "v[16:17],v2 offset:25600\n"
TR "v[18:19],v2 offset:31744\n"
MF "a[56:59],v[24:27],v[108:111],a[56:59]\n"
"s_nop 0\n"
TR "v[20:21],v3 offset:25600\n"
TR "v[22:23],v3 offset:31744\n"
MF "a[76:79],v[28:31],v[108:111],a[76:79]\n"
"v_exp_f32 v87,v87\n"
MF "a[72:75],v[28:31],v[104:107],a[72:75]\n"
"v_add_f32 v55,v60,v84\n"
"v_add_f32 v55,v55,v85\n"
MF "a[52:55],v[24:27],v[104:107],a[52:55]\n"
CV "v84,v84,v85\n"
"v_add_f32 v55,v55,v86\n"
MF "a[48:51],v[24:27],v[100:103],a[48:51]\n"
"v_add_f32 v55,v55,v87\n"
CV "v85,v86,v87\n"
MF "a[68:71],v[28:31],v[100:103],a[68:71]\n"
"v_exp_f32 v88,v88\n"
MF "a[64:67],v[28:31],v[96:99],a[64:67]\n"
"v_exp_f32 v89,v89\n"
MF "a[44:47],v[24:27],v[96:99],a[44:47]\n"
"v_exp_f32 v90,v90\n"
MF "a[40:43],v[24:27],v[92:95],a[40:43]\n"
"v_exp_f32 v91,v91\n"
MF "a[60:63],v[28:31],v[92:95],a[60:63]\n"
"s_waitcnt lgkmcnt(0)\n"
TR "v[24:25],v4 offset:25600\n"
TR "v[26:27],v4 offset:31744\n"
MF "a[80:83],v[16:19],v[92:95],a[80:83]\n"
"s_nop 0\n"
TR "v[28:29],v5 offset:25600\n"
TR "v[30:31],v5 offset:31744\n"
MF "a[100:103],v[20:23],v[92:95],a[100:103]\n"
"v_add_f32 v56,v61,v88\n"
"v_add_f32 v56,v56,v89\n"
MF "a[104:107],v[20:23],v[96:99],a[104:107]\n"
CV "v88,v88,v89\n"
"v_add_f32 v56,v56,v90\n"
MF "a[84:87],v[16:19],v[96:99],a[84:87]\n"
"v_add_f32 v56,v56,v91\n"
CV "v89,v90,v91\n"
MF "a[88:91],v[16:19],v[100:103],a[88:91]\n"
"v_exp_f32 v112,v112\n"
MF "a[108:111],v[20:23],v[100:103],a[108:111]\n"
"v_exp_f32 v113,v113\n"
MF "a[112:115],v[20:23],v[104:107],a[112:115]\n"
"v_exp_f32 v114,v114\n"
MF "a[92:95],v[16:19],v[104:107],a[92:95]\n"
"v_exp_f32 v115,v115\n"
MF "a[96:99],v[16:19],v[108:111],a[96:99]\n"
"v_add_f32 v52,v52,v112\n"
"v_add_f32 v52,v52,v113\n"
MF "a[116:119],v[20:23],v[108:111],a[116:119]\n"
"s_waitcnt lgkmcnt(0)\n"
TR "v[16:17],v2 offset:26624\n"
TR "v[18:19],v2 offset:32768\n"
MF "a[136:139],v[24:27],v[108:111],a[136:139]\n"
"s_nop 0\n"
TR "v[20:21],v3 offset:26624\n"
TR "v[22:23],v3 offset:32768\n"
MF "a[156:159],v[28:31],v[108:111],a[156:159]\n"
CV "v74,v112,v113\n"
"v_add_f32 v52,v52,v114\n"
MF "a[152:155],v[28:31],v[104:107],a[152:155]\n"
"v_add_f32 v52,v52,v115\n"
CV "v75,v114,v115\n"
MF "a[132:135],v[24:27],v[104:107],a[132:135]\n"
"v_exp_f32 v116,v116\n"
MF "a[128:131],v[24:27],v[100:103],a[128:131]\n"
"v_exp_f32 v117,v117\n"
MF "a[148:151],v[28:31],v[100:103],a[148:151]\n"
"v_exp_f32 v118,v118\n"
MF "a[144:147],v[28:31],v[96:99],a[144:147]\n"
"v_exp_f32 v119,v119\n"
MF "a[124:127],v[24:27],v[96:99],a[124:127]\n"
"v_add_f32 v53,v53,v116\n"
"v_add_f32 v53,v53,v117\n"
MF "a[120:123],v[24:27],v[92:95],a[120:123]\n"
CV "v78,v116,v117\n"
"v_add_f32 v53,v53,v118\n"
MF "a[140:143],v[28:31],v[92:95],a[140:143]\n"
"s_waitcnt lgkmcnt(0)\n"
TR "v[24:25],v4 offset:26624\n"
TR "v[26:27],v4 offset:32768\n"
MF "a[160:163],v[16:19],v[92:95],a[160:163]\n"
"s_nop 0\n"
TR "v[28:29],v5 offset:26624\n"
TR "v[30:31],v5 offset:32768\n"
MF "a[180:183],v[20:23],v[92:95],a[180:183]\n"
"v_add_f32 v53,v53,v119\n"
CV "v79,v118,v119\n"
MF "a[184:187],v[20:23],v[96:99],a[184:187]\n"
"v_exp_f32 v120,v120\n"
MF "a[164:167],v[16:19],v[96:99],a[164:167]\n"
"v_exp_f32 v121,v121\n"
MF "a[168:171],v[16:19],v[100:103],a[168:171]\n"
"v_exp_f32 v122,v122\n"
MF "a[188:191],v[20:23],v[100:103],a[188:191]\n"
"v_exp_f32 v123,v123\n"
MF "a[192:195],v[20:23],v[104:107],a[192:195]\n"
"v_add_f32 v54,v54,v120\n"
"v_add_f32 v54,v54,v121\n"
MF "a[172:175],v[16:19],v[104:107],a[172:175]\n"
"s_waitcnt vmcnt(12) lgkmcnt(0)\n"
CV "v82,v120,v121\n"
"v_add_f32 v54,v54,v122\n"
MF "a[176:179],v[16:19],v[108:111],a[176:179]\n"
"s_barrier\n"
MF "a[196:199],v[20:23],v[108:111],a[196:199]\n"
"s_add_u32 s73,s73,12288\n"
"ds_read_b128 v[16:19],v0 offset:24576\n"
"ds_read_b128 v[20:23],v1 offset:24576\n"
MF "a[216:219],v[24:27],v[108:111],a[216:219]\n"
"s_cmp_eq_u32 s73,s19\n"
"s_cselect_b32 s73,s18,s73\n"
"s_add_u32 m0,s65,73728\n"
MF "a[236:239],v[28:31],v[108:111],a[236:239]\n"
LD "v8,s[36:39],s73 offen nt lds\n"
MF "a[232:235],v[28:31],v[104:107],a[232:235]\n"
"s_add_u32 m0,s65,74624\n"
"v_add_f32 v54,v54,v123\n"
CV "v83,v122,v123\n"
MF "a[212:215],v[24:27],v[104:107],a[212:215]\n"
LD "v8,s[36:39],s73 offen offset:128 nt lds\n"
MF "a[208:211],v[24:27],v[100:103],a[208:211]\n"
"s_add_u32 m0,s65,75520\n"
MF "a[228:231],v[28:31],v[100:103],a[228:231]\n"
LD "v8,s[36:39],s73 offen offset:256 nt lds\n"
MF "a[224:227],v[28:31],v[96:99],a[224:227]\n"
MF "a[204:207],v[24:27],v[96:99],a[204:207]\n"
MF "a[200:203],v[24:27],v[92:95],a[200:203]\n"
MF "a[220:223],v[28:31],v[92:95],a[220:223]\n"
"s_waitcnt lgkmcnt(0)\n"
MF "v[92:95],v[16:19],v[136:139],v[32:35]\n"
"s_add_u32 m0,s65,73728\n"
"ds_read_b128 v[24:27],v0 offset:25600\n"
"ds_read_b128 v[28:31],v1 offset:25600\n"
MF "v[92:95],v[20:23],v[156:159],v[92:95]\n"
LD "v8,s[36:39],s73 offen offset:3072 nt lds\n"
MF "v[96:99],v[20:23],v[160:163],v[36:39]\n"
"v_exp_f32 v124,v124\n"
MF "v[96:99],v[16:19],v[140:143],v[96:99]\n"
"v_exp_f32 v125,v125\n"
MF "v[100:103],v[16:19],v[144:147],v[40:43]\n"
"s_add_u32 m0,s65,74624\n"
"v_exp_f32 v126,v126\n"
MF "v[100:103],v[20:23],v[164:167],v[100:103]\n"
LD "v8,s[36:39],s73 offen offset:3200 nt lds\n"
MF "v[104:107],v[20:23],v[168:171],v[44:47]\n"
"v_exp_f32 v127,v127\n"
MF "v[104:107],v[16:19],v[148:151],v[104:107]\n"
"s_add_u32 m0,s65,75520\n"
"v_add_f32 v55,v55,v124\n"
"v_add_f32 v55,v55,v125\n"
MF "v[108:111],v[16:19],v[152:155],v[48:51]\n"
LD "v8,s[36:39],s73 offen offset:3328 nt lds\n"
MF "v[108:111],v[20:23],v[172:175],v[108:111]\n"
"s_waitcnt lgkmcnt(0)\n"
"ds_read_b128 v[16:19],v0 offset:26624\n"
"ds_read_b128 v[20:23],v1 offset:26624\n"
MF "v[108:111],v[24:27],v[192:195],v[108:111]\n"
CV "v86,v124,v125\n"
"v_add_f32 v55,v55,v126\n"
MF "v[108:111],v[28:31],v[212:215],v[108:111]\n"
"v_add_f32 v55,v55,v127\n"
CV "v87,v126,v127\n"
MF "v[104:107],v[28:31],v[208:211],v[104:107]\n"
"v_exp_f32 v128,v128\n"
MF "v[104:107],v[24:27],v[188:191],v[104:107]\n"
"v_exp_f32 v129,v129\n"
MF "v[100:103],v[24:27],v[184:187],v[100:103]\n"
"v_exp_f32 v130,v130\n"
MF "v[100:103],v[28:31],v[204:207],v[100:103]\n"
"v_exp_f32 v131,v131\n"
MF "v[96:99],v[28:31],v[200:203],v[96:99]\n"
"v_add_f32 v56,v56,v128\n"
"v_add_f32 v56,v56,v129\n"
MF "v[96:99],v[24:27],v[180:183],v[96:99]\n"
CV "v90,v128,v129\n"
"v_add_f32 v56,v56,v130\n"
MF "v[92:95],v[24:27],v[176:179],v[92:95]\n"
"v_add_f32 v56,v56,v131\n"
CV "v91,v130,v131\n"
MF "v[92:95],v[28:31],v[196:199],v[92:95]\n"
"s_waitcnt lgkmcnt(0)\n"
"ds_read_b128 v[24:27],v0 offset:30720\n"
"ds_read_b128 v[28:31],v1 offset:30720\n"
MF "v[92:95],v[16:19],v[216:219],v[92:95]\n"
MF "v[92:95],v[20:23],v[236:239],v[92:95]\n"
MF "v[96:99],v[20:23],v[240:243],v[96:99]\n"
MF "v[96:99],v[16:19],v[220:223],v[96:99]\n"
MF "v[100:103],v[16:19],v[224:227],v[100:103]\n"
MF "v[100:103],v[20:23],v[244:247],v[100:103]\n"
MF "v[104:107],v[20:23],v[248:251],v[104:107]\n"
MF "v[104:107],v[16:19],v[228:231],v[104:107]\n"
"v_max3_f32 v10,v52,v53,v54\n"
"v_max3_f32 v10,v10,v55,v56\n"
MF "v[108:111],v[16:19],v[232:235],v[108:111]\n"
"v_cmp_ngt_f32 vcc,s95,v10\n"
MF "v[108:111],v[20:23],v[252:255],v[108:111]\n"
"s_waitcnt lgkmcnt(0)\n"
"s_cbranch_vccnz .Lstub_7\n"
".Lback_7:\n"
MF "v[112:115],v[24:27],v[136:139],v[32:35]\n"
"s_add_u32 s74,s74,12288\n"
"ds_read_b128 v[16:19],v0 offset:31744\n"
"ds_read_b128 v[20:23],v1 offset:31744\n"
MF "v[112:115],v[28:31],v[156:159],v[112:115]\n"
"s_cmp_eq_u32 s74,s20\n"
"s_cselect_b32 s74,s70,s74\n"
MF "v[116:119],v[28:31],v[160:163],v[36:39]\n"
"v_exp_f32 v92,v92\n"
MF "v[116:119],v[24:27],v[140:143],v[116:119]\n"
"v_exp_f32 v93,v93\n"
MF "v[120:123],v[24:27],v[144:147],v[40:43]\n"
"v_exp_f32 v94,v94\n"
MF "v[120:123],v[28:31],v[164:167],v[120:123]\n"
"v_exp_f32 v95,v95\n"
MF "v[124:127],v[28:31],v[168:171],v[44:47]\n"
"v_add_f32 v57,v52,v92\n"
"v_add_f32 v57,v57,v93\n"
MF "v[124:127],v[24:27],v[148:151],v[124:127]\n"
CV "v92,v92,v93\n"
"v_add_f32 v57,v57,v94\n"
MF "v[128:131],v[24:27],v[152:155],v[48:51]\n"
"v_add_f32 v57,v57,v95\n"
CV "v93,v94,v95\n"
MF "v[128:131],v[28:31],v[172:175],v[128:131]\n"
"s_waitcnt lgkmcnt(0)\n"
"ds_read_b128 v[24:27],v0 offset:32768\n"
"ds_read_b128 v[28:31],v1 offset:32768\n"
MF "v[128:131],v[16:19],v[192:195],v[128:131]\n"
"s_sub_u32 s61,s61,1\n"
"v_exp_f32 v96,v96\n"
MF "v[128:131],v[20:23],v[212:215],v[128:131]\n"
"s_cmp_eq_u32 s61,1\n"
"s_cselect_b32 s95,0,s95\n"
"v_add_f32 v58,v53,v96\n"
MF "v[124:127],v[20:23],v[208:211],v[124:127]\n"
"v_exp_f32 v97,v97\n"
MF "v[124:127],v[16:19],v[188:191],v[124:127]\n"
"v_exp_f32 v98,v98\n"
MF "v[120:123],v[16:19],v[184:187],v[120:123]\n"
"v_exp_f32 v99,v99\n"
MF "v[120:123],v[20:23],v[204:207],v[120:123]\n"
"v_add_f32 v58,v58,v97\n"
CV "v96,v96,v97\n"
MF "v[116:119],v[20:23],v[200:203],v[116:119]\n"
"s_waitcnt vmcnt(12) lgkmcnt(0)\n"
"v_add_f32 v58,v58,v98\n"
"v_add_f32 v58,v58,v99\n"
MF "v[116:119],v[16:19],v[180:183],v[116:119]\n"
"s_barrier\n"
MF "v[112:115],v[16:19],v[176:179],v[112:115]\n"
CV "v97,v98,v99\n"
MF "v[112:115],v[20:23],v[196:199],v[112:115]\n"
"s_nop 0\n"
TR "v[16:17],v2\n"
TR "v[18:19],v2 offset:6144\n"
MF "v[112:115],v[24:27],v[216:219],v[112:115]\n"
"s_add_u32 s72,s72,6144\n"
TR "v[20:21],v3\n"
TR "v[22:23],v3 offset:6144\n"
MF "v[112:115],v[28:31],v[236:239],v[112:115]\n"
"s_cmp_eq_u32 s72,s17\n"
"s_cselect_b32 s72,s16,s72\n"
"s_add_u32 m0,s64,24576\n"
MF "v[116:119],v[28:31],v[240:243],v[116:119]\n"
LD "v7,s[32:35],s72 offen nt lds\n"
MF "v[116:119],v[24:27],v[220:223],v[116:119]\n"
"s_add_u32 m0,s64,25472\n"
"v_exp_f32 v100,v100\n"
MF "v[120:123],v[24:27],v[224:227],v[120:123]\n"
LD "v7,s[32:35],s72 offen offset:128 nt lds\n"
MF "v[120:123],v[28:31],v[244:247],v[120:123]\n"
"v_exp_f32 v101,v101\n"
MF "v[124:127],v[28:31],v[248:251],v[124:127]\n"
"s_add_u32 m0,s64,26368\n"
"v_exp_f32 v102,v102\n"
MF "v[124:127],v[24:27],v[228:231],v[124:127]\n"
LD "v7,s[32:35],s72 offen offset:256 nt lds\n"
MF "v[128:131],v[24:27],v[232:235],v[128:131]\n"
"s_add_u32 s72,s72,6144\n"
"v_exp_f32 v103,v103\n"
MF "v[128:131],v[28:31],v[252:255],v[128:131]\n"
"s_waitcnt lgkmcnt(0)\n"
TR "v[24:25],v4\n"
TR "v[26:27],v4 offset:6144\n"
MF "a[0:3],v[16:19],v[72:75],a[0:3]\n"
"s_add_u32 m0,s64,30720\n"
TR "v[28:29],v5\n"
TR "v[30:31],v5 offset:6144\n"
MF "a[20:23],v[20:23],v[72:75],a[20:23]\n"
LD "v7,s[32:35],s72 offen nt lds\n"
MF "a[24:27],v[20:23],v[76:79],a[24:27]\n"
"v_add_f32 v59,v54,v100\n"
"v_add_f32 v59,v59,v101\n"
MF "a[4:7],v[16:19],v[76:79],a[4:7]\n"
"s_add_u32 m0,s64,31616\n"
CV "v100,v100,v101\n"
"v_add_f32 v59,v59,v102\n"
MF "a[8:11],v[16:19],v[80:83],a[8:11]\n"
LD "v7,s[32:35],s72 offen offset:128 nt lds\n"
MF "a[28:31],v[20:23],v[80:83],a[28:31]\n"
"v_add_f32 v59,v59,v103\n"
CV "v101,v102,v103\n"
MF "a[32:35],v[20:23],v[84:87],a[32:35]\n"
"s_add_u32 m0,s64,32512\n"
"v_exp_f32 v104,v104\n"
MF "a[12:15],v[16:19],v[84:87],a[12:15]\n"
LD "v7,s[32:35],s72 offen offset:256 nt lds\n"
MF "a[16:19],v[16:19],v[88:91],a[16:19]\n"
"v_exp_f32 v105,v105\n"
MF "a[36:39],v[20:23],v[88:91],a[36:39]\n"
"s_waitcnt lgkmcnt(0)\n"
TR "v[16:17],v2 offset:1024\n"
TR "v[18:19],v2 offset:7168\n"
MF "a[56:59],v[24:27],v[88:91],a[56:59]\n"
"s_nop 0\n"
TR "v[20:21],v3 offset:1024\n"
TR "v[22:23],v3 offset:7168\n"
MF "a[76:79],v[28:31],v[88:91],a[76:79]\n"
"v_exp_f32 v106,v106\n"
MF "a[72:75],v[28:31],v[84:87],a[72:75]\n"
"v_exp_f32 v107,v107\n"
MF "a[52:55],v[24:27],v[84:87],a[52:55]\n"
"v_add_f32 v60,v55,v104\n"
"v_add_f32 v60,v60,v105\n"
MF "a[48:51],v[24:27],v[80:83],a[48:51]\n"
CV "v104,v104,v105\n"
"v_add_f32 v60,v60,v106\n"
MF "a[68:71],v[28:31],v[80:83],a[68:71]\n"
".Lpg11b:\n"
"s_cbranch_execz .Lpgqj_a\n"
".Lpg11d:\n"
"s_cbranch_execz .Lisr2\n"
"v_add_f32 v60,v60,v107\n"
CV "v105,v106,v107\n"
MF "a[64:67],v[28:31],v[76:79],a[64:67]\n"
"v_exp_f32 v108,v108\n"
MF "a[44:47],v[24:27],v[76:79],a[44:47]\n"
"v_exp_f32 v109,v109\n"
MF "a[40:43],v[24:27],v[72:75],a[40:43]\n"
"v_exp_f32 v110,v110\n"
MF "a[60:63],v[28:31],v[72:75],a[60:63]\n"
"s_waitcnt lgkmcnt(0)\n"
TR "v[24:25],v4 offset:1024\n"
TR "v[26:27],v4 offset:7168\n"
MF "a[80:83],v[16:19],v[72:75],a[80:83]\n"
"s_nop 0\n"
TR "v[28:29],v5 offset:1024\n"
TR "v[30:31],v5 offset:7168\n"
MF "a[100:103],v[20:23],v[72:75],a[100:103]\n"
"v_exp_f32 v111,v111\n"
MF "a[104:107],v[20:23],v[76:79],a[104:107]\n"
"v_add_f32 v61,v56,v108\n"
"v_add_f32 v61,v61,v109\n"
MF "a[84:87],v[16:19],v[76:79],a[84:87]\n"
CV "v108,v108,v109\n"
"v_add_f32 v61,v61,v110\n"
MF "a[88:91],v[16:19],v[80:83],a[88:91]\n"
"v_add_f32 v61,v61,v111\n"
CV "v109,v110,v111\n"
MF "a[108:111],v[20:23],v[80:83],a[108:111]\n"
"v_exp_f32 v112,v112\n"
MF "a[112:115],v[20:23],v[84:87],a[112:115]\n"
"v_exp_f32 v113,v113\n"
MF "a[92:95],v[16:19],v[84:87],a[92:95]\n"
"v_exp_f32 v114,v114\n"
MF "a[96:99],v[16:19],v[88:91],a[96:99]\n"
"v_exp_f32 v115,v115\n"
MF "a[116:119],v[20:23],v[88:91],a[116:119]\n"
"s_waitcnt lgkmcnt(0)\n"
TR "v[16:17],v2 offset:2048\n"
TR "v[18:19],v2 offset:8192\n"
MF "a[136:139],v[24:27],v[88:91],a[136:139]\n"
"s_nop 0\n"
TR "v[20:21],v3 offset:2048\n"
TR "v[22:23],v3 offset:8192\n"
MF "a[156:159],v[28:31],v[88:91],a[156:159]\n"
"v_add_f32 v57,v57,v112\n"
"v_add_f32 v57,v57,v113\n"
MF "a[152:155],v[28:31],v[84:87],a[152:155]\n"
CV "v94,v112,v113\n"
"v_add_f32 v57,v57,v114\n"
MF "a[132:135],v[24:27],v[84:87],a[132:135]\n"
"v_add_f32 v57,v57,v115\n"
CV "v95,v114,v115\n"
MF "a[128:131],v[24:27],v[80:83],a[128:131]\n"
"v_exp_f32 v116,v116\n"
MF "a[148:151],v[28:31],v[80:83],a[148:151]\n"
"v_exp_f32 v117,v117\n"
MF "a[144:147],v[28:31],v[76:79],a[144:147]\n"
"v_exp_f32 v118,v118\n"
MF "a[124:127],v[24:27],v[76:79],a[124:127]\n"
"v_exp_f32 v119,v119\n"
MF "a[120:123],v[24:27],v[72:75],a[120:123]\n"
"v_add_f32 v58,v58,v116\n"
"v_add_f32 v58,v58,v117\n"
MF "a[140:143],v[28:31],v[72:75],a[140:143]\n"
"s_waitcnt lgkmcnt(0)\n"
TR "v[24:25],v4 offset:2048\n"
TR "v[26:27],v4 offset:8192\n"
MF "a[160:163],v[16:19],v[72:75],a[160:163]\n"
"s_nop 0\n"
TR "v[28:29],v5 offset:2048\n"
TR "v[30:31],v5 offset:8192\n"
MF "a[180:183],v[20:23],v[72:75],a[180:183]\n"
CV "v98,v116,v117\n"
"v_add_f32 v58,v58,v118\n"
MF "a[184:187],v[20:23],v[76:79],a[184:187]\n"
"v_add_f32 v58,v58,v119\n"
CV "v99,v118,v119\n"
MF "a[164:167],v[16:19],v[76:79],a[164:167]\n"
"v_exp_f32 v120,v120\n"
MF "a[168:171],v[16:19],v[80:83],a[168:171]\n"
"v_exp_f32 v121,v121\n"
MF "a[188:191],v[20:23],v[80:83],a[188:191]\n"
"v_exp_f32 v122,v122\n"
MF "a[192:195],v[20:23],v[84:87],a[192:195]\n"
"v_exp_f32 v123,v123\n"
MF "a[172:175],v[16:19],v[84:87],a[172:175]\n"
"s_waitcnt vmcnt(12) lgkmcnt(0)\n"
"v_add_f32 v59,v59,v120\n"
"v_add_f32 v59,v59,v121\n"
MF "a[176:179],v[16:19],v[88:91],a[176:179]\n"
"s_barrier\n"
MF "a[196:199],v[20:23],v[88:91],a[196:199]\n"
"s_add_u32 s73,s73,12288\n"
"ds_read_b128 v[16:19],v0\n"
"ds_read_b128 v[20:23],v1\n"
MF "a[216:219],v[24:27],v[88:91],a[216:219]\n"
"s_cmp_eq_u32 s73,s19\n"
"s_cselect_b32 s73,s18,s73\n"
"s_add_u32 m0,s65,49152\n"
MF "a[236:239],v[28:31],v[88:91],a[236:239]\n"
LD "v8,s[36:39],s73 offen nt lds\n"
MF "a[232:235],v[28:31],v[84:87],a[232:235]\n"
"s_add_u32 m0,s65,50048\n"
CV "v102,v120,v121\n"
"v_add_f32 v59,v59,v122\n"
MF "a[212:215],v[24:27],v[84:87],a[212:215]\n"
LD "v8,s[36:39],s73 offen offset:128 nt lds\n"
MF "a[208:211],v[24:27],v[80:83],a[208:211]\n"
"s_add_u32 m0,s65,50944\n"
"v_add_f32 v59,v59,v123\n"
CV "v103,v122,v123\n"
MF "a[228:231],v[28:31],v[80:83],a[228:231]\n"
LD "v8,s[36:39],s73 offen offset:256 nt lds\n"
MF "a[224:227],v[28:31],v[76:79],a[224:227]\n"
MF "a[204:207],v[24:27],v[76:79],a[204:207]\n"
"s_waitcnt lgkmcnt(0)\n"
MF "a[200:203],v[24:27],v[72:75],a[200:203]\n"
"s_cmp_lg_u32 s61,0\n"
MF "a[220:223],v[28:31],v[72:75],a[220:223]\n"
"s_cbranch_scc1 .Laloopx\n"
".Lepi1:\n"
"v_exp_f32 v124,v124\n"
"v_exp_f32 v125,v125\n"
"v_exp_f32 v126,v126\n"
"v_exp_f32 v127,v127\n"
"v_exp_f32 v128,v128\n"
"v_exp_f32 v129,v129\n"
"v_exp_f32 v130,v130\n"
"v_exp_f32 v131,v131\n"
"v_add_f32 v60,v60,v124\n"
"v_add_f32 v61,v61,v128\n"
"v_add_f32 v60,v60,v125\n"
"v_add_f32 v61,v61,v129\n"
"v_add_f32 v60,v60,v126\n"
"v_add_f32 v61,v61,v130\n"
"v_add_f32 v60,v60,v127\n"
"v_add_f32 v61,v61,v131\n"
CV "v106,v124,v125\n"
CV "v107,v126,v127\n"
CV "v110,v128,v129\n"
CV "v111,v130,v131\n"
"v_max3_f32 v10,v57,v58,v59\n"
"v_max3_f32 v10,v10,v60,v61\n"
"v_cmp_le_f32 vcc,0x47800000,v10\n"
"s_cbranch_vccnz .Lstub_8\n"
".Lback_8:\n"
"s_waitcnt vmcnt(9) lgkmcnt(0)\n"
"s_barrier\n"
"s_cmp_eq_u32 s57,0\n"
"s_cbranch_scc0 .Lep\n"
".Lepx:\n"
"s_add_u32 s66,s67,4718592\n"
"s_sub_u32 s81,s81,384\n"
"s_mov_b32 s83,7077888\n"
AR "v6,a243\n"
"v_add_u32 v11,97280,v6\n"
"v_add_u32 v12,194560,v6\n"
"v_add_u32 v13,291840,v6\n"
"v_mov_b32 v10,1.0\n"
TR "v[136:137],v2 offset:24576\n"
TR "v[138:139],v2 offset:30720\n"
"v_mfma_f32_16x16x4_f32 v[16:19],v10,v57,0\n"
TR "v[140:141],v3 offset:24576\n"
TR "v[142:143],v3 offset:30720\n"
TR "v[144:145],v4 offset:24576\n"
TR "v[146:147],v4 offset:30720\n"
TR "v[148:149],v5 offset:24576\n"
TR "v[150:151],v5 offset:30720\n"
"v_mfma_f32_16x16x4_f32 v[20:23],v10,v58,0\n"
TR "v[152:153],v2 offset:25600\n"
TR "v[154:155],v2 offset:31744\n"
TR "v[156:157],v3 offset:25600\n"
TR "v[158:159],v3 offset:31744\n"
TR "v[160:161],v4 offset:25600\n"
TR "v[162:163],v4 offset:31744\n"
"v_mfma_f32_16x16x4_f32 v[24:27],v10,v59,0\n"
TR "v[164:165],v5 offset:25600\n"
TR "v[166:167],v5 offset:31744\n"
TR "v[168:169],v2 offset:26624\n"
TR "v[170:171],v2 offset:32768\n"
TR "v[172:173],v3 offset:26624\n"
TR "v[174:175],v3 offset:32768\n"
"v_mfma_f32_16x16x4_f32 v[28:31],v10,v60,0\n"
TR "v[176:177],v4 offset:26624\n"
TR "v[178:179],v4 offset:32768\n"
TR "v[180:181],v5 offset:26624\n"
TR "v[182:183],v5 offset:32768\n"
"v_mfma_f32_16x16x4_f32 v[32:35],v10,v61,0\n"
"s_add_u32 m0,s62,0\n"
"s_add_u32 s82,s66,2359296\n"
"s_sub_u32 s15,s82,7864320\n"
"s_min_u32 s82,s82,s15\n"
LD "v6,s[28:31],s82 offen lds\n"
LD "v11,s[28:31],s82 offen offset:1024 lds\n"
"s_waitcnt lgkmcnt(15)\n"
MF "a[0:3],v[136:139],v[92:95],a[0:3]\n"
MF "a[20:23],v[140:143],v[92:95],a[20:23]\n"
LD "v12,s[28:31],s82 offen offset:2048 lds\n"
LD "v13,s[28:31],s82 offen offset:3072 lds\n"
"v_rcp_f32 v57,v16\n"
"v_rcp_f32 v58,v20\n"
"v_rcp_f32 v59,v24\n"
"v_rcp_f32 v60,v28\n"
"v_rcp_f32 v61,v32\n"
MF "a[4:7],v[136:139],v[96:99],a[4:7]\n"
"v_mul_f32_e64 v52,-s13,v57\n"
"v_mul_f32_e64 v53,-s13,v58\n"
"v_mul_f32_e64 v54,-s13,v59\n"
"v_mul_f32_e64 v55,-s13,v60\n"
"v_mul_f32_e64 v56,-s13,v61\n"
AR "v72,a0\n"
AR "v73,a1\n"
AR "v74,a2\n"
MF "a[24:27],v[140:143],v[96:99],a[24:27]\n"
AR "v75,a3\n"
AR "v76,a20\n"
AR "v77,a21\n"
AR "v78,a22\n"
AR "v79,a23\n"
"v_pk_fma_f32 v[72:73],v[72:73],v[56:57],v[52:53] op_sel:[0,1,0] op_sel_hi:[1,1,0]\n"
"v_pk_fma_f32 v[74:75],v[74:75],v[56:57],v[52:53] op_sel:[0,1,0] op_sel_hi:[1,1,0]\n"
"v_pk_fma_f32 v[76:77],v[76:77],v[56:57],v[52:53] op_sel:[0,1,0] op_sel_hi:[1,1,0]\n"
"v_pk_fma_f32 v[78:79],v[78:79],v[56:57],v[52:53] op_sel:[0,1,0] op_sel_hi:[1,1,0]\n"
CV "v112,v72,v73\n"
CV "v113,v74,v75\n"
CV "v114,v76,v77\n"
CV "v115,v78,v79\n"
MF "a[8:11],v[136:139],v[100:103],a[8:11]\n"
AW "a0,v112\n"
AW "a1,v113\n"
AW "a2,v114\n"
AW "a3,v115\n"
"s_add_u32 m0,s62,16384\n"
"s_add_u32 s82,s66,2359424\n"
"s_sub_u32 s15,s82,7864320\n"
"s_min_u32 s82,s82,s15\n"
"s_nop 0\n"
LD "v6,s[28:31],s82 offen lds\n"
AR "v80,a4\n"
AR "v81,a5\n"
AR "v82,a6\n"
MF "a[28:31],v[140:143],v[100:103],a[28:31]\n"
AR "v83,a7\n"
AR "v84,a24\n"
AR "v85,a25\n"
AR "v86,a26\n"
AR "v87,a27\n"
"v_pk_fma_f32 v[80:81],v[80:81],v[58:59],v[52:53] op_sel:[0,0,1] op_sel_hi:[1,0,1]\n"
"v_pk_fma_f32 v[82:83],v[82:83],v[58:59],v[52:53] op_sel:[0,0,1] op_sel_hi:[1,0,1]\n"
"v_pk_fma_f32 v[84:85],v[84:85],v[58:59],v[52:53] op_sel:[0,0,1] op_sel_hi:[1,0,1]\n"
"v_pk_fma_f32 v[86:87],v[86:87],v[58:59],v[52:53] op_sel:[0,0,1] op_sel_hi:[1,0,1]\n"
CV "v116,v80,v81\n"
CV "v117,v82,v83\n"
CV "v118,v84,v85\n"
CV "v119,v86,v87\n"
MF "a[12:15],v[136:139],v[104:107],a[12:15]\n"
AW "a4,v116\n"
AW "a5,v117\n"
AW "a6,v118\n"
AW "a7,v119\n"
LD "v11,s[28:31],s82 offen offset:1024 lds\n"
AR "v72,a8\n"
AR "v73,a9\n"
AR "v74,a10\n"
MF "a[32:35],v[140:143],v[104:107],a[32:35]\n"
AR "v75,a11\n"
AR "v76,a28\n"
AR "v77,a29\n"
AR "v78,a30\n"
AR "v79,a31\n"
"v_pk_fma_f32 v[72:73],v[72:73],v[58:59],v[54:55] op_sel:[0,1,0] op_sel_hi:[1,1,0]\n"
"v_pk_fma_f32 v[74:75],v[74:75],v[58:59],v[54:55] op_sel:[0,1,0] op_sel_hi:[1,1,0]\n"
"v_pk_fma_f32 v[76:77],v[76:77],v[58:59],v[54:55] op_sel:[0,1,0] op_sel_hi:[1,1,0]\n"
"v_pk_fma_f32 v[78:79],v[78:79],v[58:59],v[54:55] op_sel:[0,1,0] op_sel_hi:[1,1,0]\n"
CV "v112,v72,v73\n"
CV "v113,v74,v75\n"
CV "v114,v76,v77\n"
CV "v115,v78,v79\n"
MF "a[16:19],v[136:139],v[108:111],a[16:19]\n"
AW "a8,v112\n"
AW "a9,v113\n"
AW "a10,v114\n"
AW "a11,v115\n"
"s_waitcnt lgkmcnt(0)\n"
"s_barrier\n"
AR "v80,a12\n"
AR "v81,a13\n"
AR "v82,a14\n"
MF "a[36:39],v[140:143],v[108:111],a[36:39]\n"
AR "v83,a15\n"
AR "v84,a32\n"
AR "v85,a33\n"
AR "v86,a34\n"
AR "v87,a35\n"
"v_pk_fma_f32 v[80:81],v[80:81],v[60:61],v[54:55] op_sel:[0,0,1] op_sel_hi:[1,0,1]\n"
"v_pk_fma_f32 v[82:83],v[82:83],v[60:61],v[54:55] op_sel:[0,0,1] op_sel_hi:[1,0,1]\n"
"v_pk_fma_f32 v[84:85],v[84:85],v[60:61],v[54:55] op_sel:[0,0,1] op_sel_hi:[1,0,1]\n"
"v_pk_fma_f32 v[86:87],v[86:87],v[60:61],v[54:55] op_sel:[0,0,1] op_sel_hi:[1,0,1]\n"
CV "v116,v80,v81\n"
CV "v117,v82,v83\n"
CV "v118,v84,v85\n"
CV "v119,v86,v87\n"
MF "a[40:43],v[144:147],v[92:95],a[40:43]\n"
AW "a12,v116\n"
AW "a13,v117\n"
AW "a14,v118\n"
AW "a15,v119\n"
LD "v12,s[28:31],s82 offen offset:2048 lds\n"
AR "v72,a16\n"
AR "v73,a17\n"
AR "v74,a18\n"
MF "a[60:63],v[148:151],v[92:95],a[60:63]\n"
AR "v75,a19\n"
AR "v76,a36\n"
AR "v77,a37\n"
AR "v78,a38\n"
AR "v79,a39\n"
"v_pk_fma_f32 v[72:73],v[72:73],v[60:61],v[56:57] op_sel:[0,1,0] op_sel_hi:[1,1,0]\n"
"v_pk_fma_f32 v[74:75],v[74:75],v[60:61],v[56:57] op_sel:[0,1,0] op_sel_hi:[1,1,0]\n"
"v_pk_fma_f32 v[76:77],v[76:77],v[60:61],v[56:57] op_sel:[0,1,0] op_sel_hi:[1,1,0]\n"
"v_pk_fma_f32 v[78:79],v[78:79],v[60:61],v[56:57] op_sel:[0,1,0] op_sel_hi:[1,1,0]\n"
CV "v112,v72,v73\n"
CV "v113,v74,v75\n"
CV "v114,v76,v77\n"
CV "v115,v78,v79\n"
MF "a[44:47],v[144:147],v[96:99],a[44:47]\n"
AW "a16,v112\n"
AW "a17,v113\n"
AW "a18,v114\n"
AW "a19,v115\n"
LD "v13,s[28:31],s82 offen offset:3072 lds\n"
AR "v80,a40\n"
AR "v81,a41\n"
AR "v82,a42\n"
MF "a[64:67],v[148:151],v[96:99],a[64:67]\n"
AR "v83,a43\n"
AR "v84,a60\n"
AR "v85,a61\n"
AR "v86,a62\n"
AR "v87,a63\n"
"v_pk_fma_f32 v[80:81],v[80:81],v[56:57],v[52:53] op_sel:[0,1,0] op_sel_hi:[1,1,0]\n"
"v_pk_fma_f32 v[82:83],v[82:83],v[56:57],v[52:53] op_sel:[0,1,0] op_sel_hi:[1,1,0]\n"
"v_pk_fma_f32 v[84:85],v[84:85],v[56:57],v[52:53] op_sel:[0,1,0] op_sel_hi:[1,1,0]\n"
"v_pk_fma_f32 v[86:87],v[86:87],v[56:57],v[52:53] op_sel:[0,1,0] op_sel_hi:[1,1,0]\n"
CV "v116,v80,v81\n"
CV "v117,v82,v83\n"
CV "v118,v84,v85\n"
CV "v119,v86,v87\n"
MF "a[48:51],v[144:147],v[100:103],a[48:51]\n"
AW "a40,v116\n"
AW "a41,v117\n"
AW "a42,v118\n"
AW "a43,v119\n"
AR "v72,a44\n"
AR "v73,a45\n"
AR "v74,a46\n"
MF "a[68:71],v[148:151],v[100:103],a[68:71]\n"
AR "v75,a47\n"
AR "v76,a64\n"
AR "v77,a65\n"
AR "v78,a66\n"
AR "v79,a67\n"
"v_pk_fma_f32 v[72:73],v[72:73],v[58:59],v[52:53] op_sel:[0,0,1] op_sel_hi:[1,0,1]\n"
"v_pk_fma_f32 v[74:75],v[74:75],v[58:59],v[52:53] op_sel:[0,0,1] op_sel_hi:[1,0,1]\n"
"v_pk_fma_f32 v[76:77],v[76:77],v[58:59],v[52:53] op_sel:[0,0,1] op_sel_hi:[1,0,1]\n"
"v_pk_fma_f32 v[78:79],v[78:79],v[58:59],v[52:53] op_sel:[0,0,1] op_sel_hi:[1,0,1]\n"
CV "v112,v72,v73\n"
CV "v113,v74,v75\n"
CV "v114,v76,v77\n"
CV "v115,v78,v79\n"
MF "a[52:55],v[144:147],v[104:107],a[52:55]\n"
AW "a44,v112\n"
AW "a45,v113\n"
AW "a46,v114\n"
AW "a47,v115\n"
"s_add_u32 m0,s62,32768\n"
"s_add_u32 s82,s66,2359552\n"
"s_sub_u32 s15,s82,7864320\n"
"s_min_u32 s82,s82,s15\n"
"s_nop 0\n"
LD "v6,s[28:31],s82 offen lds\n"
AR "v80,a48\n"
AR "v81,a49\n"
AR "v82,a50\n"
MF "a[72:75],v[148:151],v[104:107],a[72:75]\n"
AR "v83,a51\n"
AR "v84,a68\n"
AR "v85,a69\n"
AR "v86,a70\n"
AR "v87,a71\n"
"v_pk_fma_f32 v[80:81],v[80:81],v[58:59],v[54:55] op_sel:[0,1,0] op_sel_hi:[1,1,0]\n"
"v_pk_fma_f32 v[82:83],v[82:83],v[58:59],v[54:55] op_sel:[0,1,0] op_sel_hi:[1,1,0]\n"
"v_pk_fma_f32 v[84:85],v[84:85],v[58:59],v[54:55] op_sel:[0,1,0] op_sel_hi:[1,1,0]\n"
"v_pk_fma_f32 v[86:87],v[86:87],v[58:59],v[54:55] op_sel:[0,1,0] op_sel_hi:[1,1,0]\n"
CV "v116,v80,v81\n"
CV "v117,v82,v83\n"
CV "v118,v84,v85\n"
CV "v119,v86,v87\n"
MF "a[56:59],v[144:147],v[108:111],a[56:59]\n"
AW "a48,v116\n"
AW "a49,v117\n"
AW "a50,v118\n"
AW "a51,v119\n"
LD "v11,s[28:31],s82 offen offset:1024 lds\n"
AR "v72,a52\n"
AR "v73,a53\n"
AR "v74,a54\n"
MF "a[76:79],v[148:151],v[108:111],a[76:79]\n"
AR "v75,a55\n"
AR "v76,a72\n"
AR "v77,a73\n"
AR "v78,a74\n"
AR "v79,a75\n"
"v_pk_fma_f32 v[72:73],v[72:73],v[60:61],v[54:55] op_sel:[0,0,1] op_sel_hi:[1,0,1]\n"
"v_pk_fma_f32 v[74:75],v[74:75],v[60:61],v[54:55] op_sel:[0,0,1] op_sel_hi:[1,0,1]\n"
"v_pk_fma_f32 v[76:77],v[76:77],v[60:61],v[54:55] op_sel:[0,0,1] op_sel_hi:[1,0,1]\n"
"v_pk_fma_f32 v[78:79],v[78:79],v[60:61],v[54:55] op_sel:[0,0,1] op_sel_hi:[1,0,1]\n"
CV "v112,v72,v73\n"
CV "v113,v74,v75\n"
CV "v114,v76,v77\n"
CV "v115,v78,v79\n"
MF "a[80:83],v[152:155],v[92:95],a[80:83]\n"
AW "a52,v112\n"
AW "a53,v113\n"
AW "a54,v114\n"
AW "a55,v115\n"
AR "v80,a56\n"
AR "v81,a57\n"
AR "v82,a58\n"
MF "a[100:103],v[156:159],v[92:95],a[100:103]\n"
AR "v83,a59\n"
AR "v84,a76\n"
AR "v85,a77\n"
AR "v86,a78\n"
AR "v87,a79\n"
"v_pk_fma_f32 v[80:81],v[80:81],v[60:61],v[56:57] op_sel:[0,1,0] op_sel_hi:[1,1,0]\n"
"v_pk_fma_f32 v[82:83],v[82:83],v[60:61],v[56:57] op_sel:[0,1,0] op_sel_hi:[1,1,0]\n"
"v_pk_fma_f32 v[84:85],v[84:85],v[60:61],v[56:57] op_sel:[0,1,0] op_sel_hi:[1,1,0]\n"
"v_pk_fma_f32 v[86:87],v[86:87],v[60:61],v[56:57] op_sel:[0,1,0] op_sel_hi:[1,1,0]\n"
CV "v116,v80,v81\n"
CV "v117,v82,v83\n"
CV "v118,v84,v85\n"
CV "v119,v86,v87\n"
MF "a[84:87],v[152:155],v[96:99],a[84:87]\n"
AW "a56,v116\n"
AW "a57,v117\n"
AW "a58,v118\n"
AW "a59,v119\n"
LD "v12,s[28:31],s82 offen offset:2048 lds\n"
AR "v72,a80\n"
AR "v73,a81\n"
AR "v74,a82\n"
MF "a[104:107],v[156:159],v[96:99],a[104:107]\n"
AR "v75,a83\n"
AR "v76,a100\n"
AR "v77,a101\n"
AR "v78,a102\n"
AR "v79,a103\n"
"v_pk_fma_f32 v[72:73],v[72:73],v[56:57],v[52:53] op_sel:[0,1,0] op_sel_hi:[1,1,0]\n"
"v_pk_fma_f32 v[74:75],v[74:75],v[56:57],v[52:53] op_sel:[0,1,0] op_sel_hi:[1,1,0]\n"
"v_pk_fma_f32 v[76:77],v[76:77],v[56:57],v[52:53] op_sel:[0,1,0] op_sel_hi:[1,1,0]\n"
"v_pk_fma_f32 v[78:79],v[78:79],v[56:57],v[52:53] op_sel:[0,1,0] op_sel_hi:[1,1,0]\n"
CV "v112,v72,v73\n"
CV "v113,v74,v75\n"
CV "v114,v76,v77\n"
CV "v115,v78,v79\n"
MF "a[88:91],v[152:155],v[100:103],a[88:91]\n"
AW "a80,v112\n"
AW "a81,v113\n"
AW "a82,v114\n"
AW "a83,v115\n"
LD "v13,s[28:31],s82 offen offset:3072 lds\n"
AR "v80,a84\n"
AR "v81,a85\n"
AR "v82,a86\n"
MF "a[108:111],v[156:159],v[100:103],a[108:111]\n"
AR "v83,a87\n"
AR "v84,a104\n"
AR "v85,a105\n"
AR "v86,a106\n"
AR "v87,a107\n"
"v_pk_fma_f32 v[80:81],v[80:81],v[58:59],v[52:53] op_sel:[0,0,1] op_sel_hi:[1,0,1]\n"
"v_pk_fma_f32 v[82:83],v[82:83],v[58:59],v[52:53] op_sel:[0,0,1] op_sel_hi:[1,0,1]\n"
"v_pk_fma_f32 v[84:85],v[84:85],v[58:59],v[52:53] op_sel:[0,0,1] op_sel_hi:[1,0,1]\n"
"v_pk_fma_f32 v[86:87],v[86:87],v[58:59],v[52:53] op_sel:[0,0,1] op_sel_hi:[1,0,1]\n"
CV "v116,v80,v81\n"
CV "v117,v82,v83\n"
CV "v118,v84,v85\n"
CV "v119,v86,v87\n"
MF "a[92:95],v[152:155],v[104:107],a[92:95]\n"
AW "a84,v116\n"
AW "a85,v117\n"
AW "a86,v118\n"
AW "a87,v119\n"
AR "v72,a88\n"
AR "v73,a89\n"
AR "v74,a90\n"
MF "a[112:115],v[156:159],v[104:107],a[112:115]\n"
AR "v75,a91\n"
AR "v76,a108\n"
AR "v77,a109\n"
AR "v78,a110\n"
AR "v79,a111\n"
"v_pk_fma_f32 v[72:73],v[72:73],v[58:59],v[54:55] op_sel:[0,1,0] op_sel_hi:[1,1,0]\n"
"v_pk_fma_f32 v[74:75],v[74:75],v[58:59],v[54:55] op_sel:[0,1,0] op_sel_hi:[1,1,0]\n"
"v_pk_fma_f32 v[76:77],v[76:77],v[58:59],v[54:55] op_sel:[0,1,0] op_sel_hi:[1,1,0]\n"
"v_pk_fma_f32 v[78:79],v[78:79],v[58:59],v[54:55] op_sel:[0,1,0] op_sel_hi:[1,1,0]\n"
CV "v112,v72,v73\n"
CV "v113,v74,v75\n"
CV "v114,v76,v77\n"
CV "v115,v78,v79\n"
MF "a[96:99],v[152:155],v[108:111],a[96:99]\n"
AW "a88,v112\n"
AW "a89,v113\n"
AW "a90,v114\n"
AW "a91,v115\n"
AR "v80,a92\n"
AR "v81,a93\n"
AR "v82,a94\n"
MF "a[116:119],v[156:159],v[108:111],a[116:119]\n"
AR "v83,a95\n"
AR "v84,a112\n"
AR "v85,a113\n"
AR "v86,a114\n"
AR "v87,a115\n"
"v_pk_fma_f32 v[80:81],v[80:81],v[60:61],v[54:55] op_sel:[0,0,1] op_sel_hi:[1,0,1]\n"
"v_pk_fma_f32 v[82:83],v[82:83],v[60:61],v[54:55] op_sel:[0,0,1] op_sel_hi:[1,0,1]\n"
"v_pk_fma_f32 v[84:85],v[84:85],v[60:61],v[54:55] op_sel:[0,0,1] op_sel_hi:[1,0,1]\n"
"v_pk_fma_f32 v[86:87],v[86:87],v[60:61],v[54:55] op_sel:[0,0,1] op_sel_hi:[1,0,1]\n"
CV "v116,v80,v81\n"
CV "v117,v82,v83\n"
CV "v118,v84,v85\n"
CV "v119,v86,v87\n"
MF "a[120:123],v[160:163],v[92:95],a[120:123]\n"
AW "a92,v116\n"
AW "a93,v117\n"
AW "a94,v118\n"
AW "a95,v119\n"
AR "v72,a96\n"
AR "v73,a97\n"
AR "v74,a98\n"
MF "a[140:143],v[164:167],v[92:95],a[140:143]\n"
AR "v75,a99\n"
AR "v76,a116\n"
AR "v77,a117\n"
AR "v78,a118\n"
AR "v79,a119\n"
"v_pk_fma_f32 v[72:73],v[72:73],v[60:61],v[56:57] op_sel:[0,1,0] op_sel_hi:[1,1,0]\n"
"v_pk_fma_f32 v[74:75],v[74:75],v[60:61],v[56:57] op_sel:[0,1,0] op_sel_hi:[1,1,0]\n"
"v_pk_fma_f32 v[76:77],v[76:77],v[60:61],v[56:57] op_sel:[0,1,0] op_sel_hi:[1,1,0]\n"
"v_pk_fma_f32 v[78:79],v[78:79],v[60:61],v[56:57] op_sel:[0,1,0] op_sel_hi:[1,1,0]\n"
CV "v112,v72,v73\n"
CV "v113,v74,v75\n"
CV "v114,v76,v77\n"
CV "v115,v78,v79\n"
MF "a[124:127],v[160:163],v[96:99],a[124:127]\n"
".Lpg12f:\n"
"s_cbranch_execz .Lisr3\n"
AW "a96,v112\n"
AW "a97,v113\n"
AW "a98,v114\n"
AW "a99,v115\n"
AR "v0,a240\n"
AR "v2,a241\n"
AR "v4,a242\n"
"v_xor_b32 v1,64,v0\n"
"v_xor_b32 v3,64,v2\n"
"v_xor_b32 v5,64,v4\n"
AR "v80,a120\n"
AR "v81,a121\n"
AR "v82,a122\n"
MF "a[144:147],v[164:167],v[96:99],a[144:147]\n"
AR "v83,a123\n"
AR "v84,a140\n"
AR "v85,a141\n"
AR "v86,a142\n"
AR "v87,a143\n"
"v_pk_fma_f32 v[80:81],v[80:81],v[56:57],v[52:53] op_sel:[0,1,0] op_sel_hi:[1,1,0]\n"
"v_pk_fma_f32 v[82:83],v[82:83],v[56:57],v[52:53] op_sel:[0,1,0] op_sel_hi:[1,1,0]\n"
"v_pk_fma_f32 v[84:85],v[84:85],v[56:57],v[52:53] op_sel:[0,1,0] op_sel_hi:[1,1,0]\n"
"v_pk_fma_f32 v[86:87],v[86:87],v[56:57],v[52:53] op_sel:[0,1,0] op_sel_hi:[1,1,0]\n"
CV "v116,v80,v81\n"
CV "v117,v82,v83\n"
CV "v118,v84,v85\n"
CV "v119,v86,v87\n"
MF "a[128:131],v[160:163],v[100:103],a[128:131]\n"
AW "a120,v116\n"
AW "a121,v117\n"
AW "a122,v118\n"
AW "a123,v119\n"
AR "v72,a124\n"
AR "v73,a125\n"
AR "v74,a126\n"
MF "a[148:151],v[164:167],v[100:103],a[148:151]\n"
AR "v75,a127\n"
AR "v76,a144\n"
AR "v77,a145\n"
AR "v78,a146\n"
AR "v79,a147\n"
"v_pk_fma_f32 v[72:73],v[72:73],v[58:59],v[52:53] op_sel:[0,0,1] op_sel_hi:[1,0,1]\n"
"v_pk_fma_f32 v[74:75],v[74:75],v[58:59],v[52:53] op_sel:[0,0,1] op_sel_hi:[1,0,1]\n"
"v_pk_fma_f32 v[76:77],v[76:77],v[58:59],v[52:53] op_sel:[0,0,1] op_sel_hi:[1,0,1]\n"
"v_pk_fma_f32 v[78:79],v[78:79],v[58:59],v[52:53] op_sel:[0,0,1] op_sel_hi:[1,0,1]\n"
CV "v112,v72,v73\n"
CV "v113,v74,v75\n"
CV "v114,v76,v77\n"
CV "v115,v78,v79\n"
MF "a[132:135],v[160:163],v[104:107],a[132:135]\n"
"ds_read_b128 a[60:63],v2\n"
AW "a124,v112\n"
AW "a125,v113\n"
AW "a126,v114\n"
AW "a127,v115\n"
AR "v80,a128\n"
AR "v81,a129\n"
AR "v82,a130\n"
MF "a[152:155],v[164:167],v[104:107],a[152:155]\n"
AR "v83,a131\n"
AR "v84,a148\n"
AR "v85,a149\n"
AR "v86,a150\n"
AR "v87,a151\n"
"v_pk_fma_f32 v[80:81],v[80:81],v[58:59],v[54:55] op_sel:[0,1,0] op_sel_hi:[1,1,0]\n"
"v_pk_fma_f32 v[82:83],v[82:83],v[58:59],v[54:55] op_sel:[0,1,0] op_sel_hi:[1,1,0]\n"
"v_pk_fma_f32 v[84:85],v[84:85],v[58:59],v[54:55] op_sel:[0,1,0] op_sel_hi:[1,1,0]\n"
"v_pk_fma_f32 v[86:87],v[86:87],v[58:59],v[54:55] op_sel:[0,1,0] op_sel_hi:[1,1,0]\n"
CV "v116,v80,v81\n"
CV "v117,v82,v83\n"
CV "v118,v84,v85\n"
CV "v119,v86,v87\n"
MF "a[136:139],v[160:163],v[108:111],a[136:139]\n"
"ds_read_b128 a[100:103],v3\n"
AW "a128,v116\n"
AW "a129,v117\n"
AW "a130,v118\n"
AW "a131,v119\n"
AR "v72,a132\n"
AR "v73,a133\n"
AR "v74,a134\n"
MF "a[156:159],v[164:167],v[108:111],a[156:159]\n"
AR "v75,a135\n"
AR "v76,a152\n"
AR "v77,a153\n"
AR "v78,a154\n"
AR "v79,a155\n"
"v_pk_fma_f32 v[72:73],v[72:73],v[60:61],v[54:55] op_sel:[0,0,1] op_sel_hi:[1,0,1]\n"
"v_pk_fma_f32 v[74:75],v[74:75],v[60:61],v[54:55] op_sel:[0,0,1] op_sel_hi:[1,0,1]\n"
"v_pk_fma_f32 v[76:77],v[76:77],v[60:61],v[54:55] op_sel:[0,0,1] op_sel_hi:[1,0,1]\n"
"v_pk_fma_f32 v[78:79],v[78:79],v[60:61],v[54:55] op_sel:[0,0,1] op_sel_hi:[1,0,1]\n"
CV "v112,v72,v73\n"
CV "v113,v74,v75\n"
CV "v114,v76,v77\n"
CV "v115,v78,v79\n"
MF "a[160:163],v[168:171],v[92:95],a[160:163]\n"
"ds_read_b128 a[64:67],v2 offset:2048\n"
AW "a132,v112\n"
AW "a133,v113\n"
AW "a134,v114\n"
AW "a135,v115\n"
AR "v80,a136\n"
AR "v81,a137\n"
AR "v82,a138\n"
MF "a[180:183],v[172:175],v[92:95],a[180:183]\n"
AR "v83,a139\n"
AR "v84,a156\n"
AR "v85,a157\n"
AR "v86,a158\n"
AR "v87,a159\n"
"v_pk_fma_f32 v[80:81],v[80:81],v[60:61],v[56:57] op_sel:[0,1,0] op_sel_hi:[1,1,0]\n"
"v_pk_fma_f32 v[82:83],v[82:83],v[60:61],v[56:57] op_sel:[0,1,0] op_sel_hi:[1,1,0]\n"
"v_pk_fma_f32 v[84:85],v[84:85],v[60:61],v[56:57] op_sel:[0,1,0] op_sel_hi:[1,1,0]\n"
"v_pk_fma_f32 v[86:87],v[86:87],v[60:61],v[56:57] op_sel:[0,1,0] op_sel_hi:[1,1,0]\n"
CV "v116,v80,v81\n"
CV "v117,v82,v83\n"
CV "v118,v84,v85\n"
CV "v119,v86,v87\n"
MF "a[164:167],v[168:171],v[96:99],a[164:167]\n"
"ds_read_b128 a[104:107],v3 offset:2048\n"
AW "a136,v116\n"
AW "a137,v117\n"
AW "a138,v118\n"
AW "a139,v119\n"
AR "v72,a160\n"
AR "v73,a161\n"
AR "v74,a162\n"
MF "a[184:187],v[172:175],v[96:99],a[184:187]\n"
AR "v75,a163\n"
AR "v76,a180\n"
AR "v77,a181\n"
AR "v78,a182\n"
AR "v79,a183\n"
"v_pk_fma_f32 v[72:73],v[72:73],v[56:57],v[52:53] op_sel:[0,1,0] op_sel_hi:[1,1,0]\n"
"v_pk_fma_f32 v[74:75],v[74:75],v[56:57],v[52:53] op_sel:[0,1,0] op_sel_hi:[1,1,0]\n"
"v_pk_fma_f32 v[76:77],v[76:77],v[56:57],v[52:53] op_sel:[0,1,0] op_sel_hi:[1,1,0]\n"
"v_pk_fma_f32 v[78:79],v[78:79],v[56:57],v[52:53] op_sel:[0,1,0] op_sel_hi:[1,1,0]\n"
CV "v112,v72,v73\n"
CV "v113,v74,v75\n"
CV "v114,v76,v77\n"
CV "v115,v78,v79\n"
MF "a[168:171],v[168:171],v[100:103],a[168:171]\n"
"ds_read_b128 a[68:71],v2 offset:4096\n"
AW "a160,v112\n"
AW "a161,v113\n"
AW "a162,v114\n"
AW "a163,v115\n"
AR "v80,a164\n"
AR "v81,a165\n"
AR "v82,a166\n"
MF "a[188:191],v[172:175],v[100:103],a[188:191]\n"
AR "v83,a167\n"
AR "v84,a184\n"
AR "v85,a185\n"
AR "v86,a186\n"
AR "v87,a187\n"
"v_pk_fma_f32 v[80:81],v[80:81],v[58:59],v[52:53] op_sel:[0,0,1] op_sel_hi:[1,0,1]\n"
"v_pk_fma_f32 v[82:83],v[82:83],v[58:59],v[52:53] op_sel:[0,0,1] op_sel_hi:[1,0,1]\n"
"v_pk_fma_f32 v[84:85],v[84:85],v[58:59],v[52:53] op_sel:[0,0,1] op_sel_hi:[1,0,1]\n"
"v_pk_fma_f32 v[86:87],v[86:87],v[58:59],v[52:53] op_sel:[0,0,1] op_sel_hi:[1,0,1]\n"
CV "v116,v80,v81\n"
CV "v117,v82,v83\n"
CV "v118,v84,v85\n"
CV "v119,v86,v87\n"
MF "a[172:175],v[168:171],v[104:107],a[172:175]\n"
"ds_read_b128 a[108:111],v3 offset:4096\n"
AW "a164,v116\n"
AW "a165,v117\n"
AW "a166,v118\n"
AW "a167,v119\n"
AR "v72,a168\n"
AR "v73,a169\n"
AR "v74,a170\n"
MF "a[192:195],v[172:175],v[104:107],a[192:195]\n"
AR "v75,a171\n"
AR "v76,a188\n"
AR "v77,a189\n"
AR "v78,a190\n"
AR "v79,a191\n"
"v_pk_fma_f32 v[72:73],v[72:73],v[58:59],v[54:55] op_sel:[0,1,0] op_sel_hi:[1,1,0]\n"
"v_pk_fma_f32 v[74:75],v[74:75],v[58:59],v[54:55] op_sel:[0,1,0] op_sel_hi:[1,1,0]\n"
"v_pk_fma_f32 v[76:77],v[76:77],v[58:59],v[54:55] op_sel:[0,1,0] op_sel_hi:[1,1,0]\n"
"v_pk_fma_f32 v[78:79],v[78:79],v[58:59],v[54:55] op_sel:[0,1,0] op_sel_hi:[1,1,0]\n"
CV "v112,v72,v73\n"
CV "v113,v74,v75\n"
CV "v114,v76,v77\n"
CV "v115,v78,v79\n"
MF "a[176:179],v[168:171],v[108:111],a[176:179]\n"
"ds_read_b128 a[72:75],v2 offset:6144\n"
AW "a168,v112\n"
AW "a169,v113\n"
AW "a170,v114\n"
AW "a171,v115\n"
AR "v80,a172\n"
AR "v81,a173\n"
AR "v82,a174\n"
MF "a[196:199],v[172:175],v[108:111],a[196:199]\n"
AR "v83,a175\n"
AR "v84,a192\n"
AR "v85,a193\n"
AR "v86,a194\n"
AR "v87,a195\n"
"v_pk_fma_f32 v[80:81],v[80:81],v[60:61],v[54:55] op_sel:[0,0,1] op_sel_hi:[1,0,1]\n"
"v_pk_fma_f32 v[82:83],v[82:83],v[60:61],v[54:55] op_sel:[0,0,1] op_sel_hi:[1,0,1]\n"
"v_pk_fma_f32 v[84:85],v[84:85],v[60:61],v[54:55] op_sel:[0,0,1] op_sel_hi:[1,0,1]\n"
"v_pk_fma_f32 v[86:87],v[86:87],v[60:61],v[54:55] op_sel:[0,0,1] op_sel_hi:[1,0,1]\n"
CV "v116,v80,v81\n"
CV "v117,v82,v83\n"
CV "v118,v84,v85\n"
CV "v119,v86,v87\n"
MF "a[200:203],v[176:179],v[92:95],a[200:203]\n"
"ds_read_b128 a[112:115],v3 offset:6144\n"
AW "a172,v116\n"
AW "a173,v117\n"
AW "a174,v118\n"
AW "a175,v119\n"
AR "v72,a176\n"
AR "v73,a177\n"
AR "v74,a178\n"
MF "a[220:223],v[180:183],v[92:95],a[220:223]\n"
AR "v75,a179\n"
AR "v76,a196\n"
AR "v77,a197\n"
AR "v78,a198\n"
AR "v79,a199\n"
"v_pk_fma_f32 v[72:73],v[72:73],v[60:61],v[56:57] op_sel:[0,1,0] op_sel_hi:[1,1,0]\n"
"v_pk_fma_f32 v[74:75],v[74:75],v[60:61],v[56:57] op_sel:[0,1,0] op_sel_hi:[1,1,0]\n"
"v_pk_fma_f32 v[76:77],v[76:77],v[60:61],v[56:57] op_sel:[0,1,0] op_sel_hi:[1,1,0]\n"
"v_pk_fma_f32 v[78:79],v[78:79],v[60:61],v[56:57] op_sel:[0,1,0] op_sel_hi:[1,1,0]\n"
CV "v112,v72,v73\n"
CV "v113,v74,v75\n"
CV "v114,v76,v77\n"
CV "v115,v78,v79\n"
MF "a[204:207],v[176:179],v[96:99],a[204:207]\n"
"ds_read_b128 a[76:79],v2 offset:8192\n"
AW "a176,v112\n"
AW "a177,v113\n"
AW "a178,v114\n"
AW "a179,v115\n"
AR "v80,a200\n"
AR "v81,a201\n"
AR "v82,a202\n"
MF "a[224:227],v[180:183],v[96:99],a[224:227]\n"
AR "v83,a203\n"
AR "v84,a220\n"
AR "v85,a221\n"
AR "v86,a222\n"
AR "v87,a223\n"
"v_pk_fma_f32 v[80:81],v[80:81],v[56:57],v[52:53] op_sel:[0,1,0] op_sel_hi:[1,1,0]\n"
"v_pk_fma_f32 v[82:83],v[82:83],v[56:57],v[52:53] op_sel:[0,1,0] op_sel_hi:[1,1,0]\n"
"v_pk_fma_f32 v[84:85],v[84:85],v[56:57],v[52:53] op_sel:[0,1,0] op_sel_hi:[1,1,0]\n"
"v_pk_fma_f32 v[86:87],v[86:87],v[56:57],v[52:53] op_sel:[0,1,0] op_sel_hi:[1,1,0]\n"
CV "v116,v80,v81\n"
CV "v117,v82,v83\n"
CV "v118,v84,v85\n"
CV "v119,v86,v87\n"
MF "a[208:211],v[176:179],v[100:103],a[208:211]\n"
"ds_read_b128 a[116:119],v3 offset:8192\n"
AW "a20,v116\n"
AW "a21,v117\n"
AW "a22,v118\n"
AW "a23,v119\n"
AR "v72,a204\n"
AR "v73,a205\n"
AR "v74,a206\n"
MF "a[228:231],v[180:183],v[100:103],a[228:231]\n"
AR "v75,a207\n"
AR "v76,a224\n"
AR "v77,a225\n"
AR "v78,a226\n"
AR "v79,a227\n"
"v_pk_fma_f32 v[72:73],v[72:73],v[58:59],v[52:53] op_sel:[0,0,1] op_sel_hi:[1,0,1]\n"
"v_pk_fma_f32 v[74:75],v[74:75],v[58:59],v[52:53] op_sel:[0,0,1] op_sel_hi:[1,0,1]\n"
"v_pk_fma_f32 v[76:77],v[76:77],v[58:59],v[52:53] op_sel:[0,0,1] op_sel_hi:[1,0,1]\n"
"v_pk_fma_f32 v[78:79],v[78:79],v[58:59],v[52:53] op_sel:[0,0,1] op_sel_hi:[1,0,1]\n"
CV "v112,v72,v73\n"
CV "v113,v74,v75\n"
CV "v114,v76,v77\n"
CV "v115,v78,v79\n"
MF "a[212:215],v[176:179],v[104:107],a[212:215]\n"
AW "a24,v112\n"
AW "a25,v113\n"
AW "a26,v114\n"
AW "a27,v115\n"
AR "v80,a208\n"
AR "v81,a209\n"
AR "v82,a210\n"
MF "a[232:235],v[180:183],v[104:107],a[232:235]\n"
AR "v83,a211\n"
AR "v84,a228\n"
AR "v85,a229\n"
AR "v86,a230\n"
AR "v87,a231\n"
"v_pk_fma_f32 v[80:81],v[80:81],v[58:59],v[54:55] op_sel:[0,1,0] op_sel_hi:[1,1,0]\n"
"v_pk_fma_f32 v[82:83],v[82:83],v[58:59],v[54:55] op_sel:[0,1,0] op_sel_hi:[1,1,0]\n"
"v_pk_fma_f32 v[84:85],v[84:85],v[58:59],v[54:55] op_sel:[0,1,0] op_sel_hi:[1,1,0]\n"
"v_pk_fma_f32 v[86:87],v[86:87],v[58:59],v[54:55] op_sel:[0,1,0] op_sel_hi:[1,1,0]\n"
CV "v116,v80,v81\n"
CV "v117,v82,v83\n"
CV "v118,v84,v85\n"
CV "v119,v86,v87\n"
MF "a[216:219],v[176:179],v[108:111],a[216:219]\n"
"s_waitcnt vmcnt(8) lgkmcnt(0)\n"
"s_barrier\n"
TR "a[200:201],v0\n"
TR "a[202:203],v0 offset:512\n"
TR "a[204:205],v0 offset:4096\n"
TR "a[206:207],v0 offset:4608\n"
AW "a28,v116\n"
AW "a29,v117\n"
AW "a30,v118\n"
AW "a31,v119\n"
AR "v72,a212\n"
AR "v73,a213\n"
AR "v74,a214\n"
MF "a[236:239],v[180:183],v[108:111],a[236:239]\n"
AR "v75,a215\n"
AR "v76,a232\n"
AR "v77,a233\n"
AR "v78,a234\n"
AR "v79,a235\n"
"v_pk_fma_f32 v[72:73],v[72:73],v[60:61],v[54:55] op_sel:[0,0,1] op_sel_hi:[1,0,1]\n"
"v_pk_fma_f32 v[74:75],v[74:75],v[60:61],v[54:55] op_sel:[0,0,1] op_sel_hi:[1,0,1]\n"
"v_pk_fma_f32 v[76:77],v[76:77],v[60:61],v[54:55] op_sel:[0,0,1] op_sel_hi:[1,0,1]\n"
"v_pk_fma_f32 v[78:79],v[78:79],v[60:61],v[54:55] op_sel:[0,0,1] op_sel_hi:[1,0,1]\n"
CV "v112,v72,v73\n"
CV "v113,v74,v75\n"
CV "v114,v76,v77\n"
CV "v115,v78,v79\n"
TR "a[208:209],v0 offset:8\n"
TR "a[210:211],v0 offset:520\n"
TR "a[212:213],v0 offset:4104\n"
TR "a[214:215],v0 offset:4616\n"
AW "a32,v112\n"
AW "a33,v113\n"
AW "a34,v114\n"
AW "a35,v115\n"
AR "v80,a216\n"
AR "v81,a217\n"
AR "v82,a218\n"
AR "v83,a219\n"
AR "v84,a236\n"
AR "v85,a237\n"
AR "v86,a238\n"
AR "v87,a239\n"
"v_pk_fma_f32 v[80:81],v[80:81],v[60:61],v[56:57] op_sel:[0,1,0] op_sel_hi:[1,1,0]\n"
"v_pk_fma_f32 v[82:83],v[82:83],v[60:61],v[56:57] op_sel:[0,1,0] op_sel_hi:[1,1,0]\n"
"v_pk_fma_f32 v[84:85],v[84:85],v[60:61],v[56:57] op_sel:[0,1,0] op_sel_hi:[1,1,0]\n"
"v_pk_fma_f32 v[86:87],v[86:87],v[60:61],v[56:57] op_sel:[0,1,0] op_sel_hi:[1,1,0]\n"
CV "v116,v80,v81\n"
CV "v117,v82,v83\n"
CV "v118,v84,v85\n"
CV "v119,v86,v87\n"
AW "a36,v116\n"
AW "a37,v117\n"
AW "a38,v118\n"
AW "a39,v119\n"
"s_mov_b32 s13,s11\n"
"s_mov_b32 s70,s71\n"
"s_mov_b32 s57,1\n"
"s_waitcnt lgkmcnt(0)\n"
"s_branch .Lq0\n"
".Lpgqy_c:\n"
"s_cbranch_execz .Lpgqj_a\n"
".Lqy:\n"
MF "v[216:219],a[216:219],a[60:63],v[216:219]\n"
TR "a[208:209],v0 offset:8\n"
MF "v[216:219],a[220:223],a[100:103],v[216:219]\n"
TR "a[210:211],v0 offset:520\n"
MF "v[224:227],a[220:223],a[104:107],v[224:227]\n"
MF "v[224:227],a[216:219],a[64:67],v[224:227]\n"
TR "a[212:213],v0 offset:4104\n"
MF "v[232:235],a[216:219],a[68:71],v[232:235]\n"
TR "a[214:215],v0 offset:4616\n"
MF "v[232:235],a[220:223],a[108:111],v[232:235]\n"
MF "v[240:243],a[220:223],a[112:115],v[240:243]\n"
MF "v[240:243],a[216:219],a[72:75],v[240:243]\n"
MF "v[248:251],a[216:219],a[76:79],v[248:251]\n"
MF "v[248:251],a[220:223],a[116:119],v[248:251]\n"
MF "v[248:251],a[200:203],a[156:159],v[248:251]\n"
TR "a[216:217],v0 offset:16392\n"
MF "v[248:251],a[204:207],a[196:199],v[248:251]\n"
TR "a[218:219],v0 offset:16904\n"
MF "v[240:243],a[204:207],a[192:195],v[240:243]\n"
MF "v[240:243],a[200:203],a[152:155],v[240:243]\n"
TR "a[220:221],v0 offset:20488\n"
MF "v[232:235],a[200:203],a[148:151],v[232:235]\n"
TR "a[222:223],v0 offset:21000\n"
MF "v[232:235],a[204:207],a[188:191],v[232:235]\n"
MF "v[224:227],a[204:207],a[184:187],v[224:227]\n"
MF "v[224:227],a[200:203],a[144:147],v[224:227]\n"
"v_fma_f32 v248,v248,s54,v14\n"
"v_fma_f32 v249,v249,s54,v14\n"
MF "v[216:219],a[200:203],a[140:143],v[216:219]\n"
"v_fma_f32 v250,v250,s54,v14\n"
"v_fma_f32 v251,v251,s54,v14\n"
MF "v[216:219],a[204:207],a[180:183],v[216:219]\n"
"s_waitcnt lgkmcnt(4)\n"
"v_fma_f32 v252,v252,s54,v14\n"
"v_fma_f32 v253,v253,s54,v14\n"
MF "v[180:183],a[208:211],a[60:63],v[180:183]\n"
TR "a[200:201],v0\n"
"v_fma_f32 v254,v254,s54,v14\n"
MF "v[180:183],a[212:215],a[100:103],v[180:183]\n"
TR "a[202:203],v0 offset:512\n"
"v_fma_f32 v255,v255,s54,v14\n"
MF "v[188:191],a[212:215],a[104:107],v[188:191]\n"
CV "v255,v254,v255\n"
CV "v254,v252,v253\n"
MF "v[188:191],a[208:211],a[64:67],v[188:191]\n"
TR "a[204:205],v0 offset:4096\n"
CV "v253,v250,v251\n"
MF "v[196:199],a[208:211],a[68:71],v[196:199]\n"
TR "a[206:207],v0 offset:4608\n"
CV "v252,v248,v249\n"
MF "v[196:199],a[212:215],a[108:111],v[196:199]\n"
"s_add_u32 s58,s75,4096\n"
"v_fma_f32 v240,v240,s54,v14\n"
"v_fma_f32 v241,v241,s54,v14\n"
MF "v[204:207],a[212:215],a[112:115],v[204:207]\n"
"buffer_store_dwordx4 v[252:255],v9,s[40:43],s58 offen offset:1024 sc1\n"
MF "v[204:207],a[208:211],a[72:75],v[204:207]\n"
"v_fma_f32 v242,v242,s54,v14\n"
"v_fma_f32 v243,v243,s54,v14\n"
MF "v[212:215],a[208:211],a[76:79],v[212:215]\n"
"v_fma_f32 v244,v244,s54,v14\n"
"v_fma_f32 v245,v245,s54,v14\n"
MF "v[212:215],a[212:215],a[116:119],v[212:215]\n"
"s_waitcnt lgkmcnt(4)\n"
"v_fma_f32 v246,v246,s54,v14\n"
"v_fma_f32 v247,v247,s54,v14\n"
MF "v[180:183],a[216:219],a[140:143],v[180:183]\n"
TR "a[208:209],v0 offset:16384\n"
CV "v251,v246,v247\n"
MF "v[180:183],a[220:223],a[180:183],v[180:183]\n"
TR "a[210:211],v0 offset:16896\n"
CV "v250,v244,v245\n"
MF "v[188:191],a[220:223],a[184:187],v[188:191]\n"
CV "v249,v242,v243\n"
CV "v248,v240,v241\n"
MF "v[188:191],a[216:219],a[144:147],v[188:191]\n"
TR "a[212:213],v0 offset:20480\n"
"v_fma_f32 v232,v232,s54,v14\n"
MF "v[196:199],a[216:219],a[148:151],v[196:199]\n"
TR "a[214:215],v0 offset:20992\n"
"v_fma_f32 v233,v233,s54,v14\n"
MF "v[196:199],a[220:223],a[188:191],v[196:199]\n"
"buffer_store_dwordx4 v[248:251],v9,s[40:43],s58 offen sc1\n"
MF "v[204:207],a[220:223],a[192:195],v[204:207]\n"
"v_fma_f32 v234,v234,s54,v14\n"
"v_fma_f32 v235,v235,s54,v14\n"
MF "v[204:207],a[216:219],a[152:155],v[204:207]\n"
"v_fma_f32 v236,v236,s54,v14\n"
"v_fma_f32 v237,v237,s54,v14\n"
MF "v[212:215],a[216:219],a[156:159],v[212:215]\n"
"s_waitcnt vmcnt(10) lgkmcnt(0)\n"
"v_fma_f32 v238,v238,s54,v14\n"
"v_fma_f32 v239,v239,s54,v14\n"
MF "v[212:215],a[220:223],a[196:199],v[212:215]\n"
"s_barrier\n"
MF "v[176:179],a[200:203],a[60:63],v[176:179]\n"
TR "a[216:217],v1 offset:32776\n"
CV "v247,v238,v239\n"
MF "v[176:179],a[204:207],a[100:103],v[176:179]\n"
TR "a[218:219],v1 offset:33288\n"
CV "v246,v236,v237\n"
MF "v[184:187],a[204:207],a[104:107],v[184:187]\n"
"s_add_u32 s66,s67,4718592\n"
"s_sub_u32 s81,s81,384\n"
"s_mov_b32 s83,7077888\n"
MF "v[184:187],a[200:203],a[64:67],v[184:187]\n"
"s_add_u32 m0,s62,0\n"
"s_add_u32 s82,s66,2359296\n"
TR "a[220:221],v1 offset:36872\n"
MF "v[192:195],a[200:203],a[68:71],v[192:195]\n"
"s_sub_u32 s15,s82,7864320\n"
"s_min_u32 s82,s82,s15\n"
TR "a[222:223],v1 offset:37384\n"
MF "v[192:195],a[204:207],a[108:111],v[192:195]\n"
LD "v6,s[28:31],s82 offen lds\n"
MF "v[200:203],a[204:207],a[112:115],v[200:203]\n"
CV "v245,v234,v235\n"
CV "v244,v232,v233\n"
MF "v[200:203],a[200:203],a[72:75],v[200:203]\n"
"v_fma_f32 v224,v224,s54,v14\n"
"v_fma_f32 v225,v225,s54,v14\n"
MF "v[208:211],a[200:203],a[76:79],v[208:211]\n"
"buffer_store_dwordx4 v[244:247],v9,s[40:43],s75 offen offset:3072 sc1\n"
MF "v[208:211],a[204:207],a[116:119],v[208:211]\n"
LD "v11,s[28:31],s82 offen offset:1024 lds\n"
MF "v[208:211],a[208:211],a[156:159],v[208:211]\n"
TR "a[200:201],v1 offset:49160\n"
"v_fma_f32 v226,v226,s54,v14\n"
MF "v[208:211],a[212:215],a[196:199],v[208:211]\n"
TR "a[202:203],v1 offset:49672\n"
"v_fma_f32 v227,v227,s54,v14\n"
MF "v[200:203],a[212:215],a[192:195],v[200:203]\n"
"v_fma_f32 v228,v228,s54,v14\n"
"v_fma_f32 v229,v229,s54,v14\n"
MF "v[200:203],a[208:211],a[152:155],v[200:203]\n"
TR "a[204:205],v1 offset:53256\n"
"v_fma_f32 v230,v230,s54,v14\n"
MF "v[192:195],a[208:211],a[148:151],v[192:195]\n"
TR "a[206:207],v1 offset:53768\n"
"v_fma_f32 v231,v231,s54,v14\n"
MF "v[192:195],a[212:215],a[188:191],v[192:195]\n"
CV "v243,v230,v231\n"
CV "v242,v228,v229\n"
MF "v[184:187],a[212:215],a[184:187],v[184:187]\n"
CV "v241,v226,v227\n"
CV "v240,v224,v225\n"
MF "v[184:187],a[208:211],a[144:147],v[184:187]\n"
"buffer_store_dwordx4 v[240:243],v9,s[40:43],s75 offen offset:2048 sc1\n"
MF "v[176:179],a[208:211],a[140:143],v[176:179]\n"
LD "v12,s[28:31],s82 offen offset:2048 lds\n"
MF "v[176:179],a[212:215],a[180:183],v[176:179]\n"
"s_waitcnt lgkmcnt(4)\n"
"v_fma_f32 v216,v216,s54,v14\n"
"v_fma_f32 v217,v217,s54,v14\n"
MF "v[140:143],a[216:219],a[60:63],v[140:143]\n"
".Lpg13c:\n"
"s_cbranch_execz .Lpg3a\n"
TR "a[208:209],v1 offset:32768\n"
"v_fma_f32 v218,v218,s54,v14\n"
MF "v[140:143],a[220:223],a[100:103],v[140:143]\n"
TR "a[210:211],v1 offset:33280\n"
"v_fma_f32 v219,v219,s54,v14\n"
MF "v[148:151],a[220:223],a[104:107],v[148:151]\n"
"v_fma_f32 v220,v220,s54,v14\n"
"v_fma_f32 v221,v221,s54,v14\n"
MF "v[148:151],a[216:219],a[64:67],v[148:151]\n"
TR "a[212:213],v1 offset:36864\n"
"v_fma_f32 v222,v222,s54,v14\n"
MF "v[156:159],a[216:219],a[68:71],v[156:159]\n"
TR "a[214:215],v1 offset:37376\n"
"v_fma_f32 v223,v223,s54,v14\n"
MF "v[156:159],a[220:223],a[108:111],v[156:159]\n"
CV "v239,v222,v223\n"
CV "v238,v220,v221\n"
MF "v[164:167],a[220:223],a[112:115],v[164:167]\n"
CV "v237,v218,v219\n"
CV "v236,v216,v217\n"
MF "v[164:167],a[216:219],a[72:75],v[164:167]\n"
LD "v13,s[28:31],s82 offen offset:3072 lds\n"
MF "v[172:175],a[216:219],a[76:79],v[172:175]\n"
"buffer_store_dwordx4 v[236:239],v9,s[40:43],s75 offen offset:1024 sc1\n"
MF "v[172:175],a[220:223],a[116:119],v[172:175]\n"
"s_waitcnt lgkmcnt(4)\n"
"v_fma_f32 v208,v208,s54,v14\n"
"v_fma_f32 v209,v209,s54,v14\n"
MF "v[140:143],a[200:203],a[140:143],v[140:143]\n"
TR "a[216:217],v1 offset:49152\n"
"v_fma_f32 v210,v210,s54,v14\n"
MF "v[140:143],a[204:207],a[180:183],v[140:143]\n"
TR "a[218:219],v1 offset:49664\n"
"v_fma_f32 v211,v211,s54,v14\n"
MF "v[148:151],a[204:207],a[184:187],v[148:151]\n"
"v_fma_f32 v212,v212,s54,v14\n"
"v_fma_f32 v213,v213,s54,v14\n"
MF "v[148:151],a[200:203],a[144:147],v[148:151]\n"
TR "a[220:221],v1 offset:53248\n"
"v_fma_f32 v214,v214,s54,v14\n"
MF "v[156:159],a[200:203],a[148:151],v[156:159]\n"
TR "a[222:223],v1 offset:53760\n"
"v_fma_f32 v215,v215,s54,v14\n"
MF "v[156:159],a[204:207],a[188:191],v[156:159]\n"
CV "v235,v214,v215\n"
CV "v234,v212,v213\n"
MF "v[164:167],a[204:207],a[192:195],v[164:167]\n"
CV "v233,v210,v211\n"
CV "v232,v208,v209\n"
MF "v[164:167],a[200:203],a[152:155],v[164:167]\n"
"v_fma_f32 v200,v200,s54,v14\n"
"v_fma_f32 v201,v201,s54,v14\n"
MF "v[172:175],a[200:203],a[156:159],v[172:175]\n"
"buffer_store_dwordx4 v[232:235],v9,s[40:43],s75 offen sc1\n"
MF "v[172:175],a[204:207],a[196:199],v[172:175]\n"
"s_waitcnt lgkmcnt(0)\n"
"v_fma_f32 v202,v202,s54,v14\n"
"v_fma_f32 v203,v203,s54,v14\n"
MF "v[136:139],a[208:211],a[60:63],v[136:139]\n"
"s_add_u32 m0,s62,16384\n"
"s_add_u32 s82,s66,2359424\n"
TR "a[200:201],v0 offset:32776\n"
MF "v[136:139],a[212:215],a[100:103],v[136:139]\n"
"s_sub_u32 s15,s82,7864320\n"
"s_min_u32 s82,s82,s15\n"
TR "a[202:203],v0 offset:33288\n"
MF "v[144:147],a[212:215],a[104:107],v[144:147]\n"
LD "v6,s[28:31],s82 offen lds\n"
MF "v[144:147],a[208:211],a[64:67],v[144:147]\n"
TR "a[204:205],v0 offset:36872\n"
"v_fma_f32 v204,v204,s54,v14\n"
MF "v[152:155],a[208:211],a[68:71],v[152:155]\n"
TR "a[206:207],v0 offset:37384\n"
"v_fma_f32 v205,v205,s54,v14\n"
MF "v[152:155],a[212:215],a[108:111],v[152:155]\n"
"v_fma_f32 v206,v206,s54,v14\n"
"v_fma_f32 v207,v207,s54,v14\n"
MF "v[160:163],a[212:215],a[112:115],v[160:163]\n"
CV "v231,v206,v207\n"
CV "v230,v204,v205\n"
MF "v[160:163],a[208:211],a[72:75],v[160:163]\n"
CV "v229,v202,v203\n"
CV "v228,v200,v201\n"
MF "v[168:171],a[208:211],a[76:79],v[168:171]\n"
AW "a84,v228\n"
AW "a85,v229\n"
MF "v[168:171],a[212:215],a[116:119],v[168:171]\n"
LD "v11,s[28:31],s82 offen offset:1024 lds\n"
MF "v[168:171],a[216:219],a[156:159],v[168:171]\n"
TR "a[208:209],v0 offset:49160\n"
AW "a86,v230\n"
MF "v[168:171],a[220:223],a[196:199],v[168:171]\n"
TR "a[210:211],v0 offset:49672\n"
AW "a87,v231\n"
MF "v[160:163],a[220:223],a[192:195],v[160:163]\n"
"v_fma_f32 v192,v192,s54,v14\n"
"v_fma_f32 v193,v193,s54,v14\n"
MF "v[160:163],a[216:219],a[152:155],v[160:163]\n"
TR "a[212:213],v0 offset:53256\n"
"v_fma_f32 v194,v194,s54,v14\n"
MF "v[152:155],a[216:219],a[148:151],v[152:155]\n"
TR "a[214:215],v0 offset:53768\n"
"v_fma_f32 v195,v195,s54,v14\n"
MF "v[152:155],a[220:223],a[188:191],v[152:155]\n"
"v_fma_f32 v196,v196,s54,v14\n"
"v_fma_f32 v197,v197,s54,v14\n"
MF "v[144:147],a[220:223],a[184:187],v[144:147]\n"
"v_fma_f32 v198,v198,s54,v14\n"
"v_fma_f32 v199,v199,s54,v14\n"
MF "v[144:147],a[216:219],a[144:147],v[144:147]\n"
CV "v227,v198,v199\n"
CV "v226,v196,v197\n"
MF "v[136:139],a[216:219],a[140:143],v[136:139]\n"
LD "v12,s[28:31],s82 offen offset:2048 lds\n"
MF "v[136:139],a[220:223],a[180:183],v[136:139]\n"
"s_waitcnt lgkmcnt(4)\n"
CV "v225,v194,v195\n"
CV "v224,v192,v193\n"
MF "v[100:103],a[200:203],a[60:63],v[100:103]\n"
TR "a[216:217],v0 offset:32768\n"
AW "a80,v224\n"
MF "v[100:103],a[204:207],a[100:103],v[100:103]\n"
TR "a[218:219],v0 offset:33280\n"
AW "a81,v225\n"
MF "v[108:111],a[204:207],a[104:107],v[108:111]\n"
AW "a82,v226\n"
AW "a83,v227\n"
MF "v[108:111],a[200:203],a[64:67],v[108:111]\n"
TR "a[220:221],v0 offset:36864\n"
"v_fma_f32 v184,v184,s54,v14\n"
MF "v[116:119],a[200:203],a[68:71],v[116:119]\n"
TR "a[222:223],v0 offset:37376\n"
"v_fma_f32 v185,v185,s54,v14\n"
MF "v[116:119],a[204:207],a[108:111],v[116:119]\n"
"v_fma_f32 v186,v186,s54,v14\n"
"v_fma_f32 v187,v187,s54,v14\n"
MF "v[124:127],a[204:207],a[112:115],v[124:127]\n"
"v_fma_f32 v188,v188,s54,v14\n"
"v_fma_f32 v189,v189,s54,v14\n"
MF "v[124:127],a[200:203],a[72:75],v[124:127]\n"
LD "v13,s[28:31],s82 offen offset:3072 lds\n"
MF "v[132:135],a[200:203],a[76:79],v[132:135]\n"
"v_fma_f32 v190,v190,s54,v14\n"
"v_fma_f32 v191,v191,s54,v14\n"
MF "v[132:135],a[204:207],a[116:119],v[132:135]\n"
"s_waitcnt lgkmcnt(4)\n"
CV "v223,v190,v191\n"
CV "v222,v188,v189\n"
MF "v[100:103],a[208:211],a[140:143],v[100:103]\n"
TR "a[200:201],v0 offset:49152\n"
CV "v221,v186,v187\n"
MF "v[100:103],a[212:215],a[180:183],v[100:103]\n"
TR "a[202:203],v0 offset:49664\n"
CV "v220,v184,v185\n"
MF "v[108:111],a[212:215],a[184:187],v[108:111]\n"
AW "a56,v220\n"
AW "a57,v221\n"
MF "v[108:111],a[208:211],a[144:147],v[108:111]\n"
TR "a[204:205],v0 offset:53248\n"
AW "a58,v222\n"
MF "v[116:119],a[208:211],a[148:151],v[116:119]\n"
TR "a[206:207],v0 offset:53760\n"
AW "a59,v223\n"
MF "v[116:119],a[212:215],a[188:191],v[116:119]\n"
"v_fma_f32 v176,v176,s54,v14\n"
"v_fma_f32 v177,v177,s54,v14\n"
MF "v[124:127],a[212:215],a[192:195],v[124:127]\n"
"v_add_u32 v0,0xc000,v0\n"
"v_fma_f32 v178,v178,s54,v14\n"
MF "v[124:127],a[208:211],a[152:155],v[124:127]\n"
"v_add_u32 v1,0xc000,v1\n"
"v_fma_f32 v179,v179,s54,v14\n"
MF "v[132:135],a[208:211],a[156:159],v[132:135]\n"
"s_waitcnt vmcnt(14) lgkmcnt(0)\n"
"v_fma_f32 v180,v180,s54,v14\n"
"v_fma_f32 v181,v181,s54,v14\n"
MF "v[132:135],a[212:215],a[196:199],v[132:135]\n"
"s_barrier\n"
MF "v[96:99],a[216:219],a[60:63],v[96:99]\n"
"s_add_u32 m0,s62,32768\n"
"s_add_u32 s82,s66,2359552\n"
TR "a[208:209],v1 offset:16392\n"
MF "v[96:99],a[220:223],a[100:103],v[96:99]\n"
"s_sub_u32 s15,s82,7864320\n"
"s_min_u32 s82,s82,s15\n"
TR "a[210:211],v1 offset:16904\n"
MF "v[104:107],a[220:223],a[104:107],v[104:107]\n"
LD "v6,s[28:31],s82 offen lds\n"
MF "v[104:107],a[216:219],a[64:67],v[104:107]\n"
TR "a[212:213],v1 offset:20488\n"
"v_fma_f32 v182,v182,s54,v14\n"
MF "v[112:115],a[216:219],a[68:71],v[112:115]\n"
TR "a[214:215],v1 offset:21000\n"
"v_fma_f32 v183,v183,s54,v14\n"
MF "v[112:115],a[220:223],a[108:111],v[112:115]\n"
CV "v219,v182,v183\n"
CV "v218,v180,v181\n"
MF "v[120:123],a[220:223],a[112:115],v[120:123]\n"
CV "v217,v178,v179\n"
CV "v216,v176,v177\n"
MF "v[120:123],a[216:219],a[72:75],v[120:123]\n"
AW "a52,v216\n"
AW "a53,v217\n"
MF "v[128:131],a[216:219],a[76:79],v[128:131]\n"
AW "a54,v218\n"
AW "a55,v219\n"
MF "v[128:131],a[220:223],a[116:119],v[128:131]\n"
LD "v11,s[28:31],s82 offen offset:1024 lds\n"
MF "v[128:131],a[200:203],a[156:159],v[128:131]\n"
TR "a[216:217],v1 offset:32776\n"
"v_fma_f32 v168,v168,s54,v14\n"
MF "v[128:131],a[204:207],a[196:199],v[128:131]\n"
TR "a[218:219],v1 offset:33288\n"
"v_fma_f32 v169,v169,s54,v14\n"
MF "v[120:123],a[204:207],a[192:195],v[120:123]\n"
"v_fma_f32 v170,v170,s54,v14\n"
"v_fma_f32 v171,v171,s54,v14\n"
MF "v[120:123],a[200:203],a[152:155],v[120:123]\n"
TR "a[220:221],v1 offset:36872\n"
"v_fma_f32 v172,v172,s54,v14\n"
MF "v[112:115],a[200:203],a[148:151],v[112:115]\n"
TR "a[222:223],v1 offset:37384\n"
"v_fma_f32 v173,v173,s54,v14\n"
MF "v[112:115],a[204:207],a[188:191],v[112:115]\n"
"v_fma_f32 v174,v174,s54,v14\n"
"v_fma_f32 v175,v175,s54,v14\n"
MF "v[104:107],a[204:207],a[184:187],v[104:107]\n"
CV "v215,v174,v175\n"
CV "v214,v172,v173\n"
MF "v[104:107],a[200:203],a[144:147],v[104:107]\n"
CV "v213,v170,v171\n"
CV "v212,v168,v169\n"
MF "v[96:99],a[200:203],a[140:143],v[96:99]\n"
LD "v12,s[28:31],s82 offen offset:2048 lds\n"
MF "v[96:99],a[204:207],a[180:183],v[96:99]\n"
"s_waitcnt lgkmcnt(4)\n"
AW "a48,v212\n"
AW "a49,v213\n"
MF "v[60:63],a[208:211],a[60:63],v[60:63]\n"
TR "a[200:201],v1 offset:16384\n"
AW "a50,v214\n"
MF "v[60:63],a[212:215],a[100:103],v[60:63]\n"
TR "a[202:203],v1 offset:16896\n"
AW "a51,v215\n"
MF "v[68:71],a[212:215],a[104:107],v[68:71]\n"
"v_fma_f32 v160,v160,s54,v14\n"
"v_fma_f32 v161,v161,s54,v14\n"
MF "v[68:71],a[208:211],a[64:67],v[68:71]\n"
TR "a[204:205],v1 offset:20480\n"
"v_fma_f32 v162,v162,s54,v14\n"
MF "v[76:79],a[208:211],a[68:71],v[76:79]\n"
TR "a[206:207],v1 offset:20992\n"
"v_fma_f32 v163,v163,s54,v14\n"
MF "v[76:79],a[212:215],a[108:111],v[76:79]\n"
"v_fma_f32 v164,v164,s54,v14\n"
"v_fma_f32 v165,v165,s54,v14\n"
MF "v[84:87],a[212:215],a[112:115],v[84:87]\n"
"v_fma_f32 v166,v166,s54,v14\n"
"v_fma_f32 v167,v167,s54,v14\n"
MF "v[84:87],a[208:211],a[72:75],v[84:87]\n"
LD "v13,s[28:31],s82 offen offset:3072 lds\n"
MF "v[92:95],a[208:211],a[76:79],v[92:95]\n"
CV "v211,v166,v167\n"
CV "v210,v164,v165\n"
MF "v[92:95],a[212:215],a[116:119],v[92:95]\n"
"s_waitcnt lgkmcnt(4)\n"
CV "v209,v162,v163\n"
CV "v208,v160,v161\n"
MF "v[60:63],a[216:219],a[140:143],v[60:63]\n"
TR "a[208:209],v1 offset:32768\n"
AW "a44,v208\n"
MF "v[60:63],a[220:223],a[180:183],v[60:63]\n"
TR "a[210:211],v1 offset:33280\n"
AW "a45,v209\n"
MF "v[68:71],a[220:223],a[184:187],v[68:71]\n"
AW "a46,v210\n"
AW "a47,v211\n"
MF "v[68:71],a[216:219],a[144:147],v[68:71]\n"
TR "a[212:213],v1 offset:36864\n"
"v_fma_f32 v152,v152,s54,v14\n"
MF "v[76:79],a[216:219],a[148:151],v[76:79]\n"
TR "a[214:215],v1 offset:37376\n"
"v_fma_f32 v153,v153,s54,v14\n"
MF "v[76:79],a[220:223],a[188:191],v[76:79]\n"
"v_fma_f32 v154,v154,s54,v14\n"
"v_fma_f32 v155,v155,s54,v14\n"
MF "v[84:87],a[220:223],a[192:195],v[84:87]\n"
"v_fma_f32 v156,v156,s54,v14\n"
"v_fma_f32 v157,v157,s54,v14\n"
MF "v[84:87],a[216:219],a[152:155],v[84:87]\n"
"v_fma_f32 v158,v158,s54,v14\n"
"v_fma_f32 v159,v159,s54,v14\n"
MF "v[92:95],a[216:219],a[156:159],v[92:95]\n"
CV "v207,v158,v159\n"
CV "v206,v156,v157\n"
MF "v[92:95],a[220:223],a[196:199],v[92:95]\n"
"s_waitcnt lgkmcnt(0)\n"
CV "v205,v154,v155\n"
CV "v204,v152,v153\n"
MF "v[56:59],a[200:203],a[60:63],v[56:59]\n"
TR "a[216:217],v0 offset:16392\n"
AW "a40,v204\n"
MF "v[56:59],a[204:207],a[100:103],v[56:59]\n"
TR "a[218:219],v0 offset:16904\n"
AW "a41,v205\n"
MF "v[64:67],a[204:207],a[104:107],v[64:67]\n"
AW "a42,v206\n"
AW "a43,v207\n"
MF "v[64:67],a[200:203],a[64:67],v[64:67]\n"
TR "a[220:221],v0 offset:20488\n"
"v_fma_f32 v144,v144,s54,v14\n"
MF "v[72:75],a[200:203],a[68:71],v[72:75]\n"
TR "a[222:223],v0 offset:21000\n"
"v_fma_f32 v145,v145,s54,v14\n"
MF "v[72:75],a[204:207],a[108:111],v[72:75]\n"
"v_fma_f32 v146,v146,s54,v14\n"
"v_fma_f32 v147,v147,s54,v14\n"
MF "v[80:83],a[204:207],a[112:115],v[80:83]\n"
"v_fma_f32 v148,v148,s54,v14\n"
"v_fma_f32 v149,v149,s54,v14\n"
MF "v[80:83],a[200:203],a[72:75],v[80:83]\n"
"v_fma_f32 v150,v150,s54,v14\n"
"v_fma_f32 v151,v151,s54,v14\n"
MF "v[88:91],a[200:203],a[76:79],v[88:91]\n"
CV "v203,v150,v151\n"
CV "v202,v148,v149\n"
MF "v[88:91],a[204:207],a[116:119],v[88:91]\n"
CV "v201,v146,v147\n"
CV "v200,v144,v145\n"
MF "v[88:91],a[208:211],a[156:159],v[88:91]\n"
TR "a[200:201],v0 offset:32776\n"
AW "a16,v200\n"
MF "v[88:91],a[212:215],a[196:199],v[88:91]\n"
TR "a[202:203],v0 offset:33288\n"
AW "a17,v201\n"
MF "v[80:83],a[212:215],a[192:195],v[80:83]\n"
AW "a18,v202\n"
AW "a19,v203\n"
MF "v[80:83],a[208:211],a[152:155],v[80:83]\n"
TR "a[204:205],v0 offset:36872\n"
"v_fma_f32 v136,v136,s54,v14\n"
MF "v[72:75],a[208:211],a[148:151],v[72:75]\n"
TR "a[206:207],v0 offset:37384\n"
"v_fma_f32 v137,v137,s54,v14\n"
MF "v[72:75],a[212:215],a[188:191],v[72:75]\n"
"v_fma_f32 v138,v138,s54,v14\n"
"v_fma_f32 v139,v139,s54,v14\n"
MF "v[64:67],a[212:215],a[184:187],v[64:67]\n"
"v_fma_f32 v140,v140,s54,v14\n"
"v_fma_f32 v141,v141,s54,v14\n"
MF "v[64:67],a[208:211],a[144:147],v[64:67]\n"
"v_fma_f32 v142,v142,s54,v14\n"
"v_fma_f32 v143,v143,s54,v14\n"
MF "v[56:59],a[208:211],a[140:143],v[56:59]\n"
CV "v199,v142,v143\n"
CV "v198,v140,v141\n"
MF "v[56:59],a[212:215],a[180:183],v[56:59]\n"
"s_waitcnt lgkmcnt(4)\n"
CV "v197,v138,v139\n"
CV "v196,v136,v137\n"
MF "v[20:23],a[216:219],a[60:63],v[20:23]\n"
TR "a[208:209],v0 offset:16384\n"
AW "a12,v196\n"
MF "v[20:23],a[220:223],a[100:103],v[20:23]\n"
TR "a[210:211],v0 offset:16896\n"
AW "a13,v197\n"
MF "v[28:31],a[220:223],a[104:107],v[28:31]\n"
AW "a14,v198\n"
AW "a15,v199\n"
MF "v[28:31],a[216:219],a[64:67],v[28:31]\n"
TR "a[212:213],v0 offset:20480\n"
"v_fma_f32 v128,v128,s54,v14\n"
MF "v[36:39],a[216:219],a[68:71],v[36:39]\n"
TR "a[214:215],v0 offset:20992\n"
"v_fma_f32 v129,v129,s54,v14\n"
MF "v[36:39],a[220:223],a[108:111],v[36:39]\n"
"v_fma_f32 v130,v130,s54,v14\n"
"v_fma_f32 v131,v131,s54,v14\n"
MF "v[44:47],a[220:223],a[112:115],v[44:47]\n"
"v_fma_f32 v132,v132,s54,v14\n"
"v_fma_f32 v133,v133,s54,v14\n"
MF "v[44:47],a[216:219],a[72:75],v[44:47]\n"
"v_fma_f32 v134,v134,s54,v14\n"
"v_fma_f32 v135,v135,s54,v14\n"
MF "v[52:55],a[216:219],a[76:79],v[52:55]\n"
CV "v195,v134,v135\n"
CV "v194,v132,v133\n"
MF "v[52:55],a[220:223],a[116:119],v[52:55]\n"
"s_waitcnt lgkmcnt(4)\n"
CV "v193,v130,v131\n"
CV "v192,v128,v129\n"
MF "v[20:23],a[200:203],a[140:143],v[20:23]\n"
TR "a[216:217],v0 offset:32768\n"
AW "a8,v192\n"
MF "v[20:23],a[204:207],a[180:183],v[20:23]\n"
TR "a[218:219],v0 offset:33280\n"
AW "a9,v193\n"
MF "v[28:31],a[204:207],a[184:187],v[28:31]\n"
AW "a10,v194\n"
AW "a11,v195\n"
MF "v[28:31],a[200:203],a[144:147],v[28:31]\n"
TR "a[220:221],v0 offset:36864\n"
"v_fma_f32 v120,v120,s54,v14\n"
MF "v[36:39],a[200:203],a[148:151],v[36:39]\n"
TR "a[222:223],v0 offset:37376\n"
"v_fma_f32 v121,v121,s54,v14\n"
MF "v[36:39],a[204:207],a[188:191],v[36:39]\n"
"v_fma_f32 v122,v122,s54,v14\n"
"v_fma_f32 v123,v123,s54,v14\n"
MF "v[44:47],a[204:207],a[192:195],v[44:47]\n"
"v_fma_f32 v124,v124,s54,v14\n"
"v_fma_f32 v125,v125,s54,v14\n"
MF "v[44:47],a[200:203],a[152:155],v[44:47]\n"
"v_fma_f32 v126,v126,s54,v14\n"
"v_fma_f32 v127,v127,s54,v14\n"
MF "v[52:55],a[200:203],a[156:159],v[52:55]\n"
"s_waitcnt lgkmcnt(0)\n"
CV "v191,v126,v127\n"
CV "v190,v124,v125\n"
MF "v[52:55],a[204:207],a[196:199],v[52:55]\n"
"s_barrier\n"
MF "v[16:19],a[208:211],a[60:63],v[16:19]\n"
CV "v189,v122,v123\n"
CV "v188,v120,v121\n"
MF "v[16:19],a[212:215],a[100:103],v[16:19]\n"
AW "a4,v188\n"
AW "a5,v189\n"
MF "v[24:27],a[212:215],a[104:107],v[24:27]\n"
AW "a6,v190\n"
AW "a7,v191\n"
MF "v[24:27],a[208:211],a[64:67],v[24:27]\n"
"v_fma_f32 v112,v112,s54,v14\n"
"v_fma_f32 v113,v113,s54,v14\n"
MF "v[32:35],a[208:211],a[68:71],v[32:35]\n"
"v_fma_f32 v114,v114,s54,v14\n"
"v_fma_f32 v115,v115,s54,v14\n"
MF "v[32:35],a[212:215],a[108:111],v[32:35]\n"
"v_fma_f32 v116,v116,s54,v14\n"
"v_fma_f32 v117,v117,s54,v14\n"
MF "v[40:43],a[212:215],a[112:115],v[40:43]\n"
"v_fma_f32 v118,v118,s54,v14\n"
"v_fma_f32 v119,v119,s54,v14\n"
MF "v[40:43],a[208:211],a[72:75],v[40:43]\n"
CV "v187,v118,v119\n"
CV "v186,v116,v117\n"
MF "v[48:51],a[208:211],a[76:79],v[48:51]\n"
CV "v185,v114,v115\n"
CV "v184,v112,v113\n"
MF "v[48:51],a[212:215],a[116:119],v[48:51]\n"
AW "a0,v184\n"
AW "a1,v185\n"
MF "v[48:51],a[216:219],a[156:159],v[48:51]\n"
AW "a2,v186\n"
AW "a3,v187\n"
MF "v[48:51],a[220:223],a[196:199],v[48:51]\n"
"v_fma_f32 v104,v104,s54,v14\n"
"v_fma_f32 v105,v105,s54,v14\n"
MF "v[40:43],a[220:223],a[192:195],v[40:43]\n"
"v_fma_f32 v106,v106,s54,v14\n"
"v_fma_f32 v107,v107,s54,v14\n"
MF "v[40:43],a[216:219],a[152:155],v[40:43]\n"
"v_fma_f32 v108,v108,s54,v14\n"
"v_fma_f32 v109,v109,s54,v14\n"
MF "v[32:35],a[216:219],a[148:151],v[32:35]\n"
"v_fma_f32 v110,v110,s54,v14\n"
"v_fma_f32 v111,v111,s54,v14\n"
MF "v[32:35],a[220:223],a[188:191],v[32:35]\n"
CV "v183,v110,v111\n"
CV "v182,v108,v109\n"
MF "v[24:27],a[220:223],a[184:187],v[24:27]\n"
CV "v181,v106,v107\n"
CV "v180,v104,v105\n"
MF "v[24:27],a[216:219],a[144:147],v[24:27]\n"
AW "a36,v180\n"
AW "a37,v181\n"
MF "v[16:19],a[216:219],a[140:143],v[16:19]\n"
AW "a38,v182\n"
AW "a39,v183\n"
MF "v[16:19],a[220:223],a[180:183],v[16:19]\n"
"s_mov_b32 s57,1\n"
"s_mov_b32 s69,99\n"
AR "v0,a240\n"
AR "v2,a241\n"
AR "v4,a242\n"
AR "v6,a243\n"
"v_xor_b32 v1,64,v0\n"
"v_xor_b32 v3,64,v2\n"
"v_xor_b32_e64 v5,64,v4\n"
"v_add_u32 v11,97280,v6\n"
"v_add_u32 v12,194560,v6\n"
"v_add_u32 v13,291840,v6\n"
"s_mov_b64 vcc,-1\n"
"s_waitcnt vmcnt(10) lgkmcnt(0)\n"
"s_barrier\n"
"s_nop 0\n"
TR "a[200:201],v0\n"
TR "a[202:203],v0 offset:512\n"
TR "a[204:205],v0 offset:4096\n"
TR "a[206:207],v0 offset:4608\n"
"ds_read_b128 a[60:63],v2\n"
"ds_read_b128 a[100:103],v3\n"
"ds_read_b128 a[64:67],v2 offset:2048\n"
"ds_read_b128 a[104:107],v3 offset:2048\n"
"ds_read_b128 a[68:71],v2 offset:4096\n"
"ds_read_b128 a[108:111],v3 offset:4096\n"
"ds_read_b128 a[72:75],v2 offset:6144\n"
"ds_read_b128 a[112:115],v3 offset:6144\n"
"ds_read_b128 a[76:79],v2 offset:8192\n"
"ds_read_b128 a[116:119],v3 offset:8192\n"
TR "a[208:209],v0 offset:8\n"
TR "a[210:211],v0 offset:520\n"
TR "a[212:213],v0 offset:4104\n"
TR "a[214:215],v0 offset:4616\n"
"v_pk_fma_f32 v[96:97],v[96:97],s[54:55],v[14:15] op_sel_hi:[1,1,0]\n"
"v_pk_fma_f32 v[98:99],v[98:99],s[54:55],v[14:15] op_sel_hi:[1,1,0]\n"
"v_pk_fma_f32 v[100:101],v[100:101],s[54:55],v[14:15] op_sel_hi:[1,1,0]\n"
"v_pk_fma_f32 v[102:103],v[102:103],s[54:55],v[14:15] op_sel_hi:[1,1,0]\n"
CV "v179,v102,v103\n"
CV "v178,v100,v101\n"
CV "v177,v98,v99\n"
CV "v176,v96,v97\n"
AW "a32,v176\n"
".Lpg14c:\n"
"s_cbranch_execz .Lpg2a\n"
"s_nop 0\n"
AW "a33,v177\n"
AW "a34,v178\n"
AW "a35,v179\n"
"v_pk_fma_f32 v[88:89],v[88:89],s[54:55],v[14:15] op_sel_hi:[1,1,0]\n"
"v_pk_fma_f32 v[90:91],v[90:91],s[54:55],v[14:15] op_sel_hi:[1,1,0]\n"
"v_pk_fma_f32 v[92:93],v[92:93],s[54:55],v[14:15] op_sel_hi:[1,1,0]\n"
"v_pk_fma_f32 v[94:95],v[94:95],s[54:55],v[14:15] op_sel_hi:[1,1,0]\n"
CV "v175,v94,v95\n"
CV "v174,v92,v93\n"
CV "v173,v90,v91\n"
CV "v172,v88,v89\n"
AW "a28,v172\n"
AW "a29,v173\n"
AW "a30,v174\n"
AW "a31,v175\n"
"v_pk_fma_f32 v[80:81],v[80:81],s[54:55],v[14:15] op_sel_hi:[1,1,0]\n"
"v_pk_fma_f32 v[82:83],v[82:83],s[54:55],v[14:15] op_sel_hi:[1,1,0]\n"
"v_pk_fma_f32 v[84:85],v[84:85],s[54:55],v[14:15] op_sel_hi:[1,1,0]\n"
"v_pk_fma_f32 v[86:87],v[86:87],s[54:55],v[14:15] op_sel_hi:[1,1,0]\n"
CV "v171,v86,v87\n"
CV "v170,v84,v85\n"
CV "v169,v82,v83\n"
CV "v168,v80,v81\n"
AW "a24,v168\n"
AW "a25,v169\n"
AW "a26,v170\n"
AW "a27,v171\n"
"v_pk_fma_f32 v[72:73],v[72:73],s[54:55],v[14:15] op_sel_hi:[1,1,0]\n"
"v_pk_fma_f32 v[74:75],v[74:75],s[54:55],v[14:15] op_sel_hi:[1,1,0]\n"
"v_pk_fma_f32 v[76:77],v[76:77],s[54:55],v[14:15] op_sel_hi:[1,1,0]\n"
"v_pk_fma_f32 v[78:79],v[78:79],s[54:55],v[14:15] op_sel_hi:[1,1,0]\n"
CV "v167,v78,v79\n"
CV "v166,v76,v77\n"
CV "v165,v74,v75\n"
CV "v164,v72,v73\n"
AW "a20,v164\n"
AW "a21,v165\n"
AW "a22,v166\n"
AW "a23,v167\n"
"v_pk_fma_f32 v[64:65],v[64:65],s[54:55],v[14:15] op_sel_hi:[1,1,0]\n"
"v_pk_fma_f32 v[66:67],v[66:67],s[54:55],v[14:15] op_sel_hi:[1,1,0]\n"
"v_pk_fma_f32 v[68:69],v[68:69],s[54:55],v[14:15] op_sel_hi:[1,1,0]\n"
"v_pk_fma_f32 v[70:71],v[70:71],s[54:55],v[14:15] op_sel_hi:[1,1,0]\n"
CV "v163,v70,v71\n"
CV "v162,v68,v69\n"
CV "v161,v66,v67\n"
CV "v160,v64,v65\n"
AW "a176,v160\n"
AW "a177,v161\n"
AW "a178,v162\n"
AW "a179,v163\n"
"v_pk_fma_f32 v[56:57],v[56:57],s[54:55],v[14:15] op_sel_hi:[1,1,0]\n"
"v_pk_fma_f32 v[58:59],v[58:59],s[54:55],v[14:15] op_sel_hi:[1,1,0]\n"
"v_pk_fma_f32 v[60:61],v[60:61],s[54:55],v[14:15] op_sel_hi:[1,1,0]\n"
"v_pk_fma_f32 v[62:63],v[62:63],s[54:55],v[14:15] op_sel_hi:[1,1,0]\n"
CV "v159,v62,v63\n"
CV "v158,v60,v61\n"
CV "v157,v58,v59\n"
CV "v156,v56,v57\n"
AW "a172,v156\n"
AW "a173,v157\n"
AW "a174,v158\n"
AW "a175,v159\n"
"v_pk_mul_f32 v[48:49],v[48:49],s[54:55]\n"
"v_pk_mul_f32 v[50:51],v[50:51],s[54:55]\n"
"v_pk_mul_f32 v[52:53],v[52:53],s[54:55]\n"
"v_pk_mul_f32 v[54:55],v[54:55],s[54:55]\n"
CV "v155,v54,v55\n"
CV "v154,v52,v53\n"
CV "v153,v50,v51\n"
CV "v152,v48,v49\n"
AW "a168,v152\n"
AW "a169,v153\n"
AW "a170,v154\n"
AW "a171,v155\n"
"v_pk_mul_f32 v[40:41],v[40:41],s[54:55]\n"
"v_pk_mul_f32 v[42:43],v[42:43],s[54:55]\n"
"v_pk_mul_f32 v[44:45],v[44:45],s[54:55]\n"
"v_pk_mul_f32 v[46:47],v[46:47],s[54:55]\n"
CV "v151,v46,v47\n"
CV "v150,v44,v45\n"
CV "v149,v42,v43\n"
CV "v148,v40,v41\n"
AW "a120,v148\n"
AW "a121,v149\n"
AW "a122,v150\n"
AW "a123,v151\n"
"v_pk_mul_f32 v[32:33],v[32:33],s[54:55]\n"
"v_pk_mul_f32 v[34:35],v[34:35],s[54:55]\n"
"v_pk_mul_f32 v[36:37],v[36:37],s[54:55]\n"
"v_pk_mul_f32 v[38:39],v[38:39],s[54:55]\n"
CV "v147,v38,v39\n"
CV "v146,v36,v37\n"
CV "v145,v34,v35\n"
CV "v144,v32,v33\n"
AW "a96,v144\n"
AW "a97,v145\n"
AW "a98,v146\n"
AW "a99,v147\n"
"v_pk_mul_f32 v[24:25],v[24:25],s[54:55]\n"
"v_pk_mul_f32 v[26:27],v[26:27],s[54:55]\n"
"v_pk_mul_f32 v[28:29],v[28:29],s[54:55]\n"
"v_pk_mul_f32 v[30:31],v[30:31],s[54:55]\n"
CV "v143,v30,v31\n"
CV "v142,v28,v29\n"
CV "v141,v26,v27\n"
CV "v140,v24,v25\n"
AW "a92,v140\n"
AW "a93,v141\n"
AW "a94,v142\n"
AW "a95,v143\n"
"v_pk_mul_f32 v[16:17],v[16:17],s[54:55]\n"
"v_pk_mul_f32 v[18:19],v[18:19],s[54:55]\n"
"v_pk_mul_f32 v[20:21],v[20:21],s[54:55]\n"
"v_pk_mul_f32 v[22:23],v[22:23],s[54:55]\n"
CV "v139,v22,v23\n"
CV "v138,v20,v21\n"
CV "v137,v18,v19\n"
CV "v136,v16,v17\n"
AW "a88,v136\n"
AW "a89,v137\n"
AW "a90,v138\n"
AW "a91,v139\n"
"s_waitcnt lgkmcnt(0)\n"
"s_branch .Lq0\n"
".Lpgpsl_b:\n"
"s_cbranch_execz .Lpg3a\n"
".Lpgpsl_d:\n"
"s_cbranch_execz .Lisr2\n"
".Lpsl:\n"
"s_sleep 1\n"
".Lpsub:\n"
"s_memrealtime s[100:101]\n"
"s_waitcnt lgkmcnt(0)\n"
"s_sub_u32 s101,s21,s100\n"
"s_cmp_gt_i32 s101,0\n"
"s_cbranch_scc1 .Lpsl\n"
"s_add_u32 s21,s100,s97\n"
"s_setpc_b64 s[52:53]\n"
".Lstub_6:\n"
"v_cmp_le_f32 vcc,0x47800000,v10\n"
"s_cmp_eq_u32 s61,1\n"
"s_cbranch_scc0 .Lhook_6\n"
"s_sub_u32 s58,s71,s70\n"
"s_add_u32 s72,s72,s58\n"
"s_add_u32 s16,s16,s58\n"
"s_add_u32 s17,s17,s58\n"
"s_cmp_eq_u32 s57,0\n"
"s_cselect_b32 s34,0,s34\n"
"s_cselect_b32 s64,163840,s64\n"
"s_mov_b64 exec,0\n"
"s_bitcmp1_b32 s23,12\n"
"s_cbranch_scc1 .Lisy3\n"
"s_cmp_eq_u32 s56,0\n"
"s_cbranch_scc1 .Lpgq0_f\n"
"s_cmp_eq_u32 s56,1\n"
"s_cbranch_scc1 .Lpg4f\n"
"s_cmp_eq_u32 s56,2\n"
"s_cbranch_scc1 .Lpg5f\n"
"s_branch .Lpg6f\n"
".Lisy3:\n"
"s_cmp_eq_u32 s56,0\n"
"s_cbranch_scc1 .Lpgep_g\n"
"s_cmp_eq_u32 s56,1\n"
"s_cbranch_scc1 .Lpg15g\n"
"s_cmp_eq_u32 s56,2\n"
"s_cbranch_scc1 .Lpgt0e_g\n"
"s_branch .Lisr3\n"
".Lisr3:\n"
"s_mov_b64 exec,-1\n"
".Lisx3:\n"
".Lhook_6:\n"
"s_cmp_eq_u32 s77,0\n"
"s_cbranch_scc0 .Lnop_6\n"
"s_call_b64 s[52:53],.Lpsub\n"
".Lnop_6:\n"
"s_cbranch_vccz .Lback_6\n"
"s_call_b64 s[52:53],.Lslow_1\n"
"s_branch .Lback_6\n"
".Lstub_7:\n"
"v_cmp_le_f32 vcc,0x47800000,v10\n"
"s_cmp_eq_u32 s61,1\n"
"s_cbranch_scc0 .Lhook_7\n"
"s_sub_u32 s58,s71,s70\n"
"s_add_u32 s73,s73,s58\n"
"s_add_u32 s18,s18,s58\n"
"s_add_u32 s19,s19,s58\n"
"s_cmp_eq_u32 s57,0\n"
"s_cselect_b32 s38,0,s38\n"
"s_cselect_b32 s65,163840,s65\n"
".Lhook_7:\n"
"s_cmp_eq_u32 s77,0\n"
"s_cbranch_scc0 .Lnop_7\n"
"s_call_b64 s[52:53],.Lpsub\n"
"s_cmp_eq_u32 s61,4\n"
"s_cselect_b32 s77,0x47800000,s77\n"
"s_cselect_b32 s95,0x47800000,s95\n"
".Lnop_7:\n"
"s_cbranch_vccz .Lback_7\n"
"s_call_b64 s[52:53],.Lslow_0\n"
"s_branch .Lback_7\n"
".Lstub_8:\n"
"s_call_b64 s[52:53],.Lslow_1\n"
"s_branch .Lback_8\n"
".Lstub_11:\n"
"v_cmp_le_f32 vcc,0x47800000,v10\n"
"s_cmp_eq_u32 s77,0\n"
"s_cbranch_scc0 .Lnop_11\n"
"s_call_b64 s[52:53],.Lpsub\n"
".Lnop_11:\n"
"s_cbranch_vccz .Lback_11\n"
"s_call_b64 s[52:53],.Lslow_1\n"
"s_branch .Lback_11\n"
".Lstub_12:\n"
"v_cmp_le_f32 vcc,0x47800000,v10\n"
"s_cmp_eq_u32 s77,0\n"
"s_cbranch_scc0 .Lnop_12\n"
"s_call_b64 s[52:53],.Lpsub\n"
".Lnop_12:\n"
"s_cbranch_vccz .Lback_12\n"
"s_call_b64 s[52:53],.Lslow_0\n"
"s_branch .Lback_12\n"
".Lstub_13:\n"
"v_cmp_le_f32 vcc,0x47800000,v10\n"
"s_cmp_eq_u32 s61,1\n"
"s_cbranch_scc0 .Lhook_13\n"
"s_mov_b64 exec,0\n"
"s_cmp_eq_u32 s56,0\n"
"s_cbranch_scc1 .Lpg24h\n"
"s_cmp_eq_u32 s56,1\n"
"s_cbranch_scc1 .Lpg23h\n"
"s_cmp_eq_u32 s56,2\n"
"s_cbranch_scc1 .Lpg22h\n"
"s_branch .Lpg21h\n"
".Lisr4:\n"
"s_mov_b64 exec,-1\n"
".Lisx4:\n"
"s_and_b32 s58,s56,1\n"
"s_mul_i32 s79,s58,12288\n"
"s_lshl_b32 s15,s58,10\n"
"s_lshr_b32 s58,s56,1\n"
"s_mul_i32 s5,s58,6144\n"
"s_add_u32 s5,s5,s79\n"
"s_lshl_b32 s59,s58,8\n"
"s_add_u32 s15,s15,s59\n"
"s_add_u32 s15,s15,159744\n"
"s_add_u32 s88,s88,1024\n"
"s_add_u32 s89,s89,1024\n"
"s_mul_i32 s58,s58,61440\n"
"s_add_u32 s2,s89,s58\n"
"s_add_u32 s3,s88,s58\n"
"s_add_u32 s0,s2,122880\n"
"s_add_u32 s1,s3,122880\n"
"s_lshl_b32 s58,s56,10\n"
"s_add_u32 s9,s58,73728\n"
"s_add_u32 s14,s58,159744\n"
"s_bfe_u32 s4,s22,0x10014\n"
"s_lshl_b32 s4,s4,5\n"
"s_mov_b32 s97,0\n"
".Lhook_13:\n"
"s_cmp_eq_u32 s77,0\n"
"s_cbranch_scc0 .Lnop_13\n"
"s_call_b64 s[52:53],.Lpsub\n"
".Lnop_13:\n"
"s_cbranch_vccz .Lback_13\n"
"s_call_b64 s[52:53],.Lslow_1\n"
"s_branch .Lback_13\n"
".Lstub_14:\n"
"v_cmp_le_f32 vcc,0x47800000,v10\n"
"s_cmp_eq_u32 s77,0\n"
"s_cbranch_scc0 .Lnop_14\n"
"s_call_b64 s[52:53],.Lpsub\n"
".Lnop_14:\n"
"s_cbranch_vccz .Lback_14\n"
"s_call_b64 s[52:53],.Lslow_0\n"
"s_branch .Lback_14\n"
".Lstub_15:\n"
"v_cmp_le_f32 vcc,0x47800000,v10\n"
"s_cmp_eq_u32 s61,1\n"
"s_cbranch_scc0 .Lhook_15\n"
"s_mov_b32 s34,0\n"
"s_mov_b32 s64,163840\n"
".Lhook_15:\n"
"s_cmp_eq_u32 s77,0\n"
"s_cbranch_scc0 .Lnop_15\n"
"s_call_b64 s[52:53],.Lpsub\n"
".Lnop_15:\n"
"s_cbranch_vccz .Lback_15\n"
"s_call_b64 s[52:53],.Lslow_1\n"
"s_branch .Lback_15\n"
".Lstub_16:\n"
"v_cmp_le_f32 vcc,0x47800000,v10\n"
"s_cmp_eq_u32 s77,0\n"
"s_cbranch_scc0 .Lnop_16\n"
"s_call_b64 s[52:53],.Lpsub\n"
".Lnop_16:\n"
"s_cbranch_vccz .Lback_16\n"
"s_call_b64 s[52:53],.Lslow_0\n"
"s_branch .Lback_16\n"
".Lstub_17:\n"
"s_call_b64 s[52:53],.Lslow_1\n"
"s_branch .Lback_17\n"
".Lackspin_q:\n"
"s_cmp_eq_u32 s98,0\n"
"s_cbranch_scc1 .Lackok_q\n"
"s_sub_u32 s98,s98,1\n"
"s_sleep 8\n"
"v_bfe_u32 v14,v9,4,5\n"
"v_lshlrev_b32 v14,6,v14\n"
"s_lshl_b32 s58,s78,2\n"
"s_add_u32 s58,s58,s86\n"
"buffer_load_dword v10,v14,s[40:43],s58 offen offset:128 sc1\n"
"s_waitcnt vmcnt(0)\n"
"s_branch .Lack_q\n"
".Lpgep_g:\n"
"s_cbranch_execz .Lisr3\n"
".Lep:\n"
"s_lshl_b32 s58,s56,10\n"
"s_add_u32 s58,s58,73728\n"
"v_add_u32_e64 v120,s58,v9\n"
"v_add_u32 v121,65536,v120\n"
"s_sub_u32 s59,s71,s70\n"
"v_mov_b32 v10,1.0\n"
TR "v[136:137],v2 offset:24576\n"
TR "v[138:139],v2 offset:30720\n"
"v_mfma_f32_16x16x4_f32 v[16:19],v10,v57,0\n"
TR "v[140:141],v3 offset:24576\n"
TR "v[142:143],v3 offset:30720\n"
TR "v[144:145],v4 offset:24576\n"
TR "v[146:147],v4 offset:30720\n"
TR "v[148:149],v5 offset:24576\n"
TR "v[150:151],v5 offset:30720\n"
"v_mfma_f32_16x16x4_f32 v[20:23],v10,v58,0\n"
TR "v[152:153],v2 offset:25600\n"
TR "v[154:155],v2 offset:31744\n"
TR "v[156:157],v3 offset:25600\n"
TR "v[158:159],v3 offset:31744\n"
TR "v[160:161],v4 offset:25600\n"
TR "v[162:163],v4 offset:31744\n"
"v_mfma_f32_16x16x4_f32 v[24:27],v10,v59,0\n"
TR "v[164:165],v5 offset:25600\n"
TR "v[166:167],v5 offset:31744\n"
TR "v[168:169],v2 offset:26624\n"
TR "v[170:171],v2 offset:32768\n"
TR "v[172:173],v3 offset:26624\n"
TR "v[174:175],v3 offset:32768\n"
"v_mfma_f32_16x16x4_f32 v[28:31],v10,v60,0\n"
TR "v[176:177],v4 offset:26624\n"
TR "v[178:179],v4 offset:32768\n"
TR "v[180:181],v5 offset:26624\n"
TR "v[182:183],v5 offset:32768\n"
"v_mfma_f32_16x16x4_f32 v[32:35],v10,v61,0\n"
"s_add_u32 m0,s65,49152\n"
"s_nop 0\n"
LD "v8,s[36:39],s73 offen offset:3072 nt lds\n"
"s_add_u32 m0,s65,50048\n"
"s_nop 0\n"
LD "v8,s[36:39],s73 offen offset:3200 nt lds\n"
"s_waitcnt lgkmcnt(15)\n"
MF "a[0:3],v[136:139],v[92:95],a[0:3]\n"
MF "a[20:23],v[140:143],v[92:95],a[20:23]\n"
"s_add_u32 m0,s65,50944\n"
"s_nop 0\n"
LD "v8,s[36:39],s73 offen offset:3328 nt lds\n"
"s_sub_u32 s74,s96,s16\n"
"s_add_u32 s74,s74,s71\n"
"s_add_u32 s20,s20,s59\n"
"s_mov_b32 s70,s71\n"
LD "v[232:235],v9,s[40:43],s75 offen sc1\n"
"v_rcp_f32 v57,v16\n"
"v_rcp_f32 v58,v20\n"
"v_rcp_f32 v59,v24\n"
"v_rcp_f32 v60,v28\n"
"v_rcp_f32_e64 v61,v32\n"
MF "a[4:7],v[136:139],v[96:99],a[4:7]\n"
"v_mul_f32_e64 v52,-s13,v57\n"
"v_mul_f32_e64 v53,-s13,v58\n"
"v_mul_f32_e64 v54,-s13,v59\n"
"v_mul_f32_e64 v55,-s13,v60\n"
"v_mul_f32_e64 v56,-s13,v61\n"
AR "v72,a0\n"
AR "v73,a1\n"
AR "v74,a2\n"
MF "a[24:27],v[140:143],v[96:99],a[24:27]\n"
AR "v75,a3\n"
AR "v76,a20\n"
AR "v77,a21\n"
AR "v78,a22\n"
AR "v79,a23\n"
"v_pk_fma_f32 v[72:73],v[72:73],v[56:57],v[52:53] op_sel:[0,1,0] op_sel_hi:[1,1,0]\n"
"v_pk_fma_f32 v[74:75],v[74:75],v[56:57],v[52:53] op_sel:[0,1,0] op_sel_hi:[1,1,0]\n"
"v_pk_fma_f32 v[76:77],v[76:77],v[56:57],v[52:53] op_sel:[0,1,0] op_sel_hi:[1,1,0]\n"
"v_pk_fma_f32 v[78:79],v[78:79],v[56:57],v[52:53] op_sel:[0,1,0] op_sel_hi:[1,1,0]\n"
CV "v112,v72,v73\n"
CV "v113,v74,v75\n"
CV "v114,v76,v77\n"
CV "v115,v78,v79\n"
MF "a[8:11],v[136:139],v[100:103],a[8:11]\n"
"ds_read_b128 v[184:187],v120 offset:24576\n"
"ds_write_b128 v120,v[112:115] offset:24576\n"
LD "v[236:239],v9,s[40:43],s75 offen offset:1024 sc1\n"
AR "v80,a4\n"
AR "v81,a5\n"
AR "v82,a6\n"
MF "a[28:31],v[140:143],v[100:103],a[28:31]\n"
AR "v83,a7\n"
AR "v84,a24\n"
AR "v85,a25\n"
AR "v86,a26\n"
AR "v87,a27\n"
"v_pk_fma_f32 v[80:81],v[80:81],v[58:59],v[52:53] op_sel:[0,0,1] op_sel_hi:[1,0,1]\n"
"v_pk_fma_f32 v[82:83],v[82:83],v[58:59],v[52:53] op_sel:[0,0,1] op_sel_hi:[1,0,1]\n"
"v_pk_fma_f32 v[84:85],v[84:85],v[58:59],v[52:53] op_sel:[0,0,1] op_sel_hi:[1,0,1]\n"
"v_pk_fma_f32 v[86:87],v[86:87],v[58:59],v[52:53] op_sel:[0,0,1] op_sel_hi:[1,0,1]\n"
CV "v116,v80,v81\n"
CV "v117,v82,v83\n"
CV "v118,v84,v85\n"
CV "v119,v86,v87\n"
MF "a[12:15],v[136:139],v[104:107],a[12:15]\n"
"ds_read_b128 v[188:191],v120 offset:28672\n"
"ds_write_b128 v120,v[116:119] offset:28672\n"
AR "v72,a8\n"
AR "v73,a9\n"
AR "v74,a10\n"
MF "a[32:35],v[140:143],v[104:107],a[32:35]\n"
AR "v75,a11\n"
AR "v76,a28\n"
AR "v77,a29\n"
AR "v78,a30\n"
AR "v79,a31\n"
"v_pk_fma_f32 v[72:73],v[72:73],v[58:59],v[54:55] op_sel:[0,1,0] op_sel_hi:[1,1,0]\n"
"v_pk_fma_f32 v[74:75],v[74:75],v[58:59],v[54:55] op_sel:[0,1,0] op_sel_hi:[1,1,0]\n"
"v_pk_fma_f32 v[76:77],v[76:77],v[58:59],v[54:55] op_sel:[0,1,0] op_sel_hi:[1,1,0]\n"
"v_pk_fma_f32 v[78:79],v[78:79],v[58:59],v[54:55] op_sel:[0,1,0] op_sel_hi:[1,1,0]\n"
CV "v112,v72,v73\n"
CV "v113,v74,v75\n"
CV "v114,v76,v77\n"
CV "v115,v78,v79\n"
MF "a[16:19],v[136:139],v[108:111],a[16:19]\n"
"ds_read_b128 v[192:195],v120 offset:32768\n"
"ds_write_b128 v120,v[112:115] offset:32768\n"
"s_waitcnt lgkmcnt(0)\n"
"s_barrier\n"
LD "v[240:243],v9,s[40:43],s75 offen offset:2048 sc1\n"
AR "v80,a12\n"
AR "v81,a13\n"
AR "v82,a14\n"
MF "a[36:39],v[140:143],v[108:111],a[36:39]\n"
AR "v83,a15\n"
AR "v84,a32\n"
AR "v85,a33\n"
AR "v86,a34\n"
AR "v87,a35\n"
"v_pk_fma_f32 v[80:81],v[80:81],v[60:61],v[54:55] op_sel:[0,0,1] op_sel_hi:[1,0,1]\n"
"v_pk_fma_f32 v[82:83],v[82:83],v[60:61],v[54:55] op_sel:[0,0,1] op_sel_hi:[1,0,1]\n"
"v_pk_fma_f32 v[84:85],v[84:85],v[60:61],v[54:55] op_sel:[0,0,1] op_sel_hi:[1,0,1]\n"
"v_pk_fma_f32 v[86:87],v[86:87],v[60:61],v[54:55] op_sel:[0,0,1] op_sel_hi:[1,0,1]\n"
CV "v116,v80,v81\n"
CV "v117,v82,v83\n"
CV "v118,v84,v85\n"
CV "v119,v86,v87\n"
MF "a[40:43],v[144:147],v[92:95],a[40:43]\n"
"ds_read_b128 v[196:199],v120 offset:36864\n"
"ds_write_b128 v120,v[116:119] offset:36864\n"
LD "v[244:247],v9,s[40:43],s75 offen offset:3072 sc1\n"
AR "v72,a16\n"
AR "v73,a17\n"
AR "v74,a18\n"
MF "a[60:63],v[148:151],v[92:95],a[60:63]\n"
AR "v75,a19\n"
AR "v76,a36\n"
AR "v77,a37\n"
AR "v78,a38\n"
AR "v79,a39\n"
"v_pk_fma_f32 v[72:73],v[72:73],v[60:61],v[56:57] op_sel:[0,1,0] op_sel_hi:[1,1,0]\n"
"v_pk_fma_f32 v[74:75],v[74:75],v[60:61],v[56:57] op_sel:[0,1,0] op_sel_hi:[1,1,0]\n"
"v_pk_fma_f32 v[76:77],v[76:77],v[60:61],v[56:57] op_sel:[0,1,0] op_sel_hi:[1,1,0]\n"
"v_pk_fma_f32 v[78:79],v[78:79],v[60:61],v[56:57] op_sel:[0,1,0] op_sel_hi:[1,1,0]\n"
CV "v112,v72,v73\n"
CV "v113,v74,v75\n"
CV "v114,v76,v77\n"
CV "v115,v78,v79\n"
MF "a[44:47],v[144:147],v[96:99],a[44:47]\n"
"ds_read_b128 v[200:203],v120 offset:40960\n"
"ds_write_b128 v120,v[112:115] offset:40960\n"
AR "v80,a40\n"
AR "v81,a41\n"
AR "v82,a42\n"
MF "a[64:67],v[148:151],v[96:99],a[64:67]\n"
AR "v83,a43\n"
AR "v84,a60\n"
AR "v85,a61\n"
AR "v86,a62\n"
AR "v87,a63\n"
"v_pk_fma_f32 v[80:81],v[80:81],v[56:57],v[52:53] op_sel:[0,1,0] op_sel_hi:[1,1,0]\n"
"v_pk_fma_f32 v[82:83],v[82:83],v[56:57],v[52:53] op_sel:[0,1,0] op_sel_hi:[1,1,0]\n"
"v_pk_fma_f32 v[84:85],v[84:85],v[56:57],v[52:53] op_sel:[0,1,0] op_sel_hi:[1,1,0]\n"
"v_pk_fma_f32 v[86:87],v[86:87],v[56:57],v[52:53] op_sel:[0,1,0] op_sel_hi:[1,1,0]\n"
CV "v116,v80,v81\n"
CV "v117,v82,v83\n"
CV "v118,v84,v85\n"
CV "v119,v86,v87\n"
MF "a[48:51],v[144:147],v[100:103],a[48:51]\n"
"ds_read_b128 v[204:207],v120 offset:45056\n"
"ds_write_b128 v120,v[116:119] offset:45056\n"
"s_add_u32 s58,s75,4096\n"
LD "v[248:251],v9,s[40:43],s58 offen sc1\n"
AR "v72,a44\n"
AR "v73,a45\n"
AR "v74,a46\n"
MF "a[68:71],v[148:151],v[100:103],a[68:71]\n"
AR "v75,a47\n"
AR "v76,a64\n"
AR "v77,a65\n"
AR "v78,a66\n"
AR "v79,a67\n"
"v_pk_fma_f32 v[72:73],v[72:73],v[58:59],v[52:53] op_sel:[0,0,1] op_sel_hi:[1,0,1]\n"
"v_pk_fma_f32 v[74:75],v[74:75],v[58:59],v[52:53] op_sel:[0,0,1] op_sel_hi:[1,0,1]\n"
"v_pk_fma_f32 v[76:77],v[76:77],v[58:59],v[52:53] op_sel:[0,0,1] op_sel_hi:[1,0,1]\n"
"v_pk_fma_f32 v[78:79],v[78:79],v[58:59],v[52:53] op_sel:[0,0,1] op_sel_hi:[1,0,1]\n"
CV "v112,v72,v73\n"
CV "v113,v74,v75\n"
CV "v114,v76,v77\n"
CV "v115,v78,v79\n"
MF "a[52:55],v[144:147],v[104:107],a[52:55]\n"
"ds_read_b128 v[208:211],v120 offset:49152\n"
"ds_write_b128 v120,v[112:115] offset:49152\n"
LD "v[252:255],v9,s[40:43],s58 offen offset:1024 sc1\n"
AR "v80,a48\n"
AR "v81,a49\n"
AR "v82,a50\n"
MF "a[72:75],v[148:151],v[104:107],a[72:75]\n"
AR "v83,a51\n"
AR "v84,a68\n"
AR "v85,a69\n"
AR "v86,a70\n"
AR "v87,a71\n"
"v_pk_fma_f32 v[80:81],v[80:81],v[58:59],v[54:55] op_sel:[0,1,0] op_sel_hi:[1,1,0]\n"
"v_pk_fma_f32 v[82:83],v[82:83],v[58:59],v[54:55] op_sel:[0,1,0] op_sel_hi:[1,1,0]\n"
"v_pk_fma_f32 v[84:85],v[84:85],v[58:59],v[54:55] op_sel:[0,1,0] op_sel_hi:[1,1,0]\n"
"v_pk_fma_f32 v[86:87],v[86:87],v[58:59],v[54:55] op_sel:[0,1,0] op_sel_hi:[1,1,0]\n"
CV "v116,v80,v81\n"
CV "v117,v82,v83\n"
CV "v118,v84,v85\n"
CV "v119,v86,v87\n"
MF "a[56:59],v[144:147],v[108:111],a[56:59]\n"
"ds_read_b128 v[212:215],v120 offset:53248\n"
"ds_write_b128 v120,v[116:119] offset:53248\n"
AR "v72,a52\n"
AR "v73,a53\n"
AR "v74,a54\n"
MF "a[76:79],v[148:151],v[108:111],a[76:79]\n"
AR "v75,a55\n"
AR "v76,a72\n"
AR "v77,a73\n"
AR "v78,a74\n"
AR "v79,a75\n"
"v_pk_fma_f32 v[72:73],v[72:73],v[60:61],v[54:55] op_sel:[0,0,1] op_sel_hi:[1,0,1]\n"
"v_pk_fma_f32 v[74:75],v[74:75],v[60:61],v[54:55] op_sel:[0,0,1] op_sel_hi:[1,0,1]\n"
"v_pk_fma_f32 v[76:77],v[76:77],v[60:61],v[54:55] op_sel:[0,0,1] op_sel_hi:[1,0,1]\n"
"v_pk_fma_f32 v[78:79],v[78:79],v[60:61],v[54:55] op_sel:[0,0,1] op_sel_hi:[1,0,1]\n"
CV "v112,v72,v73\n"
CV "v113,v74,v75\n"
CV "v114,v76,v77\n"
CV "v115,v78,v79\n"
MF "a[80:83],v[152:155],v[92:95],a[80:83]\n"
"ds_read_b128 v[216:219],v120 offset:57344\n"
"ds_write_b128 v120,v[112:115] offset:57344\n"
AR "v80,a56\n"
AR "v81,a57\n"
AR "v82,a58\n"
MF "a[100:103],v[156:159],v[92:95],a[100:103]\n"
AR "v83,a59\n"
AR "v84,a76\n"
AR "v85,a77\n"
AR "v86,a78\n"
AR "v87,a79\n"
"v_pk_fma_f32 v[80:81],v[80:81],v[60:61],v[56:57] op_sel:[0,1,0] op_sel_hi:[1,1,0]\n"
"v_pk_fma_f32 v[82:83],v[82:83],v[60:61],v[56:57] op_sel:[0,1,0] op_sel_hi:[1,1,0]\n"
"v_pk_fma_f32 v[84:85],v[84:85],v[60:61],v[56:57] op_sel:[0,1,0] op_sel_hi:[1,1,0]\n"
"v_pk_fma_f32 v[86:87],v[86:87],v[60:61],v[56:57] op_sel:[0,1,0] op_sel_hi:[1,1,0]\n"
CV "v116,v80,v81\n"
CV "v117,v82,v83\n"
CV "v118,v84,v85\n"
CV "v119,v86,v87\n"
MF "a[84:87],v[152:155],v[96:99],a[84:87]\n"
"ds_read_b128 v[220:223],v120 offset:61440\n"
"ds_write_b128 v120,v[116:119] offset:61440\n"
AR "v72,a80\n"
AR "v73,a81\n"
AR "v74,a82\n"
MF "a[104:107],v[156:159],v[96:99],a[104:107]\n"
AR "v75,a83\n"
AR "v76,a100\n"
AR "v77,a101\n"
AR "v78,a102\n"
AR "v79,a103\n"
"v_pk_fma_f32 v[72:73],v[72:73],v[56:57],v[52:53] op_sel:[0,1,0] op_sel_hi:[1,1,0]\n"
"v_pk_fma_f32 v[74:75],v[74:75],v[56:57],v[52:53] op_sel:[0,1,0] op_sel_hi:[1,1,0]\n"
"v_pk_fma_f32 v[76:77],v[76:77],v[56:57],v[52:53] op_sel:[0,1,0] op_sel_hi:[1,1,0]\n"
"v_pk_fma_f32 v[78:79],v[78:79],v[56:57],v[52:53] op_sel:[0,1,0] op_sel_hi:[1,1,0]\n"
CV "v112,v72,v73\n"
CV "v113,v74,v75\n"
CV "v114,v76,v77\n"
CV "v115,v78,v79\n"
MF "a[88:91],v[152:155],v[100:103],a[88:91]\n"
"ds_read_b128 v[224:227],v121\n"
"ds_write_b128 v121,v[112:115]\n"
AR "v80,a84\n"
AR "v81,a85\n"
AR "v82,a86\n"
MF "a[108:111],v[156:159],v[100:103],a[108:111]\n"
AR "v83,a87\n"
AR "v84,a104\n"
AR "v85,a105\n"
AR "v86,a106\n"
AR "v87,a107\n"
"v_pk_fma_f32 v[80:81],v[80:81],v[58:59],v[52:53] op_sel:[0,0,1] op_sel_hi:[1,0,1]\n"
"v_pk_fma_f32 v[82:83],v[82:83],v[58:59],v[52:53] op_sel:[0,0,1] op_sel_hi:[1,0,1]\n"
"v_pk_fma_f32 v[84:85],v[84:85],v[58:59],v[52:53] op_sel:[0,0,1] op_sel_hi:[1,0,1]\n"
".Lpg15g:\n"
"s_cbranch_execz .Lisr3\n"
"s_nop 0\n"
"v_pk_fma_f32 v[86:87],v[86:87],v[58:59],v[52:53] op_sel:[0,0,1] op_sel_hi:[1,0,1]\n"
CV "v116,v80,v81\n"
CV "v117,v82,v83\n"
CV "v118,v84,v85\n"
CV "v119,v86,v87\n"
MF "a[92:95],v[152:155],v[104:107],a[92:95]\n"
"ds_read_b128 v[228:231],v121 offset:4096\n"
"ds_write_b128 v121,v[116:119] offset:4096\n"
AR "v72,a88\n"
AR "v73,a89\n"
AR "v74,a90\n"
MF "a[112:115],v[156:159],v[104:107],a[112:115]\n"
AR "v75,a91\n"
AR "v76,a108\n"
AR "v77,a109\n"
AR "v78,a110\n"
AR "v79,a111\n"
"v_pk_fma_f32 v[72:73],v[72:73],v[58:59],v[54:55] op_sel:[0,1,0] op_sel_hi:[1,1,0]\n"
"v_pk_fma_f32 v[74:75],v[74:75],v[58:59],v[54:55] op_sel:[0,1,0] op_sel_hi:[1,1,0]\n"
"v_pk_fma_f32 v[76:77],v[76:77],v[58:59],v[54:55] op_sel:[0,1,0] op_sel_hi:[1,1,0]\n"
"v_pk_fma_f32 v[78:79],v[78:79],v[58:59],v[54:55] op_sel:[0,1,0] op_sel_hi:[1,1,0]\n"
CV "v112,v72,v73\n"
CV "v113,v74,v75\n"
CV "v114,v76,v77\n"
CV "v115,v78,v79\n"
MF "a[96:99],v[152:155],v[108:111],a[96:99]\n"
"ds_read_b128 v[136:139],v121 offset:8192\n"
"ds_write_b128 v121,v[112:115] offset:8192\n"
AR "v80,a92\n"
AR "v81,a93\n"
AR "v82,a94\n"
MF "a[116:119],v[156:159],v[108:111],a[116:119]\n"
AR "v83,a95\n"
AR "v84,a112\n"
AR "v85,a113\n"
AR "v86,a114\n"
AR "v87,a115\n"
"v_pk_fma_f32 v[80:81],v[80:81],v[60:61],v[54:55] op_sel:[0,0,1] op_sel_hi:[1,0,1]\n"
"v_pk_fma_f32 v[82:83],v[82:83],v[60:61],v[54:55] op_sel:[0,0,1] op_sel_hi:[1,0,1]\n"
"v_pk_fma_f32 v[84:85],v[84:85],v[60:61],v[54:55] op_sel:[0,0,1] op_sel_hi:[1,0,1]\n"
"v_pk_fma_f32 v[86:87],v[86:87],v[60:61],v[54:55] op_sel:[0,0,1] op_sel_hi:[1,0,1]\n"
CV "v116,v80,v81\n"
CV "v117,v82,v83\n"
CV "v118,v84,v85\n"
CV "v119,v86,v87\n"
MF "a[120:123],v[160:163],v[92:95],a[120:123]\n"
"ds_read_b128 v[140:143],v121 offset:12288\n"
"ds_write_b128 v121,v[116:119] offset:12288\n"
AR "v72,a96\n"
AR "v73,a97\n"
AR "v74,a98\n"
MF "a[140:143],v[164:167],v[92:95],a[140:143]\n"
AR "v75,a99\n"
AR "v76,a116\n"
AR "v77,a117\n"
AR "v78,a118\n"
AR "v79,a119\n"
"v_pk_fma_f32 v[72:73],v[72:73],v[60:61],v[56:57] op_sel:[0,1,0] op_sel_hi:[1,1,0]\n"
"v_pk_fma_f32 v[74:75],v[74:75],v[60:61],v[56:57] op_sel:[0,1,0] op_sel_hi:[1,1,0]\n"
"v_pk_fma_f32 v[76:77],v[76:77],v[60:61],v[56:57] op_sel:[0,1,0] op_sel_hi:[1,1,0]\n"
"v_pk_fma_f32 v[78:79],v[78:79],v[60:61],v[56:57] op_sel:[0,1,0] op_sel_hi:[1,1,0]\n"
CV "v112,v72,v73\n"
CV "v113,v74,v75\n"
CV "v114,v76,v77\n"
CV "v115,v78,v79\n"
MF "a[124:127],v[160:163],v[96:99],a[124:127]\n"
"ds_read_b128 v[144:147],v121 offset:16384\n"
"ds_write_b128 v121,v[112:115] offset:16384\n"
AR "v80,a120\n"
AR "v81,a121\n"
AR "v82,a122\n"
MF "a[144:147],v[164:167],v[96:99],a[144:147]\n"
AR "v83,a123\n"
AR "v84,a140\n"
AR "v85,a141\n"
AR "v86,a142\n"
AR "v87,a143\n"
"v_pk_fma_f32 v[80:81],v[80:81],v[56:57],v[52:53] op_sel:[0,1,0] op_sel_hi:[1,1,0]\n"
"v_pk_fma_f32 v[82:83],v[82:83],v[56:57],v[52:53] op_sel:[0,1,0] op_sel_hi:[1,1,0]\n"
"v_pk_fma_f32 v[84:85],v[84:85],v[56:57],v[52:53] op_sel:[0,1,0] op_sel_hi:[1,1,0]\n"
"v_pk_fma_f32 v[86:87],v[86:87],v[56:57],v[52:53] op_sel:[0,1,0] op_sel_hi:[1,1,0]\n"
CV "v116,v80,v81\n"
CV "v117,v82,v83\n"
CV "v118,v84,v85\n"
CV "v119,v86,v87\n"
MF "a[128:131],v[160:163],v[100:103],a[128:131]\n"
"ds_read_b128 v[148:151],v121 offset:20480\n"
"ds_write_b128 v121,v[116:119] offset:20480\n"
AR "v72,a124\n"
AR "v73,a125\n"
AR "v74,a126\n"
MF "a[148:151],v[164:167],v[100:103],a[148:151]\n"
AR "v75,a127\n"
AR "v76,a144\n"
AR "v77,a145\n"
AR "v78,a146\n"
AR "v79,a147\n"
"v_pk_fma_f32 v[72:73],v[72:73],v[58:59],v[52:53] op_sel:[0,0,1] op_sel_hi:[1,0,1]\n"
"v_pk_fma_f32 v[74:75],v[74:75],v[58:59],v[52:53] op_sel:[0,0,1] op_sel_hi:[1,0,1]\n"
"v_pk_fma_f32 v[76:77],v[76:77],v[58:59],v[52:53] op_sel:[0,0,1] op_sel_hi:[1,0,1]\n"
"v_pk_fma_f32 v[78:79],v[78:79],v[58:59],v[52:53] op_sel:[0,0,1] op_sel_hi:[1,0,1]\n"
CV "v112,v72,v73\n"
CV "v113,v74,v75\n"
CV "v114,v76,v77\n"
CV "v115,v78,v79\n"
MF "a[132:135],v[160:163],v[104:107],a[132:135]\n"
"ds_write_b128 v120,v[112:115]\n"
AR "v80,a128\n"
AR "v81,a129\n"
AR "v82,a130\n"
MF "a[152:155],v[164:167],v[104:107],a[152:155]\n"
AR "v83,a131\n"
AR "v84,a148\n"
AR "v85,a149\n"
AR "v86,a150\n"
AR "v87,a151\n"
"v_pk_fma_f32 v[80:81],v[80:81],v[58:59],v[54:55] op_sel:[0,1,0] op_sel_hi:[1,1,0]\n"
"v_pk_fma_f32 v[82:83],v[82:83],v[58:59],v[54:55] op_sel:[0,1,0] op_sel_hi:[1,1,0]\n"
"v_pk_fma_f32 v[84:85],v[84:85],v[58:59],v[54:55] op_sel:[0,1,0] op_sel_hi:[1,1,0]\n"
"v_pk_fma_f32 v[86:87],v[86:87],v[58:59],v[54:55] op_sel:[0,1,0] op_sel_hi:[1,1,0]\n"
CV "v116,v80,v81\n"
CV "v117,v82,v83\n"
CV "v118,v84,v85\n"
CV "v119,v86,v87\n"
MF "a[136:139],v[160:163],v[108:111],a[136:139]\n"
"ds_write_b128 v120,v[116:119] offset:4096\n"
AR "v72,a132\n"
AR "v73,a133\n"
AR "v74,a134\n"
MF "a[156:159],v[164:167],v[108:111],a[156:159]\n"
AR "v75,a135\n"
AR "v76,a152\n"
AR "v77,a153\n"
AR "v78,a154\n"
AR "v79,a155\n"
"v_pk_fma_f32 v[72:73],v[72:73],v[60:61],v[54:55] op_sel:[0,0,1] op_sel_hi:[1,0,1]\n"
"v_pk_fma_f32 v[74:75],v[74:75],v[60:61],v[54:55] op_sel:[0,0,1] op_sel_hi:[1,0,1]\n"
"v_pk_fma_f32 v[76:77],v[76:77],v[60:61],v[54:55] op_sel:[0,0,1] op_sel_hi:[1,0,1]\n"
"v_pk_fma_f32 v[78:79],v[78:79],v[60:61],v[54:55] op_sel:[0,0,1] op_sel_hi:[1,0,1]\n"
CV "v112,v72,v73\n"
CV "v113,v74,v75\n"
CV "v114,v76,v77\n"
CV "v115,v78,v79\n"
MF "a[160:163],v[168:171],v[92:95],a[160:163]\n"
"ds_write_b128 v120,v[112:115] offset:8192\n"
AR "v80,a136\n"
AR "v81,a137\n"
AR "v82,a138\n"
MF "a[180:183],v[172:175],v[92:95],a[180:183]\n"
AR "v83,a139\n"
AR "v84,a156\n"
AR "v85,a157\n"
AR "v86,a158\n"
AR "v87,a159\n"
"v_pk_fma_f32 v[80:81],v[80:81],v[60:61],v[56:57] op_sel:[0,1,0] op_sel_hi:[1,1,0]\n"
"v_pk_fma_f32 v[82:83],v[82:83],v[60:61],v[56:57] op_sel:[0,1,0] op_sel_hi:[1,1,0]\n"
"v_pk_fma_f32 v[84:85],v[84:85],v[60:61],v[56:57] op_sel:[0,1,0] op_sel_hi:[1,1,0]\n"
"v_pk_fma_f32 v[86:87],v[86:87],v[60:61],v[56:57] op_sel:[0,1,0] op_sel_hi:[1,1,0]\n"
CV "v116,v80,v81\n"
CV "v117,v82,v83\n"
CV "v118,v84,v85\n"
CV "v119,v86,v87\n"
MF "a[164:167],v[168:171],v[96:99],a[164:167]\n"
"ds_write_b128 v120,v[116:119] offset:12288\n"
AR "v72,a160\n"
AR "v73,a161\n"
AR "v74,a162\n"
MF "a[184:187],v[172:175],v[96:99],a[184:187]\n"
AR "v75,a163\n"
AR "v76,a180\n"
AR "v77,a181\n"
AR "v78,a182\n"
AR "v79,a183\n"
"v_pk_fma_f32 v[72:73],v[72:73],v[56:57],v[52:53] op_sel:[0,1,0] op_sel_hi:[1,1,0]\n"
"v_pk_fma_f32 v[74:75],v[74:75],v[56:57],v[52:53] op_sel:[0,1,0] op_sel_hi:[1,1,0]\n"
"v_pk_fma_f32 v[76:77],v[76:77],v[56:57],v[52:53] op_sel:[0,1,0] op_sel_hi:[1,1,0]\n"
"v_pk_fma_f32 v[78:79],v[78:79],v[56:57],v[52:53] op_sel:[0,1,0] op_sel_hi:[1,1,0]\n"
CV "v112,v72,v73\n"
CV "v113,v74,v75\n"
CV "v114,v76,v77\n"
CV "v115,v78,v79\n"
MF "a[168:171],v[168:171],v[100:103],a[168:171]\n"
"ds_write_b128 v120,v[112:115] offset:16384\n"
AR "v80,a164\n"
AR "v81,a165\n"
AR "v82,a166\n"
MF "a[188:191],v[172:175],v[100:103],a[188:191]\n"
AR "v83,a167\n"
AR "v84,a184\n"
AR "v85,a185\n"
AR "v86,a186\n"
AR "v87,a187\n"
"v_pk_fma_f32 v[80:81],v[80:81],v[58:59],v[52:53] op_sel:[0,0,1] op_sel_hi:[1,0,1]\n"
"v_pk_fma_f32 v[82:83],v[82:83],v[58:59],v[52:53] op_sel:[0,0,1] op_sel_hi:[1,0,1]\n"
"v_pk_fma_f32 v[84:85],v[84:85],v[58:59],v[52:53] op_sel:[0,0,1] op_sel_hi:[1,0,1]\n"
"v_pk_fma_f32 v[86:87],v[86:87],v[58:59],v[52:53] op_sel:[0,0,1] op_sel_hi:[1,0,1]\n"
CV "v116,v80,v81\n"
CV "v117,v82,v83\n"
CV "v118,v84,v85\n"
CV "v119,v86,v87\n"
MF "a[172:175],v[168:171],v[104:107],a[172:175]\n"
"ds_write_b128 v120,v[116:119] offset:20480\n"
AR "v72,a168\n"
AR "v73,a169\n"
AR "v74,a170\n"
MF "a[192:195],v[172:175],v[104:107],a[192:195]\n"
AR "v75,a171\n"
AR "v76,a188\n"
AR "v77,a189\n"
AR "v78,a190\n"
AR "v79,a191\n"
"v_pk_fma_f32 v[72:73],v[72:73],v[58:59],v[54:55] op_sel:[0,1,0] op_sel_hi:[1,1,0]\n"
"v_pk_fma_f32 v[74:75],v[74:75],v[58:59],v[54:55] op_sel:[0,1,0] op_sel_hi:[1,1,0]\n"
"v_pk_fma_f32 v[76:77],v[76:77],v[58:59],v[54:55] op_sel:[0,1,0] op_sel_hi:[1,1,0]\n"
"v_pk_fma_f32 v[78:79],v[78:79],v[58:59],v[54:55] op_sel:[0,1,0] op_sel_hi:[1,1,0]\n"
CV "v112,v72,v73\n"
CV "v113,v74,v75\n"
CV "v114,v76,v77\n"
CV "v115,v78,v79\n"
MF "a[176:179],v[168:171],v[108:111],a[176:179]\n"
AR "v152,a240\n"
AW "a240,v112\n"
AR "v153,a241\n"
AW "a241,v113\n"
AR "v154,a242\n"
AW "a242,v114\n"
AR "v155,a243\n"
AW "a243,v115\n"
AR "v80,a172\n"
AR "v81,a173\n"
AR "v82,a174\n"
MF "a[196:199],v[172:175],v[108:111],a[196:199]\n"
AR "v83,a175\n"
AR "v84,a192\n"
AR "v85,a193\n"
AR "v86,a194\n"
AR "v87,a195\n"
"v_pk_fma_f32 v[80:81],v[80:81],v[60:61],v[54:55] op_sel:[0,0,1] op_sel_hi:[1,0,1]\n"
"v_pk_fma_f32 v[82:83],v[82:83],v[60:61],v[54:55] op_sel:[0,0,1] op_sel_hi:[1,0,1]\n"
"v_pk_fma_f32 v[84:85],v[84:85],v[60:61],v[54:55] op_sel:[0,0,1] op_sel_hi:[1,0,1]\n"
"v_pk_fma_f32 v[86:87],v[86:87],v[60:61],v[54:55] op_sel:[0,0,1] op_sel_hi:[1,0,1]\n"
CV "v116,v80,v81\n"
CV "v117,v82,v83\n"
CV "v118,v84,v85\n"
CV "v119,v86,v87\n"
MF "a[200:203],v[176:179],v[92:95],a[200:203]\n"
AR "v156,a244\n"
AW "a244,v116\n"
AR "v157,a245\n"
AW "a245,v117\n"
AR "v158,a246\n"
AW "a246,v118\n"
AR "v159,a247\n"
AW "a247,v119\n"
AR "v72,a176\n"
AR "v73,a177\n"
AR "v74,a178\n"
MF "a[220:223],v[180:183],v[92:95],a[220:223]\n"
AR "v75,a179\n"
AR "v76,a196\n"
AR "v77,a197\n"
AR "v78,a198\n"
AR "v79,a199\n"
"v_pk_fma_f32 v[72:73],v[72:73],v[60:61],v[56:57] op_sel:[0,1,0] op_sel_hi:[1,1,0]\n"
"v_pk_fma_f32 v[74:75],v[74:75],v[60:61],v[56:57] op_sel:[0,1,0] op_sel_hi:[1,1,0]\n"
"v_pk_fma_f32 v[76:77],v[76:77],v[60:61],v[56:57] op_sel:[0,1,0] op_sel_hi:[1,1,0]\n"
"v_pk_fma_f32 v[78:79],v[78:79],v[60:61],v[56:57] op_sel:[0,1,0] op_sel_hi:[1,1,0]\n"
CV "v112,v72,v73\n"
CV "v113,v74,v75\n"
CV "v114,v76,v77\n"
CV "v115,v78,v79\n"
MF "a[204:207],v[176:179],v[96:99],a[204:207]\n"
AR "v160,a248\n"
AW "a248,v112\n"
AR "v161,a249\n"
AW "a249,v113\n"
AR "v162,a250\n"
AW "a250,v114\n"
AR "v163,a251\n"
AW "a251,v115\n"
AR "v80,a200\n"
AR "v81,a201\n"
AR "v82,a202\n"
MF "a[224:227],v[180:183],v[96:99],a[224:227]\n"
AR "v83,a203\n"
AR "v84,a220\n"
AR "v85,a221\n"
AR "v86,a222\n"
AR "v87,a223\n"
"v_pk_fma_f32 v[80:81],v[80:81],v[56:57],v[52:53] op_sel:[0,1,0] op_sel_hi:[1,1,0]\n"
"v_pk_fma_f32 v[82:83],v[82:83],v[56:57],v[52:53] op_sel:[0,1,0] op_sel_hi:[1,1,0]\n"
"v_pk_fma_f32 v[84:85],v[84:85],v[56:57],v[52:53] op_sel:[0,1,0] op_sel_hi:[1,1,0]\n"
"v_pk_fma_f32 v[86:87],v[86:87],v[56:57],v[52:53] op_sel:[0,1,0] op_sel_hi:[1,1,0]\n"
CV "v116,v80,v81\n"
CV "v117,v82,v83\n"
CV "v118,v84,v85\n"
CV "v119,v86,v87\n"
MF "a[208:211],v[176:179],v[100:103],a[208:211]\n"
"v_mov_b32 v164,v62\n"
"v_mov_b32 v62,v116\n"
"v_mov_b32 v165,v63\n"
"v_mov_b32 v63,v117\n"
"v_mov_b32 v166,v64\n"
"v_mov_b32 v64,v118\n"
"v_mov_b32 v167,v65\n"
"v_mov_b32 v65,v119\n"
AR "v72,a204\n"
AR "v73,a205\n"
AR "v74,a206\n"
MF "a[228:231],v[180:183],v[100:103],a[228:231]\n"
AR "v75,a207\n"
AR "v76,a224\n"
AR "v77,a225\n"
AR "v78,a226\n"
AR "v79,a227\n"
"v_pk_fma_f32 v[72:73],v[72:73],v[58:59],v[52:53] op_sel:[0,0,1] op_sel_hi:[1,0,1]\n"
"v_pk_fma_f32 v[74:75],v[74:75],v[58:59],v[52:53] op_sel:[0,0,1] op_sel_hi:[1,0,1]\n"
"v_pk_fma_f32 v[76:77],v[76:77],v[58:59],v[52:53] op_sel:[0,0,1] op_sel_hi:[1,0,1]\n"
"v_pk_fma_f32 v[78:79],v[78:79],v[58:59],v[52:53] op_sel:[0,0,1] op_sel_hi:[1,0,1]\n"
CV "v112,v72,v73\n"
CV "v113,v74,v75\n"
CV "v114,v76,v77\n"
CV "v115,v78,v79\n"
MF "a[212:215],v[176:179],v[104:107],a[212:215]\n"
"v_mov_b32 v168,v66\n"
"v_mov_b32 v66,v112\n"
"v_mov_b32 v169,v67\n"
"v_mov_b32 v67,v113\n"
"v_mov_b32 v170,v68\n"
"v_mov_b32 v68,v114\n"
"v_mov_b32 v171,v69\n"
"v_mov_b32 v69,v115\n"
AR "v80,a208\n"
AR "v81,a209\n"
AR "v82,a210\n"
MF "a[232:235],v[180:183],v[104:107],a[232:235]\n"
AR "v83,a211\n"
AR "v84,a228\n"
AR "v85,a229\n"
AR "v86,a230\n"
AR "v87,a231\n"
"v_pk_fma_f32 v[80:81],v[80:81],v[58:59],v[54:55] op_sel:[0,1,0] op_sel_hi:[1,1,0]\n"
"v_pk_fma_f32 v[82:83],v[82:83],v[58:59],v[54:55] op_sel:[0,1,0] op_sel_hi:[1,1,0]\n"
"v_pk_fma_f32 v[84:85],v[84:85],v[58:59],v[54:55] op_sel:[0,1,0] op_sel_hi:[1,1,0]\n"
"v_pk_fma_f32 v[86:87],v[86:87],v[58:59],v[54:55] op_sel:[0,1,0] op_sel_hi:[1,1,0]\n"
CV "v116,v80,v81\n"
CV "v117,v82,v83\n"
CV "v118,v84,v85\n"
CV "v119,v86,v87\n"
MF "a[216:219],v[176:179],v[108:111],a[216:219]\n"
"v_mov_b32 v172,v70\n"
"v_mov_b32 v70,v116\n"
"v_mov_b32 v173,v71\n"
"v_mov_b32 v71,v117\n"
"v_mov_b32 v174,v132\n"
"v_mov_b32 v132,v118\n"
"v_mov_b32 v175,v133\n"
"v_mov_b32 v133,v119\n"
AR "v72,a212\n"
AR "v73,a213\n"
AR "v74,a214\n"
MF "a[236:239],v[180:183],v[108:111],a[236:239]\n"
AR "v75,a215\n"
AR "v76,a232\n"
AR "v77,a233\n"
AR "v78,a234\n"
AR "v79,a235\n"
"v_pk_fma_f32 v[72:73],v[72:73],v[60:61],v[54:55] op_sel:[0,0,1] op_sel_hi:[1,0,1]\n"
"v_pk_fma_f32 v[74:75],v[74:75],v[60:61],v[54:55] op_sel:[0,0,1] op_sel_hi:[1,0,1]\n"
"v_pk_fma_f32 v[76:77],v[76:77],v[60:61],v[54:55] op_sel:[0,0,1] op_sel_hi:[1,0,1]\n"
"v_pk_fma_f32 v[78:79],v[78:79],v[60:61],v[54:55] op_sel:[0,0,1] op_sel_hi:[1,0,1]\n"
CV "v112,v72,v73\n"
CV "v113,v74,v75\n"
CV "v114,v76,v77\n"
CV "v115,v78,v79\n"
"s_waitcnt vmcnt(0) lgkmcnt(0)\n"
"s_barrier\n"
"ds_read_b128 v[92:95],v0\n"
"ds_read_b128 v[96:99],v1\n"
"ds_read_b128 v[100:103],v0 offset:1024\n"
"v_mov_b32 v176,v134\n"
"v_mov_b32 v134,v112\n"
"v_mov_b32 v177,v135\n"
"v_mov_b32 v135,v113\n"
"v_mov_b32 v178,v6\n"
"v_mov_b32 v6,v114\n"
"v_mov_b32 v179,v13\n"
"v_mov_b32 v13,v115\n"
AR "v80,a216\n"
AR "v81,a217\n"
AR "v82,a218\n"
AR "v83,a219\n"
AR "v84,a236\n"
AR "v85,a237\n"
AR "v86,a238\n"
AR "v87,a239\n"
"v_pk_fma_f32 v[80:81],v[80:81],v[60:61],v[56:57] op_sel:[0,1,0] op_sel_hi:[1,1,0]\n"
"v_pk_fma_f32 v[82:83],v[82:83],v[60:61],v[56:57] op_sel:[0,1,0] op_sel_hi:[1,1,0]\n"
"v_pk_fma_f32 v[84:85],v[84:85],v[60:61],v[56:57] op_sel:[0,1,0] op_sel_hi:[1,1,0]\n"
"v_pk_fma_f32 v[86:87],v[86:87],v[60:61],v[56:57] op_sel:[0,1,0] op_sel_hi:[1,1,0]\n"
CV "v116,v80,v81\n"
CV "v117,v82,v83\n"
CV "v118,v84,v85\n"
CV "v119,v86,v87\n"
"v_mov_b32 v180,v14\n"
"v_mov_b32 v14,v116\n"
"v_mov_b32 v181,v15\n"
"v_mov_b32 v15,v117\n"
AR "v182,a254\n"
AW "a254,v118\n"
AR "v183,a255\n"
AW "a255,v119\n"
"s_mov_b32 s13,s11\n"
"v_subrev_u32 v2,49152,v2\n"
"v_subrev_u32 v3,49152,v3\n"
"v_subrev_u32 v4,49152,v4\n"
"v_subrev_u32 v5,49152,v5\n"
"s_mov_b32 s57,2\n"
"s_mov_b32 s69,1\n"
"s_mov_b32 s61,3\n"
"s_mov_b32 s97,0\n"
"s_mov_b32 s95,0x47800000\n"
"s_mov_b32 s77,0x47800000\n"
"s_branch .Latt2\n"
".LpgattB_f:\n"
"s_cbranch_execz .Lisr3\n"
".LattB:\n"
AR "v2,a245\n"
"v_xor_b32 v3,32,v2\n"
"v_xor_b32 v4,64,v2\n"
"v_xor_b32 v5,96,v2\n"
"s_mov_b32 s61,3\n"
".Latt2:\n"
"s_cmp_eq_u32 s57,1\n"
"s_cbranch_scc1 .Lt0k\n"
".Lt0n:\n"
"s_waitcnt lgkmcnt(2)\n"
MF "v[72:75],v[92:95],v[136:139],0\n"
"ds_read_b128 v[104:107],v1 offset:1024\n"
MF "v[76:79],v[92:95],v[140:143],0\n"
MF "v[80:83],v[92:95],v[144:147],0\n"
MF "v[84:87],v[92:95],v[148:151],0\n"
MF "v[88:91],v[92:95],v[152:155],0\n"
"s_waitcnt lgkmcnt(2)\n"
"ds_read_b128 v[92:95],v0 offset:2048\n"
MF "v[72:75],v[96:99],v[156:159],v[72:75]\n"
MF "v[76:79],v[96:99],v[160:163],v[76:79]\n"
MF "v[80:83],v[96:99],v[164:167],v[80:83]\n"
MF "v[84:87],v[96:99],v[168:171],v[84:87]\n"
MF "v[88:91],v[96:99],v[172:175],v[88:91]\n"
"s_waitcnt lgkmcnt(2)\n"
"ds_read_b128 v[96:99],v1 offset:2048\n"
MF "v[72:75],v[100:103],v[176:179],v[72:75]\n"
MF "v[76:79],v[100:103],v[180:183],v[76:79]\n"
MF "v[80:83],v[100:103],v[184:187],v[80:83]\n"
MF "v[84:87],v[100:103],v[188:191],v[84:87]\n"
MF "v[88:91],v[100:103],v[192:195],v[88:91]\n"
"s_waitcnt lgkmcnt(2)\n"
"ds_read_b128 v[100:103],v0 offset:6144\n"
MF "v[72:75],v[104:107],v[196:199],v[72:75]\n"
MF "v[76:79],v[104:107],v[200:203],v[76:79]\n"
MF "v[80:83],v[104:107],v[204:207],v[80:83]\n"
MF "v[84:87],v[104:107],v[208:211],v[84:87]\n"
MF "v[88:91],v[104:107],v[212:215],v[88:91]\n"
"s_waitcnt lgkmcnt(2)\n"
"ds_read_b128 v[104:107],v1 offset:6144\n"
MF "v[72:75],v[92:95],v[216:219],v[72:75]\n"
MF "v[76:79],v[92:95],v[220:223],v[76:79]\n"
MF "v[80:83],v[92:95],v[224:227],v[80:83]\n"
MF "v[84:87],v[92:95],v[228:231],v[84:87]\n"
MF "v[88:91],v[92:95],v[232:235],v[88:91]\n"
"s_waitcnt lgkmcnt(2)\n"
"ds_read_b128 v[92:95],v0 offset:7168\n"
MF "v[72:75],v[96:99],v[236:239],v[72:75]\n"
MF "v[76:79],v[96:99],v[240:243],v[76:79]\n"
MF "v[80:83],v[96:99],v[244:247],v[80:83]\n"
MF "v[84:87],v[96:99],v[248:251],v[84:87]\n"
MF "v[88:91],v[96:99],v[252:255],v[88:91]\n"
"s_waitcnt lgkmcnt(2)\n"
"ds_read_b128 v[96:99],v1 offset:7168\n"
MF "v[112:115],v[100:103],v[136:139],0\n"
MF "v[116:119],v[100:103],v[140:143],0\n"
MF "v[120:123],v[100:103],v[144:147],0\n"
MF "v[124:127],v[100:103],v[148:151],0\n"
MF "v[128:131],v[100:103],v[152:155],0\n"
"s_waitcnt lgkmcnt(2)\n"
"ds_read_b128 v[100:103],v0 offset:8192\n"
MF "v[112:115],v[104:107],v[156:159],v[112:115]\n"
"v_max3_f32 v52,v72,v73,v74\n"
"v_max_f32 v52,v52,v75\n"
MF "v[116:119],v[104:107],v[160:163],v[116:119]\n"
"v_max3_f32 v53,v76,v77,v78\n"
"v_max_f32 v53,v53,v79\n"
MF "v[120:123],v[104:107],v[164:167],v[120:123]\n"
"v_max3_f32 v54,v80,v81,v82\n"
"v_max_f32 v54,v54,v83\n"
MF "v[124:127],v[104:107],v[168:171],v[124:127]\n"
"v_max3_f32 v55,v84,v85,v86\n"
"v_max_f32 v55,v55,v87\n"
MF "v[128:131],v[104:107],v[172:175],v[128:131]\n"
"s_waitcnt lgkmcnt(2)\n"
"ds_read_b128 v[104:107],v1 offset:8192\n"
"v_max3_f32 v56,v88,v89,v90\n"
MF "v[112:115],v[92:95],v[176:179],v[112:115]\n"
"v_max_f32 v56,v56,v91\n"
MF "v[116:119],v[92:95],v[180:183],v[116:119]\n"
MF "v[120:123],v[92:95],v[184:187],v[120:123]\n"
MF "v[124:127],v[92:95],v[188:191],v[124:127]\n"
MF "v[128:131],v[92:95],v[192:195],v[128:131]\n"
"s_waitcnt lgkmcnt(2)\n"
MF "v[112:115],v[96:99],v[196:199],v[112:115]\n"
"s_waitcnt lgkmcnt(0)\n"
MF "v[116:119],v[96:99],v[200:203],v[116:119]\n"
"s_barrier\n"
MF "v[120:123],v[96:99],v[204:207],v[120:123]\n"
"s_add_u32 s72,s72,6144\n"
MF "v[124:127],v[96:99],v[208:211],v[124:127]\n"
"s_cmp_eq_u32 s72,s17\n"
"s_cselect_b32 s72,s16,s72\n"
"s_add_u32 m0,s64,0\n"
MF "v[128:131],v[96:99],v[212:215],v[128:131]\n"
LD "v7,s[32:35],s72 offen nt lds\n"
MF "v[112:115],v[100:103],v[216:219],v[112:115]\n"
"s_add_u32 m0,s64,896\n"
MF "v[116:119],v[100:103],v[220:223],v[116:119]\n"
LD "v7,s[32:35],s72 offen offset:128 nt lds\n"
MF "v[120:123],v[100:103],v[224:227],v[120:123]\n"
"s_add_u32 m0,s64,1792\n"
MF "v[124:127],v[100:103],v[228:231],v[124:127]\n"
LD "v7,s[32:35],s72 offen offset:256 nt lds\n"
MF "v[128:131],v[100:103],v[232:235],v[128:131]\n"
"s_add_u32 s72,s72,6144\n"
"s_add_u32 m0,s64,6144\n"
MF "v[112:115],v[104:107],v[236:239],v[112:115]\n"
LD "v7,s[32:35],s72 offen nt lds\n"
MF "v[116:119],v[104:107],v[240:243],v[116:119]\n"
"s_add_u32 m0,s64,7040\n"
MF "v[120:123],v[104:107],v[244:247],v[120:123]\n"
LD "v7,s[32:35],s72 offen offset:128 nt lds\n"
MF "v[124:127],v[104:107],v[248:251],v[124:127]\n"
"s_add_u32 m0,s64,7936\n"
MF "v[128:131],v[104:107],v[252:255],v[128:131]\n"
LD "v7,s[32:35],s72 offen offset:256 nt lds\n"
"s_branch .Lt0e\n"
".Lpgt0k_f:\n"
"s_cbranch_execz .Lisr3\n"
".Lt0k:\n"
"s_lshl_b32 s58,s56,10\n"
"s_add_u32 s58,s58,73728\n"
"v_add_u32 v32,s58,v9\n"
"v_add_u32 v33,65536,v32\n"
"s_waitcnt lgkmcnt(2)\n"
MF "v[72:75],v[92:95],v[136:139],0\n"
"ds_read_b128 v[104:107],v1 offset:1024\n"
AR "v62,a20\n"
MF "v[76:79],v[92:95],v[140:143],0\n"
".Lpg16f:\n"
"s_cbranch_execz .Lisr3\n"
"ds_write_b128 v32,a[0:3] offset:24576\n"
MF "v[80:83],v[92:95],v[144:147],0\n"
"ds_write_b128 v32,a[4:7] offset:28672\n"
MF "v[84:87],v[92:95],v[148:151],0\n"
AR "v63,a21\n"
AR "v64,a22\n"
MF "v[88:91],v[92:95],v[152:155],0\n"
"s_waitcnt lgkmcnt(4)\n"
"ds_read_b128 v[92:95],v0 offset:2048\n"
AR "v65,a23\n"
MF "v[72:75],v[96:99],v[156:159],v[72:75]\n"
"ds_write_b128 v32,a[8:11] offset:32768\n"
MF "v[76:79],v[96:99],v[160:163],v[76:79]\n"
AR "v66,a24\n"
AR "v67,a25\n"
MF "v[80:83],v[96:99],v[164:167],v[80:83]\n"
"ds_write_b128 v32,a[12:15] offset:36864\n"
MF "v[84:87],v[96:99],v[168:171],v[84:87]\n"
AR "v68,a26\n"
AR "v69,a27\n"
MF "v[88:91],v[96:99],v[172:175],v[88:91]\n"
"s_waitcnt lgkmcnt(6)\n"
"ds_read_b128 v[96:99],v1 offset:2048\n"
AR "v70,a28\n"
MF "v[72:75],v[100:103],v[176:179],v[72:75]\n"
"ds_write_b128 v32,a[16:19] offset:40960\n"
MF "v[76:79],v[100:103],v[180:183],v[76:79]\n"
AR "v71,a29\n"
AR "v132,a30\n"
MF "v[80:83],v[100:103],v[184:187],v[80:83]\n"
AR "v133,a31\n"
AR "v134,a32\n"
MF "v[84:87],v[100:103],v[188:191],v[84:87]\n"
"ds_write_b128 v32,a[40:43] offset:45056\n"
MF "v[88:91],v[100:103],v[192:195],v[88:91]\n"
"s_waitcnt lgkmcnt(8)\n"
"ds_read_b128 v[100:103],v0 offset:6144\n"
AR "v135,a33\n"
MF "v[72:75],v[104:107],v[196:199],v[72:75]\n"
"ds_write_b128 v32,a[44:47] offset:49152\n"
MF "v[76:79],v[104:107],v[200:203],v[76:79]\n"
AR "v6,a34\n"
AR "v13,a35\n"
MF "v[80:83],v[104:107],v[204:207],v[80:83]\n"
AR "v14,a36\n"
AR "v15,a37\n"
MF "v[84:87],v[104:107],v[208:211],v[84:87]\n"
"ds_write_b128 v32,a[48:51] offset:53248\n"
MF "v[88:91],v[104:107],v[212:215],v[88:91]\n"
"s_waitcnt lgkmcnt(8)\n"
"ds_read_b128 v[104:107],v1 offset:6144\n"
"v_accvgpr_mov_b32 a254,a38\n"
MF "v[72:75],v[92:95],v[216:219],v[72:75]\n"
"v_accvgpr_mov_b32 a255,a39\n"
"v_accvgpr_mov_b32 a240,a168\n"
MF "v[76:79],v[92:95],v[220:223],v[76:79]\n"
"ds_write_b128 v32,a[52:55] offset:57344\n"
MF "v[80:83],v[92:95],v[224:227],v[80:83]\n"
"v_accvgpr_mov_b32 a241,a169\n"
"v_accvgpr_mov_b32 a242,a170\n"
MF "v[84:87],v[92:95],v[228:231],v[84:87]\n"
"ds_write_b128 v32,a[56:59] offset:61440\n"
MF "v[88:91],v[92:95],v[232:235],v[88:91]\n"
"s_waitcnt lgkmcnt(8)\n"
"ds_read_b128 v[92:95],v0 offset:7168\n"
"v_accvgpr_mov_b32 a243,a171\n"
MF "v[72:75],v[96:99],v[236:239],v[72:75]\n"
"v_accvgpr_mov_b32 a244,a172\n"
"v_accvgpr_mov_b32 a245,a173\n"
MF "v[76:79],v[96:99],v[240:243],v[76:79]\n"
"ds_write_b128 v33,a[80:83]\n"
MF "v[80:83],v[96:99],v[244:247],v[80:83]\n"
"v_accvgpr_mov_b32 a246,a174\n"
"v_accvgpr_mov_b32 a247,a175\n"
MF "v[84:87],v[96:99],v[248:251],v[84:87]\n"
"v_accvgpr_mov_b32 a248,a176\n"
"v_accvgpr_mov_b32 a249,a177\n"
MF "v[88:91],v[96:99],v[252:255],v[88:91]\n"
"s_waitcnt lgkmcnt(7)\n"
"ds_read_b128 v[96:99],v1 offset:7168\n"
"v_accvgpr_mov_b32 a250,a178\n"
MF "v[112:115],v[100:103],v[136:139],0\n"
"ds_write_b128 v33,a[84:87] offset:4096\n"
MF "v[116:119],v[100:103],v[140:143],0\n"
"ds_write_b128 v33,a[88:91] offset:8192\n"
MF "v[120:123],v[100:103],v[144:147],0\n"
"v_accvgpr_mov_b32 a251,a179\n"
MF "v[124:127],v[100:103],v[148:151],0\n"
MF "v[128:131],v[100:103],v[152:155],0\n"
"s_waitcnt lgkmcnt(7)\n"
"ds_read_b128 v[100:103],v0 offset:8192\n"
MF "v[112:115],v[104:107],v[156:159],v[112:115]\n"
"v_max3_f32 v52,v72,v73,v74\n"
"v_max_f32 v52,v52,v75\n"
MF "v[116:119],v[104:107],v[160:163],v[116:119]\n"
"v_max3_f32 v53,v76,v77,v78\n"
"v_max_f32 v53,v53,v79\n"
MF "v[120:123],v[104:107],v[164:167],v[120:123]\n"
"v_max3_f32 v54,v80,v81,v82\n"
"v_max_f32 v54,v54,v83\n"
MF "v[124:127],v[104:107],v[168:171],v[124:127]\n"
"v_max3_f32 v55,v84,v85,v86\n"
"v_max_f32 v55,v55,v87\n"
MF "v[128:131],v[104:107],v[172:175],v[128:131]\n"
"s_waitcnt lgkmcnt(5)\n"
"ds_read_b128 v[104:107],v1 offset:8192\n"
"v_max3_f32 v56,v88,v89,v90\n"
MF "v[112:115],v[92:95],v[176:179],v[112:115]\n"
"v_max_f32 v56,v56,v91\n"
MF "v[116:119],v[92:95],v[180:183],v[116:119]\n"
"ds_write_b128 v33,a[92:95] offset:12288\n"
MF "v[120:123],v[92:95],v[184:187],v[120:123]\n"
"ds_write_b128 v33,a[96:99] offset:16384\n"
MF "v[124:127],v[92:95],v[188:191],v[124:127]\n"
"ds_write_b128 v33,a[120:123] offset:20480\n"
MF "v[128:131],v[92:95],v[192:195],v[128:131]\n"
"s_waitcnt lgkmcnt(7)\n"
"ds_write_b128 v32,a[124:127]\n"
MF "v[112:115],v[96:99],v[196:199],v[112:115]\n"
"s_waitcnt lgkmcnt(0)\n"
MF "v[116:119],v[96:99],v[200:203],v[116:119]\n"
"s_barrier\n"
MF "v[120:123],v[96:99],v[204:207],v[120:123]\n"
"s_add_u32 s72,s72,6144\n"
"ds_write_b128 v32,a[128:131] offset:4096\n"
MF "v[124:127],v[96:99],v[208:211],v[124:127]\n"
"s_cmp_eq_u32 s72,s17\n"
"s_cselect_b32 s72,s16,s72\n"
"s_add_u32 m0,s64,0\n"
MF "v[128:131],v[96:99],v[212:215],v[128:131]\n"
LD "v7,s[32:35],s72 offen nt lds\n"
MF "v[112:115],v[100:103],v[216:219],v[112:115]\n"
"s_add_u32 m0,s64,896\n"
"ds_write_b128 v32,a[132:135] offset:8192\n"
MF "v[116:119],v[100:103],v[220:223],v[116:119]\n"
LD "v7,s[32:35],s72 offen offset:128 nt lds\n"
MF "v[120:123],v[100:103],v[224:227],v[120:123]\n"
"s_add_u32 m0,s64,1792\n"
"ds_write_b128 v32,a[136:139] offset:12288\n"
MF "v[124:127],v[100:103],v[228:231],v[124:127]\n"
LD "v7,s[32:35],s72 offen offset:256 nt lds\n"
MF "v[128:131],v[100:103],v[232:235],v[128:131]\n"
"s_add_u32 s72,s72,6144\n"
"s_add_u32 m0,s64,6144\n"
MF "v[112:115],v[104:107],v[236:239],v[112:115]\n"
LD "v7,s[32:35],s72 offen nt lds\n"
MF "v[116:119],v[104:107],v[240:243],v[116:119]\n"
"s_add_u32 m0,s64,7040\n"
"ds_write_b128 v32,a[160:163] offset:16384\n"
MF "v[120:123],v[104:107],v[244:247],v[120:123]\n"
LD "v7,s[32:35],s72 offen offset:128 nt lds\n"
MF "v[124:127],v[104:107],v[248:251],v[124:127]\n"
"s_add_u32 m0,s64,7936\n"
"ds_write_b128 v32,a[164:167] offset:20480\n"
MF "v[128:131],v[104:107],v[252:255],v[128:131]\n"
LD "v7,s[32:35],s72 offen offset:256 nt lds\n"
".Lpgt0e_g:\n"
"s_cbranch_execz .Lisr3\n"
".Lt0e:\n"
"s_nop 1\n"
"v_max3_f32 v52,v52,v112,v113\n"
"v_max3_f32 v53,v53,v116,v117\n"
"v_max3_f32 v54,v54,v120,v121\n"
"v_max3_f32 v55,v55,v124,v125\n"
"s_nop 1\n"
"v_max3_f32 v56,v56,v128,v129\n"
"v_max3_f32 v52,v52,v114,v115\n"
"v_max3_f32 v53,v53,v118,v119\n"
"v_max3_f32 v54,v54,v122,v123\n"
"v_max3_f32 v55,v55,v126,v127\n"
"v_max3_f32 v56,v56,v130,v131\n"
"v_mov_b32 v57,v52\n"
"v_mov_b32 v58,v53\n"
"v_mov_b32 v59,v54\n"
"v_mov_b32 v60,v55\n"
"v_mov_b32 v61,v56\n"
"v_permlane16_swap_b32 v52,v57\n"
"v_permlane16_swap_b32 v53,v58\n"
"v_permlane16_swap_b32 v54,v59\n"
"v_permlane16_swap_b32 v55,v60\n"
"v_permlane16_swap_b32 v56,v61\n"
"v_max_f32 v52,v52,v57\n"
"v_max_f32 v53,v53,v58\n"
"v_max_f32 v54,v54,v59\n"
"v_max_f32 v55,v55,v60\n"
"v_max_f32 v56,v56,v61\n"
"v_mov_b32 v57,v52\n"
"v_mov_b32 v58,v53\n"
"v_mov_b32 v59,v54\n"
"v_mov_b32 v60,v55\n"
"v_mov_b32 v61,v56\n"
"v_permlane32_swap_b32 v52,v57\n"
"v_permlane32_swap_b32 v53,v58\n"
"v_permlane32_swap_b32 v54,v59\n"
"v_permlane32_swap_b32 v55,v60\n"
"v_permlane32_swap_b32 v56,v61\n"
"v_max_f32 v52,v52,v57\n"
"v_max_f32 v53,v53,v58\n"
"v_max_f32 v54,v54,v59\n"
"v_max_f32 v55,v55,v60\n"
"v_max_f32 v56,v56,v61\n"
"v_sub_f32 v32,0xc1300000,v52\n"
"v_sub_f32 v36,0xc1300000,v53\n"
"v_sub_f32 v40,0xc1300000,v54\n"
"v_sub_f32 v44,0xc1300000,v55\n"
"v_sub_f32 v48,0xc1300000,v56\n"
"v_mov_b32 v33,v32\n"
"v_pk_mov_b32 v[34:35],v[32:33],v[32:33]\n"
"v_mov_b32 v37,v36\n"
"v_pk_mov_b32 v[38:39],v[36:37],v[36:37]\n"
"v_mov_b32 v41,v40\n"
"v_pk_mov_b32 v[42:43],v[40:41],v[40:41]\n"
"v_mov_b32 v45,v44\n"
"v_pk_mov_b32 v[46:47],v[44:45],v[44:45]\n"
"v_mov_b32_e64 v49,v48\n"
"v_pk_mov_b32 v[50:51],v[48:49],v[48:49]\n"
"v_pk_add_f32 v[72:73],v[72:73],v[32:33]\n"
"v_pk_add_f32 v[74:75],v[74:75],v[32:33]\n"
"v_pk_add_f32 v[112:113],v[112:113],v[32:33]\n"
"v_pk_add_f32 v[114:115],v[114:115],v[32:33]\n"
"v_exp_f32 v72,v72\n"
"v_exp_f32 v73,v73\n"
"v_exp_f32 v74,v74\n"
"v_exp_f32 v75,v75\n"
"v_exp_f32 v112,v112\n"
"v_exp_f32 v113,v113\n"
"v_exp_f32 v114,v114\n"
"v_exp_f32 v115,v115\n"
"v_pk_add_f32 v[10:11],v[72:73],v[74:75]\n"
"v_pk_add_f32 v[28:29],v[112:113],v[114:115]\n"
"v_pk_add_f32 v[10:11],v[10:11],v[28:29]\n"
"v_add_f32_e64 v52,v10,v11\n"
CV "v72,v72,v73\n"
CV "v73,v74,v75\n"
CV "v74,v112,v113\n"
CV "v75,v114,v115\n"
"v_pk_add_f32 v[76:77],v[76:77],v[36:37]\n"
"v_pk_add_f32 v[78:79],v[78:79],v[36:37]\n"
"v_pk_add_f32 v[116:117],v[116:117],v[36:37]\n"
"v_pk_add_f32 v[118:119],v[118:119],v[36:37]\n"
"v_exp_f32 v76,v76\n"
"v_exp_f32 v77,v77\n"
"v_exp_f32 v78,v78\n"
"v_exp_f32 v79,v79\n"
"v_exp_f32 v116,v116\n"
"v_exp_f32 v117,v117\n"
"v_exp_f32 v118,v118\n"
"v_exp_f32 v119,v119\n"
"v_pk_add_f32 v[10:11],v[76:77],v[78:79]\n"
"v_pk_add_f32 v[28:29],v[116:117],v[118:119]\n"
"v_pk_add_f32 v[10:11],v[10:11],v[28:29]\n"
"v_add_f32_e64 v53,v10,v11\n"
CV "v76,v76,v77\n"
CV "v77,v78,v79\n"
CV "v78,v116,v117\n"
CV "v79,v118,v119\n"
"v_pk_add_f32 v[80:81],v[80:81],v[40:41]\n"
"v_pk_add_f32 v[82:83],v[82:83],v[40:41]\n"
"v_pk_add_f32 v[120:121],v[120:121],v[40:41]\n"
"v_pk_add_f32 v[122:123],v[122:123],v[40:41]\n"
"v_exp_f32 v80,v80\n"
"v_exp_f32 v81,v81\n"
"v_exp_f32 v82,v82\n"
"v_exp_f32 v83,v83\n"
"v_exp_f32 v120,v120\n"
"v_exp_f32 v121,v121\n"
"v_exp_f32 v122,v122\n"
"v_exp_f32 v123,v123\n"
"v_pk_add_f32 v[10:11],v[80:81],v[82:83]\n"
"v_pk_add_f32 v[28:29],v[120:121],v[122:123]\n"
"v_pk_add_f32 v[10:11],v[10:11],v[28:29]\n"
"v_add_f32_e64 v54,v10,v11\n"
CV "v80,v80,v81\n"
CV "v81,v82,v83\n"
CV "v82,v120,v121\n"
CV "v83,v122,v123\n"
"v_pk_add_f32 v[84:85],v[84:85],v[44:45]\n"
"v_pk_add_f32 v[86:87],v[86:87],v[44:45]\n"
"v_pk_add_f32 v[124:125],v[124:125],v[44:45]\n"
"v_pk_add_f32 v[126:127],v[126:127],v[44:45]\n"
"v_mov_b32_e64 v55,0\n"
"v_pk_add_f32 v[88:89],v[88:89],v[48:49]\n"
"v_pk_add_f32 v[90:91],v[90:91],v[48:49]\n"
"v_pk_add_f32 v[128:129],v[128:129],v[48:49]\n"
"v_pk_add_f32 v[130:131],v[130:131],v[48:49]\n"
"v_mov_b32 v56,0\n"
"s_waitcnt vmcnt(12) lgkmcnt(0)\n"
"s_barrier\n"
"ds_read_b128 v[16:19],v0 offset:24576\n"
"ds_read_b128 v[20:23],v1 offset:24576\n"
AW "a220,s13\n"
AW "a221,s13\n"
AW "a222,s13\n"
AW "a223,s13\n"
"s_waitcnt lgkmcnt(1)\n"
MF "v[92:95],v[16:19],v[136:139],v[32:35]\n"
"s_nop 0\n"
"ds_read_b128 v[24:27],v0 offset:25600\n"
"ds_read_b128 v[28:31],v1 offset:25600\n"
"s_waitcnt lgkmcnt(2)\n"
MF "v[92:95],v[20:23],v[156:159],v[92:95]\n"
"v_exp_f32 v84,v84\n"
MF "v[96:99],v[20:23],v[160:163],v[36:39]\n"
"v_exp_f32 v85,v85\n"
MF "v[96:99],v[16:19],v[140:143],v[96:99]\n"
"v_exp_f32 v86,v86\n"
MF "v[100:103],v[16:19],v[144:147],v[40:43]\n"
"v_exp_f32 v87,v87\n"
MF "v[100:103],v[20:23],v[164:167],v[100:103]\n"
"v_add_f32 v55,v55,v84\n"
"v_add_f32 v55,v55,v85\n"
MF "v[104:107],v[20:23],v[168:171],v[44:47]\n"
CV "v84,v84,v85\n"
"v_add_f32 v55,v55,v86\n"
MF "v[104:107],v[16:19],v[148:151],v[104:107]\n"
"v_add_f32 v55,v55,v87\n"
CV "v85,v86,v87\n"
MF "v[108:111],v[16:19],v[152:155],v[48:51]\n"
"v_exp_f32 v124,v124\n"
MF "v[108:111],v[20:23],v[172:175],v[108:111]\n"
"s_waitcnt lgkmcnt(0)\n"
"ds_read_b128 v[16:19],v0 offset:26624\n"
"ds_read_b128 v[20:23],v1 offset:26624\n"
MF "v[108:111],v[24:27],v[192:195],v[108:111]\n"
"v_exp_f32 v125,v125\n"
MF "v[108:111],v[28:31],v[212:215],v[108:111]\n"
"v_exp_f32 v126,v126\n"
MF "v[104:107],v[28:31],v[208:211],v[104:107]\n"
"v_exp_f32 v127,v127\n"
MF "v[104:107],v[24:27],v[188:191],v[104:107]\n"
"v_add_f32 v55,v55,v124\n"
"v_add_f32 v55,v55,v125\n"
MF "v[100:103],v[24:27],v[184:187],v[100:103]\n"
CV "v86,v124,v125\n"
"v_add_f32 v55,v55,v126\n"
MF "v[100:103],v[28:31],v[204:207],v[100:103]\n"
"v_add_f32 v55,v55,v127\n"
CV "v87,v126,v127\n"
MF "v[96:99],v[28:31],v[200:203],v[96:99]\n"
"v_exp_f32 v88,v88\n"
MF "v[96:99],v[24:27],v[180:183],v[96:99]\n"
"v_exp_f32 v89,v89\n"
MF "v[92:95],v[24:27],v[176:179],v[92:95]\n"
"v_exp_f32 v90,v90\n"
MF "v[92:95],v[28:31],v[196:199],v[92:95]\n"
"s_waitcnt lgkmcnt(0)\n"
"ds_read_b128 v[24:27],v0 offset:30720\n"
"ds_read_b128 v[28:31],v1 offset:30720\n"
MF "v[92:95],v[16:19],v[216:219],v[92:95]\n"
"v_exp_f32 v91,v91\n"
MF "v[92:95],v[20:23],v[236:239],v[92:95]\n"
"v_add_f32 v56,v56,v88\n"
"v_add_f32 v56,v56,v89\n"
MF "v[96:99],v[20:23],v[240:243],v[96:99]\n"
CV "v88,v88,v89\n"
"v_add_f32 v56,v56,v90\n"
MF "v[96:99],v[16:19],v[220:223],v[96:99]\n"
"v_add_f32 v56,v56,v91\n"
CV "v89,v90,v91\n"
MF "v[100:103],v[16:19],v[224:227],v[100:103]\n"
"v_exp_f32 v128,v128\n"
MF "v[100:103],v[20:23],v[244:247],v[100:103]\n"
"v_exp_f32 v129,v129\n"
MF "v[104:107],v[20:23],v[248:251],v[104:107]\n"
"v_exp_f32 v130,v130\n"
MF "v[104:107],v[16:19],v[228:231],v[104:107]\n"
"v_exp_f32 v131,v131\n"
MF "v[108:111],v[16:19],v[232:235],v[108:111]\n"
"v_add_f32 v56,v56,v128\n"
"v_add_f32 v56,v56,v129\n"
MF "v[108:111],v[20:23],v[252:255],v[108:111]\n"
"s_waitcnt lgkmcnt(0)\n"
"ds_read_b128 v[16:19],v0 offset:31744\n"
"ds_read_b128 v[20:23],v1 offset:31744\n"
MF "v[112:115],v[24:27],v[136:139],v[32:35]\n"
CV "v90,v128,v129\n"
"v_add_f32 v56,v56,v130\n"
MF "v[112:115],v[28:31],v[156:159],v[112:115]\n"
"v_add_f32 v56,v56,v131\n"
CV "v91,v130,v131\n"
MF "v[116:119],v[28:31],v[160:163],v[36:39]\n"
"v_exp_f32 v92,v92\n"
MF "v[116:119],v[24:27],v[140:143],v[116:119]\n"
"v_exp_f32 v93,v93\n"
MF "v[120:123],v[24:27],v[144:147],v[40:43]\n"
"v_exp_f32 v94,v94\n"
MF "v[120:123],v[28:31],v[164:167],v[120:123]\n"
"v_exp_f32 v95,v95\n"
MF "v[124:127],v[28:31],v[168:171],v[44:47]\n"
"v_add_f32 v57,v52,v92\n"
"v_add_f32 v57,v57,v93\n"
MF "v[124:127],v[24:27],v[148:151],v[124:127]\n"
CV "v92,v92,v93\n"
"v_add_f32 v57,v57,v94\n"
MF "v[128:131],v[24:27],v[152:155],v[48:51]\n"
"v_add_f32 v57,v57,v95\n"
CV "v93,v94,v95\n"
MF "v[128:131],v[28:31],v[172:175],v[128:131]\n"
"s_waitcnt lgkmcnt(0)\n"
"ds_read_b128 v[24:27],v0 offset:32768\n"
"ds_read_b128 v[28:31],v1 offset:32768\n"
MF "v[128:131],v[16:19],v[192:195],v[128:131]\n"
"v_exp_f32 v96,v96\n"
MF "v[128:131],v[20:23],v[212:215],v[128:131]\n"
"v_exp_f32 v97,v97\n"
MF "v[124:127],v[20:23],v[208:211],v[124:127]\n"
"v_exp_f32 v98,v98\n"
MF "v[124:127],v[16:19],v[188:191],v[124:127]\n"
"v_exp_f32 v99,v99\n"
MF "v[120:123],v[16:19],v[184:187],v[120:123]\n"
"v_add_f32 v58,v53,v96\n"
"v_add_f32 v58,v58,v97\n"
MF "v[120:123],v[20:23],v[204:207],v[120:123]\n"
CV "v96,v96,v97\n"
"v_add_f32 v58,v58,v98\n"
MF "v[116:119],v[20:23],v[200:203],v[116:119]\n"
"s_waitcnt vmcnt(6) lgkmcnt(0)\n"
"v_add_f32 v58,v58,v99\n"
CV "v97,v98,v99\n"
MF "v[116:119],v[16:19],v[180:183],v[116:119]\n"
"s_barrier\n"
MF "v[112:115],v[16:19],v[176:179],v[112:115]\n"
"v_exp_f32 v100,v100\n"
MF "v[112:115],v[20:23],v[196:199],v[112:115]\n"
"s_nop 0\n"
TR "v[16:17],v2 offset:49152\n"
TR "v[18:19],v2 offset:55296\n"
MF "v[112:115],v[24:27],v[216:219],v[112:115]\n"
"s_add_u32 s73,s73,12288\n"
TR "v[20:21],v3 offset:49152\n"
TR "v[22:23],v3 offset:55296\n"
MF "v[112:115],v[28:31],v[236:239],v[112:115]\n"
"s_cmp_eq_u32 s73,s19\n"
"s_cselect_b32 s73,s18,s73\n"
"s_add_u32 m0,s65,24576\n"
MF "v[116:119],v[28:31],v[240:243],v[116:119]\n"
LD "v8,s[36:39],s73 offen nt lds\n"
MF "v[116:119],v[24:27],v[220:223],v[116:119]\n"
"s_add_u32 m0,s65,25472\n"
"v_exp_f32 v101,v101\n"
MF "v[120:123],v[24:27],v[224:227],v[120:123]\n"
LD "v8,s[36:39],s73 offen offset:128 nt lds\n"
MF "v[120:123],v[28:31],v[244:247],v[120:123]\n"
"v_exp_f32 v102,v102\n"
MF "v[124:127],v[28:31],v[248:251],v[124:127]\n"
"s_add_u32 m0,s65,26368\n"
"v_exp_f32 v103,v103\n"
MF "v[124:127],v[24:27],v[228:231],v[124:127]\n"
LD "v8,s[36:39],s73 offen offset:256 nt lds\n"
MF "v[128:131],v[24:27],v[232:235],v[128:131]\n"
"v_add_f32 v59,v54,v100\n"
"v_add_f32 v59,v59,v101\n"
MF "v[128:131],v[28:31],v[252:255],v[128:131]\n"
"s_waitcnt lgkmcnt(0)\n"
TR "v[24:25],v4 offset:49152\n"
TR "v[26:27],v4 offset:55296\n"
MF "a[0:3],v[16:19],v[72:75],a[220:223]\n"
"s_add_u32 m0,s65,24576\n"
TR "v[28:29],v5 offset:49152\n"
TR "v[30:31],v5 offset:55296\n"
MF "a[20:23],v[20:23],v[72:75],a[220:223]\n"
LD "v8,s[36:39],s73 offen offset:3072 nt lds\n"
MF "a[24:27],v[20:23],v[76:79],a[220:223]\n"
CV "v100,v100,v101\n"
"v_add_f32 v59,v59,v102\n"
MF "a[4:7],v[16:19],v[76:79],a[220:223]\n"
"s_add_u32 m0,s65,25472\n"
"v_add_f32 v59,v59,v103\n"
CV "v101,v102,v103\n"
MF "a[8:11],v[16:19],v[80:83],a[220:223]\n"
LD "v8,s[36:39],s73 offen offset:3200 nt lds\n"
MF "a[28:31],v[20:23],v[80:83],a[220:223]\n"
"v_exp_f32 v104,v104\n"
MF "a[32:35],v[20:23],v[84:87],a[220:223]\n"
"s_add_u32 m0,s65,26368\n"
"v_exp_f32 v105,v105\n"
MF "a[12:15],v[16:19],v[84:87],a[220:223]\n"
LD "v8,s[36:39],s73 offen offset:3328 nt lds\n"
MF "a[16:19],v[16:19],v[88:91],a[220:223]\n"
"v_exp_f32 v106,v106\n"
MF "a[36:39],v[20:23],v[88:91],a[220:223]\n"
"s_waitcnt lgkmcnt(0)\n"
TR "v[16:17],v2 offset:50176\n"
TR "v[18:19],v2 offset:56320\n"
MF "a[56:59],v[24:27],v[88:91],a[220:223]\n"
"s_nop 0\n"
TR "v[20:21],v3 offset:50176\n"
TR "v[22:23],v3 offset:56320\n"
MF "a[76:79],v[28:31],v[88:91],a[220:223]\n"
"v_exp_f32 v107,v107\n"
MF "a[72:75],v[28:31],v[84:87],a[220:223]\n"
"v_add_f32 v60,v55,v104\n"
"v_add_f32 v60,v60,v105\n"
MF "a[52:55],v[24:27],v[84:87],a[220:223]\n"
CV "v104,v104,v105\n"
"v_add_f32 v60,v60,v106\n"
MF "a[48:51],v[24:27],v[80:83],a[220:223]\n"
"v_add_f32 v60,v60,v107\n"
CV "v105,v106,v107\n"
MF "a[68:71],v[28:31],v[80:83],a[220:223]\n"
"v_exp_f32 v108,v108\n"
MF "a[64:67],v[28:31],v[76:79],a[220:223]\n"
"v_exp_f32 v109,v109\n"
MF "a[44:47],v[24:27],v[76:79],a[220:223]\n"
"v_exp_f32 v110,v110\n"
MF "a[40:43],v[24:27],v[72:75],a[220:223]\n"
"v_exp_f32 v111,v111\n"
MF "a[60:63],v[28:31],v[72:75],a[220:223]\n"
"s_waitcnt lgkmcnt(0)\n"
TR "v[24:25],v4 offset:50176\n"
TR "v[26:27],v4 offset:56320\n"
MF "a[80:83],v[16:19],v[72:75],a[220:223]\n"
"s_nop 0\n"
TR "v[28:29],v5 offset:50176\n"
TR "v[30:31],v5 offset:56320\n"
MF "a[100:103],v[20:23],v[72:75],a[220:223]\n"
"v_add_f32 v61,v56,v108\n"
"v_add_f32 v61,v61,v109\n"
MF "a[104:107],v[20:23],v[76:79],a[220:223]\n"
CV "v108,v108,v109\n"
"v_add_f32 v61,v61,v110\n"
MF "a[84:87],v[16:19],v[76:79],a[220:223]\n"
"v_add_f32 v61,v61,v111\n"
CV "v109,v110,v111\n"
MF "a[88:91],v[16:19],v[80:83],a[220:223]\n"
"v_exp_f32 v112,v112\n"
MF "a[108:111],v[20:23],v[80:83],a[220:223]\n"
"v_exp_f32 v113,v113\n"
MF "a[112:115],v[20:23],v[84:87],a[220:223]\n"
"v_exp_f32 v114,v114\n"
MF "a[92:95],v[16:19],v[84:87],a[220:223]\n"
"v_exp_f32 v115,v115\n"
MF "a[96:99],v[16:19],v[88:91],a[220:223]\n"
"v_add_f32 v57,v57,v112\n"
"v_add_f32 v57,v57,v113\n"
MF "a[116:119],v[20:23],v[88:91],a[220:223]\n"
"s_waitcnt lgkmcnt(0)\n"
TR "v[16:17],v2 offset:51200\n"
TR "v[18:19],v2 offset:57344\n"
MF "a[136:139],v[24:27],v[88:91],a[220:223]\n"
"s_nop 0\n"
TR "v[20:21],v3 offset:51200\n"
TR "v[22:23],v3 offset:57344\n"
MF "a[156:159],v[28:31],v[88:91],a[220:223]\n"
CV "v94,v112,v113\n"
"v_add_f32 v57,v57,v114\n"
MF "a[152:155],v[28:31],v[84:87],a[220:223]\n"
"v_add_f32 v57,v57,v115\n"
CV "v95,v114,v115\n"
MF "a[132:135],v[24:27],v[84:87],a[220:223]\n"
"v_exp_f32 v116,v116\n"
MF "a[128:131],v[24:27],v[80:83],a[220:223]\n"
"v_exp_f32 v117,v117\n"
MF "a[148:151],v[28:31],v[80:83],a[220:223]\n"
"v_exp_f32 v118,v118\n"
MF "a[144:147],v[28:31],v[76:79],a[220:223]\n"
"v_exp_f32 v119,v119\n"
MF "a[124:127],v[24:27],v[76:79],a[220:223]\n"
"v_add_f32 v58,v58,v116\n"
"v_add_f32 v58,v58,v117\n"
MF "a[120:123],v[24:27],v[72:75],a[220:223]\n"
CV "v98,v116,v117\n"
"v_add_f32 v58,v58,v118\n"
MF "a[140:143],v[28:31],v[72:75],a[220:223]\n"
"s_waitcnt lgkmcnt(0)\n"
TR "v[24:25],v4 offset:51200\n"
TR "v[26:27],v4 offset:57344\n"
MF "a[160:163],v[16:19],v[72:75],a[220:223]\n"
"s_nop 0\n"
TR "v[28:29],v5 offset:51200\n"
TR "v[30:31],v5 offset:57344\n"
MF "a[180:183],v[20:23],v[72:75],a[220:223]\n"
"v_add_f32 v58,v58,v119\n"
CV "v99,v118,v119\n"
MF "a[184:187],v[20:23],v[76:79],a[220:223]\n"
"v_exp_f32 v120,v120\n"
MF "a[164:167],v[16:19],v[76:79],a[220:223]\n"
"v_exp_f32 v121,v121\n"
MF "a[168:171],v[16:19],v[80:83],a[220:223]\n"
"v_exp_f32 v122,v122\n"
MF "a[188:191],v[20:23],v[80:83],a[220:223]\n"
"v_exp_f32 v123,v123\n"
MF "a[192:195],v[20:23],v[84:87],a[220:223]\n"
"v_add_f32 v59,v59,v120\n"
"v_add_f32 v59,v59,v121\n"
MF "a[172:175],v[16:19],v[84:87],a[220:223]\n"
"s_waitcnt vmcnt(6) lgkmcnt(0)\n"
CV "v102,v120,v121\n"
"v_add_f32 v59,v59,v122\n"
MF "a[176:179],v[16:19],v[88:91],a[220:223]\n"
"s_barrier\n"
MF "a[196:199],v[20:23],v[88:91],a[220:223]\n"
"s_add_u32 s72,s72,6144\n"
"ds_read_b128 v[16:19],v0\n"
"ds_read_b128 v[20:23],v1\n"
MF "a[216:219],v[24:27],v[88:91],a[220:223]\n"
"s_cmp_eq_u32 s72,s17\n"
"s_cselect_b32 s72,s16,s72\n"
"s_add_u32 m0,s64,49152\n"
MF "a[236:239],v[28:31],v[88:91],a[220:223]\n"
LD "v7,s[32:35],s72 offen nt lds\n"
MF "a[232:235],v[28:31],v[84:87],a[220:223]\n"
"s_add_u32 m0,s64,50048\n"
"v_add_f32 v59,v59,v123\n"
CV "v103,v122,v123\n"
MF "a[212:215],v[24:27],v[84:87],a[220:223]\n"
LD "v7,s[32:35],s72 offen offset:128 nt lds\n"
MF "a[208:211],v[24:27],v[80:83],a[220:223]\n"
"s_add_u32 m0,s64,50944\n"
MF "a[228:231],v[28:31],v[80:83],a[220:223]\n"
LD "v7,s[32:35],s72 offen offset:256 nt lds\n"
MF "a[224:227],v[28:31],v[76:79],a[220:223]\n"
MF "a[204:207],v[24:27],v[76:79],a[220:223]\n"
MF "a[200:203],v[24:27],v[72:75],a[220:223]\n"
MF "a[220:223],v[28:31],v[72:75],a[220:223]\n"
"s_waitcnt lgkmcnt(0)\n"
"s_branch .Lmid\n"
".Laloop:\n"
MF "v[72:75],v[16:19],v[136:139],v[32:35]\n"
"s_nop 0\n"
"ds_read_b128 v[24:27],v0 offset:25600\n"
"ds_read_b128 v[28:31],v1 offset:25600\n"
MF "v[72:75],v[20:23],v[156:159],v[72:75]\n"
MF "v[76:79],v[20:23],v[160:163],v[36:39]\n"
MF "v[76:79],v[16:19],v[140:143],v[76:79]\n"
MF "v[80:83],v[16:19],v[144:147],v[40:43]\n"
MF "v[80:83],v[20:23],v[164:167],v[80:83]\n"
MF "v[84:87],v[20:23],v[168:171],v[44:47]\n"
MF "v[84:87],v[16:19],v[148:151],v[84:87]\n"
"s_add_u32 s72,s72,6144\n"
"s_add_u32 m0,s64,6144\n"
MF "v[88:91],v[16:19],v[152:155],v[48:51]\n"
LD "v7,s[32:35],s72 offen nt lds\n"
MF "v[88:91],v[20:23],v[172:175],v[88:91]\n"
"s_waitcnt lgkmcnt(0)\n"
"ds_read_b128 v[16:19],v0 offset:26624\n"
"ds_read_b128 v[20:23],v1 offset:26624\n"
MF "v[88:91],v[24:27],v[192:195],v[88:91]\n"
"v_exp_f32 v124,v124\n"
MF "v[88:91],v[28:31],v[212:215],v[88:91]\n"
"s_add_u32 m0,s64,7040\n"
"v_exp_f32 v125,v125\n"
MF "v[84:87],v[28:31],v[208:211],v[84:87]\n"
LD "v7,s[32:35],s72 offen offset:128 nt lds\n"
MF "v[84:87],v[24:27],v[188:191],v[84:87]\n"
"v_exp_f32 v126,v126\n"
MF "v[80:83],v[24:27],v[184:187],v[80:83]\n"
"v_exp_f32 v127,v127\n"
MF "v[80:83],v[28:31],v[204:207],v[80:83]\n"
"s_add_u32 m0,s64,7936\n"
"v_add_f32 v60,v60,v124\n"
"v_add_f32 v60,v60,v125\n"
MF "v[76:79],v[28:31],v[200:203],v[76:79]\n"
LD "v7,s[32:35],s72 offen offset:256 nt lds\n"
MF "v[76:79],v[24:27],v[180:183],v[76:79]\n"
CV "v106,v124,v125\n"
"v_add_f32 v60,v60,v126\n"
MF "v[72:75],v[24:27],v[176:179],v[72:75]\n"
"v_add_f32 v60,v60,v127\n"
CV "v107,v126,v127\n"
MF "v[72:75],v[28:31],v[196:199],v[72:75]\n"
"s_waitcnt lgkmcnt(0)\n"
"ds_read_b128 v[24:27],v0 offset:30720\n"
"ds_read_b128 v[28:31],v1 offset:30720\n"
MF "v[72:75],v[16:19],v[216:219],v[72:75]\n"
"v_exp_f32 v128,v128\n"
MF "v[72:75],v[20:23],v[236:239],v[72:75]\n"
"v_exp_f32 v129,v129\n"
MF "v[76:79],v[20:23],v[240:243],v[76:79]\n"
"v_exp_f32 v130,v130\n"
MF "v[76:79],v[16:19],v[220:223],v[76:79]\n"
"v_exp_f32 v131,v131\n"
MF "v[80:83],v[16:19],v[224:227],v[80:83]\n"
"v_add_f32 v61,v61,v128\n"
"v_add_f32 v61,v61,v129\n"
MF "v[80:83],v[20:23],v[244:247],v[80:83]\n"
CV "v110,v128,v129\n"
"v_add_f32 v61,v61,v130\n"
MF "v[84:87],v[20:23],v[248:251],v[84:87]\n"
"v_add_f32 v61,v61,v131\n"
CV "v111,v130,v131\n"
MF "v[84:87],v[16:19],v[228:231],v[84:87]\n"
"v_max3_f32 v10,v57,v58,v59\n"
"v_max3_f32 v10,v10,v60,v61\n"
MF "v[88:91],v[16:19],v[232:235],v[88:91]\n"
"v_cmp_ngt_f32 vcc,s77,v10\n"
MF "v[88:91],v[20:23],v[252:255],v[88:91]\n"
"s_waitcnt lgkmcnt(0)\n"
"s_cbranch_vccnz .Lstub_11\n"
".Lback_11:\n"
MF "v[112:115],v[24:27],v[136:139],v[32:35]\n"
"s_add_u32 s74,s74,12288\n"
"ds_read_b128 v[16:19],v0 offset:31744\n"
"ds_read_b128 v[20:23],v1 offset:31744\n"
MF "v[112:115],v[28:31],v[156:159],v[112:115]\n"
"s_cmp_eq_u32 s74,s20\n"
"s_cselect_b32 s74,s70,s74\n"
MF "v[116:119],v[28:31],v[160:163],v[36:39]\n"
"v_exp_f32 v72,v72\n"
MF "v[116:119],v[24:27],v[140:143],v[116:119]\n"
"v_exp_f32 v73,v73\n"
MF "v[120:123],v[24:27],v[144:147],v[40:43]\n"
"v_exp_f32 v74,v74\n"
MF "v[120:123],v[28:31],v[164:167],v[120:123]\n"
"v_exp_f32 v75,v75\n"
MF "v[124:127],v[28:31],v[168:171],v[44:47]\n"
"v_add_f32 v52,v57,v72\n"
"v_add_f32 v52,v52,v73\n"
MF "v[124:127],v[24:27],v[148:151],v[124:127]\n"
CV "v72,v72,v73\n"
"v_add_f32 v52,v52,v74\n"
MF "v[128:131],v[24:27],v[152:155],v[48:51]\n"
"v_add_f32 v52,v52,v75\n"
CV "v73,v74,v75\n"
MF "v[128:131],v[28:31],v[172:175],v[128:131]\n"
"s_waitcnt lgkmcnt(0)\n"
"ds_read_b128 v[24:27],v0 offset:32768\n"
"ds_read_b128 v[28:31],v1 offset:32768\n"
MF "v[128:131],v[16:19],v[192:195],v[128:131]\n"
"v_exp_f32 v76,v76\n"
MF "v[128:131],v[20:23],v[212:215],v[128:131]\n"
"v_exp_f32 v77,v77\n"
MF "v[124:127],v[20:23],v[208:211],v[124:127]\n"
"v_exp_f32 v78,v78\n"
MF "v[124:127],v[16:19],v[188:191],v[124:127]\n"
"v_exp_f32 v79,v79\n"
MF "v[120:123],v[16:19],v[184:187],v[120:123]\n"
"v_add_f32 v53,v58,v76\n"
"v_add_f32 v53,v53,v77\n"
MF "v[120:123],v[20:23],v[204:207],v[120:123]\n"
CV "v76,v76,v77\n"
"v_add_f32 v53,v53,v78\n"
MF "v[116:119],v[20:23],v[200:203],v[116:119]\n"
"s_waitcnt vmcnt(6) lgkmcnt(0)\n"
"v_add_f32 v53,v53,v79\n"
CV "v77,v78,v79\n"
MF "v[116:119],v[16:19],v[180:183],v[116:119]\n"
"s_barrier\n"
MF "v[112:115],v[16:19],v[176:179],v[112:115]\n"
"v_exp_f32 v80,v80\n"
MF "v[112:115],v[20:23],v[196:199],v[112:115]\n"
"s_nop 0\n"
TR "v[16:17],v2 offset:49152\n"
TR "v[18:19],v2 offset:55296\n"
MF "v[112:115],v[24:27],v[216:219],v[112:115]\n"
"s_add_u32 s73,s73,12288\n"
TR "v[20:21],v3 offset:49152\n"
TR "v[22:23],v3 offset:55296\n"
MF "v[112:115],v[28:31],v[236:239],v[112:115]\n"
"s_cmp_eq_u32 s73,s19\n"
"s_cselect_b32 s73,s18,s73\n"
"s_add_u32 m0,s65,24576\n"
MF "v[116:119],v[28:31],v[240:243],v[116:119]\n"
LD "v8,s[36:39],s73 offen nt lds\n"
MF "v[116:119],v[24:27],v[220:223],v[116:119]\n"
"s_add_u32 m0,s65,25472\n"
"v_exp_f32 v81,v81\n"
MF "v[120:123],v[24:27],v[224:227],v[120:123]\n"
LD "v8,s[36:39],s73 offen offset:128 nt lds\n"
MF "v[120:123],v[28:31],v[244:247],v[120:123]\n"
"v_exp_f32 v82,v82\n"
MF "v[124:127],v[28:31],v[248:251],v[124:127]\n"
"s_add_u32 m0,s65,26368\n"
"v_exp_f32 v83,v83\n"
MF "v[124:127],v[24:27],v[228:231],v[124:127]\n"
LD "v8,s[36:39],s73 offen offset:256 nt lds\n"
MF "v[128:131],v[24:27],v[232:235],v[128:131]\n"
"v_add_f32 v54,v59,v80\n"
"v_add_f32 v54,v54,v81\n"
MF "v[128:131],v[28:31],v[252:255],v[128:131]\n"
"s_waitcnt lgkmcnt(0)\n"
TR "v[24:25],v4 offset:49152\n"
TR "v[26:27],v4 offset:55296\n"
MF "a[0:3],v[16:19],v[92:95],a[0:3]\n"
"s_add_u32 m0,s65,24576\n"
TR "v[28:29],v5 offset:49152\n"
TR "v[30:31],v5 offset:55296\n"
MF "a[20:23],v[20:23],v[92:95],a[20:23]\n"
LD "v8,s[36:39],s73 offen offset:3072 nt lds\n"
MF "a[24:27],v[20:23],v[96:99],a[24:27]\n"
CV "v80,v80,v81\n"
"v_add_f32 v54,v54,v82\n"
MF "a[4:7],v[16:19],v[96:99],a[4:7]\n"
"s_add_u32 m0,s65,25472\n"
"v_add_f32 v54,v54,v83\n"
CV "v81,v82,v83\n"
MF "a[8:11],v[16:19],v[100:103],a[8:11]\n"
LD "v8,s[36:39],s73 offen offset:3200 nt lds\n"
MF "a[28:31],v[20:23],v[100:103],a[28:31]\n"
"v_exp_f32 v84,v84\n"
MF "a[32:35],v[20:23],v[104:107],a[32:35]\n"
"s_add_u32 m0,s65,26368\n"
"v_exp_f32 v85,v85\n"
MF "a[12:15],v[16:19],v[104:107],a[12:15]\n"
LD "v8,s[36:39],s73 offen offset:3328 nt lds\n"
MF "a[16:19],v[16:19],v[108:111],a[16:19]\n"
"v_exp_f32 v86,v86\n"
MF "a[36:39],v[20:23],v[108:111],a[36:39]\n"
"s_waitcnt lgkmcnt(0)\n"
TR "v[16:17],v2 offset:50176\n"
TR "v[18:19],v2 offset:56320\n"
MF "a[56:59],v[24:27],v[108:111],a[56:59]\n"
"s_nop 0\n"
TR "v[20:21],v3 offset:50176\n"
TR "v[22:23],v3 offset:56320\n"
MF "a[76:79],v[28:31],v[108:111],a[76:79]\n"
"v_exp_f32 v87,v87\n"
MF "a[72:75],v[28:31],v[104:107],a[72:75]\n"
"v_add_f32 v55,v60,v84\n"
"v_add_f32 v55,v55,v85\n"
MF "a[52:55],v[24:27],v[104:107],a[52:55]\n"
CV "v84,v84,v85\n"
"v_add_f32 v55,v55,v86\n"
MF "a[48:51],v[24:27],v[100:103],a[48:51]\n"
"v_add_f32 v55,v55,v87\n"
CV "v85,v86,v87\n"
MF "a[68:71],v[28:31],v[100:103],a[68:71]\n"
"v_exp_f32 v88,v88\n"
MF "a[64:67],v[28:31],v[96:99],a[64:67]\n"
"v_exp_f32 v89,v89\n"
MF "a[44:47],v[24:27],v[96:99],a[44:47]\n"
"v_exp_f32 v90,v90\n"
MF "a[40:43],v[24:27],v[92:95],a[40:43]\n"
"v_exp_f32 v91,v91\n"
MF "a[60:63],v[28:31],v[92:95],a[60:63]\n"
"s_waitcnt lgkmcnt(0)\n"
TR "v[24:25],v4 offset:50176\n"
TR "v[26:27],v4 offset:56320\n"
MF "a[80:83],v[16:19],v[92:95],a[80:83]\n"
"s_nop 0\n"
TR "v[28:29],v5 offset:50176\n"
TR "v[30:31],v5 offset:56320\n"
MF "a[100:103],v[20:23],v[92:95],a[100:103]\n"
"v_add_f32 v56,v61,v88\n"
"v_add_f32 v56,v56,v89\n"
MF "a[104:107],v[20:23],v[96:99],a[104:107]\n"
CV "v88,v88,v89\n"
"v_add_f32 v56,v56,v90\n"
MF "a[84:87],v[16:19],v[96:99],a[84:87]\n"
"v_add_f32 v56,v56,v91\n"
CV "v89,v90,v91\n"
MF "a[88:91],v[16:19],v[100:103],a[88:91]\n"
"v_exp_f32 v112,v112\n"
MF "a[108:111],v[20:23],v[100:103],a[108:111]\n"
"v_exp_f32 v113,v113\n"
MF "a[112:115],v[20:23],v[104:107],a[112:115]\n"
"v_exp_f32 v114,v114\n"
MF "a[92:95],v[16:19],v[104:107],a[92:95]\n"
"v_exp_f32 v115,v115\n"
MF "a[96:99],v[16:19],v[108:111],a[96:99]\n"
"v_add_f32 v52,v52,v112\n"
"v_add_f32 v52,v52,v113\n"
MF "a[116:119],v[20:23],v[108:111],a[116:119]\n"
"s_waitcnt lgkmcnt(0)\n"
TR "v[16:17],v2 offset:51200\n"
TR "v[18:19],v2 offset:57344\n"
MF "a[136:139],v[24:27],v[108:111],a[136:139]\n"
"s_nop 0\n"
TR "v[20:21],v3 offset:51200\n"
TR "v[22:23],v3 offset:57344\n"
MF "a[156:159],v[28:31],v[108:111],a[156:159]\n"
CV "v74,v112,v113\n"
"v_add_f32 v52,v52,v114\n"
MF "a[152:155],v[28:31],v[104:107],a[152:155]\n"
"v_add_f32 v52,v52,v115\n"
CV "v75,v114,v115\n"
MF "a[132:135],v[24:27],v[104:107],a[132:135]\n"
"v_exp_f32 v116,v116\n"
MF "a[128:131],v[24:27],v[100:103],a[128:131]\n"
"v_exp_f32 v117,v117\n"
MF "a[148:151],v[28:31],v[100:103],a[148:151]\n"
"v_exp_f32 v118,v118\n"
MF "a[144:147],v[28:31],v[96:99],a[144:147]\n"
"v_exp_f32 v119,v119\n"
MF "a[124:127],v[24:27],v[96:99],a[124:127]\n"
"v_add_f32 v53,v53,v116\n"
"v_add_f32 v53,v53,v117\n"
MF "a[120:123],v[24:27],v[92:95],a[120:123]\n"
CV "v78,v116,v117\n"
"v_add_f32 v53,v53,v118\n"
MF "a[140:143],v[28:31],v[92:95],a[140:143]\n"
"s_waitcnt lgkmcnt(0)\n"
TR "v[24:25],v4 offset:51200\n"
TR "v[26:27],v4 offset:57344\n"
MF "a[160:163],v[16:19],v[92:95],a[160:163]\n"
"s_nop 0\n"
TR "v[28:29],v5 offset:51200\n"
TR "v[30:31],v5 offset:57344\n"
MF "a[180:183],v[20:23],v[92:95],a[180:183]\n"
"v_add_f32 v53,v53,v119\n"
CV "v79,v118,v119\n"
MF "a[184:187],v[20:23],v[96:99],a[184:187]\n"
"v_exp_f32 v120,v120\n"
MF "a[164:167],v[16:19],v[96:99],a[164:167]\n"
"v_exp_f32 v121,v121\n"
MF "a[168:171],v[16:19],v[100:103],a[168:171]\n"
"v_exp_f32 v122,v122\n"
MF "a[188:191],v[20:23],v[100:103],a[188:191]\n"
"v_exp_f32 v123,v123\n"
MF "a[192:195],v[20:23],v[104:107],a[192:195]\n"
"v_add_f32 v54,v54,v120\n"
"v_add_f32 v54,v54,v121\n"
MF "a[172:175],v[16:19],v[104:107],a[172:175]\n"
"s_waitcnt vmcnt(6) lgkmcnt(0)\n"
CV "v82,v120,v121\n"
"v_add_f32 v54,v54,v122\n"
MF "a[176:179],v[16:19],v[108:111],a[176:179]\n"
"s_barrier\n"
MF "a[196:199],v[20:23],v[108:111],a[196:199]\n"
"s_add_u32 s72,s72,6144\n"
"ds_read_b128 v[16:19],v0\n"
"ds_read_b128 v[20:23],v1\n"
MF "a[216:219],v[24:27],v[108:111],a[216:219]\n"
"s_cmp_eq_u32 s72,s17\n"
"s_cselect_b32 s72,s16,s72\n"
"s_add_u32 m0,s64,49152\n"
MF "a[236:239],v[28:31],v[108:111],a[236:239]\n"
LD "v7,s[32:35],s72 offen nt lds\n"
MF "a[232:235],v[28:31],v[104:107],a[232:235]\n"
"s_add_u32 m0,s64,50048\n"
"v_add_f32 v54,v54,v123\n"
CV "v83,v122,v123\n"
MF "a[212:215],v[24:27],v[104:107],a[212:215]\n"
LD "v7,s[32:35],s72 offen offset:128 nt lds\n"
MF "a[208:211],v[24:27],v[100:103],a[208:211]\n"
"s_add_u32 m0,s64,50944\n"
MF "a[228:231],v[28:31],v[100:103],a[228:231]\n"
LD "v7,s[32:35],s72 offen offset:256 nt lds\n"
MF "a[224:227],v[28:31],v[96:99],a[224:227]\n"
MF "a[204:207],v[24:27],v[96:99],a[204:207]\n"
MF "a[200:203],v[24:27],v[92:95],a[200:203]\n"
MF "a[220:223],v[28:31],v[92:95],a[220:223]\n"
"s_waitcnt lgkmcnt(0)\n"
MF "v[92:95],v[16:19],v[136:139],v[32:35]\n"
"s_add_u32 s72,s72,6144\n"
"ds_read_b128 v[24:27],v0 offset:1024\n"
"ds_read_b128 v[28:31],v1 offset:1024\n"
MF "v[92:95],v[20:23],v[156:159],v[92:95]\n"
"s_add_u32 m0,s64,55296\n"
"v_exp_f32 v124,v124\n"
MF "v[96:99],v[20:23],v[160:163],v[36:39]\n"
LD "v7,s[32:35],s72 offen nt lds\n"
MF "v[96:99],v[16:19],v[140:143],v[96:99]\n"
"v_exp_f32 v125,v125\n"
MF "v[100:103],v[16:19],v[144:147],v[40:43]\n"
"s_add_u32 m0,s64,56192\n"
"v_exp_f32 v126,v126\n"
MF "v[100:103],v[20:23],v[164:167],v[100:103]\n"
LD "v7,s[32:35],s72 offen offset:128 nt lds\n"
MF "v[104:107],v[20:23],v[168:171],v[44:47]\n"
"v_exp_f32 v127,v127\n"
MF "v[104:107],v[16:19],v[148:151],v[104:107]\n"
"s_add_u32 m0,s64,57088\n"
"v_add_f32 v55,v55,v124\n"
"v_add_f32 v55,v55,v125\n"
MF "v[108:111],v[16:19],v[152:155],v[48:51]\n"
LD "v7,s[32:35],s72 offen offset:256 nt lds\n"
MF "v[108:111],v[20:23],v[172:175],v[108:111]\n"
"s_waitcnt lgkmcnt(0)\n"
"ds_read_b128 v[16:19],v0 offset:2048\n"
"ds_read_b128 v[20:23],v1 offset:2048\n"
MF "v[108:111],v[24:27],v[192:195],v[108:111]\n"
CV "v86,v124,v125\n"
"v_add_f32 v55,v55,v126\n"
MF "v[108:111],v[28:31],v[212:215],v[108:111]\n"
"v_add_f32 v55,v55,v127\n"
CV "v87,v126,v127\n"
MF "v[104:107],v[28:31],v[208:211],v[104:107]\n"
"v_exp_f32 v128,v128\n"
MF "v[104:107],v[24:27],v[188:191],v[104:107]\n"
"v_exp_f32 v129,v129\n"
MF "v[100:103],v[24:27],v[184:187],v[100:103]\n"
"v_exp_f32 v130,v130\n"
MF "v[100:103],v[28:31],v[204:207],v[100:103]\n"
"v_exp_f32 v131,v131\n"
MF "v[96:99],v[28:31],v[200:203],v[96:99]\n"
"v_add_f32 v56,v56,v128\n"
"v_add_f32 v56,v56,v129\n"
MF "v[96:99],v[24:27],v[180:183],v[96:99]\n"
CV "v90,v128,v129\n"
"v_add_f32 v56,v56,v130\n"
MF "v[92:95],v[24:27],v[176:179],v[92:95]\n"
"v_add_f32 v56,v56,v131\n"
CV "v91,v130,v131\n"
MF "v[92:95],v[28:31],v[196:199],v[92:95]\n"
"s_waitcnt lgkmcnt(0)\n"
"ds_read_b128 v[24:27],v0 offset:6144\n"
"ds_read_b128 v[28:31],v1 offset:6144\n"
MF "v[92:95],v[16:19],v[216:219],v[92:95]\n"
MF "v[92:95],v[20:23],v[236:239],v[92:95]\n"
MF "v[96:99],v[20:23],v[240:243],v[96:99]\n"
MF "v[96:99],v[16:19],v[220:223],v[96:99]\n"
MF "v[100:103],v[16:19],v[224:227],v[100:103]\n"
MF "v[100:103],v[20:23],v[244:247],v[100:103]\n"
MF "v[104:107],v[20:23],v[248:251],v[104:107]\n"
MF "v[104:107],v[16:19],v[228:231],v[104:107]\n"
"v_max3_f32 v10,v52,v53,v54\n"
"v_max3_f32 v10,v10,v55,v56\n"
MF "v[108:111],v[16:19],v[232:235],v[108:111]\n"
"v_cmp_ngt_f32 vcc,s77,v10\n"
MF "v[108:111],v[20:23],v[252:255],v[108:111]\n"
"s_waitcnt lgkmcnt(0)\n"
"s_cbranch_vccnz .Lstub_12\n"
".Lback_12:\n"
MF "v[112:115],v[24:27],v[136:139],v[32:35]\n"
"s_add_u32 s74,s74,12288\n"
"ds_read_b128 v[16:19],v0 offset:7168\n"
"ds_read_b128 v[20:23],v1 offset:7168\n"
MF "v[112:115],v[28:31],v[156:159],v[112:115]\n"
"s_cmp_eq_u32 s74,s20\n"
"s_cselect_b32 s74,s70,s74\n"
MF "v[116:119],v[28:31],v[160:163],v[36:39]\n"
"v_exp_f32 v92,v92\n"
MF "v[116:119],v[24:27],v[140:143],v[116:119]\n"
"v_exp_f32 v93,v93\n"
MF "v[120:123],v[24:27],v[144:147],v[40:43]\n"
"v_exp_f32 v94,v94\n"
MF "v[120:123],v[28:31],v[164:167],v[120:123]\n"
"v_exp_f32 v95,v95\n"
MF "v[124:127],v[28:31],v[168:171],v[44:47]\n"
"v_add_f32 v57,v52,v92\n"
"v_add_f32 v57,v57,v93\n"
MF "v[124:127],v[24:27],v[148:151],v[124:127]\n"
CV "v92,v92,v93\n"
"v_add_f32 v57,v57,v94\n"
MF "v[128:131],v[24:27],v[152:155],v[48:51]\n"
"v_add_f32 v57,v57,v95\n"
CV "v93,v94,v95\n"
MF "v[128:131],v[28:31],v[172:175],v[128:131]\n"
"s_waitcnt lgkmcnt(0)\n"
"ds_read_b128 v[24:27],v0 offset:8192\n"
"ds_read_b128 v[28:31],v1 offset:8192\n"
MF "v[128:131],v[16:19],v[192:195],v[128:131]\n"
"v_exp_f32 v96,v96\n"
MF "v[128:131],v[20:23],v[212:215],v[128:131]\n"
"v_exp_f32 v97,v97\n"
MF "v[124:127],v[20:23],v[208:211],v[124:127]\n"
"v_exp_f32 v98,v98\n"
MF "v[124:127],v[16:19],v[188:191],v[124:127]\n"
"v_exp_f32 v99,v99\n"
MF "v[120:123],v[16:19],v[184:187],v[120:123]\n"
"v_add_f32 v58,v53,v96\n"
"v_add_f32 v58,v58,v97\n"
MF "v[120:123],v[20:23],v[204:207],v[120:123]\n"
CV "v96,v96,v97\n"
"v_add_f32 v58,v58,v98\n"
MF "v[116:119],v[20:23],v[200:203],v[116:119]\n"
"s_waitcnt vmcnt(6) lgkmcnt(0)\n"
"v_add_f32 v58,v58,v99\n"
CV "v97,v98,v99\n"
MF "v[116:119],v[16:19],v[180:183],v[116:119]\n"
"s_barrier\n"
MF "v[112:115],v[16:19],v[176:179],v[112:115]\n"
"v_exp_f32 v100,v100\n"
MF "v[112:115],v[20:23],v[196:199],v[112:115]\n"
"s_nop 0\n"
TR "v[16:17],v2 offset:24576\n"
TR "v[18:19],v2 offset:30720\n"
MF "v[112:115],v[24:27],v[216:219],v[112:115]\n"
"s_add_u32 s73,s73,12288\n"
TR "v[20:21],v3 offset:24576\n"
TR "v[22:23],v3 offset:30720\n"
MF "v[112:115],v[28:31],v[236:239],v[112:115]\n"
"s_cmp_eq_u32 s73,s19\n"
"s_cselect_b32 s73,s18,s73\n"
"s_add_u32 m0,s65,0\n"
MF "v[116:119],v[28:31],v[240:243],v[116:119]\n"
LD "v8,s[36:39],s73 offen nt lds\n"
MF "v[116:119],v[24:27],v[220:223],v[116:119]\n"
"s_add_u32 m0,s65,896\n"
"v_exp_f32 v101,v101\n"
MF "v[120:123],v[24:27],v[224:227],v[120:123]\n"
LD "v8,s[36:39],s73 offen offset:128 nt lds\n"
MF "v[120:123],v[28:31],v[244:247],v[120:123]\n"
"v_exp_f32 v102,v102\n"
MF "v[124:127],v[28:31],v[248:251],v[124:127]\n"
"s_add_u32 m0,s65,1792\n"
"v_exp_f32 v103,v103\n"
MF "v[124:127],v[24:27],v[228:231],v[124:127]\n"
LD "v8,s[36:39],s73 offen offset:256 nt lds\n"
MF "v[128:131],v[24:27],v[232:235],v[128:131]\n"
"v_add_f32 v59,v54,v100\n"
"v_add_f32 v59,v59,v101\n"
MF "v[128:131],v[28:31],v[252:255],v[128:131]\n"
"s_waitcnt lgkmcnt(0)\n"
TR "v[24:25],v4 offset:24576\n"
TR "v[26:27],v4 offset:30720\n"
MF "a[0:3],v[16:19],v[72:75],a[0:3]\n"
"s_add_u32 m0,s65,0\n"
TR "v[28:29],v5 offset:24576\n"
TR "v[30:31],v5 offset:30720\n"
MF "a[20:23],v[20:23],v[72:75],a[20:23]\n"
LD "v8,s[36:39],s73 offen offset:3072 nt lds\n"
MF "a[24:27],v[20:23],v[76:79],a[24:27]\n"
CV "v100,v100,v101\n"
"v_add_f32 v59,v59,v102\n"
MF "a[4:7],v[16:19],v[76:79],a[4:7]\n"
"s_add_u32 m0,s65,896\n"
"v_add_f32 v59,v59,v103\n"
CV "v101,v102,v103\n"
MF "a[8:11],v[16:19],v[80:83],a[8:11]\n"
LD "v8,s[36:39],s73 offen offset:3200 nt lds\n"
MF "a[28:31],v[20:23],v[80:83],a[28:31]\n"
"v_exp_f32 v104,v104\n"
MF "a[32:35],v[20:23],v[84:87],a[32:35]\n"
"s_add_u32 m0,s65,1792\n"
"v_exp_f32 v105,v105\n"
MF "a[12:15],v[16:19],v[84:87],a[12:15]\n"
LD "v8,s[36:39],s73 offen offset:3328 nt lds\n"
MF "a[16:19],v[16:19],v[88:91],a[16:19]\n"
"v_exp_f32 v106,v106\n"
MF "a[36:39],v[20:23],v[88:91],a[36:39]\n"
"s_waitcnt lgkmcnt(0)\n"
TR "v[16:17],v2 offset:25600\n"
TR "v[18:19],v2 offset:31744\n"
MF "a[56:59],v[24:27],v[88:91],a[56:59]\n"
"s_nop 0\n"
TR "v[20:21],v3 offset:25600\n"
TR "v[22:23],v3 offset:31744\n"
MF "a[76:79],v[28:31],v[88:91],a[76:79]\n"
"v_exp_f32 v107,v107\n"
MF "a[72:75],v[28:31],v[84:87],a[72:75]\n"
"v_add_f32 v60,v55,v104\n"
"v_add_f32 v60,v60,v105\n"
MF "a[52:55],v[24:27],v[84:87],a[52:55]\n"
CV "v104,v104,v105\n"
"v_add_f32 v60,v60,v106\n"
MF "a[48:51],v[24:27],v[80:83],a[48:51]\n"
"v_add_f32 v60,v60,v107\n"
CV "v105,v106,v107\n"
MF "a[68:71],v[28:31],v[80:83],a[68:71]\n"
"v_exp_f32 v108,v108\n"
MF "a[64:67],v[28:31],v[76:79],a[64:67]\n"
"v_exp_f32 v109,v109\n"
MF "a[44:47],v[24:27],v[76:79],a[44:47]\n"
"v_exp_f32 v110,v110\n"
MF "a[40:43],v[24:27],v[72:75],a[40:43]\n"
"v_exp_f32 v111,v111\n"
MF "a[60:63],v[28:31],v[72:75],a[60:63]\n"
"s_waitcnt lgkmcnt(0)\n"
TR "v[24:25],v4 offset:25600\n"
TR "v[26:27],v4 offset:31744\n"
MF "a[80:83],v[16:19],v[72:75],a[80:83]\n"
"s_nop 0\n"
TR "v[28:29],v5 offset:25600\n"
TR "v[30:31],v5 offset:31744\n"
MF "a[100:103],v[20:23],v[72:75],a[100:103]\n"
"v_add_f32 v61,v56,v108\n"
"v_add_f32 v61,v61,v109\n"
MF "a[104:107],v[20:23],v[76:79],a[104:107]\n"
CV "v108,v108,v109\n"
"v_add_f32 v61,v61,v110\n"
MF "a[84:87],v[16:19],v[76:79],a[84:87]\n"
"v_add_f32 v61,v61,v111\n"
CV "v109,v110,v111\n"
MF "a[88:91],v[16:19],v[80:83],a[88:91]\n"
"v_exp_f32 v112,v112\n"
MF "a[108:111],v[20:23],v[80:83],a[108:111]\n"
"v_exp_f32 v113,v113\n"
MF "a[112:115],v[20:23],v[84:87],a[112:115]\n"
"v_exp_f32 v114,v114\n"
MF "a[92:95],v[16:19],v[84:87],a[92:95]\n"
"v_exp_f32 v115,v115\n"
MF "a[96:99],v[16:19],v[88:91],a[96:99]\n"
"v_add_f32 v57,v57,v112\n"
"v_add_f32 v57,v57,v113\n"
MF "a[116:119],v[20:23],v[88:91],a[116:119]\n"
"s_waitcnt lgkmcnt(0)\n"
TR "v[16:17],v2 offset:26624\n"
TR "v[18:19],v2 offset:32768\n"
MF "a[136:139],v[24:27],v[88:91],a[136:139]\n"
"s_nop 0\n"
TR "v[20:21],v3 offset:26624\n"
TR "v[22:23],v3 offset:32768\n"
MF "a[156:159],v[28:31],v[88:91],a[156:159]\n"
CV "v94,v112,v113\n"
"v_add_f32 v57,v57,v114\n"
MF "a[152:155],v[28:31],v[84:87],a[152:155]\n"
"v_add_f32 v57,v57,v115\n"
CV "v95,v114,v115\n"
MF "a[132:135],v[24:27],v[84:87],a[132:135]\n"
"v_exp_f32 v116,v116\n"
MF "a[128:131],v[24:27],v[80:83],a[128:131]\n"
"v_exp_f32 v117,v117\n"
MF "a[148:151],v[28:31],v[80:83],a[148:151]\n"
"v_exp_f32 v118,v118\n"
MF "a[144:147],v[28:31],v[76:79],a[144:147]\n"
"v_exp_f32 v119,v119\n"
MF "a[124:127],v[24:27],v[76:79],a[124:127]\n"
"v_add_f32 v58,v58,v116\n"
"v_add_f32 v58,v58,v117\n"
MF "a[120:123],v[24:27],v[72:75],a[120:123]\n"
CV "v98,v116,v117\n"
"v_add_f32 v58,v58,v118\n"
MF "a[140:143],v[28:31],v[72:75],a[140:143]\n"
"s_waitcnt lgkmcnt(0)\n"
TR "v[24:25],v4 offset:26624\n"
TR "v[26:27],v4 offset:32768\n"
MF "a[160:163],v[16:19],v[72:75],a[160:163]\n"
"s_nop 0\n"
TR "v[28:29],v5 offset:26624\n"
TR "v[30:31],v5 offset:32768\n"
MF "a[180:183],v[20:23],v[72:75],a[180:183]\n"
"v_add_f32 v58,v58,v119\n"
CV "v99,v118,v119\n"
MF "a[184:187],v[20:23],v[76:79],a[184:187]\n"
"v_exp_f32 v120,v120\n"
MF "a[164:167],v[16:19],v[76:79],a[164:167]\n"
"v_exp_f32 v121,v121\n"
MF "a[168:171],v[16:19],v[80:83],a[168:171]\n"
"v_exp_f32 v122,v122\n"
MF "a[188:191],v[20:23],v[80:83],a[188:191]\n"
"v_exp_f32 v123,v123\n"
MF "a[192:195],v[20:23],v[84:87],a[192:195]\n"
"v_add_f32 v59,v59,v120\n"
"v_add_f32 v59,v59,v121\n"
MF "a[172:175],v[16:19],v[84:87],a[172:175]\n"
"s_waitcnt vmcnt(6) lgkmcnt(0)\n"
CV "v102,v120,v121\n"
"v_add_f32 v59,v59,v122\n"
MF "a[176:179],v[16:19],v[88:91],a[176:179]\n"
"s_barrier\n"
MF "a[196:199],v[20:23],v[88:91],a[196:199]\n"
"s_add_u32 s72,s72,6144\n"
"ds_read_b128 v[16:19],v0 offset:49152\n"
"ds_read_b128 v[20:23],v1 offset:49152\n"
MF "a[216:219],v[24:27],v[88:91],a[216:219]\n"
"s_cmp_eq_u32 s72,s17\n"
"s_cselect_b32 s72,s16,s72\n"
"s_add_u32 m0,s64,24576\n"
MF "a[236:239],v[28:31],v[88:91],a[236:239]\n"
LD "v7,s[32:35],s72 offen nt lds\n"
MF "a[232:235],v[28:31],v[84:87],a[232:235]\n"
"s_add_u32 m0,s64,25472\n"
"v_add_f32 v59,v59,v123\n"
CV "v103,v122,v123\n"
MF "a[212:215],v[24:27],v[84:87],a[212:215]\n"
LD "v7,s[32:35],s72 offen offset:128 nt lds\n"
MF "a[208:211],v[24:27],v[80:83],a[208:211]\n"
"s_add_u32 m0,s64,26368\n"
MF "a[228:231],v[28:31],v[80:83],a[228:231]\n"
LD "v7,s[32:35],s72 offen offset:256 nt lds\n"
MF "a[224:227],v[28:31],v[76:79],a[224:227]\n"
MF "a[204:207],v[24:27],v[76:79],a[204:207]\n"
MF "a[200:203],v[24:27],v[72:75],a[200:203]\n"
MF "a[220:223],v[28:31],v[72:75],a[220:223]\n"
"s_waitcnt lgkmcnt(0)\n"
MF "v[72:75],v[16:19],v[136:139],v[32:35]\n"
"s_add_u32 s72,s72,6144\n"
"ds_read_b128 v[24:27],v0 offset:50176\n"
"ds_read_b128 v[28:31],v1 offset:50176\n"
MF "v[72:75],v[20:23],v[156:159],v[72:75]\n"
"s_add_u32 m0,s64,30720\n"
"v_exp_f32 v124,v124\n"
MF "v[76:79],v[20:23],v[160:163],v[36:39]\n"
LD "v7,s[32:35],s72 offen nt lds\n"
MF "v[76:79],v[16:19],v[140:143],v[76:79]\n"
"v_exp_f32 v125,v125\n"
MF "v[80:83],v[16:19],v[144:147],v[40:43]\n"
"s_add_u32 m0,s64,31616\n"
"v_exp_f32 v126,v126\n"
MF "v[80:83],v[20:23],v[164:167],v[80:83]\n"
LD "v7,s[32:35],s72 offen offset:128 nt lds\n"
MF "v[84:87],v[20:23],v[168:171],v[44:47]\n"
"v_exp_f32 v127,v127\n"
MF "v[84:87],v[16:19],v[148:151],v[84:87]\n"
"s_add_u32 m0,s64,32512\n"
"v_add_f32 v60,v60,v124\n"
"v_add_f32 v60,v60,v125\n"
MF "v[88:91],v[16:19],v[152:155],v[48:51]\n"
LD "v7,s[32:35],s72 offen offset:256 nt lds\n"
MF "v[88:91],v[20:23],v[172:175],v[88:91]\n"
"s_waitcnt lgkmcnt(0)\n"
"ds_read_b128 v[16:19],v0 offset:51200\n"
"ds_read_b128 v[20:23],v1 offset:51200\n"
MF "v[88:91],v[24:27],v[192:195],v[88:91]\n"
CV "v106,v124,v125\n"
"v_add_f32 v60,v60,v126\n"
MF "v[88:91],v[28:31],v[212:215],v[88:91]\n"
"v_add_f32 v60,v60,v127\n"
CV "v107,v126,v127\n"
MF "v[84:87],v[28:31],v[208:211],v[84:87]\n"
"v_exp_f32 v128,v128\n"
MF "v[84:87],v[24:27],v[188:191],v[84:87]\n"
"v_exp_f32 v129,v129\n"
MF "v[80:83],v[24:27],v[184:187],v[80:83]\n"
"v_exp_f32 v130,v130\n"
MF "v[80:83],v[28:31],v[204:207],v[80:83]\n"
"v_exp_f32 v131,v131\n"
MF "v[76:79],v[28:31],v[200:203],v[76:79]\n"
"v_add_f32 v61,v61,v128\n"
"v_add_f32 v61,v61,v129\n"
MF "v[76:79],v[24:27],v[180:183],v[76:79]\n"
CV "v110,v128,v129\n"
"v_add_f32 v61,v61,v130\n"
MF "v[72:75],v[24:27],v[176:179],v[72:75]\n"
"v_add_f32 v61,v61,v131\n"
CV "v111,v130,v131\n"
MF "v[72:75],v[28:31],v[196:199],v[72:75]\n"
"s_waitcnt lgkmcnt(0)\n"
"ds_read_b128 v[24:27],v0 offset:55296\n"
"ds_read_b128 v[28:31],v1 offset:55296\n"
MF "v[72:75],v[16:19],v[216:219],v[72:75]\n"
MF "v[72:75],v[20:23],v[236:239],v[72:75]\n"
MF "v[76:79],v[20:23],v[240:243],v[76:79]\n"
MF "v[76:79],v[16:19],v[220:223],v[76:79]\n"
MF "v[80:83],v[16:19],v[224:227],v[80:83]\n"
MF "v[80:83],v[20:23],v[244:247],v[80:83]\n"
MF "v[84:87],v[20:23],v[248:251],v[84:87]\n"
MF "v[84:87],v[16:19],v[228:231],v[84:87]\n"
"v_max3_f32 v10,v57,v58,v59\n"
"v_max3_f32 v10,v10,v60,v61\n"
MF "v[88:91],v[16:19],v[232:235],v[88:91]\n"
"v_cmp_ngt_f32 vcc,s95,v10\n"
MF "v[88:91],v[20:23],v[252:255],v[88:91]\n"
"s_waitcnt lgkmcnt(0)\n"
"s_cbranch_vccnz .Lstub_13\n"
".Lback_13:\n"
MF "v[112:115],v[24:27],v[136:139],v[32:35]\n"
"s_add_u32 s74,s74,12288\n"
"ds_read_b128 v[16:19],v0 offset:56320\n"
"ds_read_b128 v[20:23],v1 offset:56320\n"
MF "v[112:115],v[28:31],v[156:159],v[112:115]\n"
"s_cmp_eq_u32 s74,s20\n"
"s_cselect_b32 s74,s70,s74\n"
MF "v[116:119],v[28:31],v[160:163],v[36:39]\n"
"v_exp_f32 v72,v72\n"
MF "v[116:119],v[24:27],v[140:143],v[116:119]\n"
"v_exp_f32 v73,v73\n"
MF "v[120:123],v[24:27],v[144:147],v[40:43]\n"
"v_exp_f32 v74,v74\n"
MF "v[120:123],v[28:31],v[164:167],v[120:123]\n"
"v_exp_f32 v75,v75\n"
MF "v[124:127],v[28:31],v[168:171],v[44:47]\n"
"v_add_f32 v52,v57,v72\n"
"v_add_f32 v52,v52,v73\n"
MF "v[124:127],v[24:27],v[148:151],v[124:127]\n"
CV "v72,v72,v73\n"
"v_add_f32 v52,v52,v74\n"
MF "v[128:131],v[24:27],v[152:155],v[48:51]\n"
"v_add_f32 v52,v52,v75\n"
CV "v73,v74,v75\n"
MF "v[128:131],v[28:31],v[172:175],v[128:131]\n"
"s_waitcnt lgkmcnt(0)\n"
"ds_read_b128 v[24:27],v0 offset:57344\n"
"ds_read_b128 v[28:31],v1 offset:57344\n"
MF "v[128:131],v[16:19],v[192:195],v[128:131]\n"
"v_exp_f32 v76,v76\n"
MF "v[128:131],v[20:23],v[212:215],v[128:131]\n"
"v_exp_f32 v77,v77\n"
MF "v[124:127],v[20:23],v[208:211],v[124:127]\n"
"v_exp_f32 v78,v78\n"
MF "v[124:127],v[16:19],v[188:191],v[124:127]\n"
"v_exp_f32 v79,v79\n"
MF "v[120:123],v[16:19],v[184:187],v[120:123]\n"
"v_add_f32 v53,v58,v76\n"
"v_add_f32 v53,v53,v77\n"
MF "v[120:123],v[20:23],v[204:207],v[120:123]\n"
CV "v76,v76,v77\n"
"v_add_f32 v53,v53,v78\n"
MF "v[116:119],v[20:23],v[200:203],v[116:119]\n"
"s_waitcnt vmcnt(6) lgkmcnt(0)\n"
"v_add_f32 v53,v53,v79\n"
CV "v77,v78,v79\n"
MF "v[116:119],v[16:19],v[180:183],v[116:119]\n"
"s_barrier\n"
MF "v[112:115],v[16:19],v[176:179],v[112:115]\n"
"v_exp_f32 v80,v80\n"
MF "v[112:115],v[20:23],v[196:199],v[112:115]\n"
"s_nop 0\n"
TR "v[16:17],v2\n"
TR "v[18:19],v2 offset:6144\n"
MF "v[112:115],v[24:27],v[216:219],v[112:115]\n"
"s_add_u32 s73,s73,12288\n"
TR "v[20:21],v3\n"
TR "v[22:23],v3 offset:6144\n"
MF "v[112:115],v[28:31],v[236:239],v[112:115]\n"
"s_cmp_eq_u32 s73,s19\n"
"s_cselect_b32 s73,s18,s73\n"
"s_add_u32 m0,s65,49152\n"
MF "v[116:119],v[28:31],v[240:243],v[116:119]\n"
LD "v8,s[36:39],s73 offen nt lds\n"
MF "v[116:119],v[24:27],v[220:223],v[116:119]\n"
"s_add_u32 m0,s65,50048\n"
"v_exp_f32 v81,v81\n"
MF "v[120:123],v[24:27],v[224:227],v[120:123]\n"
LD "v8,s[36:39],s73 offen offset:128 nt lds\n"
MF "v[120:123],v[28:31],v[244:247],v[120:123]\n"
"v_exp_f32 v82,v82\n"
MF "v[124:127],v[28:31],v[248:251],v[124:127]\n"
"s_add_u32 m0,s65,50944\n"
"v_exp_f32 v83,v83\n"
MF "v[124:127],v[24:27],v[228:231],v[124:127]\n"
LD "v8,s[36:39],s73 offen offset:256 nt lds\n"
MF "v[128:131],v[24:27],v[232:235],v[128:131]\n"
"v_add_f32 v54,v59,v80\n"
"v_add_f32 v54,v54,v81\n"
MF "v[128:131],v[28:31],v[252:255],v[128:131]\n"
"s_waitcnt lgkmcnt(0)\n"
TR "v[24:25],v4\n"
TR "v[26:27],v4 offset:6144\n"
MF "a[0:3],v[16:19],v[92:95],a[0:3]\n"
"s_add_u32 m0,s65,49152\n"
TR "v[28:29],v5\n"
TR "v[30:31],v5 offset:6144\n"
MF "a[20:23],v[20:23],v[92:95],a[20:23]\n"
LD "v8,s[36:39],s73 offen offset:3072 nt lds\n"
MF "a[24:27],v[20:23],v[96:99],a[24:27]\n"
CV "v80,v80,v81\n"
"v_add_f32 v54,v54,v82\n"
MF "a[4:7],v[16:19],v[96:99],a[4:7]\n"
"s_add_u32 m0,s65,50048\n"
"v_add_f32 v54,v54,v83\n"
CV "v81,v82,v83\n"
MF "a[8:11],v[16:19],v[100:103],a[8:11]\n"
LD "v8,s[36:39],s73 offen offset:3200 nt lds\n"
MF "a[28:31],v[20:23],v[100:103],a[28:31]\n"
"v_exp_f32 v84,v84\n"
MF "a[32:35],v[20:23],v[104:107],a[32:35]\n"
"s_add_u32 m0,s65,50944\n"
"v_exp_f32 v85,v85\n"
MF "a[12:15],v[16:19],v[104:107],a[12:15]\n"
LD "v8,s[36:39],s73 offen offset:3328 nt lds\n"
MF "a[16:19],v[16:19],v[108:111],a[16:19]\n"
"v_exp_f32 v86,v86\n"
MF "a[36:39],v[20:23],v[108:111],a[36:39]\n"
"s_waitcnt lgkmcnt(0)\n"
TR "v[16:17],v2 offset:1024\n"
TR "v[18:19],v2 offset:7168\n"
MF "a[56:59],v[24:27],v[108:111],a[56:59]\n"
"s_nop 0\n"
TR "v[20:21],v3 offset:1024\n"
TR "v[22:23],v3 offset:7168\n"
MF "a[76:79],v[28:31],v[108:111],a[76:79]\n"
"v_exp_f32 v87,v87\n"
MF "a[72:75],v[28:31],v[104:107],a[72:75]\n"
"v_add_f32 v55,v60,v84\n"
"v_add_f32 v55,v55,v85\n"
MF "a[52:55],v[24:27],v[104:107],a[52:55]\n"
CV "v84,v84,v85\n"
"v_add_f32 v55,v55,v86\n"
MF "a[48:51],v[24:27],v[100:103],a[48:51]\n"
"v_add_f32 v55,v55,v87\n"
CV "v85,v86,v87\n"
MF "a[68:71],v[28:31],v[100:103],a[68:71]\n"
"v_exp_f32 v88,v88\n"
MF "a[64:67],v[28:31],v[96:99],a[64:67]\n"
"v_exp_f32 v89,v89\n"
MF "a[44:47],v[24:27],v[96:99],a[44:47]\n"
"v_exp_f32 v90,v90\n"
MF "a[40:43],v[24:27],v[92:95],a[40:43]\n"
"v_exp_f32 v91,v91\n"
MF "a[60:63],v[28:31],v[92:95],a[60:63]\n"
"s_waitcnt lgkmcnt(0)\n"
TR "v[24:25],v4 offset:1024\n"
TR "v[26:27],v4 offset:7168\n"
MF "a[80:83],v[16:19],v[92:95],a[80:83]\n"
"s_nop 0\n"
TR "v[28:29],v5 offset:1024\n"
TR "v[30:31],v5 offset:7168\n"
MF "a[100:103],v[20:23],v[92:95],a[100:103]\n"
"v_add_f32 v56,v61,v88\n"
"v_add_f32 v56,v56,v89\n"
MF "a[104:107],v[20:23],v[96:99],a[104:107]\n"
CV "v88,v88,v89\n"
"v_add_f32 v56,v56,v90\n"
MF "a[84:87],v[16:19],v[96:99],a[84:87]\n"
"v_add_f32 v56,v56,v91\n"
CV "v89,v90,v91\n"
MF "a[88:91],v[16:19],v[100:103],a[88:91]\n"
"v_exp_f32 v112,v112\n"
MF "a[108:111],v[20:23],v[100:103],a[108:111]\n"
"v_exp_f32 v113,v113\n"
MF "a[112:115],v[20:23],v[104:107],a[112:115]\n"
"v_exp_f32 v114,v114\n"
MF "a[92:95],v[16:19],v[104:107],a[92:95]\n"
"v_exp_f32 v115,v115\n"
MF "a[96:99],v[16:19],v[108:111],a[96:99]\n"
"v_add_f32 v52,v52,v112\n"
"v_add_f32 v52,v52,v113\n"
MF "a[116:119],v[20:23],v[108:111],a[116:119]\n"
"s_waitcnt lgkmcnt(0)\n"
TR "v[16:17],v2 offset:2048\n"
TR "v[18:19],v2 offset:8192\n"
MF "a[136:139],v[24:27],v[108:111],a[136:139]\n"
"s_nop 0\n"
TR "v[20:21],v3 offset:2048\n"
TR "v[22:23],v3 offset:8192\n"
MF "a[156:159],v[28:31],v[108:111],a[156:159]\n"
CV "v74,v112,v113\n"
"v_add_f32 v52,v52,v114\n"
MF "a[152:155],v[28:31],v[104:107],a[152:155]\n"
"v_add_f32 v52,v52,v115\n"
CV "v75,v114,v115\n"
MF "a[132:135],v[24:27],v[104:107],a[132:135]\n"
"v_exp_f32 v116,v116\n"
MF "a[128:131],v[24:27],v[100:103],a[128:131]\n"
"v_exp_f32 v117,v117\n"
MF "a[148:151],v[28:31],v[100:103],a[148:151]\n"
"v_exp_f32 v118,v118\n"
MF "a[144:147],v[28:31],v[96:99],a[144:147]\n"
"v_exp_f32 v119,v119\n"
MF "a[124:127],v[24:27],v[96:99],a[124:127]\n"
"v_add_f32 v53,v53,v116\n"
"v_add_f32 v53,v53,v117\n"
MF "a[120:123],v[24:27],v[92:95],a[120:123]\n"
CV "v78,v116,v117\n"
"v_add_f32 v53,v53,v118\n"
MF "a[140:143],v[28:31],v[92:95],a[140:143]\n"
"s_waitcnt lgkmcnt(0)\n"
TR "v[24:25],v4 offset:2048\n"
TR "v[26:27],v4 offset:8192\n"
MF "a[160:163],v[16:19],v[92:95],a[160:163]\n"
"s_nop 0\n"
TR "v[28:29],v5 offset:2048\n"
TR "v[30:31],v5 offset:8192\n"
MF "a[180:183],v[20:23],v[92:95],a[180:183]\n"
"v_add_f32 v53,v53,v119\n"
CV "v79,v118,v119\n"
MF "a[184:187],v[20:23],v[96:99],a[184:187]\n"
"v_exp_f32 v120,v120\n"
MF "a[164:167],v[16:19],v[96:99],a[164:167]\n"
"v_exp_f32 v121,v121\n"
MF "a[168:171],v[16:19],v[100:103],a[168:171]\n"
"v_exp_f32 v122,v122\n"
MF "a[188:191],v[20:23],v[100:103],a[188:191]\n"
"v_exp_f32 v123,v123\n"
MF "a[192:195],v[20:23],v[104:107],a[192:195]\n"
"v_add_f32 v54,v54,v120\n"
"v_add_f32 v54,v54,v121\n"
MF "a[172:175],v[16:19],v[104:107],a[172:175]\n"
"s_waitcnt vmcnt(6) lgkmcnt(0)\n"
CV "v82,v120,v121\n"
"v_add_f32 v54,v54,v122\n"
MF "a[176:179],v[16:19],v[108:111],a[176:179]\n"
"s_barrier\n"
MF "a[196:199],v[20:23],v[108:111],a[196:199]\n"
"s_add_u32 s72,s72,6144\n"
"ds_read_b128 v[16:19],v0 offset:24576\n"
"ds_read_b128 v[20:23],v1 offset:24576\n"
MF "a[216:219],v[24:27],v[108:111],a[216:219]\n"
"s_cmp_eq_u32 s72,s17\n"
"s_cselect_b32 s72,s16,s72\n"
"s_add_u32 m0,s64,0\n"
MF "a[236:239],v[28:31],v[108:111],a[236:239]\n"
LD "v7,s[32:35],s72 offen nt lds\n"
MF "a[232:235],v[28:31],v[104:107],a[232:235]\n"
"s_add_u32 m0,s64,896\n"
"v_add_f32 v54,v54,v123\n"
CV "v83,v122,v123\n"
MF "a[212:215],v[24:27],v[104:107],a[212:215]\n"
LD "v7,s[32:35],s72 offen offset:128 nt lds\n"
MF "a[208:211],v[24:27],v[100:103],a[208:211]\n"
"s_add_u32 m0,s64,1792\n"
MF "a[228:231],v[28:31],v[100:103],a[228:231]\n"
LD "v7,s[32:35],s72 offen offset:256 nt lds\n"
MF "a[224:227],v[28:31],v[96:99],a[224:227]\n"
MF "a[204:207],v[24:27],v[96:99],a[204:207]\n"
MF "a[200:203],v[24:27],v[92:95],a[200:203]\n"
MF "a[220:223],v[28:31],v[92:95],a[220:223]\n"
"s_waitcnt lgkmcnt(0)\n"
MF "v[92:95],v[16:19],v[136:139],v[32:35]\n"
"s_add_u32 s72,s72,6144\n"
"ds_read_b128 v[24:27],v0 offset:25600\n"
"ds_read_b128 v[28:31],v1 offset:25600\n"
MF "v[92:95],v[20:23],v[156:159],v[92:95]\n"
"s_add_u32 m0,s64,6144\n"
"v_exp_f32 v124,v124\n"
MF "v[96:99],v[20:23],v[160:163],v[36:39]\n"
LD "v7,s[32:35],s72 offen nt lds\n"
MF "v[96:99],v[16:19],v[140:143],v[96:99]\n"
"v_exp_f32 v125,v125\n"
MF "v[100:103],v[16:19],v[144:147],v[40:43]\n"
"s_add_u32 m0,s64,7040\n"
"v_exp_f32 v126,v126\n"
MF "v[100:103],v[20:23],v[164:167],v[100:103]\n"
LD "v7,s[32:35],s72 offen offset:128 nt lds\n"
MF "v[104:107],v[20:23],v[168:171],v[44:47]\n"
"v_exp_f32 v127,v127\n"
MF "v[104:107],v[16:19],v[148:151],v[104:107]\n"
"s_add_u32 m0,s64,7936\n"
"v_add_f32 v55,v55,v124\n"
"v_add_f32 v55,v55,v125\n"
MF "v[108:111],v[16:19],v[152:155],v[48:51]\n"
LD "v7,s[32:35],s72 offen offset:256 nt lds\n"
MF "v[108:111],v[20:23],v[172:175],v[108:111]\n"
"s_waitcnt lgkmcnt(0)\n"
"ds_read_b128 v[16:19],v0 offset:26624\n"
"ds_read_b128 v[20:23],v1 offset:26624\n"
MF "v[108:111],v[24:27],v[192:195],v[108:111]\n"
CV "v86,v124,v125\n"
"v_add_f32 v55,v55,v126\n"
MF "v[108:111],v[28:31],v[212:215],v[108:111]\n"
"v_add_f32 v55,v55,v127\n"
CV "v87,v126,v127\n"
MF "v[104:107],v[28:31],v[208:211],v[104:107]\n"
"v_exp_f32 v128,v128\n"
MF "v[104:107],v[24:27],v[188:191],v[104:107]\n"
"v_exp_f32 v129,v129\n"
MF "v[100:103],v[24:27],v[184:187],v[100:103]\n"
"v_exp_f32 v130,v130\n"
MF "v[100:103],v[28:31],v[204:207],v[100:103]\n"
"v_exp_f32 v131,v131\n"
MF "v[96:99],v[28:31],v[200:203],v[96:99]\n"
"v_add_f32 v56,v56,v128\n"
"v_add_f32 v56,v56,v129\n"
MF "v[96:99],v[24:27],v[180:183],v[96:99]\n"
CV "v90,v128,v129\n"
"v_add_f32 v56,v56,v130\n"
MF "v[92:95],v[24:27],v[176:179],v[92:95]\n"
"v_add_f32 v56,v56,v131\n"
CV "v91,v130,v131\n"
MF "v[92:95],v[28:31],v[196:199],v[92:95]\n"
"s_waitcnt lgkmcnt(0)\n"
"ds_read_b128 v[24:27],v0 offset:30720\n"
"ds_read_b128 v[28:31],v1 offset:30720\n"
MF "v[92:95],v[16:19],v[216:219],v[92:95]\n"
MF "v[92:95],v[20:23],v[236:239],v[92:95]\n"
MF "v[96:99],v[20:23],v[240:243],v[96:99]\n"
MF "v[96:99],v[16:19],v[220:223],v[96:99]\n"
MF "v[100:103],v[16:19],v[224:227],v[100:103]\n"
MF "v[100:103],v[20:23],v[244:247],v[100:103]\n"
MF "v[104:107],v[20:23],v[248:251],v[104:107]\n"
MF "v[104:107],v[16:19],v[228:231],v[104:107]\n"
"v_max3_f32 v10,v52,v53,v54\n"
"v_max3_f32 v10,v10,v55,v56\n"
MF "v[108:111],v[16:19],v[232:235],v[108:111]\n"
"v_cmp_ngt_f32 vcc,s77,v10\n"
MF "v[108:111],v[20:23],v[252:255],v[108:111]\n"
"s_waitcnt lgkmcnt(0)\n"
"s_cbranch_vccnz .Lstub_14\n"
".Lback_14:\n"
MF "v[112:115],v[24:27],v[136:139],v[32:35]\n"
"s_add_u32 s74,s74,12288\n"
"ds_read_b128 v[16:19],v0 offset:31744\n"
"ds_read_b128 v[20:23],v1 offset:31744\n"
MF "v[112:115],v[28:31],v[156:159],v[112:115]\n"
"s_cmp_eq_u32 s74,s20\n"
"s_cselect_b32 s74,s70,s74\n"
MF "v[116:119],v[28:31],v[160:163],v[36:39]\n"
"v_exp_f32 v92,v92\n"
MF "v[116:119],v[24:27],v[140:143],v[116:119]\n"
"v_exp_f32 v93,v93\n"
MF "v[120:123],v[24:27],v[144:147],v[40:43]\n"
"v_exp_f32 v94,v94\n"
MF "v[120:123],v[28:31],v[164:167],v[120:123]\n"
"v_exp_f32 v95,v95\n"
MF "v[124:127],v[28:31],v[168:171],v[44:47]\n"
"v_add_f32 v57,v52,v92\n"
"v_add_f32 v57,v57,v93\n"
MF "v[124:127],v[24:27],v[148:151],v[124:127]\n"
CV "v92,v92,v93\n"
"v_add_f32 v57,v57,v94\n"
MF "v[128:131],v[24:27],v[152:155],v[48:51]\n"
"v_add_f32 v57,v57,v95\n"
CV "v93,v94,v95\n"
MF "v[128:131],v[28:31],v[172:175],v[128:131]\n"
"s_waitcnt lgkmcnt(0)\n"
"ds_read_b128 v[24:27],v0 offset:32768\n"
"ds_read_b128 v[28:31],v1 offset:32768\n"
MF "v[128:131],v[16:19],v[192:195],v[128:131]\n"
"v_exp_f32 v96,v96\n"
MF "v[128:131],v[20:23],v[212:215],v[128:131]\n"
"v_exp_f32 v97,v97\n"
MF "v[124:127],v[20:23],v[208:211],v[124:127]\n"
"v_exp_f32 v98,v98\n"
MF "v[124:127],v[16:19],v[188:191],v[124:127]\n"
"v_exp_f32 v99,v99\n"
MF "v[120:123],v[16:19],v[184:187],v[120:123]\n"
"v_add_f32 v58,v53,v96\n"
"v_add_f32 v58,v58,v97\n"
MF "v[120:123],v[20:23],v[204:207],v[120:123]\n"
CV "v96,v96,v97\n"
"v_add_f32 v58,v58,v98\n"
MF "v[116:119],v[20:23],v[200:203],v[116:119]\n"
"s_waitcnt vmcnt(6) lgkmcnt(0)\n"
"v_add_f32 v58,v58,v99\n"
CV "v97,v98,v99\n"
MF "v[116:119],v[16:19],v[180:183],v[116:119]\n"
"s_barrier\n"
MF "v[112:115],v[16:19],v[176:179],v[112:115]\n"
"v_exp_f32 v100,v100\n"
MF "v[112:115],v[20:23],v[196:199],v[112:115]\n"
"s_nop 0\n"
TR "v[16:17],v2 offset:49152\n"
TR "v[18:19],v2 offset:55296\n"
MF "v[112:115],v[24:27],v[216:219],v[112:115]\n"
"s_add_u32 s73,s73,12288\n"
TR "v[20:21],v3 offset:49152\n"
TR "v[22:23],v3 offset:55296\n"
MF "v[112:115],v[28:31],v[236:239],v[112:115]\n"
"s_cmp_eq_u32 s73,s19\n"
"s_cselect_b32 s73,s18,s73\n"
"s_add_u32 m0,s65,24576\n"
MF "v[116:119],v[28:31],v[240:243],v[116:119]\n"
LD "v8,s[36:39],s73 offen nt lds\n"
MF "v[116:119],v[24:27],v[220:223],v[116:119]\n"
"s_add_u32 m0,s65,25472\n"
"v_exp_f32 v101,v101\n"
MF "v[120:123],v[24:27],v[224:227],v[120:123]\n"
LD "v8,s[36:39],s73 offen offset:128 nt lds\n"
MF "v[120:123],v[28:31],v[244:247],v[120:123]\n"
"v_exp_f32 v102,v102\n"
MF "v[124:127],v[28:31],v[248:251],v[124:127]\n"
"s_add_u32 m0,s65,26368\n"
"v_exp_f32 v103,v103\n"
MF "v[124:127],v[24:27],v[228:231],v[124:127]\n"
LD "v8,s[36:39],s73 offen offset:256 nt lds\n"
MF "v[128:131],v[24:27],v[232:235],v[128:131]\n"
"v_add_f32 v59,v54,v100\n"
"v_add_f32 v59,v59,v101\n"
MF "v[128:131],v[28:31],v[252:255],v[128:131]\n"
"s_waitcnt lgkmcnt(0)\n"
TR "v[24:25],v4 offset:49152\n"
TR "v[26:27],v4 offset:55296\n"
MF "a[0:3],v[16:19],v[72:75],a[0:3]\n"
"s_add_u32 m0,s65,24576\n"
TR "v[28:29],v5 offset:49152\n"
TR "v[30:31],v5 offset:55296\n"
MF "a[20:23],v[20:23],v[72:75],a[20:23]\n"
LD "v8,s[36:39],s73 offen offset:3072 nt lds\n"
MF "a[24:27],v[20:23],v[76:79],a[24:27]\n"
CV "v100,v100,v101\n"
"v_add_f32 v59,v59,v102\n"
MF "a[4:7],v[16:19],v[76:79],a[4:7]\n"
"s_add_u32 m0,s65,25472\n"
"v_add_f32 v59,v59,v103\n"
CV "v101,v102,v103\n"
MF "a[8:11],v[16:19],v[80:83],a[8:11]\n"
LD "v8,s[36:39],s73 offen offset:3200 nt lds\n"
MF "a[28:31],v[20:23],v[80:83],a[28:31]\n"
"v_exp_f32 v104,v104\n"
MF "a[32:35],v[20:23],v[84:87],a[32:35]\n"
"s_add_u32 m0,s65,26368\n"
"v_exp_f32 v105,v105\n"
MF "a[12:15],v[16:19],v[84:87],a[12:15]\n"
LD "v8,s[36:39],s73 offen offset:3328 nt lds\n"
MF "a[16:19],v[16:19],v[88:91],a[16:19]\n"
"v_exp_f32 v106,v106\n"
MF "a[36:39],v[20:23],v[88:91],a[36:39]\n"
"s_waitcnt lgkmcnt(0)\n"
TR "v[16:17],v2 offset:50176\n"
TR "v[18:19],v2 offset:56320\n"
MF "a[56:59],v[24:27],v[88:91],a[56:59]\n"
"s_nop 0\n"
TR "v[20:21],v3 offset:50176\n"
TR "v[22:23],v3 offset:56320\n"
MF "a[76:79],v[28:31],v[88:91],a[76:79]\n"
"v_exp_f32 v107,v107\n"
MF "a[72:75],v[28:31],v[84:87],a[72:75]\n"
"v_add_f32 v60,v55,v104\n"
"v_add_f32 v60,v60,v105\n"
MF "a[52:55],v[24:27],v[84:87],a[52:55]\n"
CV "v104,v104,v105\n"
"v_add_f32 v60,v60,v106\n"
MF "a[48:51],v[24:27],v[80:83],a[48:51]\n"
"v_add_f32 v60,v60,v107\n"
CV "v105,v106,v107\n"
MF "a[68:71],v[28:31],v[80:83],a[68:71]\n"
"v_exp_f32 v108,v108\n"
MF "a[64:67],v[28:31],v[76:79],a[64:67]\n"
"v_exp_f32 v109,v109\n"
MF "a[44:47],v[24:27],v[76:79],a[44:47]\n"
"v_exp_f32 v110,v110\n"
MF "a[40:43],v[24:27],v[72:75],a[40:43]\n"
"v_exp_f32 v111,v111\n"
MF "a[60:63],v[28:31],v[72:75],a[60:63]\n"
"s_waitcnt lgkmcnt(0)\n"
TR "v[24:25],v4 offset:50176\n"
TR "v[26:27],v4 offset:56320\n"
MF "a[80:83],v[16:19],v[72:75],a[80:83]\n"
"s_nop 0\n"
TR "v[28:29],v5 offset:50176\n"
TR "v[30:31],v5 offset:56320\n"
MF "a[100:103],v[20:23],v[72:75],a[100:103]\n"
"v_add_f32 v61,v56,v108\n"
"v_add_f32 v61,v61,v109\n"
MF "a[104:107],v[20:23],v[76:79],a[104:107]\n"
CV "v108,v108,v109\n"
"v_add_f32 v61,v61,v110\n"
MF "a[84:87],v[16:19],v[76:79],a[84:87]\n"
"v_add_f32 v61,v61,v111\n"
CV "v109,v110,v111\n"
MF "a[88:91],v[16:19],v[80:83],a[88:91]\n"
"v_exp_f32 v112,v112\n"
MF "a[108:111],v[20:23],v[80:83],a[108:111]\n"
"v_exp_f32 v113,v113\n"
MF "a[112:115],v[20:23],v[84:87],a[112:115]\n"
"v_exp_f32 v114,v114\n"
MF "a[92:95],v[16:19],v[84:87],a[92:95]\n"
"v_exp_f32 v115,v115\n"
MF "a[96:99],v[16:19],v[88:91],a[96:99]\n"
"v_add_f32 v57,v57,v112\n"
"v_add_f32 v57,v57,v113\n"
MF "a[116:119],v[20:23],v[88:91],a[116:119]\n"
"s_waitcnt lgkmcnt(0)\n"
TR "v[16:17],v2 offset:51200\n"
TR "v[18:19],v2 offset:57344\n"
MF "a[136:139],v[24:27],v[88:91],a[136:139]\n"
"s_nop 0\n"
TR "v[20:21],v3 offset:51200\n"
TR "v[22:23],v3 offset:57344\n"
MF "a[156:159],v[28:31],v[88:91],a[156:159]\n"
CV "v94,v112,v113\n"
"v_add_f32 v57,v57,v114\n"
MF "a[152:155],v[28:31],v[84:87],a[152:155]\n"
"v_add_f32 v57,v57,v115\n"
CV "v95,v114,v115\n"
MF "a[132:135],v[24:27],v[84:87],a[132:135]\n"
"v_exp_f32 v116,v116\n"
MF "a[128:131],v[24:27],v[80:83],a[128:131]\n"
"v_exp_f32 v117,v117\n"
MF "a[148:151],v[28:31],v[80:83],a[148:151]\n"
"v_exp_f32 v118,v118\n"
MF "a[144:147],v[28:31],v[76:79],a[144:147]\n"
"v_exp_f32 v119,v119\n"
MF "a[124:127],v[24:27],v[76:79],a[124:127]\n"
"v_add_f32 v58,v58,v116\n"
"v_add_f32 v58,v58,v117\n"
MF "a[120:123],v[24:27],v[72:75],a[120:123]\n"
CV "v98,v116,v117\n"
"v_add_f32 v58,v58,v118\n"
MF "a[140:143],v[28:31],v[72:75],a[140:143]\n"
"s_waitcnt lgkmcnt(0)\n"
TR "v[24:25],v4 offset:51200\n"
TR "v[26:27],v4 offset:57344\n"
MF "a[160:163],v[16:19],v[72:75],a[160:163]\n"
"s_nop 0\n"
TR "v[28:29],v5 offset:51200\n"
TR "v[30:31],v5 offset:57344\n"
MF "a[180:183],v[20:23],v[72:75],a[180:183]\n"
"v_add_f32 v58,v58,v119\n"
CV "v99,v118,v119\n"
MF "a[184:187],v[20:23],v[76:79],a[184:187]\n"
"v_exp_f32 v120,v120\n"
MF "a[164:167],v[16:19],v[76:79],a[164:167]\n"
"v_exp_f32 v121,v121\n"
MF "a[168:171],v[16:19],v[80:83],a[168:171]\n"
"v_exp_f32 v122,v122\n"
MF "a[188:191],v[20:23],v[80:83],a[188:191]\n"
"v_exp_f32 v123,v123\n"
MF "a[192:195],v[20:23],v[84:87],a[192:195]\n"
"v_add_f32 v59,v59,v120\n"
"v_add_f32 v59,v59,v121\n"
MF "a[172:175],v[16:19],v[84:87],a[172:175]\n"
"s_waitcnt vmcnt(6) lgkmcnt(0)\n"
CV "v102,v120,v121\n"
"v_add_f32 v59,v59,v122\n"
MF "a[176:179],v[16:19],v[88:91],a[176:179]\n"
"s_barrier\n"
MF "a[196:199],v[20:23],v[88:91],a[196:199]\n"
"s_add_u32 s72,s72,6144\n"
"ds_read_b128 v[16:19],v0\n"
"ds_read_b128 v[20:23],v1\n"
MF "a[216:219],v[24:27],v[88:91],a[216:219]\n"
"s_cmp_eq_u32 s72,s17\n"
"s_cselect_b32 s72,s16,s72\n"
"s_add_u32 m0,s64,49152\n"
MF "a[236:239],v[28:31],v[88:91],a[236:239]\n"
LD "v7,s[32:35],s72 offen nt lds\n"
MF "a[232:235],v[28:31],v[84:87],a[232:235]\n"
"s_add_u32 m0,s64,50048\n"
"v_add_f32 v59,v59,v123\n"
CV "v103,v122,v123\n"
MF "a[212:215],v[24:27],v[84:87],a[212:215]\n"
LD "v7,s[32:35],s72 offen offset:128 nt lds\n"
MF "a[208:211],v[24:27],v[80:83],a[208:211]\n"
"s_add_u32 m0,s64,50944\n"
MF "a[228:231],v[28:31],v[80:83],a[228:231]\n"
LD "v7,s[32:35],s72 offen offset:256 nt lds\n"
MF "a[224:227],v[28:31],v[76:79],a[224:227]\n"
MF "a[204:207],v[24:27],v[76:79],a[204:207]\n"
MF "a[200:203],v[24:27],v[72:75],a[200:203]\n"
MF "a[220:223],v[28:31],v[72:75],a[220:223]\n"
"s_waitcnt lgkmcnt(0)\n"
".Lmid:\n"
MF "v[72:75],v[16:19],v[136:139],v[32:35]\n"
"s_add_u32 s72,s72,6144\n"
"ds_read_b128 v[24:27],v0 offset:1024\n"
"ds_read_b128 v[28:31],v1 offset:1024\n"
MF "v[72:75],v[20:23],v[156:159],v[72:75]\n"
"s_add_u32 m0,s64,55296\n"
"v_exp_f32 v124,v124\n"
MF "v[76:79],v[20:23],v[160:163],v[36:39]\n"
LD "v7,s[32:35],s72 offen nt lds\n"
MF "v[76:79],v[16:19],v[140:143],v[76:79]\n"
"v_exp_f32 v125,v125\n"
MF "v[80:83],v[16:19],v[144:147],v[40:43]\n"
"s_add_u32 m0,s64,56192\n"
"v_exp_f32 v126,v126\n"
MF "v[80:83],v[20:23],v[164:167],v[80:83]\n"
LD "v7,s[32:35],s72 offen offset:128 nt lds\n"
MF "v[84:87],v[20:23],v[168:171],v[44:47]\n"
"v_exp_f32 v127,v127\n"
MF "v[84:87],v[16:19],v[148:151],v[84:87]\n"
"s_add_u32 m0,s64,57088\n"
"v_add_f32 v60,v60,v124\n"
"v_add_f32 v60,v60,v125\n"
MF "v[88:91],v[16:19],v[152:155],v[48:51]\n"
LD "v7,s[32:35],s72 offen offset:256 nt lds\n"
MF "v[88:91],v[20:23],v[172:175],v[88:91]\n"
"s_waitcnt lgkmcnt(0)\n"
"ds_read_b128 v[16:19],v0 offset:2048\n"
"ds_read_b128 v[20:23],v1 offset:2048\n"
MF "v[88:91],v[24:27],v[192:195],v[88:91]\n"
CV "v106,v124,v125\n"
"v_add_f32 v60,v60,v126\n"
MF "v[88:91],v[28:31],v[212:215],v[88:91]\n"
"v_add_f32 v60,v60,v127\n"
CV "v107,v126,v127\n"
MF "v[84:87],v[28:31],v[208:211],v[84:87]\n"
"v_exp_f32 v128,v128\n"
MF "v[84:87],v[24:27],v[188:191],v[84:87]\n"
"v_exp_f32 v129,v129\n"
MF "v[80:83],v[24:27],v[184:187],v[80:83]\n"
"v_exp_f32 v130,v130\n"
MF "v[80:83],v[28:31],v[204:207],v[80:83]\n"
"v_exp_f32 v131,v131\n"
MF "v[76:79],v[28:31],v[200:203],v[76:79]\n"
"v_add_f32 v61,v61,v128\n"
"v_add_f32 v61,v61,v129\n"
MF "v[76:79],v[24:27],v[180:183],v[76:79]\n"
CV "v110,v128,v129\n"
"v_add_f32 v61,v61,v130\n"
MF "v[72:75],v[24:27],v[176:179],v[72:75]\n"
"v_add_f32 v61,v61,v131\n"
CV "v111,v130,v131\n"
MF "v[72:75],v[28:31],v[196:199],v[72:75]\n"
"s_waitcnt lgkmcnt(0)\n"
"ds_read_b128 v[24:27],v0 offset:6144\n"
"ds_read_b128 v[28:31],v1 offset:6144\n"
MF "v[72:75],v[16:19],v[216:219],v[72:75]\n"
MF "v[72:75],v[20:23],v[236:239],v[72:75]\n"
MF "v[76:79],v[20:23],v[240:243],v[76:79]\n"
MF "v[76:79],v[16:19],v[220:223],v[76:79]\n"
MF "v[80:83],v[16:19],v[224:227],v[80:83]\n"
MF "v[80:83],v[20:23],v[244:247],v[80:83]\n"
MF "v[84:87],v[20:23],v[248:251],v[84:87]\n"
MF "v[84:87],v[16:19],v[228:231],v[84:87]\n"
"v_max3_f32 v10,v57,v58,v59\n"
"v_max3_f32 v10,v10,v60,v61\n"
MF "v[88:91],v[16:19],v[232:235],v[88:91]\n"
"v_cmp_ngt_f32 vcc,s95,v10\n"
MF "v[88:91],v[20:23],v[252:255],v[88:91]\n"
"s_waitcnt lgkmcnt(0)\n"
"s_cbranch_vccnz .Lstub_15\n"
".Lback_15:\n"
MF "v[112:115],v[24:27],v[136:139],v[32:35]\n"
"s_add_u32 s74,s74,12288\n"
"ds_read_b128 v[16:19],v0 offset:7168\n"
"ds_read_b128 v[20:23],v1 offset:7168\n"
MF "v[112:115],v[28:31],v[156:159],v[112:115]\n"
"s_cmp_eq_u32 s74,s20\n"
"s_cselect_b32 s74,s70,s74\n"
MF "v[116:119],v[28:31],v[160:163],v[36:39]\n"
"v_exp_f32 v72,v72\n"
MF "v[116:119],v[24:27],v[140:143],v[116:119]\n"
"v_exp_f32 v73,v73\n"
MF "v[120:123],v[24:27],v[144:147],v[40:43]\n"
"v_exp_f32 v74,v74\n"
MF "v[120:123],v[28:31],v[164:167],v[120:123]\n"
"v_exp_f32 v75,v75\n"
MF "v[124:127],v[28:31],v[168:171],v[44:47]\n"
"v_add_f32 v52,v57,v72\n"
"v_add_f32 v52,v52,v73\n"
MF "v[124:127],v[24:27],v[148:151],v[124:127]\n"
CV "v72,v72,v73\n"
"v_add_f32 v52,v52,v74\n"
MF "v[128:131],v[24:27],v[152:155],v[48:51]\n"
"v_add_f32 v52,v52,v75\n"
CV "v73,v74,v75\n"
MF "v[128:131],v[28:31],v[172:175],v[128:131]\n"
"s_waitcnt lgkmcnt(0)\n"
"ds_read_b128 v[24:27],v0 offset:8192\n"
"ds_read_b128 v[28:31],v1 offset:8192\n"
MF "v[128:131],v[16:19],v[192:195],v[128:131]\n"
"v_exp_f32 v76,v76\n"
MF "v[128:131],v[20:23],v[212:215],v[128:131]\n"
"v_exp_f32 v77,v77\n"
MF "v[124:127],v[20:23],v[208:211],v[124:127]\n"
"v_exp_f32 v78,v78\n"
MF "v[124:127],v[16:19],v[188:191],v[124:127]\n"
"v_exp_f32 v79,v79\n"
MF "v[120:123],v[16:19],v[184:187],v[120:123]\n"
"v_add_f32 v53,v58,v76\n"
"v_add_f32 v53,v53,v77\n"
MF "v[120:123],v[20:23],v[204:207],v[120:123]\n"
CV "v76,v76,v77\n"
"v_add_f32 v53,v53,v78\n"
MF "v[116:119],v[20:23],v[200:203],v[116:119]\n"
"s_waitcnt vmcnt(6) lgkmcnt(0)\n"
"v_add_f32 v53,v53,v79\n"
CV "v77,v78,v79\n"
MF "v[116:119],v[16:19],v[180:183],v[116:119]\n"
"s_barrier\n"
MF "v[112:115],v[16:19],v[176:179],v[112:115]\n"
"v_exp_f32 v80,v80\n"
MF "v[112:115],v[20:23],v[196:199],v[112:115]\n"
"s_nop 0\n"
TR "v[16:17],v2 offset:24576\n"
TR "v[18:19],v2 offset:30720\n"
MF "v[112:115],v[24:27],v[216:219],v[112:115]\n"
"s_add_u32 s73,s73,12288\n"
TR "v[20:21],v3 offset:24576\n"
TR "v[22:23],v3 offset:30720\n"
MF "v[112:115],v[28:31],v[236:239],v[112:115]\n"
"s_cmp_eq_u32 s73,s19\n"
"s_cselect_b32 s73,s18,s73\n"
"s_add_u32 m0,s65,0\n"
MF "v[116:119],v[28:31],v[240:243],v[116:119]\n"
LD "v8,s[36:39],s73 offen nt lds\n"
MF "v[116:119],v[24:27],v[220:223],v[116:119]\n"
"s_add_u32 m0,s65,896\n"
"v_exp_f32 v81,v81\n"
MF "v[120:123],v[24:27],v[224:227],v[120:123]\n"
LD "v8,s[36:39],s73 offen offset:128 nt lds\n"
MF "v[120:123],v[28:31],v[244:247],v[120:123]\n"
"v_exp_f32 v82,v82\n"
MF "v[124:127],v[28:31],v[248:251],v[124:127]\n"
"s_add_u32 m0,s65,1792\n"
"v_exp_f32 v83,v83\n"
MF "v[124:127],v[24:27],v[228:231],v[124:127]\n"
LD "v8,s[36:39],s73 offen offset:256 nt lds\n"
MF "v[128:131],v[24:27],v[232:235],v[128:131]\n"
"v_add_f32 v54,v59,v80\n"
"v_add_f32 v54,v54,v81\n"
MF "v[128:131],v[28:31],v[252:255],v[128:131]\n"
"s_waitcnt lgkmcnt(0)\n"
TR "v[24:25],v4 offset:24576\n"
TR "v[26:27],v4 offset:30720\n"
MF "a[0:3],v[16:19],v[92:95],a[0:3]\n"
"s_add_u32 m0,s65,0\n"
TR "v[28:29],v5 offset:24576\n"
TR "v[30:31],v5 offset:30720\n"
MF "a[20:23],v[20:23],v[92:95],a[20:23]\n"
LD "v8,s[36:39],s73 offen offset:3072 nt lds\n"
MF "a[24:27],v[20:23],v[96:99],a[24:27]\n"
CV "v80,v80,v81\n"
"v_add_f32 v54,v54,v82\n"
MF "a[4:7],v[16:19],v[96:99],a[4:7]\n"
"s_add_u32 m0,s65,896\n"
"v_add_f32 v54,v54,v83\n"
CV "v81,v82,v83\n"
MF "a[8:11],v[16:19],v[100:103],a[8:11]\n"
LD "v8,s[36:39],s73 offen offset:3200 nt lds\n"
MF "a[28:31],v[20:23],v[100:103],a[28:31]\n"
"v_exp_f32 v84,v84\n"
MF "a[32:35],v[20:23],v[104:107],a[32:35]\n"
"s_add_u32 m0,s65,1792\n"
"v_exp_f32 v85,v85\n"
MF "a[12:15],v[16:19],v[104:107],a[12:15]\n"
LD "v8,s[36:39],s73 offen offset:3328 nt lds\n"
MF "a[16:19],v[16:19],v[108:111],a[16:19]\n"
"v_exp_f32 v86,v86\n"
MF "a[36:39],v[20:23],v[108:111],a[36:39]\n"
"s_waitcnt lgkmcnt(0)\n"
TR "v[16:17],v2 offset:25600\n"
TR "v[18:19],v2 offset:31744\n"
MF "a[56:59],v[24:27],v[108:111],a[56:59]\n"
"s_nop 0\n"
TR "v[20:21],v3 offset:25600\n"
TR "v[22:23],v3 offset:31744\n"
MF "a[76:79],v[28:31],v[108:111],a[76:79]\n"
"v_exp_f32 v87,v87\n"
MF "a[72:75],v[28:31],v[104:107],a[72:75]\n"
"v_add_f32 v55,v60,v84\n"
"v_add_f32 v55,v55,v85\n"
MF "a[52:55],v[24:27],v[104:107],a[52:55]\n"
CV "v84,v84,v85\n"
"v_add_f32 v55,v55,v86\n"
MF "a[48:51],v[24:27],v[100:103],a[48:51]\n"
"v_add_f32 v55,v55,v87\n"
CV "v85,v86,v87\n"
MF "a[68:71],v[28:31],v[100:103],a[68:71]\n"
"v_exp_f32 v88,v88\n"
MF "a[64:67],v[28:31],v[96:99],a[64:67]\n"
"v_exp_f32 v89,v89\n"
MF "a[44:47],v[24:27],v[96:99],a[44:47]\n"
"v_exp_f32 v90,v90\n"
MF "a[40:43],v[24:27],v[92:95],a[40:43]\n"
"v_exp_f32 v91,v91\n"
MF "a[60:63],v[28:31],v[92:95],a[60:63]\n"
"s_waitcnt lgkmcnt(0)\n"
TR "v[24:25],v4 offset:25600\n"
TR "v[26:27],v4 offset:31744\n"
MF "a[80:83],v[16:19],v[92:95],a[80:83]\n"
"s_nop 0\n"
TR "v[28:29],v5 offset:25600\n"
TR "v[30:31],v5 offset:31744\n"
MF "a[100:103],v[20:23],v[92:95],a[100:103]\n"
"v_add_f32 v56,v61,v88\n"
"v_add_f32 v56,v56,v89\n"
MF "a[104:107],v[20:23],v[96:99],a[104:107]\n"
CV "v88,v88,v89\n"
"v_add_f32 v56,v56,v90\n"
MF "a[84:87],v[16:19],v[96:99],a[84:87]\n"
"v_add_f32 v56,v56,v91\n"
CV "v89,v90,v91\n"
MF "a[88:91],v[16:19],v[100:103],a[88:91]\n"
"v_exp_f32 v112,v112\n"
MF "a[108:111],v[20:23],v[100:103],a[108:111]\n"
"v_exp_f32 v113,v113\n"
MF "a[112:115],v[20:23],v[104:107],a[112:115]\n"
"v_exp_f32 v114,v114\n"
MF "a[92:95],v[16:19],v[104:107],a[92:95]\n"
"v_exp_f32 v115,v115\n"
MF "a[96:99],v[16:19],v[108:111],a[96:99]\n"
"v_add_f32 v52,v52,v112\n"
"v_add_f32 v52,v52,v113\n"
MF "a[116:119],v[20:23],v[108:111],a[116:119]\n"
"s_waitcnt lgkmcnt(0)\n"
TR "v[16:17],v2 offset:26624\n"
TR "v[18:19],v2 offset:32768\n"
MF "a[136:139],v[24:27],v[108:111],a[136:139]\n"
"s_nop 0\n"
TR "v[20:21],v3 offset:26624\n"
TR "v[22:23],v3 offset:32768\n"
MF "a[156:159],v[28:31],v[108:111],a[156:159]\n"
CV "v74,v112,v113\n"
"v_add_f32 v52,v52,v114\n"
MF "a[152:155],v[28:31],v[104:107],a[152:155]\n"
"v_add_f32 v52,v52,v115\n"
CV "v75,v114,v115\n"
MF "a[132:135],v[24:27],v[104:107],a[132:135]\n"
"v_exp_f32 v116,v116\n"
MF "a[128:131],v[24:27],v[100:103],a[128:131]\n"
"v_exp_f32 v117,v117\n"
MF "a[148:151],v[28:31],v[100:103],a[148:151]\n"
"v_exp_f32 v118,v118\n"
MF "a[144:147],v[28:31],v[96:99],a[144:147]\n"
"v_exp_f32 v119,v119\n"
MF "a[124:127],v[24:27],v[96:99],a[124:127]\n"
"v_add_f32 v53,v53,v116\n"
"v_add_f32 v53,v53,v117\n"
MF "a[120:123],v[24:27],v[92:95],a[120:123]\n"
CV "v78,v116,v117\n"
"v_add_f32 v53,v53,v118\n"
MF "a[140:143],v[28:31],v[92:95],a[140:143]\n"
"s_waitcnt lgkmcnt(0)\n"
TR "v[24:25],v4 offset:26624\n"
TR "v[26:27],v4 offset:32768\n"
MF "a[160:163],v[16:19],v[92:95],a[160:163]\n"
"s_nop 0\n"
TR "v[28:29],v5 offset:26624\n"
TR "v[30:31],v5 offset:32768\n"
MF "a[180:183],v[20:23],v[92:95],a[180:183]\n"
"v_add_f32 v53,v53,v119\n"
CV "v79,v118,v119\n"
MF "a[184:187],v[20:23],v[96:99],a[184:187]\n"
"v_exp_f32 v120,v120\n"
MF "a[164:167],v[16:19],v[96:99],a[164:167]\n"
"v_exp_f32 v121,v121\n"
MF "a[168:171],v[16:19],v[100:103],a[168:171]\n"
"v_exp_f32 v122,v122\n"
MF "a[188:191],v[20:23],v[100:103],a[188:191]\n"
"v_exp_f32 v123,v123\n"
MF "a[192:195],v[20:23],v[104:107],a[192:195]\n"
"v_add_f32 v54,v54,v120\n"
"v_add_f32 v54,v54,v121\n"
MF "a[172:175],v[16:19],v[104:107],a[172:175]\n"
"s_waitcnt vmcnt(6) lgkmcnt(0)\n"
CV "v82,v120,v121\n"
"v_add_f32 v54,v54,v122\n"
MF "a[176:179],v[16:19],v[108:111],a[176:179]\n"
"s_barrier\n"
MF "a[196:199],v[20:23],v[108:111],a[196:199]\n"
"s_add_u32 s72,s72,6144\n"
"ds_read_b128 v[16:19],v0 offset:49152\n"
"ds_read_b128 v[20:23],v1 offset:49152\n"
MF "a[216:219],v[24:27],v[108:111],a[216:219]\n"
"s_cmp_eq_u32 s72,s17\n"
"s_cselect_b32 s72,s16,s72\n"
"s_add_u32 m0,s64,24576\n"
MF "a[236:239],v[28:31],v[108:111],a[236:239]\n"
LD "v7,s[32:35],s72 offen nt lds\n"
MF "a[232:235],v[28:31],v[104:107],a[232:235]\n"
"s_add_u32 m0,s64,25472\n"
"v_add_f32 v54,v54,v123\n"
CV "v83,v122,v123\n"
MF "a[212:215],v[24:27],v[104:107],a[212:215]\n"
LD "v7,s[32:35],s72 offen offset:128 nt lds\n"
MF "a[208:211],v[24:27],v[100:103],a[208:211]\n"
"s_add_u32 m0,s64,26368\n"
MF "a[228:231],v[28:31],v[100:103],a[228:231]\n"
LD "v7,s[32:35],s72 offen offset:256 nt lds\n"
MF "a[224:227],v[28:31],v[96:99],a[224:227]\n"
MF "a[204:207],v[24:27],v[96:99],a[204:207]\n"
MF "a[200:203],v[24:27],v[92:95],a[200:203]\n"
MF "a[220:223],v[28:31],v[92:95],a[220:223]\n"
"s_waitcnt lgkmcnt(0)\n"
MF "v[92:95],v[16:19],v[136:139],v[32:35]\n"
"s_add_u32 s72,s72,6144\n"
"ds_read_b128 v[24:27],v0 offset:50176\n"
"ds_read_b128 v[28:31],v1 offset:50176\n"
MF "v[92:95],v[20:23],v[156:159],v[92:95]\n"
"s_add_u32 m0,s64,30720\n"
"v_exp_f32 v124,v124\n"
MF "v[96:99],v[20:23],v[160:163],v[36:39]\n"
LD "v7,s[32:35],s72 offen nt lds\n"
MF "v[96:99],v[16:19],v[140:143],v[96:99]\n"
"v_exp_f32 v125,v125\n"
MF "v[100:103],v[16:19],v[144:147],v[40:43]\n"
"s_add_u32 m0,s64,31616\n"
"v_exp_f32 v126,v126\n"
MF "v[100:103],v[20:23],v[164:167],v[100:103]\n"
LD "v7,s[32:35],s72 offen offset:128 nt lds\n"
MF "v[104:107],v[20:23],v[168:171],v[44:47]\n"
"v_exp_f32 v127,v127\n"
MF "v[104:107],v[16:19],v[148:151],v[104:107]\n"
"s_add_u32 m0,s64,32512\n"
"v_add_f32 v55,v55,v124\n"
"v_add_f32 v55,v55,v125\n"
MF "v[108:111],v[16:19],v[152:155],v[48:51]\n"
LD "v7,s[32:35],s72 offen offset:256 nt lds\n"
MF "v[108:111],v[20:23],v[172:175],v[108:111]\n"
"s_waitcnt lgkmcnt(0)\n"
"ds_read_b128 v[16:19],v0 offset:51200\n"
"ds_read_b128 v[20:23],v1 offset:51200\n"
MF "v[108:111],v[24:27],v[192:195],v[108:111]\n"
CV "v86,v124,v125\n"
"v_add_f32 v55,v55,v126\n"
MF "v[108:111],v[28:31],v[212:215],v[108:111]\n"
"v_add_f32 v55,v55,v127\n"
CV "v87,v126,v127\n"
MF "v[104:107],v[28:31],v[208:211],v[104:107]\n"
"v_exp_f32 v128,v128\n"
MF "v[104:107],v[24:27],v[188:191],v[104:107]\n"
"v_exp_f32 v129,v129\n"
MF "v[100:103],v[24:27],v[184:187],v[100:103]\n"
"v_exp_f32 v130,v130\n"
MF "v[100:103],v[28:31],v[204:207],v[100:103]\n"
"v_exp_f32 v131,v131\n"
MF "v[96:99],v[28:31],v[200:203],v[96:99]\n"
"v_add_f32 v56,v56,v128\n"
"v_add_f32 v56,v56,v129\n"
MF "v[96:99],v[24:27],v[180:183],v[96:99]\n"
CV "v90,v128,v129\n"
"v_add_f32 v56,v56,v130\n"
MF "v[92:95],v[24:27],v[176:179],v[92:95]\n"
"v_add_f32 v56,v56,v131\n"
CV "v91,v130,v131\n"
MF "v[92:95],v[28:31],v[196:199],v[92:95]\n"
"s_waitcnt lgkmcnt(0)\n"
"ds_read_b128 v[24:27],v0 offset:55296\n"
"ds_read_b128 v[28:31],v1 offset:55296\n"
MF "v[92:95],v[16:19],v[216:219],v[92:95]\n"
MF "v[92:95],v[20:23],v[236:239],v[92:95]\n"
MF "v[96:99],v[20:23],v[240:243],v[96:99]\n"
MF "v[96:99],v[16:19],v[220:223],v[96:99]\n"
MF "v[100:103],v[16:19],v[224:227],v[100:103]\n"
MF "v[100:103],v[20:23],v[244:247],v[100:103]\n"
MF "v[104:107],v[20:23],v[248:251],v[104:107]\n"
MF "v[104:107],v[16:19],v[228:231],v[104:107]\n"
"v_max3_f32 v10,v52,v53,v54\n"
"v_max3_f32 v10,v10,v55,v56\n"
MF "v[108:111],v[16:19],v[232:235],v[108:111]\n"
"v_cmp_ngt_f32 vcc,s77,v10\n"
MF "v[108:111],v[20:23],v[252:255],v[108:111]\n"
"s_waitcnt lgkmcnt(0)\n"
"s_cbranch_vccnz .Lstub_16\n"
".Lback_16:\n"
MF "v[112:115],v[24:27],v[136:139],v[32:35]\n"
"s_add_u32 s74,s74,12288\n"
"ds_read_b128 v[16:19],v0 offset:56320\n"
"ds_read_b128 v[20:23],v1 offset:56320\n"
MF "v[112:115],v[28:31],v[156:159],v[112:115]\n"
"s_cmp_eq_u32 s74,s20\n"
"s_cselect_b32 s74,s70,s74\n"
MF "v[116:119],v[28:31],v[160:163],v[36:39]\n"
"v_exp_f32 v92,v92\n"
MF "v[116:119],v[24:27],v[140:143],v[116:119]\n"
"v_exp_f32 v93,v93\n"
MF "v[120:123],v[24:27],v[144:147],v[40:43]\n"
"v_exp_f32 v94,v94\n"
MF "v[120:123],v[28:31],v[164:167],v[120:123]\n"
"v_exp_f32 v95,v95\n"
MF "v[124:127],v[28:31],v[168:171],v[44:47]\n"
"v_add_f32 v57,v52,v92\n"
"v_add_f32 v57,v57,v93\n"
MF "v[124:127],v[24:27],v[148:151],v[124:127]\n"
CV "v92,v92,v93\n"
"v_add_f32 v57,v57,v94\n"
MF "v[128:131],v[24:27],v[152:155],v[48:51]\n"
"v_add_f32 v57,v57,v95\n"
CV "v93,v94,v95\n"
MF "v[128:131],v[28:31],v[172:175],v[128:131]\n"
"s_waitcnt lgkmcnt(0)\n"
"ds_read_b128 v[24:27],v0 offset:57344\n"
"ds_read_b128 v[28:31],v1 offset:57344\n"
MF "v[128:131],v[16:19],v[192:195],v[128:131]\n"
"s_sub_u32 s61,s61,1\n"
"v_exp_f32 v96,v96\n"
MF "v[128:131],v[20:23],v[212:215],v[128:131]\n"
"s_cmp_eq_u32 s61,1\n"
"s_cselect_b32 s95,0,s95\n"
"v_add_f32 v58,v53,v96\n"
MF "v[124:127],v[20:23],v[208:211],v[124:127]\n"
"v_exp_f32 v97,v97\n"
MF "v[124:127],v[16:19],v[188:191],v[124:127]\n"
"v_exp_f32 v98,v98\n"
MF "v[120:123],v[16:19],v[184:187],v[120:123]\n"
"v_exp_f32 v99,v99\n"
MF "v[120:123],v[20:23],v[204:207],v[120:123]\n"
"v_add_f32 v58,v58,v97\n"
CV "v96,v96,v97\n"
MF "v[116:119],v[20:23],v[200:203],v[116:119]\n"
"s_waitcnt vmcnt(6) lgkmcnt(0)\n"
"v_add_f32 v58,v58,v98\n"
"v_add_f32 v58,v58,v99\n"
MF "v[116:119],v[16:19],v[180:183],v[116:119]\n"
"s_barrier\n"
MF "v[112:115],v[16:19],v[176:179],v[112:115]\n"
CV "v97,v98,v99\n"
MF "v[112:115],v[20:23],v[196:199],v[112:115]\n"
"s_nop 0\n"
TR "v[16:17],v2\n"
TR "v[18:19],v2 offset:6144\n"
MF "v[112:115],v[24:27],v[216:219],v[112:115]\n"
"s_add_u32 s73,s73,12288\n"
TR "v[20:21],v3\n"
TR "v[22:23],v3 offset:6144\n"
MF "v[112:115],v[28:31],v[236:239],v[112:115]\n"
"s_cmp_eq_u32 s73,s19\n"
"s_cselect_b32 s73,s18,s73\n"
"s_add_u32 m0,s65,49152\n"
MF "v[116:119],v[28:31],v[240:243],v[116:119]\n"
LD "v8,s[36:39],s73 offen nt lds\n"
MF "v[116:119],v[24:27],v[220:223],v[116:119]\n"
"s_add_u32 m0,s65,50048\n"
"v_exp_f32 v100,v100\n"
MF "v[120:123],v[24:27],v[224:227],v[120:123]\n"
LD "v8,s[36:39],s73 offen offset:128 nt lds\n"
MF "v[120:123],v[28:31],v[244:247],v[120:123]\n"
"v_exp_f32 v101,v101\n"
MF "v[124:127],v[28:31],v[248:251],v[124:127]\n"
"s_add_u32 m0,s65,50944\n"
"v_exp_f32 v102,v102\n"
MF "v[124:127],v[24:27],v[228:231],v[124:127]\n"
LD "v8,s[36:39],s73 offen offset:256 nt lds\n"
MF "v[128:131],v[24:27],v[232:235],v[128:131]\n"
"v_exp_f32 v103,v103\n"
MF "v[128:131],v[28:31],v[252:255],v[128:131]\n"
"s_waitcnt lgkmcnt(0)\n"
TR "v[24:25],v4\n"
TR "v[26:27],v4 offset:6144\n"
MF "a[0:3],v[16:19],v[72:75],a[0:3]\n"
"s_add_u32 m0,s65,49152\n"
TR "v[28:29],v5\n"
TR "v[30:31],v5 offset:6144\n"
MF "a[20:23],v[20:23],v[72:75],a[20:23]\n"
LD "v8,s[36:39],s73 offen offset:3072 nt lds\n"
MF "a[24:27],v[20:23],v[76:79],a[24:27]\n"
"v_add_f32 v59,v54,v100\n"
"v_add_f32 v59,v59,v101\n"
MF "a[4:7],v[16:19],v[76:79],a[4:7]\n"
"s_add_u32 m0,s65,50048\n"
CV "v100,v100,v101\n"
"v_add_f32 v59,v59,v102\n"
MF "a[8:11],v[16:19],v[80:83],a[8:11]\n"
LD "v8,s[36:39],s73 offen offset:3200 nt lds\n"
MF "a[28:31],v[20:23],v[80:83],a[28:31]\n"
"v_add_f32 v59,v59,v103\n"
CV "v101,v102,v103\n"
MF "a[32:35],v[20:23],v[84:87],a[32:35]\n"
"s_add_u32 m0,s65,50944\n"
"v_exp_f32 v104,v104\n"
MF "a[12:15],v[16:19],v[84:87],a[12:15]\n"
LD "v8,s[36:39],s73 offen offset:3328 nt lds\n"
MF "a[16:19],v[16:19],v[88:91],a[16:19]\n"
"v_exp_f32 v105,v105\n"
MF "a[36:39],v[20:23],v[88:91],a[36:39]\n"
"s_waitcnt lgkmcnt(0)\n"
TR "v[16:17],v2 offset:1024\n"
TR "v[18:19],v2 offset:7168\n"
MF "a[56:59],v[24:27],v[88:91],a[56:59]\n"
"s_nop 0\n"
TR "v[20:21],v3 offset:1024\n"
TR "v[22:23],v3 offset:7168\n"
MF "a[76:79],v[28:31],v[88:91],a[76:79]\n"
"v_exp_f32 v106,v106\n"
MF "a[72:75],v[28:31],v[84:87],a[72:75]\n"
"v_exp_f32 v107,v107\n"
MF "a[52:55],v[24:27],v[84:87],a[52:55]\n"
"v_add_f32 v60,v55,v104\n"
"v_add_f32 v60,v60,v105\n"
MF "a[48:51],v[24:27],v[80:83],a[48:51]\n"
CV "v104,v104,v105\n"
"v_add_f32 v60,v60,v106\n"
MF "a[68:71],v[28:31],v[80:83],a[68:71]\n"
"v_add_f32 v60,v60,v107\n"
CV "v105,v106,v107\n"
MF "a[64:67],v[28:31],v[76:79],a[64:67]\n"
"v_exp_f32 v108,v108\n"
MF "a[44:47],v[24:27],v[76:79],a[44:47]\n"
"v_exp_f32 v109,v109\n"
MF "a[40:43],v[24:27],v[72:75],a[40:43]\n"
"v_exp_f32 v110,v110\n"
MF "a[60:63],v[28:31],v[72:75],a[60:63]\n"
"s_waitcnt lgkmcnt(0)\n"
TR "v[24:25],v4 offset:1024\n"
TR "v[26:27],v4 offset:7168\n"
MF "a[80:83],v[16:19],v[72:75],a[80:83]\n"
"s_nop 0\n"
TR "v[28:29],v5 offset:1024\n"
TR "v[30:31],v5 offset:7168\n"
MF "a[100:103],v[20:23],v[72:75],a[100:103]\n"
"v_exp_f32 v111,v111\n"
MF "a[104:107],v[20:23],v[76:79],a[104:107]\n"
"v_add_f32 v61,v56,v108\n"
"v_add_f32 v61,v61,v109\n"
MF "a[84:87],v[16:19],v[76:79],a[84:87]\n"
CV "v108,v108,v109\n"
"v_add_f32 v61,v61,v110\n"
MF "a[88:91],v[16:19],v[80:83],a[88:91]\n"
"v_add_f32 v61,v61,v111\n"
CV "v109,v110,v111\n"
MF "a[108:111],v[20:23],v[80:83],a[108:111]\n"
"v_exp_f32 v112,v112\n"
MF "a[112:115],v[20:23],v[84:87],a[112:115]\n"
"v_exp_f32 v113,v113\n"
MF "a[92:95],v[16:19],v[84:87],a[92:95]\n"
"v_exp_f32 v114,v114\n"
MF "a[96:99],v[16:19],v[88:91],a[96:99]\n"
"v_exp_f32 v115,v115\n"
MF "a[116:119],v[20:23],v[88:91],a[116:119]\n"
"s_waitcnt lgkmcnt(0)\n"
TR "v[16:17],v2 offset:2048\n"
TR "v[18:19],v2 offset:8192\n"
MF "a[136:139],v[24:27],v[88:91],a[136:139]\n"
"s_nop 0\n"
TR "v[20:21],v3 offset:2048\n"
TR "v[22:23],v3 offset:8192\n"
MF "a[156:159],v[28:31],v[88:91],a[156:159]\n"
"v_add_f32 v57,v57,v112\n"
"v_add_f32 v57,v57,v113\n"
MF "a[152:155],v[28:31],v[84:87],a[152:155]\n"
CV "v94,v112,v113\n"
"v_add_f32 v57,v57,v114\n"
MF "a[132:135],v[24:27],v[84:87],a[132:135]\n"
"v_add_f32 v57,v57,v115\n"
CV "v95,v114,v115\n"
MF "a[128:131],v[24:27],v[80:83],a[128:131]\n"
"v_exp_f32 v116,v116\n"
MF "a[148:151],v[28:31],v[80:83],a[148:151]\n"
"v_exp_f32 v117,v117\n"
MF "a[144:147],v[28:31],v[76:79],a[144:147]\n"
"v_exp_f32 v118,v118\n"
MF "a[124:127],v[24:27],v[76:79],a[124:127]\n"
"v_exp_f32 v119,v119\n"
MF "a[120:123],v[24:27],v[72:75],a[120:123]\n"
"v_add_f32 v58,v58,v116\n"
"v_add_f32 v58,v58,v117\n"
MF "a[140:143],v[28:31],v[72:75],a[140:143]\n"
"s_waitcnt lgkmcnt(0)\n"
TR "v[24:25],v4 offset:2048\n"
TR "v[26:27],v4 offset:8192\n"
MF "a[160:163],v[16:19],v[72:75],a[160:163]\n"
"s_nop 0\n"
TR "v[28:29],v5 offset:2048\n"
TR "v[30:31],v5 offset:8192\n"
MF "a[180:183],v[20:23],v[72:75],a[180:183]\n"
CV "v98,v116,v117\n"
"v_add_f32 v58,v58,v118\n"
MF "a[184:187],v[20:23],v[76:79],a[184:187]\n"
"v_add_f32 v58,v58,v119\n"
CV "v99,v118,v119\n"
MF "a[164:167],v[16:19],v[76:79],a[164:167]\n"
"v_exp_f32 v120,v120\n"
MF "a[168:171],v[16:19],v[80:83],a[168:171]\n"
"v_exp_f32 v121,v121\n"
MF "a[188:191],v[20:23],v[80:83],a[188:191]\n"
"v_exp_f32 v122,v122\n"
MF "a[192:195],v[20:23],v[84:87],a[192:195]\n"
"v_exp_f32 v123,v123\n"
MF "a[172:175],v[16:19],v[84:87],a[172:175]\n"
"s_waitcnt vmcnt(6) lgkmcnt(0)\n"
"v_add_f32 v59,v59,v120\n"
"v_add_f32 v59,v59,v121\n"
MF "a[176:179],v[16:19],v[88:91],a[176:179]\n"
"s_barrier\n"
MF "a[196:199],v[20:23],v[88:91],a[196:199]\n"
"s_add_u32 s72,s72,6144\n"
"ds_read_b128 v[16:19],v0 offset:24576\n"
"ds_read_b128 v[20:23],v1 offset:24576\n"
MF "a[216:219],v[24:27],v[88:91],a[216:219]\n"
"s_cmp_eq_u32 s72,s17\n"
"s_cselect_b32 s72,s16,s72\n"
"s_add_u32 m0,s64,0\n"
MF "a[236:239],v[28:31],v[88:91],a[236:239]\n"
LD "v7,s[32:35],s72 offen nt lds\n"
MF "a[232:235],v[28:31],v[84:87],a[232:235]\n"
"s_add_u32 m0,s64,896\n"
CV "v102,v120,v121\n"
"v_add_f32 v59,v59,v122\n"
MF "a[212:215],v[24:27],v[84:87],a[212:215]\n"
LD "v7,s[32:35],s72 offen offset:128 nt lds\n"
MF "a[208:211],v[24:27],v[80:83],a[208:211]\n"
"s_add_u32 m0,s64,1792\n"
"v_add_f32 v59,v59,v123\n"
CV "v103,v122,v123\n"
MF "a[228:231],v[28:31],v[80:83],a[228:231]\n"
LD "v7,s[32:35],s72 offen offset:256 nt lds\n"
MF "a[224:227],v[28:31],v[76:79],a[224:227]\n"
MF "a[204:207],v[24:27],v[76:79],a[204:207]\n"
"s_waitcnt lgkmcnt(0)\n"
MF "a[200:203],v[24:27],v[72:75],a[200:203]\n"
"s_cmp_lg_u32 s61,0\n"
MF "a[220:223],v[28:31],v[72:75],a[220:223]\n"
"s_cbranch_scc1 .Laloop\n"
".Lepi2:\n"
"v_exp_f32 v124,v124\n"
"v_exp_f32 v125,v125\n"
"v_exp_f32 v126,v126\n"
"v_exp_f32 v127,v127\n"
"v_exp_f32 v128,v128\n"
"v_exp_f32 v129,v129\n"
"v_exp_f32 v130,v130\n"
"v_exp_f32 v131,v131\n"
"v_add_f32 v60,v60,v124\n"
"v_add_f32 v61,v61,v128\n"
"v_add_f32 v60,v60,v125\n"
"v_add_f32 v61,v61,v129\n"
"v_add_f32 v60,v60,v126\n"
"v_add_f32 v61,v61,v130\n"
"v_add_f32 v60,v60,v127\n"
"v_add_f32 v61,v61,v131\n"
CV "v106,v124,v125\n"
CV "v107,v126,v127\n"
CV "v110,v128,v129\n"
CV "v111,v130,v131\n"
"v_max3_f32 v10,v57,v58,v59\n"
"v_max3_f32 v10,v10,v60,v61\n"
"v_cmp_le_f32 vcc,0x47800000,v10\n"
"s_cbranch_vccnz .Lstub_17\n"
".Lback_17:\n"
"s_waitcnt vmcnt(3) lgkmcnt(0)\n"
"s_barrier\n"
"v_lshrrev_b32 v192,7,v9\n"
"v_mul_u32_u24 v192,896,v192\n"
"v_add_u32 v192,v192,v8\n"
"v_add_u32 v236,s9,v9\n"
"v_add_u32 v237,65536,v236\n"
"v_mov_b32 v10,1.0\n"
TR "v[160:161],v4 offset:50176\n"
TR "v[162:163],v4 offset:56320\n"
"v_mfma_f32_16x16x4_f32 v[16:19],v10,v57,0\n"
TR "v[164:165],v5 offset:50176\n"
TR "v[166:167],v5 offset:56320\n"
TR "v[168:169],v2 offset:51200\n"
TR "v[170:171],v2 offset:57344\n"
TR "v[172:173],v3 offset:51200\n"
TR "v[174:175],v3 offset:57344\n"
"v_mfma_f32_16x16x4_f32 v[20:23],v10,v58,0\n"
TR "v[176:177],v4 offset:51200\n"
TR "v[178:179],v4 offset:57344\n"
TR "v[180:181],v5 offset:51200\n"
TR "v[182:183],v5 offset:57344\n"
TR "v[136:137],v2 offset:49152\n"
TR "v[138:139],v2 offset:55296\n"
"v_mfma_f32_16x16x4_f32 v[24:27],v10,v59,0\n"
TR "v[140:141],v3 offset:49152\n"
TR "v[142:143],v3 offset:55296\n"
TR "v[144:145],v4 offset:49152\n"
TR "v[146:147],v4 offset:55296\n"
TR "v[148:149],v5 offset:49152\n"
TR "v[150:151],v5 offset:55296\n"
"v_mfma_f32_16x16x4_f32 v[28:31],v10,v60,0\n"
TR "v[152:153],v2 offset:50176\n"
TR "v[154:155],v2 offset:56320\n"
TR "v[156:157],v3 offset:50176\n"
TR "v[158:159],v3 offset:56320\n"
"v_mfma_f32_16x16x4_f32 v[32:35],v10,v61,0\n"
"s_add_u32 m0,s5,24576\n"
"s_nop 0\n"
LD "v192,s[48:51],s0 offen lds\n"
"v_add_u32 v193,9216,v192\n"
LD "v193,s[48:51],s0 offen offset:1024 lds\n"
"s_waitcnt lgkmcnt(15)\n"
MF "a[120:123],v[160:163],v[92:95],a[120:123]\n"
MF "a[140:143],v[164:167],v[92:95],a[140:143]\n"
"v_add_u32 v194,18432,v192\n"
LD "v194,s[48:51],s0 offen offset:2048 lds\n"
"v_add_u32 v195,27648,v192\n"
LD "v195,s[48:51],s0 offen offset:3072 lds\n"
"v_rcp_f32 v208,v16\n"
"v_rcp_f32 v209,v20\n"
"v_rcp_f32 v210,v24\n"
"v_rcp_f32 v211,v28\n"
"v_rcp_f32 v212,v32\n"
MF "a[124:127],v[160:163],v[96:99],a[124:127]\n"
"v_mul_f32_e64 v214,-s13,v208\n"
"v_mul_f32_e64 v215,-s13,v209\n"
"v_mul_f32_e64 v216,-s13,v210\n"
"v_mul_f32_e64 v217,-s13,v211\n"
"v_mul_f32_e64 v218,-s13,v212\n"
"s_add_u32 m0,s5,28672\n"
"v_add_u32 v196,40960,v192\n"
LD "v196,s[48:51],s0 offen lds\n"
AR "v72,a120\n"
AR "v73,a121\n"
AR "v74,a122\n"
MF "a[144:147],v[164:167],v[96:99],a[144:147]\n"
AR "v75,a123\n"
AR "v76,a140\n"
AR "v77,a141\n"
AR "v78,a142\n"
AR "v79,a143\n"
"v_pk_fma_f32 v[72:73],v[72:73],v[208:209],v[214:215] op_sel:[0,0,0] op_sel_hi:[1,0,0]\n"
"v_pk_fma_f32 v[74:75],v[74:75],v[208:209],v[214:215] op_sel:[0,0,0] op_sel_hi:[1,0,0]\n"
"v_pk_fma_f32 v[76:77],v[76:77],v[208:209],v[214:215] op_sel:[0,0,0] op_sel_hi:[1,0,0]\n"
"v_pk_fma_f32 v[78:79],v[78:79],v[208:209],v[214:215] op_sel:[0,0,0] op_sel_hi:[1,0,0]\n"
CV "v112,v72,v73\n"
CV "v113,v74,v75\n"
CV "v114,v76,v77\n"
CV "v115,v78,v79\n"
MF "a[128:131],v[160:163],v[100:103],a[128:131]\n"
"ds_read_b128 a[140:143],v237 offset:20480\n"
AW "a120,v112\n"
AW "a121,v113\n"
AW "a122,v114\n"
AW "a123,v115\n"
"s_waitcnt lgkmcnt(7)\n"
MF "a[44:47],v[144:147],v[96:99],a[44:47]\n"
"v_add_u32 v197,50176,v192\n"
LD "v197,s[48:51],s0 offen offset:1024 lds\n"
AR "v80,a124\n"
AR "v81,a125\n"
AR "v82,a126\n"
MF "a[148:151],v[164:167],v[100:103],a[148:151]\n"
AR "v83,a127\n"
AR "v84,a144\n"
AR "v85,a145\n"
"s_nop 0\n"
AR "v86,a146\n"
AR "v87,a147\n"
"v_pk_fma_f32 v[80:81],v[80:81],v[208:209],v[214:215] op_sel:[0,1,1] op_sel_hi:[1,1,1]\n"
"v_pk_fma_f32 v[82:83],v[82:83],v[208:209],v[214:215] op_sel:[0,1,1] op_sel_hi:[1,1,1]\n"
"v_pk_fma_f32 v[84:85],v[84:85],v[208:209],v[214:215] op_sel:[0,1,1] op_sel_hi:[1,1,1]\n"
"v_pk_fma_f32 v[86:87],v[86:87],v[208:209],v[214:215] op_sel:[0,1,1] op_sel_hi:[1,1,1]\n"
CV "v116,v80,v81\n"
CV "v117,v82,v83\n"
CV "v118,v84,v85\n"
CV "v119,v86,v87\n"
MF "a[132:135],v[160:163],v[104:107],a[132:135]\n"
"ds_read_b128 a[144:147],v236\n"
AW "a124,v116\n"
AW "a125,v117\n"
AW "a126,v118\n"
AW "a127,v119\n"
"s_waitcnt lgkmcnt(6)\n"
MF "a[64:67],v[148:151],v[96:99],a[64:67]\n"
"s_add_u32 m0,s5,0\n"
"s_nop 0\n"
LD "v192,s[48:51],s1 offen lds\n"
AR "v72,a128\n"
AR "v73,a129\n"
AR "v74,a130\n"
MF "a[152:155],v[164:167],v[104:107],a[152:155]\n"
AR "v75,a131\n"
AR "v76,a148\n"
AR "v77,a149\n"
"s_nop 0\n"
AR "v78,a150\n"
AR "v79,a151\n"
"v_pk_fma_f32 v[72:73],v[72:73],v[210:211],v[216:217] op_sel:[0,0,0] op_sel_hi:[1,0,0]\n"
"v_pk_fma_f32 v[74:75],v[74:75],v[210:211],v[216:217] op_sel:[0,0,0] op_sel_hi:[1,0,0]\n"
"v_pk_fma_f32 v[76:77],v[76:77],v[210:211],v[216:217] op_sel:[0,0,0] op_sel_hi:[1,0,0]\n"
"v_pk_fma_f32 v[78:79],v[78:79],v[210:211],v[216:217] op_sel:[0,0,0] op_sel_hi:[1,0,0]\n"
LD "v193,s[48:51],s1 offen offset:1024 lds\n"
CV "v112,v72,v73\n"
CV "v113,v74,v75\n"
CV "v114,v76,v77\n"
CV "v115,v78,v79\n"
MF "a[136:139],v[160:163],v[108:111],a[136:139]\n"
"ds_read_b128 a[148:151],v236 offset:4096\n"
AW "a128,v112\n"
AW "a129,v113\n"
AW "a130,v114\n"
AW "a131,v115\n"
"s_waitcnt lgkmcnt(5)\n"
MF "a[84:87],v[152:155],v[96:99],a[84:87]\n"
LD "v194,s[48:51],s1 offen offset:2048 lds\n"
AR "v80,a132\n"
AR "v81,a133\n"
AR "v82,a134\n"
MF "a[156:159],v[164:167],v[108:111],a[156:159]\n"
AR "v83,a135\n"
AR "v84,a152\n"
AR "v85,a153\n"
"s_nop 0\n"
AR "v86,a154\n"
AR "v87,a155\n"
"v_pk_fma_f32 v[80:81],v[80:81],v[210:211],v[216:217] op_sel:[0,1,1] op_sel_hi:[1,1,1]\n"
"v_pk_fma_f32 v[82:83],v[82:83],v[210:211],v[216:217] op_sel:[0,1,1] op_sel_hi:[1,1,1]\n"
"v_pk_fma_f32 v[84:85],v[84:85],v[210:211],v[216:217] op_sel:[0,1,1] op_sel_hi:[1,1,1]\n"
"v_pk_fma_f32 v[86:87],v[86:87],v[210:211],v[216:217] op_sel:[0,1,1] op_sel_hi:[1,1,1]\n"
CV "v116,v80,v81\n"
CV "v117,v82,v83\n"
CV "v118,v84,v85\n"
CV "v119,v86,v87\n"
MF "a[160:163],v[168:171],v[92:95],a[160:163]\n"
"ds_read_b128 a[152:155],v236 offset:8192\n"
AW "a132,v116\n"
AW "a133,v117\n"
AW "a134,v118\n"
AW "a135,v119\n"
"s_waitcnt lgkmcnt(4)\n"
MF "a[104:107],v[156:159],v[96:99],a[104:107]\n"
LD "v195,s[48:51],s1 offen offset:3072 lds\n"
AR "v72,a136\n"
AR "v73,a137\n"
AR "v74,a138\n"
MF "a[180:183],v[172:175],v[92:95],a[180:183]\n"
AR "v75,a139\n"
AR "v76,a156\n"
AR "v77,a157\n"
"s_nop 0\n"
AR "v78,a158\n"
AR "v79,a159\n"
"v_pk_fma_f32 v[72:73],v[72:73],v[212:213],v[218:219] op_sel:[0,0,0] op_sel_hi:[1,0,0]\n"
"v_pk_fma_f32 v[74:75],v[74:75],v[212:213],v[218:219] op_sel:[0,0,0] op_sel_hi:[1,0,0]\n"
"v_pk_fma_f32 v[76:77],v[76:77],v[212:213],v[218:219] op_sel:[0,0,0] op_sel_hi:[1,0,0]\n"
"v_pk_fma_f32 v[78:79],v[78:79],v[212:213],v[218:219] op_sel:[0,0,0] op_sel_hi:[1,0,0]\n"
CV "v112,v72,v73\n"
CV "v113,v74,v75\n"
CV "v114,v76,v77\n"
CV "v115,v78,v79\n"
MF "a[164:167],v[168:171],v[96:99],a[164:167]\n"
"ds_read_b128 a[156:159],v236 offset:12288\n"
AW "a136,v112\n"
AW "a137,v113\n"
AW "a138,v114\n"
AW "a139,v115\n"
"v_max3_f32 v10,|v72|,|v73|,|v74|\n"
"v_max3_f32 v10,v10,|v75|,|v76|\n"
"v_max3_f32 v10,v10,|v77|,|v78|\n"
"v_max3_f32 v10,v10,|v78|,|v79|\n"
"v_mul_f32 v10,s10,v10\n"
"v_lshrrev_b32 v11,2,v9\n"
"v_readlane_b32 s12,v10,3\n"
"v_readlane_b32 s6,v10,22\n"
"v_readlane_b32 s7,v10,41\n"
"v_readlane_b32 s8,v10,60\n"
"v_add_u32 v11,s14,v11\n"
"s_min_u32 s12,s12,s6\n"
"s_min_u32 s7,s7,s8\n"
"s_min_u32 s12,s12,s7\n"
"s_and_b32 s12,s12,0x7f800000\n"
"s_sub_u32 s12,s12,0x14000000\n"
"s_cmp_lt_u32 s12,0x50000000\n"
"s_cselect_b32 s12,s12,0xe7400000\n"
"s_add_u32 s12,s12,0x18c00000\n"
"v_mov_b32 v10,s12\n"
"v_or_b32 v12,0x80000000,v10\n"
"ds_write2st64_b32 v11,v10,v12 offset1:1\n"
MF "a[8:11],v[136:139],v[100:103],a[8:11]\n"
"s_add_u32 m0,s5,4096\n"
"s_nop 0\n"
LD "v196,s[48:51],s1 offen lds\n"
AR "v80,a160\n"
AR "v81,a161\n"
AR "v82,a162\n"
MF "a[184:187],v[172:175],v[96:99],a[184:187]\n"
AR "v83,a163\n"
AR "v84,a180\n"
AR "v85,a181\n"
AR "v86,a182\n"
AR "v87,a183\n"
"v_pk_fma_f32 v[80:81],v[80:81],v[208:209],v[214:215] op_sel:[0,0,0] op_sel_hi:[1,0,0]\n"
"v_pk_fma_f32 v[82:83],v[82:83],v[208:209],v[214:215] op_sel:[0,0,0] op_sel_hi:[1,0,0]\n"
"v_pk_fma_f32 v[84:85],v[84:85],v[208:209],v[214:215] op_sel:[0,0,0] op_sel_hi:[1,0,0]\n"
"v_pk_fma_f32 v[86:87],v[86:87],v[208:209],v[214:215] op_sel:[0,0,0] op_sel_hi:[1,0,0]\n"
LD "v197,s[48:51],s1 offen offset:1024 lds\n"
CV "v116,v80,v81\n"
CV "v117,v82,v83\n"
CV "v118,v84,v85\n"
CV "v119,v86,v87\n"
MF "a[168:171],v[168:171],v[100:103],a[168:171]\n"
"ds_read_b128 a[180:183],v236 offset:16384\n"
AW "a160,v116\n"
AW "a161,v117\n"
AW "a162,v118\n"
AW "a163,v119\n"
MF "a[28:31],v[140:143],v[100:103],a[28:31]\n"
AR "v72,a164\n"
AR "v73,a165\n"
AR "v74,a166\n"
MF "a[188:191],v[172:175],v[100:103],a[188:191]\n"
AR "v75,a167\n"
AR "v76,a184\n"
AR "v77,a185\n"
AR "v78,a186\n"
AR "v79,a187\n"
"v_pk_fma_f32 v[72:73],v[72:73],v[208:209],v[214:215] op_sel:[0,1,1] op_sel_hi:[1,1,1]\n"
"v_pk_fma_f32 v[74:75],v[74:75],v[208:209],v[214:215] op_sel:[0,1,1] op_sel_hi:[1,1,1]\n"
"v_pk_fma_f32 v[76:77],v[76:77],v[208:209],v[214:215] op_sel:[0,1,1] op_sel_hi:[1,1,1]\n"
"v_pk_fma_f32 v[78:79],v[78:79],v[208:209],v[214:215] op_sel:[0,1,1] op_sel_hi:[1,1,1]\n"
CV "v112,v72,v73\n"
CV "v113,v74,v75\n"
CV "v114,v76,v77\n"
CV "v115,v78,v79\n"
MF "a[172:175],v[168:171],v[104:107],a[172:175]\n"
"ds_read_b128 a[184:187],v236 offset:20480\n"
AW "a164,v112\n"
AW "a165,v113\n"
AW "a166,v114\n"
AW "a167,v115\n"
MF "a[48:51],v[144:147],v[100:103],a[48:51]\n"
"s_waitcnt lgkmcnt(0)\n"
"s_barrier\n"
"s_add_u32 m0,s5,49152\n"
"s_nop 0\n"
"s_nop 0\n"
LD "v192,s[48:51],s2 offen lds\n"
AR "v80,a168\n"
AR "v81,a169\n"
AR "v82,a170\n"
MF "a[192:195],v[172:175],v[104:107],a[192:195]\n"
AR "v83,a171\n"
AR "v84,a188\n"
AR "v85,a189\n"
AR "v86,a190\n"
AR "v87,a191\n"
"v_pk_fma_f32 v[80:81],v[80:81],v[210:211],v[216:217] op_sel:[0,0,0] op_sel_hi:[1,0,0]\n"
"v_pk_fma_f32 v[82:83],v[82:83],v[210:211],v[216:217] op_sel:[0,0,0] op_sel_hi:[1,0,0]\n"
"v_pk_fma_f32 v[84:85],v[84:85],v[210:211],v[216:217] op_sel:[0,0,0] op_sel_hi:[1,0,0]\n"
"v_pk_fma_f32 v[86:87],v[86:87],v[210:211],v[216:217] op_sel:[0,0,0] op_sel_hi:[1,0,0]\n"
LD "v193,s[48:51],s2 offen offset:1024 lds\n"
CV "v116,v80,v81\n"
CV "v117,v82,v83\n"
CV "v118,v84,v85\n"
CV "v119,v86,v87\n"
MF "a[176:179],v[168:171],v[108:111],a[176:179]\n"
"v_accvgpr_mov_b32 a188,a240\n"
"v_accvgpr_mov_b32 a189,a241\n"
"v_accvgpr_mov_b32 a190,a242\n"
"v_accvgpr_mov_b32 a191,a243\n"
AW "a168,v116\n"
AW "a169,v117\n"
AW "a170,v118\n"
AW "a171,v119\n"
MF "a[68:71],v[148:151],v[100:103],a[68:71]\n"
LD "v194,s[48:51],s2 offen offset:2048 lds\n"
AR "v72,a172\n"
AR "v73,a173\n"
AR "v74,a174\n"
MF "a[196:199],v[172:175],v[108:111],a[196:199]\n"
AR "v75,a175\n"
AR "v76,a192\n"
AR "v77,a193\n"
AR "v78,a194\n"
AR "v79,a195\n"
"v_pk_fma_f32 v[72:73],v[72:73],v[210:211],v[216:217] op_sel:[0,1,1] op_sel_hi:[1,1,1]\n"
"v_pk_fma_f32 v[74:75],v[74:75],v[210:211],v[216:217] op_sel:[0,1,1] op_sel_hi:[1,1,1]\n"
"v_pk_fma_f32 v[76:77],v[76:77],v[210:211],v[216:217] op_sel:[0,1,1] op_sel_hi:[1,1,1]\n"
"v_pk_fma_f32 v[78:79],v[78:79],v[210:211],v[216:217] op_sel:[0,1,1] op_sel_hi:[1,1,1]\n"
LD "v195,s[48:51],s2 offen offset:3072 lds\n"
CV "v112,v72,v73\n"
CV "v113,v74,v75\n"
CV "v114,v76,v77\n"
CV "v115,v78,v79\n"
MF "a[200:203],v[176:179],v[92:95],a[200:203]\n"
"v_accvgpr_mov_b32 a192,a244\n"
"v_accvgpr_mov_b32 a193,a245\n"
"v_accvgpr_mov_b32 a194,a246\n"
"v_accvgpr_mov_b32 a195,a247\n"
AW "a172,v112\n"
AW "a173,v113\n"
AW "a174,v114\n"
AW "a175,v115\n"
MF "a[88:91],v[152:155],v[100:103],a[88:91]\n"
"s_add_u32 m0,s5,53248\n"
"s_nop 0\n"
LD "v196,s[48:51],s2 offen lds\n"
AR "v80,a176\n"
AR "v81,a177\n"
AR "v82,a178\n"
MF "a[220:223],v[180:183],v[92:95],a[220:223]\n"
AR "v83,a179\n"
AR "v84,a196\n"
AR "v85,a197\n"
"s_nop 0\n"
AR "v86,a198\n"
AR "v87,a199\n"
"v_pk_fma_f32 v[80:81],v[80:81],v[212:213],v[218:219] op_sel:[0,0,0] op_sel_hi:[1,0,0]\n"
"v_pk_fma_f32 v[82:83],v[82:83],v[212:213],v[218:219] op_sel:[0,0,0] op_sel_hi:[1,0,0]\n"
"v_pk_fma_f32 v[84:85],v[84:85],v[212:213],v[218:219] op_sel:[0,0,0] op_sel_hi:[1,0,0]\n"
"v_pk_fma_f32 v[86:87],v[86:87],v[212:213],v[218:219] op_sel:[0,0,0] op_sel_hi:[1,0,0]\n"
CV "v116,v80,v81\n"
CV "v117,v82,v83\n"
CV "v118,v84,v85\n"
CV "v119,v86,v87\n"
MF "a[204:207],v[176:179],v[96:99],a[204:207]\n"
"v_accvgpr_mov_b32 a196,a248\n"
"v_accvgpr_mov_b32 a197,a249\n"
"v_accvgpr_mov_b32 a198,a250\n"
"v_accvgpr_mov_b32 a199,a251\n"
AW "a176,v116\n"
AW "a177,v117\n"
AW "a178,v118\n"
AW "a179,v119\n"
MF "a[108:111],v[156:159],v[100:103],a[108:111]\n"
LD "v197,s[48:51],s2 offen offset:1024 lds\n"
AR "v72,a200\n"
AR "v73,a201\n"
AR "v74,a202\n"
MF "a[224:227],v[180:183],v[96:99],a[224:227]\n"
AR "v75,a203\n"
AR "v76,a220\n"
AR "v77,a221\n"
AR "v78,a222\n"
AR "v79,a223\n"
"v_pk_fma_f32 v[72:73],v[72:73],v[208:209],v[214:215] op_sel:[0,0,0] op_sel_hi:[1,0,0]\n"
"v_pk_fma_f32 v[74:75],v[74:75],v[208:209],v[214:215] op_sel:[0,0,0] op_sel_hi:[1,0,0]\n"
"v_pk_fma_f32 v[76:77],v[76:77],v[208:209],v[214:215] op_sel:[0,0,0] op_sel_hi:[1,0,0]\n"
"v_pk_fma_f32 v[78:79],v[78:79],v[208:209],v[214:215] op_sel:[0,0,0] op_sel_hi:[1,0,0]\n"
CV "v112,v72,v73\n"
CV "v113,v74,v75\n"
CV "v114,v76,v77\n"
CV "v115,v78,v79\n"
MF "a[208:211],v[176:179],v[100:103],a[208:211]\n"
AW "a220,v62\n"
AW "a221,v63\n"
AW "a222,v64\n"
AW "a223,v65\n"
AW "a200,v112\n"
AW "a201,v113\n"
AW "a202,v114\n"
AW "a203,v115\n"
MF "a[12:15],v[136:139],v[104:107],a[12:15]\n"
"s_add_u32 m0,s5,73728\n"
"s_nop 0\n"
LD "v192,s[48:51],s3 offen lds\n"
AR "v80,a204\n"
AR "v81,a205\n"
AR "v82,a206\n"
MF "a[228:231],v[180:183],v[100:103],a[228:231]\n"
AR "v83,a207\n"
AR "v84,a224\n"
AR "v85,a225\n"
"s_nop 0\n"
AR "v86,a226\n"
AR "v87,a227\n"
"v_pk_fma_f32 v[80:81],v[80:81],v[208:209],v[214:215] op_sel:[0,1,1] op_sel_hi:[1,1,1]\n"
"v_pk_fma_f32 v[82:83],v[82:83],v[208:209],v[214:215] op_sel:[0,1,1] op_sel_hi:[1,1,1]\n"
"v_pk_fma_f32 v[84:85],v[84:85],v[208:209],v[214:215] op_sel:[0,1,1] op_sel_hi:[1,1,1]\n"
"v_pk_fma_f32 v[86:87],v[86:87],v[208:209],v[214:215] op_sel:[0,1,1] op_sel_hi:[1,1,1]\n"
LD "v193,s[48:51],s3 offen offset:1024 lds\n"
CV "v116,v80,v81\n"
CV "v117,v82,v83\n"
CV "v118,v84,v85\n"
CV "v119,v86,v87\n"
MF "a[212:215],v[176:179],v[104:107],a[212:215]\n"
AW "a224,v66\n"
AW "a225,v67\n"
AW "a226,v68\n"
AW "a227,v69\n"
AW "a204,v116\n"
AW "a205,v117\n"
AW "a206,v118\n"
AW "a207,v119\n"
MF "a[32:35],v[140:143],v[104:107],a[32:35]\n"
LD "v194,s[48:51],s3 offen offset:2048 lds\n"
AR "v72,a208\n"
AR "v73,a209\n"
AR "v74,a210\n"
MF "a[232:235],v[180:183],v[104:107],a[232:235]\n"
AR "v75,a211\n"
AR "v76,a228\n"
AR "v77,a229\n"
AR "v78,a230\n"
AR "v79,a231\n"
"v_pk_fma_f32 v[72:73],v[72:73],v[210:211],v[216:217] op_sel:[0,0,0] op_sel_hi:[1,0,0]\n"
"v_pk_fma_f32 v[74:75],v[74:75],v[210:211],v[216:217] op_sel:[0,0,0] op_sel_hi:[1,0,0]\n"
"v_pk_fma_f32 v[76:77],v[76:77],v[210:211],v[216:217] op_sel:[0,0,0] op_sel_hi:[1,0,0]\n"
"v_pk_fma_f32 v[78:79],v[78:79],v[210:211],v[216:217] op_sel:[0,0,0] op_sel_hi:[1,0,0]\n"
CV "v112,v72,v73\n"
CV "v113,v74,v75\n"
CV "v114,v76,v77\n"
CV "v115,v78,v79\n"
MF "a[216:219],v[176:179],v[108:111],a[216:219]\n"
AW "a228,v70\n"
AW "a229,v71\n"
AW "a230,v132\n"
AW "a231,v133\n"
AW "a208,v112\n"
AW "a209,v113\n"
AW "a210,v114\n"
AW "a211,v115\n"
MF "a[52:55],v[144:147],v[104:107],a[52:55]\n"
LD "v195,s[48:51],s3 offen offset:3072 lds\n"
AR "v80,a212\n"
AR "v81,a213\n"
AR "v82,a214\n"
MF "a[236:239],v[180:183],v[108:111],a[236:239]\n"
AR "v83,a215\n"
AR "v84,a232\n"
AR "v85,a233\n"
AR "v86,a234\n"
AR "v87,a235\n"
"v_pk_fma_f32 v[80:81],v[80:81],v[210:211],v[216:217] op_sel:[0,1,1] op_sel_hi:[1,1,1]\n"
"v_pk_fma_f32 v[82:83],v[82:83],v[210:211],v[216:217] op_sel:[0,1,1] op_sel_hi:[1,1,1]\n"
"v_pk_fma_f32 v[84:85],v[84:85],v[210:211],v[216:217] op_sel:[0,1,1] op_sel_hi:[1,1,1]\n"
"v_pk_fma_f32 v[86:87],v[86:87],v[210:211],v[216:217] op_sel:[0,1,1] op_sel_hi:[1,1,1]\n"
CV "v116,v80,v81\n"
CV "v117,v82,v83\n"
CV "v118,v84,v85\n"
CV "v119,v86,v87\n"
MF "a[0:3],v[136:139],v[92:95],a[0:3]\n"
AW "a232,v134\n"
AW "a233,v135\n"
AW "a234,v6\n"
AW "a235,v13\n"
AW "a212,v116\n"
AW "a213,v117\n"
AW "a214,v118\n"
AW "a215,v119\n"
MF "a[72:75],v[148:151],v[104:107],a[72:75]\n"
"s_add_u32 m0,s5,77824\n"
"s_nop 0\n"
LD "v196,s[48:51],s3 offen lds\n"
AR "v72,a216\n"
AR "v73,a217\n"
AR "v74,a218\n"
MF "a[20:23],v[140:143],v[92:95],a[20:23]\n"
AR "v75,a219\n"
AR "v76,a236\n"
AR "v77,a237\n"
"s_nop 0\n"
AR "v78,a238\n"
AR "v79,a239\n"
"v_pk_fma_f32 v[72:73],v[72:73],v[212:213],v[218:219] op_sel:[0,0,0] op_sel_hi:[1,0,0]\n"
"v_pk_fma_f32 v[74:75],v[74:75],v[212:213],v[218:219] op_sel:[0,0,0] op_sel_hi:[1,0,0]\n"
"v_pk_fma_f32 v[76:77],v[76:77],v[212:213],v[218:219] op_sel:[0,0,0] op_sel_hi:[1,0,0]\n"
"v_pk_fma_f32 v[78:79],v[78:79],v[212:213],v[218:219] op_sel:[0,0,0] op_sel_hi:[1,0,0]\n"
LD "v197,s[48:51],s3 offen offset:1024 lds\n"
CV "v112,v72,v73\n"
CV "v113,v74,v75\n"
CV "v114,v76,v77\n"
CV "v115,v78,v79\n"
MF "a[40:43],v[144:147],v[92:95],a[40:43]\n"
AW "a236,v14\n"
AW "a237,v15\n"
"v_accvgpr_mov_b32 a238,a254\n"
".Lpg21h:\n"
"s_cbranch_execz .Lpg25h\n"
"s_nop 0\n"
"v_accvgpr_mov_b32 a239,a255\n"
AW "a216,v112\n"
AW "a217,v113\n"
AW "a218,v114\n"
AW "a219,v115\n"
MF "a[92:95],v[152:155],v[104:107],a[92:95]\n"
AR "v80,a0\n"
AR "v81,a1\n"
AR "v82,a2\n"
MF "a[60:63],v[148:151],v[92:95],a[60:63]\n"
AR "v83,a3\n"
AR "v84,a20\n"
AR "v85,a21\n"
AR "v86,a22\n"
AR "v87,a23\n"
"v_pk_fma_f32 v[80:81],v[80:81],v[208:209],v[214:215] op_sel:[0,0,0] op_sel_hi:[1,0,0]\n"
"v_pk_fma_f32 v[82:83],v[82:83],v[208:209],v[214:215] op_sel:[0,0,0] op_sel_hi:[1,0,0]\n"
"v_pk_fma_f32 v[84:85],v[84:85],v[208:209],v[214:215] op_sel:[0,0,0] op_sel_hi:[1,0,0]\n"
"v_pk_fma_f32 v[86:87],v[86:87],v[208:209],v[214:215] op_sel:[0,0,0] op_sel_hi:[1,0,0]\n"
CV "v116,v80,v81\n"
CV "v117,v82,v83\n"
CV "v118,v84,v85\n"
CV "v119,v86,v87\n"
MF "a[80:83],v[152:155],v[92:95],a[80:83]\n"
"ds_read_b128 a[20:23],v236 offset:24576\n"
AW "a0,v116\n"
AW "a1,v117\n"
AW "a2,v118\n"
AW "a3,v119\n"
MF "a[112:115],v[156:159],v[104:107],a[112:115]\n"
AR "v72,a40\n"
AR "v73,a41\n"
AR "v74,a42\n"
MF "a[100:103],v[156:159],v[92:95],a[100:103]\n"
AR "v75,a43\n"
AR "v76,a60\n"
AR "v77,a61\n"
AR "v78,a62\n"
AR "v79,a63\n"
"v_pk_fma_f32 v[72:73],v[72:73],v[208:209],v[214:215] op_sel:[0,0,0] op_sel_hi:[1,0,0]\n"
"v_pk_fma_f32 v[74:75],v[74:75],v[208:209],v[214:215] op_sel:[0,0,0] op_sel_hi:[1,0,0]\n"
"v_pk_fma_f32 v[76:77],v[76:77],v[208:209],v[214:215] op_sel:[0,0,0] op_sel_hi:[1,0,0]\n"
"v_pk_fma_f32 v[78:79],v[78:79],v[208:209],v[214:215] op_sel:[0,0,0] op_sel_hi:[1,0,0]\n"
CV "v112,v72,v73\n"
CV "v113,v74,v75\n"
CV "v114,v76,v77\n"
CV "v115,v78,v79\n"
MF "a[4:7],v[136:139],v[96:99],a[4:7]\n"
"ds_read_b128 a[60:63],v236 offset:45056\n"
AW "a40,v112\n"
AW "a41,v113\n"
AW "a42,v114\n"
AW "a43,v115\n"
MF "a[16:19],v[136:139],v[108:111],a[16:19]\n"
AR "v80,a80\n"
AR "v81,a81\n"
AR "v82,a82\n"
MF "a[24:27],v[140:143],v[96:99],a[24:27]\n"
AR "v83,a83\n"
AR "v84,a100\n"
AR "v85,a101\n"
AR "v86,a102\n"
AR "v87,a103\n"
"v_pk_fma_f32 v[80:81],v[80:81],v[208:209],v[214:215] op_sel:[0,0,0] op_sel_hi:[1,0,0]\n"
"v_pk_fma_f32 v[82:83],v[82:83],v[208:209],v[214:215] op_sel:[0,0,0] op_sel_hi:[1,0,0]\n"
"v_pk_fma_f32 v[84:85],v[84:85],v[208:209],v[214:215] op_sel:[0,0,0] op_sel_hi:[1,0,0]\n"
"v_pk_fma_f32 v[86:87],v[86:87],v[208:209],v[214:215] op_sel:[0,0,0] op_sel_hi:[1,0,0]\n"
CV "v116,v80,v81\n"
CV "v117,v82,v83\n"
CV "v118,v84,v85\n"
CV "v119,v86,v87\n"
MF "a[36:39],v[140:143],v[108:111],a[36:39]\n"
"ds_read_b128 a[100:103],v237\n"
AW "a80,v116\n"
AW "a81,v117\n"
AW "a82,v118\n"
AW "a83,v119\n"
MF "a[56:59],v[144:147],v[108:111],a[56:59]\n"
AR "v72,a4\n"
AR "v73,a5\n"
AR "v74,a6\n"
MF "a[76:79],v[148:151],v[108:111],a[76:79]\n"
AR "v75,a7\n"
AR "v76,a24\n"
AR "v77,a25\n"
AR "v78,a26\n"
AR "v79,a27\n"
"v_pk_fma_f32 v[72:73],v[72:73],v[208:209],v[214:215] op_sel:[0,1,1] op_sel_hi:[1,1,1]\n"
"v_pk_fma_f32 v[74:75],v[74:75],v[208:209],v[214:215] op_sel:[0,1,1] op_sel_hi:[1,1,1]\n"
"v_pk_fma_f32 v[76:77],v[76:77],v[208:209],v[214:215] op_sel:[0,1,1] op_sel_hi:[1,1,1]\n"
"v_pk_fma_f32 v[78:79],v[78:79],v[208:209],v[214:215] op_sel:[0,1,1] op_sel_hi:[1,1,1]\n"
CV "v112,v72,v73\n"
CV "v113,v74,v75\n"
CV "v114,v76,v77\n"
CV "v115,v78,v79\n"
MF "a[96:99],v[152:155],v[108:111],a[96:99]\n"
"ds_read_b128 a[24:27],v236 offset:28672\n"
AW "a4,v112\n"
AW "a5,v113\n"
AW "a6,v114\n"
AW "a7,v115\n"
MF "a[116:119],v[156:159],v[108:111],a[116:119]\n"
"v_and_b32 v0,512,v9\n"
"v_lshlrev_b32 v0,2,v0\n"
"v_sub_u32 v0,v2,v0\n"
"v_xor_b32 v0,s4,v0\n"
"v_mov_b32 v252,s15\n"
"s_waitcnt vmcnt(12) lgkmcnt(0)\n"
"s_barrier\n"
"ds_read_b128 v[252:255],v252\n"
TR "v[176:177],v0 offset:24576\n"
TR "v[178:179],v0 offset:26624\n"
TR "v[180:181],v0 offset:28672\n"
TR "v[182:183],v0 offset:30720\n"
TR "v[184:185],v0 offset:32768\n"
TR "v[186:187],v0 offset:34816\n"
"v_xor_b32 v1,32,v0\n"
"v_xor_b32 v2,64,v0\n"
"v_xor_b32 v3,96,v0\n"
"v_add_u32 v4,73728,v0\n"
"v_add_u32 v5,73728,v1\n"
"v_add_u32 v6,73728,v2\n"
"v_add_u32 v7,73728,v3\n"
"s_cmp_ge_u32 s56,2\n"
"s_cbranch_scc1 .Lrole1\n"
"s_waitcnt lgkmcnt(4)\n"
MF "v[16:19],a[120:123],v[176:179],v[252:255]\n"
"s_mov_b32 s65,128\n"
TR "v[188:189],v1 offset:24576\n"
AR "v220,a44\n"
"s_waitcnt lgkmcnt(3)\n"
MF "v[16:19],a[160:163],v[180:183],v[16:19]\n"
"s_mov_b32 s9,0x3f80\n"
TR "v[190:191],v1 offset:26624\n"
AR "v221,a45\n"
"s_waitcnt lgkmcnt(2)\n"
MF "v[16:19],a[200:203],v[184:187],v[16:19]\n"
"s_add_u32 s89,s89,s65\n"
AR "v222,a46\n"
AR "v223,a47\n"
MF "v[24:27],a[204:207],v[184:187],v[252:255]\n"
"s_bfe_u32 s58,s22,0x30014\n"
TR "a[240:241],v1 offset:28672\n"
AR "v224,a64\n"
MF "v[24:27],a[164:167],v[180:183],v[24:27]\n"
"s_add_u32 s61,s58,1\n"
TR "a[242:243],v1 offset:30720\n"
AR "v225,a65\n"
MF "v[24:27],a[124:127],v[176:179],v[24:27]\n"
"s_and_b32 s61,s61,7\n"
AR "v226,a66\n"
AR "v227,a67\n"
MF "v[32:35],a[128:131],v[176:179],v[252:255]\n"
"s_mov_b32 s64,9\n"
TR "a[244:245],v1 offset:32768\n"
"ds_read_b128 a[64:67],v236 offset:49152\n"
MF "v[32:35],a[168:171],v[180:183],v[32:35]\n"
"s_xor_b32 s67,s78,8\n"
TR "a[246:247],v1 offset:34816\n"
"v_fma_f32 v220,v220,v209,v215\n"
MF "v[32:35],a[208:211],v[184:187],v[32:35]\n"
"s_mov_b32 s71,204800\n"
"v_fma_f32 v221,v221,v209,v215\n"
"v_fma_f32 v222,v222,v209,v215\n"
MF "v[40:43],a[212:215],v[184:187],v[252:255]\n"
"s_mov_b32 s66,204800\n"
"v_fma_f32 v223,v223,v209,v215\n"
"v_fma_f32 v224,v224,v209,v215\n"
MF "v[40:43],a[172:175],v[180:183],v[40:43]\n"
"s_and_b32 s73,s56,1\n"
"v_fma_f32 v225,v225,v209,v215\n"
"v_fma_f32 v226,v226,v209,v215\n"
MF "v[40:43],a[132:135],v[176:179],v[40:43]\n"
"s_mul_i32 s73,s73,5120\n"
"v_fma_f32 v227,v227,v209,v215\n"
CV "v220,v220,v221\n"
MF "v[48:51],a[136:139],v[176:179],v[252:255]\n"
"s_add_u32 s73,s73,s93\n"
CV "v221,v222,v223\n"
CV "v222,v224,v225\n"
MF "v[48:51],a[176:179],v[180:183],v[48:51]\n"
"s_xor_b32 s58,s58,4\n"
CV "v223,v226,v227\n"
AW "a44,v220\n"
MF "v[48:51],a[216:219],v[184:187],v[48:51]\n"
"s_waitcnt lgkmcnt(0)\n"
AW "a45,v221\n"
AW "a46,v222\n"
MF "v[52:55],a[216:219],a[244:247],v[252:255]\n"
"s_mul_i32 s70,s58,204800\n"
TR "v[176:177],v2 offset:24576\n"
AW "a47,v223\n"
MF "v[52:55],a[176:179],a[240:243],v[52:55]\n"
"s_add_u32 s70,s70,s73\n"
TR "v[178:179],v2 offset:26624\n"
AR "v228,a84\n"
MF "v[52:55],a[136:139],v[188:191],v[52:55]\n"
"s_add_u32 s70,s70,0xa000\n"
AR "v229,a85\n"
AR "v230,a86\n"
MF "v[44:47],a[132:135],v[188:191],v[252:255]\n"
"s_mov_b32 s85,1\n"
TR "v[180:181],v2 offset:28672\n"
AR "v231,a87\n"
MF "v[44:47],a[172:175],a[240:243],v[44:47]\n"
"s_mov_b32 s83,0xf0000000\n"
TR "v[182:183],v2 offset:30720\n"
AR "v232,a104\n"
MF "v[44:47],a[212:215],a[244:247],v[44:47]\n"
"s_mov_b32 s82,0xf0000000\n"
AR "v233,a105\n"
AR "v234,a106\n"
MF "v[36:39],a[208:211],a[244:247],v[252:255]\n"
"s_add_u32 s58,s79,73728\n"
TR "v[184:185],v2 offset:32768\n"
AR "v235,a107\n"
MF "v[36:39],a[168:171],a[240:243],v[36:39]\n"
"s_mov_b32 s4,64\n"
TR "v[186:187],v2 offset:34816\n"
"ds_read_b128 a[104:107],v237 offset:4096\n"
MF "v[36:39],a[128:131],v[188:191],v[36:39]\n"
"s_mul_i32 s95,s92,3277\n"
"v_fma_f32 v228,v228,v209,v215\n"
"v_fma_f32 v229,v229,v209,v215\n"
MF "v[28:31],a[124:127],v[188:191],v[252:255]\n"
"s_lshr_b32 s95,s95,16\n"
"v_fma_f32 v230,v230,v209,v215\n"
"v_fma_f32 v231,v231,v209,v215\n"
MF "v[28:31],a[164:167],a[240:243],v[28:31]\n"
"s_mul_i32 s59,s95,20\n"
"v_fma_f32 v232,v232,v209,v215\n"
"v_fma_f32 v233,v233,v209,v215\n"
MF "v[28:31],a[204:207],a[244:247],v[28:31]\n"
"s_sub_u32 s59,s92,s59\n"
"v_fma_f32 v234,v234,v209,v215\n"
"v_fma_f32 v235,v235,v209,v215\n"
MF "v[20:23],a[200:203],a[244:247],v[252:255]\n"
"s_cmp_ge_u32 s59,10\n"
"s_cselect_b32 s96,4294557760,0\n"
CV "v228,v228,v229\n"
MF "v[20:23],a[160:163],a[240:243],v[20:23]\n"
"s_add_u32 s96,s96,1024\n"
CV "v229,v230,v231\n"
CV "v230,v232,v233\n"
MF "v[20:23],a[120:123],v[188:191],v[20:23]\n"
"s_waitcnt lgkmcnt(1)\n"
CV "v231,v234,v235\n"
AW "a84,v228\n"
MF "v[56:59],a[120:123],v[176:179],v[252:255]\n"
"s_mul_i32 s59,s59,40960\n"
TR "v[188:189],v3 offset:24576\n"
AW "a85,v229\n"
MF "v[56:59],a[160:163],v[180:183],v[56:59]\n"
"s_add_u32 s96,s96,s59\n"
TR "v[190:191],v3 offset:26624\n"
AW "a86,v230\n"
MF "v[56:59],a[200:203],v[184:187],v[56:59]\n"
"s_lshl_b32 s59,s95,7\n"
AW "a87,v231\n"
AR "v220,a8\n"
MF "v[64:67],a[204:207],v[184:187],v[252:255]\n"
"s_add_u32 s96,s96,s59\n"
TR "a[240:241],v3 offset:28672\n"
AR "v221,a9\n"
MF "v[64:67],a[164:167],v[180:183],v[64:67]\n"
"s_lshl_b32 s2,s92,10\n"
TR "a[242:243],v3 offset:30720\n"
AR "v222,a10\n"
MF "v[64:67],a[124:127],v[176:179],v[64:67]\n"
"s_add_u32 s2,s2,s93\n"
AR "v223,a11\n"
AR "v224,a28\n"
MF "v[72:75],a[128:131],v[176:179],v[252:255]\n"
"s_mov_b32 s0,s96\n"
TR "a[244:245],v3 offset:32768\n"
AR "v225,a29\n"
MF "v[72:75],a[168:171],v[180:183],v[72:75]\n"
"s_sub_u32 s2,s2,204800\n"
TR "a[246:247],v3 offset:34816\n"
AR "v226,a30\n"
MF "v[72:75],a[208:211],v[184:187],v[72:75]\n"
"s_mov_b32 s92,64\n"
AR "v227,a31\n"
"v_fma_f32 v220,v220,v210,v216\n"
MF "v[80:83],a[212:215],v[184:187],v[252:255]\n"
"ds_read_b128 a[28:31],v236 offset:32768\n"
"v_fma_f32 v221,v221,v210,v216\n"
MF "v[80:83],a[172:175],v[180:183],v[80:83]\n"
"v_fma_f32 v222,v222,v210,v216\n"
"v_fma_f32 v223,v223,v210,v216\n"
MF "v[80:83],a[132:135],v[176:179],v[80:83]\n"
"v_fma_f32 v224,v224,v210,v216\n"
"v_fma_f32 v225,v225,v210,v216\n"
MF "v[88:91],a[136:139],v[176:179],v[252:255]\n"
"v_fma_f32 v226,v226,v210,v216\n"
"v_fma_f32 v227,v227,v210,v216\n"
MF "v[88:91],a[176:179],v[180:183],v[88:91]\n"
"s_waitcnt vmcnt(0) lgkmcnt(0)\n"
CV "v220,v220,v221\n"
CV "v221,v222,v223\n"
MF "v[88:91],a[216:219],v[184:187],v[88:91]\n"
"s_barrier\n"
MF "v[92:95],a[216:219],a[244:247],v[252:255]\n"
TR "v[176:177],v0\n"
CV "v222,v224,v225\n"
MF "v[92:95],a[176:179],a[240:243],v[92:95]\n"
TR "v[178:179],v0 offset:2048\n"
CV "v223,v226,v227\n"
MF "v[92:95],a[136:139],v[188:191],v[92:95]\n"
TR "v[96:97],v0 offset:49152\n"
AW "a8,v220\n"
MF "v[84:87],a[132:135],v[188:191],v[252:255]\n"
TR "v[180:181],v0 offset:4096\n"
AW "a9,v221\n"
MF "v[84:87],a[172:175],a[240:243],v[84:87]\n"
TR "v[182:183],v0 offset:6144\n"
AW "a10,v222\n"
MF "v[84:87],a[212:215],a[244:247],v[84:87]\n"
TR "v[98:99],v0 offset:51200\n"
AW "a11,v223\n"
MF "v[76:79],a[208:211],a[244:247],v[252:255]\n"
TR "v[184:185],v0 offset:8192\n"
AR "v228,a48\n"
MF "v[76:79],a[168:171],a[240:243],v[76:79]\n"
TR "v[186:187],v0 offset:10240\n"
AR "v229,a49\n"
MF "v[76:79],a[128:131],v[188:191],v[76:79]\n"
TR "v[100:101],v0 offset:53248\n"
AR "v230,a50\n"
MF "v[68:71],a[124:127],v[188:191],v[252:255]\n"
TR "v[102:103],v0 offset:55296\n"
AR "v231,a51\n"
MF "v[68:71],a[164:167],a[240:243],v[68:71]\n"
TR "v[104:105],v0 offset:57344\n"
AR "v232,a68\n"
MF "v[68:71],a[204:207],a[244:247],v[68:71]\n"
TR "v[106:107],v0 offset:59392\n"
AR "v233,a69\n"
MF "v[60:63],a[200:203],a[244:247],v[252:255]\n"
TR "v[108:109],v4\n"
AR "v234,a70\n"
MF "v[60:63],a[160:163],a[240:243],v[60:63]\n"
TR "v[110:111],v4 offset:2048\n"
AR "v235,a71\n"
MF "v[60:63],a[120:123],v[188:191],v[60:63]\n"
"s_waitcnt lgkmcnt(6)\n"
TR "v[112:113],v4 offset:4096\n"
"ds_read_b128 a[68:71],v236 offset:53248\n"
MF "v[16:19],a[140:143],v[176:179],v[16:19]\n"
TR "v[188:189],v1\n"
"v_fma_f32 v228,v228,v210,v216\n"
MF "v[16:19],a[180:183],v[180:183],v[16:19]\n"
TR "v[190:191],v1 offset:2048\n"
"v_fma_f32 v229,v229,v210,v216\n"
MF "v[16:19],a[220:223],v[184:187],v[16:19]\n"
TR "v[114:115],v4 offset:6144\n"
"v_fma_f32 v230,v230,v210,v216\n"
MF "v[24:27],a[224:227],v[184:187],v[24:27]\n"
TR "a[240:241],v1 offset:4096\n"
"v_fma_f32 v231,v231,v210,v216\n"
MF "v[24:27],a[184:187],v[180:183],v[24:27]\n"
TR "a[242:243],v1 offset:6144\n"
"v_fma_f32 v232,v232,v210,v216\n"
MF "v[24:27],a[144:147],v[176:179],v[24:27]\n"
TR "v[116:117],v4 offset:8192\n"
"v_fma_f32 v233,v233,v210,v216\n"
MF "v[32:35],a[148:151],v[176:179],v[32:35]\n"
TR "a[244:245],v1 offset:8192\n"
"v_fma_f32 v234,v234,v210,v216\n"
MF "v[32:35],a[188:191],v[180:183],v[32:35]\n"
TR "a[246:247],v1 offset:10240\n"
"v_fma_f32 v235,v235,v210,v216\n"
MF "v[32:35],a[228:231],v[184:187],v[32:35]\n"
TR "v[118:119],v4 offset:10240\n"
CV "v228,v228,v229\n"
MF "v[40:43],a[232:235],v[184:187],v[40:43]\n"
TR "v[120:121],v1 offset:49152\n"
CV "v229,v230,v231\n"
MF "v[40:43],a[192:195],v[180:183],v[40:43]\n"
TR "v[122:123],v1 offset:51200\n"
CV "v230,v232,v233\n"
MF "v[40:43],a[152:155],v[176:179],v[40:43]\n"
TR "v[124:125],v1 offset:53248\n"
CV "v231,v234,v235\n"
MF "v[48:51],a[156:159],v[176:179],v[48:51]\n"
TR "v[126:127],v1 offset:55296\n"
AW "a48,v228\n"
MF "v[48:51],a[196:199],v[180:183],v[48:51]\n"
TR "v[128:129],v1 offset:57344\n"
AW "a49,v229\n"
MF "v[48:51],a[236:239],v[184:187],v[48:51]\n"
"s_waitcnt lgkmcnt(6)\n"
TR "v[130:131],v1 offset:59392\n"
AW "a50,v230\n"
MF "v[52:55],a[236:239],a[244:247],v[52:55]\n"
TR "v[176:177],v2\n"
AW "a51,v231\n"
MF "v[52:55],a[196:199],a[240:243],v[52:55]\n"
TR "v[178:179],v2 offset:2048\n"
AR "v220,a88\n"
MF "v[52:55],a[156:159],v[188:191],v[52:55]\n"
TR "v[132:133],v5\n"
AR "v221,a89\n"
MF "v[44:47],a[152:155],v[188:191],v[44:47]\n"
TR "v[180:181],v2 offset:4096\n"
AR "v222,a90\n"
MF "v[44:47],a[192:195],a[240:243],v[44:47]\n"
TR "v[182:183],v2 offset:6144\n"
AR "v223,a91\n"
MF "v[44:47],a[232:235],a[244:247],v[44:47]\n"
TR "v[134:135],v5 offset:2048\n"
AR "v224,a108\n"
MF "v[36:39],a[228:231],a[244:247],v[36:39]\n"
TR "v[184:185],v2 offset:8192\n"
AR "v225,a109\n"
MF "v[36:39],a[188:191],a[240:243],v[36:39]\n"
TR "v[186:187],v2 offset:10240\n"
AR "v226,a110\n"
MF "v[36:39],a[148:151],v[188:191],v[36:39]\n"
TR "v[136:137],v5 offset:4096\n"
AR "v227,a111\n"
MF "v[28:31],a[144:147],v[188:191],v[28:31]\n"
"s_nop 0\n"
TR "v[138:139],v5 offset:6144\n"
"ds_read_b128 a[108:111],v237 offset:8192\n"
MF "v[28:31],a[184:187],a[240:243],v[28:31]\n"
TR "v[140:141],v5 offset:8192\n"
"v_fma_f32 v220,v220,v210,v216\n"
MF "v[28:31],a[224:227],a[244:247],v[28:31]\n"
TR "v[142:143],v5 offset:10240\n"
"v_fma_f32 v221,v221,v210,v216\n"
MF "v[20:23],a[220:223],a[244:247],v[20:23]\n"
TR "v[144:145],v2 offset:49152\n"
"v_fma_f32 v222,v222,v210,v216\n"
MF "v[20:23],a[180:183],a[240:243],v[20:23]\n"
TR "v[146:147],v2 offset:51200\n"
"v_fma_f32 v223,v223,v210,v216\n"
MF "v[20:23],a[140:143],v[188:191],v[20:23]\n"
"s_waitcnt lgkmcnt(7)\n"
TR "v[148:149],v2 offset:53248\n"
"v_fma_f32 v224,v224,v210,v216\n"
MF "v[56:59],a[140:143],v[176:179],v[56:59]\n"
TR "v[188:189],v3\n"
"v_add_u32 v198,59392,v192\n"
MF "v[56:59],a[180:183],v[180:183],v[56:59]\n"
TR "v[190:191],v3 offset:2048\n"
"v_add_u32 v199,68608,v192\n"
MF "v[56:59],a[220:223],v[184:187],v[56:59]\n"
TR "v[150:151],v2 offset:55296\n"
"v_add_u32 v200,81920,v192\n"
"v_add_u32 v201,91136,v192\n"
MF "v[64:67],a[224:227],v[184:187],v[64:67]\n"
TR "a[240:241],v3 offset:4096\n"
"v_add_u32 v202,100352,v192\n"
MF "v[64:67],a[184:187],v[180:183],v[64:67]\n"
TR "a[242:243],v3 offset:6144\n"
"v_add_u32 v203,109568,v192\n"
MF "v[64:67],a[144:147],v[176:179],v[64:67]\n"
TR "v[152:153],v2 offset:57344\n"
"v_fma_f32 v225,v225,v210,v216\n"
MF "v[72:75],a[148:151],v[176:179],v[72:75]\n"
TR "a[244:245],v3 offset:8192\n"
"v_fma_f32 v226,v226,v210,v216\n"
MF "v[72:75],a[188:191],v[180:183],v[72:75]\n"
TR "a[246:247],v3 offset:10240\n"
"v_fma_f32 v227,v227,v210,v216\n"
MF "v[72:75],a[228:231],v[184:187],v[72:75]\n"
TR "v[154:155],v2 offset:59392\n"
CV "v220,v220,v221\n"
MF "v[80:83],a[232:235],v[184:187],v[80:83]\n"
TR "v[156:157],v6\n"
CV "v221,v222,v223\n"
MF "v[80:83],a[192:195],v[180:183],v[80:83]\n"
TR "v[158:159],v6 offset:2048\n"
CV "v222,v224,v225\n"
MF "v[80:83],a[152:155],v[176:179],v[80:83]\n"
TR "v[160:161],v6 offset:4096\n"
CV "v223,v226,v227\n"
MF "v[88:91],a[156:159],v[176:179],v[88:91]\n"
TR "v[162:163],v6 offset:6144\n"
AW "a88,v220\n"
MF "v[88:91],a[196:199],v[180:183],v[88:91]\n"
TR "v[164:165],v6 offset:8192\n"
"s_waitcnt lgkmcnt(0)\n"
MF "v[88:91],a[236:239],v[184:187],v[88:91]\n"
"s_barrier\n"
MF "v[92:95],a[236:239],a[244:247],v[92:95]\n"
"s_add_u32 s80,s89,122880\n"
"s_add_u32 m0,s79,0\n"
TR "v[166:167],v6 offset:10240\n"
MF "v[92:95],a[196:199],a[240:243],v[92:95]\n"
LD "v192,s[48:51],s80 offen lds\n"
MF "v[92:95],a[156:159],v[188:191],v[92:95]\n"
TR "v[168:169],v3 offset:49152\n"
AW "a89,v221\n"
MF "v[84:87],a[152:155],v[188:191],v[84:87]\n"
TR "v[170:171],v3 offset:51200\n"
AW "a90,v222\n"
MF "v[84:87],a[192:195],a[240:243],v[84:87]\n"
TR "v[172:173],v3 offset:53248\n"
AW "a91,v223\n"
MF "v[84:87],a[232:235],a[244:247],v[84:87]\n"
LD "v193,s[48:51],s80 offen offset:1024 lds\n"
MF "v[76:79],a[228:231],a[244:247],v[76:79]\n"
TR "v[174:175],v3 offset:55296\n"
AR "v228,a12\n"
MF "v[76:79],a[188:191],a[240:243],v[76:79]\n"
TR "v[204:205],v3 offset:57344\n"
AR "v229,a13\n"
MF "v[76:79],a[148:151],v[188:191],v[76:79]\n"
TR "v[206:207],v3 offset:59392\n"
AR "v230,a14\n"
MF "v[68:71],a[144:147],v[188:191],v[68:71]\n"
TR "v[244:245],v7\n"
AR "v231,a15\n"
MF "v[68:71],a[184:187],a[240:243],v[68:71]\n"
LD "v194,s[48:51],s80 offen offset:2048 lds\n"
MF "v[68:71],a[224:227],a[244:247],v[68:71]\n"
TR "v[246:247],v7 offset:2048\n"
AR "v232,a32\n"
MF "v[60:63],a[220:223],a[244:247],v[60:63]\n"
TR "v[248:249],v7 offset:4096\n"
AR "v233,a33\n"
MF "v[60:63],a[180:183],a[240:243],v[60:63]\n"
TR "v[250:251],v7 offset:6144\n"
AR "v234,a34\n"
MF "v[60:63],a[140:143],v[188:191],v[60:63]\n"
LD "v195,s[48:51],s80 offen offset:3072 lds\n"
MF "v[16:19],a[0:3],v[96:99],v[16:19]\n"
TR "v[238:239],v7 offset:8192\n"
AR "v235,a35\n"
MF "v[16:19],a[40:43],v[100:103],v[16:19]\n"
"s_nop 0\n"
TR "v[240:241],v7 offset:10240\n"
"ds_read_b128 a[32:35],v236 offset:36864\n"
MF "v[16:19],a[80:83],v[104:107],v[16:19]\n"
".Lpg22h:\n"
"s_cbranch_execz .Lpg26h\n"
"v_fma_f32 v228,v228,v211,v217\n"
"v_fma_f32 v229,v229,v211,v217\n"
MF "v[16:19],a[20:23],v[108:111],v[16:19]\n"
"s_add_u32 m0,s79,4096\n"
"v_fma_f32 v230,v230,v211,v217\n"
"v_fma_f32 v231,v231,v211,v217\n"
MF "v[16:19],a[60:63],v[112:115],v[16:19]\n"
LD "v196,s[48:51],s80 offen lds\n"
MF "v[16:19],a[100:103],v[116:119],v[16:19]\n"
"v_fma_f32 v232,v232,v211,v217\n"
"v_fma_f32 v233,v233,v211,v217\n"
MF "v[20:23],a[100:103],v[140:143],v[20:23]\n"
"v_fma_f32 v234,v234,v211,v217\n"
"v_fma_f32 v235,v235,v211,v217\n"
MF "v[20:23],a[60:63],v[136:139],v[20:23]\n"
CV "v228,v228,v229\n"
CV "v229,v230,v231\n"
MF "v[20:23],a[20:23],v[132:135],v[20:23]\n"
LD "v197,s[48:51],s80 offen offset:1024 lds\n"
MF "v[20:23],a[80:83],v[128:131],v[20:23]\n"
CV "v230,v232,v233\n"
CV "v231,v234,v235\n"
MF "v[20:23],a[40:43],v[124:127],v[20:23]\n"
AW "a12,v228\n"
AW "a13,v229\n"
MF "v[20:23],a[0:3],v[120:123],v[20:23]\n"
"s_waitcnt lgkmcnt(13)\n"
AW "a14,v230\n"
AW "a15,v231\n"
MF "v[56:59],a[0:3],v[144:147],v[56:59]\n"
AR "v220,a52\n"
AR "v221,a53\n"
MF "v[56:59],a[40:43],v[148:151],v[56:59]\n"
LD "v198,s[48:51],s80 offen offset:2048 lds\n"
MF "v[56:59],a[80:83],v[152:155],v[56:59]\n"
AR "v222,a54\n"
AR "v223,a55\n"
MF "v[56:59],a[20:23],v[156:159],v[56:59]\n"
AR "v224,a72\n"
AR "v225,a73\n"
MF "v[56:59],a[60:63],v[160:163],v[56:59]\n"
AR "v226,a74\n"
AR "v227,a75\n"
MF "v[56:59],a[100:103],v[164:167],v[56:59]\n"
"s_waitcnt lgkmcnt(1)\n"
"ds_read_b128 a[72:75],v236 offset:57344\n"
"v_fma_f32 v220,v220,v211,v217\n"
MF "v[60:63],a[100:103],v[238:241],v[60:63]\n"
LD "v199,s[48:51],s80 offen offset:3072 lds\n"
MF "v[60:63],a[60:63],v[248:251],v[60:63]\n"
"v_fma_f32 v221,v221,v211,v217\n"
"v_fma_f32 v222,v222,v211,v217\n"
MF "v[60:63],a[20:23],v[244:247],v[60:63]\n"
"v_fma_f32 v223,v223,v211,v217\n"
"v_fma_f32 v224,v224,v211,v217\n"
MF "v[60:63],a[80:83],v[204:207],v[60:63]\n"
"s_add_u32 m0,s79,8192\n"
"v_fma_f32 v225,v225,v211,v217\n"
"v_fma_f32 v226,v226,v211,v217\n"
MF "v[60:63],a[40:43],v[172:175],v[60:63]\n"
LD "v200,s[48:51],s80 offen lds\n"
MF "v[60:63],a[0:3],v[168:171],v[60:63]\n"
"v_fma_f32 v227,v227,v211,v217\n"
CV "v220,v220,v221\n"
MF "v[68:71],a[4:7],v[168:171],v[68:71]\n"
CV "v221,v222,v223\n"
CV "v222,v224,v225\n"
MF "v[68:71],a[44:47],v[172:175],v[68:71]\n"
CV "v223,v226,v227\n"
AW "a52,v220\n"
MF "v[68:71],a[84:87],v[204:207],v[68:71]\n"
AW "a53,v221\n"
AW "a54,v222\n"
MF "v[68:71],a[24:27],v[244:247],v[68:71]\n"
LD "v201,s[48:51],s80 offen offset:1024 lds\n"
MF "v[68:71],a[64:67],v[248:251],v[68:71]\n"
AW "a55,v223\n"
AR "v228,a92\n"
MF "v[68:71],a[104:107],v[238:241],v[68:71]\n"
"s_waitcnt lgkmcnt(0)\n"
AR "v229,a93\n"
AR "v230,a94\n"
MF "v[64:67],a[104:107],v[164:167],v[64:67]\n"
"s_barrier\n"
MF "v[64:67],a[64:67],v[160:163],v[64:67]\n"
LD "v202,s[48:51],s80 offen offset:2048 lds\n"
MF "v[64:67],a[24:27],v[156:159],v[64:67]\n"
AR "v231,a95\n"
AR "v232,a112\n"
MF "v[64:67],a[84:87],v[152:155],v[64:67]\n"
AR "v233,a113\n"
AR "v234,a114\n"
MF "v[64:67],a[44:47],v[148:151],v[64:67]\n"
AR "v235,a115\n"
"v_fma_f32 v228,v228,v211,v217\n"
MF "v[64:67],a[4:7],v[144:147],v[64:67]\n"
"ds_read_b128 a[112:115],v237 offset:12288\n"
"v_fma_f32 v229,v229,v211,v217\n"
MF "v[28:31],a[4:7],v[120:123],v[28:31]\n"
LD "v203,s[48:51],s80 offen offset:3072 lds\n"
MF "v[28:31],a[44:47],v[124:127],v[28:31]\n"
"v_fma_f32 v230,v230,v211,v217\n"
"v_fma_f32 v231,v231,v211,v217\n"
MF "v[28:31],a[84:87],v[128:131],v[28:31]\n"
"v_fma_f32 v232,v232,v211,v217\n"
"v_fma_f32 v233,v233,v211,v217\n"
MF "v[28:31],a[24:27],v[132:135],v[28:31]\n"
"v_fma_f32 v234,v234,v211,v217\n"
"v_fma_f32 v235,v235,v211,v217\n"
MF "v[28:31],a[64:67],v[136:139],v[28:31]\n"
CV "v228,v228,v229\n"
CV "v229,v230,v231\n"
MF "v[28:31],a[104:107],v[140:143],v[28:31]\n"
"s_add_u32 s88,s88,s65\n"
"s_add_u32 m0,s79,49152\n"
CV "v230,v232,v233\n"
MF "v[24:27],a[104:107],v[116:119],v[24:27]\n"
LD "v192,s[48:51],s88 offen lds\n"
MF "v[24:27],a[64:67],v[112:115],v[24:27]\n"
CV "v231,v234,v235\n"
AW "a92,v228\n"
MF "v[24:27],a[24:27],v[108:111],v[24:27]\n"
AW "a93,v229\n"
AW "a94,v230\n"
MF "v[24:27],a[84:87],v[104:107],v[24:27]\n"
AW "a95,v231\n"
AR "v220,a16\n"
MF "v[24:27],a[44:47],v[100:103],v[24:27]\n"
LD "v193,s[48:51],s88 offen offset:1024 lds\n"
MF "v[24:27],a[4:7],v[96:99],v[24:27]\n"
AR "v221,a17\n"
AR "v222,a18\n"
MF "v[32:35],a[8:11],v[96:99],v[32:35]\n"
AR "v223,a19\n"
AR "v224,a36\n"
MF "v[32:35],a[48:51],v[100:103],v[32:35]\n"
AR "v225,a37\n"
AR "v226,a38\n"
MF "v[32:35],a[88:91],v[104:107],v[32:35]\n"
AR "v227,a39\n"
"v_fma_f32 v220,v220,v212,v218\n"
MF "v[32:35],a[28:31],v[108:111],v[32:35]\n"
LD "v194,s[48:51],s88 offen offset:2048 lds\n"
MF "v[32:35],a[68:71],v[112:115],v[32:35]\n"
"ds_read_b128 a[36:39],v236 offset:40960\n"
"v_fma_f32 v221,v221,v212,v218\n"
MF "v[32:35],a[108:111],v[116:119],v[32:35]\n"
"v_fma_f32 v222,v222,v212,v218\n"
"v_fma_f32 v223,v223,v212,v218\n"
MF "v[36:39],a[108:111],v[140:143],v[36:39]\n"
"v_fma_f32 v224,v224,v212,v218\n"
"v_fma_f32 v225,v225,v212,v218\n"
MF "v[36:39],a[68:71],v[136:139],v[36:39]\n"
"v_fma_f32 v226,v226,v212,v218\n"
"v_fma_f32 v227,v227,v212,v218\n"
MF "v[36:39],a[28:31],v[132:135],v[36:39]\n"
LD "v195,s[48:51],s88 offen offset:3072 lds\n"
MF "v[36:39],a[88:91],v[128:131],v[36:39]\n"
CV "v220,v220,v221\n"
CV "v221,v222,v223\n"
MF "v[36:39],a[48:51],v[124:127],v[36:39]\n"
CV "v222,v224,v225\n"
CV "v223,v226,v227\n"
MF "v[36:39],a[8:11],v[120:123],v[36:39]\n"
AW "a16,v220\n"
AW "a17,v221\n"
MF "v[72:75],a[8:11],v[144:147],v[72:75]\n"
"s_add_u32 m0,s79,53248\n"
AW "a18,v222\n"
AW "a19,v223\n"
MF "v[72:75],a[48:51],v[148:151],v[72:75]\n"
LD "v196,s[48:51],s88 offen lds\n"
MF "v[72:75],a[88:91],v[152:155],v[72:75]\n"
AR "v228,a56\n"
AR "v229,a57\n"
MF "v[72:75],a[28:31],v[156:159],v[72:75]\n"
AR "v230,a58\n"
AR "v231,a59\n"
MF "v[72:75],a[68:71],v[160:163],v[72:75]\n"
AR "v232,a76\n"
AR "v233,a77\n"
MF "v[72:75],a[108:111],v[164:167],v[72:75]\n"
AR "v234,a78\n"
AR "v235,a79\n"
MF "v[76:79],a[108:111],v[238:241],v[76:79]\n"
LD "v197,s[48:51],s88 offen offset:1024 lds\n"
MF "v[76:79],a[68:71],v[248:251],v[76:79]\n"
"ds_read_b128 a[76:79],v236 offset:61440\n"
"v_fma_f32 v228,v228,v212,v218\n"
MF "v[76:79],a[28:31],v[244:247],v[76:79]\n"
"v_fma_f32 v229,v229,v212,v218\n"
"v_fma_f32 v230,v230,v212,v218\n"
MF "v[76:79],a[88:91],v[204:207],v[76:79]\n"
"v_fma_f32 v231,v231,v212,v218\n"
"v_fma_f32 v232,v232,v212,v218\n"
MF "v[76:79],a[48:51],v[172:175],v[76:79]\n"
"v_fma_f32 v233,v233,v212,v218\n"
"v_fma_f32 v234,v234,v212,v218\n"
MF "v[76:79],a[8:11],v[168:171],v[76:79]\n"
"s_waitcnt lgkmcnt(2)\n"
LD "v198,s[48:51],s88 offen offset:2048 lds\n"
MF "v[84:87],a[12:15],v[168:171],v[84:87]\n"
"v_fma_f32 v235,v235,v212,v218\n"
CV "v228,v228,v229\n"
MF "v[84:87],a[52:55],v[172:175],v[84:87]\n"
CV "v229,v230,v231\n"
CV "v230,v232,v233\n"
MF "v[84:87],a[92:95],v[204:207],v[84:87]\n"
CV "v231,v234,v235\n"
AW "a56,v228\n"
MF "v[84:87],a[32:35],v[244:247],v[84:87]\n"
AW "a57,v229\n"
AW "a58,v230\n"
MF "v[84:87],a[72:75],v[248:251],v[84:87]\n"
LD "v199,s[48:51],s88 offen offset:3072 lds\n"
MF "v[84:87],a[112:115],v[238:241],v[84:87]\n"
AW "a59,v231\n"
AR "v220,a96\n"
MF "v[80:83],a[112:115],v[164:167],v[80:83]\n"
AR "v221,a97\n"
AR "v222,a98\n"
MF "v[80:83],a[72:75],v[160:163],v[80:83]\n"
AR "v223,a99\n"
AR "v224,a116\n"
MF "v[80:83],a[32:35],v[156:159],v[80:83]\n"
"s_add_u32 m0,s79,57344\n"
AR "v225,a117\n"
AR "v226,a118\n"
MF "v[80:83],a[92:95],v[152:155],v[80:83]\n"
LD "v200,s[48:51],s88 offen lds\n"
MF "v[80:83],a[52:55],v[148:151],v[80:83]\n"
AR "v227,a119\n"
"v_fma_f32 v220,v220,v212,v218\n"
MF "v[80:83],a[12:15],v[144:147],v[80:83]\n"
"ds_read_b128 a[116:119],v237 offset:16384\n"
"v_fma_f32 v221,v221,v212,v218\n"
MF "v[44:47],a[12:15],v[120:123],v[44:47]\n"
"v_fma_f32 v222,v222,v212,v218\n"
"v_fma_f32 v223,v223,v212,v218\n"
MF "v[44:47],a[52:55],v[124:127],v[44:47]\n"
"v_fma_f32 v224,v224,v212,v218\n"
"v_fma_f32 v225,v225,v212,v218\n"
MF "v[44:47],a[92:95],v[128:131],v[44:47]\n"
LD "v201,s[48:51],s88 offen offset:1024 lds\n"
MF "v[44:47],a[32:35],v[132:135],v[44:47]\n"
"v_fma_f32 v226,v226,v212,v218\n"
"v_fma_f32 v227,v227,v212,v218\n"
MF "v[44:47],a[72:75],v[136:139],v[44:47]\n"
CV "v220,v220,v221\n"
CV "v221,v222,v223\n"
MF "v[44:47],a[112:115],v[140:143],v[44:47]\n"
CV "v222,v224,v225\n"
CV "v223,v226,v227\n"
MF "v[40:43],a[112:115],v[116:119],v[40:43]\n"
AW "a96,v220\n"
AW "a97,v221\n"
MF "v[40:43],a[72:75],v[112:115],v[40:43]\n"
LD "v202,s[48:51],s88 offen offset:2048 lds\n"
MF "v[40:43],a[32:35],v[108:111],v[40:43]\n"
AW "a98,v222\n"
AW "a99,v223\n"
MF "v[40:43],a[92:95],v[104:107],v[40:43]\n"
AR "v8,a252\n"
AR "v11,a253\n"
MF "v[40:43],a[52:55],v[100:103],v[40:43]\n"
"v_bfe_u32 v224,v9,5,3\n"
"v_bfe_u32 v225,v9,8,2\n"
MF "v[40:43],a[12:15],v[96:99],v[40:43]\n"
"s_waitcnt lgkmcnt(0)\n"
"v_lshlrev_b32 v225,1,v225\n"
"v_and_b32 v226,16,v9\n"
MF "v[48:51],a[16:19],v[96:99],v[48:51]\n"
LD "v203,s[48:51],s88 offen offset:3072 lds\n"
MF "v[48:51],a[56:59],v[100:103],v[48:51]\n"
"v_lshlrev_b32_e64 v226,v226,s9\n"
"v_sub_u32 v224,v224,v225\n"
MF "v[48:51],a[96:99],v[104:107],v[48:51]\n"
"v_cmp_eq_u32 vcc,0,v224\n"
MF "v[48:51],a[36:39],v[108:111],v[48:51]\n"
"s_nop 0\n"
"v_cndmask_b32 v220,0,v226,vcc\n"
"v_cmp_eq_u32 vcc,1,v224\n"
MF "v[48:51],a[76:79],v[112:115],v[48:51]\n"
"s_nop 0\n"
"v_cndmask_b32 v221,0,v226,vcc\n"
AW "a248,0\n"
MF "v[48:51],a[116:119],v[116:119],v[48:51]\n"
AW "a249,0\n"
AW "a250,v220\n"
MF "v[52:55],a[116:119],v[140:143],v[52:55]\n"
AW "a251,v221\n"
AW "a252,v220\n"
MF "v[52:55],a[76:79],v[136:139],v[52:55]\n"
"s_waitcnt lgkmcnt(0)\n"
AW "a253,v221\n"
AW "a254,0\n"
MF "v[52:55],a[36:39],v[132:135],v[52:55]\n"
"s_barrier\n"
MF "v[52:55],a[96:99],v[128:131],v[52:55]\n"
"s_add_u32 s80,s88,122880\n"
TR "v[176:177],v0 offset:24576\n"
AW "a255,0\n"
MF "v[52:55],a[56:59],v[124:127],v[52:55]\n"
"s_add_u32 m0,s79,98304\n"
TR "v[178:179],v0 offset:26624\n"
"v_bfe_u32 v12,v9,4,4\n"
MF "v[52:55],a[16:19],v[120:123],v[52:55]\n"
LD "v192,s[48:51],s80 offen lds\n"
MF "v[88:91],a[16:19],v[144:147],v[88:91]\n"
"s_nop 0\n"
TR "v[180:181],v0 offset:28672\n"
"ds_write_addtid_b32 v16 offset:24576\n"
MF "v[88:91],a[56:59],v[148:151],v[88:91]\n"
"s_nop 0\n"
TR "v[182:183],v0 offset:30720\n"
"ds_write_addtid_b32 v17 offset:24832\n"
MF "v[88:91],a[96:99],v[152:155],v[88:91]\n"
LD "v193,s[48:51],s80 offen offset:1024 lds\n"
MF "v[88:91],a[36:39],v[156:159],v[88:91]\n"
"s_nop 0\n"
TR "v[184:185],v0 offset:32768\n"
"ds_write_addtid_b32 v18 offset:25088\n"
MF "v[88:91],a[76:79],v[160:163],v[88:91]\n"
"s_nop 0\n"
TR "v[186:187],v0 offset:34816\n"
"ds_write_addtid_b32 v19 offset:25344\n"
MF "v[88:91],a[116:119],v[164:167],v[88:91]\n"
"s_nop 0\n"
"ds_write_addtid_b32 v20 offset:25600\n"
"ds_write_addtid_b32 v21 offset:25856\n"
MF "v[92:95],a[116:119],v[238:241],v[92:95]\n"
LD "v194,s[48:51],s80 offen offset:2048 lds\n"
MF "v[92:95],a[76:79],v[248:251],v[92:95]\n"
"s_nop 0\n"
"ds_write_addtid_b32 v22 offset:26112\n"
"ds_write_addtid_b32 v23 offset:26368\n"
MF "v[92:95],a[36:39],v[244:247],v[92:95]\n"
"s_nop 0\n"
"ds_write_addtid_b32 v24 offset:26624\n"
"ds_write_addtid_b32 v25 offset:26880\n"
MF "v[92:95],a[96:99],v[204:207],v[92:95]\n"
"s_nop 0\n"
"ds_write_addtid_b32 v26 offset:27136\n"
"ds_write_addtid_b32 v27 offset:27392\n"
MF "v[92:95],a[56:59],v[172:175],v[92:95]\n"
"v_lshlrev_b32 v12,2,v12\n"
"v_lshrrev_b32 v13,2,v9\n"
MF "v[92:95],a[16:19],v[168:171],v[92:95]\n"
"s_waitcnt lgkmcnt(9)\n"
"v_add_u32 v13,s58,v13\n"
"v_add_u32 v4,24576,v4\n"
"v_add_u32 v5,24576,v5\n"
"v_add_u32 v6,24576,v6\n"
"v_add_u32 v7,24576,v7\n"
".Lpl05:\n"
MF "v[96:99],a[0:3],v[176:179],v[252:255]\n"
"s_nop 0\n"
TR "v[188:189],v1 offset:24576\n"
"ds_write_addtid_b32 v28 offset:27648\n"
MF "v[96:99],a[40:43],v[180:183],v[96:99]\n"
"s_nop 0\n"
TR "v[190:191],v1 offset:26624\n"
"ds_write_addtid_b32 v29 offset:27904\n"
MF "v[96:99],a[80:83],v[184:187],v[96:99]\n"
LD "v195,s[48:51],s80 offen offset:3072 lds\n"
MF "v[104:107],a[84:87],v[184:187],v[252:255]\n"
"s_add_u32 s60,s64,1\n"
TR "a[240:241],v1 offset:28672\n"
"ds_write_addtid_b32 v30 offset:28160\n"
MF "v[104:107],a[44:47],v[180:183],v[104:107]\n"
"s_add_u32 m0,s79,102400\n"
TR "a[242:243],v1 offset:30720\n"
"ds_write_addtid_b32 v31 offset:24320\n"
MF "v[104:107],a[4:7],v[176:179],v[104:107]\n"
LD "v196,s[48:51],s80 offen lds\n"
MF "v[112:115],a[8:11],v[176:179],v[252:255]\n"
"s_cmp_eq_u32 s60,8\n"
"s_cselect_b32 s60,0,s60\n"
TR "a[244:245],v1 offset:32768\n"
MF "v[112:115],a[48:51],v[180:183],v[112:115]\n"
"s_cmp_eq_u32 s60,10\n"
"s_cselect_b32 s60,s61,s60\n"
TR "a[246:247],v1 offset:34816\n"
MF "v[112:115],a[88:91],v[184:187],v[112:115]\n"
"s_sub_u32 s65,s60,s64\n"
"ds_write_addtid_b32 v32 offset:24576\n"
"ds_write_addtid_b32 v33 offset:24832\n"
MF "v[120:123],a[92:95],v[184:187],v[252:255]\n"
"s_lshl_b32 s65,s65,7\n"
"ds_write_addtid_b32 v34 offset:25088\n"
"ds_write_addtid_b32 v35 offset:25344\n"
MF "v[120:123],a[52:55],v[180:183],v[120:123]\n"
LD "v197,s[48:51],s80 offen offset:1024 lds\n"
MF "v[120:123],a[12:15],v[176:179],v[120:123]\n"
"s_mov_b32 s64,s60\n"
"ds_write_addtid_b32 v36 offset:25600\n"
"ds_write_addtid_b32 v37 offset:25856\n"
MF "v[128:131],a[16:19],v[176:179],v[252:255]\n"
"s_nop 0\n"
"ds_write_addtid_b32 v38 offset:26112\n"
"ds_write_addtid_b32 v39 offset:26368\n"
MF "v[128:131],a[56:59],v[180:183],v[128:131]\n"
"s_nop 0\n"
"ds_write_addtid_b32 v40 offset:26624\n"
"ds_write_addtid_b32 v41 offset:26880\n"
MF "v[128:131],a[96:99],v[184:187],v[128:131]\n"
"s_waitcnt lgkmcnt(10)\n"
LD "v198,s[48:51],s80 offen offset:2048 lds\n"
MF "v[132:135],a[96:99],a[244:247],v[252:255]\n"
"s_nop 0\n"
TR "v[176:177],v2 offset:24576\n"
"ds_write_addtid_b32 v42 offset:27136\n"
MF "v[132:135],a[56:59],a[240:243],v[132:135]\n"
"s_nop 0\n"
TR "v[178:179],v2 offset:26624\n"
"ds_write_addtid_b32 v43 offset:27392\n"
MF "v[132:135],a[16:19],v[188:191],v[132:135]\n"
"s_nop 0\n"
"ds_write_addtid_b32 v44 offset:27648\n"
"ds_write_addtid_b32 v45 offset:27904\n"
MF "v[124:127],a[12:15],v[188:191],v[252:255]\n"
"s_nop 0\n"
TR "v[180:181],v2 offset:28672\n"
"ds_write_addtid_b32 v46 offset:28160\n"
MF "v[124:127],a[52:55],a[240:243],v[124:127]\n"
"s_nop 0\n"
TR "v[182:183],v2 offset:30720\n"
"ds_write_addtid_b32 v47 offset:28416\n"
MF "v[124:127],a[92:95],a[244:247],v[124:127]\n"
LD "v199,s[48:51],s80 offen offset:3072 lds\n"
MF "v[116:119],a[88:91],a[244:247],v[252:255]\n"
"s_nop 0\n"
TR "v[184:185],v2 offset:32768\n"
"ds_write_addtid_b32 v48 offset:28672\n"
MF "v[116:119],a[48:51],a[240:243],v[116:119]\n"
"s_add_u32 m0,s79,106496\n"
TR "v[186:187],v2 offset:34816\n"
"ds_write_addtid_b32 v49 offset:24832\n"
MF "v[116:119],a[8:11],v[188:191],v[116:119]\n"
LD "v200,s[48:51],s80 offen lds\n"
MF "v[108:111],a[4:7],v[188:191],v[252:255]\n"
"s_nop 0\n"
"ds_write_addtid_b32 v50 offset:25088\n"
"ds_write_addtid_b32 v51 offset:25344\n"
MF "v[108:111],a[44:47],a[240:243],v[108:111]\n"
"s_nop 0\n"
"ds_write_addtid_b32 v52 offset:25600\n"
"ds_write_addtid_b32 v53 offset:25856\n"
MF "v[108:111],a[84:87],a[244:247],v[108:111]\n"
"s_nop 0\n"
"ds_write_addtid_b32 v54 offset:26112\n"
"ds_write_addtid_b32 v55 offset:26368\n"
MF "v[100:103],a[80:83],a[244:247],v[252:255]\n"
MF "v[100:103],a[40:43],a[240:243],v[100:103]\n"
LD "v201,s[48:51],s80 offen offset:1024 lds\n"
MF "v[100:103],a[0:3],v[188:191],v[100:103]\n"
"s_waitcnt lgkmcnt(7)\n"
MF "v[136:139],a[0:3],v[176:179],v[252:255]\n"
TR "v[188:189],v3 offset:24576\n"
MF "v[136:139],a[40:43],v[180:183],v[136:139]\n"
TR "v[190:191],v3 offset:26624\n"
MF "v[136:139],a[80:83],v[184:187],v[136:139]\n"
LD "v202,s[48:51],s80 offen offset:2048 lds\n"
MF "v[144:147],a[84:87],v[184:187],v[252:255]\n"
TR "a[240:241],v3 offset:28672\n"
MF "v[144:147],a[44:47],v[180:183],v[144:147]\n"
TR "a[242:243],v3 offset:30720\n"
MF "v[144:147],a[4:7],v[176:179],v[144:147]\n"
LD "v203,s[48:51],s80 offen offset:3072 lds\n"
MF "v[152:155],a[8:11],v[176:179],v[252:255]\n"
TR "a[244:245],v3 offset:32768\n"
MF "v[152:155],a[48:51],v[180:183],v[152:155]\n"
TR "a[246:247],v3 offset:34816\n"
MF "v[152:155],a[88:91],v[184:187],v[152:155]\n"
MF "v[160:163],a[92:95],v[184:187],v[252:255]\n"
MF "v[160:163],a[52:55],v[180:183],v[160:163]\n"
MF "v[160:163],a[12:15],v[176:179],v[160:163]\n"
MF "v[168:171],a[16:19],v[176:179],v[252:255]\n"
MF "v[168:171],a[56:59],v[180:183],v[168:171]\n"
"s_waitcnt vmcnt(24) lgkmcnt(0)\n"
MF "v[168:171],a[96:99],v[184:187],v[168:171]\n"
"s_barrier\n"
MF "v[172:175],a[96:99],a[244:247],v[252:255]\n"
"s_add_u32 s89,s89,s65\n"
TR "v[176:177],v0\n"
MF "v[172:175],a[56:59],a[240:243],v[172:175]\n"
"s_add_u32 m0,s79,24576\n"
TR "v[178:179],v0 offset:2048\n"
MF "v[172:175],a[16:19],v[188:191],v[172:175]\n"
LD "v192,s[48:51],s89 offen lds\n"
MF "v[164:167],a[12:15],v[188:191],v[252:255]\n"
TR "v[180:181],v0 offset:4096\n"
MF "v[164:167],a[52:55],a[240:243],v[164:167]\n"
TR "v[182:183],v0 offset:6144\n"
MF "v[164:167],a[92:95],a[244:247],v[164:167]\n"
LD "v193,s[48:51],s89 offen offset:1024 lds\n"
MF "v[156:159],a[88:91],a[244:247],v[252:255]\n"
TR "v[184:185],v0 offset:8192\n"
MF "v[156:159],a[48:51],a[240:243],v[156:159]\n"
"s_nop 0\n"
TR "v[186:187],v0 offset:10240\n"
"ds_write_addtid_b32 v56 offset:49152\n"
MF "v[156:159],a[8:11],v[188:191],v[156:159]\n"
"s_nop 0\n"
"ds_write_addtid_b32 v57 offset:49408\n"
"ds_write_addtid_b32 v58 offset:49664\n"
MF "v[148:151],a[4:7],v[188:191],v[252:255]\n"
"s_nop 0\n"
"ds_write_addtid_b32 v59 offset:49920\n"
"ds_write_addtid_b32 v60 offset:50176\n"
MF "v[148:151],a[44:47],a[240:243],v[148:151]\n"
LD "v194,s[48:51],s89 offen offset:2048 lds\n"
MF "v[148:151],a[84:87],a[244:247],v[148:151]\n"
"s_nop 0\n"
"ds_write_addtid_b32 v61 offset:50432\n"
"ds_write_addtid_b32 v62 offset:50688\n"
MF "v[140:143],a[80:83],a[244:247],v[252:255]\n"
"s_nop 0\n"
"ds_write_addtid_b32 v63 offset:50944\n"
"ds_write_addtid_b32 v64 offset:51200\n"
MF "v[140:143],a[40:43],a[240:243],v[140:143]\n"
"s_nop 0\n"
"ds_write_addtid_b32 v65 offset:51456\n"
"ds_write_addtid_b32 v66 offset:51712\n"
MF "v[140:143],a[0:3],v[188:191],v[140:143]\n"
"s_waitcnt lgkmcnt(11)\n"
LD "v195,s[48:51],s89 offen offset:3072 lds\n"
MF "v[96:99],a[120:123],v[176:179],v[96:99]\n"
"s_nop 0\n"
TR "v[188:189],v1\n"
"ds_write_addtid_b32 v67 offset:51968\n"
MF "v[96:99],a[160:163],v[180:183],v[96:99]\n"
"s_nop 0\n"
TR "v[190:191],v1 offset:2048\n"
"ds_write_addtid_b32 v68 offset:52224\n"
MF "v[96:99],a[200:203],v[184:187],v[96:99]\n"
"s_nop 0\n"
"ds_write_addtid_b32 v69 offset:52480\n"
"ds_write_addtid_b32 v70 offset:52736\n"
MF "v[104:107],a[204:207],v[184:187],v[104:107]\n"
"s_nop 0\n"
TR "a[240:241],v1 offset:4096\n"
"ds_write_addtid_b32 v71 offset:52992\n"
MF "v[104:107],a[164:167],v[180:183],v[104:107]\n"
".Lpg23h:\n"
"s_cbranch_execz .Lpg27h\n"
"s_add_u32 m0,s79,28672\n"
TR "a[242:243],v1 offset:6144\n"
"ds_write_addtid_b32 v72 offset:49152\n"
MF "v[104:107],a[124:127],v[176:179],v[104:107]\n"
LD "v196,s[48:51],s89 offen lds\n"
MF "v[112:115],a[128:131],v[176:179],v[112:115]\n"
"s_nop 0\n"
TR "a[244:245],v1 offset:8192\n"
"ds_write_addtid_b32 v73 offset:49408\n"
MF "v[112:115],a[168:171],v[180:183],v[112:115]\n"
"s_nop 0\n"
TR "a[246:247],v1 offset:10240\n"
"ds_write_addtid_b32 v74 offset:49664\n"
MF "v[112:115],a[208:211],v[184:187],v[112:115]\n"
LD "v197,s[48:51],s89 offen offset:1024 lds\n"
MF "v[120:123],a[212:215],v[184:187],v[120:123]\n"
"s_nop 0\n"
"ds_write_addtid_b32 v75 offset:49920\n"
"ds_write_addtid_b32 v76 offset:50176\n"
MF "v[120:123],a[172:175],v[180:183],v[120:123]\n"
"s_nop 0\n"
"ds_write_addtid_b32 v77 offset:50432\n"
"ds_write_addtid_b32 v78 offset:50688\n"
MF "v[120:123],a[132:135],v[176:179],v[120:123]\n"
"s_nop 0\n"
"ds_write_addtid_b32 v79 offset:50944\n"
"ds_write_addtid_b32 v80 offset:51200\n"
MF "v[128:131],a[136:139],v[176:179],v[128:131]\n"
"s_nop 0\n"
"ds_write_addtid_b32 v81 offset:51456\n"
"ds_write_addtid_b32 v82 offset:51712\n"
MF "v[128:131],a[176:179],v[180:183],v[128:131]\n"
LD "v198,s[48:51],s89 offen offset:2048 lds\n"
MF "v[128:131],a[216:219],v[184:187],v[128:131]\n"
"s_waitcnt lgkmcnt(9)\n"
"ds_write_addtid_b32 v83 offset:51968\n"
"ds_write_addtid_b32 v84 offset:52224\n"
MF "v[132:135],a[216:219],a[244:247],v[132:135]\n"
"s_nop 0\n"
TR "v[176:177],v2\n"
"ds_write_addtid_b32 v85 offset:52480\n"
MF "v[132:135],a[176:179],a[240:243],v[132:135]\n"
"s_nop 0\n"
TR "v[178:179],v2 offset:2048\n"
"ds_write_addtid_b32 v86 offset:52736\n"
MF "v[132:135],a[136:139],v[188:191],v[132:135]\n"
LD "v199,s[48:51],s89 offen offset:3072 lds\n"
MF "v[124:127],a[132:135],v[188:191],v[124:127]\n"
"s_nop 0\n"
TR "v[180:181],v2 offset:4096\n"
"ds_write_addtid_b32 v87 offset:52992\n"
MF "v[124:127],a[172:175],a[240:243],v[124:127]\n"
"s_nop 0\n"
TR "v[182:183],v2 offset:6144\n"
"ds_write_addtid_b32 v88 offset:53248\n"
MF "v[124:127],a[212:215],a[244:247],v[124:127]\n"
"s_nop 0\n"
"ds_write_addtid_b32 v89 offset:53504\n"
"ds_write_addtid_b32 v90 offset:53760\n"
MF "v[116:119],a[208:211],a[244:247],v[116:119]\n"
"s_nop 0\n"
TR "v[184:185],v2 offset:8192\n"
"ds_write_addtid_b32 v91 offset:54016\n"
MF "v[116:119],a[168:171],a[240:243],v[116:119]\n"
"s_add_u32 m0,s79,32768\n"
TR "v[186:187],v2 offset:10240\n"
"ds_write_addtid_b32 v92 offset:50176\n"
MF "v[116:119],a[128:131],v[188:191],v[116:119]\n"
LD "v200,s[48:51],s89 offen lds\n"
MF "v[108:111],a[124:127],v[188:191],v[108:111]\n"
"s_nop 0\n"
"ds_write_addtid_b32 v93 offset:50432\n"
"ds_write_addtid_b32 v94 offset:50688\n"
MF "v[108:111],a[164:167],a[240:243],v[108:111]\n"
"ds_write_addtid_b32 v95 offset:50944\n"
MF "v[108:111],a[204:207],a[244:247],v[108:111]\n"
MF "v[100:103],a[200:203],a[244:247],v[100:103]\n"
LD "v201,s[48:51],s89 offen offset:1024 lds\n"
MF "v[100:103],a[160:163],a[240:243],v[100:103]\n"
MF "v[100:103],a[120:123],v[188:191],v[100:103]\n"
"s_waitcnt lgkmcnt(4)\n"
MF "v[136:139],a[120:123],v[176:179],v[136:139]\n"
TR "v[188:189],v3\n"
MF "v[136:139],a[160:163],v[180:183],v[136:139]\n"
TR "v[190:191],v3 offset:2048\n"
MF "v[136:139],a[200:203],v[184:187],v[136:139]\n"
LD "v202,s[48:51],s89 offen offset:2048 lds\n"
MF "v[144:147],a[204:207],v[184:187],v[144:147]\n"
TR "a[240:241],v3 offset:4096\n"
MF "v[144:147],a[164:167],v[180:183],v[144:147]\n"
TR "a[242:243],v3 offset:6144\n"
MF "v[144:147],a[124:127],v[176:179],v[144:147]\n"
LD "v203,s[48:51],s89 offen offset:3072 lds\n"
MF "v[152:155],a[128:131],v[176:179],v[152:155]\n"
TR "a[244:245],v3 offset:8192\n"
MF "v[152:155],a[168:171],v[180:183],v[152:155]\n"
TR "a[246:247],v3 offset:10240\n"
MF "v[152:155],a[208:211],v[184:187],v[152:155]\n"
MF "v[160:163],a[212:215],v[184:187],v[160:163]\n"
MF "v[160:163],a[172:175],v[180:183],v[160:163]\n"
MF "v[160:163],a[132:135],v[176:179],v[160:163]\n"
MF "v[168:171],a[136:139],v[176:179],v[168:171]\n"
MF "v[168:171],a[176:179],v[180:183],v[168:171]\n"
"s_waitcnt vmcnt(24) lgkmcnt(0)\n"
MF "v[168:171],a[216:219],v[184:187],v[168:171]\n"
"s_barrier\n"
MF "v[172:175],a[216:219],a[244:247],v[172:175]\n"
"s_add_u32 s80,s89,122880\n"
TR "v[176:177],v0 offset:49152\n"
MF "v[172:175],a[176:179],a[240:243],v[172:175]\n"
"s_add_u32 m0,s79,0\n"
TR "v[178:179],v0 offset:51200\n"
MF "v[172:175],a[136:139],v[188:191],v[172:175]\n"
LD "v192,s[48:51],s80 offen lds\n"
MF "v[164:167],a[132:135],v[188:191],v[164:167]\n"
TR "v[180:181],v0 offset:53248\n"
MF "v[164:167],a[172:175],a[240:243],v[164:167]\n"
TR "v[182:183],v0 offset:55296\n"
MF "v[164:167],a[212:215],a[244:247],v[164:167]\n"
LD "v193,s[48:51],s80 offen offset:1024 lds\n"
MF "v[156:159],a[208:211],a[244:247],v[156:159]\n"
TR "v[184:185],v0 offset:57344\n"
MF "v[156:159],a[168:171],a[240:243],v[156:159]\n"
TR "v[186:187],v0 offset:59392\n"
MF "v[156:159],a[128:131],v[188:191],v[156:159]\n"
MF "v[148:151],a[124:127],v[188:191],v[148:151]\n"
MF "v[148:151],a[164:167],a[240:243],v[148:151]\n"
LD "v194,s[48:51],s80 offen offset:2048 lds\n"
MF "v[148:151],a[204:207],a[244:247],v[148:151]\n"
MF "v[140:143],a[200:203],a[244:247],v[140:143]\n"
MF "v[140:143],a[160:163],a[240:243],v[140:143]\n"
MF "v[140:143],a[120:123],v[188:191],v[140:143]\n"
"s_waitcnt lgkmcnt(0)\n"
LD "v195,s[48:51],s80 offen offset:3072 lds\n"
MF "v[96:99],a[20:23],v[176:179],v[96:99]\n"
TR "v[188:189],v1 offset:49152\n"
MF "v[96:99],a[60:63],v[180:183],v[96:99]\n"
TR "v[190:191],v1 offset:51200\n"
MF "v[96:99],a[100:103],v[184:187],v[96:99]\n"
MF "v[104:107],a[104:107],v[184:187],v[104:107]\n"
TR "a[240:241],v1 offset:53248\n"
MF "v[104:107],a[64:67],v[180:183],v[104:107]\n"
"s_add_u32 m0,s79,4096\n"
TR "a[242:243],v1 offset:55296\n"
MF "v[104:107],a[24:27],v[176:179],v[104:107]\n"
LD "v196,s[48:51],s80 offen lds\n"
MF "v[112:115],a[28:31],v[176:179],v[112:115]\n"
TR "a[244:245],v1 offset:57344\n"
MF "v[112:115],a[68:71],v[180:183],v[112:115]\n"
TR "a[246:247],v1 offset:59392\n"
MF "v[112:115],a[108:111],v[184:187],v[112:115]\n"
LD "v197,s[48:51],s80 offen offset:1024 lds\n"
MF "v[120:123],a[112:115],v[184:187],v[120:123]\n"
MF "v[120:123],a[72:75],v[180:183],v[120:123]\n"
MF "v[120:123],a[32:35],v[176:179],v[120:123]\n"
MF "v[128:131],a[36:39],v[176:179],v[128:131]\n"
MF "v[128:131],a[76:79],v[180:183],v[128:131]\n"
LD "v198,s[48:51],s80 offen offset:2048 lds\n"
MF "v[128:131],a[116:119],v[184:187],v[128:131]\n"
"s_waitcnt lgkmcnt(0)\n"
MF "v[132:135],a[116:119],a[244:247],v[132:135]\n"
TR "v[176:177],v2 offset:49152\n"
MF "v[132:135],a[76:79],a[240:243],v[132:135]\n"
TR "v[178:179],v2 offset:51200\n"
MF "v[132:135],a[36:39],v[188:191],v[132:135]\n"
LD "v199,s[48:51],s80 offen offset:3072 lds\n"
MF "v[124:127],a[32:35],v[188:191],v[124:127]\n"
TR "v[180:181],v2 offset:53248\n"
MF "v[124:127],a[72:75],a[240:243],v[124:127]\n"
TR "v[182:183],v2 offset:55296\n"
MF "v[124:127],a[112:115],a[244:247],v[124:127]\n"
MF "v[116:119],a[108:111],a[244:247],v[116:119]\n"
TR "v[184:185],v2 offset:57344\n"
MF "v[116:119],a[68:71],a[240:243],v[116:119]\n"
"s_add_u32 m0,s79,8192\n"
TR "v[186:187],v2 offset:59392\n"
MF "v[116:119],a[28:31],v[188:191],v[116:119]\n"
LD "v200,s[48:51],s80 offen lds\n"
MF "v[108:111],a[24:27],v[188:191],v[108:111]\n"
MF "v[108:111],a[64:67],a[240:243],v[108:111]\n"
MF "v[108:111],a[104:107],a[244:247],v[108:111]\n"
MF "v[100:103],a[100:103],a[244:247],v[100:103]\n"
LD "v201,s[48:51],s80 offen offset:1024 lds\n"
MF "v[100:103],a[60:63],a[240:243],v[100:103]\n"
MF "v[100:103],a[20:23],v[188:191],v[100:103]\n"
"s_waitcnt lgkmcnt(0)\n"
MF "v[136:139],a[20:23],v[176:179],v[136:139]\n"
TR "v[188:189],v3 offset:49152\n"
MF "v[136:139],a[60:63],v[180:183],v[136:139]\n"
TR "v[190:191],v3 offset:51200\n"
MF "v[136:139],a[100:103],v[184:187],v[136:139]\n"
LD "v202,s[48:51],s80 offen offset:2048 lds\n"
MF "v[144:147],a[104:107],v[184:187],v[144:147]\n"
TR "a[240:241],v3 offset:53248\n"
MF "v[144:147],a[64:67],v[180:183],v[144:147]\n"
TR "a[242:243],v3 offset:55296\n"
MF "v[144:147],a[24:27],v[176:179],v[144:147]\n"
LD "v203,s[48:51],s80 offen offset:3072 lds\n"
MF "v[152:155],a[28:31],v[176:179],v[152:155]\n"
TR "a[244:245],v3 offset:57344\n"
MF "v[152:155],a[68:71],v[180:183],v[152:155]\n"
TR "a[246:247],v3 offset:59392\n"
MF "v[152:155],a[108:111],v[184:187],v[152:155]\n"
MF "v[160:163],a[112:115],v[184:187],v[160:163]\n"
MF "v[160:163],a[72:75],v[180:183],v[160:163]\n"
MF "v[160:163],a[32:35],v[176:179],v[160:163]\n"
MF "v[168:171],a[36:39],v[176:179],v[168:171]\n"
MF "v[168:171],a[76:79],v[180:183],v[168:171]\n"
"s_waitcnt vmcnt(24) lgkmcnt(0)\n"
MF "v[168:171],a[116:119],v[184:187],v[168:171]\n"
"s_barrier\n"
MF "v[172:175],a[116:119],a[244:247],v[172:175]\n"
"s_add_u32 s88,s88,s65\n"
TR "v[176:177],v4\n"
MF "v[172:175],a[76:79],a[240:243],v[172:175]\n"
"s_add_u32 m0,s79,49152\n"
TR "v[178:179],v4 offset:2048\n"
MF "v[172:175],a[36:39],v[188:191],v[172:175]\n"
LD "v192,s[48:51],s88 offen lds\n"
MF "v[164:167],a[32:35],v[188:191],v[164:167]\n"
TR "v[180:181],v4 offset:4096\n"
MF "v[164:167],a[72:75],a[240:243],v[164:167]\n"
TR "v[182:183],v4 offset:6144\n"
MF "v[164:167],a[112:115],a[244:247],v[164:167]\n"
LD "v193,s[48:51],s88 offen offset:1024 lds\n"
MF "v[156:159],a[108:111],a[244:247],v[156:159]\n"
TR "v[184:185],v4 offset:8192\n"
MF "v[156:159],a[68:71],a[240:243],v[156:159]\n"
TR "v[186:187],v4 offset:10240\n"
MF "v[156:159],a[28:31],v[188:191],v[156:159]\n"
MF "v[148:151],a[24:27],v[188:191],v[148:151]\n"
MF "v[148:151],a[64:67],a[240:243],v[148:151]\n"
LD "v194,s[48:51],s88 offen offset:2048 lds\n"
MF "v[148:151],a[104:107],a[244:247],v[148:151]\n"
MF "v[140:143],a[100:103],a[244:247],v[140:143]\n"
MF "v[140:143],a[60:63],a[240:243],v[140:143]\n"
MF "v[140:143],a[20:23],v[188:191],v[140:143]\n"
"s_waitcnt lgkmcnt(0)\n"
LD "v195,s[48:51],s88 offen offset:3072 lds\n"
MF "v[96:99],a[140:143],v[176:179],v[96:99]\n"
TR "v[188:189],v5\n"
MF "v[96:99],a[180:183],v[180:183],v[96:99]\n"
TR "v[190:191],v5 offset:2048\n"
MF "v[96:99],a[220:223],v[184:187],v[96:99]\n"
MF "v[104:107],a[224:227],v[184:187],v[104:107]\n"
TR "a[240:241],v5 offset:4096\n"
MF "v[104:107],a[184:187],v[180:183],v[104:107]\n"
"s_add_u32 m0,s79,53248\n"
TR "a[242:243],v5 offset:6144\n"
MF "v[104:107],a[144:147],v[176:179],v[104:107]\n"
LD "v196,s[48:51],s88 offen lds\n"
MF "v[112:115],a[148:151],v[176:179],v[112:115]\n"
TR "a[244:245],v5 offset:8192\n"
MF "v[112:115],a[188:191],v[180:183],v[112:115]\n"
TR "a[246:247],v5 offset:10240\n"
MF "v[112:115],a[228:231],v[184:187],v[112:115]\n"
LD "v197,s[48:51],s88 offen offset:1024 lds\n"
MF "v[120:123],a[232:235],v[184:187],v[120:123]\n"
MF "v[120:123],a[192:195],v[180:183],v[120:123]\n"
MF "v[120:123],a[152:155],v[176:179],v[120:123]\n"
MF "v[128:131],a[156:159],v[176:179],v[128:131]\n"
MF "v[128:131],a[196:199],v[180:183],v[128:131]\n"
LD "v198,s[48:51],s88 offen offset:2048 lds\n"
MF "v[128:131],a[236:239],v[184:187],v[128:131]\n"
"s_waitcnt lgkmcnt(0)\n"
MF "v[132:135],a[236:239],a[244:247],v[132:135]\n"
TR "v[176:177],v6\n"
MF "v[132:135],a[196:199],a[240:243],v[132:135]\n"
TR "v[178:179],v6 offset:2048\n"
MF "v[132:135],a[156:159],v[188:191],v[132:135]\n"
LD "v199,s[48:51],s88 offen offset:3072 lds\n"
MF "v[124:127],a[152:155],v[188:191],v[124:127]\n"
TR "v[180:181],v6 offset:4096\n"
MF "v[124:127],a[192:195],a[240:243],v[124:127]\n"
TR "v[182:183],v6 offset:6144\n"
MF "v[124:127],a[232:235],a[244:247],v[124:127]\n"
MF "v[116:119],a[228:231],a[244:247],v[116:119]\n"
TR "v[184:185],v6 offset:8192\n"
MF "v[116:119],a[188:191],a[240:243],v[116:119]\n"
"s_add_u32 m0,s79,57344\n"
TR "v[186:187],v6 offset:10240\n"
MF "v[116:119],a[148:151],v[188:191],v[116:119]\n"
LD "v200,s[48:51],s88 offen lds\n"
MF "v[108:111],a[144:147],v[188:191],v[108:111]\n"
MF "v[108:111],a[184:187],a[240:243],v[108:111]\n"
MF "v[108:111],a[224:227],a[244:247],v[108:111]\n"
MF "v[100:103],a[220:223],a[244:247],v[100:103]\n"
LD "v201,s[48:51],s88 offen offset:1024 lds\n"
MF "v[100:103],a[180:183],a[240:243],v[100:103]\n"
MF "v[100:103],a[140:143],v[188:191],v[100:103]\n"
"s_waitcnt lgkmcnt(0)\n"
MF "v[136:139],a[140:143],v[176:179],v[136:139]\n"
TR "v[188:189],v7\n"
MF "v[136:139],a[180:183],v[180:183],v[136:139]\n"
TR "v[190:191],v7 offset:2048\n"
MF "v[136:139],a[220:223],v[184:187],v[136:139]\n"
LD "v202,s[48:51],s88 offen offset:2048 lds\n"
MF "v[144:147],a[224:227],v[184:187],v[144:147]\n"
TR "a[240:241],v7 offset:4096\n"
MF "v[144:147],a[184:187],v[180:183],v[144:147]\n"
TR "a[242:243],v7 offset:6144\n"
MF "v[144:147],a[144:147],v[176:179],v[144:147]\n"
LD "v203,s[48:51],s88 offen offset:3072 lds\n"
MF "v[152:155],a[148:151],v[176:179],v[152:155]\n"
TR "a[244:245],v7 offset:8192\n"
MF "v[152:155],a[188:191],v[180:183],v[152:155]\n"
TR "a[246:247],v7 offset:10240\n"
MF "v[152:155],a[228:231],v[184:187],v[152:155]\n"
MF "v[160:163],a[232:235],v[184:187],v[160:163]\n"
MF "v[160:163],a[192:195],v[180:183],v[160:163]\n"
MF "v[160:163],a[152:155],v[176:179],v[160:163]\n"
MF "v[168:171],a[156:159],v[176:179],v[168:171]\n"
MF "v[168:171],a[196:199],v[180:183],v[168:171]\n"
"s_waitcnt vmcnt(24) lgkmcnt(0)\n"
MF "v[168:171],a[236:239],v[184:187],v[168:171]\n"
"s_barrier\n"
MF "v[172:175],a[236:239],a[244:247],v[172:175]\n"
"s_add_u32 s80,s88,122880\n"
TR "v[176:177],v0 offset:24576\n"
MF "v[172:175],a[196:199],a[240:243],v[172:175]\n"
"s_add_u32 m0,s79,98304\n"
"s_add_u32 s85,s85,1\n"
TR "v[178:179],v0 offset:26624\n"
MF "v[172:175],a[156:159],v[188:191],v[172:175]\n"
LD "v192,s[48:51],s80 offen lds\n"
MF "v[164:167],a[152:155],v[188:191],v[164:167]\n"
"s_nop 0\n"
TR "v[180:181],v0 offset:28672\n"
"ds_write_addtid_b32 v96 offset:24576\n"
MF "v[164:167],a[192:195],a[240:243],v[164:167]\n"
"s_nop 0\n"
TR "v[182:183],v0 offset:30720\n"
"ds_write_addtid_b32 v97 offset:24832\n"
MF "v[164:167],a[232:235],a[244:247],v[164:167]\n"
LD "v193,s[48:51],s80 offen offset:1024 lds\n"
MF "v[156:159],a[228:231],a[244:247],v[156:159]\n"
"s_nop 0\n"
TR "v[184:185],v0 offset:32768\n"
"ds_write_addtid_b32 v98 offset:25088\n"
MF "v[156:159],a[188:191],a[240:243],v[156:159]\n"
"s_nop 0\n"
TR "v[186:187],v0 offset:34816\n"
"ds_write_addtid_b32 v99 offset:25344\n"
MF "v[156:159],a[148:151],v[188:191],v[156:159]\n"
"s_nop 0\n"
"ds_write_addtid_b32 v100 offset:25600\n"
"ds_write_addtid_b32 v101 offset:25856\n"
MF "v[148:151],a[144:147],v[188:191],v[148:151]\n"
LD "v194,s[48:51],s80 offen offset:2048 lds\n"
MF "v[148:151],a[184:187],a[240:243],v[148:151]\n"
"s_nop 0\n"
"ds_write_addtid_b32 v102 offset:26112\n"
"ds_write_addtid_b32 v103 offset:26368\n"
MF "v[148:151],a[224:227],a[244:247],v[148:151]\n"
"s_nop 0\n"
"ds_write_addtid_b32 v104 offset:26624\n"
"ds_write_addtid_b32 v105 offset:26880\n"
MF "v[140:143],a[220:223],a[244:247],v[140:143]\n"
"s_nop 0\n"
"ds_write_addtid_b32 v106 offset:27136\n"
"ds_write_addtid_b32 v107 offset:27392\n"
MF "v[140:143],a[180:183],a[240:243],v[140:143]\n"
MF "v[140:143],a[140:143],v[188:191],v[140:143]\n"
"s_waitcnt lgkmcnt(9)\n"
MF "v[16:19],a[0:3],v[176:179],v[252:255]\n"
"s_nop 0\n"
TR "v[188:189],v1 offset:24576\n"
"ds_write_addtid_b32 v108 offset:27648\n"
MF "v[16:19],a[40:43],v[180:183],v[16:19]\n"
"s_nop 0\n"
TR "v[190:191],v1 offset:26624\n"
"ds_write_addtid_b32 v109 offset:27904\n"
MF "v[16:19],a[80:83],v[184:187],v[16:19]\n"
LD "v195,s[48:51],s80 offen offset:3072 lds\n"
MF "v[24:27],a[84:87],v[184:187],v[252:255]\n"
"s_add_u32 s60,s64,1\n"
TR "a[240:241],v1 offset:28672\n"
"ds_write_addtid_b32 v110 offset:28160\n"
MF "v[24:27],a[44:47],v[180:183],v[24:27]\n"
"s_add_u32 m0,s79,102400\n"
TR "a[242:243],v1 offset:30720\n"
"ds_write_addtid_b32 v111 offset:24320\n"
MF "v[24:27],a[4:7],v[176:179],v[24:27]\n"
LD "v196,s[48:51],s80 offen lds\n"
MF "v[32:35],a[8:11],v[176:179],v[252:255]\n"
"s_cmp_eq_u32 s60,8\n"
"s_cselect_b32 s60,0,s60\n"
TR "a[244:245],v1 offset:32768\n"
MF "v[32:35],a[48:51],v[180:183],v[32:35]\n"
"s_cmp_eq_u32 s60,10\n"
"s_cselect_b32 s60,s61,s60\n"
TR "a[246:247],v1 offset:34816\n"
MF "v[32:35],a[88:91],v[184:187],v[32:35]\n"
"s_sub_u32 s65,s60,s64\n"
"ds_write_addtid_b32 v112 offset:24576\n"
"ds_write_addtid_b32 v113 offset:24832\n"
MF "v[40:43],a[92:95],v[184:187],v[252:255]\n"
"s_lshl_b32 s65,s65,7\n"
"ds_write_addtid_b32 v114 offset:25088\n"
"ds_write_addtid_b32 v115 offset:25344\n"
MF "v[40:43],a[52:55],v[180:183],v[40:43]\n"
LD "v197,s[48:51],s80 offen offset:1024 lds\n"
MF "v[40:43],a[12:15],v[176:179],v[40:43]\n"
"s_mov_b32 s64,s60\n"
"ds_write_addtid_b32 v116 offset:25600\n"
"ds_write_addtid_b32 v117 offset:25856\n"
MF "v[48:51],a[16:19],v[176:179],v[252:255]\n"
"s_nop 0\n"
"ds_write_addtid_b32 v118 offset:26112\n"
"ds_write_addtid_b32 v119 offset:26368\n"
MF "v[48:51],a[56:59],v[180:183],v[48:51]\n"
"s_nop 0\n"
"ds_write_addtid_b32 v120 offset:26624\n"
"ds_write_addtid_b32 v121 offset:26880\n"
MF "v[48:51],a[96:99],v[184:187],v[48:51]\n"
"s_waitcnt lgkmcnt(10)\n"
LD "v198,s[48:51],s80 offen offset:2048 lds\n"
MF "v[52:55],a[96:99],a[244:247],v[252:255]\n"
"s_nop 0\n"
TR "v[176:177],v2 offset:24576\n"
"ds_write_addtid_b32 v122 offset:27136\n"
MF "v[52:55],a[56:59],a[240:243],v[52:55]\n"
"s_nop 0\n"
TR "v[178:179],v2 offset:26624\n"
"ds_write_addtid_b32 v123 offset:27392\n"
MF "v[52:55],a[16:19],v[188:191],v[52:55]\n"
"s_nop 0\n"
"ds_write_addtid_b32 v124 offset:27648\n"
"ds_write_addtid_b32 v125 offset:27904\n"
MF "v[44:47],a[12:15],v[188:191],v[252:255]\n"
"s_nop 0\n"
TR "v[180:181],v2 offset:28672\n"
"ds_write_addtid_b32 v126 offset:28160\n"
MF "v[44:47],a[52:55],a[240:243],v[44:47]\n"
"s_nop 0\n"
TR "v[182:183],v2 offset:30720\n"
"ds_write_addtid_b32 v127 offset:28416\n"
MF "v[44:47],a[92:95],a[244:247],v[44:47]\n"
LD "v199,s[48:51],s80 offen offset:3072 lds\n"
MF "v[36:39],a[88:91],a[244:247],v[252:255]\n"
"s_nop 0\n"
TR "v[184:185],v2 offset:32768\n"
"ds_write_addtid_b32 v128 offset:28672\n"
MF "v[36:39],a[48:51],a[240:243],v[36:39]\n"
"s_add_u32 m0,s79,106496\n"
TR "v[186:187],v2 offset:34816\n"
"ds_write_addtid_b32 v129 offset:24832\n"
MF "v[36:39],a[8:11],v[188:191],v[36:39]\n"
LD "v200,s[48:51],s80 offen lds\n"
MF "v[28:31],a[4:7],v[188:191],v[252:255]\n"
"s_nop 0\n"
"ds_write_addtid_b32 v130 offset:25088\n"
"ds_write_addtid_b32 v131 offset:25344\n"
MF "v[28:31],a[44:47],a[240:243],v[28:31]\n"
"s_nop 0\n"
"ds_write_addtid_b32 v132 offset:25600\n"
"ds_write_addtid_b32 v133 offset:25856\n"
MF "v[28:31],a[84:87],a[244:247],v[28:31]\n"
"s_nop 0\n"
"ds_write_addtid_b32 v134 offset:26112\n"
"ds_write_addtid_b32 v135 offset:26368\n"
MF "v[20:23],a[80:83],a[244:247],v[252:255]\n"
MF "v[20:23],a[40:43],a[240:243],v[20:23]\n"
LD "v201,s[48:51],s80 offen offset:1024 lds\n"
MF "v[20:23],a[0:3],v[188:191],v[20:23]\n"
"s_waitcnt lgkmcnt(7)\n"
MF "v[56:59],a[0:3],v[176:179],v[252:255]\n"
TR "v[188:189],v3 offset:24576\n"
MF "v[56:59],a[40:43],v[180:183],v[56:59]\n"
TR "v[190:191],v3 offset:26624\n"
MF "v[56:59],a[80:83],v[184:187],v[56:59]\n"
LD "v202,s[48:51],s80 offen offset:2048 lds\n"
MF "v[64:67],a[84:87],v[184:187],v[252:255]\n"
TR "a[240:241],v3 offset:28672\n"
MF "v[64:67],a[44:47],v[180:183],v[64:67]\n"
TR "a[242:243],v3 offset:30720\n"
MF "v[64:67],a[4:7],v[176:179],v[64:67]\n"
LD "v203,s[48:51],s80 offen offset:3072 lds\n"
MF "v[72:75],a[8:11],v[176:179],v[252:255]\n"
TR "a[244:245],v3 offset:32768\n"
MF "v[72:75],a[48:51],v[180:183],v[72:75]\n"
TR "a[246:247],v3 offset:34816\n"
MF "v[72:75],a[88:91],v[184:187],v[72:75]\n"
MF "v[80:83],a[92:95],v[184:187],v[252:255]\n"
MF "v[80:83],a[52:55],v[180:183],v[80:83]\n"
MF "v[80:83],a[12:15],v[176:179],v[80:83]\n"
MF "v[88:91],a[16:19],v[176:179],v[252:255]\n"
MF "v[88:91],a[56:59],v[180:183],v[88:91]\n"
"s_waitcnt vmcnt(24) lgkmcnt(0)\n"
MF "v[88:91],a[96:99],v[184:187],v[88:91]\n"
"s_barrier\n"
MF "v[92:95],a[96:99],a[244:247],v[252:255]\n"
"s_add_u32 s89,s89,s65\n"
TR "v[176:177],v0\n"
MF "v[92:95],a[56:59],a[240:243],v[92:95]\n"
"s_add_u32 m0,s79,24576\n"
TR "v[178:179],v0 offset:2048\n"
MF "v[92:95],a[16:19],v[188:191],v[92:95]\n"
LD "v192,s[48:51],s89 offen lds\n"
MF "v[84:87],a[12:15],v[188:191],v[252:255]\n"
TR "v[180:181],v0 offset:4096\n"
MF "v[84:87],a[52:55],a[240:243],v[84:87]\n"
TR "v[182:183],v0 offset:6144\n"
MF "v[84:87],a[92:95],a[244:247],v[84:87]\n"
LD "v193,s[48:51],s89 offen offset:1024 lds\n"
MF "v[76:79],a[88:91],a[244:247],v[252:255]\n"
TR "v[184:185],v0 offset:8192\n"
MF "v[76:79],a[48:51],a[240:243],v[76:79]\n"
"s_nop 0\n"
TR "v[186:187],v0 offset:10240\n"
"ds_write_addtid_b32 v136 offset:49152\n"
MF "v[76:79],a[8:11],v[188:191],v[76:79]\n"
"s_nop 0\n"
"ds_write_addtid_b32 v137 offset:49408\n"
"ds_write_addtid_b32 v138 offset:49664\n"
MF "v[68:71],a[4:7],v[188:191],v[252:255]\n"
"s_nop 0\n"
"ds_write_addtid_b32 v139 offset:49920\n"
"ds_write_addtid_b32 v140 offset:50176\n"
MF "v[68:71],a[44:47],a[240:243],v[68:71]\n"
LD "v194,s[48:51],s89 offen offset:2048 lds\n"
MF "v[68:71],a[84:87],a[244:247],v[68:71]\n"
"s_nop 0\n"
"ds_write_addtid_b32 v141 offset:50432\n"
"ds_write_addtid_b32 v142 offset:50688\n"
MF "v[60:63],a[80:83],a[244:247],v[252:255]\n"
"s_nop 0\n"
"ds_write_addtid_b32 v143 offset:50944\n"
"ds_write_addtid_b32 v144 offset:51200\n"
MF "v[60:63],a[40:43],a[240:243],v[60:63]\n"
"s_nop 0\n"
"ds_write_addtid_b32 v145 offset:51456\n"
"ds_write_addtid_b32 v146 offset:51712\n"
MF "v[60:63],a[0:3],v[188:191],v[60:63]\n"
"s_waitcnt lgkmcnt(11)\n"
LD "v195,s[48:51],s89 offen offset:3072 lds\n"
MF "v[16:19],a[120:123],v[176:179],v[16:19]\n"
"s_nop 0\n"
TR "v[188:189],v1\n"
"ds_write_addtid_b32 v147 offset:51968\n"
MF "v[16:19],a[160:163],v[180:183],v[16:19]\n"
"s_nop 0\n"
TR "v[190:191],v1 offset:2048\n"
"ds_write_addtid_b32 v148 offset:52224\n"
MF "v[16:19],a[200:203],v[184:187],v[16:19]\n"
"s_nop 0\n"
"ds_write_addtid_b32 v149 offset:52480\n"
"ds_write_addtid_b32 v150 offset:52736\n"
MF "v[24:27],a[204:207],v[184:187],v[24:27]\n"
".Lpg24h:\n"
"s_cbranch_execz .Lpg28h\n"
"s_nop 0\n"
TR "a[240:241],v1 offset:4096\n"
"ds_write_addtid_b32 v151 offset:52992\n"
MF "v[24:27],a[164:167],v[180:183],v[24:27]\n"
"s_add_u32 m0,s79,28672\n"
TR "a[242:243],v1 offset:6144\n"
"ds_write_addtid_b32 v152 offset:49152\n"
MF "v[24:27],a[124:127],v[176:179],v[24:27]\n"
LD "v196,s[48:51],s89 offen lds\n"
MF "v[32:35],a[128:131],v[176:179],v[32:35]\n"
"s_nop 0\n"
TR "a[244:245],v1 offset:8192\n"
"ds_write_addtid_b32 v153 offset:49408\n"
MF "v[32:35],a[168:171],v[180:183],v[32:35]\n"
"s_nop 0\n"
TR "a[246:247],v1 offset:10240\n"
"ds_write_addtid_b32 v154 offset:49664\n"
MF "v[32:35],a[208:211],v[184:187],v[32:35]\n"
LD "v197,s[48:51],s89 offen offset:1024 lds\n"
MF "v[40:43],a[212:215],v[184:187],v[40:43]\n"
"s_nop 0\n"
"ds_write_addtid_b32 v155 offset:49920\n"
"ds_write_addtid_b32 v156 offset:50176\n"
MF "v[40:43],a[172:175],v[180:183],v[40:43]\n"
"s_nop 0\n"
"ds_write_addtid_b32 v157 offset:50432\n"
"ds_write_addtid_b32 v158 offset:50688\n"
MF "v[40:43],a[132:135],v[176:179],v[40:43]\n"
"s_nop 0\n"
"ds_write_addtid_b32 v159 offset:50944\n"
"ds_write_addtid_b32 v160 offset:51200\n"
MF "v[48:51],a[136:139],v[176:179],v[48:51]\n"
"s_nop 0\n"
"ds_write_addtid_b32 v161 offset:51456\n"
"ds_write_addtid_b32 v162 offset:51712\n"
MF "v[48:51],a[176:179],v[180:183],v[48:51]\n"
LD "v198,s[48:51],s89 offen offset:2048 lds\n"
MF "v[48:51],a[216:219],v[184:187],v[48:51]\n"
"s_waitcnt lgkmcnt(9)\n"
"ds_write_addtid_b32 v163 offset:51968\n"
"ds_write_addtid_b32 v164 offset:52224\n"
MF "v[52:55],a[216:219],a[244:247],v[52:55]\n"
"s_nop 0\n"
TR "v[176:177],v2\n"
"ds_write_addtid_b32 v165 offset:52480\n"
MF "v[52:55],a[176:179],a[240:243],v[52:55]\n"
"s_nop 0\n"
TR "v[178:179],v2 offset:2048\n"
"ds_write_addtid_b32 v166 offset:52736\n"
MF "v[52:55],a[136:139],v[188:191],v[52:55]\n"
LD "v199,s[48:51],s89 offen offset:3072 lds\n"
MF "v[44:47],a[132:135],v[188:191],v[44:47]\n"
"s_nop 0\n"
TR "v[180:181],v2 offset:4096\n"
"ds_write_addtid_b32 v167 offset:52992\n"
MF "v[44:47],a[172:175],a[240:243],v[44:47]\n"
"s_nop 0\n"
TR "v[182:183],v2 offset:6144\n"
"ds_write_addtid_b32 v168 offset:53248\n"
MF "v[44:47],a[212:215],a[244:247],v[44:47]\n"
"s_nop 0\n"
"ds_write_addtid_b32 v169 offset:53504\n"
"ds_write_addtid_b32 v170 offset:53760\n"
MF "v[36:39],a[208:211],a[244:247],v[36:39]\n"
"s_nop 0\n"
TR "v[184:185],v2 offset:8192\n"
"ds_write_addtid_b32 v171 offset:54016\n"
MF "v[36:39],a[168:171],a[240:243],v[36:39]\n"
"s_add_u32 m0,s79,32768\n"
TR "v[186:187],v2 offset:10240\n"
"ds_write_addtid_b32 v172 offset:50176\n"
MF "v[36:39],a[128:131],v[188:191],v[36:39]\n"
LD "v200,s[48:51],s89 offen lds\n"
MF "v[28:31],a[124:127],v[188:191],v[28:31]\n"
"s_nop 0\n"
"ds_write_addtid_b32 v173 offset:50432\n"
"ds_write_addtid_b32 v174 offset:50688\n"
MF "v[28:31],a[164:167],a[240:243],v[28:31]\n"
"ds_write_addtid_b32 v175 offset:50944\n"
MF "v[28:31],a[204:207],a[244:247],v[28:31]\n"
MF "v[20:23],a[200:203],a[244:247],v[20:23]\n"
LD "v201,s[48:51],s89 offen offset:1024 lds\n"
MF "v[20:23],a[160:163],a[240:243],v[20:23]\n"
MF "v[20:23],a[120:123],v[188:191],v[20:23]\n"
"s_waitcnt lgkmcnt(4)\n"
MF "v[56:59],a[120:123],v[176:179],v[56:59]\n"
TR "v[188:189],v3\n"
MF "v[56:59],a[160:163],v[180:183],v[56:59]\n"
TR "v[190:191],v3 offset:2048\n"
MF "v[56:59],a[200:203],v[184:187],v[56:59]\n"
LD "v202,s[48:51],s89 offen offset:2048 lds\n"
MF "v[64:67],a[204:207],v[184:187],v[64:67]\n"
TR "a[240:241],v3 offset:4096\n"
MF "v[64:67],a[164:167],v[180:183],v[64:67]\n"
TR "a[242:243],v3 offset:6144\n"
MF "v[64:67],a[124:127],v[176:179],v[64:67]\n"
LD "v203,s[48:51],s89 offen offset:3072 lds\n"
MF "v[72:75],a[128:131],v[176:179],v[72:75]\n"
TR "a[244:245],v3 offset:8192\n"
MF "v[72:75],a[168:171],v[180:183],v[72:75]\n"
TR "a[246:247],v3 offset:10240\n"
MF "v[72:75],a[208:211],v[184:187],v[72:75]\n"
MF "v[80:83],a[212:215],v[184:187],v[80:83]\n"
MF "v[80:83],a[172:175],v[180:183],v[80:83]\n"
MF "v[80:83],a[132:135],v[176:179],v[80:83]\n"
MF "v[88:91],a[136:139],v[176:179],v[88:91]\n"
MF "v[88:91],a[176:179],v[180:183],v[88:91]\n"
"s_waitcnt vmcnt(24) lgkmcnt(0)\n"
MF "v[88:91],a[216:219],v[184:187],v[88:91]\n"
"s_barrier\n"
MF "v[92:95],a[216:219],a[244:247],v[92:95]\n"
"s_add_u32 s80,s89,122880\n"
TR "v[176:177],v0 offset:49152\n"
MF "v[92:95],a[176:179],a[240:243],v[92:95]\n"
"s_add_u32 m0,s79,0\n"
TR "v[178:179],v0 offset:51200\n"
MF "v[92:95],a[136:139],v[188:191],v[92:95]\n"
LD "v192,s[48:51],s80 offen lds\n"
MF "v[84:87],a[132:135],v[188:191],v[84:87]\n"
TR "v[180:181],v0 offset:53248\n"
MF "v[84:87],a[172:175],a[240:243],v[84:87]\n"
TR "v[182:183],v0 offset:55296\n"
MF "v[84:87],a[212:215],a[244:247],v[84:87]\n"
LD "v193,s[48:51],s80 offen offset:1024 lds\n"
MF "v[76:79],a[208:211],a[244:247],v[76:79]\n"
TR "v[184:185],v0 offset:57344\n"
MF "v[76:79],a[168:171],a[240:243],v[76:79]\n"
TR "v[186:187],v0 offset:59392\n"
MF "v[76:79],a[128:131],v[188:191],v[76:79]\n"
MF "v[68:71],a[124:127],v[188:191],v[68:71]\n"
MF "v[68:71],a[164:167],a[240:243],v[68:71]\n"
LD "v194,s[48:51],s80 offen offset:2048 lds\n"
MF "v[68:71],a[204:207],a[244:247],v[68:71]\n"
MF "v[60:63],a[200:203],a[244:247],v[60:63]\n"
MF "v[60:63],a[160:163],a[240:243],v[60:63]\n"
MF "v[60:63],a[120:123],v[188:191],v[60:63]\n"
"s_waitcnt lgkmcnt(0)\n"
LD "v195,s[48:51],s80 offen offset:3072 lds\n"
MF "v[16:19],a[20:23],v[176:179],v[16:19]\n"
TR "v[188:189],v1 offset:49152\n"
MF "v[16:19],a[60:63],v[180:183],v[16:19]\n"
TR "v[190:191],v1 offset:51200\n"
MF "v[16:19],a[100:103],v[184:187],v[16:19]\n"
MF "v[24:27],a[104:107],v[184:187],v[24:27]\n"
TR "a[240:241],v1 offset:53248\n"
MF "v[24:27],a[64:67],v[180:183],v[24:27]\n"
"s_add_u32 m0,s79,4096\n"
TR "a[242:243],v1 offset:55296\n"
MF "v[24:27],a[24:27],v[176:179],v[24:27]\n"
LD "v196,s[48:51],s80 offen lds\n"
MF "v[32:35],a[28:31],v[176:179],v[32:35]\n"
TR "a[244:245],v1 offset:57344\n"
MF "v[32:35],a[68:71],v[180:183],v[32:35]\n"
TR "a[246:247],v1 offset:59392\n"
MF "v[32:35],a[108:111],v[184:187],v[32:35]\n"
LD "v197,s[48:51],s80 offen offset:1024 lds\n"
MF "v[40:43],a[112:115],v[184:187],v[40:43]\n"
MF "v[40:43],a[72:75],v[180:183],v[40:43]\n"
MF "v[40:43],a[32:35],v[176:179],v[40:43]\n"
MF "v[48:51],a[36:39],v[176:179],v[48:51]\n"
MF "v[48:51],a[76:79],v[180:183],v[48:51]\n"
LD "v198,s[48:51],s80 offen offset:2048 lds\n"
MF "v[48:51],a[116:119],v[184:187],v[48:51]\n"
"s_waitcnt lgkmcnt(0)\n"
MF "v[52:55],a[116:119],a[244:247],v[52:55]\n"
TR "v[176:177],v2 offset:49152\n"
MF "v[52:55],a[76:79],a[240:243],v[52:55]\n"
TR "v[178:179],v2 offset:51200\n"
MF "v[52:55],a[36:39],v[188:191],v[52:55]\n"
LD "v199,s[48:51],s80 offen offset:3072 lds\n"
MF "v[44:47],a[32:35],v[188:191],v[44:47]\n"
TR "v[180:181],v2 offset:53248\n"
MF "v[44:47],a[72:75],a[240:243],v[44:47]\n"
TR "v[182:183],v2 offset:55296\n"
MF "v[44:47],a[112:115],a[244:247],v[44:47]\n"
MF "v[36:39],a[108:111],a[244:247],v[36:39]\n"
TR "v[184:185],v2 offset:57344\n"
MF "v[36:39],a[68:71],a[240:243],v[36:39]\n"
"s_add_u32 m0,s79,8192\n"
TR "v[186:187],v2 offset:59392\n"
MF "v[36:39],a[28:31],v[188:191],v[36:39]\n"
LD "v200,s[48:51],s80 offen lds\n"
MF "v[28:31],a[24:27],v[188:191],v[28:31]\n"
MF "v[28:31],a[64:67],a[240:243],v[28:31]\n"
MF "v[28:31],a[104:107],a[244:247],v[28:31]\n"
MF "v[20:23],a[100:103],a[244:247],v[20:23]\n"
LD "v201,s[48:51],s80 offen offset:1024 lds\n"
MF "v[20:23],a[60:63],a[240:243],v[20:23]\n"
MF "v[20:23],a[20:23],v[188:191],v[20:23]\n"
"s_waitcnt lgkmcnt(0)\n"
MF "v[56:59],a[20:23],v[176:179],v[56:59]\n"
TR "v[188:189],v3 offset:49152\n"
MF "v[56:59],a[60:63],v[180:183],v[56:59]\n"
TR "v[190:191],v3 offset:51200\n"
MF "v[56:59],a[100:103],v[184:187],v[56:59]\n"
LD "v202,s[48:51],s80 offen offset:2048 lds\n"
MF "v[64:67],a[104:107],v[184:187],v[64:67]\n"
TR "a[240:241],v3 offset:53248\n"
MF "v[64:67],a[64:67],v[180:183],v[64:67]\n"
TR "a[242:243],v3 offset:55296\n"
MF "v[64:67],a[24:27],v[176:179],v[64:67]\n"
LD "v203,s[48:51],s80 offen offset:3072 lds\n"
MF "v[72:75],a[28:31],v[176:179],v[72:75]\n"
TR "a[244:245],v3 offset:57344\n"
MF "v[72:75],a[68:71],v[180:183],v[72:75]\n"
TR "a[246:247],v3 offset:59392\n"
MF "v[72:75],a[108:111],v[184:187],v[72:75]\n"
MF "v[80:83],a[112:115],v[184:187],v[80:83]\n"
MF "v[80:83],a[72:75],v[180:183],v[80:83]\n"
MF "v[80:83],a[32:35],v[176:179],v[80:83]\n"
MF "v[88:91],a[36:39],v[176:179],v[88:91]\n"
MF "v[88:91],a[76:79],v[180:183],v[88:91]\n"
"s_waitcnt vmcnt(24) lgkmcnt(0)\n"
MF "v[88:91],a[116:119],v[184:187],v[88:91]\n"
"s_barrier\n"
MF "v[92:95],a[116:119],a[244:247],v[92:95]\n"
"s_add_u32 s88,s88,s65\n"
TR "v[176:177],v4\n"
MF "v[92:95],a[76:79],a[240:243],v[92:95]\n"
"s_add_u32 m0,s79,49152\n"
TR "v[178:179],v4 offset:2048\n"
MF "v[92:95],a[36:39],v[188:191],v[92:95]\n"
LD "v192,s[48:51],s88 offen lds\n"
MF "v[84:87],a[32:35],v[188:191],v[84:87]\n"
TR "v[180:181],v4 offset:4096\n"
MF "v[84:87],a[72:75],a[240:243],v[84:87]\n"
TR "v[182:183],v4 offset:6144\n"
MF "v[84:87],a[112:115],a[244:247],v[84:87]\n"
LD "v193,s[48:51],s88 offen offset:1024 lds\n"
MF "v[76:79],a[108:111],a[244:247],v[76:79]\n"
TR "v[184:185],v4 offset:8192\n"
MF "v[76:79],a[68:71],a[240:243],v[76:79]\n"
TR "v[186:187],v4 offset:10240\n"
MF "v[76:79],a[28:31],v[188:191],v[76:79]\n"
MF "v[68:71],a[24:27],v[188:191],v[68:71]\n"
MF "v[68:71],a[64:67],a[240:243],v[68:71]\n"
LD "v194,s[48:51],s88 offen offset:2048 lds\n"
MF "v[68:71],a[104:107],a[244:247],v[68:71]\n"
MF "v[60:63],a[100:103],a[244:247],v[60:63]\n"
MF "v[60:63],a[60:63],a[240:243],v[60:63]\n"
MF "v[60:63],a[20:23],v[188:191],v[60:63]\n"
"s_waitcnt lgkmcnt(0)\n"
LD "v195,s[48:51],s88 offen offset:3072 lds\n"
MF "v[16:19],a[140:143],v[176:179],v[16:19]\n"
TR "v[188:189],v5\n"
MF "v[16:19],a[180:183],v[180:183],v[16:19]\n"
TR "v[190:191],v5 offset:2048\n"
MF "v[16:19],a[220:223],v[184:187],v[16:19]\n"
MF "v[24:27],a[224:227],v[184:187],v[24:27]\n"
TR "a[240:241],v5 offset:4096\n"
MF "v[24:27],a[184:187],v[180:183],v[24:27]\n"
"s_add_u32 m0,s79,53248\n"
TR "a[242:243],v5 offset:6144\n"
MF "v[24:27],a[144:147],v[176:179],v[24:27]\n"
LD "v196,s[48:51],s88 offen lds\n"
MF "v[32:35],a[148:151],v[176:179],v[32:35]\n"
TR "a[244:245],v5 offset:8192\n"
MF "v[32:35],a[188:191],v[180:183],v[32:35]\n"
TR "a[246:247],v5 offset:10240\n"
MF "v[32:35],a[228:231],v[184:187],v[32:35]\n"
LD "v197,s[48:51],s88 offen offset:1024 lds\n"
MF "v[40:43],a[232:235],v[184:187],v[40:43]\n"
MF "v[40:43],a[192:195],v[180:183],v[40:43]\n"
MF "v[40:43],a[152:155],v[176:179],v[40:43]\n"
MF "v[48:51],a[156:159],v[176:179],v[48:51]\n"
MF "v[48:51],a[196:199],v[180:183],v[48:51]\n"
LD "v198,s[48:51],s88 offen offset:2048 lds\n"
MF "v[48:51],a[236:239],v[184:187],v[48:51]\n"
"s_waitcnt lgkmcnt(0)\n"
MF "v[52:55],a[236:239],a[244:247],v[52:55]\n"
TR "v[176:177],v6\n"
MF "v[52:55],a[196:199],a[240:243],v[52:55]\n"
TR "v[178:179],v6 offset:2048\n"
MF "v[52:55],a[156:159],v[188:191],v[52:55]\n"
LD "v199,s[48:51],s88 offen offset:3072 lds\n"
MF "v[44:47],a[152:155],v[188:191],v[44:47]\n"
TR "v[180:181],v6 offset:4096\n"
MF "v[44:47],a[192:195],a[240:243],v[44:47]\n"
TR "v[182:183],v6 offset:6144\n"
MF "v[44:47],a[232:235],a[244:247],v[44:47]\n"
MF "v[36:39],a[228:231],a[244:247],v[36:39]\n"
TR "v[184:185],v6 offset:8192\n"
MF "v[36:39],a[188:191],a[240:243],v[36:39]\n"
"s_add_u32 m0,s79,57344\n"
TR "v[186:187],v6 offset:10240\n"
MF "v[36:39],a[148:151],v[188:191],v[36:39]\n"
LD "v200,s[48:51],s88 offen lds\n"
MF "v[28:31],a[144:147],v[188:191],v[28:31]\n"
MF "v[28:31],a[184:187],a[240:243],v[28:31]\n"
MF "v[28:31],a[224:227],a[244:247],v[28:31]\n"
MF "v[20:23],a[220:223],a[244:247],v[20:23]\n"
LD "v201,s[48:51],s88 offen offset:1024 lds\n"
MF "v[20:23],a[180:183],a[240:243],v[20:23]\n"
MF "v[20:23],a[140:143],v[188:191],v[20:23]\n"
"s_waitcnt lgkmcnt(0)\n"
MF "v[56:59],a[140:143],v[176:179],v[56:59]\n"
TR "v[188:189],v7\n"
MF "v[56:59],a[180:183],v[180:183],v[56:59]\n"
TR "v[190:191],v7 offset:2048\n"
MF "v[56:59],a[220:223],v[184:187],v[56:59]\n"
LD "v202,s[48:51],s88 offen offset:2048 lds\n"
MF "v[64:67],a[224:227],v[184:187],v[64:67]\n"
TR "a[240:241],v7 offset:4096\n"
"v_mov_b32 v244,0\n"
MF "v[64:67],a[184:187],v[180:183],v[64:67]\n"
TR "a[242:243],v7 offset:6144\n"
"v_mov_b32 v245,0\n"
MF "v[64:67],a[144:147],v[176:179],v[64:67]\n"
LD "v203,s[48:51],s88 offen offset:3072 lds\n"
MF "v[72:75],a[148:151],v[176:179],v[72:75]\n"
TR "a[244:245],v7 offset:8192\n"
"v_mov_b32 v246,0\n"
MF "v[72:75],a[188:191],v[180:183],v[72:75]\n"
TR "a[246:247],v7 offset:10240\n"
"v_mov_b32 v247,0\n"
MF "v[72:75],a[228:231],v[184:187],v[72:75]\n"
"v_mov_b32 v248,0\n"
"v_mov_b32 v249,0\n"
MF "v[80:83],a[232:235],v[184:187],v[80:83]\n"
"v_mov_b32 v250,0\n"
"v_mov_b32 v251,0\n"
MF "v[80:83],a[192:195],v[180:183],v[80:83]\n"
MF "v[80:83],a[152:155],v[176:179],v[80:83]\n"
MF "v[88:91],a[156:159],v[176:179],v[88:91]\n"
MF "v[88:91],a[196:199],v[180:183],v[88:91]\n"
"s_waitcnt vmcnt(24) lgkmcnt(0)\n"
MF "v[88:91],a[236:239],v[184:187],v[88:91]\n"
"s_barrier\n"
MF "v[92:95],a[236:239],a[244:247],v[92:95]\n"
"s_add_u32 s80,s88,122880\n"
TR "v[176:177],v0 offset:24576\n"
MF "v[92:95],a[196:199],a[240:243],v[92:95]\n"
"s_add_u32 m0,s79,98304\n"
"s_add_u32 s85,s85,1\n"
TR "v[178:179],v0 offset:26624\n"
MF "v[92:95],a[156:159],v[188:191],v[92:95]\n"
LD "v192,s[48:51],s80 offen lds\n"
MF "v[84:87],a[152:155],v[188:191],v[84:87]\n"
"s_nop 0\n"
TR "v[180:181],v0 offset:28672\n"
"ds_write_addtid_b32 v16 offset:24576\n"
MF "v[84:87],a[192:195],a[240:243],v[84:87]\n"
"s_nop 0\n"
TR "v[182:183],v0 offset:30720\n"
"ds_write_addtid_b32 v17 offset:24832\n"
MF "v[84:87],a[232:235],a[244:247],v[84:87]\n"
LD "v193,s[48:51],s80 offen offset:1024 lds\n"
MF "v[76:79],a[228:231],a[244:247],v[76:79]\n"
"s_nop 0\n"
TR "v[184:185],v0 offset:32768\n"
"ds_write_addtid_b32 v18 offset:25088\n"
MF "v[76:79],a[188:191],a[240:243],v[76:79]\n"
"s_nop 0\n"
TR "v[186:187],v0 offset:34816\n"
"ds_write_addtid_b32 v19 offset:25344\n"
MF "v[76:79],a[148:151],v[188:191],v[76:79]\n"
"s_nop 0\n"
"ds_write_addtid_b32 v20 offset:25600\n"
"ds_write_addtid_b32 v21 offset:25856\n"
MF "v[68:71],a[144:147],v[188:191],v[68:71]\n"
LD "v194,s[48:51],s80 offen offset:2048 lds\n"
MF "v[68:71],a[184:187],a[240:243],v[68:71]\n"
"s_nop 0\n"
"ds_write_addtid_b32 v22 offset:26112\n"
"ds_write_addtid_b32 v23 offset:26368\n"
MF "v[68:71],a[224:227],a[244:247],v[68:71]\n"
"s_nop 0\n"
"ds_write_addtid_b32 v24 offset:26624\n"
"ds_write_addtid_b32 v25 offset:26880\n"
MF "v[60:63],a[220:223],a[244:247],v[60:63]\n"
"s_nop 0\n"
"ds_write_addtid_b32 v26 offset:27136\n"
"ds_write_addtid_b32 v27 offset:27392\n"
MF "v[60:63],a[180:183],a[240:243],v[60:63]\n"
MF "v[60:63],a[140:143],v[188:191],v[60:63]\n"
"s_waitcnt lgkmcnt(9)\n"
"s_cmp_lg_u32 s85,5\n"
"s_cbranch_scc1 .Lpl05\n"
".Lpl09:\n"
MF "v[96:99],a[0:3],v[176:179],v[252:255]\n"
"s_nop 0\n"
TR "v[188:189],v1 offset:24576\n"
"ds_write_addtid_b32 v28 offset:27648\n"
MF "v[96:99],a[40:43],v[180:183],v[96:99]\n"
"s_nop 0\n"
TR "v[190:191],v1 offset:26624\n"
"ds_write_addtid_b32 v29 offset:27904\n"
MF "v[96:99],a[80:83],v[184:187],v[96:99]\n"
LD "v195,s[48:51],s80 offen offset:3072 lds\n"
MF "v[104:107],a[84:87],v[184:187],v[252:255]\n"
"s_add_u32 s60,s64,1\n"
TR "a[240:241],v1 offset:28672\n"
"ds_write_addtid_b32 v30 offset:28160\n"
MF "v[104:107],a[44:47],v[180:183],v[104:107]\n"
"s_add_u32 m0,s79,102400\n"
TR "a[242:243],v1 offset:30720\n"
"ds_write_addtid_b32 v31 offset:24320\n"
MF "v[104:107],a[4:7],v[176:179],v[104:107]\n"
LD "v196,s[48:51],s80 offen lds\n"
MF "v[112:115],a[8:11],v[176:179],v[252:255]\n"
"s_cmp_eq_u32 s60,8\n"
"s_cselect_b32 s60,0,s60\n"
TR "a[244:245],v1 offset:32768\n"
MF "v[112:115],a[48:51],v[180:183],v[112:115]\n"
"s_cmp_eq_u32 s60,10\n"
"s_cselect_b32 s60,s61,s60\n"
TR "a[246:247],v1 offset:34816\n"
MF "v[112:115],a[88:91],v[184:187],v[112:115]\n"
"s_sub_u32 s65,s60,s64\n"
"ds_write_addtid_b32 v32 offset:24576\n"
"ds_write_addtid_b32 v33 offset:24832\n"
MF "v[120:123],a[92:95],v[184:187],v[252:255]\n"
"s_lshl_b32 s65,s65,7\n"
"ds_write_addtid_b32 v34 offset:25088\n"
"ds_write_addtid_b32 v35 offset:25344\n"
MF "v[120:123],a[52:55],v[180:183],v[120:123]\n"
LD "v197,s[48:51],s80 offen offset:1024 lds\n"
MF "v[120:123],a[12:15],v[176:179],v[120:123]\n"
"s_mov_b32 s64,s60\n"
"ds_write_addtid_b32 v36 offset:25600\n"
"ds_write_addtid_b32 v37 offset:25856\n"
MF "v[128:131],a[16:19],v[176:179],v[252:255]\n"
"s_nop 0\n"
"ds_write_addtid_b32 v38 offset:26112\n"
"ds_write_addtid_b32 v39 offset:26368\n"
MF "v[128:131],a[56:59],v[180:183],v[128:131]\n"
"s_nop 0\n"
"ds_write_addtid_b32 v40 offset:26624\n"
"ds_write_addtid_b32 v41 offset:26880\n"
MF "v[128:131],a[96:99],v[184:187],v[128:131]\n"
"s_waitcnt lgkmcnt(10)\n"
LD "v198,s[48:51],s80 offen offset:2048 lds\n"
MF "v[132:135],a[96:99],a[244:247],v[252:255]\n"
"s_nop 0\n"
TR "v[176:177],v2 offset:24576\n"
"ds_write_addtid_b32 v42 offset:27136\n"
MF "v[132:135],a[56:59],a[240:243],v[132:135]\n"
"s_nop 0\n"
TR "v[178:179],v2 offset:26624\n"
"ds_write_addtid_b32 v43 offset:27392\n"
MF "v[132:135],a[16:19],v[188:191],v[132:135]\n"
"s_nop 0\n"
"ds_write_addtid_b32 v44 offset:27648\n"
"ds_write_addtid_b32 v45 offset:27904\n"
MF "v[124:127],a[12:15],v[188:191],v[252:255]\n"
"s_nop 0\n"
TR "v[180:181],v2 offset:28672\n"
"ds_write_addtid_b32 v46 offset:28160\n"
MF "v[124:127],a[52:55],a[240:243],v[124:127]\n"
"s_nop 0\n"
TR "v[182:183],v2 offset:30720\n"
"ds_write_addtid_b32 v47 offset:28416\n"
MF "v[124:127],a[92:95],a[244:247],v[124:127]\n"
LD "v199,s[48:51],s80 offen offset:3072 lds\n"
MF "v[116:119],a[88:91],a[244:247],v[252:255]\n"
"s_nop 0\n"
TR "v[184:185],v2 offset:32768\n"
"ds_write_addtid_b32 v48 offset:28672\n"
MF "v[116:119],a[48:51],a[240:243],v[116:119]\n"
"s_add_u32 m0,s79,106496\n"
TR "v[186:187],v2 offset:34816\n"
"ds_write_addtid_b32 v49 offset:24832\n"
MF "v[116:119],a[8:11],v[188:191],v[116:119]\n"
LD "v200,s[48:51],s80 offen lds\n"
MF "v[108:111],a[4:7],v[188:191],v[252:255]\n"
"s_nop 0\n"
"ds_write_addtid_b32 v50 offset:25088\n"
"ds_write_addtid_b32 v51 offset:25344\n"
MF "v[108:111],a[44:47],a[240:243],v[108:111]\n"
"s_nop 0\n"
"ds_write_addtid_b32 v52 offset:25600\n"
"ds_write_addtid_b32 v53 offset:25856\n"
MF "v[108:111],a[84:87],a[244:247],v[108:111]\n"
"s_nop 0\n"
"ds_write_addtid_b32 v54 offset:26112\n"
"ds_write_addtid_b32 v55 offset:26368\n"
MF "v[100:103],a[80:83],a[244:247],v[252:255]\n"
MF "v[100:103],a[40:43],a[240:243],v[100:103]\n"
LD "v201,s[48:51],s80 offen offset:1024 lds\n"
MF "v[100:103],a[0:3],v[188:191],v[100:103]\n"
"s_waitcnt lgkmcnt(7)\n"
MF "v[136:139],a[0:3],v[176:179],v[252:255]\n"
TR "v[188:189],v3 offset:24576\n"
MF "v[136:139],a[40:43],v[180:183],v[136:139]\n"
TR "v[190:191],v3 offset:26624\n"
MF "v[136:139],a[80:83],v[184:187],v[136:139]\n"
LD "v202,s[48:51],s80 offen offset:2048 lds\n"
MF "v[144:147],a[84:87],v[184:187],v[252:255]\n"
TR "a[240:241],v3 offset:28672\n"
MF "v[144:147],a[44:47],v[180:183],v[144:147]\n"
TR "a[242:243],v3 offset:30720\n"
MF "v[144:147],a[4:7],v[176:179],v[144:147]\n"
LD "v203,s[48:51],s80 offen offset:3072 lds\n"
MF "v[152:155],a[8:11],v[176:179],v[252:255]\n"
TR "a[244:245],v3 offset:32768\n"
MF "v[152:155],a[48:51],v[180:183],v[152:155]\n"
TR "a[246:247],v3 offset:34816\n"
MF "v[152:155],a[88:91],v[184:187],v[152:155]\n"
MF "v[160:163],a[92:95],v[184:187],v[252:255]\n"
MF "v[160:163],a[52:55],v[180:183],v[160:163]\n"
MF "v[160:163],a[12:15],v[176:179],v[160:163]\n"
MF "v[168:171],a[16:19],v[176:179],v[252:255]\n"
MF "v[168:171],a[56:59],v[180:183],v[168:171]\n"
"s_waitcnt vmcnt(24) lgkmcnt(0)\n"
MF "v[168:171],a[96:99],v[184:187],v[168:171]\n"
"s_barrier\n"
MF "v[172:175],a[96:99],a[244:247],v[252:255]\n"
"s_add_u32 s89,s89,s65\n"
TR "v[176:177],v0\n"
MF "v[172:175],a[56:59],a[240:243],v[172:175]\n"
"s_add_u32 m0,s79,24576\n"
TR "v[178:179],v0 offset:2048\n"
MF "v[172:175],a[16:19],v[188:191],v[172:175]\n"
LD "v192,s[48:51],s89 offen lds\n"
MF "v[164:167],a[12:15],v[188:191],v[252:255]\n"
TR "v[180:181],v0 offset:4096\n"
MF "v[164:167],a[52:55],a[240:243],v[164:167]\n"
TR "v[182:183],v0 offset:6144\n"
MF "v[164:167],a[92:95],a[244:247],v[164:167]\n"
LD "v193,s[48:51],s89 offen offset:1024 lds\n"
MF "v[156:159],a[88:91],a[244:247],v[252:255]\n"
TR "v[184:185],v0 offset:8192\n"
MF "v[156:159],a[48:51],a[240:243],v[156:159]\n"
"s_nop 0\n"
TR "v[186:187],v0 offset:10240\n"
"ds_write_addtid_b32 v56 offset:49152\n"
MF "v[156:159],a[8:11],v[188:191],v[156:159]\n"
"s_nop 0\n"
"ds_write_addtid_b32 v57 offset:49408\n"
"ds_write_addtid_b32 v58 offset:49664\n"
MF "v[148:151],a[4:7],v[188:191],v[252:255]\n"
LD "v194,s[48:51],s89 offen offset:2048 lds\n"
MF "v[148:151],a[44:47],a[240:243],v[148:151]\n"
"buffer_load_dword v10,v12,s[40:43],s86 offen sc1\n"
MF "v[148:151],a[84:87],a[244:247],v[148:151]\n"
"s_nop 0\n"
"ds_write_addtid_b32 v59 offset:49920\n"
"ds_write_addtid_b32 v60 offset:50176\n"
MF "v[140:143],a[80:83],a[244:247],v[252:255]\n"
"s_nop 0\n"
"ds_write_addtid_b32 v61 offset:50432\n"
"ds_write_addtid_b32 v62 offset:50688\n"
MF "v[140:143],a[40:43],a[240:243],v[140:143]\n"
"s_nop 0\n"
"ds_write_addtid_b32 v63 offset:50944\n"
"ds_write_addtid_b32 v64 offset:51200\n"
MF "v[140:143],a[0:3],v[188:191],v[140:143]\n"
"s_waitcnt lgkmcnt(9)\n"
LD "v195,s[48:51],s89 offen offset:3072 lds\n"
MF "v[96:99],a[120:123],v[176:179],v[96:99]\n"
"s_nop 0\n"
TR "v[188:189],v1\n"
"ds_write_addtid_b32 v65 offset:51456\n"
MF "v[96:99],a[160:163],v[180:183],v[96:99]\n"
".Lpg25h:\n"
"s_cbranch_execz .Lpgrole1_h\n"
"s_nop 0\n"
TR "v[190:191],v1 offset:2048\n"
"ds_write_addtid_b32 v66 offset:51712\n"
MF "v[96:99],a[200:203],v[184:187],v[96:99]\n"
"s_nop 0\n"
"ds_write_addtid_b32 v67 offset:51968\n"
"ds_write_addtid_b32 v68 offset:52224\n"
MF "v[104:107],a[204:207],v[184:187],v[104:107]\n"
"s_nop 0\n"
TR "a[240:241],v1 offset:4096\n"
"ds_write_addtid_b32 v69 offset:52480\n"
MF "v[104:107],a[164:167],v[180:183],v[104:107]\n"
"s_add_u32 m0,s79,28672\n"
TR "a[242:243],v1 offset:6144\n"
"ds_write_addtid_b32 v70 offset:48640\n"
MF "v[104:107],a[124:127],v[176:179],v[104:107]\n"
LD "v196,s[48:51],s89 offen lds\n"
MF "v[112:115],a[128:131],v[176:179],v[112:115]\n"
"s_nop 0\n"
TR "a[244:245],v1 offset:8192\n"
"ds_write_addtid_b32 v71 offset:48896\n"
MF "v[112:115],a[168:171],v[180:183],v[112:115]\n"
"s_nop 0\n"
TR "a[246:247],v1 offset:10240\n"
"ds_write_addtid_b32 v72 offset:49152\n"
MF "v[112:115],a[208:211],v[184:187],v[112:115]\n"
LD "v197,s[48:51],s89 offen offset:1024 lds\n"
MF "v[120:123],a[212:215],v[184:187],v[120:123]\n"
"s_nop 0\n"
"ds_write_addtid_b32 v73 offset:49408\n"
"ds_write_addtid_b32 v74 offset:49664\n"
MF "v[120:123],a[172:175],v[180:183],v[120:123]\n"
"s_nop 0\n"
"ds_write_addtid_b32 v75 offset:49920\n"
"ds_write_addtid_b32 v76 offset:50176\n"
MF "v[120:123],a[132:135],v[176:179],v[120:123]\n"
"s_nop 0\n"
"ds_write_addtid_b32 v77 offset:50432\n"
"ds_write_addtid_b32 v78 offset:50688\n"
MF "v[128:131],a[136:139],v[176:179],v[128:131]\n"
"s_nop 0\n"
"ds_write_addtid_b32 v79 offset:50944\n"
"ds_write_addtid_b32 v80 offset:51200\n"
MF "v[128:131],a[176:179],v[180:183],v[128:131]\n"
LD "v198,s[48:51],s89 offen offset:2048 lds\n"
MF "v[128:131],a[216:219],v[184:187],v[128:131]\n"
"s_waitcnt lgkmcnt(9)\n"
"ds_write_addtid_b32 v81 offset:51456\n"
"ds_write_addtid_b32 v82 offset:51712\n"
MF "v[132:135],a[216:219],a[244:247],v[132:135]\n"
"s_nop 0\n"
TR "v[176:177],v2\n"
"ds_write_addtid_b32 v83 offset:51968\n"
MF "v[132:135],a[176:179],a[240:243],v[132:135]\n"
"s_nop 0\n"
TR "v[178:179],v2 offset:2048\n"
"ds_write_addtid_b32 v84 offset:52224\n"
MF "v[132:135],a[136:139],v[188:191],v[132:135]\n"
LD "v199,s[48:51],s89 offen offset:3072 lds\n"
MF "v[124:127],a[132:135],v[188:191],v[124:127]\n"
"s_nop 0\n"
TR "v[180:181],v2 offset:4096\n"
"ds_write_addtid_b32 v85 offset:52480\n"
MF "v[124:127],a[172:175],a[240:243],v[124:127]\n"
"s_nop 0\n"
TR "v[182:183],v2 offset:6144\n"
"ds_write_addtid_b32 v86 offset:52736\n"
MF "v[124:127],a[212:215],a[244:247],v[124:127]\n"
"s_nop 0\n"
"ds_write_addtid_b32 v87 offset:52992\n"
"ds_write_addtid_b32 v88 offset:53248\n"
MF "v[116:119],a[208:211],a[244:247],v[116:119]\n"
"s_nop 0\n"
TR "v[184:185],v2 offset:8192\n"
"ds_write_addtid_b32 v89 offset:53504\n"
MF "v[116:119],a[168:171],a[240:243],v[116:119]\n"
"s_add_u32 m0,s79,32768\n"
TR "v[186:187],v2 offset:10240\n"
"ds_write_addtid_b32 v90 offset:49664\n"
MF "v[116:119],a[128:131],v[188:191],v[116:119]\n"
LD "v200,s[48:51],s89 offen lds\n"
MF "v[108:111],a[124:127],v[188:191],v[108:111]\n"
"s_nop 0\n"
"ds_write_addtid_b32 v91 offset:49920\n"
"ds_write_addtid_b32 v92 offset:50176\n"
MF "v[108:111],a[164:167],a[240:243],v[108:111]\n"
"s_nop 0\n"
"ds_write_addtid_b32 v93 offset:50432\n"
"ds_write_addtid_b32 v94 offset:50688\n"
MF "v[108:111],a[204:207],a[244:247],v[108:111]\n"
"ds_write_addtid_b32 v95 offset:50944\n"
MF "v[100:103],a[200:203],a[244:247],v[100:103]\n"
LD "v201,s[48:51],s89 offen offset:1024 lds\n"
MF "v[100:103],a[160:163],a[240:243],v[100:103]\n"
MF "v[100:103],a[120:123],v[188:191],v[100:103]\n"
"s_waitcnt lgkmcnt(6)\n"
MF "v[136:139],a[120:123],v[176:179],v[136:139]\n"
TR "v[188:189],v3\n"
MF "v[136:139],a[160:163],v[180:183],v[136:139]\n"
TR "v[190:191],v3 offset:2048\n"
MF "v[136:139],a[200:203],v[184:187],v[136:139]\n"
LD "v202,s[48:51],s89 offen offset:2048 lds\n"
MF "v[144:147],a[204:207],v[184:187],v[144:147]\n"
TR "a[240:241],v3 offset:4096\n"
MF "v[144:147],a[164:167],v[180:183],v[144:147]\n"
TR "a[242:243],v3 offset:6144\n"
MF "v[144:147],a[124:127],v[176:179],v[144:147]\n"
LD "v203,s[48:51],s89 offen offset:3072 lds\n"
MF "v[152:155],a[128:131],v[176:179],v[152:155]\n"
TR "a[244:245],v3 offset:8192\n"
MF "v[152:155],a[168:171],v[180:183],v[152:155]\n"
TR "a[246:247],v3 offset:10240\n"
MF "v[152:155],a[208:211],v[184:187],v[152:155]\n"
MF "v[160:163],a[212:215],v[184:187],v[160:163]\n"
MF "v[160:163],a[172:175],v[180:183],v[160:163]\n"
MF "v[160:163],a[132:135],v[176:179],v[160:163]\n"
MF "v[168:171],a[136:139],v[176:179],v[168:171]\n"
MF "v[168:171],a[176:179],v[180:183],v[168:171]\n"
"s_waitcnt vmcnt(25) lgkmcnt(0)\n"
MF "v[168:171],a[216:219],v[184:187],v[168:171]\n"
"s_barrier\n"
MF "v[172:175],a[216:219],a[244:247],v[172:175]\n"
"s_add_u32 s80,s89,122880\n"
TR "v[176:177],v0 offset:49152\n"
MF "v[172:175],a[176:179],a[240:243],v[172:175]\n"
"s_add_u32 m0,s79,0\n"
"s_mov_b32 s82,s70\n"
TR "v[178:179],v0 offset:51200\n"
MF "v[172:175],a[136:139],v[188:191],v[172:175]\n"
LD "v192,s[48:51],s80 offen lds\n"
MF "v[164:167],a[132:135],v[188:191],v[164:167]\n"
TR "v[180:181],v0 offset:53248\n"
MF "v[164:167],a[172:175],a[240:243],v[164:167]\n"
TR "v[182:183],v0 offset:55296\n"
MF "v[164:167],a[212:215],a[244:247],v[164:167]\n"
LD "v193,s[48:51],s80 offen offset:1024 lds\n"
MF "v[156:159],a[208:211],a[244:247],v[156:159]\n"
TR "v[184:185],v0 offset:57344\n"
MF "v[156:159],a[168:171],a[240:243],v[156:159]\n"
TR "v[186:187],v0 offset:59392\n"
MF "v[156:159],a[128:131],v[188:191],v[156:159]\n"
MF "v[148:151],a[124:127],v[188:191],v[148:151]\n"
MF "v[148:151],a[164:167],a[240:243],v[148:151]\n"
LD "v194,s[48:51],s80 offen offset:2048 lds\n"
MF "v[148:151],a[204:207],a[244:247],v[148:151]\n"
MF "v[140:143],a[200:203],a[244:247],v[140:143]\n"
MF "v[140:143],a[160:163],a[240:243],v[140:143]\n"
MF "v[140:143],a[120:123],v[188:191],v[140:143]\n"
"s_waitcnt lgkmcnt(0)\n"
LD "v195,s[48:51],s80 offen offset:3072 lds\n"
MF "v[96:99],a[20:23],v[176:179],v[96:99]\n"
TR "v[188:189],v1 offset:49152\n"
MF "v[96:99],a[60:63],v[180:183],v[96:99]\n"
TR "v[190:191],v1 offset:51200\n"
MF "v[96:99],a[100:103],v[184:187],v[96:99]\n"
MF "v[104:107],a[104:107],v[184:187],v[104:107]\n"
TR "a[240:241],v1 offset:53248\n"
MF "v[104:107],a[64:67],v[180:183],v[104:107]\n"
"s_add_u32 m0,s79,4096\n"
TR "a[242:243],v1 offset:55296\n"
MF "v[104:107],a[24:27],v[176:179],v[104:107]\n"
LD "v196,s[48:51],s80 offen lds\n"
MF "v[112:115],a[28:31],v[176:179],v[112:115]\n"
TR "a[244:245],v1 offset:57344\n"
MF "v[112:115],a[68:71],v[180:183],v[112:115]\n"
TR "a[246:247],v1 offset:59392\n"
MF "v[112:115],a[108:111],v[184:187],v[112:115]\n"
LD "v197,s[48:51],s80 offen offset:1024 lds\n"
MF "v[120:123],a[112:115],v[184:187],v[120:123]\n"
MF "v[120:123],a[72:75],v[180:183],v[120:123]\n"
MF "v[120:123],a[32:35],v[176:179],v[120:123]\n"
MF "v[128:131],a[36:39],v[176:179],v[128:131]\n"
MF "v[128:131],a[76:79],v[180:183],v[128:131]\n"
LD "v198,s[48:51],s80 offen offset:2048 lds\n"
MF "v[128:131],a[116:119],v[184:187],v[128:131]\n"
"s_waitcnt lgkmcnt(0)\n"
MF "v[132:135],a[116:119],a[244:247],v[132:135]\n"
TR "v[176:177],v2 offset:49152\n"
MF "v[132:135],a[76:79],a[240:243],v[132:135]\n"
TR "v[178:179],v2 offset:51200\n"
MF "v[132:135],a[36:39],v[188:191],v[132:135]\n"
LD "v199,s[48:51],s80 offen offset:3072 lds\n"
MF "v[124:127],a[32:35],v[188:191],v[124:127]\n"
TR "v[180:181],v2 offset:53248\n"
MF "v[124:127],a[72:75],a[240:243],v[124:127]\n"
TR "v[182:183],v2 offset:55296\n"
MF "v[124:127],a[112:115],a[244:247],v[124:127]\n"
MF "v[116:119],a[108:111],a[244:247],v[116:119]\n"
TR "v[184:185],v2 offset:57344\n"
MF "v[116:119],a[68:71],a[240:243],v[116:119]\n"
"s_add_u32 m0,s79,8192\n"
TR "v[186:187],v2 offset:59392\n"
MF "v[116:119],a[28:31],v[188:191],v[116:119]\n"
LD "v200,s[48:51],s80 offen lds\n"
MF "v[108:111],a[24:27],v[188:191],v[108:111]\n"
MF "v[108:111],a[64:67],a[240:243],v[108:111]\n"
MF "v[108:111],a[104:107],a[244:247],v[108:111]\n"
MF "v[100:103],a[100:103],a[244:247],v[100:103]\n"
LD "v201,s[48:51],s80 offen offset:1024 lds\n"
MF "v[100:103],a[60:63],a[240:243],v[100:103]\n"
"s_waitcnt vmcnt(19)\n"
"s_cmp_lt_u32 s85,6\n"
MF "v[100:103],a[20:23],v[188:191],v[100:103]\n"
"s_cbranch_scc1 .Ljs4\n"
"v_mov_b32 v236,v228\n"
"v_mov_b32 v237,v229\n"
MF "v[96:99],a[252:255],v[208:211],v[96:99]\n"
MF "v[104:107],a[252:255],v[212:215],v[104:107]\n"
MF "v[112:115],a[252:255],v[216:219],v[112:115]\n"
MF "v[120:123],a[252:255],v[220:223],v[120:123]\n"
MF "v[128:131],a[252:255],v[224:227],v[128:131]\n"
MF "v[100:103],a[248:251],v[208:211],v[100:103]\n"
MF "v[108:111],a[248:251],v[212:215],v[108:111]\n"
MF "v[116:119],a[248:251],v[216:219],v[116:119]\n"
MF "v[124:127],a[248:251],v[220:223],v[124:127]\n"
MF "v[132:135],a[248:251],v[224:227],v[132:135]\n"
MF "v[248:251],v[230:233],a[250:253],v[248:251]\n"
MF "v[244:247],v[234:237],a[250:253],v[244:247]\n"
".Ljs4:\n"
"s_waitcnt lgkmcnt(0)\n"
MF "v[136:139],a[20:23],v[176:179],v[136:139]\n"
TR "v[188:189],v3 offset:49152\n"
"v_sub_u32 v10,v10,v11\n"
MF "v[136:139],a[60:63],v[180:183],v[136:139]\n"
TR "v[190:191],v3 offset:51200\n"
MF "v[136:139],a[100:103],v[184:187],v[136:139]\n"
LD "v202,s[48:51],s80 offen offset:2048 lds\n"
MF "v[144:147],a[104:107],v[184:187],v[144:147]\n"
TR "a[240:241],v3 offset:53248\n"
MF "v[144:147],a[64:67],v[180:183],v[144:147]\n"
TR "a[242:243],v3 offset:55296\n"
MF "v[144:147],a[24:27],v[176:179],v[144:147]\n"
LD "v203,s[48:51],s80 offen offset:3072 lds\n"
MF "v[152:155],a[28:31],v[176:179],v[152:155]\n"
TR "a[244:245],v3 offset:57344\n"
MF "v[152:155],a[68:71],v[180:183],v[152:155]\n"
TR "a[246:247],v3 offset:59392\n"
MF "v[152:155],a[108:111],v[184:187],v[152:155]\n"
".Lnf0:\n"
"v_readlane_b32 s58,v10,s67\n"
"s_cmp_lt_u32 s58,s85\n"
"s_cbranch_scc1 .Lsp0\n"
".Lnb0:\n"
MF "v[160:163],a[112:115],v[184:187],v[160:163]\n"
"s_add_u32 s70,s70,20480\n"
".Lnf1:\n"
"v_cmp_gt_u32 vcc,4,v10\n"
"s_cbranch_vccnz .Lsp1\n"
".Lnb1:\n"
MF "v[160:163],a[72:75],v[180:183],v[160:163]\n"
MF "v[160:163],a[32:35],v[176:179],v[160:163]\n"
MF "v[168:171],a[36:39],v[176:179],v[168:171]\n"
MF "v[168:171],a[76:79],v[180:183],v[168:171]\n"
"s_waitcnt vmcnt(25) lgkmcnt(0)\n"
MF "v[168:171],a[116:119],v[184:187],v[168:171]\n"
"s_barrier\n"
MF "v[172:175],a[116:119],a[244:247],v[172:175]\n"
"s_add_u32 s88,s88,s65\n"
TR "v[176:177],v4\n"
MF "v[172:175],a[76:79],a[240:243],v[172:175]\n"
"s_add_u32 m0,s79,49152\n"
TR "v[178:179],v4 offset:2048\n"
MF "v[172:175],a[36:39],v[188:191],v[172:175]\n"
LD "v192,s[48:51],s88 offen lds\n"
MF "v[164:167],a[32:35],v[188:191],v[164:167]\n"
TR "v[180:181],v4 offset:4096\n"
MF "v[164:167],a[72:75],a[240:243],v[164:167]\n"
TR "v[182:183],v4 offset:6144\n"
MF "v[164:167],a[112:115],a[244:247],v[164:167]\n"
LD "v193,s[48:51],s88 offen offset:1024 lds\n"
MF "v[156:159],a[108:111],a[244:247],v[156:159]\n"
TR "v[184:185],v4 offset:8192\n"
MF "v[156:159],a[68:71],a[240:243],v[156:159]\n"
"s_add_u32 s2,s2,204800\n"
TR "v[186:187],v4 offset:10240\n"
MF "v[156:159],a[28:31],v[188:191],v[156:159]\n"
LD "v[228:231],v9,s[40:43],s2 offen sc1\n"
MF "v[148:151],a[24:27],v[188:191],v[148:151]\n"
MF "v[148:151],a[64:67],a[240:243],v[148:151]\n"
LD "v194,s[48:51],s88 offen offset:2048 lds\n"
MF "v[148:151],a[104:107],a[244:247],v[148:151]\n"
MF "v[140:143],a[100:103],a[244:247],v[140:143]\n"
MF "v[140:143],a[60:63],a[240:243],v[140:143]\n"
MF "v[140:143],a[20:23],v[188:191],v[140:143]\n"
"s_waitcnt lgkmcnt(0)\n"
LD "v195,s[48:51],s88 offen offset:3072 lds\n"
MF "v[96:99],a[140:143],v[176:179],v[96:99]\n"
TR "v[188:189],v5\n"
MF "v[96:99],a[180:183],v[180:183],v[96:99]\n"
"s_add_u32 s2,s2,204800\n"
TR "v[190:191],v5 offset:2048\n"
MF "v[96:99],a[220:223],v[184:187],v[96:99]\n"
LD "v[232:235],v9,s[40:43],s2 offen sc1\n"
MF "v[104:107],a[224:227],v[184:187],v[104:107]\n"
TR "a[240:241],v5 offset:4096\n"
MF "v[104:107],a[184:187],v[180:183],v[104:107]\n"
"s_add_u32 m0,s79,53248\n"
TR "a[242:243],v5 offset:6144\n"
MF "v[104:107],a[144:147],v[176:179],v[104:107]\n"
LD "v196,s[48:51],s88 offen lds\n"
MF "v[112:115],a[148:151],v[176:179],v[112:115]\n"
TR "a[244:245],v5 offset:8192\n"
MF "v[112:115],a[188:191],v[180:183],v[112:115]\n"
TR "a[246:247],v5 offset:10240\n"
MF "v[112:115],a[228:231],v[184:187],v[112:115]\n"
LD "v197,s[48:51],s88 offen offset:1024 lds\n"
MF "v[120:123],a[232:235],v[184:187],v[120:123]\n"
MF "v[120:123],a[192:195],v[180:183],v[120:123]\n"
MF "v[120:123],a[152:155],v[176:179],v[120:123]\n"
LD "v[208:211],v9,s[40:43],s82 offen sc1\n"
MF "v[128:131],a[156:159],v[176:179],v[128:131]\n"
MF "v[128:131],a[196:199],v[180:183],v[128:131]\n"
LD "v198,s[48:51],s88 offen offset:2048 lds\n"
MF "v[128:131],a[236:239],v[184:187],v[128:131]\n"
"s_waitcnt lgkmcnt(0)\n"
MF "v[132:135],a[236:239],a[244:247],v[132:135]\n"
TR "v[176:177],v6\n"
MF "v[132:135],a[196:199],a[240:243],v[132:135]\n"
TR "v[178:179],v6 offset:2048\n"
MF "v[132:135],a[156:159],v[188:191],v[132:135]\n"
LD "v199,s[48:51],s88 offen offset:3072 lds\n"
MF "v[124:127],a[152:155],v[188:191],v[124:127]\n"
TR "v[180:181],v6 offset:4096\n"
MF "v[124:127],a[192:195],a[240:243],v[124:127]\n"
TR "v[182:183],v6 offset:6144\n"
MF "v[124:127],a[232:235],a[244:247],v[124:127]\n"
LD "v[212:215],v9,s[40:43],s82 offen offset:1024 sc1\n"
MF "v[116:119],a[228:231],a[244:247],v[116:119]\n"
TR "v[184:185],v6 offset:8192\n"
MF "v[116:119],a[188:191],a[240:243],v[116:119]\n"
"s_add_u32 m0,s79,57344\n"
TR "v[186:187],v6 offset:10240\n"
MF "v[116:119],a[148:151],v[188:191],v[116:119]\n"
LD "v200,s[48:51],s88 offen lds\n"
MF "v[108:111],a[144:147],v[188:191],v[108:111]\n"
MF "v[108:111],a[184:187],a[240:243],v[108:111]\n"
MF "v[108:111],a[224:227],a[244:247],v[108:111]\n"
MF "v[100:103],a[220:223],a[244:247],v[100:103]\n"
LD "v201,s[48:51],s88 offen offset:1024 lds\n"
MF "v[100:103],a[180:183],a[240:243],v[100:103]\n"
MF "v[100:103],a[140:143],v[188:191],v[100:103]\n"
"s_waitcnt lgkmcnt(0)\n"
LD "v[216:219],v9,s[40:43],s82 offen offset:2048 sc1\n"
MF "v[136:139],a[140:143],v[176:179],v[136:139]\n"
TR "v[188:189],v7\n"
MF "v[136:139],a[180:183],v[180:183],v[136:139]\n"
TR "v[190:191],v7 offset:2048\n"
MF "v[136:139],a[220:223],v[184:187],v[136:139]\n"
LD "v202,s[48:51],s88 offen offset:2048 lds\n"
MF "v[144:147],a[224:227],v[184:187],v[144:147]\n"
TR "a[240:241],v7 offset:4096\n"
MF "v[144:147],a[184:187],v[180:183],v[144:147]\n"
TR "a[242:243],v7 offset:6144\n"
MF "v[144:147],a[144:147],v[176:179],v[144:147]\n"
LD "v203,s[48:51],s88 offen offset:3072 lds\n"
MF "v[152:155],a[148:151],v[176:179],v[152:155]\n"
TR "a[244:245],v7 offset:8192\n"
MF "v[152:155],a[188:191],v[180:183],v[152:155]\n"
TR "a[246:247],v7 offset:10240\n"
MF "v[152:155],a[228:231],v[184:187],v[152:155]\n"
LD "v[220:223],v9,s[40:43],s82 offen offset:3072 sc1\n"
MF "v[160:163],a[232:235],v[184:187],v[160:163]\n"
MF "v[160:163],a[192:195],v[180:183],v[160:163]\n"
MF "v[160:163],a[152:155],v[176:179],v[160:163]\n"
MF "v[168:171],a[156:159],v[176:179],v[168:171]\n"
"s_add_u32 s82,s82,4096\n"
MF "v[168:171],a[196:199],v[180:183],v[168:171]\n"
"s_waitcnt vmcnt(30) lgkmcnt(0)\n"
LD "v[224:227],v9,s[40:43],s82 offen sc1\n"
MF "v[168:171],a[236:239],v[184:187],v[168:171]\n"
"s_barrier\n"
MF "v[172:175],a[236:239],a[244:247],v[172:175]\n"
"s_add_u32 s80,s88,122880\n"
TR "v[176:177],v0 offset:24576\n"
MF "v[172:175],a[196:199],a[240:243],v[172:175]\n"
"s_add_u32 m0,s79,98304\n"
"s_add_u32 s85,s85,1\n"
TR "v[178:179],v0 offset:26624\n"
MF "v[172:175],a[156:159],v[188:191],v[172:175]\n"
LD "v192,s[48:51],s80 offen lds\n"
MF "v[164:167],a[152:155],v[188:191],v[164:167]\n"
"s_nop 0\n"
TR "v[180:181],v0 offset:28672\n"
"ds_write_addtid_b32 v96 offset:24576\n"
MF "v[164:167],a[192:195],a[240:243],v[164:167]\n"
"s_nop 0\n"
TR "v[182:183],v0 offset:30720\n"
"ds_write_addtid_b32 v97 offset:24832\n"
MF "v[164:167],a[232:235],a[244:247],v[164:167]\n"
LD "v193,s[48:51],s80 offen offset:1024 lds\n"
MF "v[156:159],a[228:231],a[244:247],v[156:159]\n"
"s_nop 0\n"
TR "v[184:185],v0 offset:32768\n"
"ds_write_addtid_b32 v98 offset:25088\n"
MF "v[156:159],a[188:191],a[240:243],v[156:159]\n"
"s_nop 0\n"
TR "v[186:187],v0 offset:34816\n"
"ds_write_addtid_b32 v99 offset:25344\n"
MF "v[156:159],a[148:151],v[188:191],v[156:159]\n"
"s_nop 0\n"
"ds_write_addtid_b32 v100 offset:25600\n"
"ds_write_addtid_b32 v101 offset:25856\n"
MF "v[148:151],a[144:147],v[188:191],v[148:151]\n"
LD "v194,s[48:51],s80 offen offset:2048 lds\n"
MF "v[148:151],a[184:187],a[240:243],v[148:151]\n"
"s_nop 0\n"
"ds_write_addtid_b32 v102 offset:26112\n"
"ds_write_addtid_b32 v103 offset:26368\n"
MF "v[148:151],a[224:227],a[244:247],v[148:151]\n"
"s_nop 0\n"
"ds_write_addtid_b32 v104 offset:26624\n"
"ds_write_addtid_b32 v105 offset:26880\n"
MF "v[140:143],a[220:223],a[244:247],v[140:143]\n"
"s_nop 0\n"
"ds_write_addtid_b32 v106 offset:27136\n"
"ds_write_addtid_b32 v107 offset:27392\n"
MF "v[140:143],a[180:183],a[240:243],v[140:143]\n"
MF "v[140:143],a[140:143],v[188:191],v[140:143]\n"
"s_waitcnt lgkmcnt(9)\n"
MF "v[16:19],a[0:3],v[176:179],v[252:255]\n"
"s_nop 0\n"
TR "v[188:189],v1 offset:24576\n"
"ds_write_addtid_b32 v108 offset:27648\n"
MF "v[16:19],a[40:43],v[180:183],v[16:19]\n"
"s_nop 0\n"
TR "v[190:191],v1 offset:26624\n"
"ds_write_addtid_b32 v109 offset:27904\n"
MF "v[16:19],a[80:83],v[184:187],v[16:19]\n"
LD "v195,s[48:51],s80 offen offset:3072 lds\n"
MF "v[24:27],a[84:87],v[184:187],v[252:255]\n"
"s_add_u32 s60,s64,1\n"
TR "a[240:241],v1 offset:28672\n"
"ds_write_addtid_b32 v110 offset:28160\n"
MF "v[24:27],a[44:47],v[180:183],v[24:27]\n"
"s_add_u32 m0,s79,102400\n"
TR "a[242:243],v1 offset:30720\n"
"ds_write_addtid_b32 v111 offset:24320\n"
MF "v[24:27],a[4:7],v[176:179],v[24:27]\n"
LD "v196,s[48:51],s80 offen lds\n"
MF "v[32:35],a[8:11],v[176:179],v[252:255]\n"
"s_cmp_eq_u32 s60,8\n"
"s_cselect_b32 s60,0,s60\n"
TR "a[244:245],v1 offset:32768\n"
MF "v[32:35],a[48:51],v[180:183],v[32:35]\n"
"s_cmp_eq_u32 s60,10\n"
"s_cselect_b32 s60,s61,s60\n"
TR "a[246:247],v1 offset:34816\n"
MF "v[32:35],a[88:91],v[184:187],v[32:35]\n"
"s_sub_u32 s65,s60,s64\n"
"ds_write_addtid_b32 v112 offset:24576\n"
"ds_write_addtid_b32 v113 offset:24832\n"
MF "v[40:43],a[92:95],v[184:187],v[252:255]\n"
"s_lshl_b32 s65,s65,7\n"
"ds_write_addtid_b32 v114 offset:25088\n"
"ds_write_addtid_b32 v115 offset:25344\n"
MF "v[40:43],a[52:55],v[180:183],v[40:43]\n"
LD "v197,s[48:51],s80 offen offset:1024 lds\n"
MF "v[40:43],a[12:15],v[176:179],v[40:43]\n"
"s_mov_b32 s64,s60\n"
"ds_write_addtid_b32 v116 offset:25600\n"
"ds_write_addtid_b32 v117 offset:25856\n"
MF "v[48:51],a[16:19],v[176:179],v[252:255]\n"
"s_nop 0\n"
"ds_write_addtid_b32 v118 offset:26112\n"
"ds_write_addtid_b32 v119 offset:26368\n"
MF "v[48:51],a[56:59],v[180:183],v[48:51]\n"
"s_nop 0\n"
"ds_write_addtid_b32 v120 offset:26624\n"
"ds_write_addtid_b32 v121 offset:26880\n"
MF "v[48:51],a[96:99],v[184:187],v[48:51]\n"
"s_waitcnt lgkmcnt(10)\n"
LD "v198,s[48:51],s80 offen offset:2048 lds\n"
MF "v[52:55],a[96:99],a[244:247],v[252:255]\n"
"s_nop 0\n"
TR "v[176:177],v2 offset:24576\n"
"ds_write_addtid_b32 v122 offset:27136\n"
MF "v[52:55],a[56:59],a[240:243],v[52:55]\n"
"s_nop 0\n"
TR "v[178:179],v2 offset:26624\n"
"ds_write_addtid_b32 v123 offset:27392\n"
MF "v[52:55],a[16:19],v[188:191],v[52:55]\n"
"s_nop 0\n"
"ds_write_addtid_b32 v124 offset:27648\n"
"ds_write_addtid_b32 v125 offset:27904\n"
MF "v[44:47],a[12:15],v[188:191],v[252:255]\n"
"s_nop 0\n"
TR "v[180:181],v2 offset:28672\n"
"ds_write_addtid_b32 v126 offset:28160\n"
MF "v[44:47],a[52:55],a[240:243],v[44:47]\n"
"s_nop 0\n"
TR "v[182:183],v2 offset:30720\n"
"ds_write_addtid_b32 v127 offset:28416\n"
MF "v[44:47],a[92:95],a[244:247],v[44:47]\n"
LD "v199,s[48:51],s80 offen offset:3072 lds\n"
MF "v[36:39],a[88:91],a[244:247],v[252:255]\n"
"s_nop 0\n"
TR "v[184:185],v2 offset:32768\n"
"ds_write_addtid_b32 v128 offset:28672\n"
MF "v[36:39],a[48:51],a[240:243],v[36:39]\n"
"s_add_u32 m0,s79,106496\n"
TR "v[186:187],v2 offset:34816\n"
"ds_write_addtid_b32 v129 offset:24832\n"
MF "v[36:39],a[8:11],v[188:191],v[36:39]\n"
LD "v200,s[48:51],s80 offen lds\n"
MF "v[28:31],a[4:7],v[188:191],v[252:255]\n"
"s_nop 0\n"
"ds_write_addtid_b32 v130 offset:25088\n"
"ds_write_addtid_b32 v131 offset:25344\n"
MF "v[28:31],a[44:47],a[240:243],v[28:31]\n"
"s_nop 0\n"
"ds_write_addtid_b32 v132 offset:25600\n"
"ds_write_addtid_b32 v133 offset:25856\n"
MF "v[28:31],a[84:87],a[244:247],v[28:31]\n"
"s_nop 0\n"
"ds_write_addtid_b32 v134 offset:26112\n"
"ds_write_addtid_b32 v135 offset:26368\n"
MF "v[20:23],a[80:83],a[244:247],v[252:255]\n"
MF "v[20:23],a[40:43],a[240:243],v[20:23]\n"
LD "v201,s[48:51],s80 offen offset:1024 lds\n"
MF "v[20:23],a[0:3],v[188:191],v[20:23]\n"
"s_waitcnt lgkmcnt(7)\n"
MF "v[56:59],a[0:3],v[176:179],v[252:255]\n"
TR "v[188:189],v3 offset:24576\n"
MF "v[56:59],a[40:43],v[180:183],v[56:59]\n"
TR "v[190:191],v3 offset:26624\n"
MF "v[56:59],a[80:83],v[184:187],v[56:59]\n"
LD "v202,s[48:51],s80 offen offset:2048 lds\n"
MF "v[64:67],a[84:87],v[184:187],v[252:255]\n"
TR "a[240:241],v3 offset:28672\n"
MF "v[64:67],a[44:47],v[180:183],v[64:67]\n"
TR "a[242:243],v3 offset:30720\n"
MF "v[64:67],a[4:7],v[176:179],v[64:67]\n"
LD "v203,s[48:51],s80 offen offset:3072 lds\n"
MF "v[72:75],a[8:11],v[176:179],v[252:255]\n"
TR "a[244:245],v3 offset:32768\n"
MF "v[72:75],a[48:51],v[180:183],v[72:75]\n"
TR "a[246:247],v3 offset:34816\n"
MF "v[72:75],a[88:91],v[184:187],v[72:75]\n"
MF "v[80:83],a[92:95],v[184:187],v[252:255]\n"
MF "v[80:83],a[52:55],v[180:183],v[80:83]\n"
MF "v[80:83],a[12:15],v[176:179],v[80:83]\n"
MF "v[88:91],a[16:19],v[176:179],v[252:255]\n"
MF "v[88:91],a[56:59],v[180:183],v[88:91]\n"
"s_waitcnt vmcnt(31) lgkmcnt(0)\n"
MF "v[88:91],a[96:99],v[184:187],v[88:91]\n"
"s_barrier\n"
MF "v[92:95],a[96:99],a[244:247],v[252:255]\n"
"s_add_u32 s89,s89,s65\n"
TR "v[176:177],v0\n"
MF "v[92:95],a[56:59],a[240:243],v[92:95]\n"
"s_add_u32 m0,s79,24576\n"
TR "v[178:179],v0 offset:2048\n"
MF "v[92:95],a[16:19],v[188:191],v[92:95]\n"
LD "v192,s[48:51],s89 offen lds\n"
MF "v[84:87],a[12:15],v[188:191],v[252:255]\n"
TR "v[180:181],v0 offset:4096\n"
MF "v[84:87],a[52:55],a[240:243],v[84:87]\n"
".Lpg26h:\n"
"s_cbranch_execz .Lpg29h\n"
TR "v[182:183],v0 offset:6144\n"
MF "v[84:87],a[92:95],a[244:247],v[84:87]\n"
LD "v193,s[48:51],s89 offen offset:1024 lds\n"
MF "v[76:79],a[88:91],a[244:247],v[252:255]\n"
TR "v[184:185],v0 offset:8192\n"
MF "v[76:79],a[48:51],a[240:243],v[76:79]\n"
"s_nop 0\n"
TR "v[186:187],v0 offset:10240\n"
"ds_write_addtid_b32 v136 offset:49152\n"
MF "v[76:79],a[8:11],v[188:191],v[76:79]\n"
"s_nop 0\n"
"ds_write_addtid_b32 v137 offset:49408\n"
"ds_write_addtid_b32 v138 offset:49664\n"
MF "v[68:71],a[4:7],v[188:191],v[252:255]\n"
LD "v194,s[48:51],s89 offen offset:2048 lds\n"
MF "v[68:71],a[44:47],a[240:243],v[68:71]\n"
"buffer_load_dword v10,v12,s[40:43],s86 offen sc1\n"
MF "v[68:71],a[84:87],a[244:247],v[68:71]\n"
"s_nop 0\n"
"ds_write_addtid_b32 v139 offset:49920\n"
"ds_write_addtid_b32 v140 offset:50176\n"
MF "v[60:63],a[80:83],a[244:247],v[252:255]\n"
"s_nop 0\n"
"ds_write_addtid_b32 v141 offset:50432\n"
"ds_write_addtid_b32 v142 offset:50688\n"
MF "v[60:63],a[40:43],a[240:243],v[60:63]\n"
"s_nop 0\n"
"ds_write_addtid_b32 v143 offset:50944\n"
"ds_write_addtid_b32 v144 offset:51200\n"
MF "v[60:63],a[0:3],v[188:191],v[60:63]\n"
"s_waitcnt lgkmcnt(9)\n"
LD "v195,s[48:51],s89 offen offset:3072 lds\n"
MF "v[16:19],a[120:123],v[176:179],v[16:19]\n"
"s_nop 0\n"
TR "v[188:189],v1\n"
"ds_write_addtid_b32 v145 offset:51456\n"
MF "v[16:19],a[160:163],v[180:183],v[16:19]\n"
"s_nop 0\n"
TR "v[190:191],v1 offset:2048\n"
"ds_write_addtid_b32 v146 offset:51712\n"
MF "v[16:19],a[200:203],v[184:187],v[16:19]\n"
"s_nop 0\n"
"ds_write_addtid_b32 v147 offset:51968\n"
"ds_write_addtid_b32 v148 offset:52224\n"
MF "v[24:27],a[204:207],v[184:187],v[24:27]\n"
"s_nop 0\n"
TR "a[240:241],v1 offset:4096\n"
"ds_write_addtid_b32 v149 offset:52480\n"
MF "v[24:27],a[164:167],v[180:183],v[24:27]\n"
"s_add_u32 m0,s79,28672\n"
TR "a[242:243],v1 offset:6144\n"
"ds_write_addtid_b32 v150 offset:48640\n"
MF "v[24:27],a[124:127],v[176:179],v[24:27]\n"
LD "v196,s[48:51],s89 offen lds\n"
MF "v[32:35],a[128:131],v[176:179],v[32:35]\n"
"s_nop 0\n"
TR "a[244:245],v1 offset:8192\n"
"ds_write_addtid_b32 v151 offset:48896\n"
MF "v[32:35],a[168:171],v[180:183],v[32:35]\n"
"s_nop 0\n"
TR "a[246:247],v1 offset:10240\n"
"ds_write_addtid_b32 v152 offset:49152\n"
MF "v[32:35],a[208:211],v[184:187],v[32:35]\n"
LD "v197,s[48:51],s89 offen offset:1024 lds\n"
MF "v[40:43],a[212:215],v[184:187],v[40:43]\n"
"s_nop 0\n"
"ds_write_addtid_b32 v153 offset:49408\n"
"ds_write_addtid_b32 v154 offset:49664\n"
MF "v[40:43],a[172:175],v[180:183],v[40:43]\n"
"s_nop 0\n"
"ds_write_addtid_b32 v155 offset:49920\n"
"ds_write_addtid_b32 v156 offset:50176\n"
MF "v[40:43],a[132:135],v[176:179],v[40:43]\n"
"s_nop 0\n"
"ds_write_addtid_b32 v157 offset:50432\n"
"ds_write_addtid_b32 v158 offset:50688\n"
MF "v[48:51],a[136:139],v[176:179],v[48:51]\n"
"s_nop 0\n"
"ds_write_addtid_b32 v159 offset:50944\n"
"ds_write_addtid_b32 v160 offset:51200\n"
MF "v[48:51],a[176:179],v[180:183],v[48:51]\n"
LD "v198,s[48:51],s89 offen offset:2048 lds\n"
MF "v[48:51],a[216:219],v[184:187],v[48:51]\n"
"s_waitcnt lgkmcnt(9)\n"
"ds_write_addtid_b32 v161 offset:51456\n"
"ds_write_addtid_b32 v162 offset:51712\n"
MF "v[52:55],a[216:219],a[244:247],v[52:55]\n"
"s_nop 0\n"
TR "v[176:177],v2\n"
"ds_write_addtid_b32 v163 offset:51968\n"
MF "v[52:55],a[176:179],a[240:243],v[52:55]\n"
"s_nop 0\n"
TR "v[178:179],v2 offset:2048\n"
"ds_write_addtid_b32 v164 offset:52224\n"
MF "v[52:55],a[136:139],v[188:191],v[52:55]\n"
LD "v199,s[48:51],s89 offen offset:3072 lds\n"
MF "v[44:47],a[132:135],v[188:191],v[44:47]\n"
"s_nop 0\n"
TR "v[180:181],v2 offset:4096\n"
"ds_write_addtid_b32 v165 offset:52480\n"
MF "v[44:47],a[172:175],a[240:243],v[44:47]\n"
"s_nop 0\n"
TR "v[182:183],v2 offset:6144\n"
"ds_write_addtid_b32 v166 offset:52736\n"
MF "v[44:47],a[212:215],a[244:247],v[44:47]\n"
"s_nop 0\n"
"ds_write_addtid_b32 v167 offset:52992\n"
"ds_write_addtid_b32 v168 offset:53248\n"
MF "v[36:39],a[208:211],a[244:247],v[36:39]\n"
"s_nop 0\n"
TR "v[184:185],v2 offset:8192\n"
"ds_write_addtid_b32 v169 offset:53504\n"
MF "v[36:39],a[168:171],a[240:243],v[36:39]\n"
"s_add_u32 m0,s79,32768\n"
TR "v[186:187],v2 offset:10240\n"
"ds_write_addtid_b32 v170 offset:49664\n"
MF "v[36:39],a[128:131],v[188:191],v[36:39]\n"
LD "v200,s[48:51],s89 offen lds\n"
MF "v[28:31],a[124:127],v[188:191],v[28:31]\n"
"s_nop 0\n"
"ds_write_addtid_b32 v171 offset:49920\n"
"ds_write_addtid_b32 v172 offset:50176\n"
MF "v[28:31],a[164:167],a[240:243],v[28:31]\n"
"s_nop 0\n"
"ds_write_addtid_b32 v173 offset:50432\n"
"ds_write_addtid_b32 v174 offset:50688\n"
MF "v[28:31],a[204:207],a[244:247],v[28:31]\n"
"ds_write_addtid_b32 v175 offset:50944\n"
MF "v[20:23],a[200:203],a[244:247],v[20:23]\n"
LD "v201,s[48:51],s89 offen offset:1024 lds\n"
MF "v[20:23],a[160:163],a[240:243],v[20:23]\n"
MF "v[20:23],a[120:123],v[188:191],v[20:23]\n"
"s_waitcnt lgkmcnt(6)\n"
MF "v[56:59],a[120:123],v[176:179],v[56:59]\n"
TR "v[188:189],v3\n"
MF "v[56:59],a[160:163],v[180:183],v[56:59]\n"
TR "v[190:191],v3 offset:2048\n"
MF "v[56:59],a[200:203],v[184:187],v[56:59]\n"
LD "v202,s[48:51],s89 offen offset:2048 lds\n"
MF "v[64:67],a[204:207],v[184:187],v[64:67]\n"
TR "a[240:241],v3 offset:4096\n"
MF "v[64:67],a[164:167],v[180:183],v[64:67]\n"
TR "a[242:243],v3 offset:6144\n"
MF "v[64:67],a[124:127],v[176:179],v[64:67]\n"
LD "v203,s[48:51],s89 offen offset:3072 lds\n"
MF "v[72:75],a[128:131],v[176:179],v[72:75]\n"
TR "a[244:245],v3 offset:8192\n"
MF "v[72:75],a[168:171],v[180:183],v[72:75]\n"
TR "a[246:247],v3 offset:10240\n"
MF "v[72:75],a[208:211],v[184:187],v[72:75]\n"
MF "v[80:83],a[212:215],v[184:187],v[80:83]\n"
MF "v[80:83],a[172:175],v[180:183],v[80:83]\n"
MF "v[80:83],a[132:135],v[176:179],v[80:83]\n"
MF "v[88:91],a[136:139],v[176:179],v[88:91]\n"
MF "v[88:91],a[176:179],v[180:183],v[88:91]\n"
"s_waitcnt vmcnt(27) lgkmcnt(0)\n"
MF "v[88:91],a[216:219],v[184:187],v[88:91]\n"
"s_barrier\n"
MF "v[92:95],a[216:219],a[244:247],v[92:95]\n"
"s_add_u32 s80,s89,122880\n"
TR "v[176:177],v0 offset:49152\n"
MF "v[92:95],a[176:179],a[240:243],v[92:95]\n"
"s_add_u32 m0,s79,0\n"
"s_mov_b32 s82,s70\n"
TR "v[178:179],v0 offset:51200\n"
MF "v[92:95],a[136:139],v[188:191],v[92:95]\n"
LD "v192,s[48:51],s80 offen lds\n"
MF "v[84:87],a[132:135],v[188:191],v[84:87]\n"
TR "v[180:181],v0 offset:53248\n"
MF "v[84:87],a[172:175],a[240:243],v[84:87]\n"
TR "v[182:183],v0 offset:55296\n"
MF "v[84:87],a[212:215],a[244:247],v[84:87]\n"
LD "v193,s[48:51],s80 offen offset:1024 lds\n"
MF "v[76:79],a[208:211],a[244:247],v[76:79]\n"
TR "v[184:185],v0 offset:57344\n"
MF "v[76:79],a[168:171],a[240:243],v[76:79]\n"
TR "v[186:187],v0 offset:59392\n"
MF "v[76:79],a[128:131],v[188:191],v[76:79]\n"
MF "v[68:71],a[124:127],v[188:191],v[68:71]\n"
MF "v[68:71],a[164:167],a[240:243],v[68:71]\n"
LD "v194,s[48:51],s80 offen offset:2048 lds\n"
MF "v[68:71],a[204:207],a[244:247],v[68:71]\n"
MF "v[60:63],a[200:203],a[244:247],v[60:63]\n"
MF "v[60:63],a[160:163],a[240:243],v[60:63]\n"
MF "v[60:63],a[120:123],v[188:191],v[60:63]\n"
"s_waitcnt lgkmcnt(0)\n"
LD "v195,s[48:51],s80 offen offset:3072 lds\n"
MF "v[16:19],a[20:23],v[176:179],v[16:19]\n"
TR "v[188:189],v1 offset:49152\n"
MF "v[16:19],a[60:63],v[180:183],v[16:19]\n"
TR "v[190:191],v1 offset:51200\n"
MF "v[16:19],a[100:103],v[184:187],v[16:19]\n"
MF "v[24:27],a[104:107],v[184:187],v[24:27]\n"
TR "a[240:241],v1 offset:53248\n"
MF "v[24:27],a[64:67],v[180:183],v[24:27]\n"
"s_add_u32 m0,s79,4096\n"
TR "a[242:243],v1 offset:55296\n"
MF "v[24:27],a[24:27],v[176:179],v[24:27]\n"
LD "v196,s[48:51],s80 offen lds\n"
MF "v[32:35],a[28:31],v[176:179],v[32:35]\n"
TR "a[244:245],v1 offset:57344\n"
MF "v[32:35],a[68:71],v[180:183],v[32:35]\n"
TR "a[246:247],v1 offset:59392\n"
MF "v[32:35],a[108:111],v[184:187],v[32:35]\n"
LD "v197,s[48:51],s80 offen offset:1024 lds\n"
MF "v[40:43],a[112:115],v[184:187],v[40:43]\n"
MF "v[40:43],a[72:75],v[180:183],v[40:43]\n"
MF "v[40:43],a[32:35],v[176:179],v[40:43]\n"
MF "v[48:51],a[36:39],v[176:179],v[48:51]\n"
MF "v[48:51],a[76:79],v[180:183],v[48:51]\n"
LD "v198,s[48:51],s80 offen offset:2048 lds\n"
MF "v[48:51],a[116:119],v[184:187],v[48:51]\n"
"s_waitcnt lgkmcnt(0)\n"
MF "v[52:55],a[116:119],a[244:247],v[52:55]\n"
TR "v[176:177],v2 offset:49152\n"
MF "v[52:55],a[76:79],a[240:243],v[52:55]\n"
TR "v[178:179],v2 offset:51200\n"
MF "v[52:55],a[36:39],v[188:191],v[52:55]\n"
LD "v199,s[48:51],s80 offen offset:3072 lds\n"
MF "v[44:47],a[32:35],v[188:191],v[44:47]\n"
TR "v[180:181],v2 offset:53248\n"
MF "v[44:47],a[72:75],a[240:243],v[44:47]\n"
TR "v[182:183],v2 offset:55296\n"
MF "v[44:47],a[112:115],a[244:247],v[44:47]\n"
MF "v[36:39],a[108:111],a[244:247],v[36:39]\n"
TR "v[184:185],v2 offset:57344\n"
MF "v[36:39],a[68:71],a[240:243],v[36:39]\n"
"s_add_u32 m0,s79,8192\n"
TR "v[186:187],v2 offset:59392\n"
MF "v[36:39],a[28:31],v[188:191],v[36:39]\n"
LD "v200,s[48:51],s80 offen lds\n"
MF "v[28:31],a[24:27],v[188:191],v[28:31]\n"
MF "v[28:31],a[64:67],a[240:243],v[28:31]\n"
MF "v[28:31],a[104:107],a[244:247],v[28:31]\n"
MF "v[20:23],a[100:103],a[244:247],v[20:23]\n"
LD "v201,s[48:51],s80 offen offset:1024 lds\n"
MF "v[20:23],a[60:63],a[240:243],v[20:23]\n"
"s_waitcnt vmcnt(19)\n"
MF "v[20:23],a[20:23],v[188:191],v[20:23]\n"
"v_mov_b32 v236,v228\n"
"v_mov_b32 v237,v229\n"
MF "v[16:19],a[252:255],v[208:211],v[16:19]\n"
MF "v[24:27],a[252:255],v[212:215],v[24:27]\n"
MF "v[32:35],a[252:255],v[216:219],v[32:35]\n"
MF "v[40:43],a[252:255],v[220:223],v[40:43]\n"
MF "v[48:51],a[252:255],v[224:227],v[48:51]\n"
MF "v[20:23],a[248:251],v[208:211],v[20:23]\n"
MF "v[28:31],a[248:251],v[212:215],v[28:31]\n"
MF "v[36:39],a[248:251],v[216:219],v[36:39]\n"
MF "v[44:47],a[248:251],v[220:223],v[44:47]\n"
MF "v[52:55],a[248:251],v[224:227],v[52:55]\n"
MF "v[248:251],v[230:233],a[250:253],v[248:251]\n"
MF "v[244:247],v[234:237],a[250:253],v[244:247]\n"
"s_waitcnt lgkmcnt(0)\n"
MF "v[56:59],a[20:23],v[176:179],v[56:59]\n"
TR "v[188:189],v3 offset:49152\n"
"v_sub_u32 v10,v10,v11\n"
MF "v[56:59],a[60:63],v[180:183],v[56:59]\n"
TR "v[190:191],v3 offset:51200\n"
MF "v[56:59],a[100:103],v[184:187],v[56:59]\n"
LD "v202,s[48:51],s80 offen offset:2048 lds\n"
MF "v[64:67],a[104:107],v[184:187],v[64:67]\n"
TR "a[240:241],v3 offset:53248\n"
MF "v[64:67],a[64:67],v[180:183],v[64:67]\n"
TR "a[242:243],v3 offset:55296\n"
MF "v[64:67],a[24:27],v[176:179],v[64:67]\n"
LD "v203,s[48:51],s80 offen offset:3072 lds\n"
MF "v[72:75],a[28:31],v[176:179],v[72:75]\n"
TR "a[244:245],v3 offset:57344\n"
MF "v[72:75],a[68:71],v[180:183],v[72:75]\n"
TR "a[246:247],v3 offset:59392\n"
MF "v[72:75],a[108:111],v[184:187],v[72:75]\n"
".Lnf2:\n"
"v_readlane_b32 s58,v10,s67\n"
"s_cmp_lt_u32 s58,s85\n"
"s_cbranch_scc1 .Lsp2\n"
".Lnb2:\n"
MF "v[80:83],a[112:115],v[184:187],v[80:83]\n"
"s_add_u32 s70,s70,20480\n"
MF "v[80:83],a[72:75],v[180:183],v[80:83]\n"
MF "v[80:83],a[32:35],v[176:179],v[80:83]\n"
MF "v[88:91],a[36:39],v[176:179],v[88:91]\n"
"s_add_u32 s2,s2,204800\n"
MF "v[88:91],a[76:79],v[180:183],v[88:91]\n"
"s_waitcnt vmcnt(25) lgkmcnt(0)\n"
LD "v[228:231],v9,s[40:43],s2 offen sc1\n"
MF "v[88:91],a[116:119],v[184:187],v[88:91]\n"
"s_barrier\n"
MF "v[92:95],a[116:119],a[244:247],v[92:95]\n"
"s_add_u32 s88,s88,s65\n"
TR "v[176:177],v4\n"
MF "v[92:95],a[76:79],a[240:243],v[92:95]\n"
"s_add_u32 m0,s79,49152\n"
TR "v[178:179],v4 offset:2048\n"
MF "v[92:95],a[36:39],v[188:191],v[92:95]\n"
LD "v192,s[48:51],s88 offen lds\n"
MF "v[84:87],a[32:35],v[188:191],v[84:87]\n"
TR "v[180:181],v4 offset:4096\n"
MF "v[84:87],a[72:75],a[240:243],v[84:87]\n"
TR "v[182:183],v4 offset:6144\n"
MF "v[84:87],a[112:115],a[244:247],v[84:87]\n"
LD "v193,s[48:51],s88 offen offset:1024 lds\n"
MF "v[76:79],a[108:111],a[244:247],v[76:79]\n"
TR "v[184:185],v4 offset:8192\n"
MF "v[76:79],a[68:71],a[240:243],v[76:79]\n"
"s_add_u32 s2,s2,204800\n"
TR "v[186:187],v4 offset:10240\n"
MF "v[76:79],a[28:31],v[188:191],v[76:79]\n"
LD "v[232:235],v9,s[40:43],s2 offen sc1\n"
MF "v[68:71],a[24:27],v[188:191],v[68:71]\n"
MF "v[68:71],a[64:67],a[240:243],v[68:71]\n"
LD "v194,s[48:51],s88 offen offset:2048 lds\n"
MF "v[68:71],a[104:107],a[244:247],v[68:71]\n"
MF "v[60:63],a[100:103],a[244:247],v[60:63]\n"
MF "v[60:63],a[60:63],a[240:243],v[60:63]\n"
MF "v[60:63],a[20:23],v[188:191],v[60:63]\n"
"s_waitcnt lgkmcnt(0)\n"
LD "v195,s[48:51],s88 offen offset:3072 lds\n"
MF "v[16:19],a[140:143],v[176:179],v[16:19]\n"
TR "v[188:189],v5\n"
MF "v[16:19],a[180:183],v[180:183],v[16:19]\n"
TR "v[190:191],v5 offset:2048\n"
MF "v[16:19],a[220:223],v[184:187],v[16:19]\n"
LD "v[208:211],v9,s[40:43],s82 offen sc1\n"
MF "v[24:27],a[224:227],v[184:187],v[24:27]\n"
TR "a[240:241],v5 offset:4096\n"
MF "v[24:27],a[184:187],v[180:183],v[24:27]\n"
"s_add_u32 m0,s79,53248\n"
TR "a[242:243],v5 offset:6144\n"
MF "v[24:27],a[144:147],v[176:179],v[24:27]\n"
LD "v196,s[48:51],s88 offen lds\n"
MF "v[32:35],a[148:151],v[176:179],v[32:35]\n"
TR "a[244:245],v5 offset:8192\n"
MF "v[32:35],a[188:191],v[180:183],v[32:35]\n"
TR "a[246:247],v5 offset:10240\n"
MF "v[32:35],a[228:231],v[184:187],v[32:35]\n"
LD "v197,s[48:51],s88 offen offset:1024 lds\n"
MF "v[40:43],a[232:235],v[184:187],v[40:43]\n"
MF "v[40:43],a[192:195],v[180:183],v[40:43]\n"
MF "v[40:43],a[152:155],v[176:179],v[40:43]\n"
MF "v[48:51],a[156:159],v[176:179],v[48:51]\n"
LD "v[212:215],v9,s[40:43],s82 offen offset:1024 sc1\n"
MF "v[48:51],a[196:199],v[180:183],v[48:51]\n"
LD "v198,s[48:51],s88 offen offset:2048 lds\n"
MF "v[48:51],a[236:239],v[184:187],v[48:51]\n"
"s_waitcnt lgkmcnt(0)\n"
MF "v[52:55],a[236:239],a[244:247],v[52:55]\n"
TR "v[176:177],v6\n"
MF "v[52:55],a[196:199],a[240:243],v[52:55]\n"
TR "v[178:179],v6 offset:2048\n"
MF "v[52:55],a[156:159],v[188:191],v[52:55]\n"
LD "v199,s[48:51],s88 offen offset:3072 lds\n"
MF "v[44:47],a[152:155],v[188:191],v[44:47]\n"
TR "v[180:181],v6 offset:4096\n"
MF "v[44:47],a[192:195],a[240:243],v[44:47]\n"
TR "v[182:183],v6 offset:6144\n"
MF "v[44:47],a[232:235],a[244:247],v[44:47]\n"
MF "v[36:39],a[228:231],a[244:247],v[36:39]\n"
TR "v[184:185],v6 offset:8192\n"
MF "v[36:39],a[188:191],a[240:243],v[36:39]\n"
"s_add_u32 m0,s79,57344\n"
TR "v[186:187],v6 offset:10240\n"
MF "v[36:39],a[148:151],v[188:191],v[36:39]\n"
LD "v200,s[48:51],s88 offen lds\n"
MF "v[28:31],a[144:147],v[188:191],v[28:31]\n"
LD "v[216:219],v9,s[40:43],s82 offen offset:2048 sc1\n"
MF "v[28:31],a[184:187],a[240:243],v[28:31]\n"
MF "v[28:31],a[224:227],a[244:247],v[28:31]\n"
MF "v[20:23],a[220:223],a[244:247],v[20:23]\n"
LD "v201,s[48:51],s88 offen offset:1024 lds\n"
MF "v[20:23],a[180:183],a[240:243],v[20:23]\n"
MF "v[20:23],a[140:143],v[188:191],v[20:23]\n"
"s_waitcnt lgkmcnt(0)\n"
MF "v[56:59],a[140:143],v[176:179],v[56:59]\n"
TR "v[188:189],v7\n"
MF "v[56:59],a[180:183],v[180:183],v[56:59]\n"
TR "v[190:191],v7 offset:2048\n"
MF "v[56:59],a[220:223],v[184:187],v[56:59]\n"
LD "v202,s[48:51],s88 offen offset:2048 lds\n"
MF "v[64:67],a[224:227],v[184:187],v[64:67]\n"
TR "a[240:241],v7 offset:4096\n"
MF "v[64:67],a[184:187],v[180:183],v[64:67]\n"
TR "a[242:243],v7 offset:6144\n"
MF "v[64:67],a[144:147],v[176:179],v[64:67]\n"
LD "v203,s[48:51],s88 offen offset:3072 lds\n"
MF "v[72:75],a[148:151],v[176:179],v[72:75]\n"
TR "a[244:245],v7 offset:8192\n"
MF "v[72:75],a[188:191],v[180:183],v[72:75]\n"
TR "a[246:247],v7 offset:10240\n"
MF "v[72:75],a[228:231],v[184:187],v[72:75]\n"
LD "v[220:223],v9,s[40:43],s82 offen offset:3072 sc1\n"
MF "v[80:83],a[232:235],v[184:187],v[80:83]\n"
MF "v[80:83],a[192:195],v[180:183],v[80:83]\n"
MF "v[80:83],a[152:155],v[176:179],v[80:83]\n"
MF "v[88:91],a[156:159],v[176:179],v[88:91]\n"
"s_add_u32 s82,s82,4096\n"
MF "v[88:91],a[196:199],v[180:183],v[88:91]\n"
"s_waitcnt vmcnt(30) lgkmcnt(0)\n"
LD "v[224:227],v9,s[40:43],s82 offen sc1\n"
MF "v[88:91],a[236:239],v[184:187],v[88:91]\n"
"s_barrier\n"
MF "v[92:95],a[236:239],a[244:247],v[92:95]\n"
"s_add_u32 s80,s88,122880\n"
TR "v[176:177],v0 offset:24576\n"
MF "v[92:95],a[196:199],a[240:243],v[92:95]\n"
"s_add_u32 m0,s79,98304\n"
"s_add_u32 s85,s85,1\n"
TR "v[178:179],v0 offset:26624\n"
MF "v[92:95],a[156:159],v[188:191],v[92:95]\n"
LD "v192,s[48:51],s80 offen lds\n"
MF "v[84:87],a[152:155],v[188:191],v[84:87]\n"
"s_nop 0\n"
TR "v[180:181],v0 offset:28672\n"
"ds_write_addtid_b32 v16 offset:24576\n"
MF "v[84:87],a[192:195],a[240:243],v[84:87]\n"
"s_nop 0\n"
TR "v[182:183],v0 offset:30720\n"
"ds_write_addtid_b32 v17 offset:24832\n"
MF "v[84:87],a[232:235],a[244:247],v[84:87]\n"
LD "v193,s[48:51],s80 offen offset:1024 lds\n"
MF "v[76:79],a[228:231],a[244:247],v[76:79]\n"
"s_nop 0\n"
TR "v[184:185],v0 offset:32768\n"
"ds_write_addtid_b32 v18 offset:25088\n"
MF "v[76:79],a[188:191],a[240:243],v[76:79]\n"
"s_nop 0\n"
TR "v[186:187],v0 offset:34816\n"
"ds_write_addtid_b32 v19 offset:25344\n"
MF "v[76:79],a[148:151],v[188:191],v[76:79]\n"
"s_nop 0\n"
"ds_write_addtid_b32 v20 offset:25600\n"
"ds_write_addtid_b32 v21 offset:25856\n"
MF "v[68:71],a[144:147],v[188:191],v[68:71]\n"
LD "v194,s[48:51],s80 offen offset:2048 lds\n"
MF "v[68:71],a[184:187],a[240:243],v[68:71]\n"
"s_nop 0\n"
"ds_write_addtid_b32 v22 offset:26112\n"
"ds_write_addtid_b32 v23 offset:26368\n"
MF "v[68:71],a[224:227],a[244:247],v[68:71]\n"
"s_nop 0\n"
"ds_write_addtid_b32 v24 offset:26624\n"
"ds_write_addtid_b32 v25 offset:26880\n"
MF "v[60:63],a[220:223],a[244:247],v[60:63]\n"
"s_nop 0\n"
"ds_write_addtid_b32 v26 offset:27136\n"
"ds_write_addtid_b32 v27 offset:27392\n"
MF "v[60:63],a[180:183],a[240:243],v[60:63]\n"
MF "v[60:63],a[140:143],v[188:191],v[60:63]\n"
"s_waitcnt lgkmcnt(9)\n"
"s_cmp_lg_u32 s85,9\n"
"s_cbranch_scc1 .Lpl09\n"
".Lplast0:\n"
MF "v[96:99],v[176:179],a[0:3],v[252:255]\n"
"s_nop 0\n"
TR "v[188:189],v1 offset:24576\n"
TR "v[190:191],v1 offset:26624\n"
MF "v[96:99],v[180:183],a[40:43],v[96:99]\n"
"s_nop 0\n"
TR "a[240:241],v1 offset:28672\n"
TR "a[242:243],v1 offset:30720\n"
MF "v[96:99],v[184:187],a[80:83],v[96:99]\n"
"s_add_u32 s90,s91,163840\n"
TR "a[244:245],v1 offset:32768\n"
TR "a[246:247],v1 offset:34816\n"
MF "v[104:107],v[184:187],a[84:87],v[252:255]\n"
LD "v195,s[48:51],s80 offen offset:3072 lds\n"
MF "v[104:107],v[180:183],a[44:47],v[104:107]\n"
"s_nop 0\n"
"ds_write_addtid_b32 v28 offset:27648\n"
"ds_write_addtid_b32 v29 offset:27904\n"
MF "v[104:107],v[176:179],a[4:7],v[104:107]\n"
"s_nop 0\n"
"ds_write_addtid_b32 v30 offset:28160\n"
"ds_write_addtid_b32 v31 offset:28416\n"
MF "v[112:115],v[176:179],a[8:11],v[252:255]\n"
"s_nop 0\n"
"ds_write_addtid_b32 v32 offset:28672\n"
"ds_write_addtid_b32 v33 offset:28928\n"
MF "v[112:115],v[180:183],a[48:51],v[112:115]\n"
"s_add_u32 m0,s79,102400\n"
"s_nop 0\n"
"ds_write_addtid_b32 v34 offset:25088\n"
"ds_write_addtid_b32 v35 offset:25344\n"
MF "v[112:115],v[184:187],a[88:91],v[112:115]\n"
LD "v196,s[48:51],s80 offen lds\n"
MF "v[120:123],v[184:187],a[92:95],v[252:255]\n"
"s_nop 0\n"
"ds_write_addtid_b32 v36 offset:25600\n"
"ds_write_addtid_b32 v37 offset:25856\n"
MF "v[120:123],v[180:183],a[52:55],v[120:123]\n"
"ds_write_addtid_b32 v38 offset:26112\n"
MF "v[120:123],v[176:179],a[12:15],v[120:123]\n"
"ds_write_addtid_b32 v39 offset:26368\n"
MF "v[128:131],v[176:179],a[16:19],v[252:255]\n"
"ds_write_addtid_b32 v40 offset:26624\n"
MF "v[128:131],v[180:183],a[56:59],v[128:131]\n"
"ds_write_addtid_b32 v41 offset:26880\n"
MF "v[128:131],v[184:187],a[96:99],v[128:131]\n"
"s_waitcnt lgkmcnt(14)\n"
LD "v197,s[48:51],s80 offen offset:1024 lds\n"
MF "v[132:135],a[244:247],a[96:99],v[252:255]\n"
"s_nop 0\n"
TR "v[176:177],v2 offset:24576\n"
TR "v[178:179],v2 offset:26624\n"
MF "v[132:135],a[240:243],a[56:59],v[132:135]\n"
"s_nop 0\n"
TR "v[180:181],v2 offset:28672\n"
TR "v[182:183],v2 offset:30720\n"
MF "v[132:135],v[188:191],a[16:19],v[132:135]\n"
"s_nop 0\n"
TR "v[184:185],v2 offset:32768\n"
TR "v[186:187],v2 offset:34816\n"
MF "v[124:127],v[188:191],a[12:15],v[252:255]\n"
"s_nop 0\n"
"ds_write_addtid_b32 v42 offset:27136\n"
"ds_write_addtid_b32 v43 offset:27392\n"
MF "v[124:127],a[240:243],a[52:55],v[124:127]\n"
"s_nop 0\n"
"ds_write_addtid_b32 v44 offset:27648\n"
"ds_write_addtid_b32 v45 offset:27904\n"
MF "v[124:127],a[244:247],a[92:95],v[124:127]\n"
"s_nop 0\n"
"ds_write_addtid_b32 v46 offset:28160\n"
"ds_write_addtid_b32 v47 offset:28416\n"
MF "v[116:119],a[244:247],a[88:91],v[252:255]\n"
LD "v198,s[48:51],s80 offen offset:2048 lds\n"
MF "v[116:119],a[240:243],a[48:51],v[116:119]\n"
"s_nop 0\n"
"ds_write_addtid_b32 v48 offset:28672\n"
"ds_write_addtid_b32 v49 offset:28928\n"
MF "v[116:119],v[188:191],a[8:11],v[116:119]\n"
"s_nop 0\n"
"ds_write_addtid_b32 v50 offset:29184\n"
"ds_write_addtid_b32 v51 offset:29440\n"
MF "v[108:111],v[188:191],a[4:7],v[252:255]\n"
"ds_write_addtid_b32 v52 offset:29696\n"
MF "v[108:111],a[240:243],a[44:47],v[108:111]\n"
"ds_write_addtid_b32 v53 offset:29952\n"
MF "v[108:111],a[244:247],a[84:87],v[108:111]\n"
"ds_write_addtid_b32 v54 offset:30208\n"
MF "v[100:103],a[244:247],a[80:83],v[252:255]\n"
"ds_write_addtid_b32 v55 offset:30464\n"
MF "v[100:103],a[240:243],a[40:43],v[100:103]\n"
LD "v199,s[48:51],s80 offen offset:3072 lds\n"
MF "v[100:103],v[188:191],a[0:3],v[100:103]\n"
"s_waitcnt lgkmcnt(14)\n"
MF "v[136:139],v[176:179],a[0:3],v[252:255]\n"
"s_nop 0\n"
TR "v[188:189],v3 offset:24576\n"
TR "v[190:191],v3 offset:26624\n"
MF "v[136:139],v[180:183],a[40:43],v[136:139]\n"
"s_add_u32 m0,s79,106496\n"
TR "a[240:241],v3 offset:28672\n"
TR "a[242:243],v3 offset:30720\n"
MF "v[136:139],v[184:187],a[80:83],v[136:139]\n"
".Lpg27h:\n"
"s_cbranch_execz .Lpg30h\n"
"s_nop 0\n"
TR "a[244:245],v3 offset:32768\n"
TR "a[246:247],v3 offset:34816\n"
"s_waitcnt lgkmcnt(0)\n"
MF "v[144:147],v[184:187],a[84:87],v[252:255]\n"
"s_barrier\n"
MF "v[144:147],v[180:183],a[44:47],v[144:147]\n"
LD "v200,s[48:51],s80 offen lds\n"
MF "v[144:147],v[176:179],a[4:7],v[144:147]\n"
"ds_read2st64_b32 v[16:17],v13 offset0:0 offset1:1\n"
MF "v[152:155],v[176:179],a[8:11],v[252:255]\n"
"ds_read2st64_b32 v[18:19],v13 offset0:2 offset1:3\n"
MF "v[152:155],v[180:183],a[48:51],v[152:155]\n"
"ds_read2st64_b32 v[20:21],v13 offset0:4 offset1:5\n"
MF "v[152:155],v[184:187],a[88:91],v[152:155]\n"
"ds_read2st64_b32 v[22:23],v13 offset0:6 offset1:7\n"
MF "v[160:163],v[184:187],a[92:95],v[252:255]\n"
"ds_read2st64_b32 v[24:25],v13 offset0:8 offset1:9\n"
MF "v[160:163],v[180:183],a[52:55],v[160:163]\n"
"ds_read2st64_b32 v[26:27],v13 offset0:10 offset1:11\n"
MF "v[160:163],v[176:179],a[12:15],v[160:163]\n"
LD "v201,s[48:51],s80 offen offset:1024 lds\n"
MF "v[168:171],v[176:179],a[16:19],v[252:255]\n"
"ds_read2st64_b32 v[28:29],v13 offset0:12 offset1:13\n"
MF "v[168:171],v[180:183],a[56:59],v[168:171]\n"
"ds_read2st64_b32 v[30:31],v13 offset0:14 offset1:15\n"
"s_waitcnt vmcnt(29) lgkmcnt(0)\n"
MF "v[168:171],v[184:187],a[96:99],v[168:171]\n"
"s_barrier\n"
MF "v[172:175],a[244:247],a[96:99],v[252:255]\n"
"s_nop 0\n"
TR "v[176:177],v0\n"
TR "v[178:179],v0 offset:2048\n"
MF "v[172:175],a[240:243],a[56:59],v[172:175]\n"
"s_nop 0\n"
TR "v[180:181],v0 offset:4096\n"
TR "v[182:183],v0 offset:6144\n"
MF "v[172:175],v[188:191],a[16:19],v[172:175]\n"
"s_nop 0\n"
TR "v[184:185],v0 offset:8192\n"
TR "v[186:187],v0 offset:10240\n"
MF "v[164:167],v[188:191],a[12:15],v[252:255]\n"
LD "v202,s[48:51],s80 offen offset:2048 lds\n"
MF "v[164:167],a[240:243],a[52:55],v[164:167]\n"
"ds_read2st64_b32 v[32:33],v13 offset0:16 offset1:17\n"
"v_add_f32 v56,v56,v16\n"
MF "v[164:167],a[244:247],a[92:95],v[164:167]\n"
"ds_read2st64_b32 v[34:35],v13 offset0:18 offset1:19\n"
"v_add_f32 v57,v57,v17\n"
MF "v[156:159],a[244:247],a[88:91],v[252:255]\n"
"ds_read2st64_b32 v[36:37],v13 offset0:20 offset1:21\n"
"v_add_f32 v58,v58,v18\n"
MF "v[156:159],a[240:243],a[48:51],v[156:159]\n"
"ds_read2st64_b32 v[38:39],v13 offset0:22 offset1:23\n"
"v_add_f32 v59,v59,v19\n"
MF "v[156:159],v[188:191],a[8:11],v[156:159]\n"
"ds_read2st64_b32 v[40:41],v13 offset0:24 offset1:25\n"
"v_add_f32 v60,v60,v20\n"
MF "v[148:151],v[188:191],a[4:7],v[252:255]\n"
LD "v203,s[48:51],s80 offen offset:3072 lds\n"
MF "v[148:151],a[240:243],a[44:47],v[148:151]\n"
"ds_read2st64_b32 v[42:43],v13 offset0:26 offset1:27\n"
"v_add_f32 v61,v61,v21\n"
MF "v[148:151],a[244:247],a[84:87],v[148:151]\n"
"ds_read2st64_b32 v[44:45],v13 offset0:28 offset1:29\n"
"v_add_f32 v62,v62,v22\n"
MF "v[140:143],a[244:247],a[80:83],v[252:255]\n"
"ds_read2st64_b32 v[46:47],v13 offset0:30 offset1:31\n"
"v_add_f32 v63,v63,v23\n"
MF "v[140:143],a[240:243],a[40:43],v[140:143]\n"
"ds_read2st64_b32 v[48:49],v13 offset0:32 offset1:33\n"
CV "v56,v56,v57\n"
MF "v[140:143],v[188:191],a[0:3],v[140:143]\n"
"s_waitcnt lgkmcnt(9)\n"
"ds_read2st64_b32 v[50:51],v13 offset0:34 offset1:35\n"
CV "v57,v58,v59\n"
MF "v[96:99],v[176:179],a[120:123],v[96:99]\n"
"s_nop 0\n"
TR "v[188:189],v1\n"
TR "v[190:191],v1 offset:2048\n"
MF "v[96:99],v[180:183],a[160:163],v[96:99]\n"
"s_nop 0\n"
TR "a[240:241],v1 offset:4096\n"
TR "a[242:243],v1 offset:6144\n"
MF "v[96:99],v[184:187],a[200:203],v[96:99]\n"
"s_nop 0\n"
TR "a[244:245],v1 offset:8192\n"
TR "a[246:247],v1 offset:10240\n"
MF "v[104:107],v[184:187],a[204:207],v[104:107]\n"
"ds_read2st64_b32 v[52:53],v13 offset0:36 offset1:37\n"
CV "v58,v60,v61\n"
MF "v[104:107],v[180:183],a[164:167],v[104:107]\n"
"s_add_u32 s87,s90,10240\n"
"ds_read2st64_b32 v[54:55],v13 offset0:38 offset1:39\n"
CV "v59,v62,v63\n"
MF "v[104:107],v[176:179],a[124:127],v[104:107]\n"
"buffer_store_dwordx4 v[56:59],v9,s[40:43],s87 offen sc1\n"
MF "v[112:115],v[176:179],a[128:131],v[112:115]\n"
"v_add_f32 v64,v64,v24\n"
"v_add_f32 v65,v65,v25\n"
MF "v[112:115],v[180:183],a[168:171],v[112:115]\n"
"v_add_f32 v66,v66,v26\n"
"v_add_f32 v67,v67,v27\n"
MF "v[112:115],v[184:187],a[208:211],v[112:115]\n"
"v_add_f32 v68,v68,v28\n"
"v_add_f32 v69,v69,v29\n"
MF "v[120:123],v[184:187],a[212:215],v[120:123]\n"
"v_add_f32 v70,v70,v30\n"
"v_add_f32 v71,v71,v31\n"
MF "v[120:123],v[180:183],a[172:175],v[120:123]\n"
CV "v64,v64,v65\n"
CV "v65,v66,v67\n"
MF "v[120:123],v[176:179],a[132:135],v[120:123]\n"
"s_add_u32 s87,s90,11264\n"
CV "v66,v68,v69\n"
CV "v67,v70,v71\n"
MF "v[128:131],v[176:179],a[136:139],v[128:131]\n"
"s_waitcnt lgkmcnt(14)\n"
"buffer_store_dwordx4 v[64:67],v9,s[40:43],s87 offen sc1\n"
MF "v[128:131],v[180:183],a[176:179],v[128:131]\n"
"v_add_f32 v72,v72,v32\n"
"v_add_f32 v73,v73,v33\n"
MF "v[128:131],v[184:187],a[216:219],v[128:131]\n"
"s_waitcnt lgkmcnt(2)\n"
"v_add_f32 v74,v74,v34\n"
"v_add_f32 v75,v75,v35\n"
MF "v[132:135],a[244:247],a[216:219],v[132:135]\n"
"s_nop 0\n"
TR "v[176:177],v2\n"
TR "v[178:179],v2 offset:2048\n"
MF "v[132:135],a[240:243],a[176:179],v[132:135]\n"
"s_nop 0\n"
TR "v[180:181],v2 offset:4096\n"
TR "v[182:183],v2 offset:6144\n"
MF "v[132:135],v[188:191],a[136:139],v[132:135]\n"
"s_nop 0\n"
TR "v[184:185],v2 offset:8192\n"
TR "v[186:187],v2 offset:10240\n"
MF "v[124:127],v[188:191],a[132:135],v[124:127]\n"
"v_add_f32 v76,v76,v36\n"
"v_add_f32 v77,v77,v37\n"
MF "v[124:127],a[240:243],a[172:175],v[124:127]\n"
"v_add_f32 v78,v78,v38\n"
"v_add_f32 v79,v79,v39\n"
MF "v[124:127],a[244:247],a[212:215],v[124:127]\n"
CV "v72,v72,v73\n"
CV "v73,v74,v75\n"
MF "v[116:119],a[244:247],a[208:211],v[116:119]\n"
"s_add_u32 s87,s90,12288\n"
CV "v74,v76,v77\n"
CV "v75,v78,v79\n"
MF "v[116:119],a[240:243],a[168:171],v[116:119]\n"
"buffer_store_dwordx4 v[72:75],v9,s[40:43],s87 offen sc1\n"
MF "v[116:119],v[188:191],a[128:131],v[116:119]\n"
"v_add_f32 v80,v80,v40\n"
"v_add_f32 v81,v81,v41\n"
MF "v[108:111],v[188:191],a[124:127],v[108:111]\n"
"v_add_f32 v82,v82,v42\n"
"v_add_f32 v83,v83,v43\n"
MF "v[108:111],a[240:243],a[164:167],v[108:111]\n"
"v_add_f32 v84,v84,v44\n"
"v_add_f32 v85,v85,v45\n"
MF "v[108:111],a[244:247],a[204:207],v[108:111]\n"
"v_add_f32 v86,v86,v46\n"
"v_add_f32 v87,v87,v47\n"
MF "v[100:103],a[244:247],a[200:203],v[100:103]\n"
CV "v80,v80,v81\n"
CV "v81,v82,v83\n"
MF "v[100:103],a[240:243],a[160:163],v[100:103]\n"
"s_add_u32 s87,s90,13312\n"
CV "v82,v84,v85\n"
CV "v83,v86,v87\n"
MF "v[100:103],v[188:191],a[120:123],v[100:103]\n"
"s_waitcnt lgkmcnt(0)\n"
"buffer_store_dwordx4 v[80:83],v9,s[40:43],s87 offen sc1\n"
MF "v[136:139],v[176:179],a[120:123],v[136:139]\n"
"s_nop 0\n"
TR "v[188:189],v3\n"
TR "v[190:191],v3 offset:2048\n"
MF "v[136:139],v[180:183],a[160:163],v[136:139]\n"
"s_nop 0\n"
TR "a[240:241],v3 offset:4096\n"
TR "a[242:243],v3 offset:6144\n"
MF "v[136:139],v[184:187],a[200:203],v[136:139]\n"
"s_nop 0\n"
TR "a[244:245],v3 offset:8192\n"
TR "a[246:247],v3 offset:10240\n"
MF "v[144:147],v[184:187],a[204:207],v[144:147]\n"
"v_add_f32 v88,v88,v48\n"
"v_add_f32 v89,v89,v49\n"
MF "v[144:147],v[180:183],a[164:167],v[144:147]\n"
"v_add_f32 v90,v90,v50\n"
"v_add_f32 v91,v91,v51\n"
MF "v[144:147],v[176:179],a[124:127],v[144:147]\n"
"v_add_f32 v92,v92,v52\n"
"v_add_f32 v93,v93,v53\n"
MF "v[152:155],v[176:179],a[128:131],v[152:155]\n"
"v_add_f32 v94,v94,v54\n"
"v_add_f32 v95,v95,v55\n"
MF "v[152:155],v[180:183],a[168:171],v[152:155]\n"
CV "v88,v88,v89\n"
CV "v89,v90,v91\n"
MF "v[152:155],v[184:187],a[208:211],v[152:155]\n"
"s_add_u32 s87,s90,14336\n"
CV "v90,v92,v93\n"
CV "v91,v94,v95\n"
MF "v[160:163],v[184:187],a[212:215],v[160:163]\n"
"buffer_store_dwordx4 v[88:91],v9,s[40:43],s87 offen sc1\n"
MF "v[160:163],v[180:183],a[172:175],v[160:163]\n"
MF "v[160:163],v[176:179],a[132:135],v[160:163]\n"
MF "v[168:171],v[176:179],a[136:139],v[168:171]\n"
MF "v[168:171],v[180:183],a[176:179],v[168:171]\n"
"s_waitcnt vmcnt(19) lgkmcnt(0)\n"
MF "v[168:171],v[184:187],a[216:219],v[168:171]\n"
"s_barrier\n"
MF "v[172:175],a[244:247],a[216:219],v[172:175]\n"
"s_nop 0\n"
TR "v[176:177],v0 offset:49152\n"
TR "v[178:179],v0 offset:51200\n"
MF "v[172:175],a[240:243],a[176:179],v[172:175]\n"
"s_nop 0\n"
TR "v[180:181],v0 offset:53248\n"
TR "v[182:183],v0 offset:55296\n"
MF "v[172:175],v[188:191],a[136:139],v[172:175]\n"
"s_nop 0\n"
TR "v[184:185],v0 offset:57344\n"
TR "v[186:187],v0 offset:59392\n"
MF "v[164:167],v[188:191],a[132:135],v[164:167]\n"
"buffer_load_dword v10,v12,s[40:43],s86 offen sc1\n"
MF "v[164:167],a[240:243],a[172:175],v[164:167]\n"
MF "v[164:167],a[244:247],a[212:215],v[164:167]\n"
MF "v[156:159],a[244:247],a[208:211],v[156:159]\n"
MF "v[156:159],a[240:243],a[168:171],v[156:159]\n"
MF "v[156:159],v[188:191],a[128:131],v[156:159]\n"
MF "v[148:151],v[188:191],a[124:127],v[148:151]\n"
MF "v[148:151],a[240:243],a[164:167],v[148:151]\n"
MF "v[148:151],a[244:247],a[204:207],v[148:151]\n"
MF "v[140:143],a[244:247],a[200:203],v[140:143]\n"
MF "v[140:143],a[240:243],a[160:163],v[140:143]\n"
MF "v[140:143],v[188:191],a[120:123],v[140:143]\n"
"s_waitcnt lgkmcnt(0)\n"
MF "v[96:99],v[176:179],a[20:23],v[96:99]\n"
"s_nop 0\n"
TR "v[188:189],v1 offset:49152\n"
TR "v[190:191],v1 offset:51200\n"
MF "v[96:99],v[180:183],a[60:63],v[96:99]\n"
"s_nop 0\n"
TR "a[240:241],v1 offset:53248\n"
TR "a[242:243],v1 offset:55296\n"
MF "v[96:99],v[184:187],a[100:103],v[96:99]\n"
"s_nop 0\n"
TR "a[244:245],v1 offset:57344\n"
TR "a[246:247],v1 offset:59392\n"
MF "v[104:107],v[184:187],a[104:107],v[104:107]\n"
MF "v[104:107],v[180:183],a[64:67],v[104:107]\n"
MF "v[104:107],v[176:179],a[24:27],v[104:107]\n"
"s_waitcnt vmcnt(18)\n"
"v_mov_b32 v236,v228\n"
"v_mov_b32 v237,v229\n"
MF "v[96:99],v[208:211],a[252:255],v[96:99]\n"
MF "v[104:107],v[212:215],a[252:255],v[104:107]\n"
MF "v[112:115],v[216:219],a[252:255],v[112:115]\n"
MF "v[120:123],v[220:223],a[252:255],v[120:123]\n"
MF "v[128:131],v[224:227],a[252:255],v[128:131]\n"
MF "v[100:103],v[208:211],a[248:251],v[100:103]\n"
MF "v[108:111],v[212:215],a[248:251],v[108:111]\n"
MF "v[116:119],v[216:219],a[248:251],v[116:119]\n"
MF "v[124:127],v[220:223],a[248:251],v[124:127]\n"
MF "v[132:135],v[224:227],a[248:251],v[132:135]\n"
MF "v[248:251],v[230:233],a[250:253],v[248:251]\n"
MF "v[244:247],v[234:237],a[250:253],v[244:247]\n"
MF "v[112:115],v[176:179],a[28:31],v[112:115]\n"
MF "v[112:115],v[180:183],a[68:71],v[112:115]\n"
MF "v[112:115],v[184:187],a[108:111],v[112:115]\n"
MF "v[120:123],v[184:187],a[112:115],v[120:123]\n"
"s_nop 3\n"
CV "v244,v244,v245\n"
CV "v245,v246,v247\n"
MF "v[120:123],v[180:183],a[72:75],v[120:123]\n"
"s_bfe_u32 s87,s22,0x30014\n"
CV "v246,v248,v249\n"
CV "v247,v250,v251\n"
MF "v[120:123],v[176:179],a[32:35],v[120:123]\n"
"s_lshl_b32 s87,s87,7\n"
"v_permlane16_swap_b32 v244,v246\n"
"v_permlane16_swap_b32 v245,v247\n"
MF "v[128:131],v[176:179],a[36:39],v[128:131]\n"
"buffer_store_dwordx4 v[244:247],v8,s[44:47],s0 offen sc1\n"
MF "v[128:131],v[180:183],a[76:79],v[128:131]\n"
"s_and_b32 s60,s56,1\n"
MF "v[128:131],v[184:187],a[116:119],v[128:131]\n"
"s_waitcnt lgkmcnt(0)\n"
MF "v[132:135],a[244:247],a[116:119],v[132:135]\n"
"s_mul_i32 s60,s60,204800\n"
TR "v[176:177],v2 offset:49152\n"
TR "v[178:179],v2 offset:51200\n"
MF "v[132:135],a[240:243],a[76:79],v[132:135]\n"
"s_add_u32 s87,s87,s60\n"
TR "v[180:181],v2 offset:53248\n"
TR "v[182:183],v2 offset:55296\n"
MF "v[132:135],v[188:191],a[36:39],v[132:135]\n"
"s_mov_b32 s71,204800\n"
TR "v[184:185],v2 offset:57344\n"
TR "v[186:187],v2 offset:59392\n"
MF "v[124:127],v[188:191],a[32:35],v[124:127]\n"
"s_mov_b32 s66,204800\n"
MF "v[124:127],a[240:243],a[72:75],v[124:127]\n"
"s_add_u32 s72,s78,2\n"
MF "v[124:127],a[244:247],a[112:115],v[124:127]\n"
"s_and_b32 s72,s72,15\n"
MF "v[116:119],a[244:247],a[108:111],v[116:119]\n"
"s_bfe_u32 s60,s22,0x10014\n"
MF "v[116:119],a[240:243],a[68:71],v[116:119]\n"
"s_lshl_b32 s60,s60,5\n"
MF "v[116:119],v[188:191],a[28:31],v[116:119]\n"
"v_xor_b32 v242,s60,v8\n"
MF "v[108:111],v[188:191],a[24:27],v[108:111]\n"
"s_add_u32 s16,s78,6\n"
MF "v[108:111],a[240:243],a[64:67],v[108:111]\n"
"s_and_b32 s16,s16,15\n"
MF "v[108:111],a[244:247],a[104:107],v[108:111]\n"
"s_add_u32 s17,s78,4\n"
MF "v[100:103],a[244:247],a[100:103],v[100:103]\n"
"s_and_b32 s17,s17,15\n"
MF "v[100:103],a[240:243],a[60:63],v[100:103]\n"
MF "v[100:103],v[188:191],a[20:23],v[100:103]\n"
"s_waitcnt lgkmcnt(0)\n"
MF "v[136:139],v[176:179],a[20:23],v[136:139]\n"
"s_nop 0\n"
TR "v[188:189],v3 offset:49152\n"
TR "v[190:191],v3 offset:51200\n"
MF "v[136:139],v[180:183],a[60:63],v[136:139]\n"
"s_nop 0\n"
TR "a[240:241],v3 offset:53248\n"
TR "a[242:243],v3 offset:55296\n"
MF "v[136:139],v[184:187],a[100:103],v[136:139]\n"
"s_nop 0\n"
TR "a[244:245],v3 offset:57344\n"
TR "a[246:247],v3 offset:59392\n"
MF "v[144:147],v[184:187],a[104:107],v[144:147]\n"
MF "v[144:147],v[180:183],a[64:67],v[144:147]\n"
MF "v[144:147],v[176:179],a[24:27],v[144:147]\n"
MF "v[152:155],v[176:179],a[28:31],v[152:155]\n"
MF "v[152:155],v[180:183],a[68:71],v[152:155]\n"
MF "v[152:155],v[184:187],a[108:111],v[152:155]\n"
MF "v[160:163],v[184:187],a[112:115],v[160:163]\n"
MF "v[160:163],v[180:183],a[72:75],v[160:163]\n"
MF "v[160:163],v[176:179],a[32:35],v[160:163]\n"
MF "v[168:171],v[176:179],a[36:39],v[168:171]\n"
MF "v[168:171],v[180:183],a[76:79],v[168:171]\n"
"s_waitcnt vmcnt(1) lgkmcnt(0)\n"
MF "v[168:171],v[184:187],a[116:119],v[168:171]\n"
"s_barrier\n"
MF "v[172:175],a[244:247],a[116:119],v[172:175]\n"
"v_sub_u32 v10,v10,v11\n"
MF "v[172:175],a[240:243],a[76:79],v[172:175]\n"
"s_nop 0\n"
TR "v[176:177],v4\n"
TR "v[178:179],v4 offset:2048\n"
MF "v[172:175],v[188:191],a[36:39],v[172:175]\n"
"s_nop 0\n"
TR "v[180:181],v4 offset:4096\n"
TR "v[182:183],v4 offset:6144\n"
MF "v[164:167],v[188:191],a[32:35],v[164:167]\n"
"s_nop 0\n"
TR "v[184:185],v4 offset:8192\n"
TR "v[186:187],v4 offset:10240\n"
MF "v[164:167],a[240:243],a[72:75],v[164:167]\n"
"v_readlane_b32 s16,v10,s16\n"
MF "v[164:167],a[244:247],a[112:115],v[164:167]\n"
"s_cmp_lt_u32 s16,9\n"
"s_cselect_b32 s16,0xf0000000,0\n"
MF "v[156:159],a[244:247],a[108:111],v[156:159]\n"
"v_readlane_b32 s17,v10,s17\n"
MF "v[156:159],a[240:243],a[68:71],v[156:159]\n"
"s_cmp_lt_u32 s17,10\n"
"s_cselect_b32 s17,0xf0000000,0\n"
MF "v[156:159],v[188:191],a[28:31],v[156:159]\n"
"s_bfe_u32 s62,s22,0x30014\n"
MF "v[148:151],v[188:191],a[24:27],v[148:151]\n"
"s_add_u32 s62,s62,3\n"
"s_and_b32 s62,s62,7\n"
"s_mul_i32 s62,s62,204800\n"
MF "v[148:151],a[240:243],a[64:67],v[148:151]\n"
"s_add_u32 s62,s62,s73\n"
"s_add_u32 s62,s62,133120\n"
"s_or_b32 s62,s62,s16\n"
MF "v[148:151],a[244:247],a[104:107],v[148:151]\n"
LD "v[16:19],v9,s[40:43],s62 offen sc1\n"
MF "v[140:143],a[244:247],a[100:103],v[140:143]\n"
LD "v[24:27],v9,s[40:43],s62 offen offset:1024 sc1\n"
MF "v[140:143],a[240:243],a[60:63],v[140:143]\n"
MF "v[140:143],v[188:191],a[20:23],v[140:143]\n"
"s_waitcnt lgkmcnt(0)\n"
LD "v[32:35],v9,s[40:43],s62 offen offset:2048 sc1\n"
MF "v[96:99],v[176:179],a[140:143],v[96:99]\n"
"s_nop 0\n"
TR "v[188:189],v5\n"
TR "v[190:191],v5 offset:2048\n"
MF "v[96:99],v[180:183],a[180:183],v[96:99]\n"
"s_nop 0\n"
TR "a[240:241],v5 offset:4096\n"
TR "a[242:243],v5 offset:6144\n"
MF "v[96:99],v[184:187],a[220:223],v[96:99]\n"
"s_nop 0\n"
TR "a[244:245],v5 offset:8192\n"
TR "a[246:247],v5 offset:10240\n"
MF "v[104:107],v[184:187],a[224:227],v[104:107]\n"
LD "v[40:43],v9,s[40:43],s62 offen offset:3072 sc1\n"
MF "v[104:107],v[180:183],a[184:187],v[104:107]\n"
MF "v[104:107],v[176:179],a[144:147],v[104:107]\n"
"s_add_u32 s62,s62,4096\n"
MF "v[112:115],v[176:179],a[148:151],v[112:115]\n"
LD "v[48:51],v9,s[40:43],s62 offen sc1\n"
MF "v[112:115],v[180:183],a[188:191],v[112:115]\n"
"s_bfe_u32 s62,s22,0x30014\n"
"ds_write_addtid_b32 v96 offset:16384\n"
"ds_write_addtid_b32 v97 offset:16640\n"
MF "v[112:115],v[184:187],a[228:231],v[112:115]\n"
"s_add_u32 s62,s62,2\n"
"s_and_b32 s62,s62,7\n"
"s_mul_i32 s62,s62,204800\n"
MF "v[120:123],v[184:187],a[232:235],v[120:123]\n"
"s_add_u32 s62,s62,s73\n"
"s_add_u32 s62,s62,153600\n"
"s_or_b32 s62,s62,s17\n"
MF "v[120:123],v[180:183],a[192:195],v[120:123]\n"
LD "v[20:23],v9,s[40:43],s62 offen sc1\n"
MF "v[120:123],v[176:179],a[152:155],v[120:123]\n"
"s_nop 0\n"
"ds_write_addtid_b32 v98 offset:16896\n"
"ds_write_addtid_b32 v99 offset:17152\n"
MF "v[128:131],v[176:179],a[156:159],v[128:131]\n"
LD "v[28:31],v9,s[40:43],s62 offen offset:1024 sc1\n"
MF "v[128:131],v[180:183],a[196:199],v[128:131]\n"
"s_nop 0\n"
"ds_write_addtid_b32 v104 offset:18432\n"
"ds_write_addtid_b32 v105 offset:18688\n"
MF "v[128:131],v[184:187],a[236:239],v[128:131]\n"
"s_waitcnt lgkmcnt(6)\n"
LD "v[36:39],v9,s[40:43],s62 offen offset:2048 sc1\n"
MF "v[100:103],v[188:191],a[140:143],v[100:103]\n"
"s_nop 0\n"
TR "v[176:177],v6\n"
TR "v[178:179],v6 offset:2048\n"
MF "v[100:103],a[240:243],a[180:183],v[100:103]\n"
"s_nop 0\n"
TR "v[180:181],v6 offset:4096\n"
TR "v[182:183],v6 offset:6144\n"
MF "v[100:103],a[244:247],a[220:223],v[100:103]\n"
"s_nop 0\n"
TR "v[184:185],v6 offset:8192\n"
TR "v[186:187],v6 offset:10240\n"
MF "v[108:111],a[244:247],a[224:227],v[108:111]\n"
LD "v[44:47],v9,s[40:43],s62 offen offset:3072 sc1\n"
MF "v[108:111],a[240:243],a[184:187],v[108:111]\n"
"s_nop 0\n"
"ds_write_addtid_b32 v106 offset:18944\n"
"ds_write_addtid_b32 v107 offset:19200\n"
MF "v[108:111],v[188:191],a[144:147],v[108:111]\n"
"s_add_u32 s62,s62,4096\n"
"ds_write_addtid_b32 v112 offset:20480\n"
"ds_write_addtid_b32 v113 offset:20736\n"
MF "v[116:119],v[188:191],a[148:151],v[116:119]\n"
LD "v[52:55],v9,s[40:43],s62 offen sc1\n"
MF "v[116:119],a[240:243],a[188:191],v[116:119]\n"
"s_nop 0\n"
"ds_write_addtid_b32 v114 offset:20992\n"
"ds_write_addtid_b32 v115 offset:21248\n"
MF "v[116:119],a[244:247],a[228:231],v[116:119]\n"
"s_nop 0\n"
"ds_write_addtid_b32 v120 offset:22528\n"
"ds_write_addtid_b32 v121 offset:22784\n"
MF "v[124:127],a[244:247],a[232:235],v[124:127]\n"
"s_nop 0\n"
"ds_write_addtid_b32 v122 offset:23040\n"
"ds_write_addtid_b32 v123 offset:23296\n"
MF "v[124:127],a[240:243],a[192:195],v[124:127]\n"
"s_nop 0\n"
"ds_write_addtid_b32 v128 offset:24576\n"
"ds_write_addtid_b32 v129 offset:24832\n"
MF "v[124:127],v[188:191],a[152:155],v[124:127]\n"
"buffer_load_dword v10,v12,s[40:43],s86 offen sc1\n"
MF "v[132:135],v[188:191],a[156:159],v[132:135]\n"
"s_nop 0\n"
"ds_write_addtid_b32 v130 offset:25088\n"
"ds_write_addtid_b32 v131 offset:25344\n"
MF "v[132:135],a[240:243],a[196:199],v[132:135]\n"
"s_nop 0\n"
"ds_write_addtid_b32 v100 offset:17408\n"
"ds_write_addtid_b32 v101 offset:17664\n"
MF "v[132:135],a[244:247],a[236:239],v[132:135]\n"
"s_waitcnt lgkmcnt(15)\n"
"ds_write_addtid_b32 v102 offset:17920\n"
"ds_write_addtid_b32 v103 offset:18176\n"
MF "v[136:139],v[176:179],a[140:143],v[136:139]\n"
"s_nop 0\n"
TR "v[188:189],v7\n"
TR "v[190:191],v7 offset:2048\n"
MF "v[136:139],v[180:183],a[180:183],v[136:139]\n"
"s_nop 0\n"
TR "a[240:241],v7 offset:4096\n"
TR "a[242:243],v7 offset:6144\n"
MF "v[136:139],v[184:187],a[220:223],v[136:139]\n"
"s_nop 0\n"
TR "a[244:245],v7 offset:8192\n"
TR "a[246:247],v7 offset:10240\n"
MF "v[144:147],v[184:187],a[224:227],v[144:147]\n"
"s_nop 0\n"
"ds_write_addtid_b32 v108 offset:19456\n"
"ds_write_addtid_b32 v109 offset:19712\n"
MF "v[144:147],v[180:183],a[184:187],v[144:147]\n"
"s_nop 0\n"
"ds_write_addtid_b32 v110 offset:19968\n"
"ds_write_addtid_b32 v111 offset:20224\n"
MF "v[144:147],v[176:179],a[144:147],v[144:147]\n"
"s_nop 0\n"
"ds_write_addtid_b32 v116 offset:21504\n"
"ds_write_addtid_b32 v117 offset:21760\n"
MF "v[152:155],v[176:179],a[148:151],v[152:155]\n"
"s_nop 0\n"
"ds_write_addtid_b32 v118 offset:22016\n"
"ds_write_addtid_b32 v119 offset:22272\n"
MF "v[152:155],v[180:183],a[188:191],v[152:155]\n"
"ds_write_addtid_b32 v124 offset:23552\n"
MF "v[152:155],v[184:187],a[228:231],v[152:155]\n"
"ds_write_addtid_b32 v125 offset:23808\n"
MF "v[160:163],v[184:187],a[232:235],v[160:163]\n"
"ds_write_addtid_b32 v126 offset:24064\n"
MF "v[160:163],v[180:183],a[192:195],v[160:163]\n"
"ds_write_addtid_b32 v127 offset:24320\n"
MF "v[160:163],v[176:179],a[152:155],v[160:163]\n"
"ds_write_addtid_b32 v132 offset:25600\n"
MF "v[168:171],v[176:179],a[156:159],v[168:171]\n"
"ds_write_addtid_b32 v133 offset:25856\n"
MF "v[168:171],v[180:183],a[196:199],v[168:171]\n"
"ds_write_addtid_b32 v134 offset:26112\n"
MF "v[168:171],v[184:187],a[236:239],v[168:171]\n"
"s_waitcnt lgkmcnt(15)\n"
"ds_write_addtid_b32 v135 offset:26368\n"
MF "v[140:143],v[188:191],a[140:143],v[140:143]\n"
MF "v[140:143],a[240:243],a[180:183],v[140:143]\n"
MF "v[140:143],a[244:247],a[220:223],v[140:143]\n"
MF "v[148:151],a[244:247],a[224:227],v[148:151]\n"
"s_waitcnt lgkmcnt(0)\n"
MF "v[148:151],a[240:243],a[184:187],v[148:151]\n"
"s_barrier\n"
MF "v[148:151],v[188:191],a[144:147],v[148:151]\n"
"ds_read2st64_b32 v[56:57],v13 offset0:0 offset1:1\n"
MF "v[156:159],v[188:191],a[148:151],v[156:159]\n"
"ds_read2st64_b32 v[58:59],v13 offset0:2 offset1:3\n"
MF "v[156:159],a[240:243],a[188:191],v[156:159]\n"
"ds_read2st64_b32 v[60:61],v13 offset0:4 offset1:5\n"
MF "v[156:159],a[244:247],a[228:231],v[156:159]\n"
"ds_read2st64_b32 v[62:63],v13 offset0:6 offset1:7\n"
MF "v[164:167],a[244:247],a[232:235],v[164:167]\n"
"ds_read2st64_b32 v[64:65],v13 offset0:8 offset1:9\n"
MF "v[164:167],a[240:243],a[192:195],v[164:167]\n"
"ds_read2st64_b32 v[66:67],v13 offset0:10 offset1:11\n"
MF "v[164:167],v[188:191],a[152:155],v[164:167]\n"
"ds_read2st64_b32 v[68:69],v13 offset0:12 offset1:13\n"
MF "v[172:175],v[188:191],a[156:159],v[172:175]\n"
"ds_read2st64_b32 v[70:71],v13 offset0:14 offset1:15\n"
MF "v[172:175],a[240:243],a[196:199],v[172:175]\n"
"ds_read2st64_b32 v[72:73],v13 offset0:16 offset1:17\n"
MF "v[172:175],a[244:247],a[236:239],v[172:175]\n"
"ds_read2st64_b32 v[74:75],v13 offset0:18 offset1:19\n"
"ds_read2st64_b32 v[76:77],v13 offset0:20 offset1:21\n"
"ds_read2st64_b32 v[78:79],v13 offset0:22 offset1:23\n"
"s_nop 0\n"
"ds_read2st64_b32 v[80:81],v13 offset0:24 offset1:25\n"
"ds_read2st64_b32 v[82:83],v13 offset0:26 offset1:27\n"
"ds_read2st64_b32 v[84:85],v13 offset0:28 offset1:29\n"
"ds_read2st64_b32 v[86:87],v13 offset0:30 offset1:31\n"
"ds_read2st64_b32 v[88:89],v13 offset0:32 offset1:33\n"
"ds_read2st64_b32 v[90:91],v13 offset0:34 offset1:35\n"
"ds_read2st64_b32 v[92:93],v13 offset0:36 offset1:37\n"
".Lpg28h:\n"
"s_cbranch_execz .Lpg31h\n"
"s_nop 0\n"
"ds_read2st64_b32 v[94:95],v13 offset0:38 offset1:39\n"
"s_waitcnt vmcnt(0)\n"
"v_sub_u32 v10,v10,v11\n"
"s_cmp_eq_u32 s16,0\n"
"s_cbranch_scc1 .Lhave03\n"
"s_add_u32 s59,s78,6\n"
"s_and_b32 s59,s59,15\n"
"s_nop 3\n"
".Lnf3:\n"
"v_readlane_b32 s58,v10,s59\n"
"s_cmp_lt_u32 s58,9\n"
"s_cbranch_scc1 .Lsp3\n"
".Lnb3:\n"
"s_bfe_u32 s62,s22,0x30014\n"
"s_add_u32 s62,s62,3\n"
"s_and_b32 s62,s62,7\n"
"s_mul_i32 s62,s62,204800\n"
"s_add_u32 s62,s62,s73\n"
"s_add_u32 s62,s62,133120\n"
LD "v[16:19],v9,s[40:43],s62 offen sc1\n"
LD "v[24:27],v9,s[40:43],s62 offen offset:1024 sc1\n"
LD "v[32:35],v9,s[40:43],s62 offen offset:2048 sc1\n"
LD "v[40:43],v9,s[40:43],s62 offen offset:3072 sc1\n"
"s_add_u32 s62,s62,4096\n"
LD "v[48:51],v9,s[40:43],s62 offen sc1\n"
".Lhave03:\n"
"s_cmp_eq_u32 s17,0\n"
"s_cbranch_scc1 .Lhave02\n"
"s_add_u32 s59,s78,4\n"
"s_and_b32 s59,s59,15\n"
"s_nop 3\n"
".Lnf4:\n"
"v_readlane_b32 s58,v10,s59\n"
"s_cmp_lt_u32 s58,10\n"
"s_cbranch_scc1 .Lsp4\n"
".Lnb4:\n"
"s_bfe_u32 s62,s22,0x30014\n"
"s_add_u32 s62,s62,2\n"
"s_and_b32 s62,s62,7\n"
"s_mul_i32 s62,s62,204800\n"
"s_add_u32 s62,s62,s73\n"
"s_add_u32 s62,s62,153600\n"
LD "v[20:23],v9,s[40:43],s62 offen sc1\n"
LD "v[28:31],v9,s[40:43],s62 offen offset:1024 sc1\n"
LD "v[36:39],v9,s[40:43],s62 offen offset:2048 sc1\n"
LD "v[44:47],v9,s[40:43],s62 offen offset:3072 sc1\n"
"s_add_u32 s62,s62,4096\n"
LD "v[52:55],v9,s[40:43],s62 offen sc1\n"
".Lhave02:\n"
".Lnf5:\n"
"v_readlane_b32 s58,v10,s72\n"
"s_cmp_lt_u32 s58,11\n"
"s_cbranch_scc1 .Lsp5\n"
".Lnb5:\n"
"s_bfe_u32 s62,s22,0x30014\n"
"s_add_u32 s62,s62,1\n"
"s_and_b32 s62,s62,7\n"
"s_mul_i32 s62,s62,204800\n"
"s_add_u32 s62,s62,s73\n"
"s_add_u32 s62,s62,174080\n"
LD "v[208:211],v9,s[40:43],s62 offen sc1\n"
LD "v[212:215],v9,s[40:43],s62 offen offset:1024 sc1\n"
LD "v[216:219],v9,s[40:43],s62 offen offset:2048 sc1\n"
LD "v[220:223],v9,s[40:43],s62 offen offset:3072 sc1\n"
"s_add_u32 s62,s62,4096\n"
LD "v[224:227],v9,s[40:43],s62 offen sc1\n"
"s_waitcnt lgkmcnt(15)\n"
"v_pk_add_f32 v[136:137],v[136:137],v[56:57]\n"
"v_pk_add_f32 v[138:139],v[138:139],v[58:59]\n"
"v_pk_add_f32 v[140:141],v[140:141],v[60:61]\n"
"v_pk_add_f32 v[142:143],v[142:143],v[62:63]\n"
"v_pk_add_f32 v[144:145],v[144:145],v[64:65]\n"
"s_waitcnt lgkmcnt(14)\n"
"v_pk_add_f32 v[146:147],v[146:147],v[66:67]\n"
"s_waitcnt lgkmcnt(13)\n"
"v_pk_add_f32 v[148:149],v[148:149],v[68:69]\n"
"s_waitcnt lgkmcnt(12)\n"
"v_pk_add_f32 v[150:151],v[150:151],v[70:71]\n"
"s_waitcnt lgkmcnt(11)\n"
"v_pk_add_f32 v[152:153],v[152:153],v[72:73]\n"
"s_waitcnt lgkmcnt(10)\n"
"v_pk_add_f32 v[154:155],v[154:155],v[74:75]\n"
"s_waitcnt lgkmcnt(9)\n"
"v_pk_add_f32 v[156:157],v[156:157],v[76:77]\n"
"s_waitcnt lgkmcnt(8)\n"
"v_pk_add_f32 v[158:159],v[158:159],v[78:79]\n"
"s_waitcnt lgkmcnt(7)\n"
"v_pk_add_f32 v[160:161],v[160:161],v[80:81]\n"
"s_waitcnt lgkmcnt(6)\n"
"v_pk_add_f32 v[162:163],v[162:163],v[82:83]\n"
"s_waitcnt lgkmcnt(5)\n"
"v_pk_add_f32 v[164:165],v[164:165],v[84:85]\n"
"s_waitcnt lgkmcnt(4)\n"
"v_pk_add_f32 v[166:167],v[166:167],v[86:87]\n"
"s_waitcnt lgkmcnt(3)\n"
"v_pk_add_f32 v[168:169],v[168:169],v[88:89]\n"
"s_waitcnt lgkmcnt(2)\n"
"v_pk_add_f32 v[170:171],v[170:171],v[90:91]\n"
"s_waitcnt lgkmcnt(1)\n"
"v_pk_add_f32 v[172:173],v[172:173],v[92:93]\n"
"s_waitcnt lgkmcnt(0)\n"
"v_pk_add_f32 v[174:175],v[174:175],v[94:95]\n"
"s_waitcnt vmcnt(5)\n"
"s_nop 0\n"
MF "v[140:143],v[16:19],a[252:255],v[140:143]\n"
MF "v[148:151],v[24:27],a[252:255],v[148:151]\n"
MF "v[156:159],v[32:35],a[252:255],v[156:159]\n"
MF "v[164:167],v[40:43],a[252:255],v[164:167]\n"
MF "v[172:175],v[48:51],a[252:255],v[172:175]\n"
MF "v[136:139],v[16:19],a[248:251],v[136:139]\n"
MF "v[144:147],v[24:27],a[248:251],v[144:147]\n"
MF "v[152:155],v[32:35],a[248:251],v[152:155]\n"
MF "v[160:163],v[40:43],a[248:251],v[160:163]\n"
MF "v[168:171],v[48:51],a[248:251],v[168:171]\n"
MF "v[136:139],v[20:23],a[252:255],v[136:139]\n"
MF "v[144:147],v[28:31],a[252:255],v[144:147]\n"
MF "v[152:155],v[36:39],a[252:255],v[152:155]\n"
MF "v[160:163],v[44:47],a[252:255],v[160:163]\n"
MF "v[168:171],v[52:55],a[252:255],v[168:171]\n"
MF "v[140:143],v[20:23],a[248:251],v[140:143]\n"
MF "v[148:151],v[28:31],a[248:251],v[148:151]\n"
MF "v[156:159],v[36:39],a[248:251],v[156:159]\n"
MF "v[164:167],v[44:47],a[248:251],v[164:167]\n"
MF "v[172:175],v[52:55],a[248:251],v[172:175]\n"
"s_waitcnt vmcnt(0)\n"
MF "v[140:143],v[208:211],a[252:255],v[140:143]\n"
MF "v[136:139],v[208:211],a[248:251],v[136:139]\n"
MF "v[148:151],v[212:215],a[252:255],v[148:151]\n"
MF "v[144:147],v[212:215],a[248:251],v[144:147]\n"
MF "v[156:159],v[216:219],a[252:255],v[156:159]\n"
"s_nop 4\n"
CV "v136,v136,v137\n"
CV "v137,v138,v139\n"
MF "v[152:155],v[216:219],a[248:251],v[152:155]\n"
CV "v138,v140,v141\n"
CV "v139,v142,v143\n"
MF "v[164:167],v[220:223],a[252:255],v[164:167]\n"
CV "v144,v144,v145\n"
CV "v145,v146,v147\n"
MF "v[160:163],v[220:223],a[248:251],v[160:163]\n"
CV "v146,v148,v149\n"
CV "v147,v150,v151\n"
"v_permlane16_swap_b32 v136,v138\n"
"v_permlane16_swap_b32 v137,v139\n"
"buffer_store_dwordx4 v[136:139],v242,s[44:47],s87 offen offset:64 sc1\n"
"s_add_u32 s87,s87,40960\n"
MF "v[172:175],v[224:227],a[252:255],v[172:175]\n"
CV "v152,v152,v153\n"
CV "v153,v154,v155\n"
MF "v[168:171],v[224:227],a[248:251],v[168:171]\n"
CV "v154,v156,v157\n"
CV "v155,v158,v159\n"
"v_permlane16_swap_b32 v144,v146\n"
"v_permlane16_swap_b32 v145,v147\n"
"buffer_store_dwordx4 v[144:147],v242,s[44:47],s87 offen offset:64 sc1\n"
"s_add_u32 s87,s87,40960\n"
CV "v160,v160,v161\n"
CV "v161,v162,v163\n"
CV "v162,v164,v165\n"
CV "v163,v166,v167\n"
"v_permlane16_swap_b32 v152,v154\n"
"v_permlane16_swap_b32 v153,v155\n"
"buffer_store_dwordx4 v[152:155],v242,s[44:47],s87 offen offset:64 sc1\n"
"s_add_u32 s87,s87,40960\n"
CV "v168,v168,v169\n"
CV "v169,v170,v171\n"
CV "v170,v172,v173\n"
CV "v171,v174,v175\n"
"v_permlane16_swap_b32 v160,v162\n"
"v_permlane16_swap_b32 v161,v163\n"
"buffer_store_dwordx4 v[160:163],v242,s[44:47],s87 offen offset:64 sc1\n"
"s_add_u32 s87,s87,40960\n"
"v_permlane16_swap_b32 v168,v170\n"
"v_permlane16_swap_b32 v169,v171\n"
"buffer_store_dwordx4 v[168:171],v242,s[44:47],s87 offen offset:64 sc1\n"
"s_add_u32 s87,s87,40960\n"
"s_endpgm\n"
".Lpgrole1_h:\n"
"s_cbranch_execz .Lpg32h\n"
".Lrole1:\n"
"s_waitcnt lgkmcnt(4)\n"
MF "v[16:19],a[120:123],v[176:179],v[252:255]\n"
"s_mov_b32 s65,128\n"
TR "v[188:189],v1 offset:24576\n"
AR "v220,a44\n"
"s_waitcnt lgkmcnt(3)\n"
MF "v[16:19],a[160:163],v[180:183],v[16:19]\n"
"s_mov_b32 s9,0x3f80\n"
TR "v[190:191],v1 offset:26624\n"
AR "v221,a45\n"
"s_waitcnt lgkmcnt(2)\n"
MF "v[16:19],a[200:203],v[184:187],v[16:19]\n"
AR "v222,a46\n"
AR "v223,a47\n"
MF "v[24:27],a[204:207],v[184:187],v[252:255]\n"
"s_bfe_u32 s58,s22,0x30014\n"
TR "a[240:241],v1 offset:28672\n"
AR "v224,a64\n"
MF "v[24:27],a[164:167],v[180:183],v[24:27]\n"
"s_add_u32 s61,s58,1\n"
TR "a[242:243],v1 offset:30720\n"
AR "v225,a65\n"
MF "v[24:27],a[124:127],v[176:179],v[24:27]\n"
"s_and_b32 s61,s61,7\n"
AR "v226,a66\n"
AR "v227,a67\n"
MF "v[32:35],a[128:131],v[176:179],v[252:255]\n"
"s_mov_b32 s64,9\n"
TR "a[244:245],v1 offset:32768\n"
"ds_read_b128 a[64:67],v236 offset:49152\n"
MF "v[32:35],a[168:171],v[180:183],v[32:35]\n"
"s_xor_b32 s67,s78,8\n"
TR "a[246:247],v1 offset:34816\n"
"v_fma_f32 v220,v220,v209,v215\n"
MF "v[32:35],a[208:211],v[184:187],v[32:35]\n"
"s_mov_b32 s71,204800\n"
"v_fma_f32 v221,v221,v209,v215\n"
"v_fma_f32 v222,v222,v209,v215\n"
MF "v[40:43],a[212:215],v[184:187],v[252:255]\n"
"s_mov_b32 s66,204800\n"
"v_fma_f32 v223,v223,v209,v215\n"
"v_fma_f32 v224,v224,v209,v215\n"
MF "v[40:43],a[172:175],v[180:183],v[40:43]\n"
"s_and_b32 s73,s56,1\n"
"v_fma_f32 v225,v225,v209,v215\n"
"v_fma_f32 v226,v226,v209,v215\n"
MF "v[40:43],a[132:135],v[176:179],v[40:43]\n"
"s_mul_i32 s73,s73,5120\n"
"v_fma_f32 v227,v227,v209,v215\n"
CV "v220,v220,v221\n"
MF "v[48:51],a[136:139],v[176:179],v[252:255]\n"
"s_add_u32 s73,s73,s93\n"
CV "v221,v222,v223\n"
CV "v222,v224,v225\n"
MF "v[48:51],a[176:179],v[180:183],v[48:51]\n"
"s_xor_b32 s58,s58,4\n"
CV "v223,v226,v227\n"
AW "a44,v220\n"
MF "v[48:51],a[216:219],v[184:187],v[48:51]\n"
"s_waitcnt lgkmcnt(0)\n"
AW "a45,v221\n"
AW "a46,v222\n"
MF "v[52:55],a[216:219],a[244:247],v[252:255]\n"
"s_mul_i32 s70,s58,204800\n"
TR "v[176:177],v2 offset:24576\n"
AW "a47,v223\n"
MF "v[52:55],a[176:179],a[240:243],v[52:55]\n"
"s_add_u32 s70,s70,s73\n"
TR "v[178:179],v2 offset:26624\n"
AR "v228,a84\n"
MF "v[52:55],a[136:139],v[188:191],v[52:55]\n"
"s_add_u32 s70,s70,0xc800\n"
AR "v229,a85\n"
AR "v230,a86\n"
MF "v[44:47],a[132:135],v[188:191],v[252:255]\n"
"s_mov_b32 s85,1\n"
TR "v[180:181],v2 offset:28672\n"
AR "v231,a87\n"
MF "v[44:47],a[172:175],a[240:243],v[44:47]\n"
"s_mov_b32 s83,0xf0000000\n"
TR "v[182:183],v2 offset:30720\n"
AR "v232,a104\n"
MF "v[44:47],a[212:215],a[244:247],v[44:47]\n"
"s_mov_b32 s82,0xf0000000\n"
AR "v233,a105\n"
AR "v234,a106\n"
MF "v[36:39],a[208:211],a[244:247],v[252:255]\n"
"s_add_u32 s58,s79,73728\n"
TR "v[184:185],v2 offset:32768\n"
AR "v235,a107\n"
MF "v[36:39],a[168:171],a[240:243],v[36:39]\n"
"s_mov_b32 s90,s91\n"
TR "v[186:187],v2 offset:34816\n"
"ds_read_b128 a[104:107],v237 offset:4096\n"
MF "v[36:39],a[128:131],v[188:191],v[36:39]\n"
"s_add_u32 s91,s91,20480\n"
"v_fma_f32 v228,v228,v209,v215\n"
"v_fma_f32 v229,v229,v209,v215\n"
MF "v[28:31],a[124:127],v[188:191],v[252:255]\n"
"s_bfe_u32 s74,s23,0x70000\n"
"v_fma_f32 v230,v230,v209,v215\n"
"v_fma_f32 v231,v231,v209,v215\n"
MF "v[28:31],a[164:167],a[240:243],v[28:31]\n"
"s_lshl_b32 s74,s74,1\n"
"v_fma_f32 v232,v232,v209,v215\n"
"v_fma_f32 v233,v233,v209,v215\n"
MF "v[28:31],a[204:207],a[244:247],v[28:31]\n"
"s_add_u32 s74,s74,s56\n"
"v_fma_f32 v234,v234,v209,v215\n"
"v_fma_f32 v235,v235,v209,v215\n"
MF "v[20:23],a[200:203],a[244:247],v[252:255]\n"
"s_sub_u32 s74,s74,2\n"
CV "v228,v228,v229\n"
CV "v229,v230,v231\n"
MF "v[20:23],a[160:163],a[240:243],v[20:23]\n"
"s_mul_i32 s74,s74,30720\n"
CV "v230,v232,v233\n"
CV "v231,v234,v235\n"
MF "v[20:23],a[120:123],v[188:191],v[20:23]\n"
"s_waitcnt lgkmcnt(1)\n"
AW "a84,v228\n"
AW "a85,v229\n"
MF "v[56:59],a[120:123],v[176:179],v[252:255]\n"
"s_bfe_u32 s68,s23,0x30003\n"
TR "v[188:189],v3 offset:24576\n"
"v_add_u32 v198,59392,v192\n"
MF "v[56:59],a[160:163],v[180:183],v[56:59]\n"
"s_add_u32 s68,s68,7\n"
TR "v[190:191],v3 offset:26624\n"
"v_add_u32 v199,68608,v192\n"
MF "v[56:59],a[200:203],v[184:187],v[56:59]\n"
"s_lshl_b32 s68,s68,7\n"
"v_add_u32 v200,81920,v192\n"
"v_add_u32 v201,91136,v192\n"
MF "v[64:67],a[204:207],v[184:187],v[252:255]\n"
"s_and_b32 s68,s68,768\n"
TR "a[240:241],v3 offset:28672\n"
"v_add_u32 v202,100352,v192\n"
MF "v[64:67],a[164:167],v[180:183],v[64:67]\n"
"s_bfe_u32 s4,s22,0x30014\n"
TR "a[242:243],v3 offset:30720\n"
"v_add_u32 v203,109568,v192\n"
MF "v[64:67],a[124:127],v[176:179],v[64:67]\n"
"s_add_u32 s4,s4,32\n"
AW "a86,v230\n"
AW "a87,v231\n"
MF "v[72:75],a[128:131],v[176:179],v[252:255]\n"
"s_sub_u32 s4,s4,s92\n"
TR "a[244:245],v3 offset:32768\n"
AR "v220,a8\n"
MF "v[72:75],a[168:171],v[180:183],v[72:75]\n"
"s_cmp_eq_u32 s56,2\n"
"s_cselect_b32 s4,s4,64\n"
TR "a[246:247],v3 offset:34816\n"
MF "v[72:75],a[208:211],v[184:187],v[72:75]\n"
"s_mul_i32 s95,s92,3277\n"
AR "v221,a9\n"
AR "v222,a10\n"
MF "v[80:83],a[212:215],v[184:187],v[252:255]\n"
"s_lshr_b32 s95,s95,16\n"
AR "v223,a11\n"
AR "v224,a28\n"
MF "v[80:83],a[172:175],v[180:183],v[80:83]\n"
"s_mul_i32 s59,s95,20\n"
AR "v225,a29\n"
AR "v226,a30\n"
MF "v[80:83],a[132:135],v[176:179],v[80:83]\n"
"s_sub_u32 s59,s92,s59\n"
AR "v227,a31\n"
"v_fma_f32 v220,v220,v210,v216\n"
MF "v[88:91],a[136:139],v[176:179],v[252:255]\n"
"s_cmp_ge_u32 s59,10\n"
"s_cselect_b32 s96,4294557760,0\n"
"ds_read_b128 a[28:31],v236 offset:32768\n"
MF "v[88:91],a[176:179],v[180:183],v[88:91]\n"
"s_waitcnt vmcnt(0) lgkmcnt(0)\n"
"v_fma_f32 v221,v221,v210,v216\n"
"v_fma_f32 v222,v222,v210,v216\n"
MF "v[88:91],a[216:219],v[184:187],v[88:91]\n"
"s_barrier\n"
MF "v[92:95],a[216:219],a[244:247],v[252:255]\n"
"s_add_u32 s89,s89,s65\n"
TR "v[176:177],v0\n"
"v_fma_f32 v223,v223,v210,v216\n"
MF "v[92:95],a[176:179],a[240:243],v[92:95]\n"
"s_add_u32 m0,s79,24576\n"
TR "v[178:179],v0 offset:2048\n"
"v_fma_f32 v224,v224,v210,v216\n"
MF "v[92:95],a[136:139],v[188:191],v[92:95]\n"
LD "v192,s[48:51],s89 offen lds\n"
MF "v[84:87],a[132:135],v[188:191],v[252:255]\n"
"s_add_u32 s96,s96,1024\n"
TR "v[180:181],v0 offset:4096\n"
"v_fma_f32 v225,v225,v210,v216\n"
MF "v[84:87],a[172:175],a[240:243],v[84:87]\n"
"s_mul_i32 s59,s59,40960\n"
TR "v[182:183],v0 offset:6144\n"
"v_fma_f32 v226,v226,v210,v216\n"
MF "v[84:87],a[212:215],a[244:247],v[84:87]\n"
LD "v193,s[48:51],s89 offen offset:1024 lds\n"
MF "v[76:79],a[208:211],a[244:247],v[252:255]\n"
"s_add_u32 s96,s96,s59\n"
TR "v[184:185],v0 offset:8192\n"
"v_fma_f32 v227,v227,v210,v216\n"
MF "v[76:79],a[168:171],a[240:243],v[76:79]\n"
"s_lshl_b32 s59,s95,7\n"
TR "v[186:187],v0 offset:10240\n"
CV "v220,v220,v221\n"
MF "v[76:79],a[128:131],v[188:191],v[76:79]\n"
"s_add_u32 s96,s96,s59\n"
TR "v[96:97],v0 offset:49152\n"
CV "v221,v222,v223\n"
MF "v[68:71],a[124:127],v[188:191],v[252:255]\n"
"s_lshl_b32 s2,s92,10\n"
TR "v[98:99],v0 offset:51200\n"
CV "v222,v224,v225\n"
MF "v[68:71],a[164:167],a[240:243],v[68:71]\n"
LD "v194,s[48:51],s89 offen offset:2048 lds\n"
MF "v[68:71],a[204:207],a[244:247],v[68:71]\n"
"s_add_u32 s2,s2,s93\n"
TR "v[100:101],v0 offset:53248\n"
CV "v223,v226,v227\n"
MF "v[60:63],a[200:203],a[244:247],v[252:255]\n"
"s_mov_b32 s0,s96\n"
TR "v[102:103],v0 offset:55296\n"
AW "a8,v220\n"
MF "v[60:63],a[160:163],a[240:243],v[60:63]\n"
"s_add_u32 s92,s92,s4\n"
TR "v[104:105],v0 offset:57344\n"
AW "a9,v221\n"
MF "v[60:63],a[120:123],v[188:191],v[60:63]\n"
"s_waitcnt lgkmcnt(5)\n"
LD "v195,s[48:51],s89 offen offset:3072 lds\n"
MF "v[16:19],a[140:143],v[176:179],v[16:19]\n"
"s_mul_i32 s95,s92,3277\n"
TR "v[188:189],v1\n"
AW "a10,v222\n"
MF "v[16:19],a[180:183],v[180:183],v[16:19]\n"
"s_lshr_b32 s95,s95,16\n"
TR "v[190:191],v1 offset:2048\n"
AW "a11,v223\n"
MF "v[16:19],a[220:223],v[184:187],v[16:19]\n"
"s_mul_i32 s59,s95,20\n"
TR "v[106:107],v0 offset:59392\n"
AR "v228,a48\n"
MF "v[24:27],a[224:227],v[184:187],v[24:27]\n"
"s_sub_u32 s59,s92,s59\n"
TR "a[240:241],v1 offset:4096\n"
AR "v229,a49\n"
MF "v[24:27],a[184:187],v[180:183],v[24:27]\n"
"s_add_u32 m0,s79,28672\n"
TR "a[242:243],v1 offset:6144\n"
AR "v230,a50\n"
MF "v[24:27],a[144:147],v[176:179],v[24:27]\n"
LD "v196,s[48:51],s89 offen lds\n"
MF "v[32:35],a[148:151],v[176:179],v[32:35]\n"
"s_cmp_ge_u32 s59,10\n"
"s_cselect_b32 s96,4294557760,0\n"
TR "a[244:245],v1 offset:8192\n"
MF "v[32:35],a[188:191],v[180:183],v[32:35]\n"
"s_add_u32 s96,s96,1024\n"
TR "a[246:247],v1 offset:10240\n"
AR "v231,a51\n"
MF "v[32:35],a[228:231],v[184:187],v[32:35]\n"
LD "v197,s[48:51],s89 offen offset:1024 lds\n"
MF "v[40:43],a[232:235],v[184:187],v[40:43]\n"
"s_mul_i32 s59,s59,40960\n"
TR "v[108:109],v4\n"
AR "v232,a68\n"
MF "v[40:43],a[192:195],v[180:183],v[40:43]\n"
"s_add_u32 s96,s96,s59\n"
TR "v[110:111],v4 offset:2048\n"
AR "v233,a69\n"
MF "v[40:43],a[152:155],v[176:179],v[40:43]\n"
"s_lshl_b32 s59,s95,7\n"
TR "v[112:113],v4 offset:4096\n"
AR "v234,a70\n"
MF "v[48:51],a[156:159],v[176:179],v[48:51]\n"
"s_add_u32 s96,s96,s59\n"
TR "v[114:115],v4 offset:6144\n"
AR "v235,a71\n"
MF "v[48:51],a[196:199],v[180:183],v[48:51]\n"
LD "v198,s[48:51],s89 offen offset:2048 lds\n"
MF "v[48:51],a[236:239],v[184:187],v[48:51]\n"
"s_waitcnt lgkmcnt(4)\n"
TR "v[116:117],v4 offset:8192\n"
"ds_read_b128 a[68:71],v236 offset:53248\n"
MF "v[52:55],a[236:239],a[244:247],v[52:55]\n"
"s_lshl_b32 s3,s92,10\n"
TR "v[176:177],v2\n"
"v_fma_f32 v228,v228,v210,v216\n"
MF "v[52:55],a[196:199],a[240:243],v[52:55]\n"
"s_add_u32 s3,s3,s93\n"
TR "v[178:179],v2 offset:2048\n"
"v_fma_f32 v229,v229,v210,v216\n"
MF "v[52:55],a[156:159],v[188:191],v[52:55]\n"
LD "v199,s[48:51],s89 offen offset:3072 lds\n"
MF "v[44:47],a[152:155],v[188:191],v[44:47]\n"
"s_cmp_lt_u32 s92,40\n"
"s_cselect_b32 s3,s3,0xf0000000\n"
TR "v[180:181],v2 offset:4096\n"
MF "v[44:47],a[192:195],a[240:243],v[44:47]\n"
"s_cmp_lt_u32 s92,40\n"
"s_cselect_b32 s1,s96,0xf0000000\n"
TR "v[182:183],v2 offset:6144\n"
MF "v[44:47],a[232:235],a[244:247],v[44:47]\n"
"s_sub_u32 s92,s92,s4\n"
TR "v[118:119],v4 offset:10240\n"
"v_fma_f32 v230,v230,v210,v216\n"
MF "v[36:39],a[228:231],a[244:247],v[36:39]\n"
"s_sub_u32 s2,s2,204800\n"
TR "v[184:185],v2 offset:8192\n"
"v_fma_f32 v231,v231,v210,v216\n"
MF "v[36:39],a[188:191],a[240:243],v[36:39]\n"
"s_add_u32 m0,s79,32768\n"
TR "v[186:187],v2 offset:10240\n"
"v_fma_f32 v232,v232,v210,v216\n"
MF "v[36:39],a[148:151],v[188:191],v[36:39]\n"
LD "v200,s[48:51],s89 offen lds\n"
MF "v[28:31],a[144:147],v[188:191],v[28:31]\n"
"s_mov_b32 s92,64\n"
TR "v[120:121],v1 offset:49152\n"
"v_fma_f32 v233,v233,v210,v216\n"
MF "v[28:31],a[184:187],a[240:243],v[28:31]\n"
TR "v[122:123],v1 offset:51200\n"
"v_fma_f32 v234,v234,v210,v216\n"
MF "v[28:31],a[224:227],a[244:247],v[28:31]\n"
TR "v[124:125],v1 offset:53248\n"
"v_fma_f32 v235,v235,v210,v216\n"
MF "v[20:23],a[220:223],a[244:247],v[20:23]\n"
LD "v201,s[48:51],s89 offen offset:1024 lds\n"
MF "v[20:23],a[180:183],a[240:243],v[20:23]\n"
TR "v[126:127],v1 offset:55296\n"
CV "v228,v228,v229\n"
MF "v[20:23],a[140:143],v[188:191],v[20:23]\n"
"s_waitcnt lgkmcnt(4)\n"
TR "v[128:129],v1 offset:57344\n"
CV "v229,v230,v231\n"
MF "v[56:59],a[140:143],v[176:179],v[56:59]\n"
TR "v[188:189],v3\n"
CV "v230,v232,v233\n"
MF "v[56:59],a[180:183],v[180:183],v[56:59]\n"
TR "v[190:191],v3 offset:2048\n"
CV "v231,v234,v235\n"
MF "v[56:59],a[220:223],v[184:187],v[56:59]\n"
LD "v202,s[48:51],s89 offen offset:2048 lds\n"
MF "v[64:67],a[224:227],v[184:187],v[64:67]\n"
TR "a[240:241],v3 offset:4096\n"
AW "a48,v228\n"
MF "v[64:67],a[184:187],v[180:183],v[64:67]\n"
TR "a[242:243],v3 offset:6144\n"
AW "a49,v229\n"
MF "v[64:67],a[144:147],v[176:179],v[64:67]\n"
LD "v203,s[48:51],s89 offen offset:3072 lds\n"
MF "v[72:75],a[148:151],v[176:179],v[72:75]\n"
TR "a[244:245],v3 offset:8192\n"
AW "a50,v230\n"
MF "v[72:75],a[188:191],v[180:183],v[72:75]\n"
TR "a[246:247],v3 offset:10240\n"
AW "a51,v231\n"
MF "v[72:75],a[228:231],v[184:187],v[72:75]\n"
TR "v[130:131],v1 offset:59392\n"
AR "v220,a88\n"
MF "v[80:83],a[232:235],v[184:187],v[80:83]\n"
TR "v[132:133],v5\n"
AR "v221,a89\n"
MF "v[80:83],a[192:195],v[180:183],v[80:83]\n"
TR "v[134:135],v5 offset:2048\n"
AR "v222,a90\n"
MF "v[80:83],a[152:155],v[176:179],v[80:83]\n"
TR "v[136:137],v5 offset:4096\n"
AR "v223,a91\n"
MF "v[88:91],a[156:159],v[176:179],v[88:91]\n"
TR "v[138:139],v5 offset:6144\n"
AR "v224,a108\n"
MF "v[88:91],a[196:199],v[180:183],v[88:91]\n"
TR "v[140:141],v5 offset:8192\n"
"s_waitcnt lgkmcnt(0)\n"
MF "v[88:91],a[236:239],v[184:187],v[88:91]\n"
"s_barrier\n"
MF "v[92:95],a[236:239],a[244:247],v[92:95]\n"
TR "v[142:143],v5 offset:10240\n"
AR "v225,a109\n"
MF "v[92:95],a[196:199],a[240:243],v[92:95]\n"
TR "v[144:145],v2 offset:49152\n"
AR "v226,a110\n"
MF "v[92:95],a[156:159],v[188:191],v[92:95]\n"
TR "v[146:147],v2 offset:51200\n"
AR "v227,a111\n"
MF "v[84:87],a[152:155],v[188:191],v[84:87]\n"
"s_nop 0\n"
TR "v[148:149],v2 offset:53248\n"
"ds_read_b128 a[108:111],v237 offset:8192\n"
MF "v[84:87],a[192:195],a[240:243],v[84:87]\n"
TR "v[150:151],v2 offset:55296\n"
"v_fma_f32 v220,v220,v210,v216\n"
MF "v[84:87],a[232:235],a[244:247],v[84:87]\n"
TR "v[152:153],v2 offset:57344\n"
"v_fma_f32 v221,v221,v210,v216\n"
MF "v[76:79],a[228:231],a[244:247],v[76:79]\n"
".Lpg29h:\n"
"s_cbranch_execz .Lpg33h\n"
TR "v[154:155],v2 offset:59392\n"
"v_fma_f32 v222,v222,v210,v216\n"
MF "v[76:79],a[188:191],a[240:243],v[76:79]\n"
TR "v[156:157],v6\n"
"v_fma_f32 v223,v223,v210,v216\n"
MF "v[76:79],a[148:151],v[188:191],v[76:79]\n"
TR "v[158:159],v6 offset:2048\n"
"v_fma_f32 v224,v224,v210,v216\n"
MF "v[68:71],a[144:147],v[188:191],v[68:71]\n"
TR "v[160:161],v6 offset:4096\n"
"v_fma_f32 v225,v225,v210,v216\n"
MF "v[68:71],a[184:187],a[240:243],v[68:71]\n"
TR "v[162:163],v6 offset:6144\n"
"v_fma_f32 v226,v226,v210,v216\n"
MF "v[68:71],a[224:227],a[244:247],v[68:71]\n"
TR "v[164:165],v6 offset:8192\n"
"v_fma_f32 v227,v227,v210,v216\n"
MF "v[60:63],a[220:223],a[244:247],v[60:63]\n"
TR "v[166:167],v6 offset:10240\n"
CV "v220,v220,v221\n"
MF "v[60:63],a[180:183],a[240:243],v[60:63]\n"
TR "v[168:169],v3 offset:49152\n"
CV "v221,v222,v223\n"
MF "v[60:63],a[140:143],v[188:191],v[60:63]\n"
TR "v[170:171],v3 offset:51200\n"
CV "v222,v224,v225\n"
MF "v[16:19],a[0:3],v[96:99],v[16:19]\n"
TR "v[172:173],v3 offset:53248\n"
CV "v223,v226,v227\n"
MF "v[16:19],a[40:43],v[100:103],v[16:19]\n"
TR "v[174:175],v3 offset:55296\n"
AW "a88,v220\n"
MF "v[16:19],a[80:83],v[104:107],v[16:19]\n"
TR "v[204:205],v3 offset:57344\n"
AW "a89,v221\n"
MF "v[16:19],a[20:23],v[108:111],v[16:19]\n"
TR "v[206:207],v3 offset:59392\n"
AW "a90,v222\n"
MF "v[16:19],a[60:63],v[112:115],v[16:19]\n"
TR "v[244:245],v7\n"
AW "a91,v223\n"
MF "v[16:19],a[100:103],v[116:119],v[16:19]\n"
"s_waitcnt lgkmcnt(15)\n"
TR "v[246:247],v7 offset:2048\n"
AR "v228,a12\n"
MF "v[20:23],a[100:103],v[140:143],v[20:23]\n"
TR "v[248:249],v7 offset:4096\n"
AR "v229,a13\n"
MF "v[20:23],a[60:63],v[136:139],v[20:23]\n"
TR "v[250:251],v7 offset:6144\n"
AR "v230,a14\n"
MF "v[20:23],a[20:23],v[132:135],v[20:23]\n"
TR "v[238:239],v7 offset:8192\n"
AR "v231,a15\n"
MF "v[20:23],a[80:83],v[128:131],v[20:23]\n"
TR "v[240:241],v7 offset:10240\n"
AR "v232,a32\n"
MF "v[20:23],a[40:43],v[124:127],v[20:23]\n"
AR "v233,a33\n"
AR "v234,a34\n"
MF "v[20:23],a[0:3],v[120:123],v[20:23]\n"
"s_waitcnt lgkmcnt(12)\n"
AR "v235,a35\n"
"v_fma_f32 v228,v228,v211,v217\n"
MF "v[56:59],a[0:3],v[144:147],v[56:59]\n"
"ds_read_b128 a[32:35],v236 offset:36864\n"
"v_fma_f32 v229,v229,v211,v217\n"
MF "v[56:59],a[40:43],v[148:151],v[56:59]\n"
"v_fma_f32 v230,v230,v211,v217\n"
"v_fma_f32 v231,v231,v211,v217\n"
MF "v[56:59],a[80:83],v[152:155],v[56:59]\n"
"v_fma_f32 v232,v232,v211,v217\n"
"v_fma_f32 v233,v233,v211,v217\n"
MF "v[56:59],a[20:23],v[156:159],v[56:59]\n"
"v_fma_f32 v234,v234,v211,v217\n"
"v_fma_f32 v235,v235,v211,v217\n"
MF "v[56:59],a[60:63],v[160:163],v[56:59]\n"
CV "v228,v228,v229\n"
CV "v229,v230,v231\n"
MF "v[56:59],a[100:103],v[164:167],v[56:59]\n"
"s_waitcnt lgkmcnt(1)\n"
CV "v230,v232,v233\n"
CV "v231,v234,v235\n"
MF "v[60:63],a[100:103],v[238:241],v[60:63]\n"
AW "a12,v228\n"
AW "a13,v229\n"
MF "v[60:63],a[60:63],v[248:251],v[60:63]\n"
AW "a14,v230\n"
AW "a15,v231\n"
MF "v[60:63],a[20:23],v[244:247],v[60:63]\n"
AR "v220,a52\n"
AR "v221,a53\n"
MF "v[60:63],a[80:83],v[204:207],v[60:63]\n"
AR "v222,a54\n"
AR "v223,a55\n"
MF "v[60:63],a[40:43],v[172:175],v[60:63]\n"
AR "v224,a72\n"
AR "v225,a73\n"
MF "v[60:63],a[0:3],v[168:171],v[60:63]\n"
AR "v226,a74\n"
AR "v227,a75\n"
MF "v[68:71],a[4:7],v[168:171],v[68:71]\n"
"ds_read_b128 a[72:75],v236 offset:57344\n"
"v_fma_f32 v220,v220,v211,v217\n"
MF "v[68:71],a[44:47],v[172:175],v[68:71]\n"
"v_fma_f32 v221,v221,v211,v217\n"
"v_fma_f32 v222,v222,v211,v217\n"
MF "v[68:71],a[84:87],v[204:207],v[68:71]\n"
"v_fma_f32 v223,v223,v211,v217\n"
"v_fma_f32 v224,v224,v211,v217\n"
MF "v[68:71],a[24:27],v[244:247],v[68:71]\n"
"v_fma_f32 v225,v225,v211,v217\n"
"v_fma_f32 v226,v226,v211,v217\n"
MF "v[68:71],a[64:67],v[248:251],v[68:71]\n"
"v_fma_f32 v227,v227,v211,v217\n"
CV "v220,v220,v221\n"
MF "v[68:71],a[104:107],v[238:241],v[68:71]\n"
"s_waitcnt lgkmcnt(0)\n"
CV "v221,v222,v223\n"
CV "v222,v224,v225\n"
MF "v[64:67],a[104:107],v[164:167],v[64:67]\n"
"s_barrier\n"
MF "v[64:67],a[64:67],v[160:163],v[64:67]\n"
CV "v223,v226,v227\n"
AW "a52,v220\n"
MF "v[64:67],a[24:27],v[156:159],v[64:67]\n"
AW "a53,v221\n"
AW "a54,v222\n"
MF "v[64:67],a[84:87],v[152:155],v[64:67]\n"
AW "a55,v223\n"
AR "v228,a92\n"
MF "v[64:67],a[44:47],v[148:151],v[64:67]\n"
AR "v229,a93\n"
AR "v230,a94\n"
MF "v[64:67],a[4:7],v[144:147],v[64:67]\n"
AR "v231,a95\n"
AR "v232,a112\n"
MF "v[28:31],a[4:7],v[120:123],v[28:31]\n"
AR "v233,a113\n"
AR "v234,a114\n"
MF "v[28:31],a[44:47],v[124:127],v[28:31]\n"
AR "v235,a115\n"
"v_fma_f32 v228,v228,v211,v217\n"
MF "v[28:31],a[84:87],v[128:131],v[28:31]\n"
"ds_read_b128 a[112:115],v237 offset:12288\n"
"v_fma_f32 v229,v229,v211,v217\n"
MF "v[28:31],a[24:27],v[132:135],v[28:31]\n"
"v_fma_f32 v230,v230,v211,v217\n"
"v_fma_f32 v231,v231,v211,v217\n"
MF "v[28:31],a[64:67],v[136:139],v[28:31]\n"
"v_fma_f32 v232,v232,v211,v217\n"
"v_fma_f32 v233,v233,v211,v217\n"
MF "v[28:31],a[104:107],v[140:143],v[28:31]\n"
"v_fma_f32 v234,v234,v211,v217\n"
"v_fma_f32 v235,v235,v211,v217\n"
MF "v[24:27],a[104:107],v[116:119],v[24:27]\n"
CV "v228,v228,v229\n"
CV "v229,v230,v231\n"
MF "v[24:27],a[64:67],v[112:115],v[24:27]\n"
CV "v230,v232,v233\n"
CV "v231,v234,v235\n"
MF "v[24:27],a[24:27],v[108:111],v[24:27]\n"
AW "a92,v228\n"
AW "a93,v229\n"
MF "v[24:27],a[84:87],v[104:107],v[24:27]\n"
AW "a94,v230\n"
AW "a95,v231\n"
MF "v[24:27],a[44:47],v[100:103],v[24:27]\n"
AR "v220,a16\n"
AR "v221,a17\n"
MF "v[24:27],a[4:7],v[96:99],v[24:27]\n"
AR "v222,a18\n"
AR "v223,a19\n"
MF "v[32:35],a[8:11],v[96:99],v[32:35]\n"
AR "v224,a36\n"
AR "v225,a37\n"
MF "v[32:35],a[48:51],v[100:103],v[32:35]\n"
AR "v226,a38\n"
AR "v227,a39\n"
MF "v[32:35],a[88:91],v[104:107],v[32:35]\n"
"ds_read_b128 a[36:39],v236 offset:40960\n"
"v_fma_f32 v220,v220,v212,v218\n"
MF "v[32:35],a[28:31],v[108:111],v[32:35]\n"
"v_fma_f32 v221,v221,v212,v218\n"
"v_fma_f32 v222,v222,v212,v218\n"
MF "v[32:35],a[68:71],v[112:115],v[32:35]\n"
"v_fma_f32 v223,v223,v212,v218\n"
"v_fma_f32 v224,v224,v212,v218\n"
MF "v[32:35],a[108:111],v[116:119],v[32:35]\n"
"v_fma_f32 v225,v225,v212,v218\n"
"v_fma_f32 v226,v226,v212,v218\n"
MF "v[36:39],a[108:111],v[140:143],v[36:39]\n"
"v_fma_f32 v227,v227,v212,v218\n"
CV "v220,v220,v221\n"
MF "v[36:39],a[68:71],v[136:139],v[36:39]\n"
CV "v221,v222,v223\n"
CV "v222,v224,v225\n"
MF "v[36:39],a[28:31],v[132:135],v[36:39]\n"
CV "v223,v226,v227\n"
AW "a16,v220\n"
MF "v[36:39],a[88:91],v[128:131],v[36:39]\n"
AW "a17,v221\n"
AW "a18,v222\n"
MF "v[36:39],a[48:51],v[124:127],v[36:39]\n"
AW "a19,v223\n"
AR "v228,a56\n"
MF "v[36:39],a[8:11],v[120:123],v[36:39]\n"
AR "v229,a57\n"
AR "v230,a58\n"
MF "v[72:75],a[8:11],v[144:147],v[72:75]\n"
AR "v231,a59\n"
AR "v232,a76\n"
MF "v[72:75],a[48:51],v[148:151],v[72:75]\n"
AR "v233,a77\n"
AR "v234,a78\n"
MF "v[72:75],a[88:91],v[152:155],v[72:75]\n"
AR "v235,a79\n"
"v_fma_f32 v228,v228,v212,v218\n"
MF "v[72:75],a[28:31],v[156:159],v[72:75]\n"
"ds_read_b128 a[76:79],v236 offset:61440\n"
"v_fma_f32 v229,v229,v212,v218\n"
MF "v[72:75],a[68:71],v[160:163],v[72:75]\n"
"v_fma_f32 v230,v230,v212,v218\n"
"v_fma_f32 v231,v231,v212,v218\n"
MF "v[72:75],a[108:111],v[164:167],v[72:75]\n"
"v_fma_f32 v232,v232,v212,v218\n"
"v_fma_f32 v233,v233,v212,v218\n"
MF "v[76:79],a[108:111],v[238:241],v[76:79]\n"
"v_fma_f32 v234,v234,v212,v218\n"
"v_fma_f32 v235,v235,v212,v218\n"
MF "v[76:79],a[68:71],v[248:251],v[76:79]\n"
CV "v228,v228,v229\n"
CV "v229,v230,v231\n"
MF "v[76:79],a[28:31],v[244:247],v[76:79]\n"
CV "v230,v232,v233\n"
CV "v231,v234,v235\n"
MF "v[76:79],a[88:91],v[204:207],v[76:79]\n"
AW "a56,v228\n"
AW "a57,v229\n"
MF "v[76:79],a[48:51],v[172:175],v[76:79]\n"
AW "a58,v230\n"
AW "a59,v231\n"
MF "v[76:79],a[8:11],v[168:171],v[76:79]\n"
"s_waitcnt lgkmcnt(2)\n"
AR "v220,a96\n"
AR "v221,a97\n"
MF "v[84:87],a[12:15],v[168:171],v[84:87]\n"
AR "v222,a98\n"
AR "v223,a99\n"
MF "v[84:87],a[52:55],v[172:175],v[84:87]\n"
AR "v224,a116\n"
AR "v225,a117\n"
MF "v[84:87],a[92:95],v[204:207],v[84:87]\n"
AR "v226,a118\n"
AR "v227,a119\n"
MF "v[84:87],a[32:35],v[244:247],v[84:87]\n"
"ds_read_b128 a[116:119],v237 offset:16384\n"
"v_fma_f32 v220,v220,v212,v218\n"
MF "v[84:87],a[72:75],v[248:251],v[84:87]\n"
"v_fma_f32 v221,v221,v212,v218\n"
"v_fma_f32 v222,v222,v212,v218\n"
MF "v[84:87],a[112:115],v[238:241],v[84:87]\n"
"v_fma_f32 v223,v223,v212,v218\n"
"v_fma_f32 v224,v224,v212,v218\n"
MF "v[80:83],a[112:115],v[164:167],v[80:83]\n"
"v_fma_f32 v225,v225,v212,v218\n"
"v_fma_f32 v226,v226,v212,v218\n"
MF "v[80:83],a[72:75],v[160:163],v[80:83]\n"
"v_fma_f32 v227,v227,v212,v218\n"
CV "v220,v220,v221\n"
MF "v[80:83],a[32:35],v[156:159],v[80:83]\n"
CV "v221,v222,v223\n"
CV "v222,v224,v225\n"
MF "v[80:83],a[92:95],v[152:155],v[80:83]\n"
CV "v223,v226,v227\n"
AW "a96,v220\n"
MF "v[80:83],a[52:55],v[148:151],v[80:83]\n"
AW "a97,v221\n"
AW "a98,v222\n"
MF "v[80:83],a[12:15],v[144:147],v[80:83]\n"
AW "a99,v223\n"
"v_mov_b32 v15,0\n"
MF "v[44:47],a[12:15],v[120:123],v[44:47]\n"
"v_lshrrev_b32 v242,5,v9\n"
"v_min_u32 v242,23,v242\n"
MF "v[44:47],a[52:55],v[124:127],v[44:47]\n"
"v_mul_u32_u24 v242,160,v242\n"
"v_and_or_b32 v242,v9,16,v242\n"
MF "v[44:47],a[92:95],v[128:131],v[44:47]\n"
"v_lshlrev_b32 v242,3,v242\n"
AR "v8,a252\n"
MF "v[44:47],a[32:35],v[132:135],v[44:47]\n"
AR "v11,a253\n"
"v_bfe_u32 v224,v9,5,3\n"
MF "v[44:47],a[72:75],v[136:139],v[44:47]\n"
"s_add_u32 s62,s74,s68\n"
"s_add_u32 s68,s68,256\n"
"s_and_b32 s68,s68,768\n"
MF "v[44:47],a[112:115],v[140:143],v[44:47]\n"
"buffer_load_dword v243,v242,s[48:51],s62 offen\n"
MF "v[40:43],a[112:115],v[116:119],v[40:43]\n"
"v_bfe_u32 v225,v9,8,2\n"
"v_lshlrev_b32 v225,1,v225\n"
MF "v[40:43],a[72:75],v[112:115],v[40:43]\n"
"v_mov_b32 v14,s76\n"
"s_add_u32 s76,s76,1\n"
MF "v[40:43],a[32:35],v[108:111],v[40:43]\n"
"buffer_store_dword v14,v15,s[40:43],s94 offen sc1\n"
MF "v[40:43],a[92:95],v[104:107],v[40:43]\n"
"v_and_b32 v226,16,v9\n"
"v_lshlrev_b32_e64 v226,v226,s9\n"
MF "v[40:43],a[52:55],v[100:103],v[40:43]\n"
"v_sub_u32 v224,v224,v225\n"
"v_cmp_eq_u32 vcc,0,v224\n"
MF "v[40:43],a[12:15],v[96:99],v[40:43]\n"
"s_waitcnt lgkmcnt(0)\n"
"v_cndmask_b32 v220,0,v226,vcc\n"
"v_cmp_eq_u32 vcc,1,v224\n"
MF "v[48:51],a[16:19],v[96:99],v[48:51]\n"
"s_nop 0\n"
"v_cndmask_b32 v221,0,v226,vcc\n"
AW "a248,0\n"
MF "v[48:51],a[56:59],v[100:103],v[48:51]\n"
AW "a249,0\n"
AW "a250,v220\n"
MF "v[48:51],a[96:99],v[104:107],v[48:51]\n"
AW "a251,v221\n"
AW "a252,v220\n"
MF "v[48:51],a[36:39],v[108:111],v[48:51]\n"
AW "a253,v221\n"
AW "a254,0\n"
MF "v[48:51],a[76:79],v[112:115],v[48:51]\n"
AW "a255,0\n"
"v_bfe_u32 v12,v9,4,4\n"
MF "v[48:51],a[116:119],v[116:119],v[48:51]\n"
"v_lshlrev_b32 v12,2,v12\n"
"v_lshrrev_b32 v13,2,v9\n"
MF "v[52:55],a[116:119],v[140:143],v[52:55]\n"
"v_add_u32 v13,s58,v13\n"
"v_add_u32 v4,24576,v4\n"
MF "v[52:55],a[76:79],v[136:139],v[52:55]\n"
"s_waitcnt vmcnt(2) lgkmcnt(0)\n"
"v_add_u32 v5,24576,v5\n"
"v_add_u32 v6,24576,v6\n"
MF "v[52:55],a[36:39],v[132:135],v[52:55]\n"
"s_barrier\n"
MF "v[52:55],a[96:99],v[128:131],v[52:55]\n"
TR "v[176:177],v0 offset:24576\n"
"v_add_u32 v7,24576,v7\n"
MF "v[52:55],a[56:59],v[124:127],v[52:55]\n"
TR "v[178:179],v0 offset:26624\n"
MF "v[52:55],a[16:19],v[120:123],v[52:55]\n"
MF "v[88:91],a[16:19],v[144:147],v[88:91]\n"
TR "v[180:181],v0 offset:28672\n"
MF "v[88:91],a[56:59],v[148:151],v[88:91]\n"
TR "v[182:183],v0 offset:30720\n"
MF "v[88:91],a[96:99],v[152:155],v[88:91]\n"
MF "v[88:91],a[36:39],v[156:159],v[88:91]\n"
TR "v[184:185],v0 offset:32768\n"
MF "v[88:91],a[76:79],v[160:163],v[88:91]\n"
TR "v[186:187],v0 offset:34816\n"
MF "v[88:91],a[116:119],v[164:167],v[88:91]\n"
MF "v[92:95],a[116:119],v[238:241],v[92:95]\n"
MF "v[92:95],a[76:79],v[248:251],v[92:95]\n"
MF "v[92:95],a[36:39],v[244:247],v[92:95]\n"
MF "v[92:95],a[96:99],v[204:207],v[92:95]\n"
MF "v[92:95],a[56:59],v[172:175],v[92:95]\n"
MF "v[92:95],a[16:19],v[168:171],v[92:95]\n"
"s_waitcnt lgkmcnt(0)\n"
".Lpl15:\n"
MF "v[96:99],a[0:3],v[176:179],v[252:255]\n"
TR "v[188:189],v1 offset:24576\n"
MF "v[96:99],a[40:43],v[180:183],v[96:99]\n"
TR "v[190:191],v1 offset:26624\n"
MF "v[96:99],a[80:83],v[184:187],v[96:99]\n"
MF "v[104:107],a[84:87],v[184:187],v[252:255]\n"
TR "a[240:241],v1 offset:28672\n"
MF "v[104:107],a[44:47],v[180:183],v[104:107]\n"
TR "a[242:243],v1 offset:30720\n"
MF "v[104:107],a[4:7],v[176:179],v[104:107]\n"
MF "v[112:115],a[8:11],v[176:179],v[252:255]\n"
TR "a[244:245],v1 offset:32768\n"
MF "v[112:115],a[48:51],v[180:183],v[112:115]\n"
TR "a[246:247],v1 offset:34816\n"
MF "v[112:115],a[88:91],v[184:187],v[112:115]\n"
MF "v[120:123],a[92:95],v[184:187],v[252:255]\n"
MF "v[120:123],a[52:55],v[180:183],v[120:123]\n"
MF "v[120:123],a[12:15],v[176:179],v[120:123]\n"
MF "v[128:131],a[16:19],v[176:179],v[252:255]\n"
MF "v[128:131],a[56:59],v[180:183],v[128:131]\n"
MF "v[128:131],a[96:99],v[184:187],v[128:131]\n"
"s_waitcnt lgkmcnt(0)\n"
MF "v[132:135],a[96:99],a[244:247],v[252:255]\n"
TR "v[176:177],v2 offset:24576\n"
MF "v[132:135],a[56:59],a[240:243],v[132:135]\n"
TR "v[178:179],v2 offset:26624\n"
MF "v[132:135],a[16:19],v[188:191],v[132:135]\n"
MF "v[124:127],a[12:15],v[188:191],v[252:255]\n"
TR "v[180:181],v2 offset:28672\n"
MF "v[124:127],a[52:55],a[240:243],v[124:127]\n"
TR "v[182:183],v2 offset:30720\n"
MF "v[124:127],a[92:95],a[244:247],v[124:127]\n"
MF "v[116:119],a[88:91],a[244:247],v[252:255]\n"
TR "v[184:185],v2 offset:32768\n"
MF "v[116:119],a[48:51],a[240:243],v[116:119]\n"
TR "v[186:187],v2 offset:34816\n"
MF "v[116:119],a[8:11],v[188:191],v[116:119]\n"
MF "v[108:111],a[4:7],v[188:191],v[252:255]\n"
MF "v[108:111],a[44:47],a[240:243],v[108:111]\n"
MF "v[108:111],a[84:87],a[244:247],v[108:111]\n"
MF "v[100:103],a[80:83],a[244:247],v[252:255]\n"
MF "v[100:103],a[40:43],a[240:243],v[100:103]\n"
MF "v[100:103],a[0:3],v[188:191],v[100:103]\n"
"s_waitcnt lgkmcnt(0)\n"
MF "v[136:139],a[0:3],v[176:179],v[252:255]\n"
TR "v[188:189],v3 offset:24576\n"
MF "v[136:139],a[40:43],v[180:183],v[136:139]\n"
TR "v[190:191],v3 offset:26624\n"
MF "v[136:139],a[80:83],v[184:187],v[136:139]\n"
MF "v[144:147],a[84:87],v[184:187],v[252:255]\n"
TR "a[240:241],v3 offset:28672\n"
MF "v[144:147],a[44:47],v[180:183],v[144:147]\n"
TR "a[242:243],v3 offset:30720\n"
MF "v[144:147],a[4:7],v[176:179],v[144:147]\n"
MF "v[152:155],a[8:11],v[176:179],v[252:255]\n"
TR "a[244:245],v3 offset:32768\n"
MF "v[152:155],a[48:51],v[180:183],v[152:155]\n"
TR "a[246:247],v3 offset:34816\n"
MF "v[152:155],a[88:91],v[184:187],v[152:155]\n"
MF "v[160:163],a[92:95],v[184:187],v[252:255]\n"
MF "v[160:163],a[52:55],v[180:183],v[160:163]\n"
MF "v[160:163],a[12:15],v[176:179],v[160:163]\n"
MF "v[168:171],a[16:19],v[176:179],v[252:255]\n"
MF "v[168:171],a[56:59],v[180:183],v[168:171]\n"
"s_waitcnt lgkmcnt(0)\n"
MF "v[168:171],a[96:99],v[184:187],v[168:171]\n"
"s_barrier\n"
MF "v[172:175],a[96:99],a[244:247],v[252:255]\n"
TR "v[176:177],v0\n"
MF "v[172:175],a[56:59],a[240:243],v[172:175]\n"
TR "v[178:179],v0 offset:2048\n"
MF "v[172:175],a[16:19],v[188:191],v[172:175]\n"
"ds_read2st64_b32 v[192:193],v13 offset0:192 offset1:193\n"
MF "v[164:167],a[12:15],v[188:191],v[252:255]\n"
TR "v[180:181],v0 offset:4096\n"
MF "v[164:167],a[52:55],a[240:243],v[164:167]\n"
TR "v[182:183],v0 offset:6144\n"
MF "v[164:167],a[92:95],a[244:247],v[164:167]\n"
"ds_read2st64_b32 v[194:195],v13 offset0:194 offset1:195\n"
MF "v[156:159],a[88:91],a[244:247],v[252:255]\n"
TR "v[184:185],v0 offset:8192\n"
MF "v[156:159],a[48:51],a[240:243],v[156:159]\n"
TR "v[186:187],v0 offset:10240\n"
MF "v[156:159],a[8:11],v[188:191],v[156:159]\n"
"ds_read2st64_b32 v[196:197],v13 offset0:196 offset1:197\n"
MF "v[148:151],a[4:7],v[188:191],v[252:255]\n"
"ds_read2st64_b32 v[198:199],v13 offset0:198 offset1:199\n"
MF "v[148:151],a[44:47],a[240:243],v[148:151]\n"
MF "v[148:151],a[84:87],a[244:247],v[148:151]\n"
MF "v[140:143],a[80:83],a[244:247],v[252:255]\n"
MF "v[140:143],a[40:43],a[240:243],v[140:143]\n"
MF "v[140:143],a[0:3],v[188:191],v[140:143]\n"
"s_waitcnt lgkmcnt(2)\n"
MF "v[96:99],a[120:123],v[176:179],v[96:99]\n"
"s_waitcnt lgkmcnt(0)\n"
TR "v[188:189],v1\n"
"v_add_f32 v16,v16,v192\n"
MF "v[96:99],a[160:163],v[180:183],v[96:99]\n"
TR "v[190:191],v1 offset:2048\n"
"v_add_f32 v17,v17,v193\n"
MF "v[96:99],a[200:203],v[184:187],v[96:99]\n"
"ds_read2st64_b32 v[200:201],v13 offset0:200 offset1:201\n"
"v_add_f32 v18,v18,v194\n"
"s_waitcnt vmcnt(0)\n"
"v_mov_b32 v14,s76\n"
"s_add_u32 s76,s76,1\n"
MF "v[104:107],a[204:207],v[184:187],v[104:107]\n"
TR "a[240:241],v1 offset:4096\n"
"v_add_f32 v19,v19,v195\n"
"buffer_store_dword v14,v15,s[40:43],s94 offen sc1\n"
MF "v[104:107],a[164:167],v[180:183],v[104:107]\n"
TR "a[242:243],v1 offset:6144\n"
"v_add_f32 v20,v20,v196\n"
MF "v[104:107],a[124:127],v[176:179],v[104:107]\n"
"ds_read2st64_b32 v[202:203],v13 offset0:202 offset1:203\n"
"v_add_f32 v21,v21,v197\n"
MF "v[112:115],a[128:131],v[176:179],v[112:115]\n"
TR "a[244:245],v1 offset:8192\n"
"v_add_f32 v22,v22,v198\n"
MF "v[112:115],a[168:171],v[180:183],v[112:115]\n"
TR "a[246:247],v1 offset:10240\n"
"v_add_f32 v23,v23,v199\n"
MF "v[112:115],a[208:211],v[184:187],v[112:115]\n"
"ds_read2st64_b32 v[204:205],v13 offset0:204 offset1:205\n"
CV "v16,v16,v17\n"
MF "v[120:123],a[212:215],v[184:187],v[120:123]\n"
"ds_read2st64_b32 v[206:207],v13 offset0:206 offset1:207\n"
CV "v17,v18,v19\n"
MF "v[120:123],a[172:175],v[180:183],v[120:123]\n"
"s_add_u32 s87,s90,0\n"
CV "v18,v20,v21\n"
CV "v19,v22,v23\n"
MF "v[120:123],a[132:135],v[176:179],v[120:123]\n"
MF "v[128:131],a[136:139],v[176:179],v[128:131]\n"
"buffer_store_dwordx4 v[16:19],v9,s[40:43],s87 offen sc1\n"
MF "v[128:131],a[176:179],v[180:183],v[128:131]\n"
MF "v[128:131],a[216:219],v[184:187],v[128:131]\n"
"s_waitcnt lgkmcnt(2)\n"
MF "v[132:135],a[216:219],a[244:247],v[132:135]\n"
"s_waitcnt lgkmcnt(0)\n"
TR "v[176:177],v2\n"
"v_add_f32 v24,v24,v200\n"
MF "v[132:135],a[176:179],a[240:243],v[132:135]\n"
TR "v[178:179],v2 offset:2048\n"
"v_add_f32 v25,v25,v201\n"
MF "v[132:135],a[136:139],v[188:191],v[132:135]\n"
"ds_read2st64_b32 v[192:193],v13 offset0:208 offset1:209\n"
"v_add_f32 v26,v26,v202\n"
MF "v[124:127],a[132:135],v[188:191],v[124:127]\n"
TR "v[180:181],v2 offset:4096\n"
"v_add_f32 v27,v27,v203\n"
MF "v[124:127],a[172:175],a[240:243],v[124:127]\n"
TR "v[182:183],v2 offset:6144\n"
"v_add_f32 v28,v28,v204\n"
MF "v[124:127],a[212:215],a[244:247],v[124:127]\n"
"ds_read2st64_b32 v[194:195],v13 offset0:210 offset1:211\n"
"v_add_f32 v29,v29,v205\n"
MF "v[116:119],a[208:211],a[244:247],v[116:119]\n"
TR "v[184:185],v2 offset:8192\n"
"v_add_f32 v30,v30,v206\n"
MF "v[116:119],a[168:171],a[240:243],v[116:119]\n"
TR "v[186:187],v2 offset:10240\n"
"v_add_f32 v31,v31,v207\n"
MF "v[116:119],a[128:131],v[188:191],v[116:119]\n"
"ds_read2st64_b32 v[196:197],v13 offset0:212 offset1:213\n"
CV "v24,v24,v25\n"
MF "v[108:111],a[124:127],v[188:191],v[108:111]\n"
"ds_read2st64_b32 v[198:199],v13 offset0:214 offset1:215\n"
CV "v25,v26,v27\n"
MF "v[108:111],a[164:167],a[240:243],v[108:111]\n"
".Lpg30h:\n"
"s_cbranch_execz .Lpg34h\n"
"s_add_u32 s87,s90,1024\n"
CV "v26,v28,v29\n"
CV "v27,v30,v31\n"
MF "v[108:111],a[204:207],a[244:247],v[108:111]\n"
MF "v[100:103],a[200:203],a[244:247],v[100:103]\n"
"buffer_store_dwordx4 v[24:27],v9,s[40:43],s87 offen sc1\n"
MF "v[100:103],a[160:163],a[240:243],v[100:103]\n"
MF "v[100:103],a[120:123],v[188:191],v[100:103]\n"
"s_waitcnt lgkmcnt(2)\n"
MF "v[136:139],a[120:123],v[176:179],v[136:139]\n"
"s_waitcnt lgkmcnt(0)\n"
TR "v[188:189],v3\n"
"v_add_f32 v32,v32,v192\n"
MF "v[136:139],a[160:163],v[180:183],v[136:139]\n"
TR "v[190:191],v3 offset:2048\n"
"v_add_f32 v33,v33,v193\n"
MF "v[136:139],a[200:203],v[184:187],v[136:139]\n"
"ds_read2st64_b32 v[200:201],v13 offset0:216 offset1:217\n"
"v_add_f32 v34,v34,v194\n"
MF "v[144:147],a[204:207],v[184:187],v[144:147]\n"
TR "a[240:241],v3 offset:4096\n"
"v_add_f32 v35,v35,v195\n"
MF "v[144:147],a[164:167],v[180:183],v[144:147]\n"
TR "a[242:243],v3 offset:6144\n"
"v_add_f32 v36,v36,v196\n"
MF "v[144:147],a[124:127],v[176:179],v[144:147]\n"
"ds_read2st64_b32 v[202:203],v13 offset0:218 offset1:219\n"
"v_add_f32 v37,v37,v197\n"
MF "v[152:155],a[128:131],v[176:179],v[152:155]\n"
TR "a[244:245],v3 offset:8192\n"
"v_add_f32 v38,v38,v198\n"
MF "v[152:155],a[168:171],v[180:183],v[152:155]\n"
TR "a[246:247],v3 offset:10240\n"
"v_add_f32 v39,v39,v199\n"
MF "v[152:155],a[208:211],v[184:187],v[152:155]\n"
"ds_read2st64_b32 v[204:205],v13 offset0:220 offset1:221\n"
CV "v32,v32,v33\n"
MF "v[160:163],a[212:215],v[184:187],v[160:163]\n"
"ds_read2st64_b32 v[206:207],v13 offset0:222 offset1:223\n"
CV "v33,v34,v35\n"
MF "v[160:163],a[172:175],v[180:183],v[160:163]\n"
"s_add_u32 s87,s90,2048\n"
CV "v34,v36,v37\n"
CV "v35,v38,v39\n"
MF "v[160:163],a[132:135],v[176:179],v[160:163]\n"
MF "v[168:171],a[136:139],v[176:179],v[168:171]\n"
"buffer_store_dwordx4 v[32:35],v9,s[40:43],s87 offen sc1\n"
MF "v[168:171],a[176:179],v[180:183],v[168:171]\n"
"s_waitcnt lgkmcnt(0)\n"
MF "v[168:171],a[216:219],v[184:187],v[168:171]\n"
"s_barrier\n"
MF "v[172:175],a[216:219],a[244:247],v[172:175]\n"
TR "v[176:177],v0 offset:49152\n"
"v_add_f32 v40,v40,v200\n"
MF "v[172:175],a[176:179],a[240:243],v[172:175]\n"
TR "v[178:179],v0 offset:51200\n"
"v_add_f32 v41,v41,v201\n"
MF "v[172:175],a[136:139],v[188:191],v[172:175]\n"
"ds_read2st64_b32 v[192:193],v13 offset0:224 offset1:225\n"
"v_add_f32 v42,v42,v202\n"
MF "v[164:167],a[132:135],v[188:191],v[164:167]\n"
TR "v[180:181],v0 offset:53248\n"
"v_add_f32 v43,v43,v203\n"
MF "v[164:167],a[172:175],a[240:243],v[164:167]\n"
TR "v[182:183],v0 offset:55296\n"
"v_add_f32 v44,v44,v204\n"
MF "v[164:167],a[212:215],a[244:247],v[164:167]\n"
"ds_read2st64_b32 v[194:195],v13 offset0:226 offset1:227\n"
"v_add_f32 v45,v45,v205\n"
MF "v[156:159],a[208:211],a[244:247],v[156:159]\n"
TR "v[184:185],v0 offset:57344\n"
"v_add_f32 v46,v46,v206\n"
MF "v[156:159],a[168:171],a[240:243],v[156:159]\n"
TR "v[186:187],v0 offset:59392\n"
"v_add_f32 v47,v47,v207\n"
MF "v[156:159],a[128:131],v[188:191],v[156:159]\n"
"ds_read2st64_b32 v[196:197],v13 offset0:228 offset1:229\n"
CV "v40,v40,v41\n"
MF "v[148:151],a[124:127],v[188:191],v[148:151]\n"
"ds_read2st64_b32 v[198:199],v13 offset0:230 offset1:231\n"
CV "v41,v42,v43\n"
MF "v[148:151],a[164:167],a[240:243],v[148:151]\n"
"s_add_u32 s87,s90,3072\n"
CV "v42,v44,v45\n"
CV "v43,v46,v47\n"
MF "v[148:151],a[204:207],a[244:247],v[148:151]\n"
MF "v[140:143],a[200:203],a[244:247],v[140:143]\n"
"buffer_store_dwordx4 v[40:43],v9,s[40:43],s87 offen sc1\n"
MF "v[140:143],a[160:163],a[240:243],v[140:143]\n"
MF "v[140:143],a[120:123],v[188:191],v[140:143]\n"
"s_waitcnt lgkmcnt(2)\n"
MF "v[96:99],a[20:23],v[176:179],v[96:99]\n"
"s_waitcnt lgkmcnt(0)\n"
TR "v[188:189],v1 offset:49152\n"
"v_add_f32 v48,v48,v192\n"
MF "v[96:99],a[60:63],v[180:183],v[96:99]\n"
TR "v[190:191],v1 offset:51200\n"
"v_add_f32 v49,v49,v193\n"
MF "v[96:99],a[100:103],v[184:187],v[96:99]\n"
"ds_read2st64_b32 v[200:201],v13 offset0:0 offset1:1\n"
"v_add_f32 v50,v50,v194\n"
MF "v[104:107],a[104:107],v[184:187],v[104:107]\n"
TR "a[240:241],v1 offset:53248\n"
"v_add_f32 v51,v51,v195\n"
MF "v[104:107],a[64:67],v[180:183],v[104:107]\n"
TR "a[242:243],v1 offset:55296\n"
"v_add_f32 v52,v52,v196\n"
MF "v[104:107],a[24:27],v[176:179],v[104:107]\n"
"ds_read2st64_b32 v[202:203],v13 offset0:2 offset1:3\n"
"v_add_f32 v53,v53,v197\n"
MF "v[112:115],a[28:31],v[176:179],v[112:115]\n"
TR "a[244:245],v1 offset:57344\n"
"v_add_f32 v54,v54,v198\n"
MF "v[112:115],a[68:71],v[180:183],v[112:115]\n"
TR "a[246:247],v1 offset:59392\n"
"v_add_f32 v55,v55,v199\n"
MF "v[112:115],a[108:111],v[184:187],v[112:115]\n"
"ds_read2st64_b32 v[204:205],v13 offset0:4 offset1:5\n"
CV "v48,v48,v49\n"
MF "v[120:123],a[112:115],v[184:187],v[120:123]\n"
"ds_read2st64_b32 v[206:207],v13 offset0:6 offset1:7\n"
CV "v49,v50,v51\n"
MF "v[120:123],a[72:75],v[180:183],v[120:123]\n"
"s_add_u32 s87,s90,4096\n"
CV "v50,v52,v53\n"
CV "v51,v54,v55\n"
MF "v[120:123],a[32:35],v[176:179],v[120:123]\n"
MF "v[128:131],a[36:39],v[176:179],v[128:131]\n"
"buffer_store_dwordx4 v[48:51],v9,s[40:43],s87 offen sc1\n"
MF "v[128:131],a[76:79],v[180:183],v[128:131]\n"
MF "v[128:131],a[116:119],v[184:187],v[128:131]\n"
"s_waitcnt lgkmcnt(2)\n"
MF "v[132:135],a[116:119],a[244:247],v[132:135]\n"
"s_waitcnt lgkmcnt(0)\n"
TR "v[176:177],v2 offset:49152\n"
"v_add_f32 v56,v56,v200\n"
MF "v[132:135],a[76:79],a[240:243],v[132:135]\n"
TR "v[178:179],v2 offset:51200\n"
"v_add_f32 v57,v57,v201\n"
MF "v[132:135],a[36:39],v[188:191],v[132:135]\n"
"ds_read2st64_b32 v[192:193],v13 offset0:8 offset1:9\n"
"v_add_f32 v58,v58,v202\n"
MF "v[124:127],a[32:35],v[188:191],v[124:127]\n"
TR "v[180:181],v2 offset:53248\n"
"v_add_f32 v59,v59,v203\n"
MF "v[124:127],a[72:75],a[240:243],v[124:127]\n"
TR "v[182:183],v2 offset:55296\n"
"v_add_f32 v60,v60,v204\n"
MF "v[124:127],a[112:115],a[244:247],v[124:127]\n"
"ds_read2st64_b32 v[194:195],v13 offset0:10 offset1:11\n"
"v_add_f32 v61,v61,v205\n"
MF "v[116:119],a[108:111],a[244:247],v[116:119]\n"
TR "v[184:185],v2 offset:57344\n"
"v_add_f32 v62,v62,v206\n"
MF "v[116:119],a[68:71],a[240:243],v[116:119]\n"
TR "v[186:187],v2 offset:59392\n"
"v_add_f32 v63,v63,v207\n"
MF "v[116:119],a[28:31],v[188:191],v[116:119]\n"
"ds_read2st64_b32 v[196:197],v13 offset0:12 offset1:13\n"
CV "v56,v56,v57\n"
MF "v[108:111],a[24:27],v[188:191],v[108:111]\n"
"ds_read2st64_b32 v[198:199],v13 offset0:14 offset1:15\n"
CV "v57,v58,v59\n"
MF "v[108:111],a[64:67],a[240:243],v[108:111]\n"
"s_add_u32 s87,s90,10240\n"
CV "v58,v60,v61\n"
CV "v59,v62,v63\n"
MF "v[108:111],a[104:107],a[244:247],v[108:111]\n"
MF "v[100:103],a[100:103],a[244:247],v[100:103]\n"
"buffer_store_dwordx4 v[56:59],v9,s[40:43],s87 offen sc1\n"
MF "v[100:103],a[60:63],a[240:243],v[100:103]\n"
MF "v[100:103],a[20:23],v[188:191],v[100:103]\n"
"s_waitcnt lgkmcnt(2)\n"
MF "v[136:139],a[20:23],v[176:179],v[136:139]\n"
"s_waitcnt lgkmcnt(0)\n"
TR "v[188:189],v3 offset:49152\n"
"v_add_f32 v64,v64,v192\n"
MF "v[136:139],a[60:63],v[180:183],v[136:139]\n"
TR "v[190:191],v3 offset:51200\n"
"v_add_f32 v65,v65,v193\n"
MF "v[136:139],a[100:103],v[184:187],v[136:139]\n"
"ds_read2st64_b32 v[200:201],v13 offset0:16 offset1:17\n"
"v_add_f32 v66,v66,v194\n"
MF "v[144:147],a[104:107],v[184:187],v[144:147]\n"
TR "a[240:241],v3 offset:53248\n"
"v_add_f32 v67,v67,v195\n"
MF "v[144:147],a[64:67],v[180:183],v[144:147]\n"
TR "a[242:243],v3 offset:55296\n"
"v_add_f32 v68,v68,v196\n"
MF "v[144:147],a[24:27],v[176:179],v[144:147]\n"
"ds_read2st64_b32 v[202:203],v13 offset0:18 offset1:19\n"
"v_add_f32 v69,v69,v197\n"
MF "v[152:155],a[28:31],v[176:179],v[152:155]\n"
TR "a[244:245],v3 offset:57344\n"
"v_add_f32 v70,v70,v198\n"
MF "v[152:155],a[68:71],v[180:183],v[152:155]\n"
TR "a[246:247],v3 offset:59392\n"
"v_add_f32 v71,v71,v199\n"
MF "v[152:155],a[108:111],v[184:187],v[152:155]\n"
"ds_read2st64_b32 v[204:205],v13 offset0:20 offset1:21\n"
CV "v64,v64,v65\n"
MF "v[160:163],a[112:115],v[184:187],v[160:163]\n"
"ds_read2st64_b32 v[206:207],v13 offset0:22 offset1:23\n"
CV "v65,v66,v67\n"
MF "v[160:163],a[72:75],v[180:183],v[160:163]\n"
"s_add_u32 s87,s90,11264\n"
CV "v66,v68,v69\n"
CV "v67,v70,v71\n"
MF "v[160:163],a[32:35],v[176:179],v[160:163]\n"
MF "v[168:171],a[36:39],v[176:179],v[168:171]\n"
"buffer_store_dwordx4 v[64:67],v9,s[40:43],s87 offen sc1\n"
MF "v[168:171],a[76:79],v[180:183],v[168:171]\n"
"s_waitcnt lgkmcnt(0)\n"
MF "v[168:171],a[116:119],v[184:187],v[168:171]\n"
"s_barrier\n"
MF "v[172:175],a[116:119],a[244:247],v[172:175]\n"
TR "v[176:177],v4\n"
"v_add_f32 v72,v72,v200\n"
MF "v[172:175],a[76:79],a[240:243],v[172:175]\n"
TR "v[178:179],v4 offset:2048\n"
"v_add_f32 v73,v73,v201\n"
MF "v[172:175],a[36:39],v[188:191],v[172:175]\n"
"ds_read2st64_b32 v[192:193],v13 offset0:24 offset1:25\n"
"v_add_f32 v74,v74,v202\n"
MF "v[164:167],a[32:35],v[188:191],v[164:167]\n"
TR "v[180:181],v4 offset:4096\n"
"v_add_f32 v75,v75,v203\n"
MF "v[164:167],a[72:75],a[240:243],v[164:167]\n"
TR "v[182:183],v4 offset:6144\n"
"v_add_f32 v76,v76,v204\n"
MF "v[164:167],a[112:115],a[244:247],v[164:167]\n"
"ds_read2st64_b32 v[194:195],v13 offset0:26 offset1:27\n"
"v_add_f32 v77,v77,v205\n"
MF "v[156:159],a[108:111],a[244:247],v[156:159]\n"
TR "v[184:185],v4 offset:8192\n"
"v_add_f32 v78,v78,v206\n"
MF "v[156:159],a[68:71],a[240:243],v[156:159]\n"
TR "v[186:187],v4 offset:10240\n"
"v_add_f32 v79,v79,v207\n"
MF "v[156:159],a[28:31],v[188:191],v[156:159]\n"
"ds_read2st64_b32 v[196:197],v13 offset0:28 offset1:29\n"
CV "v72,v72,v73\n"
MF "v[148:151],a[24:27],v[188:191],v[148:151]\n"
"ds_read2st64_b32 v[198:199],v13 offset0:30 offset1:31\n"
CV "v73,v74,v75\n"
MF "v[148:151],a[64:67],a[240:243],v[148:151]\n"
"s_add_u32 s87,s90,12288\n"
CV "v74,v76,v77\n"
CV "v75,v78,v79\n"
MF "v[148:151],a[104:107],a[244:247],v[148:151]\n"
MF "v[140:143],a[100:103],a[244:247],v[140:143]\n"
"buffer_store_dwordx4 v[72:75],v9,s[40:43],s87 offen sc1\n"
MF "v[140:143],a[60:63],a[240:243],v[140:143]\n"
MF "v[140:143],a[20:23],v[188:191],v[140:143]\n"
"s_waitcnt lgkmcnt(2)\n"
MF "v[96:99],a[140:143],v[176:179],v[96:99]\n"
"s_waitcnt lgkmcnt(0)\n"
TR "v[188:189],v5\n"
"v_add_f32 v80,v80,v192\n"
MF "v[96:99],a[180:183],v[180:183],v[96:99]\n"
TR "v[190:191],v5 offset:2048\n"
"v_add_f32 v81,v81,v193\n"
MF "v[96:99],a[220:223],v[184:187],v[96:99]\n"
"ds_read2st64_b32 v[200:201],v13 offset0:32 offset1:33\n"
"v_add_f32 v82,v82,v194\n"
MF "v[104:107],a[224:227],v[184:187],v[104:107]\n"
TR "a[240:241],v5 offset:4096\n"
"v_add_f32 v83,v83,v195\n"
MF "v[104:107],a[184:187],v[180:183],v[104:107]\n"
TR "a[242:243],v5 offset:6144\n"
"v_add_f32 v84,v84,v196\n"
MF "v[104:107],a[144:147],v[176:179],v[104:107]\n"
"ds_read2st64_b32 v[202:203],v13 offset0:34 offset1:35\n"
"v_add_f32 v85,v85,v197\n"
MF "v[112:115],a[148:151],v[176:179],v[112:115]\n"
TR "a[244:245],v5 offset:8192\n"
"v_add_f32 v86,v86,v198\n"
MF "v[112:115],a[188:191],v[180:183],v[112:115]\n"
TR "a[246:247],v5 offset:10240\n"
"v_add_f32 v87,v87,v199\n"
MF "v[112:115],a[228:231],v[184:187],v[112:115]\n"
"ds_read2st64_b32 v[204:205],v13 offset0:36 offset1:37\n"
CV "v80,v80,v81\n"
MF "v[120:123],a[232:235],v[184:187],v[120:123]\n"
"ds_read2st64_b32 v[206:207],v13 offset0:38 offset1:39\n"
CV "v81,v82,v83\n"
MF "v[120:123],a[192:195],v[180:183],v[120:123]\n"
"s_add_u32 s87,s90,13312\n"
CV "v82,v84,v85\n"
CV "v83,v86,v87\n"
MF "v[120:123],a[152:155],v[176:179],v[120:123]\n"
MF "v[128:131],a[156:159],v[176:179],v[128:131]\n"
"buffer_store_dwordx4 v[80:83],v9,s[40:43],s87 offen sc1\n"
MF "v[128:131],a[196:199],v[180:183],v[128:131]\n"
MF "v[128:131],a[236:239],v[184:187],v[128:131]\n"
"s_waitcnt lgkmcnt(2)\n"
MF "v[132:135],a[236:239],a[244:247],v[132:135]\n"
"s_waitcnt lgkmcnt(0)\n"
TR "v[176:177],v6\n"
"v_add_f32 v88,v88,v200\n"
MF "v[132:135],a[196:199],a[240:243],v[132:135]\n"
TR "v[178:179],v6 offset:2048\n"
"v_add_f32 v89,v89,v201\n"
MF "v[132:135],a[156:159],v[188:191],v[132:135]\n"
"v_add_f32 v90,v90,v202\n"
"v_add_f32 v91,v91,v203\n"
MF "v[124:127],a[152:155],v[188:191],v[124:127]\n"
TR "v[180:181],v6 offset:4096\n"
"v_add_f32 v92,v92,v204\n"
MF "v[124:127],a[192:195],a[240:243],v[124:127]\n"
TR "v[182:183],v6 offset:6144\n"
"v_add_f32 v93,v93,v205\n"
MF "v[124:127],a[232:235],a[244:247],v[124:127]\n"
"v_add_f32 v94,v94,v206\n"
"v_add_f32 v95,v95,v207\n"
MF "v[116:119],a[228:231],a[244:247],v[116:119]\n"
TR "v[184:185],v6 offset:8192\n"
CV "v88,v88,v89\n"
MF "v[116:119],a[188:191],a[240:243],v[116:119]\n"
TR "v[186:187],v6 offset:10240\n"
CV "v89,v90,v91\n"
MF "v[116:119],a[148:151],v[188:191],v[116:119]\n"
"s_add_u32 s87,s90,14336\n"
CV "v90,v92,v93\n"
CV "v91,v94,v95\n"
MF "v[108:111],a[144:147],v[188:191],v[108:111]\n"
MF "v[108:111],a[184:187],a[240:243],v[108:111]\n"
MF "v[108:111],a[224:227],a[244:247],v[108:111]\n"
MF "v[100:103],a[220:223],a[244:247],v[100:103]\n"
"buffer_store_dwordx4 v[88:91],v9,s[40:43],s87 offen sc1\n"
MF "v[100:103],a[180:183],a[240:243],v[100:103]\n"
"s_mov_b32 s90,s91\n"
"s_add_u32 s91,s91,20480\n"
MF "v[100:103],a[140:143],v[188:191],v[100:103]\n"
"s_waitcnt lgkmcnt(0)\n"
MF "v[136:139],a[140:143],v[176:179],v[136:139]\n"
TR "v[188:189],v7\n"
MF "v[136:139],a[180:183],v[180:183],v[136:139]\n"
TR "v[190:191],v7 offset:2048\n"
MF "v[136:139],a[220:223],v[184:187],v[136:139]\n"
MF "v[144:147],a[224:227],v[184:187],v[144:147]\n"
TR "a[240:241],v7 offset:4096\n"
MF "v[144:147],a[184:187],v[180:183],v[144:147]\n"
TR "a[242:243],v7 offset:6144\n"
MF "v[144:147],a[144:147],v[176:179],v[144:147]\n"
MF "v[152:155],a[148:151],v[176:179],v[152:155]\n"
TR "a[244:245],v7 offset:8192\n"
MF "v[152:155],a[188:191],v[180:183],v[152:155]\n"
TR "a[246:247],v7 offset:10240\n"
MF "v[152:155],a[228:231],v[184:187],v[152:155]\n"
MF "v[160:163],a[232:235],v[184:187],v[160:163]\n"
MF "v[160:163],a[192:195],v[180:183],v[160:163]\n"
MF "v[160:163],a[152:155],v[176:179],v[160:163]\n"
MF "v[168:171],a[156:159],v[176:179],v[168:171]\n"
MF "v[168:171],a[196:199],v[180:183],v[168:171]\n"
"s_waitcnt lgkmcnt(0)\n"
MF "v[168:171],a[236:239],v[184:187],v[168:171]\n"
"s_barrier\n"
MF "v[172:175],a[236:239],a[244:247],v[172:175]\n"
TR "v[176:177],v0 offset:24576\n"
MF "v[172:175],a[196:199],a[240:243],v[172:175]\n"
"s_add_u32 s85,s85,1\n"
TR "v[178:179],v0 offset:26624\n"
MF "v[172:175],a[156:159],v[188:191],v[172:175]\n"
MF "v[164:167],a[152:155],v[188:191],v[164:167]\n"
TR "v[180:181],v0 offset:28672\n"
MF "v[164:167],a[192:195],a[240:243],v[164:167]\n"
TR "v[182:183],v0 offset:30720\n"
MF "v[164:167],a[232:235],a[244:247],v[164:167]\n"
MF "v[156:159],a[228:231],a[244:247],v[156:159]\n"
TR "v[184:185],v0 offset:32768\n"
MF "v[156:159],a[188:191],a[240:243],v[156:159]\n"
TR "v[186:187],v0 offset:34816\n"
MF "v[156:159],a[148:151],v[188:191],v[156:159]\n"
MF "v[148:151],a[144:147],v[188:191],v[148:151]\n"
MF "v[148:151],a[184:187],a[240:243],v[148:151]\n"
MF "v[148:151],a[224:227],a[244:247],v[148:151]\n"
MF "v[140:143],a[220:223],a[244:247],v[140:143]\n"
MF "v[140:143],a[180:183],a[240:243],v[140:143]\n"
MF "v[140:143],a[140:143],v[188:191],v[140:143]\n"
"s_waitcnt lgkmcnt(0)\n"
MF "v[16:19],a[0:3],v[176:179],v[252:255]\n"
TR "v[188:189],v1 offset:24576\n"
MF "v[16:19],a[40:43],v[180:183],v[16:19]\n"
TR "v[190:191],v1 offset:26624\n"
MF "v[16:19],a[80:83],v[184:187],v[16:19]\n"
MF "v[24:27],a[84:87],v[184:187],v[252:255]\n"
TR "a[240:241],v1 offset:28672\n"
MF "v[24:27],a[44:47],v[180:183],v[24:27]\n"
TR "a[242:243],v1 offset:30720\n"
MF "v[24:27],a[4:7],v[176:179],v[24:27]\n"
MF "v[32:35],a[8:11],v[176:179],v[252:255]\n"
TR "a[244:245],v1 offset:32768\n"
MF "v[32:35],a[48:51],v[180:183],v[32:35]\n"
TR "a[246:247],v1 offset:34816\n"
MF "v[32:35],a[88:91],v[184:187],v[32:35]\n"
MF "v[40:43],a[92:95],v[184:187],v[252:255]\n"
MF "v[40:43],a[52:55],v[180:183],v[40:43]\n"
MF "v[40:43],a[12:15],v[176:179],v[40:43]\n"
MF "v[48:51],a[16:19],v[176:179],v[252:255]\n"
MF "v[48:51],a[56:59],v[180:183],v[48:51]\n"
MF "v[48:51],a[96:99],v[184:187],v[48:51]\n"
"s_waitcnt lgkmcnt(0)\n"
MF "v[52:55],a[96:99],a[244:247],v[252:255]\n"
TR "v[176:177],v2 offset:24576\n"
MF "v[52:55],a[56:59],a[240:243],v[52:55]\n"
TR "v[178:179],v2 offset:26624\n"
MF "v[52:55],a[16:19],v[188:191],v[52:55]\n"
MF "v[44:47],a[12:15],v[188:191],v[252:255]\n"
TR "v[180:181],v2 offset:28672\n"
MF "v[44:47],a[52:55],a[240:243],v[44:47]\n"
TR "v[182:183],v2 offset:30720\n"
MF "v[44:47],a[92:95],a[244:247],v[44:47]\n"
MF "v[36:39],a[88:91],a[244:247],v[252:255]\n"
TR "v[184:185],v2 offset:32768\n"
MF "v[36:39],a[48:51],a[240:243],v[36:39]\n"
TR "v[186:187],v2 offset:34816\n"
MF "v[36:39],a[8:11],v[188:191],v[36:39]\n"
MF "v[28:31],a[4:7],v[188:191],v[252:255]\n"
MF "v[28:31],a[44:47],a[240:243],v[28:31]\n"
MF "v[28:31],a[84:87],a[244:247],v[28:31]\n"
MF "v[20:23],a[80:83],a[244:247],v[252:255]\n"
MF "v[20:23],a[40:43],a[240:243],v[20:23]\n"
MF "v[20:23],a[0:3],v[188:191],v[20:23]\n"
"s_waitcnt lgkmcnt(0)\n"
MF "v[56:59],a[0:3],v[176:179],v[252:255]\n"
TR "v[188:189],v3 offset:24576\n"
MF "v[56:59],a[40:43],v[180:183],v[56:59]\n"
TR "v[190:191],v3 offset:26624\n"
MF "v[56:59],a[80:83],v[184:187],v[56:59]\n"
MF "v[64:67],a[84:87],v[184:187],v[252:255]\n"
TR "a[240:241],v3 offset:28672\n"
MF "v[64:67],a[44:47],v[180:183],v[64:67]\n"
TR "a[242:243],v3 offset:30720\n"
MF "v[64:67],a[4:7],v[176:179],v[64:67]\n"
MF "v[72:75],a[8:11],v[176:179],v[252:255]\n"
TR "a[244:245],v3 offset:32768\n"
MF "v[72:75],a[48:51],v[180:183],v[72:75]\n"
TR "a[246:247],v3 offset:34816\n"
MF "v[72:75],a[88:91],v[184:187],v[72:75]\n"
MF "v[80:83],a[92:95],v[184:187],v[252:255]\n"
MF "v[80:83],a[52:55],v[180:183],v[80:83]\n"
MF "v[80:83],a[12:15],v[176:179],v[80:83]\n"
MF "v[88:91],a[16:19],v[176:179],v[252:255]\n"
MF "v[88:91],a[56:59],v[180:183],v[88:91]\n"
"s_waitcnt lgkmcnt(0)\n"
MF "v[88:91],a[96:99],v[184:187],v[88:91]\n"
"s_barrier\n"
MF "v[92:95],a[96:99],a[244:247],v[252:255]\n"
TR "v[176:177],v0\n"
MF "v[92:95],a[56:59],a[240:243],v[92:95]\n"
TR "v[178:179],v0 offset:2048\n"
MF "v[92:95],a[16:19],v[188:191],v[92:95]\n"
"ds_read2st64_b32 v[192:193],v13 offset0:192 offset1:193\n"
MF "v[84:87],a[12:15],v[188:191],v[252:255]\n"
TR "v[180:181],v0 offset:4096\n"
MF "v[84:87],a[52:55],a[240:243],v[84:87]\n"
TR "v[182:183],v0 offset:6144\n"
MF "v[84:87],a[92:95],a[244:247],v[84:87]\n"
"ds_read2st64_b32 v[194:195],v13 offset0:194 offset1:195\n"
MF "v[76:79],a[88:91],a[244:247],v[252:255]\n"
TR "v[184:185],v0 offset:8192\n"
MF "v[76:79],a[48:51],a[240:243],v[76:79]\n"
TR "v[186:187],v0 offset:10240\n"
MF "v[76:79],a[8:11],v[188:191],v[76:79]\n"
"ds_read2st64_b32 v[196:197],v13 offset0:196 offset1:197\n"
MF "v[68:71],a[4:7],v[188:191],v[252:255]\n"
"ds_read2st64_b32 v[198:199],v13 offset0:198 offset1:199\n"
MF "v[68:71],a[44:47],a[240:243],v[68:71]\n"
MF "v[68:71],a[84:87],a[244:247],v[68:71]\n"
MF "v[60:63],a[80:83],a[244:247],v[252:255]\n"
MF "v[60:63],a[40:43],a[240:243],v[60:63]\n"
MF "v[60:63],a[0:3],v[188:191],v[60:63]\n"
"s_waitcnt lgkmcnt(2)\n"
MF "v[16:19],a[120:123],v[176:179],v[16:19]\n"
"s_waitcnt lgkmcnt(0)\n"
TR "v[188:189],v1\n"
"v_add_f32 v96,v96,v192\n"
MF "v[16:19],a[160:163],v[180:183],v[16:19]\n"
TR "v[190:191],v1 offset:2048\n"
"v_add_f32 v97,v97,v193\n"
MF "v[16:19],a[200:203],v[184:187],v[16:19]\n"
"ds_read2st64_b32 v[200:201],v13 offset0:200 offset1:201\n"
"v_add_f32 v98,v98,v194\n"
"s_waitcnt vmcnt(0)\n"
"v_mov_b32 v14,s76\n"
"s_add_u32 s76,s76,1\n"
MF "v[24:27],a[204:207],v[184:187],v[24:27]\n"
TR "a[240:241],v1 offset:4096\n"
"v_add_f32 v99,v99,v195\n"
"buffer_store_dword v14,v15,s[40:43],s94 offen sc1\n"
MF "v[24:27],a[164:167],v[180:183],v[24:27]\n"
TR "a[242:243],v1 offset:6144\n"
"v_add_f32 v100,v100,v196\n"
MF "v[24:27],a[124:127],v[176:179],v[24:27]\n"
"ds_read2st64_b32 v[202:203],v13 offset0:202 offset1:203\n"
"v_add_f32 v101,v101,v197\n"
MF "v[32:35],a[128:131],v[176:179],v[32:35]\n"
TR "a[244:245],v1 offset:8192\n"
"v_add_f32 v102,v102,v198\n"
MF "v[32:35],a[168:171],v[180:183],v[32:35]\n"
TR "a[246:247],v1 offset:10240\n"
"v_add_f32 v103,v103,v199\n"
MF "v[32:35],a[208:211],v[184:187],v[32:35]\n"
"ds_read2st64_b32 v[204:205],v13 offset0:204 offset1:205\n"
CV "v96,v96,v97\n"
MF "v[40:43],a[212:215],v[184:187],v[40:43]\n"
"ds_read2st64_b32 v[206:207],v13 offset0:206 offset1:207\n"
CV "v97,v98,v99\n"
MF "v[40:43],a[172:175],v[180:183],v[40:43]\n"
"s_add_u32 s87,s90,0\n"
CV "v98,v100,v101\n"
CV "v99,v102,v103\n"
MF "v[40:43],a[132:135],v[176:179],v[40:43]\n"
MF "v[48:51],a[136:139],v[176:179],v[48:51]\n"
"buffer_store_dwordx4 v[96:99],v9,s[40:43],s87 offen sc1\n"
MF "v[48:51],a[176:179],v[180:183],v[48:51]\n"
MF "v[48:51],a[216:219],v[184:187],v[48:51]\n"
"s_waitcnt lgkmcnt(2)\n"
MF "v[52:55],a[216:219],a[244:247],v[52:55]\n"
"s_waitcnt lgkmcnt(0)\n"
TR "v[176:177],v2\n"
"v_add_f32 v104,v104,v200\n"
MF "v[52:55],a[176:179],a[240:243],v[52:55]\n"
TR "v[178:179],v2 offset:2048\n"
"v_add_f32 v105,v105,v201\n"
MF "v[52:55],a[136:139],v[188:191],v[52:55]\n"
"ds_read2st64_b32 v[192:193],v13 offset0:208 offset1:209\n"
"v_add_f32 v106,v106,v202\n"
MF "v[44:47],a[132:135],v[188:191],v[44:47]\n"
TR "v[180:181],v2 offset:4096\n"
"v_add_f32 v107,v107,v203\n"
MF "v[44:47],a[172:175],a[240:243],v[44:47]\n"
TR "v[182:183],v2 offset:6144\n"
"v_add_f32 v108,v108,v204\n"
MF "v[44:47],a[212:215],a[244:247],v[44:47]\n"
"ds_read2st64_b32 v[194:195],v13 offset0:210 offset1:211\n"
"v_add_f32 v109,v109,v205\n"
MF "v[36:39],a[208:211],a[244:247],v[36:39]\n"
TR "v[184:185],v2 offset:8192\n"
"v_add_f32 v110,v110,v206\n"
MF "v[36:39],a[168:171],a[240:243],v[36:39]\n"
TR "v[186:187],v2 offset:10240\n"
"v_add_f32 v111,v111,v207\n"
MF "v[36:39],a[128:131],v[188:191],v[36:39]\n"
"ds_read2st64_b32 v[196:197],v13 offset0:212 offset1:213\n"
CV "v104,v104,v105\n"
MF "v[28:31],a[124:127],v[188:191],v[28:31]\n"
"ds_read2st64_b32 v[198:199],v13 offset0:214 offset1:215\n"
CV "v105,v106,v107\n"
MF "v[28:31],a[164:167],a[240:243],v[28:31]\n"
"s_add_u32 s87,s90,1024\n"
CV "v106,v108,v109\n"
CV "v107,v110,v111\n"
MF "v[28:31],a[204:207],a[244:247],v[28:31]\n"
MF "v[20:23],a[200:203],a[244:247],v[20:23]\n"
"buffer_store_dwordx4 v[104:107],v9,s[40:43],s87 offen sc1\n"
MF "v[20:23],a[160:163],a[240:243],v[20:23]\n"
MF "v[20:23],a[120:123],v[188:191],v[20:23]\n"
"s_waitcnt lgkmcnt(2)\n"
MF "v[56:59],a[120:123],v[176:179],v[56:59]\n"
"s_waitcnt lgkmcnt(0)\n"
TR "v[188:189],v3\n"
"v_add_f32 v112,v112,v192\n"
MF "v[56:59],a[160:163],v[180:183],v[56:59]\n"
TR "v[190:191],v3 offset:2048\n"
"v_add_f32 v113,v113,v193\n"
MF "v[56:59],a[200:203],v[184:187],v[56:59]\n"
"ds_read2st64_b32 v[200:201],v13 offset0:216 offset1:217\n"
"v_add_f32 v114,v114,v194\n"
MF "v[64:67],a[204:207],v[184:187],v[64:67]\n"
".Lpg31h:\n"
"s_cbranch_execz .Lpg35h\n"
TR "a[240:241],v3 offset:4096\n"
"v_add_f32 v115,v115,v195\n"
MF "v[64:67],a[164:167],v[180:183],v[64:67]\n"
TR "a[242:243],v3 offset:6144\n"
"v_add_f32 v116,v116,v196\n"
MF "v[64:67],a[124:127],v[176:179],v[64:67]\n"
"ds_read2st64_b32 v[202:203],v13 offset0:218 offset1:219\n"
"v_add_f32 v117,v117,v197\n"
MF "v[72:75],a[128:131],v[176:179],v[72:75]\n"
TR "a[244:245],v3 offset:8192\n"
"v_add_f32 v118,v118,v198\n"
MF "v[72:75],a[168:171],v[180:183],v[72:75]\n"
TR "a[246:247],v3 offset:10240\n"
"v_add_f32 v119,v119,v199\n"
MF "v[72:75],a[208:211],v[184:187],v[72:75]\n"
"ds_read2st64_b32 v[204:205],v13 offset0:220 offset1:221\n"
CV "v112,v112,v113\n"
MF "v[80:83],a[212:215],v[184:187],v[80:83]\n"
"ds_read2st64_b32 v[206:207],v13 offset0:222 offset1:223\n"
CV "v113,v114,v115\n"
MF "v[80:83],a[172:175],v[180:183],v[80:83]\n"
"s_add_u32 s87,s90,2048\n"
CV "v114,v116,v117\n"
CV "v115,v118,v119\n"
MF "v[80:83],a[132:135],v[176:179],v[80:83]\n"
MF "v[88:91],a[136:139],v[176:179],v[88:91]\n"
"buffer_store_dwordx4 v[112:115],v9,s[40:43],s87 offen sc1\n"
MF "v[88:91],a[176:179],v[180:183],v[88:91]\n"
"s_waitcnt lgkmcnt(0)\n"
MF "v[88:91],a[216:219],v[184:187],v[88:91]\n"
"s_barrier\n"
MF "v[92:95],a[216:219],a[244:247],v[92:95]\n"
TR "v[176:177],v0 offset:49152\n"
"v_add_f32 v120,v120,v200\n"
MF "v[92:95],a[176:179],a[240:243],v[92:95]\n"
TR "v[178:179],v0 offset:51200\n"
"v_add_f32 v121,v121,v201\n"
MF "v[92:95],a[136:139],v[188:191],v[92:95]\n"
"ds_read2st64_b32 v[192:193],v13 offset0:224 offset1:225\n"
"v_add_f32 v122,v122,v202\n"
MF "v[84:87],a[132:135],v[188:191],v[84:87]\n"
TR "v[180:181],v0 offset:53248\n"
"v_add_f32 v123,v123,v203\n"
MF "v[84:87],a[172:175],a[240:243],v[84:87]\n"
TR "v[182:183],v0 offset:55296\n"
"v_add_f32 v124,v124,v204\n"
MF "v[84:87],a[212:215],a[244:247],v[84:87]\n"
"ds_read2st64_b32 v[194:195],v13 offset0:226 offset1:227\n"
"v_add_f32 v125,v125,v205\n"
MF "v[76:79],a[208:211],a[244:247],v[76:79]\n"
TR "v[184:185],v0 offset:57344\n"
"v_add_f32 v126,v126,v206\n"
MF "v[76:79],a[168:171],a[240:243],v[76:79]\n"
TR "v[186:187],v0 offset:59392\n"
"v_add_f32 v127,v127,v207\n"
MF "v[76:79],a[128:131],v[188:191],v[76:79]\n"
"ds_read2st64_b32 v[196:197],v13 offset0:228 offset1:229\n"
CV "v120,v120,v121\n"
MF "v[68:71],a[124:127],v[188:191],v[68:71]\n"
"ds_read2st64_b32 v[198:199],v13 offset0:230 offset1:231\n"
CV "v121,v122,v123\n"
MF "v[68:71],a[164:167],a[240:243],v[68:71]\n"
"s_add_u32 s87,s90,3072\n"
CV "v122,v124,v125\n"
CV "v123,v126,v127\n"
MF "v[68:71],a[204:207],a[244:247],v[68:71]\n"
MF "v[60:63],a[200:203],a[244:247],v[60:63]\n"
"buffer_store_dwordx4 v[120:123],v9,s[40:43],s87 offen sc1\n"
MF "v[60:63],a[160:163],a[240:243],v[60:63]\n"
MF "v[60:63],a[120:123],v[188:191],v[60:63]\n"
"s_waitcnt lgkmcnt(2)\n"
MF "v[16:19],a[20:23],v[176:179],v[16:19]\n"
"s_waitcnt lgkmcnt(0)\n"
TR "v[188:189],v1 offset:49152\n"
"v_add_f32 v128,v128,v192\n"
MF "v[16:19],a[60:63],v[180:183],v[16:19]\n"
TR "v[190:191],v1 offset:51200\n"
"v_add_f32 v129,v129,v193\n"
MF "v[16:19],a[100:103],v[184:187],v[16:19]\n"
"ds_read2st64_b32 v[200:201],v13 offset0:0 offset1:1\n"
"v_add_f32 v130,v130,v194\n"
MF "v[24:27],a[104:107],v[184:187],v[24:27]\n"
TR "a[240:241],v1 offset:53248\n"
"v_add_f32 v131,v131,v195\n"
MF "v[24:27],a[64:67],v[180:183],v[24:27]\n"
TR "a[242:243],v1 offset:55296\n"
"v_add_f32 v132,v132,v196\n"
MF "v[24:27],a[24:27],v[176:179],v[24:27]\n"
"ds_read2st64_b32 v[202:203],v13 offset0:2 offset1:3\n"
"v_add_f32 v133,v133,v197\n"
MF "v[32:35],a[28:31],v[176:179],v[32:35]\n"
TR "a[244:245],v1 offset:57344\n"
"v_add_f32 v134,v134,v198\n"
MF "v[32:35],a[68:71],v[180:183],v[32:35]\n"
TR "a[246:247],v1 offset:59392\n"
"v_add_f32 v135,v135,v199\n"
MF "v[32:35],a[108:111],v[184:187],v[32:35]\n"
"ds_read2st64_b32 v[204:205],v13 offset0:4 offset1:5\n"
CV "v128,v128,v129\n"
MF "v[40:43],a[112:115],v[184:187],v[40:43]\n"
"ds_read2st64_b32 v[206:207],v13 offset0:6 offset1:7\n"
CV "v129,v130,v131\n"
MF "v[40:43],a[72:75],v[180:183],v[40:43]\n"
"s_add_u32 s87,s90,4096\n"
CV "v130,v132,v133\n"
CV "v131,v134,v135\n"
MF "v[40:43],a[32:35],v[176:179],v[40:43]\n"
MF "v[48:51],a[36:39],v[176:179],v[48:51]\n"
"buffer_store_dwordx4 v[128:131],v9,s[40:43],s87 offen sc1\n"
MF "v[48:51],a[76:79],v[180:183],v[48:51]\n"
MF "v[48:51],a[116:119],v[184:187],v[48:51]\n"
"s_waitcnt lgkmcnt(2)\n"
MF "v[52:55],a[116:119],a[244:247],v[52:55]\n"
"s_waitcnt lgkmcnt(0)\n"
TR "v[176:177],v2 offset:49152\n"
"v_add_f32 v136,v136,v200\n"
MF "v[52:55],a[76:79],a[240:243],v[52:55]\n"
TR "v[178:179],v2 offset:51200\n"
"v_add_f32 v137,v137,v201\n"
MF "v[52:55],a[36:39],v[188:191],v[52:55]\n"
"ds_read2st64_b32 v[192:193],v13 offset0:8 offset1:9\n"
"v_add_f32 v138,v138,v202\n"
MF "v[44:47],a[32:35],v[188:191],v[44:47]\n"
TR "v[180:181],v2 offset:53248\n"
"v_add_f32 v139,v139,v203\n"
MF "v[44:47],a[72:75],a[240:243],v[44:47]\n"
TR "v[182:183],v2 offset:55296\n"
"v_add_f32 v140,v140,v204\n"
MF "v[44:47],a[112:115],a[244:247],v[44:47]\n"
"ds_read2st64_b32 v[194:195],v13 offset0:10 offset1:11\n"
"v_add_f32 v141,v141,v205\n"
MF "v[36:39],a[108:111],a[244:247],v[36:39]\n"
TR "v[184:185],v2 offset:57344\n"
"v_add_f32 v142,v142,v206\n"
MF "v[36:39],a[68:71],a[240:243],v[36:39]\n"
TR "v[186:187],v2 offset:59392\n"
"v_add_f32 v143,v143,v207\n"
MF "v[36:39],a[28:31],v[188:191],v[36:39]\n"
"ds_read2st64_b32 v[196:197],v13 offset0:12 offset1:13\n"
CV "v136,v136,v137\n"
MF "v[28:31],a[24:27],v[188:191],v[28:31]\n"
"ds_read2st64_b32 v[198:199],v13 offset0:14 offset1:15\n"
CV "v137,v138,v139\n"
MF "v[28:31],a[64:67],a[240:243],v[28:31]\n"
"s_add_u32 s87,s90,10240\n"
CV "v138,v140,v141\n"
CV "v139,v142,v143\n"
MF "v[28:31],a[104:107],a[244:247],v[28:31]\n"
MF "v[20:23],a[100:103],a[244:247],v[20:23]\n"
"buffer_store_dwordx4 v[136:139],v9,s[40:43],s87 offen sc1\n"
MF "v[20:23],a[60:63],a[240:243],v[20:23]\n"
MF "v[20:23],a[20:23],v[188:191],v[20:23]\n"
"s_waitcnt lgkmcnt(2)\n"
MF "v[56:59],a[20:23],v[176:179],v[56:59]\n"
"s_waitcnt lgkmcnt(0)\n"
TR "v[188:189],v3 offset:49152\n"
"v_add_f32 v144,v144,v192\n"
MF "v[56:59],a[60:63],v[180:183],v[56:59]\n"
TR "v[190:191],v3 offset:51200\n"
"v_add_f32 v145,v145,v193\n"
MF "v[56:59],a[100:103],v[184:187],v[56:59]\n"
"ds_read2st64_b32 v[200:201],v13 offset0:16 offset1:17\n"
"v_add_f32 v146,v146,v194\n"
MF "v[64:67],a[104:107],v[184:187],v[64:67]\n"
TR "a[240:241],v3 offset:53248\n"
"v_add_f32 v147,v147,v195\n"
MF "v[64:67],a[64:67],v[180:183],v[64:67]\n"
TR "a[242:243],v3 offset:55296\n"
"v_add_f32 v148,v148,v196\n"
MF "v[64:67],a[24:27],v[176:179],v[64:67]\n"
"ds_read2st64_b32 v[202:203],v13 offset0:18 offset1:19\n"
"v_add_f32 v149,v149,v197\n"
MF "v[72:75],a[28:31],v[176:179],v[72:75]\n"
TR "a[244:245],v3 offset:57344\n"
"v_add_f32 v150,v150,v198\n"
MF "v[72:75],a[68:71],v[180:183],v[72:75]\n"
TR "a[246:247],v3 offset:59392\n"
"v_add_f32 v151,v151,v199\n"
MF "v[72:75],a[108:111],v[184:187],v[72:75]\n"
"ds_read2st64_b32 v[204:205],v13 offset0:20 offset1:21\n"
CV "v144,v144,v145\n"
MF "v[80:83],a[112:115],v[184:187],v[80:83]\n"
"ds_read2st64_b32 v[206:207],v13 offset0:22 offset1:23\n"
CV "v145,v146,v147\n"
MF "v[80:83],a[72:75],v[180:183],v[80:83]\n"
"s_add_u32 s87,s90,11264\n"
CV "v146,v148,v149\n"
CV "v147,v150,v151\n"
MF "v[80:83],a[32:35],v[176:179],v[80:83]\n"
MF "v[88:91],a[36:39],v[176:179],v[88:91]\n"
"buffer_store_dwordx4 v[144:147],v9,s[40:43],s87 offen sc1\n"
MF "v[88:91],a[76:79],v[180:183],v[88:91]\n"
"s_waitcnt lgkmcnt(0)\n"
MF "v[88:91],a[116:119],v[184:187],v[88:91]\n"
"s_barrier\n"
MF "v[92:95],a[116:119],a[244:247],v[92:95]\n"
TR "v[176:177],v4\n"
"v_add_f32 v152,v152,v200\n"
MF "v[92:95],a[76:79],a[240:243],v[92:95]\n"
TR "v[178:179],v4 offset:2048\n"
"v_add_f32 v153,v153,v201\n"
MF "v[92:95],a[36:39],v[188:191],v[92:95]\n"
"ds_read2st64_b32 v[192:193],v13 offset0:24 offset1:25\n"
"v_add_f32 v154,v154,v202\n"
MF "v[84:87],a[32:35],v[188:191],v[84:87]\n"
TR "v[180:181],v4 offset:4096\n"
"v_add_f32 v155,v155,v203\n"
MF "v[84:87],a[72:75],a[240:243],v[84:87]\n"
TR "v[182:183],v4 offset:6144\n"
"v_add_f32 v156,v156,v204\n"
MF "v[84:87],a[112:115],a[244:247],v[84:87]\n"
"ds_read2st64_b32 v[194:195],v13 offset0:26 offset1:27\n"
"v_add_f32 v157,v157,v205\n"
MF "v[76:79],a[108:111],a[244:247],v[76:79]\n"
TR "v[184:185],v4 offset:8192\n"
"v_add_f32 v158,v158,v206\n"
MF "v[76:79],a[68:71],a[240:243],v[76:79]\n"
TR "v[186:187],v4 offset:10240\n"
"v_add_f32 v159,v159,v207\n"
MF "v[76:79],a[28:31],v[188:191],v[76:79]\n"
"ds_read2st64_b32 v[196:197],v13 offset0:28 offset1:29\n"
CV "v152,v152,v153\n"
MF "v[68:71],a[24:27],v[188:191],v[68:71]\n"
"ds_read2st64_b32 v[198:199],v13 offset0:30 offset1:31\n"
CV "v153,v154,v155\n"
MF "v[68:71],a[64:67],a[240:243],v[68:71]\n"
"s_add_u32 s87,s90,12288\n"
CV "v154,v156,v157\n"
CV "v155,v158,v159\n"
MF "v[68:71],a[104:107],a[244:247],v[68:71]\n"
MF "v[60:63],a[100:103],a[244:247],v[60:63]\n"
"buffer_store_dwordx4 v[152:155],v9,s[40:43],s87 offen sc1\n"
MF "v[60:63],a[60:63],a[240:243],v[60:63]\n"
MF "v[60:63],a[20:23],v[188:191],v[60:63]\n"
"s_waitcnt lgkmcnt(2)\n"
MF "v[16:19],a[140:143],v[176:179],v[16:19]\n"
"s_waitcnt lgkmcnt(0)\n"
TR "v[188:189],v5\n"
"v_add_f32 v160,v160,v192\n"
MF "v[16:19],a[180:183],v[180:183],v[16:19]\n"
TR "v[190:191],v5 offset:2048\n"
"v_add_f32 v161,v161,v193\n"
MF "v[16:19],a[220:223],v[184:187],v[16:19]\n"
"ds_read2st64_b32 v[200:201],v13 offset0:32 offset1:33\n"
"v_add_f32 v162,v162,v194\n"
MF "v[24:27],a[224:227],v[184:187],v[24:27]\n"
TR "a[240:241],v5 offset:4096\n"
"v_add_f32 v163,v163,v195\n"
MF "v[24:27],a[184:187],v[180:183],v[24:27]\n"
TR "a[242:243],v5 offset:6144\n"
"v_add_f32 v164,v164,v196\n"
MF "v[24:27],a[144:147],v[176:179],v[24:27]\n"
"ds_read2st64_b32 v[202:203],v13 offset0:34 offset1:35\n"
"v_add_f32 v165,v165,v197\n"
MF "v[32:35],a[148:151],v[176:179],v[32:35]\n"
TR "a[244:245],v5 offset:8192\n"
"v_add_f32 v166,v166,v198\n"
MF "v[32:35],a[188:191],v[180:183],v[32:35]\n"
TR "a[246:247],v5 offset:10240\n"
"v_add_f32 v167,v167,v199\n"
MF "v[32:35],a[228:231],v[184:187],v[32:35]\n"
"ds_read2st64_b32 v[204:205],v13 offset0:36 offset1:37\n"
CV "v160,v160,v161\n"
MF "v[40:43],a[232:235],v[184:187],v[40:43]\n"
"ds_read2st64_b32 v[206:207],v13 offset0:38 offset1:39\n"
CV "v161,v162,v163\n"
MF "v[40:43],a[192:195],v[180:183],v[40:43]\n"
"s_add_u32 s87,s90,13312\n"
CV "v162,v164,v165\n"
CV "v163,v166,v167\n"
MF "v[40:43],a[152:155],v[176:179],v[40:43]\n"
MF "v[48:51],a[156:159],v[176:179],v[48:51]\n"
"buffer_store_dwordx4 v[160:163],v9,s[40:43],s87 offen sc1\n"
MF "v[48:51],a[196:199],v[180:183],v[48:51]\n"
MF "v[48:51],a[236:239],v[184:187],v[48:51]\n"
"s_waitcnt lgkmcnt(2)\n"
MF "v[52:55],a[236:239],a[244:247],v[52:55]\n"
"s_waitcnt lgkmcnt(0)\n"
TR "v[176:177],v6\n"
"v_add_f32 v168,v168,v200\n"
MF "v[52:55],a[196:199],a[240:243],v[52:55]\n"
TR "v[178:179],v6 offset:2048\n"
"v_add_f32 v169,v169,v201\n"
MF "v[52:55],a[156:159],v[188:191],v[52:55]\n"
"v_add_f32 v170,v170,v202\n"
"v_add_f32 v171,v171,v203\n"
MF "v[44:47],a[152:155],v[188:191],v[44:47]\n"
TR "v[180:181],v6 offset:4096\n"
"v_add_f32 v172,v172,v204\n"
MF "v[44:47],a[192:195],a[240:243],v[44:47]\n"
TR "v[182:183],v6 offset:6144\n"
"v_add_f32 v173,v173,v205\n"
MF "v[44:47],a[232:235],a[244:247],v[44:47]\n"
"v_add_f32 v174,v174,v206\n"
"v_add_f32 v175,v175,v207\n"
MF "v[36:39],a[228:231],a[244:247],v[36:39]\n"
TR "v[184:185],v6 offset:8192\n"
CV "v168,v168,v169\n"
MF "v[36:39],a[188:191],a[240:243],v[36:39]\n"
TR "v[186:187],v6 offset:10240\n"
CV "v169,v170,v171\n"
MF "v[36:39],a[148:151],v[188:191],v[36:39]\n"
"s_add_u32 s87,s90,14336\n"
CV "v170,v172,v173\n"
CV "v171,v174,v175\n"
MF "v[28:31],a[144:147],v[188:191],v[28:31]\n"
MF "v[28:31],a[184:187],a[240:243],v[28:31]\n"
MF "v[28:31],a[224:227],a[244:247],v[28:31]\n"
MF "v[20:23],a[220:223],a[244:247],v[20:23]\n"
"buffer_store_dwordx4 v[168:171],v9,s[40:43],s87 offen sc1\n"
MF "v[20:23],a[180:183],a[240:243],v[20:23]\n"
"s_mov_b32 s90,s91\n"
"s_add_u32 s91,s91,20480\n"
MF "v[20:23],a[140:143],v[188:191],v[20:23]\n"
"s_waitcnt lgkmcnt(0)\n"
MF "v[56:59],a[140:143],v[176:179],v[56:59]\n"
TR "v[188:189],v7\n"
MF "v[56:59],a[180:183],v[180:183],v[56:59]\n"
TR "v[190:191],v7 offset:2048\n"
MF "v[56:59],a[220:223],v[184:187],v[56:59]\n"
"v_mov_b32 v244,0\n"
"v_mov_b32 v245,0\n"
MF "v[64:67],a[224:227],v[184:187],v[64:67]\n"
TR "a[240:241],v7 offset:4096\n"
"v_mov_b32 v246,0\n"
MF "v[64:67],a[184:187],v[180:183],v[64:67]\n"
TR "a[242:243],v7 offset:6144\n"
"v_mov_b32 v247,0\n"
MF "v[64:67],a[144:147],v[176:179],v[64:67]\n"
"v_mov_b32 v248,0\n"
"v_mov_b32 v249,0\n"
MF "v[72:75],a[148:151],v[176:179],v[72:75]\n"
TR "a[244:245],v7 offset:8192\n"
"v_mov_b32 v250,0\n"
MF "v[72:75],a[188:191],v[180:183],v[72:75]\n"
TR "a[246:247],v7 offset:10240\n"
"v_mov_b32 v251,0\n"
MF "v[72:75],a[228:231],v[184:187],v[72:75]\n"
MF "v[80:83],a[232:235],v[184:187],v[80:83]\n"
MF "v[80:83],a[192:195],v[180:183],v[80:83]\n"
MF "v[80:83],a[152:155],v[176:179],v[80:83]\n"
MF "v[88:91],a[156:159],v[176:179],v[88:91]\n"
MF "v[88:91],a[196:199],v[180:183],v[88:91]\n"
"s_waitcnt lgkmcnt(0)\n"
MF "v[88:91],a[236:239],v[184:187],v[88:91]\n"
"s_barrier\n"
MF "v[92:95],a[236:239],a[244:247],v[92:95]\n"
TR "v[176:177],v0 offset:24576\n"
MF "v[92:95],a[196:199],a[240:243],v[92:95]\n"
"s_add_u32 s85,s85,1\n"
TR "v[178:179],v0 offset:26624\n"
MF "v[92:95],a[156:159],v[188:191],v[92:95]\n"
MF "v[84:87],a[152:155],v[188:191],v[84:87]\n"
TR "v[180:181],v0 offset:28672\n"
MF "v[84:87],a[192:195],a[240:243],v[84:87]\n"
TR "v[182:183],v0 offset:30720\n"
MF "v[84:87],a[232:235],a[244:247],v[84:87]\n"
MF "v[76:79],a[228:231],a[244:247],v[76:79]\n"
TR "v[184:185],v0 offset:32768\n"
MF "v[76:79],a[188:191],a[240:243],v[76:79]\n"
TR "v[186:187],v0 offset:34816\n"
MF "v[76:79],a[148:151],v[188:191],v[76:79]\n"
MF "v[68:71],a[144:147],v[188:191],v[68:71]\n"
MF "v[68:71],a[184:187],a[240:243],v[68:71]\n"
MF "v[68:71],a[224:227],a[244:247],v[68:71]\n"
MF "v[60:63],a[220:223],a[244:247],v[60:63]\n"
MF "v[60:63],a[180:183],a[240:243],v[60:63]\n"
MF "v[60:63],a[140:143],v[188:191],v[60:63]\n"
"s_waitcnt lgkmcnt(0)\n"
"s_cmp_lg_u32 s85,5\n"
"s_cbranch_scc1 .Lpl15\n"
".Lpl19:\n"
MF "v[96:99],a[0:3],v[176:179],v[252:255]\n"
TR "v[188:189],v1 offset:24576\n"
MF "v[96:99],a[40:43],v[180:183],v[96:99]\n"
"s_add_u32 s82,s82,4096\n"
TR "v[190:191],v1 offset:26624\n"
MF "v[96:99],a[80:83],v[184:187],v[96:99]\n"
LD "v[224:227],v9,s[40:43],s82 offen sc1\n"
MF "v[104:107],a[84:87],v[184:187],v[252:255]\n"
TR "a[240:241],v1 offset:28672\n"
MF "v[104:107],a[44:47],v[180:183],v[104:107]\n"
TR "a[242:243],v1 offset:30720\n"
MF "v[104:107],a[4:7],v[176:179],v[104:107]\n"
MF "v[112:115],a[8:11],v[176:179],v[252:255]\n"
TR "a[244:245],v1 offset:32768\n"
MF "v[112:115],a[48:51],v[180:183],v[112:115]\n"
TR "a[246:247],v1 offset:34816\n"
MF "v[112:115],a[88:91],v[184:187],v[112:115]\n"
MF "v[120:123],a[92:95],v[184:187],v[252:255]\n"
MF "v[120:123],a[52:55],v[180:183],v[120:123]\n"
MF "v[120:123],a[12:15],v[176:179],v[120:123]\n"
MF "v[128:131],a[16:19],v[176:179],v[252:255]\n"
MF "v[128:131],a[56:59],v[180:183],v[128:131]\n"
MF "v[128:131],a[96:99],v[184:187],v[128:131]\n"
"s_waitcnt lgkmcnt(0)\n"
MF "v[132:135],a[96:99],a[244:247],v[252:255]\n"
TR "v[176:177],v2 offset:24576\n"
MF "v[132:135],a[56:59],a[240:243],v[132:135]\n"
TR "v[178:179],v2 offset:26624\n"
MF "v[132:135],a[16:19],v[188:191],v[132:135]\n"
MF "v[124:127],a[12:15],v[188:191],v[252:255]\n"
TR "v[180:181],v2 offset:28672\n"
MF "v[124:127],a[52:55],a[240:243],v[124:127]\n"
TR "v[182:183],v2 offset:30720\n"
MF "v[124:127],a[92:95],a[244:247],v[124:127]\n"
MF "v[116:119],a[88:91],a[244:247],v[252:255]\n"
TR "v[184:185],v2 offset:32768\n"
MF "v[116:119],a[48:51],a[240:243],v[116:119]\n"
TR "v[186:187],v2 offset:34816\n"
MF "v[116:119],a[8:11],v[188:191],v[116:119]\n"
MF "v[108:111],a[4:7],v[188:191],v[252:255]\n"
MF "v[108:111],a[44:47],a[240:243],v[108:111]\n"
MF "v[108:111],a[84:87],a[244:247],v[108:111]\n"
MF "v[100:103],a[80:83],a[244:247],v[252:255]\n"
MF "v[100:103],a[40:43],a[240:243],v[100:103]\n"
MF "v[100:103],a[0:3],v[188:191],v[100:103]\n"
"s_waitcnt lgkmcnt(0)\n"
MF "v[136:139],a[0:3],v[176:179],v[252:255]\n"
TR "v[188:189],v3 offset:24576\n"
MF "v[136:139],a[40:43],v[180:183],v[136:139]\n"
TR "v[190:191],v3 offset:26624\n"
MF "v[136:139],a[80:83],v[184:187],v[136:139]\n"
MF "v[144:147],a[84:87],v[184:187],v[252:255]\n"
TR "a[240:241],v3 offset:28672\n"
MF "v[144:147],a[44:47],v[180:183],v[144:147]\n"
TR "a[242:243],v3 offset:30720\n"
MF "v[144:147],a[4:7],v[176:179],v[144:147]\n"
MF "v[152:155],a[8:11],v[176:179],v[252:255]\n"
TR "a[244:245],v3 offset:32768\n"
MF "v[152:155],a[48:51],v[180:183],v[152:155]\n"
TR "a[246:247],v3 offset:34816\n"
MF "v[152:155],a[88:91],v[184:187],v[152:155]\n"
MF "v[160:163],a[92:95],v[184:187],v[252:255]\n"
MF "v[160:163],a[52:55],v[180:183],v[160:163]\n"
MF "v[160:163],a[12:15],v[176:179],v[160:163]\n"
MF "v[168:171],a[16:19],v[176:179],v[252:255]\n"
MF "v[168:171],a[56:59],v[180:183],v[168:171]\n"
"s_waitcnt lgkmcnt(0)\n"
MF "v[168:171],a[96:99],v[184:187],v[168:171]\n"
"s_barrier\n"
MF "v[172:175],a[96:99],a[244:247],v[252:255]\n"
TR "v[176:177],v0\n"
MF "v[172:175],a[56:59],a[240:243],v[172:175]\n"
TR "v[178:179],v0 offset:2048\n"
MF "v[172:175],a[16:19],v[188:191],v[172:175]\n"
"ds_read2st64_b32 v[192:193],v13 offset0:192 offset1:193\n"
MF "v[164:167],a[12:15],v[188:191],v[252:255]\n"
TR "v[180:181],v0 offset:4096\n"
MF "v[164:167],a[52:55],a[240:243],v[164:167]\n"
TR "v[182:183],v0 offset:6144\n"
MF "v[164:167],a[92:95],a[244:247],v[164:167]\n"
"ds_read2st64_b32 v[194:195],v13 offset0:194 offset1:195\n"
MF "v[156:159],a[88:91],a[244:247],v[252:255]\n"
TR "v[184:185],v0 offset:8192\n"
MF "v[156:159],a[48:51],a[240:243],v[156:159]\n"
TR "v[186:187],v0 offset:10240\n"
MF "v[156:159],a[8:11],v[188:191],v[156:159]\n"
"ds_read2st64_b32 v[196:197],v13 offset0:196 offset1:197\n"
MF "v[148:151],a[4:7],v[188:191],v[252:255]\n"
"ds_read2st64_b32 v[198:199],v13 offset0:198 offset1:199\n"
MF "v[148:151],a[44:47],a[240:243],v[148:151]\n"
"buffer_load_dword v10,v12,s[40:43],s86 offen sc1\n"
MF "v[148:151],a[84:87],a[244:247],v[148:151]\n"
MF "v[140:143],a[80:83],a[244:247],v[252:255]\n"
MF "v[140:143],a[40:43],a[240:243],v[140:143]\n"
MF "v[140:143],a[0:3],v[188:191],v[140:143]\n"
"s_waitcnt lgkmcnt(2)\n"
MF "v[96:99],a[120:123],v[176:179],v[96:99]\n"
"s_waitcnt lgkmcnt(0)\n"
TR "v[188:189],v1\n"
"v_add_f32 v16,v16,v192\n"
MF "v[96:99],a[160:163],v[180:183],v[96:99]\n"
TR "v[190:191],v1 offset:2048\n"
"v_add_f32 v17,v17,v193\n"
MF "v[96:99],a[200:203],v[184:187],v[96:99]\n"
"ds_read2st64_b32 v[200:201],v13 offset0:200 offset1:201\n"
"v_add_f32 v18,v18,v194\n"
"s_waitcnt vmcnt(2)\n"
"v_mov_b32 v14,s76\n"
"s_add_u32 s76,s76,1\n"
MF "v[104:107],a[204:207],v[184:187],v[104:107]\n"
TR "a[240:241],v1 offset:4096\n"
"v_add_f32 v19,v19,v195\n"
"buffer_store_dword v14,v15,s[40:43],s94 offen sc1\n"
MF "v[104:107],a[164:167],v[180:183],v[104:107]\n"
TR "a[242:243],v1 offset:6144\n"
"v_add_f32 v20,v20,v196\n"
MF "v[104:107],a[124:127],v[176:179],v[104:107]\n"
"ds_read2st64_b32 v[202:203],v13 offset0:202 offset1:203\n"
"v_add_f32 v21,v21,v197\n"
MF "v[112:115],a[128:131],v[176:179],v[112:115]\n"
TR "a[244:245],v1 offset:8192\n"
"v_add_f32 v22,v22,v198\n"
MF "v[112:115],a[168:171],v[180:183],v[112:115]\n"
TR "a[246:247],v1 offset:10240\n"
"v_add_f32 v23,v23,v199\n"
MF "v[112:115],a[208:211],v[184:187],v[112:115]\n"
"ds_read2st64_b32 v[204:205],v13 offset0:204 offset1:205\n"
CV "v16,v16,v17\n"
MF "v[120:123],a[212:215],v[184:187],v[120:123]\n"
"ds_read2st64_b32 v[206:207],v13 offset0:206 offset1:207\n"
CV "v17,v18,v19\n"
MF "v[120:123],a[172:175],v[180:183],v[120:123]\n"
"s_add_u32 s87,s90,0\n"
CV "v18,v20,v21\n"
CV "v19,v22,v23\n"
MF "v[120:123],a[132:135],v[176:179],v[120:123]\n"
MF "v[128:131],a[136:139],v[176:179],v[128:131]\n"
"buffer_store_dwordx4 v[16:19],v9,s[40:43],s87 offen sc1\n"
MF "v[128:131],a[176:179],v[180:183],v[128:131]\n"
MF "v[128:131],a[216:219],v[184:187],v[128:131]\n"
"s_waitcnt lgkmcnt(2)\n"
MF "v[132:135],a[216:219],a[244:247],v[132:135]\n"
"s_waitcnt lgkmcnt(0)\n"
TR "v[176:177],v2\n"
"v_add_f32 v24,v24,v200\n"
MF "v[132:135],a[176:179],a[240:243],v[132:135]\n"
TR "v[178:179],v2 offset:2048\n"
"v_add_f32 v25,v25,v201\n"
MF "v[132:135],a[136:139],v[188:191],v[132:135]\n"
"ds_read2st64_b32 v[192:193],v13 offset0:208 offset1:209\n"
"v_add_f32 v26,v26,v202\n"
MF "v[124:127],a[132:135],v[188:191],v[124:127]\n"
TR "v[180:181],v2 offset:4096\n"
"v_add_f32 v27,v27,v203\n"
MF "v[124:127],a[172:175],a[240:243],v[124:127]\n"
TR "v[182:183],v2 offset:6144\n"
"v_add_f32 v28,v28,v204\n"
MF "v[124:127],a[212:215],a[244:247],v[124:127]\n"
"ds_read2st64_b32 v[194:195],v13 offset0:210 offset1:211\n"
"v_add_f32 v29,v29,v205\n"
MF "v[116:119],a[208:211],a[244:247],v[116:119]\n"
TR "v[184:185],v2 offset:8192\n"
"v_add_f32 v30,v30,v206\n"
MF "v[116:119],a[168:171],a[240:243],v[116:119]\n"
TR "v[186:187],v2 offset:10240\n"
"v_add_f32 v31,v31,v207\n"
MF "v[116:119],a[128:131],v[188:191],v[116:119]\n"
"ds_read2st64_b32 v[196:197],v13 offset0:212 offset1:213\n"
CV "v24,v24,v25\n"
MF "v[108:111],a[124:127],v[188:191],v[108:111]\n"
"ds_read2st64_b32 v[198:199],v13 offset0:214 offset1:215\n"
CV "v25,v26,v27\n"
MF "v[108:111],a[164:167],a[240:243],v[108:111]\n"
"s_add_u32 s87,s90,1024\n"
CV "v26,v28,v29\n"
CV "v27,v30,v31\n"
MF "v[108:111],a[204:207],a[244:247],v[108:111]\n"
MF "v[100:103],a[200:203],a[244:247],v[100:103]\n"
"buffer_store_dwordx4 v[24:27],v9,s[40:43],s87 offen sc1\n"
MF "v[100:103],a[160:163],a[240:243],v[100:103]\n"
MF "v[100:103],a[120:123],v[188:191],v[100:103]\n"
"s_waitcnt lgkmcnt(2)\n"
MF "v[136:139],a[120:123],v[176:179],v[136:139]\n"
"s_waitcnt lgkmcnt(0)\n"
TR "v[188:189],v3\n"
"v_add_f32 v32,v32,v192\n"
MF "v[136:139],a[160:163],v[180:183],v[136:139]\n"
TR "v[190:191],v3 offset:2048\n"
"v_add_f32 v33,v33,v193\n"
MF "v[136:139],a[200:203],v[184:187],v[136:139]\n"
"ds_read2st64_b32 v[200:201],v13 offset0:216 offset1:217\n"
"v_add_f32 v34,v34,v194\n"
MF "v[144:147],a[204:207],v[184:187],v[144:147]\n"
TR "a[240:241],v3 offset:4096\n"
"v_add_f32 v35,v35,v195\n"
MF "v[144:147],a[164:167],v[180:183],v[144:147]\n"
TR "a[242:243],v3 offset:6144\n"
"v_add_f32 v36,v36,v196\n"
MF "v[144:147],a[124:127],v[176:179],v[144:147]\n"
"ds_read2st64_b32 v[202:203],v13 offset0:218 offset1:219\n"
"v_add_f32 v37,v37,v197\n"
MF "v[152:155],a[128:131],v[176:179],v[152:155]\n"
TR "a[244:245],v3 offset:8192\n"
"v_add_f32 v38,v38,v198\n"
MF "v[152:155],a[168:171],v[180:183],v[152:155]\n"
".Lpg32h:\n"
"s_cbranch_execz .Lisr4\n"
TR "a[246:247],v3 offset:10240\n"
"v_add_f32 v39,v39,v199\n"
MF "v[152:155],a[208:211],v[184:187],v[152:155]\n"
"ds_read2st64_b32 v[204:205],v13 offset0:220 offset1:221\n"
CV "v32,v32,v33\n"
MF "v[160:163],a[212:215],v[184:187],v[160:163]\n"
"ds_read2st64_b32 v[206:207],v13 offset0:222 offset1:223\n"
CV "v33,v34,v35\n"
MF "v[160:163],a[172:175],v[180:183],v[160:163]\n"
"s_add_u32 s87,s90,2048\n"
CV "v34,v36,v37\n"
CV "v35,v38,v39\n"
MF "v[160:163],a[132:135],v[176:179],v[160:163]\n"
MF "v[168:171],a[136:139],v[176:179],v[168:171]\n"
"buffer_store_dwordx4 v[32:35],v9,s[40:43],s87 offen sc1\n"
MF "v[168:171],a[176:179],v[180:183],v[168:171]\n"
"s_waitcnt lgkmcnt(0)\n"
MF "v[168:171],a[216:219],v[184:187],v[168:171]\n"
"s_barrier\n"
MF "v[172:175],a[216:219],a[244:247],v[172:175]\n"
TR "v[176:177],v0 offset:49152\n"
"v_add_f32 v40,v40,v200\n"
MF "v[172:175],a[176:179],a[240:243],v[172:175]\n"
"s_mov_b32 s82,s70\n"
TR "v[178:179],v0 offset:51200\n"
"v_add_f32 v41,v41,v201\n"
MF "v[172:175],a[136:139],v[188:191],v[172:175]\n"
"ds_read2st64_b32 v[192:193],v13 offset0:224 offset1:225\n"
"v_add_f32 v42,v42,v202\n"
MF "v[164:167],a[132:135],v[188:191],v[164:167]\n"
TR "v[180:181],v0 offset:53248\n"
"v_add_f32 v43,v43,v203\n"
MF "v[164:167],a[172:175],a[240:243],v[164:167]\n"
TR "v[182:183],v0 offset:55296\n"
"v_add_f32 v44,v44,v204\n"
MF "v[164:167],a[212:215],a[244:247],v[164:167]\n"
"ds_read2st64_b32 v[194:195],v13 offset0:226 offset1:227\n"
"v_add_f32 v45,v45,v205\n"
MF "v[156:159],a[208:211],a[244:247],v[156:159]\n"
TR "v[184:185],v0 offset:57344\n"
"v_add_f32 v46,v46,v206\n"
MF "v[156:159],a[168:171],a[240:243],v[156:159]\n"
TR "v[186:187],v0 offset:59392\n"
"v_add_f32 v47,v47,v207\n"
MF "v[156:159],a[128:131],v[188:191],v[156:159]\n"
"ds_read2st64_b32 v[196:197],v13 offset0:228 offset1:229\n"
CV "v40,v40,v41\n"
MF "v[148:151],a[124:127],v[188:191],v[148:151]\n"
"ds_read2st64_b32 v[198:199],v13 offset0:230 offset1:231\n"
CV "v41,v42,v43\n"
MF "v[148:151],a[164:167],a[240:243],v[148:151]\n"
"s_add_u32 s87,s90,3072\n"
CV "v42,v44,v45\n"
CV "v43,v46,v47\n"
MF "v[148:151],a[204:207],a[244:247],v[148:151]\n"
MF "v[140:143],a[200:203],a[244:247],v[140:143]\n"
"buffer_store_dwordx4 v[40:43],v9,s[40:43],s87 offen sc1\n"
MF "v[140:143],a[160:163],a[240:243],v[140:143]\n"
MF "v[140:143],a[120:123],v[188:191],v[140:143]\n"
"s_waitcnt lgkmcnt(2)\n"
MF "v[96:99],a[20:23],v[176:179],v[96:99]\n"
"s_waitcnt lgkmcnt(0)\n"
TR "v[188:189],v1 offset:49152\n"
"v_add_f32 v48,v48,v192\n"
MF "v[96:99],a[60:63],v[180:183],v[96:99]\n"
TR "v[190:191],v1 offset:51200\n"
"v_add_f32 v49,v49,v193\n"
MF "v[96:99],a[100:103],v[184:187],v[96:99]\n"
"ds_read2st64_b32 v[200:201],v13 offset0:0 offset1:1\n"
"v_add_f32 v50,v50,v194\n"
MF "v[104:107],a[104:107],v[184:187],v[104:107]\n"
TR "a[240:241],v1 offset:53248\n"
"v_add_f32 v51,v51,v195\n"
MF "v[104:107],a[64:67],v[180:183],v[104:107]\n"
TR "a[242:243],v1 offset:55296\n"
"v_add_f32 v52,v52,v196\n"
MF "v[104:107],a[24:27],v[176:179],v[104:107]\n"
"ds_read2st64_b32 v[202:203],v13 offset0:2 offset1:3\n"
"v_add_f32 v53,v53,v197\n"
MF "v[112:115],a[28:31],v[176:179],v[112:115]\n"
TR "a[244:245],v1 offset:57344\n"
"v_add_f32 v54,v54,v198\n"
MF "v[112:115],a[68:71],v[180:183],v[112:115]\n"
TR "a[246:247],v1 offset:59392\n"
"v_add_f32 v55,v55,v199\n"
MF "v[112:115],a[108:111],v[184:187],v[112:115]\n"
"ds_read2st64_b32 v[204:205],v13 offset0:4 offset1:5\n"
CV "v48,v48,v49\n"
MF "v[120:123],a[112:115],v[184:187],v[120:123]\n"
"ds_read2st64_b32 v[206:207],v13 offset0:6 offset1:7\n"
CV "v49,v50,v51\n"
MF "v[120:123],a[72:75],v[180:183],v[120:123]\n"
"s_add_u32 s87,s90,4096\n"
CV "v50,v52,v53\n"
CV "v51,v54,v55\n"
MF "v[120:123],a[32:35],v[176:179],v[120:123]\n"
MF "v[128:131],a[36:39],v[176:179],v[128:131]\n"
"buffer_store_dwordx4 v[48:51],v9,s[40:43],s87 offen sc1\n"
MF "v[128:131],a[76:79],v[180:183],v[128:131]\n"
MF "v[128:131],a[116:119],v[184:187],v[128:131]\n"
"s_waitcnt lgkmcnt(2)\n"
MF "v[132:135],a[116:119],a[244:247],v[132:135]\n"
"s_waitcnt lgkmcnt(0)\n"
TR "v[176:177],v2 offset:49152\n"
"v_add_f32 v56,v56,v200\n"
MF "v[132:135],a[76:79],a[240:243],v[132:135]\n"
TR "v[178:179],v2 offset:51200\n"
"v_add_f32 v57,v57,v201\n"
MF "v[132:135],a[36:39],v[188:191],v[132:135]\n"
"ds_read2st64_b32 v[192:193],v13 offset0:8 offset1:9\n"
"v_add_f32 v58,v58,v202\n"
MF "v[124:127],a[32:35],v[188:191],v[124:127]\n"
TR "v[180:181],v2 offset:53248\n"
"v_add_f32 v59,v59,v203\n"
MF "v[124:127],a[72:75],a[240:243],v[124:127]\n"
TR "v[182:183],v2 offset:55296\n"
"v_add_f32 v60,v60,v204\n"
MF "v[124:127],a[112:115],a[244:247],v[124:127]\n"
"ds_read2st64_b32 v[194:195],v13 offset0:10 offset1:11\n"
"v_add_f32 v61,v61,v205\n"
MF "v[116:119],a[108:111],a[244:247],v[116:119]\n"
TR "v[184:185],v2 offset:57344\n"
"v_add_f32 v62,v62,v206\n"
MF "v[116:119],a[68:71],a[240:243],v[116:119]\n"
TR "v[186:187],v2 offset:59392\n"
"v_add_f32 v63,v63,v207\n"
MF "v[116:119],a[28:31],v[188:191],v[116:119]\n"
"ds_read2st64_b32 v[196:197],v13 offset0:12 offset1:13\n"
CV "v56,v56,v57\n"
MF "v[108:111],a[24:27],v[188:191],v[108:111]\n"
"ds_read2st64_b32 v[198:199],v13 offset0:14 offset1:15\n"
CV "v57,v58,v59\n"
MF "v[108:111],a[64:67],a[240:243],v[108:111]\n"
"s_add_u32 s87,s90,10240\n"
CV "v58,v60,v61\n"
CV "v59,v62,v63\n"
MF "v[108:111],a[104:107],a[244:247],v[108:111]\n"
MF "v[100:103],a[100:103],a[244:247],v[100:103]\n"
"buffer_store_dwordx4 v[56:59],v9,s[40:43],s87 offen sc1\n"
MF "v[100:103],a[60:63],a[240:243],v[100:103]\n"
"s_waitcnt vmcnt(7)\n"
"s_cmp_lt_u32 s85,6\n"
MF "v[100:103],a[20:23],v[188:191],v[100:103]\n"
"s_cbranch_scc1 .Ljs10\n"
"v_mov_b32 v236,v228\n"
"v_mov_b32 v237,v229\n"
MF "v[136:139],a[252:255],v[208:211],v[136:139]\n"
MF "v[144:147],a[252:255],v[212:215],v[144:147]\n"
MF "v[152:155],a[252:255],v[216:219],v[152:155]\n"
MF "v[160:163],a[252:255],v[220:223],v[160:163]\n"
MF "v[168:171],a[252:255],v[224:227],v[168:171]\n"
MF "v[140:143],a[248:251],v[208:211],v[140:143]\n"
MF "v[148:151],a[248:251],v[212:215],v[148:151]\n"
MF "v[156:159],a[248:251],v[216:219],v[156:159]\n"
MF "v[164:167],a[248:251],v[220:223],v[164:167]\n"
MF "v[172:175],a[248:251],v[224:227],v[172:175]\n"
MF "v[248:251],v[230:233],a[250:253],v[248:251]\n"
MF "v[244:247],v[234:237],a[250:253],v[244:247]\n"
".Ljs10:\n"
"s_waitcnt lgkmcnt(2)\n"
MF "v[136:139],a[20:23],v[176:179],v[136:139]\n"
"s_waitcnt lgkmcnt(0)\n"
TR "v[188:189],v3 offset:49152\n"
"v_add_f32 v64,v64,v192\n"
MF "v[136:139],a[60:63],v[180:183],v[136:139]\n"
TR "v[190:191],v3 offset:51200\n"
"v_add_f32 v65,v65,v193\n"
MF "v[136:139],a[100:103],v[184:187],v[136:139]\n"
"ds_read2st64_b32 v[200:201],v13 offset0:16 offset1:17\n"
"v_add_f32 v66,v66,v194\n"
MF "v[144:147],a[104:107],v[184:187],v[144:147]\n"
TR "a[240:241],v3 offset:53248\n"
"v_add_f32 v67,v67,v195\n"
MF "v[144:147],a[64:67],v[180:183],v[144:147]\n"
TR "a[242:243],v3 offset:55296\n"
"v_add_f32 v68,v68,v196\n"
MF "v[144:147],a[24:27],v[176:179],v[144:147]\n"
"ds_read2st64_b32 v[202:203],v13 offset0:18 offset1:19\n"
"v_add_f32 v69,v69,v197\n"
MF "v[152:155],a[28:31],v[176:179],v[152:155]\n"
TR "a[244:245],v3 offset:57344\n"
"v_add_f32 v70,v70,v198\n"
MF "v[152:155],a[68:71],v[180:183],v[152:155]\n"
TR "a[246:247],v3 offset:59392\n"
"v_add_f32 v71,v71,v199\n"
MF "v[152:155],a[108:111],v[184:187],v[152:155]\n"
"ds_read2st64_b32 v[204:205],v13 offset0:20 offset1:21\n"
CV "v64,v64,v65\n"
MF "v[160:163],a[112:115],v[184:187],v[160:163]\n"
"ds_read2st64_b32 v[206:207],v13 offset0:22 offset1:23\n"
CV "v65,v66,v67\n"
MF "v[160:163],a[72:75],v[180:183],v[160:163]\n"
"s_add_u32 s87,s90,11264\n"
CV "v66,v68,v69\n"
CV "v67,v70,v71\n"
MF "v[160:163],a[32:35],v[176:179],v[160:163]\n"
"v_sub_u32 v10,v10,v11\n"
MF "v[168:171],a[36:39],v[176:179],v[168:171]\n"
"buffer_store_dwordx4 v[64:67],v9,s[40:43],s87 offen sc1\n"
MF "v[168:171],a[76:79],v[180:183],v[168:171]\n"
"s_waitcnt lgkmcnt(0)\n"
MF "v[168:171],a[116:119],v[184:187],v[168:171]\n"
"s_barrier\n"
MF "v[172:175],a[116:119],a[244:247],v[172:175]\n"
TR "v[176:177],v4\n"
"v_add_f32 v72,v72,v200\n"
MF "v[172:175],a[76:79],a[240:243],v[172:175]\n"
TR "v[178:179],v4 offset:2048\n"
"v_add_f32 v73,v73,v201\n"
MF "v[172:175],a[36:39],v[188:191],v[172:175]\n"
"ds_read2st64_b32 v[192:193],v13 offset0:24 offset1:25\n"
"v_add_f32 v74,v74,v202\n"
MF "v[164:167],a[32:35],v[188:191],v[164:167]\n"
TR "v[180:181],v4 offset:4096\n"
"v_add_f32 v75,v75,v203\n"
MF "v[164:167],a[72:75],a[240:243],v[164:167]\n"
TR "v[182:183],v4 offset:6144\n"
"v_add_f32 v76,v76,v204\n"
MF "v[164:167],a[112:115],a[244:247],v[164:167]\n"
"ds_read2st64_b32 v[194:195],v13 offset0:26 offset1:27\n"
"v_add_f32 v77,v77,v205\n"
MF "v[156:159],a[108:111],a[244:247],v[156:159]\n"
TR "v[184:185],v4 offset:8192\n"
"v_add_f32 v78,v78,v206\n"
MF "v[156:159],a[68:71],a[240:243],v[156:159]\n"
TR "v[186:187],v4 offset:10240\n"
"v_add_f32 v79,v79,v207\n"
MF "v[156:159],a[28:31],v[188:191],v[156:159]\n"
"ds_read2st64_b32 v[196:197],v13 offset0:28 offset1:29\n"
CV "v72,v72,v73\n"
MF "v[148:151],a[24:27],v[188:191],v[148:151]\n"
"ds_read2st64_b32 v[198:199],v13 offset0:30 offset1:31\n"
CV "v73,v74,v75\n"
MF "v[148:151],a[64:67],a[240:243],v[148:151]\n"
"s_add_u32 s87,s90,12288\n"
CV "v74,v76,v77\n"
CV "v75,v78,v79\n"
MF "v[148:151],a[104:107],a[244:247],v[148:151]\n"
".Lnf6:\n"
"v_readlane_b32 s58,v10,s67\n"
"s_cmp_lt_u32 s58,s85\n"
"s_cbranch_scc1 .Lsp6\n"
".Lnb6:\n"
MF "v[140:143],a[100:103],a[244:247],v[140:143]\n"
"buffer_store_dwordx4 v[72:75],v9,s[40:43],s87 offen sc1\n"
MF "v[140:143],a[60:63],a[240:243],v[140:143]\n"
"s_add_u32 s70,s70,20480\n"
".Lnf7:\n"
"v_cmp_gt_u32 vcc,4,v10\n"
"s_cbranch_vccnz .Lsp7\n"
".Lnb7:\n"
MF "v[140:143],a[20:23],v[188:191],v[140:143]\n"
"s_waitcnt lgkmcnt(2)\n"
MF "v[96:99],a[140:143],v[176:179],v[96:99]\n"
"s_waitcnt lgkmcnt(0)\n"
TR "v[188:189],v5\n"
"v_add_f32 v80,v80,v192\n"
MF "v[96:99],a[180:183],v[180:183],v[96:99]\n"
TR "v[190:191],v5 offset:2048\n"
"v_add_f32 v81,v81,v193\n"
MF "v[96:99],a[220:223],v[184:187],v[96:99]\n"
"ds_read2st64_b32 v[200:201],v13 offset0:32 offset1:33\n"
"v_add_f32 v82,v82,v194\n"
MF "v[104:107],a[224:227],v[184:187],v[104:107]\n"
TR "a[240:241],v5 offset:4096\n"
"v_add_f32 v83,v83,v195\n"
MF "v[104:107],a[184:187],v[180:183],v[104:107]\n"
TR "a[242:243],v5 offset:6144\n"
"v_add_f32 v84,v84,v196\n"
MF "v[104:107],a[144:147],v[176:179],v[104:107]\n"
"ds_read2st64_b32 v[202:203],v13 offset0:34 offset1:35\n"
"v_add_f32 v85,v85,v197\n"
MF "v[112:115],a[148:151],v[176:179],v[112:115]\n"
TR "a[244:245],v5 offset:8192\n"
"v_add_f32 v86,v86,v198\n"
MF "v[112:115],a[188:191],v[180:183],v[112:115]\n"
TR "a[246:247],v5 offset:10240\n"
"v_add_f32 v87,v87,v199\n"
MF "v[112:115],a[228:231],v[184:187],v[112:115]\n"
"ds_read2st64_b32 v[204:205],v13 offset0:36 offset1:37\n"
CV "v80,v80,v81\n"
MF "v[120:123],a[232:235],v[184:187],v[120:123]\n"
"s_add_u32 s2,s2,204800\n"
"ds_read2st64_b32 v[206:207],v13 offset0:38 offset1:39\n"
CV "v81,v82,v83\n"
MF "v[120:123],a[192:195],v[180:183],v[120:123]\n"
"s_add_u32 s87,s90,13312\n"
CV "v82,v84,v85\n"
CV "v83,v86,v87\n"
MF "v[120:123],a[152:155],v[176:179],v[120:123]\n"
LD "v[228:231],v9,s[40:43],s2 offen sc1\n"
MF "v[128:131],a[156:159],v[176:179],v[128:131]\n"
"buffer_store_dwordx4 v[80:83],v9,s[40:43],s87 offen sc1\n"
MF "v[128:131],a[196:199],v[180:183],v[128:131]\n"
"s_add_u32 s2,s2,204800\n"
MF "v[128:131],a[236:239],v[184:187],v[128:131]\n"
"s_waitcnt lgkmcnt(2)\n"
LD "v[232:235],v9,s[40:43],s2 offen sc1\n"
MF "v[132:135],a[236:239],a[244:247],v[132:135]\n"
"s_waitcnt lgkmcnt(0)\n"
TR "v[176:177],v6\n"
"v_add_f32 v88,v88,v200\n"
MF "v[132:135],a[196:199],a[240:243],v[132:135]\n"
TR "v[178:179],v6 offset:2048\n"
"v_add_f32 v89,v89,v201\n"
MF "v[132:135],a[156:159],v[188:191],v[132:135]\n"
"v_add_f32 v90,v90,v202\n"
"v_add_f32 v91,v91,v203\n"
MF "v[124:127],a[152:155],v[188:191],v[124:127]\n"
TR "v[180:181],v6 offset:4096\n"
"v_add_f32 v92,v92,v204\n"
MF "v[124:127],a[192:195],a[240:243],v[124:127]\n"
TR "v[182:183],v6 offset:6144\n"
"v_add_f32 v93,v93,v205\n"
MF "v[124:127],a[232:235],a[244:247],v[124:127]\n"
"v_add_f32 v94,v94,v206\n"
"v_add_f32 v95,v95,v207\n"
MF "v[116:119],a[228:231],a[244:247],v[116:119]\n"
TR "v[184:185],v6 offset:8192\n"
CV "v88,v88,v89\n"
MF "v[116:119],a[188:191],a[240:243],v[116:119]\n"
TR "v[186:187],v6 offset:10240\n"
CV "v89,v90,v91\n"
MF "v[116:119],a[148:151],v[188:191],v[116:119]\n"
"s_add_u32 s87,s90,14336\n"
CV "v90,v92,v93\n"
CV "v91,v94,v95\n"
MF "v[108:111],a[144:147],v[188:191],v[108:111]\n"
LD "v[208:211],v9,s[40:43],s82 offen sc1\n"
MF "v[108:111],a[184:187],a[240:243],v[108:111]\n"
MF "v[108:111],a[224:227],a[244:247],v[108:111]\n"
MF "v[100:103],a[220:223],a[244:247],v[100:103]\n"
"buffer_store_dwordx4 v[88:91],v9,s[40:43],s87 offen sc1\n"
MF "v[100:103],a[180:183],a[240:243],v[100:103]\n"
"s_mov_b32 s90,s91\n"
"s_add_u32 s91,s91,20480\n"
MF "v[100:103],a[140:143],v[188:191],v[100:103]\n"
"s_waitcnt lgkmcnt(0)\n"
MF "v[136:139],a[140:143],v[176:179],v[136:139]\n"
TR "v[188:189],v7\n"
MF "v[136:139],a[180:183],v[180:183],v[136:139]\n"
TR "v[190:191],v7 offset:2048\n"
MF "v[136:139],a[220:223],v[184:187],v[136:139]\n"
LD "v[212:215],v9,s[40:43],s82 offen offset:1024 sc1\n"
MF "v[144:147],a[224:227],v[184:187],v[144:147]\n"
TR "a[240:241],v7 offset:4096\n"
MF "v[144:147],a[184:187],v[180:183],v[144:147]\n"
TR "a[242:243],v7 offset:6144\n"
MF "v[144:147],a[144:147],v[176:179],v[144:147]\n"
MF "v[152:155],a[148:151],v[176:179],v[152:155]\n"
TR "a[244:245],v7 offset:8192\n"
MF "v[152:155],a[188:191],v[180:183],v[152:155]\n"
TR "a[246:247],v7 offset:10240\n"
MF "v[152:155],a[228:231],v[184:187],v[152:155]\n"
MF "v[160:163],a[232:235],v[184:187],v[160:163]\n"
MF "v[160:163],a[192:195],v[180:183],v[160:163]\n"
LD "v[216:219],v9,s[40:43],s82 offen offset:2048 sc1\n"
MF "v[160:163],a[152:155],v[176:179],v[160:163]\n"
MF "v[168:171],a[156:159],v[176:179],v[168:171]\n"
MF "v[168:171],a[196:199],v[180:183],v[168:171]\n"
"s_waitcnt lgkmcnt(0)\n"
MF "v[168:171],a[236:239],v[184:187],v[168:171]\n"
"s_barrier\n"
MF "v[172:175],a[236:239],a[244:247],v[172:175]\n"
TR "v[176:177],v0 offset:24576\n"
MF "v[172:175],a[196:199],a[240:243],v[172:175]\n"
"s_add_u32 s85,s85,1\n"
TR "v[178:179],v0 offset:26624\n"
MF "v[172:175],a[156:159],v[188:191],v[172:175]\n"
MF "v[164:167],a[152:155],v[188:191],v[164:167]\n"
TR "v[180:181],v0 offset:28672\n"
MF "v[164:167],a[192:195],a[240:243],v[164:167]\n"
TR "v[182:183],v0 offset:30720\n"
MF "v[164:167],a[232:235],a[244:247],v[164:167]\n"
LD "v[220:223],v9,s[40:43],s82 offen offset:3072 sc1\n"
MF "v[156:159],a[228:231],a[244:247],v[156:159]\n"
TR "v[184:185],v0 offset:32768\n"
MF "v[156:159],a[188:191],a[240:243],v[156:159]\n"
TR "v[186:187],v0 offset:34816\n"
MF "v[156:159],a[148:151],v[188:191],v[156:159]\n"
MF "v[148:151],a[144:147],v[188:191],v[148:151]\n"
MF "v[148:151],a[184:187],a[240:243],v[148:151]\n"
MF "v[148:151],a[224:227],a[244:247],v[148:151]\n"
MF "v[140:143],a[220:223],a[244:247],v[140:143]\n"
MF "v[140:143],a[180:183],a[240:243],v[140:143]\n"
MF "v[140:143],a[140:143],v[188:191],v[140:143]\n"
"s_waitcnt lgkmcnt(0)\n"
MF "v[16:19],a[0:3],v[176:179],v[252:255]\n"
TR "v[188:189],v1 offset:24576\n"
MF "v[16:19],a[40:43],v[180:183],v[16:19]\n"
"s_add_u32 s82,s82,4096\n"
TR "v[190:191],v1 offset:26624\n"
MF "v[16:19],a[80:83],v[184:187],v[16:19]\n"
LD "v[224:227],v9,s[40:43],s82 offen sc1\n"
MF "v[24:27],a[84:87],v[184:187],v[252:255]\n"
TR "a[240:241],v1 offset:28672\n"
MF "v[24:27],a[44:47],v[180:183],v[24:27]\n"
TR "a[242:243],v1 offset:30720\n"
MF "v[24:27],a[4:7],v[176:179],v[24:27]\n"
MF "v[32:35],a[8:11],v[176:179],v[252:255]\n"
TR "a[244:245],v1 offset:32768\n"
MF "v[32:35],a[48:51],v[180:183],v[32:35]\n"
TR "a[246:247],v1 offset:34816\n"
MF "v[32:35],a[88:91],v[184:187],v[32:35]\n"
MF "v[40:43],a[92:95],v[184:187],v[252:255]\n"
MF "v[40:43],a[52:55],v[180:183],v[40:43]\n"
MF "v[40:43],a[12:15],v[176:179],v[40:43]\n"
MF "v[48:51],a[16:19],v[176:179],v[252:255]\n"
MF "v[48:51],a[56:59],v[180:183],v[48:51]\n"
MF "v[48:51],a[96:99],v[184:187],v[48:51]\n"
"s_waitcnt lgkmcnt(0)\n"
MF "v[52:55],a[96:99],a[244:247],v[252:255]\n"
TR "v[176:177],v2 offset:24576\n"
MF "v[52:55],a[56:59],a[240:243],v[52:55]\n"
TR "v[178:179],v2 offset:26624\n"
MF "v[52:55],a[16:19],v[188:191],v[52:55]\n"
MF "v[44:47],a[12:15],v[188:191],v[252:255]\n"
TR "v[180:181],v2 offset:28672\n"
MF "v[44:47],a[52:55],a[240:243],v[44:47]\n"
TR "v[182:183],v2 offset:30720\n"
MF "v[44:47],a[92:95],a[244:247],v[44:47]\n"
MF "v[36:39],a[88:91],a[244:247],v[252:255]\n"
TR "v[184:185],v2 offset:32768\n"
MF "v[36:39],a[48:51],a[240:243],v[36:39]\n"
TR "v[186:187],v2 offset:34816\n"
MF "v[36:39],a[8:11],v[188:191],v[36:39]\n"
MF "v[28:31],a[4:7],v[188:191],v[252:255]\n"
MF "v[28:31],a[44:47],a[240:243],v[28:31]\n"
MF "v[28:31],a[84:87],a[244:247],v[28:31]\n"
MF "v[20:23],a[80:83],a[244:247],v[252:255]\n"
MF "v[20:23],a[40:43],a[240:243],v[20:23]\n"
MF "v[20:23],a[0:3],v[188:191],v[20:23]\n"
"s_waitcnt lgkmcnt(0)\n"
MF "v[56:59],a[0:3],v[176:179],v[252:255]\n"
TR "v[188:189],v3 offset:24576\n"
MF "v[56:59],a[40:43],v[180:183],v[56:59]\n"
TR "v[190:191],v3 offset:26624\n"
MF "v[56:59],a[80:83],v[184:187],v[56:59]\n"
MF "v[64:67],a[84:87],v[184:187],v[252:255]\n"
TR "a[240:241],v3 offset:28672\n"
MF "v[64:67],a[44:47],v[180:183],v[64:67]\n"
TR "a[242:243],v3 offset:30720\n"
MF "v[64:67],a[4:7],v[176:179],v[64:67]\n"
MF "v[72:75],a[8:11],v[176:179],v[252:255]\n"
TR "a[244:245],v3 offset:32768\n"
MF "v[72:75],a[48:51],v[180:183],v[72:75]\n"
TR "a[246:247],v3 offset:34816\n"
MF "v[72:75],a[88:91],v[184:187],v[72:75]\n"
MF "v[80:83],a[92:95],v[184:187],v[252:255]\n"
MF "v[80:83],a[52:55],v[180:183],v[80:83]\n"
MF "v[80:83],a[12:15],v[176:179],v[80:83]\n"
MF "v[88:91],a[16:19],v[176:179],v[252:255]\n"
MF "v[88:91],a[56:59],v[180:183],v[88:91]\n"
"s_waitcnt lgkmcnt(0)\n"
MF "v[88:91],a[96:99],v[184:187],v[88:91]\n"
"s_barrier\n"
MF "v[92:95],a[96:99],a[244:247],v[252:255]\n"
TR "v[176:177],v0\n"
MF "v[92:95],a[56:59],a[240:243],v[92:95]\n"
TR "v[178:179],v0 offset:2048\n"
MF "v[92:95],a[16:19],v[188:191],v[92:95]\n"
"ds_read2st64_b32 v[192:193],v13 offset0:192 offset1:193\n"
MF "v[84:87],a[12:15],v[188:191],v[252:255]\n"
TR "v[180:181],v0 offset:4096\n"
MF "v[84:87],a[52:55],a[240:243],v[84:87]\n"
TR "v[182:183],v0 offset:6144\n"
MF "v[84:87],a[92:95],a[244:247],v[84:87]\n"
"ds_read2st64_b32 v[194:195],v13 offset0:194 offset1:195\n"
MF "v[76:79],a[88:91],a[244:247],v[252:255]\n"
TR "v[184:185],v0 offset:8192\n"
MF "v[76:79],a[48:51],a[240:243],v[76:79]\n"
TR "v[186:187],v0 offset:10240\n"
MF "v[76:79],a[8:11],v[188:191],v[76:79]\n"
"ds_read2st64_b32 v[196:197],v13 offset0:196 offset1:197\n"
MF "v[68:71],a[4:7],v[188:191],v[252:255]\n"
"ds_read2st64_b32 v[198:199],v13 offset0:198 offset1:199\n"
MF "v[68:71],a[44:47],a[240:243],v[68:71]\n"
"buffer_load_dword v10,v12,s[40:43],s86 offen sc1\n"
MF "v[68:71],a[84:87],a[244:247],v[68:71]\n"
MF "v[60:63],a[80:83],a[244:247],v[252:255]\n"
MF "v[60:63],a[40:43],a[240:243],v[60:63]\n"
MF "v[60:63],a[0:3],v[188:191],v[60:63]\n"
"s_waitcnt lgkmcnt(2)\n"
MF "v[16:19],a[120:123],v[176:179],v[16:19]\n"
"s_waitcnt lgkmcnt(0)\n"
TR "v[188:189],v1\n"
"v_add_f32 v96,v96,v192\n"
MF "v[16:19],a[160:163],v[180:183],v[16:19]\n"
TR "v[190:191],v1 offset:2048\n"
"v_add_f32 v97,v97,v193\n"
MF "v[16:19],a[200:203],v[184:187],v[16:19]\n"
"ds_read2st64_b32 v[200:201],v13 offset0:200 offset1:201\n"
"v_add_f32 v98,v98,v194\n"
"s_waitcnt vmcnt(5)\n"
"v_mov_b32 v14,s76\n"
"s_add_u32 s76,s76,1\n"
MF "v[24:27],a[204:207],v[184:187],v[24:27]\n"
TR "a[240:241],v1 offset:4096\n"
"v_add_f32 v99,v99,v195\n"
"buffer_store_dword v14,v15,s[40:43],s94 offen sc1\n"
MF "v[24:27],a[164:167],v[180:183],v[24:27]\n"
TR "a[242:243],v1 offset:6144\n"
"v_add_f32 v100,v100,v196\n"
MF "v[24:27],a[124:127],v[176:179],v[24:27]\n"
"ds_read2st64_b32 v[202:203],v13 offset0:202 offset1:203\n"
"v_add_f32 v101,v101,v197\n"
MF "v[32:35],a[128:131],v[176:179],v[32:35]\n"
TR "a[244:245],v1 offset:8192\n"
"v_add_f32 v102,v102,v198\n"
MF "v[32:35],a[168:171],v[180:183],v[32:35]\n"
TR "a[246:247],v1 offset:10240\n"
"v_add_f32 v103,v103,v199\n"
MF "v[32:35],a[208:211],v[184:187],v[32:35]\n"
"ds_read2st64_b32 v[204:205],v13 offset0:204 offset1:205\n"
CV "v96,v96,v97\n"
MF "v[40:43],a[212:215],v[184:187],v[40:43]\n"
"ds_read2st64_b32 v[206:207],v13 offset0:206 offset1:207\n"
CV "v97,v98,v99\n"
MF "v[40:43],a[172:175],v[180:183],v[40:43]\n"
"s_add_u32 s87,s90,0\n"
CV "v98,v100,v101\n"
CV "v99,v102,v103\n"
MF "v[40:43],a[132:135],v[176:179],v[40:43]\n"
MF "v[48:51],a[136:139],v[176:179],v[48:51]\n"
"buffer_store_dwordx4 v[96:99],v9,s[40:43],s87 offen sc1\n"
MF "v[48:51],a[176:179],v[180:183],v[48:51]\n"
MF "v[48:51],a[216:219],v[184:187],v[48:51]\n"
"s_waitcnt lgkmcnt(2)\n"
MF "v[52:55],a[216:219],a[244:247],v[52:55]\n"
"s_waitcnt lgkmcnt(0)\n"
TR "v[176:177],v2\n"
"v_add_f32 v104,v104,v200\n"
MF "v[52:55],a[176:179],a[240:243],v[52:55]\n"
TR "v[178:179],v2 offset:2048\n"
"v_add_f32 v105,v105,v201\n"
MF "v[52:55],a[136:139],v[188:191],v[52:55]\n"
"ds_read2st64_b32 v[192:193],v13 offset0:208 offset1:209\n"
"v_add_f32 v106,v106,v202\n"
MF "v[44:47],a[132:135],v[188:191],v[44:47]\n"
TR "v[180:181],v2 offset:4096\n"
"v_add_f32 v107,v107,v203\n"
MF "v[44:47],a[172:175],a[240:243],v[44:47]\n"
TR "v[182:183],v2 offset:6144\n"
"v_add_f32 v108,v108,v204\n"
MF "v[44:47],a[212:215],a[244:247],v[44:47]\n"
"ds_read2st64_b32 v[194:195],v13 offset0:210 offset1:211\n"
"v_add_f32 v109,v109,v205\n"
MF "v[36:39],a[208:211],a[244:247],v[36:39]\n"
TR "v[184:185],v2 offset:8192\n"
"v_add_f32 v110,v110,v206\n"
MF "v[36:39],a[168:171],a[240:243],v[36:39]\n"
TR "v[186:187],v2 offset:10240\n"
"v_add_f32 v111,v111,v207\n"
MF "v[36:39],a[128:131],v[188:191],v[36:39]\n"
"ds_read2st64_b32 v[196:197],v13 offset0:212 offset1:213\n"
CV "v104,v104,v105\n"
MF "v[28:31],a[124:127],v[188:191],v[28:31]\n"
"ds_read2st64_b32 v[198:199],v13 offset0:214 offset1:215\n"
CV "v105,v106,v107\n"
MF "v[28:31],a[164:167],a[240:243],v[28:31]\n"
"s_add_u32 s87,s90,1024\n"
CV "v106,v108,v109\n"
CV "v107,v110,v111\n"
MF "v[28:31],a[204:207],a[244:247],v[28:31]\n"
MF "v[20:23],a[200:203],a[244:247],v[20:23]\n"
"buffer_store_dwordx4 v[104:107],v9,s[40:43],s87 offen sc1\n"
MF "v[20:23],a[160:163],a[240:243],v[20:23]\n"
MF "v[20:23],a[120:123],v[188:191],v[20:23]\n"
"s_waitcnt lgkmcnt(2)\n"
MF "v[56:59],a[120:123],v[176:179],v[56:59]\n"
"s_waitcnt lgkmcnt(0)\n"
TR "v[188:189],v3\n"
"v_add_f32 v112,v112,v192\n"
MF "v[56:59],a[160:163],v[180:183],v[56:59]\n"
TR "v[190:191],v3 offset:2048\n"
"v_add_f32 v113,v113,v193\n"
MF "v[56:59],a[200:203],v[184:187],v[56:59]\n"
".Lpg33h:\n"
"s_cbranch_execz .Lisr4\n"
"ds_read2st64_b32 v[200:201],v13 offset0:216 offset1:217\n"
"v_add_f32 v114,v114,v194\n"
MF "v[64:67],a[204:207],v[184:187],v[64:67]\n"
TR "a[240:241],v3 offset:4096\n"
"v_add_f32 v115,v115,v195\n"
MF "v[64:67],a[164:167],v[180:183],v[64:67]\n"
TR "a[242:243],v3 offset:6144\n"
"v_add_f32 v116,v116,v196\n"
MF "v[64:67],a[124:127],v[176:179],v[64:67]\n"
"ds_read2st64_b32 v[202:203],v13 offset0:218 offset1:219\n"
"v_add_f32 v117,v117,v197\n"
MF "v[72:75],a[128:131],v[176:179],v[72:75]\n"
TR "a[244:245],v3 offset:8192\n"
"v_add_f32 v118,v118,v198\n"
MF "v[72:75],a[168:171],v[180:183],v[72:75]\n"
TR "a[246:247],v3 offset:10240\n"
"v_add_f32 v119,v119,v199\n"
MF "v[72:75],a[208:211],v[184:187],v[72:75]\n"
"ds_read2st64_b32 v[204:205],v13 offset0:220 offset1:221\n"
CV "v112,v112,v113\n"
MF "v[80:83],a[212:215],v[184:187],v[80:83]\n"
"ds_read2st64_b32 v[206:207],v13 offset0:222 offset1:223\n"
CV "v113,v114,v115\n"
MF "v[80:83],a[172:175],v[180:183],v[80:83]\n"
"s_add_u32 s87,s90,2048\n"
CV "v114,v116,v117\n"
CV "v115,v118,v119\n"
MF "v[80:83],a[132:135],v[176:179],v[80:83]\n"
MF "v[88:91],a[136:139],v[176:179],v[88:91]\n"
"buffer_store_dwordx4 v[112:115],v9,s[40:43],s87 offen sc1\n"
MF "v[88:91],a[176:179],v[180:183],v[88:91]\n"
"s_waitcnt lgkmcnt(0)\n"
MF "v[88:91],a[216:219],v[184:187],v[88:91]\n"
"s_barrier\n"
MF "v[92:95],a[216:219],a[244:247],v[92:95]\n"
TR "v[176:177],v0 offset:49152\n"
"v_add_f32 v120,v120,v200\n"
MF "v[92:95],a[176:179],a[240:243],v[92:95]\n"
"s_mov_b32 s82,s70\n"
TR "v[178:179],v0 offset:51200\n"
"v_add_f32 v121,v121,v201\n"
MF "v[92:95],a[136:139],v[188:191],v[92:95]\n"
"ds_read2st64_b32 v[192:193],v13 offset0:224 offset1:225\n"
"v_add_f32 v122,v122,v202\n"
MF "v[84:87],a[132:135],v[188:191],v[84:87]\n"
TR "v[180:181],v0 offset:53248\n"
"v_add_f32 v123,v123,v203\n"
MF "v[84:87],a[172:175],a[240:243],v[84:87]\n"
TR "v[182:183],v0 offset:55296\n"
"v_add_f32 v124,v124,v204\n"
MF "v[84:87],a[212:215],a[244:247],v[84:87]\n"
"ds_read2st64_b32 v[194:195],v13 offset0:226 offset1:227\n"
"v_add_f32 v125,v125,v205\n"
MF "v[76:79],a[208:211],a[244:247],v[76:79]\n"
TR "v[184:185],v0 offset:57344\n"
"v_add_f32 v126,v126,v206\n"
MF "v[76:79],a[168:171],a[240:243],v[76:79]\n"
TR "v[186:187],v0 offset:59392\n"
"v_add_f32 v127,v127,v207\n"
MF "v[76:79],a[128:131],v[188:191],v[76:79]\n"
"ds_read2st64_b32 v[196:197],v13 offset0:228 offset1:229\n"
CV "v120,v120,v121\n"
MF "v[68:71],a[124:127],v[188:191],v[68:71]\n"
"ds_read2st64_b32 v[198:199],v13 offset0:230 offset1:231\n"
CV "v121,v122,v123\n"
MF "v[68:71],a[164:167],a[240:243],v[68:71]\n"
"s_add_u32 s87,s90,3072\n"
CV "v122,v124,v125\n"
CV "v123,v126,v127\n"
MF "v[68:71],a[204:207],a[244:247],v[68:71]\n"
MF "v[60:63],a[200:203],a[244:247],v[60:63]\n"
"buffer_store_dwordx4 v[120:123],v9,s[40:43],s87 offen sc1\n"
MF "v[60:63],a[160:163],a[240:243],v[60:63]\n"
MF "v[60:63],a[120:123],v[188:191],v[60:63]\n"
"s_waitcnt lgkmcnt(2)\n"
MF "v[16:19],a[20:23],v[176:179],v[16:19]\n"
"s_waitcnt lgkmcnt(0)\n"
TR "v[188:189],v1 offset:49152\n"
"v_add_f32 v128,v128,v192\n"
MF "v[16:19],a[60:63],v[180:183],v[16:19]\n"
TR "v[190:191],v1 offset:51200\n"
"v_add_f32 v129,v129,v193\n"
MF "v[16:19],a[100:103],v[184:187],v[16:19]\n"
"ds_read2st64_b32 v[200:201],v13 offset0:0 offset1:1\n"
"v_add_f32 v130,v130,v194\n"
MF "v[24:27],a[104:107],v[184:187],v[24:27]\n"
TR "a[240:241],v1 offset:53248\n"
"v_add_f32 v131,v131,v195\n"
MF "v[24:27],a[64:67],v[180:183],v[24:27]\n"
TR "a[242:243],v1 offset:55296\n"
"v_add_f32 v132,v132,v196\n"
MF "v[24:27],a[24:27],v[176:179],v[24:27]\n"
"ds_read2st64_b32 v[202:203],v13 offset0:2 offset1:3\n"
"v_add_f32 v133,v133,v197\n"
MF "v[32:35],a[28:31],v[176:179],v[32:35]\n"
TR "a[244:245],v1 offset:57344\n"
"v_add_f32 v134,v134,v198\n"
MF "v[32:35],a[68:71],v[180:183],v[32:35]\n"
TR "a[246:247],v1 offset:59392\n"
"v_add_f32 v135,v135,v199\n"
MF "v[32:35],a[108:111],v[184:187],v[32:35]\n"
"ds_read2st64_b32 v[204:205],v13 offset0:4 offset1:5\n"
CV "v128,v128,v129\n"
MF "v[40:43],a[112:115],v[184:187],v[40:43]\n"
"ds_read2st64_b32 v[206:207],v13 offset0:6 offset1:7\n"
CV "v129,v130,v131\n"
MF "v[40:43],a[72:75],v[180:183],v[40:43]\n"
"s_add_u32 s87,s90,4096\n"
CV "v130,v132,v133\n"
CV "v131,v134,v135\n"
MF "v[40:43],a[32:35],v[176:179],v[40:43]\n"
MF "v[48:51],a[36:39],v[176:179],v[48:51]\n"
"buffer_store_dwordx4 v[128:131],v9,s[40:43],s87 offen sc1\n"
MF "v[48:51],a[76:79],v[180:183],v[48:51]\n"
MF "v[48:51],a[116:119],v[184:187],v[48:51]\n"
"s_waitcnt lgkmcnt(2)\n"
MF "v[52:55],a[116:119],a[244:247],v[52:55]\n"
"s_waitcnt lgkmcnt(0)\n"
TR "v[176:177],v2 offset:49152\n"
"v_add_f32 v136,v136,v200\n"
MF "v[52:55],a[76:79],a[240:243],v[52:55]\n"
TR "v[178:179],v2 offset:51200\n"
"v_add_f32 v137,v137,v201\n"
MF "v[52:55],a[36:39],v[188:191],v[52:55]\n"
"ds_read2st64_b32 v[192:193],v13 offset0:8 offset1:9\n"
"v_add_f32 v138,v138,v202\n"
MF "v[44:47],a[32:35],v[188:191],v[44:47]\n"
TR "v[180:181],v2 offset:53248\n"
"v_add_f32 v139,v139,v203\n"
MF "v[44:47],a[72:75],a[240:243],v[44:47]\n"
TR "v[182:183],v2 offset:55296\n"
"v_add_f32 v140,v140,v204\n"
MF "v[44:47],a[112:115],a[244:247],v[44:47]\n"
"ds_read2st64_b32 v[194:195],v13 offset0:10 offset1:11\n"
"v_add_f32 v141,v141,v205\n"
MF "v[36:39],a[108:111],a[244:247],v[36:39]\n"
TR "v[184:185],v2 offset:57344\n"
"v_add_f32 v142,v142,v206\n"
MF "v[36:39],a[68:71],a[240:243],v[36:39]\n"
TR "v[186:187],v2 offset:59392\n"
"v_add_f32 v143,v143,v207\n"
MF "v[36:39],a[28:31],v[188:191],v[36:39]\n"
"ds_read2st64_b32 v[196:197],v13 offset0:12 offset1:13\n"
CV "v136,v136,v137\n"
MF "v[28:31],a[24:27],v[188:191],v[28:31]\n"
"ds_read2st64_b32 v[198:199],v13 offset0:14 offset1:15\n"
CV "v137,v138,v139\n"
MF "v[28:31],a[64:67],a[240:243],v[28:31]\n"
"s_add_u32 s87,s90,10240\n"
CV "v138,v140,v141\n"
CV "v139,v142,v143\n"
MF "v[28:31],a[104:107],a[244:247],v[28:31]\n"
MF "v[20:23],a[100:103],a[244:247],v[20:23]\n"
"buffer_store_dwordx4 v[136:139],v9,s[40:43],s87 offen sc1\n"
MF "v[20:23],a[60:63],a[240:243],v[20:23]\n"
"s_waitcnt vmcnt(7)\n"
MF "v[20:23],a[20:23],v[188:191],v[20:23]\n"
"v_mov_b32 v236,v228\n"
"v_mov_b32 v237,v229\n"
MF "v[56:59],a[252:255],v[208:211],v[56:59]\n"
MF "v[64:67],a[252:255],v[212:215],v[64:67]\n"
MF "v[72:75],a[252:255],v[216:219],v[72:75]\n"
MF "v[80:83],a[252:255],v[220:223],v[80:83]\n"
MF "v[88:91],a[252:255],v[224:227],v[88:91]\n"
MF "v[60:63],a[248:251],v[208:211],v[60:63]\n"
MF "v[68:71],a[248:251],v[212:215],v[68:71]\n"
MF "v[76:79],a[248:251],v[216:219],v[76:79]\n"
MF "v[84:87],a[248:251],v[220:223],v[84:87]\n"
MF "v[92:95],a[248:251],v[224:227],v[92:95]\n"
MF "v[248:251],v[230:233],a[250:253],v[248:251]\n"
MF "v[244:247],v[234:237],a[250:253],v[244:247]\n"
"s_waitcnt lgkmcnt(2)\n"
MF "v[56:59],a[20:23],v[176:179],v[56:59]\n"
"s_waitcnt lgkmcnt(0)\n"
TR "v[188:189],v3 offset:49152\n"
"v_add_f32 v144,v144,v192\n"
MF "v[56:59],a[60:63],v[180:183],v[56:59]\n"
TR "v[190:191],v3 offset:51200\n"
"v_add_f32 v145,v145,v193\n"
MF "v[56:59],a[100:103],v[184:187],v[56:59]\n"
"ds_read2st64_b32 v[200:201],v13 offset0:16 offset1:17\n"
"v_add_f32 v146,v146,v194\n"
MF "v[64:67],a[104:107],v[184:187],v[64:67]\n"
TR "a[240:241],v3 offset:53248\n"
"v_add_f32 v147,v147,v195\n"
MF "v[64:67],a[64:67],v[180:183],v[64:67]\n"
TR "a[242:243],v3 offset:55296\n"
"v_add_f32 v148,v148,v196\n"
MF "v[64:67],a[24:27],v[176:179],v[64:67]\n"
"ds_read2st64_b32 v[202:203],v13 offset0:18 offset1:19\n"
"v_add_f32 v149,v149,v197\n"
MF "v[72:75],a[28:31],v[176:179],v[72:75]\n"
TR "a[244:245],v3 offset:57344\n"
"v_add_f32 v150,v150,v198\n"
MF "v[72:75],a[68:71],v[180:183],v[72:75]\n"
TR "a[246:247],v3 offset:59392\n"
"v_add_f32 v151,v151,v199\n"
MF "v[72:75],a[108:111],v[184:187],v[72:75]\n"
"ds_read2st64_b32 v[204:205],v13 offset0:20 offset1:21\n"
CV "v144,v144,v145\n"
MF "v[80:83],a[112:115],v[184:187],v[80:83]\n"
"ds_read2st64_b32 v[206:207],v13 offset0:22 offset1:23\n"
CV "v145,v146,v147\n"
MF "v[80:83],a[72:75],v[180:183],v[80:83]\n"
"s_add_u32 s87,s90,11264\n"
CV "v146,v148,v149\n"
CV "v147,v150,v151\n"
MF "v[80:83],a[32:35],v[176:179],v[80:83]\n"
"v_sub_u32 v10,v10,v11\n"
MF "v[88:91],a[36:39],v[176:179],v[88:91]\n"
"buffer_store_dwordx4 v[144:147],v9,s[40:43],s87 offen sc1\n"
MF "v[88:91],a[76:79],v[180:183],v[88:91]\n"
"s_waitcnt lgkmcnt(0)\n"
MF "v[88:91],a[116:119],v[184:187],v[88:91]\n"
"s_barrier\n"
MF "v[92:95],a[116:119],a[244:247],v[92:95]\n"
TR "v[176:177],v4\n"
"v_add_f32 v152,v152,v200\n"
MF "v[92:95],a[76:79],a[240:243],v[92:95]\n"
TR "v[178:179],v4 offset:2048\n"
"v_add_f32 v153,v153,v201\n"
MF "v[92:95],a[36:39],v[188:191],v[92:95]\n"
"ds_read2st64_b32 v[192:193],v13 offset0:24 offset1:25\n"
"v_add_f32 v154,v154,v202\n"
MF "v[84:87],a[32:35],v[188:191],v[84:87]\n"
TR "v[180:181],v4 offset:4096\n"
"v_add_f32 v155,v155,v203\n"
MF "v[84:87],a[72:75],a[240:243],v[84:87]\n"
TR "v[182:183],v4 offset:6144\n"
"v_add_f32 v156,v156,v204\n"
MF "v[84:87],a[112:115],a[244:247],v[84:87]\n"
"ds_read2st64_b32 v[194:195],v13 offset0:26 offset1:27\n"
"v_add_f32 v157,v157,v205\n"
MF "v[76:79],a[108:111],a[244:247],v[76:79]\n"
TR "v[184:185],v4 offset:8192\n"
"v_add_f32 v158,v158,v206\n"
MF "v[76:79],a[68:71],a[240:243],v[76:79]\n"
TR "v[186:187],v4 offset:10240\n"
"v_add_f32 v159,v159,v207\n"
MF "v[76:79],a[28:31],v[188:191],v[76:79]\n"
"ds_read2st64_b32 v[196:197],v13 offset0:28 offset1:29\n"
CV "v152,v152,v153\n"
MF "v[68:71],a[24:27],v[188:191],v[68:71]\n"
"ds_read2st64_b32 v[198:199],v13 offset0:30 offset1:31\n"
CV "v153,v154,v155\n"
MF "v[68:71],a[64:67],a[240:243],v[68:71]\n"
"s_add_u32 s87,s90,12288\n"
CV "v154,v156,v157\n"
CV "v155,v158,v159\n"
MF "v[68:71],a[104:107],a[244:247],v[68:71]\n"
".Lnf8:\n"
"v_readlane_b32 s58,v10,s67\n"
"s_cmp_lt_u32 s58,s85\n"
"s_cbranch_scc1 .Lsp8\n"
".Lnb8:\n"
MF "v[60:63],a[100:103],a[244:247],v[60:63]\n"
"buffer_store_dwordx4 v[152:155],v9,s[40:43],s87 offen sc1\n"
MF "v[60:63],a[60:63],a[240:243],v[60:63]\n"
"s_add_u32 s70,s70,20480\n"
"s_add_u32 s2,s2,204800\n"
MF "v[60:63],a[20:23],v[188:191],v[60:63]\n"
"s_waitcnt lgkmcnt(2)\n"
LD "v[228:231],v9,s[40:43],s2 offen sc1\n"
MF "v[16:19],a[140:143],v[176:179],v[16:19]\n"
"s_waitcnt lgkmcnt(0)\n"
TR "v[188:189],v5\n"
"v_add_f32 v160,v160,v192\n"
MF "v[16:19],a[180:183],v[180:183],v[16:19]\n"
TR "v[190:191],v5 offset:2048\n"
"v_add_f32 v161,v161,v193\n"
MF "v[16:19],a[220:223],v[184:187],v[16:19]\n"
"ds_read2st64_b32 v[200:201],v13 offset0:32 offset1:33\n"
"v_add_f32 v162,v162,v194\n"
MF "v[24:27],a[224:227],v[184:187],v[24:27]\n"
TR "a[240:241],v5 offset:4096\n"
"v_add_f32 v163,v163,v195\n"
MF "v[24:27],a[184:187],v[180:183],v[24:27]\n"
TR "a[242:243],v5 offset:6144\n"
"v_add_f32 v164,v164,v196\n"
MF "v[24:27],a[144:147],v[176:179],v[24:27]\n"
"ds_read2st64_b32 v[202:203],v13 offset0:34 offset1:35\n"
"v_add_f32 v165,v165,v197\n"
MF "v[32:35],a[148:151],v[176:179],v[32:35]\n"
TR "a[244:245],v5 offset:8192\n"
"v_add_f32 v166,v166,v198\n"
MF "v[32:35],a[188:191],v[180:183],v[32:35]\n"
TR "a[246:247],v5 offset:10240\n"
"v_add_f32 v167,v167,v199\n"
MF "v[32:35],a[228:231],v[184:187],v[32:35]\n"
"ds_read2st64_b32 v[204:205],v13 offset0:36 offset1:37\n"
CV "v160,v160,v161\n"
MF "v[40:43],a[232:235],v[184:187],v[40:43]\n"
"s_add_u32 s2,s2,204800\n"
"ds_read2st64_b32 v[206:207],v13 offset0:38 offset1:39\n"
CV "v161,v162,v163\n"
MF "v[40:43],a[192:195],v[180:183],v[40:43]\n"
"s_add_u32 s87,s90,13312\n"
CV "v162,v164,v165\n"
CV "v163,v166,v167\n"
MF "v[40:43],a[152:155],v[176:179],v[40:43]\n"
LD "v[232:235],v9,s[40:43],s2 offen sc1\n"
MF "v[48:51],a[156:159],v[176:179],v[48:51]\n"
"buffer_store_dwordx4 v[160:163],v9,s[40:43],s87 offen sc1\n"
MF "v[48:51],a[196:199],v[180:183],v[48:51]\n"
MF "v[48:51],a[236:239],v[184:187],v[48:51]\n"
"s_waitcnt lgkmcnt(2)\n"
MF "v[52:55],a[236:239],a[244:247],v[52:55]\n"
"s_waitcnt lgkmcnt(0)\n"
TR "v[176:177],v6\n"
"v_add_f32 v168,v168,v200\n"
MF "v[52:55],a[196:199],a[240:243],v[52:55]\n"
TR "v[178:179],v6 offset:2048\n"
"v_add_f32 v169,v169,v201\n"
MF "v[52:55],a[156:159],v[188:191],v[52:55]\n"
"v_add_f32 v170,v170,v202\n"
"v_add_f32 v171,v171,v203\n"
MF "v[44:47],a[152:155],v[188:191],v[44:47]\n"
TR "v[180:181],v6 offset:4096\n"
"v_add_f32 v172,v172,v204\n"
MF "v[44:47],a[192:195],a[240:243],v[44:47]\n"
TR "v[182:183],v6 offset:6144\n"
"v_add_f32 v173,v173,v205\n"
MF "v[44:47],a[232:235],a[244:247],v[44:47]\n"
"v_add_f32 v174,v174,v206\n"
"v_add_f32 v175,v175,v207\n"
MF "v[36:39],a[228:231],a[244:247],v[36:39]\n"
TR "v[184:185],v6 offset:8192\n"
CV "v168,v168,v169\n"
MF "v[36:39],a[188:191],a[240:243],v[36:39]\n"
TR "v[186:187],v6 offset:10240\n"
CV "v169,v170,v171\n"
MF "v[36:39],a[148:151],v[188:191],v[36:39]\n"
"s_add_u32 s87,s90,14336\n"
CV "v170,v172,v173\n"
CV "v171,v174,v175\n"
MF "v[28:31],a[144:147],v[188:191],v[28:31]\n"
LD "v[208:211],v9,s[40:43],s82 offen sc1\n"
MF "v[28:31],a[184:187],a[240:243],v[28:31]\n"
MF "v[28:31],a[224:227],a[244:247],v[28:31]\n"
MF "v[20:23],a[220:223],a[244:247],v[20:23]\n"
"buffer_store_dwordx4 v[168:171],v9,s[40:43],s87 offen sc1\n"
MF "v[20:23],a[180:183],a[240:243],v[20:23]\n"
"s_mov_b32 s90,s91\n"
"s_add_u32 s91,s91,20480\n"
MF "v[20:23],a[140:143],v[188:191],v[20:23]\n"
"s_waitcnt lgkmcnt(0)\n"
MF "v[56:59],a[140:143],v[176:179],v[56:59]\n"
TR "v[188:189],v7\n"
MF "v[56:59],a[180:183],v[180:183],v[56:59]\n"
TR "v[190:191],v7 offset:2048\n"
MF "v[56:59],a[220:223],v[184:187],v[56:59]\n"
LD "v[212:215],v9,s[40:43],s82 offen offset:1024 sc1\n"
MF "v[64:67],a[224:227],v[184:187],v[64:67]\n"
TR "a[240:241],v7 offset:4096\n"
MF "v[64:67],a[184:187],v[180:183],v[64:67]\n"
TR "a[242:243],v7 offset:6144\n"
MF "v[64:67],a[144:147],v[176:179],v[64:67]\n"
MF "v[72:75],a[148:151],v[176:179],v[72:75]\n"
TR "a[244:245],v7 offset:8192\n"
MF "v[72:75],a[188:191],v[180:183],v[72:75]\n"
TR "a[246:247],v7 offset:10240\n"
MF "v[72:75],a[228:231],v[184:187],v[72:75]\n"
MF "v[80:83],a[232:235],v[184:187],v[80:83]\n"
MF "v[80:83],a[192:195],v[180:183],v[80:83]\n"
LD "v[216:219],v9,s[40:43],s82 offen offset:2048 sc1\n"
MF "v[80:83],a[152:155],v[176:179],v[80:83]\n"
MF "v[88:91],a[156:159],v[176:179],v[88:91]\n"
MF "v[88:91],a[196:199],v[180:183],v[88:91]\n"
"s_waitcnt lgkmcnt(0)\n"
MF "v[88:91],a[236:239],v[184:187],v[88:91]\n"
"s_barrier\n"
MF "v[92:95],a[236:239],a[244:247],v[92:95]\n"
TR "v[176:177],v0 offset:24576\n"
MF "v[92:95],a[196:199],a[240:243],v[92:95]\n"
"s_add_u32 s85,s85,1\n"
TR "v[178:179],v0 offset:26624\n"
MF "v[92:95],a[156:159],v[188:191],v[92:95]\n"
MF "v[84:87],a[152:155],v[188:191],v[84:87]\n"
TR "v[180:181],v0 offset:28672\n"
MF "v[84:87],a[192:195],a[240:243],v[84:87]\n"
TR "v[182:183],v0 offset:30720\n"
MF "v[84:87],a[232:235],a[244:247],v[84:87]\n"
LD "v[220:223],v9,s[40:43],s82 offen offset:3072 sc1\n"
MF "v[76:79],a[228:231],a[244:247],v[76:79]\n"
TR "v[184:185],v0 offset:32768\n"
MF "v[76:79],a[188:191],a[240:243],v[76:79]\n"
TR "v[186:187],v0 offset:34816\n"
MF "v[76:79],a[148:151],v[188:191],v[76:79]\n"
MF "v[68:71],a[144:147],v[188:191],v[68:71]\n"
MF "v[68:71],a[184:187],a[240:243],v[68:71]\n"
MF "v[68:71],a[224:227],a[244:247],v[68:71]\n"
MF "v[60:63],a[220:223],a[244:247],v[60:63]\n"
MF "v[60:63],a[180:183],a[240:243],v[60:63]\n"
MF "v[60:63],a[140:143],v[188:191],v[60:63]\n"
"s_waitcnt lgkmcnt(0)\n"
"s_cmp_lg_u32 s85,9\n"
"s_cbranch_scc1 .Lpl19\n"
".Lplast1:\n"
MF "v[96:99],v[176:179],a[0:3],v[252:255]\n"
"s_add_u32 m0,s79,73728\n"
TR "v[188:189],v1 offset:24576\n"
TR "v[190:191],v1 offset:26624\n"
MF "v[96:99],v[180:183],a[40:43],v[96:99]\n"
"s_nop 0\n"
TR "a[240:241],v1 offset:28672\n"
TR "a[242:243],v1 offset:30720\n"
MF "v[96:99],v[184:187],a[80:83],v[96:99]\n"
"s_add_u32 s82,s82,4096\n"
TR "a[244:245],v1 offset:32768\n"
TR "a[246:247],v1 offset:34816\n"
MF "v[104:107],v[184:187],a[84:87],v[252:255]\n"
LD "v[224:227],v9,s[40:43],s82 offen sc1\n"
MF "v[104:107],v[180:183],a[44:47],v[104:107]\n"
"s_nop 0\n"
"ds_write_addtid_b32 v56\n"
"ds_write_addtid_b32 v57 offset:256\n"
MF "v[104:107],v[176:179],a[4:7],v[104:107]\n"
"s_nop 0\n"
"ds_write_addtid_b32 v58 offset:512\n"
"ds_write_addtid_b32 v59 offset:768\n"
MF "v[112:115],v[176:179],a[8:11],v[252:255]\n"
"s_nop 0\n"
"ds_write_addtid_b32 v60 offset:1024\n"
"ds_write_addtid_b32 v61 offset:1280\n"
MF "v[112:115],v[180:183],a[48:51],v[112:115]\n"
"s_nop 0\n"
"ds_write_addtid_b32 v62 offset:1536\n"
"ds_write_addtid_b32 v63 offset:1792\n"
MF "v[112:115],v[184:187],a[88:91],v[112:115]\n"
"s_nop 0\n"
"ds_write_addtid_b32 v64 offset:2048\n"
"ds_write_addtid_b32 v65 offset:2304\n"
MF "v[120:123],v[184:187],a[92:95],v[252:255]\n"
"s_nop 0\n"
"ds_write_addtid_b32 v66 offset:2560\n"
"ds_write_addtid_b32 v67 offset:2816\n"
MF "v[120:123],v[180:183],a[52:55],v[120:123]\n"
"s_nop 0\n"
"ds_write_addtid_b32 v68 offset:3072\n"
"ds_write_addtid_b32 v69 offset:3328\n"
MF "v[120:123],v[176:179],a[12:15],v[120:123]\n"
"s_nop 0\n"
"ds_write_addtid_b32 v70 offset:3584\n"
"ds_write_addtid_b32 v71 offset:3840\n"
MF "v[128:131],v[176:179],a[16:19],v[252:255]\n"
"s_nop 0\n"
"ds_write_addtid_b32 v72 offset:4096\n"
"ds_write_addtid_b32 v73 offset:4352\n"
MF "v[128:131],v[180:183],a[56:59],v[128:131]\n"
"s_nop 0\n"
"ds_write_addtid_b32 v74 offset:4608\n"
"ds_write_addtid_b32 v75 offset:4864\n"
MF "v[128:131],v[184:187],a[96:99],v[128:131]\n"
"s_waitcnt lgkmcnt(15)\n"
"ds_write_addtid_b32 v76 offset:5120\n"
MF "v[132:135],a[244:247],a[96:99],v[252:255]\n"
"s_nop 0\n"
TR "v[176:177],v2 offset:24576\n"
TR "v[178:179],v2 offset:26624\n"
MF "v[132:135],a[240:243],a[56:59],v[132:135]\n"
"s_nop 0\n"
TR "v[180:181],v2 offset:28672\n"
TR "v[182:183],v2 offset:30720\n"
MF "v[132:135],v[188:191],a[16:19],v[132:135]\n"
"s_nop 0\n"
TR "v[184:185],v2 offset:32768\n"
TR "v[186:187],v2 offset:34816\n"
MF "v[124:127],v[188:191],a[12:15],v[252:255]\n"
"s_nop 0\n"
"ds_write_addtid_b32 v77 offset:5376\n"
"ds_write_addtid_b32 v78 offset:5632\n"
MF "v[124:127],a[240:243],a[52:55],v[124:127]\n"
"s_nop 0\n"
"ds_write_addtid_b32 v79 offset:5888\n"
"ds_write_addtid_b32 v80 offset:6144\n"
MF "v[124:127],a[244:247],a[92:95],v[124:127]\n"
"s_nop 0\n"
"ds_write_addtid_b32 v81 offset:6400\n"
"ds_write_addtid_b32 v82 offset:6656\n"
MF "v[116:119],a[244:247],a[88:91],v[252:255]\n"
"s_nop 0\n"
"ds_write_addtid_b32 v83 offset:6912\n"
"ds_write_addtid_b32 v84 offset:7168\n"
MF "v[116:119],a[240:243],a[48:51],v[116:119]\n"
"s_nop 0\n"
"ds_write_addtid_b32 v85 offset:7424\n"
"ds_write_addtid_b32 v86 offset:7680\n"
MF "v[116:119],v[188:191],a[8:11],v[116:119]\n"
"s_nop 0\n"
"ds_write_addtid_b32 v87 offset:7936\n"
"ds_write_addtid_b32 v88 offset:8192\n"
MF "v[108:111],v[188:191],a[4:7],v[252:255]\n"
"s_nop 0\n"
"ds_write_addtid_b32 v89 offset:8448\n"
"ds_write_addtid_b32 v90 offset:8704\n"
MF "v[108:111],a[240:243],a[44:47],v[108:111]\n"
"s_nop 0\n"
"ds_write_addtid_b32 v91 offset:8960\n"
"ds_write_addtid_b32 v92 offset:9216\n"
MF "v[108:111],a[244:247],a[84:87],v[108:111]\n"
"s_nop 0\n"
"ds_write_addtid_b32 v93 offset:9472\n"
"ds_write_addtid_b32 v94 offset:9728\n"
MF "v[100:103],a[244:247],a[80:83],v[252:255]\n"
"ds_write_addtid_b32 v95 offset:9984\n"
MF "v[100:103],a[240:243],a[40:43],v[100:103]\n"
MF "v[100:103],v[188:191],a[0:3],v[100:103]\n"
"s_waitcnt lgkmcnt(15)\n"
MF "v[136:139],v[176:179],a[0:3],v[252:255]\n"
"s_nop 0\n"
TR "v[188:189],v3 offset:24576\n"
TR "v[190:191],v3 offset:26624\n"
MF "v[136:139],v[180:183],a[40:43],v[136:139]\n"
"s_nop 0\n"
TR "a[240:241],v3 offset:28672\n"
TR "a[242:243],v3 offset:30720\n"
MF "v[136:139],v[184:187],a[80:83],v[136:139]\n"
"s_nop 0\n"
TR "a[244:245],v3 offset:32768\n"
TR "a[246:247],v3 offset:34816\n"
"s_waitcnt lgkmcnt(0)\n"
MF "v[144:147],v[184:187],a[84:87],v[252:255]\n"
"s_barrier\n"
MF "v[144:147],v[180:183],a[44:47],v[144:147]\n"
"ds_read2st64_b32 v[56:57],v13 offset0:192 offset1:193\n"
MF "v[144:147],v[176:179],a[4:7],v[144:147]\n"
"ds_read2st64_b32 v[58:59],v13 offset0:194 offset1:195\n"
MF "v[152:155],v[176:179],a[8:11],v[252:255]\n"
"ds_read2st64_b32 v[60:61],v13 offset0:196 offset1:197\n"
MF "v[152:155],v[180:183],a[48:51],v[152:155]\n"
"ds_read2st64_b32 v[62:63],v13 offset0:198 offset1:199\n"
MF "v[152:155],v[184:187],a[88:91],v[152:155]\n"
"ds_read2st64_b32 v[64:65],v13 offset0:200 offset1:201\n"
MF "v[160:163],v[184:187],a[92:95],v[252:255]\n"
"ds_read2st64_b32 v[66:67],v13 offset0:202 offset1:203\n"
MF "v[160:163],v[180:183],a[52:55],v[160:163]\n"
"ds_read2st64_b32 v[68:69],v13 offset0:204 offset1:205\n"
MF "v[160:163],v[176:179],a[12:15],v[160:163]\n"
"ds_read2st64_b32 v[70:71],v13 offset0:206 offset1:207\n"
MF "v[168:171],v[176:179],a[16:19],v[252:255]\n"
"s_waitcnt lgkmcnt(4)\n"
"ds_read2st64_b32 v[72:73],v13 offset0:208 offset1:209\n"
"v_add_f32 v16,v16,v56\n"
MF "v[168:171],v[180:183],a[56:59],v[168:171]\n"
"ds_read2st64_b32 v[74:75],v13 offset0:210 offset1:211\n"
"s_waitcnt lgkmcnt(0)\n"
MF "v[168:171],v[184:187],a[96:99],v[168:171]\n"
"s_barrier\n"
MF "v[172:175],a[244:247],a[96:99],v[252:255]\n"
"s_nop 0\n"
TR "v[176:177],v0\n"
TR "v[178:179],v0 offset:2048\n"
MF "v[172:175],a[240:243],a[56:59],v[172:175]\n"
"s_nop 0\n"
TR "v[180:181],v0 offset:4096\n"
TR "v[182:183],v0 offset:6144\n"
MF "v[172:175],v[188:191],a[16:19],v[172:175]\n"
"s_nop 0\n"
TR "v[184:185],v0 offset:8192\n"
TR "v[186:187],v0 offset:10240\n"
MF "v[164:167],v[188:191],a[12:15],v[252:255]\n"
"ds_read2st64_b32 v[76:77],v13 offset0:212 offset1:213\n"
"v_add_f32 v17,v17,v57\n"
MF "v[164:167],a[240:243],a[52:55],v[164:167]\n"
"ds_read2st64_b32 v[78:79],v13 offset0:214 offset1:215\n"
"v_add_f32 v18,v18,v58\n"
MF "v[164:167],a[244:247],a[92:95],v[164:167]\n"
"ds_read2st64_b32 v[80:81],v13 offset0:216 offset1:217\n"
"v_add_f32 v19,v19,v59\n"
MF "v[156:159],a[244:247],a[88:91],v[252:255]\n"
"ds_read2st64_b32 v[82:83],v13 offset0:218 offset1:219\n"
"v_add_f32 v20,v20,v60\n"
MF "v[156:159],a[240:243],a[48:51],v[156:159]\n"
"ds_read2st64_b32 v[84:85],v13 offset0:220 offset1:221\n"
"v_add_f32 v21,v21,v61\n"
MF "v[156:159],v[188:191],a[8:11],v[156:159]\n"
"ds_read2st64_b32 v[86:87],v13 offset0:222 offset1:223\n"
"v_add_f32 v22,v22,v62\n"
MF "v[148:151],v[188:191],a[4:7],v[252:255]\n"
"s_waitcnt vmcnt(4)\n"
"v_mov_b32 v14,s76\n"
"s_add_u32 s76,s76,1\n"
MF "v[148:151],a[240:243],a[44:47],v[148:151]\n"
".Lpg34h:\n"
"s_cbranch_execz .Lisr4\n"
"buffer_store_dword v14,v15,s[40:43],s94 offen sc1\n"
MF "v[148:151],a[244:247],a[84:87],v[148:151]\n"
"ds_read2st64_b32 v[88:89],v13 offset0:224 offset1:225\n"
"v_add_f32 v23,v23,v63\n"
MF "v[140:143],a[244:247],a[80:83],v[252:255]\n"
"ds_read2st64_b32 v[90:91],v13 offset0:226 offset1:227\n"
CV "v16,v16,v17\n"
MF "v[140:143],a[240:243],a[40:43],v[140:143]\n"
"ds_read2st64_b32 v[92:93],v13 offset0:228 offset1:229\n"
CV "v17,v18,v19\n"
MF "v[140:143],v[188:191],a[0:3],v[140:143]\n"
"s_waitcnt lgkmcnt(9)\n"
"ds_read2st64_b32 v[94:95],v13 offset0:230 offset1:231\n"
CV "v18,v20,v21\n"
MF "v[96:99],v[176:179],a[120:123],v[96:99]\n"
"s_nop 0\n"
TR "v[188:189],v1\n"
TR "v[190:191],v1 offset:2048\n"
MF "v[96:99],v[180:183],a[160:163],v[96:99]\n"
"s_nop 0\n"
TR "a[240:241],v1 offset:4096\n"
TR "a[242:243],v1 offset:6144\n"
MF "v[96:99],v[184:187],a[200:203],v[96:99]\n"
"s_nop 0\n"
TR "a[244:245],v1 offset:8192\n"
TR "a[246:247],v1 offset:10240\n"
MF "v[104:107],v[184:187],a[204:207],v[104:107]\n"
"s_add_u32 s87,s90,0\n"
CV "v19,v22,v23\n"
MF "v[104:107],v[180:183],a[164:167],v[104:107]\n"
"buffer_store_dwordx4 v[16:19],v9,s[40:43],s87 offen sc1\n"
MF "v[104:107],v[176:179],a[124:127],v[104:107]\n"
"v_add_f32 v24,v24,v64\n"
"v_add_f32 v25,v25,v65\n"
MF "v[112:115],v[176:179],a[128:131],v[112:115]\n"
"v_add_f32 v26,v26,v66\n"
"v_add_f32 v27,v27,v67\n"
MF "v[112:115],v[180:183],a[168:171],v[112:115]\n"
"v_add_f32 v28,v28,v68\n"
"v_add_f32 v29,v29,v69\n"
MF "v[112:115],v[184:187],a[208:211],v[112:115]\n"
"v_add_f32 v30,v30,v70\n"
"v_add_f32 v31,v31,v71\n"
MF "v[120:123],v[184:187],a[212:215],v[120:123]\n"
CV "v24,v24,v25\n"
CV "v25,v26,v27\n"
MF "v[120:123],v[180:183],a[172:175],v[120:123]\n"
"s_add_u32 s87,s90,1024\n"
CV "v26,v28,v29\n"
CV "v27,v30,v31\n"
MF "v[120:123],v[176:179],a[132:135],v[120:123]\n"
"s_waitcnt lgkmcnt(14)\n"
"buffer_store_dwordx4 v[24:27],v9,s[40:43],s87 offen sc1\n"
MF "v[128:131],v[176:179],a[136:139],v[128:131]\n"
"v_add_f32 v32,v32,v72\n"
"v_add_f32 v33,v33,v73\n"
MF "v[128:131],v[180:183],a[176:179],v[128:131]\n"
"v_add_f32 v34,v34,v74\n"
"v_add_f32 v35,v35,v75\n"
MF "v[128:131],v[184:187],a[216:219],v[128:131]\n"
"s_waitcnt lgkmcnt(0)\n"
"v_add_f32 v36,v36,v76\n"
"v_add_f32 v37,v37,v77\n"
MF "v[132:135],a[244:247],a[216:219],v[132:135]\n"
"s_nop 0\n"
TR "v[176:177],v2\n"
TR "v[178:179],v2 offset:2048\n"
MF "v[132:135],a[240:243],a[176:179],v[132:135]\n"
"s_nop 0\n"
TR "v[180:181],v2 offset:4096\n"
TR "v[182:183],v2 offset:6144\n"
MF "v[132:135],v[188:191],a[136:139],v[132:135]\n"
"s_nop 0\n"
TR "v[184:185],v2 offset:8192\n"
TR "v[186:187],v2 offset:10240\n"
MF "v[124:127],v[188:191],a[132:135],v[124:127]\n"
"v_add_f32 v38,v38,v78\n"
"v_add_f32 v39,v39,v79\n"
MF "v[124:127],a[240:243],a[172:175],v[124:127]\n"
CV "v32,v32,v33\n"
CV "v33,v34,v35\n"
MF "v[124:127],a[244:247],a[212:215],v[124:127]\n"
"s_add_u32 s87,s90,2048\n"
CV "v34,v36,v37\n"
CV "v35,v38,v39\n"
MF "v[116:119],a[244:247],a[208:211],v[116:119]\n"
"buffer_store_dwordx4 v[32:35],v9,s[40:43],s87 offen sc1\n"
MF "v[116:119],a[240:243],a[168:171],v[116:119]\n"
"v_add_f32 v40,v40,v80\n"
"v_add_f32 v41,v41,v81\n"
MF "v[116:119],v[188:191],a[128:131],v[116:119]\n"
"v_add_f32 v42,v42,v82\n"
"v_add_f32 v43,v43,v83\n"
MF "v[108:111],v[188:191],a[124:127],v[108:111]\n"
"v_add_f32 v44,v44,v84\n"
"v_add_f32 v45,v45,v85\n"
MF "v[108:111],a[240:243],a[164:167],v[108:111]\n"
"v_add_f32 v46,v46,v86\n"
"v_add_f32 v47,v47,v87\n"
MF "v[108:111],a[244:247],a[204:207],v[108:111]\n"
CV "v40,v40,v41\n"
CV "v41,v42,v43\n"
MF "v[100:103],a[244:247],a[200:203],v[100:103]\n"
"s_add_u32 s87,s90,3072\n"
CV "v42,v44,v45\n"
CV "v43,v46,v47\n"
MF "v[100:103],a[240:243],a[160:163],v[100:103]\n"
"buffer_store_dwordx4 v[40:43],v9,s[40:43],s87 offen sc1\n"
MF "v[100:103],v[188:191],a[120:123],v[100:103]\n"
"s_waitcnt lgkmcnt(0)\n"
"v_add_f32 v48,v48,v88\n"
"v_add_f32 v49,v49,v89\n"
MF "v[136:139],v[176:179],a[120:123],v[136:139]\n"
"s_nop 0\n"
TR "v[188:189],v3\n"
TR "v[190:191],v3 offset:2048\n"
MF "v[136:139],v[180:183],a[160:163],v[136:139]\n"
"s_nop 0\n"
TR "a[240:241],v3 offset:4096\n"
TR "a[242:243],v3 offset:6144\n"
MF "v[136:139],v[184:187],a[200:203],v[136:139]\n"
"s_nop 0\n"
TR "a[244:245],v3 offset:8192\n"
TR "a[246:247],v3 offset:10240\n"
MF "v[144:147],v[184:187],a[204:207],v[144:147]\n"
"v_add_f32 v50,v50,v90\n"
"v_add_f32 v51,v51,v91\n"
MF "v[144:147],v[180:183],a[164:167],v[144:147]\n"
"v_add_f32 v52,v52,v92\n"
"v_add_f32 v53,v53,v93\n"
MF "v[144:147],v[176:179],a[124:127],v[144:147]\n"
"v_add_f32 v54,v54,v94\n"
"v_add_f32 v55,v55,v95\n"
MF "v[152:155],v[176:179],a[128:131],v[152:155]\n"
CV "v48,v48,v49\n"
CV "v49,v50,v51\n"
MF "v[152:155],v[180:183],a[168:171],v[152:155]\n"
"s_add_u32 s87,s90,4096\n"
CV "v50,v52,v53\n"
CV "v51,v54,v55\n"
MF "v[152:155],v[184:187],a[208:211],v[152:155]\n"
"buffer_store_dwordx4 v[48:51],v9,s[40:43],s87 offen sc1\n"
MF "v[160:163],v[184:187],a[212:215],v[160:163]\n"
MF "v[160:163],v[180:183],a[172:175],v[160:163]\n"
MF "v[160:163],v[176:179],a[132:135],v[160:163]\n"
MF "v[168:171],v[176:179],a[136:139],v[168:171]\n"
MF "v[168:171],v[180:183],a[176:179],v[168:171]\n"
"s_waitcnt lgkmcnt(0)\n"
MF "v[168:171],v[184:187],a[216:219],v[168:171]\n"
"s_barrier\n"
MF "v[172:175],a[244:247],a[216:219],v[172:175]\n"
"s_nop 0\n"
TR "v[176:177],v0 offset:49152\n"
TR "v[178:179],v0 offset:51200\n"
MF "v[172:175],a[240:243],a[176:179],v[172:175]\n"
"s_nop 0\n"
TR "v[180:181],v0 offset:53248\n"
TR "v[182:183],v0 offset:55296\n"
MF "v[172:175],v[188:191],a[136:139],v[172:175]\n"
"s_nop 0\n"
TR "v[184:185],v0 offset:57344\n"
TR "v[186:187],v0 offset:59392\n"
MF "v[164:167],v[188:191],a[132:135],v[164:167]\n"
"buffer_load_dword v10,v12,s[40:43],s86 offen sc1\n"
MF "v[164:167],a[240:243],a[172:175],v[164:167]\n"
MF "v[164:167],a[244:247],a[212:215],v[164:167]\n"
LD "a[0:3],v9,s[40:43],s3 offen sc1\n"
MF "v[156:159],a[244:247],a[208:211],v[156:159]\n"
MF "v[156:159],a[240:243],a[168:171],v[156:159]\n"
MF "v[156:159],v[188:191],a[128:131],v[156:159]\n"
MF "v[148:151],v[188:191],a[124:127],v[148:151]\n"
MF "v[148:151],a[240:243],a[164:167],v[148:151]\n"
"s_add_u32 s3,s3,204800\n"
MF "v[148:151],a[244:247],a[204:207],v[148:151]\n"
LD "a[4:7],v9,s[40:43],s3 offen sc1\n"
MF "v[140:143],a[244:247],a[200:203],v[140:143]\n"
MF "v[140:143],a[240:243],a[160:163],v[140:143]\n"
MF "v[140:143],v[188:191],a[120:123],v[140:143]\n"
"s_waitcnt lgkmcnt(0)\n"
MF "v[96:99],v[176:179],a[20:23],v[96:99]\n"
"s_nop 0\n"
TR "v[188:189],v1 offset:49152\n"
TR "v[190:191],v1 offset:51200\n"
MF "v[96:99],v[180:183],a[60:63],v[96:99]\n"
"s_nop 0\n"
TR "a[240:241],v1 offset:53248\n"
TR "a[242:243],v1 offset:55296\n"
MF "v[96:99],v[184:187],a[100:103],v[96:99]\n"
"s_add_u32 s3,s3,204800\n"
TR "a[244:245],v1 offset:57344\n"
TR "a[246:247],v1 offset:59392\n"
MF "v[104:107],v[184:187],a[104:107],v[104:107]\n"
LD "a[8:11],v9,s[40:43],s3 offen sc1\n"
MF "v[104:107],v[180:183],a[64:67],v[104:107]\n"
MF "v[104:107],v[176:179],a[24:27],v[104:107]\n"
"s_waitcnt vmcnt(10)\n"
"v_mov_b32 v236,v228\n"
"v_mov_b32 v237,v229\n"
MF "v[136:139],v[208:211],a[252:255],v[136:139]\n"
MF "v[144:147],v[212:215],a[252:255],v[144:147]\n"
MF "v[152:155],v[216:219],a[252:255],v[152:155]\n"
MF "v[160:163],v[220:223],a[252:255],v[160:163]\n"
MF "v[168:171],v[224:227],a[252:255],v[168:171]\n"
MF "v[140:143],v[208:211],a[248:251],v[140:143]\n"
MF "v[148:151],v[212:215],a[248:251],v[148:151]\n"
MF "v[156:159],v[216:219],a[248:251],v[156:159]\n"
MF "v[164:167],v[220:223],a[248:251],v[164:167]\n"
MF "v[172:175],v[224:227],a[248:251],v[172:175]\n"
MF "v[248:251],v[230:233],a[250:253],v[248:251]\n"
MF "v[244:247],v[234:237],a[250:253],v[244:247]\n"
MF "v[112:115],v[176:179],a[28:31],v[112:115]\n"
MF "v[112:115],v[180:183],a[68:71],v[112:115]\n"
"s_add_u32 s3,s3,204800\n"
MF "v[112:115],v[184:187],a[108:111],v[112:115]\n"
LD "a[12:15],v9,s[40:43],s3 offen sc1\n"
MF "v[120:123],v[184:187],a[112:115],v[120:123]\n"
"s_nop 1\n"
CV "v244,v244,v245\n"
CV "v245,v246,v247\n"
MF "v[120:123],v[180:183],a[72:75],v[120:123]\n"
"s_bfe_u32 s87,s22,0x30014\n"
CV "v246,v248,v249\n"
CV "v247,v250,v251\n"
MF "v[120:123],v[176:179],a[32:35],v[120:123]\n"
"s_lshl_b32 s87,s87,7\n"
"v_permlane16_swap_b32 v244,v246\n"
"v_permlane16_swap_b32 v245,v247\n"
MF "v[128:131],v[176:179],a[36:39],v[128:131]\n"
"buffer_store_dwordx4 v[244:247],v8,s[44:47],s0 offen sc1\n"
MF "v[128:131],v[180:183],a[76:79],v[128:131]\n"
"s_add_u32 s3,s3,204800\n"
MF "v[128:131],v[184:187],a[116:119],v[128:131]\n"
"s_waitcnt lgkmcnt(0)\n"
LD "a[40:43],v9,s[40:43],s3 offen sc1\n"
MF "v[132:135],a[244:247],a[116:119],v[132:135]\n"
"s_and_b32 s60,s56,1\n"
TR "v[176:177],v2 offset:49152\n"
TR "v[178:179],v2 offset:51200\n"
MF "v[132:135],a[240:243],a[76:79],v[132:135]\n"
"s_mul_i32 s60,s60,204800\n"
TR "v[180:181],v2 offset:53248\n"
TR "v[182:183],v2 offset:55296\n"
MF "v[132:135],v[188:191],a[36:39],v[132:135]\n"
"s_add_u32 s87,s87,s60\n"
TR "v[184:185],v2 offset:57344\n"
TR "v[186:187],v2 offset:59392\n"
MF "v[124:127],v[188:191],a[32:35],v[124:127]\n"
"s_mov_b32 s71,204800\n"
MF "v[124:127],a[240:243],a[72:75],v[124:127]\n"
"s_mov_b32 s66,204800\n"
MF "v[124:127],a[244:247],a[112:115],v[124:127]\n"
"s_add_u32 s72,s78,2\n"
MF "v[116:119],a[244:247],a[108:111],v[116:119]\n"
"s_add_u32 s3,s3,204800\n"
MF "v[116:119],a[240:243],a[68:71],v[116:119]\n"
LD "a[44:47],v9,s[40:43],s3 offen sc1\n"
MF "v[116:119],v[188:191],a[28:31],v[116:119]\n"
"s_and_b32 s72,s72,15\n"
MF "v[108:111],v[188:191],a[24:27],v[108:111]\n"
"s_add_u32 s16,s78,6\n"
MF "v[108:111],a[240:243],a[64:67],v[108:111]\n"
"s_and_b32 s16,s16,15\n"
MF "v[108:111],a[244:247],a[104:107],v[108:111]\n"
"s_add_u32 s17,s78,4\n"
MF "v[100:103],a[244:247],a[100:103],v[100:103]\n"
"s_and_b32 s17,s17,15\n"
MF "v[100:103],a[240:243],a[60:63],v[100:103]\n"
"s_add_u32 s3,s3,204800\n"
MF "v[100:103],v[188:191],a[20:23],v[100:103]\n"
"s_waitcnt lgkmcnt(0)\n"
LD "a[48:51],v9,s[40:43],s3 offen sc1\n"
MF "v[136:139],v[176:179],a[20:23],v[136:139]\n"
"s_bfe_u32 s60,s22,0x10014\n"
TR "v[188:189],v3 offset:49152\n"
TR "v[190:191],v3 offset:51200\n"
MF "v[136:139],v[180:183],a[60:63],v[136:139]\n"
"s_lshl_b32 s60,s60,5\n"
TR "a[240:241],v3 offset:53248\n"
TR "a[242:243],v3 offset:55296\n"
MF "v[136:139],v[184:187],a[100:103],v[136:139]\n"
"s_nop 0\n"
TR "a[244:245],v3 offset:57344\n"
TR "a[246:247],v3 offset:59392\n"
MF "v[144:147],v[184:187],a[104:107],v[144:147]\n"
"v_xor_b32 v242,s60,v8\n"
MF "v[144:147],v[180:183],a[64:67],v[144:147]\n"
"s_add_u32 s3,s3,204800\n"
"v_mov_b32 v14,s76\n"
"s_add_u32 s76,s76,1\n"
MF "v[144:147],v[176:179],a[24:27],v[144:147]\n"
LD "a[52:55],v9,s[40:43],s3 offen sc1\n"
MF "v[152:155],v[176:179],a[28:31],v[152:155]\n"
MF "v[152:155],v[180:183],a[68:71],v[152:155]\n"
MF "v[152:155],v[184:187],a[108:111],v[152:155]\n"
MF "v[160:163],v[184:187],a[112:115],v[160:163]\n"
MF "v[160:163],v[180:183],a[72:75],v[160:163]\n"
MF "v[160:163],v[176:179],a[32:35],v[160:163]\n"
MF "v[168:171],v[176:179],a[36:39],v[168:171]\n"
MF "v[168:171],v[180:183],a[76:79],v[168:171]\n"
"s_waitcnt vmcnt(9) lgkmcnt(0)\n"
MF "v[168:171],v[184:187],a[116:119],v[168:171]\n"
"s_barrier\n"
MF "v[172:175],a[244:247],a[116:119],v[172:175]\n"
"buffer_store_dword v14,v15,s[40:43],s94 offen sc1\n"
MF "v[172:175],a[240:243],a[76:79],v[172:175]\n"
"s_nop 0\n"
TR "v[176:177],v6\n"
TR "v[178:179],v6 offset:2048\n"
MF "v[172:175],v[188:191],a[36:39],v[172:175]\n"
"s_nop 0\n"
TR "v[180:181],v6 offset:4096\n"
TR "v[182:183],v6 offset:6144\n"
MF "v[164:167],v[188:191],a[32:35],v[164:167]\n"
"s_nop 0\n"
TR "v[184:185],v6 offset:8192\n"
TR "v[186:187],v6 offset:10240\n"
MF "v[164:167],a[240:243],a[72:75],v[164:167]\n"
"v_sub_u32 v10,v10,v11\n"
MF "v[164:167],a[244:247],a[112:115],v[164:167]\n"
"v_readlane_b32 s16,v10,s16\n"
MF "v[156:159],a[244:247],a[108:111],v[156:159]\n"
"s_cmp_lt_u32 s16,9\n"
"s_cselect_b32 s16,0xf0000000,0\n"
MF "v[156:159],a[240:243],a[68:71],v[156:159]\n"
"v_readlane_b32 s17,v10,s17\n"
MF "v[156:159],v[188:191],a[28:31],v[156:159]\n"
"s_cmp_lt_u32 s17,10\n"
"s_cselect_b32 s17,0xf0000000,0\n"
MF "v[148:151],v[188:191],a[24:27],v[148:151]\n"
"s_bfe_u32 s62,s22,0x30014\n"
MF "v[148:151],a[240:243],a[64:67],v[148:151]\n"
"s_add_u32 s62,s62,3\n"
"s_and_b32 s62,s62,7\n"
"s_mul_i32 s62,s62,204800\n"
MF "v[148:151],a[244:247],a[104:107],v[148:151]\n"
"s_add_u32 s62,s62,s73\n"
"s_add_u32 s62,s62,122880\n"
"s_or_b32 s62,s62,s16\n"
MF "v[140:143],a[244:247],a[100:103],v[140:143]\n"
LD "v[56:59],v9,s[40:43],s62 offen sc1\n"
MF "v[140:143],a[240:243],a[60:63],v[140:143]\n"
LD "v[64:67],v9,s[40:43],s62 offen offset:1024 sc1\n"
MF "v[140:143],v[188:191],a[20:23],v[140:143]\n"
"s_waitcnt lgkmcnt(0)\n"
LD "v[72:75],v9,s[40:43],s62 offen offset:2048 sc1\n"
MF "v[136:139],v[176:179],a[140:143],v[136:139]\n"
"s_nop 0\n"
TR "v[188:189],v7\n"
TR "v[190:191],v7 offset:2048\n"
MF "v[136:139],v[180:183],a[180:183],v[136:139]\n"
"s_nop 0\n"
TR "a[240:241],v7 offset:4096\n"
TR "a[242:243],v7 offset:6144\n"
MF "v[136:139],v[184:187],a[220:223],v[136:139]\n"
"s_nop 0\n"
TR "a[244:245],v7 offset:8192\n"
TR "a[246:247],v7 offset:10240\n"
MF "v[144:147],v[184:187],a[224:227],v[144:147]\n"
LD "v[80:83],v9,s[40:43],s62 offen offset:3072 sc1\n"
MF "v[144:147],v[180:183],a[184:187],v[144:147]\n"
MF "v[144:147],v[176:179],a[144:147],v[144:147]\n"
"s_add_u32 s62,s62,4096\n"
MF "v[152:155],v[176:179],a[148:151],v[152:155]\n"
LD "v[88:91],v9,s[40:43],s62 offen sc1\n"
MF "v[152:155],v[180:183],a[188:191],v[152:155]\n"
"s_bfe_u32 s62,s22,0x30014\n"
"ds_write_addtid_b32 v136\n"
"ds_write_addtid_b32 v137 offset:256\n"
MF "v[152:155],v[184:187],a[228:231],v[152:155]\n"
"s_add_u32 s62,s62,2\n"
"s_and_b32 s62,s62,7\n"
"s_mul_i32 s62,s62,204800\n"
MF "v[160:163],v[184:187],a[232:235],v[160:163]\n"
"s_add_u32 s62,s62,s73\n"
"s_add_u32 s62,s62,143360\n"
"s_or_b32 s62,s62,s17\n"
MF "v[160:163],v[180:183],a[192:195],v[160:163]\n"
LD "v[60:63],v9,s[40:43],s62 offen sc1\n"
MF "v[160:163],v[176:179],a[152:155],v[160:163]\n"
"s_nop 0\n"
"ds_write_addtid_b32 v138 offset:512\n"
"ds_write_addtid_b32 v139 offset:768\n"
MF "v[168:171],v[176:179],a[156:159],v[168:171]\n"
LD "v[68:71],v9,s[40:43],s62 offen offset:1024 sc1\n"
MF "v[168:171],v[180:183],a[196:199],v[168:171]\n"
"s_nop 0\n"
"ds_write_addtid_b32 v144 offset:2048\n"
"ds_write_addtid_b32 v145 offset:2304\n"
MF "v[168:171],v[184:187],a[236:239],v[168:171]\n"
"s_waitcnt lgkmcnt(6)\n"
LD "v[76:79],v9,s[40:43],s62 offen offset:2048 sc1\n"
MF "v[140:143],v[188:191],a[140:143],v[140:143]\n"
"s_nop 0\n"
TR "v[176:177],v4\n"
TR "v[178:179],v4 offset:2048\n"
MF "v[140:143],a[240:243],a[180:183],v[140:143]\n"
"s_nop 0\n"
TR "v[180:181],v4 offset:4096\n"
TR "v[182:183],v4 offset:6144\n"
MF "v[140:143],a[244:247],a[220:223],v[140:143]\n"
"s_nop 0\n"
TR "v[184:185],v4 offset:8192\n"
TR "v[186:187],v4 offset:10240\n"
MF "v[148:151],a[244:247],a[224:227],v[148:151]\n"
LD "v[84:87],v9,s[40:43],s62 offen offset:3072 sc1\n"
MF "v[148:151],a[240:243],a[184:187],v[148:151]\n"
"s_nop 0\n"
"ds_write_addtid_b32 v146 offset:2560\n"
"ds_write_addtid_b32 v147 offset:2816\n"
MF "v[148:151],v[188:191],a[144:147],v[148:151]\n"
"s_add_u32 s62,s62,4096\n"
"ds_write_addtid_b32 v152 offset:4096\n"
"ds_write_addtid_b32 v153 offset:4352\n"
MF "v[156:159],v[188:191],a[148:151],v[156:159]\n"
LD "v[92:95],v9,s[40:43],s62 offen sc1\n"
MF "v[156:159],a[240:243],a[188:191],v[156:159]\n"
"s_nop 0\n"
"ds_write_addtid_b32 v154 offset:4608\n"
"ds_write_addtid_b32 v155 offset:4864\n"
MF "v[156:159],a[244:247],a[228:231],v[156:159]\n"
"s_nop 0\n"
"ds_write_addtid_b32 v160 offset:6144\n"
"ds_write_addtid_b32 v161 offset:6400\n"
MF "v[164:167],a[244:247],a[232:235],v[164:167]\n"
"s_nop 0\n"
"ds_write_addtid_b32 v162 offset:6656\n"
"ds_write_addtid_b32 v163 offset:6912\n"
MF "v[164:167],a[240:243],a[192:195],v[164:167]\n"
"s_nop 0\n"
"ds_write_addtid_b32 v168 offset:8192\n"
"ds_write_addtid_b32 v169 offset:8448\n"
MF "v[164:167],v[188:191],a[152:155],v[164:167]\n"
"buffer_load_dword v10,v12,s[40:43],s86 offen sc1\n"
MF "v[172:175],v[188:191],a[156:159],v[172:175]\n"
"s_nop 0\n"
"ds_write_addtid_b32 v170 offset:8704\n"
"ds_write_addtid_b32 v171 offset:8960\n"
MF "v[172:175],a[240:243],a[196:199],v[172:175]\n"
"s_nop 0\n"
"ds_write_addtid_b32 v140 offset:1024\n"
"ds_write_addtid_b32 v141 offset:1280\n"
MF "v[172:175],a[244:247],a[236:239],v[172:175]\n"
"s_waitcnt lgkmcnt(15)\n"
"ds_write_addtid_b32 v142 offset:1536\n"
"ds_write_addtid_b32 v143 offset:1792\n"
MF "v[96:99],v[176:179],a[140:143],v[96:99]\n"
"s_nop 0\n"
TR "v[188:189],v5\n"
TR "v[190:191],v5 offset:2048\n"
MF "v[96:99],v[180:183],a[180:183],v[96:99]\n"
"s_nop 0\n"
TR "a[240:241],v5 offset:4096\n"
TR "a[242:243],v5 offset:6144\n"
MF "v[96:99],v[184:187],a[220:223],v[96:99]\n"
"s_nop 0\n"
TR "a[244:245],v5 offset:8192\n"
TR "a[246:247],v5 offset:10240\n"
MF "v[104:107],v[184:187],a[224:227],v[104:107]\n"
"s_nop 0\n"
"ds_write_addtid_b32 v148 offset:3072\n"
"ds_write_addtid_b32 v149 offset:3328\n"
MF "v[104:107],v[180:183],a[184:187],v[104:107]\n"
"s_nop 0\n"
"ds_write_addtid_b32 v150 offset:3584\n"
"ds_write_addtid_b32 v151 offset:3840\n"
MF "v[104:107],v[176:179],a[144:147],v[104:107]\n"
"s_nop 0\n"
"ds_write_addtid_b32 v156 offset:5120\n"
"ds_write_addtid_b32 v157 offset:5376\n"
MF "v[112:115],v[176:179],a[148:151],v[112:115]\n"
"s_nop 0\n"
"ds_write_addtid_b32 v158 offset:5632\n"
"ds_write_addtid_b32 v159 offset:5888\n"
MF "v[112:115],v[180:183],a[188:191],v[112:115]\n"
"ds_write_addtid_b32 v164 offset:7168\n"
MF "v[112:115],v[184:187],a[228:231],v[112:115]\n"
"ds_write_addtid_b32 v165 offset:7424\n"
MF "v[120:123],v[184:187],a[232:235],v[120:123]\n"
"ds_write_addtid_b32 v166 offset:7680\n"
MF "v[120:123],v[180:183],a[192:195],v[120:123]\n"
"ds_write_addtid_b32 v167 offset:7936\n"
MF "v[120:123],v[176:179],a[152:155],v[120:123]\n"
"ds_write_addtid_b32 v172 offset:9216\n"
MF "v[128:131],v[176:179],a[156:159],v[128:131]\n"
"ds_write_addtid_b32 v173 offset:9472\n"
MF "v[128:131],v[180:183],a[196:199],v[128:131]\n"
"ds_write_addtid_b32 v174 offset:9728\n"
MF "v[128:131],v[184:187],a[236:239],v[128:131]\n"
"s_waitcnt lgkmcnt(15)\n"
"ds_write_addtid_b32 v175 offset:9984\n"
MF "v[100:103],v[188:191],a[140:143],v[100:103]\n"
MF "v[100:103],a[240:243],a[180:183],v[100:103]\n"
MF "v[100:103],a[244:247],a[220:223],v[100:103]\n"
MF "v[108:111],a[244:247],a[224:227],v[108:111]\n"
"s_waitcnt lgkmcnt(0)\n"
MF "v[108:111],a[240:243],a[184:187],v[108:111]\n"
"s_barrier\n"
MF "v[108:111],v[188:191],a[144:147],v[108:111]\n"
"ds_read2st64_b32 v[16:17],v13 offset0:192 offset1:193\n"
MF "v[116:119],v[188:191],a[148:151],v[116:119]\n"
"ds_read2st64_b32 v[18:19],v13 offset0:194 offset1:195\n"
MF "v[116:119],a[240:243],a[188:191],v[116:119]\n"
"ds_read2st64_b32 v[20:21],v13 offset0:196 offset1:197\n"
MF "v[116:119],a[244:247],a[228:231],v[116:119]\n"
"ds_read2st64_b32 v[22:23],v13 offset0:198 offset1:199\n"
MF "v[124:127],a[244:247],a[232:235],v[124:127]\n"
"ds_read2st64_b32 v[24:25],v13 offset0:200 offset1:201\n"
MF "v[124:127],a[240:243],a[192:195],v[124:127]\n"
"ds_read2st64_b32 v[26:27],v13 offset0:202 offset1:203\n"
MF "v[124:127],v[188:191],a[152:155],v[124:127]\n"
"ds_read2st64_b32 v[28:29],v13 offset0:204 offset1:205\n"
MF "v[132:135],v[188:191],a[156:159],v[132:135]\n"
"ds_read2st64_b32 v[30:31],v13 offset0:206 offset1:207\n"
MF "v[132:135],a[240:243],a[196:199],v[132:135]\n"
"ds_read2st64_b32 v[32:33],v13 offset0:208 offset1:209\n"
MF "v[132:135],a[244:247],a[236:239],v[132:135]\n"
"ds_read2st64_b32 v[34:35],v13 offset0:210 offset1:211\n"
"ds_read2st64_b32 v[36:37],v13 offset0:212 offset1:213\n"
"ds_read2st64_b32 v[38:39],v13 offset0:214 offset1:215\n"
"s_nop 0\n"
"ds_read2st64_b32 v[40:41],v13 offset0:216 offset1:217\n"
"ds_read2st64_b32 v[42:43],v13 offset0:218 offset1:219\n"
"ds_read2st64_b32 v[44:45],v13 offset0:220 offset1:221\n"
"ds_read2st64_b32 v[46:47],v13 offset0:222 offset1:223\n"
"ds_read2st64_b32 v[48:49],v13 offset0:224 offset1:225\n"
"ds_read2st64_b32 v[50:51],v13 offset0:226 offset1:227\n"
"ds_read2st64_b32 v[52:53],v13 offset0:228 offset1:229\n"
"ds_read2st64_b32 v[54:55],v13 offset0:230 offset1:231\n"
"s_waitcnt vmcnt(0)\n"
"v_sub_u32 v10,v10,v11\n"
"s_cmp_eq_u32 s16,0\n"
"s_cbranch_scc1 .Lhave13\n"
"s_add_u32 s59,s78,6\n"
"s_and_b32 s59,s59,15\n"
"s_nop 3\n"
".Lnf9:\n"
"v_readlane_b32 s58,v10,s59\n"
"s_cmp_lt_u32 s58,9\n"
"s_cbranch_scc1 .Lsp9\n"
".Lnb9:\n"
"s_bfe_u32 s62,s22,0x30014\n"
"s_add_u32 s62,s62,3\n"
"s_and_b32 s62,s62,7\n"
"s_mul_i32 s62,s62,204800\n"
"s_add_u32 s62,s62,s73\n"
"s_add_u32 s62,s62,122880\n"
LD "v[56:59],v9,s[40:43],s62 offen sc1\n"
LD "v[64:67],v9,s[40:43],s62 offen offset:1024 sc1\n"
LD "v[72:75],v9,s[40:43],s62 offen offset:2048 sc1\n"
LD "v[80:83],v9,s[40:43],s62 offen offset:3072 sc1\n"
"s_add_u32 s62,s62,4096\n"
LD "v[88:91],v9,s[40:43],s62 offen sc1\n"
".Lhave13:\n"
"s_cmp_eq_u32 s17,0\n"
"s_cbranch_scc1 .Lhave12\n"
"s_add_u32 s59,s78,4\n"
"s_and_b32 s59,s59,15\n"
"s_nop 3\n"
".Lnf10:\n"
"v_readlane_b32 s58,v10,s59\n"
"s_cmp_lt_u32 s58,10\n"
"s_cbranch_scc1 .Lsp10\n"
".Lnb10:\n"
"s_bfe_u32 s62,s22,0x30014\n"
"s_add_u32 s62,s62,2\n"
"s_and_b32 s62,s62,7\n"
"s_mul_i32 s62,s62,204800\n"
"s_add_u32 s62,s62,s73\n"
"s_add_u32 s62,s62,143360\n"
LD "v[60:63],v9,s[40:43],s62 offen sc1\n"
LD "v[68:71],v9,s[40:43],s62 offen offset:1024 sc1\n"
LD "v[76:79],v9,s[40:43],s62 offen offset:2048 sc1\n"
LD "v[84:87],v9,s[40:43],s62 offen offset:3072 sc1\n"
"s_add_u32 s62,s62,4096\n"
LD "v[92:95],v9,s[40:43],s62 offen sc1\n"
".Lhave12:\n"
".Lnf11:\n"
"v_readlane_b32 s58,v10,s72\n"
"s_cmp_lt_u32 s58,11\n"
"s_cbranch_scc1 .Lsp11\n"
".Lnb11:\n"
"s_bfe_u32 s62,s22,0x30014\n"
"s_add_u32 s62,s62,1\n"
"s_and_b32 s62,s62,7\n"
"s_mul_i32 s62,s62,204800\n"
"s_add_u32 s62,s62,s73\n"
"s_nop 0\n"
"s_add_u32 s62,s62,163840\n"
LD "v[208:211],v9,s[40:43],s62 offen sc1\n"
LD "v[212:215],v9,s[40:43],s62 offen offset:1024 sc1\n"
LD "v[216:219],v9,s[40:43],s62 offen offset:2048 sc1\n"
LD "v[220:223],v9,s[40:43],s62 offen offset:3072 sc1\n"
"s_add_u32 s62,s62,4096\n"
".Lpg35h:\n"
"s_cbranch_execz .Lisr4\n"
"s_nop 0\n"
LD "v[224:227],v9,s[40:43],s62 offen sc1\n"
"s_cmp_eq_u32 s1,0xf0000000\n"
"s_cbranch_scc1 .Ljx1\n"
"v_accvgpr_mov_b32 a16,a0\n"
"v_accvgpr_mov_b32 a17,a1\n"
"v_accvgpr_mov_b32 a56,a40\n"
"v_accvgpr_mov_b32 a57,a41\n"
MF "v[196:199],a[2:5],a[250:253],0\n"
MF "v[192:195],a[6:9],a[250:253],0\n"
MF "v[196:199],a[10:13],a[250:253],v[196:199]\n"
MF "v[192:195],a[14:17],a[250:253],v[192:195]\n"
MF "v[196:199],a[42:45],a[250:253],v[196:199]\n"
MF "v[192:195],a[46:49],a[250:253],v[192:195]\n"
MF "v[196:199],a[50:53],a[250:253],v[196:199]\n"
MF "v[192:195],a[54:57],a[250:253],v[192:195]\n"
"s_nop 7\n"
CV "v192,v192,v193\n"
CV "v193,v194,v195\n"
CV "v194,v196,v197\n"
CV "v195,v198,v199\n"
"s_nop 0\n"
"v_permlane16_swap_b32 v192,v194\n"
"v_permlane16_swap_b32 v193,v195\n"
"buffer_store_dwordx4 v[192:195],v8,s[44:47],s1 offen sc1\n"
".Ljx1:\n"
"s_waitcnt lgkmcnt(15)\n"
"v_pk_add_f32 v[96:97],v[96:97],v[16:17]\n"
"v_pk_add_f32 v[98:99],v[98:99],v[18:19]\n"
"v_pk_add_f32 v[100:101],v[100:101],v[20:21]\n"
"v_pk_add_f32 v[102:103],v[102:103],v[22:23]\n"
"v_pk_add_f32 v[104:105],v[104:105],v[24:25]\n"
"s_waitcnt lgkmcnt(14)\n"
"v_pk_add_f32 v[106:107],v[106:107],v[26:27]\n"
"s_waitcnt lgkmcnt(13)\n"
"v_pk_add_f32 v[108:109],v[108:109],v[28:29]\n"
"s_waitcnt lgkmcnt(12)\n"
"v_pk_add_f32 v[110:111],v[110:111],v[30:31]\n"
"s_waitcnt lgkmcnt(11)\n"
"v_pk_add_f32 v[112:113],v[112:113],v[32:33]\n"
"s_waitcnt lgkmcnt(10)\n"
"v_pk_add_f32 v[114:115],v[114:115],v[34:35]\n"
"s_waitcnt lgkmcnt(9)\n"
"v_pk_add_f32 v[116:117],v[116:117],v[36:37]\n"
"s_waitcnt lgkmcnt(8)\n"
"v_pk_add_f32 v[118:119],v[118:119],v[38:39]\n"
"s_waitcnt lgkmcnt(7)\n"
"v_pk_add_f32 v[120:121],v[120:121],v[40:41]\n"
"s_waitcnt lgkmcnt(6)\n"
"v_pk_add_f32 v[122:123],v[122:123],v[42:43]\n"
"s_waitcnt lgkmcnt(5)\n"
"v_pk_add_f32 v[124:125],v[124:125],v[44:45]\n"
"s_waitcnt lgkmcnt(4)\n"
"v_pk_add_f32 v[126:127],v[126:127],v[46:47]\n"
"s_waitcnt lgkmcnt(3)\n"
"v_pk_add_f32 v[128:129],v[128:129],v[48:49]\n"
"s_waitcnt lgkmcnt(2)\n"
"v_pk_add_f32 v[130:131],v[130:131],v[50:51]\n"
"s_waitcnt lgkmcnt(1)\n"
"v_pk_add_f32 v[132:133],v[132:133],v[52:53]\n"
"s_waitcnt lgkmcnt(0)\n"
"v_pk_add_f32 v[134:135],v[134:135],v[54:55]\n"
"s_waitcnt vmcnt(5)\n"
"s_nop 0\n"
MF "v[100:103],v[56:59],a[252:255],v[100:103]\n"
MF "v[108:111],v[64:67],a[252:255],v[108:111]\n"
MF "v[116:119],v[72:75],a[252:255],v[116:119]\n"
MF "v[124:127],v[80:83],a[252:255],v[124:127]\n"
MF "v[132:135],v[88:91],a[252:255],v[132:135]\n"
MF "v[96:99],v[56:59],a[248:251],v[96:99]\n"
MF "v[104:107],v[64:67],a[248:251],v[104:107]\n"
MF "v[112:115],v[72:75],a[248:251],v[112:115]\n"
MF "v[120:123],v[80:83],a[248:251],v[120:123]\n"
MF "v[128:131],v[88:91],a[248:251],v[128:131]\n"
MF "v[96:99],v[60:63],a[252:255],v[96:99]\n"
MF "v[104:107],v[68:71],a[252:255],v[104:107]\n"
MF "v[112:115],v[76:79],a[252:255],v[112:115]\n"
MF "v[120:123],v[84:87],a[252:255],v[120:123]\n"
MF "v[128:131],v[92:95],a[252:255],v[128:131]\n"
MF "v[100:103],v[60:63],a[248:251],v[100:103]\n"
MF "v[108:111],v[68:71],a[248:251],v[108:111]\n"
MF "v[116:119],v[76:79],a[248:251],v[116:119]\n"
MF "v[124:127],v[84:87],a[248:251],v[124:127]\n"
MF "v[132:135],v[92:95],a[248:251],v[132:135]\n"
"s_waitcnt vmcnt(0)\n"
MF "v[100:103],v[208:211],a[252:255],v[100:103]\n"
MF "v[96:99],v[208:211],a[248:251],v[96:99]\n"
MF "v[108:111],v[212:215],a[252:255],v[108:111]\n"
MF "v[104:107],v[212:215],a[248:251],v[104:107]\n"
MF "v[116:119],v[216:219],a[252:255],v[116:119]\n"
"s_nop 4\n"
CV "v96,v96,v97\n"
CV "v97,v98,v99\n"
MF "v[112:115],v[216:219],a[248:251],v[112:115]\n"
CV "v98,v100,v101\n"
CV "v99,v102,v103\n"
MF "v[124:127],v[220:223],a[252:255],v[124:127]\n"
CV "v104,v104,v105\n"
CV "v105,v106,v107\n"
MF "v[120:123],v[220:223],a[248:251],v[120:123]\n"
CV "v106,v108,v109\n"
CV "v107,v110,v111\n"
"v_permlane16_swap_b32 v96,v98\n"
"v_permlane16_swap_b32 v97,v99\n"
"buffer_store_dwordx4 v[96:99],v242,s[44:47],s87 offen sc1\n"
"s_add_u32 s87,s87,40960\n"
MF "v[132:135],v[224:227],a[252:255],v[132:135]\n"
CV "v112,v112,v113\n"
CV "v113,v114,v115\n"
MF "v[128:131],v[224:227],a[248:251],v[128:131]\n"
CV "v114,v116,v117\n"
CV "v115,v118,v119\n"
"v_permlane16_swap_b32 v104,v106\n"
"v_permlane16_swap_b32 v105,v107\n"
"buffer_store_dwordx4 v[104:107],v242,s[44:47],s87 offen sc1\n"
"s_add_u32 s87,s87,40960\n"
CV "v120,v120,v121\n"
CV "v121,v122,v123\n"
CV "v122,v124,v125\n"
CV "v123,v126,v127\n"
"v_permlane16_swap_b32 v112,v114\n"
"v_permlane16_swap_b32 v113,v115\n"
"buffer_store_dwordx4 v[112:115],v242,s[44:47],s87 offen sc1\n"
"s_add_u32 s87,s87,40960\n"
CV "v128,v128,v129\n"
CV "v129,v130,v131\n"
CV "v130,v132,v133\n"
CV "v131,v134,v135\n"
"v_permlane16_swap_b32 v120,v122\n"
"v_permlane16_swap_b32 v121,v123\n"
"buffer_store_dwordx4 v[120:123],v242,s[44:47],s87 offen sc1\n"
"s_add_u32 s87,s87,40960\n"
"v_permlane16_swap_b32 v128,v130\n"
"v_permlane16_swap_b32 v129,v131\n"
"buffer_store_dwordx4 v[128:131],v242,s[44:47],s87 offen sc1\n"
"s_add_u32 s87,s87,40960\n"
"s_endpgm\n"
".Ljobs:\n"
"s_cmp_ge_u32 s92,40\n"
"s_cbranch_scc1 .Ldone\n"
"buffer_load_dword v10,v12,s[40:43],s86 offen sc1\n"
"s_mul_i32 s95,s92,3277\n"
"s_lshr_b32 s95,s95,16\n"
"s_mul_i32 s59,s95,20\n"
"s_sub_u32 s59,s92,s59\n"
"s_cmp_ge_u32 s59,10\n"
"s_cselect_b32 s96,4294557760,0\n"
"s_add_u32 s96,s96,1024\n"
"s_mul_i32 s59,s59,40960\n"
"s_add_u32 s96,s96,s59\n"
"s_lshl_b32 s59,s95,7\n"
"s_add_u32 s96,s96,s59\n"
"s_add_u32 s95,s95,3\n"
"s_waitcnt vmcnt(0)\n"
".Ljtry:\n"
"s_cmp_eq_u32 s98,0\n"
"s_cselect_b32 s95,0,s95\n"
"v_sub_u32 v10,v10,v11\n"
"v_cmp_gt_u32 vcc,s95,v10\n"
"s_lshl_b32 s82,s92,10\n"
"s_add_u32 s82,s82,s93\n"
"s_cmp_eq_u64 vcc,0\n"
"s_cselect_b32 s82,s82,0xf0000000\n"
"s_cselect_b32 s84,s96,0xf0000000\n"
"s_cselect_b32 s59,s4,0\n"
"s_add_u32 s92,s92,s59\n"
"s_cmp_eq_u32 s59,0\n"
"s_cbranch_scc0 .Ljgo\n"
"s_sub_u32 s98,s98,1\n"
"s_sleep 2\n"
"s_branch .Ljobs\n"
".Ljgo:\n"
"s_mov_b32 s83,s84\n"
LD "v[208:211],v9,s[40:43],s82 offen sc1\n"
"s_add_u32 s82,s82,s71\n"
LD "v[212:215],v9,s[40:43],s82 offen sc1\n"
"s_add_u32 s82,s82,s71\n"
LD "v[216:219],v9,s[40:43],s82 offen sc1\n"
"s_add_u32 s82,s82,s71\n"
LD "v[220:223],v9,s[40:43],s82 offen sc1\n"
"s_add_u32 s82,s82,s71\n"
LD "v[224:227],v9,s[40:43],s82 offen sc1\n"
"s_add_u32 s82,s82,s66\n"
LD "v[228:231],v9,s[40:43],s82 offen sc1\n"
"s_add_u32 s82,s82,s71\n"
LD "v[232:235],v9,s[40:43],s82 offen sc1\n"
"s_add_u32 s82,s82,s71\n"
LD "v[236:239],v9,s[40:43],s82 offen sc1\n"
"buffer_load_dword v10,v12,s[40:43],s86 offen sc1\n"
"s_waitcnt vmcnt(0)\n"
".Ljfin:\n"
"s_nop 1\n"
"v_mov_b32 v240,v208\n"
"v_mov_b32 v241,v209\n"
MF "v[248:251],v[210:213],a[250:253],0\n"
MF "v[244:247],v[214:217],a[250:253],0\n"
MF "v[248:251],v[218:221],a[250:253],v[248:251]\n"
MF "v[244:247],v[222:225],a[250:253],v[244:247]\n"
MF "v[248:251],v[226:229],a[250:253],v[248:251]\n"
MF "v[244:247],v[230:233],a[250:253],v[244:247]\n"
MF "v[248:251],v[234:237],a[250:253],v[248:251]\n"
MF "v[244:247],v[238:241],a[250:253],v[244:247]\n"
"s_nop 7\n"
CV "v244,v244,v245\n"
CV "v245,v246,v247\n"
CV "v246,v248,v249\n"
CV "v247,v250,v251\n"
"s_nop 0\n"
"v_permlane16_swap_b32 v244,v246\n"
"v_permlane16_swap_b32 v245,v247\n"
"buffer_store_dwordx4 v[244:247],v8,s[44:47],s83 offen sc1\n"
"s_cmp_ge_u32 s92,40\n"
"s_cbranch_scc1 .Ldone\n"
"s_mul_i32 s95,s92,3277\n"
"s_lshr_b32 s95,s95,16\n"
"s_mul_i32 s59,s95,20\n"
"s_sub_u32 s59,s92,s59\n"
"s_cmp_ge_u32 s59,10\n"
"s_cselect_b32 s96,4294557760,0\n"
"s_add_u32 s96,s96,1024\n"
"s_mul_i32 s59,s59,40960\n"
"s_add_u32 s96,s96,s59\n"
"s_lshl_b32 s59,s95,7\n"
"s_add_u32 s96,s96,s59\n"
"s_add_u32 s95,s95,3\n"
"s_branch .Ljtry\n"
".Lool:\n"
".Lsp0:\n"
"s_cmp_eq_u32 s98,0\n"
"s_cbranch_scc1 .Lnb0\n"
"s_sub_u32 s98,s98,1\n"
"s_sleep 2\n"
"buffer_load_dword v10,v12,s[40:43],s86 offen sc1\n"
"s_waitcnt vmcnt(0)\n"
"v_sub_u32 v10,v10,v11\n"
"s_nop 1\n"
"s_branch .Lnf0\n"
".Lsp1:\n"
"s_cmp_eq_u32 s98,0\n"
"s_cbranch_scc1 .Lnb1\n"
"s_sub_u32 s98,s98,1\n"
"s_sleep 2\n"
"buffer_load_dword v10,v12,s[40:43],s86 offen sc1\n"
"s_waitcnt vmcnt(0)\n"
"v_sub_u32 v10,v10,v11\n"
"s_nop 1\n"
"s_branch .Lnf1\n"
".Lsp2:\n"
"s_cmp_eq_u32 s98,0\n"
"s_cbranch_scc1 .Lnb2\n"
"s_sub_u32 s98,s98,1\n"
"s_sleep 2\n"
"buffer_load_dword v10,v12,s[40:43],s86 offen sc1\n"
"s_waitcnt vmcnt(0)\n"
"v_sub_u32 v10,v10,v11\n"
"s_nop 1\n"
"s_branch .Lnf2\n"
".Lsp3:\n"
"s_cmp_eq_u32 s98,0\n"
"s_cbranch_scc1 .Lnb3\n"
"s_sub_u32 s98,s98,1\n"
"s_sleep 2\n"
"buffer_load_dword v10,v12,s[40:43],s86 offen sc1\n"
"s_waitcnt vmcnt(0)\n"
"v_sub_u32 v10,v10,v11\n"
"s_nop 1\n"
"s_branch .Lnf3\n"
".Lsp4:\n"
"s_cmp_eq_u32 s98,0\n"
"s_cbranch_scc1 .Lnb4\n"
"s_sub_u32 s98,s98,1\n"
"s_sleep 2\n"
"buffer_load_dword v10,v12,s[40:43],s86 offen sc1\n"
"s_waitcnt vmcnt(0)\n"
"v_sub_u32 v10,v10,v11\n"
"s_nop 1\n"
"s_branch .Lnf4\n"
".Lsp5:\n"
"s_cmp_eq_u32 s98,0\n"
"s_cbranch_scc1 .Lnb5\n"
"s_sub_u32 s98,s98,1\n"
"s_sleep 2\n"
"buffer_load_dword v10,v12,s[40:43],s86 offen sc1\n"
"s_waitcnt vmcnt(0)\n"
"v_sub_u32 v10,v10,v11\n"
"s_nop 1\n"
"s_branch .Lnf5\n"
".Lsp6:\n"
"s_cmp_eq_u32 s98,0\n"
"s_cbranch_scc1 .Lnb6\n"
"s_sub_u32 s98,s98,1\n"
"s_sleep 2\n"
"buffer_load_dword v10,v12,s[40:43],s86 offen sc1\n"
"s_waitcnt vmcnt(0)\n"
"v_sub_u32 v10,v10,v11\n"
"s_nop 1\n"
"s_branch .Lnf6\n"
".Lsp7:\n"
"s_cmp_eq_u32 s98,0\n"
"s_cbranch_scc1 .Lnb7\n"
"s_sub_u32 s98,s98,1\n"
"s_sleep 2\n"
"buffer_load_dword v10,v12,s[40:43],s86 offen sc1\n"
"s_waitcnt vmcnt(0)\n"
"v_sub_u32 v10,v10,v11\n"
"s_nop 1\n"
"s_branch .Lnf7\n"
".Lsp8:\n"
"s_cmp_eq_u32 s98,0\n"
"s_cbranch_scc1 .Lnb8\n"
"s_sub_u32 s98,s98,1\n"
"s_sleep 2\n"
"buffer_load_dword v10,v12,s[40:43],s86 offen sc1\n"
"s_waitcnt vmcnt(0)\n"
"v_sub_u32 v10,v10,v11\n"
"s_nop 1\n"
"s_branch .Lnf8\n"
".Lsp9:\n"
"s_cmp_eq_u32 s98,0\n"
"s_cbranch_scc1 .Lnb9\n"
"s_sub_u32 s98,s98,1\n"
"s_sleep 2\n"
"buffer_load_dword v10,v12,s[40:43],s86 offen sc1\n"
"s_waitcnt vmcnt(0)\n"
"v_sub_u32 v10,v10,v11\n"
"s_nop 1\n"
"s_branch .Lnf9\n"
".Lsp10:\n"
"s_cmp_eq_u32 s98,0\n"
"s_cbranch_scc1 .Lnb10\n"
"s_sub_u32 s98,s98,1\n"
"s_sleep 2\n"
"buffer_load_dword v10,v12,s[40:43],s86 offen sc1\n"
"s_waitcnt vmcnt(0)\n"
"v_sub_u32 v10,v10,v11\n"
"s_nop 1\n"
"s_branch .Lnf10\n"
".Lsp11:\n"
"s_cmp_eq_u32 s98,0\n"
"s_cbranch_scc1 .Lnb11\n"
"s_sub_u32 s98,s98,1\n"
"s_sleep 2\n"
"buffer_load_dword v10,v12,s[40:43],s86 offen sc1\n"
"s_waitcnt vmcnt(0)\n"
"v_sub_u32 v10,v10,v11\n"
"s_nop 1\n"
"s_branch .Lnf11\n"
".Lcode1:\n"
".Lslow_0:\n"
"s_nop 7\n"
"s_nop 7\n"
"v_and_b32 v10,240,v9\n"
"v_mul_u32_u24 v10,24,v10\n"
"v_lshrrev_b32 v11,4,v9\n"
"v_and_or_b32 v10,v11,48,v10\n"
"s_mov_b32 s59,s34\n"
"s_mov_b32 s34,s20\n"
"s_add_u32 s58,s74,0\n"
"s_nop 3\n"
LD "v[52:55],v10,s[32:35],s58 offen\n"
"s_waitcnt vmcnt(0)\n"
MF "v[72:75],v[52:55],v[136:139],v[32:35]\n"
MF "v[76:79],v[52:55],v[140:143],v[36:39]\n"
MF "v[80:83],v[52:55],v[144:147],v[40:43]\n"
MF "v[84:87],v[52:55],v[148:151],v[44:47]\n"
MF "v[88:91],v[52:55],v[152:155],v[48:51]\n"
"s_add_u32 s58,s74,0\n"
"s_nop 3\n"
LD "v[52:55],v10,s[32:35],s58 offen offset:64\n"
"s_waitcnt vmcnt(0)\n"
MF "v[72:75],v[52:55],v[156:159],v[72:75]\n"
MF "v[76:79],v[52:55],v[160:163],v[76:79]\n"
MF "v[80:83],v[52:55],v[164:167],v[80:83]\n"
MF "v[84:87],v[52:55],v[168:171],v[84:87]\n"
MF "v[88:91],v[52:55],v[172:175],v[88:91]\n"
"s_add_u32 s58,s74,0\n"
"s_nop 3\n"
LD "v[52:55],v10,s[32:35],s58 offen offset:128\n"
"s_waitcnt vmcnt(0)\n"
MF "v[72:75],v[52:55],v[176:179],v[72:75]\n"
MF "v[76:79],v[52:55],v[180:183],v[76:79]\n"
MF "v[80:83],v[52:55],v[184:187],v[80:83]\n"
MF "v[84:87],v[52:55],v[188:191],v[84:87]\n"
MF "v[88:91],v[52:55],v[192:195],v[88:91]\n"
"s_add_u32 s58,s74,0\n"
"s_nop 3\n"
LD "v[52:55],v10,s[32:35],s58 offen offset:192\n"
"s_waitcnt vmcnt(0)\n"
MF "v[72:75],v[52:55],v[196:199],v[72:75]\n"
MF "v[76:79],v[52:55],v[200:203],v[76:79]\n"
MF "v[80:83],v[52:55],v[204:207],v[80:83]\n"
MF "v[84:87],v[52:55],v[208:211],v[84:87]\n"
MF "v[88:91],v[52:55],v[212:215],v[88:91]\n"
"s_add_u32 s58,s74,0\n"
"s_nop 3\n"
LD "v[52:55],v10,s[32:35],s58 offen offset:256\n"
"s_waitcnt vmcnt(0)\n"
MF "v[72:75],v[52:55],v[216:219],v[72:75]\n"
MF "v[76:79],v[52:55],v[220:223],v[76:79]\n"
MF "v[80:83],v[52:55],v[224:227],v[80:83]\n"
MF "v[84:87],v[52:55],v[228:231],v[84:87]\n"
MF "v[88:91],v[52:55],v[232:235],v[88:91]\n"
"s_add_u32 s58,s74,0\n"
"s_nop 3\n"
LD "v[52:55],v10,s[32:35],s58 offen offset:320\n"
"s_waitcnt vmcnt(0)\n"
MF "v[72:75],v[52:55],v[236:239],v[72:75]\n"
MF "v[76:79],v[52:55],v[240:243],v[76:79]\n"
MF "v[80:83],v[52:55],v[244:247],v[80:83]\n"
MF "v[84:87],v[52:55],v[248:251],v[84:87]\n"
MF "v[88:91],v[52:55],v[252:255],v[88:91]\n"
"s_add_u32 s58,s74,6144\n"
"s_nop 3\n"
LD "v[52:55],v10,s[32:35],s58 offen\n"
"s_waitcnt vmcnt(0)\n"
MF "v[112:115],v[52:55],v[136:139],v[32:35]\n"
MF "v[116:119],v[52:55],v[140:143],v[36:39]\n"
MF "v[120:123],v[52:55],v[144:147],v[40:43]\n"
MF "v[124:127],v[52:55],v[148:151],v[44:47]\n"
MF "v[128:131],v[52:55],v[152:155],v[48:51]\n"
"s_add_u32 s58,s74,6144\n"
"s_nop 3\n"
LD "v[52:55],v10,s[32:35],s58 offen offset:64\n"
"s_waitcnt vmcnt(0)\n"
MF "v[112:115],v[52:55],v[156:159],v[112:115]\n"
MF "v[116:119],v[52:55],v[160:163],v[116:119]\n"
MF "v[120:123],v[52:55],v[164:167],v[120:123]\n"
MF "v[124:127],v[52:55],v[168:171],v[124:127]\n"
MF "v[128:131],v[52:55],v[172:175],v[128:131]\n"
"s_add_u32 s58,s74,6144\n"
"s_nop 3\n"
LD "v[52:55],v10,s[32:35],s58 offen offset:128\n"
"s_waitcnt vmcnt(0)\n"
MF "v[112:115],v[52:55],v[176:179],v[112:115]\n"
MF "v[116:119],v[52:55],v[180:183],v[116:119]\n"
MF "v[120:123],v[52:55],v[184:187],v[120:123]\n"
MF "v[124:127],v[52:55],v[188:191],v[124:127]\n"
MF "v[128:131],v[52:55],v[192:195],v[128:131]\n"
"s_add_u32 s58,s74,6144\n"
"s_nop 3\n"
LD "v[52:55],v10,s[32:35],s58 offen offset:192\n"
"s_waitcnt vmcnt(0)\n"
MF "v[112:115],v[52:55],v[196:199],v[112:115]\n"
MF "v[116:119],v[52:55],v[200:203],v[116:119]\n"
MF "v[120:123],v[52:55],v[204:207],v[120:123]\n"
MF "v[124:127],v[52:55],v[208:211],v[124:127]\n"
MF "v[128:131],v[52:55],v[212:215],v[128:131]\n"
"s_add_u32 s58,s74,6144\n"
"s_nop 3\n"
LD "v[52:55],v10,s[32:35],s58 offen offset:256\n"
"s_waitcnt vmcnt(0)\n"
MF "v[112:115],v[52:55],v[216:219],v[112:115]\n"
MF "v[116:119],v[52:55],v[220:223],v[116:119]\n"
MF "v[120:123],v[52:55],v[224:227],v[120:123]\n"
MF "v[124:127],v[52:55],v[228:231],v[124:127]\n"
MF "v[128:131],v[52:55],v[232:235],v[128:131]\n"
"s_add_u32 s58,s74,6144\n"
"s_nop 3\n"
LD "v[52:55],v10,s[32:35],s58 offen offset:320\n"
"s_waitcnt vmcnt(0)\n"
MF "v[112:115],v[52:55],v[236:239],v[112:115]\n"
MF "v[116:119],v[52:55],v[240:243],v[116:119]\n"
MF "v[120:123],v[52:55],v[244:247],v[120:123]\n"
MF "v[124:127],v[52:55],v[248:251],v[124:127]\n"
MF "v[128:131],v[52:55],v[252:255],v[128:131]\n"
"s_nop 7\n"
"s_mov_b32 s34,s59\n"
"v_max3_f32 v10,v72,v73,v74\n"
"s_nop 0\n"
"v_max3_f32 v11,v75,v112,v113\n"
"v_max3_f32 v10,v10,v114,v115\n"
"v_max_f32 v10,v10,v11\n"
"v_mov_b32 v11,v10\n"
"s_nop 1\n"
"v_permlane16_swap_b32 v10,v11\n"
"v_max_f32 v10,v10,v11\n"
"v_mov_b32 v11,v10\n"
"s_nop 1\n"
"v_permlane32_swap_b32 v10,v11\n"
"v_max_f32 v10,v10,v11\n"
"v_add_f32 v10,0x41300000,v10\n"
"v_max_f32_e64 v10,0,v10\n"
"v_exp_f32 v11,-v10\n"
AR "v12,a0\n"
"v_subrev_f32 v12,s13,v12\n"
"v_fma_f32 v12,v12,v11,s13\n"
AW "a0,v12\n"
AR "v12,a20\n"
"v_subrev_f32 v12,s13,v12\n"
"v_fma_f32 v12,v12,v11,s13\n"
AW "a20,v12\n"
AR "v12,a40\n"
"v_subrev_f32 v12,s13,v12\n"
"v_fma_f32 v12,v12,v11,s13\n"
AW "a40,v12\n"
AR "v12,a60\n"
"v_subrev_f32 v12,s13,v12\n"
"v_fma_f32 v12,v12,v11,s13\n"
AW "a60,v12\n"
AR "v12,a80\n"
"v_subrev_f32 v12,s13,v12\n"
"v_fma_f32 v12,v12,v11,s13\n"
AW "a80,v12\n"
AR "v12,a100\n"
"v_subrev_f32 v12,s13,v12\n"
"v_fma_f32 v12,v12,v11,s13\n"
AW "a100,v12\n"
AR "v12,a120\n"
"v_subrev_f32 v12,s13,v12\n"
"v_fma_f32 v12,v12,v11,s13\n"
AW "a120,v12\n"
AR "v12,a140\n"
"v_subrev_f32 v12,s13,v12\n"
"v_fma_f32 v12,v12,v11,s13\n"
AW "a140,v12\n"
AR "v12,a160\n"
"v_subrev_f32 v12,s13,v12\n"
"v_fma_f32 v12,v12,v11,s13\n"
AW "a160,v12\n"
AR "v12,a180\n"
"v_subrev_f32 v12,s13,v12\n"
"v_fma_f32 v12,v12,v11,s13\n"
AW "a180,v12\n"
AR "v12,a200\n"
"v_subrev_f32 v12,s13,v12\n"
"v_fma_f32 v12,v12,v11,s13\n"
AW "a200,v12\n"
AR "v12,a220\n"
"v_subrev_f32 v12,s13,v12\n"
"v_fma_f32 v12,v12,v11,s13\n"
AW "a220,v12\n"
AR "v12,a1\n"
"v_subrev_f32 v12,s13,v12\n"
"v_fma_f32 v12,v12,v11,s13\n"
AW "a1,v12\n"
AR "v12,a21\n"
"v_subrev_f32 v12,s13,v12\n"
"v_fma_f32 v12,v12,v11,s13\n"
AW "a21,v12\n"
AR "v12,a41\n"
"v_subrev_f32 v12,s13,v12\n"
"v_fma_f32 v12,v12,v11,s13\n"
AW "a41,v12\n"
AR "v12,a61\n"
"v_subrev_f32 v12,s13,v12\n"
"v_fma_f32 v12,v12,v11,s13\n"
AW "a61,v12\n"
AR "v12,a81\n"
"v_subrev_f32 v12,s13,v12\n"
"v_fma_f32 v12,v12,v11,s13\n"
AW "a81,v12\n"
AR "v12,a101\n"
"v_subrev_f32 v12,s13,v12\n"
"v_fma_f32 v12,v12,v11,s13\n"
AW "a101,v12\n"
AR "v12,a121\n"
"v_subrev_f32 v12,s13,v12\n"
"v_fma_f32 v12,v12,v11,s13\n"
AW "a121,v12\n"
AR "v12,a141\n"
"v_subrev_f32 v12,s13,v12\n"
"v_fma_f32 v12,v12,v11,s13\n"
AW "a141,v12\n"
AR "v12,a161\n"
"v_subrev_f32 v12,s13,v12\n"
"v_fma_f32 v12,v12,v11,s13\n"
AW "a161,v12\n"
AR "v12,a181\n"
"v_subrev_f32 v12,s13,v12\n"
"v_fma_f32 v12,v12,v11,s13\n"
AW "a181,v12\n"
AR "v12,a201\n"
"v_subrev_f32 v12,s13,v12\n"
"v_fma_f32 v12,v12,v11,s13\n"
AW "a201,v12\n"
AR "v12,a221\n"
"v_subrev_f32 v12,s13,v12\n"
"v_fma_f32 v12,v12,v11,s13\n"
AW "a221,v12\n"
AR "v12,a2\n"
"v_subrev_f32 v12,s13,v12\n"
"v_fma_f32 v12,v12,v11,s13\n"
AW "a2,v12\n"
AR "v12,a22\n"
"v_subrev_f32 v12,s13,v12\n"
"v_fma_f32 v12,v12,v11,s13\n"
AW "a22,v12\n"
AR "v12,a42\n"
"v_subrev_f32 v12,s13,v12\n"
"v_fma_f32 v12,v12,v11,s13\n"
AW "a42,v12\n"
AR "v12,a62\n"
"v_subrev_f32 v12,s13,v12\n"
"v_fma_f32 v12,v12,v11,s13\n"
AW "a62,v12\n"
AR "v12,a82\n"
"v_subrev_f32 v12,s13,v12\n"
"v_fma_f32 v12,v12,v11,s13\n"
AW "a82,v12\n"
AR "v12,a102\n"
"v_subrev_f32 v12,s13,v12\n"
"v_fma_f32 v12,v12,v11,s13\n"
AW "a102,v12\n"
AR "v12,a122\n"
"v_subrev_f32 v12,s13,v12\n"
"v_fma_f32 v12,v12,v11,s13\n"
AW "a122,v12\n"
AR "v12,a142\n"
"v_subrev_f32 v12,s13,v12\n"
"v_fma_f32 v12,v12,v11,s13\n"
AW "a142,v12\n"
AR "v12,a162\n"
"v_subrev_f32 v12,s13,v12\n"
"v_fma_f32 v12,v12,v11,s13\n"
AW "a162,v12\n"
AR "v12,a182\n"
"v_subrev_f32 v12,s13,v12\n"
"v_fma_f32 v12,v12,v11,s13\n"
AW "a182,v12\n"
AR "v12,a202\n"
"v_subrev_f32 v12,s13,v12\n"
"v_fma_f32 v12,v12,v11,s13\n"
AW "a202,v12\n"
AR "v12,a222\n"
"v_subrev_f32 v12,s13,v12\n"
"v_fma_f32 v12,v12,v11,s13\n"
AW "a222,v12\n"
AR "v12,a3\n"
"v_subrev_f32 v12,s13,v12\n"
"v_fma_f32 v12,v12,v11,s13\n"
AW "a3,v12\n"
AR "v12,a23\n"
"v_subrev_f32 v12,s13,v12\n"
"v_fma_f32 v12,v12,v11,s13\n"
AW "a23,v12\n"
AR "v12,a43\n"
"v_subrev_f32 v12,s13,v12\n"
"v_fma_f32 v12,v12,v11,s13\n"
AW "a43,v12\n"
AR "v12,a63\n"
"v_subrev_f32 v12,s13,v12\n"
"v_fma_f32 v12,v12,v11,s13\n"
AW "a63,v12\n"
AR "v12,a83\n"
"v_subrev_f32 v12,s13,v12\n"
"v_fma_f32 v12,v12,v11,s13\n"
AW "a83,v12\n"
AR "v12,a103\n"
"v_subrev_f32 v12,s13,v12\n"
"v_fma_f32 v12,v12,v11,s13\n"
AW "a103,v12\n"
AR "v12,a123\n"
"v_subrev_f32 v12,s13,v12\n"
"v_fma_f32 v12,v12,v11,s13\n"
AW "a123,v12\n"
AR "v12,a143\n"
"v_subrev_f32 v12,s13,v12\n"
"v_fma_f32 v12,v12,v11,s13\n"
AW "a143,v12\n"
AR "v12,a163\n"
"v_subrev_f32 v12,s13,v12\n"
"v_fma_f32 v12,v12,v11,s13\n"
AW "a163,v12\n"
AR "v12,a183\n"
"v_subrev_f32 v12,s13,v12\n"
"v_fma_f32 v12,v12,v11,s13\n"
AW "a183,v12\n"
AR "v12,a203\n"
"v_subrev_f32 v12,s13,v12\n"
"v_fma_f32 v12,v12,v11,s13\n"
AW "a203,v12\n"
AR "v12,a223\n"
"v_subrev_f32 v12,s13,v12\n"
"v_fma_f32 v12,v12,v11,s13\n"
AW "a223,v12\n"
"v_mul_f32 v52,v57,v11\n"
"v_sub_f32 v32,v32,v10\n"
"v_sub_f32 v33,v33,v10\n"
"v_sub_f32 v34,v34,v10\n"
"v_sub_f32 v35,v35,v10\n"
"v_sub_f32 v92,v92,v10\n"
"v_sub_f32 v93,v93,v10\n"
"v_sub_f32 v94,v94,v10\n"
"v_sub_f32 v95,v95,v10\n"
"v_sub_f32 v72,v72,v10\n"
"v_sub_f32 v73,v73,v10\n"
"v_sub_f32 v74,v74,v10\n"
"v_sub_f32 v75,v75,v10\n"
"v_sub_f32 v112,v112,v10\n"
"v_sub_f32 v113,v113,v10\n"
"v_sub_f32 v114,v114,v10\n"
"v_sub_f32 v115,v115,v10\n"
"v_exp_f32 v72,v72\n"
"v_exp_f32 v73,v73\n"
"v_exp_f32 v74,v74\n"
"v_exp_f32 v75,v75\n"
"v_exp_f32 v112,v112\n"
"v_exp_f32 v113,v113\n"
"v_exp_f32 v114,v114\n"
"v_exp_f32 v115,v115\n"
"v_add_f32 v52,v52,v72\n"
"v_add_f32 v52,v52,v73\n"
"v_add_f32 v52,v52,v74\n"
"v_add_f32 v52,v52,v75\n"
"v_add_f32 v52,v52,v112\n"
"v_add_f32 v52,v52,v113\n"
"v_add_f32 v52,v52,v114\n"
"v_add_f32_e64 v52,v52,v115\n"
CV "v72,v72,v73\n"
CV "v73,v74,v75\n"
CV "v74,v112,v113\n"
CV "v75,v114,v115\n"
"v_max3_f32 v10,v76,v77,v78\n"
"v_max3_f32 v11,v79,v116,v117\n"
"v_max3_f32 v10,v10,v118,v119\n"
"v_max_f32 v10,v10,v11\n"
"v_mov_b32 v11,v10\n"
"s_nop 1\n"
"v_permlane16_swap_b32 v10,v11\n"
"v_max_f32 v10,v10,v11\n"
"v_mov_b32 v11,v10\n"
"s_nop 1\n"
"v_permlane32_swap_b32 v10,v11\n"
"v_max_f32 v10,v10,v11\n"
"v_add_f32 v10,0x41300000,v10\n"
"v_max_f32 v10,0,v10\n"
"v_exp_f32 v11,-v10\n"
AR "v12,a4\n"
"v_subrev_f32 v12,s13,v12\n"
"v_fma_f32 v12,v12,v11,s13\n"
AW "a4,v12\n"
AR "v12,a24\n"
"v_subrev_f32 v12,s13,v12\n"
"v_fma_f32 v12,v12,v11,s13\n"
AW "a24,v12\n"
AR "v12,a44\n"
"v_subrev_f32 v12,s13,v12\n"
"v_fma_f32 v12,v12,v11,s13\n"
AW "a44,v12\n"
AR "v12,a64\n"
"v_subrev_f32 v12,s13,v12\n"
"v_fma_f32 v12,v12,v11,s13\n"
AW "a64,v12\n"
AR "v12,a84\n"
"v_subrev_f32 v12,s13,v12\n"
"v_fma_f32 v12,v12,v11,s13\n"
AW "a84,v12\n"
AR "v12,a104\n"
"v_subrev_f32 v12,s13,v12\n"
"v_fma_f32 v12,v12,v11,s13\n"
AW "a104,v12\n"
AR "v12,a124\n"
"v_subrev_f32 v12,s13,v12\n"
"v_fma_f32 v12,v12,v11,s13\n"
AW "a124,v12\n"
AR "v12,a144\n"
"v_subrev_f32 v12,s13,v12\n"
"v_fma_f32 v12,v12,v11,s13\n"
AW "a144,v12\n"
AR "v12,a164\n"
"v_subrev_f32 v12,s13,v12\n"
"v_fma_f32 v12,v12,v11,s13\n"
AW "a164,v12\n"
AR "v12,a184\n"
"v_subrev_f32 v12,s13,v12\n"
"v_fma_f32 v12,v12,v11,s13\n"
AW "a184,v12\n"
AR "v12,a204\n"
"v_subrev_f32 v12,s13,v12\n"
"v_fma_f32 v12,v12,v11,s13\n"
AW "a204,v12\n"
AR "v12,a224\n"
"v_subrev_f32 v12,s13,v12\n"
"v_fma_f32 v12,v12,v11,s13\n"
AW "a224,v12\n"
AR "v12,a5\n"
"v_subrev_f32 v12,s13,v12\n"
"v_fma_f32 v12,v12,v11,s13\n"
AW "a5,v12\n"
AR "v12,a25\n"
"v_subrev_f32 v12,s13,v12\n"
"v_fma_f32 v12,v12,v11,s13\n"
AW "a25,v12\n"
AR "v12,a45\n"
"v_subrev_f32 v12,s13,v12\n"
"v_fma_f32 v12,v12,v11,s13\n"
AW "a45,v12\n"
AR "v12,a65\n"
"v_subrev_f32 v12,s13,v12\n"
"v_fma_f32 v12,v12,v11,s13\n"
AW "a65,v12\n"
AR "v12,a85\n"
"v_subrev_f32 v12,s13,v12\n"
"v_fma_f32 v12,v12,v11,s13\n"
AW "a85,v12\n"
AR "v12,a105\n"
"v_subrev_f32 v12,s13,v12\n"
"v_fma_f32 v12,v12,v11,s13\n"
AW "a105,v12\n"
AR "v12,a125\n"
"v_subrev_f32 v12,s13,v12\n"
"v_fma_f32 v12,v12,v11,s13\n"
AW "a125,v12\n"
AR "v12,a145\n"
"v_subrev_f32 v12,s13,v12\n"
"v_fma_f32 v12,v12,v11,s13\n"
AW "a145,v12\n"
AR "v12,a165\n"
"v_subrev_f32 v12,s13,v12\n"
"v_fma_f32 v12,v12,v11,s13\n"
AW "a165,v12\n"
AR "v12,a185\n"
"v_subrev_f32 v12,s13,v12\n"
"v_fma_f32 v12,v12,v11,s13\n"
AW "a185,v12\n"
AR "v12,a205\n"
"v_subrev_f32 v12,s13,v12\n"
"v_fma_f32 v12,v12,v11,s13\n"
AW "a205,v12\n"
AR "v12,a225\n"
"v_subrev_f32 v12,s13,v12\n"
"v_fma_f32 v12,v12,v11,s13\n"
AW "a225,v12\n"
AR "v12,a6\n"
"v_subrev_f32 v12,s13,v12\n"
"v_fma_f32 v12,v12,v11,s13\n"
AW "a6,v12\n"
AR "v12,a26\n"
"v_subrev_f32 v12,s13,v12\n"
"v_fma_f32 v12,v12,v11,s13\n"
AW "a26,v12\n"
AR "v12,a46\n"
"v_subrev_f32 v12,s13,v12\n"
"v_fma_f32 v12,v12,v11,s13\n"
AW "a46,v12\n"
AR "v12,a66\n"
"v_subrev_f32 v12,s13,v12\n"
"v_fma_f32 v12,v12,v11,s13\n"
AW "a66,v12\n"
AR "v12,a86\n"
"v_subrev_f32 v12,s13,v12\n"
"v_fma_f32 v12,v12,v11,s13\n"
AW "a86,v12\n"
AR "v12,a106\n"
"v_subrev_f32 v12,s13,v12\n"
"v_fma_f32 v12,v12,v11,s13\n"
AW "a106,v12\n"
AR "v12,a126\n"
"v_subrev_f32 v12,s13,v12\n"
"v_fma_f32 v12,v12,v11,s13\n"
AW "a126,v12\n"
AR "v12,a146\n"
"v_subrev_f32 v12,s13,v12\n"
"v_fma_f32 v12,v12,v11,s13\n"
AW "a146,v12\n"
AR "v12,a166\n"
"v_subrev_f32 v12,s13,v12\n"
"v_fma_f32 v12,v12,v11,s13\n"
AW "a166,v12\n"
AR "v12,a186\n"
"v_subrev_f32 v12,s13,v12\n"
"v_fma_f32 v12,v12,v11,s13\n"
AW "a186,v12\n"
AR "v12,a206\n"
"v_subrev_f32 v12,s13,v12\n"
"v_fma_f32 v12,v12,v11,s13\n"
AW "a206,v12\n"
AR "v12,a226\n"
"v_subrev_f32 v12,s13,v12\n"
"v_fma_f32 v12,v12,v11,s13\n"
AW "a226,v12\n"
AR "v12,a7\n"
"v_subrev_f32 v12,s13,v12\n"
"v_fma_f32 v12,v12,v11,s13\n"
AW "a7,v12\n"
AR "v12,a27\n"
"v_subrev_f32 v12,s13,v12\n"
"v_fma_f32 v12,v12,v11,s13\n"
AW "a27,v12\n"
AR "v12,a47\n"
"v_subrev_f32 v12,s13,v12\n"
"v_fma_f32 v12,v12,v11,s13\n"
AW "a47,v12\n"
AR "v12,a67\n"
"v_subrev_f32 v12,s13,v12\n"
"v_fma_f32 v12,v12,v11,s13\n"
AW "a67,v12\n"
AR "v12,a87\n"
"v_subrev_f32 v12,s13,v12\n"
"v_fma_f32 v12,v12,v11,s13\n"
AW "a87,v12\n"
AR "v12,a107\n"
"v_subrev_f32 v12,s13,v12\n"
"v_fma_f32 v12,v12,v11,s13\n"
AW "a107,v12\n"
AR "v12,a127\n"
"v_subrev_f32 v12,s13,v12\n"
"v_fma_f32 v12,v12,v11,s13\n"
AW "a127,v12\n"
AR "v12,a147\n"
"v_subrev_f32 v12,s13,v12\n"
"v_fma_f32 v12,v12,v11,s13\n"
AW "a147,v12\n"
AR "v12,a167\n"
"v_subrev_f32 v12,s13,v12\n"
"v_fma_f32 v12,v12,v11,s13\n"
AW "a167,v12\n"
AR "v12,a187\n"
"v_subrev_f32 v12,s13,v12\n"
"v_fma_f32 v12,v12,v11,s13\n"
AW "a187,v12\n"
AR "v12,a207\n"
"v_subrev_f32 v12,s13,v12\n"
"v_fma_f32 v12,v12,v11,s13\n"
AW "a207,v12\n"
AR "v12,a227\n"
"v_subrev_f32 v12,s13,v12\n"
"v_fma_f32 v12,v12,v11,s13\n"
AW "a227,v12\n"
"v_mul_f32 v53,v58,v11\n"
"v_sub_f32 v36,v36,v10\n"
"v_sub_f32 v37,v37,v10\n"
"v_sub_f32 v38,v38,v10\n"
"v_sub_f32 v39,v39,v10\n"
"v_sub_f32 v96,v96,v10\n"
"v_sub_f32 v97,v97,v10\n"
"v_sub_f32 v98,v98,v10\n"
"v_sub_f32 v99,v99,v10\n"
"v_sub_f32 v76,v76,v10\n"
"v_sub_f32 v77,v77,v10\n"
"v_sub_f32 v78,v78,v10\n"
"v_sub_f32 v79,v79,v10\n"
"v_sub_f32 v116,v116,v10\n"
"v_sub_f32 v117,v117,v10\n"
"v_sub_f32 v118,v118,v10\n"
"v_sub_f32 v119,v119,v10\n"
"v_exp_f32 v76,v76\n"
"v_exp_f32 v77,v77\n"
"v_exp_f32 v78,v78\n"
"v_exp_f32 v79,v79\n"
"v_exp_f32 v116,v116\n"
"v_exp_f32 v117,v117\n"
"v_exp_f32 v118,v118\n"
"v_exp_f32 v119,v119\n"
"v_add_f32 v53,v53,v76\n"
"v_add_f32 v53,v53,v77\n"
"v_add_f32 v53,v53,v78\n"
"v_add_f32 v53,v53,v79\n"
"v_add_f32 v53,v53,v116\n"
"v_add_f32 v53,v53,v117\n"
"v_add_f32 v53,v53,v118\n"
"v_add_f32_e64 v53,v53,v119\n"
CV "v76,v76,v77\n"
CV "v77,v78,v79\n"
CV "v78,v116,v117\n"
CV "v79,v118,v119\n"
"v_max3_f32 v10,v80,v81,v82\n"
"v_max3_f32 v11,v83,v120,v121\n"
"v_max3_f32 v10,v10,v122,v123\n"
"v_max_f32 v10,v10,v11\n"
"v_mov_b32 v11,v10\n"
"s_nop 1\n"
"v_permlane16_swap_b32 v10,v11\n"
"v_max_f32 v10,v10,v11\n"
"v_mov_b32 v11,v10\n"
"s_nop 1\n"
"v_permlane32_swap_b32 v10,v11\n"
"v_max_f32 v10,v10,v11\n"
"v_add_f32 v10,0x41300000,v10\n"
"v_max_f32 v10,0,v10\n"
"v_exp_f32 v11,-v10\n"
AR "v12,a8\n"
"v_subrev_f32 v12,s13,v12\n"
"v_fma_f32 v12,v12,v11,s13\n"
AW "a8,v12\n"
AR "v12,a28\n"
"v_subrev_f32 v12,s13,v12\n"
"v_fma_f32 v12,v12,v11,s13\n"
AW "a28,v12\n"
AR "v12,a48\n"
"v_subrev_f32 v12,s13,v12\n"
"v_fma_f32 v12,v12,v11,s13\n"
AW "a48,v12\n"
AR "v12,a68\n"
"v_subrev_f32 v12,s13,v12\n"
"v_fma_f32 v12,v12,v11,s13\n"
AW "a68,v12\n"
AR "v12,a88\n"
"v_subrev_f32 v12,s13,v12\n"
"v_fma_f32 v12,v12,v11,s13\n"
AW "a88,v12\n"
AR "v12,a108\n"
"v_subrev_f32 v12,s13,v12\n"
"v_fma_f32 v12,v12,v11,s13\n"
AW "a108,v12\n"
AR "v12,a128\n"
"v_subrev_f32 v12,s13,v12\n"
"v_fma_f32 v12,v12,v11,s13\n"
AW "a128,v12\n"
AR "v12,a148\n"
"v_subrev_f32 v12,s13,v12\n"
"v_fma_f32 v12,v12,v11,s13\n"
AW "a148,v12\n"
AR "v12,a168\n"
"v_subrev_f32 v12,s13,v12\n"
"v_fma_f32 v12,v12,v11,s13\n"
AW "a168,v12\n"
AR "v12,a188\n"
"v_subrev_f32 v12,s13,v12\n"
"v_fma_f32 v12,v12,v11,s13\n"
AW "a188,v12\n"
AR "v12,a208\n"
"v_subrev_f32 v12,s13,v12\n"
"v_fma_f32 v12,v12,v11,s13\n"
AW "a208,v12\n"
AR "v12,a228\n"
"v_subrev_f32 v12,s13,v12\n"
"v_fma_f32 v12,v12,v11,s13\n"
AW "a228,v12\n"
AR "v12,a9\n"
"v_subrev_f32 v12,s13,v12\n"
"v_fma_f32 v12,v12,v11,s13\n"
AW "a9,v12\n"
AR "v12,a29\n"
"v_subrev_f32 v12,s13,v12\n"
"v_fma_f32 v12,v12,v11,s13\n"
AW "a29,v12\n"
AR "v12,a49\n"
"v_subrev_f32 v12,s13,v12\n"
"v_fma_f32 v12,v12,v11,s13\n"
AW "a49,v12\n"
AR "v12,a69\n"
"v_subrev_f32 v12,s13,v12\n"
"v_fma_f32 v12,v12,v11,s13\n"
AW "a69,v12\n"
AR "v12,a89\n"
"v_subrev_f32 v12,s13,v12\n"
"v_fma_f32 v12,v12,v11,s13\n"
AW "a89,v12\n"
AR "v12,a109\n"
"v_subrev_f32 v12,s13,v12\n"
"v_fma_f32 v12,v12,v11,s13\n"
AW "a109,v12\n"
AR "v12,a129\n"
"v_subrev_f32 v12,s13,v12\n"
"v_fma_f32 v12,v12,v11,s13\n"
AW "a129,v12\n"
AR "v12,a149\n"
"v_subrev_f32 v12,s13,v12\n"
"v_fma_f32 v12,v12,v11,s13\n"
AW "a149,v12\n"
AR "v12,a169\n"
"v_subrev_f32 v12,s13,v12\n"
"v_fma_f32 v12,v12,v11,s13\n"
AW "a169,v12\n"
AR "v12,a189\n"
"v_subrev_f32 v12,s13,v12\n"
"v_fma_f32 v12,v12,v11,s13\n"
AW "a189,v12\n"
AR "v12,a209\n"
"v_subrev_f32 v12,s13,v12\n"
"v_fma_f32 v12,v12,v11,s13\n"
AW "a209,v12\n"
AR "v12,a229\n"
"v_subrev_f32 v12,s13,v12\n"
"v_fma_f32 v12,v12,v11,s13\n"
AW "a229,v12\n"
AR "v12,a10\n"
"v_subrev_f32 v12,s13,v12\n"
"v_fma_f32 v12,v12,v11,s13\n"
AW "a10,v12\n"
AR "v12,a30\n"
"v_subrev_f32 v12,s13,v12\n"
"v_fma_f32 v12,v12,v11,s13\n"
AW "a30,v12\n"
AR "v12,a50\n"
"v_subrev_f32 v12,s13,v12\n"
"v_fma_f32 v12,v12,v11,s13\n"
AW "a50,v12\n"
AR "v12,a70\n"
"v_subrev_f32 v12,s13,v12\n"
"v_fma_f32 v12,v12,v11,s13\n"
AW "a70,v12\n"
AR "v12,a90\n"
"v_subrev_f32 v12,s13,v12\n"
"v_fma_f32 v12,v12,v11,s13\n"
AW "a90,v12\n"
AR "v12,a110\n"
"v_subrev_f32 v12,s13,v12\n"
"v_fma_f32 v12,v12,v11,s13\n"
AW "a110,v12\n"
AR "v12,a130\n"
"v_subrev_f32 v12,s13,v12\n"
"v_fma_f32 v12,v12,v11,s13\n"
AW "a130,v12\n"
AR "v12,a150\n"
"v_subrev_f32 v12,s13,v12\n"
"v_fma_f32 v12,v12,v11,s13\n"
AW "a150,v12\n"
AR "v12,a170\n"
"v_subrev_f32 v12,s13,v12\n"
"v_fma_f32 v12,v12,v11,s13\n"
AW "a170,v12\n"
AR "v12,a190\n"
"v_subrev_f32 v12,s13,v12\n"
"v_fma_f32 v12,v12,v11,s13\n"
AW "a190,v12\n"
AR "v12,a210\n"
"v_subrev_f32 v12,s13,v12\n"
"v_fma_f32 v12,v12,v11,s13\n"
AW "a210,v12\n"
AR "v12,a230\n"
"v_subrev_f32 v12,s13,v12\n"
"v_fma_f32 v12,v12,v11,s13\n"
AW "a230,v12\n"
AR "v12,a11\n"
"v_subrev_f32 v12,s13,v12\n"
"v_fma_f32 v12,v12,v11,s13\n"
AW "a11,v12\n"
AR "v12,a31\n"
"v_subrev_f32 v12,s13,v12\n"
"v_fma_f32 v12,v12,v11,s13\n"
AW "a31,v12\n"
AR "v12,a51\n"
"v_subrev_f32 v12,s13,v12\n"
"v_fma_f32 v12,v12,v11,s13\n"
AW "a51,v12\n"
AR "v12,a71\n"
"v_subrev_f32 v12,s13,v12\n"
"v_fma_f32 v12,v12,v11,s13\n"
AW "a71,v12\n"
AR "v12,a91\n"
"v_subrev_f32 v12,s13,v12\n"
"v_fma_f32 v12,v12,v11,s13\n"
AW "a91,v12\n"
AR "v12,a111\n"
"v_subrev_f32 v12,s13,v12\n"
"v_fma_f32 v12,v12,v11,s13\n"
AW "a111,v12\n"
AR "v12,a131\n"
"v_subrev_f32 v12,s13,v12\n"
"v_fma_f32 v12,v12,v11,s13\n"
AW "a131,v12\n"
AR "v12,a151\n"
"v_subrev_f32 v12,s13,v12\n"
"v_fma_f32 v12,v12,v11,s13\n"
AW "a151,v12\n"
AR "v12,a171\n"
"v_subrev_f32 v12,s13,v12\n"
"v_fma_f32 v12,v12,v11,s13\n"
AW "a171,v12\n"
AR "v12,a191\n"
"v_subrev_f32 v12,s13,v12\n"
"v_fma_f32 v12,v12,v11,s13\n"
AW "a191,v12\n"
AR "v12,a211\n"
"v_subrev_f32 v12,s13,v12\n"
"v_fma_f32 v12,v12,v11,s13\n"
AW "a211,v12\n"
AR "v12,a231\n"
"v_subrev_f32 v12,s13,v12\n"
"v_fma_f32 v12,v12,v11,s13\n"
AW "a231,v12\n"
"v_mul_f32 v54,v59,v11\n"
"v_sub_f32 v40,v40,v10\n"
"v_sub_f32 v41,v41,v10\n"
"v_sub_f32 v42,v42,v10\n"
"v_sub_f32 v43,v43,v10\n"
"v_sub_f32 v100,v100,v10\n"
"v_sub_f32 v101,v101,v10\n"
"v_sub_f32 v102,v102,v10\n"
"v_sub_f32 v103,v103,v10\n"
"v_sub_f32 v80,v80,v10\n"
"v_sub_f32 v81,v81,v10\n"
"v_sub_f32 v82,v82,v10\n"
"v_sub_f32 v83,v83,v10\n"
"v_sub_f32 v120,v120,v10\n"
"v_sub_f32 v121,v121,v10\n"
"v_sub_f32 v122,v122,v10\n"
"v_sub_f32 v123,v123,v10\n"
"v_exp_f32 v80,v80\n"
"v_exp_f32 v81,v81\n"
"v_exp_f32 v82,v82\n"
"v_exp_f32 v83,v83\n"
"v_exp_f32 v120,v120\n"
"v_exp_f32 v121,v121\n"
"v_exp_f32 v122,v122\n"
"v_exp_f32 v123,v123\n"
"v_add_f32 v54,v54,v80\n"
"v_add_f32 v54,v54,v81\n"
"v_add_f32 v54,v54,v82\n"
"v_add_f32 v54,v54,v83\n"
"v_add_f32 v54,v54,v120\n"
"v_add_f32 v54,v54,v121\n"
"v_add_f32 v54,v54,v122\n"
"v_add_f32_e64 v54,v54,v123\n"
CV "v80,v80,v81\n"
CV "v81,v82,v83\n"
CV "v82,v120,v121\n"
CV "v83,v122,v123\n"
"v_max3_f32 v10,v84,v85,v86\n"
"v_max3_f32 v11,v87,v124,v125\n"
"v_max3_f32 v10,v10,v126,v127\n"
"v_max_f32 v10,v10,v11\n"
"v_mov_b32 v11,v10\n"
"s_nop 1\n"
"v_permlane16_swap_b32 v10,v11\n"
"v_max_f32 v10,v10,v11\n"
"v_mov_b32 v11,v10\n"
"s_nop 1\n"
"v_permlane32_swap_b32 v10,v11\n"
"v_max_f32_e64 v10,v10,v11\n"
"v_add_f32 v10,0x41300000,v10\n"
"v_max_f32 v10,0,v10\n"
"v_exp_f32 v11,-v10\n"
AR "v12,a12\n"
"v_subrev_f32 v12,s13,v12\n"
"v_fma_f32 v12,v12,v11,s13\n"
AW "a12,v12\n"
AR "v12,a32\n"
"v_subrev_f32 v12,s13,v12\n"
"v_fma_f32 v12,v12,v11,s13\n"
AW "a32,v12\n"
AR "v12,a52\n"
"v_subrev_f32 v12,s13,v12\n"
"v_fma_f32 v12,v12,v11,s13\n"
AW "a52,v12\n"
AR "v12,a72\n"
"v_subrev_f32 v12,s13,v12\n"
"v_fma_f32 v12,v12,v11,s13\n"
AW "a72,v12\n"
AR "v12,a92\n"
"v_subrev_f32 v12,s13,v12\n"
"v_fma_f32 v12,v12,v11,s13\n"
AW "a92,v12\n"
AR "v12,a112\n"
"v_subrev_f32 v12,s13,v12\n"
"v_fma_f32 v12,v12,v11,s13\n"
AW "a112,v12\n"
AR "v12,a132\n"
"v_subrev_f32 v12,s13,v12\n"
"v_fma_f32 v12,v12,v11,s13\n"
AW "a132,v12\n"
AR "v12,a152\n"
"v_subrev_f32 v12,s13,v12\n"
"v_fma_f32 v12,v12,v11,s13\n"
AW "a152,v12\n"
AR "v12,a172\n"
"v_subrev_f32 v12,s13,v12\n"
"v_fma_f32 v12,v12,v11,s13\n"
AW "a172,v12\n"
AR "v12,a192\n"
"v_subrev_f32 v12,s13,v12\n"
"v_fma_f32 v12,v12,v11,s13\n"
AW "a192,v12\n"
AR "v12,a212\n"
"v_subrev_f32 v12,s13,v12\n"
"v_fma_f32 v12,v12,v11,s13\n"
AW "a212,v12\n"
AR "v12,a232\n"
"v_subrev_f32 v12,s13,v12\n"
"v_fma_f32 v12,v12,v11,s13\n"
AW "a232,v12\n"
AR "v12,a13\n"
"v_subrev_f32 v12,s13,v12\n"
"v_fma_f32 v12,v12,v11,s13\n"
AW "a13,v12\n"
AR "v12,a33\n"
"v_subrev_f32 v12,s13,v12\n"
"v_fma_f32 v12,v12,v11,s13\n"
AW "a33,v12\n"
AR "v12,a53\n"
"v_subrev_f32 v12,s13,v12\n"
"v_fma_f32 v12,v12,v11,s13\n"
AW "a53,v12\n"
AR "v12,a73\n"
"v_subrev_f32 v12,s13,v12\n"
"v_fma_f32 v12,v12,v11,s13\n"
AW "a73,v12\n"
AR "v12,a93\n"
"v_subrev_f32 v12,s13,v12\n"
"v_fma_f32 v12,v12,v11,s13\n"
AW "a93,v12\n"
AR "v12,a113\n"
"v_subrev_f32 v12,s13,v12\n"
"v_fma_f32 v12,v12,v11,s13\n"
AW "a113,v12\n"
AR "v12,a133\n"
"v_subrev_f32 v12,s13,v12\n"
"v_fma_f32 v12,v12,v11,s13\n"
AW "a133,v12\n"
AR "v12,a153\n"
"v_subrev_f32 v12,s13,v12\n"
"v_fma_f32 v12,v12,v11,s13\n"
AW "a153,v12\n"
AR "v12,a173\n"
"v_subrev_f32 v12,s13,v12\n"
"v_fma_f32 v12,v12,v11,s13\n"
AW "a173,v12\n"
AR "v12,a193\n"
"v_subrev_f32 v12,s13,v12\n"
"v_fma_f32 v12,v12,v11,s13\n"
AW "a193,v12\n"
AR "v12,a213\n"
"v_subrev_f32 v12,s13,v12\n"
"v_fma_f32 v12,v12,v11,s13\n"
AW "a213,v12\n"
AR "v12,a233\n"
"v_subrev_f32 v12,s13,v12\n"
"v_fma_f32 v12,v12,v11,s13\n"
AW "a233,v12\n"
AR "v12,a14\n"
"v_subrev_f32 v12,s13,v12\n"
"v_fma_f32 v12,v12,v11,s13\n"
AW "a14,v12\n"
AR "v12,a34\n"
"v_subrev_f32 v12,s13,v12\n"
"v_fma_f32 v12,v12,v11,s13\n"
AW "a34,v12\n"
AR "v12,a54\n"
"v_subrev_f32 v12,s13,v12\n"
"v_fma_f32 v12,v12,v11,s13\n"
AW "a54,v12\n"
AR "v12,a74\n"
"v_subrev_f32 v12,s13,v12\n"
"v_fma_f32 v12,v12,v11,s13\n"
AW "a74,v12\n"
AR "v12,a94\n"
"v_subrev_f32 v12,s13,v12\n"
"v_fma_f32 v12,v12,v11,s13\n"
AW "a94,v12\n"
AR "v12,a114\n"
"v_subrev_f32 v12,s13,v12\n"
"v_fma_f32 v12,v12,v11,s13\n"
AW "a114,v12\n"
AR "v12,a134\n"
"v_subrev_f32 v12,s13,v12\n"
"v_fma_f32 v12,v12,v11,s13\n"
AW "a134,v12\n"
AR "v12,a154\n"
"v_subrev_f32 v12,s13,v12\n"
"v_fma_f32 v12,v12,v11,s13\n"
AW "a154,v12\n"
AR "v12,a174\n"
"v_subrev_f32 v12,s13,v12\n"
"v_fma_f32 v12,v12,v11,s13\n"
AW "a174,v12\n"
AR "v12,a194\n"
"v_subrev_f32 v12,s13,v12\n"
"v_fma_f32 v12,v12,v11,s13\n"
AW "a194,v12\n"
AR "v12,a214\n"
"v_subrev_f32 v12,s13,v12\n"
"v_fma_f32 v12,v12,v11,s13\n"
AW "a214,v12\n"
AR "v12,a234\n"
"v_subrev_f32 v12,s13,v12\n"
"v_fma_f32 v12,v12,v11,s13\n"
AW "a234,v12\n"
AR "v12,a15\n"
"v_subrev_f32 v12,s13,v12\n"
"v_fma_f32 v12,v12,v11,s13\n"
AW "a15,v12\n"
AR "v12,a35\n"
"v_subrev_f32 v12,s13,v12\n"
"v_fma_f32 v12,v12,v11,s13\n"
AW "a35,v12\n"
AR "v12,a55\n"
"v_subrev_f32 v12,s13,v12\n"
"v_fma_f32 v12,v12,v11,s13\n"
AW "a55,v12\n"
AR "v12,a75\n"
"v_subrev_f32 v12,s13,v12\n"
"v_fma_f32 v12,v12,v11,s13\n"
AW "a75,v12\n"
AR "v12,a95\n"
"v_subrev_f32 v12,s13,v12\n"
"v_fma_f32 v12,v12,v11,s13\n"
AW "a95,v12\n"
AR "v12,a115\n"
"v_subrev_f32 v12,s13,v12\n"
"v_fma_f32 v12,v12,v11,s13\n"
AW "a115,v12\n"
AR "v12,a135\n"
"v_subrev_f32 v12,s13,v12\n"
"v_fma_f32 v12,v12,v11,s13\n"
AW "a135,v12\n"
AR "v12,a155\n"
"v_subrev_f32 v12,s13,v12\n"
"v_fma_f32 v12,v12,v11,s13\n"
AW "a155,v12\n"
AR "v12,a175\n"
"v_subrev_f32 v12,s13,v12\n"
"v_fma_f32 v12,v12,v11,s13\n"
AW "a175,v12\n"
AR "v12,a195\n"
"v_subrev_f32 v12,s13,v12\n"
"v_fma_f32 v12,v12,v11,s13\n"
AW "a195,v12\n"
AR "v12,a215\n"
"v_subrev_f32 v12,s13,v12\n"
"v_fma_f32 v12,v12,v11,s13\n"
AW "a215,v12\n"
AR "v12,a235\n"
"v_subrev_f32 v12,s13,v12\n"
"v_fma_f32 v12,v12,v11,s13\n"
AW "a235,v12\n"
"v_mul_f32 v55,v60,v11\n"
"v_sub_f32 v44,v44,v10\n"
"v_sub_f32 v45,v45,v10\n"
"v_sub_f32 v46,v46,v10\n"
"v_sub_f32 v47,v47,v10\n"
"v_sub_f32 v104,v104,v10\n"
"v_sub_f32 v105,v105,v10\n"
"v_sub_f32 v106,v106,v10\n"
"v_sub_f32 v107,v107,v10\n"
"v_sub_f32 v84,v84,v10\n"
"v_sub_f32 v85,v85,v10\n"
"v_sub_f32 v86,v86,v10\n"
"v_sub_f32 v87,v87,v10\n"
"v_sub_f32 v124,v124,v10\n"
"v_sub_f32 v125,v125,v10\n"
"v_sub_f32 v126,v126,v10\n"
"v_sub_f32 v127,v127,v10\n"
"v_exp_f32 v84,v84\n"
"v_exp_f32 v85,v85\n"
"v_exp_f32 v86,v86\n"
"v_exp_f32 v87,v87\n"
"v_exp_f32 v124,v124\n"
"v_exp_f32 v125,v125\n"
"v_exp_f32 v126,v126\n"
"v_exp_f32 v127,v127\n"
"v_add_f32 v55,v55,v84\n"
"v_add_f32 v55,v55,v85\n"
"v_add_f32 v55,v55,v86\n"
"v_add_f32 v55,v55,v87\n"
"v_add_f32 v55,v55,v124\n"
"v_add_f32 v55,v55,v125\n"
"v_add_f32 v55,v55,v126\n"
"v_add_f32 v55,v55,v127\n"
CV "v84,v84,v85\n"
CV "v85,v86,v87\n"
CV "v86,v124,v125\n"
CV "v87,v126,v127\n"
"v_max3_f32 v10,v88,v89,v90\n"
"v_max3_f32 v11,v91,v128,v129\n"
"v_max3_f32 v10,v10,v130,v131\n"
"v_max_f32 v10,v10,v11\n"
"v_mov_b32 v11,v10\n"
"s_nop 1\n"
"v_permlane16_swap_b32 v10,v11\n"
"v_max_f32 v10,v10,v11\n"
"v_mov_b32 v11,v10\n"
"s_nop 1\n"
"v_permlane32_swap_b32 v10,v11\n"
"v_max_f32 v10,v10,v11\n"
"v_add_f32 v10,0x41300000,v10\n"
"v_max_f32 v10,0,v10\n"
"v_exp_f32 v11,-v10\n"
AR "v12,a16\n"
"v_subrev_f32 v12,s13,v12\n"
"v_fma_f32 v12,v12,v11,s13\n"
AW "a16,v12\n"
AR "v12,a36\n"
"v_subrev_f32 v12,s13,v12\n"
"v_fma_f32 v12,v12,v11,s13\n"
AW "a36,v12\n"
AR "v12,a56\n"
"v_subrev_f32 v12,s13,v12\n"
"v_fma_f32 v12,v12,v11,s13\n"
AW "a56,v12\n"
AR "v12,a76\n"
"v_subrev_f32 v12,s13,v12\n"
"v_fma_f32 v12,v12,v11,s13\n"
AW "a76,v12\n"
AR "v12,a96\n"
"v_subrev_f32 v12,s13,v12\n"
"v_fma_f32 v12,v12,v11,s13\n"
AW "a96,v12\n"
AR "v12,a116\n"
"v_subrev_f32 v12,s13,v12\n"
"v_fma_f32 v12,v12,v11,s13\n"
AW "a116,v12\n"
AR "v12,a136\n"
"v_subrev_f32 v12,s13,v12\n"
"v_fma_f32 v12,v12,v11,s13\n"
AW "a136,v12\n"
AR "v12,a156\n"
"v_subrev_f32 v12,s13,v12\n"
"v_fma_f32 v12,v12,v11,s13\n"
AW "a156,v12\n"
AR "v12,a176\n"
"v_subrev_f32 v12,s13,v12\n"
"v_fma_f32 v12,v12,v11,s13\n"
AW "a176,v12\n"
AR "v12,a196\n"
"v_subrev_f32 v12,s13,v12\n"
"v_fma_f32 v12,v12,v11,s13\n"
AW "a196,v12\n"
AR "v12,a216\n"
"v_subrev_f32 v12,s13,v12\n"
"v_fma_f32 v12,v12,v11,s13\n"
AW "a216,v12\n"
AR "v12,a236\n"
"v_subrev_f32 v12,s13,v12\n"
"v_fma_f32 v12,v12,v11,s13\n"
AW "a236,v12\n"
AR "v12,a17\n"
"v_subrev_f32 v12,s13,v12\n"
"v_fma_f32 v12,v12,v11,s13\n"
AW "a17,v12\n"
AR "v12,a37\n"
"v_subrev_f32 v12,s13,v12\n"
"v_fma_f32 v12,v12,v11,s13\n"
AW "a37,v12\n"
AR "v12,a57\n"
"v_subrev_f32 v12,s13,v12\n"
"v_fma_f32 v12,v12,v11,s13\n"
AW "a57,v12\n"
AR "v12,a77\n"
"v_subrev_f32 v12,s13,v12\n"
"v_fma_f32 v12,v12,v11,s13\n"
AW "a77,v12\n"
AR "v12,a97\n"
"v_subrev_f32 v12,s13,v12\n"
"v_fma_f32 v12,v12,v11,s13\n"
AW "a97,v12\n"
AR "v12,a117\n"
"v_subrev_f32 v12,s13,v12\n"
"v_fma_f32 v12,v12,v11,s13\n"
AW "a117,v12\n"
AR "v12,a137\n"
"v_subrev_f32 v12,s13,v12\n"
"v_fma_f32 v12,v12,v11,s13\n"
AW "a137,v12\n"
AR "v12,a157\n"
"v_subrev_f32 v12,s13,v12\n"
"v_fma_f32 v12,v12,v11,s13\n"
AW "a157,v12\n"
AR "v12,a177\n"
"v_subrev_f32 v12,s13,v12\n"
"v_fma_f32 v12,v12,v11,s13\n"
AW "a177,v12\n"
AR "v12,a197\n"
"v_subrev_f32 v12,s13,v12\n"
"v_fma_f32 v12,v12,v11,s13\n"
AW "a197,v12\n"
AR "v12,a217\n"
"v_subrev_f32 v12,s13,v12\n"
"v_fma_f32 v12,v12,v11,s13\n"
AW "a217,v12\n"
AR "v12,a237\n"
"v_subrev_f32 v12,s13,v12\n"
"v_fma_f32 v12,v12,v11,s13\n"
AW "a237,v12\n"
AR "v12,a18\n"
"v_subrev_f32 v12,s13,v12\n"
"v_fma_f32 v12,v12,v11,s13\n"
AW "a18,v12\n"
AR "v12,a38\n"
"v_subrev_f32 v12,s13,v12\n"
"v_fma_f32 v12,v12,v11,s13\n"
AW "a38,v12\n"
AR "v12,a58\n"
"v_subrev_f32 v12,s13,v12\n"
"v_fma_f32 v12,v12,v11,s13\n"
AW "a58,v12\n"
AR "v12,a78\n"
"v_subrev_f32 v12,s13,v12\n"
"v_fma_f32 v12,v12,v11,s13\n"
AW "a78,v12\n"
AR "v12,a98\n"
"v_subrev_f32 v12,s13,v12\n"
"v_fma_f32 v12,v12,v11,s13\n"
AW "a98,v12\n"
AR "v12,a118\n"
"v_subrev_f32 v12,s13,v12\n"
"v_fma_f32 v12,v12,v11,s13\n"
AW "a118,v12\n"
AR "v12,a138\n"
"v_subrev_f32 v12,s13,v12\n"
"v_fma_f32 v12,v12,v11,s13\n"
AW "a138,v12\n"
AR "v12,a158\n"
"v_subrev_f32 v12,s13,v12\n"
"v_fma_f32 v12,v12,v11,s13\n"
AW "a158,v12\n"
AR "v12,a178\n"
"v_subrev_f32 v12,s13,v12\n"
"v_fma_f32 v12,v12,v11,s13\n"
AW "a178,v12\n"
AR "v12,a198\n"
"v_subrev_f32 v12,s13,v12\n"
"v_fma_f32 v12,v12,v11,s13\n"
AW "a198,v12\n"
AR "v12,a218\n"
"v_subrev_f32 v12,s13,v12\n"
"v_fma_f32 v12,v12,v11,s13\n"
AW "a218,v12\n"
AR "v12,a238\n"
"v_subrev_f32 v12,s13,v12\n"
"v_fma_f32 v12,v12,v11,s13\n"
AW "a238,v12\n"
AR "v12,a19\n"
"v_subrev_f32 v12,s13,v12\n"
"v_fma_f32 v12,v12,v11,s13\n"
AW "a19,v12\n"
AR "v12,a39\n"
"v_subrev_f32 v12,s13,v12\n"
"v_fma_f32 v12,v12,v11,s13\n"
AW "a39,v12\n"
AR "v12,a59\n"
"v_subrev_f32 v12,s13,v12\n"
"v_fma_f32 v12,v12,v11,s13\n"
AW "a59,v12\n"
AR "v12,a79\n"
"v_subrev_f32 v12,s13,v12\n"
"v_fma_f32 v12,v12,v11,s13\n"
AW "a79,v12\n"
AR "v12,a99\n"
"v_subrev_f32 v12,s13,v12\n"
"v_fma_f32 v12,v12,v11,s13\n"
AW "a99,v12\n"
AR "v12,a119\n"
"v_subrev_f32 v12,s13,v12\n"
"v_fma_f32 v12,v12,v11,s13\n"
AW "a119,v12\n"
AR "v12,a139\n"
"v_subrev_f32 v12,s13,v12\n"
"v_fma_f32 v12,v12,v11,s13\n"
AW "a139,v12\n"
AR "v12,a159\n"
"v_subrev_f32 v12,s13,v12\n"
"v_fma_f32 v12,v12,v11,s13\n"
AW "a159,v12\n"
AR "v12,a179\n"
"v_subrev_f32 v12,s13,v12\n"
"v_fma_f32 v12,v12,v11,s13\n"
AW "a179,v12\n"
AR "v12,a199\n"
"v_subrev_f32 v12,s13,v12\n"
"v_fma_f32 v12,v12,v11,s13\n"
AW "a199,v12\n"
AR "v12,a219\n"
"v_subrev_f32 v12,s13,v12\n"
"v_fma_f32 v12,v12,v11,s13\n"
AW "a219,v12\n"
AR "v12,a239\n"
"v_subrev_f32 v12,s13,v12\n"
"v_fma_f32 v12,v12,v11,s13\n"
AW "a239,v12\n"
"v_mul_f32 v56,v61,v11\n"
"v_sub_f32 v48,v48,v10\n"
"v_sub_f32 v49,v49,v10\n"
"v_sub_f32 v50,v50,v10\n"
"v_sub_f32 v51,v51,v10\n"
"v_sub_f32 v108,v108,v10\n"
"v_sub_f32 v109,v109,v10\n"
"v_sub_f32 v110,v110,v10\n"
"v_sub_f32 v111,v111,v10\n"
"v_sub_f32 v88,v88,v10\n"
"v_sub_f32 v89,v89,v10\n"
"v_sub_f32 v90,v90,v10\n"
"v_sub_f32 v91,v91,v10\n"
"v_sub_f32 v128,v128,v10\n"
"v_sub_f32 v129,v129,v10\n"
"v_sub_f32 v130,v130,v10\n"
"v_sub_f32 v131,v131,v10\n"
"v_exp_f32 v88,v88\n"
"v_exp_f32 v89,v89\n"
"v_exp_f32 v90,v90\n"
"v_exp_f32 v91,v91\n"
"v_exp_f32 v128,v128\n"
"v_exp_f32 v129,v129\n"
"v_exp_f32 v130,v130\n"
"v_exp_f32 v131,v131\n"
"v_add_f32 v56,v56,v88\n"
"v_add_f32 v56,v56,v89\n"
"v_add_f32 v56,v56,v90\n"
"v_add_f32 v56,v56,v91\n"
"v_add_f32 v56,v56,v128\n"
"v_add_f32 v56,v56,v129\n"
"v_add_f32 v56,v56,v130\n"
"v_add_f32_e64 v56,v56,v131\n"
CV "v88,v88,v89\n"
CV "v89,v90,v91\n"
CV "v90,v128,v129\n"
CV "v91,v130,v131\n"
"s_nop 7\n"
"s_setpc_b64 s[52:53]\n"
".Lslow_1:\n"
"s_nop 7\n"
"s_nop 7\n"
"v_and_b32 v10,240,v9\n"
"v_mul_u32_u24 v10,24,v10\n"
"v_lshrrev_b32 v11,4,v9\n"
"v_and_or_b32 v10,v11,48,v10\n"
"s_mov_b32 s59,s34\n"
"s_mov_b32 s34,s20\n"
"s_add_u32 s58,s74,0\n"
"s_nop 3\n"
LD "v[58:61],v10,s[32:35],s58 offen\n"
"s_waitcnt vmcnt(0)\n"
MF "v[92:95],v[58:61],v[136:139],v[32:35]\n"
MF "v[96:99],v[58:61],v[140:143],v[36:39]\n"
MF "v[100:103],v[58:61],v[144:147],v[40:43]\n"
MF "v[104:107],v[58:61],v[148:151],v[44:47]\n"
MF "v[108:111],v[58:61],v[152:155],v[48:51]\n"
"s_add_u32 s58,s74,0\n"
"s_nop 3\n"
LD "v[58:61],v10,s[32:35],s58 offen offset:64\n"
"s_waitcnt vmcnt(0)\n"
MF "v[92:95],v[58:61],v[156:159],v[92:95]\n"
MF "v[96:99],v[58:61],v[160:163],v[96:99]\n"
MF "v[100:103],v[58:61],v[164:167],v[100:103]\n"
MF "v[104:107],v[58:61],v[168:171],v[104:107]\n"
MF "v[108:111],v[58:61],v[172:175],v[108:111]\n"
"s_add_u32 s58,s74,0\n"
"s_nop 3\n"
LD "v[58:61],v10,s[32:35],s58 offen offset:128\n"
"s_waitcnt vmcnt(0)\n"
MF "v[92:95],v[58:61],v[176:179],v[92:95]\n"
MF "v[96:99],v[58:61],v[180:183],v[96:99]\n"
MF "v[100:103],v[58:61],v[184:187],v[100:103]\n"
MF "v[104:107],v[58:61],v[188:191],v[104:107]\n"
MF "v[108:111],v[58:61],v[192:195],v[108:111]\n"
"s_add_u32 s58,s74,0\n"
"s_nop 3\n"
LD "v[58:61],v10,s[32:35],s58 offen offset:192\n"
"s_waitcnt vmcnt(0)\n"
MF "v[92:95],v[58:61],v[196:199],v[92:95]\n"
MF "v[96:99],v[58:61],v[200:203],v[96:99]\n"
MF "v[100:103],v[58:61],v[204:207],v[100:103]\n"
MF "v[104:107],v[58:61],v[208:211],v[104:107]\n"
MF "v[108:111],v[58:61],v[212:215],v[108:111]\n"
"s_add_u32 s58,s74,0\n"
"s_nop 3\n"
LD "v[58:61],v10,s[32:35],s58 offen offset:256\n"
"s_waitcnt vmcnt(0)\n"
MF "v[92:95],v[58:61],v[216:219],v[92:95]\n"
MF "v[96:99],v[58:61],v[220:223],v[96:99]\n"
MF "v[100:103],v[58:61],v[224:227],v[100:103]\n"
MF "v[104:107],v[58:61],v[228:231],v[104:107]\n"
MF "v[108:111],v[58:61],v[232:235],v[108:111]\n"
"s_add_u32 s58,s74,0\n"
"s_nop 3\n"
LD "v[58:61],v10,s[32:35],s58 offen offset:320\n"
"s_waitcnt vmcnt(0)\n"
MF "v[92:95],v[58:61],v[236:239],v[92:95]\n"
MF "v[96:99],v[58:61],v[240:243],v[96:99]\n"
MF "v[100:103],v[58:61],v[244:247],v[100:103]\n"
MF "v[104:107],v[58:61],v[248:251],v[104:107]\n"
MF "v[108:111],v[58:61],v[252:255],v[108:111]\n"
"s_add_u32 s58,s74,6144\n"
"s_nop 3\n"
LD "v[58:61],v10,s[32:35],s58 offen\n"
"s_waitcnt vmcnt(0)\n"
MF "v[112:115],v[58:61],v[136:139],v[32:35]\n"
MF "v[116:119],v[58:61],v[140:143],v[36:39]\n"
MF "v[120:123],v[58:61],v[144:147],v[40:43]\n"
MF "v[124:127],v[58:61],v[148:151],v[44:47]\n"
MF "v[128:131],v[58:61],v[152:155],v[48:51]\n"
"s_add_u32 s58,s74,6144\n"
"s_nop 3\n"
LD "v[58:61],v10,s[32:35],s58 offen offset:64\n"
"s_waitcnt vmcnt(0)\n"
MF "v[112:115],v[58:61],v[156:159],v[112:115]\n"
MF "v[116:119],v[58:61],v[160:163],v[116:119]\n"
MF "v[120:123],v[58:61],v[164:167],v[120:123]\n"
MF "v[124:127],v[58:61],v[168:171],v[124:127]\n"
MF "v[128:131],v[58:61],v[172:175],v[128:131]\n"
"s_add_u32 s58,s74,6144\n"
"s_nop 3\n"
LD "v[58:61],v10,s[32:35],s58 offen offset:128\n"
"s_waitcnt vmcnt(0)\n"
MF "v[112:115],v[58:61],v[176:179],v[112:115]\n"
MF "v[116:119],v[58:61],v[180:183],v[116:119]\n"
MF "v[120:123],v[58:61],v[184:187],v[120:123]\n"
MF "v[124:127],v[58:61],v[188:191],v[124:127]\n"
MF "v[128:131],v[58:61],v[192:195],v[128:131]\n"
"s_add_u32 s58,s74,6144\n"
"s_nop 3\n"
LD "v[58:61],v10,s[32:35],s58 offen offset:192\n"
"s_waitcnt vmcnt(0)\n"
MF "v[112:115],v[58:61],v[196:199],v[112:115]\n"
MF "v[116:119],v[58:61],v[200:203],v[116:119]\n"
MF "v[120:123],v[58:61],v[204:207],v[120:123]\n"
MF "v[124:127],v[58:61],v[208:211],v[124:127]\n"
MF "v[128:131],v[58:61],v[212:215],v[128:131]\n"
"s_add_u32 s58,s74,6144\n"
"s_nop 3\n"
LD "v[58:61],v10,s[32:35],s58 offen offset:256\n"
"s_waitcnt vmcnt(0)\n"
MF "v[112:115],v[58:61],v[216:219],v[112:115]\n"
MF "v[116:119],v[58:61],v[220:223],v[116:119]\n"
MF "v[120:123],v[58:61],v[224:227],v[120:123]\n"
MF "v[124:127],v[58:61],v[228:231],v[124:127]\n"
MF "v[128:131],v[58:61],v[232:235],v[128:131]\n"
"s_add_u32 s58,s74,6144\n"
"s_nop 3\n"
LD "v[58:61],v10,s[32:35],s58 offen offset:320\n"
"s_waitcnt vmcnt(0)\n"
MF "v[112:115],v[58:61],v[236:239],v[112:115]\n"
MF "v[116:119],v[58:61],v[240:243],v[116:119]\n"
MF "v[120:123],v[58:61],v[244:247],v[120:123]\n"
MF "v[124:127],v[58:61],v[248:251],v[124:127]\n"
MF "v[128:131],v[58:61],v[252:255],v[128:131]\n"
"s_nop 7\n"
"s_mov_b32 s34,s59\n"
"v_max3_f32 v10,v92,v93,v94\n"
"s_nop 0\n"
"v_max3_f32 v11,v95,v112,v113\n"
"v_max3_f32 v10,v10,v114,v115\n"
"v_max_f32 v10,v10,v11\n"
"v_mov_b32 v11,v10\n"
"s_nop 1\n"
"v_permlane16_swap_b32 v10,v11\n"
"v_max_f32 v10,v10,v11\n"
"v_mov_b32 v11,v10\n"
"s_nop 1\n"
"v_permlane32_swap_b32 v10,v11\n"
"v_max_f32 v10,v10,v11\n"
"v_add_f32 v10,0x41300000,v10\n"
"v_max_f32_e64 v10,0,v10\n"
"v_exp_f32 v11,-v10\n"
AR "v12,a0\n"
"v_subrev_f32 v12,s13,v12\n"
"v_fma_f32 v12,v12,v11,s13\n"
AW "a0,v12\n"
AR "v12,a20\n"
"v_subrev_f32 v12,s13,v12\n"
"v_fma_f32 v12,v12,v11,s13\n"
AW "a20,v12\n"
AR "v12,a40\n"
"v_subrev_f32 v12,s13,v12\n"
"v_fma_f32 v12,v12,v11,s13\n"
AW "a40,v12\n"
AR "v12,a60\n"
"v_subrev_f32 v12,s13,v12\n"
"v_fma_f32 v12,v12,v11,s13\n"
AW "a60,v12\n"
AR "v12,a80\n"
"v_subrev_f32 v12,s13,v12\n"
"v_fma_f32 v12,v12,v11,s13\n"
AW "a80,v12\n"
AR "v12,a100\n"
"v_subrev_f32 v12,s13,v12\n"
"v_fma_f32 v12,v12,v11,s13\n"
AW "a100,v12\n"
AR "v12,a120\n"
"v_subrev_f32 v12,s13,v12\n"
"v_fma_f32 v12,v12,v11,s13\n"
AW "a120,v12\n"
AR "v12,a140\n"
"v_subrev_f32 v12,s13,v12\n"
"v_fma_f32 v12,v12,v11,s13\n"
AW "a140,v12\n"
AR "v12,a160\n"
"v_subrev_f32 v12,s13,v12\n"
"v_fma_f32 v12,v12,v11,s13\n"
AW "a160,v12\n"
AR "v12,a180\n"
"v_subrev_f32 v12,s13,v12\n"
"v_fma_f32 v12,v12,v11,s13\n"
AW "a180,v12\n"
AR "v12,a200\n"
"v_subrev_f32 v12,s13,v12\n"
"v_fma_f32 v12,v12,v11,s13\n"
AW "a200,v12\n"
AR "v12,a220\n"
"v_subrev_f32 v12,s13,v12\n"
"v_fma_f32 v12,v12,v11,s13\n"
AW "a220,v12\n"
AR "v12,a1\n"
"v_subrev_f32 v12,s13,v12\n"
"v_fma_f32 v12,v12,v11,s13\n"
AW "a1,v12\n"
AR "v12,a21\n"
"v_subrev_f32 v12,s13,v12\n"
"v_fma_f32 v12,v12,v11,s13\n"
AW "a21,v12\n"
AR "v12,a41\n"
"v_subrev_f32 v12,s13,v12\n"
"v_fma_f32 v12,v12,v11,s13\n"
AW "a41,v12\n"
AR "v12,a61\n"
"v_subrev_f32 v12,s13,v12\n"
"v_fma_f32 v12,v12,v11,s13\n"
AW "a61,v12\n"
AR "v12,a81\n"
"v_subrev_f32 v12,s13,v12\n"
"v_fma_f32 v12,v12,v11,s13\n"
AW "a81,v12\n"
AR "v12,a101\n"
"v_subrev_f32 v12,s13,v12\n"
"v_fma_f32 v12,v12,v11,s13\n"
AW "a101,v12\n"
AR "v12,a121\n"
"v_subrev_f32 v12,s13,v12\n"
"v_fma_f32 v12,v12,v11,s13\n"
AW "a121,v12\n"
AR "v12,a141\n"
"v_subrev_f32 v12,s13,v12\n"
"v_fma_f32 v12,v12,v11,s13\n"
AW "a141,v12\n"
AR "v12,a161\n"
"v_subrev_f32 v12,s13,v12\n"
"v_fma_f32 v12,v12,v11,s13\n"
AW "a161,v12\n"
AR "v12,a181\n"
"v_subrev_f32 v12,s13,v12\n"
"v_fma_f32 v12,v12,v11,s13\n"
AW "a181,v12\n"
AR "v12,a201\n"
"v_subrev_f32 v12,s13,v12\n"
"v_fma_f32 v12,v12,v11,s13\n"
AW "a201,v12\n"
AR "v12,a221\n"
"v_subrev_f32 v12,s13,v12\n"
"v_fma_f32 v12,v12,v11,s13\n"
AW "a221,v12\n"
AR "v12,a2\n"
"v_subrev_f32 v12,s13,v12\n"
"v_fma_f32 v12,v12,v11,s13\n"
AW "a2,v12\n"
AR "v12,a22\n"
"v_subrev_f32 v12,s13,v12\n"
"v_fma_f32 v12,v12,v11,s13\n"
AW "a22,v12\n"
AR "v12,a42\n"
"v_subrev_f32 v12,s13,v12\n"
"v_fma_f32 v12,v12,v11,s13\n"
AW "a42,v12\n"
AR "v12,a62\n"
"v_subrev_f32 v12,s13,v12\n"
"v_fma_f32 v12,v12,v11,s13\n"
AW "a62,v12\n"
AR "v12,a82\n"
"v_subrev_f32 v12,s13,v12\n"
"v_fma_f32 v12,v12,v11,s13\n"
AW "a82,v12\n"
AR "v12,a102\n"
"v_subrev_f32 v12,s13,v12\n"
"v_fma_f32 v12,v12,v11,s13\n"
AW "a102,v12\n"
AR "v12,a122\n"
"v_subrev_f32 v12,s13,v12\n"
"v_fma_f32 v12,v12,v11,s13\n"
AW "a122,v12\n"
AR "v12,a142\n"
"v_subrev_f32 v12,s13,v12\n"
"v_fma_f32 v12,v12,v11,s13\n"
AW "a142,v12\n"
AR "v12,a162\n"
"v_subrev_f32 v12,s13,v12\n"
"v_fma_f32 v12,v12,v11,s13\n"
AW "a162,v12\n"
AR "v12,a182\n"
"v_subrev_f32 v12,s13,v12\n"
"v_fma_f32 v12,v12,v11,s13\n"
AW "a182,v12\n"
AR "v12,a202\n"
"v_subrev_f32 v12,s13,v12\n"
"v_fma_f32 v12,v12,v11,s13\n"
AW "a202,v12\n"
AR "v12,a222\n"
"v_subrev_f32 v12,s13,v12\n"
"v_fma_f32 v12,v12,v11,s13\n"
AW "a222,v12\n"
AR "v12,a3\n"
"v_subrev_f32 v12,s13,v12\n"
"v_fma_f32 v12,v12,v11,s13\n"
AW "a3,v12\n"
AR "v12,a23\n"
"v_subrev_f32 v12,s13,v12\n"
"v_fma_f32 v12,v12,v11,s13\n"
AW "a23,v12\n"
AR "v12,a43\n"
"v_subrev_f32 v12,s13,v12\n"
"v_fma_f32 v12,v12,v11,s13\n"
AW "a43,v12\n"
AR "v12,a63\n"
"v_subrev_f32 v12,s13,v12\n"
"v_fma_f32 v12,v12,v11,s13\n"
AW "a63,v12\n"
AR "v12,a83\n"
"v_subrev_f32 v12,s13,v12\n"
"v_fma_f32 v12,v12,v11,s13\n"
AW "a83,v12\n"
AR "v12,a103\n"
"v_subrev_f32 v12,s13,v12\n"
"v_fma_f32 v12,v12,v11,s13\n"
AW "a103,v12\n"
AR "v12,a123\n"
"v_subrev_f32 v12,s13,v12\n"
"v_fma_f32 v12,v12,v11,s13\n"
AW "a123,v12\n"
AR "v12,a143\n"
"v_subrev_f32 v12,s13,v12\n"
"v_fma_f32 v12,v12,v11,s13\n"
AW "a143,v12\n"
AR "v12,a163\n"
"v_subrev_f32 v12,s13,v12\n"
"v_fma_f32 v12,v12,v11,s13\n"
AW "a163,v12\n"
AR "v12,a183\n"
"v_subrev_f32 v12,s13,v12\n"
"v_fma_f32 v12,v12,v11,s13\n"
AW "a183,v12\n"
AR "v12,a203\n"
"v_subrev_f32 v12,s13,v12\n"
"v_fma_f32 v12,v12,v11,s13\n"
AW "a203,v12\n"
AR "v12,a223\n"
"v_subrev_f32 v12,s13,v12\n"
"v_fma_f32 v12,v12,v11,s13\n"
AW "a223,v12\n"
"v_mul_f32 v57,v52,v11\n"
"v_sub_f32 v32,v32,v10\n"
"v_sub_f32 v33,v33,v10\n"
"v_sub_f32 v34,v34,v10\n"
"v_sub_f32 v35,v35,v10\n"
"v_sub_f32 v72,v72,v10\n"
"v_sub_f32 v73,v73,v10\n"
"v_sub_f32 v74,v74,v10\n"
"v_sub_f32 v75,v75,v10\n"
"v_sub_f32 v92,v92,v10\n"
"v_sub_f32 v93,v93,v10\n"
"v_sub_f32 v94,v94,v10\n"
"v_sub_f32 v95,v95,v10\n"
"v_sub_f32 v112,v112,v10\n"
"v_sub_f32 v113,v113,v10\n"
"v_sub_f32 v114,v114,v10\n"
"v_sub_f32 v115,v115,v10\n"
"v_exp_f32 v92,v92\n"
"v_exp_f32 v93,v93\n"
"v_exp_f32 v94,v94\n"
"v_exp_f32 v95,v95\n"
"v_exp_f32 v112,v112\n"
"v_exp_f32 v113,v113\n"
"v_exp_f32 v114,v114\n"
"v_exp_f32 v115,v115\n"
"v_add_f32 v57,v57,v92\n"
"v_add_f32 v57,v57,v93\n"
"v_add_f32 v57,v57,v94\n"
"v_add_f32 v57,v57,v95\n"
"v_add_f32 v57,v57,v112\n"
"v_add_f32 v57,v57,v113\n"
"v_add_f32 v57,v57,v114\n"
"v_add_f32_e64 v57,v57,v115\n"
CV "v92,v92,v93\n"
CV "v93,v94,v95\n"
CV "v94,v112,v113\n"
CV "v95,v114,v115\n"
"v_max3_f32 v10,v96,v97,v98\n"
"v_max3_f32 v11,v99,v116,v117\n"
"v_max3_f32 v10,v10,v118,v119\n"
"v_max_f32 v10,v10,v11\n"
"v_mov_b32 v11,v10\n"
"s_nop 1\n"
"v_permlane16_swap_b32 v10,v11\n"
"v_max_f32 v10,v10,v11\n"
"v_mov_b32 v11,v10\n"
"s_nop 1\n"
"v_permlane32_swap_b32 v10,v11\n"
"v_max_f32 v10,v10,v11\n"
"v_add_f32 v10,0x41300000,v10\n"
"v_max_f32 v10,0,v10\n"
"v_exp_f32 v11,-v10\n"
AR "v12,a4\n"
"v_subrev_f32 v12,s13,v12\n"
"v_fma_f32 v12,v12,v11,s13\n"
AW "a4,v12\n"
AR "v12,a24\n"
"v_subrev_f32 v12,s13,v12\n"
"v_fma_f32 v12,v12,v11,s13\n"
AW "a24,v12\n"
AR "v12,a44\n"
"v_subrev_f32 v12,s13,v12\n"
"v_fma_f32 v12,v12,v11,s13\n"
AW "a44,v12\n"
AR "v12,a64\n"
"v_subrev_f32 v12,s13,v12\n"
"v_fma_f32 v12,v12,v11,s13\n"
AW "a64,v12\n"
AR "v12,a84\n"
"v_subrev_f32 v12,s13,v12\n"
"v_fma_f32 v12,v12,v11,s13\n"
AW "a84,v12\n"
AR "v12,a104\n"
"v_subrev_f32 v12,s13,v12\n"
"v_fma_f32 v12,v12,v11,s13\n"
AW "a104,v12\n"
AR "v12,a124\n"
"v_subrev_f32 v12,s13,v12\n"
"v_fma_f32 v12,v12,v11,s13\n"
AW "a124,v12\n"
AR "v12,a144\n"
"v_subrev_f32 v12,s13,v12\n"
"v_fma_f32 v12,v12,v11,s13\n"
AW "a144,v12\n"
AR "v12,a164\n"
"v_subrev_f32 v12,s13,v12\n"
"v_fma_f32 v12,v12,v11,s13\n"
AW "a164,v12\n"
AR "v12,a184\n"
"v_subrev_f32 v12,s13,v12\n"
"v_fma_f32 v12,v12,v11,s13\n"
AW "a184,v12\n"
AR "v12,a204\n"
"v_subrev_f32 v12,s13,v12\n"
"v_fma_f32 v12,v12,v11,s13\n"
AW "a204,v12\n"
AR "v12,a224\n"
"v_subrev_f32 v12,s13,v12\n"
"v_fma_f32 v12,v12,v11,s13\n"
AW "a224,v12\n"
AR "v12,a5\n"
"v_subrev_f32 v12,s13,v12\n"
"v_fma_f32 v12,v12,v11,s13\n"
AW "a5,v12\n"
AR "v12,a25\n"
"v_subrev_f32 v12,s13,v12\n"
"v_fma_f32 v12,v12,v11,s13\n"
AW "a25,v12\n"
AR "v12,a45\n"
"v_subrev_f32 v12,s13,v12\n"
"v_fma_f32 v12,v12,v11,s13\n"
AW "a45,v12\n"
AR "v12,a65\n"
"v_subrev_f32 v12,s13,v12\n"
"v_fma_f32 v12,v12,v11,s13\n"
AW "a65,v12\n"
AR "v12,a85\n"
"v_subrev_f32 v12,s13,v12\n"
"v_fma_f32 v12,v12,v11,s13\n"
AW "a85,v12\n"
AR "v12,a105\n"
"v_subrev_f32 v12,s13,v12\n"
"v_fma_f32 v12,v12,v11,s13\n"
AW "a105,v12\n"
AR "v12,a125\n"
"v_subrev_f32 v12,s13,v12\n"
"v_fma_f32 v12,v12,v11,s13\n"
AW "a125,v12\n"
AR "v12,a145\n"
"v_subrev_f32 v12,s13,v12\n"
"v_fma_f32 v12,v12,v11,s13\n"
AW "a145,v12\n"
AR "v12,a165\n"
"v_subrev_f32 v12,s13,v12\n"
"v_fma_f32 v12,v12,v11,s13\n"
AW "a165,v12\n"
AR "v12,a185\n"
"v_subrev_f32 v12,s13,v12\n"
"v_fma_f32 v12,v12,v11,s13\n"
AW "a185,v12\n"
AR "v12,a205\n"
"v_subrev_f32 v12,s13,v12\n"
"v_fma_f32 v12,v12,v11,s13\n"
AW "a205,v12\n"
AR "v12,a225\n"
"v_subrev_f32 v12,s13,v12\n"
"v_fma_f32 v12,v12,v11,s13\n"
AW "a225,v12\n"
AR "v12,a6\n"
"v_subrev_f32 v12,s13,v12\n"
"v_fma_f32 v12,v12,v11,s13\n"
AW "a6,v12\n"
AR "v12,a26\n"
"v_subrev_f32 v12,s13,v12\n"
"v_fma_f32 v12,v12,v11,s13\n"
AW "a26,v12\n"
AR "v12,a46\n"
"v_subrev_f32 v12,s13,v12\n"
"v_fma_f32 v12,v12,v11,s13\n"
AW "a46,v12\n"
AR "v12,a66\n"
"v_subrev_f32 v12,s13,v12\n"
"v_fma_f32 v12,v12,v11,s13\n"
AW "a66,v12\n"
AR "v12,a86\n"
"v_subrev_f32 v12,s13,v12\n"
"v_fma_f32 v12,v12,v11,s13\n"
AW "a86,v12\n"
AR "v12,a106\n"
"v_subrev_f32 v12,s13,v12\n"
"v_fma_f32 v12,v12,v11,s13\n"
AW "a106,v12\n"
AR "v12,a126\n"
"v_subrev_f32 v12,s13,v12\n"
"v_fma_f32 v12,v12,v11,s13\n"
AW "a126,v12\n"
AR "v12,a146\n"
"v_subrev_f32 v12,s13,v12\n"
"v_fma_f32 v12,v12,v11,s13\n"
AW "a146,v12\n"
AR "v12,a166\n"
"v_subrev_f32 v12,s13,v12\n"
"v_fma_f32 v12,v12,v11,s13\n"
AW "a166,v12\n"
AR "v12,a186\n"
"v_subrev_f32 v12,s13,v12\n"
"v_fma_f32 v12,v12,v11,s13\n"
AW "a186,v12\n"
AR "v12,a206\n"
"v_subrev_f32 v12,s13,v12\n"
"v_fma_f32 v12,v12,v11,s13\n"
AW "a206,v12\n"
AR "v12,a226\n"
"v_subrev_f32 v12,s13,v12\n"
"v_fma_f32 v12,v12,v11,s13\n"
AW "a226,v12\n"
AR "v12,a7\n"
"v_subrev_f32 v12,s13,v12\n"
"v_fma_f32 v12,v12,v11,s13\n"
AW "a7,v12\n"
AR "v12,a27\n"
"v_subrev_f32 v12,s13,v12\n"
"v_fma_f32 v12,v12,v11,s13\n"
AW "a27,v12\n"
AR "v12,a47\n"
"v_subrev_f32 v12,s13,v12\n"
"v_fma_f32 v12,v12,v11,s13\n"
AW "a47,v12\n"
AR "v12,a67\n"
"v_subrev_f32 v12,s13,v12\n"
"v_fma_f32 v12,v12,v11,s13\n"
AW "a67,v12\n"
AR "v12,a87\n"
"v_subrev_f32 v12,s13,v12\n"
"v_fma_f32 v12,v12,v11,s13\n"
AW "a87,v12\n"
AR "v12,a107\n"
"v_subrev_f32 v12,s13,v12\n"
"v_fma_f32 v12,v12,v11,s13\n"
AW "a107,v12\n"
AR "v12,a127\n"
"v_subrev_f32 v12,s13,v12\n"
"v_fma_f32 v12,v12,v11,s13\n"
AW "a127,v12\n"
AR "v12,a147\n"
"v_subrev_f32 v12,s13,v12\n"
"v_fma_f32 v12,v12,v11,s13\n"
AW "a147,v12\n"
AR "v12,a167\n"
"v_subrev_f32 v12,s13,v12\n"
"v_fma_f32 v12,v12,v11,s13\n"
AW "a167,v12\n"
AR "v12,a187\n"
"v_subrev_f32 v12,s13,v12\n"
"v_fma_f32 v12,v12,v11,s13\n"
AW "a187,v12\n"
AR "v12,a207\n"
"v_subrev_f32 v12,s13,v12\n"
"v_fma_f32 v12,v12,v11,s13\n"
AW "a207,v12\n"
AR "v12,a227\n"
"v_subrev_f32 v12,s13,v12\n"
"v_fma_f32 v12,v12,v11,s13\n"
AW "a227,v12\n"
"v_mul_f32 v58,v53,v11\n"
"v_sub_f32 v36,v36,v10\n"
"v_sub_f32 v37,v37,v10\n"
"v_sub_f32 v38,v38,v10\n"
"v_sub_f32 v39,v39,v10\n"
"v_sub_f32 v76,v76,v10\n"
"v_sub_f32 v77,v77,v10\n"
"v_sub_f32 v78,v78,v10\n"
"v_sub_f32 v79,v79,v10\n"
"v_sub_f32 v96,v96,v10\n"
"v_sub_f32 v97,v97,v10\n"
"v_sub_f32 v98,v98,v10\n"
"v_sub_f32 v99,v99,v10\n"
"v_sub_f32 v116,v116,v10\n"
"v_sub_f32 v117,v117,v10\n"
"v_sub_f32 v118,v118,v10\n"
"v_sub_f32 v119,v119,v10\n"
"v_exp_f32 v96,v96\n"
"v_exp_f32 v97,v97\n"
"v_exp_f32 v98,v98\n"
"v_exp_f32 v99,v99\n"
"v_exp_f32 v116,v116\n"
"v_exp_f32 v117,v117\n"
"v_exp_f32 v118,v118\n"
"v_exp_f32 v119,v119\n"
"v_add_f32 v58,v58,v96\n"
"v_add_f32 v58,v58,v97\n"
"v_add_f32 v58,v58,v98\n"
"v_add_f32 v58,v58,v99\n"
"v_add_f32 v58,v58,v116\n"
"v_add_f32 v58,v58,v117\n"
"v_add_f32 v58,v58,v118\n"
"v_add_f32_e64 v58,v58,v119\n"
CV "v96,v96,v97\n"
CV "v97,v98,v99\n"
CV "v98,v116,v117\n"
CV "v99,v118,v119\n"
"v_max3_f32 v10,v100,v101,v102\n"
"v_max3_f32 v11,v103,v120,v121\n"
"v_max3_f32 v10,v10,v122,v123\n"
"v_max_f32 v10,v10,v11\n"
"v_mov_b32 v11,v10\n"
"s_nop 1\n"
"v_permlane16_swap_b32 v10,v11\n"
"v_max_f32 v10,v10,v11\n"
"v_mov_b32 v11,v10\n"
"s_nop 1\n"
"v_permlane32_swap_b32 v10,v11\n"
"v_max_f32 v10,v10,v11\n"
"v_add_f32 v10,0x41300000,v10\n"
"v_max_f32 v10,0,v10\n"
"v_exp_f32 v11,-v10\n"
AR "v12,a8\n"
"v_subrev_f32 v12,s13,v12\n"
"v_fma_f32 v12,v12,v11,s13\n"
AW "a8,v12\n"
AR "v12,a28\n"
"v_subrev_f32 v12,s13,v12\n"
"v_fma_f32 v12,v12,v11,s13\n"
AW "a28,v12\n"
AR "v12,a48\n"
"v_subrev_f32 v12,s13,v12\n"
"v_fma_f32 v12,v12,v11,s13\n"
AW "a48,v12\n"
AR "v12,a68\n"
"v_subrev_f32 v12,s13,v12\n"
"v_fma_f32 v12,v12,v11,s13\n"
AW "a68,v12\n"
AR "v12,a88\n"
"v_subrev_f32 v12,s13,v12\n"
"v_fma_f32 v12,v12,v11,s13\n"
AW "a88,v12\n"
AR "v12,a108\n"
"v_subrev_f32 v12,s13,v12\n"
"v_fma_f32 v12,v12,v11,s13\n"
AW "a108,v12\n"
AR "v12,a128\n"
"v_subrev_f32 v12,s13,v12\n"
"v_fma_f32 v12,v12,v11,s13\n"
AW "a128,v12\n"
AR "v12,a148\n"
"v_subrev_f32 v12,s13,v12\n"
"v_fma_f32 v12,v12,v11,s13\n"
AW "a148,v12\n"
AR "v12,a168\n"
"v_subrev_f32 v12,s13,v12\n"
"v_fma_f32 v12,v12,v11,s13\n"
AW "a168,v12\n"
AR "v12,a188\n"
"v_subrev_f32 v12,s13,v12\n"
"v_fma_f32 v12,v12,v11,s13\n"
AW "a188,v12\n"
AR "v12,a208\n"
"v_subrev_f32 v12,s13,v12\n"
"v_fma_f32 v12,v12,v11,s13\n"
AW "a208,v12\n"
AR "v12,a228\n"
"v_subrev_f32 v12,s13,v12\n"
"v_fma_f32 v12,v12,v11,s13\n"
AW "a228,v12\n"
AR "v12,a9\n"
"v_subrev_f32 v12,s13,v12\n"
"v_fma_f32 v12,v12,v11,s13\n"
AW "a9,v12\n"
AR "v12,a29\n"
"v_subrev_f32 v12,s13,v12\n"
"v_fma_f32 v12,v12,v11,s13\n"
AW "a29,v12\n"
AR "v12,a49\n"
"v_subrev_f32 v12,s13,v12\n"
"v_fma_f32 v12,v12,v11,s13\n"
AW "a49,v12\n"
AR "v12,a69\n"
"v_subrev_f32 v12,s13,v12\n"
"v_fma_f32 v12,v12,v11,s13\n"
AW "a69,v12\n"
AR "v12,a89\n"
"v_subrev_f32 v12,s13,v12\n"
"v_fma_f32 v12,v12,v11,s13\n"
AW "a89,v12\n"
AR "v12,a109\n"
"v_subrev_f32 v12,s13,v12\n"
"v_fma_f32 v12,v12,v11,s13\n"
AW "a109,v12\n"
AR "v12,a129\n"
"v_subrev_f32 v12,s13,v12\n"
"v_fma_f32 v12,v12,v11,s13\n"
AW "a129,v12\n"
AR "v12,a149\n"
"v_subrev_f32 v12,s13,v12\n"
"v_fma_f32 v12,v12,v11,s13\n"
AW "a149,v12\n"
AR "v12,a169\n"
"v_subrev_f32 v12,s13,v12\n"
"v_fma_f32 v12,v12,v11,s13\n"
AW "a169,v12\n"
AR "v12,a189\n"
"v_subrev_f32 v12,s13,v12\n"
"v_fma_f32 v12,v12,v11,s13\n"
AW "a189,v12\n"
AR "v12,a209\n"
"v_subrev_f32 v12,s13,v12\n"
"v_fma_f32 v12,v12,v11,s13\n"
AW "a209,v12\n"
AR "v12,a229\n"
"v_subrev_f32 v12,s13,v12\n"
"v_fma_f32 v12,v12,v11,s13\n"
AW "a229,v12\n"
AR "v12,a10\n"
"v_subrev_f32 v12,s13,v12\n"
"v_fma_f32 v12,v12,v11,s13\n"
AW "a10,v12\n"
AR "v12,a30\n"
"v_subrev_f32 v12,s13,v12\n"
"v_fma_f32 v12,v12,v11,s13\n"
AW "a30,v12\n"
AR "v12,a50\n"
"v_subrev_f32 v12,s13,v12\n"
"v_fma_f32 v12,v12,v11,s13\n"
AW "a50,v12\n"
AR "v12,a70\n"
"v_subrev_f32 v12,s13,v12\n"
"v_fma_f32 v12,v12,v11,s13\n"
AW "a70,v12\n"
AR "v12,a90\n"
"v_subrev_f32 v12,s13,v12\n"
"v_fma_f32 v12,v12,v11,s13\n"
AW "a90,v12\n"
AR "v12,a110\n"
"v_subrev_f32 v12,s13,v12\n"
"v_fma_f32 v12,v12,v11,s13\n"
AW "a110,v12\n"
AR "v12,a130\n"
"v_subrev_f32 v12,s13,v12\n"
"v_fma_f32 v12,v12,v11,s13\n"
AW "a130,v12\n"
AR "v12,a150\n"
"v_subrev_f32 v12,s13,v12\n"
"v_fma_f32 v12,v12,v11,s13\n"
AW "a150,v12\n"
AR "v12,a170\n"
"v_subrev_f32 v12,s13,v12\n"
"v_fma_f32 v12,v12,v11,s13\n"
AW "a170,v12\n"
AR "v12,a190\n"
"v_subrev_f32 v12,s13,v12\n"
"v_fma_f32 v12,v12,v11,s13\n"
AW "a190,v12\n"
AR "v12,a210\n"
"v_subrev_f32 v12,s13,v12\n"
"v_fma_f32 v12,v12,v11,s13\n"
AW "a210,v12\n"
AR "v12,a230\n"
"v_subrev_f32 v12,s13,v12\n"
"v_fma_f32 v12,v12,v11,s13\n"
AW "a230,v12\n"
AR "v12,a11\n"
"v_subrev_f32 v12,s13,v12\n"
"v_fma_f32 v12,v12,v11,s13\n"
AW "a11,v12\n"
AR "v12,a31\n"
"v_subrev_f32 v12,s13,v12\n"
"v_fma_f32 v12,v12,v11,s13\n"
AW "a31,v12\n"
AR "v12,a51\n"
"v_subrev_f32 v12,s13,v12\n"
"v_fma_f32 v12,v12,v11,s13\n"
AW "a51,v12\n"
AR "v12,a71\n"
"v_subrev_f32 v12,s13,v12\n"
"v_fma_f32 v12,v12,v11,s13\n"
AW "a71,v12\n"
AR "v12,a91\n"
"v_subrev_f32 v12,s13,v12\n"
"v_fma_f32 v12,v12,v11,s13\n"
AW "a91,v12\n"
AR "v12,a111\n"
"v_subrev_f32 v12,s13,v12\n"
"v_fma_f32 v12,v12,v11,s13\n"
AW "a111,v12\n"
AR "v12,a131\n"
"v_subrev_f32 v12,s13,v12\n"
"v_fma_f32 v12,v12,v11,s13\n"
AW "a131,v12\n"
AR "v12,a151\n"
"v_subrev_f32 v12,s13,v12\n"
"v_fma_f32 v12,v12,v11,s13\n"
AW "a151,v12\n"
AR "v12,a171\n"
"v_subrev_f32 v12,s13,v12\n"
"v_fma_f32 v12,v12,v11,s13\n"
AW "a171,v12\n"
AR "v12,a191\n"
"v_subrev_f32 v12,s13,v12\n"
"v_fma_f32 v12,v12,v11,s13\n"
AW "a191,v12\n"
AR "v12,a211\n"
"v_subrev_f32 v12,s13,v12\n"
"v_fma_f32 v12,v12,v11,s13\n"
AW "a211,v12\n"
AR "v12,a231\n"
"v_subrev_f32 v12,s13,v12\n"
"v_fma_f32 v12,v12,v11,s13\n"
AW "a231,v12\n"
"v_mul_f32 v59,v54,v11\n"
"v_sub_f32 v40,v40,v10\n"
"v_sub_f32 v41,v41,v10\n"
"v_sub_f32 v42,v42,v10\n"
"v_sub_f32 v43,v43,v10\n"
"v_sub_f32 v80,v80,v10\n"
"v_sub_f32 v81,v81,v10\n"
"v_sub_f32 v82,v82,v10\n"
"v_sub_f32 v83,v83,v10\n"
"v_sub_f32 v100,v100,v10\n"
"v_sub_f32 v101,v101,v10\n"
"v_sub_f32 v102,v102,v10\n"
"v_sub_f32 v103,v103,v10\n"
"v_sub_f32 v120,v120,v10\n"
"v_sub_f32 v121,v121,v10\n"
"v_sub_f32 v122,v122,v10\n"
"v_sub_f32 v123,v123,v10\n"
"v_exp_f32 v100,v100\n"
"v_exp_f32 v101,v101\n"
"v_exp_f32 v102,v102\n"
"v_exp_f32 v103,v103\n"
"v_exp_f32 v120,v120\n"
"v_exp_f32 v121,v121\n"
"v_exp_f32 v122,v122\n"
"v_exp_f32 v123,v123\n"
"v_add_f32 v59,v59,v100\n"
"v_add_f32 v59,v59,v101\n"
"v_add_f32 v59,v59,v102\n"
"v_add_f32 v59,v59,v103\n"
"v_add_f32 v59,v59,v120\n"
"v_add_f32 v59,v59,v121\n"
"v_add_f32 v59,v59,v122\n"
"v_add_f32_e64 v59,v59,v123\n"
CV "v100,v100,v101\n"
CV "v101,v102,v103\n"
CV "v102,v120,v121\n"
CV "v103,v122,v123\n"
"v_max3_f32 v10,v104,v105,v106\n"
"v_max3_f32 v11,v107,v124,v125\n"
"v_max3_f32 v10,v10,v126,v127\n"
"v_max_f32 v10,v10,v11\n"
"v_mov_b32 v11,v10\n"
"s_nop 1\n"
"v_permlane16_swap_b32 v10,v11\n"
"v_max_f32 v10,v10,v11\n"
"v_mov_b32 v11,v10\n"
"s_nop 1\n"
"v_permlane32_swap_b32 v10,v11\n"
"v_max_f32_e64 v10,v10,v11\n"
"v_add_f32 v10,0x41300000,v10\n"
"v_max_f32 v10,0,v10\n"
"v_exp_f32 v11,-v10\n"
AR "v12,a12\n"
"v_subrev_f32 v12,s13,v12\n"
"v_fma_f32 v12,v12,v11,s13\n"
AW "a12,v12\n"
AR "v12,a32\n"
"v_subrev_f32 v12,s13,v12\n"
"v_fma_f32 v12,v12,v11,s13\n"
AW "a32,v12\n"
AR "v12,a52\n"
"v_subrev_f32 v12,s13,v12\n"
"v_fma_f32 v12,v12,v11,s13\n"
AW "a52,v12\n"
AR "v12,a72\n"
"v_subrev_f32 v12,s13,v12\n"
"v_fma_f32 v12,v12,v11,s13\n"
AW "a72,v12\n"
AR "v12,a92\n"
"v_subrev_f32 v12,s13,v12\n"
"v_fma_f32 v12,v12,v11,s13\n"
AW "a92,v12\n"
AR "v12,a112\n"
"v_subrev_f32 v12,s13,v12\n"
"v_fma_f32 v12,v12,v11,s13\n"
AW "a112,v12\n"
AR "v12,a132\n"
"v_subrev_f32 v12,s13,v12\n"
"v_fma_f32 v12,v12,v11,s13\n"
AW "a132,v12\n"
AR "v12,a152\n"
"v_subrev_f32 v12,s13,v12\n"
"v_fma_f32 v12,v12,v11,s13\n"
AW "a152,v12\n"
AR "v12,a172\n"
"v_subrev_f32 v12,s13,v12\n"
"v_fma_f32 v12,v12,v11,s13\n"
AW "a172,v12\n"
AR "v12,a192\n"
"v_subrev_f32 v12,s13,v12\n"
"v_fma_f32 v12,v12,v11,s13\n"
AW "a192,v12\n"
AR "v12,a212\n"
"v_subrev_f32 v12,s13,v12\n"
"v_fma_f32 v12,v12,v11,s13\n"
AW "a212,v12\n"
AR "v12,a232\n"
"v_subrev_f32 v12,s13,v12\n"
"v_fma_f32 v12,v12,v11,s13\n"
AW "a232,v12\n"
AR "v12,a13\n"
"v_subrev_f32 v12,s13,v12\n"
"v_fma_f32 v12,v12,v11,s13\n"
AW "a13,v12\n"
AR "v12,a33\n"
"v_subrev_f32 v12,s13,v12\n"
"v_fma_f32 v12,v12,v11,s13\n"
AW "a33,v12\n"
AR "v12,a53\n"
"v_subrev_f32 v12,s13,v12\n"
"v_fma_f32 v12,v12,v11,s13\n"
AW "a53,v12\n"
AR "v12,a73\n"
"v_subrev_f32 v12,s13,v12\n"
"v_fma_f32 v12,v12,v11,s13\n"
AW "a73,v12\n"
AR "v12,a93\n"
"v_subrev_f32 v12,s13,v12\n"
"v_fma_f32 v12,v12,v11,s13\n"
AW "a93,v12\n"
AR "v12,a113\n"
"v_subrev_f32 v12,s13,v12\n"
"v_fma_f32 v12,v12,v11,s13\n"
AW "a113,v12\n"
AR "v12,a133\n"
"v_subrev_f32 v12,s13,v12\n"
"v_fma_f32 v12,v12,v11,s13\n"
AW "a133,v12\n"
AR "v12,a153\n"
"v_subrev_f32 v12,s13,v12\n"
"v_fma_f32 v12,v12,v11,s13\n"
AW "a153,v12\n"
AR "v12,a173\n"
"v_subrev_f32 v12,s13,v12\n"
"v_fma_f32 v12,v12,v11,s13\n"
AW "a173,v12\n"
AR "v12,a193\n"
"v_subrev_f32 v12,s13,v12\n"
"v_fma_f32 v12,v12,v11,s13\n"
AW "a193,v12\n"
AR "v12,a213\n"
"v_subrev_f32 v12,s13,v12\n"
"v_fma_f32 v12,v12,v11,s13\n"
AW "a213,v12\n"
AR "v12,a233\n"
"v_subrev_f32 v12,s13,v12\n"
"v_fma_f32 v12,v12,v11,s13\n"
AW "a233,v12\n"
AR "v12,a14\n"
"v_subrev_f32 v12,s13,v12\n"
"v_fma_f32 v12,v12,v11,s13\n"
AW "a14,v12\n"
AR "v12,a34\n"
"v_subrev_f32 v12,s13,v12\n"
"v_fma_f32 v12,v12,v11,s13\n"
AW "a34,v12\n"
AR "v12,a54\n"
"v_subrev_f32 v12,s13,v12\n"
"v_fma_f32 v12,v12,v11,s13\n"
AW "a54,v12\n"
AR "v12,a74\n"
"v_subrev_f32 v12,s13,v12\n"
"v_fma_f32 v12,v12,v11,s13\n"
AW "a74,v12\n"
AR "v12,a94\n"
"v_subrev_f32 v12,s13,v12\n"
"v_fma_f32 v12,v12,v11,s13\n"
AW "a94,v12\n"
AR "v12,a114\n"
"v_subrev_f32 v12,s13,v12\n"
"v_fma_f32 v12,v12,v11,s13\n"
AW "a114,v12\n"
AR "v12,a134\n"
"v_subrev_f32 v12,s13,v12\n"
"v_fma_f32 v12,v12,v11,s13\n"
AW "a134,v12\n"
AR "v12,a154\n"
"v_subrev_f32 v12,s13,v12\n"
"v_fma_f32 v12,v12,v11,s13\n"
AW "a154,v12\n"
AR "v12,a174\n"
"v_subrev_f32 v12,s13,v12\n"
"v_fma_f32 v12,v12,v11,s13\n"
AW "a174,v12\n"
AR "v12,a194\n"
"v_subrev_f32 v12,s13,v12\n"
"v_fma_f32 v12,v12,v11,s13\n"
AW "a194,v12\n"
AR "v12,a214\n"
"v_subrev_f32 v12,s13,v12\n"
"v_fma_f32 v12,v12,v11,s13\n"
AW "a214,v12\n"
AR "v12,a234\n"
"v_subrev_f32 v12,s13,v12\n"
"v_fma_f32 v12,v12,v11,s13\n"
AW "a234,v12\n"
AR "v12,a15\n"
"v_subrev_f32 v12,s13,v12\n"
"v_fma_f32 v12,v12,v11,s13\n"
AW "a15,v12\n"
AR "v12,a35\n"
"v_subrev_f32 v12,s13,v12\n"
"v_fma_f32 v12,v12,v11,s13\n"
AW "a35,v12\n"
AR "v12,a55\n"
"v_subrev_f32 v12,s13,v12\n"
"v_fma_f32 v12,v12,v11,s13\n"
AW "a55,v12\n"
AR "v12,a75\n"
"v_subrev_f32 v12,s13,v12\n"
"v_fma_f32 v12,v12,v11,s13\n"
AW "a75,v12\n"
AR "v12,a95\n"
"v_subrev_f32 v12,s13,v12\n"
"v_fma_f32 v12,v12,v11,s13\n"
AW "a95,v12\n"
AR "v12,a115\n"
"v_subrev_f32 v12,s13,v12\n"
"v_fma_f32 v12,v12,v11,s13\n"
AW "a115,v12\n"
AR "v12,a135\n"
"v_subrev_f32 v12,s13,v12\n"
"v_fma_f32 v12,v12,v11,s13\n"
AW "a135,v12\n"
AR "v12,a155\n"
"v_subrev_f32 v12,s13,v12\n"
"v_fma_f32 v12,v12,v11,s13\n"
AW "a155,v12\n"
AR "v12,a175\n"
"v_subrev_f32 v12,s13,v12\n"
"v_fma_f32 v12,v12,v11,s13\n"
AW "a175,v12\n"
AR "v12,a195\n"
"v_subrev_f32 v12,s13,v12\n"
"v_fma_f32 v12,v12,v11,s13\n"
AW "a195,v12\n"
AR "v12,a215\n"
"v_subrev_f32 v12,s13,v12\n"
"v_fma_f32 v12,v12,v11,s13\n"
AW "a215,v12\n"
AR "v12,a235\n"
"v_subrev_f32 v12,s13,v12\n"
"v_fma_f32 v12,v12,v11,s13\n"
AW "a235,v12\n"
"v_mul_f32 v60,v55,v11\n"
"v_sub_f32 v44,v44,v10\n"
"v_sub_f32 v45,v45,v10\n"
"v_sub_f32 v46,v46,v10\n"
"v_sub_f32 v47,v47,v10\n"
"v_sub_f32 v84,v84,v10\n"
"v_sub_f32 v85,v85,v10\n"
"v_sub_f32 v86,v86,v10\n"
"v_sub_f32 v87,v87,v10\n"
"v_sub_f32 v104,v104,v10\n"
"v_sub_f32 v105,v105,v10\n"
"v_sub_f32 v106,v106,v10\n"
"v_sub_f32 v107,v107,v10\n"
"v_sub_f32 v124,v124,v10\n"
"v_sub_f32 v125,v125,v10\n"
"v_sub_f32 v126,v126,v10\n"
"v_sub_f32 v127,v127,v10\n"
"v_exp_f32 v104,v104\n"
"v_exp_f32 v105,v105\n"
"v_exp_f32 v106,v106\n"
"v_exp_f32 v107,v107\n"
"v_exp_f32 v124,v124\n"
"v_exp_f32 v125,v125\n"
"v_exp_f32 v126,v126\n"
"v_exp_f32 v127,v127\n"
"v_add_f32 v60,v60,v104\n"
"v_add_f32 v60,v60,v105\n"
"v_add_f32 v60,v60,v106\n"
"v_add_f32 v60,v60,v107\n"
"v_add_f32 v60,v60,v124\n"
"v_add_f32 v60,v60,v125\n"
"v_add_f32 v60,v60,v126\n"
"v_add_f32 v60,v60,v127\n"
CV "v104,v104,v105\n"
CV "v105,v106,v107\n"
CV "v106,v124,v125\n"
CV "v107,v126,v127\n"
"v_max3_f32 v10,v108,v109,v110\n"
"v_max3_f32 v11,v111,v128,v129\n"
"v_max3_f32 v10,v10,v130,v131\n"
"v_max_f32 v10,v10,v11\n"
"v_mov_b32 v11,v10\n"
"s_nop 1\n"
"v_permlane16_swap_b32 v10,v11\n"
"v_max_f32 v10,v10,v11\n"
"v_mov_b32 v11,v10\n"
"s_nop 1\n"
"v_permlane32_swap_b32 v10,v11\n"
"v_max_f32 v10,v10,v11\n"
"v_add_f32 v10,0x41300000,v10\n"
"v_max_f32 v10,0,v10\n"
"v_exp_f32 v11,-v10\n"
AR "v12,a16\n"
"v_subrev_f32 v12,s13,v12\n"
"v_fma_f32 v12,v12,v11,s13\n"
AW "a16,v12\n"
AR "v12,a36\n"
"v_subrev_f32 v12,s13,v12\n"
"v_fma_f32 v12,v12,v11,s13\n"
AW "a36,v12\n"
AR "v12,a56\n"
"v_subrev_f32 v12,s13,v12\n"
"v_fma_f32 v12,v12,v11,s13\n"
AW "a56,v12\n"
AR "v12,a76\n"
"v_subrev_f32 v12,s13,v12\n"
"v_fma_f32 v12,v12,v11,s13\n"
AW "a76,v12\n"
AR "v12,a96\n"
"v_subrev_f32 v12,s13,v12\n"
"v_fma_f32 v12,v12,v11,s13\n"
AW "a96,v12\n"
AR "v12,a116\n"
"v_subrev_f32 v12,s13,v12\n"
"v_fma_f32 v12,v12,v11,s13\n"
AW "a116,v12\n"
AR "v12,a136\n"
"v_subrev_f32 v12,s13,v12\n"
"v_fma_f32 v12,v12,v11,s13\n"
AW "a136,v12\n"
AR "v12,a156\n"
"v_subrev_f32 v12,s13,v12\n"
"v_fma_f32 v12,v12,v11,s13\n"
AW "a156,v12\n"
AR "v12,a176\n"
"v_subrev_f32 v12,s13,v12\n"
"v_fma_f32 v12,v12,v11,s13\n"
AW "a176,v12\n"
AR "v12,a196\n"
"v_subrev_f32 v12,s13,v12\n"
"v_fma_f32 v12,v12,v11,s13\n"
AW "a196,v12\n"
AR "v12,a216\n"
"v_subrev_f32 v12,s13,v12\n"
"v_fma_f32 v12,v12,v11,s13\n"
AW "a216,v12\n"
AR "v12,a236\n"
"v_subrev_f32 v12,s13,v12\n"
"v_fma_f32 v12,v12,v11,s13\n"
AW "a236,v12\n"
AR "v12,a17\n"
"v_subrev_f32 v12,s13,v12\n"
"v_fma_f32 v12,v12,v11,s13\n"
AW "a17,v12\n"
AR "v12,a37\n"
"v_subrev_f32 v12,s13,v12\n"
"v_fma_f32 v12,v12,v11,s13\n"
AW "a37,v12\n"
AR "v12,a57\n"
"v_subrev_f32 v12,s13,v12\n"
"v_fma_f32 v12,v12,v11,s13\n"
AW "a57,v12\n"
AR "v12,a77\n"
"v_subrev_f32 v12,s13,v12\n"
"v_fma_f32 v12,v12,v11,s13\n"
AW "a77,v12\n"
AR "v12,a97\n"
"v_subrev_f32 v12,s13,v12\n"
"v_fma_f32 v12,v12,v11,s13\n"
AW "a97,v12\n"
AR "v12,a117\n"
"v_subrev_f32 v12,s13,v12\n"
"v_fma_f32 v12,v12,v11,s13\n"
AW "a117,v12\n"
AR "v12,a137\n"
"v_subrev_f32 v12,s13,v12\n"
"v_fma_f32 v12,v12,v11,s13\n"
AW "a137,v12\n"
AR "v12,a157\n"
"v_subrev_f32 v12,s13,v12\n"
"v_fma_f32 v12,v12,v11,s13\n"
AW "a157,v12\n"
AR "v12,a177\n"
"v_subrev_f32 v12,s13,v12\n"
"v_fma_f32 v12,v12,v11,s13\n"
AW "a177,v12\n"
AR "v12,a197\n"
"v_subrev_f32 v12,s13,v12\n"
"v_fma_f32 v12,v12,v11,s13\n"
AW "a197,v12\n"
AR "v12,a217\n"
"v_subrev_f32 v12,s13,v12\n"
"v_fma_f32 v12,v12,v11,s13\n"
AW "a217,v12\n"
AR "v12,a237\n"
"v_subrev_f32 v12,s13,v12\n"
"v_fma_f32 v12,v12,v11,s13\n"
AW "a237,v12\n"
AR "v12,a18\n"
"v_subrev_f32 v12,s13,v12\n"
"v_fma_f32 v12,v12,v11,s13\n"
AW "a18,v12\n"
AR "v12,a38\n"
"v_subrev_f32 v12,s13,v12\n"
"v_fma_f32 v12,v12,v11,s13\n"
AW "a38,v12\n"
AR "v12,a58\n"
"v_subrev_f32 v12,s13,v12\n"
"v_fma_f32 v12,v12,v11,s13\n"
AW "a58,v12\n"
AR "v12,a78\n"
"v_subrev_f32 v12,s13,v12\n"
"v_fma_f32 v12,v12,v11,s13\n"
AW "a78,v12\n"
AR "v12,a98\n"
"v_subrev_f32 v12,s13,v12\n"
"v_fma_f32 v12,v12,v11,s13\n"
AW "a98,v12\n"
AR "v12,a118\n"
"v_subrev_f32 v12,s13,v12\n"
"v_fma_f32 v12,v12,v11,s13\n"
AW "a118,v12\n"
AR "v12,a138\n"
"v_subrev_f32 v12,s13,v12\n"
"v_fma_f32 v12,v12,v11,s13\n"
AW "a138,v12\n"
AR "v12,a158\n"
"v_subrev_f32 v12,s13,v12\n"
"v_fma_f32 v12,v12,v11,s13\n"
AW "a158,v12\n"
AR "v12,a178\n"
"v_subrev_f32 v12,s13,v12\n"
"v_fma_f32 v12,v12,v11,s13\n"
AW "a178,v12\n"
AR "v12,a198\n"
"v_subrev_f32 v12,s13,v12\n"
"v_fma_f32 v12,v12,v11,s13\n"
AW "a198,v12\n"
AR "v12,a218\n"
"v_subrev_f32 v12,s13,v12\n"
"v_fma_f32 v12,v12,v11,s13\n"
AW "a218,v12\n"
AR "v12,a238\n"
"v_subrev_f32 v12,s13,v12\n"
"v_fma_f32 v12,v12,v11,s13\n"
AW "a238,v12\n"
AR "v12,a19\n"
"v_subrev_f32 v12,s13,v12\n"
"v_fma_f32 v12,v12,v11,s13\n"
AW "a19,v12\n"
AR "v12,a39\n"
"v_subrev_f32 v12,s13,v12\n"
"v_fma_f32 v12,v12,v11,s13\n"
AW "a39,v12\n"
AR "v12,a59\n"
"v_subrev_f32 v12,s13,v12\n"
"v_fma_f32 v12,v12,v11,s13\n"
AW "a59,v12\n"
AR "v12,a79\n"
"v_subrev_f32 v12,s13,v12\n"
"v_fma_f32 v12,v12,v11,s13\n"
AW "a79,v12\n"
AR "v12,a99\n"
"v_subrev_f32 v12,s13,v12\n"
"v_fma_f32 v12,v12,v11,s13\n"
AW "a99,v12\n"
AR "v12,a119\n"
"v_subrev_f32 v12,s13,v12\n"
"v_fma_f32 v12,v12,v11,s13\n"
AW "a119,v12\n"
AR "v12,a139\n"
"v_subrev_f32 v12,s13,v12\n"
"v_fma_f32 v12,v12,v11,s13\n"
AW "a139,v12\n"
AR "v12,a159\n"
"v_subrev_f32 v12,s13,v12\n"
"v_fma_f32 v12,v12,v11,s13\n"
AW "a159,v12\n"
AR "v12,a179\n"
"v_subrev_f32 v12,s13,v12\n"
"v_fma_f32 v12,v12,v11,s13\n"
AW "a179,v12\n"
AR "v12,a199\n"
"v_subrev_f32 v12,s13,v12\n"
"v_fma_f32 v12,v12,v11,s13\n"
AW "a199,v12\n"
AR "v12,a219\n"
"v_subrev_f32 v12,s13,v12\n"
"v_fma_f32 v12,v12,v11,s13\n"
AW "a219,v12\n"
AR "v12,a239\n"
"v_subrev_f32 v12,s13,v12\n"
"v_fma_f32 v12,v12,v11,s13\n"
AW "a239,v12\n"
"v_mul_f32 v61,v56,v11\n"
"v_sub_f32 v48,v48,v10\n"
"v_sub_f32 v49,v49,v10\n"
"v_sub_f32 v50,v50,v10\n"
"v_sub_f32 v51,v51,v10\n"
"v_sub_f32 v88,v88,v10\n"
"v_sub_f32 v89,v89,v10\n"
"v_sub_f32 v90,v90,v10\n"
"v_sub_f32 v91,v91,v10\n"
"v_sub_f32 v108,v108,v10\n"
"v_sub_f32 v109,v109,v10\n"
"v_sub_f32 v110,v110,v10\n"
"v_sub_f32 v111,v111,v10\n"
"v_sub_f32 v128,v128,v10\n"
"v_sub_f32 v129,v129,v10\n"
"v_sub_f32 v130,v130,v10\n"
"v_sub_f32 v131,v131,v10\n"
"v_exp_f32 v108,v108\n"
"v_exp_f32 v109,v109\n"
"v_exp_f32 v110,v110\n"
"v_exp_f32 v111,v111\n"
"v_exp_f32 v128,v128\n"
"v_exp_f32 v129,v129\n"
"v_exp_f32 v130,v130\n"
"v_exp_f32 v131,v131\n"
"v_add_f32 v61,v61,v108\n"
"v_add_f32 v61,v61,v109\n"
"v_add_f32 v61,v61,v110\n"
"v_add_f32 v61,v61,v111\n"
"v_add_f32 v61,v61,v128\n"
"v_add_f32 v61,v61,v129\n"
"v_add_f32 v61,v61,v130\n"
"v_add_f32_e64 v61,v61,v131\n"
CV "v108,v108,v109\n"
CV "v109,v110,v111\n"
CV "v110,v128,v129\n"
CV "v111,v130,v131\n"
"s_nop 7\n"
"s_setpc_b64 s[52:53]\n"
".Ldone:\n"
    : "+{v0}"(tid), "+{s23}"(bid), "+{s[20:21]}"(ka)
    :
    : "v1", "v2", "v3", "v4", "v5", "v6", "v7", "v8", "v9", "v10", "v11", "v12", "v13", "v14", "v15", "v16", "v17", "v18", "v19", "v20", "v21", "v22", "v23", "v24", "v25", "v26", "v27", "v28", "v29", "v30", "v31", "v32", "v33", "v34", "v35", "v36", "v37", "v38", "v39", "v40", "v41", "v42", "v43", "v44", "v45", "v46", "v47", "v48", "v49", "v50", "v51", "v52", "v53", "v54", "v55", "v56", "v57", "v58", "v59", "v60", "v61", "v62", "v63", "v64", "v65", "v66", "v67", "v68", "v69", "v70", "v71", "v72", "v73", "v74", "v75", "v76", "v77", "v78", "v79", "v80", "v81", "v82", "v83", "v84", "v85", "v86", "v87", "v88", "v89", "v90", "v91", "v92", "v93", "v94", "v95", "v96", "v97", "v98", "v99", "v100", "v101", "v102", "v103", "v104", "v105", "v106", "v107", "v108", "v109", "v110", "v111", "v112", "v113", "v114", "v115", "v116", "v117", "v118", "v119", "v120", "v121", "v122", "v123", "v124", "v125", "v126", "v127", "v128", "v129", "v130", "v131", "v132", "v133", "v134", "v135", "v136", "v137", "v138", "v139", "v140", "v141", "v142", "v143", "v144", "v145", "v146", "v147", "v148", "v149", "v150", "v151", "v152", "v153", "v154", "v155", "v156", "v157", "v158", "v159", "v160", "v161", "v162", "v163", "v164", "v165", "v166", "v167", "v168", "v169", "v170", "v171", "v172", "v173", "v174", "v175", "v176", "v177", "v178", "v179", "v180", "v181", "v182", "v183", "v184", "v185", "v186", "v187", "v188", "v189", "v190", "v191", "v192", "v193", "v194", "v195", "v196", "v197", "v198", "v199", "v200", "v201", "v202", "v203", "v204", "v205", "v206", "v207", "v208", "v209", "v210", "v211", "v212", "v213", "v214", "v215", "v216", "v217", "v218", "v219", "v220", "v221", "v222", "v223", "v224", "v225", "v226", "v227", "v228", "v229", "v230", "v231", "v232", "v233", "v234", "v235", "v236", "v237", "v238", "v239", "v240", "v241", "v242", "v243", "v244", "v245", "v246", "v247", "v248", "v249", "v250", "v251", "v252", "v253", "v254", "v255", "a0", "a1", "a2", "a3", "a4", "a5", "a6", "a7", "a8", "a9", "a10", "a11", "a12", "a13", "a14", "a15", "a16", "a17", "a18", "a19", "a20", "a21", "a22", "a23", "a24", "a25", "a26", "a27", "a28", "a29", "a30", "a31", "a32", "a33", "a34", "a35", "a36", "a37", "a38", "a39", "a40", "a41", "a42", "a43", "a44", "a45", "a46", "a47", "a48", "a49", "a50", "a51", "a52", "a53", "a54", "a55", "a56", "a57", "a58", "a59", "a60", "a61", "a62", "a63", "a64", "a65", "a66", "a67", "a68", "a69", "a70", "a71", "a72", "a73", "a74", "a75", "a76", "a77", "a78", "a79", "a80", "a81", "a82", "a83", "a84", "a85", "a86", "a87", "a88", "a89", "a90", "a91", "a92", "a93", "a94", "a95", "a96", "a97", "a98", "a99", "a100", "a101", "a102", "a103", "a104", "a105", "a106", "a107", "a108", "a109", "a110", "a111", "a112", "a113", "a114", "a115", "a116", "a117", "a118", "a119", "a120", "a121", "a122", "a123", "a124", "a125", "a126", "a127", "a128", "a129", "a130", "a131", "a132", "a133", "a134", "a135", "a136", "a137", "a138", "a139", "a140", "a141", "a142", "a143", "a144", "a145", "a146", "a147", "a148", "a149", "a150", "a151", "a152", "a153", "a154", "a155", "a156", "a157", "a158", "a159", "a160", "a161", "a162", "a163", "a164", "a165", "a166", "a167", "a168", "a169", "a170", "a171", "a172", "a173", "a174", "a175", "a176", "a177", "a178", "a179", "a180", "a181", "a182", "a183", "a184", "a185", "a186", "a187", "a188", "a189", "a190", "a191", "a192", "a193", "a194", "a195", "a196", "a197", "a198", "a199", "a200", "a201", "a202", "a203", "a204", "a205", "a206", "a207", "a208", "a209", "a210", "a211", "a212", "a213", "a214", "a215", "a216", "a217", "a218", "a219", "a220", "a221", "a222", "a223", "a224", "a225", "a226", "a227", "a228", "a229", "a230", "a231", "a232", "a233", "a234", "a235", "a236", "a237", "a238", "a239", "a240", "a241", "a242", "a243", "a244", "a245", "a246", "a247", "a248", "a249", "a250", "a251", "a252", "a253", "a254", "a255", "s0", "s1", "s2", "s3", "s4", "s5", "s6", "s7", "s8", "s9", "s10", "s11", "s12", "s13", "s14", "s15", "s16", "s17", "s18", "s19", "s22", "s24", "s25", "s26", "s27", "s28", "s29", "s30", "s31", "s32", "s33", "s34", "s35", "s36", "s37", "s38", "s39", "s40", "s41", "s42", "s43", "s44", "s45", "s46", "s47", "s48", "s49", "s50", "s51", "s52", "s53", "s54", "s55", "s56", "s57", "s58", "s59", "s60", "s61", "s62", "s63", "s64", "s65", "s66", "s67", "s68", "s69", "s70", "s71", "s72", "s73", "s74", "s75", "s76", "s77", "s78", "s79", "s80", "s81", "s82", "s83", "s84", "s85", "s86", "s87", "s88", "s89", "s90", "s91", "s92", "s93", "s94", "s95", "s96", "s97", "s98", "s99", "s100", "s101", "vcc", "scc", "memory");
}

extern "C" void kernel_launch(void* const* d_in, const int* in_sizes, int n_in, void* d_out, int out_size, void* d_ws, size_t ws_size, hipStream_t stream) {
  Ptrs P;
  for (int br = 0; br < 2; ++br) { P.x[br] = d_in[5 * br]; P.k[br] = d_in[5 * br + 1]; P.v[br] = d_in[5 * br + 2]; P.wq[br] = d_in[5 * br + 3]; P.wo[br] = d_in[5 * br + 4]; }
  (void)hipFuncSetAttribute((const void*)fused_kernel, hipFuncAttributeMaxDynamicSharedMemorySize, LDS1);
  fused_kernel<<<256, 256, LDS1, stream>>>(P, (char*)d_ws, (char*)d_out);
}
```

```cpp
#include <hip/hip_runtime.h>
#include <hip/hip_bf16.h>

#define MF "v_mfma_f32_16x16x32_bf16 "
#define TR "ds_read_b64_tr_b16 "
#define AR "v_accvgpr_read_b32 "
#define AW "v_accvgpr_write_b32 "
#define CV "v_cvt_pk_bf16_f32 "
#define LD "buffer_load_dwordx4 "
constexpr unsigned LDS1 = 160 * 1024;

struct Ptrs { const void* x[2]; const void* k[2]; const void* v[2]; const void* wq[2]; const void* wo[2]; };

__global__ __launch_bounds__(256, 1) __attribute__((aligned(4096))) void fused_kernel(Ptrs P, char* __restrict__ ws, char* __restrict__ out) {
  unsigned tid = threadIdx.x, bid = blockIdx.x;
  unsigned long long ka = (unsigned long long)__builtin_amdgcn_kernarg_segment_ptr();
  asm volatile(
".p2align 5\n"
".Lcode0:\n"
"s_and_b32 s58,s23,4\n"
"s_lshl_b32 s58,s58,1\n"
"s_load_dwordx2 s[28:29],s[20:21],s58 offset:48\n"
"s_load_dwordx2 s[24:25],s[20:21],s58\n"
"s_load_dwordx2 s[32:33],s[20:21],s58 offset:16\n"
"s_load_dwordx2 s[36:37],s[20:21],s58 offset:32\n"
"s_load_dwordx2 s[48:49],s[20:21],s58 offset:64\n"
"s_memtime s[100:101]\n"
"s_load_dwordx2 s[40:41],s[20:21],0x50\n"
"s_load_dwordx2 s[44:45],s[20:21],0x58\n"
"v_readfirstlane_b32 s22,v0\n"
"v_and_b32 v9,63,v0\n"
"v_bfe_u32 v10,v0,3,3\n"
"v_and_b32 v11,7,v0\n"
"s_lshr_b32 s22,s22,6\n"
"s_or_b32 s22,s22,s58\n"
"s_and_b32 s58,s23,1\n"
"s_lshl_b32 s58,s58,3\n"
"s_lshr_b32 s59,s23,5\n"
"s_add_u32 s59,s59,s58\n"
"s_mul_i32 s59,s59,4112\n"
"s_or_b32 s22,s22,s59\n"
"s_and_b32 s59,s23,2\n"
"s_lshl_b32 s59,s59,1\n"
"s_bfe_u32 s60,s23,0x20003\n"
"s_or_b32 s59,s59,s60\n"
"s_lshl_b32 s60,s59,20\n"
"s_or_b32 s22,s22,s60\n"
"s_or_b32 s59,s59,s58\n"
"s_mul_i32 s59,s59,65792\n"
"s_xor_b32 s59,s59,524288\n"
"s_or_b32 s22,s22,s59\n"
"v_lshrrev_b32 v6,2,v0\n"
"v_and_b32 v6,4,v6\n"
"v_xor_b32 v6,v6,v11\n"
"v_lshlrev_b32 v6,4,v6\n"
"s_mov_b32 s58,12288\n"
"v_mad_u32_u24 v6,v10,s58,v6\n"
"v_bfe_u32 v4,v0,4,3\n"
"v_xor_b32 v4,v4,v11\n"
"v_lshlrev_b32 v4,4,v4\n"
"s_mov_b32 s58,1280\n"
"v_mad_u32_u24 v4,v10,s58,v4\n"
"v_lshlrev_b32 v9,4,v9\n"
"s_mov_b32 s26,3276800\n"
"s_mov_b32 s27,0x00020000\n"
"s_mov_b32 s30,7864320\n"
"s_mov_b32 s31,0x00020000\n"
"s_mov_b32 s34,0\n"
"s_mov_b32 s35,0x00020000\n"
"s_mov_b32 s38,0\n"
"s_mov_b32 s39,0x00020000\n"
"s_mov_b32 s42,268435456\n"
"s_mov_b32 s43,0x00020000\n"
"s_mov_b32 s46,6553600\n"
"s_mov_b32 s47,0x00020000\n"
"s_mov_b32 s50,7864320\n"
"s_mov_b32 s51,0x00020000\n"
"s_mov_b32 s98,20000\n"
"s_bfe_u32 s56,s22,0x20000\n"
"s_bfe_u32 s72,s22,0x10003\n"
"s_lshr_b32 s73,s56,1\n"
"s_and_b32 s74,s56,1\n"
"s_mul_i32 s62,s56,4096\n"
"s_mul_i32 s63,s56,5120\n"
"s_add_u32 s63,s63,98304\n"
"s_bfe_u32 s58,s22,0x40004\n"
"s_bfe_u32 s59,s22,0x40008\n"
"s_mul_i32 s68,s58,204800\n"
"s_mul_i32 s60,s56,51200\n"
"s_add_u32 s68,s68,s60\n"
"s_lshl_b32 s59,s59,1\n"
"s_add_u32 s59,s59,s73\n"
"s_mul_i32 s66,s59,384\n"
"s_mul_i32 s60,s74,393216\n"
"s_add_u32 s66,s66,s60\n"
"s_lshr_b32 s58,s22,6\n"
"s_xor_b32 s58,s58,s22\n"
"s_bfe_u32 s58,s58,0x10004\n"
"s_mul_i32 s81,s58,640\n"
"s_mul_i32 s60,s81,6144\n"
"s_add_u32 s66,s66,s60\n"
"v_xor_b32 v19,64,v4\n"
"v_add_u32 v16,97280,v6\n"
"v_add_u32 v17,194560,v6\n"
"v_add_u32 v18,291840,v6\n"
"s_waitcnt lgkmcnt(0)\n"
"s_mov_b32 s80,s100\n"
"s_mov_b32 s87,2\n"
"s_add_u32 m0,s62,0\n"
"s_add_u32 s82,s66,0\n"
"s_sub_u32 s15,s82,7864320\n"
"s_min_u32 s82,s82,s15\n"
LD "v6,s[28:31],s82 offen lds\n"
LD "v16,s[28:31],s82 offen offset:1024 lds\n"
LD "v17,s[28:31],s82 offen offset:2048 lds\n"
LD "v18,s[28:31],s82 offen offset:3072 lds\n"
"s_add_u32 s14,s81,0\n"
"s_sub_u32 s15,s14,1280\n"
"s_min_u32 s14,s14,s15\n"
"s_add_u32 s14,s14,s68\n"
"s_add_u32 s84,s14,0\n"
"s_add_u32 m0,s63,0\n"
"s_nop 0\n"
LD "v4,s[24:27],s84 offen lds\n"
"s_add_u32 s84,s14,9216\n"
LD "v19,s[24:27],s84 offen offset:1024 lds\n"
"s_add_u32 s84,s14,18432\n"
LD "v4,s[24:27],s84 offen offset:2048 lds\n"
"s_add_u32 s84,s14,27648\n"
LD "v19,s[24:27],s84 offen offset:3072 lds\n"
"s_add_u32 s84,s14,40960\n"
"s_add_u32 m0,s63,4096\n"
"s_nop 0\n"
LD "v4,s[24:27],s84 offen lds\n"
"s_lshr_b32 s58,s23,3\n"
"s_mul_i32 s59,s58,7\n"
"s_and_b32 s60,s23,7\n"
"s_mul_i32 s60,s60,3\n"
"s_add_u32 s59,s59,s60\n"
"s_and_b32 s59,s59,15\n"
"s_lshl_b32 s59,s59,8\n"
"s_or_b32 s58,s58,s59\n"
"s_and_b32 s59,s23,3\n"
"s_lshl_b32 s59,s59,5\n"
"s_or_b32 s58,s58,s59\n"
"s_bfe_u32 s59,s23,0x10001\n"
"s_lshl_b32 s59,s59,12\n"
"s_or_b32 s58,s58,s59\n"
"s_lshl_b32 s23,s23,16\n"
"s_or_b32 s23,s23,s58\n"
"v_bfe_u32 v20,v0,4,2\n"
"v_mul_u32_u24 v21,384,v10\n"
"v_xor_b32 v22,v11,v20\n"
"s_add_u32 m0,s62,16384\n"
"s_add_u32 s82,s66,128\n"
"s_sub_u32 s15,s82,7864320\n"
"s_min_u32 s82,s82,s15\n"
LD "v6,s[28:31],s82 offen lds\n"
"v_lshl_add_u32 v7,v22,4,v21\n"
"s_lshl_b32 s58,s74,6\n"
"v_xor_b32 v7,s58,v7\n"
"v_lshlrev_b32 v22,1,v20\n"
"v_xor_b32 v22,v11,v22\n"
"v_lshl_add_u32 v8,v22,4,v21\n"
"v_bfe_u32 v21,v0,1,3\n"
"v_xor_b32 v21,v21,v20\n"
"v_lshlrev_b32 v21,4,v21\n"
"v_lshl_add_u32 v21,v11,7,v21\n"
"v_bfe_u32 v22,v0,3,1\n"
"s_mul_i32 s58,s73,12288\n"
"v_mul_u32_u24 v23,3072,v22\n"
"v_add3_u32 v1,v23,v21,s58\n"
"s_mul_i32 s59,s74,10240\n"
"s_add_u32 s59,s59,98304\n"
"v_lshl_add_u32 v2,v22,10,v21\n"
"v_add_u32_e64 v2,s59,v2\n"
"v_bfe_u32 v21,v0,2,3\n"
LD "v16,s[28:31],s82 offen offset:1024 lds\n"
"v_lshlrev_b32_e64 v21,7,v21\n"
"v_bfe_u32 v23,v0,3,2\n"
"v_lshl_add_u32 v21,v23,5,v21\n"
"v_and_b32_e64 v23,3,v0\n"
"v_lshl_add_u32 v21,v23,3,v21\n"
"v_bfe_u32 v23,v0,5,1\n"
"v_mul_u32_u24 v3,3072,v23\n"
"v_add3_u32 v3,v3,v21,s58\n"
"s_bfe_u32 s58,s22,0x40004\n"
"s_mul_i32 s58,s58,409600\n"
"s_mul_i32 s59,s72,1280\n"
"s_add_u32 s58,s58,s59\n"
"v_and_b32 v21,15,v0\n"
"v_mul_u32_u24 v21,2560,v21\n"
"v_lshl_add_u32 v21,v23,4,v21\n"
"v_bfe_u32 v23,v0,4,1\n"
"v_lshl_add_u32 v21,v23,5,v21\n"
"v_add_u32_e64 v12,s58,v21\n"
"v_bfe_u32 v21,v0,2,2\n"
LD "v17,s[28:31],s82 offen offset:2048 lds\n"
"v_lshlrev_b32 v21,7,v21\n"
"v_lshl_add_u32 v21,v20,10,v21\n"
"v_lshl_add_u32 v21,v22,6,v21\n"
"v_and_b32 v23,3,v0\n"
"v_lshl_add_u32 v21,v23,4,v21\n"
"s_lshl_b32 s58,s73,13\n"
"v_add_u32 v0,s58,v21\n"
"s_getpc_b64 s[52:53]\n"
".Lcts:\n"
"s_sub_u32 s52,s52,.Lcts-.Lcode0\n"
"s_subb_u32 s53,s53,0\n"
"s_bfe_u32 s58,s23,0x50000\n"
"s_lshl_b32 s58,s58,2\n"
"s_add_u32 s58,s58,s56\n"
"s_mul_i32 s58,s58,1280\n"
"v_min_u32 v15,144,v9\n"
"v_lshl_add_u32 v15,v15,3,s58\n"
"v_min_u32 v15,.Lcode1-.Lcode0,v15\n"
LD "v18,s[28:31],s82 offen offset:3072 lds\n"
"s_bfe_u32 s58,s22,0x4000c\n"
"s_bfe_u32 s59,s22,0x40010\n"
"s_mul_i32 s69,s58,204800\n"
"s_mul_i32 s60,s56,51200\n"
"s_add_u32 s69,s69,s60\n"
"s_lshl_b32 s59,s59,1\n"
"s_add_u32 s59,s59,s73\n"
"s_mul_i32 s67,s59,384\n"
"s_mul_i32 s60,s74,393216\n"
"s_add_u32 s67,s67,s60\n"
"s_mul_i32 s60,s81,6144\n"
"s_add_u32 s67,s67,s60\n"
"s_bfe_u32 s58,s22,0x40004\n"
"s_bfe_u32 s59,s22,0x40008\n"
"s_lshl_b32 s59,s59,1\n"
"s_add_u32 s88,s59,s74\n"
"s_mul_i32 s88,s88,245760\n"
"s_add_u32 s59,s59,s73\n"
"s_lshl_b32 s70,s58,5\n"
"s_add_u32 m0,s62,32768\n"
"s_add_u32 s82,s66,256\n"
"s_sub_u32 s15,s82,7864320\n"
"s_min_u32 s82,s82,s15\n"
LD "v6,s[28:31],s82 offen lds\n"
"s_add_u32 s70,s70,s59\n"
"s_mul_i32 s70,s70,196608\n"
"s_bfe_u32 s58,s22,0x4000c\n"
"s_bfe_u32 s59,s22,0x40010\n"
"s_lshl_b32 s59,s59,1\n"
"s_add_u32 s89,s59,s74\n"
"s_mul_i32 s89,s89,245760\n"
"s_add_u32 s59,s59,s73\n"
"s_lshl_b32 s71,s58,5\n"
"s_add_u32 s71,s71,s59\n"
"s_mul_i32 s71,s71,196608\n"
"s_bitcmp1_b32 s23,12\n"
"s_cselect_b32 s58,s71,s70\n"
"s_cselect_b32 s71,s70,s71\n"
"s_mov_b32 s70,s58\n"
"s_cselect_b32 s58,s89,s88\n"
"s_cselect_b32 s89,s88,s89\n"
"s_mov_b32 s88,s58\n"
"s_cselect_b32 s69,98,1\n"
LD "v16,s[28:31],s82 offen offset:1024 lds\n"
"s_mov_b32 s54,1037384597\n"
"s_mov_b32 s55,1037384597\n"
"s_bfe_u32 s58,s22,0x40004\n"
"s_lshl_b32 s59,s72,4\n"
"s_add_u32 s59,s59,s58\n"
"s_lshl_b32 s86,s59,12\n"
"s_add_u32 s86,s86,100663296\n"
"s_mul_i32 s93,s59,1638400\n"
"s_add_u32 s93,s93,33554432\n"
"s_bfe_u32 s58,s22,0x30014\n"
"s_lshl_b32 s78,s58,1\n"
"s_add_u32 s78,s78,s74\n"
"s_lshl_b32 s94,s78,2\n"
"s_add_u32 s94,s94,s86\n"
"s_lshl_b32 s92,s58,2\n"
"s_add_u32 s92,s92,s56\n"
"s_mul_i32 s91,s58,204800\n"
"s_add_u32 s91,s91,s93\n"
"s_mul_i32 s75,s56,30720\n"
LD "v17,s[28:31],s82 offen offset:2048 lds\n"
"s_add_u32 s75,s75,s91\n"
"s_add_u32 s75,s75,0\n"
"s_mul_i32 s58,s74,5120\n"
"s_add_u32 s91,s91,s58\n"
"s_mov_b32 s90,0xf0000000\n"
"s_mov_b32 s77,0xf0000000\n"
"s_mov_b32 s85,0xf0000000\n"
AW "a240,v0\n"
AW "a244,v1\n"
AW "a241,v2\n"
AW "a245,v3\n"
AW "a242,v4\n"
AW "a243,v6\n"
AW "a252,v12\n"
"s_mov_b32 s57,0\n"
"s_mov_b32 s83,0\n"
LD "v18,s[28:31],s82 offen offset:3072 lds\n"
"global_load_dword a249,v15,s[52:53]\n"
AR "v0,a240\n"
AR "v2,a241\n"
AR "v4,a242\n"
AR "v6,a243\n"
"v_xor_b32 v1,64,v0\n"
"v_xor_b32 v3,64,v2\n"
"v_xor_b32_e64 v5,64,v4\n"
"v_add_u32 v11,97280,v6\n"
"v_add_u32 v12,194560,v6\n"
"v_add_u32 v13,291840,v6\n"
"s_mov_b64 vcc,0\n"
"s_waitcnt vmcnt(9) lgkmcnt(0)\n"
"s_barrier\n"
"s_nop 0\n"
TR "a[200:201],v0\n"
TR "a[202:203],v0 offset:512\n"
TR "a[204:205],v0 offset:4096\n"
TR "a[206:207],v0 offset:4608\n"
"ds_read_b128 a[60:63],v2\n"
"ds_read_b128 a[100:103],v3\n"
"ds_read_b128 a[64:67],v2 offset:2048\n"
"ds_read_b128 a[104:107],v3 offset:2048\n"
"ds_read_b128 a[68:71],v2 offset:4096\n"
"ds_read_b128 a[108:111],v3 offset:4096\n"
"ds_read_b128 a[72:75],v2 offset:6144\n"
"ds_read_b128 a[112:115],v3 offset:6144\n"
"ds_read_b128 a[76:79],v2 offset:8192\n"
"ds_read_b128 a[116:119],v3 offset:8192\n"
TR "a[208:209],v0 offset:8\n"
TR "a[210:211],v0 offset:520\n"
TR "a[212:213],v0 offset:4104\n"
TR "a[214:215],v0 offset:4616\n"
"s_waitcnt lgkmcnt(0)\n"
MF "v[16:19],a[200:203],a[60:63],0\n"
TR "a[216:217],v1\n"
MF "v[16:19],a[204:207],a[100:103],v[16:19]\n"
"s_add_u32 s14,s81,128\n"
TR "a[218:219],v1 offset:512\n"
MF "v[24:27],a[204:207],a[104:107],0\n"
"s_sub_u32 s15,s14,1280\n"
"v_lshrrev_b32 v15,5,v9\n"
"v_min_u32 v15,11,v15\n"
MF "v[24:27],a[200:203],a[64:67],v[24:27]\n"
"s_min_u32 s14,s14,s15\n"
TR "a[220:221],v1 offset:4096\n"
"v_mul_u32_u24 v15,160,v15\n"
MF "v[32:35],a[200:203],a[68:71],0\n"
"s_add_u32 s14,s14,s68\n"
TR "a[222:223],v1 offset:4608\n"
"v_and_or_b32 v15,v9,16,v15\n"
MF "v[32:35],a[204:207],a[108:111],v[32:35]\n"
"s_add_u32 s84,s14,0\n"
"v_max3_f32 v220,|v16|,|v17|,|v18|\n"
"v_max_f32_e64 v220,v220,|v19|\n"
MF "v[40:43],a[204:207],a[112:115],0\n"
"s_add_u32 m0,s63,20480\n"
"v_readlane_b32 s12,v220,3\n"
"v_readlane_b32 s6,v220,22\n"
MF "v[40:43],a[200:203],a[72:75],v[40:43]\n"
"s_min_u32 s12,s12,s6\n"
"v_readlane_b32 s7,v220,41\n"
"v_readlane_b32 s8,v220,60\n"
MF "v[48:51],a[200:203],a[76:79],0\n"
"s_min_u32 s7,s7,s8\n"
"v_lshlrev_b32 v15,3,v15\n"
MF "v[48:51],a[204:207],a[116:119],v[48:51]\n"
LD "v4,s[24:27],s84 offen lds\n"
MF "v[52:55],a[212:215],a[116:119],0\n"
"s_min_u32 s12,s12,s7\n"
TR "a[200:201],v1 offset:8\n"
MF "v[52:55],a[208:211],a[76:79],v[52:55]\n"
"s_and_b32 s12,s12,0x7f800000\n"
TR "a[202:203],v1 offset:520\n"
MF "v[44:47],a[208:211],a[72:75],0\n"
"s_sub_u32 s12,s12,0x14000000\n"
MF "v[44:47],a[212:215],a[112:115],v[44:47]\n"
"s_cmp_lt_u32 s12,0x50000000\n"
"s_cselect_b32 s12,s12,0xe8400000\n"
TR "a[204:205],v1 offset:4104\n"
MF "v[36:39],a[212:215],a[108:111],0\n"
"s_add_u32 s12,s12,0x17c00000\n"
TR "a[206:207],v1 offset:4616\n"
MF "v[36:39],a[208:211],a[68:71],v[36:39]\n"
"s_bfe_u32 s60,s23,0x70000\n"
"v_mov_b32 v220,s12\n"
"v_mov_b32 v221,s12\n"
MF "v[28:31],a[208:211],a[64:67],0\n"
"s_add_u32 s84,s14,9216\n"
"v_mov_b32 v222,s12\n"
"v_mov_b32 v223,s12\n"
MF "v[28:31],a[212:215],a[104:107],v[28:31]\n"
LD "v5,s[24:27],s84 offen offset:1024 lds\n"
MF "v[20:23],a[212:215],a[100:103],0\n"
"s_waitcnt vmcnt(7) lgkmcnt(0)\n"
MF "v[20:23],a[208:211],a[60:63],v[20:23]\n"
"s_barrier\n"
MF "v[56:59],a[216:219],a[60:63],v[220:223]\n"
"s_lshl_b32 s60,s60,2\n"
TR "a[208:209],v0 offset:16384\n"
MF "v[56:59],a[220:223],a[100:103],v[56:59]\n"
"s_add_u32 s84,s14,18432\n"
TR "a[210:211],v0 offset:16896\n"
MF "v[64:67],a[220:223],a[104:107],v[220:223]\n"
LD "v4,s[24:27],s84 offen offset:2048 lds\n"
MF "v[64:67],a[216:219],a[64:67],v[64:67]\n"
"s_add_u32 s60,s60,s56\n"
TR "a[212:213],v0 offset:20480\n"
MF "v[72:75],a[216:219],a[68:71],v[220:223]\n"
"s_mul_i32 s60,s60,15360\n"
TR "a[214:215],v0 offset:20992\n"
MF "v[72:75],a[220:223],a[108:111],v[72:75]\n"
MF "v[80:83],a[220:223],a[112:115],v[220:223]\n"
"s_add_u32 s84,s14,27648\n"
MF "v[80:83],a[216:219],a[72:75],v[80:83]\n"
LD "v5,s[24:27],s84 offen offset:3072 lds\n"
MF "v[88:91],a[216:219],a[76:79],v[220:223]\n"
MF "v[88:91],a[220:223],a[116:119],v[88:91]\n"
MF "v[92:95],a[204:207],a[116:119],v[220:223]\n"
"s_add_u32 s84,s14,40960\n"
TR "a[216:217],v0 offset:16392\n"
MF "v[92:95],a[200:203],a[76:79],v[92:95]\n"
"s_add_u32 m0,s63,24576\n"
TR "a[218:219],v0 offset:16904\n"
MF "v[84:87],a[200:203],a[72:75],v[220:223]\n"
LD "v4,s[24:27],s84 offen lds\n"
MF "v[84:87],a[204:207],a[112:115],v[84:87]\n"
"s_add_u32 m0,s62,49152\n"
TR "a[220:221],v0 offset:20488\n"
MF "v[76:79],a[204:207],a[108:111],v[220:223]\n"
"s_add_u32 s82,s66,786432\n"
TR "a[222:223],v0 offset:21000\n"
MF "v[76:79],a[200:203],a[68:71],v[76:79]\n"
"s_sub_u32 s15,s82,7864320\n"
MF "v[68:71],a[200:203],a[64:67],v[220:223]\n"
"s_min_u32 s82,s82,s15\n"
MF "v[68:71],a[204:207],a[104:107],v[68:71]\n"
LD "v6,s[28:31],s82 offen lds\n"
MF "v[60:63],a[204:207],a[100:103],v[220:223]\n"
MF "v[60:63],a[200:203],a[60:63],v[60:63]\n"
"s_waitcnt lgkmcnt(4)\n"
MF "v[96:99],a[208:211],a[60:63],v[220:223]\n"
TR "a[200:201],v1 offset:16384\n"
MF "v[96:99],a[212:215],a[100:103],v[96:99]\n"
TR "a[202:203],v1 offset:16896\n"
MF "v[104:107],a[212:215],a[104:107],v[220:223]\n"
LD "v11,s[28:31],s82 offen offset:1024 lds\n"
MF "v[104:107],a[208:211],a[64:67],v[104:107]\n"
TR "a[204:205],v1 offset:20480\n"
MF "v[112:115],a[208:211],a[68:71],v[220:223]\n"
TR "a[206:207],v1 offset:20992\n"
MF "v[112:115],a[212:215],a[108:111],v[112:115]\n"
MF "v[120:123],a[212:215],a[112:115],v[220:223]\n"
MF "v[120:123],a[208:211],a[72:75],v[120:123]\n"
LD "v12,s[28:31],s82 offen offset:2048 lds\n"
MF "v[128:131],a[208:211],a[76:79],v[220:223]\n"
MF "v[128:131],a[212:215],a[116:119],v[128:131]\n"
"s_waitcnt lgkmcnt(4)\n"
MF "v[132:135],a[220:223],a[116:119],v[220:223]\n"
TR "a[208:209],v1 offset:16392\n"
MF "v[132:135],a[216:219],a[76:79],v[132:135]\n"
TR "a[210:211],v1 offset:16904\n"
MF "v[124:127],a[216:219],a[72:75],v[220:223]\n"
LD "v13,s[28:31],s82 offen offset:3072 lds\n"
MF "v[124:127],a[220:223],a[112:115],v[124:127]\n"
"s_add_u32 m0,s62,65536\n"
TR "a[212:213],v1 offset:20488\n"
MF "v[116:119],a[220:223],a[108:111],v[220:223]\n"
"s_add_u32 s82,s66,786560\n"
TR "a[214:215],v1 offset:21000\n"
MF "v[116:119],a[216:219],a[68:71],v[116:119]\n"
"s_sub_u32 s15,s82,7864320\n"
MF "v[108:111],a[216:219],a[64:67],v[220:223]\n"
"s_min_u32 s82,s82,s15\n"
MF "v[108:111],a[220:223],a[104:107],v[108:111]\n"
LD "v6,s[28:31],s82 offen lds\n"
MF "v[100:103],a[220:223],a[100:103],v[220:223]\n"
"s_waitcnt vmcnt(5) lgkmcnt(0)\n"
MF "v[100:103],a[216:219],a[60:63],v[100:103]\n"
"s_barrier\n"
MF "v[136:139],a[200:203],a[60:63],v[220:223]\n"
TR "a[216:217],v0 offset:32768\n"
MF "v[136:139],a[204:207],a[100:103],v[136:139]\n"
TR "a[218:219],v0 offset:33280\n"
MF "v[144:147],a[204:207],a[104:107],v[220:223]\n"
"ds_read_b128 a[140:143],v2 offset:20480\n"
MF "v[144:147],a[200:203],a[64:67],v[144:147]\n"
TR "a[220:221],v0 offset:36864\n"
MF "v[152:155],a[200:203],a[68:71],v[220:223]\n"
TR "a[222:223],v0 offset:37376\n"
MF "v[152:155],a[204:207],a[108:111],v[152:155]\n"
LD "v11,s[28:31],s82 offen offset:1024 lds\n"
MF "v[160:163],a[204:207],a[112:115],v[220:223]\n"
"ds_read_b128 a[180:183],v3 offset:20480\n"
MF "v[160:163],a[200:203],a[72:75],v[160:163]\n"
MF "v[168:171],a[200:203],a[76:79],v[220:223]\n"
MF "v[168:171],a[204:207],a[116:119],v[168:171]\n"
LD "v12,s[28:31],s82 offen offset:2048 lds\n"
MF "v[172:175],a[212:215],a[116:119],v[220:223]\n"
TR "a[200:201],v0 offset:32776\n"
MF "v[172:175],a[208:211],a[76:79],v[172:175]\n"
TR "a[202:203],v0 offset:33288\n"
MF "v[164:167],a[208:211],a[72:75],v[220:223]\n"
"ds_read_b128 a[144:147],v2 offset:22528\n"
MF "v[164:167],a[212:215],a[112:115],v[164:167]\n"
TR "a[204:205],v0 offset:36872\n"
MF "v[156:159],a[212:215],a[108:111],v[220:223]\n"
TR "a[206:207],v0 offset:37384\n"
MF "v[156:159],a[208:211],a[68:71],v[156:159]\n"
LD "v13,s[28:31],s82 offen offset:3072 lds\n"
MF "v[148:151],a[208:211],a[64:67],v[220:223]\n"
"ds_read_b128 a[184:187],v3 offset:22528\n"
MF "v[148:151],a[212:215],a[104:107],v[148:151]\n"
"s_add_u32 m0,s62,81920\n"
MF "v[140:143],a[212:215],a[100:103],v[220:223]\n"
"s_add_u32 s82,s66,786688\n"
"s_sub_u32 s15,s82,7864320\n"
"s_min_u32 s82,s82,s15\n"
MF "v[140:143],a[208:211],a[60:63],v[140:143]\n"
"s_waitcnt lgkmcnt(7)\n"
LD "v6,s[28:31],s82 offen lds\n"
MF "v[176:179],a[216:219],a[60:63],v[220:223]\n"
TR "a[208:209],v1 offset:32768\n"
MF "v[176:179],a[220:223],a[100:103],v[176:179]\n"
TR "a[210:211],v1 offset:33280\n"
MF "v[184:187],a[220:223],a[104:107],v[220:223]\n"
"ds_read_b128 a[148:151],v2 offset:24576\n"
MF "v[184:187],a[216:219],a[64:67],v[184:187]\n"
TR "a[212:213],v1 offset:36864\n"
MF "v[192:195],a[216:219],a[68:71],v[220:223]\n"
TR "a[214:215],v1 offset:37376\n"
MF "v[192:195],a[220:223],a[108:111],v[192:195]\n"
LD "v11,s[28:31],s82 offen offset:1024 lds\n"
MF "v[200:203],a[220:223],a[112:115],v[220:223]\n"
"ds_read_b128 a[188:191],v3 offset:24576\n"
MF "v[200:203],a[216:219],a[72:75],v[200:203]\n"
MF "v[208:211],a[216:219],a[76:79],v[220:223]\n"
MF "v[208:211],a[220:223],a[116:119],v[208:211]\n"
"s_waitcnt lgkmcnt(7)\n"
LD "v12,s[28:31],s82 offen offset:2048 lds\n"
MF "v[212:215],a[204:207],a[116:119],v[220:223]\n"
TR "a[216:217],v1 offset:32776\n"
MF "v[212:215],a[200:203],a[76:79],v[212:215]\n"
TR "a[218:219],v1 offset:33288\n"
MF "v[204:207],a[200:203],a[72:75],v[220:223]\n"
"ds_read_b128 a[152:155],v2 offset:26624\n"
MF "v[204:207],a[204:207],a[112:115],v[204:207]\n"
TR "a[220:221],v1 offset:36872\n"
MF "v[196:199],a[204:207],a[108:111],v[220:223]\n"
TR "a[222:223],v1 offset:37384\n"
MF "v[196:199],a[200:203],a[68:71],v[196:199]\n"
LD "v13,s[28:31],s82 offen offset:3072 lds\n"
MF "v[188:191],a[200:203],a[64:67],v[220:223]\n"
"v_add_u32 v0,0xc000,v0\n"
MF "v[188:191],a[204:207],a[104:107],v[188:191]\n"
"s_add_u32 s14,s81,256\n"
"v_add_u32 v1,0xc000,v1\n"
MF "v[180:183],a[204:207],a[100:103],v[220:223]\n"
"s_waitcnt vmcnt(8) lgkmcnt(0)\n"
"s_mov_b64 exec,0\n"
"s_bitcmp1_b32 s23,12\n"
"s_cbranch_scc1 .Lisy1\n"
"s_cmp_eq_u32 s56,0\n"
"s_cbranch_scc1 .Lpg8b\n"
"s_cmp_eq_u32 s56,1\n"
"s_cbranch_scc1 .Lpg9b\n"
"s_cmp_eq_u32 s56,2\n"
"s_cbranch_scc1 .Lpgt0ex_b\n"
"s_branch .Lpg10b\n"
".Lisy1:\n"
"s_cmp_eq_u32 s56,0\n"
"s_cbranch_scc1 .Lpgq0_c\n"
"s_cmp_eq_u32 s56,1\n"
"s_cbranch_scc1 .Lpg4c\n"
"s_cmp_eq_u32 s56,2\n"
"s_cbranch_scc1 .Lpg5c\n"
"s_branch .Lpg6c\n"
".Lisr1:\n"
"s_mov_b64 exec,-1\n"
".Lisx1:\n"
MF "v[180:183],a[200:203],a[60:63],v[180:183]\n"
"s_barrier\n"
MF "v[216:219],a[208:211],a[60:63],v[220:223]\n"
"s_sub_u32 s15,s14,1280\n"
TR "a[200:201],v0\n"
MF "v[216:219],a[212:215],a[100:103],v[216:219]\n"
"s_min_u32 s14,s14,s15\n"
TR "a[202:203],v0 offset:512\n"
MF "v[224:227],a[212:215],a[104:107],v[220:223]\n"
"s_add_u32 s14,s14,s68\n"
"ds_read_b128 a[192:195],v3 offset:26624\n"
MF "v[224:227],a[208:211],a[64:67],v[224:227]\n"
"s_add_u32 s84,s14,0\n"
TR "a[204:205],v0 offset:4096\n"
MF "v[232:235],a[208:211],a[68:71],v[220:223]\n"
"s_add_u32 m0,s63,40960\n"
TR "a[206:207],v0 offset:4608\n"
MF "v[232:235],a[212:215],a[108:111],v[232:235]\n"
LD "v4,s[24:27],s84 offen lds\n"
MF "v[240:243],a[212:215],a[112:115],v[220:223]\n"
"ds_read_b128 a[156:159],v2 offset:28672\n"
MF "v[240:243],a[208:211],a[72:75],v[240:243]\n"
"ds_read_b128 a[196:199],v3 offset:28672\n"
MF "v[248:251],a[208:211],a[76:79],v[220:223]\n"
"s_add_u32 s84,s14,9216\n"
MF "v[248:251],a[212:215],a[116:119],v[248:251]\n"
LD "v5,s[24:27],s84 offen offset:1024 lds\n"
MF "v[252:255],a[220:223],a[116:119],v[220:223]\n"
TR "a[208:209],v0 offset:8\n"
MF "v[252:255],a[216:219],a[76:79],v[252:255]\n"
TR "a[210:211],v0 offset:520\n"
MF "v[244:247],a[216:219],a[72:75],v[220:223]\n"
MF "v[244:247],a[220:223],a[112:115],v[244:247]\n"
TR "a[212:213],v0 offset:4104\n"
MF "v[236:239],a[220:223],a[108:111],v[220:223]\n"
"s_add_u32 s84,s14,18432\n"
TR "a[214:215],v0 offset:4616\n"
MF "v[236:239],a[216:219],a[68:71],v[236:239]\n"
LD "v4,s[24:27],s84 offen offset:2048 lds\n"
MF "v[228:231],a[216:219],a[64:67],v[220:223]\n"
MF "v[228:231],a[220:223],a[104:107],v[228:231]\n"
MF "v[220:223],a[220:223],a[100:103],v[220:223]\n"
".Lpg1a:\n"
"s_cbranch_execz .Lpg7a\n"
"s_add_u32 s84,s14,27648\n"
MF "v[220:223],a[216:219],a[60:63],v[220:223]\n"
"s_waitcnt lgkmcnt(4)\n"
LD "v5,s[24:27],s84 offen offset:3072 lds\n"
MF "v[16:19],a[200:203],a[140:143],v[16:19]\n"
TR "a[216:217],v1\n"
MF "v[16:19],a[204:207],a[180:183],v[16:19]\n"
TR "a[218:219],v1 offset:512\n"
MF "v[24:27],a[204:207],a[184:187],v[24:27]\n"
MF "v[24:27],a[200:203],a[144:147],v[24:27]\n"
"s_add_u32 s84,s14,40960\n"
TR "a[220:221],v1 offset:4096\n"
MF "v[32:35],a[200:203],a[148:151],v[32:35]\n"
"s_add_u32 m0,s63,45056\n"
TR "a[222:223],v1 offset:4608\n"
MF "v[32:35],a[204:207],a[188:191],v[32:35]\n"
LD "v4,s[24:27],s84 offen lds\n"
MF "v[40:43],a[204:207],a[192:195],v[40:43]\n"
MF "v[40:43],a[200:203],a[152:155],v[40:43]\n"
"s_add_u32 m0,s62,0\n"
MF "v[48:51],a[200:203],a[156:159],v[48:51]\n"
"s_add_u32 s82,s66,1572864\n"
"s_sub_u32 s15,s82,7864320\n"
"s_min_u32 s82,s82,s15\n"
MF "v[48:51],a[204:207],a[196:199],v[48:51]\n"
"s_waitcnt lgkmcnt(4)\n"
LD "v6,s[28:31],s82 offen lds\n"
MF "v[52:55],a[212:215],a[196:199],v[52:55]\n"
TR "a[200:201],v1 offset:8\n"
MF "v[52:55],a[208:211],a[156:159],v[52:55]\n"
TR "a[202:203],v1 offset:520\n"
MF "v[44:47],a[208:211],a[152:155],v[44:47]\n"
MF "v[44:47],a[212:215],a[192:195],v[44:47]\n"
TR "a[204:205],v1 offset:4104\n"
MF "v[36:39],a[212:215],a[188:191],v[36:39]\n"
TR "a[206:207],v1 offset:4616\n"
MF "v[36:39],a[208:211],a[148:151],v[36:39]\n"
LD "v11,s[28:31],s82 offen offset:1024 lds\n"
MF "v[28:31],a[208:211],a[144:147],v[28:31]\n"
MF "v[28:31],a[212:215],a[184:187],v[28:31]\n"
MF "v[20:23],a[212:215],a[180:183],v[20:23]\n"
"s_waitcnt vmcnt(11) lgkmcnt(0)\n"
LD "v12,s[28:31],s82 offen offset:2048 lds\n"
MF "v[20:23],a[208:211],a[140:143],v[20:23]\n"
"s_barrier\n"
"v_bfe_u32 v14,v9,4,4\n"
"v_lshlrev_b32 v14,2,v14\n"
"buffer_load_dword v10,v14,s[40:43],s86 offen sc1\n"
".Lhs2:\n"
MF "v[56:59],a[216:219],a[140:143],v[56:59]\n"
TR "a[208:209],v0 offset:16384\n"
MF "v[56:59],a[220:223],a[180:183],v[56:59]\n"
TR "a[210:211],v0 offset:16896\n"
MF "v[64:67],a[220:223],a[184:187],v[64:67]\n"
LD "v13,s[28:31],s82 offen offset:3072 lds\n"
MF "v[64:67],a[216:219],a[144:147],v[64:67]\n"
TR "a[212:213],v0 offset:20480\n"
MF "v[72:75],a[216:219],a[148:151],v[72:75]\n"
TR "a[214:215],v0 offset:20992\n"
MF "v[72:75],a[220:223],a[188:191],v[72:75]\n"
"s_add_u32 s14,s81,384\n"
"s_sub_u32 s15,s14,1280\n"
"s_min_u32 s14,s14,s15\n"
MF "v[80:83],a[220:223],a[192:195],v[80:83]\n"
"s_add_u32 s14,s14,s68\n"
"s_add_u32 s84,s14,0\n"
"s_add_u32 m0,s63,0\n"
MF "v[80:83],a[216:219],a[152:155],v[80:83]\n"
LD "v4,s[24:27],s84 offen lds\n"
MF "v[88:91],a[216:219],a[156:159],v[88:91]\n"
MF "v[88:91],a[220:223],a[196:199],v[88:91]\n"
MF "v[92:95],a[204:207],a[196:199],v[92:95]\n"
TR "a[216:217],v0 offset:16392\n"
MF "v[92:95],a[200:203],a[156:159],v[92:95]\n"
"s_add_u32 s84,s14,9216\n"
TR "a[218:219],v0 offset:16904\n"
MF "v[84:87],a[200:203],a[152:155],v[84:87]\n"
LD "v5,s[24:27],s84 offen offset:1024 lds\n"
MF "v[84:87],a[204:207],a[192:195],v[84:87]\n"
"s_add_u32 m0,s62,16384\n"
TR "a[220:221],v0 offset:20488\n"
MF "v[76:79],a[204:207],a[188:191],v[76:79]\n"
"s_add_u32 s82,s66,1572992\n"
TR "a[222:223],v0 offset:21000\n"
MF "v[76:79],a[200:203],a[148:151],v[76:79]\n"
"s_sub_u32 s15,s82,7864320\n"
MF "v[68:71],a[200:203],a[144:147],v[68:71]\n"
"s_min_u32 s82,s82,s15\n"
MF "v[68:71],a[204:207],a[184:187],v[68:71]\n"
LD "v6,s[28:31],s82 offen lds\n"
MF "v[60:63],a[204:207],a[180:183],v[60:63]\n"
MF "v[60:63],a[200:203],a[140:143],v[60:63]\n"
"s_waitcnt lgkmcnt(4)\n"
MF "v[96:99],a[208:211],a[140:143],v[96:99]\n"
TR "a[200:201],v1 offset:16384\n"
MF "v[96:99],a[212:215],a[180:183],v[96:99]\n"
TR "a[202:203],v1 offset:16896\n"
MF "v[104:107],a[212:215],a[184:187],v[104:107]\n"
LD "v11,s[28:31],s82 offen offset:1024 lds\n"
MF "v[104:107],a[208:211],a[144:147],v[104:107]\n"
TR "a[204:205],v1 offset:20480\n"
MF "v[112:115],a[208:211],a[148:151],v[112:115]\n"
TR "a[206:207],v1 offset:20992\n"
MF "v[112:115],a[212:215],a[188:191],v[112:115]\n"
MF "v[120:123],a[212:215],a[192:195],v[120:123]\n"
MF "v[120:123],a[208:211],a[152:155],v[120:123]\n"
LD "v12,s[28:31],s82 offen offset:2048 lds\n"
MF "v[128:131],a[208:211],a[156:159],v[128:131]\n"
MF "v[128:131],a[212:215],a[196:199],v[128:131]\n"
"s_waitcnt lgkmcnt(4)\n"
MF "v[132:135],a[220:223],a[196:199],v[132:135]\n"
TR "a[208:209],v1 offset:16392\n"
MF "v[132:135],a[216:219],a[156:159],v[132:135]\n"
TR "a[210:211],v1 offset:16904\n"
MF "v[124:127],a[216:219],a[152:155],v[124:127]\n"
LD "v13,s[28:31],s82 offen offset:3072 lds\n"
MF "v[124:127],a[220:223],a[192:195],v[124:127]\n"
TR "a[212:213],v1 offset:20488\n"
MF "v[116:119],a[220:223],a[188:191],v[116:119]\n"
TR "a[214:215],v1 offset:21000\n"
MF "v[116:119],a[216:219],a[148:151],v[116:119]\n"
"s_add_u32 s84,s14,20480\n"
MF "v[108:111],a[216:219],a[144:147],v[108:111]\n"
"s_add_u32 m0,s63,2048\n"
MF "v[108:111],a[220:223],a[184:187],v[108:111]\n"
LD "v4,s[24:27],s84 offen lds\n"
MF "v[100:103],a[220:223],a[180:183],v[100:103]\n"
"s_waitcnt vmcnt(11) lgkmcnt(0)\n"
MF "v[100:103],a[216:219],a[140:143],v[100:103]\n"
"s_barrier\n"
MF "v[136:139],a[200:203],a[140:143],v[136:139]\n"
TR "a[216:217],v0 offset:32768\n"
MF "v[136:139],a[204:207],a[180:183],v[136:139]\n"
TR "a[218:219],v0 offset:33280\n"
MF "v[144:147],a[204:207],a[184:187],v[144:147]\n"
"ds_read_b128 a[60:63],v2 offset:40960\n"
MF "v[144:147],a[200:203],a[144:147],v[144:147]\n"
TR "a[220:221],v0 offset:36864\n"
MF "v[152:155],a[200:203],a[148:151],v[152:155]\n"
"s_add_u32 s84,s14,29696\n"
TR "a[222:223],v0 offset:37376\n"
MF "v[152:155],a[204:207],a[188:191],v[152:155]\n"
LD "v5,s[24:27],s84 offen offset:1024 lds\n"
MF "v[160:163],a[204:207],a[192:195],v[160:163]\n"
"ds_read_b128 a[100:103],v3 offset:40960\n"
MF "v[160:163],a[200:203],a[152:155],v[160:163]\n"
"s_add_u32 m0,s62,32768\n"
MF "v[168:171],a[200:203],a[156:159],v[168:171]\n"
"s_add_u32 s82,s66,1573120\n"
"s_sub_u32 s15,s82,7864320\n"
"s_min_u32 s82,s82,s15\n"
MF "v[168:171],a[204:207],a[196:199],v[168:171]\n"
LD "v6,s[28:31],s82 offen lds\n"
MF "v[172:175],a[212:215],a[196:199],v[172:175]\n"
TR "a[200:201],v0 offset:32776\n"
MF "v[172:175],a[208:211],a[156:159],v[172:175]\n"
TR "a[202:203],v0 offset:33288\n"
MF "v[164:167],a[208:211],a[152:155],v[164:167]\n"
"ds_read_b128 a[64:67],v2 offset:43008\n"
MF "v[164:167],a[212:215],a[192:195],v[164:167]\n"
TR "a[204:205],v0 offset:36872\n"
MF "v[156:159],a[212:215],a[188:191],v[156:159]\n"
TR "a[206:207],v0 offset:37384\n"
MF "v[156:159],a[208:211],a[148:151],v[156:159]\n"
LD "v11,s[28:31],s82 offen offset:1024 lds\n"
MF "v[148:151],a[208:211],a[144:147],v[148:151]\n"
"ds_read_b128 a[104:107],v3 offset:43008\n"
MF "v[148:151],a[212:215],a[184:187],v[148:151]\n"
MF "v[140:143],a[212:215],a[180:183],v[140:143]\n"
MF "v[140:143],a[208:211],a[140:143],v[140:143]\n"
"s_waitcnt lgkmcnt(7)\n"
LD "v12,s[28:31],s82 offen offset:2048 lds\n"
MF "v[176:179],a[216:219],a[140:143],v[176:179]\n"
TR "a[208:209],v1 offset:32768\n"
MF "v[176:179],a[220:223],a[180:183],v[176:179]\n"
TR "a[210:211],v1 offset:33280\n"
MF "v[184:187],a[220:223],a[184:187],v[184:187]\n"
"ds_read_b128 a[68:71],v2 offset:45056\n"
MF "v[184:187],a[216:219],a[144:147],v[184:187]\n"
TR "a[212:213],v1 offset:36864\n"
MF "v[192:195],a[216:219],a[148:151],v[192:195]\n"
TR "a[214:215],v1 offset:37376\n"
MF "v[192:195],a[220:223],a[188:191],v[192:195]\n"
LD "v13,s[28:31],s82 offen offset:3072 lds\n"
MF "v[200:203],a[220:223],a[192:195],v[200:203]\n"
"ds_read_b128 a[108:111],v3 offset:45056\n"
MF "v[200:203],a[216:219],a[152:155],v[200:203]\n"
"s_add_u32 s84,s14,40960\n"
MF "v[208:211],a[216:219],a[156:159],v[208:211]\n"
"s_add_u32 m0,s63,4096\n"
MF "v[208:211],a[220:223],a[196:199],v[208:211]\n"
"s_waitcnt lgkmcnt(7)\n"
LD "v4,s[24:27],s84 offen lds\n"
MF "v[212:215],a[204:207],a[196:199],v[212:215]\n"
"s_sub_u32 s66,s66,s83\n"
TR "a[216:217],v1 offset:32776\n"
MF "v[212:215],a[200:203],a[156:159],v[212:215]\n"
"s_add_u32 m0,s62,49152\n"
TR "a[218:219],v1 offset:33288\n"
MF "v[204:207],a[200:203],a[152:155],v[204:207]\n"
"s_add_u32 s82,s66,2359296\n"
"ds_read_b128 a[72:75],v2 offset:47104\n"
MF "v[204:207],a[204:207],a[192:195],v[204:207]\n"
"s_sub_u32 s15,s82,7864320\n"
TR "a[220:221],v1 offset:36872\n"
MF "v[196:199],a[204:207],a[188:191],v[196:199]\n"
"s_min_u32 s82,s82,s15\n"
TR "a[222:223],v1 offset:37384\n"
MF "v[196:199],a[200:203],a[148:151],v[196:199]\n"
LD "v6,s[28:31],s82 offen lds\n"
MF "v[188:191],a[200:203],a[144:147],v[188:191]\n"
"v_add_u32 v0,0xffff4000,v0\n"
MF "v[188:191],a[204:207],a[184:187],v[188:191]\n"
"v_add_u32 v1,0xffff4000,v1\n"
MF "v[180:183],a[204:207],a[180:183],v[180:183]\n"
"s_waitcnt vmcnt(14) lgkmcnt(0)\n"
LD "v11,s[28:31],s82 offen offset:1024 lds\n"
MF "v[180:183],a[200:203],a[140:143],v[180:183]\n"
"s_barrier\n"
MF "v[216:219],a[208:211],a[140:143],v[216:219]\n"
TR "a[200:201],v0\n"
MF "v[216:219],a[212:215],a[180:183],v[216:219]\n"
TR "a[202:203],v0 offset:512\n"
MF "v[224:227],a[212:215],a[184:187],v[224:227]\n"
"ds_read_b128 a[112:115],v3 offset:47104\n"
MF "v[224:227],a[208:211],a[144:147],v[224:227]\n"
TR "a[204:205],v0 offset:4096\n"
MF "v[232:235],a[208:211],a[148:151],v[232:235]\n"
TR "a[206:207],v0 offset:4608\n"
MF "v[232:235],a[212:215],a[188:191],v[232:235]\n"
LD "v12,s[28:31],s82 offen offset:2048 lds\n"
MF "v[240:243],a[212:215],a[192:195],v[240:243]\n"
"ds_read_b128 a[76:79],v2 offset:49152\n"
MF "v[240:243],a[208:211],a[152:155],v[240:243]\n"
"ds_read_b128 a[116:119],v3 offset:49152\n"
MF "v[248:251],a[208:211],a[156:159],v[248:251]\n"
MF "v[248:251],a[212:215],a[196:199],v[248:251]\n"
LD "v13,s[28:31],s82 offen offset:3072 lds\n"
MF "v[252:255],a[220:223],a[196:199],v[252:255]\n"
TR "a[208:209],v0 offset:8\n"
MF "v[252:255],a[216:219],a[156:159],v[252:255]\n"
TR "a[210:211],v0 offset:520\n"
MF "v[244:247],a[216:219],a[152:155],v[244:247]\n"
"s_add_u32 s14,s81,512\n"
"s_sub_u32 s15,s14,1280\n"
MF "v[244:247],a[220:223],a[192:195],v[244:247]\n"
"s_min_u32 s14,s14,s15\n"
"s_add_u32 s14,s14,s68\n"
TR "a[212:213],v0 offset:4104\n"
MF "v[236:239],a[220:223],a[188:191],v[236:239]\n"
"s_add_u32 s84,s14,0\n"
"s_add_u32 m0,s63,20480\n"
TR "a[214:215],v0 offset:4616\n"
MF "v[236:239],a[216:219],a[148:151],v[236:239]\n"
LD "v4,s[24:27],s84 offen lds\n"
MF "v[228:231],a[216:219],a[144:147],v[228:231]\n"
MF "v[228:231],a[220:223],a[184:187],v[228:231]\n"
MF "v[220:223],a[220:223],a[180:183],v[220:223]\n"
"s_add_u32 s84,s14,9216\n"
MF "v[220:223],a[216:219],a[140:143],v[220:223]\n"
"s_waitcnt lgkmcnt(4)\n"
LD "v5,s[24:27],s84 offen offset:1024 lds\n"
MF "v[16:19],a[200:203],a[60:63],v[16:19]\n"
TR "a[216:217],v1\n"
MF "v[16:19],a[204:207],a[100:103],v[16:19]\n"
"s_add_u32 m0,s62,65536\n"
TR "a[218:219],v1 offset:512\n"
MF "v[24:27],a[204:207],a[104:107],v[24:27]\n"
"s_add_u32 s82,s66,2359424\n"
MF "v[24:27],a[200:203],a[64:67],v[24:27]\n"
"s_sub_u32 s15,s82,7864320\n"
TR "a[220:221],v1 offset:4096\n"
MF "v[32:35],a[200:203],a[68:71],v[32:35]\n"
"s_min_u32 s82,s82,s15\n"
TR "a[222:223],v1 offset:4608\n"
MF "v[32:35],a[204:207],a[108:111],v[32:35]\n"
LD "v6,s[28:31],s82 offen lds\n"
MF "v[40:43],a[204:207],a[112:115],v[40:43]\n"
MF "v[40:43],a[200:203],a[72:75],v[40:43]\n"
MF "v[48:51],a[200:203],a[76:79],v[48:51]\n"
MF "v[48:51],a[204:207],a[116:119],v[48:51]\n"
"s_waitcnt lgkmcnt(4)\n"
LD "v11,s[28:31],s82 offen offset:1024 lds\n"
MF "v[52:55],a[212:215],a[116:119],v[52:55]\n"
TR "a[200:201],v1 offset:8\n"
MF "v[52:55],a[208:211],a[76:79],v[52:55]\n"
TR "a[202:203],v1 offset:520\n"
MF "v[44:47],a[208:211],a[72:75],v[44:47]\n"
MF "v[44:47],a[212:215],a[112:115],v[44:47]\n"
TR "a[204:205],v1 offset:4104\n"
MF "v[36:39],a[212:215],a[108:111],v[36:39]\n"
TR "a[206:207],v1 offset:4616\n"
MF "v[36:39],a[208:211],a[68:71],v[36:39]\n"
LD "v12,s[28:31],s82 offen offset:2048 lds\n"
MF "v[28:31],a[208:211],a[64:67],v[28:31]\n"
MF "v[28:31],a[212:215],a[104:107],v[28:31]\n"
MF "v[20:23],a[212:215],a[100:103],v[20:23]\n"
"s_waitcnt vmcnt(16) lgkmcnt(0)\n"
LD "v13,s[28:31],s82 offen offset:3072 lds\n"
MF "v[20:23],a[208:211],a[60:63],v[20:23]\n"
"s_barrier\n"
MF "v[56:59],a[216:219],a[60:63],v[56:59]\n"
"s_add_u32 s84,s14,20480\n"
TR "a[208:209],v0 offset:16384\n"
MF "v[56:59],a[220:223],a[100:103],v[56:59]\n"
"s_add_u32 m0,s63,22528\n"
TR "a[210:211],v0 offset:16896\n"
MF "v[64:67],a[220:223],a[104:107],v[64:67]\n"
LD "v4,s[24:27],s84 offen lds\n"
MF "v[64:67],a[216:219],a[64:67],v[64:67]\n"
TR "a[212:213],v0 offset:20480\n"
MF "v[72:75],a[216:219],a[68:71],v[72:75]\n"
TR "a[214:215],v0 offset:20992\n"
MF "v[72:75],a[220:223],a[108:111],v[72:75]\n"
MF "v[80:83],a[220:223],a[112:115],v[80:83]\n"
"s_add_u32 s84,s14,29696\n"
MF "v[80:83],a[216:219],a[72:75],v[80:83]\n"
LD "v5,s[24:27],s84 offen offset:1024 lds\n"
MF "v[88:91],a[216:219],a[76:79],v[88:91]\n"
"s_add_u32 m0,s62,81920\n"
MF "v[88:91],a[220:223],a[116:119],v[88:91]\n"
"s_add_u32 s82,s66,2359552\n"
MF "v[92:95],a[204:207],a[116:119],v[92:95]\n"
"s_sub_u32 s15,s82,7864320\n"
TR "a[216:217],v0 offset:16392\n"
MF "v[92:95],a[200:203],a[76:79],v[92:95]\n"
"s_min_u32 s82,s82,s15\n"
TR "a[218:219],v0 offset:16904\n"
MF "v[84:87],a[200:203],a[72:75],v[84:87]\n"
LD "v6,s[28:31],s82 offen lds\n"
MF "v[84:87],a[204:207],a[112:115],v[84:87]\n"
TR "a[220:221],v0 offset:20488\n"
MF "v[76:79],a[204:207],a[108:111],v[76:79]\n"
TR "a[222:223],v0 offset:21000\n"
MF "v[76:79],a[200:203],a[68:71],v[76:79]\n"
MF "v[68:71],a[200:203],a[64:67],v[68:71]\n"
MF "v[68:71],a[204:207],a[104:107],v[68:71]\n"
LD "v11,s[28:31],s82 offen offset:1024 lds\n"
MF "v[60:63],a[204:207],a[100:103],v[60:63]\n"
MF "v[60:63],a[200:203],a[60:63],v[60:63]\n"
"s_waitcnt lgkmcnt(4)\n"
MF "v[96:99],a[208:211],a[60:63],v[96:99]\n"
TR "a[200:201],v1 offset:16384\n"
MF "v[96:99],a[212:215],a[100:103],v[96:99]\n"
TR "a[202:203],v1 offset:16896\n"
MF "v[104:107],a[212:215],a[104:107],v[104:107]\n"
LD "v12,s[28:31],s82 offen offset:2048 lds\n"
MF "v[104:107],a[208:211],a[64:67],v[104:107]\n"
TR "a[204:205],v1 offset:20480\n"
MF "v[112:115],a[208:211],a[68:71],v[112:115]\n"
TR "a[206:207],v1 offset:20992\n"
MF "v[112:115],a[212:215],a[108:111],v[112:115]\n"
MF "v[120:123],a[212:215],a[112:115],v[120:123]\n"
MF "v[120:123],a[208:211],a[72:75],v[120:123]\n"
LD "v13,s[28:31],s82 offen offset:3072 lds\n"
MF "v[128:131],a[208:211],a[76:79],v[128:131]\n"
MF "v[128:131],a[212:215],a[116:119],v[128:131]\n"
"s_waitcnt lgkmcnt(4)\n"
MF "v[132:135],a[220:223],a[116:119],v[132:135]\n"
"s_add_u32 s84,s14,40960\n"
TR "a[208:209],v1 offset:16392\n"
MF "v[132:135],a[216:219],a[76:79],v[132:135]\n"
"s_add_u32 m0,s63,24576\n"
TR "a[210:211],v1 offset:16904\n"
MF "v[124:127],a[216:219],a[72:75],v[124:127]\n"
LD "v4,s[24:27],s84 offen lds\n"
MF "v[124:127],a[220:223],a[112:115],v[124:127]\n"
"s_add_u32 m0,s62,0\n"
TR "a[212:213],v1 offset:20488\n"
MF "v[116:119],a[220:223],a[108:111],v[116:119]\n"
"s_add_u32 s82,s66,3145728\n"
TR "a[214:215],v1 offset:21000\n"
MF "v[116:119],a[216:219],a[68:71],v[116:119]\n"
"s_sub_u32 s15,s82,7864320\n"
MF "v[108:111],a[216:219],a[64:67],v[108:111]\n"
"s_min_u32 s82,s82,s15\n"
MF "v[108:111],a[220:223],a[104:107],v[108:111]\n"
LD "v6,s[28:31],s82 offen lds\n"
MF "v[100:103],a[220:223],a[100:103],v[100:103]\n"
"s_waitcnt vmcnt(18) lgkmcnt(0)\n"
MF "v[100:103],a[216:219],a[60:63],v[100:103]\n"
"s_barrier\n"
MF "v[136:139],a[200:203],a[60:63],v[136:139]\n"
TR "a[216:217],v0 offset:32768\n"
MF "v[136:139],a[204:207],a[100:103],v[136:139]\n"
TR "a[218:219],v0 offset:33280\n"
MF "v[144:147],a[204:207],a[104:107],v[144:147]\n"
"ds_read_b128 a[140:143],v2\n"
MF "v[144:147],a[200:203],a[64:67],v[144:147]\n"
TR "a[220:221],v0 offset:36864\n"
MF "v[152:155],a[200:203],a[68:71],v[152:155]\n"
TR "a[222:223],v0 offset:37376\n"
MF "v[152:155],a[204:207],a[108:111],v[152:155]\n"
LD "v11,s[28:31],s82 offen offset:1024 lds\n"
MF "v[160:163],a[204:207],a[112:115],v[160:163]\n"
"ds_read_b128 a[180:183],v3\n"
MF "v[160:163],a[200:203],a[72:75],v[160:163]\n"
MF "v[168:171],a[200:203],a[76:79],v[168:171]\n"
MF "v[168:171],a[204:207],a[116:119],v[168:171]\n"
LD "v12,s[28:31],s82 offen offset:2048 lds\n"
MF "v[172:175],a[212:215],a[116:119],v[172:175]\n"
TR "a[200:201],v0 offset:32776\n"
MF "v[172:175],a[208:211],a[76:79],v[172:175]\n"
TR "a[202:203],v0 offset:33288\n"
MF "v[164:167],a[208:211],a[72:75],v[164:167]\n"
"ds_read_b128 a[144:147],v2 offset:2048\n"
MF "v[164:167],a[212:215],a[112:115],v[164:167]\n"
TR "a[204:205],v0 offset:36872\n"
MF "v[156:159],a[212:215],a[108:111],v[156:159]\n"
TR "a[206:207],v0 offset:37384\n"
MF "v[156:159],a[208:211],a[68:71],v[156:159]\n"
LD "v13,s[28:31],s82 offen offset:3072 lds\n"
MF "v[148:151],a[208:211],a[64:67],v[148:151]\n"
"ds_read_b128 a[184:187],v3 offset:2048\n"
MF "v[148:151],a[212:215],a[104:107],v[148:151]\n"
"s_add_u32 s14,s81,640\n"
"s_sub_u32 s15,s14,1280\n"
"s_min_u32 s14,s14,s15\n"
MF "v[140:143],a[212:215],a[100:103],v[140:143]\n"
"s_add_u32 s14,s14,s68\n"
"s_add_u32 s84,s14,0\n"
"s_add_u32 m0,s63,40960\n"
MF "v[140:143],a[208:211],a[60:63],v[140:143]\n"
"s_waitcnt lgkmcnt(7)\n"
LD "v4,s[24:27],s84 offen lds\n"
MF "v[176:179],a[216:219],a[60:63],v[176:179]\n"
TR "a[208:209],v1 offset:32768\n"
MF "v[176:179],a[220:223],a[100:103],v[176:179]\n"
TR "a[210:211],v1 offset:33280\n"
MF "v[184:187],a[220:223],a[104:107],v[184:187]\n"
"ds_read_b128 a[148:151],v2 offset:4096\n"
MF "v[184:187],a[216:219],a[64:67],v[184:187]\n"
TR "a[212:213],v1 offset:36864\n"
MF "v[192:195],a[216:219],a[68:71],v[192:195]\n"
"s_add_u32 s84,s14,9216\n"
TR "a[214:215],v1 offset:37376\n"
MF "v[192:195],a[220:223],a[108:111],v[192:195]\n"
LD "v5,s[24:27],s84 offen offset:1024 lds\n"
MF "v[200:203],a[220:223],a[112:115],v[200:203]\n"
"ds_read_b128 a[188:191],v3 offset:4096\n"
MF "v[200:203],a[216:219],a[72:75],v[200:203]\n"
"s_add_u32 m0,s62,16384\n"
MF "v[208:211],a[216:219],a[76:79],v[208:211]\n"
"s_add_u32 s82,s66,3145856\n"
"s_sub_u32 s15,s82,7864320\n"
"s_min_u32 s82,s82,s15\n"
MF "v[208:211],a[220:223],a[116:119],v[208:211]\n"
"s_waitcnt lgkmcnt(7)\n"
LD "v6,s[28:31],s82 offen lds\n"
MF "v[212:215],a[204:207],a[116:119],v[212:215]\n"
TR "a[216:217],v1 offset:32776\n"
MF "v[212:215],a[200:203],a[76:79],v[212:215]\n"
TR "a[218:219],v1 offset:33288\n"
MF "v[204:207],a[200:203],a[72:75],v[204:207]\n"
"ds_read_b128 a[152:155],v2 offset:6144\n"
MF "v[204:207],a[204:207],a[112:115],v[204:207]\n"
TR "a[220:221],v1 offset:36872\n"
MF "v[196:199],a[204:207],a[108:111],v[196:199]\n"
TR "a[222:223],v1 offset:37384\n"
MF "v[196:199],a[200:203],a[68:71],v[196:199]\n"
LD "v11,s[28:31],s82 offen offset:1024 lds\n"
MF "v[188:191],a[200:203],a[64:67],v[188:191]\n"
"v_add_u32 v0,0xc000,v0\n"
MF "v[188:191],a[204:207],a[104:107],v[188:191]\n"
"v_add_u32 v1,0xc000,v1\n"
MF "v[180:183],a[204:207],a[100:103],v[180:183]\n"
"s_waitcnt vmcnt(21) lgkmcnt(0)\n"
LD "v12,s[28:31],s82 offen offset:2048 lds\n"
MF "v[180:183],a[200:203],a[60:63],v[180:183]\n"
"s_barrier\n"
MF "v[216:219],a[208:211],a[60:63],v[216:219]\n"
TR "a[200:201],v0\n"
MF "v[216:219],a[212:215],a[100:103],v[216:219]\n"
TR "a[202:203],v0 offset:512\n"
MF "v[224:227],a[212:215],a[104:107],v[224:227]\n"
"ds_read_b128 a[192:195],v3 offset:6144\n"
MF "v[224:227],a[208:211],a[64:67],v[224:227]\n"
TR "a[204:205],v0 offset:4096\n"
MF "v[232:235],a[208:211],a[68:71],v[232:235]\n"
TR "a[206:207],v0 offset:4608\n"
MF "v[232:235],a[212:215],a[108:111],v[232:235]\n"
LD "v13,s[28:31],s82 offen offset:3072 lds\n"
MF "v[240:243],a[212:215],a[112:115],v[240:243]\n"
"ds_read_b128 a[156:159],v2 offset:8192\n"
MF "v[240:243],a[208:211],a[72:75],v[240:243]\n"
"s_add_u32 s84,s14,20480\n"
"ds_read_b128 a[196:199],v3 offset:8192\n"
MF "v[248:251],a[208:211],a[76:79],v[248:251]\n"
"s_add_u32 m0,s63,43008\n"
MF "v[248:251],a[212:215],a[116:119],v[248:251]\n"
LD "v4,s[24:27],s84 offen lds\n"
MF "v[252:255],a[220:223],a[116:119],v[252:255]\n"
TR "a[208:209],v0 offset:8\n"
MF "v[252:255],a[216:219],a[76:79],v[252:255]\n"
TR "a[210:211],v0 offset:520\n"
MF "v[244:247],a[216:219],a[72:75],v[244:247]\n"
MF "v[244:247],a[220:223],a[112:115],v[244:247]\n"
TR "a[212:213],v0 offset:4104\n"
MF "v[236:239],a[220:223],a[108:111],v[236:239]\n"
"s_add_u32 s84,s14,29696\n"
TR "a[214:215],v0 offset:4616\n"
MF "v[236:239],a[216:219],a[68:71],v[236:239]\n"
LD "v5,s[24:27],s84 offen offset:1024 lds\n"
MF "v[228:231],a[216:219],a[64:67],v[228:231]\n"
MF "v[228:231],a[220:223],a[104:107],v[228:231]\n"
"s_add_u32 m0,s62,32768\n"
MF "v[220:223],a[220:223],a[100:103],v[220:223]\n"
"s_add_u32 s82,s66,3145984\n"
"s_sub_u32 s15,s82,7864320\n"
"s_min_u32 s82,s82,s15\n"
MF "v[220:223],a[216:219],a[60:63],v[220:223]\n"
"s_waitcnt lgkmcnt(4)\n"
LD "v6,s[28:31],s82 offen lds\n"
MF "v[16:19],a[200:203],a[140:143],v[16:19]\n"
TR "a[216:217],v1\n"
MF "v[16:19],a[204:207],a[180:183],v[16:19]\n"
TR "a[218:219],v1 offset:512\n"
MF "v[24:27],a[204:207],a[184:187],v[24:27]\n"
MF "v[24:27],a[200:203],a[144:147],v[24:27]\n"
TR "a[220:221],v1 offset:4096\n"
MF "v[32:35],a[200:203],a[148:151],v[32:35]\n"
TR "a[222:223],v1 offset:4608\n"
MF "v[32:35],a[204:207],a[188:191],v[32:35]\n"
LD "v11,s[28:31],s82 offen offset:1024 lds\n"
MF "v[40:43],a[204:207],a[192:195],v[40:43]\n"
MF "v[40:43],a[200:203],a[152:155],v[40:43]\n"
MF "v[48:51],a[200:203],a[156:159],v[48:51]\n"
MF "v[48:51],a[204:207],a[196:199],v[48:51]\n"
"s_waitcnt lgkmcnt(4)\n"
LD "v12,s[28:31],s82 offen offset:2048 lds\n"
MF "v[52:55],a[212:215],a[196:199],v[52:55]\n"
TR "a[200:201],v1 offset:8\n"
MF "v[52:55],a[208:211],a[156:159],v[52:55]\n"
TR "a[202:203],v1 offset:520\n"
MF "v[44:47],a[208:211],a[152:155],v[44:47]\n"
MF "v[44:47],a[212:215],a[192:195],v[44:47]\n"
TR "a[204:205],v1 offset:4104\n"
MF "v[36:39],a[212:215],a[188:191],v[36:39]\n"
".Lpg2a:\n"
"s_cbranch_execz .Lisr1\n"
TR "a[206:207],v1 offset:4616\n"
MF "v[36:39],a[208:211],a[148:151],v[36:39]\n"
LD "v13,s[28:31],s82 offen offset:3072 lds\n"
MF "v[28:31],a[208:211],a[144:147],v[28:31]\n"
"s_add_u32 s84,s14,40960\n"
MF "v[28:31],a[212:215],a[184:187],v[28:31]\n"
"s_add_u32 m0,s63,45056\n"
MF "v[20:23],a[212:215],a[180:183],v[20:23]\n"
"s_waitcnt vmcnt(23) lgkmcnt(0)\n"
LD "v4,s[24:27],s84 offen lds\n"
MF "v[20:23],a[208:211],a[140:143],v[20:23]\n"
"s_barrier\n"
AW "a253,v10\n"
"v_add_u32 v10,1,v10\n"
"s_bfe_u32 s58,s22,0x30014\n"
"s_lshl_b32 s58,s58,2\n"
"s_add_u32 s58,s58,s56\n"
"s_lshl_b32 s58,s58,6\n"
"s_add_u32 s58,s58,s86\n"
"v_readlane_b32 s76,v10,s78\n"
"buffer_store_dword v10,v14,s[40:43],s58 offen offset:128 sc1\n"
".Lhs5:\n"
MF "v[56:59],a[216:219],a[140:143],v[56:59]\n"
"s_add_u32 m0,s62,49152\n"
"s_add_u32 s82,s66,3932160\n"
TR "a[208:209],v0 offset:16384\n"
MF "v[56:59],a[220:223],a[180:183],v[56:59]\n"
"s_sub_u32 s15,s82,7864320\n"
"s_min_u32 s82,s82,s15\n"
TR "a[210:211],v0 offset:16896\n"
MF "v[64:67],a[220:223],a[184:187],v[64:67]\n"
LD "v6,s[28:31],s82 offen lds\n"
MF "v[64:67],a[216:219],a[144:147],v[64:67]\n"
TR "a[212:213],v0 offset:20480\n"
MF "v[72:75],a[216:219],a[148:151],v[72:75]\n"
TR "a[214:215],v0 offset:20992\n"
MF "v[72:75],a[220:223],a[188:191],v[72:75]\n"
"buffer_load_dword a249,v15,s[48:51],s60 offen offset:1024\n"
MF "v[80:83],a[220:223],a[192:195],v[80:83]\n"
"s_bfe_u32 s59,s23,0x30003\n"
MF "v[80:83],a[216:219],a[152:155],v[80:83]\n"
LD "v11,s[28:31],s82 offen offset:1024 lds\n"
MF "v[88:91],a[216:219],a[156:159],v[88:91]\n"
"s_add_u32 s59,s59,1\n"
MF "v[88:91],a[220:223],a[196:199],v[88:91]\n"
"s_lshl_b32 s59,s59,7\n"
MF "v[92:95],a[204:207],a[196:199],v[92:95]\n"
"s_and_b32 s59,s59,768\n"
TR "a[216:217],v0 offset:16392\n"
MF "v[92:95],a[200:203],a[156:159],v[92:95]\n"
"s_add_u32 s59,s59,s60\n"
TR "a[218:219],v0 offset:16904\n"
MF "v[84:87],a[200:203],a[152:155],v[84:87]\n"
LD "v12,s[28:31],s82 offen offset:2048 lds\n"
MF "v[84:87],a[204:207],a[192:195],v[84:87]\n"
TR "a[220:221],v0 offset:20488\n"
MF "v[76:79],a[204:207],a[188:191],v[76:79]\n"
TR "a[222:223],v0 offset:21000\n"
MF "v[76:79],a[200:203],a[148:151],v[76:79]\n"
MF "v[68:71],a[200:203],a[144:147],v[68:71]\n"
MF "v[68:71],a[204:207],a[184:187],v[68:71]\n"
LD "v13,s[28:31],s82 offen offset:3072 lds\n"
MF "v[60:63],a[204:207],a[180:183],v[60:63]\n"
MF "v[60:63],a[200:203],a[140:143],v[60:63]\n"
"s_add_u32 s14,s81,768\n"
"s_sub_u32 s15,s14,1280\n"
"s_waitcnt lgkmcnt(4)\n"
MF "v[96:99],a[208:211],a[140:143],v[96:99]\n"
"s_min_u32 s14,s14,s15\n"
"s_add_u32 s14,s14,s68\n"
TR "a[200:201],v1 offset:16384\n"
MF "v[96:99],a[212:215],a[180:183],v[96:99]\n"
"s_add_u32 s84,s14,0\n"
"s_add_u32 m0,s63,0\n"
TR "a[202:203],v1 offset:16896\n"
MF "v[104:107],a[212:215],a[184:187],v[104:107]\n"
LD "v4,s[24:27],s84 offen lds\n"
MF "v[104:107],a[208:211],a[144:147],v[104:107]\n"
TR "a[204:205],v1 offset:20480\n"
MF "v[112:115],a[208:211],a[148:151],v[112:115]\n"
TR "a[206:207],v1 offset:20992\n"
MF "v[112:115],a[212:215],a[188:191],v[112:115]\n"
MF "v[120:123],a[212:215],a[192:195],v[120:123]\n"
"s_add_u32 s84,s14,9216\n"
MF "v[120:123],a[208:211],a[152:155],v[120:123]\n"
LD "v5,s[24:27],s84 offen offset:1024 lds\n"
MF "v[128:131],a[208:211],a[156:159],v[128:131]\n"
MF "v[128:131],a[212:215],a[196:199],v[128:131]\n"
"s_waitcnt lgkmcnt(4)\n"
MF "v[132:135],a[220:223],a[196:199],v[132:135]\n"
TR "a[208:209],v1 offset:16392\n"
MF "v[132:135],a[216:219],a[156:159],v[132:135]\n"
TR "a[210:211],v1 offset:16904\n"
MF "v[124:127],a[216:219],a[152:155],v[124:127]\n"
MF "v[124:127],a[220:223],a[192:195],v[124:127]\n"
TR "a[212:213],v1 offset:20488\n"
MF "v[116:119],a[220:223],a[188:191],v[116:119]\n"
TR "a[214:215],v1 offset:21000\n"
MF "v[116:119],a[216:219],a[148:151],v[116:119]\n"
MF "v[108:111],a[216:219],a[144:147],v[108:111]\n"
MF "v[108:111],a[220:223],a[184:187],v[108:111]\n"
MF "v[100:103],a[220:223],a[180:183],v[100:103]\n"
"s_waitcnt vmcnt(24) lgkmcnt(0)\n"
MF "v[100:103],a[216:219],a[140:143],v[100:103]\n"
"s_barrier\n"
MF "v[136:139],a[200:203],a[140:143],v[136:139]\n"
TR "a[216:217],v0 offset:32768\n"
MF "v[136:139],a[204:207],a[180:183],v[136:139]\n"
"s_add_u32 m0,s62,65536\n"
TR "a[218:219],v0 offset:33280\n"
MF "v[144:147],a[204:207],a[184:187],v[144:147]\n"
"s_add_u32 s82,s66,3932288\n"
"ds_read_b128 a[60:63],v2 offset:20480\n"
MF "v[144:147],a[200:203],a[144:147],v[144:147]\n"
"s_sub_u32 s15,s82,7864320\n"
TR "a[220:221],v0 offset:36864\n"
MF "v[152:155],a[200:203],a[148:151],v[152:155]\n"
"s_min_u32 s82,s82,s15\n"
TR "a[222:223],v0 offset:37376\n"
MF "v[152:155],a[204:207],a[188:191],v[152:155]\n"
LD "v6,s[28:31],s82 offen lds\n"
MF "v[160:163],a[204:207],a[192:195],v[160:163]\n"
"ds_read_b128 a[100:103],v3 offset:20480\n"
MF "v[160:163],a[200:203],a[152:155],v[160:163]\n"
MF "v[168:171],a[200:203],a[156:159],v[168:171]\n"
MF "v[168:171],a[204:207],a[196:199],v[168:171]\n"
LD "v11,s[28:31],s82 offen offset:1024 lds\n"
MF "v[172:175],a[212:215],a[196:199],v[172:175]\n"
TR "a[200:201],v0 offset:32776\n"
MF "v[172:175],a[208:211],a[156:159],v[172:175]\n"
TR "a[202:203],v0 offset:33288\n"
MF "v[164:167],a[208:211],a[152:155],v[164:167]\n"
"ds_read_b128 a[64:67],v2 offset:22528\n"
MF "v[164:167],a[212:215],a[192:195],v[164:167]\n"
TR "a[204:205],v0 offset:36872\n"
MF "v[156:159],a[212:215],a[188:191],v[156:159]\n"
TR "a[206:207],v0 offset:37384\n"
MF "v[156:159],a[208:211],a[148:151],v[156:159]\n"
LD "v12,s[28:31],s82 offen offset:2048 lds\n"
MF "v[148:151],a[208:211],a[144:147],v[148:151]\n"
"ds_read_b128 a[104:107],v3 offset:22528\n"
MF "v[148:151],a[212:215],a[184:187],v[148:151]\n"
MF "v[140:143],a[212:215],a[180:183],v[140:143]\n"
MF "v[140:143],a[208:211],a[140:143],v[140:143]\n"
"s_waitcnt lgkmcnt(7)\n"
LD "v13,s[28:31],s82 offen offset:3072 lds\n"
MF "v[176:179],a[216:219],a[140:143],v[176:179]\n"
TR "a[208:209],v1 offset:32768\n"
MF "v[176:179],a[220:223],a[180:183],v[176:179]\n"
TR "a[210:211],v1 offset:33280\n"
MF "v[184:187],a[220:223],a[184:187],v[184:187]\n"
"ds_read_b128 a[68:71],v2 offset:24576\n"
MF "v[184:187],a[216:219],a[144:147],v[184:187]\n"
"s_add_u32 s84,s14,20480\n"
TR "a[212:213],v1 offset:36864\n"
MF "v[192:195],a[216:219],a[148:151],v[192:195]\n"
"s_add_u32 m0,s63,2048\n"
TR "a[214:215],v1 offset:37376\n"
MF "v[192:195],a[220:223],a[188:191],v[192:195]\n"
LD "v4,s[24:27],s84 offen lds\n"
MF "v[200:203],a[220:223],a[192:195],v[200:203]\n"
"ds_read_b128 a[108:111],v3 offset:24576\n"
MF "v[200:203],a[216:219],a[152:155],v[200:203]\n"
MF "v[208:211],a[216:219],a[156:159],v[208:211]\n"
"s_add_u32 s84,s14,29696\n"
MF "v[208:211],a[220:223],a[196:199],v[208:211]\n"
"s_waitcnt lgkmcnt(7)\n"
LD "v5,s[24:27],s84 offen offset:1024 lds\n"
MF "v[212:215],a[204:207],a[196:199],v[212:215]\n"
TR "a[216:217],v1 offset:32776\n"
MF "v[212:215],a[200:203],a[156:159],v[212:215]\n"
TR "a[218:219],v1 offset:33288\n"
MF "v[204:207],a[200:203],a[152:155],v[204:207]\n"
"ds_read_b128 a[72:75],v2 offset:26624\n"
MF "v[204:207],a[204:207],a[192:195],v[204:207]\n"
TR "a[220:221],v1 offset:36872\n"
MF "v[196:199],a[204:207],a[188:191],v[196:199]\n"
TR "a[222:223],v1 offset:37384\n"
MF "v[196:199],a[200:203],a[148:151],v[196:199]\n"
MF "v[188:191],a[200:203],a[144:147],v[188:191]\n"
"v_add_u32 v0,0xffff4000,v0\n"
MF "v[188:191],a[204:207],a[184:187],v[188:191]\n"
"v_add_u32 v1,0xffff4000,v1\n"
MF "v[180:183],a[204:207],a[180:183],v[180:183]\n"
"s_waitcnt vmcnt(26) lgkmcnt(0)\n"
MF "v[180:183],a[200:203],a[140:143],v[180:183]\n"
"s_barrier\n"
MF "v[216:219],a[208:211],a[140:143],v[216:219]\n"
TR "a[200:201],v0\n"
MF "v[216:219],a[212:215],a[180:183],v[216:219]\n"
"s_add_u32 m0,s62,81920\n"
TR "a[202:203],v0 offset:512\n"
MF "v[224:227],a[212:215],a[184:187],v[224:227]\n"
"s_add_u32 s82,s66,3932416\n"
"ds_read_b128 a[112:115],v3 offset:26624\n"
MF "v[224:227],a[208:211],a[144:147],v[224:227]\n"
"s_sub_u32 s15,s82,7864320\n"
TR "a[204:205],v0 offset:4096\n"
MF "v[232:235],a[208:211],a[148:151],v[232:235]\n"
"s_min_u32 s82,s82,s15\n"
TR "a[206:207],v0 offset:4608\n"
MF "v[232:235],a[212:215],a[188:191],v[232:235]\n"
LD "v6,s[28:31],s82 offen lds\n"
MF "v[240:243],a[212:215],a[192:195],v[240:243]\n"
"ds_read_b128 a[76:79],v2 offset:28672\n"
MF "v[240:243],a[208:211],a[152:155],v[240:243]\n"
"ds_read_b128 a[116:119],v3 offset:28672\n"
MF "v[248:251],a[208:211],a[156:159],v[248:251]\n"
MF "v[248:251],a[212:215],a[196:199],v[248:251]\n"
LD "v11,s[28:31],s82 offen offset:1024 lds\n"
MF "v[252:255],a[220:223],a[196:199],v[252:255]\n"
TR "a[208:209],v0 offset:8\n"
MF "v[252:255],a[216:219],a[156:159],v[252:255]\n"
TR "a[210:211],v0 offset:520\n"
MF "v[244:247],a[216:219],a[152:155],v[244:247]\n"
MF "v[244:247],a[220:223],a[192:195],v[244:247]\n"
TR "a[212:213],v0 offset:4104\n"
MF "v[236:239],a[220:223],a[188:191],v[236:239]\n"
TR "a[214:215],v0 offset:4616\n"
MF "v[236:239],a[216:219],a[148:151],v[236:239]\n"
LD "v12,s[28:31],s82 offen offset:2048 lds\n"
MF "v[228:231],a[216:219],a[144:147],v[228:231]\n"
MF "v[228:231],a[220:223],a[184:187],v[228:231]\n"
MF "v[220:223],a[220:223],a[180:183],v[220:223]\n"
MF "v[220:223],a[216:219],a[140:143],v[220:223]\n"
"s_waitcnt lgkmcnt(4)\n"
LD "v13,s[28:31],s82 offen offset:3072 lds\n"
MF "v[16:19],a[200:203],a[60:63],v[16:19]\n"
TR "a[216:217],v1\n"
MF "v[16:19],a[204:207],a[100:103],v[16:19]\n"
TR "a[218:219],v1 offset:512\n"
MF "v[24:27],a[204:207],a[104:107],v[24:27]\n"
MF "v[24:27],a[200:203],a[64:67],v[24:27]\n"
"s_add_u32 s84,s14,40960\n"
TR "a[220:221],v1 offset:4096\n"
MF "v[32:35],a[200:203],a[68:71],v[32:35]\n"
"s_add_u32 m0,s63,4096\n"
TR "a[222:223],v1 offset:4608\n"
MF "v[32:35],a[204:207],a[108:111],v[32:35]\n"
LD "v4,s[24:27],s84 offen lds\n"
MF "v[40:43],a[204:207],a[112:115],v[40:43]\n"
MF "v[40:43],a[200:203],a[72:75],v[40:43]\n"
MF "v[48:51],a[200:203],a[76:79],v[48:51]\n"
MF "v[48:51],a[204:207],a[116:119],v[48:51]\n"
"s_waitcnt lgkmcnt(4)\n"
MF "v[52:55],a[212:215],a[116:119],v[52:55]\n"
TR "a[200:201],v1 offset:8\n"
MF "v[52:55],a[208:211],a[76:79],v[52:55]\n"
TR "a[202:203],v1 offset:520\n"
MF "v[44:47],a[208:211],a[72:75],v[44:47]\n"
MF "v[44:47],a[212:215],a[112:115],v[44:47]\n"
TR "a[204:205],v1 offset:4104\n"
MF "v[36:39],a[212:215],a[108:111],v[36:39]\n"
TR "a[206:207],v1 offset:4616\n"
MF "v[36:39],a[208:211],a[68:71],v[36:39]\n"
MF "v[28:31],a[208:211],a[64:67],v[28:31]\n"
MF "v[28:31],a[212:215],a[104:107],v[28:31]\n"
MF "v[20:23],a[212:215],a[100:103],v[20:23]\n"
"s_waitcnt vmcnt(25) lgkmcnt(0)\n"
MF "v[20:23],a[208:211],a[60:63],v[20:23]\n"
"s_barrier\n"
MF "v[56:59],a[216:219],a[60:63],v[56:59]\n"
"s_add_u32 m0,s62,0\n"
"s_add_u32 s82,s66,4718592\n"
TR "a[208:209],v0 offset:16384\n"
MF "v[56:59],a[220:223],a[100:103],v[56:59]\n"
"s_sub_u32 s15,s82,7864320\n"
"s_min_u32 s82,s82,s15\n"
TR "a[210:211],v0 offset:16896\n"
MF "v[64:67],a[220:223],a[104:107],v[64:67]\n"
LD "v6,s[28:31],s82 offen lds\n"
MF "v[64:67],a[216:219],a[64:67],v[64:67]\n"
TR "a[212:213],v0 offset:20480\n"
MF "v[72:75],a[216:219],a[68:71],v[72:75]\n"
TR "a[214:215],v0 offset:20992\n"
MF "v[72:75],a[220:223],a[108:111],v[72:75]\n"
"buffer_load_dword a249,v15,s[48:51],s59 offen\n"
MF "v[80:83],a[220:223],a[112:115],v[80:83]\n"
"s_bfe_u32 s59,s23,0x30003\n"
MF "v[80:83],a[216:219],a[72:75],v[80:83]\n"
LD "v11,s[28:31],s82 offen offset:1024 lds\n"
MF "v[88:91],a[216:219],a[76:79],v[88:91]\n"
"s_add_u32 s59,s59,3\n"
MF "v[88:91],a[220:223],a[116:119],v[88:91]\n"
"s_lshl_b32 s59,s59,7\n"
MF "v[92:95],a[204:207],a[116:119],v[92:95]\n"
"s_and_b32 s59,s59,768\n"
TR "a[216:217],v0 offset:16392\n"
MF "v[92:95],a[200:203],a[76:79],v[92:95]\n"
"s_add_u32 s59,s59,s60\n"
TR "a[218:219],v0 offset:16904\n"
MF "v[84:87],a[200:203],a[72:75],v[84:87]\n"
LD "v12,s[28:31],s82 offen offset:2048 lds\n"
MF "v[84:87],a[204:207],a[112:115],v[84:87]\n"
TR "a[220:221],v0 offset:20488\n"
MF "v[76:79],a[204:207],a[108:111],v[76:79]\n"
TR "a[222:223],v0 offset:21000\n"
MF "v[76:79],a[200:203],a[68:71],v[76:79]\n"
MF "v[68:71],a[200:203],a[64:67],v[68:71]\n"
MF "v[68:71],a[204:207],a[104:107],v[68:71]\n"
LD "v13,s[28:31],s82 offen offset:3072 lds\n"
MF "v[60:63],a[204:207],a[100:103],v[60:63]\n"
MF "v[60:63],a[200:203],a[60:63],v[60:63]\n"
"s_add_u32 s14,s81,896\n"
"s_sub_u32 s15,s14,1280\n"
"s_waitcnt lgkmcnt(4)\n"
MF "v[96:99],a[208:211],a[60:63],v[96:99]\n"
"s_min_u32 s14,s14,s15\n"
"s_add_u32 s14,s14,s68\n"
TR "a[200:201],v1 offset:16384\n"
MF "v[96:99],a[212:215],a[100:103],v[96:99]\n"
"s_add_u32 s84,s14,0\n"
"s_add_u32 m0,s63,20480\n"
TR "a[202:203],v1 offset:16896\n"
MF "v[104:107],a[212:215],a[104:107],v[104:107]\n"
LD "v4,s[24:27],s84 offen lds\n"
MF "v[104:107],a[208:211],a[64:67],v[104:107]\n"
TR "a[204:205],v1 offset:20480\n"
MF "v[112:115],a[208:211],a[68:71],v[112:115]\n"
TR "a[206:207],v1 offset:20992\n"
MF "v[112:115],a[212:215],a[108:111],v[112:115]\n"
MF "v[120:123],a[212:215],a[112:115],v[120:123]\n"
"s_add_u32 s84,s14,9216\n"
MF "v[120:123],a[208:211],a[72:75],v[120:123]\n"
LD "v5,s[24:27],s84 offen offset:1024 lds\n"
MF "v[128:131],a[208:211],a[76:79],v[128:131]\n"
MF "v[128:131],a[212:215],a[116:119],v[128:131]\n"
"s_waitcnt lgkmcnt(4)\n"
MF "v[132:135],a[220:223],a[116:119],v[132:135]\n"
TR "a[208:209],v1 offset:16392\n"
MF "v[132:135],a[216:219],a[76:79],v[132:135]\n"
TR "a[210:211],v1 offset:16904\n"
MF "v[124:127],a[216:219],a[72:75],v[124:127]\n"
MF "v[124:127],a[220:223],a[112:115],v[124:127]\n"
TR "a[212:213],v1 offset:20488\n"
MF "v[116:119],a[220:223],a[108:111],v[116:119]\n"
TR "a[214:215],v1 offset:21000\n"
MF "v[116:119],a[216:219],a[68:71],v[116:119]\n"
MF "v[108:111],a[216:219],a[64:67],v[108:111]\n"
MF "v[108:111],a[220:223],a[104:107],v[108:111]\n"
MF "v[100:103],a[220:223],a[100:103],v[100:103]\n"
"s_waitcnt vmcnt(25) lgkmcnt(0)\n"
MF "v[100:103],a[216:219],a[60:63],v[100:103]\n"
"s_barrier\n"
MF "v[136:139],a[200:203],a[60:63],v[136:139]\n"
TR "a[216:217],v0 offset:32768\n"
MF "v[136:139],a[204:207],a[100:103],v[136:139]\n"
"s_add_u32 m0,s62,16384\n"
TR "a[218:219],v0 offset:33280\n"
MF "v[144:147],a[204:207],a[104:107],v[144:147]\n"
"s_add_u32 s82,s66,4718720\n"
"ds_read_b128 a[140:143],v2 offset:40960\n"
MF "v[144:147],a[200:203],a[64:67],v[144:147]\n"
"s_sub_u32 s15,s82,7864320\n"
TR "a[220:221],v0 offset:36864\n"
MF "v[152:155],a[200:203],a[68:71],v[152:155]\n"
"s_min_u32 s82,s82,s15\n"
TR "a[222:223],v0 offset:37376\n"
MF "v[152:155],a[204:207],a[108:111],v[152:155]\n"
LD "v6,s[28:31],s82 offen lds\n"
MF "v[160:163],a[204:207],a[112:115],v[160:163]\n"
"ds_read_b128 a[180:183],v3 offset:40960\n"
MF "v[160:163],a[200:203],a[72:75],v[160:163]\n"
MF "v[168:171],a[200:203],a[76:79],v[168:171]\n"
MF "v[168:171],a[204:207],a[116:119],v[168:171]\n"
LD "v11,s[28:31],s82 offen offset:1024 lds\n"
MF "v[172:175],a[212:215],a[116:119],v[172:175]\n"
TR "a[200:201],v0 offset:32776\n"
MF "v[172:175],a[208:211],a[76:79],v[172:175]\n"
TR "a[202:203],v0 offset:33288\n"
MF "v[164:167],a[208:211],a[72:75],v[164:167]\n"
"ds_read_b128 a[144:147],v2 offset:43008\n"
MF "v[164:167],a[212:215],a[112:115],v[164:167]\n"
TR "a[204:205],v0 offset:36872\n"
MF "v[156:159],a[212:215],a[108:111],v[156:159]\n"
TR "a[206:207],v0 offset:37384\n"
MF "v[156:159],a[208:211],a[68:71],v[156:159]\n"
LD "v12,s[28:31],s82 offen offset:2048 lds\n"
MF "v[148:151],a[208:211],a[64:67],v[148:151]\n"
"ds_read_b128 a[184:187],v3 offset:43008\n"
MF "v[148:151],a[212:215],a[104:107],v[148:151]\n"
MF "v[140:143],a[212:215],a[100:103],v[140:143]\n"
MF "v[140:143],a[208:211],a[60:63],v[140:143]\n"
"s_waitcnt lgkmcnt(7)\n"
LD "v13,s[28:31],s82 offen offset:3072 lds\n"
MF "v[176:179],a[216:219],a[60:63],v[176:179]\n"
TR "a[208:209],v1 offset:32768\n"
MF "v[176:179],a[220:223],a[100:103],v[176:179]\n"
TR "a[210:211],v1 offset:33280\n"
MF "v[184:187],a[220:223],a[104:107],v[184:187]\n"
"ds_read_b128 a[148:151],v2 offset:45056\n"
MF "v[184:187],a[216:219],a[64:67],v[184:187]\n"
"s_add_u32 s84,s14,20480\n"
TR "a[212:213],v1 offset:36864\n"
MF "v[192:195],a[216:219],a[68:71],v[192:195]\n"
"s_add_u32 m0,s63,22528\n"
TR "a[214:215],v1 offset:37376\n"
MF "v[192:195],a[220:223],a[108:111],v[192:195]\n"
LD "v4,s[24:27],s84 offen lds\n"
MF "v[200:203],a[220:223],a[112:115],v[200:203]\n"
"ds_read_b128 a[188:191],v3 offset:45056\n"
MF "v[200:203],a[216:219],a[72:75],v[200:203]\n"
MF "v[208:211],a[216:219],a[76:79],v[208:211]\n"
"s_add_u32 s84,s14,29696\n"
MF "v[208:211],a[220:223],a[116:119],v[208:211]\n"
"s_waitcnt lgkmcnt(7)\n"
LD "v5,s[24:27],s84 offen offset:1024 lds\n"
MF "v[212:215],a[204:207],a[116:119],v[212:215]\n"
TR "a[216:217],v1 offset:32776\n"
MF "v[212:215],a[200:203],a[76:79],v[212:215]\n"
TR "a[218:219],v1 offset:33288\n"
MF "v[204:207],a[200:203],a[72:75],v[204:207]\n"
"ds_read_b128 a[152:155],v2 offset:47104\n"
MF "v[204:207],a[204:207],a[112:115],v[204:207]\n"
TR "a[220:221],v1 offset:36872\n"
MF "v[196:199],a[204:207],a[108:111],v[196:199]\n"
TR "a[222:223],v1 offset:37384\n"
MF "v[196:199],a[200:203],a[68:71],v[196:199]\n"
MF "v[188:191],a[200:203],a[64:67],v[188:191]\n"
"v_add_u32 v0,0xc000,v0\n"
MF "v[188:191],a[204:207],a[104:107],v[188:191]\n"
"v_add_u32 v1,0xc000,v1\n"
MF "v[180:183],a[204:207],a[100:103],v[180:183]\n"
"s_waitcnt vmcnt(26) lgkmcnt(0)\n"
MF "v[180:183],a[200:203],a[60:63],v[180:183]\n"
"s_barrier\n"
MF "v[216:219],a[208:211],a[60:63],v[216:219]\n"
TR "a[200:201],v0\n"
MF "v[216:219],a[212:215],a[100:103],v[216:219]\n"
"s_add_u32 m0,s62,32768\n"
TR "a[202:203],v0 offset:512\n"
MF "v[224:227],a[212:215],a[104:107],v[224:227]\n"
"s_add_u32 s82,s66,4718848\n"
"ds_read_b128 a[192:195],v3 offset:47104\n"
MF "v[224:227],a[208:211],a[64:67],v[224:227]\n"
"s_sub_u32 s15,s82,7864320\n"
TR "a[204:205],v0 offset:4096\n"
MF "v[232:235],a[208:211],a[68:71],v[232:235]\n"
"s_min_u32 s82,s82,s15\n"
TR "a[206:207],v0 offset:4608\n"
MF "v[232:235],a[212:215],a[108:111],v[232:235]\n"
LD "v6,s[28:31],s82 offen lds\n"
MF "v[240:243],a[212:215],a[112:115],v[240:243]\n"
"ds_read_b128 a[156:159],v2 offset:49152\n"
MF "v[240:243],a[208:211],a[72:75],v[240:243]\n"
"ds_read_b128 a[196:199],v3 offset:49152\n"
MF "v[248:251],a[208:211],a[76:79],v[248:251]\n"
MF "v[248:251],a[212:215],a[116:119],v[248:251]\n"
LD "v11,s[28:31],s82 offen offset:1024 lds\n"
MF "v[252:255],a[220:223],a[116:119],v[252:255]\n"
TR "a[208:209],v0 offset:8\n"
MF "v[252:255],a[216:219],a[76:79],v[252:255]\n"
TR "a[210:211],v0 offset:520\n"
MF "v[244:247],a[216:219],a[72:75],v[244:247]\n"
MF "v[244:247],a[220:223],a[112:115],v[244:247]\n"
TR "a[212:213],v0 offset:4104\n"
MF "v[236:239],a[220:223],a[108:111],v[236:239]\n"
TR "a[214:215],v0 offset:4616\n"
MF "v[236:239],a[216:219],a[68:71],v[236:239]\n"
LD "v12,s[28:31],s82 offen offset:2048 lds\n"
MF "v[228:231],a[216:219],a[64:67],v[228:231]\n"
MF "v[228:231],a[220:223],a[104:107],v[228:231]\n"
MF "v[220:223],a[220:223],a[100:103],v[220:223]\n"
MF "v[220:223],a[216:219],a[60:63],v[220:223]\n"
"s_waitcnt lgkmcnt(4)\n"
LD "v13,s[28:31],s82 offen offset:3072 lds\n"
MF "v[16:19],a[200:203],a[140:143],v[16:19]\n"
TR "a[216:217],v1\n"
MF "v[16:19],a[204:207],a[180:183],v[16:19]\n"
TR "a[218:219],v1 offset:512\n"
MF "v[24:27],a[204:207],a[184:187],v[24:27]\n"
MF "v[24:27],a[200:203],a[144:147],v[24:27]\n"
"s_add_u32 s84,s14,40960\n"
TR "a[220:221],v1 offset:4096\n"
MF "v[32:35],a[200:203],a[148:151],v[32:35]\n"
"s_add_u32 m0,s63,24576\n"
TR "a[222:223],v1 offset:4608\n"
MF "v[32:35],a[204:207],a[188:191],v[32:35]\n"
LD "v4,s[24:27],s84 offen lds\n"
MF "v[40:43],a[204:207],a[192:195],v[40:43]\n"
MF "v[40:43],a[200:203],a[152:155],v[40:43]\n"
MF "v[48:51],a[200:203],a[156:159],v[48:51]\n"
MF "v[48:51],a[204:207],a[196:199],v[48:51]\n"
"s_waitcnt lgkmcnt(4)\n"
MF "v[52:55],a[212:215],a[196:199],v[52:55]\n"
TR "a[200:201],v1 offset:8\n"
MF "v[52:55],a[208:211],a[156:159],v[52:55]\n"
TR "a[202:203],v1 offset:520\n"
MF "v[44:47],a[208:211],a[152:155],v[44:47]\n"
MF "v[44:47],a[212:215],a[192:195],v[44:47]\n"
TR "a[204:205],v1 offset:4104\n"
MF "v[36:39],a[212:215],a[188:191],v[36:39]\n"
TR "a[206:207],v1 offset:4616\n"
MF "v[36:39],a[208:211],a[148:151],v[36:39]\n"
MF "v[28:31],a[208:211],a[144:147],v[28:31]\n"
MF "v[28:31],a[212:215],a[184:187],v[28:31]\n"
MF "v[20:23],a[212:215],a[180:183],v[20:23]\n"
"s_waitcnt vmcnt(25) lgkmcnt(0)\n"
MF "v[20:23],a[208:211],a[140:143],v[20:23]\n"
"s_barrier\n"
MF "v[56:59],a[216:219],a[140:143],v[56:59]\n"
"s_add_u32 m0,s62,49152\n"
"s_add_u32 s82,s66,5505024\n"
TR "a[208:209],v0 offset:16384\n"
MF "v[56:59],a[220:223],a[180:183],v[56:59]\n"
"s_sub_u32 s15,s82,7864320\n"
"s_min_u32 s82,s82,s15\n"
TR "a[210:211],v0 offset:16896\n"
MF "v[64:67],a[220:223],a[184:187],v[64:67]\n"
LD "v6,s[28:31],s82 offen lds\n"
MF "v[64:67],a[216:219],a[144:147],v[64:67]\n"
TR "a[212:213],v0 offset:20480\n"
MF "v[72:75],a[216:219],a[148:151],v[72:75]\n"
TR "a[214:215],v0 offset:20992\n"
MF "v[72:75],a[220:223],a[188:191],v[72:75]\n"
"buffer_load_dword a249,v15,s[48:51],s59 offen\n"
MF "v[80:83],a[220:223],a[192:195],v[80:83]\n"
"s_bfe_u32 s59,s23,0x30003\n"
MF "v[80:83],a[216:219],a[152:155],v[80:83]\n"
LD "v11,s[28:31],s82 offen offset:1024 lds\n"
MF "v[88:91],a[216:219],a[156:159],v[88:91]\n"
"s_add_u32 s59,s59,5\n"
MF "v[88:91],a[220:223],a[196:199],v[88:91]\n"
"s_lshl_b32 s59,s59,7\n"
MF "v[92:95],a[204:207],a[196:199],v[92:95]\n"
"s_and_b32 s59,s59,768\n"
TR "a[216:217],v0 offset:16392\n"
MF "v[92:95],a[200:203],a[156:159],v[92:95]\n"
"s_add_u32 s59,s59,s60\n"
TR "a[218:219],v0 offset:16904\n"
MF "v[84:87],a[200:203],a[152:155],v[84:87]\n"
LD "v12,s[28:31],s82 offen offset:2048 lds\n"
MF "v[84:87],a[204:207],a[192:195],v[84:87]\n"
TR "a[220:221],v0 offset:20488\n"
MF "v[76:79],a[204:207],a[188:191],v[76:79]\n"
TR "a[222:223],v0 offset:21000\n"
MF "v[76:79],a[200:203],a[148:151],v[76:79]\n"
MF "v[68:71],a[200:203],a[144:147],v[68:71]\n"
MF "v[68:71],a[204:207],a[184:187],v[68:71]\n"
".Lpg3a:\n"
"s_cbranch_execz .Lisr1\n"
LD "v13,s[28:31],s82 offen offset:3072 lds\n"
MF "v[60:63],a[204:207],a[180:183],v[60:63]\n"
MF "v[60:63],a[200:203],a[140:143],v[60:63]\n"
"s_add_u32 s14,s81,1024\n"
"s_sub_u32 s15,s14,1280\n"
"s_waitcnt lgkmcnt(4)\n"
MF "v[96:99],a[208:211],a[140:143],v[96:99]\n"
"s_min_u32 s14,s14,s15\n"
"s_add_u32 s14,s14,s68\n"
TR "a[200:201],v1 offset:16384\n"
MF "v[96:99],a[212:215],a[180:183],v[96:99]\n"
"s_add_u32 s84,s14,0\n"
"s_add_u32 m0,s63,40960\n"
TR "a[202:203],v1 offset:16896\n"
MF "v[104:107],a[212:215],a[184:187],v[104:107]\n"
LD "v4,s[24:27],s84 offen lds\n"
MF "v[104:107],a[208:211],a[144:147],v[104:107]\n"
TR "a[204:205],v1 offset:20480\n"
MF "v[112:115],a[208:211],a[148:151],v[112:115]\n"
TR "a[206:207],v1 offset:20992\n"
MF "v[112:115],a[212:215],a[188:191],v[112:115]\n"
MF "v[120:123],a[212:215],a[192:195],v[120:123]\n"
"s_add_u32 s84,s14,9216\n"
MF "v[120:123],a[208:211],a[152:155],v[120:123]\n"
LD "v5,s[24:27],s84 offen offset:1024 lds\n"
MF "v[128:131],a[208:211],a[156:159],v[128:131]\n"
MF "v[128:131],a[212:215],a[196:199],v[128:131]\n"
"s_waitcnt lgkmcnt(4)\n"
MF "v[132:135],a[220:223],a[196:199],v[132:135]\n"
TR "a[208:209],v1 offset:16392\n"
MF "v[132:135],a[216:219],a[156:159],v[132:135]\n"
TR "a[210:211],v1 offset:16904\n"
MF "v[124:127],a[216:219],a[152:155],v[124:127]\n"
MF "v[124:127],a[220:223],a[192:195],v[124:127]\n"
TR "a[212:213],v1 offset:20488\n"
MF "v[116:119],a[220:223],a[188:191],v[116:119]\n"
TR "a[214:215],v1 offset:21000\n"
MF "v[116:119],a[216:219],a[148:151],v[116:119]\n"
MF "v[108:111],a[216:219],a[144:147],v[108:111]\n"
MF "v[108:111],a[220:223],a[184:187],v[108:111]\n"
MF "v[100:103],a[220:223],a[180:183],v[100:103]\n"
"s_waitcnt vmcnt(25) lgkmcnt(0)\n"
MF "v[100:103],a[216:219],a[140:143],v[100:103]\n"
"s_barrier\n"
MF "v[136:139],a[200:203],a[140:143],v[136:139]\n"
TR "a[216:217],v0 offset:32768\n"
MF "v[136:139],a[204:207],a[180:183],v[136:139]\n"
"s_add_u32 m0,s62,65536\n"
TR "a[218:219],v0 offset:33280\n"
MF "v[144:147],a[204:207],a[184:187],v[144:147]\n"
"s_add_u32 s82,s66,5505152\n"
"ds_read_b128 a[60:63],v2\n"
MF "v[144:147],a[200:203],a[144:147],v[144:147]\n"
"s_sub_u32 s15,s82,7864320\n"
TR "a[220:221],v0 offset:36864\n"
MF "v[152:155],a[200:203],a[148:151],v[152:155]\n"
"s_min_u32 s82,s82,s15\n"
TR "a[222:223],v0 offset:37376\n"
MF "v[152:155],a[204:207],a[188:191],v[152:155]\n"
LD "v6,s[28:31],s82 offen lds\n"
MF "v[160:163],a[204:207],a[192:195],v[160:163]\n"
"ds_read_b128 a[100:103],v3\n"
MF "v[160:163],a[200:203],a[152:155],v[160:163]\n"
MF "v[168:171],a[200:203],a[156:159],v[168:171]\n"
MF "v[168:171],a[204:207],a[196:199],v[168:171]\n"
LD "v11,s[28:31],s82 offen offset:1024 lds\n"
MF "v[172:175],a[212:215],a[196:199],v[172:175]\n"
TR "a[200:201],v0 offset:32776\n"
MF "v[172:175],a[208:211],a[156:159],v[172:175]\n"
TR "a[202:203],v0 offset:33288\n"
MF "v[164:167],a[208:211],a[152:155],v[164:167]\n"
"ds_read_b128 a[64:67],v2 offset:2048\n"
MF "v[164:167],a[212:215],a[192:195],v[164:167]\n"
TR "a[204:205],v0 offset:36872\n"
MF "v[156:159],a[212:215],a[188:191],v[156:159]\n"
TR "a[206:207],v0 offset:37384\n"
MF "v[156:159],a[208:211],a[148:151],v[156:159]\n"
LD "v12,s[28:31],s82 offen offset:2048 lds\n"
MF "v[148:151],a[208:211],a[144:147],v[148:151]\n"
"ds_read_b128 a[104:107],v3 offset:2048\n"
MF "v[148:151],a[212:215],a[184:187],v[148:151]\n"
MF "v[140:143],a[212:215],a[180:183],v[140:143]\n"
MF "v[140:143],a[208:211],a[140:143],v[140:143]\n"
"s_waitcnt lgkmcnt(7)\n"
LD "v13,s[28:31],s82 offen offset:3072 lds\n"
MF "v[176:179],a[216:219],a[140:143],v[176:179]\n"
TR "a[208:209],v1 offset:32768\n"
MF "v[176:179],a[220:223],a[180:183],v[176:179]\n"
TR "a[210:211],v1 offset:33280\n"
MF "v[184:187],a[220:223],a[184:187],v[184:187]\n"
"ds_read_b128 a[68:71],v2 offset:4096\n"
MF "v[184:187],a[216:219],a[144:147],v[184:187]\n"
"s_add_u32 s84,s14,20480\n"
TR "a[212:213],v1 offset:36864\n"
MF "v[192:195],a[216:219],a[148:151],v[192:195]\n"
"s_add_u32 m0,s63,43008\n"
TR "a[214:215],v1 offset:37376\n"
MF "v[192:195],a[220:223],a[188:191],v[192:195]\n"
LD "v4,s[24:27],s84 offen lds\n"
MF "v[200:203],a[220:223],a[192:195],v[200:203]\n"
"ds_read_b128 a[108:111],v3 offset:4096\n"
MF "v[200:203],a[216:219],a[152:155],v[200:203]\n"
MF "v[208:211],a[216:219],a[156:159],v[208:211]\n"
"s_add_u32 s84,s14,29696\n"
MF "v[208:211],a[220:223],a[196:199],v[208:211]\n"
"s_waitcnt lgkmcnt(7)\n"
LD "v5,s[24:27],s84 offen offset:1024 lds\n"
MF "v[212:215],a[204:207],a[196:199],v[212:215]\n"
TR "a[216:217],v1 offset:32776\n"
MF "v[212:215],a[200:203],a[156:159],v[212:215]\n"
TR "a[218:219],v1 offset:33288\n"
MF "v[204:207],a[200:203],a[152:155],v[204:207]\n"
"ds_read_b128 a[72:75],v2 offset:6144\n"
MF "v[204:207],a[204:207],a[192:195],v[204:207]\n"
TR "a[220:221],v1 offset:36872\n"
MF "v[196:199],a[204:207],a[188:191],v[196:199]\n"
TR "a[222:223],v1 offset:37384\n"
MF "v[196:199],a[200:203],a[148:151],v[196:199]\n"
MF "v[188:191],a[200:203],a[144:147],v[188:191]\n"
"v_add_u32 v0,0xffff4000,v0\n"
MF "v[188:191],a[204:207],a[184:187],v[188:191]\n"
"v_add_u32 v1,0xffff4000,v1\n"
MF "v[180:183],a[204:207],a[180:183],v[180:183]\n"
"s_waitcnt vmcnt(26) lgkmcnt(0)\n"
MF "v[180:183],a[200:203],a[140:143],v[180:183]\n"
"s_barrier\n"
MF "v[216:219],a[208:211],a[140:143],v[216:219]\n"
TR "a[200:201],v0\n"
MF "v[216:219],a[212:215],a[180:183],v[216:219]\n"
"s_add_u32 m0,s62,81920\n"
TR "a[202:203],v0 offset:512\n"
MF "v[224:227],a[212:215],a[184:187],v[224:227]\n"
"s_add_u32 s82,s66,5505280\n"
"ds_read_b128 a[112:115],v3 offset:6144\n"
MF "v[224:227],a[208:211],a[144:147],v[224:227]\n"
"s_sub_u32 s15,s82,7864320\n"
TR "a[204:205],v0 offset:4096\n"
MF "v[232:235],a[208:211],a[148:151],v[232:235]\n"
"s_min_u32 s82,s82,s15\n"
TR "a[206:207],v0 offset:4608\n"
MF "v[232:235],a[212:215],a[188:191],v[232:235]\n"
LD "v6,s[28:31],s82 offen lds\n"
MF "v[240:243],a[212:215],a[192:195],v[240:243]\n"
"ds_read_b128 a[76:79],v2 offset:8192\n"
MF "v[240:243],a[208:211],a[152:155],v[240:243]\n"
"ds_read_b128 a[116:119],v3 offset:8192\n"
MF "v[248:251],a[208:211],a[156:159],v[248:251]\n"
MF "v[248:251],a[212:215],a[196:199],v[248:251]\n"
LD "v11,s[28:31],s82 offen offset:1024 lds\n"
MF "v[252:255],a[220:223],a[196:199],v[252:255]\n"
TR "a[208:209],v0 offset:8\n"
MF "v[252:255],a[216:219],a[156:159],v[252:255]\n"
TR "a[210:211],v0 offset:520\n"
MF "v[244:247],a[216:219],a[152:155],v[244:247]\n"
MF "v[244:247],a[220:223],a[192:195],v[244:247]\n"
TR "a[212:213],v0 offset:4104\n"
MF "v[236:239],a[220:223],a[188:191],v[236:239]\n"
TR "a[214:215],v0 offset:4616\n"
MF "v[236:239],a[216:219],a[148:151],v[236:239]\n"
LD "v12,s[28:31],s82 offen offset:2048 lds\n"
MF "v[228:231],a[216:219],a[144:147],v[228:231]\n"
MF "v[228:231],a[220:223],a[184:187],v[228:231]\n"
MF "v[220:223],a[220:223],a[180:183],v[220:223]\n"
MF "v[220:223],a[216:219],a[140:143],v[220:223]\n"
"s_waitcnt lgkmcnt(4)\n"
LD "v13,s[28:31],s82 offen offset:3072 lds\n"
MF "v[16:19],a[200:203],a[60:63],v[16:19]\n"
TR "a[216:217],v1\n"
MF "v[16:19],a[204:207],a[100:103],v[16:19]\n"
TR "a[218:219],v1 offset:512\n"
MF "v[24:27],a[204:207],a[104:107],v[24:27]\n"
MF "v[24:27],a[200:203],a[64:67],v[24:27]\n"
"s_add_u32 s84,s14,40960\n"
TR "a[220:221],v1 offset:4096\n"
MF "v[32:35],a[200:203],a[68:71],v[32:35]\n"
"s_add_u32 m0,s63,45056\n"
TR "a[222:223],v1 offset:4608\n"
MF "v[32:35],a[204:207],a[108:111],v[32:35]\n"
LD "v4,s[24:27],s84 offen lds\n"
MF "v[40:43],a[204:207],a[112:115],v[40:43]\n"
MF "v[40:43],a[200:203],a[72:75],v[40:43]\n"
MF "v[48:51],a[200:203],a[76:79],v[48:51]\n"
MF "v[48:51],a[204:207],a[116:119],v[48:51]\n"
"s_waitcnt lgkmcnt(4)\n"
MF "v[52:55],a[212:215],a[116:119],v[52:55]\n"
TR "a[200:201],v1 offset:8\n"
MF "v[52:55],a[208:211],a[76:79],v[52:55]\n"
TR "a[202:203],v1 offset:520\n"
MF "v[44:47],a[208:211],a[72:75],v[44:47]\n"
MF "v[44:47],a[212:215],a[112:115],v[44:47]\n"
TR "a[204:205],v1 offset:4104\n"
MF "v[36:39],a[212:215],a[108:111],v[36:39]\n"
TR "a[206:207],v1 offset:4616\n"
MF "v[36:39],a[208:211],a[68:71],v[36:39]\n"
MF "v[28:31],a[208:211],a[64:67],v[28:31]\n"
MF "v[28:31],a[212:215],a[104:107],v[28:31]\n"
MF "v[20:23],a[212:215],a[100:103],v[20:23]\n"
"s_waitcnt vmcnt(25) lgkmcnt(0)\n"
MF "v[20:23],a[208:211],a[60:63],v[20:23]\n"
"s_barrier\n"
MF "v[56:59],a[216:219],a[60:63],v[56:59]\n"
"s_add_u32 m0,s62,0\n"
"s_add_u32 s82,s66,6291712\n"
TR "a[208:209],v0 offset:16384\n"
MF "v[56:59],a[220:223],a[100:103],v[56:59]\n"
"s_sub_u32 s15,s82,7864320\n"
"s_min_u32 s82,s82,s15\n"
TR "a[210:211],v0 offset:16896\n"
MF "v[64:67],a[220:223],a[104:107],v[64:67]\n"
LD "v6,s[28:31],s82 offen lds\n"
MF "v[64:67],a[216:219],a[64:67],v[64:67]\n"
TR "a[212:213],v0 offset:20480\n"
MF "v[72:75],a[216:219],a[68:71],v[72:75]\n"
TR "a[214:215],v0 offset:20992\n"
MF "v[72:75],a[220:223],a[108:111],v[72:75]\n"
"buffer_load_dword a249,v15,s[48:51],s59 offen\n"
MF "v[80:83],a[220:223],a[112:115],v[80:83]\n"
MF "v[80:83],a[216:219],a[72:75],v[80:83]\n"
LD "v11,s[28:31],s82 offen offset:1024 lds\n"
MF "v[88:91],a[216:219],a[76:79],v[88:91]\n"
MF "v[88:91],a[220:223],a[116:119],v[88:91]\n"
MF "v[92:95],a[204:207],a[116:119],v[92:95]\n"
TR "a[216:217],v0 offset:16392\n"
MF "v[92:95],a[200:203],a[76:79],v[92:95]\n"
TR "a[218:219],v0 offset:16904\n"
MF "v[84:87],a[200:203],a[72:75],v[84:87]\n"
LD "v12,s[28:31],s82 offen offset:2048 lds\n"
MF "v[84:87],a[204:207],a[112:115],v[84:87]\n"
TR "a[220:221],v0 offset:20488\n"
MF "v[76:79],a[204:207],a[108:111],v[76:79]\n"
TR "a[222:223],v0 offset:21000\n"
MF "v[76:79],a[200:203],a[68:71],v[76:79]\n"
MF "v[68:71],a[200:203],a[64:67],v[68:71]\n"
MF "v[68:71],a[204:207],a[104:107],v[68:71]\n"
LD "v13,s[28:31],s82 offen offset:3072 lds\n"
MF "v[60:63],a[204:207],a[100:103],v[60:63]\n"
MF "v[60:63],a[200:203],a[60:63],v[60:63]\n"
"s_add_u32 s14,s81,1152\n"
"s_sub_u32 s15,s14,1280\n"
"s_waitcnt lgkmcnt(4)\n"
MF "v[96:99],a[208:211],a[60:63],v[96:99]\n"
"s_min_u32 s14,s14,s15\n"
"s_add_u32 s14,s14,s68\n"
TR "a[200:201],v1 offset:16384\n"
MF "v[96:99],a[212:215],a[100:103],v[96:99]\n"
"s_add_u32 s84,s14,0\n"
"s_add_u32 m0,s63,0\n"
TR "a[202:203],v1 offset:16896\n"
MF "v[104:107],a[212:215],a[104:107],v[104:107]\n"
LD "v4,s[24:27],s84 offen lds\n"
MF "v[104:107],a[208:211],a[64:67],v[104:107]\n"
TR "a[204:205],v1 offset:20480\n"
MF "v[112:115],a[208:211],a[68:71],v[112:115]\n"
TR "a[206:207],v1 offset:20992\n"
MF "v[112:115],a[212:215],a[108:111],v[112:115]\n"
MF "v[120:123],a[212:215],a[112:115],v[120:123]\n"
"s_add_u32 s84,s14,9216\n"
MF "v[120:123],a[208:211],a[72:75],v[120:123]\n"
LD "v5,s[24:27],s84 offen offset:1024 lds\n"
MF "v[128:131],a[208:211],a[76:79],v[128:131]\n"
MF "v[128:131],a[212:215],a[116:119],v[128:131]\n"
"s_waitcnt lgkmcnt(4)\n"
MF "v[132:135],a[220:223],a[116:119],v[132:135]\n"
TR "a[208:209],v1 offset:16392\n"
MF "v[132:135],a[216:219],a[76:79],v[132:135]\n"
"s_add_u32 s84,s14,18432\n"
TR "a[210:211],v1 offset:16904\n"
MF "v[124:127],a[216:219],a[72:75],v[124:127]\n"
LD "v4,s[24:27],s84 offen offset:2048 lds\n"
MF "v[124:127],a[220:223],a[112:115],v[124:127]\n"
TR "a[212:213],v1 offset:20488\n"
MF "v[116:119],a[220:223],a[108:111],v[116:119]\n"
TR "a[214:215],v1 offset:21000\n"
MF "v[116:119],a[216:219],a[68:71],v[116:119]\n"
MF "v[108:111],a[216:219],a[64:67],v[108:111]\n"
MF "v[108:111],a[220:223],a[104:107],v[108:111]\n"
MF "v[100:103],a[220:223],a[100:103],v[100:103]\n"
"s_waitcnt vmcnt(26) lgkmcnt(0)\n"
MF "v[100:103],a[216:219],a[60:63],v[100:103]\n"
"s_barrier\n"
MF "v[136:139],a[200:203],a[60:63],v[136:139]\n"
TR "a[216:217],v0 offset:32768\n"
MF "v[136:139],a[204:207],a[100:103],v[136:139]\n"
"s_add_u32 m0,s62,16384\n"
TR "a[218:219],v0 offset:33280\n"
MF "v[144:147],a[204:207],a[104:107],v[144:147]\n"
"s_add_u32 s82,s66,7078144\n"
"ds_read_b128 a[140:143],v2 offset:20480\n"
MF "v[144:147],a[200:203],a[64:67],v[144:147]\n"
"s_sub_u32 s15,s82,7864320\n"
TR "a[220:221],v0 offset:36864\n"
MF "v[152:155],a[200:203],a[68:71],v[152:155]\n"
"s_min_u32 s82,s82,s15\n"
TR "a[222:223],v0 offset:37376\n"
MF "v[152:155],a[204:207],a[108:111],v[152:155]\n"
LD "v6,s[28:31],s82 offen lds\n"
MF "v[160:163],a[204:207],a[112:115],v[160:163]\n"
"ds_read_b128 a[180:183],v3 offset:20480\n"
MF "v[160:163],a[200:203],a[72:75],v[160:163]\n"
MF "v[168:171],a[200:203],a[76:79],v[168:171]\n"
MF "v[168:171],a[204:207],a[116:119],v[168:171]\n"
LD "v11,s[28:31],s82 offen offset:1024 lds\n"
MF "v[172:175],a[212:215],a[116:119],v[172:175]\n"
TR "a[200:201],v0 offset:32776\n"
MF "v[172:175],a[208:211],a[76:79],v[172:175]\n"
TR "a[202:203],v0 offset:33288\n"
MF "v[164:167],a[208:211],a[72:75],v[164:167]\n"
"ds_read_b128 a[144:147],v2 offset:22528\n"
MF "v[164:167],a[212:215],a[112:115],v[164:167]\n"
TR "a[204:205],v0 offset:36872\n"
MF "v[156:159],a[212:215],a[108:111],v[156:159]\n"
TR "a[206:207],v0 offset:37384\n"
MF "v[156:159],a[208:211],a[68:71],v[156:159]\n"
LD "v12,s[28:31],s82 offen offset:2048 lds\n"
MF "v[148:151],a[208:211],a[64:67],v[148:151]\n"
"ds_read_b128 a[184:187],v3 offset:22528\n"
MF "v[148:151],a[212:215],a[104:107],v[148:151]\n"
MF "v[140:143],a[212:215],a[100:103],v[140:143]\n"
MF "v[140:143],a[208:211],a[60:63],v[140:143]\n"
"s_waitcnt lgkmcnt(7)\n"
LD "v13,s[28:31],s82 offen offset:3072 lds\n"
MF "v[176:179],a[216:219],a[60:63],v[176:179]\n"
TR "a[208:209],v1 offset:32768\n"
MF "v[176:179],a[220:223],a[100:103],v[176:179]\n"
TR "a[210:211],v1 offset:33280\n"
MF "v[184:187],a[220:223],a[104:107],v[184:187]\n"
"ds_read_b128 a[148:151],v2 offset:24576\n"
MF "v[184:187],a[216:219],a[64:67],v[184:187]\n"
"s_add_u32 s84,s14,30720\n"
TR "a[212:213],v1 offset:36864\n"
MF "v[192:195],a[216:219],a[68:71],v[192:195]\n"
"s_add_u32 m0,s63,3072\n"
TR "a[214:215],v1 offset:37376\n"
MF "v[192:195],a[220:223],a[108:111],v[192:195]\n"
LD "v5,s[24:27],s84 offen lds\n"
MF "v[200:203],a[220:223],a[112:115],v[200:203]\n"
"ds_read_b128 a[188:191],v3 offset:24576\n"
MF "v[200:203],a[216:219],a[72:75],v[200:203]\n"
MF "v[208:211],a[216:219],a[76:79],v[208:211]\n"
"s_add_u32 s84,s14,39936\n"
MF "v[208:211],a[220:223],a[116:119],v[208:211]\n"
"s_waitcnt lgkmcnt(7)\n"
LD "v4,s[24:27],s84 offen offset:1024 lds\n"
MF "v[212:215],a[204:207],a[116:119],v[212:215]\n"
TR "a[216:217],v1 offset:32776\n"
MF "v[212:215],a[200:203],a[76:79],v[212:215]\n"
TR "a[218:219],v1 offset:33288\n"
MF "v[204:207],a[200:203],a[72:75],v[204:207]\n"
"ds_read_b128 a[152:155],v2 offset:26624\n"
MF "v[204:207],a[204:207],a[112:115],v[204:207]\n"
TR "a[220:221],v1 offset:36872\n"
MF "v[196:199],a[204:207],a[108:111],v[196:199]\n"
TR "a[222:223],v1 offset:37384\n"
MF "v[196:199],a[200:203],a[68:71],v[196:199]\n"
MF "v[188:191],a[200:203],a[64:67],v[188:191]\n"
"v_add_u32 v0,0xc000,v0\n"
MF "v[188:191],a[204:207],a[104:107],v[188:191]\n"
"v_add_u32 v1,0xc000,v1\n"
MF "v[180:183],a[204:207],a[100:103],v[180:183]\n"
"s_waitcnt vmcnt(27) lgkmcnt(0)\n"
"s_branch .Lqj\n"
".Lpgq0_c:\n"
"s_cbranch_execz .Lpgqy_c\n"
".Lpgq0_f:\n"
"s_cbranch_execz .Lpg12f\n"
".Lq0:\n"
MF "v[16:19],a[200:203],a[60:63],0\n"
"s_add_u32 m0,s62,49152\n"
"s_add_u32 s82,s66,786432\n"
TR "a[216:217],v1\n"
MF "v[16:19],a[204:207],a[100:103],v[16:19]\n"
"s_sub_u32 s15,s82,7864320\n"
"s_min_u32 s82,s82,s15\n"
TR "a[218:219],v1 offset:512\n"
MF "v[24:27],a[204:207],a[104:107],0\n"
LD "v6,s[28:31],s82 offen lds\n"
MF "v[24:27],a[200:203],a[64:67],v[24:27]\n"
TR "a[220:221],v1 offset:4096\n"
MF "v[32:35],a[200:203],a[68:71],0\n"
TR "a[222:223],v1 offset:4608\n"
MF "v[32:35],a[204:207],a[108:111],v[32:35]\n"
"v_max3_f32 v220,|v16|,|v17|,|v18|\n"
"v_max_f32_e64 v220,v220,|v19|\n"
MF "v[40:43],a[204:207],a[112:115],0\n"
"v_readlane_b32 s12,v220,3\n"
"v_readlane_b32 s6,v220,22\n"
MF "v[40:43],a[200:203],a[72:75],v[40:43]\n"
"s_min_u32 s12,s12,s6\n"
"v_readlane_b32 s7,v220,41\n"
"v_readlane_b32 s8,v220,60\n"
MF "v[48:51],a[200:203],a[76:79],0\n"
"s_min_u32 s7,s7,s8\n"
MF "v[48:51],a[204:207],a[116:119],v[48:51]\n"
LD "v11,s[28:31],s82 offen offset:1024 lds\n"
MF "v[52:55],a[212:215],a[116:119],0\n"
"s_min_u32 s12,s12,s7\n"
TR "a[200:201],v1 offset:8\n"
MF "v[52:55],a[208:211],a[76:79],v[52:55]\n"
"s_and_b32 s12,s12,0x7f800000\n"
TR "a[202:203],v1 offset:520\n"
MF "v[44:47],a[208:211],a[72:75],0\n"
"s_sub_u32 s12,s12,0x14000000\n"
MF "v[44:47],a[212:215],a[112:115],v[44:47]\n"
"s_cmp_lt_u32 s12,0x50000000\n"
"s_cselect_b32 s12,s12,0xe8400000\n"
TR "a[204:205],v1 offset:4104\n"
MF "v[36:39],a[212:215],a[108:111],0\n"
"s_add_u32 s12,s12,0x17c00000\n"
TR "a[206:207],v1 offset:4616\n"
MF "v[36:39],a[208:211],a[68:71],v[36:39]\n"
"v_mov_b32 v220,s12\n"
"v_mov_b32 v221,s12\n"
MF "v[28:31],a[208:211],a[64:67],0\n"
"v_mov_b32 v222,s12\n"
"v_mov_b32 v223,s12\n"
MF "v[28:31],a[212:215],a[104:107],v[28:31]\n"
LD "v12,s[28:31],s82 offen offset:2048 lds\n"
MF "v[20:23],a[212:215],a[100:103],0\n"
"s_waitcnt vmcnt(7) lgkmcnt(0)\n"
MF "v[20:23],a[208:211],a[60:63],v[20:23]\n"
"s_barrier\n"
MF "v[56:59],a[216:219],a[60:63],v[220:223]\n"
TR "a[208:209],v0 offset:16384\n"
MF "v[56:59],a[220:223],a[100:103],v[56:59]\n"
TR "a[210:211],v0 offset:16896\n"
MF "v[64:67],a[220:223],a[104:107],v[220:223]\n"
LD "v13,s[28:31],s82 offen offset:3072 lds\n"
MF "v[64:67],a[216:219],a[64:67],v[64:67]\n"
"s_add_u32 m0,s62,65536\n"
TR "a[212:213],v0 offset:20480\n"
MF "v[72:75],a[216:219],a[68:71],v[220:223]\n"
"s_add_u32 s82,s66,786560\n"
TR "a[214:215],v0 offset:20992\n"
MF "v[72:75],a[220:223],a[108:111],v[72:75]\n"
"s_sub_u32 s15,s82,7864320\n"
MF "v[80:83],a[220:223],a[112:115],v[220:223]\n"
"s_min_u32 s82,s82,s15\n"
MF "v[80:83],a[216:219],a[72:75],v[80:83]\n"
LD "v6,s[28:31],s82 offen lds\n"
MF "v[88:91],a[216:219],a[76:79],v[220:223]\n"
MF "v[88:91],a[220:223],a[116:119],v[88:91]\n"
MF "v[92:95],a[204:207],a[116:119],v[220:223]\n"
TR "a[216:217],v0 offset:16392\n"
MF "v[92:95],a[200:203],a[76:79],v[92:95]\n"
TR "a[218:219],v0 offset:16904\n"
MF "v[84:87],a[200:203],a[72:75],v[220:223]\n"
LD "v11,s[28:31],s82 offen offset:1024 lds\n"
MF "v[84:87],a[204:207],a[112:115],v[84:87]\n"
TR "a[220:221],v0 offset:20488\n"
MF "v[76:79],a[204:207],a[108:111],v[220:223]\n"
TR "a[222:223],v0 offset:21000\n"
MF "v[76:79],a[200:203],a[68:71],v[76:79]\n"
MF "v[68:71],a[200:203],a[64:67],v[220:223]\n"
MF "v[68:71],a[204:207],a[104:107],v[68:71]\n"
LD "v12,s[28:31],s82 offen offset:2048 lds\n"
MF "v[60:63],a[204:207],a[100:103],v[220:223]\n"
MF "v[60:63],a[200:203],a[60:63],v[60:63]\n"
"s_waitcnt lgkmcnt(4)\n"
MF "v[96:99],a[208:211],a[60:63],v[220:223]\n"
TR "a[200:201],v1 offset:16384\n"
MF "v[96:99],a[212:215],a[100:103],v[96:99]\n"
TR "a[202:203],v1 offset:16896\n"
MF "v[104:107],a[212:215],a[104:107],v[220:223]\n"
LD "v13,s[28:31],s82 offen offset:3072 lds\n"
MF "v[104:107],a[208:211],a[64:67],v[104:107]\n"
"s_add_u32 m0,s62,81920\n"
TR "a[204:205],v1 offset:20480\n"
MF "v[112:115],a[208:211],a[68:71],v[220:223]\n"
"s_add_u32 s82,s66,786688\n"
TR "a[206:207],v1 offset:20992\n"
MF "v[112:115],a[212:215],a[108:111],v[112:115]\n"
"s_sub_u32 s15,s82,7864320\n"
MF "v[120:123],a[212:215],a[112:115],v[220:223]\n"
"s_min_u32 s82,s82,s15\n"
MF "v[120:123],a[208:211],a[72:75],v[120:123]\n"
LD "v6,s[28:31],s82 offen lds\n"
MF "v[128:131],a[208:211],a[76:79],v[220:223]\n"
MF "v[128:131],a[212:215],a[116:119],v[128:131]\n"
"s_waitcnt lgkmcnt(4)\n"
MF "v[132:135],a[220:223],a[116:119],v[220:223]\n"
TR "a[208:209],v1 offset:16392\n"
MF "v[132:135],a[216:219],a[76:79],v[132:135]\n"
TR "a[210:211],v1 offset:16904\n"
MF "v[124:127],a[216:219],a[72:75],v[220:223]\n"
LD "v11,s[28:31],s82 offen offset:1024 lds\n"
MF "v[124:127],a[220:223],a[112:115],v[124:127]\n"
TR "a[212:213],v1 offset:20488\n"
MF "v[116:119],a[220:223],a[108:111],v[220:223]\n"
TR "a[214:215],v1 offset:21000\n"
MF "v[116:119],a[216:219],a[68:71],v[116:119]\n"
MF "v[108:111],a[216:219],a[64:67],v[220:223]\n"
MF "v[108:111],a[220:223],a[104:107],v[108:111]\n"
LD "v12,s[28:31],s82 offen offset:2048 lds\n"
MF "v[100:103],a[220:223],a[100:103],v[220:223]\n"
"s_waitcnt vmcnt(11) lgkmcnt(0)\n"
MF "v[100:103],a[216:219],a[60:63],v[100:103]\n"
"s_barrier\n"
MF "v[136:139],a[200:203],a[60:63],v[220:223]\n"
TR "a[216:217],v0 offset:32768\n"
MF "v[136:139],a[204:207],a[100:103],v[136:139]\n"
TR "a[218:219],v0 offset:33280\n"
MF "v[144:147],a[204:207],a[104:107],v[220:223]\n"
"ds_read_b128 a[140:143],v2 offset:20480\n"
MF "v[144:147],a[200:203],a[64:67],v[144:147]\n"
TR "a[220:221],v0 offset:36864\n"
MF "v[152:155],a[200:203],a[68:71],v[220:223]\n"
TR "a[222:223],v0 offset:37376\n"
MF "v[152:155],a[204:207],a[108:111],v[152:155]\n"
LD "v13,s[28:31],s82 offen offset:3072 lds\n"
MF "v[160:163],a[204:207],a[112:115],v[220:223]\n"
"ds_read_b128 a[180:183],v3 offset:20480\n"
MF "v[160:163],a[200:203],a[72:75],v[160:163]\n"
"s_add_u32 m0,s62,0\n"
MF "v[168:171],a[200:203],a[76:79],v[220:223]\n"
"s_add_u32 s82,s66,1572864\n"
"s_sub_u32 s15,s82,7864320\n"
"s_min_u32 s82,s82,s15\n"
MF "v[168:171],a[204:207],a[116:119],v[168:171]\n"
LD "v6,s[28:31],s82 offen lds\n"
MF "v[172:175],a[212:215],a[116:119],v[220:223]\n"
TR "a[200:201],v0 offset:32776\n"
MF "v[172:175],a[208:211],a[76:79],v[172:175]\n"
TR "a[202:203],v0 offset:33288\n"
MF "v[164:167],a[208:211],a[72:75],v[220:223]\n"
"ds_read_b128 a[144:147],v2 offset:22528\n"
MF "v[164:167],a[212:215],a[112:115],v[164:167]\n"
TR "a[204:205],v0 offset:36872\n"
MF "v[156:159],a[212:215],a[108:111],v[220:223]\n"
TR "a[206:207],v0 offset:37384\n"
MF "v[156:159],a[208:211],a[68:71],v[156:159]\n"
LD "v11,s[28:31],s82 offen offset:1024 lds\n"
MF "v[148:151],a[208:211],a[64:67],v[220:223]\n"
"ds_read_b128 a[184:187],v3 offset:22528\n"
MF "v[148:151],a[212:215],a[104:107],v[148:151]\n"
MF "v[140:143],a[212:215],a[100:103],v[220:223]\n"
MF "v[140:143],a[208:211],a[60:63],v[140:143]\n"
"s_waitcnt lgkmcnt(7)\n"
LD "v12,s[28:31],s82 offen offset:2048 lds\n"
MF "v[176:179],a[216:219],a[60:63],v[220:223]\n"
TR "a[208:209],v1 offset:32768\n"
MF "v[176:179],a[220:223],a[100:103],v[176:179]\n"
".Lpg4c:\n"
"s_cbranch_execz .Lpg14c\n"
".Lpg4f:\n"
"s_cbranch_execz .Lpg13f\n"
TR "a[210:211],v1 offset:33280\n"
MF "v[184:187],a[220:223],a[104:107],v[220:223]\n"
"ds_read_b128 a[148:151],v2 offset:24576\n"
MF "v[184:187],a[216:219],a[64:67],v[184:187]\n"
TR "a[212:213],v1 offset:36864\n"
MF "v[192:195],a[216:219],a[68:71],v[220:223]\n"
TR "a[214:215],v1 offset:37376\n"
MF "v[192:195],a[220:223],a[108:111],v[192:195]\n"
LD "v13,s[28:31],s82 offen offset:3072 lds\n"
MF "v[200:203],a[220:223],a[112:115],v[220:223]\n"
"ds_read_b128 a[188:191],v3 offset:24576\n"
MF "v[200:203],a[216:219],a[72:75],v[200:203]\n"
"s_add_u32 s14,s81,384\n"
"s_sub_u32 s15,s14,1280\n"
"s_min_u32 s14,s14,s15\n"
MF "v[208:211],a[216:219],a[76:79],v[220:223]\n"
"s_add_u32 s14,s14,s68\n"
"s_add_u32 s84,s14,0\n"
"s_add_u32 m0,s63,0\n"
MF "v[208:211],a[220:223],a[116:119],v[208:211]\n"
"s_waitcnt lgkmcnt(7)\n"
LD "v4,s[24:27],s84 offen lds\n"
MF "v[212:215],a[204:207],a[116:119],v[220:223]\n"
TR "a[216:217],v1 offset:32776\n"
MF "v[212:215],a[200:203],a[76:79],v[212:215]\n"
TR "a[218:219],v1 offset:33288\n"
MF "v[204:207],a[200:203],a[72:75],v[220:223]\n"
"ds_read_b128 a[152:155],v2 offset:26624\n"
MF "v[204:207],a[204:207],a[112:115],v[204:207]\n"
TR "a[220:221],v1 offset:36872\n"
MF "v[196:199],a[204:207],a[108:111],v[220:223]\n"
"s_add_u32 s84,s14,9216\n"
TR "a[222:223],v1 offset:37384\n"
MF "v[196:199],a[200:203],a[68:71],v[196:199]\n"
LD "v5,s[24:27],s84 offen offset:1024 lds\n"
MF "v[188:191],a[200:203],a[64:67],v[220:223]\n"
"s_add_u32 m0,s62,16384\n"
"s_add_u32 s82,s66,1572992\n"
"v_add_u32 v0,0xc000,v0\n"
MF "v[188:191],a[204:207],a[104:107],v[188:191]\n"
"s_sub_u32 s15,s82,7864320\n"
"s_min_u32 s82,s82,s15\n"
"v_add_u32 v1,0xc000,v1\n"
MF "v[180:183],a[204:207],a[100:103],v[220:223]\n"
"s_waitcnt vmcnt(14) lgkmcnt(0)\n"
LD "v6,s[28:31],s82 offen lds\n"
MF "v[180:183],a[200:203],a[60:63],v[180:183]\n"
"s_barrier\n"
MF "v[216:219],a[208:211],a[60:63],v[220:223]\n"
TR "a[200:201],v0\n"
MF "v[216:219],a[212:215],a[100:103],v[216:219]\n"
TR "a[202:203],v0 offset:512\n"
MF "v[224:227],a[212:215],a[104:107],v[220:223]\n"
"ds_read_b128 a[192:195],v3 offset:26624\n"
MF "v[224:227],a[208:211],a[64:67],v[224:227]\n"
TR "a[204:205],v0 offset:4096\n"
MF "v[232:235],a[208:211],a[68:71],v[220:223]\n"
TR "a[206:207],v0 offset:4608\n"
MF "v[232:235],a[212:215],a[108:111],v[232:235]\n"
LD "v11,s[28:31],s82 offen offset:1024 lds\n"
MF "v[240:243],a[212:215],a[112:115],v[220:223]\n"
"ds_read_b128 a[156:159],v2 offset:28672\n"
MF "v[240:243],a[208:211],a[72:75],v[240:243]\n"
"ds_read_b128 a[196:199],v3 offset:28672\n"
MF "v[248:251],a[208:211],a[76:79],v[220:223]\n"
MF "v[248:251],a[212:215],a[116:119],v[248:251]\n"
LD "v12,s[28:31],s82 offen offset:2048 lds\n"
MF "v[252:255],a[220:223],a[116:119],v[220:223]\n"
TR "a[208:209],v0 offset:8\n"
MF "v[252:255],a[216:219],a[76:79],v[252:255]\n"
TR "a[210:211],v0 offset:520\n"
MF "v[244:247],a[216:219],a[72:75],v[220:223]\n"
MF "v[244:247],a[220:223],a[112:115],v[244:247]\n"
TR "a[212:213],v0 offset:4104\n"
MF "v[236:239],a[220:223],a[108:111],v[220:223]\n"
TR "a[214:215],v0 offset:4616\n"
MF "v[236:239],a[216:219],a[68:71],v[236:239]\n"
LD "v13,s[28:31],s82 offen offset:3072 lds\n"
MF "v[228:231],a[216:219],a[64:67],v[220:223]\n"
MF "v[228:231],a[220:223],a[104:107],v[228:231]\n"
"s_add_u32 s84,s14,20480\n"
MF "v[220:223],a[220:223],a[100:103],v[220:223]\n"
"s_add_u32 m0,s63,2048\n"
MF "v[220:223],a[216:219],a[60:63],v[220:223]\n"
"s_waitcnt lgkmcnt(4)\n"
LD "v4,s[24:27],s84 offen lds\n"
MF "v[16:19],a[200:203],a[140:143],v[16:19]\n"
TR "a[216:217],v1\n"
MF "v[16:19],a[204:207],a[180:183],v[16:19]\n"
TR "a[218:219],v1 offset:512\n"
MF "v[24:27],a[204:207],a[184:187],v[24:27]\n"
MF "v[24:27],a[200:203],a[144:147],v[24:27]\n"
TR "a[220:221],v1 offset:4096\n"
MF "v[32:35],a[200:203],a[148:151],v[32:35]\n"
"s_add_u32 s84,s14,29696\n"
TR "a[222:223],v1 offset:4608\n"
MF "v[32:35],a[204:207],a[188:191],v[32:35]\n"
LD "v5,s[24:27],s84 offen offset:1024 lds\n"
MF "v[40:43],a[204:207],a[192:195],v[40:43]\n"
MF "v[40:43],a[200:203],a[152:155],v[40:43]\n"
"s_add_u32 m0,s62,32768\n"
MF "v[48:51],a[200:203],a[156:159],v[48:51]\n"
"s_add_u32 s82,s66,1573120\n"
"s_sub_u32 s15,s82,7864320\n"
"s_min_u32 s82,s82,s15\n"
MF "v[48:51],a[204:207],a[196:199],v[48:51]\n"
"s_waitcnt lgkmcnt(4)\n"
LD "v6,s[28:31],s82 offen lds\n"
MF "v[52:55],a[212:215],a[196:199],v[52:55]\n"
TR "a[200:201],v1 offset:8\n"
MF "v[52:55],a[208:211],a[156:159],v[52:55]\n"
TR "a[202:203],v1 offset:520\n"
MF "v[44:47],a[208:211],a[152:155],v[44:47]\n"
MF "v[44:47],a[212:215],a[192:195],v[44:47]\n"
TR "a[204:205],v1 offset:4104\n"
MF "v[36:39],a[212:215],a[188:191],v[36:39]\n"
TR "a[206:207],v1 offset:4616\n"
MF "v[36:39],a[208:211],a[148:151],v[36:39]\n"
LD "v11,s[28:31],s82 offen offset:1024 lds\n"
MF "v[28:31],a[208:211],a[144:147],v[28:31]\n"
MF "v[28:31],a[212:215],a[184:187],v[28:31]\n"
MF "v[20:23],a[212:215],a[180:183],v[20:23]\n"
"s_waitcnt vmcnt(18) lgkmcnt(0)\n"
LD "v12,s[28:31],s82 offen offset:2048 lds\n"
"s_bitcmp0_b32 s23,12\n"
"s_cbranch_scc1 .Lisx2\n"
"s_mov_b64 exec,0\n"
"s_cmp_eq_u32 s56,0\n"
"s_cbranch_scc1 .Lpg11d\n"
"s_cmp_eq_u32 s56,1\n"
"s_cbranch_scc1 .Lpg10d\n"
"s_cmp_eq_u32 s56,2\n"
"s_cbranch_scc1 .Lpgt0kx_d\n"
"s_branch .Lpg8d\n"
".Lisr2:\n"
"s_mov_b64 exec,-1\n"
".Lisx2:\n"
MF "v[20:23],a[208:211],a[140:143],v[20:23]\n"
"s_barrier\n"
MF "v[56:59],a[216:219],a[140:143],v[56:59]\n"
TR "a[208:209],v0 offset:16384\n"
MF "v[56:59],a[220:223],a[180:183],v[56:59]\n"
TR "a[210:211],v0 offset:16896\n"
MF "v[64:67],a[220:223],a[184:187],v[64:67]\n"
LD "v13,s[28:31],s82 offen offset:3072 lds\n"
MF "v[64:67],a[216:219],a[144:147],v[64:67]\n"
TR "a[212:213],v0 offset:20480\n"
MF "v[72:75],a[216:219],a[148:151],v[72:75]\n"
TR "a[214:215],v0 offset:20992\n"
MF "v[72:75],a[220:223],a[188:191],v[72:75]\n"
"s_add_u32 s84,s14,40960\n"
MF "v[80:83],a[220:223],a[192:195],v[80:83]\n"
"s_add_u32 m0,s63,4096\n"
MF "v[80:83],a[216:219],a[152:155],v[80:83]\n"
LD "v4,s[24:27],s84 offen lds\n"
MF "v[88:91],a[216:219],a[156:159],v[88:91]\n"
MF "v[88:91],a[220:223],a[196:199],v[88:91]\n"
"s_sub_u32 s66,s66,s83\n"
MF "v[92:95],a[204:207],a[196:199],v[92:95]\n"
"s_add_u32 m0,s62,49152\n"
"s_add_u32 s82,s66,2359296\n"
TR "a[216:217],v0 offset:16392\n"
MF "v[92:95],a[200:203],a[156:159],v[92:95]\n"
"s_sub_u32 s15,s82,7864320\n"
"s_min_u32 s82,s82,s15\n"
TR "a[218:219],v0 offset:16904\n"
MF "v[84:87],a[200:203],a[152:155],v[84:87]\n"
LD "v6,s[28:31],s82 offen lds\n"
MF "v[84:87],a[204:207],a[192:195],v[84:87]\n"
TR "a[220:221],v0 offset:20488\n"
MF "v[76:79],a[204:207],a[188:191],v[76:79]\n"
TR "a[222:223],v0 offset:21000\n"
MF "v[76:79],a[200:203],a[148:151],v[76:79]\n"
MF "v[68:71],a[200:203],a[144:147],v[68:71]\n"
MF "v[68:71],a[204:207],a[184:187],v[68:71]\n"
LD "v11,s[28:31],s82 offen offset:1024 lds\n"
MF "v[60:63],a[204:207],a[180:183],v[60:63]\n"
MF "v[60:63],a[200:203],a[140:143],v[60:63]\n"
"s_waitcnt lgkmcnt(4)\n"
MF "v[96:99],a[208:211],a[140:143],v[96:99]\n"
TR "a[200:201],v1 offset:16384\n"
MF "v[96:99],a[212:215],a[180:183],v[96:99]\n"
TR "a[202:203],v1 offset:16896\n"
MF "v[104:107],a[212:215],a[184:187],v[104:107]\n"
LD "v12,s[28:31],s82 offen offset:2048 lds\n"
MF "v[104:107],a[208:211],a[144:147],v[104:107]\n"
TR "a[204:205],v1 offset:20480\n"
MF "v[112:115],a[208:211],a[148:151],v[112:115]\n"
TR "a[206:207],v1 offset:20992\n"
MF "v[112:115],a[212:215],a[188:191],v[112:115]\n"
MF "v[120:123],a[212:215],a[192:195],v[120:123]\n"
MF "v[120:123],a[208:211],a[152:155],v[120:123]\n"
LD "v13,s[28:31],s82 offen offset:3072 lds\n"
MF "v[128:131],a[208:211],a[156:159],v[128:131]\n"
MF "v[128:131],a[212:215],a[196:199],v[128:131]\n"
"s_add_u32 s14,s81,512\n"
"s_sub_u32 s15,s14,1280\n"
"s_waitcnt lgkmcnt(4)\n"
MF "v[132:135],a[220:223],a[196:199],v[132:135]\n"
"s_min_u32 s14,s14,s15\n"
"s_add_u32 s14,s14,s68\n"
TR "a[208:209],v1 offset:16392\n"
MF "v[132:135],a[216:219],a[156:159],v[132:135]\n"
"s_add_u32 s84,s14,0\n"
"s_add_u32 m0,s63,20480\n"
TR "a[210:211],v1 offset:16904\n"
MF "v[124:127],a[216:219],a[152:155],v[124:127]\n"
LD "v4,s[24:27],s84 offen lds\n"
MF "v[124:127],a[220:223],a[192:195],v[124:127]\n"
TR "a[212:213],v1 offset:20488\n"
MF "v[116:119],a[220:223],a[188:191],v[116:119]\n"
TR "a[214:215],v1 offset:21000\n"
MF "v[116:119],a[216:219],a[148:151],v[116:119]\n"
MF "v[108:111],a[216:219],a[144:147],v[108:111]\n"
"s_add_u32 s84,s14,9216\n"
MF "v[108:111],a[220:223],a[184:187],v[108:111]\n"
LD "v5,s[24:27],s84 offen offset:1024 lds\n"
MF "v[100:103],a[220:223],a[180:183],v[100:103]\n"
"s_waitcnt vmcnt(23) lgkmcnt(0)\n"
MF "v[100:103],a[216:219],a[140:143],v[100:103]\n"
"s_barrier\n"
MF "v[136:139],a[200:203],a[140:143],v[136:139]\n"
TR "a[216:217],v0 offset:32768\n"
MF "v[136:139],a[204:207],a[180:183],v[136:139]\n"
"s_add_u32 m0,s62,65536\n"
TR "a[218:219],v0 offset:33280\n"
MF "v[144:147],a[204:207],a[184:187],v[144:147]\n"
"s_add_u32 s82,s66,2359424\n"
"ds_read_b128 a[60:63],v2 offset:40960\n"
MF "v[144:147],a[200:203],a[144:147],v[144:147]\n"
"s_sub_u32 s15,s82,7864320\n"
TR "a[220:221],v0 offset:36864\n"
MF "v[152:155],a[200:203],a[148:151],v[152:155]\n"
"s_min_u32 s82,s82,s15\n"
TR "a[222:223],v0 offset:37376\n"
MF "v[152:155],a[204:207],a[188:191],v[152:155]\n"
LD "v6,s[28:31],s82 offen lds\n"
MF "v[160:163],a[204:207],a[192:195],v[160:163]\n"
"ds_read_b128 a[100:103],v3 offset:40960\n"
MF "v[160:163],a[200:203],a[152:155],v[160:163]\n"
LD "v11,s[28:31],s82 offen offset:1024 lds\n"
MF "v[168:171],a[200:203],a[156:159],v[168:171]\n"
MF "v[168:171],a[204:207],a[196:199],v[168:171]\n"
LD "v12,s[28:31],s82 offen offset:2048 lds\n"
MF "v[172:175],a[212:215],a[196:199],v[172:175]\n"
TR "a[200:201],v0 offset:32776\n"
MF "v[172:175],a[208:211],a[156:159],v[172:175]\n"
TR "a[202:203],v0 offset:33288\n"
MF "v[164:167],a[208:211],a[152:155],v[164:167]\n"
"ds_read_b128 a[64:67],v2 offset:43008\n"
MF "v[164:167],a[212:215],a[192:195],v[164:167]\n"
TR "a[204:205],v0 offset:36872\n"
MF "v[156:159],a[212:215],a[188:191],v[156:159]\n"
TR "a[206:207],v0 offset:37384\n"
MF "v[156:159],a[208:211],a[148:151],v[156:159]\n"
MF "v[148:151],a[208:211],a[144:147],v[148:151]\n"
"ds_read_b128 a[104:107],v3 offset:43008\n"
MF "v[148:151],a[212:215],a[184:187],v[148:151]\n"
MF "v[140:143],a[212:215],a[180:183],v[140:143]\n"
LD "v13,s[28:31],s82 offen offset:3072 lds\n"
MF "v[140:143],a[208:211],a[140:143],v[140:143]\n"
"s_waitcnt lgkmcnt(7)\n"
MF "v[176:179],a[216:219],a[140:143],v[176:179]\n"
TR "a[208:209],v1 offset:32768\n"
MF "v[176:179],a[220:223],a[180:183],v[176:179]\n"
TR "a[210:211],v1 offset:33280\n"
MF "v[184:187],a[220:223],a[184:187],v[184:187]\n"
"ds_read_b128 a[68:71],v2 offset:45056\n"
MF "v[184:187],a[216:219],a[144:147],v[184:187]\n"
"s_add_u32 s84,s14,20480\n"
TR "a[212:213],v1 offset:36864\n"
MF "v[192:195],a[216:219],a[148:151],v[192:195]\n"
"s_add_u32 m0,s63,22528\n"
TR "a[214:215],v1 offset:37376\n"
MF "v[192:195],a[220:223],a[188:191],v[192:195]\n"
LD "v4,s[24:27],s84 offen lds\n"
MF "v[200:203],a[220:223],a[192:195],v[200:203]\n"
"ds_read_b128 a[108:111],v3 offset:45056\n"
MF "v[200:203],a[216:219],a[152:155],v[200:203]\n"
MF "v[208:211],a[216:219],a[156:159],v[208:211]\n"
"s_add_u32 s84,s14,29696\n"
MF "v[208:211],a[220:223],a[196:199],v[208:211]\n"
"s_waitcnt lgkmcnt(7)\n"
LD "v5,s[24:27],s84 offen offset:1024 lds\n"
MF "v[212:215],a[204:207],a[196:199],v[212:215]\n"
TR "a[216:217],v1 offset:32776\n"
MF "v[212:215],a[200:203],a[156:159],v[212:215]\n"
TR "a[218:219],v1 offset:33288\n"
MF "v[204:207],a[200:203],a[152:155],v[204:207]\n"
"ds_read_b128 a[72:75],v2 offset:47104\n"
MF "v[204:207],a[204:207],a[192:195],v[204:207]\n"
TR "a[220:221],v1 offset:36872\n"
MF "v[196:199],a[204:207],a[188:191],v[196:199]\n"
TR "a[222:223],v1 offset:37384\n"
MF "v[196:199],a[200:203],a[148:151],v[196:199]\n"
MF "v[188:191],a[200:203],a[144:147],v[188:191]\n"
"v_add_u32 v0,0xffff4000,v0\n"
MF "v[188:191],a[204:207],a[184:187],v[188:191]\n"
"v_add_u32 v1,0xffff4000,v1\n"
MF "v[180:183],a[204:207],a[180:183],v[180:183]\n"
"s_waitcnt vmcnt(25) lgkmcnt(0)\n"
MF "v[180:183],a[200:203],a[140:143],v[180:183]\n"
"s_barrier\n"
MF "v[216:219],a[208:211],a[140:143],v[216:219]\n"
TR "a[200:201],v0\n"
MF "v[216:219],a[212:215],a[180:183],v[216:219]\n"
"s_add_u32 m0,s62,81920\n"
TR "a[202:203],v0 offset:512\n"
MF "v[224:227],a[212:215],a[184:187],v[224:227]\n"
"s_add_u32 s82,s66,2359552\n"
"ds_read_b128 a[112:115],v3 offset:47104\n"
MF "v[224:227],a[208:211],a[144:147],v[224:227]\n"
"s_sub_u32 s15,s82,7864320\n"
TR "a[204:205],v0 offset:4096\n"
MF "v[232:235],a[208:211],a[148:151],v[232:235]\n"
"s_min_u32 s82,s82,s15\n"
TR "a[206:207],v0 offset:4608\n"
MF "v[232:235],a[212:215],a[188:191],v[232:235]\n"
LD "v6,s[28:31],s82 offen lds\n"
MF "v[240:243],a[212:215],a[192:195],v[240:243]\n"
"ds_read_b128 a[76:79],v2 offset:49152\n"
MF "v[240:243],a[208:211],a[152:155],v[240:243]\n"
"ds_read_b128 a[116:119],v3 offset:49152\n"
MF "v[248:251],a[208:211],a[156:159],v[248:251]\n"
LD "v11,s[28:31],s82 offen offset:1024 lds\n"
MF "v[248:251],a[212:215],a[196:199],v[248:251]\n"
MF "v[252:255],a[220:223],a[196:199],v[252:255]\n"
TR "a[208:209],v0 offset:8\n"
MF "v[252:255],a[216:219],a[156:159],v[252:255]\n"
TR "a[210:211],v0 offset:520\n"
MF "v[244:247],a[216:219],a[152:155],v[244:247]\n"
MF "v[244:247],a[220:223],a[192:195],v[244:247]\n"
TR "a[212:213],v0 offset:4104\n"
MF "v[236:239],a[220:223],a[188:191],v[236:239]\n"
TR "a[214:215],v0 offset:4616\n"
MF "v[236:239],a[216:219],a[148:151],v[236:239]\n"
LD "v12,s[28:31],s82 offen offset:2048 lds\n"
MF "v[228:231],a[216:219],a[144:147],v[228:231]\n"
MF "v[228:231],a[220:223],a[184:187],v[228:231]\n"
MF "v[220:223],a[220:223],a[180:183],v[220:223]\n"
MF "v[220:223],a[216:219],a[140:143],v[220:223]\n"
"s_waitcnt lgkmcnt(4)\n"
MF "v[16:19],a[200:203],a[60:63],v[16:19]\n"
TR "a[216:217],v1\n"
MF "v[16:19],a[204:207],a[100:103],v[16:19]\n"
TR "a[218:219],v1 offset:512\n"
MF "v[24:27],a[204:207],a[104:107],v[24:27]\n"
LD "v13,s[28:31],s82 offen offset:3072 lds\n"
MF "v[24:27],a[200:203],a[64:67],v[24:27]\n"
TR "a[220:221],v1 offset:4096\n"
MF "v[32:35],a[200:203],a[68:71],v[32:35]\n"
TR "a[222:223],v1 offset:4608\n"
MF "v[32:35],a[204:207],a[108:111],v[32:35]\n"
MF "v[40:43],a[204:207],a[112:115],v[40:43]\n"
MF "v[40:43],a[200:203],a[72:75],v[40:43]\n"
"s_add_u32 s84,s14,40960\n"
MF "v[48:51],a[200:203],a[76:79],v[48:51]\n"
"s_add_u32 m0,s63,24576\n"
MF "v[48:51],a[204:207],a[116:119],v[48:51]\n"
"s_waitcnt lgkmcnt(4)\n"
LD "v4,s[24:27],s84 offen lds\n"
MF "v[52:55],a[212:215],a[116:119],v[52:55]\n"
TR "a[200:201],v1 offset:8\n"
MF "v[52:55],a[208:211],a[76:79],v[52:55]\n"
TR "a[202:203],v1 offset:520\n"
MF "v[44:47],a[208:211],a[72:75],v[44:47]\n"
MF "v[44:47],a[212:215],a[112:115],v[44:47]\n"
TR "a[204:205],v1 offset:4104\n"
MF "v[36:39],a[212:215],a[108:111],v[36:39]\n"
TR "a[206:207],v1 offset:4616\n"
MF "v[36:39],a[208:211],a[68:71],v[36:39]\n"
MF "v[28:31],a[208:211],a[64:67],v[28:31]\n"
MF "v[28:31],a[212:215],a[104:107],v[28:31]\n"
MF "v[20:23],a[212:215],a[100:103],v[20:23]\n"
"s_waitcnt vmcnt(24) lgkmcnt(0)\n"
MF "v[20:23],a[208:211],a[60:63],v[20:23]\n"
"s_barrier\n"
MF "v[56:59],a[216:219],a[60:63],v[56:59]\n"
"s_add_u32 m0,s62,0\n"
"s_add_u32 s82,s66,3145728\n"
TR "a[208:209],v0 offset:16384\n"
MF "v[56:59],a[220:223],a[100:103],v[56:59]\n"
"s_sub_u32 s15,s82,7864320\n"
"s_min_u32 s82,s82,s15\n"
TR "a[210:211],v0 offset:16896\n"
MF "v[64:67],a[220:223],a[104:107],v[64:67]\n"
LD "v6,s[28:31],s82 offen lds\n"
MF "v[64:67],a[216:219],a[64:67],v[64:67]\n"
TR "a[212:213],v0 offset:20480\n"
MF "v[72:75],a[216:219],a[68:71],v[72:75]\n"
TR "a[214:215],v0 offset:20992\n"
MF "v[72:75],a[220:223],a[108:111],v[72:75]\n"
MF "v[80:83],a[220:223],a[112:115],v[80:83]\n"
LD "v11,s[28:31],s82 offen offset:1024 lds\n"
MF "v[80:83],a[216:219],a[72:75],v[80:83]\n"
MF "v[88:91],a[216:219],a[76:79],v[88:91]\n"
MF "v[88:91],a[220:223],a[116:119],v[88:91]\n"
MF "v[92:95],a[204:207],a[116:119],v[92:95]\n"
TR "a[216:217],v0 offset:16392\n"
MF "v[92:95],a[200:203],a[76:79],v[92:95]\n"
TR "a[218:219],v0 offset:16904\n"
MF "v[84:87],a[200:203],a[72:75],v[84:87]\n"
LD "v12,s[28:31],s82 offen offset:2048 lds\n"
MF "v[84:87],a[204:207],a[112:115],v[84:87]\n"
TR "a[220:221],v0 offset:20488\n"
MF "v[76:79],a[204:207],a[108:111],v[76:79]\n"
TR "a[222:223],v0 offset:21000\n"
MF "v[76:79],a[200:203],a[68:71],v[76:79]\n"
MF "v[68:71],a[200:203],a[64:67],v[68:71]\n"
MF "v[68:71],a[204:207],a[104:107],v[68:71]\n"
MF "v[60:63],a[204:207],a[100:103],v[60:63]\n"
LD "v13,s[28:31],s82 offen offset:3072 lds\n"
MF "v[60:63],a[200:203],a[60:63],v[60:63]\n"
"s_waitcnt lgkmcnt(4)\n"
MF "v[96:99],a[208:211],a[60:63],v[96:99]\n"
TR "a[200:201],v1 offset:16384\n"
MF "v[96:99],a[212:215],a[100:103],v[96:99]\n"
TR "a[202:203],v1 offset:16896\n"
MF "v[104:107],a[212:215],a[104:107],v[104:107]\n"
"s_add_u32 s14,s81,640\n"
"s_sub_u32 s15,s14,1280\n"
MF "v[104:107],a[208:211],a[64:67],v[104:107]\n"
"s_min_u32 s14,s14,s15\n"
"s_add_u32 s14,s14,s68\n"
TR "a[204:205],v1 offset:20480\n"
MF "v[112:115],a[208:211],a[68:71],v[112:115]\n"
"s_add_u32 s84,s14,0\n"
"s_add_u32 m0,s63,40960\n"
TR "a[206:207],v1 offset:20992\n"
MF "v[112:115],a[212:215],a[108:111],v[112:115]\n"
LD "v4,s[24:27],s84 offen lds\n"
MF "v[120:123],a[212:215],a[112:115],v[120:123]\n"
MF "v[120:123],a[208:211],a[72:75],v[120:123]\n"
MF "v[128:131],a[208:211],a[76:79],v[128:131]\n"
"s_add_u32 s84,s14,9216\n"
MF "v[128:131],a[212:215],a[116:119],v[128:131]\n"
"s_waitcnt lgkmcnt(4)\n"
LD "v5,s[24:27],s84 offen offset:1024 lds\n"
MF "v[132:135],a[220:223],a[116:119],v[132:135]\n"
TR "a[208:209],v1 offset:16392\n"
MF "v[132:135],a[216:219],a[76:79],v[132:135]\n"
TR "a[210:211],v1 offset:16904\n"
MF "v[124:127],a[216:219],a[72:75],v[124:127]\n"
MF "v[124:127],a[220:223],a[112:115],v[124:127]\n"
TR "a[212:213],v1 offset:20488\n"
MF "v[116:119],a[220:223],a[108:111],v[116:119]\n"
TR "a[214:215],v1 offset:21000\n"
MF "v[116:119],a[216:219],a[68:71],v[116:119]\n"
MF "v[108:111],a[216:219],a[64:67],v[108:111]\n"
MF "v[108:111],a[220:223],a[104:107],v[108:111]\n"
MF "v[100:103],a[220:223],a[100:103],v[100:103]\n"
"s_waitcnt vmcnt(23) lgkmcnt(0)\n"
MF "v[100:103],a[216:219],a[60:63],v[100:103]\n"
"s_barrier\n"
MF "v[136:139],a[200:203],a[60:63],v[136:139]\n"
TR "a[216:217],v0 offset:32768\n"
MF "v[136:139],a[204:207],a[100:103],v[136:139]\n"
"s_add_u32 m0,s62,16384\n"
TR "a[218:219],v0 offset:33280\n"
MF "v[144:147],a[204:207],a[104:107],v[144:147]\n"
"s_add_u32 s82,s66,3145856\n"
"ds_read_b128 a[140:143],v2\n"
MF "v[144:147],a[200:203],a[64:67],v[144:147]\n"
"s_sub_u32 s15,s82,7864320\n"
TR "a[220:221],v0 offset:36864\n"
MF "v[152:155],a[200:203],a[68:71],v[152:155]\n"
"s_min_u32 s82,s82,s15\n"
TR "a[222:223],v0 offset:37376\n"
MF "v[152:155],a[204:207],a[108:111],v[152:155]\n"
LD "v6,s[28:31],s82 offen lds\n"
MF "v[160:163],a[204:207],a[112:115],v[160:163]\n"
"ds_read_b128 a[180:183],v3\n"
MF "v[160:163],a[200:203],a[72:75],v[160:163]\n"
LD "v11,s[28:31],s82 offen offset:1024 lds\n"
MF "v[168:171],a[200:203],a[76:79],v[168:171]\n"
MF "v[168:171],a[204:207],a[116:119],v[168:171]\n"
LD "v12,s[28:31],s82 offen offset:2048 lds\n"
MF "v[172:175],a[212:215],a[116:119],v[172:175]\n"
TR "a[200:201],v0 offset:32776\n"
MF "v[172:175],a[208:211],a[76:79],v[172:175]\n"
TR "a[202:203],v0 offset:33288\n"
MF "v[164:167],a[208:211],a[72:75],v[164:167]\n"
"ds_read_b128 a[144:147],v2 offset:2048\n"
MF "v[164:167],a[212:215],a[112:115],v[164:167]\n"
TR "a[204:205],v0 offset:36872\n"
MF "v[156:159],a[212:215],a[108:111],v[156:159]\n"
TR "a[206:207],v0 offset:37384\n"
MF "v[156:159],a[208:211],a[68:71],v[156:159]\n"
MF "v[148:151],a[208:211],a[64:67],v[148:151]\n"
"ds_read_b128 a[184:187],v3 offset:2048\n"
MF "v[148:151],a[212:215],a[104:107],v[148:151]\n"
MF "v[140:143],a[212:215],a[100:103],v[140:143]\n"
LD "v13,s[28:31],s82 offen offset:3072 lds\n"
MF "v[140:143],a[208:211],a[60:63],v[140:143]\n"
"s_waitcnt lgkmcnt(7)\n"
MF "v[176:179],a[216:219],a[60:63],v[176:179]\n"
TR "a[208:209],v1 offset:32768\n"
MF "v[176:179],a[220:223],a[100:103],v[176:179]\n"
TR "a[210:211],v1 offset:33280\n"
MF "v[184:187],a[220:223],a[104:107],v[184:187]\n"
"ds_read_b128 a[148:151],v2 offset:4096\n"
MF "v[184:187],a[216:219],a[64:67],v[184:187]\n"
"s_add_u32 s84,s14,20480\n"
TR "a[212:213],v1 offset:36864\n"
MF "v[192:195],a[216:219],a[68:71],v[192:195]\n"
"s_add_u32 m0,s63,43008\n"
TR "a[214:215],v1 offset:37376\n"
MF "v[192:195],a[220:223],a[108:111],v[192:195]\n"
LD "v4,s[24:27],s84 offen lds\n"
MF "v[200:203],a[220:223],a[112:115],v[200:203]\n"
"ds_read_b128 a[188:191],v3 offset:4096\n"
MF "v[200:203],a[216:219],a[72:75],v[200:203]\n"
MF "v[208:211],a[216:219],a[76:79],v[208:211]\n"
"s_add_u32 s84,s14,29696\n"
MF "v[208:211],a[220:223],a[116:119],v[208:211]\n"
"s_waitcnt lgkmcnt(7)\n"
LD "v5,s[24:27],s84 offen offset:1024 lds\n"
MF "v[212:215],a[204:207],a[116:119],v[212:215]\n"
TR "a[216:217],v1 offset:32776\n"
MF "v[212:215],a[200:203],a[76:79],v[212:215]\n"
TR "a[218:219],v1 offset:33288\n"
MF "v[204:207],a[200:203],a[72:75],v[204:207]\n"
"ds_read_b128 a[152:155],v2 offset:6144\n"
MF "v[204:207],a[204:207],a[112:115],v[204:207]\n"
TR "a[220:221],v1 offset:36872\n"
MF "v[196:199],a[204:207],a[108:111],v[196:199]\n"
TR "a[222:223],v1 offset:37384\n"
MF "v[196:199],a[200:203],a[68:71],v[196:199]\n"
MF "v[188:191],a[200:203],a[64:67],v[188:191]\n"
"v_add_u32 v0,0xc000,v0\n"
MF "v[188:191],a[204:207],a[104:107],v[188:191]\n"
"v_add_u32 v1,0xc000,v1\n"
MF "v[180:183],a[204:207],a[100:103],v[180:183]\n"
"s_waitcnt vmcnt(25) lgkmcnt(0)\n"
MF "v[180:183],a[200:203],a[60:63],v[180:183]\n"
"s_barrier\n"
MF "v[216:219],a[208:211],a[60:63],v[216:219]\n"
TR "a[200:201],v0\n"
MF "v[216:219],a[212:215],a[100:103],v[216:219]\n"
"s_add_u32 m0,s62,32768\n"
TR "a[202:203],v0 offset:512\n"
MF "v[224:227],a[212:215],a[104:107],v[224:227]\n"
".Lpg5c:\n"
"s_cbranch_execz .Lpg15c\n"
".Lpg5f:\n"
"s_cbranch_execz .LpgattB_f\n"
"s_add_u32 s82,s66,3145984\n"
"ds_read_b128 a[192:195],v3 offset:6144\n"
MF "v[224:227],a[208:211],a[64:67],v[224:227]\n"
"s_sub_u32 s15,s82,7864320\n"
TR "a[204:205],v0 offset:4096\n"
MF "v[232:235],a[208:211],a[68:71],v[232:235]\n"
"s_min_u32 s82,s82,s15\n"
TR "a[206:207],v0 offset:4608\n"
MF "v[232:235],a[212:215],a[108:111],v[232:235]\n"
LD "v6,s[28:31],s82 offen lds\n"
MF "v[240:243],a[212:215],a[112:115],v[240:243]\n"
"ds_read_b128 a[156:159],v2 offset:8192\n"
MF "v[240:243],a[208:211],a[72:75],v[240:243]\n"
"ds_read_b128 a[196:199],v3 offset:8192\n"
MF "v[248:251],a[208:211],a[76:79],v[248:251]\n"
LD "v11,s[28:31],s82 offen offset:1024 lds\n"
MF "v[248:251],a[212:215],a[116:119],v[248:251]\n"
MF "v[252:255],a[220:223],a[116:119],v[252:255]\n"
TR "a[208:209],v0 offset:8\n"
MF "v[252:255],a[216:219],a[76:79],v[252:255]\n"
TR "a[210:211],v0 offset:520\n"
MF "v[244:247],a[216:219],a[72:75],v[244:247]\n"
MF "v[244:247],a[220:223],a[112:115],v[244:247]\n"
TR "a[212:213],v0 offset:4104\n"
MF "v[236:239],a[220:223],a[108:111],v[236:239]\n"
TR "a[214:215],v0 offset:4616\n"
MF "v[236:239],a[216:219],a[68:71],v[236:239]\n"
LD "v12,s[28:31],s82 offen offset:2048 lds\n"
MF "v[228:231],a[216:219],a[64:67],v[228:231]\n"
MF "v[228:231],a[220:223],a[104:107],v[228:231]\n"
MF "v[220:223],a[220:223],a[100:103],v[220:223]\n"
MF "v[220:223],a[216:219],a[60:63],v[220:223]\n"
"s_waitcnt lgkmcnt(4)\n"
MF "v[16:19],a[200:203],a[140:143],v[16:19]\n"
TR "a[216:217],v1\n"
MF "v[16:19],a[204:207],a[180:183],v[16:19]\n"
TR "a[218:219],v1 offset:512\n"
MF "v[24:27],a[204:207],a[184:187],v[24:27]\n"
LD "v13,s[28:31],s82 offen offset:3072 lds\n"
MF "v[24:27],a[200:203],a[144:147],v[24:27]\n"
TR "a[220:221],v1 offset:4096\n"
MF "v[32:35],a[200:203],a[148:151],v[32:35]\n"
TR "a[222:223],v1 offset:4608\n"
MF "v[32:35],a[204:207],a[188:191],v[32:35]\n"
MF "v[40:43],a[204:207],a[192:195],v[40:43]\n"
MF "v[40:43],a[200:203],a[152:155],v[40:43]\n"
"s_add_u32 s84,s14,40960\n"
MF "v[48:51],a[200:203],a[156:159],v[48:51]\n"
"s_add_u32 m0,s63,45056\n"
MF "v[48:51],a[204:207],a[196:199],v[48:51]\n"
"s_waitcnt lgkmcnt(4)\n"
LD "v4,s[24:27],s84 offen lds\n"
MF "v[52:55],a[212:215],a[196:199],v[52:55]\n"
TR "a[200:201],v1 offset:8\n"
MF "v[52:55],a[208:211],a[156:159],v[52:55]\n"
TR "a[202:203],v1 offset:520\n"
MF "v[44:47],a[208:211],a[152:155],v[44:47]\n"
MF "v[44:47],a[212:215],a[192:195],v[44:47]\n"
TR "a[204:205],v1 offset:4104\n"
MF "v[36:39],a[212:215],a[188:191],v[36:39]\n"
TR "a[206:207],v1 offset:4616\n"
MF "v[36:39],a[208:211],a[148:151],v[36:39]\n"
MF "v[28:31],a[208:211],a[144:147],v[28:31]\n"
MF "v[28:31],a[212:215],a[184:187],v[28:31]\n"
MF "v[20:23],a[212:215],a[180:183],v[20:23]\n"
"s_waitcnt vmcnt(24) lgkmcnt(0)\n"
MF "v[20:23],a[208:211],a[140:143],v[20:23]\n"
"s_barrier\n"
MF "v[56:59],a[216:219],a[140:143],v[56:59]\n"
"s_add_u32 m0,s62,49152\n"
"s_add_u32 s82,s66,3932160\n"
TR "a[208:209],v0 offset:16384\n"
MF "v[56:59],a[220:223],a[180:183],v[56:59]\n"
"s_sub_u32 s15,s82,7864320\n"
"s_min_u32 s82,s82,s15\n"
TR "a[210:211],v0 offset:16896\n"
MF "v[64:67],a[220:223],a[184:187],v[64:67]\n"
LD "v6,s[28:31],s82 offen lds\n"
MF "v[64:67],a[216:219],a[144:147],v[64:67]\n"
TR "a[212:213],v0 offset:20480\n"
MF "v[72:75],a[216:219],a[148:151],v[72:75]\n"
TR "a[214:215],v0 offset:20992\n"
MF "v[72:75],a[220:223],a[188:191],v[72:75]\n"
MF "v[80:83],a[220:223],a[192:195],v[80:83]\n"
LD "v11,s[28:31],s82 offen offset:1024 lds\n"
MF "v[80:83],a[216:219],a[152:155],v[80:83]\n"
MF "v[88:91],a[216:219],a[156:159],v[88:91]\n"
MF "v[88:91],a[220:223],a[196:199],v[88:91]\n"
MF "v[92:95],a[204:207],a[196:199],v[92:95]\n"
TR "a[216:217],v0 offset:16392\n"
MF "v[92:95],a[200:203],a[156:159],v[92:95]\n"
TR "a[218:219],v0 offset:16904\n"
MF "v[84:87],a[200:203],a[152:155],v[84:87]\n"
LD "v12,s[28:31],s82 offen offset:2048 lds\n"
MF "v[84:87],a[204:207],a[192:195],v[84:87]\n"
TR "a[220:221],v0 offset:20488\n"
MF "v[76:79],a[204:207],a[188:191],v[76:79]\n"
TR "a[222:223],v0 offset:21000\n"
MF "v[76:79],a[200:203],a[148:151],v[76:79]\n"
MF "v[68:71],a[200:203],a[144:147],v[68:71]\n"
MF "v[68:71],a[204:207],a[184:187],v[68:71]\n"
MF "v[60:63],a[204:207],a[180:183],v[60:63]\n"
LD "v13,s[28:31],s82 offen offset:3072 lds\n"
MF "v[60:63],a[200:203],a[140:143],v[60:63]\n"
"s_waitcnt lgkmcnt(4)\n"
MF "v[96:99],a[208:211],a[140:143],v[96:99]\n"
TR "a[200:201],v1 offset:16384\n"
MF "v[96:99],a[212:215],a[180:183],v[96:99]\n"
TR "a[202:203],v1 offset:16896\n"
MF "v[104:107],a[212:215],a[184:187],v[104:107]\n"
"s_add_u32 s14,s81,768\n"
"s_sub_u32 s15,s14,1280\n"
MF "v[104:107],a[208:211],a[144:147],v[104:107]\n"
"s_min_u32 s14,s14,s15\n"
"s_add_u32 s14,s14,s68\n"
TR "a[204:205],v1 offset:20480\n"
MF "v[112:115],a[208:211],a[148:151],v[112:115]\n"
"s_add_u32 s84,s14,0\n"
"s_add_u32 m0,s63,0\n"
TR "a[206:207],v1 offset:20992\n"
MF "v[112:115],a[212:215],a[188:191],v[112:115]\n"
LD "v4,s[24:27],s84 offen lds\n"
MF "v[120:123],a[212:215],a[192:195],v[120:123]\n"
MF "v[120:123],a[208:211],a[152:155],v[120:123]\n"
MF "v[128:131],a[208:211],a[156:159],v[128:131]\n"
"s_add_u32 s84,s14,9216\n"
MF "v[128:131],a[212:215],a[196:199],v[128:131]\n"
"s_waitcnt lgkmcnt(4)\n"
LD "v5,s[24:27],s84 offen offset:1024 lds\n"
MF "v[132:135],a[220:223],a[196:199],v[132:135]\n"
TR "a[208:209],v1 offset:16392\n"
MF "v[132:135],a[216:219],a[156:159],v[132:135]\n"
TR "a[210:211],v1 offset:16904\n"
MF "v[124:127],a[216:219],a[152:155],v[124:127]\n"
MF "v[124:127],a[220:223],a[192:195],v[124:127]\n"
TR "a[212:213],v1 offset:20488\n"
MF "v[116:119],a[220:223],a[188:191],v[116:119]\n"
TR "a[214:215],v1 offset:21000\n"
MF "v[116:119],a[216:219],a[148:151],v[116:119]\n"
MF "v[108:111],a[216:219],a[144:147],v[108:111]\n"
MF "v[108:111],a[220:223],a[184:187],v[108:111]\n"
MF "v[100:103],a[220:223],a[180:183],v[100:103]\n"
"s_waitcnt vmcnt(23) lgkmcnt(0)\n"
MF "v[100:103],a[216:219],a[140:143],v[100:103]\n"
"s_barrier\n"
MF "v[136:139],a[200:203],a[140:143],v[136:139]\n"
TR "a[216:217],v0 offset:32768\n"
MF "v[136:139],a[204:207],a[180:183],v[136:139]\n"
"s_add_u32 m0,s62,65536\n"
TR "a[218:219],v0 offset:33280\n"
MF "v[144:147],a[204:207],a[184:187],v[144:147]\n"
"s_add_u32 s82,s66,3932288\n"
"ds_read_b128 a[60:63],v2 offset:20480\n"
MF "v[144:147],a[200:203],a[144:147],v[144:147]\n"
"s_sub_u32 s15,s82,7864320\n"
TR "a[220:221],v0 offset:36864\n"
MF "v[152:155],a[200:203],a[148:151],v[152:155]\n"
"s_min_u32 s82,s82,s15\n"
TR "a[222:223],v0 offset:37376\n"
MF "v[152:155],a[204:207],a[188:191],v[152:155]\n"
LD "v6,s[28:31],s82 offen lds\n"
MF "v[160:163],a[204:207],a[192:195],v[160:163]\n"
"ds_read_b128 a[100:103],v3 offset:20480\n"
MF "v[160:163],a[200:203],a[152:155],v[160:163]\n"
LD "v11,s[28:31],s82 offen offset:1024 lds\n"
MF "v[168:171],a[200:203],a[156:159],v[168:171]\n"
MF "v[168:171],a[204:207],a[196:199],v[168:171]\n"
LD "v12,s[28:31],s82 offen offset:2048 lds\n"
MF "v[172:175],a[212:215],a[196:199],v[172:175]\n"
TR "a[200:201],v0 offset:32776\n"
MF "v[172:175],a[208:211],a[156:159],v[172:175]\n"
TR "a[202:203],v0 offset:33288\n"
MF "v[164:167],a[208:211],a[152:155],v[164:167]\n"
"ds_read_b128 a[64:67],v2 offset:22528\n"
MF "v[164:167],a[212:215],a[192:195],v[164:167]\n"
TR "a[204:205],v0 offset:36872\n"
MF "v[156:159],a[212:215],a[188:191],v[156:159]\n"
TR "a[206:207],v0 offset:37384\n"
MF "v[156:159],a[208:211],a[148:151],v[156:159]\n"
MF "v[148:151],a[208:211],a[144:147],v[148:151]\n"
"ds_read_b128 a[104:107],v3 offset:22528\n"
MF "v[148:151],a[212:215],a[184:187],v[148:151]\n"
MF "v[140:143],a[212:215],a[180:183],v[140:143]\n"
LD "v13,s[28:31],s82 offen offset:3072 lds\n"
MF "v[140:143],a[208:211],a[140:143],v[140:143]\n"
"s_waitcnt lgkmcnt(7)\n"
MF "v[176:179],a[216:219],a[140:143],v[176:179]\n"
TR "a[208:209],v1 offset:32768\n"
MF "v[176:179],a[220:223],a[180:183],v[176:179]\n"
TR "a[210:211],v1 offset:33280\n"
MF "v[184:187],a[220:223],a[184:187],v[184:187]\n"
"ds_read_b128 a[68:71],v2 offset:24576\n"
MF "v[184:187],a[216:219],a[144:147],v[184:187]\n"
"s_add_u32 s84,s14,20480\n"
TR "a[212:213],v1 offset:36864\n"
MF "v[192:195],a[216:219],a[148:151],v[192:195]\n"
"s_add_u32 m0,s63,2048\n"
TR "a[214:215],v1 offset:37376\n"
MF "v[192:195],a[220:223],a[188:191],v[192:195]\n"
LD "v4,s[24:27],s84 offen lds\n"
MF "v[200:203],a[220:223],a[192:195],v[200:203]\n"
"ds_read_b128 a[108:111],v3 offset:24576\n"
MF "v[200:203],a[216:219],a[152:155],v[200:203]\n"
MF "v[208:211],a[216:219],a[156:159],v[208:211]\n"
"s_add_u32 s84,s14,29696\n"
MF "v[208:211],a[220:223],a[196:199],v[208:211]\n"
"s_waitcnt lgkmcnt(7)\n"
LD "v5,s[24:27],s84 offen offset:1024 lds\n"
MF "v[212:215],a[204:207],a[196:199],v[212:215]\n"
TR "a[216:217],v1 offset:32776\n"
MF "v[212:215],a[200:203],a[156:159],v[212:215]\n"
TR "a[218:219],v1 offset:33288\n"
MF "v[204:207],a[200:203],a[152:155],v[204:207]\n"
"ds_read_b128 a[72:75],v2 offset:26624\n"
MF "v[204:207],a[204:207],a[192:195],v[204:207]\n"
TR "a[220:221],v1 offset:36872\n"
MF "v[196:199],a[204:207],a[188:191],v[196:199]\n"
TR "a[222:223],v1 offset:37384\n"
MF "v[196:199],a[200:203],a[148:151],v[196:199]\n"
MF "v[188:191],a[200:203],a[144:147],v[188:191]\n"
"v_add_u32 v0,0xffff4000,v0\n"
MF "v[188:191],a[204:207],a[184:187],v[188:191]\n"
"v_add_u32 v1,0xffff4000,v1\n"
MF "v[180:183],a[204:207],a[180:183],v[180:183]\n"
"s_waitcnt vmcnt(25) lgkmcnt(0)\n"
MF "v[180:183],a[200:203],a[140:143],v[180:183]\n"
"s_barrier\n"
MF "v[216:219],a[208:211],a[140:143],v[216:219]\n"
TR "a[200:201],v0\n"
MF "v[216:219],a[212:215],a[180:183],v[216:219]\n"
"s_add_u32 m0,s62,81920\n"
TR "a[202:203],v0 offset:512\n"
MF "v[224:227],a[212:215],a[184:187],v[224:227]\n"
"s_add_u32 s82,s66,3932416\n"
"ds_read_b128 a[112:115],v3 offset:26624\n"
MF "v[224:227],a[208:211],a[144:147],v[224:227]\n"
"s_sub_u32 s15,s82,7864320\n"
TR "a[204:205],v0 offset:4096\n"
MF "v[232:235],a[208:211],a[148:151],v[232:235]\n"
"s_min_u32 s82,s82,s15\n"
TR "a[206:207],v0 offset:4608\n"
MF "v[232:235],a[212:215],a[188:191],v[232:235]\n"
LD "v6,s[28:31],s82 offen lds\n"
MF "v[240:243],a[212:215],a[192:195],v[240:243]\n"
"ds_read_b128 a[76:79],v2 offset:28672\n"
MF "v[240:243],a[208:211],a[152:155],v[240:243]\n"
"ds_read_b128 a[116:119],v3 offset:28672\n"
MF "v[248:251],a[208:211],a[156:159],v[248:251]\n"
LD "v11,s[28:31],s82 offen offset:1024 lds\n"
MF "v[248:251],a[212:215],a[196:199],v[248:251]\n"
MF "v[252:255],a[220:223],a[196:199],v[252:255]\n"
TR "a[208:209],v0 offset:8\n"
MF "v[252:255],a[216:219],a[156:159],v[252:255]\n"
TR "a[210:211],v0 offset:520\n"
MF "v[244:247],a[216:219],a[152:155],v[244:247]\n"
MF "v[244:247],a[220:223],a[192:195],v[244:247]\n"
TR "a[212:213],v0 offset:4104\n"
MF "v[236:239],a[220:223],a[188:191],v[236:239]\n"
TR "a[214:215],v0 offset:4616\n"
MF "v[236:239],a[216:219],a[148:151],v[236:239]\n"
LD "v12,s[28:31],s82 offen offset:2048 lds\n"
MF "v[228:231],a[216:219],a[144:147],v[228:231]\n"
MF "v[228:231],a[220:223],a[184:187],v[228:231]\n"
MF "v[220:223],a[220:223],a[180:183],v[220:223]\n"
MF "v[220:223],a[216:219],a[140:143],v[220:223]\n"
"s_waitcnt lgkmcnt(4)\n"
MF "v[16:19],a[200:203],a[60:63],v[16:19]\n"
TR "a[216:217],v1\n"
MF "v[16:19],a[204:207],a[100:103],v[16:19]\n"
TR "a[218:219],v1 offset:512\n"
MF "v[24:27],a[204:207],a[104:107],v[24:27]\n"
LD "v13,s[28:31],s82 offen offset:3072 lds\n"
MF "v[24:27],a[200:203],a[64:67],v[24:27]\n"
TR "a[220:221],v1 offset:4096\n"
MF "v[32:35],a[200:203],a[68:71],v[32:35]\n"
TR "a[222:223],v1 offset:4608\n"
MF "v[32:35],a[204:207],a[108:111],v[32:35]\n"
MF "v[40:43],a[204:207],a[112:115],v[40:43]\n"
MF "v[40:43],a[200:203],a[72:75],v[40:43]\n"
"s_add_u32 s84,s14,40960\n"
MF "v[48:51],a[200:203],a[76:79],v[48:51]\n"
"s_add_u32 m0,s63,4096\n"
MF "v[48:51],a[204:207],a[116:119],v[48:51]\n"
"s_waitcnt lgkmcnt(4)\n"
LD "v4,s[24:27],s84 offen lds\n"
MF "v[52:55],a[212:215],a[116:119],v[52:55]\n"
TR "a[200:201],v1 offset:8\n"
MF "v[52:55],a[208:211],a[76:79],v[52:55]\n"
TR "a[202:203],v1 offset:520\n"
MF "v[44:47],a[208:211],a[72:75],v[44:47]\n"
MF "v[44:47],a[212:215],a[112:115],v[44:47]\n"
TR "a[204:205],v1 offset:4104\n"
MF "v[36:39],a[212:215],a[108:111],v[36:39]\n"
TR "a[206:207],v1 offset:4616\n"
MF "v[36:39],a[208:211],a[68:71],v[36:39]\n"
MF "v[28:31],a[208:211],a[64:67],v[28:31]\n"
MF "v[28:31],a[212:215],a[104:107],v[28:31]\n"
MF "v[20:23],a[212:215],a[100:103],v[20:23]\n"
"s_waitcnt vmcnt(24) lgkmcnt(0)\n"
MF "v[20:23],a[208:211],a[60:63],v[20:23]\n"
"s_barrier\n"
MF "v[56:59],a[216:219],a[60:63],v[56:59]\n"
"s_add_u32 m0,s62,0\n"
"s_add_u32 s82,s66,4718592\n"
TR "a[208:209],v0 offset:16384\n"
MF "v[56:59],a[220:223],a[100:103],v[56:59]\n"
"s_sub_u32 s15,s82,7864320\n"
"s_min_u32 s82,s82,s15\n"
TR "a[210:211],v0 offset:16896\n"
MF "v[64:67],a[220:223],a[104:107],v[64:67]\n"
LD "v6,s[28:31],s82 offen lds\n"
MF "v[64:67],a[216:219],a[64:67],v[64:67]\n"
TR "a[212:213],v0 offset:20480\n"
MF "v[72:75],a[216:219],a[68:71],v[72:75]\n"
TR "a[214:215],v0 offset:20992\n"
MF "v[72:75],a[220:223],a[108:111],v[72:75]\n"
MF "v[80:83],a[220:223],a[112:115],v[80:83]\n"
LD "v11,s[28:31],s82 offen offset:1024 lds\n"
MF "v[80:83],a[216:219],a[72:75],v[80:83]\n"
MF "v[88:91],a[216:219],a[76:79],v[88:91]\n"
MF "v[88:91],a[220:223],a[116:119],v[88:91]\n"
MF "v[92:95],a[204:207],a[116:119],v[92:95]\n"
TR "a[216:217],v0 offset:16392\n"
MF "v[92:95],a[200:203],a[76:79],v[92:95]\n"
TR "a[218:219],v0 offset:16904\n"
MF "v[84:87],a[200:203],a[72:75],v[84:87]\n"
LD "v12,s[28:31],s82 offen offset:2048 lds\n"
MF "v[84:87],a[204:207],a[112:115],v[84:87]\n"
TR "a[220:221],v0 offset:20488\n"
MF "v[76:79],a[204:207],a[108:111],v[76:79]\n"
TR "a[222:223],v0 offset:21000\n"
MF "v[76:79],a[200:203],a[68:71],v[76:79]\n"
MF "v[68:71],a[200:203],a[64:67],v[68:71]\n"
MF "v[68:71],a[204:207],a[104:107],v[68:71]\n"
MF "v[60:63],a[204:207],a[100:103],v[60:63]\n"
LD "v13,s[28:31],s82 offen offset:3072 lds\n"
MF "v[60:63],a[200:203],a[60:63],v[60:63]\n"
"s_waitcnt lgkmcnt(4)\n"
MF "v[96:99],a[208:211],a[60:63],v[96:99]\n"
TR "a[200:201],v1 offset:16384\n"
MF "v[96:99],a[212:215],a[100:103],v[96:99]\n"
TR "a[202:203],v1 offset:16896\n"
MF "v[104:107],a[212:215],a[104:107],v[104:107]\n"
"s_add_u32 s14,s81,896\n"
"s_sub_u32 s15,s14,1280\n"
MF "v[104:107],a[208:211],a[64:67],v[104:107]\n"
"s_min_u32 s14,s14,s15\n"
"s_add_u32 s14,s14,s68\n"
TR "a[204:205],v1 offset:20480\n"
MF "v[112:115],a[208:211],a[68:71],v[112:115]\n"
"s_add_u32 s84,s14,0\n"
"s_add_u32 m0,s63,20480\n"
TR "a[206:207],v1 offset:20992\n"
MF "v[112:115],a[212:215],a[108:111],v[112:115]\n"
LD "v4,s[24:27],s84 offen lds\n"
MF "v[120:123],a[212:215],a[112:115],v[120:123]\n"
MF "v[120:123],a[208:211],a[72:75],v[120:123]\n"
MF "v[128:131],a[208:211],a[76:79],v[128:131]\n"
"s_add_u32 s84,s14,9216\n"
MF "v[128:131],a[212:215],a[116:119],v[128:131]\n"
"s_waitcnt lgkmcnt(4)\n"
LD "v5,s[24:27],s84 offen offset:1024 lds\n"
MF "v[132:135],a[220:223],a[116:119],v[132:135]\n"
TR "a[208:209],v1 offset:16392\n"
MF "v[132:135],a[216:219],a[76:79],v[132:135]\n"
TR "a[210:211],v1 offset:16904\n"
MF "v[124:127],a[216:219],a[72:75],v[124:127]\n"
MF "v[124:127],a[220:223],a[112:115],v[124:127]\n"
TR "a[212:213],v1 offset:20488\n"
MF "v[116:119],a[220:223],a[108:111],v[116:119]\n"
TR "a[214:215],v1 offset:21000\n"
MF "v[116:119],a[216:219],a[68:71],v[116:119]\n"
MF "v[108:111],a[216:219],a[64:67],v[108:111]\n"
MF "v[108:111],a[220:223],a[104:107],v[108:111]\n"
MF "v[100:103],a[220:223],a[100:103],v[100:103]\n"
"s_waitcnt vmcnt(23) lgkmcnt(0)\n"
MF "v[100:103],a[216:219],a[60:63],v[100:103]\n"
"s_barrier\n"
MF "v[136:139],a[200:203],a[60:63],v[136:139]\n"
TR "a[216:217],v0 offset:32768\n"
MF "v[136:139],a[204:207],a[100:103],v[136:139]\n"
"s_add_u32 m0,s62,16384\n"
TR "a[218:219],v0 offset:33280\n"
MF "v[144:147],a[204:207],a[104:107],v[144:147]\n"
"s_add_u32 s82,s66,4718720\n"
"ds_read_b128 a[140:143],v2 offset:40960\n"
MF "v[144:147],a[200:203],a[64:67],v[144:147]\n"
"s_sub_u32 s15,s82,7864320\n"
TR "a[220:221],v0 offset:36864\n"
MF "v[152:155],a[200:203],a[68:71],v[152:155]\n"
"s_min_u32 s82,s82,s15\n"
TR "a[222:223],v0 offset:37376\n"
MF "v[152:155],a[204:207],a[108:111],v[152:155]\n"
LD "v6,s[28:31],s82 offen lds\n"
MF "v[160:163],a[204:207],a[112:115],v[160:163]\n"
"ds_read_b128 a[180:183],v3 offset:40960\n"
MF "v[160:163],a[200:203],a[72:75],v[160:163]\n"
LD "v11,s[28:31],s82 offen offset:1024 lds\n"
MF "v[168:171],a[200:203],a[76:79],v[168:171]\n"
MF "v[168:171],a[204:207],a[116:119],v[168:171]\n"
LD "v12,s[28:31],s82 offen offset:2048 lds\n"
MF "v[172:175],a[212:215],a[116:119],v[172:175]\n"
TR "a[200:201],v0 offset:32776\n"
MF "v[172:175],a[208:211],a[76:79],v[172:175]\n"
TR "a[202:203],v0 offset:33288\n"
MF "v[164:167],a[208:211],a[72:75],v[164:167]\n"
"ds_read_b128 a[144:147],v2 offset:43008\n"
MF "v[164:167],a[212:215],a[112:115],v[164:167]\n"
TR "a[204:205],v0 offset:36872\n"
MF "v[156:159],a[212:215],a[108:111],v[156:159]\n"
TR "a[206:207],v0 offset:37384\n"
MF "v[156:159],a[208:211],a[68:71],v[156:159]\n"
MF "v[148:151],a[208:211],a[64:67],v[148:151]\n"
"ds_read_b128 a[184:187],v3 offset:43008\n"
MF "v[148:151],a[212:215],a[104:107],v[148:151]\n"
MF "v[140:143],a[212:215],a[100:103],v[140:143]\n"
LD "v13,s[28:31],s82 offen offset:3072 lds\n"
MF "v[140:143],a[208:211],a[60:63],v[140:143]\n"
"s_waitcnt lgkmcnt(7)\n"
MF "v[176:179],a[216:219],a[60:63],v[176:179]\n"
TR "a[208:209],v1 offset:32768\n"
MF "v[176:179],a[220:223],a[100:103],v[176:179]\n"
TR "a[210:211],v1 offset:33280\n"
MF "v[184:187],a[220:223],a[104:107],v[184:187]\n"
"ds_read_b128 a[148:151],v2 offset:45056\n"
MF "v[184:187],a[216:219],a[64:67],v[184:187]\n"
"s_add_u32 s84,s14,20480\n"
TR "a[212:213],v1 offset:36864\n"
MF "v[192:195],a[216:219],a[68:71],v[192:195]\n"
"s_add_u32 m0,s63,22528\n"
TR "a[214:215],v1 offset:37376\n"
MF "v[192:195],a[220:223],a[108:111],v[192:195]\n"
LD "v4,s[24:27],s84 offen lds\n"
MF "v[200:203],a[220:223],a[112:115],v[200:203]\n"
"ds_read_b128 a[188:191],v3 offset:45056\n"
MF "v[200:203],a[216:219],a[72:75],v[200:203]\n"
MF "v[208:211],a[216:219],a[76:79],v[208:211]\n"
"s_add_u32 s84,s14,29696\n"
MF "v[208:211],a[220:223],a[116:119],v[208:211]\n"
"s_waitcnt lgkmcnt(7)\n"
LD "v5,s[24:27],s84 offen offset:1024 lds\n"
MF "v[212:215],a[204:207],a[116:119],v[212:215]\n"
TR "a[216:217],v1 offset:32776\n"
MF "v[212:215],a[200:203],a[76:79],v[212:215]\n"
TR "a[218:219],v1 offset:33288\n"
MF "v[204:207],a[200:203],a[72:75],v[204:207]\n"
"ds_read_b128 a[152:155],v2 offset:47104\n"
MF "v[204:207],a[204:207],a[112:115],v[204:207]\n"
TR "a[220:221],v1 offset:36872\n"
MF "v[196:199],a[204:207],a[108:111],v[196:199]\n"
TR "a[222:223],v1 offset:37384\n"
MF "v[196:199],a[200:203],a[68:71],v[196:199]\n"
MF "v[188:191],a[200:203],a[64:67],v[188:191]\n"
"v_add_u32 v0,0xc000,v0\n"
MF "v[188:191],a[204:207],a[104:107],v[188:191]\n"
"v_add_u32 v1,0xc000,v1\n"
MF "v[180:183],a[204:207],a[100:103],v[180:183]\n"
"s_waitcnt vmcnt(25) lgkmcnt(0)\n"
MF "v[180:183],a[200:203],a[60:63],v[180:183]\n"
"s_barrier\n"
MF "v[216:219],a[208:211],a[60:63],v[216:219]\n"
TR "a[200:201],v0\n"
MF "v[216:219],a[212:215],a[100:103],v[216:219]\n"
"s_add_u32 m0,s62,32768\n"
TR "a[202:203],v0 offset:512\n"
MF "v[224:227],a[212:215],a[104:107],v[224:227]\n"
"s_add_u32 s82,s66,4718848\n"
"ds_read_b128 a[192:195],v3 offset:47104\n"
MF "v[224:227],a[208:211],a[64:67],v[224:227]\n"
"s_sub_u32 s15,s82,7864320\n"
TR "a[204:205],v0 offset:4096\n"
MF "v[232:235],a[208:211],a[68:71],v[232:235]\n"
"s_min_u32 s82,s82,s15\n"
TR "a[206:207],v0 offset:4608\n"
MF "v[232:235],a[212:215],a[108:111],v[232:235]\n"
LD "v6,s[28:31],s82 offen lds\n"
MF "v[240:243],a[212:215],a[112:115],v[240:243]\n"
"ds_read_b128 a[156:159],v2 offset:49152\n"
MF "v[240:243],a[208:211],a[72:75],v[240:243]\n"
"ds_read_b128 a[196:199],v3 offset:49152\n"
MF "v[248:251],a[208:211],a[76:79],v[248:251]\n"
LD "v11,s[28:31],s82 offen offset:1024 lds\n"
MF "v[248:251],a[212:215],a[116:119],v[248:251]\n"
MF "v[252:255],a[220:223],a[116:119],v[252:255]\n"
TR "a[208:209],v0 offset:8\n"
MF "v[252:255],a[216:219],a[76:79],v[252:255]\n"
TR "a[210:211],v0 offset:520\n"
MF "v[244:247],a[216:219],a[72:75],v[244:247]\n"
MF "v[244:247],a[220:223],a[112:115],v[244:247]\n"
TR "a[212:213],v0 offset:4104\n"
MF "v[236:239],a[220:223],a[108:111],v[236:239]\n"
TR "a[214:215],v0 offset:4616\n"
MF "v[236:239],a[216:219],a[68:71],v[236:239]\n"
LD "v12,s[28:31],s82 offen offset:2048 lds\n"
MF "v[228:231],a[216:219],a[64:67],v[228:231]\n"
MF "v[228:231],a[220:223],a[104:107],v[228:231]\n"
MF "v[220:223],a[220:223],a[100:103],v[220:223]\n"
MF "v[220:223],a[216:219],a[60:63],v[220:223]\n"
"s_waitcnt lgkmcnt(4)\n"
MF "v[16:19],a[200:203],a[140:143],v[16:19]\n"
TR "a[216:217],v1\n"
MF "v[16:19],a[204:207],a[180:183],v[16:19]\n"
TR "a[218:219],v1 offset:512\n"
MF "v[24:27],a[204:207],a[184:187],v[24:27]\n"
LD "v13,s[28:31],s82 offen offset:3072 lds\n"
MF "v[24:27],a[200:203],a[144:147],v[24:27]\n"
TR "a[220:221],v1 offset:4096\n"
MF "v[32:35],a[200:203],a[148:151],v[32:35]\n"
TR "a[222:223],v1 offset:4608\n"
MF "v[32:35],a[204:207],a[188:191],v[32:35]\n"
MF "v[40:43],a[204:207],a[192:195],v[40:43]\n"
MF "v[40:43],a[200:203],a[152:155],v[40:43]\n"
"s_add_u32 s84,s14,40960\n"
MF "v[48:51],a[200:203],a[156:159],v[48:51]\n"
"s_add_u32 m0,s63,24576\n"
MF "v[48:51],a[204:207],a[196:199],v[48:51]\n"
"s_waitcnt lgkmcnt(4)\n"
LD "v4,s[24:27],s84 offen lds\n"
MF "v[52:55],a[212:215],a[196:199],v[52:55]\n"
TR "a[200:201],v1 offset:8\n"
MF "v[52:55],a[208:211],a[156:159],v[52:55]\n"
TR "a[202:203],v1 offset:520\n"
MF "v[44:47],a[208:211],a[152:155],v[44:47]\n"
MF "v[44:47],a[212:215],a[192:195],v[44:47]\n"
TR "a[204:205],v1 offset:4104\n"
MF "v[36:39],a[212:215],a[188:191],v[36:39]\n"
TR "a[206:207],v1 offset:4616\n"
MF "v[36:39],a[208:211],a[148:151],v[36:39]\n"
MF "v[28:31],a[208:211],a[144:147],v[28:31]\n"
MF "v[28:31],a[212:215],a[184:187],v[28:31]\n"
MF "v[20:23],a[212:215],a[180:183],v[20:23]\n"
".Lpg6c:\n"
"s_cbranch_execz .Lpg1a\n"
".Lpg6f:\n"
"s_cbranch_execz .Lpgt0k_f\n"
"s_waitcnt vmcnt(24) lgkmcnt(0)\n"
MF "v[20:23],a[208:211],a[140:143],v[20:23]\n"
"s_barrier\n"
MF "v[56:59],a[216:219],a[140:143],v[56:59]\n"
"s_add_u32 m0,s62,49152\n"
"s_add_u32 s82,s66,5505024\n"
TR "a[208:209],v0 offset:16384\n"
MF "v[56:59],a[220:223],a[180:183],v[56:59]\n"
"s_sub_u32 s15,s82,7864320\n"
"s_min_u32 s82,s82,s15\n"
TR "a[210:211],v0 offset:16896\n"
MF "v[64:67],a[220:223],a[184:187],v[64:67]\n"
LD "v6,s[28:31],s82 offen lds\n"
MF "v[64:67],a[216:219],a[144:147],v[64:67]\n"
TR "a[212:213],v0 offset:20480\n"
MF "v[72:75],a[216:219],a[148:151],v[72:75]\n"
TR "a[214:215],v0 offset:20992\n"
MF "v[72:75],a[220:223],a[188:191],v[72:75]\n"
MF "v[80:83],a[220:223],a[192:195],v[80:83]\n"
LD "v11,s[28:31],s82 offen offset:1024 lds\n"
MF "v[80:83],a[216:219],a[152:155],v[80:83]\n"
MF "v[88:91],a[216:219],a[156:159],v[88:91]\n"
MF "v[88:91],a[220:223],a[196:199],v[88:91]\n"
MF "v[92:95],a[204:207],a[196:199],v[92:95]\n"
TR "a[216:217],v0 offset:16392\n"
MF "v[92:95],a[200:203],a[156:159],v[92:95]\n"
TR "a[218:219],v0 offset:16904\n"
MF "v[84:87],a[200:203],a[152:155],v[84:87]\n"
LD "v12,s[28:31],s82 offen offset:2048 lds\n"
MF "v[84:87],a[204:207],a[192:195],v[84:87]\n"
TR "a[220:221],v0 offset:20488\n"
MF "v[76:79],a[204:207],a[188:191],v[76:79]\n"
TR "a[222:223],v0 offset:21000\n"
MF "v[76:79],a[200:203],a[148:151],v[76:79]\n"
MF "v[68:71],a[200:203],a[144:147],v[68:71]\n"
MF "v[68:71],a[204:207],a[184:187],v[68:71]\n"
MF "v[60:63],a[204:207],a[180:183],v[60:63]\n"
LD "v13,s[28:31],s82 offen offset:3072 lds\n"
MF "v[60:63],a[200:203],a[140:143],v[60:63]\n"
"s_waitcnt lgkmcnt(4)\n"
MF "v[96:99],a[208:211],a[140:143],v[96:99]\n"
TR "a[200:201],v1 offset:16384\n"
MF "v[96:99],a[212:215],a[180:183],v[96:99]\n"
TR "a[202:203],v1 offset:16896\n"
MF "v[104:107],a[212:215],a[184:187],v[104:107]\n"
"s_add_u32 s14,s81,1024\n"
"s_sub_u32 s15,s14,1280\n"
MF "v[104:107],a[208:211],a[144:147],v[104:107]\n"
"s_min_u32 s14,s14,s15\n"
"s_add_u32 s14,s14,s68\n"
TR "a[204:205],v1 offset:20480\n"
MF "v[112:115],a[208:211],a[148:151],v[112:115]\n"
"s_add_u32 s84,s14,0\n"
"s_add_u32 m0,s63,40960\n"
TR "a[206:207],v1 offset:20992\n"
MF "v[112:115],a[212:215],a[188:191],v[112:115]\n"
LD "v4,s[24:27],s84 offen lds\n"
MF "v[120:123],a[212:215],a[192:195],v[120:123]\n"
MF "v[120:123],a[208:211],a[152:155],v[120:123]\n"
MF "v[128:131],a[208:211],a[156:159],v[128:131]\n"
"s_add_u32 s84,s14,9216\n"
MF "v[128:131],a[212:215],a[196:199],v[128:131]\n"
"s_waitcnt lgkmcnt(4)\n"
LD "v5,s[24:27],s84 offen offset:1024 lds\n"
MF "v[132:135],a[220:223],a[196:199],v[132:135]\n"
TR "a[208:209],v1 offset:16392\n"
MF "v[132:135],a[216:219],a[156:159],v[132:135]\n"
TR "a[210:211],v1 offset:16904\n"
MF "v[124:127],a[216:219],a[152:155],v[124:127]\n"
MF "v[124:127],a[220:223],a[192:195],v[124:127]\n"
TR "a[212:213],v1 offset:20488\n"
MF "v[116:119],a[220:223],a[188:191],v[116:119]\n"
TR "a[214:215],v1 offset:21000\n"
MF "v[116:119],a[216:219],a[148:151],v[116:119]\n"
MF "v[108:111],a[216:219],a[144:147],v[108:111]\n"
MF "v[108:111],a[220:223],a[184:187],v[108:111]\n"
MF "v[100:103],a[220:223],a[180:183],v[100:103]\n"
"s_waitcnt vmcnt(23) lgkmcnt(0)\n"
MF "v[100:103],a[216:219],a[140:143],v[100:103]\n"
"s_barrier\n"
"s_cmp_eq_u32 s57,1\n"
"s_cbranch_scc0 .Lhs12\n"
"v_bfe_u32 v14,v9,4,5\n"
"v_lshlrev_b32 v14,6,v14\n"
"s_lshl_b32 s58,s78,2\n"
"s_add_u32 s58,s58,s86\n"
"buffer_load_dword v10,v14,s[40:43],s58 offen offset:128 sc1\n"
".Lhs12:\n"
MF "v[136:139],a[200:203],a[140:143],v[136:139]\n"
TR "a[216:217],v0 offset:32768\n"
MF "v[136:139],a[204:207],a[180:183],v[136:139]\n"
"s_add_u32 m0,s62,65536\n"
TR "a[218:219],v0 offset:33280\n"
MF "v[144:147],a[204:207],a[184:187],v[144:147]\n"
"s_add_u32 s82,s66,5505152\n"
"ds_read_b128 a[60:63],v2\n"
MF "v[144:147],a[200:203],a[144:147],v[144:147]\n"
"s_sub_u32 s15,s82,7864320\n"
TR "a[220:221],v0 offset:36864\n"
MF "v[152:155],a[200:203],a[148:151],v[152:155]\n"
"s_min_u32 s82,s82,s15\n"
TR "a[222:223],v0 offset:37376\n"
MF "v[152:155],a[204:207],a[188:191],v[152:155]\n"
LD "v6,s[28:31],s82 offen lds\n"
MF "v[160:163],a[204:207],a[192:195],v[160:163]\n"
"ds_read_b128 a[100:103],v3\n"
MF "v[160:163],a[200:203],a[152:155],v[160:163]\n"
LD "v11,s[28:31],s82 offen offset:1024 lds\n"
MF "v[168:171],a[200:203],a[156:159],v[168:171]\n"
MF "v[168:171],a[204:207],a[196:199],v[168:171]\n"
LD "v12,s[28:31],s82 offen offset:2048 lds\n"
MF "v[172:175],a[212:215],a[196:199],v[172:175]\n"
TR "a[200:201],v0 offset:32776\n"
MF "v[172:175],a[208:211],a[156:159],v[172:175]\n"
TR "a[202:203],v0 offset:33288\n"
MF "v[164:167],a[208:211],a[152:155],v[164:167]\n"
"ds_read_b128 a[64:67],v2 offset:2048\n"
MF "v[164:167],a[212:215],a[192:195],v[164:167]\n"
TR "a[204:205],v0 offset:36872\n"
MF "v[156:159],a[212:215],a[188:191],v[156:159]\n"
TR "a[206:207],v0 offset:37384\n"
MF "v[156:159],a[208:211],a[148:151],v[156:159]\n"
MF "v[148:151],a[208:211],a[144:147],v[148:151]\n"
"ds_read_b128 a[104:107],v3 offset:2048\n"
MF "v[148:151],a[212:215],a[184:187],v[148:151]\n"
MF "v[140:143],a[212:215],a[180:183],v[140:143]\n"
LD "v13,s[28:31],s82 offen offset:3072 lds\n"
MF "v[140:143],a[208:211],a[140:143],v[140:143]\n"
"s_waitcnt lgkmcnt(7)\n"
MF "v[176:179],a[216:219],a[140:143],v[176:179]\n"
TR "a[208:209],v1 offset:32768\n"
MF "v[176:179],a[220:223],a[180:183],v[176:179]\n"
TR "a[210:211],v1 offset:33280\n"
MF "v[184:187],a[220:223],a[184:187],v[184:187]\n"
"ds_read_b128 a[68:71],v2 offset:4096\n"
MF "v[184:187],a[216:219],a[144:147],v[184:187]\n"
"s_add_u32 s84,s14,20480\n"
TR "a[212:213],v1 offset:36864\n"
MF "v[192:195],a[216:219],a[148:151],v[192:195]\n"
"s_add_u32 m0,s63,43008\n"
TR "a[214:215],v1 offset:37376\n"
MF "v[192:195],a[220:223],a[188:191],v[192:195]\n"
LD "v4,s[24:27],s84 offen lds\n"
MF "v[200:203],a[220:223],a[192:195],v[200:203]\n"
"ds_read_b128 a[108:111],v3 offset:4096\n"
MF "v[200:203],a[216:219],a[152:155],v[200:203]\n"
MF "v[208:211],a[216:219],a[156:159],v[208:211]\n"
"s_add_u32 s84,s14,29696\n"
MF "v[208:211],a[220:223],a[196:199],v[208:211]\n"
"s_waitcnt lgkmcnt(7)\n"
LD "v5,s[24:27],s84 offen offset:1024 lds\n"
MF "v[212:215],a[204:207],a[196:199],v[212:215]\n"
TR "a[216:217],v1 offset:32776\n"
MF "v[212:215],a[200:203],a[156:159],v[212:215]\n"
TR "a[218:219],v1 offset:33288\n"
MF "v[204:207],a[200:203],a[152:155],v[204:207]\n"
"ds_read_b128 a[72:75],v2 offset:6144\n"
MF "v[204:207],a[204:207],a[192:195],v[204:207]\n"
TR "a[220:221],v1 offset:36872\n"
MF "v[196:199],a[204:207],a[188:191],v[196:199]\n"
TR "a[222:223],v1 offset:37384\n"
MF "v[196:199],a[200:203],a[148:151],v[196:199]\n"
MF "v[188:191],a[200:203],a[144:147],v[188:191]\n"
"v_add_u32 v0,0xffff4000,v0\n"
MF "v[188:191],a[204:207],a[184:187],v[188:191]\n"
"v_add_u32 v1,0xffff4000,v1\n"
MF "v[180:183],a[204:207],a[180:183],v[180:183]\n"
"s_waitcnt vmcnt(25) lgkmcnt(0)\n"
MF "v[180:183],a[200:203],a[140:143],v[180:183]\n"
"s_barrier\n"
MF "v[216:219],a[208:211],a[140:143],v[216:219]\n"
TR "a[200:201],v0\n"
MF "v[216:219],a[212:215],a[180:183],v[216:219]\n"
"s_add_u32 m0,s62,81920\n"
TR "a[202:203],v0 offset:512\n"
MF "v[224:227],a[212:215],a[184:187],v[224:227]\n"
"s_add_u32 s82,s66,5505280\n"
"ds_read_b128 a[112:115],v3 offset:6144\n"
MF "v[224:227],a[208:211],a[144:147],v[224:227]\n"
"s_sub_u32 s15,s82,7864320\n"
TR "a[204:205],v0 offset:4096\n"
MF "v[232:235],a[208:211],a[148:151],v[232:235]\n"
"s_min_u32 s82,s82,s15\n"
TR "a[206:207],v0 offset:4608\n"
MF "v[232:235],a[212:215],a[188:191],v[232:235]\n"
LD "v6,s[28:31],s82 offen lds\n"
MF "v[240:243],a[212:215],a[192:195],v[240:243]\n"
"ds_read_b128 a[76:79],v2 offset:8192\n"
MF "v[240:243],a[208:211],a[152:155],v[240:243]\n"
"ds_read_b128 a[116:119],v3 offset:8192\n"
MF "v[248:251],a[208:211],a[156:159],v[248:251]\n"
LD "v11,s[28:31],s82 offen offset:1024 lds\n"
MF "v[248:251],a[212:215],a[196:199],v[248:251]\n"
MF "v[252:255],a[220:223],a[196:199],v[252:255]\n"
TR "a[208:209],v0 offset:8\n"
MF "v[252:255],a[216:219],a[156:159],v[252:255]\n"
TR "a[210:211],v0 offset:520\n"
MF "v[244:247],a[216:219],a[152:155],v[244:247]\n"
MF "v[244:247],a[220:223],a[192:195],v[244:247]\n"
TR "a[212:213],v0 offset:4104\n"
MF "v[236:239],a[220:223],a[188:191],v[236:239]\n"
TR "a[214:215],v0 offset:4616\n"
MF "v[236:239],a[216:219],a[148:151],v[236:239]\n"
LD "v12,s[28:31],s82 offen offset:2048 lds\n"
MF "v[228:231],a[216:219],a[144:147],v[228:231]\n"
MF "v[228:231],a[220:223],a[184:187],v[228:231]\n"
MF "v[220:223],a[220:223],a[180:183],v[220:223]\n"
MF "v[220:223],a[216:219],a[140:143],v[220:223]\n"
"s_waitcnt lgkmcnt(4)\n"
MF "v[16:19],a[200:203],a[60:63],v[16:19]\n"
TR "a[216:217],v1\n"
MF "v[16:19],a[204:207],a[100:103],v[16:19]\n"
TR "a[218:219],v1 offset:512\n"
MF "v[24:27],a[204:207],a[104:107],v[24:27]\n"
LD "v13,s[28:31],s82 offen offset:3072 lds\n"
MF "v[24:27],a[200:203],a[64:67],v[24:27]\n"
TR "a[220:221],v1 offset:4096\n"
MF "v[32:35],a[200:203],a[68:71],v[32:35]\n"
TR "a[222:223],v1 offset:4608\n"
MF "v[32:35],a[204:207],a[108:111],v[32:35]\n"
MF "v[40:43],a[204:207],a[112:115],v[40:43]\n"
MF "v[40:43],a[200:203],a[72:75],v[40:43]\n"
"s_add_u32 s84,s14,40960\n"
MF "v[48:51],a[200:203],a[76:79],v[48:51]\n"
"s_add_u32 m0,s63,45056\n"
MF "v[48:51],a[204:207],a[116:119],v[48:51]\n"
"s_waitcnt lgkmcnt(4)\n"
LD "v4,s[24:27],s84 offen lds\n"
MF "v[52:55],a[212:215],a[116:119],v[52:55]\n"
TR "a[200:201],v1 offset:8\n"
MF "v[52:55],a[208:211],a[76:79],v[52:55]\n"
TR "a[202:203],v1 offset:520\n"
MF "v[44:47],a[208:211],a[72:75],v[44:47]\n"
MF "v[44:47],a[212:215],a[112:115],v[44:47]\n"
TR "a[204:205],v1 offset:4104\n"
MF "v[36:39],a[212:215],a[108:111],v[36:39]\n"
TR "a[206:207],v1 offset:4616\n"
MF "v[36:39],a[208:211],a[68:71],v[36:39]\n"
MF "v[28:31],a[208:211],a[64:67],v[28:31]\n"
MF "v[28:31],a[212:215],a[104:107],v[28:31]\n"
MF "v[20:23],a[212:215],a[100:103],v[20:23]\n"
"s_waitcnt vmcnt(24) lgkmcnt(0)\n"
MF "v[20:23],a[208:211],a[60:63],v[20:23]\n"
"s_barrier\n"
MF "v[56:59],a[216:219],a[60:63],v[56:59]\n"
"s_add_u32 m0,s62,0\n"
"s_add_u32 s82,s66,6291712\n"
TR "a[208:209],v0 offset:16384\n"
MF "v[56:59],a[220:223],a[100:103],v[56:59]\n"
"s_sub_u32 s15,s82,7864320\n"
"s_min_u32 s82,s82,s15\n"
TR "a[210:211],v0 offset:16896\n"
MF "v[64:67],a[220:223],a[104:107],v[64:67]\n"
LD "v6,s[28:31],s82 offen lds\n"
MF "v[64:67],a[216:219],a[64:67],v[64:67]\n"
TR "a[212:213],v0 offset:20480\n"
MF "v[72:75],a[216:219],a[68:71],v[72:75]\n"
TR "a[214:215],v0 offset:20992\n"
MF "v[72:75],a[220:223],a[108:111],v[72:75]\n"
MF "v[80:83],a[220:223],a[112:115],v[80:83]\n"
LD "v11,s[28:31],s82 offen offset:1024 lds\n"
MF "v[80:83],a[216:219],a[72:75],v[80:83]\n"
MF "v[88:91],a[216:219],a[76:79],v[88:91]\n"
MF "v[88:91],a[220:223],a[116:119],v[88:91]\n"
MF "v[92:95],a[204:207],a[116:119],v[92:95]\n"
TR "a[216:217],v0 offset:16392\n"
MF "v[92:95],a[200:203],a[76:79],v[92:95]\n"
TR "a[218:219],v0 offset:16904\n"
MF "v[84:87],a[200:203],a[72:75],v[84:87]\n"
LD "v12,s[28:31],s82 offen offset:2048 lds\n"
MF "v[84:87],a[204:207],a[112:115],v[84:87]\n"
TR "a[220:221],v0 offset:20488\n"
MF "v[76:79],a[204:207],a[108:111],v[76:79]\n"
TR "a[222:223],v0 offset:21000\n"
MF "v[76:79],a[200:203],a[68:71],v[76:79]\n"
MF "v[68:71],a[200:203],a[64:67],v[68:71]\n"
LD "v13,s[28:31],s82 offen offset:3072 lds\n"
MF "v[68:71],a[204:207],a[104:107],v[68:71]\n"
MF "v[60:63],a[204:207],a[100:103],v[60:63]\n"
MF "v[60:63],a[200:203],a[60:63],v[60:63]\n"
"s_add_u32 s14,s81,1152\n"
"s_sub_u32 s15,s14,1280\n"
"s_waitcnt lgkmcnt(4)\n"
MF "v[96:99],a[208:211],a[60:63],v[96:99]\n"
"s_min_u32 s14,s14,s15\n"
"s_add_u32 s14,s14,s68\n"
TR "a[200:201],v1 offset:16384\n"
MF "v[96:99],a[212:215],a[100:103],v[96:99]\n"
"s_add_u32 s84,s14,0\n"
"s_add_u32 m0,s63,0\n"
TR "a[202:203],v1 offset:16896\n"
MF "v[104:107],a[212:215],a[104:107],v[104:107]\n"
LD "v4,s[24:27],s84 offen lds\n"
MF "v[104:107],a[208:211],a[64:67],v[104:107]\n"
TR "a[204:205],v1 offset:20480\n"
MF "v[112:115],a[208:211],a[68:71],v[112:115]\n"
TR "a[206:207],v1 offset:20992\n"
MF "v[112:115],a[212:215],a[108:111],v[112:115]\n"
"s_add_u32 s84,s14,9216\n"
MF "v[120:123],a[212:215],a[112:115],v[120:123]\n"
LD "v5,s[24:27],s84 offen offset:1024 lds\n"
MF "v[120:123],a[208:211],a[72:75],v[120:123]\n"
MF "v[128:131],a[208:211],a[76:79],v[128:131]\n"
MF "v[128:131],a[212:215],a[116:119],v[128:131]\n"
"s_waitcnt lgkmcnt(4)\n"
MF "v[132:135],a[220:223],a[116:119],v[132:135]\n"
TR "a[208:209],v1 offset:16392\n"
MF "v[132:135],a[216:219],a[76:79],v[132:135]\n"
"s_add_u32 s84,s14,18432\n"
TR "a[210:211],v1 offset:16904\n"
MF "v[124:127],a[216:219],a[72:75],v[124:127]\n"
LD "v4,s[24:27],s84 offen offset:2048 lds\n"
MF "v[124:127],a[220:223],a[112:115],v[124:127]\n"
TR "a[212:213],v1 offset:20488\n"
MF "v[116:119],a[220:223],a[108:111],v[116:119]\n"
TR "a[214:215],v1 offset:21000\n"
MF "v[116:119],a[216:219],a[68:71],v[116:119]\n"
MF "v[108:111],a[216:219],a[64:67],v[108:111]\n"
MF "v[108:111],a[220:223],a[104:107],v[108:111]\n"
MF "v[100:103],a[220:223],a[100:103],v[100:103]\n"
"s_waitcnt vmcnt(24) lgkmcnt(0)\n"
MF "v[100:103],a[216:219],a[60:63],v[100:103]\n"
"s_barrier\n"
MF "v[136:139],a[200:203],a[60:63],v[136:139]\n"
TR "a[216:217],v0 offset:32768\n"
MF "v[136:139],a[204:207],a[100:103],v[136:139]\n"
"s_add_u32 m0,s62,16384\n"
TR "a[218:219],v0 offset:33280\n"
MF "v[144:147],a[204:207],a[104:107],v[144:147]\n"
"s_add_u32 s82,s66,7078144\n"
"ds_read_b128 a[140:143],v2 offset:20480\n"
MF "v[144:147],a[200:203],a[64:67],v[144:147]\n"
"s_sub_u32 s15,s82,7864320\n"
TR "a[220:221],v0 offset:36864\n"
MF "v[152:155],a[200:203],a[68:71],v[152:155]\n"
"s_min_u32 s82,s82,s15\n"
TR "a[222:223],v0 offset:37376\n"
MF "v[152:155],a[204:207],a[108:111],v[152:155]\n"
LD "v6,s[28:31],s82 offen lds\n"
MF "v[160:163],a[204:207],a[112:115],v[160:163]\n"
"ds_read_b128 a[180:183],v3 offset:20480\n"
MF "v[160:163],a[200:203],a[72:75],v[160:163]\n"
LD "v11,s[28:31],s82 offen offset:1024 lds\n"
MF "v[168:171],a[200:203],a[76:79],v[168:171]\n"
MF "v[168:171],a[204:207],a[116:119],v[168:171]\n"
LD "v12,s[28:31],s82 offen offset:2048 lds\n"
MF "v[172:175],a[212:215],a[116:119],v[172:175]\n"
TR "a[200:201],v0 offset:32776\n"
MF "v[172:175],a[208:211],a[76:79],v[172:175]\n"
TR "a[202:203],v0 offset:33288\n"
MF "v[164:167],a[208:211],a[72:75],v[164:167]\n"
"ds_read_b128 a[144:147],v2 offset:22528\n"
MF "v[164:167],a[212:215],a[112:115],v[164:167]\n"
TR "a[204:205],v0 offset:36872\n"
MF "v[156:159],a[212:215],a[108:111],v[156:159]\n"
TR "a[206:207],v0 offset:37384\n"
MF "v[156:159],a[208:211],a[68:71],v[156:159]\n"
MF "v[148:151],a[208:211],a[64:67],v[148:151]\n"
"ds_read_b128 a[184:187],v3 offset:22528\n"
MF "v[148:151],a[212:215],a[104:107],v[148:151]\n"
MF "v[140:143],a[212:215],a[100:103],v[140:143]\n"
LD "v13,s[28:31],s82 offen offset:3072 lds\n"
MF "v[140:143],a[208:211],a[60:63],v[140:143]\n"
"s_waitcnt lgkmcnt(7)\n"
MF "v[176:179],a[216:219],a[60:63],v[176:179]\n"
TR "a[208:209],v1 offset:32768\n"
MF "v[176:179],a[220:223],a[100:103],v[176:179]\n"
TR "a[210:211],v1 offset:33280\n"
MF "v[184:187],a[220:223],a[104:107],v[184:187]\n"
"ds_read_b128 a[148:151],v2 offset:24576\n"
MF "v[184:187],a[216:219],a[64:67],v[184:187]\n"
"s_add_u32 s84,s14,30720\n"
TR "a[212:213],v1 offset:36864\n"
MF "v[192:195],a[216:219],a[68:71],v[192:195]\n"
"s_add_u32 m0,s63,3072\n"
TR "a[214:215],v1 offset:37376\n"
MF "v[192:195],a[220:223],a[108:111],v[192:195]\n"
LD "v5,s[24:27],s84 offen lds\n"
MF "v[200:203],a[220:223],a[112:115],v[200:203]\n"
"ds_read_b128 a[188:191],v3 offset:24576\n"
MF "v[200:203],a[216:219],a[72:75],v[200:203]\n"
MF "v[208:211],a[216:219],a[76:79],v[208:211]\n"
"s_add_u32 s84,s14,39936\n"
MF "v[208:211],a[220:223],a[116:119],v[208:211]\n"
"s_waitcnt lgkmcnt(7)\n"
LD "v4,s[24:27],s84 offen offset:1024 lds\n"
MF "v[212:215],a[204:207],a[116:119],v[212:215]\n"
TR "a[216:217],v1 offset:32776\n"
MF "v[212:215],a[200:203],a[76:79],v[212:215]\n"
TR "a[218:219],v1 offset:33288\n"
MF "v[204:207],a[200:203],a[72:75],v[204:207]\n"
"ds_read_b128 a[152:155],v2 offset:26624\n"
MF "v[204:207],a[204:207],a[112:115],v[204:207]\n"
TR "a[220:221],v1 offset:36872\n"
MF "v[196:199],a[204:207],a[108:111],v[196:199]\n"
TR "a[222:223],v1 offset:37384\n"
MF "v[196:199],a[200:203],a[68:71],v[196:199]\n"
MF "v[188:191],a[200:203],a[64:67],v[188:191]\n"
"v_add_u32 v0,0xc000,v0\n"
MF "v[188:191],a[204:207],a[104:107],v[188:191]\n"
"v_add_u32 v1,0xc000,v1\n"
MF "v[180:183],a[204:207],a[100:103],v[180:183]\n"
"s_waitcnt vmcnt(26) lgkmcnt(0)\n"
".Lpgqj_a:\n"
"s_cbranch_execz .Lisr1\n"
".Lqj:\n"
MF "v[180:183],a[200:203],a[60:63],v[180:183]\n"
"s_barrier\n"
"s_getpc_b64 s[52:53]\n"
".Lctq:\n"
"s_sub_u32 s52,s52,.Lctq-.Lcode0\n"
"s_subb_u32 s53,s53,0\n"
"s_bfe_u32 s58,s23,0x50000\n"
"s_lshl_b32 s58,s58,2\n"
"s_add_u32 s58,s58,s56\n"
"s_nop 0\n"
"s_mul_i32 s58,s58,1280\n"
"v_min_u32 v15,144,v9\n"
"v_lshl_add_u32 v15,v15,3,s58\n"
"v_min_u32 v15,.Lcode1-.Lcode0,v15\n"
"global_load_dword a249,v15,s[52:53]\n"
MF "v[216:219],a[208:211],a[60:63],v[216:219]\n"
TR "a[200:201],v0\n"
MF "v[216:219],a[212:215],a[100:103],v[216:219]\n"
"s_add_u32 m0,s62,32768\n"
TR "a[202:203],v0 offset:512\n"
MF "v[224:227],a[212:215],a[104:107],v[224:227]\n"
"s_add_u32 s82,s66,6291584\n"
"ds_read_b128 a[192:195],v3 offset:26624\n"
MF "v[224:227],a[208:211],a[64:67],v[224:227]\n"
"s_sub_u32 s15,s82,7864320\n"
TR "a[204:205],v0 offset:4096\n"
MF "v[232:235],a[208:211],a[68:71],v[232:235]\n"
"s_min_u32 s82,s82,s15\n"
TR "a[206:207],v0 offset:4608\n"
MF "v[232:235],a[212:215],a[108:111],v[232:235]\n"
LD "v6,s[28:31],s82 offen lds\n"
MF "v[240:243],a[212:215],a[112:115],v[240:243]\n"
"ds_read_b128 a[156:159],v2 offset:28672\n"
MF "v[240:243],a[208:211],a[72:75],v[240:243]\n"
"ds_read_b128 a[196:199],v3 offset:28672\n"
MF "v[248:251],a[208:211],a[76:79],v[248:251]\n"
MF "v[248:251],a[212:215],a[116:119],v[248:251]\n"
LD "v11,s[28:31],s82 offen offset:1024 lds\n"
MF "v[252:255],a[220:223],a[116:119],v[252:255]\n"
TR "a[208:209],v0 offset:8\n"
MF "v[252:255],a[216:219],a[76:79],v[252:255]\n"
TR "a[210:211],v0 offset:520\n"
MF "v[244:247],a[216:219],a[72:75],v[244:247]\n"
MF "v[244:247],a[220:223],a[112:115],v[244:247]\n"
TR "a[212:213],v0 offset:4104\n"
MF "v[236:239],a[220:223],a[108:111],v[236:239]\n"
TR "a[214:215],v0 offset:4616\n"
MF "v[236:239],a[216:219],a[68:71],v[236:239]\n"
MF "v[228:231],a[216:219],a[64:67],v[228:231]\n"
MF "v[228:231],a[220:223],a[104:107],v[228:231]\n"
MF "v[220:223],a[220:223],a[100:103],v[220:223]\n"
LD "v12,s[28:31],s82 offen offset:2048 lds\n"
MF "v[220:223],a[216:219],a[60:63],v[220:223]\n"
"s_waitcnt lgkmcnt(4)\n"
MF "v[16:19],a[200:203],a[140:143],v[16:19]\n"
TR "a[216:217],v1\n"
MF "v[16:19],a[204:207],a[180:183],v[16:19]\n"
TR "a[218:219],v1 offset:512\n"
MF "v[24:27],a[204:207],a[184:187],v[24:27]\n"
MF "v[24:27],a[200:203],a[144:147],v[24:27]\n"
TR "a[220:221],v1 offset:4096\n"
MF "v[32:35],a[200:203],a[148:151],v[32:35]\n"
TR "a[222:223],v1 offset:4608\n"
MF "v[32:35],a[204:207],a[188:191],v[32:35]\n"
MF "v[40:43],a[204:207],a[192:195],v[40:43]\n"
MF "v[40:43],a[200:203],a[152:155],v[40:43]\n"
LD "v13,s[28:31],s82 offen offset:3072 lds\n"
MF "v[48:51],a[200:203],a[156:159],v[48:51]\n"
"s_memtime s[52:53]\n"
"s_memrealtime s[100:101]\n"
MF "v[48:51],a[204:207],a[196:199],v[48:51]\n"
"s_waitcnt lgkmcnt(4)\n"
MF "v[52:55],a[212:215],a[196:199],v[52:55]\n"
TR "a[200:201],v1 offset:8\n"
MF "v[52:55],a[208:211],a[156:159],v[52:55]\n"
TR "a[202:203],v1 offset:520\n"
MF "v[44:47],a[208:211],a[152:155],v[44:47]\n"
MF "v[44:47],a[212:215],a[192:195],v[44:47]\n"
TR "a[204:205],v1 offset:4104\n"
MF "v[36:39],a[212:215],a[188:191],v[36:39]\n"
TR "a[206:207],v1 offset:4616\n"
MF "v[36:39],a[208:211],a[148:151],v[36:39]\n"
MF "v[28:31],a[208:211],a[144:147],v[28:31]\n"
MF "v[28:31],a[212:215],a[184:187],v[28:31]\n"
MF "v[20:23],a[212:215],a[180:183],v[20:23]\n"
"s_waitcnt vmcnt(25) lgkmcnt(0)\n"
MF "v[20:23],a[208:211],a[140:143],v[20:23]\n"
"s_barrier\n"
MF "v[56:59],a[216:219],a[140:143],v[56:59]\n"
"s_add_u32 m0,s62,49152\n"
"s_add_u32 s82,s66,7078016\n"
TR "a[208:209],v0 offset:16384\n"
MF "v[56:59],a[220:223],a[180:183],v[56:59]\n"
"s_sub_u32 s15,s82,7864320\n"
"s_min_u32 s82,s82,s15\n"
TR "a[210:211],v0 offset:16896\n"
MF "v[64:67],a[220:223],a[184:187],v[64:67]\n"
LD "v6,s[28:31],s82 offen lds\n"
MF "v[64:67],a[216:219],a[144:147],v[64:67]\n"
"s_sub_u32 s53,s52,s80\n"
TR "a[212:213],v0 offset:20480\n"
"v_lshrrev_b32 v15,8,v9\n"
MF "v[72:75],a[216:219],a[148:151],v[72:75]\n"
TR "a[214:215],v0 offset:20992\n"
"v_mul_u32_u24 v15,48960,v15\n"
MF "v[72:75],a[220:223],a[188:191],v[72:75]\n"
"s_lshr_b32 s7,s56,1\n"
"v_add_u32 v15,v15,v9\n"
MF "v[80:83],a[220:223],a[192:195],v[80:83]\n"
"s_and_b32 s8,s56,1\n"
MF "v[80:83],a[216:219],a[152:155],v[80:83]\n"
"s_sub_u32 s7,s7,s8\n"
MF "v[88:91],a[216:219],a[156:159],v[88:91]\n"
"s_mul_i32 s7,s7,245760\n"
MF "v[88:91],a[220:223],a[196:199],v[88:91]\n"
LD "v11,s[28:31],s82 offen offset:1024 lds\n"
MF "v[92:95],a[204:207],a[196:199],v[92:95]\n"
"s_add_u32 s7,s7,s89\n"
TR "a[216:217],v0 offset:16392\n"
MF "v[92:95],a[200:203],a[156:159],v[92:95]\n"
"s_cmp_gt_u32 s53,24100\n"
"s_cselect_b32 s101,1,0\n"
TR "a[218:219],v0 offset:16904\n"
MF "v[84:87],a[200:203],a[152:155],v[84:87]\n"
"s_cmp_eq_u32 s87,2\n"
"s_cselect_b32 s87,s101,s87\n"
MF "v[84:87],a[204:207],a[192:195],v[84:87]\n"
TR "a[220:221],v0 offset:20488\n"
MF "v[76:79],a[204:207],a[188:191],v[76:79]\n"
TR "a[222:223],v0 offset:21000\n"
MF "v[76:79],a[200:203],a[148:151],v[76:79]\n"
"s_cmp_gt_u32 s87,s57\n"
"s_cselect_b32 s97,130,0\n"
"s_cselect_b32 s95,0,0x47800000\n"
MF "v[68:71],a[200:203],a[144:147],v[68:71]\n"
"s_mov_b32 s21,s100\n"
MF "v[68:71],a[204:207],a[184:187],v[68:71]\n"
"s_mov_b32 s77,s95\n"
MF "v[60:63],a[204:207],a[180:183],v[60:63]\n"
LD "v12,s[28:31],s82 offen offset:2048 lds\n"
MF "v[60:63],a[200:203],a[140:143],v[60:63]\n"
"s_waitcnt lgkmcnt(4)\n"
MF "v[96:99],a[208:211],a[140:143],v[96:99]\n"
".Lpg7a:\n"
"s_cbranch_execz .Lisr1\n"
TR "a[200:201],v1 offset:16384\n"
MF "v[96:99],a[212:215],a[180:183],v[96:99]\n"
TR "a[202:203],v1 offset:16896\n"
MF "v[104:107],a[212:215],a[184:187],v[104:107]\n"
MF "v[104:107],a[208:211],a[144:147],v[104:107]\n"
TR "a[204:205],v1 offset:20480\n"
MF "v[112:115],a[208:211],a[148:151],v[112:115]\n"
TR "a[206:207],v1 offset:20992\n"
MF "v[112:115],a[212:215],a[188:191],v[112:115]\n"
MF "v[120:123],a[212:215],a[192:195],v[120:123]\n"
MF "v[120:123],a[208:211],a[152:155],v[120:123]\n"
LD "v13,s[28:31],s82 offen offset:3072 lds\n"
MF "v[128:131],a[208:211],a[156:159],v[128:131]\n"
MF "v[128:131],a[212:215],a[196:199],v[128:131]\n"
"s_waitcnt lgkmcnt(4)\n"
MF "v[132:135],a[220:223],a[196:199],v[132:135]\n"
TR "a[208:209],v1 offset:16392\n"
MF "v[132:135],a[216:219],a[156:159],v[132:135]\n"
TR "a[210:211],v1 offset:16904\n"
MF "v[124:127],a[216:219],a[152:155],v[124:127]\n"
MF "v[124:127],a[220:223],a[192:195],v[124:127]\n"
TR "a[212:213],v1 offset:20488\n"
MF "v[116:119],a[220:223],a[188:191],v[116:119]\n"
TR "a[214:215],v1 offset:21000\n"
MF "v[116:119],a[216:219],a[148:151],v[116:119]\n"
MF "v[108:111],a[216:219],a[144:147],v[108:111]\n"
MF "v[108:111],a[220:223],a[184:187],v[108:111]\n"
MF "v[100:103],a[220:223],a[180:183],v[100:103]\n"
"s_waitcnt vmcnt(22) lgkmcnt(0)\n"
MF "v[100:103],a[216:219],a[140:143],v[100:103]\n"
"s_barrier\n"
"s_cmp_eq_u32 s57,1\n"
"s_cbranch_scc0 .Lhs15\n"
".Lack_q:\n"
"v_cmp_ne_u32 vcc,s76,v10\n"
"s_cbranch_vccnz .Lackspin_q\n"
".Lackok_q:\n"
".Lhs15:\n"
MF "v[136:139],a[200:203],a[140:143],v[136:139]\n"
"s_and_b32 s58,s56,1\n"
TR "a[216:217],v0 offset:32768\n"
MF "v[136:139],a[204:207],a[180:183],v[136:139]\n"
"s_add_u32 m0,s62,65536\n"
TR "a[218:219],v0 offset:33280\n"
MF "v[144:147],a[204:207],a[184:187],v[144:147]\n"
"s_add_u32 s82,s66,6291456\n"
"ds_read_b128 a[60:63],v2 offset:40960\n"
MF "v[144:147],a[200:203],a[144:147],v[144:147]\n"
"s_sub_u32 s15,s82,7864320\n"
TR "a[220:221],v0 offset:36864\n"
MF "v[152:155],a[200:203],a[148:151],v[152:155]\n"
"s_min_u32 s82,s82,s15\n"
TR "a[222:223],v0 offset:37376\n"
MF "v[152:155],a[204:207],a[188:191],v[152:155]\n"
LD "v6,s[28:31],s82 offen lds\n"
MF "v[160:163],a[204:207],a[192:195],v[160:163]\n"
"s_mul_i32 s59,s58,3072\n"
"ds_read_b128 a[100:103],v3 offset:40960\n"
MF "v[160:163],a[200:203],a[152:155],v[160:163]\n"
"s_add_u32 s16,s70,s59\n"
MF "v[168:171],a[200:203],a[156:159],v[168:171]\n"
"s_mul_i32 s59,s58,6144\n"
MF "v[168:171],a[204:207],a[196:199],v[168:171]\n"
LD "v11,s[28:31],s82 offen offset:1024 lds\n"
MF "v[172:175],a[212:215],a[196:199],v[172:175]\n"
"s_add_u32 s18,s70,s59\n"
TR "a[200:201],v0 offset:32776\n"
MF "v[172:175],a[208:211],a[156:159],v[172:175]\n"
"s_add_u32 s17,s16,196608\n"
TR "a[202:203],v0 offset:33288\n"
MF "v[164:167],a[208:211],a[152:155],v[164:167]\n"
"s_add_u32 s19,s18,196608\n"
"ds_read_b128 a[64:67],v2 offset:43008\n"
MF "v[164:167],a[212:215],a[192:195],v[164:167]\n"
"s_add_u32 s20,s70,196608\n"
TR "a[204:205],v0 offset:36872\n"
MF "v[156:159],a[212:215],a[188:191],v[156:159]\n"
"s_bfe_u32 s58,s23,0x40008\n"
TR "a[206:207],v0 offset:37384\n"
MF "v[156:159],a[208:211],a[148:151],v[156:159]\n"
"s_lshr_b32 s59,s56,1\n"
MF "v[148:151],a[208:211],a[144:147],v[148:151]\n"
"s_mul_i32 s59,s59,5\n"
"ds_read_b128 a[104:107],v3 offset:43008\n"
MF "v[148:151],a[212:215],a[184:187],v[148:151]\n"
"s_add_u32 s58,s58,s59\n"
MF "v[140:143],a[212:215],a[180:183],v[140:143]\n"
LD "v12,s[28:31],s82 offen offset:2048 lds\n"
MF "v[140:143],a[208:211],a[140:143],v[140:143]\n"
"s_waitcnt lgkmcnt(7)\n"
MF "v[176:179],a[216:219],a[140:143],v[176:179]\n"
"s_and_b32 s58,s58,15\n"
TR "a[208:209],v1 offset:32768\n"
MF "v[176:179],a[220:223],a[180:183],v[176:179]\n"
"s_mul_i32 s58,s58,12288\n"
TR "a[210:211],v1 offset:33280\n"
MF "v[184:187],a[220:223],a[184:187],v[184:187]\n"
"s_add_u32 s72,s16,s58\n"
"ds_read_b128 a[68:71],v2 offset:45056\n"
MF "v[184:187],a[216:219],a[144:147],v[184:187]\n"
"s_add_u32 s73,s18,s58\n"
TR "a[212:213],v1 offset:36864\n"
MF "v[192:195],a[216:219],a[148:151],v[192:195]\n"
"s_add_u32 s74,s70,s58\n"
TR "a[214:215],v1 offset:37376\n"
MF "v[192:195],a[220:223],a[188:191],v[192:195]\n"
"s_add_u32 s74,s74,12288\n"
MF "v[200:203],a[220:223],a[192:195],v[200:203]\n"
"s_cmp_eq_u32 s74,s20\n"
"s_cselect_b32 s74,s70,s74\n"
"ds_read_b128 a[108:111],v3 offset:45056\n"
MF "v[200:203],a[216:219],a[152:155],v[200:203]\n"
LD "v13,s[28:31],s82 offen offset:3072 lds\n"
MF "v[208:211],a[216:219],a[156:159],v[208:211]\n"
"s_lshr_b32 s58,s56,1\n"
MF "v[208:211],a[220:223],a[196:199],v[208:211]\n"
"s_waitcnt lgkmcnt(7)\n"
MF "v[212:215],a[204:207],a[196:199],v[212:215]\n"
"s_mul_i32 s58,s58,6144\n"
TR "a[216:217],v1 offset:32776\n"
MF "v[212:215],a[200:203],a[156:159],v[212:215]\n"
"s_mul_i32 s64,s56,3072\n"
TR "a[218:219],v1 offset:33288\n"
MF "v[204:207],a[200:203],a[152:155],v[204:207]\n"
"s_add_u32 s64,s64,s58\n"
"ds_read_b128 a[72:75],v2 offset:47104\n"
MF "v[204:207],a[204:207],a[192:195],v[204:207]\n"
"s_mul_i32 s65,s56,6144\n"
TR "a[220:221],v1 offset:36872\n"
MF "v[196:199],a[204:207],a[188:191],v[196:199]\n"
"s_sub_u32 s58,s71,s70\n"
TR "a[222:223],v1 offset:37384\n"
MF "v[196:199],a[200:203],a[148:151],v[196:199]\n"
"s_add_u32 s96,s74,s58\n"
MF "v[188:191],a[200:203],a[144:147],v[188:191]\n"
"s_sub_u32 s58,s16,s70\n"
"v_add_u32 v0,0xffff4000,v0\n"
MF "v[188:191],a[204:207],a[184:187],v[188:191]\n"
"s_add_u32 s96,s96,s58\n"
"v_add_u32 v1,0xffff4000,v1\n"
MF "v[180:183],a[204:207],a[180:183],v[180:183]\n"
"s_waitcnt vmcnt(13) lgkmcnt(0)\n"
MF "v[180:183],a[200:203],a[140:143],v[180:183]\n"
"s_barrier\n"
MF "v[216:219],a[208:211],a[140:143],v[216:219]\n"
"s_max_u32 s60,s70,s71\n"
TR "a[200:201],v1 offset:8\n"
MF "v[216:219],a[212:215],a[180:183],v[216:219]\n"
"s_add_u32 m0,s62,81920\n"
TR "a[202:203],v1 offset:520\n"
"v_mov_b32 v14,s12\n"
MF "v[224:227],a[212:215],a[184:187],v[224:227]\n"
"s_add_u32 s82,s66,7077888\n"
"ds_read_b128 a[112:115],v3 offset:47104\n"
"v_mul_f32 v14,0xbdd53b95,v14\n"
MF "v[224:227],a[208:211],a[144:147],v[224:227]\n"
"s_sub_u32 s15,s82,7864320\n"
TR "a[204:205],v1 offset:4104\n"
MF "v[232:235],a[208:211],a[148:151],v[232:235]\n"
"s_min_u32 s82,s82,s15\n"
TR "a[206:207],v1 offset:4616\n"
MF "v[232:235],a[212:215],a[188:191],v[232:235]\n"
LD "v6,s[28:31],s82 offen lds\n"
MF "v[240:243],a[212:215],a[192:195],v[240:243]\n"
"s_add_u32 s34,s60,196608\n"
"ds_read_b128 a[76:79],v2 offset:49152\n"
MF "v[240:243],a[208:211],a[152:155],v[240:243]\n"
"s_add_u32 s38,s60,196608\n"
"ds_read_b128 a[116:119],v3 offset:49152\n"
MF "v[248:251],a[208:211],a[156:159],v[248:251]\n"
MF "v[248:251],a[212:215],a[196:199],v[248:251]\n"
LD "v11,s[28:31],s82 offen offset:1024 lds\n"
MF "v[220:223],a[216:219],a[140:143],v[220:223]\n"
TR "a[208:209],v1 offset:16392\n"
MF "v[220:223],a[220:223],a[180:183],v[220:223]\n"
TR "a[210:211],v1 offset:16904\n"
MF "v[228:231],a[220:223],a[184:187],v[228:231]\n"
"ds_read_b128 a[140:143],v2\n"
MF "v[228:231],a[216:219],a[144:147],v[228:231]\n"
TR "a[212:213],v1 offset:20488\n"
MF "v[236:239],a[216:219],a[148:151],v[236:239]\n"
TR "a[214:215],v1 offset:21000\n"
MF "v[236:239],a[220:223],a[188:191],v[236:239]\n"
"ds_read_b128 a[180:183],v3\n"
MF "v[244:247],a[220:223],a[192:195],v[244:247]\n"
"ds_read_b128 a[144:147],v2 offset:2048\n"
MF "v[244:247],a[216:219],a[152:155],v[244:247]\n"
"ds_read_b128 a[184:187],v3 offset:2048\n"
MF "v[252:255],a[216:219],a[156:159],v[252:255]\n"
"ds_read_b128 a[148:151],v2 offset:4096\n"
MF "v[252:255],a[220:223],a[196:199],v[252:255]\n"
"s_waitcnt lgkmcnt(9)\n"
"ds_read_b128 a[188:191],v3 offset:4096\n"
MF "v[220:223],a[200:203],a[60:63],v[220:223]\n"
TR "a[216:217],v1\n"
MF "v[220:223],a[204:207],a[100:103],v[220:223]\n"
TR "a[218:219],v1 offset:512\n"
MF "v[228:231],a[204:207],a[104:107],v[228:231]\n"
"ds_read_b128 a[152:155],v2 offset:6144\n"
MF "v[228:231],a[200:203],a[64:67],v[228:231]\n"
TR "a[220:221],v1 offset:4096\n"
MF "v[236:239],a[200:203],a[68:71],v[236:239]\n"
TR "a[222:223],v1 offset:4608\n"
MF "v[236:239],a[204:207],a[108:111],v[236:239]\n"
"ds_read_b128 a[192:195],v3 offset:6144\n"
MF "v[244:247],a[204:207],a[112:115],v[244:247]\n"
"ds_read_b128 a[156:159],v2 offset:8192\n"
MF "v[244:247],a[200:203],a[72:75],v[244:247]\n"
"ds_read_b128 a[196:199],v3 offset:8192\n"
MF "v[252:255],a[200:203],a[76:79],v[252:255]\n"
LD "v12,s[28:31],s82 offen offset:2048 lds\n"
MF "v[252:255],a[204:207],a[116:119],v[252:255]\n"
"s_waitcnt lgkmcnt(8)\n"
LD "v13,s[28:31],s82 offen offset:3072 lds\n"
MF "v[220:223],a[208:211],a[140:143],v[220:223]\n"
TR "a[200:201],v1 offset:16384\n"
MF "v[220:223],a[212:215],a[180:183],v[220:223]\n"
TR "a[202:203],v1 offset:16896\n"
MF "v[228:231],a[212:215],a[184:187],v[228:231]\n"
MF "v[228:231],a[208:211],a[144:147],v[228:231]\n"
TR "a[204:205],v1 offset:20480\n"
MF "v[236:239],a[208:211],a[148:151],v[236:239]\n"
TR "a[206:207],v1 offset:20992\n"
MF "v[236:239],a[212:215],a[188:191],v[236:239]\n"
"s_waitcnt lgkmcnt(4)\n"
MF "v[244:247],a[212:215],a[192:195],v[244:247]\n"
MF "v[244:247],a[208:211],a[152:155],v[244:247]\n"
MF "v[252:255],a[208:211],a[156:159],v[252:255]\n"
MF "v[252:255],a[212:215],a[196:199],v[252:255]\n"
"s_waitcnt lgkmcnt(0)\n"
"s_cmp_eq_u32 s69,98\n"
"s_cbranch_scc1 .Lqy\n"
".Lqf:\n"
MF "v[216:219],a[216:219],a[60:63],v[216:219]\n"
TR "a[208:209],v0 offset:8\n"
MF "v[216:219],a[220:223],a[100:103],v[216:219]\n"
TR "a[210:211],v0 offset:520\n"
MF "v[224:227],a[220:223],a[104:107],v[224:227]\n"
"buffer_load_dword a224,v15,s[36:39],s70 offen offset:2688 nt\n"
MF "v[224:227],a[216:219],a[64:67],v[224:227]\n"
TR "a[212:213],v0 offset:4104\n"
MF "v[232:235],a[216:219],a[68:71],v[232:235]\n"
TR "a[214:215],v0 offset:4616\n"
MF "v[232:235],a[220:223],a[108:111],v[232:235]\n"
"buffer_load_dword a225,v15,s[36:39],s71 offen offset:2688 nt\n"
MF "v[240:243],a[220:223],a[112:115],v[240:243]\n"
MF "v[240:243],a[216:219],a[72:75],v[240:243]\n"
"buffer_load_dword a226,v15,s[48:51],s7 offen offset:2688\n"
MF "v[248:251],a[216:219],a[76:79],v[248:251]\n"
MF "v[248:251],a[220:223],a[116:119],v[248:251]\n"
MF "v[248:251],a[200:203],a[156:159],v[248:251]\n"
TR "a[216:217],v0 offset:16392\n"
MF "v[248:251],a[204:207],a[196:199],v[248:251]\n"
TR "a[218:219],v0 offset:16904\n"
MF "v[240:243],a[204:207],a[192:195],v[240:243]\n"
MF "v[240:243],a[200:203],a[152:155],v[240:243]\n"
TR "a[220:221],v0 offset:20488\n"
MF "v[232:235],a[200:203],a[148:151],v[232:235]\n"
TR "a[222:223],v0 offset:21000\n"
MF "v[232:235],a[204:207],a[188:191],v[232:235]\n"
MF "v[224:227],a[204:207],a[184:187],v[224:227]\n"
MF "v[224:227],a[200:203],a[144:147],v[224:227]\n"
"v_fma_f32 v248,v248,s54,v14\n"
"v_fma_f32 v249,v249,s54,v14\n"
MF "v[216:219],a[200:203],a[140:143],v[216:219]\n"
"v_fma_f32 v250,v250,s54,v14\n"
"v_fma_f32 v251,v251,s54,v14\n"
MF "v[216:219],a[204:207],a[180:183],v[216:219]\n"
"s_waitcnt lgkmcnt(4)\n"
"v_fma_f32 v252,v252,s54,v14\n"
"v_fma_f32 v253,v253,s54,v14\n"
MF "v[180:183],a[208:211],a[60:63],v[180:183]\n"
TR "a[200:201],v0\n"
"v_fma_f32 v254,v254,s54,v14\n"
MF "v[180:183],a[212:215],a[100:103],v[180:183]\n"
TR "a[202:203],v0 offset:512\n"
"v_fma_f32 v255,v255,s54,v14\n"
MF "v[188:191],a[212:215],a[104:107],v[188:191]\n"
CV "v255,v254,v255\n"
CV "v254,v252,v253\n"
MF "v[188:191],a[208:211],a[64:67],v[188:191]\n"
TR "a[204:205],v0 offset:4096\n"
CV "v253,v250,v251\n"
MF "v[196:199],a[208:211],a[68:71],v[196:199]\n"
TR "a[206:207],v0 offset:4608\n"
CV "v252,v248,v249\n"
MF "v[196:199],a[212:215],a[108:111],v[196:199]\n"
"v_fma_f32 v240,v240,s54,v14\n"
"v_fma_f32 v241,v241,s54,v14\n"
MF "v[204:207],a[212:215],a[112:115],v[204:207]\n"
"v_fma_f32 v242,v242,s54,v14\n"
"v_fma_f32 v243,v243,s54,v14\n"
MF "v[204:207],a[208:211],a[72:75],v[204:207]\n"
"v_fma_f32 v244,v244,s54,v14\n"
"v_fma_f32 v245,v245,s54,v14\n"
MF "v[212:215],a[208:211],a[76:79],v[212:215]\n"
"v_fma_f32 v246,v246,s54,v14\n"
"v_fma_f32 v247,v247,s54,v14\n"
MF "v[212:215],a[212:215],a[116:119],v[212:215]\n"
"s_waitcnt lgkmcnt(4)\n"
CV "v251,v246,v247\n"
CV "v250,v244,v245\n"
MF "v[180:183],a[216:219],a[140:143],v[180:183]\n"
TR "a[208:209],v0 offset:16384\n"
CV "v249,v242,v243\n"
MF "v[180:183],a[220:223],a[180:183],v[180:183]\n"
TR "a[210:211],v0 offset:16896\n"
CV "v248,v240,v241\n"
MF "v[188:191],a[220:223],a[184:187],v[188:191]\n"
"v_fma_f32 v232,v232,s54,v14\n"
"v_fma_f32 v233,v233,s54,v14\n"
MF "v[188:191],a[216:219],a[144:147],v[188:191]\n"
TR "a[212:213],v0 offset:20480\n"
"v_fma_f32 v234,v234,s54,v14\n"
MF "v[196:199],a[216:219],a[148:151],v[196:199]\n"
TR "a[214:215],v0 offset:20992\n"
"v_fma_f32 v235,v235,s54,v14\n"
MF "v[196:199],a[220:223],a[188:191],v[196:199]\n"
"v_fma_f32 v236,v236,s54,v14\n"
"v_fma_f32 v237,v237,s54,v14\n"
MF "v[204:207],a[220:223],a[192:195],v[204:207]\n"
"v_fma_f32 v238,v238,s54,v14\n"
"v_fma_f32 v239,v239,s54,v14\n"
MF "v[204:207],a[216:219],a[152:155],v[204:207]\n"
CV "v247,v238,v239\n"
CV "v246,v236,v237\n"
MF "v[212:215],a[216:219],a[156:159],v[212:215]\n"
"s_waitcnt vmcnt(11) lgkmcnt(0)\n"
CV "v245,v234,v235\n"
CV "v244,v232,v233\n"
MF "v[212:215],a[220:223],a[196:199],v[212:215]\n"
"s_barrier\n"
MF "v[176:179],a[200:203],a[60:63],v[176:179]\n"
TR "a[216:217],v1 offset:32776\n"
"v_fma_f32 v224,v224,s54,v14\n"
MF "v[176:179],a[204:207],a[100:103],v[176:179]\n"
"s_add_u32 m0,s64,0\n"
TR "a[218:219],v1 offset:33288\n"
"v_fma_f32 v225,v225,s54,v14\n"
MF "v[184:187],a[204:207],a[104:107],v[184:187]\n"
LD "v7,s[32:35],s72 offen nt lds\n"
MF "v[184:187],a[200:203],a[64:67],v[184:187]\n"
TR "a[220:221],v1 offset:36872\n"
"v_fma_f32 v226,v226,s54,v14\n"
MF "v[192:195],a[200:203],a[68:71],v[192:195]\n"
TR "a[222:223],v1 offset:37384\n"
"v_fma_f32 v227,v227,s54,v14\n"
MF "v[192:195],a[204:207],a[108:111],v[192:195]\n"
"s_add_u32 m0,s64,896\n"
"v_fma_f32 v228,v228,s54,v14\n"
"v_fma_f32 v229,v229,s54,v14\n"
MF "v[200:203],a[204:207],a[112:115],v[200:203]\n"
LD "v7,s[32:35],s72 offen offset:128 nt lds\n"
MF "v[200:203],a[200:203],a[72:75],v[200:203]\n"
"v_fma_f32 v230,v230,s54,v14\n"
"v_fma_f32 v231,v231,s54,v14\n"
MF "v[208:211],a[200:203],a[76:79],v[208:211]\n"
CV "v243,v230,v231\n"
CV "v242,v228,v229\n"
MF "v[208:211],a[204:207],a[116:119],v[208:211]\n"
CV "v241,v226,v227\n"
CV "v240,v224,v225\n"
MF "v[208:211],a[208:211],a[156:159],v[208:211]\n"
TR "a[200:201],v1 offset:49160\n"
"v_fma_f32 v216,v216,s54,v14\n"
MF "v[208:211],a[212:215],a[196:199],v[208:211]\n"
"s_add_u32 m0,s64,1792\n"
TR "a[202:203],v1 offset:49672\n"
"v_fma_f32 v217,v217,s54,v14\n"
MF "v[200:203],a[212:215],a[192:195],v[200:203]\n"
LD "v7,s[32:35],s72 offen offset:256 nt lds\n"
MF "v[200:203],a[208:211],a[152:155],v[200:203]\n"
TR "a[204:205],v1 offset:53256\n"
"v_fma_f32 v218,v218,s54,v14\n"
MF "v[192:195],a[208:211],a[148:151],v[192:195]\n"
TR "a[206:207],v1 offset:53768\n"
"v_fma_f32 v219,v219,s54,v14\n"
MF "v[192:195],a[212:215],a[188:191],v[192:195]\n"
"v_fma_f32 v220,v220,s54,v14\n"
"v_fma_f32 v221,v221,s54,v14\n"
MF "v[184:187],a[212:215],a[184:187],v[184:187]\n"
"s_add_u32 s72,s72,6144\n"
"v_fma_f32 v222,v222,s54,v14\n"
"v_fma_f32 v223,v223,s54,v14\n"
MF "v[184:187],a[208:211],a[144:147],v[184:187]\n"
"s_add_u32 m0,s64,6144\n"
CV "v239,v222,v223\n"
CV "v238,v220,v221\n"
MF "v[176:179],a[208:211],a[140:143],v[176:179]\n"
LD "v7,s[32:35],s72 offen nt lds\n"
MF "v[176:179],a[212:215],a[180:183],v[176:179]\n"
"s_waitcnt lgkmcnt(4)\n"
CV "v237,v218,v219\n"
CV "v236,v216,v217\n"
MF "v[140:143],a[216:219],a[60:63],v[140:143]\n"
TR "a[208:209],v1 offset:32768\n"
"v_fma_f32 v208,v208,s54,v14\n"
MF "v[140:143],a[220:223],a[100:103],v[140:143]\n"
TR "a[210:211],v1 offset:33280\n"
"v_fma_f32 v209,v209,s54,v14\n"
MF "v[148:151],a[220:223],a[104:107],v[148:151]\n"
"v_fma_f32 v210,v210,s54,v14\n"
"v_fma_f32 v211,v211,s54,v14\n"
MF "v[148:151],a[216:219],a[64:67],v[148:151]\n"
TR "a[212:213],v1 offset:36864\n"
"v_fma_f32 v212,v212,s54,v14\n"
MF "v[156:159],a[216:219],a[68:71],v[156:159]\n"
"s_add_u32 m0,s64,7040\n"
TR "a[214:215],v1 offset:37376\n"
"v_fma_f32 v213,v213,s54,v14\n"
MF "v[156:159],a[220:223],a[108:111],v[156:159]\n"
LD "v7,s[32:35],s72 offen offset:128 nt lds\n"
MF "v[164:167],a[220:223],a[112:115],v[164:167]\n"
"v_fma_f32 v214,v214,s54,v14\n"
"v_fma_f32 v215,v215,s54,v14\n"
MF "v[164:167],a[216:219],a[72:75],v[164:167]\n"
CV "v235,v214,v215\n"
CV "v234,v212,v213\n"
MF "v[172:175],a[216:219],a[76:79],v[172:175]\n"
"s_add_u32 m0,s64,7936\n"
CV "v233,v210,v211\n"
CV "v232,v208,v209\n"
MF "v[172:175],a[220:223],a[116:119],v[172:175]\n"
"s_waitcnt lgkmcnt(4)\n"
LD "v7,s[32:35],s72 offen offset:256 nt lds\n"
MF "v[140:143],a[200:203],a[140:143],v[140:143]\n"
TR "a[216:217],v1 offset:49152\n"
"v_fma_f32 v200,v200,s54,v14\n"
MF "v[140:143],a[204:207],a[180:183],v[140:143]\n"
TR "a[218:219],v1 offset:49664\n"
"v_fma_f32 v201,v201,s54,v14\n"
MF "v[148:151],a[204:207],a[184:187],v[148:151]\n"
"v_fma_f32 v202,v202,s54,v14\n"
"v_fma_f32 v203,v203,s54,v14\n"
MF "v[148:151],a[200:203],a[144:147],v[148:151]\n"
TR "a[220:221],v1 offset:53248\n"
"v_fma_f32 v204,v204,s54,v14\n"
MF "v[156:159],a[200:203],a[148:151],v[156:159]\n"
TR "a[222:223],v1 offset:53760\n"
"v_fma_f32 v205,v205,s54,v14\n"
MF "v[156:159],a[204:207],a[188:191],v[156:159]\n"
"v_fma_f32 v206,v206,s54,v14\n"
"v_fma_f32 v207,v207,s54,v14\n"
MF "v[164:167],a[204:207],a[192:195],v[164:167]\n"
CV "v231,v206,v207\n"
CV "v230,v204,v205\n"
MF "v[164:167],a[200:203],a[152:155],v[164:167]\n"
CV "v229,v202,v203\n"
CV "v228,v200,v201\n"
MF "v[172:175],a[200:203],a[156:159],v[172:175]\n"
"v_fma_f32 v192,v192,s54,v14\n"
"v_fma_f32 v193,v193,s54,v14\n"
MF "v[172:175],a[204:207],a[196:199],v[172:175]\n"
"s_waitcnt lgkmcnt(0)\n"
"v_fma_f32 v194,v194,s54,v14\n"
"v_fma_f32 v195,v195,s54,v14\n"
MF "v[136:139],a[208:211],a[60:63],v[136:139]\n"
TR "a[200:201],v0 offset:32776\n"
"v_fma_f32 v196,v196,s54,v14\n"
MF "v[136:139],a[212:215],a[100:103],v[136:139]\n"
TR "a[202:203],v0 offset:33288\n"
"v_fma_f32 v197,v197,s54,v14\n"
MF "v[144:147],a[212:215],a[104:107],v[144:147]\n"
"v_fma_f32 v198,v198,s54,v14\n"
"v_fma_f32 v199,v199,s54,v14\n"
MF "v[144:147],a[208:211],a[64:67],v[144:147]\n"
TR "a[204:205],v0 offset:36872\n"
CV "v227,v198,v199\n"
MF "v[152:155],a[208:211],a[68:71],v[152:155]\n"
TR "a[206:207],v0 offset:37384\n"
CV "v226,v196,v197\n"
MF "v[152:155],a[212:215],a[108:111],v[152:155]\n"
CV "v225,v194,v195\n"
CV "v224,v192,v193\n"
MF "v[160:163],a[212:215],a[112:115],v[160:163]\n"
"v_fma_f32 v184,v184,s54,v14\n"
"v_fma_f32 v185,v185,s54,v14\n"
MF "v[160:163],a[208:211],a[72:75],v[160:163]\n"
"v_fma_f32 v186,v186,s54,v14\n"
"v_fma_f32 v187,v187,s54,v14\n"
MF "v[168:171],a[208:211],a[76:79],v[168:171]\n"
"v_fma_f32 v188,v188,s54,v14\n"
"v_fma_f32 v189,v189,s54,v14\n"
MF "v[168:171],a[212:215],a[116:119],v[168:171]\n"
"v_fma_f32 v190,v190,s54,v14\n"
"v_fma_f32 v191,v191,s54,v14\n"
MF "v[168:171],a[216:219],a[156:159],v[168:171]\n"
TR "a[208:209],v0 offset:49160\n"
CV "v223,v190,v191\n"
MF "v[168:171],a[220:223],a[196:199],v[168:171]\n"
TR "a[210:211],v0 offset:49672\n"
CV "v222,v188,v189\n"
MF "v[160:163],a[220:223],a[192:195],v[160:163]\n"
CV "v221,v186,v187\n"
CV "v220,v184,v185\n"
MF "v[160:163],a[216:219],a[152:155],v[160:163]\n"
TR "a[212:213],v0 offset:53256\n"
"v_fma_f32 v176,v176,s54,v14\n"
MF "v[152:155],a[216:219],a[148:151],v[152:155]\n"
TR "a[214:215],v0 offset:53768\n"
"v_fma_f32 v177,v177,s54,v14\n"
MF "v[152:155],a[220:223],a[188:191],v[152:155]\n"
"v_fma_f32 v178,v178,s54,v14\n"
"v_fma_f32 v179,v179,s54,v14\n"
MF "v[144:147],a[220:223],a[184:187],v[144:147]\n"
"v_fma_f32 v180,v180,s54,v14\n"
"v_fma_f32 v181,v181,s54,v14\n"
MF "v[144:147],a[216:219],a[144:147],v[144:147]\n"
"v_fma_f32 v182,v182,s54,v14\n"
"v_fma_f32 v183,v183,s54,v14\n"
MF "v[136:139],a[216:219],a[140:143],v[136:139]\n"
CV "v219,v182,v183\n"
CV "v218,v180,v181\n"
MF "v[136:139],a[220:223],a[180:183],v[136:139]\n"
"s_waitcnt lgkmcnt(4)\n"
CV "v217,v178,v179\n"
CV "v216,v176,v177\n"
MF "v[100:103],a[200:203],a[60:63],v[100:103]\n"
TR "a[216:217],v0 offset:32768\n"
"v_fma_f32 v168,v168,s54,v14\n"
MF "v[100:103],a[204:207],a[100:103],v[100:103]\n"
TR "a[218:219],v0 offset:33280\n"
"v_fma_f32 v169,v169,s54,v14\n"
MF "v[108:111],a[204:207],a[104:107],v[108:111]\n"
"v_fma_f32 v170,v170,s54,v14\n"
"v_fma_f32 v171,v171,s54,v14\n"
MF "v[108:111],a[200:203],a[64:67],v[108:111]\n"
TR "a[220:221],v0 offset:36864\n"
"v_fma_f32 v172,v172,s54,v14\n"
MF "v[116:119],a[200:203],a[68:71],v[116:119]\n"
TR "a[222:223],v0 offset:37376\n"
"v_fma_f32 v173,v173,s54,v14\n"
MF "v[116:119],a[204:207],a[108:111],v[116:119]\n"
"v_fma_f32 v174,v174,s54,v14\n"
"v_fma_f32 v175,v175,s54,v14\n"
MF "v[124:127],a[204:207],a[112:115],v[124:127]\n"
CV "v215,v174,v175\n"
CV "v214,v172,v173\n"
MF "v[124:127],a[200:203],a[72:75],v[124:127]\n"
".Lpg8b:\n"
"s_cbranch_execz .Lpg11b\n"
".Lpg8d:\n"
"s_cbranch_execz .Lpgep_d\n"
CV "v213,v170,v171\n"
CV "v212,v168,v169\n"
MF "v[132:135],a[200:203],a[76:79],v[132:135]\n"
"v_fma_f32 v160,v160,s54,v14\n"
"v_fma_f32 v161,v161,s54,v14\n"
MF "v[132:135],a[204:207],a[116:119],v[132:135]\n"
"s_waitcnt lgkmcnt(4)\n"
"v_fma_f32 v162,v162,s54,v14\n"
"v_fma_f32 v163,v163,s54,v14\n"
MF "v[100:103],a[208:211],a[140:143],v[100:103]\n"
TR "a[200:201],v0 offset:49152\n"
"v_fma_f32 v164,v164,s54,v14\n"
MF "v[100:103],a[212:215],a[180:183],v[100:103]\n"
TR "a[202:203],v0 offset:49664\n"
"v_fma_f32 v165,v165,s54,v14\n"
MF "v[108:111],a[212:215],a[184:187],v[108:111]\n"
"v_fma_f32 v166,v166,s54,v14\n"
"v_fma_f32 v167,v167,s54,v14\n"
MF "v[108:111],a[208:211],a[144:147],v[108:111]\n"
TR "a[204:205],v0 offset:53248\n"
CV "v211,v166,v167\n"
MF "v[116:119],a[208:211],a[148:151],v[116:119]\n"
TR "a[206:207],v0 offset:53760\n"
CV "v210,v164,v165\n"
MF "v[116:119],a[212:215],a[188:191],v[116:119]\n"
CV "v209,v162,v163\n"
CV "v208,v160,v161\n"
MF "v[124:127],a[212:215],a[192:195],v[124:127]\n"
"v_add_u32 v0,0xc000,v0\n"
"v_fma_f32 v152,v152,s54,v14\n"
MF "v[124:127],a[208:211],a[152:155],v[124:127]\n"
"v_add_u32 v1,0xc000,v1\n"
"v_fma_f32 v153,v153,s54,v14\n"
MF "v[132:135],a[208:211],a[156:159],v[132:135]\n"
"s_waitcnt vmcnt(9) lgkmcnt(0)\n"
"v_fma_f32 v154,v154,s54,v14\n"
"v_fma_f32 v155,v155,s54,v14\n"
MF "v[132:135],a[212:215],a[196:199],v[132:135]\n"
"s_barrier\n"
MF "v[96:99],a[216:219],a[60:63],v[96:99]\n"
TR "a[208:209],v1 offset:16392\n"
"v_fma_f32 v156,v156,s54,v14\n"
MF "v[96:99],a[220:223],a[100:103],v[96:99]\n"
TR "a[210:211],v1 offset:16904\n"
"v_fma_f32 v157,v157,s54,v14\n"
MF "v[104:107],a[220:223],a[104:107],v[104:107]\n"
"s_add_u32 s72,s72,6144\n"
"v_fma_f32 v158,v158,s54,v14\n"
"v_fma_f32 v159,v159,s54,v14\n"
MF "v[104:107],a[216:219],a[64:67],v[104:107]\n"
"s_cmp_eq_u32 s72,s17\n"
"s_cselect_b32 s72,s16,s72\n"
TR "a[212:213],v1 offset:20488\n"
MF "v[112:115],a[216:219],a[68:71],v[112:115]\n"
"s_add_u32 m0,s64,24576\n"
TR "a[214:215],v1 offset:21000\n"
CV "v207,v158,v159\n"
MF "v[112:115],a[220:223],a[108:111],v[112:115]\n"
LD "v7,s[32:35],s72 offen nt lds\n"
MF "v[120:123],a[220:223],a[112:115],v[120:123]\n"
"s_add_u32 m0,s64,25472\n"
CV "v206,v156,v157\n"
CV "v205,v154,v155\n"
MF "v[120:123],a[216:219],a[72:75],v[120:123]\n"
LD "v7,s[32:35],s72 offen offset:128 nt lds\n"
MF "v[128:131],a[216:219],a[76:79],v[128:131]\n"
CV "v204,v152,v153\n"
"v_fma_f32 v144,v144,s54,v14\n"
MF "v[128:131],a[220:223],a[116:119],v[128:131]\n"
"v_fma_f32 v145,v145,s54,v14\n"
"v_fma_f32 v146,v146,s54,v14\n"
MF "v[128:131],a[200:203],a[156:159],v[128:131]\n"
TR "a[216:217],v1 offset:32776\n"
"v_fma_f32 v147,v147,s54,v14\n"
MF "v[128:131],a[204:207],a[196:199],v[128:131]\n"
"s_add_u32 m0,s64,26368\n"
TR "a[218:219],v1 offset:33288\n"
"v_fma_f32 v148,v148,s54,v14\n"
MF "v[120:123],a[204:207],a[192:195],v[120:123]\n"
LD "v7,s[32:35],s72 offen offset:256 nt lds\n"
MF "v[120:123],a[200:203],a[152:155],v[120:123]\n"
TR "a[220:221],v1 offset:36872\n"
"v_fma_f32 v149,v149,s54,v14\n"
MF "v[112:115],a[200:203],a[148:151],v[112:115]\n"
TR "a[222:223],v1 offset:37384\n"
"v_fma_f32 v150,v150,s54,v14\n"
MF "v[112:115],a[204:207],a[188:191],v[112:115]\n"
"v_fma_f32 v151,v151,s54,v14\n"
CV "v203,v150,v151\n"
MF "v[104:107],a[204:207],a[184:187],v[104:107]\n"
"s_add_u32 s72,s72,6144\n"
CV "v202,v148,v149\n"
CV "v201,v146,v147\n"
MF "v[104:107],a[200:203],a[144:147],v[104:107]\n"
"s_add_u32 m0,s64,30720\n"
CV "v200,v144,v145\n"
"v_fma_f32 v136,v136,s54,v14\n"
MF "v[96:99],a[200:203],a[140:143],v[96:99]\n"
LD "v7,s[32:35],s72 offen nt lds\n"
MF "v[96:99],a[204:207],a[180:183],v[96:99]\n"
"s_waitcnt lgkmcnt(4)\n"
"v_fma_f32 v137,v137,s54,v14\n"
"v_fma_f32 v138,v138,s54,v14\n"
MF "v[60:63],a[208:211],a[60:63],v[60:63]\n"
TR "a[200:201],v1 offset:16384\n"
"v_fma_f32 v139,v139,s54,v14\n"
MF "v[60:63],a[212:215],a[100:103],v[60:63]\n"
TR "a[202:203],v1 offset:16896\n"
"v_fma_f32 v140,v140,s54,v14\n"
MF "v[68:71],a[212:215],a[104:107],v[68:71]\n"
"v_fma_f32 v141,v141,s54,v14\n"
"v_fma_f32 v142,v142,s54,v14\n"
MF "v[68:71],a[208:211],a[64:67],v[68:71]\n"
TR "a[204:205],v1 offset:20480\n"
"v_fma_f32 v143,v143,s54,v14\n"
MF "v[76:79],a[208:211],a[68:71],v[76:79]\n"
"s_add_u32 m0,s64,31616\n"
TR "a[206:207],v1 offset:20992\n"
CV "v199,v142,v143\n"
MF "v[76:79],a[212:215],a[108:111],v[76:79]\n"
LD "v7,s[32:35],s72 offen offset:128 nt lds\n"
MF "v[84:87],a[212:215],a[112:115],v[84:87]\n"
CV "v198,v140,v141\n"
CV "v197,v138,v139\n"
MF "v[84:87],a[208:211],a[72:75],v[84:87]\n"
CV "v196,v136,v137\n"
"v_fma_f32 v128,v128,s54,v14\n"
MF "v[92:95],a[208:211],a[76:79],v[92:95]\n"
"s_add_u32 m0,s64,32512\n"
"v_fma_f32 v129,v129,s54,v14\n"
"v_fma_f32 v130,v130,s54,v14\n"
MF "v[92:95],a[212:215],a[116:119],v[92:95]\n"
"s_waitcnt lgkmcnt(4)\n"
LD "v7,s[32:35],s72 offen offset:256 nt lds\n"
MF "v[60:63],a[216:219],a[140:143],v[60:63]\n"
TR "a[208:209],v1 offset:32768\n"
"v_fma_f32 v131,v131,s54,v14\n"
MF "v[60:63],a[220:223],a[180:183],v[60:63]\n"
TR "a[210:211],v1 offset:33280\n"
"v_fma_f32 v132,v132,s54,v14\n"
MF "v[68:71],a[220:223],a[184:187],v[68:71]\n"
"v_fma_f32 v133,v133,s54,v14\n"
"v_fma_f32 v134,v134,s54,v14\n"
MF "v[68:71],a[216:219],a[144:147],v[68:71]\n"
TR "a[212:213],v1 offset:36864\n"
"v_fma_f32 v135,v135,s54,v14\n"
MF "v[76:79],a[216:219],a[148:151],v[76:79]\n"
TR "a[214:215],v1 offset:37376\n"
CV "v195,v134,v135\n"
MF "v[76:79],a[220:223],a[188:191],v[76:79]\n"
CV "v194,v132,v133\n"
CV "v193,v130,v131\n"
MF "v[84:87],a[220:223],a[192:195],v[84:87]\n"
CV "v192,v128,v129\n"
"v_fma_f32 v120,v120,s54,v14\n"
MF "v[84:87],a[216:219],a[152:155],v[84:87]\n"
"v_fma_f32 v121,v121,s54,v14\n"
"v_fma_f32 v122,v122,s54,v14\n"
MF "v[92:95],a[216:219],a[156:159],v[92:95]\n"
"v_fma_f32 v123,v123,s54,v14\n"
"v_fma_f32 v124,v124,s54,v14\n"
MF "v[92:95],a[220:223],a[196:199],v[92:95]\n"
"s_waitcnt lgkmcnt(0)\n"
"v_fma_f32 v125,v125,s54,v14\n"
"v_fma_f32 v126,v126,s54,v14\n"
MF "v[56:59],a[200:203],a[60:63],v[56:59]\n"
TR "a[216:217],v0 offset:16392\n"
"v_fma_f32 v127,v127,s54,v14\n"
MF "v[56:59],a[204:207],a[100:103],v[56:59]\n"
TR "a[218:219],v0 offset:16904\n"
CV "v191,v126,v127\n"
MF "v[64:67],a[204:207],a[104:107],v[64:67]\n"
CV "v190,v124,v125\n"
CV "v189,v122,v123\n"
MF "v[64:67],a[200:203],a[64:67],v[64:67]\n"
TR "a[220:221],v0 offset:20488\n"
CV "v188,v120,v121\n"
MF "v[72:75],a[200:203],a[68:71],v[72:75]\n"
TR "a[222:223],v0 offset:21000\n"
"v_fma_f32 v112,v112,s54,v14\n"
MF "v[72:75],a[204:207],a[108:111],v[72:75]\n"
"v_fma_f32 v113,v113,s54,v14\n"
"v_fma_f32 v114,v114,s54,v14\n"
MF "v[80:83],a[204:207],a[112:115],v[80:83]\n"
"v_fma_f32 v115,v115,s54,v14\n"
"v_fma_f32 v116,v116,s54,v14\n"
MF "v[80:83],a[200:203],a[72:75],v[80:83]\n"
"v_fma_f32 v117,v117,s54,v14\n"
"v_fma_f32 v118,v118,s54,v14\n"
MF "v[88:91],a[200:203],a[76:79],v[88:91]\n"
"v_fma_f32 v119,v119,s54,v14\n"
CV "v187,v118,v119\n"
MF "v[88:91],a[204:207],a[116:119],v[88:91]\n"
CV "v186,v116,v117\n"
CV "v185,v114,v115\n"
MF "v[88:91],a[208:211],a[156:159],v[88:91]\n"
TR "a[200:201],v0 offset:32776\n"
CV "v184,v112,v113\n"
MF "v[88:91],a[212:215],a[196:199],v[88:91]\n"
TR "a[202:203],v0 offset:33288\n"
"v_fma_f32 v104,v104,s54,v14\n"
MF "v[80:83],a[212:215],a[192:195],v[80:83]\n"
"v_fma_f32 v105,v105,s54,v14\n"
"v_fma_f32 v106,v106,s54,v14\n"
MF "v[80:83],a[208:211],a[152:155],v[80:83]\n"
TR "a[204:205],v0 offset:36872\n"
"v_fma_f32 v107,v107,s54,v14\n"
MF "v[72:75],a[208:211],a[148:151],v[72:75]\n"
TR "a[206:207],v0 offset:37384\n"
"v_fma_f32 v108,v108,s54,v14\n"
MF "v[72:75],a[212:215],a[188:191],v[72:75]\n"
"v_fma_f32 v109,v109,s54,v14\n"
"v_fma_f32 v110,v110,s54,v14\n"
MF "v[64:67],a[212:215],a[184:187],v[64:67]\n"
"v_fma_f32 v111,v111,s54,v14\n"
CV "v183,v110,v111\n"
MF "v[64:67],a[208:211],a[144:147],v[64:67]\n"
CV "v182,v108,v109\n"
CV "v181,v106,v107\n"
MF "v[56:59],a[208:211],a[140:143],v[56:59]\n"
CV "v180,v104,v105\n"
"v_fma_f32 v96,v96,s54,v14\n"
MF "v[56:59],a[212:215],a[180:183],v[56:59]\n"
"s_waitcnt lgkmcnt(4)\n"
"v_fma_f32 v97,v97,s54,v14\n"
"v_fma_f32 v98,v98,s54,v14\n"
MF "v[20:23],a[216:219],a[60:63],v[20:23]\n"
TR "a[208:209],v0 offset:16384\n"
"v_fma_f32 v99,v99,s54,v14\n"
MF "v[20:23],a[220:223],a[100:103],v[20:23]\n"
TR "a[210:211],v0 offset:16896\n"
"v_fma_f32 v100,v100,s54,v14\n"
MF "v[28:31],a[220:223],a[104:107],v[28:31]\n"
"v_fma_f32 v101,v101,s54,v14\n"
"v_fma_f32 v102,v102,s54,v14\n"
MF "v[28:31],a[216:219],a[64:67],v[28:31]\n"
TR "a[212:213],v0 offset:20480\n"
"v_fma_f32 v103,v103,s54,v14\n"
MF "v[36:39],a[216:219],a[68:71],v[36:39]\n"
TR "a[214:215],v0 offset:20992\n"
CV "v179,v102,v103\n"
MF "v[36:39],a[220:223],a[108:111],v[36:39]\n"
CV "v178,v100,v101\n"
CV "v177,v98,v99\n"
MF "v[44:47],a[220:223],a[112:115],v[44:47]\n"
CV "v176,v96,v97\n"
"v_fma_f32 v88,v88,s54,v14\n"
MF "v[44:47],a[216:219],a[72:75],v[44:47]\n"
"v_fma_f32 v89,v89,s54,v14\n"
"v_fma_f32 v90,v90,s54,v14\n"
MF "v[52:55],a[216:219],a[76:79],v[52:55]\n"
"v_fma_f32 v91,v91,s54,v14\n"
"v_fma_f32 v92,v92,s54,v14\n"
MF "v[52:55],a[220:223],a[116:119],v[52:55]\n"
"s_waitcnt lgkmcnt(4)\n"
"v_fma_f32 v93,v93,s54,v14\n"
"v_fma_f32 v94,v94,s54,v14\n"
MF "v[20:23],a[200:203],a[140:143],v[20:23]\n"
TR "a[216:217],v0 offset:32768\n"
"v_fma_f32 v95,v95,s54,v14\n"
MF "v[20:23],a[204:207],a[180:183],v[20:23]\n"
TR "a[218:219],v0 offset:33280\n"
CV "v175,v94,v95\n"
MF "v[28:31],a[204:207],a[184:187],v[28:31]\n"
CV "v174,v92,v93\n"
CV "v173,v90,v91\n"
MF "v[28:31],a[200:203],a[144:147],v[28:31]\n"
TR "a[220:221],v0 offset:36864\n"
CV "v172,v88,v89\n"
MF "v[36:39],a[200:203],a[148:151],v[36:39]\n"
TR "a[222:223],v0 offset:37376\n"
"v_fma_f32 v80,v80,s54,v14\n"
MF "v[36:39],a[204:207],a[188:191],v[36:39]\n"
"v_fma_f32 v81,v81,s54,v14\n"
"v_fma_f32 v82,v82,s54,v14\n"
MF "v[44:47],a[204:207],a[192:195],v[44:47]\n"
"v_fma_f32 v83,v83,s54,v14\n"
"v_fma_f32 v84,v84,s54,v14\n"
MF "v[44:47],a[200:203],a[152:155],v[44:47]\n"
"v_fma_f32 v85,v85,s54,v14\n"
"v_fma_f32 v86,v86,s54,v14\n"
MF "v[52:55],a[200:203],a[156:159],v[52:55]\n"
"s_waitcnt lgkmcnt(0)\n"
"v_fma_f32 v87,v87,s54,v14\n"
CV "v171,v86,v87\n"
MF "v[52:55],a[204:207],a[196:199],v[52:55]\n"
"s_barrier\n"
MF "v[16:19],a[208:211],a[60:63],v[16:19]\n"
"s_add_u32 m0,s65,49152\n"
CV "v170,v84,v85\n"
CV "v169,v82,v83\n"
MF "v[16:19],a[212:215],a[100:103],v[16:19]\n"
LD "v8,s[36:39],s73 offen nt lds\n"
MF "v[24:27],a[212:215],a[104:107],v[24:27]\n"
"s_add_u32 m0,s65,50048\n"
CV "v168,v80,v81\n"
"v_fma_f32 v72,v72,s54,v14\n"
MF "v[24:27],a[208:211],a[64:67],v[24:27]\n"
LD "v8,s[36:39],s73 offen offset:128 nt lds\n"
MF "v[32:35],a[208:211],a[68:71],v[32:35]\n"
"v_fma_f32 v73,v73,s54,v14\n"
"v_fma_f32 v74,v74,s54,v14\n"
MF "v[32:35],a[212:215],a[108:111],v[32:35]\n"
"s_add_u32 m0,s65,50944\n"
"v_fma_f32 v75,v75,s54,v14\n"
"v_fma_f32 v76,v76,s54,v14\n"
MF "v[40:43],a[212:215],a[112:115],v[40:43]\n"
LD "v8,s[36:39],s73 offen offset:256 nt lds\n"
MF "v[40:43],a[208:211],a[72:75],v[40:43]\n"
"v_fma_f32 v77,v77,s54,v14\n"
"v_fma_f32 v78,v78,s54,v14\n"
MF "v[48:51],a[208:211],a[76:79],v[48:51]\n"
"s_add_u32 m0,s65,49152\n"
"v_fma_f32 v79,v79,s54,v14\n"
CV "v167,v78,v79\n"
MF "v[48:51],a[212:215],a[116:119],v[48:51]\n"
LD "v8,s[36:39],s73 offen offset:3072 nt lds\n"
MF "v[48:51],a[216:219],a[156:159],v[48:51]\n"
"s_add_u32 m0,s65,50048\n"
CV "v166,v76,v77\n"
CV "v165,v74,v75\n"
MF "v[48:51],a[220:223],a[196:199],v[48:51]\n"
LD "v8,s[36:39],s73 offen offset:3200 nt lds\n"
MF "v[40:43],a[220:223],a[192:195],v[40:43]\n"
CV "v164,v72,v73\n"
"v_fma_f32 v64,v64,s54,v14\n"
MF "v[40:43],a[216:219],a[152:155],v[40:43]\n"
"s_add_u32 m0,s65,50944\n"
"v_fma_f32 v65,v65,s54,v14\n"
"v_fma_f32 v66,v66,s54,v14\n"
MF "v[32:35],a[216:219],a[148:151],v[32:35]\n"
LD "v8,s[36:39],s73 offen offset:3328 nt lds\n"
MF "v[32:35],a[220:223],a[188:191],v[32:35]\n"
"v_fma_f32 v67,v67,s54,v14\n"
"v_fma_f32 v68,v68,s54,v14\n"
MF "v[24:27],a[220:223],a[184:187],v[24:27]\n"
"v_fma_f32 v69,v69,s54,v14\n"
"v_fma_f32 v70,v70,s54,v14\n"
MF "v[24:27],a[216:219],a[144:147],v[24:27]\n"
"v_fma_f32 v71,v71,s54,v14\n"
CV "v163,v70,v71\n"
MF "v[16:19],a[216:219],a[140:143],v[16:19]\n"
CV "v162,v68,v69\n"
CV "v161,v66,v67\n"
MF "v[16:19],a[220:223],a[180:183],v[16:19]\n"
CV "v160,v64,v65\n"
AR "v0,a244\n"
"v_xor_b32 v1,64,v0\n"
"s_waitcnt vmcnt(12) lgkmcnt(0)\n"
"s_barrier\n"
"s_nop 0\n"
"ds_read_b128 v[92:95],v0\n"
"ds_read_b128 v[96:99],v1\n"
"ds_read_b128 v[100:103],v0 offset:1024\n"
"v_pk_fma_f32 v[56:57],v[56:57],s[54:55],v[14:15] op_sel_hi:[1,1,0]\n"
"v_pk_fma_f32 v[58:59],v[58:59],s[54:55],v[14:15] op_sel_hi:[1,1,0]\n"
"v_pk_fma_f32 v[60:61],v[60:61],s[54:55],v[14:15] op_sel_hi:[1,1,0]\n"
"v_pk_fma_f32 v[62:63],v[62:63],s[54:55],v[14:15] op_sel_hi:[1,1,0]\n"
CV "v159,v62,v63\n"
CV "v158,v60,v61\n"
CV "v157,v58,v59\n"
CV "v156,v56,v57\n"
"v_pk_mul_f32 v[48:49],v[48:49],s[54:55]\n"
"v_pk_mul_f32 v[50:51],v[50:51],s[54:55]\n"
"v_pk_mul_f32 v[52:53],v[52:53],s[54:55]\n"
"v_pk_mul_f32 v[54:55],v[54:55],s[54:55]\n"
CV "v155,v54,v55\n"
CV "v154,v52,v53\n"
CV "v153,v50,v51\n"
CV "v152,v48,v49\n"
"v_pk_mul_f32 v[40:41],v[40:41],s[54:55]\n"
"v_pk_mul_f32 v[42:43],v[42:43],s[54:55]\n"
"v_pk_mul_f32 v[44:45],v[44:45],s[54:55]\n"
"v_pk_mul_f32 v[46:47],v[46:47],s[54:55]\n"
CV "v151,v46,v47\n"
CV "v150,v44,v45\n"
CV "v149,v42,v43\n"
CV "v148,v40,v41\n"
"v_pk_mul_f32 v[32:33],v[32:33],s[54:55]\n"
"v_pk_mul_f32 v[34:35],v[34:35],s[54:55]\n"
"v_pk_mul_f32 v[36:37],v[36:37],s[54:55]\n"
"v_pk_mul_f32 v[38:39],v[38:39],s[54:55]\n"
CV "v147,v38,v39\n"
CV "v146,v36,v37\n"
CV "v145,v34,v35\n"
CV "v144,v32,v33\n"
"v_pk_mul_f32 v[24:25],v[24:25],s[54:55]\n"
"v_pk_mul_f32 v[26:27],v[26:27],s[54:55]\n"
"v_pk_mul_f32 v[28:29],v[28:29],s[54:55]\n"
"v_pk_mul_f32 v[30:31],v[30:31],s[54:55]\n"
CV "v143,v30,v31\n"
CV "v142,v28,v29\n"
CV "v141,v26,v27\n"
CV "v140,v24,v25\n"
"v_pk_mul_f32 v[16:17],v[16:17],s[54:55]\n"
"v_pk_mul_f32 v[18:19],v[18:19],s[54:55]\n"
"v_pk_mul_f32 v[20:21],v[20:21],s[54:55]\n"
"v_pk_mul_f32 v[22:23],v[22:23],s[54:55]\n"
CV "v139,v22,v23\n"
CV "v138,v20,v21\n"
CV "v137,v18,v19\n"
CV "v136,v16,v17\n"
"s_cmp_eq_u32 s57,s69\n"
"s_cbranch_scc1 .LattB\n"
AR "v2,a245\n"
"v_add_u32 v2,49152,v2\n"
"v_xor_b32 v3,32,v2\n"
"v_xor_b32 v4,64,v2\n"
"v_xor_b32 v5,96,v2\n"
"s_mov_b32 s61,7\n"
".Latt2x:\n"
"s_cmp_eq_u32 s57,1\n"
"s_cbranch_scc1 .Lt0kx\n"
".Lt0nx:\n"
"s_waitcnt lgkmcnt(2)\n"
MF "v[72:75],v[92:95],v[136:139],0\n"
"s_add_u32 s73,s73,12288\n"
"ds_read_b128 v[104:107],v1 offset:1024\n"
AR "v10,a224\n"
MF "v[76:79],v[92:95],v[140:143],0\n"
"s_cmp_eq_u32 s73,s19\n"
"s_cselect_b32 s73,s18,s73\n"
"s_add_u32 m0,s65,73728\n"
MF "v[80:83],v[92:95],v[144:147],0\n"
LD "v8,s[36:39],s73 offen nt lds\n"
MF "v[84:87],v[92:95],v[148:151],0\n"
"v_max_f32_dpp v10,|v10|,|v10| quad_perm:[1,0,3,2] row_mask:0xf bank_mask:0xf\n"
AR "v11,a225\n"
MF "v[88:91],v[92:95],v[152:155],0\n"
"s_waitcnt lgkmcnt(2)\n"
"ds_read_b128 v[92:95],v0 offset:2048\n"
AR "v12,a226\n"
MF "v[72:75],v[96:99],v[156:159],v[72:75]\n"
"s_add_u32 m0,s65,74624\n"
"v_max_f32_dpp v10,v10,v10 quad_perm:[2,3,0,1] row_mask:0xf bank_mask:0xf\n"
"v_max_f32_dpp v11,|v11|,|v11| quad_perm:[1,0,3,2] row_mask:0xf bank_mask:0xf\n"
MF "v[76:79],v[96:99],v[160:163],v[76:79]\n"
LD "v8,s[36:39],s73 offen offset:128 nt lds\n"
MF "v[80:83],v[96:99],v[164:167],v[80:83]\n"
"v_max_f32_dpp v10,v10,v10 row_half_mirror row_mask:0xf bank_mask:0xf\n"
"v_max_f32_dpp v11,v11,v11 quad_perm:[2,3,0,1] row_mask:0xf bank_mask:0xf\n"
MF "v[84:87],v[96:99],v[168:171],v[84:87]\n"
"v_readlane_b32 s13,v10,0\n"
"v_readlane_b32 s0,v10,24\n"
MF "v[88:91],v[96:99],v[172:175],v[88:91]\n"
"s_waitcnt lgkmcnt(2)\n"
"ds_read_b128 v[96:99],v1 offset:2048\n"
"v_readlane_b32 s1,v10,32\n"
MF "v[72:75],v[100:103],v[176:179],v[72:75]\n"
"s_add_u32 m0,s65,75520\n"
"v_readlane_b32 s2,v10,56\n"
"v_max_f32_dpp v11,v11,v11 row_half_mirror row_mask:0xf bank_mask:0xf\n"
MF "v[76:79],v[100:103],v[180:183],v[76:79]\n"
LD "v8,s[36:39],s73 offen offset:256 nt lds\n"
MF "v[80:83],v[100:103],v[184:187],v[80:83]\n"
"s_min_u32 s13,s13,s0\n"
"v_readlane_b32 s11,v11,0\n"
"v_readlane_b32 s3,v11,24\n"
MF "v[84:87],v[100:103],v[188:191],v[84:87]\n"
"s_min_u32 s1,s1,s2\n"
"v_readlane_b32 s4,v11,32\n"
"v_readlane_b32 s5,v11,56\n"
MF "v[88:91],v[100:103],v[192:195],v[88:91]\n"
"s_waitcnt lgkmcnt(2)\n"
"ds_read_b128 v[100:103],v0 offset:6144\n"
"v_max_f32_dpp v12,|v12|,|v12| quad_perm:[1,0,3,2] row_mask:0xf bank_mask:0xf\n"
MF "v[72:75],v[104:107],v[196:199],v[72:75]\n"
"s_add_u32 m0,s65,73728\n"
MF "v[76:79],v[104:107],v[200:203],v[76:79]\n"
LD "v8,s[36:39],s73 offen offset:3072 nt lds\n"
MF "v[80:83],v[104:107],v[204:207],v[80:83]\n"
"s_min_u32 s13,s13,s1\n"
"v_max_f32_dpp v12,v12,v12 quad_perm:[2,3,0,1] row_mask:0xf bank_mask:0xf\n"
MF "v[84:87],v[104:107],v[208:211],v[84:87]\n"
"s_and_b32 s13,s13,0x7f800000\n"
MF "v[88:91],v[104:107],v[212:215],v[88:91]\n"
"s_waitcnt lgkmcnt(2)\n"
"ds_read_b128 v[104:107],v1 offset:6144\n"
"v_max_f32_dpp v12,v12,v12 row_half_mirror row_mask:0xf bank_mask:0xf\n"
MF "v[72:75],v[92:95],v[216:219],v[72:75]\n"
"s_add_u32 m0,s65,74624\n"
"v_readlane_b32 s10,v12,0\n"
"v_readlane_b32 s6,v12,24\n"
MF "v[76:79],v[92:95],v[220:223],v[76:79]\n"
LD "v8,s[36:39],s73 offen offset:3200 nt lds\n"
MF "v[80:83],v[92:95],v[224:227],v[80:83]\n"
"s_sub_u32 s13,s13,0x14000000\n"
"v_readlane_b32 s7,v12,32\n"
"v_readlane_b32 s8,v12,56\n"
MF "v[84:87],v[92:95],v[228:231],v[84:87]\n"
"s_cmp_lt_u32 s13,0x50000000\n"
"s_cselect_b32 s13,s13,0xed400000\n"
MF "v[88:91],v[92:95],v[232:235],v[88:91]\n"
"s_waitcnt lgkmcnt(2)\n"
"ds_read_b128 v[92:95],v0 offset:7168\n"
MF "v[72:75],v[96:99],v[236:239],v[72:75]\n"
"s_add_u32 m0,s65,75520\n"
MF "v[76:79],v[96:99],v[240:243],v[76:79]\n"
LD "v8,s[36:39],s73 offen offset:3328 nt lds\n"
MF "v[80:83],v[96:99],v[244:247],v[80:83]\n"
"s_add_u32 s13,s13,0x12c00000\n"
MF "v[84:87],v[96:99],v[248:251],v[84:87]\n"
"s_min_u32 s11,s11,s3\n"
MF "v[88:91],v[96:99],v[252:255],v[88:91]\n"
"s_waitcnt lgkmcnt(2)\n"
"ds_read_b128 v[96:99],v1 offset:7168\n"
MF "v[112:115],v[100:103],v[136:139],0\n"
"s_min_u32 s4,s4,s5\n"
MF "v[116:119],v[100:103],v[140:143],0\n"
"s_min_u32 s11,s11,s4\n"
MF "v[120:123],v[100:103],v[144:147],0\n"
"s_and_b32 s11,s11,0x7f800000\n"
MF "v[124:127],v[100:103],v[148:151],0\n"
"s_sub_u32 s11,s11,0x14000000\n"
MF "v[128:131],v[100:103],v[152:155],0\n"
"s_waitcnt lgkmcnt(2)\n"
"ds_read_b128 v[100:103],v0 offset:8192\n"
MF "v[112:115],v[104:107],v[156:159],v[112:115]\n"
"s_min_u32 s10,s10,s6\n"
"v_max3_f32 v52,v72,v73,v74\n"
"v_max_f32 v52,v52,v75\n"
MF "v[116:119],v[104:107],v[160:163],v[116:119]\n"
"s_min_u32 s7,s7,s8\n"
"v_max3_f32 v53,v76,v77,v78\n"
"v_max_f32 v53,v53,v79\n"
MF "v[120:123],v[104:107],v[164:167],v[120:123]\n"
"s_min_u32 s10,s10,s7\n"
"v_max3_f32 v54,v80,v81,v82\n"
"v_max_f32 v54,v54,v83\n"
MF "v[124:127],v[104:107],v[168:171],v[124:127]\n"
"v_max3_f32 v55,v84,v85,v86\n"
"v_max_f32 v55,v55,v87\n"
MF "v[128:131],v[104:107],v[172:175],v[128:131]\n"
"s_waitcnt lgkmcnt(2)\n"
"ds_read_b128 v[104:107],v1 offset:8192\n"
"v_max3_f32 v56,v88,v89,v90\n"
MF "v[112:115],v[92:95],v[176:179],v[112:115]\n"
".Lpg9b:\n"
"s_cbranch_execz .Lpgpsl_b\n"
"s_cmp_lt_u32 s11,0x50000000\n"
"s_cselect_b32 s11,s11,0xed400000\n"
"v_max_f32 v56,v56,v91\n"
MF "v[116:119],v[92:95],v[180:183],v[116:119]\n"
"s_add_u32 s11,s11,0x12c00000\n"
MF "v[120:123],v[92:95],v[184:187],v[120:123]\n"
MF "v[124:127],v[92:95],v[188:191],v[124:127]\n"
MF "v[128:131],v[92:95],v[192:195],v[128:131]\n"
"s_waitcnt lgkmcnt(2)\n"
MF "v[112:115],v[96:99],v[196:199],v[112:115]\n"
"s_waitcnt lgkmcnt(0)\n"
MF "v[116:119],v[96:99],v[200:203],v[116:119]\n"
"s_barrier\n"
MF "v[120:123],v[96:99],v[204:207],v[120:123]\n"
"s_add_u32 s72,s72,6144\n"
MF "v[124:127],v[96:99],v[208:211],v[124:127]\n"
"s_cmp_eq_u32 s72,s17\n"
"s_cselect_b32 s72,s16,s72\n"
"s_add_u32 m0,s64,0\n"
MF "v[128:131],v[96:99],v[212:215],v[128:131]\n"
LD "v7,s[32:35],s72 offen nt lds\n"
MF "v[112:115],v[100:103],v[216:219],v[112:115]\n"
"s_add_u32 m0,s64,896\n"
MF "v[116:119],v[100:103],v[220:223],v[116:119]\n"
LD "v7,s[32:35],s72 offen offset:128 nt lds\n"
MF "v[120:123],v[100:103],v[224:227],v[120:123]\n"
"s_add_u32 m0,s64,1792\n"
MF "v[124:127],v[100:103],v[228:231],v[124:127]\n"
LD "v7,s[32:35],s72 offen offset:256 nt lds\n"
MF "v[128:131],v[100:103],v[232:235],v[128:131]\n"
"s_add_u32 s72,s72,6144\n"
"s_add_u32 m0,s64,6144\n"
MF "v[112:115],v[104:107],v[236:239],v[112:115]\n"
LD "v7,s[32:35],s72 offen nt lds\n"
MF "v[116:119],v[104:107],v[240:243],v[116:119]\n"
"s_add_u32 m0,s64,7040\n"
MF "v[120:123],v[104:107],v[244:247],v[120:123]\n"
LD "v7,s[32:35],s72 offen offset:128 nt lds\n"
MF "v[124:127],v[104:107],v[248:251],v[124:127]\n"
"s_add_u32 m0,s64,7936\n"
MF "v[128:131],v[104:107],v[252:255],v[128:131]\n"
LD "v7,s[32:35],s72 offen offset:256 nt lds\n"
"s_branch .Lt0ex\n"
".Lpgt0kx_d:\n"
"s_cbranch_execz .Lpg16d\n"
".Lt0kx:\n"
"s_lshl_b32 s58,s56,10\n"
"s_add_u32 s58,s58,73728\n"
"v_add_u32 v32,s58,v9\n"
"v_add_u32 v33,65536,v32\n"
"s_waitcnt lgkmcnt(2)\n"
MF "v[72:75],v[92:95],v[136:139],0\n"
"s_add_u32 s73,s73,12288\n"
"ds_read_b128 v[104:107],v1 offset:1024\n"
AR "v10,a224\n"
MF "v[76:79],v[92:95],v[140:143],0\n"
"s_cmp_eq_u32 s73,s19\n"
"s_cselect_b32 s73,s18,s73\n"
"s_add_u32 m0,s65,73728\n"
MF "v[80:83],v[92:95],v[144:147],0\n"
LD "v8,s[36:39],s73 offen nt lds\n"
MF "v[84:87],v[92:95],v[148:151],0\n"
"v_max_f32_dpp v10,|v10|,|v10| quad_perm:[1,0,3,2] row_mask:0xf bank_mask:0xf\n"
AR "v11,a225\n"
MF "v[88:91],v[92:95],v[152:155],0\n"
"s_waitcnt lgkmcnt(2)\n"
"ds_read_b128 v[92:95],v0 offset:2048\n"
AR "v12,a226\n"
MF "v[72:75],v[96:99],v[156:159],v[72:75]\n"
"s_add_u32 m0,s65,74624\n"
"v_max_f32_dpp v10,v10,v10 quad_perm:[2,3,0,1] row_mask:0xf bank_mask:0xf\n"
"v_max_f32_dpp v11,|v11|,|v11| quad_perm:[1,0,3,2] row_mask:0xf bank_mask:0xf\n"
MF "v[76:79],v[96:99],v[160:163],v[76:79]\n"
LD "v8,s[36:39],s73 offen offset:128 nt lds\n"
MF "v[80:83],v[96:99],v[164:167],v[80:83]\n"
"v_max_f32_dpp v10,v10,v10 row_half_mirror row_mask:0xf bank_mask:0xf\n"
"v_max_f32_dpp v11,v11,v11 quad_perm:[2,3,0,1] row_mask:0xf bank_mask:0xf\n"
MF "v[84:87],v[96:99],v[168:171],v[84:87]\n"
"v_readlane_b32 s13,v10,0\n"
"v_readlane_b32 s0,v10,24\n"
MF "v[88:91],v[96:99],v[172:175],v[88:91]\n"
"s_waitcnt lgkmcnt(2)\n"
"ds_read_b128 v[96:99],v1 offset:2048\n"
"v_readlane_b32 s1,v10,32\n"
MF "v[72:75],v[100:103],v[176:179],v[72:75]\n"
"s_add_u32 m0,s65,75520\n"
"v_readlane_b32 s2,v10,56\n"
"v_max_f32_dpp v11,v11,v11 row_half_mirror row_mask:0xf bank_mask:0xf\n"
MF "v[76:79],v[100:103],v[180:183],v[76:79]\n"
LD "v8,s[36:39],s73 offen offset:256 nt lds\n"
MF "v[80:83],v[100:103],v[184:187],v[80:83]\n"
"s_min_u32 s13,s13,s0\n"
"v_readlane_b32 s11,v11,0\n"
"v_readlane_b32 s3,v11,24\n"
MF "v[84:87],v[100:103],v[188:191],v[84:87]\n"
"s_min_u32 s1,s1,s2\n"
"v_readlane_b32 s4,v11,32\n"
"v_readlane_b32 s5,v11,56\n"
MF "v[88:91],v[100:103],v[192:195],v[88:91]\n"
"s_waitcnt lgkmcnt(2)\n"
"ds_read_b128 v[100:103],v0 offset:6144\n"
"v_max_f32_dpp v12,|v12|,|v12| quad_perm:[1,0,3,2] row_mask:0xf bank_mask:0xf\n"
MF "v[72:75],v[104:107],v[196:199],v[72:75]\n"
"s_add_u32 m0,s65,73728\n"
"ds_write_b128 v32,a[0:3] offset:24576\n"
MF "v[76:79],v[104:107],v[200:203],v[76:79]\n"
LD "v8,s[36:39],s73 offen offset:3072 nt lds\n"
MF "v[80:83],v[104:107],v[204:207],v[80:83]\n"
"s_min_u32 s13,s13,s1\n"
"v_max_f32_dpp v12,v12,v12 quad_perm:[2,3,0,1] row_mask:0xf bank_mask:0xf\n"
AR "v62,a20\n"
MF "v[84:87],v[104:107],v[208:211],v[84:87]\n"
"s_and_b32 s13,s13,0x7f800000\n"
"ds_write_b128 v32,a[4:7] offset:28672\n"
MF "v[88:91],v[104:107],v[212:215],v[88:91]\n"
"s_waitcnt lgkmcnt(4)\n"
"ds_read_b128 v[104:107],v1 offset:6144\n"
"v_max_f32_dpp v12,v12,v12 row_half_mirror row_mask:0xf bank_mask:0xf\n"
MF "v[72:75],v[92:95],v[216:219],v[72:75]\n"
"s_add_u32 m0,s65,74624\n"
"v_readlane_b32 s10,v12,0\n"
"v_readlane_b32 s6,v12,24\n"
MF "v[76:79],v[92:95],v[220:223],v[76:79]\n"
LD "v8,s[36:39],s73 offen offset:3200 nt lds\n"
MF "v[80:83],v[92:95],v[224:227],v[80:83]\n"
"s_sub_u32 s13,s13,0x14000000\n"
"v_readlane_b32 s7,v12,32\n"
"v_readlane_b32 s8,v12,56\n"
MF "v[84:87],v[92:95],v[228:231],v[84:87]\n"
"s_cmp_lt_u32 s13,0x50000000\n"
"s_cselect_b32 s13,s13,0xed400000\n"
AR "v63,a21\n"
MF "v[88:91],v[92:95],v[232:235],v[88:91]\n"
"s_waitcnt lgkmcnt(4)\n"
"ds_read_b128 v[92:95],v0 offset:7168\n"
AR "v64,a22\n"
MF "v[72:75],v[96:99],v[236:239],v[72:75]\n"
"s_add_u32 m0,s65,75520\n"
"ds_write_b128 v32,a[8:11] offset:32768\n"
MF "v[76:79],v[96:99],v[240:243],v[76:79]\n"
LD "v8,s[36:39],s73 offen offset:3328 nt lds\n"
MF "v[80:83],v[96:99],v[244:247],v[80:83]\n"
"s_add_u32 s13,s13,0x12c00000\n"
"ds_write_b128 v32,a[12:15] offset:36864\n"
MF "v[84:87],v[96:99],v[248:251],v[84:87]\n"
"s_min_u32 s11,s11,s3\n"
"ds_write_b128 v32,a[16:19] offset:40960\n"
MF "v[88:91],v[96:99],v[252:255],v[88:91]\n"
"s_waitcnt lgkmcnt(7)\n"
"ds_read_b128 v[96:99],v1 offset:7168\n"
AR "v65,a23\n"
MF "v[112:115],v[100:103],v[136:139],0\n"
"s_min_u32 s4,s4,s5\n"
"ds_write_b128 v32,a[40:43] offset:45056\n"
MF "v[116:119],v[100:103],v[140:143],0\n"
"s_min_u32 s11,s11,s4\n"
"ds_write_b128 v32,a[44:47] offset:49152\n"
MF "v[120:123],v[100:103],v[144:147],0\n"
"s_and_b32 s11,s11,0x7f800000\n"
"ds_write_b128 v32,a[48:51] offset:53248\n"
MF "v[124:127],v[100:103],v[148:151],0\n"
"s_sub_u32 s11,s11,0x14000000\n"
"ds_write_b128 v32,a[52:55] offset:57344\n"
MF "v[128:131],v[100:103],v[152:155],0\n"
"s_waitcnt lgkmcnt(9)\n"
"ds_read_b128 v[100:103],v0 offset:8192\n"
AR "v66,a24\n"
MF "v[112:115],v[104:107],v[156:159],v[112:115]\n"
"s_min_u32 s10,s10,s6\n"
"v_max3_f32 v52,v72,v73,v74\n"
"v_max_f32 v52,v52,v75\n"
MF "v[116:119],v[104:107],v[160:163],v[116:119]\n"
"s_min_u32 s7,s7,s8\n"
"v_max3_f32 v53,v76,v77,v78\n"
"v_max_f32 v53,v53,v79\n"
MF "v[120:123],v[104:107],v[164:167],v[120:123]\n"
"s_min_u32 s10,s10,s7\n"
"v_max3_f32 v54,v80,v81,v82\n"
"v_max_f32 v54,v54,v83\n"
MF "v[124:127],v[104:107],v[168:171],v[124:127]\n"
"v_max3_f32 v55,v84,v85,v86\n"
"v_max_f32 v55,v55,v87\n"
MF "v[128:131],v[104:107],v[172:175],v[128:131]\n"
"s_waitcnt lgkmcnt(9)\n"
"ds_read_b128 v[104:107],v1 offset:8192\n"
"v_max3_f32 v56,v88,v89,v90\n"
MF "v[112:115],v[92:95],v[176:179],v[112:115]\n"
"s_cmp_lt_u32 s11,0x50000000\n"
"s_cselect_b32 s11,s11,0xed400000\n"
"v_max_f32 v56,v56,v91\n"
MF "v[116:119],v[92:95],v[180:183],v[116:119]\n"
"s_add_u32 s11,s11,0x12c00000\n"
"ds_write_b128 v32,a[56:59] offset:61440\n"
MF "v[120:123],v[92:95],v[184:187],v[120:123]\n"
"ds_write_b128 v33,a[80:83]\n"
MF "v[124:127],v[92:95],v[188:191],v[124:127]\n"
"ds_write_b128 v33,a[84:87] offset:4096\n"
MF "v[128:131],v[92:95],v[192:195],v[128:131]\n"
"s_waitcnt lgkmcnt(9)\n"
"ds_write_b128 v33,a[88:91] offset:8192\n"
MF "v[112:115],v[96:99],v[196:199],v[112:115]\n"
"s_waitcnt lgkmcnt(0)\n"
AR "v67,a25\n"
AR "v68,a26\n"
MF "v[116:119],v[96:99],v[200:203],v[116:119]\n"
"s_barrier\n"
MF "v[120:123],v[96:99],v[204:207],v[120:123]\n"
"s_add_u32 s72,s72,6144\n"
"ds_write_b128 v33,a[92:95] offset:12288\n"
MF "v[124:127],v[96:99],v[208:211],v[124:127]\n"
"s_cmp_eq_u32 s72,s17\n"
"s_cselect_b32 s72,s16,s72\n"
"s_add_u32 m0,s64,0\n"
MF "v[128:131],v[96:99],v[212:215],v[128:131]\n"
LD "v7,s[32:35],s72 offen nt lds\n"
MF "v[112:115],v[100:103],v[216:219],v[112:115]\n"
"s_add_u32 m0,s64,896\n"
"ds_write_b128 v33,a[96:99] offset:16384\n"
MF "v[116:119],v[100:103],v[220:223],v[116:119]\n"
LD "v7,s[32:35],s72 offen offset:128 nt lds\n"
MF "v[120:123],v[100:103],v[224:227],v[120:123]\n"
"s_add_u32 m0,s64,1792\n"
AR "v69,a27\n"
AR "v70,a28\n"
MF "v[124:127],v[100:103],v[228:231],v[124:127]\n"
LD "v7,s[32:35],s72 offen offset:256 nt lds\n"
MF "v[128:131],v[100:103],v[232:235],v[128:131]\n"
"s_add_u32 s72,s72,6144\n"
"s_add_u32 m0,s64,6144\n"
AR "v71,a29\n"
MF "v[112:115],v[104:107],v[236:239],v[112:115]\n"
LD "v7,s[32:35],s72 offen nt lds\n"
MF "v[116:119],v[104:107],v[240:243],v[116:119]\n"
"s_add_u32 m0,s64,7040\n"
"ds_write_b128 v33,a[120:123] offset:20480\n"
MF "v[120:123],v[104:107],v[244:247],v[120:123]\n"
LD "v7,s[32:35],s72 offen offset:128 nt lds\n"
MF "v[124:127],v[104:107],v[248:251],v[124:127]\n"
"s_add_u32 m0,s64,7936\n"
AR "v132,a30\n"
AR "v133,a31\n"
MF "v[128:131],v[104:107],v[252:255],v[128:131]\n"
LD "v7,s[32:35],s72 offen offset:256 nt lds\n"
AR "v134,a32\n"
AR "v135,a33\n"
AR "v6,a34\n"
AR "v13,a35\n"
AR "v14,a36\n"
AR "v15,a37\n"
"v_accvgpr_mov_b32 a254,a38\n"
"v_accvgpr_mov_b32 a255,a39\n"
"v_accvgpr_mov_b32 a240,a168\n"
"v_accvgpr_mov_b32 a241,a169\n"
"v_accvgpr_mov_b32 a242,a170\n"
"v_accvgpr_mov_b32 a243,a171\n"
"v_accvgpr_mov_b32 a244,a172\n"
"v_accvgpr_mov_b32 a245,a173\n"
"v_accvgpr_mov_b32 a246,a174\n"
"v_accvgpr_mov_b32 a247,a175\n"
"v_accvgpr_mov_b32 a248,a176\n"
"v_accvgpr_mov_b32 a249,a177\n"
"v_accvgpr_mov_b32 a250,a178\n"
"v_accvgpr_mov_b32 a251,a179\n"
".Lpgt0ex_b:\n"
"s_cbranch_execz .Lpg2a\n"
".Lt0ex:\n"
"s_nop 1\n"
"v_max3_f32 v52,v52,v112,v113\n"
"v_max3_f32 v53,v53,v116,v117\n"
"v_max3_f32 v54,v54,v120,v121\n"
"v_max3_f32 v55,v55,v124,v125\n"
"v_max3_f32 v56,v56,v128,v129\n"
"v_max3_f32 v52,v52,v114,v115\n"
"v_max3_f32 v53,v53,v118,v119\n"
"v_max3_f32 v54,v54,v122,v123\n"
"v_max3_f32 v55,v55,v126,v127\n"
"v_max3_f32 v56,v56,v130,v131\n"
"v_mov_b32 v57,v52\n"
"v_mov_b32 v58,v53\n"
"v_mov_b32 v59,v54\n"
"v_mov_b32 v60,v55\n"
"v_mov_b32 v61,v56\n"
"v_permlane16_swap_b32 v52,v57\n"
"v_permlane16_swap_b32 v53,v58\n"
"v_permlane16_swap_b32 v54,v59\n"
"v_permlane16_swap_b32 v55,v60\n"
"v_permlane16_swap_b32 v56,v61\n"
"v_max_f32 v52,v52,v57\n"
"v_max_f32 v53,v53,v58\n"
"v_max_f32 v54,v54,v59\n"
"v_max_f32 v55,v55,v60\n"
"v_max_f32 v56,v56,v61\n"
"v_mov_b32 v57,v52\n"
"v_mov_b32 v58,v53\n"
"v_mov_b32 v59,v54\n"
"v_mov_b32 v60,v55\n"
"v_mov_b32 v61,v56\n"
"v_permlane32_swap_b32 v52,v57\n"
"v_permlane32_swap_b32 v53,v58\n"
"v_permlane32_swap_b32 v54,v59\n"
"v_permlane32_swap_b32 v55,v60\n"
"v_permlane32_swap_b32 v56,v61\n"
"v_max_f32 v52,v52,v57\n"
"v_max_f32 v53,v53,v58\n"
"v_max_f32 v54,v54,v59\n"
"v_max_f32 v55,v55,v60\n"
"v_max_f32 v56,v56,v61\n"
"v_sub_f32 v32,0xc1300000,v52\n"
"v_sub_f32 v36,0xc1300000,v53\n"
"v_sub_f32 v40,0xc1300000,v54\n"
"v_sub_f32 v44,0xc1300000,v55\n"
"v_sub_f32 v48,0xc1300000,v56\n"
"v_mov_b32 v33,v32\n"
"v_pk_mov_b32 v[34:35],v[32:33],v[32:33]\n"
"v_mov_b32 v37,v36\n"
"v_pk_mov_b32 v[38:39],v[36:37],v[36:37]\n"
"v_mov_b32 v41,v40\n"
"v_pk_mov_b32 v[42:43],v[40:41],v[40:41]\n"
"v_mov_b32 v45,v44\n"
"v_pk_mov_b32 v[46:47],v[44:45],v[44:45]\n"
"v_mov_b32_e64 v49,v48\n"
"v_pk_mov_b32 v[50:51],v[48:49],v[48:49]\n"
"v_pk_add_f32 v[72:73],v[72:73],v[32:33]\n"
"v_pk_add_f32 v[74:75],v[74:75],v[32:33]\n"
"v_pk_add_f32 v[112:113],v[112:113],v[32:33]\n"
"v_pk_add_f32 v[114:115],v[114:115],v[32:33]\n"
"v_exp_f32 v72,v72\n"
"v_exp_f32 v73,v73\n"
"v_exp_f32 v74,v74\n"
"v_exp_f32 v75,v75\n"
"v_exp_f32 v112,v112\n"
"v_exp_f32 v113,v113\n"
"v_exp_f32 v114,v114\n"
"v_exp_f32 v115,v115\n"
"v_pk_add_f32 v[10:11],v[72:73],v[74:75]\n"
"v_pk_add_f32 v[28:29],v[112:113],v[114:115]\n"
"v_pk_add_f32 v[10:11],v[10:11],v[28:29]\n"
"v_add_f32_e64 v52,v10,v11\n"
CV "v72,v72,v73\n"
CV "v73,v74,v75\n"
CV "v74,v112,v113\n"
CV "v75,v114,v115\n"
"v_pk_add_f32 v[76:77],v[76:77],v[36:37]\n"
"v_pk_add_f32 v[78:79],v[78:79],v[36:37]\n"
"v_pk_add_f32 v[116:117],v[116:117],v[36:37]\n"
"v_pk_add_f32 v[118:119],v[118:119],v[36:37]\n"
"v_exp_f32 v76,v76\n"
"v_exp_f32 v77,v77\n"
"v_exp_f32 v78,v78\n"
"v_exp_f32 v79,v79\n"
"v_exp_f32 v116,v116\n"
"v_exp_f32 v117,v117\n"
"v_exp_f32 v118,v118\n"
"v_exp_f32 v119,v119\n"
"v_pk_add_f32 v[10:11],v[76:77],v[78:79]\n"
"v_pk_add_f32 v[28:29],v[116:117],v[118:119]\n"
"v_pk_add_f32 v[10:11],v[10:11],v[28:29]\n"
"v_add_f32_e64 v53,v10,v11\n"
CV "v76,v76,v77\n"
CV "v77,v78,v79\n"
CV "v78,v116,v117\n"
CV "v79,v118,v119\n"
"v_pk_add_f32 v[80:81],v[80:81],v[40:41]\n"
"v_pk_add_f32 v[82:83],v[82:83],v[40:41]\n"
"v_pk_add_f32 v[120:121],v[120:121],v[40:41]\n"
"v_pk_add_f32 v[122:123],v[122:123],v[40:41]\n"
"v_exp_f32 v80,v80\n"
"v_exp_f32 v81,v81\n"
"v_exp_f32 v82,v82\n"
"v_exp_f32 v83,v83\n"
"v_exp_f32 v120,v120\n"
"v_exp_f32 v121,v121\n"
"v_exp_f32 v122,v122\n"
"v_exp_f32 v123,v123\n"
"v_pk_add_f32 v[10:11],v[80:81],v[82:83]\n"
"v_pk_add_f32 v[28:29],v[120:121],v[122:123]\n"
"v_pk_add_f32 v[10:11],v[10:11],v[28:29]\n"
"v_add_f32_e64 v54,v10,v11\n"
CV "v80,v80,v81\n"
CV "v81,v82,v83\n"
CV "v82,v120,v121\n"
CV "v83,v122,v123\n"
"v_pk_add_f32 v[84:85],v[84:85],v[44:45]\n"
"v_pk_add_f32 v[86:87],v[86:87],v[44:45]\n"
"v_pk_add_f32 v[124:125],v[124:125],v[44:45]\n"
"v_pk_add_f32 v[126:127],v[126:127],v[44:45]\n"
"v_mov_b32_e64 v55,0\n"
"v_pk_add_f32 v[88:89],v[88:89],v[48:49]\n"
"v_pk_add_f32 v[90:91],v[90:91],v[48:49]\n"
"v_pk_add_f32 v[128:129],v[128:129],v[48:49]\n"
"v_pk_add_f32 v[130:131],v[130:131],v[48:49]\n"
"v_mov_b32 v56,0\n"
"s_waitcnt vmcnt(18) lgkmcnt(0)\n"
"s_barrier\n"
"ds_read_b128 v[16:19],v0 offset:24576\n"
"ds_read_b128 v[20:23],v1 offset:24576\n"
AW "a220,s13\n"
AW "a221,s13\n"
AW "a222,s13\n"
AW "a223,s13\n"
"s_waitcnt lgkmcnt(1)\n"
MF "v[92:95],v[16:19],v[136:139],v[32:35]\n"
"s_nop 0\n"
"ds_read_b128 v[24:27],v0 offset:25600\n"
"ds_read_b128 v[28:31],v1 offset:25600\n"
"s_waitcnt lgkmcnt(2)\n"
MF "v[92:95],v[20:23],v[156:159],v[92:95]\n"
"v_exp_f32 v84,v84\n"
MF "v[96:99],v[20:23],v[160:163],v[36:39]\n"
"v_exp_f32 v85,v85\n"
MF "v[96:99],v[16:19],v[140:143],v[96:99]\n"
"v_exp_f32 v86,v86\n"
MF "v[100:103],v[16:19],v[144:147],v[40:43]\n"
"v_exp_f32 v87,v87\n"
MF "v[100:103],v[20:23],v[164:167],v[100:103]\n"
"v_add_f32 v55,v55,v84\n"
"v_add_f32 v55,v55,v85\n"
MF "v[104:107],v[20:23],v[168:171],v[44:47]\n"
CV "v84,v84,v85\n"
"v_add_f32 v55,v55,v86\n"
MF "v[104:107],v[16:19],v[148:151],v[104:107]\n"
"v_add_f32 v55,v55,v87\n"
CV "v85,v86,v87\n"
MF "v[108:111],v[16:19],v[152:155],v[48:51]\n"
"v_exp_f32 v124,v124\n"
MF "v[108:111],v[20:23],v[172:175],v[108:111]\n"
"s_waitcnt lgkmcnt(0)\n"
"ds_read_b128 v[16:19],v0 offset:26624\n"
"ds_read_b128 v[20:23],v1 offset:26624\n"
MF "v[108:111],v[24:27],v[192:195],v[108:111]\n"
"v_exp_f32 v125,v125\n"
MF "v[108:111],v[28:31],v[212:215],v[108:111]\n"
"v_exp_f32 v126,v126\n"
MF "v[104:107],v[28:31],v[208:211],v[104:107]\n"
"v_exp_f32 v127,v127\n"
MF "v[104:107],v[24:27],v[188:191],v[104:107]\n"
"v_add_f32 v55,v55,v124\n"
"v_add_f32 v55,v55,v125\n"
MF "v[100:103],v[24:27],v[184:187],v[100:103]\n"
CV "v86,v124,v125\n"
"v_add_f32 v55,v55,v126\n"
MF "v[100:103],v[28:31],v[204:207],v[100:103]\n"
"v_add_f32 v55,v55,v127\n"
CV "v87,v126,v127\n"
MF "v[96:99],v[28:31],v[200:203],v[96:99]\n"
"v_exp_f32 v88,v88\n"
MF "v[96:99],v[24:27],v[180:183],v[96:99]\n"
"v_exp_f32 v89,v89\n"
MF "v[92:95],v[24:27],v[176:179],v[92:95]\n"
"v_exp_f32 v90,v90\n"
MF "v[92:95],v[28:31],v[196:199],v[92:95]\n"
"s_waitcnt lgkmcnt(0)\n"
"ds_read_b128 v[24:27],v0 offset:30720\n"
"ds_read_b128 v[28:31],v1 offset:30720\n"
MF "v[92:95],v[16:19],v[216:219],v[92:95]\n"
"v_exp_f32 v91,v91\n"
MF "v[92:95],v[20:23],v[236:239],v[92:95]\n"
"v_add_f32 v56,v56,v88\n"
"v_add_f32 v56,v56,v89\n"
MF "v[96:99],v[20:23],v[240:243],v[96:99]\n"
CV "v88,v88,v89\n"
"v_add_f32 v56,v56,v90\n"
MF "v[96:99],v[16:19],v[220:223],v[96:99]\n"
"v_add_f32 v56,v56,v91\n"
CV "v89,v90,v91\n"
MF "v[100:103],v[16:19],v[224:227],v[100:103]\n"
"v_exp_f32 v128,v128\n"
MF "v[100:103],v[20:23],v[244:247],v[100:103]\n"
"v_exp_f32 v129,v129\n"
MF "v[104:107],v[20:23],v[248:251],v[104:107]\n"
"v_exp_f32 v130,v130\n"
MF "v[104:107],v[16:19],v[228:231],v[104:107]\n"
"v_exp_f32 v131,v131\n"
MF "v[108:111],v[16:19],v[232:235],v[108:111]\n"
"v_add_f32 v56,v56,v128\n"
"v_add_f32 v56,v56,v129\n"
MF "v[108:111],v[20:23],v[252:255],v[108:111]\n"
"s_waitcnt lgkmcnt(0)\n"
"ds_read_b128 v[16:19],v0 offset:31744\n"
"ds_read_b128 v[20:23],v1 offset:31744\n"
MF "v[112:115],v[24:27],v[136:139],v[32:35]\n"
CV "v90,v128,v129\n"
"v_add_f32 v56,v56,v130\n"
MF "v[112:115],v[28:31],v[156:159],v[112:115]\n"
"v_add_f32 v56,v56,v131\n"
CV "v91,v130,v131\n"
MF "v[116:119],v[28:31],v[160:163],v[36:39]\n"
"v_exp_f32 v92,v92\n"
MF "v[116:119],v[24:27],v[140:143],v[116:119]\n"
"v_exp_f32 v93,v93\n"
MF "v[120:123],v[24:27],v[144:147],v[40:43]\n"
"v_exp_f32 v94,v94\n"
MF "v[120:123],v[28:31],v[164:167],v[120:123]\n"
"v_exp_f32 v95,v95\n"
MF "v[124:127],v[28:31],v[168:171],v[44:47]\n"
"v_add_f32 v57,v52,v92\n"
"v_add_f32 v57,v57,v93\n"
MF "v[124:127],v[24:27],v[148:151],v[124:127]\n"
CV "v92,v92,v93\n"
"v_add_f32 v57,v57,v94\n"
MF "v[128:131],v[24:27],v[152:155],v[48:51]\n"
"v_add_f32 v57,v57,v95\n"
CV "v93,v94,v95\n"
MF "v[128:131],v[28:31],v[172:175],v[128:131]\n"
"s_waitcnt lgkmcnt(0)\n"
"ds_read_b128 v[24:27],v0 offset:32768\n"
"ds_read_b128 v[28:31],v1 offset:32768\n"
MF "v[128:131],v[16:19],v[192:195],v[128:131]\n"
"v_exp_f32 v96,v96\n"
MF "v[128:131],v[20:23],v[212:215],v[128:131]\n"
"v_exp_f32 v97,v97\n"
MF "v[124:127],v[20:23],v[208:211],v[124:127]\n"
"v_exp_f32 v98,v98\n"
MF "v[124:127],v[16:19],v[188:191],v[124:127]\n"
"v_exp_f32 v99,v99\n"
MF "v[120:123],v[16:19],v[184:187],v[120:123]\n"
"v_add_f32 v58,v53,v96\n"
"v_add_f32 v58,v58,v97\n"
MF "v[120:123],v[20:23],v[204:207],v[120:123]\n"
CV "v96,v96,v97\n"
"v_add_f32 v58,v58,v98\n"
MF "v[116:119],v[20:23],v[200:203],v[116:119]\n"
"s_waitcnt vmcnt(12) lgkmcnt(0)\n"
"v_add_f32 v58,v58,v99\n"
CV "v97,v98,v99\n"
MF "v[116:119],v[16:19],v[180:183],v[116:119]\n"
"s_barrier\n"
MF "v[112:115],v[16:19],v[176:179],v[112:115]\n"
"v_exp_f32 v100,v100\n"
MF "v[112:115],v[20:23],v[196:199],v[112:115]\n"
"s_nop 0\n"
TR "v[16:17],v2\n"
TR "v[18:19],v2 offset:6144\n"
MF "v[112:115],v[24:27],v[216:219],v[112:115]\n"
"s_add_u32 s72,s72,6144\n"
TR "v[20:21],v3\n"
TR "v[22:23],v3 offset:6144\n"
MF "v[112:115],v[28:31],v[236:239],v[112:115]\n"
"s_cmp_eq_u32 s72,s17\n"
"s_cselect_b32 s72,s16,s72\n"
"s_add_u32 m0,s64,24576\n"
MF "v[116:119],v[28:31],v[240:243],v[116:119]\n"
LD "v7,s[32:35],s72 offen nt lds\n"
MF "v[116:119],v[24:27],v[220:223],v[116:119]\n"
"s_add_u32 m0,s64,25472\n"
"v_exp_f32 v101,v101\n"
MF "v[120:123],v[24:27],v[224:227],v[120:123]\n"
LD "v7,s[32:35],s72 offen offset:128 nt lds\n"
MF "v[120:123],v[28:31],v[244:247],v[120:123]\n"
"v_exp_f32 v102,v102\n"
MF "v[124:127],v[28:31],v[248:251],v[124:127]\n"
"s_add_u32 m0,s64,26368\n"
"v_exp_f32 v103,v103\n"
MF "v[124:127],v[24:27],v[228:231],v[124:127]\n"
LD "v7,s[32:35],s72 offen offset:256 nt lds\n"
MF "v[128:131],v[24:27],v[232:235],v[128:131]\n"
"s_add_u32 s72,s72,6144\n"
"v_add_f32 v59,v54,v100\n"
"v_add_f32 v59,v59,v101\n"
MF "v[128:131],v[28:31],v[252:255],v[128:131]\n"
"s_waitcnt lgkmcnt(0)\n"
TR "v[24:25],v4\n"
TR "v[26:27],v4 offset:6144\n"
MF "a[0:3],v[16:19],v[72:75],a[220:223]\n"
"s_add_u32 m0,s64,30720\n"
TR "v[28:29],v5\n"
TR "v[30:31],v5 offset:6144\n"
MF "a[20:23],v[20:23],v[72:75],a[220:223]\n"
LD "v7,s[32:35],s72 offen nt lds\n"
MF "a[24:27],v[20:23],v[76:79],a[220:223]\n"
CV "v100,v100,v101\n"
"v_add_f32 v59,v59,v102\n"
MF "a[4:7],v[16:19],v[76:79],a[220:223]\n"
"s_add_u32 m0,s64,31616\n"
"v_add_f32 v59,v59,v103\n"
CV "v101,v102,v103\n"
MF "a[8:11],v[16:19],v[80:83],a[220:223]\n"
LD "v7,s[32:35],s72 offen offset:128 nt lds\n"
MF "a[28:31],v[20:23],v[80:83],a[220:223]\n"
"v_exp_f32 v104,v104\n"
MF "a[32:35],v[20:23],v[84:87],a[220:223]\n"
"s_add_u32 m0,s64,32512\n"
"v_exp_f32 v105,v105\n"
MF "a[12:15],v[16:19],v[84:87],a[220:223]\n"
LD "v7,s[32:35],s72 offen offset:256 nt lds\n"
MF "a[16:19],v[16:19],v[88:91],a[220:223]\n"
"v_exp_f32 v106,v106\n"
MF "a[36:39],v[20:23],v[88:91],a[220:223]\n"
"s_waitcnt lgkmcnt(0)\n"
TR "v[16:17],v2 offset:1024\n"
TR "v[18:19],v2 offset:7168\n"
MF "a[56:59],v[24:27],v[88:91],a[220:223]\n"
"s_nop 0\n"
TR "v[20:21],v3 offset:1024\n"
TR "v[22:23],v3 offset:7168\n"
MF "a[76:79],v[28:31],v[88:91],a[220:223]\n"
"v_exp_f32 v107,v107\n"
MF "a[72:75],v[28:31],v[84:87],a[220:223]\n"
"v_add_f32 v60,v55,v104\n"
"v_add_f32 v60,v60,v105\n"
MF "a[52:55],v[24:27],v[84:87],a[220:223]\n"
CV "v104,v104,v105\n"
"v_add_f32 v60,v60,v106\n"
MF "a[48:51],v[24:27],v[80:83],a[220:223]\n"
"v_add_f32 v60,v60,v107\n"
CV "v105,v106,v107\n"
MF "a[68:71],v[28:31],v[80:83],a[220:223]\n"
"v_exp_f32 v108,v108\n"
MF "a[64:67],v[28:31],v[76:79],a[220:223]\n"
"v_exp_f32 v109,v109\n"
MF "a[44:47],v[24:27],v[76:79],a[220:223]\n"
"v_exp_f32 v110,v110\n"
MF "a[40:43],v[24:27],v[72:75],a[220:223]\n"
"v_exp_f32 v111,v111\n"
MF "a[60:63],v[28:31],v[72:75],a[220:223]\n"
".Lpg10b:\n"
"s_cbranch_execz .Lpg1a\n"
".Lpg10d:\n"
"s_cbranch_execz .Lpg17d\n"
"s_waitcnt lgkmcnt(0)\n"
TR "v[24:25],v4 offset:1024\n"
TR "v[26:27],v4 offset:7168\n"
MF "a[80:83],v[16:19],v[72:75],a[220:223]\n"
"s_nop 0\n"
TR "v[28:29],v5 offset:1024\n"
TR "v[30:31],v5 offset:7168\n"
MF "a[100:103],v[20:23],v[72:75],a[220:223]\n"
"v_add_f32 v61,v56,v108\n"
"v_add_f32 v61,v61,v109\n"
MF "a[104:107],v[20:23],v[76:79],a[220:223]\n"
CV "v108,v108,v109\n"
"v_add_f32 v61,v61,v110\n"
MF "a[84:87],v[16:19],v[76:79],a[220:223]\n"
"v_add_f32 v61,v61,v111\n"
CV "v109,v110,v111\n"
MF "a[88:91],v[16:19],v[80:83],a[220:223]\n"
"v_exp_f32 v112,v112\n"
MF "a[108:111],v[20:23],v[80:83],a[220:223]\n"
"v_exp_f32 v113,v113\n"
MF "a[112:115],v[20:23],v[84:87],a[220:223]\n"
"v_exp_f32 v114,v114\n"
MF "a[92:95],v[16:19],v[84:87],a[220:223]\n"
"v_exp_f32 v115,v115\n"
MF "a[96:99],v[16:19],v[88:91],a[220:223]\n"
"v_add_f32 v57,v57,v112\n"
"v_add_f32 v57,v57,v113\n"
MF "a[116:119],v[20:23],v[88:91],a[220:223]\n"
"s_waitcnt lgkmcnt(0)\n"
TR "v[16:17],v2 offset:2048\n"
TR "v[18:19],v2 offset:8192\n"
MF "a[136:139],v[24:27],v[88:91],a[220:223]\n"
"s_nop 0\n"
TR "v[20:21],v3 offset:2048\n"
TR "v[22:23],v3 offset:8192\n"
MF "a[156:159],v[28:31],v[88:91],a[220:223]\n"
CV "v94,v112,v113\n"
"v_add_f32 v57,v57,v114\n"
MF "a[152:155],v[28:31],v[84:87],a[220:223]\n"
"v_add_f32 v57,v57,v115\n"
CV "v95,v114,v115\n"
MF "a[132:135],v[24:27],v[84:87],a[220:223]\n"
"v_exp_f32 v116,v116\n"
MF "a[128:131],v[24:27],v[80:83],a[220:223]\n"
"v_exp_f32 v117,v117\n"
MF "a[148:151],v[28:31],v[80:83],a[220:223]\n"
"v_exp_f32 v118,v118\n"
MF "a[144:147],v[28:31],v[76:79],a[220:223]\n"
"v_exp_f32 v119,v119\n"
MF "a[124:127],v[24:27],v[76:79],a[220:223]\n"
"v_add_f32 v58,v58,v116\n"
"v_add_f32 v58,v58,v117\n"
MF "a[120:123],v[24:27],v[72:75],a[220:223]\n"
CV "v98,v116,v117\n"
"v_add_f32 v58,v58,v118\n"
MF "a[140:143],v[28:31],v[72:75],a[220:223]\n"
"s_waitcnt lgkmcnt(0)\n"
TR "v[24:25],v4 offset:2048\n"
TR "v[26:27],v4 offset:8192\n"
MF "a[160:163],v[16:19],v[72:75],a[220:223]\n"
"s_nop 0\n"
TR "v[28:29],v5 offset:2048\n"
TR "v[30:31],v5 offset:8192\n"
MF "a[180:183],v[20:23],v[72:75],a[220:223]\n"
"v_add_f32 v58,v58,v119\n"
CV "v99,v118,v119\n"
MF "a[184:187],v[20:23],v[76:79],a[220:223]\n"
"v_exp_f32 v120,v120\n"
MF "a[164:167],v[16:19],v[76:79],a[220:223]\n"
"v_exp_f32 v121,v121\n"
MF "a[168:171],v[16:19],v[80:83],a[220:223]\n"
"v_exp_f32 v122,v122\n"
MF "a[188:191],v[20:23],v[80:83],a[220:223]\n"
"v_exp_f32 v123,v123\n"
MF "a[192:195],v[20:23],v[84:87],a[220:223]\n"
"v_add_f32 v59,v59,v120\n"
"v_add_f32 v59,v59,v121\n"
MF "a[172:175],v[16:19],v[84:87],a[220:223]\n"
"s_waitcnt vmcnt(6) lgkmcnt(0)\n"
CV "v102,v120,v121\n"
"v_add_f32 v59,v59,v122\n"
MF "a[176:179],v[16:19],v[88:91],a[220:223]\n"
"s_barrier\n"
MF "a[196:199],v[20:23],v[88:91],a[220:223]\n"
"s_add_u32 s73,s73,12288\n"
"ds_read_b128 v[16:19],v0\n"
"ds_read_b128 v[20:23],v1\n"
MF "a[216:219],v[24:27],v[88:91],a[220:223]\n"
"s_cmp_eq_u32 s73,s19\n"
"s_cselect_b32 s73,s18,s73\n"
"s_add_u32 m0,s65,49152\n"
MF "a[236:239],v[28:31],v[88:91],a[220:223]\n"
LD "v8,s[36:39],s73 offen nt lds\n"
MF "a[232:235],v[28:31],v[84:87],a[220:223]\n"
"s_add_u32 m0,s65,50048\n"
"v_add_f32 v59,v59,v123\n"
CV "v103,v122,v123\n"
MF "a[212:215],v[24:27],v[84:87],a[220:223]\n"
LD "v8,s[36:39],s73 offen offset:128 nt lds\n"
MF "a[208:211],v[24:27],v[80:83],a[220:223]\n"
"s_add_u32 m0,s65,50944\n"
MF "a[228:231],v[28:31],v[80:83],a[220:223]\n"
LD "v8,s[36:39],s73 offen offset:256 nt lds\n"
MF "a[224:227],v[28:31],v[76:79],a[220:223]\n"
MF "a[204:207],v[24:27],v[76:79],a[220:223]\n"
MF "a[200:203],v[24:27],v[72:75],a[220:223]\n"
MF "a[220:223],v[28:31],v[72:75],a[220:223]\n"
"s_waitcnt lgkmcnt(0)\n"
".Laloopx:\n"
MF "v[72:75],v[16:19],v[136:139],v[32:35]\n"
"s_nop 0\n"
"ds_read_b128 v[24:27],v0 offset:1024\n"
"ds_read_b128 v[28:31],v1 offset:1024\n"
MF "v[72:75],v[20:23],v[156:159],v[72:75]\n"
MF "v[76:79],v[20:23],v[160:163],v[36:39]\n"
MF "v[76:79],v[16:19],v[140:143],v[76:79]\n"
MF "v[80:83],v[16:19],v[144:147],v[40:43]\n"
MF "v[80:83],v[20:23],v[164:167],v[80:83]\n"
MF "v[84:87],v[20:23],v[168:171],v[44:47]\n"
MF "v[84:87],v[16:19],v[148:151],v[84:87]\n"
"s_add_u32 m0,s65,49152\n"
MF "v[88:91],v[16:19],v[152:155],v[48:51]\n"
LD "v8,s[36:39],s73 offen offset:3072 nt lds\n"
MF "v[88:91],v[20:23],v[172:175],v[88:91]\n"
"s_waitcnt lgkmcnt(0)\n"
"ds_read_b128 v[16:19],v0 offset:2048\n"
"ds_read_b128 v[20:23],v1 offset:2048\n"
MF "v[88:91],v[24:27],v[192:195],v[88:91]\n"
"v_exp_f32 v124,v124\n"
MF "v[88:91],v[28:31],v[212:215],v[88:91]\n"
"s_add_u32 m0,s65,50048\n"
"v_exp_f32 v125,v125\n"
MF "v[84:87],v[28:31],v[208:211],v[84:87]\n"
LD "v8,s[36:39],s73 offen offset:3200 nt lds\n"
MF "v[84:87],v[24:27],v[188:191],v[84:87]\n"
"v_exp_f32 v126,v126\n"
MF "v[80:83],v[24:27],v[184:187],v[80:83]\n"
"v_exp_f32 v127,v127\n"
MF "v[80:83],v[28:31],v[204:207],v[80:83]\n"
"s_add_u32 m0,s65,50944\n"
"v_add_f32 v60,v60,v124\n"
"v_add_f32 v60,v60,v125\n"
MF "v[76:79],v[28:31],v[200:203],v[76:79]\n"
LD "v8,s[36:39],s73 offen offset:3328 nt lds\n"
MF "v[76:79],v[24:27],v[180:183],v[76:79]\n"
CV "v106,v124,v125\n"
"v_add_f32 v60,v60,v126\n"
MF "v[72:75],v[24:27],v[176:179],v[72:75]\n"
"v_add_f32 v60,v60,v127\n"
CV "v107,v126,v127\n"
MF "v[72:75],v[28:31],v[196:199],v[72:75]\n"
"s_waitcnt lgkmcnt(0)\n"
"ds_read_b128 v[24:27],v0 offset:6144\n"
"ds_read_b128 v[28:31],v1 offset:6144\n"
MF "v[72:75],v[16:19],v[216:219],v[72:75]\n"
"v_exp_f32 v128,v128\n"
MF "v[72:75],v[20:23],v[236:239],v[72:75]\n"
"v_exp_f32 v129,v129\n"
MF "v[76:79],v[20:23],v[240:243],v[76:79]\n"
"v_exp_f32 v130,v130\n"
MF "v[76:79],v[16:19],v[220:223],v[76:79]\n"
"v_exp_f32 v131,v131\n"
MF "v[80:83],v[16:19],v[224:227],v[80:83]\n"
"v_add_f32 v61,v61,v128\n"
"v_add_f32 v61,v61,v129\n"
MF "v[80:83],v[20:23],v[244:247],v[80:83]\n"
CV "v110,v128,v129\n"
"v_add_f32 v61,v61,v130\n"
MF "v[84:87],v[20:23],v[248:251],v[84:87]\n"
"v_add_f32 v61,v61,v131\n"
CV "v111,v130,v131\n"
MF "v[84:87],v[16:19],v[228:231],v[84:87]\n"
"v_max3_f32 v10,v57,v58,v59\n"
"v_max3_f32 v10,v10,v60,v61\n"
MF "v[88:91],v[16:19],v[232:235],v[88:91]\n"
"v_cmp_ngt_f32 vcc,s77,v10\n"
MF "v[88:91],v[20:23],v[252:255],v[88:91]\n"
"s_waitcnt lgkmcnt(0)\n"
"s_cbranch_vccnz .Lstub_6\n"
".Lback_6:\n"
MF "v[112:115],v[24:27],v[136:139],v[32:35]\n"
"s_add_u32 s74,s74,12288\n"
"ds_read_b128 v[16:19],v0 offset:7168\n"
"ds_read_b128 v[20:23],v1 offset:7168\n"
MF "v[112:115],v[28:31],v[156:159],v[112:115]\n"
"s_cmp_eq_u32 s74,s20\n"
"s_cselect_b32 s74,s70,s74\n"
MF "v[116:119],v[28:31],v[160:163],v[36:39]\n"
"v_exp_f32 v72,v72\n"
MF "v[116:119],v[24:27],v[140:143],v[116:119]\n"
"v_exp_f32 v73,v73\n"
MF "v[120:123],v[24:27],v[144:147],v[40:43]\n"
"v_exp_f32 v74,v74\n"
MF "v[120:123],v[28:31],v[164:167],v[120:123]\n"
"v_exp_f32 v75,v75\n"
MF "v[124:127],v[28:31],v[168:171],v[44:47]\n"
"v_add_f32 v52,v57,v72\n"
"v_add_f32 v52,v52,v73\n"
MF "v[124:127],v[24:27],v[148:151],v[124:127]\n"
CV "v72,v72,v73\n"
"v_add_f32 v52,v52,v74\n"
MF "v[128:131],v[24:27],v[152:155],v[48:51]\n"
"v_add_f32 v52,v52,v75\n"
CV "v73,v74,v75\n"
MF "v[128:131],v[28:31],v[172:175],v[128:131]\n"
"s_waitcnt lgkmcnt(0)\n"
"ds_read_b128 v[24:27],v0 offset:8192\n"
"ds_read_b128 v[28:31],v1 offset:8192\n"
MF "v[128:131],v[16:19],v[192:195],v[128:131]\n"
"v_exp_f32 v76,v76\n"
MF "v[128:131],v[20:23],v[212:215],v[128:131]\n"
"v_exp_f32 v77,v77\n"
MF "v[124:127],v[20:23],v[208:211],v[124:127]\n"
"v_exp_f32 v78,v78\n"
MF "v[124:127],v[16:19],v[188:191],v[124:127]\n"
"v_exp_f32 v79,v79\n"
MF "v[120:123],v[16:19],v[184:187],v[120:123]\n"
"v_add_f32 v53,v58,v76\n"
"v_add_f32 v53,v53,v77\n"
MF "v[120:123],v[20:23],v[204:207],v[120:123]\n"
CV "v76,v76,v77\n"
"v_add_f32 v53,v53,v78\n"
MF "v[116:119],v[20:23],v[200:203],v[116:119]\n"
"s_waitcnt vmcnt(12) lgkmcnt(0)\n"
"v_add_f32 v53,v53,v79\n"
CV "v77,v78,v79\n"
MF "v[116:119],v[16:19],v[180:183],v[116:119]\n"
"s_barrier\n"
MF "v[112:115],v[16:19],v[176:179],v[112:115]\n"
"v_exp_f32 v80,v80\n"
MF "v[112:115],v[20:23],v[196:199],v[112:115]\n"
"s_nop 0\n"
TR "v[16:17],v2 offset:24576\n"
TR "v[18:19],v2 offset:30720\n"
MF "v[112:115],v[24:27],v[216:219],v[112:115]\n"
"s_add_u32 s72,s72,6144\n"
TR "v[20:21],v3 offset:24576\n"
TR "v[22:23],v3 offset:30720\n"
MF "v[112:115],v[28:31],v[236:239],v[112:115]\n"
"s_cmp_eq_u32 s72,s17\n"
"s_cselect_b32 s72,s16,s72\n"
"s_add_u32 m0,s64,0\n"
MF "v[116:119],v[28:31],v[240:243],v[116:119]\n"
LD "v7,s[32:35],s72 offen nt lds\n"
MF "v[116:119],v[24:27],v[220:223],v[116:119]\n"
"s_add_u32 m0,s64,896\n"
"v_exp_f32 v81,v81\n"
MF "v[120:123],v[24:27],v[224:227],v[120:123]\n"
LD "v7,s[32:35],s72 offen offset:128 nt lds\n"
MF "v[120:123],v[28:31],v[244:247],v[120:123]\n"
"v_exp_f32 v82,v82\n"
MF "v[124:127],v[28:31],v[248:251],v[124:127]\n"
"s_add_u32 m0,s64,1792\n"
"v_exp_f32 v83,v83\n"
MF "v[124:127],v[24:27],v[228:231],v[124:127]\n"
LD "v7,s[32:35],s72 offen offset:256 nt lds\n"
MF "v[128:131],v[24:27],v[232:235],v[128:131]\n"
"s_add_u32 s72,s72,6144\n"
"v_add_f32 v54,v59,v80\n"
"v_add_f32 v54,v54,v81\n"
MF "v[128:131],v[28:31],v[252:255],v[128:131]\n"
"s_waitcnt lgkmcnt(0)\n"
TR "v[24:25],v4 offset:24576\n"
TR "v[26:27],v4 offset:30720\n"
MF "a[0:3],v[16:19],v[92:95],a[0:3]\n"
"s_add_u32 m0,s64,6144\n"
TR "v[28:29],v5 offset:24576\n"
TR "v[30:31],v5 offset:30720\n"
MF "a[20:23],v[20:23],v[92:95],a[20:23]\n"
LD "v7,s[32:35],s72 offen nt lds\n"
MF "a[24:27],v[20:23],v[96:99],a[24:27]\n"
CV "v80,v80,v81\n"
"v_add_f32 v54,v54,v82\n"
MF "a[4:7],v[16:19],v[96:99],a[4:7]\n"
"s_add_u32 m0,s64,7040\n"
"v_add_f32 v54,v54,v83\n"
CV "v81,v82,v83\n"
MF "a[8:11],v[16:19],v[100:103],a[8:11]\n"
LD "v7,s[32:35],s72 offen offset:128 nt lds\n"
MF "a[28:31],v[20:23],v[100:103],a[28:31]\n"
"v_exp_f32 v84,v84\n"
MF "a[32:35],v[20:23],v[104:107],a[32:35]\n"
"s_add_u32 m0,s64,7936\n"
"v_exp_f32 v85,v85\n"
MF "a[12:15],v[16:19],v[104:107],a[12:15]\n"
LD "v7,s[32:35],s72 offen offset:256 nt lds\n"
MF "a[16:19],v[16:19],v[108:111],a[16:19]\n"
"v_exp_f32 v86,v86\n"
MF "a[36:39],v[20:23],v[108:111],a[36:39]\n"
"s_waitcnt lgkmcnt(0)\n"
TR "v[16:17],v2 offset:25600\n"
TR "v[18:19],v2 offset:31744\n"
MF "a[56:59],v[24:27],v[108:111],a[56:59]\n"
"s_nop 0\n"
TR "v[20:21],v3 offset:25600\n"
TR "v[22:23],v3 offset:31744\n"
MF "a[76:79],v[28:31],v[108:111],a[76:79]\n"
"v_exp_f32 v87,v87\n"
MF "a[72:75],v[28:31],v[104:107],a[72:75]\n"
"v_add_f32 v55,v60,v84\n"
"v_add_f32 v55,v55,v85\n"
MF "a[52:55],v[24:27],v[104:107],a[52:55]\n"
CV "v84,v84,v85\n"
"v_add_f32 v55,v55,v86\n"
MF "a[48:51],v[24:27],v[100:103],a[48:51]\n"
"v_add_f32 v55,v55,v87\n"
CV "v85,v86,v87\n"
MF "a[68:71],v[28:31],v[100:103],a[68:71]\n"
"v_exp_f32 v88,v88\n"
MF "a[64:67],v[28:31],v[96:99],a[64:67]\n"
"v_exp_f32 v89,v89\n"
MF "a[44:47],v[24:27],v[96:99],a[44:47]\n"
"v_exp_f32 v90,v90\n"
MF "a[40:43],v[24:27],v[92:95],a[40:43]\n"
"v_exp_f32 v91,v91\n"
MF "a[60:63],v[28:31],v[92:95],a[60:63]\n"
"s_waitcnt lgkmcnt(0)\n"
TR "v[24:25],v4 offset:25600\n"
TR "v[26:27],v4 offset:31744\n"
MF "a[80:83],v[16:19],v[92:95],a[80:83]\n"
"s_nop 0\n"
TR "v[28:29],v5 offset:25600\n"
TR "v[30:31],v5 offset:31744\n"
MF "a[100:103],v[20:23],v[92:95],a[100:103]\n"
"v_add_f32 v56,v61,v88\n"
"v_add_f32 v56,v56,v89\n"
MF "a[104:107],v[20:23],v[96:99],a[104:107]\n"
CV "v88,v88,v89\n"
"v_add_f32 v56,v56,v90\n"
MF "a[84:87],v[16:19],v[96:99],a[84:87]\n"
"v_add_f32 v56,v56,v91\n"
CV "v89,v90,v91\n"
MF "a[88:91],v[16:19],v[100:103],a[88:91]\n"
"v_exp_f32 v112,v112\n"
MF "a[108:111],v[20:23],v[100:103],a[108:111]\n"
"v_exp_f32 v113,v113\n"
MF "a[112:115],v[20:23],v[104:107],a[112:115]\n"
"v_exp_f32 v114,v114\n"
MF "a[92:95],v[16:19],v[104:107],a[92:95]\n"
"v_exp_f32 v115,v115\n"
MF "a[96:99],v[16:19],v[108:111],a[96:99]\n"
"v_add_f32 v52,v52,v112\n"
"v_add_f32 v52,v52,v113\n"
MF "a[116:119],v[20:23],v[108:111],a[116:119]\n"
"s_waitcnt lgkmcnt(0)\n"
TR "v[16:17],v2 offset:26624\n"
TR "v[18:19],v2 offset:32768\n"
MF "a[136:139],v[24:27],v[108:111],a[136:139]\n"
"s_nop 0\n"
TR "v[20:21],v3 offset:26624\n"
TR "v[22:23],v3 offset:32768\n"
MF "a[156:159],v[28:31],v[108:111],a[156:159]\n"
CV "v74,v112,v113\n"
"v_add_f32 v52,v52,v114\n"
MF "a[152:155],v[28:31],v[104:107],a[152:155]\n"
"v_add_f32 v52,v52,v115\n"
CV "v75,v114,v115\n"
MF "a[132:135],v[24:27],v[104:107],a[132:135]\n"
"v_exp_f32 v116,v116\n"
MF "a[128:131],v[24:27],v[100:103],a[128:131]\n"
"v_exp_f32 v117,v117\n"
MF "a[148:151],v[28:31],v[100:103],a[148:151]\n"
"v_exp_f32 v118,v118\n"
MF "a[144:147],v[28:31],v[96:99],a[144:147]\n"
"v_exp_f32 v119,v119\n"
MF "a[124:127],v[24:27],v[96:99],a[124:127]\n"
"v_add_f32 v53,v53,v116\n"
"v_add_f32 v53,v53,v117\n"
MF "a[120:123],v[24:27],v[92:95],a[120:123]\n"
CV "v78,v116,v117\n"
"v_add_f32 v53,v53,v118\n"
MF "a[140:143],v[28:31],v[92:95],a[140:143]\n"
"s_waitcnt lgkmcnt(0)\n"
TR "v[24:25],v4 offset:26624\n"
TR "v[26:27],v4 offset:32768\n"
MF "a[160:163],v[16:19],v[92:95],a[160:163]\n"
"s_nop 0\n"
TR "v[28:29],v5 offset:26624\n"
TR "v[30:31],v5 offset:32768\n"
MF "a[180:183],v[20:23],v[92:95],a[180:183]\n"
"v_add_f32 v53,v53,v119\n"
CV "v79,v118,v119\n"
MF "a[184:187],v[20:23],v[96:99],a[184:187]\n"
"v_exp_f32 v120,v120\n"
MF "a[164:167],v[16:19],v[96:99],a[164:167]\n"
"v_exp_f32 v121,v121\n"
MF "a[168:171],v[16:19],v[100:103],a[168:171]\n"
"v_exp_f32 v122,v122\n"
MF "a[188:191],v[20:23],v[100:103],a[188:191]\n"
"v_exp_f32 v123,v123\n"
MF "a[192:195],v[20:23],v[104:107],a[192:195]\n"
"v_add_f32 v54,v54,v120\n"
"v_add_f32 v54,v54,v121\n"
MF "a[172:175],v[16:19],v[104:107],a[172:175]\n"
"s_waitcnt vmcnt(12) lgkmcnt(0)\n"
CV "v82,v120,v121\n"
"v_add_f32 v54,v54,v122\n"
MF "a[176:179],v[16:19],v[108:111],a[176:179]\n"
"s_barrier\n"
MF "a[196:199],v[20:23],v[108:111],a[196:199]\n"
"s_add_u32 s73,s73,12288\n"
"ds_read_b128 v[16:19],v0 offset:24576\n"
"ds_read_b128 v[20:23],v1 offset:24576\n"
MF "a[216:219],v[24:27],v[108:111],a[216:219]\n"
"s_cmp_eq_u32 s73,s19\n"
"s_cselect_b32 s73,s18,s73\n"
"s_add_u32 m0,s65,73728\n"
MF "a[236:239],v[28:31],v[108:111],a[236:239]\n"
LD "v8,s[36:39],s73 offen nt lds\n"
MF "a[232:235],v[28:31],v[104:107],a[232:235]\n"
"s_add_u32 m0,s65,74624\n"
"v_add_f32 v54,v54,v123\n"
CV "v83,v122,v123\n"
MF "a[212:215],v[24:27],v[104:107],a[212:215]\n"
LD "v8,s[36:39],s73 offen offset:128 nt lds\n"
MF "a[208:211],v[24:27],v[100:103],a[208:211]\n"
"s_add_u32 m0,s65,75520\n"
MF "a[228:231],v[28:31],v[100:103],a[228:231]\n"
LD "v8,s[36:39],s73 offen offset:256 nt lds\n"
MF "a[224:227],v[28:31],v[96:99],a[224:227]\n"
MF "a[204:207],v[24:27],v[96:99],a[204:207]\n"
MF "a[200:203],v[24:27],v[92:95],a[200:203]\n"
MF "a[220:223],v[28:31],v[92:95],a[220:223]\n"
"s_waitcnt lgkmcnt(0)\n"
MF "v[92:95],v[16:19],v[136:139],v[32:35]\n"
"s_add_u32 m0,s65,73728\n"
"ds_read_b128 v[24:27],v0 offset:25600\n"
"ds_read_b128 v[28:31],v1 offset:25600\n"
MF "v[92:95],v[20:23],v[156:159],v[92:95]\n"
LD "v8,s[36:39],s73 offen offset:3072 nt lds\n"
MF "v[96:99],v[20:23],v[160:163],v[36:39]\n"
"v_exp_f32 v124,v124\n"
MF "v[96:99],v[16:19],v[140:143],v[96:99]\n"
"v_exp_f32 v125,v125\n"
MF "v[100:103],v[16:19],v[144:147],v[40:43]\n"
"s_add_u32 m0,s65,74624\n"
"v_exp_f32 v126,v126\n"
MF "v[100:103],v[20:23],v[164:167],v[100:103]\n"
LD "v8,s[36:39],s73 offen offset:3200 nt lds\n"
MF "v[104:107],v[20:23],v[168:171],v[44:47]\n"
"v_exp_f32 v127,v127\n"
MF "v[104:107],v[16:19],v[148:151],v[104:107]\n"
"s_add_u32 m0,s65,75520\n"
"v_add_f32 v55,v55,v124\n"
"v_add_f32 v55,v55,v125\n"
MF "v[108:111],v[16:19],v[152:155],v[48:51]\n"
LD "v8,s[36:39],s73 offen offset:3328 nt lds\n"
MF "v[108:111],v[20:23],v[172:175],v[108:111]\n"
"s_waitcnt lgkmcnt(0)\n"
"ds_read_b128 v[16:19],v0 offset:26624\n"
"ds_read_b128 v[20:23],v1 offset:26624\n"
MF "v[108:111],v[24:27],v[192:195],v[108:111]\n"
CV "v86,v124,v125\n"
"v_add_f32 v55,v55,v126\n"
MF "v[108:111],v[28:31],v[212:215],v[108:111]\n"
"v_add_f32 v55,v55,v127\n"
CV "v87,v126,v127\n"
MF "v[104:107],v[28:31],v[208:211],v[104:107]\n"
"v_exp_f32 v128,v128\n"
MF "v[104:107],v[24:27],v[188:191],v[104:107]\n"
"v_exp_f32 v129,v129\n"
MF "v[100:103],v[24:27],v[184:187],v[100:103]\n"
"v_exp_f32 v130,v130\n"
MF "v[100:103],v[28:31],v[204:207],v[100:103]\n"
"v_exp_f32 v131,v131\n"
MF "v[96:99],v[28:31],v[200:203],v[96:99]\n"
"v_add_f32 v56,v56,v128\n"
"v_add_f32 v56,v56,v129\n"
MF "v[96:99],v[24:27],v[180:183],v[96:99]\n"
CV "v90,v128,v129\n"
"v_add_f32 v56,v56,v130\n"
MF "v[92:95],v[24:27],v[176:179],v[92:95]\n"
"v_add_f32 v56,v56,v131\n"
CV "v91,v130,v131\n"
MF "v[92:95],v[28:31],v[196:199],v[92:95]\n"
"s_waitcnt lgkmcnt(0)\n"
"ds_read_b128 v[24:27],v0 offset:30720\n"
"ds_read_b128 v[28:31],v1 offset:30720\n"
MF "v[92:95],v[16:19],v[216:219],v[92:95]\n"
MF "v[92:95],v[20:23],v[236:239],v[92:95]\n"
MF "v[96:99],v[20:23],v[240:243],v[96:99]\n"
MF "v[96:99],v[16:19],v[220:223],v[96:99]\n"
MF "v[100:103],v[16:19],v[224:227],v[100:103]\n"
MF "v[100:103],v[20:23],v[244:247],v[100:103]\n"
MF "v[104:107],v[20:23],v[248:251],v[104:107]\n"
MF "v[104:107],v[16:19],v[228:231],v[104:107]\n"
"v_max3_f32 v10,v52,v53,v54\n"
"v_max3_f32 v10,v10,v55,v56\n"
MF "v[108:111],v[16:19],v[232:235],v[108:111]\n"
"v_cmp_ngt_f32 vcc,s77,v10\n"
MF "v[108:111],v[20:23],v[252:255],v[108:111]\n"
"s_waitcnt lgkmcnt(0)\n"
"s_cbranch_vccnz .Lstub_7\n"
".Lback_7:\n"
MF "v[112:115],v[24:27],v[136:139],v[32:35]\n"
"s_add_u32 s74,s74,12288\n"
"ds_read_b128 v[16:19],v0 offset:31744\n"
"ds_read_b128 v[20:23],v1 offset:31744\n"
MF "v[112:115],v[28:31],v[156:159],v[112:115]\n"
"s_cmp_eq_u32 s74,s20\n"
"s_cselect_b32 s74,s70,s74\n"
MF "v[116:119],v[28:31],v[160:163],v[36:39]\n"
"v_exp_f32 v92,v92\n"
MF "v[116:119],v[24:27],v[140:143],v[116:119]\n"
"v_exp_f32 v93,v93\n"
MF "v[120:123],v[24:27],v[144:147],v[40:43]\n"
"v_exp_f32 v94,v94\n"
MF "v[120:123],v[28:31],v[164:167],v[120:123]\n"
"v_exp_f32 v95,v95\n"
MF "v[124:127],v[28:31],v[168:171],v[44:47]\n"
"v_add_f32 v57,v52,v92\n"
"v_add_f32 v57,v57,v93\n"
MF "v[124:127],v[24:27],v[148:151],v[124:127]\n"
CV "v92,v92,v93\n"
"v_add_f32 v57,v57,v94\n"
MF "v[128:131],v[24:27],v[152:155],v[48:51]\n"
"v_add_f32 v57,v57,v95\n"
CV "v93,v94,v95\n"
MF "v[128:131],v[28:31],v[172:175],v[128:131]\n"
"s_waitcnt lgkmcnt(0)\n"
"ds_read_b128 v[24:27],v0 offset:32768\n"
"ds_read_b128 v[28:31],v1 offset:32768\n"
MF "v[128:131],v[16:19],v[192:195],v[128:131]\n"
"s_sub_u32 s61,s61,1\n"
"v_exp_f32 v96,v96\n"
MF "v[128:131],v[20:23],v[212:215],v[128:131]\n"
"v_exp_f32 v97,v97\n"
MF "v[124:127],v[20:23],v[208:211],v[124:127]\n"
"v_exp_f32 v98,v98\n"
MF "v[124:127],v[16:19],v[188:191],v[124:127]\n"
"v_exp_f32 v99,v99\n"
MF "v[120:123],v[16:19],v[184:187],v[120:123]\n"
"v_add_f32 v58,v53,v96\n"
"v_add_f32 v58,v58,v97\n"
MF "v[120:123],v[20:23],v[204:207],v[120:123]\n"
CV "v96,v96,v97\n"
"v_add_f32 v58,v58,v98\n"
MF "v[116:119],v[20:23],v[200:203],v[116:119]\n"
"s_waitcnt vmcnt(12) lgkmcnt(0)\n"
"v_add_f32 v58,v58,v99\n"
CV "v97,v98,v99\n"
MF "v[116:119],v[16:19],v[180:183],v[116:119]\n"
"s_barrier\n"
MF "v[112:115],v[16:19],v[176:179],v[112:115]\n"
"v_exp_f32 v100,v100\n"
MF "v[112:115],v[20:23],v[196:199],v[112:115]\n"
"s_nop 0\n"
TR "v[16:17],v2\n"
TR "v[18:19],v2 offset:6144\n"
MF "v[112:115],v[24:27],v[216:219],v[112:115]\n"
"s_add_u32 s72,s72,6144\n"
TR "v[20:21],v3\n"
TR "v[22:23],v3 offset:6144\n"
MF "v[112:115],v[28:31],v[236:239],v[112:115]\n"
"s_cmp_eq_u32 s72,s17\n"
"s_cselect_b32 s72,s16,s72\n"
"s_add_u32 m0,s64,24576\n"
MF "v[116:119],v[28:31],v[240:243],v[116:119]\n"
LD "v7,s[32:35],s72 offen nt lds\n"
MF "v[116:119],v[24:27],v[220:223],v[116:119]\n"
"s_add_u32 m0,s64,25472\n"
"v_exp_f32 v101,v101\n"
MF "v[120:123],v[24:27],v[224:227],v[120:123]\n"
LD "v7,s[32:35],s72 offen offset:128 nt lds\n"
MF "v[120:123],v[28:31],v[244:247],v[120:123]\n"
"v_exp_f32 v102,v102\n"
MF "v[124:127],v[28:31],v[248:251],v[124:127]\n"
"s_add_u32 m0,s64,26368\n"
"v_exp_f32 v103,v103\n"
MF "v[124:127],v[24:27],v[228:231],v[124:127]\n"
LD "v7,s[32:35],s72 offen offset:256 nt lds\n"
MF "v[128:131],v[24:27],v[232:235],v[128:131]\n"
"s_add_u32 s72,s72,6144\n"
"v_add_f32 v59,v54,v100\n"
"v_add_f32 v59,v59,v101\n"
MF "v[128:131],v[28:31],v[252:255],v[128:131]\n"
"s_waitcnt lgkmcnt(0)\n"
TR "v[24:25],v4\n"
TR "v[26:27],v4 offset:6144\n"
MF "a[0:3],v[16:19],v[72:75],a[0:3]\n"
"s_add_u32 m0,s64,30720\n"
TR "v[28:29],v5\n"
TR "v[30:31],v5 offset:6144\n"
MF "a[20:23],v[20:23],v[72:75],a[20:23]\n"
LD "v7,s[32:35],s72 offen nt lds\n"
MF "a[24:27],v[20:23],v[76:79],a[24:27]\n"
CV "v100,v100,v101\n"
"v_add_f32 v59,v59,v102\n"
MF "a[4:7],v[16:19],v[76:79],a[4:7]\n"
"s_add_u32 m0,s64,31616\n"
"v_add_f32 v59,v59,v103\n"
CV "v101,v102,v103\n"
MF "a[8:11],v[16:19],v[80:83],a[8:11]\n"
LD "v7,s[32:35],s72 offen offset:128 nt lds\n"
MF "a[28:31],v[20:23],v[80:83],a[28:31]\n"
"v_exp_f32 v104,v104\n"
MF "a[32:35],v[20:23],v[84:87],a[32:35]\n"
"s_add_u32 m0,s64,32512\n"
"v_exp_f32 v105,v105\n"
MF "a[12:15],v[16:19],v[84:87],a[12:15]\n"
LD "v7,s[32:35],s72 offen offset:256 nt lds\n"
MF "a[16:19],v[16:19],v[88:91],a[16:19]\n"
"v_exp_f32 v106,v106\n"
MF "a[36:39],v[20:23],v[88:91],a[36:39]\n"
"s_waitcnt lgkmcnt(0)\n"
TR "v[16:17],v2 offset:1024\n"
TR "v[18:19],v2 offset:7168\n"
MF "a[56:59],v[24:27],v[88:91],a[56:59]\n"
"s_nop 0\n"
TR "v[20:21],v3 offset:1024\n"
TR "v[22:23],v3 offset:7168\n"
MF "a[76:79],v[28:31],v[88:91],a[76:79]\n"
"v_exp_f32 v107,v107\n"
MF "a[72:75],v[28:31],v[84:87],a[72:75]\n"
"v_add_f32 v60,v55,v104\n"
"v_add_f32 v60,v60,v105\n"
MF "a[52:55],v[24:27],v[84:87],a[52:55]\n"
CV "v104,v104,v105\n"
"v_add_f32 v60,v60,v106\n"
MF "a[48:51],v[24:27],v[80:83],a[48:51]\n"
".Lpg11b:\n"
"s_cbranch_execz .Lpgqj_a\n"
".Lpg11d:\n"
"s_cbranch_execz .Lpgt0e_d\n"
"v_add_f32 v60,v60,v107\n"
CV "v105,v106,v107\n"
MF "a[68:71],v[28:31],v[80:83],a[68:71]\n"
"v_exp_f32 v108,v108\n"
MF "a[64:67],v[28:31],v[76:79],a[64:67]\n"
"v_exp_f32 v109,v109\n"
MF "a[44:47],v[24:27],v[76:79],a[44:47]\n"
"v_exp_f32 v110,v110\n"
MF "a[40:43],v[24:27],v[72:75],a[40:43]\n"
"v_exp_f32 v111,v111\n"
MF "a[60:63],v[28:31],v[72:75],a[60:63]\n"
"s_waitcnt lgkmcnt(0)\n"
TR "v[24:25],v4 offset:1024\n"
TR "v[26:27],v4 offset:7168\n"
MF "a[80:83],v[16:19],v[72:75],a[80:83]\n"
"s_nop 0\n"
TR "v[28:29],v5 offset:1024\n"
TR "v[30:31],v5 offset:7168\n"
MF "a[100:103],v[20:23],v[72:75],a[100:103]\n"
"v_add_f32 v61,v56,v108\n"
"v_add_f32 v61,v61,v109\n"
MF "a[104:107],v[20:23],v[76:79],a[104:107]\n"
CV "v108,v108,v109\n"
"v_add_f32 v61,v61,v110\n"
MF "a[84:87],v[16:19],v[76:79],a[84:87]\n"
"v_add_f32 v61,v61,v111\n"
CV "v109,v110,v111\n"
MF "a[88:91],v[16:19],v[80:83],a[88:91]\n"
"v_exp_f32 v112,v112\n"
MF "a[108:111],v[20:23],v[80:83],a[108:111]\n"
"v_exp_f32 v113,v113\n"
MF "a[112:115],v[20:23],v[84:87],a[112:115]\n"
"v_exp_f32 v114,v114\n"
MF "a[92:95],v[16:19],v[84:87],a[92:95]\n"
"v_exp_f32 v115,v115\n"
MF "a[96:99],v[16:19],v[88:91],a[96:99]\n"
"v_add_f32 v57,v57,v112\n"
"v_add_f32 v57,v57,v113\n"
MF "a[116:119],v[20:23],v[88:91],a[116:119]\n"
"s_waitcnt lgkmcnt(0)\n"
TR "v[16:17],v2 offset:2048\n"
TR "v[18:19],v2 offset:8192\n"
MF "a[136:139],v[24:27],v[88:91],a[136:139]\n"
"s_nop 0\n"
TR "v[20:21],v3 offset:2048\n"
TR "v[22:23],v3 offset:8192\n"
MF "a[156:159],v[28:31],v[88:91],a[156:159]\n"
CV "v94,v112,v113\n"
"v_add_f32 v57,v57,v114\n"
MF "a[152:155],v[28:31],v[84:87],a[152:155]\n"
"v_add_f32 v57,v57,v115\n"
CV "v95,v114,v115\n"
MF "a[132:135],v[24:27],v[84:87],a[132:135]\n"
"v_exp_f32 v116,v116\n"
MF "a[128:131],v[24:27],v[80:83],a[128:131]\n"
"v_exp_f32 v117,v117\n"
MF "a[148:151],v[28:31],v[80:83],a[148:151]\n"
"v_exp_f32 v118,v118\n"
MF "a[144:147],v[28:31],v[76:79],a[144:147]\n"
"v_exp_f32 v119,v119\n"
MF "a[124:127],v[24:27],v[76:79],a[124:127]\n"
"v_add_f32 v58,v58,v116\n"
"v_add_f32 v58,v58,v117\n"
MF "a[120:123],v[24:27],v[72:75],a[120:123]\n"
CV "v98,v116,v117\n"
"v_add_f32 v58,v58,v118\n"
MF "a[140:143],v[28:31],v[72:75],a[140:143]\n"
"s_waitcnt lgkmcnt(0)\n"
TR "v[24:25],v4 offset:2048\n"
TR "v[26:27],v4 offset:8192\n"
MF "a[160:163],v[16:19],v[72:75],a[160:163]\n"
"s_nop 0\n"
TR "v[28:29],v5 offset:2048\n"
TR "v[30:31],v5 offset:8192\n"
MF "a[180:183],v[20:23],v[72:75],a[180:183]\n"
"v_add_f32 v58,v58,v119\n"
CV "v99,v118,v119\n"
MF "a[184:187],v[20:23],v[76:79],a[184:187]\n"
"v_exp_f32 v120,v120\n"
MF "a[164:167],v[16:19],v[76:79],a[164:167]\n"
"v_exp_f32 v121,v121\n"
MF "a[168:171],v[16:19],v[80:83],a[168:171]\n"
"v_exp_f32 v122,v122\n"
MF "a[188:191],v[20:23],v[80:83],a[188:191]\n"
"v_exp_f32 v123,v123\n"
MF "a[192:195],v[20:23],v[84:87],a[192:195]\n"
"v_add_f32 v59,v59,v120\n"
"v_add_f32 v59,v59,v121\n"
MF "a[172:175],v[16:19],v[84:87],a[172:175]\n"
"s_waitcnt vmcnt(12) lgkmcnt(0)\n"
CV "v102,v120,v121\n"
"v_add_f32 v59,v59,v122\n"
MF "a[176:179],v[16:19],v[88:91],a[176:179]\n"
"s_barrier\n"
MF "a[196:199],v[20:23],v[88:91],a[196:199]\n"
"s_add_u32 s73,s73,12288\n"
"ds_read_b128 v[16:19],v0\n"
"ds_read_b128 v[20:23],v1\n"
MF "a[216:219],v[24:27],v[88:91],a[216:219]\n"
"s_cmp_eq_u32 s73,s19\n"
"s_cselect_b32 s73,s18,s73\n"
"s_add_u32 m0,s65,49152\n"
MF "a[236:239],v[28:31],v[88:91],a[236:239]\n"
LD "v8,s[36:39],s73 offen nt lds\n"
MF "a[232:235],v[28:31],v[84:87],a[232:235]\n"
"s_add_u32 m0,s65,50048\n"
"v_add_f32 v59,v59,v123\n"
CV "v103,v122,v123\n"
MF "a[212:215],v[24:27],v[84:87],a[212:215]\n"
LD "v8,s[36:39],s73 offen offset:128 nt lds\n"
MF "a[208:211],v[24:27],v[80:83],a[208:211]\n"
"s_add_u32 m0,s65,50944\n"
MF "a[228:231],v[28:31],v[80:83],a[228:231]\n"
LD "v8,s[36:39],s73 offen offset:256 nt lds\n"
MF "a[224:227],v[28:31],v[76:79],a[224:227]\n"
MF "a[204:207],v[24:27],v[76:79],a[204:207]\n"
"s_waitcnt lgkmcnt(0)\n"
MF "a[200:203],v[24:27],v[72:75],a[200:203]\n"
"s_cmp_lg_u32 s61,1\n"
MF "a[220:223],v[28:31],v[72:75],a[220:223]\n"
"s_cbranch_scc1 .Laloopx\n"
"s_cmp_eq_u32 s57,0\n"
"s_cbranch_scc0 .Lep\n"
".Lepx:\n"
MF "v[72:75],v[16:19],v[136:139],v[32:35]\n"
"s_add_u32 m0,s65,49152\n"
"ds_read_b128 v[24:27],v0 offset:1024\n"
"ds_read_b128 v[28:31],v1 offset:1024\n"
MF "v[72:75],v[20:23],v[156:159],v[72:75]\n"
LD "v8,s[36:39],s73 offen offset:3072 nt lds\n"
MF "v[76:79],v[20:23],v[160:163],v[36:39]\n"
"v_exp_f32 v124,v124\n"
MF "v[76:79],v[16:19],v[140:143],v[76:79]\n"
"v_exp_f32 v125,v125\n"
MF "v[80:83],v[16:19],v[144:147],v[40:43]\n"
"s_add_u32 m0,s65,50048\n"
"v_exp_f32 v126,v126\n"
MF "v[80:83],v[20:23],v[164:167],v[80:83]\n"
LD "v8,s[36:39],s73 offen offset:3200 nt lds\n"
MF "v[84:87],v[20:23],v[168:171],v[44:47]\n"
"v_exp_f32 v127,v127\n"
MF "v[84:87],v[16:19],v[148:151],v[84:87]\n"
"s_add_u32 m0,s65,50944\n"
"v_add_f32 v60,v60,v124\n"
"v_add_f32 v60,v60,v125\n"
MF "v[88:91],v[16:19],v[152:155],v[48:51]\n"
LD "v8,s[36:39],s73 offen offset:3328 nt lds\n"
MF "v[88:91],v[20:23],v[172:175],v[88:91]\n"
"s_waitcnt lgkmcnt(0)\n"
"ds_read_b128 v[16:19],v0 offset:2048\n"
"ds_read_b128 v[20:23],v1 offset:2048\n"
MF "v[88:91],v[24:27],v[192:195],v[88:91]\n"
CV "v106,v124,v125\n"
"v_add_f32 v60,v60,v126\n"
MF "v[88:91],v[28:31],v[212:215],v[88:91]\n"
"v_add_f32 v60,v60,v127\n"
CV "v107,v126,v127\n"
MF "v[84:87],v[28:31],v[208:211],v[84:87]\n"
"v_exp_f32 v128,v128\n"
MF "v[84:87],v[24:27],v[188:191],v[84:87]\n"
"v_exp_f32 v129,v129\n"
MF "v[80:83],v[24:27],v[184:187],v[80:83]\n"
"v_exp_f32 v130,v130\n"
MF "v[80:83],v[28:31],v[204:207],v[80:83]\n"
"v_exp_f32 v131,v131\n"
MF "v[76:79],v[28:31],v[200:203],v[76:79]\n"
"v_add_f32 v61,v61,v128\n"
"v_add_f32 v61,v61,v129\n"
MF "v[76:79],v[24:27],v[180:183],v[76:79]\n"
CV "v110,v128,v129\n"
"v_add_f32 v61,v61,v130\n"
MF "v[72:75],v[24:27],v[176:179],v[72:75]\n"
"v_add_f32 v61,v61,v131\n"
CV "v111,v130,v131\n"
MF "v[72:75],v[28:31],v[196:199],v[72:75]\n"
"s_waitcnt lgkmcnt(0)\n"
"ds_read_b128 v[24:27],v0 offset:6144\n"
"ds_read_b128 v[28:31],v1 offset:6144\n"
MF "v[72:75],v[16:19],v[216:219],v[72:75]\n"
MF "v[72:75],v[20:23],v[236:239],v[72:75]\n"
MF "v[76:79],v[20:23],v[240:243],v[76:79]\n"
MF "v[76:79],v[16:19],v[220:223],v[76:79]\n"
MF "v[80:83],v[16:19],v[224:227],v[80:83]\n"
MF "v[80:83],v[20:23],v[244:247],v[80:83]\n"
MF "v[84:87],v[20:23],v[248:251],v[84:87]\n"
MF "v[84:87],v[16:19],v[228:231],v[84:87]\n"
"v_max3_f32 v10,v57,v58,v59\n"
"v_max3_f32 v10,v10,v60,v61\n"
MF "v[88:91],v[16:19],v[232:235],v[88:91]\n"
"v_cmp_le_f32 vcc,0x47800000,v10\n"
MF "v[88:91],v[20:23],v[252:255],v[88:91]\n"
"s_waitcnt lgkmcnt(0)\n"
"s_cbranch_vccnz .Lstub_8\n"
".Lback_8:\n"
MF "v[112:115],v[24:27],v[136:139],v[32:35]\n"
"s_add_u32 s74,s74,12288\n"
"ds_read_b128 v[16:19],v0 offset:7168\n"
"ds_read_b128 v[20:23],v1 offset:7168\n"
MF "v[112:115],v[28:31],v[156:159],v[112:115]\n"
"s_cmp_eq_u32 s74,s20\n"
"s_cselect_b32 s74,s70,s74\n"
MF "v[116:119],v[28:31],v[160:163],v[36:39]\n"
"v_exp_f32 v72,v72\n"
MF "v[116:119],v[24:27],v[140:143],v[116:119]\n"
"v_exp_f32 v73,v73\n"
MF "v[120:123],v[24:27],v[144:147],v[40:43]\n"
"v_exp_f32 v74,v74\n"
MF "v[120:123],v[28:31],v[164:167],v[120:123]\n"
"v_exp_f32 v75,v75\n"
MF "v[124:127],v[28:31],v[168:171],v[44:47]\n"
"v_add_f32 v52,v57,v72\n"
"v_add_f32 v52,v52,v73\n"
MF "v[124:127],v[24:27],v[148:151],v[124:127]\n"
CV "v72,v72,v73\n"
"v_add_f32 v52,v52,v74\n"
MF "v[128:131],v[24:27],v[152:155],v[48:51]\n"
"v_add_f32 v52,v52,v75\n"
CV "v73,v74,v75\n"
MF "v[128:131],v[28:31],v[172:175],v[128:131]\n"
"s_waitcnt lgkmcnt(0)\n"
"ds_read_b128 v[24:27],v0 offset:8192\n"
"ds_read_b128 v[28:31],v1 offset:8192\n"
MF "v[128:131],v[16:19],v[192:195],v[128:131]\n"
"v_exp_f32 v76,v76\n"
MF "v[128:131],v[20:23],v[212:215],v[128:131]\n"
"v_exp_f32 v77,v77\n"
MF "v[124:127],v[20:23],v[208:211],v[124:127]\n"
"v_exp_f32 v78,v78\n"
MF "v[124:127],v[16:19],v[188:191],v[124:127]\n"
"v_exp_f32 v79,v79\n"
MF "v[120:123],v[16:19],v[184:187],v[120:123]\n"
"v_add_f32 v53,v58,v76\n"
"v_add_f32 v53,v53,v77\n"
MF "v[120:123],v[20:23],v[204:207],v[120:123]\n"
CV "v76,v76,v77\n"
"v_add_f32 v53,v53,v78\n"
MF "v[116:119],v[20:23],v[200:203],v[116:119]\n"
"s_waitcnt vmcnt(12) lgkmcnt(0)\n"
"v_add_f32 v53,v53,v79\n"
CV "v77,v78,v79\n"
"s_mov_b64 exec,0\n"
"s_cmp_eq_u32 s56,0\n"
"s_cbranch_scc1 .Lpg6f\n"
"s_cmp_eq_u32 s56,1\n"
"s_cbranch_scc1 .Lpg5f\n"
"s_cmp_eq_u32 s56,2\n"
"s_cbranch_scc1 .Lpg4f\n"
"s_branch .Lpgq0_f\n"
".Lisr3:\n"
"s_mov_b64 exec,-1\n"
".Lisx3:\n"
MF "v[116:119],v[16:19],v[180:183],v[116:119]\n"
"s_barrier\n"
MF "v[112:115],v[16:19],v[176:179],v[112:115]\n"
"v_exp_f32 v80,v80\n"
MF "v[112:115],v[20:23],v[196:199],v[112:115]\n"
"s_nop 0\n"
TR "v[16:17],v2 offset:24576\n"
TR "v[18:19],v2 offset:30720\n"
MF "v[112:115],v[24:27],v[216:219],v[112:115]\n"
"s_add_u32 s66,s67,4718592\n"
TR "v[20:21],v3 offset:24576\n"
TR "v[22:23],v3 offset:30720\n"
MF "v[112:115],v[28:31],v[236:239],v[112:115]\n"
"s_sub_u32 s81,s81,384\n"
"s_mov_b32 s83,7077888\n"
"s_add_u32 m0,s62,0\n"
MF "v[116:119],v[28:31],v[240:243],v[116:119]\n"
"s_add_u32 s82,s66,2359296\n"
"s_sub_u32 s15,s82,7864320\n"
"s_min_u32 s82,s82,s15\n"
MF "v[116:119],v[24:27],v[220:223],v[116:119]\n"
LD "v6,s[28:31],s82 offen lds\n"
MF "v[120:123],v[24:27],v[224:227],v[120:123]\n"
"s_add_u32 s82,s82,97280\n"
"v_exp_f32 v81,v81\n"
MF "v[120:123],v[28:31],v[244:247],v[120:123]\n"
LD "v6,s[28:31],s82 offen offset:1024 lds\n"
MF "v[124:127],v[28:31],v[248:251],v[124:127]\n"
"v_exp_f32 v82,v82\n"
MF "v[124:127],v[24:27],v[228:231],v[124:127]\n"
"v_exp_f32 v83,v83\n"
MF "v[128:131],v[24:27],v[232:235],v[128:131]\n"
"v_add_f32 v54,v59,v80\n"
"v_add_f32 v54,v54,v81\n"
MF "v[128:131],v[28:31],v[252:255],v[128:131]\n"
"s_waitcnt lgkmcnt(0)\n"
TR "v[24:25],v4 offset:24576\n"
TR "v[26:27],v4 offset:30720\n"
MF "a[0:3],v[16:19],v[92:95],a[0:3]\n"
"s_add_u32 s82,s82,97280\n"
TR "v[28:29],v5 offset:24576\n"
TR "v[30:31],v5 offset:30720\n"
MF "a[20:23],v[20:23],v[92:95],a[20:23]\n"
LD "v6,s[28:31],s82 offen offset:2048 lds\n"
MF "a[24:27],v[20:23],v[96:99],a[24:27]\n"
CV "v80,v80,v81\n"
"v_add_f32 v54,v54,v82\n"
MF "a[4:7],v[16:19],v[96:99],a[4:7]\n"
"v_add_f32 v54,v54,v83\n"
CV "v81,v82,v83\n"
MF "a[8:11],v[16:19],v[100:103],a[8:11]\n"
"s_add_u32 s82,s82,97280\n"
"v_exp_f32 v84,v84\n"
MF "a[28:31],v[20:23],v[100:103],a[28:31]\n"
LD "v6,s[28:31],s82 offen offset:3072 lds\n"
MF "a[32:35],v[20:23],v[104:107],a[32:35]\n"
"v_exp_f32 v85,v85\n"
MF "a[12:15],v[16:19],v[104:107],a[12:15]\n"
"v_exp_f32 v86,v86\n"
MF "a[16:19],v[16:19],v[108:111],a[16:19]\n"
"v_exp_f32 v87,v87\n"
MF "a[36:39],v[20:23],v[108:111],a[36:39]\n"
"s_waitcnt lgkmcnt(0)\n"
TR "v[16:17],v2 offset:25600\n"
TR "v[18:19],v2 offset:31744\n"
MF "a[56:59],v[24:27],v[108:111],a[56:59]\n"
"s_nop 0\n"
TR "v[20:21],v3 offset:25600\n"
TR "v[22:23],v3 offset:31744\n"
MF "a[76:79],v[28:31],v[108:111],a[76:79]\n"
"v_add_f32 v55,v60,v84\n"
"v_add_f32 v55,v55,v85\n"
MF "a[72:75],v[28:31],v[104:107],a[72:75]\n"
CV "v84,v84,v85\n"
"v_add_f32 v55,v55,v86\n"
MF "a[52:55],v[24:27],v[104:107],a[52:55]\n"
"v_add_f32 v55,v55,v87\n"
CV "v85,v86,v87\n"
MF "a[48:51],v[24:27],v[100:103],a[48:51]\n"
"v_exp_f32 v88,v88\n"
MF "a[68:71],v[28:31],v[100:103],a[68:71]\n"
"v_exp_f32 v89,v89\n"
MF "a[64:67],v[28:31],v[96:99],a[64:67]\n"
"v_exp_f32 v90,v90\n"
MF "a[44:47],v[24:27],v[96:99],a[44:47]\n"
"v_exp_f32 v91,v91\n"
MF "a[40:43],v[24:27],v[92:95],a[40:43]\n"
"v_add_f32 v56,v61,v88\n"
"v_add_f32 v56,v56,v89\n"
MF "a[60:63],v[28:31],v[92:95],a[60:63]\n"
"s_waitcnt lgkmcnt(0)\n"
TR "v[24:25],v4 offset:25600\n"
TR "v[26:27],v4 offset:31744\n"
MF "a[80:83],v[16:19],v[92:95],a[80:83]\n"
"s_nop 0\n"
TR "v[28:29],v5 offset:25600\n"
TR "v[30:31],v5 offset:31744\n"
MF "a[100:103],v[20:23],v[92:95],a[100:103]\n"
CV "v88,v88,v89\n"
"v_add_f32 v56,v56,v90\n"
MF "a[104:107],v[20:23],v[96:99],a[104:107]\n"
"v_add_f32 v56,v56,v91\n"
CV "v89,v90,v91\n"
MF "a[84:87],v[16:19],v[96:99],a[84:87]\n"
"v_exp_f32 v112,v112\n"
MF "a[88:91],v[16:19],v[100:103],a[88:91]\n"
"v_exp_f32 v113,v113\n"
MF "a[108:111],v[20:23],v[100:103],a[108:111]\n"
"v_exp_f32 v114,v114\n"
MF "a[112:115],v[20:23],v[104:107],a[112:115]\n"
"v_exp_f32 v115,v115\n"
MF "a[92:95],v[16:19],v[104:107],a[92:95]\n"
"v_add_f32 v52,v52,v112\n"
"v_add_f32 v52,v52,v113\n"
MF "a[96:99],v[16:19],v[108:111],a[96:99]\n"
CV "v74,v112,v113\n"
"v_add_f32 v52,v52,v114\n"
MF "a[116:119],v[20:23],v[108:111],a[116:119]\n"
"s_waitcnt lgkmcnt(0)\n"
TR "v[16:17],v2 offset:26624\n"
TR "v[18:19],v2 offset:32768\n"
MF "a[136:139],v[24:27],v[108:111],a[136:139]\n"
"s_nop 0\n"
TR "v[20:21],v3 offset:26624\n"
TR "v[22:23],v3 offset:32768\n"
MF "a[156:159],v[28:31],v[108:111],a[156:159]\n"
"v_add_f32 v52,v52,v115\n"
CV "v75,v114,v115\n"
MF "a[152:155],v[28:31],v[104:107],a[152:155]\n"
"v_exp_f32 v116,v116\n"
MF "a[132:135],v[24:27],v[104:107],a[132:135]\n"
"v_exp_f32 v117,v117\n"
MF "a[128:131],v[24:27],v[100:103],a[128:131]\n"
"v_exp_f32 v118,v118\n"
MF "a[148:151],v[28:31],v[100:103],a[148:151]\n"
"v_exp_f32 v119,v119\n"
MF "a[144:147],v[28:31],v[96:99],a[144:147]\n"
"v_add_f32 v53,v53,v116\n"
"v_add_f32 v53,v53,v117\n"
MF "a[124:127],v[24:27],v[96:99],a[124:127]\n"
CV "v78,v116,v117\n"
"v_add_f32 v53,v53,v118\n"
MF "a[120:123],v[24:27],v[92:95],a[120:123]\n"
"v_add_f32 v53,v53,v119\n"
CV "v79,v118,v119\n"
MF "a[140:143],v[28:31],v[92:95],a[140:143]\n"
"s_waitcnt lgkmcnt(0)\n"
TR "v[24:25],v4 offset:26624\n"
TR "v[26:27],v4 offset:32768\n"
MF "a[160:163],v[16:19],v[92:95],a[160:163]\n"
"s_nop 0\n"
TR "v[28:29],v5 offset:26624\n"
TR "v[30:31],v5 offset:32768\n"
MF "a[180:183],v[20:23],v[92:95],a[180:183]\n"
"v_exp_f32 v120,v120\n"
MF "a[184:187],v[20:23],v[96:99],a[184:187]\n"
"v_exp_f32 v121,v121\n"
MF "a[164:167],v[16:19],v[96:99],a[164:167]\n"
"v_exp_f32 v122,v122\n"
MF "a[168:171],v[16:19],v[100:103],a[168:171]\n"
"v_exp_f32 v123,v123\n"
MF "a[188:191],v[20:23],v[100:103],a[188:191]\n"
"v_add_f32 v54,v54,v120\n"
"v_add_f32 v54,v54,v121\n"
MF "a[192:195],v[20:23],v[104:107],a[192:195]\n"
CV "v82,v120,v121\n"
"v_add_f32 v54,v54,v122\n"
MF "a[172:175],v[16:19],v[104:107],a[172:175]\n"
"s_waitcnt vmcnt(10) lgkmcnt(0)\n"
"v_add_f32 v54,v54,v123\n"
CV "v83,v122,v123\n"
MF "a[176:179],v[16:19],v[108:111],a[176:179]\n"
"s_barrier\n"
MF "a[196:199],v[20:23],v[108:111],a[196:199]\n"
"s_add_u32 s73,s73,12288\n"
"ds_read_b128 v[16:19],v0 offset:24576\n"
"ds_read_b128 v[20:23],v1 offset:24576\n"
MF "a[216:219],v[24:27],v[108:111],a[216:219]\n"
"s_cmp_eq_u32 s73,s19\n"
"s_cselect_b32 s73,s18,s73\n"
"s_add_u32 m0,s65,73728\n"
MF "a[236:239],v[28:31],v[108:111],a[236:239]\n"
LD "v8,s[36:39],s73 offen nt lds\n"
MF "a[232:235],v[28:31],v[104:107],a[232:235]\n"
"s_add_u32 m0,s65,74624\n"
MF "a[212:215],v[24:27],v[104:107],a[212:215]\n"
LD "v8,s[36:39],s73 offen offset:128 nt lds\n"
MF "a[208:211],v[24:27],v[100:103],a[208:211]\n"
"s_add_u32 m0,s65,75520\n"
MF "a[228:231],v[28:31],v[100:103],a[228:231]\n"
LD "v8,s[36:39],s73 offen offset:256 nt lds\n"
MF "a[224:227],v[28:31],v[96:99],a[224:227]\n"
MF "a[204:207],v[24:27],v[96:99],a[204:207]\n"
MF "a[200:203],v[24:27],v[92:95],a[200:203]\n"
MF "a[220:223],v[28:31],v[92:95],a[220:223]\n"
"s_waitcnt lgkmcnt(0)\n"
MF "v[92:95],v[16:19],v[136:139],v[32:35]\n"
"s_add_u32 m0,s65,73728\n"
"ds_read_b128 v[24:27],v0 offset:25600\n"
"ds_read_b128 v[28:31],v1 offset:25600\n"
MF "v[92:95],v[20:23],v[156:159],v[92:95]\n"
LD "v8,s[36:39],s73 offen offset:3072 nt lds\n"
MF "v[96:99],v[20:23],v[160:163],v[36:39]\n"
"v_exp_f32 v124,v124\n"
MF "v[96:99],v[16:19],v[140:143],v[96:99]\n"
"v_exp_f32 v125,v125\n"
MF "v[100:103],v[16:19],v[144:147],v[40:43]\n"
"s_add_u32 m0,s65,74624\n"
"v_exp_f32 v126,v126\n"
MF "v[100:103],v[20:23],v[164:167],v[100:103]\n"
LD "v8,s[36:39],s73 offen offset:3200 nt lds\n"
MF "v[104:107],v[20:23],v[168:171],v[44:47]\n"
"v_exp_f32 v127,v127\n"
MF "v[104:107],v[16:19],v[148:151],v[104:107]\n"
"s_add_u32 m0,s65,75520\n"
"v_add_f32 v55,v55,v124\n"
"v_add_f32 v55,v55,v125\n"
MF "v[108:111],v[16:19],v[152:155],v[48:51]\n"
LD "v8,s[36:39],s73 offen offset:3328 nt lds\n"
MF "v[108:111],v[20:23],v[172:175],v[108:111]\n"
"s_waitcnt lgkmcnt(0)\n"
"ds_read_b128 v[16:19],v0 offset:26624\n"
"ds_read_b128 v[20:23],v1 offset:26624\n"
MF "v[108:111],v[24:27],v[192:195],v[108:111]\n"
CV "v86,v124,v125\n"
"v_add_f32 v55,v55,v126\n"
MF "v[108:111],v[28:31],v[212:215],v[108:111]\n"
"v_add_f32 v55,v55,v127\n"
CV "v87,v126,v127\n"
MF "v[104:107],v[28:31],v[208:211],v[104:107]\n"
"v_exp_f32 v128,v128\n"
MF "v[104:107],v[24:27],v[188:191],v[104:107]\n"
"v_exp_f32 v129,v129\n"
MF "v[100:103],v[24:27],v[184:187],v[100:103]\n"
"v_exp_f32 v130,v130\n"
MF "v[100:103],v[28:31],v[204:207],v[100:103]\n"
"v_exp_f32 v131,v131\n"
MF "v[96:99],v[28:31],v[200:203],v[96:99]\n"
"v_add_f32 v56,v56,v128\n"
"v_add_f32 v56,v56,v129\n"
MF "v[96:99],v[24:27],v[180:183],v[96:99]\n"
CV "v90,v128,v129\n"
"v_add_f32 v56,v56,v130\n"
MF "v[92:95],v[24:27],v[176:179],v[92:95]\n"
"v_add_f32 v56,v56,v131\n"
CV "v91,v130,v131\n"
MF "v[92:95],v[28:31],v[196:199],v[92:95]\n"
"s_waitcnt lgkmcnt(0)\n"
"ds_read_b128 v[24:27],v0 offset:30720\n"
"ds_read_b128 v[28:31],v1 offset:30720\n"
MF "v[92:95],v[16:19],v[216:219],v[92:95]\n"
MF "v[92:95],v[20:23],v[236:239],v[92:95]\n"
MF "v[96:99],v[20:23],v[240:243],v[96:99]\n"
MF "v[96:99],v[16:19],v[220:223],v[96:99]\n"
MF "v[100:103],v[16:19],v[224:227],v[100:103]\n"
MF "v[100:103],v[20:23],v[244:247],v[100:103]\n"
MF "v[104:107],v[20:23],v[248:251],v[104:107]\n"
MF "v[104:107],v[16:19],v[228:231],v[104:107]\n"
"v_max3_f32 v10,v52,v53,v54\n"
"v_max3_f32 v10,v10,v55,v56\n"
MF "v[108:111],v[16:19],v[232:235],v[108:111]\n"
"v_cmp_le_f32 vcc,0x47800000,v10\n"
MF "v[108:111],v[20:23],v[252:255],v[108:111]\n"
"s_waitcnt lgkmcnt(0)\n"
"s_cbranch_vccnz .Lstub_9\n"
".Lback_9:\n"
MF "v[112:115],v[24:27],v[136:139],v[32:35]\n"
"s_add_u32 s74,s74,12288\n"
"ds_read_b128 v[16:19],v0 offset:31744\n"
"ds_read_b128 v[20:23],v1 offset:31744\n"
MF "v[112:115],v[28:31],v[156:159],v[112:115]\n"
"s_cmp_eq_u32 s74,s20\n"
"s_cselect_b32 s74,s70,s74\n"
MF "v[116:119],v[28:31],v[160:163],v[36:39]\n"
"v_exp_f32 v92,v92\n"
MF "v[116:119],v[24:27],v[140:143],v[116:119]\n"
"v_exp_f32 v93,v93\n"
MF "v[120:123],v[24:27],v[144:147],v[40:43]\n"
"v_exp_f32 v94,v94\n"
MF "v[120:123],v[28:31],v[164:167],v[120:123]\n"
"v_exp_f32 v95,v95\n"
MF "v[124:127],v[28:31],v[168:171],v[44:47]\n"
"v_add_f32 v57,v52,v92\n"
"v_add_f32 v57,v57,v93\n"
MF "v[124:127],v[24:27],v[148:151],v[124:127]\n"
CV "v92,v92,v93\n"
"v_add_f32 v57,v57,v94\n"
MF "v[128:131],v[24:27],v[152:155],v[48:51]\n"
"v_add_f32 v57,v57,v95\n"
CV "v93,v94,v95\n"
MF "v[128:131],v[28:31],v[172:175],v[128:131]\n"
"s_waitcnt lgkmcnt(0)\n"
"ds_read_b128 v[24:27],v0 offset:32768\n"
"ds_read_b128 v[28:31],v1 offset:32768\n"
MF "v[128:131],v[16:19],v[192:195],v[128:131]\n"
"v_exp_f32 v96,v96\n"
MF "v[128:131],v[20:23],v[212:215],v[128:131]\n"
"v_exp_f32 v97,v97\n"
MF "v[124:127],v[20:23],v[208:211],v[124:127]\n"
"v_exp_f32 v98,v98\n"
MF "v[124:127],v[16:19],v[188:191],v[124:127]\n"
"v_exp_f32 v99,v99\n"
MF "v[120:123],v[16:19],v[184:187],v[120:123]\n"
"v_add_f32 v58,v53,v96\n"
"v_add_f32 v58,v58,v97\n"
MF "v[120:123],v[20:23],v[204:207],v[120:123]\n"
CV "v96,v96,v97\n"
"v_add_f32 v58,v58,v98\n"
MF "v[116:119],v[20:23],v[200:203],v[116:119]\n"
"s_waitcnt vmcnt(10) lgkmcnt(0)\n"
"v_add_f32 v58,v58,v99\n"
CV "v97,v98,v99\n"
MF "v[116:119],v[16:19],v[180:183],v[116:119]\n"
"s_barrier\n"
MF "v[112:115],v[16:19],v[176:179],v[112:115]\n"
"v_exp_f32 v100,v100\n"
MF "v[112:115],v[20:23],v[196:199],v[112:115]\n"
"s_nop 0\n"
TR "v[16:17],v2\n"
TR "v[18:19],v2 offset:6144\n"
MF "v[112:115],v[24:27],v[216:219],v[112:115]\n"
"s_add_u32 m0,s62,16384\n"
TR "v[20:21],v3\n"
TR "v[22:23],v3 offset:6144\n"
MF "v[112:115],v[28:31],v[236:239],v[112:115]\n"
"s_add_u32 s82,s66,2359424\n"
"s_sub_u32 s15,s82,7864320\n"
"s_min_u32 s82,s82,s15\n"
MF "v[116:119],v[28:31],v[240:243],v[116:119]\n"
LD "v6,s[28:31],s82 offen lds\n"
MF "v[116:119],v[24:27],v[220:223],v[116:119]\n"
"s_add_u32 s82,s82,97280\n"
"v_exp_f32 v101,v101\n"
MF "v[120:123],v[24:27],v[224:227],v[120:123]\n"
LD "v6,s[28:31],s82 offen offset:1024 lds\n"
MF "v[120:123],v[28:31],v[244:247],v[120:123]\n"
"v_exp_f32 v102,v102\n"
MF "v[124:127],v[28:31],v[248:251],v[124:127]\n"
"s_add_u32 s82,s82,97280\n"
"v_exp_f32 v103,v103\n"
MF "v[124:127],v[24:27],v[228:231],v[124:127]\n"
LD "v6,s[28:31],s82 offen offset:2048 lds\n"
MF "v[128:131],v[24:27],v[232:235],v[128:131]\n"
"v_add_f32 v59,v54,v100\n"
"v_add_f32 v59,v59,v101\n"
MF "v[128:131],v[28:31],v[252:255],v[128:131]\n"
"s_waitcnt lgkmcnt(0)\n"
TR "v[24:25],v4\n"
TR "v[26:27],v4 offset:6144\n"
MF "a[0:3],v[16:19],v[72:75],a[0:3]\n"
"s_add_u32 s82,s82,97280\n"
TR "v[28:29],v5\n"
TR "v[30:31],v5 offset:6144\n"
MF "a[20:23],v[20:23],v[72:75],a[20:23]\n"
LD "v6,s[28:31],s82 offen offset:3072 lds\n"
MF "a[24:27],v[20:23],v[76:79],a[24:27]\n"
"s_add_u32 m0,s62,32768\n"
CV "v100,v100,v101\n"
"v_add_f32 v59,v59,v102\n"
MF "a[4:7],v[16:19],v[76:79],a[4:7]\n"
"s_add_u32 s82,s66,2359552\n"
"s_sub_u32 s15,s82,7864320\n"
"s_min_u32 s82,s82,s15\n"
MF "a[8:11],v[16:19],v[80:83],a[8:11]\n"
LD "v6,s[28:31],s82 offen lds\n"
MF "a[28:31],v[20:23],v[80:83],a[28:31]\n"
"v_add_f32 v59,v59,v103\n"
CV "v101,v102,v103\n"
MF "a[32:35],v[20:23],v[84:87],a[32:35]\n"
"s_add_u32 s82,s82,97280\n"
"v_exp_f32 v104,v104\n"
MF "a[12:15],v[16:19],v[84:87],a[12:15]\n"
LD "v6,s[28:31],s82 offen offset:1024 lds\n"
MF "a[16:19],v[16:19],v[88:91],a[16:19]\n"
".Lpg12f:\n"
"s_cbranch_execz .Lpg18f\n"
"v_exp_f32 v105,v105\n"
MF "a[36:39],v[20:23],v[88:91],a[36:39]\n"
"s_waitcnt lgkmcnt(0)\n"
TR "v[16:17],v2 offset:1024\n"
TR "v[18:19],v2 offset:7168\n"
MF "a[56:59],v[24:27],v[88:91],a[56:59]\n"
"s_nop 0\n"
TR "v[20:21],v3 offset:1024\n"
TR "v[22:23],v3 offset:7168\n"
MF "a[76:79],v[28:31],v[88:91],a[76:79]\n"
"v_exp_f32 v106,v106\n"
MF "a[72:75],v[28:31],v[84:87],a[72:75]\n"
"v_exp_f32 v107,v107\n"
MF "a[52:55],v[24:27],v[84:87],a[52:55]\n"
"v_add_f32 v60,v55,v104\n"
"v_add_f32 v60,v60,v105\n"
MF "a[48:51],v[24:27],v[80:83],a[48:51]\n"
CV "v104,v104,v105\n"
"v_add_f32 v60,v60,v106\n"
MF "a[68:71],v[28:31],v[80:83],a[68:71]\n"
"v_add_f32 v60,v60,v107\n"
CV "v105,v106,v107\n"
MF "a[64:67],v[28:31],v[76:79],a[64:67]\n"
"v_exp_f32 v108,v108\n"
MF "a[44:47],v[24:27],v[76:79],a[44:47]\n"
"v_exp_f32 v109,v109\n"
MF "a[40:43],v[24:27],v[72:75],a[40:43]\n"
"v_exp_f32 v110,v110\n"
MF "a[60:63],v[28:31],v[72:75],a[60:63]\n"
"s_waitcnt lgkmcnt(0)\n"
TR "v[24:25],v4 offset:1024\n"
TR "v[26:27],v4 offset:7168\n"
MF "a[80:83],v[16:19],v[72:75],a[80:83]\n"
"s_nop 0\n"
TR "v[28:29],v5 offset:1024\n"
TR "v[30:31],v5 offset:7168\n"
MF "a[100:103],v[20:23],v[72:75],a[100:103]\n"
"v_exp_f32 v111,v111\n"
MF "a[104:107],v[20:23],v[76:79],a[104:107]\n"
"v_add_f32 v61,v56,v108\n"
"v_add_f32 v61,v61,v109\n"
MF "a[84:87],v[16:19],v[76:79],a[84:87]\n"
CV "v108,v108,v109\n"
"v_add_f32 v61,v61,v110\n"
MF "a[88:91],v[16:19],v[80:83],a[88:91]\n"
"v_add_f32 v61,v61,v111\n"
CV "v109,v110,v111\n"
MF "a[108:111],v[20:23],v[80:83],a[108:111]\n"
"v_exp_f32 v112,v112\n"
MF "a[112:115],v[20:23],v[84:87],a[112:115]\n"
"v_exp_f32 v113,v113\n"
MF "a[92:95],v[16:19],v[84:87],a[92:95]\n"
"v_exp_f32 v114,v114\n"
MF "a[96:99],v[16:19],v[88:91],a[96:99]\n"
"v_exp_f32 v115,v115\n"
MF "a[116:119],v[20:23],v[88:91],a[116:119]\n"
"s_waitcnt lgkmcnt(0)\n"
TR "v[16:17],v2 offset:2048\n"
TR "v[18:19],v2 offset:8192\n"
MF "a[136:139],v[24:27],v[88:91],a[136:139]\n"
"s_nop 0\n"
TR "v[20:21],v3 offset:2048\n"
TR "v[22:23],v3 offset:8192\n"
MF "a[156:159],v[28:31],v[88:91],a[156:159]\n"
"v_add_f32 v57,v57,v112\n"
"v_add_f32 v57,v57,v113\n"
MF "a[152:155],v[28:31],v[84:87],a[152:155]\n"
CV "v94,v112,v113\n"
"v_add_f32 v57,v57,v114\n"
MF "a[132:135],v[24:27],v[84:87],a[132:135]\n"
"v_add_f32 v57,v57,v115\n"
CV "v95,v114,v115\n"
MF "a[128:131],v[24:27],v[80:83],a[128:131]\n"
"v_exp_f32 v116,v116\n"
MF "a[148:151],v[28:31],v[80:83],a[148:151]\n"
"v_exp_f32 v117,v117\n"
MF "a[144:147],v[28:31],v[76:79],a[144:147]\n"
"v_exp_f32 v118,v118\n"
MF "a[124:127],v[24:27],v[76:79],a[124:127]\n"
"v_exp_f32 v119,v119\n"
MF "a[120:123],v[24:27],v[72:75],a[120:123]\n"
"v_add_f32 v58,v58,v116\n"
"v_add_f32 v58,v58,v117\n"
MF "a[140:143],v[28:31],v[72:75],a[140:143]\n"
"s_waitcnt lgkmcnt(0)\n"
TR "v[24:25],v4 offset:2048\n"
TR "v[26:27],v4 offset:8192\n"
MF "a[160:163],v[16:19],v[72:75],a[160:163]\n"
"s_nop 0\n"
TR "v[28:29],v5 offset:2048\n"
TR "v[30:31],v5 offset:8192\n"
MF "a[180:183],v[20:23],v[72:75],a[180:183]\n"
CV "v98,v116,v117\n"
"v_add_f32 v58,v58,v118\n"
MF "a[184:187],v[20:23],v[76:79],a[184:187]\n"
"v_add_f32 v58,v58,v119\n"
CV "v99,v118,v119\n"
MF "a[164:167],v[16:19],v[76:79],a[164:167]\n"
"v_exp_f32 v120,v120\n"
MF "a[168:171],v[16:19],v[80:83],a[168:171]\n"
"v_exp_f32 v121,v121\n"
MF "a[188:191],v[20:23],v[80:83],a[188:191]\n"
"v_exp_f32 v122,v122\n"
MF "a[192:195],v[20:23],v[84:87],a[192:195]\n"
"v_exp_f32 v123,v123\n"
MF "a[172:175],v[16:19],v[84:87],a[172:175]\n"
"v_add_f32 v59,v59,v120\n"
"v_add_f32 v59,v59,v121\n"
MF "a[176:179],v[16:19],v[88:91],a[176:179]\n"
"s_waitcnt lgkmcnt(0)\n"
CV "v102,v120,v121\n"
"v_add_f32 v59,v59,v122\n"
MF "a[196:199],v[20:23],v[88:91],a[196:199]\n"
"s_add_u32 s82,s82,97280\n"
"v_add_f32 v59,v59,v123\n"
CV "v103,v122,v123\n"
MF "a[216:219],v[24:27],v[88:91],a[216:219]\n"
LD "v6,s[28:31],s82 offen offset:2048 lds\n"
MF "a[236:239],v[28:31],v[88:91],a[236:239]\n"
"v_exp_f32 v124,v124\n"
MF "a[232:235],v[28:31],v[84:87],a[232:235]\n"
"s_add_u32 s82,s82,97280\n"
"v_exp_f32 v125,v125\n"
MF "a[212:215],v[24:27],v[84:87],a[212:215]\n"
LD "v6,s[28:31],s82 offen offset:3072 lds\n"
MF "a[208:211],v[24:27],v[80:83],a[208:211]\n"
"v_exp_f32 v126,v126\n"
MF "a[228:231],v[28:31],v[80:83],a[228:231]\n"
"v_exp_f32 v127,v127\n"
MF "a[224:227],v[28:31],v[76:79],a[224:227]\n"
"v_add_f32 v60,v60,v124\n"
"v_add_f32 v60,v60,v125\n"
MF "a[204:207],v[24:27],v[76:79],a[204:207]\n"
CV "v106,v124,v125\n"
"v_add_f32 v60,v60,v126\n"
MF "a[200:203],v[24:27],v[72:75],a[200:203]\n"
"v_add_f32 v60,v60,v127\n"
CV "v107,v126,v127\n"
MF "a[220:223],v[28:31],v[72:75],a[220:223]\n"
"v_exp_f32 v128,v128\n"
"v_exp_f32 v129,v129\n"
"v_exp_f32 v130,v130\n"
"v_exp_f32 v131,v131\n"
"v_add_f32 v61,v61,v128\n"
"v_add_f32 v61,v61,v129\n"
CV "v110,v128,v129\n"
"v_add_f32 v61,v61,v130\n"
"v_add_f32 v61,v61,v131\n"
CV "v111,v130,v131\n"
"v_max3_f32 v10,v57,v58,v59\n"
"v_max3_f32 v10,v10,v60,v61\n"
"v_cmp_le_f32 vcc,0x47800000,v10\n"
"s_cbranch_vccnz .Lstub_10\n"
".Lback_10:\n"
"s_waitcnt vmcnt(8) lgkmcnt(0)\n"
"s_barrier\n"
"v_add_u32 v11,97280,v6\n"
"v_add_u32 v12,194560,v6\n"
"v_add_u32 v13,291840,v6\n"
"v_mov_b32 v10,1.0\n"
TR "v[136:137],v2 offset:24576\n"
TR "v[138:139],v2 offset:30720\n"
"v_mfma_f32_16x16x4_f32 v[16:19],v10,v57,0\n"
TR "v[140:141],v3 offset:24576\n"
TR "v[142:143],v3 offset:30720\n"
TR "v[144:145],v4 offset:24576\n"
TR "v[146:147],v4 offset:30720\n"
TR "v[148:149],v5 offset:24576\n"
TR "v[150:151],v5 offset:30720\n"
"v_mfma_f32_16x16x4_f32 v[20:23],v10,v58,0\n"
TR "v[152:153],v2 offset:25600\n"
TR "v[154:155],v2 offset:31744\n"
TR "v[156:157],v3 offset:25600\n"
TR "v[158:159],v3 offset:31744\n"
TR "v[160:161],v4 offset:25600\n"
TR "v[162:163],v4 offset:31744\n"
"v_mfma_f32_16x16x4_f32 v[24:27],v10,v59,0\n"
TR "v[164:165],v5 offset:25600\n"
TR "v[166:167],v5 offset:31744\n"
TR "v[168:169],v2 offset:26624\n"
TR "v[170:171],v2 offset:32768\n"
TR "v[172:173],v3 offset:26624\n"
TR "v[174:175],v3 offset:32768\n"
"v_mfma_f32_16x16x4_f32 v[28:31],v10,v60,0\n"
TR "v[176:177],v4 offset:26624\n"
TR "v[178:179],v4 offset:32768\n"
TR "v[180:181],v5 offset:26624\n"
TR "v[182:183],v5 offset:32768\n"
"v_mfma_f32_16x16x4_f32 v[32:35],v10,v61,0\n"
"s_waitcnt lgkmcnt(15)\n"
MF "a[0:3],v[136:139],v[92:95],a[0:3]\n"
MF "a[20:23],v[140:143],v[92:95],a[20:23]\n"
"v_rcp_f32 v57,v16\n"
"v_rcp_f32 v58,v20\n"
"v_rcp_f32 v59,v24\n"
"v_rcp_f32 v60,v28\n"
"s_nop 2\n"
"v_rcp_f32_e64 v61,v32\n"
MF "a[4:7],v[136:139],v[96:99],a[4:7]\n"
"v_mul_f32_e64 v52,-s13,v57\n"
"v_mul_f32_e64 v53,-s13,v58\n"
"v_mul_f32_e64 v54,-s13,v59\n"
"v_mul_f32_e64 v55,-s13,v60\n"
"v_mul_f32_e64 v56,-s13,v61\n"
AR "v72,a0\n"
AR "v73,a1\n"
AR "v74,a2\n"
MF "a[24:27],v[140:143],v[96:99],a[24:27]\n"
AR "v75,a3\n"
AR "v76,a20\n"
AR "v77,a21\n"
AR "v78,a22\n"
AR "v79,a23\n"
"v_pk_fma_f32 v[72:73],v[72:73],v[56:57],v[52:53] op_sel:[0,1,0] op_sel_hi:[1,1,0]\n"
"v_pk_fma_f32 v[74:75],v[74:75],v[56:57],v[52:53] op_sel:[0,1,0] op_sel_hi:[1,1,0]\n"
"v_pk_fma_f32 v[76:77],v[76:77],v[56:57],v[52:53] op_sel:[0,1,0] op_sel_hi:[1,1,0]\n"
"v_pk_fma_f32 v[78:79],v[78:79],v[56:57],v[52:53] op_sel:[0,1,0] op_sel_hi:[1,1,0]\n"
CV "v112,v72,v73\n"
CV "v113,v74,v75\n"
CV "v114,v76,v77\n"
CV "v115,v78,v79\n"
MF "a[8:11],v[136:139],v[100:103],a[8:11]\n"
AW "a0,v112\n"
AW "a1,v113\n"
AW "a2,v114\n"
AW "a3,v115\n"
AR "v80,a4\n"
AR "v81,a5\n"
AR "v82,a6\n"
MF "a[28:31],v[140:143],v[100:103],a[28:31]\n"
AR "v83,a7\n"
AR "v84,a24\n"
AR "v85,a25\n"
AR "v86,a26\n"
AR "v87,a27\n"
"v_pk_fma_f32 v[80:81],v[80:81],v[58:59],v[52:53] op_sel:[0,0,1] op_sel_hi:[1,0,1]\n"
"v_pk_fma_f32 v[82:83],v[82:83],v[58:59],v[52:53] op_sel:[0,0,1] op_sel_hi:[1,0,1]\n"
"v_pk_fma_f32 v[84:85],v[84:85],v[58:59],v[52:53] op_sel:[0,0,1] op_sel_hi:[1,0,1]\n"
"v_pk_fma_f32 v[86:87],v[86:87],v[58:59],v[52:53] op_sel:[0,0,1] op_sel_hi:[1,0,1]\n"
CV "v116,v80,v81\n"
CV "v117,v82,v83\n"
CV "v118,v84,v85\n"
CV "v119,v86,v87\n"
MF "a[12:15],v[136:139],v[104:107],a[12:15]\n"
AW "a4,v116\n"
AW "a5,v117\n"
AW "a6,v118\n"
AW "a7,v119\n"
AR "v72,a8\n"
AR "v73,a9\n"
AR "v74,a10\n"
MF "a[32:35],v[140:143],v[104:107],a[32:35]\n"
AR "v75,a11\n"
AR "v76,a28\n"
AR "v77,a29\n"
AR "v78,a30\n"
AR "v79,a31\n"
"v_pk_fma_f32 v[72:73],v[72:73],v[58:59],v[54:55] op_sel:[0,1,0] op_sel_hi:[1,1,0]\n"
"v_pk_fma_f32 v[74:75],v[74:75],v[58:59],v[54:55] op_sel:[0,1,0] op_sel_hi:[1,1,0]\n"
"v_pk_fma_f32 v[76:77],v[76:77],v[58:59],v[54:55] op_sel:[0,1,0] op_sel_hi:[1,1,0]\n"
"v_pk_fma_f32 v[78:79],v[78:79],v[58:59],v[54:55] op_sel:[0,1,0] op_sel_hi:[1,1,0]\n"
CV "v112,v72,v73\n"
CV "v113,v74,v75\n"
CV "v114,v76,v77\n"
CV "v115,v78,v79\n"
MF "a[16:19],v[136:139],v[108:111],a[16:19]\n"
AW "a8,v112\n"
AW "a9,v113\n"
AW "a10,v114\n"
AW "a11,v115\n"
AR "v80,a12\n"
AR "v81,a13\n"
AR "v82,a14\n"
MF "a[36:39],v[140:143],v[108:111],a[36:39]\n"
AR "v83,a15\n"
AR "v84,a32\n"
AR "v85,a33\n"
AR "v86,a34\n"
AR "v87,a35\n"
"v_pk_fma_f32 v[80:81],v[80:81],v[60:61],v[54:55] op_sel:[0,0,1] op_sel_hi:[1,0,1]\n"
"v_pk_fma_f32 v[82:83],v[82:83],v[60:61],v[54:55] op_sel:[0,0,1] op_sel_hi:[1,0,1]\n"
"v_pk_fma_f32 v[84:85],v[84:85],v[60:61],v[54:55] op_sel:[0,0,1] op_sel_hi:[1,0,1]\n"
"v_pk_fma_f32 v[86:87],v[86:87],v[60:61],v[54:55] op_sel:[0,0,1] op_sel_hi:[1,0,1]\n"
CV "v116,v80,v81\n"
CV "v117,v82,v83\n"
CV "v118,v84,v85\n"
CV "v119,v86,v87\n"
MF "a[40:43],v[144:147],v[92:95],a[40:43]\n"
AW "a12,v116\n"
AW "a13,v117\n"
AW "a14,v118\n"
AW "a15,v119\n"
AR "v72,a16\n"
AR "v73,a17\n"
AR "v74,a18\n"
MF "a[60:63],v[148:151],v[92:95],a[60:63]\n"
AR "v75,a19\n"
AR "v76,a36\n"
AR "v77,a37\n"
AR "v78,a38\n"
AR "v79,a39\n"
"v_pk_fma_f32 v[72:73],v[72:73],v[60:61],v[56:57] op_sel:[0,1,0] op_sel_hi:[1,1,0]\n"
"v_pk_fma_f32 v[74:75],v[74:75],v[60:61],v[56:57] op_sel:[0,1,0] op_sel_hi:[1,1,0]\n"
"v_pk_fma_f32 v[76:77],v[76:77],v[60:61],v[56:57] op_sel:[0,1,0] op_sel_hi:[1,1,0]\n"
"v_pk_fma_f32 v[78:79],v[78:79],v[60:61],v[56:57] op_sel:[0,1,0] op_sel_hi:[1,1,0]\n"
CV "v112,v72,v73\n"
CV "v113,v74,v75\n"
CV "v114,v76,v77\n"
CV "v115,v78,v79\n"
MF "a[44:47],v[144:147],v[96:99],a[44:47]\n"
AW "a16,v112\n"
AW "a17,v113\n"
AW "a18,v114\n"
AW "a19,v115\n"
AR "v80,a40\n"
AR "v81,a41\n"
AR "v82,a42\n"
MF "a[64:67],v[148:151],v[96:99],a[64:67]\n"
AR "v83,a43\n"
AR "v84,a60\n"
AR "v85,a61\n"
AR "v86,a62\n"
AR "v87,a63\n"
"v_pk_fma_f32 v[80:81],v[80:81],v[56:57],v[52:53] op_sel:[0,1,0] op_sel_hi:[1,1,0]\n"
"v_pk_fma_f32 v[82:83],v[82:83],v[56:57],v[52:53] op_sel:[0,1,0] op_sel_hi:[1,1,0]\n"
"v_pk_fma_f32 v[84:85],v[84:85],v[56:57],v[52:53] op_sel:[0,1,0] op_sel_hi:[1,1,0]\n"
"v_pk_fma_f32 v[86:87],v[86:87],v[56:57],v[52:53] op_sel:[0,1,0] op_sel_hi:[1,1,0]\n"
CV "v116,v80,v81\n"
CV "v117,v82,v83\n"
CV "v118,v84,v85\n"
CV "v119,v86,v87\n"
MF "a[48:51],v[144:147],v[100:103],a[48:51]\n"
AW "a40,v116\n"
AW "a41,v117\n"
AW "a42,v118\n"
AW "a43,v119\n"
AR "v72,a44\n"
AR "v73,a45\n"
AR "v74,a46\n"
MF "a[68:71],v[148:151],v[100:103],a[68:71]\n"
AR "v75,a47\n"
AR "v76,a64\n"
AR "v77,a65\n"
AR "v78,a66\n"
AR "v79,a67\n"
"v_pk_fma_f32 v[72:73],v[72:73],v[58:59],v[52:53] op_sel:[0,0,1] op_sel_hi:[1,0,1]\n"
"v_pk_fma_f32 v[74:75],v[74:75],v[58:59],v[52:53] op_sel:[0,0,1] op_sel_hi:[1,0,1]\n"
"v_pk_fma_f32 v[76:77],v[76:77],v[58:59],v[52:53] op_sel:[0,0,1] op_sel_hi:[1,0,1]\n"
"v_pk_fma_f32 v[78:79],v[78:79],v[58:59],v[52:53] op_sel:[0,0,1] op_sel_hi:[1,0,1]\n"
CV "v112,v72,v73\n"
CV "v113,v74,v75\n"
CV "v114,v76,v77\n"
CV "v115,v78,v79\n"
MF "a[52:55],v[144:147],v[104:107],a[52:55]\n"
AW "a44,v112\n"
AW "a45,v113\n"
AW "a46,v114\n"
AW "a47,v115\n"
AR "v80,a48\n"
AR "v81,a49\n"
AR "v82,a50\n"
MF "a[72:75],v[148:151],v[104:107],a[72:75]\n"
AR "v83,a51\n"
AR "v84,a68\n"
AR "v85,a69\n"
AR "v86,a70\n"
AR "v87,a71\n"
"v_pk_fma_f32 v[80:81],v[80:81],v[58:59],v[54:55] op_sel:[0,1,0] op_sel_hi:[1,1,0]\n"
"v_pk_fma_f32 v[82:83],v[82:83],v[58:59],v[54:55] op_sel:[0,1,0] op_sel_hi:[1,1,0]\n"
"v_pk_fma_f32 v[84:85],v[84:85],v[58:59],v[54:55] op_sel:[0,1,0] op_sel_hi:[1,1,0]\n"
"v_pk_fma_f32 v[86:87],v[86:87],v[58:59],v[54:55] op_sel:[0,1,0] op_sel_hi:[1,1,0]\n"
CV "v116,v80,v81\n"
CV "v117,v82,v83\n"
CV "v118,v84,v85\n"
CV "v119,v86,v87\n"
MF "a[56:59],v[144:147],v[108:111],a[56:59]\n"
AW "a48,v116\n"
AW "a49,v117\n"
AW "a50,v118\n"
AW "a51,v119\n"
AR "v72,a52\n"
AR "v73,a53\n"
AR "v74,a54\n"
MF "a[76:79],v[148:151],v[108:111],a[76:79]\n"
AR "v75,a55\n"
AR "v76,a72\n"
AR "v77,a73\n"
AR "v78,a74\n"
AR "v79,a75\n"
"v_pk_fma_f32 v[72:73],v[72:73],v[60:61],v[54:55] op_sel:[0,0,1] op_sel_hi:[1,0,1]\n"
"v_pk_fma_f32 v[74:75],v[74:75],v[60:61],v[54:55] op_sel:[0,0,1] op_sel_hi:[1,0,1]\n"
"v_pk_fma_f32 v[76:77],v[76:77],v[60:61],v[54:55] op_sel:[0,0,1] op_sel_hi:[1,0,1]\n"
"v_pk_fma_f32 v[78:79],v[78:79],v[60:61],v[54:55] op_sel:[0,0,1] op_sel_hi:[1,0,1]\n"
CV "v112,v72,v73\n"
CV "v113,v74,v75\n"
CV "v114,v76,v77\n"
CV "v115,v78,v79\n"
"s_waitcnt lgkmcnt(14)\n"
MF "a[80:83],v[152:155],v[92:95],a[80:83]\n"
AW "a52,v112\n"
AW "a53,v113\n"
AW "a54,v114\n"
AW "a55,v115\n"
AR "v80,a56\n"
AR "v81,a57\n"
AR "v82,a58\n"
"s_waitcnt lgkmcnt(12)\n"
MF "a[100:103],v[156:159],v[92:95],a[100:103]\n"
AR "v83,a59\n"
AR "v84,a76\n"
AR "v85,a77\n"
AR "v86,a78\n"
AR "v87,a79\n"
"v_pk_fma_f32 v[80:81],v[80:81],v[60:61],v[56:57] op_sel:[0,1,0] op_sel_hi:[1,1,0]\n"
"v_pk_fma_f32 v[82:83],v[82:83],v[60:61],v[56:57] op_sel:[0,1,0] op_sel_hi:[1,1,0]\n"
"v_pk_fma_f32 v[84:85],v[84:85],v[60:61],v[56:57] op_sel:[0,1,0] op_sel_hi:[1,1,0]\n"
"v_pk_fma_f32 v[86:87],v[86:87],v[60:61],v[56:57] op_sel:[0,1,0] op_sel_hi:[1,1,0]\n"
CV "v116,v80,v81\n"
CV "v117,v82,v83\n"
CV "v118,v84,v85\n"
CV "v119,v86,v87\n"
MF "a[84:87],v[152:155],v[96:99],a[84:87]\n"
AW "a56,v116\n"
AW "a57,v117\n"
AW "a58,v118\n"
AW "a59,v119\n"
AR "v72,a80\n"
AR "v73,a81\n"
AR "v74,a82\n"
MF "a[104:107],v[156:159],v[96:99],a[104:107]\n"
AR "v75,a83\n"
AR "v76,a100\n"
AR "v77,a101\n"
AR "v78,a102\n"
AR "v79,a103\n"
"v_pk_fma_f32 v[72:73],v[72:73],v[56:57],v[52:53] op_sel:[0,1,0] op_sel_hi:[1,1,0]\n"
"v_pk_fma_f32 v[74:75],v[74:75],v[56:57],v[52:53] op_sel:[0,1,0] op_sel_hi:[1,1,0]\n"
"v_pk_fma_f32 v[76:77],v[76:77],v[56:57],v[52:53] op_sel:[0,1,0] op_sel_hi:[1,1,0]\n"
"v_pk_fma_f32 v[78:79],v[78:79],v[56:57],v[52:53] op_sel:[0,1,0] op_sel_hi:[1,1,0]\n"
CV "v112,v72,v73\n"
CV "v113,v74,v75\n"
CV "v114,v76,v77\n"
CV "v115,v78,v79\n"
MF "a[88:91],v[152:155],v[100:103],a[88:91]\n"
AW "a80,v112\n"
AW "a81,v113\n"
AW "a82,v114\n"
AW "a83,v115\n"
AR "v80,a84\n"
AR "v81,a85\n"
AR "v82,a86\n"
MF "a[108:111],v[156:159],v[100:103],a[108:111]\n"
AR "v83,a87\n"
AR "v84,a104\n"
AR "v85,a105\n"
AR "v86,a106\n"
AR "v87,a107\n"
"v_pk_fma_f32 v[80:81],v[80:81],v[58:59],v[52:53] op_sel:[0,0,1] op_sel_hi:[1,0,1]\n"
"v_pk_fma_f32 v[82:83],v[82:83],v[58:59],v[52:53] op_sel:[0,0,1] op_sel_hi:[1,0,1]\n"
"v_pk_fma_f32 v[84:85],v[84:85],v[58:59],v[52:53] op_sel:[0,0,1] op_sel_hi:[1,0,1]\n"
"v_pk_fma_f32 v[86:87],v[86:87],v[58:59],v[52:53] op_sel:[0,0,1] op_sel_hi:[1,0,1]\n"
CV "v116,v80,v81\n"
CV "v117,v82,v83\n"
CV "v118,v84,v85\n"
CV "v119,v86,v87\n"
MF "a[92:95],v[152:155],v[104:107],a[92:95]\n"
AW "a84,v116\n"
AW "a85,v117\n"
AW "a86,v118\n"
AW "a87,v119\n"
AR "v72,a88\n"
AR "v73,a89\n"
AR "v74,a90\n"
MF "a[112:115],v[156:159],v[104:107],a[112:115]\n"
AR "v75,a91\n"
AR "v76,a108\n"
AR "v77,a109\n"
AR "v78,a110\n"
AR "v79,a111\n"
"v_pk_fma_f32 v[72:73],v[72:73],v[58:59],v[54:55] op_sel:[0,1,0] op_sel_hi:[1,1,0]\n"
"v_pk_fma_f32 v[74:75],v[74:75],v[58:59],v[54:55] op_sel:[0,1,0] op_sel_hi:[1,1,0]\n"
"v_pk_fma_f32 v[76:77],v[76:77],v[58:59],v[54:55] op_sel:[0,1,0] op_sel_hi:[1,1,0]\n"
"v_pk_fma_f32 v[78:79],v[78:79],v[58:59],v[54:55] op_sel:[0,1,0] op_sel_hi:[1,1,0]\n"
CV "v112,v72,v73\n"
CV "v113,v74,v75\n"
CV "v114,v76,v77\n"
CV "v115,v78,v79\n"
MF "a[96:99],v[152:155],v[108:111],a[96:99]\n"
AW "a88,v112\n"
AW "a89,v113\n"
AW "a90,v114\n"
AW "a91,v115\n"
AR "v80,a92\n"
AR "v81,a93\n"
AR "v82,a94\n"
MF "a[116:119],v[156:159],v[108:111],a[116:119]\n"
AR "v83,a95\n"
AR "v84,a112\n"
AR "v85,a113\n"
AR "v86,a114\n"
AR "v87,a115\n"
"v_pk_fma_f32 v[80:81],v[80:81],v[60:61],v[54:55] op_sel:[0,0,1] op_sel_hi:[1,0,1]\n"
"v_pk_fma_f32 v[82:83],v[82:83],v[60:61],v[54:55] op_sel:[0,0,1] op_sel_hi:[1,0,1]\n"
"v_pk_fma_f32 v[84:85],v[84:85],v[60:61],v[54:55] op_sel:[0,0,1] op_sel_hi:[1,0,1]\n"
"v_pk_fma_f32 v[86:87],v[86:87],v[60:61],v[54:55] op_sel:[0,0,1] op_sel_hi:[1,0,1]\n"
CV "v116,v80,v81\n"
CV "v117,v82,v83\n"
CV "v118,v84,v85\n"
CV "v119,v86,v87\n"
"s_waitcnt lgkmcnt(10)\n"
MF "a[120:123],v[160:163],v[92:95],a[120:123]\n"
AW "a92,v116\n"
AW "a93,v117\n"
AW "a94,v118\n"
AW "a95,v119\n"
AR "v72,a96\n"
AR "v73,a97\n"
AR "v74,a98\n"
"s_waitcnt lgkmcnt(8)\n"
MF "a[140:143],v[164:167],v[92:95],a[140:143]\n"
AR "v75,a99\n"
AR "v76,a116\n"
AR "v77,a117\n"
AR "v78,a118\n"
AR "v79,a119\n"
"v_pk_fma_f32 v[72:73],v[72:73],v[60:61],v[56:57] op_sel:[0,1,0] op_sel_hi:[1,1,0]\n"
"v_pk_fma_f32 v[74:75],v[74:75],v[60:61],v[56:57] op_sel:[0,1,0] op_sel_hi:[1,1,0]\n"
"v_pk_fma_f32 v[76:77],v[76:77],v[60:61],v[56:57] op_sel:[0,1,0] op_sel_hi:[1,1,0]\n"
"v_pk_fma_f32 v[78:79],v[78:79],v[60:61],v[56:57] op_sel:[0,1,0] op_sel_hi:[1,1,0]\n"
CV "v112,v72,v73\n"
CV "v113,v74,v75\n"
CV "v114,v76,v77\n"
CV "v115,v78,v79\n"
MF "a[124:127],v[160:163],v[96:99],a[124:127]\n"
AW "a96,v112\n"
AW "a97,v113\n"
AW "a98,v114\n"
AW "a99,v115\n"
AR "v0,a240\n"
AR "v2,a241\n"
AR "v4,a242\n"
"v_xor_b32 v1,64,v0\n"
"v_xor_b32 v3,64,v2\n"
"v_xor_b32_e64 v5,64,v4\n"
AR "v80,a120\n"
AR "v81,a121\n"
AR "v82,a122\n"
MF "a[144:147],v[164:167],v[96:99],a[144:147]\n"
AR "v83,a123\n"
AR "v84,a140\n"
AR "v85,a141\n"
AR "v86,a142\n"
".Lpg13f:\n"
"s_cbranch_execz .Lisr3\n"
"s_nop 0\n"
AR "v87,a143\n"
"v_pk_fma_f32 v[80:81],v[80:81],v[56:57],v[52:53] op_sel:[0,1,0] op_sel_hi:[1,1,0]\n"
"v_pk_fma_f32 v[82:83],v[82:83],v[56:57],v[52:53] op_sel:[0,1,0] op_sel_hi:[1,1,0]\n"
"v_pk_fma_f32 v[84:85],v[84:85],v[56:57],v[52:53] op_sel:[0,1,0] op_sel_hi:[1,1,0]\n"
"v_pk_fma_f32 v[86:87],v[86:87],v[56:57],v[52:53] op_sel:[0,1,0] op_sel_hi:[1,1,0]\n"
CV "v116,v80,v81\n"
CV "v117,v82,v83\n"
CV "v118,v84,v85\n"
CV "v119,v86,v87\n"
MF "a[128:131],v[160:163],v[100:103],a[128:131]\n"
AW "a120,v116\n"
AW "a121,v117\n"
AW "a122,v118\n"
AW "a123,v119\n"
AR "v72,a124\n"
AR "v73,a125\n"
AR "v74,a126\n"
MF "a[148:151],v[164:167],v[100:103],a[148:151]\n"
AR "v75,a127\n"
AR "v76,a144\n"
AR "v77,a145\n"
AR "v78,a146\n"
AR "v79,a147\n"
"v_pk_fma_f32 v[72:73],v[72:73],v[58:59],v[52:53] op_sel:[0,0,1] op_sel_hi:[1,0,1]\n"
"v_pk_fma_f32 v[74:75],v[74:75],v[58:59],v[52:53] op_sel:[0,0,1] op_sel_hi:[1,0,1]\n"
"v_pk_fma_f32 v[76:77],v[76:77],v[58:59],v[52:53] op_sel:[0,0,1] op_sel_hi:[1,0,1]\n"
"v_pk_fma_f32 v[78:79],v[78:79],v[58:59],v[52:53] op_sel:[0,0,1] op_sel_hi:[1,0,1]\n"
CV "v112,v72,v73\n"
CV "v113,v74,v75\n"
CV "v114,v76,v77\n"
CV "v115,v78,v79\n"
MF "a[132:135],v[160:163],v[104:107],a[132:135]\n"
"ds_read_b128 a[60:63],v2\n"
AW "a124,v112\n"
AW "a125,v113\n"
AW "a126,v114\n"
AW "a127,v115\n"
AR "v80,a128\n"
AR "v81,a129\n"
AR "v82,a130\n"
MF "a[152:155],v[164:167],v[104:107],a[152:155]\n"
AR "v83,a131\n"
AR "v84,a148\n"
AR "v85,a149\n"
AR "v86,a150\n"
AR "v87,a151\n"
"v_pk_fma_f32 v[80:81],v[80:81],v[58:59],v[54:55] op_sel:[0,1,0] op_sel_hi:[1,1,0]\n"
"v_pk_fma_f32 v[82:83],v[82:83],v[58:59],v[54:55] op_sel:[0,1,0] op_sel_hi:[1,1,0]\n"
"v_pk_fma_f32 v[84:85],v[84:85],v[58:59],v[54:55] op_sel:[0,1,0] op_sel_hi:[1,1,0]\n"
"v_pk_fma_f32 v[86:87],v[86:87],v[58:59],v[54:55] op_sel:[0,1,0] op_sel_hi:[1,1,0]\n"
CV "v116,v80,v81\n"
CV "v117,v82,v83\n"
CV "v118,v84,v85\n"
CV "v119,v86,v87\n"
MF "a[136:139],v[160:163],v[108:111],a[136:139]\n"
"ds_read_b128 a[100:103],v3\n"
AW "a128,v116\n"
AW "a129,v117\n"
AW "a130,v118\n"
AW "a131,v119\n"
AR "v72,a132\n"
AR "v73,a133\n"
AR "v74,a134\n"
MF "a[156:159],v[164:167],v[108:111],a[156:159]\n"
AR "v75,a135\n"
AR "v76,a152\n"
AR "v77,a153\n"
AR "v78,a154\n"
AR "v79,a155\n"
"v_pk_fma_f32 v[72:73],v[72:73],v[60:61],v[54:55] op_sel:[0,0,1] op_sel_hi:[1,0,1]\n"
"v_pk_fma_f32 v[74:75],v[74:75],v[60:61],v[54:55] op_sel:[0,0,1] op_sel_hi:[1,0,1]\n"
"v_pk_fma_f32 v[76:77],v[76:77],v[60:61],v[54:55] op_sel:[0,0,1] op_sel_hi:[1,0,1]\n"
"v_pk_fma_f32 v[78:79],v[78:79],v[60:61],v[54:55] op_sel:[0,0,1] op_sel_hi:[1,0,1]\n"
CV "v112,v72,v73\n"
CV "v113,v74,v75\n"
CV "v114,v76,v77\n"
CV "v115,v78,v79\n"
"s_waitcnt lgkmcnt(8)\n"
MF "a[160:163],v[168:171],v[92:95],a[160:163]\n"
"ds_read_b128 a[64:67],v2 offset:2048\n"
AW "a132,v112\n"
AW "a133,v113\n"
AW "a134,v114\n"
AW "a135,v115\n"
AR "v80,a136\n"
AR "v81,a137\n"
AR "v82,a138\n"
"s_waitcnt lgkmcnt(7)\n"
MF "a[180:183],v[172:175],v[92:95],a[180:183]\n"
AR "v83,a139\n"
AR "v84,a156\n"
AR "v85,a157\n"
AR "v86,a158\n"
AR "v87,a159\n"
"v_pk_fma_f32 v[80:81],v[80:81],v[60:61],v[56:57] op_sel:[0,1,0] op_sel_hi:[1,1,0]\n"
"v_pk_fma_f32 v[82:83],v[82:83],v[60:61],v[56:57] op_sel:[0,1,0] op_sel_hi:[1,1,0]\n"
"v_pk_fma_f32 v[84:85],v[84:85],v[60:61],v[56:57] op_sel:[0,1,0] op_sel_hi:[1,1,0]\n"
"v_pk_fma_f32 v[86:87],v[86:87],v[60:61],v[56:57] op_sel:[0,1,0] op_sel_hi:[1,1,0]\n"
CV "v116,v80,v81\n"
CV "v117,v82,v83\n"
CV "v118,v84,v85\n"
CV "v119,v86,v87\n"
MF "a[164:167],v[168:171],v[96:99],a[164:167]\n"
"ds_read_b128 a[104:107],v3 offset:2048\n"
AW "a136,v116\n"
AW "a137,v117\n"
AW "a138,v118\n"
AW "a139,v119\n"
AR "v72,a160\n"
AR "v73,a161\n"
AR "v74,a162\n"
MF "a[184:187],v[172:175],v[96:99],a[184:187]\n"
AR "v75,a163\n"
AR "v76,a180\n"
AR "v77,a181\n"
AR "v78,a182\n"
AR "v79,a183\n"
"v_pk_fma_f32 v[72:73],v[72:73],v[56:57],v[52:53] op_sel:[0,1,0] op_sel_hi:[1,1,0]\n"
"v_pk_fma_f32 v[74:75],v[74:75],v[56:57],v[52:53] op_sel:[0,1,0] op_sel_hi:[1,1,0]\n"
"v_pk_fma_f32 v[76:77],v[76:77],v[56:57],v[52:53] op_sel:[0,1,0] op_sel_hi:[1,1,0]\n"
"v_pk_fma_f32 v[78:79],v[78:79],v[56:57],v[52:53] op_sel:[0,1,0] op_sel_hi:[1,1,0]\n"
CV "v112,v72,v73\n"
CV "v113,v74,v75\n"
CV "v114,v76,v77\n"
CV "v115,v78,v79\n"
MF "a[168:171],v[168:171],v[100:103],a[168:171]\n"
"ds_read_b128 a[68:71],v2 offset:4096\n"
AW "a160,v112\n"
AW "a161,v113\n"
AW "a162,v114\n"
AW "a163,v115\n"
AR "v80,a164\n"
AR "v81,a165\n"
AR "v82,a166\n"
MF "a[188:191],v[172:175],v[100:103],a[188:191]\n"
AR "v83,a167\n"
AR "v84,a184\n"
AR "v85,a185\n"
AR "v86,a186\n"
AR "v87,a187\n"
"v_pk_fma_f32 v[80:81],v[80:81],v[58:59],v[52:53] op_sel:[0,0,1] op_sel_hi:[1,0,1]\n"
"v_pk_fma_f32 v[82:83],v[82:83],v[58:59],v[52:53] op_sel:[0,0,1] op_sel_hi:[1,0,1]\n"
"v_pk_fma_f32 v[84:85],v[84:85],v[58:59],v[52:53] op_sel:[0,0,1] op_sel_hi:[1,0,1]\n"
"v_pk_fma_f32 v[86:87],v[86:87],v[58:59],v[52:53] op_sel:[0,0,1] op_sel_hi:[1,0,1]\n"
CV "v116,v80,v81\n"
CV "v117,v82,v83\n"
CV "v118,v84,v85\n"
CV "v119,v86,v87\n"
MF "a[172:175],v[168:171],v[104:107],a[172:175]\n"
"ds_read_b128 a[108:111],v3 offset:4096\n"
AW "a164,v116\n"
AW "a165,v117\n"
AW "a166,v118\n"
AW "a167,v119\n"
AR "v72,a168\n"
AR "v73,a169\n"
AR "v74,a170\n"
MF "a[192:195],v[172:175],v[104:107],a[192:195]\n"
AR "v75,a171\n"
AR "v76,a188\n"
AR "v77,a189\n"
AR "v78,a190\n"
AR "v79,a191\n"
"v_pk_fma_f32 v[72:73],v[72:73],v[58:59],v[54:55] op_sel:[0,1,0] op_sel_hi:[1,1,0]\n"
"v_pk_fma_f32 v[74:75],v[74:75],v[58:59],v[54:55] op_sel:[0,1,0] op_sel_hi:[1,1,0]\n"
"v_pk_fma_f32 v[76:77],v[76:77],v[58:59],v[54:55] op_sel:[0,1,0] op_sel_hi:[1,1,0]\n"
"v_pk_fma_f32 v[78:79],v[78:79],v[58:59],v[54:55] op_sel:[0,1,0] op_sel_hi:[1,1,0]\n"
CV "v112,v72,v73\n"
CV "v113,v74,v75\n"
CV "v114,v76,v77\n"
CV "v115,v78,v79\n"
MF "a[176:179],v[168:171],v[108:111],a[176:179]\n"
"ds_read_b128 a[72:75],v2 offset:6144\n"
AW "a168,v112\n"
AW "a169,v113\n"
AW "a170,v114\n"
AW "a171,v115\n"
AR "v80,a172\n"
AR "v81,a173\n"
AR "v82,a174\n"
MF "a[196:199],v[172:175],v[108:111],a[196:199]\n"
AR "v83,a175\n"
AR "v84,a192\n"
AR "v85,a193\n"
AR "v86,a194\n"
AR "v87,a195\n"
"v_pk_fma_f32 v[80:81],v[80:81],v[60:61],v[54:55] op_sel:[0,0,1] op_sel_hi:[1,0,1]\n"
"v_pk_fma_f32 v[82:83],v[82:83],v[60:61],v[54:55] op_sel:[0,0,1] op_sel_hi:[1,0,1]\n"
"v_pk_fma_f32 v[84:85],v[84:85],v[60:61],v[54:55] op_sel:[0,0,1] op_sel_hi:[1,0,1]\n"
"v_pk_fma_f32 v[86:87],v[86:87],v[60:61],v[54:55] op_sel:[0,0,1] op_sel_hi:[1,0,1]\n"
CV "v116,v80,v81\n"
CV "v117,v82,v83\n"
CV "v118,v84,v85\n"
CV "v119,v86,v87\n"
"s_waitcnt lgkmcnt(9)\n"
MF "a[200:203],v[176:179],v[92:95],a[200:203]\n"
"ds_read_b128 a[112:115],v3 offset:6144\n"
AW "a172,v116\n"
AW "a173,v117\n"
AW "a174,v118\n"
AW "a175,v119\n"
AR "v72,a176\n"
AR "v73,a177\n"
AR "v74,a178\n"
"s_waitcnt lgkmcnt(8)\n"
MF "a[220:223],v[180:183],v[92:95],a[220:223]\n"
AR "v75,a179\n"
AR "v76,a196\n"
AR "v77,a197\n"
AR "v78,a198\n"
AR "v79,a199\n"
"v_pk_fma_f32 v[72:73],v[72:73],v[60:61],v[56:57] op_sel:[0,1,0] op_sel_hi:[1,1,0]\n"
"v_pk_fma_f32 v[74:75],v[74:75],v[60:61],v[56:57] op_sel:[0,1,0] op_sel_hi:[1,1,0]\n"
"v_pk_fma_f32 v[76:77],v[76:77],v[60:61],v[56:57] op_sel:[0,1,0] op_sel_hi:[1,1,0]\n"
"v_pk_fma_f32 v[78:79],v[78:79],v[60:61],v[56:57] op_sel:[0,1,0] op_sel_hi:[1,1,0]\n"
CV "v112,v72,v73\n"
CV "v113,v74,v75\n"
CV "v114,v76,v77\n"
CV "v115,v78,v79\n"
MF "a[204:207],v[176:179],v[96:99],a[204:207]\n"
"ds_read_b128 a[76:79],v2 offset:8192\n"
AW "a176,v112\n"
AW "a177,v113\n"
AW "a178,v114\n"
AW "a179,v115\n"
AR "v80,a200\n"
AR "v81,a201\n"
AR "v82,a202\n"
MF "a[224:227],v[180:183],v[96:99],a[224:227]\n"
AR "v83,a203\n"
AR "v84,a220\n"
AR "v85,a221\n"
AR "v86,a222\n"
AR "v87,a223\n"
"v_pk_fma_f32 v[80:81],v[80:81],v[56:57],v[52:53] op_sel:[0,1,0] op_sel_hi:[1,1,0]\n"
"v_pk_fma_f32 v[82:83],v[82:83],v[56:57],v[52:53] op_sel:[0,1,0] op_sel_hi:[1,1,0]\n"
"v_pk_fma_f32 v[84:85],v[84:85],v[56:57],v[52:53] op_sel:[0,1,0] op_sel_hi:[1,1,0]\n"
"v_pk_fma_f32 v[86:87],v[86:87],v[56:57],v[52:53] op_sel:[0,1,0] op_sel_hi:[1,1,0]\n"
CV "v116,v80,v81\n"
CV "v117,v82,v83\n"
CV "v118,v84,v85\n"
CV "v119,v86,v87\n"
MF "a[208:211],v[176:179],v[100:103],a[208:211]\n"
"ds_read_b128 a[116:119],v3 offset:8192\n"
AW "a20,v116\n"
AW "a21,v117\n"
AW "a22,v118\n"
AW "a23,v119\n"
AR "v72,a204\n"
AR "v73,a205\n"
AR "v74,a206\n"
MF "a[228:231],v[180:183],v[100:103],a[228:231]\n"
AR "v75,a207\n"
AR "v76,a224\n"
AR "v77,a225\n"
AR "v78,a226\n"
AR "v79,a227\n"
"v_pk_fma_f32 v[72:73],v[72:73],v[58:59],v[52:53] op_sel:[0,0,1] op_sel_hi:[1,0,1]\n"
"v_pk_fma_f32 v[74:75],v[74:75],v[58:59],v[52:53] op_sel:[0,0,1] op_sel_hi:[1,0,1]\n"
"v_pk_fma_f32 v[76:77],v[76:77],v[58:59],v[52:53] op_sel:[0,0,1] op_sel_hi:[1,0,1]\n"
"v_pk_fma_f32 v[78:79],v[78:79],v[58:59],v[52:53] op_sel:[0,0,1] op_sel_hi:[1,0,1]\n"
CV "v112,v72,v73\n"
CV "v113,v74,v75\n"
CV "v114,v76,v77\n"
CV "v115,v78,v79\n"
MF "a[212:215],v[176:179],v[104:107],a[212:215]\n"
AW "a24,v112\n"
AW "a25,v113\n"
AW "a26,v114\n"
AW "a27,v115\n"
AR "v80,a208\n"
AR "v81,a209\n"
AR "v82,a210\n"
MF "a[232:235],v[180:183],v[104:107],a[232:235]\n"
AR "v83,a211\n"
AR "v84,a228\n"
AR "v85,a229\n"
AR "v86,a230\n"
AR "v87,a231\n"
"v_pk_fma_f32 v[80:81],v[80:81],v[58:59],v[54:55] op_sel:[0,1,0] op_sel_hi:[1,1,0]\n"
"v_pk_fma_f32 v[82:83],v[82:83],v[58:59],v[54:55] op_sel:[0,1,0] op_sel_hi:[1,1,0]\n"
"v_pk_fma_f32 v[84:85],v[84:85],v[58:59],v[54:55] op_sel:[0,1,0] op_sel_hi:[1,1,0]\n"
"v_pk_fma_f32 v[86:87],v[86:87],v[58:59],v[54:55] op_sel:[0,1,0] op_sel_hi:[1,1,0]\n"
CV "v116,v80,v81\n"
CV "v117,v82,v83\n"
CV "v118,v84,v85\n"
CV "v119,v86,v87\n"
MF "a[216:219],v[176:179],v[108:111],a[216:219]\n"
"s_waitcnt vmcnt(14) lgkmcnt(0)\n"
"s_barrier\n"
TR "a[200:201],v0\n"
TR "a[202:203],v0 offset:512\n"
TR "a[204:205],v0 offset:4096\n"
TR "a[206:207],v0 offset:4608\n"
AW "a28,v116\n"
AW "a29,v117\n"
AW "a30,v118\n"
AW "a31,v119\n"
AR "v72,a212\n"
AR "v73,a213\n"
AR "v74,a214\n"
MF "a[236:239],v[180:183],v[108:111],a[236:239]\n"
AR "v75,a215\n"
AR "v76,a232\n"
AR "v77,a233\n"
AR "v78,a234\n"
AR "v79,a235\n"
"v_pk_fma_f32 v[72:73],v[72:73],v[60:61],v[54:55] op_sel:[0,0,1] op_sel_hi:[1,0,1]\n"
"v_pk_fma_f32 v[74:75],v[74:75],v[60:61],v[54:55] op_sel:[0,0,1] op_sel_hi:[1,0,1]\n"
"v_pk_fma_f32 v[76:77],v[76:77],v[60:61],v[54:55] op_sel:[0,0,1] op_sel_hi:[1,0,1]\n"
"v_pk_fma_f32 v[78:79],v[78:79],v[60:61],v[54:55] op_sel:[0,0,1] op_sel_hi:[1,0,1]\n"
CV "v112,v72,v73\n"
CV "v113,v74,v75\n"
CV "v114,v76,v77\n"
CV "v115,v78,v79\n"
TR "a[208:209],v0 offset:8\n"
TR "a[210:211],v0 offset:520\n"
TR "a[212:213],v0 offset:4104\n"
TR "a[214:215],v0 offset:4616\n"
AW "a32,v112\n"
AW "a33,v113\n"
AW "a34,v114\n"
AW "a35,v115\n"
AR "v80,a216\n"
AR "v81,a217\n"
AR "v82,a218\n"
AR "v83,a219\n"
AR "v84,a236\n"
AR "v85,a237\n"
AR "v86,a238\n"
AR "v87,a239\n"
"v_pk_fma_f32 v[80:81],v[80:81],v[60:61],v[56:57] op_sel:[0,1,0] op_sel_hi:[1,1,0]\n"
"v_pk_fma_f32 v[82:83],v[82:83],v[60:61],v[56:57] op_sel:[0,1,0] op_sel_hi:[1,1,0]\n"
"v_pk_fma_f32 v[84:85],v[84:85],v[60:61],v[56:57] op_sel:[0,1,0] op_sel_hi:[1,1,0]\n"
"v_pk_fma_f32 v[86:87],v[86:87],v[60:61],v[56:57] op_sel:[0,1,0] op_sel_hi:[1,1,0]\n"
CV "v116,v80,v81\n"
CV "v117,v82,v83\n"
CV "v118,v84,v85\n"
CV "v119,v86,v87\n"
AW "a36,v116\n"
AW "a37,v117\n"
AW "a38,v118\n"
AW "a39,v119\n"
"s_mov_b32 s13,s11\n"
"s_mov_b32 s70,s71\n"
"s_mov_b32 s57,1\n"
"s_waitcnt lgkmcnt(0)\n"
"s_branch .Lq0\n"
".Lpgqy_c:\n"
"s_cbranch_execz .Lpgqj_a\n"
".Lqy:\n"
MF "v[216:219],a[216:219],a[60:63],v[216:219]\n"
TR "a[208:209],v0 offset:8\n"
MF "v[216:219],a[220:223],a[100:103],v[216:219]\n"
TR "a[210:211],v0 offset:520\n"
MF "v[224:227],a[220:223],a[104:107],v[224:227]\n"
MF "v[224:227],a[216:219],a[64:67],v[224:227]\n"
TR "a[212:213],v0 offset:4104\n"
MF "v[232:235],a[216:219],a[68:71],v[232:235]\n"
TR "a[214:215],v0 offset:4616\n"
MF "v[232:235],a[220:223],a[108:111],v[232:235]\n"
MF "v[240:243],a[220:223],a[112:115],v[240:243]\n"
MF "v[240:243],a[216:219],a[72:75],v[240:243]\n"
MF "v[248:251],a[216:219],a[76:79],v[248:251]\n"
MF "v[248:251],a[220:223],a[116:119],v[248:251]\n"
MF "v[248:251],a[200:203],a[156:159],v[248:251]\n"
TR "a[216:217],v0 offset:16392\n"
MF "v[248:251],a[204:207],a[196:199],v[248:251]\n"
TR "a[218:219],v0 offset:16904\n"
MF "v[240:243],a[204:207],a[192:195],v[240:243]\n"
MF "v[240:243],a[200:203],a[152:155],v[240:243]\n"
TR "a[220:221],v0 offset:20488\n"
MF "v[232:235],a[200:203],a[148:151],v[232:235]\n"
TR "a[222:223],v0 offset:21000\n"
MF "v[232:235],a[204:207],a[188:191],v[232:235]\n"
MF "v[224:227],a[204:207],a[184:187],v[224:227]\n"
MF "v[224:227],a[200:203],a[144:147],v[224:227]\n"
"v_fma_f32 v248,v248,s54,v14\n"
"v_fma_f32 v249,v249,s54,v14\n"
MF "v[216:219],a[200:203],a[140:143],v[216:219]\n"
"v_fma_f32 v250,v250,s54,v14\n"
"v_fma_f32 v251,v251,s54,v14\n"
MF "v[216:219],a[204:207],a[180:183],v[216:219]\n"
"s_waitcnt lgkmcnt(4)\n"
"v_fma_f32 v252,v252,s54,v14\n"
"v_fma_f32 v253,v253,s54,v14\n"
MF "v[180:183],a[208:211],a[60:63],v[180:183]\n"
TR "a[200:201],v0\n"
"v_fma_f32 v254,v254,s54,v14\n"
MF "v[180:183],a[212:215],a[100:103],v[180:183]\n"
TR "a[202:203],v0 offset:512\n"
"v_fma_f32 v255,v255,s54,v14\n"
MF "v[188:191],a[212:215],a[104:107],v[188:191]\n"
CV "v255,v254,v255\n"
CV "v254,v252,v253\n"
MF "v[188:191],a[208:211],a[64:67],v[188:191]\n"
TR "a[204:205],v0 offset:4096\n"
CV "v253,v250,v251\n"
MF "v[196:199],a[208:211],a[68:71],v[196:199]\n"
TR "a[206:207],v0 offset:4608\n"
CV "v252,v248,v249\n"
MF "v[196:199],a[212:215],a[108:111],v[196:199]\n"
"s_add_u32 s58,s75,4096\n"
"v_fma_f32 v240,v240,s54,v14\n"
"v_fma_f32 v241,v241,s54,v14\n"
MF "v[204:207],a[212:215],a[112:115],v[204:207]\n"
"buffer_store_dwordx4 v[252:255],v9,s[40:43],s58 offen offset:1024 sc1\n"
MF "v[204:207],a[208:211],a[72:75],v[204:207]\n"
"v_fma_f32 v242,v242,s54,v14\n"
"v_fma_f32 v243,v243,s54,v14\n"
MF "v[212:215],a[208:211],a[76:79],v[212:215]\n"
"v_fma_f32 v244,v244,s54,v14\n"
"v_fma_f32 v245,v245,s54,v14\n"
MF "v[212:215],a[212:215],a[116:119],v[212:215]\n"
"s_waitcnt lgkmcnt(4)\n"
"v_fma_f32 v246,v246,s54,v14\n"
"v_fma_f32 v247,v247,s54,v14\n"
MF "v[180:183],a[216:219],a[140:143],v[180:183]\n"
TR "a[208:209],v0 offset:16384\n"
CV "v251,v246,v247\n"
MF "v[180:183],a[220:223],a[180:183],v[180:183]\n"
TR "a[210:211],v0 offset:16896\n"
CV "v250,v244,v245\n"
MF "v[188:191],a[220:223],a[184:187],v[188:191]\n"
CV "v249,v242,v243\n"
CV "v248,v240,v241\n"
MF "v[188:191],a[216:219],a[144:147],v[188:191]\n"
TR "a[212:213],v0 offset:20480\n"
"v_fma_f32 v232,v232,s54,v14\n"
MF "v[196:199],a[216:219],a[148:151],v[196:199]\n"
TR "a[214:215],v0 offset:20992\n"
"v_fma_f32 v233,v233,s54,v14\n"
MF "v[196:199],a[220:223],a[188:191],v[196:199]\n"
"buffer_store_dwordx4 v[248:251],v9,s[40:43],s58 offen sc1\n"
MF "v[204:207],a[220:223],a[192:195],v[204:207]\n"
"v_fma_f32 v234,v234,s54,v14\n"
"v_fma_f32 v235,v235,s54,v14\n"
MF "v[204:207],a[216:219],a[152:155],v[204:207]\n"
"v_fma_f32 v236,v236,s54,v14\n"
"v_fma_f32 v237,v237,s54,v14\n"
MF "v[212:215],a[216:219],a[156:159],v[212:215]\n"
"s_waitcnt vmcnt(10) lgkmcnt(0)\n"
"v_fma_f32 v238,v238,s54,v14\n"
"v_fma_f32 v239,v239,s54,v14\n"
MF "v[212:215],a[220:223],a[196:199],v[212:215]\n"
"s_barrier\n"
MF "v[176:179],a[200:203],a[60:63],v[176:179]\n"
TR "a[216:217],v1 offset:32776\n"
CV "v247,v238,v239\n"
MF "v[176:179],a[204:207],a[100:103],v[176:179]\n"
TR "a[218:219],v1 offset:33288\n"
CV "v246,v236,v237\n"
MF "v[184:187],a[204:207],a[104:107],v[184:187]\n"
"s_add_u32 s66,s67,4718592\n"
"s_sub_u32 s81,s81,384\n"
"s_mov_b32 s83,7077888\n"
MF "v[184:187],a[200:203],a[64:67],v[184:187]\n"
"s_add_u32 m0,s62,0\n"
"s_add_u32 s82,s66,2359296\n"
TR "a[220:221],v1 offset:36872\n"
MF "v[192:195],a[200:203],a[68:71],v[192:195]\n"
"s_sub_u32 s15,s82,7864320\n"
"s_min_u32 s82,s82,s15\n"
TR "a[222:223],v1 offset:37384\n"
MF "v[192:195],a[204:207],a[108:111],v[192:195]\n"
LD "v6,s[28:31],s82 offen lds\n"
MF "v[200:203],a[204:207],a[112:115],v[200:203]\n"
CV "v245,v234,v235\n"
CV "v244,v232,v233\n"
MF "v[200:203],a[200:203],a[72:75],v[200:203]\n"
"v_fma_f32 v224,v224,s54,v14\n"
"v_fma_f32 v225,v225,s54,v14\n"
MF "v[208:211],a[200:203],a[76:79],v[208:211]\n"
"buffer_store_dwordx4 v[244:247],v9,s[40:43],s75 offen offset:3072 sc1\n"
MF "v[208:211],a[204:207],a[116:119],v[208:211]\n"
LD "v11,s[28:31],s82 offen offset:1024 lds\n"
MF "v[208:211],a[208:211],a[156:159],v[208:211]\n"
TR "a[200:201],v1 offset:49160\n"
"v_fma_f32 v226,v226,s54,v14\n"
MF "v[208:211],a[212:215],a[196:199],v[208:211]\n"
TR "a[202:203],v1 offset:49672\n"
"v_fma_f32 v227,v227,s54,v14\n"
MF "v[200:203],a[212:215],a[192:195],v[200:203]\n"
"v_fma_f32 v228,v228,s54,v14\n"
"v_fma_f32 v229,v229,s54,v14\n"
MF "v[200:203],a[208:211],a[152:155],v[200:203]\n"
TR "a[204:205],v1 offset:53256\n"
"v_fma_f32 v230,v230,s54,v14\n"
MF "v[192:195],a[208:211],a[148:151],v[192:195]\n"
TR "a[206:207],v1 offset:53768\n"
"v_fma_f32 v231,v231,s54,v14\n"
MF "v[192:195],a[212:215],a[188:191],v[192:195]\n"
CV "v243,v230,v231\n"
CV "v242,v228,v229\n"
MF "v[184:187],a[212:215],a[184:187],v[184:187]\n"
CV "v241,v226,v227\n"
CV "v240,v224,v225\n"
MF "v[184:187],a[208:211],a[144:147],v[184:187]\n"
"buffer_store_dwordx4 v[240:243],v9,s[40:43],s75 offen offset:2048 sc1\n"
MF "v[176:179],a[208:211],a[140:143],v[176:179]\n"
LD "v12,s[28:31],s82 offen offset:2048 lds\n"
MF "v[176:179],a[212:215],a[180:183],v[176:179]\n"
"s_waitcnt lgkmcnt(4)\n"
"v_fma_f32 v216,v216,s54,v14\n"
"v_fma_f32 v217,v217,s54,v14\n"
MF "v[140:143],a[216:219],a[60:63],v[140:143]\n"
TR "a[208:209],v1 offset:32768\n"
"v_fma_f32 v218,v218,s54,v14\n"
MF "v[140:143],a[220:223],a[100:103],v[140:143]\n"
TR "a[210:211],v1 offset:33280\n"
"v_fma_f32 v219,v219,s54,v14\n"
MF "v[148:151],a[220:223],a[104:107],v[148:151]\n"
"v_fma_f32 v220,v220,s54,v14\n"
"v_fma_f32 v221,v221,s54,v14\n"
MF "v[148:151],a[216:219],a[64:67],v[148:151]\n"
TR "a[212:213],v1 offset:36864\n"
"v_fma_f32 v222,v222,s54,v14\n"
MF "v[156:159],a[216:219],a[68:71],v[156:159]\n"
TR "a[214:215],v1 offset:37376\n"
"v_fma_f32 v223,v223,s54,v14\n"
MF "v[156:159],a[220:223],a[108:111],v[156:159]\n"
CV "v239,v222,v223\n"
CV "v238,v220,v221\n"
MF "v[164:167],a[220:223],a[112:115],v[164:167]\n"
".Lpg14c:\n"
"s_cbranch_execz .Lpg3a\n"
CV "v237,v218,v219\n"
CV "v236,v216,v217\n"
MF "v[164:167],a[216:219],a[72:75],v[164:167]\n"
LD "v13,s[28:31],s82 offen offset:3072 lds\n"
MF "v[172:175],a[216:219],a[76:79],v[172:175]\n"
"buffer_store_dwordx4 v[236:239],v9,s[40:43],s75 offen offset:1024 sc1\n"
MF "v[172:175],a[220:223],a[116:119],v[172:175]\n"
"s_waitcnt lgkmcnt(4)\n"
"v_fma_f32 v208,v208,s54,v14\n"
"v_fma_f32 v209,v209,s54,v14\n"
MF "v[140:143],a[200:203],a[140:143],v[140:143]\n"
TR "a[216:217],v1 offset:49152\n"
"v_fma_f32 v210,v210,s54,v14\n"
MF "v[140:143],a[204:207],a[180:183],v[140:143]\n"
TR "a[218:219],v1 offset:49664\n"
"v_fma_f32 v211,v211,s54,v14\n"
MF "v[148:151],a[204:207],a[184:187],v[148:151]\n"
"v_fma_f32 v212,v212,s54,v14\n"
"v_fma_f32 v213,v213,s54,v14\n"
MF "v[148:151],a[200:203],a[144:147],v[148:151]\n"
TR "a[220:221],v1 offset:53248\n"
"v_fma_f32 v214,v214,s54,v14\n"
MF "v[156:159],a[200:203],a[148:151],v[156:159]\n"
TR "a[222:223],v1 offset:53760\n"
"v_fma_f32 v215,v215,s54,v14\n"
MF "v[156:159],a[204:207],a[188:191],v[156:159]\n"
CV "v235,v214,v215\n"
CV "v234,v212,v213\n"
MF "v[164:167],a[204:207],a[192:195],v[164:167]\n"
CV "v233,v210,v211\n"
CV "v232,v208,v209\n"
MF "v[164:167],a[200:203],a[152:155],v[164:167]\n"
"v_fma_f32 v200,v200,s54,v14\n"
"v_fma_f32 v201,v201,s54,v14\n"
MF "v[172:175],a[200:203],a[156:159],v[172:175]\n"
"buffer_store_dwordx4 v[232:235],v9,s[40:43],s75 offen sc1\n"
MF "v[172:175],a[204:207],a[196:199],v[172:175]\n"
"s_waitcnt lgkmcnt(0)\n"
"v_fma_f32 v202,v202,s54,v14\n"
"v_fma_f32 v203,v203,s54,v14\n"
MF "v[136:139],a[208:211],a[60:63],v[136:139]\n"
"s_add_u32 m0,s62,16384\n"
"s_add_u32 s82,s66,2359424\n"
TR "a[200:201],v0 offset:32776\n"
MF "v[136:139],a[212:215],a[100:103],v[136:139]\n"
"s_sub_u32 s15,s82,7864320\n"
"s_min_u32 s82,s82,s15\n"
TR "a[202:203],v0 offset:33288\n"
MF "v[144:147],a[212:215],a[104:107],v[144:147]\n"
LD "v6,s[28:31],s82 offen lds\n"
MF "v[144:147],a[208:211],a[64:67],v[144:147]\n"
TR "a[204:205],v0 offset:36872\n"
"v_fma_f32 v204,v204,s54,v14\n"
MF "v[152:155],a[208:211],a[68:71],v[152:155]\n"
TR "a[206:207],v0 offset:37384\n"
"v_fma_f32 v205,v205,s54,v14\n"
MF "v[152:155],a[212:215],a[108:111],v[152:155]\n"
"v_fma_f32 v206,v206,s54,v14\n"
"v_fma_f32 v207,v207,s54,v14\n"
MF "v[160:163],a[212:215],a[112:115],v[160:163]\n"
CV "v231,v206,v207\n"
CV "v230,v204,v205\n"
MF "v[160:163],a[208:211],a[72:75],v[160:163]\n"
CV "v229,v202,v203\n"
CV "v228,v200,v201\n"
MF "v[168:171],a[208:211],a[76:79],v[168:171]\n"
AW "a84,v228\n"
AW "a85,v229\n"
MF "v[168:171],a[212:215],a[116:119],v[168:171]\n"
LD "v11,s[28:31],s82 offen offset:1024 lds\n"
MF "v[168:171],a[216:219],a[156:159],v[168:171]\n"
TR "a[208:209],v0 offset:49160\n"
AW "a86,v230\n"
MF "v[168:171],a[220:223],a[196:199],v[168:171]\n"
TR "a[210:211],v0 offset:49672\n"
AW "a87,v231\n"
MF "v[160:163],a[220:223],a[192:195],v[160:163]\n"
"v_fma_f32 v192,v192,s54,v14\n"
"v_fma_f32 v193,v193,s54,v14\n"
MF "v[160:163],a[216:219],a[152:155],v[160:163]\n"
TR "a[212:213],v0 offset:53256\n"
"v_fma_f32 v194,v194,s54,v14\n"
MF "v[152:155],a[216:219],a[148:151],v[152:155]\n"
TR "a[214:215],v0 offset:53768\n"
"v_fma_f32 v195,v195,s54,v14\n"
MF "v[152:155],a[220:223],a[188:191],v[152:155]\n"
"v_fma_f32 v196,v196,s54,v14\n"
"v_fma_f32 v197,v197,s54,v14\n"
MF "v[144:147],a[220:223],a[184:187],v[144:147]\n"
"v_fma_f32 v198,v198,s54,v14\n"
"v_fma_f32 v199,v199,s54,v14\n"
MF "v[144:147],a[216:219],a[144:147],v[144:147]\n"
CV "v227,v198,v199\n"
CV "v226,v196,v197\n"
MF "v[136:139],a[216:219],a[140:143],v[136:139]\n"
LD "v12,s[28:31],s82 offen offset:2048 lds\n"
MF "v[136:139],a[220:223],a[180:183],v[136:139]\n"
"s_waitcnt lgkmcnt(4)\n"
CV "v225,v194,v195\n"
CV "v224,v192,v193\n"
MF "v[100:103],a[200:203],a[60:63],v[100:103]\n"
TR "a[216:217],v0 offset:32768\n"
AW "a80,v224\n"
MF "v[100:103],a[204:207],a[100:103],v[100:103]\n"
TR "a[218:219],v0 offset:33280\n"
AW "a81,v225\n"
MF "v[108:111],a[204:207],a[104:107],v[108:111]\n"
AW "a82,v226\n"
AW "a83,v227\n"
MF "v[108:111],a[200:203],a[64:67],v[108:111]\n"
TR "a[220:221],v0 offset:36864\n"
"v_fma_f32 v184,v184,s54,v14\n"
MF "v[116:119],a[200:203],a[68:71],v[116:119]\n"
TR "a[222:223],v0 offset:37376\n"
"v_fma_f32 v185,v185,s54,v14\n"
MF "v[116:119],a[204:207],a[108:111],v[116:119]\n"
"v_fma_f32 v186,v186,s54,v14\n"
"v_fma_f32 v187,v187,s54,v14\n"
MF "v[124:127],a[204:207],a[112:115],v[124:127]\n"
"v_fma_f32 v188,v188,s54,v14\n"
"v_fma_f32 v189,v189,s54,v14\n"
MF "v[124:127],a[200:203],a[72:75],v[124:127]\n"
LD "v13,s[28:31],s82 offen offset:3072 lds\n"
MF "v[132:135],a[200:203],a[76:79],v[132:135]\n"
"v_fma_f32 v190,v190,s54,v14\n"
"v_fma_f32 v191,v191,s54,v14\n"
MF "v[132:135],a[204:207],a[116:119],v[132:135]\n"
"s_waitcnt lgkmcnt(4)\n"
CV "v223,v190,v191\n"
CV "v222,v188,v189\n"
MF "v[100:103],a[208:211],a[140:143],v[100:103]\n"
TR "a[200:201],v0 offset:49152\n"
CV "v221,v186,v187\n"
MF "v[100:103],a[212:215],a[180:183],v[100:103]\n"
TR "a[202:203],v0 offset:49664\n"
CV "v220,v184,v185\n"
MF "v[108:111],a[212:215],a[184:187],v[108:111]\n"
AW "a56,v220\n"
AW "a57,v221\n"
MF "v[108:111],a[208:211],a[144:147],v[108:111]\n"
TR "a[204:205],v0 offset:53248\n"
AW "a58,v222\n"
MF "v[116:119],a[208:211],a[148:151],v[116:119]\n"
TR "a[206:207],v0 offset:53760\n"
AW "a59,v223\n"
MF "v[116:119],a[212:215],a[188:191],v[116:119]\n"
"v_fma_f32 v176,v176,s54,v14\n"
"v_fma_f32 v177,v177,s54,v14\n"
MF "v[124:127],a[212:215],a[192:195],v[124:127]\n"
"v_add_u32 v0,0xc000,v0\n"
"v_fma_f32 v178,v178,s54,v14\n"
MF "v[124:127],a[208:211],a[152:155],v[124:127]\n"
"v_add_u32 v1,0xc000,v1\n"
"v_fma_f32 v179,v179,s54,v14\n"
MF "v[132:135],a[208:211],a[156:159],v[132:135]\n"
"s_waitcnt vmcnt(14) lgkmcnt(0)\n"
"v_fma_f32 v180,v180,s54,v14\n"
"v_fma_f32 v181,v181,s54,v14\n"
MF "v[132:135],a[212:215],a[196:199],v[132:135]\n"
"s_barrier\n"
MF "v[96:99],a[216:219],a[60:63],v[96:99]\n"
"s_add_u32 m0,s62,32768\n"
"s_add_u32 s82,s66,2359552\n"
TR "a[208:209],v1 offset:16392\n"
MF "v[96:99],a[220:223],a[100:103],v[96:99]\n"
"s_sub_u32 s15,s82,7864320\n"
"s_min_u32 s82,s82,s15\n"
TR "a[210:211],v1 offset:16904\n"
MF "v[104:107],a[220:223],a[104:107],v[104:107]\n"
LD "v6,s[28:31],s82 offen lds\n"
MF "v[104:107],a[216:219],a[64:67],v[104:107]\n"
TR "a[212:213],v1 offset:20488\n"
"v_fma_f32 v182,v182,s54,v14\n"
MF "v[112:115],a[216:219],a[68:71],v[112:115]\n"
TR "a[214:215],v1 offset:21000\n"
"v_fma_f32 v183,v183,s54,v14\n"
MF "v[112:115],a[220:223],a[108:111],v[112:115]\n"
CV "v219,v182,v183\n"
CV "v218,v180,v181\n"
MF "v[120:123],a[220:223],a[112:115],v[120:123]\n"
CV "v217,v178,v179\n"
CV "v216,v176,v177\n"
MF "v[120:123],a[216:219],a[72:75],v[120:123]\n"
AW "a52,v216\n"
AW "a53,v217\n"
MF "v[128:131],a[216:219],a[76:79],v[128:131]\n"
AW "a54,v218\n"
AW "a55,v219\n"
MF "v[128:131],a[220:223],a[116:119],v[128:131]\n"
LD "v11,s[28:31],s82 offen offset:1024 lds\n"
MF "v[128:131],a[200:203],a[156:159],v[128:131]\n"
TR "a[216:217],v1 offset:32776\n"
"v_fma_f32 v168,v168,s54,v14\n"
MF "v[128:131],a[204:207],a[196:199],v[128:131]\n"
TR "a[218:219],v1 offset:33288\n"
"v_fma_f32 v169,v169,s54,v14\n"
MF "v[120:123],a[204:207],a[192:195],v[120:123]\n"
"v_fma_f32 v170,v170,s54,v14\n"
"v_fma_f32 v171,v171,s54,v14\n"
MF "v[120:123],a[200:203],a[152:155],v[120:123]\n"
TR "a[220:221],v1 offset:36872\n"
"v_fma_f32 v172,v172,s54,v14\n"
MF "v[112:115],a[200:203],a[148:151],v[112:115]\n"
TR "a[222:223],v1 offset:37384\n"
"v_fma_f32 v173,v173,s54,v14\n"
MF "v[112:115],a[204:207],a[188:191],v[112:115]\n"
"v_fma_f32 v174,v174,s54,v14\n"
"v_fma_f32 v175,v175,s54,v14\n"
MF "v[104:107],a[204:207],a[184:187],v[104:107]\n"
CV "v215,v174,v175\n"
CV "v214,v172,v173\n"
MF "v[104:107],a[200:203],a[144:147],v[104:107]\n"
CV "v213,v170,v171\n"
CV "v212,v168,v169\n"
MF "v[96:99],a[200:203],a[140:143],v[96:99]\n"
LD "v12,s[28:31],s82 offen offset:2048 lds\n"
MF "v[96:99],a[204:207],a[180:183],v[96:99]\n"
"s_waitcnt lgkmcnt(4)\n"
AW "a48,v212\n"
AW "a49,v213\n"
MF "v[60:63],a[208:211],a[60:63],v[60:63]\n"
TR "a[200:201],v1 offset:16384\n"
AW "a50,v214\n"
MF "v[60:63],a[212:215],a[100:103],v[60:63]\n"
TR "a[202:203],v1 offset:16896\n"
AW "a51,v215\n"
MF "v[68:71],a[212:215],a[104:107],v[68:71]\n"
"v_fma_f32 v160,v160,s54,v14\n"
"v_fma_f32 v161,v161,s54,v14\n"
MF "v[68:71],a[208:211],a[64:67],v[68:71]\n"
TR "a[204:205],v1 offset:20480\n"
"v_fma_f32 v162,v162,s54,v14\n"
MF "v[76:79],a[208:211],a[68:71],v[76:79]\n"
TR "a[206:207],v1 offset:20992\n"
"v_fma_f32 v163,v163,s54,v14\n"
MF "v[76:79],a[212:215],a[108:111],v[76:79]\n"
"v_fma_f32 v164,v164,s54,v14\n"
"v_fma_f32 v165,v165,s54,v14\n"
MF "v[84:87],a[212:215],a[112:115],v[84:87]\n"
"v_fma_f32 v166,v166,s54,v14\n"
"v_fma_f32 v167,v167,s54,v14\n"
MF "v[84:87],a[208:211],a[72:75],v[84:87]\n"
LD "v13,s[28:31],s82 offen offset:3072 lds\n"
MF "v[92:95],a[208:211],a[76:79],v[92:95]\n"
CV "v211,v166,v167\n"
CV "v210,v164,v165\n"
MF "v[92:95],a[212:215],a[116:119],v[92:95]\n"
"s_waitcnt lgkmcnt(4)\n"
CV "v209,v162,v163\n"
CV "v208,v160,v161\n"
MF "v[60:63],a[216:219],a[140:143],v[60:63]\n"
TR "a[208:209],v1 offset:32768\n"
AW "a44,v208\n"
MF "v[60:63],a[220:223],a[180:183],v[60:63]\n"
TR "a[210:211],v1 offset:33280\n"
AW "a45,v209\n"
MF "v[68:71],a[220:223],a[184:187],v[68:71]\n"
AW "a46,v210\n"
AW "a47,v211\n"
MF "v[68:71],a[216:219],a[144:147],v[68:71]\n"
TR "a[212:213],v1 offset:36864\n"
"v_fma_f32 v152,v152,s54,v14\n"
MF "v[76:79],a[216:219],a[148:151],v[76:79]\n"
TR "a[214:215],v1 offset:37376\n"
"v_fma_f32 v153,v153,s54,v14\n"
MF "v[76:79],a[220:223],a[188:191],v[76:79]\n"
"v_fma_f32 v154,v154,s54,v14\n"
"v_fma_f32 v155,v155,s54,v14\n"
MF "v[84:87],a[220:223],a[192:195],v[84:87]\n"
"v_fma_f32 v156,v156,s54,v14\n"
"v_fma_f32 v157,v157,s54,v14\n"
MF "v[84:87],a[216:219],a[152:155],v[84:87]\n"
"v_fma_f32 v158,v158,s54,v14\n"
"v_fma_f32 v159,v159,s54,v14\n"
MF "v[92:95],a[216:219],a[156:159],v[92:95]\n"
CV "v207,v158,v159\n"
CV "v206,v156,v157\n"
MF "v[92:95],a[220:223],a[196:199],v[92:95]\n"
"s_waitcnt lgkmcnt(0)\n"
CV "v205,v154,v155\n"
CV "v204,v152,v153\n"
MF "v[56:59],a[200:203],a[60:63],v[56:59]\n"
TR "a[216:217],v0 offset:16392\n"
AW "a40,v204\n"
MF "v[56:59],a[204:207],a[100:103],v[56:59]\n"
TR "a[218:219],v0 offset:16904\n"
AW "a41,v205\n"
MF "v[64:67],a[204:207],a[104:107],v[64:67]\n"
AW "a42,v206\n"
AW "a43,v207\n"
MF "v[64:67],a[200:203],a[64:67],v[64:67]\n"
TR "a[220:221],v0 offset:20488\n"
"v_fma_f32 v144,v144,s54,v14\n"
MF "v[72:75],a[200:203],a[68:71],v[72:75]\n"
TR "a[222:223],v0 offset:21000\n"
"v_fma_f32 v145,v145,s54,v14\n"
MF "v[72:75],a[204:207],a[108:111],v[72:75]\n"
"v_fma_f32 v146,v146,s54,v14\n"
"v_fma_f32 v147,v147,s54,v14\n"
MF "v[80:83],a[204:207],a[112:115],v[80:83]\n"
"v_fma_f32 v148,v148,s54,v14\n"
"v_fma_f32 v149,v149,s54,v14\n"
MF "v[80:83],a[200:203],a[72:75],v[80:83]\n"
"v_fma_f32 v150,v150,s54,v14\n"
"v_fma_f32 v151,v151,s54,v14\n"
MF "v[88:91],a[200:203],a[76:79],v[88:91]\n"
CV "v203,v150,v151\n"
CV "v202,v148,v149\n"
MF "v[88:91],a[204:207],a[116:119],v[88:91]\n"
CV "v201,v146,v147\n"
CV "v200,v144,v145\n"
MF "v[88:91],a[208:211],a[156:159],v[88:91]\n"
TR "a[200:201],v0 offset:32776\n"
AW "a16,v200\n"
MF "v[88:91],a[212:215],a[196:199],v[88:91]\n"
TR "a[202:203],v0 offset:33288\n"
AW "a17,v201\n"
MF "v[80:83],a[212:215],a[192:195],v[80:83]\n"
AW "a18,v202\n"
AW "a19,v203\n"
MF "v[80:83],a[208:211],a[152:155],v[80:83]\n"
TR "a[204:205],v0 offset:36872\n"
"v_fma_f32 v136,v136,s54,v14\n"
MF "v[72:75],a[208:211],a[148:151],v[72:75]\n"
TR "a[206:207],v0 offset:37384\n"
"v_fma_f32 v137,v137,s54,v14\n"
MF "v[72:75],a[212:215],a[188:191],v[72:75]\n"
"v_fma_f32 v138,v138,s54,v14\n"
"v_fma_f32 v139,v139,s54,v14\n"
MF "v[64:67],a[212:215],a[184:187],v[64:67]\n"
"v_fma_f32 v140,v140,s54,v14\n"
"v_fma_f32 v141,v141,s54,v14\n"
MF "v[64:67],a[208:211],a[144:147],v[64:67]\n"
"v_fma_f32 v142,v142,s54,v14\n"
"v_fma_f32 v143,v143,s54,v14\n"
MF "v[56:59],a[208:211],a[140:143],v[56:59]\n"
CV "v199,v142,v143\n"
CV "v198,v140,v141\n"
MF "v[56:59],a[212:215],a[180:183],v[56:59]\n"
"s_waitcnt lgkmcnt(4)\n"
CV "v197,v138,v139\n"
CV "v196,v136,v137\n"
MF "v[20:23],a[216:219],a[60:63],v[20:23]\n"
TR "a[208:209],v0 offset:16384\n"
AW "a12,v196\n"
MF "v[20:23],a[220:223],a[100:103],v[20:23]\n"
TR "a[210:211],v0 offset:16896\n"
AW "a13,v197\n"
MF "v[28:31],a[220:223],a[104:107],v[28:31]\n"
AW "a14,v198\n"
AW "a15,v199\n"
MF "v[28:31],a[216:219],a[64:67],v[28:31]\n"
TR "a[212:213],v0 offset:20480\n"
"v_fma_f32 v128,v128,s54,v14\n"
MF "v[36:39],a[216:219],a[68:71],v[36:39]\n"
TR "a[214:215],v0 offset:20992\n"
"v_fma_f32 v129,v129,s54,v14\n"
MF "v[36:39],a[220:223],a[108:111],v[36:39]\n"
"v_fma_f32 v130,v130,s54,v14\n"
"v_fma_f32 v131,v131,s54,v14\n"
MF "v[44:47],a[220:223],a[112:115],v[44:47]\n"
"v_fma_f32 v132,v132,s54,v14\n"
"v_fma_f32 v133,v133,s54,v14\n"
MF "v[44:47],a[216:219],a[72:75],v[44:47]\n"
"v_fma_f32 v134,v134,s54,v14\n"
"v_fma_f32 v135,v135,s54,v14\n"
MF "v[52:55],a[216:219],a[76:79],v[52:55]\n"
CV "v195,v134,v135\n"
CV "v194,v132,v133\n"
MF "v[52:55],a[220:223],a[116:119],v[52:55]\n"
"s_waitcnt lgkmcnt(4)\n"
CV "v193,v130,v131\n"
CV "v192,v128,v129\n"
MF "v[20:23],a[200:203],a[140:143],v[20:23]\n"
TR "a[216:217],v0 offset:32768\n"
AW "a8,v192\n"
MF "v[20:23],a[204:207],a[180:183],v[20:23]\n"
TR "a[218:219],v0 offset:33280\n"
AW "a9,v193\n"
MF "v[28:31],a[204:207],a[184:187],v[28:31]\n"
AW "a10,v194\n"
AW "a11,v195\n"
MF "v[28:31],a[200:203],a[144:147],v[28:31]\n"
TR "a[220:221],v0 offset:36864\n"
"v_fma_f32 v120,v120,s54,v14\n"
MF "v[36:39],a[200:203],a[148:151],v[36:39]\n"
TR "a[222:223],v0 offset:37376\n"
"v_fma_f32 v121,v121,s54,v14\n"
MF "v[36:39],a[204:207],a[188:191],v[36:39]\n"
"v_fma_f32 v122,v122,s54,v14\n"
"v_fma_f32 v123,v123,s54,v14\n"
MF "v[44:47],a[204:207],a[192:195],v[44:47]\n"
"v_fma_f32 v124,v124,s54,v14\n"
"v_fma_f32 v125,v125,s54,v14\n"
MF "v[44:47],a[200:203],a[152:155],v[44:47]\n"
"v_fma_f32 v126,v126,s54,v14\n"
"v_fma_f32 v127,v127,s54,v14\n"
MF "v[52:55],a[200:203],a[156:159],v[52:55]\n"
"s_waitcnt lgkmcnt(0)\n"
CV "v191,v126,v127\n"
CV "v190,v124,v125\n"
MF "v[52:55],a[204:207],a[196:199],v[52:55]\n"
"s_barrier\n"
MF "v[16:19],a[208:211],a[60:63],v[16:19]\n"
CV "v189,v122,v123\n"
CV "v188,v120,v121\n"
MF "v[16:19],a[212:215],a[100:103],v[16:19]\n"
AW "a4,v188\n"
AW "a5,v189\n"
MF "v[24:27],a[212:215],a[104:107],v[24:27]\n"
AW "a6,v190\n"
AW "a7,v191\n"
MF "v[24:27],a[208:211],a[64:67],v[24:27]\n"
"v_fma_f32 v112,v112,s54,v14\n"
"v_fma_f32 v113,v113,s54,v14\n"
MF "v[32:35],a[208:211],a[68:71],v[32:35]\n"
"v_fma_f32 v114,v114,s54,v14\n"
"v_fma_f32 v115,v115,s54,v14\n"
MF "v[32:35],a[212:215],a[108:111],v[32:35]\n"
"v_fma_f32 v116,v116,s54,v14\n"
"v_fma_f32 v117,v117,s54,v14\n"
MF "v[40:43],a[212:215],a[112:115],v[40:43]\n"
"v_fma_f32 v118,v118,s54,v14\n"
"v_fma_f32 v119,v119,s54,v14\n"
MF "v[40:43],a[208:211],a[72:75],v[40:43]\n"
CV "v187,v118,v119\n"
CV "v186,v116,v117\n"
MF "v[48:51],a[208:211],a[76:79],v[48:51]\n"
CV "v185,v114,v115\n"
CV "v184,v112,v113\n"
MF "v[48:51],a[212:215],a[116:119],v[48:51]\n"
AW "a0,v184\n"
AW "a1,v185\n"
MF "v[48:51],a[216:219],a[156:159],v[48:51]\n"
AW "a2,v186\n"
AW "a3,v187\n"
MF "v[48:51],a[220:223],a[196:199],v[48:51]\n"
"v_fma_f32 v104,v104,s54,v14\n"
"v_fma_f32 v105,v105,s54,v14\n"
MF "v[40:43],a[220:223],a[192:195],v[40:43]\n"
"v_fma_f32 v106,v106,s54,v14\n"
"v_fma_f32 v107,v107,s54,v14\n"
MF "v[40:43],a[216:219],a[152:155],v[40:43]\n"
"v_fma_f32 v108,v108,s54,v14\n"
"v_fma_f32 v109,v109,s54,v14\n"
MF "v[32:35],a[216:219],a[148:151],v[32:35]\n"
"v_fma_f32 v110,v110,s54,v14\n"
"v_fma_f32 v111,v111,s54,v14\n"
MF "v[32:35],a[220:223],a[188:191],v[32:35]\n"
CV "v183,v110,v111\n"
CV "v182,v108,v109\n"
MF "v[24:27],a[220:223],a[184:187],v[24:27]\n"
CV "v181,v106,v107\n"
CV "v180,v104,v105\n"
MF "v[24:27],a[216:219],a[144:147],v[24:27]\n"
AW "a36,v180\n"
AW "a37,v181\n"
MF "v[16:19],a[216:219],a[140:143],v[16:19]\n"
AW "a38,v182\n"
AW "a39,v183\n"
MF "v[16:19],a[220:223],a[180:183],v[16:19]\n"
"s_mov_b32 s57,1\n"
"s_mov_b32 s69,99\n"
AR "v0,a240\n"
AR "v2,a241\n"
AR "v4,a242\n"
AR "v6,a243\n"
"v_xor_b32 v1,64,v0\n"
"v_xor_b32 v3,64,v2\n"
"v_xor_b32_e64 v5,64,v4\n"
"v_add_u32 v11,97280,v6\n"
"v_add_u32 v12,194560,v6\n"
"v_add_u32 v13,291840,v6\n"
"s_mov_b64 vcc,-1\n"
"s_waitcnt vmcnt(10) lgkmcnt(0)\n"
"s_barrier\n"
"s_nop 0\n"
TR "a[200:201],v0\n"
TR "a[202:203],v0 offset:512\n"
TR "a[204:205],v0 offset:4096\n"
TR "a[206:207],v0 offset:4608\n"
"ds_read_b128 a[60:63],v2\n"
"ds_read_b128 a[100:103],v3\n"
"ds_read_b128 a[64:67],v2 offset:2048\n"
"ds_read_b128 a[104:107],v3 offset:2048\n"
"ds_read_b128 a[68:71],v2 offset:4096\n"
"ds_read_b128 a[108:111],v3 offset:4096\n"
"ds_read_b128 a[72:75],v2 offset:6144\n"
"ds_read_b128 a[112:115],v3 offset:6144\n"
"ds_read_b128 a[76:79],v2 offset:8192\n"
"ds_read_b128 a[116:119],v3 offset:8192\n"
TR "a[208:209],v0 offset:8\n"
TR "a[210:211],v0 offset:520\n"
TR "a[212:213],v0 offset:4104\n"
TR "a[214:215],v0 offset:4616\n"
"v_pk_fma_f32 v[96:97],v[96:97],s[54:55],v[14:15] op_sel_hi:[1,1,0]\n"
"v_pk_fma_f32 v[98:99],v[98:99],s[54:55],v[14:15] op_sel_hi:[1,1,0]\n"
"v_pk_fma_f32 v[100:101],v[100:101],s[54:55],v[14:15] op_sel_hi:[1,1,0]\n"
"v_pk_fma_f32 v[102:103],v[102:103],s[54:55],v[14:15] op_sel_hi:[1,1,0]\n"
CV "v179,v102,v103\n"
CV "v178,v100,v101\n"
CV "v177,v98,v99\n"
CV "v176,v96,v97\n"
AW "a32,v176\n"
AW "a33,v177\n"
AW "a34,v178\n"
AW "a35,v179\n"
"v_pk_fma_f32 v[88:89],v[88:89],s[54:55],v[14:15] op_sel_hi:[1,1,0]\n"
"v_pk_fma_f32 v[90:91],v[90:91],s[54:55],v[14:15] op_sel_hi:[1,1,0]\n"
"v_pk_fma_f32 v[92:93],v[92:93],s[54:55],v[14:15] op_sel_hi:[1,1,0]\n"
"v_pk_fma_f32 v[94:95],v[94:95],s[54:55],v[14:15] op_sel_hi:[1,1,0]\n"
CV "v175,v94,v95\n"
CV "v174,v92,v93\n"
CV "v173,v90,v91\n"
CV "v172,v88,v89\n"
AW "a28,v172\n"
AW "a29,v173\n"
AW "a30,v174\n"
AW "a31,v175\n"
"v_pk_fma_f32 v[80:81],v[80:81],s[54:55],v[14:15] op_sel_hi:[1,1,0]\n"
"v_pk_fma_f32 v[82:83],v[82:83],s[54:55],v[14:15] op_sel_hi:[1,1,0]\n"
".Lpg15c:\n"
"s_cbranch_execz .Lpg2a\n"
"s_nop 0\n"
"v_pk_fma_f32 v[84:85],v[84:85],s[54:55],v[14:15] op_sel_hi:[1,1,0]\n"
"v_pk_fma_f32 v[86:87],v[86:87],s[54:55],v[14:15] op_sel_hi:[1,1,0]\n"
CV "v171,v86,v87\n"
CV "v170,v84,v85\n"
CV "v169,v82,v83\n"
CV "v168,v80,v81\n"
AW "a24,v168\n"
AW "a25,v169\n"
AW "a26,v170\n"
AW "a27,v171\n"
"v_pk_fma_f32 v[72:73],v[72:73],s[54:55],v[14:15] op_sel_hi:[1,1,0]\n"
"v_pk_fma_f32 v[74:75],v[74:75],s[54:55],v[14:15] op_sel_hi:[1,1,0]\n"
"v_pk_fma_f32 v[76:77],v[76:77],s[54:55],v[14:15] op_sel_hi:[1,1,0]\n"
"v_pk_fma_f32 v[78:79],v[78:79],s[54:55],v[14:15] op_sel_hi:[1,1,0]\n"
CV "v167,v78,v79\n"
CV "v166,v76,v77\n"
CV "v165,v74,v75\n"
CV "v164,v72,v73\n"
AW "a20,v164\n"
AW "a21,v165\n"
AW "a22,v166\n"
AW "a23,v167\n"
"v_pk_fma_f32 v[64:65],v[64:65],s[54:55],v[14:15] op_sel_hi:[1,1,0]\n"
"v_pk_fma_f32 v[66:67],v[66:67],s[54:55],v[14:15] op_sel_hi:[1,1,0]\n"
"v_pk_fma_f32 v[68:69],v[68:69],s[54:55],v[14:15] op_sel_hi:[1,1,0]\n"
"v_pk_fma_f32 v[70:71],v[70:71],s[54:55],v[14:15] op_sel_hi:[1,1,0]\n"
CV "v163,v70,v71\n"
CV "v162,v68,v69\n"
CV "v161,v66,v67\n"
CV "v160,v64,v65\n"
AW "a176,v160\n"
AW "a177,v161\n"
AW "a178,v162\n"
AW "a179,v163\n"
"v_pk_fma_f32 v[56:57],v[56:57],s[54:55],v[14:15] op_sel_hi:[1,1,0]\n"
"v_pk_fma_f32 v[58:59],v[58:59],s[54:55],v[14:15] op_sel_hi:[1,1,0]\n"
"v_pk_fma_f32 v[60:61],v[60:61],s[54:55],v[14:15] op_sel_hi:[1,1,0]\n"
"v_pk_fma_f32 v[62:63],v[62:63],s[54:55],v[14:15] op_sel_hi:[1,1,0]\n"
CV "v159,v62,v63\n"
CV "v158,v60,v61\n"
CV "v157,v58,v59\n"
CV "v156,v56,v57\n"
AW "a172,v156\n"
AW "a173,v157\n"
AW "a174,v158\n"
AW "a175,v159\n"
"v_pk_mul_f32 v[48:49],v[48:49],s[54:55]\n"
"v_pk_mul_f32 v[50:51],v[50:51],s[54:55]\n"
"v_pk_mul_f32 v[52:53],v[52:53],s[54:55]\n"
"v_pk_mul_f32 v[54:55],v[54:55],s[54:55]\n"
CV "v155,v54,v55\n"
CV "v154,v52,v53\n"
CV "v153,v50,v51\n"
CV "v152,v48,v49\n"
AW "a168,v152\n"
AW "a169,v153\n"
AW "a170,v154\n"
AW "a171,v155\n"
"v_pk_mul_f32 v[40:41],v[40:41],s[54:55]\n"
"v_pk_mul_f32 v[42:43],v[42:43],s[54:55]\n"
"v_pk_mul_f32 v[44:45],v[44:45],s[54:55]\n"
"v_pk_mul_f32 v[46:47],v[46:47],s[54:55]\n"
CV "v151,v46,v47\n"
CV "v150,v44,v45\n"
CV "v149,v42,v43\n"
CV "v148,v40,v41\n"
AW "a120,v148\n"
AW "a121,v149\n"
AW "a122,v150\n"
AW "a123,v151\n"
"v_pk_mul_f32 v[32:33],v[32:33],s[54:55]\n"
"v_pk_mul_f32 v[34:35],v[34:35],s[54:55]\n"
"v_pk_mul_f32 v[36:37],v[36:37],s[54:55]\n"
"v_pk_mul_f32 v[38:39],v[38:39],s[54:55]\n"
CV "v147,v38,v39\n"
CV "v146,v36,v37\n"
CV "v145,v34,v35\n"
CV "v144,v32,v33\n"
AW "a96,v144\n"
AW "a97,v145\n"
AW "a98,v146\n"
AW "a99,v147\n"
"v_pk_mul_f32 v[24:25],v[24:25],s[54:55]\n"
"v_pk_mul_f32 v[26:27],v[26:27],s[54:55]\n"
"v_pk_mul_f32 v[28:29],v[28:29],s[54:55]\n"
"v_pk_mul_f32 v[30:31],v[30:31],s[54:55]\n"
CV "v143,v30,v31\n"
CV "v142,v28,v29\n"
CV "v141,v26,v27\n"
CV "v140,v24,v25\n"
AW "a92,v140\n"
AW "a93,v141\n"
AW "a94,v142\n"
AW "a95,v143\n"
"v_pk_mul_f32 v[16:17],v[16:17],s[54:55]\n"
"v_pk_mul_f32 v[18:19],v[18:19],s[54:55]\n"
"v_pk_mul_f32 v[20:21],v[20:21],s[54:55]\n"
"v_pk_mul_f32 v[22:23],v[22:23],s[54:55]\n"
CV "v139,v22,v23\n"
CV "v138,v20,v21\n"
CV "v137,v18,v19\n"
CV "v136,v16,v17\n"
AW "a88,v136\n"
AW "a89,v137\n"
AW "a90,v138\n"
AW "a91,v139\n"
"s_waitcnt lgkmcnt(0)\n"
"s_branch .Lq0\n"
".Lpgpsl_b:\n"
"s_cbranch_execz .Lpg3a\n"
".Lpsl:\n"
"s_sleep 1\n"
".Lpsub:\n"
"s_memrealtime s[100:101]\n"
"s_waitcnt lgkmcnt(0)\n"
"s_sub_u32 s101,s21,s100\n"
"s_cmp_gt_i32 s101,0\n"
"s_cbranch_scc1 .Lpsl\n"
"s_add_u32 s21,s100,s97\n"
"s_setpc_b64 s[52:53]\n"
".Lstub_6:\n"
"v_cmp_le_f32 vcc,0x47800000,v10\n"
"s_cmp_eq_u32 s77,0\n"
"s_cbranch_scc0 .Lnop_6\n"
"s_call_b64 s[52:53],.Lpsub\n"
".Lnop_6:\n"
"s_cbranch_vccz .Lback_6\n"
"s_call_b64 s[52:53],.Lslow_1\n"
"s_branch .Lback_6\n"
".Lstub_7:\n"
"v_cmp_le_f32 vcc,0x47800000,v10\n"
"s_cmp_eq_u32 s77,0\n"
"s_cbranch_scc0 .Lnop_7\n"
"s_call_b64 s[52:53],.Lpsub\n"
"s_cmp_eq_u32 s61,4\n"
"s_cselect_b32 s77,0x47800000,s77\n"
"s_cselect_b32 s95,0x47800000,s95\n"
".Lnop_7:\n"
"s_cbranch_vccz .Lback_7\n"
"s_call_b64 s[52:53],.Lslow_0\n"
"s_branch .Lback_7\n"
".Lstub_8:\n"
"s_call_b64 s[52:53],.Lslow_1\n"
"s_branch .Lback_8\n"
".Lstub_9:\n"
"s_call_b64 s[52:53],.Lslow_0\n"
"s_branch .Lback_9\n"
".Lstub_10:\n"
"s_call_b64 s[52:53],.Lslow_1\n"
"s_branch .Lback_10\n"
".Lstub_11:\n"
"s_call_b64 s[52:53],.Lslow_1\n"
"s_branch .Lback_11\n"
".Lstub_12:\n"
"s_call_b64 s[52:53],.Lslow_0\n"
"s_branch .Lback_12\n"
".Lstub_13:\n"
"s_call_b64 s[52:53],.Lslow_1\n"
"s_branch .Lback_13\n"
".Lstub_20:\n"
"v_cmp_le_f32 vcc,0x47800000,v10\n"
"s_cmp_eq_u32 s77,0\n"
"s_cbranch_scc0 .Lnop_20\n"
"s_call_b64 s[52:53],.Lpsub\n"
".Lnop_20:\n"
"s_cbranch_vccz .Lback_20\n"
"s_call_b64 s[52:53],.Lslow_1\n"
"s_branch .Lback_20\n"
".Lstub_21:\n"
"v_cmp_le_f32 vcc,0x47800000,v10\n"
"s_cmp_eq_u32 s77,0\n"
"s_cbranch_scc0 .Lnop_21\n"
"s_call_b64 s[52:53],.Lpsub\n"
".Lnop_21:\n"
"s_cbranch_vccz .Lback_21\n"
"s_call_b64 s[52:53],.Lslow_0\n"
"s_branch .Lback_21\n"
".Lstub_22:\n"
"v_cmp_le_f32 vcc,0x47800000,v10\n"
"s_cmp_eq_u32 s77,0\n"
"s_cbranch_scc0 .Lnop_22\n"
"s_call_b64 s[52:53],.Lpsub\n"
".Lnop_22:\n"
"s_cbranch_vccz .Lback_22\n"
"s_call_b64 s[52:53],.Lslow_1\n"
"s_branch .Lback_22\n"
".Lstub_23:\n"
"v_cmp_le_f32 vcc,0x47800000,v10\n"
"s_cmp_eq_u32 s77,0\n"
"s_cbranch_scc0 .Lnop_23\n"
"s_call_b64 s[52:53],.Lpsub\n"
".Lnop_23:\n"
"s_cbranch_vccz .Lback_23\n"
"s_call_b64 s[52:53],.Lslow_0\n"
"s_branch .Lback_23\n"
".Lstub_24:\n"
"v_cmp_le_f32 vcc,0x47800000,v10\n"
"s_cmp_eq_u32 s61,1\n"
"s_cbranch_scc0 .Lhook_24\n"
"s_mov_b64 exec,0\n"
"s_cmp_eq_u32 s56,0\n"
"s_cbranch_scc1 .Lpg26h\n"
"s_cmp_eq_u32 s56,1\n"
"s_cbranch_scc1 .Lpg25h\n"
"s_cmp_eq_u32 s56,2\n"
"s_cbranch_scc1 .Lpg24h\n"
"s_branch .Lpg23h\n"
".Lisr4:\n"
"s_mov_b64 exec,-1\n"
".Lisx4:\n"
"s_and_b32 s58,s56,1\n"
"s_mul_i32 s79,s58,12288\n"
"s_lshl_b32 s15,s58,10\n"
"s_lshr_b32 s58,s56,1\n"
"s_mul_i32 s5,s58,6144\n"
"s_add_u32 s5,s5,s79\n"
"s_lshl_b32 s59,s58,8\n"
"s_add_u32 s15,s15,s59\n"
"s_add_u32 s15,s15,159744\n"
"s_add_u32 s88,s88,1024\n"
"s_add_u32 s89,s89,1024\n"
"s_mul_i32 s58,s58,61440\n"
"s_add_u32 s2,s89,s58\n"
"s_add_u32 s3,s88,s58\n"
"s_add_u32 s0,s2,122880\n"
"s_add_u32 s1,s3,122880\n"
"s_lshl_b32 s58,s56,10\n"
"s_add_u32 s9,s58,73728\n"
"s_add_u32 s14,s58,159744\n"
"s_bfe_u32 s4,s22,0x10014\n"
"s_lshl_b32 s4,s4,5\n"
"s_mov_b32 s6,896\n"
"s_mov_b32 s97,0\n"
".Lhook_24:\n"
"s_cmp_eq_u32 s77,0\n"
"s_cbranch_scc0 .Lnop_24\n"
"s_call_b64 s[52:53],.Lpsub\n"
".Lnop_24:\n"
"s_cbranch_vccz .Lback_24\n"
"s_call_b64 s[52:53],.Lslow_1\n"
"s_branch .Lback_24\n"
".Lstub_25:\n"
"v_cmp_le_f32 vcc,0x47800000,v10\n"
"s_cmp_eq_u32 s77,0\n"
"s_cbranch_scc0 .Lnop_25\n"
"s_call_b64 s[52:53],.Lpsub\n"
".Lnop_25:\n"
"s_cbranch_vccz .Lback_25\n"
"s_call_b64 s[52:53],.Lslow_0\n"
"s_branch .Lback_25\n"
".Lstub_26:\n"
"s_call_b64 s[52:53],.Lslow_1\n"
"s_branch .Lback_26\n"
".Lstub_27:\n"
"s_call_b64 s[52:53],.Lslow_0\n"
"s_branch .Lback_27\n"
".Lstub_28:\n"
"s_call_b64 s[52:53],.Lslow_1\n"
"s_branch .Lback_28\n"
".Lackspin_q:\n"
"s_cmp_eq_u32 s98,0\n"
"s_cbranch_scc1 .Lackok_q\n"
"s_sub_u32 s98,s98,1\n"
"s_sleep 8\n"
"v_bfe_u32 v14,v9,4,5\n"
"v_lshlrev_b32 v14,6,v14\n"
"s_lshl_b32 s58,s78,2\n"
"s_add_u32 s58,s58,s86\n"
"buffer_load_dword v10,v14,s[40:43],s58 offen offset:128 sc1\n"
"s_waitcnt vmcnt(0)\n"
"s_branch .Lack_q\n"
".Lpgep_d:\n"
"s_cbranch_execz .Lisr2\n"
".Lep:\n"
MF "v[72:75],v[16:19],v[136:139],v[32:35]\n"
"s_nop 0\n"
"ds_read_b128 v[24:27],v0 offset:1024\n"
"ds_read_b128 v[28:31],v1 offset:1024\n"
MF "v[72:75],v[20:23],v[156:159],v[72:75]\n"
MF "v[76:79],v[20:23],v[160:163],v[36:39]\n"
MF "v[76:79],v[16:19],v[140:143],v[76:79]\n"
MF "v[80:83],v[16:19],v[144:147],v[40:43]\n"
MF "v[80:83],v[20:23],v[164:167],v[80:83]\n"
MF "v[84:87],v[20:23],v[168:171],v[44:47]\n"
MF "v[84:87],v[16:19],v[148:151],v[84:87]\n"
"s_add_u32 m0,s65,49152\n"
MF "v[88:91],v[16:19],v[152:155],v[48:51]\n"
LD "v8,s[36:39],s73 offen offset:3072 nt lds\n"
MF "v[88:91],v[20:23],v[172:175],v[88:91]\n"
"s_waitcnt lgkmcnt(0)\n"
"ds_read_b128 v[16:19],v0 offset:2048\n"
"ds_read_b128 v[20:23],v1 offset:2048\n"
MF "v[88:91],v[24:27],v[192:195],v[88:91]\n"
"v_exp_f32 v124,v124\n"
MF "v[88:91],v[28:31],v[212:215],v[88:91]\n"
"s_add_u32 m0,s65,50048\n"
"v_exp_f32 v125,v125\n"
MF "v[84:87],v[28:31],v[208:211],v[84:87]\n"
LD "v8,s[36:39],s73 offen offset:3200 nt lds\n"
MF "v[84:87],v[24:27],v[188:191],v[84:87]\n"
"v_exp_f32 v126,v126\n"
MF "v[80:83],v[24:27],v[184:187],v[80:83]\n"
"v_exp_f32 v127,v127\n"
MF "v[80:83],v[28:31],v[204:207],v[80:83]\n"
"s_add_u32 m0,s65,50944\n"
"v_add_f32 v60,v60,v124\n"
"v_add_f32 v60,v60,v125\n"
MF "v[76:79],v[28:31],v[200:203],v[76:79]\n"
LD "v8,s[36:39],s73 offen offset:3328 nt lds\n"
MF "v[76:79],v[24:27],v[180:183],v[76:79]\n"
CV "v106,v124,v125\n"
"v_add_f32 v60,v60,v126\n"
MF "v[72:75],v[24:27],v[176:179],v[72:75]\n"
"v_add_f32 v60,v60,v127\n"
CV "v107,v126,v127\n"
MF "v[72:75],v[28:31],v[196:199],v[72:75]\n"
"s_waitcnt lgkmcnt(0)\n"
"ds_read_b128 v[24:27],v0 offset:6144\n"
"ds_read_b128 v[28:31],v1 offset:6144\n"
MF "v[72:75],v[16:19],v[216:219],v[72:75]\n"
"v_exp_f32 v128,v128\n"
MF "v[72:75],v[20:23],v[236:239],v[72:75]\n"
"v_exp_f32 v129,v129\n"
MF "v[76:79],v[20:23],v[240:243],v[76:79]\n"
"v_exp_f32 v130,v130\n"
MF "v[76:79],v[16:19],v[220:223],v[76:79]\n"
"v_exp_f32 v131,v131\n"
MF "v[80:83],v[16:19],v[224:227],v[80:83]\n"
"v_add_f32 v61,v61,v128\n"
"v_add_f32 v61,v61,v129\n"
MF "v[80:83],v[20:23],v[244:247],v[80:83]\n"
CV "v110,v128,v129\n"
"v_add_f32 v61,v61,v130\n"
MF "v[84:87],v[20:23],v[248:251],v[84:87]\n"
"v_add_f32 v61,v61,v131\n"
CV "v111,v130,v131\n"
MF "v[84:87],v[16:19],v[228:231],v[84:87]\n"
"v_max3_f32 v10,v57,v58,v59\n"
"v_max3_f32 v10,v10,v60,v61\n"
MF "v[88:91],v[16:19],v[232:235],v[88:91]\n"
"v_cmp_le_f32 vcc,0x47800000,v10\n"
MF "v[88:91],v[20:23],v[252:255],v[88:91]\n"
"s_waitcnt lgkmcnt(0)\n"
"s_cbranch_vccnz .Lstub_11\n"
".Lback_11:\n"
MF "v[112:115],v[24:27],v[136:139],v[32:35]\n"
"s_add_u32 s74,s74,12288\n"
"ds_read_b128 v[16:19],v0 offset:7168\n"
"ds_read_b128 v[20:23],v1 offset:7168\n"
MF "v[112:115],v[28:31],v[156:159],v[112:115]\n"
"s_cmp_eq_u32 s74,s20\n"
"s_cselect_b32 s74,s70,s74\n"
MF "v[116:119],v[28:31],v[160:163],v[36:39]\n"
"v_exp_f32 v72,v72\n"
MF "v[116:119],v[24:27],v[140:143],v[116:119]\n"
"v_exp_f32 v73,v73\n"
MF "v[120:123],v[24:27],v[144:147],v[40:43]\n"
"v_exp_f32 v74,v74\n"
MF "v[120:123],v[28:31],v[164:167],v[120:123]\n"
"v_exp_f32 v75,v75\n"
MF "v[124:127],v[28:31],v[168:171],v[44:47]\n"
"v_add_f32 v52,v57,v72\n"
"v_add_f32 v52,v52,v73\n"
MF "v[124:127],v[24:27],v[148:151],v[124:127]\n"
CV "v72,v72,v73\n"
"v_add_f32 v52,v52,v74\n"
MF "v[128:131],v[24:27],v[152:155],v[48:51]\n"
"v_add_f32 v52,v52,v75\n"
CV "v73,v74,v75\n"
MF "v[128:131],v[28:31],v[172:175],v[128:131]\n"
"s_waitcnt lgkmcnt(0)\n"
"ds_read_b128 v[24:27],v0 offset:8192\n"
"ds_read_b128 v[28:31],v1 offset:8192\n"
MF "v[128:131],v[16:19],v[192:195],v[128:131]\n"
"v_exp_f32 v76,v76\n"
MF "v[128:131],v[20:23],v[212:215],v[128:131]\n"
"v_exp_f32 v77,v77\n"
MF "v[124:127],v[20:23],v[208:211],v[124:127]\n"
"v_exp_f32 v78,v78\n"
MF "v[124:127],v[16:19],v[188:191],v[124:127]\n"
"v_exp_f32 v79,v79\n"
MF "v[120:123],v[16:19],v[184:187],v[120:123]\n"
"v_add_f32 v53,v58,v76\n"
"v_add_f32 v53,v53,v77\n"
MF "v[120:123],v[20:23],v[204:207],v[120:123]\n"
CV "v76,v76,v77\n"
"v_add_f32 v53,v53,v78\n"
MF "v[116:119],v[20:23],v[200:203],v[116:119]\n"
"s_waitcnt vmcnt(12) lgkmcnt(0)\n"
"v_add_f32 v53,v53,v79\n"
CV "v77,v78,v79\n"
MF "v[116:119],v[16:19],v[180:183],v[116:119]\n"
"s_barrier\n"
MF "v[112:115],v[16:19],v[176:179],v[112:115]\n"
"v_exp_f32 v80,v80\n"
MF "v[112:115],v[20:23],v[196:199],v[112:115]\n"
"s_sub_u32 s58,s71,s70\n"
TR "v[16:17],v2 offset:24576\n"
TR "v[18:19],v2 offset:30720\n"
MF "v[112:115],v[24:27],v[216:219],v[112:115]\n"
"s_add_u32 s72,s72,s58\n"
TR "v[20:21],v3 offset:24576\n"
TR "v[22:23],v3 offset:30720\n"
MF "v[112:115],v[28:31],v[236:239],v[112:115]\n"
"s_add_u32 s16,s16,s58\n"
"s_add_u32 s17,s17,s58\n"
"s_add_u32 s72,s72,6144\n"
MF "v[116:119],v[28:31],v[240:243],v[116:119]\n"
"s_cmp_eq_u32 s72,s17\n"
"s_cselect_b32 s72,s16,s72\n"
"s_add_u32 m0,s64,0\n"
MF "v[116:119],v[24:27],v[220:223],v[116:119]\n"
LD "v7,s[32:35],s72 offen nt lds\n"
MF "v[120:123],v[24:27],v[224:227],v[120:123]\n"
"s_add_u32 m0,s64,896\n"
"v_exp_f32 v81,v81\n"
MF "v[120:123],v[28:31],v[244:247],v[120:123]\n"
LD "v7,s[32:35],s72 offen offset:128 nt lds\n"
MF "v[124:127],v[28:31],v[248:251],v[124:127]\n"
"s_add_u32 m0,s64,1792\n"
"v_exp_f32 v82,v82\n"
MF "v[124:127],v[24:27],v[228:231],v[124:127]\n"
LD "v7,s[32:35],s72 offen offset:256 nt lds\n"
MF "v[128:131],v[24:27],v[232:235],v[128:131]\n"
"s_add_u32 s72,s72,6144\n"
"v_exp_f32 v83,v83\n"
MF "v[128:131],v[28:31],v[252:255],v[128:131]\n"
"s_waitcnt lgkmcnt(0)\n"
TR "v[24:25],v4 offset:24576\n"
TR "v[26:27],v4 offset:30720\n"
MF "a[0:3],v[16:19],v[92:95],a[0:3]\n"
"s_add_u32 m0,s64,6144\n"
TR "v[28:29],v5 offset:24576\n"
TR "v[30:31],v5 offset:30720\n"
MF "a[20:23],v[20:23],v[92:95],a[20:23]\n"
LD "v7,s[32:35],s72 offen nt lds\n"
MF "a[24:27],v[20:23],v[96:99],a[24:27]\n"
"v_add_f32 v54,v59,v80\n"
"v_add_f32 v54,v54,v81\n"
MF "a[4:7],v[16:19],v[96:99],a[4:7]\n"
"s_add_u32 m0,s64,7040\n"
CV "v80,v80,v81\n"
"v_add_f32 v54,v54,v82\n"
MF "a[8:11],v[16:19],v[100:103],a[8:11]\n"
LD "v7,s[32:35],s72 offen offset:128 nt lds\n"
MF "a[28:31],v[20:23],v[100:103],a[28:31]\n"
"v_add_f32 v54,v54,v83\n"
CV "v81,v82,v83\n"
MF "a[32:35],v[20:23],v[104:107],a[32:35]\n"
"s_add_u32 m0,s64,7936\n"
"v_exp_f32 v84,v84\n"
MF "a[12:15],v[16:19],v[104:107],a[12:15]\n"
LD "v7,s[32:35],s72 offen offset:256 nt lds\n"
MF "a[16:19],v[16:19],v[108:111],a[16:19]\n"
"v_exp_f32 v85,v85\n"
MF "a[36:39],v[20:23],v[108:111],a[36:39]\n"
"s_waitcnt lgkmcnt(0)\n"
TR "v[16:17],v2 offset:25600\n"
TR "v[18:19],v2 offset:31744\n"
MF "a[56:59],v[24:27],v[108:111],a[56:59]\n"
"s_nop 0\n"
TR "v[20:21],v3 offset:25600\n"
TR "v[22:23],v3 offset:31744\n"
MF "a[76:79],v[28:31],v[108:111],a[76:79]\n"
"v_exp_f32 v86,v86\n"
MF "a[72:75],v[28:31],v[104:107],a[72:75]\n"
"v_exp_f32 v87,v87\n"
MF "a[52:55],v[24:27],v[104:107],a[52:55]\n"
"v_add_f32 v55,v60,v84\n"
"v_add_f32 v55,v55,v85\n"
MF "a[48:51],v[24:27],v[100:103],a[48:51]\n"
CV "v84,v84,v85\n"
"v_add_f32 v55,v55,v86\n"
MF "a[68:71],v[28:31],v[100:103],a[68:71]\n"
"v_add_f32 v55,v55,v87\n"
CV "v85,v86,v87\n"
MF "a[64:67],v[28:31],v[96:99],a[64:67]\n"
"v_exp_f32 v88,v88\n"
MF "a[44:47],v[24:27],v[96:99],a[44:47]\n"
"v_exp_f32 v89,v89\n"
MF "a[40:43],v[24:27],v[92:95],a[40:43]\n"
"v_exp_f32 v90,v90\n"
MF "a[60:63],v[28:31],v[92:95],a[60:63]\n"
"s_waitcnt lgkmcnt(0)\n"
TR "v[24:25],v4 offset:25600\n"
TR "v[26:27],v4 offset:31744\n"
MF "a[80:83],v[16:19],v[92:95],a[80:83]\n"
"s_nop 0\n"
TR "v[28:29],v5 offset:25600\n"
TR "v[30:31],v5 offset:31744\n"
MF "a[100:103],v[20:23],v[92:95],a[100:103]\n"
"v_exp_f32 v91,v91\n"
MF "a[104:107],v[20:23],v[96:99],a[104:107]\n"
"v_add_f32 v56,v61,v88\n"
"v_add_f32 v56,v56,v89\n"
MF "a[84:87],v[16:19],v[96:99],a[84:87]\n"
CV "v88,v88,v89\n"
"v_add_f32 v56,v56,v90\n"
MF "a[88:91],v[16:19],v[100:103],a[88:91]\n"
"v_add_f32 v56,v56,v91\n"
CV "v89,v90,v91\n"
MF "a[108:111],v[20:23],v[100:103],a[108:111]\n"
"v_exp_f32 v112,v112\n"
MF "a[112:115],v[20:23],v[104:107],a[112:115]\n"
"v_exp_f32 v113,v113\n"
MF "a[92:95],v[16:19],v[104:107],a[92:95]\n"
"v_exp_f32 v114,v114\n"
MF "a[96:99],v[16:19],v[108:111],a[96:99]\n"
"v_exp_f32 v115,v115\n"
MF "a[116:119],v[20:23],v[108:111],a[116:119]\n"
"s_waitcnt lgkmcnt(0)\n"
TR "v[16:17],v2 offset:26624\n"
TR "v[18:19],v2 offset:32768\n"
MF "a[136:139],v[24:27],v[108:111],a[136:139]\n"
"s_nop 0\n"
TR "v[20:21],v3 offset:26624\n"
TR "v[22:23],v3 offset:32768\n"
MF "a[156:159],v[28:31],v[108:111],a[156:159]\n"
"v_add_f32 v52,v52,v112\n"
"v_add_f32 v52,v52,v113\n"
MF "a[152:155],v[28:31],v[104:107],a[152:155]\n"
CV "v74,v112,v113\n"
"v_add_f32 v52,v52,v114\n"
MF "a[132:135],v[24:27],v[104:107],a[132:135]\n"
"v_add_f32 v52,v52,v115\n"
CV "v75,v114,v115\n"
MF "a[128:131],v[24:27],v[100:103],a[128:131]\n"
"v_exp_f32 v116,v116\n"
MF "a[148:151],v[28:31],v[100:103],a[148:151]\n"
"v_exp_f32 v117,v117\n"
MF "a[144:147],v[28:31],v[96:99],a[144:147]\n"
"v_exp_f32 v118,v118\n"
MF "a[124:127],v[24:27],v[96:99],a[124:127]\n"
"v_exp_f32 v119,v119\n"
MF "a[120:123],v[24:27],v[92:95],a[120:123]\n"
"v_add_f32 v53,v53,v116\n"
"v_add_f32 v53,v53,v117\n"
MF "a[140:143],v[28:31],v[92:95],a[140:143]\n"
"s_waitcnt lgkmcnt(0)\n"
TR "v[24:25],v4 offset:26624\n"
TR "v[26:27],v4 offset:32768\n"
MF "a[160:163],v[16:19],v[92:95],a[160:163]\n"
"s_nop 0\n"
TR "v[28:29],v5 offset:26624\n"
TR "v[30:31],v5 offset:32768\n"
MF "a[180:183],v[20:23],v[92:95],a[180:183]\n"
CV "v78,v116,v117\n"
"v_add_f32 v53,v53,v118\n"
MF "a[184:187],v[20:23],v[96:99],a[184:187]\n"
"v_add_f32 v53,v53,v119\n"
CV "v79,v118,v119\n"
MF "a[164:167],v[16:19],v[96:99],a[164:167]\n"
"v_exp_f32 v120,v120\n"
MF "a[168:171],v[16:19],v[100:103],a[168:171]\n"
"v_exp_f32 v121,v121\n"
MF "a[188:191],v[20:23],v[100:103],a[188:191]\n"
"v_exp_f32 v122,v122\n"
MF "a[192:195],v[20:23],v[104:107],a[192:195]\n"
"v_exp_f32 v123,v123\n"
MF "a[172:175],v[16:19],v[104:107],a[172:175]\n"
"s_waitcnt vmcnt(12) lgkmcnt(0)\n"
"v_add_f32 v54,v54,v120\n"
"v_add_f32 v54,v54,v121\n"
MF "a[176:179],v[16:19],v[108:111],a[176:179]\n"
"s_barrier\n"
MF "a[196:199],v[20:23],v[108:111],a[196:199]\n"
"s_add_u32 s73,s73,12288\n"
"ds_read_b128 v[16:19],v0 offset:24576\n"
"ds_read_b128 v[20:23],v1 offset:24576\n"
MF "a[216:219],v[24:27],v[108:111],a[216:219]\n"
"s_cmp_eq_u32 s73,s19\n"
"s_cselect_b32 s73,s18,s73\n"
"s_add_u32 m0,s65,73728\n"
MF "a[236:239],v[28:31],v[108:111],a[236:239]\n"
LD "v8,s[36:39],s73 offen nt lds\n"
MF "a[232:235],v[28:31],v[104:107],a[232:235]\n"
"s_add_u32 m0,s65,74624\n"
CV "v82,v120,v121\n"
"v_add_f32 v54,v54,v122\n"
MF "a[212:215],v[24:27],v[104:107],a[212:215]\n"
LD "v8,s[36:39],s73 offen offset:128 nt lds\n"
MF "a[208:211],v[24:27],v[100:103],a[208:211]\n"
"s_add_u32 m0,s65,75520\n"
"v_add_f32 v54,v54,v123\n"
CV "v83,v122,v123\n"
MF "a[228:231],v[28:31],v[100:103],a[228:231]\n"
LD "v8,s[36:39],s73 offen offset:256 nt lds\n"
MF "a[224:227],v[28:31],v[96:99],a[224:227]\n"
MF "a[204:207],v[24:27],v[96:99],a[204:207]\n"
MF "a[200:203],v[24:27],v[92:95],a[200:203]\n"
MF "a[220:223],v[28:31],v[92:95],a[220:223]\n"
"s_waitcnt lgkmcnt(0)\n"
MF "v[92:95],v[16:19],v[136:139],v[32:35]\n"
"s_add_u32 m0,s65,73728\n"
"ds_read_b128 v[24:27],v0 offset:25600\n"
"ds_read_b128 v[28:31],v1 offset:25600\n"
MF "v[92:95],v[20:23],v[156:159],v[92:95]\n"
LD "v8,s[36:39],s73 offen offset:3072 nt lds\n"
MF "v[96:99],v[20:23],v[160:163],v[36:39]\n"
"v_exp_f32 v124,v124\n"
MF "v[96:99],v[16:19],v[140:143],v[96:99]\n"
"v_exp_f32 v125,v125\n"
MF "v[100:103],v[16:19],v[144:147],v[40:43]\n"
"s_add_u32 m0,s65,74624\n"
"v_exp_f32 v126,v126\n"
MF "v[100:103],v[20:23],v[164:167],v[100:103]\n"
LD "v8,s[36:39],s73 offen offset:3200 nt lds\n"
MF "v[104:107],v[20:23],v[168:171],v[44:47]\n"
"v_exp_f32 v127,v127\n"
MF "v[104:107],v[16:19],v[148:151],v[104:107]\n"
"s_add_u32 m0,s65,75520\n"
"v_add_f32 v55,v55,v124\n"
"v_add_f32 v55,v55,v125\n"
MF "v[108:111],v[16:19],v[152:155],v[48:51]\n"
LD "v8,s[36:39],s73 offen offset:3328 nt lds\n"
MF "v[108:111],v[20:23],v[172:175],v[108:111]\n"
"s_waitcnt lgkmcnt(0)\n"
"ds_read_b128 v[16:19],v0 offset:26624\n"
"ds_read_b128 v[20:23],v1 offset:26624\n"
MF "v[108:111],v[24:27],v[192:195],v[108:111]\n"
CV "v86,v124,v125\n"
"v_add_f32 v55,v55,v126\n"
MF "v[108:111],v[28:31],v[212:215],v[108:111]\n"
"v_add_f32 v55,v55,v127\n"
CV "v87,v126,v127\n"
MF "v[104:107],v[28:31],v[208:211],v[104:107]\n"
"v_exp_f32 v128,v128\n"
MF "v[104:107],v[24:27],v[188:191],v[104:107]\n"
"v_exp_f32 v129,v129\n"
MF "v[100:103],v[24:27],v[184:187],v[100:103]\n"
"v_exp_f32 v130,v130\n"
MF "v[100:103],v[28:31],v[204:207],v[100:103]\n"
"v_exp_f32 v131,v131\n"
MF "v[96:99],v[28:31],v[200:203],v[96:99]\n"
"v_add_f32 v56,v56,v128\n"
"v_add_f32 v56,v56,v129\n"
MF "v[96:99],v[24:27],v[180:183],v[96:99]\n"
CV "v90,v128,v129\n"
"v_add_f32 v56,v56,v130\n"
MF "v[92:95],v[24:27],v[176:179],v[92:95]\n"
"v_add_f32 v56,v56,v131\n"
CV "v91,v130,v131\n"
MF "v[92:95],v[28:31],v[196:199],v[92:95]\n"
"s_waitcnt lgkmcnt(0)\n"
"ds_read_b128 v[24:27],v0 offset:30720\n"
"ds_read_b128 v[28:31],v1 offset:30720\n"
MF "v[92:95],v[16:19],v[216:219],v[92:95]\n"
MF "v[92:95],v[20:23],v[236:239],v[92:95]\n"
MF "v[96:99],v[20:23],v[240:243],v[96:99]\n"
MF "v[96:99],v[16:19],v[220:223],v[96:99]\n"
MF "v[100:103],v[16:19],v[224:227],v[100:103]\n"
MF "v[100:103],v[20:23],v[244:247],v[100:103]\n"
MF "v[104:107],v[20:23],v[248:251],v[104:107]\n"
MF "v[104:107],v[16:19],v[228:231],v[104:107]\n"
"v_max3_f32 v10,v52,v53,v54\n"
"v_max3_f32 v10,v10,v55,v56\n"
MF "v[108:111],v[16:19],v[232:235],v[108:111]\n"
"v_cmp_le_f32 vcc,0x47800000,v10\n"
MF "v[108:111],v[20:23],v[252:255],v[108:111]\n"
"s_waitcnt lgkmcnt(0)\n"
"s_cbranch_vccnz .Lstub_12\n"
".Lback_12:\n"
MF "v[112:115],v[24:27],v[136:139],v[32:35]\n"
"s_add_u32 s74,s74,12288\n"
"ds_read_b128 v[16:19],v0 offset:31744\n"
"ds_read_b128 v[20:23],v1 offset:31744\n"
MF "v[112:115],v[28:31],v[156:159],v[112:115]\n"
".Lpg16d:\n"
"s_cbranch_execz .Lisr2\n"
"s_cmp_eq_u32 s74,s20\n"
"s_cselect_b32 s74,s70,s74\n"
MF "v[116:119],v[28:31],v[160:163],v[36:39]\n"
"v_exp_f32 v92,v92\n"
MF "v[116:119],v[24:27],v[140:143],v[116:119]\n"
"v_exp_f32 v93,v93\n"
MF "v[120:123],v[24:27],v[144:147],v[40:43]\n"
"v_exp_f32 v94,v94\n"
MF "v[120:123],v[28:31],v[164:167],v[120:123]\n"
"v_exp_f32 v95,v95\n"
MF "v[124:127],v[28:31],v[168:171],v[44:47]\n"
"v_add_f32 v57,v52,v92\n"
"v_add_f32 v57,v57,v93\n"
MF "v[124:127],v[24:27],v[148:151],v[124:127]\n"
CV "v92,v92,v93\n"
"v_add_f32 v57,v57,v94\n"
MF "v[128:131],v[24:27],v[152:155],v[48:51]\n"
"v_add_f32 v57,v57,v95\n"
CV "v93,v94,v95\n"
MF "v[128:131],v[28:31],v[172:175],v[128:131]\n"
"s_waitcnt lgkmcnt(0)\n"
"ds_read_b128 v[24:27],v0 offset:32768\n"
"ds_read_b128 v[28:31],v1 offset:32768\n"
MF "v[128:131],v[16:19],v[192:195],v[128:131]\n"
"v_exp_f32 v96,v96\n"
MF "v[128:131],v[20:23],v[212:215],v[128:131]\n"
"v_exp_f32 v97,v97\n"
MF "v[124:127],v[20:23],v[208:211],v[124:127]\n"
"v_exp_f32 v98,v98\n"
MF "v[124:127],v[16:19],v[188:191],v[124:127]\n"
"v_exp_f32 v99,v99\n"
MF "v[120:123],v[16:19],v[184:187],v[120:123]\n"
"v_add_f32 v58,v53,v96\n"
"v_add_f32 v58,v58,v97\n"
MF "v[120:123],v[20:23],v[204:207],v[120:123]\n"
CV "v96,v96,v97\n"
"v_add_f32 v58,v58,v98\n"
MF "v[116:119],v[20:23],v[200:203],v[116:119]\n"
"s_waitcnt vmcnt(12) lgkmcnt(0)\n"
"v_add_f32 v58,v58,v99\n"
CV "v97,v98,v99\n"
MF "v[116:119],v[16:19],v[180:183],v[116:119]\n"
"s_barrier\n"
MF "v[112:115],v[16:19],v[176:179],v[112:115]\n"
"v_exp_f32 v100,v100\n"
MF "v[112:115],v[20:23],v[196:199],v[112:115]\n"
"s_nop 0\n"
TR "v[16:17],v2\n"
TR "v[18:19],v2 offset:6144\n"
MF "v[112:115],v[24:27],v[216:219],v[112:115]\n"
"s_add_u32 s72,s72,6144\n"
TR "v[20:21],v3\n"
TR "v[22:23],v3 offset:6144\n"
MF "v[112:115],v[28:31],v[236:239],v[112:115]\n"
"s_cmp_eq_u32 s72,s17\n"
"s_cselect_b32 s72,s16,s72\n"
"s_add_u32 m0,s64,24576\n"
MF "v[116:119],v[28:31],v[240:243],v[116:119]\n"
LD "v7,s[32:35],s72 offen nt lds\n"
MF "v[116:119],v[24:27],v[220:223],v[116:119]\n"
"s_add_u32 m0,s64,25472\n"
"v_exp_f32 v101,v101\n"
MF "v[120:123],v[24:27],v[224:227],v[120:123]\n"
LD "v7,s[32:35],s72 offen offset:128 nt lds\n"
MF "v[120:123],v[28:31],v[244:247],v[120:123]\n"
"v_exp_f32 v102,v102\n"
MF "v[124:127],v[28:31],v[248:251],v[124:127]\n"
"s_add_u32 m0,s64,26368\n"
"v_exp_f32 v103,v103\n"
MF "v[124:127],v[24:27],v[228:231],v[124:127]\n"
LD "v7,s[32:35],s72 offen offset:256 nt lds\n"
MF "v[128:131],v[24:27],v[232:235],v[128:131]\n"
"s_add_u32 s72,s72,6144\n"
"v_add_f32 v59,v54,v100\n"
"v_add_f32 v59,v59,v101\n"
MF "v[128:131],v[28:31],v[252:255],v[128:131]\n"
"s_waitcnt lgkmcnt(0)\n"
TR "v[24:25],v4\n"
TR "v[26:27],v4 offset:6144\n"
MF "a[0:3],v[16:19],v[72:75],a[0:3]\n"
"s_add_u32 m0,s64,30720\n"
TR "v[28:29],v5\n"
TR "v[30:31],v5 offset:6144\n"
MF "a[20:23],v[20:23],v[72:75],a[20:23]\n"
LD "v7,s[32:35],s72 offen nt lds\n"
MF "a[24:27],v[20:23],v[76:79],a[24:27]\n"
CV "v100,v100,v101\n"
"v_add_f32 v59,v59,v102\n"
MF "a[4:7],v[16:19],v[76:79],a[4:7]\n"
"s_add_u32 m0,s64,31616\n"
"v_add_f32 v59,v59,v103\n"
CV "v101,v102,v103\n"
MF "a[8:11],v[16:19],v[80:83],a[8:11]\n"
LD "v7,s[32:35],s72 offen offset:128 nt lds\n"
MF "a[28:31],v[20:23],v[80:83],a[28:31]\n"
"v_exp_f32 v104,v104\n"
MF "a[32:35],v[20:23],v[84:87],a[32:35]\n"
"s_add_u32 m0,s64,32512\n"
"v_exp_f32 v105,v105\n"
MF "a[12:15],v[16:19],v[84:87],a[12:15]\n"
LD "v7,s[32:35],s72 offen offset:256 nt lds\n"
MF "a[16:19],v[16:19],v[88:91],a[16:19]\n"
"v_exp_f32 v106,v106\n"
MF "a[36:39],v[20:23],v[88:91],a[36:39]\n"
"s_waitcnt lgkmcnt(0)\n"
TR "v[16:17],v2 offset:1024\n"
TR "v[18:19],v2 offset:7168\n"
MF "a[56:59],v[24:27],v[88:91],a[56:59]\n"
"s_nop 0\n"
TR "v[20:21],v3 offset:1024\n"
TR "v[22:23],v3 offset:7168\n"
MF "a[76:79],v[28:31],v[88:91],a[76:79]\n"
"v_exp_f32 v107,v107\n"
MF "a[72:75],v[28:31],v[84:87],a[72:75]\n"
"v_add_f32 v60,v55,v104\n"
"v_add_f32 v60,v60,v105\n"
MF "a[52:55],v[24:27],v[84:87],a[52:55]\n"
CV "v104,v104,v105\n"
"v_add_f32 v60,v60,v106\n"
MF "a[48:51],v[24:27],v[80:83],a[48:51]\n"
"v_add_f32 v60,v60,v107\n"
CV "v105,v106,v107\n"
MF "a[68:71],v[28:31],v[80:83],a[68:71]\n"
"v_exp_f32 v108,v108\n"
MF "a[64:67],v[28:31],v[76:79],a[64:67]\n"
"v_exp_f32 v109,v109\n"
MF "a[44:47],v[24:27],v[76:79],a[44:47]\n"
"v_exp_f32 v110,v110\n"
MF "a[40:43],v[24:27],v[72:75],a[40:43]\n"
"v_exp_f32 v111,v111\n"
MF "a[60:63],v[28:31],v[72:75],a[60:63]\n"
"s_waitcnt lgkmcnt(0)\n"
TR "v[24:25],v4 offset:1024\n"
TR "v[26:27],v4 offset:7168\n"
MF "a[80:83],v[16:19],v[72:75],a[80:83]\n"
"s_nop 0\n"
TR "v[28:29],v5 offset:1024\n"
TR "v[30:31],v5 offset:7168\n"
MF "a[100:103],v[20:23],v[72:75],a[100:103]\n"
"v_add_f32 v61,v56,v108\n"
"v_add_f32 v61,v61,v109\n"
MF "a[104:107],v[20:23],v[76:79],a[104:107]\n"
CV "v108,v108,v109\n"
"v_add_f32 v61,v61,v110\n"
MF "a[84:87],v[16:19],v[76:79],a[84:87]\n"
"v_add_f32 v61,v61,v111\n"
CV "v109,v110,v111\n"
MF "a[88:91],v[16:19],v[80:83],a[88:91]\n"
"v_exp_f32 v112,v112\n"
MF "a[108:111],v[20:23],v[80:83],a[108:111]\n"
"v_exp_f32 v113,v113\n"
MF "a[112:115],v[20:23],v[84:87],a[112:115]\n"
"v_exp_f32 v114,v114\n"
MF "a[92:95],v[16:19],v[84:87],a[92:95]\n"
"v_exp_f32 v115,v115\n"
MF "a[96:99],v[16:19],v[88:91],a[96:99]\n"
"v_add_f32 v57,v57,v112\n"
"v_add_f32 v57,v57,v113\n"
MF "a[116:119],v[20:23],v[88:91],a[116:119]\n"
"s_waitcnt lgkmcnt(0)\n"
TR "v[16:17],v2 offset:2048\n"
TR "v[18:19],v2 offset:8192\n"
MF "a[136:139],v[24:27],v[88:91],a[136:139]\n"
"s_nop 0\n"
TR "v[20:21],v3 offset:2048\n"
TR "v[22:23],v3 offset:8192\n"
MF "a[156:159],v[28:31],v[88:91],a[156:159]\n"
CV "v94,v112,v113\n"
"v_add_f32 v57,v57,v114\n"
MF "a[152:155],v[28:31],v[84:87],a[152:155]\n"
"v_add_f32 v57,v57,v115\n"
CV "v95,v114,v115\n"
MF "a[132:135],v[24:27],v[84:87],a[132:135]\n"
"v_exp_f32 v116,v116\n"
MF "a[128:131],v[24:27],v[80:83],a[128:131]\n"
"v_exp_f32 v117,v117\n"
MF "a[148:151],v[28:31],v[80:83],a[148:151]\n"
"v_exp_f32 v118,v118\n"
MF "a[144:147],v[28:31],v[76:79],a[144:147]\n"
"v_exp_f32 v119,v119\n"
MF "a[124:127],v[24:27],v[76:79],a[124:127]\n"
"v_add_f32 v58,v58,v116\n"
"v_add_f32 v58,v58,v117\n"
MF "a[120:123],v[24:27],v[72:75],a[120:123]\n"
CV "v98,v116,v117\n"
"v_add_f32 v58,v58,v118\n"
MF "a[140:143],v[28:31],v[72:75],a[140:143]\n"
"s_waitcnt lgkmcnt(0)\n"
TR "v[24:25],v4 offset:2048\n"
TR "v[26:27],v4 offset:8192\n"
MF "a[160:163],v[16:19],v[72:75],a[160:163]\n"
"s_nop 0\n"
TR "v[28:29],v5 offset:2048\n"
TR "v[30:31],v5 offset:8192\n"
MF "a[180:183],v[20:23],v[72:75],a[180:183]\n"
"v_add_f32 v58,v58,v119\n"
CV "v99,v118,v119\n"
MF "a[184:187],v[20:23],v[76:79],a[184:187]\n"
"v_exp_f32 v120,v120\n"
MF "a[164:167],v[16:19],v[76:79],a[164:167]\n"
"v_exp_f32 v121,v121\n"
MF "a[168:171],v[16:19],v[80:83],a[168:171]\n"
"v_exp_f32 v122,v122\n"
MF "a[188:191],v[20:23],v[80:83],a[188:191]\n"
"v_exp_f32 v123,v123\n"
MF "a[192:195],v[20:23],v[84:87],a[192:195]\n"
"v_add_f32 v59,v59,v120\n"
"v_add_f32 v59,v59,v121\n"
MF "a[172:175],v[16:19],v[84:87],a[172:175]\n"
"s_waitcnt lgkmcnt(0)\n"
CV "v102,v120,v121\n"
"v_add_f32 v59,v59,v122\n"
MF "a[176:179],v[16:19],v[88:91],a[176:179]\n"
"s_barrier\n"
MF "a[196:199],v[20:23],v[88:91],a[196:199]\n"
"s_sub_u32 s58,s71,s70\n"
"s_add_u32 s73,s73,s58\n"
"v_add_f32 v59,v59,v123\n"
MF "a[216:219],v[24:27],v[88:91],a[216:219]\n"
"s_add_u32 s18,s18,s58\n"
"s_add_u32 s19,s19,s58\n"
"s_add_u32 s73,s73,12288\n"
MF "a[236:239],v[28:31],v[88:91],a[236:239]\n"
"s_cmp_eq_u32 s73,s19\n"
"s_cselect_b32 s73,s18,s73\n"
"s_add_u32 m0,s65,49152\n"
MF "a[232:235],v[28:31],v[84:87],a[232:235]\n"
LD "v8,s[36:39],s73 offen nt lds\n"
MF "a[212:215],v[24:27],v[84:87],a[212:215]\n"
"s_add_u32 m0,s65,50048\n"
CV "v103,v122,v123\n"
MF "a[208:211],v[24:27],v[80:83],a[208:211]\n"
LD "v8,s[36:39],s73 offen offset:128 nt lds\n"
MF "a[228:231],v[28:31],v[80:83],a[228:231]\n"
"s_add_u32 m0,s65,50944\n"
"v_exp_f32 v124,v124\n"
MF "a[224:227],v[28:31],v[76:79],a[224:227]\n"
LD "v8,s[36:39],s73 offen offset:256 nt lds\n"
MF "a[204:207],v[24:27],v[76:79],a[204:207]\n"
"v_exp_f32 v125,v125\n"
MF "a[200:203],v[24:27],v[72:75],a[200:203]\n"
"v_exp_f32 v126,v126\n"
MF "a[220:223],v[28:31],v[72:75],a[220:223]\n"
"v_exp_f32 v127,v127\n"
"v_add_f32 v60,v60,v124\n"
"v_add_f32 v60,v60,v125\n"
CV "v106,v124,v125\n"
"v_add_f32 v60,v60,v126\n"
"v_add_f32 v60,v60,v127\n"
CV "v107,v126,v127\n"
"v_exp_f32 v128,v128\n"
"v_exp_f32 v129,v129\n"
"v_exp_f32 v130,v130\n"
"v_exp_f32 v131,v131\n"
"v_add_f32 v61,v61,v128\n"
"v_add_f32 v61,v61,v129\n"
CV "v110,v128,v129\n"
"v_add_f32 v61,v61,v130\n"
"v_add_f32 v61,v61,v131\n"
CV "v111,v130,v131\n"
"v_max3_f32 v10,v57,v58,v59\n"
"v_max3_f32 v10,v10,v60,v61\n"
"v_cmp_le_f32 vcc,0x47800000,v10\n"
"s_cbranch_vccnz .Lstub_13\n"
".Lback_13:\n"
"s_waitcnt vmcnt(9) lgkmcnt(0)\n"
"s_barrier\n"
"s_lshl_b32 s58,s56,10\n"
"s_add_u32 s58,s58,73728\n"
"v_add_u32 v120,s58,v9\n"
"v_add_u32 v121,65536,v120\n"
"s_sub_u32 s59,s71,s70\n"
"v_mov_b32 v10,1.0\n"
TR "v[136:137],v2 offset:24576\n"
TR "v[138:139],v2 offset:30720\n"
"v_mfma_f32_16x16x4_f32 v[16:19],v10,v57,0\n"
TR "v[140:141],v3 offset:24576\n"
TR "v[142:143],v3 offset:30720\n"
TR "v[144:145],v4 offset:24576\n"
TR "v[146:147],v4 offset:30720\n"
TR "v[148:149],v5 offset:24576\n"
TR "v[150:151],v5 offset:30720\n"
"v_mfma_f32_16x16x4_f32 v[20:23],v10,v58,0\n"
TR "v[152:153],v2 offset:25600\n"
TR "v[154:155],v2 offset:31744\n"
TR "v[156:157],v3 offset:25600\n"
TR "v[158:159],v3 offset:31744\n"
TR "v[160:161],v4 offset:25600\n"
TR "v[162:163],v4 offset:31744\n"
"v_mfma_f32_16x16x4_f32 v[24:27],v10,v59,0\n"
TR "v[164:165],v5 offset:25600\n"
TR "v[166:167],v5 offset:31744\n"
TR "v[168:169],v2 offset:26624\n"
TR "v[170:171],v2 offset:32768\n"
TR "v[172:173],v3 offset:26624\n"
TR "v[174:175],v3 offset:32768\n"
"v_mfma_f32_16x16x4_f32 v[28:31],v10,v60,0\n"
TR "v[176:177],v4 offset:26624\n"
TR "v[178:179],v4 offset:32768\n"
TR "v[180:181],v5 offset:26624\n"
TR "v[182:183],v5 offset:32768\n"
"v_mfma_f32_16x16x4_f32 v[32:35],v10,v61,0\n"
"s_add_u32 m0,s65,49152\n"
"s_nop 0\n"
LD "v8,s[36:39],s73 offen offset:3072 nt lds\n"
"s_add_u32 m0,s65,50048\n"
"s_nop 0\n"
LD "v8,s[36:39],s73 offen offset:3200 nt lds\n"
"s_waitcnt lgkmcnt(15)\n"
MF "a[0:3],v[136:139],v[92:95],a[0:3]\n"
MF "a[20:23],v[140:143],v[92:95],a[20:23]\n"
"s_add_u32 m0,s65,50944\n"
"s_nop 0\n"
LD "v8,s[36:39],s73 offen offset:3328 nt lds\n"
"s_sub_u32 s74,s96,s16\n"
"s_add_u32 s74,s74,s71\n"
"s_add_u32 s20,s20,s59\n"
"s_mov_b32 s70,s71\n"
LD "v[232:235],v9,s[40:43],s75 offen sc1\n"
"v_rcp_f32 v57,v16\n"
"v_rcp_f32 v58,v20\n"
"v_rcp_f32 v59,v24\n"
"v_rcp_f32 v60,v28\n"
"v_rcp_f32 v61,v32\n"
MF "a[4:7],v[136:139],v[96:99],a[4:7]\n"
"v_mul_f32_e64 v52,-s13,v57\n"
"v_mul_f32_e64 v53,-s13,v58\n"
"v_mul_f32_e64 v54,-s13,v59\n"
"v_mul_f32_e64 v55,-s13,v60\n"
"v_mul_f32_e64 v56,-s13,v61\n"
AR "v72,a0\n"
AR "v73,a1\n"
AR "v74,a2\n"
MF "a[24:27],v[140:143],v[96:99],a[24:27]\n"
AR "v75,a3\n"
AR "v76,a20\n"
AR "v77,a21\n"
AR "v78,a22\n"
AR "v79,a23\n"
"v_pk_fma_f32 v[72:73],v[72:73],v[56:57],v[52:53] op_sel:[0,1,0] op_sel_hi:[1,1,0]\n"
"v_pk_fma_f32 v[74:75],v[74:75],v[56:57],v[52:53] op_sel:[0,1,0] op_sel_hi:[1,1,0]\n"
"v_pk_fma_f32 v[76:77],v[76:77],v[56:57],v[52:53] op_sel:[0,1,0] op_sel_hi:[1,1,0]\n"
"v_pk_fma_f32 v[78:79],v[78:79],v[56:57],v[52:53] op_sel:[0,1,0] op_sel_hi:[1,1,0]\n"
CV "v112,v72,v73\n"
CV "v113,v74,v75\n"
CV "v114,v76,v77\n"
CV "v115,v78,v79\n"
MF "a[8:11],v[136:139],v[100:103],a[8:11]\n"
"ds_read_b128 v[184:187],v120 offset:24576\n"
"ds_write_b128 v120,v[112:115] offset:24576\n"
LD "v[236:239],v9,s[40:43],s75 offen offset:1024 sc1\n"
AR "v80,a4\n"
AR "v81,a5\n"
AR "v82,a6\n"
MF "a[28:31],v[140:143],v[100:103],a[28:31]\n"
AR "v83,a7\n"
AR "v84,a24\n"
AR "v85,a25\n"
AR "v86,a26\n"
AR "v87,a27\n"
"v_pk_fma_f32 v[80:81],v[80:81],v[58:59],v[52:53] op_sel:[0,0,1] op_sel_hi:[1,0,1]\n"
"v_pk_fma_f32 v[82:83],v[82:83],v[58:59],v[52:53] op_sel:[0,0,1] op_sel_hi:[1,0,1]\n"
"v_pk_fma_f32 v[84:85],v[84:85],v[58:59],v[52:53] op_sel:[0,0,1] op_sel_hi:[1,0,1]\n"
"v_pk_fma_f32 v[86:87],v[86:87],v[58:59],v[52:53] op_sel:[0,0,1] op_sel_hi:[1,0,1]\n"
CV "v116,v80,v81\n"
CV "v117,v82,v83\n"
CV "v118,v84,v85\n"
CV "v119,v86,v87\n"
MF "a[12:15],v[136:139],v[104:107],a[12:15]\n"
"ds_read_b128 v[188:191],v120 offset:28672\n"
"ds_write_b128 v120,v[116:119] offset:28672\n"
AR "v72,a8\n"
AR "v73,a9\n"
AR "v74,a10\n"
MF "a[32:35],v[140:143],v[104:107],a[32:35]\n"
AR "v75,a11\n"
AR "v76,a28\n"
AR "v77,a29\n"
AR "v78,a30\n"
AR "v79,a31\n"
"v_pk_fma_f32 v[72:73],v[72:73],v[58:59],v[54:55] op_sel:[0,1,0] op_sel_hi:[1,1,0]\n"
"v_pk_fma_f32 v[74:75],v[74:75],v[58:59],v[54:55] op_sel:[0,1,0] op_sel_hi:[1,1,0]\n"
"v_pk_fma_f32 v[76:77],v[76:77],v[58:59],v[54:55] op_sel:[0,1,0] op_sel_hi:[1,1,0]\n"
"v_pk_fma_f32 v[78:79],v[78:79],v[58:59],v[54:55] op_sel:[0,1,0] op_sel_hi:[1,1,0]\n"
CV "v112,v72,v73\n"
CV "v113,v74,v75\n"
CV "v114,v76,v77\n"
CV "v115,v78,v79\n"
MF "a[16:19],v[136:139],v[108:111],a[16:19]\n"
"ds_read_b128 v[192:195],v120 offset:32768\n"
"ds_write_b128 v120,v[112:115] offset:32768\n"
"s_waitcnt lgkmcnt(0)\n"
"s_barrier\n"
LD "v[240:243],v9,s[40:43],s75 offen offset:2048 sc1\n"
AR "v80,a12\n"
AR "v81,a13\n"
AR "v82,a14\n"
MF "a[36:39],v[140:143],v[108:111],a[36:39]\n"
AR "v83,a15\n"
AR "v84,a32\n"
AR "v85,a33\n"
AR "v86,a34\n"
AR "v87,a35\n"
"v_pk_fma_f32 v[80:81],v[80:81],v[60:61],v[54:55] op_sel:[0,0,1] op_sel_hi:[1,0,1]\n"
"v_pk_fma_f32 v[82:83],v[82:83],v[60:61],v[54:55] op_sel:[0,0,1] op_sel_hi:[1,0,1]\n"
"v_pk_fma_f32 v[84:85],v[84:85],v[60:61],v[54:55] op_sel:[0,0,1] op_sel_hi:[1,0,1]\n"
"v_pk_fma_f32 v[86:87],v[86:87],v[60:61],v[54:55] op_sel:[0,0,1] op_sel_hi:[1,0,1]\n"
CV "v116,v80,v81\n"
CV "v117,v82,v83\n"
CV "v118,v84,v85\n"
CV "v119,v86,v87\n"
MF "a[40:43],v[144:147],v[92:95],a[40:43]\n"
"ds_read_b128 v[196:199],v120 offset:36864\n"
"ds_write_b128 v120,v[116:119] offset:36864\n"
LD "v[244:247],v9,s[40:43],s75 offen offset:3072 sc1\n"
AR "v72,a16\n"
AR "v73,a17\n"
AR "v74,a18\n"
MF "a[60:63],v[148:151],v[92:95],a[60:63]\n"
AR "v75,a19\n"
AR "v76,a36\n"
AR "v77,a37\n"
AR "v78,a38\n"
AR "v79,a39\n"
"v_pk_fma_f32 v[72:73],v[72:73],v[60:61],v[56:57] op_sel:[0,1,0] op_sel_hi:[1,1,0]\n"
"v_pk_fma_f32 v[74:75],v[74:75],v[60:61],v[56:57] op_sel:[0,1,0] op_sel_hi:[1,1,0]\n"
"v_pk_fma_f32 v[76:77],v[76:77],v[60:61],v[56:57] op_sel:[0,1,0] op_sel_hi:[1,1,0]\n"
"v_pk_fma_f32 v[78:79],v[78:79],v[60:61],v[56:57] op_sel:[0,1,0] op_sel_hi:[1,1,0]\n"
CV "v112,v72,v73\n"
CV "v113,v74,v75\n"
CV "v114,v76,v77\n"
CV "v115,v78,v79\n"
MF "a[44:47],v[144:147],v[96:99],a[44:47]\n"
"ds_read_b128 v[200:203],v120 offset:40960\n"
"ds_write_b128 v120,v[112:115] offset:40960\n"
AR "v80,a40\n"
AR "v81,a41\n"
AR "v82,a42\n"
MF "a[64:67],v[148:151],v[96:99],a[64:67]\n"
AR "v83,a43\n"
AR "v84,a60\n"
AR "v85,a61\n"
AR "v86,a62\n"
AR "v87,a63\n"
"v_pk_fma_f32 v[80:81],v[80:81],v[56:57],v[52:53] op_sel:[0,1,0] op_sel_hi:[1,1,0]\n"
"v_pk_fma_f32 v[82:83],v[82:83],v[56:57],v[52:53] op_sel:[0,1,0] op_sel_hi:[1,1,0]\n"
"v_pk_fma_f32 v[84:85],v[84:85],v[56:57],v[52:53] op_sel:[0,1,0] op_sel_hi:[1,1,0]\n"
"v_pk_fma_f32 v[86:87],v[86:87],v[56:57],v[52:53] op_sel:[0,1,0] op_sel_hi:[1,1,0]\n"
CV "v116,v80,v81\n"
CV "v117,v82,v83\n"
CV "v118,v84,v85\n"
CV "v119,v86,v87\n"
MF "a[48:51],v[144:147],v[100:103],a[48:51]\n"
"ds_read_b128 v[204:207],v120 offset:45056\n"
"ds_write_b128 v120,v[116:119] offset:45056\n"
"s_add_u32 s58,s75,4096\n"
LD "v[248:251],v9,s[40:43],s58 offen sc1\n"
AR "v72,a44\n"
AR "v73,a45\n"
AR "v74,a46\n"
MF "a[68:71],v[148:151],v[100:103],a[68:71]\n"
AR "v75,a47\n"
AR "v76,a64\n"
AR "v77,a65\n"
AR "v78,a66\n"
AR "v79,a67\n"
"v_pk_fma_f32 v[72:73],v[72:73],v[58:59],v[52:53] op_sel:[0,0,1] op_sel_hi:[1,0,1]\n"
"v_pk_fma_f32 v[74:75],v[74:75],v[58:59],v[52:53] op_sel:[0,0,1] op_sel_hi:[1,0,1]\n"
"v_pk_fma_f32 v[76:77],v[76:77],v[58:59],v[52:53] op_sel:[0,0,1] op_sel_hi:[1,0,1]\n"
"v_pk_fma_f32 v[78:79],v[78:79],v[58:59],v[52:53] op_sel:[0,0,1] op_sel_hi:[1,0,1]\n"
CV "v112,v72,v73\n"
CV "v113,v74,v75\n"
CV "v114,v76,v77\n"
CV "v115,v78,v79\n"
MF "a[52:55],v[144:147],v[104:107],a[52:55]\n"
"ds_read_b128 v[208:211],v120 offset:49152\n"
"ds_write_b128 v120,v[112:115] offset:49152\n"
LD "v[252:255],v9,s[40:43],s58 offen offset:1024 sc1\n"
AR "v80,a48\n"
AR "v81,a49\n"
AR "v82,a50\n"
MF "a[72:75],v[148:151],v[104:107],a[72:75]\n"
AR "v83,a51\n"
AR "v84,a68\n"
AR "v85,a69\n"
AR "v86,a70\n"
AR "v87,a71\n"
"v_pk_fma_f32 v[80:81],v[80:81],v[58:59],v[54:55] op_sel:[0,1,0] op_sel_hi:[1,1,0]\n"
"v_pk_fma_f32 v[82:83],v[82:83],v[58:59],v[54:55] op_sel:[0,1,0] op_sel_hi:[1,1,0]\n"
"v_pk_fma_f32 v[84:85],v[84:85],v[58:59],v[54:55] op_sel:[0,1,0] op_sel_hi:[1,1,0]\n"
"v_pk_fma_f32 v[86:87],v[86:87],v[58:59],v[54:55] op_sel:[0,1,0] op_sel_hi:[1,1,0]\n"
CV "v116,v80,v81\n"
CV "v117,v82,v83\n"
CV "v118,v84,v85\n"
CV "v119,v86,v87\n"
MF "a[56:59],v[144:147],v[108:111],a[56:59]\n"
"ds_read_b128 v[212:215],v120 offset:53248\n"
"ds_write_b128 v120,v[116:119] offset:53248\n"
AR "v72,a52\n"
AR "v73,a53\n"
AR "v74,a54\n"
MF "a[76:79],v[148:151],v[108:111],a[76:79]\n"
AR "v75,a55\n"
AR "v76,a72\n"
AR "v77,a73\n"
AR "v78,a74\n"
AR "v79,a75\n"
"v_pk_fma_f32 v[72:73],v[72:73],v[60:61],v[54:55] op_sel:[0,0,1] op_sel_hi:[1,0,1]\n"
"v_pk_fma_f32 v[74:75],v[74:75],v[60:61],v[54:55] op_sel:[0,0,1] op_sel_hi:[1,0,1]\n"
"v_pk_fma_f32 v[76:77],v[76:77],v[60:61],v[54:55] op_sel:[0,0,1] op_sel_hi:[1,0,1]\n"
"v_pk_fma_f32 v[78:79],v[78:79],v[60:61],v[54:55] op_sel:[0,0,1] op_sel_hi:[1,0,1]\n"
CV "v112,v72,v73\n"
CV "v113,v74,v75\n"
CV "v114,v76,v77\n"
CV "v115,v78,v79\n"
MF "a[80:83],v[152:155],v[92:95],a[80:83]\n"
"ds_read_b128 v[216:219],v120 offset:57344\n"
"ds_write_b128 v120,v[112:115] offset:57344\n"
AR "v80,a56\n"
AR "v81,a57\n"
AR "v82,a58\n"
MF "a[100:103],v[156:159],v[92:95],a[100:103]\n"
AR "v83,a59\n"
AR "v84,a76\n"
AR "v85,a77\n"
AR "v86,a78\n"
AR "v87,a79\n"
"v_pk_fma_f32 v[80:81],v[80:81],v[60:61],v[56:57] op_sel:[0,1,0] op_sel_hi:[1,1,0]\n"
"v_pk_fma_f32 v[82:83],v[82:83],v[60:61],v[56:57] op_sel:[0,1,0] op_sel_hi:[1,1,0]\n"
"v_pk_fma_f32 v[84:85],v[84:85],v[60:61],v[56:57] op_sel:[0,1,0] op_sel_hi:[1,1,0]\n"
"v_pk_fma_f32 v[86:87],v[86:87],v[60:61],v[56:57] op_sel:[0,1,0] op_sel_hi:[1,1,0]\n"
CV "v116,v80,v81\n"
CV "v117,v82,v83\n"
CV "v118,v84,v85\n"
CV "v119,v86,v87\n"
MF "a[84:87],v[152:155],v[96:99],a[84:87]\n"
"ds_read_b128 v[220:223],v120 offset:61440\n"
"ds_write_b128 v120,v[116:119] offset:61440\n"
AR "v72,a80\n"
AR "v73,a81\n"
AR "v74,a82\n"
MF "a[104:107],v[156:159],v[96:99],a[104:107]\n"
AR "v75,a83\n"
AR "v76,a100\n"
AR "v77,a101\n"
AR "v78,a102\n"
AR "v79,a103\n"
"v_pk_fma_f32 v[72:73],v[72:73],v[56:57],v[52:53] op_sel:[0,1,0] op_sel_hi:[1,1,0]\n"
"v_pk_fma_f32 v[74:75],v[74:75],v[56:57],v[52:53] op_sel:[0,1,0] op_sel_hi:[1,1,0]\n"
"v_pk_fma_f32 v[76:77],v[76:77],v[56:57],v[52:53] op_sel:[0,1,0] op_sel_hi:[1,1,0]\n"
"v_pk_fma_f32 v[78:79],v[78:79],v[56:57],v[52:53] op_sel:[0,1,0] op_sel_hi:[1,1,0]\n"
CV "v112,v72,v73\n"
CV "v113,v74,v75\n"
CV "v114,v76,v77\n"
CV "v115,v78,v79\n"
MF "a[88:91],v[152:155],v[100:103],a[88:91]\n"
"ds_read_b128 v[224:227],v121\n"
"ds_write_b128 v121,v[112:115]\n"
AR "v80,a84\n"
AR "v81,a85\n"
AR "v82,a86\n"
MF "a[108:111],v[156:159],v[100:103],a[108:111]\n"
AR "v83,a87\n"
AR "v84,a104\n"
AR "v85,a105\n"
AR "v86,a106\n"
AR "v87,a107\n"
"v_pk_fma_f32 v[80:81],v[80:81],v[58:59],v[52:53] op_sel:[0,0,1] op_sel_hi:[1,0,1]\n"
"v_pk_fma_f32 v[82:83],v[82:83],v[58:59],v[52:53] op_sel:[0,0,1] op_sel_hi:[1,0,1]\n"
"v_pk_fma_f32 v[84:85],v[84:85],v[58:59],v[52:53] op_sel:[0,0,1] op_sel_hi:[1,0,1]\n"
"v_pk_fma_f32 v[86:87],v[86:87],v[58:59],v[52:53] op_sel:[0,0,1] op_sel_hi:[1,0,1]\n"
CV "v116,v80,v81\n"
CV "v117,v82,v83\n"
CV "v118,v84,v85\n"
CV "v119,v86,v87\n"
MF "a[92:95],v[152:155],v[104:107],a[92:95]\n"
".Lpg17d:\n"
"s_cbranch_execz .Lisr2\n"
"ds_read_b128 v[228:231],v121 offset:4096\n"
"ds_write_b128 v121,v[116:119] offset:4096\n"
AR "v72,a88\n"
AR "v73,a89\n"
AR "v74,a90\n"
MF "a[112:115],v[156:159],v[104:107],a[112:115]\n"
AR "v75,a91\n"
AR "v76,a108\n"
AR "v77,a109\n"
"s_nop 0\n"
AR "v78,a110\n"
AR "v79,a111\n"
"v_pk_fma_f32 v[72:73],v[72:73],v[58:59],v[54:55] op_sel:[0,1,0] op_sel_hi:[1,1,0]\n"
"v_pk_fma_f32 v[74:75],v[74:75],v[58:59],v[54:55] op_sel:[0,1,0] op_sel_hi:[1,1,0]\n"
"v_pk_fma_f32 v[76:77],v[76:77],v[58:59],v[54:55] op_sel:[0,1,0] op_sel_hi:[1,1,0]\n"
"v_pk_fma_f32 v[78:79],v[78:79],v[58:59],v[54:55] op_sel:[0,1,0] op_sel_hi:[1,1,0]\n"
CV "v112,v72,v73\n"
CV "v113,v74,v75\n"
CV "v114,v76,v77\n"
CV "v115,v78,v79\n"
MF "a[96:99],v[152:155],v[108:111],a[96:99]\n"
"ds_read_b128 v[136:139],v121 offset:8192\n"
"ds_write_b128 v121,v[112:115] offset:8192\n"
AR "v80,a92\n"
AR "v81,a93\n"
AR "v82,a94\n"
MF "a[116:119],v[156:159],v[108:111],a[116:119]\n"
AR "v83,a95\n"
AR "v84,a112\n"
AR "v85,a113\n"
AR "v86,a114\n"
AR "v87,a115\n"
"v_pk_fma_f32 v[80:81],v[80:81],v[60:61],v[54:55] op_sel:[0,0,1] op_sel_hi:[1,0,1]\n"
"v_pk_fma_f32 v[82:83],v[82:83],v[60:61],v[54:55] op_sel:[0,0,1] op_sel_hi:[1,0,1]\n"
"v_pk_fma_f32 v[84:85],v[84:85],v[60:61],v[54:55] op_sel:[0,0,1] op_sel_hi:[1,0,1]\n"
"v_pk_fma_f32 v[86:87],v[86:87],v[60:61],v[54:55] op_sel:[0,0,1] op_sel_hi:[1,0,1]\n"
CV "v116,v80,v81\n"
CV "v117,v82,v83\n"
CV "v118,v84,v85\n"
CV "v119,v86,v87\n"
MF "a[120:123],v[160:163],v[92:95],a[120:123]\n"
"ds_read_b128 v[140:143],v121 offset:12288\n"
"ds_write_b128 v121,v[116:119] offset:12288\n"
AR "v72,a96\n"
AR "v73,a97\n"
AR "v74,a98\n"
MF "a[140:143],v[164:167],v[92:95],a[140:143]\n"
AR "v75,a99\n"
AR "v76,a116\n"
AR "v77,a117\n"
AR "v78,a118\n"
AR "v79,a119\n"
"v_pk_fma_f32 v[72:73],v[72:73],v[60:61],v[56:57] op_sel:[0,1,0] op_sel_hi:[1,1,0]\n"
"v_pk_fma_f32 v[74:75],v[74:75],v[60:61],v[56:57] op_sel:[0,1,0] op_sel_hi:[1,1,0]\n"
"v_pk_fma_f32 v[76:77],v[76:77],v[60:61],v[56:57] op_sel:[0,1,0] op_sel_hi:[1,1,0]\n"
"v_pk_fma_f32 v[78:79],v[78:79],v[60:61],v[56:57] op_sel:[0,1,0] op_sel_hi:[1,1,0]\n"
CV "v112,v72,v73\n"
CV "v113,v74,v75\n"
CV "v114,v76,v77\n"
CV "v115,v78,v79\n"
MF "a[124:127],v[160:163],v[96:99],a[124:127]\n"
"ds_read_b128 v[144:147],v121 offset:16384\n"
"ds_write_b128 v121,v[112:115] offset:16384\n"
AR "v80,a120\n"
AR "v81,a121\n"
AR "v82,a122\n"
MF "a[144:147],v[164:167],v[96:99],a[144:147]\n"
AR "v83,a123\n"
AR "v84,a140\n"
AR "v85,a141\n"
AR "v86,a142\n"
AR "v87,a143\n"
"v_pk_fma_f32 v[80:81],v[80:81],v[56:57],v[52:53] op_sel:[0,1,0] op_sel_hi:[1,1,0]\n"
"v_pk_fma_f32 v[82:83],v[82:83],v[56:57],v[52:53] op_sel:[0,1,0] op_sel_hi:[1,1,0]\n"
"v_pk_fma_f32 v[84:85],v[84:85],v[56:57],v[52:53] op_sel:[0,1,0] op_sel_hi:[1,1,0]\n"
"v_pk_fma_f32 v[86:87],v[86:87],v[56:57],v[52:53] op_sel:[0,1,0] op_sel_hi:[1,1,0]\n"
CV "v116,v80,v81\n"
CV "v117,v82,v83\n"
CV "v118,v84,v85\n"
CV "v119,v86,v87\n"
MF "a[128:131],v[160:163],v[100:103],a[128:131]\n"
"ds_read_b128 v[148:151],v121 offset:20480\n"
"ds_write_b128 v121,v[116:119] offset:20480\n"
AR "v72,a124\n"
AR "v73,a125\n"
AR "v74,a126\n"
MF "a[148:151],v[164:167],v[100:103],a[148:151]\n"
AR "v75,a127\n"
AR "v76,a144\n"
AR "v77,a145\n"
AR "v78,a146\n"
AR "v79,a147\n"
"v_pk_fma_f32 v[72:73],v[72:73],v[58:59],v[52:53] op_sel:[0,0,1] op_sel_hi:[1,0,1]\n"
"v_pk_fma_f32 v[74:75],v[74:75],v[58:59],v[52:53] op_sel:[0,0,1] op_sel_hi:[1,0,1]\n"
"v_pk_fma_f32 v[76:77],v[76:77],v[58:59],v[52:53] op_sel:[0,0,1] op_sel_hi:[1,0,1]\n"
"v_pk_fma_f32 v[78:79],v[78:79],v[58:59],v[52:53] op_sel:[0,0,1] op_sel_hi:[1,0,1]\n"
CV "v112,v72,v73\n"
CV "v113,v74,v75\n"
CV "v114,v76,v77\n"
CV "v115,v78,v79\n"
MF "a[132:135],v[160:163],v[104:107],a[132:135]\n"
"ds_write_b128 v120,v[112:115]\n"
AR "v80,a128\n"
AR "v81,a129\n"
AR "v82,a130\n"
MF "a[152:155],v[164:167],v[104:107],a[152:155]\n"
AR "v83,a131\n"
AR "v84,a148\n"
AR "v85,a149\n"
AR "v86,a150\n"
AR "v87,a151\n"
"v_pk_fma_f32 v[80:81],v[80:81],v[58:59],v[54:55] op_sel:[0,1,0] op_sel_hi:[1,1,0]\n"
"v_pk_fma_f32 v[82:83],v[82:83],v[58:59],v[54:55] op_sel:[0,1,0] op_sel_hi:[1,1,0]\n"
"v_pk_fma_f32 v[84:85],v[84:85],v[58:59],v[54:55] op_sel:[0,1,0] op_sel_hi:[1,1,0]\n"
"v_pk_fma_f32 v[86:87],v[86:87],v[58:59],v[54:55] op_sel:[0,1,0] op_sel_hi:[1,1,0]\n"
CV "v116,v80,v81\n"
CV "v117,v82,v83\n"
CV "v118,v84,v85\n"
CV "v119,v86,v87\n"
MF "a[136:139],v[160:163],v[108:111],a[136:139]\n"
"ds_write_b128 v120,v[116:119] offset:4096\n"
AR "v72,a132\n"
AR "v73,a133\n"
AR "v74,a134\n"
MF "a[156:159],v[164:167],v[108:111],a[156:159]\n"
AR "v75,a135\n"
AR "v76,a152\n"
AR "v77,a153\n"
AR "v78,a154\n"
AR "v79,a155\n"
"v_pk_fma_f32 v[72:73],v[72:73],v[60:61],v[54:55] op_sel:[0,0,1] op_sel_hi:[1,0,1]\n"
"v_pk_fma_f32 v[74:75],v[74:75],v[60:61],v[54:55] op_sel:[0,0,1] op_sel_hi:[1,0,1]\n"
"v_pk_fma_f32 v[76:77],v[76:77],v[60:61],v[54:55] op_sel:[0,0,1] op_sel_hi:[1,0,1]\n"
"v_pk_fma_f32 v[78:79],v[78:79],v[60:61],v[54:55] op_sel:[0,0,1] op_sel_hi:[1,0,1]\n"
CV "v112,v72,v73\n"
CV "v113,v74,v75\n"
CV "v114,v76,v77\n"
CV "v115,v78,v79\n"
MF "a[160:163],v[168:171],v[92:95],a[160:163]\n"
"ds_write_b128 v120,v[112:115] offset:8192\n"
AR "v80,a136\n"
AR "v81,a137\n"
AR "v82,a138\n"
MF "a[180:183],v[172:175],v[92:95],a[180:183]\n"
AR "v83,a139\n"
AR "v84,a156\n"
AR "v85,a157\n"
AR "v86,a158\n"
AR "v87,a159\n"
"v_pk_fma_f32 v[80:81],v[80:81],v[60:61],v[56:57] op_sel:[0,1,0] op_sel_hi:[1,1,0]\n"
"v_pk_fma_f32 v[82:83],v[82:83],v[60:61],v[56:57] op_sel:[0,1,0] op_sel_hi:[1,1,0]\n"
"v_pk_fma_f32 v[84:85],v[84:85],v[60:61],v[56:57] op_sel:[0,1,0] op_sel_hi:[1,1,0]\n"
"v_pk_fma_f32 v[86:87],v[86:87],v[60:61],v[56:57] op_sel:[0,1,0] op_sel_hi:[1,1,0]\n"
CV "v116,v80,v81\n"
CV "v117,v82,v83\n"
CV "v118,v84,v85\n"
CV "v119,v86,v87\n"
MF "a[164:167],v[168:171],v[96:99],a[164:167]\n"
"ds_write_b128 v120,v[116:119] offset:12288\n"
AR "v72,a160\n"
AR "v73,a161\n"
AR "v74,a162\n"
MF "a[184:187],v[172:175],v[96:99],a[184:187]\n"
AR "v75,a163\n"
AR "v76,a180\n"
AR "v77,a181\n"
AR "v78,a182\n"
AR "v79,a183\n"
"v_pk_fma_f32 v[72:73],v[72:73],v[56:57],v[52:53] op_sel:[0,1,0] op_sel_hi:[1,1,0]\n"
"v_pk_fma_f32 v[74:75],v[74:75],v[56:57],v[52:53] op_sel:[0,1,0] op_sel_hi:[1,1,0]\n"
"v_pk_fma_f32 v[76:77],v[76:77],v[56:57],v[52:53] op_sel:[0,1,0] op_sel_hi:[1,1,0]\n"
"v_pk_fma_f32 v[78:79],v[78:79],v[56:57],v[52:53] op_sel:[0,1,0] op_sel_hi:[1,1,0]\n"
CV "v112,v72,v73\n"
CV "v113,v74,v75\n"
CV "v114,v76,v77\n"
CV "v115,v78,v79\n"
MF "a[168:171],v[168:171],v[100:103],a[168:171]\n"
"ds_write_b128 v120,v[112:115] offset:16384\n"
AR "v80,a164\n"
AR "v81,a165\n"
AR "v82,a166\n"
MF "a[188:191],v[172:175],v[100:103],a[188:191]\n"
AR "v83,a167\n"
AR "v84,a184\n"
AR "v85,a185\n"
AR "v86,a186\n"
AR "v87,a187\n"
"v_pk_fma_f32 v[80:81],v[80:81],v[58:59],v[52:53] op_sel:[0,0,1] op_sel_hi:[1,0,1]\n"
"v_pk_fma_f32 v[82:83],v[82:83],v[58:59],v[52:53] op_sel:[0,0,1] op_sel_hi:[1,0,1]\n"
"v_pk_fma_f32 v[84:85],v[84:85],v[58:59],v[52:53] op_sel:[0,0,1] op_sel_hi:[1,0,1]\n"
"v_pk_fma_f32 v[86:87],v[86:87],v[58:59],v[52:53] op_sel:[0,0,1] op_sel_hi:[1,0,1]\n"
CV "v116,v80,v81\n"
CV "v117,v82,v83\n"
CV "v118,v84,v85\n"
CV "v119,v86,v87\n"
MF "a[172:175],v[168:171],v[104:107],a[172:175]\n"
"ds_write_b128 v120,v[116:119] offset:20480\n"
AR "v72,a168\n"
AR "v73,a169\n"
AR "v74,a170\n"
MF "a[192:195],v[172:175],v[104:107],a[192:195]\n"
AR "v75,a171\n"
AR "v76,a188\n"
AR "v77,a189\n"
AR "v78,a190\n"
AR "v79,a191\n"
"v_pk_fma_f32 v[72:73],v[72:73],v[58:59],v[54:55] op_sel:[0,1,0] op_sel_hi:[1,1,0]\n"
"v_pk_fma_f32 v[74:75],v[74:75],v[58:59],v[54:55] op_sel:[0,1,0] op_sel_hi:[1,1,0]\n"
"v_pk_fma_f32 v[76:77],v[76:77],v[58:59],v[54:55] op_sel:[0,1,0] op_sel_hi:[1,1,0]\n"
"v_pk_fma_f32 v[78:79],v[78:79],v[58:59],v[54:55] op_sel:[0,1,0] op_sel_hi:[1,1,0]\n"
CV "v112,v72,v73\n"
CV "v113,v74,v75\n"
CV "v114,v76,v77\n"
CV "v115,v78,v79\n"
MF "a[176:179],v[168:171],v[108:111],a[176:179]\n"
AR "v152,a240\n"
AW "a240,v112\n"
AR "v153,a241\n"
AW "a241,v113\n"
AR "v154,a242\n"
AW "a242,v114\n"
AR "v155,a243\n"
AW "a243,v115\n"
AR "v80,a172\n"
AR "v81,a173\n"
AR "v82,a174\n"
MF "a[196:199],v[172:175],v[108:111],a[196:199]\n"
AR "v83,a175\n"
AR "v84,a192\n"
AR "v85,a193\n"
AR "v86,a194\n"
AR "v87,a195\n"
"v_pk_fma_f32 v[80:81],v[80:81],v[60:61],v[54:55] op_sel:[0,0,1] op_sel_hi:[1,0,1]\n"
"v_pk_fma_f32 v[82:83],v[82:83],v[60:61],v[54:55] op_sel:[0,0,1] op_sel_hi:[1,0,1]\n"
"v_pk_fma_f32 v[84:85],v[84:85],v[60:61],v[54:55] op_sel:[0,0,1] op_sel_hi:[1,0,1]\n"
"v_pk_fma_f32 v[86:87],v[86:87],v[60:61],v[54:55] op_sel:[0,0,1] op_sel_hi:[1,0,1]\n"
CV "v116,v80,v81\n"
CV "v117,v82,v83\n"
CV "v118,v84,v85\n"
CV "v119,v86,v87\n"
MF "a[200:203],v[176:179],v[92:95],a[200:203]\n"
AR "v156,a244\n"
AW "a244,v116\n"
AR "v157,a245\n"
AW "a245,v117\n"
AR "v158,a246\n"
AW "a246,v118\n"
AR "v159,a247\n"
AW "a247,v119\n"
AR "v72,a176\n"
AR "v73,a177\n"
AR "v74,a178\n"
MF "a[220:223],v[180:183],v[92:95],a[220:223]\n"
AR "v75,a179\n"
AR "v76,a196\n"
AR "v77,a197\n"
AR "v78,a198\n"
AR "v79,a199\n"
"v_pk_fma_f32 v[72:73],v[72:73],v[60:61],v[56:57] op_sel:[0,1,0] op_sel_hi:[1,1,0]\n"
"v_pk_fma_f32 v[74:75],v[74:75],v[60:61],v[56:57] op_sel:[0,1,0] op_sel_hi:[1,1,0]\n"
"v_pk_fma_f32 v[76:77],v[76:77],v[60:61],v[56:57] op_sel:[0,1,0] op_sel_hi:[1,1,0]\n"
"v_pk_fma_f32 v[78:79],v[78:79],v[60:61],v[56:57] op_sel:[0,1,0] op_sel_hi:[1,1,0]\n"
CV "v112,v72,v73\n"
CV "v113,v74,v75\n"
CV "v114,v76,v77\n"
CV "v115,v78,v79\n"
MF "a[204:207],v[176:179],v[96:99],a[204:207]\n"
AR "v160,a248\n"
AW "a248,v112\n"
AR "v161,a249\n"
AW "a249,v113\n"
AR "v162,a250\n"
AW "a250,v114\n"
AR "v163,a251\n"
AW "a251,v115\n"
AR "v80,a200\n"
AR "v81,a201\n"
AR "v82,a202\n"
MF "a[224:227],v[180:183],v[96:99],a[224:227]\n"
AR "v83,a203\n"
AR "v84,a220\n"
AR "v85,a221\n"
AR "v86,a222\n"
AR "v87,a223\n"
"v_pk_fma_f32 v[80:81],v[80:81],v[56:57],v[52:53] op_sel:[0,1,0] op_sel_hi:[1,1,0]\n"
"v_pk_fma_f32 v[82:83],v[82:83],v[56:57],v[52:53] op_sel:[0,1,0] op_sel_hi:[1,1,0]\n"
"v_pk_fma_f32 v[84:85],v[84:85],v[56:57],v[52:53] op_sel:[0,1,0] op_sel_hi:[1,1,0]\n"
"v_pk_fma_f32 v[86:87],v[86:87],v[56:57],v[52:53] op_sel:[0,1,0] op_sel_hi:[1,1,0]\n"
CV "v116,v80,v81\n"
CV "v117,v82,v83\n"
CV "v118,v84,v85\n"
CV "v119,v86,v87\n"
MF "a[208:211],v[176:179],v[100:103],a[208:211]\n"
"v_mov_b32 v164,v62\n"
"v_mov_b32 v62,v116\n"
"v_mov_b32 v165,v63\n"
"v_mov_b32 v63,v117\n"
"v_mov_b32 v166,v64\n"
"v_mov_b32 v64,v118\n"
"v_mov_b32 v167,v65\n"
"v_mov_b32 v65,v119\n"
AR "v72,a204\n"
AR "v73,a205\n"
AR "v74,a206\n"
MF "a[228:231],v[180:183],v[100:103],a[228:231]\n"
AR "v75,a207\n"
AR "v76,a224\n"
AR "v77,a225\n"
AR "v78,a226\n"
AR "v79,a227\n"
"v_pk_fma_f32 v[72:73],v[72:73],v[58:59],v[52:53] op_sel:[0,0,1] op_sel_hi:[1,0,1]\n"
"v_pk_fma_f32 v[74:75],v[74:75],v[58:59],v[52:53] op_sel:[0,0,1] op_sel_hi:[1,0,1]\n"
"v_pk_fma_f32 v[76:77],v[76:77],v[58:59],v[52:53] op_sel:[0,0,1] op_sel_hi:[1,0,1]\n"
"v_pk_fma_f32 v[78:79],v[78:79],v[58:59],v[52:53] op_sel:[0,0,1] op_sel_hi:[1,0,1]\n"
CV "v112,v72,v73\n"
CV "v113,v74,v75\n"
CV "v114,v76,v77\n"
CV "v115,v78,v79\n"
MF "a[212:215],v[176:179],v[104:107],a[212:215]\n"
"v_mov_b32 v168,v66\n"
"v_mov_b32 v66,v112\n"
"v_mov_b32 v169,v67\n"
"v_mov_b32 v67,v113\n"
"v_mov_b32 v170,v68\n"
"v_mov_b32 v68,v114\n"
"v_mov_b32 v171,v69\n"
"v_mov_b32 v69,v115\n"
AR "v80,a208\n"
AR "v81,a209\n"
AR "v82,a210\n"
MF "a[232:235],v[180:183],v[104:107],a[232:235]\n"
AR "v83,a211\n"
AR "v84,a228\n"
AR "v85,a229\n"
AR "v86,a230\n"
AR "v87,a231\n"
"v_pk_fma_f32 v[80:81],v[80:81],v[58:59],v[54:55] op_sel:[0,1,0] op_sel_hi:[1,1,0]\n"
"v_pk_fma_f32 v[82:83],v[82:83],v[58:59],v[54:55] op_sel:[0,1,0] op_sel_hi:[1,1,0]\n"
"v_pk_fma_f32 v[84:85],v[84:85],v[58:59],v[54:55] op_sel:[0,1,0] op_sel_hi:[1,1,0]\n"
"v_pk_fma_f32 v[86:87],v[86:87],v[58:59],v[54:55] op_sel:[0,1,0] op_sel_hi:[1,1,0]\n"
CV "v116,v80,v81\n"
CV "v117,v82,v83\n"
CV "v118,v84,v85\n"
CV "v119,v86,v87\n"
MF "a[216:219],v[176:179],v[108:111],a[216:219]\n"
"v_mov_b32 v172,v70\n"
"v_mov_b32 v70,v116\n"
"v_mov_b32 v173,v71\n"
"v_mov_b32 v71,v117\n"
"v_mov_b32 v174,v132\n"
"v_mov_b32 v132,v118\n"
"v_mov_b32 v175,v133\n"
"v_mov_b32 v133,v119\n"
AR "v72,a212\n"
AR "v73,a213\n"
AR "v74,a214\n"
MF "a[236:239],v[180:183],v[108:111],a[236:239]\n"
AR "v75,a215\n"
AR "v76,a232\n"
AR "v77,a233\n"
AR "v78,a234\n"
AR "v79,a235\n"
"v_pk_fma_f32 v[72:73],v[72:73],v[60:61],v[54:55] op_sel:[0,0,1] op_sel_hi:[1,0,1]\n"
"v_pk_fma_f32 v[74:75],v[74:75],v[60:61],v[54:55] op_sel:[0,0,1] op_sel_hi:[1,0,1]\n"
"v_pk_fma_f32 v[76:77],v[76:77],v[60:61],v[54:55] op_sel:[0,0,1] op_sel_hi:[1,0,1]\n"
"v_pk_fma_f32 v[78:79],v[78:79],v[60:61],v[54:55] op_sel:[0,0,1] op_sel_hi:[1,0,1]\n"
CV "v112,v72,v73\n"
CV "v113,v74,v75\n"
CV "v114,v76,v77\n"
CV "v115,v78,v79\n"
"s_waitcnt vmcnt(0) lgkmcnt(0)\n"
"s_barrier\n"
"ds_read_b128 v[92:95],v0\n"
"ds_read_b128 v[96:99],v1\n"
"ds_read_b128 v[100:103],v0 offset:1024\n"
"v_mov_b32 v176,v134\n"
"v_mov_b32 v134,v112\n"
"v_mov_b32 v177,v135\n"
"v_mov_b32 v135,v113\n"
"v_mov_b32 v178,v6\n"
"v_mov_b32 v6,v114\n"
"v_mov_b32 v179,v13\n"
"v_mov_b32 v13,v115\n"
AR "v80,a216\n"
AR "v81,a217\n"
AR "v82,a218\n"
AR "v83,a219\n"
AR "v84,a236\n"
AR "v85,a237\n"
AR "v86,a238\n"
AR "v87,a239\n"
"v_pk_fma_f32 v[80:81],v[80:81],v[60:61],v[56:57] op_sel:[0,1,0] op_sel_hi:[1,1,0]\n"
"v_pk_fma_f32 v[82:83],v[82:83],v[60:61],v[56:57] op_sel:[0,1,0] op_sel_hi:[1,1,0]\n"
"v_pk_fma_f32 v[84:85],v[84:85],v[60:61],v[56:57] op_sel:[0,1,0] op_sel_hi:[1,1,0]\n"
"v_pk_fma_f32 v[86:87],v[86:87],v[60:61],v[56:57] op_sel:[0,1,0] op_sel_hi:[1,1,0]\n"
CV "v116,v80,v81\n"
CV "v117,v82,v83\n"
CV "v118,v84,v85\n"
CV "v119,v86,v87\n"
"v_mov_b32 v180,v14\n"
"v_mov_b32 v14,v116\n"
"v_mov_b32 v181,v15\n"
"v_mov_b32 v15,v117\n"
AR "v182,a254\n"
AW "a254,v118\n"
AR "v183,a255\n"
AW "a255,v119\n"
"s_mov_b32 s13,s11\n"
"v_subrev_u32 v2,49152,v2\n"
"v_subrev_u32 v3,49152,v3\n"
"v_subrev_u32 v4,49152,v4\n"
"v_subrev_u32 v5,49152,v5\n"
"s_mov_b32 s57,2\n"
"s_mov_b32 s69,1\n"
"s_mov_b32 s61,2\n"
"s_mov_b32 s97,0\n"
"s_mov_b32 s95,0x47800000\n"
"s_mov_b32 s77,0x47800000\n"
"s_branch .Latt2\n"
".LpgattB_f:\n"
"s_cbranch_execz .Lisr3\n"
".LattB:\n"
AR "v2,a245\n"
"v_xor_b32 v3,32,v2\n"
"v_xor_b32_e64 v4,64,v2\n"
"v_xor_b32 v5,96,v2\n"
"s_mov_b32 s61,2\n"
".Latt2:\n"
"s_cmp_eq_u32 s57,1\n"
"s_cbranch_scc1 .Lt0k\n"
".Lt0n:\n"
"s_waitcnt lgkmcnt(2)\n"
MF "v[72:75],v[92:95],v[136:139],0\n"
"ds_read_b128 v[104:107],v1 offset:1024\n"
MF "v[76:79],v[92:95],v[140:143],0\n"
MF "v[80:83],v[92:95],v[144:147],0\n"
MF "v[84:87],v[92:95],v[148:151],0\n"
MF "v[88:91],v[92:95],v[152:155],0\n"
"s_waitcnt lgkmcnt(2)\n"
"ds_read_b128 v[92:95],v0 offset:2048\n"
MF "v[72:75],v[96:99],v[156:159],v[72:75]\n"
MF "v[76:79],v[96:99],v[160:163],v[76:79]\n"
MF "v[80:83],v[96:99],v[164:167],v[80:83]\n"
MF "v[84:87],v[96:99],v[168:171],v[84:87]\n"
MF "v[88:91],v[96:99],v[172:175],v[88:91]\n"
"s_waitcnt lgkmcnt(2)\n"
"ds_read_b128 v[96:99],v1 offset:2048\n"
MF "v[72:75],v[100:103],v[176:179],v[72:75]\n"
MF "v[76:79],v[100:103],v[180:183],v[76:79]\n"
MF "v[80:83],v[100:103],v[184:187],v[80:83]\n"
MF "v[84:87],v[100:103],v[188:191],v[84:87]\n"
MF "v[88:91],v[100:103],v[192:195],v[88:91]\n"
"s_waitcnt lgkmcnt(2)\n"
"ds_read_b128 v[100:103],v0 offset:6144\n"
MF "v[72:75],v[104:107],v[196:199],v[72:75]\n"
MF "v[76:79],v[104:107],v[200:203],v[76:79]\n"
MF "v[80:83],v[104:107],v[204:207],v[80:83]\n"
MF "v[84:87],v[104:107],v[208:211],v[84:87]\n"
MF "v[88:91],v[104:107],v[212:215],v[88:91]\n"
"s_waitcnt lgkmcnt(2)\n"
"ds_read_b128 v[104:107],v1 offset:6144\n"
MF "v[72:75],v[92:95],v[216:219],v[72:75]\n"
MF "v[76:79],v[92:95],v[220:223],v[76:79]\n"
MF "v[80:83],v[92:95],v[224:227],v[80:83]\n"
MF "v[84:87],v[92:95],v[228:231],v[84:87]\n"
MF "v[88:91],v[92:95],v[232:235],v[88:91]\n"
"s_waitcnt lgkmcnt(2)\n"
"ds_read_b128 v[92:95],v0 offset:7168\n"
MF "v[72:75],v[96:99],v[236:239],v[72:75]\n"
MF "v[76:79],v[96:99],v[240:243],v[76:79]\n"
MF "v[80:83],v[96:99],v[244:247],v[80:83]\n"
MF "v[84:87],v[96:99],v[248:251],v[84:87]\n"
MF "v[88:91],v[96:99],v[252:255],v[88:91]\n"
"s_waitcnt lgkmcnt(2)\n"
"ds_read_b128 v[96:99],v1 offset:7168\n"
MF "v[112:115],v[100:103],v[136:139],0\n"
MF "v[116:119],v[100:103],v[140:143],0\n"
MF "v[120:123],v[100:103],v[144:147],0\n"
MF "v[124:127],v[100:103],v[148:151],0\n"
MF "v[128:131],v[100:103],v[152:155],0\n"
"s_waitcnt lgkmcnt(2)\n"
"ds_read_b128 v[100:103],v0 offset:8192\n"
MF "v[112:115],v[104:107],v[156:159],v[112:115]\n"
"v_max3_f32 v52,v72,v73,v74\n"
"v_max_f32 v52,v52,v75\n"
MF "v[116:119],v[104:107],v[160:163],v[116:119]\n"
"v_max3_f32 v53,v76,v77,v78\n"
"v_max_f32 v53,v53,v79\n"
MF "v[120:123],v[104:107],v[164:167],v[120:123]\n"
"v_max3_f32 v54,v80,v81,v82\n"
"v_max_f32 v54,v54,v83\n"
MF "v[124:127],v[104:107],v[168:171],v[124:127]\n"
"v_max3_f32 v55,v84,v85,v86\n"
"v_max_f32 v55,v55,v87\n"
MF "v[128:131],v[104:107],v[172:175],v[128:131]\n"
"s_waitcnt lgkmcnt(2)\n"
"ds_read_b128 v[104:107],v1 offset:8192\n"
"v_max3_f32 v56,v88,v89,v90\n"
MF "v[112:115],v[92:95],v[176:179],v[112:115]\n"
"v_max_f32 v56,v56,v91\n"
MF "v[116:119],v[92:95],v[180:183],v[116:119]\n"
MF "v[120:123],v[92:95],v[184:187],v[120:123]\n"
MF "v[124:127],v[92:95],v[188:191],v[124:127]\n"
MF "v[128:131],v[92:95],v[192:195],v[128:131]\n"
"s_waitcnt lgkmcnt(2)\n"
MF "v[112:115],v[96:99],v[196:199],v[112:115]\n"
"s_waitcnt lgkmcnt(0)\n"
MF "v[116:119],v[96:99],v[200:203],v[116:119]\n"
"s_barrier\n"
MF "v[120:123],v[96:99],v[204:207],v[120:123]\n"
"s_add_u32 s72,s72,6144\n"
MF "v[124:127],v[96:99],v[208:211],v[124:127]\n"
"s_cmp_eq_u32 s72,s17\n"
"s_cselect_b32 s72,s16,s72\n"
"s_add_u32 m0,s64,0\n"
MF "v[128:131],v[96:99],v[212:215],v[128:131]\n"
LD "v7,s[32:35],s72 offen nt lds\n"
MF "v[112:115],v[100:103],v[216:219],v[112:115]\n"
"s_add_u32 m0,s64,896\n"
MF "v[116:119],v[100:103],v[220:223],v[116:119]\n"
LD "v7,s[32:35],s72 offen offset:128 nt lds\n"
MF "v[120:123],v[100:103],v[224:227],v[120:123]\n"
"s_add_u32 m0,s64,1792\n"
MF "v[124:127],v[100:103],v[228:231],v[124:127]\n"
LD "v7,s[32:35],s72 offen offset:256 nt lds\n"
MF "v[128:131],v[100:103],v[232:235],v[128:131]\n"
"s_add_u32 s72,s72,6144\n"
"s_add_u32 m0,s64,6144\n"
MF "v[112:115],v[104:107],v[236:239],v[112:115]\n"
LD "v7,s[32:35],s72 offen nt lds\n"
MF "v[116:119],v[104:107],v[240:243],v[116:119]\n"
"s_add_u32 m0,s64,7040\n"
MF "v[120:123],v[104:107],v[244:247],v[120:123]\n"
LD "v7,s[32:35],s72 offen offset:128 nt lds\n"
MF "v[124:127],v[104:107],v[248:251],v[124:127]\n"
"s_add_u32 m0,s64,7936\n"
MF "v[128:131],v[104:107],v[252:255],v[128:131]\n"
LD "v7,s[32:35],s72 offen offset:256 nt lds\n"
"s_branch .Lt0e\n"
".Lpgt0k_f:\n"
"s_cbranch_execz .Lisr3\n"
".Lt0k:\n"
"s_lshl_b32 s58,s56,10\n"
"s_add_u32 s58,s58,73728\n"
"v_add_u32 v32,s58,v9\n"
"v_add_u32 v33,65536,v32\n"
"s_waitcnt lgkmcnt(2)\n"
MF "v[72:75],v[92:95],v[136:139],0\n"
"ds_read_b128 v[104:107],v1 offset:1024\n"
AR "v62,a20\n"
MF "v[76:79],v[92:95],v[140:143],0\n"
"ds_write_b128 v32,a[0:3] offset:24576\n"
MF "v[80:83],v[92:95],v[144:147],0\n"
"ds_write_b128 v32,a[4:7] offset:28672\n"
MF "v[84:87],v[92:95],v[148:151],0\n"
".Lpg18f:\n"
"s_cbranch_execz .Lisr3\n"
AR "v63,a21\n"
AR "v64,a22\n"
MF "v[88:91],v[92:95],v[152:155],0\n"
"s_waitcnt lgkmcnt(4)\n"
"ds_read_b128 v[92:95],v0 offset:2048\n"
AR "v65,a23\n"
MF "v[72:75],v[96:99],v[156:159],v[72:75]\n"
"ds_write_b128 v32,a[8:11] offset:32768\n"
MF "v[76:79],v[96:99],v[160:163],v[76:79]\n"
AR "v66,a24\n"
AR "v67,a25\n"
MF "v[80:83],v[96:99],v[164:167],v[80:83]\n"
"ds_write_b128 v32,a[12:15] offset:36864\n"
MF "v[84:87],v[96:99],v[168:171],v[84:87]\n"
AR "v68,a26\n"
AR "v69,a27\n"
MF "v[88:91],v[96:99],v[172:175],v[88:91]\n"
"s_waitcnt lgkmcnt(6)\n"
"ds_read_b128 v[96:99],v1 offset:2048\n"
AR "v70,a28\n"
MF "v[72:75],v[100:103],v[176:179],v[72:75]\n"
"ds_write_b128 v32,a[16:19] offset:40960\n"
MF "v[76:79],v[100:103],v[180:183],v[76:79]\n"
AR "v71,a29\n"
AR "v132,a30\n"
MF "v[80:83],v[100:103],v[184:187],v[80:83]\n"
AR "v133,a31\n"
AR "v134,a32\n"
MF "v[84:87],v[100:103],v[188:191],v[84:87]\n"
"ds_write_b128 v32,a[40:43] offset:45056\n"
MF "v[88:91],v[100:103],v[192:195],v[88:91]\n"
"s_waitcnt lgkmcnt(8)\n"
"ds_read_b128 v[100:103],v0 offset:6144\n"
AR "v135,a33\n"
MF "v[72:75],v[104:107],v[196:199],v[72:75]\n"
"ds_write_b128 v32,a[44:47] offset:49152\n"
MF "v[76:79],v[104:107],v[200:203],v[76:79]\n"
AR "v6,a34\n"
AR "v13,a35\n"
MF "v[80:83],v[104:107],v[204:207],v[80:83]\n"
AR "v14,a36\n"
AR "v15,a37\n"
MF "v[84:87],v[104:107],v[208:211],v[84:87]\n"
"ds_write_b128 v32,a[48:51] offset:53248\n"
MF "v[88:91],v[104:107],v[212:215],v[88:91]\n"
"s_waitcnt lgkmcnt(8)\n"
"ds_read_b128 v[104:107],v1 offset:6144\n"
"v_accvgpr_mov_b32 a254,a38\n"
MF "v[72:75],v[92:95],v[216:219],v[72:75]\n"
"v_accvgpr_mov_b32 a255,a39\n"
"v_accvgpr_mov_b32 a240,a168\n"
MF "v[76:79],v[92:95],v[220:223],v[76:79]\n"
"ds_write_b128 v32,a[52:55] offset:57344\n"
MF "v[80:83],v[92:95],v[224:227],v[80:83]\n"
"v_accvgpr_mov_b32 a241,a169\n"
"v_accvgpr_mov_b32 a242,a170\n"
MF "v[84:87],v[92:95],v[228:231],v[84:87]\n"
"ds_write_b128 v32,a[56:59] offset:61440\n"
MF "v[88:91],v[92:95],v[232:235],v[88:91]\n"
"s_waitcnt lgkmcnt(8)\n"
"ds_read_b128 v[92:95],v0 offset:7168\n"
"v_accvgpr_mov_b32 a243,a171\n"
MF "v[72:75],v[96:99],v[236:239],v[72:75]\n"
"v_accvgpr_mov_b32 a244,a172\n"
"v_accvgpr_mov_b32 a245,a173\n"
MF "v[76:79],v[96:99],v[240:243],v[76:79]\n"
"ds_write_b128 v33,a[80:83]\n"
MF "v[80:83],v[96:99],v[244:247],v[80:83]\n"
"v_accvgpr_mov_b32 a246,a174\n"
"v_accvgpr_mov_b32 a247,a175\n"
MF "v[84:87],v[96:99],v[248:251],v[84:87]\n"
"v_accvgpr_mov_b32 a248,a176\n"
"v_accvgpr_mov_b32 a249,a177\n"
MF "v[88:91],v[96:99],v[252:255],v[88:91]\n"
"s_waitcnt lgkmcnt(7)\n"
"ds_read_b128 v[96:99],v1 offset:7168\n"
"v_accvgpr_mov_b32 a250,a178\n"
MF "v[112:115],v[100:103],v[136:139],0\n"
"ds_write_b128 v33,a[84:87] offset:4096\n"
MF "v[116:119],v[100:103],v[140:143],0\n"
"ds_write_b128 v33,a[88:91] offset:8192\n"
MF "v[120:123],v[100:103],v[144:147],0\n"
"v_accvgpr_mov_b32 a251,a179\n"
MF "v[124:127],v[100:103],v[148:151],0\n"
MF "v[128:131],v[100:103],v[152:155],0\n"
"s_waitcnt lgkmcnt(7)\n"
"ds_read_b128 v[100:103],v0 offset:8192\n"
MF "v[112:115],v[104:107],v[156:159],v[112:115]\n"
"v_max3_f32 v52,v72,v73,v74\n"
"v_max_f32 v52,v52,v75\n"
MF "v[116:119],v[104:107],v[160:163],v[116:119]\n"
"v_max3_f32 v53,v76,v77,v78\n"
"v_max_f32 v53,v53,v79\n"
MF "v[120:123],v[104:107],v[164:167],v[120:123]\n"
"v_max3_f32 v54,v80,v81,v82\n"
"v_max_f32 v54,v54,v83\n"
MF "v[124:127],v[104:107],v[168:171],v[124:127]\n"
"v_max3_f32 v55,v84,v85,v86\n"
"v_max_f32 v55,v55,v87\n"
MF "v[128:131],v[104:107],v[172:175],v[128:131]\n"
"s_waitcnt lgkmcnt(5)\n"
"ds_read_b128 v[104:107],v1 offset:8192\n"
"v_max3_f32 v56,v88,v89,v90\n"
MF "v[112:115],v[92:95],v[176:179],v[112:115]\n"
"v_max_f32 v56,v56,v91\n"
MF "v[116:119],v[92:95],v[180:183],v[116:119]\n"
"ds_write_b128 v33,a[92:95] offset:12288\n"
MF "v[120:123],v[92:95],v[184:187],v[120:123]\n"
"ds_write_b128 v33,a[96:99] offset:16384\n"
MF "v[124:127],v[92:95],v[188:191],v[124:127]\n"
"ds_write_b128 v33,a[120:123] offset:20480\n"
MF "v[128:131],v[92:95],v[192:195],v[128:131]\n"
"s_waitcnt lgkmcnt(7)\n"
"ds_write_b128 v32,a[124:127]\n"
MF "v[112:115],v[96:99],v[196:199],v[112:115]\n"
"s_waitcnt lgkmcnt(0)\n"
MF "v[116:119],v[96:99],v[200:203],v[116:119]\n"
"s_barrier\n"
MF "v[120:123],v[96:99],v[204:207],v[120:123]\n"
"s_add_u32 s72,s72,6144\n"
"ds_write_b128 v32,a[128:131] offset:4096\n"
MF "v[124:127],v[96:99],v[208:211],v[124:127]\n"
"s_cmp_eq_u32 s72,s17\n"
"s_cselect_b32 s72,s16,s72\n"
"s_add_u32 m0,s64,0\n"
MF "v[128:131],v[96:99],v[212:215],v[128:131]\n"
LD "v7,s[32:35],s72 offen nt lds\n"
MF "v[112:115],v[100:103],v[216:219],v[112:115]\n"
"s_add_u32 m0,s64,896\n"
"ds_write_b128 v32,a[132:135] offset:8192\n"
MF "v[116:119],v[100:103],v[220:223],v[116:119]\n"
LD "v7,s[32:35],s72 offen offset:128 nt lds\n"
MF "v[120:123],v[100:103],v[224:227],v[120:123]\n"
"s_add_u32 m0,s64,1792\n"
"ds_write_b128 v32,a[136:139] offset:12288\n"
MF "v[124:127],v[100:103],v[228:231],v[124:127]\n"
LD "v7,s[32:35],s72 offen offset:256 nt lds\n"
MF "v[128:131],v[100:103],v[232:235],v[128:131]\n"
"s_add_u32 s72,s72,6144\n"
"s_add_u32 m0,s64,6144\n"
MF "v[112:115],v[104:107],v[236:239],v[112:115]\n"
LD "v7,s[32:35],s72 offen nt lds\n"
MF "v[116:119],v[104:107],v[240:243],v[116:119]\n"
"s_add_u32 m0,s64,7040\n"
"ds_write_b128 v32,a[160:163] offset:16384\n"
MF "v[120:123],v[104:107],v[244:247],v[120:123]\n"
LD "v7,s[32:35],s72 offen offset:128 nt lds\n"
MF "v[124:127],v[104:107],v[248:251],v[124:127]\n"
"s_add_u32 m0,s64,7936\n"
"ds_write_b128 v32,a[164:167] offset:20480\n"
MF "v[128:131],v[104:107],v[252:255],v[128:131]\n"
LD "v7,s[32:35],s72 offen offset:256 nt lds\n"
".Lpgt0e_d:\n"
"s_cbranch_execz .Lisr2\n"
".Lt0e:\n"
"s_nop 1\n"
"v_max3_f32 v52,v52,v112,v113\n"
"v_max3_f32 v53,v53,v116,v117\n"
"v_max3_f32 v54,v54,v120,v121\n"
"v_max3_f32 v55,v55,v124,v125\n"
"s_nop 1\n"
"v_max3_f32 v56,v56,v128,v129\n"
"v_max3_f32 v52,v52,v114,v115\n"
"v_max3_f32 v53,v53,v118,v119\n"
"v_max3_f32 v54,v54,v122,v123\n"
"v_max3_f32 v55,v55,v126,v127\n"
"v_max3_f32 v56,v56,v130,v131\n"
"v_mov_b32 v57,v52\n"
"v_mov_b32 v58,v53\n"
"v_mov_b32 v59,v54\n"
"v_mov_b32 v60,v55\n"
"v_mov_b32 v61,v56\n"
"v_permlane16_swap_b32 v52,v57\n"
"v_permlane16_swap_b32 v53,v58\n"
"v_permlane16_swap_b32 v54,v59\n"
"v_permlane16_swap_b32 v55,v60\n"
"v_permlane16_swap_b32 v56,v61\n"
"v_max_f32 v52,v52,v57\n"
"v_max_f32 v53,v53,v58\n"
"v_max_f32 v54,v54,v59\n"
"v_max_f32 v55,v55,v60\n"
"v_max_f32 v56,v56,v61\n"
"v_mov_b32 v57,v52\n"
"v_mov_b32 v58,v53\n"
"v_mov_b32 v59,v54\n"
"v_mov_b32 v60,v55\n"
"v_mov_b32 v61,v56\n"
"v_permlane32_swap_b32 v52,v57\n"
"v_permlane32_swap_b32 v53,v58\n"
"v_permlane32_swap_b32 v54,v59\n"
"v_permlane32_swap_b32 v55,v60\n"
"v_permlane32_swap_b32 v56,v61\n"
"v_max_f32 v52,v52,v57\n"
"v_max_f32 v53,v53,v58\n"
"v_max_f32 v54,v54,v59\n"
"v_max_f32 v55,v55,v60\n"
"v_max_f32_e64 v56,v56,v61\n"
"v_sub_f32 v32,0xc1300000,v52\n"
"v_sub_f32 v36,0xc1300000,v53\n"
"v_sub_f32 v40,0xc1300000,v54\n"
"v_sub_f32 v44,0xc1300000,v55\n"
"v_sub_f32 v48,0xc1300000,v56\n"
"v_mov_b32 v33,v32\n"
"v_pk_mov_b32 v[34:35],v[32:33],v[32:33]\n"
"v_mov_b32 v37,v36\n"
"v_pk_mov_b32 v[38:39],v[36:37],v[36:37]\n"
"v_mov_b32_e64 v41,v40\n"
"v_pk_mov_b32 v[42:43],v[40:41],v[40:41]\n"
"v_mov_b32 v45,v44\n"
"v_pk_mov_b32 v[46:47],v[44:45],v[44:45]\n"
"v_mov_b32 v49,v48\n"
"v_pk_mov_b32 v[50:51],v[48:49],v[48:49]\n"
"v_pk_add_f32 v[72:73],v[72:73],v[32:33]\n"
"v_pk_add_f32 v[74:75],v[74:75],v[32:33]\n"
"v_pk_add_f32 v[112:113],v[112:113],v[32:33]\n"
"v_pk_add_f32 v[114:115],v[114:115],v[32:33]\n"
"v_exp_f32 v72,v72\n"
"v_exp_f32 v73,v73\n"
"v_exp_f32 v74,v74\n"
"v_exp_f32 v75,v75\n"
"v_exp_f32 v112,v112\n"
"v_exp_f32 v113,v113\n"
"v_exp_f32 v114,v114\n"
"v_exp_f32 v115,v115\n"
"v_pk_add_f32 v[10:11],v[72:73],v[74:75]\n"
"v_pk_add_f32 v[28:29],v[112:113],v[114:115]\n"
"v_pk_add_f32 v[10:11],v[10:11],v[28:29]\n"
"v_add_f32_e64 v52,v10,v11\n"
CV "v72,v72,v73\n"
CV "v73,v74,v75\n"
CV "v74,v112,v113\n"
CV "v75,v114,v115\n"
"v_pk_add_f32 v[76:77],v[76:77],v[36:37]\n"
"v_pk_add_f32 v[78:79],v[78:79],v[36:37]\n"
"v_pk_add_f32 v[116:117],v[116:117],v[36:37]\n"
"v_pk_add_f32 v[118:119],v[118:119],v[36:37]\n"
"v_exp_f32 v76,v76\n"
"v_exp_f32 v77,v77\n"
"v_exp_f32 v78,v78\n"
"v_exp_f32 v79,v79\n"
"v_exp_f32 v116,v116\n"
"v_exp_f32 v117,v117\n"
"v_exp_f32 v118,v118\n"
"v_exp_f32 v119,v119\n"
"v_pk_add_f32 v[10:11],v[76:77],v[78:79]\n"
"v_pk_add_f32 v[28:29],v[116:117],v[118:119]\n"
"v_pk_add_f32 v[10:11],v[10:11],v[28:29]\n"
"v_add_f32_e64 v53,v10,v11\n"
CV "v76,v76,v77\n"
CV "v77,v78,v79\n"
CV "v78,v116,v117\n"
CV "v79,v118,v119\n"
"v_pk_add_f32 v[80:81],v[80:81],v[40:41]\n"
"v_pk_add_f32 v[82:83],v[82:83],v[40:41]\n"
"v_pk_add_f32 v[120:121],v[120:121],v[40:41]\n"
"v_pk_add_f32 v[122:123],v[122:123],v[40:41]\n"
"v_exp_f32 v80,v80\n"
"v_exp_f32 v81,v81\n"
"v_exp_f32 v82,v82\n"
"v_exp_f32 v83,v83\n"
"v_exp_f32 v120,v120\n"
"v_exp_f32 v121,v121\n"
"v_exp_f32 v122,v122\n"
"v_exp_f32 v123,v123\n"
"v_pk_add_f32 v[10:11],v[80:81],v[82:83]\n"
"v_pk_add_f32 v[28:29],v[120:121],v[122:123]\n"
"v_pk_add_f32 v[10:11],v[10:11],v[28:29]\n"
"v_add_f32_e64 v54,v10,v11\n"
CV "v80,v80,v81\n"
CV "v81,v82,v83\n"
CV "v82,v120,v121\n"
CV "v83,v122,v123\n"
"v_pk_add_f32 v[84:85],v[84:85],v[44:45]\n"
"v_pk_add_f32 v[86:87],v[86:87],v[44:45]\n"
"v_pk_add_f32 v[124:125],v[124:125],v[44:45]\n"
"v_pk_add_f32 v[126:127],v[126:127],v[44:45]\n"
"v_mov_b32_e64 v55,0\n"
"v_pk_add_f32 v[88:89],v[88:89],v[48:49]\n"
"v_pk_add_f32 v[90:91],v[90:91],v[48:49]\n"
"v_pk_add_f32 v[128:129],v[128:129],v[48:49]\n"
"v_pk_add_f32 v[130:131],v[130:131],v[48:49]\n"
"v_mov_b32 v56,0\n"
"s_waitcnt vmcnt(12) lgkmcnt(0)\n"
"s_barrier\n"
"ds_read_b128 v[16:19],v0 offset:24576\n"
"ds_read_b128 v[20:23],v1 offset:24576\n"
AW "a220,s13\n"
AW "a221,s13\n"
AW "a222,s13\n"
AW "a223,s13\n"
"s_waitcnt lgkmcnt(1)\n"
MF "v[92:95],v[16:19],v[136:139],v[32:35]\n"
"s_nop 0\n"
"ds_read_b128 v[24:27],v0 offset:25600\n"
"ds_read_b128 v[28:31],v1 offset:25600\n"
"s_waitcnt lgkmcnt(2)\n"
MF "v[92:95],v[20:23],v[156:159],v[92:95]\n"
"v_exp_f32 v84,v84\n"
MF "v[96:99],v[20:23],v[160:163],v[36:39]\n"
"v_exp_f32 v85,v85\n"
MF "v[96:99],v[16:19],v[140:143],v[96:99]\n"
"v_exp_f32 v86,v86\n"
MF "v[100:103],v[16:19],v[144:147],v[40:43]\n"
"v_exp_f32 v87,v87\n"
MF "v[100:103],v[20:23],v[164:167],v[100:103]\n"
"v_add_f32 v55,v55,v84\n"
"v_add_f32 v55,v55,v85\n"
MF "v[104:107],v[20:23],v[168:171],v[44:47]\n"
CV "v84,v84,v85\n"
"v_add_f32 v55,v55,v86\n"
MF "v[104:107],v[16:19],v[148:151],v[104:107]\n"
"v_add_f32 v55,v55,v87\n"
CV "v85,v86,v87\n"
MF "v[108:111],v[16:19],v[152:155],v[48:51]\n"
"v_exp_f32 v124,v124\n"
MF "v[108:111],v[20:23],v[172:175],v[108:111]\n"
"s_waitcnt lgkmcnt(0)\n"
"ds_read_b128 v[16:19],v0 offset:26624\n"
"ds_read_b128 v[20:23],v1 offset:26624\n"
MF "v[108:111],v[24:27],v[192:195],v[108:111]\n"
"v_exp_f32 v125,v125\n"
MF "v[108:111],v[28:31],v[212:215],v[108:111]\n"
"v_exp_f32 v126,v126\n"
MF "v[104:107],v[28:31],v[208:211],v[104:107]\n"
"v_exp_f32 v127,v127\n"
MF "v[104:107],v[24:27],v[188:191],v[104:107]\n"
"v_add_f32 v55,v55,v124\n"
"v_add_f32 v55,v55,v125\n"
MF "v[100:103],v[24:27],v[184:187],v[100:103]\n"
CV "v86,v124,v125\n"
"v_add_f32 v55,v55,v126\n"
MF "v[100:103],v[28:31],v[204:207],v[100:103]\n"
"v_add_f32 v55,v55,v127\n"
CV "v87,v126,v127\n"
MF "v[96:99],v[28:31],v[200:203],v[96:99]\n"
"v_exp_f32 v88,v88\n"
MF "v[96:99],v[24:27],v[180:183],v[96:99]\n"
"v_exp_f32 v89,v89\n"
MF "v[92:95],v[24:27],v[176:179],v[92:95]\n"
"v_exp_f32 v90,v90\n"
MF "v[92:95],v[28:31],v[196:199],v[92:95]\n"
"s_waitcnt lgkmcnt(0)\n"
"ds_read_b128 v[24:27],v0 offset:30720\n"
"ds_read_b128 v[28:31],v1 offset:30720\n"
MF "v[92:95],v[16:19],v[216:219],v[92:95]\n"
"v_exp_f32 v91,v91\n"
MF "v[92:95],v[20:23],v[236:239],v[92:95]\n"
"v_add_f32 v56,v56,v88\n"
"v_add_f32 v56,v56,v89\n"
MF "v[96:99],v[20:23],v[240:243],v[96:99]\n"
CV "v88,v88,v89\n"
"v_add_f32 v56,v56,v90\n"
MF "v[96:99],v[16:19],v[220:223],v[96:99]\n"
"v_add_f32 v56,v56,v91\n"
CV "v89,v90,v91\n"
MF "v[100:103],v[16:19],v[224:227],v[100:103]\n"
"v_exp_f32 v128,v128\n"
MF "v[100:103],v[20:23],v[244:247],v[100:103]\n"
"v_exp_f32 v129,v129\n"
MF "v[104:107],v[20:23],v[248:251],v[104:107]\n"
"v_exp_f32 v130,v130\n"
MF "v[104:107],v[16:19],v[228:231],v[104:107]\n"
"v_exp_f32 v131,v131\n"
MF "v[108:111],v[16:19],v[232:235],v[108:111]\n"
"v_add_f32 v56,v56,v128\n"
"v_add_f32 v56,v56,v129\n"
MF "v[108:111],v[20:23],v[252:255],v[108:111]\n"
"s_waitcnt lgkmcnt(0)\n"
"ds_read_b128 v[16:19],v0 offset:31744\n"
"ds_read_b128 v[20:23],v1 offset:31744\n"
MF "v[112:115],v[24:27],v[136:139],v[32:35]\n"
CV "v90,v128,v129\n"
"v_add_f32 v56,v56,v130\n"
MF "v[112:115],v[28:31],v[156:159],v[112:115]\n"
"v_add_f32 v56,v56,v131\n"
CV "v91,v130,v131\n"
MF "v[116:119],v[28:31],v[160:163],v[36:39]\n"
"v_exp_f32 v92,v92\n"
MF "v[116:119],v[24:27],v[140:143],v[116:119]\n"
"v_exp_f32 v93,v93\n"
MF "v[120:123],v[24:27],v[144:147],v[40:43]\n"
"v_exp_f32 v94,v94\n"
MF "v[120:123],v[28:31],v[164:167],v[120:123]\n"
"v_exp_f32 v95,v95\n"
MF "v[124:127],v[28:31],v[168:171],v[44:47]\n"
"v_add_f32 v57,v52,v92\n"
"v_add_f32 v57,v57,v93\n"
MF "v[124:127],v[24:27],v[148:151],v[124:127]\n"
CV "v92,v92,v93\n"
"v_add_f32 v57,v57,v94\n"
MF "v[128:131],v[24:27],v[152:155],v[48:51]\n"
"v_add_f32 v57,v57,v95\n"
CV "v93,v94,v95\n"
MF "v[128:131],v[28:31],v[172:175],v[128:131]\n"
"s_waitcnt lgkmcnt(0)\n"
"ds_read_b128 v[24:27],v0 offset:32768\n"
"ds_read_b128 v[28:31],v1 offset:32768\n"
MF "v[128:131],v[16:19],v[192:195],v[128:131]\n"
"v_exp_f32 v96,v96\n"
MF "v[128:131],v[20:23],v[212:215],v[128:131]\n"
"v_exp_f32 v97,v97\n"
MF "v[124:127],v[20:23],v[208:211],v[124:127]\n"
"v_exp_f32 v98,v98\n"
MF "v[124:127],v[16:19],v[188:191],v[124:127]\n"
"v_exp_f32 v99,v99\n"
MF "v[120:123],v[16:19],v[184:187],v[120:123]\n"
"v_add_f32 v58,v53,v96\n"
"v_add_f32 v58,v58,v97\n"
MF "v[120:123],v[20:23],v[204:207],v[120:123]\n"
CV "v96,v96,v97\n"
"v_add_f32 v58,v58,v98\n"
MF "v[116:119],v[20:23],v[200:203],v[116:119]\n"
"s_waitcnt vmcnt(6) lgkmcnt(0)\n"
"v_add_f32 v58,v58,v99\n"
CV "v97,v98,v99\n"
MF "v[116:119],v[16:19],v[180:183],v[116:119]\n"
"s_barrier\n"
MF "v[112:115],v[16:19],v[176:179],v[112:115]\n"
"v_exp_f32 v100,v100\n"
MF "v[112:115],v[20:23],v[196:199],v[112:115]\n"
"s_nop 0\n"
TR "v[16:17],v2 offset:49152\n"
TR "v[18:19],v2 offset:55296\n"
MF "v[112:115],v[24:27],v[216:219],v[112:115]\n"
"s_add_u32 s73,s73,12288\n"
TR "v[20:21],v3 offset:49152\n"
TR "v[22:23],v3 offset:55296\n"
MF "v[112:115],v[28:31],v[236:239],v[112:115]\n"
"s_cmp_eq_u32 s73,s19\n"
"s_cselect_b32 s73,s18,s73\n"
"s_add_u32 m0,s65,24576\n"
MF "v[116:119],v[28:31],v[240:243],v[116:119]\n"
LD "v8,s[36:39],s73 offen nt lds\n"
MF "v[116:119],v[24:27],v[220:223],v[116:119]\n"
"s_add_u32 m0,s65,25472\n"
"v_exp_f32 v101,v101\n"
MF "v[120:123],v[24:27],v[224:227],v[120:123]\n"
LD "v8,s[36:39],s73 offen offset:128 nt lds\n"
MF "v[120:123],v[28:31],v[244:247],v[120:123]\n"
"v_exp_f32 v102,v102\n"
MF "v[124:127],v[28:31],v[248:251],v[124:127]\n"
"s_add_u32 m0,s65,26368\n"
"v_exp_f32 v103,v103\n"
MF "v[124:127],v[24:27],v[228:231],v[124:127]\n"
LD "v8,s[36:39],s73 offen offset:256 nt lds\n"
MF "v[128:131],v[24:27],v[232:235],v[128:131]\n"
"v_add_f32 v59,v54,v100\n"
"v_add_f32 v59,v59,v101\n"
MF "v[128:131],v[28:31],v[252:255],v[128:131]\n"
"s_waitcnt lgkmcnt(0)\n"
TR "v[24:25],v4 offset:49152\n"
TR "v[26:27],v4 offset:55296\n"
MF "a[0:3],v[16:19],v[72:75],a[220:223]\n"
"s_add_u32 m0,s65,24576\n"
TR "v[28:29],v5 offset:49152\n"
TR "v[30:31],v5 offset:55296\n"
MF "a[20:23],v[20:23],v[72:75],a[220:223]\n"
LD "v8,s[36:39],s73 offen offset:3072 nt lds\n"
MF "a[24:27],v[20:23],v[76:79],a[220:223]\n"
CV "v100,v100,v101\n"
"v_add_f32 v59,v59,v102\n"
MF "a[4:7],v[16:19],v[76:79],a[220:223]\n"
"s_add_u32 m0,s65,25472\n"
"v_add_f32 v59,v59,v103\n"
CV "v101,v102,v103\n"
MF "a[8:11],v[16:19],v[80:83],a[220:223]\n"
LD "v8,s[36:39],s73 offen offset:3200 nt lds\n"
MF "a[28:31],v[20:23],v[80:83],a[220:223]\n"
"v_exp_f32 v104,v104\n"
MF "a[32:35],v[20:23],v[84:87],a[220:223]\n"
"s_add_u32 m0,s65,26368\n"
"v_exp_f32 v105,v105\n"
MF "a[12:15],v[16:19],v[84:87],a[220:223]\n"
LD "v8,s[36:39],s73 offen offset:3328 nt lds\n"
MF "a[16:19],v[16:19],v[88:91],a[220:223]\n"
"v_exp_f32 v106,v106\n"
MF "a[36:39],v[20:23],v[88:91],a[220:223]\n"
"s_waitcnt lgkmcnt(0)\n"
TR "v[16:17],v2 offset:50176\n"
TR "v[18:19],v2 offset:56320\n"
MF "a[56:59],v[24:27],v[88:91],a[220:223]\n"
"s_nop 0\n"
TR "v[20:21],v3 offset:50176\n"
TR "v[22:23],v3 offset:56320\n"
MF "a[76:79],v[28:31],v[88:91],a[220:223]\n"
"v_exp_f32 v107,v107\n"
MF "a[72:75],v[28:31],v[84:87],a[220:223]\n"
"v_add_f32 v60,v55,v104\n"
"v_add_f32 v60,v60,v105\n"
MF "a[52:55],v[24:27],v[84:87],a[220:223]\n"
CV "v104,v104,v105\n"
"v_add_f32 v60,v60,v106\n"
MF "a[48:51],v[24:27],v[80:83],a[220:223]\n"
"v_add_f32 v60,v60,v107\n"
CV "v105,v106,v107\n"
MF "a[68:71],v[28:31],v[80:83],a[220:223]\n"
"v_exp_f32 v108,v108\n"
MF "a[64:67],v[28:31],v[76:79],a[220:223]\n"
"v_exp_f32 v109,v109\n"
MF "a[44:47],v[24:27],v[76:79],a[220:223]\n"
"v_exp_f32 v110,v110\n"
MF "a[40:43],v[24:27],v[72:75],a[220:223]\n"
"v_exp_f32 v111,v111\n"
MF "a[60:63],v[28:31],v[72:75],a[220:223]\n"
"s_waitcnt lgkmcnt(0)\n"
TR "v[24:25],v4 offset:50176\n"
TR "v[26:27],v4 offset:56320\n"
MF "a[80:83],v[16:19],v[72:75],a[220:223]\n"
"s_nop 0\n"
TR "v[28:29],v5 offset:50176\n"
TR "v[30:31],v5 offset:56320\n"
MF "a[100:103],v[20:23],v[72:75],a[220:223]\n"
"v_add_f32 v61,v56,v108\n"
"v_add_f32 v61,v61,v109\n"
MF "a[104:107],v[20:23],v[76:79],a[220:223]\n"
CV "v108,v108,v109\n"
"v_add_f32 v61,v61,v110\n"
MF "a[84:87],v[16:19],v[76:79],a[220:223]\n"
"v_add_f32 v61,v61,v111\n"
CV "v109,v110,v111\n"
MF "a[88:91],v[16:19],v[80:83],a[220:223]\n"
"v_exp_f32 v112,v112\n"
MF "a[108:111],v[20:23],v[80:83],a[220:223]\n"
"v_exp_f32 v113,v113\n"
MF "a[112:115],v[20:23],v[84:87],a[220:223]\n"
"v_exp_f32 v114,v114\n"
MF "a[92:95],v[16:19],v[84:87],a[220:223]\n"
"v_exp_f32 v115,v115\n"
MF "a[96:99],v[16:19],v[88:91],a[220:223]\n"
"v_add_f32 v57,v57,v112\n"
"v_add_f32 v57,v57,v113\n"
MF "a[116:119],v[20:23],v[88:91],a[220:223]\n"
"s_waitcnt lgkmcnt(0)\n"
TR "v[16:17],v2 offset:51200\n"
TR "v[18:19],v2 offset:57344\n"
MF "a[136:139],v[24:27],v[88:91],a[220:223]\n"
"s_nop 0\n"
TR "v[20:21],v3 offset:51200\n"
TR "v[22:23],v3 offset:57344\n"
MF "a[156:159],v[28:31],v[88:91],a[220:223]\n"
CV "v94,v112,v113\n"
"v_add_f32 v57,v57,v114\n"
MF "a[152:155],v[28:31],v[84:87],a[220:223]\n"
"v_add_f32 v57,v57,v115\n"
CV "v95,v114,v115\n"
MF "a[132:135],v[24:27],v[84:87],a[220:223]\n"
"v_exp_f32 v116,v116\n"
MF "a[128:131],v[24:27],v[80:83],a[220:223]\n"
"v_exp_f32 v117,v117\n"
MF "a[148:151],v[28:31],v[80:83],a[220:223]\n"
"v_exp_f32 v118,v118\n"
MF "a[144:147],v[28:31],v[76:79],a[220:223]\n"
"v_exp_f32 v119,v119\n"
MF "a[124:127],v[24:27],v[76:79],a[220:223]\n"
"v_add_f32 v58,v58,v116\n"
"v_add_f32 v58,v58,v117\n"
MF "a[120:123],v[24:27],v[72:75],a[220:223]\n"
CV "v98,v116,v117\n"
"v_add_f32 v58,v58,v118\n"
MF "a[140:143],v[28:31],v[72:75],a[220:223]\n"
"s_waitcnt lgkmcnt(0)\n"
TR "v[24:25],v4 offset:51200\n"
TR "v[26:27],v4 offset:57344\n"
MF "a[160:163],v[16:19],v[72:75],a[220:223]\n"
"s_nop 0\n"
TR "v[28:29],v5 offset:51200\n"
TR "v[30:31],v5 offset:57344\n"
MF "a[180:183],v[20:23],v[72:75],a[220:223]\n"
"v_add_f32 v58,v58,v119\n"
CV "v99,v118,v119\n"
MF "a[184:187],v[20:23],v[76:79],a[220:223]\n"
"v_exp_f32 v120,v120\n"
MF "a[164:167],v[16:19],v[76:79],a[220:223]\n"
"v_exp_f32 v121,v121\n"
MF "a[168:171],v[16:19],v[80:83],a[220:223]\n"
"v_exp_f32 v122,v122\n"
MF "a[188:191],v[20:23],v[80:83],a[220:223]\n"
"v_exp_f32 v123,v123\n"
MF "a[192:195],v[20:23],v[84:87],a[220:223]\n"
"v_add_f32 v59,v59,v120\n"
"v_add_f32 v59,v59,v121\n"
MF "a[172:175],v[16:19],v[84:87],a[220:223]\n"
"s_waitcnt vmcnt(6) lgkmcnt(0)\n"
CV "v102,v120,v121\n"
"v_add_f32 v59,v59,v122\n"
MF "a[176:179],v[16:19],v[88:91],a[220:223]\n"
"s_barrier\n"
MF "a[196:199],v[20:23],v[88:91],a[220:223]\n"
"s_add_u32 s72,s72,6144\n"
"ds_read_b128 v[16:19],v0\n"
"ds_read_b128 v[20:23],v1\n"
MF "a[216:219],v[24:27],v[88:91],a[220:223]\n"
"s_cmp_eq_u32 s72,s17\n"
"s_cselect_b32 s72,s16,s72\n"
"s_add_u32 m0,s64,49152\n"
MF "a[236:239],v[28:31],v[88:91],a[220:223]\n"
LD "v7,s[32:35],s72 offen nt lds\n"
MF "a[232:235],v[28:31],v[84:87],a[220:223]\n"
"s_add_u32 m0,s64,50048\n"
"v_add_f32 v59,v59,v123\n"
CV "v103,v122,v123\n"
MF "a[212:215],v[24:27],v[84:87],a[220:223]\n"
LD "v7,s[32:35],s72 offen offset:128 nt lds\n"
MF "a[208:211],v[24:27],v[80:83],a[220:223]\n"
"s_add_u32 m0,s64,50944\n"
MF "a[228:231],v[28:31],v[80:83],a[220:223]\n"
LD "v7,s[32:35],s72 offen offset:256 nt lds\n"
MF "a[224:227],v[28:31],v[76:79],a[220:223]\n"
MF "a[204:207],v[24:27],v[76:79],a[220:223]\n"
MF "a[200:203],v[24:27],v[72:75],a[220:223]\n"
MF "a[220:223],v[28:31],v[72:75],a[220:223]\n"
"s_waitcnt lgkmcnt(0)\n"
".Laloop:\n"
MF "v[72:75],v[16:19],v[136:139],v[32:35]\n"
"s_nop 0\n"
"ds_read_b128 v[24:27],v0 offset:1024\n"
"ds_read_b128 v[28:31],v1 offset:1024\n"
MF "v[72:75],v[20:23],v[156:159],v[72:75]\n"
MF "v[76:79],v[20:23],v[160:163],v[36:39]\n"
MF "v[76:79],v[16:19],v[140:143],v[76:79]\n"
MF "v[80:83],v[16:19],v[144:147],v[40:43]\n"
MF "v[80:83],v[20:23],v[164:167],v[80:83]\n"
MF "v[84:87],v[20:23],v[168:171],v[44:47]\n"
MF "v[84:87],v[16:19],v[148:151],v[84:87]\n"
"s_add_u32 s72,s72,6144\n"
"s_add_u32 m0,s64,55296\n"
MF "v[88:91],v[16:19],v[152:155],v[48:51]\n"
LD "v7,s[32:35],s72 offen nt lds\n"
MF "v[88:91],v[20:23],v[172:175],v[88:91]\n"
"s_waitcnt lgkmcnt(0)\n"
"ds_read_b128 v[16:19],v0 offset:2048\n"
"ds_read_b128 v[20:23],v1 offset:2048\n"
MF "v[88:91],v[24:27],v[192:195],v[88:91]\n"
"v_exp_f32 v124,v124\n"
MF "v[88:91],v[28:31],v[212:215],v[88:91]\n"
"s_add_u32 m0,s64,56192\n"
"v_exp_f32 v125,v125\n"
MF "v[84:87],v[28:31],v[208:211],v[84:87]\n"
LD "v7,s[32:35],s72 offen offset:128 nt lds\n"
MF "v[84:87],v[24:27],v[188:191],v[84:87]\n"
"v_exp_f32 v126,v126\n"
MF "v[80:83],v[24:27],v[184:187],v[80:83]\n"
"v_exp_f32 v127,v127\n"
MF "v[80:83],v[28:31],v[204:207],v[80:83]\n"
"s_add_u32 m0,s64,57088\n"
"v_add_f32 v60,v60,v124\n"
"v_add_f32 v60,v60,v125\n"
MF "v[76:79],v[28:31],v[200:203],v[76:79]\n"
LD "v7,s[32:35],s72 offen offset:256 nt lds\n"
MF "v[76:79],v[24:27],v[180:183],v[76:79]\n"
CV "v106,v124,v125\n"
"v_add_f32 v60,v60,v126\n"
MF "v[72:75],v[24:27],v[176:179],v[72:75]\n"
"v_add_f32 v60,v60,v127\n"
CV "v107,v126,v127\n"
MF "v[72:75],v[28:31],v[196:199],v[72:75]\n"
"s_waitcnt lgkmcnt(0)\n"
"ds_read_b128 v[24:27],v0 offset:6144\n"
"ds_read_b128 v[28:31],v1 offset:6144\n"
MF "v[72:75],v[16:19],v[216:219],v[72:75]\n"
"v_exp_f32 v128,v128\n"
MF "v[72:75],v[20:23],v[236:239],v[72:75]\n"
"v_exp_f32 v129,v129\n"
MF "v[76:79],v[20:23],v[240:243],v[76:79]\n"
"v_exp_f32 v130,v130\n"
MF "v[76:79],v[16:19],v[220:223],v[76:79]\n"
"v_exp_f32 v131,v131\n"
MF "v[80:83],v[16:19],v[224:227],v[80:83]\n"
"v_add_f32 v61,v61,v128\n"
"v_add_f32 v61,v61,v129\n"
MF "v[80:83],v[20:23],v[244:247],v[80:83]\n"
CV "v110,v128,v129\n"
"v_add_f32 v61,v61,v130\n"
MF "v[84:87],v[20:23],v[248:251],v[84:87]\n"
"v_add_f32 v61,v61,v131\n"
CV "v111,v130,v131\n"
MF "v[84:87],v[16:19],v[228:231],v[84:87]\n"
"v_max3_f32 v10,v57,v58,v59\n"
"v_max3_f32 v10,v10,v60,v61\n"
MF "v[88:91],v[16:19],v[232:235],v[88:91]\n"
"v_cmp_ngt_f32 vcc,s77,v10\n"
MF "v[88:91],v[20:23],v[252:255],v[88:91]\n"
"s_waitcnt lgkmcnt(0)\n"
"s_cbranch_vccnz .Lstub_20\n"
".Lback_20:\n"
MF "v[112:115],v[24:27],v[136:139],v[32:35]\n"
"s_add_u32 s74,s74,12288\n"
"ds_read_b128 v[16:19],v0 offset:7168\n"
"ds_read_b128 v[20:23],v1 offset:7168\n"
MF "v[112:115],v[28:31],v[156:159],v[112:115]\n"
"s_cmp_eq_u32 s74,s20\n"
"s_cselect_b32 s74,s70,s74\n"
MF "v[116:119],v[28:31],v[160:163],v[36:39]\n"
"v_exp_f32 v72,v72\n"
MF "v[116:119],v[24:27],v[140:143],v[116:119]\n"
"v_exp_f32 v73,v73\n"
MF "v[120:123],v[24:27],v[144:147],v[40:43]\n"
"v_exp_f32 v74,v74\n"
MF "v[120:123],v[28:31],v[164:167],v[120:123]\n"
"v_exp_f32 v75,v75\n"
MF "v[124:127],v[28:31],v[168:171],v[44:47]\n"
"v_add_f32 v52,v57,v72\n"
"v_add_f32 v52,v52,v73\n"
MF "v[124:127],v[24:27],v[148:151],v[124:127]\n"
CV "v72,v72,v73\n"
"v_add_f32 v52,v52,v74\n"
MF "v[128:131],v[24:27],v[152:155],v[48:51]\n"
"v_add_f32 v52,v52,v75\n"
CV "v73,v74,v75\n"
MF "v[128:131],v[28:31],v[172:175],v[128:131]\n"
"s_waitcnt lgkmcnt(0)\n"
"ds_read_b128 v[24:27],v0 offset:8192\n"
"ds_read_b128 v[28:31],v1 offset:8192\n"
MF "v[128:131],v[16:19],v[192:195],v[128:131]\n"
"v_exp_f32 v76,v76\n"
MF "v[128:131],v[20:23],v[212:215],v[128:131]\n"
"v_exp_f32 v77,v77\n"
MF "v[124:127],v[20:23],v[208:211],v[124:127]\n"
"v_exp_f32 v78,v78\n"
MF "v[124:127],v[16:19],v[188:191],v[124:127]\n"
"v_exp_f32 v79,v79\n"
MF "v[120:123],v[16:19],v[184:187],v[120:123]\n"
"v_add_f32 v53,v58,v76\n"
"v_add_f32 v53,v53,v77\n"
MF "v[120:123],v[20:23],v[204:207],v[120:123]\n"
CV "v76,v76,v77\n"
"v_add_f32 v53,v53,v78\n"
MF "v[116:119],v[20:23],v[200:203],v[116:119]\n"
"s_waitcnt vmcnt(6) lgkmcnt(0)\n"
"v_add_f32 v53,v53,v79\n"
CV "v77,v78,v79\n"
MF "v[116:119],v[16:19],v[180:183],v[116:119]\n"
"s_barrier\n"
MF "v[112:115],v[16:19],v[176:179],v[112:115]\n"
"v_exp_f32 v80,v80\n"
MF "v[112:115],v[20:23],v[196:199],v[112:115]\n"
"s_nop 0\n"
TR "v[16:17],v2 offset:24576\n"
TR "v[18:19],v2 offset:30720\n"
MF "v[112:115],v[24:27],v[216:219],v[112:115]\n"
"s_add_u32 s73,s73,12288\n"
TR "v[20:21],v3 offset:24576\n"
TR "v[22:23],v3 offset:30720\n"
MF "v[112:115],v[28:31],v[236:239],v[112:115]\n"
"s_cmp_eq_u32 s73,s19\n"
"s_cselect_b32 s73,s18,s73\n"
"s_add_u32 m0,s65,0\n"
MF "v[116:119],v[28:31],v[240:243],v[116:119]\n"
LD "v8,s[36:39],s73 offen nt lds\n"
MF "v[116:119],v[24:27],v[220:223],v[116:119]\n"
"s_add_u32 m0,s65,896\n"
"v_exp_f32 v81,v81\n"
MF "v[120:123],v[24:27],v[224:227],v[120:123]\n"
LD "v8,s[36:39],s73 offen offset:128 nt lds\n"
MF "v[120:123],v[28:31],v[244:247],v[120:123]\n"
"v_exp_f32 v82,v82\n"
MF "v[124:127],v[28:31],v[248:251],v[124:127]\n"
"s_add_u32 m0,s65,1792\n"
"v_exp_f32 v83,v83\n"
MF "v[124:127],v[24:27],v[228:231],v[124:127]\n"
LD "v8,s[36:39],s73 offen offset:256 nt lds\n"
MF "v[128:131],v[24:27],v[232:235],v[128:131]\n"
"v_add_f32 v54,v59,v80\n"
"v_add_f32 v54,v54,v81\n"
MF "v[128:131],v[28:31],v[252:255],v[128:131]\n"
"s_waitcnt lgkmcnt(0)\n"
TR "v[24:25],v4 offset:24576\n"
TR "v[26:27],v4 offset:30720\n"
MF "a[0:3],v[16:19],v[92:95],a[0:3]\n"
"s_add_u32 m0,s65,0\n"
TR "v[28:29],v5 offset:24576\n"
TR "v[30:31],v5 offset:30720\n"
MF "a[20:23],v[20:23],v[92:95],a[20:23]\n"
LD "v8,s[36:39],s73 offen offset:3072 nt lds\n"
MF "a[24:27],v[20:23],v[96:99],a[24:27]\n"
CV "v80,v80,v81\n"
"v_add_f32 v54,v54,v82\n"
MF "a[4:7],v[16:19],v[96:99],a[4:7]\n"
"s_add_u32 m0,s65,896\n"
"v_add_f32 v54,v54,v83\n"
CV "v81,v82,v83\n"
MF "a[8:11],v[16:19],v[100:103],a[8:11]\n"
LD "v8,s[36:39],s73 offen offset:3200 nt lds\n"
MF "a[28:31],v[20:23],v[100:103],a[28:31]\n"
"v_exp_f32 v84,v84\n"
MF "a[32:35],v[20:23],v[104:107],a[32:35]\n"
"s_add_u32 m0,s65,1792\n"
"v_exp_f32 v85,v85\n"
MF "a[12:15],v[16:19],v[104:107],a[12:15]\n"
LD "v8,s[36:39],s73 offen offset:3328 nt lds\n"
MF "a[16:19],v[16:19],v[108:111],a[16:19]\n"
"v_exp_f32 v86,v86\n"
MF "a[36:39],v[20:23],v[108:111],a[36:39]\n"
"s_waitcnt lgkmcnt(0)\n"
TR "v[16:17],v2 offset:25600\n"
TR "v[18:19],v2 offset:31744\n"
MF "a[56:59],v[24:27],v[108:111],a[56:59]\n"
"s_nop 0\n"
TR "v[20:21],v3 offset:25600\n"
TR "v[22:23],v3 offset:31744\n"
MF "a[76:79],v[28:31],v[108:111],a[76:79]\n"
"v_exp_f32 v87,v87\n"
MF "a[72:75],v[28:31],v[104:107],a[72:75]\n"
"v_add_f32 v55,v60,v84\n"
"v_add_f32 v55,v55,v85\n"
MF "a[52:55],v[24:27],v[104:107],a[52:55]\n"
CV "v84,v84,v85\n"
"v_add_f32 v55,v55,v86\n"
MF "a[48:51],v[24:27],v[100:103],a[48:51]\n"
"v_add_f32 v55,v55,v87\n"
CV "v85,v86,v87\n"
MF "a[68:71],v[28:31],v[100:103],a[68:71]\n"
"v_exp_f32 v88,v88\n"
MF "a[64:67],v[28:31],v[96:99],a[64:67]\n"
"v_exp_f32 v89,v89\n"
MF "a[44:47],v[24:27],v[96:99],a[44:47]\n"
"v_exp_f32 v90,v90\n"
MF "a[40:43],v[24:27],v[92:95],a[40:43]\n"
"v_exp_f32 v91,v91\n"
MF "a[60:63],v[28:31],v[92:95],a[60:63]\n"
"s_waitcnt lgkmcnt(0)\n"
TR "v[24:25],v4 offset:25600\n"
TR "v[26:27],v4 offset:31744\n"
MF "a[80:83],v[16:19],v[92:95],a[80:83]\n"
"s_nop 0\n"
TR "v[28:29],v5 offset:25600\n"
TR "v[30:31],v5 offset:31744\n"
MF "a[100:103],v[20:23],v[92:95],a[100:103]\n"
"v_add_f32 v56,v61,v88\n"
"v_add_f32 v56,v56,v89\n"
MF "a[104:107],v[20:23],v[96:99],a[104:107]\n"
CV "v88,v88,v89\n"
"v_add_f32 v56,v56,v90\n"
MF "a[84:87],v[16:19],v[96:99],a[84:87]\n"
"v_add_f32 v56,v56,v91\n"
CV "v89,v90,v91\n"
MF "a[88:91],v[16:19],v[100:103],a[88:91]\n"
"v_exp_f32 v112,v112\n"
MF "a[108:111],v[20:23],v[100:103],a[108:111]\n"
"v_exp_f32 v113,v113\n"
MF "a[112:115],v[20:23],v[104:107],a[112:115]\n"
"v_exp_f32 v114,v114\n"
MF "a[92:95],v[16:19],v[104:107],a[92:95]\n"
"v_exp_f32 v115,v115\n"
MF "a[96:99],v[16:19],v[108:111],a[96:99]\n"
"v_add_f32 v52,v52,v112\n"
"v_add_f32 v52,v52,v113\n"
MF "a[116:119],v[20:23],v[108:111],a[116:119]\n"
"s_waitcnt lgkmcnt(0)\n"
TR "v[16:17],v2 offset:26624\n"
TR "v[18:19],v2 offset:32768\n"
MF "a[136:139],v[24:27],v[108:111],a[136:139]\n"
"s_nop 0\n"
TR "v[20:21],v3 offset:26624\n"
TR "v[22:23],v3 offset:32768\n"
MF "a[156:159],v[28:31],v[108:111],a[156:159]\n"
CV "v74,v112,v113\n"
"v_add_f32 v52,v52,v114\n"
MF "a[152:155],v[28:31],v[104:107],a[152:155]\n"
"v_add_f32 v52,v52,v115\n"
CV "v75,v114,v115\n"
MF "a[132:135],v[24:27],v[104:107],a[132:135]\n"
"v_exp_f32 v116,v116\n"
MF "a[128:131],v[24:27],v[100:103],a[128:131]\n"
"v_exp_f32 v117,v117\n"
MF "a[148:151],v[28:31],v[100:103],a[148:151]\n"
"v_exp_f32 v118,v118\n"
MF "a[144:147],v[28:31],v[96:99],a[144:147]\n"
"v_exp_f32 v119,v119\n"
MF "a[124:127],v[24:27],v[96:99],a[124:127]\n"
"v_add_f32 v53,v53,v116\n"
"v_add_f32 v53,v53,v117\n"
MF "a[120:123],v[24:27],v[92:95],a[120:123]\n"
CV "v78,v116,v117\n"
"v_add_f32 v53,v53,v118\n"
MF "a[140:143],v[28:31],v[92:95],a[140:143]\n"
"s_waitcnt lgkmcnt(0)\n"
TR "v[24:25],v4 offset:26624\n"
TR "v[26:27],v4 offset:32768\n"
MF "a[160:163],v[16:19],v[92:95],a[160:163]\n"
"s_nop 0\n"
TR "v[28:29],v5 offset:26624\n"
TR "v[30:31],v5 offset:32768\n"
MF "a[180:183],v[20:23],v[92:95],a[180:183]\n"
"v_add_f32 v53,v53,v119\n"
CV "v79,v118,v119\n"
MF "a[184:187],v[20:23],v[96:99],a[184:187]\n"
"v_exp_f32 v120,v120\n"
MF "a[164:167],v[16:19],v[96:99],a[164:167]\n"
"v_exp_f32 v121,v121\n"
MF "a[168:171],v[16:19],v[100:103],a[168:171]\n"
"v_exp_f32 v122,v122\n"
MF "a[188:191],v[20:23],v[100:103],a[188:191]\n"
"v_exp_f32 v123,v123\n"
MF "a[192:195],v[20:23],v[104:107],a[192:195]\n"
"v_add_f32 v54,v54,v120\n"
"v_add_f32 v54,v54,v121\n"
MF "a[172:175],v[16:19],v[104:107],a[172:175]\n"
"s_waitcnt vmcnt(6) lgkmcnt(0)\n"
CV "v82,v120,v121\n"
"v_add_f32 v54,v54,v122\n"
MF "a[176:179],v[16:19],v[108:111],a[176:179]\n"
"s_barrier\n"
MF "a[196:199],v[20:23],v[108:111],a[196:199]\n"
"s_add_u32 s72,s72,6144\n"
"ds_read_b128 v[16:19],v0 offset:49152\n"
"ds_read_b128 v[20:23],v1 offset:49152\n"
MF "a[216:219],v[24:27],v[108:111],a[216:219]\n"
"s_cmp_eq_u32 s72,s17\n"
"s_cselect_b32 s72,s16,s72\n"
"s_add_u32 m0,s64,24576\n"
MF "a[236:239],v[28:31],v[108:111],a[236:239]\n"
LD "v7,s[32:35],s72 offen nt lds\n"
MF "a[232:235],v[28:31],v[104:107],a[232:235]\n"
"s_add_u32 m0,s64,25472\n"
"v_add_f32 v54,v54,v123\n"
CV "v83,v122,v123\n"
MF "a[212:215],v[24:27],v[104:107],a[212:215]\n"
LD "v7,s[32:35],s72 offen offset:128 nt lds\n"
MF "a[208:211],v[24:27],v[100:103],a[208:211]\n"
"s_add_u32 m0,s64,26368\n"
MF "a[228:231],v[28:31],v[100:103],a[228:231]\n"
LD "v7,s[32:35],s72 offen offset:256 nt lds\n"
MF "a[224:227],v[28:31],v[96:99],a[224:227]\n"
MF "a[204:207],v[24:27],v[96:99],a[204:207]\n"
MF "a[200:203],v[24:27],v[92:95],a[200:203]\n"
MF "a[220:223],v[28:31],v[92:95],a[220:223]\n"
"s_waitcnt lgkmcnt(0)\n"
MF "v[92:95],v[16:19],v[136:139],v[32:35]\n"
"s_add_u32 s72,s72,6144\n"
"ds_read_b128 v[24:27],v0 offset:50176\n"
"ds_read_b128 v[28:31],v1 offset:50176\n"
MF "v[92:95],v[20:23],v[156:159],v[92:95]\n"
"s_add_u32 m0,s64,30720\n"
"v_exp_f32 v124,v124\n"
MF "v[96:99],v[20:23],v[160:163],v[36:39]\n"
LD "v7,s[32:35],s72 offen nt lds\n"
MF "v[96:99],v[16:19],v[140:143],v[96:99]\n"
"v_exp_f32 v125,v125\n"
MF "v[100:103],v[16:19],v[144:147],v[40:43]\n"
"s_add_u32 m0,s64,31616\n"
"v_exp_f32 v126,v126\n"
MF "v[100:103],v[20:23],v[164:167],v[100:103]\n"
LD "v7,s[32:35],s72 offen offset:128 nt lds\n"
MF "v[104:107],v[20:23],v[168:171],v[44:47]\n"
"v_exp_f32 v127,v127\n"
MF "v[104:107],v[16:19],v[148:151],v[104:107]\n"
"s_add_u32 m0,s64,32512\n"
"v_add_f32 v55,v55,v124\n"
"v_add_f32 v55,v55,v125\n"
MF "v[108:111],v[16:19],v[152:155],v[48:51]\n"
LD "v7,s[32:35],s72 offen offset:256 nt lds\n"
MF "v[108:111],v[20:23],v[172:175],v[108:111]\n"
"s_waitcnt lgkmcnt(0)\n"
"ds_read_b128 v[16:19],v0 offset:51200\n"
"ds_read_b128 v[20:23],v1 offset:51200\n"
MF "v[108:111],v[24:27],v[192:195],v[108:111]\n"
CV "v86,v124,v125\n"
"v_add_f32 v55,v55,v126\n"
MF "v[108:111],v[28:31],v[212:215],v[108:111]\n"
"v_add_f32 v55,v55,v127\n"
CV "v87,v126,v127\n"
MF "v[104:107],v[28:31],v[208:211],v[104:107]\n"
"v_exp_f32 v128,v128\n"
MF "v[104:107],v[24:27],v[188:191],v[104:107]\n"
"v_exp_f32 v129,v129\n"
MF "v[100:103],v[24:27],v[184:187],v[100:103]\n"
"v_exp_f32 v130,v130\n"
MF "v[100:103],v[28:31],v[204:207],v[100:103]\n"
"v_exp_f32 v131,v131\n"
MF "v[96:99],v[28:31],v[200:203],v[96:99]\n"
"v_add_f32 v56,v56,v128\n"
"v_add_f32 v56,v56,v129\n"
MF "v[96:99],v[24:27],v[180:183],v[96:99]\n"
CV "v90,v128,v129\n"
"v_add_f32 v56,v56,v130\n"
MF "v[92:95],v[24:27],v[176:179],v[92:95]\n"
"v_add_f32 v56,v56,v131\n"
CV "v91,v130,v131\n"
MF "v[92:95],v[28:31],v[196:199],v[92:95]\n"
"s_waitcnt lgkmcnt(0)\n"
"ds_read_b128 v[24:27],v0 offset:55296\n"
"ds_read_b128 v[28:31],v1 offset:55296\n"
MF "v[92:95],v[16:19],v[216:219],v[92:95]\n"
MF "v[92:95],v[20:23],v[236:239],v[92:95]\n"
MF "v[96:99],v[20:23],v[240:243],v[96:99]\n"
MF "v[96:99],v[16:19],v[220:223],v[96:99]\n"
MF "v[100:103],v[16:19],v[224:227],v[100:103]\n"
MF "v[100:103],v[20:23],v[244:247],v[100:103]\n"
MF "v[104:107],v[20:23],v[248:251],v[104:107]\n"
MF "v[104:107],v[16:19],v[228:231],v[104:107]\n"
"v_max3_f32 v10,v52,v53,v54\n"
"v_max3_f32 v10,v10,v55,v56\n"
MF "v[108:111],v[16:19],v[232:235],v[108:111]\n"
"v_cmp_ngt_f32 vcc,s77,v10\n"
MF "v[108:111],v[20:23],v[252:255],v[108:111]\n"
"s_waitcnt lgkmcnt(0)\n"
"s_cbranch_vccnz .Lstub_21\n"
".Lback_21:\n"
MF "v[112:115],v[24:27],v[136:139],v[32:35]\n"
"s_add_u32 s74,s74,12288\n"
"ds_read_b128 v[16:19],v0 offset:56320\n"
"ds_read_b128 v[20:23],v1 offset:56320\n"
MF "v[112:115],v[28:31],v[156:159],v[112:115]\n"
"s_cmp_eq_u32 s74,s20\n"
"s_cselect_b32 s74,s70,s74\n"
MF "v[116:119],v[28:31],v[160:163],v[36:39]\n"
"v_exp_f32 v92,v92\n"
MF "v[116:119],v[24:27],v[140:143],v[116:119]\n"
"v_exp_f32 v93,v93\n"
MF "v[120:123],v[24:27],v[144:147],v[40:43]\n"
"v_exp_f32 v94,v94\n"
MF "v[120:123],v[28:31],v[164:167],v[120:123]\n"
"v_exp_f32 v95,v95\n"
MF "v[124:127],v[28:31],v[168:171],v[44:47]\n"
"v_add_f32 v57,v52,v92\n"
"v_add_f32 v57,v57,v93\n"
MF "v[124:127],v[24:27],v[148:151],v[124:127]\n"
CV "v92,v92,v93\n"
"v_add_f32 v57,v57,v94\n"
MF "v[128:131],v[24:27],v[152:155],v[48:51]\n"
"v_add_f32 v57,v57,v95\n"
CV "v93,v94,v95\n"
MF "v[128:131],v[28:31],v[172:175],v[128:131]\n"
"s_waitcnt lgkmcnt(0)\n"
"ds_read_b128 v[24:27],v0 offset:57344\n"
"ds_read_b128 v[28:31],v1 offset:57344\n"
MF "v[128:131],v[16:19],v[192:195],v[128:131]\n"
"v_exp_f32 v96,v96\n"
MF "v[128:131],v[20:23],v[212:215],v[128:131]\n"
"v_exp_f32 v97,v97\n"
MF "v[124:127],v[20:23],v[208:211],v[124:127]\n"
"v_exp_f32 v98,v98\n"
MF "v[124:127],v[16:19],v[188:191],v[124:127]\n"
"v_exp_f32 v99,v99\n"
MF "v[120:123],v[16:19],v[184:187],v[120:123]\n"
"v_add_f32 v58,v53,v96\n"
"v_add_f32 v58,v58,v97\n"
MF "v[120:123],v[20:23],v[204:207],v[120:123]\n"
CV "v96,v96,v97\n"
"v_add_f32 v58,v58,v98\n"
MF "v[116:119],v[20:23],v[200:203],v[116:119]\n"
"s_waitcnt vmcnt(6) lgkmcnt(0)\n"
"v_add_f32 v58,v58,v99\n"
CV "v97,v98,v99\n"
MF "v[116:119],v[16:19],v[180:183],v[116:119]\n"
"s_barrier\n"
MF "v[112:115],v[16:19],v[176:179],v[112:115]\n"
"v_exp_f32 v100,v100\n"
MF "v[112:115],v[20:23],v[196:199],v[112:115]\n"
"s_nop 0\n"
TR "v[16:17],v2\n"
TR "v[18:19],v2 offset:6144\n"
MF "v[112:115],v[24:27],v[216:219],v[112:115]\n"
"s_add_u32 s73,s73,12288\n"
TR "v[20:21],v3\n"
TR "v[22:23],v3 offset:6144\n"
MF "v[112:115],v[28:31],v[236:239],v[112:115]\n"
"s_cmp_eq_u32 s73,s19\n"
"s_cselect_b32 s73,s18,s73\n"
"s_add_u32 m0,s65,49152\n"
MF "v[116:119],v[28:31],v[240:243],v[116:119]\n"
LD "v8,s[36:39],s73 offen nt lds\n"
MF "v[116:119],v[24:27],v[220:223],v[116:119]\n"
"s_add_u32 m0,s65,50048\n"
"v_exp_f32 v101,v101\n"
MF "v[120:123],v[24:27],v[224:227],v[120:123]\n"
LD "v8,s[36:39],s73 offen offset:128 nt lds\n"
MF "v[120:123],v[28:31],v[244:247],v[120:123]\n"
"v_exp_f32 v102,v102\n"
MF "v[124:127],v[28:31],v[248:251],v[124:127]\n"
"s_add_u32 m0,s65,50944\n"
"v_exp_f32 v103,v103\n"
MF "v[124:127],v[24:27],v[228:231],v[124:127]\n"
LD "v8,s[36:39],s73 offen offset:256 nt lds\n"
MF "v[128:131],v[24:27],v[232:235],v[128:131]\n"
"v_add_f32 v59,v54,v100\n"
"v_add_f32 v59,v59,v101\n"
MF "v[128:131],v[28:31],v[252:255],v[128:131]\n"
"s_waitcnt lgkmcnt(0)\n"
TR "v[24:25],v4\n"
TR "v[26:27],v4 offset:6144\n"
MF "a[0:3],v[16:19],v[72:75],a[0:3]\n"
"s_add_u32 m0,s65,49152\n"
TR "v[28:29],v5\n"
TR "v[30:31],v5 offset:6144\n"
MF "a[20:23],v[20:23],v[72:75],a[20:23]\n"
LD "v8,s[36:39],s73 offen offset:3072 nt lds\n"
MF "a[24:27],v[20:23],v[76:79],a[24:27]\n"
CV "v100,v100,v101\n"
"v_add_f32 v59,v59,v102\n"
MF "a[4:7],v[16:19],v[76:79],a[4:7]\n"
"s_add_u32 m0,s65,50048\n"
"v_add_f32 v59,v59,v103\n"
CV "v101,v102,v103\n"
MF "a[8:11],v[16:19],v[80:83],a[8:11]\n"
LD "v8,s[36:39],s73 offen offset:3200 nt lds\n"
MF "a[28:31],v[20:23],v[80:83],a[28:31]\n"
"v_exp_f32 v104,v104\n"
MF "a[32:35],v[20:23],v[84:87],a[32:35]\n"
"s_add_u32 m0,s65,50944\n"
"v_exp_f32 v105,v105\n"
MF "a[12:15],v[16:19],v[84:87],a[12:15]\n"
LD "v8,s[36:39],s73 offen offset:3328 nt lds\n"
MF "a[16:19],v[16:19],v[88:91],a[16:19]\n"
"v_exp_f32 v106,v106\n"
MF "a[36:39],v[20:23],v[88:91],a[36:39]\n"
"s_waitcnt lgkmcnt(0)\n"
TR "v[16:17],v2 offset:1024\n"
TR "v[18:19],v2 offset:7168\n"
MF "a[56:59],v[24:27],v[88:91],a[56:59]\n"
"s_nop 0\n"
TR "v[20:21],v3 offset:1024\n"
TR "v[22:23],v3 offset:7168\n"
MF "a[76:79],v[28:31],v[88:91],a[76:79]\n"
"v_exp_f32 v107,v107\n"
MF "a[72:75],v[28:31],v[84:87],a[72:75]\n"
"v_add_f32 v60,v55,v104\n"
"v_add_f32 v60,v60,v105\n"
MF "a[52:55],v[24:27],v[84:87],a[52:55]\n"
CV "v104,v104,v105\n"
"v_add_f32 v60,v60,v106\n"
MF "a[48:51],v[24:27],v[80:83],a[48:51]\n"
"v_add_f32 v60,v60,v107\n"
CV "v105,v106,v107\n"
MF "a[68:71],v[28:31],v[80:83],a[68:71]\n"
"v_exp_f32 v108,v108\n"
MF "a[64:67],v[28:31],v[76:79],a[64:67]\n"
"v_exp_f32 v109,v109\n"
MF "a[44:47],v[24:27],v[76:79],a[44:47]\n"
"v_exp_f32 v110,v110\n"
MF "a[40:43],v[24:27],v[72:75],a[40:43]\n"
"v_exp_f32 v111,v111\n"
MF "a[60:63],v[28:31],v[72:75],a[60:63]\n"
"s_waitcnt lgkmcnt(0)\n"
TR "v[24:25],v4 offset:1024\n"
TR "v[26:27],v4 offset:7168\n"
MF "a[80:83],v[16:19],v[72:75],a[80:83]\n"
"s_nop 0\n"
TR "v[28:29],v5 offset:1024\n"
TR "v[30:31],v5 offset:7168\n"
MF "a[100:103],v[20:23],v[72:75],a[100:103]\n"
"v_add_f32 v61,v56,v108\n"
"v_add_f32 v61,v61,v109\n"
MF "a[104:107],v[20:23],v[76:79],a[104:107]\n"
CV "v108,v108,v109\n"
"v_add_f32 v61,v61,v110\n"
MF "a[84:87],v[16:19],v[76:79],a[84:87]\n"
"v_add_f32 v61,v61,v111\n"
CV "v109,v110,v111\n"
MF "a[88:91],v[16:19],v[80:83],a[88:91]\n"
"v_exp_f32 v112,v112\n"
MF "a[108:111],v[20:23],v[80:83],a[108:111]\n"
"v_exp_f32 v113,v113\n"
MF "a[112:115],v[20:23],v[84:87],a[112:115]\n"
"v_exp_f32 v114,v114\n"
MF "a[92:95],v[16:19],v[84:87],a[92:95]\n"
"v_exp_f32 v115,v115\n"
MF "a[96:99],v[16:19],v[88:91],a[96:99]\n"
"v_add_f32 v57,v57,v112\n"
"v_add_f32 v57,v57,v113\n"
MF "a[116:119],v[20:23],v[88:91],a[116:119]\n"
"s_waitcnt lgkmcnt(0)\n"
TR "v[16:17],v2 offset:2048\n"
TR "v[18:19],v2 offset:8192\n"
MF "a[136:139],v[24:27],v[88:91],a[136:139]\n"
"s_nop 0\n"
TR "v[20:21],v3 offset:2048\n"
TR "v[22:23],v3 offset:8192\n"
MF "a[156:159],v[28:31],v[88:91],a[156:159]\n"
CV "v94,v112,v113\n"
"v_add_f32 v57,v57,v114\n"
MF "a[152:155],v[28:31],v[84:87],a[152:155]\n"
"v_add_f32 v57,v57,v115\n"
CV "v95,v114,v115\n"
MF "a[132:135],v[24:27],v[84:87],a[132:135]\n"
"v_exp_f32 v116,v116\n"
MF "a[128:131],v[24:27],v[80:83],a[128:131]\n"
"v_exp_f32 v117,v117\n"
MF "a[148:151],v[28:31],v[80:83],a[148:151]\n"
"v_exp_f32 v118,v118\n"
MF "a[144:147],v[28:31],v[76:79],a[144:147]\n"
"v_exp_f32 v119,v119\n"
MF "a[124:127],v[24:27],v[76:79],a[124:127]\n"
"v_add_f32 v58,v58,v116\n"
"v_add_f32 v58,v58,v117\n"
MF "a[120:123],v[24:27],v[72:75],a[120:123]\n"
CV "v98,v116,v117\n"
"v_add_f32 v58,v58,v118\n"
MF "a[140:143],v[28:31],v[72:75],a[140:143]\n"
"s_waitcnt lgkmcnt(0)\n"
TR "v[24:25],v4 offset:2048\n"
TR "v[26:27],v4 offset:8192\n"
MF "a[160:163],v[16:19],v[72:75],a[160:163]\n"
"s_nop 0\n"
TR "v[28:29],v5 offset:2048\n"
TR "v[30:31],v5 offset:8192\n"
MF "a[180:183],v[20:23],v[72:75],a[180:183]\n"
"v_add_f32 v58,v58,v119\n"
CV "v99,v118,v119\n"
MF "a[184:187],v[20:23],v[76:79],a[184:187]\n"
"v_exp_f32 v120,v120\n"
MF "a[164:167],v[16:19],v[76:79],a[164:167]\n"
"v_exp_f32 v121,v121\n"
MF "a[168:171],v[16:19],v[80:83],a[168:171]\n"
"v_exp_f32 v122,v122\n"
MF "a[188:191],v[20:23],v[80:83],a[188:191]\n"
"v_exp_f32 v123,v123\n"
MF "a[192:195],v[20:23],v[84:87],a[192:195]\n"
"v_add_f32 v59,v59,v120\n"
"v_add_f32 v59,v59,v121\n"
MF "a[172:175],v[16:19],v[84:87],a[172:175]\n"
"s_waitcnt vmcnt(6) lgkmcnt(0)\n"
CV "v102,v120,v121\n"
"v_add_f32 v59,v59,v122\n"
MF "a[176:179],v[16:19],v[88:91],a[176:179]\n"
"s_barrier\n"
MF "a[196:199],v[20:23],v[88:91],a[196:199]\n"
"s_add_u32 s72,s72,6144\n"
"ds_read_b128 v[16:19],v0 offset:24576\n"
"ds_read_b128 v[20:23],v1 offset:24576\n"
MF "a[216:219],v[24:27],v[88:91],a[216:219]\n"
"s_cmp_eq_u32 s72,s17\n"
"s_cselect_b32 s72,s16,s72\n"
"s_add_u32 m0,s64,0\n"
MF "a[236:239],v[28:31],v[88:91],a[236:239]\n"
LD "v7,s[32:35],s72 offen nt lds\n"
MF "a[232:235],v[28:31],v[84:87],a[232:235]\n"
"s_add_u32 m0,s64,896\n"
"v_add_f32 v59,v59,v123\n"
CV "v103,v122,v123\n"
MF "a[212:215],v[24:27],v[84:87],a[212:215]\n"
LD "v7,s[32:35],s72 offen offset:128 nt lds\n"
MF "a[208:211],v[24:27],v[80:83],a[208:211]\n"
"s_add_u32 m0,s64,1792\n"
MF "a[228:231],v[28:31],v[80:83],a[228:231]\n"
LD "v7,s[32:35],s72 offen offset:256 nt lds\n"
MF "a[224:227],v[28:31],v[76:79],a[224:227]\n"
MF "a[204:207],v[24:27],v[76:79],a[204:207]\n"
MF "a[200:203],v[24:27],v[72:75],a[200:203]\n"
MF "a[220:223],v[28:31],v[72:75],a[220:223]\n"
"s_waitcnt lgkmcnt(0)\n"
MF "v[72:75],v[16:19],v[136:139],v[32:35]\n"
"s_add_u32 s72,s72,6144\n"
"ds_read_b128 v[24:27],v0 offset:25600\n"
"ds_read_b128 v[28:31],v1 offset:25600\n"
MF "v[72:75],v[20:23],v[156:159],v[72:75]\n"
"s_add_u32 m0,s64,6144\n"
"v_exp_f32 v124,v124\n"
MF "v[76:79],v[20:23],v[160:163],v[36:39]\n"
LD "v7,s[32:35],s72 offen nt lds\n"
MF "v[76:79],v[16:19],v[140:143],v[76:79]\n"
"v_exp_f32 v125,v125\n"
MF "v[80:83],v[16:19],v[144:147],v[40:43]\n"
"s_add_u32 m0,s64,7040\n"
"v_exp_f32 v126,v126\n"
MF "v[80:83],v[20:23],v[164:167],v[80:83]\n"
LD "v7,s[32:35],s72 offen offset:128 nt lds\n"
MF "v[84:87],v[20:23],v[168:171],v[44:47]\n"
"v_exp_f32 v127,v127\n"
MF "v[84:87],v[16:19],v[148:151],v[84:87]\n"
"s_add_u32 m0,s64,7936\n"
"v_add_f32 v60,v60,v124\n"
"v_add_f32 v60,v60,v125\n"
MF "v[88:91],v[16:19],v[152:155],v[48:51]\n"
LD "v7,s[32:35],s72 offen offset:256 nt lds\n"
MF "v[88:91],v[20:23],v[172:175],v[88:91]\n"
"s_waitcnt lgkmcnt(0)\n"
"ds_read_b128 v[16:19],v0 offset:26624\n"
"ds_read_b128 v[20:23],v1 offset:26624\n"
MF "v[88:91],v[24:27],v[192:195],v[88:91]\n"
CV "v106,v124,v125\n"
"v_add_f32 v60,v60,v126\n"
MF "v[88:91],v[28:31],v[212:215],v[88:91]\n"
"v_add_f32 v60,v60,v127\n"
CV "v107,v126,v127\n"
MF "v[84:87],v[28:31],v[208:211],v[84:87]\n"
"v_exp_f32 v128,v128\n"
MF "v[84:87],v[24:27],v[188:191],v[84:87]\n"
"v_exp_f32 v129,v129\n"
MF "v[80:83],v[24:27],v[184:187],v[80:83]\n"
"v_exp_f32 v130,v130\n"
MF "v[80:83],v[28:31],v[204:207],v[80:83]\n"
"v_exp_f32 v131,v131\n"
MF "v[76:79],v[28:31],v[200:203],v[76:79]\n"
"v_add_f32 v61,v61,v128\n"
"v_add_f32 v61,v61,v129\n"
MF "v[76:79],v[24:27],v[180:183],v[76:79]\n"
CV "v110,v128,v129\n"
"v_add_f32 v61,v61,v130\n"
MF "v[72:75],v[24:27],v[176:179],v[72:75]\n"
"v_add_f32 v61,v61,v131\n"
CV "v111,v130,v131\n"
MF "v[72:75],v[28:31],v[196:199],v[72:75]\n"
"s_waitcnt lgkmcnt(0)\n"
"ds_read_b128 v[24:27],v0 offset:30720\n"
"ds_read_b128 v[28:31],v1 offset:30720\n"
MF "v[72:75],v[16:19],v[216:219],v[72:75]\n"
MF "v[72:75],v[20:23],v[236:239],v[72:75]\n"
MF "v[76:79],v[20:23],v[240:243],v[76:79]\n"
MF "v[76:79],v[16:19],v[220:223],v[76:79]\n"
MF "v[80:83],v[16:19],v[224:227],v[80:83]\n"
MF "v[80:83],v[20:23],v[244:247],v[80:83]\n"
MF "v[84:87],v[20:23],v[248:251],v[84:87]\n"
MF "v[84:87],v[16:19],v[228:231],v[84:87]\n"
"v_max3_f32 v10,v57,v58,v59\n"
"v_max3_f32 v10,v10,v60,v61\n"
MF "v[88:91],v[16:19],v[232:235],v[88:91]\n"
"v_cmp_ngt_f32 vcc,s77,v10\n"
MF "v[88:91],v[20:23],v[252:255],v[88:91]\n"
"s_waitcnt lgkmcnt(0)\n"
"s_cbranch_vccnz .Lstub_22\n"
".Lback_22:\n"
MF "v[112:115],v[24:27],v[136:139],v[32:35]\n"
"s_add_u32 s74,s74,12288\n"
"ds_read_b128 v[16:19],v0 offset:31744\n"
"ds_read_b128 v[20:23],v1 offset:31744\n"
MF "v[112:115],v[28:31],v[156:159],v[112:115]\n"
"s_cmp_eq_u32 s74,s20\n"
"s_cselect_b32 s74,s70,s74\n"
MF "v[116:119],v[28:31],v[160:163],v[36:39]\n"
"v_exp_f32 v72,v72\n"
MF "v[116:119],v[24:27],v[140:143],v[116:119]\n"
"v_exp_f32 v73,v73\n"
MF "v[120:123],v[24:27],v[144:147],v[40:43]\n"
"v_exp_f32 v74,v74\n"
MF "v[120:123],v[28:31],v[164:167],v[120:123]\n"
"v_exp_f32 v75,v75\n"
MF "v[124:127],v[28:31],v[168:171],v[44:47]\n"
"v_add_f32 v52,v57,v72\n"
"v_add_f32 v52,v52,v73\n"
MF "v[124:127],v[24:27],v[148:151],v[124:127]\n"
CV "v72,v72,v73\n"
"v_add_f32 v52,v52,v74\n"
MF "v[128:131],v[24:27],v[152:155],v[48:51]\n"
"v_add_f32 v52,v52,v75\n"
CV "v73,v74,v75\n"
MF "v[128:131],v[28:31],v[172:175],v[128:131]\n"
"s_waitcnt lgkmcnt(0)\n"
"ds_read_b128 v[24:27],v0 offset:32768\n"
"ds_read_b128 v[28:31],v1 offset:32768\n"
MF "v[128:131],v[16:19],v[192:195],v[128:131]\n"
"v_exp_f32 v76,v76\n"
MF "v[128:131],v[20:23],v[212:215],v[128:131]\n"
"v_exp_f32 v77,v77\n"
MF "v[124:127],v[20:23],v[208:211],v[124:127]\n"
"v_exp_f32 v78,v78\n"
MF "v[124:127],v[16:19],v[188:191],v[124:127]\n"
"v_exp_f32 v79,v79\n"
MF "v[120:123],v[16:19],v[184:187],v[120:123]\n"
"v_add_f32 v53,v58,v76\n"
"v_add_f32 v53,v53,v77\n"
MF "v[120:123],v[20:23],v[204:207],v[120:123]\n"
CV "v76,v76,v77\n"
"v_add_f32 v53,v53,v78\n"
MF "v[116:119],v[20:23],v[200:203],v[116:119]\n"
"s_waitcnt vmcnt(6) lgkmcnt(0)\n"
"v_add_f32 v53,v53,v79\n"
CV "v77,v78,v79\n"
MF "v[116:119],v[16:19],v[180:183],v[116:119]\n"
"s_barrier\n"
MF "v[112:115],v[16:19],v[176:179],v[112:115]\n"
"v_exp_f32 v80,v80\n"
MF "v[112:115],v[20:23],v[196:199],v[112:115]\n"
"s_nop 0\n"
TR "v[16:17],v2 offset:49152\n"
TR "v[18:19],v2 offset:55296\n"
MF "v[112:115],v[24:27],v[216:219],v[112:115]\n"
"s_add_u32 s73,s73,12288\n"
TR "v[20:21],v3 offset:49152\n"
TR "v[22:23],v3 offset:55296\n"
MF "v[112:115],v[28:31],v[236:239],v[112:115]\n"
"s_cmp_eq_u32 s73,s19\n"
"s_cselect_b32 s73,s18,s73\n"
"s_add_u32 m0,s65,24576\n"
MF "v[116:119],v[28:31],v[240:243],v[116:119]\n"
LD "v8,s[36:39],s73 offen nt lds\n"
MF "v[116:119],v[24:27],v[220:223],v[116:119]\n"
"s_add_u32 m0,s65,25472\n"
"v_exp_f32 v81,v81\n"
MF "v[120:123],v[24:27],v[224:227],v[120:123]\n"
LD "v8,s[36:39],s73 offen offset:128 nt lds\n"
MF "v[120:123],v[28:31],v[244:247],v[120:123]\n"
"v_exp_f32 v82,v82\n"
MF "v[124:127],v[28:31],v[248:251],v[124:127]\n"
"s_add_u32 m0,s65,26368\n"
"v_exp_f32 v83,v83\n"
MF "v[124:127],v[24:27],v[228:231],v[124:127]\n"
LD "v8,s[36:39],s73 offen offset:256 nt lds\n"
MF "v[128:131],v[24:27],v[232:235],v[128:131]\n"
"v_add_f32 v54,v59,v80\n"
"v_add_f32 v54,v54,v81\n"
MF "v[128:131],v[28:31],v[252:255],v[128:131]\n"
"s_waitcnt lgkmcnt(0)\n"
TR "v[24:25],v4 offset:49152\n"
TR "v[26:27],v4 offset:55296\n"
MF "a[0:3],v[16:19],v[92:95],a[0:3]\n"
"s_add_u32 m0,s65,24576\n"
TR "v[28:29],v5 offset:49152\n"
TR "v[30:31],v5 offset:55296\n"
MF "a[20:23],v[20:23],v[92:95],a[20:23]\n"
LD "v8,s[36:39],s73 offen offset:3072 nt lds\n"
MF "a[24:27],v[20:23],v[96:99],a[24:27]\n"
CV "v80,v80,v81\n"
"v_add_f32 v54,v54,v82\n"
MF "a[4:7],v[16:19],v[96:99],a[4:7]\n"
"s_add_u32 m0,s65,25472\n"
"v_add_f32 v54,v54,v83\n"
CV "v81,v82,v83\n"
MF "a[8:11],v[16:19],v[100:103],a[8:11]\n"
LD "v8,s[36:39],s73 offen offset:3200 nt lds\n"
MF "a[28:31],v[20:23],v[100:103],a[28:31]\n"
"v_exp_f32 v84,v84\n"
MF "a[32:35],v[20:23],v[104:107],a[32:35]\n"
"s_add_u32 m0,s65,26368\n"
"v_exp_f32 v85,v85\n"
MF "a[12:15],v[16:19],v[104:107],a[12:15]\n"
LD "v8,s[36:39],s73 offen offset:3328 nt lds\n"
MF "a[16:19],v[16:19],v[108:111],a[16:19]\n"
"v_exp_f32 v86,v86\n"
MF "a[36:39],v[20:23],v[108:111],a[36:39]\n"
"s_waitcnt lgkmcnt(0)\n"
TR "v[16:17],v2 offset:50176\n"
TR "v[18:19],v2 offset:56320\n"
MF "a[56:59],v[24:27],v[108:111],a[56:59]\n"
"s_nop 0\n"
TR "v[20:21],v3 offset:50176\n"
TR "v[22:23],v3 offset:56320\n"
MF "a[76:79],v[28:31],v[108:111],a[76:79]\n"
"v_exp_f32 v87,v87\n"
MF "a[72:75],v[28:31],v[104:107],a[72:75]\n"
"v_add_f32 v55,v60,v84\n"
"v_add_f32 v55,v55,v85\n"
MF "a[52:55],v[24:27],v[104:107],a[52:55]\n"
CV "v84,v84,v85\n"
"v_add_f32 v55,v55,v86\n"
MF "a[48:51],v[24:27],v[100:103],a[48:51]\n"
"v_add_f32 v55,v55,v87\n"
CV "v85,v86,v87\n"
MF "a[68:71],v[28:31],v[100:103],a[68:71]\n"
"v_exp_f32 v88,v88\n"
MF "a[64:67],v[28:31],v[96:99],a[64:67]\n"
"v_exp_f32 v89,v89\n"
MF "a[44:47],v[24:27],v[96:99],a[44:47]\n"
"v_exp_f32 v90,v90\n"
MF "a[40:43],v[24:27],v[92:95],a[40:43]\n"
"v_exp_f32 v91,v91\n"
MF "a[60:63],v[28:31],v[92:95],a[60:63]\n"
"s_waitcnt lgkmcnt(0)\n"
TR "v[24:25],v4 offset:50176\n"
TR "v[26:27],v4 offset:56320\n"
MF "a[80:83],v[16:19],v[92:95],a[80:83]\n"
"s_nop 0\n"
TR "v[28:29],v5 offset:50176\n"
TR "v[30:31],v5 offset:56320\n"
MF "a[100:103],v[20:23],v[92:95],a[100:103]\n"
"v_add_f32 v56,v61,v88\n"
"v_add_f32 v56,v56,v89\n"
MF "a[104:107],v[20:23],v[96:99],a[104:107]\n"
CV "v88,v88,v89\n"
"v_add_f32 v56,v56,v90\n"
MF "a[84:87],v[16:19],v[96:99],a[84:87]\n"
"v_add_f32 v56,v56,v91\n"
CV "v89,v90,v91\n"
MF "a[88:91],v[16:19],v[100:103],a[88:91]\n"
"v_exp_f32 v112,v112\n"
MF "a[108:111],v[20:23],v[100:103],a[108:111]\n"
"v_exp_f32 v113,v113\n"
MF "a[112:115],v[20:23],v[104:107],a[112:115]\n"
"v_exp_f32 v114,v114\n"
MF "a[92:95],v[16:19],v[104:107],a[92:95]\n"
"v_exp_f32 v115,v115\n"
MF "a[96:99],v[16:19],v[108:111],a[96:99]\n"
"v_add_f32 v52,v52,v112\n"
"v_add_f32 v52,v52,v113\n"
MF "a[116:119],v[20:23],v[108:111],a[116:119]\n"
"s_waitcnt lgkmcnt(0)\n"
TR "v[16:17],v2 offset:51200\n"
TR "v[18:19],v2 offset:57344\n"
MF "a[136:139],v[24:27],v[108:111],a[136:139]\n"
"s_nop 0\n"
TR "v[20:21],v3 offset:51200\n"
TR "v[22:23],v3 offset:57344\n"
MF "a[156:159],v[28:31],v[108:111],a[156:159]\n"
CV "v74,v112,v113\n"
"v_add_f32 v52,v52,v114\n"
MF "a[152:155],v[28:31],v[104:107],a[152:155]\n"
"v_add_f32 v52,v52,v115\n"
CV "v75,v114,v115\n"
MF "a[132:135],v[24:27],v[104:107],a[132:135]\n"
"v_exp_f32 v116,v116\n"
MF "a[128:131],v[24:27],v[100:103],a[128:131]\n"
"v_exp_f32 v117,v117\n"
MF "a[148:151],v[28:31],v[100:103],a[148:151]\n"
"v_exp_f32 v118,v118\n"
MF "a[144:147],v[28:31],v[96:99],a[144:147]\n"
"v_exp_f32 v119,v119\n"
MF "a[124:127],v[24:27],v[96:99],a[124:127]\n"
"v_add_f32 v53,v53,v116\n"
"v_add_f32 v53,v53,v117\n"
MF "a[120:123],v[24:27],v[92:95],a[120:123]\n"
CV "v78,v116,v117\n"
"v_add_f32 v53,v53,v118\n"
MF "a[140:143],v[28:31],v[92:95],a[140:143]\n"
"s_waitcnt lgkmcnt(0)\n"
TR "v[24:25],v4 offset:51200\n"
TR "v[26:27],v4 offset:57344\n"
MF "a[160:163],v[16:19],v[92:95],a[160:163]\n"
"s_nop 0\n"
TR "v[28:29],v5 offset:51200\n"
TR "v[30:31],v5 offset:57344\n"
MF "a[180:183],v[20:23],v[92:95],a[180:183]\n"
"v_add_f32 v53,v53,v119\n"
CV "v79,v118,v119\n"
MF "a[184:187],v[20:23],v[96:99],a[184:187]\n"
"v_exp_f32 v120,v120\n"
MF "a[164:167],v[16:19],v[96:99],a[164:167]\n"
"v_exp_f32 v121,v121\n"
MF "a[168:171],v[16:19],v[100:103],a[168:171]\n"
"v_exp_f32 v122,v122\n"
MF "a[188:191],v[20:23],v[100:103],a[188:191]\n"
"v_exp_f32 v123,v123\n"
MF "a[192:195],v[20:23],v[104:107],a[192:195]\n"
"v_add_f32 v54,v54,v120\n"
"v_add_f32 v54,v54,v121\n"
MF "a[172:175],v[16:19],v[104:107],a[172:175]\n"
"s_waitcnt vmcnt(6) lgkmcnt(0)\n"
CV "v82,v120,v121\n"
"v_add_f32 v54,v54,v122\n"
MF "a[176:179],v[16:19],v[108:111],a[176:179]\n"
"s_barrier\n"
MF "a[196:199],v[20:23],v[108:111],a[196:199]\n"
"s_add_u32 s72,s72,6144\n"
"ds_read_b128 v[16:19],v0\n"
"ds_read_b128 v[20:23],v1\n"
MF "a[216:219],v[24:27],v[108:111],a[216:219]\n"
"s_cmp_eq_u32 s72,s17\n"
"s_cselect_b32 s72,s16,s72\n"
"s_add_u32 m0,s64,49152\n"
MF "a[236:239],v[28:31],v[108:111],a[236:239]\n"
LD "v7,s[32:35],s72 offen nt lds\n"
MF "a[232:235],v[28:31],v[104:107],a[232:235]\n"
"s_add_u32 m0,s64,50048\n"
"v_add_f32 v54,v54,v123\n"
CV "v83,v122,v123\n"
MF "a[212:215],v[24:27],v[104:107],a[212:215]\n"
LD "v7,s[32:35],s72 offen offset:128 nt lds\n"
MF "a[208:211],v[24:27],v[100:103],a[208:211]\n"
"s_add_u32 m0,s64,50944\n"
MF "a[228:231],v[28:31],v[100:103],a[228:231]\n"
LD "v7,s[32:35],s72 offen offset:256 nt lds\n"
MF "a[224:227],v[28:31],v[96:99],a[224:227]\n"
MF "a[204:207],v[24:27],v[96:99],a[204:207]\n"
MF "a[200:203],v[24:27],v[92:95],a[200:203]\n"
MF "a[220:223],v[28:31],v[92:95],a[220:223]\n"
"s_waitcnt lgkmcnt(0)\n"
MF "v[92:95],v[16:19],v[136:139],v[32:35]\n"
"s_add_u32 s72,s72,6144\n"
"ds_read_b128 v[24:27],v0 offset:1024\n"
"ds_read_b128 v[28:31],v1 offset:1024\n"
MF "v[92:95],v[20:23],v[156:159],v[92:95]\n"
"s_add_u32 m0,s64,55296\n"
"v_exp_f32 v124,v124\n"
MF "v[96:99],v[20:23],v[160:163],v[36:39]\n"
LD "v7,s[32:35],s72 offen nt lds\n"
MF "v[96:99],v[16:19],v[140:143],v[96:99]\n"
"v_exp_f32 v125,v125\n"
MF "v[100:103],v[16:19],v[144:147],v[40:43]\n"
"s_add_u32 m0,s64,56192\n"
"v_exp_f32 v126,v126\n"
MF "v[100:103],v[20:23],v[164:167],v[100:103]\n"
LD "v7,s[32:35],s72 offen offset:128 nt lds\n"
MF "v[104:107],v[20:23],v[168:171],v[44:47]\n"
"v_exp_f32 v127,v127\n"
MF "v[104:107],v[16:19],v[148:151],v[104:107]\n"
"s_add_u32 m0,s64,57088\n"
"v_add_f32 v55,v55,v124\n"
"v_add_f32 v55,v55,v125\n"
MF "v[108:111],v[16:19],v[152:155],v[48:51]\n"
LD "v7,s[32:35],s72 offen offset:256 nt lds\n"
MF "v[108:111],v[20:23],v[172:175],v[108:111]\n"
"s_waitcnt lgkmcnt(0)\n"
"ds_read_b128 v[16:19],v0 offset:2048\n"
"ds_read_b128 v[20:23],v1 offset:2048\n"
MF "v[108:111],v[24:27],v[192:195],v[108:111]\n"
CV "v86,v124,v125\n"
"v_add_f32 v55,v55,v126\n"
MF "v[108:111],v[28:31],v[212:215],v[108:111]\n"
"v_add_f32 v55,v55,v127\n"
CV "v87,v126,v127\n"
MF "v[104:107],v[28:31],v[208:211],v[104:107]\n"
"v_exp_f32 v128,v128\n"
MF "v[104:107],v[24:27],v[188:191],v[104:107]\n"
"v_exp_f32 v129,v129\n"
MF "v[100:103],v[24:27],v[184:187],v[100:103]\n"
"v_exp_f32 v130,v130\n"
MF "v[100:103],v[28:31],v[204:207],v[100:103]\n"
"v_exp_f32 v131,v131\n"
MF "v[96:99],v[28:31],v[200:203],v[96:99]\n"
"v_add_f32 v56,v56,v128\n"
"v_add_f32 v56,v56,v129\n"
MF "v[96:99],v[24:27],v[180:183],v[96:99]\n"
CV "v90,v128,v129\n"
"v_add_f32 v56,v56,v130\n"
MF "v[92:95],v[24:27],v[176:179],v[92:95]\n"
"v_add_f32 v56,v56,v131\n"
CV "v91,v130,v131\n"
MF "v[92:95],v[28:31],v[196:199],v[92:95]\n"
"s_waitcnt lgkmcnt(0)\n"
"ds_read_b128 v[24:27],v0 offset:6144\n"
"ds_read_b128 v[28:31],v1 offset:6144\n"
MF "v[92:95],v[16:19],v[216:219],v[92:95]\n"
MF "v[92:95],v[20:23],v[236:239],v[92:95]\n"
MF "v[96:99],v[20:23],v[240:243],v[96:99]\n"
MF "v[96:99],v[16:19],v[220:223],v[96:99]\n"
MF "v[100:103],v[16:19],v[224:227],v[100:103]\n"
MF "v[100:103],v[20:23],v[244:247],v[100:103]\n"
MF "v[104:107],v[20:23],v[248:251],v[104:107]\n"
MF "v[104:107],v[16:19],v[228:231],v[104:107]\n"
"v_max3_f32 v10,v52,v53,v54\n"
"v_max3_f32 v10,v10,v55,v56\n"
MF "v[108:111],v[16:19],v[232:235],v[108:111]\n"
"v_cmp_ngt_f32 vcc,s77,v10\n"
MF "v[108:111],v[20:23],v[252:255],v[108:111]\n"
"s_waitcnt lgkmcnt(0)\n"
"s_cbranch_vccnz .Lstub_23\n"
".Lback_23:\n"
MF "v[112:115],v[24:27],v[136:139],v[32:35]\n"
"s_add_u32 s74,s74,12288\n"
"ds_read_b128 v[16:19],v0 offset:7168\n"
"ds_read_b128 v[20:23],v1 offset:7168\n"
MF "v[112:115],v[28:31],v[156:159],v[112:115]\n"
"s_cmp_eq_u32 s74,s20\n"
"s_cselect_b32 s74,s70,s74\n"
MF "v[116:119],v[28:31],v[160:163],v[36:39]\n"
"v_exp_f32 v92,v92\n"
MF "v[116:119],v[24:27],v[140:143],v[116:119]\n"
"v_exp_f32 v93,v93\n"
MF "v[120:123],v[24:27],v[144:147],v[40:43]\n"
"v_exp_f32 v94,v94\n"
MF "v[120:123],v[28:31],v[164:167],v[120:123]\n"
"v_exp_f32 v95,v95\n"
MF "v[124:127],v[28:31],v[168:171],v[44:47]\n"
"v_add_f32 v57,v52,v92\n"
"v_add_f32 v57,v57,v93\n"
MF "v[124:127],v[24:27],v[148:151],v[124:127]\n"
CV "v92,v92,v93\n"
"v_add_f32 v57,v57,v94\n"
MF "v[128:131],v[24:27],v[152:155],v[48:51]\n"
"v_add_f32 v57,v57,v95\n"
CV "v93,v94,v95\n"
MF "v[128:131],v[28:31],v[172:175],v[128:131]\n"
"s_waitcnt lgkmcnt(0)\n"
"ds_read_b128 v[24:27],v0 offset:8192\n"
"ds_read_b128 v[28:31],v1 offset:8192\n"
MF "v[128:131],v[16:19],v[192:195],v[128:131]\n"
"v_exp_f32 v96,v96\n"
MF "v[128:131],v[20:23],v[212:215],v[128:131]\n"
"v_exp_f32 v97,v97\n"
MF "v[124:127],v[20:23],v[208:211],v[124:127]\n"
"v_exp_f32 v98,v98\n"
MF "v[124:127],v[16:19],v[188:191],v[124:127]\n"
"v_exp_f32 v99,v99\n"
MF "v[120:123],v[16:19],v[184:187],v[120:123]\n"
"v_add_f32 v58,v53,v96\n"
"v_add_f32 v58,v58,v97\n"
MF "v[120:123],v[20:23],v[204:207],v[120:123]\n"
CV "v96,v96,v97\n"
"v_add_f32 v58,v58,v98\n"
MF "v[116:119],v[20:23],v[200:203],v[116:119]\n"
"s_waitcnt vmcnt(6) lgkmcnt(0)\n"
"v_add_f32 v58,v58,v99\n"
CV "v97,v98,v99\n"
MF "v[116:119],v[16:19],v[180:183],v[116:119]\n"
"s_barrier\n"
MF "v[112:115],v[16:19],v[176:179],v[112:115]\n"
"v_exp_f32 v100,v100\n"
MF "v[112:115],v[20:23],v[196:199],v[112:115]\n"
"s_nop 0\n"
TR "v[16:17],v2 offset:24576\n"
TR "v[18:19],v2 offset:30720\n"
MF "v[112:115],v[24:27],v[216:219],v[112:115]\n"
"s_add_u32 s73,s73,12288\n"
TR "v[20:21],v3 offset:24576\n"
TR "v[22:23],v3 offset:30720\n"
MF "v[112:115],v[28:31],v[236:239],v[112:115]\n"
"s_cmp_eq_u32 s73,s19\n"
"s_cselect_b32 s73,s18,s73\n"
"s_add_u32 m0,s65,0\n"
MF "v[116:119],v[28:31],v[240:243],v[116:119]\n"
LD "v8,s[36:39],s73 offen nt lds\n"
MF "v[116:119],v[24:27],v[220:223],v[116:119]\n"
"s_add_u32 m0,s65,896\n"
"v_exp_f32 v101,v101\n"
MF "v[120:123],v[24:27],v[224:227],v[120:123]\n"
LD "v8,s[36:39],s73 offen offset:128 nt lds\n"
MF "v[120:123],v[28:31],v[244:247],v[120:123]\n"
"v_exp_f32 v102,v102\n"
MF "v[124:127],v[28:31],v[248:251],v[124:127]\n"
"s_add_u32 m0,s65,1792\n"
"v_exp_f32 v103,v103\n"
MF "v[124:127],v[24:27],v[228:231],v[124:127]\n"
LD "v8,s[36:39],s73 offen offset:256 nt lds\n"
MF "v[128:131],v[24:27],v[232:235],v[128:131]\n"
"v_add_f32 v59,v54,v100\n"
"v_add_f32 v59,v59,v101\n"
MF "v[128:131],v[28:31],v[252:255],v[128:131]\n"
"s_waitcnt lgkmcnt(0)\n"
TR "v[24:25],v4 offset:24576\n"
TR "v[26:27],v4 offset:30720\n"
MF "a[0:3],v[16:19],v[72:75],a[0:3]\n"
"s_add_u32 m0,s65,0\n"
TR "v[28:29],v5 offset:24576\n"
TR "v[30:31],v5 offset:30720\n"
MF "a[20:23],v[20:23],v[72:75],a[20:23]\n"
LD "v8,s[36:39],s73 offen offset:3072 nt lds\n"
MF "a[24:27],v[20:23],v[76:79],a[24:27]\n"
CV "v100,v100,v101\n"
"v_add_f32 v59,v59,v102\n"
MF "a[4:7],v[16:19],v[76:79],a[4:7]\n"
"s_add_u32 m0,s65,896\n"
"v_add_f32 v59,v59,v103\n"
CV "v101,v102,v103\n"
MF "a[8:11],v[16:19],v[80:83],a[8:11]\n"
LD "v8,s[36:39],s73 offen offset:3200 nt lds\n"
MF "a[28:31],v[20:23],v[80:83],a[28:31]\n"
"v_exp_f32 v104,v104\n"
MF "a[32:35],v[20:23],v[84:87],a[32:35]\n"
"s_add_u32 m0,s65,1792\n"
"v_exp_f32 v105,v105\n"
MF "a[12:15],v[16:19],v[84:87],a[12:15]\n"
LD "v8,s[36:39],s73 offen offset:3328 nt lds\n"
MF "a[16:19],v[16:19],v[88:91],a[16:19]\n"
"v_exp_f32 v106,v106\n"
MF "a[36:39],v[20:23],v[88:91],a[36:39]\n"
"s_waitcnt lgkmcnt(0)\n"
TR "v[16:17],v2 offset:25600\n"
TR "v[18:19],v2 offset:31744\n"
MF "a[56:59],v[24:27],v[88:91],a[56:59]\n"
"s_nop 0\n"
TR "v[20:21],v3 offset:25600\n"
TR "v[22:23],v3 offset:31744\n"
MF "a[76:79],v[28:31],v[88:91],a[76:79]\n"
"v_exp_f32 v107,v107\n"
MF "a[72:75],v[28:31],v[84:87],a[72:75]\n"
"v_add_f32 v60,v55,v104\n"
"v_add_f32 v60,v60,v105\n"
MF "a[52:55],v[24:27],v[84:87],a[52:55]\n"
CV "v104,v104,v105\n"
"v_add_f32 v60,v60,v106\n"
MF "a[48:51],v[24:27],v[80:83],a[48:51]\n"
"v_add_f32 v60,v60,v107\n"
CV "v105,v106,v107\n"
MF "a[68:71],v[28:31],v[80:83],a[68:71]\n"
"v_exp_f32 v108,v108\n"
MF "a[64:67],v[28:31],v[76:79],a[64:67]\n"
"v_exp_f32 v109,v109\n"
MF "a[44:47],v[24:27],v[76:79],a[44:47]\n"
"v_exp_f32 v110,v110\n"
MF "a[40:43],v[24:27],v[72:75],a[40:43]\n"
"v_exp_f32 v111,v111\n"
MF "a[60:63],v[28:31],v[72:75],a[60:63]\n"
"s_waitcnt lgkmcnt(0)\n"
TR "v[24:25],v4 offset:25600\n"
TR "v[26:27],v4 offset:31744\n"
MF "a[80:83],v[16:19],v[72:75],a[80:83]\n"
"s_nop 0\n"
TR "v[28:29],v5 offset:25600\n"
TR "v[30:31],v5 offset:31744\n"
MF "a[100:103],v[20:23],v[72:75],a[100:103]\n"
"v_add_f32 v61,v56,v108\n"
"v_add_f32 v61,v61,v109\n"
MF "a[104:107],v[20:23],v[76:79],a[104:107]\n"
CV "v108,v108,v109\n"
"v_add_f32 v61,v61,v110\n"
MF "a[84:87],v[16:19],v[76:79],a[84:87]\n"
"v_add_f32 v61,v61,v111\n"
CV "v109,v110,v111\n"
MF "a[88:91],v[16:19],v[80:83],a[88:91]\n"
"v_exp_f32 v112,v112\n"
MF "a[108:111],v[20:23],v[80:83],a[108:111]\n"
"v_exp_f32 v113,v113\n"
MF "a[112:115],v[20:23],v[84:87],a[112:115]\n"
"v_exp_f32 v114,v114\n"
MF "a[92:95],v[16:19],v[84:87],a[92:95]\n"
"v_exp_f32 v115,v115\n"
MF "a[96:99],v[16:19],v[88:91],a[96:99]\n"
"v_add_f32 v57,v57,v112\n"
"v_add_f32 v57,v57,v113\n"
MF "a[116:119],v[20:23],v[88:91],a[116:119]\n"
"s_waitcnt lgkmcnt(0)\n"
TR "v[16:17],v2 offset:26624\n"
TR "v[18:19],v2 offset:32768\n"
MF "a[136:139],v[24:27],v[88:91],a[136:139]\n"
"s_nop 0\n"
TR "v[20:21],v3 offset:26624\n"
TR "v[22:23],v3 offset:32768\n"
MF "a[156:159],v[28:31],v[88:91],a[156:159]\n"
CV "v94,v112,v113\n"
"v_add_f32 v57,v57,v114\n"
MF "a[152:155],v[28:31],v[84:87],a[152:155]\n"
"v_add_f32 v57,v57,v115\n"
CV "v95,v114,v115\n"
MF "a[132:135],v[24:27],v[84:87],a[132:135]\n"
"v_exp_f32 v116,v116\n"
MF "a[128:131],v[24:27],v[80:83],a[128:131]\n"
"v_exp_f32 v117,v117\n"
MF "a[148:151],v[28:31],v[80:83],a[148:151]\n"
"v_exp_f32 v118,v118\n"
MF "a[144:147],v[28:31],v[76:79],a[144:147]\n"
"v_exp_f32 v119,v119\n"
MF "a[124:127],v[24:27],v[76:79],a[124:127]\n"
"v_add_f32 v58,v58,v116\n"
"v_add_f32 v58,v58,v117\n"
MF "a[120:123],v[24:27],v[72:75],a[120:123]\n"
CV "v98,v116,v117\n"
"v_add_f32 v58,v58,v118\n"
MF "a[140:143],v[28:31],v[72:75],a[140:143]\n"
"s_waitcnt lgkmcnt(0)\n"
TR "v[24:25],v4 offset:26624\n"
TR "v[26:27],v4 offset:32768\n"
MF "a[160:163],v[16:19],v[72:75],a[160:163]\n"
"s_nop 0\n"
TR "v[28:29],v5 offset:26624\n"
TR "v[30:31],v5 offset:32768\n"
MF "a[180:183],v[20:23],v[72:75],a[180:183]\n"
"v_add_f32 v58,v58,v119\n"
CV "v99,v118,v119\n"
MF "a[184:187],v[20:23],v[76:79],a[184:187]\n"
"v_exp_f32 v120,v120\n"
MF "a[164:167],v[16:19],v[76:79],a[164:167]\n"
"v_exp_f32 v121,v121\n"
MF "a[168:171],v[16:19],v[80:83],a[168:171]\n"
"v_exp_f32 v122,v122\n"
MF "a[188:191],v[20:23],v[80:83],a[188:191]\n"
"v_exp_f32 v123,v123\n"
MF "a[192:195],v[20:23],v[84:87],a[192:195]\n"
"v_add_f32 v59,v59,v120\n"
"v_add_f32 v59,v59,v121\n"
MF "a[172:175],v[16:19],v[84:87],a[172:175]\n"
"s_waitcnt vmcnt(6) lgkmcnt(0)\n"
CV "v102,v120,v121\n"
"v_add_f32 v59,v59,v122\n"
MF "a[176:179],v[16:19],v[88:91],a[176:179]\n"
"s_barrier\n"
MF "a[196:199],v[20:23],v[88:91],a[196:199]\n"
"s_add_u32 s72,s72,6144\n"
"ds_read_b128 v[16:19],v0 offset:49152\n"
"ds_read_b128 v[20:23],v1 offset:49152\n"
MF "a[216:219],v[24:27],v[88:91],a[216:219]\n"
"s_cmp_eq_u32 s72,s17\n"
"s_cselect_b32 s72,s16,s72\n"
"s_add_u32 m0,s64,24576\n"
MF "a[236:239],v[28:31],v[88:91],a[236:239]\n"
LD "v7,s[32:35],s72 offen nt lds\n"
MF "a[232:235],v[28:31],v[84:87],a[232:235]\n"
"s_add_u32 m0,s64,25472\n"
"v_add_f32 v59,v59,v123\n"
CV "v103,v122,v123\n"
MF "a[212:215],v[24:27],v[84:87],a[212:215]\n"
LD "v7,s[32:35],s72 offen offset:128 nt lds\n"
MF "a[208:211],v[24:27],v[80:83],a[208:211]\n"
"s_add_u32 m0,s64,26368\n"
MF "a[228:231],v[28:31],v[80:83],a[228:231]\n"
LD "v7,s[32:35],s72 offen offset:256 nt lds\n"
MF "a[224:227],v[28:31],v[76:79],a[224:227]\n"
MF "a[204:207],v[24:27],v[76:79],a[204:207]\n"
MF "a[200:203],v[24:27],v[72:75],a[200:203]\n"
MF "a[220:223],v[28:31],v[72:75],a[220:223]\n"
"s_waitcnt lgkmcnt(0)\n"
MF "v[72:75],v[16:19],v[136:139],v[32:35]\n"
"s_add_u32 s72,s72,6144\n"
"ds_read_b128 v[24:27],v0 offset:50176\n"
"ds_read_b128 v[28:31],v1 offset:50176\n"
MF "v[72:75],v[20:23],v[156:159],v[72:75]\n"
"s_add_u32 m0,s64,30720\n"
"v_exp_f32 v124,v124\n"
MF "v[76:79],v[20:23],v[160:163],v[36:39]\n"
LD "v7,s[32:35],s72 offen nt lds\n"
MF "v[76:79],v[16:19],v[140:143],v[76:79]\n"
"v_exp_f32 v125,v125\n"
MF "v[80:83],v[16:19],v[144:147],v[40:43]\n"
"s_add_u32 m0,s64,31616\n"
"v_exp_f32 v126,v126\n"
MF "v[80:83],v[20:23],v[164:167],v[80:83]\n"
LD "v7,s[32:35],s72 offen offset:128 nt lds\n"
MF "v[84:87],v[20:23],v[168:171],v[44:47]\n"
"v_exp_f32 v127,v127\n"
MF "v[84:87],v[16:19],v[148:151],v[84:87]\n"
"s_add_u32 m0,s64,32512\n"
"v_add_f32 v60,v60,v124\n"
"v_add_f32 v60,v60,v125\n"
MF "v[88:91],v[16:19],v[152:155],v[48:51]\n"
LD "v7,s[32:35],s72 offen offset:256 nt lds\n"
MF "v[88:91],v[20:23],v[172:175],v[88:91]\n"
"s_waitcnt lgkmcnt(0)\n"
"ds_read_b128 v[16:19],v0 offset:51200\n"
"ds_read_b128 v[20:23],v1 offset:51200\n"
MF "v[88:91],v[24:27],v[192:195],v[88:91]\n"
CV "v106,v124,v125\n"
"v_add_f32 v60,v60,v126\n"
MF "v[88:91],v[28:31],v[212:215],v[88:91]\n"
"v_add_f32 v60,v60,v127\n"
CV "v107,v126,v127\n"
MF "v[84:87],v[28:31],v[208:211],v[84:87]\n"
"v_exp_f32 v128,v128\n"
MF "v[84:87],v[24:27],v[188:191],v[84:87]\n"
"v_exp_f32 v129,v129\n"
MF "v[80:83],v[24:27],v[184:187],v[80:83]\n"
"v_exp_f32 v130,v130\n"
MF "v[80:83],v[28:31],v[204:207],v[80:83]\n"
"v_exp_f32 v131,v131\n"
MF "v[76:79],v[28:31],v[200:203],v[76:79]\n"
"v_add_f32 v61,v61,v128\n"
"v_add_f32 v61,v61,v129\n"
MF "v[76:79],v[24:27],v[180:183],v[76:79]\n"
CV "v110,v128,v129\n"
"v_add_f32 v61,v61,v130\n"
MF "v[72:75],v[24:27],v[176:179],v[72:75]\n"
"v_add_f32 v61,v61,v131\n"
CV "v111,v130,v131\n"
MF "v[72:75],v[28:31],v[196:199],v[72:75]\n"
"s_waitcnt lgkmcnt(0)\n"
"ds_read_b128 v[24:27],v0 offset:55296\n"
"ds_read_b128 v[28:31],v1 offset:55296\n"
MF "v[72:75],v[16:19],v[216:219],v[72:75]\n"
MF "v[72:75],v[20:23],v[236:239],v[72:75]\n"
MF "v[76:79],v[20:23],v[240:243],v[76:79]\n"
MF "v[76:79],v[16:19],v[220:223],v[76:79]\n"
MF "v[80:83],v[16:19],v[224:227],v[80:83]\n"
MF "v[80:83],v[20:23],v[244:247],v[80:83]\n"
MF "v[84:87],v[20:23],v[248:251],v[84:87]\n"
MF "v[84:87],v[16:19],v[228:231],v[84:87]\n"
"v_max3_f32 v10,v57,v58,v59\n"
"v_max3_f32 v10,v10,v60,v61\n"
MF "v[88:91],v[16:19],v[232:235],v[88:91]\n"
"v_cmp_ngt_f32 vcc,s95,v10\n"
MF "v[88:91],v[20:23],v[252:255],v[88:91]\n"
"s_waitcnt lgkmcnt(0)\n"
"s_cbranch_vccnz .Lstub_24\n"
".Lback_24:\n"
MF "v[112:115],v[24:27],v[136:139],v[32:35]\n"
"s_add_u32 s74,s74,12288\n"
"ds_read_b128 v[16:19],v0 offset:56320\n"
"ds_read_b128 v[20:23],v1 offset:56320\n"
MF "v[112:115],v[28:31],v[156:159],v[112:115]\n"
"s_cmp_eq_u32 s74,s20\n"
"s_cselect_b32 s74,s70,s74\n"
MF "v[116:119],v[28:31],v[160:163],v[36:39]\n"
"v_exp_f32 v72,v72\n"
MF "v[116:119],v[24:27],v[140:143],v[116:119]\n"
"v_exp_f32 v73,v73\n"
MF "v[120:123],v[24:27],v[144:147],v[40:43]\n"
"v_exp_f32 v74,v74\n"
MF "v[120:123],v[28:31],v[164:167],v[120:123]\n"
"v_exp_f32 v75,v75\n"
MF "v[124:127],v[28:31],v[168:171],v[44:47]\n"
"v_add_f32 v52,v57,v72\n"
"v_add_f32 v52,v52,v73\n"
MF "v[124:127],v[24:27],v[148:151],v[124:127]\n"
CV "v72,v72,v73\n"
"v_add_f32 v52,v52,v74\n"
MF "v[128:131],v[24:27],v[152:155],v[48:51]\n"
"v_add_f32 v52,v52,v75\n"
CV "v73,v74,v75\n"
MF "v[128:131],v[28:31],v[172:175],v[128:131]\n"
"s_waitcnt lgkmcnt(0)\n"
"ds_read_b128 v[24:27],v0 offset:57344\n"
"ds_read_b128 v[28:31],v1 offset:57344\n"
MF "v[128:131],v[16:19],v[192:195],v[128:131]\n"
"v_exp_f32 v76,v76\n"
MF "v[128:131],v[20:23],v[212:215],v[128:131]\n"
"v_exp_f32 v77,v77\n"
MF "v[124:127],v[20:23],v[208:211],v[124:127]\n"
"v_exp_f32 v78,v78\n"
MF "v[124:127],v[16:19],v[188:191],v[124:127]\n"
"v_exp_f32 v79,v79\n"
MF "v[120:123],v[16:19],v[184:187],v[120:123]\n"
"v_add_f32 v53,v58,v76\n"
"v_add_f32 v53,v53,v77\n"
MF "v[120:123],v[20:23],v[204:207],v[120:123]\n"
CV "v76,v76,v77\n"
"v_add_f32 v53,v53,v78\n"
MF "v[116:119],v[20:23],v[200:203],v[116:119]\n"
"s_waitcnt vmcnt(6) lgkmcnt(0)\n"
"v_add_f32 v53,v53,v79\n"
CV "v77,v78,v79\n"
MF "v[116:119],v[16:19],v[180:183],v[116:119]\n"
"s_barrier\n"
MF "v[112:115],v[16:19],v[176:179],v[112:115]\n"
"v_exp_f32 v80,v80\n"
MF "v[112:115],v[20:23],v[196:199],v[112:115]\n"
"s_nop 0\n"
TR "v[16:17],v2\n"
TR "v[18:19],v2 offset:6144\n"
MF "v[112:115],v[24:27],v[216:219],v[112:115]\n"
"s_add_u32 s73,s73,12288\n"
TR "v[20:21],v3\n"
TR "v[22:23],v3 offset:6144\n"
MF "v[112:115],v[28:31],v[236:239],v[112:115]\n"
"s_cmp_eq_u32 s73,s19\n"
"s_cselect_b32 s73,s18,s73\n"
"s_add_u32 m0,s65,49152\n"
MF "v[116:119],v[28:31],v[240:243],v[116:119]\n"
LD "v8,s[36:39],s73 offen nt lds\n"
MF "v[116:119],v[24:27],v[220:223],v[116:119]\n"
"s_add_u32 m0,s65,50048\n"
"v_exp_f32 v81,v81\n"
MF "v[120:123],v[24:27],v[224:227],v[120:123]\n"
LD "v8,s[36:39],s73 offen offset:128 nt lds\n"
MF "v[120:123],v[28:31],v[244:247],v[120:123]\n"
"v_exp_f32 v82,v82\n"
MF "v[124:127],v[28:31],v[248:251],v[124:127]\n"
"s_add_u32 m0,s65,50944\n"
"v_exp_f32 v83,v83\n"
MF "v[124:127],v[24:27],v[228:231],v[124:127]\n"
LD "v8,s[36:39],s73 offen offset:256 nt lds\n"
MF "v[128:131],v[24:27],v[232:235],v[128:131]\n"
"v_add_f32 v54,v59,v80\n"
"v_add_f32 v54,v54,v81\n"
MF "v[128:131],v[28:31],v[252:255],v[128:131]\n"
"s_waitcnt lgkmcnt(0)\n"
TR "v[24:25],v4\n"
TR "v[26:27],v4 offset:6144\n"
MF "a[0:3],v[16:19],v[92:95],a[0:3]\n"
"s_add_u32 m0,s65,49152\n"
TR "v[28:29],v5\n"
TR "v[30:31],v5 offset:6144\n"
MF "a[20:23],v[20:23],v[92:95],a[20:23]\n"
LD "v8,s[36:39],s73 offen offset:3072 nt lds\n"
MF "a[24:27],v[20:23],v[96:99],a[24:27]\n"
CV "v80,v80,v81\n"
"v_add_f32 v54,v54,v82\n"
MF "a[4:7],v[16:19],v[96:99],a[4:7]\n"
"s_add_u32 m0,s65,50048\n"
"v_add_f32 v54,v54,v83\n"
CV "v81,v82,v83\n"
MF "a[8:11],v[16:19],v[100:103],a[8:11]\n"
LD "v8,s[36:39],s73 offen offset:3200 nt lds\n"
MF "a[28:31],v[20:23],v[100:103],a[28:31]\n"
"v_exp_f32 v84,v84\n"
MF "a[32:35],v[20:23],v[104:107],a[32:35]\n"
"s_add_u32 m0,s65,50944\n"
"v_exp_f32 v85,v85\n"
MF "a[12:15],v[16:19],v[104:107],a[12:15]\n"
LD "v8,s[36:39],s73 offen offset:3328 nt lds\n"
MF "a[16:19],v[16:19],v[108:111],a[16:19]\n"
"v_exp_f32 v86,v86\n"
MF "a[36:39],v[20:23],v[108:111],a[36:39]\n"
"s_waitcnt lgkmcnt(0)\n"
TR "v[16:17],v2 offset:1024\n"
TR "v[18:19],v2 offset:7168\n"
MF "a[56:59],v[24:27],v[108:111],a[56:59]\n"
"s_nop 0\n"
TR "v[20:21],v3 offset:1024\n"
TR "v[22:23],v3 offset:7168\n"
MF "a[76:79],v[28:31],v[108:111],a[76:79]\n"
"v_exp_f32 v87,v87\n"
MF "a[72:75],v[28:31],v[104:107],a[72:75]\n"
"v_add_f32 v55,v60,v84\n"
"v_add_f32 v55,v55,v85\n"
MF "a[52:55],v[24:27],v[104:107],a[52:55]\n"
CV "v84,v84,v85\n"
"v_add_f32 v55,v55,v86\n"
MF "a[48:51],v[24:27],v[100:103],a[48:51]\n"
"v_add_f32 v55,v55,v87\n"
CV "v85,v86,v87\n"
MF "a[68:71],v[28:31],v[100:103],a[68:71]\n"
"v_exp_f32 v88,v88\n"
MF "a[64:67],v[28:31],v[96:99],a[64:67]\n"
"v_exp_f32 v89,v89\n"
MF "a[44:47],v[24:27],v[96:99],a[44:47]\n"
"v_exp_f32 v90,v90\n"
MF "a[40:43],v[24:27],v[92:95],a[40:43]\n"
"v_exp_f32 v91,v91\n"
MF "a[60:63],v[28:31],v[92:95],a[60:63]\n"
"s_waitcnt lgkmcnt(0)\n"
TR "v[24:25],v4 offset:1024\n"
TR "v[26:27],v4 offset:7168\n"
MF "a[80:83],v[16:19],v[92:95],a[80:83]\n"
"s_nop 0\n"
TR "v[28:29],v5 offset:1024\n"
TR "v[30:31],v5 offset:7168\n"
MF "a[100:103],v[20:23],v[92:95],a[100:103]\n"
"v_add_f32 v56,v61,v88\n"
"v_add_f32 v56,v56,v89\n"
MF "a[104:107],v[20:23],v[96:99],a[104:107]\n"
CV "v88,v88,v89\n"
"v_add_f32 v56,v56,v90\n"
MF "a[84:87],v[16:19],v[96:99],a[84:87]\n"
"v_add_f32 v56,v56,v91\n"
CV "v89,v90,v91\n"
MF "a[88:91],v[16:19],v[100:103],a[88:91]\n"
"v_exp_f32 v112,v112\n"
MF "a[108:111],v[20:23],v[100:103],a[108:111]\n"
"v_exp_f32 v113,v113\n"
MF "a[112:115],v[20:23],v[104:107],a[112:115]\n"
"v_exp_f32 v114,v114\n"
MF "a[92:95],v[16:19],v[104:107],a[92:95]\n"
"v_exp_f32 v115,v115\n"
MF "a[96:99],v[16:19],v[108:111],a[96:99]\n"
"v_add_f32 v52,v52,v112\n"
"v_add_f32 v52,v52,v113\n"
MF "a[116:119],v[20:23],v[108:111],a[116:119]\n"
"s_waitcnt lgkmcnt(0)\n"
TR "v[16:17],v2 offset:2048\n"
TR "v[18:19],v2 offset:8192\n"
MF "a[136:139],v[24:27],v[108:111],a[136:139]\n"
"s_nop 0\n"
TR "v[20:21],v3 offset:2048\n"
TR "v[22:23],v3 offset:8192\n"
MF "a[156:159],v[28:31],v[108:111],a[156:159]\n"
CV "v74,v112,v113\n"
"v_add_f32 v52,v52,v114\n"
MF "a[152:155],v[28:31],v[104:107],a[152:155]\n"
"v_add_f32 v52,v52,v115\n"
CV "v75,v114,v115\n"
MF "a[132:135],v[24:27],v[104:107],a[132:135]\n"
"v_exp_f32 v116,v116\n"
MF "a[128:131],v[24:27],v[100:103],a[128:131]\n"
"v_exp_f32 v117,v117\n"
MF "a[148:151],v[28:31],v[100:103],a[148:151]\n"
"v_exp_f32 v118,v118\n"
MF "a[144:147],v[28:31],v[96:99],a[144:147]\n"
"v_exp_f32 v119,v119\n"
MF "a[124:127],v[24:27],v[96:99],a[124:127]\n"
"v_add_f32 v53,v53,v116\n"
"v_add_f32 v53,v53,v117\n"
MF "a[120:123],v[24:27],v[92:95],a[120:123]\n"
CV "v78,v116,v117\n"
"v_add_f32 v53,v53,v118\n"
MF "a[140:143],v[28:31],v[92:95],a[140:143]\n"
"s_waitcnt lgkmcnt(0)\n"
TR "v[24:25],v4 offset:2048\n"
TR "v[26:27],v4 offset:8192\n"
MF "a[160:163],v[16:19],v[92:95],a[160:163]\n"
"s_nop 0\n"
TR "v[28:29],v5 offset:2048\n"
TR "v[30:31],v5 offset:8192\n"
MF "a[180:183],v[20:23],v[92:95],a[180:183]\n"
"v_add_f32 v53,v53,v119\n"
CV "v79,v118,v119\n"
MF "a[184:187],v[20:23],v[96:99],a[184:187]\n"
"v_exp_f32 v120,v120\n"
MF "a[164:167],v[16:19],v[96:99],a[164:167]\n"
"v_exp_f32 v121,v121\n"
MF "a[168:171],v[16:19],v[100:103],a[168:171]\n"
"v_exp_f32 v122,v122\n"
MF "a[188:191],v[20:23],v[100:103],a[188:191]\n"
"v_exp_f32 v123,v123\n"
MF "a[192:195],v[20:23],v[104:107],a[192:195]\n"
"v_add_f32 v54,v54,v120\n"
"v_add_f32 v54,v54,v121\n"
MF "a[172:175],v[16:19],v[104:107],a[172:175]\n"
"s_waitcnt vmcnt(6) lgkmcnt(0)\n"
CV "v82,v120,v121\n"
"v_add_f32 v54,v54,v122\n"
MF "a[176:179],v[16:19],v[108:111],a[176:179]\n"
"s_barrier\n"
MF "a[196:199],v[20:23],v[108:111],a[196:199]\n"
"s_add_u32 s72,s72,6144\n"
"ds_read_b128 v[16:19],v0 offset:24576\n"
"ds_read_b128 v[20:23],v1 offset:24576\n"
MF "a[216:219],v[24:27],v[108:111],a[216:219]\n"
"s_cmp_eq_u32 s72,s17\n"
"s_cselect_b32 s72,s16,s72\n"
"s_add_u32 m0,s64,0\n"
MF "a[236:239],v[28:31],v[108:111],a[236:239]\n"
LD "v7,s[32:35],s72 offen nt lds\n"
MF "a[232:235],v[28:31],v[104:107],a[232:235]\n"
"s_add_u32 m0,s64,896\n"
"v_add_f32 v54,v54,v123\n"
CV "v83,v122,v123\n"
MF "a[212:215],v[24:27],v[104:107],a[212:215]\n"
LD "v7,s[32:35],s72 offen offset:128 nt lds\n"
MF "a[208:211],v[24:27],v[100:103],a[208:211]\n"
"s_add_u32 m0,s64,1792\n"
MF "a[228:231],v[28:31],v[100:103],a[228:231]\n"
LD "v7,s[32:35],s72 offen offset:256 nt lds\n"
MF "a[224:227],v[28:31],v[96:99],a[224:227]\n"
MF "a[204:207],v[24:27],v[96:99],a[204:207]\n"
MF "a[200:203],v[24:27],v[92:95],a[200:203]\n"
MF "a[220:223],v[28:31],v[92:95],a[220:223]\n"
"s_waitcnt lgkmcnt(0)\n"
MF "v[92:95],v[16:19],v[136:139],v[32:35]\n"
"s_add_u32 s72,s72,6144\n"
"ds_read_b128 v[24:27],v0 offset:25600\n"
"ds_read_b128 v[28:31],v1 offset:25600\n"
MF "v[92:95],v[20:23],v[156:159],v[92:95]\n"
"s_add_u32 m0,s64,6144\n"
"v_exp_f32 v124,v124\n"
MF "v[96:99],v[20:23],v[160:163],v[36:39]\n"
LD "v7,s[32:35],s72 offen nt lds\n"
MF "v[96:99],v[16:19],v[140:143],v[96:99]\n"
"v_exp_f32 v125,v125\n"
MF "v[100:103],v[16:19],v[144:147],v[40:43]\n"
"s_add_u32 m0,s64,7040\n"
"v_exp_f32 v126,v126\n"
MF "v[100:103],v[20:23],v[164:167],v[100:103]\n"
LD "v7,s[32:35],s72 offen offset:128 nt lds\n"
MF "v[104:107],v[20:23],v[168:171],v[44:47]\n"
"v_exp_f32 v127,v127\n"
MF "v[104:107],v[16:19],v[148:151],v[104:107]\n"
"s_add_u32 m0,s64,7936\n"
"v_add_f32 v55,v55,v124\n"
"v_add_f32 v55,v55,v125\n"
MF "v[108:111],v[16:19],v[152:155],v[48:51]\n"
LD "v7,s[32:35],s72 offen offset:256 nt lds\n"
MF "v[108:111],v[20:23],v[172:175],v[108:111]\n"
"s_waitcnt lgkmcnt(0)\n"
"ds_read_b128 v[16:19],v0 offset:26624\n"
"ds_read_b128 v[20:23],v1 offset:26624\n"
MF "v[108:111],v[24:27],v[192:195],v[108:111]\n"
CV "v86,v124,v125\n"
"v_add_f32 v55,v55,v126\n"
MF "v[108:111],v[28:31],v[212:215],v[108:111]\n"
"v_add_f32 v55,v55,v127\n"
CV "v87,v126,v127\n"
MF "v[104:107],v[28:31],v[208:211],v[104:107]\n"
"v_exp_f32 v128,v128\n"
MF "v[104:107],v[24:27],v[188:191],v[104:107]\n"
"v_exp_f32 v129,v129\n"
MF "v[100:103],v[24:27],v[184:187],v[100:103]\n"
"v_exp_f32 v130,v130\n"
MF "v[100:103],v[28:31],v[204:207],v[100:103]\n"
"v_exp_f32 v131,v131\n"
MF "v[96:99],v[28:31],v[200:203],v[96:99]\n"
"v_add_f32 v56,v56,v128\n"
"v_add_f32 v56,v56,v129\n"
MF "v[96:99],v[24:27],v[180:183],v[96:99]\n"
CV "v90,v128,v129\n"
"v_add_f32 v56,v56,v130\n"
MF "v[92:95],v[24:27],v[176:179],v[92:95]\n"
"v_add_f32 v56,v56,v131\n"
CV "v91,v130,v131\n"
MF "v[92:95],v[28:31],v[196:199],v[92:95]\n"
"s_waitcnt lgkmcnt(0)\n"
"ds_read_b128 v[24:27],v0 offset:30720\n"
"ds_read_b128 v[28:31],v1 offset:30720\n"
MF "v[92:95],v[16:19],v[216:219],v[92:95]\n"
MF "v[92:95],v[20:23],v[236:239],v[92:95]\n"
MF "v[96:99],v[20:23],v[240:243],v[96:99]\n"
MF "v[96:99],v[16:19],v[220:223],v[96:99]\n"
MF "v[100:103],v[16:19],v[224:227],v[100:103]\n"
MF "v[100:103],v[20:23],v[244:247],v[100:103]\n"
MF "v[104:107],v[20:23],v[248:251],v[104:107]\n"
MF "v[104:107],v[16:19],v[228:231],v[104:107]\n"
"v_max3_f32 v10,v52,v53,v54\n"
"v_max3_f32 v10,v10,v55,v56\n"
MF "v[108:111],v[16:19],v[232:235],v[108:111]\n"
"v_cmp_ngt_f32 vcc,s77,v10\n"
MF "v[108:111],v[20:23],v[252:255],v[108:111]\n"
"s_waitcnt lgkmcnt(0)\n"
"s_cbranch_vccnz .Lstub_25\n"
".Lback_25:\n"
MF "v[112:115],v[24:27],v[136:139],v[32:35]\n"
"s_add_u32 s74,s74,12288\n"
"ds_read_b128 v[16:19],v0 offset:31744\n"
"ds_read_b128 v[20:23],v1 offset:31744\n"
MF "v[112:115],v[28:31],v[156:159],v[112:115]\n"
"s_cmp_eq_u32 s74,s20\n"
"s_cselect_b32 s74,s70,s74\n"
MF "v[116:119],v[28:31],v[160:163],v[36:39]\n"
"v_exp_f32 v92,v92\n"
MF "v[116:119],v[24:27],v[140:143],v[116:119]\n"
"v_exp_f32 v93,v93\n"
MF "v[120:123],v[24:27],v[144:147],v[40:43]\n"
"v_exp_f32 v94,v94\n"
MF "v[120:123],v[28:31],v[164:167],v[120:123]\n"
"v_exp_f32 v95,v95\n"
MF "v[124:127],v[28:31],v[168:171],v[44:47]\n"
"v_add_f32 v57,v52,v92\n"
"v_add_f32 v57,v57,v93\n"
MF "v[124:127],v[24:27],v[148:151],v[124:127]\n"
CV "v92,v92,v93\n"
"v_add_f32 v57,v57,v94\n"
MF "v[128:131],v[24:27],v[152:155],v[48:51]\n"
"v_add_f32 v57,v57,v95\n"
CV "v93,v94,v95\n"
MF "v[128:131],v[28:31],v[172:175],v[128:131]\n"
"s_waitcnt lgkmcnt(0)\n"
"ds_read_b128 v[24:27],v0 offset:32768\n"
"ds_read_b128 v[28:31],v1 offset:32768\n"
MF "v[128:131],v[16:19],v[192:195],v[128:131]\n"
"s_sub_u32 s61,s61,1\n"
"v_exp_f32 v96,v96\n"
MF "v[128:131],v[20:23],v[212:215],v[128:131]\n"
"s_mov_b32 s95,0\n"
"v_exp_f32 v97,v97\n"
MF "v[124:127],v[20:23],v[208:211],v[124:127]\n"
"v_exp_f32 v98,v98\n"
MF "v[124:127],v[16:19],v[188:191],v[124:127]\n"
"v_exp_f32 v99,v99\n"
MF "v[120:123],v[16:19],v[184:187],v[120:123]\n"
"v_add_f32 v58,v53,v96\n"
"v_add_f32 v58,v58,v97\n"
MF "v[120:123],v[20:23],v[204:207],v[120:123]\n"
CV "v96,v96,v97\n"
"v_add_f32 v58,v58,v98\n"
MF "v[116:119],v[20:23],v[200:203],v[116:119]\n"
"s_waitcnt vmcnt(6) lgkmcnt(0)\n"
"v_add_f32 v58,v58,v99\n"
CV "v97,v98,v99\n"
MF "v[116:119],v[16:19],v[180:183],v[116:119]\n"
"s_barrier\n"
MF "v[112:115],v[16:19],v[176:179],v[112:115]\n"
"v_exp_f32 v100,v100\n"
MF "v[112:115],v[20:23],v[196:199],v[112:115]\n"
"s_nop 0\n"
TR "v[16:17],v2 offset:49152\n"
TR "v[18:19],v2 offset:55296\n"
MF "v[112:115],v[24:27],v[216:219],v[112:115]\n"
"s_add_u32 s73,s73,12288\n"
TR "v[20:21],v3 offset:49152\n"
TR "v[22:23],v3 offset:55296\n"
MF "v[112:115],v[28:31],v[236:239],v[112:115]\n"
"s_cmp_eq_u32 s73,s19\n"
"s_cselect_b32 s73,s18,s73\n"
"s_add_u32 m0,s65,24576\n"
MF "v[116:119],v[28:31],v[240:243],v[116:119]\n"
LD "v8,s[36:39],s73 offen nt lds\n"
MF "v[116:119],v[24:27],v[220:223],v[116:119]\n"
"s_add_u32 m0,s65,25472\n"
"v_exp_f32 v101,v101\n"
MF "v[120:123],v[24:27],v[224:227],v[120:123]\n"
LD "v8,s[36:39],s73 offen offset:128 nt lds\n"
MF "v[120:123],v[28:31],v[244:247],v[120:123]\n"
"v_exp_f32 v102,v102\n"
MF "v[124:127],v[28:31],v[248:251],v[124:127]\n"
"s_add_u32 m0,s65,26368\n"
"v_exp_f32 v103,v103\n"
MF "v[124:127],v[24:27],v[228:231],v[124:127]\n"
LD "v8,s[36:39],s73 offen offset:256 nt lds\n"
MF "v[128:131],v[24:27],v[232:235],v[128:131]\n"
"v_add_f32 v59,v54,v100\n"
"v_add_f32 v59,v59,v101\n"
MF "v[128:131],v[28:31],v[252:255],v[128:131]\n"
"s_waitcnt lgkmcnt(0)\n"
TR "v[24:25],v4 offset:49152\n"
TR "v[26:27],v4 offset:55296\n"
MF "a[0:3],v[16:19],v[72:75],a[0:3]\n"
"s_add_u32 m0,s65,24576\n"
TR "v[28:29],v5 offset:49152\n"
TR "v[30:31],v5 offset:55296\n"
MF "a[20:23],v[20:23],v[72:75],a[20:23]\n"
LD "v8,s[36:39],s73 offen offset:3072 nt lds\n"
MF "a[24:27],v[20:23],v[76:79],a[24:27]\n"
CV "v100,v100,v101\n"
"v_add_f32 v59,v59,v102\n"
MF "a[4:7],v[16:19],v[76:79],a[4:7]\n"
"s_add_u32 m0,s65,25472\n"
"v_add_f32 v59,v59,v103\n"
CV "v101,v102,v103\n"
MF "a[8:11],v[16:19],v[80:83],a[8:11]\n"
LD "v8,s[36:39],s73 offen offset:3200 nt lds\n"
MF "a[28:31],v[20:23],v[80:83],a[28:31]\n"
"v_exp_f32 v104,v104\n"
MF "a[32:35],v[20:23],v[84:87],a[32:35]\n"
"s_add_u32 m0,s65,26368\n"
"v_exp_f32 v105,v105\n"
MF "a[12:15],v[16:19],v[84:87],a[12:15]\n"
LD "v8,s[36:39],s73 offen offset:3328 nt lds\n"
MF "a[16:19],v[16:19],v[88:91],a[16:19]\n"
"v_exp_f32 v106,v106\n"
MF "a[36:39],v[20:23],v[88:91],a[36:39]\n"
"s_waitcnt lgkmcnt(0)\n"
TR "v[16:17],v2 offset:50176\n"
TR "v[18:19],v2 offset:56320\n"
MF "a[56:59],v[24:27],v[88:91],a[56:59]\n"
"s_nop 0\n"
TR "v[20:21],v3 offset:50176\n"
TR "v[22:23],v3 offset:56320\n"
MF "a[76:79],v[28:31],v[88:91],a[76:79]\n"
"v_exp_f32 v107,v107\n"
MF "a[72:75],v[28:31],v[84:87],a[72:75]\n"
"v_add_f32 v60,v55,v104\n"
"v_add_f32 v60,v60,v105\n"
MF "a[52:55],v[24:27],v[84:87],a[52:55]\n"
CV "v104,v104,v105\n"
"v_add_f32 v60,v60,v106\n"
MF "a[48:51],v[24:27],v[80:83],a[48:51]\n"
"v_add_f32 v60,v60,v107\n"
CV "v105,v106,v107\n"
MF "a[68:71],v[28:31],v[80:83],a[68:71]\n"
"v_exp_f32 v108,v108\n"
MF "a[64:67],v[28:31],v[76:79],a[64:67]\n"
"v_exp_f32 v109,v109\n"
MF "a[44:47],v[24:27],v[76:79],a[44:47]\n"
"v_exp_f32 v110,v110\n"
MF "a[40:43],v[24:27],v[72:75],a[40:43]\n"
"v_exp_f32 v111,v111\n"
MF "a[60:63],v[28:31],v[72:75],a[60:63]\n"
"s_waitcnt lgkmcnt(0)\n"
TR "v[24:25],v4 offset:50176\n"
TR "v[26:27],v4 offset:56320\n"
MF "a[80:83],v[16:19],v[72:75],a[80:83]\n"
"s_nop 0\n"
TR "v[28:29],v5 offset:50176\n"
TR "v[30:31],v5 offset:56320\n"
MF "a[100:103],v[20:23],v[72:75],a[100:103]\n"
"v_add_f32 v61,v56,v108\n"
"v_add_f32 v61,v61,v109\n"
MF "a[104:107],v[20:23],v[76:79],a[104:107]\n"
CV "v108,v108,v109\n"
"v_add_f32 v61,v61,v110\n"
MF "a[84:87],v[16:19],v[76:79],a[84:87]\n"
"v_add_f32 v61,v61,v111\n"
CV "v109,v110,v111\n"
MF "a[88:91],v[16:19],v[80:83],a[88:91]\n"
"v_exp_f32 v112,v112\n"
MF "a[108:111],v[20:23],v[80:83],a[108:111]\n"
"v_exp_f32 v113,v113\n"
MF "a[112:115],v[20:23],v[84:87],a[112:115]\n"
"v_exp_f32 v114,v114\n"
MF "a[92:95],v[16:19],v[84:87],a[92:95]\n"
"v_exp_f32 v115,v115\n"
MF "a[96:99],v[16:19],v[88:91],a[96:99]\n"
"v_add_f32 v57,v57,v112\n"
"v_add_f32 v57,v57,v113\n"
MF "a[116:119],v[20:23],v[88:91],a[116:119]\n"
"s_waitcnt lgkmcnt(0)\n"
TR "v[16:17],v2 offset:51200\n"
TR "v[18:19],v2 offset:57344\n"
MF "a[136:139],v[24:27],v[88:91],a[136:139]\n"
"s_nop 0\n"
TR "v[20:21],v3 offset:51200\n"
TR "v[22:23],v3 offset:57344\n"
MF "a[156:159],v[28:31],v[88:91],a[156:159]\n"
CV "v94,v112,v113\n"
"v_add_f32 v57,v57,v114\n"
MF "a[152:155],v[28:31],v[84:87],a[152:155]\n"
"v_add_f32 v57,v57,v115\n"
CV "v95,v114,v115\n"
MF "a[132:135],v[24:27],v[84:87],a[132:135]\n"
"v_exp_f32 v116,v116\n"
MF "a[128:131],v[24:27],v[80:83],a[128:131]\n"
"v_exp_f32 v117,v117\n"
MF "a[148:151],v[28:31],v[80:83],a[148:151]\n"
"v_exp_f32 v118,v118\n"
MF "a[144:147],v[28:31],v[76:79],a[144:147]\n"
"v_exp_f32 v119,v119\n"
MF "a[124:127],v[24:27],v[76:79],a[124:127]\n"
"v_add_f32 v58,v58,v116\n"
"v_add_f32 v58,v58,v117\n"
MF "a[120:123],v[24:27],v[72:75],a[120:123]\n"
CV "v98,v116,v117\n"
"v_add_f32 v58,v58,v118\n"
MF "a[140:143],v[28:31],v[72:75],a[140:143]\n"
"s_waitcnt lgkmcnt(0)\n"
TR "v[24:25],v4 offset:51200\n"
TR "v[26:27],v4 offset:57344\n"
MF "a[160:163],v[16:19],v[72:75],a[160:163]\n"
"s_nop 0\n"
TR "v[28:29],v5 offset:51200\n"
TR "v[30:31],v5 offset:57344\n"
MF "a[180:183],v[20:23],v[72:75],a[180:183]\n"
"v_add_f32 v58,v58,v119\n"
CV "v99,v118,v119\n"
MF "a[184:187],v[20:23],v[76:79],a[184:187]\n"
"v_exp_f32 v120,v120\n"
MF "a[164:167],v[16:19],v[76:79],a[164:167]\n"
"v_exp_f32 v121,v121\n"
MF "a[168:171],v[16:19],v[80:83],a[168:171]\n"
"v_exp_f32 v122,v122\n"
MF "a[188:191],v[20:23],v[80:83],a[188:191]\n"
"v_exp_f32 v123,v123\n"
MF "a[192:195],v[20:23],v[84:87],a[192:195]\n"
"v_add_f32 v59,v59,v120\n"
"v_add_f32 v59,v59,v121\n"
MF "a[172:175],v[16:19],v[84:87],a[172:175]\n"
"s_waitcnt vmcnt(6) lgkmcnt(0)\n"
CV "v102,v120,v121\n"
"v_add_f32 v59,v59,v122\n"
MF "a[176:179],v[16:19],v[88:91],a[176:179]\n"
"s_barrier\n"
MF "a[196:199],v[20:23],v[88:91],a[196:199]\n"
"s_add_u32 s72,s72,6144\n"
"ds_read_b128 v[16:19],v0\n"
"ds_read_b128 v[20:23],v1\n"
MF "a[216:219],v[24:27],v[88:91],a[216:219]\n"
"s_cmp_eq_u32 s72,s17\n"
"s_cselect_b32 s72,s16,s72\n"
"s_add_u32 m0,s64,49152\n"
MF "a[236:239],v[28:31],v[88:91],a[236:239]\n"
LD "v7,s[32:35],s72 offen nt lds\n"
MF "a[232:235],v[28:31],v[84:87],a[232:235]\n"
"s_add_u32 m0,s64,50048\n"
"v_add_f32 v59,v59,v123\n"
CV "v103,v122,v123\n"
MF "a[212:215],v[24:27],v[84:87],a[212:215]\n"
LD "v7,s[32:35],s72 offen offset:128 nt lds\n"
MF "a[208:211],v[24:27],v[80:83],a[208:211]\n"
"s_add_u32 m0,s64,50944\n"
MF "a[228:231],v[28:31],v[80:83],a[228:231]\n"
LD "v7,s[32:35],s72 offen offset:256 nt lds\n"
MF "a[224:227],v[28:31],v[76:79],a[224:227]\n"
MF "a[204:207],v[24:27],v[76:79],a[204:207]\n"
"s_waitcnt lgkmcnt(0)\n"
MF "a[200:203],v[24:27],v[72:75],a[200:203]\n"
"s_cmp_lg_u32 s61,0\n"
MF "a[220:223],v[28:31],v[72:75],a[220:223]\n"
"s_cbranch_scc1 .Laloop\n"
".Lepi2:\n"
MF "v[72:75],v[16:19],v[136:139],v[32:35]\n"
"s_add_u32 s72,s72,6144\n"
"ds_read_b128 v[24:27],v0 offset:1024\n"
"ds_read_b128 v[28:31],v1 offset:1024\n"
MF "v[72:75],v[20:23],v[156:159],v[72:75]\n"
"s_add_u32 m0,s64,55296\n"
"v_exp_f32 v124,v124\n"
MF "v[76:79],v[20:23],v[160:163],v[36:39]\n"
LD "v7,s[32:35],s72 offen nt lds\n"
MF "v[76:79],v[16:19],v[140:143],v[76:79]\n"
"v_exp_f32 v125,v125\n"
MF "v[80:83],v[16:19],v[144:147],v[40:43]\n"
"s_add_u32 m0,s64,56192\n"
"v_exp_f32 v126,v126\n"
MF "v[80:83],v[20:23],v[164:167],v[80:83]\n"
LD "v7,s[32:35],s72 offen offset:128 nt lds\n"
MF "v[84:87],v[20:23],v[168:171],v[44:47]\n"
"v_exp_f32 v127,v127\n"
MF "v[84:87],v[16:19],v[148:151],v[84:87]\n"
"s_add_u32 m0,s64,57088\n"
"v_add_f32 v60,v60,v124\n"
"v_add_f32 v60,v60,v125\n"
MF "v[88:91],v[16:19],v[152:155],v[48:51]\n"
LD "v7,s[32:35],s72 offen offset:256 nt lds\n"
MF "v[88:91],v[20:23],v[172:175],v[88:91]\n"
"s_waitcnt lgkmcnt(0)\n"
"ds_read_b128 v[16:19],v0 offset:2048\n"
"ds_read_b128 v[20:23],v1 offset:2048\n"
MF "v[88:91],v[24:27],v[192:195],v[88:91]\n"
CV "v106,v124,v125\n"
"v_add_f32 v60,v60,v126\n"
MF "v[88:91],v[28:31],v[212:215],v[88:91]\n"
"v_add_f32 v60,v60,v127\n"
CV "v107,v126,v127\n"
MF "v[84:87],v[28:31],v[208:211],v[84:87]\n"
"v_exp_f32 v128,v128\n"
MF "v[84:87],v[24:27],v[188:191],v[84:87]\n"
"v_exp_f32 v129,v129\n"
MF "v[80:83],v[24:27],v[184:187],v[80:83]\n"
"v_exp_f32 v130,v130\n"
MF "v[80:83],v[28:31],v[204:207],v[80:83]\n"
"v_exp_f32 v131,v131\n"
MF "v[76:79],v[28:31],v[200:203],v[76:79]\n"
"v_add_f32 v61,v61,v128\n"
"v_add_f32 v61,v61,v129\n"
MF "v[76:79],v[24:27],v[180:183],v[76:79]\n"
CV "v110,v128,v129\n"
"v_add_f32 v61,v61,v130\n"
MF "v[72:75],v[24:27],v[176:179],v[72:75]\n"
"v_add_f32 v61,v61,v131\n"
CV "v111,v130,v131\n"
MF "v[72:75],v[28:31],v[196:199],v[72:75]\n"
"s_waitcnt lgkmcnt(0)\n"
"ds_read_b128 v[24:27],v0 offset:6144\n"
"ds_read_b128 v[28:31],v1 offset:6144\n"
MF "v[72:75],v[16:19],v[216:219],v[72:75]\n"
MF "v[72:75],v[20:23],v[236:239],v[72:75]\n"
MF "v[76:79],v[20:23],v[240:243],v[76:79]\n"
MF "v[76:79],v[16:19],v[220:223],v[76:79]\n"
MF "v[80:83],v[16:19],v[224:227],v[80:83]\n"
MF "v[80:83],v[20:23],v[244:247],v[80:83]\n"
MF "v[84:87],v[20:23],v[248:251],v[84:87]\n"
MF "v[84:87],v[16:19],v[228:231],v[84:87]\n"
"v_max3_f32 v10,v57,v58,v59\n"
"v_max3_f32 v10,v10,v60,v61\n"
MF "v[88:91],v[16:19],v[232:235],v[88:91]\n"
"v_cmp_le_f32 vcc,0x47800000,v10\n"
MF "v[88:91],v[20:23],v[252:255],v[88:91]\n"
"s_waitcnt lgkmcnt(0)\n"
"s_cbranch_vccnz .Lstub_26\n"
".Lback_26:\n"
MF "v[112:115],v[24:27],v[136:139],v[32:35]\n"
"s_add_u32 s74,s74,12288\n"
"ds_read_b128 v[16:19],v0 offset:7168\n"
"ds_read_b128 v[20:23],v1 offset:7168\n"
MF "v[112:115],v[28:31],v[156:159],v[112:115]\n"
"s_cmp_eq_u32 s74,s20\n"
"s_cselect_b32 s74,s70,s74\n"
MF "v[116:119],v[28:31],v[160:163],v[36:39]\n"
"v_exp_f32 v72,v72\n"
MF "v[116:119],v[24:27],v[140:143],v[116:119]\n"
"v_exp_f32 v73,v73\n"
MF "v[120:123],v[24:27],v[144:147],v[40:43]\n"
"v_exp_f32 v74,v74\n"
MF "v[120:123],v[28:31],v[164:167],v[120:123]\n"
"v_exp_f32 v75,v75\n"
MF "v[124:127],v[28:31],v[168:171],v[44:47]\n"
"v_add_f32 v52,v57,v72\n"
"v_add_f32 v52,v52,v73\n"
MF "v[124:127],v[24:27],v[148:151],v[124:127]\n"
CV "v72,v72,v73\n"
"v_add_f32 v52,v52,v74\n"
MF "v[128:131],v[24:27],v[152:155],v[48:51]\n"
"v_add_f32 v52,v52,v75\n"
CV "v73,v74,v75\n"
MF "v[128:131],v[28:31],v[172:175],v[128:131]\n"
"s_waitcnt lgkmcnt(0)\n"
"ds_read_b128 v[24:27],v0 offset:8192\n"
"ds_read_b128 v[28:31],v1 offset:8192\n"
MF "v[128:131],v[16:19],v[192:195],v[128:131]\n"
"v_exp_f32 v76,v76\n"
MF "v[128:131],v[20:23],v[212:215],v[128:131]\n"
"v_exp_f32 v77,v77\n"
MF "v[124:127],v[20:23],v[208:211],v[124:127]\n"
"v_exp_f32 v78,v78\n"
MF "v[124:127],v[16:19],v[188:191],v[124:127]\n"
"v_exp_f32 v79,v79\n"
MF "v[120:123],v[16:19],v[184:187],v[120:123]\n"
"v_add_f32 v53,v58,v76\n"
"v_add_f32 v53,v53,v77\n"
MF "v[120:123],v[20:23],v[204:207],v[120:123]\n"
CV "v76,v76,v77\n"
"v_add_f32 v53,v53,v78\n"
MF "v[116:119],v[20:23],v[200:203],v[116:119]\n"
"s_waitcnt vmcnt(6) lgkmcnt(0)\n"
"v_add_f32 v53,v53,v79\n"
CV "v77,v78,v79\n"
MF "v[116:119],v[16:19],v[180:183],v[116:119]\n"
"s_barrier\n"
MF "v[112:115],v[16:19],v[176:179],v[112:115]\n"
"v_lshrrev_b32 v7,7,v9\n"
"v_mad_u32_u24 v7,v7,s6,v8\n"
MF "v[112:115],v[20:23],v[196:199],v[112:115]\n"
"s_nop 0\n"
TR "v[16:17],v2 offset:24576\n"
TR "v[18:19],v2 offset:30720\n"
MF "v[112:115],v[24:27],v[216:219],v[112:115]\n"
"s_add_u32 s73,s73,12288\n"
TR "v[20:21],v3 offset:24576\n"
TR "v[22:23],v3 offset:30720\n"
MF "v[112:115],v[28:31],v[236:239],v[112:115]\n"
"s_cmp_eq_u32 s73,s19\n"
"s_cselect_b32 s73,s18,s73\n"
"s_add_u32 m0,s65,0\n"
MF "v[116:119],v[28:31],v[240:243],v[116:119]\n"
LD "v8,s[36:39],s73 offen nt lds\n"
MF "v[116:119],v[24:27],v[220:223],v[116:119]\n"
"s_add_u32 m0,s65,896\n"
"v_exp_f32 v80,v80\n"
MF "v[120:123],v[24:27],v[224:227],v[120:123]\n"
LD "v8,s[36:39],s73 offen offset:128 nt lds\n"
MF "v[120:123],v[28:31],v[244:247],v[120:123]\n"
"v_exp_f32 v81,v81\n"
MF "v[124:127],v[28:31],v[248:251],v[124:127]\n"
"s_add_u32 m0,s65,1792\n"
"v_exp_f32 v82,v82\n"
MF "v[124:127],v[24:27],v[228:231],v[124:127]\n"
LD "v8,s[36:39],s73 offen offset:256 nt lds\n"
MF "v[128:131],v[24:27],v[232:235],v[128:131]\n"
"v_exp_f32 v83,v83\n"
MF "v[128:131],v[28:31],v[252:255],v[128:131]\n"
"s_waitcnt lgkmcnt(0)\n"
TR "v[24:25],v4 offset:24576\n"
TR "v[26:27],v4 offset:30720\n"
MF "a[0:3],v[16:19],v[92:95],a[0:3]\n"
"s_add_u32 m0,s65,0\n"
TR "v[28:29],v5 offset:24576\n"
TR "v[30:31],v5 offset:30720\n"
MF "a[20:23],v[20:23],v[92:95],a[20:23]\n"
LD "v8,s[36:39],s73 offen offset:3072 nt lds\n"
MF "a[24:27],v[20:23],v[96:99],a[24:27]\n"
"v_add_f32 v54,v59,v80\n"
"v_add_f32 v54,v54,v81\n"
MF "a[4:7],v[16:19],v[96:99],a[4:7]\n"
"s_add_u32 m0,s65,896\n"
CV "v80,v80,v81\n"
"v_add_f32 v54,v54,v82\n"
MF "a[8:11],v[16:19],v[100:103],a[8:11]\n"
LD "v8,s[36:39],s73 offen offset:3200 nt lds\n"
MF "a[28:31],v[20:23],v[100:103],a[28:31]\n"
"v_add_f32 v54,v54,v83\n"
CV "v81,v82,v83\n"
MF "a[32:35],v[20:23],v[104:107],a[32:35]\n"
"s_add_u32 m0,s65,1792\n"
"v_exp_f32 v84,v84\n"
MF "a[12:15],v[16:19],v[104:107],a[12:15]\n"
LD "v8,s[36:39],s73 offen offset:3328 nt lds\n"
MF "a[16:19],v[16:19],v[108:111],a[16:19]\n"
"v_exp_f32 v85,v85\n"
MF "a[36:39],v[20:23],v[108:111],a[36:39]\n"
"s_waitcnt lgkmcnt(0)\n"
TR "v[16:17],v2 offset:25600\n"
TR "v[18:19],v2 offset:31744\n"
MF "a[56:59],v[24:27],v[108:111],a[56:59]\n"
"s_nop 0\n"
TR "v[20:21],v3 offset:25600\n"
TR "v[22:23],v3 offset:31744\n"
MF "a[76:79],v[28:31],v[108:111],a[76:79]\n"
"v_exp_f32 v86,v86\n"
MF "a[72:75],v[28:31],v[104:107],a[72:75]\n"
"v_exp_f32 v87,v87\n"
MF "a[52:55],v[24:27],v[104:107],a[52:55]\n"
"v_add_f32 v55,v60,v84\n"
"v_add_f32 v55,v55,v85\n"
MF "a[48:51],v[24:27],v[100:103],a[48:51]\n"
CV "v84,v84,v85\n"
"v_add_f32 v55,v55,v86\n"
MF "a[68:71],v[28:31],v[100:103],a[68:71]\n"
"v_add_f32 v55,v55,v87\n"
CV "v85,v86,v87\n"
MF "a[64:67],v[28:31],v[96:99],a[64:67]\n"
"v_exp_f32 v88,v88\n"
MF "a[44:47],v[24:27],v[96:99],a[44:47]\n"
"v_exp_f32 v89,v89\n"
MF "a[40:43],v[24:27],v[92:95],a[40:43]\n"
"v_exp_f32 v90,v90\n"
MF "a[60:63],v[28:31],v[92:95],a[60:63]\n"
"s_waitcnt lgkmcnt(0)\n"
TR "v[24:25],v4 offset:25600\n"
TR "v[26:27],v4 offset:31744\n"
MF "a[80:83],v[16:19],v[92:95],a[80:83]\n"
"s_nop 0\n"
TR "v[28:29],v5 offset:25600\n"
TR "v[30:31],v5 offset:31744\n"
MF "a[100:103],v[20:23],v[92:95],a[100:103]\n"
"v_exp_f32 v91,v91\n"
MF "a[104:107],v[20:23],v[96:99],a[104:107]\n"
"v_add_f32 v56,v61,v88\n"
"v_add_f32 v56,v56,v89\n"
MF "a[84:87],v[16:19],v[96:99],a[84:87]\n"
CV "v88,v88,v89\n"
"v_add_f32 v56,v56,v90\n"
MF "a[88:91],v[16:19],v[100:103],a[88:91]\n"
"v_add_f32 v56,v56,v91\n"
CV "v89,v90,v91\n"
MF "a[108:111],v[20:23],v[100:103],a[108:111]\n"
"v_exp_f32 v112,v112\n"
MF "a[112:115],v[20:23],v[104:107],a[112:115]\n"
"v_exp_f32 v113,v113\n"
MF "a[92:95],v[16:19],v[104:107],a[92:95]\n"
"v_exp_f32 v114,v114\n"
MF "a[96:99],v[16:19],v[108:111],a[96:99]\n"
"v_exp_f32 v115,v115\n"
MF "a[116:119],v[20:23],v[108:111],a[116:119]\n"
"s_waitcnt lgkmcnt(0)\n"
TR "v[16:17],v2 offset:26624\n"
TR "v[18:19],v2 offset:32768\n"
MF "a[136:139],v[24:27],v[108:111],a[136:139]\n"
"s_nop 0\n"
TR "v[20:21],v3 offset:26624\n"
TR "v[22:23],v3 offset:32768\n"
MF "a[156:159],v[28:31],v[108:111],a[156:159]\n"
"v_add_f32 v52,v52,v112\n"
"v_add_f32 v52,v52,v113\n"
MF "a[152:155],v[28:31],v[104:107],a[152:155]\n"
CV "v74,v112,v113\n"
"v_add_f32 v52,v52,v114\n"
MF "a[132:135],v[24:27],v[104:107],a[132:135]\n"
"v_add_f32 v52,v52,v115\n"
CV "v75,v114,v115\n"
MF "a[128:131],v[24:27],v[100:103],a[128:131]\n"
"v_exp_f32 v116,v116\n"
MF "a[148:151],v[28:31],v[100:103],a[148:151]\n"
"v_exp_f32 v117,v117\n"
MF "a[144:147],v[28:31],v[96:99],a[144:147]\n"
"v_exp_f32 v118,v118\n"
MF "a[124:127],v[24:27],v[96:99],a[124:127]\n"
"v_exp_f32 v119,v119\n"
MF "a[120:123],v[24:27],v[92:95],a[120:123]\n"
"v_add_f32 v53,v53,v116\n"
"v_add_f32 v53,v53,v117\n"
MF "a[140:143],v[28:31],v[92:95],a[140:143]\n"
"s_waitcnt lgkmcnt(0)\n"
TR "v[24:25],v4 offset:26624\n"
TR "v[26:27],v4 offset:32768\n"
MF "a[160:163],v[16:19],v[92:95],a[160:163]\n"
"s_nop 0\n"
TR "v[28:29],v5 offset:26624\n"
TR "v[30:31],v5 offset:32768\n"
MF "a[180:183],v[20:23],v[92:95],a[180:183]\n"
CV "v78,v116,v117\n"
"v_add_f32 v53,v53,v118\n"
MF "a[184:187],v[20:23],v[96:99],a[184:187]\n"
"v_add_f32 v53,v53,v119\n"
CV "v79,v118,v119\n"
MF "a[164:167],v[16:19],v[96:99],a[164:167]\n"
"v_exp_f32 v120,v120\n"
MF "a[168:171],v[16:19],v[100:103],a[168:171]\n"
"v_exp_f32 v121,v121\n"
MF "a[188:191],v[20:23],v[100:103],a[188:191]\n"
"v_exp_f32 v122,v122\n"
MF "a[192:195],v[20:23],v[104:107],a[192:195]\n"
"v_exp_f32 v123,v123\n"
MF "a[172:175],v[16:19],v[104:107],a[172:175]\n"
"s_waitcnt vmcnt(6) lgkmcnt(0)\n"
"v_add_f32 v54,v54,v120\n"
"v_add_f32 v54,v54,v121\n"
MF "a[176:179],v[16:19],v[108:111],a[176:179]\n"
"s_barrier\n"
MF "a[196:199],v[20:23],v[108:111],a[196:199]\n"
"s_add_u32 s60,s0,0\n"
"ds_read_b128 v[16:19],v0 offset:49152\n"
"ds_read_b128 v[20:23],v1 offset:49152\n"
MF "a[216:219],v[24:27],v[108:111],a[216:219]\n"
"s_add_u32 m0,s5,24576\n"
CV "v82,v120,v121\n"
"v_add_f32 v54,v54,v122\n"
MF "a[236:239],v[28:31],v[108:111],a[236:239]\n"
LD "v7,s[48:51],s60 offen lds\n"
MF "a[232:235],v[28:31],v[104:107],a[232:235]\n"
"s_add_u32 s60,s0,10240\n"
"s_add_u32 m0,s5,25600\n"
"v_add_f32 v54,v54,v123\n"
MF "a[212:215],v[24:27],v[104:107],a[212:215]\n"
LD "v7,s[48:51],s60 offen lds\n"
MF "a[208:211],v[24:27],v[100:103],a[208:211]\n"
"s_add_u32 s60,s0,20480\n"
"s_add_u32 m0,s5,26624\n"
CV "v83,v122,v123\n"
MF "a[228:231],v[28:31],v[100:103],a[228:231]\n"
LD "v7,s[48:51],s60 offen lds\n"
MF "a[224:227],v[28:31],v[96:99],a[224:227]\n"
MF "a[204:207],v[24:27],v[96:99],a[204:207]\n"
MF "a[200:203],v[24:27],v[92:95],a[200:203]\n"
MF "a[220:223],v[28:31],v[92:95],a[220:223]\n"
"s_waitcnt lgkmcnt(0)\n"
MF "v[92:95],v[16:19],v[136:139],v[32:35]\n"
"s_add_u32 s60,s0,30720\n"
"ds_read_b128 v[24:27],v0 offset:50176\n"
"ds_read_b128 v[28:31],v1 offset:50176\n"
MF "v[92:95],v[20:23],v[156:159],v[92:95]\n"
"s_add_u32 m0,s5,27648\n"
"v_exp_f32 v124,v124\n"
MF "v[96:99],v[20:23],v[160:163],v[36:39]\n"
LD "v7,s[48:51],s60 offen lds\n"
MF "v[96:99],v[16:19],v[140:143],v[96:99]\n"
"s_add_u32 s60,s0,40960\n"
"v_exp_f32 v125,v125\n"
MF "v[100:103],v[16:19],v[144:147],v[40:43]\n"
"s_add_u32 m0,s5,28672\n"
"v_exp_f32 v126,v126\n"
MF "v[100:103],v[20:23],v[164:167],v[100:103]\n"
LD "v7,s[48:51],s60 offen lds\n"
MF "v[104:107],v[20:23],v[168:171],v[44:47]\n"
"s_add_u32 s60,s0,51200\n"
"v_exp_f32 v127,v127\n"
MF "v[104:107],v[16:19],v[148:151],v[104:107]\n"
"s_add_u32 m0,s5,29696\n"
"v_add_f32 v55,v55,v124\n"
"v_add_f32 v55,v55,v125\n"
MF "v[108:111],v[16:19],v[152:155],v[48:51]\n"
LD "v7,s[48:51],s60 offen lds\n"
MF "v[108:111],v[20:23],v[172:175],v[108:111]\n"
"s_waitcnt lgkmcnt(0)\n"
"ds_read_b128 v[16:19],v0 offset:51200\n"
"ds_read_b128 v[20:23],v1 offset:51200\n"
MF "v[108:111],v[24:27],v[192:195],v[108:111]\n"
CV "v86,v124,v125\n"
"v_add_f32 v55,v55,v126\n"
MF "v[108:111],v[28:31],v[212:215],v[108:111]\n"
"v_add_f32 v55,v55,v127\n"
CV "v87,v126,v127\n"
MF "v[104:107],v[28:31],v[208:211],v[104:107]\n"
"v_exp_f32 v128,v128\n"
MF "v[104:107],v[24:27],v[188:191],v[104:107]\n"
"v_exp_f32 v129,v129\n"
MF "v[100:103],v[24:27],v[184:187],v[100:103]\n"
"v_exp_f32 v130,v130\n"
MF "v[100:103],v[28:31],v[204:207],v[100:103]\n"
"v_exp_f32 v131,v131\n"
MF "v[96:99],v[28:31],v[200:203],v[96:99]\n"
"v_add_f32 v56,v56,v128\n"
"v_add_f32 v56,v56,v129\n"
MF "v[96:99],v[24:27],v[180:183],v[96:99]\n"
CV "v90,v128,v129\n"
"v_add_f32 v56,v56,v130\n"
MF "v[92:95],v[24:27],v[176:179],v[92:95]\n"
"v_add_f32 v56,v56,v131\n"
CV "v91,v130,v131\n"
MF "v[92:95],v[28:31],v[196:199],v[92:95]\n"
"s_waitcnt lgkmcnt(0)\n"
"ds_read_b128 v[24:27],v0 offset:55296\n"
"ds_read_b128 v[28:31],v1 offset:55296\n"
MF "v[92:95],v[16:19],v[216:219],v[92:95]\n"
MF "v[92:95],v[20:23],v[236:239],v[92:95]\n"
MF "v[96:99],v[20:23],v[240:243],v[96:99]\n"
MF "v[96:99],v[16:19],v[220:223],v[96:99]\n"
MF "v[100:103],v[16:19],v[224:227],v[100:103]\n"
MF "v[100:103],v[20:23],v[244:247],v[100:103]\n"
MF "v[104:107],v[20:23],v[248:251],v[104:107]\n"
MF "v[104:107],v[16:19],v[228:231],v[104:107]\n"
"v_max3_f32 v10,v52,v53,v54\n"
"v_max3_f32 v10,v10,v55,v56\n"
MF "v[108:111],v[16:19],v[232:235],v[108:111]\n"
"v_cmp_le_f32 vcc,0x47800000,v10\n"
MF "v[108:111],v[20:23],v[252:255],v[108:111]\n"
"s_waitcnt lgkmcnt(0)\n"
"s_cbranch_vccnz .Lstub_27\n"
".Lback_27:\n"
MF "v[112:115],v[24:27],v[136:139],v[32:35]\n"
"s_add_u32 s74,s74,12288\n"
"ds_read_b128 v[16:19],v0 offset:56320\n"
"ds_read_b128 v[20:23],v1 offset:56320\n"
MF "v[112:115],v[28:31],v[156:159],v[112:115]\n"
"s_cmp_eq_u32 s74,s20\n"
"s_cselect_b32 s74,s70,s74\n"
MF "v[116:119],v[28:31],v[160:163],v[36:39]\n"
"v_exp_f32 v92,v92\n"
MF "v[116:119],v[24:27],v[140:143],v[116:119]\n"
"v_exp_f32 v93,v93\n"
MF "v[120:123],v[24:27],v[144:147],v[40:43]\n"
"v_exp_f32 v94,v94\n"
MF "v[120:123],v[28:31],v[164:167],v[120:123]\n"
"v_exp_f32 v95,v95\n"
MF "v[124:127],v[28:31],v[168:171],v[44:47]\n"
"v_add_f32 v57,v52,v92\n"
"v_add_f32 v57,v57,v93\n"
MF "v[124:127],v[24:27],v[148:151],v[124:127]\n"
CV "v92,v92,v93\n"
"v_add_f32 v57,v57,v94\n"
MF "v[128:131],v[24:27],v[152:155],v[48:51]\n"
"v_add_f32 v57,v57,v95\n"
CV "v93,v94,v95\n"
MF "v[128:131],v[28:31],v[172:175],v[128:131]\n"
"s_waitcnt lgkmcnt(0)\n"
"ds_read_b128 v[24:27],v0 offset:57344\n"
"ds_read_b128 v[28:31],v1 offset:57344\n"
MF "v[128:131],v[16:19],v[192:195],v[128:131]\n"
"v_exp_f32 v96,v96\n"
MF "v[128:131],v[20:23],v[212:215],v[128:131]\n"
"v_exp_f32 v97,v97\n"
MF "v[124:127],v[20:23],v[208:211],v[124:127]\n"
"v_exp_f32 v98,v98\n"
MF "v[124:127],v[16:19],v[188:191],v[124:127]\n"
"v_exp_f32 v99,v99\n"
MF "v[120:123],v[16:19],v[184:187],v[120:123]\n"
"v_add_f32 v58,v53,v96\n"
"v_add_f32 v58,v58,v97\n"
MF "v[120:123],v[20:23],v[204:207],v[120:123]\n"
CV "v96,v96,v97\n"
"v_add_f32 v58,v58,v98\n"
MF "v[116:119],v[20:23],v[200:203],v[116:119]\n"
"s_waitcnt vmcnt(6) lgkmcnt(0)\n"
"v_add_f32 v58,v58,v99\n"
CV "v97,v98,v99\n"
MF "v[116:119],v[16:19],v[180:183],v[116:119]\n"
"s_barrier\n"
MF "v[112:115],v[16:19],v[176:179],v[112:115]\n"
"v_exp_f32 v100,v100\n"
MF "v[112:115],v[20:23],v[196:199],v[112:115]\n"
"s_nop 0\n"
TR "v[16:17],v2\n"
TR "v[18:19],v2 offset:6144\n"
MF "v[112:115],v[24:27],v[216:219],v[112:115]\n"
"s_add_u32 s73,s73,12288\n"
TR "v[20:21],v3\n"
TR "v[22:23],v3 offset:6144\n"
MF "v[112:115],v[28:31],v[236:239],v[112:115]\n"
"s_cmp_eq_u32 s73,s19\n"
"s_cselect_b32 s73,s18,s73\n"
"s_add_u32 m0,s65,49152\n"
MF "v[116:119],v[28:31],v[240:243],v[116:119]\n"
LD "v8,s[36:39],s73 offen nt lds\n"
MF "v[116:119],v[24:27],v[220:223],v[116:119]\n"
"s_add_u32 m0,s65,50048\n"
"v_exp_f32 v101,v101\n"
MF "v[120:123],v[24:27],v[224:227],v[120:123]\n"
LD "v8,s[36:39],s73 offen offset:128 nt lds\n"
MF "v[120:123],v[28:31],v[244:247],v[120:123]\n"
"v_exp_f32 v102,v102\n"
MF "v[124:127],v[28:31],v[248:251],v[124:127]\n"
"s_add_u32 m0,s65,50944\n"
"v_exp_f32 v103,v103\n"
MF "v[124:127],v[24:27],v[228:231],v[124:127]\n"
LD "v8,s[36:39],s73 offen offset:256 nt lds\n"
MF "v[128:131],v[24:27],v[232:235],v[128:131]\n"
"v_add_f32 v59,v54,v100\n"
"v_add_f32 v59,v59,v101\n"
MF "v[128:131],v[28:31],v[252:255],v[128:131]\n"
"s_waitcnt lgkmcnt(0)\n"
TR "v[24:25],v4\n"
TR "v[26:27],v4 offset:6144\n"
MF "a[0:3],v[16:19],v[72:75],a[0:3]\n"
"s_add_u32 m0,s65,49152\n"
TR "v[28:29],v5\n"
TR "v[30:31],v5 offset:6144\n"
MF "a[20:23],v[20:23],v[72:75],a[20:23]\n"
LD "v8,s[36:39],s73 offen offset:3072 nt lds\n"
MF "a[24:27],v[20:23],v[76:79],a[24:27]\n"
CV "v100,v100,v101\n"
"v_add_f32 v59,v59,v102\n"
MF "a[4:7],v[16:19],v[76:79],a[4:7]\n"
"s_add_u32 m0,s65,50048\n"
"v_add_f32 v59,v59,v103\n"
CV "v101,v102,v103\n"
MF "a[8:11],v[16:19],v[80:83],a[8:11]\n"
LD "v8,s[36:39],s73 offen offset:3200 nt lds\n"
MF "a[28:31],v[20:23],v[80:83],a[28:31]\n"
"v_exp_f32 v104,v104\n"
MF "a[32:35],v[20:23],v[84:87],a[32:35]\n"
"s_add_u32 m0,s65,50944\n"
"v_exp_f32 v105,v105\n"
MF "a[12:15],v[16:19],v[84:87],a[12:15]\n"
LD "v8,s[36:39],s73 offen offset:3328 nt lds\n"
MF "a[16:19],v[16:19],v[88:91],a[16:19]\n"
"v_exp_f32 v106,v106\n"
MF "a[36:39],v[20:23],v[88:91],a[36:39]\n"
"s_waitcnt lgkmcnt(0)\n"
TR "v[16:17],v2 offset:1024\n"
TR "v[18:19],v2 offset:7168\n"
MF "a[56:59],v[24:27],v[88:91],a[56:59]\n"
"s_nop 0\n"
TR "v[20:21],v3 offset:1024\n"
TR "v[22:23],v3 offset:7168\n"
MF "a[76:79],v[28:31],v[88:91],a[76:79]\n"
"v_exp_f32 v107,v107\n"
MF "a[72:75],v[28:31],v[84:87],a[72:75]\n"
"v_add_f32 v60,v55,v104\n"
"v_add_f32 v60,v60,v105\n"
MF "a[52:55],v[24:27],v[84:87],a[52:55]\n"
CV "v104,v104,v105\n"
"v_add_f32 v60,v60,v106\n"
MF "a[48:51],v[24:27],v[80:83],a[48:51]\n"
"v_add_f32 v60,v60,v107\n"
CV "v105,v106,v107\n"
MF "a[68:71],v[28:31],v[80:83],a[68:71]\n"
"v_exp_f32 v108,v108\n"
MF "a[64:67],v[28:31],v[76:79],a[64:67]\n"
"v_exp_f32 v109,v109\n"
MF "a[44:47],v[24:27],v[76:79],a[44:47]\n"
"v_exp_f32 v110,v110\n"
MF "a[40:43],v[24:27],v[72:75],a[40:43]\n"
"v_exp_f32 v111,v111\n"
MF "a[60:63],v[28:31],v[72:75],a[60:63]\n"
"s_waitcnt lgkmcnt(0)\n"
TR "v[24:25],v4 offset:1024\n"
TR "v[26:27],v4 offset:7168\n"
MF "a[80:83],v[16:19],v[72:75],a[80:83]\n"
"s_nop 0\n"
TR "v[28:29],v5 offset:1024\n"
TR "v[30:31],v5 offset:7168\n"
MF "a[100:103],v[20:23],v[72:75],a[100:103]\n"
"v_add_f32 v61,v56,v108\n"
"v_add_f32 v61,v61,v109\n"
MF "a[104:107],v[20:23],v[76:79],a[104:107]\n"
CV "v108,v108,v109\n"
"v_add_f32 v61,v61,v110\n"
MF "a[84:87],v[16:19],v[76:79],a[84:87]\n"
"v_add_f32 v61,v61,v111\n"
CV "v109,v110,v111\n"
MF "a[88:91],v[16:19],v[80:83],a[88:91]\n"
"v_exp_f32 v112,v112\n"
MF "a[108:111],v[20:23],v[80:83],a[108:111]\n"
"v_exp_f32 v113,v113\n"
MF "a[112:115],v[20:23],v[84:87],a[112:115]\n"
"v_exp_f32 v114,v114\n"
MF "a[92:95],v[16:19],v[84:87],a[92:95]\n"
"v_exp_f32 v115,v115\n"
MF "a[96:99],v[16:19],v[88:91],a[96:99]\n"
"v_add_f32 v57,v57,v112\n"
"v_add_f32 v57,v57,v113\n"
MF "a[116:119],v[20:23],v[88:91],a[116:119]\n"
"s_waitcnt lgkmcnt(0)\n"
TR "v[16:17],v2 offset:2048\n"
TR "v[18:19],v2 offset:8192\n"
MF "a[136:139],v[24:27],v[88:91],a[136:139]\n"
"s_nop 0\n"
TR "v[20:21],v3 offset:2048\n"
TR "v[22:23],v3 offset:8192\n"
MF "a[156:159],v[28:31],v[88:91],a[156:159]\n"
CV "v94,v112,v113\n"
"v_add_f32 v57,v57,v114\n"
MF "a[152:155],v[28:31],v[84:87],a[152:155]\n"
"v_add_f32 v57,v57,v115\n"
CV "v95,v114,v115\n"
MF "a[132:135],v[24:27],v[84:87],a[132:135]\n"
"v_exp_f32 v116,v116\n"
MF "a[128:131],v[24:27],v[80:83],a[128:131]\n"
"v_exp_f32 v117,v117\n"
MF "a[148:151],v[28:31],v[80:83],a[148:151]\n"
"v_exp_f32 v118,v118\n"
MF "a[144:147],v[28:31],v[76:79],a[144:147]\n"
"v_exp_f32 v119,v119\n"
MF "a[124:127],v[24:27],v[76:79],a[124:127]\n"
"v_add_f32 v58,v58,v116\n"
"v_add_f32 v58,v58,v117\n"
MF "a[120:123],v[24:27],v[72:75],a[120:123]\n"
CV "v98,v116,v117\n"
"v_add_f32 v58,v58,v118\n"
MF "a[140:143],v[28:31],v[72:75],a[140:143]\n"
"s_waitcnt lgkmcnt(0)\n"
TR "v[24:25],v4 offset:2048\n"
TR "v[26:27],v4 offset:8192\n"
MF "a[160:163],v[16:19],v[72:75],a[160:163]\n"
"s_nop 0\n"
TR "v[28:29],v5 offset:2048\n"
TR "v[30:31],v5 offset:8192\n"
MF "a[180:183],v[20:23],v[72:75],a[180:183]\n"
"v_add_f32 v58,v58,v119\n"
CV "v99,v118,v119\n"
MF "a[184:187],v[20:23],v[76:79],a[184:187]\n"
"v_exp_f32 v120,v120\n"
MF "a[164:167],v[16:19],v[76:79],a[164:167]\n"
"v_exp_f32 v121,v121\n"
MF "a[168:171],v[16:19],v[80:83],a[168:171]\n"
"v_exp_f32 v122,v122\n"
MF "a[188:191],v[20:23],v[80:83],a[188:191]\n"
"v_exp_f32 v123,v123\n"
MF "a[192:195],v[20:23],v[84:87],a[192:195]\n"
"v_add_f32 v59,v59,v120\n"
"v_add_f32 v59,v59,v121\n"
MF "a[172:175],v[16:19],v[84:87],a[172:175]\n"
"s_waitcnt lgkmcnt(0)\n"
CV "v102,v120,v121\n"
"v_add_f32 v59,v59,v122\n"
MF "a[176:179],v[16:19],v[88:91],a[176:179]\n"
"s_barrier\n"
MF "a[196:199],v[20:23],v[88:91],a[196:199]\n"
"s_add_u32 s60,s1,0\n"
"s_add_u32 m0,s5,0\n"
"v_add_f32 v59,v59,v123\n"
MF "a[216:219],v[24:27],v[88:91],a[216:219]\n"
".Lpg23h:\n"
"s_cbranch_execz .Lpg27h\n"
LD "v7,s[48:51],s60 offen lds\n"
MF "a[236:239],v[28:31],v[88:91],a[236:239]\n"
"s_add_u32 s60,s1,10240\n"
"s_add_u32 m0,s5,1024\n"
CV "v103,v122,v123\n"
MF "a[232:235],v[28:31],v[84:87],a[232:235]\n"
LD "v7,s[48:51],s60 offen lds\n"
MF "a[212:215],v[24:27],v[84:87],a[212:215]\n"
"s_add_u32 s60,s1,20480\n"
"s_add_u32 m0,s5,2048\n"
MF "a[208:211],v[24:27],v[80:83],a[208:211]\n"
LD "v7,s[48:51],s60 offen lds\n"
MF "a[228:231],v[28:31],v[80:83],a[228:231]\n"
"v_exp_f32 v124,v124\n"
MF "a[224:227],v[28:31],v[76:79],a[224:227]\n"
"v_exp_f32 v125,v125\n"
MF "a[204:207],v[24:27],v[76:79],a[204:207]\n"
"v_exp_f32 v126,v126\n"
MF "a[200:203],v[24:27],v[72:75],a[200:203]\n"
"v_exp_f32 v127,v127\n"
MF "a[220:223],v[28:31],v[72:75],a[220:223]\n"
"v_add_f32 v60,v60,v124\n"
"v_add_f32 v60,v60,v125\n"
CV "v106,v124,v125\n"
"v_add_f32 v60,v60,v126\n"
"v_add_f32 v60,v60,v127\n"
CV "v107,v126,v127\n"
"v_exp_f32 v128,v128\n"
"v_exp_f32 v129,v129\n"
"v_exp_f32 v130,v130\n"
"v_exp_f32 v131,v131\n"
"v_add_f32 v61,v61,v128\n"
"v_add_f32 v61,v61,v129\n"
CV "v110,v128,v129\n"
"v_add_f32 v61,v61,v130\n"
"v_add_f32 v61,v61,v131\n"
CV "v111,v130,v131\n"
"v_max3_f32 v10,v57,v58,v59\n"
"v_max3_f32 v10,v10,v60,v61\n"
"v_cmp_le_f32 vcc,0x47800000,v10\n"
"s_cbranch_vccnz .Lstub_28\n"
".Lback_28:\n"
"s_waitcnt vmcnt(3) lgkmcnt(0)\n"
"s_barrier\n"
"v_add_u32 v236,s9,v9\n"
"v_add_u32 v237,65536,v236\n"
"v_mov_b32 v10,1.0\n"
TR "v[160:161],v4 offset:50176\n"
TR "v[162:163],v4 offset:56320\n"
"v_mfma_f32_16x16x4_f32 v[16:19],v10,v57,0\n"
TR "v[164:165],v5 offset:50176\n"
TR "v[166:167],v5 offset:56320\n"
TR "v[168:169],v2 offset:51200\n"
TR "v[170:171],v2 offset:57344\n"
TR "v[172:173],v3 offset:51200\n"
TR "v[174:175],v3 offset:57344\n"
"v_mfma_f32_16x16x4_f32 v[20:23],v10,v58,0\n"
TR "v[176:177],v4 offset:51200\n"
TR "v[178:179],v4 offset:57344\n"
TR "v[180:181],v5 offset:51200\n"
TR "v[182:183],v5 offset:57344\n"
TR "v[136:137],v2 offset:49152\n"
TR "v[138:139],v2 offset:55296\n"
"v_mfma_f32_16x16x4_f32 v[24:27],v10,v59,0\n"
TR "v[140:141],v3 offset:49152\n"
TR "v[142:143],v3 offset:55296\n"
TR "v[144:145],v4 offset:49152\n"
TR "v[146:147],v4 offset:55296\n"
TR "v[148:149],v5 offset:49152\n"
TR "v[150:151],v5 offset:55296\n"
"v_mfma_f32_16x16x4_f32 v[28:31],v10,v60,0\n"
TR "v[152:153],v2 offset:50176\n"
TR "v[154:155],v2 offset:56320\n"
TR "v[156:157],v3 offset:50176\n"
TR "v[158:159],v3 offset:56320\n"
"v_mfma_f32_16x16x4_f32 v[32:35],v10,v61,0\n"
"s_add_u32 m0,s5,0\n"
"v_add_u32 v195,27648,v7\n"
LD "v195,s[48:51],s1 offen offset:3072 lds\n"
"s_add_u32 m0,s5,4096\n"
"v_add_u32 v196,40960,v7\n"
LD "v196,s[48:51],s1 offen lds\n"
"s_waitcnt lgkmcnt(15)\n"
MF "a[120:123],v[160:163],v[92:95],a[120:123]\n"
MF "a[140:143],v[164:167],v[92:95],a[140:143]\n"
"v_add_u32 v197,50176,v7\n"
LD "v197,s[48:51],s1 offen offset:1024 lds\n"
"v_rcp_f32 v208,v16\n"
"v_rcp_f32 v209,v20\n"
"v_rcp_f32 v210,v24\n"
"v_rcp_f32 v211,v28\n"
"v_rcp_f32 v212,v32\n"
MF "a[124:127],v[160:163],v[96:99],a[124:127]\n"
"v_mul_f32_e64 v214,-s13,v208\n"
"v_mul_f32_e64 v215,-s13,v209\n"
"v_mul_f32_e64 v216,-s13,v210\n"
"v_mul_f32_e64 v217,-s13,v211\n"
"v_mul_f32_e64 v218,-s13,v212\n"
AR "v72,a120\n"
AR "v73,a121\n"
AR "v74,a122\n"
MF "a[144:147],v[164:167],v[96:99],a[144:147]\n"
AR "v75,a123\n"
AR "v76,a140\n"
AR "v77,a141\n"
AR "v78,a142\n"
AR "v79,a143\n"
"v_pk_fma_f32 v[72:73],v[72:73],v[208:209],v[214:215] op_sel:[0,0,0] op_sel_hi:[1,0,0]\n"
"v_pk_fma_f32 v[74:75],v[74:75],v[208:209],v[214:215] op_sel:[0,0,0] op_sel_hi:[1,0,0]\n"
"v_pk_fma_f32 v[76:77],v[76:77],v[208:209],v[214:215] op_sel:[0,0,0] op_sel_hi:[1,0,0]\n"
"v_pk_fma_f32 v[78:79],v[78:79],v[208:209],v[214:215] op_sel:[0,0,0] op_sel_hi:[1,0,0]\n"
CV "v112,v72,v73\n"
CV "v113,v74,v75\n"
CV "v114,v76,v77\n"
CV "v115,v78,v79\n"
MF "a[128:131],v[160:163],v[100:103],a[128:131]\n"
"ds_read_b128 a[140:143],v237 offset:20480\n"
AW "a120,v112\n"
AW "a121,v113\n"
AW "a122,v114\n"
AW "a123,v115\n"
"s_waitcnt lgkmcnt(7)\n"
MF "a[44:47],v[144:147],v[96:99],a[44:47]\n"
AR "v80,a124\n"
AR "v81,a125\n"
AR "v82,a126\n"
MF "a[148:151],v[164:167],v[100:103],a[148:151]\n"
AR "v83,a127\n"
AR "v84,a144\n"
AR "v85,a145\n"
"s_nop 0\n"
AR "v86,a146\n"
AR "v87,a147\n"
"v_pk_fma_f32 v[80:81],v[80:81],v[208:209],v[214:215] op_sel:[0,1,1] op_sel_hi:[1,1,1]\n"
"v_pk_fma_f32 v[82:83],v[82:83],v[208:209],v[214:215] op_sel:[0,1,1] op_sel_hi:[1,1,1]\n"
"v_pk_fma_f32 v[84:85],v[84:85],v[208:209],v[214:215] op_sel:[0,1,1] op_sel_hi:[1,1,1]\n"
"v_pk_fma_f32 v[86:87],v[86:87],v[208:209],v[214:215] op_sel:[0,1,1] op_sel_hi:[1,1,1]\n"
CV "v116,v80,v81\n"
CV "v117,v82,v83\n"
CV "v118,v84,v85\n"
CV "v119,v86,v87\n"
MF "a[132:135],v[160:163],v[104:107],a[132:135]\n"
"ds_read_b128 a[144:147],v236\n"
AW "a124,v116\n"
AW "a125,v117\n"
AW "a126,v118\n"
AW "a127,v119\n"
"s_waitcnt lgkmcnt(6)\n"
MF "a[64:67],v[148:151],v[96:99],a[64:67]\n"
AR "v72,a128\n"
AR "v73,a129\n"
AR "v74,a130\n"
MF "a[152:155],v[164:167],v[104:107],a[152:155]\n"
AR "v75,a131\n"
AR "v76,a148\n"
AR "v77,a149\n"
"s_nop 0\n"
AR "v78,a150\n"
AR "v79,a151\n"
"v_pk_fma_f32 v[72:73],v[72:73],v[210:211],v[216:217] op_sel:[0,0,0] op_sel_hi:[1,0,0]\n"
"v_pk_fma_f32 v[74:75],v[74:75],v[210:211],v[216:217] op_sel:[0,0,0] op_sel_hi:[1,0,0]\n"
"v_pk_fma_f32 v[76:77],v[76:77],v[210:211],v[216:217] op_sel:[0,0,0] op_sel_hi:[1,0,0]\n"
"v_pk_fma_f32 v[78:79],v[78:79],v[210:211],v[216:217] op_sel:[0,0,0] op_sel_hi:[1,0,0]\n"
CV "v112,v72,v73\n"
CV "v113,v74,v75\n"
CV "v114,v76,v77\n"
CV "v115,v78,v79\n"
MF "a[136:139],v[160:163],v[108:111],a[136:139]\n"
"ds_read_b128 a[148:151],v236 offset:4096\n"
AW "a128,v112\n"
AW "a129,v113\n"
AW "a130,v114\n"
AW "a131,v115\n"
"s_waitcnt lgkmcnt(5)\n"
MF "a[84:87],v[152:155],v[96:99],a[84:87]\n"
AR "v80,a132\n"
AR "v81,a133\n"
AR "v82,a134\n"
MF "a[156:159],v[164:167],v[108:111],a[156:159]\n"
AR "v83,a135\n"
AR "v84,a152\n"
AR "v85,a153\n"
"s_nop 0\n"
AR "v86,a154\n"
AR "v87,a155\n"
"v_pk_fma_f32 v[80:81],v[80:81],v[210:211],v[216:217] op_sel:[0,1,1] op_sel_hi:[1,1,1]\n"
"v_pk_fma_f32 v[82:83],v[82:83],v[210:211],v[216:217] op_sel:[0,1,1] op_sel_hi:[1,1,1]\n"
"v_pk_fma_f32 v[84:85],v[84:85],v[210:211],v[216:217] op_sel:[0,1,1] op_sel_hi:[1,1,1]\n"
"v_pk_fma_f32 v[86:87],v[86:87],v[210:211],v[216:217] op_sel:[0,1,1] op_sel_hi:[1,1,1]\n"
CV "v116,v80,v81\n"
CV "v117,v82,v83\n"
CV "v118,v84,v85\n"
CV "v119,v86,v87\n"
MF "a[160:163],v[168:171],v[92:95],a[160:163]\n"
"ds_read_b128 a[152:155],v236 offset:8192\n"
AW "a132,v116\n"
AW "a133,v117\n"
AW "a134,v118\n"
AW "a135,v119\n"
"s_waitcnt lgkmcnt(4)\n"
MF "a[104:107],v[156:159],v[96:99],a[104:107]\n"
AR "v72,a136\n"
AR "v73,a137\n"
AR "v74,a138\n"
MF "a[180:183],v[172:175],v[92:95],a[180:183]\n"
AR "v75,a139\n"
AR "v76,a156\n"
AR "v77,a157\n"
"s_nop 0\n"
AR "v78,a158\n"
AR "v79,a159\n"
"v_pk_fma_f32 v[72:73],v[72:73],v[212:213],v[218:219] op_sel:[0,0,0] op_sel_hi:[1,0,0]\n"
"v_pk_fma_f32 v[74:75],v[74:75],v[212:213],v[218:219] op_sel:[0,0,0] op_sel_hi:[1,0,0]\n"
"v_pk_fma_f32 v[76:77],v[76:77],v[212:213],v[218:219] op_sel:[0,0,0] op_sel_hi:[1,0,0]\n"
"v_pk_fma_f32 v[78:79],v[78:79],v[212:213],v[218:219] op_sel:[0,0,0] op_sel_hi:[1,0,0]\n"
CV "v112,v72,v73\n"
CV "v113,v74,v75\n"
CV "v114,v76,v77\n"
CV "v115,v78,v79\n"
MF "a[164:167],v[168:171],v[96:99],a[164:167]\n"
"ds_read_b128 a[156:159],v236 offset:12288\n"
AW "a136,v112\n"
AW "a137,v113\n"
AW "a138,v114\n"
AW "a139,v115\n"
"v_max3_f32 v10,|v72|,|v73|,|v74|\n"
"v_max3_f32 v10,v10,|v75|,|v76|\n"
"v_max3_f32 v10,v10,|v77|,|v78|\n"
"v_max3_f32 v10,v10,|v78|,|v79|\n"
"v_mul_f32 v10,s10,v10\n"
"v_lshrrev_b32 v11,2,v9\n"
"v_readlane_b32 s12,v10,3\n"
"v_readlane_b32 s6,v10,22\n"
"v_readlane_b32 s7,v10,41\n"
"v_readlane_b32 s8,v10,60\n"
"v_add_u32 v11,s14,v11\n"
"s_min_u32 s12,s12,s6\n"
"s_min_u32 s7,s7,s8\n"
"s_min_u32 s12,s12,s7\n"
"s_and_b32 s12,s12,0x7f800000\n"
"s_sub_u32 s12,s12,0x14000000\n"
"s_cmp_lt_u32 s12,0x50000000\n"
"s_cselect_b32 s12,s12,0xe7400000\n"
"s_add_u32 s12,s12,0x18c00000\n"
"v_mov_b32_e64 v10,s12\n"
"v_or_b32 v12,0x80000000,v10\n"
"ds_write2st64_b32 v11,v10,v12 offset1:1\n"
MF "a[8:11],v[136:139],v[100:103],a[8:11]\n"
AR "v80,a160\n"
AR "v81,a161\n"
AR "v82,a162\n"
MF "a[184:187],v[172:175],v[96:99],a[184:187]\n"
AR "v83,a163\n"
AR "v84,a180\n"
AR "v85,a181\n"
AR "v86,a182\n"
AR "v87,a183\n"
"v_pk_fma_f32 v[80:81],v[80:81],v[208:209],v[214:215] op_sel:[0,0,0] op_sel_hi:[1,0,0]\n"
"v_pk_fma_f32 v[82:83],v[82:83],v[208:209],v[214:215] op_sel:[0,0,0] op_sel_hi:[1,0,0]\n"
"v_pk_fma_f32 v[84:85],v[84:85],v[208:209],v[214:215] op_sel:[0,0,0] op_sel_hi:[1,0,0]\n"
"v_pk_fma_f32 v[86:87],v[86:87],v[208:209],v[214:215] op_sel:[0,0,0] op_sel_hi:[1,0,0]\n"
CV "v116,v80,v81\n"
CV "v117,v82,v83\n"
CV "v118,v84,v85\n"
CV "v119,v86,v87\n"
MF "a[168:171],v[168:171],v[100:103],a[168:171]\n"
"ds_read_b128 a[180:183],v236 offset:16384\n"
AW "a160,v116\n"
AW "a161,v117\n"
AW "a162,v118\n"
AW "a163,v119\n"
MF "a[28:31],v[140:143],v[100:103],a[28:31]\n"
AR "v72,a164\n"
AR "v73,a165\n"
AR "v74,a166\n"
MF "a[188:191],v[172:175],v[100:103],a[188:191]\n"
AR "v75,a167\n"
AR "v76,a184\n"
AR "v77,a185\n"
AR "v78,a186\n"
AR "v79,a187\n"
"v_pk_fma_f32 v[72:73],v[72:73],v[208:209],v[214:215] op_sel:[0,1,1] op_sel_hi:[1,1,1]\n"
"v_pk_fma_f32 v[74:75],v[74:75],v[208:209],v[214:215] op_sel:[0,1,1] op_sel_hi:[1,1,1]\n"
"v_pk_fma_f32 v[76:77],v[76:77],v[208:209],v[214:215] op_sel:[0,1,1] op_sel_hi:[1,1,1]\n"
"v_pk_fma_f32 v[78:79],v[78:79],v[208:209],v[214:215] op_sel:[0,1,1] op_sel_hi:[1,1,1]\n"
CV "v112,v72,v73\n"
CV "v113,v74,v75\n"
CV "v114,v76,v77\n"
CV "v115,v78,v79\n"
MF "a[172:175],v[168:171],v[104:107],a[172:175]\n"
"ds_read_b128 a[184:187],v236 offset:20480\n"
AW "a164,v112\n"
AW "a165,v113\n"
AW "a166,v114\n"
AW "a167,v115\n"
MF "a[48:51],v[144:147],v[100:103],a[48:51]\n"
"s_waitcnt lgkmcnt(0)\n"
"s_barrier\n"
"s_add_u32 m0,s5,49152\n"
"v_add_u32_e64 v192,0,v7\n"
LD "v192,s[48:51],s2 offen lds\n"
AR "v80,a168\n"
AR "v81,a169\n"
AR "v82,a170\n"
MF "a[192:195],v[172:175],v[104:107],a[192:195]\n"
AR "v83,a171\n"
AR "v84,a188\n"
AR "v85,a189\n"
AR "v86,a190\n"
AR "v87,a191\n"
"v_pk_fma_f32 v[80:81],v[80:81],v[210:211],v[216:217] op_sel:[0,0,0] op_sel_hi:[1,0,0]\n"
"v_pk_fma_f32 v[82:83],v[82:83],v[210:211],v[216:217] op_sel:[0,0,0] op_sel_hi:[1,0,0]\n"
"v_pk_fma_f32 v[84:85],v[84:85],v[210:211],v[216:217] op_sel:[0,0,0] op_sel_hi:[1,0,0]\n"
"v_pk_fma_f32 v[86:87],v[86:87],v[210:211],v[216:217] op_sel:[0,0,0] op_sel_hi:[1,0,0]\n"
"v_add_u32 v193,9216,v7\n"
LD "v193,s[48:51],s2 offen offset:1024 lds\n"
"v_add_u32 v194,18432,v7\n"
LD "v194,s[48:51],s2 offen offset:2048 lds\n"
LD "v195,s[48:51],s2 offen offset:3072 lds\n"
"s_add_u32 m0,s5,53248\n"
"s_nop 0\n"
LD "v196,s[48:51],s2 offen lds\n"
LD "v197,s[48:51],s2 offen offset:1024 lds\n"
CV "v116,v80,v81\n"
CV "v117,v82,v83\n"
CV "v118,v84,v85\n"
CV "v119,v86,v87\n"
MF "a[176:179],v[168:171],v[108:111],a[176:179]\n"
"v_accvgpr_mov_b32 a188,a240\n"
"v_accvgpr_mov_b32 a189,a241\n"
"v_accvgpr_mov_b32 a190,a242\n"
"v_accvgpr_mov_b32 a191,a243\n"
AW "a168,v116\n"
AW "a169,v117\n"
AW "a170,v118\n"
AW "a171,v119\n"
MF "a[68:71],v[148:151],v[100:103],a[68:71]\n"
"s_add_u32 m0,s5,73728\n"
"s_nop 0\n"
LD "v192,s[48:51],s3 offen lds\n"
AR "v72,a172\n"
AR "v73,a173\n"
AR "v74,a174\n"
MF "a[196:199],v[172:175],v[108:111],a[196:199]\n"
AR "v75,a175\n"
AR "v76,a192\n"
AR "v77,a193\n"
AR "v78,a194\n"
AR "v79,a195\n"
"v_pk_fma_f32 v[72:73],v[72:73],v[210:211],v[216:217] op_sel:[0,1,1] op_sel_hi:[1,1,1]\n"
"v_pk_fma_f32 v[74:75],v[74:75],v[210:211],v[216:217] op_sel:[0,1,1] op_sel_hi:[1,1,1]\n"
"v_pk_fma_f32 v[76:77],v[76:77],v[210:211],v[216:217] op_sel:[0,1,1] op_sel_hi:[1,1,1]\n"
"v_pk_fma_f32 v[78:79],v[78:79],v[210:211],v[216:217] op_sel:[0,1,1] op_sel_hi:[1,1,1]\n"
CV "v112,v72,v73\n"
CV "v113,v74,v75\n"
CV "v114,v76,v77\n"
CV "v115,v78,v79\n"
MF "a[200:203],v[176:179],v[92:95],a[200:203]\n"
"v_accvgpr_mov_b32 a192,a244\n"
"v_accvgpr_mov_b32 a193,a245\n"
"v_accvgpr_mov_b32 a194,a246\n"
"v_accvgpr_mov_b32 a195,a247\n"
AW "a172,v112\n"
AW "a173,v113\n"
AW "a174,v114\n"
AW "a175,v115\n"
MF "a[88:91],v[152:155],v[100:103],a[88:91]\n"
LD "v193,s[48:51],s3 offen offset:1024 lds\n"
AR "v80,a176\n"
AR "v81,a177\n"
AR "v82,a178\n"
MF "a[220:223],v[180:183],v[92:95],a[220:223]\n"
AR "v83,a179\n"
AR "v84,a196\n"
AR "v85,a197\n"
AR "v86,a198\n"
AR "v87,a199\n"
"v_pk_fma_f32 v[80:81],v[80:81],v[212:213],v[218:219] op_sel:[0,0,0] op_sel_hi:[1,0,0]\n"
"v_pk_fma_f32 v[82:83],v[82:83],v[212:213],v[218:219] op_sel:[0,0,0] op_sel_hi:[1,0,0]\n"
"v_pk_fma_f32 v[84:85],v[84:85],v[212:213],v[218:219] op_sel:[0,0,0] op_sel_hi:[1,0,0]\n"
"v_pk_fma_f32 v[86:87],v[86:87],v[212:213],v[218:219] op_sel:[0,0,0] op_sel_hi:[1,0,0]\n"
CV "v116,v80,v81\n"
CV "v117,v82,v83\n"
CV "v118,v84,v85\n"
CV "v119,v86,v87\n"
MF "a[204:207],v[176:179],v[96:99],a[204:207]\n"
"v_accvgpr_mov_b32 a196,a248\n"
"v_accvgpr_mov_b32 a197,a249\n"
"v_accvgpr_mov_b32 a198,a250\n"
"v_accvgpr_mov_b32 a199,a251\n"
AW "a176,v116\n"
AW "a177,v117\n"
AW "a178,v118\n"
AW "a179,v119\n"
MF "a[108:111],v[156:159],v[100:103],a[108:111]\n"
AR "v72,a200\n"
AR "v73,a201\n"
AR "v74,a202\n"
MF "a[224:227],v[180:183],v[96:99],a[224:227]\n"
AR "v75,a203\n"
AR "v76,a220\n"
AR "v77,a221\n"
AR "v78,a222\n"
AR "v79,a223\n"
"v_pk_fma_f32 v[72:73],v[72:73],v[208:209],v[214:215] op_sel:[0,0,0] op_sel_hi:[1,0,0]\n"
"v_pk_fma_f32 v[74:75],v[74:75],v[208:209],v[214:215] op_sel:[0,0,0] op_sel_hi:[1,0,0]\n"
"v_pk_fma_f32 v[76:77],v[76:77],v[208:209],v[214:215] op_sel:[0,0,0] op_sel_hi:[1,0,0]\n"
"v_pk_fma_f32 v[78:79],v[78:79],v[208:209],v[214:215] op_sel:[0,0,0] op_sel_hi:[1,0,0]\n"
CV "v112,v72,v73\n"
CV "v113,v74,v75\n"
CV "v114,v76,v77\n"
CV "v115,v78,v79\n"
MF "a[208:211],v[176:179],v[100:103],a[208:211]\n"
AW "a220,v62\n"
AW "a221,v63\n"
AW "a222,v64\n"
AW "a223,v65\n"
AW "a200,v112\n"
AW "a201,v113\n"
AW "a202,v114\n"
AW "a203,v115\n"
MF "a[12:15],v[136:139],v[104:107],a[12:15]\n"
LD "v194,s[48:51],s3 offen offset:2048 lds\n"
AR "v80,a204\n"
AR "v81,a205\n"
AR "v82,a206\n"
MF "a[228:231],v[180:183],v[100:103],a[228:231]\n"
AR "v83,a207\n"
AR "v84,a224\n"
AR "v85,a225\n"
AR "v86,a226\n"
AR "v87,a227\n"
"v_pk_fma_f32 v[80:81],v[80:81],v[208:209],v[214:215] op_sel:[0,1,1] op_sel_hi:[1,1,1]\n"
"v_pk_fma_f32 v[82:83],v[82:83],v[208:209],v[214:215] op_sel:[0,1,1] op_sel_hi:[1,1,1]\n"
"v_pk_fma_f32 v[84:85],v[84:85],v[208:209],v[214:215] op_sel:[0,1,1] op_sel_hi:[1,1,1]\n"
"v_pk_fma_f32 v[86:87],v[86:87],v[208:209],v[214:215] op_sel:[0,1,1] op_sel_hi:[1,1,1]\n"
CV "v116,v80,v81\n"
CV "v117,v82,v83\n"
CV "v118,v84,v85\n"
CV "v119,v86,v87\n"
MF "a[212:215],v[176:179],v[104:107],a[212:215]\n"
AW "a224,v66\n"
AW "a225,v67\n"
AW "a226,v68\n"
AW "a227,v69\n"
AW "a204,v116\n"
AW "a205,v117\n"
AW "a206,v118\n"
AW "a207,v119\n"
MF "a[32:35],v[140:143],v[104:107],a[32:35]\n"
LD "v195,s[48:51],s3 offen offset:3072 lds\n"
AR "v72,a208\n"
AR "v73,a209\n"
AR "v74,a210\n"
MF "a[232:235],v[180:183],v[104:107],a[232:235]\n"
AR "v75,a211\n"
AR "v76,a228\n"
AR "v77,a229\n"
AR "v78,a230\n"
AR "v79,a231\n"
"v_pk_fma_f32 v[72:73],v[72:73],v[210:211],v[216:217] op_sel:[0,0,0] op_sel_hi:[1,0,0]\n"
"v_pk_fma_f32 v[74:75],v[74:75],v[210:211],v[216:217] op_sel:[0,0,0] op_sel_hi:[1,0,0]\n"
"v_pk_fma_f32 v[76:77],v[76:77],v[210:211],v[216:217] op_sel:[0,0,0] op_sel_hi:[1,0,0]\n"
"v_pk_fma_f32 v[78:79],v[78:79],v[210:211],v[216:217] op_sel:[0,0,0] op_sel_hi:[1,0,0]\n"
CV "v112,v72,v73\n"
CV "v113,v74,v75\n"
CV "v114,v76,v77\n"
CV "v115,v78,v79\n"
MF "a[216:219],v[176:179],v[108:111],a[216:219]\n"
AW "a228,v70\n"
AW "a229,v71\n"
AW "a230,v132\n"
AW "a231,v133\n"
AW "a208,v112\n"
AW "a209,v113\n"
AW "a210,v114\n"
AW "a211,v115\n"
MF "a[52:55],v[144:147],v[104:107],a[52:55]\n"
"s_add_u32 m0,s5,77824\n"
"s_nop 0\n"
LD "v196,s[48:51],s3 offen lds\n"
AR "v80,a212\n"
AR "v81,a213\n"
AR "v82,a214\n"
MF "a[236:239],v[180:183],v[108:111],a[236:239]\n"
AR "v83,a215\n"
AR "v84,a232\n"
AR "v85,a233\n"
"s_nop 0\n"
AR "v86,a234\n"
AR "v87,a235\n"
"v_pk_fma_f32 v[80:81],v[80:81],v[210:211],v[216:217] op_sel:[0,1,1] op_sel_hi:[1,1,1]\n"
"v_pk_fma_f32 v[82:83],v[82:83],v[210:211],v[216:217] op_sel:[0,1,1] op_sel_hi:[1,1,1]\n"
"v_pk_fma_f32 v[84:85],v[84:85],v[210:211],v[216:217] op_sel:[0,1,1] op_sel_hi:[1,1,1]\n"
"v_pk_fma_f32 v[86:87],v[86:87],v[210:211],v[216:217] op_sel:[0,1,1] op_sel_hi:[1,1,1]\n"
CV "v116,v80,v81\n"
CV "v117,v82,v83\n"
CV "v118,v84,v85\n"
CV "v119,v86,v87\n"
MF "a[0:3],v[136:139],v[92:95],a[0:3]\n"
AW "a232,v134\n"
AW "a233,v135\n"
AW "a234,v6\n"
AW "a235,v13\n"
AW "a212,v116\n"
AW "a213,v117\n"
AW "a214,v118\n"
AW "a215,v119\n"
MF "a[72:75],v[148:151],v[104:107],a[72:75]\n"
LD "v197,s[48:51],s3 offen offset:1024 lds\n"
AR "v72,a216\n"
AR "v73,a217\n"
AR "v74,a218\n"
MF "a[20:23],v[140:143],v[92:95],a[20:23]\n"
AR "v75,a219\n"
AR "v76,a236\n"
AR "v77,a237\n"
AR "v78,a238\n"
AR "v79,a239\n"
"v_pk_fma_f32 v[72:73],v[72:73],v[212:213],v[218:219] op_sel:[0,0,0] op_sel_hi:[1,0,0]\n"
"v_pk_fma_f32 v[74:75],v[74:75],v[212:213],v[218:219] op_sel:[0,0,0] op_sel_hi:[1,0,0]\n"
"v_pk_fma_f32 v[76:77],v[76:77],v[212:213],v[218:219] op_sel:[0,0,0] op_sel_hi:[1,0,0]\n"
"v_pk_fma_f32 v[78:79],v[78:79],v[212:213],v[218:219] op_sel:[0,0,0] op_sel_hi:[1,0,0]\n"
CV "v112,v72,v73\n"
CV "v113,v74,v75\n"
CV "v114,v76,v77\n"
CV "v115,v78,v79\n"
MF "a[40:43],v[144:147],v[92:95],a[40:43]\n"
AW "a236,v14\n"
AW "a237,v15\n"
"v_accvgpr_mov_b32 a238,a254\n"
"v_accvgpr_mov_b32 a239,a255\n"
AW "a216,v112\n"
AW "a217,v113\n"
AW "a218,v114\n"
AW "a219,v115\n"
MF "a[92:95],v[152:155],v[104:107],a[92:95]\n"
AR "v80,a0\n"
AR "v81,a1\n"
AR "v82,a2\n"
MF "a[60:63],v[148:151],v[92:95],a[60:63]\n"
AR "v83,a3\n"
AR "v84,a20\n"
AR "v85,a21\n"
".Lpg24h:\n"
"s_cbranch_execz .Lpg28h\n"
"s_nop 0\n"
AR "v86,a22\n"
AR "v87,a23\n"
"v_pk_fma_f32 v[80:81],v[80:81],v[208:209],v[214:215] op_sel:[0,0,0] op_sel_hi:[1,0,0]\n"
"v_pk_fma_f32 v[82:83],v[82:83],v[208:209],v[214:215] op_sel:[0,0,0] op_sel_hi:[1,0,0]\n"
"v_pk_fma_f32 v[84:85],v[84:85],v[208:209],v[214:215] op_sel:[0,0,0] op_sel_hi:[1,0,0]\n"
"v_pk_fma_f32 v[86:87],v[86:87],v[208:209],v[214:215] op_sel:[0,0,0] op_sel_hi:[1,0,0]\n"
CV "v116,v80,v81\n"
CV "v117,v82,v83\n"
CV "v118,v84,v85\n"
CV "v119,v86,v87\n"
MF "a[80:83],v[152:155],v[92:95],a[80:83]\n"
"ds_read_b128 a[20:23],v236 offset:24576\n"
AW "a0,v116\n"
AW "a1,v117\n"
AW "a2,v118\n"
AW "a3,v119\n"
MF "a[112:115],v[156:159],v[104:107],a[112:115]\n"
AR "v72,a40\n"
AR "v73,a41\n"
AR "v74,a42\n"
MF "a[100:103],v[156:159],v[92:95],a[100:103]\n"
AR "v75,a43\n"
AR "v76,a60\n"
AR "v77,a61\n"
AR "v78,a62\n"
AR "v79,a63\n"
"v_pk_fma_f32 v[72:73],v[72:73],v[208:209],v[214:215] op_sel:[0,0,0] op_sel_hi:[1,0,0]\n"
"v_pk_fma_f32 v[74:75],v[74:75],v[208:209],v[214:215] op_sel:[0,0,0] op_sel_hi:[1,0,0]\n"
"v_pk_fma_f32 v[76:77],v[76:77],v[208:209],v[214:215] op_sel:[0,0,0] op_sel_hi:[1,0,0]\n"
"v_pk_fma_f32 v[78:79],v[78:79],v[208:209],v[214:215] op_sel:[0,0,0] op_sel_hi:[1,0,0]\n"
CV "v112,v72,v73\n"
CV "v113,v74,v75\n"
CV "v114,v76,v77\n"
CV "v115,v78,v79\n"
MF "a[4:7],v[136:139],v[96:99],a[4:7]\n"
"ds_read_b128 a[60:63],v236 offset:45056\n"
AW "a40,v112\n"
AW "a41,v113\n"
AW "a42,v114\n"
AW "a43,v115\n"
MF "a[16:19],v[136:139],v[108:111],a[16:19]\n"
AR "v80,a80\n"
AR "v81,a81\n"
AR "v82,a82\n"
MF "a[24:27],v[140:143],v[96:99],a[24:27]\n"
AR "v83,a83\n"
AR "v84,a100\n"
AR "v85,a101\n"
AR "v86,a102\n"
AR "v87,a103\n"
"v_pk_fma_f32 v[80:81],v[80:81],v[208:209],v[214:215] op_sel:[0,0,0] op_sel_hi:[1,0,0]\n"
"v_pk_fma_f32 v[82:83],v[82:83],v[208:209],v[214:215] op_sel:[0,0,0] op_sel_hi:[1,0,0]\n"
"v_pk_fma_f32 v[84:85],v[84:85],v[208:209],v[214:215] op_sel:[0,0,0] op_sel_hi:[1,0,0]\n"
"v_pk_fma_f32 v[86:87],v[86:87],v[208:209],v[214:215] op_sel:[0,0,0] op_sel_hi:[1,0,0]\n"
CV "v116,v80,v81\n"
CV "v117,v82,v83\n"
CV "v118,v84,v85\n"
CV "v119,v86,v87\n"
MF "a[36:39],v[140:143],v[108:111],a[36:39]\n"
"ds_read_b128 a[100:103],v237\n"
AW "a80,v116\n"
AW "a81,v117\n"
AW "a82,v118\n"
AW "a83,v119\n"
MF "a[56:59],v[144:147],v[108:111],a[56:59]\n"
AR "v72,a4\n"
AR "v73,a5\n"
AR "v74,a6\n"
MF "a[76:79],v[148:151],v[108:111],a[76:79]\n"
AR "v75,a7\n"
AR "v76,a24\n"
AR "v77,a25\n"
AR "v78,a26\n"
AR "v79,a27\n"
"v_pk_fma_f32 v[72:73],v[72:73],v[208:209],v[214:215] op_sel:[0,1,1] op_sel_hi:[1,1,1]\n"
"v_pk_fma_f32 v[74:75],v[74:75],v[208:209],v[214:215] op_sel:[0,1,1] op_sel_hi:[1,1,1]\n"
"v_pk_fma_f32 v[76:77],v[76:77],v[208:209],v[214:215] op_sel:[0,1,1] op_sel_hi:[1,1,1]\n"
"v_pk_fma_f32 v[78:79],v[78:79],v[208:209],v[214:215] op_sel:[0,1,1] op_sel_hi:[1,1,1]\n"
CV "v112,v72,v73\n"
CV "v113,v74,v75\n"
CV "v114,v76,v77\n"
CV "v115,v78,v79\n"
MF "a[96:99],v[152:155],v[108:111],a[96:99]\n"
"ds_read_b128 a[24:27],v236 offset:28672\n"
AW "a4,v112\n"
AW "a5,v113\n"
AW "a6,v114\n"
AW "a7,v115\n"
MF "a[116:119],v[156:159],v[108:111],a[116:119]\n"
"v_and_b32 v0,512,v9\n"
"v_lshlrev_b32 v0,2,v0\n"
"v_sub_u32 v0,v2,v0\n"
"v_xor_b32 v0,s4,v0\n"
"v_mov_b32 v252,s15\n"
"s_waitcnt vmcnt(12) lgkmcnt(0)\n"
"s_barrier\n"
"ds_read_b128 v[252:255],v252\n"
TR "v[176:177],v0 offset:24576\n"
TR "v[178:179],v0 offset:26624\n"
TR "v[180:181],v0 offset:28672\n"
TR "v[182:183],v0 offset:30720\n"
TR "v[184:185],v0 offset:32768\n"
TR "v[186:187],v0 offset:34816\n"
"v_xor_b32 v1,32,v0\n"
"v_xor_b32 v2,64,v0\n"
"v_xor_b32 v3,96,v0\n"
"v_add_u32 v4,73728,v0\n"
"v_add_u32 v5,73728,v1\n"
"v_add_u32 v6,73728,v2\n"
"v_add_u32 v7,73728,v3\n"
"s_cmp_ge_u32 s56,2\n"
"s_cbranch_scc1 .Lrole1\n"
"s_waitcnt lgkmcnt(4)\n"
MF "v[16:19],a[120:123],v[176:179],v[252:255]\n"
"s_mov_b32 s65,128\n"
TR "v[188:189],v1 offset:24576\n"
AR "v220,a44\n"
"s_waitcnt lgkmcnt(3)\n"
MF "v[16:19],a[160:163],v[180:183],v[16:19]\n"
"s_mov_b32 s9,0x3f80\n"
TR "v[190:191],v1 offset:26624\n"
AR "v221,a45\n"
"s_waitcnt lgkmcnt(2)\n"
MF "v[16:19],a[200:203],v[184:187],v[16:19]\n"
"s_add_u32 s89,s89,s65\n"
AR "v222,a46\n"
AR "v223,a47\n"
MF "v[24:27],a[204:207],v[184:187],v[252:255]\n"
"s_bfe_u32 s58,s22,0x30014\n"
TR "a[240:241],v1 offset:28672\n"
AR "v224,a64\n"
MF "v[24:27],a[164:167],v[180:183],v[24:27]\n"
"s_add_u32 s61,s58,1\n"
TR "a[242:243],v1 offset:30720\n"
AR "v225,a65\n"
MF "v[24:27],a[124:127],v[176:179],v[24:27]\n"
"s_and_b32 s61,s61,7\n"
AR "v226,a66\n"
AR "v227,a67\n"
MF "v[32:35],a[128:131],v[176:179],v[252:255]\n"
"s_mov_b32 s64,9\n"
TR "a[244:245],v1 offset:32768\n"
"ds_read_b128 a[64:67],v236 offset:49152\n"
MF "v[32:35],a[168:171],v[180:183],v[32:35]\n"
"s_xor_b32 s67,s78,8\n"
TR "a[246:247],v1 offset:34816\n"
"v_fma_f32 v220,v220,v209,v215\n"
MF "v[32:35],a[208:211],v[184:187],v[32:35]\n"
"s_mov_b32 s71,204800\n"
"v_fma_f32 v221,v221,v209,v215\n"
"v_fma_f32 v222,v222,v209,v215\n"
MF "v[40:43],a[212:215],v[184:187],v[252:255]\n"
"s_mov_b32 s66,204800\n"
"v_fma_f32 v223,v223,v209,v215\n"
"v_fma_f32 v224,v224,v209,v215\n"
MF "v[40:43],a[172:175],v[180:183],v[40:43]\n"
"s_and_b32 s73,s56,1\n"
"v_fma_f32 v225,v225,v209,v215\n"
"v_fma_f32 v226,v226,v209,v215\n"
MF "v[40:43],a[132:135],v[176:179],v[40:43]\n"
"s_mul_i32 s73,s73,5120\n"
"v_fma_f32 v227,v227,v209,v215\n"
CV "v220,v220,v221\n"
MF "v[48:51],a[136:139],v[176:179],v[252:255]\n"
"s_add_u32 s73,s73,s93\n"
CV "v221,v222,v223\n"
CV "v222,v224,v225\n"
MF "v[48:51],a[176:179],v[180:183],v[48:51]\n"
"s_xor_b32 s58,s58,4\n"
CV "v223,v226,v227\n"
AW "a44,v220\n"
MF "v[48:51],a[216:219],v[184:187],v[48:51]\n"
"s_waitcnt lgkmcnt(0)\n"
AW "a45,v221\n"
AW "a46,v222\n"
MF "v[52:55],a[216:219],a[244:247],v[252:255]\n"
"s_mul_i32 s70,s58,204800\n"
TR "v[176:177],v2 offset:24576\n"
AW "a47,v223\n"
MF "v[52:55],a[176:179],a[240:243],v[52:55]\n"
"s_add_u32 s70,s70,s73\n"
TR "v[178:179],v2 offset:26624\n"
AR "v228,a84\n"
MF "v[52:55],a[136:139],v[188:191],v[52:55]\n"
"s_add_u32 s70,s70,0xa000\n"
AR "v229,a85\n"
AR "v230,a86\n"
MF "v[44:47],a[132:135],v[188:191],v[252:255]\n"
"s_mov_b32 s85,1\n"
TR "v[180:181],v2 offset:28672\n"
AR "v231,a87\n"
MF "v[44:47],a[172:175],a[240:243],v[44:47]\n"
"s_mov_b32 s83,0xf0000000\n"
TR "v[182:183],v2 offset:30720\n"
AR "v232,a104\n"
MF "v[44:47],a[212:215],a[244:247],v[44:47]\n"
"s_mov_b32 s82,0xf0000000\n"
AR "v233,a105\n"
AR "v234,a106\n"
MF "v[36:39],a[208:211],a[244:247],v[252:255]\n"
"s_add_u32 s58,s79,73728\n"
TR "v[184:185],v2 offset:32768\n"
AR "v235,a107\n"
MF "v[36:39],a[168:171],a[240:243],v[36:39]\n"
"s_mov_b32 s4,64\n"
TR "v[186:187],v2 offset:34816\n"
"ds_read_b128 a[104:107],v237 offset:4096\n"
MF "v[36:39],a[128:131],v[188:191],v[36:39]\n"
"s_mul_i32 s95,s92,3277\n"
"v_fma_f32 v228,v228,v209,v215\n"
"v_fma_f32 v229,v229,v209,v215\n"
MF "v[28:31],a[124:127],v[188:191],v[252:255]\n"
"s_lshr_b32 s95,s95,16\n"
"v_fma_f32 v230,v230,v209,v215\n"
"v_fma_f32 v231,v231,v209,v215\n"
MF "v[28:31],a[164:167],a[240:243],v[28:31]\n"
"s_mul_i32 s59,s95,20\n"
"v_fma_f32 v232,v232,v209,v215\n"
"v_fma_f32 v233,v233,v209,v215\n"
MF "v[28:31],a[204:207],a[244:247],v[28:31]\n"
"s_sub_u32 s59,s92,s59\n"
"v_fma_f32 v234,v234,v209,v215\n"
"v_fma_f32 v235,v235,v209,v215\n"
MF "v[20:23],a[200:203],a[244:247],v[252:255]\n"
"s_cmp_ge_u32 s59,10\n"
"s_cselect_b32 s96,4294557760,0\n"
CV "v228,v228,v229\n"
MF "v[20:23],a[160:163],a[240:243],v[20:23]\n"
"s_add_u32 s96,s96,1024\n"
CV "v229,v230,v231\n"
CV "v230,v232,v233\n"
MF "v[20:23],a[120:123],v[188:191],v[20:23]\n"
"s_waitcnt lgkmcnt(1)\n"
CV "v231,v234,v235\n"
AW "a84,v228\n"
MF "v[56:59],a[120:123],v[176:179],v[252:255]\n"
"s_mul_i32 s59,s59,40960\n"
TR "v[188:189],v3 offset:24576\n"
AW "a85,v229\n"
MF "v[56:59],a[160:163],v[180:183],v[56:59]\n"
"s_add_u32 s96,s96,s59\n"
TR "v[190:191],v3 offset:26624\n"
AW "a86,v230\n"
MF "v[56:59],a[200:203],v[184:187],v[56:59]\n"
"s_lshl_b32 s59,s95,7\n"
AW "a87,v231\n"
AR "v220,a8\n"
MF "v[64:67],a[204:207],v[184:187],v[252:255]\n"
"s_add_u32 s96,s96,s59\n"
TR "a[240:241],v3 offset:28672\n"
AR "v221,a9\n"
MF "v[64:67],a[164:167],v[180:183],v[64:67]\n"
"s_lshl_b32 s2,s92,10\n"
TR "a[242:243],v3 offset:30720\n"
AR "v222,a10\n"
MF "v[64:67],a[124:127],v[176:179],v[64:67]\n"
"s_add_u32 s2,s2,s93\n"
AR "v223,a11\n"
AR "v224,a28\n"
MF "v[72:75],a[128:131],v[176:179],v[252:255]\n"
"s_mov_b32 s0,s96\n"
TR "a[244:245],v3 offset:32768\n"
AR "v225,a29\n"
MF "v[72:75],a[168:171],v[180:183],v[72:75]\n"
"s_sub_u32 s2,s2,204800\n"
TR "a[246:247],v3 offset:34816\n"
AR "v226,a30\n"
MF "v[72:75],a[208:211],v[184:187],v[72:75]\n"
"s_mov_b32 s92,64\n"
AR "v227,a31\n"
"v_fma_f32 v220,v220,v210,v216\n"
MF "v[80:83],a[212:215],v[184:187],v[252:255]\n"
"ds_read_b128 a[28:31],v236 offset:32768\n"
"v_fma_f32 v221,v221,v210,v216\n"
MF "v[80:83],a[172:175],v[180:183],v[80:83]\n"
"v_fma_f32 v222,v222,v210,v216\n"
"v_fma_f32 v223,v223,v210,v216\n"
MF "v[80:83],a[132:135],v[176:179],v[80:83]\n"
"v_fma_f32 v224,v224,v210,v216\n"
"v_fma_f32 v225,v225,v210,v216\n"
MF "v[88:91],a[136:139],v[176:179],v[252:255]\n"
"v_fma_f32 v226,v226,v210,v216\n"
"v_fma_f32 v227,v227,v210,v216\n"
MF "v[88:91],a[176:179],v[180:183],v[88:91]\n"
"s_waitcnt vmcnt(0) lgkmcnt(0)\n"
CV "v220,v220,v221\n"
CV "v221,v222,v223\n"
MF "v[88:91],a[216:219],v[184:187],v[88:91]\n"
"s_barrier\n"
MF "v[92:95],a[216:219],a[244:247],v[252:255]\n"
TR "v[176:177],v0\n"
CV "v222,v224,v225\n"
MF "v[92:95],a[176:179],a[240:243],v[92:95]\n"
TR "v[178:179],v0 offset:2048\n"
CV "v223,v226,v227\n"
MF "v[92:95],a[136:139],v[188:191],v[92:95]\n"
TR "v[96:97],v0 offset:49152\n"
AW "a8,v220\n"
MF "v[84:87],a[132:135],v[188:191],v[252:255]\n"
TR "v[180:181],v0 offset:4096\n"
AW "a9,v221\n"
MF "v[84:87],a[172:175],a[240:243],v[84:87]\n"
TR "v[182:183],v0 offset:6144\n"
AW "a10,v222\n"
MF "v[84:87],a[212:215],a[244:247],v[84:87]\n"
TR "v[98:99],v0 offset:51200\n"
AW "a11,v223\n"
MF "v[76:79],a[208:211],a[244:247],v[252:255]\n"
TR "v[184:185],v0 offset:8192\n"
AR "v228,a48\n"
MF "v[76:79],a[168:171],a[240:243],v[76:79]\n"
TR "v[186:187],v0 offset:10240\n"
AR "v229,a49\n"
MF "v[76:79],a[128:131],v[188:191],v[76:79]\n"
TR "v[100:101],v0 offset:53248\n"
AR "v230,a50\n"
MF "v[68:71],a[124:127],v[188:191],v[252:255]\n"
TR "v[102:103],v0 offset:55296\n"
AR "v231,a51\n"
MF "v[68:71],a[164:167],a[240:243],v[68:71]\n"
TR "v[104:105],v0 offset:57344\n"
AR "v232,a68\n"
MF "v[68:71],a[204:207],a[244:247],v[68:71]\n"
TR "v[106:107],v0 offset:59392\n"
AR "v233,a69\n"
MF "v[60:63],a[200:203],a[244:247],v[252:255]\n"
TR "v[108:109],v4\n"
AR "v234,a70\n"
MF "v[60:63],a[160:163],a[240:243],v[60:63]\n"
TR "v[110:111],v4 offset:2048\n"
AR "v235,a71\n"
MF "v[60:63],a[120:123],v[188:191],v[60:63]\n"
"s_waitcnt lgkmcnt(6)\n"
TR "v[112:113],v4 offset:4096\n"
"ds_read_b128 a[68:71],v236 offset:53248\n"
MF "v[16:19],a[140:143],v[176:179],v[16:19]\n"
TR "v[188:189],v1\n"
"v_fma_f32 v228,v228,v210,v216\n"
MF "v[16:19],a[180:183],v[180:183],v[16:19]\n"
TR "v[190:191],v1 offset:2048\n"
"v_fma_f32 v229,v229,v210,v216\n"
MF "v[16:19],a[220:223],v[184:187],v[16:19]\n"
TR "v[114:115],v4 offset:6144\n"
"v_fma_f32 v230,v230,v210,v216\n"
MF "v[24:27],a[224:227],v[184:187],v[24:27]\n"
TR "a[240:241],v1 offset:4096\n"
"v_fma_f32 v231,v231,v210,v216\n"
MF "v[24:27],a[184:187],v[180:183],v[24:27]\n"
TR "a[242:243],v1 offset:6144\n"
"v_fma_f32 v232,v232,v210,v216\n"
MF "v[24:27],a[144:147],v[176:179],v[24:27]\n"
TR "v[116:117],v4 offset:8192\n"
"v_fma_f32 v233,v233,v210,v216\n"
MF "v[32:35],a[148:151],v[176:179],v[32:35]\n"
TR "a[244:245],v1 offset:8192\n"
"v_fma_f32 v234,v234,v210,v216\n"
MF "v[32:35],a[188:191],v[180:183],v[32:35]\n"
TR "a[246:247],v1 offset:10240\n"
"v_fma_f32 v235,v235,v210,v216\n"
MF "v[32:35],a[228:231],v[184:187],v[32:35]\n"
TR "v[118:119],v4 offset:10240\n"
CV "v228,v228,v229\n"
MF "v[40:43],a[232:235],v[184:187],v[40:43]\n"
TR "v[120:121],v1 offset:49152\n"
CV "v229,v230,v231\n"
MF "v[40:43],a[192:195],v[180:183],v[40:43]\n"
TR "v[122:123],v1 offset:51200\n"
CV "v230,v232,v233\n"
MF "v[40:43],a[152:155],v[176:179],v[40:43]\n"
TR "v[124:125],v1 offset:53248\n"
CV "v231,v234,v235\n"
MF "v[48:51],a[156:159],v[176:179],v[48:51]\n"
TR "v[126:127],v1 offset:55296\n"
AW "a48,v228\n"
MF "v[48:51],a[196:199],v[180:183],v[48:51]\n"
TR "v[128:129],v1 offset:57344\n"
AW "a49,v229\n"
MF "v[48:51],a[236:239],v[184:187],v[48:51]\n"
"s_waitcnt lgkmcnt(6)\n"
TR "v[130:131],v1 offset:59392\n"
AW "a50,v230\n"
MF "v[52:55],a[236:239],a[244:247],v[52:55]\n"
TR "v[176:177],v2\n"
AW "a51,v231\n"
MF "v[52:55],a[196:199],a[240:243],v[52:55]\n"
TR "v[178:179],v2 offset:2048\n"
AR "v220,a88\n"
MF "v[52:55],a[156:159],v[188:191],v[52:55]\n"
TR "v[132:133],v5\n"
AR "v221,a89\n"
MF "v[44:47],a[152:155],v[188:191],v[44:47]\n"
TR "v[180:181],v2 offset:4096\n"
AR "v222,a90\n"
MF "v[44:47],a[192:195],a[240:243],v[44:47]\n"
TR "v[182:183],v2 offset:6144\n"
AR "v223,a91\n"
MF "v[44:47],a[232:235],a[244:247],v[44:47]\n"
TR "v[134:135],v5 offset:2048\n"
AR "v224,a108\n"
MF "v[36:39],a[228:231],a[244:247],v[36:39]\n"
TR "v[184:185],v2 offset:8192\n"
AR "v225,a109\n"
MF "v[36:39],a[188:191],a[240:243],v[36:39]\n"
TR "v[186:187],v2 offset:10240\n"
AR "v226,a110\n"
MF "v[36:39],a[148:151],v[188:191],v[36:39]\n"
TR "v[136:137],v5 offset:4096\n"
AR "v227,a111\n"
MF "v[28:31],a[144:147],v[188:191],v[28:31]\n"
"s_nop 0\n"
TR "v[138:139],v5 offset:6144\n"
"ds_read_b128 a[108:111],v237 offset:8192\n"
MF "v[28:31],a[184:187],a[240:243],v[28:31]\n"
TR "v[140:141],v5 offset:8192\n"
"v_fma_f32 v220,v220,v210,v216\n"
MF "v[28:31],a[224:227],a[244:247],v[28:31]\n"
TR "v[142:143],v5 offset:10240\n"
"v_fma_f32 v221,v221,v210,v216\n"
MF "v[20:23],a[220:223],a[244:247],v[20:23]\n"
TR "v[144:145],v2 offset:49152\n"
"v_fma_f32 v222,v222,v210,v216\n"
MF "v[20:23],a[180:183],a[240:243],v[20:23]\n"
TR "v[146:147],v2 offset:51200\n"
"v_fma_f32 v223,v223,v210,v216\n"
MF "v[20:23],a[140:143],v[188:191],v[20:23]\n"
"s_waitcnt lgkmcnt(7)\n"
TR "v[148:149],v2 offset:53248\n"
"v_fma_f32 v224,v224,v210,v216\n"
MF "v[56:59],a[140:143],v[176:179],v[56:59]\n"
TR "v[188:189],v3\n"
"v_add_u32 v198,59392,v192\n"
MF "v[56:59],a[180:183],v[180:183],v[56:59]\n"
TR "v[190:191],v3 offset:2048\n"
"v_add_u32 v199,68608,v192\n"
MF "v[56:59],a[220:223],v[184:187],v[56:59]\n"
TR "v[150:151],v2 offset:55296\n"
"v_add_u32 v200,81920,v192\n"
"v_add_u32 v201,91136,v192\n"
MF "v[64:67],a[224:227],v[184:187],v[64:67]\n"
TR "a[240:241],v3 offset:4096\n"
"v_add_u32 v202,100352,v192\n"
MF "v[64:67],a[184:187],v[180:183],v[64:67]\n"
TR "a[242:243],v3 offset:6144\n"
"v_add_u32 v203,109568,v192\n"
MF "v[64:67],a[144:147],v[176:179],v[64:67]\n"
TR "v[152:153],v2 offset:57344\n"
"v_fma_f32 v225,v225,v210,v216\n"
MF "v[72:75],a[148:151],v[176:179],v[72:75]\n"
TR "a[244:245],v3 offset:8192\n"
"v_fma_f32 v226,v226,v210,v216\n"
MF "v[72:75],a[188:191],v[180:183],v[72:75]\n"
TR "a[246:247],v3 offset:10240\n"
"v_fma_f32 v227,v227,v210,v216\n"
MF "v[72:75],a[228:231],v[184:187],v[72:75]\n"
TR "v[154:155],v2 offset:59392\n"
CV "v220,v220,v221\n"
MF "v[80:83],a[232:235],v[184:187],v[80:83]\n"
TR "v[156:157],v6\n"
CV "v221,v222,v223\n"
MF "v[80:83],a[192:195],v[180:183],v[80:83]\n"
TR "v[158:159],v6 offset:2048\n"
CV "v222,v224,v225\n"
MF "v[80:83],a[152:155],v[176:179],v[80:83]\n"
TR "v[160:161],v6 offset:4096\n"
CV "v223,v226,v227\n"
MF "v[88:91],a[156:159],v[176:179],v[88:91]\n"
TR "v[162:163],v6 offset:6144\n"
AW "a88,v220\n"
MF "v[88:91],a[196:199],v[180:183],v[88:91]\n"
TR "v[164:165],v6 offset:8192\n"
"s_waitcnt lgkmcnt(0)\n"
MF "v[88:91],a[236:239],v[184:187],v[88:91]\n"
"s_barrier\n"
MF "v[92:95],a[236:239],a[244:247],v[92:95]\n"
"s_add_u32 s80,s89,122880\n"
"s_add_u32 m0,s79,0\n"
TR "v[166:167],v6 offset:10240\n"
MF "v[92:95],a[196:199],a[240:243],v[92:95]\n"
LD "v192,s[48:51],s80 offen lds\n"
MF "v[92:95],a[156:159],v[188:191],v[92:95]\n"
TR "v[168:169],v3 offset:49152\n"
AW "a89,v221\n"
MF "v[84:87],a[152:155],v[188:191],v[84:87]\n"
TR "v[170:171],v3 offset:51200\n"
AW "a90,v222\n"
MF "v[84:87],a[192:195],a[240:243],v[84:87]\n"
TR "v[172:173],v3 offset:53248\n"
AW "a91,v223\n"
MF "v[84:87],a[232:235],a[244:247],v[84:87]\n"
LD "v193,s[48:51],s80 offen offset:1024 lds\n"
MF "v[76:79],a[228:231],a[244:247],v[76:79]\n"
TR "v[174:175],v3 offset:55296\n"
AR "v228,a12\n"
MF "v[76:79],a[188:191],a[240:243],v[76:79]\n"
TR "v[204:205],v3 offset:57344\n"
AR "v229,a13\n"
MF "v[76:79],a[148:151],v[188:191],v[76:79]\n"
TR "v[206:207],v3 offset:59392\n"
AR "v230,a14\n"
MF "v[68:71],a[144:147],v[188:191],v[68:71]\n"
TR "v[244:245],v7\n"
AR "v231,a15\n"
MF "v[68:71],a[184:187],a[240:243],v[68:71]\n"
LD "v194,s[48:51],s80 offen offset:2048 lds\n"
MF "v[68:71],a[224:227],a[244:247],v[68:71]\n"
TR "v[246:247],v7 offset:2048\n"
AR "v232,a32\n"
MF "v[60:63],a[220:223],a[244:247],v[60:63]\n"
TR "v[248:249],v7 offset:4096\n"
AR "v233,a33\n"
MF "v[60:63],a[180:183],a[240:243],v[60:63]\n"
TR "v[250:251],v7 offset:6144\n"
AR "v234,a34\n"
MF "v[60:63],a[140:143],v[188:191],v[60:63]\n"
LD "v195,s[48:51],s80 offen offset:3072 lds\n"
MF "v[16:19],a[0:3],v[96:99],v[16:19]\n"
TR "v[238:239],v7 offset:8192\n"
AR "v235,a35\n"
MF "v[16:19],a[40:43],v[100:103],v[16:19]\n"
"s_nop 0\n"
TR "v[240:241],v7 offset:10240\n"
"ds_read_b128 a[32:35],v236 offset:36864\n"
MF "v[16:19],a[80:83],v[104:107],v[16:19]\n"
"v_fma_f32 v228,v228,v211,v217\n"
"v_fma_f32 v229,v229,v211,v217\n"
MF "v[16:19],a[20:23],v[108:111],v[16:19]\n"
"s_add_u32 m0,s79,4096\n"
"v_fma_f32 v230,v230,v211,v217\n"
"v_fma_f32 v231,v231,v211,v217\n"
MF "v[16:19],a[60:63],v[112:115],v[16:19]\n"
LD "v196,s[48:51],s80 offen lds\n"
MF "v[16:19],a[100:103],v[116:119],v[16:19]\n"
"v_fma_f32 v232,v232,v211,v217\n"
"v_fma_f32 v233,v233,v211,v217\n"
MF "v[20:23],a[100:103],v[140:143],v[20:23]\n"
".Lpg25h:\n"
"s_cbranch_execz .Lpg29h\n"
"v_fma_f32 v234,v234,v211,v217\n"
"v_fma_f32 v235,v235,v211,v217\n"
MF "v[20:23],a[60:63],v[136:139],v[20:23]\n"
CV "v228,v228,v229\n"
CV "v229,v230,v231\n"
MF "v[20:23],a[20:23],v[132:135],v[20:23]\n"
LD "v197,s[48:51],s80 offen offset:1024 lds\n"
MF "v[20:23],a[80:83],v[128:131],v[20:23]\n"
CV "v230,v232,v233\n"
CV "v231,v234,v235\n"
MF "v[20:23],a[40:43],v[124:127],v[20:23]\n"
AW "a12,v228\n"
AW "a13,v229\n"
MF "v[20:23],a[0:3],v[120:123],v[20:23]\n"
"s_waitcnt lgkmcnt(13)\n"
AW "a14,v230\n"
AW "a15,v231\n"
MF "v[56:59],a[0:3],v[144:147],v[56:59]\n"
AR "v220,a52\n"
AR "v221,a53\n"
MF "v[56:59],a[40:43],v[148:151],v[56:59]\n"
LD "v198,s[48:51],s80 offen offset:2048 lds\n"
MF "v[56:59],a[80:83],v[152:155],v[56:59]\n"
AR "v222,a54\n"
AR "v223,a55\n"
MF "v[56:59],a[20:23],v[156:159],v[56:59]\n"
AR "v224,a72\n"
AR "v225,a73\n"
MF "v[56:59],a[60:63],v[160:163],v[56:59]\n"
AR "v226,a74\n"
AR "v227,a75\n"
MF "v[56:59],a[100:103],v[164:167],v[56:59]\n"
"s_waitcnt lgkmcnt(1)\n"
"ds_read_b128 a[72:75],v236 offset:57344\n"
"v_fma_f32 v220,v220,v211,v217\n"
MF "v[60:63],a[100:103],v[238:241],v[60:63]\n"
LD "v199,s[48:51],s80 offen offset:3072 lds\n"
MF "v[60:63],a[60:63],v[248:251],v[60:63]\n"
"v_fma_f32 v221,v221,v211,v217\n"
"v_fma_f32 v222,v222,v211,v217\n"
MF "v[60:63],a[20:23],v[244:247],v[60:63]\n"
"v_fma_f32 v223,v223,v211,v217\n"
"v_fma_f32 v224,v224,v211,v217\n"
MF "v[60:63],a[80:83],v[204:207],v[60:63]\n"
"s_add_u32 m0,s79,8192\n"
"v_fma_f32 v225,v225,v211,v217\n"
"v_fma_f32 v226,v226,v211,v217\n"
MF "v[60:63],a[40:43],v[172:175],v[60:63]\n"
LD "v200,s[48:51],s80 offen lds\n"
MF "v[60:63],a[0:3],v[168:171],v[60:63]\n"
"v_fma_f32 v227,v227,v211,v217\n"
CV "v220,v220,v221\n"
MF "v[68:71],a[4:7],v[168:171],v[68:71]\n"
CV "v221,v222,v223\n"
CV "v222,v224,v225\n"
MF "v[68:71],a[44:47],v[172:175],v[68:71]\n"
CV "v223,v226,v227\n"
AW "a52,v220\n"
MF "v[68:71],a[84:87],v[204:207],v[68:71]\n"
AW "a53,v221\n"
AW "a54,v222\n"
MF "v[68:71],a[24:27],v[244:247],v[68:71]\n"
LD "v201,s[48:51],s80 offen offset:1024 lds\n"
MF "v[68:71],a[64:67],v[248:251],v[68:71]\n"
AW "a55,v223\n"
AR "v228,a92\n"
MF "v[68:71],a[104:107],v[238:241],v[68:71]\n"
"s_waitcnt lgkmcnt(0)\n"
AR "v229,a93\n"
AR "v230,a94\n"
MF "v[64:67],a[104:107],v[164:167],v[64:67]\n"
"s_barrier\n"
MF "v[64:67],a[64:67],v[160:163],v[64:67]\n"
LD "v202,s[48:51],s80 offen offset:2048 lds\n"
MF "v[64:67],a[24:27],v[156:159],v[64:67]\n"
AR "v231,a95\n"
AR "v232,a112\n"
MF "v[64:67],a[84:87],v[152:155],v[64:67]\n"
AR "v233,a113\n"
AR "v234,a114\n"
MF "v[64:67],a[44:47],v[148:151],v[64:67]\n"
AR "v235,a115\n"
"v_fma_f32 v228,v228,v211,v217\n"
MF "v[64:67],a[4:7],v[144:147],v[64:67]\n"
"ds_read_b128 a[112:115],v237 offset:12288\n"
"v_fma_f32 v229,v229,v211,v217\n"
MF "v[28:31],a[4:7],v[120:123],v[28:31]\n"
LD "v203,s[48:51],s80 offen offset:3072 lds\n"
MF "v[28:31],a[44:47],v[124:127],v[28:31]\n"
"v_fma_f32 v230,v230,v211,v217\n"
"v_fma_f32 v231,v231,v211,v217\n"
MF "v[28:31],a[84:87],v[128:131],v[28:31]\n"
"v_fma_f32 v232,v232,v211,v217\n"
"v_fma_f32 v233,v233,v211,v217\n"
MF "v[28:31],a[24:27],v[132:135],v[28:31]\n"
"v_fma_f32 v234,v234,v211,v217\n"
"v_fma_f32 v235,v235,v211,v217\n"
MF "v[28:31],a[64:67],v[136:139],v[28:31]\n"
CV "v228,v228,v229\n"
CV "v229,v230,v231\n"
MF "v[28:31],a[104:107],v[140:143],v[28:31]\n"
"s_add_u32 s88,s88,s65\n"
"s_add_u32 m0,s79,49152\n"
CV "v230,v232,v233\n"
MF "v[24:27],a[104:107],v[116:119],v[24:27]\n"
LD "v192,s[48:51],s88 offen lds\n"
MF "v[24:27],a[64:67],v[112:115],v[24:27]\n"
CV "v231,v234,v235\n"
AW "a92,v228\n"
MF "v[24:27],a[24:27],v[108:111],v[24:27]\n"
AW "a93,v229\n"
AW "a94,v230\n"
MF "v[24:27],a[84:87],v[104:107],v[24:27]\n"
AW "a95,v231\n"
AR "v220,a16\n"
MF "v[24:27],a[44:47],v[100:103],v[24:27]\n"
LD "v193,s[48:51],s88 offen offset:1024 lds\n"
MF "v[24:27],a[4:7],v[96:99],v[24:27]\n"
AR "v221,a17\n"
AR "v222,a18\n"
MF "v[32:35],a[8:11],v[96:99],v[32:35]\n"
AR "v223,a19\n"
AR "v224,a36\n"
MF "v[32:35],a[48:51],v[100:103],v[32:35]\n"
AR "v225,a37\n"
AR "v226,a38\n"
MF "v[32:35],a[88:91],v[104:107],v[32:35]\n"
AR "v227,a39\n"
"v_fma_f32 v220,v220,v212,v218\n"
MF "v[32:35],a[28:31],v[108:111],v[32:35]\n"
LD "v194,s[48:51],s88 offen offset:2048 lds\n"
MF "v[32:35],a[68:71],v[112:115],v[32:35]\n"
"ds_read_b128 a[36:39],v236 offset:40960\n"
"v_fma_f32 v221,v221,v212,v218\n"
MF "v[32:35],a[108:111],v[116:119],v[32:35]\n"
"v_fma_f32 v222,v222,v212,v218\n"
"v_fma_f32 v223,v223,v212,v218\n"
MF "v[36:39],a[108:111],v[140:143],v[36:39]\n"
"v_fma_f32 v224,v224,v212,v218\n"
"v_fma_f32 v225,v225,v212,v218\n"
MF "v[36:39],a[68:71],v[136:139],v[36:39]\n"
"v_fma_f32 v226,v226,v212,v218\n"
"v_fma_f32 v227,v227,v212,v218\n"
MF "v[36:39],a[28:31],v[132:135],v[36:39]\n"
LD "v195,s[48:51],s88 offen offset:3072 lds\n"
MF "v[36:39],a[88:91],v[128:131],v[36:39]\n"
CV "v220,v220,v221\n"
CV "v221,v222,v223\n"
MF "v[36:39],a[48:51],v[124:127],v[36:39]\n"
CV "v222,v224,v225\n"
CV "v223,v226,v227\n"
MF "v[36:39],a[8:11],v[120:123],v[36:39]\n"
AW "a16,v220\n"
AW "a17,v221\n"
MF "v[72:75],a[8:11],v[144:147],v[72:75]\n"
"s_add_u32 m0,s79,53248\n"
AW "a18,v222\n"
AW "a19,v223\n"
MF "v[72:75],a[48:51],v[148:151],v[72:75]\n"
LD "v196,s[48:51],s88 offen lds\n"
MF "v[72:75],a[88:91],v[152:155],v[72:75]\n"
AR "v228,a56\n"
AR "v229,a57\n"
MF "v[72:75],a[28:31],v[156:159],v[72:75]\n"
AR "v230,a58\n"
AR "v231,a59\n"
MF "v[72:75],a[68:71],v[160:163],v[72:75]\n"
AR "v232,a76\n"
AR "v233,a77\n"
MF "v[72:75],a[108:111],v[164:167],v[72:75]\n"
AR "v234,a78\n"
AR "v235,a79\n"
MF "v[76:79],a[108:111],v[238:241],v[76:79]\n"
LD "v197,s[48:51],s88 offen offset:1024 lds\n"
MF "v[76:79],a[68:71],v[248:251],v[76:79]\n"
"ds_read_b128 a[76:79],v236 offset:61440\n"
"v_fma_f32 v228,v228,v212,v218\n"
MF "v[76:79],a[28:31],v[244:247],v[76:79]\n"
"v_fma_f32 v229,v229,v212,v218\n"
"v_fma_f32 v230,v230,v212,v218\n"
MF "v[76:79],a[88:91],v[204:207],v[76:79]\n"
"v_fma_f32 v231,v231,v212,v218\n"
"v_fma_f32 v232,v232,v212,v218\n"
MF "v[76:79],a[48:51],v[172:175],v[76:79]\n"
"v_fma_f32 v233,v233,v212,v218\n"
"v_fma_f32 v234,v234,v212,v218\n"
MF "v[76:79],a[8:11],v[168:171],v[76:79]\n"
"s_waitcnt lgkmcnt(2)\n"
LD "v198,s[48:51],s88 offen offset:2048 lds\n"
MF "v[84:87],a[12:15],v[168:171],v[84:87]\n"
"v_fma_f32 v235,v235,v212,v218\n"
CV "v228,v228,v229\n"
MF "v[84:87],a[52:55],v[172:175],v[84:87]\n"
CV "v229,v230,v231\n"
CV "v230,v232,v233\n"
MF "v[84:87],a[92:95],v[204:207],v[84:87]\n"
CV "v231,v234,v235\n"
AW "a56,v228\n"
MF "v[84:87],a[32:35],v[244:247],v[84:87]\n"
AW "a57,v229\n"
AW "a58,v230\n"
MF "v[84:87],a[72:75],v[248:251],v[84:87]\n"
LD "v199,s[48:51],s88 offen offset:3072 lds\n"
MF "v[84:87],a[112:115],v[238:241],v[84:87]\n"
AW "a59,v231\n"
AR "v220,a96\n"
MF "v[80:83],a[112:115],v[164:167],v[80:83]\n"
AR "v221,a97\n"
AR "v222,a98\n"
MF "v[80:83],a[72:75],v[160:163],v[80:83]\n"
AR "v223,a99\n"
AR "v224,a116\n"
MF "v[80:83],a[32:35],v[156:159],v[80:83]\n"
"s_add_u32 m0,s79,57344\n"
AR "v225,a117\n"
AR "v226,a118\n"
MF "v[80:83],a[92:95],v[152:155],v[80:83]\n"
LD "v200,s[48:51],s88 offen lds\n"
MF "v[80:83],a[52:55],v[148:151],v[80:83]\n"
AR "v227,a119\n"
"v_fma_f32 v220,v220,v212,v218\n"
MF "v[80:83],a[12:15],v[144:147],v[80:83]\n"
"ds_read_b128 a[116:119],v237 offset:16384\n"
"v_fma_f32 v221,v221,v212,v218\n"
MF "v[44:47],a[12:15],v[120:123],v[44:47]\n"
"v_fma_f32 v222,v222,v212,v218\n"
"v_fma_f32 v223,v223,v212,v218\n"
MF "v[44:47],a[52:55],v[124:127],v[44:47]\n"
"v_fma_f32 v224,v224,v212,v218\n"
"v_fma_f32 v225,v225,v212,v218\n"
MF "v[44:47],a[92:95],v[128:131],v[44:47]\n"
LD "v201,s[48:51],s88 offen offset:1024 lds\n"
MF "v[44:47],a[32:35],v[132:135],v[44:47]\n"
"v_fma_f32 v226,v226,v212,v218\n"
"v_fma_f32 v227,v227,v212,v218\n"
MF "v[44:47],a[72:75],v[136:139],v[44:47]\n"
CV "v220,v220,v221\n"
CV "v221,v222,v223\n"
MF "v[44:47],a[112:115],v[140:143],v[44:47]\n"
CV "v222,v224,v225\n"
CV "v223,v226,v227\n"
MF "v[40:43],a[112:115],v[116:119],v[40:43]\n"
AW "a96,v220\n"
AW "a97,v221\n"
MF "v[40:43],a[72:75],v[112:115],v[40:43]\n"
LD "v202,s[48:51],s88 offen offset:2048 lds\n"
MF "v[40:43],a[32:35],v[108:111],v[40:43]\n"
AW "a98,v222\n"
AW "a99,v223\n"
MF "v[40:43],a[92:95],v[104:107],v[40:43]\n"
AR "v8,a252\n"
AR "v11,a253\n"
MF "v[40:43],a[52:55],v[100:103],v[40:43]\n"
"v_bfe_u32 v224,v9,5,3\n"
"v_bfe_u32 v225,v9,8,2\n"
MF "v[40:43],a[12:15],v[96:99],v[40:43]\n"
"s_waitcnt lgkmcnt(0)\n"
"v_lshlrev_b32 v225,1,v225\n"
"v_and_b32 v226,16,v9\n"
MF "v[48:51],a[16:19],v[96:99],v[48:51]\n"
LD "v203,s[48:51],s88 offen offset:3072 lds\n"
MF "v[48:51],a[56:59],v[100:103],v[48:51]\n"
"v_lshlrev_b32_e64 v226,v226,s9\n"
"v_sub_u32 v224,v224,v225\n"
MF "v[48:51],a[96:99],v[104:107],v[48:51]\n"
"v_cmp_eq_u32 vcc,0,v224\n"
MF "v[48:51],a[36:39],v[108:111],v[48:51]\n"
"s_nop 0\n"
"v_cndmask_b32 v220,0,v226,vcc\n"
"v_cmp_eq_u32 vcc,1,v224\n"
MF "v[48:51],a[76:79],v[112:115],v[48:51]\n"
"s_nop 0\n"
"v_cndmask_b32 v221,0,v226,vcc\n"
AW "a248,0\n"
MF "v[48:51],a[116:119],v[116:119],v[48:51]\n"
AW "a249,0\n"
AW "a250,v220\n"
MF "v[52:55],a[116:119],v[140:143],v[52:55]\n"
AW "a251,v221\n"
AW "a252,v220\n"
MF "v[52:55],a[76:79],v[136:139],v[52:55]\n"
"s_waitcnt lgkmcnt(0)\n"
AW "a253,v221\n"
AW "a254,0\n"
MF "v[52:55],a[36:39],v[132:135],v[52:55]\n"
"s_barrier\n"
MF "v[52:55],a[96:99],v[128:131],v[52:55]\n"
"s_add_u32 s80,s88,122880\n"
TR "v[176:177],v0 offset:24576\n"
AW "a255,0\n"
MF "v[52:55],a[56:59],v[124:127],v[52:55]\n"
"s_add_u32 m0,s79,98304\n"
TR "v[178:179],v0 offset:26624\n"
"v_bfe_u32 v12,v9,4,4\n"
MF "v[52:55],a[16:19],v[120:123],v[52:55]\n"
LD "v192,s[48:51],s80 offen lds\n"
MF "v[88:91],a[16:19],v[144:147],v[88:91]\n"
"s_nop 0\n"
TR "v[180:181],v0 offset:28672\n"
"ds_write_addtid_b32 v16 offset:24576\n"
MF "v[88:91],a[56:59],v[148:151],v[88:91]\n"
"s_nop 0\n"
TR "v[182:183],v0 offset:30720\n"
"ds_write_addtid_b32 v17 offset:24832\n"
MF "v[88:91],a[96:99],v[152:155],v[88:91]\n"
LD "v193,s[48:51],s80 offen offset:1024 lds\n"
MF "v[88:91],a[36:39],v[156:159],v[88:91]\n"
"s_nop 0\n"
TR "v[184:185],v0 offset:32768\n"
"ds_write_addtid_b32 v18 offset:25088\n"
MF "v[88:91],a[76:79],v[160:163],v[88:91]\n"
"s_nop 0\n"
TR "v[186:187],v0 offset:34816\n"
"ds_write_addtid_b32 v19 offset:25344\n"
MF "v[88:91],a[116:119],v[164:167],v[88:91]\n"
"s_nop 0\n"
"ds_write_addtid_b32 v20 offset:25600\n"
"ds_write_addtid_b32 v21 offset:25856\n"
MF "v[92:95],a[116:119],v[238:241],v[92:95]\n"
LD "v194,s[48:51],s80 offen offset:2048 lds\n"
MF "v[92:95],a[76:79],v[248:251],v[92:95]\n"
"s_nop 0\n"
"ds_write_addtid_b32 v22 offset:26112\n"
"ds_write_addtid_b32 v23 offset:26368\n"
MF "v[92:95],a[36:39],v[244:247],v[92:95]\n"
"s_nop 0\n"
"ds_write_addtid_b32 v24 offset:26624\n"
"ds_write_addtid_b32 v25 offset:26880\n"
MF "v[92:95],a[96:99],v[204:207],v[92:95]\n"
"s_nop 0\n"
"ds_write_addtid_b32 v26 offset:27136\n"
"ds_write_addtid_b32 v27 offset:27392\n"
MF "v[92:95],a[56:59],v[172:175],v[92:95]\n"
"v_lshlrev_b32 v12,2,v12\n"
"v_lshrrev_b32 v13,2,v9\n"
MF "v[92:95],a[16:19],v[168:171],v[92:95]\n"
"s_waitcnt lgkmcnt(9)\n"
"v_add_u32 v13,s58,v13\n"
"v_add_u32 v4,24576,v4\n"
"v_add_u32 v5,24576,v5\n"
"v_add_u32 v6,24576,v6\n"
"v_add_u32 v7,24576,v7\n"
".Lpl05:\n"
MF "v[96:99],a[0:3],v[176:179],v[252:255]\n"
"s_nop 0\n"
TR "v[188:189],v1 offset:24576\n"
"ds_write_addtid_b32 v28 offset:27648\n"
MF "v[96:99],a[40:43],v[180:183],v[96:99]\n"
"s_nop 0\n"
TR "v[190:191],v1 offset:26624\n"
"ds_write_addtid_b32 v29 offset:27904\n"
MF "v[96:99],a[80:83],v[184:187],v[96:99]\n"
LD "v195,s[48:51],s80 offen offset:3072 lds\n"
MF "v[104:107],a[84:87],v[184:187],v[252:255]\n"
"s_add_u32 s60,s64,1\n"
TR "a[240:241],v1 offset:28672\n"
"ds_write_addtid_b32 v30 offset:28160\n"
MF "v[104:107],a[44:47],v[180:183],v[104:107]\n"
"s_add_u32 m0,s79,102400\n"
TR "a[242:243],v1 offset:30720\n"
"ds_write_addtid_b32 v31 offset:24320\n"
MF "v[104:107],a[4:7],v[176:179],v[104:107]\n"
LD "v196,s[48:51],s80 offen lds\n"
MF "v[112:115],a[8:11],v[176:179],v[252:255]\n"
"s_cmp_eq_u32 s60,8\n"
"s_cselect_b32 s60,0,s60\n"
TR "a[244:245],v1 offset:32768\n"
MF "v[112:115],a[48:51],v[180:183],v[112:115]\n"
"s_cmp_eq_u32 s60,10\n"
"s_cselect_b32 s60,s61,s60\n"
TR "a[246:247],v1 offset:34816\n"
MF "v[112:115],a[88:91],v[184:187],v[112:115]\n"
"s_sub_u32 s65,s60,s64\n"
"ds_write_addtid_b32 v32 offset:24576\n"
"ds_write_addtid_b32 v33 offset:24832\n"
MF "v[120:123],a[92:95],v[184:187],v[252:255]\n"
"s_lshl_b32 s65,s65,7\n"
"ds_write_addtid_b32 v34 offset:25088\n"
"ds_write_addtid_b32 v35 offset:25344\n"
MF "v[120:123],a[52:55],v[180:183],v[120:123]\n"
LD "v197,s[48:51],s80 offen offset:1024 lds\n"
MF "v[120:123],a[12:15],v[176:179],v[120:123]\n"
"s_mov_b32 s64,s60\n"
"ds_write_addtid_b32 v36 offset:25600\n"
"ds_write_addtid_b32 v37 offset:25856\n"
MF "v[128:131],a[16:19],v[176:179],v[252:255]\n"
"s_nop 0\n"
"ds_write_addtid_b32 v38 offset:26112\n"
"ds_write_addtid_b32 v39 offset:26368\n"
MF "v[128:131],a[56:59],v[180:183],v[128:131]\n"
"s_nop 0\n"
"ds_write_addtid_b32 v40 offset:26624\n"
"ds_write_addtid_b32 v41 offset:26880\n"
MF "v[128:131],a[96:99],v[184:187],v[128:131]\n"
"s_waitcnt lgkmcnt(10)\n"
LD "v198,s[48:51],s80 offen offset:2048 lds\n"
MF "v[132:135],a[96:99],a[244:247],v[252:255]\n"
"s_nop 0\n"
TR "v[176:177],v2 offset:24576\n"
"ds_write_addtid_b32 v42 offset:27136\n"
MF "v[132:135],a[56:59],a[240:243],v[132:135]\n"
"s_nop 0\n"
TR "v[178:179],v2 offset:26624\n"
"ds_write_addtid_b32 v43 offset:27392\n"
MF "v[132:135],a[16:19],v[188:191],v[132:135]\n"
"s_nop 0\n"
"ds_write_addtid_b32 v44 offset:27648\n"
"ds_write_addtid_b32 v45 offset:27904\n"
MF "v[124:127],a[12:15],v[188:191],v[252:255]\n"
"s_nop 0\n"
TR "v[180:181],v2 offset:28672\n"
"ds_write_addtid_b32 v46 offset:28160\n"
MF "v[124:127],a[52:55],a[240:243],v[124:127]\n"
"s_nop 0\n"
TR "v[182:183],v2 offset:30720\n"
"ds_write_addtid_b32 v47 offset:28416\n"
MF "v[124:127],a[92:95],a[244:247],v[124:127]\n"
LD "v199,s[48:51],s80 offen offset:3072 lds\n"
MF "v[116:119],a[88:91],a[244:247],v[252:255]\n"
"s_nop 0\n"
TR "v[184:185],v2 offset:32768\n"
"ds_write_addtid_b32 v48 offset:28672\n"
MF "v[116:119],a[48:51],a[240:243],v[116:119]\n"
"s_add_u32 m0,s79,106496\n"
TR "v[186:187],v2 offset:34816\n"
"ds_write_addtid_b32 v49 offset:24832\n"
MF "v[116:119],a[8:11],v[188:191],v[116:119]\n"
LD "v200,s[48:51],s80 offen lds\n"
MF "v[108:111],a[4:7],v[188:191],v[252:255]\n"
"s_nop 0\n"
"ds_write_addtid_b32 v50 offset:25088\n"
"ds_write_addtid_b32 v51 offset:25344\n"
MF "v[108:111],a[44:47],a[240:243],v[108:111]\n"
"s_nop 0\n"
"ds_write_addtid_b32 v52 offset:25600\n"
"ds_write_addtid_b32 v53 offset:25856\n"
MF "v[108:111],a[84:87],a[244:247],v[108:111]\n"
"s_nop 0\n"
"ds_write_addtid_b32 v54 offset:26112\n"
"ds_write_addtid_b32 v55 offset:26368\n"
MF "v[100:103],a[80:83],a[244:247],v[252:255]\n"
MF "v[100:103],a[40:43],a[240:243],v[100:103]\n"
LD "v201,s[48:51],s80 offen offset:1024 lds\n"
MF "v[100:103],a[0:3],v[188:191],v[100:103]\n"
"s_waitcnt lgkmcnt(7)\n"
MF "v[136:139],a[0:3],v[176:179],v[252:255]\n"
TR "v[188:189],v3 offset:24576\n"
MF "v[136:139],a[40:43],v[180:183],v[136:139]\n"
TR "v[190:191],v3 offset:26624\n"
MF "v[136:139],a[80:83],v[184:187],v[136:139]\n"
LD "v202,s[48:51],s80 offen offset:2048 lds\n"
MF "v[144:147],a[84:87],v[184:187],v[252:255]\n"
TR "a[240:241],v3 offset:28672\n"
MF "v[144:147],a[44:47],v[180:183],v[144:147]\n"
TR "a[242:243],v3 offset:30720\n"
MF "v[144:147],a[4:7],v[176:179],v[144:147]\n"
LD "v203,s[48:51],s80 offen offset:3072 lds\n"
MF "v[152:155],a[8:11],v[176:179],v[252:255]\n"
TR "a[244:245],v3 offset:32768\n"
MF "v[152:155],a[48:51],v[180:183],v[152:155]\n"
TR "a[246:247],v3 offset:34816\n"
MF "v[152:155],a[88:91],v[184:187],v[152:155]\n"
MF "v[160:163],a[92:95],v[184:187],v[252:255]\n"
MF "v[160:163],a[52:55],v[180:183],v[160:163]\n"
MF "v[160:163],a[12:15],v[176:179],v[160:163]\n"
MF "v[168:171],a[16:19],v[176:179],v[252:255]\n"
MF "v[168:171],a[56:59],v[180:183],v[168:171]\n"
"s_waitcnt vmcnt(24) lgkmcnt(0)\n"
MF "v[168:171],a[96:99],v[184:187],v[168:171]\n"
"s_barrier\n"
MF "v[172:175],a[96:99],a[244:247],v[252:255]\n"
"s_add_u32 s89,s89,s65\n"
TR "v[176:177],v0\n"
MF "v[172:175],a[56:59],a[240:243],v[172:175]\n"
"s_add_u32 m0,s79,24576\n"
TR "v[178:179],v0 offset:2048\n"
MF "v[172:175],a[16:19],v[188:191],v[172:175]\n"
LD "v192,s[48:51],s89 offen lds\n"
MF "v[164:167],a[12:15],v[188:191],v[252:255]\n"
TR "v[180:181],v0 offset:4096\n"
MF "v[164:167],a[52:55],a[240:243],v[164:167]\n"
TR "v[182:183],v0 offset:6144\n"
MF "v[164:167],a[92:95],a[244:247],v[164:167]\n"
LD "v193,s[48:51],s89 offen offset:1024 lds\n"
MF "v[156:159],a[88:91],a[244:247],v[252:255]\n"
TR "v[184:185],v0 offset:8192\n"
MF "v[156:159],a[48:51],a[240:243],v[156:159]\n"
"s_nop 0\n"
TR "v[186:187],v0 offset:10240\n"
"ds_write_addtid_b32 v56 offset:49152\n"
MF "v[156:159],a[8:11],v[188:191],v[156:159]\n"
"s_nop 0\n"
"ds_write_addtid_b32 v57 offset:49408\n"
"ds_write_addtid_b32 v58 offset:49664\n"
MF "v[148:151],a[4:7],v[188:191],v[252:255]\n"
"s_nop 0\n"
"ds_write_addtid_b32 v59 offset:49920\n"
"ds_write_addtid_b32 v60 offset:50176\n"
MF "v[148:151],a[44:47],a[240:243],v[148:151]\n"
LD "v194,s[48:51],s89 offen offset:2048 lds\n"
MF "v[148:151],a[84:87],a[244:247],v[148:151]\n"
"s_nop 0\n"
"ds_write_addtid_b32 v61 offset:50432\n"
"ds_write_addtid_b32 v62 offset:50688\n"
MF "v[140:143],a[80:83],a[244:247],v[252:255]\n"
"s_nop 0\n"
"ds_write_addtid_b32 v63 offset:50944\n"
"ds_write_addtid_b32 v64 offset:51200\n"
MF "v[140:143],a[40:43],a[240:243],v[140:143]\n"
"s_nop 0\n"
"ds_write_addtid_b32 v65 offset:51456\n"
"ds_write_addtid_b32 v66 offset:51712\n"
MF "v[140:143],a[0:3],v[188:191],v[140:143]\n"
"s_waitcnt lgkmcnt(11)\n"
LD "v195,s[48:51],s89 offen offset:3072 lds\n"
MF "v[96:99],a[120:123],v[176:179],v[96:99]\n"
"s_nop 0\n"
TR "v[188:189],v1\n"
"ds_write_addtid_b32 v67 offset:51968\n"
MF "v[96:99],a[160:163],v[180:183],v[96:99]\n"
"s_nop 0\n"
TR "v[190:191],v1 offset:2048\n"
"ds_write_addtid_b32 v68 offset:52224\n"
MF "v[96:99],a[200:203],v[184:187],v[96:99]\n"
"s_nop 0\n"
"ds_write_addtid_b32 v69 offset:52480\n"
"ds_write_addtid_b32 v70 offset:52736\n"
MF "v[104:107],a[204:207],v[184:187],v[104:107]\n"
"s_nop 0\n"
TR "a[240:241],v1 offset:4096\n"
"ds_write_addtid_b32 v71 offset:52992\n"
MF "v[104:107],a[164:167],v[180:183],v[104:107]\n"
"s_add_u32 m0,s79,28672\n"
TR "a[242:243],v1 offset:6144\n"
"ds_write_addtid_b32 v72 offset:49152\n"
MF "v[104:107],a[124:127],v[176:179],v[104:107]\n"
LD "v196,s[48:51],s89 offen lds\n"
MF "v[112:115],a[128:131],v[176:179],v[112:115]\n"
"s_nop 0\n"
TR "a[244:245],v1 offset:8192\n"
"ds_write_addtid_b32 v73 offset:49408\n"
MF "v[112:115],a[168:171],v[180:183],v[112:115]\n"
"s_nop 0\n"
TR "a[246:247],v1 offset:10240\n"
"ds_write_addtid_b32 v74 offset:49664\n"
MF "v[112:115],a[208:211],v[184:187],v[112:115]\n"
".Lpg26h:\n"
"s_cbranch_execz .Lpg30h\n"
LD "v197,s[48:51],s89 offen offset:1024 lds\n"
MF "v[120:123],a[212:215],v[184:187],v[120:123]\n"
"s_nop 0\n"
"ds_write_addtid_b32 v75 offset:49920\n"
"ds_write_addtid_b32 v76 offset:50176\n"
MF "v[120:123],a[172:175],v[180:183],v[120:123]\n"
"s_nop 0\n"
"ds_write_addtid_b32 v77 offset:50432\n"
"ds_write_addtid_b32 v78 offset:50688\n"
MF "v[120:123],a[132:135],v[176:179],v[120:123]\n"
"s_nop 0\n"
"ds_write_addtid_b32 v79 offset:50944\n"
"ds_write_addtid_b32 v80 offset:51200\n"
MF "v[128:131],a[136:139],v[176:179],v[128:131]\n"
"s_nop 0\n"
"ds_write_addtid_b32 v81 offset:51456\n"
"ds_write_addtid_b32 v82 offset:51712\n"
MF "v[128:131],a[176:179],v[180:183],v[128:131]\n"
LD "v198,s[48:51],s89 offen offset:2048 lds\n"
MF "v[128:131],a[216:219],v[184:187],v[128:131]\n"
"s_waitcnt lgkmcnt(9)\n"
"ds_write_addtid_b32 v83 offset:51968\n"
"ds_write_addtid_b32 v84 offset:52224\n"
MF "v[132:135],a[216:219],a[244:247],v[132:135]\n"
"s_nop 0\n"
TR "v[176:177],v2\n"
"ds_write_addtid_b32 v85 offset:52480\n"
MF "v[132:135],a[176:179],a[240:243],v[132:135]\n"
"s_nop 0\n"
TR "v[178:179],v2 offset:2048\n"
"ds_write_addtid_b32 v86 offset:52736\n"
MF "v[132:135],a[136:139],v[188:191],v[132:135]\n"
LD "v199,s[48:51],s89 offen offset:3072 lds\n"
MF "v[124:127],a[132:135],v[188:191],v[124:127]\n"
"s_nop 0\n"
TR "v[180:181],v2 offset:4096\n"
"ds_write_addtid_b32 v87 offset:52992\n"
MF "v[124:127],a[172:175],a[240:243],v[124:127]\n"
"s_nop 0\n"
TR "v[182:183],v2 offset:6144\n"
"ds_write_addtid_b32 v88 offset:53248\n"
MF "v[124:127],a[212:215],a[244:247],v[124:127]\n"
"s_nop 0\n"
"ds_write_addtid_b32 v89 offset:53504\n"
"ds_write_addtid_b32 v90 offset:53760\n"
MF "v[116:119],a[208:211],a[244:247],v[116:119]\n"
"s_nop 0\n"
TR "v[184:185],v2 offset:8192\n"
"ds_write_addtid_b32 v91 offset:54016\n"
MF "v[116:119],a[168:171],a[240:243],v[116:119]\n"
"s_add_u32 m0,s79,32768\n"
TR "v[186:187],v2 offset:10240\n"
"ds_write_addtid_b32 v92 offset:50176\n"
MF "v[116:119],a[128:131],v[188:191],v[116:119]\n"
LD "v200,s[48:51],s89 offen lds\n"
MF "v[108:111],a[124:127],v[188:191],v[108:111]\n"
"s_nop 0\n"
"ds_write_addtid_b32 v93 offset:50432\n"
"ds_write_addtid_b32 v94 offset:50688\n"
MF "v[108:111],a[164:167],a[240:243],v[108:111]\n"
"ds_write_addtid_b32 v95 offset:50944\n"
MF "v[108:111],a[204:207],a[244:247],v[108:111]\n"
MF "v[100:103],a[200:203],a[244:247],v[100:103]\n"
LD "v201,s[48:51],s89 offen offset:1024 lds\n"
MF "v[100:103],a[160:163],a[240:243],v[100:103]\n"
MF "v[100:103],a[120:123],v[188:191],v[100:103]\n"
"s_waitcnt lgkmcnt(4)\n"
MF "v[136:139],a[120:123],v[176:179],v[136:139]\n"
TR "v[188:189],v3\n"
MF "v[136:139],a[160:163],v[180:183],v[136:139]\n"
TR "v[190:191],v3 offset:2048\n"
MF "v[136:139],a[200:203],v[184:187],v[136:139]\n"
LD "v202,s[48:51],s89 offen offset:2048 lds\n"
MF "v[144:147],a[204:207],v[184:187],v[144:147]\n"
TR "a[240:241],v3 offset:4096\n"
MF "v[144:147],a[164:167],v[180:183],v[144:147]\n"
TR "a[242:243],v3 offset:6144\n"
MF "v[144:147],a[124:127],v[176:179],v[144:147]\n"
LD "v203,s[48:51],s89 offen offset:3072 lds\n"
MF "v[152:155],a[128:131],v[176:179],v[152:155]\n"
TR "a[244:245],v3 offset:8192\n"
MF "v[152:155],a[168:171],v[180:183],v[152:155]\n"
TR "a[246:247],v3 offset:10240\n"
MF "v[152:155],a[208:211],v[184:187],v[152:155]\n"
MF "v[160:163],a[212:215],v[184:187],v[160:163]\n"
MF "v[160:163],a[172:175],v[180:183],v[160:163]\n"
MF "v[160:163],a[132:135],v[176:179],v[160:163]\n"
MF "v[168:171],a[136:139],v[176:179],v[168:171]\n"
MF "v[168:171],a[176:179],v[180:183],v[168:171]\n"
"s_waitcnt vmcnt(24) lgkmcnt(0)\n"
MF "v[168:171],a[216:219],v[184:187],v[168:171]\n"
"s_barrier\n"
MF "v[172:175],a[216:219],a[244:247],v[172:175]\n"
"s_add_u32 s80,s89,122880\n"
TR "v[176:177],v0 offset:49152\n"
MF "v[172:175],a[176:179],a[240:243],v[172:175]\n"
"s_add_u32 m0,s79,0\n"
TR "v[178:179],v0 offset:51200\n"
MF "v[172:175],a[136:139],v[188:191],v[172:175]\n"
LD "v192,s[48:51],s80 offen lds\n"
MF "v[164:167],a[132:135],v[188:191],v[164:167]\n"
TR "v[180:181],v0 offset:53248\n"
MF "v[164:167],a[172:175],a[240:243],v[164:167]\n"
TR "v[182:183],v0 offset:55296\n"
MF "v[164:167],a[212:215],a[244:247],v[164:167]\n"
LD "v193,s[48:51],s80 offen offset:1024 lds\n"
MF "v[156:159],a[208:211],a[244:247],v[156:159]\n"
TR "v[184:185],v0 offset:57344\n"
MF "v[156:159],a[168:171],a[240:243],v[156:159]\n"
TR "v[186:187],v0 offset:59392\n"
MF "v[156:159],a[128:131],v[188:191],v[156:159]\n"
MF "v[148:151],a[124:127],v[188:191],v[148:151]\n"
MF "v[148:151],a[164:167],a[240:243],v[148:151]\n"
LD "v194,s[48:51],s80 offen offset:2048 lds\n"
MF "v[148:151],a[204:207],a[244:247],v[148:151]\n"
MF "v[140:143],a[200:203],a[244:247],v[140:143]\n"
MF "v[140:143],a[160:163],a[240:243],v[140:143]\n"
MF "v[140:143],a[120:123],v[188:191],v[140:143]\n"
"s_waitcnt lgkmcnt(0)\n"
LD "v195,s[48:51],s80 offen offset:3072 lds\n"
MF "v[96:99],a[20:23],v[176:179],v[96:99]\n"
TR "v[188:189],v1 offset:49152\n"
MF "v[96:99],a[60:63],v[180:183],v[96:99]\n"
TR "v[190:191],v1 offset:51200\n"
MF "v[96:99],a[100:103],v[184:187],v[96:99]\n"
MF "v[104:107],a[104:107],v[184:187],v[104:107]\n"
TR "a[240:241],v1 offset:53248\n"
MF "v[104:107],a[64:67],v[180:183],v[104:107]\n"
"s_add_u32 m0,s79,4096\n"
TR "a[242:243],v1 offset:55296\n"
MF "v[104:107],a[24:27],v[176:179],v[104:107]\n"
LD "v196,s[48:51],s80 offen lds\n"
MF "v[112:115],a[28:31],v[176:179],v[112:115]\n"
TR "a[244:245],v1 offset:57344\n"
MF "v[112:115],a[68:71],v[180:183],v[112:115]\n"
TR "a[246:247],v1 offset:59392\n"
MF "v[112:115],a[108:111],v[184:187],v[112:115]\n"
LD "v197,s[48:51],s80 offen offset:1024 lds\n"
MF "v[120:123],a[112:115],v[184:187],v[120:123]\n"
MF "v[120:123],a[72:75],v[180:183],v[120:123]\n"
MF "v[120:123],a[32:35],v[176:179],v[120:123]\n"
MF "v[128:131],a[36:39],v[176:179],v[128:131]\n"
MF "v[128:131],a[76:79],v[180:183],v[128:131]\n"
LD "v198,s[48:51],s80 offen offset:2048 lds\n"
MF "v[128:131],a[116:119],v[184:187],v[128:131]\n"
"s_waitcnt lgkmcnt(0)\n"
MF "v[132:135],a[116:119],a[244:247],v[132:135]\n"
TR "v[176:177],v2 offset:49152\n"
MF "v[132:135],a[76:79],a[240:243],v[132:135]\n"
TR "v[178:179],v2 offset:51200\n"
MF "v[132:135],a[36:39],v[188:191],v[132:135]\n"
LD "v199,s[48:51],s80 offen offset:3072 lds\n"
MF "v[124:127],a[32:35],v[188:191],v[124:127]\n"
TR "v[180:181],v2 offset:53248\n"
MF "v[124:127],a[72:75],a[240:243],v[124:127]\n"
TR "v[182:183],v2 offset:55296\n"
MF "v[124:127],a[112:115],a[244:247],v[124:127]\n"
MF "v[116:119],a[108:111],a[244:247],v[116:119]\n"
TR "v[184:185],v2 offset:57344\n"
MF "v[116:119],a[68:71],a[240:243],v[116:119]\n"
"s_add_u32 m0,s79,8192\n"
TR "v[186:187],v2 offset:59392\n"
MF "v[116:119],a[28:31],v[188:191],v[116:119]\n"
LD "v200,s[48:51],s80 offen lds\n"
MF "v[108:111],a[24:27],v[188:191],v[108:111]\n"
MF "v[108:111],a[64:67],a[240:243],v[108:111]\n"
MF "v[108:111],a[104:107],a[244:247],v[108:111]\n"
MF "v[100:103],a[100:103],a[244:247],v[100:103]\n"
LD "v201,s[48:51],s80 offen offset:1024 lds\n"
MF "v[100:103],a[60:63],a[240:243],v[100:103]\n"
MF "v[100:103],a[20:23],v[188:191],v[100:103]\n"
"s_waitcnt lgkmcnt(0)\n"
MF "v[136:139],a[20:23],v[176:179],v[136:139]\n"
TR "v[188:189],v3 offset:49152\n"
MF "v[136:139],a[60:63],v[180:183],v[136:139]\n"
TR "v[190:191],v3 offset:51200\n"
MF "v[136:139],a[100:103],v[184:187],v[136:139]\n"
LD "v202,s[48:51],s80 offen offset:2048 lds\n"
MF "v[144:147],a[104:107],v[184:187],v[144:147]\n"
TR "a[240:241],v3 offset:53248\n"
MF "v[144:147],a[64:67],v[180:183],v[144:147]\n"
TR "a[242:243],v3 offset:55296\n"
MF "v[144:147],a[24:27],v[176:179],v[144:147]\n"
LD "v203,s[48:51],s80 offen offset:3072 lds\n"
MF "v[152:155],a[28:31],v[176:179],v[152:155]\n"
TR "a[244:245],v3 offset:57344\n"
MF "v[152:155],a[68:71],v[180:183],v[152:155]\n"
TR "a[246:247],v3 offset:59392\n"
MF "v[152:155],a[108:111],v[184:187],v[152:155]\n"
MF "v[160:163],a[112:115],v[184:187],v[160:163]\n"
MF "v[160:163],a[72:75],v[180:183],v[160:163]\n"
MF "v[160:163],a[32:35],v[176:179],v[160:163]\n"
MF "v[168:171],a[36:39],v[176:179],v[168:171]\n"
MF "v[168:171],a[76:79],v[180:183],v[168:171]\n"
"s_waitcnt vmcnt(24) lgkmcnt(0)\n"
MF "v[168:171],a[116:119],v[184:187],v[168:171]\n"
"s_barrier\n"
MF "v[172:175],a[116:119],a[244:247],v[172:175]\n"
"s_add_u32 s88,s88,s65\n"
TR "v[176:177],v4\n"
MF "v[172:175],a[76:79],a[240:243],v[172:175]\n"
"s_add_u32 m0,s79,49152\n"
TR "v[178:179],v4 offset:2048\n"
MF "v[172:175],a[36:39],v[188:191],v[172:175]\n"
LD "v192,s[48:51],s88 offen lds\n"
MF "v[164:167],a[32:35],v[188:191],v[164:167]\n"
TR "v[180:181],v4 offset:4096\n"
MF "v[164:167],a[72:75],a[240:243],v[164:167]\n"
TR "v[182:183],v4 offset:6144\n"
MF "v[164:167],a[112:115],a[244:247],v[164:167]\n"
LD "v193,s[48:51],s88 offen offset:1024 lds\n"
MF "v[156:159],a[108:111],a[244:247],v[156:159]\n"
TR "v[184:185],v4 offset:8192\n"
MF "v[156:159],a[68:71],a[240:243],v[156:159]\n"
TR "v[186:187],v4 offset:10240\n"
MF "v[156:159],a[28:31],v[188:191],v[156:159]\n"
MF "v[148:151],a[24:27],v[188:191],v[148:151]\n"
MF "v[148:151],a[64:67],a[240:243],v[148:151]\n"
LD "v194,s[48:51],s88 offen offset:2048 lds\n"
MF "v[148:151],a[104:107],a[244:247],v[148:151]\n"
MF "v[140:143],a[100:103],a[244:247],v[140:143]\n"
MF "v[140:143],a[60:63],a[240:243],v[140:143]\n"
MF "v[140:143],a[20:23],v[188:191],v[140:143]\n"
"s_waitcnt lgkmcnt(0)\n"
LD "v195,s[48:51],s88 offen offset:3072 lds\n"
MF "v[96:99],a[140:143],v[176:179],v[96:99]\n"
TR "v[188:189],v5\n"
MF "v[96:99],a[180:183],v[180:183],v[96:99]\n"
TR "v[190:191],v5 offset:2048\n"
MF "v[96:99],a[220:223],v[184:187],v[96:99]\n"
MF "v[104:107],a[224:227],v[184:187],v[104:107]\n"
TR "a[240:241],v5 offset:4096\n"
MF "v[104:107],a[184:187],v[180:183],v[104:107]\n"
"s_add_u32 m0,s79,53248\n"
TR "a[242:243],v5 offset:6144\n"
MF "v[104:107],a[144:147],v[176:179],v[104:107]\n"
LD "v196,s[48:51],s88 offen lds\n"
MF "v[112:115],a[148:151],v[176:179],v[112:115]\n"
TR "a[244:245],v5 offset:8192\n"
MF "v[112:115],a[188:191],v[180:183],v[112:115]\n"
TR "a[246:247],v5 offset:10240\n"
MF "v[112:115],a[228:231],v[184:187],v[112:115]\n"
LD "v197,s[48:51],s88 offen offset:1024 lds\n"
MF "v[120:123],a[232:235],v[184:187],v[120:123]\n"
MF "v[120:123],a[192:195],v[180:183],v[120:123]\n"
MF "v[120:123],a[152:155],v[176:179],v[120:123]\n"
MF "v[128:131],a[156:159],v[176:179],v[128:131]\n"
MF "v[128:131],a[196:199],v[180:183],v[128:131]\n"
LD "v198,s[48:51],s88 offen offset:2048 lds\n"
MF "v[128:131],a[236:239],v[184:187],v[128:131]\n"
"s_waitcnt lgkmcnt(0)\n"
MF "v[132:135],a[236:239],a[244:247],v[132:135]\n"
TR "v[176:177],v6\n"
MF "v[132:135],a[196:199],a[240:243],v[132:135]\n"
TR "v[178:179],v6 offset:2048\n"
MF "v[132:135],a[156:159],v[188:191],v[132:135]\n"
LD "v199,s[48:51],s88 offen offset:3072 lds\n"
MF "v[124:127],a[152:155],v[188:191],v[124:127]\n"
TR "v[180:181],v6 offset:4096\n"
MF "v[124:127],a[192:195],a[240:243],v[124:127]\n"
TR "v[182:183],v6 offset:6144\n"
MF "v[124:127],a[232:235],a[244:247],v[124:127]\n"
MF "v[116:119],a[228:231],a[244:247],v[116:119]\n"
TR "v[184:185],v6 offset:8192\n"
MF "v[116:119],a[188:191],a[240:243],v[116:119]\n"
"s_add_u32 m0,s79,57344\n"
TR "v[186:187],v6 offset:10240\n"
MF "v[116:119],a[148:151],v[188:191],v[116:119]\n"
LD "v200,s[48:51],s88 offen lds\n"
MF "v[108:111],a[144:147],v[188:191],v[108:111]\n"
MF "v[108:111],a[184:187],a[240:243],v[108:111]\n"
MF "v[108:111],a[224:227],a[244:247],v[108:111]\n"
MF "v[100:103],a[220:223],a[244:247],v[100:103]\n"
LD "v201,s[48:51],s88 offen offset:1024 lds\n"
MF "v[100:103],a[180:183],a[240:243],v[100:103]\n"
MF "v[100:103],a[140:143],v[188:191],v[100:103]\n"
"s_waitcnt lgkmcnt(0)\n"
MF "v[136:139],a[140:143],v[176:179],v[136:139]\n"
TR "v[188:189],v7\n"
MF "v[136:139],a[180:183],v[180:183],v[136:139]\n"
TR "v[190:191],v7 offset:2048\n"
MF "v[136:139],a[220:223],v[184:187],v[136:139]\n"
LD "v202,s[48:51],s88 offen offset:2048 lds\n"
MF "v[144:147],a[224:227],v[184:187],v[144:147]\n"
TR "a[240:241],v7 offset:4096\n"
MF "v[144:147],a[184:187],v[180:183],v[144:147]\n"
TR "a[242:243],v7 offset:6144\n"
MF "v[144:147],a[144:147],v[176:179],v[144:147]\n"
LD "v203,s[48:51],s88 offen offset:3072 lds\n"
MF "v[152:155],a[148:151],v[176:179],v[152:155]\n"
TR "a[244:245],v7 offset:8192\n"
MF "v[152:155],a[188:191],v[180:183],v[152:155]\n"
TR "a[246:247],v7 offset:10240\n"
MF "v[152:155],a[228:231],v[184:187],v[152:155]\n"
MF "v[160:163],a[232:235],v[184:187],v[160:163]\n"
MF "v[160:163],a[192:195],v[180:183],v[160:163]\n"
MF "v[160:163],a[152:155],v[176:179],v[160:163]\n"
MF "v[168:171],a[156:159],v[176:179],v[168:171]\n"
MF "v[168:171],a[196:199],v[180:183],v[168:171]\n"
"s_waitcnt vmcnt(24) lgkmcnt(0)\n"
MF "v[168:171],a[236:239],v[184:187],v[168:171]\n"
"s_barrier\n"
MF "v[172:175],a[236:239],a[244:247],v[172:175]\n"
"s_add_u32 s80,s88,122880\n"
TR "v[176:177],v0 offset:24576\n"
MF "v[172:175],a[196:199],a[240:243],v[172:175]\n"
"s_add_u32 m0,s79,98304\n"
"s_add_u32 s85,s85,1\n"
TR "v[178:179],v0 offset:26624\n"
MF "v[172:175],a[156:159],v[188:191],v[172:175]\n"
LD "v192,s[48:51],s80 offen lds\n"
MF "v[164:167],a[152:155],v[188:191],v[164:167]\n"
"s_nop 0\n"
TR "v[180:181],v0 offset:28672\n"
"ds_write_addtid_b32 v96 offset:24576\n"
MF "v[164:167],a[192:195],a[240:243],v[164:167]\n"
"s_nop 0\n"
TR "v[182:183],v0 offset:30720\n"
"ds_write_addtid_b32 v97 offset:24832\n"
MF "v[164:167],a[232:235],a[244:247],v[164:167]\n"
LD "v193,s[48:51],s80 offen offset:1024 lds\n"
MF "v[156:159],a[228:231],a[244:247],v[156:159]\n"
"s_nop 0\n"
TR "v[184:185],v0 offset:32768\n"
"ds_write_addtid_b32 v98 offset:25088\n"
MF "v[156:159],a[188:191],a[240:243],v[156:159]\n"
"s_nop 0\n"
TR "v[186:187],v0 offset:34816\n"
"ds_write_addtid_b32 v99 offset:25344\n"
MF "v[156:159],a[148:151],v[188:191],v[156:159]\n"
"s_nop 0\n"
"ds_write_addtid_b32 v100 offset:25600\n"
"ds_write_addtid_b32 v101 offset:25856\n"
MF "v[148:151],a[144:147],v[188:191],v[148:151]\n"
LD "v194,s[48:51],s80 offen offset:2048 lds\n"
MF "v[148:151],a[184:187],a[240:243],v[148:151]\n"
"s_nop 0\n"
"ds_write_addtid_b32 v102 offset:26112\n"
"ds_write_addtid_b32 v103 offset:26368\n"
MF "v[148:151],a[224:227],a[244:247],v[148:151]\n"
"s_nop 0\n"
"ds_write_addtid_b32 v104 offset:26624\n"
"ds_write_addtid_b32 v105 offset:26880\n"
MF "v[140:143],a[220:223],a[244:247],v[140:143]\n"
"s_nop 0\n"
"ds_write_addtid_b32 v106 offset:27136\n"
"ds_write_addtid_b32 v107 offset:27392\n"
MF "v[140:143],a[180:183],a[240:243],v[140:143]\n"
MF "v[140:143],a[140:143],v[188:191],v[140:143]\n"
"s_waitcnt lgkmcnt(9)\n"
MF "v[16:19],a[0:3],v[176:179],v[252:255]\n"
"s_nop 0\n"
TR "v[188:189],v1 offset:24576\n"
"ds_write_addtid_b32 v108 offset:27648\n"
MF "v[16:19],a[40:43],v[180:183],v[16:19]\n"
"s_nop 0\n"
TR "v[190:191],v1 offset:26624\n"
"ds_write_addtid_b32 v109 offset:27904\n"
MF "v[16:19],a[80:83],v[184:187],v[16:19]\n"
LD "v195,s[48:51],s80 offen offset:3072 lds\n"
MF "v[24:27],a[84:87],v[184:187],v[252:255]\n"
"s_add_u32 s60,s64,1\n"
TR "a[240:241],v1 offset:28672\n"
"ds_write_addtid_b32 v110 offset:28160\n"
MF "v[24:27],a[44:47],v[180:183],v[24:27]\n"
"s_add_u32 m0,s79,102400\n"
TR "a[242:243],v1 offset:30720\n"
"ds_write_addtid_b32 v111 offset:24320\n"
MF "v[24:27],a[4:7],v[176:179],v[24:27]\n"
LD "v196,s[48:51],s80 offen lds\n"
MF "v[32:35],a[8:11],v[176:179],v[252:255]\n"
"s_cmp_eq_u32 s60,8\n"
"s_cselect_b32 s60,0,s60\n"
TR "a[244:245],v1 offset:32768\n"
MF "v[32:35],a[48:51],v[180:183],v[32:35]\n"
"s_cmp_eq_u32 s60,10\n"
"s_cselect_b32 s60,s61,s60\n"
TR "a[246:247],v1 offset:34816\n"
MF "v[32:35],a[88:91],v[184:187],v[32:35]\n"
"s_sub_u32 s65,s60,s64\n"
"ds_write_addtid_b32 v112 offset:24576\n"
"ds_write_addtid_b32 v113 offset:24832\n"
MF "v[40:43],a[92:95],v[184:187],v[252:255]\n"
"s_lshl_b32 s65,s65,7\n"
"ds_write_addtid_b32 v114 offset:25088\n"
"ds_write_addtid_b32 v115 offset:25344\n"
MF "v[40:43],a[52:55],v[180:183],v[40:43]\n"
LD "v197,s[48:51],s80 offen offset:1024 lds\n"
MF "v[40:43],a[12:15],v[176:179],v[40:43]\n"
"s_mov_b32 s64,s60\n"
"ds_write_addtid_b32 v116 offset:25600\n"
"ds_write_addtid_b32 v117 offset:25856\n"
MF "v[48:51],a[16:19],v[176:179],v[252:255]\n"
"s_nop 0\n"
"ds_write_addtid_b32 v118 offset:26112\n"
"ds_write_addtid_b32 v119 offset:26368\n"
MF "v[48:51],a[56:59],v[180:183],v[48:51]\n"
"s_nop 0\n"
"ds_write_addtid_b32 v120 offset:26624\n"
"ds_write_addtid_b32 v121 offset:26880\n"
MF "v[48:51],a[96:99],v[184:187],v[48:51]\n"
"s_waitcnt lgkmcnt(10)\n"
LD "v198,s[48:51],s80 offen offset:2048 lds\n"
MF "v[52:55],a[96:99],a[244:247],v[252:255]\n"
"s_nop 0\n"
TR "v[176:177],v2 offset:24576\n"
"ds_write_addtid_b32 v122 offset:27136\n"
MF "v[52:55],a[56:59],a[240:243],v[52:55]\n"
"s_nop 0\n"
TR "v[178:179],v2 offset:26624\n"
"ds_write_addtid_b32 v123 offset:27392\n"
MF "v[52:55],a[16:19],v[188:191],v[52:55]\n"
"s_nop 0\n"
"ds_write_addtid_b32 v124 offset:27648\n"
"ds_write_addtid_b32 v125 offset:27904\n"
MF "v[44:47],a[12:15],v[188:191],v[252:255]\n"
"s_nop 0\n"
TR "v[180:181],v2 offset:28672\n"
"ds_write_addtid_b32 v126 offset:28160\n"
MF "v[44:47],a[52:55],a[240:243],v[44:47]\n"
"s_nop 0\n"
TR "v[182:183],v2 offset:30720\n"
"ds_write_addtid_b32 v127 offset:28416\n"
MF "v[44:47],a[92:95],a[244:247],v[44:47]\n"
LD "v199,s[48:51],s80 offen offset:3072 lds\n"
MF "v[36:39],a[88:91],a[244:247],v[252:255]\n"
"s_nop 0\n"
TR "v[184:185],v2 offset:32768\n"
"ds_write_addtid_b32 v128 offset:28672\n"
MF "v[36:39],a[48:51],a[240:243],v[36:39]\n"
"s_add_u32 m0,s79,106496\n"
TR "v[186:187],v2 offset:34816\n"
"ds_write_addtid_b32 v129 offset:24832\n"
MF "v[36:39],a[8:11],v[188:191],v[36:39]\n"
LD "v200,s[48:51],s80 offen lds\n"
MF "v[28:31],a[4:7],v[188:191],v[252:255]\n"
"s_nop 0\n"
"ds_write_addtid_b32 v130 offset:25088\n"
"ds_write_addtid_b32 v131 offset:25344\n"
MF "v[28:31],a[44:47],a[240:243],v[28:31]\n"
"s_nop 0\n"
"ds_write_addtid_b32 v132 offset:25600\n"
"ds_write_addtid_b32 v133 offset:25856\n"
MF "v[28:31],a[84:87],a[244:247],v[28:31]\n"
"s_nop 0\n"
"ds_write_addtid_b32 v134 offset:26112\n"
"ds_write_addtid_b32 v135 offset:26368\n"
MF "v[20:23],a[80:83],a[244:247],v[252:255]\n"
MF "v[20:23],a[40:43],a[240:243],v[20:23]\n"
LD "v201,s[48:51],s80 offen offset:1024 lds\n"
MF "v[20:23],a[0:3],v[188:191],v[20:23]\n"
"s_waitcnt lgkmcnt(7)\n"
MF "v[56:59],a[0:3],v[176:179],v[252:255]\n"
TR "v[188:189],v3 offset:24576\n"
MF "v[56:59],a[40:43],v[180:183],v[56:59]\n"
TR "v[190:191],v3 offset:26624\n"
MF "v[56:59],a[80:83],v[184:187],v[56:59]\n"
LD "v202,s[48:51],s80 offen offset:2048 lds\n"
MF "v[64:67],a[84:87],v[184:187],v[252:255]\n"
TR "a[240:241],v3 offset:28672\n"
MF "v[64:67],a[44:47],v[180:183],v[64:67]\n"
TR "a[242:243],v3 offset:30720\n"
MF "v[64:67],a[4:7],v[176:179],v[64:67]\n"
LD "v203,s[48:51],s80 offen offset:3072 lds\n"
MF "v[72:75],a[8:11],v[176:179],v[252:255]\n"
TR "a[244:245],v3 offset:32768\n"
MF "v[72:75],a[48:51],v[180:183],v[72:75]\n"
TR "a[246:247],v3 offset:34816\n"
MF "v[72:75],a[88:91],v[184:187],v[72:75]\n"
MF "v[80:83],a[92:95],v[184:187],v[252:255]\n"
MF "v[80:83],a[52:55],v[180:183],v[80:83]\n"
MF "v[80:83],a[12:15],v[176:179],v[80:83]\n"
MF "v[88:91],a[16:19],v[176:179],v[252:255]\n"
MF "v[88:91],a[56:59],v[180:183],v[88:91]\n"
"s_waitcnt vmcnt(24) lgkmcnt(0)\n"
MF "v[88:91],a[96:99],v[184:187],v[88:91]\n"
"s_barrier\n"
MF "v[92:95],a[96:99],a[244:247],v[252:255]\n"
"s_add_u32 s89,s89,s65\n"
TR "v[176:177],v0\n"
MF "v[92:95],a[56:59],a[240:243],v[92:95]\n"
"s_add_u32 m0,s79,24576\n"
TR "v[178:179],v0 offset:2048\n"
MF "v[92:95],a[16:19],v[188:191],v[92:95]\n"
LD "v192,s[48:51],s89 offen lds\n"
MF "v[84:87],a[12:15],v[188:191],v[252:255]\n"
TR "v[180:181],v0 offset:4096\n"
MF "v[84:87],a[52:55],a[240:243],v[84:87]\n"
TR "v[182:183],v0 offset:6144\n"
MF "v[84:87],a[92:95],a[244:247],v[84:87]\n"
LD "v193,s[48:51],s89 offen offset:1024 lds\n"
MF "v[76:79],a[88:91],a[244:247],v[252:255]\n"
TR "v[184:185],v0 offset:8192\n"
MF "v[76:79],a[48:51],a[240:243],v[76:79]\n"
"s_nop 0\n"
TR "v[186:187],v0 offset:10240\n"
"ds_write_addtid_b32 v136 offset:49152\n"
MF "v[76:79],a[8:11],v[188:191],v[76:79]\n"
"s_nop 0\n"
"ds_write_addtid_b32 v137 offset:49408\n"
"ds_write_addtid_b32 v138 offset:49664\n"
MF "v[68:71],a[4:7],v[188:191],v[252:255]\n"
"s_nop 0\n"
"ds_write_addtid_b32 v139 offset:49920\n"
"ds_write_addtid_b32 v140 offset:50176\n"
MF "v[68:71],a[44:47],a[240:243],v[68:71]\n"
LD "v194,s[48:51],s89 offen offset:2048 lds\n"
MF "v[68:71],a[84:87],a[244:247],v[68:71]\n"
"s_nop 0\n"
"ds_write_addtid_b32 v141 offset:50432\n"
"ds_write_addtid_b32 v142 offset:50688\n"
MF "v[60:63],a[80:83],a[244:247],v[252:255]\n"
"s_nop 0\n"
"ds_write_addtid_b32 v143 offset:50944\n"
"ds_write_addtid_b32 v144 offset:51200\n"
MF "v[60:63],a[40:43],a[240:243],v[60:63]\n"
"s_nop 0\n"
"ds_write_addtid_b32 v145 offset:51456\n"
"ds_write_addtid_b32 v146 offset:51712\n"
MF "v[60:63],a[0:3],v[188:191],v[60:63]\n"
"s_waitcnt lgkmcnt(11)\n"
LD "v195,s[48:51],s89 offen offset:3072 lds\n"
MF "v[16:19],a[120:123],v[176:179],v[16:19]\n"
"s_nop 0\n"
TR "v[188:189],v1\n"
"ds_write_addtid_b32 v147 offset:51968\n"
MF "v[16:19],a[160:163],v[180:183],v[16:19]\n"
"s_nop 0\n"
TR "v[190:191],v1 offset:2048\n"
"ds_write_addtid_b32 v148 offset:52224\n"
MF "v[16:19],a[200:203],v[184:187],v[16:19]\n"
"s_nop 0\n"
"ds_write_addtid_b32 v149 offset:52480\n"
"ds_write_addtid_b32 v150 offset:52736\n"
MF "v[24:27],a[204:207],v[184:187],v[24:27]\n"
"s_nop 0\n"
TR "a[240:241],v1 offset:4096\n"
"ds_write_addtid_b32 v151 offset:52992\n"
MF "v[24:27],a[164:167],v[180:183],v[24:27]\n"
"s_add_u32 m0,s79,28672\n"
TR "a[242:243],v1 offset:6144\n"
"ds_write_addtid_b32 v152 offset:49152\n"
MF "v[24:27],a[124:127],v[176:179],v[24:27]\n"
LD "v196,s[48:51],s89 offen lds\n"
MF "v[32:35],a[128:131],v[176:179],v[32:35]\n"
"s_nop 0\n"
TR "a[244:245],v1 offset:8192\n"
"ds_write_addtid_b32 v153 offset:49408\n"
MF "v[32:35],a[168:171],v[180:183],v[32:35]\n"
".Lpg27h:\n"
"s_cbranch_execz .Lpg31h\n"
"s_nop 0\n"
TR "a[246:247],v1 offset:10240\n"
"ds_write_addtid_b32 v154 offset:49664\n"
MF "v[32:35],a[208:211],v[184:187],v[32:35]\n"
LD "v197,s[48:51],s89 offen offset:1024 lds\n"
MF "v[40:43],a[212:215],v[184:187],v[40:43]\n"
"s_nop 0\n"
"ds_write_addtid_b32 v155 offset:49920\n"
"ds_write_addtid_b32 v156 offset:50176\n"
MF "v[40:43],a[172:175],v[180:183],v[40:43]\n"
"s_nop 0\n"
"ds_write_addtid_b32 v157 offset:50432\n"
"ds_write_addtid_b32 v158 offset:50688\n"
MF "v[40:43],a[132:135],v[176:179],v[40:43]\n"
"s_nop 0\n"
"ds_write_addtid_b32 v159 offset:50944\n"
"ds_write_addtid_b32 v160 offset:51200\n"
MF "v[48:51],a[136:139],v[176:179],v[48:51]\n"
"s_nop 0\n"
"ds_write_addtid_b32 v161 offset:51456\n"
"ds_write_addtid_b32 v162 offset:51712\n"
MF "v[48:51],a[176:179],v[180:183],v[48:51]\n"
LD "v198,s[48:51],s89 offen offset:2048 lds\n"
MF "v[48:51],a[216:219],v[184:187],v[48:51]\n"
"s_waitcnt lgkmcnt(9)\n"
"ds_write_addtid_b32 v163 offset:51968\n"
"ds_write_addtid_b32 v164 offset:52224\n"
MF "v[52:55],a[216:219],a[244:247],v[52:55]\n"
"s_nop 0\n"
TR "v[176:177],v2\n"
"ds_write_addtid_b32 v165 offset:52480\n"
MF "v[52:55],a[176:179],a[240:243],v[52:55]\n"
"s_nop 0\n"
TR "v[178:179],v2 offset:2048\n"
"ds_write_addtid_b32 v166 offset:52736\n"
MF "v[52:55],a[136:139],v[188:191],v[52:55]\n"
LD "v199,s[48:51],s89 offen offset:3072 lds\n"
MF "v[44:47],a[132:135],v[188:191],v[44:47]\n"
"s_nop 0\n"
TR "v[180:181],v2 offset:4096\n"
"ds_write_addtid_b32 v167 offset:52992\n"
MF "v[44:47],a[172:175],a[240:243],v[44:47]\n"
"s_nop 0\n"
TR "v[182:183],v2 offset:6144\n"
"ds_write_addtid_b32 v168 offset:53248\n"
MF "v[44:47],a[212:215],a[244:247],v[44:47]\n"
"s_nop 0\n"
"ds_write_addtid_b32 v169 offset:53504\n"
"ds_write_addtid_b32 v170 offset:53760\n"
MF "v[36:39],a[208:211],a[244:247],v[36:39]\n"
"s_nop 0\n"
TR "v[184:185],v2 offset:8192\n"
"ds_write_addtid_b32 v171 offset:54016\n"
MF "v[36:39],a[168:171],a[240:243],v[36:39]\n"
"s_add_u32 m0,s79,32768\n"
TR "v[186:187],v2 offset:10240\n"
"ds_write_addtid_b32 v172 offset:50176\n"
MF "v[36:39],a[128:131],v[188:191],v[36:39]\n"
LD "v200,s[48:51],s89 offen lds\n"
MF "v[28:31],a[124:127],v[188:191],v[28:31]\n"
"s_nop 0\n"
"ds_write_addtid_b32 v173 offset:50432\n"
"ds_write_addtid_b32 v174 offset:50688\n"
MF "v[28:31],a[164:167],a[240:243],v[28:31]\n"
"ds_write_addtid_b32 v175 offset:50944\n"
MF "v[28:31],a[204:207],a[244:247],v[28:31]\n"
MF "v[20:23],a[200:203],a[244:247],v[20:23]\n"
LD "v201,s[48:51],s89 offen offset:1024 lds\n"
MF "v[20:23],a[160:163],a[240:243],v[20:23]\n"
MF "v[20:23],a[120:123],v[188:191],v[20:23]\n"
"s_waitcnt lgkmcnt(4)\n"
MF "v[56:59],a[120:123],v[176:179],v[56:59]\n"
TR "v[188:189],v3\n"
MF "v[56:59],a[160:163],v[180:183],v[56:59]\n"
TR "v[190:191],v3 offset:2048\n"
MF "v[56:59],a[200:203],v[184:187],v[56:59]\n"
LD "v202,s[48:51],s89 offen offset:2048 lds\n"
MF "v[64:67],a[204:207],v[184:187],v[64:67]\n"
TR "a[240:241],v3 offset:4096\n"
MF "v[64:67],a[164:167],v[180:183],v[64:67]\n"
TR "a[242:243],v3 offset:6144\n"
MF "v[64:67],a[124:127],v[176:179],v[64:67]\n"
LD "v203,s[48:51],s89 offen offset:3072 lds\n"
MF "v[72:75],a[128:131],v[176:179],v[72:75]\n"
TR "a[244:245],v3 offset:8192\n"
MF "v[72:75],a[168:171],v[180:183],v[72:75]\n"
TR "a[246:247],v3 offset:10240\n"
MF "v[72:75],a[208:211],v[184:187],v[72:75]\n"
MF "v[80:83],a[212:215],v[184:187],v[80:83]\n"
MF "v[80:83],a[172:175],v[180:183],v[80:83]\n"
MF "v[80:83],a[132:135],v[176:179],v[80:83]\n"
MF "v[88:91],a[136:139],v[176:179],v[88:91]\n"
MF "v[88:91],a[176:179],v[180:183],v[88:91]\n"
"s_waitcnt vmcnt(24) lgkmcnt(0)\n"
MF "v[88:91],a[216:219],v[184:187],v[88:91]\n"
"s_barrier\n"
MF "v[92:95],a[216:219],a[244:247],v[92:95]\n"
"s_add_u32 s80,s89,122880\n"
TR "v[176:177],v0 offset:49152\n"
MF "v[92:95],a[176:179],a[240:243],v[92:95]\n"
"s_add_u32 m0,s79,0\n"
TR "v[178:179],v0 offset:51200\n"
MF "v[92:95],a[136:139],v[188:191],v[92:95]\n"
LD "v192,s[48:51],s80 offen lds\n"
MF "v[84:87],a[132:135],v[188:191],v[84:87]\n"
TR "v[180:181],v0 offset:53248\n"
MF "v[84:87],a[172:175],a[240:243],v[84:87]\n"
TR "v[182:183],v0 offset:55296\n"
MF "v[84:87],a[212:215],a[244:247],v[84:87]\n"
LD "v193,s[48:51],s80 offen offset:1024 lds\n"
MF "v[76:79],a[208:211],a[244:247],v[76:79]\n"
TR "v[184:185],v0 offset:57344\n"
MF "v[76:79],a[168:171],a[240:243],v[76:79]\n"
TR "v[186:187],v0 offset:59392\n"
MF "v[76:79],a[128:131],v[188:191],v[76:79]\n"
MF "v[68:71],a[124:127],v[188:191],v[68:71]\n"
MF "v[68:71],a[164:167],a[240:243],v[68:71]\n"
LD "v194,s[48:51],s80 offen offset:2048 lds\n"
MF "v[68:71],a[204:207],a[244:247],v[68:71]\n"
MF "v[60:63],a[200:203],a[244:247],v[60:63]\n"
MF "v[60:63],a[160:163],a[240:243],v[60:63]\n"
MF "v[60:63],a[120:123],v[188:191],v[60:63]\n"
"s_waitcnt lgkmcnt(0)\n"
LD "v195,s[48:51],s80 offen offset:3072 lds\n"
MF "v[16:19],a[20:23],v[176:179],v[16:19]\n"
TR "v[188:189],v1 offset:49152\n"
MF "v[16:19],a[60:63],v[180:183],v[16:19]\n"
TR "v[190:191],v1 offset:51200\n"
MF "v[16:19],a[100:103],v[184:187],v[16:19]\n"
MF "v[24:27],a[104:107],v[184:187],v[24:27]\n"
TR "a[240:241],v1 offset:53248\n"
MF "v[24:27],a[64:67],v[180:183],v[24:27]\n"
"s_add_u32 m0,s79,4096\n"
TR "a[242:243],v1 offset:55296\n"
MF "v[24:27],a[24:27],v[176:179],v[24:27]\n"
LD "v196,s[48:51],s80 offen lds\n"
MF "v[32:35],a[28:31],v[176:179],v[32:35]\n"
TR "a[244:245],v1 offset:57344\n"
MF "v[32:35],a[68:71],v[180:183],v[32:35]\n"
TR "a[246:247],v1 offset:59392\n"
MF "v[32:35],a[108:111],v[184:187],v[32:35]\n"
LD "v197,s[48:51],s80 offen offset:1024 lds\n"
MF "v[40:43],a[112:115],v[184:187],v[40:43]\n"
MF "v[40:43],a[72:75],v[180:183],v[40:43]\n"
MF "v[40:43],a[32:35],v[176:179],v[40:43]\n"
MF "v[48:51],a[36:39],v[176:179],v[48:51]\n"
MF "v[48:51],a[76:79],v[180:183],v[48:51]\n"
LD "v198,s[48:51],s80 offen offset:2048 lds\n"
MF "v[48:51],a[116:119],v[184:187],v[48:51]\n"
"s_waitcnt lgkmcnt(0)\n"
MF "v[52:55],a[116:119],a[244:247],v[52:55]\n"
TR "v[176:177],v2 offset:49152\n"
MF "v[52:55],a[76:79],a[240:243],v[52:55]\n"
TR "v[178:179],v2 offset:51200\n"
MF "v[52:55],a[36:39],v[188:191],v[52:55]\n"
LD "v199,s[48:51],s80 offen offset:3072 lds\n"
MF "v[44:47],a[32:35],v[188:191],v[44:47]\n"
TR "v[180:181],v2 offset:53248\n"
MF "v[44:47],a[72:75],a[240:243],v[44:47]\n"
TR "v[182:183],v2 offset:55296\n"
MF "v[44:47],a[112:115],a[244:247],v[44:47]\n"
MF "v[36:39],a[108:111],a[244:247],v[36:39]\n"
TR "v[184:185],v2 offset:57344\n"
MF "v[36:39],a[68:71],a[240:243],v[36:39]\n"
"s_add_u32 m0,s79,8192\n"
TR "v[186:187],v2 offset:59392\n"
MF "v[36:39],a[28:31],v[188:191],v[36:39]\n"
LD "v200,s[48:51],s80 offen lds\n"
MF "v[28:31],a[24:27],v[188:191],v[28:31]\n"
MF "v[28:31],a[64:67],a[240:243],v[28:31]\n"
MF "v[28:31],a[104:107],a[244:247],v[28:31]\n"
MF "v[20:23],a[100:103],a[244:247],v[20:23]\n"
LD "v201,s[48:51],s80 offen offset:1024 lds\n"
MF "v[20:23],a[60:63],a[240:243],v[20:23]\n"
MF "v[20:23],a[20:23],v[188:191],v[20:23]\n"
"s_waitcnt lgkmcnt(0)\n"
MF "v[56:59],a[20:23],v[176:179],v[56:59]\n"
TR "v[188:189],v3 offset:49152\n"
MF "v[56:59],a[60:63],v[180:183],v[56:59]\n"
TR "v[190:191],v3 offset:51200\n"
MF "v[56:59],a[100:103],v[184:187],v[56:59]\n"
LD "v202,s[48:51],s80 offen offset:2048 lds\n"
MF "v[64:67],a[104:107],v[184:187],v[64:67]\n"
TR "a[240:241],v3 offset:53248\n"
MF "v[64:67],a[64:67],v[180:183],v[64:67]\n"
TR "a[242:243],v3 offset:55296\n"
MF "v[64:67],a[24:27],v[176:179],v[64:67]\n"
LD "v203,s[48:51],s80 offen offset:3072 lds\n"
MF "v[72:75],a[28:31],v[176:179],v[72:75]\n"
TR "a[244:245],v3 offset:57344\n"
MF "v[72:75],a[68:71],v[180:183],v[72:75]\n"
TR "a[246:247],v3 offset:59392\n"
MF "v[72:75],a[108:111],v[184:187],v[72:75]\n"
MF "v[80:83],a[112:115],v[184:187],v[80:83]\n"
MF "v[80:83],a[72:75],v[180:183],v[80:83]\n"
MF "v[80:83],a[32:35],v[176:179],v[80:83]\n"
MF "v[88:91],a[36:39],v[176:179],v[88:91]\n"
MF "v[88:91],a[76:79],v[180:183],v[88:91]\n"
"s_waitcnt vmcnt(24) lgkmcnt(0)\n"
MF "v[88:91],a[116:119],v[184:187],v[88:91]\n"
"s_barrier\n"
MF "v[92:95],a[116:119],a[244:247],v[92:95]\n"
"s_add_u32 s88,s88,s65\n"
TR "v[176:177],v4\n"
MF "v[92:95],a[76:79],a[240:243],v[92:95]\n"
"s_add_u32 m0,s79,49152\n"
TR "v[178:179],v4 offset:2048\n"
MF "v[92:95],a[36:39],v[188:191],v[92:95]\n"
LD "v192,s[48:51],s88 offen lds\n"
MF "v[84:87],a[32:35],v[188:191],v[84:87]\n"
TR "v[180:181],v4 offset:4096\n"
MF "v[84:87],a[72:75],a[240:243],v[84:87]\n"
TR "v[182:183],v4 offset:6144\n"
MF "v[84:87],a[112:115],a[244:247],v[84:87]\n"
LD "v193,s[48:51],s88 offen offset:1024 lds\n"
MF "v[76:79],a[108:111],a[244:247],v[76:79]\n"
TR "v[184:185],v4 offset:8192\n"
MF "v[76:79],a[68:71],a[240:243],v[76:79]\n"
TR "v[186:187],v4 offset:10240\n"
MF "v[76:79],a[28:31],v[188:191],v[76:79]\n"
MF "v[68:71],a[24:27],v[188:191],v[68:71]\n"
MF "v[68:71],a[64:67],a[240:243],v[68:71]\n"
LD "v194,s[48:51],s88 offen offset:2048 lds\n"
MF "v[68:71],a[104:107],a[244:247],v[68:71]\n"
MF "v[60:63],a[100:103],a[244:247],v[60:63]\n"
MF "v[60:63],a[60:63],a[240:243],v[60:63]\n"
MF "v[60:63],a[20:23],v[188:191],v[60:63]\n"
"s_waitcnt lgkmcnt(0)\n"
LD "v195,s[48:51],s88 offen offset:3072 lds\n"
MF "v[16:19],a[140:143],v[176:179],v[16:19]\n"
TR "v[188:189],v5\n"
MF "v[16:19],a[180:183],v[180:183],v[16:19]\n"
TR "v[190:191],v5 offset:2048\n"
MF "v[16:19],a[220:223],v[184:187],v[16:19]\n"
MF "v[24:27],a[224:227],v[184:187],v[24:27]\n"
TR "a[240:241],v5 offset:4096\n"
MF "v[24:27],a[184:187],v[180:183],v[24:27]\n"
"s_add_u32 m0,s79,53248\n"
TR "a[242:243],v5 offset:6144\n"
MF "v[24:27],a[144:147],v[176:179],v[24:27]\n"
LD "v196,s[48:51],s88 offen lds\n"
MF "v[32:35],a[148:151],v[176:179],v[32:35]\n"
TR "a[244:245],v5 offset:8192\n"
MF "v[32:35],a[188:191],v[180:183],v[32:35]\n"
TR "a[246:247],v5 offset:10240\n"
MF "v[32:35],a[228:231],v[184:187],v[32:35]\n"
LD "v197,s[48:51],s88 offen offset:1024 lds\n"
MF "v[40:43],a[232:235],v[184:187],v[40:43]\n"
MF "v[40:43],a[192:195],v[180:183],v[40:43]\n"
MF "v[40:43],a[152:155],v[176:179],v[40:43]\n"
MF "v[48:51],a[156:159],v[176:179],v[48:51]\n"
MF "v[48:51],a[196:199],v[180:183],v[48:51]\n"
LD "v198,s[48:51],s88 offen offset:2048 lds\n"
MF "v[48:51],a[236:239],v[184:187],v[48:51]\n"
"s_waitcnt lgkmcnt(0)\n"
MF "v[52:55],a[236:239],a[244:247],v[52:55]\n"
TR "v[176:177],v6\n"
MF "v[52:55],a[196:199],a[240:243],v[52:55]\n"
TR "v[178:179],v6 offset:2048\n"
MF "v[52:55],a[156:159],v[188:191],v[52:55]\n"
LD "v199,s[48:51],s88 offen offset:3072 lds\n"
MF "v[44:47],a[152:155],v[188:191],v[44:47]\n"
TR "v[180:181],v6 offset:4096\n"
MF "v[44:47],a[192:195],a[240:243],v[44:47]\n"
TR "v[182:183],v6 offset:6144\n"
MF "v[44:47],a[232:235],a[244:247],v[44:47]\n"
MF "v[36:39],a[228:231],a[244:247],v[36:39]\n"
TR "v[184:185],v6 offset:8192\n"
MF "v[36:39],a[188:191],a[240:243],v[36:39]\n"
"s_add_u32 m0,s79,57344\n"
TR "v[186:187],v6 offset:10240\n"
MF "v[36:39],a[148:151],v[188:191],v[36:39]\n"
LD "v200,s[48:51],s88 offen lds\n"
MF "v[28:31],a[144:147],v[188:191],v[28:31]\n"
MF "v[28:31],a[184:187],a[240:243],v[28:31]\n"
MF "v[28:31],a[224:227],a[244:247],v[28:31]\n"
MF "v[20:23],a[220:223],a[244:247],v[20:23]\n"
LD "v201,s[48:51],s88 offen offset:1024 lds\n"
MF "v[20:23],a[180:183],a[240:243],v[20:23]\n"
MF "v[20:23],a[140:143],v[188:191],v[20:23]\n"
"s_waitcnt lgkmcnt(0)\n"
MF "v[56:59],a[140:143],v[176:179],v[56:59]\n"
TR "v[188:189],v7\n"
MF "v[56:59],a[180:183],v[180:183],v[56:59]\n"
TR "v[190:191],v7 offset:2048\n"
MF "v[56:59],a[220:223],v[184:187],v[56:59]\n"
LD "v202,s[48:51],s88 offen offset:2048 lds\n"
MF "v[64:67],a[224:227],v[184:187],v[64:67]\n"
TR "a[240:241],v7 offset:4096\n"
"v_mov_b32 v244,0\n"
MF "v[64:67],a[184:187],v[180:183],v[64:67]\n"
TR "a[242:243],v7 offset:6144\n"
"v_mov_b32 v245,0\n"
MF "v[64:67],a[144:147],v[176:179],v[64:67]\n"
LD "v203,s[48:51],s88 offen offset:3072 lds\n"
MF "v[72:75],a[148:151],v[176:179],v[72:75]\n"
TR "a[244:245],v7 offset:8192\n"
"v_mov_b32 v246,0\n"
MF "v[72:75],a[188:191],v[180:183],v[72:75]\n"
TR "a[246:247],v7 offset:10240\n"
"v_mov_b32 v247,0\n"
MF "v[72:75],a[228:231],v[184:187],v[72:75]\n"
"v_mov_b32 v248,0\n"
"v_mov_b32 v249,0\n"
MF "v[80:83],a[232:235],v[184:187],v[80:83]\n"
"v_mov_b32 v250,0\n"
"v_mov_b32 v251,0\n"
MF "v[80:83],a[192:195],v[180:183],v[80:83]\n"
MF "v[80:83],a[152:155],v[176:179],v[80:83]\n"
MF "v[88:91],a[156:159],v[176:179],v[88:91]\n"
MF "v[88:91],a[196:199],v[180:183],v[88:91]\n"
"s_waitcnt vmcnt(24) lgkmcnt(0)\n"
MF "v[88:91],a[236:239],v[184:187],v[88:91]\n"
"s_barrier\n"
MF "v[92:95],a[236:239],a[244:247],v[92:95]\n"
"s_add_u32 s80,s88,122880\n"
TR "v[176:177],v0 offset:24576\n"
MF "v[92:95],a[196:199],a[240:243],v[92:95]\n"
"s_add_u32 m0,s79,98304\n"
"s_add_u32 s85,s85,1\n"
TR "v[178:179],v0 offset:26624\n"
MF "v[92:95],a[156:159],v[188:191],v[92:95]\n"
LD "v192,s[48:51],s80 offen lds\n"
MF "v[84:87],a[152:155],v[188:191],v[84:87]\n"
"s_nop 0\n"
TR "v[180:181],v0 offset:28672\n"
"ds_write_addtid_b32 v16 offset:24576\n"
MF "v[84:87],a[192:195],a[240:243],v[84:87]\n"
"s_nop 0\n"
TR "v[182:183],v0 offset:30720\n"
"ds_write_addtid_b32 v17 offset:24832\n"
MF "v[84:87],a[232:235],a[244:247],v[84:87]\n"
LD "v193,s[48:51],s80 offen offset:1024 lds\n"
MF "v[76:79],a[228:231],a[244:247],v[76:79]\n"
"s_nop 0\n"
TR "v[184:185],v0 offset:32768\n"
"ds_write_addtid_b32 v18 offset:25088\n"
MF "v[76:79],a[188:191],a[240:243],v[76:79]\n"
"s_nop 0\n"
TR "v[186:187],v0 offset:34816\n"
"ds_write_addtid_b32 v19 offset:25344\n"
MF "v[76:79],a[148:151],v[188:191],v[76:79]\n"
"s_nop 0\n"
"ds_write_addtid_b32 v20 offset:25600\n"
"ds_write_addtid_b32 v21 offset:25856\n"
MF "v[68:71],a[144:147],v[188:191],v[68:71]\n"
LD "v194,s[48:51],s80 offen offset:2048 lds\n"
MF "v[68:71],a[184:187],a[240:243],v[68:71]\n"
"s_nop 0\n"
"ds_write_addtid_b32 v22 offset:26112\n"
"ds_write_addtid_b32 v23 offset:26368\n"
MF "v[68:71],a[224:227],a[244:247],v[68:71]\n"
"s_nop 0\n"
"ds_write_addtid_b32 v24 offset:26624\n"
"ds_write_addtid_b32 v25 offset:26880\n"
MF "v[60:63],a[220:223],a[244:247],v[60:63]\n"
"s_nop 0\n"
"ds_write_addtid_b32 v26 offset:27136\n"
"ds_write_addtid_b32 v27 offset:27392\n"
MF "v[60:63],a[180:183],a[240:243],v[60:63]\n"
MF "v[60:63],a[140:143],v[188:191],v[60:63]\n"
"s_waitcnt lgkmcnt(9)\n"
"s_cmp_lg_u32 s85,5\n"
"s_cbranch_scc1 .Lpl05\n"
".Lpl09:\n"
MF "v[96:99],a[0:3],v[176:179],v[252:255]\n"
"s_nop 0\n"
TR "v[188:189],v1 offset:24576\n"
"ds_write_addtid_b32 v28 offset:27648\n"
MF "v[96:99],a[40:43],v[180:183],v[96:99]\n"
"s_nop 0\n"
TR "v[190:191],v1 offset:26624\n"
"ds_write_addtid_b32 v29 offset:27904\n"
MF "v[96:99],a[80:83],v[184:187],v[96:99]\n"
LD "v195,s[48:51],s80 offen offset:3072 lds\n"
MF "v[104:107],a[84:87],v[184:187],v[252:255]\n"
"s_add_u32 s60,s64,1\n"
TR "a[240:241],v1 offset:28672\n"
"ds_write_addtid_b32 v30 offset:28160\n"
MF "v[104:107],a[44:47],v[180:183],v[104:107]\n"
"s_add_u32 m0,s79,102400\n"
TR "a[242:243],v1 offset:30720\n"
"ds_write_addtid_b32 v31 offset:24320\n"
MF "v[104:107],a[4:7],v[176:179],v[104:107]\n"
LD "v196,s[48:51],s80 offen lds\n"
MF "v[112:115],a[8:11],v[176:179],v[252:255]\n"
"s_cmp_eq_u32 s60,8\n"
"s_cselect_b32 s60,0,s60\n"
TR "a[244:245],v1 offset:32768\n"
MF "v[112:115],a[48:51],v[180:183],v[112:115]\n"
"s_cmp_eq_u32 s60,10\n"
"s_cselect_b32 s60,s61,s60\n"
TR "a[246:247],v1 offset:34816\n"
MF "v[112:115],a[88:91],v[184:187],v[112:115]\n"
"s_sub_u32 s65,s60,s64\n"
"ds_write_addtid_b32 v32 offset:24576\n"
"ds_write_addtid_b32 v33 offset:24832\n"
MF "v[120:123],a[92:95],v[184:187],v[252:255]\n"
"s_lshl_b32 s65,s65,7\n"
"ds_write_addtid_b32 v34 offset:25088\n"
"ds_write_addtid_b32 v35 offset:25344\n"
MF "v[120:123],a[52:55],v[180:183],v[120:123]\n"
LD "v197,s[48:51],s80 offen offset:1024 lds\n"
MF "v[120:123],a[12:15],v[176:179],v[120:123]\n"
"s_mov_b32 s64,s60\n"
"ds_write_addtid_b32 v36 offset:25600\n"
"ds_write_addtid_b32 v37 offset:25856\n"
MF "v[128:131],a[16:19],v[176:179],v[252:255]\n"
"s_nop 0\n"
"ds_write_addtid_b32 v38 offset:26112\n"
"ds_write_addtid_b32 v39 offset:26368\n"
MF "v[128:131],a[56:59],v[180:183],v[128:131]\n"
"s_nop 0\n"
"ds_write_addtid_b32 v40 offset:26624\n"
"ds_write_addtid_b32 v41 offset:26880\n"
MF "v[128:131],a[96:99],v[184:187],v[128:131]\n"
"s_waitcnt lgkmcnt(10)\n"
LD "v198,s[48:51],s80 offen offset:2048 lds\n"
MF "v[132:135],a[96:99],a[244:247],v[252:255]\n"
"s_nop 0\n"
TR "v[176:177],v2 offset:24576\n"
"ds_write_addtid_b32 v42 offset:27136\n"
MF "v[132:135],a[56:59],a[240:243],v[132:135]\n"
"s_nop 0\n"
TR "v[178:179],v2 offset:26624\n"
"ds_write_addtid_b32 v43 offset:27392\n"
MF "v[132:135],a[16:19],v[188:191],v[132:135]\n"
"s_nop 0\n"
"ds_write_addtid_b32 v44 offset:27648\n"
"ds_write_addtid_b32 v45 offset:27904\n"
MF "v[124:127],a[12:15],v[188:191],v[252:255]\n"
"s_nop 0\n"
TR "v[180:181],v2 offset:28672\n"
"ds_write_addtid_b32 v46 offset:28160\n"
MF "v[124:127],a[52:55],a[240:243],v[124:127]\n"
"s_nop 0\n"
TR "v[182:183],v2 offset:30720\n"
"ds_write_addtid_b32 v47 offset:28416\n"
MF "v[124:127],a[92:95],a[244:247],v[124:127]\n"
LD "v199,s[48:51],s80 offen offset:3072 lds\n"
MF "v[116:119],a[88:91],a[244:247],v[252:255]\n"
"s_nop 0\n"
TR "v[184:185],v2 offset:32768\n"
"ds_write_addtid_b32 v48 offset:28672\n"
MF "v[116:119],a[48:51],a[240:243],v[116:119]\n"
"s_add_u32 m0,s79,106496\n"
TR "v[186:187],v2 offset:34816\n"
"ds_write_addtid_b32 v49 offset:24832\n"
MF "v[116:119],a[8:11],v[188:191],v[116:119]\n"
LD "v200,s[48:51],s80 offen lds\n"
MF "v[108:111],a[4:7],v[188:191],v[252:255]\n"
"s_nop 0\n"
"ds_write_addtid_b32 v50 offset:25088\n"
"ds_write_addtid_b32 v51 offset:25344\n"
MF "v[108:111],a[44:47],a[240:243],v[108:111]\n"
"s_nop 0\n"
"ds_write_addtid_b32 v52 offset:25600\n"
"ds_write_addtid_b32 v53 offset:25856\n"
MF "v[108:111],a[84:87],a[244:247],v[108:111]\n"
"s_nop 0\n"
"ds_write_addtid_b32 v54 offset:26112\n"
"ds_write_addtid_b32 v55 offset:26368\n"
MF "v[100:103],a[80:83],a[244:247],v[252:255]\n"
MF "v[100:103],a[40:43],a[240:243],v[100:103]\n"
LD "v201,s[48:51],s80 offen offset:1024 lds\n"
MF "v[100:103],a[0:3],v[188:191],v[100:103]\n"
"s_waitcnt lgkmcnt(7)\n"
MF "v[136:139],a[0:3],v[176:179],v[252:255]\n"
TR "v[188:189],v3 offset:24576\n"
MF "v[136:139],a[40:43],v[180:183],v[136:139]\n"
TR "v[190:191],v3 offset:26624\n"
MF "v[136:139],a[80:83],v[184:187],v[136:139]\n"
LD "v202,s[48:51],s80 offen offset:2048 lds\n"
MF "v[144:147],a[84:87],v[184:187],v[252:255]\n"
TR "a[240:241],v3 offset:28672\n"
MF "v[144:147],a[44:47],v[180:183],v[144:147]\n"
TR "a[242:243],v3 offset:30720\n"
MF "v[144:147],a[4:7],v[176:179],v[144:147]\n"
LD "v203,s[48:51],s80 offen offset:3072 lds\n"
MF "v[152:155],a[8:11],v[176:179],v[252:255]\n"
TR "a[244:245],v3 offset:32768\n"
MF "v[152:155],a[48:51],v[180:183],v[152:155]\n"
TR "a[246:247],v3 offset:34816\n"
MF "v[152:155],a[88:91],v[184:187],v[152:155]\n"
MF "v[160:163],a[92:95],v[184:187],v[252:255]\n"
MF "v[160:163],a[52:55],v[180:183],v[160:163]\n"
MF "v[160:163],a[12:15],v[176:179],v[160:163]\n"
MF "v[168:171],a[16:19],v[176:179],v[252:255]\n"
MF "v[168:171],a[56:59],v[180:183],v[168:171]\n"
"s_waitcnt vmcnt(24) lgkmcnt(0)\n"
MF "v[168:171],a[96:99],v[184:187],v[168:171]\n"
"s_barrier\n"
MF "v[172:175],a[96:99],a[244:247],v[252:255]\n"
"s_add_u32 s89,s89,s65\n"
TR "v[176:177],v0\n"
MF "v[172:175],a[56:59],a[240:243],v[172:175]\n"
"s_add_u32 m0,s79,24576\n"
TR "v[178:179],v0 offset:2048\n"
MF "v[172:175],a[16:19],v[188:191],v[172:175]\n"
LD "v192,s[48:51],s89 offen lds\n"
MF "v[164:167],a[12:15],v[188:191],v[252:255]\n"
TR "v[180:181],v0 offset:4096\n"
MF "v[164:167],a[52:55],a[240:243],v[164:167]\n"
TR "v[182:183],v0 offset:6144\n"
MF "v[164:167],a[92:95],a[244:247],v[164:167]\n"
LD "v193,s[48:51],s89 offen offset:1024 lds\n"
MF "v[156:159],a[88:91],a[244:247],v[252:255]\n"
TR "v[184:185],v0 offset:8192\n"
MF "v[156:159],a[48:51],a[240:243],v[156:159]\n"
"s_nop 0\n"
TR "v[186:187],v0 offset:10240\n"
"ds_write_addtid_b32 v56 offset:49152\n"
MF "v[156:159],a[8:11],v[188:191],v[156:159]\n"
"s_nop 0\n"
"ds_write_addtid_b32 v57 offset:49408\n"
"ds_write_addtid_b32 v58 offset:49664\n"
MF "v[148:151],a[4:7],v[188:191],v[252:255]\n"
LD "v194,s[48:51],s89 offen offset:2048 lds\n"
MF "v[148:151],a[44:47],a[240:243],v[148:151]\n"
"buffer_load_dword v10,v12,s[40:43],s86 offen sc1\n"
MF "v[148:151],a[84:87],a[244:247],v[148:151]\n"
"s_nop 0\n"
"ds_write_addtid_b32 v59 offset:49920\n"
"ds_write_addtid_b32 v60 offset:50176\n"
MF "v[140:143],a[80:83],a[244:247],v[252:255]\n"
"s_nop 0\n"
"ds_write_addtid_b32 v61 offset:50432\n"
"ds_write_addtid_b32 v62 offset:50688\n"
MF "v[140:143],a[40:43],a[240:243],v[140:143]\n"
"s_nop 0\n"
"ds_write_addtid_b32 v63 offset:50944\n"
"ds_write_addtid_b32 v64 offset:51200\n"
MF "v[140:143],a[0:3],v[188:191],v[140:143]\n"
"s_waitcnt lgkmcnt(9)\n"
LD "v195,s[48:51],s89 offen offset:3072 lds\n"
MF "v[96:99],a[120:123],v[176:179],v[96:99]\n"
"s_nop 0\n"
TR "v[188:189],v1\n"
"ds_write_addtid_b32 v65 offset:51456\n"
MF "v[96:99],a[160:163],v[180:183],v[96:99]\n"
"s_nop 0\n"
TR "v[190:191],v1 offset:2048\n"
"ds_write_addtid_b32 v66 offset:51712\n"
MF "v[96:99],a[200:203],v[184:187],v[96:99]\n"
"s_nop 0\n"
"ds_write_addtid_b32 v67 offset:51968\n"
"ds_write_addtid_b32 v68 offset:52224\n"
MF "v[104:107],a[204:207],v[184:187],v[104:107]\n"
"s_nop 0\n"
TR "a[240:241],v1 offset:4096\n"
"ds_write_addtid_b32 v69 offset:52480\n"
MF "v[104:107],a[164:167],v[180:183],v[104:107]\n"
"s_add_u32 m0,s79,28672\n"
TR "a[242:243],v1 offset:6144\n"
"ds_write_addtid_b32 v70 offset:48640\n"
MF "v[104:107],a[124:127],v[176:179],v[104:107]\n"
".Lpg28h:\n"
"s_cbranch_execz .Lpgrole1_h\n"
LD "v196,s[48:51],s89 offen lds\n"
MF "v[112:115],a[128:131],v[176:179],v[112:115]\n"
"s_nop 0\n"
TR "a[244:245],v1 offset:8192\n"
"ds_write_addtid_b32 v71 offset:48896\n"
MF "v[112:115],a[168:171],v[180:183],v[112:115]\n"
"s_nop 0\n"
TR "a[246:247],v1 offset:10240\n"
"ds_write_addtid_b32 v72 offset:49152\n"
MF "v[112:115],a[208:211],v[184:187],v[112:115]\n"
LD "v197,s[48:51],s89 offen offset:1024 lds\n"
MF "v[120:123],a[212:215],v[184:187],v[120:123]\n"
"s_nop 0\n"
"ds_write_addtid_b32 v73 offset:49408\n"
"ds_write_addtid_b32 v74 offset:49664\n"
MF "v[120:123],a[172:175],v[180:183],v[120:123]\n"
"s_nop 0\n"
"ds_write_addtid_b32 v75 offset:49920\n"
"ds_write_addtid_b32 v76 offset:50176\n"
MF "v[120:123],a[132:135],v[176:179],v[120:123]\n"
"s_nop 0\n"
"ds_write_addtid_b32 v77 offset:50432\n"
"ds_write_addtid_b32 v78 offset:50688\n"
MF "v[128:131],a[136:139],v[176:179],v[128:131]\n"
"s_nop 0\n"
"ds_write_addtid_b32 v79 offset:50944\n"
"ds_write_addtid_b32 v80 offset:51200\n"
MF "v[128:131],a[176:179],v[180:183],v[128:131]\n"
LD "v198,s[48:51],s89 offen offset:2048 lds\n"
MF "v[128:131],a[216:219],v[184:187],v[128:131]\n"
"s_waitcnt lgkmcnt(9)\n"
"ds_write_addtid_b32 v81 offset:51456\n"
"ds_write_addtid_b32 v82 offset:51712\n"
MF "v[132:135],a[216:219],a[244:247],v[132:135]\n"
"s_nop 0\n"
TR "v[176:177],v2\n"
"ds_write_addtid_b32 v83 offset:51968\n"
MF "v[132:135],a[176:179],a[240:243],v[132:135]\n"
"s_nop 0\n"
TR "v[178:179],v2 offset:2048\n"
"ds_write_addtid_b32 v84 offset:52224\n"
MF "v[132:135],a[136:139],v[188:191],v[132:135]\n"
LD "v199,s[48:51],s89 offen offset:3072 lds\n"
MF "v[124:127],a[132:135],v[188:191],v[124:127]\n"
"s_nop 0\n"
TR "v[180:181],v2 offset:4096\n"
"ds_write_addtid_b32 v85 offset:52480\n"
MF "v[124:127],a[172:175],a[240:243],v[124:127]\n"
"s_nop 0\n"
TR "v[182:183],v2 offset:6144\n"
"ds_write_addtid_b32 v86 offset:52736\n"
MF "v[124:127],a[212:215],a[244:247],v[124:127]\n"
"s_nop 0\n"
"ds_write_addtid_b32 v87 offset:52992\n"
"ds_write_addtid_b32 v88 offset:53248\n"
MF "v[116:119],a[208:211],a[244:247],v[116:119]\n"
"s_nop 0\n"
TR "v[184:185],v2 offset:8192\n"
"ds_write_addtid_b32 v89 offset:53504\n"
MF "v[116:119],a[168:171],a[240:243],v[116:119]\n"
"s_add_u32 m0,s79,32768\n"
TR "v[186:187],v2 offset:10240\n"
"ds_write_addtid_b32 v90 offset:49664\n"
MF "v[116:119],a[128:131],v[188:191],v[116:119]\n"
LD "v200,s[48:51],s89 offen lds\n"
MF "v[108:111],a[124:127],v[188:191],v[108:111]\n"
"s_nop 0\n"
"ds_write_addtid_b32 v91 offset:49920\n"
"ds_write_addtid_b32 v92 offset:50176\n"
MF "v[108:111],a[164:167],a[240:243],v[108:111]\n"
"s_nop 0\n"
"ds_write_addtid_b32 v93 offset:50432\n"
"ds_write_addtid_b32 v94 offset:50688\n"
MF "v[108:111],a[204:207],a[244:247],v[108:111]\n"
"ds_write_addtid_b32 v95 offset:50944\n"
MF "v[100:103],a[200:203],a[244:247],v[100:103]\n"
LD "v201,s[48:51],s89 offen offset:1024 lds\n"
MF "v[100:103],a[160:163],a[240:243],v[100:103]\n"
MF "v[100:103],a[120:123],v[188:191],v[100:103]\n"
"s_waitcnt lgkmcnt(6)\n"
MF "v[136:139],a[120:123],v[176:179],v[136:139]\n"
TR "v[188:189],v3\n"
MF "v[136:139],a[160:163],v[180:183],v[136:139]\n"
TR "v[190:191],v3 offset:2048\n"
MF "v[136:139],a[200:203],v[184:187],v[136:139]\n"
LD "v202,s[48:51],s89 offen offset:2048 lds\n"
MF "v[144:147],a[204:207],v[184:187],v[144:147]\n"
TR "a[240:241],v3 offset:4096\n"
MF "v[144:147],a[164:167],v[180:183],v[144:147]\n"
TR "a[242:243],v3 offset:6144\n"
MF "v[144:147],a[124:127],v[176:179],v[144:147]\n"
LD "v203,s[48:51],s89 offen offset:3072 lds\n"
MF "v[152:155],a[128:131],v[176:179],v[152:155]\n"
TR "a[244:245],v3 offset:8192\n"
MF "v[152:155],a[168:171],v[180:183],v[152:155]\n"
TR "a[246:247],v3 offset:10240\n"
MF "v[152:155],a[208:211],v[184:187],v[152:155]\n"
MF "v[160:163],a[212:215],v[184:187],v[160:163]\n"
MF "v[160:163],a[172:175],v[180:183],v[160:163]\n"
MF "v[160:163],a[132:135],v[176:179],v[160:163]\n"
MF "v[168:171],a[136:139],v[176:179],v[168:171]\n"
MF "v[168:171],a[176:179],v[180:183],v[168:171]\n"
"s_waitcnt vmcnt(25) lgkmcnt(0)\n"
MF "v[168:171],a[216:219],v[184:187],v[168:171]\n"
"s_barrier\n"
MF "v[172:175],a[216:219],a[244:247],v[172:175]\n"
"s_add_u32 s80,s89,122880\n"
TR "v[176:177],v0 offset:49152\n"
MF "v[172:175],a[176:179],a[240:243],v[172:175]\n"
"s_add_u32 m0,s79,0\n"
"s_mov_b32 s82,s70\n"
TR "v[178:179],v0 offset:51200\n"
MF "v[172:175],a[136:139],v[188:191],v[172:175]\n"
LD "v192,s[48:51],s80 offen lds\n"
MF "v[164:167],a[132:135],v[188:191],v[164:167]\n"
TR "v[180:181],v0 offset:53248\n"
MF "v[164:167],a[172:175],a[240:243],v[164:167]\n"
TR "v[182:183],v0 offset:55296\n"
MF "v[164:167],a[212:215],a[244:247],v[164:167]\n"
LD "v193,s[48:51],s80 offen offset:1024 lds\n"
MF "v[156:159],a[208:211],a[244:247],v[156:159]\n"
TR "v[184:185],v0 offset:57344\n"
MF "v[156:159],a[168:171],a[240:243],v[156:159]\n"
TR "v[186:187],v0 offset:59392\n"
MF "v[156:159],a[128:131],v[188:191],v[156:159]\n"
MF "v[148:151],a[124:127],v[188:191],v[148:151]\n"
MF "v[148:151],a[164:167],a[240:243],v[148:151]\n"
LD "v194,s[48:51],s80 offen offset:2048 lds\n"
MF "v[148:151],a[204:207],a[244:247],v[148:151]\n"
MF "v[140:143],a[200:203],a[244:247],v[140:143]\n"
MF "v[140:143],a[160:163],a[240:243],v[140:143]\n"
MF "v[140:143],a[120:123],v[188:191],v[140:143]\n"
"s_waitcnt lgkmcnt(0)\n"
LD "v195,s[48:51],s80 offen offset:3072 lds\n"
MF "v[96:99],a[20:23],v[176:179],v[96:99]\n"
TR "v[188:189],v1 offset:49152\n"
MF "v[96:99],a[60:63],v[180:183],v[96:99]\n"
TR "v[190:191],v1 offset:51200\n"
MF "v[96:99],a[100:103],v[184:187],v[96:99]\n"
MF "v[104:107],a[104:107],v[184:187],v[104:107]\n"
TR "a[240:241],v1 offset:53248\n"
MF "v[104:107],a[64:67],v[180:183],v[104:107]\n"
"s_add_u32 m0,s79,4096\n"
TR "a[242:243],v1 offset:55296\n"
MF "v[104:107],a[24:27],v[176:179],v[104:107]\n"
LD "v196,s[48:51],s80 offen lds\n"
MF "v[112:115],a[28:31],v[176:179],v[112:115]\n"
TR "a[244:245],v1 offset:57344\n"
MF "v[112:115],a[68:71],v[180:183],v[112:115]\n"
TR "a[246:247],v1 offset:59392\n"
MF "v[112:115],a[108:111],v[184:187],v[112:115]\n"
LD "v197,s[48:51],s80 offen offset:1024 lds\n"
MF "v[120:123],a[112:115],v[184:187],v[120:123]\n"
MF "v[120:123],a[72:75],v[180:183],v[120:123]\n"
MF "v[120:123],a[32:35],v[176:179],v[120:123]\n"
MF "v[128:131],a[36:39],v[176:179],v[128:131]\n"
MF "v[128:131],a[76:79],v[180:183],v[128:131]\n"
LD "v198,s[48:51],s80 offen offset:2048 lds\n"
MF "v[128:131],a[116:119],v[184:187],v[128:131]\n"
"s_waitcnt lgkmcnt(0)\n"
MF "v[132:135],a[116:119],a[244:247],v[132:135]\n"
TR "v[176:177],v2 offset:49152\n"
MF "v[132:135],a[76:79],a[240:243],v[132:135]\n"
TR "v[178:179],v2 offset:51200\n"
MF "v[132:135],a[36:39],v[188:191],v[132:135]\n"
LD "v199,s[48:51],s80 offen offset:3072 lds\n"
MF "v[124:127],a[32:35],v[188:191],v[124:127]\n"
TR "v[180:181],v2 offset:53248\n"
MF "v[124:127],a[72:75],a[240:243],v[124:127]\n"
TR "v[182:183],v2 offset:55296\n"
MF "v[124:127],a[112:115],a[244:247],v[124:127]\n"
MF "v[116:119],a[108:111],a[244:247],v[116:119]\n"
TR "v[184:185],v2 offset:57344\n"
MF "v[116:119],a[68:71],a[240:243],v[116:119]\n"
"s_add_u32 m0,s79,8192\n"
TR "v[186:187],v2 offset:59392\n"
MF "v[116:119],a[28:31],v[188:191],v[116:119]\n"
LD "v200,s[48:51],s80 offen lds\n"
MF "v[108:111],a[24:27],v[188:191],v[108:111]\n"
MF "v[108:111],a[64:67],a[240:243],v[108:111]\n"
MF "v[108:111],a[104:107],a[244:247],v[108:111]\n"
MF "v[100:103],a[100:103],a[244:247],v[100:103]\n"
LD "v201,s[48:51],s80 offen offset:1024 lds\n"
MF "v[100:103],a[60:63],a[240:243],v[100:103]\n"
"s_waitcnt vmcnt(19)\n"
"s_cmp_lt_u32 s85,6\n"
MF "v[100:103],a[20:23],v[188:191],v[100:103]\n"
"s_cbranch_scc1 .Ljs4\n"
"v_mov_b32 v236,v228\n"
"v_mov_b32 v237,v229\n"
MF "v[96:99],a[252:255],v[208:211],v[96:99]\n"
MF "v[104:107],a[252:255],v[212:215],v[104:107]\n"
MF "v[112:115],a[252:255],v[216:219],v[112:115]\n"
MF "v[120:123],a[252:255],v[220:223],v[120:123]\n"
MF "v[128:131],a[252:255],v[224:227],v[128:131]\n"
MF "v[100:103],a[248:251],v[208:211],v[100:103]\n"
MF "v[108:111],a[248:251],v[212:215],v[108:111]\n"
MF "v[116:119],a[248:251],v[216:219],v[116:119]\n"
MF "v[124:127],a[248:251],v[220:223],v[124:127]\n"
MF "v[132:135],a[248:251],v[224:227],v[132:135]\n"
MF "v[248:251],v[230:233],a[250:253],v[248:251]\n"
MF "v[244:247],v[234:237],a[250:253],v[244:247]\n"
".Ljs4:\n"
"s_waitcnt lgkmcnt(0)\n"
MF "v[136:139],a[20:23],v[176:179],v[136:139]\n"
TR "v[188:189],v3 offset:49152\n"
"v_sub_u32 v10,v10,v11\n"
MF "v[136:139],a[60:63],v[180:183],v[136:139]\n"
TR "v[190:191],v3 offset:51200\n"
MF "v[136:139],a[100:103],v[184:187],v[136:139]\n"
LD "v202,s[48:51],s80 offen offset:2048 lds\n"
MF "v[144:147],a[104:107],v[184:187],v[144:147]\n"
TR "a[240:241],v3 offset:53248\n"
MF "v[144:147],a[64:67],v[180:183],v[144:147]\n"
TR "a[242:243],v3 offset:55296\n"
MF "v[144:147],a[24:27],v[176:179],v[144:147]\n"
LD "v203,s[48:51],s80 offen offset:3072 lds\n"
MF "v[152:155],a[28:31],v[176:179],v[152:155]\n"
TR "a[244:245],v3 offset:57344\n"
MF "v[152:155],a[68:71],v[180:183],v[152:155]\n"
TR "a[246:247],v3 offset:59392\n"
MF "v[152:155],a[108:111],v[184:187],v[152:155]\n"
".Lnf0:\n"
"v_readlane_b32 s58,v10,s67\n"
"s_cmp_lt_u32 s58,s85\n"
"s_cbranch_scc1 .Lsp0\n"
".Lnb0:\n"
MF "v[160:163],a[112:115],v[184:187],v[160:163]\n"
"s_add_u32 s70,s70,20480\n"
".Lnf1:\n"
"v_cmp_gt_u32 vcc,4,v10\n"
"s_cbranch_vccnz .Lsp1\n"
".Lnb1:\n"
MF "v[160:163],a[72:75],v[180:183],v[160:163]\n"
MF "v[160:163],a[32:35],v[176:179],v[160:163]\n"
MF "v[168:171],a[36:39],v[176:179],v[168:171]\n"
MF "v[168:171],a[76:79],v[180:183],v[168:171]\n"
"s_waitcnt vmcnt(25) lgkmcnt(0)\n"
MF "v[168:171],a[116:119],v[184:187],v[168:171]\n"
"s_barrier\n"
MF "v[172:175],a[116:119],a[244:247],v[172:175]\n"
"s_add_u32 s88,s88,s65\n"
TR "v[176:177],v4\n"
MF "v[172:175],a[76:79],a[240:243],v[172:175]\n"
"s_add_u32 m0,s79,49152\n"
TR "v[178:179],v4 offset:2048\n"
MF "v[172:175],a[36:39],v[188:191],v[172:175]\n"
LD "v192,s[48:51],s88 offen lds\n"
MF "v[164:167],a[32:35],v[188:191],v[164:167]\n"
TR "v[180:181],v4 offset:4096\n"
MF "v[164:167],a[72:75],a[240:243],v[164:167]\n"
TR "v[182:183],v4 offset:6144\n"
MF "v[164:167],a[112:115],a[244:247],v[164:167]\n"
LD "v193,s[48:51],s88 offen offset:1024 lds\n"
MF "v[156:159],a[108:111],a[244:247],v[156:159]\n"
TR "v[184:185],v4 offset:8192\n"
MF "v[156:159],a[68:71],a[240:243],v[156:159]\n"
"s_add_u32 s2,s2,204800\n"
TR "v[186:187],v4 offset:10240\n"
MF "v[156:159],a[28:31],v[188:191],v[156:159]\n"
LD "v[228:231],v9,s[40:43],s2 offen sc1\n"
MF "v[148:151],a[24:27],v[188:191],v[148:151]\n"
MF "v[148:151],a[64:67],a[240:243],v[148:151]\n"
LD "v194,s[48:51],s88 offen offset:2048 lds\n"
MF "v[148:151],a[104:107],a[244:247],v[148:151]\n"
MF "v[140:143],a[100:103],a[244:247],v[140:143]\n"
MF "v[140:143],a[60:63],a[240:243],v[140:143]\n"
MF "v[140:143],a[20:23],v[188:191],v[140:143]\n"
"s_waitcnt lgkmcnt(0)\n"
LD "v195,s[48:51],s88 offen offset:3072 lds\n"
MF "v[96:99],a[140:143],v[176:179],v[96:99]\n"
TR "v[188:189],v5\n"
MF "v[96:99],a[180:183],v[180:183],v[96:99]\n"
"s_add_u32 s2,s2,204800\n"
TR "v[190:191],v5 offset:2048\n"
MF "v[96:99],a[220:223],v[184:187],v[96:99]\n"
LD "v[232:235],v9,s[40:43],s2 offen sc1\n"
MF "v[104:107],a[224:227],v[184:187],v[104:107]\n"
TR "a[240:241],v5 offset:4096\n"
MF "v[104:107],a[184:187],v[180:183],v[104:107]\n"
"s_add_u32 m0,s79,53248\n"
TR "a[242:243],v5 offset:6144\n"
MF "v[104:107],a[144:147],v[176:179],v[104:107]\n"
LD "v196,s[48:51],s88 offen lds\n"
MF "v[112:115],a[148:151],v[176:179],v[112:115]\n"
TR "a[244:245],v5 offset:8192\n"
MF "v[112:115],a[188:191],v[180:183],v[112:115]\n"
TR "a[246:247],v5 offset:10240\n"
MF "v[112:115],a[228:231],v[184:187],v[112:115]\n"
LD "v197,s[48:51],s88 offen offset:1024 lds\n"
MF "v[120:123],a[232:235],v[184:187],v[120:123]\n"
MF "v[120:123],a[192:195],v[180:183],v[120:123]\n"
MF "v[120:123],a[152:155],v[176:179],v[120:123]\n"
LD "v[208:211],v9,s[40:43],s82 offen sc1\n"
MF "v[128:131],a[156:159],v[176:179],v[128:131]\n"
MF "v[128:131],a[196:199],v[180:183],v[128:131]\n"
LD "v198,s[48:51],s88 offen offset:2048 lds\n"
MF "v[128:131],a[236:239],v[184:187],v[128:131]\n"
"s_waitcnt lgkmcnt(0)\n"
MF "v[132:135],a[236:239],a[244:247],v[132:135]\n"
TR "v[176:177],v6\n"
MF "v[132:135],a[196:199],a[240:243],v[132:135]\n"
TR "v[178:179],v6 offset:2048\n"
MF "v[132:135],a[156:159],v[188:191],v[132:135]\n"
LD "v199,s[48:51],s88 offen offset:3072 lds\n"
MF "v[124:127],a[152:155],v[188:191],v[124:127]\n"
TR "v[180:181],v6 offset:4096\n"
MF "v[124:127],a[192:195],a[240:243],v[124:127]\n"
TR "v[182:183],v6 offset:6144\n"
MF "v[124:127],a[232:235],a[244:247],v[124:127]\n"
LD "v[212:215],v9,s[40:43],s82 offen offset:1024 sc1\n"
MF "v[116:119],a[228:231],a[244:247],v[116:119]\n"
TR "v[184:185],v6 offset:8192\n"
MF "v[116:119],a[188:191],a[240:243],v[116:119]\n"
"s_add_u32 m0,s79,57344\n"
TR "v[186:187],v6 offset:10240\n"
MF "v[116:119],a[148:151],v[188:191],v[116:119]\n"
LD "v200,s[48:51],s88 offen lds\n"
MF "v[108:111],a[144:147],v[188:191],v[108:111]\n"
MF "v[108:111],a[184:187],a[240:243],v[108:111]\n"
MF "v[108:111],a[224:227],a[244:247],v[108:111]\n"
MF "v[100:103],a[220:223],a[244:247],v[100:103]\n"
LD "v201,s[48:51],s88 offen offset:1024 lds\n"
MF "v[100:103],a[180:183],a[240:243],v[100:103]\n"
MF "v[100:103],a[140:143],v[188:191],v[100:103]\n"
"s_waitcnt lgkmcnt(0)\n"
LD "v[216:219],v9,s[40:43],s82 offen offset:2048 sc1\n"
MF "v[136:139],a[140:143],v[176:179],v[136:139]\n"
TR "v[188:189],v7\n"
MF "v[136:139],a[180:183],v[180:183],v[136:139]\n"
TR "v[190:191],v7 offset:2048\n"
MF "v[136:139],a[220:223],v[184:187],v[136:139]\n"
LD "v202,s[48:51],s88 offen offset:2048 lds\n"
MF "v[144:147],a[224:227],v[184:187],v[144:147]\n"
TR "a[240:241],v7 offset:4096\n"
MF "v[144:147],a[184:187],v[180:183],v[144:147]\n"
TR "a[242:243],v7 offset:6144\n"
MF "v[144:147],a[144:147],v[176:179],v[144:147]\n"
LD "v203,s[48:51],s88 offen offset:3072 lds\n"
MF "v[152:155],a[148:151],v[176:179],v[152:155]\n"
TR "a[244:245],v7 offset:8192\n"
MF "v[152:155],a[188:191],v[180:183],v[152:155]\n"
TR "a[246:247],v7 offset:10240\n"
MF "v[152:155],a[228:231],v[184:187],v[152:155]\n"
LD "v[220:223],v9,s[40:43],s82 offen offset:3072 sc1\n"
MF "v[160:163],a[232:235],v[184:187],v[160:163]\n"
MF "v[160:163],a[192:195],v[180:183],v[160:163]\n"
MF "v[160:163],a[152:155],v[176:179],v[160:163]\n"
MF "v[168:171],a[156:159],v[176:179],v[168:171]\n"
"s_add_u32 s82,s82,4096\n"
MF "v[168:171],a[196:199],v[180:183],v[168:171]\n"
"s_waitcnt vmcnt(30) lgkmcnt(0)\n"
LD "v[224:227],v9,s[40:43],s82 offen sc1\n"
MF "v[168:171],a[236:239],v[184:187],v[168:171]\n"
"s_barrier\n"
MF "v[172:175],a[236:239],a[244:247],v[172:175]\n"
"s_add_u32 s80,s88,122880\n"
TR "v[176:177],v0 offset:24576\n"
MF "v[172:175],a[196:199],a[240:243],v[172:175]\n"
"s_add_u32 m0,s79,98304\n"
"s_add_u32 s85,s85,1\n"
TR "v[178:179],v0 offset:26624\n"
MF "v[172:175],a[156:159],v[188:191],v[172:175]\n"
LD "v192,s[48:51],s80 offen lds\n"
MF "v[164:167],a[152:155],v[188:191],v[164:167]\n"
"s_nop 0\n"
TR "v[180:181],v0 offset:28672\n"
"ds_write_addtid_b32 v96 offset:24576\n"
MF "v[164:167],a[192:195],a[240:243],v[164:167]\n"
"s_nop 0\n"
TR "v[182:183],v0 offset:30720\n"
"ds_write_addtid_b32 v97 offset:24832\n"
MF "v[164:167],a[232:235],a[244:247],v[164:167]\n"
LD "v193,s[48:51],s80 offen offset:1024 lds\n"
MF "v[156:159],a[228:231],a[244:247],v[156:159]\n"
"s_nop 0\n"
TR "v[184:185],v0 offset:32768\n"
"ds_write_addtid_b32 v98 offset:25088\n"
MF "v[156:159],a[188:191],a[240:243],v[156:159]\n"
"s_nop 0\n"
TR "v[186:187],v0 offset:34816\n"
"ds_write_addtid_b32 v99 offset:25344\n"
MF "v[156:159],a[148:151],v[188:191],v[156:159]\n"
"s_nop 0\n"
"ds_write_addtid_b32 v100 offset:25600\n"
"ds_write_addtid_b32 v101 offset:25856\n"
MF "v[148:151],a[144:147],v[188:191],v[148:151]\n"
LD "v194,s[48:51],s80 offen offset:2048 lds\n"
MF "v[148:151],a[184:187],a[240:243],v[148:151]\n"
"s_nop 0\n"
"ds_write_addtid_b32 v102 offset:26112\n"
"ds_write_addtid_b32 v103 offset:26368\n"
MF "v[148:151],a[224:227],a[244:247],v[148:151]\n"
"s_nop 0\n"
"ds_write_addtid_b32 v104 offset:26624\n"
"ds_write_addtid_b32 v105 offset:26880\n"
MF "v[140:143],a[220:223],a[244:247],v[140:143]\n"
"s_nop 0\n"
"ds_write_addtid_b32 v106 offset:27136\n"
"ds_write_addtid_b32 v107 offset:27392\n"
MF "v[140:143],a[180:183],a[240:243],v[140:143]\n"
MF "v[140:143],a[140:143],v[188:191],v[140:143]\n"
"s_waitcnt lgkmcnt(9)\n"
MF "v[16:19],a[0:3],v[176:179],v[252:255]\n"
"s_nop 0\n"
TR "v[188:189],v1 offset:24576\n"
"ds_write_addtid_b32 v108 offset:27648\n"
MF "v[16:19],a[40:43],v[180:183],v[16:19]\n"
"s_nop 0\n"
TR "v[190:191],v1 offset:26624\n"
"ds_write_addtid_b32 v109 offset:27904\n"
MF "v[16:19],a[80:83],v[184:187],v[16:19]\n"
LD "v195,s[48:51],s80 offen offset:3072 lds\n"
MF "v[24:27],a[84:87],v[184:187],v[252:255]\n"
"s_add_u32 s60,s64,1\n"
TR "a[240:241],v1 offset:28672\n"
"ds_write_addtid_b32 v110 offset:28160\n"
MF "v[24:27],a[44:47],v[180:183],v[24:27]\n"
"s_add_u32 m0,s79,102400\n"
TR "a[242:243],v1 offset:30720\n"
"ds_write_addtid_b32 v111 offset:24320\n"
MF "v[24:27],a[4:7],v[176:179],v[24:27]\n"
LD "v196,s[48:51],s80 offen lds\n"
MF "v[32:35],a[8:11],v[176:179],v[252:255]\n"
"s_cmp_eq_u32 s60,8\n"
"s_cselect_b32 s60,0,s60\n"
TR "a[244:245],v1 offset:32768\n"
MF "v[32:35],a[48:51],v[180:183],v[32:35]\n"
"s_cmp_eq_u32 s60,10\n"
"s_cselect_b32 s60,s61,s60\n"
TR "a[246:247],v1 offset:34816\n"
MF "v[32:35],a[88:91],v[184:187],v[32:35]\n"
"s_sub_u32 s65,s60,s64\n"
"ds_write_addtid_b32 v112 offset:24576\n"
"ds_write_addtid_b32 v113 offset:24832\n"
MF "v[40:43],a[92:95],v[184:187],v[252:255]\n"
"s_lshl_b32 s65,s65,7\n"
"ds_write_addtid_b32 v114 offset:25088\n"
"ds_write_addtid_b32 v115 offset:25344\n"
MF "v[40:43],a[52:55],v[180:183],v[40:43]\n"
LD "v197,s[48:51],s80 offen offset:1024 lds\n"
MF "v[40:43],a[12:15],v[176:179],v[40:43]\n"
"s_mov_b32 s64,s60\n"
"ds_write_addtid_b32 v116 offset:25600\n"
"ds_write_addtid_b32 v117 offset:25856\n"
MF "v[48:51],a[16:19],v[176:179],v[252:255]\n"
"s_nop 0\n"
"ds_write_addtid_b32 v118 offset:26112\n"
"ds_write_addtid_b32 v119 offset:26368\n"
MF "v[48:51],a[56:59],v[180:183],v[48:51]\n"
"s_nop 0\n"
"ds_write_addtid_b32 v120 offset:26624\n"
"ds_write_addtid_b32 v121 offset:26880\n"
MF "v[48:51],a[96:99],v[184:187],v[48:51]\n"
"s_waitcnt lgkmcnt(10)\n"
LD "v198,s[48:51],s80 offen offset:2048 lds\n"
MF "v[52:55],a[96:99],a[244:247],v[252:255]\n"
"s_nop 0\n"
TR "v[176:177],v2 offset:24576\n"
"ds_write_addtid_b32 v122 offset:27136\n"
MF "v[52:55],a[56:59],a[240:243],v[52:55]\n"
"s_nop 0\n"
TR "v[178:179],v2 offset:26624\n"
"ds_write_addtid_b32 v123 offset:27392\n"
MF "v[52:55],a[16:19],v[188:191],v[52:55]\n"
"s_nop 0\n"
"ds_write_addtid_b32 v124 offset:27648\n"
"ds_write_addtid_b32 v125 offset:27904\n"
MF "v[44:47],a[12:15],v[188:191],v[252:255]\n"
"s_nop 0\n"
TR "v[180:181],v2 offset:28672\n"
"ds_write_addtid_b32 v126 offset:28160\n"
MF "v[44:47],a[52:55],a[240:243],v[44:47]\n"
"s_nop 0\n"
TR "v[182:183],v2 offset:30720\n"
"ds_write_addtid_b32 v127 offset:28416\n"
MF "v[44:47],a[92:95],a[244:247],v[44:47]\n"
LD "v199,s[48:51],s80 offen offset:3072 lds\n"
MF "v[36:39],a[88:91],a[244:247],v[252:255]\n"
"s_nop 0\n"
TR "v[184:185],v2 offset:32768\n"
"ds_write_addtid_b32 v128 offset:28672\n"
MF "v[36:39],a[48:51],a[240:243],v[36:39]\n"
"s_add_u32 m0,s79,106496\n"
TR "v[186:187],v2 offset:34816\n"
"ds_write_addtid_b32 v129 offset:24832\n"
MF "v[36:39],a[8:11],v[188:191],v[36:39]\n"
LD "v200,s[48:51],s80 offen lds\n"
MF "v[28:31],a[4:7],v[188:191],v[252:255]\n"
"s_nop 0\n"
"ds_write_addtid_b32 v130 offset:25088\n"
"ds_write_addtid_b32 v131 offset:25344\n"
MF "v[28:31],a[44:47],a[240:243],v[28:31]\n"
"s_nop 0\n"
"ds_write_addtid_b32 v132 offset:25600\n"
"ds_write_addtid_b32 v133 offset:25856\n"
MF "v[28:31],a[84:87],a[244:247],v[28:31]\n"
"s_nop 0\n"
"ds_write_addtid_b32 v134 offset:26112\n"
"ds_write_addtid_b32 v135 offset:26368\n"
MF "v[20:23],a[80:83],a[244:247],v[252:255]\n"
MF "v[20:23],a[40:43],a[240:243],v[20:23]\n"
LD "v201,s[48:51],s80 offen offset:1024 lds\n"
MF "v[20:23],a[0:3],v[188:191],v[20:23]\n"
"s_waitcnt lgkmcnt(7)\n"
MF "v[56:59],a[0:3],v[176:179],v[252:255]\n"
TR "v[188:189],v3 offset:24576\n"
MF "v[56:59],a[40:43],v[180:183],v[56:59]\n"
TR "v[190:191],v3 offset:26624\n"
MF "v[56:59],a[80:83],v[184:187],v[56:59]\n"
LD "v202,s[48:51],s80 offen offset:2048 lds\n"
MF "v[64:67],a[84:87],v[184:187],v[252:255]\n"
TR "a[240:241],v3 offset:28672\n"
MF "v[64:67],a[44:47],v[180:183],v[64:67]\n"
TR "a[242:243],v3 offset:30720\n"
MF "v[64:67],a[4:7],v[176:179],v[64:67]\n"
LD "v203,s[48:51],s80 offen offset:3072 lds\n"
MF "v[72:75],a[8:11],v[176:179],v[252:255]\n"
TR "a[244:245],v3 offset:32768\n"
MF "v[72:75],a[48:51],v[180:183],v[72:75]\n"
TR "a[246:247],v3 offset:34816\n"
MF "v[72:75],a[88:91],v[184:187],v[72:75]\n"
MF "v[80:83],a[92:95],v[184:187],v[252:255]\n"
MF "v[80:83],a[52:55],v[180:183],v[80:83]\n"
MF "v[80:83],a[12:15],v[176:179],v[80:83]\n"
MF "v[88:91],a[16:19],v[176:179],v[252:255]\n"
MF "v[88:91],a[56:59],v[180:183],v[88:91]\n"
"s_waitcnt vmcnt(31) lgkmcnt(0)\n"
MF "v[88:91],a[96:99],v[184:187],v[88:91]\n"
"s_barrier\n"
MF "v[92:95],a[96:99],a[244:247],v[252:255]\n"
"s_add_u32 s89,s89,s65\n"
TR "v[176:177],v0\n"
MF "v[92:95],a[56:59],a[240:243],v[92:95]\n"
"s_add_u32 m0,s79,24576\n"
TR "v[178:179],v0 offset:2048\n"
MF "v[92:95],a[16:19],v[188:191],v[92:95]\n"
LD "v192,s[48:51],s89 offen lds\n"
MF "v[84:87],a[12:15],v[188:191],v[252:255]\n"
TR "v[180:181],v0 offset:4096\n"
MF "v[84:87],a[52:55],a[240:243],v[84:87]\n"
TR "v[182:183],v0 offset:6144\n"
MF "v[84:87],a[92:95],a[244:247],v[84:87]\n"
LD "v193,s[48:51],s89 offen offset:1024 lds\n"
MF "v[76:79],a[88:91],a[244:247],v[252:255]\n"
TR "v[184:185],v0 offset:8192\n"
MF "v[76:79],a[48:51],a[240:243],v[76:79]\n"
"s_nop 0\n"
TR "v[186:187],v0 offset:10240\n"
"ds_write_addtid_b32 v136 offset:49152\n"
MF "v[76:79],a[8:11],v[188:191],v[76:79]\n"
"s_nop 0\n"
"ds_write_addtid_b32 v137 offset:49408\n"
"ds_write_addtid_b32 v138 offset:49664\n"
MF "v[68:71],a[4:7],v[188:191],v[252:255]\n"
".Lpg29h:\n"
"s_cbranch_execz .Lpg32h\n"
LD "v194,s[48:51],s89 offen offset:2048 lds\n"
MF "v[68:71],a[44:47],a[240:243],v[68:71]\n"
"buffer_load_dword v10,v12,s[40:43],s86 offen sc1\n"
MF "v[68:71],a[84:87],a[244:247],v[68:71]\n"
"s_nop 0\n"
"ds_write_addtid_b32 v139 offset:49920\n"
"ds_write_addtid_b32 v140 offset:50176\n"
MF "v[60:63],a[80:83],a[244:247],v[252:255]\n"
"s_nop 0\n"
"ds_write_addtid_b32 v141 offset:50432\n"
"ds_write_addtid_b32 v142 offset:50688\n"
MF "v[60:63],a[40:43],a[240:243],v[60:63]\n"
"s_nop 0\n"
"ds_write_addtid_b32 v143 offset:50944\n"
"ds_write_addtid_b32 v144 offset:51200\n"
MF "v[60:63],a[0:3],v[188:191],v[60:63]\n"
"s_waitcnt lgkmcnt(9)\n"
LD "v195,s[48:51],s89 offen offset:3072 lds\n"
MF "v[16:19],a[120:123],v[176:179],v[16:19]\n"
"s_nop 0\n"
TR "v[188:189],v1\n"
"ds_write_addtid_b32 v145 offset:51456\n"
MF "v[16:19],a[160:163],v[180:183],v[16:19]\n"
"s_nop 0\n"
TR "v[190:191],v1 offset:2048\n"
"ds_write_addtid_b32 v146 offset:51712\n"
MF "v[16:19],a[200:203],v[184:187],v[16:19]\n"
"s_nop 0\n"
"ds_write_addtid_b32 v147 offset:51968\n"
"ds_write_addtid_b32 v148 offset:52224\n"
MF "v[24:27],a[204:207],v[184:187],v[24:27]\n"
"s_nop 0\n"
TR "a[240:241],v1 offset:4096\n"
"ds_write_addtid_b32 v149 offset:52480\n"
MF "v[24:27],a[164:167],v[180:183],v[24:27]\n"
"s_add_u32 m0,s79,28672\n"
TR "a[242:243],v1 offset:6144\n"
"ds_write_addtid_b32 v150 offset:48640\n"
MF "v[24:27],a[124:127],v[176:179],v[24:27]\n"
LD "v196,s[48:51],s89 offen lds\n"
MF "v[32:35],a[128:131],v[176:179],v[32:35]\n"
"s_nop 0\n"
TR "a[244:245],v1 offset:8192\n"
"ds_write_addtid_b32 v151 offset:48896\n"
MF "v[32:35],a[168:171],v[180:183],v[32:35]\n"
"s_nop 0\n"
TR "a[246:247],v1 offset:10240\n"
"ds_write_addtid_b32 v152 offset:49152\n"
MF "v[32:35],a[208:211],v[184:187],v[32:35]\n"
LD "v197,s[48:51],s89 offen offset:1024 lds\n"
MF "v[40:43],a[212:215],v[184:187],v[40:43]\n"
"s_nop 0\n"
"ds_write_addtid_b32 v153 offset:49408\n"
"ds_write_addtid_b32 v154 offset:49664\n"
MF "v[40:43],a[172:175],v[180:183],v[40:43]\n"
"s_nop 0\n"
"ds_write_addtid_b32 v155 offset:49920\n"
"ds_write_addtid_b32 v156 offset:50176\n"
MF "v[40:43],a[132:135],v[176:179],v[40:43]\n"
"s_nop 0\n"
"ds_write_addtid_b32 v157 offset:50432\n"
"ds_write_addtid_b32 v158 offset:50688\n"
MF "v[48:51],a[136:139],v[176:179],v[48:51]\n"
"s_nop 0\n"
"ds_write_addtid_b32 v159 offset:50944\n"
"ds_write_addtid_b32 v160 offset:51200\n"
MF "v[48:51],a[176:179],v[180:183],v[48:51]\n"
LD "v198,s[48:51],s89 offen offset:2048 lds\n"
MF "v[48:51],a[216:219],v[184:187],v[48:51]\n"
"s_waitcnt lgkmcnt(9)\n"
"ds_write_addtid_b32 v161 offset:51456\n"
"ds_write_addtid_b32 v162 offset:51712\n"
MF "v[52:55],a[216:219],a[244:247],v[52:55]\n"
"s_nop 0\n"
TR "v[176:177],v2\n"
"ds_write_addtid_b32 v163 offset:51968\n"
MF "v[52:55],a[176:179],a[240:243],v[52:55]\n"
"s_nop 0\n"
TR "v[178:179],v2 offset:2048\n"
"ds_write_addtid_b32 v164 offset:52224\n"
MF "v[52:55],a[136:139],v[188:191],v[52:55]\n"
LD "v199,s[48:51],s89 offen offset:3072 lds\n"
MF "v[44:47],a[132:135],v[188:191],v[44:47]\n"
"s_nop 0\n"
TR "v[180:181],v2 offset:4096\n"
"ds_write_addtid_b32 v165 offset:52480\n"
MF "v[44:47],a[172:175],a[240:243],v[44:47]\n"
"s_nop 0\n"
TR "v[182:183],v2 offset:6144\n"
"ds_write_addtid_b32 v166 offset:52736\n"
MF "v[44:47],a[212:215],a[244:247],v[44:47]\n"
"s_nop 0\n"
"ds_write_addtid_b32 v167 offset:52992\n"
"ds_write_addtid_b32 v168 offset:53248\n"
MF "v[36:39],a[208:211],a[244:247],v[36:39]\n"
"s_nop 0\n"
TR "v[184:185],v2 offset:8192\n"
"ds_write_addtid_b32 v169 offset:53504\n"
MF "v[36:39],a[168:171],a[240:243],v[36:39]\n"
"s_add_u32 m0,s79,32768\n"
TR "v[186:187],v2 offset:10240\n"
"ds_write_addtid_b32 v170 offset:49664\n"
MF "v[36:39],a[128:131],v[188:191],v[36:39]\n"
LD "v200,s[48:51],s89 offen lds\n"
MF "v[28:31],a[124:127],v[188:191],v[28:31]\n"
"s_nop 0\n"
"ds_write_addtid_b32 v171 offset:49920\n"
"ds_write_addtid_b32 v172 offset:50176\n"
MF "v[28:31],a[164:167],a[240:243],v[28:31]\n"
"s_nop 0\n"
"ds_write_addtid_b32 v173 offset:50432\n"
"ds_write_addtid_b32 v174 offset:50688\n"
MF "v[28:31],a[204:207],a[244:247],v[28:31]\n"
"ds_write_addtid_b32 v175 offset:50944\n"
MF "v[20:23],a[200:203],a[244:247],v[20:23]\n"
LD "v201,s[48:51],s89 offen offset:1024 lds\n"
MF "v[20:23],a[160:163],a[240:243],v[20:23]\n"
MF "v[20:23],a[120:123],v[188:191],v[20:23]\n"
"s_waitcnt lgkmcnt(6)\n"
MF "v[56:59],a[120:123],v[176:179],v[56:59]\n"
TR "v[188:189],v3\n"
MF "v[56:59],a[160:163],v[180:183],v[56:59]\n"
TR "v[190:191],v3 offset:2048\n"
MF "v[56:59],a[200:203],v[184:187],v[56:59]\n"
LD "v202,s[48:51],s89 offen offset:2048 lds\n"
MF "v[64:67],a[204:207],v[184:187],v[64:67]\n"
TR "a[240:241],v3 offset:4096\n"
MF "v[64:67],a[164:167],v[180:183],v[64:67]\n"
TR "a[242:243],v3 offset:6144\n"
MF "v[64:67],a[124:127],v[176:179],v[64:67]\n"
LD "v203,s[48:51],s89 offen offset:3072 lds\n"
MF "v[72:75],a[128:131],v[176:179],v[72:75]\n"
TR "a[244:245],v3 offset:8192\n"
MF "v[72:75],a[168:171],v[180:183],v[72:75]\n"
TR "a[246:247],v3 offset:10240\n"
MF "v[72:75],a[208:211],v[184:187],v[72:75]\n"
MF "v[80:83],a[212:215],v[184:187],v[80:83]\n"
MF "v[80:83],a[172:175],v[180:183],v[80:83]\n"
MF "v[80:83],a[132:135],v[176:179],v[80:83]\n"
MF "v[88:91],a[136:139],v[176:179],v[88:91]\n"
MF "v[88:91],a[176:179],v[180:183],v[88:91]\n"
"s_waitcnt vmcnt(27) lgkmcnt(0)\n"
MF "v[88:91],a[216:219],v[184:187],v[88:91]\n"
"s_barrier\n"
MF "v[92:95],a[216:219],a[244:247],v[92:95]\n"
"s_add_u32 s80,s89,122880\n"
TR "v[176:177],v0 offset:49152\n"
MF "v[92:95],a[176:179],a[240:243],v[92:95]\n"
"s_add_u32 m0,s79,0\n"
"s_mov_b32 s82,s70\n"
TR "v[178:179],v0 offset:51200\n"
MF "v[92:95],a[136:139],v[188:191],v[92:95]\n"
LD "v192,s[48:51],s80 offen lds\n"
MF "v[84:87],a[132:135],v[188:191],v[84:87]\n"
TR "v[180:181],v0 offset:53248\n"
MF "v[84:87],a[172:175],a[240:243],v[84:87]\n"
TR "v[182:183],v0 offset:55296\n"
MF "v[84:87],a[212:215],a[244:247],v[84:87]\n"
LD "v193,s[48:51],s80 offen offset:1024 lds\n"
MF "v[76:79],a[208:211],a[244:247],v[76:79]\n"
TR "v[184:185],v0 offset:57344\n"
MF "v[76:79],a[168:171],a[240:243],v[76:79]\n"
TR "v[186:187],v0 offset:59392\n"
MF "v[76:79],a[128:131],v[188:191],v[76:79]\n"
MF "v[68:71],a[124:127],v[188:191],v[68:71]\n"
MF "v[68:71],a[164:167],a[240:243],v[68:71]\n"
LD "v194,s[48:51],s80 offen offset:2048 lds\n"
MF "v[68:71],a[204:207],a[244:247],v[68:71]\n"
MF "v[60:63],a[200:203],a[244:247],v[60:63]\n"
MF "v[60:63],a[160:163],a[240:243],v[60:63]\n"
MF "v[60:63],a[120:123],v[188:191],v[60:63]\n"
"s_waitcnt lgkmcnt(0)\n"
LD "v195,s[48:51],s80 offen offset:3072 lds\n"
MF "v[16:19],a[20:23],v[176:179],v[16:19]\n"
TR "v[188:189],v1 offset:49152\n"
MF "v[16:19],a[60:63],v[180:183],v[16:19]\n"
TR "v[190:191],v1 offset:51200\n"
MF "v[16:19],a[100:103],v[184:187],v[16:19]\n"
MF "v[24:27],a[104:107],v[184:187],v[24:27]\n"
TR "a[240:241],v1 offset:53248\n"
MF "v[24:27],a[64:67],v[180:183],v[24:27]\n"
"s_add_u32 m0,s79,4096\n"
TR "a[242:243],v1 offset:55296\n"
MF "v[24:27],a[24:27],v[176:179],v[24:27]\n"
LD "v196,s[48:51],s80 offen lds\n"
MF "v[32:35],a[28:31],v[176:179],v[32:35]\n"
TR "a[244:245],v1 offset:57344\n"
MF "v[32:35],a[68:71],v[180:183],v[32:35]\n"
TR "a[246:247],v1 offset:59392\n"
MF "v[32:35],a[108:111],v[184:187],v[32:35]\n"
LD "v197,s[48:51],s80 offen offset:1024 lds\n"
MF "v[40:43],a[112:115],v[184:187],v[40:43]\n"
MF "v[40:43],a[72:75],v[180:183],v[40:43]\n"
MF "v[40:43],a[32:35],v[176:179],v[40:43]\n"
MF "v[48:51],a[36:39],v[176:179],v[48:51]\n"
MF "v[48:51],a[76:79],v[180:183],v[48:51]\n"
LD "v198,s[48:51],s80 offen offset:2048 lds\n"
MF "v[48:51],a[116:119],v[184:187],v[48:51]\n"
"s_waitcnt lgkmcnt(0)\n"
MF "v[52:55],a[116:119],a[244:247],v[52:55]\n"
TR "v[176:177],v2 offset:49152\n"
MF "v[52:55],a[76:79],a[240:243],v[52:55]\n"
TR "v[178:179],v2 offset:51200\n"
MF "v[52:55],a[36:39],v[188:191],v[52:55]\n"
LD "v199,s[48:51],s80 offen offset:3072 lds\n"
MF "v[44:47],a[32:35],v[188:191],v[44:47]\n"
TR "v[180:181],v2 offset:53248\n"
MF "v[44:47],a[72:75],a[240:243],v[44:47]\n"
TR "v[182:183],v2 offset:55296\n"
MF "v[44:47],a[112:115],a[244:247],v[44:47]\n"
MF "v[36:39],a[108:111],a[244:247],v[36:39]\n"
TR "v[184:185],v2 offset:57344\n"
MF "v[36:39],a[68:71],a[240:243],v[36:39]\n"
"s_add_u32 m0,s79,8192\n"
TR "v[186:187],v2 offset:59392\n"
MF "v[36:39],a[28:31],v[188:191],v[36:39]\n"
LD "v200,s[48:51],s80 offen lds\n"
MF "v[28:31],a[24:27],v[188:191],v[28:31]\n"
MF "v[28:31],a[64:67],a[240:243],v[28:31]\n"
MF "v[28:31],a[104:107],a[244:247],v[28:31]\n"
MF "v[20:23],a[100:103],a[244:247],v[20:23]\n"
LD "v201,s[48:51],s80 offen offset:1024 lds\n"
MF "v[20:23],a[60:63],a[240:243],v[20:23]\n"
"s_waitcnt vmcnt(19)\n"
MF "v[20:23],a[20:23],v[188:191],v[20:23]\n"
"v_mov_b32 v236,v228\n"
"v_mov_b32 v237,v229\n"
MF "v[16:19],a[252:255],v[208:211],v[16:19]\n"
MF "v[24:27],a[252:255],v[212:215],v[24:27]\n"
MF "v[32:35],a[252:255],v[216:219],v[32:35]\n"
MF "v[40:43],a[252:255],v[220:223],v[40:43]\n"
MF "v[48:51],a[252:255],v[224:227],v[48:51]\n"
MF "v[20:23],a[248:251],v[208:211],v[20:23]\n"
MF "v[28:31],a[248:251],v[212:215],v[28:31]\n"
MF "v[36:39],a[248:251],v[216:219],v[36:39]\n"
MF "v[44:47],a[248:251],v[220:223],v[44:47]\n"
MF "v[52:55],a[248:251],v[224:227],v[52:55]\n"
MF "v[248:251],v[230:233],a[250:253],v[248:251]\n"
MF "v[244:247],v[234:237],a[250:253],v[244:247]\n"
"s_waitcnt lgkmcnt(0)\n"
MF "v[56:59],a[20:23],v[176:179],v[56:59]\n"
TR "v[188:189],v3 offset:49152\n"
"v_sub_u32 v10,v10,v11\n"
MF "v[56:59],a[60:63],v[180:183],v[56:59]\n"
TR "v[190:191],v3 offset:51200\n"
MF "v[56:59],a[100:103],v[184:187],v[56:59]\n"
LD "v202,s[48:51],s80 offen offset:2048 lds\n"
MF "v[64:67],a[104:107],v[184:187],v[64:67]\n"
TR "a[240:241],v3 offset:53248\n"
MF "v[64:67],a[64:67],v[180:183],v[64:67]\n"
TR "a[242:243],v3 offset:55296\n"
MF "v[64:67],a[24:27],v[176:179],v[64:67]\n"
LD "v203,s[48:51],s80 offen offset:3072 lds\n"
MF "v[72:75],a[28:31],v[176:179],v[72:75]\n"
TR "a[244:245],v3 offset:57344\n"
MF "v[72:75],a[68:71],v[180:183],v[72:75]\n"
TR "a[246:247],v3 offset:59392\n"
MF "v[72:75],a[108:111],v[184:187],v[72:75]\n"
".Lnf2:\n"
"v_readlane_b32 s58,v10,s67\n"
"s_cmp_lt_u32 s58,s85\n"
"s_cbranch_scc1 .Lsp2\n"
".Lnb2:\n"
MF "v[80:83],a[112:115],v[184:187],v[80:83]\n"
"s_add_u32 s70,s70,20480\n"
MF "v[80:83],a[72:75],v[180:183],v[80:83]\n"
MF "v[80:83],a[32:35],v[176:179],v[80:83]\n"
MF "v[88:91],a[36:39],v[176:179],v[88:91]\n"
"s_add_u32 s2,s2,204800\n"
MF "v[88:91],a[76:79],v[180:183],v[88:91]\n"
"s_waitcnt vmcnt(25) lgkmcnt(0)\n"
LD "v[228:231],v9,s[40:43],s2 offen sc1\n"
MF "v[88:91],a[116:119],v[184:187],v[88:91]\n"
"s_barrier\n"
MF "v[92:95],a[116:119],a[244:247],v[92:95]\n"
"s_add_u32 s88,s88,s65\n"
TR "v[176:177],v4\n"
MF "v[92:95],a[76:79],a[240:243],v[92:95]\n"
"s_add_u32 m0,s79,49152\n"
TR "v[178:179],v4 offset:2048\n"
MF "v[92:95],a[36:39],v[188:191],v[92:95]\n"
LD "v192,s[48:51],s88 offen lds\n"
MF "v[84:87],a[32:35],v[188:191],v[84:87]\n"
TR "v[180:181],v4 offset:4096\n"
MF "v[84:87],a[72:75],a[240:243],v[84:87]\n"
TR "v[182:183],v4 offset:6144\n"
MF "v[84:87],a[112:115],a[244:247],v[84:87]\n"
LD "v193,s[48:51],s88 offen offset:1024 lds\n"
MF "v[76:79],a[108:111],a[244:247],v[76:79]\n"
TR "v[184:185],v4 offset:8192\n"
MF "v[76:79],a[68:71],a[240:243],v[76:79]\n"
"s_add_u32 s2,s2,204800\n"
TR "v[186:187],v4 offset:10240\n"
MF "v[76:79],a[28:31],v[188:191],v[76:79]\n"
LD "v[232:235],v9,s[40:43],s2 offen sc1\n"
MF "v[68:71],a[24:27],v[188:191],v[68:71]\n"
MF "v[68:71],a[64:67],a[240:243],v[68:71]\n"
LD "v194,s[48:51],s88 offen offset:2048 lds\n"
MF "v[68:71],a[104:107],a[244:247],v[68:71]\n"
MF "v[60:63],a[100:103],a[244:247],v[60:63]\n"
MF "v[60:63],a[60:63],a[240:243],v[60:63]\n"
MF "v[60:63],a[20:23],v[188:191],v[60:63]\n"
"s_waitcnt lgkmcnt(0)\n"
LD "v195,s[48:51],s88 offen offset:3072 lds\n"
MF "v[16:19],a[140:143],v[176:179],v[16:19]\n"
TR "v[188:189],v5\n"
MF "v[16:19],a[180:183],v[180:183],v[16:19]\n"
TR "v[190:191],v5 offset:2048\n"
MF "v[16:19],a[220:223],v[184:187],v[16:19]\n"
LD "v[208:211],v9,s[40:43],s82 offen sc1\n"
MF "v[24:27],a[224:227],v[184:187],v[24:27]\n"
TR "a[240:241],v5 offset:4096\n"
MF "v[24:27],a[184:187],v[180:183],v[24:27]\n"
"s_add_u32 m0,s79,53248\n"
TR "a[242:243],v5 offset:6144\n"
MF "v[24:27],a[144:147],v[176:179],v[24:27]\n"
LD "v196,s[48:51],s88 offen lds\n"
MF "v[32:35],a[148:151],v[176:179],v[32:35]\n"
TR "a[244:245],v5 offset:8192\n"
MF "v[32:35],a[188:191],v[180:183],v[32:35]\n"
TR "a[246:247],v5 offset:10240\n"
MF "v[32:35],a[228:231],v[184:187],v[32:35]\n"
LD "v197,s[48:51],s88 offen offset:1024 lds\n"
MF "v[40:43],a[232:235],v[184:187],v[40:43]\n"
MF "v[40:43],a[192:195],v[180:183],v[40:43]\n"
MF "v[40:43],a[152:155],v[176:179],v[40:43]\n"
MF "v[48:51],a[156:159],v[176:179],v[48:51]\n"
LD "v[212:215],v9,s[40:43],s82 offen offset:1024 sc1\n"
MF "v[48:51],a[196:199],v[180:183],v[48:51]\n"
LD "v198,s[48:51],s88 offen offset:2048 lds\n"
MF "v[48:51],a[236:239],v[184:187],v[48:51]\n"
"s_waitcnt lgkmcnt(0)\n"
MF "v[52:55],a[236:239],a[244:247],v[52:55]\n"
TR "v[176:177],v6\n"
MF "v[52:55],a[196:199],a[240:243],v[52:55]\n"
TR "v[178:179],v6 offset:2048\n"
MF "v[52:55],a[156:159],v[188:191],v[52:55]\n"
LD "v199,s[48:51],s88 offen offset:3072 lds\n"
MF "v[44:47],a[152:155],v[188:191],v[44:47]\n"
TR "v[180:181],v6 offset:4096\n"
MF "v[44:47],a[192:195],a[240:243],v[44:47]\n"
TR "v[182:183],v6 offset:6144\n"
MF "v[44:47],a[232:235],a[244:247],v[44:47]\n"
MF "v[36:39],a[228:231],a[244:247],v[36:39]\n"
TR "v[184:185],v6 offset:8192\n"
MF "v[36:39],a[188:191],a[240:243],v[36:39]\n"
"s_add_u32 m0,s79,57344\n"
TR "v[186:187],v6 offset:10240\n"
MF "v[36:39],a[148:151],v[188:191],v[36:39]\n"
LD "v200,s[48:51],s88 offen lds\n"
MF "v[28:31],a[144:147],v[188:191],v[28:31]\n"
LD "v[216:219],v9,s[40:43],s82 offen offset:2048 sc1\n"
MF "v[28:31],a[184:187],a[240:243],v[28:31]\n"
MF "v[28:31],a[224:227],a[244:247],v[28:31]\n"
MF "v[20:23],a[220:223],a[244:247],v[20:23]\n"
LD "v201,s[48:51],s88 offen offset:1024 lds\n"
MF "v[20:23],a[180:183],a[240:243],v[20:23]\n"
MF "v[20:23],a[140:143],v[188:191],v[20:23]\n"
"s_waitcnt lgkmcnt(0)\n"
MF "v[56:59],a[140:143],v[176:179],v[56:59]\n"
TR "v[188:189],v7\n"
MF "v[56:59],a[180:183],v[180:183],v[56:59]\n"
TR "v[190:191],v7 offset:2048\n"
MF "v[56:59],a[220:223],v[184:187],v[56:59]\n"
LD "v202,s[48:51],s88 offen offset:2048 lds\n"
MF "v[64:67],a[224:227],v[184:187],v[64:67]\n"
TR "a[240:241],v7 offset:4096\n"
MF "v[64:67],a[184:187],v[180:183],v[64:67]\n"
TR "a[242:243],v7 offset:6144\n"
MF "v[64:67],a[144:147],v[176:179],v[64:67]\n"
LD "v203,s[48:51],s88 offen offset:3072 lds\n"
MF "v[72:75],a[148:151],v[176:179],v[72:75]\n"
TR "a[244:245],v7 offset:8192\n"
MF "v[72:75],a[188:191],v[180:183],v[72:75]\n"
TR "a[246:247],v7 offset:10240\n"
MF "v[72:75],a[228:231],v[184:187],v[72:75]\n"
LD "v[220:223],v9,s[40:43],s82 offen offset:3072 sc1\n"
MF "v[80:83],a[232:235],v[184:187],v[80:83]\n"
MF "v[80:83],a[192:195],v[180:183],v[80:83]\n"
MF "v[80:83],a[152:155],v[176:179],v[80:83]\n"
MF "v[88:91],a[156:159],v[176:179],v[88:91]\n"
"s_add_u32 s82,s82,4096\n"
MF "v[88:91],a[196:199],v[180:183],v[88:91]\n"
"s_waitcnt vmcnt(30) lgkmcnt(0)\n"
LD "v[224:227],v9,s[40:43],s82 offen sc1\n"
MF "v[88:91],a[236:239],v[184:187],v[88:91]\n"
"s_barrier\n"
MF "v[92:95],a[236:239],a[244:247],v[92:95]\n"
"s_add_u32 s80,s88,122880\n"
TR "v[176:177],v0 offset:24576\n"
MF "v[92:95],a[196:199],a[240:243],v[92:95]\n"
"s_add_u32 m0,s79,98304\n"
"s_add_u32 s85,s85,1\n"
TR "v[178:179],v0 offset:26624\n"
MF "v[92:95],a[156:159],v[188:191],v[92:95]\n"
LD "v192,s[48:51],s80 offen lds\n"
MF "v[84:87],a[152:155],v[188:191],v[84:87]\n"
"s_nop 0\n"
TR "v[180:181],v0 offset:28672\n"
"ds_write_addtid_b32 v16 offset:24576\n"
MF "v[84:87],a[192:195],a[240:243],v[84:87]\n"
"s_nop 0\n"
TR "v[182:183],v0 offset:30720\n"
"ds_write_addtid_b32 v17 offset:24832\n"
MF "v[84:87],a[232:235],a[244:247],v[84:87]\n"
LD "v193,s[48:51],s80 offen offset:1024 lds\n"
MF "v[76:79],a[228:231],a[244:247],v[76:79]\n"
"s_nop 0\n"
TR "v[184:185],v0 offset:32768\n"
"ds_write_addtid_b32 v18 offset:25088\n"
MF "v[76:79],a[188:191],a[240:243],v[76:79]\n"
"s_nop 0\n"
TR "v[186:187],v0 offset:34816\n"
"ds_write_addtid_b32 v19 offset:25344\n"
MF "v[76:79],a[148:151],v[188:191],v[76:79]\n"
"s_nop 0\n"
"ds_write_addtid_b32 v20 offset:25600\n"
"ds_write_addtid_b32 v21 offset:25856\n"
MF "v[68:71],a[144:147],v[188:191],v[68:71]\n"
LD "v194,s[48:51],s80 offen offset:2048 lds\n"
MF "v[68:71],a[184:187],a[240:243],v[68:71]\n"
"s_nop 0\n"
"ds_write_addtid_b32 v22 offset:26112\n"
"ds_write_addtid_b32 v23 offset:26368\n"
MF "v[68:71],a[224:227],a[244:247],v[68:71]\n"
"s_nop 0\n"
"ds_write_addtid_b32 v24 offset:26624\n"
"ds_write_addtid_b32 v25 offset:26880\n"
MF "v[60:63],a[220:223],a[244:247],v[60:63]\n"
"s_nop 0\n"
"ds_write_addtid_b32 v26 offset:27136\n"
"ds_write_addtid_b32 v27 offset:27392\n"
MF "v[60:63],a[180:183],a[240:243],v[60:63]\n"
MF "v[60:63],a[140:143],v[188:191],v[60:63]\n"
"s_waitcnt lgkmcnt(9)\n"
"s_cmp_lg_u32 s85,9\n"
"s_cbranch_scc1 .Lpl09\n"
".Lplast0:\n"
MF "v[96:99],v[176:179],a[0:3],v[252:255]\n"
"s_nop 0\n"
TR "v[188:189],v1 offset:24576\n"
TR "v[190:191],v1 offset:26624\n"
MF "v[96:99],v[180:183],a[40:43],v[96:99]\n"
"s_nop 0\n"
TR "a[240:241],v1 offset:28672\n"
TR "a[242:243],v1 offset:30720\n"
MF "v[96:99],v[184:187],a[80:83],v[96:99]\n"
"s_add_u32 s90,s91,163840\n"
TR "a[244:245],v1 offset:32768\n"
TR "a[246:247],v1 offset:34816\n"
MF "v[104:107],v[184:187],a[84:87],v[252:255]\n"
LD "v195,s[48:51],s80 offen offset:3072 lds\n"
MF "v[104:107],v[180:183],a[44:47],v[104:107]\n"
"s_nop 0\n"
"ds_write_addtid_b32 v28 offset:27648\n"
"ds_write_addtid_b32 v29 offset:27904\n"
MF "v[104:107],v[176:179],a[4:7],v[104:107]\n"
"s_nop 0\n"
"ds_write_addtid_b32 v30 offset:28160\n"
"ds_write_addtid_b32 v31 offset:28416\n"
MF "v[112:115],v[176:179],a[8:11],v[252:255]\n"
"s_nop 0\n"
"ds_write_addtid_b32 v32 offset:28672\n"
"ds_write_addtid_b32 v33 offset:28928\n"
MF "v[112:115],v[180:183],a[48:51],v[112:115]\n"
"s_add_u32 m0,s79,102400\n"
"s_nop 0\n"
"ds_write_addtid_b32 v34 offset:25088\n"
"ds_write_addtid_b32 v35 offset:25344\n"
MF "v[112:115],v[184:187],a[88:91],v[112:115]\n"
LD "v196,s[48:51],s80 offen lds\n"
MF "v[120:123],v[184:187],a[92:95],v[252:255]\n"
"s_nop 0\n"
"ds_write_addtid_b32 v36 offset:25600\n"
"ds_write_addtid_b32 v37 offset:25856\n"
MF "v[120:123],v[180:183],a[52:55],v[120:123]\n"
"ds_write_addtid_b32 v38 offset:26112\n"
MF "v[120:123],v[176:179],a[12:15],v[120:123]\n"
"ds_write_addtid_b32 v39 offset:26368\n"
MF "v[128:131],v[176:179],a[16:19],v[252:255]\n"
"ds_write_addtid_b32 v40 offset:26624\n"
MF "v[128:131],v[180:183],a[56:59],v[128:131]\n"
"ds_write_addtid_b32 v41 offset:26880\n"
MF "v[128:131],v[184:187],a[96:99],v[128:131]\n"
"s_waitcnt lgkmcnt(14)\n"
LD "v197,s[48:51],s80 offen offset:1024 lds\n"
MF "v[132:135],a[244:247],a[96:99],v[252:255]\n"
"s_nop 0\n"
TR "v[176:177],v2 offset:24576\n"
TR "v[178:179],v2 offset:26624\n"
MF "v[132:135],a[240:243],a[56:59],v[132:135]\n"
"s_nop 0\n"
TR "v[180:181],v2 offset:28672\n"
TR "v[182:183],v2 offset:30720\n"
MF "v[132:135],v[188:191],a[16:19],v[132:135]\n"
"s_nop 0\n"
TR "v[184:185],v2 offset:32768\n"
TR "v[186:187],v2 offset:34816\n"
MF "v[124:127],v[188:191],a[12:15],v[252:255]\n"
"s_nop 0\n"
"ds_write_addtid_b32 v42 offset:27136\n"
"ds_write_addtid_b32 v43 offset:27392\n"
MF "v[124:127],a[240:243],a[52:55],v[124:127]\n"
"s_nop 0\n"
"ds_write_addtid_b32 v44 offset:27648\n"
"ds_write_addtid_b32 v45 offset:27904\n"
MF "v[124:127],a[244:247],a[92:95],v[124:127]\n"
"s_nop 0\n"
"ds_write_addtid_b32 v46 offset:28160\n"
"ds_write_addtid_b32 v47 offset:28416\n"
MF "v[116:119],a[244:247],a[88:91],v[252:255]\n"
LD "v198,s[48:51],s80 offen offset:2048 lds\n"
MF "v[116:119],a[240:243],a[48:51],v[116:119]\n"
"s_nop 0\n"
"ds_write_addtid_b32 v48 offset:28672\n"
"ds_write_addtid_b32 v49 offset:28928\n"
MF "v[116:119],v[188:191],a[8:11],v[116:119]\n"
"s_nop 0\n"
"ds_write_addtid_b32 v50 offset:29184\n"
"ds_write_addtid_b32 v51 offset:29440\n"
MF "v[108:111],v[188:191],a[4:7],v[252:255]\n"
"ds_write_addtid_b32 v52 offset:29696\n"
MF "v[108:111],a[240:243],a[44:47],v[108:111]\n"
"ds_write_addtid_b32 v53 offset:29952\n"
MF "v[108:111],a[244:247],a[84:87],v[108:111]\n"
"ds_write_addtid_b32 v54 offset:30208\n"
MF "v[100:103],a[244:247],a[80:83],v[252:255]\n"
"ds_write_addtid_b32 v55 offset:30464\n"
MF "v[100:103],a[240:243],a[40:43],v[100:103]\n"
LD "v199,s[48:51],s80 offen offset:3072 lds\n"
MF "v[100:103],v[188:191],a[0:3],v[100:103]\n"
"s_waitcnt lgkmcnt(14)\n"
MF "v[136:139],v[176:179],a[0:3],v[252:255]\n"
"s_nop 0\n"
TR "v[188:189],v3 offset:24576\n"
TR "v[190:191],v3 offset:26624\n"
MF "v[136:139],v[180:183],a[40:43],v[136:139]\n"
"s_add_u32 m0,s79,106496\n"
TR "a[240:241],v3 offset:28672\n"
TR "a[242:243],v3 offset:30720\n"
MF "v[136:139],v[184:187],a[80:83],v[136:139]\n"
"s_nop 0\n"
TR "a[244:245],v3 offset:32768\n"
TR "a[246:247],v3 offset:34816\n"
"s_waitcnt lgkmcnt(0)\n"
MF "v[144:147],v[184:187],a[84:87],v[252:255]\n"
"s_barrier\n"
MF "v[144:147],v[180:183],a[44:47],v[144:147]\n"
LD "v200,s[48:51],s80 offen lds\n"
MF "v[144:147],v[176:179],a[4:7],v[144:147]\n"
"ds_read2st64_b32 v[16:17],v13 offset0:0 offset1:1\n"
MF "v[152:155],v[176:179],a[8:11],v[252:255]\n"
".Lpg30h:\n"
"s_cbranch_execz .Lpg33h\n"
"ds_read2st64_b32 v[18:19],v13 offset0:2 offset1:3\n"
MF "v[152:155],v[180:183],a[48:51],v[152:155]\n"
"ds_read2st64_b32 v[20:21],v13 offset0:4 offset1:5\n"
MF "v[152:155],v[184:187],a[88:91],v[152:155]\n"
"ds_read2st64_b32 v[22:23],v13 offset0:6 offset1:7\n"
MF "v[160:163],v[184:187],a[92:95],v[252:255]\n"
"ds_read2st64_b32 v[24:25],v13 offset0:8 offset1:9\n"
MF "v[160:163],v[180:183],a[52:55],v[160:163]\n"
"ds_read2st64_b32 v[26:27],v13 offset0:10 offset1:11\n"
MF "v[160:163],v[176:179],a[12:15],v[160:163]\n"
LD "v201,s[48:51],s80 offen offset:1024 lds\n"
MF "v[168:171],v[176:179],a[16:19],v[252:255]\n"
"ds_read2st64_b32 v[28:29],v13 offset0:12 offset1:13\n"
MF "v[168:171],v[180:183],a[56:59],v[168:171]\n"
"ds_read2st64_b32 v[30:31],v13 offset0:14 offset1:15\n"
"s_waitcnt vmcnt(29) lgkmcnt(0)\n"
MF "v[168:171],v[184:187],a[96:99],v[168:171]\n"
"s_barrier\n"
MF "v[172:175],a[244:247],a[96:99],v[252:255]\n"
"s_nop 0\n"
TR "v[176:177],v0\n"
TR "v[178:179],v0 offset:2048\n"
MF "v[172:175],a[240:243],a[56:59],v[172:175]\n"
"s_nop 0\n"
TR "v[180:181],v0 offset:4096\n"
TR "v[182:183],v0 offset:6144\n"
MF "v[172:175],v[188:191],a[16:19],v[172:175]\n"
"s_nop 0\n"
TR "v[184:185],v0 offset:8192\n"
TR "v[186:187],v0 offset:10240\n"
MF "v[164:167],v[188:191],a[12:15],v[252:255]\n"
LD "v202,s[48:51],s80 offen offset:2048 lds\n"
MF "v[164:167],a[240:243],a[52:55],v[164:167]\n"
"ds_read2st64_b32 v[32:33],v13 offset0:16 offset1:17\n"
"v_add_f32 v56,v56,v16\n"
MF "v[164:167],a[244:247],a[92:95],v[164:167]\n"
"ds_read2st64_b32 v[34:35],v13 offset0:18 offset1:19\n"
"v_add_f32 v57,v57,v17\n"
MF "v[156:159],a[244:247],a[88:91],v[252:255]\n"
"ds_read2st64_b32 v[36:37],v13 offset0:20 offset1:21\n"
"v_add_f32 v58,v58,v18\n"
MF "v[156:159],a[240:243],a[48:51],v[156:159]\n"
"ds_read2st64_b32 v[38:39],v13 offset0:22 offset1:23\n"
"v_add_f32 v59,v59,v19\n"
MF "v[156:159],v[188:191],a[8:11],v[156:159]\n"
"ds_read2st64_b32 v[40:41],v13 offset0:24 offset1:25\n"
"v_add_f32 v60,v60,v20\n"
MF "v[148:151],v[188:191],a[4:7],v[252:255]\n"
LD "v203,s[48:51],s80 offen offset:3072 lds\n"
MF "v[148:151],a[240:243],a[44:47],v[148:151]\n"
"ds_read2st64_b32 v[42:43],v13 offset0:26 offset1:27\n"
"v_add_f32 v61,v61,v21\n"
MF "v[148:151],a[244:247],a[84:87],v[148:151]\n"
"ds_read2st64_b32 v[44:45],v13 offset0:28 offset1:29\n"
"v_add_f32 v62,v62,v22\n"
MF "v[140:143],a[244:247],a[80:83],v[252:255]\n"
"ds_read2st64_b32 v[46:47],v13 offset0:30 offset1:31\n"
"v_add_f32 v63,v63,v23\n"
MF "v[140:143],a[240:243],a[40:43],v[140:143]\n"
"ds_read2st64_b32 v[48:49],v13 offset0:32 offset1:33\n"
CV "v56,v56,v57\n"
MF "v[140:143],v[188:191],a[0:3],v[140:143]\n"
"s_waitcnt lgkmcnt(9)\n"
"ds_read2st64_b32 v[50:51],v13 offset0:34 offset1:35\n"
CV "v57,v58,v59\n"
MF "v[96:99],v[176:179],a[120:123],v[96:99]\n"
"s_nop 0\n"
TR "v[188:189],v1\n"
TR "v[190:191],v1 offset:2048\n"
MF "v[96:99],v[180:183],a[160:163],v[96:99]\n"
"s_nop 0\n"
TR "a[240:241],v1 offset:4096\n"
TR "a[242:243],v1 offset:6144\n"
MF "v[96:99],v[184:187],a[200:203],v[96:99]\n"
"s_nop 0\n"
TR "a[244:245],v1 offset:8192\n"
TR "a[246:247],v1 offset:10240\n"
MF "v[104:107],v[184:187],a[204:207],v[104:107]\n"
"ds_read2st64_b32 v[52:53],v13 offset0:36 offset1:37\n"
CV "v58,v60,v61\n"
MF "v[104:107],v[180:183],a[164:167],v[104:107]\n"
"s_add_u32 s87,s90,10240\n"
"ds_read2st64_b32 v[54:55],v13 offset0:38 offset1:39\n"
CV "v59,v62,v63\n"
MF "v[104:107],v[176:179],a[124:127],v[104:107]\n"
"buffer_store_dwordx4 v[56:59],v9,s[40:43],s87 offen sc1\n"
MF "v[112:115],v[176:179],a[128:131],v[112:115]\n"
"v_add_f32 v64,v64,v24\n"
"v_add_f32 v65,v65,v25\n"
MF "v[112:115],v[180:183],a[168:171],v[112:115]\n"
"v_add_f32 v66,v66,v26\n"
"v_add_f32 v67,v67,v27\n"
MF "v[112:115],v[184:187],a[208:211],v[112:115]\n"
"v_add_f32 v68,v68,v28\n"
"v_add_f32 v69,v69,v29\n"
MF "v[120:123],v[184:187],a[212:215],v[120:123]\n"
"v_add_f32 v70,v70,v30\n"
"v_add_f32 v71,v71,v31\n"
MF "v[120:123],v[180:183],a[172:175],v[120:123]\n"
CV "v64,v64,v65\n"
CV "v65,v66,v67\n"
MF "v[120:123],v[176:179],a[132:135],v[120:123]\n"
"s_add_u32 s87,s90,11264\n"
CV "v66,v68,v69\n"
CV "v67,v70,v71\n"
MF "v[128:131],v[176:179],a[136:139],v[128:131]\n"
"s_waitcnt lgkmcnt(14)\n"
"buffer_store_dwordx4 v[64:67],v9,s[40:43],s87 offen sc1\n"
MF "v[128:131],v[180:183],a[176:179],v[128:131]\n"
"v_add_f32 v72,v72,v32\n"
"v_add_f32 v73,v73,v33\n"
MF "v[128:131],v[184:187],a[216:219],v[128:131]\n"
"s_waitcnt lgkmcnt(2)\n"
"v_add_f32 v74,v74,v34\n"
"v_add_f32 v75,v75,v35\n"
MF "v[132:135],a[244:247],a[216:219],v[132:135]\n"
"s_nop 0\n"
TR "v[176:177],v2\n"
TR "v[178:179],v2 offset:2048\n"
MF "v[132:135],a[240:243],a[176:179],v[132:135]\n"
"s_nop 0\n"
TR "v[180:181],v2 offset:4096\n"
TR "v[182:183],v2 offset:6144\n"
MF "v[132:135],v[188:191],a[136:139],v[132:135]\n"
"s_nop 0\n"
TR "v[184:185],v2 offset:8192\n"
TR "v[186:187],v2 offset:10240\n"
MF "v[124:127],v[188:191],a[132:135],v[124:127]\n"
"v_add_f32 v76,v76,v36\n"
"v_add_f32 v77,v77,v37\n"
MF "v[124:127],a[240:243],a[172:175],v[124:127]\n"
"v_add_f32 v78,v78,v38\n"
"v_add_f32 v79,v79,v39\n"
MF "v[124:127],a[244:247],a[212:215],v[124:127]\n"
CV "v72,v72,v73\n"
CV "v73,v74,v75\n"
MF "v[116:119],a[244:247],a[208:211],v[116:119]\n"
"s_add_u32 s87,s90,12288\n"
CV "v74,v76,v77\n"
CV "v75,v78,v79\n"
MF "v[116:119],a[240:243],a[168:171],v[116:119]\n"
"buffer_store_dwordx4 v[72:75],v9,s[40:43],s87 offen sc1\n"
MF "v[116:119],v[188:191],a[128:131],v[116:119]\n"
"v_add_f32 v80,v80,v40\n"
"v_add_f32 v81,v81,v41\n"
MF "v[108:111],v[188:191],a[124:127],v[108:111]\n"
"v_add_f32 v82,v82,v42\n"
"v_add_f32 v83,v83,v43\n"
MF "v[108:111],a[240:243],a[164:167],v[108:111]\n"
"v_add_f32 v84,v84,v44\n"
"v_add_f32 v85,v85,v45\n"
MF "v[108:111],a[244:247],a[204:207],v[108:111]\n"
"v_add_f32 v86,v86,v46\n"
"v_add_f32 v87,v87,v47\n"
MF "v[100:103],a[244:247],a[200:203],v[100:103]\n"
CV "v80,v80,v81\n"
CV "v81,v82,v83\n"
MF "v[100:103],a[240:243],a[160:163],v[100:103]\n"
"s_add_u32 s87,s90,13312\n"
CV "v82,v84,v85\n"
CV "v83,v86,v87\n"
MF "v[100:103],v[188:191],a[120:123],v[100:103]\n"
"s_waitcnt lgkmcnt(0)\n"
"buffer_store_dwordx4 v[80:83],v9,s[40:43],s87 offen sc1\n"
MF "v[136:139],v[176:179],a[120:123],v[136:139]\n"
"s_nop 0\n"
TR "v[188:189],v3\n"
TR "v[190:191],v3 offset:2048\n"
MF "v[136:139],v[180:183],a[160:163],v[136:139]\n"
"s_nop 0\n"
TR "a[240:241],v3 offset:4096\n"
TR "a[242:243],v3 offset:6144\n"
MF "v[136:139],v[184:187],a[200:203],v[136:139]\n"
"s_nop 0\n"
TR "a[244:245],v3 offset:8192\n"
TR "a[246:247],v3 offset:10240\n"
MF "v[144:147],v[184:187],a[204:207],v[144:147]\n"
"v_add_f32 v88,v88,v48\n"
"v_add_f32 v89,v89,v49\n"
MF "v[144:147],v[180:183],a[164:167],v[144:147]\n"
"v_add_f32 v90,v90,v50\n"
"v_add_f32 v91,v91,v51\n"
MF "v[144:147],v[176:179],a[124:127],v[144:147]\n"
"v_add_f32 v92,v92,v52\n"
"v_add_f32 v93,v93,v53\n"
MF "v[152:155],v[176:179],a[128:131],v[152:155]\n"
"v_add_f32 v94,v94,v54\n"
"v_add_f32 v95,v95,v55\n"
MF "v[152:155],v[180:183],a[168:171],v[152:155]\n"
CV "v88,v88,v89\n"
CV "v89,v90,v91\n"
MF "v[152:155],v[184:187],a[208:211],v[152:155]\n"
"s_add_u32 s87,s90,14336\n"
CV "v90,v92,v93\n"
CV "v91,v94,v95\n"
MF "v[160:163],v[184:187],a[212:215],v[160:163]\n"
"buffer_store_dwordx4 v[88:91],v9,s[40:43],s87 offen sc1\n"
MF "v[160:163],v[180:183],a[172:175],v[160:163]\n"
MF "v[160:163],v[176:179],a[132:135],v[160:163]\n"
MF "v[168:171],v[176:179],a[136:139],v[168:171]\n"
MF "v[168:171],v[180:183],a[176:179],v[168:171]\n"
"s_waitcnt vmcnt(19) lgkmcnt(0)\n"
MF "v[168:171],v[184:187],a[216:219],v[168:171]\n"
"s_barrier\n"
MF "v[172:175],a[244:247],a[216:219],v[172:175]\n"
"s_nop 0\n"
TR "v[176:177],v0 offset:49152\n"
TR "v[178:179],v0 offset:51200\n"
MF "v[172:175],a[240:243],a[176:179],v[172:175]\n"
"s_nop 0\n"
TR "v[180:181],v0 offset:53248\n"
TR "v[182:183],v0 offset:55296\n"
MF "v[172:175],v[188:191],a[136:139],v[172:175]\n"
"s_nop 0\n"
TR "v[184:185],v0 offset:57344\n"
TR "v[186:187],v0 offset:59392\n"
MF "v[164:167],v[188:191],a[132:135],v[164:167]\n"
"buffer_load_dword v10,v12,s[40:43],s86 offen sc1\n"
MF "v[164:167],a[240:243],a[172:175],v[164:167]\n"
MF "v[164:167],a[244:247],a[212:215],v[164:167]\n"
MF "v[156:159],a[244:247],a[208:211],v[156:159]\n"
MF "v[156:159],a[240:243],a[168:171],v[156:159]\n"
MF "v[156:159],v[188:191],a[128:131],v[156:159]\n"
MF "v[148:151],v[188:191],a[124:127],v[148:151]\n"
MF "v[148:151],a[240:243],a[164:167],v[148:151]\n"
MF "v[148:151],a[244:247],a[204:207],v[148:151]\n"
MF "v[140:143],a[244:247],a[200:203],v[140:143]\n"
MF "v[140:143],a[240:243],a[160:163],v[140:143]\n"
MF "v[140:143],v[188:191],a[120:123],v[140:143]\n"
"s_waitcnt lgkmcnt(0)\n"
MF "v[96:99],v[176:179],a[20:23],v[96:99]\n"
"s_nop 0\n"
TR "v[188:189],v1 offset:49152\n"
TR "v[190:191],v1 offset:51200\n"
MF "v[96:99],v[180:183],a[60:63],v[96:99]\n"
"s_nop 0\n"
TR "a[240:241],v1 offset:53248\n"
TR "a[242:243],v1 offset:55296\n"
MF "v[96:99],v[184:187],a[100:103],v[96:99]\n"
"s_nop 0\n"
TR "a[244:245],v1 offset:57344\n"
TR "a[246:247],v1 offset:59392\n"
MF "v[104:107],v[184:187],a[104:107],v[104:107]\n"
MF "v[104:107],v[180:183],a[64:67],v[104:107]\n"
MF "v[104:107],v[176:179],a[24:27],v[104:107]\n"
"s_waitcnt vmcnt(18)\n"
"v_mov_b32 v236,v228\n"
"v_mov_b32 v237,v229\n"
MF "v[96:99],v[208:211],a[252:255],v[96:99]\n"
MF "v[104:107],v[212:215],a[252:255],v[104:107]\n"
MF "v[112:115],v[216:219],a[252:255],v[112:115]\n"
MF "v[120:123],v[220:223],a[252:255],v[120:123]\n"
MF "v[128:131],v[224:227],a[252:255],v[128:131]\n"
MF "v[100:103],v[208:211],a[248:251],v[100:103]\n"
MF "v[108:111],v[212:215],a[248:251],v[108:111]\n"
MF "v[116:119],v[216:219],a[248:251],v[116:119]\n"
MF "v[124:127],v[220:223],a[248:251],v[124:127]\n"
MF "v[132:135],v[224:227],a[248:251],v[132:135]\n"
MF "v[248:251],v[230:233],a[250:253],v[248:251]\n"
MF "v[244:247],v[234:237],a[250:253],v[244:247]\n"
MF "v[112:115],v[176:179],a[28:31],v[112:115]\n"
MF "v[112:115],v[180:183],a[68:71],v[112:115]\n"
MF "v[112:115],v[184:187],a[108:111],v[112:115]\n"
MF "v[120:123],v[184:187],a[112:115],v[120:123]\n"
"s_nop 3\n"
CV "v244,v244,v245\n"
CV "v245,v246,v247\n"
MF "v[120:123],v[180:183],a[72:75],v[120:123]\n"
"s_bfe_u32 s87,s22,0x30014\n"
CV "v246,v248,v249\n"
CV "v247,v250,v251\n"
MF "v[120:123],v[176:179],a[32:35],v[120:123]\n"
"s_lshl_b32 s87,s87,7\n"
"v_permlane16_swap_b32 v244,v246\n"
"v_permlane16_swap_b32 v245,v247\n"
MF "v[128:131],v[176:179],a[36:39],v[128:131]\n"
"buffer_store_dwordx4 v[244:247],v8,s[44:47],s0 offen sc1\n"
MF "v[128:131],v[180:183],a[76:79],v[128:131]\n"
"s_and_b32 s60,s56,1\n"
MF "v[128:131],v[184:187],a[116:119],v[128:131]\n"
"s_waitcnt lgkmcnt(0)\n"
MF "v[132:135],a[244:247],a[116:119],v[132:135]\n"
"s_mul_i32 s60,s60,204800\n"
TR "v[176:177],v2 offset:49152\n"
TR "v[178:179],v2 offset:51200\n"
MF "v[132:135],a[240:243],a[76:79],v[132:135]\n"
"s_add_u32 s87,s87,s60\n"
TR "v[180:181],v2 offset:53248\n"
TR "v[182:183],v2 offset:55296\n"
MF "v[132:135],v[188:191],a[36:39],v[132:135]\n"
"s_mov_b32 s71,204800\n"
TR "v[184:185],v2 offset:57344\n"
TR "v[186:187],v2 offset:59392\n"
MF "v[124:127],v[188:191],a[32:35],v[124:127]\n"
"s_mov_b32 s66,204800\n"
MF "v[124:127],a[240:243],a[72:75],v[124:127]\n"
"s_add_u32 s72,s78,2\n"
MF "v[124:127],a[244:247],a[112:115],v[124:127]\n"
"s_and_b32 s72,s72,15\n"
MF "v[116:119],a[244:247],a[108:111],v[116:119]\n"
"s_bfe_u32 s60,s22,0x10014\n"
MF "v[116:119],a[240:243],a[68:71],v[116:119]\n"
"s_lshl_b32 s60,s60,5\n"
MF "v[116:119],v[188:191],a[28:31],v[116:119]\n"
"v_xor_b32 v242,s60,v8\n"
MF "v[108:111],v[188:191],a[24:27],v[108:111]\n"
"s_add_u32 s16,s78,6\n"
MF "v[108:111],a[240:243],a[64:67],v[108:111]\n"
"s_and_b32 s16,s16,15\n"
MF "v[108:111],a[244:247],a[104:107],v[108:111]\n"
"s_add_u32 s17,s78,4\n"
MF "v[100:103],a[244:247],a[100:103],v[100:103]\n"
"s_and_b32 s17,s17,15\n"
MF "v[100:103],a[240:243],a[60:63],v[100:103]\n"
MF "v[100:103],v[188:191],a[20:23],v[100:103]\n"
"s_waitcnt lgkmcnt(0)\n"
MF "v[136:139],v[176:179],a[20:23],v[136:139]\n"
"s_nop 0\n"
TR "v[188:189],v3 offset:49152\n"
TR "v[190:191],v3 offset:51200\n"
MF "v[136:139],v[180:183],a[60:63],v[136:139]\n"
"s_nop 0\n"
TR "a[240:241],v3 offset:53248\n"
TR "a[242:243],v3 offset:55296\n"
MF "v[136:139],v[184:187],a[100:103],v[136:139]\n"
"s_nop 0\n"
TR "a[244:245],v3 offset:57344\n"
TR "a[246:247],v3 offset:59392\n"
MF "v[144:147],v[184:187],a[104:107],v[144:147]\n"
MF "v[144:147],v[180:183],a[64:67],v[144:147]\n"
MF "v[144:147],v[176:179],a[24:27],v[144:147]\n"
MF "v[152:155],v[176:179],a[28:31],v[152:155]\n"
MF "v[152:155],v[180:183],a[68:71],v[152:155]\n"
MF "v[152:155],v[184:187],a[108:111],v[152:155]\n"
MF "v[160:163],v[184:187],a[112:115],v[160:163]\n"
MF "v[160:163],v[180:183],a[72:75],v[160:163]\n"
MF "v[160:163],v[176:179],a[32:35],v[160:163]\n"
MF "v[168:171],v[176:179],a[36:39],v[168:171]\n"
MF "v[168:171],v[180:183],a[76:79],v[168:171]\n"
"s_waitcnt vmcnt(1) lgkmcnt(0)\n"
MF "v[168:171],v[184:187],a[116:119],v[168:171]\n"
"s_barrier\n"
MF "v[172:175],a[244:247],a[116:119],v[172:175]\n"
"v_sub_u32 v10,v10,v11\n"
MF "v[172:175],a[240:243],a[76:79],v[172:175]\n"
"s_nop 0\n"
TR "v[176:177],v4\n"
TR "v[178:179],v4 offset:2048\n"
MF "v[172:175],v[188:191],a[36:39],v[172:175]\n"
"s_nop 0\n"
TR "v[180:181],v4 offset:4096\n"
TR "v[182:183],v4 offset:6144\n"
MF "v[164:167],v[188:191],a[32:35],v[164:167]\n"
"s_nop 0\n"
TR "v[184:185],v4 offset:8192\n"
TR "v[186:187],v4 offset:10240\n"
MF "v[164:167],a[240:243],a[72:75],v[164:167]\n"
"v_readlane_b32 s16,v10,s16\n"
MF "v[164:167],a[244:247],a[112:115],v[164:167]\n"
"s_cmp_lt_u32 s16,9\n"
"s_cselect_b32 s16,0xf0000000,0\n"
MF "v[156:159],a[244:247],a[108:111],v[156:159]\n"
"v_readlane_b32 s17,v10,s17\n"
MF "v[156:159],a[240:243],a[68:71],v[156:159]\n"
"s_cmp_lt_u32 s17,10\n"
"s_cselect_b32 s17,0xf0000000,0\n"
MF "v[156:159],v[188:191],a[28:31],v[156:159]\n"
"s_bfe_u32 s62,s22,0x30014\n"
MF "v[148:151],v[188:191],a[24:27],v[148:151]\n"
"s_add_u32 s62,s62,3\n"
"s_and_b32 s62,s62,7\n"
"s_mul_i32 s62,s62,204800\n"
MF "v[148:151],a[240:243],a[64:67],v[148:151]\n"
"s_add_u32 s62,s62,s73\n"
"s_add_u32 s62,s62,133120\n"
"s_or_b32 s62,s62,s16\n"
MF "v[148:151],a[244:247],a[104:107],v[148:151]\n"
LD "v[16:19],v9,s[40:43],s62 offen sc1\n"
MF "v[140:143],a[244:247],a[100:103],v[140:143]\n"
LD "v[24:27],v9,s[40:43],s62 offen offset:1024 sc1\n"
MF "v[140:143],a[240:243],a[60:63],v[140:143]\n"
MF "v[140:143],v[188:191],a[20:23],v[140:143]\n"
"s_waitcnt lgkmcnt(0)\n"
LD "v[32:35],v9,s[40:43],s62 offen offset:2048 sc1\n"
MF "v[96:99],v[176:179],a[140:143],v[96:99]\n"
"s_nop 0\n"
TR "v[188:189],v5\n"
TR "v[190:191],v5 offset:2048\n"
MF "v[96:99],v[180:183],a[180:183],v[96:99]\n"
"s_nop 0\n"
TR "a[240:241],v5 offset:4096\n"
TR "a[242:243],v5 offset:6144\n"
MF "v[96:99],v[184:187],a[220:223],v[96:99]\n"
"s_nop 0\n"
TR "a[244:245],v5 offset:8192\n"
TR "a[246:247],v5 offset:10240\n"
MF "v[104:107],v[184:187],a[224:227],v[104:107]\n"
LD "v[40:43],v9,s[40:43],s62 offen offset:3072 sc1\n"
MF "v[104:107],v[180:183],a[184:187],v[104:107]\n"
MF "v[104:107],v[176:179],a[144:147],v[104:107]\n"
"s_add_u32 s62,s62,4096\n"
MF "v[112:115],v[176:179],a[148:151],v[112:115]\n"
LD "v[48:51],v9,s[40:43],s62 offen sc1\n"
MF "v[112:115],v[180:183],a[188:191],v[112:115]\n"
"s_bfe_u32 s62,s22,0x30014\n"
"ds_write_addtid_b32 v96 offset:16384\n"
"ds_write_addtid_b32 v97 offset:16640\n"
MF "v[112:115],v[184:187],a[228:231],v[112:115]\n"
"s_add_u32 s62,s62,2\n"
"s_and_b32 s62,s62,7\n"
"s_mul_i32 s62,s62,204800\n"
MF "v[120:123],v[184:187],a[232:235],v[120:123]\n"
"s_add_u32 s62,s62,s73\n"
"s_add_u32 s62,s62,153600\n"
"s_or_b32 s62,s62,s17\n"
MF "v[120:123],v[180:183],a[192:195],v[120:123]\n"
LD "v[20:23],v9,s[40:43],s62 offen sc1\n"
MF "v[120:123],v[176:179],a[152:155],v[120:123]\n"
"s_nop 0\n"
"ds_write_addtid_b32 v98 offset:16896\n"
"ds_write_addtid_b32 v99 offset:17152\n"
MF "v[128:131],v[176:179],a[156:159],v[128:131]\n"
LD "v[28:31],v9,s[40:43],s62 offen offset:1024 sc1\n"
MF "v[128:131],v[180:183],a[196:199],v[128:131]\n"
"s_nop 0\n"
"ds_write_addtid_b32 v104 offset:18432\n"
"ds_write_addtid_b32 v105 offset:18688\n"
MF "v[128:131],v[184:187],a[236:239],v[128:131]\n"
"s_waitcnt lgkmcnt(6)\n"
LD "v[36:39],v9,s[40:43],s62 offen offset:2048 sc1\n"
MF "v[100:103],v[188:191],a[140:143],v[100:103]\n"
"s_nop 0\n"
TR "v[176:177],v6\n"
TR "v[178:179],v6 offset:2048\n"
MF "v[100:103],a[240:243],a[180:183],v[100:103]\n"
"s_nop 0\n"
TR "v[180:181],v6 offset:4096\n"
TR "v[182:183],v6 offset:6144\n"
MF "v[100:103],a[244:247],a[220:223],v[100:103]\n"
"s_nop 0\n"
TR "v[184:185],v6 offset:8192\n"
TR "v[186:187],v6 offset:10240\n"
MF "v[108:111],a[244:247],a[224:227],v[108:111]\n"
LD "v[44:47],v9,s[40:43],s62 offen offset:3072 sc1\n"
MF "v[108:111],a[240:243],a[184:187],v[108:111]\n"
"s_nop 0\n"
"ds_write_addtid_b32 v106 offset:18944\n"
"ds_write_addtid_b32 v107 offset:19200\n"
MF "v[108:111],v[188:191],a[144:147],v[108:111]\n"
"s_add_u32 s62,s62,4096\n"
"ds_write_addtid_b32 v112 offset:20480\n"
"ds_write_addtid_b32 v113 offset:20736\n"
MF "v[116:119],v[188:191],a[148:151],v[116:119]\n"
LD "v[52:55],v9,s[40:43],s62 offen sc1\n"
MF "v[116:119],a[240:243],a[188:191],v[116:119]\n"
"s_nop 0\n"
"ds_write_addtid_b32 v114 offset:20992\n"
"ds_write_addtid_b32 v115 offset:21248\n"
MF "v[116:119],a[244:247],a[228:231],v[116:119]\n"
"s_nop 0\n"
"ds_write_addtid_b32 v120 offset:22528\n"
"ds_write_addtid_b32 v121 offset:22784\n"
MF "v[124:127],a[244:247],a[232:235],v[124:127]\n"
"s_nop 0\n"
"ds_write_addtid_b32 v122 offset:23040\n"
"ds_write_addtid_b32 v123 offset:23296\n"
MF "v[124:127],a[240:243],a[192:195],v[124:127]\n"
"s_nop 0\n"
"ds_write_addtid_b32 v128 offset:24576\n"
"ds_write_addtid_b32 v129 offset:24832\n"
MF "v[124:127],v[188:191],a[152:155],v[124:127]\n"
"buffer_load_dword v10,v12,s[40:43],s86 offen sc1\n"
MF "v[132:135],v[188:191],a[156:159],v[132:135]\n"
"s_nop 0\n"
"ds_write_addtid_b32 v130 offset:25088\n"
"ds_write_addtid_b32 v131 offset:25344\n"
MF "v[132:135],a[240:243],a[196:199],v[132:135]\n"
"s_nop 0\n"
"ds_write_addtid_b32 v100 offset:17408\n"
"ds_write_addtid_b32 v101 offset:17664\n"
MF "v[132:135],a[244:247],a[236:239],v[132:135]\n"
"s_waitcnt lgkmcnt(15)\n"
"ds_write_addtid_b32 v102 offset:17920\n"
"ds_write_addtid_b32 v103 offset:18176\n"
MF "v[136:139],v[176:179],a[140:143],v[136:139]\n"
"s_nop 0\n"
TR "v[188:189],v7\n"
TR "v[190:191],v7 offset:2048\n"
MF "v[136:139],v[180:183],a[180:183],v[136:139]\n"
"s_nop 0\n"
TR "a[240:241],v7 offset:4096\n"
TR "a[242:243],v7 offset:6144\n"
MF "v[136:139],v[184:187],a[220:223],v[136:139]\n"
"s_nop 0\n"
TR "a[244:245],v7 offset:8192\n"
TR "a[246:247],v7 offset:10240\n"
MF "v[144:147],v[184:187],a[224:227],v[144:147]\n"
"s_nop 0\n"
"ds_write_addtid_b32 v108 offset:19456\n"
"ds_write_addtid_b32 v109 offset:19712\n"
MF "v[144:147],v[180:183],a[184:187],v[144:147]\n"
"s_nop 0\n"
"ds_write_addtid_b32 v110 offset:19968\n"
"ds_write_addtid_b32 v111 offset:20224\n"
MF "v[144:147],v[176:179],a[144:147],v[144:147]\n"
"s_nop 0\n"
"ds_write_addtid_b32 v116 offset:21504\n"
"ds_write_addtid_b32 v117 offset:21760\n"
MF "v[152:155],v[176:179],a[148:151],v[152:155]\n"
"s_nop 0\n"
"ds_write_addtid_b32 v118 offset:22016\n"
"ds_write_addtid_b32 v119 offset:22272\n"
MF "v[152:155],v[180:183],a[188:191],v[152:155]\n"
"ds_write_addtid_b32 v124 offset:23552\n"
MF "v[152:155],v[184:187],a[228:231],v[152:155]\n"
"ds_write_addtid_b32 v125 offset:23808\n"
MF "v[160:163],v[184:187],a[232:235],v[160:163]\n"
"ds_write_addtid_b32 v126 offset:24064\n"
MF "v[160:163],v[180:183],a[192:195],v[160:163]\n"
"ds_write_addtid_b32 v127 offset:24320\n"
MF "v[160:163],v[176:179],a[152:155],v[160:163]\n"
"ds_write_addtid_b32 v132 offset:25600\n"
MF "v[168:171],v[176:179],a[156:159],v[168:171]\n"
"ds_write_addtid_b32 v133 offset:25856\n"
MF "v[168:171],v[180:183],a[196:199],v[168:171]\n"
"ds_write_addtid_b32 v134 offset:26112\n"
MF "v[168:171],v[184:187],a[236:239],v[168:171]\n"
"s_waitcnt lgkmcnt(15)\n"
"ds_write_addtid_b32 v135 offset:26368\n"
MF "v[140:143],v[188:191],a[140:143],v[140:143]\n"
MF "v[140:143],a[240:243],a[180:183],v[140:143]\n"
MF "v[140:143],a[244:247],a[220:223],v[140:143]\n"
MF "v[148:151],a[244:247],a[224:227],v[148:151]\n"
"s_waitcnt lgkmcnt(0)\n"
MF "v[148:151],a[240:243],a[184:187],v[148:151]\n"
"s_barrier\n"
MF "v[148:151],v[188:191],a[144:147],v[148:151]\n"
"ds_read2st64_b32 v[56:57],v13 offset0:0 offset1:1\n"
MF "v[156:159],v[188:191],a[148:151],v[156:159]\n"
"ds_read2st64_b32 v[58:59],v13 offset0:2 offset1:3\n"
MF "v[156:159],a[240:243],a[188:191],v[156:159]\n"
"ds_read2st64_b32 v[60:61],v13 offset0:4 offset1:5\n"
MF "v[156:159],a[244:247],a[228:231],v[156:159]\n"
"ds_read2st64_b32 v[62:63],v13 offset0:6 offset1:7\n"
MF "v[164:167],a[244:247],a[232:235],v[164:167]\n"
"ds_read2st64_b32 v[64:65],v13 offset0:8 offset1:9\n"
MF "v[164:167],a[240:243],a[192:195],v[164:167]\n"
"ds_read2st64_b32 v[66:67],v13 offset0:10 offset1:11\n"
MF "v[164:167],v[188:191],a[152:155],v[164:167]\n"
"ds_read2st64_b32 v[68:69],v13 offset0:12 offset1:13\n"
MF "v[172:175],v[188:191],a[156:159],v[172:175]\n"
"ds_read2st64_b32 v[70:71],v13 offset0:14 offset1:15\n"
MF "v[172:175],a[240:243],a[196:199],v[172:175]\n"
"ds_read2st64_b32 v[72:73],v13 offset0:16 offset1:17\n"
MF "v[172:175],a[244:247],a[236:239],v[172:175]\n"
"ds_read2st64_b32 v[74:75],v13 offset0:18 offset1:19\n"
"ds_read2st64_b32 v[76:77],v13 offset0:20 offset1:21\n"
"ds_read2st64_b32 v[78:79],v13 offset0:22 offset1:23\n"
"s_nop 0\n"
"ds_read2st64_b32 v[80:81],v13 offset0:24 offset1:25\n"
"ds_read2st64_b32 v[82:83],v13 offset0:26 offset1:27\n"
"ds_read2st64_b32 v[84:85],v13 offset0:28 offset1:29\n"
"ds_read2st64_b32 v[86:87],v13 offset0:30 offset1:31\n"
"ds_read2st64_b32 v[88:89],v13 offset0:32 offset1:33\n"
"ds_read2st64_b32 v[90:91],v13 offset0:34 offset1:35\n"
"ds_read2st64_b32 v[92:93],v13 offset0:36 offset1:37\n"
"ds_read2st64_b32 v[94:95],v13 offset0:38 offset1:39\n"
"s_waitcnt vmcnt(0)\n"
"v_sub_u32 v10,v10,v11\n"
"s_cmp_eq_u32 s16,0\n"
"s_cbranch_scc1 .Lhave03\n"
"s_add_u32 s59,s78,6\n"
"s_and_b32 s59,s59,15\n"
"s_nop 3\n"
".Lnf3:\n"
"v_readlane_b32 s58,v10,s59\n"
"s_cmp_lt_u32 s58,9\n"
"s_cbranch_scc1 .Lsp3\n"
".Lnb3:\n"
"s_bfe_u32 s62,s22,0x30014\n"
"s_add_u32 s62,s62,3\n"
"s_and_b32 s62,s62,7\n"
"s_mul_i32 s62,s62,204800\n"
"s_add_u32 s62,s62,s73\n"
"s_add_u32 s62,s62,133120\n"
LD "v[16:19],v9,s[40:43],s62 offen sc1\n"
".Lpg31h:\n"
"s_cbranch_execz .Lpg34h\n"
"s_nop 0\n"
LD "v[24:27],v9,s[40:43],s62 offen offset:1024 sc1\n"
LD "v[32:35],v9,s[40:43],s62 offen offset:2048 sc1\n"
LD "v[40:43],v9,s[40:43],s62 offen offset:3072 sc1\n"
"s_add_u32 s62,s62,4096\n"
LD "v[48:51],v9,s[40:43],s62 offen sc1\n"
".Lhave03:\n"
"s_cmp_eq_u32 s17,0\n"
"s_cbranch_scc1 .Lhave02\n"
"s_add_u32 s59,s78,4\n"
"s_and_b32 s59,s59,15\n"
"s_nop 3\n"
".Lnf4:\n"
"v_readlane_b32 s58,v10,s59\n"
"s_cmp_lt_u32 s58,10\n"
"s_cbranch_scc1 .Lsp4\n"
".Lnb4:\n"
"s_bfe_u32 s62,s22,0x30014\n"
"s_add_u32 s62,s62,2\n"
"s_and_b32 s62,s62,7\n"
"s_mul_i32 s62,s62,204800\n"
"s_add_u32 s62,s62,s73\n"
"s_add_u32 s62,s62,153600\n"
LD "v[20:23],v9,s[40:43],s62 offen sc1\n"
LD "v[28:31],v9,s[40:43],s62 offen offset:1024 sc1\n"
LD "v[36:39],v9,s[40:43],s62 offen offset:2048 sc1\n"
LD "v[44:47],v9,s[40:43],s62 offen offset:3072 sc1\n"
"s_add_u32 s62,s62,4096\n"
LD "v[52:55],v9,s[40:43],s62 offen sc1\n"
".Lhave02:\n"
".Lnf5:\n"
"v_readlane_b32 s58,v10,s72\n"
"s_cmp_lt_u32 s58,11\n"
"s_cbranch_scc1 .Lsp5\n"
".Lnb5:\n"
"s_bfe_u32 s62,s22,0x30014\n"
"s_add_u32 s62,s62,1\n"
"s_and_b32 s62,s62,7\n"
"s_mul_i32 s62,s62,204800\n"
"s_add_u32 s62,s62,s73\n"
"s_add_u32 s62,s62,174080\n"
LD "v[208:211],v9,s[40:43],s62 offen sc1\n"
LD "v[212:215],v9,s[40:43],s62 offen offset:1024 sc1\n"
LD "v[216:219],v9,s[40:43],s62 offen offset:2048 sc1\n"
LD "v[220:223],v9,s[40:43],s62 offen offset:3072 sc1\n"
"s_add_u32 s62,s62,4096\n"
LD "v[224:227],v9,s[40:43],s62 offen sc1\n"
"s_waitcnt lgkmcnt(15)\n"
"v_pk_add_f32 v[136:137],v[136:137],v[56:57]\n"
"v_pk_add_f32 v[138:139],v[138:139],v[58:59]\n"
"v_pk_add_f32 v[140:141],v[140:141],v[60:61]\n"
"v_pk_add_f32 v[142:143],v[142:143],v[62:63]\n"
"v_pk_add_f32 v[144:145],v[144:145],v[64:65]\n"
"s_waitcnt lgkmcnt(14)\n"
"v_pk_add_f32 v[146:147],v[146:147],v[66:67]\n"
"s_waitcnt lgkmcnt(13)\n"
"v_pk_add_f32 v[148:149],v[148:149],v[68:69]\n"
"s_waitcnt lgkmcnt(12)\n"
"v_pk_add_f32 v[150:151],v[150:151],v[70:71]\n"
"s_waitcnt lgkmcnt(11)\n"
"v_pk_add_f32 v[152:153],v[152:153],v[72:73]\n"
"s_waitcnt lgkmcnt(10)\n"
"v_pk_add_f32 v[154:155],v[154:155],v[74:75]\n"
"s_waitcnt lgkmcnt(9)\n"
"v_pk_add_f32 v[156:157],v[156:157],v[76:77]\n"
"s_waitcnt lgkmcnt(8)\n"
"v_pk_add_f32 v[158:159],v[158:159],v[78:79]\n"
"s_waitcnt lgkmcnt(7)\n"
"v_pk_add_f32 v[160:161],v[160:161],v[80:81]\n"
"s_waitcnt lgkmcnt(6)\n"
"v_pk_add_f32 v[162:163],v[162:163],v[82:83]\n"
"s_waitcnt lgkmcnt(5)\n"
"v_pk_add_f32 v[164:165],v[164:165],v[84:85]\n"
"s_waitcnt lgkmcnt(4)\n"
"v_pk_add_f32 v[166:167],v[166:167],v[86:87]\n"
"s_waitcnt lgkmcnt(3)\n"
"v_pk_add_f32 v[168:169],v[168:169],v[88:89]\n"
"s_waitcnt lgkmcnt(2)\n"
"v_pk_add_f32 v[170:171],v[170:171],v[90:91]\n"
"s_waitcnt lgkmcnt(1)\n"
"v_pk_add_f32 v[172:173],v[172:173],v[92:93]\n"
"s_waitcnt lgkmcnt(0)\n"
"v_pk_add_f32 v[174:175],v[174:175],v[94:95]\n"
"s_waitcnt vmcnt(5)\n"
"s_nop 0\n"
MF "v[140:143],v[16:19],a[252:255],v[140:143]\n"
MF "v[148:151],v[24:27],a[252:255],v[148:151]\n"
MF "v[156:159],v[32:35],a[252:255],v[156:159]\n"
MF "v[164:167],v[40:43],a[252:255],v[164:167]\n"
MF "v[172:175],v[48:51],a[252:255],v[172:175]\n"
MF "v[136:139],v[16:19],a[248:251],v[136:139]\n"
MF "v[144:147],v[24:27],a[248:251],v[144:147]\n"
MF "v[152:155],v[32:35],a[248:251],v[152:155]\n"
MF "v[160:163],v[40:43],a[248:251],v[160:163]\n"
MF "v[168:171],v[48:51],a[248:251],v[168:171]\n"
MF "v[136:139],v[20:23],a[252:255],v[136:139]\n"
MF "v[144:147],v[28:31],a[252:255],v[144:147]\n"
MF "v[152:155],v[36:39],a[252:255],v[152:155]\n"
MF "v[160:163],v[44:47],a[252:255],v[160:163]\n"
MF "v[168:171],v[52:55],a[252:255],v[168:171]\n"
MF "v[140:143],v[20:23],a[248:251],v[140:143]\n"
MF "v[148:151],v[28:31],a[248:251],v[148:151]\n"
MF "v[156:159],v[36:39],a[248:251],v[156:159]\n"
MF "v[164:167],v[44:47],a[248:251],v[164:167]\n"
MF "v[172:175],v[52:55],a[248:251],v[172:175]\n"
"s_waitcnt vmcnt(0)\n"
MF "v[140:143],v[208:211],a[252:255],v[140:143]\n"
MF "v[136:139],v[208:211],a[248:251],v[136:139]\n"
MF "v[148:151],v[212:215],a[252:255],v[148:151]\n"
MF "v[144:147],v[212:215],a[248:251],v[144:147]\n"
MF "v[156:159],v[216:219],a[252:255],v[156:159]\n"
"s_nop 4\n"
CV "v136,v136,v137\n"
CV "v137,v138,v139\n"
MF "v[152:155],v[216:219],a[248:251],v[152:155]\n"
CV "v138,v140,v141\n"
CV "v139,v142,v143\n"
MF "v[164:167],v[220:223],a[252:255],v[164:167]\n"
CV "v144,v144,v145\n"
CV "v145,v146,v147\n"
MF "v[160:163],v[220:223],a[248:251],v[160:163]\n"
CV "v146,v148,v149\n"
CV "v147,v150,v151\n"
"v_permlane16_swap_b32 v136,v138\n"
"v_permlane16_swap_b32 v137,v139\n"
"buffer_store_dwordx4 v[136:139],v242,s[44:47],s87 offen offset:64 sc1\n"
"s_add_u32 s87,s87,40960\n"
MF "v[172:175],v[224:227],a[252:255],v[172:175]\n"
CV "v152,v152,v153\n"
CV "v153,v154,v155\n"
MF "v[168:171],v[224:227],a[248:251],v[168:171]\n"
CV "v154,v156,v157\n"
CV "v155,v158,v159\n"
"v_permlane16_swap_b32 v144,v146\n"
"v_permlane16_swap_b32 v145,v147\n"
"buffer_store_dwordx4 v[144:147],v242,s[44:47],s87 offen offset:64 sc1\n"
"s_add_u32 s87,s87,40960\n"
CV "v160,v160,v161\n"
CV "v161,v162,v163\n"
CV "v162,v164,v165\n"
CV "v163,v166,v167\n"
"v_permlane16_swap_b32 v152,v154\n"
"v_permlane16_swap_b32 v153,v155\n"
"buffer_store_dwordx4 v[152:155],v242,s[44:47],s87 offen offset:64 sc1\n"
"s_add_u32 s87,s87,40960\n"
CV "v168,v168,v169\n"
CV "v169,v170,v171\n"
CV "v170,v172,v173\n"
CV "v171,v174,v175\n"
"v_permlane16_swap_b32 v160,v162\n"
"v_permlane16_swap_b32 v161,v163\n"
"buffer_store_dwordx4 v[160:163],v242,s[44:47],s87 offen offset:64 sc1\n"
"s_add_u32 s87,s87,40960\n"
"v_permlane16_swap_b32 v168,v170\n"
"v_permlane16_swap_b32 v169,v171\n"
"buffer_store_dwordx4 v[168:171],v242,s[44:47],s87 offen offset:64 sc1\n"
"s_add_u32 s87,s87,40960\n"
"s_endpgm\n"
".Lpgrole1_h:\n"
"s_cbranch_execz .Lpg35h\n"
".Lrole1:\n"
"s_waitcnt lgkmcnt(4)\n"
MF "v[16:19],a[120:123],v[176:179],v[252:255]\n"
"s_mov_b32 s65,128\n"
TR "v[188:189],v1 offset:24576\n"
AR "v220,a44\n"
"s_waitcnt lgkmcnt(3)\n"
MF "v[16:19],a[160:163],v[180:183],v[16:19]\n"
"s_mov_b32 s9,0x3f80\n"
TR "v[190:191],v1 offset:26624\n"
AR "v221,a45\n"
"s_waitcnt lgkmcnt(2)\n"
MF "v[16:19],a[200:203],v[184:187],v[16:19]\n"
AR "v222,a46\n"
AR "v223,a47\n"
MF "v[24:27],a[204:207],v[184:187],v[252:255]\n"
"s_bfe_u32 s58,s22,0x30014\n"
TR "a[240:241],v1 offset:28672\n"
AR "v224,a64\n"
MF "v[24:27],a[164:167],v[180:183],v[24:27]\n"
"s_add_u32 s61,s58,1\n"
TR "a[242:243],v1 offset:30720\n"
AR "v225,a65\n"
MF "v[24:27],a[124:127],v[176:179],v[24:27]\n"
"s_and_b32 s61,s61,7\n"
AR "v226,a66\n"
AR "v227,a67\n"
MF "v[32:35],a[128:131],v[176:179],v[252:255]\n"
"s_mov_b32 s64,9\n"
TR "a[244:245],v1 offset:32768\n"
"ds_read_b128 a[64:67],v236 offset:49152\n"
MF "v[32:35],a[168:171],v[180:183],v[32:35]\n"
"s_xor_b32 s67,s78,8\n"
TR "a[246:247],v1 offset:34816\n"
"v_fma_f32 v220,v220,v209,v215\n"
MF "v[32:35],a[208:211],v[184:187],v[32:35]\n"
"s_mov_b32 s71,204800\n"
"v_fma_f32 v221,v221,v209,v215\n"
"v_fma_f32 v222,v222,v209,v215\n"
MF "v[40:43],a[212:215],v[184:187],v[252:255]\n"
"s_mov_b32 s66,204800\n"
"v_fma_f32 v223,v223,v209,v215\n"
"v_fma_f32 v224,v224,v209,v215\n"
MF "v[40:43],a[172:175],v[180:183],v[40:43]\n"
"s_and_b32 s73,s56,1\n"
"v_fma_f32 v225,v225,v209,v215\n"
"v_fma_f32 v226,v226,v209,v215\n"
MF "v[40:43],a[132:135],v[176:179],v[40:43]\n"
"s_mul_i32 s73,s73,5120\n"
"v_fma_f32 v227,v227,v209,v215\n"
CV "v220,v220,v221\n"
MF "v[48:51],a[136:139],v[176:179],v[252:255]\n"
"s_add_u32 s73,s73,s93\n"
CV "v221,v222,v223\n"
CV "v222,v224,v225\n"
MF "v[48:51],a[176:179],v[180:183],v[48:51]\n"
"s_xor_b32 s58,s58,4\n"
CV "v223,v226,v227\n"
AW "a44,v220\n"
MF "v[48:51],a[216:219],v[184:187],v[48:51]\n"
"s_waitcnt lgkmcnt(0)\n"
AW "a45,v221\n"
AW "a46,v222\n"
MF "v[52:55],a[216:219],a[244:247],v[252:255]\n"
"s_mul_i32 s70,s58,204800\n"
TR "v[176:177],v2 offset:24576\n"
AW "a47,v223\n"
MF "v[52:55],a[176:179],a[240:243],v[52:55]\n"
"s_add_u32 s70,s70,s73\n"
TR "v[178:179],v2 offset:26624\n"
AR "v228,a84\n"
MF "v[52:55],a[136:139],v[188:191],v[52:55]\n"
"s_add_u32 s70,s70,0xc800\n"
AR "v229,a85\n"
AR "v230,a86\n"
MF "v[44:47],a[132:135],v[188:191],v[252:255]\n"
"s_mov_b32 s85,1\n"
TR "v[180:181],v2 offset:28672\n"
AR "v231,a87\n"
MF "v[44:47],a[172:175],a[240:243],v[44:47]\n"
"s_mov_b32 s83,0xf0000000\n"
TR "v[182:183],v2 offset:30720\n"
AR "v232,a104\n"
MF "v[44:47],a[212:215],a[244:247],v[44:47]\n"
"s_mov_b32 s82,0xf0000000\n"
AR "v233,a105\n"
AR "v234,a106\n"
MF "v[36:39],a[208:211],a[244:247],v[252:255]\n"
"s_add_u32 s58,s79,73728\n"
TR "v[184:185],v2 offset:32768\n"
AR "v235,a107\n"
MF "v[36:39],a[168:171],a[240:243],v[36:39]\n"
"s_mov_b32 s90,s91\n"
TR "v[186:187],v2 offset:34816\n"
"ds_read_b128 a[104:107],v237 offset:4096\n"
MF "v[36:39],a[128:131],v[188:191],v[36:39]\n"
"s_add_u32 s91,s91,20480\n"
"v_fma_f32 v228,v228,v209,v215\n"
"v_fma_f32 v229,v229,v209,v215\n"
MF "v[28:31],a[124:127],v[188:191],v[252:255]\n"
"s_bfe_u32 s74,s23,0x70000\n"
"v_fma_f32 v230,v230,v209,v215\n"
"v_fma_f32 v231,v231,v209,v215\n"
MF "v[28:31],a[164:167],a[240:243],v[28:31]\n"
"s_lshl_b32 s74,s74,1\n"
"v_fma_f32 v232,v232,v209,v215\n"
"v_fma_f32 v233,v233,v209,v215\n"
MF "v[28:31],a[204:207],a[244:247],v[28:31]\n"
"s_add_u32 s74,s74,s56\n"
"v_fma_f32 v234,v234,v209,v215\n"
"v_fma_f32 v235,v235,v209,v215\n"
MF "v[20:23],a[200:203],a[244:247],v[252:255]\n"
"s_sub_u32 s74,s74,2\n"
CV "v228,v228,v229\n"
CV "v229,v230,v231\n"
MF "v[20:23],a[160:163],a[240:243],v[20:23]\n"
"s_mul_i32 s74,s74,30720\n"
CV "v230,v232,v233\n"
CV "v231,v234,v235\n"
MF "v[20:23],a[120:123],v[188:191],v[20:23]\n"
"s_waitcnt lgkmcnt(1)\n"
AW "a84,v228\n"
AW "a85,v229\n"
MF "v[56:59],a[120:123],v[176:179],v[252:255]\n"
"s_bfe_u32 s68,s23,0x30003\n"
TR "v[188:189],v3 offset:24576\n"
"v_add_u32 v198,59392,v192\n"
MF "v[56:59],a[160:163],v[180:183],v[56:59]\n"
"s_add_u32 s68,s68,7\n"
TR "v[190:191],v3 offset:26624\n"
"v_add_u32 v199,68608,v192\n"
MF "v[56:59],a[200:203],v[184:187],v[56:59]\n"
"s_lshl_b32 s68,s68,7\n"
"v_add_u32 v200,81920,v192\n"
"v_add_u32 v201,91136,v192\n"
MF "v[64:67],a[204:207],v[184:187],v[252:255]\n"
"s_and_b32 s68,s68,768\n"
TR "a[240:241],v3 offset:28672\n"
"v_add_u32 v202,100352,v192\n"
MF "v[64:67],a[164:167],v[180:183],v[64:67]\n"
"s_bfe_u32 s4,s22,0x30014\n"
TR "a[242:243],v3 offset:30720\n"
"v_add_u32 v203,109568,v192\n"
MF "v[64:67],a[124:127],v[176:179],v[64:67]\n"
"s_add_u32 s4,s4,32\n"
AW "a86,v230\n"
AW "a87,v231\n"
MF "v[72:75],a[128:131],v[176:179],v[252:255]\n"
"s_sub_u32 s4,s4,s92\n"
TR "a[244:245],v3 offset:32768\n"
AR "v220,a8\n"
MF "v[72:75],a[168:171],v[180:183],v[72:75]\n"
"s_cmp_eq_u32 s56,2\n"
"s_cselect_b32 s4,s4,64\n"
TR "a[246:247],v3 offset:34816\n"
MF "v[72:75],a[208:211],v[184:187],v[72:75]\n"
"s_mul_i32 s95,s92,3277\n"
AR "v221,a9\n"
AR "v222,a10\n"
MF "v[80:83],a[212:215],v[184:187],v[252:255]\n"
"s_lshr_b32 s95,s95,16\n"
AR "v223,a11\n"
AR "v224,a28\n"
MF "v[80:83],a[172:175],v[180:183],v[80:83]\n"
"s_mul_i32 s59,s95,20\n"
AR "v225,a29\n"
AR "v226,a30\n"
MF "v[80:83],a[132:135],v[176:179],v[80:83]\n"
"s_sub_u32 s59,s92,s59\n"
AR "v227,a31\n"
"v_fma_f32 v220,v220,v210,v216\n"
MF "v[88:91],a[136:139],v[176:179],v[252:255]\n"
"s_cmp_ge_u32 s59,10\n"
"s_cselect_b32 s96,4294557760,0\n"
"ds_read_b128 a[28:31],v236 offset:32768\n"
MF "v[88:91],a[176:179],v[180:183],v[88:91]\n"
"s_waitcnt vmcnt(0) lgkmcnt(0)\n"
"v_fma_f32 v221,v221,v210,v216\n"
"v_fma_f32 v222,v222,v210,v216\n"
MF "v[88:91],a[216:219],v[184:187],v[88:91]\n"
"s_barrier\n"
MF "v[92:95],a[216:219],a[244:247],v[252:255]\n"
"s_add_u32 s89,s89,s65\n"
TR "v[176:177],v0\n"
"v_fma_f32 v223,v223,v210,v216\n"
MF "v[92:95],a[176:179],a[240:243],v[92:95]\n"
"s_add_u32 m0,s79,24576\n"
TR "v[178:179],v0 offset:2048\n"
"v_fma_f32 v224,v224,v210,v216\n"
MF "v[92:95],a[136:139],v[188:191],v[92:95]\n"
LD "v192,s[48:51],s89 offen lds\n"
MF "v[84:87],a[132:135],v[188:191],v[252:255]\n"
"s_add_u32 s96,s96,1024\n"
TR "v[180:181],v0 offset:4096\n"
"v_fma_f32 v225,v225,v210,v216\n"
MF "v[84:87],a[172:175],a[240:243],v[84:87]\n"
"s_mul_i32 s59,s59,40960\n"
TR "v[182:183],v0 offset:6144\n"
"v_fma_f32 v226,v226,v210,v216\n"
MF "v[84:87],a[212:215],a[244:247],v[84:87]\n"
LD "v193,s[48:51],s89 offen offset:1024 lds\n"
MF "v[76:79],a[208:211],a[244:247],v[252:255]\n"
"s_add_u32 s96,s96,s59\n"
TR "v[184:185],v0 offset:8192\n"
"v_fma_f32 v227,v227,v210,v216\n"
MF "v[76:79],a[168:171],a[240:243],v[76:79]\n"
"s_lshl_b32 s59,s95,7\n"
TR "v[186:187],v0 offset:10240\n"
CV "v220,v220,v221\n"
MF "v[76:79],a[128:131],v[188:191],v[76:79]\n"
"s_add_u32 s96,s96,s59\n"
TR "v[96:97],v0 offset:49152\n"
CV "v221,v222,v223\n"
MF "v[68:71],a[124:127],v[188:191],v[252:255]\n"
"s_lshl_b32 s2,s92,10\n"
TR "v[98:99],v0 offset:51200\n"
CV "v222,v224,v225\n"
MF "v[68:71],a[164:167],a[240:243],v[68:71]\n"
LD "v194,s[48:51],s89 offen offset:2048 lds\n"
MF "v[68:71],a[204:207],a[244:247],v[68:71]\n"
"s_add_u32 s2,s2,s93\n"
TR "v[100:101],v0 offset:53248\n"
CV "v223,v226,v227\n"
MF "v[60:63],a[200:203],a[244:247],v[252:255]\n"
"s_mov_b32 s0,s96\n"
TR "v[102:103],v0 offset:55296\n"
AW "a8,v220\n"
MF "v[60:63],a[160:163],a[240:243],v[60:63]\n"
"s_add_u32 s92,s92,s4\n"
TR "v[104:105],v0 offset:57344\n"
AW "a9,v221\n"
MF "v[60:63],a[120:123],v[188:191],v[60:63]\n"
"s_waitcnt lgkmcnt(5)\n"
LD "v195,s[48:51],s89 offen offset:3072 lds\n"
MF "v[16:19],a[140:143],v[176:179],v[16:19]\n"
"s_mul_i32 s95,s92,3277\n"
TR "v[188:189],v1\n"
AW "a10,v222\n"
MF "v[16:19],a[180:183],v[180:183],v[16:19]\n"
"s_lshr_b32 s95,s95,16\n"
TR "v[190:191],v1 offset:2048\n"
AW "a11,v223\n"
MF "v[16:19],a[220:223],v[184:187],v[16:19]\n"
"s_mul_i32 s59,s95,20\n"
TR "v[106:107],v0 offset:59392\n"
AR "v228,a48\n"
MF "v[24:27],a[224:227],v[184:187],v[24:27]\n"
"s_sub_u32 s59,s92,s59\n"
TR "a[240:241],v1 offset:4096\n"
AR "v229,a49\n"
MF "v[24:27],a[184:187],v[180:183],v[24:27]\n"
"s_add_u32 m0,s79,28672\n"
TR "a[242:243],v1 offset:6144\n"
AR "v230,a50\n"
MF "v[24:27],a[144:147],v[176:179],v[24:27]\n"
LD "v196,s[48:51],s89 offen lds\n"
MF "v[32:35],a[148:151],v[176:179],v[32:35]\n"
"s_cmp_ge_u32 s59,10\n"
"s_cselect_b32 s96,4294557760,0\n"
TR "a[244:245],v1 offset:8192\n"
MF "v[32:35],a[188:191],v[180:183],v[32:35]\n"
"s_add_u32 s96,s96,1024\n"
TR "a[246:247],v1 offset:10240\n"
AR "v231,a51\n"
MF "v[32:35],a[228:231],v[184:187],v[32:35]\n"
LD "v197,s[48:51],s89 offen offset:1024 lds\n"
MF "v[40:43],a[232:235],v[184:187],v[40:43]\n"
"s_mul_i32 s59,s59,40960\n"
TR "v[108:109],v4\n"
AR "v232,a68\n"
MF "v[40:43],a[192:195],v[180:183],v[40:43]\n"
"s_add_u32 s96,s96,s59\n"
TR "v[110:111],v4 offset:2048\n"
AR "v233,a69\n"
MF "v[40:43],a[152:155],v[176:179],v[40:43]\n"
"s_lshl_b32 s59,s95,7\n"
TR "v[112:113],v4 offset:4096\n"
AR "v234,a70\n"
MF "v[48:51],a[156:159],v[176:179],v[48:51]\n"
"s_add_u32 s96,s96,s59\n"
TR "v[114:115],v4 offset:6144\n"
AR "v235,a71\n"
MF "v[48:51],a[196:199],v[180:183],v[48:51]\n"
LD "v198,s[48:51],s89 offen offset:2048 lds\n"
MF "v[48:51],a[236:239],v[184:187],v[48:51]\n"
"s_waitcnt lgkmcnt(4)\n"
TR "v[116:117],v4 offset:8192\n"
"ds_read_b128 a[68:71],v236 offset:53248\n"
MF "v[52:55],a[236:239],a[244:247],v[52:55]\n"
"s_lshl_b32 s3,s92,10\n"
TR "v[176:177],v2\n"
"v_fma_f32 v228,v228,v210,v216\n"
MF "v[52:55],a[196:199],a[240:243],v[52:55]\n"
"s_add_u32 s3,s3,s93\n"
TR "v[178:179],v2 offset:2048\n"
"v_fma_f32 v229,v229,v210,v216\n"
MF "v[52:55],a[156:159],v[188:191],v[52:55]\n"
LD "v199,s[48:51],s89 offen offset:3072 lds\n"
MF "v[44:47],a[152:155],v[188:191],v[44:47]\n"
"s_cmp_lt_u32 s92,40\n"
"s_cselect_b32 s3,s3,0xf0000000\n"
TR "v[180:181],v2 offset:4096\n"
MF "v[44:47],a[192:195],a[240:243],v[44:47]\n"
"s_cmp_lt_u32 s92,40\n"
"s_cselect_b32 s1,s96,0xf0000000\n"
TR "v[182:183],v2 offset:6144\n"
MF "v[44:47],a[232:235],a[244:247],v[44:47]\n"
"s_sub_u32 s92,s92,s4\n"
TR "v[118:119],v4 offset:10240\n"
"v_fma_f32 v230,v230,v210,v216\n"
MF "v[36:39],a[228:231],a[244:247],v[36:39]\n"
"s_sub_u32 s2,s2,204800\n"
TR "v[184:185],v2 offset:8192\n"
"v_fma_f32 v231,v231,v210,v216\n"
MF "v[36:39],a[188:191],a[240:243],v[36:39]\n"
"s_add_u32 m0,s79,32768\n"
TR "v[186:187],v2 offset:10240\n"
"v_fma_f32 v232,v232,v210,v216\n"
MF "v[36:39],a[148:151],v[188:191],v[36:39]\n"
LD "v200,s[48:51],s89 offen lds\n"
MF "v[28:31],a[144:147],v[188:191],v[28:31]\n"
"s_mov_b32 s92,64\n"
TR "v[120:121],v1 offset:49152\n"
"v_fma_f32 v233,v233,v210,v216\n"
MF "v[28:31],a[184:187],a[240:243],v[28:31]\n"
TR "v[122:123],v1 offset:51200\n"
"v_fma_f32 v234,v234,v210,v216\n"
MF "v[28:31],a[224:227],a[244:247],v[28:31]\n"
TR "v[124:125],v1 offset:53248\n"
"v_fma_f32 v235,v235,v210,v216\n"
MF "v[20:23],a[220:223],a[244:247],v[20:23]\n"
LD "v201,s[48:51],s89 offen offset:1024 lds\n"
MF "v[20:23],a[180:183],a[240:243],v[20:23]\n"
TR "v[126:127],v1 offset:55296\n"
CV "v228,v228,v229\n"
MF "v[20:23],a[140:143],v[188:191],v[20:23]\n"
"s_waitcnt lgkmcnt(4)\n"
TR "v[128:129],v1 offset:57344\n"
CV "v229,v230,v231\n"
MF "v[56:59],a[140:143],v[176:179],v[56:59]\n"
TR "v[188:189],v3\n"
CV "v230,v232,v233\n"
MF "v[56:59],a[180:183],v[180:183],v[56:59]\n"
TR "v[190:191],v3 offset:2048\n"
CV "v231,v234,v235\n"
MF "v[56:59],a[220:223],v[184:187],v[56:59]\n"
LD "v202,s[48:51],s89 offen offset:2048 lds\n"
MF "v[64:67],a[224:227],v[184:187],v[64:67]\n"
TR "a[240:241],v3 offset:4096\n"
AW "a48,v228\n"
MF "v[64:67],a[184:187],v[180:183],v[64:67]\n"
TR "a[242:243],v3 offset:6144\n"
AW "a49,v229\n"
MF "v[64:67],a[144:147],v[176:179],v[64:67]\n"
LD "v203,s[48:51],s89 offen offset:3072 lds\n"
MF "v[72:75],a[148:151],v[176:179],v[72:75]\n"
TR "a[244:245],v3 offset:8192\n"
AW "a50,v230\n"
MF "v[72:75],a[188:191],v[180:183],v[72:75]\n"
TR "a[246:247],v3 offset:10240\n"
AW "a51,v231\n"
MF "v[72:75],a[228:231],v[184:187],v[72:75]\n"
TR "v[130:131],v1 offset:59392\n"
AR "v220,a88\n"
MF "v[80:83],a[232:235],v[184:187],v[80:83]\n"
TR "v[132:133],v5\n"
AR "v221,a89\n"
MF "v[80:83],a[192:195],v[180:183],v[80:83]\n"
TR "v[134:135],v5 offset:2048\n"
AR "v222,a90\n"
MF "v[80:83],a[152:155],v[176:179],v[80:83]\n"
TR "v[136:137],v5 offset:4096\n"
AR "v223,a91\n"
MF "v[88:91],a[156:159],v[176:179],v[88:91]\n"
TR "v[138:139],v5 offset:6144\n"
AR "v224,a108\n"
MF "v[88:91],a[196:199],v[180:183],v[88:91]\n"
TR "v[140:141],v5 offset:8192\n"
"s_waitcnt lgkmcnt(0)\n"
MF "v[88:91],a[236:239],v[184:187],v[88:91]\n"
"s_barrier\n"
MF "v[92:95],a[236:239],a[244:247],v[92:95]\n"
TR "v[142:143],v5 offset:10240\n"
AR "v225,a109\n"
MF "v[92:95],a[196:199],a[240:243],v[92:95]\n"
TR "v[144:145],v2 offset:49152\n"
AR "v226,a110\n"
MF "v[92:95],a[156:159],v[188:191],v[92:95]\n"
TR "v[146:147],v2 offset:51200\n"
AR "v227,a111\n"
MF "v[84:87],a[152:155],v[188:191],v[84:87]\n"
"s_nop 0\n"
TR "v[148:149],v2 offset:53248\n"
"ds_read_b128 a[108:111],v237 offset:8192\n"
MF "v[84:87],a[192:195],a[240:243],v[84:87]\n"
TR "v[150:151],v2 offset:55296\n"
"v_fma_f32 v220,v220,v210,v216\n"
MF "v[84:87],a[232:235],a[244:247],v[84:87]\n"
TR "v[152:153],v2 offset:57344\n"
"v_fma_f32 v221,v221,v210,v216\n"
MF "v[76:79],a[228:231],a[244:247],v[76:79]\n"
TR "v[154:155],v2 offset:59392\n"
"v_fma_f32 v222,v222,v210,v216\n"
MF "v[76:79],a[188:191],a[240:243],v[76:79]\n"
TR "v[156:157],v6\n"
"v_fma_f32 v223,v223,v210,v216\n"
MF "v[76:79],a[148:151],v[188:191],v[76:79]\n"
TR "v[158:159],v6 offset:2048\n"
"v_fma_f32 v224,v224,v210,v216\n"
MF "v[68:71],a[144:147],v[188:191],v[68:71]\n"
TR "v[160:161],v6 offset:4096\n"
"v_fma_f32 v225,v225,v210,v216\n"
MF "v[68:71],a[184:187],a[240:243],v[68:71]\n"
".Lpg32h:\n"
"s_cbranch_execz .Lpg36h\n"
TR "v[162:163],v6 offset:6144\n"
"v_fma_f32 v226,v226,v210,v216\n"
MF "v[68:71],a[224:227],a[244:247],v[68:71]\n"
TR "v[164:165],v6 offset:8192\n"
"v_fma_f32 v227,v227,v210,v216\n"
MF "v[60:63],a[220:223],a[244:247],v[60:63]\n"
TR "v[166:167],v6 offset:10240\n"
CV "v220,v220,v221\n"
MF "v[60:63],a[180:183],a[240:243],v[60:63]\n"
TR "v[168:169],v3 offset:49152\n"
CV "v221,v222,v223\n"
MF "v[60:63],a[140:143],v[188:191],v[60:63]\n"
TR "v[170:171],v3 offset:51200\n"
CV "v222,v224,v225\n"
MF "v[16:19],a[0:3],v[96:99],v[16:19]\n"
TR "v[172:173],v3 offset:53248\n"
CV "v223,v226,v227\n"
MF "v[16:19],a[40:43],v[100:103],v[16:19]\n"
TR "v[174:175],v3 offset:55296\n"
AW "a88,v220\n"
MF "v[16:19],a[80:83],v[104:107],v[16:19]\n"
TR "v[204:205],v3 offset:57344\n"
AW "a89,v221\n"
MF "v[16:19],a[20:23],v[108:111],v[16:19]\n"
TR "v[206:207],v3 offset:59392\n"
AW "a90,v222\n"
MF "v[16:19],a[60:63],v[112:115],v[16:19]\n"
TR "v[244:245],v7\n"
AW "a91,v223\n"
MF "v[16:19],a[100:103],v[116:119],v[16:19]\n"
"s_waitcnt lgkmcnt(15)\n"
TR "v[246:247],v7 offset:2048\n"
AR "v228,a12\n"
MF "v[20:23],a[100:103],v[140:143],v[20:23]\n"
TR "v[248:249],v7 offset:4096\n"
AR "v229,a13\n"
MF "v[20:23],a[60:63],v[136:139],v[20:23]\n"
TR "v[250:251],v7 offset:6144\n"
AR "v230,a14\n"
MF "v[20:23],a[20:23],v[132:135],v[20:23]\n"
TR "v[238:239],v7 offset:8192\n"
AR "v231,a15\n"
MF "v[20:23],a[80:83],v[128:131],v[20:23]\n"
TR "v[240:241],v7 offset:10240\n"
AR "v232,a32\n"
MF "v[20:23],a[40:43],v[124:127],v[20:23]\n"
AR "v233,a33\n"
AR "v234,a34\n"
MF "v[20:23],a[0:3],v[120:123],v[20:23]\n"
"s_waitcnt lgkmcnt(12)\n"
AR "v235,a35\n"
"v_fma_f32 v228,v228,v211,v217\n"
MF "v[56:59],a[0:3],v[144:147],v[56:59]\n"
"ds_read_b128 a[32:35],v236 offset:36864\n"
"v_fma_f32 v229,v229,v211,v217\n"
MF "v[56:59],a[40:43],v[148:151],v[56:59]\n"
"v_fma_f32 v230,v230,v211,v217\n"
"v_fma_f32 v231,v231,v211,v217\n"
MF "v[56:59],a[80:83],v[152:155],v[56:59]\n"
"v_fma_f32 v232,v232,v211,v217\n"
"v_fma_f32 v233,v233,v211,v217\n"
MF "v[56:59],a[20:23],v[156:159],v[56:59]\n"
"v_fma_f32 v234,v234,v211,v217\n"
"v_fma_f32 v235,v235,v211,v217\n"
MF "v[56:59],a[60:63],v[160:163],v[56:59]\n"
CV "v228,v228,v229\n"
CV "v229,v230,v231\n"
MF "v[56:59],a[100:103],v[164:167],v[56:59]\n"
"s_waitcnt lgkmcnt(1)\n"
CV "v230,v232,v233\n"
CV "v231,v234,v235\n"
MF "v[60:63],a[100:103],v[238:241],v[60:63]\n"
AW "a12,v228\n"
AW "a13,v229\n"
MF "v[60:63],a[60:63],v[248:251],v[60:63]\n"
AW "a14,v230\n"
AW "a15,v231\n"
MF "v[60:63],a[20:23],v[244:247],v[60:63]\n"
AR "v220,a52\n"
AR "v221,a53\n"
MF "v[60:63],a[80:83],v[204:207],v[60:63]\n"
AR "v222,a54\n"
AR "v223,a55\n"
MF "v[60:63],a[40:43],v[172:175],v[60:63]\n"
AR "v224,a72\n"
AR "v225,a73\n"
MF "v[60:63],a[0:3],v[168:171],v[60:63]\n"
AR "v226,a74\n"
AR "v227,a75\n"
MF "v[68:71],a[4:7],v[168:171],v[68:71]\n"
"ds_read_b128 a[72:75],v236 offset:57344\n"
"v_fma_f32 v220,v220,v211,v217\n"
MF "v[68:71],a[44:47],v[172:175],v[68:71]\n"
"v_fma_f32 v221,v221,v211,v217\n"
"v_fma_f32 v222,v222,v211,v217\n"
MF "v[68:71],a[84:87],v[204:207],v[68:71]\n"
"v_fma_f32 v223,v223,v211,v217\n"
"v_fma_f32 v224,v224,v211,v217\n"
MF "v[68:71],a[24:27],v[244:247],v[68:71]\n"
"v_fma_f32 v225,v225,v211,v217\n"
"v_fma_f32 v226,v226,v211,v217\n"
MF "v[68:71],a[64:67],v[248:251],v[68:71]\n"
"v_fma_f32 v227,v227,v211,v217\n"
CV "v220,v220,v221\n"
MF "v[68:71],a[104:107],v[238:241],v[68:71]\n"
"s_waitcnt lgkmcnt(0)\n"
CV "v221,v222,v223\n"
CV "v222,v224,v225\n"
MF "v[64:67],a[104:107],v[164:167],v[64:67]\n"
"s_barrier\n"
MF "v[64:67],a[64:67],v[160:163],v[64:67]\n"
CV "v223,v226,v227\n"
AW "a52,v220\n"
MF "v[64:67],a[24:27],v[156:159],v[64:67]\n"
AW "a53,v221\n"
AW "a54,v222\n"
MF "v[64:67],a[84:87],v[152:155],v[64:67]\n"
AW "a55,v223\n"
AR "v228,a92\n"
MF "v[64:67],a[44:47],v[148:151],v[64:67]\n"
AR "v229,a93\n"
AR "v230,a94\n"
MF "v[64:67],a[4:7],v[144:147],v[64:67]\n"
AR "v231,a95\n"
AR "v232,a112\n"
MF "v[28:31],a[4:7],v[120:123],v[28:31]\n"
AR "v233,a113\n"
AR "v234,a114\n"
MF "v[28:31],a[44:47],v[124:127],v[28:31]\n"
AR "v235,a115\n"
"v_fma_f32 v228,v228,v211,v217\n"
MF "v[28:31],a[84:87],v[128:131],v[28:31]\n"
"ds_read_b128 a[112:115],v237 offset:12288\n"
"v_fma_f32 v229,v229,v211,v217\n"
MF "v[28:31],a[24:27],v[132:135],v[28:31]\n"
"v_fma_f32 v230,v230,v211,v217\n"
"v_fma_f32 v231,v231,v211,v217\n"
MF "v[28:31],a[64:67],v[136:139],v[28:31]\n"
"v_fma_f32 v232,v232,v211,v217\n"
"v_fma_f32 v233,v233,v211,v217\n"
MF "v[28:31],a[104:107],v[140:143],v[28:31]\n"
"v_fma_f32 v234,v234,v211,v217\n"
"v_fma_f32 v235,v235,v211,v217\n"
MF "v[24:27],a[104:107],v[116:119],v[24:27]\n"
CV "v228,v228,v229\n"
CV "v229,v230,v231\n"
MF "v[24:27],a[64:67],v[112:115],v[24:27]\n"
CV "v230,v232,v233\n"
CV "v231,v234,v235\n"
MF "v[24:27],a[24:27],v[108:111],v[24:27]\n"
AW "a92,v228\n"
AW "a93,v229\n"
MF "v[24:27],a[84:87],v[104:107],v[24:27]\n"
AW "a94,v230\n"
AW "a95,v231\n"
MF "v[24:27],a[44:47],v[100:103],v[24:27]\n"
AR "v220,a16\n"
AR "v221,a17\n"
MF "v[24:27],a[4:7],v[96:99],v[24:27]\n"
AR "v222,a18\n"
AR "v223,a19\n"
MF "v[32:35],a[8:11],v[96:99],v[32:35]\n"
AR "v224,a36\n"
AR "v225,a37\n"
MF "v[32:35],a[48:51],v[100:103],v[32:35]\n"
AR "v226,a38\n"
AR "v227,a39\n"
MF "v[32:35],a[88:91],v[104:107],v[32:35]\n"
"ds_read_b128 a[36:39],v236 offset:40960\n"
"v_fma_f32 v220,v220,v212,v218\n"
MF "v[32:35],a[28:31],v[108:111],v[32:35]\n"
"v_fma_f32 v221,v221,v212,v218\n"
"v_fma_f32 v222,v222,v212,v218\n"
MF "v[32:35],a[68:71],v[112:115],v[32:35]\n"
"v_fma_f32 v223,v223,v212,v218\n"
"v_fma_f32 v224,v224,v212,v218\n"
MF "v[32:35],a[108:111],v[116:119],v[32:35]\n"
"v_fma_f32 v225,v225,v212,v218\n"
"v_fma_f32 v226,v226,v212,v218\n"
MF "v[36:39],a[108:111],v[140:143],v[36:39]\n"
"v_fma_f32 v227,v227,v212,v218\n"
CV "v220,v220,v221\n"
MF "v[36:39],a[68:71],v[136:139],v[36:39]\n"
CV "v221,v222,v223\n"
CV "v222,v224,v225\n"
MF "v[36:39],a[28:31],v[132:135],v[36:39]\n"
CV "v223,v226,v227\n"
AW "a16,v220\n"
MF "v[36:39],a[88:91],v[128:131],v[36:39]\n"
AW "a17,v221\n"
AW "a18,v222\n"
MF "v[36:39],a[48:51],v[124:127],v[36:39]\n"
AW "a19,v223\n"
AR "v228,a56\n"
MF "v[36:39],a[8:11],v[120:123],v[36:39]\n"
AR "v229,a57\n"
AR "v230,a58\n"
MF "v[72:75],a[8:11],v[144:147],v[72:75]\n"
AR "v231,a59\n"
AR "v232,a76\n"
MF "v[72:75],a[48:51],v[148:151],v[72:75]\n"
AR "v233,a77\n"
AR "v234,a78\n"
MF "v[72:75],a[88:91],v[152:155],v[72:75]\n"
AR "v235,a79\n"
"v_fma_f32 v228,v228,v212,v218\n"
MF "v[72:75],a[28:31],v[156:159],v[72:75]\n"
"ds_read_b128 a[76:79],v236 offset:61440\n"
"v_fma_f32 v229,v229,v212,v218\n"
MF "v[72:75],a[68:71],v[160:163],v[72:75]\n"
"v_fma_f32 v230,v230,v212,v218\n"
"v_fma_f32 v231,v231,v212,v218\n"
MF "v[72:75],a[108:111],v[164:167],v[72:75]\n"
"v_fma_f32 v232,v232,v212,v218\n"
"v_fma_f32 v233,v233,v212,v218\n"
MF "v[76:79],a[108:111],v[238:241],v[76:79]\n"
"v_fma_f32 v234,v234,v212,v218\n"
"v_fma_f32 v235,v235,v212,v218\n"
MF "v[76:79],a[68:71],v[248:251],v[76:79]\n"
CV "v228,v228,v229\n"
CV "v229,v230,v231\n"
MF "v[76:79],a[28:31],v[244:247],v[76:79]\n"
CV "v230,v232,v233\n"
CV "v231,v234,v235\n"
MF "v[76:79],a[88:91],v[204:207],v[76:79]\n"
AW "a56,v228\n"
AW "a57,v229\n"
MF "v[76:79],a[48:51],v[172:175],v[76:79]\n"
AW "a58,v230\n"
AW "a59,v231\n"
MF "v[76:79],a[8:11],v[168:171],v[76:79]\n"
"s_waitcnt lgkmcnt(2)\n"
AR "v220,a96\n"
AR "v221,a97\n"
MF "v[84:87],a[12:15],v[168:171],v[84:87]\n"
AR "v222,a98\n"
AR "v223,a99\n"
MF "v[84:87],a[52:55],v[172:175],v[84:87]\n"
AR "v224,a116\n"
AR "v225,a117\n"
MF "v[84:87],a[92:95],v[204:207],v[84:87]\n"
AR "v226,a118\n"
AR "v227,a119\n"
MF "v[84:87],a[32:35],v[244:247],v[84:87]\n"
"ds_read_b128 a[116:119],v237 offset:16384\n"
"v_fma_f32 v220,v220,v212,v218\n"
MF "v[84:87],a[72:75],v[248:251],v[84:87]\n"
"v_fma_f32 v221,v221,v212,v218\n"
"v_fma_f32 v222,v222,v212,v218\n"
MF "v[84:87],a[112:115],v[238:241],v[84:87]\n"
"v_fma_f32 v223,v223,v212,v218\n"
"v_fma_f32 v224,v224,v212,v218\n"
MF "v[80:83],a[112:115],v[164:167],v[80:83]\n"
"v_fma_f32 v225,v225,v212,v218\n"
"v_fma_f32 v226,v226,v212,v218\n"
MF "v[80:83],a[72:75],v[160:163],v[80:83]\n"
"v_fma_f32 v227,v227,v212,v218\n"
CV "v220,v220,v221\n"
MF "v[80:83],a[32:35],v[156:159],v[80:83]\n"
CV "v221,v222,v223\n"
CV "v222,v224,v225\n"
MF "v[80:83],a[92:95],v[152:155],v[80:83]\n"
CV "v223,v226,v227\n"
AW "a96,v220\n"
MF "v[80:83],a[52:55],v[148:151],v[80:83]\n"
AW "a97,v221\n"
AW "a98,v222\n"
MF "v[80:83],a[12:15],v[144:147],v[80:83]\n"
AW "a99,v223\n"
"v_mov_b32 v15,0\n"
MF "v[44:47],a[12:15],v[120:123],v[44:47]\n"
"v_lshrrev_b32 v242,5,v9\n"
"v_min_u32 v242,23,v242\n"
MF "v[44:47],a[52:55],v[124:127],v[44:47]\n"
"v_mul_u32_u24 v242,160,v242\n"
"v_and_or_b32 v242,v9,16,v242\n"
MF "v[44:47],a[92:95],v[128:131],v[44:47]\n"
"v_lshlrev_b32 v242,3,v242\n"
AR "v8,a252\n"
MF "v[44:47],a[32:35],v[132:135],v[44:47]\n"
AR "v11,a253\n"
"v_bfe_u32 v224,v9,5,3\n"
MF "v[44:47],a[72:75],v[136:139],v[44:47]\n"
"s_add_u32 s62,s74,s68\n"
"s_add_u32 s68,s68,256\n"
"s_and_b32 s68,s68,768\n"
MF "v[44:47],a[112:115],v[140:143],v[44:47]\n"
"buffer_load_dword v243,v242,s[48:51],s62 offen\n"
MF "v[40:43],a[112:115],v[116:119],v[40:43]\n"
"v_bfe_u32 v225,v9,8,2\n"
"v_lshlrev_b32 v225,1,v225\n"
MF "v[40:43],a[72:75],v[112:115],v[40:43]\n"
"v_mov_b32 v14,s76\n"
"s_add_u32 s76,s76,1\n"
MF "v[40:43],a[32:35],v[108:111],v[40:43]\n"
"buffer_store_dword v14,v15,s[40:43],s94 offen sc1\n"
MF "v[40:43],a[92:95],v[104:107],v[40:43]\n"
"v_and_b32 v226,16,v9\n"
"v_lshlrev_b32_e64 v226,v226,s9\n"
MF "v[40:43],a[52:55],v[100:103],v[40:43]\n"
"v_sub_u32 v224,v224,v225\n"
"v_cmp_eq_u32 vcc,0,v224\n"
MF "v[40:43],a[12:15],v[96:99],v[40:43]\n"
"s_waitcnt lgkmcnt(0)\n"
"v_cndmask_b32 v220,0,v226,vcc\n"
"v_cmp_eq_u32 vcc,1,v224\n"
MF "v[48:51],a[16:19],v[96:99],v[48:51]\n"
"s_nop 0\n"
"v_cndmask_b32 v221,0,v226,vcc\n"
AW "a248,0\n"
MF "v[48:51],a[56:59],v[100:103],v[48:51]\n"
AW "a249,0\n"
AW "a250,v220\n"
MF "v[48:51],a[96:99],v[104:107],v[48:51]\n"
AW "a251,v221\n"
AW "a252,v220\n"
MF "v[48:51],a[36:39],v[108:111],v[48:51]\n"
AW "a253,v221\n"
AW "a254,0\n"
MF "v[48:51],a[76:79],v[112:115],v[48:51]\n"
AW "a255,0\n"
"v_bfe_u32 v12,v9,4,4\n"
MF "v[48:51],a[116:119],v[116:119],v[48:51]\n"
"v_lshlrev_b32 v12,2,v12\n"
"v_lshrrev_b32 v13,2,v9\n"
MF "v[52:55],a[116:119],v[140:143],v[52:55]\n"
"v_add_u32 v13,s58,v13\n"
"v_add_u32 v4,24576,v4\n"
MF "v[52:55],a[76:79],v[136:139],v[52:55]\n"
"s_waitcnt vmcnt(2) lgkmcnt(0)\n"
"v_add_u32 v5,24576,v5\n"
"v_add_u32 v6,24576,v6\n"
MF "v[52:55],a[36:39],v[132:135],v[52:55]\n"
"s_barrier\n"
MF "v[52:55],a[96:99],v[128:131],v[52:55]\n"
TR "v[176:177],v0 offset:24576\n"
"v_add_u32 v7,24576,v7\n"
MF "v[52:55],a[56:59],v[124:127],v[52:55]\n"
TR "v[178:179],v0 offset:26624\n"
MF "v[52:55],a[16:19],v[120:123],v[52:55]\n"
MF "v[88:91],a[16:19],v[144:147],v[88:91]\n"
TR "v[180:181],v0 offset:28672\n"
MF "v[88:91],a[56:59],v[148:151],v[88:91]\n"
TR "v[182:183],v0 offset:30720\n"
MF "v[88:91],a[96:99],v[152:155],v[88:91]\n"
MF "v[88:91],a[36:39],v[156:159],v[88:91]\n"
TR "v[184:185],v0 offset:32768\n"
MF "v[88:91],a[76:79],v[160:163],v[88:91]\n"
TR "v[186:187],v0 offset:34816\n"
MF "v[88:91],a[116:119],v[164:167],v[88:91]\n"
MF "v[92:95],a[116:119],v[238:241],v[92:95]\n"
MF "v[92:95],a[76:79],v[248:251],v[92:95]\n"
MF "v[92:95],a[36:39],v[244:247],v[92:95]\n"
MF "v[92:95],a[96:99],v[204:207],v[92:95]\n"
MF "v[92:95],a[56:59],v[172:175],v[92:95]\n"
MF "v[92:95],a[16:19],v[168:171],v[92:95]\n"
"s_waitcnt lgkmcnt(0)\n"
".Lpl15:\n"
MF "v[96:99],a[0:3],v[176:179],v[252:255]\n"
TR "v[188:189],v1 offset:24576\n"
MF "v[96:99],a[40:43],v[180:183],v[96:99]\n"
TR "v[190:191],v1 offset:26624\n"
MF "v[96:99],a[80:83],v[184:187],v[96:99]\n"
MF "v[104:107],a[84:87],v[184:187],v[252:255]\n"
TR "a[240:241],v1 offset:28672\n"
MF "v[104:107],a[44:47],v[180:183],v[104:107]\n"
TR "a[242:243],v1 offset:30720\n"
MF "v[104:107],a[4:7],v[176:179],v[104:107]\n"
MF "v[112:115],a[8:11],v[176:179],v[252:255]\n"
TR "a[244:245],v1 offset:32768\n"
MF "v[112:115],a[48:51],v[180:183],v[112:115]\n"
TR "a[246:247],v1 offset:34816\n"
MF "v[112:115],a[88:91],v[184:187],v[112:115]\n"
MF "v[120:123],a[92:95],v[184:187],v[252:255]\n"
MF "v[120:123],a[52:55],v[180:183],v[120:123]\n"
MF "v[120:123],a[12:15],v[176:179],v[120:123]\n"
MF "v[128:131],a[16:19],v[176:179],v[252:255]\n"
MF "v[128:131],a[56:59],v[180:183],v[128:131]\n"
MF "v[128:131],a[96:99],v[184:187],v[128:131]\n"
"s_waitcnt lgkmcnt(0)\n"
MF "v[132:135],a[96:99],a[244:247],v[252:255]\n"
TR "v[176:177],v2 offset:24576\n"
MF "v[132:135],a[56:59],a[240:243],v[132:135]\n"
TR "v[178:179],v2 offset:26624\n"
MF "v[132:135],a[16:19],v[188:191],v[132:135]\n"
MF "v[124:127],a[12:15],v[188:191],v[252:255]\n"
TR "v[180:181],v2 offset:28672\n"
MF "v[124:127],a[52:55],a[240:243],v[124:127]\n"
TR "v[182:183],v2 offset:30720\n"
MF "v[124:127],a[92:95],a[244:247],v[124:127]\n"
MF "v[116:119],a[88:91],a[244:247],v[252:255]\n"
TR "v[184:185],v2 offset:32768\n"
MF "v[116:119],a[48:51],a[240:243],v[116:119]\n"
TR "v[186:187],v2 offset:34816\n"
MF "v[116:119],a[8:11],v[188:191],v[116:119]\n"
MF "v[108:111],a[4:7],v[188:191],v[252:255]\n"
MF "v[108:111],a[44:47],a[240:243],v[108:111]\n"
MF "v[108:111],a[84:87],a[244:247],v[108:111]\n"
MF "v[100:103],a[80:83],a[244:247],v[252:255]\n"
MF "v[100:103],a[40:43],a[240:243],v[100:103]\n"
MF "v[100:103],a[0:3],v[188:191],v[100:103]\n"
"s_waitcnt lgkmcnt(0)\n"
MF "v[136:139],a[0:3],v[176:179],v[252:255]\n"
TR "v[188:189],v3 offset:24576\n"
MF "v[136:139],a[40:43],v[180:183],v[136:139]\n"
TR "v[190:191],v3 offset:26624\n"
MF "v[136:139],a[80:83],v[184:187],v[136:139]\n"
MF "v[144:147],a[84:87],v[184:187],v[252:255]\n"
TR "a[240:241],v3 offset:28672\n"
MF "v[144:147],a[44:47],v[180:183],v[144:147]\n"
TR "a[242:243],v3 offset:30720\n"
MF "v[144:147],a[4:7],v[176:179],v[144:147]\n"
MF "v[152:155],a[8:11],v[176:179],v[252:255]\n"
TR "a[244:245],v3 offset:32768\n"
MF "v[152:155],a[48:51],v[180:183],v[152:155]\n"
TR "a[246:247],v3 offset:34816\n"
MF "v[152:155],a[88:91],v[184:187],v[152:155]\n"
MF "v[160:163],a[92:95],v[184:187],v[252:255]\n"
MF "v[160:163],a[52:55],v[180:183],v[160:163]\n"
MF "v[160:163],a[12:15],v[176:179],v[160:163]\n"
MF "v[168:171],a[16:19],v[176:179],v[252:255]\n"
MF "v[168:171],a[56:59],v[180:183],v[168:171]\n"
"s_waitcnt lgkmcnt(0)\n"
MF "v[168:171],a[96:99],v[184:187],v[168:171]\n"
"s_barrier\n"
MF "v[172:175],a[96:99],a[244:247],v[252:255]\n"
TR "v[176:177],v0\n"
MF "v[172:175],a[56:59],a[240:243],v[172:175]\n"
TR "v[178:179],v0 offset:2048\n"
MF "v[172:175],a[16:19],v[188:191],v[172:175]\n"
"ds_read2st64_b32 v[192:193],v13 offset0:192 offset1:193\n"
MF "v[164:167],a[12:15],v[188:191],v[252:255]\n"
TR "v[180:181],v0 offset:4096\n"
MF "v[164:167],a[52:55],a[240:243],v[164:167]\n"
TR "v[182:183],v0 offset:6144\n"
MF "v[164:167],a[92:95],a[244:247],v[164:167]\n"
"ds_read2st64_b32 v[194:195],v13 offset0:194 offset1:195\n"
MF "v[156:159],a[88:91],a[244:247],v[252:255]\n"
TR "v[184:185],v0 offset:8192\n"
MF "v[156:159],a[48:51],a[240:243],v[156:159]\n"
TR "v[186:187],v0 offset:10240\n"
MF "v[156:159],a[8:11],v[188:191],v[156:159]\n"
"ds_read2st64_b32 v[196:197],v13 offset0:196 offset1:197\n"
MF "v[148:151],a[4:7],v[188:191],v[252:255]\n"
"ds_read2st64_b32 v[198:199],v13 offset0:198 offset1:199\n"
MF "v[148:151],a[44:47],a[240:243],v[148:151]\n"
MF "v[148:151],a[84:87],a[244:247],v[148:151]\n"
MF "v[140:143],a[80:83],a[244:247],v[252:255]\n"
MF "v[140:143],a[40:43],a[240:243],v[140:143]\n"
MF "v[140:143],a[0:3],v[188:191],v[140:143]\n"
"s_waitcnt lgkmcnt(2)\n"
MF "v[96:99],a[120:123],v[176:179],v[96:99]\n"
"s_waitcnt lgkmcnt(0)\n"
TR "v[188:189],v1\n"
"v_add_f32 v16,v16,v192\n"
MF "v[96:99],a[160:163],v[180:183],v[96:99]\n"
TR "v[190:191],v1 offset:2048\n"
"v_add_f32 v17,v17,v193\n"
MF "v[96:99],a[200:203],v[184:187],v[96:99]\n"
"ds_read2st64_b32 v[200:201],v13 offset0:200 offset1:201\n"
"v_add_f32 v18,v18,v194\n"
"s_waitcnt vmcnt(0)\n"
"v_mov_b32 v14,s76\n"
"s_add_u32 s76,s76,1\n"
MF "v[104:107],a[204:207],v[184:187],v[104:107]\n"
TR "a[240:241],v1 offset:4096\n"
"v_add_f32 v19,v19,v195\n"
"buffer_store_dword v14,v15,s[40:43],s94 offen sc1\n"
MF "v[104:107],a[164:167],v[180:183],v[104:107]\n"
TR "a[242:243],v1 offset:6144\n"
"v_add_f32 v20,v20,v196\n"
MF "v[104:107],a[124:127],v[176:179],v[104:107]\n"
"ds_read2st64_b32 v[202:203],v13 offset0:202 offset1:203\n"
"v_add_f32 v21,v21,v197\n"
MF "v[112:115],a[128:131],v[176:179],v[112:115]\n"
TR "a[244:245],v1 offset:8192\n"
"v_add_f32 v22,v22,v198\n"
MF "v[112:115],a[168:171],v[180:183],v[112:115]\n"
TR "a[246:247],v1 offset:10240\n"
"v_add_f32 v23,v23,v199\n"
MF "v[112:115],a[208:211],v[184:187],v[112:115]\n"
"ds_read2st64_b32 v[204:205],v13 offset0:204 offset1:205\n"
CV "v16,v16,v17\n"
MF "v[120:123],a[212:215],v[184:187],v[120:123]\n"
"ds_read2st64_b32 v[206:207],v13 offset0:206 offset1:207\n"
CV "v17,v18,v19\n"
MF "v[120:123],a[172:175],v[180:183],v[120:123]\n"
"s_add_u32 s87,s90,0\n"
CV "v18,v20,v21\n"
CV "v19,v22,v23\n"
MF "v[120:123],a[132:135],v[176:179],v[120:123]\n"
MF "v[128:131],a[136:139],v[176:179],v[128:131]\n"
"buffer_store_dwordx4 v[16:19],v9,s[40:43],s87 offen sc1\n"
MF "v[128:131],a[176:179],v[180:183],v[128:131]\n"
MF "v[128:131],a[216:219],v[184:187],v[128:131]\n"
"s_waitcnt lgkmcnt(2)\n"
MF "v[132:135],a[216:219],a[244:247],v[132:135]\n"
"s_waitcnt lgkmcnt(0)\n"
TR "v[176:177],v2\n"
"v_add_f32 v24,v24,v200\n"
MF "v[132:135],a[176:179],a[240:243],v[132:135]\n"
TR "v[178:179],v2 offset:2048\n"
"v_add_f32 v25,v25,v201\n"
MF "v[132:135],a[136:139],v[188:191],v[132:135]\n"
"ds_read2st64_b32 v[192:193],v13 offset0:208 offset1:209\n"
"v_add_f32 v26,v26,v202\n"
MF "v[124:127],a[132:135],v[188:191],v[124:127]\n"
TR "v[180:181],v2 offset:4096\n"
"v_add_f32 v27,v27,v203\n"
MF "v[124:127],a[172:175],a[240:243],v[124:127]\n"
TR "v[182:183],v2 offset:6144\n"
"v_add_f32 v28,v28,v204\n"
MF "v[124:127],a[212:215],a[244:247],v[124:127]\n"
"ds_read2st64_b32 v[194:195],v13 offset0:210 offset1:211\n"
"v_add_f32 v29,v29,v205\n"
MF "v[116:119],a[208:211],a[244:247],v[116:119]\n"
TR "v[184:185],v2 offset:8192\n"
"v_add_f32 v30,v30,v206\n"
MF "v[116:119],a[168:171],a[240:243],v[116:119]\n"
TR "v[186:187],v2 offset:10240\n"
"v_add_f32 v31,v31,v207\n"
MF "v[116:119],a[128:131],v[188:191],v[116:119]\n"
"ds_read2st64_b32 v[196:197],v13 offset0:212 offset1:213\n"
CV "v24,v24,v25\n"
MF "v[108:111],a[124:127],v[188:191],v[108:111]\n"
"ds_read2st64_b32 v[198:199],v13 offset0:214 offset1:215\n"
CV "v25,v26,v27\n"
MF "v[108:111],a[164:167],a[240:243],v[108:111]\n"
"s_add_u32 s87,s90,1024\n"
CV "v26,v28,v29\n"
CV "v27,v30,v31\n"
MF "v[108:111],a[204:207],a[244:247],v[108:111]\n"
MF "v[100:103],a[200:203],a[244:247],v[100:103]\n"
"buffer_store_dwordx4 v[24:27],v9,s[40:43],s87 offen sc1\n"
MF "v[100:103],a[160:163],a[240:243],v[100:103]\n"
MF "v[100:103],a[120:123],v[188:191],v[100:103]\n"
"s_waitcnt lgkmcnt(2)\n"
MF "v[136:139],a[120:123],v[176:179],v[136:139]\n"
"s_waitcnt lgkmcnt(0)\n"
TR "v[188:189],v3\n"
"v_add_f32 v32,v32,v192\n"
MF "v[136:139],a[160:163],v[180:183],v[136:139]\n"
".Lpg33h:\n"
"s_cbranch_execz .Lpg37h\n"
TR "v[190:191],v3 offset:2048\n"
"v_add_f32 v33,v33,v193\n"
MF "v[136:139],a[200:203],v[184:187],v[136:139]\n"
"ds_read2st64_b32 v[200:201],v13 offset0:216 offset1:217\n"
"v_add_f32 v34,v34,v194\n"
MF "v[144:147],a[204:207],v[184:187],v[144:147]\n"
TR "a[240:241],v3 offset:4096\n"
"v_add_f32 v35,v35,v195\n"
MF "v[144:147],a[164:167],v[180:183],v[144:147]\n"
TR "a[242:243],v3 offset:6144\n"
"v_add_f32 v36,v36,v196\n"
MF "v[144:147],a[124:127],v[176:179],v[144:147]\n"
"ds_read2st64_b32 v[202:203],v13 offset0:218 offset1:219\n"
"v_add_f32 v37,v37,v197\n"
MF "v[152:155],a[128:131],v[176:179],v[152:155]\n"
TR "a[244:245],v3 offset:8192\n"
"v_add_f32 v38,v38,v198\n"
MF "v[152:155],a[168:171],v[180:183],v[152:155]\n"
TR "a[246:247],v3 offset:10240\n"
"v_add_f32 v39,v39,v199\n"
MF "v[152:155],a[208:211],v[184:187],v[152:155]\n"
"ds_read2st64_b32 v[204:205],v13 offset0:220 offset1:221\n"
CV "v32,v32,v33\n"
MF "v[160:163],a[212:215],v[184:187],v[160:163]\n"
"ds_read2st64_b32 v[206:207],v13 offset0:222 offset1:223\n"
CV "v33,v34,v35\n"
MF "v[160:163],a[172:175],v[180:183],v[160:163]\n"
"s_add_u32 s87,s90,2048\n"
CV "v34,v36,v37\n"
CV "v35,v38,v39\n"
MF "v[160:163],a[132:135],v[176:179],v[160:163]\n"
MF "v[168:171],a[136:139],v[176:179],v[168:171]\n"
"buffer_store_dwordx4 v[32:35],v9,s[40:43],s87 offen sc1\n"
MF "v[168:171],a[176:179],v[180:183],v[168:171]\n"
"s_waitcnt lgkmcnt(0)\n"
MF "v[168:171],a[216:219],v[184:187],v[168:171]\n"
"s_barrier\n"
MF "v[172:175],a[216:219],a[244:247],v[172:175]\n"
TR "v[176:177],v0 offset:49152\n"
"v_add_f32 v40,v40,v200\n"
MF "v[172:175],a[176:179],a[240:243],v[172:175]\n"
TR "v[178:179],v0 offset:51200\n"
"v_add_f32 v41,v41,v201\n"
MF "v[172:175],a[136:139],v[188:191],v[172:175]\n"
"ds_read2st64_b32 v[192:193],v13 offset0:224 offset1:225\n"
"v_add_f32 v42,v42,v202\n"
MF "v[164:167],a[132:135],v[188:191],v[164:167]\n"
TR "v[180:181],v0 offset:53248\n"
"v_add_f32 v43,v43,v203\n"
MF "v[164:167],a[172:175],a[240:243],v[164:167]\n"
TR "v[182:183],v0 offset:55296\n"
"v_add_f32 v44,v44,v204\n"
MF "v[164:167],a[212:215],a[244:247],v[164:167]\n"
"ds_read2st64_b32 v[194:195],v13 offset0:226 offset1:227\n"
"v_add_f32 v45,v45,v205\n"
MF "v[156:159],a[208:211],a[244:247],v[156:159]\n"
TR "v[184:185],v0 offset:57344\n"
"v_add_f32 v46,v46,v206\n"
MF "v[156:159],a[168:171],a[240:243],v[156:159]\n"
TR "v[186:187],v0 offset:59392\n"
"v_add_f32 v47,v47,v207\n"
MF "v[156:159],a[128:131],v[188:191],v[156:159]\n"
"ds_read2st64_b32 v[196:197],v13 offset0:228 offset1:229\n"
CV "v40,v40,v41\n"
MF "v[148:151],a[124:127],v[188:191],v[148:151]\n"
"ds_read2st64_b32 v[198:199],v13 offset0:230 offset1:231\n"
CV "v41,v42,v43\n"
MF "v[148:151],a[164:167],a[240:243],v[148:151]\n"
"s_add_u32 s87,s90,3072\n"
CV "v42,v44,v45\n"
CV "v43,v46,v47\n"
MF "v[148:151],a[204:207],a[244:247],v[148:151]\n"
MF "v[140:143],a[200:203],a[244:247],v[140:143]\n"
"buffer_store_dwordx4 v[40:43],v9,s[40:43],s87 offen sc1\n"
MF "v[140:143],a[160:163],a[240:243],v[140:143]\n"
MF "v[140:143],a[120:123],v[188:191],v[140:143]\n"
"s_waitcnt lgkmcnt(2)\n"
MF "v[96:99],a[20:23],v[176:179],v[96:99]\n"
"s_waitcnt lgkmcnt(0)\n"
TR "v[188:189],v1 offset:49152\n"
"v_add_f32 v48,v48,v192\n"
MF "v[96:99],a[60:63],v[180:183],v[96:99]\n"
TR "v[190:191],v1 offset:51200\n"
"v_add_f32 v49,v49,v193\n"
MF "v[96:99],a[100:103],v[184:187],v[96:99]\n"
"ds_read2st64_b32 v[200:201],v13 offset0:0 offset1:1\n"
"v_add_f32 v50,v50,v194\n"
MF "v[104:107],a[104:107],v[184:187],v[104:107]\n"
TR "a[240:241],v1 offset:53248\n"
"v_add_f32 v51,v51,v195\n"
MF "v[104:107],a[64:67],v[180:183],v[104:107]\n"
TR "a[242:243],v1 offset:55296\n"
"v_add_f32 v52,v52,v196\n"
MF "v[104:107],a[24:27],v[176:179],v[104:107]\n"
"ds_read2st64_b32 v[202:203],v13 offset0:2 offset1:3\n"
"v_add_f32 v53,v53,v197\n"
MF "v[112:115],a[28:31],v[176:179],v[112:115]\n"
TR "a[244:245],v1 offset:57344\n"
"v_add_f32 v54,v54,v198\n"
MF "v[112:115],a[68:71],v[180:183],v[112:115]\n"
TR "a[246:247],v1 offset:59392\n"
"v_add_f32 v55,v55,v199\n"
MF "v[112:115],a[108:111],v[184:187],v[112:115]\n"
"ds_read2st64_b32 v[204:205],v13 offset0:4 offset1:5\n"
CV "v48,v48,v49\n"
MF "v[120:123],a[112:115],v[184:187],v[120:123]\n"
"ds_read2st64_b32 v[206:207],v13 offset0:6 offset1:7\n"
CV "v49,v50,v51\n"
MF "v[120:123],a[72:75],v[180:183],v[120:123]\n"
"s_add_u32 s87,s90,4096\n"
CV "v50,v52,v53\n"
CV "v51,v54,v55\n"
MF "v[120:123],a[32:35],v[176:179],v[120:123]\n"
MF "v[128:131],a[36:39],v[176:179],v[128:131]\n"
"buffer_store_dwordx4 v[48:51],v9,s[40:43],s87 offen sc1\n"
MF "v[128:131],a[76:79],v[180:183],v[128:131]\n"
MF "v[128:131],a[116:119],v[184:187],v[128:131]\n"
"s_waitcnt lgkmcnt(2)\n"
MF "v[132:135],a[116:119],a[244:247],v[132:135]\n"
"s_waitcnt lgkmcnt(0)\n"
TR "v[176:177],v2 offset:49152\n"
"v_add_f32 v56,v56,v200\n"
MF "v[132:135],a[76:79],a[240:243],v[132:135]\n"
TR "v[178:179],v2 offset:51200\n"
"v_add_f32 v57,v57,v201\n"
MF "v[132:135],a[36:39],v[188:191],v[132:135]\n"
"ds_read2st64_b32 v[192:193],v13 offset0:8 offset1:9\n"
"v_add_f32 v58,v58,v202\n"
MF "v[124:127],a[32:35],v[188:191],v[124:127]\n"
TR "v[180:181],v2 offset:53248\n"
"v_add_f32 v59,v59,v203\n"
MF "v[124:127],a[72:75],a[240:243],v[124:127]\n"
TR "v[182:183],v2 offset:55296\n"
"v_add_f32 v60,v60,v204\n"
MF "v[124:127],a[112:115],a[244:247],v[124:127]\n"
"ds_read2st64_b32 v[194:195],v13 offset0:10 offset1:11\n"
"v_add_f32 v61,v61,v205\n"
MF "v[116:119],a[108:111],a[244:247],v[116:119]\n"
TR "v[184:185],v2 offset:57344\n"
"v_add_f32 v62,v62,v206\n"
MF "v[116:119],a[68:71],a[240:243],v[116:119]\n"
TR "v[186:187],v2 offset:59392\n"
"v_add_f32 v63,v63,v207\n"
MF "v[116:119],a[28:31],v[188:191],v[116:119]\n"
"ds_read2st64_b32 v[196:197],v13 offset0:12 offset1:13\n"
CV "v56,v56,v57\n"
MF "v[108:111],a[24:27],v[188:191],v[108:111]\n"
"ds_read2st64_b32 v[198:199],v13 offset0:14 offset1:15\n"
CV "v57,v58,v59\n"
MF "v[108:111],a[64:67],a[240:243],v[108:111]\n"
"s_add_u32 s87,s90,10240\n"
CV "v58,v60,v61\n"
CV "v59,v62,v63\n"
MF "v[108:111],a[104:107],a[244:247],v[108:111]\n"
MF "v[100:103],a[100:103],a[244:247],v[100:103]\n"
"buffer_store_dwordx4 v[56:59],v9,s[40:43],s87 offen sc1\n"
MF "v[100:103],a[60:63],a[240:243],v[100:103]\n"
MF "v[100:103],a[20:23],v[188:191],v[100:103]\n"
"s_waitcnt lgkmcnt(2)\n"
MF "v[136:139],a[20:23],v[176:179],v[136:139]\n"
"s_waitcnt lgkmcnt(0)\n"
TR "v[188:189],v3 offset:49152\n"
"v_add_f32 v64,v64,v192\n"
MF "v[136:139],a[60:63],v[180:183],v[136:139]\n"
TR "v[190:191],v3 offset:51200\n"
"v_add_f32 v65,v65,v193\n"
MF "v[136:139],a[100:103],v[184:187],v[136:139]\n"
"ds_read2st64_b32 v[200:201],v13 offset0:16 offset1:17\n"
"v_add_f32 v66,v66,v194\n"
MF "v[144:147],a[104:107],v[184:187],v[144:147]\n"
TR "a[240:241],v3 offset:53248\n"
"v_add_f32 v67,v67,v195\n"
MF "v[144:147],a[64:67],v[180:183],v[144:147]\n"
TR "a[242:243],v3 offset:55296\n"
"v_add_f32 v68,v68,v196\n"
MF "v[144:147],a[24:27],v[176:179],v[144:147]\n"
"ds_read2st64_b32 v[202:203],v13 offset0:18 offset1:19\n"
"v_add_f32 v69,v69,v197\n"
MF "v[152:155],a[28:31],v[176:179],v[152:155]\n"
TR "a[244:245],v3 offset:57344\n"
"v_add_f32 v70,v70,v198\n"
MF "v[152:155],a[68:71],v[180:183],v[152:155]\n"
TR "a[246:247],v3 offset:59392\n"
"v_add_f32 v71,v71,v199\n"
MF "v[152:155],a[108:111],v[184:187],v[152:155]\n"
"ds_read2st64_b32 v[204:205],v13 offset0:20 offset1:21\n"
CV "v64,v64,v65\n"
MF "v[160:163],a[112:115],v[184:187],v[160:163]\n"
"ds_read2st64_b32 v[206:207],v13 offset0:22 offset1:23\n"
CV "v65,v66,v67\n"
MF "v[160:163],a[72:75],v[180:183],v[160:163]\n"
"s_add_u32 s87,s90,11264\n"
CV "v66,v68,v69\n"
CV "v67,v70,v71\n"
MF "v[160:163],a[32:35],v[176:179],v[160:163]\n"
MF "v[168:171],a[36:39],v[176:179],v[168:171]\n"
"buffer_store_dwordx4 v[64:67],v9,s[40:43],s87 offen sc1\n"
MF "v[168:171],a[76:79],v[180:183],v[168:171]\n"
"s_waitcnt lgkmcnt(0)\n"
MF "v[168:171],a[116:119],v[184:187],v[168:171]\n"
"s_barrier\n"
MF "v[172:175],a[116:119],a[244:247],v[172:175]\n"
TR "v[176:177],v4\n"
"v_add_f32 v72,v72,v200\n"
MF "v[172:175],a[76:79],a[240:243],v[172:175]\n"
TR "v[178:179],v4 offset:2048\n"
"v_add_f32 v73,v73,v201\n"
MF "v[172:175],a[36:39],v[188:191],v[172:175]\n"
"ds_read2st64_b32 v[192:193],v13 offset0:24 offset1:25\n"
"v_add_f32 v74,v74,v202\n"
MF "v[164:167],a[32:35],v[188:191],v[164:167]\n"
TR "v[180:181],v4 offset:4096\n"
"v_add_f32 v75,v75,v203\n"
MF "v[164:167],a[72:75],a[240:243],v[164:167]\n"
TR "v[182:183],v4 offset:6144\n"
"v_add_f32 v76,v76,v204\n"
MF "v[164:167],a[112:115],a[244:247],v[164:167]\n"
"ds_read2st64_b32 v[194:195],v13 offset0:26 offset1:27\n"
"v_add_f32 v77,v77,v205\n"
MF "v[156:159],a[108:111],a[244:247],v[156:159]\n"
TR "v[184:185],v4 offset:8192\n"
"v_add_f32 v78,v78,v206\n"
MF "v[156:159],a[68:71],a[240:243],v[156:159]\n"
TR "v[186:187],v4 offset:10240\n"
"v_add_f32 v79,v79,v207\n"
MF "v[156:159],a[28:31],v[188:191],v[156:159]\n"
"ds_read2st64_b32 v[196:197],v13 offset0:28 offset1:29\n"
CV "v72,v72,v73\n"
MF "v[148:151],a[24:27],v[188:191],v[148:151]\n"
"ds_read2st64_b32 v[198:199],v13 offset0:30 offset1:31\n"
CV "v73,v74,v75\n"
MF "v[148:151],a[64:67],a[240:243],v[148:151]\n"
"s_add_u32 s87,s90,12288\n"
CV "v74,v76,v77\n"
CV "v75,v78,v79\n"
MF "v[148:151],a[104:107],a[244:247],v[148:151]\n"
MF "v[140:143],a[100:103],a[244:247],v[140:143]\n"
"buffer_store_dwordx4 v[72:75],v9,s[40:43],s87 offen sc1\n"
MF "v[140:143],a[60:63],a[240:243],v[140:143]\n"
MF "v[140:143],a[20:23],v[188:191],v[140:143]\n"
"s_waitcnt lgkmcnt(2)\n"
MF "v[96:99],a[140:143],v[176:179],v[96:99]\n"
"s_waitcnt lgkmcnt(0)\n"
TR "v[188:189],v5\n"
"v_add_f32 v80,v80,v192\n"
MF "v[96:99],a[180:183],v[180:183],v[96:99]\n"
TR "v[190:191],v5 offset:2048\n"
"v_add_f32 v81,v81,v193\n"
MF "v[96:99],a[220:223],v[184:187],v[96:99]\n"
"ds_read2st64_b32 v[200:201],v13 offset0:32 offset1:33\n"
"v_add_f32 v82,v82,v194\n"
MF "v[104:107],a[224:227],v[184:187],v[104:107]\n"
TR "a[240:241],v5 offset:4096\n"
"v_add_f32 v83,v83,v195\n"
MF "v[104:107],a[184:187],v[180:183],v[104:107]\n"
TR "a[242:243],v5 offset:6144\n"
"v_add_f32 v84,v84,v196\n"
MF "v[104:107],a[144:147],v[176:179],v[104:107]\n"
"ds_read2st64_b32 v[202:203],v13 offset0:34 offset1:35\n"
"v_add_f32 v85,v85,v197\n"
MF "v[112:115],a[148:151],v[176:179],v[112:115]\n"
TR "a[244:245],v5 offset:8192\n"
"v_add_f32 v86,v86,v198\n"
MF "v[112:115],a[188:191],v[180:183],v[112:115]\n"
TR "a[246:247],v5 offset:10240\n"
"v_add_f32 v87,v87,v199\n"
MF "v[112:115],a[228:231],v[184:187],v[112:115]\n"
"ds_read2st64_b32 v[204:205],v13 offset0:36 offset1:37\n"
CV "v80,v80,v81\n"
MF "v[120:123],a[232:235],v[184:187],v[120:123]\n"
"ds_read2st64_b32 v[206:207],v13 offset0:38 offset1:39\n"
CV "v81,v82,v83\n"
MF "v[120:123],a[192:195],v[180:183],v[120:123]\n"
"s_add_u32 s87,s90,13312\n"
CV "v82,v84,v85\n"
CV "v83,v86,v87\n"
MF "v[120:123],a[152:155],v[176:179],v[120:123]\n"
MF "v[128:131],a[156:159],v[176:179],v[128:131]\n"
"buffer_store_dwordx4 v[80:83],v9,s[40:43],s87 offen sc1\n"
MF "v[128:131],a[196:199],v[180:183],v[128:131]\n"
MF "v[128:131],a[236:239],v[184:187],v[128:131]\n"
"s_waitcnt lgkmcnt(2)\n"
MF "v[132:135],a[236:239],a[244:247],v[132:135]\n"
"s_waitcnt lgkmcnt(0)\n"
TR "v[176:177],v6\n"
"v_add_f32 v88,v88,v200\n"
MF "v[132:135],a[196:199],a[240:243],v[132:135]\n"
TR "v[178:179],v6 offset:2048\n"
"v_add_f32 v89,v89,v201\n"
MF "v[132:135],a[156:159],v[188:191],v[132:135]\n"
"v_add_f32 v90,v90,v202\n"
"v_add_f32 v91,v91,v203\n"
MF "v[124:127],a[152:155],v[188:191],v[124:127]\n"
TR "v[180:181],v6 offset:4096\n"
"v_add_f32 v92,v92,v204\n"
MF "v[124:127],a[192:195],a[240:243],v[124:127]\n"
TR "v[182:183],v6 offset:6144\n"
"v_add_f32 v93,v93,v205\n"
MF "v[124:127],a[232:235],a[244:247],v[124:127]\n"
"v_add_f32 v94,v94,v206\n"
"v_add_f32 v95,v95,v207\n"
MF "v[116:119],a[228:231],a[244:247],v[116:119]\n"
TR "v[184:185],v6 offset:8192\n"
CV "v88,v88,v89\n"
MF "v[116:119],a[188:191],a[240:243],v[116:119]\n"
TR "v[186:187],v6 offset:10240\n"
CV "v89,v90,v91\n"
MF "v[116:119],a[148:151],v[188:191],v[116:119]\n"
"s_add_u32 s87,s90,14336\n"
CV "v90,v92,v93\n"
CV "v91,v94,v95\n"
MF "v[108:111],a[144:147],v[188:191],v[108:111]\n"
MF "v[108:111],a[184:187],a[240:243],v[108:111]\n"
MF "v[108:111],a[224:227],a[244:247],v[108:111]\n"
MF "v[100:103],a[220:223],a[244:247],v[100:103]\n"
"buffer_store_dwordx4 v[88:91],v9,s[40:43],s87 offen sc1\n"
MF "v[100:103],a[180:183],a[240:243],v[100:103]\n"
"s_mov_b32 s90,s91\n"
"s_add_u32 s91,s91,20480\n"
MF "v[100:103],a[140:143],v[188:191],v[100:103]\n"
"s_waitcnt lgkmcnt(0)\n"
MF "v[136:139],a[140:143],v[176:179],v[136:139]\n"
TR "v[188:189],v7\n"
MF "v[136:139],a[180:183],v[180:183],v[136:139]\n"
TR "v[190:191],v7 offset:2048\n"
MF "v[136:139],a[220:223],v[184:187],v[136:139]\n"
MF "v[144:147],a[224:227],v[184:187],v[144:147]\n"
TR "a[240:241],v7 offset:4096\n"
MF "v[144:147],a[184:187],v[180:183],v[144:147]\n"
TR "a[242:243],v7 offset:6144\n"
MF "v[144:147],a[144:147],v[176:179],v[144:147]\n"
MF "v[152:155],a[148:151],v[176:179],v[152:155]\n"
TR "a[244:245],v7 offset:8192\n"
MF "v[152:155],a[188:191],v[180:183],v[152:155]\n"
TR "a[246:247],v7 offset:10240\n"
MF "v[152:155],a[228:231],v[184:187],v[152:155]\n"
MF "v[160:163],a[232:235],v[184:187],v[160:163]\n"
MF "v[160:163],a[192:195],v[180:183],v[160:163]\n"
MF "v[160:163],a[152:155],v[176:179],v[160:163]\n"
MF "v[168:171],a[156:159],v[176:179],v[168:171]\n"
MF "v[168:171],a[196:199],v[180:183],v[168:171]\n"
"s_waitcnt lgkmcnt(0)\n"
MF "v[168:171],a[236:239],v[184:187],v[168:171]\n"
"s_barrier\n"
MF "v[172:175],a[236:239],a[244:247],v[172:175]\n"
TR "v[176:177],v0 offset:24576\n"
MF "v[172:175],a[196:199],a[240:243],v[172:175]\n"
"s_add_u32 s85,s85,1\n"
TR "v[178:179],v0 offset:26624\n"
MF "v[172:175],a[156:159],v[188:191],v[172:175]\n"
MF "v[164:167],a[152:155],v[188:191],v[164:167]\n"
TR "v[180:181],v0 offset:28672\n"
MF "v[164:167],a[192:195],a[240:243],v[164:167]\n"
TR "v[182:183],v0 offset:30720\n"
MF "v[164:167],a[232:235],a[244:247],v[164:167]\n"
MF "v[156:159],a[228:231],a[244:247],v[156:159]\n"
TR "v[184:185],v0 offset:32768\n"
MF "v[156:159],a[188:191],a[240:243],v[156:159]\n"
TR "v[186:187],v0 offset:34816\n"
MF "v[156:159],a[148:151],v[188:191],v[156:159]\n"
MF "v[148:151],a[144:147],v[188:191],v[148:151]\n"
MF "v[148:151],a[184:187],a[240:243],v[148:151]\n"
MF "v[148:151],a[224:227],a[244:247],v[148:151]\n"
MF "v[140:143],a[220:223],a[244:247],v[140:143]\n"
MF "v[140:143],a[180:183],a[240:243],v[140:143]\n"
MF "v[140:143],a[140:143],v[188:191],v[140:143]\n"
"s_waitcnt lgkmcnt(0)\n"
MF "v[16:19],a[0:3],v[176:179],v[252:255]\n"
TR "v[188:189],v1 offset:24576\n"
MF "v[16:19],a[40:43],v[180:183],v[16:19]\n"
TR "v[190:191],v1 offset:26624\n"
MF "v[16:19],a[80:83],v[184:187],v[16:19]\n"
MF "v[24:27],a[84:87],v[184:187],v[252:255]\n"
TR "a[240:241],v1 offset:28672\n"
MF "v[24:27],a[44:47],v[180:183],v[24:27]\n"
TR "a[242:243],v1 offset:30720\n"
MF "v[24:27],a[4:7],v[176:179],v[24:27]\n"
MF "v[32:35],a[8:11],v[176:179],v[252:255]\n"
TR "a[244:245],v1 offset:32768\n"
MF "v[32:35],a[48:51],v[180:183],v[32:35]\n"
TR "a[246:247],v1 offset:34816\n"
MF "v[32:35],a[88:91],v[184:187],v[32:35]\n"
MF "v[40:43],a[92:95],v[184:187],v[252:255]\n"
MF "v[40:43],a[52:55],v[180:183],v[40:43]\n"
MF "v[40:43],a[12:15],v[176:179],v[40:43]\n"
MF "v[48:51],a[16:19],v[176:179],v[252:255]\n"
MF "v[48:51],a[56:59],v[180:183],v[48:51]\n"
MF "v[48:51],a[96:99],v[184:187],v[48:51]\n"
"s_waitcnt lgkmcnt(0)\n"
MF "v[52:55],a[96:99],a[244:247],v[252:255]\n"
TR "v[176:177],v2 offset:24576\n"
MF "v[52:55],a[56:59],a[240:243],v[52:55]\n"
TR "v[178:179],v2 offset:26624\n"
MF "v[52:55],a[16:19],v[188:191],v[52:55]\n"
MF "v[44:47],a[12:15],v[188:191],v[252:255]\n"
TR "v[180:181],v2 offset:28672\n"
MF "v[44:47],a[52:55],a[240:243],v[44:47]\n"
TR "v[182:183],v2 offset:30720\n"
MF "v[44:47],a[92:95],a[244:247],v[44:47]\n"
MF "v[36:39],a[88:91],a[244:247],v[252:255]\n"
TR "v[184:185],v2 offset:32768\n"
MF "v[36:39],a[48:51],a[240:243],v[36:39]\n"
TR "v[186:187],v2 offset:34816\n"
MF "v[36:39],a[8:11],v[188:191],v[36:39]\n"
MF "v[28:31],a[4:7],v[188:191],v[252:255]\n"
MF "v[28:31],a[44:47],a[240:243],v[28:31]\n"
MF "v[28:31],a[84:87],a[244:247],v[28:31]\n"
MF "v[20:23],a[80:83],a[244:247],v[252:255]\n"
MF "v[20:23],a[40:43],a[240:243],v[20:23]\n"
MF "v[20:23],a[0:3],v[188:191],v[20:23]\n"
"s_waitcnt lgkmcnt(0)\n"
MF "v[56:59],a[0:3],v[176:179],v[252:255]\n"
TR "v[188:189],v3 offset:24576\n"
MF "v[56:59],a[40:43],v[180:183],v[56:59]\n"
TR "v[190:191],v3 offset:26624\n"
MF "v[56:59],a[80:83],v[184:187],v[56:59]\n"
MF "v[64:67],a[84:87],v[184:187],v[252:255]\n"
TR "a[240:241],v3 offset:28672\n"
MF "v[64:67],a[44:47],v[180:183],v[64:67]\n"
TR "a[242:243],v3 offset:30720\n"
MF "v[64:67],a[4:7],v[176:179],v[64:67]\n"
MF "v[72:75],a[8:11],v[176:179],v[252:255]\n"
TR "a[244:245],v3 offset:32768\n"
MF "v[72:75],a[48:51],v[180:183],v[72:75]\n"
TR "a[246:247],v3 offset:34816\n"
MF "v[72:75],a[88:91],v[184:187],v[72:75]\n"
MF "v[80:83],a[92:95],v[184:187],v[252:255]\n"
MF "v[80:83],a[52:55],v[180:183],v[80:83]\n"
MF "v[80:83],a[12:15],v[176:179],v[80:83]\n"
MF "v[88:91],a[16:19],v[176:179],v[252:255]\n"
MF "v[88:91],a[56:59],v[180:183],v[88:91]\n"
"s_waitcnt lgkmcnt(0)\n"
MF "v[88:91],a[96:99],v[184:187],v[88:91]\n"
"s_barrier\n"
MF "v[92:95],a[96:99],a[244:247],v[252:255]\n"
TR "v[176:177],v0\n"
MF "v[92:95],a[56:59],a[240:243],v[92:95]\n"
TR "v[178:179],v0 offset:2048\n"
MF "v[92:95],a[16:19],v[188:191],v[92:95]\n"
"ds_read2st64_b32 v[192:193],v13 offset0:192 offset1:193\n"
MF "v[84:87],a[12:15],v[188:191],v[252:255]\n"
TR "v[180:181],v0 offset:4096\n"
MF "v[84:87],a[52:55],a[240:243],v[84:87]\n"
TR "v[182:183],v0 offset:6144\n"
MF "v[84:87],a[92:95],a[244:247],v[84:87]\n"
"ds_read2st64_b32 v[194:195],v13 offset0:194 offset1:195\n"
MF "v[76:79],a[88:91],a[244:247],v[252:255]\n"
TR "v[184:185],v0 offset:8192\n"
MF "v[76:79],a[48:51],a[240:243],v[76:79]\n"
TR "v[186:187],v0 offset:10240\n"
MF "v[76:79],a[8:11],v[188:191],v[76:79]\n"
"ds_read2st64_b32 v[196:197],v13 offset0:196 offset1:197\n"
MF "v[68:71],a[4:7],v[188:191],v[252:255]\n"
"ds_read2st64_b32 v[198:199],v13 offset0:198 offset1:199\n"
MF "v[68:71],a[44:47],a[240:243],v[68:71]\n"
MF "v[68:71],a[84:87],a[244:247],v[68:71]\n"
MF "v[60:63],a[80:83],a[244:247],v[252:255]\n"
MF "v[60:63],a[40:43],a[240:243],v[60:63]\n"
MF "v[60:63],a[0:3],v[188:191],v[60:63]\n"
"s_waitcnt lgkmcnt(2)\n"
MF "v[16:19],a[120:123],v[176:179],v[16:19]\n"
"s_waitcnt lgkmcnt(0)\n"
TR "v[188:189],v1\n"
"v_add_f32 v96,v96,v192\n"
MF "v[16:19],a[160:163],v[180:183],v[16:19]\n"
TR "v[190:191],v1 offset:2048\n"
"v_add_f32 v97,v97,v193\n"
MF "v[16:19],a[200:203],v[184:187],v[16:19]\n"
"ds_read2st64_b32 v[200:201],v13 offset0:200 offset1:201\n"
"v_add_f32 v98,v98,v194\n"
"s_waitcnt vmcnt(0)\n"
"v_mov_b32 v14,s76\n"
"s_add_u32 s76,s76,1\n"
MF "v[24:27],a[204:207],v[184:187],v[24:27]\n"
TR "a[240:241],v1 offset:4096\n"
"v_add_f32 v99,v99,v195\n"
"buffer_store_dword v14,v15,s[40:43],s94 offen sc1\n"
MF "v[24:27],a[164:167],v[180:183],v[24:27]\n"
TR "a[242:243],v1 offset:6144\n"
"v_add_f32 v100,v100,v196\n"
MF "v[24:27],a[124:127],v[176:179],v[24:27]\n"
"ds_read2st64_b32 v[202:203],v13 offset0:202 offset1:203\n"
"v_add_f32 v101,v101,v197\n"
MF "v[32:35],a[128:131],v[176:179],v[32:35]\n"
TR "a[244:245],v1 offset:8192\n"
"v_add_f32 v102,v102,v198\n"
MF "v[32:35],a[168:171],v[180:183],v[32:35]\n"
TR "a[246:247],v1 offset:10240\n"
"v_add_f32 v103,v103,v199\n"
MF "v[32:35],a[208:211],v[184:187],v[32:35]\n"
"ds_read2st64_b32 v[204:205],v13 offset0:204 offset1:205\n"
CV "v96,v96,v97\n"
MF "v[40:43],a[212:215],v[184:187],v[40:43]\n"
"ds_read2st64_b32 v[206:207],v13 offset0:206 offset1:207\n"
CV "v97,v98,v99\n"
MF "v[40:43],a[172:175],v[180:183],v[40:43]\n"
"s_add_u32 s87,s90,0\n"
CV "v98,v100,v101\n"
CV "v99,v102,v103\n"
MF "v[40:43],a[132:135],v[176:179],v[40:43]\n"
MF "v[48:51],a[136:139],v[176:179],v[48:51]\n"
"buffer_store_dwordx4 v[96:99],v9,s[40:43],s87 offen sc1\n"
MF "v[48:51],a[176:179],v[180:183],v[48:51]\n"
MF "v[48:51],a[216:219],v[184:187],v[48:51]\n"
"s_waitcnt lgkmcnt(2)\n"
MF "v[52:55],a[216:219],a[244:247],v[52:55]\n"
"s_waitcnt lgkmcnt(0)\n"
TR "v[176:177],v2\n"
"v_add_f32 v104,v104,v200\n"
MF "v[52:55],a[176:179],a[240:243],v[52:55]\n"
TR "v[178:179],v2 offset:2048\n"
"v_add_f32 v105,v105,v201\n"
MF "v[52:55],a[136:139],v[188:191],v[52:55]\n"
"ds_read2st64_b32 v[192:193],v13 offset0:208 offset1:209\n"
"v_add_f32 v106,v106,v202\n"
MF "v[44:47],a[132:135],v[188:191],v[44:47]\n"
TR "v[180:181],v2 offset:4096\n"
"v_add_f32 v107,v107,v203\n"
MF "v[44:47],a[172:175],a[240:243],v[44:47]\n"
TR "v[182:183],v2 offset:6144\n"
"v_add_f32 v108,v108,v204\n"
MF "v[44:47],a[212:215],a[244:247],v[44:47]\n"
"ds_read2st64_b32 v[194:195],v13 offset0:210 offset1:211\n"
"v_add_f32 v109,v109,v205\n"
MF "v[36:39],a[208:211],a[244:247],v[36:39]\n"
TR "v[184:185],v2 offset:8192\n"
"v_add_f32 v110,v110,v206\n"
MF "v[36:39],a[168:171],a[240:243],v[36:39]\n"
TR "v[186:187],v2 offset:10240\n"
"v_add_f32 v111,v111,v207\n"
MF "v[36:39],a[128:131],v[188:191],v[36:39]\n"
"ds_read2st64_b32 v[196:197],v13 offset0:212 offset1:213\n"
CV "v104,v104,v105\n"
MF "v[28:31],a[124:127],v[188:191],v[28:31]\n"
"ds_read2st64_b32 v[198:199],v13 offset0:214 offset1:215\n"
CV "v105,v106,v107\n"
MF "v[28:31],a[164:167],a[240:243],v[28:31]\n"
"s_add_u32 s87,s90,1024\n"
CV "v106,v108,v109\n"
CV "v107,v110,v111\n"
MF "v[28:31],a[204:207],a[244:247],v[28:31]\n"
MF "v[20:23],a[200:203],a[244:247],v[20:23]\n"
"buffer_store_dwordx4 v[104:107],v9,s[40:43],s87 offen sc1\n"
MF "v[20:23],a[160:163],a[240:243],v[20:23]\n"
MF "v[20:23],a[120:123],v[188:191],v[20:23]\n"
"s_waitcnt lgkmcnt(2)\n"
MF "v[56:59],a[120:123],v[176:179],v[56:59]\n"
"s_waitcnt lgkmcnt(0)\n"
TR "v[188:189],v3\n"
"v_add_f32 v112,v112,v192\n"
MF "v[56:59],a[160:163],v[180:183],v[56:59]\n"
TR "v[190:191],v3 offset:2048\n"
"v_add_f32 v113,v113,v193\n"
MF "v[56:59],a[200:203],v[184:187],v[56:59]\n"
"ds_read2st64_b32 v[200:201],v13 offset0:216 offset1:217\n"
"v_add_f32 v114,v114,v194\n"
MF "v[64:67],a[204:207],v[184:187],v[64:67]\n"
TR "a[240:241],v3 offset:4096\n"
"v_add_f32 v115,v115,v195\n"
MF "v[64:67],a[164:167],v[180:183],v[64:67]\n"
TR "a[242:243],v3 offset:6144\n"
"v_add_f32 v116,v116,v196\n"
MF "v[64:67],a[124:127],v[176:179],v[64:67]\n"
"ds_read2st64_b32 v[202:203],v13 offset0:218 offset1:219\n"
"v_add_f32 v117,v117,v197\n"
MF "v[72:75],a[128:131],v[176:179],v[72:75]\n"
TR "a[244:245],v3 offset:8192\n"
"v_add_f32 v118,v118,v198\n"
MF "v[72:75],a[168:171],v[180:183],v[72:75]\n"
TR "a[246:247],v3 offset:10240\n"
"v_add_f32 v119,v119,v199\n"
MF "v[72:75],a[208:211],v[184:187],v[72:75]\n"
".Lpg34h:\n"
"s_cbranch_execz .Lpg38h\n"
"ds_read2st64_b32 v[204:205],v13 offset0:220 offset1:221\n"
CV "v112,v112,v113\n"
MF "v[80:83],a[212:215],v[184:187],v[80:83]\n"
"ds_read2st64_b32 v[206:207],v13 offset0:222 offset1:223\n"
CV "v113,v114,v115\n"
MF "v[80:83],a[172:175],v[180:183],v[80:83]\n"
"s_add_u32 s87,s90,2048\n"
CV "v114,v116,v117\n"
CV "v115,v118,v119\n"
MF "v[80:83],a[132:135],v[176:179],v[80:83]\n"
MF "v[88:91],a[136:139],v[176:179],v[88:91]\n"
"buffer_store_dwordx4 v[112:115],v9,s[40:43],s87 offen sc1\n"
MF "v[88:91],a[176:179],v[180:183],v[88:91]\n"
"s_waitcnt lgkmcnt(0)\n"
MF "v[88:91],a[216:219],v[184:187],v[88:91]\n"
"s_barrier\n"
MF "v[92:95],a[216:219],a[244:247],v[92:95]\n"
TR "v[176:177],v0 offset:49152\n"
"v_add_f32 v120,v120,v200\n"
MF "v[92:95],a[176:179],a[240:243],v[92:95]\n"
TR "v[178:179],v0 offset:51200\n"
"v_add_f32 v121,v121,v201\n"
MF "v[92:95],a[136:139],v[188:191],v[92:95]\n"
"ds_read2st64_b32 v[192:193],v13 offset0:224 offset1:225\n"
"v_add_f32 v122,v122,v202\n"
MF "v[84:87],a[132:135],v[188:191],v[84:87]\n"
TR "v[180:181],v0 offset:53248\n"
"v_add_f32 v123,v123,v203\n"
MF "v[84:87],a[172:175],a[240:243],v[84:87]\n"
TR "v[182:183],v0 offset:55296\n"
"v_add_f32 v124,v124,v204\n"
MF "v[84:87],a[212:215],a[244:247],v[84:87]\n"
"ds_read2st64_b32 v[194:195],v13 offset0:226 offset1:227\n"
"v_add_f32 v125,v125,v205\n"
MF "v[76:79],a[208:211],a[244:247],v[76:79]\n"
TR "v[184:185],v0 offset:57344\n"
"v_add_f32 v126,v126,v206\n"
MF "v[76:79],a[168:171],a[240:243],v[76:79]\n"
TR "v[186:187],v0 offset:59392\n"
"v_add_f32 v127,v127,v207\n"
MF "v[76:79],a[128:131],v[188:191],v[76:79]\n"
"ds_read2st64_b32 v[196:197],v13 offset0:228 offset1:229\n"
CV "v120,v120,v121\n"
MF "v[68:71],a[124:127],v[188:191],v[68:71]\n"
"ds_read2st64_b32 v[198:199],v13 offset0:230 offset1:231\n"
CV "v121,v122,v123\n"
MF "v[68:71],a[164:167],a[240:243],v[68:71]\n"
"s_add_u32 s87,s90,3072\n"
CV "v122,v124,v125\n"
CV "v123,v126,v127\n"
MF "v[68:71],a[204:207],a[244:247],v[68:71]\n"
MF "v[60:63],a[200:203],a[244:247],v[60:63]\n"
"buffer_store_dwordx4 v[120:123],v9,s[40:43],s87 offen sc1\n"
MF "v[60:63],a[160:163],a[240:243],v[60:63]\n"
MF "v[60:63],a[120:123],v[188:191],v[60:63]\n"
"s_waitcnt lgkmcnt(2)\n"
MF "v[16:19],a[20:23],v[176:179],v[16:19]\n"
"s_waitcnt lgkmcnt(0)\n"
TR "v[188:189],v1 offset:49152\n"
"v_add_f32 v128,v128,v192\n"
MF "v[16:19],a[60:63],v[180:183],v[16:19]\n"
TR "v[190:191],v1 offset:51200\n"
"v_add_f32 v129,v129,v193\n"
MF "v[16:19],a[100:103],v[184:187],v[16:19]\n"
"ds_read2st64_b32 v[200:201],v13 offset0:0 offset1:1\n"
"v_add_f32 v130,v130,v194\n"
MF "v[24:27],a[104:107],v[184:187],v[24:27]\n"
TR "a[240:241],v1 offset:53248\n"
"v_add_f32 v131,v131,v195\n"
MF "v[24:27],a[64:67],v[180:183],v[24:27]\n"
TR "a[242:243],v1 offset:55296\n"
"v_add_f32 v132,v132,v196\n"
MF "v[24:27],a[24:27],v[176:179],v[24:27]\n"
"ds_read2st64_b32 v[202:203],v13 offset0:2 offset1:3\n"
"v_add_f32 v133,v133,v197\n"
MF "v[32:35],a[28:31],v[176:179],v[32:35]\n"
TR "a[244:245],v1 offset:57344\n"
"v_add_f32 v134,v134,v198\n"
MF "v[32:35],a[68:71],v[180:183],v[32:35]\n"
TR "a[246:247],v1 offset:59392\n"
"v_add_f32 v135,v135,v199\n"
MF "v[32:35],a[108:111],v[184:187],v[32:35]\n"
"ds_read2st64_b32 v[204:205],v13 offset0:4 offset1:5\n"
CV "v128,v128,v129\n"
MF "v[40:43],a[112:115],v[184:187],v[40:43]\n"
"ds_read2st64_b32 v[206:207],v13 offset0:6 offset1:7\n"
CV "v129,v130,v131\n"
MF "v[40:43],a[72:75],v[180:183],v[40:43]\n"
"s_add_u32 s87,s90,4096\n"
CV "v130,v132,v133\n"
CV "v131,v134,v135\n"
MF "v[40:43],a[32:35],v[176:179],v[40:43]\n"
MF "v[48:51],a[36:39],v[176:179],v[48:51]\n"
"buffer_store_dwordx4 v[128:131],v9,s[40:43],s87 offen sc1\n"
MF "v[48:51],a[76:79],v[180:183],v[48:51]\n"
MF "v[48:51],a[116:119],v[184:187],v[48:51]\n"
"s_waitcnt lgkmcnt(2)\n"
MF "v[52:55],a[116:119],a[244:247],v[52:55]\n"
"s_waitcnt lgkmcnt(0)\n"
TR "v[176:177],v2 offset:49152\n"
"v_add_f32 v136,v136,v200\n"
MF "v[52:55],a[76:79],a[240:243],v[52:55]\n"
TR "v[178:179],v2 offset:51200\n"
"v_add_f32 v137,v137,v201\n"
MF "v[52:55],a[36:39],v[188:191],v[52:55]\n"
"ds_read2st64_b32 v[192:193],v13 offset0:8 offset1:9\n"
"v_add_f32 v138,v138,v202\n"
MF "v[44:47],a[32:35],v[188:191],v[44:47]\n"
TR "v[180:181],v2 offset:53248\n"
"v_add_f32 v139,v139,v203\n"
MF "v[44:47],a[72:75],a[240:243],v[44:47]\n"
TR "v[182:183],v2 offset:55296\n"
"v_add_f32 v140,v140,v204\n"
MF "v[44:47],a[112:115],a[244:247],v[44:47]\n"
"ds_read2st64_b32 v[194:195],v13 offset0:10 offset1:11\n"
"v_add_f32 v141,v141,v205\n"
MF "v[36:39],a[108:111],a[244:247],v[36:39]\n"
TR "v[184:185],v2 offset:57344\n"
"v_add_f32 v142,v142,v206\n"
MF "v[36:39],a[68:71],a[240:243],v[36:39]\n"
TR "v[186:187],v2 offset:59392\n"
"v_add_f32 v143,v143,v207\n"
MF "v[36:39],a[28:31],v[188:191],v[36:39]\n"
"ds_read2st64_b32 v[196:197],v13 offset0:12 offset1:13\n"
CV "v136,v136,v137\n"
MF "v[28:31],a[24:27],v[188:191],v[28:31]\n"
"ds_read2st64_b32 v[198:199],v13 offset0:14 offset1:15\n"
CV "v137,v138,v139\n"
MF "v[28:31],a[64:67],a[240:243],v[28:31]\n"
"s_add_u32 s87,s90,10240\n"
CV "v138,v140,v141\n"
CV "v139,v142,v143\n"
MF "v[28:31],a[104:107],a[244:247],v[28:31]\n"
MF "v[20:23],a[100:103],a[244:247],v[20:23]\n"
"buffer_store_dwordx4 v[136:139],v9,s[40:43],s87 offen sc1\n"
MF "v[20:23],a[60:63],a[240:243],v[20:23]\n"
MF "v[20:23],a[20:23],v[188:191],v[20:23]\n"
"s_waitcnt lgkmcnt(2)\n"
MF "v[56:59],a[20:23],v[176:179],v[56:59]\n"
"s_waitcnt lgkmcnt(0)\n"
TR "v[188:189],v3 offset:49152\n"
"v_add_f32 v144,v144,v192\n"
MF "v[56:59],a[60:63],v[180:183],v[56:59]\n"
TR "v[190:191],v3 offset:51200\n"
"v_add_f32 v145,v145,v193\n"
MF "v[56:59],a[100:103],v[184:187],v[56:59]\n"
"ds_read2st64_b32 v[200:201],v13 offset0:16 offset1:17\n"
"v_add_f32 v146,v146,v194\n"
MF "v[64:67],a[104:107],v[184:187],v[64:67]\n"
TR "a[240:241],v3 offset:53248\n"
"v_add_f32 v147,v147,v195\n"
MF "v[64:67],a[64:67],v[180:183],v[64:67]\n"
TR "a[242:243],v3 offset:55296\n"
"v_add_f32 v148,v148,v196\n"
MF "v[64:67],a[24:27],v[176:179],v[64:67]\n"
"ds_read2st64_b32 v[202:203],v13 offset0:18 offset1:19\n"
"v_add_f32 v149,v149,v197\n"
MF "v[72:75],a[28:31],v[176:179],v[72:75]\n"
TR "a[244:245],v3 offset:57344\n"
"v_add_f32 v150,v150,v198\n"
MF "v[72:75],a[68:71],v[180:183],v[72:75]\n"
TR "a[246:247],v3 offset:59392\n"
"v_add_f32 v151,v151,v199\n"
MF "v[72:75],a[108:111],v[184:187],v[72:75]\n"
"ds_read2st64_b32 v[204:205],v13 offset0:20 offset1:21\n"
CV "v144,v144,v145\n"
MF "v[80:83],a[112:115],v[184:187],v[80:83]\n"
"ds_read2st64_b32 v[206:207],v13 offset0:22 offset1:23\n"
CV "v145,v146,v147\n"
MF "v[80:83],a[72:75],v[180:183],v[80:83]\n"
"s_add_u32 s87,s90,11264\n"
CV "v146,v148,v149\n"
CV "v147,v150,v151\n"
MF "v[80:83],a[32:35],v[176:179],v[80:83]\n"
MF "v[88:91],a[36:39],v[176:179],v[88:91]\n"
"buffer_store_dwordx4 v[144:147],v9,s[40:43],s87 offen sc1\n"
MF "v[88:91],a[76:79],v[180:183],v[88:91]\n"
"s_waitcnt lgkmcnt(0)\n"
MF "v[88:91],a[116:119],v[184:187],v[88:91]\n"
"s_barrier\n"
MF "v[92:95],a[116:119],a[244:247],v[92:95]\n"
TR "v[176:177],v4\n"
"v_add_f32 v152,v152,v200\n"
MF "v[92:95],a[76:79],a[240:243],v[92:95]\n"
TR "v[178:179],v4 offset:2048\n"
"v_add_f32 v153,v153,v201\n"
MF "v[92:95],a[36:39],v[188:191],v[92:95]\n"
"ds_read2st64_b32 v[192:193],v13 offset0:24 offset1:25\n"
"v_add_f32 v154,v154,v202\n"
MF "v[84:87],a[32:35],v[188:191],v[84:87]\n"
TR "v[180:181],v4 offset:4096\n"
"v_add_f32 v155,v155,v203\n"
MF "v[84:87],a[72:75],a[240:243],v[84:87]\n"
TR "v[182:183],v4 offset:6144\n"
"v_add_f32 v156,v156,v204\n"
MF "v[84:87],a[112:115],a[244:247],v[84:87]\n"
"ds_read2st64_b32 v[194:195],v13 offset0:26 offset1:27\n"
"v_add_f32 v157,v157,v205\n"
MF "v[76:79],a[108:111],a[244:247],v[76:79]\n"
TR "v[184:185],v4 offset:8192\n"
"v_add_f32 v158,v158,v206\n"
MF "v[76:79],a[68:71],a[240:243],v[76:79]\n"
TR "v[186:187],v4 offset:10240\n"
"v_add_f32 v159,v159,v207\n"
MF "v[76:79],a[28:31],v[188:191],v[76:79]\n"
"ds_read2st64_b32 v[196:197],v13 offset0:28 offset1:29\n"
CV "v152,v152,v153\n"
MF "v[68:71],a[24:27],v[188:191],v[68:71]\n"
"ds_read2st64_b32 v[198:199],v13 offset0:30 offset1:31\n"
CV "v153,v154,v155\n"
MF "v[68:71],a[64:67],a[240:243],v[68:71]\n"
"s_add_u32 s87,s90,12288\n"
CV "v154,v156,v157\n"
CV "v155,v158,v159\n"
MF "v[68:71],a[104:107],a[244:247],v[68:71]\n"
MF "v[60:63],a[100:103],a[244:247],v[60:63]\n"
"buffer_store_dwordx4 v[152:155],v9,s[40:43],s87 offen sc1\n"
MF "v[60:63],a[60:63],a[240:243],v[60:63]\n"
MF "v[60:63],a[20:23],v[188:191],v[60:63]\n"
"s_waitcnt lgkmcnt(2)\n"
MF "v[16:19],a[140:143],v[176:179],v[16:19]\n"
"s_waitcnt lgkmcnt(0)\n"
TR "v[188:189],v5\n"
"v_add_f32 v160,v160,v192\n"
MF "v[16:19],a[180:183],v[180:183],v[16:19]\n"
TR "v[190:191],v5 offset:2048\n"
"v_add_f32 v161,v161,v193\n"
MF "v[16:19],a[220:223],v[184:187],v[16:19]\n"
"ds_read2st64_b32 v[200:201],v13 offset0:32 offset1:33\n"
"v_add_f32 v162,v162,v194\n"
MF "v[24:27],a[224:227],v[184:187],v[24:27]\n"
TR "a[240:241],v5 offset:4096\n"
"v_add_f32 v163,v163,v195\n"
MF "v[24:27],a[184:187],v[180:183],v[24:27]\n"
TR "a[242:243],v5 offset:6144\n"
"v_add_f32 v164,v164,v196\n"
MF "v[24:27],a[144:147],v[176:179],v[24:27]\n"
"ds_read2st64_b32 v[202:203],v13 offset0:34 offset1:35\n"
"v_add_f32 v165,v165,v197\n"
MF "v[32:35],a[148:151],v[176:179],v[32:35]\n"
TR "a[244:245],v5 offset:8192\n"
"v_add_f32 v166,v166,v198\n"
MF "v[32:35],a[188:191],v[180:183],v[32:35]\n"
TR "a[246:247],v5 offset:10240\n"
"v_add_f32 v167,v167,v199\n"
MF "v[32:35],a[228:231],v[184:187],v[32:35]\n"
"ds_read2st64_b32 v[204:205],v13 offset0:36 offset1:37\n"
CV "v160,v160,v161\n"
MF "v[40:43],a[232:235],v[184:187],v[40:43]\n"
"ds_read2st64_b32 v[206:207],v13 offset0:38 offset1:39\n"
CV "v161,v162,v163\n"
MF "v[40:43],a[192:195],v[180:183],v[40:43]\n"
"s_add_u32 s87,s90,13312\n"
CV "v162,v164,v165\n"
CV "v163,v166,v167\n"
MF "v[40:43],a[152:155],v[176:179],v[40:43]\n"
MF "v[48:51],a[156:159],v[176:179],v[48:51]\n"
"buffer_store_dwordx4 v[160:163],v9,s[40:43],s87 offen sc1\n"
MF "v[48:51],a[196:199],v[180:183],v[48:51]\n"
MF "v[48:51],a[236:239],v[184:187],v[48:51]\n"
"s_waitcnt lgkmcnt(2)\n"
MF "v[52:55],a[236:239],a[244:247],v[52:55]\n"
"s_waitcnt lgkmcnt(0)\n"
TR "v[176:177],v6\n"
"v_add_f32 v168,v168,v200\n"
MF "v[52:55],a[196:199],a[240:243],v[52:55]\n"
TR "v[178:179],v6 offset:2048\n"
"v_add_f32 v169,v169,v201\n"
MF "v[52:55],a[156:159],v[188:191],v[52:55]\n"
"v_add_f32 v170,v170,v202\n"
"v_add_f32 v171,v171,v203\n"
MF "v[44:47],a[152:155],v[188:191],v[44:47]\n"
TR "v[180:181],v6 offset:4096\n"
"v_add_f32 v172,v172,v204\n"
MF "v[44:47],a[192:195],a[240:243],v[44:47]\n"
TR "v[182:183],v6 offset:6144\n"
"v_add_f32 v173,v173,v205\n"
MF "v[44:47],a[232:235],a[244:247],v[44:47]\n"
"v_add_f32 v174,v174,v206\n"
"v_add_f32 v175,v175,v207\n"
MF "v[36:39],a[228:231],a[244:247],v[36:39]\n"
TR "v[184:185],v6 offset:8192\n"
CV "v168,v168,v169\n"
MF "v[36:39],a[188:191],a[240:243],v[36:39]\n"
TR "v[186:187],v6 offset:10240\n"
CV "v169,v170,v171\n"
MF "v[36:39],a[148:151],v[188:191],v[36:39]\n"
"s_add_u32 s87,s90,14336\n"
CV "v170,v172,v173\n"
CV "v171,v174,v175\n"
MF "v[28:31],a[144:147],v[188:191],v[28:31]\n"
MF "v[28:31],a[184:187],a[240:243],v[28:31]\n"
MF "v[28:31],a[224:227],a[244:247],v[28:31]\n"
MF "v[20:23],a[220:223],a[244:247],v[20:23]\n"
"buffer_store_dwordx4 v[168:171],v9,s[40:43],s87 offen sc1\n"
MF "v[20:23],a[180:183],a[240:243],v[20:23]\n"
"s_mov_b32 s90,s91\n"
"s_add_u32 s91,s91,20480\n"
MF "v[20:23],a[140:143],v[188:191],v[20:23]\n"
"s_waitcnt lgkmcnt(0)\n"
MF "v[56:59],a[140:143],v[176:179],v[56:59]\n"
TR "v[188:189],v7\n"
MF "v[56:59],a[180:183],v[180:183],v[56:59]\n"
TR "v[190:191],v7 offset:2048\n"
MF "v[56:59],a[220:223],v[184:187],v[56:59]\n"
"v_mov_b32 v244,0\n"
"v_mov_b32 v245,0\n"
MF "v[64:67],a[224:227],v[184:187],v[64:67]\n"
TR "a[240:241],v7 offset:4096\n"
"v_mov_b32 v246,0\n"
MF "v[64:67],a[184:187],v[180:183],v[64:67]\n"
TR "a[242:243],v7 offset:6144\n"
"v_mov_b32 v247,0\n"
MF "v[64:67],a[144:147],v[176:179],v[64:67]\n"
"v_mov_b32 v248,0\n"
"v_mov_b32 v249,0\n"
MF "v[72:75],a[148:151],v[176:179],v[72:75]\n"
TR "a[244:245],v7 offset:8192\n"
"v_mov_b32 v250,0\n"
MF "v[72:75],a[188:191],v[180:183],v[72:75]\n"
TR "a[246:247],v7 offset:10240\n"
"v_mov_b32 v251,0\n"
MF "v[72:75],a[228:231],v[184:187],v[72:75]\n"
MF "v[80:83],a[232:235],v[184:187],v[80:83]\n"
MF "v[80:83],a[192:195],v[180:183],v[80:83]\n"
MF "v[80:83],a[152:155],v[176:179],v[80:83]\n"
MF "v[88:91],a[156:159],v[176:179],v[88:91]\n"
MF "v[88:91],a[196:199],v[180:183],v[88:91]\n"
"s_waitcnt lgkmcnt(0)\n"
MF "v[88:91],a[236:239],v[184:187],v[88:91]\n"
"s_barrier\n"
MF "v[92:95],a[236:239],a[244:247],v[92:95]\n"
TR "v[176:177],v0 offset:24576\n"
MF "v[92:95],a[196:199],a[240:243],v[92:95]\n"
"s_add_u32 s85,s85,1\n"
TR "v[178:179],v0 offset:26624\n"
MF "v[92:95],a[156:159],v[188:191],v[92:95]\n"
MF "v[84:87],a[152:155],v[188:191],v[84:87]\n"
TR "v[180:181],v0 offset:28672\n"
MF "v[84:87],a[192:195],a[240:243],v[84:87]\n"
TR "v[182:183],v0 offset:30720\n"
MF "v[84:87],a[232:235],a[244:247],v[84:87]\n"
MF "v[76:79],a[228:231],a[244:247],v[76:79]\n"
TR "v[184:185],v0 offset:32768\n"
MF "v[76:79],a[188:191],a[240:243],v[76:79]\n"
TR "v[186:187],v0 offset:34816\n"
MF "v[76:79],a[148:151],v[188:191],v[76:79]\n"
MF "v[68:71],a[144:147],v[188:191],v[68:71]\n"
MF "v[68:71],a[184:187],a[240:243],v[68:71]\n"
MF "v[68:71],a[224:227],a[244:247],v[68:71]\n"
MF "v[60:63],a[220:223],a[244:247],v[60:63]\n"
MF "v[60:63],a[180:183],a[240:243],v[60:63]\n"
MF "v[60:63],a[140:143],v[188:191],v[60:63]\n"
"s_waitcnt lgkmcnt(0)\n"
"s_cmp_lg_u32 s85,5\n"
"s_cbranch_scc1 .Lpl15\n"
".Lpl19:\n"
MF "v[96:99],a[0:3],v[176:179],v[252:255]\n"
TR "v[188:189],v1 offset:24576\n"
MF "v[96:99],a[40:43],v[180:183],v[96:99]\n"
"s_add_u32 s82,s82,4096\n"
TR "v[190:191],v1 offset:26624\n"
MF "v[96:99],a[80:83],v[184:187],v[96:99]\n"
LD "v[224:227],v9,s[40:43],s82 offen sc1\n"
MF "v[104:107],a[84:87],v[184:187],v[252:255]\n"
TR "a[240:241],v1 offset:28672\n"
MF "v[104:107],a[44:47],v[180:183],v[104:107]\n"
TR "a[242:243],v1 offset:30720\n"
MF "v[104:107],a[4:7],v[176:179],v[104:107]\n"
MF "v[112:115],a[8:11],v[176:179],v[252:255]\n"
TR "a[244:245],v1 offset:32768\n"
MF "v[112:115],a[48:51],v[180:183],v[112:115]\n"
TR "a[246:247],v1 offset:34816\n"
MF "v[112:115],a[88:91],v[184:187],v[112:115]\n"
MF "v[120:123],a[92:95],v[184:187],v[252:255]\n"
MF "v[120:123],a[52:55],v[180:183],v[120:123]\n"
MF "v[120:123],a[12:15],v[176:179],v[120:123]\n"
MF "v[128:131],a[16:19],v[176:179],v[252:255]\n"
MF "v[128:131],a[56:59],v[180:183],v[128:131]\n"
MF "v[128:131],a[96:99],v[184:187],v[128:131]\n"
"s_waitcnt lgkmcnt(0)\n"
MF "v[132:135],a[96:99],a[244:247],v[252:255]\n"
TR "v[176:177],v2 offset:24576\n"
MF "v[132:135],a[56:59],a[240:243],v[132:135]\n"
TR "v[178:179],v2 offset:26624\n"
MF "v[132:135],a[16:19],v[188:191],v[132:135]\n"
MF "v[124:127],a[12:15],v[188:191],v[252:255]\n"
TR "v[180:181],v2 offset:28672\n"
MF "v[124:127],a[52:55],a[240:243],v[124:127]\n"
TR "v[182:183],v2 offset:30720\n"
MF "v[124:127],a[92:95],a[244:247],v[124:127]\n"
MF "v[116:119],a[88:91],a[244:247],v[252:255]\n"
TR "v[184:185],v2 offset:32768\n"
MF "v[116:119],a[48:51],a[240:243],v[116:119]\n"
TR "v[186:187],v2 offset:34816\n"
MF "v[116:119],a[8:11],v[188:191],v[116:119]\n"
MF "v[108:111],a[4:7],v[188:191],v[252:255]\n"
MF "v[108:111],a[44:47],a[240:243],v[108:111]\n"
MF "v[108:111],a[84:87],a[244:247],v[108:111]\n"
MF "v[100:103],a[80:83],a[244:247],v[252:255]\n"
MF "v[100:103],a[40:43],a[240:243],v[100:103]\n"
MF "v[100:103],a[0:3],v[188:191],v[100:103]\n"
"s_waitcnt lgkmcnt(0)\n"
MF "v[136:139],a[0:3],v[176:179],v[252:255]\n"
TR "v[188:189],v3 offset:24576\n"
MF "v[136:139],a[40:43],v[180:183],v[136:139]\n"
TR "v[190:191],v3 offset:26624\n"
MF "v[136:139],a[80:83],v[184:187],v[136:139]\n"
MF "v[144:147],a[84:87],v[184:187],v[252:255]\n"
TR "a[240:241],v3 offset:28672\n"
MF "v[144:147],a[44:47],v[180:183],v[144:147]\n"
TR "a[242:243],v3 offset:30720\n"
MF "v[144:147],a[4:7],v[176:179],v[144:147]\n"
MF "v[152:155],a[8:11],v[176:179],v[252:255]\n"
TR "a[244:245],v3 offset:32768\n"
MF "v[152:155],a[48:51],v[180:183],v[152:155]\n"
TR "a[246:247],v3 offset:34816\n"
MF "v[152:155],a[88:91],v[184:187],v[152:155]\n"
MF "v[160:163],a[92:95],v[184:187],v[252:255]\n"
MF "v[160:163],a[52:55],v[180:183],v[160:163]\n"
MF "v[160:163],a[12:15],v[176:179],v[160:163]\n"
MF "v[168:171],a[16:19],v[176:179],v[252:255]\n"
MF "v[168:171],a[56:59],v[180:183],v[168:171]\n"
"s_waitcnt lgkmcnt(0)\n"
MF "v[168:171],a[96:99],v[184:187],v[168:171]\n"
"s_barrier\n"
MF "v[172:175],a[96:99],a[244:247],v[252:255]\n"
TR "v[176:177],v0\n"
MF "v[172:175],a[56:59],a[240:243],v[172:175]\n"
TR "v[178:179],v0 offset:2048\n"
MF "v[172:175],a[16:19],v[188:191],v[172:175]\n"
"ds_read2st64_b32 v[192:193],v13 offset0:192 offset1:193\n"
MF "v[164:167],a[12:15],v[188:191],v[252:255]\n"
TR "v[180:181],v0 offset:4096\n"
MF "v[164:167],a[52:55],a[240:243],v[164:167]\n"
TR "v[182:183],v0 offset:6144\n"
MF "v[164:167],a[92:95],a[244:247],v[164:167]\n"
"ds_read2st64_b32 v[194:195],v13 offset0:194 offset1:195\n"
MF "v[156:159],a[88:91],a[244:247],v[252:255]\n"
TR "v[184:185],v0 offset:8192\n"
MF "v[156:159],a[48:51],a[240:243],v[156:159]\n"
TR "v[186:187],v0 offset:10240\n"
MF "v[156:159],a[8:11],v[188:191],v[156:159]\n"
"ds_read2st64_b32 v[196:197],v13 offset0:196 offset1:197\n"
MF "v[148:151],a[4:7],v[188:191],v[252:255]\n"
"ds_read2st64_b32 v[198:199],v13 offset0:198 offset1:199\n"
MF "v[148:151],a[44:47],a[240:243],v[148:151]\n"
"buffer_load_dword v10,v12,s[40:43],s86 offen sc1\n"
MF "v[148:151],a[84:87],a[244:247],v[148:151]\n"
MF "v[140:143],a[80:83],a[244:247],v[252:255]\n"
MF "v[140:143],a[40:43],a[240:243],v[140:143]\n"
MF "v[140:143],a[0:3],v[188:191],v[140:143]\n"
"s_waitcnt lgkmcnt(2)\n"
MF "v[96:99],a[120:123],v[176:179],v[96:99]\n"
"s_waitcnt lgkmcnt(0)\n"
TR "v[188:189],v1\n"
"v_add_f32 v16,v16,v192\n"
MF "v[96:99],a[160:163],v[180:183],v[96:99]\n"
TR "v[190:191],v1 offset:2048\n"
"v_add_f32 v17,v17,v193\n"
MF "v[96:99],a[200:203],v[184:187],v[96:99]\n"
"ds_read2st64_b32 v[200:201],v13 offset0:200 offset1:201\n"
"v_add_f32 v18,v18,v194\n"
"s_waitcnt vmcnt(2)\n"
"v_mov_b32 v14,s76\n"
"s_add_u32 s76,s76,1\n"
MF "v[104:107],a[204:207],v[184:187],v[104:107]\n"
TR "a[240:241],v1 offset:4096\n"
"v_add_f32 v19,v19,v195\n"
"buffer_store_dword v14,v15,s[40:43],s94 offen sc1\n"
MF "v[104:107],a[164:167],v[180:183],v[104:107]\n"
TR "a[242:243],v1 offset:6144\n"
"v_add_f32 v20,v20,v196\n"
MF "v[104:107],a[124:127],v[176:179],v[104:107]\n"
"ds_read2st64_b32 v[202:203],v13 offset0:202 offset1:203\n"
"v_add_f32 v21,v21,v197\n"
MF "v[112:115],a[128:131],v[176:179],v[112:115]\n"
TR "a[244:245],v1 offset:8192\n"
"v_add_f32 v22,v22,v198\n"
MF "v[112:115],a[168:171],v[180:183],v[112:115]\n"
TR "a[246:247],v1 offset:10240\n"
"v_add_f32 v23,v23,v199\n"
MF "v[112:115],a[208:211],v[184:187],v[112:115]\n"
"ds_read2st64_b32 v[204:205],v13 offset0:204 offset1:205\n"
CV "v16,v16,v17\n"
MF "v[120:123],a[212:215],v[184:187],v[120:123]\n"
"ds_read2st64_b32 v[206:207],v13 offset0:206 offset1:207\n"
CV "v17,v18,v19\n"
MF "v[120:123],a[172:175],v[180:183],v[120:123]\n"
"s_add_u32 s87,s90,0\n"
CV "v18,v20,v21\n"
CV "v19,v22,v23\n"
MF "v[120:123],a[132:135],v[176:179],v[120:123]\n"
MF "v[128:131],a[136:139],v[176:179],v[128:131]\n"
"buffer_store_dwordx4 v[16:19],v9,s[40:43],s87 offen sc1\n"
MF "v[128:131],a[176:179],v[180:183],v[128:131]\n"
MF "v[128:131],a[216:219],v[184:187],v[128:131]\n"
"s_waitcnt lgkmcnt(2)\n"
MF "v[132:135],a[216:219],a[244:247],v[132:135]\n"
"s_waitcnt lgkmcnt(0)\n"
TR "v[176:177],v2\n"
"v_add_f32 v24,v24,v200\n"
MF "v[132:135],a[176:179],a[240:243],v[132:135]\n"
TR "v[178:179],v2 offset:2048\n"
"v_add_f32 v25,v25,v201\n"
MF "v[132:135],a[136:139],v[188:191],v[132:135]\n"
"ds_read2st64_b32 v[192:193],v13 offset0:208 offset1:209\n"
"v_add_f32 v26,v26,v202\n"
MF "v[124:127],a[132:135],v[188:191],v[124:127]\n"
TR "v[180:181],v2 offset:4096\n"
"v_add_f32 v27,v27,v203\n"
MF "v[124:127],a[172:175],a[240:243],v[124:127]\n"
TR "v[182:183],v2 offset:6144\n"
"v_add_f32 v28,v28,v204\n"
MF "v[124:127],a[212:215],a[244:247],v[124:127]\n"
"ds_read2st64_b32 v[194:195],v13 offset0:210 offset1:211\n"
"v_add_f32 v29,v29,v205\n"
MF "v[116:119],a[208:211],a[244:247],v[116:119]\n"
TR "v[184:185],v2 offset:8192\n"
"v_add_f32 v30,v30,v206\n"
MF "v[116:119],a[168:171],a[240:243],v[116:119]\n"
TR "v[186:187],v2 offset:10240\n"
"v_add_f32 v31,v31,v207\n"
MF "v[116:119],a[128:131],v[188:191],v[116:119]\n"
"ds_read2st64_b32 v[196:197],v13 offset0:212 offset1:213\n"
CV "v24,v24,v25\n"
MF "v[108:111],a[124:127],v[188:191],v[108:111]\n"
"ds_read2st64_b32 v[198:199],v13 offset0:214 offset1:215\n"
CV "v25,v26,v27\n"
MF "v[108:111],a[164:167],a[240:243],v[108:111]\n"
"s_add_u32 s87,s90,1024\n"
CV "v26,v28,v29\n"
CV "v27,v30,v31\n"
MF "v[108:111],a[204:207],a[244:247],v[108:111]\n"
MF "v[100:103],a[200:203],a[244:247],v[100:103]\n"
"buffer_store_dwordx4 v[24:27],v9,s[40:43],s87 offen sc1\n"
MF "v[100:103],a[160:163],a[240:243],v[100:103]\n"
MF "v[100:103],a[120:123],v[188:191],v[100:103]\n"
"s_waitcnt lgkmcnt(2)\n"
MF "v[136:139],a[120:123],v[176:179],v[136:139]\n"
"s_waitcnt lgkmcnt(0)\n"
TR "v[188:189],v3\n"
"v_add_f32 v32,v32,v192\n"
MF "v[136:139],a[160:163],v[180:183],v[136:139]\n"
TR "v[190:191],v3 offset:2048\n"
"v_add_f32 v33,v33,v193\n"
MF "v[136:139],a[200:203],v[184:187],v[136:139]\n"
"ds_read2st64_b32 v[200:201],v13 offset0:216 offset1:217\n"
"v_add_f32 v34,v34,v194\n"
MF "v[144:147],a[204:207],v[184:187],v[144:147]\n"
TR "a[240:241],v3 offset:4096\n"
"v_add_f32 v35,v35,v195\n"
MF "v[144:147],a[164:167],v[180:183],v[144:147]\n"
TR "a[242:243],v3 offset:6144\n"
"v_add_f32 v36,v36,v196\n"
MF "v[144:147],a[124:127],v[176:179],v[144:147]\n"
"ds_read2st64_b32 v[202:203],v13 offset0:218 offset1:219\n"
"v_add_f32 v37,v37,v197\n"
MF "v[152:155],a[128:131],v[176:179],v[152:155]\n"
TR "a[244:245],v3 offset:8192\n"
"v_add_f32 v38,v38,v198\n"
MF "v[152:155],a[168:171],v[180:183],v[152:155]\n"
TR "a[246:247],v3 offset:10240\n"
"v_add_f32 v39,v39,v199\n"
MF "v[152:155],a[208:211],v[184:187],v[152:155]\n"
"ds_read2st64_b32 v[204:205],v13 offset0:220 offset1:221\n"
CV "v32,v32,v33\n"
MF "v[160:163],a[212:215],v[184:187],v[160:163]\n"
"ds_read2st64_b32 v[206:207],v13 offset0:222 offset1:223\n"
CV "v33,v34,v35\n"
MF "v[160:163],a[172:175],v[180:183],v[160:163]\n"
"s_add_u32 s87,s90,2048\n"
CV "v34,v36,v37\n"
CV "v35,v38,v39\n"
MF "v[160:163],a[132:135],v[176:179],v[160:163]\n"
".Lpg35h:\n"
"s_cbranch_execz .Lisr4\n"
MF "v[168:171],a[136:139],v[176:179],v[168:171]\n"
"buffer_store_dwordx4 v[32:35],v9,s[40:43],s87 offen sc1\n"
MF "v[168:171],a[176:179],v[180:183],v[168:171]\n"
"s_waitcnt lgkmcnt(0)\n"
MF "v[168:171],a[216:219],v[184:187],v[168:171]\n"
"s_barrier\n"
MF "v[172:175],a[216:219],a[244:247],v[172:175]\n"
TR "v[176:177],v0 offset:49152\n"
"v_add_f32 v40,v40,v200\n"
MF "v[172:175],a[176:179],a[240:243],v[172:175]\n"
"s_mov_b32 s82,s70\n"
TR "v[178:179],v0 offset:51200\n"
"v_add_f32 v41,v41,v201\n"
MF "v[172:175],a[136:139],v[188:191],v[172:175]\n"
"ds_read2st64_b32 v[192:193],v13 offset0:224 offset1:225\n"
"v_add_f32 v42,v42,v202\n"
MF "v[164:167],a[132:135],v[188:191],v[164:167]\n"
TR "v[180:181],v0 offset:53248\n"
"v_add_f32 v43,v43,v203\n"
MF "v[164:167],a[172:175],a[240:243],v[164:167]\n"
TR "v[182:183],v0 offset:55296\n"
"v_add_f32 v44,v44,v204\n"
MF "v[164:167],a[212:215],a[244:247],v[164:167]\n"
"ds_read2st64_b32 v[194:195],v13 offset0:226 offset1:227\n"
"v_add_f32 v45,v45,v205\n"
MF "v[156:159],a[208:211],a[244:247],v[156:159]\n"
TR "v[184:185],v0 offset:57344\n"
"v_add_f32 v46,v46,v206\n"
MF "v[156:159],a[168:171],a[240:243],v[156:159]\n"
TR "v[186:187],v0 offset:59392\n"
"v_add_f32 v47,v47,v207\n"
MF "v[156:159],a[128:131],v[188:191],v[156:159]\n"
"ds_read2st64_b32 v[196:197],v13 offset0:228 offset1:229\n"
CV "v40,v40,v41\n"
MF "v[148:151],a[124:127],v[188:191],v[148:151]\n"
"ds_read2st64_b32 v[198:199],v13 offset0:230 offset1:231\n"
CV "v41,v42,v43\n"
MF "v[148:151],a[164:167],a[240:243],v[148:151]\n"
"s_add_u32 s87,s90,3072\n"
CV "v42,v44,v45\n"
CV "v43,v46,v47\n"
MF "v[148:151],a[204:207],a[244:247],v[148:151]\n"
MF "v[140:143],a[200:203],a[244:247],v[140:143]\n"
"buffer_store_dwordx4 v[40:43],v9,s[40:43],s87 offen sc1\n"
MF "v[140:143],a[160:163],a[240:243],v[140:143]\n"
MF "v[140:143],a[120:123],v[188:191],v[140:143]\n"
"s_waitcnt lgkmcnt(2)\n"
MF "v[96:99],a[20:23],v[176:179],v[96:99]\n"
"s_waitcnt lgkmcnt(0)\n"
TR "v[188:189],v1 offset:49152\n"
"v_add_f32 v48,v48,v192\n"
MF "v[96:99],a[60:63],v[180:183],v[96:99]\n"
TR "v[190:191],v1 offset:51200\n"
"v_add_f32 v49,v49,v193\n"
MF "v[96:99],a[100:103],v[184:187],v[96:99]\n"
"ds_read2st64_b32 v[200:201],v13 offset0:0 offset1:1\n"
"v_add_f32 v50,v50,v194\n"
MF "v[104:107],a[104:107],v[184:187],v[104:107]\n"
TR "a[240:241],v1 offset:53248\n"
"v_add_f32 v51,v51,v195\n"
MF "v[104:107],a[64:67],v[180:183],v[104:107]\n"
TR "a[242:243],v1 offset:55296\n"
"v_add_f32 v52,v52,v196\n"
MF "v[104:107],a[24:27],v[176:179],v[104:107]\n"
"ds_read2st64_b32 v[202:203],v13 offset0:2 offset1:3\n"
"v_add_f32 v53,v53,v197\n"
MF "v[112:115],a[28:31],v[176:179],v[112:115]\n"
TR "a[244:245],v1 offset:57344\n"
"v_add_f32 v54,v54,v198\n"
MF "v[112:115],a[68:71],v[180:183],v[112:115]\n"
TR "a[246:247],v1 offset:59392\n"
"v_add_f32 v55,v55,v199\n"
MF "v[112:115],a[108:111],v[184:187],v[112:115]\n"
"ds_read2st64_b32 v[204:205],v13 offset0:4 offset1:5\n"
CV "v48,v48,v49\n"
MF "v[120:123],a[112:115],v[184:187],v[120:123]\n"
"ds_read2st64_b32 v[206:207],v13 offset0:6 offset1:7\n"
CV "v49,v50,v51\n"
MF "v[120:123],a[72:75],v[180:183],v[120:123]\n"
"s_add_u32 s87,s90,4096\n"
CV "v50,v52,v53\n"
CV "v51,v54,v55\n"
MF "v[120:123],a[32:35],v[176:179],v[120:123]\n"
MF "v[128:131],a[36:39],v[176:179],v[128:131]\n"
"buffer_store_dwordx4 v[48:51],v9,s[40:43],s87 offen sc1\n"
MF "v[128:131],a[76:79],v[180:183],v[128:131]\n"
MF "v[128:131],a[116:119],v[184:187],v[128:131]\n"
"s_waitcnt lgkmcnt(2)\n"
MF "v[132:135],a[116:119],a[244:247],v[132:135]\n"
"s_waitcnt lgkmcnt(0)\n"
TR "v[176:177],v2 offset:49152\n"
"v_add_f32 v56,v56,v200\n"
MF "v[132:135],a[76:79],a[240:243],v[132:135]\n"
TR "v[178:179],v2 offset:51200\n"
"v_add_f32 v57,v57,v201\n"
MF "v[132:135],a[36:39],v[188:191],v[132:135]\n"
"ds_read2st64_b32 v[192:193],v13 offset0:8 offset1:9\n"
"v_add_f32 v58,v58,v202\n"
MF "v[124:127],a[32:35],v[188:191],v[124:127]\n"
TR "v[180:181],v2 offset:53248\n"
"v_add_f32 v59,v59,v203\n"
MF "v[124:127],a[72:75],a[240:243],v[124:127]\n"
TR "v[182:183],v2 offset:55296\n"
"v_add_f32 v60,v60,v204\n"
MF "v[124:127],a[112:115],a[244:247],v[124:127]\n"
"ds_read2st64_b32 v[194:195],v13 offset0:10 offset1:11\n"
"v_add_f32 v61,v61,v205\n"
MF "v[116:119],a[108:111],a[244:247],v[116:119]\n"
TR "v[184:185],v2 offset:57344\n"
"v_add_f32 v62,v62,v206\n"
MF "v[116:119],a[68:71],a[240:243],v[116:119]\n"
TR "v[186:187],v2 offset:59392\n"
"v_add_f32 v63,v63,v207\n"
MF "v[116:119],a[28:31],v[188:191],v[116:119]\n"
"ds_read2st64_b32 v[196:197],v13 offset0:12 offset1:13\n"
CV "v56,v56,v57\n"
MF "v[108:111],a[24:27],v[188:191],v[108:111]\n"
"ds_read2st64_b32 v[198:199],v13 offset0:14 offset1:15\n"
CV "v57,v58,v59\n"
MF "v[108:111],a[64:67],a[240:243],v[108:111]\n"
"s_add_u32 s87,s90,10240\n"
CV "v58,v60,v61\n"
CV "v59,v62,v63\n"
MF "v[108:111],a[104:107],a[244:247],v[108:111]\n"
MF "v[100:103],a[100:103],a[244:247],v[100:103]\n"
"buffer_store_dwordx4 v[56:59],v9,s[40:43],s87 offen sc1\n"
MF "v[100:103],a[60:63],a[240:243],v[100:103]\n"
"s_waitcnt vmcnt(7)\n"
"s_cmp_lt_u32 s85,6\n"
MF "v[100:103],a[20:23],v[188:191],v[100:103]\n"
"s_cbranch_scc1 .Ljs10\n"
"v_mov_b32 v236,v228\n"
"v_mov_b32 v237,v229\n"
MF "v[136:139],a[252:255],v[208:211],v[136:139]\n"
MF "v[144:147],a[252:255],v[212:215],v[144:147]\n"
MF "v[152:155],a[252:255],v[216:219],v[152:155]\n"
MF "v[160:163],a[252:255],v[220:223],v[160:163]\n"
MF "v[168:171],a[252:255],v[224:227],v[168:171]\n"
MF "v[140:143],a[248:251],v[208:211],v[140:143]\n"
MF "v[148:151],a[248:251],v[212:215],v[148:151]\n"
MF "v[156:159],a[248:251],v[216:219],v[156:159]\n"
MF "v[164:167],a[248:251],v[220:223],v[164:167]\n"
MF "v[172:175],a[248:251],v[224:227],v[172:175]\n"
MF "v[248:251],v[230:233],a[250:253],v[248:251]\n"
MF "v[244:247],v[234:237],a[250:253],v[244:247]\n"
".Ljs10:\n"
"s_waitcnt lgkmcnt(2)\n"
MF "v[136:139],a[20:23],v[176:179],v[136:139]\n"
"s_waitcnt lgkmcnt(0)\n"
TR "v[188:189],v3 offset:49152\n"
"v_add_f32 v64,v64,v192\n"
MF "v[136:139],a[60:63],v[180:183],v[136:139]\n"
TR "v[190:191],v3 offset:51200\n"
"v_add_f32 v65,v65,v193\n"
MF "v[136:139],a[100:103],v[184:187],v[136:139]\n"
"ds_read2st64_b32 v[200:201],v13 offset0:16 offset1:17\n"
"v_add_f32 v66,v66,v194\n"
MF "v[144:147],a[104:107],v[184:187],v[144:147]\n"
TR "a[240:241],v3 offset:53248\n"
"v_add_f32 v67,v67,v195\n"
MF "v[144:147],a[64:67],v[180:183],v[144:147]\n"
TR "a[242:243],v3 offset:55296\n"
"v_add_f32 v68,v68,v196\n"
MF "v[144:147],a[24:27],v[176:179],v[144:147]\n"
"ds_read2st64_b32 v[202:203],v13 offset0:18 offset1:19\n"
"v_add_f32 v69,v69,v197\n"
MF "v[152:155],a[28:31],v[176:179],v[152:155]\n"
TR "a[244:245],v3 offset:57344\n"
"v_add_f32 v70,v70,v198\n"
MF "v[152:155],a[68:71],v[180:183],v[152:155]\n"
TR "a[246:247],v3 offset:59392\n"
"v_add_f32 v71,v71,v199\n"
MF "v[152:155],a[108:111],v[184:187],v[152:155]\n"
"ds_read2st64_b32 v[204:205],v13 offset0:20 offset1:21\n"
CV "v64,v64,v65\n"
MF "v[160:163],a[112:115],v[184:187],v[160:163]\n"
"ds_read2st64_b32 v[206:207],v13 offset0:22 offset1:23\n"
CV "v65,v66,v67\n"
MF "v[160:163],a[72:75],v[180:183],v[160:163]\n"
"s_add_u32 s87,s90,11264\n"
CV "v66,v68,v69\n"
CV "v67,v70,v71\n"
MF "v[160:163],a[32:35],v[176:179],v[160:163]\n"
"v_sub_u32 v10,v10,v11\n"
MF "v[168:171],a[36:39],v[176:179],v[168:171]\n"
"buffer_store_dwordx4 v[64:67],v9,s[40:43],s87 offen sc1\n"
MF "v[168:171],a[76:79],v[180:183],v[168:171]\n"
"s_waitcnt lgkmcnt(0)\n"
MF "v[168:171],a[116:119],v[184:187],v[168:171]\n"
"s_barrier\n"
MF "v[172:175],a[116:119],a[244:247],v[172:175]\n"
TR "v[176:177],v4\n"
"v_add_f32 v72,v72,v200\n"
MF "v[172:175],a[76:79],a[240:243],v[172:175]\n"
TR "v[178:179],v4 offset:2048\n"
"v_add_f32 v73,v73,v201\n"
MF "v[172:175],a[36:39],v[188:191],v[172:175]\n"
"ds_read2st64_b32 v[192:193],v13 offset0:24 offset1:25\n"
"v_add_f32 v74,v74,v202\n"
MF "v[164:167],a[32:35],v[188:191],v[164:167]\n"
TR "v[180:181],v4 offset:4096\n"
"v_add_f32 v75,v75,v203\n"
MF "v[164:167],a[72:75],a[240:243],v[164:167]\n"
TR "v[182:183],v4 offset:6144\n"
"v_add_f32 v76,v76,v204\n"
MF "v[164:167],a[112:115],a[244:247],v[164:167]\n"
"ds_read2st64_b32 v[194:195],v13 offset0:26 offset1:27\n"
"v_add_f32 v77,v77,v205\n"
MF "v[156:159],a[108:111],a[244:247],v[156:159]\n"
TR "v[184:185],v4 offset:8192\n"
"v_add_f32 v78,v78,v206\n"
MF "v[156:159],a[68:71],a[240:243],v[156:159]\n"
TR "v[186:187],v4 offset:10240\n"
"v_add_f32 v79,v79,v207\n"
MF "v[156:159],a[28:31],v[188:191],v[156:159]\n"
"ds_read2st64_b32 v[196:197],v13 offset0:28 offset1:29\n"
CV "v72,v72,v73\n"
MF "v[148:151],a[24:27],v[188:191],v[148:151]\n"
"ds_read2st64_b32 v[198:199],v13 offset0:30 offset1:31\n"
CV "v73,v74,v75\n"
MF "v[148:151],a[64:67],a[240:243],v[148:151]\n"
"s_add_u32 s87,s90,12288\n"
CV "v74,v76,v77\n"
CV "v75,v78,v79\n"
MF "v[148:151],a[104:107],a[244:247],v[148:151]\n"
".Lnf6:\n"
"v_readlane_b32 s58,v10,s67\n"
"s_cmp_lt_u32 s58,s85\n"
"s_cbranch_scc1 .Lsp6\n"
".Lnb6:\n"
MF "v[140:143],a[100:103],a[244:247],v[140:143]\n"
"buffer_store_dwordx4 v[72:75],v9,s[40:43],s87 offen sc1\n"
MF "v[140:143],a[60:63],a[240:243],v[140:143]\n"
"s_add_u32 s70,s70,20480\n"
".Lnf7:\n"
"v_cmp_gt_u32 vcc,4,v10\n"
"s_cbranch_vccnz .Lsp7\n"
".Lnb7:\n"
MF "v[140:143],a[20:23],v[188:191],v[140:143]\n"
"s_waitcnt lgkmcnt(2)\n"
MF "v[96:99],a[140:143],v[176:179],v[96:99]\n"
"s_waitcnt lgkmcnt(0)\n"
TR "v[188:189],v5\n"
"v_add_f32 v80,v80,v192\n"
MF "v[96:99],a[180:183],v[180:183],v[96:99]\n"
TR "v[190:191],v5 offset:2048\n"
"v_add_f32 v81,v81,v193\n"
MF "v[96:99],a[220:223],v[184:187],v[96:99]\n"
"ds_read2st64_b32 v[200:201],v13 offset0:32 offset1:33\n"
"v_add_f32 v82,v82,v194\n"
MF "v[104:107],a[224:227],v[184:187],v[104:107]\n"
TR "a[240:241],v5 offset:4096\n"
"v_add_f32 v83,v83,v195\n"
MF "v[104:107],a[184:187],v[180:183],v[104:107]\n"
TR "a[242:243],v5 offset:6144\n"
"v_add_f32 v84,v84,v196\n"
MF "v[104:107],a[144:147],v[176:179],v[104:107]\n"
"ds_read2st64_b32 v[202:203],v13 offset0:34 offset1:35\n"
"v_add_f32 v85,v85,v197\n"
MF "v[112:115],a[148:151],v[176:179],v[112:115]\n"
TR "a[244:245],v5 offset:8192\n"
"v_add_f32 v86,v86,v198\n"
MF "v[112:115],a[188:191],v[180:183],v[112:115]\n"
TR "a[246:247],v5 offset:10240\n"
"v_add_f32 v87,v87,v199\n"
MF "v[112:115],a[228:231],v[184:187],v[112:115]\n"
"ds_read2st64_b32 v[204:205],v13 offset0:36 offset1:37\n"
CV "v80,v80,v81\n"
MF "v[120:123],a[232:235],v[184:187],v[120:123]\n"
"s_add_u32 s2,s2,204800\n"
"ds_read2st64_b32 v[206:207],v13 offset0:38 offset1:39\n"
CV "v81,v82,v83\n"
MF "v[120:123],a[192:195],v[180:183],v[120:123]\n"
"s_add_u32 s87,s90,13312\n"
CV "v82,v84,v85\n"
CV "v83,v86,v87\n"
MF "v[120:123],a[152:155],v[176:179],v[120:123]\n"
LD "v[228:231],v9,s[40:43],s2 offen sc1\n"
MF "v[128:131],a[156:159],v[176:179],v[128:131]\n"
"buffer_store_dwordx4 v[80:83],v9,s[40:43],s87 offen sc1\n"
MF "v[128:131],a[196:199],v[180:183],v[128:131]\n"
"s_add_u32 s2,s2,204800\n"
MF "v[128:131],a[236:239],v[184:187],v[128:131]\n"
"s_waitcnt lgkmcnt(2)\n"
LD "v[232:235],v9,s[40:43],s2 offen sc1\n"
MF "v[132:135],a[236:239],a[244:247],v[132:135]\n"
"s_waitcnt lgkmcnt(0)\n"
TR "v[176:177],v6\n"
"v_add_f32 v88,v88,v200\n"
MF "v[132:135],a[196:199],a[240:243],v[132:135]\n"
TR "v[178:179],v6 offset:2048\n"
"v_add_f32 v89,v89,v201\n"
MF "v[132:135],a[156:159],v[188:191],v[132:135]\n"
"v_add_f32 v90,v90,v202\n"
"v_add_f32 v91,v91,v203\n"
MF "v[124:127],a[152:155],v[188:191],v[124:127]\n"
TR "v[180:181],v6 offset:4096\n"
"v_add_f32 v92,v92,v204\n"
MF "v[124:127],a[192:195],a[240:243],v[124:127]\n"
TR "v[182:183],v6 offset:6144\n"
"v_add_f32 v93,v93,v205\n"
MF "v[124:127],a[232:235],a[244:247],v[124:127]\n"
"v_add_f32 v94,v94,v206\n"
"v_add_f32 v95,v95,v207\n"
MF "v[116:119],a[228:231],a[244:247],v[116:119]\n"
TR "v[184:185],v6 offset:8192\n"
CV "v88,v88,v89\n"
MF "v[116:119],a[188:191],a[240:243],v[116:119]\n"
TR "v[186:187],v6 offset:10240\n"
CV "v89,v90,v91\n"
MF "v[116:119],a[148:151],v[188:191],v[116:119]\n"
"s_add_u32 s87,s90,14336\n"
CV "v90,v92,v93\n"
CV "v91,v94,v95\n"
MF "v[108:111],a[144:147],v[188:191],v[108:111]\n"
LD "v[208:211],v9,s[40:43],s82 offen sc1\n"
MF "v[108:111],a[184:187],a[240:243],v[108:111]\n"
MF "v[108:111],a[224:227],a[244:247],v[108:111]\n"
MF "v[100:103],a[220:223],a[244:247],v[100:103]\n"
"buffer_store_dwordx4 v[88:91],v9,s[40:43],s87 offen sc1\n"
MF "v[100:103],a[180:183],a[240:243],v[100:103]\n"
"s_mov_b32 s90,s91\n"
"s_add_u32 s91,s91,20480\n"
MF "v[100:103],a[140:143],v[188:191],v[100:103]\n"
"s_waitcnt lgkmcnt(0)\n"
MF "v[136:139],a[140:143],v[176:179],v[136:139]\n"
TR "v[188:189],v7\n"
MF "v[136:139],a[180:183],v[180:183],v[136:139]\n"
TR "v[190:191],v7 offset:2048\n"
MF "v[136:139],a[220:223],v[184:187],v[136:139]\n"
LD "v[212:215],v9,s[40:43],s82 offen offset:1024 sc1\n"
MF "v[144:147],a[224:227],v[184:187],v[144:147]\n"
TR "a[240:241],v7 offset:4096\n"
MF "v[144:147],a[184:187],v[180:183],v[144:147]\n"
TR "a[242:243],v7 offset:6144\n"
MF "v[144:147],a[144:147],v[176:179],v[144:147]\n"
MF "v[152:155],a[148:151],v[176:179],v[152:155]\n"
TR "a[244:245],v7 offset:8192\n"
MF "v[152:155],a[188:191],v[180:183],v[152:155]\n"
TR "a[246:247],v7 offset:10240\n"
MF "v[152:155],a[228:231],v[184:187],v[152:155]\n"
MF "v[160:163],a[232:235],v[184:187],v[160:163]\n"
MF "v[160:163],a[192:195],v[180:183],v[160:163]\n"
LD "v[216:219],v9,s[40:43],s82 offen offset:2048 sc1\n"
MF "v[160:163],a[152:155],v[176:179],v[160:163]\n"
MF "v[168:171],a[156:159],v[176:179],v[168:171]\n"
MF "v[168:171],a[196:199],v[180:183],v[168:171]\n"
"s_waitcnt lgkmcnt(0)\n"
MF "v[168:171],a[236:239],v[184:187],v[168:171]\n"
"s_barrier\n"
MF "v[172:175],a[236:239],a[244:247],v[172:175]\n"
TR "v[176:177],v0 offset:24576\n"
MF "v[172:175],a[196:199],a[240:243],v[172:175]\n"
"s_add_u32 s85,s85,1\n"
TR "v[178:179],v0 offset:26624\n"
MF "v[172:175],a[156:159],v[188:191],v[172:175]\n"
MF "v[164:167],a[152:155],v[188:191],v[164:167]\n"
TR "v[180:181],v0 offset:28672\n"
MF "v[164:167],a[192:195],a[240:243],v[164:167]\n"
TR "v[182:183],v0 offset:30720\n"
MF "v[164:167],a[232:235],a[244:247],v[164:167]\n"
LD "v[220:223],v9,s[40:43],s82 offen offset:3072 sc1\n"
MF "v[156:159],a[228:231],a[244:247],v[156:159]\n"
TR "v[184:185],v0 offset:32768\n"
MF "v[156:159],a[188:191],a[240:243],v[156:159]\n"
TR "v[186:187],v0 offset:34816\n"
MF "v[156:159],a[148:151],v[188:191],v[156:159]\n"
MF "v[148:151],a[144:147],v[188:191],v[148:151]\n"
MF "v[148:151],a[184:187],a[240:243],v[148:151]\n"
MF "v[148:151],a[224:227],a[244:247],v[148:151]\n"
MF "v[140:143],a[220:223],a[244:247],v[140:143]\n"
MF "v[140:143],a[180:183],a[240:243],v[140:143]\n"
MF "v[140:143],a[140:143],v[188:191],v[140:143]\n"
"s_waitcnt lgkmcnt(0)\n"
MF "v[16:19],a[0:3],v[176:179],v[252:255]\n"
TR "v[188:189],v1 offset:24576\n"
MF "v[16:19],a[40:43],v[180:183],v[16:19]\n"
"s_add_u32 s82,s82,4096\n"
TR "v[190:191],v1 offset:26624\n"
MF "v[16:19],a[80:83],v[184:187],v[16:19]\n"
LD "v[224:227],v9,s[40:43],s82 offen sc1\n"
MF "v[24:27],a[84:87],v[184:187],v[252:255]\n"
TR "a[240:241],v1 offset:28672\n"
MF "v[24:27],a[44:47],v[180:183],v[24:27]\n"
TR "a[242:243],v1 offset:30720\n"
MF "v[24:27],a[4:7],v[176:179],v[24:27]\n"
MF "v[32:35],a[8:11],v[176:179],v[252:255]\n"
TR "a[244:245],v1 offset:32768\n"
MF "v[32:35],a[48:51],v[180:183],v[32:35]\n"
TR "a[246:247],v1 offset:34816\n"
MF "v[32:35],a[88:91],v[184:187],v[32:35]\n"
MF "v[40:43],a[92:95],v[184:187],v[252:255]\n"
MF "v[40:43],a[52:55],v[180:183],v[40:43]\n"
MF "v[40:43],a[12:15],v[176:179],v[40:43]\n"
MF "v[48:51],a[16:19],v[176:179],v[252:255]\n"
MF "v[48:51],a[56:59],v[180:183],v[48:51]\n"
MF "v[48:51],a[96:99],v[184:187],v[48:51]\n"
"s_waitcnt lgkmcnt(0)\n"
MF "v[52:55],a[96:99],a[244:247],v[252:255]\n"
TR "v[176:177],v2 offset:24576\n"
MF "v[52:55],a[56:59],a[240:243],v[52:55]\n"
TR "v[178:179],v2 offset:26624\n"
MF "v[52:55],a[16:19],v[188:191],v[52:55]\n"
MF "v[44:47],a[12:15],v[188:191],v[252:255]\n"
TR "v[180:181],v2 offset:28672\n"
MF "v[44:47],a[52:55],a[240:243],v[44:47]\n"
TR "v[182:183],v2 offset:30720\n"
MF "v[44:47],a[92:95],a[244:247],v[44:47]\n"
MF "v[36:39],a[88:91],a[244:247],v[252:255]\n"
TR "v[184:185],v2 offset:32768\n"
MF "v[36:39],a[48:51],a[240:243],v[36:39]\n"
TR "v[186:187],v2 offset:34816\n"
MF "v[36:39],a[8:11],v[188:191],v[36:39]\n"
MF "v[28:31],a[4:7],v[188:191],v[252:255]\n"
MF "v[28:31],a[44:47],a[240:243],v[28:31]\n"
MF "v[28:31],a[84:87],a[244:247],v[28:31]\n"
MF "v[20:23],a[80:83],a[244:247],v[252:255]\n"
MF "v[20:23],a[40:43],a[240:243],v[20:23]\n"
MF "v[20:23],a[0:3],v[188:191],v[20:23]\n"
"s_waitcnt lgkmcnt(0)\n"
MF "v[56:59],a[0:3],v[176:179],v[252:255]\n"
TR "v[188:189],v3 offset:24576\n"
MF "v[56:59],a[40:43],v[180:183],v[56:59]\n"
TR "v[190:191],v3 offset:26624\n"
MF "v[56:59],a[80:83],v[184:187],v[56:59]\n"
MF "v[64:67],a[84:87],v[184:187],v[252:255]\n"
TR "a[240:241],v3 offset:28672\n"
MF "v[64:67],a[44:47],v[180:183],v[64:67]\n"
TR "a[242:243],v3 offset:30720\n"
MF "v[64:67],a[4:7],v[176:179],v[64:67]\n"
MF "v[72:75],a[8:11],v[176:179],v[252:255]\n"
TR "a[244:245],v3 offset:32768\n"
MF "v[72:75],a[48:51],v[180:183],v[72:75]\n"
TR "a[246:247],v3 offset:34816\n"
MF "v[72:75],a[88:91],v[184:187],v[72:75]\n"
MF "v[80:83],a[92:95],v[184:187],v[252:255]\n"
MF "v[80:83],a[52:55],v[180:183],v[80:83]\n"
MF "v[80:83],a[12:15],v[176:179],v[80:83]\n"
MF "v[88:91],a[16:19],v[176:179],v[252:255]\n"
MF "v[88:91],a[56:59],v[180:183],v[88:91]\n"
"s_waitcnt lgkmcnt(0)\n"
MF "v[88:91],a[96:99],v[184:187],v[88:91]\n"
"s_barrier\n"
MF "v[92:95],a[96:99],a[244:247],v[252:255]\n"
TR "v[176:177],v0\n"
MF "v[92:95],a[56:59],a[240:243],v[92:95]\n"
TR "v[178:179],v0 offset:2048\n"
MF "v[92:95],a[16:19],v[188:191],v[92:95]\n"
"ds_read2st64_b32 v[192:193],v13 offset0:192 offset1:193\n"
MF "v[84:87],a[12:15],v[188:191],v[252:255]\n"
TR "v[180:181],v0 offset:4096\n"
MF "v[84:87],a[52:55],a[240:243],v[84:87]\n"
TR "v[182:183],v0 offset:6144\n"
MF "v[84:87],a[92:95],a[244:247],v[84:87]\n"
"ds_read2st64_b32 v[194:195],v13 offset0:194 offset1:195\n"
MF "v[76:79],a[88:91],a[244:247],v[252:255]\n"
TR "v[184:185],v0 offset:8192\n"
MF "v[76:79],a[48:51],a[240:243],v[76:79]\n"
TR "v[186:187],v0 offset:10240\n"
MF "v[76:79],a[8:11],v[188:191],v[76:79]\n"
"ds_read2st64_b32 v[196:197],v13 offset0:196 offset1:197\n"
MF "v[68:71],a[4:7],v[188:191],v[252:255]\n"
"ds_read2st64_b32 v[198:199],v13 offset0:198 offset1:199\n"
MF "v[68:71],a[44:47],a[240:243],v[68:71]\n"
"buffer_load_dword v10,v12,s[40:43],s86 offen sc1\n"
MF "v[68:71],a[84:87],a[244:247],v[68:71]\n"
MF "v[60:63],a[80:83],a[244:247],v[252:255]\n"
MF "v[60:63],a[40:43],a[240:243],v[60:63]\n"
MF "v[60:63],a[0:3],v[188:191],v[60:63]\n"
"s_waitcnt lgkmcnt(2)\n"
MF "v[16:19],a[120:123],v[176:179],v[16:19]\n"
"s_waitcnt lgkmcnt(0)\n"
TR "v[188:189],v1\n"
"v_add_f32 v96,v96,v192\n"
MF "v[16:19],a[160:163],v[180:183],v[16:19]\n"
TR "v[190:191],v1 offset:2048\n"
"v_add_f32 v97,v97,v193\n"
MF "v[16:19],a[200:203],v[184:187],v[16:19]\n"
"ds_read2st64_b32 v[200:201],v13 offset0:200 offset1:201\n"
"v_add_f32 v98,v98,v194\n"
"s_waitcnt vmcnt(5)\n"
"v_mov_b32 v14,s76\n"
"s_add_u32 s76,s76,1\n"
MF "v[24:27],a[204:207],v[184:187],v[24:27]\n"
TR "a[240:241],v1 offset:4096\n"
"v_add_f32 v99,v99,v195\n"
"buffer_store_dword v14,v15,s[40:43],s94 offen sc1\n"
MF "v[24:27],a[164:167],v[180:183],v[24:27]\n"
TR "a[242:243],v1 offset:6144\n"
"v_add_f32 v100,v100,v196\n"
MF "v[24:27],a[124:127],v[176:179],v[24:27]\n"
"ds_read2st64_b32 v[202:203],v13 offset0:202 offset1:203\n"
"v_add_f32 v101,v101,v197\n"
MF "v[32:35],a[128:131],v[176:179],v[32:35]\n"
TR "a[244:245],v1 offset:8192\n"
"v_add_f32 v102,v102,v198\n"
MF "v[32:35],a[168:171],v[180:183],v[32:35]\n"
TR "a[246:247],v1 offset:10240\n"
"v_add_f32 v103,v103,v199\n"
MF "v[32:35],a[208:211],v[184:187],v[32:35]\n"
"ds_read2st64_b32 v[204:205],v13 offset0:204 offset1:205\n"
CV "v96,v96,v97\n"
MF "v[40:43],a[212:215],v[184:187],v[40:43]\n"
"ds_read2st64_b32 v[206:207],v13 offset0:206 offset1:207\n"
CV "v97,v98,v99\n"
MF "v[40:43],a[172:175],v[180:183],v[40:43]\n"
"s_add_u32 s87,s90,0\n"
CV "v98,v100,v101\n"
CV "v99,v102,v103\n"
MF "v[40:43],a[132:135],v[176:179],v[40:43]\n"
MF "v[48:51],a[136:139],v[176:179],v[48:51]\n"
"buffer_store_dwordx4 v[96:99],v9,s[40:43],s87 offen sc1\n"
MF "v[48:51],a[176:179],v[180:183],v[48:51]\n"
MF "v[48:51],a[216:219],v[184:187],v[48:51]\n"
"s_waitcnt lgkmcnt(2)\n"
MF "v[52:55],a[216:219],a[244:247],v[52:55]\n"
"s_waitcnt lgkmcnt(0)\n"
TR "v[176:177],v2\n"
"v_add_f32 v104,v104,v200\n"
MF "v[52:55],a[176:179],a[240:243],v[52:55]\n"
TR "v[178:179],v2 offset:2048\n"
"v_add_f32 v105,v105,v201\n"
MF "v[52:55],a[136:139],v[188:191],v[52:55]\n"
"ds_read2st64_b32 v[192:193],v13 offset0:208 offset1:209\n"
"v_add_f32 v106,v106,v202\n"
MF "v[44:47],a[132:135],v[188:191],v[44:47]\n"
TR "v[180:181],v2 offset:4096\n"
"v_add_f32 v107,v107,v203\n"
MF "v[44:47],a[172:175],a[240:243],v[44:47]\n"
TR "v[182:183],v2 offset:6144\n"
"v_add_f32 v108,v108,v204\n"
MF "v[44:47],a[212:215],a[244:247],v[44:47]\n"
"ds_read2st64_b32 v[194:195],v13 offset0:210 offset1:211\n"
"v_add_f32 v109,v109,v205\n"
MF "v[36:39],a[208:211],a[244:247],v[36:39]\n"
TR "v[184:185],v2 offset:8192\n"
"v_add_f32 v110,v110,v206\n"
MF "v[36:39],a[168:171],a[240:243],v[36:39]\n"
TR "v[186:187],v2 offset:10240\n"
"v_add_f32 v111,v111,v207\n"
MF "v[36:39],a[128:131],v[188:191],v[36:39]\n"
"ds_read2st64_b32 v[196:197],v13 offset0:212 offset1:213\n"
CV "v104,v104,v105\n"
MF "v[28:31],a[124:127],v[188:191],v[28:31]\n"
"ds_read2st64_b32 v[198:199],v13 offset0:214 offset1:215\n"
CV "v105,v106,v107\n"
MF "v[28:31],a[164:167],a[240:243],v[28:31]\n"
"s_add_u32 s87,s90,1024\n"
CV "v106,v108,v109\n"
CV "v107,v110,v111\n"
MF "v[28:31],a[204:207],a[244:247],v[28:31]\n"
MF "v[20:23],a[200:203],a[244:247],v[20:23]\n"
"buffer_store_dwordx4 v[104:107],v9,s[40:43],s87 offen sc1\n"
MF "v[20:23],a[160:163],a[240:243],v[20:23]\n"
MF "v[20:23],a[120:123],v[188:191],v[20:23]\n"
"s_waitcnt lgkmcnt(2)\n"
MF "v[56:59],a[120:123],v[176:179],v[56:59]\n"
"s_waitcnt lgkmcnt(0)\n"
TR "v[188:189],v3\n"
"v_add_f32 v112,v112,v192\n"
MF "v[56:59],a[160:163],v[180:183],v[56:59]\n"
TR "v[190:191],v3 offset:2048\n"
"v_add_f32 v113,v113,v193\n"
MF "v[56:59],a[200:203],v[184:187],v[56:59]\n"
"ds_read2st64_b32 v[200:201],v13 offset0:216 offset1:217\n"
"v_add_f32 v114,v114,v194\n"
MF "v[64:67],a[204:207],v[184:187],v[64:67]\n"
TR "a[240:241],v3 offset:4096\n"
"v_add_f32 v115,v115,v195\n"
MF "v[64:67],a[164:167],v[180:183],v[64:67]\n"
TR "a[242:243],v3 offset:6144\n"
"v_add_f32 v116,v116,v196\n"
MF "v[64:67],a[124:127],v[176:179],v[64:67]\n"
"ds_read2st64_b32 v[202:203],v13 offset0:218 offset1:219\n"
"v_add_f32 v117,v117,v197\n"
MF "v[72:75],a[128:131],v[176:179],v[72:75]\n"
TR "a[244:245],v3 offset:8192\n"
"v_add_f32 v118,v118,v198\n"
MF "v[72:75],a[168:171],v[180:183],v[72:75]\n"
".Lpg36h:\n"
"s_cbranch_execz .Lisr4\n"
TR "a[246:247],v3 offset:10240\n"
"v_add_f32 v119,v119,v199\n"
MF "v[72:75],a[208:211],v[184:187],v[72:75]\n"
"ds_read2st64_b32 v[204:205],v13 offset0:220 offset1:221\n"
CV "v112,v112,v113\n"
MF "v[80:83],a[212:215],v[184:187],v[80:83]\n"
"ds_read2st64_b32 v[206:207],v13 offset0:222 offset1:223\n"
CV "v113,v114,v115\n"
MF "v[80:83],a[172:175],v[180:183],v[80:83]\n"
"s_add_u32 s87,s90,2048\n"
CV "v114,v116,v117\n"
CV "v115,v118,v119\n"
MF "v[80:83],a[132:135],v[176:179],v[80:83]\n"
MF "v[88:91],a[136:139],v[176:179],v[88:91]\n"
"buffer_store_dwordx4 v[112:115],v9,s[40:43],s87 offen sc1\n"
MF "v[88:91],a[176:179],v[180:183],v[88:91]\n"
"s_waitcnt lgkmcnt(0)\n"
MF "v[88:91],a[216:219],v[184:187],v[88:91]\n"
"s_barrier\n"
MF "v[92:95],a[216:219],a[244:247],v[92:95]\n"
TR "v[176:177],v0 offset:49152\n"
"v_add_f32 v120,v120,v200\n"
MF "v[92:95],a[176:179],a[240:243],v[92:95]\n"
"s_mov_b32 s82,s70\n"
TR "v[178:179],v0 offset:51200\n"
"v_add_f32 v121,v121,v201\n"
MF "v[92:95],a[136:139],v[188:191],v[92:95]\n"
"ds_read2st64_b32 v[192:193],v13 offset0:224 offset1:225\n"
"v_add_f32 v122,v122,v202\n"
MF "v[84:87],a[132:135],v[188:191],v[84:87]\n"
TR "v[180:181],v0 offset:53248\n"
"v_add_f32 v123,v123,v203\n"
MF "v[84:87],a[172:175],a[240:243],v[84:87]\n"
TR "v[182:183],v0 offset:55296\n"
"v_add_f32 v124,v124,v204\n"
MF "v[84:87],a[212:215],a[244:247],v[84:87]\n"
"ds_read2st64_b32 v[194:195],v13 offset0:226 offset1:227\n"
"v_add_f32 v125,v125,v205\n"
MF "v[76:79],a[208:211],a[244:247],v[76:79]\n"
TR "v[184:185],v0 offset:57344\n"
"v_add_f32 v126,v126,v206\n"
MF "v[76:79],a[168:171],a[240:243],v[76:79]\n"
TR "v[186:187],v0 offset:59392\n"
"v_add_f32 v127,v127,v207\n"
MF "v[76:79],a[128:131],v[188:191],v[76:79]\n"
"ds_read2st64_b32 v[196:197],v13 offset0:228 offset1:229\n"
CV "v120,v120,v121\n"
MF "v[68:71],a[124:127],v[188:191],v[68:71]\n"
"ds_read2st64_b32 v[198:199],v13 offset0:230 offset1:231\n"
CV "v121,v122,v123\n"
MF "v[68:71],a[164:167],a[240:243],v[68:71]\n"
"s_add_u32 s87,s90,3072\n"
CV "v122,v124,v125\n"
CV "v123,v126,v127\n"
MF "v[68:71],a[204:207],a[244:247],v[68:71]\n"
MF "v[60:63],a[200:203],a[244:247],v[60:63]\n"
"buffer_store_dwordx4 v[120:123],v9,s[40:43],s87 offen sc1\n"
MF "v[60:63],a[160:163],a[240:243],v[60:63]\n"
MF "v[60:63],a[120:123],v[188:191],v[60:63]\n"
"s_waitcnt lgkmcnt(2)\n"
MF "v[16:19],a[20:23],v[176:179],v[16:19]\n"
"s_waitcnt lgkmcnt(0)\n"
TR "v[188:189],v1 offset:49152\n"
"v_add_f32 v128,v128,v192\n"
MF "v[16:19],a[60:63],v[180:183],v[16:19]\n"
TR "v[190:191],v1 offset:51200\n"
"v_add_f32 v129,v129,v193\n"
MF "v[16:19],a[100:103],v[184:187],v[16:19]\n"
"ds_read2st64_b32 v[200:201],v13 offset0:0 offset1:1\n"
"v_add_f32 v130,v130,v194\n"
MF "v[24:27],a[104:107],v[184:187],v[24:27]\n"
TR "a[240:241],v1 offset:53248\n"
"v_add_f32 v131,v131,v195\n"
MF "v[24:27],a[64:67],v[180:183],v[24:27]\n"
TR "a[242:243],v1 offset:55296\n"
"v_add_f32 v132,v132,v196\n"
MF "v[24:27],a[24:27],v[176:179],v[24:27]\n"
"ds_read2st64_b32 v[202:203],v13 offset0:2 offset1:3\n"
"v_add_f32 v133,v133,v197\n"
MF "v[32:35],a[28:31],v[176:179],v[32:35]\n"
TR "a[244:245],v1 offset:57344\n"
"v_add_f32 v134,v134,v198\n"
MF "v[32:35],a[68:71],v[180:183],v[32:35]\n"
TR "a[246:247],v1 offset:59392\n"
"v_add_f32 v135,v135,v199\n"
MF "v[32:35],a[108:111],v[184:187],v[32:35]\n"
"ds_read2st64_b32 v[204:205],v13 offset0:4 offset1:5\n"
CV "v128,v128,v129\n"
MF "v[40:43],a[112:115],v[184:187],v[40:43]\n"
"ds_read2st64_b32 v[206:207],v13 offset0:6 offset1:7\n"
CV "v129,v130,v131\n"
MF "v[40:43],a[72:75],v[180:183],v[40:43]\n"
"s_add_u32 s87,s90,4096\n"
CV "v130,v132,v133\n"
CV "v131,v134,v135\n"
MF "v[40:43],a[32:35],v[176:179],v[40:43]\n"
MF "v[48:51],a[36:39],v[176:179],v[48:51]\n"
"buffer_store_dwordx4 v[128:131],v9,s[40:43],s87 offen sc1\n"
MF "v[48:51],a[76:79],v[180:183],v[48:51]\n"
MF "v[48:51],a[116:119],v[184:187],v[48:51]\n"
"s_waitcnt lgkmcnt(2)\n"
MF "v[52:55],a[116:119],a[244:247],v[52:55]\n"
"s_waitcnt lgkmcnt(0)\n"
TR "v[176:177],v2 offset:49152\n"
"v_add_f32 v136,v136,v200\n"
MF "v[52:55],a[76:79],a[240:243],v[52:55]\n"
TR "v[178:179],v2 offset:51200\n"
"v_add_f32 v137,v137,v201\n"
MF "v[52:55],a[36:39],v[188:191],v[52:55]\n"
"ds_read2st64_b32 v[192:193],v13 offset0:8 offset1:9\n"
"v_add_f32 v138,v138,v202\n"
MF "v[44:47],a[32:35],v[188:191],v[44:47]\n"
TR "v[180:181],v2 offset:53248\n"
"v_add_f32 v139,v139,v203\n"
MF "v[44:47],a[72:75],a[240:243],v[44:47]\n"
TR "v[182:183],v2 offset:55296\n"
"v_add_f32 v140,v140,v204\n"
MF "v[44:47],a[112:115],a[244:247],v[44:47]\n"
"ds_read2st64_b32 v[194:195],v13 offset0:10 offset1:11\n"
"v_add_f32 v141,v141,v205\n"
MF "v[36:39],a[108:111],a[244:247],v[36:39]\n"
TR "v[184:185],v2 offset:57344\n"
"v_add_f32 v142,v142,v206\n"
MF "v[36:39],a[68:71],a[240:243],v[36:39]\n"
TR "v[186:187],v2 offset:59392\n"
"v_add_f32 v143,v143,v207\n"
MF "v[36:39],a[28:31],v[188:191],v[36:39]\n"
"ds_read2st64_b32 v[196:197],v13 offset0:12 offset1:13\n"
CV "v136,v136,v137\n"
MF "v[28:31],a[24:27],v[188:191],v[28:31]\n"
"ds_read2st64_b32 v[198:199],v13 offset0:14 offset1:15\n"
CV "v137,v138,v139\n"
MF "v[28:31],a[64:67],a[240:243],v[28:31]\n"
"s_add_u32 s87,s90,10240\n"
CV "v138,v140,v141\n"
CV "v139,v142,v143\n"
MF "v[28:31],a[104:107],a[244:247],v[28:31]\n"
MF "v[20:23],a[100:103],a[244:247],v[20:23]\n"
"buffer_store_dwordx4 v[136:139],v9,s[40:43],s87 offen sc1\n"
MF "v[20:23],a[60:63],a[240:243],v[20:23]\n"
"s_waitcnt vmcnt(7)\n"
MF "v[20:23],a[20:23],v[188:191],v[20:23]\n"
"v_mov_b32 v236,v228\n"
"v_mov_b32 v237,v229\n"
MF "v[56:59],a[252:255],v[208:211],v[56:59]\n"
MF "v[64:67],a[252:255],v[212:215],v[64:67]\n"
MF "v[72:75],a[252:255],v[216:219],v[72:75]\n"
MF "v[80:83],a[252:255],v[220:223],v[80:83]\n"
MF "v[88:91],a[252:255],v[224:227],v[88:91]\n"
MF "v[60:63],a[248:251],v[208:211],v[60:63]\n"
MF "v[68:71],a[248:251],v[212:215],v[68:71]\n"
MF "v[76:79],a[248:251],v[216:219],v[76:79]\n"
MF "v[84:87],a[248:251],v[220:223],v[84:87]\n"
MF "v[92:95],a[248:251],v[224:227],v[92:95]\n"
MF "v[248:251],v[230:233],a[250:253],v[248:251]\n"
MF "v[244:247],v[234:237],a[250:253],v[244:247]\n"
"s_waitcnt lgkmcnt(2)\n"
MF "v[56:59],a[20:23],v[176:179],v[56:59]\n"
"s_waitcnt lgkmcnt(0)\n"
TR "v[188:189],v3 offset:49152\n"
"v_add_f32 v144,v144,v192\n"
MF "v[56:59],a[60:63],v[180:183],v[56:59]\n"
TR "v[190:191],v3 offset:51200\n"
"v_add_f32 v145,v145,v193\n"
MF "v[56:59],a[100:103],v[184:187],v[56:59]\n"
"ds_read2st64_b32 v[200:201],v13 offset0:16 offset1:17\n"
"v_add_f32 v146,v146,v194\n"
MF "v[64:67],a[104:107],v[184:187],v[64:67]\n"
TR "a[240:241],v3 offset:53248\n"
"v_add_f32 v147,v147,v195\n"
MF "v[64:67],a[64:67],v[180:183],v[64:67]\n"
TR "a[242:243],v3 offset:55296\n"
"v_add_f32 v148,v148,v196\n"
MF "v[64:67],a[24:27],v[176:179],v[64:67]\n"
"ds_read2st64_b32 v[202:203],v13 offset0:18 offset1:19\n"
"v_add_f32 v149,v149,v197\n"
MF "v[72:75],a[28:31],v[176:179],v[72:75]\n"
TR "a[244:245],v3 offset:57344\n"
"v_add_f32 v150,v150,v198\n"
MF "v[72:75],a[68:71],v[180:183],v[72:75]\n"
TR "a[246:247],v3 offset:59392\n"
"v_add_f32 v151,v151,v199\n"
MF "v[72:75],a[108:111],v[184:187],v[72:75]\n"
"ds_read2st64_b32 v[204:205],v13 offset0:20 offset1:21\n"
CV "v144,v144,v145\n"
MF "v[80:83],a[112:115],v[184:187],v[80:83]\n"
"ds_read2st64_b32 v[206:207],v13 offset0:22 offset1:23\n"
CV "v145,v146,v147\n"
MF "v[80:83],a[72:75],v[180:183],v[80:83]\n"
"s_add_u32 s87,s90,11264\n"
CV "v146,v148,v149\n"
CV "v147,v150,v151\n"
MF "v[80:83],a[32:35],v[176:179],v[80:83]\n"
"v_sub_u32 v10,v10,v11\n"
MF "v[88:91],a[36:39],v[176:179],v[88:91]\n"
"buffer_store_dwordx4 v[144:147],v9,s[40:43],s87 offen sc1\n"
MF "v[88:91],a[76:79],v[180:183],v[88:91]\n"
"s_waitcnt lgkmcnt(0)\n"
MF "v[88:91],a[116:119],v[184:187],v[88:91]\n"
"s_barrier\n"
MF "v[92:95],a[116:119],a[244:247],v[92:95]\n"
TR "v[176:177],v4\n"
"v_add_f32 v152,v152,v200\n"
MF "v[92:95],a[76:79],a[240:243],v[92:95]\n"
TR "v[178:179],v4 offset:2048\n"
"v_add_f32 v153,v153,v201\n"
MF "v[92:95],a[36:39],v[188:191],v[92:95]\n"
"ds_read2st64_b32 v[192:193],v13 offset0:24 offset1:25\n"
"v_add_f32 v154,v154,v202\n"
MF "v[84:87],a[32:35],v[188:191],v[84:87]\n"
TR "v[180:181],v4 offset:4096\n"
"v_add_f32 v155,v155,v203\n"
MF "v[84:87],a[72:75],a[240:243],v[84:87]\n"
TR "v[182:183],v4 offset:6144\n"
"v_add_f32 v156,v156,v204\n"
MF "v[84:87],a[112:115],a[244:247],v[84:87]\n"
"ds_read2st64_b32 v[194:195],v13 offset0:26 offset1:27\n"
"v_add_f32 v157,v157,v205\n"
MF "v[76:79],a[108:111],a[244:247],v[76:79]\n"
TR "v[184:185],v4 offset:8192\n"
"v_add_f32 v158,v158,v206\n"
MF "v[76:79],a[68:71],a[240:243],v[76:79]\n"
TR "v[186:187],v4 offset:10240\n"
"v_add_f32 v159,v159,v207\n"
MF "v[76:79],a[28:31],v[188:191],v[76:79]\n"
"ds_read2st64_b32 v[196:197],v13 offset0:28 offset1:29\n"
CV "v152,v152,v153\n"
MF "v[68:71],a[24:27],v[188:191],v[68:71]\n"
"ds_read2st64_b32 v[198:199],v13 offset0:30 offset1:31\n"
CV "v153,v154,v155\n"
MF "v[68:71],a[64:67],a[240:243],v[68:71]\n"
"s_add_u32 s87,s90,12288\n"
CV "v154,v156,v157\n"
CV "v155,v158,v159\n"
MF "v[68:71],a[104:107],a[244:247],v[68:71]\n"
".Lnf8:\n"
"v_readlane_b32 s58,v10,s67\n"
"s_cmp_lt_u32 s58,s85\n"
"s_cbranch_scc1 .Lsp8\n"
".Lnb8:\n"
MF "v[60:63],a[100:103],a[244:247],v[60:63]\n"
"buffer_store_dwordx4 v[152:155],v9,s[40:43],s87 offen sc1\n"
MF "v[60:63],a[60:63],a[240:243],v[60:63]\n"
"s_add_u32 s70,s70,20480\n"
"s_add_u32 s2,s2,204800\n"
MF "v[60:63],a[20:23],v[188:191],v[60:63]\n"
"s_waitcnt lgkmcnt(2)\n"
LD "v[228:231],v9,s[40:43],s2 offen sc1\n"
MF "v[16:19],a[140:143],v[176:179],v[16:19]\n"
"s_waitcnt lgkmcnt(0)\n"
TR "v[188:189],v5\n"
"v_add_f32 v160,v160,v192\n"
MF "v[16:19],a[180:183],v[180:183],v[16:19]\n"
TR "v[190:191],v5 offset:2048\n"
"v_add_f32 v161,v161,v193\n"
MF "v[16:19],a[220:223],v[184:187],v[16:19]\n"
"ds_read2st64_b32 v[200:201],v13 offset0:32 offset1:33\n"
"v_add_f32 v162,v162,v194\n"
MF "v[24:27],a[224:227],v[184:187],v[24:27]\n"
TR "a[240:241],v5 offset:4096\n"
"v_add_f32 v163,v163,v195\n"
MF "v[24:27],a[184:187],v[180:183],v[24:27]\n"
TR "a[242:243],v5 offset:6144\n"
"v_add_f32 v164,v164,v196\n"
MF "v[24:27],a[144:147],v[176:179],v[24:27]\n"
"ds_read2st64_b32 v[202:203],v13 offset0:34 offset1:35\n"
"v_add_f32 v165,v165,v197\n"
MF "v[32:35],a[148:151],v[176:179],v[32:35]\n"
TR "a[244:245],v5 offset:8192\n"
"v_add_f32 v166,v166,v198\n"
MF "v[32:35],a[188:191],v[180:183],v[32:35]\n"
TR "a[246:247],v5 offset:10240\n"
"v_add_f32 v167,v167,v199\n"
MF "v[32:35],a[228:231],v[184:187],v[32:35]\n"
"ds_read2st64_b32 v[204:205],v13 offset0:36 offset1:37\n"
CV "v160,v160,v161\n"
MF "v[40:43],a[232:235],v[184:187],v[40:43]\n"
"s_add_u32 s2,s2,204800\n"
"ds_read2st64_b32 v[206:207],v13 offset0:38 offset1:39\n"
CV "v161,v162,v163\n"
MF "v[40:43],a[192:195],v[180:183],v[40:43]\n"
"s_add_u32 s87,s90,13312\n"
CV "v162,v164,v165\n"
CV "v163,v166,v167\n"
MF "v[40:43],a[152:155],v[176:179],v[40:43]\n"
LD "v[232:235],v9,s[40:43],s2 offen sc1\n"
MF "v[48:51],a[156:159],v[176:179],v[48:51]\n"
"buffer_store_dwordx4 v[160:163],v9,s[40:43],s87 offen sc1\n"
MF "v[48:51],a[196:199],v[180:183],v[48:51]\n"
MF "v[48:51],a[236:239],v[184:187],v[48:51]\n"
"s_waitcnt lgkmcnt(2)\n"
MF "v[52:55],a[236:239],a[244:247],v[52:55]\n"
"s_waitcnt lgkmcnt(0)\n"
TR "v[176:177],v6\n"
"v_add_f32 v168,v168,v200\n"
MF "v[52:55],a[196:199],a[240:243],v[52:55]\n"
TR "v[178:179],v6 offset:2048\n"
"v_add_f32 v169,v169,v201\n"
MF "v[52:55],a[156:159],v[188:191],v[52:55]\n"
"v_add_f32 v170,v170,v202\n"
"v_add_f32 v171,v171,v203\n"
MF "v[44:47],a[152:155],v[188:191],v[44:47]\n"
TR "v[180:181],v6 offset:4096\n"
"v_add_f32 v172,v172,v204\n"
MF "v[44:47],a[192:195],a[240:243],v[44:47]\n"
TR "v[182:183],v6 offset:6144\n"
"v_add_f32 v173,v173,v205\n"
MF "v[44:47],a[232:235],a[244:247],v[44:47]\n"
"v_add_f32 v174,v174,v206\n"
"v_add_f32 v175,v175,v207\n"
MF "v[36:39],a[228:231],a[244:247],v[36:39]\n"
TR "v[184:185],v6 offset:8192\n"
CV "v168,v168,v169\n"
MF "v[36:39],a[188:191],a[240:243],v[36:39]\n"
TR "v[186:187],v6 offset:10240\n"
CV "v169,v170,v171\n"
MF "v[36:39],a[148:151],v[188:191],v[36:39]\n"
"s_add_u32 s87,s90,14336\n"
CV "v170,v172,v173\n"
CV "v171,v174,v175\n"
MF "v[28:31],a[144:147],v[188:191],v[28:31]\n"
LD "v[208:211],v9,s[40:43],s82 offen sc1\n"
MF "v[28:31],a[184:187],a[240:243],v[28:31]\n"
MF "v[28:31],a[224:227],a[244:247],v[28:31]\n"
MF "v[20:23],a[220:223],a[244:247],v[20:23]\n"
"buffer_store_dwordx4 v[168:171],v9,s[40:43],s87 offen sc1\n"
MF "v[20:23],a[180:183],a[240:243],v[20:23]\n"
"s_mov_b32 s90,s91\n"
"s_add_u32 s91,s91,20480\n"
MF "v[20:23],a[140:143],v[188:191],v[20:23]\n"
"s_waitcnt lgkmcnt(0)\n"
MF "v[56:59],a[140:143],v[176:179],v[56:59]\n"
TR "v[188:189],v7\n"
MF "v[56:59],a[180:183],v[180:183],v[56:59]\n"
TR "v[190:191],v7 offset:2048\n"
MF "v[56:59],a[220:223],v[184:187],v[56:59]\n"
LD "v[212:215],v9,s[40:43],s82 offen offset:1024 sc1\n"
MF "v[64:67],a[224:227],v[184:187],v[64:67]\n"
TR "a[240:241],v7 offset:4096\n"
MF "v[64:67],a[184:187],v[180:183],v[64:67]\n"
TR "a[242:243],v7 offset:6144\n"
MF "v[64:67],a[144:147],v[176:179],v[64:67]\n"
MF "v[72:75],a[148:151],v[176:179],v[72:75]\n"
TR "a[244:245],v7 offset:8192\n"
MF "v[72:75],a[188:191],v[180:183],v[72:75]\n"
TR "a[246:247],v7 offset:10240\n"
MF "v[72:75],a[228:231],v[184:187],v[72:75]\n"
MF "v[80:83],a[232:235],v[184:187],v[80:83]\n"
MF "v[80:83],a[192:195],v[180:183],v[80:83]\n"
LD "v[216:219],v9,s[40:43],s82 offen offset:2048 sc1\n"
MF "v[80:83],a[152:155],v[176:179],v[80:83]\n"
MF "v[88:91],a[156:159],v[176:179],v[88:91]\n"
MF "v[88:91],a[196:199],v[180:183],v[88:91]\n"
"s_waitcnt lgkmcnt(0)\n"
MF "v[88:91],a[236:239],v[184:187],v[88:91]\n"
"s_barrier\n"
MF "v[92:95],a[236:239],a[244:247],v[92:95]\n"
TR "v[176:177],v0 offset:24576\n"
MF "v[92:95],a[196:199],a[240:243],v[92:95]\n"
"s_add_u32 s85,s85,1\n"
TR "v[178:179],v0 offset:26624\n"
MF "v[92:95],a[156:159],v[188:191],v[92:95]\n"
MF "v[84:87],a[152:155],v[188:191],v[84:87]\n"
TR "v[180:181],v0 offset:28672\n"
MF "v[84:87],a[192:195],a[240:243],v[84:87]\n"
TR "v[182:183],v0 offset:30720\n"
MF "v[84:87],a[232:235],a[244:247],v[84:87]\n"
LD "v[220:223],v9,s[40:43],s82 offen offset:3072 sc1\n"
MF "v[76:79],a[228:231],a[244:247],v[76:79]\n"
TR "v[184:185],v0 offset:32768\n"
MF "v[76:79],a[188:191],a[240:243],v[76:79]\n"
TR "v[186:187],v0 offset:34816\n"
MF "v[76:79],a[148:151],v[188:191],v[76:79]\n"
MF "v[68:71],a[144:147],v[188:191],v[68:71]\n"
MF "v[68:71],a[184:187],a[240:243],v[68:71]\n"
MF "v[68:71],a[224:227],a[244:247],v[68:71]\n"
MF "v[60:63],a[220:223],a[244:247],v[60:63]\n"
MF "v[60:63],a[180:183],a[240:243],v[60:63]\n"
MF "v[60:63],a[140:143],v[188:191],v[60:63]\n"
"s_waitcnt lgkmcnt(0)\n"
"s_cmp_lg_u32 s85,9\n"
"s_cbranch_scc1 .Lpl19\n"
".Lplast1:\n"
MF "v[96:99],v[176:179],a[0:3],v[252:255]\n"
"s_add_u32 m0,s79,73728\n"
TR "v[188:189],v1 offset:24576\n"
TR "v[190:191],v1 offset:26624\n"
MF "v[96:99],v[180:183],a[40:43],v[96:99]\n"
"s_nop 0\n"
TR "a[240:241],v1 offset:28672\n"
TR "a[242:243],v1 offset:30720\n"
MF "v[96:99],v[184:187],a[80:83],v[96:99]\n"
"s_add_u32 s82,s82,4096\n"
TR "a[244:245],v1 offset:32768\n"
TR "a[246:247],v1 offset:34816\n"
MF "v[104:107],v[184:187],a[84:87],v[252:255]\n"
LD "v[224:227],v9,s[40:43],s82 offen sc1\n"
MF "v[104:107],v[180:183],a[44:47],v[104:107]\n"
"s_nop 0\n"
"ds_write_addtid_b32 v56\n"
"ds_write_addtid_b32 v57 offset:256\n"
MF "v[104:107],v[176:179],a[4:7],v[104:107]\n"
"s_nop 0\n"
"ds_write_addtid_b32 v58 offset:512\n"
"ds_write_addtid_b32 v59 offset:768\n"
MF "v[112:115],v[176:179],a[8:11],v[252:255]\n"
"s_nop 0\n"
"ds_write_addtid_b32 v60 offset:1024\n"
"ds_write_addtid_b32 v61 offset:1280\n"
MF "v[112:115],v[180:183],a[48:51],v[112:115]\n"
"s_nop 0\n"
"ds_write_addtid_b32 v62 offset:1536\n"
"ds_write_addtid_b32 v63 offset:1792\n"
MF "v[112:115],v[184:187],a[88:91],v[112:115]\n"
"s_nop 0\n"
"ds_write_addtid_b32 v64 offset:2048\n"
"ds_write_addtid_b32 v65 offset:2304\n"
MF "v[120:123],v[184:187],a[92:95],v[252:255]\n"
"s_nop 0\n"
"ds_write_addtid_b32 v66 offset:2560\n"
"ds_write_addtid_b32 v67 offset:2816\n"
MF "v[120:123],v[180:183],a[52:55],v[120:123]\n"
"s_nop 0\n"
"ds_write_addtid_b32 v68 offset:3072\n"
"ds_write_addtid_b32 v69 offset:3328\n"
MF "v[120:123],v[176:179],a[12:15],v[120:123]\n"
"s_nop 0\n"
"ds_write_addtid_b32 v70 offset:3584\n"
"ds_write_addtid_b32 v71 offset:3840\n"
MF "v[128:131],v[176:179],a[16:19],v[252:255]\n"
"s_nop 0\n"
"ds_write_addtid_b32 v72 offset:4096\n"
"ds_write_addtid_b32 v73 offset:4352\n"
MF "v[128:131],v[180:183],a[56:59],v[128:131]\n"
"s_nop 0\n"
"ds_write_addtid_b32 v74 offset:4608\n"
"ds_write_addtid_b32 v75 offset:4864\n"
MF "v[128:131],v[184:187],a[96:99],v[128:131]\n"
"s_waitcnt lgkmcnt(15)\n"
"ds_write_addtid_b32 v76 offset:5120\n"
MF "v[132:135],a[244:247],a[96:99],v[252:255]\n"
"s_nop 0\n"
TR "v[176:177],v2 offset:24576\n"
TR "v[178:179],v2 offset:26624\n"
MF "v[132:135],a[240:243],a[56:59],v[132:135]\n"
"s_nop 0\n"
TR "v[180:181],v2 offset:28672\n"
TR "v[182:183],v2 offset:30720\n"
MF "v[132:135],v[188:191],a[16:19],v[132:135]\n"
"s_nop 0\n"
TR "v[184:185],v2 offset:32768\n"
TR "v[186:187],v2 offset:34816\n"
MF "v[124:127],v[188:191],a[12:15],v[252:255]\n"
"s_nop 0\n"
"ds_write_addtid_b32 v77 offset:5376\n"
"ds_write_addtid_b32 v78 offset:5632\n"
MF "v[124:127],a[240:243],a[52:55],v[124:127]\n"
"s_nop 0\n"
"ds_write_addtid_b32 v79 offset:5888\n"
"ds_write_addtid_b32 v80 offset:6144\n"
MF "v[124:127],a[244:247],a[92:95],v[124:127]\n"
"s_nop 0\n"
"ds_write_addtid_b32 v81 offset:6400\n"
"ds_write_addtid_b32 v82 offset:6656\n"
MF "v[116:119],a[244:247],a[88:91],v[252:255]\n"
"s_nop 0\n"
"ds_write_addtid_b32 v83 offset:6912\n"
"ds_write_addtid_b32 v84 offset:7168\n"
MF "v[116:119],a[240:243],a[48:51],v[116:119]\n"
"s_nop 0\n"
"ds_write_addtid_b32 v85 offset:7424\n"
"ds_write_addtid_b32 v86 offset:7680\n"
MF "v[116:119],v[188:191],a[8:11],v[116:119]\n"
"s_nop 0\n"
"ds_write_addtid_b32 v87 offset:7936\n"
"ds_write_addtid_b32 v88 offset:8192\n"
MF "v[108:111],v[188:191],a[4:7],v[252:255]\n"
"s_nop 0\n"
"ds_write_addtid_b32 v89 offset:8448\n"
"ds_write_addtid_b32 v90 offset:8704\n"
MF "v[108:111],a[240:243],a[44:47],v[108:111]\n"
"s_nop 0\n"
"ds_write_addtid_b32 v91 offset:8960\n"
"ds_write_addtid_b32 v92 offset:9216\n"
MF "v[108:111],a[244:247],a[84:87],v[108:111]\n"
"s_nop 0\n"
"ds_write_addtid_b32 v93 offset:9472\n"
"ds_write_addtid_b32 v94 offset:9728\n"
MF "v[100:103],a[244:247],a[80:83],v[252:255]\n"
"ds_write_addtid_b32 v95 offset:9984\n"
MF "v[100:103],a[240:243],a[40:43],v[100:103]\n"
MF "v[100:103],v[188:191],a[0:3],v[100:103]\n"
"s_waitcnt lgkmcnt(15)\n"
MF "v[136:139],v[176:179],a[0:3],v[252:255]\n"
"s_nop 0\n"
TR "v[188:189],v3 offset:24576\n"
TR "v[190:191],v3 offset:26624\n"
MF "v[136:139],v[180:183],a[40:43],v[136:139]\n"
"s_nop 0\n"
TR "a[240:241],v3 offset:28672\n"
TR "a[242:243],v3 offset:30720\n"
MF "v[136:139],v[184:187],a[80:83],v[136:139]\n"
"s_nop 0\n"
TR "a[244:245],v3 offset:32768\n"
TR "a[246:247],v3 offset:34816\n"
"s_waitcnt lgkmcnt(0)\n"
MF "v[144:147],v[184:187],a[84:87],v[252:255]\n"
"s_barrier\n"
MF "v[144:147],v[180:183],a[44:47],v[144:147]\n"
"ds_read2st64_b32 v[56:57],v13 offset0:192 offset1:193\n"
MF "v[144:147],v[176:179],a[4:7],v[144:147]\n"
"ds_read2st64_b32 v[58:59],v13 offset0:194 offset1:195\n"
MF "v[152:155],v[176:179],a[8:11],v[252:255]\n"
"ds_read2st64_b32 v[60:61],v13 offset0:196 offset1:197\n"
MF "v[152:155],v[180:183],a[48:51],v[152:155]\n"
"ds_read2st64_b32 v[62:63],v13 offset0:198 offset1:199\n"
MF "v[152:155],v[184:187],a[88:91],v[152:155]\n"
"ds_read2st64_b32 v[64:65],v13 offset0:200 offset1:201\n"
MF "v[160:163],v[184:187],a[92:95],v[252:255]\n"
"ds_read2st64_b32 v[66:67],v13 offset0:202 offset1:203\n"
MF "v[160:163],v[180:183],a[52:55],v[160:163]\n"
"ds_read2st64_b32 v[68:69],v13 offset0:204 offset1:205\n"
MF "v[160:163],v[176:179],a[12:15],v[160:163]\n"
"ds_read2st64_b32 v[70:71],v13 offset0:206 offset1:207\n"
MF "v[168:171],v[176:179],a[16:19],v[252:255]\n"
"s_waitcnt lgkmcnt(4)\n"
"ds_read2st64_b32 v[72:73],v13 offset0:208 offset1:209\n"
"v_add_f32 v16,v16,v56\n"
MF "v[168:171],v[180:183],a[56:59],v[168:171]\n"
"ds_read2st64_b32 v[74:75],v13 offset0:210 offset1:211\n"
"s_waitcnt lgkmcnt(0)\n"
MF "v[168:171],v[184:187],a[96:99],v[168:171]\n"
"s_barrier\n"
MF "v[172:175],a[244:247],a[96:99],v[252:255]\n"
"s_nop 0\n"
TR "v[176:177],v0\n"
TR "v[178:179],v0 offset:2048\n"
MF "v[172:175],a[240:243],a[56:59],v[172:175]\n"
"s_nop 0\n"
TR "v[180:181],v0 offset:4096\n"
TR "v[182:183],v0 offset:6144\n"
MF "v[172:175],v[188:191],a[16:19],v[172:175]\n"
"s_nop 0\n"
TR "v[184:185],v0 offset:8192\n"
TR "v[186:187],v0 offset:10240\n"
MF "v[164:167],v[188:191],a[12:15],v[252:255]\n"
"ds_read2st64_b32 v[76:77],v13 offset0:212 offset1:213\n"
"v_add_f32 v17,v17,v57\n"
MF "v[164:167],a[240:243],a[52:55],v[164:167]\n"
"ds_read2st64_b32 v[78:79],v13 offset0:214 offset1:215\n"
"v_add_f32 v18,v18,v58\n"
MF "v[164:167],a[244:247],a[92:95],v[164:167]\n"
"ds_read2st64_b32 v[80:81],v13 offset0:216 offset1:217\n"
"v_add_f32 v19,v19,v59\n"
MF "v[156:159],a[244:247],a[88:91],v[252:255]\n"
"ds_read2st64_b32 v[82:83],v13 offset0:218 offset1:219\n"
"v_add_f32 v20,v20,v60\n"
MF "v[156:159],a[240:243],a[48:51],v[156:159]\n"
"ds_read2st64_b32 v[84:85],v13 offset0:220 offset1:221\n"
"v_add_f32 v21,v21,v61\n"
MF "v[156:159],v[188:191],a[8:11],v[156:159]\n"
"ds_read2st64_b32 v[86:87],v13 offset0:222 offset1:223\n"
"v_add_f32 v22,v22,v62\n"
MF "v[148:151],v[188:191],a[4:7],v[252:255]\n"
"s_waitcnt vmcnt(4)\n"
"v_mov_b32 v14,s76\n"
"s_add_u32 s76,s76,1\n"
MF "v[148:151],a[240:243],a[44:47],v[148:151]\n"
"buffer_store_dword v14,v15,s[40:43],s94 offen sc1\n"
MF "v[148:151],a[244:247],a[84:87],v[148:151]\n"
"ds_read2st64_b32 v[88:89],v13 offset0:224 offset1:225\n"
"v_add_f32 v23,v23,v63\n"
MF "v[140:143],a[244:247],a[80:83],v[252:255]\n"
"ds_read2st64_b32 v[90:91],v13 offset0:226 offset1:227\n"
CV "v16,v16,v17\n"
MF "v[140:143],a[240:243],a[40:43],v[140:143]\n"
"ds_read2st64_b32 v[92:93],v13 offset0:228 offset1:229\n"
CV "v17,v18,v19\n"
MF "v[140:143],v[188:191],a[0:3],v[140:143]\n"
".Lpg37h:\n"
"s_cbranch_execz .Lisr4\n"
"s_waitcnt lgkmcnt(9)\n"
"ds_read2st64_b32 v[94:95],v13 offset0:230 offset1:231\n"
CV "v18,v20,v21\n"
MF "v[96:99],v[176:179],a[120:123],v[96:99]\n"
"s_nop 0\n"
TR "v[188:189],v1\n"
TR "v[190:191],v1 offset:2048\n"
MF "v[96:99],v[180:183],a[160:163],v[96:99]\n"
"s_nop 0\n"
TR "a[240:241],v1 offset:4096\n"
TR "a[242:243],v1 offset:6144\n"
MF "v[96:99],v[184:187],a[200:203],v[96:99]\n"
"s_nop 0\n"
TR "a[244:245],v1 offset:8192\n"
TR "a[246:247],v1 offset:10240\n"
MF "v[104:107],v[184:187],a[204:207],v[104:107]\n"
"s_add_u32 s87,s90,0\n"
CV "v19,v22,v23\n"
MF "v[104:107],v[180:183],a[164:167],v[104:107]\n"
"buffer_store_dwordx4 v[16:19],v9,s[40:43],s87 offen sc1\n"
MF "v[104:107],v[176:179],a[124:127],v[104:107]\n"
"v_add_f32 v24,v24,v64\n"
"v_add_f32 v25,v25,v65\n"
MF "v[112:115],v[176:179],a[128:131],v[112:115]\n"
"v_add_f32 v26,v26,v66\n"
"v_add_f32 v27,v27,v67\n"
MF "v[112:115],v[180:183],a[168:171],v[112:115]\n"
"v_add_f32 v28,v28,v68\n"
"v_add_f32 v29,v29,v69\n"
MF "v[112:115],v[184:187],a[208:211],v[112:115]\n"
"v_add_f32 v30,v30,v70\n"
"v_add_f32 v31,v31,v71\n"
MF "v[120:123],v[184:187],a[212:215],v[120:123]\n"
CV "v24,v24,v25\n"
CV "v25,v26,v27\n"
MF "v[120:123],v[180:183],a[172:175],v[120:123]\n"
"s_add_u32 s87,s90,1024\n"
CV "v26,v28,v29\n"
CV "v27,v30,v31\n"
MF "v[120:123],v[176:179],a[132:135],v[120:123]\n"
"s_waitcnt lgkmcnt(14)\n"
"buffer_store_dwordx4 v[24:27],v9,s[40:43],s87 offen sc1\n"
MF "v[128:131],v[176:179],a[136:139],v[128:131]\n"
"v_add_f32 v32,v32,v72\n"
"v_add_f32 v33,v33,v73\n"
MF "v[128:131],v[180:183],a[176:179],v[128:131]\n"
"v_add_f32 v34,v34,v74\n"
"v_add_f32 v35,v35,v75\n"
MF "v[128:131],v[184:187],a[216:219],v[128:131]\n"
"s_waitcnt lgkmcnt(0)\n"
"v_add_f32 v36,v36,v76\n"
"v_add_f32 v37,v37,v77\n"
MF "v[132:135],a[244:247],a[216:219],v[132:135]\n"
"s_nop 0\n"
TR "v[176:177],v2\n"
TR "v[178:179],v2 offset:2048\n"
MF "v[132:135],a[240:243],a[176:179],v[132:135]\n"
"s_nop 0\n"
TR "v[180:181],v2 offset:4096\n"
TR "v[182:183],v2 offset:6144\n"
MF "v[132:135],v[188:191],a[136:139],v[132:135]\n"
"s_nop 0\n"
TR "v[184:185],v2 offset:8192\n"
TR "v[186:187],v2 offset:10240\n"
MF "v[124:127],v[188:191],a[132:135],v[124:127]\n"
"v_add_f32 v38,v38,v78\n"
"v_add_f32 v39,v39,v79\n"
MF "v[124:127],a[240:243],a[172:175],v[124:127]\n"
CV "v32,v32,v33\n"
CV "v33,v34,v35\n"
MF "v[124:127],a[244:247],a[212:215],v[124:127]\n"
"s_add_u32 s87,s90,2048\n"
CV "v34,v36,v37\n"
CV "v35,v38,v39\n"
MF "v[116:119],a[244:247],a[208:211],v[116:119]\n"
"buffer_store_dwordx4 v[32:35],v9,s[40:43],s87 offen sc1\n"
MF "v[116:119],a[240:243],a[168:171],v[116:119]\n"
"v_add_f32 v40,v40,v80\n"
"v_add_f32 v41,v41,v81\n"
MF "v[116:119],v[188:191],a[128:131],v[116:119]\n"
"v_add_f32 v42,v42,v82\n"
"v_add_f32 v43,v43,v83\n"
MF "v[108:111],v[188:191],a[124:127],v[108:111]\n"
"v_add_f32 v44,v44,v84\n"
"v_add_f32 v45,v45,v85\n"
MF "v[108:111],a[240:243],a[164:167],v[108:111]\n"
"v_add_f32 v46,v46,v86\n"
"v_add_f32 v47,v47,v87\n"
MF "v[108:111],a[244:247],a[204:207],v[108:111]\n"
CV "v40,v40,v41\n"
CV "v41,v42,v43\n"
MF "v[100:103],a[244:247],a[200:203],v[100:103]\n"
"s_add_u32 s87,s90,3072\n"
CV "v42,v44,v45\n"
CV "v43,v46,v47\n"
MF "v[100:103],a[240:243],a[160:163],v[100:103]\n"
"buffer_store_dwordx4 v[40:43],v9,s[40:43],s87 offen sc1\n"
MF "v[100:103],v[188:191],a[120:123],v[100:103]\n"
"s_waitcnt lgkmcnt(0)\n"
"v_add_f32 v48,v48,v88\n"
"v_add_f32 v49,v49,v89\n"
MF "v[136:139],v[176:179],a[120:123],v[136:139]\n"
"s_nop 0\n"
TR "v[188:189],v3\n"
TR "v[190:191],v3 offset:2048\n"
MF "v[136:139],v[180:183],a[160:163],v[136:139]\n"
"s_nop 0\n"
TR "a[240:241],v3 offset:4096\n"
TR "a[242:243],v3 offset:6144\n"
MF "v[136:139],v[184:187],a[200:203],v[136:139]\n"
"s_nop 0\n"
TR "a[244:245],v3 offset:8192\n"
TR "a[246:247],v3 offset:10240\n"
MF "v[144:147],v[184:187],a[204:207],v[144:147]\n"
"v_add_f32 v50,v50,v90\n"
"v_add_f32 v51,v51,v91\n"
MF "v[144:147],v[180:183],a[164:167],v[144:147]\n"
"v_add_f32 v52,v52,v92\n"
"v_add_f32 v53,v53,v93\n"
MF "v[144:147],v[176:179],a[124:127],v[144:147]\n"
"v_add_f32 v54,v54,v94\n"
"v_add_f32 v55,v55,v95\n"
MF "v[152:155],v[176:179],a[128:131],v[152:155]\n"
CV "v48,v48,v49\n"
CV "v49,v50,v51\n"
MF "v[152:155],v[180:183],a[168:171],v[152:155]\n"
"s_add_u32 s87,s90,4096\n"
CV "v50,v52,v53\n"
CV "v51,v54,v55\n"
MF "v[152:155],v[184:187],a[208:211],v[152:155]\n"
"buffer_store_dwordx4 v[48:51],v9,s[40:43],s87 offen sc1\n"
MF "v[160:163],v[184:187],a[212:215],v[160:163]\n"
MF "v[160:163],v[180:183],a[172:175],v[160:163]\n"
MF "v[160:163],v[176:179],a[132:135],v[160:163]\n"
MF "v[168:171],v[176:179],a[136:139],v[168:171]\n"
MF "v[168:171],v[180:183],a[176:179],v[168:171]\n"
"s_waitcnt lgkmcnt(0)\n"
MF "v[168:171],v[184:187],a[216:219],v[168:171]\n"
"s_barrier\n"
MF "v[172:175],a[244:247],a[216:219],v[172:175]\n"
"s_nop 0\n"
TR "v[176:177],v0 offset:49152\n"
TR "v[178:179],v0 offset:51200\n"
MF "v[172:175],a[240:243],a[176:179],v[172:175]\n"
"s_nop 0\n"
TR "v[180:181],v0 offset:53248\n"
TR "v[182:183],v0 offset:55296\n"
MF "v[172:175],v[188:191],a[136:139],v[172:175]\n"
"s_nop 0\n"
TR "v[184:185],v0 offset:57344\n"
TR "v[186:187],v0 offset:59392\n"
MF "v[164:167],v[188:191],a[132:135],v[164:167]\n"
"buffer_load_dword v10,v12,s[40:43],s86 offen sc1\n"
MF "v[164:167],a[240:243],a[172:175],v[164:167]\n"
MF "v[164:167],a[244:247],a[212:215],v[164:167]\n"
LD "a[0:3],v9,s[40:43],s3 offen sc1\n"
MF "v[156:159],a[244:247],a[208:211],v[156:159]\n"
MF "v[156:159],a[240:243],a[168:171],v[156:159]\n"
MF "v[156:159],v[188:191],a[128:131],v[156:159]\n"
MF "v[148:151],v[188:191],a[124:127],v[148:151]\n"
MF "v[148:151],a[240:243],a[164:167],v[148:151]\n"
"s_add_u32 s3,s3,204800\n"
MF "v[148:151],a[244:247],a[204:207],v[148:151]\n"
LD "a[4:7],v9,s[40:43],s3 offen sc1\n"
MF "v[140:143],a[244:247],a[200:203],v[140:143]\n"
MF "v[140:143],a[240:243],a[160:163],v[140:143]\n"
MF "v[140:143],v[188:191],a[120:123],v[140:143]\n"
"s_waitcnt lgkmcnt(0)\n"
MF "v[96:99],v[176:179],a[20:23],v[96:99]\n"
"s_nop 0\n"
TR "v[188:189],v1 offset:49152\n"
TR "v[190:191],v1 offset:51200\n"
MF "v[96:99],v[180:183],a[60:63],v[96:99]\n"
"s_nop 0\n"
TR "a[240:241],v1 offset:53248\n"
TR "a[242:243],v1 offset:55296\n"
MF "v[96:99],v[184:187],a[100:103],v[96:99]\n"
"s_add_u32 s3,s3,204800\n"
TR "a[244:245],v1 offset:57344\n"
TR "a[246:247],v1 offset:59392\n"
MF "v[104:107],v[184:187],a[104:107],v[104:107]\n"
LD "a[8:11],v9,s[40:43],s3 offen sc1\n"
MF "v[104:107],v[180:183],a[64:67],v[104:107]\n"
MF "v[104:107],v[176:179],a[24:27],v[104:107]\n"
"s_waitcnt vmcnt(10)\n"
"v_mov_b32 v236,v228\n"
"v_mov_b32 v237,v229\n"
MF "v[136:139],v[208:211],a[252:255],v[136:139]\n"
MF "v[144:147],v[212:215],a[252:255],v[144:147]\n"
MF "v[152:155],v[216:219],a[252:255],v[152:155]\n"
MF "v[160:163],v[220:223],a[252:255],v[160:163]\n"
MF "v[168:171],v[224:227],a[252:255],v[168:171]\n"
MF "v[140:143],v[208:211],a[248:251],v[140:143]\n"
MF "v[148:151],v[212:215],a[248:251],v[148:151]\n"
MF "v[156:159],v[216:219],a[248:251],v[156:159]\n"
MF "v[164:167],v[220:223],a[248:251],v[164:167]\n"
MF "v[172:175],v[224:227],a[248:251],v[172:175]\n"
MF "v[248:251],v[230:233],a[250:253],v[248:251]\n"
MF "v[244:247],v[234:237],a[250:253],v[244:247]\n"
MF "v[112:115],v[176:179],a[28:31],v[112:115]\n"
MF "v[112:115],v[180:183],a[68:71],v[112:115]\n"
"s_add_u32 s3,s3,204800\n"
MF "v[112:115],v[184:187],a[108:111],v[112:115]\n"
LD "a[12:15],v9,s[40:43],s3 offen sc1\n"
MF "v[120:123],v[184:187],a[112:115],v[120:123]\n"
"s_nop 1\n"
CV "v244,v244,v245\n"
CV "v245,v246,v247\n"
MF "v[120:123],v[180:183],a[72:75],v[120:123]\n"
"s_bfe_u32 s87,s22,0x30014\n"
CV "v246,v248,v249\n"
CV "v247,v250,v251\n"
MF "v[120:123],v[176:179],a[32:35],v[120:123]\n"
"s_lshl_b32 s87,s87,7\n"
"v_permlane16_swap_b32 v244,v246\n"
"v_permlane16_swap_b32 v245,v247\n"
MF "v[128:131],v[176:179],a[36:39],v[128:131]\n"
"buffer_store_dwordx4 v[244:247],v8,s[44:47],s0 offen sc1\n"
MF "v[128:131],v[180:183],a[76:79],v[128:131]\n"
"s_add_u32 s3,s3,204800\n"
MF "v[128:131],v[184:187],a[116:119],v[128:131]\n"
"s_waitcnt lgkmcnt(0)\n"
LD "a[40:43],v9,s[40:43],s3 offen sc1\n"
MF "v[132:135],a[244:247],a[116:119],v[132:135]\n"
"s_and_b32 s60,s56,1\n"
TR "v[176:177],v2 offset:49152\n"
TR "v[178:179],v2 offset:51200\n"
MF "v[132:135],a[240:243],a[76:79],v[132:135]\n"
"s_mul_i32 s60,s60,204800\n"
TR "v[180:181],v2 offset:53248\n"
TR "v[182:183],v2 offset:55296\n"
MF "v[132:135],v[188:191],a[36:39],v[132:135]\n"
"s_add_u32 s87,s87,s60\n"
TR "v[184:185],v2 offset:57344\n"
TR "v[186:187],v2 offset:59392\n"
MF "v[124:127],v[188:191],a[32:35],v[124:127]\n"
"s_mov_b32 s71,204800\n"
MF "v[124:127],a[240:243],a[72:75],v[124:127]\n"
"s_mov_b32 s66,204800\n"
MF "v[124:127],a[244:247],a[112:115],v[124:127]\n"
"s_add_u32 s72,s78,2\n"
MF "v[116:119],a[244:247],a[108:111],v[116:119]\n"
"s_add_u32 s3,s3,204800\n"
MF "v[116:119],a[240:243],a[68:71],v[116:119]\n"
LD "a[44:47],v9,s[40:43],s3 offen sc1\n"
MF "v[116:119],v[188:191],a[28:31],v[116:119]\n"
"s_and_b32 s72,s72,15\n"
MF "v[108:111],v[188:191],a[24:27],v[108:111]\n"
"s_add_u32 s16,s78,6\n"
MF "v[108:111],a[240:243],a[64:67],v[108:111]\n"
"s_and_b32 s16,s16,15\n"
MF "v[108:111],a[244:247],a[104:107],v[108:111]\n"
"s_add_u32 s17,s78,4\n"
MF "v[100:103],a[244:247],a[100:103],v[100:103]\n"
"s_and_b32 s17,s17,15\n"
MF "v[100:103],a[240:243],a[60:63],v[100:103]\n"
"s_add_u32 s3,s3,204800\n"
MF "v[100:103],v[188:191],a[20:23],v[100:103]\n"
"s_waitcnt lgkmcnt(0)\n"
LD "a[48:51],v9,s[40:43],s3 offen sc1\n"
MF "v[136:139],v[176:179],a[20:23],v[136:139]\n"
"s_bfe_u32 s60,s22,0x10014\n"
TR "v[188:189],v3 offset:49152\n"
TR "v[190:191],v3 offset:51200\n"
MF "v[136:139],v[180:183],a[60:63],v[136:139]\n"
"s_lshl_b32 s60,s60,5\n"
TR "a[240:241],v3 offset:53248\n"
TR "a[242:243],v3 offset:55296\n"
MF "v[136:139],v[184:187],a[100:103],v[136:139]\n"
"s_nop 0\n"
TR "a[244:245],v3 offset:57344\n"
TR "a[246:247],v3 offset:59392\n"
MF "v[144:147],v[184:187],a[104:107],v[144:147]\n"
"v_xor_b32 v242,s60,v8\n"
MF "v[144:147],v[180:183],a[64:67],v[144:147]\n"
"s_add_u32 s3,s3,204800\n"
"v_mov_b32 v14,s76\n"
"s_add_u32 s76,s76,1\n"
MF "v[144:147],v[176:179],a[24:27],v[144:147]\n"
LD "a[52:55],v9,s[40:43],s3 offen sc1\n"
MF "v[152:155],v[176:179],a[28:31],v[152:155]\n"
MF "v[152:155],v[180:183],a[68:71],v[152:155]\n"
MF "v[152:155],v[184:187],a[108:111],v[152:155]\n"
MF "v[160:163],v[184:187],a[112:115],v[160:163]\n"
MF "v[160:163],v[180:183],a[72:75],v[160:163]\n"
MF "v[160:163],v[176:179],a[32:35],v[160:163]\n"
MF "v[168:171],v[176:179],a[36:39],v[168:171]\n"
MF "v[168:171],v[180:183],a[76:79],v[168:171]\n"
"s_waitcnt vmcnt(9) lgkmcnt(0)\n"
MF "v[168:171],v[184:187],a[116:119],v[168:171]\n"
"s_barrier\n"
MF "v[172:175],a[244:247],a[116:119],v[172:175]\n"
"buffer_store_dword v14,v15,s[40:43],s94 offen sc1\n"
MF "v[172:175],a[240:243],a[76:79],v[172:175]\n"
"s_nop 0\n"
TR "v[176:177],v6\n"
TR "v[178:179],v6 offset:2048\n"
MF "v[172:175],v[188:191],a[36:39],v[172:175]\n"
"s_nop 0\n"
TR "v[180:181],v6 offset:4096\n"
TR "v[182:183],v6 offset:6144\n"
MF "v[164:167],v[188:191],a[32:35],v[164:167]\n"
"s_nop 0\n"
TR "v[184:185],v6 offset:8192\n"
TR "v[186:187],v6 offset:10240\n"
MF "v[164:167],a[240:243],a[72:75],v[164:167]\n"
"v_sub_u32 v10,v10,v11\n"
MF "v[164:167],a[244:247],a[112:115],v[164:167]\n"
"v_readlane_b32 s16,v10,s16\n"
MF "v[156:159],a[244:247],a[108:111],v[156:159]\n"
"s_cmp_lt_u32 s16,9\n"
"s_cselect_b32 s16,0xf0000000,0\n"
MF "v[156:159],a[240:243],a[68:71],v[156:159]\n"
"v_readlane_b32 s17,v10,s17\n"
MF "v[156:159],v[188:191],a[28:31],v[156:159]\n"
"s_cmp_lt_u32 s17,10\n"
"s_cselect_b32 s17,0xf0000000,0\n"
MF "v[148:151],v[188:191],a[24:27],v[148:151]\n"
"s_bfe_u32 s62,s22,0x30014\n"
MF "v[148:151],a[240:243],a[64:67],v[148:151]\n"
"s_add_u32 s62,s62,3\n"
"s_and_b32 s62,s62,7\n"
"s_mul_i32 s62,s62,204800\n"
MF "v[148:151],a[244:247],a[104:107],v[148:151]\n"
"s_add_u32 s62,s62,s73\n"
"s_add_u32 s62,s62,122880\n"
"s_or_b32 s62,s62,s16\n"
MF "v[140:143],a[244:247],a[100:103],v[140:143]\n"
LD "v[56:59],v9,s[40:43],s62 offen sc1\n"
MF "v[140:143],a[240:243],a[60:63],v[140:143]\n"
LD "v[64:67],v9,s[40:43],s62 offen offset:1024 sc1\n"
MF "v[140:143],v[188:191],a[20:23],v[140:143]\n"
"s_waitcnt lgkmcnt(0)\n"
LD "v[72:75],v9,s[40:43],s62 offen offset:2048 sc1\n"
MF "v[136:139],v[176:179],a[140:143],v[136:139]\n"
"s_nop 0\n"
TR "v[188:189],v7\n"
TR "v[190:191],v7 offset:2048\n"
MF "v[136:139],v[180:183],a[180:183],v[136:139]\n"
"s_nop 0\n"
TR "a[240:241],v7 offset:4096\n"
TR "a[242:243],v7 offset:6144\n"
MF "v[136:139],v[184:187],a[220:223],v[136:139]\n"
"s_nop 0\n"
TR "a[244:245],v7 offset:8192\n"
TR "a[246:247],v7 offset:10240\n"
MF "v[144:147],v[184:187],a[224:227],v[144:147]\n"
LD "v[80:83],v9,s[40:43],s62 offen offset:3072 sc1\n"
MF "v[144:147],v[180:183],a[184:187],v[144:147]\n"
MF "v[144:147],v[176:179],a[144:147],v[144:147]\n"
"s_add_u32 s62,s62,4096\n"
MF "v[152:155],v[176:179],a[148:151],v[152:155]\n"
LD "v[88:91],v9,s[40:43],s62 offen sc1\n"
MF "v[152:155],v[180:183],a[188:191],v[152:155]\n"
"s_bfe_u32 s62,s22,0x30014\n"
"ds_write_addtid_b32 v136\n"
"ds_write_addtid_b32 v137 offset:256\n"
MF "v[152:155],v[184:187],a[228:231],v[152:155]\n"
"s_add_u32 s62,s62,2\n"
"s_and_b32 s62,s62,7\n"
"s_mul_i32 s62,s62,204800\n"
MF "v[160:163],v[184:187],a[232:235],v[160:163]\n"
"s_add_u32 s62,s62,s73\n"
"s_add_u32 s62,s62,143360\n"
"s_or_b32 s62,s62,s17\n"
MF "v[160:163],v[180:183],a[192:195],v[160:163]\n"
LD "v[60:63],v9,s[40:43],s62 offen sc1\n"
MF "v[160:163],v[176:179],a[152:155],v[160:163]\n"
"s_nop 0\n"
"ds_write_addtid_b32 v138 offset:512\n"
"ds_write_addtid_b32 v139 offset:768\n"
MF "v[168:171],v[176:179],a[156:159],v[168:171]\n"
LD "v[68:71],v9,s[40:43],s62 offen offset:1024 sc1\n"
MF "v[168:171],v[180:183],a[196:199],v[168:171]\n"
"s_nop 0\n"
"ds_write_addtid_b32 v144 offset:2048\n"
"ds_write_addtid_b32 v145 offset:2304\n"
MF "v[168:171],v[184:187],a[236:239],v[168:171]\n"
"s_waitcnt lgkmcnt(6)\n"
LD "v[76:79],v9,s[40:43],s62 offen offset:2048 sc1\n"
MF "v[140:143],v[188:191],a[140:143],v[140:143]\n"
"s_nop 0\n"
TR "v[176:177],v4\n"
TR "v[178:179],v4 offset:2048\n"
MF "v[140:143],a[240:243],a[180:183],v[140:143]\n"
"s_nop 0\n"
TR "v[180:181],v4 offset:4096\n"
TR "v[182:183],v4 offset:6144\n"
MF "v[140:143],a[244:247],a[220:223],v[140:143]\n"
"s_nop 0\n"
TR "v[184:185],v4 offset:8192\n"
TR "v[186:187],v4 offset:10240\n"
MF "v[148:151],a[244:247],a[224:227],v[148:151]\n"
LD "v[84:87],v9,s[40:43],s62 offen offset:3072 sc1\n"
MF "v[148:151],a[240:243],a[184:187],v[148:151]\n"
"s_nop 0\n"
"ds_write_addtid_b32 v146 offset:2560\n"
"ds_write_addtid_b32 v147 offset:2816\n"
MF "v[148:151],v[188:191],a[144:147],v[148:151]\n"
"s_add_u32 s62,s62,4096\n"
"ds_write_addtid_b32 v152 offset:4096\n"
"ds_write_addtid_b32 v153 offset:4352\n"
MF "v[156:159],v[188:191],a[148:151],v[156:159]\n"
LD "v[92:95],v9,s[40:43],s62 offen sc1\n"
MF "v[156:159],a[240:243],a[188:191],v[156:159]\n"
"s_nop 0\n"
"ds_write_addtid_b32 v154 offset:4608\n"
"ds_write_addtid_b32 v155 offset:4864\n"
MF "v[156:159],a[244:247],a[228:231],v[156:159]\n"
"s_nop 0\n"
"ds_write_addtid_b32 v160 offset:6144\n"
"ds_write_addtid_b32 v161 offset:6400\n"
MF "v[164:167],a[244:247],a[232:235],v[164:167]\n"
"s_nop 0\n"
"ds_write_addtid_b32 v162 offset:6656\n"
"ds_write_addtid_b32 v163 offset:6912\n"
MF "v[164:167],a[240:243],a[192:195],v[164:167]\n"
"s_nop 0\n"
"ds_write_addtid_b32 v168 offset:8192\n"
"ds_write_addtid_b32 v169 offset:8448\n"
MF "v[164:167],v[188:191],a[152:155],v[164:167]\n"
"buffer_load_dword v10,v12,s[40:43],s86 offen sc1\n"
MF "v[172:175],v[188:191],a[156:159],v[172:175]\n"
"s_nop 0\n"
"ds_write_addtid_b32 v170 offset:8704\n"
"ds_write_addtid_b32 v171 offset:8960\n"
MF "v[172:175],a[240:243],a[196:199],v[172:175]\n"
"s_nop 0\n"
"ds_write_addtid_b32 v140 offset:1024\n"
"ds_write_addtid_b32 v141 offset:1280\n"
MF "v[172:175],a[244:247],a[236:239],v[172:175]\n"
"s_waitcnt lgkmcnt(15)\n"
"ds_write_addtid_b32 v142 offset:1536\n"
"ds_write_addtid_b32 v143 offset:1792\n"
MF "v[96:99],v[176:179],a[140:143],v[96:99]\n"
"s_nop 0\n"
TR "v[188:189],v5\n"
TR "v[190:191],v5 offset:2048\n"
MF "v[96:99],v[180:183],a[180:183],v[96:99]\n"
"s_nop 0\n"
TR "a[240:241],v5 offset:4096\n"
TR "a[242:243],v5 offset:6144\n"
MF "v[96:99],v[184:187],a[220:223],v[96:99]\n"
"s_nop 0\n"
TR "a[244:245],v5 offset:8192\n"
TR "a[246:247],v5 offset:10240\n"
MF "v[104:107],v[184:187],a[224:227],v[104:107]\n"
"s_nop 0\n"
"ds_write_addtid_b32 v148 offset:3072\n"
"ds_write_addtid_b32 v149 offset:3328\n"
MF "v[104:107],v[180:183],a[184:187],v[104:107]\n"
"s_nop 0\n"
"ds_write_addtid_b32 v150 offset:3584\n"
"ds_write_addtid_b32 v151 offset:3840\n"
MF "v[104:107],v[176:179],a[144:147],v[104:107]\n"
"s_nop 0\n"
"ds_write_addtid_b32 v156 offset:5120\n"
"ds_write_addtid_b32 v157 offset:5376\n"
MF "v[112:115],v[176:179],a[148:151],v[112:115]\n"
"s_nop 0\n"
"ds_write_addtid_b32 v158 offset:5632\n"
"ds_write_addtid_b32 v159 offset:5888\n"
MF "v[112:115],v[180:183],a[188:191],v[112:115]\n"
"ds_write_addtid_b32 v164 offset:7168\n"
MF "v[112:115],v[184:187],a[228:231],v[112:115]\n"
"ds_write_addtid_b32 v165 offset:7424\n"
MF "v[120:123],v[184:187],a[232:235],v[120:123]\n"
"ds_write_addtid_b32 v166 offset:7680\n"
MF "v[120:123],v[180:183],a[192:195],v[120:123]\n"
"ds_write_addtid_b32 v167 offset:7936\n"
MF "v[120:123],v[176:179],a[152:155],v[120:123]\n"
"ds_write_addtid_b32 v172 offset:9216\n"
MF "v[128:131],v[176:179],a[156:159],v[128:131]\n"
"ds_write_addtid_b32 v173 offset:9472\n"
MF "v[128:131],v[180:183],a[196:199],v[128:131]\n"
"ds_write_addtid_b32 v174 offset:9728\n"
MF "v[128:131],v[184:187],a[236:239],v[128:131]\n"
"s_waitcnt lgkmcnt(15)\n"
"ds_write_addtid_b32 v175 offset:9984\n"
MF "v[100:103],v[188:191],a[140:143],v[100:103]\n"
MF "v[100:103],a[240:243],a[180:183],v[100:103]\n"
MF "v[100:103],a[244:247],a[220:223],v[100:103]\n"
MF "v[108:111],a[244:247],a[224:227],v[108:111]\n"
"s_waitcnt lgkmcnt(0)\n"
MF "v[108:111],a[240:243],a[184:187],v[108:111]\n"
"s_barrier\n"
MF "v[108:111],v[188:191],a[144:147],v[108:111]\n"
"ds_read2st64_b32 v[16:17],v13 offset0:192 offset1:193\n"
MF "v[116:119],v[188:191],a[148:151],v[116:119]\n"
"ds_read2st64_b32 v[18:19],v13 offset0:194 offset1:195\n"
MF "v[116:119],a[240:243],a[188:191],v[116:119]\n"
"ds_read2st64_b32 v[20:21],v13 offset0:196 offset1:197\n"
MF "v[116:119],a[244:247],a[228:231],v[116:119]\n"
"ds_read2st64_b32 v[22:23],v13 offset0:198 offset1:199\n"
MF "v[124:127],a[244:247],a[232:235],v[124:127]\n"
"ds_read2st64_b32 v[24:25],v13 offset0:200 offset1:201\n"
MF "v[124:127],a[240:243],a[192:195],v[124:127]\n"
"ds_read2st64_b32 v[26:27],v13 offset0:202 offset1:203\n"
MF "v[124:127],v[188:191],a[152:155],v[124:127]\n"
"ds_read2st64_b32 v[28:29],v13 offset0:204 offset1:205\n"
MF "v[132:135],v[188:191],a[156:159],v[132:135]\n"
"ds_read2st64_b32 v[30:31],v13 offset0:206 offset1:207\n"
MF "v[132:135],a[240:243],a[196:199],v[132:135]\n"
"ds_read2st64_b32 v[32:33],v13 offset0:208 offset1:209\n"
MF "v[132:135],a[244:247],a[236:239],v[132:135]\n"
"ds_read2st64_b32 v[34:35],v13 offset0:210 offset1:211\n"
"ds_read2st64_b32 v[36:37],v13 offset0:212 offset1:213\n"
"ds_read2st64_b32 v[38:39],v13 offset0:214 offset1:215\n"
"s_nop 0\n"
"ds_read2st64_b32 v[40:41],v13 offset0:216 offset1:217\n"
"ds_read2st64_b32 v[42:43],v13 offset0:218 offset1:219\n"
"ds_read2st64_b32 v[44:45],v13 offset0:220 offset1:221\n"
"ds_read2st64_b32 v[46:47],v13 offset0:222 offset1:223\n"
"ds_read2st64_b32 v[48:49],v13 offset0:224 offset1:225\n"
"ds_read2st64_b32 v[50:51],v13 offset0:226 offset1:227\n"
"ds_read2st64_b32 v[52:53],v13 offset0:228 offset1:229\n"
"ds_read2st64_b32 v[54:55],v13 offset0:230 offset1:231\n"
"s_waitcnt vmcnt(0)\n"
"v_sub_u32 v10,v10,v11\n"
"s_cmp_eq_u32 s16,0\n"
"s_cbranch_scc1 .Lhave13\n"
"s_add_u32 s59,s78,6\n"
"s_and_b32 s59,s59,15\n"
"s_nop 3\n"
".Lnf9:\n"
"v_readlane_b32 s58,v10,s59\n"
"s_cmp_lt_u32 s58,9\n"
"s_cbranch_scc1 .Lsp9\n"
".Lnb9:\n"
"s_bfe_u32 s62,s22,0x30014\n"
"s_add_u32 s62,s62,3\n"
"s_and_b32 s62,s62,7\n"
"s_mul_i32 s62,s62,204800\n"
"s_add_u32 s62,s62,s73\n"
"s_add_u32 s62,s62,122880\n"
LD "v[56:59],v9,s[40:43],s62 offen sc1\n"
LD "v[64:67],v9,s[40:43],s62 offen offset:1024 sc1\n"
LD "v[72:75],v9,s[40:43],s62 offen offset:2048 sc1\n"
LD "v[80:83],v9,s[40:43],s62 offen offset:3072 sc1\n"
"s_add_u32 s62,s62,4096\n"
LD "v[88:91],v9,s[40:43],s62 offen sc1\n"
".Lhave13:\n"
"s_cmp_eq_u32 s17,0\n"
"s_cbranch_scc1 .Lhave12\n"
"s_add_u32 s59,s78,4\n"
"s_and_b32 s59,s59,15\n"
"s_nop 3\n"
".Lnf10:\n"
"v_readlane_b32 s58,v10,s59\n"
"s_cmp_lt_u32 s58,10\n"
"s_cbranch_scc1 .Lsp10\n"
".Lnb10:\n"
"s_bfe_u32 s62,s22,0x30014\n"
"s_add_u32 s62,s62,2\n"
"s_and_b32 s62,s62,7\n"
"s_mul_i32 s62,s62,204800\n"
"s_add_u32 s62,s62,s73\n"
"s_add_u32 s62,s62,143360\n"
LD "v[60:63],v9,s[40:43],s62 offen sc1\n"
LD "v[68:71],v9,s[40:43],s62 offen offset:1024 sc1\n"
LD "v[76:79],v9,s[40:43],s62 offen offset:2048 sc1\n"
LD "v[84:87],v9,s[40:43],s62 offen offset:3072 sc1\n"
"s_add_u32 s62,s62,4096\n"
LD "v[92:95],v9,s[40:43],s62 offen sc1\n"
".Lhave12:\n"
".Lnf11:\n"
"v_readlane_b32 s58,v10,s72\n"
"s_cmp_lt_u32 s58,11\n"
"s_cbranch_scc1 .Lsp11\n"
".Lnb11:\n"
"s_bfe_u32 s62,s22,0x30014\n"
"s_add_u32 s62,s62,1\n"
"s_and_b32 s62,s62,7\n"
"s_mul_i32 s62,s62,204800\n"
"s_add_u32 s62,s62,s73\n"
"s_nop 0\n"
"s_add_u32 s62,s62,163840\n"
LD "v[208:211],v9,s[40:43],s62 offen sc1\n"
LD "v[212:215],v9,s[40:43],s62 offen offset:1024 sc1\n"
LD "v[216:219],v9,s[40:43],s62 offen offset:2048 sc1\n"
LD "v[220:223],v9,s[40:43],s62 offen offset:3072 sc1\n"
"s_add_u32 s62,s62,4096\n"
LD "v[224:227],v9,s[40:43],s62 offen sc1\n"
"s_cmp_eq_u32 s1,0xf0000000\n"
"s_cbranch_scc1 .Ljx1\n"
"v_accvgpr_mov_b32 a16,a0\n"
"v_accvgpr_mov_b32 a17,a1\n"
"v_accvgpr_mov_b32 a56,a40\n"
"v_accvgpr_mov_b32 a57,a41\n"
MF "v[196:199],a[2:5],a[250:253],0\n"
MF "v[192:195],a[6:9],a[250:253],0\n"
MF "v[196:199],a[10:13],a[250:253],v[196:199]\n"
MF "v[192:195],a[14:17],a[250:253],v[192:195]\n"
MF "v[196:199],a[42:45],a[250:253],v[196:199]\n"
MF "v[192:195],a[46:49],a[250:253],v[192:195]\n"
MF "v[196:199],a[50:53],a[250:253],v[196:199]\n"
MF "v[192:195],a[54:57],a[250:253],v[192:195]\n"
".Lpg38h:\n"
"s_cbranch_execz .Lisr4\n"
"s_nop 7\n"
CV "v192,v192,v193\n"
CV "v193,v194,v195\n"
CV "v194,v196,v197\n"
CV "v195,v198,v199\n"
"s_nop 0\n"
"v_permlane16_swap_b32 v192,v194\n"
"v_permlane16_swap_b32 v193,v195\n"
"buffer_store_dwordx4 v[192:195],v8,s[44:47],s1 offen sc1\n"
".Ljx1:\n"
"s_waitcnt lgkmcnt(15)\n"
"v_pk_add_f32 v[96:97],v[96:97],v[16:17]\n"
"v_pk_add_f32 v[98:99],v[98:99],v[18:19]\n"
"v_pk_add_f32 v[100:101],v[100:101],v[20:21]\n"
"v_pk_add_f32 v[102:103],v[102:103],v[22:23]\n"
"v_pk_add_f32 v[104:105],v[104:105],v[24:25]\n"
"s_waitcnt lgkmcnt(14)\n"
"v_pk_add_f32 v[106:107],v[106:107],v[26:27]\n"
"s_waitcnt lgkmcnt(13)\n"
"v_pk_add_f32 v[108:109],v[108:109],v[28:29]\n"
"s_waitcnt lgkmcnt(12)\n"
"v_pk_add_f32 v[110:111],v[110:111],v[30:31]\n"
"s_waitcnt lgkmcnt(11)\n"
"v_pk_add_f32 v[112:113],v[112:113],v[32:33]\n"
"s_waitcnt lgkmcnt(10)\n"
"v_pk_add_f32 v[114:115],v[114:115],v[34:35]\n"
"s_waitcnt lgkmcnt(9)\n"
"v_pk_add_f32 v[116:117],v[116:117],v[36:37]\n"
"s_waitcnt lgkmcnt(8)\n"
"v_pk_add_f32 v[118:119],v[118:119],v[38:39]\n"
"s_waitcnt lgkmcnt(7)\n"
"v_pk_add_f32 v[120:121],v[120:121],v[40:41]\n"
"s_waitcnt lgkmcnt(6)\n"
"v_pk_add_f32 v[122:123],v[122:123],v[42:43]\n"
"s_waitcnt lgkmcnt(5)\n"
"v_pk_add_f32 v[124:125],v[124:125],v[44:45]\n"
"s_waitcnt lgkmcnt(4)\n"
"v_pk_add_f32 v[126:127],v[126:127],v[46:47]\n"
"s_waitcnt lgkmcnt(3)\n"
"v_pk_add_f32 v[128:129],v[128:129],v[48:49]\n"
"s_waitcnt lgkmcnt(2)\n"
"v_pk_add_f32 v[130:131],v[130:131],v[50:51]\n"
"s_waitcnt lgkmcnt(1)\n"
"v_pk_add_f32 v[132:133],v[132:133],v[52:53]\n"
"s_waitcnt lgkmcnt(0)\n"
"v_pk_add_f32 v[134:135],v[134:135],v[54:55]\n"
"s_waitcnt vmcnt(5)\n"
"s_nop 0\n"
MF "v[100:103],v[56:59],a[252:255],v[100:103]\n"
MF "v[108:111],v[64:67],a[252:255],v[108:111]\n"
MF "v[116:119],v[72:75],a[252:255],v[116:119]\n"
MF "v[124:127],v[80:83],a[252:255],v[124:127]\n"
MF "v[132:135],v[88:91],a[252:255],v[132:135]\n"
MF "v[96:99],v[56:59],a[248:251],v[96:99]\n"
MF "v[104:107],v[64:67],a[248:251],v[104:107]\n"
MF "v[112:115],v[72:75],a[248:251],v[112:115]\n"
MF "v[120:123],v[80:83],a[248:251],v[120:123]\n"
MF "v[128:131],v[88:91],a[248:251],v[128:131]\n"
MF "v[96:99],v[60:63],a[252:255],v[96:99]\n"
MF "v[104:107],v[68:71],a[252:255],v[104:107]\n"
MF "v[112:115],v[76:79],a[252:255],v[112:115]\n"
MF "v[120:123],v[84:87],a[252:255],v[120:123]\n"
MF "v[128:131],v[92:95],a[252:255],v[128:131]\n"
MF "v[100:103],v[60:63],a[248:251],v[100:103]\n"
MF "v[108:111],v[68:71],a[248:251],v[108:111]\n"
MF "v[116:119],v[76:79],a[248:251],v[116:119]\n"
MF "v[124:127],v[84:87],a[248:251],v[124:127]\n"
MF "v[132:135],v[92:95],a[248:251],v[132:135]\n"
"s_waitcnt vmcnt(0)\n"
MF "v[100:103],v[208:211],a[252:255],v[100:103]\n"
MF "v[96:99],v[208:211],a[248:251],v[96:99]\n"
MF "v[108:111],v[212:215],a[252:255],v[108:111]\n"
MF "v[104:107],v[212:215],a[248:251],v[104:107]\n"
MF "v[116:119],v[216:219],a[252:255],v[116:119]\n"
"s_nop 4\n"
CV "v96,v96,v97\n"
CV "v97,v98,v99\n"
MF "v[112:115],v[216:219],a[248:251],v[112:115]\n"
CV "v98,v100,v101\n"
CV "v99,v102,v103\n"
MF "v[124:127],v[220:223],a[252:255],v[124:127]\n"
CV "v104,v104,v105\n"
CV "v105,v106,v107\n"
MF "v[120:123],v[220:223],a[248:251],v[120:123]\n"
CV "v106,v108,v109\n"
CV "v107,v110,v111\n"
"v_permlane16_swap_b32 v96,v98\n"
"v_permlane16_swap_b32 v97,v99\n"
"buffer_store_dwordx4 v[96:99],v242,s[44:47],s87 offen sc1\n"
"s_add_u32 s87,s87,40960\n"
MF "v[132:135],v[224:227],a[252:255],v[132:135]\n"
CV "v112,v112,v113\n"
CV "v113,v114,v115\n"
MF "v[128:131],v[224:227],a[248:251],v[128:131]\n"
CV "v114,v116,v117\n"
CV "v115,v118,v119\n"
"v_permlane16_swap_b32 v104,v106\n"
"v_permlane16_swap_b32 v105,v107\n"
"buffer_store_dwordx4 v[104:107],v242,s[44:47],s87 offen sc1\n"
"s_add_u32 s87,s87,40960\n"
CV "v120,v120,v121\n"
CV "v121,v122,v123\n"
CV "v122,v124,v125\n"
CV "v123,v126,v127\n"
"v_permlane16_swap_b32 v112,v114\n"
"v_permlane16_swap_b32 v113,v115\n"
"buffer_store_dwordx4 v[112:115],v242,s[44:47],s87 offen sc1\n"
"s_add_u32 s87,s87,40960\n"
CV "v128,v128,v129\n"
CV "v129,v130,v131\n"
CV "v130,v132,v133\n"
CV "v131,v134,v135\n"
"v_permlane16_swap_b32 v120,v122\n"
"v_permlane16_swap_b32 v121,v123\n"
"buffer_store_dwordx4 v[120:123],v242,s[44:47],s87 offen sc1\n"
"s_add_u32 s87,s87,40960\n"
"v_permlane16_swap_b32 v128,v130\n"
"v_permlane16_swap_b32 v129,v131\n"
"buffer_store_dwordx4 v[128:131],v242,s[44:47],s87 offen sc1\n"
"s_add_u32 s87,s87,40960\n"
"s_endpgm\n"
".Ljobs:\n"
"s_cmp_ge_u32 s92,40\n"
"s_cbranch_scc1 .Ldone\n"
"buffer_load_dword v10,v12,s[40:43],s86 offen sc1\n"
"s_mul_i32 s95,s92,3277\n"
"s_lshr_b32 s95,s95,16\n"
"s_mul_i32 s59,s95,20\n"
"s_sub_u32 s59,s92,s59\n"
"s_cmp_ge_u32 s59,10\n"
"s_cselect_b32 s96,4294557760,0\n"
"s_add_u32 s96,s96,1024\n"
"s_mul_i32 s59,s59,40960\n"
"s_add_u32 s96,s96,s59\n"
"s_lshl_b32 s59,s95,7\n"
"s_add_u32 s96,s96,s59\n"
"s_add_u32 s95,s95,3\n"
"s_waitcnt vmcnt(0)\n"
".Ljtry:\n"
"s_cmp_eq_u32 s98,0\n"
"s_cselect_b32 s95,0,s95\n"
"v_sub_u32 v10,v10,v11\n"
"v_cmp_gt_u32 vcc,s95,v10\n"
"s_lshl_b32 s82,s92,10\n"
"s_add_u32 s82,s82,s93\n"
"s_cmp_eq_u64 vcc,0\n"
"s_cselect_b32 s82,s82,0xf0000000\n"
"s_cselect_b32 s84,s96,0xf0000000\n"
"s_cselect_b32 s59,s4,0\n"
"s_add_u32 s92,s92,s59\n"
"s_cmp_eq_u32 s59,0\n"
"s_cbranch_scc0 .Ljgo\n"
"s_sub_u32 s98,s98,1\n"
"s_sleep 2\n"
"s_branch .Ljobs\n"
".Ljgo:\n"
"s_mov_b32 s83,s84\n"
LD "v[208:211],v9,s[40:43],s82 offen sc1\n"
"s_add_u32 s82,s82,s71\n"
LD "v[212:215],v9,s[40:43],s82 offen sc1\n"
"s_add_u32 s82,s82,s71\n"
LD "v[216:219],v9,s[40:43],s82 offen sc1\n"
"s_add_u32 s82,s82,s71\n"
LD "v[220:223],v9,s[40:43],s82 offen sc1\n"
"s_add_u32 s82,s82,s71\n"
LD "v[224:227],v9,s[40:43],s82 offen sc1\n"
"s_add_u32 s82,s82,s66\n"
LD "v[228:231],v9,s[40:43],s82 offen sc1\n"
"s_add_u32 s82,s82,s71\n"
LD "v[232:235],v9,s[40:43],s82 offen sc1\n"
"s_add_u32 s82,s82,s71\n"
LD "v[236:239],v9,s[40:43],s82 offen sc1\n"
"buffer_load_dword v10,v12,s[40:43],s86 offen sc1\n"
"s_waitcnt vmcnt(0)\n"
".Ljfin:\n"
"s_nop 1\n"
"v_mov_b32 v240,v208\n"
"v_mov_b32 v241,v209\n"
MF "v[248:251],v[210:213],a[250:253],0\n"
MF "v[244:247],v[214:217],a[250:253],0\n"
MF "v[248:251],v[218:221],a[250:253],v[248:251]\n"
MF "v[244:247],v[222:225],a[250:253],v[244:247]\n"
MF "v[248:251],v[226:229],a[250:253],v[248:251]\n"
MF "v[244:247],v[230:233],a[250:253],v[244:247]\n"
MF "v[248:251],v[234:237],a[250:253],v[248:251]\n"
MF "v[244:247],v[238:241],a[250:253],v[244:247]\n"
"s_nop 7\n"
CV "v244,v244,v245\n"
CV "v245,v246,v247\n"
CV "v246,v248,v249\n"
CV "v247,v250,v251\n"
"s_nop 0\n"
"v_permlane16_swap_b32 v244,v246\n"
"v_permlane16_swap_b32 v245,v247\n"
"buffer_store_dwordx4 v[244:247],v8,s[44:47],s83 offen sc1\n"
"s_cmp_ge_u32 s92,40\n"
"s_cbranch_scc1 .Ldone\n"
"s_mul_i32 s95,s92,3277\n"
"s_lshr_b32 s95,s95,16\n"
"s_mul_i32 s59,s95,20\n"
"s_sub_u32 s59,s92,s59\n"
"s_cmp_ge_u32 s59,10\n"
"s_cselect_b32 s96,4294557760,0\n"
"s_add_u32 s96,s96,1024\n"
"s_mul_i32 s59,s59,40960\n"
"s_add_u32 s96,s96,s59\n"
"s_lshl_b32 s59,s95,7\n"
"s_add_u32 s96,s96,s59\n"
"s_add_u32 s95,s95,3\n"
"s_branch .Ljtry\n"
".Lool:\n"
".Lsp0:\n"
"s_cmp_eq_u32 s98,0\n"
"s_cbranch_scc1 .Lnb0\n"
"s_sub_u32 s98,s98,1\n"
"s_sleep 2\n"
"buffer_load_dword v10,v12,s[40:43],s86 offen sc1\n"
"s_waitcnt vmcnt(0)\n"
"v_sub_u32 v10,v10,v11\n"
"s_nop 1\n"
"s_branch .Lnf0\n"
".Lsp1:\n"
"s_cmp_eq_u32 s98,0\n"
"s_cbranch_scc1 .Lnb1\n"
"s_sub_u32 s98,s98,1\n"
"s_sleep 2\n"
"buffer_load_dword v10,v12,s[40:43],s86 offen sc1\n"
"s_waitcnt vmcnt(0)\n"
"v_sub_u32 v10,v10,v11\n"
"s_nop 1\n"
"s_branch .Lnf1\n"
".Lsp2:\n"
"s_cmp_eq_u32 s98,0\n"
"s_cbranch_scc1 .Lnb2\n"
"s_sub_u32 s98,s98,1\n"
"s_sleep 2\n"
"buffer_load_dword v10,v12,s[40:43],s86 offen sc1\n"
"s_waitcnt vmcnt(0)\n"
"v_sub_u32 v10,v10,v11\n"
"s_nop 1\n"
"s_branch .Lnf2\n"
".Lsp3:\n"
"s_cmp_eq_u32 s98,0\n"
"s_cbranch_scc1 .Lnb3\n"
"s_sub_u32 s98,s98,1\n"
"s_sleep 2\n"
"buffer_load_dword v10,v12,s[40:43],s86 offen sc1\n"
"s_waitcnt vmcnt(0)\n"
"v_sub_u32 v10,v10,v11\n"
"s_nop 1\n"
"s_branch .Lnf3\n"
".Lsp4:\n"
"s_cmp_eq_u32 s98,0\n"
"s_cbranch_scc1 .Lnb4\n"
"s_sub_u32 s98,s98,1\n"
"s_sleep 2\n"
"buffer_load_dword v10,v12,s[40:43],s86 offen sc1\n"
"s_waitcnt vmcnt(0)\n"
"v_sub_u32 v10,v10,v11\n"
"s_nop 1\n"
"s_branch .Lnf4\n"
".Lsp5:\n"
"s_cmp_eq_u32 s98,0\n"
"s_cbranch_scc1 .Lnb5\n"
"s_sub_u32 s98,s98,1\n"
"s_sleep 2\n"
"buffer_load_dword v10,v12,s[40:43],s86 offen sc1\n"
"s_waitcnt vmcnt(0)\n"
"v_sub_u32 v10,v10,v11\n"
"s_nop 1\n"
"s_branch .Lnf5\n"
".Lsp6:\n"
"s_cmp_eq_u32 s98,0\n"
"s_cbranch_scc1 .Lnb6\n"
"s_sub_u32 s98,s98,1\n"
"s_sleep 2\n"
"buffer_load_dword v10,v12,s[40:43],s86 offen sc1\n"
"s_waitcnt vmcnt(0)\n"
"v_sub_u32 v10,v10,v11\n"
"s_nop 1\n"
"s_branch .Lnf6\n"
".Lsp7:\n"
"s_cmp_eq_u32 s98,0\n"
"s_cbranch_scc1 .Lnb7\n"
"s_sub_u32 s98,s98,1\n"
"s_sleep 2\n"
"buffer_load_dword v10,v12,s[40:43],s86 offen sc1\n"
"s_waitcnt vmcnt(0)\n"
"v_sub_u32 v10,v10,v11\n"
"s_nop 1\n"
"s_branch .Lnf7\n"
".Lsp8:\n"
"s_cmp_eq_u32 s98,0\n"
"s_cbranch_scc1 .Lnb8\n"
"s_sub_u32 s98,s98,1\n"
"s_sleep 2\n"
"buffer_load_dword v10,v12,s[40:43],s86 offen sc1\n"
"s_waitcnt vmcnt(0)\n"
"v_sub_u32 v10,v10,v11\n"
"s_nop 1\n"
"s_branch .Lnf8\n"
".Lsp9:\n"
"s_cmp_eq_u32 s98,0\n"
"s_cbranch_scc1 .Lnb9\n"
"s_sub_u32 s98,s98,1\n"
"s_sleep 2\n"
"buffer_load_dword v10,v12,s[40:43],s86 offen sc1\n"
"s_waitcnt vmcnt(0)\n"
"v_sub_u32 v10,v10,v11\n"
"s_nop 1\n"
"s_branch .Lnf9\n"
".Lsp10:\n"
"s_cmp_eq_u32 s98,0\n"
"s_cbranch_scc1 .Lnb10\n"
"s_sub_u32 s98,s98,1\n"
"s_sleep 2\n"
"buffer_load_dword v10,v12,s[40:43],s86 offen sc1\n"
"s_waitcnt vmcnt(0)\n"
"v_sub_u32 v10,v10,v11\n"
"s_nop 1\n"
"s_branch .Lnf10\n"
".Lsp11:\n"
"s_cmp_eq_u32 s98,0\n"
"s_cbranch_scc1 .Lnb11\n"
"s_sub_u32 s98,s98,1\n"
"s_sleep 2\n"
"buffer_load_dword v10,v12,s[40:43],s86 offen sc1\n"
"s_waitcnt vmcnt(0)\n"
"v_sub_u32 v10,v10,v11\n"
"s_nop 1\n"
"s_branch .Lnf11\n"
".Lcode1:\n"
".Lslow_0:\n"
"s_nop 7\n"
"s_nop 7\n"
"v_and_b32 v10,240,v9\n"
"v_mul_u32_u24 v10,24,v10\n"
"v_lshrrev_b32 v11,4,v9\n"
"v_and_or_b32 v10,v11,48,v10\n"
"s_mov_b32 s59,s34\n"
"s_mov_b32 s34,s20\n"
"s_add_u32 s58,s74,0\n"
"s_nop 3\n"
LD "v[52:55],v10,s[32:35],s58 offen\n"
"s_waitcnt vmcnt(0)\n"
MF "v[72:75],v[52:55],v[136:139],v[32:35]\n"
MF "v[76:79],v[52:55],v[140:143],v[36:39]\n"
MF "v[80:83],v[52:55],v[144:147],v[40:43]\n"
MF "v[84:87],v[52:55],v[148:151],v[44:47]\n"
MF "v[88:91],v[52:55],v[152:155],v[48:51]\n"
"s_add_u32 s58,s74,0\n"
"s_nop 3\n"
LD "v[52:55],v10,s[32:35],s58 offen offset:64\n"
"s_waitcnt vmcnt(0)\n"
MF "v[72:75],v[52:55],v[156:159],v[72:75]\n"
MF "v[76:79],v[52:55],v[160:163],v[76:79]\n"
MF "v[80:83],v[52:55],v[164:167],v[80:83]\n"
MF "v[84:87],v[52:55],v[168:171],v[84:87]\n"
MF "v[88:91],v[52:55],v[172:175],v[88:91]\n"
"s_add_u32 s58,s74,0\n"
"s_nop 3\n"
LD "v[52:55],v10,s[32:35],s58 offen offset:128\n"
"s_waitcnt vmcnt(0)\n"
MF "v[72:75],v[52:55],v[176:179],v[72:75]\n"
MF "v[76:79],v[52:55],v[180:183],v[76:79]\n"
MF "v[80:83],v[52:55],v[184:187],v[80:83]\n"
MF "v[84:87],v[52:55],v[188:191],v[84:87]\n"
MF "v[88:91],v[52:55],v[192:195],v[88:91]\n"
"s_add_u32 s58,s74,0\n"
"s_nop 3\n"
LD "v[52:55],v10,s[32:35],s58 offen offset:192\n"
"s_waitcnt vmcnt(0)\n"
MF "v[72:75],v[52:55],v[196:199],v[72:75]\n"
MF "v[76:79],v[52:55],v[200:203],v[76:79]\n"
MF "v[80:83],v[52:55],v[204:207],v[80:83]\n"
MF "v[84:87],v[52:55],v[208:211],v[84:87]\n"
MF "v[88:91],v[52:55],v[212:215],v[88:91]\n"
"s_add_u32 s58,s74,0\n"
"s_nop 3\n"
LD "v[52:55],v10,s[32:35],s58 offen offset:256\n"
"s_waitcnt vmcnt(0)\n"
MF "v[72:75],v[52:55],v[216:219],v[72:75]\n"
MF "v[76:79],v[52:55],v[220:223],v[76:79]\n"
MF "v[80:83],v[52:55],v[224:227],v[80:83]\n"
MF "v[84:87],v[52:55],v[228:231],v[84:87]\n"
MF "v[88:91],v[52:55],v[232:235],v[88:91]\n"
"s_add_u32 s58,s74,0\n"
"s_nop 3\n"
LD "v[52:55],v10,s[32:35],s58 offen offset:320\n"
"s_waitcnt vmcnt(0)\n"
MF "v[72:75],v[52:55],v[236:239],v[72:75]\n"
MF "v[76:79],v[52:55],v[240:243],v[76:79]\n"
MF "v[80:83],v[52:55],v[244:247],v[80:83]\n"
MF "v[84:87],v[52:55],v[248:251],v[84:87]\n"
MF "v[88:91],v[52:55],v[252:255],v[88:91]\n"
"s_add_u32 s58,s74,6144\n"
"s_nop 3\n"
LD "v[52:55],v10,s[32:35],s58 offen\n"
"s_waitcnt vmcnt(0)\n"
MF "v[112:115],v[52:55],v[136:139],v[32:35]\n"
MF "v[116:119],v[52:55],v[140:143],v[36:39]\n"
MF "v[120:123],v[52:55],v[144:147],v[40:43]\n"
MF "v[124:127],v[52:55],v[148:151],v[44:47]\n"
MF "v[128:131],v[52:55],v[152:155],v[48:51]\n"
"s_add_u32 s58,s74,6144\n"
"s_nop 3\n"
LD "v[52:55],v10,s[32:35],s58 offen offset:64\n"
"s_waitcnt vmcnt(0)\n"
MF "v[112:115],v[52:55],v[156:159],v[112:115]\n"
MF "v[116:119],v[52:55],v[160:163],v[116:119]\n"
MF "v[120:123],v[52:55],v[164:167],v[120:123]\n"
MF "v[124:127],v[52:55],v[168:171],v[124:127]\n"
MF "v[128:131],v[52:55],v[172:175],v[128:131]\n"
"s_add_u32 s58,s74,6144\n"
"s_nop 3\n"
LD "v[52:55],v10,s[32:35],s58 offen offset:128\n"
"s_waitcnt vmcnt(0)\n"
MF "v[112:115],v[52:55],v[176:179],v[112:115]\n"
MF "v[116:119],v[52:55],v[180:183],v[116:119]\n"
MF "v[120:123],v[52:55],v[184:187],v[120:123]\n"
MF "v[124:127],v[52:55],v[188:191],v[124:127]\n"
MF "v[128:131],v[52:55],v[192:195],v[128:131]\n"
"s_add_u32 s58,s74,6144\n"
"s_nop 3\n"
LD "v[52:55],v10,s[32:35],s58 offen offset:192\n"
"s_waitcnt vmcnt(0)\n"
MF "v[112:115],v[52:55],v[196:199],v[112:115]\n"
MF "v[116:119],v[52:55],v[200:203],v[116:119]\n"
MF "v[120:123],v[52:55],v[204:207],v[120:123]\n"
MF "v[124:127],v[52:55],v[208:211],v[124:127]\n"
MF "v[128:131],v[52:55],v[212:215],v[128:131]\n"
"s_add_u32 s58,s74,6144\n"
"s_nop 3\n"
LD "v[52:55],v10,s[32:35],s58 offen offset:256\n"
"s_waitcnt vmcnt(0)\n"
MF "v[112:115],v[52:55],v[216:219],v[112:115]\n"
MF "v[116:119],v[52:55],v[220:223],v[116:119]\n"
MF "v[120:123],v[52:55],v[224:227],v[120:123]\n"
MF "v[124:127],v[52:55],v[228:231],v[124:127]\n"
MF "v[128:131],v[52:55],v[232:235],v[128:131]\n"
"s_add_u32 s58,s74,6144\n"
"s_nop 3\n"
LD "v[52:55],v10,s[32:35],s58 offen offset:320\n"
"s_waitcnt vmcnt(0)\n"
MF "v[112:115],v[52:55],v[236:239],v[112:115]\n"
MF "v[116:119],v[52:55],v[240:243],v[116:119]\n"
MF "v[120:123],v[52:55],v[244:247],v[120:123]\n"
MF "v[124:127],v[52:55],v[248:251],v[124:127]\n"
MF "v[128:131],v[52:55],v[252:255],v[128:131]\n"
"s_nop 7\n"
"s_mov_b32 s34,s59\n"
"v_max3_f32 v10,v72,v73,v74\n"
"s_nop 0\n"
"v_max3_f32 v11,v75,v112,v113\n"
"v_max3_f32 v10,v10,v114,v115\n"
"v_max_f32 v10,v10,v11\n"
"v_mov_b32 v11,v10\n"
"s_nop 1\n"
"v_permlane16_swap_b32 v10,v11\n"
"v_max_f32 v10,v10,v11\n"
"v_mov_b32 v11,v10\n"
"s_nop 1\n"
"v_permlane32_swap_b32 v10,v11\n"
"v_max_f32 v10,v10,v11\n"
"v_add_f32 v10,0x41300000,v10\n"
"v_max_f32 v10,0,v10\n"
"v_exp_f32 v11,-v10\n"
AR "v12,a0\n"
"v_subrev_f32 v12,s13,v12\n"
"v_fma_f32 v12,v12,v11,s13\n"
AW "a0,v12\n"
AR "v12,a20\n"
"v_subrev_f32 v12,s13,v12\n"
"v_fma_f32 v12,v12,v11,s13\n"
AW "a20,v12\n"
AR "v12,a40\n"
"v_subrev_f32 v12,s13,v12\n"
"v_fma_f32 v12,v12,v11,s13\n"
AW "a40,v12\n"
AR "v12,a60\n"
"v_subrev_f32 v12,s13,v12\n"
"v_fma_f32 v12,v12,v11,s13\n"
AW "a60,v12\n"
AR "v12,a80\n"
"v_subrev_f32 v12,s13,v12\n"
"v_fma_f32 v12,v12,v11,s13\n"
AW "a80,v12\n"
AR "v12,a100\n"
"v_subrev_f32 v12,s13,v12\n"
"v_fma_f32 v12,v12,v11,s13\n"
AW "a100,v12\n"
AR "v12,a120\n"
"v_subrev_f32 v12,s13,v12\n"
"v_fma_f32 v12,v12,v11,s13\n"
AW "a120,v12\n"
AR "v12,a140\n"
"v_subrev_f32 v12,s13,v12\n"
"v_fma_f32 v12,v12,v11,s13\n"
AW "a140,v12\n"
AR "v12,a160\n"
"v_subrev_f32 v12,s13,v12\n"
"v_fma_f32 v12,v12,v11,s13\n"
AW "a160,v12\n"
AR "v12,a180\n"
"v_subrev_f32 v12,s13,v12\n"
"v_fma_f32 v12,v12,v11,s13\n"
AW "a180,v12\n"
AR "v12,a200\n"
"v_subrev_f32 v12,s13,v12\n"
"v_fma_f32 v12,v12,v11,s13\n"
AW "a200,v12\n"
AR "v12,a220\n"
"v_subrev_f32 v12,s13,v12\n"
"v_fma_f32 v12,v12,v11,s13\n"
AW "a220,v12\n"
AR "v12,a1\n"
"v_subrev_f32 v12,s13,v12\n"
"v_fma_f32 v12,v12,v11,s13\n"
AW "a1,v12\n"
AR "v12,a21\n"
"v_subrev_f32 v12,s13,v12\n"
"v_fma_f32 v12,v12,v11,s13\n"
AW "a21,v12\n"
AR "v12,a41\n"
"v_subrev_f32 v12,s13,v12\n"
"v_fma_f32 v12,v12,v11,s13\n"
AW "a41,v12\n"
AR "v12,a61\n"
"v_subrev_f32 v12,s13,v12\n"
"v_fma_f32 v12,v12,v11,s13\n"
AW "a61,v12\n"
AR "v12,a81\n"
"v_subrev_f32 v12,s13,v12\n"
"v_fma_f32 v12,v12,v11,s13\n"
AW "a81,v12\n"
AR "v12,a101\n"
"v_subrev_f32 v12,s13,v12\n"
"v_fma_f32 v12,v12,v11,s13\n"
AW "a101,v12\n"
AR "v12,a121\n"
"v_subrev_f32 v12,s13,v12\n"
"v_fma_f32 v12,v12,v11,s13\n"
AW "a121,v12\n"
AR "v12,a141\n"
"v_subrev_f32 v12,s13,v12\n"
"v_fma_f32 v12,v12,v11,s13\n"
AW "a141,v12\n"
AR "v12,a161\n"
"v_subrev_f32 v12,s13,v12\n"
"v_fma_f32 v12,v12,v11,s13\n"
AW "a161,v12\n"
AR "v12,a181\n"
"v_subrev_f32 v12,s13,v12\n"
"v_fma_f32 v12,v12,v11,s13\n"
AW "a181,v12\n"
AR "v12,a201\n"
"v_subrev_f32 v12,s13,v12\n"
"v_fma_f32 v12,v12,v11,s13\n"
AW "a201,v12\n"
AR "v12,a221\n"
"v_subrev_f32 v12,s13,v12\n"
"v_fma_f32 v12,v12,v11,s13\n"
AW "a221,v12\n"
AR "v12,a2\n"
"v_subrev_f32 v12,s13,v12\n"
"v_fma_f32 v12,v12,v11,s13\n"
AW "a2,v12\n"
AR "v12,a22\n"
"v_subrev_f32 v12,s13,v12\n"
"v_fma_f32 v12,v12,v11,s13\n"
AW "a22,v12\n"
AR "v12,a42\n"
"v_subrev_f32 v12,s13,v12\n"
"v_fma_f32 v12,v12,v11,s13\n"
AW "a42,v12\n"
AR "v12,a62\n"
"v_subrev_f32 v12,s13,v12\n"
"v_fma_f32 v12,v12,v11,s13\n"
AW "a62,v12\n"
AR "v12,a82\n"
"v_subrev_f32 v12,s13,v12\n"
"v_fma_f32 v12,v12,v11,s13\n"
AW "a82,v12\n"
AR "v12,a102\n"
"v_subrev_f32 v12,s13,v12\n"
"v_fma_f32 v12,v12,v11,s13\n"
AW "a102,v12\n"
AR "v12,a122\n"
"v_subrev_f32 v12,s13,v12\n"
"v_fma_f32 v12,v12,v11,s13\n"
AW "a122,v12\n"
AR "v12,a142\n"
"v_subrev_f32 v12,s13,v12\n"
"v_fma_f32 v12,v12,v11,s13\n"
AW "a142,v12\n"
AR "v12,a162\n"
"v_subrev_f32 v12,s13,v12\n"
"v_fma_f32 v12,v12,v11,s13\n"
AW "a162,v12\n"
AR "v12,a182\n"
"v_subrev_f32 v12,s13,v12\n"
"v_fma_f32 v12,v12,v11,s13\n"
AW "a182,v12\n"
AR "v12,a202\n"
"v_subrev_f32 v12,s13,v12\n"
"v_fma_f32 v12,v12,v11,s13\n"
AW "a202,v12\n"
AR "v12,a222\n"
"v_subrev_f32 v12,s13,v12\n"
"v_fma_f32 v12,v12,v11,s13\n"
AW "a222,v12\n"
AR "v12,a3\n"
"v_subrev_f32 v12,s13,v12\n"
"v_fma_f32 v12,v12,v11,s13\n"
AW "a3,v12\n"
AR "v12,a23\n"
"v_subrev_f32 v12,s13,v12\n"
"v_fma_f32 v12,v12,v11,s13\n"
AW "a23,v12\n"
AR "v12,a43\n"
"v_subrev_f32 v12,s13,v12\n"
"v_fma_f32 v12,v12,v11,s13\n"
AW "a43,v12\n"
AR "v12,a63\n"
"v_subrev_f32 v12,s13,v12\n"
"v_fma_f32 v12,v12,v11,s13\n"
AW "a63,v12\n"
AR "v12,a83\n"
"v_subrev_f32 v12,s13,v12\n"
"v_fma_f32 v12,v12,v11,s13\n"
AW "a83,v12\n"
AR "v12,a103\n"
"v_subrev_f32 v12,s13,v12\n"
"v_fma_f32 v12,v12,v11,s13\n"
AW "a103,v12\n"
AR "v12,a123\n"
"v_subrev_f32 v12,s13,v12\n"
"v_fma_f32 v12,v12,v11,s13\n"
AW "a123,v12\n"
AR "v12,a143\n"
"v_subrev_f32 v12,s13,v12\n"
"v_fma_f32 v12,v12,v11,s13\n"
AW "a143,v12\n"
AR "v12,a163\n"
"v_subrev_f32 v12,s13,v12\n"
"v_fma_f32 v12,v12,v11,s13\n"
AW "a163,v12\n"
AR "v12,a183\n"
"v_subrev_f32 v12,s13,v12\n"
"v_fma_f32 v12,v12,v11,s13\n"
AW "a183,v12\n"
AR "v12,a203\n"
"v_subrev_f32 v12,s13,v12\n"
"v_fma_f32 v12,v12,v11,s13\n"
AW "a203,v12\n"
AR "v12,a223\n"
"v_subrev_f32 v12,s13,v12\n"
"v_fma_f32 v12,v12,v11,s13\n"
AW "a223,v12\n"
"v_mul_f32 v52,v57,v11\n"
"v_sub_f32 v32,v32,v10\n"
"v_sub_f32 v33,v33,v10\n"
"v_sub_f32 v34,v34,v10\n"
"v_sub_f32 v35,v35,v10\n"
"v_sub_f32 v92,v92,v10\n"
"v_sub_f32 v93,v93,v10\n"
"v_sub_f32 v94,v94,v10\n"
"v_sub_f32 v95,v95,v10\n"
"v_sub_f32 v72,v72,v10\n"
"v_sub_f32 v73,v73,v10\n"
"v_sub_f32 v74,v74,v10\n"
"v_sub_f32 v75,v75,v10\n"
"v_sub_f32 v112,v112,v10\n"
"v_sub_f32 v113,v113,v10\n"
"v_sub_f32 v114,v114,v10\n"
"v_sub_f32 v115,v115,v10\n"
"v_exp_f32 v72,v72\n"
"v_exp_f32 v73,v73\n"
"v_exp_f32 v74,v74\n"
"v_exp_f32 v75,v75\n"
"v_exp_f32 v112,v112\n"
"v_exp_f32 v113,v113\n"
"v_exp_f32 v114,v114\n"
"v_exp_f32 v115,v115\n"
"v_add_f32 v52,v52,v72\n"
"v_add_f32 v52,v52,v73\n"
"v_add_f32 v52,v52,v74\n"
"v_add_f32 v52,v52,v75\n"
"v_add_f32 v52,v52,v112\n"
"v_add_f32 v52,v52,v113\n"
"v_add_f32 v52,v52,v114\n"
"v_add_f32_e64 v52,v52,v115\n"
CV "v72,v72,v73\n"
CV "v73,v74,v75\n"
CV "v74,v112,v113\n"
CV "v75,v114,v115\n"
"v_max3_f32 v10,v76,v77,v78\n"
"v_max3_f32 v11,v79,v116,v117\n"
"v_max3_f32 v10,v10,v118,v119\n"
"v_max_f32 v10,v10,v11\n"
"v_mov_b32 v11,v10\n"
"s_nop 1\n"
"v_permlane16_swap_b32 v10,v11\n"
"v_max_f32 v10,v10,v11\n"
"v_mov_b32 v11,v10\n"
"s_nop 1\n"
"v_permlane32_swap_b32 v10,v11\n"
"v_max_f32 v10,v10,v11\n"
"v_add_f32 v10,0x41300000,v10\n"
"v_max_f32 v10,0,v10\n"
"v_exp_f32 v11,-v10\n"
AR "v12,a4\n"
"v_subrev_f32 v12,s13,v12\n"
"v_fma_f32 v12,v12,v11,s13\n"
AW "a4,v12\n"
AR "v12,a24\n"
"v_subrev_f32 v12,s13,v12\n"
"v_fma_f32 v12,v12,v11,s13\n"
AW "a24,v12\n"
AR "v12,a44\n"
"v_subrev_f32 v12,s13,v12\n"
"v_fma_f32 v12,v12,v11,s13\n"
AW "a44,v12\n"
AR "v12,a64\n"
"v_subrev_f32 v12,s13,v12\n"
"v_fma_f32 v12,v12,v11,s13\n"
AW "a64,v12\n"
AR "v12,a84\n"
"v_subrev_f32 v12,s13,v12\n"
"v_fma_f32 v12,v12,v11,s13\n"
AW "a84,v12\n"
AR "v12,a104\n"
"v_subrev_f32 v12,s13,v12\n"
"v_fma_f32 v12,v12,v11,s13\n"
AW "a104,v12\n"
AR "v12,a124\n"
"v_subrev_f32 v12,s13,v12\n"
"v_fma_f32 v12,v12,v11,s13\n"
AW "a124,v12\n"
AR "v12,a144\n"
"v_subrev_f32 v12,s13,v12\n"
"v_fma_f32 v12,v12,v11,s13\n"
AW "a144,v12\n"
AR "v12,a164\n"
"v_subrev_f32 v12,s13,v12\n"
"v_fma_f32 v12,v12,v11,s13\n"
AW "a164,v12\n"
AR "v12,a184\n"
"v_subrev_f32 v12,s13,v12\n"
"v_fma_f32 v12,v12,v11,s13\n"
AW "a184,v12\n"
AR "v12,a204\n"
"v_subrev_f32 v12,s13,v12\n"
"v_fma_f32 v12,v12,v11,s13\n"
AW "a204,v12\n"
AR "v12,a224\n"
"v_subrev_f32 v12,s13,v12\n"
"v_fma_f32 v12,v12,v11,s13\n"
AW "a224,v12\n"
AR "v12,a5\n"
"v_subrev_f32 v12,s13,v12\n"
"v_fma_f32 v12,v12,v11,s13\n"
AW "a5,v12\n"
AR "v12,a25\n"
"v_subrev_f32 v12,s13,v12\n"
"v_fma_f32 v12,v12,v11,s13\n"
AW "a25,v12\n"
AR "v12,a45\n"
"v_subrev_f32 v12,s13,v12\n"
"v_fma_f32 v12,v12,v11,s13\n"
AW "a45,v12\n"
AR "v12,a65\n"
"v_subrev_f32 v12,s13,v12\n"
"v_fma_f32 v12,v12,v11,s13\n"
AW "a65,v12\n"
AR "v12,a85\n"
"v_subrev_f32 v12,s13,v12\n"
"v_fma_f32 v12,v12,v11,s13\n"
AW "a85,v12\n"
AR "v12,a105\n"
"v_subrev_f32 v12,s13,v12\n"
"v_fma_f32 v12,v12,v11,s13\n"
AW "a105,v12\n"
AR "v12,a125\n"
"v_subrev_f32 v12,s13,v12\n"
"v_fma_f32 v12,v12,v11,s13\n"
AW "a125,v12\n"
AR "v12,a145\n"
"v_subrev_f32 v12,s13,v12\n"
"v_fma_f32 v12,v12,v11,s13\n"
AW "a145,v12\n"
AR "v12,a165\n"
"v_subrev_f32 v12,s13,v12\n"
"v_fma_f32 v12,v12,v11,s13\n"
AW "a165,v12\n"
AR "v12,a185\n"
"v_subrev_f32 v12,s13,v12\n"
"v_fma_f32 v12,v12,v11,s13\n"
AW "a185,v12\n"
AR "v12,a205\n"
"v_subrev_f32 v12,s13,v12\n"
"v_fma_f32 v12,v12,v11,s13\n"
AW "a205,v12\n"
AR "v12,a225\n"
"v_subrev_f32 v12,s13,v12\n"
"v_fma_f32 v12,v12,v11,s13\n"
AW "a225,v12\n"
AR "v12,a6\n"
"v_subrev_f32 v12,s13,v12\n"
"v_fma_f32 v12,v12,v11,s13\n"
AW "a6,v12\n"
AR "v12,a26\n"
"v_subrev_f32 v12,s13,v12\n"
"v_fma_f32 v12,v12,v11,s13\n"
AW "a26,v12\n"
AR "v12,a46\n"
"v_subrev_f32 v12,s13,v12\n"
"v_fma_f32 v12,v12,v11,s13\n"
AW "a46,v12\n"
AR "v12,a66\n"
"v_subrev_f32 v12,s13,v12\n"
"v_fma_f32 v12,v12,v11,s13\n"
AW "a66,v12\n"
AR "v12,a86\n"
"v_subrev_f32 v12,s13,v12\n"
"v_fma_f32 v12,v12,v11,s13\n"
AW "a86,v12\n"
AR "v12,a106\n"
"v_subrev_f32 v12,s13,v12\n"
"v_fma_f32 v12,v12,v11,s13\n"
AW "a106,v12\n"
AR "v12,a126\n"
"v_subrev_f32 v12,s13,v12\n"
"v_fma_f32 v12,v12,v11,s13\n"
AW "a126,v12\n"
AR "v12,a146\n"
"v_subrev_f32 v12,s13,v12\n"
"v_fma_f32 v12,v12,v11,s13\n"
AW "a146,v12\n"
AR "v12,a166\n"
"v_subrev_f32 v12,s13,v12\n"
"v_fma_f32 v12,v12,v11,s13\n"
AW "a166,v12\n"
AR "v12,a186\n"
"v_subrev_f32 v12,s13,v12\n"
"v_fma_f32 v12,v12,v11,s13\n"
AW "a186,v12\n"
AR "v12,a206\n"
"v_subrev_f32 v12,s13,v12\n"
"v_fma_f32 v12,v12,v11,s13\n"
AW "a206,v12\n"
AR "v12,a226\n"
"v_subrev_f32 v12,s13,v12\n"
"v_fma_f32 v12,v12,v11,s13\n"
AW "a226,v12\n"
AR "v12,a7\n"
"v_subrev_f32 v12,s13,v12\n"
"v_fma_f32 v12,v12,v11,s13\n"
AW "a7,v12\n"
AR "v12,a27\n"
"v_subrev_f32 v12,s13,v12\n"
"v_fma_f32 v12,v12,v11,s13\n"
AW "a27,v12\n"
AR "v12,a47\n"
"v_subrev_f32 v12,s13,v12\n"
"v_fma_f32 v12,v12,v11,s13\n"
AW "a47,v12\n"
AR "v12,a67\n"
"v_subrev_f32 v12,s13,v12\n"
"v_fma_f32 v12,v12,v11,s13\n"
AW "a67,v12\n"
AR "v12,a87\n"
"v_subrev_f32 v12,s13,v12\n"
"v_fma_f32 v12,v12,v11,s13\n"
AW "a87,v12\n"
AR "v12,a107\n"
"v_subrev_f32 v12,s13,v12\n"
"v_fma_f32 v12,v12,v11,s13\n"
AW "a107,v12\n"
AR "v12,a127\n"
"v_subrev_f32 v12,s13,v12\n"
"v_fma_f32 v12,v12,v11,s13\n"
AW "a127,v12\n"
AR "v12,a147\n"
"v_subrev_f32 v12,s13,v12\n"
"v_fma_f32 v12,v12,v11,s13\n"
AW "a147,v12\n"
AR "v12,a167\n"
"v_subrev_f32 v12,s13,v12\n"
"v_fma_f32 v12,v12,v11,s13\n"
AW "a167,v12\n"
AR "v12,a187\n"
"v_subrev_f32 v12,s13,v12\n"
"v_fma_f32 v12,v12,v11,s13\n"
AW "a187,v12\n"
AR "v12,a207\n"
"v_subrev_f32 v12,s13,v12\n"
"v_fma_f32 v12,v12,v11,s13\n"
AW "a207,v12\n"
AR "v12,a227\n"
"v_subrev_f32 v12,s13,v12\n"
"v_fma_f32 v12,v12,v11,s13\n"
AW "a227,v12\n"
"v_mul_f32 v53,v58,v11\n"
"v_sub_f32 v36,v36,v10\n"
"v_sub_f32 v37,v37,v10\n"
"v_sub_f32 v38,v38,v10\n"
"v_sub_f32 v39,v39,v10\n"
"v_sub_f32 v96,v96,v10\n"
"v_sub_f32 v97,v97,v10\n"
"v_sub_f32 v98,v98,v10\n"
"v_sub_f32 v99,v99,v10\n"
"v_sub_f32 v76,v76,v10\n"
"v_sub_f32 v77,v77,v10\n"
"v_sub_f32 v78,v78,v10\n"
"v_sub_f32 v79,v79,v10\n"
"v_sub_f32 v116,v116,v10\n"
"v_sub_f32 v117,v117,v10\n"
"v_sub_f32 v118,v118,v10\n"
"v_sub_f32 v119,v119,v10\n"
"v_exp_f32 v76,v76\n"
"v_exp_f32 v77,v77\n"
"v_exp_f32 v78,v78\n"
"v_exp_f32 v79,v79\n"
"v_exp_f32 v116,v116\n"
"v_exp_f32 v117,v117\n"
"v_exp_f32 v118,v118\n"
"v_exp_f32 v119,v119\n"
"v_add_f32 v53,v53,v76\n"
"v_add_f32 v53,v53,v77\n"
"v_add_f32 v53,v53,v78\n"
"v_add_f32 v53,v53,v79\n"
"v_add_f32 v53,v53,v116\n"
"v_add_f32 v53,v53,v117\n"
"v_add_f32 v53,v53,v118\n"
"v_add_f32_e64 v53,v53,v119\n"
CV "v76,v76,v77\n"
CV "v77,v78,v79\n"
CV "v78,v116,v117\n"
CV "v79,v118,v119\n"
"v_max3_f32 v10,v80,v81,v82\n"
"v_max3_f32 v11,v83,v120,v121\n"
"v_max3_f32 v10,v10,v122,v123\n"
"v_max_f32 v10,v10,v11\n"
"v_mov_b32 v11,v10\n"
"s_nop 1\n"
"v_permlane16_swap_b32 v10,v11\n"
"v_max_f32 v10,v10,v11\n"
"v_mov_b32 v11,v10\n"
"s_nop 1\n"
"v_permlane32_swap_b32 v10,v11\n"
"v_max_f32_e64 v10,v10,v11\n"
"v_add_f32 v10,0x41300000,v10\n"
"v_max_f32 v10,0,v10\n"
"v_exp_f32 v11,-v10\n"
AR "v12,a8\n"
"v_subrev_f32 v12,s13,v12\n"
"v_fma_f32 v12,v12,v11,s13\n"
AW "a8,v12\n"
AR "v12,a28\n"
"v_subrev_f32 v12,s13,v12\n"
"v_fma_f32 v12,v12,v11,s13\n"
AW "a28,v12\n"
AR "v12,a48\n"
"v_subrev_f32 v12,s13,v12\n"
"v_fma_f32 v12,v12,v11,s13\n"
AW "a48,v12\n"
AR "v12,a68\n"
"v_subrev_f32 v12,s13,v12\n"
"v_fma_f32 v12,v12,v11,s13\n"
AW "a68,v12\n"
AR "v12,a88\n"
"v_subrev_f32 v12,s13,v12\n"
"v_fma_f32 v12,v12,v11,s13\n"
AW "a88,v12\n"
AR "v12,a108\n"
"v_subrev_f32 v12,s13,v12\n"
"v_fma_f32 v12,v12,v11,s13\n"
AW "a108,v12\n"
AR "v12,a128\n"
"v_subrev_f32 v12,s13,v12\n"
"v_fma_f32 v12,v12,v11,s13\n"
AW "a128,v12\n"
AR "v12,a148\n"
"v_subrev_f32 v12,s13,v12\n"
"v_fma_f32 v12,v12,v11,s13\n"
AW "a148,v12\n"
AR "v12,a168\n"
"v_subrev_f32 v12,s13,v12\n"
"v_fma_f32 v12,v12,v11,s13\n"
AW "a168,v12\n"
AR "v12,a188\n"
"v_subrev_f32 v12,s13,v12\n"
"v_fma_f32 v12,v12,v11,s13\n"
AW "a188,v12\n"
AR "v12,a208\n"
"v_subrev_f32 v12,s13,v12\n"
"v_fma_f32 v12,v12,v11,s13\n"
AW "a208,v12\n"
AR "v12,a228\n"
"v_subrev_f32 v12,s13,v12\n"
"v_fma_f32 v12,v12,v11,s13\n"
AW "a228,v12\n"
AR "v12,a9\n"
"v_subrev_f32 v12,s13,v12\n"
"v_fma_f32 v12,v12,v11,s13\n"
AW "a9,v12\n"
AR "v12,a29\n"
"v_subrev_f32 v12,s13,v12\n"
"v_fma_f32 v12,v12,v11,s13\n"
AW "a29,v12\n"
AR "v12,a49\n"
"v_subrev_f32 v12,s13,v12\n"
"v_fma_f32 v12,v12,v11,s13\n"
AW "a49,v12\n"
AR "v12,a69\n"
"v_subrev_f32 v12,s13,v12\n"
"v_fma_f32 v12,v12,v11,s13\n"
AW "a69,v12\n"
AR "v12,a89\n"
"v_subrev_f32 v12,s13,v12\n"
"v_fma_f32 v12,v12,v11,s13\n"
AW "a89,v12\n"
AR "v12,a109\n"
"v_subrev_f32 v12,s13,v12\n"
"v_fma_f32 v12,v12,v11,s13\n"
AW "a109,v12\n"
AR "v12,a129\n"
"v_subrev_f32 v12,s13,v12\n"
"v_fma_f32 v12,v12,v11,s13\n"
AW "a129,v12\n"
AR "v12,a149\n"
"v_subrev_f32 v12,s13,v12\n"
"v_fma_f32 v12,v12,v11,s13\n"
AW "a149,v12\n"
AR "v12,a169\n"
"v_subrev_f32 v12,s13,v12\n"
"v_fma_f32 v12,v12,v11,s13\n"
AW "a169,v12\n"
AR "v12,a189\n"
"v_subrev_f32 v12,s13,v12\n"
"v_fma_f32 v12,v12,v11,s13\n"
AW "a189,v12\n"
AR "v12,a209\n"
"v_subrev_f32 v12,s13,v12\n"
"v_fma_f32 v12,v12,v11,s13\n"
AW "a209,v12\n"
AR "v12,a229\n"
"v_subrev_f32 v12,s13,v12\n"
"v_fma_f32 v12,v12,v11,s13\n"
AW "a229,v12\n"
AR "v12,a10\n"
"v_subrev_f32 v12,s13,v12\n"
"v_fma_f32 v12,v12,v11,s13\n"
AW "a10,v12\n"
AR "v12,a30\n"
"v_subrev_f32 v12,s13,v12\n"
"v_fma_f32 v12,v12,v11,s13\n"
AW "a30,v12\n"
AR "v12,a50\n"
"v_subrev_f32 v12,s13,v12\n"
"v_fma_f32 v12,v12,v11,s13\n"
AW "a50,v12\n"
AR "v12,a70\n"
"v_subrev_f32 v12,s13,v12\n"
"v_fma_f32 v12,v12,v11,s13\n"
AW "a70,v12\n"
AR "v12,a90\n"
"v_subrev_f32 v12,s13,v12\n"
"v_fma_f32 v12,v12,v11,s13\n"
AW "a90,v12\n"
AR "v12,a110\n"
"v_subrev_f32 v12,s13,v12\n"
"v_fma_f32 v12,v12,v11,s13\n"
AW "a110,v12\n"
AR "v12,a130\n"
"v_subrev_f32 v12,s13,v12\n"
"v_fma_f32 v12,v12,v11,s13\n"
AW "a130,v12\n"
AR "v12,a150\n"
"v_subrev_f32 v12,s13,v12\n"
"v_fma_f32 v12,v12,v11,s13\n"
AW "a150,v12\n"
AR "v12,a170\n"
"v_subrev_f32 v12,s13,v12\n"
"v_fma_f32 v12,v12,v11,s13\n"
AW "a170,v12\n"
AR "v12,a190\n"
"v_subrev_f32 v12,s13,v12\n"
"v_fma_f32 v12,v12,v11,s13\n"
AW "a190,v12\n"
AR "v12,a210\n"
"v_subrev_f32 v12,s13,v12\n"
"v_fma_f32 v12,v12,v11,s13\n"
AW "a210,v12\n"
AR "v12,a230\n"
"v_subrev_f32 v12,s13,v12\n"
"v_fma_f32 v12,v12,v11,s13\n"
AW "a230,v12\n"
AR "v12,a11\n"
"v_subrev_f32 v12,s13,v12\n"
"v_fma_f32 v12,v12,v11,s13\n"
AW "a11,v12\n"
AR "v12,a31\n"
"v_subrev_f32 v12,s13,v12\n"
"v_fma_f32 v12,v12,v11,s13\n"
AW "a31,v12\n"
AR "v12,a51\n"
"v_subrev_f32 v12,s13,v12\n"
"v_fma_f32 v12,v12,v11,s13\n"
AW "a51,v12\n"
AR "v12,a71\n"
"v_subrev_f32 v12,s13,v12\n"
"v_fma_f32 v12,v12,v11,s13\n"
AW "a71,v12\n"
AR "v12,a91\n"
"v_subrev_f32 v12,s13,v12\n"
"v_fma_f32 v12,v12,v11,s13\n"
AW "a91,v12\n"
AR "v12,a111\n"
"v_subrev_f32 v12,s13,v12\n"
"v_fma_f32 v12,v12,v11,s13\n"
AW "a111,v12\n"
AR "v12,a131\n"
"v_subrev_f32 v12,s13,v12\n"
"v_fma_f32 v12,v12,v11,s13\n"
AW "a131,v12\n"
AR "v12,a151\n"
"v_subrev_f32 v12,s13,v12\n"
"v_fma_f32 v12,v12,v11,s13\n"
AW "a151,v12\n"
AR "v12,a171\n"
"v_subrev_f32 v12,s13,v12\n"
"v_fma_f32 v12,v12,v11,s13\n"
AW "a171,v12\n"
AR "v12,a191\n"
"v_subrev_f32 v12,s13,v12\n"
"v_fma_f32 v12,v12,v11,s13\n"
AW "a191,v12\n"
AR "v12,a211\n"
"v_subrev_f32 v12,s13,v12\n"
"v_fma_f32 v12,v12,v11,s13\n"
AW "a211,v12\n"
AR "v12,a231\n"
"v_subrev_f32 v12,s13,v12\n"
"v_fma_f32 v12,v12,v11,s13\n"
AW "a231,v12\n"
"v_mul_f32 v54,v59,v11\n"
"v_sub_f32 v40,v40,v10\n"
"v_sub_f32 v41,v41,v10\n"
"v_sub_f32 v42,v42,v10\n"
"v_sub_f32 v43,v43,v10\n"
"v_sub_f32 v100,v100,v10\n"
"v_sub_f32 v101,v101,v10\n"
"v_sub_f32 v102,v102,v10\n"
"v_sub_f32 v103,v103,v10\n"
"v_sub_f32 v80,v80,v10\n"
"v_sub_f32 v81,v81,v10\n"
"v_sub_f32 v82,v82,v10\n"
"v_sub_f32 v83,v83,v10\n"
"v_sub_f32 v120,v120,v10\n"
"v_sub_f32 v121,v121,v10\n"
"v_sub_f32 v122,v122,v10\n"
"v_sub_f32 v123,v123,v10\n"
"v_exp_f32 v80,v80\n"
"v_exp_f32 v81,v81\n"
"v_exp_f32 v82,v82\n"
"v_exp_f32 v83,v83\n"
"v_exp_f32 v120,v120\n"
"v_exp_f32 v121,v121\n"
"v_exp_f32 v122,v122\n"
"v_exp_f32 v123,v123\n"
"v_add_f32 v54,v54,v80\n"
"v_add_f32 v54,v54,v81\n"
"v_add_f32 v54,v54,v82\n"
"v_add_f32 v54,v54,v83\n"
"v_add_f32 v54,v54,v120\n"
"v_add_f32 v54,v54,v121\n"
"v_add_f32 v54,v54,v122\n"
"v_add_f32 v54,v54,v123\n"
CV "v80,v80,v81\n"
CV "v81,v82,v83\n"
CV "v82,v120,v121\n"
CV "v83,v122,v123\n"
"v_max3_f32 v10,v84,v85,v86\n"
"v_max3_f32 v11,v87,v124,v125\n"
"v_max3_f32 v10,v10,v126,v127\n"
"v_max_f32 v10,v10,v11\n"
"v_mov_b32 v11,v10\n"
"s_nop 1\n"
"v_permlane16_swap_b32 v10,v11\n"
"v_max_f32 v10,v10,v11\n"
"v_mov_b32 v11,v10\n"
"s_nop 1\n"
"v_permlane32_swap_b32 v10,v11\n"
"v_max_f32 v10,v10,v11\n"
"v_add_f32 v10,0x41300000,v10\n"
"v_max_f32 v10,0,v10\n"
"v_exp_f32 v11,-v10\n"
AR "v12,a12\n"
"v_subrev_f32 v12,s13,v12\n"
"v_fma_f32 v12,v12,v11,s13\n"
AW "a12,v12\n"
AR "v12,a32\n"
"v_subrev_f32 v12,s13,v12\n"
"v_fma_f32 v12,v12,v11,s13\n"
AW "a32,v12\n"
AR "v12,a52\n"
"v_subrev_f32 v12,s13,v12\n"
"v_fma_f32 v12,v12,v11,s13\n"
AW "a52,v12\n"
AR "v12,a72\n"
"v_subrev_f32 v12,s13,v12\n"
"v_fma_f32 v12,v12,v11,s13\n"
AW "a72,v12\n"
AR "v12,a92\n"
"v_subrev_f32 v12,s13,v12\n"
"v_fma_f32 v12,v12,v11,s13\n"
AW "a92,v12\n"
AR "v12,a112\n"
"v_subrev_f32 v12,s13,v12\n"
"v_fma_f32 v12,v12,v11,s13\n"
AW "a112,v12\n"
AR "v12,a132\n"
"v_subrev_f32 v12,s13,v12\n"
"v_fma_f32 v12,v12,v11,s13\n"
AW "a132,v12\n"
AR "v12,a152\n"
"v_subrev_f32 v12,s13,v12\n"
"v_fma_f32 v12,v12,v11,s13\n"
AW "a152,v12\n"
AR "v12,a172\n"
"v_subrev_f32 v12,s13,v12\n"
"v_fma_f32 v12,v12,v11,s13\n"
AW "a172,v12\n"
AR "v12,a192\n"
"v_subrev_f32 v12,s13,v12\n"
"v_fma_f32 v12,v12,v11,s13\n"
AW "a192,v12\n"
AR "v12,a212\n"
"v_subrev_f32 v12,s13,v12\n"
"v_fma_f32 v12,v12,v11,s13\n"
AW "a212,v12\n"
AR "v12,a232\n"
"v_subrev_f32 v12,s13,v12\n"
"v_fma_f32 v12,v12,v11,s13\n"
AW "a232,v12\n"
AR "v12,a13\n"
"v_subrev_f32 v12,s13,v12\n"
"v_fma_f32 v12,v12,v11,s13\n"
AW "a13,v12\n"
AR "v12,a33\n"
"v_subrev_f32 v12,s13,v12\n"
"v_fma_f32 v12,v12,v11,s13\n"
AW "a33,v12\n"
AR "v12,a53\n"
"v_subrev_f32 v12,s13,v12\n"
"v_fma_f32 v12,v12,v11,s13\n"
AW "a53,v12\n"
AR "v12,a73\n"
"v_subrev_f32 v12,s13,v12\n"
"v_fma_f32 v12,v12,v11,s13\n"
AW "a73,v12\n"
AR "v12,a93\n"
"v_subrev_f32 v12,s13,v12\n"
"v_fma_f32 v12,v12,v11,s13\n"
AW "a93,v12\n"
AR "v12,a113\n"
"v_subrev_f32 v12,s13,v12\n"
"v_fma_f32 v12,v12,v11,s13\n"
AW "a113,v12\n"
AR "v12,a133\n"
"v_subrev_f32 v12,s13,v12\n"
"v_fma_f32 v12,v12,v11,s13\n"
AW "a133,v12\n"
AR "v12,a153\n"
"v_subrev_f32 v12,s13,v12\n"
"v_fma_f32 v12,v12,v11,s13\n"
AW "a153,v12\n"
AR "v12,a173\n"
"v_subrev_f32 v12,s13,v12\n"
"v_fma_f32 v12,v12,v11,s13\n"
AW "a173,v12\n"
AR "v12,a193\n"
"v_subrev_f32 v12,s13,v12\n"
"v_fma_f32 v12,v12,v11,s13\n"
AW "a193,v12\n"
AR "v12,a213\n"
"v_subrev_f32 v12,s13,v12\n"
"v_fma_f32 v12,v12,v11,s13\n"
AW "a213,v12\n"
AR "v12,a233\n"
"v_subrev_f32 v12,s13,v12\n"
"v_fma_f32 v12,v12,v11,s13\n"
AW "a233,v12\n"
AR "v12,a14\n"
"v_subrev_f32 v12,s13,v12\n"
"v_fma_f32 v12,v12,v11,s13\n"
AW "a14,v12\n"
AR "v12,a34\n"
"v_subrev_f32 v12,s13,v12\n"
"v_fma_f32 v12,v12,v11,s13\n"
AW "a34,v12\n"
AR "v12,a54\n"
"v_subrev_f32 v12,s13,v12\n"
"v_fma_f32 v12,v12,v11,s13\n"
AW "a54,v12\n"
AR "v12,a74\n"
"v_subrev_f32 v12,s13,v12\n"
"v_fma_f32 v12,v12,v11,s13\n"
AW "a74,v12\n"
AR "v12,a94\n"
"v_subrev_f32 v12,s13,v12\n"
"v_fma_f32 v12,v12,v11,s13\n"
AW "a94,v12\n"
AR "v12,a114\n"
"v_subrev_f32 v12,s13,v12\n"
"v_fma_f32 v12,v12,v11,s13\n"
AW "a114,v12\n"
AR "v12,a134\n"
"v_subrev_f32 v12,s13,v12\n"
"v_fma_f32 v12,v12,v11,s13\n"
AW "a134,v12\n"
AR "v12,a154\n"
"v_subrev_f32 v12,s13,v12\n"
"v_fma_f32 v12,v12,v11,s13\n"
AW "a154,v12\n"
AR "v12,a174\n"
"v_subrev_f32 v12,s13,v12\n"
"v_fma_f32 v12,v12,v11,s13\n"
AW "a174,v12\n"
AR "v12,a194\n"
"v_subrev_f32 v12,s13,v12\n"
"v_fma_f32 v12,v12,v11,s13\n"
AW "a194,v12\n"
AR "v12,a214\n"
"v_subrev_f32 v12,s13,v12\n"
"v_fma_f32 v12,v12,v11,s13\n"
AW "a214,v12\n"
AR "v12,a234\n"
"v_subrev_f32 v12,s13,v12\n"
"v_fma_f32 v12,v12,v11,s13\n"
AW "a234,v12\n"
AR "v12,a15\n"
"v_subrev_f32 v12,s13,v12\n"
"v_fma_f32 v12,v12,v11,s13\n"
AW "a15,v12\n"
AR "v12,a35\n"
"v_subrev_f32 v12,s13,v12\n"
"v_fma_f32 v12,v12,v11,s13\n"
AW "a35,v12\n"
AR "v12,a55\n"
"v_subrev_f32 v12,s13,v12\n"
"v_fma_f32 v12,v12,v11,s13\n"
AW "a55,v12\n"
AR "v12,a75\n"
"v_subrev_f32 v12,s13,v12\n"
"v_fma_f32 v12,v12,v11,s13\n"
AW "a75,v12\n"
AR "v12,a95\n"
"v_subrev_f32 v12,s13,v12\n"
"v_fma_f32 v12,v12,v11,s13\n"
AW "a95,v12\n"
AR "v12,a115\n"
"v_subrev_f32 v12,s13,v12\n"
"v_fma_f32 v12,v12,v11,s13\n"
AW "a115,v12\n"
AR "v12,a135\n"
"v_subrev_f32 v12,s13,v12\n"
"v_fma_f32 v12,v12,v11,s13\n"
AW "a135,v12\n"
AR "v12,a155\n"
"v_subrev_f32 v12,s13,v12\n"
"v_fma_f32 v12,v12,v11,s13\n"
AW "a155,v12\n"
AR "v12,a175\n"
"v_subrev_f32 v12,s13,v12\n"
"v_fma_f32 v12,v12,v11,s13\n"
AW "a175,v12\n"
AR "v12,a195\n"
"v_subrev_f32 v12,s13,v12\n"
"v_fma_f32 v12,v12,v11,s13\n"
AW "a195,v12\n"
AR "v12,a215\n"
"v_subrev_f32 v12,s13,v12\n"
"v_fma_f32 v12,v12,v11,s13\n"
AW "a215,v12\n"
AR "v12,a235\n"
"v_subrev_f32 v12,s13,v12\n"
"v_fma_f32 v12,v12,v11,s13\n"
AW "a235,v12\n"
"v_mul_f32 v55,v60,v11\n"
"v_sub_f32 v44,v44,v10\n"
"v_sub_f32 v45,v45,v10\n"
"v_sub_f32 v46,v46,v10\n"
"v_sub_f32 v47,v47,v10\n"
"v_sub_f32 v104,v104,v10\n"
"v_sub_f32 v105,v105,v10\n"
"v_sub_f32 v106,v106,v10\n"
"v_sub_f32 v107,v107,v10\n"
"v_sub_f32 v84,v84,v10\n"
"v_sub_f32 v85,v85,v10\n"
"v_sub_f32 v86,v86,v10\n"
"v_sub_f32 v87,v87,v10\n"
"v_sub_f32 v124,v124,v10\n"
"v_sub_f32 v125,v125,v10\n"
"v_sub_f32 v126,v126,v10\n"
"v_sub_f32 v127,v127,v10\n"
"v_exp_f32 v84,v84\n"
"v_exp_f32 v85,v85\n"
"v_exp_f32 v86,v86\n"
"v_exp_f32 v87,v87\n"
"v_exp_f32 v124,v124\n"
"v_exp_f32 v125,v125\n"
"v_exp_f32 v126,v126\n"
"v_exp_f32 v127,v127\n"
"v_add_f32 v55,v55,v84\n"
"v_add_f32 v55,v55,v85\n"
"v_add_f32 v55,v55,v86\n"
"v_add_f32 v55,v55,v87\n"
"v_add_f32 v55,v55,v124\n"
"v_add_f32 v55,v55,v125\n"
"v_add_f32 v55,v55,v126\n"
"v_add_f32_e64 v55,v55,v127\n"
CV "v84,v84,v85\n"
CV "v85,v86,v87\n"
CV "v86,v124,v125\n"
CV "v87,v126,v127\n"
"v_max3_f32 v10,v88,v89,v90\n"
"v_max3_f32 v11,v91,v128,v129\n"
"v_max3_f32 v10,v10,v130,v131\n"
"v_max_f32 v10,v10,v11\n"
"v_mov_b32 v11,v10\n"
"s_nop 1\n"
"v_permlane16_swap_b32 v10,v11\n"
"v_max_f32 v10,v10,v11\n"
"v_mov_b32 v11,v10\n"
"s_nop 1\n"
"v_permlane32_swap_b32 v10,v11\n"
"v_max_f32 v10,v10,v11\n"
"v_add_f32 v10,0x41300000,v10\n"
"v_max_f32 v10,0,v10\n"
"v_exp_f32 v11,-v10\n"
AR "v12,a16\n"
"v_subrev_f32 v12,s13,v12\n"
"v_fma_f32 v12,v12,v11,s13\n"
AW "a16,v12\n"
AR "v12,a36\n"
"v_subrev_f32 v12,s13,v12\n"
"v_fma_f32 v12,v12,v11,s13\n"
AW "a36,v12\n"
AR "v12,a56\n"
"v_subrev_f32 v12,s13,v12\n"
"v_fma_f32 v12,v12,v11,s13\n"
AW "a56,v12\n"
AR "v12,a76\n"
"v_subrev_f32 v12,s13,v12\n"
"v_fma_f32 v12,v12,v11,s13\n"
AW "a76,v12\n"
AR "v12,a96\n"
"v_subrev_f32 v12,s13,v12\n"
"v_fma_f32 v12,v12,v11,s13\n"
AW "a96,v12\n"
AR "v12,a116\n"
"v_subrev_f32 v12,s13,v12\n"
"v_fma_f32 v12,v12,v11,s13\n"
AW "a116,v12\n"
AR "v12,a136\n"
"v_subrev_f32 v12,s13,v12\n"
"v_fma_f32 v12,v12,v11,s13\n"
AW "a136,v12\n"
AR "v12,a156\n"
"v_subrev_f32 v12,s13,v12\n"
"v_fma_f32 v12,v12,v11,s13\n"
AW "a156,v12\n"
AR "v12,a176\n"
"v_subrev_f32 v12,s13,v12\n"
"v_fma_f32 v12,v12,v11,s13\n"
AW "a176,v12\n"
AR "v12,a196\n"
"v_subrev_f32 v12,s13,v12\n"
"v_fma_f32 v12,v12,v11,s13\n"
AW "a196,v12\n"
AR "v12,a216\n"
"v_subrev_f32 v12,s13,v12\n"
"v_fma_f32 v12,v12,v11,s13\n"
AW "a216,v12\n"
AR "v12,a236\n"
"v_subrev_f32 v12,s13,v12\n"
"v_fma_f32 v12,v12,v11,s13\n"
AW "a236,v12\n"
AR "v12,a17\n"
"v_subrev_f32 v12,s13,v12\n"
"v_fma_f32 v12,v12,v11,s13\n"
AW "a17,v12\n"
AR "v12,a37\n"
"v_subrev_f32 v12,s13,v12\n"
"v_fma_f32 v12,v12,v11,s13\n"
AW "a37,v12\n"
AR "v12,a57\n"
"v_subrev_f32 v12,s13,v12\n"
"v_fma_f32 v12,v12,v11,s13\n"
AW "a57,v12\n"
AR "v12,a77\n"
"v_subrev_f32 v12,s13,v12\n"
"v_fma_f32 v12,v12,v11,s13\n"
AW "a77,v12\n"
AR "v12,a97\n"
"v_subrev_f32 v12,s13,v12\n"
"v_fma_f32 v12,v12,v11,s13\n"
AW "a97,v12\n"
AR "v12,a117\n"
"v_subrev_f32 v12,s13,v12\n"
"v_fma_f32 v12,v12,v11,s13\n"
AW "a117,v12\n"
AR "v12,a137\n"
"v_subrev_f32 v12,s13,v12\n"
"v_fma_f32 v12,v12,v11,s13\n"
AW "a137,v12\n"
AR "v12,a157\n"
"v_subrev_f32 v12,s13,v12\n"
"v_fma_f32 v12,v12,v11,s13\n"
AW "a157,v12\n"
AR "v12,a177\n"
"v_subrev_f32 v12,s13,v12\n"
"v_fma_f32 v12,v12,v11,s13\n"
AW "a177,v12\n"
AR "v12,a197\n"
"v_subrev_f32 v12,s13,v12\n"
"v_fma_f32 v12,v12,v11,s13\n"
AW "a197,v12\n"
AR "v12,a217\n"
"v_subrev_f32 v12,s13,v12\n"
"v_fma_f32 v12,v12,v11,s13\n"
AW "a217,v12\n"
AR "v12,a237\n"
"v_subrev_f32 v12,s13,v12\n"
"v_fma_f32 v12,v12,v11,s13\n"
AW "a237,v12\n"
AR "v12,a18\n"
"v_subrev_f32 v12,s13,v12\n"
"v_fma_f32 v12,v12,v11,s13\n"
AW "a18,v12\n"
AR "v12,a38\n"
"v_subrev_f32 v12,s13,v12\n"
"v_fma_f32 v12,v12,v11,s13\n"
AW "a38,v12\n"
AR "v12,a58\n"
"v_subrev_f32 v12,s13,v12\n"
"v_fma_f32 v12,v12,v11,s13\n"
AW "a58,v12\n"
AR "v12,a78\n"
"v_subrev_f32 v12,s13,v12\n"
"v_fma_f32 v12,v12,v11,s13\n"
AW "a78,v12\n"
AR "v12,a98\n"
"v_subrev_f32 v12,s13,v12\n"
"v_fma_f32 v12,v12,v11,s13\n"
AW "a98,v12\n"
AR "v12,a118\n"
"v_subrev_f32 v12,s13,v12\n"
"v_fma_f32 v12,v12,v11,s13\n"
AW "a118,v12\n"
AR "v12,a138\n"
"v_subrev_f32 v12,s13,v12\n"
"v_fma_f32 v12,v12,v11,s13\n"
AW "a138,v12\n"
AR "v12,a158\n"
"v_subrev_f32 v12,s13,v12\n"
"v_fma_f32 v12,v12,v11,s13\n"
AW "a158,v12\n"
AR "v12,a178\n"
"v_subrev_f32 v12,s13,v12\n"
"v_fma_f32 v12,v12,v11,s13\n"
AW "a178,v12\n"
AR "v12,a198\n"
"v_subrev_f32 v12,s13,v12\n"
"v_fma_f32 v12,v12,v11,s13\n"
AW "a198,v12\n"
AR "v12,a218\n"
"v_subrev_f32 v12,s13,v12\n"
"v_fma_f32 v12,v12,v11,s13\n"
AW "a218,v12\n"
AR "v12,a238\n"
"v_subrev_f32 v12,s13,v12\n"
"v_fma_f32 v12,v12,v11,s13\n"
AW "a238,v12\n"
AR "v12,a19\n"
"v_subrev_f32 v12,s13,v12\n"
"v_fma_f32 v12,v12,v11,s13\n"
AW "a19,v12\n"
AR "v12,a39\n"
"v_subrev_f32 v12,s13,v12\n"
"v_fma_f32 v12,v12,v11,s13\n"
AW "a39,v12\n"
AR "v12,a59\n"
"v_subrev_f32 v12,s13,v12\n"
"v_fma_f32 v12,v12,v11,s13\n"
AW "a59,v12\n"
AR "v12,a79\n"
"v_subrev_f32 v12,s13,v12\n"
"v_fma_f32 v12,v12,v11,s13\n"
AW "a79,v12\n"
AR "v12,a99\n"
"v_subrev_f32 v12,s13,v12\n"
"v_fma_f32 v12,v12,v11,s13\n"
AW "a99,v12\n"
AR "v12,a119\n"
"v_subrev_f32 v12,s13,v12\n"
"v_fma_f32 v12,v12,v11,s13\n"
AW "a119,v12\n"
AR "v12,a139\n"
"v_subrev_f32 v12,s13,v12\n"
"v_fma_f32 v12,v12,v11,s13\n"
AW "a139,v12\n"
AR "v12,a159\n"
"v_subrev_f32 v12,s13,v12\n"
"v_fma_f32 v12,v12,v11,s13\n"
AW "a159,v12\n"
AR "v12,a179\n"
"v_subrev_f32 v12,s13,v12\n"
"v_fma_f32 v12,v12,v11,s13\n"
AW "a179,v12\n"
AR "v12,a199\n"
"v_subrev_f32 v12,s13,v12\n"
"v_fma_f32 v12,v12,v11,s13\n"
AW "a199,v12\n"
AR "v12,a219\n"
"v_subrev_f32 v12,s13,v12\n"
"v_fma_f32 v12,v12,v11,s13\n"
AW "a219,v12\n"
AR "v12,a239\n"
"v_subrev_f32 v12,s13,v12\n"
"v_fma_f32 v12,v12,v11,s13\n"
AW "a239,v12\n"
"v_mul_f32 v56,v61,v11\n"
"v_sub_f32 v48,v48,v10\n"
"v_sub_f32 v49,v49,v10\n"
"v_sub_f32 v50,v50,v10\n"
"v_sub_f32 v51,v51,v10\n"
"v_sub_f32 v108,v108,v10\n"
"v_sub_f32 v109,v109,v10\n"
"v_sub_f32 v110,v110,v10\n"
"v_sub_f32 v111,v111,v10\n"
"v_sub_f32 v88,v88,v10\n"
"v_sub_f32 v89,v89,v10\n"
"v_sub_f32 v90,v90,v10\n"
"v_sub_f32 v91,v91,v10\n"
"v_sub_f32 v128,v128,v10\n"
"v_sub_f32 v129,v129,v10\n"
"v_sub_f32 v130,v130,v10\n"
"v_sub_f32 v131,v131,v10\n"
"v_exp_f32 v88,v88\n"
"v_exp_f32 v89,v89\n"
"v_exp_f32 v90,v90\n"
"v_exp_f32 v91,v91\n"
"v_exp_f32 v128,v128\n"
"v_exp_f32 v129,v129\n"
"v_exp_f32 v130,v130\n"
"v_exp_f32 v131,v131\n"
"v_add_f32 v56,v56,v88\n"
"v_add_f32 v56,v56,v89\n"
"v_add_f32 v56,v56,v90\n"
"v_add_f32 v56,v56,v91\n"
"v_add_f32 v56,v56,v128\n"
"v_add_f32 v56,v56,v129\n"
"v_add_f32 v56,v56,v130\n"
"v_add_f32_e64 v56,v56,v131\n"
CV "v88,v88,v89\n"
CV "v89,v90,v91\n"
CV "v90,v128,v129\n"
CV "v91,v130,v131\n"
"s_nop 7\n"
"s_setpc_b64 s[52:53]\n"
".Lslow_1:\n"
"s_nop 7\n"
"s_nop 7\n"
"v_and_b32 v10,240,v9\n"
"v_mul_u32_u24 v10,24,v10\n"
"v_lshrrev_b32 v11,4,v9\n"
"v_and_or_b32 v10,v11,48,v10\n"
"s_mov_b32 s59,s34\n"
"s_mov_b32 s34,s20\n"
"s_add_u32 s58,s74,0\n"
"s_nop 3\n"
LD "v[58:61],v10,s[32:35],s58 offen\n"
"s_waitcnt vmcnt(0)\n"
MF "v[92:95],v[58:61],v[136:139],v[32:35]\n"
MF "v[96:99],v[58:61],v[140:143],v[36:39]\n"
MF "v[100:103],v[58:61],v[144:147],v[40:43]\n"
MF "v[104:107],v[58:61],v[148:151],v[44:47]\n"
MF "v[108:111],v[58:61],v[152:155],v[48:51]\n"
"s_add_u32 s58,s74,0\n"
"s_nop 3\n"
LD "v[58:61],v10,s[32:35],s58 offen offset:64\n"
"s_waitcnt vmcnt(0)\n"
MF "v[92:95],v[58:61],v[156:159],v[92:95]\n"
MF "v[96:99],v[58:61],v[160:163],v[96:99]\n"
MF "v[100:103],v[58:61],v[164:167],v[100:103]\n"
MF "v[104:107],v[58:61],v[168:171],v[104:107]\n"
MF "v[108:111],v[58:61],v[172:175],v[108:111]\n"
"s_add_u32 s58,s74,0\n"
"s_nop 3\n"
LD "v[58:61],v10,s[32:35],s58 offen offset:128\n"
"s_waitcnt vmcnt(0)\n"
MF "v[92:95],v[58:61],v[176:179],v[92:95]\n"
MF "v[96:99],v[58:61],v[180:183],v[96:99]\n"
MF "v[100:103],v[58:61],v[184:187],v[100:103]\n"
MF "v[104:107],v[58:61],v[188:191],v[104:107]\n"
MF "v[108:111],v[58:61],v[192:195],v[108:111]\n"
"s_add_u32 s58,s74,0\n"
"s_nop 3\n"
LD "v[58:61],v10,s[32:35],s58 offen offset:192\n"
"s_waitcnt vmcnt(0)\n"
MF "v[92:95],v[58:61],v[196:199],v[92:95]\n"
MF "v[96:99],v[58:61],v[200:203],v[96:99]\n"
MF "v[100:103],v[58:61],v[204:207],v[100:103]\n"
MF "v[104:107],v[58:61],v[208:211],v[104:107]\n"
MF "v[108:111],v[58:61],v[212:215],v[108:111]\n"
"s_add_u32 s58,s74,0\n"
"s_nop 3\n"
LD "v[58:61],v10,s[32:35],s58 offen offset:256\n"
"s_waitcnt vmcnt(0)\n"
MF "v[92:95],v[58:61],v[216:219],v[92:95]\n"
MF "v[96:99],v[58:61],v[220:223],v[96:99]\n"
MF "v[100:103],v[58:61],v[224:227],v[100:103]\n"
MF "v[104:107],v[58:61],v[228:231],v[104:107]\n"
MF "v[108:111],v[58:61],v[232:235],v[108:111]\n"
"s_add_u32 s58,s74,0\n"
"s_nop 3\n"
LD "v[58:61],v10,s[32:35],s58 offen offset:320\n"
"s_waitcnt vmcnt(0)\n"
MF "v[92:95],v[58:61],v[236:239],v[92:95]\n"
MF "v[96:99],v[58:61],v[240:243],v[96:99]\n"
MF "v[100:103],v[58:61],v[244:247],v[100:103]\n"
MF "v[104:107],v[58:61],v[248:251],v[104:107]\n"
MF "v[108:111],v[58:61],v[252:255],v[108:111]\n"
"s_add_u32 s58,s74,6144\n"
"s_nop 3\n"
LD "v[58:61],v10,s[32:35],s58 offen\n"
"s_waitcnt vmcnt(0)\n"
MF "v[112:115],v[58:61],v[136:139],v[32:35]\n"
MF "v[116:119],v[58:61],v[140:143],v[36:39]\n"
MF "v[120:123],v[58:61],v[144:147],v[40:43]\n"
MF "v[124:127],v[58:61],v[148:151],v[44:47]\n"
MF "v[128:131],v[58:61],v[152:155],v[48:51]\n"
"s_add_u32 s58,s74,6144\n"
"s_nop 3\n"
LD "v[58:61],v10,s[32:35],s58 offen offset:64\n"
"s_waitcnt vmcnt(0)\n"
MF "v[112:115],v[58:61],v[156:159],v[112:115]\n"
MF "v[116:119],v[58:61],v[160:163],v[116:119]\n"
MF "v[120:123],v[58:61],v[164:167],v[120:123]\n"
MF "v[124:127],v[58:61],v[168:171],v[124:127]\n"
MF "v[128:131],v[58:61],v[172:175],v[128:131]\n"
"s_add_u32 s58,s74,6144\n"
"s_nop 3\n"
LD "v[58:61],v10,s[32:35],s58 offen offset:128\n"
"s_waitcnt vmcnt(0)\n"
MF "v[112:115],v[58:61],v[176:179],v[112:115]\n"
MF "v[116:119],v[58:61],v[180:183],v[116:119]\n"
MF "v[120:123],v[58:61],v[184:187],v[120:123]\n"
MF "v[124:127],v[58:61],v[188:191],v[124:127]\n"
MF "v[128:131],v[58:61],v[192:195],v[128:131]\n"
"s_add_u32 s58,s74,6144\n"
"s_nop 3\n"
LD "v[58:61],v10,s[32:35],s58 offen offset:192\n"
"s_waitcnt vmcnt(0)\n"
MF "v[112:115],v[58:61],v[196:199],v[112:115]\n"
MF "v[116:119],v[58:61],v[200:203],v[116:119]\n"
MF "v[120:123],v[58:61],v[204:207],v[120:123]\n"
MF "v[124:127],v[58:61],v[208:211],v[124:127]\n"
MF "v[128:131],v[58:61],v[212:215],v[128:131]\n"
"s_add_u32 s58,s74,6144\n"
"s_nop 3\n"
LD "v[58:61],v10,s[32:35],s58 offen offset:256\n"
"s_waitcnt vmcnt(0)\n"
MF "v[112:115],v[58:61],v[216:219],v[112:115]\n"
MF "v[116:119],v[58:61],v[220:223],v[116:119]\n"
MF "v[120:123],v[58:61],v[224:227],v[120:123]\n"
MF "v[124:127],v[58:61],v[228:231],v[124:127]\n"
MF "v[128:131],v[58:61],v[232:235],v[128:131]\n"
"s_add_u32 s58,s74,6144\n"
"s_nop 3\n"
LD "v[58:61],v10,s[32:35],s58 offen offset:320\n"
"s_waitcnt vmcnt(0)\n"
MF "v[112:115],v[58:61],v[236:239],v[112:115]\n"
MF "v[116:119],v[58:61],v[240:243],v[116:119]\n"
MF "v[120:123],v[58:61],v[244:247],v[120:123]\n"
MF "v[124:127],v[58:61],v[248:251],v[124:127]\n"
MF "v[128:131],v[58:61],v[252:255],v[128:131]\n"
"s_nop 7\n"
"s_mov_b32 s34,s59\n"
"v_max3_f32 v10,v92,v93,v94\n"
"s_nop 0\n"
"v_max3_f32 v11,v95,v112,v113\n"
"v_max3_f32 v10,v10,v114,v115\n"
"v_max_f32 v10,v10,v11\n"
"v_mov_b32 v11,v10\n"
"s_nop 1\n"
"v_permlane16_swap_b32 v10,v11\n"
"v_max_f32 v10,v10,v11\n"
"v_mov_b32 v11,v10\n"
"s_nop 1\n"
"v_permlane32_swap_b32 v10,v11\n"
"v_max_f32 v10,v10,v11\n"
"v_add_f32 v10,0x41300000,v10\n"
"v_max_f32_e64 v10,0,v10\n"
"v_exp_f32 v11,-v10\n"
AR "v12,a0\n"
"v_subrev_f32 v12,s13,v12\n"
"v_fma_f32 v12,v12,v11,s13\n"
AW "a0,v12\n"
AR "v12,a20\n"
"v_subrev_f32 v12,s13,v12\n"
"v_fma_f32 v12,v12,v11,s13\n"
AW "a20,v12\n"
AR "v12,a40\n"
"v_subrev_f32 v12,s13,v12\n"
"v_fma_f32 v12,v12,v11,s13\n"
AW "a40,v12\n"
AR "v12,a60\n"
"v_subrev_f32 v12,s13,v12\n"
"v_fma_f32 v12,v12,v11,s13\n"
AW "a60,v12\n"
AR "v12,a80\n"
"v_subrev_f32 v12,s13,v12\n"
"v_fma_f32 v12,v12,v11,s13\n"
AW "a80,v12\n"
AR "v12,a100\n"
"v_subrev_f32 v12,s13,v12\n"
"v_fma_f32 v12,v12,v11,s13\n"
AW "a100,v12\n"
AR "v12,a120\n"
"v_subrev_f32 v12,s13,v12\n"
"v_fma_f32 v12,v12,v11,s13\n"
AW "a120,v12\n"
AR "v12,a140\n"
"v_subrev_f32 v12,s13,v12\n"
"v_fma_f32 v12,v12,v11,s13\n"
AW "a140,v12\n"
AR "v12,a160\n"
"v_subrev_f32 v12,s13,v12\n"
"v_fma_f32 v12,v12,v11,s13\n"
AW "a160,v12\n"
AR "v12,a180\n"
"v_subrev_f32 v12,s13,v12\n"
"v_fma_f32 v12,v12,v11,s13\n"
AW "a180,v12\n"
AR "v12,a200\n"
"v_subrev_f32 v12,s13,v12\n"
"v_fma_f32 v12,v12,v11,s13\n"
AW "a200,v12\n"
AR "v12,a220\n"
"v_subrev_f32 v12,s13,v12\n"
"v_fma_f32 v12,v12,v11,s13\n"
AW "a220,v12\n"
AR "v12,a1\n"
"v_subrev_f32 v12,s13,v12\n"
"v_fma_f32 v12,v12,v11,s13\n"
AW "a1,v12\n"
AR "v12,a21\n"
"v_subrev_f32 v12,s13,v12\n"
"v_fma_f32 v12,v12,v11,s13\n"
AW "a21,v12\n"
AR "v12,a41\n"
"v_subrev_f32 v12,s13,v12\n"
"v_fma_f32 v12,v12,v11,s13\n"
AW "a41,v12\n"
AR "v12,a61\n"
"v_subrev_f32 v12,s13,v12\n"
"v_fma_f32 v12,v12,v11,s13\n"
AW "a61,v12\n"
AR "v12,a81\n"
"v_subrev_f32 v12,s13,v12\n"
"v_fma_f32 v12,v12,v11,s13\n"
AW "a81,v12\n"
AR "v12,a101\n"
"v_subrev_f32 v12,s13,v12\n"
"v_fma_f32 v12,v12,v11,s13\n"
AW "a101,v12\n"
AR "v12,a121\n"
"v_subrev_f32 v12,s13,v12\n"
"v_fma_f32 v12,v12,v11,s13\n"
AW "a121,v12\n"
AR "v12,a141\n"
"v_subrev_f32 v12,s13,v12\n"
"v_fma_f32 v12,v12,v11,s13\n"
AW "a141,v12\n"
AR "v12,a161\n"
"v_subrev_f32 v12,s13,v12\n"
"v_fma_f32 v12,v12,v11,s13\n"
AW "a161,v12\n"
AR "v12,a181\n"
"v_subrev_f32 v12,s13,v12\n"
"v_fma_f32 v12,v12,v11,s13\n"
AW "a181,v12\n"
AR "v12,a201\n"
"v_subrev_f32 v12,s13,v12\n"
"v_fma_f32 v12,v12,v11,s13\n"
AW "a201,v12\n"
AR "v12,a221\n"
"v_subrev_f32 v12,s13,v12\n"
"v_fma_f32 v12,v12,v11,s13\n"
AW "a221,v12\n"
AR "v12,a2\n"
"v_subrev_f32 v12,s13,v12\n"
"v_fma_f32 v12,v12,v11,s13\n"
AW "a2,v12\n"
AR "v12,a22\n"
"v_subrev_f32 v12,s13,v12\n"
"v_fma_f32 v12,v12,v11,s13\n"
AW "a22,v12\n"
AR "v12,a42\n"
"v_subrev_f32 v12,s13,v12\n"
"v_fma_f32 v12,v12,v11,s13\n"
AW "a42,v12\n"
AR "v12,a62\n"
"v_subrev_f32 v12,s13,v12\n"
"v_fma_f32 v12,v12,v11,s13\n"
AW "a62,v12\n"
AR "v12,a82\n"
"v_subrev_f32 v12,s13,v12\n"
"v_fma_f32 v12,v12,v11,s13\n"
AW "a82,v12\n"
AR "v12,a102\n"
"v_subrev_f32 v12,s13,v12\n"
"v_fma_f32 v12,v12,v11,s13\n"
AW "a102,v12\n"
AR "v12,a122\n"
"v_subrev_f32 v12,s13,v12\n"
"v_fma_f32 v12,v12,v11,s13\n"
AW "a122,v12\n"
AR "v12,a142\n"
"v_subrev_f32 v12,s13,v12\n"
"v_fma_f32 v12,v12,v11,s13\n"
AW "a142,v12\n"
AR "v12,a162\n"
"v_subrev_f32 v12,s13,v12\n"
"v_fma_f32 v12,v12,v11,s13\n"
AW "a162,v12\n"
AR "v12,a182\n"
"v_subrev_f32 v12,s13,v12\n"
"v_fma_f32 v12,v12,v11,s13\n"
AW "a182,v12\n"
AR "v12,a202\n"
"v_subrev_f32 v12,s13,v12\n"
"v_fma_f32 v12,v12,v11,s13\n"
AW "a202,v12\n"
AR "v12,a222\n"
"v_subrev_f32 v12,s13,v12\n"
"v_fma_f32 v12,v12,v11,s13\n"
AW "a222,v12\n"
AR "v12,a3\n"
"v_subrev_f32 v12,s13,v12\n"
"v_fma_f32 v12,v12,v11,s13\n"
AW "a3,v12\n"
AR "v12,a23\n"
"v_subrev_f32 v12,s13,v12\n"
"v_fma_f32 v12,v12,v11,s13\n"
AW "a23,v12\n"
AR "v12,a43\n"
"v_subrev_f32 v12,s13,v12\n"
"v_fma_f32 v12,v12,v11,s13\n"
AW "a43,v12\n"
AR "v12,a63\n"
"v_subrev_f32 v12,s13,v12\n"
"v_fma_f32 v12,v12,v11,s13\n"
AW "a63,v12\n"
AR "v12,a83\n"
"v_subrev_f32 v12,s13,v12\n"
"v_fma_f32 v12,v12,v11,s13\n"
AW "a83,v12\n"
AR "v12,a103\n"
"v_subrev_f32 v12,s13,v12\n"
"v_fma_f32 v12,v12,v11,s13\n"
AW "a103,v12\n"
AR "v12,a123\n"
"v_subrev_f32 v12,s13,v12\n"
"v_fma_f32 v12,v12,v11,s13\n"
AW "a123,v12\n"
AR "v12,a143\n"
"v_subrev_f32 v12,s13,v12\n"
"v_fma_f32 v12,v12,v11,s13\n"
AW "a143,v12\n"
AR "v12,a163\n"
"v_subrev_f32 v12,s13,v12\n"
"v_fma_f32 v12,v12,v11,s13\n"
AW "a163,v12\n"
AR "v12,a183\n"
"v_subrev_f32 v12,s13,v12\n"
"v_fma_f32 v12,v12,v11,s13\n"
AW "a183,v12\n"
AR "v12,a203\n"
"v_subrev_f32 v12,s13,v12\n"
"v_fma_f32 v12,v12,v11,s13\n"
AW "a203,v12\n"
AR "v12,a223\n"
"v_subrev_f32 v12,s13,v12\n"
"v_fma_f32 v12,v12,v11,s13\n"
AW "a223,v12\n"
"v_mul_f32 v57,v52,v11\n"
"v_sub_f32 v32,v32,v10\n"
"v_sub_f32 v33,v33,v10\n"
"v_sub_f32 v34,v34,v10\n"
"v_sub_f32 v35,v35,v10\n"
"v_sub_f32 v72,v72,v10\n"
"v_sub_f32 v73,v73,v10\n"
"v_sub_f32 v74,v74,v10\n"
"v_sub_f32 v75,v75,v10\n"
"v_sub_f32 v92,v92,v10\n"
"v_sub_f32 v93,v93,v10\n"
"v_sub_f32 v94,v94,v10\n"
"v_sub_f32 v95,v95,v10\n"
"v_sub_f32 v112,v112,v10\n"
"v_sub_f32 v113,v113,v10\n"
"v_sub_f32 v114,v114,v10\n"
"v_sub_f32 v115,v115,v10\n"
"v_exp_f32 v92,v92\n"
"v_exp_f32 v93,v93\n"
"v_exp_f32 v94,v94\n"
"v_exp_f32 v95,v95\n"
"v_exp_f32 v112,v112\n"
"v_exp_f32 v113,v113\n"
"v_exp_f32 v114,v114\n"
"v_exp_f32 v115,v115\n"
"v_add_f32 v57,v57,v92\n"
"v_add_f32 v57,v57,v93\n"
"v_add_f32 v57,v57,v94\n"
"v_add_f32 v57,v57,v95\n"
"v_add_f32 v57,v57,v112\n"
"v_add_f32 v57,v57,v113\n"
"v_add_f32 v57,v57,v114\n"
"v_add_f32_e64 v57,v57,v115\n"
CV "v92,v92,v93\n"
CV "v93,v94,v95\n"
CV "v94,v112,v113\n"
CV "v95,v114,v115\n"
"v_max3_f32 v10,v96,v97,v98\n"
"v_max3_f32 v11,v99,v116,v117\n"
"v_max3_f32 v10,v10,v118,v119\n"
"v_max_f32 v10,v10,v11\n"
"v_mov_b32 v11,v10\n"
"s_nop 1\n"
"v_permlane16_swap_b32 v10,v11\n"
"v_max_f32 v10,v10,v11\n"
"v_mov_b32 v11,v10\n"
"s_nop 1\n"
"v_permlane32_swap_b32 v10,v11\n"
"v_max_f32 v10,v10,v11\n"
"v_add_f32 v10,0x41300000,v10\n"
"v_max_f32 v10,0,v10\n"
"v_exp_f32 v11,-v10\n"
AR "v12,a4\n"
"v_subrev_f32 v12,s13,v12\n"
"v_fma_f32 v12,v12,v11,s13\n"
AW "a4,v12\n"
AR "v12,a24\n"
"v_subrev_f32 v12,s13,v12\n"
"v_fma_f32 v12,v12,v11,s13\n"
AW "a24,v12\n"
AR "v12,a44\n"
"v_subrev_f32 v12,s13,v12\n"
"v_fma_f32 v12,v12,v11,s13\n"
AW "a44,v12\n"
AR "v12,a64\n"
"v_subrev_f32 v12,s13,v12\n"
"v_fma_f32 v12,v12,v11,s13\n"
AW "a64,v12\n"
AR "v12,a84\n"
"v_subrev_f32 v12,s13,v12\n"
"v_fma_f32 v12,v12,v11,s13\n"
AW "a84,v12\n"
AR "v12,a104\n"
"v_subrev_f32 v12,s13,v12\n"
"v_fma_f32 v12,v12,v11,s13\n"
AW "a104,v12\n"
AR "v12,a124\n"
"v_subrev_f32 v12,s13,v12\n"
"v_fma_f32 v12,v12,v11,s13\n"
AW "a124,v12\n"
AR "v12,a144\n"
"v_subrev_f32 v12,s13,v12\n"
"v_fma_f32 v12,v12,v11,s13\n"
AW "a144,v12\n"
AR "v12,a164\n"
"v_subrev_f32 v12,s13,v12\n"
"v_fma_f32 v12,v12,v11,s13\n"
AW "a164,v12\n"
AR "v12,a184\n"
"v_subrev_f32 v12,s13,v12\n"
"v_fma_f32 v12,v12,v11,s13\n"
AW "a184,v12\n"
AR "v12,a204\n"
"v_subrev_f32 v12,s13,v12\n"
"v_fma_f32 v12,v12,v11,s13\n"
AW "a204,v12\n"
AR "v12,a224\n"
"v_subrev_f32 v12,s13,v12\n"
"v_fma_f32 v12,v12,v11,s13\n"
AW "a224,v12\n"
AR "v12,a5\n"
"v_subrev_f32 v12,s13,v12\n"
"v_fma_f32 v12,v12,v11,s13\n"
AW "a5,v12\n"
AR "v12,a25\n"
"v_subrev_f32 v12,s13,v12\n"
"v_fma_f32 v12,v12,v11,s13\n"
AW "a25,v12\n"
AR "v12,a45\n"
"v_subrev_f32 v12,s13,v12\n"
"v_fma_f32 v12,v12,v11,s13\n"
AW "a45,v12\n"
AR "v12,a65\n"
"v_subrev_f32 v12,s13,v12\n"
"v_fma_f32 v12,v12,v11,s13\n"
AW "a65,v12\n"
AR "v12,a85\n"
"v_subrev_f32 v12,s13,v12\n"
"v_fma_f32 v12,v12,v11,s13\n"
AW "a85,v12\n"
AR "v12,a105\n"
"v_subrev_f32 v12,s13,v12\n"
"v_fma_f32 v12,v12,v11,s13\n"
AW "a105,v12\n"
AR "v12,a125\n"
"v_subrev_f32 v12,s13,v12\n"
"v_fma_f32 v12,v12,v11,s13\n"
AW "a125,v12\n"
AR "v12,a145\n"
"v_subrev_f32 v12,s13,v12\n"
"v_fma_f32 v12,v12,v11,s13\n"
AW "a145,v12\n"
AR "v12,a165\n"
"v_subrev_f32 v12,s13,v12\n"
"v_fma_f32 v12,v12,v11,s13\n"
AW "a165,v12\n"
AR "v12,a185\n"
"v_subrev_f32 v12,s13,v12\n"
"v_fma_f32 v12,v12,v11,s13\n"
AW "a185,v12\n"
AR "v12,a205\n"
"v_subrev_f32 v12,s13,v12\n"
"v_fma_f32 v12,v12,v11,s13\n"
AW "a205,v12\n"
AR "v12,a225\n"
"v_subrev_f32 v12,s13,v12\n"
"v_fma_f32 v12,v12,v11,s13\n"
AW "a225,v12\n"
AR "v12,a6\n"
"v_subrev_f32 v12,s13,v12\n"
"v_fma_f32 v12,v12,v11,s13\n"
AW "a6,v12\n"
AR "v12,a26\n"
"v_subrev_f32 v12,s13,v12\n"
"v_fma_f32 v12,v12,v11,s13\n"
AW "a26,v12\n"
AR "v12,a46\n"
"v_subrev_f32 v12,s13,v12\n"
"v_fma_f32 v12,v12,v11,s13\n"
AW "a46,v12\n"
AR "v12,a66\n"
"v_subrev_f32 v12,s13,v12\n"
"v_fma_f32 v12,v12,v11,s13\n"
AW "a66,v12\n"
AR "v12,a86\n"
"v_subrev_f32 v12,s13,v12\n"
"v_fma_f32 v12,v12,v11,s13\n"
AW "a86,v12\n"
AR "v12,a106\n"
"v_subrev_f32 v12,s13,v12\n"
"v_fma_f32 v12,v12,v11,s13\n"
AW "a106,v12\n"
AR "v12,a126\n"
"v_subrev_f32 v12,s13,v12\n"
"v_fma_f32 v12,v12,v11,s13\n"
AW "a126,v12\n"
AR "v12,a146\n"
"v_subrev_f32 v12,s13,v12\n"
"v_fma_f32 v12,v12,v11,s13\n"
AW "a146,v12\n"
AR "v12,a166\n"
"v_subrev_f32 v12,s13,v12\n"
"v_fma_f32 v12,v12,v11,s13\n"
AW "a166,v12\n"
AR "v12,a186\n"
"v_subrev_f32 v12,s13,v12\n"
"v_fma_f32 v12,v12,v11,s13\n"
AW "a186,v12\n"
AR "v12,a206\n"
"v_subrev_f32 v12,s13,v12\n"
"v_fma_f32 v12,v12,v11,s13\n"
AW "a206,v12\n"
AR "v12,a226\n"
"v_subrev_f32 v12,s13,v12\n"
"v_fma_f32 v12,v12,v11,s13\n"
AW "a226,v12\n"
AR "v12,a7\n"
"v_subrev_f32 v12,s13,v12\n"
"v_fma_f32 v12,v12,v11,s13\n"
AW "a7,v12\n"
AR "v12,a27\n"
"v_subrev_f32 v12,s13,v12\n"
"v_fma_f32 v12,v12,v11,s13\n"
AW "a27,v12\n"
AR "v12,a47\n"
"v_subrev_f32 v12,s13,v12\n"
"v_fma_f32 v12,v12,v11,s13\n"
AW "a47,v12\n"
AR "v12,a67\n"
"v_subrev_f32 v12,s13,v12\n"
"v_fma_f32 v12,v12,v11,s13\n"
AW "a67,v12\n"
AR "v12,a87\n"
"v_subrev_f32 v12,s13,v12\n"
"v_fma_f32 v12,v12,v11,s13\n"
AW "a87,v12\n"
AR "v12,a107\n"
"v_subrev_f32 v12,s13,v12\n"
"v_fma_f32 v12,v12,v11,s13\n"
AW "a107,v12\n"
AR "v12,a127\n"
"v_subrev_f32 v12,s13,v12\n"
"v_fma_f32 v12,v12,v11,s13\n"
AW "a127,v12\n"
AR "v12,a147\n"
"v_subrev_f32 v12,s13,v12\n"
"v_fma_f32 v12,v12,v11,s13\n"
AW "a147,v12\n"
AR "v12,a167\n"
"v_subrev_f32 v12,s13,v12\n"
"v_fma_f32 v12,v12,v11,s13\n"
AW "a167,v12\n"
AR "v12,a187\n"
"v_subrev_f32 v12,s13,v12\n"
"v_fma_f32 v12,v12,v11,s13\n"
AW "a187,v12\n"
AR "v12,a207\n"
"v_subrev_f32 v12,s13,v12\n"
"v_fma_f32 v12,v12,v11,s13\n"
AW "a207,v12\n"
AR "v12,a227\n"
"v_subrev_f32 v12,s13,v12\n"
"v_fma_f32 v12,v12,v11,s13\n"
AW "a227,v12\n"
"v_mul_f32 v58,v53,v11\n"
"v_sub_f32 v36,v36,v10\n"
"v_sub_f32 v37,v37,v10\n"
"v_sub_f32 v38,v38,v10\n"
"v_sub_f32 v39,v39,v10\n"
"v_sub_f32 v76,v76,v10\n"
"v_sub_f32 v77,v77,v10\n"
"v_sub_f32 v78,v78,v10\n"
"v_sub_f32 v79,v79,v10\n"
"v_sub_f32 v96,v96,v10\n"
"v_sub_f32 v97,v97,v10\n"
"v_sub_f32 v98,v98,v10\n"
"v_sub_f32 v99,v99,v10\n"
"v_sub_f32 v116,v116,v10\n"
"v_sub_f32 v117,v117,v10\n"
"v_sub_f32 v118,v118,v10\n"
"v_sub_f32 v119,v119,v10\n"
"v_exp_f32 v96,v96\n"
"v_exp_f32 v97,v97\n"
"v_exp_f32 v98,v98\n"
"v_exp_f32 v99,v99\n"
"v_exp_f32 v116,v116\n"
"v_exp_f32 v117,v117\n"
"v_exp_f32 v118,v118\n"
"v_exp_f32 v119,v119\n"
"v_add_f32 v58,v58,v96\n"
"v_add_f32 v58,v58,v97\n"
"v_add_f32 v58,v58,v98\n"
"v_add_f32 v58,v58,v99\n"
"v_add_f32 v58,v58,v116\n"
"v_add_f32 v58,v58,v117\n"
"v_add_f32 v58,v58,v118\n"
"v_add_f32_e64 v58,v58,v119\n"
CV "v96,v96,v97\n"
CV "v97,v98,v99\n"
CV "v98,v116,v117\n"
CV "v99,v118,v119\n"
"v_max3_f32 v10,v100,v101,v102\n"
"v_max3_f32 v11,v103,v120,v121\n"
"v_max3_f32 v10,v10,v122,v123\n"
"v_max_f32 v10,v10,v11\n"
"v_mov_b32 v11,v10\n"
"s_nop 1\n"
"v_permlane16_swap_b32 v10,v11\n"
"v_max_f32 v10,v10,v11\n"
"v_mov_b32 v11,v10\n"
"s_nop 1\n"
"v_permlane32_swap_b32 v10,v11\n"
"v_max_f32_e64 v10,v10,v11\n"
"v_add_f32 v10,0x41300000,v10\n"
"v_max_f32 v10,0,v10\n"
"v_exp_f32 v11,-v10\n"
AR "v12,a8\n"
"v_subrev_f32 v12,s13,v12\n"
"v_fma_f32 v12,v12,v11,s13\n"
AW "a8,v12\n"
AR "v12,a28\n"
"v_subrev_f32 v12,s13,v12\n"
"v_fma_f32 v12,v12,v11,s13\n"
AW "a28,v12\n"
AR "v12,a48\n"
"v_subrev_f32 v12,s13,v12\n"
"v_fma_f32 v12,v12,v11,s13\n"
AW "a48,v12\n"
AR "v12,a68\n"
"v_subrev_f32 v12,s13,v12\n"
"v_fma_f32 v12,v12,v11,s13\n"
AW "a68,v12\n"
AR "v12,a88\n"
"v_subrev_f32 v12,s13,v12\n"
"v_fma_f32 v12,v12,v11,s13\n"
AW "a88,v12\n"
AR "v12,a108\n"
"v_subrev_f32 v12,s13,v12\n"
"v_fma_f32 v12,v12,v11,s13\n"
AW "a108,v12\n"
AR "v12,a128\n"
"v_subrev_f32 v12,s13,v12\n"
"v_fma_f32 v12,v12,v11,s13\n"
AW "a128,v12\n"
AR "v12,a148\n"
"v_subrev_f32 v12,s13,v12\n"
"v_fma_f32 v12,v12,v11,s13\n"
AW "a148,v12\n"
AR "v12,a168\n"
"v_subrev_f32 v12,s13,v12\n"
"v_fma_f32 v12,v12,v11,s13\n"
AW "a168,v12\n"
AR "v12,a188\n"
"v_subrev_f32 v12,s13,v12\n"
"v_fma_f32 v12,v12,v11,s13\n"
AW "a188,v12\n"
AR "v12,a208\n"
"v_subrev_f32 v12,s13,v12\n"
"v_fma_f32 v12,v12,v11,s13\n"
AW "a208,v12\n"
AR "v12,a228\n"
"v_subrev_f32 v12,s13,v12\n"
"v_fma_f32 v12,v12,v11,s13\n"
AW "a228,v12\n"
AR "v12,a9\n"
"v_subrev_f32 v12,s13,v12\n"
"v_fma_f32 v12,v12,v11,s13\n"
AW "a9,v12\n"
AR "v12,a29\n"
"v_subrev_f32 v12,s13,v12\n"
"v_fma_f32 v12,v12,v11,s13\n"
AW "a29,v12\n"
AR "v12,a49\n"
"v_subrev_f32 v12,s13,v12\n"
"v_fma_f32 v12,v12,v11,s13\n"
AW "a49,v12\n"
AR "v12,a69\n"
"v_subrev_f32 v12,s13,v12\n"
"v_fma_f32 v12,v12,v11,s13\n"
AW "a69,v12\n"
AR "v12,a89\n"
"v_subrev_f32 v12,s13,v12\n"
"v_fma_f32 v12,v12,v11,s13\n"
AW "a89,v12\n"
AR "v12,a109\n"
"v_subrev_f32 v12,s13,v12\n"
"v_fma_f32 v12,v12,v11,s13\n"
AW "a109,v12\n"
AR "v12,a129\n"
"v_subrev_f32 v12,s13,v12\n"
"v_fma_f32 v12,v12,v11,s13\n"
AW "a129,v12\n"
AR "v12,a149\n"
"v_subrev_f32 v12,s13,v12\n"
"v_fma_f32 v12,v12,v11,s13\n"
AW "a149,v12\n"
AR "v12,a169\n"
"v_subrev_f32 v12,s13,v12\n"
"v_fma_f32 v12,v12,v11,s13\n"
AW "a169,v12\n"
AR "v12,a189\n"
"v_subrev_f32 v12,s13,v12\n"
"v_fma_f32 v12,v12,v11,s13\n"
AW "a189,v12\n"
AR "v12,a209\n"
"v_subrev_f32 v12,s13,v12\n"
"v_fma_f32 v12,v12,v11,s13\n"
AW "a209,v12\n"
AR "v12,a229\n"
"v_subrev_f32 v12,s13,v12\n"
"v_fma_f32 v12,v12,v11,s13\n"
AW "a229,v12\n"
AR "v12,a10\n"
"v_subrev_f32 v12,s13,v12\n"
"v_fma_f32 v12,v12,v11,s13\n"
AW "a10,v12\n"
AR "v12,a30\n"
"v_subrev_f32 v12,s13,v12\n"
"v_fma_f32 v12,v12,v11,s13\n"
AW "a30,v12\n"
AR "v12,a50\n"
"v_subrev_f32 v12,s13,v12\n"
"v_fma_f32 v12,v12,v11,s13\n"
AW "a50,v12\n"
AR "v12,a70\n"
"v_subrev_f32 v12,s13,v12\n"
"v_fma_f32 v12,v12,v11,s13\n"
AW "a70,v12\n"
AR "v12,a90\n"
"v_subrev_f32 v12,s13,v12\n"
"v_fma_f32 v12,v12,v11,s13\n"
AW "a90,v12\n"
AR "v12,a110\n"
"v_subrev_f32 v12,s13,v12\n"
"v_fma_f32 v12,v12,v11,s13\n"
AW "a110,v12\n"
AR "v12,a130\n"
"v_subrev_f32 v12,s13,v12\n"
"v_fma_f32 v12,v12,v11,s13\n"
AW "a130,v12\n"
AR "v12,a150\n"
"v_subrev_f32 v12,s13,v12\n"
"v_fma_f32 v12,v12,v11,s13\n"
AW "a150,v12\n"
AR "v12,a170\n"
"v_subrev_f32 v12,s13,v12\n"
"v_fma_f32 v12,v12,v11,s13\n"
AW "a170,v12\n"
AR "v12,a190\n"
"v_subrev_f32 v12,s13,v12\n"
"v_fma_f32 v12,v12,v11,s13\n"
AW "a190,v12\n"
AR "v12,a210\n"
"v_subrev_f32 v12,s13,v12\n"
"v_fma_f32 v12,v12,v11,s13\n"
AW "a210,v12\n"
AR "v12,a230\n"
"v_subrev_f32 v12,s13,v12\n"
"v_fma_f32 v12,v12,v11,s13\n"
AW "a230,v12\n"
AR "v12,a11\n"
"v_subrev_f32 v12,s13,v12\n"
"v_fma_f32 v12,v12,v11,s13\n"
AW "a11,v12\n"
AR "v12,a31\n"
"v_subrev_f32 v12,s13,v12\n"
"v_fma_f32 v12,v12,v11,s13\n"
AW "a31,v12\n"
AR "v12,a51\n"
"v_subrev_f32 v12,s13,v12\n"
"v_fma_f32 v12,v12,v11,s13\n"
AW "a51,v12\n"
AR "v12,a71\n"
"v_subrev_f32 v12,s13,v12\n"
"v_fma_f32 v12,v12,v11,s13\n"
AW "a71,v12\n"
AR "v12,a91\n"
"v_subrev_f32 v12,s13,v12\n"
"v_fma_f32 v12,v12,v11,s13\n"
AW "a91,v12\n"
AR "v12,a111\n"
"v_subrev_f32 v12,s13,v12\n"
"v_fma_f32 v12,v12,v11,s13\n"
AW "a111,v12\n"
AR "v12,a131\n"
"v_subrev_f32 v12,s13,v12\n"
"v_fma_f32 v12,v12,v11,s13\n"
AW "a131,v12\n"
AR "v12,a151\n"
"v_subrev_f32 v12,s13,v12\n"
"v_fma_f32 v12,v12,v11,s13\n"
AW "a151,v12\n"
AR "v12,a171\n"
"v_subrev_f32 v12,s13,v12\n"
"v_fma_f32 v12,v12,v11,s13\n"
AW "a171,v12\n"
AR "v12,a191\n"
"v_subrev_f32 v12,s13,v12\n"
"v_fma_f32 v12,v12,v11,s13\n"
AW "a191,v12\n"
AR "v12,a211\n"
"v_subrev_f32 v12,s13,v12\n"
"v_fma_f32 v12,v12,v11,s13\n"
AW "a211,v12\n"
AR "v12,a231\n"
"v_subrev_f32 v12,s13,v12\n"
"v_fma_f32 v12,v12,v11,s13\n"
AW "a231,v12\n"
"v_mul_f32 v59,v54,v11\n"
"v_sub_f32 v40,v40,v10\n"
"v_sub_f32 v41,v41,v10\n"
"v_sub_f32 v42,v42,v10\n"
"v_sub_f32 v43,v43,v10\n"
"v_sub_f32 v80,v80,v10\n"
"v_sub_f32 v81,v81,v10\n"
"v_sub_f32 v82,v82,v10\n"
"v_sub_f32 v83,v83,v10\n"
"v_sub_f32 v100,v100,v10\n"
"v_sub_f32 v101,v101,v10\n"
"v_sub_f32 v102,v102,v10\n"
"v_sub_f32 v103,v103,v10\n"
"v_sub_f32 v120,v120,v10\n"
"v_sub_f32 v121,v121,v10\n"
"v_sub_f32 v122,v122,v10\n"
"v_sub_f32 v123,v123,v10\n"
"v_exp_f32 v100,v100\n"
"v_exp_f32 v101,v101\n"
"v_exp_f32 v102,v102\n"
"v_exp_f32 v103,v103\n"
"v_exp_f32 v120,v120\n"
"v_exp_f32 v121,v121\n"
"v_exp_f32 v122,v122\n"
"v_exp_f32 v123,v123\n"
"v_add_f32 v59,v59,v100\n"
"v_add_f32 v59,v59,v101\n"
"v_add_f32 v59,v59,v102\n"
"v_add_f32 v59,v59,v103\n"
"v_add_f32 v59,v59,v120\n"
"v_add_f32 v59,v59,v121\n"
"v_add_f32 v59,v59,v122\n"
"v_add_f32 v59,v59,v123\n"
CV "v100,v100,v101\n"
CV "v101,v102,v103\n"
CV "v102,v120,v121\n"
CV "v103,v122,v123\n"
"v_max3_f32 v10,v104,v105,v106\n"
"v_max3_f32 v11,v107,v124,v125\n"
"v_max3_f32 v10,v10,v126,v127\n"
"v_max_f32 v10,v10,v11\n"
"v_mov_b32 v11,v10\n"
"s_nop 1\n"
"v_permlane16_swap_b32 v10,v11\n"
"v_max_f32 v10,v10,v11\n"
"v_mov_b32 v11,v10\n"
"s_nop 1\n"
"v_permlane32_swap_b32 v10,v11\n"
"v_max_f32 v10,v10,v11\n"
"v_add_f32 v10,0x41300000,v10\n"
"v_max_f32 v10,0,v10\n"
"v_exp_f32 v11,-v10\n"
AR "v12,a12\n"
"v_subrev_f32 v12,s13,v12\n"
"v_fma_f32 v12,v12,v11,s13\n"
AW "a12,v12\n"
AR "v12,a32\n"
"v_subrev_f32 v12,s13,v12\n"
"v_fma_f32 v12,v12,v11,s13\n"
AW "a32,v12\n"
AR "v12,a52\n"
"v_subrev_f32 v12,s13,v12\n"
"v_fma_f32 v12,v12,v11,s13\n"
AW "a52,v12\n"
AR "v12,a72\n"
"v_subrev_f32 v12,s13,v12\n"
"v_fma_f32 v12,v12,v11,s13\n"
AW "a72,v12\n"
AR "v12,a92\n"
"v_subrev_f32 v12,s13,v12\n"
"v_fma_f32 v12,v12,v11,s13\n"
AW "a92,v12\n"
AR "v12,a112\n"
"v_subrev_f32 v12,s13,v12\n"
"v_fma_f32 v12,v12,v11,s13\n"
AW "a112,v12\n"
AR "v12,a132\n"
"v_subrev_f32 v12,s13,v12\n"
"v_fma_f32 v12,v12,v11,s13\n"
AW "a132,v12\n"
AR "v12,a152\n"
"v_subrev_f32 v12,s13,v12\n"
"v_fma_f32 v12,v12,v11,s13\n"
AW "a152,v12\n"
AR "v12,a172\n"
"v_subrev_f32 v12,s13,v12\n"
"v_fma_f32 v12,v12,v11,s13\n"
AW "a172,v12\n"
AR "v12,a192\n"
"v_subrev_f32 v12,s13,v12\n"
"v_fma_f32 v12,v12,v11,s13\n"
AW "a192,v12\n"
AR "v12,a212\n"
"v_subrev_f32 v12,s13,v12\n"
"v_fma_f32 v12,v12,v11,s13\n"
AW "a212,v12\n"
AR "v12,a232\n"
"v_subrev_f32 v12,s13,v12\n"
"v_fma_f32 v12,v12,v11,s13\n"
AW "a232,v12\n"
AR "v12,a13\n"
"v_subrev_f32 v12,s13,v12\n"
"v_fma_f32 v12,v12,v11,s13\n"
AW "a13,v12\n"
AR "v12,a33\n"
"v_subrev_f32 v12,s13,v12\n"
"v_fma_f32 v12,v12,v11,s13\n"
AW "a33,v12\n"
AR "v12,a53\n"
"v_subrev_f32 v12,s13,v12\n"
"v_fma_f32 v12,v12,v11,s13\n"
AW "a53,v12\n"
AR "v12,a73\n"
"v_subrev_f32 v12,s13,v12\n"
"v_fma_f32 v12,v12,v11,s13\n"
AW "a73,v12\n"
AR "v12,a93\n"
"v_subrev_f32 v12,s13,v12\n"
"v_fma_f32 v12,v12,v11,s13\n"
AW "a93,v12\n"
AR "v12,a113\n"
"v_subrev_f32 v12,s13,v12\n"
"v_fma_f32 v12,v12,v11,s13\n"
AW "a113,v12\n"
AR "v12,a133\n"
"v_subrev_f32 v12,s13,v12\n"
"v_fma_f32 v12,v12,v11,s13\n"
AW "a133,v12\n"
AR "v12,a153\n"
"v_subrev_f32 v12,s13,v12\n"
"v_fma_f32 v12,v12,v11,s13\n"
AW "a153,v12\n"
AR "v12,a173\n"
"v_subrev_f32 v12,s13,v12\n"
"v_fma_f32 v12,v12,v11,s13\n"
AW "a173,v12\n"
AR "v12,a193\n"
"v_subrev_f32 v12,s13,v12\n"
"v_fma_f32 v12,v12,v11,s13\n"
AW "a193,v12\n"
AR "v12,a213\n"
"v_subrev_f32 v12,s13,v12\n"
"v_fma_f32 v12,v12,v11,s13\n"
AW "a213,v12\n"
AR "v12,a233\n"
"v_subrev_f32 v12,s13,v12\n"
"v_fma_f32 v12,v12,v11,s13\n"
AW "a233,v12\n"
AR "v12,a14\n"
"v_subrev_f32 v12,s13,v12\n"
"v_fma_f32 v12,v12,v11,s13\n"
AW "a14,v12\n"
AR "v12,a34\n"
"v_subrev_f32 v12,s13,v12\n"
"v_fma_f32 v12,v12,v11,s13\n"
AW "a34,v12\n"
AR "v12,a54\n"
"v_subrev_f32 v12,s13,v12\n"
"v_fma_f32 v12,v12,v11,s13\n"
AW "a54,v12\n"
AR "v12,a74\n"
"v_subrev_f32 v12,s13,v12\n"
"v_fma_f32 v12,v12,v11,s13\n"
AW "a74,v12\n"
AR "v12,a94\n"
"v_subrev_f32 v12,s13,v12\n"
"v_fma_f32 v12,v12,v11,s13\n"
AW "a94,v12\n"
AR "v12,a114\n"
"v_subrev_f32 v12,s13,v12\n"
"v_fma_f32 v12,v12,v11,s13\n"
AW "a114,v12\n"
AR "v12,a134\n"
"v_subrev_f32 v12,s13,v12\n"
"v_fma_f32 v12,v12,v11,s13\n"
AW "a134,v12\n"
AR "v12,a154\n"
"v_subrev_f32 v12,s13,v12\n"
"v_fma_f32 v12,v12,v11,s13\n"
AW "a154,v12\n"
AR "v12,a174\n"
"v_subrev_f32 v12,s13,v12\n"
"v_fma_f32 v12,v12,v11,s13\n"
AW "a174,v12\n"
AR "v12,a194\n"
"v_subrev_f32 v12,s13,v12\n"
"v_fma_f32 v12,v12,v11,s13\n"
AW "a194,v12\n"
AR "v12,a214\n"
"v_subrev_f32 v12,s13,v12\n"
"v_fma_f32 v12,v12,v11,s13\n"
AW "a214,v12\n"
AR "v12,a234\n"
"v_subrev_f32 v12,s13,v12\n"
"v_fma_f32 v12,v12,v11,s13\n"
AW "a234,v12\n"
AR "v12,a15\n"
"v_subrev_f32 v12,s13,v12\n"
"v_fma_f32 v12,v12,v11,s13\n"
AW "a15,v12\n"
AR "v12,a35\n"
"v_subrev_f32 v12,s13,v12\n"
"v_fma_f32 v12,v12,v11,s13\n"
AW "a35,v12\n"
AR "v12,a55\n"
"v_subrev_f32 v12,s13,v12\n"
"v_fma_f32 v12,v12,v11,s13\n"
AW "a55,v12\n"
AR "v12,a75\n"
"v_subrev_f32 v12,s13,v12\n"
"v_fma_f32 v12,v12,v11,s13\n"
AW "a75,v12\n"
AR "v12,a95\n"
"v_subrev_f32 v12,s13,v12\n"
"v_fma_f32 v12,v12,v11,s13\n"
AW "a95,v12\n"
AR "v12,a115\n"
"v_subrev_f32 v12,s13,v12\n"
"v_fma_f32 v12,v12,v11,s13\n"
AW "a115,v12\n"
AR "v12,a135\n"
"v_subrev_f32 v12,s13,v12\n"
"v_fma_f32 v12,v12,v11,s13\n"
AW "a135,v12\n"
AR "v12,a155\n"
"v_subrev_f32 v12,s13,v12\n"
"v_fma_f32 v12,v12,v11,s13\n"
AW "a155,v12\n"
AR "v12,a175\n"
"v_subrev_f32 v12,s13,v12\n"
"v_fma_f32 v12,v12,v11,s13\n"
AW "a175,v12\n"
AR "v12,a195\n"
"v_subrev_f32 v12,s13,v12\n"
"v_fma_f32 v12,v12,v11,s13\n"
AW "a195,v12\n"
AR "v12,a215\n"
"v_subrev_f32 v12,s13,v12\n"
"v_fma_f32 v12,v12,v11,s13\n"
AW "a215,v12\n"
AR "v12,a235\n"
"v_subrev_f32 v12,s13,v12\n"
"v_fma_f32 v12,v12,v11,s13\n"
AW "a235,v12\n"
"v_mul_f32 v60,v55,v11\n"
"v_sub_f32 v44,v44,v10\n"
"v_sub_f32 v45,v45,v10\n"
"v_sub_f32 v46,v46,v10\n"
"v_sub_f32 v47,v47,v10\n"
"v_sub_f32 v84,v84,v10\n"
"v_sub_f32 v85,v85,v10\n"
"v_sub_f32 v86,v86,v10\n"
"v_sub_f32 v87,v87,v10\n"
"v_sub_f32 v104,v104,v10\n"
"v_sub_f32 v105,v105,v10\n"
"v_sub_f32 v106,v106,v10\n"
"v_sub_f32 v107,v107,v10\n"
"v_sub_f32 v124,v124,v10\n"
"v_sub_f32 v125,v125,v10\n"
"v_sub_f32 v126,v126,v10\n"
"v_sub_f32 v127,v127,v10\n"
"v_exp_f32 v104,v104\n"
"v_exp_f32 v105,v105\n"
"v_exp_f32 v106,v106\n"
"v_exp_f32 v107,v107\n"
"v_exp_f32 v124,v124\n"
"v_exp_f32 v125,v125\n"
"v_exp_f32 v126,v126\n"
"v_exp_f32 v127,v127\n"
"v_add_f32 v60,v60,v104\n"
"v_add_f32 v60,v60,v105\n"
"v_add_f32 v60,v60,v106\n"
"v_add_f32 v60,v60,v107\n"
"v_add_f32 v60,v60,v124\n"
"v_add_f32 v60,v60,v125\n"
"v_add_f32 v60,v60,v126\n"
"v_add_f32_e64 v60,v60,v127\n"
CV "v104,v104,v105\n"
CV "v105,v106,v107\n"
CV "v106,v124,v125\n"
CV "v107,v126,v127\n"
"v_max3_f32 v10,v108,v109,v110\n"
"v_max3_f32 v11,v111,v128,v129\n"
"v_max3_f32 v10,v10,v130,v131\n"
"v_max_f32 v10,v10,v11\n"
"v_mov_b32 v11,v10\n"
"s_nop 1\n"
"v_permlane16_swap_b32 v10,v11\n"
"v_max_f32 v10,v10,v11\n"
"v_mov_b32 v11,v10\n"
"s_nop 1\n"
"v_permlane32_swap_b32 v10,v11\n"
"v_max_f32 v10,v10,v11\n"
"v_add_f32 v10,0x41300000,v10\n"
"v_max_f32 v10,0,v10\n"
"v_exp_f32 v11,-v10\n"
AR "v12,a16\n"
"v_subrev_f32 v12,s13,v12\n"
"v_fma_f32 v12,v12,v11,s13\n"
AW "a16,v12\n"
AR "v12,a36\n"
"v_subrev_f32 v12,s13,v12\n"
"v_fma_f32 v12,v12,v11,s13\n"
AW "a36,v12\n"
AR "v12,a56\n"
"v_subrev_f32 v12,s13,v12\n"
"v_fma_f32 v12,v12,v11,s13\n"
AW "a56,v12\n"
AR "v12,a76\n"
"v_subrev_f32 v12,s13,v12\n"
"v_fma_f32 v12,v12,v11,s13\n"
AW "a76,v12\n"
AR "v12,a96\n"
"v_subrev_f32 v12,s13,v12\n"
"v_fma_f32 v12,v12,v11,s13\n"
AW "a96,v12\n"
AR "v12,a116\n"
"v_subrev_f32 v12,s13,v12\n"
"v_fma_f32 v12,v12,v11,s13\n"
AW "a116,v12\n"
AR "v12,a136\n"
"v_subrev_f32 v12,s13,v12\n"
"v_fma_f32 v12,v12,v11,s13\n"
AW "a136,v12\n"
AR "v12,a156\n"
"v_subrev_f32 v12,s13,v12\n"
"v_fma_f32 v12,v12,v11,s13\n"
AW "a156,v12\n"
AR "v12,a176\n"
"v_subrev_f32 v12,s13,v12\n"
"v_fma_f32 v12,v12,v11,s13\n"
AW "a176,v12\n"
AR "v12,a196\n"
"v_subrev_f32 v12,s13,v12\n"
"v_fma_f32 v12,v12,v11,s13\n"
AW "a196,v12\n"
AR "v12,a216\n"
"v_subrev_f32 v12,s13,v12\n"
"v_fma_f32 v12,v12,v11,s13\n"
AW "a216,v12\n"
AR "v12,a236\n"
"v_subrev_f32 v12,s13,v12\n"
"v_fma_f32 v12,v12,v11,s13\n"
AW "a236,v12\n"
AR "v12,a17\n"
"v_subrev_f32 v12,s13,v12\n"
"v_fma_f32 v12,v12,v11,s13\n"
AW "a17,v12\n"
AR "v12,a37\n"
"v_subrev_f32 v12,s13,v12\n"
"v_fma_f32 v12,v12,v11,s13\n"
AW "a37,v12\n"
AR "v12,a57\n"
"v_subrev_f32 v12,s13,v12\n"
"v_fma_f32 v12,v12,v11,s13\n"
AW "a57,v12\n"
AR "v12,a77\n"
"v_subrev_f32 v12,s13,v12\n"
"v_fma_f32 v12,v12,v11,s13\n"
AW "a77,v12\n"
AR "v12,a97\n"
"v_subrev_f32 v12,s13,v12\n"
"v_fma_f32 v12,v12,v11,s13\n"
AW "a97,v12\n"
AR "v12,a117\n"
"v_subrev_f32 v12,s13,v12\n"
"v_fma_f32 v12,v12,v11,s13\n"
AW "a117,v12\n"
AR "v12,a137\n"
"v_subrev_f32 v12,s13,v12\n"
"v_fma_f32 v12,v12,v11,s13\n"
AW "a137,v12\n"
AR "v12,a157\n"
"v_subrev_f32 v12,s13,v12\n"
"v_fma_f32 v12,v12,v11,s13\n"
AW "a157,v12\n"
AR "v12,a177\n"
"v_subrev_f32 v12,s13,v12\n"
"v_fma_f32 v12,v12,v11,s13\n"
AW "a177,v12\n"
AR "v12,a197\n"
"v_subrev_f32 v12,s13,v12\n"
"v_fma_f32 v12,v12,v11,s13\n"
AW "a197,v12\n"
AR "v12,a217\n"
"v_subrev_f32 v12,s13,v12\n"
"v_fma_f32 v12,v12,v11,s13\n"
AW "a217,v12\n"
AR "v12,a237\n"
"v_subrev_f32 v12,s13,v12\n"
"v_fma_f32 v12,v12,v11,s13\n"
AW "a237,v12\n"
AR "v12,a18\n"
"v_subrev_f32 v12,s13,v12\n"
"v_fma_f32 v12,v12,v11,s13\n"
AW "a18,v12\n"
AR "v12,a38\n"
"v_subrev_f32 v12,s13,v12\n"
"v_fma_f32 v12,v12,v11,s13\n"
AW "a38,v12\n"
AR "v12,a58\n"
"v_subrev_f32 v12,s13,v12\n"
"v_fma_f32 v12,v12,v11,s13\n"
AW "a58,v12\n"
AR "v12,a78\n"
"v_subrev_f32 v12,s13,v12\n"
"v_fma_f32 v12,v12,v11,s13\n"
AW "a78,v12\n"
AR "v12,a98\n"
"v_subrev_f32 v12,s13,v12\n"
"v_fma_f32 v12,v12,v11,s13\n"
AW "a98,v12\n"
AR "v12,a118\n"
"v_subrev_f32 v12,s13,v12\n"
"v_fma_f32 v12,v12,v11,s13\n"
AW "a118,v12\n"
AR "v12,a138\n"
"v_subrev_f32 v12,s13,v12\n"
"v_fma_f32 v12,v12,v11,s13\n"
AW "a138,v12\n"
AR "v12,a158\n"
"v_subrev_f32 v12,s13,v12\n"
"v_fma_f32 v12,v12,v11,s13\n"
AW "a158,v12\n"
AR "v12,a178\n"
"v_subrev_f32 v12,s13,v12\n"
"v_fma_f32 v12,v12,v11,s13\n"
AW "a178,v12\n"
AR "v12,a198\n"
"v_subrev_f32 v12,s13,v12\n"
"v_fma_f32 v12,v12,v11,s13\n"
AW "a198,v12\n"
AR "v12,a218\n"
"v_subrev_f32 v12,s13,v12\n"
"v_fma_f32 v12,v12,v11,s13\n"
AW "a218,v12\n"
AR "v12,a238\n"
"v_subrev_f32 v12,s13,v12\n"
"v_fma_f32 v12,v12,v11,s13\n"
AW "a238,v12\n"
AR "v12,a19\n"
"v_subrev_f32 v12,s13,v12\n"
"v_fma_f32 v12,v12,v11,s13\n"
AW "a19,v12\n"
AR "v12,a39\n"
"v_subrev_f32 v12,s13,v12\n"
"v_fma_f32 v12,v12,v11,s13\n"
AW "a39,v12\n"
AR "v12,a59\n"
"v_subrev_f32 v12,s13,v12\n"
"v_fma_f32 v12,v12,v11,s13\n"
AW "a59,v12\n"
AR "v12,a79\n"
"v_subrev_f32 v12,s13,v12\n"
"v_fma_f32 v12,v12,v11,s13\n"
AW "a79,v12\n"
AR "v12,a99\n"
"v_subrev_f32 v12,s13,v12\n"
"v_fma_f32 v12,v12,v11,s13\n"
AW "a99,v12\n"
AR "v12,a119\n"
"v_subrev_f32 v12,s13,v12\n"
"v_fma_f32 v12,v12,v11,s13\n"
AW "a119,v12\n"
AR "v12,a139\n"
"v_subrev_f32 v12,s13,v12\n"
"v_fma_f32 v12,v12,v11,s13\n"
AW "a139,v12\n"
AR "v12,a159\n"
"v_subrev_f32 v12,s13,v12\n"
"v_fma_f32 v12,v12,v11,s13\n"
AW "a159,v12\n"
AR "v12,a179\n"
"v_subrev_f32 v12,s13,v12\n"
"v_fma_f32 v12,v12,v11,s13\n"
AW "a179,v12\n"
AR "v12,a199\n"
"v_subrev_f32 v12,s13,v12\n"
"v_fma_f32 v12,v12,v11,s13\n"
AW "a199,v12\n"
AR "v12,a219\n"
"v_subrev_f32 v12,s13,v12\n"
"v_fma_f32 v12,v12,v11,s13\n"
AW "a219,v12\n"
AR "v12,a239\n"
"v_subrev_f32 v12,s13,v12\n"
"v_fma_f32 v12,v12,v11,s13\n"
AW "a239,v12\n"
"v_mul_f32 v61,v56,v11\n"
"v_sub_f32 v48,v48,v10\n"
"v_sub_f32 v49,v49,v10\n"
"v_sub_f32 v50,v50,v10\n"
"v_sub_f32 v51,v51,v10\n"
"v_sub_f32 v88,v88,v10\n"
"v_sub_f32 v89,v89,v10\n"
"v_sub_f32 v90,v90,v10\n"
"v_sub_f32 v91,v91,v10\n"
"v_sub_f32 v108,v108,v10\n"
"v_sub_f32 v109,v109,v10\n"
"v_sub_f32 v110,v110,v10\n"
"v_sub_f32 v111,v111,v10\n"
"v_sub_f32 v128,v128,v10\n"
"v_sub_f32 v129,v129,v10\n"
"v_sub_f32 v130,v130,v10\n"
"v_sub_f32 v131,v131,v10\n"
"v_exp_f32 v108,v108\n"
"v_exp_f32 v109,v109\n"
"v_exp_f32 v110,v110\n"
"v_exp_f32 v111,v111\n"
"v_exp_f32 v128,v128\n"
"v_exp_f32 v129,v129\n"
"v_exp_f32 v130,v130\n"
"v_exp_f32 v131,v131\n"
"v_add_f32 v61,v61,v108\n"
"v_add_f32 v61,v61,v109\n"
"v_add_f32 v61,v61,v110\n"
"v_add_f32 v61,v61,v111\n"
"v_add_f32 v61,v61,v128\n"
"v_add_f32 v61,v61,v129\n"
"v_add_f32 v61,v61,v130\n"
"v_add_f32_e64 v61,v61,v131\n"
CV "v108,v108,v109\n"
CV "v109,v110,v111\n"
CV "v110,v128,v129\n"
CV "v111,v130,v131\n"
"s_nop 7\n"
"s_setpc_b64 s[52:53]\n"
".Ldone:\n"
    : "+{v0}"(tid), "+{s23}"(bid), "+{s[20:21]}"(ka)
    :
    : "v1", "v2", "v3", "v4", "v5", "v6", "v7", "v8", "v9", "v10", "v11", "v12", "v13", "v14", "v15", "v16", "v17", "v18", "v19", "v20", "v21", "v22", "v23", "v24", "v25", "v26", "v27", "v28", "v29", "v30", "v31", "v32", "v33", "v34", "v35", "v36", "v37", "v38", "v39", "v40", "v41", "v42", "v43", "v44", "v45", "v46", "v47", "v48", "v49", "v50", "v51", "v52", "v53", "v54", "v55", "v56", "v57", "v58", "v59", "v60", "v61", "v62", "v63", "v64", "v65", "v66", "v67", "v68", "v69", "v70", "v71", "v72", "v73", "v74", "v75", "v76", "v77", "v78", "v79", "v80", "v81", "v82", "v83", "v84", "v85", "v86", "v87", "v88", "v89", "v90", "v91", "v92", "v93", "v94", "v95", "v96", "v97", "v98", "v99", "v100", "v101", "v102", "v103", "v104", "v105", "v106", "v107", "v108", "v109", "v110", "v111", "v112", "v113", "v114", "v115", "v116", "v117", "v118", "v119", "v120", "v121", "v122", "v123", "v124", "v125", "v126", "v127", "v128", "v129", "v130", "v131", "v132", "v133", "v134", "v135", "v136", "v137", "v138", "v139", "v140", "v141", "v142", "v143", "v144", "v145", "v146", "v147", "v148", "v149", "v150", "v151", "v152", "v153", "v154", "v155", "v156", "v157", "v158", "v159", "v160", "v161", "v162", "v163", "v164", "v165", "v166", "v167", "v168", "v169", "v170", "v171", "v172", "v173", "v174", "v175", "v176", "v177", "v178", "v179", "v180", "v181", "v182", "v183", "v184", "v185", "v186", "v187", "v188", "v189", "v190", "v191", "v192", "v193", "v194", "v195", "v196", "v197", "v198", "v199", "v200", "v201", "v202", "v203", "v204", "v205", "v206", "v207", "v208", "v209", "v210", "v211", "v212", "v213", "v214", "v215", "v216", "v217", "v218", "v219", "v220", "v221", "v222", "v223", "v224", "v225", "v226", "v227", "v228", "v229", "v230", "v231", "v232", "v233", "v234", "v235", "v236", "v237", "v238", "v239", "v240", "v241", "v242", "v243", "v244", "v245", "v246", "v247", "v248", "v249", "v250", "v251", "v252", "v253", "v254", "v255", "a0", "a1", "a2", "a3", "a4", "a5", "a6", "a7", "a8", "a9", "a10", "a11", "a12", "a13", "a14", "a15", "a16", "a17", "a18", "a19", "a20", "a21", "a22", "a23", "a24", "a25", "a26", "a27", "a28", "a29", "a30", "a31", "a32", "a33", "a34", "a35", "a36", "a37", "a38", "a39", "a40", "a41", "a42", "a43", "a44", "a45", "a46", "a47", "a48", "a49", "a50", "a51", "a52", "a53", "a54", "a55", "a56", "a57", "a58", "a59", "a60", "a61", "a62", "a63", "a64", "a65", "a66", "a67", "a68", "a69", "a70", "a71", "a72", "a73", "a74", "a75", "a76", "a77", "a78", "a79", "a80", "a81", "a82", "a83", "a84", "a85", "a86", "a87", "a88", "a89", "a90", "a91", "a92", "a93", "a94", "a95", "a96", "a97", "a98", "a99", "a100", "a101", "a102", "a103", "a104", "a105", "a106", "a107", "a108", "a109", "a110", "a111", "a112", "a113", "a114", "a115", "a116", "a117", "a118", "a119", "a120", "a121", "a122", "a123", "a124", "a125", "a126", "a127", "a128", "a129", "a130", "a131", "a132", "a133", "a134", "a135", "a136", "a137", "a138", "a139", "a140", "a141", "a142", "a143", "a144", "a145", "a146", "a147", "a148", "a149", "a150", "a151", "a152", "a153", "a154", "a155", "a156", "a157", "a158", "a159", "a160", "a161", "a162", "a163", "a164", "a165", "a166", "a167", "a168", "a169", "a170", "a171", "a172", "a173", "a174", "a175", "a176", "a177", "a178", "a179", "a180", "a181", "a182", "a183", "a184", "a185", "a186", "a187", "a188", "a189", "a190", "a191", "a192", "a193", "a194", "a195", "a196", "a197", "a198", "a199", "a200", "a201", "a202", "a203", "a204", "a205", "a206", "a207", "a208", "a209", "a210", "a211", "a212", "a213", "a214", "a215", "a216", "a217", "a218", "a219", "a220", "a221", "a222", "a223", "a224", "a225", "a226", "a227", "a228", "a229", "a230", "a231", "a232", "a233", "a234", "a235", "a236", "a237", "a238", "a239", "a240", "a241", "a242", "a243", "a244", "a245", "a246", "a247", "a248", "a249", "a250", "a251", "a252", "a253", "a254", "a255", "s0", "s1", "s2", "s3", "s4", "s5", "s6", "s7", "s8", "s9", "s10", "s11", "s12", "s13", "s14", "s15", "s16", "s17", "s18", "s19", "s22", "s24", "s25", "s26", "s27", "s28", "s29", "s30", "s31", "s32", "s33", "s34", "s35", "s36", "s37", "s38", "s39", "s40", "s41", "s42", "s43", "s44", "s45", "s46", "s47", "s48", "s49", "s50", "s51", "s52", "s53", "s54", "s55", "s56", "s57", "s58", "s59", "s60", "s61", "s62", "s63", "s64", "s65", "s66", "s67", "s68", "s69", "s70", "s71", "s72", "s73", "s74", "s75", "s76", "s77", "s78", "s79", "s80", "s81", "s82", "s83", "s84", "s85", "s86", "s87", "s88", "s89", "s90", "s91", "s92", "s93", "s94", "s95", "s96", "s97", "s98", "s99", "s100", "s101", "vcc", "scc", "memory");
}

extern "C" void kernel_launch(void* const* d_in, const int* in_sizes, int n_in, void* d_out, int out_size, void* d_ws, size_t ws_size, hipStream_t stream) {
  Ptrs P;
  for (int br = 0; br < 2; ++br) { P.x[br] = d_in[5 * br]; P.k[br] = d_in[5 * br + 1]; P.v[br] = d_in[5 * br + 2]; P.wq[br] = d_in[5 * br + 3]; P.wo[br] = d_in[5 * br + 4]; }
  (void)hipFuncSetAttribute((const void*)fused_kernel, hipFuncAttributeMaxDynamicSharedMemorySize, LDS1);
  fused_kernel<<<256, 256, LDS1, stream>>>(P, (char*)d_ws, (char*)d_out);
}
```

```cpp
#include <hip/hip_runtime.h>
#include <hip/hip_bf16.h>

#define MF "v_mfma_f32_16x16x32_bf16 "
#define TR "ds_read_b64_tr_b16 "
#define AR "v_accvgpr_read_b32 "
#define AW "v_accvgpr_write_b32 "
#define CV "v_cvt_pk_bf16_f32 "
#define LD "buffer_load_dwordx4 "
constexpr unsigned LDS1 = 160 * 1024;

struct Ptrs { const void* x[2]; const void* k[2]; const void* v[2]; const void* wq[2]; const void* wo[2]; };

__global__ __launch_bounds__(256, 1) __attribute__((aligned(4096))) void fused_kernel(Ptrs P, char* __restrict__ ws, char* __restrict__ out) {
  unsigned tid = threadIdx.x, bid = blockIdx.x;
  unsigned long long ka = (unsigned long long)__builtin_amdgcn_kernarg_segment_ptr();
  asm volatile(
".p2align 5\n"
".Lcode0:\n"
"s_and_b32 s58,s23,4\n"
"s_lshl_b32 s58,s58,1\n"
"s_load_dwordx2 s[28:29],s[20:21],s58 offset:48\n"
"s_load_dwordx2 s[24:25],s[20:21],s58\n"
"s_load_dwordx2 s[32:33],s[20:21],s58 offset:16\n"
"s_load_dwordx2 s[36:37],s[20:21],s58 offset:32\n"
"s_load_dwordx2 s[48:49],s[20:21],s58 offset:64\n"
"s_memtime s[100:101]\n"
"s_load_dwordx2 s[40:41],s[20:21],0x50\n"
"s_load_dwordx2 s[44:45],s[20:21],0x58\n"
"v_readfirstlane_b32 s22,v0\n"
"v_and_b32 v9,63,v0\n"
"v_bfe_u32 v10,v0,3,3\n"
"v_and_b32 v11,7,v0\n"
"s_lshr_b32 s22,s22,6\n"
"s_or_b32 s22,s22,s58\n"
"s_and_b32 s58,s23,1\n"
"s_lshl_b32 s58,s58,3\n"
"s_lshr_b32 s59,s23,5\n"
"s_add_u32 s59,s59,s58\n"
"s_mul_i32 s59,s59,4112\n"
"s_or_b32 s22,s22,s59\n"
"s_and_b32 s59,s23,2\n"
"s_lshl_b32 s59,s59,1\n"
"s_bfe_u32 s60,s23,0x20003\n"
"s_or_b32 s59,s59,s60\n"
"s_lshl_b32 s60,s59,20\n"
"s_or_b32 s22,s22,s60\n"
"s_or_b32 s59,s59,s58\n"
"s_mul_i32 s59,s59,65792\n"
"s_xor_b32 s59,s59,524288\n"
"s_or_b32 s22,s22,s59\n"
"v_lshrrev_b32 v6,2,v0\n"
"v_and_b32 v6,4,v6\n"
"v_xor_b32 v6,v6,v11\n"
"v_lshlrev_b32 v6,4,v6\n"
"s_mov_b32 s58,12288\n"
"v_mad_u32_u24 v6,v10,s58,v6\n"
"v_bfe_u32 v4,v0,4,3\n"
"v_xor_b32 v4,v4,v11\n"
"v_lshlrev_b32 v4,4,v4\n"
"s_mov_b32 s58,1280\n"
"v_mad_u32_u24 v4,v10,s58,v4\n"
"v_lshlrev_b32 v9,4,v9\n"
"s_mov_b32 s26,3276800\n"
"s_mov_b32 s27,0x00020000\n"
"s_mov_b32 s30,7864320\n"
"s_mov_b32 s31,0x00020000\n"
"s_mov_b32 s34,0\n"
"s_mov_b32 s35,0x00020000\n"
"s_mov_b32 s38,0\n"
"s_mov_b32 s39,0x00020000\n"
"s_mov_b32 s42,268435456\n"
"s_mov_b32 s43,0x00020000\n"
"s_mov_b32 s46,6553600\n"
"s_mov_b32 s47,0x00020000\n"
"s_mov_b32 s50,7864320\n"
"s_mov_b32 s51,0x00020000\n"
"s_mov_b32 s98,20000\n"
"s_bfe_u32 s56,s22,0x20000\n"
"s_bfe_u32 s72,s22,0x10003\n"
"s_lshr_b32 s73,s56,1\n"
"s_and_b32 s74,s56,1\n"
"s_mul_i32 s62,s56,4096\n"
"s_mul_i32 s63,s56,5120\n"
"s_add_u32 s63,s63,98304\n"
"s_bfe_u32 s58,s22,0x40004\n"
"s_bfe_u32 s59,s22,0x40008\n"
"s_mul_i32 s68,s58,204800\n"
"s_mul_i32 s60,s56,51200\n"
"s_add_u32 s68,s68,s60\n"
"s_lshl_b32 s59,s59,1\n"
"s_add_u32 s59,s59,s73\n"
"s_mul_i32 s66,s59,384\n"
"s_mul_i32 s60,s74,393216\n"
"s_add_u32 s66,s66,s60\n"
"s_lshr_b32 s58,s22,6\n"
"s_xor_b32 s58,s58,s22\n"
"s_bfe_u32 s58,s58,0x10004\n"
"s_mul_i32 s81,s58,640\n"
"s_mul_i32 s60,s81,6144\n"
"s_add_u32 s66,s66,s60\n"
"v_xor_b32 v19,64,v4\n"
"v_add_u32 v16,97280,v6\n"
"v_add_u32 v17,194560,v6\n"
"v_add_u32 v18,291840,v6\n"
"s_waitcnt lgkmcnt(0)\n"
"s_mov_b32 s80,s100\n"
"s_mov_b32 s87,2\n"
"s_add_u32 m0,s62,0\n"
"s_add_u32 s82,s66,0\n"
"s_sub_u32 s15,s82,7864320\n"
"s_min_u32 s82,s82,s15\n"
LD "v6,s[28:31],s82 offen lds\n"
LD "v16,s[28:31],s82 offen offset:1024 lds\n"
LD "v17,s[28:31],s82 offen offset:2048 lds\n"
LD "v18,s[28:31],s82 offen offset:3072 lds\n"
"s_add_u32 s14,s81,0\n"
"s_sub_u32 s15,s14,1280\n"
"s_min_u32 s14,s14,s15\n"
"s_add_u32 s14,s14,s68\n"
"s_add_u32 s84,s14,0\n"
"s_add_u32 m0,s63,0\n"
"s_nop 0\n"
LD "v4,s[24:27],s84 offen lds\n"
"s_add_u32 s84,s14,9216\n"
LD "v19,s[24:27],s84 offen offset:1024 lds\n"
"s_add_u32 s84,s14,18432\n"
LD "v4,s[24:27],s84 offen offset:2048 lds\n"
"s_add_u32 s84,s14,27648\n"
LD "v19,s[24:27],s84 offen offset:3072 lds\n"
"s_add_u32 s84,s14,40960\n"
"s_add_u32 m0,s63,4096\n"
"s_nop 0\n"
LD "v4,s[24:27],s84 offen lds\n"
"s_lshr_b32 s58,s23,3\n"
"s_mul_i32 s59,s58,7\n"
"s_and_b32 s60,s23,7\n"
"s_mul_i32 s60,s60,3\n"
"s_add_u32 s59,s59,s60\n"
"s_and_b32 s59,s59,15\n"
"s_lshl_b32 s59,s59,8\n"
"s_or_b32 s58,s58,s59\n"
"s_and_b32 s59,s23,3\n"
"s_lshl_b32 s59,s59,5\n"
"s_or_b32 s58,s58,s59\n"
"s_bfe_u32 s59,s23,0x10001\n"
"s_lshl_b32 s59,s59,12\n"
"s_or_b32 s58,s58,s59\n"
"s_lshl_b32 s23,s23,16\n"
"s_or_b32 s23,s23,s58\n"
"v_bfe_u32 v20,v0,4,2\n"
"v_mul_u32_u24 v21,384,v10\n"
"v_xor_b32 v22,v11,v20\n"
"s_add_u32 m0,s62,16384\n"
"s_add_u32 s82,s66,128\n"
"s_sub_u32 s15,s82,7864320\n"
"s_min_u32 s82,s82,s15\n"
LD "v6,s[28:31],s82 offen lds\n"
"v_lshl_add_u32 v7,v22,4,v21\n"
"s_lshl_b32 s58,s74,6\n"
"v_xor_b32 v7,s58,v7\n"
"v_lshlrev_b32 v22,1,v20\n"
"v_xor_b32 v22,v11,v22\n"
"v_lshl_add_u32 v8,v22,4,v21\n"
"v_bfe_u32 v21,v0,1,3\n"
"v_xor_b32 v21,v21,v20\n"
"v_lshlrev_b32 v21,4,v21\n"
"v_lshl_add_u32 v21,v11,7,v21\n"
"v_bfe_u32 v22,v0,3,1\n"
"s_mul_i32 s58,s73,12288\n"
"v_mul_u32_u24 v23,3072,v22\n"
"v_add3_u32 v1,v23,v21,s58\n"
"s_mul_i32 s59,s74,10240\n"
"s_add_u32 s59,s59,98304\n"
"v_lshl_add_u32 v2,v22,10,v21\n"
"v_add_u32_e64 v2,s59,v2\n"
"v_bfe_u32 v21,v0,2,3\n"
LD "v16,s[28:31],s82 offen offset:1024 lds\n"
"v_lshlrev_b32_e64 v21,7,v21\n"
"v_bfe_u32 v23,v0,3,2\n"
"v_lshl_add_u32 v21,v23,5,v21\n"
"v_and_b32_e64 v23,3,v0\n"
"v_lshl_add_u32 v21,v23,3,v21\n"
"v_bfe_u32 v23,v0,5,1\n"
"v_mul_u32_u24 v3,3072,v23\n"
"v_add3_u32 v3,v3,v21,s58\n"
"s_bfe_u32 s58,s22,0x40004\n"
"s_mul_i32 s58,s58,409600\n"
"s_mul_i32 s59,s72,1280\n"
"s_add_u32 s58,s58,s59\n"
"v_and_b32 v21,15,v0\n"
"v_mul_u32_u24 v21,2560,v21\n"
"v_lshl_add_u32 v21,v23,4,v21\n"
"v_bfe_u32 v23,v0,4,1\n"
"v_lshl_add_u32 v21,v23,5,v21\n"
"v_add_u32_e64 v12,s58,v21\n"
"v_bfe_u32 v21,v0,2,2\n"
LD "v17,s[28:31],s82 offen offset:2048 lds\n"
"v_lshlrev_b32 v21,7,v21\n"
"v_lshl_add_u32 v21,v20,10,v21\n"
"v_lshl_add_u32 v21,v22,6,v21\n"
"v_and_b32 v23,3,v0\n"
"v_lshl_add_u32 v21,v23,4,v21\n"
"s_lshl_b32 s58,s73,13\n"
"v_add_u32 v0,s58,v21\n"
"s_getpc_b64 s[52:53]\n"
".Lcts:\n"
"s_sub_u32 s52,s52,.Lcts-.Lcode0\n"
"s_subb_u32 s53,s53,0\n"
"s_bfe_u32 s58,s23,0x50000\n"
"s_lshl_b32 s58,s58,2\n"
"s_add_u32 s58,s58,s56\n"
"s_mul_i32 s58,s58,1280\n"
"v_min_u32 v15,144,v9\n"
"v_lshl_add_u32 v15,v15,3,s58\n"
"v_min_u32 v15,.Lcode1-.Lcode0,v15\n"
LD "v18,s[28:31],s82 offen offset:3072 lds\n"
"s_bfe_u32 s58,s22,0x4000c\n"
"s_bfe_u32 s59,s22,0x40010\n"
"s_mul_i32 s69,s58,204800\n"
"s_mul_i32 s60,s56,51200\n"
"s_add_u32 s69,s69,s60\n"
"s_lshl_b32 s59,s59,1\n"
"s_add_u32 s59,s59,s73\n"
"s_mul_i32 s67,s59,384\n"
"s_mul_i32 s60,s74,393216\n"
"s_add_u32 s67,s67,s60\n"
"s_mul_i32 s60,s81,6144\n"
"s_add_u32 s67,s67,s60\n"
"s_bfe_u32 s58,s22,0x40004\n"
"s_bfe_u32 s59,s22,0x40008\n"
"s_lshl_b32 s59,s59,1\n"
"s_add_u32 s88,s59,s74\n"
"s_mul_i32 s88,s88,245760\n"
"s_add_u32 s59,s59,s73\n"
"s_lshl_b32 s70,s58,5\n"
"s_add_u32 m0,s62,32768\n"
"s_add_u32 s82,s66,256\n"
"s_sub_u32 s15,s82,7864320\n"
"s_min_u32 s82,s82,s15\n"
LD "v6,s[28:31],s82 offen lds\n"
"s_add_u32 s70,s70,s59\n"
"s_mul_i32 s70,s70,196608\n"
"s_bfe_u32 s58,s22,0x4000c\n"
"s_bfe_u32 s59,s22,0x40010\n"
"s_lshl_b32 s59,s59,1\n"
"s_add_u32 s89,s59,s74\n"
"s_mul_i32 s89,s89,245760\n"
"s_add_u32 s59,s59,s73\n"
"s_lshl_b32 s71,s58,5\n"
"s_add_u32 s71,s71,s59\n"
"s_mul_i32 s71,s71,196608\n"
"s_bitcmp1_b32 s23,12\n"
"s_cselect_b32 s58,s71,s70\n"
"s_cselect_b32 s71,s70,s71\n"
"s_mov_b32 s70,s58\n"
"s_cselect_b32 s58,s89,s88\n"
"s_cselect_b32 s89,s88,s89\n"
"s_mov_b32 s88,s58\n"
"s_cselect_b32 s69,98,1\n"
LD "v16,s[28:31],s82 offen offset:1024 lds\n"
"s_mov_b32 s54,1037384597\n"
"s_mov_b32 s55,1037384597\n"
"s_bfe_u32 s58,s22,0x40004\n"
"s_lshl_b32 s59,s72,4\n"
"s_add_u32 s59,s59,s58\n"
"s_lshl_b32 s86,s59,12\n"
"s_add_u32 s86,s86,100663296\n"
"s_mul_i32 s93,s59,1638400\n"
"s_add_u32 s93,s93,33554432\n"
"s_bfe_u32 s58,s22,0x30014\n"
"s_lshl_b32 s78,s58,1\n"
"s_add_u32 s78,s78,s74\n"
"s_lshl_b32 s94,s78,2\n"
"s_add_u32 s94,s94,s86\n"
"s_lshl_b32 s92,s58,2\n"
"s_add_u32 s92,s92,s56\n"
"s_mul_i32 s91,s58,204800\n"
"s_add_u32 s91,s91,s93\n"
"s_mul_i32 s75,s56,30720\n"
LD "v17,s[28:31],s82 offen offset:2048 lds\n"
"s_add_u32 s75,s75,s91\n"
"s_add_u32 s75,s75,0\n"
"s_mul_i32 s58,s74,5120\n"
"s_add_u32 s91,s91,s58\n"
"s_mov_b32 s90,0xf0000000\n"
"s_mov_b32 s77,0xf0000000\n"
"s_mov_b32 s85,0xf0000000\n"
AW "a240,v0\n"
AW "a244,v1\n"
AW "a241,v2\n"
AW "a245,v3\n"
AW "a242,v4\n"
AW "a243,v6\n"
AW "a252,v12\n"
"s_mov_b32 s57,0\n"
"s_mov_b32 s83,0\n"
LD "v18,s[28:31],s82 offen offset:3072 lds\n"
"global_load_dword a249,v15,s[52:53]\n"
AR "v0,a240\n"
AR "v2,a241\n"
AR "v4,a242\n"
AR "v6,a243\n"
"v_xor_b32 v1,64,v0\n"
"v_xor_b32 v3,64,v2\n"
"v_xor_b32_e64 v5,64,v4\n"
"v_add_u32 v11,97280,v6\n"
"v_add_u32 v12,194560,v6\n"
"v_add_u32 v13,291840,v6\n"
"s_mov_b64 vcc,0\n"
"s_waitcnt vmcnt(9) lgkmcnt(0)\n"
"s_barrier\n"
"s_nop 0\n"
TR "a[200:201],v0\n"
TR "a[202:203],v0 offset:512\n"
TR "a[204:205],v0 offset:4096\n"
TR "a[206:207],v0 offset:4608\n"
"ds_read_b128 a[60:63],v2\n"
"ds_read_b128 a[100:103],v3\n"
"ds_read_b128 a[64:67],v2 offset:2048\n"
"ds_read_b128 a[104:107],v3 offset:2048\n"
"ds_read_b128 a[68:71],v2 offset:4096\n"
"ds_read_b128 a[108:111],v3 offset:4096\n"
"ds_read_b128 a[72:75],v2 offset:6144\n"
"ds_read_b128 a[112:115],v3 offset:6144\n"
"ds_read_b128 a[76:79],v2 offset:8192\n"
"ds_read_b128 a[116:119],v3 offset:8192\n"
TR "a[208:209],v0 offset:8\n"
TR "a[210:211],v0 offset:520\n"
TR "a[212:213],v0 offset:4104\n"
TR "a[214:215],v0 offset:4616\n"
"s_waitcnt lgkmcnt(0)\n"
MF "v[16:19],a[200:203],a[60:63],0\n"
TR "a[216:217],v1\n"
MF "v[16:19],a[204:207],a[100:103],v[16:19]\n"
"s_add_u32 s14,s81,128\n"
TR "a[218:219],v1 offset:512\n"
MF "v[24:27],a[204:207],a[104:107],0\n"
"s_sub_u32 s15,s14,1280\n"
"v_lshrrev_b32 v15,5,v9\n"
"v_min_u32 v15,11,v15\n"
MF "v[24:27],a[200:203],a[64:67],v[24:27]\n"
"s_min_u32 s14,s14,s15\n"
TR "a[220:221],v1 offset:4096\n"
"v_mul_u32_u24 v15,160,v15\n"
MF "v[32:35],a[200:203],a[68:71],0\n"
"s_add_u32 s14,s14,s68\n"
TR "a[222:223],v1 offset:4608\n"
"v_and_or_b32 v15,v9,16,v15\n"
MF "v[32:35],a[204:207],a[108:111],v[32:35]\n"
"s_add_u32 s84,s14,0\n"
"v_max3_f32 v220,|v16|,|v17|,|v18|\n"
"v_max_f32_e64 v220,v220,|v19|\n"
MF "v[40:43],a[204:207],a[112:115],0\n"
"s_add_u32 m0,s63,20480\n"
"v_readlane_b32 s12,v220,3\n"
"v_readlane_b32 s6,v220,22\n"
MF "v[40:43],a[200:203],a[72:75],v[40:43]\n"
"s_min_u32 s12,s12,s6\n"
"v_readlane_b32 s7,v220,41\n"
"v_readlane_b32 s8,v220,60\n"
MF "v[48:51],a[200:203],a[76:79],0\n"
"s_min_u32 s7,s7,s8\n"
"v_lshlrev_b32 v15,3,v15\n"
MF "v[48:51],a[204:207],a[116:119],v[48:51]\n"
LD "v4,s[24:27],s84 offen lds\n"
MF "v[52:55],a[212:215],a[116:119],0\n"
"s_min_u32 s12,s12,s7\n"
TR "a[200:201],v1 offset:8\n"
MF "v[52:55],a[208:211],a[76:79],v[52:55]\n"
"s_and_b32 s12,s12,0x7f800000\n"
TR "a[202:203],v1 offset:520\n"
MF "v[44:47],a[208:211],a[72:75],0\n"
"s_sub_u32 s12,s12,0x14000000\n"
MF "v[44:47],a[212:215],a[112:115],v[44:47]\n"
"s_cmp_lt_u32 s12,0x50000000\n"
"s_cselect_b32 s12,s12,0xe8400000\n"
TR "a[204:205],v1 offset:4104\n"
MF "v[36:39],a[212:215],a[108:111],0\n"
"s_add_u32 s12,s12,0x17c00000\n"
TR "a[206:207],v1 offset:4616\n"
MF "v[36:39],a[208:211],a[68:71],v[36:39]\n"
"s_bfe_u32 s60,s23,0x70000\n"
"v_mov_b32 v220,s12\n"
"v_mov_b32 v221,s12\n"
MF "v[28:31],a[208:211],a[64:67],0\n"
"s_add_u32 s84,s14,9216\n"
"v_mov_b32 v222,s12\n"
"v_mov_b32 v223,s12\n"
MF "v[28:31],a[212:215],a[104:107],v[28:31]\n"
LD "v5,s[24:27],s84 offen offset:1024 lds\n"
MF "v[20:23],a[212:215],a[100:103],0\n"
"s_waitcnt vmcnt(7) lgkmcnt(0)\n"
MF "v[20:23],a[208:211],a[60:63],v[20:23]\n"
"s_barrier\n"
MF "v[56:59],a[216:219],a[60:63],v[220:223]\n"
"s_lshl_b32 s60,s60,2\n"
TR "a[208:209],v0 offset:16384\n"
MF "v[56:59],a[220:223],a[100:103],v[56:59]\n"
"s_add_u32 s84,s14,18432\n"
TR "a[210:211],v0 offset:16896\n"
MF "v[64:67],a[220:223],a[104:107],v[220:223]\n"
LD "v4,s[24:27],s84 offen offset:2048 lds\n"
MF "v[64:67],a[216:219],a[64:67],v[64:67]\n"
"s_add_u32 s60,s60,s56\n"
TR "a[212:213],v0 offset:20480\n"
MF "v[72:75],a[216:219],a[68:71],v[220:223]\n"
"s_mul_i32 s60,s60,15360\n"
TR "a[214:215],v0 offset:20992\n"
MF "v[72:75],a[220:223],a[108:111],v[72:75]\n"
MF "v[80:83],a[220:223],a[112:115],v[220:223]\n"
"s_add_u32 s84,s14,27648\n"
MF "v[80:83],a[216:219],a[72:75],v[80:83]\n"
LD "v5,s[24:27],s84 offen offset:3072 lds\n"
MF "v[88:91],a[216:219],a[76:79],v[220:223]\n"
MF "v[88:91],a[220:223],a[116:119],v[88:91]\n"
MF "v[92:95],a[204:207],a[116:119],v[220:223]\n"
"s_add_u32 s84,s14,40960\n"
TR "a[216:217],v0 offset:16392\n"
MF "v[92:95],a[200:203],a[76:79],v[92:95]\n"
"s_add_u32 m0,s63,24576\n"
TR "a[218:219],v0 offset:16904\n"
MF "v[84:87],a[200:203],a[72:75],v[220:223]\n"
LD "v4,s[24:27],s84 offen lds\n"
MF "v[84:87],a[204:207],a[112:115],v[84:87]\n"
"s_add_u32 m0,s62,49152\n"
TR "a[220:221],v0 offset:20488\n"
MF "v[76:79],a[204:207],a[108:111],v[220:223]\n"
"s_add_u32 s82,s66,786432\n"
TR "a[222:223],v0 offset:21000\n"
MF "v[76:79],a[200:203],a[68:71],v[76:79]\n"
"s_sub_u32 s15,s82,7864320\n"
MF "v[68:71],a[200:203],a[64:67],v[220:223]\n"
"s_min_u32 s82,s82,s15\n"
MF "v[68:71],a[204:207],a[104:107],v[68:71]\n"
LD "v6,s[28:31],s82 offen lds\n"
MF "v[60:63],a[204:207],a[100:103],v[220:223]\n"
MF "v[60:63],a[200:203],a[60:63],v[60:63]\n"
"s_waitcnt lgkmcnt(4)\n"
MF "v[96:99],a[208:211],a[60:63],v[220:223]\n"
TR "a[200:201],v1 offset:16384\n"
MF "v[96:99],a[212:215],a[100:103],v[96:99]\n"
TR "a[202:203],v1 offset:16896\n"
MF "v[104:107],a[212:215],a[104:107],v[220:223]\n"
LD "v11,s[28:31],s82 offen offset:1024 lds\n"
MF "v[104:107],a[208:211],a[64:67],v[104:107]\n"
TR "a[204:205],v1 offset:20480\n"
MF "v[112:115],a[208:211],a[68:71],v[220:223]\n"
TR "a[206:207],v1 offset:20992\n"
MF "v[112:115],a[212:215],a[108:111],v[112:115]\n"
MF "v[120:123],a[212:215],a[112:115],v[220:223]\n"
MF "v[120:123],a[208:211],a[72:75],v[120:123]\n"
LD "v12,s[28:31],s82 offen offset:2048 lds\n"
MF "v[128:131],a[208:211],a[76:79],v[220:223]\n"
MF "v[128:131],a[212:215],a[116:119],v[128:131]\n"
"s_waitcnt lgkmcnt(4)\n"
MF "v[132:135],a[220:223],a[116:119],v[220:223]\n"
TR "a[208:209],v1 offset:16392\n"
MF "v[132:135],a[216:219],a[76:79],v[132:135]\n"
TR "a[210:211],v1 offset:16904\n"
MF "v[124:127],a[216:219],a[72:75],v[220:223]\n"
LD "v13,s[28:31],s82 offen offset:3072 lds\n"
MF "v[124:127],a[220:223],a[112:115],v[124:127]\n"
"s_add_u32 m0,s62,65536\n"
TR "a[212:213],v1 offset:20488\n"
MF "v[116:119],a[220:223],a[108:111],v[220:223]\n"
"s_add_u32 s82,s66,786560\n"
TR "a[214:215],v1 offset:21000\n"
MF "v[116:119],a[216:219],a[68:71],v[116:119]\n"
"s_sub_u32 s15,s82,7864320\n"
MF "v[108:111],a[216:219],a[64:67],v[220:223]\n"
"s_min_u32 s82,s82,s15\n"
MF "v[108:111],a[220:223],a[104:107],v[108:111]\n"
LD "v6,s[28:31],s82 offen lds\n"
MF "v[100:103],a[220:223],a[100:103],v[220:223]\n"
"s_waitcnt vmcnt(5) lgkmcnt(0)\n"
MF "v[100:103],a[216:219],a[60:63],v[100:103]\n"
"s_barrier\n"
MF "v[136:139],a[200:203],a[60:63],v[220:223]\n"
TR "a[216:217],v0 offset:32768\n"
MF "v[136:139],a[204:207],a[100:103],v[136:139]\n"
TR "a[218:219],v0 offset:33280\n"
MF "v[144:147],a[204:207],a[104:107],v[220:223]\n"
"ds_read_b128 a[140:143],v2 offset:20480\n"
MF "v[144:147],a[200:203],a[64:67],v[144:147]\n"
TR "a[220:221],v0 offset:36864\n"
MF "v[152:155],a[200:203],a[68:71],v[220:223]\n"
TR "a[222:223],v0 offset:37376\n"
MF "v[152:155],a[204:207],a[108:111],v[152:155]\n"
LD "v11,s[28:31],s82 offen offset:1024 lds\n"
MF "v[160:163],a[204:207],a[112:115],v[220:223]\n"
"ds_read_b128 a[180:183],v3 offset:20480\n"
MF "v[160:163],a[200:203],a[72:75],v[160:163]\n"
MF "v[168:171],a[200:203],a[76:79],v[220:223]\n"
MF "v[168:171],a[204:207],a[116:119],v[168:171]\n"
LD "v12,s[28:31],s82 offen offset:2048 lds\n"
MF "v[172:175],a[212:215],a[116:119],v[220:223]\n"
TR "a[200:201],v0 offset:32776\n"
MF "v[172:175],a[208:211],a[76:79],v[172:175]\n"
TR "a[202:203],v0 offset:33288\n"
MF "v[164:167],a[208:211],a[72:75],v[220:223]\n"
"ds_read_b128 a[144:147],v2 offset:22528\n"
MF "v[164:167],a[212:215],a[112:115],v[164:167]\n"
TR "a[204:205],v0 offset:36872\n"
MF "v[156:159],a[212:215],a[108:111],v[220:223]\n"
TR "a[206:207],v0 offset:37384\n"
MF "v[156:159],a[208:211],a[68:71],v[156:159]\n"
LD "v13,s[28:31],s82 offen offset:3072 lds\n"
MF "v[148:151],a[208:211],a[64:67],v[220:223]\n"
"ds_read_b128 a[184:187],v3 offset:22528\n"
MF "v[148:151],a[212:215],a[104:107],v[148:151]\n"
"s_add_u32 m0,s62,81920\n"
MF "v[140:143],a[212:215],a[100:103],v[220:223]\n"
"s_add_u32 s82,s66,786688\n"
"s_sub_u32 s15,s82,7864320\n"
"s_min_u32 s82,s82,s15\n"
MF "v[140:143],a[208:211],a[60:63],v[140:143]\n"
"s_waitcnt lgkmcnt(7)\n"
LD "v6,s[28:31],s82 offen lds\n"
MF "v[176:179],a[216:219],a[60:63],v[220:223]\n"
TR "a[208:209],v1 offset:32768\n"
MF "v[176:179],a[220:223],a[100:103],v[176:179]\n"
TR "a[210:211],v1 offset:33280\n"
MF "v[184:187],a[220:223],a[104:107],v[220:223]\n"
"ds_read_b128 a[148:151],v2 offset:24576\n"
MF "v[184:187],a[216:219],a[64:67],v[184:187]\n"
TR "a[212:213],v1 offset:36864\n"
MF "v[192:195],a[216:219],a[68:71],v[220:223]\n"
TR "a[214:215],v1 offset:37376\n"
MF "v[192:195],a[220:223],a[108:111],v[192:195]\n"
LD "v11,s[28:31],s82 offen offset:1024 lds\n"
MF "v[200:203],a[220:223],a[112:115],v[220:223]\n"
"ds_read_b128 a[188:191],v3 offset:24576\n"
MF "v[200:203],a[216:219],a[72:75],v[200:203]\n"
MF "v[208:211],a[216:219],a[76:79],v[220:223]\n"
MF "v[208:211],a[220:223],a[116:119],v[208:211]\n"
"s_waitcnt lgkmcnt(7)\n"
LD "v12,s[28:31],s82 offen offset:2048 lds\n"
MF "v[212:215],a[204:207],a[116:119],v[220:223]\n"
TR "a[216:217],v1 offset:32776\n"
MF "v[212:215],a[200:203],a[76:79],v[212:215]\n"
TR "a[218:219],v1 offset:33288\n"
MF "v[204:207],a[200:203],a[72:75],v[220:223]\n"
"ds_read_b128 a[152:155],v2 offset:26624\n"
MF "v[204:207],a[204:207],a[112:115],v[204:207]\n"
TR "a[220:221],v1 offset:36872\n"
MF "v[196:199],a[204:207],a[108:111],v[220:223]\n"
TR "a[222:223],v1 offset:37384\n"
MF "v[196:199],a[200:203],a[68:71],v[196:199]\n"
LD "v13,s[28:31],s82 offen offset:3072 lds\n"
MF "v[188:191],a[200:203],a[64:67],v[220:223]\n"
"v_add_u32 v0,0xc000,v0\n"
MF "v[188:191],a[204:207],a[104:107],v[188:191]\n"
"s_add_u32 s14,s81,256\n"
"v_add_u32 v1,0xc000,v1\n"
MF "v[180:183],a[204:207],a[100:103],v[220:223]\n"
"s_waitcnt vmcnt(8) lgkmcnt(0)\n"
"s_mov_b64 exec,0\n"
"s_bitcmp1_b32 s23,12\n"
"s_cbranch_scc1 .Lisy1\n"
"s_cmp_eq_u32 s56,0\n"
"s_cbranch_scc1 .Lpg8b\n"
"s_cmp_eq_u32 s56,1\n"
"s_cbranch_scc1 .Lpg9b\n"
"s_cmp_eq_u32 s56,2\n"
"s_cbranch_scc1 .Lpgt0ex_b\n"
"s_branch .Lpg10b\n"
".Lisy1:\n"
"s_cmp_eq_u32 s56,0\n"
"s_cbranch_scc1 .Lpgq0_c\n"
"s_cmp_eq_u32 s56,1\n"
"s_cbranch_scc1 .Lpg4c\n"
"s_cmp_eq_u32 s56,2\n"
"s_cbranch_scc1 .Lpg5c\n"
"s_branch .Lpg6c\n"
".Lisr1:\n"
"s_mov_b64 exec,-1\n"
".Lisx1:\n"
MF "v[180:183],a[200:203],a[60:63],v[180:183]\n"
"s_barrier\n"
MF "v[216:219],a[208:211],a[60:63],v[220:223]\n"
"s_sub_u32 s15,s14,1280\n"
TR "a[200:201],v0\n"
MF "v[216:219],a[212:215],a[100:103],v[216:219]\n"
"s_min_u32 s14,s14,s15\n"
TR "a[202:203],v0 offset:512\n"
MF "v[224:227],a[212:215],a[104:107],v[220:223]\n"
"s_add_u32 s14,s14,s68\n"
"ds_read_b128 a[192:195],v3 offset:26624\n"
MF "v[224:227],a[208:211],a[64:67],v[224:227]\n"
"s_add_u32 s84,s14,0\n"
TR "a[204:205],v0 offset:4096\n"
MF "v[232:235],a[208:211],a[68:71],v[220:223]\n"
"s_add_u32 m0,s63,40960\n"
TR "a[206:207],v0 offset:4608\n"
MF "v[232:235],a[212:215],a[108:111],v[232:235]\n"
LD "v4,s[24:27],s84 offen lds\n"
MF "v[240:243],a[212:215],a[112:115],v[220:223]\n"
"ds_read_b128 a[156:159],v2 offset:28672\n"
MF "v[240:243],a[208:211],a[72:75],v[240:243]\n"
"ds_read_b128 a[196:199],v3 offset:28672\n"
MF "v[248:251],a[208:211],a[76:79],v[220:223]\n"
"s_add_u32 s84,s14,9216\n"
MF "v[248:251],a[212:215],a[116:119],v[248:251]\n"
LD "v5,s[24:27],s84 offen offset:1024 lds\n"
MF "v[252:255],a[220:223],a[116:119],v[220:223]\n"
TR "a[208:209],v0 offset:8\n"
MF "v[252:255],a[216:219],a[76:79],v[252:255]\n"
TR "a[210:211],v0 offset:520\n"
MF "v[244:247],a[216:219],a[72:75],v[220:223]\n"
MF "v[244:247],a[220:223],a[112:115],v[244:247]\n"
TR "a[212:213],v0 offset:4104\n"
MF "v[236:239],a[220:223],a[108:111],v[220:223]\n"
"s_add_u32 s84,s14,18432\n"
TR "a[214:215],v0 offset:4616\n"
MF "v[236:239],a[216:219],a[68:71],v[236:239]\n"
LD "v4,s[24:27],s84 offen offset:2048 lds\n"
MF "v[228:231],a[216:219],a[64:67],v[220:223]\n"
MF "v[228:231],a[220:223],a[104:107],v[228:231]\n"
MF "v[220:223],a[220:223],a[100:103],v[220:223]\n"
".Lpg1a:\n"
"s_cbranch_execz .Lpg7a\n"
"s_add_u32 s84,s14,27648\n"
MF "v[220:223],a[216:219],a[60:63],v[220:223]\n"
"s_waitcnt lgkmcnt(4)\n"
LD "v5,s[24:27],s84 offen offset:3072 lds\n"
MF "v[16:19],a[200:203],a[140:143],v[16:19]\n"
TR "a[216:217],v1\n"
MF "v[16:19],a[204:207],a[180:183],v[16:19]\n"
TR "a[218:219],v1 offset:512\n"
MF "v[24:27],a[204:207],a[184:187],v[24:27]\n"
MF "v[24:27],a[200:203],a[144:147],v[24:27]\n"
"s_add_u32 s84,s14,40960\n"
TR "a[220:221],v1 offset:4096\n"
MF "v[32:35],a[200:203],a[148:151],v[32:35]\n"
"s_add_u32 m0,s63,45056\n"
TR "a[222:223],v1 offset:4608\n"
MF "v[32:35],a[204:207],a[188:191],v[32:35]\n"
LD "v4,s[24:27],s84 offen lds\n"
MF "v[40:43],a[204:207],a[192:195],v[40:43]\n"
MF "v[40:43],a[200:203],a[152:155],v[40:43]\n"
"s_add_u32 m0,s62,0\n"
MF "v[48:51],a[200:203],a[156:159],v[48:51]\n"
"s_add_u32 s82,s66,1572864\n"
"s_sub_u32 s15,s82,7864320\n"
"s_min_u32 s82,s82,s15\n"
MF "v[48:51],a[204:207],a[196:199],v[48:51]\n"
"s_waitcnt lgkmcnt(4)\n"
LD "v6,s[28:31],s82 offen lds\n"
MF "v[52:55],a[212:215],a[196:199],v[52:55]\n"
TR "a[200:201],v1 offset:8\n"
MF "v[52:55],a[208:211],a[156:159],v[52:55]\n"
TR "a[202:203],v1 offset:520\n"
MF "v[44:47],a[208:211],a[152:155],v[44:47]\n"
MF "v[44:47],a[212:215],a[192:195],v[44:47]\n"
TR "a[204:205],v1 offset:4104\n"
MF "v[36:39],a[212:215],a[188:191],v[36:39]\n"
TR "a[206:207],v1 offset:4616\n"
MF "v[36:39],a[208:211],a[148:151],v[36:39]\n"
LD "v11,s[28:31],s82 offen offset:1024 lds\n"
MF "v[28:31],a[208:211],a[144:147],v[28:31]\n"
MF "v[28:31],a[212:215],a[184:187],v[28:31]\n"
MF "v[20:23],a[212:215],a[180:183],v[20:23]\n"
"s_waitcnt vmcnt(11) lgkmcnt(0)\n"
LD "v12,s[28:31],s82 offen offset:2048 lds\n"
MF "v[20:23],a[208:211],a[140:143],v[20:23]\n"
"s_barrier\n"
"v_bfe_u32 v14,v9,4,4\n"
"v_lshlrev_b32 v14,2,v14\n"
"buffer_load_dword v10,v14,s[40:43],s86 offen sc1\n"
".Lhs2:\n"
MF "v[56:59],a[216:219],a[140:143],v[56:59]\n"
TR "a[208:209],v0 offset:16384\n"
MF "v[56:59],a[220:223],a[180:183],v[56:59]\n"
TR "a[210:211],v0 offset:16896\n"
MF "v[64:67],a[220:223],a[184:187],v[64:67]\n"
LD "v13,s[28:31],s82 offen offset:3072 lds\n"
MF "v[64:67],a[216:219],a[144:147],v[64:67]\n"
TR "a[212:213],v0 offset:20480\n"
MF "v[72:75],a[216:219],a[148:151],v[72:75]\n"
TR "a[214:215],v0 offset:20992\n"
MF "v[72:75],a[220:223],a[188:191],v[72:75]\n"
"s_add_u32 s14,s81,384\n"
"s_sub_u32 s15,s14,1280\n"
"s_min_u32 s14,s14,s15\n"
MF "v[80:83],a[220:223],a[192:195],v[80:83]\n"
"s_add_u32 s14,s14,s68\n"
"s_add_u32 s84,s14,0\n"
"s_add_u32 m0,s63,0\n"
MF "v[80:83],a[216:219],a[152:155],v[80:83]\n"
LD "v4,s[24:27],s84 offen lds\n"
MF "v[88:91],a[216:219],a[156:159],v[88:91]\n"
MF "v[88:91],a[220:223],a[196:199],v[88:91]\n"
MF "v[92:95],a[204:207],a[196:199],v[92:95]\n"
TR "a[216:217],v0 offset:16392\n"
MF "v[92:95],a[200:203],a[156:159],v[92:95]\n"
"s_add_u32 s84,s14,9216\n"
TR "a[218:219],v0 offset:16904\n"
MF "v[84:87],a[200:203],a[152:155],v[84:87]\n"
LD "v5,s[24:27],s84 offen offset:1024 lds\n"
MF "v[84:87],a[204:207],a[192:195],v[84:87]\n"
"s_add_u32 m0,s62,16384\n"
TR "a[220:221],v0 offset:20488\n"
MF "v[76:79],a[204:207],a[188:191],v[76:79]\n"
"s_add_u32 s82,s66,1572992\n"
TR "a[222:223],v0 offset:21000\n"
MF "v[76:79],a[200:203],a[148:151],v[76:79]\n"
"s_sub_u32 s15,s82,7864320\n"
MF "v[68:71],a[200:203],a[144:147],v[68:71]\n"
"s_min_u32 s82,s82,s15\n"
MF "v[68:71],a[204:207],a[184:187],v[68:71]\n"
LD "v6,s[28:31],s82 offen lds\n"
MF "v[60:63],a[204:207],a[180:183],v[60:63]\n"
MF "v[60:63],a[200:203],a[140:143],v[60:63]\n"
"s_waitcnt lgkmcnt(4)\n"
MF "v[96:99],a[208:211],a[140:143],v[96:99]\n"
TR "a[200:201],v1 offset:16384\n"
MF "v[96:99],a[212:215],a[180:183],v[96:99]\n"
TR "a[202:203],v1 offset:16896\n"
MF "v[104:107],a[212:215],a[184:187],v[104:107]\n"
LD "v11,s[28:31],s82 offen offset:1024 lds\n"
MF "v[104:107],a[208:211],a[144:147],v[104:107]\n"
TR "a[204:205],v1 offset:20480\n"
MF "v[112:115],a[208:211],a[148:151],v[112:115]\n"
TR "a[206:207],v1 offset:20992\n"
MF "v[112:115],a[212:215],a[188:191],v[112:115]\n"
MF "v[120:123],a[212:215],a[192:195],v[120:123]\n"
MF "v[120:123],a[208:211],a[152:155],v[120:123]\n"
LD "v12,s[28:31],s82 offen offset:2048 lds\n"
MF "v[128:131],a[208:211],a[156:159],v[128:131]\n"
MF "v[128:131],a[212:215],a[196:199],v[128:131]\n"
"s_waitcnt lgkmcnt(4)\n"
MF "v[132:135],a[220:223],a[196:199],v[132:135]\n"
TR "a[208:209],v1 offset:16392\n"
MF "v[132:135],a[216:219],a[156:159],v[132:135]\n"
TR "a[210:211],v1 offset:16904\n"
MF "v[124:127],a[216:219],a[152:155],v[124:127]\n"
LD "v13,s[28:31],s82 offen offset:3072 lds\n"
MF "v[124:127],a[220:223],a[192:195],v[124:127]\n"
TR "a[212:213],v1 offset:20488\n"
MF "v[116:119],a[220:223],a[188:191],v[116:119]\n"
TR "a[214:215],v1 offset:21000\n"
MF "v[116:119],a[216:219],a[148:151],v[116:119]\n"
"s_add_u32 s84,s14,20480\n"
MF "v[108:111],a[216:219],a[144:147],v[108:111]\n"
"s_add_u32 m0,s63,2048\n"
MF "v[108:111],a[220:223],a[184:187],v[108:111]\n"
LD "v4,s[24:27],s84 offen lds\n"
MF "v[100:103],a[220:223],a[180:183],v[100:103]\n"
"s_waitcnt vmcnt(11) lgkmcnt(0)\n"
MF "v[100:103],a[216:219],a[140:143],v[100:103]\n"
"s_barrier\n"
MF "v[136:139],a[200:203],a[140:143],v[136:139]\n"
TR "a[216:217],v0 offset:32768\n"
MF "v[136:139],a[204:207],a[180:183],v[136:139]\n"
TR "a[218:219],v0 offset:33280\n"
MF "v[144:147],a[204:207],a[184:187],v[144:147]\n"
"ds_read_b128 a[60:63],v2 offset:40960\n"
MF "v[144:147],a[200:203],a[144:147],v[144:147]\n"
TR "a[220:221],v0 offset:36864\n"
MF "v[152:155],a[200:203],a[148:151],v[152:155]\n"
"s_add_u32 s84,s14,29696\n"
TR "a[222:223],v0 offset:37376\n"
MF "v[152:155],a[204:207],a[188:191],v[152:155]\n"
LD "v5,s[24:27],s84 offen offset:1024 lds\n"
MF "v[160:163],a[204:207],a[192:195],v[160:163]\n"
"ds_read_b128 a[100:103],v3 offset:40960\n"
MF "v[160:163],a[200:203],a[152:155],v[160:163]\n"
"s_add_u32 m0,s62,32768\n"
MF "v[168:171],a[200:203],a[156:159],v[168:171]\n"
"s_add_u32 s82,s66,1573120\n"
"s_sub_u32 s15,s82,7864320\n"
"s_min_u32 s82,s82,s15\n"
MF "v[168:171],a[204:207],a[196:199],v[168:171]\n"
LD "v6,s[28:31],s82 offen lds\n"
MF "v[172:175],a[212:215],a[196:199],v[172:175]\n"
TR "a[200:201],v0 offset:32776\n"
MF "v[172:175],a[208:211],a[156:159],v[172:175]\n"
TR "a[202:203],v0 offset:33288\n"
MF "v[164:167],a[208:211],a[152:155],v[164:167]\n"
"ds_read_b128 a[64:67],v2 offset:43008\n"
MF "v[164:167],a[212:215],a[192:195],v[164:167]\n"
TR "a[204:205],v0 offset:36872\n"
MF "v[156:159],a[212:215],a[188:191],v[156:159]\n"
TR "a[206:207],v0 offset:37384\n"
MF "v[156:159],a[208:211],a[148:151],v[156:159]\n"
LD "v11,s[28:31],s82 offen offset:1024 lds\n"
MF "v[148:151],a[208:211],a[144:147],v[148:151]\n"
"ds_read_b128 a[104:107],v3 offset:43008\n"
MF "v[148:151],a[212:215],a[184:187],v[148:151]\n"
MF "v[140:143],a[212:215],a[180:183],v[140:143]\n"
MF "v[140:143],a[208:211],a[140:143],v[140:143]\n"
"s_waitcnt lgkmcnt(7)\n"
LD "v12,s[28:31],s82 offen offset:2048 lds\n"
MF "v[176:179],a[216:219],a[140:143],v[176:179]\n"
TR "a[208:209],v1 offset:32768\n"
MF "v[176:179],a[220:223],a[180:183],v[176:179]\n"
TR "a[210:211],v1 offset:33280\n"
MF "v[184:187],a[220:223],a[184:187],v[184:187]\n"
"ds_read_b128 a[68:71],v2 offset:45056\n"
MF "v[184:187],a[216:219],a[144:147],v[184:187]\n"
TR "a[212:213],v1 offset:36864\n"
MF "v[192:195],a[216:219],a[148:151],v[192:195]\n"
TR "a[214:215],v1 offset:37376\n"
MF "v[192:195],a[220:223],a[188:191],v[192:195]\n"
LD "v13,s[28:31],s82 offen offset:3072 lds\n"
MF "v[200:203],a[220:223],a[192:195],v[200:203]\n"
"ds_read_b128 a[108:111],v3 offset:45056\n"
MF "v[200:203],a[216:219],a[152:155],v[200:203]\n"
"s_add_u32 s84,s14,40960\n"
MF "v[208:211],a[216:219],a[156:159],v[208:211]\n"
"s_add_u32 m0,s63,4096\n"
MF "v[208:211],a[220:223],a[196:199],v[208:211]\n"
"s_waitcnt lgkmcnt(7)\n"
LD "v4,s[24:27],s84 offen lds\n"
MF "v[212:215],a[204:207],a[196:199],v[212:215]\n"
"s_sub_u32 s66,s66,s83\n"
TR "a[216:217],v1 offset:32776\n"
MF "v[212:215],a[200:203],a[156:159],v[212:215]\n"
"s_add_u32 m0,s62,49152\n"
TR "a[218:219],v1 offset:33288\n"
MF "v[204:207],a[200:203],a[152:155],v[204:207]\n"
"s_add_u32 s82,s66,2359296\n"
"ds_read_b128 a[72:75],v2 offset:47104\n"
MF "v[204:207],a[204:207],a[192:195],v[204:207]\n"
"s_sub_u32 s15,s82,7864320\n"
TR "a[220:221],v1 offset:36872\n"
MF "v[196:199],a[204:207],a[188:191],v[196:199]\n"
"s_min_u32 s82,s82,s15\n"
TR "a[222:223],v1 offset:37384\n"
MF "v[196:199],a[200:203],a[148:151],v[196:199]\n"
LD "v6,s[28:31],s82 offen lds\n"
MF "v[188:191],a[200:203],a[144:147],v[188:191]\n"
"v_add_u32 v0,0xffff4000,v0\n"
MF "v[188:191],a[204:207],a[184:187],v[188:191]\n"
"v_add_u32 v1,0xffff4000,v1\n"
MF "v[180:183],a[204:207],a[180:183],v[180:183]\n"
"s_waitcnt vmcnt(14) lgkmcnt(0)\n"
LD "v11,s[28:31],s82 offen offset:1024 lds\n"
MF "v[180:183],a[200:203],a[140:143],v[180:183]\n"
"s_barrier\n"
MF "v[216:219],a[208:211],a[140:143],v[216:219]\n"
TR "a[200:201],v0\n"
MF "v[216:219],a[212:215],a[180:183],v[216:219]\n"
TR "a[202:203],v0 offset:512\n"
MF "v[224:227],a[212:215],a[184:187],v[224:227]\n"
"ds_read_b128 a[112:115],v3 offset:47104\n"
MF "v[224:227],a[208:211],a[144:147],v[224:227]\n"
TR "a[204:205],v0 offset:4096\n"
MF "v[232:235],a[208:211],a[148:151],v[232:235]\n"
TR "a[206:207],v0 offset:4608\n"
MF "v[232:235],a[212:215],a[188:191],v[232:235]\n"
LD "v12,s[28:31],s82 offen offset:2048 lds\n"
MF "v[240:243],a[212:215],a[192:195],v[240:243]\n"
"ds_read_b128 a[76:79],v2 offset:49152\n"
MF "v[240:243],a[208:211],a[152:155],v[240:243]\n"
"ds_read_b128 a[116:119],v3 offset:49152\n"
MF "v[248:251],a[208:211],a[156:159],v[248:251]\n"
MF "v[248:251],a[212:215],a[196:199],v[248:251]\n"
LD "v13,s[28:31],s82 offen offset:3072 lds\n"
MF "v[252:255],a[220:223],a[196:199],v[252:255]\n"
TR "a[208:209],v0 offset:8\n"
MF "v[252:255],a[216:219],a[156:159],v[252:255]\n"
TR "a[210:211],v0 offset:520\n"
MF "v[244:247],a[216:219],a[152:155],v[244:247]\n"
"s_add_u32 s14,s81,512\n"
"s_sub_u32 s15,s14,1280\n"
MF "v[244:247],a[220:223],a[192:195],v[244:247]\n"
"s_min_u32 s14,s14,s15\n"
"s_add_u32 s14,s14,s68\n"
TR "a[212:213],v0 offset:4104\n"
MF "v[236:239],a[220:223],a[188:191],v[236:239]\n"
"s_add_u32 s84,s14,0\n"
"s_add_u32 m0,s63,20480\n"
TR "a[214:215],v0 offset:4616\n"
MF "v[236:239],a[216:219],a[148:151],v[236:239]\n"
LD "v4,s[24:27],s84 offen lds\n"
MF "v[228:231],a[216:219],a[144:147],v[228:231]\n"
MF "v[228:231],a[220:223],a[184:187],v[228:231]\n"
MF "v[220:223],a[220:223],a[180:183],v[220:223]\n"
"s_add_u32 s84,s14,9216\n"
MF "v[220:223],a[216:219],a[140:143],v[220:223]\n"
"s_waitcnt lgkmcnt(4)\n"
LD "v5,s[24:27],s84 offen offset:1024 lds\n"
MF "v[16:19],a[200:203],a[60:63],v[16:19]\n"
TR "a[216:217],v1\n"
MF "v[16:19],a[204:207],a[100:103],v[16:19]\n"
"s_add_u32 m0,s62,65536\n"
TR "a[218:219],v1 offset:512\n"
MF "v[24:27],a[204:207],a[104:107],v[24:27]\n"
"s_add_u32 s82,s66,2359424\n"
MF "v[24:27],a[200:203],a[64:67],v[24:27]\n"
"s_sub_u32 s15,s82,7864320\n"
TR "a[220:221],v1 offset:4096\n"
MF "v[32:35],a[200:203],a[68:71],v[32:35]\n"
"s_min_u32 s82,s82,s15\n"
TR "a[222:223],v1 offset:4608\n"
MF "v[32:35],a[204:207],a[108:111],v[32:35]\n"
LD "v6,s[28:31],s82 offen lds\n"
MF "v[40:43],a[204:207],a[112:115],v[40:43]\n"
MF "v[40:43],a[200:203],a[72:75],v[40:43]\n"
MF "v[48:51],a[200:203],a[76:79],v[48:51]\n"
MF "v[48:51],a[204:207],a[116:119],v[48:51]\n"
"s_waitcnt lgkmcnt(4)\n"
LD "v11,s[28:31],s82 offen offset:1024 lds\n"
MF "v[52:55],a[212:215],a[116:119],v[52:55]\n"
TR "a[200:201],v1 offset:8\n"
MF "v[52:55],a[208:211],a[76:79],v[52:55]\n"
TR "a[202:203],v1 offset:520\n"
MF "v[44:47],a[208:211],a[72:75],v[44:47]\n"
MF "v[44:47],a[212:215],a[112:115],v[44:47]\n"
TR "a[204:205],v1 offset:4104\n"
MF "v[36:39],a[212:215],a[108:111],v[36:39]\n"
TR "a[206:207],v1 offset:4616\n"
MF "v[36:39],a[208:211],a[68:71],v[36:39]\n"
LD "v12,s[28:31],s82 offen offset:2048 lds\n"
MF "v[28:31],a[208:211],a[64:67],v[28:31]\n"
MF "v[28:31],a[212:215],a[104:107],v[28:31]\n"
MF "v[20:23],a[212:215],a[100:103],v[20:23]\n"
"s_waitcnt vmcnt(16) lgkmcnt(0)\n"
LD "v13,s[28:31],s82 offen offset:3072 lds\n"
MF "v[20:23],a[208:211],a[60:63],v[20:23]\n"
"s_barrier\n"
MF "v[56:59],a[216:219],a[60:63],v[56:59]\n"
"s_add_u32 s84,s14,20480\n"
TR "a[208:209],v0 offset:16384\n"
MF "v[56:59],a[220:223],a[100:103],v[56:59]\n"
"s_add_u32 m0,s63,22528\n"
TR "a[210:211],v0 offset:16896\n"
MF "v[64:67],a[220:223],a[104:107],v[64:67]\n"
LD "v4,s[24:27],s84 offen lds\n"
MF "v[64:67],a[216:219],a[64:67],v[64:67]\n"
TR "a[212:213],v0 offset:20480\n"
MF "v[72:75],a[216:219],a[68:71],v[72:75]\n"
TR "a[214:215],v0 offset:20992\n"
MF "v[72:75],a[220:223],a[108:111],v[72:75]\n"
MF "v[80:83],a[220:223],a[112:115],v[80:83]\n"
"s_add_u32 s84,s14,29696\n"
MF "v[80:83],a[216:219],a[72:75],v[80:83]\n"
LD "v5,s[24:27],s84 offen offset:1024 lds\n"
MF "v[88:91],a[216:219],a[76:79],v[88:91]\n"
"s_add_u32 m0,s62,81920\n"
MF "v[88:91],a[220:223],a[116:119],v[88:91]\n"
"s_add_u32 s82,s66,2359552\n"
MF "v[92:95],a[204:207],a[116:119],v[92:95]\n"
"s_sub_u32 s15,s82,7864320\n"
TR "a[216:217],v0 offset:16392\n"
MF "v[92:95],a[200:203],a[76:79],v[92:95]\n"
"s_min_u32 s82,s82,s15\n"
TR "a[218:219],v0 offset:16904\n"
MF "v[84:87],a[200:203],a[72:75],v[84:87]\n"
LD "v6,s[28:31],s82 offen lds\n"
MF "v[84:87],a[204:207],a[112:115],v[84:87]\n"
TR "a[220:221],v0 offset:20488\n"
MF "v[76:79],a[204:207],a[108:111],v[76:79]\n"
TR "a[222:223],v0 offset:21000\n"
MF "v[76:79],a[200:203],a[68:71],v[76:79]\n"
MF "v[68:71],a[200:203],a[64:67],v[68:71]\n"
MF "v[68:71],a[204:207],a[104:107],v[68:71]\n"
LD "v11,s[28:31],s82 offen offset:1024 lds\n"
MF "v[60:63],a[204:207],a[100:103],v[60:63]\n"
MF "v[60:63],a[200:203],a[60:63],v[60:63]\n"
"s_waitcnt lgkmcnt(4)\n"
MF "v[96:99],a[208:211],a[60:63],v[96:99]\n"
TR "a[200:201],v1 offset:16384\n"
MF "v[96:99],a[212:215],a[100:103],v[96:99]\n"
TR "a[202:203],v1 offset:16896\n"
MF "v[104:107],a[212:215],a[104:107],v[104:107]\n"
LD "v12,s[28:31],s82 offen offset:2048 lds\n"
MF "v[104:107],a[208:211],a[64:67],v[104:107]\n"
TR "a[204:205],v1 offset:20480\n"
MF "v[112:115],a[208:211],a[68:71],v[112:115]\n"
TR "a[206:207],v1 offset:20992\n"
MF "v[112:115],a[212:215],a[108:111],v[112:115]\n"
MF "v[120:123],a[212:215],a[112:115],v[120:123]\n"
MF "v[120:123],a[208:211],a[72:75],v[120:123]\n"
LD "v13,s[28:31],s82 offen offset:3072 lds\n"
MF "v[128:131],a[208:211],a[76:79],v[128:131]\n"
MF "v[128:131],a[212:215],a[116:119],v[128:131]\n"
"s_waitcnt lgkmcnt(4)\n"
MF "v[132:135],a[220:223],a[116:119],v[132:135]\n"
"s_add_u32 s84,s14,40960\n"
TR "a[208:209],v1 offset:16392\n"
MF "v[132:135],a[216:219],a[76:79],v[132:135]\n"
"s_add_u32 m0,s63,24576\n"
TR "a[210:211],v1 offset:16904\n"
MF "v[124:127],a[216:219],a[72:75],v[124:127]\n"
LD "v4,s[24:27],s84 offen lds\n"
MF "v[124:127],a[220:223],a[112:115],v[124:127]\n"
"s_add_u32 m0,s62,0\n"
TR "a[212:213],v1 offset:20488\n"
MF "v[116:119],a[220:223],a[108:111],v[116:119]\n"
"s_add_u32 s82,s66,3145728\n"
TR "a[214:215],v1 offset:21000\n"
MF "v[116:119],a[216:219],a[68:71],v[116:119]\n"
"s_sub_u32 s15,s82,7864320\n"
MF "v[108:111],a[216:219],a[64:67],v[108:111]\n"
"s_min_u32 s82,s82,s15\n"
MF "v[108:111],a[220:223],a[104:107],v[108:111]\n"
LD "v6,s[28:31],s82 offen lds\n"
MF "v[100:103],a[220:223],a[100:103],v[100:103]\n"
"s_waitcnt vmcnt(18) lgkmcnt(0)\n"
MF "v[100:103],a[216:219],a[60:63],v[100:103]\n"
"s_barrier\n"
MF "v[136:139],a[200:203],a[60:63],v[136:139]\n"
TR "a[216:217],v0 offset:32768\n"
MF "v[136:139],a[204:207],a[100:103],v[136:139]\n"
TR "a[218:219],v0 offset:33280\n"
MF "v[144:147],a[204:207],a[104:107],v[144:147]\n"
"ds_read_b128 a[140:143],v2\n"
MF "v[144:147],a[200:203],a[64:67],v[144:147]\n"
TR "a[220:221],v0 offset:36864\n"
MF "v[152:155],a[200:203],a[68:71],v[152:155]\n"
TR "a[222:223],v0 offset:37376\n"
MF "v[152:155],a[204:207],a[108:111],v[152:155]\n"
LD "v11,s[28:31],s82 offen offset:1024 lds\n"
MF "v[160:163],a[204:207],a[112:115],v[160:163]\n"
"ds_read_b128 a[180:183],v3\n"
MF "v[160:163],a[200:203],a[72:75],v[160:163]\n"
MF "v[168:171],a[200:203],a[76:79],v[168:171]\n"
MF "v[168:171],a[204:207],a[116:119],v[168:171]\n"
LD "v12,s[28:31],s82 offen offset:2048 lds\n"
MF "v[172:175],a[212:215],a[116:119],v[172:175]\n"
TR "a[200:201],v0 offset:32776\n"
MF "v[172:175],a[208:211],a[76:79],v[172:175]\n"
TR "a[202:203],v0 offset:33288\n"
MF "v[164:167],a[208:211],a[72:75],v[164:167]\n"
"ds_read_b128 a[144:147],v2 offset:2048\n"
MF "v[164:167],a[212:215],a[112:115],v[164:167]\n"
TR "a[204:205],v0 offset:36872\n"
MF "v[156:159],a[212:215],a[108:111],v[156:159]\n"
TR "a[206:207],v0 offset:37384\n"
MF "v[156:159],a[208:211],a[68:71],v[156:159]\n"
LD "v13,s[28:31],s82 offen offset:3072 lds\n"
MF "v[148:151],a[208:211],a[64:67],v[148:151]\n"
"ds_read_b128 a[184:187],v3 offset:2048\n"
MF "v[148:151],a[212:215],a[104:107],v[148:151]\n"
"s_add_u32 s14,s81,640\n"
"s_sub_u32 s15,s14,1280\n"
"s_min_u32 s14,s14,s15\n"
MF "v[140:143],a[212:215],a[100:103],v[140:143]\n"
"s_add_u32 s14,s14,s68\n"
"s_add_u32 s84,s14,0\n"
"s_add_u32 m0,s63,40960\n"
MF "v[140:143],a[208:211],a[60:63],v[140:143]\n"
"s_waitcnt lgkmcnt(7)\n"
LD "v4,s[24:27],s84 offen lds\n"
MF "v[176:179],a[216:219],a[60:63],v[176:179]\n"
TR "a[208:209],v1 offset:32768\n"
MF "v[176:179],a[220:223],a[100:103],v[176:179]\n"
TR "a[210:211],v1 offset:33280\n"
MF "v[184:187],a[220:223],a[104:107],v[184:187]\n"
"ds_read_b128 a[148:151],v2 offset:4096\n"
MF "v[184:187],a[216:219],a[64:67],v[184:187]\n"
TR "a[212:213],v1 offset:36864\n"
MF "v[192:195],a[216:219],a[68:71],v[192:195]\n"
"s_add_u32 s84,s14,9216\n"
TR "a[214:215],v1 offset:37376\n"
MF "v[192:195],a[220:223],a[108:111],v[192:195]\n"
LD "v5,s[24:27],s84 offen offset:1024 lds\n"
MF "v[200:203],a[220:223],a[112:115],v[200:203]\n"
"ds_read_b128 a[188:191],v3 offset:4096\n"
MF "v[200:203],a[216:219],a[72:75],v[200:203]\n"
"s_add_u32 m0,s62,16384\n"
MF "v[208:211],a[216:219],a[76:79],v[208:211]\n"
"s_add_u32 s82,s66,3145856\n"
"s_sub_u32 s15,s82,7864320\n"
"s_min_u32 s82,s82,s15\n"
MF "v[208:211],a[220:223],a[116:119],v[208:211]\n"
"s_waitcnt lgkmcnt(7)\n"
LD "v6,s[28:31],s82 offen lds\n"
MF "v[212:215],a[204:207],a[116:119],v[212:215]\n"
TR "a[216:217],v1 offset:32776\n"
MF "v[212:215],a[200:203],a[76:79],v[212:215]\n"
TR "a[218:219],v1 offset:33288\n"
MF "v[204:207],a[200:203],a[72:75],v[204:207]\n"
"ds_read_b128 a[152:155],v2 offset:6144\n"
MF "v[204:207],a[204:207],a[112:115],v[204:207]\n"
TR "a[220:221],v1 offset:36872\n"
MF "v[196:199],a[204:207],a[108:111],v[196:199]\n"
TR "a[222:223],v1 offset:37384\n"
MF "v[196:199],a[200:203],a[68:71],v[196:199]\n"
LD "v11,s[28:31],s82 offen offset:1024 lds\n"
MF "v[188:191],a[200:203],a[64:67],v[188:191]\n"
"v_add_u32 v0,0xc000,v0\n"
MF "v[188:191],a[204:207],a[104:107],v[188:191]\n"
"v_add_u32 v1,0xc000,v1\n"
MF "v[180:183],a[204:207],a[100:103],v[180:183]\n"
"s_waitcnt vmcnt(21) lgkmcnt(0)\n"
LD "v12,s[28:31],s82 offen offset:2048 lds\n"
MF "v[180:183],a[200:203],a[60:63],v[180:183]\n"
"s_barrier\n"
MF "v[216:219],a[208:211],a[60:63],v[216:219]\n"
TR "a[200:201],v0\n"
MF "v[216:219],a[212:215],a[100:103],v[216:219]\n"
TR "a[202:203],v0 offset:512\n"
MF "v[224:227],a[212:215],a[104:107],v[224:227]\n"
"ds_read_b128 a[192:195],v3 offset:6144\n"
MF "v[224:227],a[208:211],a[64:67],v[224:227]\n"
TR "a[204:205],v0 offset:4096\n"
MF "v[232:235],a[208:211],a[68:71],v[232:235]\n"
TR "a[206:207],v0 offset:4608\n"
MF "v[232:235],a[212:215],a[108:111],v[232:235]\n"
LD "v13,s[28:31],s82 offen offset:3072 lds\n"
MF "v[240:243],a[212:215],a[112:115],v[240:243]\n"
"ds_read_b128 a[156:159],v2 offset:8192\n"
MF "v[240:243],a[208:211],a[72:75],v[240:243]\n"
"s_add_u32 s84,s14,20480\n"
"ds_read_b128 a[196:199],v3 offset:8192\n"
MF "v[248:251],a[208:211],a[76:79],v[248:251]\n"
"s_add_u32 m0,s63,43008\n"
MF "v[248:251],a[212:215],a[116:119],v[248:251]\n"
LD "v4,s[24:27],s84 offen lds\n"
MF "v[252:255],a[220:223],a[116:119],v[252:255]\n"
TR "a[208:209],v0 offset:8\n"
MF "v[252:255],a[216:219],a[76:79],v[252:255]\n"
TR "a[210:211],v0 offset:520\n"
MF "v[244:247],a[216:219],a[72:75],v[244:247]\n"
MF "v[244:247],a[220:223],a[112:115],v[244:247]\n"
TR "a[212:213],v0 offset:4104\n"
MF "v[236:239],a[220:223],a[108:111],v[236:239]\n"
"s_add_u32 s84,s14,29696\n"
TR "a[214:215],v0 offset:4616\n"
MF "v[236:239],a[216:219],a[68:71],v[236:239]\n"
LD "v5,s[24:27],s84 offen offset:1024 lds\n"
MF "v[228:231],a[216:219],a[64:67],v[228:231]\n"
MF "v[228:231],a[220:223],a[104:107],v[228:231]\n"
"s_add_u32 m0,s62,32768\n"
MF "v[220:223],a[220:223],a[100:103],v[220:223]\n"
"s_add_u32 s82,s66,3145984\n"
"s_sub_u32 s15,s82,7864320\n"
"s_min_u32 s82,s82,s15\n"
MF "v[220:223],a[216:219],a[60:63],v[220:223]\n"
"s_waitcnt lgkmcnt(4)\n"
LD "v6,s[28:31],s82 offen lds\n"
MF "v[16:19],a[200:203],a[140:143],v[16:19]\n"
TR "a[216:217],v1\n"
MF "v[16:19],a[204:207],a[180:183],v[16:19]\n"
TR "a[218:219],v1 offset:512\n"
MF "v[24:27],a[204:207],a[184:187],v[24:27]\n"
MF "v[24:27],a[200:203],a[144:147],v[24:27]\n"
TR "a[220:221],v1 offset:4096\n"
MF "v[32:35],a[200:203],a[148:151],v[32:35]\n"
TR "a[222:223],v1 offset:4608\n"
MF "v[32:35],a[204:207],a[188:191],v[32:35]\n"
LD "v11,s[28:31],s82 offen offset:1024 lds\n"
MF "v[40:43],a[204:207],a[192:195],v[40:43]\n"
MF "v[40:43],a[200:203],a[152:155],v[40:43]\n"
MF "v[48:51],a[200:203],a[156:159],v[48:51]\n"
MF "v[48:51],a[204:207],a[196:199],v[48:51]\n"
"s_waitcnt lgkmcnt(4)\n"
LD "v12,s[28:31],s82 offen offset:2048 lds\n"
MF "v[52:55],a[212:215],a[196:199],v[52:55]\n"
TR "a[200:201],v1 offset:8\n"
MF "v[52:55],a[208:211],a[156:159],v[52:55]\n"
TR "a[202:203],v1 offset:520\n"
MF "v[44:47],a[208:211],a[152:155],v[44:47]\n"
MF "v[44:47],a[212:215],a[192:195],v[44:47]\n"
TR "a[204:205],v1 offset:4104\n"
MF "v[36:39],a[212:215],a[188:191],v[36:39]\n"
".Lpg2a:\n"
"s_cbranch_execz .Lisr1\n"
TR "a[206:207],v1 offset:4616\n"
MF "v[36:39],a[208:211],a[148:151],v[36:39]\n"
LD "v13,s[28:31],s82 offen offset:3072 lds\n"
MF "v[28:31],a[208:211],a[144:147],v[28:31]\n"
"s_add_u32 s84,s14,40960\n"
MF "v[28:31],a[212:215],a[184:187],v[28:31]\n"
"s_add_u32 m0,s63,45056\n"
MF "v[20:23],a[212:215],a[180:183],v[20:23]\n"
"s_waitcnt vmcnt(23) lgkmcnt(0)\n"
LD "v4,s[24:27],s84 offen lds\n"
MF "v[20:23],a[208:211],a[140:143],v[20:23]\n"
"s_barrier\n"
AW "a253,v10\n"
"v_add_u32 v10,1,v10\n"
"s_bfe_u32 s58,s22,0x30014\n"
"s_lshl_b32 s58,s58,2\n"
"s_add_u32 s58,s58,s56\n"
"s_lshl_b32 s58,s58,6\n"
"s_add_u32 s58,s58,s86\n"
"v_readlane_b32 s76,v10,s78\n"
"buffer_store_dword v10,v14,s[40:43],s58 offen offset:128 sc1\n"
".Lhs5:\n"
MF "v[56:59],a[216:219],a[140:143],v[56:59]\n"
"s_add_u32 m0,s62,49152\n"
"s_add_u32 s82,s66,3932160\n"
TR "a[208:209],v0 offset:16384\n"
MF "v[56:59],a[220:223],a[180:183],v[56:59]\n"
"s_sub_u32 s15,s82,7864320\n"
"s_min_u32 s82,s82,s15\n"
TR "a[210:211],v0 offset:16896\n"
MF "v[64:67],a[220:223],a[184:187],v[64:67]\n"
LD "v6,s[28:31],s82 offen lds\n"
MF "v[64:67],a[216:219],a[144:147],v[64:67]\n"
TR "a[212:213],v0 offset:20480\n"
MF "v[72:75],a[216:219],a[148:151],v[72:75]\n"
TR "a[214:215],v0 offset:20992\n"
MF "v[72:75],a[220:223],a[188:191],v[72:75]\n"
"buffer_load_dword a249,v15,s[48:51],s60 offen offset:1024\n"
MF "v[80:83],a[220:223],a[192:195],v[80:83]\n"
"s_bfe_u32 s59,s23,0x30003\n"
MF "v[80:83],a[216:219],a[152:155],v[80:83]\n"
LD "v11,s[28:31],s82 offen offset:1024 lds\n"
MF "v[88:91],a[216:219],a[156:159],v[88:91]\n"
"s_add_u32 s59,s59,1\n"
MF "v[88:91],a[220:223],a[196:199],v[88:91]\n"
"s_lshl_b32 s59,s59,7\n"
MF "v[92:95],a[204:207],a[196:199],v[92:95]\n"
"s_and_b32 s59,s59,768\n"
TR "a[216:217],v0 offset:16392\n"
MF "v[92:95],a[200:203],a[156:159],v[92:95]\n"
"s_add_u32 s59,s59,s60\n"
TR "a[218:219],v0 offset:16904\n"
MF "v[84:87],a[200:203],a[152:155],v[84:87]\n"
LD "v12,s[28:31],s82 offen offset:2048 lds\n"
MF "v[84:87],a[204:207],a[192:195],v[84:87]\n"
TR "a[220:221],v0 offset:20488\n"
MF "v[76:79],a[204:207],a[188:191],v[76:79]\n"
TR "a[222:223],v0 offset:21000\n"
MF "v[76:79],a[200:203],a[148:151],v[76:79]\n"
MF "v[68:71],a[200:203],a[144:147],v[68:71]\n"
MF "v[68:71],a[204:207],a[184:187],v[68:71]\n"
LD "v13,s[28:31],s82 offen offset:3072 lds\n"
MF "v[60:63],a[204:207],a[180:183],v[60:63]\n"
MF "v[60:63],a[200:203],a[140:143],v[60:63]\n"
"s_add_u32 s14,s81,768\n"
"s_sub_u32 s15,s14,1280\n"
"s_waitcnt lgkmcnt(4)\n"
MF "v[96:99],a[208:211],a[140:143],v[96:99]\n"
"s_min_u32 s14,s14,s15\n"
"s_add_u32 s14,s14,s68\n"
TR "a[200:201],v1 offset:16384\n"
MF "v[96:99],a[212:215],a[180:183],v[96:99]\n"
"s_add_u32 s84,s14,0\n"
"s_add_u32 m0,s63,0\n"
TR "a[202:203],v1 offset:16896\n"
MF "v[104:107],a[212:215],a[184:187],v[104:107]\n"
LD "v4,s[24:27],s84 offen lds\n"
MF "v[104:107],a[208:211],a[144:147],v[104:107]\n"
TR "a[204:205],v1 offset:20480\n"
MF "v[112:115],a[208:211],a[148:151],v[112:115]\n"
TR "a[206:207],v1 offset:20992\n"
MF "v[112:115],a[212:215],a[188:191],v[112:115]\n"
MF "v[120:123],a[212:215],a[192:195],v[120:123]\n"
"s_add_u32 s84,s14,9216\n"
MF "v[120:123],a[208:211],a[152:155],v[120:123]\n"
LD "v5,s[24:27],s84 offen offset:1024 lds\n"
MF "v[128:131],a[208:211],a[156:159],v[128:131]\n"
MF "v[128:131],a[212:215],a[196:199],v[128:131]\n"
"s_waitcnt lgkmcnt(4)\n"
MF "v[132:135],a[220:223],a[196:199],v[132:135]\n"
TR "a[208:209],v1 offset:16392\n"
MF "v[132:135],a[216:219],a[156:159],v[132:135]\n"
TR "a[210:211],v1 offset:16904\n"
MF "v[124:127],a[216:219],a[152:155],v[124:127]\n"
MF "v[124:127],a[220:223],a[192:195],v[124:127]\n"
TR "a[212:213],v1 offset:20488\n"
MF "v[116:119],a[220:223],a[188:191],v[116:119]\n"
TR "a[214:215],v1 offset:21000\n"
MF "v[116:119],a[216:219],a[148:151],v[116:119]\n"
MF "v[108:111],a[216:219],a[144:147],v[108:111]\n"
MF "v[108:111],a[220:223],a[184:187],v[108:111]\n"
MF "v[100:103],a[220:223],a[180:183],v[100:103]\n"
"s_waitcnt vmcnt(24) lgkmcnt(0)\n"
MF "v[100:103],a[216:219],a[140:143],v[100:103]\n"
"s_barrier\n"
MF "v[136:139],a[200:203],a[140:143],v[136:139]\n"
TR "a[216:217],v0 offset:32768\n"
MF "v[136:139],a[204:207],a[180:183],v[136:139]\n"
"s_add_u32 m0,s62,65536\n"
TR "a[218:219],v0 offset:33280\n"
MF "v[144:147],a[204:207],a[184:187],v[144:147]\n"
"s_add_u32 s82,s66,3932288\n"
"ds_read_b128 a[60:63],v2 offset:20480\n"
MF "v[144:147],a[200:203],a[144:147],v[144:147]\n"
"s_sub_u32 s15,s82,7864320\n"
TR "a[220:221],v0 offset:36864\n"
MF "v[152:155],a[200:203],a[148:151],v[152:155]\n"
"s_min_u32 s82,s82,s15\n"
TR "a[222:223],v0 offset:37376\n"
MF "v[152:155],a[204:207],a[188:191],v[152:155]\n"
LD "v6,s[28:31],s82 offen lds\n"
MF "v[160:163],a[204:207],a[192:195],v[160:163]\n"
"ds_read_b128 a[100:103],v3 offset:20480\n"
MF "v[160:163],a[200:203],a[152:155],v[160:163]\n"
MF "v[168:171],a[200:203],a[156:159],v[168:171]\n"
MF "v[168:171],a[204:207],a[196:199],v[168:171]\n"
LD "v11,s[28:31],s82 offen offset:1024 lds\n"
MF "v[172:175],a[212:215],a[196:199],v[172:175]\n"
TR "a[200:201],v0 offset:32776\n"
MF "v[172:175],a[208:211],a[156:159],v[172:175]\n"
TR "a[202:203],v0 offset:33288\n"
MF "v[164:167],a[208:211],a[152:155],v[164:167]\n"
"ds_read_b128 a[64:67],v2 offset:22528\n"
MF "v[164:167],a[212:215],a[192:195],v[164:167]\n"
TR "a[204:205],v0 offset:36872\n"
MF "v[156:159],a[212:215],a[188:191],v[156:159]\n"
TR "a[206:207],v0 offset:37384\n"
MF "v[156:159],a[208:211],a[148:151],v[156:159]\n"
LD "v12,s[28:31],s82 offen offset:2048 lds\n"
MF "v[148:151],a[208:211],a[144:147],v[148:151]\n"
"ds_read_b128 a[104:107],v3 offset:22528\n"
MF "v[148:151],a[212:215],a[184:187],v[148:151]\n"
MF "v[140:143],a[212:215],a[180:183],v[140:143]\n"
MF "v[140:143],a[208:211],a[140:143],v[140:143]\n"
"s_waitcnt lgkmcnt(7)\n"
LD "v13,s[28:31],s82 offen offset:3072 lds\n"
MF "v[176:179],a[216:219],a[140:143],v[176:179]\n"
TR "a[208:209],v1 offset:32768\n"
MF "v[176:179],a[220:223],a[180:183],v[176:179]\n"
TR "a[210:211],v1 offset:33280\n"
MF "v[184:187],a[220:223],a[184:187],v[184:187]\n"
"ds_read_b128 a[68:71],v2 offset:24576\n"
MF "v[184:187],a[216:219],a[144:147],v[184:187]\n"
"s_add_u32 s84,s14,20480\n"
TR "a[212:213],v1 offset:36864\n"
MF "v[192:195],a[216:219],a[148:151],v[192:195]\n"
"s_add_u32 m0,s63,2048\n"
TR "a[214:215],v1 offset:37376\n"
MF "v[192:195],a[220:223],a[188:191],v[192:195]\n"
LD "v4,s[24:27],s84 offen lds\n"
MF "v[200:203],a[220:223],a[192:195],v[200:203]\n"
"ds_read_b128 a[108:111],v3 offset:24576\n"
MF "v[200:203],a[216:219],a[152:155],v[200:203]\n"
MF "v[208:211],a[216:219],a[156:159],v[208:211]\n"
"s_add_u32 s84,s14,29696\n"
MF "v[208:211],a[220:223],a[196:199],v[208:211]\n"
"s_waitcnt lgkmcnt(7)\n"
LD "v5,s[24:27],s84 offen offset:1024 lds\n"
MF "v[212:215],a[204:207],a[196:199],v[212:215]\n"
TR "a[216:217],v1 offset:32776\n"
MF "v[212:215],a[200:203],a[156:159],v[212:215]\n"
TR "a[218:219],v1 offset:33288\n"
MF "v[204:207],a[200:203],a[152:155],v[204:207]\n"
"ds_read_b128 a[72:75],v2 offset:26624\n"
MF "v[204:207],a[204:207],a[192:195],v[204:207]\n"
TR "a[220:221],v1 offset:36872\n"
MF "v[196:199],a[204:207],a[188:191],v[196:199]\n"
TR "a[222:223],v1 offset:37384\n"
MF "v[196:199],a[200:203],a[148:151],v[196:199]\n"
MF "v[188:191],a[200:203],a[144:147],v[188:191]\n"
"v_add_u32 v0,0xffff4000,v0\n"
MF "v[188:191],a[204:207],a[184:187],v[188:191]\n"
"v_add_u32 v1,0xffff4000,v1\n"
MF "v[180:183],a[204:207],a[180:183],v[180:183]\n"
"s_waitcnt vmcnt(26) lgkmcnt(0)\n"
MF "v[180:183],a[200:203],a[140:143],v[180:183]\n"
"s_barrier\n"
MF "v[216:219],a[208:211],a[140:143],v[216:219]\n"
TR "a[200:201],v0\n"
MF "v[216:219],a[212:215],a[180:183],v[216:219]\n"
"s_add_u32 m0,s62,81920\n"
TR "a[202:203],v0 offset:512\n"
MF "v[224:227],a[212:215],a[184:187],v[224:227]\n"
"s_add_u32 s82,s66,3932416\n"
"ds_read_b128 a[112:115],v3 offset:26624\n"
MF "v[224:227],a[208:211],a[144:147],v[224:227]\n"
"s_sub_u32 s15,s82,7864320\n"
TR "a[204:205],v0 offset:4096\n"
MF "v[232:235],a[208:211],a[148:151],v[232:235]\n"
"s_min_u32 s82,s82,s15\n"
TR "a[206:207],v0 offset:4608\n"
MF "v[232:235],a[212:215],a[188:191],v[232:235]\n"
LD "v6,s[28:31],s82 offen lds\n"
MF "v[240:243],a[212:215],a[192:195],v[240:243]\n"
"ds_read_b128 a[76:79],v2 offset:28672\n"
MF "v[240:243],a[208:211],a[152:155],v[240:243]\n"
"ds_read_b128 a[116:119],v3 offset:28672\n"
MF "v[248:251],a[208:211],a[156:159],v[248:251]\n"
MF "v[248:251],a[212:215],a[196:199],v[248:251]\n"
LD "v11,s[28:31],s82 offen offset:1024 lds\n"
MF "v[252:255],a[220:223],a[196:199],v[252:255]\n"
TR "a[208:209],v0 offset:8\n"
MF "v[252:255],a[216:219],a[156:159],v[252:255]\n"
TR "a[210:211],v0 offset:520\n"
MF "v[244:247],a[216:219],a[152:155],v[244:247]\n"
MF "v[244:247],a[220:223],a[192:195],v[244:247]\n"
TR "a[212:213],v0 offset:4104\n"
MF "v[236:239],a[220:223],a[188:191],v[236:239]\n"
TR "a[214:215],v0 offset:4616\n"
MF "v[236:239],a[216:219],a[148:151],v[236:239]\n"
LD "v12,s[28:31],s82 offen offset:2048 lds\n"
MF "v[228:231],a[216:219],a[144:147],v[228:231]\n"
MF "v[228:231],a[220:223],a[184:187],v[228:231]\n"
MF "v[220:223],a[220:223],a[180:183],v[220:223]\n"
MF "v[220:223],a[216:219],a[140:143],v[220:223]\n"
"s_waitcnt lgkmcnt(4)\n"
LD "v13,s[28:31],s82 offen offset:3072 lds\n"
MF "v[16:19],a[200:203],a[60:63],v[16:19]\n"
TR "a[216:217],v1\n"
MF "v[16:19],a[204:207],a[100:103],v[16:19]\n"
TR "a[218:219],v1 offset:512\n"
MF "v[24:27],a[204:207],a[104:107],v[24:27]\n"
MF "v[24:27],a[200:203],a[64:67],v[24:27]\n"
"s_add_u32 s84,s14,40960\n"
TR "a[220:221],v1 offset:4096\n"
MF "v[32:35],a[200:203],a[68:71],v[32:35]\n"
"s_add_u32 m0,s63,4096\n"
TR "a[222:223],v1 offset:4608\n"
MF "v[32:35],a[204:207],a[108:111],v[32:35]\n"
LD "v4,s[24:27],s84 offen lds\n"
MF "v[40:43],a[204:207],a[112:115],v[40:43]\n"
MF "v[40:43],a[200:203],a[72:75],v[40:43]\n"
MF "v[48:51],a[200:203],a[76:79],v[48:51]\n"
MF "v[48:51],a[204:207],a[116:119],v[48:51]\n"
"s_waitcnt lgkmcnt(4)\n"
MF "v[52:55],a[212:215],a[116:119],v[52:55]\n"
TR "a[200:201],v1 offset:8\n"
MF "v[52:55],a[208:211],a[76:79],v[52:55]\n"
TR "a[202:203],v1 offset:520\n"
MF "v[44:47],a[208:211],a[72:75],v[44:47]\n"
MF "v[44:47],a[212:215],a[112:115],v[44:47]\n"
TR "a[204:205],v1 offset:4104\n"
MF "v[36:39],a[212:215],a[108:111],v[36:39]\n"
TR "a[206:207],v1 offset:4616\n"
MF "v[36:39],a[208:211],a[68:71],v[36:39]\n"
MF "v[28:31],a[208:211],a[64:67],v[28:31]\n"
MF "v[28:31],a[212:215],a[104:107],v[28:31]\n"
MF "v[20:23],a[212:215],a[100:103],v[20:23]\n"
"s_waitcnt vmcnt(25) lgkmcnt(0)\n"
MF "v[20:23],a[208:211],a[60:63],v[20:23]\n"
"s_barrier\n"
MF "v[56:59],a[216:219],a[60:63],v[56:59]\n"
"s_add_u32 m0,s62,0\n"
"s_add_u32 s82,s66,4718592\n"
TR "a[208:209],v0 offset:16384\n"
MF "v[56:59],a[220:223],a[100:103],v[56:59]\n"
"s_sub_u32 s15,s82,7864320\n"
"s_min_u32 s82,s82,s15\n"
TR "a[210:211],v0 offset:16896\n"
MF "v[64:67],a[220:223],a[104:107],v[64:67]\n"
LD "v6,s[28:31],s82 offen lds\n"
MF "v[64:67],a[216:219],a[64:67],v[64:67]\n"
TR "a[212:213],v0 offset:20480\n"
MF "v[72:75],a[216:219],a[68:71],v[72:75]\n"
TR "a[214:215],v0 offset:20992\n"
MF "v[72:75],a[220:223],a[108:111],v[72:75]\n"
"buffer_load_dword a249,v15,s[48:51],s59 offen\n"
MF "v[80:83],a[220:223],a[112:115],v[80:83]\n"
"s_bfe_u32 s59,s23,0x30003\n"
MF "v[80:83],a[216:219],a[72:75],v[80:83]\n"
LD "v11,s[28:31],s82 offen offset:1024 lds\n"
MF "v[88:91],a[216:219],a[76:79],v[88:91]\n"
"s_add_u32 s59,s59,3\n"
MF "v[88:91],a[220:223],a[116:119],v[88:91]\n"
"s_lshl_b32 s59,s59,7\n"
MF "v[92:95],a[204:207],a[116:119],v[92:95]\n"
"s_and_b32 s59,s59,768\n"
TR "a[216:217],v0 offset:16392\n"
MF "v[92:95],a[200:203],a[76:79],v[92:95]\n"
"s_add_u32 s59,s59,s60\n"
TR "a[218:219],v0 offset:16904\n"
MF "v[84:87],a[200:203],a[72:75],v[84:87]\n"
LD "v12,s[28:31],s82 offen offset:2048 lds\n"
MF "v[84:87],a[204:207],a[112:115],v[84:87]\n"
TR "a[220:221],v0 offset:20488\n"
MF "v[76:79],a[204:207],a[108:111],v[76:79]\n"
TR "a[222:223],v0 offset:21000\n"
MF "v[76:79],a[200:203],a[68:71],v[76:79]\n"
MF "v[68:71],a[200:203],a[64:67],v[68:71]\n"
MF "v[68:71],a[204:207],a[104:107],v[68:71]\n"
LD "v13,s[28:31],s82 offen offset:3072 lds\n"
MF "v[60:63],a[204:207],a[100:103],v[60:63]\n"
MF "v[60:63],a[200:203],a[60:63],v[60:63]\n"
"s_add_u32 s14,s81,896\n"
"s_sub_u32 s15,s14,1280\n"
"s_waitcnt lgkmcnt(4)\n"
MF "v[96:99],a[208:211],a[60:63],v[96:99]\n"
"s_min_u32 s14,s14,s15\n"
"s_add_u32 s14,s14,s68\n"
TR "a[200:201],v1 offset:16384\n"
MF "v[96:99],a[212:215],a[100:103],v[96:99]\n"
"s_add_u32 s84,s14,0\n"
"s_add_u32 m0,s63,20480\n"
TR "a[202:203],v1 offset:16896\n"
MF "v[104:107],a[212:215],a[104:107],v[104:107]\n"
LD "v4,s[24:27],s84 offen lds\n"
MF "v[104:107],a[208:211],a[64:67],v[104:107]\n"
TR "a[204:205],v1 offset:20480\n"
MF "v[112:115],a[208:211],a[68:71],v[112:115]\n"
TR "a[206:207],v1 offset:20992\n"
MF "v[112:115],a[212:215],a[108:111],v[112:115]\n"
MF "v[120:123],a[212:215],a[112:115],v[120:123]\n"
"s_add_u32 s84,s14,9216\n"
MF "v[120:123],a[208:211],a[72:75],v[120:123]\n"
LD "v5,s[24:27],s84 offen offset:1024 lds\n"
MF "v[128:131],a[208:211],a[76:79],v[128:131]\n"
MF "v[128:131],a[212:215],a[116:119],v[128:131]\n"
"s_waitcnt lgkmcnt(4)\n"
MF "v[132:135],a[220:223],a[116:119],v[132:135]\n"
TR "a[208:209],v1 offset:16392\n"
MF "v[132:135],a[216:219],a[76:79],v[132:135]\n"
TR "a[210:211],v1 offset:16904\n"
MF "v[124:127],a[216:219],a[72:75],v[124:127]\n"
MF "v[124:127],a[220:223],a[112:115],v[124:127]\n"
TR "a[212:213],v1 offset:20488\n"
MF "v[116:119],a[220:223],a[108:111],v[116:119]\n"
TR "a[214:215],v1 offset:21000\n"
MF "v[116:119],a[216:219],a[68:71],v[116:119]\n"
MF "v[108:111],a[216:219],a[64:67],v[108:111]\n"
MF "v[108:111],a[220:223],a[104:107],v[108:111]\n"
MF "v[100:103],a[220:223],a[100:103],v[100:103]\n"
"s_waitcnt vmcnt(25) lgkmcnt(0)\n"
MF "v[100:103],a[216:219],a[60:63],v[100:103]\n"
"s_barrier\n"
MF "v[136:139],a[200:203],a[60:63],v[136:139]\n"
TR "a[216:217],v0 offset:32768\n"
MF "v[136:139],a[204:207],a[100:103],v[136:139]\n"
"s_add_u32 m0,s62,16384\n"
TR "a[218:219],v0 offset:33280\n"
MF "v[144:147],a[204:207],a[104:107],v[144:147]\n"
"s_add_u32 s82,s66,4718720\n"
"ds_read_b128 a[140:143],v2 offset:40960\n"
MF "v[144:147],a[200:203],a[64:67],v[144:147]\n"
"s_sub_u32 s15,s82,7864320\n"
TR "a[220:221],v0 offset:36864\n"
MF "v[152:155],a[200:203],a[68:71],v[152:155]\n"
"s_min_u32 s82,s82,s15\n"
TR "a[222:223],v0 offset:37376\n"
MF "v[152:155],a[204:207],a[108:111],v[152:155]\n"
LD "v6,s[28:31],s82 offen lds\n"
MF "v[160:163],a[204:207],a[112:115],v[160:163]\n"
"ds_read_b128 a[180:183],v3 offset:40960\n"
MF "v[160:163],a[200:203],a[72:75],v[160:163]\n"
MF "v[168:171],a[200:203],a[76:79],v[168:171]\n"
MF "v[168:171],a[204:207],a[116:119],v[168:171]\n"
LD "v11,s[28:31],s82 offen offset:1024 lds\n"
MF "v[172:175],a[212:215],a[116:119],v[172:175]\n"
TR "a[200:201],v0 offset:32776\n"
MF "v[172:175],a[208:211],a[76:79],v[172:175]\n"
TR "a[202:203],v0 offset:33288\n"
MF "v[164:167],a[208:211],a[72:75],v[164:167]\n"
"ds_read_b128 a[144:147],v2 offset:43008\n"
MF "v[164:167],a[212:215],a[112:115],v[164:167]\n"
TR "a[204:205],v0 offset:36872\n"
MF "v[156:159],a[212:215],a[108:111],v[156:159]\n"
TR "a[206:207],v0 offset:37384\n"
MF "v[156:159],a[208:211],a[68:71],v[156:159]\n"
LD "v12,s[28:31],s82 offen offset:2048 lds\n"
MF "v[148:151],a[208:211],a[64:67],v[148:151]\n"
"ds_read_b128 a[184:187],v3 offset:43008\n"
MF "v[148:151],a[212:215],a[104:107],v[148:151]\n"
MF "v[140:143],a[212:215],a[100:103],v[140:143]\n"
MF "v[140:143],a[208:211],a[60:63],v[140:143]\n"
"s_waitcnt lgkmcnt(7)\n"
LD "v13,s[28:31],s82 offen offset:3072 lds\n"
MF "v[176:179],a[216:219],a[60:63],v[176:179]\n"
TR "a[208:209],v1 offset:32768\n"
MF "v[176:179],a[220:223],a[100:103],v[176:179]\n"
TR "a[210:211],v1 offset:33280\n"
MF "v[184:187],a[220:223],a[104:107],v[184:187]\n"
"ds_read_b128 a[148:151],v2 offset:45056\n"
MF "v[184:187],a[216:219],a[64:67],v[184:187]\n"
"s_add_u32 s84,s14,20480\n"
TR "a[212:213],v1 offset:36864\n"
MF "v[192:195],a[216:219],a[68:71],v[192:195]\n"
"s_add_u32 m0,s63,22528\n"
TR "a[214:215],v1 offset:37376\n"
MF "v[192:195],a[220:223],a[108:111],v[192:195]\n"
LD "v4,s[24:27],s84 offen lds\n"
MF "v[200:203],a[220:223],a[112:115],v[200:203]\n"
"ds_read_b128 a[188:191],v3 offset:45056\n"
MF "v[200:203],a[216:219],a[72:75],v[200:203]\n"
MF "v[208:211],a[216:219],a[76:79],v[208:211]\n"
"s_add_u32 s84,s14,29696\n"
MF "v[208:211],a[220:223],a[116:119],v[208:211]\n"
"s_waitcnt lgkmcnt(7)\n"
LD "v5,s[24:27],s84 offen offset:1024 lds\n"
MF "v[212:215],a[204:207],a[116:119],v[212:215]\n"
TR "a[216:217],v1 offset:32776\n"
MF "v[212:215],a[200:203],a[76:79],v[212:215]\n"
TR "a[218:219],v1 offset:33288\n"
MF "v[204:207],a[200:203],a[72:75],v[204:207]\n"
"ds_read_b128 a[152:155],v2 offset:47104\n"
MF "v[204:207],a[204:207],a[112:115],v[204:207]\n"
TR "a[220:221],v1 offset:36872\n"
MF "v[196:199],a[204:207],a[108:111],v[196:199]\n"
TR "a[222:223],v1 offset:37384\n"
MF "v[196:199],a[200:203],a[68:71],v[196:199]\n"
MF "v[188:191],a[200:203],a[64:67],v[188:191]\n"
"v_add_u32 v0,0xc000,v0\n"
MF "v[188:191],a[204:207],a[104:107],v[188:191]\n"
"v_add_u32 v1,0xc000,v1\n"
MF "v[180:183],a[204:207],a[100:103],v[180:183]\n"
"s_waitcnt vmcnt(26) lgkmcnt(0)\n"
MF "v[180:183],a[200:203],a[60:63],v[180:183]\n"
"s_barrier\n"
MF "v[216:219],a[208:211],a[60:63],v[216:219]\n"
TR "a[200:201],v0\n"
MF "v[216:219],a[212:215],a[100:103],v[216:219]\n"
"s_add_u32 m0,s62,32768\n"
TR "a[202:203],v0 offset:512\n"
MF "v[224:227],a[212:215],a[104:107],v[224:227]\n"
"s_add_u32 s82,s66,4718848\n"
"ds_read_b128 a[192:195],v3 offset:47104\n"
MF "v[224:227],a[208:211],a[64:67],v[224:227]\n"
"s_sub_u32 s15,s82,7864320\n"
TR "a[204:205],v0 offset:4096\n"
MF "v[232:235],a[208:211],a[68:71],v[232:235]\n"
"s_min_u32 s82,s82,s15\n"
TR "a[206:207],v0 offset:4608\n"
MF "v[232:235],a[212:215],a[108:111],v[232:235]\n"
LD "v6,s[28:31],s82 offen lds\n"
MF "v[240:243],a[212:215],a[112:115],v[240:243]\n"
"ds_read_b128 a[156:159],v2 offset:49152\n"
MF "v[240:243],a[208:211],a[72:75],v[240:243]\n"
"ds_read_b128 a[196:199],v3 offset:49152\n"
MF "v[248:251],a[208:211],a[76:79],v[248:251]\n"
MF "v[248:251],a[212:215],a[116:119],v[248:251]\n"
LD "v11,s[28:31],s82 offen offset:1024 lds\n"
MF "v[252:255],a[220:223],a[116:119],v[252:255]\n"
TR "a[208:209],v0 offset:8\n"
MF "v[252:255],a[216:219],a[76:79],v[252:255]\n"
TR "a[210:211],v0 offset:520\n"
MF "v[244:247],a[216:219],a[72:75],v[244:247]\n"
MF "v[244:247],a[220:223],a[112:115],v[244:247]\n"
TR "a[212:213],v0 offset:4104\n"
MF "v[236:239],a[220:223],a[108:111],v[236:239]\n"
TR "a[214:215],v0 offset:4616\n"
MF "v[236:239],a[216:219],a[68:71],v[236:239]\n"
LD "v12,s[28:31],s82 offen offset:2048 lds\n"
MF "v[228:231],a[216:219],a[64:67],v[228:231]\n"
MF "v[228:231],a[220:223],a[104:107],v[228:231]\n"
MF "v[220:223],a[220:223],a[100:103],v[220:223]\n"
MF "v[220:223],a[216:219],a[60:63],v[220:223]\n"
"s_waitcnt lgkmcnt(4)\n"
LD "v13,s[28:31],s82 offen offset:3072 lds\n"
MF "v[16:19],a[200:203],a[140:143],v[16:19]\n"
TR "a[216:217],v1\n"
MF "v[16:19],a[204:207],a[180:183],v[16:19]\n"
TR "a[218:219],v1 offset:512\n"
MF "v[24:27],a[204:207],a[184:187],v[24:27]\n"
MF "v[24:27],a[200:203],a[144:147],v[24:27]\n"
"s_add_u32 s84,s14,40960\n"
TR "a[220:221],v1 offset:4096\n"
MF "v[32:35],a[200:203],a[148:151],v[32:35]\n"
"s_add_u32 m0,s63,24576\n"
TR "a[222:223],v1 offset:4608\n"
MF "v[32:35],a[204:207],a[188:191],v[32:35]\n"
LD "v4,s[24:27],s84 offen lds\n"
MF "v[40:43],a[204:207],a[192:195],v[40:43]\n"
MF "v[40:43],a[200:203],a[152:155],v[40:43]\n"
MF "v[48:51],a[200:203],a[156:159],v[48:51]\n"
MF "v[48:51],a[204:207],a[196:199],v[48:51]\n"
"s_waitcnt lgkmcnt(4)\n"
MF "v[52:55],a[212:215],a[196:199],v[52:55]\n"
TR "a[200:201],v1 offset:8\n"
MF "v[52:55],a[208:211],a[156:159],v[52:55]\n"
TR "a[202:203],v1 offset:520\n"
MF "v[44:47],a[208:211],a[152:155],v[44:47]\n"
MF "v[44:47],a[212:215],a[192:195],v[44:47]\n"
TR "a[204:205],v1 offset:4104\n"
MF "v[36:39],a[212:215],a[188:191],v[36:39]\n"
TR "a[206:207],v1 offset:4616\n"
MF "v[36:39],a[208:211],a[148:151],v[36:39]\n"
MF "v[28:31],a[208:211],a[144:147],v[28:31]\n"
MF "v[28:31],a[212:215],a[184:187],v[28:31]\n"
MF "v[20:23],a[212:215],a[180:183],v[20:23]\n"
"s_waitcnt vmcnt(25) lgkmcnt(0)\n"
MF "v[20:23],a[208:211],a[140:143],v[20:23]\n"
"s_barrier\n"
MF "v[56:59],a[216:219],a[140:143],v[56:59]\n"
"s_add_u32 m0,s62,49152\n"
"s_add_u32 s82,s66,5505024\n"
TR "a[208:209],v0 offset:16384\n"
MF "v[56:59],a[220:223],a[180:183],v[56:59]\n"
"s_sub_u32 s15,s82,7864320\n"
"s_min_u32 s82,s82,s15\n"
TR "a[210:211],v0 offset:16896\n"
MF "v[64:67],a[220:223],a[184:187],v[64:67]\n"
LD "v6,s[28:31],s82 offen lds\n"
MF "v[64:67],a[216:219],a[144:147],v[64:67]\n"
TR "a[212:213],v0 offset:20480\n"
MF "v[72:75],a[216:219],a[148:151],v[72:75]\n"
TR "a[214:215],v0 offset:20992\n"
MF "v[72:75],a[220:223],a[188:191],v[72:75]\n"
"buffer_load_dword a249,v15,s[48:51],s59 offen\n"
MF "v[80:83],a[220:223],a[192:195],v[80:83]\n"
"s_bfe_u32 s59,s23,0x30003\n"
MF "v[80:83],a[216:219],a[152:155],v[80:83]\n"
LD "v11,s[28:31],s82 offen offset:1024 lds\n"
MF "v[88:91],a[216:219],a[156:159],v[88:91]\n"
"s_add_u32 s59,s59,5\n"
MF "v[88:91],a[220:223],a[196:199],v[88:91]\n"
"s_lshl_b32 s59,s59,7\n"
MF "v[92:95],a[204:207],a[196:199],v[92:95]\n"
"s_and_b32 s59,s59,768\n"
TR "a[216:217],v0 offset:16392\n"
MF "v[92:95],a[200:203],a[156:159],v[92:95]\n"
"s_add_u32 s59,s59,s60\n"
TR "a[218:219],v0 offset:16904\n"
MF "v[84:87],a[200:203],a[152:155],v[84:87]\n"
LD "v12,s[28:31],s82 offen offset:2048 lds\n"
MF "v[84:87],a[204:207],a[192:195],v[84:87]\n"
TR "a[220:221],v0 offset:20488\n"
MF "v[76:79],a[204:207],a[188:191],v[76:79]\n"
TR "a[222:223],v0 offset:21000\n"
MF "v[76:79],a[200:203],a[148:151],v[76:79]\n"
MF "v[68:71],a[200:203],a[144:147],v[68:71]\n"
MF "v[68:71],a[204:207],a[184:187],v[68:71]\n"
".Lpg3a:\n"
"s_cbranch_execz .Lisr1\n"
LD "v13,s[28:31],s82 offen offset:3072 lds\n"
MF "v[60:63],a[204:207],a[180:183],v[60:63]\n"
MF "v[60:63],a[200:203],a[140:143],v[60:63]\n"
"s_add_u32 s14,s81,1024\n"
"s_sub_u32 s15,s14,1280\n"
"s_waitcnt lgkmcnt(4)\n"
MF "v[96:99],a[208:211],a[140:143],v[96:99]\n"
"s_min_u32 s14,s14,s15\n"
"s_add_u32 s14,s14,s68\n"
TR "a[200:201],v1 offset:16384\n"
MF "v[96:99],a[212:215],a[180:183],v[96:99]\n"
"s_add_u32 s84,s14,0\n"
"s_add_u32 m0,s63,40960\n"
TR "a[202:203],v1 offset:16896\n"
MF "v[104:107],a[212:215],a[184:187],v[104:107]\n"
LD "v4,s[24:27],s84 offen lds\n"
MF "v[104:107],a[208:211],a[144:147],v[104:107]\n"
TR "a[204:205],v1 offset:20480\n"
MF "v[112:115],a[208:211],a[148:151],v[112:115]\n"
TR "a[206:207],v1 offset:20992\n"
MF "v[112:115],a[212:215],a[188:191],v[112:115]\n"
MF "v[120:123],a[212:215],a[192:195],v[120:123]\n"
"s_add_u32 s84,s14,9216\n"
MF "v[120:123],a[208:211],a[152:155],v[120:123]\n"
LD "v5,s[24:27],s84 offen offset:1024 lds\n"
MF "v[128:131],a[208:211],a[156:159],v[128:131]\n"
MF "v[128:131],a[212:215],a[196:199],v[128:131]\n"
"s_waitcnt lgkmcnt(4)\n"
MF "v[132:135],a[220:223],a[196:199],v[132:135]\n"
TR "a[208:209],v1 offset:16392\n"
MF "v[132:135],a[216:219],a[156:159],v[132:135]\n"
TR "a[210:211],v1 offset:16904\n"
MF "v[124:127],a[216:219],a[152:155],v[124:127]\n"
MF "v[124:127],a[220:223],a[192:195],v[124:127]\n"
TR "a[212:213],v1 offset:20488\n"
MF "v[116:119],a[220:223],a[188:191],v[116:119]\n"
TR "a[214:215],v1 offset:21000\n"
MF "v[116:119],a[216:219],a[148:151],v[116:119]\n"
MF "v[108:111],a[216:219],a[144:147],v[108:111]\n"
MF "v[108:111],a[220:223],a[184:187],v[108:111]\n"
MF "v[100:103],a[220:223],a[180:183],v[100:103]\n"
"s_waitcnt vmcnt(25) lgkmcnt(0)\n"
MF "v[100:103],a[216:219],a[140:143],v[100:103]\n"
"s_barrier\n"
MF "v[136:139],a[200:203],a[140:143],v[136:139]\n"
TR "a[216:217],v0 offset:32768\n"
MF "v[136:139],a[204:207],a[180:183],v[136:139]\n"
"s_add_u32 m0,s62,65536\n"
TR "a[218:219],v0 offset:33280\n"
MF "v[144:147],a[204:207],a[184:187],v[144:147]\n"
"s_add_u32 s82,s66,5505152\n"
"ds_read_b128 a[60:63],v2\n"
MF "v[144:147],a[200:203],a[144:147],v[144:147]\n"
"s_sub_u32 s15,s82,7864320\n"
TR "a[220:221],v0 offset:36864\n"
MF "v[152:155],a[200:203],a[148:151],v[152:155]\n"
"s_min_u32 s82,s82,s15\n"
TR "a[222:223],v0 offset:37376\n"
MF "v[152:155],a[204:207],a[188:191],v[152:155]\n"
LD "v6,s[28:31],s82 offen lds\n"
MF "v[160:163],a[204:207],a[192:195],v[160:163]\n"
"ds_read_b128 a[100:103],v3\n"
MF "v[160:163],a[200:203],a[152:155],v[160:163]\n"
MF "v[168:171],a[200:203],a[156:159],v[168:171]\n"
MF "v[168:171],a[204:207],a[196:199],v[168:171]\n"
LD "v11,s[28:31],s82 offen offset:1024 lds\n"
MF "v[172:175],a[212:215],a[196:199],v[172:175]\n"
TR "a[200:201],v0 offset:32776\n"
MF "v[172:175],a[208:211],a[156:159],v[172:175]\n"
TR "a[202:203],v0 offset:33288\n"
MF "v[164:167],a[208:211],a[152:155],v[164:167]\n"
"ds_read_b128 a[64:67],v2 offset:2048\n"
MF "v[164:167],a[212:215],a[192:195],v[164:167]\n"
TR "a[204:205],v0 offset:36872\n"
MF "v[156:159],a[212:215],a[188:191],v[156:159]\n"
TR "a[206:207],v0 offset:37384\n"
MF "v[156:159],a[208:211],a[148:151],v[156:159]\n"
LD "v12,s[28:31],s82 offen offset:2048 lds\n"
MF "v[148:151],a[208:211],a[144:147],v[148:151]\n"
"ds_read_b128 a[104:107],v3 offset:2048\n"
MF "v[148:151],a[212:215],a[184:187],v[148:151]\n"
MF "v[140:143],a[212:215],a[180:183],v[140:143]\n"
MF "v[140:143],a[208:211],a[140:143],v[140:143]\n"
"s_waitcnt lgkmcnt(7)\n"
LD "v13,s[28:31],s82 offen offset:3072 lds\n"
MF "v[176:179],a[216:219],a[140:143],v[176:179]\n"
TR "a[208:209],v1 offset:32768\n"
MF "v[176:179],a[220:223],a[180:183],v[176:179]\n"
TR "a[210:211],v1 offset:33280\n"
MF "v[184:187],a[220:223],a[184:187],v[184:187]\n"
"ds_read_b128 a[68:71],v2 offset:4096\n"
MF "v[184:187],a[216:219],a[144:147],v[184:187]\n"
"s_add_u32 s84,s14,20480\n"
TR "a[212:213],v1 offset:36864\n"
MF "v[192:195],a[216:219],a[148:151],v[192:195]\n"
"s_add_u32 m0,s63,43008\n"
TR "a[214:215],v1 offset:37376\n"
MF "v[192:195],a[220:223],a[188:191],v[192:195]\n"
LD "v4,s[24:27],s84 offen lds\n"
MF "v[200:203],a[220:223],a[192:195],v[200:203]\n"
"ds_read_b128 a[108:111],v3 offset:4096\n"
MF "v[200:203],a[216:219],a[152:155],v[200:203]\n"
MF "v[208:211],a[216:219],a[156:159],v[208:211]\n"
"s_add_u32 s84,s14,29696\n"
MF "v[208:211],a[220:223],a[196:199],v[208:211]\n"
"s_waitcnt lgkmcnt(7)\n"
LD "v5,s[24:27],s84 offen offset:1024 lds\n"
MF "v[212:215],a[204:207],a[196:199],v[212:215]\n"
TR "a[216:217],v1 offset:32776\n"
MF "v[212:215],a[200:203],a[156:159],v[212:215]\n"
TR "a[218:219],v1 offset:33288\n"
MF "v[204:207],a[200:203],a[152:155],v[204:207]\n"
"ds_read_b128 a[72:75],v2 offset:6144\n"
MF "v[204:207],a[204:207],a[192:195],v[204:207]\n"
TR "a[220:221],v1 offset:36872\n"
MF "v[196:199],a[204:207],a[188:191],v[196:199]\n"
TR "a[222:223],v1 offset:37384\n"
MF "v[196:199],a[200:203],a[148:151],v[196:199]\n"
MF "v[188:191],a[200:203],a[144:147],v[188:191]\n"
"v_add_u32 v0,0xffff4000,v0\n"
MF "v[188:191],a[204:207],a[184:187],v[188:191]\n"
"v_add_u32 v1,0xffff4000,v1\n"
MF "v[180:183],a[204:207],a[180:183],v[180:183]\n"
"s_waitcnt vmcnt(26) lgkmcnt(0)\n"
MF "v[180:183],a[200:203],a[140:143],v[180:183]\n"
"s_barrier\n"
MF "v[216:219],a[208:211],a[140:143],v[216:219]\n"
TR "a[200:201],v0\n"
MF "v[216:219],a[212:215],a[180:183],v[216:219]\n"
"s_add_u32 m0,s62,81920\n"
TR "a[202:203],v0 offset:512\n"
MF "v[224:227],a[212:215],a[184:187],v[224:227]\n"
"s_add_u32 s82,s66,5505280\n"
"ds_read_b128 a[112:115],v3 offset:6144\n"
MF "v[224:227],a[208:211],a[144:147],v[224:227]\n"
"s_sub_u32 s15,s82,7864320\n"
TR "a[204:205],v0 offset:4096\n"
MF "v[232:235],a[208:211],a[148:151],v[232:235]\n"
"s_min_u32 s82,s82,s15\n"
TR "a[206:207],v0 offset:4608\n"
MF "v[232:235],a[212:215],a[188:191],v[232:235]\n"
LD "v6,s[28:31],s82 offen lds\n"
MF "v[240:243],a[212:215],a[192:195],v[240:243]\n"
"ds_read_b128 a[76:79],v2 offset:8192\n"
MF "v[240:243],a[208:211],a[152:155],v[240:243]\n"
"ds_read_b128 a[116:119],v3 offset:8192\n"
MF "v[248:251],a[208:211],a[156:159],v[248:251]\n"
MF "v[248:251],a[212:215],a[196:199],v[248:251]\n"
LD "v11,s[28:31],s82 offen offset:1024 lds\n"
MF "v[252:255],a[220:223],a[196:199],v[252:255]\n"
TR "a[208:209],v0 offset:8\n"
MF "v[252:255],a[216:219],a[156:159],v[252:255]\n"
TR "a[210:211],v0 offset:520\n"
MF "v[244:247],a[216:219],a[152:155],v[244:247]\n"
MF "v[244:247],a[220:223],a[192:195],v[244:247]\n"
TR "a[212:213],v0 offset:4104\n"
MF "v[236:239],a[220:223],a[188:191],v[236:239]\n"
TR "a[214:215],v0 offset:4616\n"
MF "v[236:239],a[216:219],a[148:151],v[236:239]\n"
LD "v12,s[28:31],s82 offen offset:2048 lds\n"
MF "v[228:231],a[216:219],a[144:147],v[228:231]\n"
MF "v[228:231],a[220:223],a[184:187],v[228:231]\n"
MF "v[220:223],a[220:223],a[180:183],v[220:223]\n"
MF "v[220:223],a[216:219],a[140:143],v[220:223]\n"
"s_waitcnt lgkmcnt(4)\n"
LD "v13,s[28:31],s82 offen offset:3072 lds\n"
MF "v[16:19],a[200:203],a[60:63],v[16:19]\n"
TR "a[216:217],v1\n"
MF "v[16:19],a[204:207],a[100:103],v[16:19]\n"
TR "a[218:219],v1 offset:512\n"
MF "v[24:27],a[204:207],a[104:107],v[24:27]\n"
MF "v[24:27],a[200:203],a[64:67],v[24:27]\n"
"s_add_u32 s84,s14,40960\n"
TR "a[220:221],v1 offset:4096\n"
MF "v[32:35],a[200:203],a[68:71],v[32:35]\n"
"s_add_u32 m0,s63,45056\n"
TR "a[222:223],v1 offset:4608\n"
MF "v[32:35],a[204:207],a[108:111],v[32:35]\n"
LD "v4,s[24:27],s84 offen lds\n"
MF "v[40:43],a[204:207],a[112:115],v[40:43]\n"
MF "v[40:43],a[200:203],a[72:75],v[40:43]\n"
MF "v[48:51],a[200:203],a[76:79],v[48:51]\n"
MF "v[48:51],a[204:207],a[116:119],v[48:51]\n"
"s_waitcnt lgkmcnt(4)\n"
MF "v[52:55],a[212:215],a[116:119],v[52:55]\n"
TR "a[200:201],v1 offset:8\n"
MF "v[52:55],a[208:211],a[76:79],v[52:55]\n"
TR "a[202:203],v1 offset:520\n"
MF "v[44:47],a[208:211],a[72:75],v[44:47]\n"
MF "v[44:47],a[212:215],a[112:115],v[44:47]\n"
TR "a[204:205],v1 offset:4104\n"
MF "v[36:39],a[212:215],a[108:111],v[36:39]\n"
TR "a[206:207],v1 offset:4616\n"
MF "v[36:39],a[208:211],a[68:71],v[36:39]\n"
MF "v[28:31],a[208:211],a[64:67],v[28:31]\n"
MF "v[28:31],a[212:215],a[104:107],v[28:31]\n"
MF "v[20:23],a[212:215],a[100:103],v[20:23]\n"
"s_waitcnt vmcnt(25) lgkmcnt(0)\n"
MF "v[20:23],a[208:211],a[60:63],v[20:23]\n"
"s_barrier\n"
MF "v[56:59],a[216:219],a[60:63],v[56:59]\n"
"s_add_u32 m0,s62,0\n"
"s_add_u32 s82,s66,6291712\n"
TR "a[208:209],v0 offset:16384\n"
MF "v[56:59],a[220:223],a[100:103],v[56:59]\n"
"s_sub_u32 s15,s82,7864320\n"
"s_min_u32 s82,s82,s15\n"
TR "a[210:211],v0 offset:16896\n"
MF "v[64:67],a[220:223],a[104:107],v[64:67]\n"
LD "v6,s[28:31],s82 offen lds\n"
MF "v[64:67],a[216:219],a[64:67],v[64:67]\n"
TR "a[212:213],v0 offset:20480\n"
MF "v[72:75],a[216:219],a[68:71],v[72:75]\n"
TR "a[214:215],v0 offset:20992\n"
MF "v[72:75],a[220:223],a[108:111],v[72:75]\n"
"buffer_load_dword a249,v15,s[48:51],s59 offen\n"
MF "v[80:83],a[220:223],a[112:115],v[80:83]\n"
MF "v[80:83],a[216:219],a[72:75],v[80:83]\n"
LD "v11,s[28:31],s82 offen offset:1024 lds\n"
MF "v[88:91],a[216:219],a[76:79],v[88:91]\n"
MF "v[88:91],a[220:223],a[116:119],v[88:91]\n"
MF "v[92:95],a[204:207],a[116:119],v[92:95]\n"
TR "a[216:217],v0 offset:16392\n"
MF "v[92:95],a[200:203],a[76:79],v[92:95]\n"
TR "a[218:219],v0 offset:16904\n"
MF "v[84:87],a[200:203],a[72:75],v[84:87]\n"
LD "v12,s[28:31],s82 offen offset:2048 lds\n"
MF "v[84:87],a[204:207],a[112:115],v[84:87]\n"
TR "a[220:221],v0 offset:20488\n"
MF "v[76:79],a[204:207],a[108:111],v[76:79]\n"
TR "a[222:223],v0 offset:21000\n"
MF "v[76:79],a[200:203],a[68:71],v[76:79]\n"
MF "v[68:71],a[200:203],a[64:67],v[68:71]\n"
MF "v[68:71],a[204:207],a[104:107],v[68:71]\n"
LD "v13,s[28:31],s82 offen offset:3072 lds\n"
MF "v[60:63],a[204:207],a[100:103],v[60:63]\n"
MF "v[60:63],a[200:203],a[60:63],v[60:63]\n"
"s_add_u32 s14,s81,1152\n"
"s_sub_u32 s15,s14,1280\n"
"s_waitcnt lgkmcnt(4)\n"
MF "v[96:99],a[208:211],a[60:63],v[96:99]\n"
"s_min_u32 s14,s14,s15\n"
"s_add_u32 s14,s14,s68\n"
TR "a[200:201],v1 offset:16384\n"
MF "v[96:99],a[212:215],a[100:103],v[96:99]\n"
"s_add_u32 s84,s14,0\n"
"s_add_u32 m0,s63,0\n"
TR "a[202:203],v1 offset:16896\n"
MF "v[104:107],a[212:215],a[104:107],v[104:107]\n"
LD "v4,s[24:27],s84 offen lds\n"
MF "v[104:107],a[208:211],a[64:67],v[104:107]\n"
TR "a[204:205],v1 offset:20480\n"
MF "v[112:115],a[208:211],a[68:71],v[112:115]\n"
TR "a[206:207],v1 offset:20992\n"
MF "v[112:115],a[212:215],a[108:111],v[112:115]\n"
MF "v[120:123],a[212:215],a[112:115],v[120:123]\n"
"s_add_u32 s84,s14,9216\n"
MF "v[120:123],a[208:211],a[72:75],v[120:123]\n"
LD "v5,s[24:27],s84 offen offset:1024 lds\n"
MF "v[128:131],a[208:211],a[76:79],v[128:131]\n"
MF "v[128:131],a[212:215],a[116:119],v[128:131]\n"
"s_waitcnt lgkmcnt(4)\n"
MF "v[132:135],a[220:223],a[116:119],v[132:135]\n"
TR "a[208:209],v1 offset:16392\n"
MF "v[132:135],a[216:219],a[76:79],v[132:135]\n"
"s_add_u32 s84,s14,18432\n"
TR "a[210:211],v1 offset:16904\n"
MF "v[124:127],a[216:219],a[72:75],v[124:127]\n"
LD "v4,s[24:27],s84 offen offset:2048 lds\n"
MF "v[124:127],a[220:223],a[112:115],v[124:127]\n"
TR "a[212:213],v1 offset:20488\n"
MF "v[116:119],a[220:223],a[108:111],v[116:119]\n"
TR "a[214:215],v1 offset:21000\n"
MF "v[116:119],a[216:219],a[68:71],v[116:119]\n"
MF "v[108:111],a[216:219],a[64:67],v[108:111]\n"
MF "v[108:111],a[220:223],a[104:107],v[108:111]\n"
MF "v[100:103],a[220:223],a[100:103],v[100:103]\n"
"s_waitcnt vmcnt(26) lgkmcnt(0)\n"
MF "v[100:103],a[216:219],a[60:63],v[100:103]\n"
"s_barrier\n"
MF "v[136:139],a[200:203],a[60:63],v[136:139]\n"
TR "a[216:217],v0 offset:32768\n"
MF "v[136:139],a[204:207],a[100:103],v[136:139]\n"
"s_add_u32 m0,s62,16384\n"
TR "a[218:219],v0 offset:33280\n"
MF "v[144:147],a[204:207],a[104:107],v[144:147]\n"
"s_add_u32 s82,s66,7078144\n"
"ds_read_b128 a[140:143],v2 offset:20480\n"
MF "v[144:147],a[200:203],a[64:67],v[144:147]\n"
"s_sub_u32 s15,s82,7864320\n"
TR "a[220:221],v0 offset:36864\n"
MF "v[152:155],a[200:203],a[68:71],v[152:155]\n"
"s_min_u32 s82,s82,s15\n"
TR "a[222:223],v0 offset:37376\n"
MF "v[152:155],a[204:207],a[108:111],v[152:155]\n"
LD "v6,s[28:31],s82 offen lds\n"
MF "v[160:163],a[204:207],a[112:115],v[160:163]\n"
"ds_read_b128 a[180:183],v3 offset:20480\n"
MF "v[160:163],a[200:203],a[72:75],v[160:163]\n"
MF "v[168:171],a[200:203],a[76:79],v[168:171]\n"
MF "v[168:171],a[204:207],a[116:119],v[168:171]\n"
LD "v11,s[28:31],s82 offen offset:1024 lds\n"
MF "v[172:175],a[212:215],a[116:119],v[172:175]\n"
TR "a[200:201],v0 offset:32776\n"
MF "v[172:175],a[208:211],a[76:79],v[172:175]\n"
TR "a[202:203],v0 offset:33288\n"
MF "v[164:167],a[208:211],a[72:75],v[164:167]\n"
"ds_read_b128 a[144:147],v2 offset:22528\n"
MF "v[164:167],a[212:215],a[112:115],v[164:167]\n"
TR "a[204:205],v0 offset:36872\n"
MF "v[156:159],a[212:215],a[108:111],v[156:159]\n"
TR "a[206:207],v0 offset:37384\n"
MF "v[156:159],a[208:211],a[68:71],v[156:159]\n"
LD "v12,s[28:31],s82 offen offset:2048 lds\n"
MF "v[148:151],a[208:211],a[64:67],v[148:151]\n"
"ds_read_b128 a[184:187],v3 offset:22528\n"
MF "v[148:151],a[212:215],a[104:107],v[148:151]\n"
MF "v[140:143],a[212:215],a[100:103],v[140:143]\n"
MF "v[140:143],a[208:211],a[60:63],v[140:143]\n"
"s_waitcnt lgkmcnt(7)\n"
LD "v13,s[28:31],s82 offen offset:3072 lds\n"
MF "v[176:179],a[216:219],a[60:63],v[176:179]\n"
TR "a[208:209],v1 offset:32768\n"
MF "v[176:179],a[220:223],a[100:103],v[176:179]\n"
TR "a[210:211],v1 offset:33280\n"
MF "v[184:187],a[220:223],a[104:107],v[184:187]\n"
"ds_read_b128 a[148:151],v2 offset:24576\n"
MF "v[184:187],a[216:219],a[64:67],v[184:187]\n"
"s_add_u32 s84,s14,30720\n"
TR "a[212:213],v1 offset:36864\n"
MF "v[192:195],a[216:219],a[68:71],v[192:195]\n"
"s_add_u32 m0,s63,3072\n"
TR "a[214:215],v1 offset:37376\n"
MF "v[192:195],a[220:223],a[108:111],v[192:195]\n"
LD "v5,s[24:27],s84 offen lds\n"
MF "v[200:203],a[220:223],a[112:115],v[200:203]\n"
"ds_read_b128 a[188:191],v3 offset:24576\n"
MF "v[200:203],a[216:219],a[72:75],v[200:203]\n"
MF "v[208:211],a[216:219],a[76:79],v[208:211]\n"
"s_add_u32 s84,s14,39936\n"
MF "v[208:211],a[220:223],a[116:119],v[208:211]\n"
"s_waitcnt lgkmcnt(7)\n"
LD "v4,s[24:27],s84 offen offset:1024 lds\n"
MF "v[212:215],a[204:207],a[116:119],v[212:215]\n"
TR "a[216:217],v1 offset:32776\n"
MF "v[212:215],a[200:203],a[76:79],v[212:215]\n"
TR "a[218:219],v1 offset:33288\n"
MF "v[204:207],a[200:203],a[72:75],v[204:207]\n"
"ds_read_b128 a[152:155],v2 offset:26624\n"
MF "v[204:207],a[204:207],a[112:115],v[204:207]\n"
TR "a[220:221],v1 offset:36872\n"
MF "v[196:199],a[204:207],a[108:111],v[196:199]\n"
TR "a[222:223],v1 offset:37384\n"
MF "v[196:199],a[200:203],a[68:71],v[196:199]\n"
MF "v[188:191],a[200:203],a[64:67],v[188:191]\n"
"v_add_u32 v0,0xc000,v0\n"
MF "v[188:191],a[204:207],a[104:107],v[188:191]\n"
"v_add_u32 v1,0xc000,v1\n"
MF "v[180:183],a[204:207],a[100:103],v[180:183]\n"
"s_waitcnt vmcnt(27) lgkmcnt(0)\n"
"s_branch .Lqj\n"
".Lpgq0_c:\n"
"s_cbranch_execz .Lpgqy_c\n"
".Lpgq0_f:\n"
"s_cbranch_execz .Lpg12f\n"
".Lq0:\n"
MF "v[16:19],a[200:203],a[60:63],0\n"
"s_add_u32 m0,s62,49152\n"
"s_add_u32 s82,s66,786432\n"
TR "a[216:217],v1\n"
MF "v[16:19],a[204:207],a[100:103],v[16:19]\n"
"s_sub_u32 s15,s82,7864320\n"
"s_min_u32 s82,s82,s15\n"
TR "a[218:219],v1 offset:512\n"
MF "v[24:27],a[204:207],a[104:107],0\n"
LD "v6,s[28:31],s82 offen lds\n"
MF "v[24:27],a[200:203],a[64:67],v[24:27]\n"
TR "a[220:221],v1 offset:4096\n"
MF "v[32:35],a[200:203],a[68:71],0\n"
TR "a[222:223],v1 offset:4608\n"
MF "v[32:35],a[204:207],a[108:111],v[32:35]\n"
"v_max3_f32 v220,|v16|,|v17|,|v18|\n"
"v_max_f32_e64 v220,v220,|v19|\n"
MF "v[40:43],a[204:207],a[112:115],0\n"
"v_readlane_b32 s12,v220,3\n"
"v_readlane_b32 s6,v220,22\n"
MF "v[40:43],a[200:203],a[72:75],v[40:43]\n"
"s_min_u32 s12,s12,s6\n"
"v_readlane_b32 s7,v220,41\n"
"v_readlane_b32 s8,v220,60\n"
MF "v[48:51],a[200:203],a[76:79],0\n"
"s_min_u32 s7,s7,s8\n"
MF "v[48:51],a[204:207],a[116:119],v[48:51]\n"
LD "v11,s[28:31],s82 offen offset:1024 lds\n"
MF "v[52:55],a[212:215],a[116:119],0\n"
"s_min_u32 s12,s12,s7\n"
TR "a[200:201],v1 offset:8\n"
MF "v[52:55],a[208:211],a[76:79],v[52:55]\n"
"s_and_b32 s12,s12,0x7f800000\n"
TR "a[202:203],v1 offset:520\n"
MF "v[44:47],a[208:211],a[72:75],0\n"
"s_sub_u32 s12,s12,0x14000000\n"
MF "v[44:47],a[212:215],a[112:115],v[44:47]\n"
"s_cmp_lt_u32 s12,0x50000000\n"
"s_cselect_b32 s12,s12,0xe8400000\n"
TR "a[204:205],v1 offset:4104\n"
MF "v[36:39],a[212:215],a[108:111],0\n"
"s_add_u32 s12,s12,0x17c00000\n"
TR "a[206:207],v1 offset:4616\n"
MF "v[36:39],a[208:211],a[68:71],v[36:39]\n"
"v_mov_b32 v220,s12\n"
"v_mov_b32 v221,s12\n"
MF "v[28:31],a[208:211],a[64:67],0\n"
"v_mov_b32 v222,s12\n"
"v_mov_b32 v223,s12\n"
MF "v[28:31],a[212:215],a[104:107],v[28:31]\n"
LD "v12,s[28:31],s82 offen offset:2048 lds\n"
MF "v[20:23],a[212:215],a[100:103],0\n"
"s_waitcnt vmcnt(7) lgkmcnt(0)\n"
MF "v[20:23],a[208:211],a[60:63],v[20:23]\n"
"s_barrier\n"
MF "v[56:59],a[216:219],a[60:63],v[220:223]\n"
TR "a[208:209],v0 offset:16384\n"
MF "v[56:59],a[220:223],a[100:103],v[56:59]\n"
TR "a[210:211],v0 offset:16896\n"
MF "v[64:67],a[220:223],a[104:107],v[220:223]\n"
LD "v13,s[28:31],s82 offen offset:3072 lds\n"
MF "v[64:67],a[216:219],a[64:67],v[64:67]\n"
"s_add_u32 m0,s62,65536\n"
TR "a[212:213],v0 offset:20480\n"
MF "v[72:75],a[216:219],a[68:71],v[220:223]\n"
"s_add_u32 s82,s66,786560\n"
TR "a[214:215],v0 offset:20992\n"
MF "v[72:75],a[220:223],a[108:111],v[72:75]\n"
"s_sub_u32 s15,s82,7864320\n"
MF "v[80:83],a[220:223],a[112:115],v[220:223]\n"
"s_min_u32 s82,s82,s15\n"
MF "v[80:83],a[216:219],a[72:75],v[80:83]\n"
LD "v6,s[28:31],s82 offen lds\n"
MF "v[88:91],a[216:219],a[76:79],v[220:223]\n"
MF "v[88:91],a[220:223],a[116:119],v[88:91]\n"
MF "v[92:95],a[204:207],a[116:119],v[220:223]\n"
TR "a[216:217],v0 offset:16392\n"
MF "v[92:95],a[200:203],a[76:79],v[92:95]\n"
TR "a[218:219],v0 offset:16904\n"
MF "v[84:87],a[200:203],a[72:75],v[220:223]\n"
LD "v11,s[28:31],s82 offen offset:1024 lds\n"
MF "v[84:87],a[204:207],a[112:115],v[84:87]\n"
TR "a[220:221],v0 offset:20488\n"
MF "v[76:79],a[204:207],a[108:111],v[220:223]\n"
TR "a[222:223],v0 offset:21000\n"
MF "v[76:79],a[200:203],a[68:71],v[76:79]\n"
MF "v[68:71],a[200:203],a[64:67],v[220:223]\n"
MF "v[68:71],a[204:207],a[104:107],v[68:71]\n"
LD "v12,s[28:31],s82 offen offset:2048 lds\n"
MF "v[60:63],a[204:207],a[100:103],v[220:223]\n"
MF "v[60:63],a[200:203],a[60:63],v[60:63]\n"
"s_waitcnt lgkmcnt(4)\n"
MF "v[96:99],a[208:211],a[60:63],v[220:223]\n"
TR "a[200:201],v1 offset:16384\n"
MF "v[96:99],a[212:215],a[100:103],v[96:99]\n"
TR "a[202:203],v1 offset:16896\n"
MF "v[104:107],a[212:215],a[104:107],v[220:223]\n"
LD "v13,s[28:31],s82 offen offset:3072 lds\n"
MF "v[104:107],a[208:211],a[64:67],v[104:107]\n"
"s_add_u32 m0,s62,81920\n"
TR "a[204:205],v1 offset:20480\n"
MF "v[112:115],a[208:211],a[68:71],v[220:223]\n"
"s_add_u32 s82,s66,786688\n"
TR "a[206:207],v1 offset:20992\n"
MF "v[112:115],a[212:215],a[108:111],v[112:115]\n"
"s_sub_u32 s15,s82,7864320\n"
MF "v[120:123],a[212:215],a[112:115],v[220:223]\n"
"s_min_u32 s82,s82,s15\n"
MF "v[120:123],a[208:211],a[72:75],v[120:123]\n"
LD "v6,s[28:31],s82 offen lds\n"
MF "v[128:131],a[208:211],a[76:79],v[220:223]\n"
MF "v[128:131],a[212:215],a[116:119],v[128:131]\n"
"s_waitcnt lgkmcnt(4)\n"
MF "v[132:135],a[220:223],a[116:119],v[220:223]\n"
TR "a[208:209],v1 offset:16392\n"
MF "v[132:135],a[216:219],a[76:79],v[132:135]\n"
TR "a[210:211],v1 offset:16904\n"
MF "v[124:127],a[216:219],a[72:75],v[220:223]\n"
LD "v11,s[28:31],s82 offen offset:1024 lds\n"
MF "v[124:127],a[220:223],a[112:115],v[124:127]\n"
TR "a[212:213],v1 offset:20488\n"
MF "v[116:119],a[220:223],a[108:111],v[220:223]\n"
TR "a[214:215],v1 offset:21000\n"
MF "v[116:119],a[216:219],a[68:71],v[116:119]\n"
MF "v[108:111],a[216:219],a[64:67],v[220:223]\n"
MF "v[108:111],a[220:223],a[104:107],v[108:111]\n"
LD "v12,s[28:31],s82 offen offset:2048 lds\n"
MF "v[100:103],a[220:223],a[100:103],v[220:223]\n"
"s_waitcnt vmcnt(11) lgkmcnt(0)\n"
MF "v[100:103],a[216:219],a[60:63],v[100:103]\n"
"s_barrier\n"
MF "v[136:139],a[200:203],a[60:63],v[220:223]\n"
TR "a[216:217],v0 offset:32768\n"
MF "v[136:139],a[204:207],a[100:103],v[136:139]\n"
TR "a[218:219],v0 offset:33280\n"
MF "v[144:147],a[204:207],a[104:107],v[220:223]\n"
"ds_read_b128 a[140:143],v2 offset:20480\n"
MF "v[144:147],a[200:203],a[64:67],v[144:147]\n"
TR "a[220:221],v0 offset:36864\n"
MF "v[152:155],a[200:203],a[68:71],v[220:223]\n"
TR "a[222:223],v0 offset:37376\n"
MF "v[152:155],a[204:207],a[108:111],v[152:155]\n"
LD "v13,s[28:31],s82 offen offset:3072 lds\n"
MF "v[160:163],a[204:207],a[112:115],v[220:223]\n"
"ds_read_b128 a[180:183],v3 offset:20480\n"
MF "v[160:163],a[200:203],a[72:75],v[160:163]\n"
"s_add_u32 m0,s62,0\n"
MF "v[168:171],a[200:203],a[76:79],v[220:223]\n"
"s_add_u32 s82,s66,1572864\n"
"s_sub_u32 s15,s82,7864320\n"
"s_min_u32 s82,s82,s15\n"
MF "v[168:171],a[204:207],a[116:119],v[168:171]\n"
LD "v6,s[28:31],s82 offen lds\n"
MF "v[172:175],a[212:215],a[116:119],v[220:223]\n"
TR "a[200:201],v0 offset:32776\n"
MF "v[172:175],a[208:211],a[76:79],v[172:175]\n"
TR "a[202:203],v0 offset:33288\n"
MF "v[164:167],a[208:211],a[72:75],v[220:223]\n"
"ds_read_b128 a[144:147],v2 offset:22528\n"
MF "v[164:167],a[212:215],a[112:115],v[164:167]\n"
TR "a[204:205],v0 offset:36872\n"
MF "v[156:159],a[212:215],a[108:111],v[220:223]\n"
TR "a[206:207],v0 offset:37384\n"
MF "v[156:159],a[208:211],a[68:71],v[156:159]\n"
LD "v11,s[28:31],s82 offen offset:1024 lds\n"
MF "v[148:151],a[208:211],a[64:67],v[220:223]\n"
"ds_read_b128 a[184:187],v3 offset:22528\n"
MF "v[148:151],a[212:215],a[104:107],v[148:151]\n"
MF "v[140:143],a[212:215],a[100:103],v[220:223]\n"
MF "v[140:143],a[208:211],a[60:63],v[140:143]\n"
"s_waitcnt lgkmcnt(7)\n"
LD "v12,s[28:31],s82 offen offset:2048 lds\n"
MF "v[176:179],a[216:219],a[60:63],v[220:223]\n"
TR "a[208:209],v1 offset:32768\n"
MF "v[176:179],a[220:223],a[100:103],v[176:179]\n"
".Lpg4c:\n"
"s_cbranch_execz .Lpg14c\n"
".Lpg4f:\n"
"s_cbranch_execz .Lpg13f\n"
TR "a[210:211],v1 offset:33280\n"
MF "v[184:187],a[220:223],a[104:107],v[220:223]\n"
"ds_read_b128 a[148:151],v2 offset:24576\n"
MF "v[184:187],a[216:219],a[64:67],v[184:187]\n"
TR "a[212:213],v1 offset:36864\n"
MF "v[192:195],a[216:219],a[68:71],v[220:223]\n"
TR "a[214:215],v1 offset:37376\n"
MF "v[192:195],a[220:223],a[108:111],v[192:195]\n"
LD "v13,s[28:31],s82 offen offset:3072 lds\n"
MF "v[200:203],a[220:223],a[112:115],v[220:223]\n"
"ds_read_b128 a[188:191],v3 offset:24576\n"
MF "v[200:203],a[216:219],a[72:75],v[200:203]\n"
"s_add_u32 s14,s81,384\n"
"s_sub_u32 s15,s14,1280\n"
"s_min_u32 s14,s14,s15\n"
MF "v[208:211],a[216:219],a[76:79],v[220:223]\n"
"s_add_u32 s14,s14,s68\n"
"s_add_u32 s84,s14,0\n"
"s_add_u32 m0,s63,0\n"
MF "v[208:211],a[220:223],a[116:119],v[208:211]\n"
"s_waitcnt lgkmcnt(7)\n"
LD "v4,s[24:27],s84 offen lds\n"
MF "v[212:215],a[204:207],a[116:119],v[220:223]\n"
TR "a[216:217],v1 offset:32776\n"
MF "v[212:215],a[200:203],a[76:79],v[212:215]\n"
TR "a[218:219],v1 offset:33288\n"
MF "v[204:207],a[200:203],a[72:75],v[220:223]\n"
"ds_read_b128 a[152:155],v2 offset:26624\n"
MF "v[204:207],a[204:207],a[112:115],v[204:207]\n"
TR "a[220:221],v1 offset:36872\n"
MF "v[196:199],a[204:207],a[108:111],v[220:223]\n"
"s_add_u32 s84,s14,9216\n"
TR "a[222:223],v1 offset:37384\n"
MF "v[196:199],a[200:203],a[68:71],v[196:199]\n"
LD "v5,s[24:27],s84 offen offset:1024 lds\n"
MF "v[188:191],a[200:203],a[64:67],v[220:223]\n"
"s_add_u32 m0,s62,16384\n"
"s_add_u32 s82,s66,1572992\n"
"v_add_u32 v0,0xc000,v0\n"
MF "v[188:191],a[204:207],a[104:107],v[188:191]\n"
"s_sub_u32 s15,s82,7864320\n"
"s_min_u32 s82,s82,s15\n"
"v_add_u32 v1,0xc000,v1\n"
MF "v[180:183],a[204:207],a[100:103],v[220:223]\n"
"s_waitcnt vmcnt(14) lgkmcnt(0)\n"
LD "v6,s[28:31],s82 offen lds\n"
MF "v[180:183],a[200:203],a[60:63],v[180:183]\n"
"s_barrier\n"
MF "v[216:219],a[208:211],a[60:63],v[220:223]\n"
TR "a[200:201],v0\n"
MF "v[216:219],a[212:215],a[100:103],v[216:219]\n"
TR "a[202:203],v0 offset:512\n"
MF "v[224:227],a[212:215],a[104:107],v[220:223]\n"
"ds_read_b128 a[192:195],v3 offset:26624\n"
MF "v[224:227],a[208:211],a[64:67],v[224:227]\n"
TR "a[204:205],v0 offset:4096\n"
MF "v[232:235],a[208:211],a[68:71],v[220:223]\n"
TR "a[206:207],v0 offset:4608\n"
MF "v[232:235],a[212:215],a[108:111],v[232:235]\n"
LD "v11,s[28:31],s82 offen offset:1024 lds\n"
MF "v[240:243],a[212:215],a[112:115],v[220:223]\n"
"ds_read_b128 a[156:159],v2 offset:28672\n"
MF "v[240:243],a[208:211],a[72:75],v[240:243]\n"
"ds_read_b128 a[196:199],v3 offset:28672\n"
MF "v[248:251],a[208:211],a[76:79],v[220:223]\n"
MF "v[248:251],a[212:215],a[116:119],v[248:251]\n"
LD "v12,s[28:31],s82 offen offset:2048 lds\n"
MF "v[252:255],a[220:223],a[116:119],v[220:223]\n"
TR "a[208:209],v0 offset:8\n"
MF "v[252:255],a[216:219],a[76:79],v[252:255]\n"
TR "a[210:211],v0 offset:520\n"
MF "v[244:247],a[216:219],a[72:75],v[220:223]\n"
MF "v[244:247],a[220:223],a[112:115],v[244:247]\n"
TR "a[212:213],v0 offset:4104\n"
MF "v[236:239],a[220:223],a[108:111],v[220:223]\n"
TR "a[214:215],v0 offset:4616\n"
MF "v[236:239],a[216:219],a[68:71],v[236:239]\n"
LD "v13,s[28:31],s82 offen offset:3072 lds\n"
MF "v[228:231],a[216:219],a[64:67],v[220:223]\n"
MF "v[228:231],a[220:223],a[104:107],v[228:231]\n"
"s_add_u32 s84,s14,20480\n"
MF "v[220:223],a[220:223],a[100:103],v[220:223]\n"
"s_add_u32 m0,s63,2048\n"
MF "v[220:223],a[216:219],a[60:63],v[220:223]\n"
"s_waitcnt lgkmcnt(4)\n"
LD "v4,s[24:27],s84 offen lds\n"
MF "v[16:19],a[200:203],a[140:143],v[16:19]\n"
TR "a[216:217],v1\n"
MF "v[16:19],a[204:207],a[180:183],v[16:19]\n"
TR "a[218:219],v1 offset:512\n"
MF "v[24:27],a[204:207],a[184:187],v[24:27]\n"
MF "v[24:27],a[200:203],a[144:147],v[24:27]\n"
TR "a[220:221],v1 offset:4096\n"
MF "v[32:35],a[200:203],a[148:151],v[32:35]\n"
"s_add_u32 s84,s14,29696\n"
TR "a[222:223],v1 offset:4608\n"
MF "v[32:35],a[204:207],a[188:191],v[32:35]\n"
LD "v5,s[24:27],s84 offen offset:1024 lds\n"
MF "v[40:43],a[204:207],a[192:195],v[40:43]\n"
MF "v[40:43],a[200:203],a[152:155],v[40:43]\n"
"s_add_u32 m0,s62,32768\n"
MF "v[48:51],a[200:203],a[156:159],v[48:51]\n"
"s_add_u32 s82,s66,1573120\n"
"s_sub_u32 s15,s82,7864320\n"
"s_min_u32 s82,s82,s15\n"
MF "v[48:51],a[204:207],a[196:199],v[48:51]\n"
"s_waitcnt lgkmcnt(4)\n"
LD "v6,s[28:31],s82 offen lds\n"
MF "v[52:55],a[212:215],a[196:199],v[52:55]\n"
TR "a[200:201],v1 offset:8\n"
MF "v[52:55],a[208:211],a[156:159],v[52:55]\n"
TR "a[202:203],v1 offset:520\n"
MF "v[44:47],a[208:211],a[152:155],v[44:47]\n"
MF "v[44:47],a[212:215],a[192:195],v[44:47]\n"
TR "a[204:205],v1 offset:4104\n"
MF "v[36:39],a[212:215],a[188:191],v[36:39]\n"
TR "a[206:207],v1 offset:4616\n"
MF "v[36:39],a[208:211],a[148:151],v[36:39]\n"
LD "v11,s[28:31],s82 offen offset:1024 lds\n"
MF "v[28:31],a[208:211],a[144:147],v[28:31]\n"
MF "v[28:31],a[212:215],a[184:187],v[28:31]\n"
MF "v[20:23],a[212:215],a[180:183],v[20:23]\n"
"s_waitcnt vmcnt(18) lgkmcnt(0)\n"
LD "v12,s[28:31],s82 offen offset:2048 lds\n"
"s_bitcmp0_b32 s23,12\n"
"s_cbranch_scc1 .Lisx2\n"
"s_mov_b64 exec,0\n"
"s_cmp_eq_u32 s56,0\n"
"s_cbranch_scc1 .Lpg11d\n"
"s_cmp_eq_u32 s56,1\n"
"s_cbranch_scc1 .Lpg10d\n"
"s_cmp_eq_u32 s56,2\n"
"s_cbranch_scc1 .Lpgt0kx_d\n"
"s_branch .Lpg8d\n"
".Lisr2:\n"
"s_mov_b64 exec,-1\n"
".Lisx2:\n"
MF "v[20:23],a[208:211],a[140:143],v[20:23]\n"
"s_barrier\n"
MF "v[56:59],a[216:219],a[140:143],v[56:59]\n"
TR "a[208:209],v0 offset:16384\n"
MF "v[56:59],a[220:223],a[180:183],v[56:59]\n"
TR "a[210:211],v0 offset:16896\n"
MF "v[64:67],a[220:223],a[184:187],v[64:67]\n"
LD "v13,s[28:31],s82 offen offset:3072 lds\n"
MF "v[64:67],a[216:219],a[144:147],v[64:67]\n"
TR "a[212:213],v0 offset:20480\n"
MF "v[72:75],a[216:219],a[148:151],v[72:75]\n"
TR "a[214:215],v0 offset:20992\n"
MF "v[72:75],a[220:223],a[188:191],v[72:75]\n"
"s_add_u32 s84,s14,40960\n"
MF "v[80:83],a[220:223],a[192:195],v[80:83]\n"
"s_add_u32 m0,s63,4096\n"
MF "v[80:83],a[216:219],a[152:155],v[80:83]\n"
LD "v4,s[24:27],s84 offen lds\n"
MF "v[88:91],a[216:219],a[156:159],v[88:91]\n"
MF "v[88:91],a[220:223],a[196:199],v[88:91]\n"
"s_sub_u32 s66,s66,s83\n"
MF "v[92:95],a[204:207],a[196:199],v[92:95]\n"
"s_add_u32 m0,s62,49152\n"
"s_add_u32 s82,s66,2359296\n"
TR "a[216:217],v0 offset:16392\n"
MF "v[92:95],a[200:203],a[156:159],v[92:95]\n"
"s_sub_u32 s15,s82,7864320\n"
"s_min_u32 s82,s82,s15\n"
TR "a[218:219],v0 offset:16904\n"
MF "v[84:87],a[200:203],a[152:155],v[84:87]\n"
LD "v6,s[28:31],s82 offen lds\n"
MF "v[84:87],a[204:207],a[192:195],v[84:87]\n"
TR "a[220:221],v0 offset:20488\n"
MF "v[76:79],a[204:207],a[188:191],v[76:79]\n"
TR "a[222:223],v0 offset:21000\n"
MF "v[76:79],a[200:203],a[148:151],v[76:79]\n"
MF "v[68:71],a[200:203],a[144:147],v[68:71]\n"
MF "v[68:71],a[204:207],a[184:187],v[68:71]\n"
LD "v11,s[28:31],s82 offen offset:1024 lds\n"
MF "v[60:63],a[204:207],a[180:183],v[60:63]\n"
MF "v[60:63],a[200:203],a[140:143],v[60:63]\n"
"s_waitcnt lgkmcnt(4)\n"
MF "v[96:99],a[208:211],a[140:143],v[96:99]\n"
TR "a[200:201],v1 offset:16384\n"
MF "v[96:99],a[212:215],a[180:183],v[96:99]\n"
TR "a[202:203],v1 offset:16896\n"
MF "v[104:107],a[212:215],a[184:187],v[104:107]\n"
LD "v12,s[28:31],s82 offen offset:2048 lds\n"
MF "v[104:107],a[208:211],a[144:147],v[104:107]\n"
TR "a[204:205],v1 offset:20480\n"
MF "v[112:115],a[208:211],a[148:151],v[112:115]\n"
TR "a[206:207],v1 offset:20992\n"
MF "v[112:115],a[212:215],a[188:191],v[112:115]\n"
MF "v[120:123],a[212:215],a[192:195],v[120:123]\n"
MF "v[120:123],a[208:211],a[152:155],v[120:123]\n"
LD "v13,s[28:31],s82 offen offset:3072 lds\n"
MF "v[128:131],a[208:211],a[156:159],v[128:131]\n"
MF "v[128:131],a[212:215],a[196:199],v[128:131]\n"
"s_add_u32 s14,s81,512\n"
"s_sub_u32 s15,s14,1280\n"
"s_waitcnt lgkmcnt(4)\n"
MF "v[132:135],a[220:223],a[196:199],v[132:135]\n"
"s_min_u32 s14,s14,s15\n"
"s_add_u32 s14,s14,s68\n"
TR "a[208:209],v1 offset:16392\n"
MF "v[132:135],a[216:219],a[156:159],v[132:135]\n"
"s_add_u32 s84,s14,0\n"
"s_add_u32 m0,s63,20480\n"
TR "a[210:211],v1 offset:16904\n"
MF "v[124:127],a[216:219],a[152:155],v[124:127]\n"
LD "v4,s[24:27],s84 offen lds\n"
MF "v[124:127],a[220:223],a[192:195],v[124:127]\n"
TR "a[212:213],v1 offset:20488\n"
MF "v[116:119],a[220:223],a[188:191],v[116:119]\n"
TR "a[214:215],v1 offset:21000\n"
MF "v[116:119],a[216:219],a[148:151],v[116:119]\n"
MF "v[108:111],a[216:219],a[144:147],v[108:111]\n"
"s_add_u32 s84,s14,9216\n"
MF "v[108:111],a[220:223],a[184:187],v[108:111]\n"
LD "v5,s[24:27],s84 offen offset:1024 lds\n"
MF "v[100:103],a[220:223],a[180:183],v[100:103]\n"
"s_waitcnt vmcnt(23) lgkmcnt(0)\n"
MF "v[100:103],a[216:219],a[140:143],v[100:103]\n"
"s_barrier\n"
MF "v[136:139],a[200:203],a[140:143],v[136:139]\n"
TR "a[216:217],v0 offset:32768\n"
MF "v[136:139],a[204:207],a[180:183],v[136:139]\n"
"s_add_u32 m0,s62,65536\n"
TR "a[218:219],v0 offset:33280\n"
MF "v[144:147],a[204:207],a[184:187],v[144:147]\n"
"s_add_u32 s82,s66,2359424\n"
"ds_read_b128 a[60:63],v2 offset:40960\n"
MF "v[144:147],a[200:203],a[144:147],v[144:147]\n"
"s_sub_u32 s15,s82,7864320\n"
TR "a[220:221],v0 offset:36864\n"
MF "v[152:155],a[200:203],a[148:151],v[152:155]\n"
"s_min_u32 s82,s82,s15\n"
TR "a[222:223],v0 offset:37376\n"
MF "v[152:155],a[204:207],a[188:191],v[152:155]\n"
LD "v6,s[28:31],s82 offen lds\n"
MF "v[160:163],a[204:207],a[192:195],v[160:163]\n"
"ds_read_b128 a[100:103],v3 offset:40960\n"
MF "v[160:163],a[200:203],a[152:155],v[160:163]\n"
LD "v11,s[28:31],s82 offen offset:1024 lds\n"
MF "v[168:171],a[200:203],a[156:159],v[168:171]\n"
MF "v[168:171],a[204:207],a[196:199],v[168:171]\n"
LD "v12,s[28:31],s82 offen offset:2048 lds\n"
MF "v[172:175],a[212:215],a[196:199],v[172:175]\n"
TR "a[200:201],v0 offset:32776\n"
MF "v[172:175],a[208:211],a[156:159],v[172:175]\n"
TR "a[202:203],v0 offset:33288\n"
MF "v[164:167],a[208:211],a[152:155],v[164:167]\n"
"ds_read_b128 a[64:67],v2 offset:43008\n"
MF "v[164:167],a[212:215],a[192:195],v[164:167]\n"
TR "a[204:205],v0 offset:36872\n"
MF "v[156:159],a[212:215],a[188:191],v[156:159]\n"
TR "a[206:207],v0 offset:37384\n"
MF "v[156:159],a[208:211],a[148:151],v[156:159]\n"
MF "v[148:151],a[208:211],a[144:147],v[148:151]\n"
"ds_read_b128 a[104:107],v3 offset:43008\n"
MF "v[148:151],a[212:215],a[184:187],v[148:151]\n"
MF "v[140:143],a[212:215],a[180:183],v[140:143]\n"
LD "v13,s[28:31],s82 offen offset:3072 lds\n"
MF "v[140:143],a[208:211],a[140:143],v[140:143]\n"
"s_waitcnt lgkmcnt(7)\n"
MF "v[176:179],a[216:219],a[140:143],v[176:179]\n"
TR "a[208:209],v1 offset:32768\n"
MF "v[176:179],a[220:223],a[180:183],v[176:179]\n"
TR "a[210:211],v1 offset:33280\n"
MF "v[184:187],a[220:223],a[184:187],v[184:187]\n"
"ds_read_b128 a[68:71],v2 offset:45056\n"
MF "v[184:187],a[216:219],a[144:147],v[184:187]\n"
"s_add_u32 s84,s14,20480\n"
TR "a[212:213],v1 offset:36864\n"
MF "v[192:195],a[216:219],a[148:151],v[192:195]\n"
"s_add_u32 m0,s63,22528\n"
TR "a[214:215],v1 offset:37376\n"
MF "v[192:195],a[220:223],a[188:191],v[192:195]\n"
LD "v4,s[24:27],s84 offen lds\n"
MF "v[200:203],a[220:223],a[192:195],v[200:203]\n"
"ds_read_b128 a[108:111],v3 offset:45056\n"
MF "v[200:203],a[216:219],a[152:155],v[200:203]\n"
MF "v[208:211],a[216:219],a[156:159],v[208:211]\n"
"s_add_u32 s84,s14,29696\n"
MF "v[208:211],a[220:223],a[196:199],v[208:211]\n"
"s_waitcnt lgkmcnt(7)\n"
LD "v5,s[24:27],s84 offen offset:1024 lds\n"
MF "v[212:215],a[204:207],a[196:199],v[212:215]\n"
TR "a[216:217],v1 offset:32776\n"
MF "v[212:215],a[200:203],a[156:159],v[212:215]\n"
TR "a[218:219],v1 offset:33288\n"
MF "v[204:207],a[200:203],a[152:155],v[204:207]\n"
"ds_read_b128 a[72:75],v2 offset:47104\n"
MF "v[204:207],a[204:207],a[192:195],v[204:207]\n"
TR "a[220:221],v1 offset:36872\n"
MF "v[196:199],a[204:207],a[188:191],v[196:199]\n"
TR "a[222:223],v1 offset:37384\n"
MF "v[196:199],a[200:203],a[148:151],v[196:199]\n"
MF "v[188:191],a[200:203],a[144:147],v[188:191]\n"
"v_add_u32 v0,0xffff4000,v0\n"
MF "v[188:191],a[204:207],a[184:187],v[188:191]\n"
"v_add_u32 v1,0xffff4000,v1\n"
MF "v[180:183],a[204:207],a[180:183],v[180:183]\n"
"s_waitcnt vmcnt(25) lgkmcnt(0)\n"
MF "v[180:183],a[200:203],a[140:143],v[180:183]\n"
"s_barrier\n"
MF "v[216:219],a[208:211],a[140:143],v[216:219]\n"
TR "a[200:201],v0\n"
MF "v[216:219],a[212:215],a[180:183],v[216:219]\n"
"s_add_u32 m0,s62,81920\n"
TR "a[202:203],v0 offset:512\n"
MF "v[224:227],a[212:215],a[184:187],v[224:227]\n"
"s_add_u32 s82,s66,2359552\n"
"ds_read_b128 a[112:115],v3 offset:47104\n"
MF "v[224:227],a[208:211],a[144:147],v[224:227]\n"
"s_sub_u32 s15,s82,7864320\n"
TR "a[204:205],v0 offset:4096\n"
MF "v[232:235],a[208:211],a[148:151],v[232:235]\n"
"s_min_u32 s82,s82,s15\n"
TR "a[206:207],v0 offset:4608\n"
MF "v[232:235],a[212:215],a[188:191],v[232:235]\n"
LD "v6,s[28:31],s82 offen lds\n"
MF "v[240:243],a[212:215],a[192:195],v[240:243]\n"
"ds_read_b128 a[76:79],v2 offset:49152\n"
MF "v[240:243],a[208:211],a[152:155],v[240:243]\n"
"ds_read_b128 a[116:119],v3 offset:49152\n"
MF "v[248:251],a[208:211],a[156:159],v[248:251]\n"
LD "v11,s[28:31],s82 offen offset:1024 lds\n"
MF "v[248:251],a[212:215],a[196:199],v[248:251]\n"
MF "v[252:255],a[220:223],a[196:199],v[252:255]\n"
TR "a[208:209],v0 offset:8\n"
MF "v[252:255],a[216:219],a[156:159],v[252:255]\n"
TR "a[210:211],v0 offset:520\n"
MF "v[244:247],a[216:219],a[152:155],v[244:247]\n"
MF "v[244:247],a[220:223],a[192:195],v[244:247]\n"
TR "a[212:213],v0 offset:4104\n"
MF "v[236:239],a[220:223],a[188:191],v[236:239]\n"
TR "a[214:215],v0 offset:4616\n"
MF "v[236:239],a[216:219],a[148:151],v[236:239]\n"
LD "v12,s[28:31],s82 offen offset:2048 lds\n"
MF "v[228:231],a[216:219],a[144:147],v[228:231]\n"
MF "v[228:231],a[220:223],a[184:187],v[228:231]\n"
MF "v[220:223],a[220:223],a[180:183],v[220:223]\n"
MF "v[220:223],a[216:219],a[140:143],v[220:223]\n"
"s_waitcnt lgkmcnt(4)\n"
MF "v[16:19],a[200:203],a[60:63],v[16:19]\n"
TR "a[216:217],v1\n"
MF "v[16:19],a[204:207],a[100:103],v[16:19]\n"
TR "a[218:219],v1 offset:512\n"
MF "v[24:27],a[204:207],a[104:107],v[24:27]\n"
LD "v13,s[28:31],s82 offen offset:3072 lds\n"
MF "v[24:27],a[200:203],a[64:67],v[24:27]\n"
TR "a[220:221],v1 offset:4096\n"
MF "v[32:35],a[200:203],a[68:71],v[32:35]\n"
TR "a[222:223],v1 offset:4608\n"
MF "v[32:35],a[204:207],a[108:111],v[32:35]\n"
MF "v[40:43],a[204:207],a[112:115],v[40:43]\n"
MF "v[40:43],a[200:203],a[72:75],v[40:43]\n"
"s_add_u32 s84,s14,40960\n"
MF "v[48:51],a[200:203],a[76:79],v[48:51]\n"
"s_add_u32 m0,s63,24576\n"
MF "v[48:51],a[204:207],a[116:119],v[48:51]\n"
"s_waitcnt lgkmcnt(4)\n"
LD "v4,s[24:27],s84 offen lds\n"
MF "v[52:55],a[212:215],a[116:119],v[52:55]\n"
TR "a[200:201],v1 offset:8\n"
MF "v[52:55],a[208:211],a[76:79],v[52:55]\n"
TR "a[202:203],v1 offset:520\n"
MF "v[44:47],a[208:211],a[72:75],v[44:47]\n"
MF "v[44:47],a[212:215],a[112:115],v[44:47]\n"
TR "a[204:205],v1 offset:4104\n"
MF "v[36:39],a[212:215],a[108:111],v[36:39]\n"
TR "a[206:207],v1 offset:4616\n"
MF "v[36:39],a[208:211],a[68:71],v[36:39]\n"
MF "v[28:31],a[208:211],a[64:67],v[28:31]\n"
MF "v[28:31],a[212:215],a[104:107],v[28:31]\n"
MF "v[20:23],a[212:215],a[100:103],v[20:23]\n"
"s_waitcnt vmcnt(24) lgkmcnt(0)\n"
MF "v[20:23],a[208:211],a[60:63],v[20:23]\n"
"s_barrier\n"
MF "v[56:59],a[216:219],a[60:63],v[56:59]\n"
"s_add_u32 m0,s62,0\n"
"s_add_u32 s82,s66,3145728\n"
TR "a[208:209],v0 offset:16384\n"
MF "v[56:59],a[220:223],a[100:103],v[56:59]\n"
"s_sub_u32 s15,s82,7864320\n"
"s_min_u32 s82,s82,s15\n"
TR "a[210:211],v0 offset:16896\n"
MF "v[64:67],a[220:223],a[104:107],v[64:67]\n"
LD "v6,s[28:31],s82 offen lds\n"
MF "v[64:67],a[216:219],a[64:67],v[64:67]\n"
TR "a[212:213],v0 offset:20480\n"
MF "v[72:75],a[216:219],a[68:71],v[72:75]\n"
TR "a[214:215],v0 offset:20992\n"
MF "v[72:75],a[220:223],a[108:111],v[72:75]\n"
MF "v[80:83],a[220:223],a[112:115],v[80:83]\n"
LD "v11,s[28:31],s82 offen offset:1024 lds\n"
MF "v[80:83],a[216:219],a[72:75],v[80:83]\n"
MF "v[88:91],a[216:219],a[76:79],v[88:91]\n"
MF "v[88:91],a[220:223],a[116:119],v[88:91]\n"
MF "v[92:95],a[204:207],a[116:119],v[92:95]\n"
TR "a[216:217],v0 offset:16392\n"
MF "v[92:95],a[200:203],a[76:79],v[92:95]\n"
TR "a[218:219],v0 offset:16904\n"
MF "v[84:87],a[200:203],a[72:75],v[84:87]\n"
LD "v12,s[28:31],s82 offen offset:2048 lds\n"
MF "v[84:87],a[204:207],a[112:115],v[84:87]\n"
TR "a[220:221],v0 offset:20488\n"
MF "v[76:79],a[204:207],a[108:111],v[76:79]\n"
TR "a[222:223],v0 offset:21000\n"
MF "v[76:79],a[200:203],a[68:71],v[76:79]\n"
MF "v[68:71],a[200:203],a[64:67],v[68:71]\n"
MF "v[68:71],a[204:207],a[104:107],v[68:71]\n"
MF "v[60:63],a[204:207],a[100:103],v[60:63]\n"
LD "v13,s[28:31],s82 offen offset:3072 lds\n"
MF "v[60:63],a[200:203],a[60:63],v[60:63]\n"
"s_waitcnt lgkmcnt(4)\n"
MF "v[96:99],a[208:211],a[60:63],v[96:99]\n"
TR "a[200:201],v1 offset:16384\n"
MF "v[96:99],a[212:215],a[100:103],v[96:99]\n"
TR "a[202:203],v1 offset:16896\n"
MF "v[104:107],a[212:215],a[104:107],v[104:107]\n"
"s_add_u32 s14,s81,640\n"
"s_sub_u32 s15,s14,1280\n"
MF "v[104:107],a[208:211],a[64:67],v[104:107]\n"
"s_min_u32 s14,s14,s15\n"
"s_add_u32 s14,s14,s68\n"
TR "a[204:205],v1 offset:20480\n"
MF "v[112:115],a[208:211],a[68:71],v[112:115]\n"
"s_add_u32 s84,s14,0\n"
"s_add_u32 m0,s63,40960\n"
TR "a[206:207],v1 offset:20992\n"
MF "v[112:115],a[212:215],a[108:111],v[112:115]\n"
LD "v4,s[24:27],s84 offen lds\n"
MF "v[120:123],a[212:215],a[112:115],v[120:123]\n"
MF "v[120:123],a[208:211],a[72:75],v[120:123]\n"
MF "v[128:131],a[208:211],a[76:79],v[128:131]\n"
"s_add_u32 s84,s14,9216\n"
MF "v[128:131],a[212:215],a[116:119],v[128:131]\n"
"s_waitcnt lgkmcnt(4)\n"
LD "v5,s[24:27],s84 offen offset:1024 lds\n"
MF "v[132:135],a[220:223],a[116:119],v[132:135]\n"
TR "a[208:209],v1 offset:16392\n"
MF "v[132:135],a[216:219],a[76:79],v[132:135]\n"
TR "a[210:211],v1 offset:16904\n"
MF "v[124:127],a[216:219],a[72:75],v[124:127]\n"
MF "v[124:127],a[220:223],a[112:115],v[124:127]\n"
TR "a[212:213],v1 offset:20488\n"
MF "v[116:119],a[220:223],a[108:111],v[116:119]\n"
TR "a[214:215],v1 offset:21000\n"
MF "v[116:119],a[216:219],a[68:71],v[116:119]\n"
MF "v[108:111],a[216:219],a[64:67],v[108:111]\n"
MF "v[108:111],a[220:223],a[104:107],v[108:111]\n"
MF "v[100:103],a[220:223],a[100:103],v[100:103]\n"
"s_waitcnt vmcnt(23) lgkmcnt(0)\n"
MF "v[100:103],a[216:219],a[60:63],v[100:103]\n"
"s_barrier\n"
MF "v[136:139],a[200:203],a[60:63],v[136:139]\n"
TR "a[216:217],v0 offset:32768\n"
MF "v[136:139],a[204:207],a[100:103],v[136:139]\n"
"s_add_u32 m0,s62,16384\n"
TR "a[218:219],v0 offset:33280\n"
MF "v[144:147],a[204:207],a[104:107],v[144:147]\n"
"s_add_u32 s82,s66,3145856\n"
"ds_read_b128 a[140:143],v2\n"
MF "v[144:147],a[200:203],a[64:67],v[144:147]\n"
"s_sub_u32 s15,s82,7864320\n"
TR "a[220:221],v0 offset:36864\n"
MF "v[152:155],a[200:203],a[68:71],v[152:155]\n"
"s_min_u32 s82,s82,s15\n"
TR "a[222:223],v0 offset:37376\n"
MF "v[152:155],a[204:207],a[108:111],v[152:155]\n"
LD "v6,s[28:31],s82 offen lds\n"
MF "v[160:163],a[204:207],a[112:115],v[160:163]\n"
"ds_read_b128 a[180:183],v3\n"
MF "v[160:163],a[200:203],a[72:75],v[160:163]\n"
LD "v11,s[28:31],s82 offen offset:1024 lds\n"
MF "v[168:171],a[200:203],a[76:79],v[168:171]\n"
MF "v[168:171],a[204:207],a[116:119],v[168:171]\n"
LD "v12,s[28:31],s82 offen offset:2048 lds\n"
MF "v[172:175],a[212:215],a[116:119],v[172:175]\n"
TR "a[200:201],v0 offset:32776\n"
MF "v[172:175],a[208:211],a[76:79],v[172:175]\n"
TR "a[202:203],v0 offset:33288\n"
MF "v[164:167],a[208:211],a[72:75],v[164:167]\n"
"ds_read_b128 a[144:147],v2 offset:2048\n"
MF "v[164:167],a[212:215],a[112:115],v[164:167]\n"
TR "a[204:205],v0 offset:36872\n"
MF "v[156:159],a[212:215],a[108:111],v[156:159]\n"
TR "a[206:207],v0 offset:37384\n"
MF "v[156:159],a[208:211],a[68:71],v[156:159]\n"
MF "v[148:151],a[208:211],a[64:67],v[148:151]\n"
"ds_read_b128 a[184:187],v3 offset:2048\n"
MF "v[148:151],a[212:215],a[104:107],v[148:151]\n"
MF "v[140:143],a[212:215],a[100:103],v[140:143]\n"
LD "v13,s[28:31],s82 offen offset:3072 lds\n"
MF "v[140:143],a[208:211],a[60:63],v[140:143]\n"
"s_waitcnt lgkmcnt(7)\n"
MF "v[176:179],a[216:219],a[60:63],v[176:179]\n"
TR "a[208:209],v1 offset:32768\n"
MF "v[176:179],a[220:223],a[100:103],v[176:179]\n"
TR "a[210:211],v1 offset:33280\n"
MF "v[184:187],a[220:223],a[104:107],v[184:187]\n"
"ds_read_b128 a[148:151],v2 offset:4096\n"
MF "v[184:187],a[216:219],a[64:67],v[184:187]\n"
"s_add_u32 s84,s14,20480\n"
TR "a[212:213],v1 offset:36864\n"
MF "v[192:195],a[216:219],a[68:71],v[192:195]\n"
"s_add_u32 m0,s63,43008\n"
TR "a[214:215],v1 offset:37376\n"
MF "v[192:195],a[220:223],a[108:111],v[192:195]\n"
LD "v4,s[24:27],s84 offen lds\n"
MF "v[200:203],a[220:223],a[112:115],v[200:203]\n"
"ds_read_b128 a[188:191],v3 offset:4096\n"
MF "v[200:203],a[216:219],a[72:75],v[200:203]\n"
MF "v[208:211],a[216:219],a[76:79],v[208:211]\n"
"s_add_u32 s84,s14,29696\n"
MF "v[208:211],a[220:223],a[116:119],v[208:211]\n"
"s_waitcnt lgkmcnt(7)\n"
LD "v5,s[24:27],s84 offen offset:1024 lds\n"
MF "v[212:215],a[204:207],a[116:119],v[212:215]\n"
TR "a[216:217],v1 offset:32776\n"
MF "v[212:215],a[200:203],a[76:79],v[212:215]\n"
TR "a[218:219],v1 offset:33288\n"
MF "v[204:207],a[200:203],a[72:75],v[204:207]\n"
"ds_read_b128 a[152:155],v2 offset:6144\n"
MF "v[204:207],a[204:207],a[112:115],v[204:207]\n"
TR "a[220:221],v1 offset:36872\n"
MF "v[196:199],a[204:207],a[108:111],v[196:199]\n"
TR "a[222:223],v1 offset:37384\n"
MF "v[196:199],a[200:203],a[68:71],v[196:199]\n"
MF "v[188:191],a[200:203],a[64:67],v[188:191]\n"
"v_add_u32 v0,0xc000,v0\n"
MF "v[188:191],a[204:207],a[104:107],v[188:191]\n"
"v_add_u32 v1,0xc000,v1\n"
MF "v[180:183],a[204:207],a[100:103],v[180:183]\n"
"s_waitcnt vmcnt(25) lgkmcnt(0)\n"
MF "v[180:183],a[200:203],a[60:63],v[180:183]\n"
"s_barrier\n"
MF "v[216:219],a[208:211],a[60:63],v[216:219]\n"
TR "a[200:201],v0\n"
MF "v[216:219],a[212:215],a[100:103],v[216:219]\n"
"s_add_u32 m0,s62,32768\n"
TR "a[202:203],v0 offset:512\n"
MF "v[224:227],a[212:215],a[104:107],v[224:227]\n"
".Lpg5c:\n"
"s_cbranch_execz .Lpg15c\n"
".Lpg5f:\n"
"s_cbranch_execz .LpgattB_f\n"
"s_add_u32 s82,s66,3145984\n"
"ds_read_b128 a[192:195],v3 offset:6144\n"
MF "v[224:227],a[208:211],a[64:67],v[224:227]\n"
"s_sub_u32 s15,s82,7864320\n"
TR "a[204:205],v0 offset:4096\n"
MF "v[232:235],a[208:211],a[68:71],v[232:235]\n"
"s_min_u32 s82,s82,s15\n"
TR "a[206:207],v0 offset:4608\n"
MF "v[232:235],a[212:215],a[108:111],v[232:235]\n"
LD "v6,s[28:31],s82 offen lds\n"
MF "v[240:243],a[212:215],a[112:115],v[240:243]\n"
"ds_read_b128 a[156:159],v2 offset:8192\n"
MF "v[240:243],a[208:211],a[72:75],v[240:243]\n"
"ds_read_b128 a[196:199],v3 offset:8192\n"
MF "v[248:251],a[208:211],a[76:79],v[248:251]\n"
LD "v11,s[28:31],s82 offen offset:1024 lds\n"
MF "v[248:251],a[212:215],a[116:119],v[248:251]\n"
MF "v[252:255],a[220:223],a[116:119],v[252:255]\n"
TR "a[208:209],v0 offset:8\n"
MF "v[252:255],a[216:219],a[76:79],v[252:255]\n"
TR "a[210:211],v0 offset:520\n"
MF "v[244:247],a[216:219],a[72:75],v[244:247]\n"
MF "v[244:247],a[220:223],a[112:115],v[244:247]\n"
TR "a[212:213],v0 offset:4104\n"
MF "v[236:239],a[220:223],a[108:111],v[236:239]\n"
TR "a[214:215],v0 offset:4616\n"
MF "v[236:239],a[216:219],a[68:71],v[236:239]\n"
LD "v12,s[28:31],s82 offen offset:2048 lds\n"
MF "v[228:231],a[216:219],a[64:67],v[228:231]\n"
MF "v[228:231],a[220:223],a[104:107],v[228:231]\n"
MF "v[220:223],a[220:223],a[100:103],v[220:223]\n"
MF "v[220:223],a[216:219],a[60:63],v[220:223]\n"
"s_waitcnt lgkmcnt(4)\n"
MF "v[16:19],a[200:203],a[140:143],v[16:19]\n"
TR "a[216:217],v1\n"
MF "v[16:19],a[204:207],a[180:183],v[16:19]\n"
TR "a[218:219],v1 offset:512\n"
MF "v[24:27],a[204:207],a[184:187],v[24:27]\n"
LD "v13,s[28:31],s82 offen offset:3072 lds\n"
MF "v[24:27],a[200:203],a[144:147],v[24:27]\n"
TR "a[220:221],v1 offset:4096\n"
MF "v[32:35],a[200:203],a[148:151],v[32:35]\n"
TR "a[222:223],v1 offset:4608\n"
MF "v[32:35],a[204:207],a[188:191],v[32:35]\n"
MF "v[40:43],a[204:207],a[192:195],v[40:43]\n"
MF "v[40:43],a[200:203],a[152:155],v[40:43]\n"
"s_add_u32 s84,s14,40960\n"
MF "v[48:51],a[200:203],a[156:159],v[48:51]\n"
"s_add_u32 m0,s63,45056\n"
MF "v[48:51],a[204:207],a[196:199],v[48:51]\n"
"s_waitcnt lgkmcnt(4)\n"
LD "v4,s[24:27],s84 offen lds\n"
MF "v[52:55],a[212:215],a[196:199],v[52:55]\n"
TR "a[200:201],v1 offset:8\n"
MF "v[52:55],a[208:211],a[156:159],v[52:55]\n"
TR "a[202:203],v1 offset:520\n"
MF "v[44:47],a[208:211],a[152:155],v[44:47]\n"
MF "v[44:47],a[212:215],a[192:195],v[44:47]\n"
TR "a[204:205],v1 offset:4104\n"
MF "v[36:39],a[212:215],a[188:191],v[36:39]\n"
TR "a[206:207],v1 offset:4616\n"
MF "v[36:39],a[208:211],a[148:151],v[36:39]\n"
MF "v[28:31],a[208:211],a[144:147],v[28:31]\n"
MF "v[28:31],a[212:215],a[184:187],v[28:31]\n"
MF "v[20:23],a[212:215],a[180:183],v[20:23]\n"
"s_waitcnt vmcnt(24) lgkmcnt(0)\n"
MF "v[20:23],a[208:211],a[140:143],v[20:23]\n"
"s_barrier\n"
MF "v[56:59],a[216:219],a[140:143],v[56:59]\n"
"s_add_u32 m0,s62,49152\n"
"s_add_u32 s82,s66,3932160\n"
TR "a[208:209],v0 offset:16384\n"
MF "v[56:59],a[220:223],a[180:183],v[56:59]\n"
"s_sub_u32 s15,s82,7864320\n"
"s_min_u32 s82,s82,s15\n"
TR "a[210:211],v0 offset:16896\n"
MF "v[64:67],a[220:223],a[184:187],v[64:67]\n"
LD "v6,s[28:31],s82 offen lds\n"
MF "v[64:67],a[216:219],a[144:147],v[64:67]\n"
TR "a[212:213],v0 offset:20480\n"
MF "v[72:75],a[216:219],a[148:151],v[72:75]\n"
TR "a[214:215],v0 offset:20992\n"
MF "v[72:75],a[220:223],a[188:191],v[72:75]\n"
MF "v[80:83],a[220:223],a[192:195],v[80:83]\n"
LD "v11,s[28:31],s82 offen offset:1024 lds\n"
MF "v[80:83],a[216:219],a[152:155],v[80:83]\n"
MF "v[88:91],a[216:219],a[156:159],v[88:91]\n"
MF "v[88:91],a[220:223],a[196:199],v[88:91]\n"
MF "v[92:95],a[204:207],a[196:199],v[92:95]\n"
TR "a[216:217],v0 offset:16392\n"
MF "v[92:95],a[200:203],a[156:159],v[92:95]\n"
TR "a[218:219],v0 offset:16904\n"
MF "v[84:87],a[200:203],a[152:155],v[84:87]\n"
LD "v12,s[28:31],s82 offen offset:2048 lds\n"
MF "v[84:87],a[204:207],a[192:195],v[84:87]\n"
TR "a[220:221],v0 offset:20488\n"
MF "v[76:79],a[204:207],a[188:191],v[76:79]\n"
TR "a[222:223],v0 offset:21000\n"
MF "v[76:79],a[200:203],a[148:151],v[76:79]\n"
MF "v[68:71],a[200:203],a[144:147],v[68:71]\n"
MF "v[68:71],a[204:207],a[184:187],v[68:71]\n"
MF "v[60:63],a[204:207],a[180:183],v[60:63]\n"
LD "v13,s[28:31],s82 offen offset:3072 lds\n"
MF "v[60:63],a[200:203],a[140:143],v[60:63]\n"
"s_waitcnt lgkmcnt(4)\n"
MF "v[96:99],a[208:211],a[140:143],v[96:99]\n"
TR "a[200:201],v1 offset:16384\n"
MF "v[96:99],a[212:215],a[180:183],v[96:99]\n"
TR "a[202:203],v1 offset:16896\n"
MF "v[104:107],a[212:215],a[184:187],v[104:107]\n"
"s_add_u32 s14,s81,768\n"
"s_sub_u32 s15,s14,1280\n"
MF "v[104:107],a[208:211],a[144:147],v[104:107]\n"
"s_min_u32 s14,s14,s15\n"
"s_add_u32 s14,s14,s68\n"
TR "a[204:205],v1 offset:20480\n"
MF "v[112:115],a[208:211],a[148:151],v[112:115]\n"
"s_add_u32 s84,s14,0\n"
"s_add_u32 m0,s63,0\n"
TR "a[206:207],v1 offset:20992\n"
MF "v[112:115],a[212:215],a[188:191],v[112:115]\n"
LD "v4,s[24:27],s84 offen lds\n"
MF "v[120:123],a[212:215],a[192:195],v[120:123]\n"
MF "v[120:123],a[208:211],a[152:155],v[120:123]\n"
MF "v[128:131],a[208:211],a[156:159],v[128:131]\n"
"s_add_u32 s84,s14,9216\n"
MF "v[128:131],a[212:215],a[196:199],v[128:131]\n"
"s_waitcnt lgkmcnt(4)\n"
LD "v5,s[24:27],s84 offen offset:1024 lds\n"
MF "v[132:135],a[220:223],a[196:199],v[132:135]\n"
TR "a[208:209],v1 offset:16392\n"
MF "v[132:135],a[216:219],a[156:159],v[132:135]\n"
TR "a[210:211],v1 offset:16904\n"
MF "v[124:127],a[216:219],a[152:155],v[124:127]\n"
MF "v[124:127],a[220:223],a[192:195],v[124:127]\n"
TR "a[212:213],v1 offset:20488\n"
MF "v[116:119],a[220:223],a[188:191],v[116:119]\n"
TR "a[214:215],v1 offset:21000\n"
MF "v[116:119],a[216:219],a[148:151],v[116:119]\n"
MF "v[108:111],a[216:219],a[144:147],v[108:111]\n"
MF "v[108:111],a[220:223],a[184:187],v[108:111]\n"
MF "v[100:103],a[220:223],a[180:183],v[100:103]\n"
"s_waitcnt vmcnt(23) lgkmcnt(0)\n"
MF "v[100:103],a[216:219],a[140:143],v[100:103]\n"
"s_barrier\n"
MF "v[136:139],a[200:203],a[140:143],v[136:139]\n"
TR "a[216:217],v0 offset:32768\n"
MF "v[136:139],a[204:207],a[180:183],v[136:139]\n"
"s_add_u32 m0,s62,65536\n"
TR "a[218:219],v0 offset:33280\n"
MF "v[144:147],a[204:207],a[184:187],v[144:147]\n"
"s_add_u32 s82,s66,3932288\n"
"ds_read_b128 a[60:63],v2 offset:20480\n"
MF "v[144:147],a[200:203],a[144:147],v[144:147]\n"
"s_sub_u32 s15,s82,7864320\n"
TR "a[220:221],v0 offset:36864\n"
MF "v[152:155],a[200:203],a[148:151],v[152:155]\n"
"s_min_u32 s82,s82,s15\n"
TR "a[222:223],v0 offset:37376\n"
MF "v[152:155],a[204:207],a[188:191],v[152:155]\n"
LD "v6,s[28:31],s82 offen lds\n"
MF "v[160:163],a[204:207],a[192:195],v[160:163]\n"
"ds_read_b128 a[100:103],v3 offset:20480\n"
MF "v[160:163],a[200:203],a[152:155],v[160:163]\n"
LD "v11,s[28:31],s82 offen offset:1024 lds\n"
MF "v[168:171],a[200:203],a[156:159],v[168:171]\n"
MF "v[168:171],a[204:207],a[196:199],v[168:171]\n"
LD "v12,s[28:31],s82 offen offset:2048 lds\n"
MF "v[172:175],a[212:215],a[196:199],v[172:175]\n"
TR "a[200:201],v0 offset:32776\n"
MF "v[172:175],a[208:211],a[156:159],v[172:175]\n"
TR "a[202:203],v0 offset:33288\n"
MF "v[164:167],a[208:211],a[152:155],v[164:167]\n"
"ds_read_b128 a[64:67],v2 offset:22528\n"
MF "v[164:167],a[212:215],a[192:195],v[164:167]\n"
TR "a[204:205],v0 offset:36872\n"
MF "v[156:159],a[212:215],a[188:191],v[156:159]\n"
TR "a[206:207],v0 offset:37384\n"
MF "v[156:159],a[208:211],a[148:151],v[156:159]\n"
MF "v[148:151],a[208:211],a[144:147],v[148:151]\n"
"ds_read_b128 a[104:107],v3 offset:22528\n"
MF "v[148:151],a[212:215],a[184:187],v[148:151]\n"
MF "v[140:143],a[212:215],a[180:183],v[140:143]\n"
LD "v13,s[28:31],s82 offen offset:3072 lds\n"
MF "v[140:143],a[208:211],a[140:143],v[140:143]\n"
"s_waitcnt lgkmcnt(7)\n"
MF "v[176:179],a[216:219],a[140:143],v[176:179]\n"
TR "a[208:209],v1 offset:32768\n"
MF "v[176:179],a[220:223],a[180:183],v[176:179]\n"
TR "a[210:211],v1 offset:33280\n"
MF "v[184:187],a[220:223],a[184:187],v[184:187]\n"
"ds_read_b128 a[68:71],v2 offset:24576\n"
MF "v[184:187],a[216:219],a[144:147],v[184:187]\n"
"s_add_u32 s84,s14,20480\n"
TR "a[212:213],v1 offset:36864\n"
MF "v[192:195],a[216:219],a[148:151],v[192:195]\n"
"s_add_u32 m0,s63,2048\n"
TR "a[214:215],v1 offset:37376\n"
MF "v[192:195],a[220:223],a[188:191],v[192:195]\n"
LD "v4,s[24:27],s84 offen lds\n"
MF "v[200:203],a[220:223],a[192:195],v[200:203]\n"
"ds_read_b128 a[108:111],v3 offset:24576\n"
MF "v[200:203],a[216:219],a[152:155],v[200:203]\n"
MF "v[208:211],a[216:219],a[156:159],v[208:211]\n"
"s_add_u32 s84,s14,29696\n"
MF "v[208:211],a[220:223],a[196:199],v[208:211]\n"
"s_waitcnt lgkmcnt(7)\n"
LD "v5,s[24:27],s84 offen offset:1024 lds\n"
MF "v[212:215],a[204:207],a[196:199],v[212:215]\n"
TR "a[216:217],v1 offset:32776\n"
MF "v[212:215],a[200:203],a[156:159],v[212:215]\n"
TR "a[218:219],v1 offset:33288\n"
MF "v[204:207],a[200:203],a[152:155],v[204:207]\n"
"ds_read_b128 a[72:75],v2 offset:26624\n"
MF "v[204:207],a[204:207],a[192:195],v[204:207]\n"
TR "a[220:221],v1 offset:36872\n"
MF "v[196:199],a[204:207],a[188:191],v[196:199]\n"
TR "a[222:223],v1 offset:37384\n"
MF "v[196:199],a[200:203],a[148:151],v[196:199]\n"
MF "v[188:191],a[200:203],a[144:147],v[188:191]\n"
"v_add_u32 v0,0xffff4000,v0\n"
MF "v[188:191],a[204:207],a[184:187],v[188:191]\n"
"v_add_u32 v1,0xffff4000,v1\n"
MF "v[180:183],a[204:207],a[180:183],v[180:183]\n"
"s_waitcnt vmcnt(25) lgkmcnt(0)\n"
MF "v[180:183],a[200:203],a[140:143],v[180:183]\n"
"s_barrier\n"
MF "v[216:219],a[208:211],a[140:143],v[216:219]\n"
TR "a[200:201],v0\n"
MF "v[216:219],a[212:215],a[180:183],v[216:219]\n"
"s_add_u32 m0,s62,81920\n"
TR "a[202:203],v0 offset:512\n"
MF "v[224:227],a[212:215],a[184:187],v[224:227]\n"
"s_add_u32 s82,s66,3932416\n"
"ds_read_b128 a[112:115],v3 offset:26624\n"
MF "v[224:227],a[208:211],a[144:147],v[224:227]\n"
"s_sub_u32 s15,s82,7864320\n"
TR "a[204:205],v0 offset:4096\n"
MF "v[232:235],a[208:211],a[148:151],v[232:235]\n"
"s_min_u32 s82,s82,s15\n"
TR "a[206:207],v0 offset:4608\n"
MF "v[232:235],a[212:215],a[188:191],v[232:235]\n"
LD "v6,s[28:31],s82 offen lds\n"
MF "v[240:243],a[212:215],a[192:195],v[240:243]\n"
"ds_read_b128 a[76:79],v2 offset:28672\n"
MF "v[240:243],a[208:211],a[152:155],v[240:243]\n"
"ds_read_b128 a[116:119],v3 offset:28672\n"
MF "v[248:251],a[208:211],a[156:159],v[248:251]\n"
LD "v11,s[28:31],s82 offen offset:1024 lds\n"
MF "v[248:251],a[212:215],a[196:199],v[248:251]\n"
MF "v[252:255],a[220:223],a[196:199],v[252:255]\n"
TR "a[208:209],v0 offset:8\n"
MF "v[252:255],a[216:219],a[156:159],v[252:255]\n"
TR "a[210:211],v0 offset:520\n"
MF "v[244:247],a[216:219],a[152:155],v[244:247]\n"
MF "v[244:247],a[220:223],a[192:195],v[244:247]\n"
TR "a[212:213],v0 offset:4104\n"
MF "v[236:239],a[220:223],a[188:191],v[236:239]\n"
TR "a[214:215],v0 offset:4616\n"
MF "v[236:239],a[216:219],a[148:151],v[236:239]\n"
LD "v12,s[28:31],s82 offen offset:2048 lds\n"
MF "v[228:231],a[216:219],a[144:147],v[228:231]\n"
MF "v[228:231],a[220:223],a[184:187],v[228:231]\n"
MF "v[220:223],a[220:223],a[180:183],v[220:223]\n"
MF "v[220:223],a[216:219],a[140:143],v[220:223]\n"
"s_waitcnt lgkmcnt(4)\n"
MF "v[16:19],a[200:203],a[60:63],v[16:19]\n"
TR "a[216:217],v1\n"
MF "v[16:19],a[204:207],a[100:103],v[16:19]\n"
TR "a[218:219],v1 offset:512\n"
MF "v[24:27],a[204:207],a[104:107],v[24:27]\n"
LD "v13,s[28:31],s82 offen offset:3072 lds\n"
MF "v[24:27],a[200:203],a[64:67],v[24:27]\n"
TR "a[220:221],v1 offset:4096\n"
MF "v[32:35],a[200:203],a[68:71],v[32:35]\n"
TR "a[222:223],v1 offset:4608\n"
MF "v[32:35],a[204:207],a[108:111],v[32:35]\n"
MF "v[40:43],a[204:207],a[112:115],v[40:43]\n"
MF "v[40:43],a[200:203],a[72:75],v[40:43]\n"
"s_add_u32 s84,s14,40960\n"
MF "v[48:51],a[200:203],a[76:79],v[48:51]\n"
"s_add_u32 m0,s63,4096\n"
MF "v[48:51],a[204:207],a[116:119],v[48:51]\n"
"s_waitcnt lgkmcnt(4)\n"
LD "v4,s[24:27],s84 offen lds\n"
MF "v[52:55],a[212:215],a[116:119],v[52:55]\n"
TR "a[200:201],v1 offset:8\n"
MF "v[52:55],a[208:211],a[76:79],v[52:55]\n"
TR "a[202:203],v1 offset:520\n"
MF "v[44:47],a[208:211],a[72:75],v[44:47]\n"
MF "v[44:47],a[212:215],a[112:115],v[44:47]\n"
TR "a[204:205],v1 offset:4104\n"
MF "v[36:39],a[212:215],a[108:111],v[36:39]\n"
TR "a[206:207],v1 offset:4616\n"
MF "v[36:39],a[208:211],a[68:71],v[36:39]\n"
MF "v[28:31],a[208:211],a[64:67],v[28:31]\n"
MF "v[28:31],a[212:215],a[104:107],v[28:31]\n"
MF "v[20:23],a[212:215],a[100:103],v[20:23]\n"
"s_waitcnt vmcnt(24) lgkmcnt(0)\n"
MF "v[20:23],a[208:211],a[60:63],v[20:23]\n"
"s_barrier\n"
MF "v[56:59],a[216:219],a[60:63],v[56:59]\n"
"s_add_u32 m0,s62,0\n"
"s_add_u32 s82,s66,4718592\n"
TR "a[208:209],v0 offset:16384\n"
MF "v[56:59],a[220:223],a[100:103],v[56:59]\n"
"s_sub_u32 s15,s82,7864320\n"
"s_min_u32 s82,s82,s15\n"
TR "a[210:211],v0 offset:16896\n"
MF "v[64:67],a[220:223],a[104:107],v[64:67]\n"
LD "v6,s[28:31],s82 offen lds\n"
MF "v[64:67],a[216:219],a[64:67],v[64:67]\n"
TR "a[212:213],v0 offset:20480\n"
MF "v[72:75],a[216:219],a[68:71],v[72:75]\n"
TR "a[214:215],v0 offset:20992\n"
MF "v[72:75],a[220:223],a[108:111],v[72:75]\n"
MF "v[80:83],a[220:223],a[112:115],v[80:83]\n"
LD "v11,s[28:31],s82 offen offset:1024 lds\n"
MF "v[80:83],a[216:219],a[72:75],v[80:83]\n"
MF "v[88:91],a[216:219],a[76:79],v[88:91]\n"
MF "v[88:91],a[220:223],a[116:119],v[88:91]\n"
MF "v[92:95],a[204:207],a[116:119],v[92:95]\n"
TR "a[216:217],v0 offset:16392\n"
MF "v[92:95],a[200:203],a[76:79],v[92:95]\n"
TR "a[218:219],v0 offset:16904\n"
MF "v[84:87],a[200:203],a[72:75],v[84:87]\n"
LD "v12,s[28:31],s82 offen offset:2048 lds\n"
MF "v[84:87],a[204:207],a[112:115],v[84:87]\n"
TR "a[220:221],v0 offset:20488\n"
MF "v[76:79],a[204:207],a[108:111],v[76:79]\n"
TR "a[222:223],v0 offset:21000\n"
MF "v[76:79],a[200:203],a[68:71],v[76:79]\n"
MF "v[68:71],a[200:203],a[64:67],v[68:71]\n"
MF "v[68:71],a[204:207],a[104:107],v[68:71]\n"
MF "v[60:63],a[204:207],a[100:103],v[60:63]\n"
LD "v13,s[28:31],s82 offen offset:3072 lds\n"
MF "v[60:63],a[200:203],a[60:63],v[60:63]\n"
"s_waitcnt lgkmcnt(4)\n"
MF "v[96:99],a[208:211],a[60:63],v[96:99]\n"
TR "a[200:201],v1 offset:16384\n"
MF "v[96:99],a[212:215],a[100:103],v[96:99]\n"
TR "a[202:203],v1 offset:16896\n"
MF "v[104:107],a[212:215],a[104:107],v[104:107]\n"
"s_add_u32 s14,s81,896\n"
"s_sub_u32 s15,s14,1280\n"
MF "v[104:107],a[208:211],a[64:67],v[104:107]\n"
"s_min_u32 s14,s14,s15\n"
"s_add_u32 s14,s14,s68\n"
TR "a[204:205],v1 offset:20480\n"
MF "v[112:115],a[208:211],a[68:71],v[112:115]\n"
"s_add_u32 s84,s14,0\n"
"s_add_u32 m0,s63,20480\n"
TR "a[206:207],v1 offset:20992\n"
MF "v[112:115],a[212:215],a[108:111],v[112:115]\n"
LD "v4,s[24:27],s84 offen lds\n"
MF "v[120:123],a[212:215],a[112:115],v[120:123]\n"
MF "v[120:123],a[208:211],a[72:75],v[120:123]\n"
MF "v[128:131],a[208:211],a[76:79],v[128:131]\n"
"s_add_u32 s84,s14,9216\n"
MF "v[128:131],a[212:215],a[116:119],v[128:131]\n"
"s_waitcnt lgkmcnt(4)\n"
LD "v5,s[24:27],s84 offen offset:1024 lds\n"
MF "v[132:135],a[220:223],a[116:119],v[132:135]\n"
TR "a[208:209],v1 offset:16392\n"
MF "v[132:135],a[216:219],a[76:79],v[132:135]\n"
TR "a[210:211],v1 offset:16904\n"
MF "v[124:127],a[216:219],a[72:75],v[124:127]\n"
MF "v[124:127],a[220:223],a[112:115],v[124:127]\n"
TR "a[212:213],v1 offset:20488\n"
MF "v[116:119],a[220:223],a[108:111],v[116:119]\n"
TR "a[214:215],v1 offset:21000\n"
MF "v[116:119],a[216:219],a[68:71],v[116:119]\n"
MF "v[108:111],a[216:219],a[64:67],v[108:111]\n"
MF "v[108:111],a[220:223],a[104:107],v[108:111]\n"
MF "v[100:103],a[220:223],a[100:103],v[100:103]\n"
"s_waitcnt vmcnt(23) lgkmcnt(0)\n"
MF "v[100:103],a[216:219],a[60:63],v[100:103]\n"
"s_barrier\n"
MF "v[136:139],a[200:203],a[60:63],v[136:139]\n"
TR "a[216:217],v0 offset:32768\n"
MF "v[136:139],a[204:207],a[100:103],v[136:139]\n"
"s_add_u32 m0,s62,16384\n"
TR "a[218:219],v0 offset:33280\n"
MF "v[144:147],a[204:207],a[104:107],v[144:147]\n"
"s_add_u32 s82,s66,4718720\n"
"ds_read_b128 a[140:143],v2 offset:40960\n"
MF "v[144:147],a[200:203],a[64:67],v[144:147]\n"
"s_sub_u32 s15,s82,7864320\n"
TR "a[220:221],v0 offset:36864\n"
MF "v[152:155],a[200:203],a[68:71],v[152:155]\n"
"s_min_u32 s82,s82,s15\n"
TR "a[222:223],v0 offset:37376\n"
MF "v[152:155],a[204:207],a[108:111],v[152:155]\n"
LD "v6,s[28:31],s82 offen lds\n"
MF "v[160:163],a[204:207],a[112:115],v[160:163]\n"
"ds_read_b128 a[180:183],v3 offset:40960\n"
MF "v[160:163],a[200:203],a[72:75],v[160:163]\n"
LD "v11,s[28:31],s82 offen offset:1024 lds\n"
MF "v[168:171],a[200:203],a[76:79],v[168:171]\n"
MF "v[168:171],a[204:207],a[116:119],v[168:171]\n"
LD "v12,s[28:31],s82 offen offset:2048 lds\n"
MF "v[172:175],a[212:215],a[116:119],v[172:175]\n"
TR "a[200:201],v0 offset:32776\n"
MF "v[172:175],a[208:211],a[76:79],v[172:175]\n"
TR "a[202:203],v0 offset:33288\n"
MF "v[164:167],a[208:211],a[72:75],v[164:167]\n"
"ds_read_b128 a[144:147],v2 offset:43008\n"
MF "v[164:167],a[212:215],a[112:115],v[164:167]\n"
TR "a[204:205],v0 offset:36872\n"
MF "v[156:159],a[212:215],a[108:111],v[156:159]\n"
TR "a[206:207],v0 offset:37384\n"
MF "v[156:159],a[208:211],a[68:71],v[156:159]\n"
MF "v[148:151],a[208:211],a[64:67],v[148:151]\n"
"ds_read_b128 a[184:187],v3 offset:43008\n"
MF "v[148:151],a[212:215],a[104:107],v[148:151]\n"
MF "v[140:143],a[212:215],a[100:103],v[140:143]\n"
LD "v13,s[28:31],s82 offen offset:3072 lds\n"
MF "v[140:143],a[208:211],a[60:63],v[140:143]\n"
"s_waitcnt lgkmcnt(7)\n"
MF "v[176:179],a[216:219],a[60:63],v[176:179]\n"
TR "a[208:209],v1 offset:32768\n"
MF "v[176:179],a[220:223],a[100:103],v[176:179]\n"
TR "a[210:211],v1 offset:33280\n"
MF "v[184:187],a[220:223],a[104:107],v[184:187]\n"
"ds_read_b128 a[148:151],v2 offset:45056\n"
MF "v[184:187],a[216:219],a[64:67],v[184:187]\n"
"s_add_u32 s84,s14,20480\n"
TR "a[212:213],v1 offset:36864\n"
MF "v[192:195],a[216:219],a[68:71],v[192:195]\n"
"s_add_u32 m0,s63,22528\n"
TR "a[214:215],v1 offset:37376\n"
MF "v[192:195],a[220:223],a[108:111],v[192:195]\n"
LD "v4,s[24:27],s84 offen lds\n"
MF "v[200:203],a[220:223],a[112:115],v[200:203]\n"
"ds_read_b128 a[188:191],v3 offset:45056\n"
MF "v[200:203],a[216:219],a[72:75],v[200:203]\n"
MF "v[208:211],a[216:219],a[76:79],v[208:211]\n"
"s_add_u32 s84,s14,29696\n"
MF "v[208:211],a[220:223],a[116:119],v[208:211]\n"
"s_waitcnt lgkmcnt(7)\n"
LD "v5,s[24:27],s84 offen offset:1024 lds\n"
MF "v[212:215],a[204:207],a[116:119],v[212:215]\n"
TR "a[216:217],v1 offset:32776\n"
MF "v[212:215],a[200:203],a[76:79],v[212:215]\n"
TR "a[218:219],v1 offset:33288\n"
MF "v[204:207],a[200:203],a[72:75],v[204:207]\n"
"ds_read_b128 a[152:155],v2 offset:47104\n"
MF "v[204:207],a[204:207],a[112:115],v[204:207]\n"
TR "a[220:221],v1 offset:36872\n"
MF "v[196:199],a[204:207],a[108:111],v[196:199]\n"
TR "a[222:223],v1 offset:37384\n"
MF "v[196:199],a[200:203],a[68:71],v[196:199]\n"
MF "v[188:191],a[200:203],a[64:67],v[188:191]\n"
"v_add_u32 v0,0xc000,v0\n"
MF "v[188:191],a[204:207],a[104:107],v[188:191]\n"
"v_add_u32 v1,0xc000,v1\n"
MF "v[180:183],a[204:207],a[100:103],v[180:183]\n"
"s_waitcnt vmcnt(25) lgkmcnt(0)\n"
MF "v[180:183],a[200:203],a[60:63],v[180:183]\n"
"s_barrier\n"
MF "v[216:219],a[208:211],a[60:63],v[216:219]\n"
TR "a[200:201],v0\n"
MF "v[216:219],a[212:215],a[100:103],v[216:219]\n"
"s_add_u32 m0,s62,32768\n"
TR "a[202:203],v0 offset:512\n"
MF "v[224:227],a[212:215],a[104:107],v[224:227]\n"
"s_add_u32 s82,s66,4718848\n"
"ds_read_b128 a[192:195],v3 offset:47104\n"
MF "v[224:227],a[208:211],a[64:67],v[224:227]\n"
"s_sub_u32 s15,s82,7864320\n"
TR "a[204:205],v0 offset:4096\n"
MF "v[232:235],a[208:211],a[68:71],v[232:235]\n"
"s_min_u32 s82,s82,s15\n"
TR "a[206:207],v0 offset:4608\n"
MF "v[232:235],a[212:215],a[108:111],v[232:235]\n"
LD "v6,s[28:31],s82 offen lds\n"
MF "v[240:243],a[212:215],a[112:115],v[240:243]\n"
"ds_read_b128 a[156:159],v2 offset:49152\n"
MF "v[240:243],a[208:211],a[72:75],v[240:243]\n"
"ds_read_b128 a[196:199],v3 offset:49152\n"
MF "v[248:251],a[208:211],a[76:79],v[248:251]\n"
LD "v11,s[28:31],s82 offen offset:1024 lds\n"
MF "v[248:251],a[212:215],a[116:119],v[248:251]\n"
MF "v[252:255],a[220:223],a[116:119],v[252:255]\n"
TR "a[208:209],v0 offset:8\n"
MF "v[252:255],a[216:219],a[76:79],v[252:255]\n"
TR "a[210:211],v0 offset:520\n"
MF "v[244:247],a[216:219],a[72:75],v[244:247]\n"
MF "v[244:247],a[220:223],a[112:115],v[244:247]\n"
TR "a[212:213],v0 offset:4104\n"
MF "v[236:239],a[220:223],a[108:111],v[236:239]\n"
TR "a[214:215],v0 offset:4616\n"
MF "v[236:239],a[216:219],a[68:71],v[236:239]\n"
LD "v12,s[28:31],s82 offen offset:2048 lds\n"
MF "v[228:231],a[216:219],a[64:67],v[228:231]\n"
MF "v[228:231],a[220:223],a[104:107],v[228:231]\n"
MF "v[220:223],a[220:223],a[100:103],v[220:223]\n"
MF "v[220:223],a[216:219],a[60:63],v[220:223]\n"
"s_waitcnt lgkmcnt(4)\n"
MF "v[16:19],a[200:203],a[140:143],v[16:19]\n"
TR "a[216:217],v1\n"
MF "v[16:19],a[204:207],a[180:183],v[16:19]\n"
TR "a[218:219],v1 offset:512\n"
MF "v[24:27],a[204:207],a[184:187],v[24:27]\n"
LD "v13,s[28:31],s82 offen offset:3072 lds\n"
MF "v[24:27],a[200:203],a[144:147],v[24:27]\n"
TR "a[220:221],v1 offset:4096\n"
MF "v[32:35],a[200:203],a[148:151],v[32:35]\n"
TR "a[222:223],v1 offset:4608\n"
MF "v[32:35],a[204:207],a[188:191],v[32:35]\n"
MF "v[40:43],a[204:207],a[192:195],v[40:43]\n"
MF "v[40:43],a[200:203],a[152:155],v[40:43]\n"
"s_add_u32 s84,s14,40960\n"
MF "v[48:51],a[200:203],a[156:159],v[48:51]\n"
"s_add_u32 m0,s63,24576\n"
MF "v[48:51],a[204:207],a[196:199],v[48:51]\n"
"s_waitcnt lgkmcnt(4)\n"
LD "v4,s[24:27],s84 offen lds\n"
MF "v[52:55],a[212:215],a[196:199],v[52:55]\n"
TR "a[200:201],v1 offset:8\n"
MF "v[52:55],a[208:211],a[156:159],v[52:55]\n"
TR "a[202:203],v1 offset:520\n"
MF "v[44:47],a[208:211],a[152:155],v[44:47]\n"
MF "v[44:47],a[212:215],a[192:195],v[44:47]\n"
TR "a[204:205],v1 offset:4104\n"
MF "v[36:39],a[212:215],a[188:191],v[36:39]\n"
TR "a[206:207],v1 offset:4616\n"
MF "v[36:39],a[208:211],a[148:151],v[36:39]\n"
MF "v[28:31],a[208:211],a[144:147],v[28:31]\n"
MF "v[28:31],a[212:215],a[184:187],v[28:31]\n"
MF "v[20:23],a[212:215],a[180:183],v[20:23]\n"
".Lpg6c:\n"
"s_cbranch_execz .Lpg1a\n"
".Lpg6f:\n"
"s_cbranch_execz .Lpgt0k_f\n"
"s_waitcnt vmcnt(24) lgkmcnt(0)\n"
MF "v[20:23],a[208:211],a[140:143],v[20:23]\n"
"s_barrier\n"
MF "v[56:59],a[216:219],a[140:143],v[56:59]\n"
"s_add_u32 m0,s62,49152\n"
"s_add_u32 s82,s66,5505024\n"
TR "a[208:209],v0 offset:16384\n"
MF "v[56:59],a[220:223],a[180:183],v[56:59]\n"
"s_sub_u32 s15,s82,7864320\n"
"s_min_u32 s82,s82,s15\n"
TR "a[210:211],v0 offset:16896\n"
MF "v[64:67],a[220:223],a[184:187],v[64:67]\n"
LD "v6,s[28:31],s82 offen lds\n"
MF "v[64:67],a[216:219],a[144:147],v[64:67]\n"
TR "a[212:213],v0 offset:20480\n"
MF "v[72:75],a[216:219],a[148:151],v[72:75]\n"
TR "a[214:215],v0 offset:20992\n"
MF "v[72:75],a[220:223],a[188:191],v[72:75]\n"
MF "v[80:83],a[220:223],a[192:195],v[80:83]\n"
LD "v11,s[28:31],s82 offen offset:1024 lds\n"
MF "v[80:83],a[216:219],a[152:155],v[80:83]\n"
MF "v[88:91],a[216:219],a[156:159],v[88:91]\n"
MF "v[88:91],a[220:223],a[196:199],v[88:91]\n"
MF "v[92:95],a[204:207],a[196:199],v[92:95]\n"
TR "a[216:217],v0 offset:16392\n"
MF "v[92:95],a[200:203],a[156:159],v[92:95]\n"
TR "a[218:219],v0 offset:16904\n"
MF "v[84:87],a[200:203],a[152:155],v[84:87]\n"
LD "v12,s[28:31],s82 offen offset:2048 lds\n"
MF "v[84:87],a[204:207],a[192:195],v[84:87]\n"
TR "a[220:221],v0 offset:20488\n"
MF "v[76:79],a[204:207],a[188:191],v[76:79]\n"
TR "a[222:223],v0 offset:21000\n"
MF "v[76:79],a[200:203],a[148:151],v[76:79]\n"
MF "v[68:71],a[200:203],a[144:147],v[68:71]\n"
MF "v[68:71],a[204:207],a[184:187],v[68:71]\n"
MF "v[60:63],a[204:207],a[180:183],v[60:63]\n"
LD "v13,s[28:31],s82 offen offset:3072 lds\n"
MF "v[60:63],a[200:203],a[140:143],v[60:63]\n"
"s_waitcnt lgkmcnt(4)\n"
MF "v[96:99],a[208:211],a[140:143],v[96:99]\n"
TR "a[200:201],v1 offset:16384\n"
MF "v[96:99],a[212:215],a[180:183],v[96:99]\n"
TR "a[202:203],v1 offset:16896\n"
MF "v[104:107],a[212:215],a[184:187],v[104:107]\n"
"s_add_u32 s14,s81,1024\n"
"s_sub_u32 s15,s14,1280\n"
MF "v[104:107],a[208:211],a[144:147],v[104:107]\n"
"s_min_u32 s14,s14,s15\n"
"s_add_u32 s14,s14,s68\n"
TR "a[204:205],v1 offset:20480\n"
MF "v[112:115],a[208:211],a[148:151],v[112:115]\n"
"s_add_u32 s84,s14,0\n"
"s_add_u32 m0,s63,40960\n"
TR "a[206:207],v1 offset:20992\n"
MF "v[112:115],a[212:215],a[188:191],v[112:115]\n"
LD "v4,s[24:27],s84 offen lds\n"
MF "v[120:123],a[212:215],a[192:195],v[120:123]\n"
MF "v[120:123],a[208:211],a[152:155],v[120:123]\n"
MF "v[128:131],a[208:211],a[156:159],v[128:131]\n"
"s_add_u32 s84,s14,9216\n"
MF "v[128:131],a[212:215],a[196:199],v[128:131]\n"
"s_waitcnt lgkmcnt(4)\n"
LD "v5,s[24:27],s84 offen offset:1024 lds\n"
MF "v[132:135],a[220:223],a[196:199],v[132:135]\n"
TR "a[208:209],v1 offset:16392\n"
MF "v[132:135],a[216:219],a[156:159],v[132:135]\n"
TR "a[210:211],v1 offset:16904\n"
MF "v[124:127],a[216:219],a[152:155],v[124:127]\n"
MF "v[124:127],a[220:223],a[192:195],v[124:127]\n"
TR "a[212:213],v1 offset:20488\n"
MF "v[116:119],a[220:223],a[188:191],v[116:119]\n"
TR "a[214:215],v1 offset:21000\n"
MF "v[116:119],a[216:219],a[148:151],v[116:119]\n"
MF "v[108:111],a[216:219],a[144:147],v[108:111]\n"
MF "v[108:111],a[220:223],a[184:187],v[108:111]\n"
MF "v[100:103],a[220:223],a[180:183],v[100:103]\n"
"s_waitcnt vmcnt(23) lgkmcnt(0)\n"
MF "v[100:103],a[216:219],a[140:143],v[100:103]\n"
"s_barrier\n"
"s_cmp_eq_u32 s57,1\n"
"s_cbranch_scc0 .Lhs12\n"
"v_bfe_u32 v14,v9,4,5\n"
"v_lshlrev_b32 v14,6,v14\n"
"s_lshl_b32 s58,s78,2\n"
"s_add_u32 s58,s58,s86\n"
"buffer_load_dword v10,v14,s[40:43],s58 offen offset:128 sc1\n"
".Lhs12:\n"
MF "v[136:139],a[200:203],a[140:143],v[136:139]\n"
TR "a[216:217],v0 offset:32768\n"
MF "v[136:139],a[204:207],a[180:183],v[136:139]\n"
"s_add_u32 m0,s62,65536\n"
TR "a[218:219],v0 offset:33280\n"
MF "v[144:147],a[204:207],a[184:187],v[144:147]\n"
"s_add_u32 s82,s66,5505152\n"
"ds_read_b128 a[60:63],v2\n"
MF "v[144:147],a[200:203],a[144:147],v[144:147]\n"
"s_sub_u32 s15,s82,7864320\n"
TR "a[220:221],v0 offset:36864\n"
MF "v[152:155],a[200:203],a[148:151],v[152:155]\n"
"s_min_u32 s82,s82,s15\n"
TR "a[222:223],v0 offset:37376\n"
MF "v[152:155],a[204:207],a[188:191],v[152:155]\n"
LD "v6,s[28:31],s82 offen lds\n"
MF "v[160:163],a[204:207],a[192:195],v[160:163]\n"
"ds_read_b128 a[100:103],v3\n"
MF "v[160:163],a[200:203],a[152:155],v[160:163]\n"
LD "v11,s[28:31],s82 offen offset:1024 lds\n"
MF "v[168:171],a[200:203],a[156:159],v[168:171]\n"
MF "v[168:171],a[204:207],a[196:199],v[168:171]\n"
LD "v12,s[28:31],s82 offen offset:2048 lds\n"
MF "v[172:175],a[212:215],a[196:199],v[172:175]\n"
TR "a[200:201],v0 offset:32776\n"
MF "v[172:175],a[208:211],a[156:159],v[172:175]\n"
TR "a[202:203],v0 offset:33288\n"
MF "v[164:167],a[208:211],a[152:155],v[164:167]\n"
"ds_read_b128 a[64:67],v2 offset:2048\n"
MF "v[164:167],a[212:215],a[192:195],v[164:167]\n"
TR "a[204:205],v0 offset:36872\n"
MF "v[156:159],a[212:215],a[188:191],v[156:159]\n"
TR "a[206:207],v0 offset:37384\n"
MF "v[156:159],a[208:211],a[148:151],v[156:159]\n"
MF "v[148:151],a[208:211],a[144:147],v[148:151]\n"
"ds_read_b128 a[104:107],v3 offset:2048\n"
MF "v[148:151],a[212:215],a[184:187],v[148:151]\n"
MF "v[140:143],a[212:215],a[180:183],v[140:143]\n"
LD "v13,s[28:31],s82 offen offset:3072 lds\n"
MF "v[140:143],a[208:211],a[140:143],v[140:143]\n"
"s_waitcnt lgkmcnt(7)\n"
MF "v[176:179],a[216:219],a[140:143],v[176:179]\n"
TR "a[208:209],v1 offset:32768\n"
MF "v[176:179],a[220:223],a[180:183],v[176:179]\n"
TR "a[210:211],v1 offset:33280\n"
MF "v[184:187],a[220:223],a[184:187],v[184:187]\n"
"ds_read_b128 a[68:71],v2 offset:4096\n"
MF "v[184:187],a[216:219],a[144:147],v[184:187]\n"
"s_add_u32 s84,s14,20480\n"
TR "a[212:213],v1 offset:36864\n"
MF "v[192:195],a[216:219],a[148:151],v[192:195]\n"
"s_add_u32 m0,s63,43008\n"
TR "a[214:215],v1 offset:37376\n"
MF "v[192:195],a[220:223],a[188:191],v[192:195]\n"
LD "v4,s[24:27],s84 offen lds\n"
MF "v[200:203],a[220:223],a[192:195],v[200:203]\n"
"ds_read_b128 a[108:111],v3 offset:4096\n"
MF "v[200:203],a[216:219],a[152:155],v[200:203]\n"
MF "v[208:211],a[216:219],a[156:159],v[208:211]\n"
"s_add_u32 s84,s14,29696\n"
MF "v[208:211],a[220:223],a[196:199],v[208:211]\n"
"s_waitcnt lgkmcnt(7)\n"
LD "v5,s[24:27],s84 offen offset:1024 lds\n"
MF "v[212:215],a[204:207],a[196:199],v[212:215]\n"
TR "a[216:217],v1 offset:32776\n"
MF "v[212:215],a[200:203],a[156:159],v[212:215]\n"
TR "a[218:219],v1 offset:33288\n"
MF "v[204:207],a[200:203],a[152:155],v[204:207]\n"
"ds_read_b128 a[72:75],v2 offset:6144\n"
MF "v[204:207],a[204:207],a[192:195],v[204:207]\n"
TR "a[220:221],v1 offset:36872\n"
MF "v[196:199],a[204:207],a[188:191],v[196:199]\n"
TR "a[222:223],v1 offset:37384\n"
MF "v[196:199],a[200:203],a[148:151],v[196:199]\n"
MF "v[188:191],a[200:203],a[144:147],v[188:191]\n"
"v_add_u32 v0,0xffff4000,v0\n"
MF "v[188:191],a[204:207],a[184:187],v[188:191]\n"
"v_add_u32 v1,0xffff4000,v1\n"
MF "v[180:183],a[204:207],a[180:183],v[180:183]\n"
"s_waitcnt vmcnt(25) lgkmcnt(0)\n"
MF "v[180:183],a[200:203],a[140:143],v[180:183]\n"
"s_barrier\n"
MF "v[216:219],a[208:211],a[140:143],v[216:219]\n"
TR "a[200:201],v0\n"
MF "v[216:219],a[212:215],a[180:183],v[216:219]\n"
"s_add_u32 m0,s62,81920\n"
TR "a[202:203],v0 offset:512\n"
MF "v[224:227],a[212:215],a[184:187],v[224:227]\n"
"s_add_u32 s82,s66,5505280\n"
"ds_read_b128 a[112:115],v3 offset:6144\n"
MF "v[224:227],a[208:211],a[144:147],v[224:227]\n"
"s_sub_u32 s15,s82,7864320\n"
TR "a[204:205],v0 offset:4096\n"
MF "v[232:235],a[208:211],a[148:151],v[232:235]\n"
"s_min_u32 s82,s82,s15\n"
TR "a[206:207],v0 offset:4608\n"
MF "v[232:235],a[212:215],a[188:191],v[232:235]\n"
LD "v6,s[28:31],s82 offen lds\n"
MF "v[240:243],a[212:215],a[192:195],v[240:243]\n"
"ds_read_b128 a[76:79],v2 offset:8192\n"
MF "v[240:243],a[208:211],a[152:155],v[240:243]\n"
"ds_read_b128 a[116:119],v3 offset:8192\n"
MF "v[248:251],a[208:211],a[156:159],v[248:251]\n"
LD "v11,s[28:31],s82 offen offset:1024 lds\n"
MF "v[248:251],a[212:215],a[196:199],v[248:251]\n"
MF "v[252:255],a[220:223],a[196:199],v[252:255]\n"
TR "a[208:209],v0 offset:8\n"
MF "v[252:255],a[216:219],a[156:159],v[252:255]\n"
TR "a[210:211],v0 offset:520\n"
MF "v[244:247],a[216:219],a[152:155],v[244:247]\n"
MF "v[244:247],a[220:223],a[192:195],v[244:247]\n"
TR "a[212:213],v0 offset:4104\n"
MF "v[236:239],a[220:223],a[188:191],v[236:239]\n"
TR "a[214:215],v0 offset:4616\n"
MF "v[236:239],a[216:219],a[148:151],v[236:239]\n"
LD "v12,s[28:31],s82 offen offset:2048 lds\n"
MF "v[228:231],a[216:219],a[144:147],v[228:231]\n"
MF "v[228:231],a[220:223],a[184:187],v[228:231]\n"
MF "v[220:223],a[220:223],a[180:183],v[220:223]\n"
MF "v[220:223],a[216:219],a[140:143],v[220:223]\n"
"s_waitcnt lgkmcnt(4)\n"
MF "v[16:19],a[200:203],a[60:63],v[16:19]\n"
TR "a[216:217],v1\n"
MF "v[16:19],a[204:207],a[100:103],v[16:19]\n"
TR "a[218:219],v1 offset:512\n"
MF "v[24:27],a[204:207],a[104:107],v[24:27]\n"
LD "v13,s[28:31],s82 offen offset:3072 lds\n"
MF "v[24:27],a[200:203],a[64:67],v[24:27]\n"
TR "a[220:221],v1 offset:4096\n"
MF "v[32:35],a[200:203],a[68:71],v[32:35]\n"
TR "a[222:223],v1 offset:4608\n"
MF "v[32:35],a[204:207],a[108:111],v[32:35]\n"
MF "v[40:43],a[204:207],a[112:115],v[40:43]\n"
MF "v[40:43],a[200:203],a[72:75],v[40:43]\n"
"s_add_u32 s84,s14,40960\n"
MF "v[48:51],a[200:203],a[76:79],v[48:51]\n"
"s_add_u32 m0,s63,45056\n"
MF "v[48:51],a[204:207],a[116:119],v[48:51]\n"
"s_waitcnt lgkmcnt(4)\n"
LD "v4,s[24:27],s84 offen lds\n"
MF "v[52:55],a[212:215],a[116:119],v[52:55]\n"
TR "a[200:201],v1 offset:8\n"
MF "v[52:55],a[208:211],a[76:79],v[52:55]\n"
TR "a[202:203],v1 offset:520\n"
MF "v[44:47],a[208:211],a[72:75],v[44:47]\n"
MF "v[44:47],a[212:215],a[112:115],v[44:47]\n"
TR "a[204:205],v1 offset:4104\n"
MF "v[36:39],a[212:215],a[108:111],v[36:39]\n"
TR "a[206:207],v1 offset:4616\n"
MF "v[36:39],a[208:211],a[68:71],v[36:39]\n"
MF "v[28:31],a[208:211],a[64:67],v[28:31]\n"
MF "v[28:31],a[212:215],a[104:107],v[28:31]\n"
MF "v[20:23],a[212:215],a[100:103],v[20:23]\n"
"s_waitcnt vmcnt(24) lgkmcnt(0)\n"
MF "v[20:23],a[208:211],a[60:63],v[20:23]\n"
"s_barrier\n"
MF "v[56:59],a[216:219],a[60:63],v[56:59]\n"
"s_add_u32 m0,s62,0\n"
"s_add_u32 s82,s66,6291712\n"
TR "a[208:209],v0 offset:16384\n"
MF "v[56:59],a[220:223],a[100:103],v[56:59]\n"
"s_sub_u32 s15,s82,7864320\n"
"s_min_u32 s82,s82,s15\n"
TR "a[210:211],v0 offset:16896\n"
MF "v[64:67],a[220:223],a[104:107],v[64:67]\n"
LD "v6,s[28:31],s82 offen lds\n"
MF "v[64:67],a[216:219],a[64:67],v[64:67]\n"
TR "a[212:213],v0 offset:20480\n"
MF "v[72:75],a[216:219],a[68:71],v[72:75]\n"
TR "a[214:215],v0 offset:20992\n"
MF "v[72:75],a[220:223],a[108:111],v[72:75]\n"
MF "v[80:83],a[220:223],a[112:115],v[80:83]\n"
LD "v11,s[28:31],s82 offen offset:1024 lds\n"
MF "v[80:83],a[216:219],a[72:75],v[80:83]\n"
MF "v[88:91],a[216:219],a[76:79],v[88:91]\n"
MF "v[88:91],a[220:223],a[116:119],v[88:91]\n"
MF "v[92:95],a[204:207],a[116:119],v[92:95]\n"
TR "a[216:217],v0 offset:16392\n"
MF "v[92:95],a[200:203],a[76:79],v[92:95]\n"
TR "a[218:219],v0 offset:16904\n"
MF "v[84:87],a[200:203],a[72:75],v[84:87]\n"
LD "v12,s[28:31],s82 offen offset:2048 lds\n"
MF "v[84:87],a[204:207],a[112:115],v[84:87]\n"
TR "a[220:221],v0 offset:20488\n"
MF "v[76:79],a[204:207],a[108:111],v[76:79]\n"
TR "a[222:223],v0 offset:21000\n"
MF "v[76:79],a[200:203],a[68:71],v[76:79]\n"
MF "v[68:71],a[200:203],a[64:67],v[68:71]\n"
LD "v13,s[28:31],s82 offen offset:3072 lds\n"
MF "v[68:71],a[204:207],a[104:107],v[68:71]\n"
MF "v[60:63],a[204:207],a[100:103],v[60:63]\n"
MF "v[60:63],a[200:203],a[60:63],v[60:63]\n"
"s_add_u32 s14,s81,1152\n"
"s_sub_u32 s15,s14,1280\n"
"s_waitcnt lgkmcnt(4)\n"
MF "v[96:99],a[208:211],a[60:63],v[96:99]\n"
"s_min_u32 s14,s14,s15\n"
"s_add_u32 s14,s14,s68\n"
TR "a[200:201],v1 offset:16384\n"
MF "v[96:99],a[212:215],a[100:103],v[96:99]\n"
"s_add_u32 s84,s14,0\n"
"s_add_u32 m0,s63,0\n"
TR "a[202:203],v1 offset:16896\n"
MF "v[104:107],a[212:215],a[104:107],v[104:107]\n"
LD "v4,s[24:27],s84 offen lds\n"
MF "v[104:107],a[208:211],a[64:67],v[104:107]\n"
TR "a[204:205],v1 offset:20480\n"
MF "v[112:115],a[208:211],a[68:71],v[112:115]\n"
TR "a[206:207],v1 offset:20992\n"
MF "v[112:115],a[212:215],a[108:111],v[112:115]\n"
"s_add_u32 s84,s14,9216\n"
MF "v[120:123],a[212:215],a[112:115],v[120:123]\n"
LD "v5,s[24:27],s84 offen offset:1024 lds\n"
MF "v[120:123],a[208:211],a[72:75],v[120:123]\n"
MF "v[128:131],a[208:211],a[76:79],v[128:131]\n"
MF "v[128:131],a[212:215],a[116:119],v[128:131]\n"
"s_waitcnt lgkmcnt(4)\n"
MF "v[132:135],a[220:223],a[116:119],v[132:135]\n"
TR "a[208:209],v1 offset:16392\n"
MF "v[132:135],a[216:219],a[76:79],v[132:135]\n"
"s_add_u32 s84,s14,18432\n"
TR "a[210:211],v1 offset:16904\n"
MF "v[124:127],a[216:219],a[72:75],v[124:127]\n"
LD "v4,s[24:27],s84 offen offset:2048 lds\n"
MF "v[124:127],a[220:223],a[112:115],v[124:127]\n"
TR "a[212:213],v1 offset:20488\n"
MF "v[116:119],a[220:223],a[108:111],v[116:119]\n"
TR "a[214:215],v1 offset:21000\n"
MF "v[116:119],a[216:219],a[68:71],v[116:119]\n"
MF "v[108:111],a[216:219],a[64:67],v[108:111]\n"
MF "v[108:111],a[220:223],a[104:107],v[108:111]\n"
MF "v[100:103],a[220:223],a[100:103],v[100:103]\n"
"s_waitcnt vmcnt(24) lgkmcnt(0)\n"
MF "v[100:103],a[216:219],a[60:63],v[100:103]\n"
"s_barrier\n"
MF "v[136:139],a[200:203],a[60:63],v[136:139]\n"
TR "a[216:217],v0 offset:32768\n"
MF "v[136:139],a[204:207],a[100:103],v[136:139]\n"
"s_add_u32 m0,s62,16384\n"
TR "a[218:219],v0 offset:33280\n"
MF "v[144:147],a[204:207],a[104:107],v[144:147]\n"
"s_add_u32 s82,s66,7078144\n"
"ds_read_b128 a[140:143],v2 offset:20480\n"
MF "v[144:147],a[200:203],a[64:67],v[144:147]\n"
"s_sub_u32 s15,s82,7864320\n"
TR "a[220:221],v0 offset:36864\n"
MF "v[152:155],a[200:203],a[68:71],v[152:155]\n"
"s_min_u32 s82,s82,s15\n"
TR "a[222:223],v0 offset:37376\n"
MF "v[152:155],a[204:207],a[108:111],v[152:155]\n"
LD "v6,s[28:31],s82 offen lds\n"
MF "v[160:163],a[204:207],a[112:115],v[160:163]\n"
"ds_read_b128 a[180:183],v3 offset:20480\n"
MF "v[160:163],a[200:203],a[72:75],v[160:163]\n"
LD "v11,s[28:31],s82 offen offset:1024 lds\n"
MF "v[168:171],a[200:203],a[76:79],v[168:171]\n"
MF "v[168:171],a[204:207],a[116:119],v[168:171]\n"
LD "v12,s[28:31],s82 offen offset:2048 lds\n"
MF "v[172:175],a[212:215],a[116:119],v[172:175]\n"
TR "a[200:201],v0 offset:32776\n"
MF "v[172:175],a[208:211],a[76:79],v[172:175]\n"
TR "a[202:203],v0 offset:33288\n"
MF "v[164:167],a[208:211],a[72:75],v[164:167]\n"
"ds_read_b128 a[144:147],v2 offset:22528\n"
MF "v[164:167],a[212:215],a[112:115],v[164:167]\n"
TR "a[204:205],v0 offset:36872\n"
MF "v[156:159],a[212:215],a[108:111],v[156:159]\n"
TR "a[206:207],v0 offset:37384\n"
MF "v[156:159],a[208:211],a[68:71],v[156:159]\n"
MF "v[148:151],a[208:211],a[64:67],v[148:151]\n"
"ds_read_b128 a[184:187],v3 offset:22528\n"
MF "v[148:151],a[212:215],a[104:107],v[148:151]\n"
MF "v[140:143],a[212:215],a[100:103],v[140:143]\n"
LD "v13,s[28:31],s82 offen offset:3072 lds\n"
MF "v[140:143],a[208:211],a[60:63],v[140:143]\n"
"s_waitcnt lgkmcnt(7)\n"
MF "v[176:179],a[216:219],a[60:63],v[176:179]\n"
TR "a[208:209],v1 offset:32768\n"
MF "v[176:179],a[220:223],a[100:103],v[176:179]\n"
TR "a[210:211],v1 offset:33280\n"
MF "v[184:187],a[220:223],a[104:107],v[184:187]\n"
"ds_read_b128 a[148:151],v2 offset:24576\n"
MF "v[184:187],a[216:219],a[64:67],v[184:187]\n"
"s_add_u32 s84,s14,30720\n"
TR "a[212:213],v1 offset:36864\n"
MF "v[192:195],a[216:219],a[68:71],v[192:195]\n"
"s_add_u32 m0,s63,3072\n"
TR "a[214:215],v1 offset:37376\n"
MF "v[192:195],a[220:223],a[108:111],v[192:195]\n"
LD "v5,s[24:27],s84 offen lds\n"
MF "v[200:203],a[220:223],a[112:115],v[200:203]\n"
"ds_read_b128 a[188:191],v3 offset:24576\n"
MF "v[200:203],a[216:219],a[72:75],v[200:203]\n"
MF "v[208:211],a[216:219],a[76:79],v[208:211]\n"
"s_add_u32 s84,s14,39936\n"
MF "v[208:211],a[220:223],a[116:119],v[208:211]\n"
"s_waitcnt lgkmcnt(7)\n"
LD "v4,s[24:27],s84 offen offset:1024 lds\n"
MF "v[212:215],a[204:207],a[116:119],v[212:215]\n"
TR "a[216:217],v1 offset:32776\n"
MF "v[212:215],a[200:203],a[76:79],v[212:215]\n"
TR "a[218:219],v1 offset:33288\n"
MF "v[204:207],a[200:203],a[72:75],v[204:207]\n"
"ds_read_b128 a[152:155],v2 offset:26624\n"
MF "v[204:207],a[204:207],a[112:115],v[204:207]\n"
TR "a[220:221],v1 offset:36872\n"
MF "v[196:199],a[204:207],a[108:111],v[196:199]\n"
TR "a[222:223],v1 offset:37384\n"
MF "v[196:199],a[200:203],a[68:71],v[196:199]\n"
MF "v[188:191],a[200:203],a[64:67],v[188:191]\n"
"v_add_u32 v0,0xc000,v0\n"
MF "v[188:191],a[204:207],a[104:107],v[188:191]\n"
"v_add_u32 v1,0xc000,v1\n"
MF "v[180:183],a[204:207],a[100:103],v[180:183]\n"
"s_waitcnt vmcnt(26) lgkmcnt(0)\n"
".Lpgqj_a:\n"
"s_cbranch_execz .Lisr1\n"
".Lqj:\n"
MF "v[180:183],a[200:203],a[60:63],v[180:183]\n"
"s_barrier\n"
"s_getpc_b64 s[52:53]\n"
".Lctq:\n"
"s_sub_u32 s52,s52,.Lctq-.Lcode0\n"
"s_subb_u32 s53,s53,0\n"
"s_bfe_u32 s58,s23,0x50000\n"
"s_lshl_b32 s58,s58,2\n"
"s_add_u32 s58,s58,s56\n"
"s_nop 0\n"
"s_mul_i32 s58,s58,1280\n"
"v_min_u32 v15,144,v9\n"
"v_lshl_add_u32 v15,v15,3,s58\n"
"v_min_u32 v15,.Lcode1-.Lcode0,v15\n"
"global_load_dword a249,v15,s[52:53]\n"
MF "v[216:219],a[208:211],a[60:63],v[216:219]\n"
TR "a[200:201],v0\n"
MF "v[216:219],a[212:215],a[100:103],v[216:219]\n"
"s_add_u32 m0,s62,32768\n"
TR "a[202:203],v0 offset:512\n"
MF "v[224:227],a[212:215],a[104:107],v[224:227]\n"
"s_add_u32 s82,s66,6291584\n"
"ds_read_b128 a[192:195],v3 offset:26624\n"
MF "v[224:227],a[208:211],a[64:67],v[224:227]\n"
"s_sub_u32 s15,s82,7864320\n"
TR "a[204:205],v0 offset:4096\n"
MF "v[232:235],a[208:211],a[68:71],v[232:235]\n"
"s_min_u32 s82,s82,s15\n"
TR "a[206:207],v0 offset:4608\n"
MF "v[232:235],a[212:215],a[108:111],v[232:235]\n"
LD "v6,s[28:31],s82 offen lds\n"
MF "v[240:243],a[212:215],a[112:115],v[240:243]\n"
"ds_read_b128 a[156:159],v2 offset:28672\n"
MF "v[240:243],a[208:211],a[72:75],v[240:243]\n"
"ds_read_b128 a[196:199],v3 offset:28672\n"
MF "v[248:251],a[208:211],a[76:79],v[248:251]\n"
MF "v[248:251],a[212:215],a[116:119],v[248:251]\n"
LD "v11,s[28:31],s82 offen offset:1024 lds\n"
MF "v[252:255],a[220:223],a[116:119],v[252:255]\n"
TR "a[208:209],v0 offset:8\n"
MF "v[252:255],a[216:219],a[76:79],v[252:255]\n"
TR "a[210:211],v0 offset:520\n"
MF "v[244:247],a[216:219],a[72:75],v[244:247]\n"
MF "v[244:247],a[220:223],a[112:115],v[244:247]\n"
TR "a[212:213],v0 offset:4104\n"
MF "v[236:239],a[220:223],a[108:111],v[236:239]\n"
TR "a[214:215],v0 offset:4616\n"
MF "v[236:239],a[216:219],a[68:71],v[236:239]\n"
MF "v[228:231],a[216:219],a[64:67],v[228:231]\n"
MF "v[228:231],a[220:223],a[104:107],v[228:231]\n"
MF "v[220:223],a[220:223],a[100:103],v[220:223]\n"
LD "v12,s[28:31],s82 offen offset:2048 lds\n"
MF "v[220:223],a[216:219],a[60:63],v[220:223]\n"
"s_waitcnt lgkmcnt(4)\n"
MF "v[16:19],a[200:203],a[140:143],v[16:19]\n"
TR "a[216:217],v1\n"
MF "v[16:19],a[204:207],a[180:183],v[16:19]\n"
TR "a[218:219],v1 offset:512\n"
MF "v[24:27],a[204:207],a[184:187],v[24:27]\n"
MF "v[24:27],a[200:203],a[144:147],v[24:27]\n"
TR "a[220:221],v1 offset:4096\n"
MF "v[32:35],a[200:203],a[148:151],v[32:35]\n"
TR "a[222:223],v1 offset:4608\n"
MF "v[32:35],a[204:207],a[188:191],v[32:35]\n"
MF "v[40:43],a[204:207],a[192:195],v[40:43]\n"
MF "v[40:43],a[200:203],a[152:155],v[40:43]\n"
LD "v13,s[28:31],s82 offen offset:3072 lds\n"
MF "v[48:51],a[200:203],a[156:159],v[48:51]\n"
"s_memtime s[52:53]\n"
"s_memrealtime s[100:101]\n"
MF "v[48:51],a[204:207],a[196:199],v[48:51]\n"
"s_waitcnt lgkmcnt(4)\n"
MF "v[52:55],a[212:215],a[196:199],v[52:55]\n"
TR "a[200:201],v1 offset:8\n"
MF "v[52:55],a[208:211],a[156:159],v[52:55]\n"
TR "a[202:203],v1 offset:520\n"
MF "v[44:47],a[208:211],a[152:155],v[44:47]\n"
MF "v[44:47],a[212:215],a[192:195],v[44:47]\n"
TR "a[204:205],v1 offset:4104\n"
MF "v[36:39],a[212:215],a[188:191],v[36:39]\n"
TR "a[206:207],v1 offset:4616\n"
MF "v[36:39],a[208:211],a[148:151],v[36:39]\n"
MF "v[28:31],a[208:211],a[144:147],v[28:31]\n"
MF "v[28:31],a[212:215],a[184:187],v[28:31]\n"
MF "v[20:23],a[212:215],a[180:183],v[20:23]\n"
"s_waitcnt vmcnt(25) lgkmcnt(0)\n"
MF "v[20:23],a[208:211],a[140:143],v[20:23]\n"
"s_barrier\n"
MF "v[56:59],a[216:219],a[140:143],v[56:59]\n"
"s_add_u32 m0,s62,49152\n"
"s_add_u32 s82,s66,7078016\n"
TR "a[208:209],v0 offset:16384\n"
MF "v[56:59],a[220:223],a[180:183],v[56:59]\n"
"s_sub_u32 s15,s82,7864320\n"
"s_min_u32 s82,s82,s15\n"
TR "a[210:211],v0 offset:16896\n"
MF "v[64:67],a[220:223],a[184:187],v[64:67]\n"
LD "v6,s[28:31],s82 offen lds\n"
MF "v[64:67],a[216:219],a[144:147],v[64:67]\n"
"s_sub_u32 s53,s52,s80\n"
TR "a[212:213],v0 offset:20480\n"
"v_lshrrev_b32 v15,8,v9\n"
MF "v[72:75],a[216:219],a[148:151],v[72:75]\n"
TR "a[214:215],v0 offset:20992\n"
"v_mul_u32_u24 v15,48960,v15\n"
MF "v[72:75],a[220:223],a[188:191],v[72:75]\n"
"s_lshr_b32 s7,s56,1\n"
"v_add_u32 v15,v15,v9\n"
MF "v[80:83],a[220:223],a[192:195],v[80:83]\n"
"s_and_b32 s8,s56,1\n"
MF "v[80:83],a[216:219],a[152:155],v[80:83]\n"
"s_sub_u32 s7,s7,s8\n"
MF "v[88:91],a[216:219],a[156:159],v[88:91]\n"
"s_mul_i32 s7,s7,245760\n"
MF "v[88:91],a[220:223],a[196:199],v[88:91]\n"
LD "v11,s[28:31],s82 offen offset:1024 lds\n"
MF "v[92:95],a[204:207],a[196:199],v[92:95]\n"
"s_add_u32 s7,s7,s89\n"
TR "a[216:217],v0 offset:16392\n"
MF "v[92:95],a[200:203],a[156:159],v[92:95]\n"
"s_cmp_gt_u32 s53,24100\n"
"s_cselect_b32 s101,1,0\n"
TR "a[218:219],v0 offset:16904\n"
MF "v[84:87],a[200:203],a[152:155],v[84:87]\n"
"s_cmp_eq_u32 s87,2\n"
"s_cselect_b32 s87,s101,s87\n"
MF "v[84:87],a[204:207],a[192:195],v[84:87]\n"
TR "a[220:221],v0 offset:20488\n"
MF "v[76:79],a[204:207],a[188:191],v[76:79]\n"
TR "a[222:223],v0 offset:21000\n"
MF "v[76:79],a[200:203],a[148:151],v[76:79]\n"
"s_cmp_gt_u32 s87,s57\n"
"s_cselect_b32 s97,130,0\n"
"s_cselect_b32 s95,0,0x47800000\n"
MF "v[68:71],a[200:203],a[144:147],v[68:71]\n"
"s_mov_b32 s21,s100\n"
MF "v[68:71],a[204:207],a[184:187],v[68:71]\n"
"s_mov_b32 s77,s95\n"
MF "v[60:63],a[204:207],a[180:183],v[60:63]\n"
LD "v12,s[28:31],s82 offen offset:2048 lds\n"
MF "v[60:63],a[200:203],a[140:143],v[60:63]\n"
"s_waitcnt lgkmcnt(4)\n"
MF "v[96:99],a[208:211],a[140:143],v[96:99]\n"
".Lpg7a:\n"
"s_cbranch_execz .Lisr1\n"
TR "a[200:201],v1 offset:16384\n"
MF "v[96:99],a[212:215],a[180:183],v[96:99]\n"
TR "a[202:203],v1 offset:16896\n"
MF "v[104:107],a[212:215],a[184:187],v[104:107]\n"
MF "v[104:107],a[208:211],a[144:147],v[104:107]\n"
TR "a[204:205],v1 offset:20480\n"
MF "v[112:115],a[208:211],a[148:151],v[112:115]\n"
TR "a[206:207],v1 offset:20992\n"
MF "v[112:115],a[212:215],a[188:191],v[112:115]\n"
MF "v[120:123],a[212:215],a[192:195],v[120:123]\n"
MF "v[120:123],a[208:211],a[152:155],v[120:123]\n"
LD "v13,s[28:31],s82 offen offset:3072 lds\n"
MF "v[128:131],a[208:211],a[156:159],v[128:131]\n"
MF "v[128:131],a[212:215],a[196:199],v[128:131]\n"
"s_waitcnt lgkmcnt(4)\n"
MF "v[132:135],a[220:223],a[196:199],v[132:135]\n"
TR "a[208:209],v1 offset:16392\n"
MF "v[132:135],a[216:219],a[156:159],v[132:135]\n"
TR "a[210:211],v1 offset:16904\n"
MF "v[124:127],a[216:219],a[152:155],v[124:127]\n"
MF "v[124:127],a[220:223],a[192:195],v[124:127]\n"
TR "a[212:213],v1 offset:20488\n"
MF "v[116:119],a[220:223],a[188:191],v[116:119]\n"
TR "a[214:215],v1 offset:21000\n"
MF "v[116:119],a[216:219],a[148:151],v[116:119]\n"
MF "v[108:111],a[216:219],a[144:147],v[108:111]\n"
MF "v[108:111],a[220:223],a[184:187],v[108:111]\n"
MF "v[100:103],a[220:223],a[180:183],v[100:103]\n"
"s_waitcnt vmcnt(22) lgkmcnt(0)\n"
MF "v[100:103],a[216:219],a[140:143],v[100:103]\n"
"s_barrier\n"
"s_cmp_eq_u32 s57,1\n"
"s_cbranch_scc0 .Lhs15\n"
".Lack_q:\n"
"v_cmp_ne_u32 vcc,s76,v10\n"
"s_cbranch_vccnz .Lackspin_q\n"
".Lackok_q:\n"
".Lhs15:\n"
MF "v[136:139],a[200:203],a[140:143],v[136:139]\n"
"s_and_b32 s58,s56,1\n"
TR "a[216:217],v0 offset:32768\n"
MF "v[136:139],a[204:207],a[180:183],v[136:139]\n"
"s_add_u32 m0,s62,65536\n"
TR "a[218:219],v0 offset:33280\n"
MF "v[144:147],a[204:207],a[184:187],v[144:147]\n"
"s_add_u32 s82,s66,6291456\n"
"ds_read_b128 a[60:63],v2 offset:40960\n"
MF "v[144:147],a[200:203],a[144:147],v[144:147]\n"
"s_sub_u32 s15,s82,7864320\n"
TR "a[220:221],v0 offset:36864\n"
MF "v[152:155],a[200:203],a[148:151],v[152:155]\n"
"s_min_u32 s82,s82,s15\n"
TR "a[222:223],v0 offset:37376\n"
MF "v[152:155],a[204:207],a[188:191],v[152:155]\n"
LD "v6,s[28:31],s82 offen lds\n"
MF "v[160:163],a[204:207],a[192:195],v[160:163]\n"
"s_mul_i32 s59,s58,3072\n"
"ds_read_b128 a[100:103],v3 offset:40960\n"
MF "v[160:163],a[200:203],a[152:155],v[160:163]\n"
"s_add_u32 s16,s70,s59\n"
MF "v[168:171],a[200:203],a[156:159],v[168:171]\n"
"s_mul_i32 s59,s58,6144\n"
MF "v[168:171],a[204:207],a[196:199],v[168:171]\n"
LD "v11,s[28:31],s82 offen offset:1024 lds\n"
MF "v[172:175],a[212:215],a[196:199],v[172:175]\n"
"s_add_u32 s18,s70,s59\n"
TR "a[200:201],v0 offset:32776\n"
MF "v[172:175],a[208:211],a[156:159],v[172:175]\n"
"s_add_u32 s17,s16,196608\n"
TR "a[202:203],v0 offset:33288\n"
MF "v[164:167],a[208:211],a[152:155],v[164:167]\n"
"s_add_u32 s19,s18,196608\n"
"ds_read_b128 a[64:67],v2 offset:43008\n"
MF "v[164:167],a[212:215],a[192:195],v[164:167]\n"
"s_add_u32 s20,s70,196608\n"
TR "a[204:205],v0 offset:36872\n"
MF "v[156:159],a[212:215],a[188:191],v[156:159]\n"
"s_bfe_u32 s58,s23,0x40008\n"
TR "a[206:207],v0 offset:37384\n"
MF "v[156:159],a[208:211],a[148:151],v[156:159]\n"
"s_lshr_b32 s59,s56,1\n"
MF "v[148:151],a[208:211],a[144:147],v[148:151]\n"
"s_mul_i32 s59,s59,5\n"
"ds_read_b128 a[104:107],v3 offset:43008\n"
MF "v[148:151],a[212:215],a[184:187],v[148:151]\n"
"s_add_u32 s58,s58,s59\n"
MF "v[140:143],a[212:215],a[180:183],v[140:143]\n"
LD "v12,s[28:31],s82 offen offset:2048 lds\n"
MF "v[140:143],a[208:211],a[140:143],v[140:143]\n"
"s_waitcnt lgkmcnt(7)\n"
MF "v[176:179],a[216:219],a[140:143],v[176:179]\n"
"s_and_b32 s58,s58,15\n"
TR "a[208:209],v1 offset:32768\n"
MF "v[176:179],a[220:223],a[180:183],v[176:179]\n"
"s_mul_i32 s58,s58,12288\n"
TR "a[210:211],v1 offset:33280\n"
MF "v[184:187],a[220:223],a[184:187],v[184:187]\n"
"s_add_u32 s72,s16,s58\n"
"ds_read_b128 a[68:71],v2 offset:45056\n"
MF "v[184:187],a[216:219],a[144:147],v[184:187]\n"
"s_add_u32 s73,s18,s58\n"
TR "a[212:213],v1 offset:36864\n"
MF "v[192:195],a[216:219],a[148:151],v[192:195]\n"
"s_add_u32 s74,s70,s58\n"
TR "a[214:215],v1 offset:37376\n"
MF "v[192:195],a[220:223],a[188:191],v[192:195]\n"
"s_add_u32 s74,s74,12288\n"
MF "v[200:203],a[220:223],a[192:195],v[200:203]\n"
"s_cmp_eq_u32 s74,s20\n"
"s_cselect_b32 s74,s70,s74\n"
"ds_read_b128 a[108:111],v3 offset:45056\n"
MF "v[200:203],a[216:219],a[152:155],v[200:203]\n"
LD "v13,s[28:31],s82 offen offset:3072 lds\n"
MF "v[208:211],a[216:219],a[156:159],v[208:211]\n"
"s_lshr_b32 s58,s56,1\n"
MF "v[208:211],a[220:223],a[196:199],v[208:211]\n"
"s_waitcnt lgkmcnt(7)\n"
MF "v[212:215],a[204:207],a[196:199],v[212:215]\n"
"s_mul_i32 s58,s58,6144\n"
TR "a[216:217],v1 offset:32776\n"
MF "v[212:215],a[200:203],a[156:159],v[212:215]\n"
"s_mul_i32 s64,s56,3072\n"
TR "a[218:219],v1 offset:33288\n"
MF "v[204:207],a[200:203],a[152:155],v[204:207]\n"
"s_add_u32 s64,s64,s58\n"
"ds_read_b128 a[72:75],v2 offset:47104\n"
MF "v[204:207],a[204:207],a[192:195],v[204:207]\n"
"s_mul_i32 s65,s56,6144\n"
TR "a[220:221],v1 offset:36872\n"
MF "v[196:199],a[204:207],a[188:191],v[196:199]\n"
"s_sub_u32 s58,s71,s70\n"
TR "a[222:223],v1 offset:37384\n"
MF "v[196:199],a[200:203],a[148:151],v[196:199]\n"
"s_add_u32 s96,s74,s58\n"
MF "v[188:191],a[200:203],a[144:147],v[188:191]\n"
"s_sub_u32 s58,s16,s70\n"
"v_add_u32 v0,0xffff4000,v0\n"
MF "v[188:191],a[204:207],a[184:187],v[188:191]\n"
"s_add_u32 s96,s96,s58\n"
"v_add_u32 v1,0xffff4000,v1\n"
MF "v[180:183],a[204:207],a[180:183],v[180:183]\n"
"s_waitcnt vmcnt(13) lgkmcnt(0)\n"
MF "v[180:183],a[200:203],a[140:143],v[180:183]\n"
"s_barrier\n"
MF "v[216:219],a[208:211],a[140:143],v[216:219]\n"
"s_max_u32 s60,s70,s71\n"
TR "a[200:201],v1 offset:8\n"
MF "v[216:219],a[212:215],a[180:183],v[216:219]\n"
"s_add_u32 m0,s62,81920\n"
TR "a[202:203],v1 offset:520\n"
"v_mov_b32 v14,s12\n"
MF "v[224:227],a[212:215],a[184:187],v[224:227]\n"
"s_add_u32 s82,s66,7077888\n"
"ds_read_b128 a[112:115],v3 offset:47104\n"
"v_mul_f32 v14,0xbdd53b95,v14\n"
MF "v[224:227],a[208:211],a[144:147],v[224:227]\n"
"s_sub_u32 s15,s82,7864320\n"
TR "a[204:205],v1 offset:4104\n"
MF "v[232:235],a[208:211],a[148:151],v[232:235]\n"
"s_min_u32 s82,s82,s15\n"
TR "a[206:207],v1 offset:4616\n"
MF "v[232:235],a[212:215],a[188:191],v[232:235]\n"
LD "v6,s[28:31],s82 offen lds\n"
MF "v[240:243],a[212:215],a[192:195],v[240:243]\n"
"s_add_u32 s34,s60,196608\n"
"ds_read_b128 a[76:79],v2 offset:49152\n"
MF "v[240:243],a[208:211],a[152:155],v[240:243]\n"
"s_add_u32 s38,s60,196608\n"
"ds_read_b128 a[116:119],v3 offset:49152\n"
MF "v[248:251],a[208:211],a[156:159],v[248:251]\n"
MF "v[248:251],a[212:215],a[196:199],v[248:251]\n"
LD "v11,s[28:31],s82 offen offset:1024 lds\n"
MF "v[220:223],a[216:219],a[140:143],v[220:223]\n"
TR "a[208:209],v1 offset:16392\n"
MF "v[220:223],a[220:223],a[180:183],v[220:223]\n"
TR "a[210:211],v1 offset:16904\n"
MF "v[228:231],a[220:223],a[184:187],v[228:231]\n"
"ds_read_b128 a[140:143],v2\n"
MF "v[228:231],a[216:219],a[144:147],v[228:231]\n"
TR "a[212:213],v1 offset:20488\n"
MF "v[236:239],a[216:219],a[148:151],v[236:239]\n"
TR "a[214:215],v1 offset:21000\n"
MF "v[236:239],a[220:223],a[188:191],v[236:239]\n"
"ds_read_b128 a[180:183],v3\n"
MF "v[244:247],a[220:223],a[192:195],v[244:247]\n"
"ds_read_b128 a[144:147],v2 offset:2048\n"
MF "v[244:247],a[216:219],a[152:155],v[244:247]\n"
"ds_read_b128 a[184:187],v3 offset:2048\n"
MF "v[252:255],a[216:219],a[156:159],v[252:255]\n"
"ds_read_b128 a[148:151],v2 offset:4096\n"
MF "v[252:255],a[220:223],a[196:199],v[252:255]\n"
"s_waitcnt lgkmcnt(9)\n"
"ds_read_b128 a[188:191],v3 offset:4096\n"
MF "v[220:223],a[200:203],a[60:63],v[220:223]\n"
TR "a[216:217],v1\n"
MF "v[220:223],a[204:207],a[100:103],v[220:223]\n"
TR "a[218:219],v1 offset:512\n"
MF "v[228:231],a[204:207],a[104:107],v[228:231]\n"
"ds_read_b128 a[152:155],v2 offset:6144\n"
MF "v[228:231],a[200:203],a[64:67],v[228:231]\n"
TR "a[220:221],v1 offset:4096\n"
MF "v[236:239],a[200:203],a[68:71],v[236:239]\n"
TR "a[222:223],v1 offset:4608\n"
MF "v[236:239],a[204:207],a[108:111],v[236:239]\n"
"ds_read_b128 a[192:195],v3 offset:6144\n"
MF "v[244:247],a[204:207],a[112:115],v[244:247]\n"
"ds_read_b128 a[156:159],v2 offset:8192\n"
MF "v[244:247],a[200:203],a[72:75],v[244:247]\n"
"ds_read_b128 a[196:199],v3 offset:8192\n"
MF "v[252:255],a[200:203],a[76:79],v[252:255]\n"
LD "v12,s[28:31],s82 offen offset:2048 lds\n"
MF "v[252:255],a[204:207],a[116:119],v[252:255]\n"
"s_waitcnt lgkmcnt(8)\n"
LD "v13,s[28:31],s82 offen offset:3072 lds\n"
MF "v[220:223],a[208:211],a[140:143],v[220:223]\n"
TR "a[200:201],v1 offset:16384\n"
MF "v[220:223],a[212:215],a[180:183],v[220:223]\n"
TR "a[202:203],v1 offset:16896\n"
MF "v[228:231],a[212:215],a[184:187],v[228:231]\n"
MF "v[228:231],a[208:211],a[144:147],v[228:231]\n"
TR "a[204:205],v1 offset:20480\n"
MF "v[236:239],a[208:211],a[148:151],v[236:239]\n"
TR "a[206:207],v1 offset:20992\n"
MF "v[236:239],a[212:215],a[188:191],v[236:239]\n"
"s_waitcnt lgkmcnt(4)\n"
MF "v[244:247],a[212:215],a[192:195],v[244:247]\n"
MF "v[244:247],a[208:211],a[152:155],v[244:247]\n"
MF "v[252:255],a[208:211],a[156:159],v[252:255]\n"
MF "v[252:255],a[212:215],a[196:199],v[252:255]\n"
"s_waitcnt lgkmcnt(0)\n"
"s_cmp_eq_u32 s69,98\n"
"s_cbranch_scc1 .Lqy\n"
".Lqf:\n"
MF "v[216:219],a[216:219],a[60:63],v[216:219]\n"
TR "a[208:209],v0 offset:8\n"
MF "v[216:219],a[220:223],a[100:103],v[216:219]\n"
TR "a[210:211],v0 offset:520\n"
MF "v[224:227],a[220:223],a[104:107],v[224:227]\n"
"buffer_load_dword a224,v15,s[36:39],s70 offen offset:2688 nt\n"
MF "v[224:227],a[216:219],a[64:67],v[224:227]\n"
TR "a[212:213],v0 offset:4104\n"
MF "v[232:235],a[216:219],a[68:71],v[232:235]\n"
TR "a[214:215],v0 offset:4616\n"
MF "v[232:235],a[220:223],a[108:111],v[232:235]\n"
"buffer_load_dword a225,v15,s[36:39],s71 offen offset:2688 nt\n"
MF "v[240:243],a[220:223],a[112:115],v[240:243]\n"
MF "v[240:243],a[216:219],a[72:75],v[240:243]\n"
"buffer_load_dword a226,v15,s[48:51],s7 offen offset:2688\n"
MF "v[248:251],a[216:219],a[76:79],v[248:251]\n"
MF "v[248:251],a[220:223],a[116:119],v[248:251]\n"
MF "v[248:251],a[200:203],a[156:159],v[248:251]\n"
TR "a[216:217],v0 offset:16392\n"
MF "v[248:251],a[204:207],a[196:199],v[248:251]\n"
TR "a[218:219],v0 offset:16904\n"
MF "v[240:243],a[204:207],a[192:195],v[240:243]\n"
MF "v[240:243],a[200:203],a[152:155],v[240:243]\n"
TR "a[220:221],v0 offset:20488\n"
MF "v[232:235],a[200:203],a[148:151],v[232:235]\n"
TR "a[222:223],v0 offset:21000\n"
MF "v[232:235],a[204:207],a[188:191],v[232:235]\n"
MF "v[224:227],a[204:207],a[184:187],v[224:227]\n"
MF "v[224:227],a[200:203],a[144:147],v[224:227]\n"
"v_fma_f32 v248,v248,s54,v14\n"
"v_fma_f32 v249,v249,s54,v14\n"
MF "v[216:219],a[200:203],a[140:143],v[216:219]\n"
"v_fma_f32 v250,v250,s54,v14\n"
"v_fma_f32 v251,v251,s54,v14\n"
MF "v[216:219],a[204:207],a[180:183],v[216:219]\n"
"s_waitcnt lgkmcnt(4)\n"
"v_fma_f32 v252,v252,s54,v14\n"
"v_fma_f32 v253,v253,s54,v14\n"
MF "v[180:183],a[208:211],a[60:63],v[180:183]\n"
TR "a[200:201],v0\n"
"v_fma_f32 v254,v254,s54,v14\n"
MF "v[180:183],a[212:215],a[100:103],v[180:183]\n"
TR "a[202:203],v0 offset:512\n"
"v_fma_f32 v255,v255,s54,v14\n"
MF "v[188:191],a[212:215],a[104:107],v[188:191]\n"
CV "v255,v254,v255\n"
CV "v254,v252,v253\n"
MF "v[188:191],a[208:211],a[64:67],v[188:191]\n"
TR "a[204:205],v0 offset:4096\n"
CV "v253,v250,v251\n"
MF "v[196:199],a[208:211],a[68:71],v[196:199]\n"
TR "a[206:207],v0 offset:4608\n"
CV "v252,v248,v249\n"
MF "v[196:199],a[212:215],a[108:111],v[196:199]\n"
"v_fma_f32 v240,v240,s54,v14\n"
"v_fma_f32 v241,v241,s54,v14\n"
MF "v[204:207],a[212:215],a[112:115],v[204:207]\n"
"v_fma_f32 v242,v242,s54,v14\n"
"v_fma_f32 v243,v243,s54,v14\n"
MF "v[204:207],a[208:211],a[72:75],v[204:207]\n"
"v_fma_f32 v244,v244,s54,v14\n"
"v_fma_f32 v245,v245,s54,v14\n"
MF "v[212:215],a[208:211],a[76:79],v[212:215]\n"
"v_fma_f32 v246,v246,s54,v14\n"
"v_fma_f32 v247,v247,s54,v14\n"
MF "v[212:215],a[212:215],a[116:119],v[212:215]\n"
"s_waitcnt lgkmcnt(4)\n"
CV "v251,v246,v247\n"
CV "v250,v244,v245\n"
MF "v[180:183],a[216:219],a[140:143],v[180:183]\n"
TR "a[208:209],v0 offset:16384\n"
CV "v249,v242,v243\n"
MF "v[180:183],a[220:223],a[180:183],v[180:183]\n"
TR "a[210:211],v0 offset:16896\n"
CV "v248,v240,v241\n"
MF "v[188:191],a[220:223],a[184:187],v[188:191]\n"
"v_fma_f32 v232,v232,s54,v14\n"
"v_fma_f32 v233,v233,s54,v14\n"
MF "v[188:191],a[216:219],a[144:147],v[188:191]\n"
TR "a[212:213],v0 offset:20480\n"
"v_fma_f32 v234,v234,s54,v14\n"
MF "v[196:199],a[216:219],a[148:151],v[196:199]\n"
TR "a[214:215],v0 offset:20992\n"
"v_fma_f32 v235,v235,s54,v14\n"
MF "v[196:199],a[220:223],a[188:191],v[196:199]\n"
"v_fma_f32 v236,v236,s54,v14\n"
"v_fma_f32 v237,v237,s54,v14\n"
MF "v[204:207],a[220:223],a[192:195],v[204:207]\n"
"v_fma_f32 v238,v238,s54,v14\n"
"v_fma_f32 v239,v239,s54,v14\n"
MF "v[204:207],a[216:219],a[152:155],v[204:207]\n"
CV "v247,v238,v239\n"
CV "v246,v236,v237\n"
MF "v[212:215],a[216:219],a[156:159],v[212:215]\n"
"s_waitcnt vmcnt(11) lgkmcnt(0)\n"
CV "v245,v234,v235\n"
CV "v244,v232,v233\n"
MF "v[212:215],a[220:223],a[196:199],v[212:215]\n"
"s_barrier\n"
MF "v[176:179],a[200:203],a[60:63],v[176:179]\n"
TR "a[216:217],v1 offset:32776\n"
"v_fma_f32 v224,v224,s54,v14\n"
MF "v[176:179],a[204:207],a[100:103],v[176:179]\n"
"s_add_u32 m0,s64,0\n"
TR "a[218:219],v1 offset:33288\n"
"v_fma_f32 v225,v225,s54,v14\n"
MF "v[184:187],a[204:207],a[104:107],v[184:187]\n"
LD "v7,s[32:35],s72 offen nt lds\n"
MF "v[184:187],a[200:203],a[64:67],v[184:187]\n"
TR "a[220:221],v1 offset:36872\n"
"v_fma_f32 v226,v226,s54,v14\n"
MF "v[192:195],a[200:203],a[68:71],v[192:195]\n"
TR "a[222:223],v1 offset:37384\n"
"v_fma_f32 v227,v227,s54,v14\n"
MF "v[192:195],a[204:207],a[108:111],v[192:195]\n"
"s_add_u32 m0,s64,896\n"
"v_fma_f32 v228,v228,s54,v14\n"
"v_fma_f32 v229,v229,s54,v14\n"
MF "v[200:203],a[204:207],a[112:115],v[200:203]\n"
LD "v7,s[32:35],s72 offen offset:128 nt lds\n"
MF "v[200:203],a[200:203],a[72:75],v[200:203]\n"
"v_fma_f32 v230,v230,s54,v14\n"
"v_fma_f32 v231,v231,s54,v14\n"
MF "v[208:211],a[200:203],a[76:79],v[208:211]\n"
CV "v243,v230,v231\n"
CV "v242,v228,v229\n"
MF "v[208:211],a[204:207],a[116:119],v[208:211]\n"
CV "v241,v226,v227\n"
CV "v240,v224,v225\n"
MF "v[208:211],a[208:211],a[156:159],v[208:211]\n"
TR "a[200:201],v1 offset:49160\n"
"v_fma_f32 v216,v216,s54,v14\n"
MF "v[208:211],a[212:215],a[196:199],v[208:211]\n"
"s_add_u32 m0,s64,1792\n"
TR "a[202:203],v1 offset:49672\n"
"v_fma_f32 v217,v217,s54,v14\n"
MF "v[200:203],a[212:215],a[192:195],v[200:203]\n"
LD "v7,s[32:35],s72 offen offset:256 nt lds\n"
MF "v[200:203],a[208:211],a[152:155],v[200:203]\n"
TR "a[204:205],v1 offset:53256\n"
"v_fma_f32 v218,v218,s54,v14\n"
MF "v[192:195],a[208:211],a[148:151],v[192:195]\n"
TR "a[206:207],v1 offset:53768\n"
"v_fma_f32 v219,v219,s54,v14\n"
MF "v[192:195],a[212:215],a[188:191],v[192:195]\n"
"v_fma_f32 v220,v220,s54,v14\n"
"v_fma_f32 v221,v221,s54,v14\n"
MF "v[184:187],a[212:215],a[184:187],v[184:187]\n"
"s_add_u32 s72,s72,6144\n"
"v_fma_f32 v222,v222,s54,v14\n"
"v_fma_f32 v223,v223,s54,v14\n"
MF "v[184:187],a[208:211],a[144:147],v[184:187]\n"
"s_add_u32 m0,s64,6144\n"
CV "v239,v222,v223\n"
CV "v238,v220,v221\n"
MF "v[176:179],a[208:211],a[140:143],v[176:179]\n"
LD "v7,s[32:35],s72 offen nt lds\n"
MF "v[176:179],a[212:215],a[180:183],v[176:179]\n"
"s_waitcnt lgkmcnt(4)\n"
CV "v237,v218,v219\n"
CV "v236,v216,v217\n"
MF "v[140:143],a[216:219],a[60:63],v[140:143]\n"
TR "a[208:209],v1 offset:32768\n"
"v_fma_f32 v208,v208,s54,v14\n"
MF "v[140:143],a[220:223],a[100:103],v[140:143]\n"
TR "a[210:211],v1 offset:33280\n"
"v_fma_f32 v209,v209,s54,v14\n"
MF "v[148:151],a[220:223],a[104:107],v[148:151]\n"
"v_fma_f32 v210,v210,s54,v14\n"
"v_fma_f32 v211,v211,s54,v14\n"
MF "v[148:151],a[216:219],a[64:67],v[148:151]\n"
TR "a[212:213],v1 offset:36864\n"
"v_fma_f32 v212,v212,s54,v14\n"
MF "v[156:159],a[216:219],a[68:71],v[156:159]\n"
"s_add_u32 m0,s64,7040\n"
TR "a[214:215],v1 offset:37376\n"
"v_fma_f32 v213,v213,s54,v14\n"
MF "v[156:159],a[220:223],a[108:111],v[156:159]\n"
LD "v7,s[32:35],s72 offen offset:128 nt lds\n"
MF "v[164:167],a[220:223],a[112:115],v[164:167]\n"
"v_fma_f32 v214,v214,s54,v14\n"
"v_fma_f32 v215,v215,s54,v14\n"
MF "v[164:167],a[216:219],a[72:75],v[164:167]\n"
CV "v235,v214,v215\n"
CV "v234,v212,v213\n"
MF "v[172:175],a[216:219],a[76:79],v[172:175]\n"
"s_add_u32 m0,s64,7936\n"
CV "v233,v210,v211\n"
CV "v232,v208,v209\n"
MF "v[172:175],a[220:223],a[116:119],v[172:175]\n"
"s_waitcnt lgkmcnt(4)\n"
LD "v7,s[32:35],s72 offen offset:256 nt lds\n"
MF "v[140:143],a[200:203],a[140:143],v[140:143]\n"
TR "a[216:217],v1 offset:49152\n"
"v_fma_f32 v200,v200,s54,v14\n"
MF "v[140:143],a[204:207],a[180:183],v[140:143]\n"
TR "a[218:219],v1 offset:49664\n"
"v_fma_f32 v201,v201,s54,v14\n"
MF "v[148:151],a[204:207],a[184:187],v[148:151]\n"
"v_fma_f32 v202,v202,s54,v14\n"
"v_fma_f32 v203,v203,s54,v14\n"
MF "v[148:151],a[200:203],a[144:147],v[148:151]\n"
TR "a[220:221],v1 offset:53248\n"
"v_fma_f32 v204,v204,s54,v14\n"
MF "v[156:159],a[200:203],a[148:151],v[156:159]\n"
TR "a[222:223],v1 offset:53760\n"
"v_fma_f32 v205,v205,s54,v14\n"
MF "v[156:159],a[204:207],a[188:191],v[156:159]\n"
"v_fma_f32 v206,v206,s54,v14\n"
"v_fma_f32 v207,v207,s54,v14\n"
MF "v[164:167],a[204:207],a[192:195],v[164:167]\n"
CV "v231,v206,v207\n"
CV "v230,v204,v205\n"
MF "v[164:167],a[200:203],a[152:155],v[164:167]\n"
CV "v229,v202,v203\n"
CV "v228,v200,v201\n"
MF "v[172:175],a[200:203],a[156:159],v[172:175]\n"
"v_fma_f32 v192,v192,s54,v14\n"
"v_fma_f32 v193,v193,s54,v14\n"
MF "v[172:175],a[204:207],a[196:199],v[172:175]\n"
"s_waitcnt lgkmcnt(0)\n"
"v_fma_f32 v194,v194,s54,v14\n"
"v_fma_f32 v195,v195,s54,v14\n"
MF "v[136:139],a[208:211],a[60:63],v[136:139]\n"
TR "a[200:201],v0 offset:32776\n"
"v_fma_f32 v196,v196,s54,v14\n"
MF "v[136:139],a[212:215],a[100:103],v[136:139]\n"
TR "a[202:203],v0 offset:33288\n"
"v_fma_f32 v197,v197,s54,v14\n"
MF "v[144:147],a[212:215],a[104:107],v[144:147]\n"
"v_fma_f32 v198,v198,s54,v14\n"
"v_fma_f32 v199,v199,s54,v14\n"
MF "v[144:147],a[208:211],a[64:67],v[144:147]\n"
TR "a[204:205],v0 offset:36872\n"
CV "v227,v198,v199\n"
MF "v[152:155],a[208:211],a[68:71],v[152:155]\n"
TR "a[206:207],v0 offset:37384\n"
CV "v226,v196,v197\n"
MF "v[152:155],a[212:215],a[108:111],v[152:155]\n"
CV "v225,v194,v195\n"
CV "v224,v192,v193\n"
MF "v[160:163],a[212:215],a[112:115],v[160:163]\n"
"v_fma_f32 v184,v184,s54,v14\n"
"v_fma_f32 v185,v185,s54,v14\n"
MF "v[160:163],a[208:211],a[72:75],v[160:163]\n"
"v_fma_f32 v186,v186,s54,v14\n"
"v_fma_f32 v187,v187,s54,v14\n"
MF "v[168:171],a[208:211],a[76:79],v[168:171]\n"
"v_fma_f32 v188,v188,s54,v14\n"
"v_fma_f32 v189,v189,s54,v14\n"
MF "v[168:171],a[212:215],a[116:119],v[168:171]\n"
"v_fma_f32 v190,v190,s54,v14\n"
"v_fma_f32 v191,v191,s54,v14\n"
MF "v[168:171],a[216:219],a[156:159],v[168:171]\n"
TR "a[208:209],v0 offset:49160\n"
CV "v223,v190,v191\n"
MF "v[168:171],a[220:223],a[196:199],v[168:171]\n"
TR "a[210:211],v0 offset:49672\n"
CV "v222,v188,v189\n"
MF "v[160:163],a[220:223],a[192:195],v[160:163]\n"
CV "v221,v186,v187\n"
CV "v220,v184,v185\n"
MF "v[160:163],a[216:219],a[152:155],v[160:163]\n"
TR "a[212:213],v0 offset:53256\n"
"v_fma_f32 v176,v176,s54,v14\n"
MF "v[152:155],a[216:219],a[148:151],v[152:155]\n"
TR "a[214:215],v0 offset:53768\n"
"v_fma_f32 v177,v177,s54,v14\n"
MF "v[152:155],a[220:223],a[188:191],v[152:155]\n"
"v_fma_f32 v178,v178,s54,v14\n"
"v_fma_f32 v179,v179,s54,v14\n"
MF "v[144:147],a[220:223],a[184:187],v[144:147]\n"
"v_fma_f32 v180,v180,s54,v14\n"
"v_fma_f32 v181,v181,s54,v14\n"
MF "v[144:147],a[216:219],a[144:147],v[144:147]\n"
"v_fma_f32 v182,v182,s54,v14\n"
"v_fma_f32 v183,v183,s54,v14\n"
MF "v[136:139],a[216:219],a[140:143],v[136:139]\n"
CV "v219,v182,v183\n"
CV "v218,v180,v181\n"
MF "v[136:139],a[220:223],a[180:183],v[136:139]\n"
"s_waitcnt lgkmcnt(4)\n"
CV "v217,v178,v179\n"
CV "v216,v176,v177\n"
MF "v[100:103],a[200:203],a[60:63],v[100:103]\n"
TR "a[216:217],v0 offset:32768\n"
"v_fma_f32 v168,v168,s54,v14\n"
MF "v[100:103],a[204:207],a[100:103],v[100:103]\n"
TR "a[218:219],v0 offset:33280\n"
"v_fma_f32 v169,v169,s54,v14\n"
MF "v[108:111],a[204:207],a[104:107],v[108:111]\n"
"v_fma_f32 v170,v170,s54,v14\n"
"v_fma_f32 v171,v171,s54,v14\n"
MF "v[108:111],a[200:203],a[64:67],v[108:111]\n"
TR "a[220:221],v0 offset:36864\n"
"v_fma_f32 v172,v172,s54,v14\n"
MF "v[116:119],a[200:203],a[68:71],v[116:119]\n"
TR "a[222:223],v0 offset:37376\n"
"v_fma_f32 v173,v173,s54,v14\n"
MF "v[116:119],a[204:207],a[108:111],v[116:119]\n"
"v_fma_f32 v174,v174,s54,v14\n"
"v_fma_f32 v175,v175,s54,v14\n"
MF "v[124:127],a[204:207],a[112:115],v[124:127]\n"
CV "v215,v174,v175\n"
CV "v214,v172,v173\n"
MF "v[124:127],a[200:203],a[72:75],v[124:127]\n"
".Lpg8b:\n"
"s_cbranch_execz .Lpg11b\n"
".Lpg8d:\n"
"s_cbranch_execz .Lisr2\n"
CV "v213,v170,v171\n"
CV "v212,v168,v169\n"
MF "v[132:135],a[200:203],a[76:79],v[132:135]\n"
"v_fma_f32 v160,v160,s54,v14\n"
"v_fma_f32 v161,v161,s54,v14\n"
MF "v[132:135],a[204:207],a[116:119],v[132:135]\n"
"s_waitcnt lgkmcnt(4)\n"
"v_fma_f32 v162,v162,s54,v14\n"
"v_fma_f32 v163,v163,s54,v14\n"
MF "v[100:103],a[208:211],a[140:143],v[100:103]\n"
TR "a[200:201],v0 offset:49152\n"
"v_fma_f32 v164,v164,s54,v14\n"
MF "v[100:103],a[212:215],a[180:183],v[100:103]\n"
TR "a[202:203],v0 offset:49664\n"
"v_fma_f32 v165,v165,s54,v14\n"
MF "v[108:111],a[212:215],a[184:187],v[108:111]\n"
"v_fma_f32 v166,v166,s54,v14\n"
"v_fma_f32 v167,v167,s54,v14\n"
MF "v[108:111],a[208:211],a[144:147],v[108:111]\n"
TR "a[204:205],v0 offset:53248\n"
CV "v211,v166,v167\n"
MF "v[116:119],a[208:211],a[148:151],v[116:119]\n"
TR "a[206:207],v0 offset:53760\n"
CV "v210,v164,v165\n"
MF "v[116:119],a[212:215],a[188:191],v[116:119]\n"
CV "v209,v162,v163\n"
CV "v208,v160,v161\n"
MF "v[124:127],a[212:215],a[192:195],v[124:127]\n"
"v_add_u32 v0,0xc000,v0\n"
"v_fma_f32 v152,v152,s54,v14\n"
MF "v[124:127],a[208:211],a[152:155],v[124:127]\n"
"v_add_u32 v1,0xc000,v1\n"
"v_fma_f32 v153,v153,s54,v14\n"
MF "v[132:135],a[208:211],a[156:159],v[132:135]\n"
"s_waitcnt vmcnt(9) lgkmcnt(0)\n"
"v_fma_f32 v154,v154,s54,v14\n"
"v_fma_f32 v155,v155,s54,v14\n"
MF "v[132:135],a[212:215],a[196:199],v[132:135]\n"
"s_barrier\n"
MF "v[96:99],a[216:219],a[60:63],v[96:99]\n"
TR "a[208:209],v1 offset:16392\n"
"v_fma_f32 v156,v156,s54,v14\n"
MF "v[96:99],a[220:223],a[100:103],v[96:99]\n"
TR "a[210:211],v1 offset:16904\n"
"v_fma_f32 v157,v157,s54,v14\n"
MF "v[104:107],a[220:223],a[104:107],v[104:107]\n"
"s_add_u32 s72,s72,6144\n"
"v_fma_f32 v158,v158,s54,v14\n"
"v_fma_f32 v159,v159,s54,v14\n"
MF "v[104:107],a[216:219],a[64:67],v[104:107]\n"
"s_cmp_eq_u32 s72,s17\n"
"s_cselect_b32 s72,s16,s72\n"
TR "a[212:213],v1 offset:20488\n"
MF "v[112:115],a[216:219],a[68:71],v[112:115]\n"
"s_add_u32 m0,s64,24576\n"
TR "a[214:215],v1 offset:21000\n"
CV "v207,v158,v159\n"
MF "v[112:115],a[220:223],a[108:111],v[112:115]\n"
LD "v7,s[32:35],s72 offen nt lds\n"
MF "v[120:123],a[220:223],a[112:115],v[120:123]\n"
"s_add_u32 m0,s64,25472\n"
CV "v206,v156,v157\n"
CV "v205,v154,v155\n"
MF "v[120:123],a[216:219],a[72:75],v[120:123]\n"
LD "v7,s[32:35],s72 offen offset:128 nt lds\n"
MF "v[128:131],a[216:219],a[76:79],v[128:131]\n"
CV "v204,v152,v153\n"
"v_fma_f32 v144,v144,s54,v14\n"
MF "v[128:131],a[220:223],a[116:119],v[128:131]\n"
"v_fma_f32 v145,v145,s54,v14\n"
"v_fma_f32 v146,v146,s54,v14\n"
MF "v[128:131],a[200:203],a[156:159],v[128:131]\n"
TR "a[216:217],v1 offset:32776\n"
"v_fma_f32 v147,v147,s54,v14\n"
MF "v[128:131],a[204:207],a[196:199],v[128:131]\n"
"s_add_u32 m0,s64,26368\n"
TR "a[218:219],v1 offset:33288\n"
"v_fma_f32 v148,v148,s54,v14\n"
MF "v[120:123],a[204:207],a[192:195],v[120:123]\n"
LD "v7,s[32:35],s72 offen offset:256 nt lds\n"
MF "v[120:123],a[200:203],a[152:155],v[120:123]\n"
TR "a[220:221],v1 offset:36872\n"
"v_fma_f32 v149,v149,s54,v14\n"
MF "v[112:115],a[200:203],a[148:151],v[112:115]\n"
TR "a[222:223],v1 offset:37384\n"
"v_fma_f32 v150,v150,s54,v14\n"
MF "v[112:115],a[204:207],a[188:191],v[112:115]\n"
"v_fma_f32 v151,v151,s54,v14\n"
CV "v203,v150,v151\n"
MF "v[104:107],a[204:207],a[184:187],v[104:107]\n"
"s_add_u32 s72,s72,6144\n"
CV "v202,v148,v149\n"
CV "v201,v146,v147\n"
MF "v[104:107],a[200:203],a[144:147],v[104:107]\n"
"s_add_u32 m0,s64,30720\n"
CV "v200,v144,v145\n"
"v_fma_f32 v136,v136,s54,v14\n"
MF "v[96:99],a[200:203],a[140:143],v[96:99]\n"
LD "v7,s[32:35],s72 offen nt lds\n"
MF "v[96:99],a[204:207],a[180:183],v[96:99]\n"
"s_waitcnt lgkmcnt(4)\n"
"v_fma_f32 v137,v137,s54,v14\n"
"v_fma_f32 v138,v138,s54,v14\n"
MF "v[60:63],a[208:211],a[60:63],v[60:63]\n"
TR "a[200:201],v1 offset:16384\n"
"v_fma_f32 v139,v139,s54,v14\n"
MF "v[60:63],a[212:215],a[100:103],v[60:63]\n"
TR "a[202:203],v1 offset:16896\n"
"v_fma_f32 v140,v140,s54,v14\n"
MF "v[68:71],a[212:215],a[104:107],v[68:71]\n"
"v_fma_f32 v141,v141,s54,v14\n"
"v_fma_f32 v142,v142,s54,v14\n"
MF "v[68:71],a[208:211],a[64:67],v[68:71]\n"
TR "a[204:205],v1 offset:20480\n"
"v_fma_f32 v143,v143,s54,v14\n"
MF "v[76:79],a[208:211],a[68:71],v[76:79]\n"
"s_add_u32 m0,s64,31616\n"
TR "a[206:207],v1 offset:20992\n"
CV "v199,v142,v143\n"
MF "v[76:79],a[212:215],a[108:111],v[76:79]\n"
LD "v7,s[32:35],s72 offen offset:128 nt lds\n"
MF "v[84:87],a[212:215],a[112:115],v[84:87]\n"
CV "v198,v140,v141\n"
CV "v197,v138,v139\n"
MF "v[84:87],a[208:211],a[72:75],v[84:87]\n"
CV "v196,v136,v137\n"
"v_fma_f32 v128,v128,s54,v14\n"
MF "v[92:95],a[208:211],a[76:79],v[92:95]\n"
"s_add_u32 m0,s64,32512\n"
"v_fma_f32 v129,v129,s54,v14\n"
"v_fma_f32 v130,v130,s54,v14\n"
MF "v[92:95],a[212:215],a[116:119],v[92:95]\n"
"s_waitcnt lgkmcnt(4)\n"
LD "v7,s[32:35],s72 offen offset:256 nt lds\n"
MF "v[60:63],a[216:219],a[140:143],v[60:63]\n"
TR "a[208:209],v1 offset:32768\n"
"v_fma_f32 v131,v131,s54,v14\n"
MF "v[60:63],a[220:223],a[180:183],v[60:63]\n"
TR "a[210:211],v1 offset:33280\n"
"v_fma_f32 v132,v132,s54,v14\n"
MF "v[68:71],a[220:223],a[184:187],v[68:71]\n"
"v_fma_f32 v133,v133,s54,v14\n"
"v_fma_f32 v134,v134,s54,v14\n"
MF "v[68:71],a[216:219],a[144:147],v[68:71]\n"
TR "a[212:213],v1 offset:36864\n"
"v_fma_f32 v135,v135,s54,v14\n"
MF "v[76:79],a[216:219],a[148:151],v[76:79]\n"
TR "a[214:215],v1 offset:37376\n"
CV "v195,v134,v135\n"
MF "v[76:79],a[220:223],a[188:191],v[76:79]\n"
CV "v194,v132,v133\n"
CV "v193,v130,v131\n"
MF "v[84:87],a[220:223],a[192:195],v[84:87]\n"
CV "v192,v128,v129\n"
"v_fma_f32 v120,v120,s54,v14\n"
MF "v[84:87],a[216:219],a[152:155],v[84:87]\n"
"v_fma_f32 v121,v121,s54,v14\n"
"v_fma_f32 v122,v122,s54,v14\n"
MF "v[92:95],a[216:219],a[156:159],v[92:95]\n"
"v_fma_f32 v123,v123,s54,v14\n"
"v_fma_f32 v124,v124,s54,v14\n"
MF "v[92:95],a[220:223],a[196:199],v[92:95]\n"
"s_waitcnt lgkmcnt(0)\n"
"v_fma_f32 v125,v125,s54,v14\n"
"v_fma_f32 v126,v126,s54,v14\n"
MF "v[56:59],a[200:203],a[60:63],v[56:59]\n"
TR "a[216:217],v0 offset:16392\n"
"v_fma_f32 v127,v127,s54,v14\n"
MF "v[56:59],a[204:207],a[100:103],v[56:59]\n"
TR "a[218:219],v0 offset:16904\n"
CV "v191,v126,v127\n"
MF "v[64:67],a[204:207],a[104:107],v[64:67]\n"
CV "v190,v124,v125\n"
CV "v189,v122,v123\n"
MF "v[64:67],a[200:203],a[64:67],v[64:67]\n"
TR "a[220:221],v0 offset:20488\n"
CV "v188,v120,v121\n"
MF "v[72:75],a[200:203],a[68:71],v[72:75]\n"
TR "a[222:223],v0 offset:21000\n"
"v_fma_f32 v112,v112,s54,v14\n"
MF "v[72:75],a[204:207],a[108:111],v[72:75]\n"
"v_fma_f32 v113,v113,s54,v14\n"
"v_fma_f32 v114,v114,s54,v14\n"
MF "v[80:83],a[204:207],a[112:115],v[80:83]\n"
"v_fma_f32 v115,v115,s54,v14\n"
"v_fma_f32 v116,v116,s54,v14\n"
MF "v[80:83],a[200:203],a[72:75],v[80:83]\n"
"v_fma_f32 v117,v117,s54,v14\n"
"v_fma_f32 v118,v118,s54,v14\n"
MF "v[88:91],a[200:203],a[76:79],v[88:91]\n"
"v_fma_f32 v119,v119,s54,v14\n"
CV "v187,v118,v119\n"
MF "v[88:91],a[204:207],a[116:119],v[88:91]\n"
CV "v186,v116,v117\n"
CV "v185,v114,v115\n"
MF "v[88:91],a[208:211],a[156:159],v[88:91]\n"
TR "a[200:201],v0 offset:32776\n"
CV "v184,v112,v113\n"
MF "v[88:91],a[212:215],a[196:199],v[88:91]\n"
TR "a[202:203],v0 offset:33288\n"
"v_fma_f32 v104,v104,s54,v14\n"
MF "v[80:83],a[212:215],a[192:195],v[80:83]\n"
"v_fma_f32 v105,v105,s54,v14\n"
"v_fma_f32 v106,v106,s54,v14\n"
MF "v[80:83],a[208:211],a[152:155],v[80:83]\n"
TR "a[204:205],v0 offset:36872\n"
"v_fma_f32 v107,v107,s54,v14\n"
MF "v[72:75],a[208:211],a[148:151],v[72:75]\n"
TR "a[206:207],v0 offset:37384\n"
"v_fma_f32 v108,v108,s54,v14\n"
MF "v[72:75],a[212:215],a[188:191],v[72:75]\n"
"v_fma_f32 v109,v109,s54,v14\n"
"v_fma_f32 v110,v110,s54,v14\n"
MF "v[64:67],a[212:215],a[184:187],v[64:67]\n"
"v_fma_f32 v111,v111,s54,v14\n"
CV "v183,v110,v111\n"
MF "v[64:67],a[208:211],a[144:147],v[64:67]\n"
CV "v182,v108,v109\n"
CV "v181,v106,v107\n"
MF "v[56:59],a[208:211],a[140:143],v[56:59]\n"
CV "v180,v104,v105\n"
"v_fma_f32 v96,v96,s54,v14\n"
MF "v[56:59],a[212:215],a[180:183],v[56:59]\n"
"s_waitcnt lgkmcnt(4)\n"
"v_fma_f32 v97,v97,s54,v14\n"
"v_fma_f32 v98,v98,s54,v14\n"
MF "v[20:23],a[216:219],a[60:63],v[20:23]\n"
TR "a[208:209],v0 offset:16384\n"
"v_fma_f32 v99,v99,s54,v14\n"
MF "v[20:23],a[220:223],a[100:103],v[20:23]\n"
TR "a[210:211],v0 offset:16896\n"
"v_fma_f32 v100,v100,s54,v14\n"
MF "v[28:31],a[220:223],a[104:107],v[28:31]\n"
"v_fma_f32 v101,v101,s54,v14\n"
"v_fma_f32 v102,v102,s54,v14\n"
MF "v[28:31],a[216:219],a[64:67],v[28:31]\n"
TR "a[212:213],v0 offset:20480\n"
"v_fma_f32 v103,v103,s54,v14\n"
MF "v[36:39],a[216:219],a[68:71],v[36:39]\n"
TR "a[214:215],v0 offset:20992\n"
CV "v179,v102,v103\n"
MF "v[36:39],a[220:223],a[108:111],v[36:39]\n"
CV "v178,v100,v101\n"
CV "v177,v98,v99\n"
MF "v[44:47],a[220:223],a[112:115],v[44:47]\n"
CV "v176,v96,v97\n"
"v_fma_f32 v88,v88,s54,v14\n"
MF "v[44:47],a[216:219],a[72:75],v[44:47]\n"
"v_fma_f32 v89,v89,s54,v14\n"
"v_fma_f32 v90,v90,s54,v14\n"
MF "v[52:55],a[216:219],a[76:79],v[52:55]\n"
"v_fma_f32 v91,v91,s54,v14\n"
"v_fma_f32 v92,v92,s54,v14\n"
MF "v[52:55],a[220:223],a[116:119],v[52:55]\n"
"s_waitcnt lgkmcnt(4)\n"
"v_fma_f32 v93,v93,s54,v14\n"
"v_fma_f32 v94,v94,s54,v14\n"
MF "v[20:23],a[200:203],a[140:143],v[20:23]\n"
TR "a[216:217],v0 offset:32768\n"
"v_fma_f32 v95,v95,s54,v14\n"
MF "v[20:23],a[204:207],a[180:183],v[20:23]\n"
TR "a[218:219],v0 offset:33280\n"
CV "v175,v94,v95\n"
MF "v[28:31],a[204:207],a[184:187],v[28:31]\n"
CV "v174,v92,v93\n"
CV "v173,v90,v91\n"
MF "v[28:31],a[200:203],a[144:147],v[28:31]\n"
TR "a[220:221],v0 offset:36864\n"
CV "v172,v88,v89\n"
MF "v[36:39],a[200:203],a[148:151],v[36:39]\n"
TR "a[222:223],v0 offset:37376\n"
"v_fma_f32 v80,v80,s54,v14\n"
MF "v[36:39],a[204:207],a[188:191],v[36:39]\n"
"v_fma_f32 v81,v81,s54,v14\n"
"v_fma_f32 v82,v82,s54,v14\n"
MF "v[44:47],a[204:207],a[192:195],v[44:47]\n"
"v_fma_f32 v83,v83,s54,v14\n"
"v_fma_f32 v84,v84,s54,v14\n"
MF "v[44:47],a[200:203],a[152:155],v[44:47]\n"
"v_fma_f32 v85,v85,s54,v14\n"
"v_fma_f32 v86,v86,s54,v14\n"
MF "v[52:55],a[200:203],a[156:159],v[52:55]\n"
"s_waitcnt lgkmcnt(0)\n"
"v_fma_f32 v87,v87,s54,v14\n"
CV "v171,v86,v87\n"
MF "v[52:55],a[204:207],a[196:199],v[52:55]\n"
"s_barrier\n"
MF "v[16:19],a[208:211],a[60:63],v[16:19]\n"
"s_add_u32 m0,s65,49152\n"
CV "v170,v84,v85\n"
CV "v169,v82,v83\n"
MF "v[16:19],a[212:215],a[100:103],v[16:19]\n"
LD "v8,s[36:39],s73 offen nt lds\n"
MF "v[24:27],a[212:215],a[104:107],v[24:27]\n"
"s_add_u32 m0,s65,50048\n"
CV "v168,v80,v81\n"
"v_fma_f32 v72,v72,s54,v14\n"
MF "v[24:27],a[208:211],a[64:67],v[24:27]\n"
LD "v8,s[36:39],s73 offen offset:128 nt lds\n"
MF "v[32:35],a[208:211],a[68:71],v[32:35]\n"
"v_fma_f32 v73,v73,s54,v14\n"
"v_fma_f32 v74,v74,s54,v14\n"
MF "v[32:35],a[212:215],a[108:111],v[32:35]\n"
"s_add_u32 m0,s65,50944\n"
"v_fma_f32 v75,v75,s54,v14\n"
"v_fma_f32 v76,v76,s54,v14\n"
MF "v[40:43],a[212:215],a[112:115],v[40:43]\n"
LD "v8,s[36:39],s73 offen offset:256 nt lds\n"
MF "v[40:43],a[208:211],a[72:75],v[40:43]\n"
"v_fma_f32 v77,v77,s54,v14\n"
"v_fma_f32 v78,v78,s54,v14\n"
MF "v[48:51],a[208:211],a[76:79],v[48:51]\n"
"s_add_u32 m0,s65,49152\n"
"v_fma_f32 v79,v79,s54,v14\n"
CV "v167,v78,v79\n"
MF "v[48:51],a[212:215],a[116:119],v[48:51]\n"
LD "v8,s[36:39],s73 offen offset:3072 nt lds\n"
MF "v[48:51],a[216:219],a[156:159],v[48:51]\n"
"s_add_u32 m0,s65,50048\n"
CV "v166,v76,v77\n"
CV "v165,v74,v75\n"
MF "v[48:51],a[220:223],a[196:199],v[48:51]\n"
LD "v8,s[36:39],s73 offen offset:3200 nt lds\n"
MF "v[40:43],a[220:223],a[192:195],v[40:43]\n"
CV "v164,v72,v73\n"
"v_fma_f32 v64,v64,s54,v14\n"
MF "v[40:43],a[216:219],a[152:155],v[40:43]\n"
"s_add_u32 m0,s65,50944\n"
"v_fma_f32 v65,v65,s54,v14\n"
"v_fma_f32 v66,v66,s54,v14\n"
MF "v[32:35],a[216:219],a[148:151],v[32:35]\n"
LD "v8,s[36:39],s73 offen offset:3328 nt lds\n"
MF "v[32:35],a[220:223],a[188:191],v[32:35]\n"
"v_fma_f32 v67,v67,s54,v14\n"
"v_fma_f32 v68,v68,s54,v14\n"
MF "v[24:27],a[220:223],a[184:187],v[24:27]\n"
"v_fma_f32 v69,v69,s54,v14\n"
"v_fma_f32 v70,v70,s54,v14\n"
MF "v[24:27],a[216:219],a[144:147],v[24:27]\n"
"v_fma_f32 v71,v71,s54,v14\n"
CV "v163,v70,v71\n"
MF "v[16:19],a[216:219],a[140:143],v[16:19]\n"
CV "v162,v68,v69\n"
CV "v161,v66,v67\n"
MF "v[16:19],a[220:223],a[180:183],v[16:19]\n"
CV "v160,v64,v65\n"
AR "v0,a244\n"
"v_xor_b32 v1,64,v0\n"
"s_waitcnt vmcnt(12) lgkmcnt(0)\n"
"s_barrier\n"
"s_nop 0\n"
"ds_read_b128 v[92:95],v0\n"
"ds_read_b128 v[96:99],v1\n"
"ds_read_b128 v[100:103],v0 offset:1024\n"
"v_pk_fma_f32 v[56:57],v[56:57],s[54:55],v[14:15] op_sel_hi:[1,1,0]\n"
"v_pk_fma_f32 v[58:59],v[58:59],s[54:55],v[14:15] op_sel_hi:[1,1,0]\n"
"v_pk_fma_f32 v[60:61],v[60:61],s[54:55],v[14:15] op_sel_hi:[1,1,0]\n"
"v_pk_fma_f32 v[62:63],v[62:63],s[54:55],v[14:15] op_sel_hi:[1,1,0]\n"
CV "v159,v62,v63\n"
CV "v158,v60,v61\n"
CV "v157,v58,v59\n"
CV "v156,v56,v57\n"
"v_pk_mul_f32 v[48:49],v[48:49],s[54:55]\n"
"v_pk_mul_f32 v[50:51],v[50:51],s[54:55]\n"
"v_pk_mul_f32 v[52:53],v[52:53],s[54:55]\n"
"v_pk_mul_f32 v[54:55],v[54:55],s[54:55]\n"
CV "v155,v54,v55\n"
CV "v154,v52,v53\n"
CV "v153,v50,v51\n"
CV "v152,v48,v49\n"
"v_pk_mul_f32 v[40:41],v[40:41],s[54:55]\n"
"v_pk_mul_f32 v[42:43],v[42:43],s[54:55]\n"
"v_pk_mul_f32 v[44:45],v[44:45],s[54:55]\n"
"v_pk_mul_f32 v[46:47],v[46:47],s[54:55]\n"
CV "v151,v46,v47\n"
CV "v150,v44,v45\n"
CV "v149,v42,v43\n"
CV "v148,v40,v41\n"
"v_pk_mul_f32 v[32:33],v[32:33],s[54:55]\n"
"v_pk_mul_f32 v[34:35],v[34:35],s[54:55]\n"
"v_pk_mul_f32 v[36:37],v[36:37],s[54:55]\n"
"v_pk_mul_f32 v[38:39],v[38:39],s[54:55]\n"
CV "v147,v38,v39\n"
CV "v146,v36,v37\n"
CV "v145,v34,v35\n"
CV "v144,v32,v33\n"
"v_pk_mul_f32 v[24:25],v[24:25],s[54:55]\n"
"v_pk_mul_f32 v[26:27],v[26:27],s[54:55]\n"
"v_pk_mul_f32 v[28:29],v[28:29],s[54:55]\n"
"v_pk_mul_f32 v[30:31],v[30:31],s[54:55]\n"
CV "v143,v30,v31\n"
CV "v142,v28,v29\n"
CV "v141,v26,v27\n"
CV "v140,v24,v25\n"
"v_pk_mul_f32 v[16:17],v[16:17],s[54:55]\n"
"v_pk_mul_f32 v[18:19],v[18:19],s[54:55]\n"
"v_pk_mul_f32 v[20:21],v[20:21],s[54:55]\n"
"v_pk_mul_f32 v[22:23],v[22:23],s[54:55]\n"
CV "v139,v22,v23\n"
CV "v138,v20,v21\n"
CV "v137,v18,v19\n"
CV "v136,v16,v17\n"
"s_cmp_eq_u32 s57,s69\n"
"s_cbranch_scc1 .LattB\n"
AR "v2,a245\n"
"v_add_u32 v2,49152,v2\n"
"v_xor_b32 v3,32,v2\n"
"v_xor_b32 v4,64,v2\n"
"v_xor_b32 v5,96,v2\n"
"s_mov_b32 s61,7\n"
".Latt2x:\n"
"s_cmp_eq_u32 s57,1\n"
"s_cbranch_scc1 .Lt0kx\n"
".Lt0nx:\n"
"s_waitcnt lgkmcnt(2)\n"
MF "v[72:75],v[92:95],v[136:139],0\n"
"s_add_u32 s73,s73,12288\n"
"ds_read_b128 v[104:107],v1 offset:1024\n"
AR "v10,a224\n"
MF "v[76:79],v[92:95],v[140:143],0\n"
"s_cmp_eq_u32 s73,s19\n"
"s_cselect_b32 s73,s18,s73\n"
"s_add_u32 m0,s65,73728\n"
MF "v[80:83],v[92:95],v[144:147],0\n"
LD "v8,s[36:39],s73 offen nt lds\n"
MF "v[84:87],v[92:95],v[148:151],0\n"
"v_max_f32_dpp v10,|v10|,|v10| quad_perm:[1,0,3,2] row_mask:0xf bank_mask:0xf\n"
AR "v11,a225\n"
MF "v[88:91],v[92:95],v[152:155],0\n"
"s_waitcnt lgkmcnt(2)\n"
"ds_read_b128 v[92:95],v0 offset:2048\n"
AR "v12,a226\n"
MF "v[72:75],v[96:99],v[156:159],v[72:75]\n"
"s_add_u32 m0,s65,74624\n"
"v_max_f32_dpp v10,v10,v10 quad_perm:[2,3,0,1] row_mask:0xf bank_mask:0xf\n"
"v_max_f32_dpp v11,|v11|,|v11| quad_perm:[1,0,3,2] row_mask:0xf bank_mask:0xf\n"
MF "v[76:79],v[96:99],v[160:163],v[76:79]\n"
LD "v8,s[36:39],s73 offen offset:128 nt lds\n"
MF "v[80:83],v[96:99],v[164:167],v[80:83]\n"
"v_max_f32_dpp v10,v10,v10 row_half_mirror row_mask:0xf bank_mask:0xf\n"
"v_max_f32_dpp v11,v11,v11 quad_perm:[2,3,0,1] row_mask:0xf bank_mask:0xf\n"
MF "v[84:87],v[96:99],v[168:171],v[84:87]\n"
"v_readlane_b32 s13,v10,0\n"
"v_readlane_b32 s0,v10,24\n"
MF "v[88:91],v[96:99],v[172:175],v[88:91]\n"
"s_waitcnt lgkmcnt(2)\n"
"ds_read_b128 v[96:99],v1 offset:2048\n"
"v_readlane_b32 s1,v10,32\n"
MF "v[72:75],v[100:103],v[176:179],v[72:75]\n"
"s_add_u32 m0,s65,75520\n"
"v_readlane_b32 s2,v10,56\n"
"v_max_f32_dpp v11,v11,v11 row_half_mirror row_mask:0xf bank_mask:0xf\n"
MF "v[76:79],v[100:103],v[180:183],v[76:79]\n"
LD "v8,s[36:39],s73 offen offset:256 nt lds\n"
MF "v[80:83],v[100:103],v[184:187],v[80:83]\n"
"s_min_u32 s13,s13,s0\n"
"v_readlane_b32 s11,v11,0\n"
"v_readlane_b32 s3,v11,24\n"
MF "v[84:87],v[100:103],v[188:191],v[84:87]\n"
"s_min_u32 s1,s1,s2\n"
"v_readlane_b32 s4,v11,32\n"
"v_readlane_b32 s5,v11,56\n"
MF "v[88:91],v[100:103],v[192:195],v[88:91]\n"
"s_waitcnt lgkmcnt(2)\n"
"ds_read_b128 v[100:103],v0 offset:6144\n"
"v_max_f32_dpp v12,|v12|,|v12| quad_perm:[1,0,3,2] row_mask:0xf bank_mask:0xf\n"
MF "v[72:75],v[104:107],v[196:199],v[72:75]\n"
"s_add_u32 m0,s65,73728\n"
MF "v[76:79],v[104:107],v[200:203],v[76:79]\n"
LD "v8,s[36:39],s73 offen offset:3072 nt lds\n"
MF "v[80:83],v[104:107],v[204:207],v[80:83]\n"
"s_min_u32 s13,s13,s1\n"
"v_max_f32_dpp v12,v12,v12 quad_perm:[2,3,0,1] row_mask:0xf bank_mask:0xf\n"
MF "v[84:87],v[104:107],v[208:211],v[84:87]\n"
"s_and_b32 s13,s13,0x7f800000\n"
MF "v[88:91],v[104:107],v[212:215],v[88:91]\n"
"s_waitcnt lgkmcnt(2)\n"
"ds_read_b128 v[104:107],v1 offset:6144\n"
"v_max_f32_dpp v12,v12,v12 row_half_mirror row_mask:0xf bank_mask:0xf\n"
MF "v[72:75],v[92:95],v[216:219],v[72:75]\n"
"s_add_u32 m0,s65,74624\n"
"v_readlane_b32 s10,v12,0\n"
"v_readlane_b32 s6,v12,24\n"
MF "v[76:79],v[92:95],v[220:223],v[76:79]\n"
LD "v8,s[36:39],s73 offen offset:3200 nt lds\n"
MF "v[80:83],v[92:95],v[224:227],v[80:83]\n"
"s_sub_u32 s13,s13,0x14000000\n"
"v_readlane_b32 s7,v12,32\n"
"v_readlane_b32 s8,v12,56\n"
MF "v[84:87],v[92:95],v[228:231],v[84:87]\n"
"s_cmp_lt_u32 s13,0x50000000\n"
"s_cselect_b32 s13,s13,0xed400000\n"
MF "v[88:91],v[92:95],v[232:235],v[88:91]\n"
"s_waitcnt lgkmcnt(2)\n"
"ds_read_b128 v[92:95],v0 offset:7168\n"
MF "v[72:75],v[96:99],v[236:239],v[72:75]\n"
"s_add_u32 m0,s65,75520\n"
MF "v[76:79],v[96:99],v[240:243],v[76:79]\n"
LD "v8,s[36:39],s73 offen offset:3328 nt lds\n"
MF "v[80:83],v[96:99],v[244:247],v[80:83]\n"
"s_add_u32 s13,s13,0x12c00000\n"
MF "v[84:87],v[96:99],v[248:251],v[84:87]\n"
"s_min_u32 s11,s11,s3\n"
MF "v[88:91],v[96:99],v[252:255],v[88:91]\n"
"s_waitcnt lgkmcnt(2)\n"
"ds_read_b128 v[96:99],v1 offset:7168\n"
MF "v[112:115],v[100:103],v[136:139],0\n"
"s_min_u32 s4,s4,s5\n"
MF "v[116:119],v[100:103],v[140:143],0\n"
"s_min_u32 s11,s11,s4\n"
MF "v[120:123],v[100:103],v[144:147],0\n"
"s_and_b32 s11,s11,0x7f800000\n"
MF "v[124:127],v[100:103],v[148:151],0\n"
"s_sub_u32 s11,s11,0x14000000\n"
MF "v[128:131],v[100:103],v[152:155],0\n"
"s_waitcnt lgkmcnt(2)\n"
"ds_read_b128 v[100:103],v0 offset:8192\n"
MF "v[112:115],v[104:107],v[156:159],v[112:115]\n"
"s_min_u32 s10,s10,s6\n"
"v_max3_f32 v52,v72,v73,v74\n"
"v_max_f32 v52,v52,v75\n"
MF "v[116:119],v[104:107],v[160:163],v[116:119]\n"
"s_min_u32 s7,s7,s8\n"
"v_max3_f32 v53,v76,v77,v78\n"
"v_max_f32 v53,v53,v79\n"
MF "v[120:123],v[104:107],v[164:167],v[120:123]\n"
"s_min_u32 s10,s10,s7\n"
"v_max3_f32 v54,v80,v81,v82\n"
"v_max_f32 v54,v54,v83\n"
MF "v[124:127],v[104:107],v[168:171],v[124:127]\n"
"v_max3_f32 v55,v84,v85,v86\n"
"v_max_f32 v55,v55,v87\n"
MF "v[128:131],v[104:107],v[172:175],v[128:131]\n"
"s_waitcnt lgkmcnt(2)\n"
"ds_read_b128 v[104:107],v1 offset:8192\n"
"v_max3_f32 v56,v88,v89,v90\n"
MF "v[112:115],v[92:95],v[176:179],v[112:115]\n"
".Lpg9b:\n"
"s_cbranch_execz .Lpgpsl_b\n"
"s_cmp_lt_u32 s11,0x50000000\n"
"s_cselect_b32 s11,s11,0xed400000\n"
"v_max_f32 v56,v56,v91\n"
MF "v[116:119],v[92:95],v[180:183],v[116:119]\n"
"s_add_u32 s11,s11,0x12c00000\n"
MF "v[120:123],v[92:95],v[184:187],v[120:123]\n"
MF "v[124:127],v[92:95],v[188:191],v[124:127]\n"
MF "v[128:131],v[92:95],v[192:195],v[128:131]\n"
"s_waitcnt lgkmcnt(2)\n"
MF "v[112:115],v[96:99],v[196:199],v[112:115]\n"
"s_waitcnt lgkmcnt(0)\n"
MF "v[116:119],v[96:99],v[200:203],v[116:119]\n"
"s_barrier\n"
MF "v[120:123],v[96:99],v[204:207],v[120:123]\n"
"s_add_u32 s72,s72,6144\n"
MF "v[124:127],v[96:99],v[208:211],v[124:127]\n"
"s_cmp_eq_u32 s72,s17\n"
"s_cselect_b32 s72,s16,s72\n"
"s_add_u32 m0,s64,0\n"
MF "v[128:131],v[96:99],v[212:215],v[128:131]\n"
LD "v7,s[32:35],s72 offen nt lds\n"
MF "v[112:115],v[100:103],v[216:219],v[112:115]\n"
"s_add_u32 m0,s64,896\n"
MF "v[116:119],v[100:103],v[220:223],v[116:119]\n"
LD "v7,s[32:35],s72 offen offset:128 nt lds\n"
MF "v[120:123],v[100:103],v[224:227],v[120:123]\n"
"s_add_u32 m0,s64,1792\n"
MF "v[124:127],v[100:103],v[228:231],v[124:127]\n"
LD "v7,s[32:35],s72 offen offset:256 nt lds\n"
MF "v[128:131],v[100:103],v[232:235],v[128:131]\n"
"s_add_u32 s72,s72,6144\n"
"s_add_u32 m0,s64,6144\n"
MF "v[112:115],v[104:107],v[236:239],v[112:115]\n"
LD "v7,s[32:35],s72 offen nt lds\n"
MF "v[116:119],v[104:107],v[240:243],v[116:119]\n"
"s_add_u32 m0,s64,7040\n"
MF "v[120:123],v[104:107],v[244:247],v[120:123]\n"
LD "v7,s[32:35],s72 offen offset:128 nt lds\n"
MF "v[124:127],v[104:107],v[248:251],v[124:127]\n"
"s_add_u32 m0,s64,7936\n"
MF "v[128:131],v[104:107],v[252:255],v[128:131]\n"
LD "v7,s[32:35],s72 offen offset:256 nt lds\n"
"s_branch .Lt0ex\n"
".Lpgt0kx_d:\n"
"s_cbranch_execz .Lisr2\n"
".Lt0kx:\n"
"s_lshl_b32 s58,s56,10\n"
"s_add_u32 s58,s58,73728\n"
"v_add_u32 v32,s58,v9\n"
"v_add_u32 v33,65536,v32\n"
"s_waitcnt lgkmcnt(2)\n"
MF "v[72:75],v[92:95],v[136:139],0\n"
"s_add_u32 s73,s73,12288\n"
"ds_read_b128 v[104:107],v1 offset:1024\n"
AR "v10,a224\n"
MF "v[76:79],v[92:95],v[140:143],0\n"
"s_cmp_eq_u32 s73,s19\n"
"s_cselect_b32 s73,s18,s73\n"
"s_add_u32 m0,s65,73728\n"
MF "v[80:83],v[92:95],v[144:147],0\n"
LD "v8,s[36:39],s73 offen nt lds\n"
MF "v[84:87],v[92:95],v[148:151],0\n"
"v_max_f32_dpp v10,|v10|,|v10| quad_perm:[1,0,3,2] row_mask:0xf bank_mask:0xf\n"
AR "v11,a225\n"
MF "v[88:91],v[92:95],v[152:155],0\n"
"s_waitcnt lgkmcnt(2)\n"
"ds_read_b128 v[92:95],v0 offset:2048\n"
AR "v12,a226\n"
MF "v[72:75],v[96:99],v[156:159],v[72:75]\n"
"s_add_u32 m0,s65,74624\n"
"v_max_f32_dpp v10,v10,v10 quad_perm:[2,3,0,1] row_mask:0xf bank_mask:0xf\n"
"v_max_f32_dpp v11,|v11|,|v11| quad_perm:[1,0,3,2] row_mask:0xf bank_mask:0xf\n"
MF "v[76:79],v[96:99],v[160:163],v[76:79]\n"
LD "v8,s[36:39],s73 offen offset:128 nt lds\n"
MF "v[80:83],v[96:99],v[164:167],v[80:83]\n"
"v_max_f32_dpp v10,v10,v10 row_half_mirror row_mask:0xf bank_mask:0xf\n"
"v_max_f32_dpp v11,v11,v11 quad_perm:[2,3,0,1] row_mask:0xf bank_mask:0xf\n"
MF "v[84:87],v[96:99],v[168:171],v[84:87]\n"
"v_readlane_b32 s13,v10,0\n"
"v_readlane_b32 s0,v10,24\n"
MF "v[88:91],v[96:99],v[172:175],v[88:91]\n"
"s_waitcnt lgkmcnt(2)\n"
"ds_read_b128 v[96:99],v1 offset:2048\n"
"v_readlane_b32 s1,v10,32\n"
MF "v[72:75],v[100:103],v[176:179],v[72:75]\n"
"s_add_u32 m0,s65,75520\n"
"v_readlane_b32 s2,v10,56\n"
"v_max_f32_dpp v11,v11,v11 row_half_mirror row_mask:0xf bank_mask:0xf\n"
MF "v[76:79],v[100:103],v[180:183],v[76:79]\n"
LD "v8,s[36:39],s73 offen offset:256 nt lds\n"
MF "v[80:83],v[100:103],v[184:187],v[80:83]\n"
"s_min_u32 s13,s13,s0\n"
"v_readlane_b32 s11,v11,0\n"
"v_readlane_b32 s3,v11,24\n"
MF "v[84:87],v[100:103],v[188:191],v[84:87]\n"
"s_min_u32 s1,s1,s2\n"
"v_readlane_b32 s4,v11,32\n"
"v_readlane_b32 s5,v11,56\n"
MF "v[88:91],v[100:103],v[192:195],v[88:91]\n"
"s_waitcnt lgkmcnt(2)\n"
"ds_read_b128 v[100:103],v0 offset:6144\n"
"v_max_f32_dpp v12,|v12|,|v12| quad_perm:[1,0,3,2] row_mask:0xf bank_mask:0xf\n"
MF "v[72:75],v[104:107],v[196:199],v[72:75]\n"
"s_add_u32 m0,s65,73728\n"
"ds_write_b128 v32,a[0:3] offset:24576\n"
MF "v[76:79],v[104:107],v[200:203],v[76:79]\n"
LD "v8,s[36:39],s73 offen offset:3072 nt lds\n"
MF "v[80:83],v[104:107],v[204:207],v[80:83]\n"
"s_min_u32 s13,s13,s1\n"
"v_max_f32_dpp v12,v12,v12 quad_perm:[2,3,0,1] row_mask:0xf bank_mask:0xf\n"
AR "v62,a20\n"
MF "v[84:87],v[104:107],v[208:211],v[84:87]\n"
"s_and_b32 s13,s13,0x7f800000\n"
"ds_write_b128 v32,a[4:7] offset:28672\n"
MF "v[88:91],v[104:107],v[212:215],v[88:91]\n"
"s_waitcnt lgkmcnt(4)\n"
"ds_read_b128 v[104:107],v1 offset:6144\n"
"v_max_f32_dpp v12,v12,v12 row_half_mirror row_mask:0xf bank_mask:0xf\n"
MF "v[72:75],v[92:95],v[216:219],v[72:75]\n"
"s_add_u32 m0,s65,74624\n"
"v_readlane_b32 s10,v12,0\n"
"v_readlane_b32 s6,v12,24\n"
MF "v[76:79],v[92:95],v[220:223],v[76:79]\n"
LD "v8,s[36:39],s73 offen offset:3200 nt lds\n"
MF "v[80:83],v[92:95],v[224:227],v[80:83]\n"
"s_sub_u32 s13,s13,0x14000000\n"
"v_readlane_b32 s7,v12,32\n"
"v_readlane_b32 s8,v12,56\n"
MF "v[84:87],v[92:95],v[228:231],v[84:87]\n"
"s_cmp_lt_u32 s13,0x50000000\n"
"s_cselect_b32 s13,s13,0xed400000\n"
AR "v63,a21\n"
MF "v[88:91],v[92:95],v[232:235],v[88:91]\n"
"s_waitcnt lgkmcnt(4)\n"
"ds_read_b128 v[92:95],v0 offset:7168\n"
AR "v64,a22\n"
MF "v[72:75],v[96:99],v[236:239],v[72:75]\n"
"s_add_u32 m0,s65,75520\n"
"ds_write_b128 v32,a[8:11] offset:32768\n"
MF "v[76:79],v[96:99],v[240:243],v[76:79]\n"
LD "v8,s[36:39],s73 offen offset:3328 nt lds\n"
MF "v[80:83],v[96:99],v[244:247],v[80:83]\n"
"s_add_u32 s13,s13,0x12c00000\n"
"ds_write_b128 v32,a[12:15] offset:36864\n"
MF "v[84:87],v[96:99],v[248:251],v[84:87]\n"
"s_min_u32 s11,s11,s3\n"
"ds_write_b128 v32,a[16:19] offset:40960\n"
MF "v[88:91],v[96:99],v[252:255],v[88:91]\n"
"s_waitcnt lgkmcnt(7)\n"
"ds_read_b128 v[96:99],v1 offset:7168\n"
AR "v65,a23\n"
MF "v[112:115],v[100:103],v[136:139],0\n"
"s_min_u32 s4,s4,s5\n"
"ds_write_b128 v32,a[40:43] offset:45056\n"
MF "v[116:119],v[100:103],v[140:143],0\n"
"s_min_u32 s11,s11,s4\n"
"ds_write_b128 v32,a[44:47] offset:49152\n"
MF "v[120:123],v[100:103],v[144:147],0\n"
"s_and_b32 s11,s11,0x7f800000\n"
"ds_write_b128 v32,a[48:51] offset:53248\n"
MF "v[124:127],v[100:103],v[148:151],0\n"
"s_sub_u32 s11,s11,0x14000000\n"
"ds_write_b128 v32,a[52:55] offset:57344\n"
MF "v[128:131],v[100:103],v[152:155],0\n"
"s_waitcnt lgkmcnt(9)\n"
"ds_read_b128 v[100:103],v0 offset:8192\n"
AR "v66,a24\n"
MF "v[112:115],v[104:107],v[156:159],v[112:115]\n"
"s_min_u32 s10,s10,s6\n"
"v_max3_f32 v52,v72,v73,v74\n"
"v_max_f32 v52,v52,v75\n"
MF "v[116:119],v[104:107],v[160:163],v[116:119]\n"
"s_min_u32 s7,s7,s8\n"
"v_max3_f32 v53,v76,v77,v78\n"
"v_max_f32 v53,v53,v79\n"
MF "v[120:123],v[104:107],v[164:167],v[120:123]\n"
"s_min_u32 s10,s10,s7\n"
"v_max3_f32 v54,v80,v81,v82\n"
"v_max_f32 v54,v54,v83\n"
MF "v[124:127],v[104:107],v[168:171],v[124:127]\n"
"v_max3_f32 v55,v84,v85,v86\n"
"v_max_f32 v55,v55,v87\n"
MF "v[128:131],v[104:107],v[172:175],v[128:131]\n"
"s_waitcnt lgkmcnt(9)\n"
"ds_read_b128 v[104:107],v1 offset:8192\n"
"v_max3_f32 v56,v88,v89,v90\n"
MF "v[112:115],v[92:95],v[176:179],v[112:115]\n"
"s_cmp_lt_u32 s11,0x50000000\n"
"s_cselect_b32 s11,s11,0xed400000\n"
"v_max_f32 v56,v56,v91\n"
MF "v[116:119],v[92:95],v[180:183],v[116:119]\n"
"s_add_u32 s11,s11,0x12c00000\n"
"ds_write_b128 v32,a[56:59] offset:61440\n"
MF "v[120:123],v[92:95],v[184:187],v[120:123]\n"
"ds_write_b128 v33,a[80:83]\n"
MF "v[124:127],v[92:95],v[188:191],v[124:127]\n"
"ds_write_b128 v33,a[84:87] offset:4096\n"
MF "v[128:131],v[92:95],v[192:195],v[128:131]\n"
"s_waitcnt lgkmcnt(9)\n"
"ds_write_b128 v33,a[88:91] offset:8192\n"
MF "v[112:115],v[96:99],v[196:199],v[112:115]\n"
"s_waitcnt lgkmcnt(0)\n"
AR "v67,a25\n"
AR "v68,a26\n"
MF "v[116:119],v[96:99],v[200:203],v[116:119]\n"
"s_barrier\n"
MF "v[120:123],v[96:99],v[204:207],v[120:123]\n"
"s_add_u32 s72,s72,6144\n"
"ds_write_b128 v33,a[92:95] offset:12288\n"
MF "v[124:127],v[96:99],v[208:211],v[124:127]\n"
"s_cmp_eq_u32 s72,s17\n"
"s_cselect_b32 s72,s16,s72\n"
"s_add_u32 m0,s64,0\n"
MF "v[128:131],v[96:99],v[212:215],v[128:131]\n"
LD "v7,s[32:35],s72 offen nt lds\n"
MF "v[112:115],v[100:103],v[216:219],v[112:115]\n"
"s_add_u32 m0,s64,896\n"
"ds_write_b128 v33,a[96:99] offset:16384\n"
MF "v[116:119],v[100:103],v[220:223],v[116:119]\n"
LD "v7,s[32:35],s72 offen offset:128 nt lds\n"
MF "v[120:123],v[100:103],v[224:227],v[120:123]\n"
"s_add_u32 m0,s64,1792\n"
AR "v69,a27\n"
AR "v70,a28\n"
MF "v[124:127],v[100:103],v[228:231],v[124:127]\n"
LD "v7,s[32:35],s72 offen offset:256 nt lds\n"
MF "v[128:131],v[100:103],v[232:235],v[128:131]\n"
"s_add_u32 s72,s72,6144\n"
"s_add_u32 m0,s64,6144\n"
AR "v71,a29\n"
MF "v[112:115],v[104:107],v[236:239],v[112:115]\n"
LD "v7,s[32:35],s72 offen nt lds\n"
MF "v[116:119],v[104:107],v[240:243],v[116:119]\n"
"s_add_u32 m0,s64,7040\n"
"ds_write_b128 v33,a[120:123] offset:20480\n"
MF "v[120:123],v[104:107],v[244:247],v[120:123]\n"
LD "v7,s[32:35],s72 offen offset:128 nt lds\n"
MF "v[124:127],v[104:107],v[248:251],v[124:127]\n"
"s_add_u32 m0,s64,7936\n"
AR "v132,a30\n"
AR "v133,a31\n"
MF "v[128:131],v[104:107],v[252:255],v[128:131]\n"
LD "v7,s[32:35],s72 offen offset:256 nt lds\n"
AR "v134,a32\n"
AR "v135,a33\n"
AR "v6,a34\n"
AR "v13,a35\n"
AR "v14,a36\n"
AR "v15,a37\n"
"v_accvgpr_mov_b32 a254,a38\n"
"v_accvgpr_mov_b32 a255,a39\n"
"v_accvgpr_mov_b32 a240,a168\n"
"v_accvgpr_mov_b32 a241,a169\n"
"v_accvgpr_mov_b32 a242,a170\n"
"v_accvgpr_mov_b32 a243,a171\n"
"v_accvgpr_mov_b32 a244,a172\n"
"v_accvgpr_mov_b32 a245,a173\n"
"v_accvgpr_mov_b32 a246,a174\n"
"v_accvgpr_mov_b32 a247,a175\n"
"v_accvgpr_mov_b32 a248,a176\n"
"v_accvgpr_mov_b32 a249,a177\n"
"v_accvgpr_mov_b32 a250,a178\n"
"v_accvgpr_mov_b32 a251,a179\n"
".Lpgt0ex_b:\n"
"s_cbranch_execz .Lpg2a\n"
".Lt0ex:\n"
"s_nop 1\n"
"v_max3_f32 v52,v52,v112,v113\n"
"v_max3_f32 v53,v53,v116,v117\n"
"v_max3_f32 v54,v54,v120,v121\n"
"v_max3_f32 v55,v55,v124,v125\n"
"v_max3_f32 v56,v56,v128,v129\n"
"v_max3_f32 v52,v52,v114,v115\n"
"v_max3_f32 v53,v53,v118,v119\n"
"v_max3_f32 v54,v54,v122,v123\n"
"v_max3_f32 v55,v55,v126,v127\n"
"v_max3_f32 v56,v56,v130,v131\n"
"v_mov_b32 v57,v52\n"
"v_mov_b32 v58,v53\n"
"v_mov_b32 v59,v54\n"
"v_mov_b32 v60,v55\n"
"v_mov_b32 v61,v56\n"
"v_permlane16_swap_b32 v52,v57\n"
"v_permlane16_swap_b32 v53,v58\n"
"v_permlane16_swap_b32 v54,v59\n"
"v_permlane16_swap_b32 v55,v60\n"
"v_permlane16_swap_b32 v56,v61\n"
"v_max_f32 v52,v52,v57\n"
"v_max_f32 v53,v53,v58\n"
"v_max_f32 v54,v54,v59\n"
"v_max_f32 v55,v55,v60\n"
"v_max_f32 v56,v56,v61\n"
"v_mov_b32 v57,v52\n"
"v_mov_b32 v58,v53\n"
"v_mov_b32 v59,v54\n"
"v_mov_b32 v60,v55\n"
"v_mov_b32 v61,v56\n"
"v_permlane32_swap_b32 v52,v57\n"
"v_permlane32_swap_b32 v53,v58\n"
"v_permlane32_swap_b32 v54,v59\n"
"v_permlane32_swap_b32 v55,v60\n"
"v_permlane32_swap_b32 v56,v61\n"
"v_max_f32 v52,v52,v57\n"
"v_max_f32 v53,v53,v58\n"
"v_max_f32 v54,v54,v59\n"
"v_max_f32 v55,v55,v60\n"
"v_max_f32 v56,v56,v61\n"
"v_sub_f32 v32,0xc1300000,v52\n"
"v_sub_f32 v36,0xc1300000,v53\n"
"v_sub_f32 v40,0xc1300000,v54\n"
"v_sub_f32 v44,0xc1300000,v55\n"
"v_sub_f32 v48,0xc1300000,v56\n"
"v_mov_b32 v33,v32\n"
"v_pk_mov_b32 v[34:35],v[32:33],v[32:33]\n"
"v_mov_b32 v37,v36\n"
"v_pk_mov_b32 v[38:39],v[36:37],v[36:37]\n"
"v_mov_b32 v41,v40\n"
"v_pk_mov_b32 v[42:43],v[40:41],v[40:41]\n"
"v_mov_b32 v45,v44\n"
"v_pk_mov_b32 v[46:47],v[44:45],v[44:45]\n"
"v_mov_b32_e64 v49,v48\n"
"v_pk_mov_b32 v[50:51],v[48:49],v[48:49]\n"
"v_pk_add_f32 v[72:73],v[72:73],v[32:33]\n"
"v_pk_add_f32 v[74:75],v[74:75],v[32:33]\n"
"v_pk_add_f32 v[112:113],v[112:113],v[32:33]\n"
"v_pk_add_f32 v[114:115],v[114:115],v[32:33]\n"
"v_exp_f32 v72,v72\n"
"v_exp_f32 v73,v73\n"
"v_exp_f32 v74,v74\n"
"v_exp_f32 v75,v75\n"
"v_exp_f32 v112,v112\n"
"v_exp_f32 v113,v113\n"
"v_exp_f32 v114,v114\n"
"v_exp_f32 v115,v115\n"
"v_pk_add_f32 v[10:11],v[72:73],v[74:75]\n"
"v_pk_add_f32 v[28:29],v[112:113],v[114:115]\n"
"v_pk_add_f32 v[10:11],v[10:11],v[28:29]\n"
"v_add_f32_e64 v52,v10,v11\n"
CV "v72,v72,v73\n"
CV "v73,v74,v75\n"
CV "v74,v112,v113\n"
CV "v75,v114,v115\n"
"v_pk_add_f32 v[76:77],v[76:77],v[36:37]\n"
"v_pk_add_f32 v[78:79],v[78:79],v[36:37]\n"
"v_pk_add_f32 v[116:117],v[116:117],v[36:37]\n"
"v_pk_add_f32 v[118:119],v[118:119],v[36:37]\n"
"v_exp_f32 v76,v76\n"
"v_exp_f32 v77,v77\n"
"v_exp_f32 v78,v78\n"
"v_exp_f32 v79,v79\n"
"v_exp_f32 v116,v116\n"
"v_exp_f32 v117,v117\n"
"v_exp_f32 v118,v118\n"
"v_exp_f32 v119,v119\n"
"v_pk_add_f32 v[10:11],v[76:77],v[78:79]\n"
"v_pk_add_f32 v[28:29],v[116:117],v[118:119]\n"
"v_pk_add_f32 v[10:11],v[10:11],v[28:29]\n"
"v_add_f32_e64 v53,v10,v11\n"
CV "v76,v76,v77\n"
CV "v77,v78,v79\n"
CV "v78,v116,v117\n"
CV "v79,v118,v119\n"
"v_pk_add_f32 v[80:81],v[80:81],v[40:41]\n"
"v_pk_add_f32 v[82:83],v[82:83],v[40:41]\n"
"v_pk_add_f32 v[120:121],v[120:121],v[40:41]\n"
"v_pk_add_f32 v[122:123],v[122:123],v[40:41]\n"
"v_exp_f32 v80,v80\n"
"v_exp_f32 v81,v81\n"
"v_exp_f32 v82,v82\n"
"v_exp_f32 v83,v83\n"
"v_exp_f32 v120,v120\n"
"v_exp_f32 v121,v121\n"
"v_exp_f32 v122,v122\n"
"v_exp_f32 v123,v123\n"
"v_pk_add_f32 v[10:11],v[80:81],v[82:83]\n"
"v_pk_add_f32 v[28:29],v[120:121],v[122:123]\n"
"v_pk_add_f32 v[10:11],v[10:11],v[28:29]\n"
"v_add_f32_e64 v54,v10,v11\n"
CV "v80,v80,v81\n"
CV "v81,v82,v83\n"
CV "v82,v120,v121\n"
CV "v83,v122,v123\n"
"v_pk_add_f32 v[84:85],v[84:85],v[44:45]\n"
"v_pk_add_f32 v[86:87],v[86:87],v[44:45]\n"
"v_pk_add_f32 v[124:125],v[124:125],v[44:45]\n"
"v_pk_add_f32 v[126:127],v[126:127],v[44:45]\n"
"v_mov_b32_e64 v55,0\n"
"v_pk_add_f32 v[88:89],v[88:89],v[48:49]\n"
"v_pk_add_f32 v[90:91],v[90:91],v[48:49]\n"
"v_pk_add_f32 v[128:129],v[128:129],v[48:49]\n"
"v_pk_add_f32 v[130:131],v[130:131],v[48:49]\n"
"v_mov_b32 v56,0\n"
"s_waitcnt vmcnt(18) lgkmcnt(0)\n"
"s_barrier\n"
"ds_read_b128 v[16:19],v0 offset:24576\n"
"ds_read_b128 v[20:23],v1 offset:24576\n"
AW "a220,s13\n"
AW "a221,s13\n"
AW "a222,s13\n"
AW "a223,s13\n"
"s_waitcnt lgkmcnt(1)\n"
MF "v[92:95],v[16:19],v[136:139],v[32:35]\n"
"s_nop 0\n"
"ds_read_b128 v[24:27],v0 offset:25600\n"
"ds_read_b128 v[28:31],v1 offset:25600\n"
"s_waitcnt lgkmcnt(2)\n"
MF "v[92:95],v[20:23],v[156:159],v[92:95]\n"
"v_exp_f32 v84,v84\n"
MF "v[96:99],v[20:23],v[160:163],v[36:39]\n"
"v_exp_f32 v85,v85\n"
MF "v[96:99],v[16:19],v[140:143],v[96:99]\n"
"v_exp_f32 v86,v86\n"
MF "v[100:103],v[16:19],v[144:147],v[40:43]\n"
"v_exp_f32 v87,v87\n"
MF "v[100:103],v[20:23],v[164:167],v[100:103]\n"
"v_add_f32 v55,v55,v84\n"
"v_add_f32 v55,v55,v85\n"
MF "v[104:107],v[20:23],v[168:171],v[44:47]\n"
CV "v84,v84,v85\n"
"v_add_f32 v55,v55,v86\n"
MF "v[104:107],v[16:19],v[148:151],v[104:107]\n"
"v_add_f32 v55,v55,v87\n"
CV "v85,v86,v87\n"
MF "v[108:111],v[16:19],v[152:155],v[48:51]\n"
"v_exp_f32 v124,v124\n"
MF "v[108:111],v[20:23],v[172:175],v[108:111]\n"
"s_waitcnt lgkmcnt(0)\n"
"ds_read_b128 v[16:19],v0 offset:26624\n"
"ds_read_b128 v[20:23],v1 offset:26624\n"
MF "v[108:111],v[24:27],v[192:195],v[108:111]\n"
"v_exp_f32 v125,v125\n"
MF "v[108:111],v[28:31],v[212:215],v[108:111]\n"
"v_exp_f32 v126,v126\n"
MF "v[104:107],v[28:31],v[208:211],v[104:107]\n"
"v_exp_f32 v127,v127\n"
MF "v[104:107],v[24:27],v[188:191],v[104:107]\n"
"v_add_f32 v55,v55,v124\n"
"v_add_f32 v55,v55,v125\n"
MF "v[100:103],v[24:27],v[184:187],v[100:103]\n"
CV "v86,v124,v125\n"
"v_add_f32 v55,v55,v126\n"
MF "v[100:103],v[28:31],v[204:207],v[100:103]\n"
"v_add_f32 v55,v55,v127\n"
CV "v87,v126,v127\n"
MF "v[96:99],v[28:31],v[200:203],v[96:99]\n"
"v_exp_f32 v88,v88\n"
MF "v[96:99],v[24:27],v[180:183],v[96:99]\n"
"v_exp_f32 v89,v89\n"
MF "v[92:95],v[24:27],v[176:179],v[92:95]\n"
"v_exp_f32 v90,v90\n"
MF "v[92:95],v[28:31],v[196:199],v[92:95]\n"
"s_waitcnt lgkmcnt(0)\n"
"ds_read_b128 v[24:27],v0 offset:30720\n"
"ds_read_b128 v[28:31],v1 offset:30720\n"
MF "v[92:95],v[16:19],v[216:219],v[92:95]\n"
"v_exp_f32 v91,v91\n"
MF "v[92:95],v[20:23],v[236:239],v[92:95]\n"
"v_add_f32 v56,v56,v88\n"
"v_add_f32 v56,v56,v89\n"
MF "v[96:99],v[20:23],v[240:243],v[96:99]\n"
CV "v88,v88,v89\n"
"v_add_f32 v56,v56,v90\n"
MF "v[96:99],v[16:19],v[220:223],v[96:99]\n"
"v_add_f32 v56,v56,v91\n"
CV "v89,v90,v91\n"
MF "v[100:103],v[16:19],v[224:227],v[100:103]\n"
"v_exp_f32 v128,v128\n"
MF "v[100:103],v[20:23],v[244:247],v[100:103]\n"
"v_exp_f32 v129,v129\n"
MF "v[104:107],v[20:23],v[248:251],v[104:107]\n"
"v_exp_f32 v130,v130\n"
MF "v[104:107],v[16:19],v[228:231],v[104:107]\n"
"v_exp_f32 v131,v131\n"
MF "v[108:111],v[16:19],v[232:235],v[108:111]\n"
"v_add_f32 v56,v56,v128\n"
"v_add_f32 v56,v56,v129\n"
MF "v[108:111],v[20:23],v[252:255],v[108:111]\n"
"s_waitcnt lgkmcnt(0)\n"
"ds_read_b128 v[16:19],v0 offset:31744\n"
"ds_read_b128 v[20:23],v1 offset:31744\n"
MF "v[112:115],v[24:27],v[136:139],v[32:35]\n"
CV "v90,v128,v129\n"
"v_add_f32 v56,v56,v130\n"
MF "v[112:115],v[28:31],v[156:159],v[112:115]\n"
"v_add_f32 v56,v56,v131\n"
CV "v91,v130,v131\n"
MF "v[116:119],v[28:31],v[160:163],v[36:39]\n"
"v_exp_f32 v92,v92\n"
MF "v[116:119],v[24:27],v[140:143],v[116:119]\n"
"v_exp_f32 v93,v93\n"
MF "v[120:123],v[24:27],v[144:147],v[40:43]\n"
"v_exp_f32 v94,v94\n"
MF "v[120:123],v[28:31],v[164:167],v[120:123]\n"
"v_exp_f32 v95,v95\n"
MF "v[124:127],v[28:31],v[168:171],v[44:47]\n"
"v_add_f32 v57,v52,v92\n"
"v_add_f32 v57,v57,v93\n"
MF "v[124:127],v[24:27],v[148:151],v[124:127]\n"
CV "v92,v92,v93\n"
"v_add_f32 v57,v57,v94\n"
MF "v[128:131],v[24:27],v[152:155],v[48:51]\n"
"v_add_f32 v57,v57,v95\n"
CV "v93,v94,v95\n"
MF "v[128:131],v[28:31],v[172:175],v[128:131]\n"
"s_waitcnt lgkmcnt(0)\n"
"ds_read_b128 v[24:27],v0 offset:32768\n"
"ds_read_b128 v[28:31],v1 offset:32768\n"
MF "v[128:131],v[16:19],v[192:195],v[128:131]\n"
"v_exp_f32 v96,v96\n"
MF "v[128:131],v[20:23],v[212:215],v[128:131]\n"
"v_exp_f32 v97,v97\n"
MF "v[124:127],v[20:23],v[208:211],v[124:127]\n"
"v_exp_f32 v98,v98\n"
MF "v[124:127],v[16:19],v[188:191],v[124:127]\n"
"v_exp_f32 v99,v99\n"
MF "v[120:123],v[16:19],v[184:187],v[120:123]\n"
"v_add_f32 v58,v53,v96\n"
"v_add_f32 v58,v58,v97\n"
MF "v[120:123],v[20:23],v[204:207],v[120:123]\n"
CV "v96,v96,v97\n"
"v_add_f32 v58,v58,v98\n"
MF "v[116:119],v[20:23],v[200:203],v[116:119]\n"
"s_waitcnt vmcnt(12) lgkmcnt(0)\n"
"v_add_f32 v58,v58,v99\n"
CV "v97,v98,v99\n"
MF "v[116:119],v[16:19],v[180:183],v[116:119]\n"
"s_barrier\n"
MF "v[112:115],v[16:19],v[176:179],v[112:115]\n"
"v_exp_f32 v100,v100\n"
MF "v[112:115],v[20:23],v[196:199],v[112:115]\n"
"s_nop 0\n"
TR "v[16:17],v2\n"
TR "v[18:19],v2 offset:6144\n"
MF "v[112:115],v[24:27],v[216:219],v[112:115]\n"
"s_add_u32 s72,s72,6144\n"
TR "v[20:21],v3\n"
TR "v[22:23],v3 offset:6144\n"
MF "v[112:115],v[28:31],v[236:239],v[112:115]\n"
"s_cmp_eq_u32 s72,s17\n"
"s_cselect_b32 s72,s16,s72\n"
"s_add_u32 m0,s64,24576\n"
MF "v[116:119],v[28:31],v[240:243],v[116:119]\n"
LD "v7,s[32:35],s72 offen nt lds\n"
MF "v[116:119],v[24:27],v[220:223],v[116:119]\n"
"s_add_u32 m0,s64,25472\n"
"v_exp_f32 v101,v101\n"
MF "v[120:123],v[24:27],v[224:227],v[120:123]\n"
LD "v7,s[32:35],s72 offen offset:128 nt lds\n"
MF "v[120:123],v[28:31],v[244:247],v[120:123]\n"
"v_exp_f32 v102,v102\n"
MF "v[124:127],v[28:31],v[248:251],v[124:127]\n"
"s_add_u32 m0,s64,26368\n"
"v_exp_f32 v103,v103\n"
MF "v[124:127],v[24:27],v[228:231],v[124:127]\n"
LD "v7,s[32:35],s72 offen offset:256 nt lds\n"
MF "v[128:131],v[24:27],v[232:235],v[128:131]\n"
"s_add_u32 s72,s72,6144\n"
"v_add_f32 v59,v54,v100\n"
"v_add_f32 v59,v59,v101\n"
MF "v[128:131],v[28:31],v[252:255],v[128:131]\n"
"s_waitcnt lgkmcnt(0)\n"
TR "v[24:25],v4\n"
TR "v[26:27],v4 offset:6144\n"
MF "a[0:3],v[16:19],v[72:75],a[220:223]\n"
"s_add_u32 m0,s64,30720\n"
TR "v[28:29],v5\n"
TR "v[30:31],v5 offset:6144\n"
MF "a[20:23],v[20:23],v[72:75],a[220:223]\n"
LD "v7,s[32:35],s72 offen nt lds\n"
MF "a[24:27],v[20:23],v[76:79],a[220:223]\n"
CV "v100,v100,v101\n"
"v_add_f32 v59,v59,v102\n"
MF "a[4:7],v[16:19],v[76:79],a[220:223]\n"
"s_add_u32 m0,s64,31616\n"
"v_add_f32 v59,v59,v103\n"
CV "v101,v102,v103\n"
MF "a[8:11],v[16:19],v[80:83],a[220:223]\n"
LD "v7,s[32:35],s72 offen offset:128 nt lds\n"
MF "a[28:31],v[20:23],v[80:83],a[220:223]\n"
"v_exp_f32 v104,v104\n"
MF "a[32:35],v[20:23],v[84:87],a[220:223]\n"
"s_add_u32 m0,s64,32512\n"
"v_exp_f32 v105,v105\n"
MF "a[12:15],v[16:19],v[84:87],a[220:223]\n"
LD "v7,s[32:35],s72 offen offset:256 nt lds\n"
MF "a[16:19],v[16:19],v[88:91],a[220:223]\n"
"v_exp_f32 v106,v106\n"
MF "a[36:39],v[20:23],v[88:91],a[220:223]\n"
"s_waitcnt lgkmcnt(0)\n"
TR "v[16:17],v2 offset:1024\n"
TR "v[18:19],v2 offset:7168\n"
MF "a[56:59],v[24:27],v[88:91],a[220:223]\n"
"s_nop 0\n"
TR "v[20:21],v3 offset:1024\n"
TR "v[22:23],v3 offset:7168\n"
MF "a[76:79],v[28:31],v[88:91],a[220:223]\n"
"v_exp_f32 v107,v107\n"
MF "a[72:75],v[28:31],v[84:87],a[220:223]\n"
"v_add_f32 v60,v55,v104\n"
"v_add_f32 v60,v60,v105\n"
MF "a[52:55],v[24:27],v[84:87],a[220:223]\n"
CV "v104,v104,v105\n"
"v_add_f32 v60,v60,v106\n"
MF "a[48:51],v[24:27],v[80:83],a[220:223]\n"
"v_add_f32 v60,v60,v107\n"
CV "v105,v106,v107\n"
MF "a[68:71],v[28:31],v[80:83],a[220:223]\n"
"v_exp_f32 v108,v108\n"
MF "a[64:67],v[28:31],v[76:79],a[220:223]\n"
"v_exp_f32 v109,v109\n"
MF "a[44:47],v[24:27],v[76:79],a[220:223]\n"
"v_exp_f32 v110,v110\n"
MF "a[40:43],v[24:27],v[72:75],a[220:223]\n"
"v_exp_f32 v111,v111\n"
MF "a[60:63],v[28:31],v[72:75],a[220:223]\n"
".Lpg10b:\n"
"s_cbranch_execz .Lpg1a\n"
".Lpg10d:\n"
"s_cbranch_execz .Lisr2\n"
"s_waitcnt lgkmcnt(0)\n"
TR "v[24:25],v4 offset:1024\n"
TR "v[26:27],v4 offset:7168\n"
MF "a[80:83],v[16:19],v[72:75],a[220:223]\n"
"s_nop 0\n"
TR "v[28:29],v5 offset:1024\n"
TR "v[30:31],v5 offset:7168\n"
MF "a[100:103],v[20:23],v[72:75],a[220:223]\n"
"v_add_f32 v61,v56,v108\n"
"v_add_f32 v61,v61,v109\n"
MF "a[104:107],v[20:23],v[76:79],a[220:223]\n"
CV "v108,v108,v109\n"
"v_add_f32 v61,v61,v110\n"
MF "a[84:87],v[16:19],v[76:79],a[220:223]\n"
"v_add_f32 v61,v61,v111\n"
CV "v109,v110,v111\n"
MF "a[88:91],v[16:19],v[80:83],a[220:223]\n"
"v_exp_f32 v112,v112\n"
MF "a[108:111],v[20:23],v[80:83],a[220:223]\n"
"v_exp_f32 v113,v113\n"
MF "a[112:115],v[20:23],v[84:87],a[220:223]\n"
"v_exp_f32 v114,v114\n"
MF "a[92:95],v[16:19],v[84:87],a[220:223]\n"
"v_exp_f32 v115,v115\n"
MF "a[96:99],v[16:19],v[88:91],a[220:223]\n"
"v_add_f32 v57,v57,v112\n"
"v_add_f32 v57,v57,v113\n"
MF "a[116:119],v[20:23],v[88:91],a[220:223]\n"
"s_waitcnt lgkmcnt(0)\n"
TR "v[16:17],v2 offset:2048\n"
TR "v[18:19],v2 offset:8192\n"
MF "a[136:139],v[24:27],v[88:91],a[220:223]\n"
"s_nop 0\n"
TR "v[20:21],v3 offset:2048\n"
TR "v[22:23],v3 offset:8192\n"
MF "a[156:159],v[28:31],v[88:91],a[220:223]\n"
CV "v94,v112,v113\n"
"v_add_f32 v57,v57,v114\n"
MF "a[152:155],v[28:31],v[84:87],a[220:223]\n"
"v_add_f32 v57,v57,v115\n"
CV "v95,v114,v115\n"
MF "a[132:135],v[24:27],v[84:87],a[220:223]\n"
"v_exp_f32 v116,v116\n"
MF "a[128:131],v[24:27],v[80:83],a[220:223]\n"
"v_exp_f32 v117,v117\n"
MF "a[148:151],v[28:31],v[80:83],a[220:223]\n"
"v_exp_f32 v118,v118\n"
MF "a[144:147],v[28:31],v[76:79],a[220:223]\n"
"v_exp_f32 v119,v119\n"
MF "a[124:127],v[24:27],v[76:79],a[220:223]\n"
"v_add_f32 v58,v58,v116\n"
"v_add_f32 v58,v58,v117\n"
MF "a[120:123],v[24:27],v[72:75],a[220:223]\n"
CV "v98,v116,v117\n"
"v_add_f32 v58,v58,v118\n"
MF "a[140:143],v[28:31],v[72:75],a[220:223]\n"
"s_waitcnt lgkmcnt(0)\n"
TR "v[24:25],v4 offset:2048\n"
TR "v[26:27],v4 offset:8192\n"
MF "a[160:163],v[16:19],v[72:75],a[220:223]\n"
"s_nop 0\n"
TR "v[28:29],v5 offset:2048\n"
TR "v[30:31],v5 offset:8192\n"
MF "a[180:183],v[20:23],v[72:75],a[220:223]\n"
"v_add_f32 v58,v58,v119\n"
CV "v99,v118,v119\n"
MF "a[184:187],v[20:23],v[76:79],a[220:223]\n"
"v_exp_f32 v120,v120\n"
MF "a[164:167],v[16:19],v[76:79],a[220:223]\n"
"v_exp_f32 v121,v121\n"
MF "a[168:171],v[16:19],v[80:83],a[220:223]\n"
"v_exp_f32 v122,v122\n"
MF "a[188:191],v[20:23],v[80:83],a[220:223]\n"
"v_exp_f32 v123,v123\n"
MF "a[192:195],v[20:23],v[84:87],a[220:223]\n"
"v_add_f32 v59,v59,v120\n"
"v_add_f32 v59,v59,v121\n"
"s_cmp_eq_u32 s57,0\n"
"s_cbranch_scc1 .Lisx6\n"
"s_mov_b64 exec,0\n"
"s_cmp_eq_u32 s56,0\n"
"s_cbranch_scc1 .Lpgt0e_g\n"
"s_cmp_eq_u32 s56,1\n"
"s_cbranch_scc1 .Lpg17g\n"
"s_cmp_eq_u32 s56,2\n"
"s_cbranch_scc1 .Lpg16g\n"
"s_branch .Lpgep_g\n"
".Lisr6:\n"
"s_mov_b64 exec,-1\n"
".Lisx6:\n"
MF "a[172:175],v[16:19],v[84:87],a[220:223]\n"
"s_waitcnt vmcnt(6) lgkmcnt(0)\n"
CV "v102,v120,v121\n"
"v_add_f32 v59,v59,v122\n"
MF "a[176:179],v[16:19],v[88:91],a[220:223]\n"
"s_barrier\n"
MF "a[196:199],v[20:23],v[88:91],a[220:223]\n"
"s_add_u32 s73,s73,12288\n"
"ds_read_b128 v[16:19],v0\n"
"ds_read_b128 v[20:23],v1\n"
MF "a[216:219],v[24:27],v[88:91],a[220:223]\n"
"s_cmp_eq_u32 s73,s19\n"
"s_cselect_b32 s73,s18,s73\n"
"s_add_u32 m0,s65,49152\n"
MF "a[236:239],v[28:31],v[88:91],a[220:223]\n"
LD "v8,s[36:39],s73 offen nt lds\n"
MF "a[232:235],v[28:31],v[84:87],a[220:223]\n"
"s_add_u32 m0,s65,50048\n"
"v_add_f32 v59,v59,v123\n"
CV "v103,v122,v123\n"
MF "a[212:215],v[24:27],v[84:87],a[220:223]\n"
LD "v8,s[36:39],s73 offen offset:128 nt lds\n"
MF "a[208:211],v[24:27],v[80:83],a[220:223]\n"
"s_add_u32 m0,s65,50944\n"
MF "a[228:231],v[28:31],v[80:83],a[220:223]\n"
LD "v8,s[36:39],s73 offen offset:256 nt lds\n"
MF "a[224:227],v[28:31],v[76:79],a[220:223]\n"
MF "a[204:207],v[24:27],v[76:79],a[220:223]\n"
MF "a[200:203],v[24:27],v[72:75],a[220:223]\n"
MF "a[220:223],v[28:31],v[72:75],a[220:223]\n"
"s_waitcnt lgkmcnt(0)\n"
".Laloopx:\n"
MF "v[72:75],v[16:19],v[136:139],v[32:35]\n"
"s_nop 0\n"
"ds_read_b128 v[24:27],v0 offset:1024\n"
"ds_read_b128 v[28:31],v1 offset:1024\n"
MF "v[72:75],v[20:23],v[156:159],v[72:75]\n"
MF "v[76:79],v[20:23],v[160:163],v[36:39]\n"
MF "v[76:79],v[16:19],v[140:143],v[76:79]\n"
MF "v[80:83],v[16:19],v[144:147],v[40:43]\n"
MF "v[80:83],v[20:23],v[164:167],v[80:83]\n"
MF "v[84:87],v[20:23],v[168:171],v[44:47]\n"
MF "v[84:87],v[16:19],v[148:151],v[84:87]\n"
"s_add_u32 m0,s65,49152\n"
MF "v[88:91],v[16:19],v[152:155],v[48:51]\n"
LD "v8,s[36:39],s73 offen offset:3072 nt lds\n"
MF "v[88:91],v[20:23],v[172:175],v[88:91]\n"
"s_waitcnt lgkmcnt(0)\n"
"ds_read_b128 v[16:19],v0 offset:2048\n"
"ds_read_b128 v[20:23],v1 offset:2048\n"
MF "v[88:91],v[24:27],v[192:195],v[88:91]\n"
"v_exp_f32 v124,v124\n"
MF "v[88:91],v[28:31],v[212:215],v[88:91]\n"
"s_add_u32 m0,s65,50048\n"
"v_exp_f32 v125,v125\n"
MF "v[84:87],v[28:31],v[208:211],v[84:87]\n"
LD "v8,s[36:39],s73 offen offset:3200 nt lds\n"
MF "v[84:87],v[24:27],v[188:191],v[84:87]\n"
"v_exp_f32 v126,v126\n"
MF "v[80:83],v[24:27],v[184:187],v[80:83]\n"
"v_exp_f32 v127,v127\n"
MF "v[80:83],v[28:31],v[204:207],v[80:83]\n"
"s_add_u32 m0,s65,50944\n"
"v_add_f32 v60,v60,v124\n"
"v_add_f32 v60,v60,v125\n"
MF "v[76:79],v[28:31],v[200:203],v[76:79]\n"
LD "v8,s[36:39],s73 offen offset:3328 nt lds\n"
MF "v[76:79],v[24:27],v[180:183],v[76:79]\n"
CV "v106,v124,v125\n"
"v_add_f32 v60,v60,v126\n"
MF "v[72:75],v[24:27],v[176:179],v[72:75]\n"
"v_add_f32 v60,v60,v127\n"
CV "v107,v126,v127\n"
MF "v[72:75],v[28:31],v[196:199],v[72:75]\n"
"s_waitcnt lgkmcnt(0)\n"
"ds_read_b128 v[24:27],v0 offset:6144\n"
"ds_read_b128 v[28:31],v1 offset:6144\n"
MF "v[72:75],v[16:19],v[216:219],v[72:75]\n"
"v_exp_f32 v128,v128\n"
MF "v[72:75],v[20:23],v[236:239],v[72:75]\n"
"v_exp_f32 v129,v129\n"
MF "v[76:79],v[20:23],v[240:243],v[76:79]\n"
"v_exp_f32 v130,v130\n"
MF "v[76:79],v[16:19],v[220:223],v[76:79]\n"
"v_exp_f32 v131,v131\n"
MF "v[80:83],v[16:19],v[224:227],v[80:83]\n"
"v_add_f32 v61,v61,v128\n"
"v_add_f32 v61,v61,v129\n"
MF "v[80:83],v[20:23],v[244:247],v[80:83]\n"
CV "v110,v128,v129\n"
"v_add_f32 v61,v61,v130\n"
MF "v[84:87],v[20:23],v[248:251],v[84:87]\n"
"v_add_f32 v61,v61,v131\n"
CV "v111,v130,v131\n"
MF "v[84:87],v[16:19],v[228:231],v[84:87]\n"
"v_max3_f32 v10,v57,v58,v59\n"
"v_max3_f32 v10,v10,v60,v61\n"
MF "v[88:91],v[16:19],v[232:235],v[88:91]\n"
"v_cmp_ngt_f32 vcc,s77,v10\n"
MF "v[88:91],v[20:23],v[252:255],v[88:91]\n"
"s_waitcnt lgkmcnt(0)\n"
"s_cbranch_vccnz .Lstub_6\n"
".Lback_6:\n"
MF "v[112:115],v[24:27],v[136:139],v[32:35]\n"
"s_add_u32 s74,s74,12288\n"
"ds_read_b128 v[16:19],v0 offset:7168\n"
"ds_read_b128 v[20:23],v1 offset:7168\n"
MF "v[112:115],v[28:31],v[156:159],v[112:115]\n"
"s_cmp_eq_u32 s74,s20\n"
"s_cselect_b32 s74,s70,s74\n"
MF "v[116:119],v[28:31],v[160:163],v[36:39]\n"
"v_exp_f32 v72,v72\n"
MF "v[116:119],v[24:27],v[140:143],v[116:119]\n"
"v_exp_f32 v73,v73\n"
MF "v[120:123],v[24:27],v[144:147],v[40:43]\n"
"v_exp_f32 v74,v74\n"
MF "v[120:123],v[28:31],v[164:167],v[120:123]\n"
"v_exp_f32 v75,v75\n"
MF "v[124:127],v[28:31],v[168:171],v[44:47]\n"
"v_add_f32 v52,v57,v72\n"
"v_add_f32 v52,v52,v73\n"
MF "v[124:127],v[24:27],v[148:151],v[124:127]\n"
CV "v72,v72,v73\n"
"v_add_f32 v52,v52,v74\n"
MF "v[128:131],v[24:27],v[152:155],v[48:51]\n"
"v_add_f32 v52,v52,v75\n"
CV "v73,v74,v75\n"
MF "v[128:131],v[28:31],v[172:175],v[128:131]\n"
"s_waitcnt lgkmcnt(0)\n"
"ds_read_b128 v[24:27],v0 offset:8192\n"
"ds_read_b128 v[28:31],v1 offset:8192\n"
MF "v[128:131],v[16:19],v[192:195],v[128:131]\n"
"v_exp_f32 v76,v76\n"
MF "v[128:131],v[20:23],v[212:215],v[128:131]\n"
"v_exp_f32 v77,v77\n"
MF "v[124:127],v[20:23],v[208:211],v[124:127]\n"
"v_exp_f32 v78,v78\n"
MF "v[124:127],v[16:19],v[188:191],v[124:127]\n"
"v_exp_f32 v79,v79\n"
MF "v[120:123],v[16:19],v[184:187],v[120:123]\n"
"v_add_f32 v53,v58,v76\n"
"v_add_f32 v53,v53,v77\n"
MF "v[120:123],v[20:23],v[204:207],v[120:123]\n"
CV "v76,v76,v77\n"
"v_add_f32 v53,v53,v78\n"
MF "v[116:119],v[20:23],v[200:203],v[116:119]\n"
"s_waitcnt vmcnt(12) lgkmcnt(0)\n"
"v_add_f32 v53,v53,v79\n"
CV "v77,v78,v79\n"
MF "v[116:119],v[16:19],v[180:183],v[116:119]\n"
"s_barrier\n"
MF "v[112:115],v[16:19],v[176:179],v[112:115]\n"
"v_exp_f32 v80,v80\n"
MF "v[112:115],v[20:23],v[196:199],v[112:115]\n"
"s_nop 0\n"
TR "v[16:17],v2 offset:24576\n"
TR "v[18:19],v2 offset:30720\n"
MF "v[112:115],v[24:27],v[216:219],v[112:115]\n"
"s_add_u32 s72,s72,6144\n"
TR "v[20:21],v3 offset:24576\n"
TR "v[22:23],v3 offset:30720\n"
MF "v[112:115],v[28:31],v[236:239],v[112:115]\n"
"s_cmp_eq_u32 s72,s17\n"
"s_cselect_b32 s72,s16,s72\n"
"s_add_u32 m0,s64,0\n"
MF "v[116:119],v[28:31],v[240:243],v[116:119]\n"
LD "v7,s[32:35],s72 offen nt lds\n"
MF "v[116:119],v[24:27],v[220:223],v[116:119]\n"
"s_add_u32 m0,s64,896\n"
"v_exp_f32 v81,v81\n"
MF "v[120:123],v[24:27],v[224:227],v[120:123]\n"
LD "v7,s[32:35],s72 offen offset:128 nt lds\n"
MF "v[120:123],v[28:31],v[244:247],v[120:123]\n"
"v_exp_f32 v82,v82\n"
MF "v[124:127],v[28:31],v[248:251],v[124:127]\n"
"s_add_u32 m0,s64,1792\n"
"v_exp_f32 v83,v83\n"
MF "v[124:127],v[24:27],v[228:231],v[124:127]\n"
LD "v7,s[32:35],s72 offen offset:256 nt lds\n"
MF "v[128:131],v[24:27],v[232:235],v[128:131]\n"
"s_add_u32 s72,s72,6144\n"
"v_add_f32 v54,v59,v80\n"
"v_add_f32 v54,v54,v81\n"
MF "v[128:131],v[28:31],v[252:255],v[128:131]\n"
"s_waitcnt lgkmcnt(0)\n"
TR "v[24:25],v4 offset:24576\n"
TR "v[26:27],v4 offset:30720\n"
MF "a[0:3],v[16:19],v[92:95],a[0:3]\n"
"s_add_u32 m0,s64,6144\n"
TR "v[28:29],v5 offset:24576\n"
TR "v[30:31],v5 offset:30720\n"
MF "a[20:23],v[20:23],v[92:95],a[20:23]\n"
LD "v7,s[32:35],s72 offen nt lds\n"
MF "a[24:27],v[20:23],v[96:99],a[24:27]\n"
CV "v80,v80,v81\n"
"v_add_f32 v54,v54,v82\n"
MF "a[4:7],v[16:19],v[96:99],a[4:7]\n"
"s_add_u32 m0,s64,7040\n"
"v_add_f32 v54,v54,v83\n"
CV "v81,v82,v83\n"
MF "a[8:11],v[16:19],v[100:103],a[8:11]\n"
LD "v7,s[32:35],s72 offen offset:128 nt lds\n"
MF "a[28:31],v[20:23],v[100:103],a[28:31]\n"
"v_exp_f32 v84,v84\n"
MF "a[32:35],v[20:23],v[104:107],a[32:35]\n"
"s_add_u32 m0,s64,7936\n"
"v_exp_f32 v85,v85\n"
MF "a[12:15],v[16:19],v[104:107],a[12:15]\n"
LD "v7,s[32:35],s72 offen offset:256 nt lds\n"
MF "a[16:19],v[16:19],v[108:111],a[16:19]\n"
"v_exp_f32 v86,v86\n"
MF "a[36:39],v[20:23],v[108:111],a[36:39]\n"
"s_waitcnt lgkmcnt(0)\n"
TR "v[16:17],v2 offset:25600\n"
TR "v[18:19],v2 offset:31744\n"
MF "a[56:59],v[24:27],v[108:111],a[56:59]\n"
"s_nop 0\n"
TR "v[20:21],v3 offset:25600\n"
TR "v[22:23],v3 offset:31744\n"
MF "a[76:79],v[28:31],v[108:111],a[76:79]\n"
"v_exp_f32 v87,v87\n"
MF "a[72:75],v[28:31],v[104:107],a[72:75]\n"
"v_add_f32 v55,v60,v84\n"
"v_add_f32 v55,v55,v85\n"
MF "a[52:55],v[24:27],v[104:107],a[52:55]\n"
CV "v84,v84,v85\n"
"v_add_f32 v55,v55,v86\n"
MF "a[48:51],v[24:27],v[100:103],a[48:51]\n"
"v_add_f32 v55,v55,v87\n"
CV "v85,v86,v87\n"
MF "a[68:71],v[28:31],v[100:103],a[68:71]\n"
"v_exp_f32 v88,v88\n"
MF "a[64:67],v[28:31],v[96:99],a[64:67]\n"
"v_exp_f32 v89,v89\n"
MF "a[44:47],v[24:27],v[96:99],a[44:47]\n"
"v_exp_f32 v90,v90\n"
MF "a[40:43],v[24:27],v[92:95],a[40:43]\n"
"v_exp_f32 v91,v91\n"
MF "a[60:63],v[28:31],v[92:95],a[60:63]\n"
"s_waitcnt lgkmcnt(0)\n"
TR "v[24:25],v4 offset:25600\n"
TR "v[26:27],v4 offset:31744\n"
MF "a[80:83],v[16:19],v[92:95],a[80:83]\n"
"s_nop 0\n"
TR "v[28:29],v5 offset:25600\n"
TR "v[30:31],v5 offset:31744\n"
MF "a[100:103],v[20:23],v[92:95],a[100:103]\n"
"v_add_f32 v56,v61,v88\n"
"v_add_f32 v56,v56,v89\n"
MF "a[104:107],v[20:23],v[96:99],a[104:107]\n"
CV "v88,v88,v89\n"
"v_add_f32 v56,v56,v90\n"
MF "a[84:87],v[16:19],v[96:99],a[84:87]\n"
"v_add_f32 v56,v56,v91\n"
CV "v89,v90,v91\n"
MF "a[88:91],v[16:19],v[100:103],a[88:91]\n"
"v_exp_f32 v112,v112\n"
MF "a[108:111],v[20:23],v[100:103],a[108:111]\n"
"v_exp_f32 v113,v113\n"
MF "a[112:115],v[20:23],v[104:107],a[112:115]\n"
"v_exp_f32 v114,v114\n"
MF "a[92:95],v[16:19],v[104:107],a[92:95]\n"
"v_exp_f32 v115,v115\n"
MF "a[96:99],v[16:19],v[108:111],a[96:99]\n"
"v_add_f32 v52,v52,v112\n"
"v_add_f32 v52,v52,v113\n"
MF "a[116:119],v[20:23],v[108:111],a[116:119]\n"
"s_waitcnt lgkmcnt(0)\n"
TR "v[16:17],v2 offset:26624\n"
TR "v[18:19],v2 offset:32768\n"
MF "a[136:139],v[24:27],v[108:111],a[136:139]\n"
"s_nop 0\n"
TR "v[20:21],v3 offset:26624\n"
TR "v[22:23],v3 offset:32768\n"
MF "a[156:159],v[28:31],v[108:111],a[156:159]\n"
CV "v74,v112,v113\n"
"v_add_f32 v52,v52,v114\n"
MF "a[152:155],v[28:31],v[104:107],a[152:155]\n"
"v_add_f32 v52,v52,v115\n"
CV "v75,v114,v115\n"
MF "a[132:135],v[24:27],v[104:107],a[132:135]\n"
"v_exp_f32 v116,v116\n"
MF "a[128:131],v[24:27],v[100:103],a[128:131]\n"
"v_exp_f32 v117,v117\n"
MF "a[148:151],v[28:31],v[100:103],a[148:151]\n"
"v_exp_f32 v118,v118\n"
MF "a[144:147],v[28:31],v[96:99],a[144:147]\n"
"v_exp_f32 v119,v119\n"
MF "a[124:127],v[24:27],v[96:99],a[124:127]\n"
"v_add_f32 v53,v53,v116\n"
"v_add_f32 v53,v53,v117\n"
MF "a[120:123],v[24:27],v[92:95],a[120:123]\n"
CV "v78,v116,v117\n"
"v_add_f32 v53,v53,v118\n"
MF "a[140:143],v[28:31],v[92:95],a[140:143]\n"
"s_waitcnt lgkmcnt(0)\n"
TR "v[24:25],v4 offset:26624\n"
TR "v[26:27],v4 offset:32768\n"
MF "a[160:163],v[16:19],v[92:95],a[160:163]\n"
"s_nop 0\n"
TR "v[28:29],v5 offset:26624\n"
TR "v[30:31],v5 offset:32768\n"
MF "a[180:183],v[20:23],v[92:95],a[180:183]\n"
"v_add_f32 v53,v53,v119\n"
CV "v79,v118,v119\n"
MF "a[184:187],v[20:23],v[96:99],a[184:187]\n"
"v_exp_f32 v120,v120\n"
MF "a[164:167],v[16:19],v[96:99],a[164:167]\n"
"v_exp_f32 v121,v121\n"
MF "a[168:171],v[16:19],v[100:103],a[168:171]\n"
"v_exp_f32 v122,v122\n"
MF "a[188:191],v[20:23],v[100:103],a[188:191]\n"
"v_exp_f32 v123,v123\n"
MF "a[192:195],v[20:23],v[104:107],a[192:195]\n"
"v_add_f32 v54,v54,v120\n"
"v_add_f32 v54,v54,v121\n"
MF "a[172:175],v[16:19],v[104:107],a[172:175]\n"
"s_waitcnt vmcnt(12) lgkmcnt(0)\n"
CV "v82,v120,v121\n"
"v_add_f32 v54,v54,v122\n"
MF "a[176:179],v[16:19],v[108:111],a[176:179]\n"
"s_barrier\n"
MF "a[196:199],v[20:23],v[108:111],a[196:199]\n"
"s_add_u32 s73,s73,12288\n"
"ds_read_b128 v[16:19],v0 offset:24576\n"
"ds_read_b128 v[20:23],v1 offset:24576\n"
MF "a[216:219],v[24:27],v[108:111],a[216:219]\n"
"s_cmp_eq_u32 s73,s19\n"
"s_cselect_b32 s73,s18,s73\n"
"s_add_u32 m0,s65,73728\n"
MF "a[236:239],v[28:31],v[108:111],a[236:239]\n"
LD "v8,s[36:39],s73 offen nt lds\n"
MF "a[232:235],v[28:31],v[104:107],a[232:235]\n"
"s_add_u32 m0,s65,74624\n"
"v_add_f32 v54,v54,v123\n"
CV "v83,v122,v123\n"
MF "a[212:215],v[24:27],v[104:107],a[212:215]\n"
LD "v8,s[36:39],s73 offen offset:128 nt lds\n"
MF "a[208:211],v[24:27],v[100:103],a[208:211]\n"
"s_add_u32 m0,s65,75520\n"
MF "a[228:231],v[28:31],v[100:103],a[228:231]\n"
LD "v8,s[36:39],s73 offen offset:256 nt lds\n"
MF "a[224:227],v[28:31],v[96:99],a[224:227]\n"
MF "a[204:207],v[24:27],v[96:99],a[204:207]\n"
MF "a[200:203],v[24:27],v[92:95],a[200:203]\n"
MF "a[220:223],v[28:31],v[92:95],a[220:223]\n"
"s_waitcnt lgkmcnt(0)\n"
MF "v[92:95],v[16:19],v[136:139],v[32:35]\n"
"s_add_u32 m0,s65,73728\n"
"ds_read_b128 v[24:27],v0 offset:25600\n"
"ds_read_b128 v[28:31],v1 offset:25600\n"
MF "v[92:95],v[20:23],v[156:159],v[92:95]\n"
LD "v8,s[36:39],s73 offen offset:3072 nt lds\n"
MF "v[96:99],v[20:23],v[160:163],v[36:39]\n"
"v_exp_f32 v124,v124\n"
MF "v[96:99],v[16:19],v[140:143],v[96:99]\n"
"v_exp_f32 v125,v125\n"
MF "v[100:103],v[16:19],v[144:147],v[40:43]\n"
"s_add_u32 m0,s65,74624\n"
"v_exp_f32 v126,v126\n"
MF "v[100:103],v[20:23],v[164:167],v[100:103]\n"
LD "v8,s[36:39],s73 offen offset:3200 nt lds\n"
MF "v[104:107],v[20:23],v[168:171],v[44:47]\n"
"v_exp_f32 v127,v127\n"
MF "v[104:107],v[16:19],v[148:151],v[104:107]\n"
"s_add_u32 m0,s65,75520\n"
"v_add_f32 v55,v55,v124\n"
"v_add_f32 v55,v55,v125\n"
MF "v[108:111],v[16:19],v[152:155],v[48:51]\n"
LD "v8,s[36:39],s73 offen offset:3328 nt lds\n"
MF "v[108:111],v[20:23],v[172:175],v[108:111]\n"
"s_waitcnt lgkmcnt(0)\n"
"ds_read_b128 v[16:19],v0 offset:26624\n"
"ds_read_b128 v[20:23],v1 offset:26624\n"
MF "v[108:111],v[24:27],v[192:195],v[108:111]\n"
CV "v86,v124,v125\n"
"v_add_f32 v55,v55,v126\n"
MF "v[108:111],v[28:31],v[212:215],v[108:111]\n"
"v_add_f32 v55,v55,v127\n"
CV "v87,v126,v127\n"
MF "v[104:107],v[28:31],v[208:211],v[104:107]\n"
"v_exp_f32 v128,v128\n"
MF "v[104:107],v[24:27],v[188:191],v[104:107]\n"
"v_exp_f32 v129,v129\n"
MF "v[100:103],v[24:27],v[184:187],v[100:103]\n"
"v_exp_f32 v130,v130\n"
MF "v[100:103],v[28:31],v[204:207],v[100:103]\n"
"v_exp_f32 v131,v131\n"
MF "v[96:99],v[28:31],v[200:203],v[96:99]\n"
"v_add_f32 v56,v56,v128\n"
"v_add_f32 v56,v56,v129\n"
MF "v[96:99],v[24:27],v[180:183],v[96:99]\n"
CV "v90,v128,v129\n"
"v_add_f32 v56,v56,v130\n"
MF "v[92:95],v[24:27],v[176:179],v[92:95]\n"
"v_add_f32 v56,v56,v131\n"
CV "v91,v130,v131\n"
MF "v[92:95],v[28:31],v[196:199],v[92:95]\n"
"s_waitcnt lgkmcnt(0)\n"
"ds_read_b128 v[24:27],v0 offset:30720\n"
"ds_read_b128 v[28:31],v1 offset:30720\n"
MF "v[92:95],v[16:19],v[216:219],v[92:95]\n"
MF "v[92:95],v[20:23],v[236:239],v[92:95]\n"
MF "v[96:99],v[20:23],v[240:243],v[96:99]\n"
MF "v[96:99],v[16:19],v[220:223],v[96:99]\n"
MF "v[100:103],v[16:19],v[224:227],v[100:103]\n"
MF "v[100:103],v[20:23],v[244:247],v[100:103]\n"
MF "v[104:107],v[20:23],v[248:251],v[104:107]\n"
MF "v[104:107],v[16:19],v[228:231],v[104:107]\n"
"v_max3_f32 v10,v52,v53,v54\n"
"v_max3_f32 v10,v10,v55,v56\n"
MF "v[108:111],v[16:19],v[232:235],v[108:111]\n"
"v_cmp_ngt_f32 vcc,s77,v10\n"
MF "v[108:111],v[20:23],v[252:255],v[108:111]\n"
"s_waitcnt lgkmcnt(0)\n"
"s_cbranch_vccnz .Lstub_7\n"
".Lback_7:\n"
MF "v[112:115],v[24:27],v[136:139],v[32:35]\n"
"s_add_u32 s74,s74,12288\n"
"ds_read_b128 v[16:19],v0 offset:31744\n"
"ds_read_b128 v[20:23],v1 offset:31744\n"
MF "v[112:115],v[28:31],v[156:159],v[112:115]\n"
"s_cmp_eq_u32 s74,s20\n"
"s_cselect_b32 s74,s70,s74\n"
MF "v[116:119],v[28:31],v[160:163],v[36:39]\n"
"v_exp_f32 v92,v92\n"
MF "v[116:119],v[24:27],v[140:143],v[116:119]\n"
"v_exp_f32 v93,v93\n"
MF "v[120:123],v[24:27],v[144:147],v[40:43]\n"
"v_exp_f32 v94,v94\n"
MF "v[120:123],v[28:31],v[164:167],v[120:123]\n"
"v_exp_f32 v95,v95\n"
MF "v[124:127],v[28:31],v[168:171],v[44:47]\n"
"v_add_f32 v57,v52,v92\n"
"v_add_f32 v57,v57,v93\n"
MF "v[124:127],v[24:27],v[148:151],v[124:127]\n"
CV "v92,v92,v93\n"
"v_add_f32 v57,v57,v94\n"
MF "v[128:131],v[24:27],v[152:155],v[48:51]\n"
"v_add_f32 v57,v57,v95\n"
CV "v93,v94,v95\n"
MF "v[128:131],v[28:31],v[172:175],v[128:131]\n"
"s_waitcnt lgkmcnt(0)\n"
"ds_read_b128 v[24:27],v0 offset:32768\n"
"ds_read_b128 v[28:31],v1 offset:32768\n"
MF "v[128:131],v[16:19],v[192:195],v[128:131]\n"
"s_sub_u32 s61,s61,1\n"
"v_exp_f32 v96,v96\n"
MF "v[128:131],v[20:23],v[212:215],v[128:131]\n"
"v_exp_f32 v97,v97\n"
MF "v[124:127],v[20:23],v[208:211],v[124:127]\n"
"v_exp_f32 v98,v98\n"
MF "v[124:127],v[16:19],v[188:191],v[124:127]\n"
"v_exp_f32 v99,v99\n"
MF "v[120:123],v[16:19],v[184:187],v[120:123]\n"
"v_add_f32 v58,v53,v96\n"
"v_add_f32 v58,v58,v97\n"
MF "v[120:123],v[20:23],v[204:207],v[120:123]\n"
CV "v96,v96,v97\n"
"v_add_f32 v58,v58,v98\n"
MF "v[116:119],v[20:23],v[200:203],v[116:119]\n"
"s_waitcnt vmcnt(12) lgkmcnt(0)\n"
"v_add_f32 v58,v58,v99\n"
CV "v97,v98,v99\n"
MF "v[116:119],v[16:19],v[180:183],v[116:119]\n"
"s_barrier\n"
MF "v[112:115],v[16:19],v[176:179],v[112:115]\n"
"v_exp_f32 v100,v100\n"
MF "v[112:115],v[20:23],v[196:199],v[112:115]\n"
"s_nop 0\n"
TR "v[16:17],v2\n"
TR "v[18:19],v2 offset:6144\n"
MF "v[112:115],v[24:27],v[216:219],v[112:115]\n"
"s_add_u32 s72,s72,6144\n"
TR "v[20:21],v3\n"
TR "v[22:23],v3 offset:6144\n"
MF "v[112:115],v[28:31],v[236:239],v[112:115]\n"
"s_cmp_eq_u32 s72,s17\n"
"s_cselect_b32 s72,s16,s72\n"
"s_add_u32 m0,s64,24576\n"
MF "v[116:119],v[28:31],v[240:243],v[116:119]\n"
LD "v7,s[32:35],s72 offen nt lds\n"
MF "v[116:119],v[24:27],v[220:223],v[116:119]\n"
"s_add_u32 m0,s64,25472\n"
"v_exp_f32 v101,v101\n"
MF "v[120:123],v[24:27],v[224:227],v[120:123]\n"
LD "v7,s[32:35],s72 offen offset:128 nt lds\n"
MF "v[120:123],v[28:31],v[244:247],v[120:123]\n"
"v_exp_f32 v102,v102\n"
MF "v[124:127],v[28:31],v[248:251],v[124:127]\n"
"s_add_u32 m0,s64,26368\n"
"v_exp_f32 v103,v103\n"
MF "v[124:127],v[24:27],v[228:231],v[124:127]\n"
LD "v7,s[32:35],s72 offen offset:256 nt lds\n"
MF "v[128:131],v[24:27],v[232:235],v[128:131]\n"
"s_add_u32 s72,s72,6144\n"
"v_add_f32 v59,v54,v100\n"
"v_add_f32 v59,v59,v101\n"
MF "v[128:131],v[28:31],v[252:255],v[128:131]\n"
"s_waitcnt lgkmcnt(0)\n"
TR "v[24:25],v4\n"
TR "v[26:27],v4 offset:6144\n"
MF "a[0:3],v[16:19],v[72:75],a[0:3]\n"
"s_add_u32 m0,s64,30720\n"
TR "v[28:29],v5\n"
TR "v[30:31],v5 offset:6144\n"
MF "a[20:23],v[20:23],v[72:75],a[20:23]\n"
LD "v7,s[32:35],s72 offen nt lds\n"
MF "a[24:27],v[20:23],v[76:79],a[24:27]\n"
CV "v100,v100,v101\n"
"v_add_f32 v59,v59,v102\n"
MF "a[4:7],v[16:19],v[76:79],a[4:7]\n"
"s_add_u32 m0,s64,31616\n"
"v_add_f32 v59,v59,v103\n"
CV "v101,v102,v103\n"
MF "a[8:11],v[16:19],v[80:83],a[8:11]\n"
LD "v7,s[32:35],s72 offen offset:128 nt lds\n"
MF "a[28:31],v[20:23],v[80:83],a[28:31]\n"
"v_exp_f32 v104,v104\n"
MF "a[32:35],v[20:23],v[84:87],a[32:35]\n"
"s_add_u32 m0,s64,32512\n"
"v_exp_f32 v105,v105\n"
MF "a[12:15],v[16:19],v[84:87],a[12:15]\n"
LD "v7,s[32:35],s72 offen offset:256 nt lds\n"
MF "a[16:19],v[16:19],v[88:91],a[16:19]\n"
"v_exp_f32 v106,v106\n"
MF "a[36:39],v[20:23],v[88:91],a[36:39]\n"
"s_waitcnt lgkmcnt(0)\n"
TR "v[16:17],v2 offset:1024\n"
TR "v[18:19],v2 offset:7168\n"
MF "a[56:59],v[24:27],v[88:91],a[56:59]\n"
"s_nop 0\n"
TR "v[20:21],v3 offset:1024\n"
TR "v[22:23],v3 offset:7168\n"
MF "a[76:79],v[28:31],v[88:91],a[76:79]\n"
"v_exp_f32 v107,v107\n"
MF "a[72:75],v[28:31],v[84:87],a[72:75]\n"
".Lpg11b:\n"
"s_cbranch_execz .Lpgqj_a\n"
".Lpg11d:\n"
"s_cbranch_execz .Lisr2\n"
"v_add_f32 v60,v55,v104\n"
"v_add_f32 v60,v60,v105\n"
MF "a[52:55],v[24:27],v[84:87],a[52:55]\n"
CV "v104,v104,v105\n"
"v_add_f32 v60,v60,v106\n"
MF "a[48:51],v[24:27],v[80:83],a[48:51]\n"
"v_add_f32 v60,v60,v107\n"
CV "v105,v106,v107\n"
MF "a[68:71],v[28:31],v[80:83],a[68:71]\n"
"v_exp_f32 v108,v108\n"
MF "a[64:67],v[28:31],v[76:79],a[64:67]\n"
"v_exp_f32 v109,v109\n"
MF "a[44:47],v[24:27],v[76:79],a[44:47]\n"
"v_exp_f32 v110,v110\n"
MF "a[40:43],v[24:27],v[72:75],a[40:43]\n"
"v_exp_f32 v111,v111\n"
MF "a[60:63],v[28:31],v[72:75],a[60:63]\n"
"s_waitcnt lgkmcnt(0)\n"
TR "v[24:25],v4 offset:1024\n"
TR "v[26:27],v4 offset:7168\n"
MF "a[80:83],v[16:19],v[72:75],a[80:83]\n"
"s_nop 0\n"
TR "v[28:29],v5 offset:1024\n"
TR "v[30:31],v5 offset:7168\n"
MF "a[100:103],v[20:23],v[72:75],a[100:103]\n"
"v_add_f32 v61,v56,v108\n"
"v_add_f32 v61,v61,v109\n"
MF "a[104:107],v[20:23],v[76:79],a[104:107]\n"
CV "v108,v108,v109\n"
"v_add_f32 v61,v61,v110\n"
MF "a[84:87],v[16:19],v[76:79],a[84:87]\n"
"v_add_f32 v61,v61,v111\n"
CV "v109,v110,v111\n"
MF "a[88:91],v[16:19],v[80:83],a[88:91]\n"
"v_exp_f32 v112,v112\n"
MF "a[108:111],v[20:23],v[80:83],a[108:111]\n"
"v_exp_f32 v113,v113\n"
MF "a[112:115],v[20:23],v[84:87],a[112:115]\n"
"v_exp_f32 v114,v114\n"
MF "a[92:95],v[16:19],v[84:87],a[92:95]\n"
"v_exp_f32 v115,v115\n"
MF "a[96:99],v[16:19],v[88:91],a[96:99]\n"
"v_add_f32 v57,v57,v112\n"
"v_add_f32 v57,v57,v113\n"
MF "a[116:119],v[20:23],v[88:91],a[116:119]\n"
"s_waitcnt lgkmcnt(0)\n"
TR "v[16:17],v2 offset:2048\n"
TR "v[18:19],v2 offset:8192\n"
MF "a[136:139],v[24:27],v[88:91],a[136:139]\n"
"s_nop 0\n"
TR "v[20:21],v3 offset:2048\n"
TR "v[22:23],v3 offset:8192\n"
MF "a[156:159],v[28:31],v[88:91],a[156:159]\n"
CV "v94,v112,v113\n"
"v_add_f32 v57,v57,v114\n"
MF "a[152:155],v[28:31],v[84:87],a[152:155]\n"
"v_add_f32 v57,v57,v115\n"
CV "v95,v114,v115\n"
MF "a[132:135],v[24:27],v[84:87],a[132:135]\n"
"v_exp_f32 v116,v116\n"
MF "a[128:131],v[24:27],v[80:83],a[128:131]\n"
"v_exp_f32 v117,v117\n"
MF "a[148:151],v[28:31],v[80:83],a[148:151]\n"
"v_exp_f32 v118,v118\n"
MF "a[144:147],v[28:31],v[76:79],a[144:147]\n"
"v_exp_f32 v119,v119\n"
MF "a[124:127],v[24:27],v[76:79],a[124:127]\n"
"v_add_f32 v58,v58,v116\n"
"v_add_f32 v58,v58,v117\n"
MF "a[120:123],v[24:27],v[72:75],a[120:123]\n"
CV "v98,v116,v117\n"
"v_add_f32 v58,v58,v118\n"
MF "a[140:143],v[28:31],v[72:75],a[140:143]\n"
"s_waitcnt lgkmcnt(0)\n"
TR "v[24:25],v4 offset:2048\n"
TR "v[26:27],v4 offset:8192\n"
MF "a[160:163],v[16:19],v[72:75],a[160:163]\n"
"s_nop 0\n"
TR "v[28:29],v5 offset:2048\n"
TR "v[30:31],v5 offset:8192\n"
MF "a[180:183],v[20:23],v[72:75],a[180:183]\n"
"v_add_f32 v58,v58,v119\n"
CV "v99,v118,v119\n"
MF "a[184:187],v[20:23],v[76:79],a[184:187]\n"
"v_exp_f32 v120,v120\n"
MF "a[164:167],v[16:19],v[76:79],a[164:167]\n"
"v_exp_f32 v121,v121\n"
MF "a[168:171],v[16:19],v[80:83],a[168:171]\n"
"v_exp_f32 v122,v122\n"
MF "a[188:191],v[20:23],v[80:83],a[188:191]\n"
"v_exp_f32 v123,v123\n"
MF "a[192:195],v[20:23],v[84:87],a[192:195]\n"
"v_add_f32 v59,v59,v120\n"
"v_add_f32 v59,v59,v121\n"
MF "a[172:175],v[16:19],v[84:87],a[172:175]\n"
"s_waitcnt vmcnt(12) lgkmcnt(0)\n"
CV "v102,v120,v121\n"
"v_add_f32 v59,v59,v122\n"
MF "a[176:179],v[16:19],v[88:91],a[176:179]\n"
"s_barrier\n"
MF "a[196:199],v[20:23],v[88:91],a[196:199]\n"
"s_add_u32 s73,s73,12288\n"
"ds_read_b128 v[16:19],v0\n"
"ds_read_b128 v[20:23],v1\n"
MF "a[216:219],v[24:27],v[88:91],a[216:219]\n"
"s_cmp_eq_u32 s73,s19\n"
"s_cselect_b32 s73,s18,s73\n"
"s_add_u32 m0,s65,49152\n"
MF "a[236:239],v[28:31],v[88:91],a[236:239]\n"
LD "v8,s[36:39],s73 offen nt lds\n"
MF "a[232:235],v[28:31],v[84:87],a[232:235]\n"
"s_add_u32 m0,s65,50048\n"
"v_add_f32 v59,v59,v123\n"
CV "v103,v122,v123\n"
MF "a[212:215],v[24:27],v[84:87],a[212:215]\n"
LD "v8,s[36:39],s73 offen offset:128 nt lds\n"
MF "a[208:211],v[24:27],v[80:83],a[208:211]\n"
"s_add_u32 m0,s65,50944\n"
MF "a[228:231],v[28:31],v[80:83],a[228:231]\n"
LD "v8,s[36:39],s73 offen offset:256 nt lds\n"
MF "a[224:227],v[28:31],v[76:79],a[224:227]\n"
MF "a[204:207],v[24:27],v[76:79],a[204:207]\n"
"s_waitcnt lgkmcnt(0)\n"
MF "a[200:203],v[24:27],v[72:75],a[200:203]\n"
"s_cmp_lg_u32 s61,1\n"
MF "a[220:223],v[28:31],v[72:75],a[220:223]\n"
"s_cbranch_scc1 .Laloopx\n"
"s_cmp_eq_u32 s57,0\n"
"s_cbranch_scc0 .Lep\n"
".Lepx:\n"
MF "v[72:75],v[16:19],v[136:139],v[32:35]\n"
"s_add_u32 m0,s65,49152\n"
"ds_read_b128 v[24:27],v0 offset:1024\n"
"ds_read_b128 v[28:31],v1 offset:1024\n"
MF "v[72:75],v[20:23],v[156:159],v[72:75]\n"
LD "v8,s[36:39],s73 offen offset:3072 nt lds\n"
MF "v[76:79],v[20:23],v[160:163],v[36:39]\n"
"v_exp_f32 v124,v124\n"
MF "v[76:79],v[16:19],v[140:143],v[76:79]\n"
"v_exp_f32 v125,v125\n"
MF "v[80:83],v[16:19],v[144:147],v[40:43]\n"
"s_add_u32 m0,s65,50048\n"
"v_exp_f32 v126,v126\n"
MF "v[80:83],v[20:23],v[164:167],v[80:83]\n"
LD "v8,s[36:39],s73 offen offset:3200 nt lds\n"
MF "v[84:87],v[20:23],v[168:171],v[44:47]\n"
"v_exp_f32 v127,v127\n"
MF "v[84:87],v[16:19],v[148:151],v[84:87]\n"
"s_add_u32 m0,s65,50944\n"
"v_add_f32 v60,v60,v124\n"
"v_add_f32 v60,v60,v125\n"
MF "v[88:91],v[16:19],v[152:155],v[48:51]\n"
LD "v8,s[36:39],s73 offen offset:3328 nt lds\n"
MF "v[88:91],v[20:23],v[172:175],v[88:91]\n"
"s_waitcnt lgkmcnt(0)\n"
"ds_read_b128 v[16:19],v0 offset:2048\n"
"ds_read_b128 v[20:23],v1 offset:2048\n"
MF "v[88:91],v[24:27],v[192:195],v[88:91]\n"
CV "v106,v124,v125\n"
"v_add_f32 v60,v60,v126\n"
MF "v[88:91],v[28:31],v[212:215],v[88:91]\n"
"v_add_f32 v60,v60,v127\n"
CV "v107,v126,v127\n"
MF "v[84:87],v[28:31],v[208:211],v[84:87]\n"
"v_exp_f32 v128,v128\n"
MF "v[84:87],v[24:27],v[188:191],v[84:87]\n"
"v_exp_f32 v129,v129\n"
MF "v[80:83],v[24:27],v[184:187],v[80:83]\n"
"v_exp_f32 v130,v130\n"
MF "v[80:83],v[28:31],v[204:207],v[80:83]\n"
"v_exp_f32 v131,v131\n"
MF "v[76:79],v[28:31],v[200:203],v[76:79]\n"
"v_add_f32 v61,v61,v128\n"
"v_add_f32 v61,v61,v129\n"
MF "v[76:79],v[24:27],v[180:183],v[76:79]\n"
CV "v110,v128,v129\n"
"v_add_f32 v61,v61,v130\n"
MF "v[72:75],v[24:27],v[176:179],v[72:75]\n"
"v_add_f32 v61,v61,v131\n"
CV "v111,v130,v131\n"
MF "v[72:75],v[28:31],v[196:199],v[72:75]\n"
"s_waitcnt lgkmcnt(0)\n"
"ds_read_b128 v[24:27],v0 offset:6144\n"
"ds_read_b128 v[28:31],v1 offset:6144\n"
MF "v[72:75],v[16:19],v[216:219],v[72:75]\n"
MF "v[72:75],v[20:23],v[236:239],v[72:75]\n"
MF "v[76:79],v[20:23],v[240:243],v[76:79]\n"
MF "v[76:79],v[16:19],v[220:223],v[76:79]\n"
MF "v[80:83],v[16:19],v[224:227],v[80:83]\n"
MF "v[80:83],v[20:23],v[244:247],v[80:83]\n"
MF "v[84:87],v[20:23],v[248:251],v[84:87]\n"
MF "v[84:87],v[16:19],v[228:231],v[84:87]\n"
"v_max3_f32 v10,v57,v58,v59\n"
"v_max3_f32 v10,v10,v60,v61\n"
MF "v[88:91],v[16:19],v[232:235],v[88:91]\n"
"v_cmp_le_f32 vcc,0x47800000,v10\n"
MF "v[88:91],v[20:23],v[252:255],v[88:91]\n"
"s_waitcnt lgkmcnt(0)\n"
"s_cbranch_vccnz .Lstub_8\n"
".Lback_8:\n"
MF "v[112:115],v[24:27],v[136:139],v[32:35]\n"
"s_add_u32 s74,s74,12288\n"
"ds_read_b128 v[16:19],v0 offset:7168\n"
"ds_read_b128 v[20:23],v1 offset:7168\n"
MF "v[112:115],v[28:31],v[156:159],v[112:115]\n"
"s_cmp_eq_u32 s74,s20\n"
"s_cselect_b32 s74,s70,s74\n"
MF "v[116:119],v[28:31],v[160:163],v[36:39]\n"
"v_exp_f32 v72,v72\n"
MF "v[116:119],v[24:27],v[140:143],v[116:119]\n"
"v_exp_f32 v73,v73\n"
MF "v[120:123],v[24:27],v[144:147],v[40:43]\n"
"v_exp_f32 v74,v74\n"
MF "v[120:123],v[28:31],v[164:167],v[120:123]\n"
"v_exp_f32 v75,v75\n"
MF "v[124:127],v[28:31],v[168:171],v[44:47]\n"
"v_add_f32 v52,v57,v72\n"
"v_add_f32 v52,v52,v73\n"
MF "v[124:127],v[24:27],v[148:151],v[124:127]\n"
CV "v72,v72,v73\n"
"v_add_f32 v52,v52,v74\n"
MF "v[128:131],v[24:27],v[152:155],v[48:51]\n"
"v_add_f32 v52,v52,v75\n"
CV "v73,v74,v75\n"
MF "v[128:131],v[28:31],v[172:175],v[128:131]\n"
"s_waitcnt lgkmcnt(0)\n"
"ds_read_b128 v[24:27],v0 offset:8192\n"
"ds_read_b128 v[28:31],v1 offset:8192\n"
MF "v[128:131],v[16:19],v[192:195],v[128:131]\n"
"v_exp_f32 v76,v76\n"
MF "v[128:131],v[20:23],v[212:215],v[128:131]\n"
"v_exp_f32 v77,v77\n"
MF "v[124:127],v[20:23],v[208:211],v[124:127]\n"
"v_exp_f32 v78,v78\n"
MF "v[124:127],v[16:19],v[188:191],v[124:127]\n"
"v_exp_f32 v79,v79\n"
MF "v[120:123],v[16:19],v[184:187],v[120:123]\n"
"v_add_f32 v53,v58,v76\n"
"v_add_f32 v53,v53,v77\n"
MF "v[120:123],v[20:23],v[204:207],v[120:123]\n"
CV "v76,v76,v77\n"
"v_add_f32 v53,v53,v78\n"
MF "v[116:119],v[20:23],v[200:203],v[116:119]\n"
"s_waitcnt vmcnt(12) lgkmcnt(0)\n"
"v_add_f32 v53,v53,v79\n"
CV "v77,v78,v79\n"
"s_mov_b64 exec,0\n"
"s_cmp_eq_u32 s56,0\n"
"s_cbranch_scc1 .Lpg6f\n"
"s_cmp_eq_u32 s56,1\n"
"s_cbranch_scc1 .Lpg5f\n"
"s_cmp_eq_u32 s56,2\n"
"s_cbranch_scc1 .Lpg4f\n"
"s_branch .Lpgq0_f\n"
".Lisr3:\n"
"s_mov_b64 exec,-1\n"
".Lisx3:\n"
MF "v[116:119],v[16:19],v[180:183],v[116:119]\n"
"s_barrier\n"
MF "v[112:115],v[16:19],v[176:179],v[112:115]\n"
"v_exp_f32 v80,v80\n"
MF "v[112:115],v[20:23],v[196:199],v[112:115]\n"
"s_nop 0\n"
TR "v[16:17],v2 offset:24576\n"
TR "v[18:19],v2 offset:30720\n"
MF "v[112:115],v[24:27],v[216:219],v[112:115]\n"
"s_add_u32 s66,s67,4718592\n"
TR "v[20:21],v3 offset:24576\n"
TR "v[22:23],v3 offset:30720\n"
MF "v[112:115],v[28:31],v[236:239],v[112:115]\n"
"s_sub_u32 s81,s81,384\n"
"s_mov_b32 s83,7077888\n"
"s_add_u32 m0,s62,0\n"
MF "v[116:119],v[28:31],v[240:243],v[116:119]\n"
"s_add_u32 s82,s66,2359296\n"
"s_sub_u32 s15,s82,7864320\n"
"s_min_u32 s82,s82,s15\n"
MF "v[116:119],v[24:27],v[220:223],v[116:119]\n"
LD "v6,s[28:31],s82 offen lds\n"
MF "v[120:123],v[24:27],v[224:227],v[120:123]\n"
"s_add_u32 s82,s82,97280\n"
"v_exp_f32 v81,v81\n"
MF "v[120:123],v[28:31],v[244:247],v[120:123]\n"
LD "v6,s[28:31],s82 offen offset:1024 lds\n"
MF "v[124:127],v[28:31],v[248:251],v[124:127]\n"
"v_exp_f32 v82,v82\n"
MF "v[124:127],v[24:27],v[228:231],v[124:127]\n"
"v_exp_f32 v83,v83\n"
MF "v[128:131],v[24:27],v[232:235],v[128:131]\n"
"v_add_f32 v54,v59,v80\n"
"v_add_f32 v54,v54,v81\n"
MF "v[128:131],v[28:31],v[252:255],v[128:131]\n"
"s_waitcnt lgkmcnt(0)\n"
TR "v[24:25],v4 offset:24576\n"
TR "v[26:27],v4 offset:30720\n"
MF "a[0:3],v[16:19],v[92:95],a[0:3]\n"
"s_add_u32 s82,s82,97280\n"
TR "v[28:29],v5 offset:24576\n"
TR "v[30:31],v5 offset:30720\n"
MF "a[20:23],v[20:23],v[92:95],a[20:23]\n"
LD "v6,s[28:31],s82 offen offset:2048 lds\n"
MF "a[24:27],v[20:23],v[96:99],a[24:27]\n"
CV "v80,v80,v81\n"
"v_add_f32 v54,v54,v82\n"
MF "a[4:7],v[16:19],v[96:99],a[4:7]\n"
"v_add_f32 v54,v54,v83\n"
CV "v81,v82,v83\n"
MF "a[8:11],v[16:19],v[100:103],a[8:11]\n"
"s_add_u32 s82,s82,97280\n"
"v_exp_f32 v84,v84\n"
MF "a[28:31],v[20:23],v[100:103],a[28:31]\n"
LD "v6,s[28:31],s82 offen offset:3072 lds\n"
MF "a[32:35],v[20:23],v[104:107],a[32:35]\n"
"v_exp_f32 v85,v85\n"
MF "a[12:15],v[16:19],v[104:107],a[12:15]\n"
"v_exp_f32 v86,v86\n"
MF "a[16:19],v[16:19],v[108:111],a[16:19]\n"
"v_exp_f32 v87,v87\n"
MF "a[36:39],v[20:23],v[108:111],a[36:39]\n"
"s_waitcnt lgkmcnt(0)\n"
TR "v[16:17],v2 offset:25600\n"
TR "v[18:19],v2 offset:31744\n"
MF "a[56:59],v[24:27],v[108:111],a[56:59]\n"
"s_nop 0\n"
TR "v[20:21],v3 offset:25600\n"
TR "v[22:23],v3 offset:31744\n"
MF "a[76:79],v[28:31],v[108:111],a[76:79]\n"
"v_add_f32 v55,v60,v84\n"
"v_add_f32 v55,v55,v85\n"
MF "a[72:75],v[28:31],v[104:107],a[72:75]\n"
CV "v84,v84,v85\n"
"v_add_f32 v55,v55,v86\n"
MF "a[52:55],v[24:27],v[104:107],a[52:55]\n"
"v_add_f32 v55,v55,v87\n"
CV "v85,v86,v87\n"
MF "a[48:51],v[24:27],v[100:103],a[48:51]\n"
"v_exp_f32 v88,v88\n"
MF "a[68:71],v[28:31],v[100:103],a[68:71]\n"
"v_exp_f32 v89,v89\n"
MF "a[64:67],v[28:31],v[96:99],a[64:67]\n"
"v_exp_f32 v90,v90\n"
MF "a[44:47],v[24:27],v[96:99],a[44:47]\n"
"v_exp_f32 v91,v91\n"
MF "a[40:43],v[24:27],v[92:95],a[40:43]\n"
"v_add_f32 v56,v61,v88\n"
"v_add_f32 v56,v56,v89\n"
MF "a[60:63],v[28:31],v[92:95],a[60:63]\n"
"s_waitcnt lgkmcnt(0)\n"
TR "v[24:25],v4 offset:25600\n"
TR "v[26:27],v4 offset:31744\n"
MF "a[80:83],v[16:19],v[92:95],a[80:83]\n"
"s_nop 0\n"
TR "v[28:29],v5 offset:25600\n"
TR "v[30:31],v5 offset:31744\n"
MF "a[100:103],v[20:23],v[92:95],a[100:103]\n"
CV "v88,v88,v89\n"
"v_add_f32 v56,v56,v90\n"
MF "a[104:107],v[20:23],v[96:99],a[104:107]\n"
"v_add_f32 v56,v56,v91\n"
CV "v89,v90,v91\n"
MF "a[84:87],v[16:19],v[96:99],a[84:87]\n"
"v_exp_f32 v112,v112\n"
MF "a[88:91],v[16:19],v[100:103],a[88:91]\n"
"v_exp_f32 v113,v113\n"
MF "a[108:111],v[20:23],v[100:103],a[108:111]\n"
"v_exp_f32 v114,v114\n"
MF "a[112:115],v[20:23],v[104:107],a[112:115]\n"
"v_exp_f32 v115,v115\n"
MF "a[92:95],v[16:19],v[104:107],a[92:95]\n"
"v_add_f32 v52,v52,v112\n"
"v_add_f32 v52,v52,v113\n"
MF "a[96:99],v[16:19],v[108:111],a[96:99]\n"
CV "v74,v112,v113\n"
"v_add_f32 v52,v52,v114\n"
MF "a[116:119],v[20:23],v[108:111],a[116:119]\n"
"s_waitcnt lgkmcnt(0)\n"
TR "v[16:17],v2 offset:26624\n"
TR "v[18:19],v2 offset:32768\n"
MF "a[136:139],v[24:27],v[108:111],a[136:139]\n"
"s_nop 0\n"
TR "v[20:21],v3 offset:26624\n"
TR "v[22:23],v3 offset:32768\n"
MF "a[156:159],v[28:31],v[108:111],a[156:159]\n"
"v_add_f32 v52,v52,v115\n"
CV "v75,v114,v115\n"
MF "a[152:155],v[28:31],v[104:107],a[152:155]\n"
"v_exp_f32 v116,v116\n"
MF "a[132:135],v[24:27],v[104:107],a[132:135]\n"
"v_exp_f32 v117,v117\n"
MF "a[128:131],v[24:27],v[100:103],a[128:131]\n"
"v_exp_f32 v118,v118\n"
MF "a[148:151],v[28:31],v[100:103],a[148:151]\n"
"v_exp_f32 v119,v119\n"
MF "a[144:147],v[28:31],v[96:99],a[144:147]\n"
"v_add_f32 v53,v53,v116\n"
"v_add_f32 v53,v53,v117\n"
MF "a[124:127],v[24:27],v[96:99],a[124:127]\n"
CV "v78,v116,v117\n"
"v_add_f32 v53,v53,v118\n"
MF "a[120:123],v[24:27],v[92:95],a[120:123]\n"
"v_add_f32 v53,v53,v119\n"
CV "v79,v118,v119\n"
MF "a[140:143],v[28:31],v[92:95],a[140:143]\n"
"s_waitcnt lgkmcnt(0)\n"
TR "v[24:25],v4 offset:26624\n"
TR "v[26:27],v4 offset:32768\n"
MF "a[160:163],v[16:19],v[92:95],a[160:163]\n"
"s_nop 0\n"
TR "v[28:29],v5 offset:26624\n"
TR "v[30:31],v5 offset:32768\n"
MF "a[180:183],v[20:23],v[92:95],a[180:183]\n"
"v_exp_f32 v120,v120\n"
MF "a[184:187],v[20:23],v[96:99],a[184:187]\n"
"v_exp_f32 v121,v121\n"
MF "a[164:167],v[16:19],v[96:99],a[164:167]\n"
"v_exp_f32 v122,v122\n"
MF "a[168:171],v[16:19],v[100:103],a[168:171]\n"
"v_exp_f32 v123,v123\n"
MF "a[188:191],v[20:23],v[100:103],a[188:191]\n"
"v_add_f32 v54,v54,v120\n"
"v_add_f32 v54,v54,v121\n"
MF "a[192:195],v[20:23],v[104:107],a[192:195]\n"
CV "v82,v120,v121\n"
"v_add_f32 v54,v54,v122\n"
MF "a[172:175],v[16:19],v[104:107],a[172:175]\n"
"s_waitcnt vmcnt(10) lgkmcnt(0)\n"
"v_add_f32 v54,v54,v123\n"
CV "v83,v122,v123\n"
MF "a[176:179],v[16:19],v[108:111],a[176:179]\n"
"s_barrier\n"
MF "a[196:199],v[20:23],v[108:111],a[196:199]\n"
"s_add_u32 s73,s73,12288\n"
"ds_read_b128 v[16:19],v0 offset:24576\n"
"ds_read_b128 v[20:23],v1 offset:24576\n"
MF "a[216:219],v[24:27],v[108:111],a[216:219]\n"
"s_cmp_eq_u32 s73,s19\n"
"s_cselect_b32 s73,s18,s73\n"
"s_add_u32 m0,s65,73728\n"
MF "a[236:239],v[28:31],v[108:111],a[236:239]\n"
LD "v8,s[36:39],s73 offen nt lds\n"
MF "a[232:235],v[28:31],v[104:107],a[232:235]\n"
"s_add_u32 m0,s65,74624\n"
MF "a[212:215],v[24:27],v[104:107],a[212:215]\n"
LD "v8,s[36:39],s73 offen offset:128 nt lds\n"
MF "a[208:211],v[24:27],v[100:103],a[208:211]\n"
"s_add_u32 m0,s65,75520\n"
MF "a[228:231],v[28:31],v[100:103],a[228:231]\n"
LD "v8,s[36:39],s73 offen offset:256 nt lds\n"
MF "a[224:227],v[28:31],v[96:99],a[224:227]\n"
MF "a[204:207],v[24:27],v[96:99],a[204:207]\n"
MF "a[200:203],v[24:27],v[92:95],a[200:203]\n"
MF "a[220:223],v[28:31],v[92:95],a[220:223]\n"
"s_waitcnt lgkmcnt(0)\n"
MF "v[92:95],v[16:19],v[136:139],v[32:35]\n"
"s_add_u32 m0,s65,73728\n"
"ds_read_b128 v[24:27],v0 offset:25600\n"
"ds_read_b128 v[28:31],v1 offset:25600\n"
MF "v[92:95],v[20:23],v[156:159],v[92:95]\n"
LD "v8,s[36:39],s73 offen offset:3072 nt lds\n"
MF "v[96:99],v[20:23],v[160:163],v[36:39]\n"
"v_exp_f32 v124,v124\n"
MF "v[96:99],v[16:19],v[140:143],v[96:99]\n"
"v_exp_f32 v125,v125\n"
MF "v[100:103],v[16:19],v[144:147],v[40:43]\n"
"s_add_u32 m0,s65,74624\n"
"v_exp_f32 v126,v126\n"
MF "v[100:103],v[20:23],v[164:167],v[100:103]\n"
LD "v8,s[36:39],s73 offen offset:3200 nt lds\n"
MF "v[104:107],v[20:23],v[168:171],v[44:47]\n"
"v_exp_f32 v127,v127\n"
MF "v[104:107],v[16:19],v[148:151],v[104:107]\n"
"s_add_u32 m0,s65,75520\n"
"v_add_f32 v55,v55,v124\n"
"v_add_f32 v55,v55,v125\n"
MF "v[108:111],v[16:19],v[152:155],v[48:51]\n"
LD "v8,s[36:39],s73 offen offset:3328 nt lds\n"
MF "v[108:111],v[20:23],v[172:175],v[108:111]\n"
"s_waitcnt lgkmcnt(0)\n"
"ds_read_b128 v[16:19],v0 offset:26624\n"
"ds_read_b128 v[20:23],v1 offset:26624\n"
MF "v[108:111],v[24:27],v[192:195],v[108:111]\n"
CV "v86,v124,v125\n"
"v_add_f32 v55,v55,v126\n"
MF "v[108:111],v[28:31],v[212:215],v[108:111]\n"
"v_add_f32 v55,v55,v127\n"
CV "v87,v126,v127\n"
MF "v[104:107],v[28:31],v[208:211],v[104:107]\n"
"v_exp_f32 v128,v128\n"
MF "v[104:107],v[24:27],v[188:191],v[104:107]\n"
"v_exp_f32 v129,v129\n"
MF "v[100:103],v[24:27],v[184:187],v[100:103]\n"
"v_exp_f32 v130,v130\n"
MF "v[100:103],v[28:31],v[204:207],v[100:103]\n"
"v_exp_f32 v131,v131\n"
MF "v[96:99],v[28:31],v[200:203],v[96:99]\n"
"v_add_f32 v56,v56,v128\n"
"v_add_f32 v56,v56,v129\n"
MF "v[96:99],v[24:27],v[180:183],v[96:99]\n"
CV "v90,v128,v129\n"
"v_add_f32 v56,v56,v130\n"
MF "v[92:95],v[24:27],v[176:179],v[92:95]\n"
"v_add_f32 v56,v56,v131\n"
CV "v91,v130,v131\n"
MF "v[92:95],v[28:31],v[196:199],v[92:95]\n"
"s_waitcnt lgkmcnt(0)\n"
"ds_read_b128 v[24:27],v0 offset:30720\n"
"ds_read_b128 v[28:31],v1 offset:30720\n"
MF "v[92:95],v[16:19],v[216:219],v[92:95]\n"
MF "v[92:95],v[20:23],v[236:239],v[92:95]\n"
MF "v[96:99],v[20:23],v[240:243],v[96:99]\n"
MF "v[96:99],v[16:19],v[220:223],v[96:99]\n"
MF "v[100:103],v[16:19],v[224:227],v[100:103]\n"
MF "v[100:103],v[20:23],v[244:247],v[100:103]\n"
MF "v[104:107],v[20:23],v[248:251],v[104:107]\n"
MF "v[104:107],v[16:19],v[228:231],v[104:107]\n"
"v_max3_f32 v10,v52,v53,v54\n"
"v_max3_f32 v10,v10,v55,v56\n"
MF "v[108:111],v[16:19],v[232:235],v[108:111]\n"
"v_cmp_le_f32 vcc,0x47800000,v10\n"
MF "v[108:111],v[20:23],v[252:255],v[108:111]\n"
"s_waitcnt lgkmcnt(0)\n"
"s_cbranch_vccnz .Lstub_9\n"
".Lback_9:\n"
MF "v[112:115],v[24:27],v[136:139],v[32:35]\n"
"s_add_u32 s74,s74,12288\n"
"ds_read_b128 v[16:19],v0 offset:31744\n"
"ds_read_b128 v[20:23],v1 offset:31744\n"
MF "v[112:115],v[28:31],v[156:159],v[112:115]\n"
"s_cmp_eq_u32 s74,s20\n"
"s_cselect_b32 s74,s70,s74\n"
MF "v[116:119],v[28:31],v[160:163],v[36:39]\n"
"v_exp_f32 v92,v92\n"
MF "v[116:119],v[24:27],v[140:143],v[116:119]\n"
"v_exp_f32 v93,v93\n"
MF "v[120:123],v[24:27],v[144:147],v[40:43]\n"
"v_exp_f32 v94,v94\n"
MF "v[120:123],v[28:31],v[164:167],v[120:123]\n"
"v_exp_f32 v95,v95\n"
MF "v[124:127],v[28:31],v[168:171],v[44:47]\n"
"v_add_f32 v57,v52,v92\n"
"v_add_f32 v57,v57,v93\n"
MF "v[124:127],v[24:27],v[148:151],v[124:127]\n"
CV "v92,v92,v93\n"
"v_add_f32 v57,v57,v94\n"
MF "v[128:131],v[24:27],v[152:155],v[48:51]\n"
"v_add_f32 v57,v57,v95\n"
CV "v93,v94,v95\n"
MF "v[128:131],v[28:31],v[172:175],v[128:131]\n"
"s_waitcnt lgkmcnt(0)\n"
"ds_read_b128 v[24:27],v0 offset:32768\n"
"ds_read_b128 v[28:31],v1 offset:32768\n"
MF "v[128:131],v[16:19],v[192:195],v[128:131]\n"
"v_exp_f32 v96,v96\n"
MF "v[128:131],v[20:23],v[212:215],v[128:131]\n"
"v_exp_f32 v97,v97\n"
MF "v[124:127],v[20:23],v[208:211],v[124:127]\n"
"v_exp_f32 v98,v98\n"
MF "v[124:127],v[16:19],v[188:191],v[124:127]\n"
"v_exp_f32 v99,v99\n"
MF "v[120:123],v[16:19],v[184:187],v[120:123]\n"
"v_add_f32 v58,v53,v96\n"
"v_add_f32 v58,v58,v97\n"
MF "v[120:123],v[20:23],v[204:207],v[120:123]\n"
CV "v96,v96,v97\n"
"v_add_f32 v58,v58,v98\n"
MF "v[116:119],v[20:23],v[200:203],v[116:119]\n"
"s_waitcnt vmcnt(10) lgkmcnt(0)\n"
"v_add_f32 v58,v58,v99\n"
CV "v97,v98,v99\n"
MF "v[116:119],v[16:19],v[180:183],v[116:119]\n"
"s_barrier\n"
MF "v[112:115],v[16:19],v[176:179],v[112:115]\n"
"v_exp_f32 v100,v100\n"
MF "v[112:115],v[20:23],v[196:199],v[112:115]\n"
"s_nop 0\n"
TR "v[16:17],v2\n"
TR "v[18:19],v2 offset:6144\n"
MF "v[112:115],v[24:27],v[216:219],v[112:115]\n"
"s_add_u32 m0,s62,16384\n"
TR "v[20:21],v3\n"
TR "v[22:23],v3 offset:6144\n"
MF "v[112:115],v[28:31],v[236:239],v[112:115]\n"
"s_add_u32 s82,s66,2359424\n"
"s_sub_u32 s15,s82,7864320\n"
"s_min_u32 s82,s82,s15\n"
MF "v[116:119],v[28:31],v[240:243],v[116:119]\n"
LD "v6,s[28:31],s82 offen lds\n"
MF "v[116:119],v[24:27],v[220:223],v[116:119]\n"
"s_add_u32 s82,s82,97280\n"
"v_exp_f32 v101,v101\n"
MF "v[120:123],v[24:27],v[224:227],v[120:123]\n"
LD "v6,s[28:31],s82 offen offset:1024 lds\n"
MF "v[120:123],v[28:31],v[244:247],v[120:123]\n"
"v_exp_f32 v102,v102\n"
MF "v[124:127],v[28:31],v[248:251],v[124:127]\n"
"s_add_u32 s82,s82,97280\n"
"v_exp_f32 v103,v103\n"
MF "v[124:127],v[24:27],v[228:231],v[124:127]\n"
LD "v6,s[28:31],s82 offen offset:2048 lds\n"
MF "v[128:131],v[24:27],v[232:235],v[128:131]\n"
"v_add_f32 v59,v54,v100\n"
"v_add_f32 v59,v59,v101\n"
MF "v[128:131],v[28:31],v[252:255],v[128:131]\n"
"s_waitcnt lgkmcnt(0)\n"
TR "v[24:25],v4\n"
TR "v[26:27],v4 offset:6144\n"
MF "a[0:3],v[16:19],v[72:75],a[0:3]\n"
"s_add_u32 s82,s82,97280\n"
TR "v[28:29],v5\n"
TR "v[30:31],v5 offset:6144\n"
MF "a[20:23],v[20:23],v[72:75],a[20:23]\n"
LD "v6,s[28:31],s82 offen offset:3072 lds\n"
MF "a[24:27],v[20:23],v[76:79],a[24:27]\n"
"s_add_u32 m0,s62,32768\n"
CV "v100,v100,v101\n"
"v_add_f32 v59,v59,v102\n"
MF "a[4:7],v[16:19],v[76:79],a[4:7]\n"
"s_add_u32 s82,s66,2359552\n"
"s_sub_u32 s15,s82,7864320\n"
"s_min_u32 s82,s82,s15\n"
MF "a[8:11],v[16:19],v[80:83],a[8:11]\n"
LD "v6,s[28:31],s82 offen lds\n"
MF "a[28:31],v[20:23],v[80:83],a[28:31]\n"
"v_add_f32 v59,v59,v103\n"
CV "v101,v102,v103\n"
MF "a[32:35],v[20:23],v[84:87],a[32:35]\n"
".Lpg12f:\n"
"s_cbranch_execz .Lpg18f\n"
"s_add_u32 s82,s82,97280\n"
"v_exp_f32 v104,v104\n"
MF "a[12:15],v[16:19],v[84:87],a[12:15]\n"
LD "v6,s[28:31],s82 offen offset:1024 lds\n"
MF "a[16:19],v[16:19],v[88:91],a[16:19]\n"
"v_exp_f32 v105,v105\n"
MF "a[36:39],v[20:23],v[88:91],a[36:39]\n"
"s_waitcnt lgkmcnt(0)\n"
TR "v[16:17],v2 offset:1024\n"
TR "v[18:19],v2 offset:7168\n"
MF "a[56:59],v[24:27],v[88:91],a[56:59]\n"
"s_nop 0\n"
TR "v[20:21],v3 offset:1024\n"
TR "v[22:23],v3 offset:7168\n"
MF "a[76:79],v[28:31],v[88:91],a[76:79]\n"
"v_exp_f32 v106,v106\n"
MF "a[72:75],v[28:31],v[84:87],a[72:75]\n"
"v_exp_f32 v107,v107\n"
MF "a[52:55],v[24:27],v[84:87],a[52:55]\n"
"v_add_f32 v60,v55,v104\n"
"v_add_f32 v60,v60,v105\n"
MF "a[48:51],v[24:27],v[80:83],a[48:51]\n"
CV "v104,v104,v105\n"
"v_add_f32 v60,v60,v106\n"
MF "a[68:71],v[28:31],v[80:83],a[68:71]\n"
"v_add_f32 v60,v60,v107\n"
CV "v105,v106,v107\n"
MF "a[64:67],v[28:31],v[76:79],a[64:67]\n"
"v_exp_f32 v108,v108\n"
MF "a[44:47],v[24:27],v[76:79],a[44:47]\n"
"v_exp_f32 v109,v109\n"
MF "a[40:43],v[24:27],v[72:75],a[40:43]\n"
"v_exp_f32 v110,v110\n"
MF "a[60:63],v[28:31],v[72:75],a[60:63]\n"
"s_waitcnt lgkmcnt(0)\n"
TR "v[24:25],v4 offset:1024\n"
TR "v[26:27],v4 offset:7168\n"
MF "a[80:83],v[16:19],v[72:75],a[80:83]\n"
"s_nop 0\n"
TR "v[28:29],v5 offset:1024\n"
TR "v[30:31],v5 offset:7168\n"
MF "a[100:103],v[20:23],v[72:75],a[100:103]\n"
"v_exp_f32 v111,v111\n"
MF "a[104:107],v[20:23],v[76:79],a[104:107]\n"
"v_add_f32 v61,v56,v108\n"
"v_add_f32 v61,v61,v109\n"
MF "a[84:87],v[16:19],v[76:79],a[84:87]\n"
CV "v108,v108,v109\n"
"v_add_f32 v61,v61,v110\n"
MF "a[88:91],v[16:19],v[80:83],a[88:91]\n"
"v_add_f32 v61,v61,v111\n"
CV "v109,v110,v111\n"
MF "a[108:111],v[20:23],v[80:83],a[108:111]\n"
"v_exp_f32 v112,v112\n"
MF "a[112:115],v[20:23],v[84:87],a[112:115]\n"
"v_exp_f32 v113,v113\n"
MF "a[92:95],v[16:19],v[84:87],a[92:95]\n"
"v_exp_f32 v114,v114\n"
MF "a[96:99],v[16:19],v[88:91],a[96:99]\n"
"v_exp_f32 v115,v115\n"
MF "a[116:119],v[20:23],v[88:91],a[116:119]\n"
"s_waitcnt lgkmcnt(0)\n"
TR "v[16:17],v2 offset:2048\n"
TR "v[18:19],v2 offset:8192\n"
MF "a[136:139],v[24:27],v[88:91],a[136:139]\n"
"s_nop 0\n"
TR "v[20:21],v3 offset:2048\n"
TR "v[22:23],v3 offset:8192\n"
MF "a[156:159],v[28:31],v[88:91],a[156:159]\n"
"v_add_f32 v57,v57,v112\n"
"v_add_f32 v57,v57,v113\n"
MF "a[152:155],v[28:31],v[84:87],a[152:155]\n"
CV "v94,v112,v113\n"
"v_add_f32 v57,v57,v114\n"
MF "a[132:135],v[24:27],v[84:87],a[132:135]\n"
"v_add_f32 v57,v57,v115\n"
CV "v95,v114,v115\n"
MF "a[128:131],v[24:27],v[80:83],a[128:131]\n"
"v_exp_f32 v116,v116\n"
MF "a[148:151],v[28:31],v[80:83],a[148:151]\n"
"v_exp_f32 v117,v117\n"
MF "a[144:147],v[28:31],v[76:79],a[144:147]\n"
"v_exp_f32 v118,v118\n"
MF "a[124:127],v[24:27],v[76:79],a[124:127]\n"
"v_exp_f32 v119,v119\n"
MF "a[120:123],v[24:27],v[72:75],a[120:123]\n"
"v_add_f32 v58,v58,v116\n"
"v_add_f32 v58,v58,v117\n"
MF "a[140:143],v[28:31],v[72:75],a[140:143]\n"
"s_waitcnt lgkmcnt(0)\n"
TR "v[24:25],v4 offset:2048\n"
TR "v[26:27],v4 offset:8192\n"
MF "a[160:163],v[16:19],v[72:75],a[160:163]\n"
"s_nop 0\n"
TR "v[28:29],v5 offset:2048\n"
TR "v[30:31],v5 offset:8192\n"
MF "a[180:183],v[20:23],v[72:75],a[180:183]\n"
CV "v98,v116,v117\n"
"v_add_f32 v58,v58,v118\n"
MF "a[184:187],v[20:23],v[76:79],a[184:187]\n"
"v_add_f32 v58,v58,v119\n"
CV "v99,v118,v119\n"
MF "a[164:167],v[16:19],v[76:79],a[164:167]\n"
"v_exp_f32 v120,v120\n"
MF "a[168:171],v[16:19],v[80:83],a[168:171]\n"
"v_exp_f32 v121,v121\n"
MF "a[188:191],v[20:23],v[80:83],a[188:191]\n"
"v_exp_f32 v122,v122\n"
MF "a[192:195],v[20:23],v[84:87],a[192:195]\n"
"v_exp_f32 v123,v123\n"
MF "a[172:175],v[16:19],v[84:87],a[172:175]\n"
"v_add_f32 v59,v59,v120\n"
"v_add_f32 v59,v59,v121\n"
MF "a[176:179],v[16:19],v[88:91],a[176:179]\n"
"s_waitcnt lgkmcnt(0)\n"
CV "v102,v120,v121\n"
"v_add_f32 v59,v59,v122\n"
MF "a[196:199],v[20:23],v[88:91],a[196:199]\n"
"s_add_u32 s82,s82,97280\n"
"v_add_f32 v59,v59,v123\n"
CV "v103,v122,v123\n"
MF "a[216:219],v[24:27],v[88:91],a[216:219]\n"
LD "v6,s[28:31],s82 offen offset:2048 lds\n"
MF "a[236:239],v[28:31],v[88:91],a[236:239]\n"
"v_exp_f32 v124,v124\n"
MF "a[232:235],v[28:31],v[84:87],a[232:235]\n"
"s_add_u32 s82,s82,97280\n"
"v_exp_f32 v125,v125\n"
MF "a[212:215],v[24:27],v[84:87],a[212:215]\n"
LD "v6,s[28:31],s82 offen offset:3072 lds\n"
MF "a[208:211],v[24:27],v[80:83],a[208:211]\n"
"v_exp_f32 v126,v126\n"
MF "a[228:231],v[28:31],v[80:83],a[228:231]\n"
"v_exp_f32 v127,v127\n"
MF "a[224:227],v[28:31],v[76:79],a[224:227]\n"
"v_add_f32 v60,v60,v124\n"
"v_add_f32 v60,v60,v125\n"
MF "a[204:207],v[24:27],v[76:79],a[204:207]\n"
CV "v106,v124,v125\n"
"v_add_f32 v60,v60,v126\n"
MF "a[200:203],v[24:27],v[72:75],a[200:203]\n"
"v_add_f32 v60,v60,v127\n"
CV "v107,v126,v127\n"
MF "a[220:223],v[28:31],v[72:75],a[220:223]\n"
"v_exp_f32 v128,v128\n"
"v_exp_f32 v129,v129\n"
"v_exp_f32 v130,v130\n"
"v_exp_f32 v131,v131\n"
"v_add_f32 v61,v61,v128\n"
"v_add_f32 v61,v61,v129\n"
CV "v110,v128,v129\n"
"v_add_f32 v61,v61,v130\n"
"v_add_f32_e64 v61,v61,v131\n"
CV "v111,v130,v131\n"
"v_max3_f32 v10,v57,v58,v59\n"
"v_max3_f32 v10,v10,v60,v61\n"
"v_cmp_le_f32 vcc,0x47800000,v10\n"
"s_cbranch_vccnz .Lstub_10\n"
".Lback_10:\n"
"s_waitcnt vmcnt(8) lgkmcnt(0)\n"
"s_barrier\n"
"v_add_u32 v11,97280,v6\n"
"v_add_u32 v12,194560,v6\n"
"v_add_u32 v13,291840,v6\n"
"v_mov_b32 v10,1.0\n"
TR "v[136:137],v2 offset:24576\n"
TR "v[138:139],v2 offset:30720\n"
"v_mfma_f32_16x16x4_f32 v[16:19],v10,v57,0\n"
TR "v[140:141],v3 offset:24576\n"
TR "v[142:143],v3 offset:30720\n"
TR "v[144:145],v4 offset:24576\n"
TR "v[146:147],v4 offset:30720\n"
TR "v[148:149],v5 offset:24576\n"
TR "v[150:151],v5 offset:30720\n"
"v_mfma_f32_16x16x4_f32 v[20:23],v10,v58,0\n"
TR "v[152:153],v2 offset:25600\n"
TR "v[154:155],v2 offset:31744\n"
TR "v[156:157],v3 offset:25600\n"
TR "v[158:159],v3 offset:31744\n"
TR "v[160:161],v4 offset:25600\n"
TR "v[162:163],v4 offset:31744\n"
"v_mfma_f32_16x16x4_f32 v[24:27],v10,v59,0\n"
TR "v[164:165],v5 offset:25600\n"
TR "v[166:167],v5 offset:31744\n"
TR "v[168:169],v2 offset:26624\n"
TR "v[170:171],v2 offset:32768\n"
TR "v[172:173],v3 offset:26624\n"
TR "v[174:175],v3 offset:32768\n"
"v_mfma_f32_16x16x4_f32 v[28:31],v10,v60,0\n"
TR "v[176:177],v4 offset:26624\n"
TR "v[178:179],v4 offset:32768\n"
TR "v[180:181],v5 offset:26624\n"
TR "v[182:183],v5 offset:32768\n"
"v_mfma_f32_16x16x4_f32 v[32:35],v10,v61,0\n"
"s_waitcnt lgkmcnt(15)\n"
MF "a[0:3],v[136:139],v[92:95],a[0:3]\n"
MF "a[20:23],v[140:143],v[92:95],a[20:23]\n"
"v_rcp_f32 v57,v16\n"
"v_rcp_f32 v58,v20\n"
"v_rcp_f32 v59,v24\n"
"v_rcp_f32 v60,v28\n"
"s_nop 2\n"
"v_rcp_f32_e64 v61,v32\n"
MF "a[4:7],v[136:139],v[96:99],a[4:7]\n"
"v_mul_f32_e64 v52,-s13,v57\n"
"v_mul_f32_e64 v53,-s13,v58\n"
"v_mul_f32_e64 v54,-s13,v59\n"
"v_mul_f32_e64 v55,-s13,v60\n"
"v_mul_f32_e64 v56,-s13,v61\n"
AR "v72,a0\n"
AR "v73,a1\n"
AR "v74,a2\n"
MF "a[24:27],v[140:143],v[96:99],a[24:27]\n"
AR "v75,a3\n"
AR "v76,a20\n"
AR "v77,a21\n"
AR "v78,a22\n"
AR "v79,a23\n"
"v_pk_fma_f32 v[72:73],v[72:73],v[56:57],v[52:53] op_sel:[0,1,0] op_sel_hi:[1,1,0]\n"
"v_pk_fma_f32 v[74:75],v[74:75],v[56:57],v[52:53] op_sel:[0,1,0] op_sel_hi:[1,1,0]\n"
"v_pk_fma_f32 v[76:77],v[76:77],v[56:57],v[52:53] op_sel:[0,1,0] op_sel_hi:[1,1,0]\n"
"v_pk_fma_f32 v[78:79],v[78:79],v[56:57],v[52:53] op_sel:[0,1,0] op_sel_hi:[1,1,0]\n"
CV "v112,v72,v73\n"
CV "v113,v74,v75\n"
CV "v114,v76,v77\n"
CV "v115,v78,v79\n"
MF "a[8:11],v[136:139],v[100:103],a[8:11]\n"
AW "a0,v112\n"
AW "a1,v113\n"
AW "a2,v114\n"
AW "a3,v115\n"
AR "v80,a4\n"
AR "v81,a5\n"
AR "v82,a6\n"
MF "a[28:31],v[140:143],v[100:103],a[28:31]\n"
AR "v83,a7\n"
AR "v84,a24\n"
AR "v85,a25\n"
AR "v86,a26\n"
AR "v87,a27\n"
"v_pk_fma_f32 v[80:81],v[80:81],v[58:59],v[52:53] op_sel:[0,0,1] op_sel_hi:[1,0,1]\n"
"v_pk_fma_f32 v[82:83],v[82:83],v[58:59],v[52:53] op_sel:[0,0,1] op_sel_hi:[1,0,1]\n"
"v_pk_fma_f32 v[84:85],v[84:85],v[58:59],v[52:53] op_sel:[0,0,1] op_sel_hi:[1,0,1]\n"
"v_pk_fma_f32 v[86:87],v[86:87],v[58:59],v[52:53] op_sel:[0,0,1] op_sel_hi:[1,0,1]\n"
CV "v116,v80,v81\n"
CV "v117,v82,v83\n"
CV "v118,v84,v85\n"
CV "v119,v86,v87\n"
MF "a[12:15],v[136:139],v[104:107],a[12:15]\n"
AW "a4,v116\n"
AW "a5,v117\n"
AW "a6,v118\n"
AW "a7,v119\n"
AR "v72,a8\n"
AR "v73,a9\n"
AR "v74,a10\n"
MF "a[32:35],v[140:143],v[104:107],a[32:35]\n"
AR "v75,a11\n"
AR "v76,a28\n"
AR "v77,a29\n"
AR "v78,a30\n"
AR "v79,a31\n"
"v_pk_fma_f32 v[72:73],v[72:73],v[58:59],v[54:55] op_sel:[0,1,0] op_sel_hi:[1,1,0]\n"
"v_pk_fma_f32 v[74:75],v[74:75],v[58:59],v[54:55] op_sel:[0,1,0] op_sel_hi:[1,1,0]\n"
"v_pk_fma_f32 v[76:77],v[76:77],v[58:59],v[54:55] op_sel:[0,1,0] op_sel_hi:[1,1,0]\n"
"v_pk_fma_f32 v[78:79],v[78:79],v[58:59],v[54:55] op_sel:[0,1,0] op_sel_hi:[1,1,0]\n"
CV "v112,v72,v73\n"
CV "v113,v74,v75\n"
CV "v114,v76,v77\n"
CV "v115,v78,v79\n"
MF "a[16:19],v[136:139],v[108:111],a[16:19]\n"
AW "a8,v112\n"
AW "a9,v113\n"
AW "a10,v114\n"
AW "a11,v115\n"
AR "v80,a12\n"
AR "v81,a13\n"
AR "v82,a14\n"
MF "a[36:39],v[140:143],v[108:111],a[36:39]\n"
AR "v83,a15\n"
AR "v84,a32\n"
AR "v85,a33\n"
AR "v86,a34\n"
AR "v87,a35\n"
"v_pk_fma_f32 v[80:81],v[80:81],v[60:61],v[54:55] op_sel:[0,0,1] op_sel_hi:[1,0,1]\n"
"v_pk_fma_f32 v[82:83],v[82:83],v[60:61],v[54:55] op_sel:[0,0,1] op_sel_hi:[1,0,1]\n"
"v_pk_fma_f32 v[84:85],v[84:85],v[60:61],v[54:55] op_sel:[0,0,1] op_sel_hi:[1,0,1]\n"
"v_pk_fma_f32 v[86:87],v[86:87],v[60:61],v[54:55] op_sel:[0,0,1] op_sel_hi:[1,0,1]\n"
CV "v116,v80,v81\n"
CV "v117,v82,v83\n"
CV "v118,v84,v85\n"
CV "v119,v86,v87\n"
MF "a[40:43],v[144:147],v[92:95],a[40:43]\n"
AW "a12,v116\n"
AW "a13,v117\n"
AW "a14,v118\n"
AW "a15,v119\n"
AR "v72,a16\n"
AR "v73,a17\n"
AR "v74,a18\n"
MF "a[60:63],v[148:151],v[92:95],a[60:63]\n"
AR "v75,a19\n"
AR "v76,a36\n"
AR "v77,a37\n"
AR "v78,a38\n"
AR "v79,a39\n"
"v_pk_fma_f32 v[72:73],v[72:73],v[60:61],v[56:57] op_sel:[0,1,0] op_sel_hi:[1,1,0]\n"
"v_pk_fma_f32 v[74:75],v[74:75],v[60:61],v[56:57] op_sel:[0,1,0] op_sel_hi:[1,1,0]\n"
"v_pk_fma_f32 v[76:77],v[76:77],v[60:61],v[56:57] op_sel:[0,1,0] op_sel_hi:[1,1,0]\n"
"v_pk_fma_f32 v[78:79],v[78:79],v[60:61],v[56:57] op_sel:[0,1,0] op_sel_hi:[1,1,0]\n"
CV "v112,v72,v73\n"
CV "v113,v74,v75\n"
CV "v114,v76,v77\n"
CV "v115,v78,v79\n"
MF "a[44:47],v[144:147],v[96:99],a[44:47]\n"
AW "a16,v112\n"
AW "a17,v113\n"
AW "a18,v114\n"
AW "a19,v115\n"
AR "v80,a40\n"
AR "v81,a41\n"
AR "v82,a42\n"
MF "a[64:67],v[148:151],v[96:99],a[64:67]\n"
AR "v83,a43\n"
AR "v84,a60\n"
AR "v85,a61\n"
AR "v86,a62\n"
AR "v87,a63\n"
"v_pk_fma_f32 v[80:81],v[80:81],v[56:57],v[52:53] op_sel:[0,1,0] op_sel_hi:[1,1,0]\n"
"v_pk_fma_f32 v[82:83],v[82:83],v[56:57],v[52:53] op_sel:[0,1,0] op_sel_hi:[1,1,0]\n"
"v_pk_fma_f32 v[84:85],v[84:85],v[56:57],v[52:53] op_sel:[0,1,0] op_sel_hi:[1,1,0]\n"
"v_pk_fma_f32 v[86:87],v[86:87],v[56:57],v[52:53] op_sel:[0,1,0] op_sel_hi:[1,1,0]\n"
CV "v116,v80,v81\n"
CV "v117,v82,v83\n"
CV "v118,v84,v85\n"
CV "v119,v86,v87\n"
MF "a[48:51],v[144:147],v[100:103],a[48:51]\n"
AW "a40,v116\n"
AW "a41,v117\n"
AW "a42,v118\n"
AW "a43,v119\n"
AR "v72,a44\n"
AR "v73,a45\n"
AR "v74,a46\n"
MF "a[68:71],v[148:151],v[100:103],a[68:71]\n"
AR "v75,a47\n"
AR "v76,a64\n"
AR "v77,a65\n"
AR "v78,a66\n"
AR "v79,a67\n"
"v_pk_fma_f32 v[72:73],v[72:73],v[58:59],v[52:53] op_sel:[0,0,1] op_sel_hi:[1,0,1]\n"
"v_pk_fma_f32 v[74:75],v[74:75],v[58:59],v[52:53] op_sel:[0,0,1] op_sel_hi:[1,0,1]\n"
"v_pk_fma_f32 v[76:77],v[76:77],v[58:59],v[52:53] op_sel:[0,0,1] op_sel_hi:[1,0,1]\n"
"v_pk_fma_f32 v[78:79],v[78:79],v[58:59],v[52:53] op_sel:[0,0,1] op_sel_hi:[1,0,1]\n"
CV "v112,v72,v73\n"
CV "v113,v74,v75\n"
CV "v114,v76,v77\n"
CV "v115,v78,v79\n"
MF "a[52:55],v[144:147],v[104:107],a[52:55]\n"
AW "a44,v112\n"
AW "a45,v113\n"
AW "a46,v114\n"
AW "a47,v115\n"
AR "v80,a48\n"
AR "v81,a49\n"
AR "v82,a50\n"
MF "a[72:75],v[148:151],v[104:107],a[72:75]\n"
AR "v83,a51\n"
AR "v84,a68\n"
AR "v85,a69\n"
AR "v86,a70\n"
AR "v87,a71\n"
"v_pk_fma_f32 v[80:81],v[80:81],v[58:59],v[54:55] op_sel:[0,1,0] op_sel_hi:[1,1,0]\n"
"v_pk_fma_f32 v[82:83],v[82:83],v[58:59],v[54:55] op_sel:[0,1,0] op_sel_hi:[1,1,0]\n"
"v_pk_fma_f32 v[84:85],v[84:85],v[58:59],v[54:55] op_sel:[0,1,0] op_sel_hi:[1,1,0]\n"
"v_pk_fma_f32 v[86:87],v[86:87],v[58:59],v[54:55] op_sel:[0,1,0] op_sel_hi:[1,1,0]\n"
CV "v116,v80,v81\n"
CV "v117,v82,v83\n"
CV "v118,v84,v85\n"
CV "v119,v86,v87\n"
MF "a[56:59],v[144:147],v[108:111],a[56:59]\n"
AW "a48,v116\n"
AW "a49,v117\n"
AW "a50,v118\n"
AW "a51,v119\n"
AR "v72,a52\n"
AR "v73,a53\n"
AR "v74,a54\n"
MF "a[76:79],v[148:151],v[108:111],a[76:79]\n"
AR "v75,a55\n"
AR "v76,a72\n"
AR "v77,a73\n"
AR "v78,a74\n"
AR "v79,a75\n"
"v_pk_fma_f32 v[72:73],v[72:73],v[60:61],v[54:55] op_sel:[0,0,1] op_sel_hi:[1,0,1]\n"
"v_pk_fma_f32 v[74:75],v[74:75],v[60:61],v[54:55] op_sel:[0,0,1] op_sel_hi:[1,0,1]\n"
"v_pk_fma_f32 v[76:77],v[76:77],v[60:61],v[54:55] op_sel:[0,0,1] op_sel_hi:[1,0,1]\n"
"v_pk_fma_f32 v[78:79],v[78:79],v[60:61],v[54:55] op_sel:[0,0,1] op_sel_hi:[1,0,1]\n"
CV "v112,v72,v73\n"
CV "v113,v74,v75\n"
CV "v114,v76,v77\n"
CV "v115,v78,v79\n"
"s_waitcnt lgkmcnt(14)\n"
MF "a[80:83],v[152:155],v[92:95],a[80:83]\n"
AW "a52,v112\n"
AW "a53,v113\n"
AW "a54,v114\n"
AW "a55,v115\n"
AR "v80,a56\n"
AR "v81,a57\n"
AR "v82,a58\n"
"s_waitcnt lgkmcnt(12)\n"
MF "a[100:103],v[156:159],v[92:95],a[100:103]\n"
AR "v83,a59\n"
AR "v84,a76\n"
AR "v85,a77\n"
AR "v86,a78\n"
AR "v87,a79\n"
"v_pk_fma_f32 v[80:81],v[80:81],v[60:61],v[56:57] op_sel:[0,1,0] op_sel_hi:[1,1,0]\n"
"v_pk_fma_f32 v[82:83],v[82:83],v[60:61],v[56:57] op_sel:[0,1,0] op_sel_hi:[1,1,0]\n"
"v_pk_fma_f32 v[84:85],v[84:85],v[60:61],v[56:57] op_sel:[0,1,0] op_sel_hi:[1,1,0]\n"
"v_pk_fma_f32 v[86:87],v[86:87],v[60:61],v[56:57] op_sel:[0,1,0] op_sel_hi:[1,1,0]\n"
CV "v116,v80,v81\n"
CV "v117,v82,v83\n"
CV "v118,v84,v85\n"
CV "v119,v86,v87\n"
MF "a[84:87],v[152:155],v[96:99],a[84:87]\n"
AW "a56,v116\n"
AW "a57,v117\n"
AW "a58,v118\n"
AW "a59,v119\n"
AR "v72,a80\n"
AR "v73,a81\n"
AR "v74,a82\n"
MF "a[104:107],v[156:159],v[96:99],a[104:107]\n"
AR "v75,a83\n"
AR "v76,a100\n"
AR "v77,a101\n"
AR "v78,a102\n"
AR "v79,a103\n"
"v_pk_fma_f32 v[72:73],v[72:73],v[56:57],v[52:53] op_sel:[0,1,0] op_sel_hi:[1,1,0]\n"
"v_pk_fma_f32 v[74:75],v[74:75],v[56:57],v[52:53] op_sel:[0,1,0] op_sel_hi:[1,1,0]\n"
"v_pk_fma_f32 v[76:77],v[76:77],v[56:57],v[52:53] op_sel:[0,1,0] op_sel_hi:[1,1,0]\n"
"v_pk_fma_f32 v[78:79],v[78:79],v[56:57],v[52:53] op_sel:[0,1,0] op_sel_hi:[1,1,0]\n"
CV "v112,v72,v73\n"
CV "v113,v74,v75\n"
CV "v114,v76,v77\n"
CV "v115,v78,v79\n"
MF "a[88:91],v[152:155],v[100:103],a[88:91]\n"
AW "a80,v112\n"
AW "a81,v113\n"
AW "a82,v114\n"
AW "a83,v115\n"
AR "v80,a84\n"
AR "v81,a85\n"
AR "v82,a86\n"
MF "a[108:111],v[156:159],v[100:103],a[108:111]\n"
AR "v83,a87\n"
AR "v84,a104\n"
AR "v85,a105\n"
AR "v86,a106\n"
AR "v87,a107\n"
"v_pk_fma_f32 v[80:81],v[80:81],v[58:59],v[52:53] op_sel:[0,0,1] op_sel_hi:[1,0,1]\n"
"v_pk_fma_f32 v[82:83],v[82:83],v[58:59],v[52:53] op_sel:[0,0,1] op_sel_hi:[1,0,1]\n"
"v_pk_fma_f32 v[84:85],v[84:85],v[58:59],v[52:53] op_sel:[0,0,1] op_sel_hi:[1,0,1]\n"
"v_pk_fma_f32 v[86:87],v[86:87],v[58:59],v[52:53] op_sel:[0,0,1] op_sel_hi:[1,0,1]\n"
CV "v116,v80,v81\n"
CV "v117,v82,v83\n"
CV "v118,v84,v85\n"
CV "v119,v86,v87\n"
MF "a[92:95],v[152:155],v[104:107],a[92:95]\n"
AW "a84,v116\n"
AW "a85,v117\n"
AW "a86,v118\n"
AW "a87,v119\n"
AR "v72,a88\n"
AR "v73,a89\n"
AR "v74,a90\n"
MF "a[112:115],v[156:159],v[104:107],a[112:115]\n"
AR "v75,a91\n"
AR "v76,a108\n"
AR "v77,a109\n"
AR "v78,a110\n"
AR "v79,a111\n"
"v_pk_fma_f32 v[72:73],v[72:73],v[58:59],v[54:55] op_sel:[0,1,0] op_sel_hi:[1,1,0]\n"
"v_pk_fma_f32 v[74:75],v[74:75],v[58:59],v[54:55] op_sel:[0,1,0] op_sel_hi:[1,1,0]\n"
"v_pk_fma_f32 v[76:77],v[76:77],v[58:59],v[54:55] op_sel:[0,1,0] op_sel_hi:[1,1,0]\n"
"v_pk_fma_f32 v[78:79],v[78:79],v[58:59],v[54:55] op_sel:[0,1,0] op_sel_hi:[1,1,0]\n"
CV "v112,v72,v73\n"
CV "v113,v74,v75\n"
CV "v114,v76,v77\n"
CV "v115,v78,v79\n"
MF "a[96:99],v[152:155],v[108:111],a[96:99]\n"
AW "a88,v112\n"
AW "a89,v113\n"
AW "a90,v114\n"
AW "a91,v115\n"
AR "v80,a92\n"
AR "v81,a93\n"
AR "v82,a94\n"
MF "a[116:119],v[156:159],v[108:111],a[116:119]\n"
AR "v83,a95\n"
AR "v84,a112\n"
AR "v85,a113\n"
AR "v86,a114\n"
AR "v87,a115\n"
"v_pk_fma_f32 v[80:81],v[80:81],v[60:61],v[54:55] op_sel:[0,0,1] op_sel_hi:[1,0,1]\n"
"v_pk_fma_f32 v[82:83],v[82:83],v[60:61],v[54:55] op_sel:[0,0,1] op_sel_hi:[1,0,1]\n"
"v_pk_fma_f32 v[84:85],v[84:85],v[60:61],v[54:55] op_sel:[0,0,1] op_sel_hi:[1,0,1]\n"
"v_pk_fma_f32 v[86:87],v[86:87],v[60:61],v[54:55] op_sel:[0,0,1] op_sel_hi:[1,0,1]\n"
CV "v116,v80,v81\n"
CV "v117,v82,v83\n"
CV "v118,v84,v85\n"
CV "v119,v86,v87\n"
"s_waitcnt lgkmcnt(10)\n"
MF "a[120:123],v[160:163],v[92:95],a[120:123]\n"
AW "a92,v116\n"
AW "a93,v117\n"
AW "a94,v118\n"
AW "a95,v119\n"
AR "v72,a96\n"
AR "v73,a97\n"
AR "v74,a98\n"
"s_waitcnt lgkmcnt(8)\n"
MF "a[140:143],v[164:167],v[92:95],a[140:143]\n"
AR "v75,a99\n"
AR "v76,a116\n"
AR "v77,a117\n"
AR "v78,a118\n"
AR "v79,a119\n"
"v_pk_fma_f32 v[72:73],v[72:73],v[60:61],v[56:57] op_sel:[0,1,0] op_sel_hi:[1,1,0]\n"
"v_pk_fma_f32 v[74:75],v[74:75],v[60:61],v[56:57] op_sel:[0,1,0] op_sel_hi:[1,1,0]\n"
"v_pk_fma_f32 v[76:77],v[76:77],v[60:61],v[56:57] op_sel:[0,1,0] op_sel_hi:[1,1,0]\n"
"v_pk_fma_f32 v[78:79],v[78:79],v[60:61],v[56:57] op_sel:[0,1,0] op_sel_hi:[1,1,0]\n"
CV "v112,v72,v73\n"
CV "v113,v74,v75\n"
CV "v114,v76,v77\n"
CV "v115,v78,v79\n"
MF "a[124:127],v[160:163],v[96:99],a[124:127]\n"
AW "a96,v112\n"
AW "a97,v113\n"
AW "a98,v114\n"
AW "a99,v115\n"
AR "v0,a240\n"
AR "v2,a241\n"
AR "v4,a242\n"
"v_xor_b32 v1,64,v0\n"
"v_xor_b32 v3,64,v2\n"
"v_xor_b32 v5,64,v4\n"
AR "v80,a120\n"
AR "v81,a121\n"
AR "v82,a122\n"
MF "a[144:147],v[164:167],v[96:99],a[144:147]\n"
".Lpg13f:\n"
"s_cbranch_execz .Lisr3\n"
AR "v83,a123\n"
AR "v84,a140\n"
AR "v85,a141\n"
AR "v86,a142\n"
AR "v87,a143\n"
"v_pk_fma_f32 v[80:81],v[80:81],v[56:57],v[52:53] op_sel:[0,1,0] op_sel_hi:[1,1,0]\n"
"v_pk_fma_f32 v[82:83],v[82:83],v[56:57],v[52:53] op_sel:[0,1,0] op_sel_hi:[1,1,0]\n"
"v_pk_fma_f32 v[84:85],v[84:85],v[56:57],v[52:53] op_sel:[0,1,0] op_sel_hi:[1,1,0]\n"
"v_pk_fma_f32 v[86:87],v[86:87],v[56:57],v[52:53] op_sel:[0,1,0] op_sel_hi:[1,1,0]\n"
CV "v116,v80,v81\n"
CV "v117,v82,v83\n"
CV "v118,v84,v85\n"
CV "v119,v86,v87\n"
MF "a[128:131],v[160:163],v[100:103],a[128:131]\n"
AW "a120,v116\n"
AW "a121,v117\n"
AW "a122,v118\n"
AW "a123,v119\n"
AR "v72,a124\n"
AR "v73,a125\n"
AR "v74,a126\n"
MF "a[148:151],v[164:167],v[100:103],a[148:151]\n"
AR "v75,a127\n"
AR "v76,a144\n"
AR "v77,a145\n"
AR "v78,a146\n"
AR "v79,a147\n"
"v_pk_fma_f32 v[72:73],v[72:73],v[58:59],v[52:53] op_sel:[0,0,1] op_sel_hi:[1,0,1]\n"
"v_pk_fma_f32 v[74:75],v[74:75],v[58:59],v[52:53] op_sel:[0,0,1] op_sel_hi:[1,0,1]\n"
"v_pk_fma_f32 v[76:77],v[76:77],v[58:59],v[52:53] op_sel:[0,0,1] op_sel_hi:[1,0,1]\n"
"v_pk_fma_f32 v[78:79],v[78:79],v[58:59],v[52:53] op_sel:[0,0,1] op_sel_hi:[1,0,1]\n"
CV "v112,v72,v73\n"
CV "v113,v74,v75\n"
CV "v114,v76,v77\n"
CV "v115,v78,v79\n"
MF "a[132:135],v[160:163],v[104:107],a[132:135]\n"
"ds_read_b128 a[60:63],v2\n"
AW "a124,v112\n"
AW "a125,v113\n"
AW "a126,v114\n"
AW "a127,v115\n"
AR "v80,a128\n"
AR "v81,a129\n"
AR "v82,a130\n"
MF "a[152:155],v[164:167],v[104:107],a[152:155]\n"
AR "v83,a131\n"
AR "v84,a148\n"
AR "v85,a149\n"
AR "v86,a150\n"
AR "v87,a151\n"
"v_pk_fma_f32 v[80:81],v[80:81],v[58:59],v[54:55] op_sel:[0,1,0] op_sel_hi:[1,1,0]\n"
"v_pk_fma_f32 v[82:83],v[82:83],v[58:59],v[54:55] op_sel:[0,1,0] op_sel_hi:[1,1,0]\n"
"v_pk_fma_f32 v[84:85],v[84:85],v[58:59],v[54:55] op_sel:[0,1,0] op_sel_hi:[1,1,0]\n"
"v_pk_fma_f32 v[86:87],v[86:87],v[58:59],v[54:55] op_sel:[0,1,0] op_sel_hi:[1,1,0]\n"
CV "v116,v80,v81\n"
CV "v117,v82,v83\n"
CV "v118,v84,v85\n"
CV "v119,v86,v87\n"
MF "a[136:139],v[160:163],v[108:111],a[136:139]\n"
"ds_read_b128 a[100:103],v3\n"
AW "a128,v116\n"
AW "a129,v117\n"
AW "a130,v118\n"
AW "a131,v119\n"
AR "v72,a132\n"
AR "v73,a133\n"
AR "v74,a134\n"
MF "a[156:159],v[164:167],v[108:111],a[156:159]\n"
AR "v75,a135\n"
AR "v76,a152\n"
AR "v77,a153\n"
AR "v78,a154\n"
AR "v79,a155\n"
"v_pk_fma_f32 v[72:73],v[72:73],v[60:61],v[54:55] op_sel:[0,0,1] op_sel_hi:[1,0,1]\n"
"v_pk_fma_f32 v[74:75],v[74:75],v[60:61],v[54:55] op_sel:[0,0,1] op_sel_hi:[1,0,1]\n"
"v_pk_fma_f32 v[76:77],v[76:77],v[60:61],v[54:55] op_sel:[0,0,1] op_sel_hi:[1,0,1]\n"
"v_pk_fma_f32 v[78:79],v[78:79],v[60:61],v[54:55] op_sel:[0,0,1] op_sel_hi:[1,0,1]\n"
CV "v112,v72,v73\n"
CV "v113,v74,v75\n"
CV "v114,v76,v77\n"
CV "v115,v78,v79\n"
"s_waitcnt lgkmcnt(8)\n"
MF "a[160:163],v[168:171],v[92:95],a[160:163]\n"
"ds_read_b128 a[64:67],v2 offset:2048\n"
AW "a132,v112\n"
AW "a133,v113\n"
AW "a134,v114\n"
AW "a135,v115\n"
AR "v80,a136\n"
AR "v81,a137\n"
AR "v82,a138\n"
"s_waitcnt lgkmcnt(7)\n"
MF "a[180:183],v[172:175],v[92:95],a[180:183]\n"
AR "v83,a139\n"
AR "v84,a156\n"
AR "v85,a157\n"
AR "v86,a158\n"
AR "v87,a159\n"
"v_pk_fma_f32 v[80:81],v[80:81],v[60:61],v[56:57] op_sel:[0,1,0] op_sel_hi:[1,1,0]\n"
"v_pk_fma_f32 v[82:83],v[82:83],v[60:61],v[56:57] op_sel:[0,1,0] op_sel_hi:[1,1,0]\n"
"v_pk_fma_f32 v[84:85],v[84:85],v[60:61],v[56:57] op_sel:[0,1,0] op_sel_hi:[1,1,0]\n"
"v_pk_fma_f32 v[86:87],v[86:87],v[60:61],v[56:57] op_sel:[0,1,0] op_sel_hi:[1,1,0]\n"
CV "v116,v80,v81\n"
CV "v117,v82,v83\n"
CV "v118,v84,v85\n"
CV "v119,v86,v87\n"
MF "a[164:167],v[168:171],v[96:99],a[164:167]\n"
"ds_read_b128 a[104:107],v3 offset:2048\n"
AW "a136,v116\n"
AW "a137,v117\n"
AW "a138,v118\n"
AW "a139,v119\n"
AR "v72,a160\n"
AR "v73,a161\n"
AR "v74,a162\n"
MF "a[184:187],v[172:175],v[96:99],a[184:187]\n"
AR "v75,a163\n"
AR "v76,a180\n"
AR "v77,a181\n"
AR "v78,a182\n"
AR "v79,a183\n"
"v_pk_fma_f32 v[72:73],v[72:73],v[56:57],v[52:53] op_sel:[0,1,0] op_sel_hi:[1,1,0]\n"
"v_pk_fma_f32 v[74:75],v[74:75],v[56:57],v[52:53] op_sel:[0,1,0] op_sel_hi:[1,1,0]\n"
"v_pk_fma_f32 v[76:77],v[76:77],v[56:57],v[52:53] op_sel:[0,1,0] op_sel_hi:[1,1,0]\n"
"v_pk_fma_f32 v[78:79],v[78:79],v[56:57],v[52:53] op_sel:[0,1,0] op_sel_hi:[1,1,0]\n"
CV "v112,v72,v73\n"
CV "v113,v74,v75\n"
CV "v114,v76,v77\n"
CV "v115,v78,v79\n"
MF "a[168:171],v[168:171],v[100:103],a[168:171]\n"
"ds_read_b128 a[68:71],v2 offset:4096\n"
AW "a160,v112\n"
AW "a161,v113\n"
AW "a162,v114\n"
AW "a163,v115\n"
AR "v80,a164\n"
AR "v81,a165\n"
AR "v82,a166\n"
MF "a[188:191],v[172:175],v[100:103],a[188:191]\n"
AR "v83,a167\n"
AR "v84,a184\n"
AR "v85,a185\n"
AR "v86,a186\n"
AR "v87,a187\n"
"v_pk_fma_f32 v[80:81],v[80:81],v[58:59],v[52:53] op_sel:[0,0,1] op_sel_hi:[1,0,1]\n"
"v_pk_fma_f32 v[82:83],v[82:83],v[58:59],v[52:53] op_sel:[0,0,1] op_sel_hi:[1,0,1]\n"
"v_pk_fma_f32 v[84:85],v[84:85],v[58:59],v[52:53] op_sel:[0,0,1] op_sel_hi:[1,0,1]\n"
"v_pk_fma_f32 v[86:87],v[86:87],v[58:59],v[52:53] op_sel:[0,0,1] op_sel_hi:[1,0,1]\n"
CV "v116,v80,v81\n"
CV "v117,v82,v83\n"
CV "v118,v84,v85\n"
CV "v119,v86,v87\n"
MF "a[172:175],v[168:171],v[104:107],a[172:175]\n"
"ds_read_b128 a[108:111],v3 offset:4096\n"
AW "a164,v116\n"
AW "a165,v117\n"
AW "a166,v118\n"
AW "a167,v119\n"
AR "v72,a168\n"
AR "v73,a169\n"
AR "v74,a170\n"
MF "a[192:195],v[172:175],v[104:107],a[192:195]\n"
AR "v75,a171\n"
AR "v76,a188\n"
AR "v77,a189\n"
AR "v78,a190\n"
AR "v79,a191\n"
"v_pk_fma_f32 v[72:73],v[72:73],v[58:59],v[54:55] op_sel:[0,1,0] op_sel_hi:[1,1,0]\n"
"v_pk_fma_f32 v[74:75],v[74:75],v[58:59],v[54:55] op_sel:[0,1,0] op_sel_hi:[1,1,0]\n"
"v_pk_fma_f32 v[76:77],v[76:77],v[58:59],v[54:55] op_sel:[0,1,0] op_sel_hi:[1,1,0]\n"
"v_pk_fma_f32 v[78:79],v[78:79],v[58:59],v[54:55] op_sel:[0,1,0] op_sel_hi:[1,1,0]\n"
CV "v112,v72,v73\n"
CV "v113,v74,v75\n"
CV "v114,v76,v77\n"
CV "v115,v78,v79\n"
MF "a[176:179],v[168:171],v[108:111],a[176:179]\n"
"ds_read_b128 a[72:75],v2 offset:6144\n"
AW "a168,v112\n"
AW "a169,v113\n"
AW "a170,v114\n"
AW "a171,v115\n"
AR "v80,a172\n"
AR "v81,a173\n"
AR "v82,a174\n"
MF "a[196:199],v[172:175],v[108:111],a[196:199]\n"
AR "v83,a175\n"
AR "v84,a192\n"
AR "v85,a193\n"
AR "v86,a194\n"
AR "v87,a195\n"
"v_pk_fma_f32 v[80:81],v[80:81],v[60:61],v[54:55] op_sel:[0,0,1] op_sel_hi:[1,0,1]\n"
"v_pk_fma_f32 v[82:83],v[82:83],v[60:61],v[54:55] op_sel:[0,0,1] op_sel_hi:[1,0,1]\n"
"v_pk_fma_f32 v[84:85],v[84:85],v[60:61],v[54:55] op_sel:[0,0,1] op_sel_hi:[1,0,1]\n"
"v_pk_fma_f32 v[86:87],v[86:87],v[60:61],v[54:55] op_sel:[0,0,1] op_sel_hi:[1,0,1]\n"
CV "v116,v80,v81\n"
CV "v117,v82,v83\n"
CV "v118,v84,v85\n"
CV "v119,v86,v87\n"
"s_waitcnt lgkmcnt(9)\n"
MF "a[200:203],v[176:179],v[92:95],a[200:203]\n"
"ds_read_b128 a[112:115],v3 offset:6144\n"
AW "a172,v116\n"
AW "a173,v117\n"
AW "a174,v118\n"
AW "a175,v119\n"
AR "v72,a176\n"
AR "v73,a177\n"
AR "v74,a178\n"
"s_waitcnt lgkmcnt(8)\n"
MF "a[220:223],v[180:183],v[92:95],a[220:223]\n"
AR "v75,a179\n"
AR "v76,a196\n"
AR "v77,a197\n"
AR "v78,a198\n"
AR "v79,a199\n"
"v_pk_fma_f32 v[72:73],v[72:73],v[60:61],v[56:57] op_sel:[0,1,0] op_sel_hi:[1,1,0]\n"
"v_pk_fma_f32 v[74:75],v[74:75],v[60:61],v[56:57] op_sel:[0,1,0] op_sel_hi:[1,1,0]\n"
"v_pk_fma_f32 v[76:77],v[76:77],v[60:61],v[56:57] op_sel:[0,1,0] op_sel_hi:[1,1,0]\n"
"v_pk_fma_f32 v[78:79],v[78:79],v[60:61],v[56:57] op_sel:[0,1,0] op_sel_hi:[1,1,0]\n"
CV "v112,v72,v73\n"
CV "v113,v74,v75\n"
CV "v114,v76,v77\n"
CV "v115,v78,v79\n"
MF "a[204:207],v[176:179],v[96:99],a[204:207]\n"
"ds_read_b128 a[76:79],v2 offset:8192\n"
AW "a176,v112\n"
AW "a177,v113\n"
AW "a178,v114\n"
AW "a179,v115\n"
AR "v80,a200\n"
AR "v81,a201\n"
AR "v82,a202\n"
MF "a[224:227],v[180:183],v[96:99],a[224:227]\n"
AR "v83,a203\n"
AR "v84,a220\n"
AR "v85,a221\n"
AR "v86,a222\n"
AR "v87,a223\n"
"v_pk_fma_f32 v[80:81],v[80:81],v[56:57],v[52:53] op_sel:[0,1,0] op_sel_hi:[1,1,0]\n"
"v_pk_fma_f32 v[82:83],v[82:83],v[56:57],v[52:53] op_sel:[0,1,0] op_sel_hi:[1,1,0]\n"
"v_pk_fma_f32 v[84:85],v[84:85],v[56:57],v[52:53] op_sel:[0,1,0] op_sel_hi:[1,1,0]\n"
"v_pk_fma_f32 v[86:87],v[86:87],v[56:57],v[52:53] op_sel:[0,1,0] op_sel_hi:[1,1,0]\n"
CV "v116,v80,v81\n"
CV "v117,v82,v83\n"
CV "v118,v84,v85\n"
CV "v119,v86,v87\n"
MF "a[208:211],v[176:179],v[100:103],a[208:211]\n"
"ds_read_b128 a[116:119],v3 offset:8192\n"
AW "a20,v116\n"
AW "a21,v117\n"
AW "a22,v118\n"
AW "a23,v119\n"
AR "v72,a204\n"
AR "v73,a205\n"
AR "v74,a206\n"
MF "a[228:231],v[180:183],v[100:103],a[228:231]\n"
AR "v75,a207\n"
AR "v76,a224\n"
AR "v77,a225\n"
AR "v78,a226\n"
AR "v79,a227\n"
"v_pk_fma_f32 v[72:73],v[72:73],v[58:59],v[52:53] op_sel:[0,0,1] op_sel_hi:[1,0,1]\n"
"v_pk_fma_f32 v[74:75],v[74:75],v[58:59],v[52:53] op_sel:[0,0,1] op_sel_hi:[1,0,1]\n"
"v_pk_fma_f32 v[76:77],v[76:77],v[58:59],v[52:53] op_sel:[0,0,1] op_sel_hi:[1,0,1]\n"
"v_pk_fma_f32 v[78:79],v[78:79],v[58:59],v[52:53] op_sel:[0,0,1] op_sel_hi:[1,0,1]\n"
CV "v112,v72,v73\n"
CV "v113,v74,v75\n"
CV "v114,v76,v77\n"
CV "v115,v78,v79\n"
MF "a[212:215],v[176:179],v[104:107],a[212:215]\n"
AW "a24,v112\n"
AW "a25,v113\n"
AW "a26,v114\n"
AW "a27,v115\n"
AR "v80,a208\n"
AR "v81,a209\n"
AR "v82,a210\n"
MF "a[232:235],v[180:183],v[104:107],a[232:235]\n"
AR "v83,a211\n"
AR "v84,a228\n"
AR "v85,a229\n"
AR "v86,a230\n"
AR "v87,a231\n"
"v_pk_fma_f32 v[80:81],v[80:81],v[58:59],v[54:55] op_sel:[0,1,0] op_sel_hi:[1,1,0]\n"
"v_pk_fma_f32 v[82:83],v[82:83],v[58:59],v[54:55] op_sel:[0,1,0] op_sel_hi:[1,1,0]\n"
"v_pk_fma_f32 v[84:85],v[84:85],v[58:59],v[54:55] op_sel:[0,1,0] op_sel_hi:[1,1,0]\n"
"v_pk_fma_f32 v[86:87],v[86:87],v[58:59],v[54:55] op_sel:[0,1,0] op_sel_hi:[1,1,0]\n"
CV "v116,v80,v81\n"
CV "v117,v82,v83\n"
CV "v118,v84,v85\n"
CV "v119,v86,v87\n"
MF "a[216:219],v[176:179],v[108:111],a[216:219]\n"
"s_waitcnt vmcnt(14) lgkmcnt(0)\n"
"s_barrier\n"
TR "a[200:201],v0\n"
TR "a[202:203],v0 offset:512\n"
TR "a[204:205],v0 offset:4096\n"
TR "a[206:207],v0 offset:4608\n"
AW "a28,v116\n"
AW "a29,v117\n"
AW "a30,v118\n"
AW "a31,v119\n"
AR "v72,a212\n"
AR "v73,a213\n"
AR "v74,a214\n"
MF "a[236:239],v[180:183],v[108:111],a[236:239]\n"
AR "v75,a215\n"
AR "v76,a232\n"
AR "v77,a233\n"
AR "v78,a234\n"
AR "v79,a235\n"
"v_pk_fma_f32 v[72:73],v[72:73],v[60:61],v[54:55] op_sel:[0,0,1] op_sel_hi:[1,0,1]\n"
"v_pk_fma_f32 v[74:75],v[74:75],v[60:61],v[54:55] op_sel:[0,0,1] op_sel_hi:[1,0,1]\n"
"v_pk_fma_f32 v[76:77],v[76:77],v[60:61],v[54:55] op_sel:[0,0,1] op_sel_hi:[1,0,1]\n"
"v_pk_fma_f32 v[78:79],v[78:79],v[60:61],v[54:55] op_sel:[0,0,1] op_sel_hi:[1,0,1]\n"
CV "v112,v72,v73\n"
CV "v113,v74,v75\n"
CV "v114,v76,v77\n"
CV "v115,v78,v79\n"
TR "a[208:209],v0 offset:8\n"
TR "a[210:211],v0 offset:520\n"
TR "a[212:213],v0 offset:4104\n"
TR "a[214:215],v0 offset:4616\n"
AW "a32,v112\n"
AW "a33,v113\n"
AW "a34,v114\n"
AW "a35,v115\n"
AR "v80,a216\n"
AR "v81,a217\n"
AR "v82,a218\n"
AR "v83,a219\n"
AR "v84,a236\n"
AR "v85,a237\n"
AR "v86,a238\n"
AR "v87,a239\n"
"v_pk_fma_f32 v[80:81],v[80:81],v[60:61],v[56:57] op_sel:[0,1,0] op_sel_hi:[1,1,0]\n"
"v_pk_fma_f32 v[82:83],v[82:83],v[60:61],v[56:57] op_sel:[0,1,0] op_sel_hi:[1,1,0]\n"
"v_pk_fma_f32 v[84:85],v[84:85],v[60:61],v[56:57] op_sel:[0,1,0] op_sel_hi:[1,1,0]\n"
"v_pk_fma_f32 v[86:87],v[86:87],v[60:61],v[56:57] op_sel:[0,1,0] op_sel_hi:[1,1,0]\n"
CV "v116,v80,v81\n"
CV "v117,v82,v83\n"
CV "v118,v84,v85\n"
CV "v119,v86,v87\n"
AW "a36,v116\n"
AW "a37,v117\n"
AW "a38,v118\n"
AW "a39,v119\n"
"s_mov_b32 s13,s11\n"
"s_mov_b32 s70,s71\n"
"s_mov_b32 s57,1\n"
"s_waitcnt lgkmcnt(0)\n"
"s_branch .Lq0\n"
".Lpgqy_c:\n"
"s_cbranch_execz .Lpgqj_a\n"
".Lqy:\n"
MF "v[216:219],a[216:219],a[60:63],v[216:219]\n"
TR "a[208:209],v0 offset:8\n"
MF "v[216:219],a[220:223],a[100:103],v[216:219]\n"
TR "a[210:211],v0 offset:520\n"
MF "v[224:227],a[220:223],a[104:107],v[224:227]\n"
MF "v[224:227],a[216:219],a[64:67],v[224:227]\n"
TR "a[212:213],v0 offset:4104\n"
MF "v[232:235],a[216:219],a[68:71],v[232:235]\n"
TR "a[214:215],v0 offset:4616\n"
MF "v[232:235],a[220:223],a[108:111],v[232:235]\n"
MF "v[240:243],a[220:223],a[112:115],v[240:243]\n"
MF "v[240:243],a[216:219],a[72:75],v[240:243]\n"
MF "v[248:251],a[216:219],a[76:79],v[248:251]\n"
MF "v[248:251],a[220:223],a[116:119],v[248:251]\n"
MF "v[248:251],a[200:203],a[156:159],v[248:251]\n"
TR "a[216:217],v0 offset:16392\n"
MF "v[248:251],a[204:207],a[196:199],v[248:251]\n"
TR "a[218:219],v0 offset:16904\n"
MF "v[240:243],a[204:207],a[192:195],v[240:243]\n"
MF "v[240:243],a[200:203],a[152:155],v[240:243]\n"
TR "a[220:221],v0 offset:20488\n"
MF "v[232:235],a[200:203],a[148:151],v[232:235]\n"
TR "a[222:223],v0 offset:21000\n"
MF "v[232:235],a[204:207],a[188:191],v[232:235]\n"
MF "v[224:227],a[204:207],a[184:187],v[224:227]\n"
MF "v[224:227],a[200:203],a[144:147],v[224:227]\n"
"v_fma_f32 v248,v248,s54,v14\n"
"v_fma_f32 v249,v249,s54,v14\n"
MF "v[216:219],a[200:203],a[140:143],v[216:219]\n"
"v_fma_f32 v250,v250,s54,v14\n"
"v_fma_f32 v251,v251,s54,v14\n"
MF "v[216:219],a[204:207],a[180:183],v[216:219]\n"
"s_waitcnt lgkmcnt(4)\n"
"v_fma_f32 v252,v252,s54,v14\n"
"v_fma_f32 v253,v253,s54,v14\n"
MF "v[180:183],a[208:211],a[60:63],v[180:183]\n"
TR "a[200:201],v0\n"
"v_fma_f32 v254,v254,s54,v14\n"
MF "v[180:183],a[212:215],a[100:103],v[180:183]\n"
TR "a[202:203],v0 offset:512\n"
"v_fma_f32 v255,v255,s54,v14\n"
MF "v[188:191],a[212:215],a[104:107],v[188:191]\n"
CV "v255,v254,v255\n"
CV "v254,v252,v253\n"
MF "v[188:191],a[208:211],a[64:67],v[188:191]\n"
TR "a[204:205],v0 offset:4096\n"
CV "v253,v250,v251\n"
MF "v[196:199],a[208:211],a[68:71],v[196:199]\n"
TR "a[206:207],v0 offset:4608\n"
CV "v252,v248,v249\n"
MF "v[196:199],a[212:215],a[108:111],v[196:199]\n"
"s_add_u32 s58,s75,4096\n"
"v_fma_f32 v240,v240,s54,v14\n"
"v_fma_f32 v241,v241,s54,v14\n"
MF "v[204:207],a[212:215],a[112:115],v[204:207]\n"
"buffer_store_dwordx4 v[252:255],v9,s[40:43],s58 offen offset:1024 sc1\n"
MF "v[204:207],a[208:211],a[72:75],v[204:207]\n"
"v_fma_f32 v242,v242,s54,v14\n"
"v_fma_f32 v243,v243,s54,v14\n"
MF "v[212:215],a[208:211],a[76:79],v[212:215]\n"
"v_fma_f32 v244,v244,s54,v14\n"
"v_fma_f32 v245,v245,s54,v14\n"
MF "v[212:215],a[212:215],a[116:119],v[212:215]\n"
"s_waitcnt lgkmcnt(4)\n"
"v_fma_f32 v246,v246,s54,v14\n"
"v_fma_f32 v247,v247,s54,v14\n"
MF "v[180:183],a[216:219],a[140:143],v[180:183]\n"
TR "a[208:209],v0 offset:16384\n"
CV "v251,v246,v247\n"
MF "v[180:183],a[220:223],a[180:183],v[180:183]\n"
TR "a[210:211],v0 offset:16896\n"
CV "v250,v244,v245\n"
MF "v[188:191],a[220:223],a[184:187],v[188:191]\n"
CV "v249,v242,v243\n"
CV "v248,v240,v241\n"
MF "v[188:191],a[216:219],a[144:147],v[188:191]\n"
TR "a[212:213],v0 offset:20480\n"
"v_fma_f32 v232,v232,s54,v14\n"
MF "v[196:199],a[216:219],a[148:151],v[196:199]\n"
TR "a[214:215],v0 offset:20992\n"
"v_fma_f32 v233,v233,s54,v14\n"
MF "v[196:199],a[220:223],a[188:191],v[196:199]\n"
"buffer_store_dwordx4 v[248:251],v9,s[40:43],s58 offen sc1\n"
MF "v[204:207],a[220:223],a[192:195],v[204:207]\n"
"v_fma_f32 v234,v234,s54,v14\n"
"v_fma_f32 v235,v235,s54,v14\n"
MF "v[204:207],a[216:219],a[152:155],v[204:207]\n"
"v_fma_f32 v236,v236,s54,v14\n"
"v_fma_f32 v237,v237,s54,v14\n"
MF "v[212:215],a[216:219],a[156:159],v[212:215]\n"
"s_waitcnt vmcnt(10) lgkmcnt(0)\n"
"v_fma_f32 v238,v238,s54,v14\n"
"v_fma_f32 v239,v239,s54,v14\n"
MF "v[212:215],a[220:223],a[196:199],v[212:215]\n"
"s_barrier\n"
MF "v[176:179],a[200:203],a[60:63],v[176:179]\n"
TR "a[216:217],v1 offset:32776\n"
CV "v247,v238,v239\n"
MF "v[176:179],a[204:207],a[100:103],v[176:179]\n"
TR "a[218:219],v1 offset:33288\n"
CV "v246,v236,v237\n"
MF "v[184:187],a[204:207],a[104:107],v[184:187]\n"
"s_add_u32 s66,s67,4718592\n"
"s_sub_u32 s81,s81,384\n"
"s_mov_b32 s83,7077888\n"
MF "v[184:187],a[200:203],a[64:67],v[184:187]\n"
"s_add_u32 m0,s62,0\n"
"s_add_u32 s82,s66,2359296\n"
TR "a[220:221],v1 offset:36872\n"
MF "v[192:195],a[200:203],a[68:71],v[192:195]\n"
"s_sub_u32 s15,s82,7864320\n"
"s_min_u32 s82,s82,s15\n"
TR "a[222:223],v1 offset:37384\n"
MF "v[192:195],a[204:207],a[108:111],v[192:195]\n"
LD "v6,s[28:31],s82 offen lds\n"
MF "v[200:203],a[204:207],a[112:115],v[200:203]\n"
CV "v245,v234,v235\n"
CV "v244,v232,v233\n"
MF "v[200:203],a[200:203],a[72:75],v[200:203]\n"
"v_fma_f32 v224,v224,s54,v14\n"
"v_fma_f32 v225,v225,s54,v14\n"
MF "v[208:211],a[200:203],a[76:79],v[208:211]\n"
"buffer_store_dwordx4 v[244:247],v9,s[40:43],s75 offen offset:3072 sc1\n"
MF "v[208:211],a[204:207],a[116:119],v[208:211]\n"
LD "v11,s[28:31],s82 offen offset:1024 lds\n"
MF "v[208:211],a[208:211],a[156:159],v[208:211]\n"
TR "a[200:201],v1 offset:49160\n"
"v_fma_f32 v226,v226,s54,v14\n"
MF "v[208:211],a[212:215],a[196:199],v[208:211]\n"
TR "a[202:203],v1 offset:49672\n"
"v_fma_f32 v227,v227,s54,v14\n"
MF "v[200:203],a[212:215],a[192:195],v[200:203]\n"
"v_fma_f32 v228,v228,s54,v14\n"
"v_fma_f32 v229,v229,s54,v14\n"
MF "v[200:203],a[208:211],a[152:155],v[200:203]\n"
TR "a[204:205],v1 offset:53256\n"
"v_fma_f32 v230,v230,s54,v14\n"
MF "v[192:195],a[208:211],a[148:151],v[192:195]\n"
TR "a[206:207],v1 offset:53768\n"
"v_fma_f32 v231,v231,s54,v14\n"
MF "v[192:195],a[212:215],a[188:191],v[192:195]\n"
CV "v243,v230,v231\n"
CV "v242,v228,v229\n"
MF "v[184:187],a[212:215],a[184:187],v[184:187]\n"
CV "v241,v226,v227\n"
CV "v240,v224,v225\n"
MF "v[184:187],a[208:211],a[144:147],v[184:187]\n"
"buffer_store_dwordx4 v[240:243],v9,s[40:43],s75 offen offset:2048 sc1\n"
MF "v[176:179],a[208:211],a[140:143],v[176:179]\n"
LD "v12,s[28:31],s82 offen offset:2048 lds\n"
MF "v[176:179],a[212:215],a[180:183],v[176:179]\n"
"s_waitcnt lgkmcnt(4)\n"
"v_fma_f32 v216,v216,s54,v14\n"
"v_fma_f32 v217,v217,s54,v14\n"
MF "v[140:143],a[216:219],a[60:63],v[140:143]\n"
TR "a[208:209],v1 offset:32768\n"
"v_fma_f32 v218,v218,s54,v14\n"
MF "v[140:143],a[220:223],a[100:103],v[140:143]\n"
TR "a[210:211],v1 offset:33280\n"
"v_fma_f32 v219,v219,s54,v14\n"
MF "v[148:151],a[220:223],a[104:107],v[148:151]\n"
"v_fma_f32 v220,v220,s54,v14\n"
"v_fma_f32 v221,v221,s54,v14\n"
MF "v[148:151],a[216:219],a[64:67],v[148:151]\n"
TR "a[212:213],v1 offset:36864\n"
"v_fma_f32 v222,v222,s54,v14\n"
MF "v[156:159],a[216:219],a[68:71],v[156:159]\n"
".Lpg14c:\n"
"s_cbranch_execz .Lpg3a\n"
TR "a[214:215],v1 offset:37376\n"
"v_fma_f32 v223,v223,s54,v14\n"
MF "v[156:159],a[220:223],a[108:111],v[156:159]\n"
CV "v239,v222,v223\n"
CV "v238,v220,v221\n"
MF "v[164:167],a[220:223],a[112:115],v[164:167]\n"
CV "v237,v218,v219\n"
CV "v236,v216,v217\n"
MF "v[164:167],a[216:219],a[72:75],v[164:167]\n"
LD "v13,s[28:31],s82 offen offset:3072 lds\n"
MF "v[172:175],a[216:219],a[76:79],v[172:175]\n"
"buffer_store_dwordx4 v[236:239],v9,s[40:43],s75 offen offset:1024 sc1\n"
MF "v[172:175],a[220:223],a[116:119],v[172:175]\n"
"s_waitcnt lgkmcnt(4)\n"
"v_fma_f32 v208,v208,s54,v14\n"
"v_fma_f32 v209,v209,s54,v14\n"
MF "v[140:143],a[200:203],a[140:143],v[140:143]\n"
TR "a[216:217],v1 offset:49152\n"
"v_fma_f32 v210,v210,s54,v14\n"
MF "v[140:143],a[204:207],a[180:183],v[140:143]\n"
TR "a[218:219],v1 offset:49664\n"
"v_fma_f32 v211,v211,s54,v14\n"
MF "v[148:151],a[204:207],a[184:187],v[148:151]\n"
"v_fma_f32 v212,v212,s54,v14\n"
"v_fma_f32 v213,v213,s54,v14\n"
MF "v[148:151],a[200:203],a[144:147],v[148:151]\n"
TR "a[220:221],v1 offset:53248\n"
"v_fma_f32 v214,v214,s54,v14\n"
MF "v[156:159],a[200:203],a[148:151],v[156:159]\n"
TR "a[222:223],v1 offset:53760\n"
"v_fma_f32 v215,v215,s54,v14\n"
MF "v[156:159],a[204:207],a[188:191],v[156:159]\n"
CV "v235,v214,v215\n"
CV "v234,v212,v213\n"
MF "v[164:167],a[204:207],a[192:195],v[164:167]\n"
CV "v233,v210,v211\n"
CV "v232,v208,v209\n"
MF "v[164:167],a[200:203],a[152:155],v[164:167]\n"
"v_fma_f32 v200,v200,s54,v14\n"
"v_fma_f32 v201,v201,s54,v14\n"
MF "v[172:175],a[200:203],a[156:159],v[172:175]\n"
"buffer_store_dwordx4 v[232:235],v9,s[40:43],s75 offen sc1\n"
MF "v[172:175],a[204:207],a[196:199],v[172:175]\n"
"s_waitcnt lgkmcnt(0)\n"
"v_fma_f32 v202,v202,s54,v14\n"
"v_fma_f32 v203,v203,s54,v14\n"
MF "v[136:139],a[208:211],a[60:63],v[136:139]\n"
"s_add_u32 m0,s62,16384\n"
"s_add_u32 s82,s66,2359424\n"
TR "a[200:201],v0 offset:32776\n"
MF "v[136:139],a[212:215],a[100:103],v[136:139]\n"
"s_sub_u32 s15,s82,7864320\n"
"s_min_u32 s82,s82,s15\n"
TR "a[202:203],v0 offset:33288\n"
MF "v[144:147],a[212:215],a[104:107],v[144:147]\n"
LD "v6,s[28:31],s82 offen lds\n"
MF "v[144:147],a[208:211],a[64:67],v[144:147]\n"
TR "a[204:205],v0 offset:36872\n"
"v_fma_f32 v204,v204,s54,v14\n"
MF "v[152:155],a[208:211],a[68:71],v[152:155]\n"
TR "a[206:207],v0 offset:37384\n"
"v_fma_f32 v205,v205,s54,v14\n"
MF "v[152:155],a[212:215],a[108:111],v[152:155]\n"
"v_fma_f32 v206,v206,s54,v14\n"
"v_fma_f32 v207,v207,s54,v14\n"
MF "v[160:163],a[212:215],a[112:115],v[160:163]\n"
CV "v231,v206,v207\n"
CV "v230,v204,v205\n"
MF "v[160:163],a[208:211],a[72:75],v[160:163]\n"
CV "v229,v202,v203\n"
CV "v228,v200,v201\n"
MF "v[168:171],a[208:211],a[76:79],v[168:171]\n"
AW "a84,v228\n"
AW "a85,v229\n"
MF "v[168:171],a[212:215],a[116:119],v[168:171]\n"
LD "v11,s[28:31],s82 offen offset:1024 lds\n"
MF "v[168:171],a[216:219],a[156:159],v[168:171]\n"
TR "a[208:209],v0 offset:49160\n"
AW "a86,v230\n"
MF "v[168:171],a[220:223],a[196:199],v[168:171]\n"
TR "a[210:211],v0 offset:49672\n"
AW "a87,v231\n"
MF "v[160:163],a[220:223],a[192:195],v[160:163]\n"
"v_fma_f32 v192,v192,s54,v14\n"
"v_fma_f32 v193,v193,s54,v14\n"
MF "v[160:163],a[216:219],a[152:155],v[160:163]\n"
TR "a[212:213],v0 offset:53256\n"
"v_fma_f32 v194,v194,s54,v14\n"
MF "v[152:155],a[216:219],a[148:151],v[152:155]\n"
TR "a[214:215],v0 offset:53768\n"
"v_fma_f32 v195,v195,s54,v14\n"
MF "v[152:155],a[220:223],a[188:191],v[152:155]\n"
"v_fma_f32 v196,v196,s54,v14\n"
"v_fma_f32 v197,v197,s54,v14\n"
MF "v[144:147],a[220:223],a[184:187],v[144:147]\n"
"v_fma_f32 v198,v198,s54,v14\n"
"v_fma_f32 v199,v199,s54,v14\n"
MF "v[144:147],a[216:219],a[144:147],v[144:147]\n"
CV "v227,v198,v199\n"
CV "v226,v196,v197\n"
MF "v[136:139],a[216:219],a[140:143],v[136:139]\n"
LD "v12,s[28:31],s82 offen offset:2048 lds\n"
MF "v[136:139],a[220:223],a[180:183],v[136:139]\n"
"s_waitcnt lgkmcnt(4)\n"
CV "v225,v194,v195\n"
CV "v224,v192,v193\n"
MF "v[100:103],a[200:203],a[60:63],v[100:103]\n"
TR "a[216:217],v0 offset:32768\n"
AW "a80,v224\n"
MF "v[100:103],a[204:207],a[100:103],v[100:103]\n"
TR "a[218:219],v0 offset:33280\n"
AW "a81,v225\n"
MF "v[108:111],a[204:207],a[104:107],v[108:111]\n"
AW "a82,v226\n"
AW "a83,v227\n"
MF "v[108:111],a[200:203],a[64:67],v[108:111]\n"
TR "a[220:221],v0 offset:36864\n"
"v_fma_f32 v184,v184,s54,v14\n"
MF "v[116:119],a[200:203],a[68:71],v[116:119]\n"
TR "a[222:223],v0 offset:37376\n"
"v_fma_f32 v185,v185,s54,v14\n"
MF "v[116:119],a[204:207],a[108:111],v[116:119]\n"
"v_fma_f32 v186,v186,s54,v14\n"
"v_fma_f32 v187,v187,s54,v14\n"
MF "v[124:127],a[204:207],a[112:115],v[124:127]\n"
"v_fma_f32 v188,v188,s54,v14\n"
"v_fma_f32 v189,v189,s54,v14\n"
MF "v[124:127],a[200:203],a[72:75],v[124:127]\n"
LD "v13,s[28:31],s82 offen offset:3072 lds\n"
MF "v[132:135],a[200:203],a[76:79],v[132:135]\n"
"v_fma_f32 v190,v190,s54,v14\n"
"v_fma_f32 v191,v191,s54,v14\n"
MF "v[132:135],a[204:207],a[116:119],v[132:135]\n"
"s_waitcnt lgkmcnt(4)\n"
CV "v223,v190,v191\n"
CV "v222,v188,v189\n"
MF "v[100:103],a[208:211],a[140:143],v[100:103]\n"
TR "a[200:201],v0 offset:49152\n"
CV "v221,v186,v187\n"
MF "v[100:103],a[212:215],a[180:183],v[100:103]\n"
TR "a[202:203],v0 offset:49664\n"
CV "v220,v184,v185\n"
MF "v[108:111],a[212:215],a[184:187],v[108:111]\n"
AW "a56,v220\n"
AW "a57,v221\n"
MF "v[108:111],a[208:211],a[144:147],v[108:111]\n"
TR "a[204:205],v0 offset:53248\n"
AW "a58,v222\n"
MF "v[116:119],a[208:211],a[148:151],v[116:119]\n"
TR "a[206:207],v0 offset:53760\n"
AW "a59,v223\n"
MF "v[116:119],a[212:215],a[188:191],v[116:119]\n"
"v_fma_f32 v176,v176,s54,v14\n"
"v_fma_f32 v177,v177,s54,v14\n"
MF "v[124:127],a[212:215],a[192:195],v[124:127]\n"
"v_add_u32 v0,0xc000,v0\n"
"v_fma_f32 v178,v178,s54,v14\n"
MF "v[124:127],a[208:211],a[152:155],v[124:127]\n"
"v_add_u32 v1,0xc000,v1\n"
"v_fma_f32 v179,v179,s54,v14\n"
MF "v[132:135],a[208:211],a[156:159],v[132:135]\n"
"s_waitcnt vmcnt(14) lgkmcnt(0)\n"
"v_fma_f32 v180,v180,s54,v14\n"
"v_fma_f32 v181,v181,s54,v14\n"
MF "v[132:135],a[212:215],a[196:199],v[132:135]\n"
"s_barrier\n"
MF "v[96:99],a[216:219],a[60:63],v[96:99]\n"
"s_add_u32 m0,s62,32768\n"
"s_add_u32 s82,s66,2359552\n"
TR "a[208:209],v1 offset:16392\n"
MF "v[96:99],a[220:223],a[100:103],v[96:99]\n"
"s_sub_u32 s15,s82,7864320\n"
"s_min_u32 s82,s82,s15\n"
TR "a[210:211],v1 offset:16904\n"
MF "v[104:107],a[220:223],a[104:107],v[104:107]\n"
LD "v6,s[28:31],s82 offen lds\n"
MF "v[104:107],a[216:219],a[64:67],v[104:107]\n"
TR "a[212:213],v1 offset:20488\n"
"v_fma_f32 v182,v182,s54,v14\n"
MF "v[112:115],a[216:219],a[68:71],v[112:115]\n"
TR "a[214:215],v1 offset:21000\n"
"v_fma_f32 v183,v183,s54,v14\n"
MF "v[112:115],a[220:223],a[108:111],v[112:115]\n"
CV "v219,v182,v183\n"
CV "v218,v180,v181\n"
MF "v[120:123],a[220:223],a[112:115],v[120:123]\n"
CV "v217,v178,v179\n"
CV "v216,v176,v177\n"
MF "v[120:123],a[216:219],a[72:75],v[120:123]\n"
AW "a52,v216\n"
AW "a53,v217\n"
MF "v[128:131],a[216:219],a[76:79],v[128:131]\n"
AW "a54,v218\n"
AW "a55,v219\n"
MF "v[128:131],a[220:223],a[116:119],v[128:131]\n"
LD "v11,s[28:31],s82 offen offset:1024 lds\n"
MF "v[128:131],a[200:203],a[156:159],v[128:131]\n"
TR "a[216:217],v1 offset:32776\n"
"v_fma_f32 v168,v168,s54,v14\n"
MF "v[128:131],a[204:207],a[196:199],v[128:131]\n"
TR "a[218:219],v1 offset:33288\n"
"v_fma_f32 v169,v169,s54,v14\n"
MF "v[120:123],a[204:207],a[192:195],v[120:123]\n"
"v_fma_f32 v170,v170,s54,v14\n"
"v_fma_f32 v171,v171,s54,v14\n"
MF "v[120:123],a[200:203],a[152:155],v[120:123]\n"
TR "a[220:221],v1 offset:36872\n"
"v_fma_f32 v172,v172,s54,v14\n"
MF "v[112:115],a[200:203],a[148:151],v[112:115]\n"
TR "a[222:223],v1 offset:37384\n"
"v_fma_f32 v173,v173,s54,v14\n"
MF "v[112:115],a[204:207],a[188:191],v[112:115]\n"
"v_fma_f32 v174,v174,s54,v14\n"
"v_fma_f32 v175,v175,s54,v14\n"
MF "v[104:107],a[204:207],a[184:187],v[104:107]\n"
CV "v215,v174,v175\n"
CV "v214,v172,v173\n"
MF "v[104:107],a[200:203],a[144:147],v[104:107]\n"
CV "v213,v170,v171\n"
CV "v212,v168,v169\n"
MF "v[96:99],a[200:203],a[140:143],v[96:99]\n"
LD "v12,s[28:31],s82 offen offset:2048 lds\n"
MF "v[96:99],a[204:207],a[180:183],v[96:99]\n"
"s_waitcnt lgkmcnt(4)\n"
AW "a48,v212\n"
AW "a49,v213\n"
MF "v[60:63],a[208:211],a[60:63],v[60:63]\n"
TR "a[200:201],v1 offset:16384\n"
AW "a50,v214\n"
MF "v[60:63],a[212:215],a[100:103],v[60:63]\n"
TR "a[202:203],v1 offset:16896\n"
AW "a51,v215\n"
MF "v[68:71],a[212:215],a[104:107],v[68:71]\n"
"v_fma_f32 v160,v160,s54,v14\n"
"v_fma_f32 v161,v161,s54,v14\n"
MF "v[68:71],a[208:211],a[64:67],v[68:71]\n"
TR "a[204:205],v1 offset:20480\n"
"v_fma_f32 v162,v162,s54,v14\n"
MF "v[76:79],a[208:211],a[68:71],v[76:79]\n"
TR "a[206:207],v1 offset:20992\n"
"v_fma_f32 v163,v163,s54,v14\n"
MF "v[76:79],a[212:215],a[108:111],v[76:79]\n"
"v_fma_f32 v164,v164,s54,v14\n"
"v_fma_f32 v165,v165,s54,v14\n"
MF "v[84:87],a[212:215],a[112:115],v[84:87]\n"
"v_fma_f32 v166,v166,s54,v14\n"
"v_fma_f32 v167,v167,s54,v14\n"
MF "v[84:87],a[208:211],a[72:75],v[84:87]\n"
LD "v13,s[28:31],s82 offen offset:3072 lds\n"
MF "v[92:95],a[208:211],a[76:79],v[92:95]\n"
CV "v211,v166,v167\n"
CV "v210,v164,v165\n"
MF "v[92:95],a[212:215],a[116:119],v[92:95]\n"
"s_waitcnt lgkmcnt(4)\n"
CV "v209,v162,v163\n"
CV "v208,v160,v161\n"
MF "v[60:63],a[216:219],a[140:143],v[60:63]\n"
TR "a[208:209],v1 offset:32768\n"
AW "a44,v208\n"
MF "v[60:63],a[220:223],a[180:183],v[60:63]\n"
TR "a[210:211],v1 offset:33280\n"
AW "a45,v209\n"
MF "v[68:71],a[220:223],a[184:187],v[68:71]\n"
AW "a46,v210\n"
AW "a47,v211\n"
MF "v[68:71],a[216:219],a[144:147],v[68:71]\n"
TR "a[212:213],v1 offset:36864\n"
"v_fma_f32 v152,v152,s54,v14\n"
MF "v[76:79],a[216:219],a[148:151],v[76:79]\n"
TR "a[214:215],v1 offset:37376\n"
"v_fma_f32 v153,v153,s54,v14\n"
MF "v[76:79],a[220:223],a[188:191],v[76:79]\n"
"v_fma_f32 v154,v154,s54,v14\n"
"v_fma_f32 v155,v155,s54,v14\n"
MF "v[84:87],a[220:223],a[192:195],v[84:87]\n"
"v_fma_f32 v156,v156,s54,v14\n"
"v_fma_f32 v157,v157,s54,v14\n"
MF "v[84:87],a[216:219],a[152:155],v[84:87]\n"
"v_fma_f32 v158,v158,s54,v14\n"
"v_fma_f32 v159,v159,s54,v14\n"
MF "v[92:95],a[216:219],a[156:159],v[92:95]\n"
CV "v207,v158,v159\n"
CV "v206,v156,v157\n"
MF "v[92:95],a[220:223],a[196:199],v[92:95]\n"
"s_waitcnt lgkmcnt(0)\n"
CV "v205,v154,v155\n"
CV "v204,v152,v153\n"
MF "v[56:59],a[200:203],a[60:63],v[56:59]\n"
TR "a[216:217],v0 offset:16392\n"
AW "a40,v204\n"
MF "v[56:59],a[204:207],a[100:103],v[56:59]\n"
TR "a[218:219],v0 offset:16904\n"
AW "a41,v205\n"
MF "v[64:67],a[204:207],a[104:107],v[64:67]\n"
AW "a42,v206\n"
AW "a43,v207\n"
MF "v[64:67],a[200:203],a[64:67],v[64:67]\n"
TR "a[220:221],v0 offset:20488\n"
"v_fma_f32 v144,v144,s54,v14\n"
MF "v[72:75],a[200:203],a[68:71],v[72:75]\n"
TR "a[222:223],v0 offset:21000\n"
"v_fma_f32 v145,v145,s54,v14\n"
MF "v[72:75],a[204:207],a[108:111],v[72:75]\n"
"v_fma_f32 v146,v146,s54,v14\n"
"v_fma_f32 v147,v147,s54,v14\n"
MF "v[80:83],a[204:207],a[112:115],v[80:83]\n"
"v_fma_f32 v148,v148,s54,v14\n"
"v_fma_f32 v149,v149,s54,v14\n"
MF "v[80:83],a[200:203],a[72:75],v[80:83]\n"
"v_fma_f32 v150,v150,s54,v14\n"
"v_fma_f32 v151,v151,s54,v14\n"
MF "v[88:91],a[200:203],a[76:79],v[88:91]\n"
CV "v203,v150,v151\n"
CV "v202,v148,v149\n"
MF "v[88:91],a[204:207],a[116:119],v[88:91]\n"
CV "v201,v146,v147\n"
CV "v200,v144,v145\n"
MF "v[88:91],a[208:211],a[156:159],v[88:91]\n"
TR "a[200:201],v0 offset:32776\n"
AW "a16,v200\n"
MF "v[88:91],a[212:215],a[196:199],v[88:91]\n"
TR "a[202:203],v0 offset:33288\n"
AW "a17,v201\n"
MF "v[80:83],a[212:215],a[192:195],v[80:83]\n"
AW "a18,v202\n"
AW "a19,v203\n"
MF "v[80:83],a[208:211],a[152:155],v[80:83]\n"
TR "a[204:205],v0 offset:36872\n"
"v_fma_f32 v136,v136,s54,v14\n"
MF "v[72:75],a[208:211],a[148:151],v[72:75]\n"
TR "a[206:207],v0 offset:37384\n"
"v_fma_f32 v137,v137,s54,v14\n"
MF "v[72:75],a[212:215],a[188:191],v[72:75]\n"
"v_fma_f32 v138,v138,s54,v14\n"
"v_fma_f32 v139,v139,s54,v14\n"
MF "v[64:67],a[212:215],a[184:187],v[64:67]\n"
"v_fma_f32 v140,v140,s54,v14\n"
"v_fma_f32 v141,v141,s54,v14\n"
MF "v[64:67],a[208:211],a[144:147],v[64:67]\n"
"v_fma_f32 v142,v142,s54,v14\n"
"v_fma_f32 v143,v143,s54,v14\n"
MF "v[56:59],a[208:211],a[140:143],v[56:59]\n"
CV "v199,v142,v143\n"
CV "v198,v140,v141\n"
MF "v[56:59],a[212:215],a[180:183],v[56:59]\n"
"s_waitcnt lgkmcnt(4)\n"
CV "v197,v138,v139\n"
CV "v196,v136,v137\n"
MF "v[20:23],a[216:219],a[60:63],v[20:23]\n"
TR "a[208:209],v0 offset:16384\n"
AW "a12,v196\n"
MF "v[20:23],a[220:223],a[100:103],v[20:23]\n"
TR "a[210:211],v0 offset:16896\n"
AW "a13,v197\n"
MF "v[28:31],a[220:223],a[104:107],v[28:31]\n"
AW "a14,v198\n"
AW "a15,v199\n"
MF "v[28:31],a[216:219],a[64:67],v[28:31]\n"
TR "a[212:213],v0 offset:20480\n"
"v_fma_f32 v128,v128,s54,v14\n"
MF "v[36:39],a[216:219],a[68:71],v[36:39]\n"
TR "a[214:215],v0 offset:20992\n"
"v_fma_f32 v129,v129,s54,v14\n"
MF "v[36:39],a[220:223],a[108:111],v[36:39]\n"
"v_fma_f32 v130,v130,s54,v14\n"
"v_fma_f32 v131,v131,s54,v14\n"
MF "v[44:47],a[220:223],a[112:115],v[44:47]\n"
"v_fma_f32 v132,v132,s54,v14\n"
"v_fma_f32 v133,v133,s54,v14\n"
MF "v[44:47],a[216:219],a[72:75],v[44:47]\n"
"v_fma_f32 v134,v134,s54,v14\n"
"v_fma_f32 v135,v135,s54,v14\n"
MF "v[52:55],a[216:219],a[76:79],v[52:55]\n"
CV "v195,v134,v135\n"
CV "v194,v132,v133\n"
MF "v[52:55],a[220:223],a[116:119],v[52:55]\n"
"s_waitcnt lgkmcnt(4)\n"
CV "v193,v130,v131\n"
CV "v192,v128,v129\n"
MF "v[20:23],a[200:203],a[140:143],v[20:23]\n"
TR "a[216:217],v0 offset:32768\n"
AW "a8,v192\n"
MF "v[20:23],a[204:207],a[180:183],v[20:23]\n"
TR "a[218:219],v0 offset:33280\n"
AW "a9,v193\n"
MF "v[28:31],a[204:207],a[184:187],v[28:31]\n"
AW "a10,v194\n"
AW "a11,v195\n"
MF "v[28:31],a[200:203],a[144:147],v[28:31]\n"
TR "a[220:221],v0 offset:36864\n"
"v_fma_f32 v120,v120,s54,v14\n"
MF "v[36:39],a[200:203],a[148:151],v[36:39]\n"
TR "a[222:223],v0 offset:37376\n"
"v_fma_f32 v121,v121,s54,v14\n"
MF "v[36:39],a[204:207],a[188:191],v[36:39]\n"
"v_fma_f32 v122,v122,s54,v14\n"
"v_fma_f32 v123,v123,s54,v14\n"
MF "v[44:47],a[204:207],a[192:195],v[44:47]\n"
"v_fma_f32 v124,v124,s54,v14\n"
"v_fma_f32 v125,v125,s54,v14\n"
MF "v[44:47],a[200:203],a[152:155],v[44:47]\n"
"v_fma_f32 v126,v126,s54,v14\n"
"v_fma_f32 v127,v127,s54,v14\n"
MF "v[52:55],a[200:203],a[156:159],v[52:55]\n"
"s_waitcnt lgkmcnt(0)\n"
CV "v191,v126,v127\n"
CV "v190,v124,v125\n"
MF "v[52:55],a[204:207],a[196:199],v[52:55]\n"
"s_barrier\n"
MF "v[16:19],a[208:211],a[60:63],v[16:19]\n"
CV "v189,v122,v123\n"
CV "v188,v120,v121\n"
MF "v[16:19],a[212:215],a[100:103],v[16:19]\n"
AW "a4,v188\n"
AW "a5,v189\n"
MF "v[24:27],a[212:215],a[104:107],v[24:27]\n"
AW "a6,v190\n"
AW "a7,v191\n"
MF "v[24:27],a[208:211],a[64:67],v[24:27]\n"
"v_fma_f32 v112,v112,s54,v14\n"
"v_fma_f32 v113,v113,s54,v14\n"
MF "v[32:35],a[208:211],a[68:71],v[32:35]\n"
"v_fma_f32 v114,v114,s54,v14\n"
"v_fma_f32 v115,v115,s54,v14\n"
MF "v[32:35],a[212:215],a[108:111],v[32:35]\n"
"v_fma_f32 v116,v116,s54,v14\n"
"v_fma_f32 v117,v117,s54,v14\n"
MF "v[40:43],a[212:215],a[112:115],v[40:43]\n"
"v_fma_f32 v118,v118,s54,v14\n"
"v_fma_f32 v119,v119,s54,v14\n"
MF "v[40:43],a[208:211],a[72:75],v[40:43]\n"
CV "v187,v118,v119\n"
CV "v186,v116,v117\n"
MF "v[48:51],a[208:211],a[76:79],v[48:51]\n"
CV "v185,v114,v115\n"
CV "v184,v112,v113\n"
MF "v[48:51],a[212:215],a[116:119],v[48:51]\n"
AW "a0,v184\n"
AW "a1,v185\n"
MF "v[48:51],a[216:219],a[156:159],v[48:51]\n"
AW "a2,v186\n"
AW "a3,v187\n"
MF "v[48:51],a[220:223],a[196:199],v[48:51]\n"
"v_fma_f32 v104,v104,s54,v14\n"
"v_fma_f32 v105,v105,s54,v14\n"
MF "v[40:43],a[220:223],a[192:195],v[40:43]\n"
"v_fma_f32 v106,v106,s54,v14\n"
"v_fma_f32 v107,v107,s54,v14\n"
MF "v[40:43],a[216:219],a[152:155],v[40:43]\n"
"v_fma_f32 v108,v108,s54,v14\n"
"v_fma_f32 v109,v109,s54,v14\n"
MF "v[32:35],a[216:219],a[148:151],v[32:35]\n"
"v_fma_f32 v110,v110,s54,v14\n"
"v_fma_f32 v111,v111,s54,v14\n"
MF "v[32:35],a[220:223],a[188:191],v[32:35]\n"
CV "v183,v110,v111\n"
CV "v182,v108,v109\n"
MF "v[24:27],a[220:223],a[184:187],v[24:27]\n"
CV "v181,v106,v107\n"
CV "v180,v104,v105\n"
MF "v[24:27],a[216:219],a[144:147],v[24:27]\n"
AW "a36,v180\n"
AW "a37,v181\n"
MF "v[16:19],a[216:219],a[140:143],v[16:19]\n"
AW "a38,v182\n"
AW "a39,v183\n"
MF "v[16:19],a[220:223],a[180:183],v[16:19]\n"
"s_mov_b32 s57,1\n"
"s_mov_b32 s69,99\n"
AR "v0,a240\n"
AR "v2,a241\n"
AR "v4,a242\n"
AR "v6,a243\n"
"v_xor_b32 v1,64,v0\n"
"v_xor_b32 v3,64,v2\n"
"v_xor_b32_e64 v5,64,v4\n"
"v_add_u32 v11,97280,v6\n"
"v_add_u32 v12,194560,v6\n"
"v_add_u32 v13,291840,v6\n"
"s_mov_b64 vcc,-1\n"
"s_waitcnt vmcnt(10) lgkmcnt(0)\n"
"s_barrier\n"
"s_nop 0\n"
TR "a[200:201],v0\n"
TR "a[202:203],v0 offset:512\n"
TR "a[204:205],v0 offset:4096\n"
TR "a[206:207],v0 offset:4608\n"
"ds_read_b128 a[60:63],v2\n"
"ds_read_b128 a[100:103],v3\n"
"ds_read_b128 a[64:67],v2 offset:2048\n"
"ds_read_b128 a[104:107],v3 offset:2048\n"
"ds_read_b128 a[68:71],v2 offset:4096\n"
"ds_read_b128 a[108:111],v3 offset:4096\n"
"ds_read_b128 a[72:75],v2 offset:6144\n"
"ds_read_b128 a[112:115],v3 offset:6144\n"
"ds_read_b128 a[76:79],v2 offset:8192\n"
"ds_read_b128 a[116:119],v3 offset:8192\n"
TR "a[208:209],v0 offset:8\n"
TR "a[210:211],v0 offset:520\n"
TR "a[212:213],v0 offset:4104\n"
TR "a[214:215],v0 offset:4616\n"
"v_pk_fma_f32 v[96:97],v[96:97],s[54:55],v[14:15] op_sel_hi:[1,1,0]\n"
"v_pk_fma_f32 v[98:99],v[98:99],s[54:55],v[14:15] op_sel_hi:[1,1,0]\n"
"v_pk_fma_f32 v[100:101],v[100:101],s[54:55],v[14:15] op_sel_hi:[1,1,0]\n"
"v_pk_fma_f32 v[102:103],v[102:103],s[54:55],v[14:15] op_sel_hi:[1,1,0]\n"
CV "v179,v102,v103\n"
CV "v178,v100,v101\n"
CV "v177,v98,v99\n"
CV "v176,v96,v97\n"
AW "a32,v176\n"
AW "a33,v177\n"
AW "a34,v178\n"
AW "a35,v179\n"
"v_pk_fma_f32 v[88:89],v[88:89],s[54:55],v[14:15] op_sel_hi:[1,1,0]\n"
"v_pk_fma_f32 v[90:91],v[90:91],s[54:55],v[14:15] op_sel_hi:[1,1,0]\n"
"v_pk_fma_f32 v[92:93],v[92:93],s[54:55],v[14:15] op_sel_hi:[1,1,0]\n"
"v_pk_fma_f32 v[94:95],v[94:95],s[54:55],v[14:15] op_sel_hi:[1,1,0]\n"
CV "v175,v94,v95\n"
CV "v174,v92,v93\n"
CV "v173,v90,v91\n"
CV "v172,v88,v89\n"
AW "a28,v172\n"
".Lpg15c:\n"
"s_cbranch_execz .Lpg2a\n"
"s_nop 0\n"
AW "a29,v173\n"
AW "a30,v174\n"
AW "a31,v175\n"
"v_pk_fma_f32 v[80:81],v[80:81],s[54:55],v[14:15] op_sel_hi:[1,1,0]\n"
"v_pk_fma_f32 v[82:83],v[82:83],s[54:55],v[14:15] op_sel_hi:[1,1,0]\n"
"v_pk_fma_f32 v[84:85],v[84:85],s[54:55],v[14:15] op_sel_hi:[1,1,0]\n"
"v_pk_fma_f32 v[86:87],v[86:87],s[54:55],v[14:15] op_sel_hi:[1,1,0]\n"
CV "v171,v86,v87\n"
CV "v170,v84,v85\n"
CV "v169,v82,v83\n"
CV "v168,v80,v81\n"
AW "a24,v168\n"
AW "a25,v169\n"
AW "a26,v170\n"
AW "a27,v171\n"
"v_pk_fma_f32 v[72:73],v[72:73],s[54:55],v[14:15] op_sel_hi:[1,1,0]\n"
"v_pk_fma_f32 v[74:75],v[74:75],s[54:55],v[14:15] op_sel_hi:[1,1,0]\n"
"v_pk_fma_f32 v[76:77],v[76:77],s[54:55],v[14:15] op_sel_hi:[1,1,0]\n"
"v_pk_fma_f32 v[78:79],v[78:79],s[54:55],v[14:15] op_sel_hi:[1,1,0]\n"
CV "v167,v78,v79\n"
CV "v166,v76,v77\n"
CV "v165,v74,v75\n"
CV "v164,v72,v73\n"
AW "a20,v164\n"
AW "a21,v165\n"
AW "a22,v166\n"
AW "a23,v167\n"
"v_pk_fma_f32 v[64:65],v[64:65],s[54:55],v[14:15] op_sel_hi:[1,1,0]\n"
"v_pk_fma_f32 v[66:67],v[66:67],s[54:55],v[14:15] op_sel_hi:[1,1,0]\n"
"v_pk_fma_f32 v[68:69],v[68:69],s[54:55],v[14:15] op_sel_hi:[1,1,0]\n"
"v_pk_fma_f32 v[70:71],v[70:71],s[54:55],v[14:15] op_sel_hi:[1,1,0]\n"
CV "v163,v70,v71\n"
CV "v162,v68,v69\n"
CV "v161,v66,v67\n"
CV "v160,v64,v65\n"
AW "a176,v160\n"
AW "a177,v161\n"
AW "a178,v162\n"
AW "a179,v163\n"
"v_pk_fma_f32 v[56:57],v[56:57],s[54:55],v[14:15] op_sel_hi:[1,1,0]\n"
"v_pk_fma_f32 v[58:59],v[58:59],s[54:55],v[14:15] op_sel_hi:[1,1,0]\n"
"v_pk_fma_f32 v[60:61],v[60:61],s[54:55],v[14:15] op_sel_hi:[1,1,0]\n"
"v_pk_fma_f32 v[62:63],v[62:63],s[54:55],v[14:15] op_sel_hi:[1,1,0]\n"
CV "v159,v62,v63\n"
CV "v158,v60,v61\n"
CV "v157,v58,v59\n"
CV "v156,v56,v57\n"
AW "a172,v156\n"
AW "a173,v157\n"
AW "a174,v158\n"
AW "a175,v159\n"
"v_pk_mul_f32 v[48:49],v[48:49],s[54:55]\n"
"v_pk_mul_f32 v[50:51],v[50:51],s[54:55]\n"
"v_pk_mul_f32 v[52:53],v[52:53],s[54:55]\n"
"v_pk_mul_f32 v[54:55],v[54:55],s[54:55]\n"
CV "v155,v54,v55\n"
CV "v154,v52,v53\n"
CV "v153,v50,v51\n"
CV "v152,v48,v49\n"
AW "a168,v152\n"
AW "a169,v153\n"
AW "a170,v154\n"
AW "a171,v155\n"
"v_pk_mul_f32 v[40:41],v[40:41],s[54:55]\n"
"v_pk_mul_f32 v[42:43],v[42:43],s[54:55]\n"
"v_pk_mul_f32 v[44:45],v[44:45],s[54:55]\n"
"v_pk_mul_f32 v[46:47],v[46:47],s[54:55]\n"
CV "v151,v46,v47\n"
CV "v150,v44,v45\n"
CV "v149,v42,v43\n"
CV "v148,v40,v41\n"
AW "a120,v148\n"
AW "a121,v149\n"
AW "a122,v150\n"
AW "a123,v151\n"
"v_pk_mul_f32 v[32:33],v[32:33],s[54:55]\n"
"v_pk_mul_f32 v[34:35],v[34:35],s[54:55]\n"
"v_pk_mul_f32 v[36:37],v[36:37],s[54:55]\n"
"v_pk_mul_f32 v[38:39],v[38:39],s[54:55]\n"
CV "v147,v38,v39\n"
CV "v146,v36,v37\n"
CV "v145,v34,v35\n"
CV "v144,v32,v33\n"
AW "a96,v144\n"
AW "a97,v145\n"
AW "a98,v146\n"
AW "a99,v147\n"
"v_pk_mul_f32 v[24:25],v[24:25],s[54:55]\n"
"v_pk_mul_f32 v[26:27],v[26:27],s[54:55]\n"
"v_pk_mul_f32 v[28:29],v[28:29],s[54:55]\n"
"v_pk_mul_f32 v[30:31],v[30:31],s[54:55]\n"
CV "v143,v30,v31\n"
CV "v142,v28,v29\n"
CV "v141,v26,v27\n"
CV "v140,v24,v25\n"
AW "a92,v140\n"
AW "a93,v141\n"
AW "a94,v142\n"
AW "a95,v143\n"
"v_pk_mul_f32 v[16:17],v[16:17],s[54:55]\n"
"v_pk_mul_f32 v[18:19],v[18:19],s[54:55]\n"
"v_pk_mul_f32 v[20:21],v[20:21],s[54:55]\n"
"v_pk_mul_f32 v[22:23],v[22:23],s[54:55]\n"
CV "v139,v22,v23\n"
CV "v138,v20,v21\n"
CV "v137,v18,v19\n"
CV "v136,v16,v17\n"
AW "a88,v136\n"
AW "a89,v137\n"
AW "a90,v138\n"
AW "a91,v139\n"
"s_waitcnt lgkmcnt(0)\n"
"s_branch .Lq0\n"
".Lpgpsl_b:\n"
"s_cbranch_execz .Lpg3a\n"
".Lpsl:\n"
"s_sleep 1\n"
".Lpsub:\n"
"s_memrealtime s[100:101]\n"
"s_waitcnt lgkmcnt(0)\n"
"s_sub_u32 s101,s21,s100\n"
"s_cmp_gt_i32 s101,0\n"
"s_cbranch_scc1 .Lpsl\n"
"s_add_u32 s21,s100,s97\n"
"s_setpc_b64 s[52:53]\n"
".Lstub_6:\n"
"v_cmp_le_f32 vcc,0x47800000,v10\n"
"s_cmp_eq_u32 s77,0\n"
"s_cbranch_scc0 .Lnop_6\n"
"s_call_b64 s[52:53],.Lpsub\n"
".Lnop_6:\n"
"s_cbranch_vccz .Lback_6\n"
"s_call_b64 s[52:53],.Lslow_1\n"
"s_branch .Lback_6\n"
".Lstub_7:\n"
"v_cmp_le_f32 vcc,0x47800000,v10\n"
"s_cmp_eq_u32 s77,0\n"
"s_cbranch_scc0 .Lnop_7\n"
"s_call_b64 s[52:53],.Lpsub\n"
"s_cmp_eq_u32 s61,4\n"
"s_cselect_b32 s77,0x47800000,s77\n"
"s_cselect_b32 s95,0x47800000,s95\n"
".Lnop_7:\n"
"s_cbranch_vccz .Lback_7\n"
"s_call_b64 s[52:53],.Lslow_0\n"
"s_branch .Lback_7\n"
".Lstub_8:\n"
"s_call_b64 s[52:53],.Lslow_1\n"
"s_branch .Lback_8\n"
".Lstub_9:\n"
"s_call_b64 s[52:53],.Lslow_0\n"
"s_branch .Lback_9\n"
".Lstub_10:\n"
"s_call_b64 s[52:53],.Lslow_1\n"
"s_branch .Lback_10\n"
".Lstub_11:\n"
"s_call_b64 s[52:53],.Lslow_1\n"
"s_branch .Lback_11\n"
".Lstub_12:\n"
"s_call_b64 s[52:53],.Lslow_0\n"
"s_branch .Lback_12\n"
".Lstub_13:\n"
"s_call_b64 s[52:53],.Lslow_1\n"
"s_branch .Lback_13\n"
".Lstub_20:\n"
"v_cmp_le_f32 vcc,0x47800000,v10\n"
"s_cmp_eq_u32 s77,0\n"
"s_cbranch_scc0 .Lnop_20\n"
"s_call_b64 s[52:53],.Lpsub\n"
".Lnop_20:\n"
"s_cbranch_vccz .Lback_20\n"
"s_call_b64 s[52:53],.Lslow_1\n"
"s_branch .Lback_20\n"
".Lstub_21:\n"
"v_cmp_le_f32 vcc,0x47800000,v10\n"
"s_cmp_eq_u32 s77,0\n"
"s_cbranch_scc0 .Lnop_21\n"
"s_call_b64 s[52:53],.Lpsub\n"
".Lnop_21:\n"
"s_cbranch_vccz .Lback_21\n"
"s_call_b64 s[52:53],.Lslow_0\n"
"s_branch .Lback_21\n"
".Lstub_22:\n"
"v_cmp_le_f32 vcc,0x47800000,v10\n"
"s_cmp_eq_u32 s77,0\n"
"s_cbranch_scc0 .Lnop_22\n"
"s_call_b64 s[52:53],.Lpsub\n"
".Lnop_22:\n"
"s_cbranch_vccz .Lback_22\n"
"s_call_b64 s[52:53],.Lslow_1\n"
"s_branch .Lback_22\n"
".Lstub_23:\n"
"v_cmp_le_f32 vcc,0x47800000,v10\n"
"s_cmp_eq_u32 s77,0\n"
"s_cbranch_scc0 .Lnop_23\n"
"s_call_b64 s[52:53],.Lpsub\n"
".Lnop_23:\n"
"s_cbranch_vccz .Lback_23\n"
"s_call_b64 s[52:53],.Lslow_0\n"
"s_branch .Lback_23\n"
".Lstub_24:\n"
"v_cmp_le_f32 vcc,0x47800000,v10\n"
"s_cmp_eq_u32 s61,1\n"
"s_cbranch_scc0 .Lhook_24\n"
"s_mov_b64 exec,0\n"
"s_cmp_eq_u32 s56,0\n"
"s_cbranch_scc1 .Lpg26h\n"
"s_cmp_eq_u32 s56,1\n"
"s_cbranch_scc1 .Lpg25h\n"
"s_cmp_eq_u32 s56,2\n"
"s_cbranch_scc1 .Lpg24h\n"
"s_branch .Lpg23h\n"
".Lisr4:\n"
"s_mov_b64 exec,-1\n"
".Lisx4:\n"
"s_and_b32 s58,s56,1\n"
"s_mul_i32 s79,s58,12288\n"
"s_lshl_b32 s15,s58,10\n"
"s_lshr_b32 s58,s56,1\n"
"s_mul_i32 s5,s58,6144\n"
"s_add_u32 s5,s5,s79\n"
"s_lshl_b32 s59,s58,8\n"
"s_add_u32 s15,s15,s59\n"
"s_add_u32 s15,s15,159744\n"
"s_add_u32 s88,s88,1024\n"
"s_add_u32 s89,s89,1024\n"
"s_mul_i32 s58,s58,61440\n"
"s_add_u32 s2,s89,s58\n"
"s_add_u32 s3,s88,s58\n"
"s_add_u32 s0,s2,122880\n"
"s_add_u32 s1,s3,122880\n"
"s_lshl_b32 s58,s56,10\n"
"s_add_u32 s9,s58,73728\n"
"s_add_u32 s14,s58,159744\n"
"s_bfe_u32 s4,s22,0x10014\n"
"s_lshl_b32 s4,s4,5\n"
"s_mov_b32 s6,896\n"
"s_mov_b32 s97,0\n"
".Lhook_24:\n"
"s_cmp_eq_u32 s77,0\n"
"s_cbranch_scc0 .Lnop_24\n"
"s_call_b64 s[52:53],.Lpsub\n"
".Lnop_24:\n"
"s_cbranch_vccz .Lback_24\n"
"s_call_b64 s[52:53],.Lslow_1\n"
"s_branch .Lback_24\n"
".Lstub_25:\n"
"v_cmp_le_f32 vcc,0x47800000,v10\n"
"s_cmp_eq_u32 s77,0\n"
"s_cbranch_scc0 .Lnop_25\n"
"s_call_b64 s[52:53],.Lpsub\n"
".Lnop_25:\n"
"s_cbranch_vccz .Lback_25\n"
"s_call_b64 s[52:53],.Lslow_0\n"
"s_branch .Lback_25\n"
".Lstub_26:\n"
"s_call_b64 s[52:53],.Lslow_1\n"
"s_branch .Lback_26\n"
".Lstub_27:\n"
"s_call_b64 s[52:53],.Lslow_0\n"
"s_branch .Lback_27\n"
".Lstub_28:\n"
"s_call_b64 s[52:53],.Lslow_1\n"
"s_branch .Lback_28\n"
".Lackspin_q:\n"
"s_cmp_eq_u32 s98,0\n"
"s_cbranch_scc1 .Lackok_q\n"
"s_sub_u32 s98,s98,1\n"
"s_sleep 8\n"
"v_bfe_u32 v14,v9,4,5\n"
"v_lshlrev_b32 v14,6,v14\n"
"s_lshl_b32 s58,s78,2\n"
"s_add_u32 s58,s58,s86\n"
"buffer_load_dword v10,v14,s[40:43],s58 offen offset:128 sc1\n"
"s_waitcnt vmcnt(0)\n"
"s_branch .Lack_q\n"
".Lpgep_g:\n"
"s_cbranch_execz .Lisr6\n"
".Lep:\n"
MF "v[72:75],v[16:19],v[136:139],v[32:35]\n"
"s_nop 0\n"
"ds_read_b128 v[24:27],v0 offset:1024\n"
"ds_read_b128 v[28:31],v1 offset:1024\n"
MF "v[72:75],v[20:23],v[156:159],v[72:75]\n"
MF "v[76:79],v[20:23],v[160:163],v[36:39]\n"
MF "v[76:79],v[16:19],v[140:143],v[76:79]\n"
MF "v[80:83],v[16:19],v[144:147],v[40:43]\n"
MF "v[80:83],v[20:23],v[164:167],v[80:83]\n"
MF "v[84:87],v[20:23],v[168:171],v[44:47]\n"
MF "v[84:87],v[16:19],v[148:151],v[84:87]\n"
"s_add_u32 m0,s65,49152\n"
MF "v[88:91],v[16:19],v[152:155],v[48:51]\n"
LD "v8,s[36:39],s73 offen offset:3072 nt lds\n"
MF "v[88:91],v[20:23],v[172:175],v[88:91]\n"
"s_waitcnt lgkmcnt(0)\n"
"ds_read_b128 v[16:19],v0 offset:2048\n"
"ds_read_b128 v[20:23],v1 offset:2048\n"
MF "v[88:91],v[24:27],v[192:195],v[88:91]\n"
"v_exp_f32 v124,v124\n"
MF "v[88:91],v[28:31],v[212:215],v[88:91]\n"
"s_add_u32 m0,s65,50048\n"
"v_exp_f32 v125,v125\n"
MF "v[84:87],v[28:31],v[208:211],v[84:87]\n"
LD "v8,s[36:39],s73 offen offset:3200 nt lds\n"
MF "v[84:87],v[24:27],v[188:191],v[84:87]\n"
"v_exp_f32 v126,v126\n"
MF "v[80:83],v[24:27],v[184:187],v[80:83]\n"
"v_exp_f32 v127,v127\n"
MF "v[80:83],v[28:31],v[204:207],v[80:83]\n"
"s_add_u32 m0,s65,50944\n"
"v_add_f32 v60,v60,v124\n"
"v_add_f32 v60,v60,v125\n"
MF "v[76:79],v[28:31],v[200:203],v[76:79]\n"
LD "v8,s[36:39],s73 offen offset:3328 nt lds\n"
MF "v[76:79],v[24:27],v[180:183],v[76:79]\n"
CV "v106,v124,v125\n"
"v_add_f32 v60,v60,v126\n"
MF "v[72:75],v[24:27],v[176:179],v[72:75]\n"
"v_add_f32 v60,v60,v127\n"
CV "v107,v126,v127\n"
MF "v[72:75],v[28:31],v[196:199],v[72:75]\n"
"s_waitcnt lgkmcnt(0)\n"
"ds_read_b128 v[24:27],v0 offset:6144\n"
"ds_read_b128 v[28:31],v1 offset:6144\n"
MF "v[72:75],v[16:19],v[216:219],v[72:75]\n"
"v_exp_f32 v128,v128\n"
MF "v[72:75],v[20:23],v[236:239],v[72:75]\n"
"v_exp_f32 v129,v129\n"
MF "v[76:79],v[20:23],v[240:243],v[76:79]\n"
"v_exp_f32 v130,v130\n"
MF "v[76:79],v[16:19],v[220:223],v[76:79]\n"
"v_exp_f32 v131,v131\n"
MF "v[80:83],v[16:19],v[224:227],v[80:83]\n"
"v_add_f32 v61,v61,v128\n"
"v_add_f32 v61,v61,v129\n"
MF "v[80:83],v[20:23],v[244:247],v[80:83]\n"
CV "v110,v128,v129\n"
"v_add_f32 v61,v61,v130\n"
MF "v[84:87],v[20:23],v[248:251],v[84:87]\n"
"v_add_f32 v61,v61,v131\n"
CV "v111,v130,v131\n"
MF "v[84:87],v[16:19],v[228:231],v[84:87]\n"
"v_max3_f32 v10,v57,v58,v59\n"
"v_max3_f32 v10,v10,v60,v61\n"
MF "v[88:91],v[16:19],v[232:235],v[88:91]\n"
"v_cmp_le_f32 vcc,0x47800000,v10\n"
MF "v[88:91],v[20:23],v[252:255],v[88:91]\n"
"s_waitcnt lgkmcnt(0)\n"
"s_cbranch_vccnz .Lstub_11\n"
".Lback_11:\n"
MF "v[112:115],v[24:27],v[136:139],v[32:35]\n"
"s_add_u32 s74,s74,12288\n"
"ds_read_b128 v[16:19],v0 offset:7168\n"
"ds_read_b128 v[20:23],v1 offset:7168\n"
MF "v[112:115],v[28:31],v[156:159],v[112:115]\n"
"s_cmp_eq_u32 s74,s20\n"
"s_cselect_b32 s74,s70,s74\n"
MF "v[116:119],v[28:31],v[160:163],v[36:39]\n"
"v_exp_f32 v72,v72\n"
MF "v[116:119],v[24:27],v[140:143],v[116:119]\n"
"v_exp_f32 v73,v73\n"
MF "v[120:123],v[24:27],v[144:147],v[40:43]\n"
"v_exp_f32 v74,v74\n"
MF "v[120:123],v[28:31],v[164:167],v[120:123]\n"
"v_exp_f32 v75,v75\n"
MF "v[124:127],v[28:31],v[168:171],v[44:47]\n"
"v_add_f32 v52,v57,v72\n"
"v_add_f32 v52,v52,v73\n"
MF "v[124:127],v[24:27],v[148:151],v[124:127]\n"
CV "v72,v72,v73\n"
"v_add_f32 v52,v52,v74\n"
MF "v[128:131],v[24:27],v[152:155],v[48:51]\n"
"v_add_f32 v52,v52,v75\n"
CV "v73,v74,v75\n"
MF "v[128:131],v[28:31],v[172:175],v[128:131]\n"
"s_waitcnt lgkmcnt(0)\n"
"ds_read_b128 v[24:27],v0 offset:8192\n"
"ds_read_b128 v[28:31],v1 offset:8192\n"
MF "v[128:131],v[16:19],v[192:195],v[128:131]\n"
"v_exp_f32 v76,v76\n"
MF "v[128:131],v[20:23],v[212:215],v[128:131]\n"
"v_exp_f32 v77,v77\n"
MF "v[124:127],v[20:23],v[208:211],v[124:127]\n"
"v_exp_f32 v78,v78\n"
MF "v[124:127],v[16:19],v[188:191],v[124:127]\n"
"v_exp_f32 v79,v79\n"
MF "v[120:123],v[16:19],v[184:187],v[120:123]\n"
"v_add_f32 v53,v58,v76\n"
"v_add_f32 v53,v53,v77\n"
MF "v[120:123],v[20:23],v[204:207],v[120:123]\n"
CV "v76,v76,v77\n"
"v_add_f32 v53,v53,v78\n"
MF "v[116:119],v[20:23],v[200:203],v[116:119]\n"
"s_waitcnt vmcnt(12) lgkmcnt(0)\n"
"v_add_f32 v53,v53,v79\n"
CV "v77,v78,v79\n"
MF "v[116:119],v[16:19],v[180:183],v[116:119]\n"
"s_barrier\n"
MF "v[112:115],v[16:19],v[176:179],v[112:115]\n"
"v_exp_f32 v80,v80\n"
MF "v[112:115],v[20:23],v[196:199],v[112:115]\n"
"s_sub_u32 s58,s71,s70\n"
TR "v[16:17],v2 offset:24576\n"
TR "v[18:19],v2 offset:30720\n"
MF "v[112:115],v[24:27],v[216:219],v[112:115]\n"
"s_add_u32 s72,s72,s58\n"
TR "v[20:21],v3 offset:24576\n"
TR "v[22:23],v3 offset:30720\n"
MF "v[112:115],v[28:31],v[236:239],v[112:115]\n"
"s_add_u32 s16,s16,s58\n"
"s_add_u32 s17,s17,s58\n"
"s_add_u32 s72,s72,6144\n"
MF "v[116:119],v[28:31],v[240:243],v[116:119]\n"
"s_cmp_eq_u32 s72,s17\n"
"s_cselect_b32 s72,s16,s72\n"
"s_add_u32 m0,s64,0\n"
MF "v[116:119],v[24:27],v[220:223],v[116:119]\n"
LD "v7,s[32:35],s72 offen nt lds\n"
MF "v[120:123],v[24:27],v[224:227],v[120:123]\n"
"s_add_u32 m0,s64,896\n"
"v_exp_f32 v81,v81\n"
MF "v[120:123],v[28:31],v[244:247],v[120:123]\n"
LD "v7,s[32:35],s72 offen offset:128 nt lds\n"
MF "v[124:127],v[28:31],v[248:251],v[124:127]\n"
"s_add_u32 m0,s64,1792\n"
"v_exp_f32 v82,v82\n"
MF "v[124:127],v[24:27],v[228:231],v[124:127]\n"
LD "v7,s[32:35],s72 offen offset:256 nt lds\n"
MF "v[128:131],v[24:27],v[232:235],v[128:131]\n"
"s_add_u32 s72,s72,6144\n"
"v_exp_f32 v83,v83\n"
MF "v[128:131],v[28:31],v[252:255],v[128:131]\n"
"s_waitcnt lgkmcnt(0)\n"
TR "v[24:25],v4 offset:24576\n"
TR "v[26:27],v4 offset:30720\n"
MF "a[0:3],v[16:19],v[92:95],a[0:3]\n"
"s_add_u32 m0,s64,6144\n"
TR "v[28:29],v5 offset:24576\n"
TR "v[30:31],v5 offset:30720\n"
MF "a[20:23],v[20:23],v[92:95],a[20:23]\n"
LD "v7,s[32:35],s72 offen nt lds\n"
MF "a[24:27],v[20:23],v[96:99],a[24:27]\n"
"v_add_f32 v54,v59,v80\n"
"v_add_f32 v54,v54,v81\n"
MF "a[4:7],v[16:19],v[96:99],a[4:7]\n"
"s_add_u32 m0,s64,7040\n"
CV "v80,v80,v81\n"
"v_add_f32 v54,v54,v82\n"
MF "a[8:11],v[16:19],v[100:103],a[8:11]\n"
LD "v7,s[32:35],s72 offen offset:128 nt lds\n"
MF "a[28:31],v[20:23],v[100:103],a[28:31]\n"
"v_add_f32 v54,v54,v83\n"
CV "v81,v82,v83\n"
MF "a[32:35],v[20:23],v[104:107],a[32:35]\n"
"s_add_u32 m0,s64,7936\n"
"v_exp_f32 v84,v84\n"
MF "a[12:15],v[16:19],v[104:107],a[12:15]\n"
LD "v7,s[32:35],s72 offen offset:256 nt lds\n"
MF "a[16:19],v[16:19],v[108:111],a[16:19]\n"
"v_exp_f32 v85,v85\n"
MF "a[36:39],v[20:23],v[108:111],a[36:39]\n"
"s_waitcnt lgkmcnt(0)\n"
TR "v[16:17],v2 offset:25600\n"
TR "v[18:19],v2 offset:31744\n"
MF "a[56:59],v[24:27],v[108:111],a[56:59]\n"
"s_nop 0\n"
TR "v[20:21],v3 offset:25600\n"
TR "v[22:23],v3 offset:31744\n"
MF "a[76:79],v[28:31],v[108:111],a[76:79]\n"
"v_exp_f32 v86,v86\n"
MF "a[72:75],v[28:31],v[104:107],a[72:75]\n"
"v_exp_f32 v87,v87\n"
MF "a[52:55],v[24:27],v[104:107],a[52:55]\n"
"v_add_f32 v55,v60,v84\n"
"v_add_f32 v55,v55,v85\n"
MF "a[48:51],v[24:27],v[100:103],a[48:51]\n"
CV "v84,v84,v85\n"
"v_add_f32 v55,v55,v86\n"
MF "a[68:71],v[28:31],v[100:103],a[68:71]\n"
"v_add_f32 v55,v55,v87\n"
CV "v85,v86,v87\n"
MF "a[64:67],v[28:31],v[96:99],a[64:67]\n"
"v_exp_f32 v88,v88\n"
MF "a[44:47],v[24:27],v[96:99],a[44:47]\n"
"v_exp_f32 v89,v89\n"
MF "a[40:43],v[24:27],v[92:95],a[40:43]\n"
"v_exp_f32 v90,v90\n"
MF "a[60:63],v[28:31],v[92:95],a[60:63]\n"
"s_waitcnt lgkmcnt(0)\n"
TR "v[24:25],v4 offset:25600\n"
TR "v[26:27],v4 offset:31744\n"
MF "a[80:83],v[16:19],v[92:95],a[80:83]\n"
"s_nop 0\n"
TR "v[28:29],v5 offset:25600\n"
TR "v[30:31],v5 offset:31744\n"
MF "a[100:103],v[20:23],v[92:95],a[100:103]\n"
"v_exp_f32 v91,v91\n"
MF "a[104:107],v[20:23],v[96:99],a[104:107]\n"
"v_add_f32 v56,v61,v88\n"
"v_add_f32 v56,v56,v89\n"
MF "a[84:87],v[16:19],v[96:99],a[84:87]\n"
CV "v88,v88,v89\n"
"v_add_f32 v56,v56,v90\n"
MF "a[88:91],v[16:19],v[100:103],a[88:91]\n"
"v_add_f32 v56,v56,v91\n"
CV "v89,v90,v91\n"
MF "a[108:111],v[20:23],v[100:103],a[108:111]\n"
"v_exp_f32 v112,v112\n"
MF "a[112:115],v[20:23],v[104:107],a[112:115]\n"
"v_exp_f32 v113,v113\n"
MF "a[92:95],v[16:19],v[104:107],a[92:95]\n"
"v_exp_f32 v114,v114\n"
MF "a[96:99],v[16:19],v[108:111],a[96:99]\n"
"v_exp_f32 v115,v115\n"
MF "a[116:119],v[20:23],v[108:111],a[116:119]\n"
"s_waitcnt lgkmcnt(0)\n"
TR "v[16:17],v2 offset:26624\n"
TR "v[18:19],v2 offset:32768\n"
MF "a[136:139],v[24:27],v[108:111],a[136:139]\n"
"s_nop 0\n"
TR "v[20:21],v3 offset:26624\n"
TR "v[22:23],v3 offset:32768\n"
MF "a[156:159],v[28:31],v[108:111],a[156:159]\n"
"v_add_f32 v52,v52,v112\n"
"v_add_f32 v52,v52,v113\n"
MF "a[152:155],v[28:31],v[104:107],a[152:155]\n"
CV "v74,v112,v113\n"
"v_add_f32 v52,v52,v114\n"
MF "a[132:135],v[24:27],v[104:107],a[132:135]\n"
"v_add_f32 v52,v52,v115\n"
CV "v75,v114,v115\n"
MF "a[128:131],v[24:27],v[100:103],a[128:131]\n"
"v_exp_f32 v116,v116\n"
MF "a[148:151],v[28:31],v[100:103],a[148:151]\n"
"v_exp_f32 v117,v117\n"
MF "a[144:147],v[28:31],v[96:99],a[144:147]\n"
"v_exp_f32 v118,v118\n"
MF "a[124:127],v[24:27],v[96:99],a[124:127]\n"
"v_exp_f32 v119,v119\n"
MF "a[120:123],v[24:27],v[92:95],a[120:123]\n"
"v_add_f32 v53,v53,v116\n"
"v_add_f32 v53,v53,v117\n"
MF "a[140:143],v[28:31],v[92:95],a[140:143]\n"
"s_waitcnt lgkmcnt(0)\n"
TR "v[24:25],v4 offset:26624\n"
TR "v[26:27],v4 offset:32768\n"
MF "a[160:163],v[16:19],v[92:95],a[160:163]\n"
"s_nop 0\n"
TR "v[28:29],v5 offset:26624\n"
TR "v[30:31],v5 offset:32768\n"
MF "a[180:183],v[20:23],v[92:95],a[180:183]\n"
CV "v78,v116,v117\n"
"v_add_f32 v53,v53,v118\n"
MF "a[184:187],v[20:23],v[96:99],a[184:187]\n"
"v_add_f32 v53,v53,v119\n"
CV "v79,v118,v119\n"
MF "a[164:167],v[16:19],v[96:99],a[164:167]\n"
"v_exp_f32 v120,v120\n"
MF "a[168:171],v[16:19],v[100:103],a[168:171]\n"
"v_exp_f32 v121,v121\n"
MF "a[188:191],v[20:23],v[100:103],a[188:191]\n"
"v_exp_f32 v122,v122\n"
MF "a[192:195],v[20:23],v[104:107],a[192:195]\n"
"v_exp_f32 v123,v123\n"
MF "a[172:175],v[16:19],v[104:107],a[172:175]\n"
"s_waitcnt vmcnt(12) lgkmcnt(0)\n"
"v_add_f32 v54,v54,v120\n"
"v_add_f32 v54,v54,v121\n"
MF "a[176:179],v[16:19],v[108:111],a[176:179]\n"
"s_barrier\n"
MF "a[196:199],v[20:23],v[108:111],a[196:199]\n"
"s_add_u32 s73,s73,12288\n"
"ds_read_b128 v[16:19],v0 offset:24576\n"
"ds_read_b128 v[20:23],v1 offset:24576\n"
MF "a[216:219],v[24:27],v[108:111],a[216:219]\n"
"s_cmp_eq_u32 s73,s19\n"
"s_cselect_b32 s73,s18,s73\n"
"s_add_u32 m0,s65,73728\n"
MF "a[236:239],v[28:31],v[108:111],a[236:239]\n"
LD "v8,s[36:39],s73 offen nt lds\n"
MF "a[232:235],v[28:31],v[104:107],a[232:235]\n"
"s_add_u32 m0,s65,74624\n"
CV "v82,v120,v121\n"
"v_add_f32 v54,v54,v122\n"
MF "a[212:215],v[24:27],v[104:107],a[212:215]\n"
LD "v8,s[36:39],s73 offen offset:128 nt lds\n"
MF "a[208:211],v[24:27],v[100:103],a[208:211]\n"
"s_add_u32 m0,s65,75520\n"
"v_add_f32 v54,v54,v123\n"
CV "v83,v122,v123\n"
MF "a[228:231],v[28:31],v[100:103],a[228:231]\n"
LD "v8,s[36:39],s73 offen offset:256 nt lds\n"
MF "a[224:227],v[28:31],v[96:99],a[224:227]\n"
MF "a[204:207],v[24:27],v[96:99],a[204:207]\n"
MF "a[200:203],v[24:27],v[92:95],a[200:203]\n"
MF "a[220:223],v[28:31],v[92:95],a[220:223]\n"
"s_waitcnt lgkmcnt(0)\n"
MF "v[92:95],v[16:19],v[136:139],v[32:35]\n"
"s_add_u32 m0,s65,73728\n"
"ds_read_b128 v[24:27],v0 offset:25600\n"
"ds_read_b128 v[28:31],v1 offset:25600\n"
MF "v[92:95],v[20:23],v[156:159],v[92:95]\n"
LD "v8,s[36:39],s73 offen offset:3072 nt lds\n"
MF "v[96:99],v[20:23],v[160:163],v[36:39]\n"
"v_exp_f32 v124,v124\n"
MF "v[96:99],v[16:19],v[140:143],v[96:99]\n"
"v_exp_f32 v125,v125\n"
MF "v[100:103],v[16:19],v[144:147],v[40:43]\n"
"s_add_u32 m0,s65,74624\n"
"v_exp_f32 v126,v126\n"
MF "v[100:103],v[20:23],v[164:167],v[100:103]\n"
LD "v8,s[36:39],s73 offen offset:3200 nt lds\n"
MF "v[104:107],v[20:23],v[168:171],v[44:47]\n"
"v_exp_f32 v127,v127\n"
MF "v[104:107],v[16:19],v[148:151],v[104:107]\n"
"s_add_u32 m0,s65,75520\n"
"v_add_f32 v55,v55,v124\n"
"v_add_f32 v55,v55,v125\n"
MF "v[108:111],v[16:19],v[152:155],v[48:51]\n"
LD "v8,s[36:39],s73 offen offset:3328 nt lds\n"
MF "v[108:111],v[20:23],v[172:175],v[108:111]\n"
"s_waitcnt lgkmcnt(0)\n"
"ds_read_b128 v[16:19],v0 offset:26624\n"
"ds_read_b128 v[20:23],v1 offset:26624\n"
MF "v[108:111],v[24:27],v[192:195],v[108:111]\n"
CV "v86,v124,v125\n"
"v_add_f32 v55,v55,v126\n"
MF "v[108:111],v[28:31],v[212:215],v[108:111]\n"
"v_add_f32 v55,v55,v127\n"
CV "v87,v126,v127\n"
MF "v[104:107],v[28:31],v[208:211],v[104:107]\n"
"v_exp_f32 v128,v128\n"
MF "v[104:107],v[24:27],v[188:191],v[104:107]\n"
"v_exp_f32 v129,v129\n"
MF "v[100:103],v[24:27],v[184:187],v[100:103]\n"
"v_exp_f32 v130,v130\n"
MF "v[100:103],v[28:31],v[204:207],v[100:103]\n"
"v_exp_f32 v131,v131\n"
MF "v[96:99],v[28:31],v[200:203],v[96:99]\n"
"v_add_f32 v56,v56,v128\n"
"v_add_f32 v56,v56,v129\n"
MF "v[96:99],v[24:27],v[180:183],v[96:99]\n"
CV "v90,v128,v129\n"
"v_add_f32 v56,v56,v130\n"
MF "v[92:95],v[24:27],v[176:179],v[92:95]\n"
"v_add_f32 v56,v56,v131\n"
CV "v91,v130,v131\n"
MF "v[92:95],v[28:31],v[196:199],v[92:95]\n"
"s_waitcnt lgkmcnt(0)\n"
"ds_read_b128 v[24:27],v0 offset:30720\n"
"ds_read_b128 v[28:31],v1 offset:30720\n"
MF "v[92:95],v[16:19],v[216:219],v[92:95]\n"
MF "v[92:95],v[20:23],v[236:239],v[92:95]\n"
MF "v[96:99],v[20:23],v[240:243],v[96:99]\n"
MF "v[96:99],v[16:19],v[220:223],v[96:99]\n"
MF "v[100:103],v[16:19],v[224:227],v[100:103]\n"
MF "v[100:103],v[20:23],v[244:247],v[100:103]\n"
MF "v[104:107],v[20:23],v[248:251],v[104:107]\n"
MF "v[104:107],v[16:19],v[228:231],v[104:107]\n"
"v_max3_f32 v10,v52,v53,v54\n"
"v_max3_f32 v10,v10,v55,v56\n"
MF "v[108:111],v[16:19],v[232:235],v[108:111]\n"
"v_cmp_le_f32 vcc,0x47800000,v10\n"
MF "v[108:111],v[20:23],v[252:255],v[108:111]\n"
"s_waitcnt lgkmcnt(0)\n"
"s_cbranch_vccnz .Lstub_12\n"
".Lback_12:\n"
MF "v[112:115],v[24:27],v[136:139],v[32:35]\n"
".Lpg16g:\n"
"s_cbranch_execz .Lisr6\n"
"s_add_u32 s74,s74,12288\n"
"ds_read_b128 v[16:19],v0 offset:31744\n"
"ds_read_b128 v[20:23],v1 offset:31744\n"
MF "v[112:115],v[28:31],v[156:159],v[112:115]\n"
"s_cmp_eq_u32 s74,s20\n"
"s_cselect_b32 s74,s70,s74\n"
MF "v[116:119],v[28:31],v[160:163],v[36:39]\n"
"v_exp_f32 v92,v92\n"
MF "v[116:119],v[24:27],v[140:143],v[116:119]\n"
"v_exp_f32 v93,v93\n"
MF "v[120:123],v[24:27],v[144:147],v[40:43]\n"
"v_exp_f32 v94,v94\n"
MF "v[120:123],v[28:31],v[164:167],v[120:123]\n"
"v_exp_f32 v95,v95\n"
MF "v[124:127],v[28:31],v[168:171],v[44:47]\n"
"v_add_f32 v57,v52,v92\n"
"v_add_f32 v57,v57,v93\n"
MF "v[124:127],v[24:27],v[148:151],v[124:127]\n"
CV "v92,v92,v93\n"
"v_add_f32 v57,v57,v94\n"
MF "v[128:131],v[24:27],v[152:155],v[48:51]\n"
"v_add_f32 v57,v57,v95\n"
CV "v93,v94,v95\n"
MF "v[128:131],v[28:31],v[172:175],v[128:131]\n"
"s_waitcnt lgkmcnt(0)\n"
"ds_read_b128 v[24:27],v0 offset:32768\n"
"ds_read_b128 v[28:31],v1 offset:32768\n"
MF "v[128:131],v[16:19],v[192:195],v[128:131]\n"
"v_exp_f32 v96,v96\n"
MF "v[128:131],v[20:23],v[212:215],v[128:131]\n"
"v_exp_f32 v97,v97\n"
MF "v[124:127],v[20:23],v[208:211],v[124:127]\n"
"v_exp_f32 v98,v98\n"
MF "v[124:127],v[16:19],v[188:191],v[124:127]\n"
"v_exp_f32 v99,v99\n"
MF "v[120:123],v[16:19],v[184:187],v[120:123]\n"
"v_add_f32 v58,v53,v96\n"
"v_add_f32 v58,v58,v97\n"
MF "v[120:123],v[20:23],v[204:207],v[120:123]\n"
CV "v96,v96,v97\n"
"v_add_f32 v58,v58,v98\n"
MF "v[116:119],v[20:23],v[200:203],v[116:119]\n"
"s_waitcnt vmcnt(12) lgkmcnt(0)\n"
"v_add_f32 v58,v58,v99\n"
CV "v97,v98,v99\n"
MF "v[116:119],v[16:19],v[180:183],v[116:119]\n"
"s_barrier\n"
MF "v[112:115],v[16:19],v[176:179],v[112:115]\n"
"v_exp_f32 v100,v100\n"
MF "v[112:115],v[20:23],v[196:199],v[112:115]\n"
"s_nop 0\n"
TR "v[16:17],v2\n"
TR "v[18:19],v2 offset:6144\n"
MF "v[112:115],v[24:27],v[216:219],v[112:115]\n"
"s_add_u32 s72,s72,6144\n"
TR "v[20:21],v3\n"
TR "v[22:23],v3 offset:6144\n"
MF "v[112:115],v[28:31],v[236:239],v[112:115]\n"
"s_cmp_eq_u32 s72,s17\n"
"s_cselect_b32 s72,s16,s72\n"
"s_add_u32 m0,s64,24576\n"
MF "v[116:119],v[28:31],v[240:243],v[116:119]\n"
LD "v7,s[32:35],s72 offen nt lds\n"
MF "v[116:119],v[24:27],v[220:223],v[116:119]\n"
"s_add_u32 m0,s64,25472\n"
"v_exp_f32 v101,v101\n"
MF "v[120:123],v[24:27],v[224:227],v[120:123]\n"
LD "v7,s[32:35],s72 offen offset:128 nt lds\n"
MF "v[120:123],v[28:31],v[244:247],v[120:123]\n"
"v_exp_f32 v102,v102\n"
MF "v[124:127],v[28:31],v[248:251],v[124:127]\n"
"s_add_u32 m0,s64,26368\n"
"v_exp_f32 v103,v103\n"
MF "v[124:127],v[24:27],v[228:231],v[124:127]\n"
LD "v7,s[32:35],s72 offen offset:256 nt lds\n"
MF "v[128:131],v[24:27],v[232:235],v[128:131]\n"
"s_add_u32 s72,s72,6144\n"
"v_add_f32 v59,v54,v100\n"
"v_add_f32 v59,v59,v101\n"
MF "v[128:131],v[28:31],v[252:255],v[128:131]\n"
"s_waitcnt lgkmcnt(0)\n"
TR "v[24:25],v4\n"
TR "v[26:27],v4 offset:6144\n"
MF "a[0:3],v[16:19],v[72:75],a[0:3]\n"
"s_add_u32 m0,s64,30720\n"
TR "v[28:29],v5\n"
TR "v[30:31],v5 offset:6144\n"
MF "a[20:23],v[20:23],v[72:75],a[20:23]\n"
LD "v7,s[32:35],s72 offen nt lds\n"
MF "a[24:27],v[20:23],v[76:79],a[24:27]\n"
CV "v100,v100,v101\n"
"v_add_f32 v59,v59,v102\n"
MF "a[4:7],v[16:19],v[76:79],a[4:7]\n"
"s_add_u32 m0,s64,31616\n"
"v_add_f32 v59,v59,v103\n"
CV "v101,v102,v103\n"
MF "a[8:11],v[16:19],v[80:83],a[8:11]\n"
LD "v7,s[32:35],s72 offen offset:128 nt lds\n"
MF "a[28:31],v[20:23],v[80:83],a[28:31]\n"
"v_exp_f32 v104,v104\n"
MF "a[32:35],v[20:23],v[84:87],a[32:35]\n"
"s_add_u32 m0,s64,32512\n"
"v_exp_f32 v105,v105\n"
MF "a[12:15],v[16:19],v[84:87],a[12:15]\n"
LD "v7,s[32:35],s72 offen offset:256 nt lds\n"
MF "a[16:19],v[16:19],v[88:91],a[16:19]\n"
"v_exp_f32 v106,v106\n"
MF "a[36:39],v[20:23],v[88:91],a[36:39]\n"
"s_waitcnt lgkmcnt(0)\n"
TR "v[16:17],v2 offset:1024\n"
TR "v[18:19],v2 offset:7168\n"
MF "a[56:59],v[24:27],v[88:91],a[56:59]\n"
"s_nop 0\n"
TR "v[20:21],v3 offset:1024\n"
TR "v[22:23],v3 offset:7168\n"
MF "a[76:79],v[28:31],v[88:91],a[76:79]\n"
"v_exp_f32 v107,v107\n"
MF "a[72:75],v[28:31],v[84:87],a[72:75]\n"
"v_add_f32 v60,v55,v104\n"
"v_add_f32 v60,v60,v105\n"
MF "a[52:55],v[24:27],v[84:87],a[52:55]\n"
CV "v104,v104,v105\n"
"v_add_f32 v60,v60,v106\n"
MF "a[48:51],v[24:27],v[80:83],a[48:51]\n"
"v_add_f32 v60,v60,v107\n"
CV "v105,v106,v107\n"
MF "a[68:71],v[28:31],v[80:83],a[68:71]\n"
"v_exp_f32 v108,v108\n"
MF "a[64:67],v[28:31],v[76:79],a[64:67]\n"
"v_exp_f32 v109,v109\n"
MF "a[44:47],v[24:27],v[76:79],a[44:47]\n"
"v_exp_f32 v110,v110\n"
MF "a[40:43],v[24:27],v[72:75],a[40:43]\n"
"v_exp_f32 v111,v111\n"
MF "a[60:63],v[28:31],v[72:75],a[60:63]\n"
"s_waitcnt lgkmcnt(0)\n"
TR "v[24:25],v4 offset:1024\n"
TR "v[26:27],v4 offset:7168\n"
MF "a[80:83],v[16:19],v[72:75],a[80:83]\n"
"s_nop 0\n"
TR "v[28:29],v5 offset:1024\n"
TR "v[30:31],v5 offset:7168\n"
MF "a[100:103],v[20:23],v[72:75],a[100:103]\n"
"v_add_f32 v61,v56,v108\n"
"v_add_f32 v61,v61,v109\n"
MF "a[104:107],v[20:23],v[76:79],a[104:107]\n"
CV "v108,v108,v109\n"
"v_add_f32 v61,v61,v110\n"
MF "a[84:87],v[16:19],v[76:79],a[84:87]\n"
"v_add_f32 v61,v61,v111\n"
CV "v109,v110,v111\n"
MF "a[88:91],v[16:19],v[80:83],a[88:91]\n"
"v_exp_f32 v112,v112\n"
MF "a[108:111],v[20:23],v[80:83],a[108:111]\n"
"v_exp_f32 v113,v113\n"
MF "a[112:115],v[20:23],v[84:87],a[112:115]\n"
"v_exp_f32 v114,v114\n"
MF "a[92:95],v[16:19],v[84:87],a[92:95]\n"
"v_exp_f32 v115,v115\n"
MF "a[96:99],v[16:19],v[88:91],a[96:99]\n"
"v_add_f32 v57,v57,v112\n"
"v_add_f32 v57,v57,v113\n"
MF "a[116:119],v[20:23],v[88:91],a[116:119]\n"
"s_waitcnt lgkmcnt(0)\n"
TR "v[16:17],v2 offset:2048\n"
TR "v[18:19],v2 offset:8192\n"
MF "a[136:139],v[24:27],v[88:91],a[136:139]\n"
"s_nop 0\n"
TR "v[20:21],v3 offset:2048\n"
TR "v[22:23],v3 offset:8192\n"
MF "a[156:159],v[28:31],v[88:91],a[156:159]\n"
CV "v94,v112,v113\n"
"v_add_f32 v57,v57,v114\n"
MF "a[152:155],v[28:31],v[84:87],a[152:155]\n"
"v_add_f32 v57,v57,v115\n"
CV "v95,v114,v115\n"
MF "a[132:135],v[24:27],v[84:87],a[132:135]\n"
"v_exp_f32 v116,v116\n"
MF "a[128:131],v[24:27],v[80:83],a[128:131]\n"
"v_exp_f32 v117,v117\n"
MF "a[148:151],v[28:31],v[80:83],a[148:151]\n"
"v_exp_f32 v118,v118\n"
MF "a[144:147],v[28:31],v[76:79],a[144:147]\n"
"v_exp_f32 v119,v119\n"
MF "a[124:127],v[24:27],v[76:79],a[124:127]\n"
"v_add_f32 v58,v58,v116\n"
"v_add_f32 v58,v58,v117\n"
MF "a[120:123],v[24:27],v[72:75],a[120:123]\n"
CV "v98,v116,v117\n"
"v_add_f32 v58,v58,v118\n"
MF "a[140:143],v[28:31],v[72:75],a[140:143]\n"
"s_waitcnt lgkmcnt(0)\n"
TR "v[24:25],v4 offset:2048\n"
TR "v[26:27],v4 offset:8192\n"
MF "a[160:163],v[16:19],v[72:75],a[160:163]\n"
"s_nop 0\n"
TR "v[28:29],v5 offset:2048\n"
TR "v[30:31],v5 offset:8192\n"
MF "a[180:183],v[20:23],v[72:75],a[180:183]\n"
"v_add_f32 v58,v58,v119\n"
CV "v99,v118,v119\n"
MF "a[184:187],v[20:23],v[76:79],a[184:187]\n"
"v_exp_f32 v120,v120\n"
MF "a[164:167],v[16:19],v[76:79],a[164:167]\n"
"v_exp_f32 v121,v121\n"
MF "a[168:171],v[16:19],v[80:83],a[168:171]\n"
"v_exp_f32 v122,v122\n"
MF "a[188:191],v[20:23],v[80:83],a[188:191]\n"
"v_exp_f32 v123,v123\n"
MF "a[192:195],v[20:23],v[84:87],a[192:195]\n"
"v_add_f32 v59,v59,v120\n"
"v_add_f32 v59,v59,v121\n"
MF "a[172:175],v[16:19],v[84:87],a[172:175]\n"
"s_waitcnt lgkmcnt(0)\n"
CV "v102,v120,v121\n"
"v_add_f32 v59,v59,v122\n"
MF "a[176:179],v[16:19],v[88:91],a[176:179]\n"
"s_barrier\n"
MF "a[196:199],v[20:23],v[88:91],a[196:199]\n"
"s_sub_u32 s58,s71,s70\n"
"s_add_u32 s73,s73,s58\n"
"v_add_f32 v59,v59,v123\n"
MF "a[216:219],v[24:27],v[88:91],a[216:219]\n"
"s_add_u32 s18,s18,s58\n"
"s_add_u32 s19,s19,s58\n"
"s_add_u32 s73,s73,12288\n"
MF "a[236:239],v[28:31],v[88:91],a[236:239]\n"
"s_cmp_eq_u32 s73,s19\n"
"s_cselect_b32 s73,s18,s73\n"
"s_add_u32 m0,s65,49152\n"
MF "a[232:235],v[28:31],v[84:87],a[232:235]\n"
LD "v8,s[36:39],s73 offen nt lds\n"
MF "a[212:215],v[24:27],v[84:87],a[212:215]\n"
"s_add_u32 m0,s65,50048\n"
CV "v103,v122,v123\n"
MF "a[208:211],v[24:27],v[80:83],a[208:211]\n"
LD "v8,s[36:39],s73 offen offset:128 nt lds\n"
MF "a[228:231],v[28:31],v[80:83],a[228:231]\n"
"s_add_u32 m0,s65,50944\n"
"v_exp_f32 v124,v124\n"
MF "a[224:227],v[28:31],v[76:79],a[224:227]\n"
LD "v8,s[36:39],s73 offen offset:256 nt lds\n"
MF "a[204:207],v[24:27],v[76:79],a[204:207]\n"
"v_exp_f32 v125,v125\n"
MF "a[200:203],v[24:27],v[72:75],a[200:203]\n"
"v_exp_f32 v126,v126\n"
MF "a[220:223],v[28:31],v[72:75],a[220:223]\n"
"v_exp_f32 v127,v127\n"
"v_add_f32 v60,v60,v124\n"
"v_add_f32 v60,v60,v125\n"
CV "v106,v124,v125\n"
"v_add_f32 v60,v60,v126\n"
"v_add_f32 v60,v60,v127\n"
CV "v107,v126,v127\n"
"v_exp_f32 v128,v128\n"
"v_exp_f32 v129,v129\n"
"v_exp_f32 v130,v130\n"
"v_exp_f32 v131,v131\n"
"v_add_f32 v61,v61,v128\n"
"v_add_f32 v61,v61,v129\n"
CV "v110,v128,v129\n"
"v_add_f32 v61,v61,v130\n"
"v_add_f32 v61,v61,v131\n"
CV "v111,v130,v131\n"
"v_max3_f32 v10,v57,v58,v59\n"
"v_max3_f32 v10,v10,v60,v61\n"
"v_cmp_le_f32 vcc,0x47800000,v10\n"
"s_cbranch_vccnz .Lstub_13\n"
".Lback_13:\n"
"s_waitcnt vmcnt(9) lgkmcnt(0)\n"
"s_barrier\n"
"s_lshl_b32 s58,s56,10\n"
"s_add_u32 s58,s58,73728\n"
"v_add_u32_e64 v120,s58,v9\n"
"v_add_u32 v121,65536,v120\n"
"s_sub_u32 s59,s71,s70\n"
"v_mov_b32 v10,1.0\n"
TR "v[136:137],v2 offset:24576\n"
TR "v[138:139],v2 offset:30720\n"
"v_mfma_f32_16x16x4_f32 v[16:19],v10,v57,0\n"
TR "v[140:141],v3 offset:24576\n"
TR "v[142:143],v3 offset:30720\n"
TR "v[144:145],v4 offset:24576\n"
TR "v[146:147],v4 offset:30720\n"
TR "v[148:149],v5 offset:24576\n"
TR "v[150:151],v5 offset:30720\n"
"v_mfma_f32_16x16x4_f32 v[20:23],v10,v58,0\n"
TR "v[152:153],v2 offset:25600\n"
TR "v[154:155],v2 offset:31744\n"
TR "v[156:157],v3 offset:25600\n"
TR "v[158:159],v3 offset:31744\n"
TR "v[160:161],v4 offset:25600\n"
TR "v[162:163],v4 offset:31744\n"
"v_mfma_f32_16x16x4_f32 v[24:27],v10,v59,0\n"
TR "v[164:165],v5 offset:25600\n"
TR "v[166:167],v5 offset:31744\n"
TR "v[168:169],v2 offset:26624\n"
TR "v[170:171],v2 offset:32768\n"
TR "v[172:173],v3 offset:26624\n"
TR "v[174:175],v3 offset:32768\n"
"v_mfma_f32_16x16x4_f32 v[28:31],v10,v60,0\n"
TR "v[176:177],v4 offset:26624\n"
TR "v[178:179],v4 offset:32768\n"
TR "v[180:181],v5 offset:26624\n"
TR "v[182:183],v5 offset:32768\n"
"v_mfma_f32_16x16x4_f32 v[32:35],v10,v61,0\n"
"s_add_u32 m0,s65,49152\n"
"s_nop 0\n"
LD "v8,s[36:39],s73 offen offset:3072 nt lds\n"
"s_add_u32 m0,s65,50048\n"
"s_nop 0\n"
LD "v8,s[36:39],s73 offen offset:3200 nt lds\n"
"s_waitcnt lgkmcnt(15)\n"
MF "a[0:3],v[136:139],v[92:95],a[0:3]\n"
MF "a[20:23],v[140:143],v[92:95],a[20:23]\n"
"s_add_u32 m0,s65,50944\n"
"s_nop 0\n"
LD "v8,s[36:39],s73 offen offset:3328 nt lds\n"
"s_sub_u32 s74,s96,s16\n"
"s_add_u32 s74,s74,s71\n"
"s_add_u32 s20,s20,s59\n"
"s_mov_b32 s70,s71\n"
LD "v[232:235],v9,s[40:43],s75 offen sc1\n"
"v_rcp_f32 v57,v16\n"
"v_rcp_f32 v58,v20\n"
"v_rcp_f32 v59,v24\n"
"v_rcp_f32 v60,v28\n"
"v_rcp_f32_e64 v61,v32\n"
MF "a[4:7],v[136:139],v[96:99],a[4:7]\n"
"v_mul_f32_e64 v52,-s13,v57\n"
"v_mul_f32_e64 v53,-s13,v58\n"
"v_mul_f32_e64 v54,-s13,v59\n"
"v_mul_f32_e64 v55,-s13,v60\n"
"v_mul_f32_e64 v56,-s13,v61\n"
AR "v72,a0\n"
AR "v73,a1\n"
AR "v74,a2\n"
MF "a[24:27],v[140:143],v[96:99],a[24:27]\n"
AR "v75,a3\n"
AR "v76,a20\n"
AR "v77,a21\n"
AR "v78,a22\n"
AR "v79,a23\n"
"v_pk_fma_f32 v[72:73],v[72:73],v[56:57],v[52:53] op_sel:[0,1,0] op_sel_hi:[1,1,0]\n"
"v_pk_fma_f32 v[74:75],v[74:75],v[56:57],v[52:53] op_sel:[0,1,0] op_sel_hi:[1,1,0]\n"
"v_pk_fma_f32 v[76:77],v[76:77],v[56:57],v[52:53] op_sel:[0,1,0] op_sel_hi:[1,1,0]\n"
"v_pk_fma_f32 v[78:79],v[78:79],v[56:57],v[52:53] op_sel:[0,1,0] op_sel_hi:[1,1,0]\n"
CV "v112,v72,v73\n"
CV "v113,v74,v75\n"
CV "v114,v76,v77\n"
CV "v115,v78,v79\n"
MF "a[8:11],v[136:139],v[100:103],a[8:11]\n"
"ds_read_b128 v[184:187],v120 offset:24576\n"
"ds_write_b128 v120,v[112:115] offset:24576\n"
LD "v[236:239],v9,s[40:43],s75 offen offset:1024 sc1\n"
AR "v80,a4\n"
AR "v81,a5\n"
AR "v82,a6\n"
MF "a[28:31],v[140:143],v[100:103],a[28:31]\n"
AR "v83,a7\n"
AR "v84,a24\n"
AR "v85,a25\n"
AR "v86,a26\n"
AR "v87,a27\n"
"v_pk_fma_f32 v[80:81],v[80:81],v[58:59],v[52:53] op_sel:[0,0,1] op_sel_hi:[1,0,1]\n"
"v_pk_fma_f32 v[82:83],v[82:83],v[58:59],v[52:53] op_sel:[0,0,1] op_sel_hi:[1,0,1]\n"
"v_pk_fma_f32 v[84:85],v[84:85],v[58:59],v[52:53] op_sel:[0,0,1] op_sel_hi:[1,0,1]\n"
"v_pk_fma_f32 v[86:87],v[86:87],v[58:59],v[52:53] op_sel:[0,0,1] op_sel_hi:[1,0,1]\n"
CV "v116,v80,v81\n"
CV "v117,v82,v83\n"
CV "v118,v84,v85\n"
CV "v119,v86,v87\n"
MF "a[12:15],v[136:139],v[104:107],a[12:15]\n"
"ds_read_b128 v[188:191],v120 offset:28672\n"
"ds_write_b128 v120,v[116:119] offset:28672\n"
AR "v72,a8\n"
AR "v73,a9\n"
AR "v74,a10\n"
MF "a[32:35],v[140:143],v[104:107],a[32:35]\n"
AR "v75,a11\n"
AR "v76,a28\n"
AR "v77,a29\n"
AR "v78,a30\n"
AR "v79,a31\n"
"v_pk_fma_f32 v[72:73],v[72:73],v[58:59],v[54:55] op_sel:[0,1,0] op_sel_hi:[1,1,0]\n"
"v_pk_fma_f32 v[74:75],v[74:75],v[58:59],v[54:55] op_sel:[0,1,0] op_sel_hi:[1,1,0]\n"
"v_pk_fma_f32 v[76:77],v[76:77],v[58:59],v[54:55] op_sel:[0,1,0] op_sel_hi:[1,1,0]\n"
"v_pk_fma_f32 v[78:79],v[78:79],v[58:59],v[54:55] op_sel:[0,1,0] op_sel_hi:[1,1,0]\n"
CV "v112,v72,v73\n"
CV "v113,v74,v75\n"
CV "v114,v76,v77\n"
CV "v115,v78,v79\n"
MF "a[16:19],v[136:139],v[108:111],a[16:19]\n"
"ds_read_b128 v[192:195],v120 offset:32768\n"
"ds_write_b128 v120,v[112:115] offset:32768\n"
"s_waitcnt lgkmcnt(0)\n"
"s_barrier\n"
LD "v[240:243],v9,s[40:43],s75 offen offset:2048 sc1\n"
AR "v80,a12\n"
AR "v81,a13\n"
AR "v82,a14\n"
MF "a[36:39],v[140:143],v[108:111],a[36:39]\n"
AR "v83,a15\n"
AR "v84,a32\n"
AR "v85,a33\n"
AR "v86,a34\n"
AR "v87,a35\n"
"v_pk_fma_f32 v[80:81],v[80:81],v[60:61],v[54:55] op_sel:[0,0,1] op_sel_hi:[1,0,1]\n"
"v_pk_fma_f32 v[82:83],v[82:83],v[60:61],v[54:55] op_sel:[0,0,1] op_sel_hi:[1,0,1]\n"
"v_pk_fma_f32 v[84:85],v[84:85],v[60:61],v[54:55] op_sel:[0,0,1] op_sel_hi:[1,0,1]\n"
"v_pk_fma_f32 v[86:87],v[86:87],v[60:61],v[54:55] op_sel:[0,0,1] op_sel_hi:[1,0,1]\n"
CV "v116,v80,v81\n"
CV "v117,v82,v83\n"
CV "v118,v84,v85\n"
CV "v119,v86,v87\n"
MF "a[40:43],v[144:147],v[92:95],a[40:43]\n"
"ds_read_b128 v[196:199],v120 offset:36864\n"
"ds_write_b128 v120,v[116:119] offset:36864\n"
LD "v[244:247],v9,s[40:43],s75 offen offset:3072 sc1\n"
AR "v72,a16\n"
AR "v73,a17\n"
AR "v74,a18\n"
MF "a[60:63],v[148:151],v[92:95],a[60:63]\n"
AR "v75,a19\n"
AR "v76,a36\n"
AR "v77,a37\n"
AR "v78,a38\n"
AR "v79,a39\n"
"v_pk_fma_f32 v[72:73],v[72:73],v[60:61],v[56:57] op_sel:[0,1,0] op_sel_hi:[1,1,0]\n"
"v_pk_fma_f32 v[74:75],v[74:75],v[60:61],v[56:57] op_sel:[0,1,0] op_sel_hi:[1,1,0]\n"
"v_pk_fma_f32 v[76:77],v[76:77],v[60:61],v[56:57] op_sel:[0,1,0] op_sel_hi:[1,1,0]\n"
"v_pk_fma_f32 v[78:79],v[78:79],v[60:61],v[56:57] op_sel:[0,1,0] op_sel_hi:[1,1,0]\n"
CV "v112,v72,v73\n"
CV "v113,v74,v75\n"
CV "v114,v76,v77\n"
CV "v115,v78,v79\n"
MF "a[44:47],v[144:147],v[96:99],a[44:47]\n"
"ds_read_b128 v[200:203],v120 offset:40960\n"
"ds_write_b128 v120,v[112:115] offset:40960\n"
AR "v80,a40\n"
AR "v81,a41\n"
AR "v82,a42\n"
MF "a[64:67],v[148:151],v[96:99],a[64:67]\n"
AR "v83,a43\n"
AR "v84,a60\n"
AR "v85,a61\n"
AR "v86,a62\n"
AR "v87,a63\n"
"v_pk_fma_f32 v[80:81],v[80:81],v[56:57],v[52:53] op_sel:[0,1,0] op_sel_hi:[1,1,0]\n"
"v_pk_fma_f32 v[82:83],v[82:83],v[56:57],v[52:53] op_sel:[0,1,0] op_sel_hi:[1,1,0]\n"
"v_pk_fma_f32 v[84:85],v[84:85],v[56:57],v[52:53] op_sel:[0,1,0] op_sel_hi:[1,1,0]\n"
"v_pk_fma_f32 v[86:87],v[86:87],v[56:57],v[52:53] op_sel:[0,1,0] op_sel_hi:[1,1,0]\n"
CV "v116,v80,v81\n"
CV "v117,v82,v83\n"
CV "v118,v84,v85\n"
CV "v119,v86,v87\n"
MF "a[48:51],v[144:147],v[100:103],a[48:51]\n"
"ds_read_b128 v[204:207],v120 offset:45056\n"
"ds_write_b128 v120,v[116:119] offset:45056\n"
"s_add_u32 s58,s75,4096\n"
LD "v[248:251],v9,s[40:43],s58 offen sc1\n"
AR "v72,a44\n"
AR "v73,a45\n"
AR "v74,a46\n"
MF "a[68:71],v[148:151],v[100:103],a[68:71]\n"
AR "v75,a47\n"
AR "v76,a64\n"
AR "v77,a65\n"
AR "v78,a66\n"
AR "v79,a67\n"
"v_pk_fma_f32 v[72:73],v[72:73],v[58:59],v[52:53] op_sel:[0,0,1] op_sel_hi:[1,0,1]\n"
"v_pk_fma_f32 v[74:75],v[74:75],v[58:59],v[52:53] op_sel:[0,0,1] op_sel_hi:[1,0,1]\n"
"v_pk_fma_f32 v[76:77],v[76:77],v[58:59],v[52:53] op_sel:[0,0,1] op_sel_hi:[1,0,1]\n"
"v_pk_fma_f32 v[78:79],v[78:79],v[58:59],v[52:53] op_sel:[0,0,1] op_sel_hi:[1,0,1]\n"
CV "v112,v72,v73\n"
CV "v113,v74,v75\n"
CV "v114,v76,v77\n"
CV "v115,v78,v79\n"
MF "a[52:55],v[144:147],v[104:107],a[52:55]\n"
"ds_read_b128 v[208:211],v120 offset:49152\n"
"ds_write_b128 v120,v[112:115] offset:49152\n"
LD "v[252:255],v9,s[40:43],s58 offen offset:1024 sc1\n"
AR "v80,a48\n"
AR "v81,a49\n"
AR "v82,a50\n"
MF "a[72:75],v[148:151],v[104:107],a[72:75]\n"
AR "v83,a51\n"
AR "v84,a68\n"
AR "v85,a69\n"
AR "v86,a70\n"
AR "v87,a71\n"
"v_pk_fma_f32 v[80:81],v[80:81],v[58:59],v[54:55] op_sel:[0,1,0] op_sel_hi:[1,1,0]\n"
"v_pk_fma_f32 v[82:83],v[82:83],v[58:59],v[54:55] op_sel:[0,1,0] op_sel_hi:[1,1,0]\n"
"v_pk_fma_f32 v[84:85],v[84:85],v[58:59],v[54:55] op_sel:[0,1,0] op_sel_hi:[1,1,0]\n"
"v_pk_fma_f32 v[86:87],v[86:87],v[58:59],v[54:55] op_sel:[0,1,0] op_sel_hi:[1,1,0]\n"
CV "v116,v80,v81\n"
CV "v117,v82,v83\n"
CV "v118,v84,v85\n"
CV "v119,v86,v87\n"
MF "a[56:59],v[144:147],v[108:111],a[56:59]\n"
"ds_read_b128 v[212:215],v120 offset:53248\n"
"ds_write_b128 v120,v[116:119] offset:53248\n"
AR "v72,a52\n"
AR "v73,a53\n"
AR "v74,a54\n"
MF "a[76:79],v[148:151],v[108:111],a[76:79]\n"
AR "v75,a55\n"
AR "v76,a72\n"
AR "v77,a73\n"
AR "v78,a74\n"
AR "v79,a75\n"
"v_pk_fma_f32 v[72:73],v[72:73],v[60:61],v[54:55] op_sel:[0,0,1] op_sel_hi:[1,0,1]\n"
"v_pk_fma_f32 v[74:75],v[74:75],v[60:61],v[54:55] op_sel:[0,0,1] op_sel_hi:[1,0,1]\n"
"v_pk_fma_f32 v[76:77],v[76:77],v[60:61],v[54:55] op_sel:[0,0,1] op_sel_hi:[1,0,1]\n"
"v_pk_fma_f32 v[78:79],v[78:79],v[60:61],v[54:55] op_sel:[0,0,1] op_sel_hi:[1,0,1]\n"
CV "v112,v72,v73\n"
CV "v113,v74,v75\n"
CV "v114,v76,v77\n"
CV "v115,v78,v79\n"
MF "a[80:83],v[152:155],v[92:95],a[80:83]\n"
"ds_read_b128 v[216:219],v120 offset:57344\n"
"ds_write_b128 v120,v[112:115] offset:57344\n"
AR "v80,a56\n"
AR "v81,a57\n"
AR "v82,a58\n"
MF "a[100:103],v[156:159],v[92:95],a[100:103]\n"
AR "v83,a59\n"
AR "v84,a76\n"
AR "v85,a77\n"
AR "v86,a78\n"
AR "v87,a79\n"
"v_pk_fma_f32 v[80:81],v[80:81],v[60:61],v[56:57] op_sel:[0,1,0] op_sel_hi:[1,1,0]\n"
"v_pk_fma_f32 v[82:83],v[82:83],v[60:61],v[56:57] op_sel:[0,1,0] op_sel_hi:[1,1,0]\n"
"v_pk_fma_f32 v[84:85],v[84:85],v[60:61],v[56:57] op_sel:[0,1,0] op_sel_hi:[1,1,0]\n"
"v_pk_fma_f32 v[86:87],v[86:87],v[60:61],v[56:57] op_sel:[0,1,0] op_sel_hi:[1,1,0]\n"
CV "v116,v80,v81\n"
CV "v117,v82,v83\n"
CV "v118,v84,v85\n"
CV "v119,v86,v87\n"
MF "a[84:87],v[152:155],v[96:99],a[84:87]\n"
"ds_read_b128 v[220:223],v120 offset:61440\n"
"ds_write_b128 v120,v[116:119] offset:61440\n"
AR "v72,a80\n"
AR "v73,a81\n"
AR "v74,a82\n"
MF "a[104:107],v[156:159],v[96:99],a[104:107]\n"
AR "v75,a83\n"
AR "v76,a100\n"
AR "v77,a101\n"
AR "v78,a102\n"
AR "v79,a103\n"
"v_pk_fma_f32 v[72:73],v[72:73],v[56:57],v[52:53] op_sel:[0,1,0] op_sel_hi:[1,1,0]\n"
"v_pk_fma_f32 v[74:75],v[74:75],v[56:57],v[52:53] op_sel:[0,1,0] op_sel_hi:[1,1,0]\n"
"v_pk_fma_f32 v[76:77],v[76:77],v[56:57],v[52:53] op_sel:[0,1,0] op_sel_hi:[1,1,0]\n"
"v_pk_fma_f32 v[78:79],v[78:79],v[56:57],v[52:53] op_sel:[0,1,0] op_sel_hi:[1,1,0]\n"
CV "v112,v72,v73\n"
CV "v113,v74,v75\n"
CV "v114,v76,v77\n"
CV "v115,v78,v79\n"
MF "a[88:91],v[152:155],v[100:103],a[88:91]\n"
"ds_read_b128 v[224:227],v121\n"
"ds_write_b128 v121,v[112:115]\n"
AR "v80,a84\n"
AR "v81,a85\n"
AR "v82,a86\n"
MF "a[108:111],v[156:159],v[100:103],a[108:111]\n"
AR "v83,a87\n"
AR "v84,a104\n"
AR "v85,a105\n"
AR "v86,a106\n"
AR "v87,a107\n"
"v_pk_fma_f32 v[80:81],v[80:81],v[58:59],v[52:53] op_sel:[0,0,1] op_sel_hi:[1,0,1]\n"
"v_pk_fma_f32 v[82:83],v[82:83],v[58:59],v[52:53] op_sel:[0,0,1] op_sel_hi:[1,0,1]\n"
"v_pk_fma_f32 v[84:85],v[84:85],v[58:59],v[52:53] op_sel:[0,0,1] op_sel_hi:[1,0,1]\n"
".Lpg17g:\n"
"s_cbranch_execz .Lisr6\n"
"s_nop 0\n"
"v_pk_fma_f32 v[86:87],v[86:87],v[58:59],v[52:53] op_sel:[0,0,1] op_sel_hi:[1,0,1]\n"
CV "v116,v80,v81\n"
CV "v117,v82,v83\n"
CV "v118,v84,v85\n"
CV "v119,v86,v87\n"
MF "a[92:95],v[152:155],v[104:107],a[92:95]\n"
"ds_read_b128 v[228:231],v121 offset:4096\n"
"ds_write_b128 v121,v[116:119] offset:4096\n"
AR "v72,a88\n"
AR "v73,a89\n"
AR "v74,a90\n"
MF "a[112:115],v[156:159],v[104:107],a[112:115]\n"
AR "v75,a91\n"
AR "v76,a108\n"
AR "v77,a109\n"
AR "v78,a110\n"
AR "v79,a111\n"
"v_pk_fma_f32 v[72:73],v[72:73],v[58:59],v[54:55] op_sel:[0,1,0] op_sel_hi:[1,1,0]\n"
"v_pk_fma_f32 v[74:75],v[74:75],v[58:59],v[54:55] op_sel:[0,1,0] op_sel_hi:[1,1,0]\n"
"v_pk_fma_f32 v[76:77],v[76:77],v[58:59],v[54:55] op_sel:[0,1,0] op_sel_hi:[1,1,0]\n"
"v_pk_fma_f32 v[78:79],v[78:79],v[58:59],v[54:55] op_sel:[0,1,0] op_sel_hi:[1,1,0]\n"
CV "v112,v72,v73\n"
CV "v113,v74,v75\n"
CV "v114,v76,v77\n"
CV "v115,v78,v79\n"
MF "a[96:99],v[152:155],v[108:111],a[96:99]\n"
"ds_read_b128 v[136:139],v121 offset:8192\n"
"ds_write_b128 v121,v[112:115] offset:8192\n"
AR "v80,a92\n"
AR "v81,a93\n"
AR "v82,a94\n"
MF "a[116:119],v[156:159],v[108:111],a[116:119]\n"
AR "v83,a95\n"
AR "v84,a112\n"
AR "v85,a113\n"
AR "v86,a114\n"
AR "v87,a115\n"
"v_pk_fma_f32 v[80:81],v[80:81],v[60:61],v[54:55] op_sel:[0,0,1] op_sel_hi:[1,0,1]\n"
"v_pk_fma_f32 v[82:83],v[82:83],v[60:61],v[54:55] op_sel:[0,0,1] op_sel_hi:[1,0,1]\n"
"v_pk_fma_f32 v[84:85],v[84:85],v[60:61],v[54:55] op_sel:[0,0,1] op_sel_hi:[1,0,1]\n"
"v_pk_fma_f32 v[86:87],v[86:87],v[60:61],v[54:55] op_sel:[0,0,1] op_sel_hi:[1,0,1]\n"
CV "v116,v80,v81\n"
CV "v117,v82,v83\n"
CV "v118,v84,v85\n"
CV "v119,v86,v87\n"
MF "a[120:123],v[160:163],v[92:95],a[120:123]\n"
"ds_read_b128 v[140:143],v121 offset:12288\n"
"ds_write_b128 v121,v[116:119] offset:12288\n"
AR "v72,a96\n"
AR "v73,a97\n"
AR "v74,a98\n"
MF "a[140:143],v[164:167],v[92:95],a[140:143]\n"
AR "v75,a99\n"
AR "v76,a116\n"
AR "v77,a117\n"
AR "v78,a118\n"
AR "v79,a119\n"
"v_pk_fma_f32 v[72:73],v[72:73],v[60:61],v[56:57] op_sel:[0,1,0] op_sel_hi:[1,1,0]\n"
"v_pk_fma_f32 v[74:75],v[74:75],v[60:61],v[56:57] op_sel:[0,1,0] op_sel_hi:[1,1,0]\n"
"v_pk_fma_f32 v[76:77],v[76:77],v[60:61],v[56:57] op_sel:[0,1,0] op_sel_hi:[1,1,0]\n"
"v_pk_fma_f32 v[78:79],v[78:79],v[60:61],v[56:57] op_sel:[0,1,0] op_sel_hi:[1,1,0]\n"
CV "v112,v72,v73\n"
CV "v113,v74,v75\n"
CV "v114,v76,v77\n"
CV "v115,v78,v79\n"
MF "a[124:127],v[160:163],v[96:99],a[124:127]\n"
"ds_read_b128 v[144:147],v121 offset:16384\n"
"ds_write_b128 v121,v[112:115] offset:16384\n"
AR "v80,a120\n"
AR "v81,a121\n"
AR "v82,a122\n"
MF "a[144:147],v[164:167],v[96:99],a[144:147]\n"
AR "v83,a123\n"
AR "v84,a140\n"
AR "v85,a141\n"
AR "v86,a142\n"
AR "v87,a143\n"
"v_pk_fma_f32 v[80:81],v[80:81],v[56:57],v[52:53] op_sel:[0,1,0] op_sel_hi:[1,1,0]\n"
"v_pk_fma_f32 v[82:83],v[82:83],v[56:57],v[52:53] op_sel:[0,1,0] op_sel_hi:[1,1,0]\n"
"v_pk_fma_f32 v[84:85],v[84:85],v[56:57],v[52:53] op_sel:[0,1,0] op_sel_hi:[1,1,0]\n"
"v_pk_fma_f32 v[86:87],v[86:87],v[56:57],v[52:53] op_sel:[0,1,0] op_sel_hi:[1,1,0]\n"
CV "v116,v80,v81\n"
CV "v117,v82,v83\n"
CV "v118,v84,v85\n"
CV "v119,v86,v87\n"
MF "a[128:131],v[160:163],v[100:103],a[128:131]\n"
"ds_read_b128 v[148:151],v121 offset:20480\n"
"ds_write_b128 v121,v[116:119] offset:20480\n"
AR "v72,a124\n"
AR "v73,a125\n"
AR "v74,a126\n"
MF "a[148:151],v[164:167],v[100:103],a[148:151]\n"
AR "v75,a127\n"
AR "v76,a144\n"
AR "v77,a145\n"
AR "v78,a146\n"
AR "v79,a147\n"
"v_pk_fma_f32 v[72:73],v[72:73],v[58:59],v[52:53] op_sel:[0,0,1] op_sel_hi:[1,0,1]\n"
"v_pk_fma_f32 v[74:75],v[74:75],v[58:59],v[52:53] op_sel:[0,0,1] op_sel_hi:[1,0,1]\n"
"v_pk_fma_f32 v[76:77],v[76:77],v[58:59],v[52:53] op_sel:[0,0,1] op_sel_hi:[1,0,1]\n"
"v_pk_fma_f32 v[78:79],v[78:79],v[58:59],v[52:53] op_sel:[0,0,1] op_sel_hi:[1,0,1]\n"
CV "v112,v72,v73\n"
CV "v113,v74,v75\n"
CV "v114,v76,v77\n"
CV "v115,v78,v79\n"
MF "a[132:135],v[160:163],v[104:107],a[132:135]\n"
"ds_write_b128 v120,v[112:115]\n"
AR "v80,a128\n"
AR "v81,a129\n"
AR "v82,a130\n"
MF "a[152:155],v[164:167],v[104:107],a[152:155]\n"
AR "v83,a131\n"
AR "v84,a148\n"
AR "v85,a149\n"
AR "v86,a150\n"
AR "v87,a151\n"
"v_pk_fma_f32 v[80:81],v[80:81],v[58:59],v[54:55] op_sel:[0,1,0] op_sel_hi:[1,1,0]\n"
"v_pk_fma_f32 v[82:83],v[82:83],v[58:59],v[54:55] op_sel:[0,1,0] op_sel_hi:[1,1,0]\n"
"v_pk_fma_f32 v[84:85],v[84:85],v[58:59],v[54:55] op_sel:[0,1,0] op_sel_hi:[1,1,0]\n"
"v_pk_fma_f32 v[86:87],v[86:87],v[58:59],v[54:55] op_sel:[0,1,0] op_sel_hi:[1,1,0]\n"
CV "v116,v80,v81\n"
CV "v117,v82,v83\n"
CV "v118,v84,v85\n"
CV "v119,v86,v87\n"
MF "a[136:139],v[160:163],v[108:111],a[136:139]\n"
"ds_write_b128 v120,v[116:119] offset:4096\n"
AR "v72,a132\n"
AR "v73,a133\n"
AR "v74,a134\n"
MF "a[156:159],v[164:167],v[108:111],a[156:159]\n"
AR "v75,a135\n"
AR "v76,a152\n"
AR "v77,a153\n"
AR "v78,a154\n"
AR "v79,a155\n"
"v_pk_fma_f32 v[72:73],v[72:73],v[60:61],v[54:55] op_sel:[0,0,1] op_sel_hi:[1,0,1]\n"
"v_pk_fma_f32 v[74:75],v[74:75],v[60:61],v[54:55] op_sel:[0,0,1] op_sel_hi:[1,0,1]\n"
"v_pk_fma_f32 v[76:77],v[76:77],v[60:61],v[54:55] op_sel:[0,0,1] op_sel_hi:[1,0,1]\n"
"v_pk_fma_f32 v[78:79],v[78:79],v[60:61],v[54:55] op_sel:[0,0,1] op_sel_hi:[1,0,1]\n"
CV "v112,v72,v73\n"
CV "v113,v74,v75\n"
CV "v114,v76,v77\n"
CV "v115,v78,v79\n"
MF "a[160:163],v[168:171],v[92:95],a[160:163]\n"
"ds_write_b128 v120,v[112:115] offset:8192\n"
AR "v80,a136\n"
AR "v81,a137\n"
AR "v82,a138\n"
MF "a[180:183],v[172:175],v[92:95],a[180:183]\n"
AR "v83,a139\n"
AR "v84,a156\n"
AR "v85,a157\n"
AR "v86,a158\n"
AR "v87,a159\n"
"v_pk_fma_f32 v[80:81],v[80:81],v[60:61],v[56:57] op_sel:[0,1,0] op_sel_hi:[1,1,0]\n"
"v_pk_fma_f32 v[82:83],v[82:83],v[60:61],v[56:57] op_sel:[0,1,0] op_sel_hi:[1,1,0]\n"
"v_pk_fma_f32 v[84:85],v[84:85],v[60:61],v[56:57] op_sel:[0,1,0] op_sel_hi:[1,1,0]\n"
"v_pk_fma_f32 v[86:87],v[86:87],v[60:61],v[56:57] op_sel:[0,1,0] op_sel_hi:[1,1,0]\n"
CV "v116,v80,v81\n"
CV "v117,v82,v83\n"
CV "v118,v84,v85\n"
CV "v119,v86,v87\n"
MF "a[164:167],v[168:171],v[96:99],a[164:167]\n"
"ds_write_b128 v120,v[116:119] offset:12288\n"
AR "v72,a160\n"
AR "v73,a161\n"
AR "v74,a162\n"
MF "a[184:187],v[172:175],v[96:99],a[184:187]\n"
AR "v75,a163\n"
AR "v76,a180\n"
AR "v77,a181\n"
AR "v78,a182\n"
AR "v79,a183\n"
"v_pk_fma_f32 v[72:73],v[72:73],v[56:57],v[52:53] op_sel:[0,1,0] op_sel_hi:[1,1,0]\n"
"v_pk_fma_f32 v[74:75],v[74:75],v[56:57],v[52:53] op_sel:[0,1,0] op_sel_hi:[1,1,0]\n"
"v_pk_fma_f32 v[76:77],v[76:77],v[56:57],v[52:53] op_sel:[0,1,0] op_sel_hi:[1,1,0]\n"
"v_pk_fma_f32 v[78:79],v[78:79],v[56:57],v[52:53] op_sel:[0,1,0] op_sel_hi:[1,1,0]\n"
CV "v112,v72,v73\n"
CV "v113,v74,v75\n"
CV "v114,v76,v77\n"
CV "v115,v78,v79\n"
MF "a[168:171],v[168:171],v[100:103],a[168:171]\n"
"ds_write_b128 v120,v[112:115] offset:16384\n"
AR "v80,a164\n"
AR "v81,a165\n"
AR "v82,a166\n"
MF "a[188:191],v[172:175],v[100:103],a[188:191]\n"
AR "v83,a167\n"
AR "v84,a184\n"
AR "v85,a185\n"
AR "v86,a186\n"
AR "v87,a187\n"
"v_pk_fma_f32 v[80:81],v[80:81],v[58:59],v[52:53] op_sel:[0,0,1] op_sel_hi:[1,0,1]\n"
"v_pk_fma_f32 v[82:83],v[82:83],v[58:59],v[52:53] op_sel:[0,0,1] op_sel_hi:[1,0,1]\n"
"v_pk_fma_f32 v[84:85],v[84:85],v[58:59],v[52:53] op_sel:[0,0,1] op_sel_hi:[1,0,1]\n"
"v_pk_fma_f32 v[86:87],v[86:87],v[58:59],v[52:53] op_sel:[0,0,1] op_sel_hi:[1,0,1]\n"
CV "v116,v80,v81\n"
CV "v117,v82,v83\n"
CV "v118,v84,v85\n"
CV "v119,v86,v87\n"
MF "a[172:175],v[168:171],v[104:107],a[172:175]\n"
"ds_write_b128 v120,v[116:119] offset:20480\n"
AR "v72,a168\n"
AR "v73,a169\n"
AR "v74,a170\n"
MF "a[192:195],v[172:175],v[104:107],a[192:195]\n"
AR "v75,a171\n"
AR "v76,a188\n"
AR "v77,a189\n"
AR "v78,a190\n"
AR "v79,a191\n"
"v_pk_fma_f32 v[72:73],v[72:73],v[58:59],v[54:55] op_sel:[0,1,0] op_sel_hi:[1,1,0]\n"
"v_pk_fma_f32 v[74:75],v[74:75],v[58:59],v[54:55] op_sel:[0,1,0] op_sel_hi:[1,1,0]\n"
"v_pk_fma_f32 v[76:77],v[76:77],v[58:59],v[54:55] op_sel:[0,1,0] op_sel_hi:[1,1,0]\n"
"v_pk_fma_f32 v[78:79],v[78:79],v[58:59],v[54:55] op_sel:[0,1,0] op_sel_hi:[1,1,0]\n"
CV "v112,v72,v73\n"
CV "v113,v74,v75\n"
CV "v114,v76,v77\n"
CV "v115,v78,v79\n"
MF "a[176:179],v[168:171],v[108:111],a[176:179]\n"
AR "v152,a240\n"
AW "a240,v112\n"
AR "v153,a241\n"
AW "a241,v113\n"
AR "v154,a242\n"
AW "a242,v114\n"
AR "v155,a243\n"
AW "a243,v115\n"
AR "v80,a172\n"
AR "v81,a173\n"
AR "v82,a174\n"
MF "a[196:199],v[172:175],v[108:111],a[196:199]\n"
AR "v83,a175\n"
AR "v84,a192\n"
AR "v85,a193\n"
AR "v86,a194\n"
AR "v87,a195\n"
"v_pk_fma_f32 v[80:81],v[80:81],v[60:61],v[54:55] op_sel:[0,0,1] op_sel_hi:[1,0,1]\n"
"v_pk_fma_f32 v[82:83],v[82:83],v[60:61],v[54:55] op_sel:[0,0,1] op_sel_hi:[1,0,1]\n"
"v_pk_fma_f32 v[84:85],v[84:85],v[60:61],v[54:55] op_sel:[0,0,1] op_sel_hi:[1,0,1]\n"
"v_pk_fma_f32 v[86:87],v[86:87],v[60:61],v[54:55] op_sel:[0,0,1] op_sel_hi:[1,0,1]\n"
CV "v116,v80,v81\n"
CV "v117,v82,v83\n"
CV "v118,v84,v85\n"
CV "v119,v86,v87\n"
MF "a[200:203],v[176:179],v[92:95],a[200:203]\n"
AR "v156,a244\n"
AW "a244,v116\n"
AR "v157,a245\n"
AW "a245,v117\n"
AR "v158,a246\n"
AW "a246,v118\n"
AR "v159,a247\n"
AW "a247,v119\n"
AR "v72,a176\n"
AR "v73,a177\n"
AR "v74,a178\n"
MF "a[220:223],v[180:183],v[92:95],a[220:223]\n"
AR "v75,a179\n"
AR "v76,a196\n"
AR "v77,a197\n"
AR "v78,a198\n"
AR "v79,a199\n"
"v_pk_fma_f32 v[72:73],v[72:73],v[60:61],v[56:57] op_sel:[0,1,0] op_sel_hi:[1,1,0]\n"
"v_pk_fma_f32 v[74:75],v[74:75],v[60:61],v[56:57] op_sel:[0,1,0] op_sel_hi:[1,1,0]\n"
"v_pk_fma_f32 v[76:77],v[76:77],v[60:61],v[56:57] op_sel:[0,1,0] op_sel_hi:[1,1,0]\n"
"v_pk_fma_f32 v[78:79],v[78:79],v[60:61],v[56:57] op_sel:[0,1,0] op_sel_hi:[1,1,0]\n"
CV "v112,v72,v73\n"
CV "v113,v74,v75\n"
CV "v114,v76,v77\n"
CV "v115,v78,v79\n"
MF "a[204:207],v[176:179],v[96:99],a[204:207]\n"
AR "v160,a248\n"
AW "a248,v112\n"
AR "v161,a249\n"
AW "a249,v113\n"
AR "v162,a250\n"
AW "a250,v114\n"
AR "v163,a251\n"
AW "a251,v115\n"
AR "v80,a200\n"
AR "v81,a201\n"
AR "v82,a202\n"
MF "a[224:227],v[180:183],v[96:99],a[224:227]\n"
AR "v83,a203\n"
AR "v84,a220\n"
AR "v85,a221\n"
AR "v86,a222\n"
AR "v87,a223\n"
"v_pk_fma_f32 v[80:81],v[80:81],v[56:57],v[52:53] op_sel:[0,1,0] op_sel_hi:[1,1,0]\n"
"v_pk_fma_f32 v[82:83],v[82:83],v[56:57],v[52:53] op_sel:[0,1,0] op_sel_hi:[1,1,0]\n"
"v_pk_fma_f32 v[84:85],v[84:85],v[56:57],v[52:53] op_sel:[0,1,0] op_sel_hi:[1,1,0]\n"
"v_pk_fma_f32 v[86:87],v[86:87],v[56:57],v[52:53] op_sel:[0,1,0] op_sel_hi:[1,1,0]\n"
CV "v116,v80,v81\n"
CV "v117,v82,v83\n"
CV "v118,v84,v85\n"
CV "v119,v86,v87\n"
MF "a[208:211],v[176:179],v[100:103],a[208:211]\n"
"v_mov_b32 v164,v62\n"
"v_mov_b32 v62,v116\n"
"v_mov_b32 v165,v63\n"
"v_mov_b32 v63,v117\n"
"v_mov_b32 v166,v64\n"
"v_mov_b32 v64,v118\n"
"v_mov_b32 v167,v65\n"
"v_mov_b32 v65,v119\n"
AR "v72,a204\n"
AR "v73,a205\n"
AR "v74,a206\n"
MF "a[228:231],v[180:183],v[100:103],a[228:231]\n"
AR "v75,a207\n"
AR "v76,a224\n"
AR "v77,a225\n"
AR "v78,a226\n"
AR "v79,a227\n"
"v_pk_fma_f32 v[72:73],v[72:73],v[58:59],v[52:53] op_sel:[0,0,1] op_sel_hi:[1,0,1]\n"
"v_pk_fma_f32 v[74:75],v[74:75],v[58:59],v[52:53] op_sel:[0,0,1] op_sel_hi:[1,0,1]\n"
"v_pk_fma_f32 v[76:77],v[76:77],v[58:59],v[52:53] op_sel:[0,0,1] op_sel_hi:[1,0,1]\n"
"v_pk_fma_f32 v[78:79],v[78:79],v[58:59],v[52:53] op_sel:[0,0,1] op_sel_hi:[1,0,1]\n"
CV "v112,v72,v73\n"
CV "v113,v74,v75\n"
CV "v114,v76,v77\n"
CV "v115,v78,v79\n"
MF "a[212:215],v[176:179],v[104:107],a[212:215]\n"
"v_mov_b32 v168,v66\n"
"v_mov_b32 v66,v112\n"
"v_mov_b32 v169,v67\n"
"v_mov_b32 v67,v113\n"
"v_mov_b32 v170,v68\n"
"v_mov_b32 v68,v114\n"
"v_mov_b32 v171,v69\n"
"v_mov_b32 v69,v115\n"
AR "v80,a208\n"
AR "v81,a209\n"
AR "v82,a210\n"
MF "a[232:235],v[180:183],v[104:107],a[232:235]\n"
AR "v83,a211\n"
AR "v84,a228\n"
AR "v85,a229\n"
AR "v86,a230\n"
AR "v87,a231\n"
"v_pk_fma_f32 v[80:81],v[80:81],v[58:59],v[54:55] op_sel:[0,1,0] op_sel_hi:[1,1,0]\n"
"v_pk_fma_f32 v[82:83],v[82:83],v[58:59],v[54:55] op_sel:[0,1,0] op_sel_hi:[1,1,0]\n"
"v_pk_fma_f32 v[84:85],v[84:85],v[58:59],v[54:55] op_sel:[0,1,0] op_sel_hi:[1,1,0]\n"
"v_pk_fma_f32 v[86:87],v[86:87],v[58:59],v[54:55] op_sel:[0,1,0] op_sel_hi:[1,1,0]\n"
CV "v116,v80,v81\n"
CV "v117,v82,v83\n"
CV "v118,v84,v85\n"
CV "v119,v86,v87\n"
MF "a[216:219],v[176:179],v[108:111],a[216:219]\n"
"v_mov_b32 v172,v70\n"
"v_mov_b32 v70,v116\n"
"v_mov_b32 v173,v71\n"
"v_mov_b32 v71,v117\n"
"v_mov_b32 v174,v132\n"
"v_mov_b32 v132,v118\n"
"v_mov_b32 v175,v133\n"
"v_mov_b32 v133,v119\n"
AR "v72,a212\n"
AR "v73,a213\n"
AR "v74,a214\n"
MF "a[236:239],v[180:183],v[108:111],a[236:239]\n"
AR "v75,a215\n"
AR "v76,a232\n"
AR "v77,a233\n"
AR "v78,a234\n"
AR "v79,a235\n"
"v_pk_fma_f32 v[72:73],v[72:73],v[60:61],v[54:55] op_sel:[0,0,1] op_sel_hi:[1,0,1]\n"
"v_pk_fma_f32 v[74:75],v[74:75],v[60:61],v[54:55] op_sel:[0,0,1] op_sel_hi:[1,0,1]\n"
"v_pk_fma_f32 v[76:77],v[76:77],v[60:61],v[54:55] op_sel:[0,0,1] op_sel_hi:[1,0,1]\n"
"v_pk_fma_f32 v[78:79],v[78:79],v[60:61],v[54:55] op_sel:[0,0,1] op_sel_hi:[1,0,1]\n"
CV "v112,v72,v73\n"
CV "v113,v74,v75\n"
CV "v114,v76,v77\n"
CV "v115,v78,v79\n"
"s_waitcnt vmcnt(0) lgkmcnt(0)\n"
"s_barrier\n"
"ds_read_b128 v[92:95],v0\n"
"ds_read_b128 v[96:99],v1\n"
"ds_read_b128 v[100:103],v0 offset:1024\n"
"v_mov_b32 v176,v134\n"
"v_mov_b32 v134,v112\n"
"v_mov_b32 v177,v135\n"
"v_mov_b32 v135,v113\n"
"v_mov_b32 v178,v6\n"
"v_mov_b32 v6,v114\n"
"v_mov_b32 v179,v13\n"
"v_mov_b32 v13,v115\n"
AR "v80,a216\n"
AR "v81,a217\n"
AR "v82,a218\n"
AR "v83,a219\n"
AR "v84,a236\n"
AR "v85,a237\n"
AR "v86,a238\n"
AR "v87,a239\n"
"v_pk_fma_f32 v[80:81],v[80:81],v[60:61],v[56:57] op_sel:[0,1,0] op_sel_hi:[1,1,0]\n"
"v_pk_fma_f32 v[82:83],v[82:83],v[60:61],v[56:57] op_sel:[0,1,0] op_sel_hi:[1,1,0]\n"
"v_pk_fma_f32 v[84:85],v[84:85],v[60:61],v[56:57] op_sel:[0,1,0] op_sel_hi:[1,1,0]\n"
"v_pk_fma_f32 v[86:87],v[86:87],v[60:61],v[56:57] op_sel:[0,1,0] op_sel_hi:[1,1,0]\n"
CV "v116,v80,v81\n"
CV "v117,v82,v83\n"
CV "v118,v84,v85\n"
CV "v119,v86,v87\n"
"v_mov_b32 v180,v14\n"
"v_mov_b32 v14,v116\n"
"v_mov_b32 v181,v15\n"
"v_mov_b32 v15,v117\n"
AR "v182,a254\n"
AW "a254,v118\n"
AR "v183,a255\n"
AW "a255,v119\n"
"s_mov_b32 s13,s11\n"
"v_subrev_u32 v2,49152,v2\n"
"v_subrev_u32 v3,49152,v3\n"
"v_subrev_u32 v4,49152,v4\n"
"v_subrev_u32 v5,49152,v5\n"
"s_mov_b32 s57,2\n"
"s_mov_b32 s69,1\n"
"s_mov_b32 s61,2\n"
"s_mov_b32 s97,0\n"
"s_mov_b32 s95,0x47800000\n"
"s_mov_b32 s77,0x47800000\n"
"s_branch .Latt2\n"
".LpgattB_f:\n"
"s_cbranch_execz .Lisr3\n"
".LattB:\n"
AR "v2,a245\n"
"v_xor_b32 v3,32,v2\n"
"v_xor_b32 v4,64,v2\n"
"v_xor_b32 v5,96,v2\n"
"s_mov_b32 s61,2\n"
".Latt2:\n"
"s_cmp_eq_u32 s57,1\n"
"s_cbranch_scc1 .Lt0k\n"
".Lt0n:\n"
"s_waitcnt lgkmcnt(2)\n"
MF "v[72:75],v[92:95],v[136:139],0\n"
"ds_read_b128 v[104:107],v1 offset:1024\n"
MF "v[76:79],v[92:95],v[140:143],0\n"
MF "v[80:83],v[92:95],v[144:147],0\n"
MF "v[84:87],v[92:95],v[148:151],0\n"
MF "v[88:91],v[92:95],v[152:155],0\n"
"s_waitcnt lgkmcnt(2)\n"
"ds_read_b128 v[92:95],v0 offset:2048\n"
MF "v[72:75],v[96:99],v[156:159],v[72:75]\n"
MF "v[76:79],v[96:99],v[160:163],v[76:79]\n"
MF "v[80:83],v[96:99],v[164:167],v[80:83]\n"
MF "v[84:87],v[96:99],v[168:171],v[84:87]\n"
MF "v[88:91],v[96:99],v[172:175],v[88:91]\n"
"s_waitcnt lgkmcnt(2)\n"
"ds_read_b128 v[96:99],v1 offset:2048\n"
MF "v[72:75],v[100:103],v[176:179],v[72:75]\n"
MF "v[76:79],v[100:103],v[180:183],v[76:79]\n"
MF "v[80:83],v[100:103],v[184:187],v[80:83]\n"
MF "v[84:87],v[100:103],v[188:191],v[84:87]\n"
MF "v[88:91],v[100:103],v[192:195],v[88:91]\n"
"s_waitcnt lgkmcnt(2)\n"
"ds_read_b128 v[100:103],v0 offset:6144\n"
MF "v[72:75],v[104:107],v[196:199],v[72:75]\n"
MF "v[76:79],v[104:107],v[200:203],v[76:79]\n"
MF "v[80:83],v[104:107],v[204:207],v[80:83]\n"
MF "v[84:87],v[104:107],v[208:211],v[84:87]\n"
MF "v[88:91],v[104:107],v[212:215],v[88:91]\n"
"s_waitcnt lgkmcnt(2)\n"
"ds_read_b128 v[104:107],v1 offset:6144\n"
MF "v[72:75],v[92:95],v[216:219],v[72:75]\n"
MF "v[76:79],v[92:95],v[220:223],v[76:79]\n"
MF "v[80:83],v[92:95],v[224:227],v[80:83]\n"
MF "v[84:87],v[92:95],v[228:231],v[84:87]\n"
MF "v[88:91],v[92:95],v[232:235],v[88:91]\n"
"s_waitcnt lgkmcnt(2)\n"
"ds_read_b128 v[92:95],v0 offset:7168\n"
MF "v[72:75],v[96:99],v[236:239],v[72:75]\n"
MF "v[76:79],v[96:99],v[240:243],v[76:79]\n"
MF "v[80:83],v[96:99],v[244:247],v[80:83]\n"
MF "v[84:87],v[96:99],v[248:251],v[84:87]\n"
MF "v[88:91],v[96:99],v[252:255],v[88:91]\n"
"s_waitcnt lgkmcnt(2)\n"
"ds_read_b128 v[96:99],v1 offset:7168\n"
MF "v[112:115],v[100:103],v[136:139],0\n"
MF "v[116:119],v[100:103],v[140:143],0\n"
MF "v[120:123],v[100:103],v[144:147],0\n"
MF "v[124:127],v[100:103],v[148:151],0\n"
MF "v[128:131],v[100:103],v[152:155],0\n"
"s_waitcnt lgkmcnt(2)\n"
"ds_read_b128 v[100:103],v0 offset:8192\n"
MF "v[112:115],v[104:107],v[156:159],v[112:115]\n"
"v_max3_f32 v52,v72,v73,v74\n"
"v_max_f32 v52,v52,v75\n"
MF "v[116:119],v[104:107],v[160:163],v[116:119]\n"
"v_max3_f32 v53,v76,v77,v78\n"
"v_max_f32 v53,v53,v79\n"
MF "v[120:123],v[104:107],v[164:167],v[120:123]\n"
"v_max3_f32 v54,v80,v81,v82\n"
"v_max_f32 v54,v54,v83\n"
MF "v[124:127],v[104:107],v[168:171],v[124:127]\n"
"v_max3_f32 v55,v84,v85,v86\n"
"v_max_f32 v55,v55,v87\n"
MF "v[128:131],v[104:107],v[172:175],v[128:131]\n"
"s_waitcnt lgkmcnt(2)\n"
"ds_read_b128 v[104:107],v1 offset:8192\n"
"v_max3_f32 v56,v88,v89,v90\n"
MF "v[112:115],v[92:95],v[176:179],v[112:115]\n"
"v_max_f32 v56,v56,v91\n"
MF "v[116:119],v[92:95],v[180:183],v[116:119]\n"
MF "v[120:123],v[92:95],v[184:187],v[120:123]\n"
MF "v[124:127],v[92:95],v[188:191],v[124:127]\n"
MF "v[128:131],v[92:95],v[192:195],v[128:131]\n"
"s_waitcnt lgkmcnt(2)\n"
MF "v[112:115],v[96:99],v[196:199],v[112:115]\n"
"s_waitcnt lgkmcnt(0)\n"
MF "v[116:119],v[96:99],v[200:203],v[116:119]\n"
"s_barrier\n"
MF "v[120:123],v[96:99],v[204:207],v[120:123]\n"
"s_add_u32 s72,s72,6144\n"
MF "v[124:127],v[96:99],v[208:211],v[124:127]\n"
"s_cmp_eq_u32 s72,s17\n"
"s_cselect_b32 s72,s16,s72\n"
"s_add_u32 m0,s64,0\n"
MF "v[128:131],v[96:99],v[212:215],v[128:131]\n"
LD "v7,s[32:35],s72 offen nt lds\n"
MF "v[112:115],v[100:103],v[216:219],v[112:115]\n"
"s_add_u32 m0,s64,896\n"
MF "v[116:119],v[100:103],v[220:223],v[116:119]\n"
LD "v7,s[32:35],s72 offen offset:128 nt lds\n"
MF "v[120:123],v[100:103],v[224:227],v[120:123]\n"
"s_add_u32 m0,s64,1792\n"
MF "v[124:127],v[100:103],v[228:231],v[124:127]\n"
LD "v7,s[32:35],s72 offen offset:256 nt lds\n"
MF "v[128:131],v[100:103],v[232:235],v[128:131]\n"
"s_add_u32 s72,s72,6144\n"
"s_add_u32 m0,s64,6144\n"
MF "v[112:115],v[104:107],v[236:239],v[112:115]\n"
LD "v7,s[32:35],s72 offen nt lds\n"
MF "v[116:119],v[104:107],v[240:243],v[116:119]\n"
"s_add_u32 m0,s64,7040\n"
MF "v[120:123],v[104:107],v[244:247],v[120:123]\n"
LD "v7,s[32:35],s72 offen offset:128 nt lds\n"
MF "v[124:127],v[104:107],v[248:251],v[124:127]\n"
"s_add_u32 m0,s64,7936\n"
MF "v[128:131],v[104:107],v[252:255],v[128:131]\n"
LD "v7,s[32:35],s72 offen offset:256 nt lds\n"
"s_branch .Lt0e\n"
".Lpgt0k_f:\n"
"s_cbranch_execz .Lisr3\n"
".Lt0k:\n"
"s_lshl_b32 s58,s56,10\n"
"s_add_u32 s58,s58,73728\n"
"v_add_u32 v32,s58,v9\n"
"v_add_u32 v33,65536,v32\n"
"s_waitcnt lgkmcnt(2)\n"
MF "v[72:75],v[92:95],v[136:139],0\n"
"ds_read_b128 v[104:107],v1 offset:1024\n"
AR "v62,a20\n"
MF "v[76:79],v[92:95],v[140:143],0\n"
".Lpg18f:\n"
"s_cbranch_execz .Lisr3\n"
"ds_write_b128 v32,a[0:3] offset:24576\n"
MF "v[80:83],v[92:95],v[144:147],0\n"
"ds_write_b128 v32,a[4:7] offset:28672\n"
MF "v[84:87],v[92:95],v[148:151],0\n"
AR "v63,a21\n"
AR "v64,a22\n"
MF "v[88:91],v[92:95],v[152:155],0\n"
"s_waitcnt lgkmcnt(4)\n"
"ds_read_b128 v[92:95],v0 offset:2048\n"
AR "v65,a23\n"
MF "v[72:75],v[96:99],v[156:159],v[72:75]\n"
"ds_write_b128 v32,a[8:11] offset:32768\n"
MF "v[76:79],v[96:99],v[160:163],v[76:79]\n"
AR "v66,a24\n"
AR "v67,a25\n"
MF "v[80:83],v[96:99],v[164:167],v[80:83]\n"
"ds_write_b128 v32,a[12:15] offset:36864\n"
MF "v[84:87],v[96:99],v[168:171],v[84:87]\n"
AR "v68,a26\n"
AR "v69,a27\n"
MF "v[88:91],v[96:99],v[172:175],v[88:91]\n"
"s_waitcnt lgkmcnt(6)\n"
"ds_read_b128 v[96:99],v1 offset:2048\n"
AR "v70,a28\n"
MF "v[72:75],v[100:103],v[176:179],v[72:75]\n"
"ds_write_b128 v32,a[16:19] offset:40960\n"
MF "v[76:79],v[100:103],v[180:183],v[76:79]\n"
AR "v71,a29\n"
AR "v132,a30\n"
MF "v[80:83],v[100:103],v[184:187],v[80:83]\n"
AR "v133,a31\n"
AR "v134,a32\n"
MF "v[84:87],v[100:103],v[188:191],v[84:87]\n"
"ds_write_b128 v32,a[40:43] offset:45056\n"
MF "v[88:91],v[100:103],v[192:195],v[88:91]\n"
"s_waitcnt lgkmcnt(8)\n"
"ds_read_b128 v[100:103],v0 offset:6144\n"
AR "v135,a33\n"
MF "v[72:75],v[104:107],v[196:199],v[72:75]\n"
"ds_write_b128 v32,a[44:47] offset:49152\n"
MF "v[76:79],v[104:107],v[200:203],v[76:79]\n"
AR "v6,a34\n"
AR "v13,a35\n"
MF "v[80:83],v[104:107],v[204:207],v[80:83]\n"
AR "v14,a36\n"
AR "v15,a37\n"
MF "v[84:87],v[104:107],v[208:211],v[84:87]\n"
"ds_write_b128 v32,a[48:51] offset:53248\n"
MF "v[88:91],v[104:107],v[212:215],v[88:91]\n"
"s_waitcnt lgkmcnt(8)\n"
"ds_read_b128 v[104:107],v1 offset:6144\n"
"v_accvgpr_mov_b32 a254,a38\n"
MF "v[72:75],v[92:95],v[216:219],v[72:75]\n"
"v_accvgpr_mov_b32 a255,a39\n"
"v_accvgpr_mov_b32 a240,a168\n"
MF "v[76:79],v[92:95],v[220:223],v[76:79]\n"
"ds_write_b128 v32,a[52:55] offset:57344\n"
MF "v[80:83],v[92:95],v[224:227],v[80:83]\n"
"v_accvgpr_mov_b32 a241,a169\n"
"v_accvgpr_mov_b32 a242,a170\n"
MF "v[84:87],v[92:95],v[228:231],v[84:87]\n"
"ds_write_b128 v32,a[56:59] offset:61440\n"
MF "v[88:91],v[92:95],v[232:235],v[88:91]\n"
"s_waitcnt lgkmcnt(8)\n"
"ds_read_b128 v[92:95],v0 offset:7168\n"
"v_accvgpr_mov_b32 a243,a171\n"
MF "v[72:75],v[96:99],v[236:239],v[72:75]\n"
"v_accvgpr_mov_b32 a244,a172\n"
"v_accvgpr_mov_b32 a245,a173\n"
MF "v[76:79],v[96:99],v[240:243],v[76:79]\n"
"ds_write_b128 v33,a[80:83]\n"
MF "v[80:83],v[96:99],v[244:247],v[80:83]\n"
"v_accvgpr_mov_b32 a246,a174\n"
"v_accvgpr_mov_b32 a247,a175\n"
MF "v[84:87],v[96:99],v[248:251],v[84:87]\n"
"v_accvgpr_mov_b32 a248,a176\n"
"v_accvgpr_mov_b32 a249,a177\n"
MF "v[88:91],v[96:99],v[252:255],v[88:91]\n"
"s_waitcnt lgkmcnt(7)\n"
"ds_read_b128 v[96:99],v1 offset:7168\n"
"v_accvgpr_mov_b32 a250,a178\n"
MF "v[112:115],v[100:103],v[136:139],0\n"
"ds_write_b128 v33,a[84:87] offset:4096\n"
MF "v[116:119],v[100:103],v[140:143],0\n"
"ds_write_b128 v33,a[88:91] offset:8192\n"
MF "v[120:123],v[100:103],v[144:147],0\n"
"v_accvgpr_mov_b32 a251,a179\n"
MF "v[124:127],v[100:103],v[148:151],0\n"
MF "v[128:131],v[100:103],v[152:155],0\n"
"s_waitcnt lgkmcnt(7)\n"
"ds_read_b128 v[100:103],v0 offset:8192\n"
MF "v[112:115],v[104:107],v[156:159],v[112:115]\n"
"v_max3_f32 v52,v72,v73,v74\n"
"v_max_f32 v52,v52,v75\n"
MF "v[116:119],v[104:107],v[160:163],v[116:119]\n"
"v_max3_f32 v53,v76,v77,v78\n"
"v_max_f32 v53,v53,v79\n"
MF "v[120:123],v[104:107],v[164:167],v[120:123]\n"
"v_max3_f32 v54,v80,v81,v82\n"
"v_max_f32 v54,v54,v83\n"
MF "v[124:127],v[104:107],v[168:171],v[124:127]\n"
"v_max3_f32 v55,v84,v85,v86\n"
"v_max_f32 v55,v55,v87\n"
MF "v[128:131],v[104:107],v[172:175],v[128:131]\n"
"s_waitcnt lgkmcnt(5)\n"
"ds_read_b128 v[104:107],v1 offset:8192\n"
"v_max3_f32 v56,v88,v89,v90\n"
MF "v[112:115],v[92:95],v[176:179],v[112:115]\n"
"v_max_f32 v56,v56,v91\n"
MF "v[116:119],v[92:95],v[180:183],v[116:119]\n"
"ds_write_b128 v33,a[92:95] offset:12288\n"
MF "v[120:123],v[92:95],v[184:187],v[120:123]\n"
"ds_write_b128 v33,a[96:99] offset:16384\n"
MF "v[124:127],v[92:95],v[188:191],v[124:127]\n"
"ds_write_b128 v33,a[120:123] offset:20480\n"
MF "v[128:131],v[92:95],v[192:195],v[128:131]\n"
"s_waitcnt lgkmcnt(7)\n"
"ds_write_b128 v32,a[124:127]\n"
MF "v[112:115],v[96:99],v[196:199],v[112:115]\n"
"s_waitcnt lgkmcnt(0)\n"
MF "v[116:119],v[96:99],v[200:203],v[116:119]\n"
"s_barrier\n"
MF "v[120:123],v[96:99],v[204:207],v[120:123]\n"
"s_add_u32 s72,s72,6144\n"
"ds_write_b128 v32,a[128:131] offset:4096\n"
MF "v[124:127],v[96:99],v[208:211],v[124:127]\n"
"s_cmp_eq_u32 s72,s17\n"
"s_cselect_b32 s72,s16,s72\n"
"s_add_u32 m0,s64,0\n"
MF "v[128:131],v[96:99],v[212:215],v[128:131]\n"
LD "v7,s[32:35],s72 offen nt lds\n"
MF "v[112:115],v[100:103],v[216:219],v[112:115]\n"
"s_add_u32 m0,s64,896\n"
"ds_write_b128 v32,a[132:135] offset:8192\n"
MF "v[116:119],v[100:103],v[220:223],v[116:119]\n"
LD "v7,s[32:35],s72 offen offset:128 nt lds\n"
MF "v[120:123],v[100:103],v[224:227],v[120:123]\n"
"s_add_u32 m0,s64,1792\n"
"ds_write_b128 v32,a[136:139] offset:12288\n"
MF "v[124:127],v[100:103],v[228:231],v[124:127]\n"
LD "v7,s[32:35],s72 offen offset:256 nt lds\n"
MF "v[128:131],v[100:103],v[232:235],v[128:131]\n"
"s_add_u32 s72,s72,6144\n"
"s_add_u32 m0,s64,6144\n"
MF "v[112:115],v[104:107],v[236:239],v[112:115]\n"
LD "v7,s[32:35],s72 offen nt lds\n"
MF "v[116:119],v[104:107],v[240:243],v[116:119]\n"
"s_add_u32 m0,s64,7040\n"
"ds_write_b128 v32,a[160:163] offset:16384\n"
MF "v[120:123],v[104:107],v[244:247],v[120:123]\n"
LD "v7,s[32:35],s72 offen offset:128 nt lds\n"
MF "v[124:127],v[104:107],v[248:251],v[124:127]\n"
"s_add_u32 m0,s64,7936\n"
"ds_write_b128 v32,a[164:167] offset:20480\n"
MF "v[128:131],v[104:107],v[252:255],v[128:131]\n"
LD "v7,s[32:35],s72 offen offset:256 nt lds\n"
".Lpgt0e_g:\n"
"s_cbranch_execz .Lisr6\n"
".Lt0e:\n"
"s_nop 1\n"
"v_max3_f32 v52,v52,v112,v113\n"
"v_max3_f32 v53,v53,v116,v117\n"
"v_max3_f32 v54,v54,v120,v121\n"
"v_max3_f32 v55,v55,v124,v125\n"
"s_nop 1\n"
"v_max3_f32 v56,v56,v128,v129\n"
"v_max3_f32 v52,v52,v114,v115\n"
"v_max3_f32 v53,v53,v118,v119\n"
"v_max3_f32 v54,v54,v122,v123\n"
"v_max3_f32 v55,v55,v126,v127\n"
"v_max3_f32 v56,v56,v130,v131\n"
"v_mov_b32 v57,v52\n"
"v_mov_b32 v58,v53\n"
"v_mov_b32 v59,v54\n"
"v_mov_b32 v60,v55\n"
"v_mov_b32 v61,v56\n"
"v_permlane16_swap_b32 v52,v57\n"
"v_permlane16_swap_b32 v53,v58\n"
"v_permlane16_swap_b32 v54,v59\n"
"v_permlane16_swap_b32 v55,v60\n"
"v_permlane16_swap_b32 v56,v61\n"
"v_max_f32 v52,v52,v57\n"
"v_max_f32 v53,v53,v58\n"
"v_max_f32 v54,v54,v59\n"
"v_max_f32 v55,v55,v60\n"
"v_max_f32 v56,v56,v61\n"
"v_mov_b32 v57,v52\n"
"v_mov_b32 v58,v53\n"
"v_mov_b32 v59,v54\n"
"v_mov_b32 v60,v55\n"
"v_mov_b32 v61,v56\n"
"v_permlane32_swap_b32 v52,v57\n"
"v_permlane32_swap_b32 v53,v58\n"
"v_permlane32_swap_b32 v54,v59\n"
"v_permlane32_swap_b32 v55,v60\n"
"v_permlane32_swap_b32 v56,v61\n"
"v_max_f32 v52,v52,v57\n"
"v_max_f32 v53,v53,v58\n"
"v_max_f32 v54,v54,v59\n"
"v_max_f32 v55,v55,v60\n"
"v_max_f32 v56,v56,v61\n"
"v_sub_f32 v32,0xc1300000,v52\n"
"v_sub_f32 v36,0xc1300000,v53\n"
"v_sub_f32 v40,0xc1300000,v54\n"
"v_sub_f32 v44,0xc1300000,v55\n"
"v_sub_f32 v48,0xc1300000,v56\n"
"v_mov_b32 v33,v32\n"
"v_pk_mov_b32 v[34:35],v[32:33],v[32:33]\n"
"v_mov_b32 v37,v36\n"
"v_pk_mov_b32 v[38:39],v[36:37],v[36:37]\n"
"v_mov_b32 v41,v40\n"
"v_pk_mov_b32 v[42:43],v[40:41],v[40:41]\n"
"v_mov_b32 v45,v44\n"
"v_pk_mov_b32 v[46:47],v[44:45],v[44:45]\n"
"v_mov_b32_e64 v49,v48\n"
"v_pk_mov_b32 v[50:51],v[48:49],v[48:49]\n"
"v_pk_add_f32 v[72:73],v[72:73],v[32:33]\n"
"v_pk_add_f32 v[74:75],v[74:75],v[32:33]\n"
"v_pk_add_f32 v[112:113],v[112:113],v[32:33]\n"
"v_pk_add_f32 v[114:115],v[114:115],v[32:33]\n"
"v_exp_f32 v72,v72\n"
"v_exp_f32 v73,v73\n"
"v_exp_f32 v74,v74\n"
"v_exp_f32 v75,v75\n"
"v_exp_f32 v112,v112\n"
"v_exp_f32 v113,v113\n"
"v_exp_f32 v114,v114\n"
"v_exp_f32 v115,v115\n"
"v_pk_add_f32 v[10:11],v[72:73],v[74:75]\n"
"v_pk_add_f32 v[28:29],v[112:113],v[114:115]\n"
"v_pk_add_f32 v[10:11],v[10:11],v[28:29]\n"
"v_add_f32_e64 v52,v10,v11\n"
CV "v72,v72,v73\n"
CV "v73,v74,v75\n"
CV "v74,v112,v113\n"
CV "v75,v114,v115\n"
"v_pk_add_f32 v[76:77],v[76:77],v[36:37]\n"
"v_pk_add_f32 v[78:79],v[78:79],v[36:37]\n"
"v_pk_add_f32 v[116:117],v[116:117],v[36:37]\n"
"v_pk_add_f32 v[118:119],v[118:119],v[36:37]\n"
"v_exp_f32 v76,v76\n"
"v_exp_f32 v77,v77\n"
"v_exp_f32 v78,v78\n"
"v_exp_f32 v79,v79\n"
"v_exp_f32 v116,v116\n"
"v_exp_f32 v117,v117\n"
"v_exp_f32 v118,v118\n"
"v_exp_f32 v119,v119\n"
"v_pk_add_f32 v[10:11],v[76:77],v[78:79]\n"
"v_pk_add_f32 v[28:29],v[116:117],v[118:119]\n"
"v_pk_add_f32 v[10:11],v[10:11],v[28:29]\n"
"v_add_f32_e64 v53,v10,v11\n"
CV "v76,v76,v77\n"
CV "v77,v78,v79\n"
CV "v78,v116,v117\n"
CV "v79,v118,v119\n"
"v_pk_add_f32 v[80:81],v[80:81],v[40:41]\n"
"v_pk_add_f32 v[82:83],v[82:83],v[40:41]\n"
"v_pk_add_f32 v[120:121],v[120:121],v[40:41]\n"
"v_pk_add_f32 v[122:123],v[122:123],v[40:41]\n"
"v_exp_f32 v80,v80\n"
"v_exp_f32 v81,v81\n"
"v_exp_f32 v82,v82\n"
"v_exp_f32 v83,v83\n"
"v_exp_f32 v120,v120\n"
"v_exp_f32 v121,v121\n"
"v_exp_f32 v122,v122\n"
"v_exp_f32 v123,v123\n"
"v_pk_add_f32 v[10:11],v[80:81],v[82:83]\n"
"v_pk_add_f32 v[28:29],v[120:121],v[122:123]\n"
"v_pk_add_f32 v[10:11],v[10:11],v[28:29]\n"
"v_add_f32_e64 v54,v10,v11\n"
CV "v80,v80,v81\n"
CV "v81,v82,v83\n"
CV "v82,v120,v121\n"
CV "v83,v122,v123\n"
"v_pk_add_f32 v[84:85],v[84:85],v[44:45]\n"
"v_pk_add_f32 v[86:87],v[86:87],v[44:45]\n"
"v_pk_add_f32 v[124:125],v[124:125],v[44:45]\n"
"v_pk_add_f32 v[126:127],v[126:127],v[44:45]\n"
"v_mov_b32_e64 v55,0\n"
"v_pk_add_f32 v[88:89],v[88:89],v[48:49]\n"
"v_pk_add_f32 v[90:91],v[90:91],v[48:49]\n"
"v_pk_add_f32 v[128:129],v[128:129],v[48:49]\n"
"v_pk_add_f32 v[130:131],v[130:131],v[48:49]\n"
"v_mov_b32 v56,0\n"
"s_waitcnt vmcnt(12) lgkmcnt(0)\n"
"s_barrier\n"
"ds_read_b128 v[16:19],v0 offset:24576\n"
"ds_read_b128 v[20:23],v1 offset:24576\n"
AW "a220,s13\n"
AW "a221,s13\n"
AW "a222,s13\n"
AW "a223,s13\n"
"s_waitcnt lgkmcnt(1)\n"
MF "v[92:95],v[16:19],v[136:139],v[32:35]\n"
"s_nop 0\n"
"ds_read_b128 v[24:27],v0 offset:25600\n"
"ds_read_b128 v[28:31],v1 offset:25600\n"
"s_waitcnt lgkmcnt(2)\n"
MF "v[92:95],v[20:23],v[156:159],v[92:95]\n"
"v_exp_f32 v84,v84\n"
MF "v[96:99],v[20:23],v[160:163],v[36:39]\n"
"v_exp_f32 v85,v85\n"
MF "v[96:99],v[16:19],v[140:143],v[96:99]\n"
"v_exp_f32 v86,v86\n"
MF "v[100:103],v[16:19],v[144:147],v[40:43]\n"
"v_exp_f32 v87,v87\n"
MF "v[100:103],v[20:23],v[164:167],v[100:103]\n"
"v_add_f32 v55,v55,v84\n"
"v_add_f32 v55,v55,v85\n"
MF "v[104:107],v[20:23],v[168:171],v[44:47]\n"
CV "v84,v84,v85\n"
"v_add_f32 v55,v55,v86\n"
MF "v[104:107],v[16:19],v[148:151],v[104:107]\n"
"v_add_f32 v55,v55,v87\n"
CV "v85,v86,v87\n"
MF "v[108:111],v[16:19],v[152:155],v[48:51]\n"
"v_exp_f32 v124,v124\n"
MF "v[108:111],v[20:23],v[172:175],v[108:111]\n"
"s_waitcnt lgkmcnt(0)\n"
"ds_read_b128 v[16:19],v0 offset:26624\n"
"ds_read_b128 v[20:23],v1 offset:26624\n"
MF "v[108:111],v[24:27],v[192:195],v[108:111]\n"
"v_exp_f32 v125,v125\n"
MF "v[108:111],v[28:31],v[212:215],v[108:111]\n"
"v_exp_f32 v126,v126\n"
MF "v[104:107],v[28:31],v[208:211],v[104:107]\n"
"v_exp_f32 v127,v127\n"
MF "v[104:107],v[24:27],v[188:191],v[104:107]\n"
"v_add_f32 v55,v55,v124\n"
"v_add_f32 v55,v55,v125\n"
MF "v[100:103],v[24:27],v[184:187],v[100:103]\n"
CV "v86,v124,v125\n"
"v_add_f32 v55,v55,v126\n"
MF "v[100:103],v[28:31],v[204:207],v[100:103]\n"
"v_add_f32 v55,v55,v127\n"
CV "v87,v126,v127\n"
MF "v[96:99],v[28:31],v[200:203],v[96:99]\n"
"v_exp_f32 v88,v88\n"
MF "v[96:99],v[24:27],v[180:183],v[96:99]\n"
"v_exp_f32 v89,v89\n"
MF "v[92:95],v[24:27],v[176:179],v[92:95]\n"
"v_exp_f32 v90,v90\n"
MF "v[92:95],v[28:31],v[196:199],v[92:95]\n"
"s_waitcnt lgkmcnt(0)\n"
"ds_read_b128 v[24:27],v0 offset:30720\n"
"ds_read_b128 v[28:31],v1 offset:30720\n"
MF "v[92:95],v[16:19],v[216:219],v[92:95]\n"
"v_exp_f32 v91,v91\n"
MF "v[92:95],v[20:23],v[236:239],v[92:95]\n"
"v_add_f32 v56,v56,v88\n"
"v_add_f32 v56,v56,v89\n"
MF "v[96:99],v[20:23],v[240:243],v[96:99]\n"
CV "v88,v88,v89\n"
"v_add_f32 v56,v56,v90\n"
MF "v[96:99],v[16:19],v[220:223],v[96:99]\n"
"v_add_f32 v56,v56,v91\n"
CV "v89,v90,v91\n"
MF "v[100:103],v[16:19],v[224:227],v[100:103]\n"
"v_exp_f32 v128,v128\n"
MF "v[100:103],v[20:23],v[244:247],v[100:103]\n"
"v_exp_f32 v129,v129\n"
MF "v[104:107],v[20:23],v[248:251],v[104:107]\n"
"v_exp_f32 v130,v130\n"
MF "v[104:107],v[16:19],v[228:231],v[104:107]\n"
"v_exp_f32 v131,v131\n"
MF "v[108:111],v[16:19],v[232:235],v[108:111]\n"
"v_add_f32 v56,v56,v128\n"
"v_add_f32 v56,v56,v129\n"
MF "v[108:111],v[20:23],v[252:255],v[108:111]\n"
"s_waitcnt lgkmcnt(0)\n"
"ds_read_b128 v[16:19],v0 offset:31744\n"
"ds_read_b128 v[20:23],v1 offset:31744\n"
MF "v[112:115],v[24:27],v[136:139],v[32:35]\n"
CV "v90,v128,v129\n"
"v_add_f32 v56,v56,v130\n"
MF "v[112:115],v[28:31],v[156:159],v[112:115]\n"
"v_add_f32 v56,v56,v131\n"
CV "v91,v130,v131\n"
MF "v[116:119],v[28:31],v[160:163],v[36:39]\n"
"v_exp_f32 v92,v92\n"
MF "v[116:119],v[24:27],v[140:143],v[116:119]\n"
"v_exp_f32 v93,v93\n"
MF "v[120:123],v[24:27],v[144:147],v[40:43]\n"
"v_exp_f32 v94,v94\n"
MF "v[120:123],v[28:31],v[164:167],v[120:123]\n"
"v_exp_f32 v95,v95\n"
MF "v[124:127],v[28:31],v[168:171],v[44:47]\n"
"v_add_f32 v57,v52,v92\n"
"v_add_f32 v57,v57,v93\n"
MF "v[124:127],v[24:27],v[148:151],v[124:127]\n"
CV "v92,v92,v93\n"
"v_add_f32 v57,v57,v94\n"
MF "v[128:131],v[24:27],v[152:155],v[48:51]\n"
"v_add_f32 v57,v57,v95\n"
CV "v93,v94,v95\n"
MF "v[128:131],v[28:31],v[172:175],v[128:131]\n"
"s_waitcnt lgkmcnt(0)\n"
"ds_read_b128 v[24:27],v0 offset:32768\n"
"ds_read_b128 v[28:31],v1 offset:32768\n"
MF "v[128:131],v[16:19],v[192:195],v[128:131]\n"
"v_exp_f32 v96,v96\n"
MF "v[128:131],v[20:23],v[212:215],v[128:131]\n"
"v_exp_f32 v97,v97\n"
MF "v[124:127],v[20:23],v[208:211],v[124:127]\n"
"v_exp_f32 v98,v98\n"
MF "v[124:127],v[16:19],v[188:191],v[124:127]\n"
"v_exp_f32 v99,v99\n"
MF "v[120:123],v[16:19],v[184:187],v[120:123]\n"
"v_add_f32 v58,v53,v96\n"
"v_add_f32 v58,v58,v97\n"
MF "v[120:123],v[20:23],v[204:207],v[120:123]\n"
CV "v96,v96,v97\n"
"v_add_f32 v58,v58,v98\n"
MF "v[116:119],v[20:23],v[200:203],v[116:119]\n"
"s_waitcnt vmcnt(6) lgkmcnt(0)\n"
"v_add_f32 v58,v58,v99\n"
CV "v97,v98,v99\n"
MF "v[116:119],v[16:19],v[180:183],v[116:119]\n"
"s_barrier\n"
MF "v[112:115],v[16:19],v[176:179],v[112:115]\n"
"v_exp_f32 v100,v100\n"
MF "v[112:115],v[20:23],v[196:199],v[112:115]\n"
"s_nop 0\n"
TR "v[16:17],v2 offset:49152\n"
TR "v[18:19],v2 offset:55296\n"
MF "v[112:115],v[24:27],v[216:219],v[112:115]\n"
"s_add_u32 s73,s73,12288\n"
TR "v[20:21],v3 offset:49152\n"
TR "v[22:23],v3 offset:55296\n"
MF "v[112:115],v[28:31],v[236:239],v[112:115]\n"
"s_cmp_eq_u32 s73,s19\n"
"s_cselect_b32 s73,s18,s73\n"
"s_add_u32 m0,s65,24576\n"
MF "v[116:119],v[28:31],v[240:243],v[116:119]\n"
LD "v8,s[36:39],s73 offen nt lds\n"
MF "v[116:119],v[24:27],v[220:223],v[116:119]\n"
"s_add_u32 m0,s65,25472\n"
"v_exp_f32 v101,v101\n"
MF "v[120:123],v[24:27],v[224:227],v[120:123]\n"
LD "v8,s[36:39],s73 offen offset:128 nt lds\n"
MF "v[120:123],v[28:31],v[244:247],v[120:123]\n"
"v_exp_f32 v102,v102\n"
MF "v[124:127],v[28:31],v[248:251],v[124:127]\n"
"s_add_u32 m0,s65,26368\n"
"v_exp_f32 v103,v103\n"
MF "v[124:127],v[24:27],v[228:231],v[124:127]\n"
LD "v8,s[36:39],s73 offen offset:256 nt lds\n"
MF "v[128:131],v[24:27],v[232:235],v[128:131]\n"
"v_add_f32 v59,v54,v100\n"
"v_add_f32 v59,v59,v101\n"
MF "v[128:131],v[28:31],v[252:255],v[128:131]\n"
"s_waitcnt lgkmcnt(0)\n"
TR "v[24:25],v4 offset:49152\n"
TR "v[26:27],v4 offset:55296\n"
MF "a[0:3],v[16:19],v[72:75],a[220:223]\n"
"s_add_u32 m0,s65,24576\n"
TR "v[28:29],v5 offset:49152\n"
TR "v[30:31],v5 offset:55296\n"
MF "a[20:23],v[20:23],v[72:75],a[220:223]\n"
LD "v8,s[36:39],s73 offen offset:3072 nt lds\n"
MF "a[24:27],v[20:23],v[76:79],a[220:223]\n"
CV "v100,v100,v101\n"
"v_add_f32 v59,v59,v102\n"
MF "a[4:7],v[16:19],v[76:79],a[220:223]\n"
"s_add_u32 m0,s65,25472\n"
"v_add_f32 v59,v59,v103\n"
CV "v101,v102,v103\n"
MF "a[8:11],v[16:19],v[80:83],a[220:223]\n"
LD "v8,s[36:39],s73 offen offset:3200 nt lds\n"
MF "a[28:31],v[20:23],v[80:83],a[220:223]\n"
"v_exp_f32 v104,v104\n"
MF "a[32:35],v[20:23],v[84:87],a[220:223]\n"
"s_add_u32 m0,s65,26368\n"
"v_exp_f32 v105,v105\n"
MF "a[12:15],v[16:19],v[84:87],a[220:223]\n"
LD "v8,s[36:39],s73 offen offset:3328 nt lds\n"
MF "a[16:19],v[16:19],v[88:91],a[220:223]\n"
"v_exp_f32 v106,v106\n"
MF "a[36:39],v[20:23],v[88:91],a[220:223]\n"
"s_waitcnt lgkmcnt(0)\n"
TR "v[16:17],v2 offset:50176\n"
TR "v[18:19],v2 offset:56320\n"
MF "a[56:59],v[24:27],v[88:91],a[220:223]\n"
"s_nop 0\n"
TR "v[20:21],v3 offset:50176\n"
TR "v[22:23],v3 offset:56320\n"
MF "a[76:79],v[28:31],v[88:91],a[220:223]\n"
"v_exp_f32 v107,v107\n"
MF "a[72:75],v[28:31],v[84:87],a[220:223]\n"
"v_add_f32 v60,v55,v104\n"
"v_add_f32 v60,v60,v105\n"
MF "a[52:55],v[24:27],v[84:87],a[220:223]\n"
CV "v104,v104,v105\n"
"v_add_f32 v60,v60,v106\n"
MF "a[48:51],v[24:27],v[80:83],a[220:223]\n"
"v_add_f32 v60,v60,v107\n"
CV "v105,v106,v107\n"
MF "a[68:71],v[28:31],v[80:83],a[220:223]\n"
"v_exp_f32 v108,v108\n"
MF "a[64:67],v[28:31],v[76:79],a[220:223]\n"
"v_exp_f32 v109,v109\n"
MF "a[44:47],v[24:27],v[76:79],a[220:223]\n"
"v_exp_f32 v110,v110\n"
MF "a[40:43],v[24:27],v[72:75],a[220:223]\n"
"v_exp_f32 v111,v111\n"
MF "a[60:63],v[28:31],v[72:75],a[220:223]\n"
"s_waitcnt lgkmcnt(0)\n"
TR "v[24:25],v4 offset:50176\n"
TR "v[26:27],v4 offset:56320\n"
MF "a[80:83],v[16:19],v[72:75],a[220:223]\n"
"s_nop 0\n"
TR "v[28:29],v5 offset:50176\n"
TR "v[30:31],v5 offset:56320\n"
MF "a[100:103],v[20:23],v[72:75],a[220:223]\n"
"v_add_f32 v61,v56,v108\n"
"v_add_f32 v61,v61,v109\n"
MF "a[104:107],v[20:23],v[76:79],a[220:223]\n"
CV "v108,v108,v109\n"
"v_add_f32 v61,v61,v110\n"
MF "a[84:87],v[16:19],v[76:79],a[220:223]\n"
"v_add_f32 v61,v61,v111\n"
CV "v109,v110,v111\n"
MF "a[88:91],v[16:19],v[80:83],a[220:223]\n"
"v_exp_f32 v112,v112\n"
MF "a[108:111],v[20:23],v[80:83],a[220:223]\n"
"v_exp_f32 v113,v113\n"
MF "a[112:115],v[20:23],v[84:87],a[220:223]\n"
"v_exp_f32 v114,v114\n"
MF "a[92:95],v[16:19],v[84:87],a[220:223]\n"
"v_exp_f32 v115,v115\n"
MF "a[96:99],v[16:19],v[88:91],a[220:223]\n"
"v_add_f32 v57,v57,v112\n"
"v_add_f32 v57,v57,v113\n"
MF "a[116:119],v[20:23],v[88:91],a[220:223]\n"
"s_waitcnt lgkmcnt(0)\n"
TR "v[16:17],v2 offset:51200\n"
TR "v[18:19],v2 offset:57344\n"
MF "a[136:139],v[24:27],v[88:91],a[220:223]\n"
"s_nop 0\n"
TR "v[20:21],v3 offset:51200\n"
TR "v[22:23],v3 offset:57344\n"
MF "a[156:159],v[28:31],v[88:91],a[220:223]\n"
CV "v94,v112,v113\n"
"v_add_f32 v57,v57,v114\n"
MF "a[152:155],v[28:31],v[84:87],a[220:223]\n"
"v_add_f32 v57,v57,v115\n"
CV "v95,v114,v115\n"
MF "a[132:135],v[24:27],v[84:87],a[220:223]\n"
"v_exp_f32 v116,v116\n"
MF "a[128:131],v[24:27],v[80:83],a[220:223]\n"
"v_exp_f32 v117,v117\n"
MF "a[148:151],v[28:31],v[80:83],a[220:223]\n"
"v_exp_f32 v118,v118\n"
MF "a[144:147],v[28:31],v[76:79],a[220:223]\n"
"v_exp_f32 v119,v119\n"
MF "a[124:127],v[24:27],v[76:79],a[220:223]\n"
"v_add_f32 v58,v58,v116\n"
"v_add_f32 v58,v58,v117\n"
MF "a[120:123],v[24:27],v[72:75],a[220:223]\n"
CV "v98,v116,v117\n"
"v_add_f32 v58,v58,v118\n"
MF "a[140:143],v[28:31],v[72:75],a[220:223]\n"
"s_waitcnt lgkmcnt(0)\n"
TR "v[24:25],v4 offset:51200\n"
TR "v[26:27],v4 offset:57344\n"
MF "a[160:163],v[16:19],v[72:75],a[220:223]\n"
"s_nop 0\n"
TR "v[28:29],v5 offset:51200\n"
TR "v[30:31],v5 offset:57344\n"
MF "a[180:183],v[20:23],v[72:75],a[220:223]\n"
"v_add_f32 v58,v58,v119\n"
CV "v99,v118,v119\n"
MF "a[184:187],v[20:23],v[76:79],a[220:223]\n"
"v_exp_f32 v120,v120\n"
MF "a[164:167],v[16:19],v[76:79],a[220:223]\n"
"v_exp_f32 v121,v121\n"
MF "a[168:171],v[16:19],v[80:83],a[220:223]\n"
"v_exp_f32 v122,v122\n"
MF "a[188:191],v[20:23],v[80:83],a[220:223]\n"
"v_exp_f32 v123,v123\n"
MF "a[192:195],v[20:23],v[84:87],a[220:223]\n"
"v_add_f32 v59,v59,v120\n"
"v_add_f32 v59,v59,v121\n"
MF "a[172:175],v[16:19],v[84:87],a[220:223]\n"
"s_waitcnt vmcnt(6) lgkmcnt(0)\n"
CV "v102,v120,v121\n"
"v_add_f32 v59,v59,v122\n"
MF "a[176:179],v[16:19],v[88:91],a[220:223]\n"
"s_barrier\n"
MF "a[196:199],v[20:23],v[88:91],a[220:223]\n"
"s_add_u32 s72,s72,6144\n"
"ds_read_b128 v[16:19],v0\n"
"ds_read_b128 v[20:23],v1\n"
MF "a[216:219],v[24:27],v[88:91],a[220:223]\n"
"s_cmp_eq_u32 s72,s17\n"
"s_cselect_b32 s72,s16,s72\n"
"s_add_u32 m0,s64,49152\n"
MF "a[236:239],v[28:31],v[88:91],a[220:223]\n"
LD "v7,s[32:35],s72 offen nt lds\n"
MF "a[232:235],v[28:31],v[84:87],a[220:223]\n"
"s_add_u32 m0,s64,50048\n"
"v_add_f32 v59,v59,v123\n"
CV "v103,v122,v123\n"
MF "a[212:215],v[24:27],v[84:87],a[220:223]\n"
LD "v7,s[32:35],s72 offen offset:128 nt lds\n"
MF "a[208:211],v[24:27],v[80:83],a[220:223]\n"
"s_add_u32 m0,s64,50944\n"
MF "a[228:231],v[28:31],v[80:83],a[220:223]\n"
LD "v7,s[32:35],s72 offen offset:256 nt lds\n"
MF "a[224:227],v[28:31],v[76:79],a[220:223]\n"
MF "a[204:207],v[24:27],v[76:79],a[220:223]\n"
MF "a[200:203],v[24:27],v[72:75],a[220:223]\n"
MF "a[220:223],v[28:31],v[72:75],a[220:223]\n"
"s_waitcnt lgkmcnt(0)\n"
".Laloop:\n"
MF "v[72:75],v[16:19],v[136:139],v[32:35]\n"
"s_nop 0\n"
"ds_read_b128 v[24:27],v0 offset:1024\n"
"ds_read_b128 v[28:31],v1 offset:1024\n"
MF "v[72:75],v[20:23],v[156:159],v[72:75]\n"
MF "v[76:79],v[20:23],v[160:163],v[36:39]\n"
MF "v[76:79],v[16:19],v[140:143],v[76:79]\n"
MF "v[80:83],v[16:19],v[144:147],v[40:43]\n"
MF "v[80:83],v[20:23],v[164:167],v[80:83]\n"
MF "v[84:87],v[20:23],v[168:171],v[44:47]\n"
MF "v[84:87],v[16:19],v[148:151],v[84:87]\n"
"s_add_u32 s72,s72,6144\n"
"s_add_u32 m0,s64,55296\n"
MF "v[88:91],v[16:19],v[152:155],v[48:51]\n"
LD "v7,s[32:35],s72 offen nt lds\n"
MF "v[88:91],v[20:23],v[172:175],v[88:91]\n"
"s_waitcnt lgkmcnt(0)\n"
"ds_read_b128 v[16:19],v0 offset:2048\n"
"ds_read_b128 v[20:23],v1 offset:2048\n"
MF "v[88:91],v[24:27],v[192:195],v[88:91]\n"
"v_exp_f32 v124,v124\n"
MF "v[88:91],v[28:31],v[212:215],v[88:91]\n"
"s_add_u32 m0,s64,56192\n"
"v_exp_f32 v125,v125\n"
MF "v[84:87],v[28:31],v[208:211],v[84:87]\n"
LD "v7,s[32:35],s72 offen offset:128 nt lds\n"
MF "v[84:87],v[24:27],v[188:191],v[84:87]\n"
"v_exp_f32 v126,v126\n"
MF "v[80:83],v[24:27],v[184:187],v[80:83]\n"
"v_exp_f32 v127,v127\n"
MF "v[80:83],v[28:31],v[204:207],v[80:83]\n"
"s_add_u32 m0,s64,57088\n"
"v_add_f32 v60,v60,v124\n"
"v_add_f32 v60,v60,v125\n"
MF "v[76:79],v[28:31],v[200:203],v[76:79]\n"
LD "v7,s[32:35],s72 offen offset:256 nt lds\n"
MF "v[76:79],v[24:27],v[180:183],v[76:79]\n"
CV "v106,v124,v125\n"
"v_add_f32 v60,v60,v126\n"
MF "v[72:75],v[24:27],v[176:179],v[72:75]\n"
"v_add_f32 v60,v60,v127\n"
CV "v107,v126,v127\n"
MF "v[72:75],v[28:31],v[196:199],v[72:75]\n"
"s_waitcnt lgkmcnt(0)\n"
"ds_read_b128 v[24:27],v0 offset:6144\n"
"ds_read_b128 v[28:31],v1 offset:6144\n"
MF "v[72:75],v[16:19],v[216:219],v[72:75]\n"
"v_exp_f32 v128,v128\n"
MF "v[72:75],v[20:23],v[236:239],v[72:75]\n"
"v_exp_f32 v129,v129\n"
MF "v[76:79],v[20:23],v[240:243],v[76:79]\n"
"v_exp_f32 v130,v130\n"
MF "v[76:79],v[16:19],v[220:223],v[76:79]\n"
"v_exp_f32 v131,v131\n"
MF "v[80:83],v[16:19],v[224:227],v[80:83]\n"
"v_add_f32 v61,v61,v128\n"
"v_add_f32 v61,v61,v129\n"
MF "v[80:83],v[20:23],v[244:247],v[80:83]\n"
CV "v110,v128,v129\n"
"v_add_f32 v61,v61,v130\n"
MF "v[84:87],v[20:23],v[248:251],v[84:87]\n"
"v_add_f32 v61,v61,v131\n"
CV "v111,v130,v131\n"
MF "v[84:87],v[16:19],v[228:231],v[84:87]\n"
"v_max3_f32 v10,v57,v58,v59\n"
"v_max3_f32 v10,v10,v60,v61\n"
MF "v[88:91],v[16:19],v[232:235],v[88:91]\n"
"v_cmp_ngt_f32 vcc,s77,v10\n"
MF "v[88:91],v[20:23],v[252:255],v[88:91]\n"
"s_waitcnt lgkmcnt(0)\n"
"s_cbranch_vccnz .Lstub_20\n"
".Lback_20:\n"
MF "v[112:115],v[24:27],v[136:139],v[32:35]\n"
"s_add_u32 s74,s74,12288\n"
"ds_read_b128 v[16:19],v0 offset:7168\n"
"ds_read_b128 v[20:23],v1 offset:7168\n"
MF "v[112:115],v[28:31],v[156:159],v[112:115]\n"
"s_cmp_eq_u32 s74,s20\n"
"s_cselect_b32 s74,s70,s74\n"
MF "v[116:119],v[28:31],v[160:163],v[36:39]\n"
"v_exp_f32 v72,v72\n"
MF "v[116:119],v[24:27],v[140:143],v[116:119]\n"
"v_exp_f32 v73,v73\n"
MF "v[120:123],v[24:27],v[144:147],v[40:43]\n"
"v_exp_f32 v74,v74\n"
MF "v[120:123],v[28:31],v[164:167],v[120:123]\n"
"v_exp_f32 v75,v75\n"
MF "v[124:127],v[28:31],v[168:171],v[44:47]\n"
"v_add_f32 v52,v57,v72\n"
"v_add_f32 v52,v52,v73\n"
MF "v[124:127],v[24:27],v[148:151],v[124:127]\n"
CV "v72,v72,v73\n"
"v_add_f32 v52,v52,v74\n"
MF "v[128:131],v[24:27],v[152:155],v[48:51]\n"
"v_add_f32 v52,v52,v75\n"
CV "v73,v74,v75\n"
MF "v[128:131],v[28:31],v[172:175],v[128:131]\n"
"s_waitcnt lgkmcnt(0)\n"
"ds_read_b128 v[24:27],v0 offset:8192\n"
"ds_read_b128 v[28:31],v1 offset:8192\n"
MF "v[128:131],v[16:19],v[192:195],v[128:131]\n"
"v_exp_f32 v76,v76\n"
MF "v[128:131],v[20:23],v[212:215],v[128:131]\n"
"v_exp_f32 v77,v77\n"
MF "v[124:127],v[20:23],v[208:211],v[124:127]\n"
"v_exp_f32 v78,v78\n"
MF "v[124:127],v[16:19],v[188:191],v[124:127]\n"
"v_exp_f32 v79,v79\n"
MF "v[120:123],v[16:19],v[184:187],v[120:123]\n"
"v_add_f32 v53,v58,v76\n"
"v_add_f32 v53,v53,v77\n"
MF "v[120:123],v[20:23],v[204:207],v[120:123]\n"
CV "v76,v76,v77\n"
"v_add_f32 v53,v53,v78\n"
MF "v[116:119],v[20:23],v[200:203],v[116:119]\n"
"s_waitcnt vmcnt(6) lgkmcnt(0)\n"
"v_add_f32 v53,v53,v79\n"
CV "v77,v78,v79\n"
MF "v[116:119],v[16:19],v[180:183],v[116:119]\n"
"s_barrier\n"
MF "v[112:115],v[16:19],v[176:179],v[112:115]\n"
"v_exp_f32 v80,v80\n"
MF "v[112:115],v[20:23],v[196:199],v[112:115]\n"
"s_nop 0\n"
TR "v[16:17],v2 offset:24576\n"
TR "v[18:19],v2 offset:30720\n"
MF "v[112:115],v[24:27],v[216:219],v[112:115]\n"
"s_add_u32 s73,s73,12288\n"
TR "v[20:21],v3 offset:24576\n"
TR "v[22:23],v3 offset:30720\n"
MF "v[112:115],v[28:31],v[236:239],v[112:115]\n"
"s_cmp_eq_u32 s73,s19\n"
"s_cselect_b32 s73,s18,s73\n"
"s_add_u32 m0,s65,0\n"
MF "v[116:119],v[28:31],v[240:243],v[116:119]\n"
LD "v8,s[36:39],s73 offen nt lds\n"
MF "v[116:119],v[24:27],v[220:223],v[116:119]\n"
"s_add_u32 m0,s65,896\n"
"v_exp_f32 v81,v81\n"
MF "v[120:123],v[24:27],v[224:227],v[120:123]\n"
LD "v8,s[36:39],s73 offen offset:128 nt lds\n"
MF "v[120:123],v[28:31],v[244:247],v[120:123]\n"
"v_exp_f32 v82,v82\n"
MF "v[124:127],v[28:31],v[248:251],v[124:127]\n"
"s_add_u32 m0,s65,1792\n"
"v_exp_f32 v83,v83\n"
MF "v[124:127],v[24:27],v[228:231],v[124:127]\n"
LD "v8,s[36:39],s73 offen offset:256 nt lds\n"
MF "v[128:131],v[24:27],v[232:235],v[128:131]\n"
"v_add_f32 v54,v59,v80\n"
"v_add_f32 v54,v54,v81\n"
MF "v[128:131],v[28:31],v[252:255],v[128:131]\n"
"s_waitcnt lgkmcnt(0)\n"
TR "v[24:25],v4 offset:24576\n"
TR "v[26:27],v4 offset:30720\n"
MF "a[0:3],v[16:19],v[92:95],a[0:3]\n"
"s_add_u32 m0,s65,0\n"
TR "v[28:29],v5 offset:24576\n"
TR "v[30:31],v5 offset:30720\n"
MF "a[20:23],v[20:23],v[92:95],a[20:23]\n"
LD "v8,s[36:39],s73 offen offset:3072 nt lds\n"
MF "a[24:27],v[20:23],v[96:99],a[24:27]\n"
CV "v80,v80,v81\n"
"v_add_f32 v54,v54,v82\n"
MF "a[4:7],v[16:19],v[96:99],a[4:7]\n"
"s_add_u32 m0,s65,896\n"
"v_add_f32 v54,v54,v83\n"
CV "v81,v82,v83\n"
MF "a[8:11],v[16:19],v[100:103],a[8:11]\n"
LD "v8,s[36:39],s73 offen offset:3200 nt lds\n"
MF "a[28:31],v[20:23],v[100:103],a[28:31]\n"
"v_exp_f32 v84,v84\n"
MF "a[32:35],v[20:23],v[104:107],a[32:35]\n"
"s_add_u32 m0,s65,1792\n"
"v_exp_f32 v85,v85\n"
MF "a[12:15],v[16:19],v[104:107],a[12:15]\n"
LD "v8,s[36:39],s73 offen offset:3328 nt lds\n"
MF "a[16:19],v[16:19],v[108:111],a[16:19]\n"
"v_exp_f32 v86,v86\n"
MF "a[36:39],v[20:23],v[108:111],a[36:39]\n"
"s_waitcnt lgkmcnt(0)\n"
TR "v[16:17],v2 offset:25600\n"
TR "v[18:19],v2 offset:31744\n"
MF "a[56:59],v[24:27],v[108:111],a[56:59]\n"
"s_nop 0\n"
TR "v[20:21],v3 offset:25600\n"
TR "v[22:23],v3 offset:31744\n"
MF "a[76:79],v[28:31],v[108:111],a[76:79]\n"
"v_exp_f32 v87,v87\n"
MF "a[72:75],v[28:31],v[104:107],a[72:75]\n"
"v_add_f32 v55,v60,v84\n"
"v_add_f32 v55,v55,v85\n"
MF "a[52:55],v[24:27],v[104:107],a[52:55]\n"
CV "v84,v84,v85\n"
"v_add_f32 v55,v55,v86\n"
MF "a[48:51],v[24:27],v[100:103],a[48:51]\n"
"v_add_f32 v55,v55,v87\n"
CV "v85,v86,v87\n"
MF "a[68:71],v[28:31],v[100:103],a[68:71]\n"
"v_exp_f32 v88,v88\n"
MF "a[64:67],v[28:31],v[96:99],a[64:67]\n"
"v_exp_f32 v89,v89\n"
MF "a[44:47],v[24:27],v[96:99],a[44:47]\n"
"v_exp_f32 v90,v90\n"
MF "a[40:43],v[24:27],v[92:95],a[40:43]\n"
"v_exp_f32 v91,v91\n"
MF "a[60:63],v[28:31],v[92:95],a[60:63]\n"
"s_waitcnt lgkmcnt(0)\n"
TR "v[24:25],v4 offset:25600\n"
TR "v[26:27],v4 offset:31744\n"
MF "a[80:83],v[16:19],v[92:95],a[80:83]\n"
"s_nop 0\n"
TR "v[28:29],v5 offset:25600\n"
TR "v[30:31],v5 offset:31744\n"
MF "a[100:103],v[20:23],v[92:95],a[100:103]\n"
"v_add_f32 v56,v61,v88\n"
"v_add_f32 v56,v56,v89\n"
MF "a[104:107],v[20:23],v[96:99],a[104:107]\n"
CV "v88,v88,v89\n"
"v_add_f32 v56,v56,v90\n"
MF "a[84:87],v[16:19],v[96:99],a[84:87]\n"
"v_add_f32 v56,v56,v91\n"
CV "v89,v90,v91\n"
MF "a[88:91],v[16:19],v[100:103],a[88:91]\n"
"v_exp_f32 v112,v112\n"
MF "a[108:111],v[20:23],v[100:103],a[108:111]\n"
"v_exp_f32 v113,v113\n"
MF "a[112:115],v[20:23],v[104:107],a[112:115]\n"
"v_exp_f32 v114,v114\n"
MF "a[92:95],v[16:19],v[104:107],a[92:95]\n"
"v_exp_f32 v115,v115\n"
MF "a[96:99],v[16:19],v[108:111],a[96:99]\n"
"v_add_f32 v52,v52,v112\n"
"v_add_f32 v52,v52,v113\n"
MF "a[116:119],v[20:23],v[108:111],a[116:119]\n"
"s_waitcnt lgkmcnt(0)\n"
TR "v[16:17],v2 offset:26624\n"
TR "v[18:19],v2 offset:32768\n"
MF "a[136:139],v[24:27],v[108:111],a[136:139]\n"
"s_nop 0\n"
TR "v[20:21],v3 offset:26624\n"
TR "v[22:23],v3 offset:32768\n"
MF "a[156:159],v[28:31],v[108:111],a[156:159]\n"
CV "v74,v112,v113\n"
"v_add_f32 v52,v52,v114\n"
MF "a[152:155],v[28:31],v[104:107],a[152:155]\n"
"v_add_f32 v52,v52,v115\n"
CV "v75,v114,v115\n"
MF "a[132:135],v[24:27],v[104:107],a[132:135]\n"
"v_exp_f32 v116,v116\n"
MF "a[128:131],v[24:27],v[100:103],a[128:131]\n"
"v_exp_f32 v117,v117\n"
MF "a[148:151],v[28:31],v[100:103],a[148:151]\n"
"v_exp_f32 v118,v118\n"
MF "a[144:147],v[28:31],v[96:99],a[144:147]\n"
"v_exp_f32 v119,v119\n"
MF "a[124:127],v[24:27],v[96:99],a[124:127]\n"
"v_add_f32 v53,v53,v116\n"
"v_add_f32 v53,v53,v117\n"
MF "a[120:123],v[24:27],v[92:95],a[120:123]\n"
CV "v78,v116,v117\n"
"v_add_f32 v53,v53,v118\n"
MF "a[140:143],v[28:31],v[92:95],a[140:143]\n"
"s_waitcnt lgkmcnt(0)\n"
TR "v[24:25],v4 offset:26624\n"
TR "v[26:27],v4 offset:32768\n"
MF "a[160:163],v[16:19],v[92:95],a[160:163]\n"
"s_nop 0\n"
TR "v[28:29],v5 offset:26624\n"
TR "v[30:31],v5 offset:32768\n"
MF "a[180:183],v[20:23],v[92:95],a[180:183]\n"
"v_add_f32 v53,v53,v119\n"
CV "v79,v118,v119\n"
MF "a[184:187],v[20:23],v[96:99],a[184:187]\n"
"v_exp_f32 v120,v120\n"
MF "a[164:167],v[16:19],v[96:99],a[164:167]\n"
"v_exp_f32 v121,v121\n"
MF "a[168:171],v[16:19],v[100:103],a[168:171]\n"
"v_exp_f32 v122,v122\n"
MF "a[188:191],v[20:23],v[100:103],a[188:191]\n"
"v_exp_f32 v123,v123\n"
MF "a[192:195],v[20:23],v[104:107],a[192:195]\n"
"v_add_f32 v54,v54,v120\n"
"v_add_f32 v54,v54,v121\n"
MF "a[172:175],v[16:19],v[104:107],a[172:175]\n"
"s_waitcnt vmcnt(6) lgkmcnt(0)\n"
CV "v82,v120,v121\n"
"v_add_f32 v54,v54,v122\n"
MF "a[176:179],v[16:19],v[108:111],a[176:179]\n"
"s_barrier\n"
MF "a[196:199],v[20:23],v[108:111],a[196:199]\n"
"s_add_u32 s72,s72,6144\n"
"ds_read_b128 v[16:19],v0 offset:49152\n"
"ds_read_b128 v[20:23],v1 offset:49152\n"
MF "a[216:219],v[24:27],v[108:111],a[216:219]\n"
"s_cmp_eq_u32 s72,s17\n"
"s_cselect_b32 s72,s16,s72\n"
"s_add_u32 m0,s64,24576\n"
MF "a[236:239],v[28:31],v[108:111],a[236:239]\n"
LD "v7,s[32:35],s72 offen nt lds\n"
MF "a[232:235],v[28:31],v[104:107],a[232:235]\n"
"s_add_u32 m0,s64,25472\n"
"v_add_f32 v54,v54,v123\n"
CV "v83,v122,v123\n"
MF "a[212:215],v[24:27],v[104:107],a[212:215]\n"
LD "v7,s[32:35],s72 offen offset:128 nt lds\n"
MF "a[208:211],v[24:27],v[100:103],a[208:211]\n"
"s_add_u32 m0,s64,26368\n"
MF "a[228:231],v[28:31],v[100:103],a[228:231]\n"
LD "v7,s[32:35],s72 offen offset:256 nt lds\n"
MF "a[224:227],v[28:31],v[96:99],a[224:227]\n"
MF "a[204:207],v[24:27],v[96:99],a[204:207]\n"
MF "a[200:203],v[24:27],v[92:95],a[200:203]\n"
MF "a[220:223],v[28:31],v[92:95],a[220:223]\n"
"s_waitcnt lgkmcnt(0)\n"
MF "v[92:95],v[16:19],v[136:139],v[32:35]\n"
"s_add_u32 s72,s72,6144\n"
"ds_read_b128 v[24:27],v0 offset:50176\n"
"ds_read_b128 v[28:31],v1 offset:50176\n"
MF "v[92:95],v[20:23],v[156:159],v[92:95]\n"
"s_add_u32 m0,s64,30720\n"
"v_exp_f32 v124,v124\n"
MF "v[96:99],v[20:23],v[160:163],v[36:39]\n"
LD "v7,s[32:35],s72 offen nt lds\n"
MF "v[96:99],v[16:19],v[140:143],v[96:99]\n"
"v_exp_f32 v125,v125\n"
MF "v[100:103],v[16:19],v[144:147],v[40:43]\n"
"s_add_u32 m0,s64,31616\n"
"v_exp_f32 v126,v126\n"
MF "v[100:103],v[20:23],v[164:167],v[100:103]\n"
LD "v7,s[32:35],s72 offen offset:128 nt lds\n"
MF "v[104:107],v[20:23],v[168:171],v[44:47]\n"
"v_exp_f32 v127,v127\n"
MF "v[104:107],v[16:19],v[148:151],v[104:107]\n"
"s_add_u32 m0,s64,32512\n"
"v_add_f32 v55,v55,v124\n"
"v_add_f32 v55,v55,v125\n"
MF "v[108:111],v[16:19],v[152:155],v[48:51]\n"
LD "v7,s[32:35],s72 offen offset:256 nt lds\n"
MF "v[108:111],v[20:23],v[172:175],v[108:111]\n"
"s_waitcnt lgkmcnt(0)\n"
"ds_read_b128 v[16:19],v0 offset:51200\n"
"ds_read_b128 v[20:23],v1 offset:51200\n"
MF "v[108:111],v[24:27],v[192:195],v[108:111]\n"
CV "v86,v124,v125\n"
"v_add_f32 v55,v55,v126\n"
MF "v[108:111],v[28:31],v[212:215],v[108:111]\n"
"v_add_f32 v55,v55,v127\n"
CV "v87,v126,v127\n"
MF "v[104:107],v[28:31],v[208:211],v[104:107]\n"
"v_exp_f32 v128,v128\n"
MF "v[104:107],v[24:27],v[188:191],v[104:107]\n"
"v_exp_f32 v129,v129\n"
MF "v[100:103],v[24:27],v[184:187],v[100:103]\n"
"v_exp_f32 v130,v130\n"
MF "v[100:103],v[28:31],v[204:207],v[100:103]\n"
"v_exp_f32 v131,v131\n"
MF "v[96:99],v[28:31],v[200:203],v[96:99]\n"
"v_add_f32 v56,v56,v128\n"
"v_add_f32 v56,v56,v129\n"
MF "v[96:99],v[24:27],v[180:183],v[96:99]\n"
CV "v90,v128,v129\n"
"v_add_f32 v56,v56,v130\n"
MF "v[92:95],v[24:27],v[176:179],v[92:95]\n"
"v_add_f32 v56,v56,v131\n"
CV "v91,v130,v131\n"
MF "v[92:95],v[28:31],v[196:199],v[92:95]\n"
"s_waitcnt lgkmcnt(0)\n"
"ds_read_b128 v[24:27],v0 offset:55296\n"
"ds_read_b128 v[28:31],v1 offset:55296\n"
MF "v[92:95],v[16:19],v[216:219],v[92:95]\n"
MF "v[92:95],v[20:23],v[236:239],v[92:95]\n"
MF "v[96:99],v[20:23],v[240:243],v[96:99]\n"
MF "v[96:99],v[16:19],v[220:223],v[96:99]\n"
MF "v[100:103],v[16:19],v[224:227],v[100:103]\n"
MF "v[100:103],v[20:23],v[244:247],v[100:103]\n"
MF "v[104:107],v[20:23],v[248:251],v[104:107]\n"
MF "v[104:107],v[16:19],v[228:231],v[104:107]\n"
"v_max3_f32 v10,v52,v53,v54\n"
"v_max3_f32 v10,v10,v55,v56\n"
MF "v[108:111],v[16:19],v[232:235],v[108:111]\n"
"v_cmp_ngt_f32 vcc,s77,v10\n"
MF "v[108:111],v[20:23],v[252:255],v[108:111]\n"
"s_waitcnt lgkmcnt(0)\n"
"s_cbranch_vccnz .Lstub_21\n"
".Lback_21:\n"
MF "v[112:115],v[24:27],v[136:139],v[32:35]\n"
"s_add_u32 s74,s74,12288\n"
"ds_read_b128 v[16:19],v0 offset:56320\n"
"ds_read_b128 v[20:23],v1 offset:56320\n"
MF "v[112:115],v[28:31],v[156:159],v[112:115]\n"
"s_cmp_eq_u32 s74,s20\n"
"s_cselect_b32 s74,s70,s74\n"
MF "v[116:119],v[28:31],v[160:163],v[36:39]\n"
"v_exp_f32 v92,v92\n"
MF "v[116:119],v[24:27],v[140:143],v[116:119]\n"
"v_exp_f32 v93,v93\n"
MF "v[120:123],v[24:27],v[144:147],v[40:43]\n"
"v_exp_f32 v94,v94\n"
MF "v[120:123],v[28:31],v[164:167],v[120:123]\n"
"v_exp_f32 v95,v95\n"
MF "v[124:127],v[28:31],v[168:171],v[44:47]\n"
"v_add_f32 v57,v52,v92\n"
"v_add_f32 v57,v57,v93\n"
MF "v[124:127],v[24:27],v[148:151],v[124:127]\n"
CV "v92,v92,v93\n"
"v_add_f32 v57,v57,v94\n"
MF "v[128:131],v[24:27],v[152:155],v[48:51]\n"
"v_add_f32 v57,v57,v95\n"
CV "v93,v94,v95\n"
MF "v[128:131],v[28:31],v[172:175],v[128:131]\n"
"s_waitcnt lgkmcnt(0)\n"
"ds_read_b128 v[24:27],v0 offset:57344\n"
"ds_read_b128 v[28:31],v1 offset:57344\n"
MF "v[128:131],v[16:19],v[192:195],v[128:131]\n"
"v_exp_f32 v96,v96\n"
MF "v[128:131],v[20:23],v[212:215],v[128:131]\n"
"v_exp_f32 v97,v97\n"
MF "v[124:127],v[20:23],v[208:211],v[124:127]\n"
"v_exp_f32 v98,v98\n"
MF "v[124:127],v[16:19],v[188:191],v[124:127]\n"
"v_exp_f32 v99,v99\n"
MF "v[120:123],v[16:19],v[184:187],v[120:123]\n"
"v_add_f32 v58,v53,v96\n"
"v_add_f32 v58,v58,v97\n"
MF "v[120:123],v[20:23],v[204:207],v[120:123]\n"
CV "v96,v96,v97\n"
"v_add_f32 v58,v58,v98\n"
MF "v[116:119],v[20:23],v[200:203],v[116:119]\n"
"s_waitcnt vmcnt(6) lgkmcnt(0)\n"
"v_add_f32 v58,v58,v99\n"
CV "v97,v98,v99\n"
MF "v[116:119],v[16:19],v[180:183],v[116:119]\n"
"s_barrier\n"
MF "v[112:115],v[16:19],v[176:179],v[112:115]\n"
"v_exp_f32 v100,v100\n"
MF "v[112:115],v[20:23],v[196:199],v[112:115]\n"
"s_nop 0\n"
TR "v[16:17],v2\n"
TR "v[18:19],v2 offset:6144\n"
MF "v[112:115],v[24:27],v[216:219],v[112:115]\n"
"s_add_u32 s73,s73,12288\n"
TR "v[20:21],v3\n"
TR "v[22:23],v3 offset:6144\n"
MF "v[112:115],v[28:31],v[236:239],v[112:115]\n"
"s_cmp_eq_u32 s73,s19\n"
"s_cselect_b32 s73,s18,s73\n"
"s_add_u32 m0,s65,49152\n"
MF "v[116:119],v[28:31],v[240:243],v[116:119]\n"
LD "v8,s[36:39],s73 offen nt lds\n"
MF "v[116:119],v[24:27],v[220:223],v[116:119]\n"
"s_add_u32 m0,s65,50048\n"
"v_exp_f32 v101,v101\n"
MF "v[120:123],v[24:27],v[224:227],v[120:123]\n"
LD "v8,s[36:39],s73 offen offset:128 nt lds\n"
MF "v[120:123],v[28:31],v[244:247],v[120:123]\n"
"v_exp_f32 v102,v102\n"
MF "v[124:127],v[28:31],v[248:251],v[124:127]\n"
"s_add_u32 m0,s65,50944\n"
"v_exp_f32 v103,v103\n"
MF "v[124:127],v[24:27],v[228:231],v[124:127]\n"
LD "v8,s[36:39],s73 offen offset:256 nt lds\n"
MF "v[128:131],v[24:27],v[232:235],v[128:131]\n"
"v_add_f32 v59,v54,v100\n"
"v_add_f32 v59,v59,v101\n"
MF "v[128:131],v[28:31],v[252:255],v[128:131]\n"
"s_waitcnt lgkmcnt(0)\n"
TR "v[24:25],v4\n"
TR "v[26:27],v4 offset:6144\n"
MF "a[0:3],v[16:19],v[72:75],a[0:3]\n"
"s_add_u32 m0,s65,49152\n"
TR "v[28:29],v5\n"
TR "v[30:31],v5 offset:6144\n"
MF "a[20:23],v[20:23],v[72:75],a[20:23]\n"
LD "v8,s[36:39],s73 offen offset:3072 nt lds\n"
MF "a[24:27],v[20:23],v[76:79],a[24:27]\n"
CV "v100,v100,v101\n"
"v_add_f32 v59,v59,v102\n"
MF "a[4:7],v[16:19],v[76:79],a[4:7]\n"
"s_add_u32 m0,s65,50048\n"
"v_add_f32 v59,v59,v103\n"
CV "v101,v102,v103\n"
MF "a[8:11],v[16:19],v[80:83],a[8:11]\n"
LD "v8,s[36:39],s73 offen offset:3200 nt lds\n"
MF "a[28:31],v[20:23],v[80:83],a[28:31]\n"
"v_exp_f32 v104,v104\n"
MF "a[32:35],v[20:23],v[84:87],a[32:35]\n"
"s_add_u32 m0,s65,50944\n"
"v_exp_f32 v105,v105\n"
MF "a[12:15],v[16:19],v[84:87],a[12:15]\n"
LD "v8,s[36:39],s73 offen offset:3328 nt lds\n"
MF "a[16:19],v[16:19],v[88:91],a[16:19]\n"
"v_exp_f32 v106,v106\n"
MF "a[36:39],v[20:23],v[88:91],a[36:39]\n"
"s_waitcnt lgkmcnt(0)\n"
TR "v[16:17],v2 offset:1024\n"
TR "v[18:19],v2 offset:7168\n"
MF "a[56:59],v[24:27],v[88:91],a[56:59]\n"
"s_nop 0\n"
TR "v[20:21],v3 offset:1024\n"
TR "v[22:23],v3 offset:7168\n"
MF "a[76:79],v[28:31],v[88:91],a[76:79]\n"
"v_exp_f32 v107,v107\n"
MF "a[72:75],v[28:31],v[84:87],a[72:75]\n"
"v_add_f32 v60,v55,v104\n"
"v_add_f32 v60,v60,v105\n"
MF "a[52:55],v[24:27],v[84:87],a[52:55]\n"
CV "v104,v104,v105\n"
"v_add_f32 v60,v60,v106\n"
MF "a[48:51],v[24:27],v[80:83],a[48:51]\n"
"v_add_f32 v60,v60,v107\n"
CV "v105,v106,v107\n"
MF "a[68:71],v[28:31],v[80:83],a[68:71]\n"
"v_exp_f32 v108,v108\n"
MF "a[64:67],v[28:31],v[76:79],a[64:67]\n"
"v_exp_f32 v109,v109\n"
MF "a[44:47],v[24:27],v[76:79],a[44:47]\n"
"v_exp_f32 v110,v110\n"
MF "a[40:43],v[24:27],v[72:75],a[40:43]\n"
"v_exp_f32 v111,v111\n"
MF "a[60:63],v[28:31],v[72:75],a[60:63]\n"
"s_waitcnt lgkmcnt(0)\n"
TR "v[24:25],v4 offset:1024\n"
TR "v[26:27],v4 offset:7168\n"
MF "a[80:83],v[16:19],v[72:75],a[80:83]\n"
"s_nop 0\n"
TR "v[28:29],v5 offset:1024\n"
TR "v[30:31],v5 offset:7168\n"
MF "a[100:103],v[20:23],v[72:75],a[100:103]\n"
"v_add_f32 v61,v56,v108\n"
"v_add_f32 v61,v61,v109\n"
MF "a[104:107],v[20:23],v[76:79],a[104:107]\n"
CV "v108,v108,v109\n"
"v_add_f32 v61,v61,v110\n"
MF "a[84:87],v[16:19],v[76:79],a[84:87]\n"
"v_add_f32 v61,v61,v111\n"
CV "v109,v110,v111\n"
MF "a[88:91],v[16:19],v[80:83],a[88:91]\n"
"v_exp_f32 v112,v112\n"
MF "a[108:111],v[20:23],v[80:83],a[108:111]\n"
"v_exp_f32 v113,v113\n"
MF "a[112:115],v[20:23],v[84:87],a[112:115]\n"
"v_exp_f32 v114,v114\n"
MF "a[92:95],v[16:19],v[84:87],a[92:95]\n"
"v_exp_f32 v115,v115\n"
MF "a[96:99],v[16:19],v[88:91],a[96:99]\n"
"v_add_f32 v57,v57,v112\n"
"v_add_f32 v57,v57,v113\n"
MF "a[116:119],v[20:23],v[88:91],a[116:119]\n"
"s_waitcnt lgkmcnt(0)\n"
TR "v[16:17],v2 offset:2048\n"
TR "v[18:19],v2 offset:8192\n"
MF "a[136:139],v[24:27],v[88:91],a[136:139]\n"
"s_nop 0\n"
TR "v[20:21],v3 offset:2048\n"
TR "v[22:23],v3 offset:8192\n"
MF "a[156:159],v[28:31],v[88:91],a[156:159]\n"
CV "v94,v112,v113\n"
"v_add_f32 v57,v57,v114\n"
MF "a[152:155],v[28:31],v[84:87],a[152:155]\n"
"v_add_f32 v57,v57,v115\n"
CV "v95,v114,v115\n"
MF "a[132:135],v[24:27],v[84:87],a[132:135]\n"
"v_exp_f32 v116,v116\n"
MF "a[128:131],v[24:27],v[80:83],a[128:131]\n"
"v_exp_f32 v117,v117\n"
MF "a[148:151],v[28:31],v[80:83],a[148:151]\n"
"v_exp_f32 v118,v118\n"
MF "a[144:147],v[28:31],v[76:79],a[144:147]\n"
"v_exp_f32 v119,v119\n"
MF "a[124:127],v[24:27],v[76:79],a[124:127]\n"
"v_add_f32 v58,v58,v116\n"
"v_add_f32 v58,v58,v117\n"
MF "a[120:123],v[24:27],v[72:75],a[120:123]\n"
CV "v98,v116,v117\n"
"v_add_f32 v58,v58,v118\n"
MF "a[140:143],v[28:31],v[72:75],a[140:143]\n"
"s_waitcnt lgkmcnt(0)\n"
TR "v[24:25],v4 offset:2048\n"
TR "v[26:27],v4 offset:8192\n"
MF "a[160:163],v[16:19],v[72:75],a[160:163]\n"
"s_nop 0\n"
TR "v[28:29],v5 offset:2048\n"
TR "v[30:31],v5 offset:8192\n"
MF "a[180:183],v[20:23],v[72:75],a[180:183]\n"
"v_add_f32 v58,v58,v119\n"
CV "v99,v118,v119\n"
MF "a[184:187],v[20:23],v[76:79],a[184:187]\n"
"v_exp_f32 v120,v120\n"
MF "a[164:167],v[16:19],v[76:79],a[164:167]\n"
"v_exp_f32 v121,v121\n"
MF "a[168:171],v[16:19],v[80:83],a[168:171]\n"
"v_exp_f32 v122,v122\n"
MF "a[188:191],v[20:23],v[80:83],a[188:191]\n"
"v_exp_f32 v123,v123\n"
MF "a[192:195],v[20:23],v[84:87],a[192:195]\n"
"v_add_f32 v59,v59,v120\n"
"v_add_f32 v59,v59,v121\n"
MF "a[172:175],v[16:19],v[84:87],a[172:175]\n"
"s_waitcnt vmcnt(6) lgkmcnt(0)\n"
CV "v102,v120,v121\n"
"v_add_f32 v59,v59,v122\n"
MF "a[176:179],v[16:19],v[88:91],a[176:179]\n"
"s_barrier\n"
MF "a[196:199],v[20:23],v[88:91],a[196:199]\n"
"s_add_u32 s72,s72,6144\n"
"ds_read_b128 v[16:19],v0 offset:24576\n"
"ds_read_b128 v[20:23],v1 offset:24576\n"
MF "a[216:219],v[24:27],v[88:91],a[216:219]\n"
"s_cmp_eq_u32 s72,s17\n"
"s_cselect_b32 s72,s16,s72\n"
"s_add_u32 m0,s64,0\n"
MF "a[236:239],v[28:31],v[88:91],a[236:239]\n"
LD "v7,s[32:35],s72 offen nt lds\n"
MF "a[232:235],v[28:31],v[84:87],a[232:235]\n"
"s_add_u32 m0,s64,896\n"
"v_add_f32 v59,v59,v123\n"
CV "v103,v122,v123\n"
MF "a[212:215],v[24:27],v[84:87],a[212:215]\n"
LD "v7,s[32:35],s72 offen offset:128 nt lds\n"
MF "a[208:211],v[24:27],v[80:83],a[208:211]\n"
"s_add_u32 m0,s64,1792\n"
MF "a[228:231],v[28:31],v[80:83],a[228:231]\n"
LD "v7,s[32:35],s72 offen offset:256 nt lds\n"
MF "a[224:227],v[28:31],v[76:79],a[224:227]\n"
MF "a[204:207],v[24:27],v[76:79],a[204:207]\n"
MF "a[200:203],v[24:27],v[72:75],a[200:203]\n"
MF "a[220:223],v[28:31],v[72:75],a[220:223]\n"
"s_waitcnt lgkmcnt(0)\n"
MF "v[72:75],v[16:19],v[136:139],v[32:35]\n"
"s_add_u32 s72,s72,6144\n"
"ds_read_b128 v[24:27],v0 offset:25600\n"
"ds_read_b128 v[28:31],v1 offset:25600\n"
MF "v[72:75],v[20:23],v[156:159],v[72:75]\n"
"s_add_u32 m0,s64,6144\n"
"v_exp_f32 v124,v124\n"
MF "v[76:79],v[20:23],v[160:163],v[36:39]\n"
LD "v7,s[32:35],s72 offen nt lds\n"
MF "v[76:79],v[16:19],v[140:143],v[76:79]\n"
"v_exp_f32 v125,v125\n"
MF "v[80:83],v[16:19],v[144:147],v[40:43]\n"
"s_add_u32 m0,s64,7040\n"
"v_exp_f32 v126,v126\n"
MF "v[80:83],v[20:23],v[164:167],v[80:83]\n"
LD "v7,s[32:35],s72 offen offset:128 nt lds\n"
MF "v[84:87],v[20:23],v[168:171],v[44:47]\n"
"v_exp_f32 v127,v127\n"
MF "v[84:87],v[16:19],v[148:151],v[84:87]\n"
"s_add_u32 m0,s64,7936\n"
"v_add_f32 v60,v60,v124\n"
"v_add_f32 v60,v60,v125\n"
MF "v[88:91],v[16:19],v[152:155],v[48:51]\n"
LD "v7,s[32:35],s72 offen offset:256 nt lds\n"
MF "v[88:91],v[20:23],v[172:175],v[88:91]\n"
"s_waitcnt lgkmcnt(0)\n"
"ds_read_b128 v[16:19],v0 offset:26624\n"
"ds_read_b128 v[20:23],v1 offset:26624\n"
MF "v[88:91],v[24:27],v[192:195],v[88:91]\n"
CV "v106,v124,v125\n"
"v_add_f32 v60,v60,v126\n"
MF "v[88:91],v[28:31],v[212:215],v[88:91]\n"
"v_add_f32 v60,v60,v127\n"
CV "v107,v126,v127\n"
MF "v[84:87],v[28:31],v[208:211],v[84:87]\n"
"v_exp_f32 v128,v128\n"
MF "v[84:87],v[24:27],v[188:191],v[84:87]\n"
"v_exp_f32 v129,v129\n"
MF "v[80:83],v[24:27],v[184:187],v[80:83]\n"
"v_exp_f32 v130,v130\n"
MF "v[80:83],v[28:31],v[204:207],v[80:83]\n"
"v_exp_f32 v131,v131\n"
MF "v[76:79],v[28:31],v[200:203],v[76:79]\n"
"v_add_f32 v61,v61,v128\n"
"v_add_f32 v61,v61,v129\n"
MF "v[76:79],v[24:27],v[180:183],v[76:79]\n"
CV "v110,v128,v129\n"
"v_add_f32 v61,v61,v130\n"
MF "v[72:75],v[24:27],v[176:179],v[72:75]\n"
"v_add_f32 v61,v61,v131\n"
CV "v111,v130,v131\n"
MF "v[72:75],v[28:31],v[196:199],v[72:75]\n"
"s_waitcnt lgkmcnt(0)\n"
"ds_read_b128 v[24:27],v0 offset:30720\n"
"ds_read_b128 v[28:31],v1 offset:30720\n"
MF "v[72:75],v[16:19],v[216:219],v[72:75]\n"
MF "v[72:75],v[20:23],v[236:239],v[72:75]\n"
MF "v[76:79],v[20:23],v[240:243],v[76:79]\n"
MF "v[76:79],v[16:19],v[220:223],v[76:79]\n"
MF "v[80:83],v[16:19],v[224:227],v[80:83]\n"
MF "v[80:83],v[20:23],v[244:247],v[80:83]\n"
MF "v[84:87],v[20:23],v[248:251],v[84:87]\n"
MF "v[84:87],v[16:19],v[228:231],v[84:87]\n"
"v_max3_f32 v10,v57,v58,v59\n"
"v_max3_f32 v10,v10,v60,v61\n"
MF "v[88:91],v[16:19],v[232:235],v[88:91]\n"
"v_cmp_ngt_f32 vcc,s77,v10\n"
MF "v[88:91],v[20:23],v[252:255],v[88:91]\n"
"s_waitcnt lgkmcnt(0)\n"
"s_cbranch_vccnz .Lstub_22\n"
".Lback_22:\n"
MF "v[112:115],v[24:27],v[136:139],v[32:35]\n"
"s_add_u32 s74,s74,12288\n"
"ds_read_b128 v[16:19],v0 offset:31744\n"
"ds_read_b128 v[20:23],v1 offset:31744\n"
MF "v[112:115],v[28:31],v[156:159],v[112:115]\n"
"s_cmp_eq_u32 s74,s20\n"
"s_cselect_b32 s74,s70,s74\n"
MF "v[116:119],v[28:31],v[160:163],v[36:39]\n"
"v_exp_f32 v72,v72\n"
MF "v[116:119],v[24:27],v[140:143],v[116:119]\n"
"v_exp_f32 v73,v73\n"
MF "v[120:123],v[24:27],v[144:147],v[40:43]\n"
"v_exp_f32 v74,v74\n"
MF "v[120:123],v[28:31],v[164:167],v[120:123]\n"
"v_exp_f32 v75,v75\n"
MF "v[124:127],v[28:31],v[168:171],v[44:47]\n"
"v_add_f32 v52,v57,v72\n"
"v_add_f32 v52,v52,v73\n"
MF "v[124:127],v[24:27],v[148:151],v[124:127]\n"
CV "v72,v72,v73\n"
"v_add_f32 v52,v52,v74\n"
MF "v[128:131],v[24:27],v[152:155],v[48:51]\n"
"v_add_f32 v52,v52,v75\n"
CV "v73,v74,v75\n"
MF "v[128:131],v[28:31],v[172:175],v[128:131]\n"
"s_waitcnt lgkmcnt(0)\n"
"ds_read_b128 v[24:27],v0 offset:32768\n"
"ds_read_b128 v[28:31],v1 offset:32768\n"
MF "v[128:131],v[16:19],v[192:195],v[128:131]\n"
"v_exp_f32 v76,v76\n"
MF "v[128:131],v[20:23],v[212:215],v[128:131]\n"
"v_exp_f32 v77,v77\n"
MF "v[124:127],v[20:23],v[208:211],v[124:127]\n"
"v_exp_f32 v78,v78\n"
MF "v[124:127],v[16:19],v[188:191],v[124:127]\n"
"v_exp_f32 v79,v79\n"
MF "v[120:123],v[16:19],v[184:187],v[120:123]\n"
"v_add_f32 v53,v58,v76\n"
"v_add_f32 v53,v53,v77\n"
MF "v[120:123],v[20:23],v[204:207],v[120:123]\n"
CV "v76,v76,v77\n"
"v_add_f32 v53,v53,v78\n"
MF "v[116:119],v[20:23],v[200:203],v[116:119]\n"
"s_waitcnt vmcnt(6) lgkmcnt(0)\n"
"v_add_f32 v53,v53,v79\n"
CV "v77,v78,v79\n"
MF "v[116:119],v[16:19],v[180:183],v[116:119]\n"
"s_barrier\n"
MF "v[112:115],v[16:19],v[176:179],v[112:115]\n"
"v_exp_f32 v80,v80\n"
MF "v[112:115],v[20:23],v[196:199],v[112:115]\n"
"s_nop 0\n"
TR "v[16:17],v2 offset:49152\n"
TR "v[18:19],v2 offset:55296\n"
MF "v[112:115],v[24:27],v[216:219],v[112:115]\n"
"s_add_u32 s73,s73,12288\n"
TR "v[20:21],v3 offset:49152\n"
TR "v[22:23],v3 offset:55296\n"
MF "v[112:115],v[28:31],v[236:239],v[112:115]\n"
"s_cmp_eq_u32 s73,s19\n"
"s_cselect_b32 s73,s18,s73\n"
"s_add_u32 m0,s65,24576\n"
MF "v[116:119],v[28:31],v[240:243],v[116:119]\n"
LD "v8,s[36:39],s73 offen nt lds\n"
MF "v[116:119],v[24:27],v[220:223],v[116:119]\n"
"s_add_u32 m0,s65,25472\n"
"v_exp_f32 v81,v81\n"
MF "v[120:123],v[24:27],v[224:227],v[120:123]\n"
LD "v8,s[36:39],s73 offen offset:128 nt lds\n"
MF "v[120:123],v[28:31],v[244:247],v[120:123]\n"
"v_exp_f32 v82,v82\n"
MF "v[124:127],v[28:31],v[248:251],v[124:127]\n"
"s_add_u32 m0,s65,26368\n"
"v_exp_f32 v83,v83\n"
MF "v[124:127],v[24:27],v[228:231],v[124:127]\n"
LD "v8,s[36:39],s73 offen offset:256 nt lds\n"
MF "v[128:131],v[24:27],v[232:235],v[128:131]\n"
"v_add_f32 v54,v59,v80\n"
"v_add_f32 v54,v54,v81\n"
MF "v[128:131],v[28:31],v[252:255],v[128:131]\n"
"s_waitcnt lgkmcnt(0)\n"
TR "v[24:25],v4 offset:49152\n"
TR "v[26:27],v4 offset:55296\n"
MF "a[0:3],v[16:19],v[92:95],a[0:3]\n"
"s_add_u32 m0,s65,24576\n"
TR "v[28:29],v5 offset:49152\n"
TR "v[30:31],v5 offset:55296\n"
MF "a[20:23],v[20:23],v[92:95],a[20:23]\n"
LD "v8,s[36:39],s73 offen offset:3072 nt lds\n"
MF "a[24:27],v[20:23],v[96:99],a[24:27]\n"
CV "v80,v80,v81\n"
"v_add_f32 v54,v54,v82\n"
MF "a[4:7],v[16:19],v[96:99],a[4:7]\n"
"s_add_u32 m0,s65,25472\n"
"v_add_f32 v54,v54,v83\n"
CV "v81,v82,v83\n"
MF "a[8:11],v[16:19],v[100:103],a[8:11]\n"
LD "v8,s[36:39],s73 offen offset:3200 nt lds\n"
MF "a[28:31],v[20:23],v[100:103],a[28:31]\n"
"v_exp_f32 v84,v84\n"
MF "a[32:35],v[20:23],v[104:107],a[32:35]\n"
"s_add_u32 m0,s65,26368\n"
"v_exp_f32 v85,v85\n"
MF "a[12:15],v[16:19],v[104:107],a[12:15]\n"
LD "v8,s[36:39],s73 offen offset:3328 nt lds\n"
MF "a[16:19],v[16:19],v[108:111],a[16:19]\n"
"v_exp_f32 v86,v86\n"
MF "a[36:39],v[20:23],v[108:111],a[36:39]\n"
"s_waitcnt lgkmcnt(0)\n"
TR "v[16:17],v2 offset:50176\n"
TR "v[18:19],v2 offset:56320\n"
MF "a[56:59],v[24:27],v[108:111],a[56:59]\n"
"s_nop 0\n"
TR "v[20:21],v3 offset:50176\n"
TR "v[22:23],v3 offset:56320\n"
MF "a[76:79],v[28:31],v[108:111],a[76:79]\n"
"v_exp_f32 v87,v87\n"
MF "a[72:75],v[28:31],v[104:107],a[72:75]\n"
"v_add_f32 v55,v60,v84\n"
"v_add_f32 v55,v55,v85\n"
MF "a[52:55],v[24:27],v[104:107],a[52:55]\n"
CV "v84,v84,v85\n"
"v_add_f32 v55,v55,v86\n"
MF "a[48:51],v[24:27],v[100:103],a[48:51]\n"
"v_add_f32 v55,v55,v87\n"
CV "v85,v86,v87\n"
MF "a[68:71],v[28:31],v[100:103],a[68:71]\n"
"v_exp_f32 v88,v88\n"
MF "a[64:67],v[28:31],v[96:99],a[64:67]\n"
"v_exp_f32 v89,v89\n"
MF "a[44:47],v[24:27],v[96:99],a[44:47]\n"
"v_exp_f32 v90,v90\n"
MF "a[40:43],v[24:27],v[92:95],a[40:43]\n"
"v_exp_f32 v91,v91\n"
MF "a[60:63],v[28:31],v[92:95],a[60:63]\n"
"s_waitcnt lgkmcnt(0)\n"
TR "v[24:25],v4 offset:50176\n"
TR "v[26:27],v4 offset:56320\n"
MF "a[80:83],v[16:19],v[92:95],a[80:83]\n"
"s_nop 0\n"
TR "v[28:29],v5 offset:50176\n"
TR "v[30:31],v5 offset:56320\n"
MF "a[100:103],v[20:23],v[92:95],a[100:103]\n"
"v_add_f32 v56,v61,v88\n"
"v_add_f32 v56,v56,v89\n"
MF "a[104:107],v[20:23],v[96:99],a[104:107]\n"
CV "v88,v88,v89\n"
"v_add_f32 v56,v56,v90\n"
MF "a[84:87],v[16:19],v[96:99],a[84:87]\n"
"v_add_f32 v56,v56,v91\n"
CV "v89,v90,v91\n"
MF "a[88:91],v[16:19],v[100:103],a[88:91]\n"
"v_exp_f32 v112,v112\n"
MF "a[108:111],v[20:23],v[100:103],a[108:111]\n"
"v_exp_f32 v113,v113\n"
MF "a[112:115],v[20:23],v[104:107],a[112:115]\n"
"v_exp_f32 v114,v114\n"
MF "a[92:95],v[16:19],v[104:107],a[92:95]\n"
"v_exp_f32 v115,v115\n"
MF "a[96:99],v[16:19],v[108:111],a[96:99]\n"
"v_add_f32 v52,v52,v112\n"
"v_add_f32 v52,v52,v113\n"
MF "a[116:119],v[20:23],v[108:111],a[116:119]\n"
"s_waitcnt lgkmcnt(0)\n"
TR "v[16:17],v2 offset:51200\n"
TR "v[18:19],v2 offset:57344\n"
MF "a[136:139],v[24:27],v[108:111],a[136:139]\n"
"s_nop 0\n"
TR "v[20:21],v3 offset:51200\n"
TR "v[22:23],v3 offset:57344\n"
MF "a[156:159],v[28:31],v[108:111],a[156:159]\n"
CV "v74,v112,v113\n"
"v_add_f32 v52,v52,v114\n"
MF "a[152:155],v[28:31],v[104:107],a[152:155]\n"
"v_add_f32 v52,v52,v115\n"
CV "v75,v114,v115\n"
MF "a[132:135],v[24:27],v[104:107],a[132:135]\n"
"v_exp_f32 v116,v116\n"
MF "a[128:131],v[24:27],v[100:103],a[128:131]\n"
"v_exp_f32 v117,v117\n"
MF "a[148:151],v[28:31],v[100:103],a[148:151]\n"
"v_exp_f32 v118,v118\n"
MF "a[144:147],v[28:31],v[96:99],a[144:147]\n"
"v_exp_f32 v119,v119\n"
MF "a[124:127],v[24:27],v[96:99],a[124:127]\n"
"v_add_f32 v53,v53,v116\n"
"v_add_f32 v53,v53,v117\n"
MF "a[120:123],v[24:27],v[92:95],a[120:123]\n"
CV "v78,v116,v117\n"
"v_add_f32 v53,v53,v118\n"
MF "a[140:143],v[28:31],v[92:95],a[140:143]\n"
"s_waitcnt lgkmcnt(0)\n"
TR "v[24:25],v4 offset:51200\n"
TR "v[26:27],v4 offset:57344\n"
MF "a[160:163],v[16:19],v[92:95],a[160:163]\n"
"s_nop 0\n"
TR "v[28:29],v5 offset:51200\n"
TR "v[30:31],v5 offset:57344\n"
MF "a[180:183],v[20:23],v[92:95],a[180:183]\n"
"v_add_f32 v53,v53,v119\n"
CV "v79,v118,v119\n"
MF "a[184:187],v[20:23],v[96:99],a[184:187]\n"
"v_exp_f32 v120,v120\n"
MF "a[164:167],v[16:19],v[96:99],a[164:167]\n"
"v_exp_f32 v121,v121\n"
MF "a[168:171],v[16:19],v[100:103],a[168:171]\n"
"v_exp_f32 v122,v122\n"
MF "a[188:191],v[20:23],v[100:103],a[188:191]\n"
"v_exp_f32 v123,v123\n"
MF "a[192:195],v[20:23],v[104:107],a[192:195]\n"
"v_add_f32 v54,v54,v120\n"
"v_add_f32 v54,v54,v121\n"
MF "a[172:175],v[16:19],v[104:107],a[172:175]\n"
"s_waitcnt vmcnt(6) lgkmcnt(0)\n"
CV "v82,v120,v121\n"
"v_add_f32 v54,v54,v122\n"
MF "a[176:179],v[16:19],v[108:111],a[176:179]\n"
"s_barrier\n"
MF "a[196:199],v[20:23],v[108:111],a[196:199]\n"
"s_add_u32 s72,s72,6144\n"
"ds_read_b128 v[16:19],v0\n"
"ds_read_b128 v[20:23],v1\n"
MF "a[216:219],v[24:27],v[108:111],a[216:219]\n"
"s_cmp_eq_u32 s72,s17\n"
"s_cselect_b32 s72,s16,s72\n"
"s_add_u32 m0,s64,49152\n"
MF "a[236:239],v[28:31],v[108:111],a[236:239]\n"
LD "v7,s[32:35],s72 offen nt lds\n"
MF "a[232:235],v[28:31],v[104:107],a[232:235]\n"
"s_add_u32 m0,s64,50048\n"
"v_add_f32 v54,v54,v123\n"
CV "v83,v122,v123\n"
MF "a[212:215],v[24:27],v[104:107],a[212:215]\n"
LD "v7,s[32:35],s72 offen offset:128 nt lds\n"
MF "a[208:211],v[24:27],v[100:103],a[208:211]\n"
"s_add_u32 m0,s64,50944\n"
MF "a[228:231],v[28:31],v[100:103],a[228:231]\n"
LD "v7,s[32:35],s72 offen offset:256 nt lds\n"
MF "a[224:227],v[28:31],v[96:99],a[224:227]\n"
MF "a[204:207],v[24:27],v[96:99],a[204:207]\n"
MF "a[200:203],v[24:27],v[92:95],a[200:203]\n"
MF "a[220:223],v[28:31],v[92:95],a[220:223]\n"
"s_waitcnt lgkmcnt(0)\n"
MF "v[92:95],v[16:19],v[136:139],v[32:35]\n"
"s_add_u32 s72,s72,6144\n"
"ds_read_b128 v[24:27],v0 offset:1024\n"
"ds_read_b128 v[28:31],v1 offset:1024\n"
MF "v[92:95],v[20:23],v[156:159],v[92:95]\n"
"s_add_u32 m0,s64,55296\n"
"v_exp_f32 v124,v124\n"
MF "v[96:99],v[20:23],v[160:163],v[36:39]\n"
LD "v7,s[32:35],s72 offen nt lds\n"
MF "v[96:99],v[16:19],v[140:143],v[96:99]\n"
"v_exp_f32 v125,v125\n"
MF "v[100:103],v[16:19],v[144:147],v[40:43]\n"
"s_add_u32 m0,s64,56192\n"
"v_exp_f32 v126,v126\n"
MF "v[100:103],v[20:23],v[164:167],v[100:103]\n"
LD "v7,s[32:35],s72 offen offset:128 nt lds\n"
MF "v[104:107],v[20:23],v[168:171],v[44:47]\n"
"v_exp_f32 v127,v127\n"
MF "v[104:107],v[16:19],v[148:151],v[104:107]\n"
"s_add_u32 m0,s64,57088\n"
"v_add_f32 v55,v55,v124\n"
"v_add_f32 v55,v55,v125\n"
MF "v[108:111],v[16:19],v[152:155],v[48:51]\n"
LD "v7,s[32:35],s72 offen offset:256 nt lds\n"
MF "v[108:111],v[20:23],v[172:175],v[108:111]\n"
"s_waitcnt lgkmcnt(0)\n"
"ds_read_b128 v[16:19],v0 offset:2048\n"
"ds_read_b128 v[20:23],v1 offset:2048\n"
MF "v[108:111],v[24:27],v[192:195],v[108:111]\n"
CV "v86,v124,v125\n"
"v_add_f32 v55,v55,v126\n"
MF "v[108:111],v[28:31],v[212:215],v[108:111]\n"
"v_add_f32 v55,v55,v127\n"
CV "v87,v126,v127\n"
MF "v[104:107],v[28:31],v[208:211],v[104:107]\n"
"v_exp_f32 v128,v128\n"
MF "v[104:107],v[24:27],v[188:191],v[104:107]\n"
"v_exp_f32 v129,v129\n"
MF "v[100:103],v[24:27],v[184:187],v[100:103]\n"
"v_exp_f32 v130,v130\n"
MF "v[100:103],v[28:31],v[204:207],v[100:103]\n"
"v_exp_f32 v131,v131\n"
MF "v[96:99],v[28:31],v[200:203],v[96:99]\n"
"v_add_f32 v56,v56,v128\n"
"v_add_f32 v56,v56,v129\n"
MF "v[96:99],v[24:27],v[180:183],v[96:99]\n"
CV "v90,v128,v129\n"
"v_add_f32 v56,v56,v130\n"
MF "v[92:95],v[24:27],v[176:179],v[92:95]\n"
"v_add_f32 v56,v56,v131\n"
CV "v91,v130,v131\n"
MF "v[92:95],v[28:31],v[196:199],v[92:95]\n"
"s_waitcnt lgkmcnt(0)\n"
"ds_read_b128 v[24:27],v0 offset:6144\n"
"ds_read_b128 v[28:31],v1 offset:6144\n"
MF "v[92:95],v[16:19],v[216:219],v[92:95]\n"
MF "v[92:95],v[20:23],v[236:239],v[92:95]\n"
MF "v[96:99],v[20:23],v[240:243],v[96:99]\n"
MF "v[96:99],v[16:19],v[220:223],v[96:99]\n"
MF "v[100:103],v[16:19],v[224:227],v[100:103]\n"
MF "v[100:103],v[20:23],v[244:247],v[100:103]\n"
MF "v[104:107],v[20:23],v[248:251],v[104:107]\n"
MF "v[104:107],v[16:19],v[228:231],v[104:107]\n"
"v_max3_f32 v10,v52,v53,v54\n"
"v_max3_f32 v10,v10,v55,v56\n"
MF "v[108:111],v[16:19],v[232:235],v[108:111]\n"
"v_cmp_ngt_f32 vcc,s77,v10\n"
MF "v[108:111],v[20:23],v[252:255],v[108:111]\n"
"s_waitcnt lgkmcnt(0)\n"
"s_cbranch_vccnz .Lstub_23\n"
".Lback_23:\n"
MF "v[112:115],v[24:27],v[136:139],v[32:35]\n"
"s_add_u32 s74,s74,12288\n"
"ds_read_b128 v[16:19],v0 offset:7168\n"
"ds_read_b128 v[20:23],v1 offset:7168\n"
MF "v[112:115],v[28:31],v[156:159],v[112:115]\n"
"s_cmp_eq_u32 s74,s20\n"
"s_cselect_b32 s74,s70,s74\n"
MF "v[116:119],v[28:31],v[160:163],v[36:39]\n"
"v_exp_f32 v92,v92\n"
MF "v[116:119],v[24:27],v[140:143],v[116:119]\n"
"v_exp_f32 v93,v93\n"
MF "v[120:123],v[24:27],v[144:147],v[40:43]\n"
"v_exp_f32 v94,v94\n"
MF "v[120:123],v[28:31],v[164:167],v[120:123]\n"
"v_exp_f32 v95,v95\n"
MF "v[124:127],v[28:31],v[168:171],v[44:47]\n"
"v_add_f32 v57,v52,v92\n"
"v_add_f32 v57,v57,v93\n"
MF "v[124:127],v[24:27],v[148:151],v[124:127]\n"
CV "v92,v92,v93\n"
"v_add_f32 v57,v57,v94\n"
MF "v[128:131],v[24:27],v[152:155],v[48:51]\n"
"v_add_f32 v57,v57,v95\n"
CV "v93,v94,v95\n"
MF "v[128:131],v[28:31],v[172:175],v[128:131]\n"
"s_waitcnt lgkmcnt(0)\n"
"ds_read_b128 v[24:27],v0 offset:8192\n"
"ds_read_b128 v[28:31],v1 offset:8192\n"
MF "v[128:131],v[16:19],v[192:195],v[128:131]\n"
"v_exp_f32 v96,v96\n"
MF "v[128:131],v[20:23],v[212:215],v[128:131]\n"
"v_exp_f32 v97,v97\n"
MF "v[124:127],v[20:23],v[208:211],v[124:127]\n"
"v_exp_f32 v98,v98\n"
MF "v[124:127],v[16:19],v[188:191],v[124:127]\n"
"v_exp_f32 v99,v99\n"
MF "v[120:123],v[16:19],v[184:187],v[120:123]\n"
"v_add_f32 v58,v53,v96\n"
"v_add_f32 v58,v58,v97\n"
MF "v[120:123],v[20:23],v[204:207],v[120:123]\n"
CV "v96,v96,v97\n"
"v_add_f32 v58,v58,v98\n"
MF "v[116:119],v[20:23],v[200:203],v[116:119]\n"
"s_waitcnt vmcnt(6) lgkmcnt(0)\n"
"v_add_f32 v58,v58,v99\n"
CV "v97,v98,v99\n"
MF "v[116:119],v[16:19],v[180:183],v[116:119]\n"
"s_barrier\n"
MF "v[112:115],v[16:19],v[176:179],v[112:115]\n"
"v_exp_f32 v100,v100\n"
MF "v[112:115],v[20:23],v[196:199],v[112:115]\n"
"s_nop 0\n"
TR "v[16:17],v2 offset:24576\n"
TR "v[18:19],v2 offset:30720\n"
MF "v[112:115],v[24:27],v[216:219],v[112:115]\n"
"s_add_u32 s73,s73,12288\n"
TR "v[20:21],v3 offset:24576\n"
TR "v[22:23],v3 offset:30720\n"
MF "v[112:115],v[28:31],v[236:239],v[112:115]\n"
"s_cmp_eq_u32 s73,s19\n"
"s_cselect_b32 s73,s18,s73\n"
"s_add_u32 m0,s65,0\n"
MF "v[116:119],v[28:31],v[240:243],v[116:119]\n"
LD "v8,s[36:39],s73 offen nt lds\n"
MF "v[116:119],v[24:27],v[220:223],v[116:119]\n"
"s_add_u32 m0,s65,896\n"
"v_exp_f32 v101,v101\n"
MF "v[120:123],v[24:27],v[224:227],v[120:123]\n"
LD "v8,s[36:39],s73 offen offset:128 nt lds\n"
MF "v[120:123],v[28:31],v[244:247],v[120:123]\n"
"v_exp_f32 v102,v102\n"
MF "v[124:127],v[28:31],v[248:251],v[124:127]\n"
"s_add_u32 m0,s65,1792\n"
"v_exp_f32 v103,v103\n"
MF "v[124:127],v[24:27],v[228:231],v[124:127]\n"
LD "v8,s[36:39],s73 offen offset:256 nt lds\n"
MF "v[128:131],v[24:27],v[232:235],v[128:131]\n"
"v_add_f32 v59,v54,v100\n"
"v_add_f32 v59,v59,v101\n"
MF "v[128:131],v[28:31],v[252:255],v[128:131]\n"
"s_waitcnt lgkmcnt(0)\n"
TR "v[24:25],v4 offset:24576\n"
TR "v[26:27],v4 offset:30720\n"
MF "a[0:3],v[16:19],v[72:75],a[0:3]\n"
"s_add_u32 m0,s65,0\n"
TR "v[28:29],v5 offset:24576\n"
TR "v[30:31],v5 offset:30720\n"
MF "a[20:23],v[20:23],v[72:75],a[20:23]\n"
LD "v8,s[36:39],s73 offen offset:3072 nt lds\n"
MF "a[24:27],v[20:23],v[76:79],a[24:27]\n"
CV "v100,v100,v101\n"
"v_add_f32 v59,v59,v102\n"
MF "a[4:7],v[16:19],v[76:79],a[4:7]\n"
"s_add_u32 m0,s65,896\n"
"v_add_f32 v59,v59,v103\n"
CV "v101,v102,v103\n"
MF "a[8:11],v[16:19],v[80:83],a[8:11]\n"
LD "v8,s[36:39],s73 offen offset:3200 nt lds\n"
MF "a[28:31],v[20:23],v[80:83],a[28:31]\n"
"v_exp_f32 v104,v104\n"
MF "a[32:35],v[20:23],v[84:87],a[32:35]\n"
"s_add_u32 m0,s65,1792\n"
"v_exp_f32 v105,v105\n"
MF "a[12:15],v[16:19],v[84:87],a[12:15]\n"
LD "v8,s[36:39],s73 offen offset:3328 nt lds\n"
MF "a[16:19],v[16:19],v[88:91],a[16:19]\n"
"v_exp_f32 v106,v106\n"
MF "a[36:39],v[20:23],v[88:91],a[36:39]\n"
"s_waitcnt lgkmcnt(0)\n"
TR "v[16:17],v2 offset:25600\n"
TR "v[18:19],v2 offset:31744\n"
MF "a[56:59],v[24:27],v[88:91],a[56:59]\n"
"s_nop 0\n"
TR "v[20:21],v3 offset:25600\n"
TR "v[22:23],v3 offset:31744\n"
MF "a[76:79],v[28:31],v[88:91],a[76:79]\n"
"v_exp_f32 v107,v107\n"
MF "a[72:75],v[28:31],v[84:87],a[72:75]\n"
"v_add_f32 v60,v55,v104\n"
"v_add_f32 v60,v60,v105\n"
MF "a[52:55],v[24:27],v[84:87],a[52:55]\n"
CV "v104,v104,v105\n"
"v_add_f32 v60,v60,v106\n"
MF "a[48:51],v[24:27],v[80:83],a[48:51]\n"
"v_add_f32 v60,v60,v107\n"
CV "v105,v106,v107\n"
MF "a[68:71],v[28:31],v[80:83],a[68:71]\n"
"v_exp_f32 v108,v108\n"
MF "a[64:67],v[28:31],v[76:79],a[64:67]\n"
"v_exp_f32 v109,v109\n"
MF "a[44:47],v[24:27],v[76:79],a[44:47]\n"
"v_exp_f32 v110,v110\n"
MF "a[40:43],v[24:27],v[72:75],a[40:43]\n"
"v_exp_f32 v111,v111\n"
MF "a[60:63],v[28:31],v[72:75],a[60:63]\n"
"s_waitcnt lgkmcnt(0)\n"
TR "v[24:25],v4 offset:25600\n"
TR "v[26:27],v4 offset:31744\n"
MF "a[80:83],v[16:19],v[72:75],a[80:83]\n"
"s_nop 0\n"
TR "v[28:29],v5 offset:25600\n"
TR "v[30:31],v5 offset:31744\n"
MF "a[100:103],v[20:23],v[72:75],a[100:103]\n"
"v_add_f32 v61,v56,v108\n"
"v_add_f32 v61,v61,v109\n"
MF "a[104:107],v[20:23],v[76:79],a[104:107]\n"
CV "v108,v108,v109\n"
"v_add_f32 v61,v61,v110\n"
MF "a[84:87],v[16:19],v[76:79],a[84:87]\n"
"v_add_f32 v61,v61,v111\n"
CV "v109,v110,v111\n"
MF "a[88:91],v[16:19],v[80:83],a[88:91]\n"
"v_exp_f32 v112,v112\n"
MF "a[108:111],v[20:23],v[80:83],a[108:111]\n"
"v_exp_f32 v113,v113\n"
MF "a[112:115],v[20:23],v[84:87],a[112:115]\n"
"v_exp_f32 v114,v114\n"
MF "a[92:95],v[16:19],v[84:87],a[92:95]\n"
"v_exp_f32 v115,v115\n"
MF "a[96:99],v[16:19],v[88:91],a[96:99]\n"
"v_add_f32 v57,v57,v112\n"
"v_add_f32 v57,v57,v113\n"
MF "a[116:119],v[20:23],v[88:91],a[116:119]\n"
"s_waitcnt lgkmcnt(0)\n"
TR "v[16:17],v2 offset:26624\n"
TR "v[18:19],v2 offset:32768\n"
MF "a[136:139],v[24:27],v[88:91],a[136:139]\n"
"s_nop 0\n"
TR "v[20:21],v3 offset:26624\n"
TR "v[22:23],v3 offset:32768\n"
MF "a[156:159],v[28:31],v[88:91],a[156:159]\n"
CV "v94,v112,v113\n"
"v_add_f32 v57,v57,v114\n"
MF "a[152:155],v[28:31],v[84:87],a[152:155]\n"
"v_add_f32 v57,v57,v115\n"
CV "v95,v114,v115\n"
MF "a[132:135],v[24:27],v[84:87],a[132:135]\n"
"v_exp_f32 v116,v116\n"
MF "a[128:131],v[24:27],v[80:83],a[128:131]\n"
"v_exp_f32 v117,v117\n"
MF "a[148:151],v[28:31],v[80:83],a[148:151]\n"
"v_exp_f32 v118,v118\n"
MF "a[144:147],v[28:31],v[76:79],a[144:147]\n"
"v_exp_f32 v119,v119\n"
MF "a[124:127],v[24:27],v[76:79],a[124:127]\n"
"v_add_f32 v58,v58,v116\n"
"v_add_f32 v58,v58,v117\n"
MF "a[120:123],v[24:27],v[72:75],a[120:123]\n"
CV "v98,v116,v117\n"
"v_add_f32 v58,v58,v118\n"
MF "a[140:143],v[28:31],v[72:75],a[140:143]\n"
"s_waitcnt lgkmcnt(0)\n"
TR "v[24:25],v4 offset:26624\n"
TR "v[26:27],v4 offset:32768\n"
MF "a[160:163],v[16:19],v[72:75],a[160:163]\n"
"s_nop 0\n"
TR "v[28:29],v5 offset:26624\n"
TR "v[30:31],v5 offset:32768\n"
MF "a[180:183],v[20:23],v[72:75],a[180:183]\n"
"v_add_f32 v58,v58,v119\n"
CV "v99,v118,v119\n"
MF "a[184:187],v[20:23],v[76:79],a[184:187]\n"
"v_exp_f32 v120,v120\n"
MF "a[164:167],v[16:19],v[76:79],a[164:167]\n"
"v_exp_f32 v121,v121\n"
MF "a[168:171],v[16:19],v[80:83],a[168:171]\n"
"v_exp_f32 v122,v122\n"
MF "a[188:191],v[20:23],v[80:83],a[188:191]\n"
"v_exp_f32 v123,v123\n"
MF "a[192:195],v[20:23],v[84:87],a[192:195]\n"
"v_add_f32 v59,v59,v120\n"
"v_add_f32 v59,v59,v121\n"
MF "a[172:175],v[16:19],v[84:87],a[172:175]\n"
"s_waitcnt vmcnt(6) lgkmcnt(0)\n"
CV "v102,v120,v121\n"
"v_add_f32 v59,v59,v122\n"
MF "a[176:179],v[16:19],v[88:91],a[176:179]\n"
"s_barrier\n"
MF "a[196:199],v[20:23],v[88:91],a[196:199]\n"
"s_add_u32 s72,s72,6144\n"
"ds_read_b128 v[16:19],v0 offset:49152\n"
"ds_read_b128 v[20:23],v1 offset:49152\n"
MF "a[216:219],v[24:27],v[88:91],a[216:219]\n"
"s_cmp_eq_u32 s72,s17\n"
"s_cselect_b32 s72,s16,s72\n"
"s_add_u32 m0,s64,24576\n"
MF "a[236:239],v[28:31],v[88:91],a[236:239]\n"
LD "v7,s[32:35],s72 offen nt lds\n"
MF "a[232:235],v[28:31],v[84:87],a[232:235]\n"
"s_add_u32 m0,s64,25472\n"
"v_add_f32 v59,v59,v123\n"
CV "v103,v122,v123\n"
MF "a[212:215],v[24:27],v[84:87],a[212:215]\n"
LD "v7,s[32:35],s72 offen offset:128 nt lds\n"
MF "a[208:211],v[24:27],v[80:83],a[208:211]\n"
"s_add_u32 m0,s64,26368\n"
MF "a[228:231],v[28:31],v[80:83],a[228:231]\n"
LD "v7,s[32:35],s72 offen offset:256 nt lds\n"
MF "a[224:227],v[28:31],v[76:79],a[224:227]\n"
MF "a[204:207],v[24:27],v[76:79],a[204:207]\n"
MF "a[200:203],v[24:27],v[72:75],a[200:203]\n"
MF "a[220:223],v[28:31],v[72:75],a[220:223]\n"
"s_waitcnt lgkmcnt(0)\n"
MF "v[72:75],v[16:19],v[136:139],v[32:35]\n"
"s_add_u32 s72,s72,6144\n"
"ds_read_b128 v[24:27],v0 offset:50176\n"
"ds_read_b128 v[28:31],v1 offset:50176\n"
MF "v[72:75],v[20:23],v[156:159],v[72:75]\n"
"s_add_u32 m0,s64,30720\n"
"v_exp_f32 v124,v124\n"
MF "v[76:79],v[20:23],v[160:163],v[36:39]\n"
LD "v7,s[32:35],s72 offen nt lds\n"
MF "v[76:79],v[16:19],v[140:143],v[76:79]\n"
"v_exp_f32 v125,v125\n"
MF "v[80:83],v[16:19],v[144:147],v[40:43]\n"
"s_add_u32 m0,s64,31616\n"
"v_exp_f32 v126,v126\n"
MF "v[80:83],v[20:23],v[164:167],v[80:83]\n"
LD "v7,s[32:35],s72 offen offset:128 nt lds\n"
MF "v[84:87],v[20:23],v[168:171],v[44:47]\n"
"v_exp_f32 v127,v127\n"
MF "v[84:87],v[16:19],v[148:151],v[84:87]\n"
"s_add_u32 m0,s64,32512\n"
"v_add_f32 v60,v60,v124\n"
"v_add_f32 v60,v60,v125\n"
MF "v[88:91],v[16:19],v[152:155],v[48:51]\n"
LD "v7,s[32:35],s72 offen offset:256 nt lds\n"
MF "v[88:91],v[20:23],v[172:175],v[88:91]\n"
"s_waitcnt lgkmcnt(0)\n"
"ds_read_b128 v[16:19],v0 offset:51200\n"
"ds_read_b128 v[20:23],v1 offset:51200\n"
MF "v[88:91],v[24:27],v[192:195],v[88:91]\n"
CV "v106,v124,v125\n"
"v_add_f32 v60,v60,v126\n"
MF "v[88:91],v[28:31],v[212:215],v[88:91]\n"
"v_add_f32 v60,v60,v127\n"
CV "v107,v126,v127\n"
MF "v[84:87],v[28:31],v[208:211],v[84:87]\n"
"v_exp_f32 v128,v128\n"
MF "v[84:87],v[24:27],v[188:191],v[84:87]\n"
"v_exp_f32 v129,v129\n"
MF "v[80:83],v[24:27],v[184:187],v[80:83]\n"
"v_exp_f32 v130,v130\n"
MF "v[80:83],v[28:31],v[204:207],v[80:83]\n"
"v_exp_f32 v131,v131\n"
MF "v[76:79],v[28:31],v[200:203],v[76:79]\n"
"v_add_f32 v61,v61,v128\n"
"v_add_f32 v61,v61,v129\n"
MF "v[76:79],v[24:27],v[180:183],v[76:79]\n"
CV "v110,v128,v129\n"
"v_add_f32 v61,v61,v130\n"
MF "v[72:75],v[24:27],v[176:179],v[72:75]\n"
"v_add_f32 v61,v61,v131\n"
CV "v111,v130,v131\n"
MF "v[72:75],v[28:31],v[196:199],v[72:75]\n"
"s_waitcnt lgkmcnt(0)\n"
"ds_read_b128 v[24:27],v0 offset:55296\n"
"ds_read_b128 v[28:31],v1 offset:55296\n"
MF "v[72:75],v[16:19],v[216:219],v[72:75]\n"
MF "v[72:75],v[20:23],v[236:239],v[72:75]\n"
MF "v[76:79],v[20:23],v[240:243],v[76:79]\n"
MF "v[76:79],v[16:19],v[220:223],v[76:79]\n"
MF "v[80:83],v[16:19],v[224:227],v[80:83]\n"
MF "v[80:83],v[20:23],v[244:247],v[80:83]\n"
MF "v[84:87],v[20:23],v[248:251],v[84:87]\n"
MF "v[84:87],v[16:19],v[228:231],v[84:87]\n"
"v_max3_f32 v10,v57,v58,v59\n"
"v_max3_f32 v10,v10,v60,v61\n"
MF "v[88:91],v[16:19],v[232:235],v[88:91]\n"
"v_cmp_ngt_f32 vcc,s95,v10\n"
MF "v[88:91],v[20:23],v[252:255],v[88:91]\n"
"s_waitcnt lgkmcnt(0)\n"
"s_cbranch_vccnz .Lstub_24\n"
".Lback_24:\n"
MF "v[112:115],v[24:27],v[136:139],v[32:35]\n"
"s_add_u32 s74,s74,12288\n"
"ds_read_b128 v[16:19],v0 offset:56320\n"
"ds_read_b128 v[20:23],v1 offset:56320\n"
MF "v[112:115],v[28:31],v[156:159],v[112:115]\n"
"s_cmp_eq_u32 s74,s20\n"
"s_cselect_b32 s74,s70,s74\n"
MF "v[116:119],v[28:31],v[160:163],v[36:39]\n"
"v_exp_f32 v72,v72\n"
MF "v[116:119],v[24:27],v[140:143],v[116:119]\n"
"v_exp_f32 v73,v73\n"
MF "v[120:123],v[24:27],v[144:147],v[40:43]\n"
"v_exp_f32 v74,v74\n"
MF "v[120:123],v[28:31],v[164:167],v[120:123]\n"
"v_exp_f32 v75,v75\n"
MF "v[124:127],v[28:31],v[168:171],v[44:47]\n"
"v_add_f32 v52,v57,v72\n"
"v_add_f32 v52,v52,v73\n"
MF "v[124:127],v[24:27],v[148:151],v[124:127]\n"
CV "v72,v72,v73\n"
"v_add_f32 v52,v52,v74\n"
MF "v[128:131],v[24:27],v[152:155],v[48:51]\n"
"v_add_f32 v52,v52,v75\n"
CV "v73,v74,v75\n"
MF "v[128:131],v[28:31],v[172:175],v[128:131]\n"
"s_waitcnt lgkmcnt(0)\n"
"ds_read_b128 v[24:27],v0 offset:57344\n"
"ds_read_b128 v[28:31],v1 offset:57344\n"
MF "v[128:131],v[16:19],v[192:195],v[128:131]\n"
"v_exp_f32 v76,v76\n"
MF "v[128:131],v[20:23],v[212:215],v[128:131]\n"
"v_exp_f32 v77,v77\n"
MF "v[124:127],v[20:23],v[208:211],v[124:127]\n"
"v_exp_f32 v78,v78\n"
MF "v[124:127],v[16:19],v[188:191],v[124:127]\n"
"v_exp_f32 v79,v79\n"
MF "v[120:123],v[16:19],v[184:187],v[120:123]\n"
"v_add_f32 v53,v58,v76\n"
"v_add_f32 v53,v53,v77\n"
MF "v[120:123],v[20:23],v[204:207],v[120:123]\n"
CV "v76,v76,v77\n"
"v_add_f32 v53,v53,v78\n"
MF "v[116:119],v[20:23],v[200:203],v[116:119]\n"
"s_waitcnt vmcnt(6) lgkmcnt(0)\n"
"v_add_f32 v53,v53,v79\n"
CV "v77,v78,v79\n"
MF "v[116:119],v[16:19],v[180:183],v[116:119]\n"
"s_barrier\n"
MF "v[112:115],v[16:19],v[176:179],v[112:115]\n"
"v_exp_f32 v80,v80\n"
MF "v[112:115],v[20:23],v[196:199],v[112:115]\n"
"s_nop 0\n"
TR "v[16:17],v2\n"
TR "v[18:19],v2 offset:6144\n"
MF "v[112:115],v[24:27],v[216:219],v[112:115]\n"
"s_add_u32 s73,s73,12288\n"
TR "v[20:21],v3\n"
TR "v[22:23],v3 offset:6144\n"
MF "v[112:115],v[28:31],v[236:239],v[112:115]\n"
"s_cmp_eq_u32 s73,s19\n"
"s_cselect_b32 s73,s18,s73\n"
"s_add_u32 m0,s65,49152\n"
MF "v[116:119],v[28:31],v[240:243],v[116:119]\n"
LD "v8,s[36:39],s73 offen nt lds\n"
MF "v[116:119],v[24:27],v[220:223],v[116:119]\n"
"s_add_u32 m0,s65,50048\n"
"v_exp_f32 v81,v81\n"
MF "v[120:123],v[24:27],v[224:227],v[120:123]\n"
LD "v8,s[36:39],s73 offen offset:128 nt lds\n"
MF "v[120:123],v[28:31],v[244:247],v[120:123]\n"
"v_exp_f32 v82,v82\n"
MF "v[124:127],v[28:31],v[248:251],v[124:127]\n"
"s_add_u32 m0,s65,50944\n"
"v_exp_f32 v83,v83\n"
MF "v[124:127],v[24:27],v[228:231],v[124:127]\n"
LD "v8,s[36:39],s73 offen offset:256 nt lds\n"
MF "v[128:131],v[24:27],v[232:235],v[128:131]\n"
"v_add_f32 v54,v59,v80\n"
"v_add_f32 v54,v54,v81\n"
MF "v[128:131],v[28:31],v[252:255],v[128:131]\n"
"s_waitcnt lgkmcnt(0)\n"
TR "v[24:25],v4\n"
TR "v[26:27],v4 offset:6144\n"
MF "a[0:3],v[16:19],v[92:95],a[0:3]\n"
"s_add_u32 m0,s65,49152\n"
TR "v[28:29],v5\n"
TR "v[30:31],v5 offset:6144\n"
MF "a[20:23],v[20:23],v[92:95],a[20:23]\n"
LD "v8,s[36:39],s73 offen offset:3072 nt lds\n"
MF "a[24:27],v[20:23],v[96:99],a[24:27]\n"
CV "v80,v80,v81\n"
"v_add_f32 v54,v54,v82\n"
MF "a[4:7],v[16:19],v[96:99],a[4:7]\n"
"s_add_u32 m0,s65,50048\n"
"v_add_f32 v54,v54,v83\n"
CV "v81,v82,v83\n"
MF "a[8:11],v[16:19],v[100:103],a[8:11]\n"
LD "v8,s[36:39],s73 offen offset:3200 nt lds\n"
MF "a[28:31],v[20:23],v[100:103],a[28:31]\n"
"v_exp_f32 v84,v84\n"
MF "a[32:35],v[20:23],v[104:107],a[32:35]\n"
"s_add_u32 m0,s65,50944\n"
"v_exp_f32 v85,v85\n"
MF "a[12:15],v[16:19],v[104:107],a[12:15]\n"
LD "v8,s[36:39],s73 offen offset:3328 nt lds\n"
MF "a[16:19],v[16:19],v[108:111],a[16:19]\n"
"v_exp_f32 v86,v86\n"
MF "a[36:39],v[20:23],v[108:111],a[36:39]\n"
"s_waitcnt lgkmcnt(0)\n"
TR "v[16:17],v2 offset:1024\n"
TR "v[18:19],v2 offset:7168\n"
MF "a[56:59],v[24:27],v[108:111],a[56:59]\n"
"s_nop 0\n"
TR "v[20:21],v3 offset:1024\n"
TR "v[22:23],v3 offset:7168\n"
MF "a[76:79],v[28:31],v[108:111],a[76:79]\n"
"v_exp_f32 v87,v87\n"
MF "a[72:75],v[28:31],v[104:107],a[72:75]\n"
"v_add_f32 v55,v60,v84\n"
"v_add_f32 v55,v55,v85\n"
MF "a[52:55],v[24:27],v[104:107],a[52:55]\n"
CV "v84,v84,v85\n"
"v_add_f32 v55,v55,v86\n"
MF "a[48:51],v[24:27],v[100:103],a[48:51]\n"
"v_add_f32 v55,v55,v87\n"
CV "v85,v86,v87\n"
MF "a[68:71],v[28:31],v[100:103],a[68:71]\n"
"v_exp_f32 v88,v88\n"
MF "a[64:67],v[28:31],v[96:99],a[64:67]\n"
"v_exp_f32 v89,v89\n"
MF "a[44:47],v[24:27],v[96:99],a[44:47]\n"
"v_exp_f32 v90,v90\n"
MF "a[40:43],v[24:27],v[92:95],a[40:43]\n"
"v_exp_f32 v91,v91\n"
MF "a[60:63],v[28:31],v[92:95],a[60:63]\n"
"s_waitcnt lgkmcnt(0)\n"
TR "v[24:25],v4 offset:1024\n"
TR "v[26:27],v4 offset:7168\n"
MF "a[80:83],v[16:19],v[92:95],a[80:83]\n"
"s_nop 0\n"
TR "v[28:29],v5 offset:1024\n"
TR "v[30:31],v5 offset:7168\n"
MF "a[100:103],v[20:23],v[92:95],a[100:103]\n"
"v_add_f32 v56,v61,v88\n"
"v_add_f32 v56,v56,v89\n"
MF "a[104:107],v[20:23],v[96:99],a[104:107]\n"
CV "v88,v88,v89\n"
"v_add_f32 v56,v56,v90\n"
MF "a[84:87],v[16:19],v[96:99],a[84:87]\n"
"v_add_f32 v56,v56,v91\n"
CV "v89,v90,v91\n"
MF "a[88:91],v[16:19],v[100:103],a[88:91]\n"
"v_exp_f32 v112,v112\n"
MF "a[108:111],v[20:23],v[100:103],a[108:111]\n"
"v_exp_f32 v113,v113\n"
MF "a[112:115],v[20:23],v[104:107],a[112:115]\n"
"v_exp_f32 v114,v114\n"
MF "a[92:95],v[16:19],v[104:107],a[92:95]\n"
"v_exp_f32 v115,v115\n"
MF "a[96:99],v[16:19],v[108:111],a[96:99]\n"
"v_add_f32 v52,v52,v112\n"
"v_add_f32 v52,v52,v113\n"
MF "a[116:119],v[20:23],v[108:111],a[116:119]\n"
"s_waitcnt lgkmcnt(0)\n"
TR "v[16:17],v2 offset:2048\n"
TR "v[18:19],v2 offset:8192\n"
MF "a[136:139],v[24:27],v[108:111],a[136:139]\n"
"s_nop 0\n"
TR "v[20:21],v3 offset:2048\n"
TR "v[22:23],v3 offset:8192\n"
MF "a[156:159],v[28:31],v[108:111],a[156:159]\n"
CV "v74,v112,v113\n"
"v_add_f32 v52,v52,v114\n"
MF "a[152:155],v[28:31],v[104:107],a[152:155]\n"
"v_add_f32 v52,v52,v115\n"
CV "v75,v114,v115\n"
MF "a[132:135],v[24:27],v[104:107],a[132:135]\n"
"v_exp_f32 v116,v116\n"
MF "a[128:131],v[24:27],v[100:103],a[128:131]\n"
"v_exp_f32 v117,v117\n"
MF "a[148:151],v[28:31],v[100:103],a[148:151]\n"
"v_exp_f32 v118,v118\n"
MF "a[144:147],v[28:31],v[96:99],a[144:147]\n"
"v_exp_f32 v119,v119\n"
MF "a[124:127],v[24:27],v[96:99],a[124:127]\n"
"v_add_f32 v53,v53,v116\n"
"v_add_f32 v53,v53,v117\n"
MF "a[120:123],v[24:27],v[92:95],a[120:123]\n"
CV "v78,v116,v117\n"
"v_add_f32 v53,v53,v118\n"
MF "a[140:143],v[28:31],v[92:95],a[140:143]\n"
"s_waitcnt lgkmcnt(0)\n"
TR "v[24:25],v4 offset:2048\n"
TR "v[26:27],v4 offset:8192\n"
MF "a[160:163],v[16:19],v[92:95],a[160:163]\n"
"s_nop 0\n"
TR "v[28:29],v5 offset:2048\n"
TR "v[30:31],v5 offset:8192\n"
MF "a[180:183],v[20:23],v[92:95],a[180:183]\n"
"v_add_f32 v53,v53,v119\n"
CV "v79,v118,v119\n"
MF "a[184:187],v[20:23],v[96:99],a[184:187]\n"
"v_exp_f32 v120,v120\n"
MF "a[164:167],v[16:19],v[96:99],a[164:167]\n"
"v_exp_f32 v121,v121\n"
MF "a[168:171],v[16:19],v[100:103],a[168:171]\n"
"v_exp_f32 v122,v122\n"
MF "a[188:191],v[20:23],v[100:103],a[188:191]\n"
"v_exp_f32 v123,v123\n"
MF "a[192:195],v[20:23],v[104:107],a[192:195]\n"
"v_add_f32 v54,v54,v120\n"
"v_add_f32 v54,v54,v121\n"
MF "a[172:175],v[16:19],v[104:107],a[172:175]\n"
"s_waitcnt vmcnt(6) lgkmcnt(0)\n"
CV "v82,v120,v121\n"
"v_add_f32 v54,v54,v122\n"
MF "a[176:179],v[16:19],v[108:111],a[176:179]\n"
"s_barrier\n"
MF "a[196:199],v[20:23],v[108:111],a[196:199]\n"
"s_add_u32 s72,s72,6144\n"
"ds_read_b128 v[16:19],v0 offset:24576\n"
"ds_read_b128 v[20:23],v1 offset:24576\n"
MF "a[216:219],v[24:27],v[108:111],a[216:219]\n"
"s_cmp_eq_u32 s72,s17\n"
"s_cselect_b32 s72,s16,s72\n"
"s_add_u32 m0,s64,0\n"
MF "a[236:239],v[28:31],v[108:111],a[236:239]\n"
LD "v7,s[32:35],s72 offen nt lds\n"
MF "a[232:235],v[28:31],v[104:107],a[232:235]\n"
"s_add_u32 m0,s64,896\n"
"v_add_f32 v54,v54,v123\n"
CV "v83,v122,v123\n"
MF "a[212:215],v[24:27],v[104:107],a[212:215]\n"
LD "v7,s[32:35],s72 offen offset:128 nt lds\n"
MF "a[208:211],v[24:27],v[100:103],a[208:211]\n"
"s_add_u32 m0,s64,1792\n"
MF "a[228:231],v[28:31],v[100:103],a[228:231]\n"
LD "v7,s[32:35],s72 offen offset:256 nt lds\n"
MF "a[224:227],v[28:31],v[96:99],a[224:227]\n"
MF "a[204:207],v[24:27],v[96:99],a[204:207]\n"
MF "a[200:203],v[24:27],v[92:95],a[200:203]\n"
MF "a[220:223],v[28:31],v[92:95],a[220:223]\n"
"s_waitcnt lgkmcnt(0)\n"
MF "v[92:95],v[16:19],v[136:139],v[32:35]\n"
"s_add_u32 s72,s72,6144\n"
"ds_read_b128 v[24:27],v0 offset:25600\n"
"ds_read_b128 v[28:31],v1 offset:25600\n"
MF "v[92:95],v[20:23],v[156:159],v[92:95]\n"
"s_add_u32 m0,s64,6144\n"
"v_exp_f32 v124,v124\n"
MF "v[96:99],v[20:23],v[160:163],v[36:39]\n"
LD "v7,s[32:35],s72 offen nt lds\n"
MF "v[96:99],v[16:19],v[140:143],v[96:99]\n"
"v_exp_f32 v125,v125\n"
MF "v[100:103],v[16:19],v[144:147],v[40:43]\n"
"s_add_u32 m0,s64,7040\n"
"v_exp_f32 v126,v126\n"
MF "v[100:103],v[20:23],v[164:167],v[100:103]\n"
LD "v7,s[32:35],s72 offen offset:128 nt lds\n"
MF "v[104:107],v[20:23],v[168:171],v[44:47]\n"
"v_exp_f32 v127,v127\n"
MF "v[104:107],v[16:19],v[148:151],v[104:107]\n"
"s_add_u32 m0,s64,7936\n"
"v_add_f32 v55,v55,v124\n"
"v_add_f32 v55,v55,v125\n"
MF "v[108:111],v[16:19],v[152:155],v[48:51]\n"
LD "v7,s[32:35],s72 offen offset:256 nt lds\n"
MF "v[108:111],v[20:23],v[172:175],v[108:111]\n"
"s_waitcnt lgkmcnt(0)\n"
"ds_read_b128 v[16:19],v0 offset:26624\n"
"ds_read_b128 v[20:23],v1 offset:26624\n"
MF "v[108:111],v[24:27],v[192:195],v[108:111]\n"
CV "v86,v124,v125\n"
"v_add_f32 v55,v55,v126\n"
MF "v[108:111],v[28:31],v[212:215],v[108:111]\n"
"v_add_f32 v55,v55,v127\n"
CV "v87,v126,v127\n"
MF "v[104:107],v[28:31],v[208:211],v[104:107]\n"
"v_exp_f32 v128,v128\n"
MF "v[104:107],v[24:27],v[188:191],v[104:107]\n"
"v_exp_f32 v129,v129\n"
MF "v[100:103],v[24:27],v[184:187],v[100:103]\n"
"v_exp_f32 v130,v130\n"
MF "v[100:103],v[28:31],v[204:207],v[100:103]\n"
"v_exp_f32 v131,v131\n"
MF "v[96:99],v[28:31],v[200:203],v[96:99]\n"
"v_add_f32 v56,v56,v128\n"
"v_add_f32 v56,v56,v129\n"
MF "v[96:99],v[24:27],v[180:183],v[96:99]\n"
CV "v90,v128,v129\n"
"v_add_f32 v56,v56,v130\n"
MF "v[92:95],v[24:27],v[176:179],v[92:95]\n"
"v_add_f32 v56,v56,v131\n"
CV "v91,v130,v131\n"
MF "v[92:95],v[28:31],v[196:199],v[92:95]\n"
"s_waitcnt lgkmcnt(0)\n"
"ds_read_b128 v[24:27],v0 offset:30720\n"
"ds_read_b128 v[28:31],v1 offset:30720\n"
MF "v[92:95],v[16:19],v[216:219],v[92:95]\n"
MF "v[92:95],v[20:23],v[236:239],v[92:95]\n"
MF "v[96:99],v[20:23],v[240:243],v[96:99]\n"
MF "v[96:99],v[16:19],v[220:223],v[96:99]\n"
MF "v[100:103],v[16:19],v[224:227],v[100:103]\n"
MF "v[100:103],v[20:23],v[244:247],v[100:103]\n"
MF "v[104:107],v[20:23],v[248:251],v[104:107]\n"
MF "v[104:107],v[16:19],v[228:231],v[104:107]\n"
"v_max3_f32 v10,v52,v53,v54\n"
"v_max3_f32 v10,v10,v55,v56\n"
MF "v[108:111],v[16:19],v[232:235],v[108:111]\n"
"v_cmp_ngt_f32 vcc,s77,v10\n"
MF "v[108:111],v[20:23],v[252:255],v[108:111]\n"
"s_waitcnt lgkmcnt(0)\n"
"s_cbranch_vccnz .Lstub_25\n"
".Lback_25:\n"
MF "v[112:115],v[24:27],v[136:139],v[32:35]\n"
"s_add_u32 s74,s74,12288\n"
"ds_read_b128 v[16:19],v0 offset:31744\n"
"ds_read_b128 v[20:23],v1 offset:31744\n"
MF "v[112:115],v[28:31],v[156:159],v[112:115]\n"
"s_cmp_eq_u32 s74,s20\n"
"s_cselect_b32 s74,s70,s74\n"
MF "v[116:119],v[28:31],v[160:163],v[36:39]\n"
"v_exp_f32 v92,v92\n"
MF "v[116:119],v[24:27],v[140:143],v[116:119]\n"
"v_exp_f32 v93,v93\n"
MF "v[120:123],v[24:27],v[144:147],v[40:43]\n"
"v_exp_f32 v94,v94\n"
MF "v[120:123],v[28:31],v[164:167],v[120:123]\n"
"v_exp_f32 v95,v95\n"
MF "v[124:127],v[28:31],v[168:171],v[44:47]\n"
"v_add_f32 v57,v52,v92\n"
"v_add_f32 v57,v57,v93\n"
MF "v[124:127],v[24:27],v[148:151],v[124:127]\n"
CV "v92,v92,v93\n"
"v_add_f32 v57,v57,v94\n"
MF "v[128:131],v[24:27],v[152:155],v[48:51]\n"
"v_add_f32 v57,v57,v95\n"
CV "v93,v94,v95\n"
MF "v[128:131],v[28:31],v[172:175],v[128:131]\n"
"s_waitcnt lgkmcnt(0)\n"
"ds_read_b128 v[24:27],v0 offset:32768\n"
"ds_read_b128 v[28:31],v1 offset:32768\n"
MF "v[128:131],v[16:19],v[192:195],v[128:131]\n"
"s_sub_u32 s61,s61,1\n"
"v_exp_f32 v96,v96\n"
MF "v[128:131],v[20:23],v[212:215],v[128:131]\n"
"s_mov_b32 s95,0\n"
"v_exp_f32 v97,v97\n"
MF "v[124:127],v[20:23],v[208:211],v[124:127]\n"
"v_exp_f32 v98,v98\n"
MF "v[124:127],v[16:19],v[188:191],v[124:127]\n"
"v_exp_f32 v99,v99\n"
MF "v[120:123],v[16:19],v[184:187],v[120:123]\n"
"v_add_f32 v58,v53,v96\n"
"v_add_f32 v58,v58,v97\n"
MF "v[120:123],v[20:23],v[204:207],v[120:123]\n"
CV "v96,v96,v97\n"
"v_add_f32 v58,v58,v98\n"
MF "v[116:119],v[20:23],v[200:203],v[116:119]\n"
"s_waitcnt vmcnt(6) lgkmcnt(0)\n"
"v_add_f32 v58,v58,v99\n"
CV "v97,v98,v99\n"
MF "v[116:119],v[16:19],v[180:183],v[116:119]\n"
"s_barrier\n"
MF "v[112:115],v[16:19],v[176:179],v[112:115]\n"
"v_exp_f32 v100,v100\n"
MF "v[112:115],v[20:23],v[196:199],v[112:115]\n"
"s_nop 0\n"
TR "v[16:17],v2 offset:49152\n"
TR "v[18:19],v2 offset:55296\n"
MF "v[112:115],v[24:27],v[216:219],v[112:115]\n"
"s_add_u32 s73,s73,12288\n"
TR "v[20:21],v3 offset:49152\n"
TR "v[22:23],v3 offset:55296\n"
MF "v[112:115],v[28:31],v[236:239],v[112:115]\n"
"s_cmp_eq_u32 s73,s19\n"
"s_cselect_b32 s73,s18,s73\n"
"s_add_u32 m0,s65,24576\n"
MF "v[116:119],v[28:31],v[240:243],v[116:119]\n"
LD "v8,s[36:39],s73 offen nt lds\n"
MF "v[116:119],v[24:27],v[220:223],v[116:119]\n"
"s_add_u32 m0,s65,25472\n"
"v_exp_f32 v101,v101\n"
MF "v[120:123],v[24:27],v[224:227],v[120:123]\n"
LD "v8,s[36:39],s73 offen offset:128 nt lds\n"
MF "v[120:123],v[28:31],v[244:247],v[120:123]\n"
"v_exp_f32 v102,v102\n"
MF "v[124:127],v[28:31],v[248:251],v[124:127]\n"
"s_add_u32 m0,s65,26368\n"
"v_exp_f32 v103,v103\n"
MF "v[124:127],v[24:27],v[228:231],v[124:127]\n"
LD "v8,s[36:39],s73 offen offset:256 nt lds\n"
MF "v[128:131],v[24:27],v[232:235],v[128:131]\n"
"v_add_f32 v59,v54,v100\n"
"v_add_f32 v59,v59,v101\n"
MF "v[128:131],v[28:31],v[252:255],v[128:131]\n"
"s_waitcnt lgkmcnt(0)\n"
TR "v[24:25],v4 offset:49152\n"
TR "v[26:27],v4 offset:55296\n"
MF "a[0:3],v[16:19],v[72:75],a[0:3]\n"
"s_add_u32 m0,s65,24576\n"
TR "v[28:29],v5 offset:49152\n"
TR "v[30:31],v5 offset:55296\n"
MF "a[20:23],v[20:23],v[72:75],a[20:23]\n"
LD "v8,s[36:39],s73 offen offset:3072 nt lds\n"
MF "a[24:27],v[20:23],v[76:79],a[24:27]\n"
CV "v100,v100,v101\n"
"v_add_f32 v59,v59,v102\n"
MF "a[4:7],v[16:19],v[76:79],a[4:7]\n"
"s_add_u32 m0,s65,25472\n"
"v_add_f32 v59,v59,v103\n"
CV "v101,v102,v103\n"
MF "a[8:11],v[16:19],v[80:83],a[8:11]\n"
LD "v8,s[36:39],s73 offen offset:3200 nt lds\n"
MF "a[28:31],v[20:23],v[80:83],a[28:31]\n"
"v_exp_f32 v104,v104\n"
MF "a[32:35],v[20:23],v[84:87],a[32:35]\n"
"s_add_u32 m0,s65,26368\n"
"v_exp_f32 v105,v105\n"
MF "a[12:15],v[16:19],v[84:87],a[12:15]\n"
LD "v8,s[36:39],s73 offen offset:3328 nt lds\n"
MF "a[16:19],v[16:19],v[88:91],a[16:19]\n"
"v_exp_f32 v106,v106\n"
MF "a[36:39],v[20:23],v[88:91],a[36:39]\n"
"s_waitcnt lgkmcnt(0)\n"
TR "v[16:17],v2 offset:50176\n"
TR "v[18:19],v2 offset:56320\n"
MF "a[56:59],v[24:27],v[88:91],a[56:59]\n"
"s_nop 0\n"
TR "v[20:21],v3 offset:50176\n"
TR "v[22:23],v3 offset:56320\n"
MF "a[76:79],v[28:31],v[88:91],a[76:79]\n"
"v_exp_f32 v107,v107\n"
MF "a[72:75],v[28:31],v[84:87],a[72:75]\n"
"v_add_f32 v60,v55,v104\n"
"v_add_f32 v60,v60,v105\n"
MF "a[52:55],v[24:27],v[84:87],a[52:55]\n"
CV "v104,v104,v105\n"
"v_add_f32 v60,v60,v106\n"
MF "a[48:51],v[24:27],v[80:83],a[48:51]\n"
"v_add_f32 v60,v60,v107\n"
CV "v105,v106,v107\n"
MF "a[68:71],v[28:31],v[80:83],a[68:71]\n"
"v_exp_f32 v108,v108\n"
MF "a[64:67],v[28:31],v[76:79],a[64:67]\n"
"v_exp_f32 v109,v109\n"
MF "a[44:47],v[24:27],v[76:79],a[44:47]\n"
"v_exp_f32 v110,v110\n"
MF "a[40:43],v[24:27],v[72:75],a[40:43]\n"
"v_exp_f32 v111,v111\n"
MF "a[60:63],v[28:31],v[72:75],a[60:63]\n"
"s_waitcnt lgkmcnt(0)\n"
TR "v[24:25],v4 offset:50176\n"
TR "v[26:27],v4 offset:56320\n"
MF "a[80:83],v[16:19],v[72:75],a[80:83]\n"
"s_nop 0\n"
TR "v[28:29],v5 offset:50176\n"
TR "v[30:31],v5 offset:56320\n"
MF "a[100:103],v[20:23],v[72:75],a[100:103]\n"
"v_add_f32 v61,v56,v108\n"
"v_add_f32 v61,v61,v109\n"
MF "a[104:107],v[20:23],v[76:79],a[104:107]\n"
CV "v108,v108,v109\n"
"v_add_f32 v61,v61,v110\n"
MF "a[84:87],v[16:19],v[76:79],a[84:87]\n"
"v_add_f32 v61,v61,v111\n"
CV "v109,v110,v111\n"
MF "a[88:91],v[16:19],v[80:83],a[88:91]\n"
"v_exp_f32 v112,v112\n"
MF "a[108:111],v[20:23],v[80:83],a[108:111]\n"
"v_exp_f32 v113,v113\n"
MF "a[112:115],v[20:23],v[84:87],a[112:115]\n"
"v_exp_f32 v114,v114\n"
MF "a[92:95],v[16:19],v[84:87],a[92:95]\n"
"v_exp_f32 v115,v115\n"
MF "a[96:99],v[16:19],v[88:91],a[96:99]\n"
"v_add_f32 v57,v57,v112\n"
"v_add_f32 v57,v57,v113\n"
MF "a[116:119],v[20:23],v[88:91],a[116:119]\n"
"s_waitcnt lgkmcnt(0)\n"
TR "v[16:17],v2 offset:51200\n"
TR "v[18:19],v2 offset:57344\n"
MF "a[136:139],v[24:27],v[88:91],a[136:139]\n"
"s_nop 0\n"
TR "v[20:21],v3 offset:51200\n"
TR "v[22:23],v3 offset:57344\n"
MF "a[156:159],v[28:31],v[88:91],a[156:159]\n"
CV "v94,v112,v113\n"
"v_add_f32 v57,v57,v114\n"
MF "a[152:155],v[28:31],v[84:87],a[152:155]\n"
"v_add_f32 v57,v57,v115\n"
CV "v95,v114,v115\n"
MF "a[132:135],v[24:27],v[84:87],a[132:135]\n"
"v_exp_f32 v116,v116\n"
MF "a[128:131],v[24:27],v[80:83],a[128:131]\n"
"v_exp_f32 v117,v117\n"
MF "a[148:151],v[28:31],v[80:83],a[148:151]\n"
"v_exp_f32 v118,v118\n"
MF "a[144:147],v[28:31],v[76:79],a[144:147]\n"
"v_exp_f32 v119,v119\n"
MF "a[124:127],v[24:27],v[76:79],a[124:127]\n"
"v_add_f32 v58,v58,v116\n"
"v_add_f32 v58,v58,v117\n"
MF "a[120:123],v[24:27],v[72:75],a[120:123]\n"
CV "v98,v116,v117\n"
"v_add_f32 v58,v58,v118\n"
MF "a[140:143],v[28:31],v[72:75],a[140:143]\n"
"s_waitcnt lgkmcnt(0)\n"
TR "v[24:25],v4 offset:51200\n"
TR "v[26:27],v4 offset:57344\n"
MF "a[160:163],v[16:19],v[72:75],a[160:163]\n"
"s_nop 0\n"
TR "v[28:29],v5 offset:51200\n"
TR "v[30:31],v5 offset:57344\n"
MF "a[180:183],v[20:23],v[72:75],a[180:183]\n"
"v_add_f32 v58,v58,v119\n"
CV "v99,v118,v119\n"
MF "a[184:187],v[20:23],v[76:79],a[184:187]\n"
"v_exp_f32 v120,v120\n"
MF "a[164:167],v[16:19],v[76:79],a[164:167]\n"
"v_exp_f32 v121,v121\n"
MF "a[168:171],v[16:19],v[80:83],a[168:171]\n"
"v_exp_f32 v122,v122\n"
MF "a[188:191],v[20:23],v[80:83],a[188:191]\n"
"v_exp_f32 v123,v123\n"
MF "a[192:195],v[20:23],v[84:87],a[192:195]\n"
"v_add_f32 v59,v59,v120\n"
"v_add_f32 v59,v59,v121\n"
MF "a[172:175],v[16:19],v[84:87],a[172:175]\n"
"s_waitcnt vmcnt(6) lgkmcnt(0)\n"
CV "v102,v120,v121\n"
"v_add_f32 v59,v59,v122\n"
MF "a[176:179],v[16:19],v[88:91],a[176:179]\n"
"s_barrier\n"
MF "a[196:199],v[20:23],v[88:91],a[196:199]\n"
"s_add_u32 s72,s72,6144\n"
"ds_read_b128 v[16:19],v0\n"
"ds_read_b128 v[20:23],v1\n"
MF "a[216:219],v[24:27],v[88:91],a[216:219]\n"
"s_cmp_eq_u32 s72,s17\n"
"s_cselect_b32 s72,s16,s72\n"
"s_add_u32 m0,s64,49152\n"
MF "a[236:239],v[28:31],v[88:91],a[236:239]\n"
LD "v7,s[32:35],s72 offen nt lds\n"
MF "a[232:235],v[28:31],v[84:87],a[232:235]\n"
"s_add_u32 m0,s64,50048\n"
"v_add_f32 v59,v59,v123\n"
CV "v103,v122,v123\n"
MF "a[212:215],v[24:27],v[84:87],a[212:215]\n"
LD "v7,s[32:35],s72 offen offset:128 nt lds\n"
MF "a[208:211],v[24:27],v[80:83],a[208:211]\n"
"s_add_u32 m0,s64,50944\n"
MF "a[228:231],v[28:31],v[80:83],a[228:231]\n"
LD "v7,s[32:35],s72 offen offset:256 nt lds\n"
MF "a[224:227],v[28:31],v[76:79],a[224:227]\n"
MF "a[204:207],v[24:27],v[76:79],a[204:207]\n"
"s_waitcnt lgkmcnt(0)\n"
MF "a[200:203],v[24:27],v[72:75],a[200:203]\n"
"s_cmp_lg_u32 s61,0\n"
MF "a[220:223],v[28:31],v[72:75],a[220:223]\n"
"s_cbranch_scc1 .Laloop\n"
".Lepi2:\n"
MF "v[72:75],v[16:19],v[136:139],v[32:35]\n"
"s_add_u32 s72,s72,6144\n"
"ds_read_b128 v[24:27],v0 offset:1024\n"
"ds_read_b128 v[28:31],v1 offset:1024\n"
MF "v[72:75],v[20:23],v[156:159],v[72:75]\n"
"s_add_u32 m0,s64,55296\n"
"v_exp_f32 v124,v124\n"
MF "v[76:79],v[20:23],v[160:163],v[36:39]\n"
LD "v7,s[32:35],s72 offen nt lds\n"
MF "v[76:79],v[16:19],v[140:143],v[76:79]\n"
"v_exp_f32 v125,v125\n"
MF "v[80:83],v[16:19],v[144:147],v[40:43]\n"
"s_add_u32 m0,s64,56192\n"
"v_exp_f32 v126,v126\n"
MF "v[80:83],v[20:23],v[164:167],v[80:83]\n"
LD "v7,s[32:35],s72 offen offset:128 nt lds\n"
MF "v[84:87],v[20:23],v[168:171],v[44:47]\n"
"v_exp_f32 v127,v127\n"
MF "v[84:87],v[16:19],v[148:151],v[84:87]\n"
"s_add_u32 m0,s64,57088\n"
"v_add_f32 v60,v60,v124\n"
"v_add_f32 v60,v60,v125\n"
MF "v[88:91],v[16:19],v[152:155],v[48:51]\n"
LD "v7,s[32:35],s72 offen offset:256 nt lds\n"
MF "v[88:91],v[20:23],v[172:175],v[88:91]\n"
"s_waitcnt lgkmcnt(0)\n"
"ds_read_b128 v[16:19],v0 offset:2048\n"
"ds_read_b128 v[20:23],v1 offset:2048\n"
MF "v[88:91],v[24:27],v[192:195],v[88:91]\n"
CV "v106,v124,v125\n"
"v_add_f32 v60,v60,v126\n"
MF "v[88:91],v[28:31],v[212:215],v[88:91]\n"
"v_add_f32 v60,v60,v127\n"
CV "v107,v126,v127\n"
MF "v[84:87],v[28:31],v[208:211],v[84:87]\n"
"v_exp_f32 v128,v128\n"
MF "v[84:87],v[24:27],v[188:191],v[84:87]\n"
"v_exp_f32 v129,v129\n"
MF "v[80:83],v[24:27],v[184:187],v[80:83]\n"
"v_exp_f32 v130,v130\n"
MF "v[80:83],v[28:31],v[204:207],v[80:83]\n"
"v_exp_f32 v131,v131\n"
MF "v[76:79],v[28:31],v[200:203],v[76:79]\n"
"v_add_f32 v61,v61,v128\n"
"v_add_f32 v61,v61,v129\n"
MF "v[76:79],v[24:27],v[180:183],v[76:79]\n"
CV "v110,v128,v129\n"
"v_add_f32 v61,v61,v130\n"
MF "v[72:75],v[24:27],v[176:179],v[72:75]\n"
"v_add_f32 v61,v61,v131\n"
CV "v111,v130,v131\n"
MF "v[72:75],v[28:31],v[196:199],v[72:75]\n"
"s_waitcnt lgkmcnt(0)\n"
"ds_read_b128 v[24:27],v0 offset:6144\n"
"ds_read_b128 v[28:31],v1 offset:6144\n"
MF "v[72:75],v[16:19],v[216:219],v[72:75]\n"
MF "v[72:75],v[20:23],v[236:239],v[72:75]\n"
MF "v[76:79],v[20:23],v[240:243],v[76:79]\n"
MF "v[76:79],v[16:19],v[220:223],v[76:79]\n"
MF "v[80:83],v[16:19],v[224:227],v[80:83]\n"
MF "v[80:83],v[20:23],v[244:247],v[80:83]\n"
MF "v[84:87],v[20:23],v[248:251],v[84:87]\n"
MF "v[84:87],v[16:19],v[228:231],v[84:87]\n"
"v_max3_f32 v10,v57,v58,v59\n"
"v_max3_f32 v10,v10,v60,v61\n"
MF "v[88:91],v[16:19],v[232:235],v[88:91]\n"
"v_cmp_le_f32 vcc,0x47800000,v10\n"
MF "v[88:91],v[20:23],v[252:255],v[88:91]\n"
"s_waitcnt lgkmcnt(0)\n"
"s_cbranch_vccnz .Lstub_26\n"
".Lback_26:\n"
MF "v[112:115],v[24:27],v[136:139],v[32:35]\n"
"s_add_u32 s74,s74,12288\n"
"ds_read_b128 v[16:19],v0 offset:7168\n"
"ds_read_b128 v[20:23],v1 offset:7168\n"
MF "v[112:115],v[28:31],v[156:159],v[112:115]\n"
"s_cmp_eq_u32 s74,s20\n"
"s_cselect_b32 s74,s70,s74\n"
MF "v[116:119],v[28:31],v[160:163],v[36:39]\n"
"v_exp_f32 v72,v72\n"
MF "v[116:119],v[24:27],v[140:143],v[116:119]\n"
"v_exp_f32 v73,v73\n"
MF "v[120:123],v[24:27],v[144:147],v[40:43]\n"
"v_exp_f32 v74,v74\n"
MF "v[120:123],v[28:31],v[164:167],v[120:123]\n"
"v_exp_f32 v75,v75\n"
MF "v[124:127],v[28:31],v[168:171],v[44:47]\n"
"v_add_f32 v52,v57,v72\n"
"v_add_f32 v52,v52,v73\n"
MF "v[124:127],v[24:27],v[148:151],v[124:127]\n"
CV "v72,v72,v73\n"
"v_add_f32 v52,v52,v74\n"
MF "v[128:131],v[24:27],v[152:155],v[48:51]\n"
"v_add_f32 v52,v52,v75\n"
CV "v73,v74,v75\n"
MF "v[128:131],v[28:31],v[172:175],v[128:131]\n"
"s_waitcnt lgkmcnt(0)\n"
"ds_read_b128 v[24:27],v0 offset:8192\n"
"ds_read_b128 v[28:31],v1 offset:8192\n"
MF "v[128:131],v[16:19],v[192:195],v[128:131]\n"
"v_exp_f32 v76,v76\n"
MF "v[128:131],v[20:23],v[212:215],v[128:131]\n"
"v_exp_f32 v77,v77\n"
MF "v[124:127],v[20:23],v[208:211],v[124:127]\n"
"v_exp_f32 v78,v78\n"
MF "v[124:127],v[16:19],v[188:191],v[124:127]\n"
"v_exp_f32 v79,v79\n"
MF "v[120:123],v[16:19],v[184:187],v[120:123]\n"
"v_add_f32 v53,v58,v76\n"
"v_add_f32 v53,v53,v77\n"
MF "v[120:123],v[20:23],v[204:207],v[120:123]\n"
CV "v76,v76,v77\n"
"v_add_f32 v53,v53,v78\n"
MF "v[116:119],v[20:23],v[200:203],v[116:119]\n"
"s_waitcnt vmcnt(6) lgkmcnt(0)\n"
"v_add_f32 v53,v53,v79\n"
CV "v77,v78,v79\n"
MF "v[116:119],v[16:19],v[180:183],v[116:119]\n"
"s_barrier\n"
MF "v[112:115],v[16:19],v[176:179],v[112:115]\n"
"v_lshrrev_b32 v7,7,v9\n"
"v_mad_u32_u24 v7,v7,s6,v8\n"
MF "v[112:115],v[20:23],v[196:199],v[112:115]\n"
"s_nop 0\n"
TR "v[16:17],v2 offset:24576\n"
TR "v[18:19],v2 offset:30720\n"
MF "v[112:115],v[24:27],v[216:219],v[112:115]\n"
"s_add_u32 s73,s73,12288\n"
TR "v[20:21],v3 offset:24576\n"
TR "v[22:23],v3 offset:30720\n"
MF "v[112:115],v[28:31],v[236:239],v[112:115]\n"
"s_cmp_eq_u32 s73,s19\n"
"s_cselect_b32 s73,s18,s73\n"
"s_add_u32 m0,s65,0\n"
MF "v[116:119],v[28:31],v[240:243],v[116:119]\n"
LD "v8,s[36:39],s73 offen nt lds\n"
MF "v[116:119],v[24:27],v[220:223],v[116:119]\n"
"s_add_u32 m0,s65,896\n"
"v_exp_f32 v80,v80\n"
MF "v[120:123],v[24:27],v[224:227],v[120:123]\n"
LD "v8,s[36:39],s73 offen offset:128 nt lds\n"
MF "v[120:123],v[28:31],v[244:247],v[120:123]\n"
"v_exp_f32 v81,v81\n"
MF "v[124:127],v[28:31],v[248:251],v[124:127]\n"
"s_add_u32 m0,s65,1792\n"
"v_exp_f32 v82,v82\n"
MF "v[124:127],v[24:27],v[228:231],v[124:127]\n"
LD "v8,s[36:39],s73 offen offset:256 nt lds\n"
MF "v[128:131],v[24:27],v[232:235],v[128:131]\n"
"v_exp_f32 v83,v83\n"
MF "v[128:131],v[28:31],v[252:255],v[128:131]\n"
"s_waitcnt lgkmcnt(0)\n"
TR "v[24:25],v4 offset:24576\n"
TR "v[26:27],v4 offset:30720\n"
MF "a[0:3],v[16:19],v[92:95],a[0:3]\n"
"s_add_u32 m0,s65,0\n"
TR "v[28:29],v5 offset:24576\n"
TR "v[30:31],v5 offset:30720\n"
MF "a[20:23],v[20:23],v[92:95],a[20:23]\n"
LD "v8,s[36:39],s73 offen offset:3072 nt lds\n"
MF "a[24:27],v[20:23],v[96:99],a[24:27]\n"
"v_add_f32 v54,v59,v80\n"
"v_add_f32 v54,v54,v81\n"
MF "a[4:7],v[16:19],v[96:99],a[4:7]\n"
"s_add_u32 m0,s65,896\n"
CV "v80,v80,v81\n"
"v_add_f32 v54,v54,v82\n"
MF "a[8:11],v[16:19],v[100:103],a[8:11]\n"
LD "v8,s[36:39],s73 offen offset:3200 nt lds\n"
MF "a[28:31],v[20:23],v[100:103],a[28:31]\n"
"v_add_f32 v54,v54,v83\n"
CV "v81,v82,v83\n"
MF "a[32:35],v[20:23],v[104:107],a[32:35]\n"
"s_add_u32 m0,s65,1792\n"
"v_exp_f32 v84,v84\n"
MF "a[12:15],v[16:19],v[104:107],a[12:15]\n"
LD "v8,s[36:39],s73 offen offset:3328 nt lds\n"
MF "a[16:19],v[16:19],v[108:111],a[16:19]\n"
"v_exp_f32 v85,v85\n"
MF "a[36:39],v[20:23],v[108:111],a[36:39]\n"
"s_waitcnt lgkmcnt(0)\n"
TR "v[16:17],v2 offset:25600\n"
TR "v[18:19],v2 offset:31744\n"
MF "a[56:59],v[24:27],v[108:111],a[56:59]\n"
"s_nop 0\n"
TR "v[20:21],v3 offset:25600\n"
TR "v[22:23],v3 offset:31744\n"
MF "a[76:79],v[28:31],v[108:111],a[76:79]\n"
"v_exp_f32 v86,v86\n"
MF "a[72:75],v[28:31],v[104:107],a[72:75]\n"
"v_exp_f32 v87,v87\n"
MF "a[52:55],v[24:27],v[104:107],a[52:55]\n"
"v_add_f32 v55,v60,v84\n"
"v_add_f32 v55,v55,v85\n"
MF "a[48:51],v[24:27],v[100:103],a[48:51]\n"
CV "v84,v84,v85\n"
"v_add_f32 v55,v55,v86\n"
MF "a[68:71],v[28:31],v[100:103],a[68:71]\n"
"v_add_f32 v55,v55,v87\n"
CV "v85,v86,v87\n"
MF "a[64:67],v[28:31],v[96:99],a[64:67]\n"
"v_exp_f32 v88,v88\n"
MF "a[44:47],v[24:27],v[96:99],a[44:47]\n"
"v_exp_f32 v89,v89\n"
MF "a[40:43],v[24:27],v[92:95],a[40:43]\n"
"v_exp_f32 v90,v90\n"
MF "a[60:63],v[28:31],v[92:95],a[60:63]\n"
"s_waitcnt lgkmcnt(0)\n"
TR "v[24:25],v4 offset:25600\n"
TR "v[26:27],v4 offset:31744\n"
MF "a[80:83],v[16:19],v[92:95],a[80:83]\n"
"s_nop 0\n"
TR "v[28:29],v5 offset:25600\n"
TR "v[30:31],v5 offset:31744\n"
MF "a[100:103],v[20:23],v[92:95],a[100:103]\n"
"v_exp_f32 v91,v91\n"
MF "a[104:107],v[20:23],v[96:99],a[104:107]\n"
"v_add_f32 v56,v61,v88\n"
"v_add_f32 v56,v56,v89\n"
MF "a[84:87],v[16:19],v[96:99],a[84:87]\n"
CV "v88,v88,v89\n"
"v_add_f32 v56,v56,v90\n"
MF "a[88:91],v[16:19],v[100:103],a[88:91]\n"
"v_add_f32 v56,v56,v91\n"
CV "v89,v90,v91\n"
MF "a[108:111],v[20:23],v[100:103],a[108:111]\n"
"v_exp_f32 v112,v112\n"
MF "a[112:115],v[20:23],v[104:107],a[112:115]\n"
"v_exp_f32 v113,v113\n"
MF "a[92:95],v[16:19],v[104:107],a[92:95]\n"
"v_exp_f32 v114,v114\n"
MF "a[96:99],v[16:19],v[108:111],a[96:99]\n"
"v_exp_f32 v115,v115\n"
MF "a[116:119],v[20:23],v[108:111],a[116:119]\n"
"s_waitcnt lgkmcnt(0)\n"
TR "v[16:17],v2 offset:26624\n"
TR "v[18:19],v2 offset:32768\n"
MF "a[136:139],v[24:27],v[108:111],a[136:139]\n"
"s_nop 0\n"
TR "v[20:21],v3 offset:26624\n"
TR "v[22:23],v3 offset:32768\n"
MF "a[156:159],v[28:31],v[108:111],a[156:159]\n"
"v_add_f32 v52,v52,v112\n"
"v_add_f32 v52,v52,v113\n"
MF "a[152:155],v[28:31],v[104:107],a[152:155]\n"
CV "v74,v112,v113\n"
"v_add_f32 v52,v52,v114\n"
MF "a[132:135],v[24:27],v[104:107],a[132:135]\n"
"v_add_f32 v52,v52,v115\n"
CV "v75,v114,v115\n"
MF "a[128:131],v[24:27],v[100:103],a[128:131]\n"
"v_exp_f32 v116,v116\n"
MF "a[148:151],v[28:31],v[100:103],a[148:151]\n"
"v_exp_f32 v117,v117\n"
MF "a[144:147],v[28:31],v[96:99],a[144:147]\n"
"v_exp_f32 v118,v118\n"
MF "a[124:127],v[24:27],v[96:99],a[124:127]\n"
"v_exp_f32 v119,v119\n"
MF "a[120:123],v[24:27],v[92:95],a[120:123]\n"
"v_add_f32 v53,v53,v116\n"
"v_add_f32 v53,v53,v117\n"
MF "a[140:143],v[28:31],v[92:95],a[140:143]\n"
"s_waitcnt lgkmcnt(0)\n"
TR "v[24:25],v4 offset:26624\n"
TR "v[26:27],v4 offset:32768\n"
MF "a[160:163],v[16:19],v[92:95],a[160:163]\n"
"s_nop 0\n"
TR "v[28:29],v5 offset:26624\n"
TR "v[30:31],v5 offset:32768\n"
MF "a[180:183],v[20:23],v[92:95],a[180:183]\n"
CV "v78,v116,v117\n"
"v_add_f32 v53,v53,v118\n"
MF "a[184:187],v[20:23],v[96:99],a[184:187]\n"
"v_add_f32 v53,v53,v119\n"
CV "v79,v118,v119\n"
MF "a[164:167],v[16:19],v[96:99],a[164:167]\n"
"v_exp_f32 v120,v120\n"
MF "a[168:171],v[16:19],v[100:103],a[168:171]\n"
"v_exp_f32 v121,v121\n"
MF "a[188:191],v[20:23],v[100:103],a[188:191]\n"
"v_exp_f32 v122,v122\n"
MF "a[192:195],v[20:23],v[104:107],a[192:195]\n"
"v_exp_f32 v123,v123\n"
MF "a[172:175],v[16:19],v[104:107],a[172:175]\n"
"s_waitcnt vmcnt(6) lgkmcnt(0)\n"
"v_add_f32 v54,v54,v120\n"
"v_add_f32 v54,v54,v121\n"
MF "a[176:179],v[16:19],v[108:111],a[176:179]\n"
"s_barrier\n"
MF "a[196:199],v[20:23],v[108:111],a[196:199]\n"
"s_add_u32 s60,s0,0\n"
"ds_read_b128 v[16:19],v0 offset:49152\n"
"ds_read_b128 v[20:23],v1 offset:49152\n"
MF "a[216:219],v[24:27],v[108:111],a[216:219]\n"
"s_add_u32 m0,s5,24576\n"
CV "v82,v120,v121\n"
"v_add_f32 v54,v54,v122\n"
MF "a[236:239],v[28:31],v[108:111],a[236:239]\n"
LD "v7,s[48:51],s60 offen lds\n"
MF "a[232:235],v[28:31],v[104:107],a[232:235]\n"
"s_add_u32 s60,s0,10240\n"
"s_add_u32 m0,s5,25600\n"
"v_add_f32 v54,v54,v123\n"
MF "a[212:215],v[24:27],v[104:107],a[212:215]\n"
LD "v7,s[48:51],s60 offen lds\n"
MF "a[208:211],v[24:27],v[100:103],a[208:211]\n"
"s_add_u32 s60,s0,20480\n"
"s_add_u32 m0,s5,26624\n"
CV "v83,v122,v123\n"
MF "a[228:231],v[28:31],v[100:103],a[228:231]\n"
LD "v7,s[48:51],s60 offen lds\n"
MF "a[224:227],v[28:31],v[96:99],a[224:227]\n"
MF "a[204:207],v[24:27],v[96:99],a[204:207]\n"
MF "a[200:203],v[24:27],v[92:95],a[200:203]\n"
MF "a[220:223],v[28:31],v[92:95],a[220:223]\n"
"s_waitcnt lgkmcnt(0)\n"
MF "v[92:95],v[16:19],v[136:139],v[32:35]\n"
"s_add_u32 s60,s0,30720\n"
"ds_read_b128 v[24:27],v0 offset:50176\n"
"ds_read_b128 v[28:31],v1 offset:50176\n"
MF "v[92:95],v[20:23],v[156:159],v[92:95]\n"
"s_add_u32 m0,s5,27648\n"
"v_exp_f32 v124,v124\n"
MF "v[96:99],v[20:23],v[160:163],v[36:39]\n"
LD "v7,s[48:51],s60 offen lds\n"
MF "v[96:99],v[16:19],v[140:143],v[96:99]\n"
"s_add_u32 s60,s0,40960\n"
"v_exp_f32 v125,v125\n"
MF "v[100:103],v[16:19],v[144:147],v[40:43]\n"
"s_add_u32 m0,s5,28672\n"
"v_exp_f32 v126,v126\n"
MF "v[100:103],v[20:23],v[164:167],v[100:103]\n"
LD "v7,s[48:51],s60 offen lds\n"
MF "v[104:107],v[20:23],v[168:171],v[44:47]\n"
"s_add_u32 s60,s0,51200\n"
"v_exp_f32 v127,v127\n"
MF "v[104:107],v[16:19],v[148:151],v[104:107]\n"
"s_add_u32 m0,s5,29696\n"
"v_add_f32 v55,v55,v124\n"
"v_add_f32 v55,v55,v125\n"
MF "v[108:111],v[16:19],v[152:155],v[48:51]\n"
LD "v7,s[48:51],s60 offen lds\n"
MF "v[108:111],v[20:23],v[172:175],v[108:111]\n"
"s_waitcnt lgkmcnt(0)\n"
"ds_read_b128 v[16:19],v0 offset:51200\n"
"ds_read_b128 v[20:23],v1 offset:51200\n"
MF "v[108:111],v[24:27],v[192:195],v[108:111]\n"
CV "v86,v124,v125\n"
"v_add_f32 v55,v55,v126\n"
MF "v[108:111],v[28:31],v[212:215],v[108:111]\n"
"v_add_f32 v55,v55,v127\n"
CV "v87,v126,v127\n"
MF "v[104:107],v[28:31],v[208:211],v[104:107]\n"
"v_exp_f32 v128,v128\n"
MF "v[104:107],v[24:27],v[188:191],v[104:107]\n"
"v_exp_f32 v129,v129\n"
MF "v[100:103],v[24:27],v[184:187],v[100:103]\n"
"v_exp_f32 v130,v130\n"
MF "v[100:103],v[28:31],v[204:207],v[100:103]\n"
"v_exp_f32 v131,v131\n"
MF "v[96:99],v[28:31],v[200:203],v[96:99]\n"
"v_add_f32 v56,v56,v128\n"
"v_add_f32 v56,v56,v129\n"
MF "v[96:99],v[24:27],v[180:183],v[96:99]\n"
CV "v90,v128,v129\n"
"v_add_f32 v56,v56,v130\n"
MF "v[92:95],v[24:27],v[176:179],v[92:95]\n"
"v_add_f32 v56,v56,v131\n"
CV "v91,v130,v131\n"
MF "v[92:95],v[28:31],v[196:199],v[92:95]\n"
"s_waitcnt lgkmcnt(0)\n"
"ds_read_b128 v[24:27],v0 offset:55296\n"
"ds_read_b128 v[28:31],v1 offset:55296\n"
MF "v[92:95],v[16:19],v[216:219],v[92:95]\n"
MF "v[92:95],v[20:23],v[236:239],v[92:95]\n"
MF "v[96:99],v[20:23],v[240:243],v[96:99]\n"
MF "v[96:99],v[16:19],v[220:223],v[96:99]\n"
MF "v[100:103],v[16:19],v[224:227],v[100:103]\n"
MF "v[100:103],v[20:23],v[244:247],v[100:103]\n"
MF "v[104:107],v[20:23],v[248:251],v[104:107]\n"
MF "v[104:107],v[16:19],v[228:231],v[104:107]\n"
"v_max3_f32 v10,v52,v53,v54\n"
"v_max3_f32 v10,v10,v55,v56\n"
MF "v[108:111],v[16:19],v[232:235],v[108:111]\n"
"v_cmp_le_f32 vcc,0x47800000,v10\n"
MF "v[108:111],v[20:23],v[252:255],v[108:111]\n"
"s_waitcnt lgkmcnt(0)\n"
"s_cbranch_vccnz .Lstub_27\n"
".Lback_27:\n"
MF "v[112:115],v[24:27],v[136:139],v[32:35]\n"
"s_add_u32 s74,s74,12288\n"
"ds_read_b128 v[16:19],v0 offset:56320\n"
"ds_read_b128 v[20:23],v1 offset:56320\n"
MF "v[112:115],v[28:31],v[156:159],v[112:115]\n"
"s_cmp_eq_u32 s74,s20\n"
"s_cselect_b32 s74,s70,s74\n"
MF "v[116:119],v[28:31],v[160:163],v[36:39]\n"
"v_exp_f32 v92,v92\n"
MF "v[116:119],v[24:27],v[140:143],v[116:119]\n"
"v_exp_f32 v93,v93\n"
MF "v[120:123],v[24:27],v[144:147],v[40:43]\n"
"v_exp_f32 v94,v94\n"
MF "v[120:123],v[28:31],v[164:167],v[120:123]\n"
"v_exp_f32 v95,v95\n"
MF "v[124:127],v[28:31],v[168:171],v[44:47]\n"
"v_add_f32 v57,v52,v92\n"
"v_add_f32 v57,v57,v93\n"
MF "v[124:127],v[24:27],v[148:151],v[124:127]\n"
CV "v92,v92,v93\n"
"v_add_f32 v57,v57,v94\n"
MF "v[128:131],v[24:27],v[152:155],v[48:51]\n"
"v_add_f32 v57,v57,v95\n"
CV "v93,v94,v95\n"
MF "v[128:131],v[28:31],v[172:175],v[128:131]\n"
"s_waitcnt lgkmcnt(0)\n"
"ds_read_b128 v[24:27],v0 offset:57344\n"
"ds_read_b128 v[28:31],v1 offset:57344\n"
MF "v[128:131],v[16:19],v[192:195],v[128:131]\n"
"v_exp_f32 v96,v96\n"
MF "v[128:131],v[20:23],v[212:215],v[128:131]\n"
"v_exp_f32 v97,v97\n"
MF "v[124:127],v[20:23],v[208:211],v[124:127]\n"
"v_exp_f32 v98,v98\n"
MF "v[124:127],v[16:19],v[188:191],v[124:127]\n"
"v_exp_f32 v99,v99\n"
MF "v[120:123],v[16:19],v[184:187],v[120:123]\n"
"v_add_f32 v58,v53,v96\n"
"v_add_f32 v58,v58,v97\n"
MF "v[120:123],v[20:23],v[204:207],v[120:123]\n"
CV "v96,v96,v97\n"
"v_add_f32 v58,v58,v98\n"
MF "v[116:119],v[20:23],v[200:203],v[116:119]\n"
"s_waitcnt vmcnt(6) lgkmcnt(0)\n"
"v_add_f32 v58,v58,v99\n"
CV "v97,v98,v99\n"
MF "v[116:119],v[16:19],v[180:183],v[116:119]\n"
"s_barrier\n"
MF "v[112:115],v[16:19],v[176:179],v[112:115]\n"
"v_exp_f32 v100,v100\n"
MF "v[112:115],v[20:23],v[196:199],v[112:115]\n"
"s_nop 0\n"
TR "v[16:17],v2\n"
TR "v[18:19],v2 offset:6144\n"
MF "v[112:115],v[24:27],v[216:219],v[112:115]\n"
"s_add_u32 s73,s73,12288\n"
TR "v[20:21],v3\n"
TR "v[22:23],v3 offset:6144\n"
MF "v[112:115],v[28:31],v[236:239],v[112:115]\n"
"s_cmp_eq_u32 s73,s19\n"
"s_cselect_b32 s73,s18,s73\n"
"s_add_u32 m0,s65,49152\n"
MF "v[116:119],v[28:31],v[240:243],v[116:119]\n"
LD "v8,s[36:39],s73 offen nt lds\n"
MF "v[116:119],v[24:27],v[220:223],v[116:119]\n"
"s_add_u32 m0,s65,50048\n"
"v_exp_f32 v101,v101\n"
MF "v[120:123],v[24:27],v[224:227],v[120:123]\n"
LD "v8,s[36:39],s73 offen offset:128 nt lds\n"
MF "v[120:123],v[28:31],v[244:247],v[120:123]\n"
"v_exp_f32 v102,v102\n"
MF "v[124:127],v[28:31],v[248:251],v[124:127]\n"
"s_add_u32 m0,s65,50944\n"
"v_exp_f32 v103,v103\n"
MF "v[124:127],v[24:27],v[228:231],v[124:127]\n"
LD "v8,s[36:39],s73 offen offset:256 nt lds\n"
MF "v[128:131],v[24:27],v[232:235],v[128:131]\n"
"v_add_f32 v59,v54,v100\n"
"v_add_f32 v59,v59,v101\n"
MF "v[128:131],v[28:31],v[252:255],v[128:131]\n"
"s_waitcnt lgkmcnt(0)\n"
TR "v[24:25],v4\n"
TR "v[26:27],v4 offset:6144\n"
MF "a[0:3],v[16:19],v[72:75],a[0:3]\n"
"s_add_u32 m0,s65,49152\n"
TR "v[28:29],v5\n"
TR "v[30:31],v5 offset:6144\n"
MF "a[20:23],v[20:23],v[72:75],a[20:23]\n"
LD "v8,s[36:39],s73 offen offset:3072 nt lds\n"
MF "a[24:27],v[20:23],v[76:79],a[24:27]\n"
CV "v100,v100,v101\n"
"v_add_f32 v59,v59,v102\n"
MF "a[4:7],v[16:19],v[76:79],a[4:7]\n"
"s_add_u32 m0,s65,50048\n"
"v_add_f32 v59,v59,v103\n"
CV "v101,v102,v103\n"
MF "a[8:11],v[16:19],v[80:83],a[8:11]\n"
LD "v8,s[36:39],s73 offen offset:3200 nt lds\n"
MF "a[28:31],v[20:23],v[80:83],a[28:31]\n"
"v_exp_f32 v104,v104\n"
MF "a[32:35],v[20:23],v[84:87],a[32:35]\n"
"s_add_u32 m0,s65,50944\n"
"v_exp_f32 v105,v105\n"
MF "a[12:15],v[16:19],v[84:87],a[12:15]\n"
LD "v8,s[36:39],s73 offen offset:3328 nt lds\n"
MF "a[16:19],v[16:19],v[88:91],a[16:19]\n"
"v_exp_f32 v106,v106\n"
MF "a[36:39],v[20:23],v[88:91],a[36:39]\n"
"s_waitcnt lgkmcnt(0)\n"
TR "v[16:17],v2 offset:1024\n"
TR "v[18:19],v2 offset:7168\n"
MF "a[56:59],v[24:27],v[88:91],a[56:59]\n"
"s_nop 0\n"
TR "v[20:21],v3 offset:1024\n"
TR "v[22:23],v3 offset:7168\n"
MF "a[76:79],v[28:31],v[88:91],a[76:79]\n"
"v_exp_f32 v107,v107\n"
MF "a[72:75],v[28:31],v[84:87],a[72:75]\n"
"v_add_f32 v60,v55,v104\n"
"v_add_f32 v60,v60,v105\n"
MF "a[52:55],v[24:27],v[84:87],a[52:55]\n"
CV "v104,v104,v105\n"
"v_add_f32 v60,v60,v106\n"
MF "a[48:51],v[24:27],v[80:83],a[48:51]\n"
"v_add_f32 v60,v60,v107\n"
CV "v105,v106,v107\n"
MF "a[68:71],v[28:31],v[80:83],a[68:71]\n"
"v_exp_f32 v108,v108\n"
MF "a[64:67],v[28:31],v[76:79],a[64:67]\n"
"v_exp_f32 v109,v109\n"
MF "a[44:47],v[24:27],v[76:79],a[44:47]\n"
"v_exp_f32 v110,v110\n"
MF "a[40:43],v[24:27],v[72:75],a[40:43]\n"
"v_exp_f32 v111,v111\n"
MF "a[60:63],v[28:31],v[72:75],a[60:63]\n"
"s_waitcnt lgkmcnt(0)\n"
TR "v[24:25],v4 offset:1024\n"
TR "v[26:27],v4 offset:7168\n"
MF "a[80:83],v[16:19],v[72:75],a[80:83]\n"
"s_nop 0\n"
TR "v[28:29],v5 offset:1024\n"
TR "v[30:31],v5 offset:7168\n"
MF "a[100:103],v[20:23],v[72:75],a[100:103]\n"
"v_add_f32 v61,v56,v108\n"
"v_add_f32 v61,v61,v109\n"
MF "a[104:107],v[20:23],v[76:79],a[104:107]\n"
CV "v108,v108,v109\n"
"v_add_f32 v61,v61,v110\n"
MF "a[84:87],v[16:19],v[76:79],a[84:87]\n"
"v_add_f32 v61,v61,v111\n"
CV "v109,v110,v111\n"
MF "a[88:91],v[16:19],v[80:83],a[88:91]\n"
"v_exp_f32 v112,v112\n"
MF "a[108:111],v[20:23],v[80:83],a[108:111]\n"
"v_exp_f32 v113,v113\n"
MF "a[112:115],v[20:23],v[84:87],a[112:115]\n"
"v_exp_f32 v114,v114\n"
MF "a[92:95],v[16:19],v[84:87],a[92:95]\n"
"v_exp_f32 v115,v115\n"
MF "a[96:99],v[16:19],v[88:91],a[96:99]\n"
"v_add_f32 v57,v57,v112\n"
"v_add_f32 v57,v57,v113\n"
MF "a[116:119],v[20:23],v[88:91],a[116:119]\n"
"s_waitcnt lgkmcnt(0)\n"
TR "v[16:17],v2 offset:2048\n"
TR "v[18:19],v2 offset:8192\n"
MF "a[136:139],v[24:27],v[88:91],a[136:139]\n"
"s_nop 0\n"
TR "v[20:21],v3 offset:2048\n"
TR "v[22:23],v3 offset:8192\n"
MF "a[156:159],v[28:31],v[88:91],a[156:159]\n"
CV "v94,v112,v113\n"
"v_add_f32 v57,v57,v114\n"
MF "a[152:155],v[28:31],v[84:87],a[152:155]\n"
"v_add_f32 v57,v57,v115\n"
CV "v95,v114,v115\n"
MF "a[132:135],v[24:27],v[84:87],a[132:135]\n"
"v_exp_f32 v116,v116\n"
MF "a[128:131],v[24:27],v[80:83],a[128:131]\n"
"v_exp_f32 v117,v117\n"
MF "a[148:151],v[28:31],v[80:83],a[148:151]\n"
"v_exp_f32 v118,v118\n"
MF "a[144:147],v[28:31],v[76:79],a[144:147]\n"
"v_exp_f32 v119,v119\n"
MF "a[124:127],v[24:27],v[76:79],a[124:127]\n"
"v_add_f32 v58,v58,v116\n"
"v_add_f32 v58,v58,v117\n"
MF "a[120:123],v[24:27],v[72:75],a[120:123]\n"
CV "v98,v116,v117\n"
"v_add_f32 v58,v58,v118\n"
MF "a[140:143],v[28:31],v[72:75],a[140:143]\n"
"s_waitcnt lgkmcnt(0)\n"
TR "v[24:25],v4 offset:2048\n"
TR "v[26:27],v4 offset:8192\n"
MF "a[160:163],v[16:19],v[72:75],a[160:163]\n"
"s_nop 0\n"
TR "v[28:29],v5 offset:2048\n"
TR "v[30:31],v5 offset:8192\n"
MF "a[180:183],v[20:23],v[72:75],a[180:183]\n"
"v_add_f32 v58,v58,v119\n"
CV "v99,v118,v119\n"
MF "a[184:187],v[20:23],v[76:79],a[184:187]\n"
"v_exp_f32 v120,v120\n"
MF "a[164:167],v[16:19],v[76:79],a[164:167]\n"
"v_exp_f32 v121,v121\n"
MF "a[168:171],v[16:19],v[80:83],a[168:171]\n"
"v_exp_f32 v122,v122\n"
MF "a[188:191],v[20:23],v[80:83],a[188:191]\n"
"v_exp_f32 v123,v123\n"
MF "a[192:195],v[20:23],v[84:87],a[192:195]\n"
"v_add_f32 v59,v59,v120\n"
"v_add_f32 v59,v59,v121\n"
MF "a[172:175],v[16:19],v[84:87],a[172:175]\n"
"s_waitcnt lgkmcnt(0)\n"
CV "v102,v120,v121\n"
"v_add_f32 v59,v59,v122\n"
MF "a[176:179],v[16:19],v[88:91],a[176:179]\n"
".Lpg23h:\n"
"s_cbranch_execz .Lpg27h\n"
"s_barrier\n"
MF "a[196:199],v[20:23],v[88:91],a[196:199]\n"
"s_add_u32 s60,s1,0\n"
"s_add_u32 m0,s5,0\n"
"v_add_f32 v59,v59,v123\n"
MF "a[216:219],v[24:27],v[88:91],a[216:219]\n"
LD "v7,s[48:51],s60 offen lds\n"
MF "a[236:239],v[28:31],v[88:91],a[236:239]\n"
"s_add_u32 s60,s1,10240\n"
"s_add_u32 m0,s5,1024\n"
CV "v103,v122,v123\n"
MF "a[232:235],v[28:31],v[84:87],a[232:235]\n"
LD "v7,s[48:51],s60 offen lds\n"
MF "a[212:215],v[24:27],v[84:87],a[212:215]\n"
"s_add_u32 s60,s1,20480\n"
"s_add_u32 m0,s5,2048\n"
MF "a[208:211],v[24:27],v[80:83],a[208:211]\n"
LD "v7,s[48:51],s60 offen lds\n"
MF "a[228:231],v[28:31],v[80:83],a[228:231]\n"
"v_exp_f32 v124,v124\n"
MF "a[224:227],v[28:31],v[76:79],a[224:227]\n"
"v_exp_f32 v125,v125\n"
MF "a[204:207],v[24:27],v[76:79],a[204:207]\n"
"v_exp_f32 v126,v126\n"
MF "a[200:203],v[24:27],v[72:75],a[200:203]\n"
"v_exp_f32 v127,v127\n"
MF "a[220:223],v[28:31],v[72:75],a[220:223]\n"
"v_add_f32 v60,v60,v124\n"
"v_add_f32 v60,v60,v125\n"
CV "v106,v124,v125\n"
"v_add_f32 v60,v60,v126\n"
"v_add_f32 v60,v60,v127\n"
CV "v107,v126,v127\n"
"v_exp_f32 v128,v128\n"
"v_exp_f32 v129,v129\n"
"v_exp_f32 v130,v130\n"
"v_exp_f32 v131,v131\n"
"v_add_f32 v61,v61,v128\n"
"v_add_f32 v61,v61,v129\n"
CV "v110,v128,v129\n"
"v_add_f32 v61,v61,v130\n"
"v_add_f32 v61,v61,v131\n"
CV "v111,v130,v131\n"
"v_max3_f32 v10,v57,v58,v59\n"
"v_max3_f32 v10,v10,v60,v61\n"
"v_cmp_le_f32 vcc,0x47800000,v10\n"
"s_cbranch_vccnz .Lstub_28\n"
".Lback_28:\n"
"s_waitcnt vmcnt(3) lgkmcnt(0)\n"
"s_barrier\n"
"v_add_u32 v236,s9,v9\n"
"v_add_u32 v237,65536,v236\n"
"v_mov_b32 v10,1.0\n"
TR "v[160:161],v4 offset:50176\n"
TR "v[162:163],v4 offset:56320\n"
"v_mfma_f32_16x16x4_f32 v[16:19],v10,v57,0\n"
TR "v[164:165],v5 offset:50176\n"
TR "v[166:167],v5 offset:56320\n"
TR "v[168:169],v2 offset:51200\n"
TR "v[170:171],v2 offset:57344\n"
TR "v[172:173],v3 offset:51200\n"
TR "v[174:175],v3 offset:57344\n"
"v_mfma_f32_16x16x4_f32 v[20:23],v10,v58,0\n"
TR "v[176:177],v4 offset:51200\n"
TR "v[178:179],v4 offset:57344\n"
TR "v[180:181],v5 offset:51200\n"
TR "v[182:183],v5 offset:57344\n"
TR "v[136:137],v2 offset:49152\n"
TR "v[138:139],v2 offset:55296\n"
"v_mfma_f32_16x16x4_f32 v[24:27],v10,v59,0\n"
TR "v[140:141],v3 offset:49152\n"
TR "v[142:143],v3 offset:55296\n"
TR "v[144:145],v4 offset:49152\n"
TR "v[146:147],v4 offset:55296\n"
TR "v[148:149],v5 offset:49152\n"
TR "v[150:151],v5 offset:55296\n"
"v_mfma_f32_16x16x4_f32 v[28:31],v10,v60,0\n"
TR "v[152:153],v2 offset:50176\n"
TR "v[154:155],v2 offset:56320\n"
TR "v[156:157],v3 offset:50176\n"
TR "v[158:159],v3 offset:56320\n"
"v_mfma_f32_16x16x4_f32 v[32:35],v10,v61,0\n"
"s_add_u32 m0,s5,0\n"
"v_add_u32 v195,27648,v7\n"
LD "v195,s[48:51],s1 offen offset:3072 lds\n"
"s_add_u32 m0,s5,4096\n"
"v_add_u32 v196,40960,v7\n"
LD "v196,s[48:51],s1 offen lds\n"
"s_waitcnt lgkmcnt(15)\n"
MF "a[120:123],v[160:163],v[92:95],a[120:123]\n"
MF "a[140:143],v[164:167],v[92:95],a[140:143]\n"
"v_add_u32 v197,50176,v7\n"
LD "v197,s[48:51],s1 offen offset:1024 lds\n"
"v_rcp_f32 v208,v16\n"
"v_rcp_f32 v209,v20\n"
"v_rcp_f32 v210,v24\n"
"v_rcp_f32 v211,v28\n"
"v_rcp_f32 v212,v32\n"
MF "a[124:127],v[160:163],v[96:99],a[124:127]\n"
"v_mul_f32_e64 v214,-s13,v208\n"
"v_mul_f32_e64 v215,-s13,v209\n"
"v_mul_f32_e64 v216,-s13,v210\n"
"v_mul_f32_e64 v217,-s13,v211\n"
"v_mul_f32_e64 v218,-s13,v212\n"
AR "v72,a120\n"
AR "v73,a121\n"
AR "v74,a122\n"
MF "a[144:147],v[164:167],v[96:99],a[144:147]\n"
AR "v75,a123\n"
AR "v76,a140\n"
AR "v77,a141\n"
AR "v78,a142\n"
AR "v79,a143\n"
"v_pk_fma_f32 v[72:73],v[72:73],v[208:209],v[214:215] op_sel:[0,0,0] op_sel_hi:[1,0,0]\n"
"v_pk_fma_f32 v[74:75],v[74:75],v[208:209],v[214:215] op_sel:[0,0,0] op_sel_hi:[1,0,0]\n"
"v_pk_fma_f32 v[76:77],v[76:77],v[208:209],v[214:215] op_sel:[0,0,0] op_sel_hi:[1,0,0]\n"
"v_pk_fma_f32 v[78:79],v[78:79],v[208:209],v[214:215] op_sel:[0,0,0] op_sel_hi:[1,0,0]\n"
CV "v112,v72,v73\n"
CV "v113,v74,v75\n"
CV "v114,v76,v77\n"
CV "v115,v78,v79\n"
MF "a[128:131],v[160:163],v[100:103],a[128:131]\n"
"ds_read_b128 a[140:143],v237 offset:20480\n"
AW "a120,v112\n"
AW "a121,v113\n"
AW "a122,v114\n"
AW "a123,v115\n"
"s_waitcnt lgkmcnt(7)\n"
MF "a[44:47],v[144:147],v[96:99],a[44:47]\n"
AR "v80,a124\n"
AR "v81,a125\n"
AR "v82,a126\n"
MF "a[148:151],v[164:167],v[100:103],a[148:151]\n"
AR "v83,a127\n"
AR "v84,a144\n"
AR "v85,a145\n"
"s_nop 0\n"
AR "v86,a146\n"
AR "v87,a147\n"
"v_pk_fma_f32 v[80:81],v[80:81],v[208:209],v[214:215] op_sel:[0,1,1] op_sel_hi:[1,1,1]\n"
"v_pk_fma_f32 v[82:83],v[82:83],v[208:209],v[214:215] op_sel:[0,1,1] op_sel_hi:[1,1,1]\n"
"v_pk_fma_f32 v[84:85],v[84:85],v[208:209],v[214:215] op_sel:[0,1,1] op_sel_hi:[1,1,1]\n"
"v_pk_fma_f32 v[86:87],v[86:87],v[208:209],v[214:215] op_sel:[0,1,1] op_sel_hi:[1,1,1]\n"
CV "v116,v80,v81\n"
CV "v117,v82,v83\n"
CV "v118,v84,v85\n"
CV "v119,v86,v87\n"
MF "a[132:135],v[160:163],v[104:107],a[132:135]\n"
"ds_read_b128 a[144:147],v236\n"
AW "a124,v116\n"
AW "a125,v117\n"
AW "a126,v118\n"
AW "a127,v119\n"
"s_waitcnt lgkmcnt(6)\n"
MF "a[64:67],v[148:151],v[96:99],a[64:67]\n"
AR "v72,a128\n"
AR "v73,a129\n"
AR "v74,a130\n"
MF "a[152:155],v[164:167],v[104:107],a[152:155]\n"
AR "v75,a131\n"
AR "v76,a148\n"
AR "v77,a149\n"
"s_nop 0\n"
AR "v78,a150\n"
AR "v79,a151\n"
"v_pk_fma_f32 v[72:73],v[72:73],v[210:211],v[216:217] op_sel:[0,0,0] op_sel_hi:[1,0,0]\n"
"v_pk_fma_f32 v[74:75],v[74:75],v[210:211],v[216:217] op_sel:[0,0,0] op_sel_hi:[1,0,0]\n"
"v_pk_fma_f32 v[76:77],v[76:77],v[210:211],v[216:217] op_sel:[0,0,0] op_sel_hi:[1,0,0]\n"
"v_pk_fma_f32 v[78:79],v[78:79],v[210:211],v[216:217] op_sel:[0,0,0] op_sel_hi:[1,0,0]\n"
CV "v112,v72,v73\n"
CV "v113,v74,v75\n"
CV "v114,v76,v77\n"
CV "v115,v78,v79\n"
MF "a[136:139],v[160:163],v[108:111],a[136:139]\n"
"ds_read_b128 a[148:151],v236 offset:4096\n"
AW "a128,v112\n"
AW "a129,v113\n"
AW "a130,v114\n"
AW "a131,v115\n"
"s_waitcnt lgkmcnt(5)\n"
MF "a[84:87],v[152:155],v[96:99],a[84:87]\n"
AR "v80,a132\n"
AR "v81,a133\n"
AR "v82,a134\n"
MF "a[156:159],v[164:167],v[108:111],a[156:159]\n"
AR "v83,a135\n"
AR "v84,a152\n"
AR "v85,a153\n"
"s_nop 0\n"
AR "v86,a154\n"
AR "v87,a155\n"
"v_pk_fma_f32 v[80:81],v[80:81],v[210:211],v[216:217] op_sel:[0,1,1] op_sel_hi:[1,1,1]\n"
"v_pk_fma_f32 v[82:83],v[82:83],v[210:211],v[216:217] op_sel:[0,1,1] op_sel_hi:[1,1,1]\n"
"v_pk_fma_f32 v[84:85],v[84:85],v[210:211],v[216:217] op_sel:[0,1,1] op_sel_hi:[1,1,1]\n"
"v_pk_fma_f32 v[86:87],v[86:87],v[210:211],v[216:217] op_sel:[0,1,1] op_sel_hi:[1,1,1]\n"
CV "v116,v80,v81\n"
CV "v117,v82,v83\n"
CV "v118,v84,v85\n"
CV "v119,v86,v87\n"
MF "a[160:163],v[168:171],v[92:95],a[160:163]\n"
"ds_read_b128 a[152:155],v236 offset:8192\n"
AW "a132,v116\n"
AW "a133,v117\n"
AW "a134,v118\n"
AW "a135,v119\n"
"s_waitcnt lgkmcnt(4)\n"
MF "a[104:107],v[156:159],v[96:99],a[104:107]\n"
AR "v72,a136\n"
AR "v73,a137\n"
AR "v74,a138\n"
MF "a[180:183],v[172:175],v[92:95],a[180:183]\n"
AR "v75,a139\n"
AR "v76,a156\n"
AR "v77,a157\n"
"s_nop 0\n"
AR "v78,a158\n"
AR "v79,a159\n"
"v_pk_fma_f32 v[72:73],v[72:73],v[212:213],v[218:219] op_sel:[0,0,0] op_sel_hi:[1,0,0]\n"
"v_pk_fma_f32 v[74:75],v[74:75],v[212:213],v[218:219] op_sel:[0,0,0] op_sel_hi:[1,0,0]\n"
"v_pk_fma_f32 v[76:77],v[76:77],v[212:213],v[218:219] op_sel:[0,0,0] op_sel_hi:[1,0,0]\n"
"v_pk_fma_f32 v[78:79],v[78:79],v[212:213],v[218:219] op_sel:[0,0,0] op_sel_hi:[1,0,0]\n"
CV "v112,v72,v73\n"
CV "v113,v74,v75\n"
CV "v114,v76,v77\n"
CV "v115,v78,v79\n"
MF "a[164:167],v[168:171],v[96:99],a[164:167]\n"
"ds_read_b128 a[156:159],v236 offset:12288\n"
AW "a136,v112\n"
AW "a137,v113\n"
AW "a138,v114\n"
AW "a139,v115\n"
"v_max3_f32 v10,|v72|,|v73|,|v74|\n"
"v_max3_f32 v10,v10,|v75|,|v76|\n"
"v_max3_f32 v10,v10,|v77|,|v78|\n"
"v_max3_f32 v10,v10,|v78|,|v79|\n"
"v_mul_f32 v10,s10,v10\n"
"v_lshrrev_b32 v11,2,v9\n"
"v_readlane_b32 s12,v10,3\n"
"v_readlane_b32 s6,v10,22\n"
"v_readlane_b32 s7,v10,41\n"
"v_readlane_b32 s8,v10,60\n"
"v_add_u32 v11,s14,v11\n"
"s_min_u32 s12,s12,s6\n"
"s_min_u32 s7,s7,s8\n"
"s_min_u32 s12,s12,s7\n"
"s_and_b32 s12,s12,0x7f800000\n"
"s_sub_u32 s12,s12,0x14000000\n"
"s_cmp_lt_u32 s12,0x50000000\n"
"s_cselect_b32 s12,s12,0xe7400000\n"
"s_add_u32 s12,s12,0x18c00000\n"
"v_mov_b32_e64 v10,s12\n"
"v_or_b32 v12,0x80000000,v10\n"
"ds_write2st64_b32 v11,v10,v12 offset1:1\n"
MF "a[8:11],v[136:139],v[100:103],a[8:11]\n"
AR "v80,a160\n"
AR "v81,a161\n"
AR "v82,a162\n"
MF "a[184:187],v[172:175],v[96:99],a[184:187]\n"
AR "v83,a163\n"
AR "v84,a180\n"
AR "v85,a181\n"
AR "v86,a182\n"
AR "v87,a183\n"
"v_pk_fma_f32 v[80:81],v[80:81],v[208:209],v[214:215] op_sel:[0,0,0] op_sel_hi:[1,0,0]\n"
"v_pk_fma_f32 v[82:83],v[82:83],v[208:209],v[214:215] op_sel:[0,0,0] op_sel_hi:[1,0,0]\n"
"v_pk_fma_f32 v[84:85],v[84:85],v[208:209],v[214:215] op_sel:[0,0,0] op_sel_hi:[1,0,0]\n"
"v_pk_fma_f32 v[86:87],v[86:87],v[208:209],v[214:215] op_sel:[0,0,0] op_sel_hi:[1,0,0]\n"
CV "v116,v80,v81\n"
CV "v117,v82,v83\n"
CV "v118,v84,v85\n"
CV "v119,v86,v87\n"
MF "a[168:171],v[168:171],v[100:103],a[168:171]\n"
"ds_read_b128 a[180:183],v236 offset:16384\n"
AW "a160,v116\n"
AW "a161,v117\n"
AW "a162,v118\n"
AW "a163,v119\n"
MF "a[28:31],v[140:143],v[100:103],a[28:31]\n"
AR "v72,a164\n"
AR "v73,a165\n"
AR "v74,a166\n"
MF "a[188:191],v[172:175],v[100:103],a[188:191]\n"
AR "v75,a167\n"
AR "v76,a184\n"
AR "v77,a185\n"
AR "v78,a186\n"
AR "v79,a187\n"
"v_pk_fma_f32 v[72:73],v[72:73],v[208:209],v[214:215] op_sel:[0,1,1] op_sel_hi:[1,1,1]\n"
"v_pk_fma_f32 v[74:75],v[74:75],v[208:209],v[214:215] op_sel:[0,1,1] op_sel_hi:[1,1,1]\n"
"v_pk_fma_f32 v[76:77],v[76:77],v[208:209],v[214:215] op_sel:[0,1,1] op_sel_hi:[1,1,1]\n"
"v_pk_fma_f32 v[78:79],v[78:79],v[208:209],v[214:215] op_sel:[0,1,1] op_sel_hi:[1,1,1]\n"
CV "v112,v72,v73\n"
CV "v113,v74,v75\n"
CV "v114,v76,v77\n"
CV "v115,v78,v79\n"
MF "a[172:175],v[168:171],v[104:107],a[172:175]\n"
"ds_read_b128 a[184:187],v236 offset:20480\n"
AW "a164,v112\n"
AW "a165,v113\n"
AW "a166,v114\n"
AW "a167,v115\n"
MF "a[48:51],v[144:147],v[100:103],a[48:51]\n"
"s_waitcnt lgkmcnt(0)\n"
"s_barrier\n"
"s_add_u32 m0,s5,49152\n"
"v_add_u32_e64 v192,0,v7\n"
LD "v192,s[48:51],s2 offen lds\n"
AR "v80,a168\n"
AR "v81,a169\n"
AR "v82,a170\n"
MF "a[192:195],v[172:175],v[104:107],a[192:195]\n"
AR "v83,a171\n"
AR "v84,a188\n"
AR "v85,a189\n"
AR "v86,a190\n"
AR "v87,a191\n"
"v_pk_fma_f32 v[80:81],v[80:81],v[210:211],v[216:217] op_sel:[0,0,0] op_sel_hi:[1,0,0]\n"
"v_pk_fma_f32 v[82:83],v[82:83],v[210:211],v[216:217] op_sel:[0,0,0] op_sel_hi:[1,0,0]\n"
"v_pk_fma_f32 v[84:85],v[84:85],v[210:211],v[216:217] op_sel:[0,0,0] op_sel_hi:[1,0,0]\n"
"v_pk_fma_f32 v[86:87],v[86:87],v[210:211],v[216:217] op_sel:[0,0,0] op_sel_hi:[1,0,0]\n"
"v_add_u32 v193,9216,v7\n"
LD "v193,s[48:51],s2 offen offset:1024 lds\n"
"v_add_u32 v194,18432,v7\n"
LD "v194,s[48:51],s2 offen offset:2048 lds\n"
LD "v195,s[48:51],s2 offen offset:3072 lds\n"
"s_add_u32 m0,s5,53248\n"
"s_nop 0\n"
LD "v196,s[48:51],s2 offen lds\n"
LD "v197,s[48:51],s2 offen offset:1024 lds\n"
CV "v116,v80,v81\n"
CV "v117,v82,v83\n"
CV "v118,v84,v85\n"
CV "v119,v86,v87\n"
MF "a[176:179],v[168:171],v[108:111],a[176:179]\n"
"v_accvgpr_mov_b32 a188,a240\n"
"v_accvgpr_mov_b32 a189,a241\n"
"v_accvgpr_mov_b32 a190,a242\n"
"v_accvgpr_mov_b32 a191,a243\n"
AW "a168,v116\n"
AW "a169,v117\n"
AW "a170,v118\n"
AW "a171,v119\n"
MF "a[68:71],v[148:151],v[100:103],a[68:71]\n"
"s_add_u32 m0,s5,73728\n"
"s_nop 0\n"
LD "v192,s[48:51],s3 offen lds\n"
AR "v72,a172\n"
AR "v73,a173\n"
AR "v74,a174\n"
MF "a[196:199],v[172:175],v[108:111],a[196:199]\n"
AR "v75,a175\n"
AR "v76,a192\n"
AR "v77,a193\n"
AR "v78,a194\n"
AR "v79,a195\n"
"v_pk_fma_f32 v[72:73],v[72:73],v[210:211],v[216:217] op_sel:[0,1,1] op_sel_hi:[1,1,1]\n"
"v_pk_fma_f32 v[74:75],v[74:75],v[210:211],v[216:217] op_sel:[0,1,1] op_sel_hi:[1,1,1]\n"
"v_pk_fma_f32 v[76:77],v[76:77],v[210:211],v[216:217] op_sel:[0,1,1] op_sel_hi:[1,1,1]\n"
"v_pk_fma_f32 v[78:79],v[78:79],v[210:211],v[216:217] op_sel:[0,1,1] op_sel_hi:[1,1,1]\n"
CV "v112,v72,v73\n"
CV "v113,v74,v75\n"
CV "v114,v76,v77\n"
CV "v115,v78,v79\n"
MF "a[200:203],v[176:179],v[92:95],a[200:203]\n"
"v_accvgpr_mov_b32 a192,a244\n"
"v_accvgpr_mov_b32 a193,a245\n"
"v_accvgpr_mov_b32 a194,a246\n"
"v_accvgpr_mov_b32 a195,a247\n"
AW "a172,v112\n"
AW "a173,v113\n"
AW "a174,v114\n"
AW "a175,v115\n"
MF "a[88:91],v[152:155],v[100:103],a[88:91]\n"
LD "v193,s[48:51],s3 offen offset:1024 lds\n"
AR "v80,a176\n"
AR "v81,a177\n"
AR "v82,a178\n"
MF "a[220:223],v[180:183],v[92:95],a[220:223]\n"
AR "v83,a179\n"
AR "v84,a196\n"
AR "v85,a197\n"
AR "v86,a198\n"
AR "v87,a199\n"
"v_pk_fma_f32 v[80:81],v[80:81],v[212:213],v[218:219] op_sel:[0,0,0] op_sel_hi:[1,0,0]\n"
"v_pk_fma_f32 v[82:83],v[82:83],v[212:213],v[218:219] op_sel:[0,0,0] op_sel_hi:[1,0,0]\n"
"v_pk_fma_f32 v[84:85],v[84:85],v[212:213],v[218:219] op_sel:[0,0,0] op_sel_hi:[1,0,0]\n"
"v_pk_fma_f32 v[86:87],v[86:87],v[212:213],v[218:219] op_sel:[0,0,0] op_sel_hi:[1,0,0]\n"
CV "v116,v80,v81\n"
CV "v117,v82,v83\n"
CV "v118,v84,v85\n"
CV "v119,v86,v87\n"
MF "a[204:207],v[176:179],v[96:99],a[204:207]\n"
"v_accvgpr_mov_b32 a196,a248\n"
"v_accvgpr_mov_b32 a197,a249\n"
"v_accvgpr_mov_b32 a198,a250\n"
"v_accvgpr_mov_b32 a199,a251\n"
AW "a176,v116\n"
AW "a177,v117\n"
AW "a178,v118\n"
AW "a179,v119\n"
MF "a[108:111],v[156:159],v[100:103],a[108:111]\n"
AR "v72,a200\n"
AR "v73,a201\n"
AR "v74,a202\n"
MF "a[224:227],v[180:183],v[96:99],a[224:227]\n"
AR "v75,a203\n"
AR "v76,a220\n"
AR "v77,a221\n"
AR "v78,a222\n"
AR "v79,a223\n"
"v_pk_fma_f32 v[72:73],v[72:73],v[208:209],v[214:215] op_sel:[0,0,0] op_sel_hi:[1,0,0]\n"
"v_pk_fma_f32 v[74:75],v[74:75],v[208:209],v[214:215] op_sel:[0,0,0] op_sel_hi:[1,0,0]\n"
"v_pk_fma_f32 v[76:77],v[76:77],v[208:209],v[214:215] op_sel:[0,0,0] op_sel_hi:[1,0,0]\n"
"v_pk_fma_f32 v[78:79],v[78:79],v[208:209],v[214:215] op_sel:[0,0,0] op_sel_hi:[1,0,0]\n"
CV "v112,v72,v73\n"
CV "v113,v74,v75\n"
CV "v114,v76,v77\n"
CV "v115,v78,v79\n"
MF "a[208:211],v[176:179],v[100:103],a[208:211]\n"
AW "a220,v62\n"
AW "a221,v63\n"
AW "a222,v64\n"
AW "a223,v65\n"
AW "a200,v112\n"
AW "a201,v113\n"
AW "a202,v114\n"
AW "a203,v115\n"
MF "a[12:15],v[136:139],v[104:107],a[12:15]\n"
LD "v194,s[48:51],s3 offen offset:2048 lds\n"
AR "v80,a204\n"
AR "v81,a205\n"
AR "v82,a206\n"
MF "a[228:231],v[180:183],v[100:103],a[228:231]\n"
AR "v83,a207\n"
AR "v84,a224\n"
AR "v85,a225\n"
AR "v86,a226\n"
AR "v87,a227\n"
"v_pk_fma_f32 v[80:81],v[80:81],v[208:209],v[214:215] op_sel:[0,1,1] op_sel_hi:[1,1,1]\n"
"v_pk_fma_f32 v[82:83],v[82:83],v[208:209],v[214:215] op_sel:[0,1,1] op_sel_hi:[1,1,1]\n"
"v_pk_fma_f32 v[84:85],v[84:85],v[208:209],v[214:215] op_sel:[0,1,1] op_sel_hi:[1,1,1]\n"
"v_pk_fma_f32 v[86:87],v[86:87],v[208:209],v[214:215] op_sel:[0,1,1] op_sel_hi:[1,1,1]\n"
CV "v116,v80,v81\n"
CV "v117,v82,v83\n"
CV "v118,v84,v85\n"
CV "v119,v86,v87\n"
MF "a[212:215],v[176:179],v[104:107],a[212:215]\n"
AW "a224,v66\n"
AW "a225,v67\n"
AW "a226,v68\n"
AW "a227,v69\n"
AW "a204,v116\n"
AW "a205,v117\n"
AW "a206,v118\n"
AW "a207,v119\n"
MF "a[32:35],v[140:143],v[104:107],a[32:35]\n"
LD "v195,s[48:51],s3 offen offset:3072 lds\n"
AR "v72,a208\n"
AR "v73,a209\n"
AR "v74,a210\n"
MF "a[232:235],v[180:183],v[104:107],a[232:235]\n"
AR "v75,a211\n"
AR "v76,a228\n"
AR "v77,a229\n"
AR "v78,a230\n"
AR "v79,a231\n"
"v_pk_fma_f32 v[72:73],v[72:73],v[210:211],v[216:217] op_sel:[0,0,0] op_sel_hi:[1,0,0]\n"
"v_pk_fma_f32 v[74:75],v[74:75],v[210:211],v[216:217] op_sel:[0,0,0] op_sel_hi:[1,0,0]\n"
"v_pk_fma_f32 v[76:77],v[76:77],v[210:211],v[216:217] op_sel:[0,0,0] op_sel_hi:[1,0,0]\n"
"v_pk_fma_f32 v[78:79],v[78:79],v[210:211],v[216:217] op_sel:[0,0,0] op_sel_hi:[1,0,0]\n"
CV "v112,v72,v73\n"
CV "v113,v74,v75\n"
CV "v114,v76,v77\n"
CV "v115,v78,v79\n"
MF "a[216:219],v[176:179],v[108:111],a[216:219]\n"
AW "a228,v70\n"
AW "a229,v71\n"
AW "a230,v132\n"
AW "a231,v133\n"
AW "a208,v112\n"
AW "a209,v113\n"
AW "a210,v114\n"
AW "a211,v115\n"
MF "a[52:55],v[144:147],v[104:107],a[52:55]\n"
"s_add_u32 m0,s5,77824\n"
"s_nop 0\n"
LD "v196,s[48:51],s3 offen lds\n"
AR "v80,a212\n"
AR "v81,a213\n"
AR "v82,a214\n"
MF "a[236:239],v[180:183],v[108:111],a[236:239]\n"
AR "v83,a215\n"
AR "v84,a232\n"
AR "v85,a233\n"
"s_nop 0\n"
AR "v86,a234\n"
AR "v87,a235\n"
"v_pk_fma_f32 v[80:81],v[80:81],v[210:211],v[216:217] op_sel:[0,1,1] op_sel_hi:[1,1,1]\n"
"v_pk_fma_f32 v[82:83],v[82:83],v[210:211],v[216:217] op_sel:[0,1,1] op_sel_hi:[1,1,1]\n"
"v_pk_fma_f32 v[84:85],v[84:85],v[210:211],v[216:217] op_sel:[0,1,1] op_sel_hi:[1,1,1]\n"
"v_pk_fma_f32 v[86:87],v[86:87],v[210:211],v[216:217] op_sel:[0,1,1] op_sel_hi:[1,1,1]\n"
CV "v116,v80,v81\n"
CV "v117,v82,v83\n"
CV "v118,v84,v85\n"
CV "v119,v86,v87\n"
MF "a[0:3],v[136:139],v[92:95],a[0:3]\n"
AW "a232,v134\n"
AW "a233,v135\n"
AW "a234,v6\n"
AW "a235,v13\n"
AW "a212,v116\n"
AW "a213,v117\n"
AW "a214,v118\n"
AW "a215,v119\n"
MF "a[72:75],v[148:151],v[104:107],a[72:75]\n"
LD "v197,s[48:51],s3 offen offset:1024 lds\n"
AR "v72,a216\n"
AR "v73,a217\n"
AR "v74,a218\n"
MF "a[20:23],v[140:143],v[92:95],a[20:23]\n"
AR "v75,a219\n"
AR "v76,a236\n"
AR "v77,a237\n"
AR "v78,a238\n"
AR "v79,a239\n"
"v_pk_fma_f32 v[72:73],v[72:73],v[212:213],v[218:219] op_sel:[0,0,0] op_sel_hi:[1,0,0]\n"
"v_pk_fma_f32 v[74:75],v[74:75],v[212:213],v[218:219] op_sel:[0,0,0] op_sel_hi:[1,0,0]\n"
"v_pk_fma_f32 v[76:77],v[76:77],v[212:213],v[218:219] op_sel:[0,0,0] op_sel_hi:[1,0,0]\n"
"v_pk_fma_f32 v[78:79],v[78:79],v[212:213],v[218:219] op_sel:[0,0,0] op_sel_hi:[1,0,0]\n"
CV "v112,v72,v73\n"
CV "v113,v74,v75\n"
CV "v114,v76,v77\n"
CV "v115,v78,v79\n"
MF "a[40:43],v[144:147],v[92:95],a[40:43]\n"
AW "a236,v14\n"
AW "a237,v15\n"
"v_accvgpr_mov_b32 a238,a254\n"
"v_accvgpr_mov_b32 a239,a255\n"
AW "a216,v112\n"
AW "a217,v113\n"
AW "a218,v114\n"
AW "a219,v115\n"
MF "a[92:95],v[152:155],v[104:107],a[92:95]\n"
".Lpg24h:\n"
"s_cbranch_execz .Lpg28h\n"
AR "v80,a0\n"
AR "v81,a1\n"
AR "v82,a2\n"
MF "a[60:63],v[148:151],v[92:95],a[60:63]\n"
AR "v83,a3\n"
AR "v84,a20\n"
AR "v85,a21\n"
"s_nop 0\n"
AR "v86,a22\n"
AR "v87,a23\n"
"v_pk_fma_f32 v[80:81],v[80:81],v[208:209],v[214:215] op_sel:[0,0,0] op_sel_hi:[1,0,0]\n"
"v_pk_fma_f32 v[82:83],v[82:83],v[208:209],v[214:215] op_sel:[0,0,0] op_sel_hi:[1,0,0]\n"
"v_pk_fma_f32 v[84:85],v[84:85],v[208:209],v[214:215] op_sel:[0,0,0] op_sel_hi:[1,0,0]\n"
"v_pk_fma_f32 v[86:87],v[86:87],v[208:209],v[214:215] op_sel:[0,0,0] op_sel_hi:[1,0,0]\n"
CV "v116,v80,v81\n"
CV "v117,v82,v83\n"
CV "v118,v84,v85\n"
CV "v119,v86,v87\n"
MF "a[80:83],v[152:155],v[92:95],a[80:83]\n"
"ds_read_b128 a[20:23],v236 offset:24576\n"
AW "a0,v116\n"
AW "a1,v117\n"
AW "a2,v118\n"
AW "a3,v119\n"
MF "a[112:115],v[156:159],v[104:107],a[112:115]\n"
AR "v72,a40\n"
AR "v73,a41\n"
AR "v74,a42\n"
MF "a[100:103],v[156:159],v[92:95],a[100:103]\n"
AR "v75,a43\n"
AR "v76,a60\n"
AR "v77,a61\n"
AR "v78,a62\n"
AR "v79,a63\n"
"v_pk_fma_f32 v[72:73],v[72:73],v[208:209],v[214:215] op_sel:[0,0,0] op_sel_hi:[1,0,0]\n"
"v_pk_fma_f32 v[74:75],v[74:75],v[208:209],v[214:215] op_sel:[0,0,0] op_sel_hi:[1,0,0]\n"
"v_pk_fma_f32 v[76:77],v[76:77],v[208:209],v[214:215] op_sel:[0,0,0] op_sel_hi:[1,0,0]\n"
"v_pk_fma_f32 v[78:79],v[78:79],v[208:209],v[214:215] op_sel:[0,0,0] op_sel_hi:[1,0,0]\n"
CV "v112,v72,v73\n"
CV "v113,v74,v75\n"
CV "v114,v76,v77\n"
CV "v115,v78,v79\n"
MF "a[4:7],v[136:139],v[96:99],a[4:7]\n"
"ds_read_b128 a[60:63],v236 offset:45056\n"
AW "a40,v112\n"
AW "a41,v113\n"
AW "a42,v114\n"
AW "a43,v115\n"
MF "a[16:19],v[136:139],v[108:111],a[16:19]\n"
AR "v80,a80\n"
AR "v81,a81\n"
AR "v82,a82\n"
MF "a[24:27],v[140:143],v[96:99],a[24:27]\n"
AR "v83,a83\n"
AR "v84,a100\n"
AR "v85,a101\n"
AR "v86,a102\n"
AR "v87,a103\n"
"v_pk_fma_f32 v[80:81],v[80:81],v[208:209],v[214:215] op_sel:[0,0,0] op_sel_hi:[1,0,0]\n"
"v_pk_fma_f32 v[82:83],v[82:83],v[208:209],v[214:215] op_sel:[0,0,0] op_sel_hi:[1,0,0]\n"
"v_pk_fma_f32 v[84:85],v[84:85],v[208:209],v[214:215] op_sel:[0,0,0] op_sel_hi:[1,0,0]\n"
"v_pk_fma_f32 v[86:87],v[86:87],v[208:209],v[214:215] op_sel:[0,0,0] op_sel_hi:[1,0,0]\n"
CV "v116,v80,v81\n"
CV "v117,v82,v83\n"
CV "v118,v84,v85\n"
CV "v119,v86,v87\n"
MF "a[36:39],v[140:143],v[108:111],a[36:39]\n"
"ds_read_b128 a[100:103],v237\n"
AW "a80,v116\n"
AW "a81,v117\n"
AW "a82,v118\n"
AW "a83,v119\n"
MF "a[56:59],v[144:147],v[108:111],a[56:59]\n"
AR "v72,a4\n"
AR "v73,a5\n"
AR "v74,a6\n"
MF "a[76:79],v[148:151],v[108:111],a[76:79]\n"
AR "v75,a7\n"
AR "v76,a24\n"
AR "v77,a25\n"
AR "v78,a26\n"
AR "v79,a27\n"
"v_pk_fma_f32 v[72:73],v[72:73],v[208:209],v[214:215] op_sel:[0,1,1] op_sel_hi:[1,1,1]\n"
"v_pk_fma_f32 v[74:75],v[74:75],v[208:209],v[214:215] op_sel:[0,1,1] op_sel_hi:[1,1,1]\n"
"v_pk_fma_f32 v[76:77],v[76:77],v[208:209],v[214:215] op_sel:[0,1,1] op_sel_hi:[1,1,1]\n"
"v_pk_fma_f32 v[78:79],v[78:79],v[208:209],v[214:215] op_sel:[0,1,1] op_sel_hi:[1,1,1]\n"
CV "v112,v72,v73\n"
CV "v113,v74,v75\n"
CV "v114,v76,v77\n"
CV "v115,v78,v79\n"
MF "a[96:99],v[152:155],v[108:111],a[96:99]\n"
"ds_read_b128 a[24:27],v236 offset:28672\n"
AW "a4,v112\n"
AW "a5,v113\n"
AW "a6,v114\n"
AW "a7,v115\n"
MF "a[116:119],v[156:159],v[108:111],a[116:119]\n"
"v_and_b32 v0,512,v9\n"
"v_lshlrev_b32 v0,2,v0\n"
"v_sub_u32 v0,v2,v0\n"
"v_xor_b32 v0,s4,v0\n"
"v_mov_b32 v252,s15\n"
"s_waitcnt vmcnt(12) lgkmcnt(0)\n"
"s_barrier\n"
"ds_read_b128 v[252:255],v252\n"
TR "v[176:177],v0 offset:24576\n"
TR "v[178:179],v0 offset:26624\n"
TR "v[180:181],v0 offset:28672\n"
TR "v[182:183],v0 offset:30720\n"
TR "v[184:185],v0 offset:32768\n"
TR "v[186:187],v0 offset:34816\n"
"v_xor_b32 v1,32,v0\n"
"v_xor_b32 v2,64,v0\n"
"v_xor_b32 v3,96,v0\n"
"v_add_u32 v4,73728,v0\n"
"v_add_u32 v5,73728,v1\n"
"v_add_u32 v6,73728,v2\n"
"v_add_u32 v7,73728,v3\n"
"s_cmp_ge_u32 s56,2\n"
"s_cbranch_scc1 .Lrole1\n"
"s_waitcnt lgkmcnt(4)\n"
MF "v[16:19],a[120:123],v[176:179],v[252:255]\n"
"s_mov_b32 s65,128\n"
TR "v[188:189],v1 offset:24576\n"
AR "v220,a44\n"
"s_waitcnt lgkmcnt(3)\n"
MF "v[16:19],a[160:163],v[180:183],v[16:19]\n"
"s_mov_b32 s9,0x3f80\n"
TR "v[190:191],v1 offset:26624\n"
AR "v221,a45\n"
"s_waitcnt lgkmcnt(2)\n"
MF "v[16:19],a[200:203],v[184:187],v[16:19]\n"
"s_add_u32 s89,s89,s65\n"
AR "v222,a46\n"
AR "v223,a47\n"
MF "v[24:27],a[204:207],v[184:187],v[252:255]\n"
"s_bfe_u32 s58,s22,0x30014\n"
TR "a[240:241],v1 offset:28672\n"
AR "v224,a64\n"
MF "v[24:27],a[164:167],v[180:183],v[24:27]\n"
"s_add_u32 s61,s58,1\n"
TR "a[242:243],v1 offset:30720\n"
AR "v225,a65\n"
MF "v[24:27],a[124:127],v[176:179],v[24:27]\n"
"s_and_b32 s61,s61,7\n"
AR "v226,a66\n"
AR "v227,a67\n"
MF "v[32:35],a[128:131],v[176:179],v[252:255]\n"
"s_mov_b32 s64,9\n"
TR "a[244:245],v1 offset:32768\n"
"ds_read_b128 a[64:67],v236 offset:49152\n"
MF "v[32:35],a[168:171],v[180:183],v[32:35]\n"
"s_xor_b32 s67,s78,8\n"
TR "a[246:247],v1 offset:34816\n"
"v_fma_f32 v220,v220,v209,v215\n"
MF "v[32:35],a[208:211],v[184:187],v[32:35]\n"
"s_mov_b32 s71,204800\n"
"v_fma_f32 v221,v221,v209,v215\n"
"v_fma_f32 v222,v222,v209,v215\n"
MF "v[40:43],a[212:215],v[184:187],v[252:255]\n"
"s_mov_b32 s66,204800\n"
"v_fma_f32 v223,v223,v209,v215\n"
"v_fma_f32 v224,v224,v209,v215\n"
MF "v[40:43],a[172:175],v[180:183],v[40:43]\n"
"s_and_b32 s73,s56,1\n"
"v_fma_f32 v225,v225,v209,v215\n"
"v_fma_f32 v226,v226,v209,v215\n"
MF "v[40:43],a[132:135],v[176:179],v[40:43]\n"
"s_mul_i32 s73,s73,5120\n"
"v_fma_f32 v227,v227,v209,v215\n"
CV "v220,v220,v221\n"
MF "v[48:51],a[136:139],v[176:179],v[252:255]\n"
"s_add_u32 s73,s73,s93\n"
CV "v221,v222,v223\n"
CV "v222,v224,v225\n"
MF "v[48:51],a[176:179],v[180:183],v[48:51]\n"
"s_xor_b32 s58,s58,4\n"
CV "v223,v226,v227\n"
AW "a44,v220\n"
MF "v[48:51],a[216:219],v[184:187],v[48:51]\n"
"s_waitcnt lgkmcnt(0)\n"
AW "a45,v221\n"
AW "a46,v222\n"
MF "v[52:55],a[216:219],a[244:247],v[252:255]\n"
"s_mul_i32 s70,s58,204800\n"
TR "v[176:177],v2 offset:24576\n"
AW "a47,v223\n"
MF "v[52:55],a[176:179],a[240:243],v[52:55]\n"
"s_add_u32 s70,s70,s73\n"
TR "v[178:179],v2 offset:26624\n"
AR "v228,a84\n"
MF "v[52:55],a[136:139],v[188:191],v[52:55]\n"
"s_add_u32 s70,s70,0xa000\n"
AR "v229,a85\n"
AR "v230,a86\n"
MF "v[44:47],a[132:135],v[188:191],v[252:255]\n"
"s_mov_b32 s85,1\n"
TR "v[180:181],v2 offset:28672\n"
AR "v231,a87\n"
MF "v[44:47],a[172:175],a[240:243],v[44:47]\n"
"s_mov_b32 s83,0xf0000000\n"
TR "v[182:183],v2 offset:30720\n"
AR "v232,a104\n"
MF "v[44:47],a[212:215],a[244:247],v[44:47]\n"
"s_mov_b32 s82,0xf0000000\n"
AR "v233,a105\n"
AR "v234,a106\n"
MF "v[36:39],a[208:211],a[244:247],v[252:255]\n"
"s_add_u32 s58,s79,73728\n"
TR "v[184:185],v2 offset:32768\n"
AR "v235,a107\n"
MF "v[36:39],a[168:171],a[240:243],v[36:39]\n"
"s_mov_b32 s4,64\n"
TR "v[186:187],v2 offset:34816\n"
"ds_read_b128 a[104:107],v237 offset:4096\n"
MF "v[36:39],a[128:131],v[188:191],v[36:39]\n"
"s_mul_i32 s95,s92,3277\n"
"v_fma_f32 v228,v228,v209,v215\n"
"v_fma_f32 v229,v229,v209,v215\n"
MF "v[28:31],a[124:127],v[188:191],v[252:255]\n"
"s_lshr_b32 s95,s95,16\n"
"v_fma_f32 v230,v230,v209,v215\n"
"v_fma_f32 v231,v231,v209,v215\n"
MF "v[28:31],a[164:167],a[240:243],v[28:31]\n"
"s_mul_i32 s59,s95,20\n"
"v_fma_f32 v232,v232,v209,v215\n"
"v_fma_f32 v233,v233,v209,v215\n"
MF "v[28:31],a[204:207],a[244:247],v[28:31]\n"
"s_sub_u32 s59,s92,s59\n"
"v_fma_f32 v234,v234,v209,v215\n"
"v_fma_f32 v235,v235,v209,v215\n"
MF "v[20:23],a[200:203],a[244:247],v[252:255]\n"
"s_cmp_ge_u32 s59,10\n"
"s_cselect_b32 s96,4294557760,0\n"
CV "v228,v228,v229\n"
MF "v[20:23],a[160:163],a[240:243],v[20:23]\n"
"s_add_u32 s96,s96,1024\n"
CV "v229,v230,v231\n"
CV "v230,v232,v233\n"
MF "v[20:23],a[120:123],v[188:191],v[20:23]\n"
"s_waitcnt lgkmcnt(1)\n"
CV "v231,v234,v235\n"
AW "a84,v228\n"
MF "v[56:59],a[120:123],v[176:179],v[252:255]\n"
"s_mul_i32 s59,s59,40960\n"
TR "v[188:189],v3 offset:24576\n"
AW "a85,v229\n"
MF "v[56:59],a[160:163],v[180:183],v[56:59]\n"
"s_add_u32 s96,s96,s59\n"
TR "v[190:191],v3 offset:26624\n"
AW "a86,v230\n"
MF "v[56:59],a[200:203],v[184:187],v[56:59]\n"
"s_lshl_b32 s59,s95,7\n"
AW "a87,v231\n"
AR "v220,a8\n"
MF "v[64:67],a[204:207],v[184:187],v[252:255]\n"
"s_add_u32 s96,s96,s59\n"
TR "a[240:241],v3 offset:28672\n"
AR "v221,a9\n"
MF "v[64:67],a[164:167],v[180:183],v[64:67]\n"
"s_lshl_b32 s2,s92,10\n"
TR "a[242:243],v3 offset:30720\n"
AR "v222,a10\n"
MF "v[64:67],a[124:127],v[176:179],v[64:67]\n"
"s_add_u32 s2,s2,s93\n"
AR "v223,a11\n"
AR "v224,a28\n"
MF "v[72:75],a[128:131],v[176:179],v[252:255]\n"
"s_mov_b32 s0,s96\n"
TR "a[244:245],v3 offset:32768\n"
AR "v225,a29\n"
MF "v[72:75],a[168:171],v[180:183],v[72:75]\n"
"s_sub_u32 s2,s2,204800\n"
TR "a[246:247],v3 offset:34816\n"
AR "v226,a30\n"
MF "v[72:75],a[208:211],v[184:187],v[72:75]\n"
"s_mov_b32 s92,64\n"
AR "v227,a31\n"
"v_fma_f32 v220,v220,v210,v216\n"
MF "v[80:83],a[212:215],v[184:187],v[252:255]\n"
"ds_read_b128 a[28:31],v236 offset:32768\n"
"v_fma_f32 v221,v221,v210,v216\n"
MF "v[80:83],a[172:175],v[180:183],v[80:83]\n"
"v_fma_f32 v222,v222,v210,v216\n"
"v_fma_f32 v223,v223,v210,v216\n"
MF "v[80:83],a[132:135],v[176:179],v[80:83]\n"
"v_fma_f32 v224,v224,v210,v216\n"
"v_fma_f32 v225,v225,v210,v216\n"
MF "v[88:91],a[136:139],v[176:179],v[252:255]\n"
"v_fma_f32 v226,v226,v210,v216\n"
"v_fma_f32 v227,v227,v210,v216\n"
MF "v[88:91],a[176:179],v[180:183],v[88:91]\n"
"s_waitcnt vmcnt(0) lgkmcnt(0)\n"
CV "v220,v220,v221\n"
CV "v221,v222,v223\n"
MF "v[88:91],a[216:219],v[184:187],v[88:91]\n"
"s_barrier\n"
MF "v[92:95],a[216:219],a[244:247],v[252:255]\n"
TR "v[176:177],v0\n"
CV "v222,v224,v225\n"
MF "v[92:95],a[176:179],a[240:243],v[92:95]\n"
TR "v[178:179],v0 offset:2048\n"
CV "v223,v226,v227\n"
MF "v[92:95],a[136:139],v[188:191],v[92:95]\n"
TR "v[96:97],v0 offset:49152\n"
AW "a8,v220\n"
MF "v[84:87],a[132:135],v[188:191],v[252:255]\n"
TR "v[180:181],v0 offset:4096\n"
AW "a9,v221\n"
MF "v[84:87],a[172:175],a[240:243],v[84:87]\n"
TR "v[182:183],v0 offset:6144\n"
AW "a10,v222\n"
MF "v[84:87],a[212:215],a[244:247],v[84:87]\n"
TR "v[98:99],v0 offset:51200\n"
AW "a11,v223\n"
MF "v[76:79],a[208:211],a[244:247],v[252:255]\n"
TR "v[184:185],v0 offset:8192\n"
AR "v228,a48\n"
MF "v[76:79],a[168:171],a[240:243],v[76:79]\n"
TR "v[186:187],v0 offset:10240\n"
AR "v229,a49\n"
MF "v[76:79],a[128:131],v[188:191],v[76:79]\n"
TR "v[100:101],v0 offset:53248\n"
AR "v230,a50\n"
MF "v[68:71],a[124:127],v[188:191],v[252:255]\n"
TR "v[102:103],v0 offset:55296\n"
AR "v231,a51\n"
MF "v[68:71],a[164:167],a[240:243],v[68:71]\n"
TR "v[104:105],v0 offset:57344\n"
AR "v232,a68\n"
MF "v[68:71],a[204:207],a[244:247],v[68:71]\n"
TR "v[106:107],v0 offset:59392\n"
AR "v233,a69\n"
MF "v[60:63],a[200:203],a[244:247],v[252:255]\n"
TR "v[108:109],v4\n"
AR "v234,a70\n"
MF "v[60:63],a[160:163],a[240:243],v[60:63]\n"
TR "v[110:111],v4 offset:2048\n"
AR "v235,a71\n"
MF "v[60:63],a[120:123],v[188:191],v[60:63]\n"
"s_waitcnt lgkmcnt(6)\n"
TR "v[112:113],v4 offset:4096\n"
"ds_read_b128 a[68:71],v236 offset:53248\n"
MF "v[16:19],a[140:143],v[176:179],v[16:19]\n"
TR "v[188:189],v1\n"
"v_fma_f32 v228,v228,v210,v216\n"
MF "v[16:19],a[180:183],v[180:183],v[16:19]\n"
TR "v[190:191],v1 offset:2048\n"
"v_fma_f32 v229,v229,v210,v216\n"
MF "v[16:19],a[220:223],v[184:187],v[16:19]\n"
TR "v[114:115],v4 offset:6144\n"
"v_fma_f32 v230,v230,v210,v216\n"
MF "v[24:27],a[224:227],v[184:187],v[24:27]\n"
TR "a[240:241],v1 offset:4096\n"
"v_fma_f32 v231,v231,v210,v216\n"
MF "v[24:27],a[184:187],v[180:183],v[24:27]\n"
TR "a[242:243],v1 offset:6144\n"
"v_fma_f32 v232,v232,v210,v216\n"
MF "v[24:27],a[144:147],v[176:179],v[24:27]\n"
TR "v[116:117],v4 offset:8192\n"
"v_fma_f32 v233,v233,v210,v216\n"
MF "v[32:35],a[148:151],v[176:179],v[32:35]\n"
TR "a[244:245],v1 offset:8192\n"
"v_fma_f32 v234,v234,v210,v216\n"
MF "v[32:35],a[188:191],v[180:183],v[32:35]\n"
TR "a[246:247],v1 offset:10240\n"
"v_fma_f32 v235,v235,v210,v216\n"
MF "v[32:35],a[228:231],v[184:187],v[32:35]\n"
TR "v[118:119],v4 offset:10240\n"
CV "v228,v228,v229\n"
MF "v[40:43],a[232:235],v[184:187],v[40:43]\n"
TR "v[120:121],v1 offset:49152\n"
CV "v229,v230,v231\n"
MF "v[40:43],a[192:195],v[180:183],v[40:43]\n"
TR "v[122:123],v1 offset:51200\n"
CV "v230,v232,v233\n"
MF "v[40:43],a[152:155],v[176:179],v[40:43]\n"
TR "v[124:125],v1 offset:53248\n"
CV "v231,v234,v235\n"
MF "v[48:51],a[156:159],v[176:179],v[48:51]\n"
TR "v[126:127],v1 offset:55296\n"
AW "a48,v228\n"
MF "v[48:51],a[196:199],v[180:183],v[48:51]\n"
TR "v[128:129],v1 offset:57344\n"
AW "a49,v229\n"
MF "v[48:51],a[236:239],v[184:187],v[48:51]\n"
"s_waitcnt lgkmcnt(6)\n"
TR "v[130:131],v1 offset:59392\n"
AW "a50,v230\n"
MF "v[52:55],a[236:239],a[244:247],v[52:55]\n"
TR "v[176:177],v2\n"
AW "a51,v231\n"
MF "v[52:55],a[196:199],a[240:243],v[52:55]\n"
TR "v[178:179],v2 offset:2048\n"
AR "v220,a88\n"
MF "v[52:55],a[156:159],v[188:191],v[52:55]\n"
TR "v[132:133],v5\n"
AR "v221,a89\n"
MF "v[44:47],a[152:155],v[188:191],v[44:47]\n"
TR "v[180:181],v2 offset:4096\n"
AR "v222,a90\n"
MF "v[44:47],a[192:195],a[240:243],v[44:47]\n"
TR "v[182:183],v2 offset:6144\n"
AR "v223,a91\n"
MF "v[44:47],a[232:235],a[244:247],v[44:47]\n"
TR "v[134:135],v5 offset:2048\n"
AR "v224,a108\n"
MF "v[36:39],a[228:231],a[244:247],v[36:39]\n"
TR "v[184:185],v2 offset:8192\n"
AR "v225,a109\n"
MF "v[36:39],a[188:191],a[240:243],v[36:39]\n"
TR "v[186:187],v2 offset:10240\n"
AR "v226,a110\n"
MF "v[36:39],a[148:151],v[188:191],v[36:39]\n"
TR "v[136:137],v5 offset:4096\n"
AR "v227,a111\n"
MF "v[28:31],a[144:147],v[188:191],v[28:31]\n"
"s_nop 0\n"
TR "v[138:139],v5 offset:6144\n"
"ds_read_b128 a[108:111],v237 offset:8192\n"
MF "v[28:31],a[184:187],a[240:243],v[28:31]\n"
TR "v[140:141],v5 offset:8192\n"
"v_fma_f32 v220,v220,v210,v216\n"
MF "v[28:31],a[224:227],a[244:247],v[28:31]\n"
TR "v[142:143],v5 offset:10240\n"
"v_fma_f32 v221,v221,v210,v216\n"
MF "v[20:23],a[220:223],a[244:247],v[20:23]\n"
TR "v[144:145],v2 offset:49152\n"
"v_fma_f32 v222,v222,v210,v216\n"
MF "v[20:23],a[180:183],a[240:243],v[20:23]\n"
TR "v[146:147],v2 offset:51200\n"
"v_fma_f32 v223,v223,v210,v216\n"
MF "v[20:23],a[140:143],v[188:191],v[20:23]\n"
"s_waitcnt lgkmcnt(7)\n"
TR "v[148:149],v2 offset:53248\n"
"v_fma_f32 v224,v224,v210,v216\n"
MF "v[56:59],a[140:143],v[176:179],v[56:59]\n"
TR "v[188:189],v3\n"
"v_add_u32 v198,59392,v192\n"
MF "v[56:59],a[180:183],v[180:183],v[56:59]\n"
TR "v[190:191],v3 offset:2048\n"
"v_add_u32 v199,68608,v192\n"
MF "v[56:59],a[220:223],v[184:187],v[56:59]\n"
TR "v[150:151],v2 offset:55296\n"
"v_add_u32 v200,81920,v192\n"
"v_add_u32 v201,91136,v192\n"
MF "v[64:67],a[224:227],v[184:187],v[64:67]\n"
TR "a[240:241],v3 offset:4096\n"
"v_add_u32 v202,100352,v192\n"
MF "v[64:67],a[184:187],v[180:183],v[64:67]\n"
TR "a[242:243],v3 offset:6144\n"
"v_add_u32 v203,109568,v192\n"
MF "v[64:67],a[144:147],v[176:179],v[64:67]\n"
TR "v[152:153],v2 offset:57344\n"
"v_fma_f32 v225,v225,v210,v216\n"
MF "v[72:75],a[148:151],v[176:179],v[72:75]\n"
TR "a[244:245],v3 offset:8192\n"
"v_fma_f32 v226,v226,v210,v216\n"
MF "v[72:75],a[188:191],v[180:183],v[72:75]\n"
TR "a[246:247],v3 offset:10240\n"
"v_fma_f32 v227,v227,v210,v216\n"
MF "v[72:75],a[228:231],v[184:187],v[72:75]\n"
TR "v[154:155],v2 offset:59392\n"
CV "v220,v220,v221\n"
MF "v[80:83],a[232:235],v[184:187],v[80:83]\n"
TR "v[156:157],v6\n"
CV "v221,v222,v223\n"
MF "v[80:83],a[192:195],v[180:183],v[80:83]\n"
TR "v[158:159],v6 offset:2048\n"
CV "v222,v224,v225\n"
MF "v[80:83],a[152:155],v[176:179],v[80:83]\n"
TR "v[160:161],v6 offset:4096\n"
CV "v223,v226,v227\n"
MF "v[88:91],a[156:159],v[176:179],v[88:91]\n"
TR "v[162:163],v6 offset:6144\n"
AW "a88,v220\n"
MF "v[88:91],a[196:199],v[180:183],v[88:91]\n"
TR "v[164:165],v6 offset:8192\n"
"s_waitcnt lgkmcnt(0)\n"
MF "v[88:91],a[236:239],v[184:187],v[88:91]\n"
"s_barrier\n"
MF "v[92:95],a[236:239],a[244:247],v[92:95]\n"
"s_add_u32 s80,s89,122880\n"
"s_add_u32 m0,s79,0\n"
TR "v[166:167],v6 offset:10240\n"
MF "v[92:95],a[196:199],a[240:243],v[92:95]\n"
LD "v192,s[48:51],s80 offen lds\n"
MF "v[92:95],a[156:159],v[188:191],v[92:95]\n"
TR "v[168:169],v3 offset:49152\n"
AW "a89,v221\n"
MF "v[84:87],a[152:155],v[188:191],v[84:87]\n"
TR "v[170:171],v3 offset:51200\n"
AW "a90,v222\n"
MF "v[84:87],a[192:195],a[240:243],v[84:87]\n"
TR "v[172:173],v3 offset:53248\n"
AW "a91,v223\n"
MF "v[84:87],a[232:235],a[244:247],v[84:87]\n"
LD "v193,s[48:51],s80 offen offset:1024 lds\n"
MF "v[76:79],a[228:231],a[244:247],v[76:79]\n"
TR "v[174:175],v3 offset:55296\n"
AR "v228,a12\n"
MF "v[76:79],a[188:191],a[240:243],v[76:79]\n"
TR "v[204:205],v3 offset:57344\n"
AR "v229,a13\n"
MF "v[76:79],a[148:151],v[188:191],v[76:79]\n"
TR "v[206:207],v3 offset:59392\n"
AR "v230,a14\n"
MF "v[68:71],a[144:147],v[188:191],v[68:71]\n"
TR "v[244:245],v7\n"
AR "v231,a15\n"
MF "v[68:71],a[184:187],a[240:243],v[68:71]\n"
LD "v194,s[48:51],s80 offen offset:2048 lds\n"
MF "v[68:71],a[224:227],a[244:247],v[68:71]\n"
TR "v[246:247],v7 offset:2048\n"
AR "v232,a32\n"
MF "v[60:63],a[220:223],a[244:247],v[60:63]\n"
TR "v[248:249],v7 offset:4096\n"
AR "v233,a33\n"
MF "v[60:63],a[180:183],a[240:243],v[60:63]\n"
TR "v[250:251],v7 offset:6144\n"
AR "v234,a34\n"
MF "v[60:63],a[140:143],v[188:191],v[60:63]\n"
LD "v195,s[48:51],s80 offen offset:3072 lds\n"
MF "v[16:19],a[0:3],v[96:99],v[16:19]\n"
TR "v[238:239],v7 offset:8192\n"
AR "v235,a35\n"
MF "v[16:19],a[40:43],v[100:103],v[16:19]\n"
"s_nop 0\n"
TR "v[240:241],v7 offset:10240\n"
"ds_read_b128 a[32:35],v236 offset:36864\n"
MF "v[16:19],a[80:83],v[104:107],v[16:19]\n"
"v_fma_f32 v228,v228,v211,v217\n"
"v_fma_f32 v229,v229,v211,v217\n"
MF "v[16:19],a[20:23],v[108:111],v[16:19]\n"
"s_add_u32 m0,s79,4096\n"
"v_fma_f32 v230,v230,v211,v217\n"
"v_fma_f32 v231,v231,v211,v217\n"
MF "v[16:19],a[60:63],v[112:115],v[16:19]\n"
".Lpg25h:\n"
"s_cbranch_execz .Lpg29h\n"
LD "v196,s[48:51],s80 offen lds\n"
MF "v[16:19],a[100:103],v[116:119],v[16:19]\n"
"v_fma_f32 v232,v232,v211,v217\n"
"v_fma_f32 v233,v233,v211,v217\n"
MF "v[20:23],a[100:103],v[140:143],v[20:23]\n"
"v_fma_f32 v234,v234,v211,v217\n"
"v_fma_f32 v235,v235,v211,v217\n"
MF "v[20:23],a[60:63],v[136:139],v[20:23]\n"
CV "v228,v228,v229\n"
CV "v229,v230,v231\n"
MF "v[20:23],a[20:23],v[132:135],v[20:23]\n"
LD "v197,s[48:51],s80 offen offset:1024 lds\n"
MF "v[20:23],a[80:83],v[128:131],v[20:23]\n"
CV "v230,v232,v233\n"
CV "v231,v234,v235\n"
MF "v[20:23],a[40:43],v[124:127],v[20:23]\n"
AW "a12,v228\n"
AW "a13,v229\n"
MF "v[20:23],a[0:3],v[120:123],v[20:23]\n"
"s_waitcnt lgkmcnt(13)\n"
AW "a14,v230\n"
AW "a15,v231\n"
MF "v[56:59],a[0:3],v[144:147],v[56:59]\n"
AR "v220,a52\n"
AR "v221,a53\n"
MF "v[56:59],a[40:43],v[148:151],v[56:59]\n"
LD "v198,s[48:51],s80 offen offset:2048 lds\n"
MF "v[56:59],a[80:83],v[152:155],v[56:59]\n"
AR "v222,a54\n"
AR "v223,a55\n"
MF "v[56:59],a[20:23],v[156:159],v[56:59]\n"
AR "v224,a72\n"
AR "v225,a73\n"
MF "v[56:59],a[60:63],v[160:163],v[56:59]\n"
AR "v226,a74\n"
AR "v227,a75\n"
MF "v[56:59],a[100:103],v[164:167],v[56:59]\n"
"s_waitcnt lgkmcnt(1)\n"
"ds_read_b128 a[72:75],v236 offset:57344\n"
"v_fma_f32 v220,v220,v211,v217\n"
MF "v[60:63],a[100:103],v[238:241],v[60:63]\n"
LD "v199,s[48:51],s80 offen offset:3072 lds\n"
MF "v[60:63],a[60:63],v[248:251],v[60:63]\n"
"v_fma_f32 v221,v221,v211,v217\n"
"v_fma_f32 v222,v222,v211,v217\n"
MF "v[60:63],a[20:23],v[244:247],v[60:63]\n"
"v_fma_f32 v223,v223,v211,v217\n"
"v_fma_f32 v224,v224,v211,v217\n"
MF "v[60:63],a[80:83],v[204:207],v[60:63]\n"
"s_add_u32 m0,s79,8192\n"
"v_fma_f32 v225,v225,v211,v217\n"
"v_fma_f32 v226,v226,v211,v217\n"
MF "v[60:63],a[40:43],v[172:175],v[60:63]\n"
LD "v200,s[48:51],s80 offen lds\n"
MF "v[60:63],a[0:3],v[168:171],v[60:63]\n"
"v_fma_f32 v227,v227,v211,v217\n"
CV "v220,v220,v221\n"
MF "v[68:71],a[4:7],v[168:171],v[68:71]\n"
CV "v221,v222,v223\n"
CV "v222,v224,v225\n"
MF "v[68:71],a[44:47],v[172:175],v[68:71]\n"
CV "v223,v226,v227\n"
AW "a52,v220\n"
MF "v[68:71],a[84:87],v[204:207],v[68:71]\n"
AW "a53,v221\n"
AW "a54,v222\n"
MF "v[68:71],a[24:27],v[244:247],v[68:71]\n"
LD "v201,s[48:51],s80 offen offset:1024 lds\n"
MF "v[68:71],a[64:67],v[248:251],v[68:71]\n"
AW "a55,v223\n"
AR "v228,a92\n"
MF "v[68:71],a[104:107],v[238:241],v[68:71]\n"
"s_waitcnt lgkmcnt(0)\n"
AR "v229,a93\n"
AR "v230,a94\n"
MF "v[64:67],a[104:107],v[164:167],v[64:67]\n"
"s_barrier\n"
MF "v[64:67],a[64:67],v[160:163],v[64:67]\n"
LD "v202,s[48:51],s80 offen offset:2048 lds\n"
MF "v[64:67],a[24:27],v[156:159],v[64:67]\n"
AR "v231,a95\n"
AR "v232,a112\n"
MF "v[64:67],a[84:87],v[152:155],v[64:67]\n"
AR "v233,a113\n"
AR "v234,a114\n"
MF "v[64:67],a[44:47],v[148:151],v[64:67]\n"
AR "v235,a115\n"
"v_fma_f32 v228,v228,v211,v217\n"
MF "v[64:67],a[4:7],v[144:147],v[64:67]\n"
"ds_read_b128 a[112:115],v237 offset:12288\n"
"v_fma_f32 v229,v229,v211,v217\n"
MF "v[28:31],a[4:7],v[120:123],v[28:31]\n"
LD "v203,s[48:51],s80 offen offset:3072 lds\n"
MF "v[28:31],a[44:47],v[124:127],v[28:31]\n"
"v_fma_f32 v230,v230,v211,v217\n"
"v_fma_f32 v231,v231,v211,v217\n"
MF "v[28:31],a[84:87],v[128:131],v[28:31]\n"
"v_fma_f32 v232,v232,v211,v217\n"
"v_fma_f32 v233,v233,v211,v217\n"
MF "v[28:31],a[24:27],v[132:135],v[28:31]\n"
"v_fma_f32 v234,v234,v211,v217\n"
"v_fma_f32 v235,v235,v211,v217\n"
MF "v[28:31],a[64:67],v[136:139],v[28:31]\n"
CV "v228,v228,v229\n"
CV "v229,v230,v231\n"
MF "v[28:31],a[104:107],v[140:143],v[28:31]\n"
"s_add_u32 s88,s88,s65\n"
"s_add_u32 m0,s79,49152\n"
CV "v230,v232,v233\n"
MF "v[24:27],a[104:107],v[116:119],v[24:27]\n"
LD "v192,s[48:51],s88 offen lds\n"
MF "v[24:27],a[64:67],v[112:115],v[24:27]\n"
CV "v231,v234,v235\n"
AW "a92,v228\n"
MF "v[24:27],a[24:27],v[108:111],v[24:27]\n"
AW "a93,v229\n"
AW "a94,v230\n"
MF "v[24:27],a[84:87],v[104:107],v[24:27]\n"
AW "a95,v231\n"
AR "v220,a16\n"
MF "v[24:27],a[44:47],v[100:103],v[24:27]\n"
LD "v193,s[48:51],s88 offen offset:1024 lds\n"
MF "v[24:27],a[4:7],v[96:99],v[24:27]\n"
AR "v221,a17\n"
AR "v222,a18\n"
MF "v[32:35],a[8:11],v[96:99],v[32:35]\n"
AR "v223,a19\n"
AR "v224,a36\n"
MF "v[32:35],a[48:51],v[100:103],v[32:35]\n"
AR "v225,a37\n"
AR "v226,a38\n"
MF "v[32:35],a[88:91],v[104:107],v[32:35]\n"
AR "v227,a39\n"
"v_fma_f32 v220,v220,v212,v218\n"
MF "v[32:35],a[28:31],v[108:111],v[32:35]\n"
LD "v194,s[48:51],s88 offen offset:2048 lds\n"
MF "v[32:35],a[68:71],v[112:115],v[32:35]\n"
"ds_read_b128 a[36:39],v236 offset:40960\n"
"v_fma_f32 v221,v221,v212,v218\n"
MF "v[32:35],a[108:111],v[116:119],v[32:35]\n"
"v_fma_f32 v222,v222,v212,v218\n"
"v_fma_f32 v223,v223,v212,v218\n"
MF "v[36:39],a[108:111],v[140:143],v[36:39]\n"
"v_fma_f32 v224,v224,v212,v218\n"
"v_fma_f32 v225,v225,v212,v218\n"
MF "v[36:39],a[68:71],v[136:139],v[36:39]\n"
"v_fma_f32 v226,v226,v212,v218\n"
"v_fma_f32 v227,v227,v212,v218\n"
MF "v[36:39],a[28:31],v[132:135],v[36:39]\n"
LD "v195,s[48:51],s88 offen offset:3072 lds\n"
MF "v[36:39],a[88:91],v[128:131],v[36:39]\n"
CV "v220,v220,v221\n"
CV "v221,v222,v223\n"
MF "v[36:39],a[48:51],v[124:127],v[36:39]\n"
CV "v222,v224,v225\n"
CV "v223,v226,v227\n"
MF "v[36:39],a[8:11],v[120:123],v[36:39]\n"
AW "a16,v220\n"
AW "a17,v221\n"
MF "v[72:75],a[8:11],v[144:147],v[72:75]\n"
"s_add_u32 m0,s79,53248\n"
AW "a18,v222\n"
AW "a19,v223\n"
MF "v[72:75],a[48:51],v[148:151],v[72:75]\n"
LD "v196,s[48:51],s88 offen lds\n"
MF "v[72:75],a[88:91],v[152:155],v[72:75]\n"
AR "v228,a56\n"
AR "v229,a57\n"
MF "v[72:75],a[28:31],v[156:159],v[72:75]\n"
AR "v230,a58\n"
AR "v231,a59\n"
MF "v[72:75],a[68:71],v[160:163],v[72:75]\n"
AR "v232,a76\n"
AR "v233,a77\n"
MF "v[72:75],a[108:111],v[164:167],v[72:75]\n"
AR "v234,a78\n"
AR "v235,a79\n"
MF "v[76:79],a[108:111],v[238:241],v[76:79]\n"
LD "v197,s[48:51],s88 offen offset:1024 lds\n"
MF "v[76:79],a[68:71],v[248:251],v[76:79]\n"
"ds_read_b128 a[76:79],v236 offset:61440\n"
"v_fma_f32 v228,v228,v212,v218\n"
MF "v[76:79],a[28:31],v[244:247],v[76:79]\n"
"v_fma_f32 v229,v229,v212,v218\n"
"v_fma_f32 v230,v230,v212,v218\n"
MF "v[76:79],a[88:91],v[204:207],v[76:79]\n"
"v_fma_f32 v231,v231,v212,v218\n"
"v_fma_f32 v232,v232,v212,v218\n"
MF "v[76:79],a[48:51],v[172:175],v[76:79]\n"
"v_fma_f32 v233,v233,v212,v218\n"
"v_fma_f32 v234,v234,v212,v218\n"
MF "v[76:79],a[8:11],v[168:171],v[76:79]\n"
"s_waitcnt lgkmcnt(2)\n"
LD "v198,s[48:51],s88 offen offset:2048 lds\n"
MF "v[84:87],a[12:15],v[168:171],v[84:87]\n"
"v_fma_f32 v235,v235,v212,v218\n"
CV "v228,v228,v229\n"
MF "v[84:87],a[52:55],v[172:175],v[84:87]\n"
CV "v229,v230,v231\n"
CV "v230,v232,v233\n"
MF "v[84:87],a[92:95],v[204:207],v[84:87]\n"
CV "v231,v234,v235\n"
AW "a56,v228\n"
MF "v[84:87],a[32:35],v[244:247],v[84:87]\n"
AW "a57,v229\n"
AW "a58,v230\n"
MF "v[84:87],a[72:75],v[248:251],v[84:87]\n"
LD "v199,s[48:51],s88 offen offset:3072 lds\n"
MF "v[84:87],a[112:115],v[238:241],v[84:87]\n"
AW "a59,v231\n"
AR "v220,a96\n"
MF "v[80:83],a[112:115],v[164:167],v[80:83]\n"
AR "v221,a97\n"
AR "v222,a98\n"
MF "v[80:83],a[72:75],v[160:163],v[80:83]\n"
AR "v223,a99\n"
AR "v224,a116\n"
MF "v[80:83],a[32:35],v[156:159],v[80:83]\n"
"s_add_u32 m0,s79,57344\n"
AR "v225,a117\n"
AR "v226,a118\n"
MF "v[80:83],a[92:95],v[152:155],v[80:83]\n"
LD "v200,s[48:51],s88 offen lds\n"
MF "v[80:83],a[52:55],v[148:151],v[80:83]\n"
AR "v227,a119\n"
"v_fma_f32 v220,v220,v212,v218\n"
MF "v[80:83],a[12:15],v[144:147],v[80:83]\n"
"ds_read_b128 a[116:119],v237 offset:16384\n"
"v_fma_f32 v221,v221,v212,v218\n"
MF "v[44:47],a[12:15],v[120:123],v[44:47]\n"
"v_fma_f32 v222,v222,v212,v218\n"
"v_fma_f32 v223,v223,v212,v218\n"
MF "v[44:47],a[52:55],v[124:127],v[44:47]\n"
"v_fma_f32 v224,v224,v212,v218\n"
"v_fma_f32 v225,v225,v212,v218\n"
MF "v[44:47],a[92:95],v[128:131],v[44:47]\n"
LD "v201,s[48:51],s88 offen offset:1024 lds\n"
MF "v[44:47],a[32:35],v[132:135],v[44:47]\n"
"v_fma_f32 v226,v226,v212,v218\n"
"v_fma_f32 v227,v227,v212,v218\n"
MF "v[44:47],a[72:75],v[136:139],v[44:47]\n"
CV "v220,v220,v221\n"
CV "v221,v222,v223\n"
MF "v[44:47],a[112:115],v[140:143],v[44:47]\n"
CV "v222,v224,v225\n"
CV "v223,v226,v227\n"
MF "v[40:43],a[112:115],v[116:119],v[40:43]\n"
AW "a96,v220\n"
AW "a97,v221\n"
MF "v[40:43],a[72:75],v[112:115],v[40:43]\n"
LD "v202,s[48:51],s88 offen offset:2048 lds\n"
MF "v[40:43],a[32:35],v[108:111],v[40:43]\n"
AW "a98,v222\n"
AW "a99,v223\n"
MF "v[40:43],a[92:95],v[104:107],v[40:43]\n"
AR "v8,a252\n"
AR "v11,a253\n"
MF "v[40:43],a[52:55],v[100:103],v[40:43]\n"
"v_bfe_u32 v224,v9,5,3\n"
"v_bfe_u32 v225,v9,8,2\n"
MF "v[40:43],a[12:15],v[96:99],v[40:43]\n"
"s_waitcnt lgkmcnt(0)\n"
"v_lshlrev_b32 v225,1,v225\n"
"v_and_b32 v226,16,v9\n"
MF "v[48:51],a[16:19],v[96:99],v[48:51]\n"
LD "v203,s[48:51],s88 offen offset:3072 lds\n"
MF "v[48:51],a[56:59],v[100:103],v[48:51]\n"
"v_lshlrev_b32_e64 v226,v226,s9\n"
"v_sub_u32 v224,v224,v225\n"
MF "v[48:51],a[96:99],v[104:107],v[48:51]\n"
"v_cmp_eq_u32 vcc,0,v224\n"
MF "v[48:51],a[36:39],v[108:111],v[48:51]\n"
"s_nop 0\n"
"v_cndmask_b32 v220,0,v226,vcc\n"
"v_cmp_eq_u32 vcc,1,v224\n"
MF "v[48:51],a[76:79],v[112:115],v[48:51]\n"
"s_nop 0\n"
"v_cndmask_b32 v221,0,v226,vcc\n"
AW "a248,0\n"
MF "v[48:51],a[116:119],v[116:119],v[48:51]\n"
AW "a249,0\n"
AW "a250,v220\n"
MF "v[52:55],a[116:119],v[140:143],v[52:55]\n"
AW "a251,v221\n"
AW "a252,v220\n"
MF "v[52:55],a[76:79],v[136:139],v[52:55]\n"
"s_waitcnt lgkmcnt(0)\n"
AW "a253,v221\n"
AW "a254,0\n"
MF "v[52:55],a[36:39],v[132:135],v[52:55]\n"
"s_barrier\n"
MF "v[52:55],a[96:99],v[128:131],v[52:55]\n"
"s_add_u32 s80,s88,122880\n"
TR "v[176:177],v0 offset:24576\n"
AW "a255,0\n"
MF "v[52:55],a[56:59],v[124:127],v[52:55]\n"
"s_add_u32 m0,s79,98304\n"
TR "v[178:179],v0 offset:26624\n"
"v_bfe_u32 v12,v9,4,4\n"
MF "v[52:55],a[16:19],v[120:123],v[52:55]\n"
LD "v192,s[48:51],s80 offen lds\n"
MF "v[88:91],a[16:19],v[144:147],v[88:91]\n"
"s_nop 0\n"
TR "v[180:181],v0 offset:28672\n"
"ds_write_addtid_b32 v16 offset:24576\n"
MF "v[88:91],a[56:59],v[148:151],v[88:91]\n"
"s_nop 0\n"
TR "v[182:183],v0 offset:30720\n"
"ds_write_addtid_b32 v17 offset:24832\n"
MF "v[88:91],a[96:99],v[152:155],v[88:91]\n"
LD "v193,s[48:51],s80 offen offset:1024 lds\n"
MF "v[88:91],a[36:39],v[156:159],v[88:91]\n"
"s_nop 0\n"
TR "v[184:185],v0 offset:32768\n"
"ds_write_addtid_b32 v18 offset:25088\n"
MF "v[88:91],a[76:79],v[160:163],v[88:91]\n"
"s_nop 0\n"
TR "v[186:187],v0 offset:34816\n"
"ds_write_addtid_b32 v19 offset:25344\n"
MF "v[88:91],a[116:119],v[164:167],v[88:91]\n"
"s_nop 0\n"
"ds_write_addtid_b32 v20 offset:25600\n"
"ds_write_addtid_b32 v21 offset:25856\n"
MF "v[92:95],a[116:119],v[238:241],v[92:95]\n"
LD "v194,s[48:51],s80 offen offset:2048 lds\n"
MF "v[92:95],a[76:79],v[248:251],v[92:95]\n"
"s_nop 0\n"
"ds_write_addtid_b32 v22 offset:26112\n"
"ds_write_addtid_b32 v23 offset:26368\n"
MF "v[92:95],a[36:39],v[244:247],v[92:95]\n"
"s_nop 0\n"
"ds_write_addtid_b32 v24 offset:26624\n"
"ds_write_addtid_b32 v25 offset:26880\n"
MF "v[92:95],a[96:99],v[204:207],v[92:95]\n"
"s_nop 0\n"
"ds_write_addtid_b32 v26 offset:27136\n"
"ds_write_addtid_b32 v27 offset:27392\n"
MF "v[92:95],a[56:59],v[172:175],v[92:95]\n"
"v_lshlrev_b32 v12,2,v12\n"
"v_lshrrev_b32 v13,2,v9\n"
MF "v[92:95],a[16:19],v[168:171],v[92:95]\n"
"s_waitcnt lgkmcnt(9)\n"
"v_add_u32 v13,s58,v13\n"
"v_add_u32 v4,24576,v4\n"
"v_add_u32 v5,24576,v5\n"
"v_add_u32 v6,24576,v6\n"
"v_add_u32 v7,24576,v7\n"
".Lpl05:\n"
MF "v[96:99],a[0:3],v[176:179],v[252:255]\n"
"s_nop 0\n"
TR "v[188:189],v1 offset:24576\n"
"ds_write_addtid_b32 v28 offset:27648\n"
MF "v[96:99],a[40:43],v[180:183],v[96:99]\n"
"s_nop 0\n"
TR "v[190:191],v1 offset:26624\n"
"ds_write_addtid_b32 v29 offset:27904\n"
MF "v[96:99],a[80:83],v[184:187],v[96:99]\n"
LD "v195,s[48:51],s80 offen offset:3072 lds\n"
MF "v[104:107],a[84:87],v[184:187],v[252:255]\n"
"s_add_u32 s60,s64,1\n"
TR "a[240:241],v1 offset:28672\n"
"ds_write_addtid_b32 v30 offset:28160\n"
MF "v[104:107],a[44:47],v[180:183],v[104:107]\n"
"s_add_u32 m0,s79,102400\n"
TR "a[242:243],v1 offset:30720\n"
"ds_write_addtid_b32 v31 offset:24320\n"
MF "v[104:107],a[4:7],v[176:179],v[104:107]\n"
LD "v196,s[48:51],s80 offen lds\n"
MF "v[112:115],a[8:11],v[176:179],v[252:255]\n"
"s_cmp_eq_u32 s60,8\n"
"s_cselect_b32 s60,0,s60\n"
TR "a[244:245],v1 offset:32768\n"
MF "v[112:115],a[48:51],v[180:183],v[112:115]\n"
"s_cmp_eq_u32 s60,10\n"
"s_cselect_b32 s60,s61,s60\n"
TR "a[246:247],v1 offset:34816\n"
MF "v[112:115],a[88:91],v[184:187],v[112:115]\n"
"s_sub_u32 s65,s60,s64\n"
"ds_write_addtid_b32 v32 offset:24576\n"
"ds_write_addtid_b32 v33 offset:24832\n"
MF "v[120:123],a[92:95],v[184:187],v[252:255]\n"
"s_lshl_b32 s65,s65,7\n"
"ds_write_addtid_b32 v34 offset:25088\n"
"ds_write_addtid_b32 v35 offset:25344\n"
MF "v[120:123],a[52:55],v[180:183],v[120:123]\n"
LD "v197,s[48:51],s80 offen offset:1024 lds\n"
MF "v[120:123],a[12:15],v[176:179],v[120:123]\n"
"s_mov_b32 s64,s60\n"
"ds_write_addtid_b32 v36 offset:25600\n"
"ds_write_addtid_b32 v37 offset:25856\n"
MF "v[128:131],a[16:19],v[176:179],v[252:255]\n"
"s_nop 0\n"
"ds_write_addtid_b32 v38 offset:26112\n"
"ds_write_addtid_b32 v39 offset:26368\n"
MF "v[128:131],a[56:59],v[180:183],v[128:131]\n"
"s_nop 0\n"
"ds_write_addtid_b32 v40 offset:26624\n"
"ds_write_addtid_b32 v41 offset:26880\n"
MF "v[128:131],a[96:99],v[184:187],v[128:131]\n"
"s_waitcnt lgkmcnt(10)\n"
LD "v198,s[48:51],s80 offen offset:2048 lds\n"
MF "v[132:135],a[96:99],a[244:247],v[252:255]\n"
"s_nop 0\n"
TR "v[176:177],v2 offset:24576\n"
"ds_write_addtid_b32 v42 offset:27136\n"
MF "v[132:135],a[56:59],a[240:243],v[132:135]\n"
"s_nop 0\n"
TR "v[178:179],v2 offset:26624\n"
"ds_write_addtid_b32 v43 offset:27392\n"
MF "v[132:135],a[16:19],v[188:191],v[132:135]\n"
"s_nop 0\n"
"ds_write_addtid_b32 v44 offset:27648\n"
"ds_write_addtid_b32 v45 offset:27904\n"
MF "v[124:127],a[12:15],v[188:191],v[252:255]\n"
"s_nop 0\n"
TR "v[180:181],v2 offset:28672\n"
"ds_write_addtid_b32 v46 offset:28160\n"
MF "v[124:127],a[52:55],a[240:243],v[124:127]\n"
"s_nop 0\n"
TR "v[182:183],v2 offset:30720\n"
"ds_write_addtid_b32 v47 offset:28416\n"
MF "v[124:127],a[92:95],a[244:247],v[124:127]\n"
LD "v199,s[48:51],s80 offen offset:3072 lds\n"
MF "v[116:119],a[88:91],a[244:247],v[252:255]\n"
"s_nop 0\n"
TR "v[184:185],v2 offset:32768\n"
"ds_write_addtid_b32 v48 offset:28672\n"
MF "v[116:119],a[48:51],a[240:243],v[116:119]\n"
"s_add_u32 m0,s79,106496\n"
TR "v[186:187],v2 offset:34816\n"
"ds_write_addtid_b32 v49 offset:24832\n"
MF "v[116:119],a[8:11],v[188:191],v[116:119]\n"
LD "v200,s[48:51],s80 offen lds\n"
MF "v[108:111],a[4:7],v[188:191],v[252:255]\n"
"s_nop 0\n"
"ds_write_addtid_b32 v50 offset:25088\n"
"ds_write_addtid_b32 v51 offset:25344\n"
MF "v[108:111],a[44:47],a[240:243],v[108:111]\n"
"s_nop 0\n"
"ds_write_addtid_b32 v52 offset:25600\n"
"ds_write_addtid_b32 v53 offset:25856\n"
MF "v[108:111],a[84:87],a[244:247],v[108:111]\n"
"s_nop 0\n"
"ds_write_addtid_b32 v54 offset:26112\n"
"ds_write_addtid_b32 v55 offset:26368\n"
MF "v[100:103],a[80:83],a[244:247],v[252:255]\n"
MF "v[100:103],a[40:43],a[240:243],v[100:103]\n"
LD "v201,s[48:51],s80 offen offset:1024 lds\n"
MF "v[100:103],a[0:3],v[188:191],v[100:103]\n"
"s_waitcnt lgkmcnt(7)\n"
MF "v[136:139],a[0:3],v[176:179],v[252:255]\n"
TR "v[188:189],v3 offset:24576\n"
MF "v[136:139],a[40:43],v[180:183],v[136:139]\n"
TR "v[190:191],v3 offset:26624\n"
MF "v[136:139],a[80:83],v[184:187],v[136:139]\n"
LD "v202,s[48:51],s80 offen offset:2048 lds\n"
MF "v[144:147],a[84:87],v[184:187],v[252:255]\n"
TR "a[240:241],v3 offset:28672\n"
MF "v[144:147],a[44:47],v[180:183],v[144:147]\n"
TR "a[242:243],v3 offset:30720\n"
MF "v[144:147],a[4:7],v[176:179],v[144:147]\n"
LD "v203,s[48:51],s80 offen offset:3072 lds\n"
MF "v[152:155],a[8:11],v[176:179],v[252:255]\n"
TR "a[244:245],v3 offset:32768\n"
MF "v[152:155],a[48:51],v[180:183],v[152:155]\n"
TR "a[246:247],v3 offset:34816\n"
MF "v[152:155],a[88:91],v[184:187],v[152:155]\n"
MF "v[160:163],a[92:95],v[184:187],v[252:255]\n"
MF "v[160:163],a[52:55],v[180:183],v[160:163]\n"
MF "v[160:163],a[12:15],v[176:179],v[160:163]\n"
MF "v[168:171],a[16:19],v[176:179],v[252:255]\n"
MF "v[168:171],a[56:59],v[180:183],v[168:171]\n"
"s_waitcnt vmcnt(24) lgkmcnt(0)\n"
MF "v[168:171],a[96:99],v[184:187],v[168:171]\n"
"s_barrier\n"
MF "v[172:175],a[96:99],a[244:247],v[252:255]\n"
"s_add_u32 s89,s89,s65\n"
TR "v[176:177],v0\n"
MF "v[172:175],a[56:59],a[240:243],v[172:175]\n"
"s_add_u32 m0,s79,24576\n"
TR "v[178:179],v0 offset:2048\n"
MF "v[172:175],a[16:19],v[188:191],v[172:175]\n"
LD "v192,s[48:51],s89 offen lds\n"
MF "v[164:167],a[12:15],v[188:191],v[252:255]\n"
TR "v[180:181],v0 offset:4096\n"
MF "v[164:167],a[52:55],a[240:243],v[164:167]\n"
TR "v[182:183],v0 offset:6144\n"
MF "v[164:167],a[92:95],a[244:247],v[164:167]\n"
LD "v193,s[48:51],s89 offen offset:1024 lds\n"
MF "v[156:159],a[88:91],a[244:247],v[252:255]\n"
TR "v[184:185],v0 offset:8192\n"
MF "v[156:159],a[48:51],a[240:243],v[156:159]\n"
"s_nop 0\n"
TR "v[186:187],v0 offset:10240\n"
"ds_write_addtid_b32 v56 offset:49152\n"
MF "v[156:159],a[8:11],v[188:191],v[156:159]\n"
"s_nop 0\n"
"ds_write_addtid_b32 v57 offset:49408\n"
"ds_write_addtid_b32 v58 offset:49664\n"
MF "v[148:151],a[4:7],v[188:191],v[252:255]\n"
"s_nop 0\n"
"ds_write_addtid_b32 v59 offset:49920\n"
"ds_write_addtid_b32 v60 offset:50176\n"
MF "v[148:151],a[44:47],a[240:243],v[148:151]\n"
LD "v194,s[48:51],s89 offen offset:2048 lds\n"
MF "v[148:151],a[84:87],a[244:247],v[148:151]\n"
"s_nop 0\n"
"ds_write_addtid_b32 v61 offset:50432\n"
"ds_write_addtid_b32 v62 offset:50688\n"
MF "v[140:143],a[80:83],a[244:247],v[252:255]\n"
"s_nop 0\n"
"ds_write_addtid_b32 v63 offset:50944\n"
"ds_write_addtid_b32 v64 offset:51200\n"
MF "v[140:143],a[40:43],a[240:243],v[140:143]\n"
"s_nop 0\n"
"ds_write_addtid_b32 v65 offset:51456\n"
"ds_write_addtid_b32 v66 offset:51712\n"
MF "v[140:143],a[0:3],v[188:191],v[140:143]\n"
"s_waitcnt lgkmcnt(11)\n"
LD "v195,s[48:51],s89 offen offset:3072 lds\n"
MF "v[96:99],a[120:123],v[176:179],v[96:99]\n"
"s_nop 0\n"
TR "v[188:189],v1\n"
"ds_write_addtid_b32 v67 offset:51968\n"
MF "v[96:99],a[160:163],v[180:183],v[96:99]\n"
"s_nop 0\n"
TR "v[190:191],v1 offset:2048\n"
"ds_write_addtid_b32 v68 offset:52224\n"
MF "v[96:99],a[200:203],v[184:187],v[96:99]\n"
"s_nop 0\n"
"ds_write_addtid_b32 v69 offset:52480\n"
"ds_write_addtid_b32 v70 offset:52736\n"
MF "v[104:107],a[204:207],v[184:187],v[104:107]\n"
"s_nop 0\n"
TR "a[240:241],v1 offset:4096\n"
"ds_write_addtid_b32 v71 offset:52992\n"
MF "v[104:107],a[164:167],v[180:183],v[104:107]\n"
"s_add_u32 m0,s79,28672\n"
TR "a[242:243],v1 offset:6144\n"
"ds_write_addtid_b32 v72 offset:49152\n"
MF "v[104:107],a[124:127],v[176:179],v[104:107]\n"
LD "v196,s[48:51],s89 offen lds\n"
MF "v[112:115],a[128:131],v[176:179],v[112:115]\n"
"s_nop 0\n"
TR "a[244:245],v1 offset:8192\n"
"ds_write_addtid_b32 v73 offset:49408\n"
MF "v[112:115],a[168:171],v[180:183],v[112:115]\n"
".Lpg26h:\n"
"s_cbranch_execz .Lpg30h\n"
"s_nop 0\n"
TR "a[246:247],v1 offset:10240\n"
"ds_write_addtid_b32 v74 offset:49664\n"
MF "v[112:115],a[208:211],v[184:187],v[112:115]\n"
LD "v197,s[48:51],s89 offen offset:1024 lds\n"
MF "v[120:123],a[212:215],v[184:187],v[120:123]\n"
"s_nop 0\n"
"ds_write_addtid_b32 v75 offset:49920\n"
"ds_write_addtid_b32 v76 offset:50176\n"
MF "v[120:123],a[172:175],v[180:183],v[120:123]\n"
"s_nop 0\n"
"ds_write_addtid_b32 v77 offset:50432\n"
"ds_write_addtid_b32 v78 offset:50688\n"
MF "v[120:123],a[132:135],v[176:179],v[120:123]\n"
"s_nop 0\n"
"ds_write_addtid_b32 v79 offset:50944\n"
"ds_write_addtid_b32 v80 offset:51200\n"
MF "v[128:131],a[136:139],v[176:179],v[128:131]\n"
"s_nop 0\n"
"ds_write_addtid_b32 v81 offset:51456\n"
"ds_write_addtid_b32 v82 offset:51712\n"
MF "v[128:131],a[176:179],v[180:183],v[128:131]\n"
LD "v198,s[48:51],s89 offen offset:2048 lds\n"
MF "v[128:131],a[216:219],v[184:187],v[128:131]\n"
"s_waitcnt lgkmcnt(9)\n"
"ds_write_addtid_b32 v83 offset:51968\n"
"ds_write_addtid_b32 v84 offset:52224\n"
MF "v[132:135],a[216:219],a[244:247],v[132:135]\n"
"s_nop 0\n"
TR "v[176:177],v2\n"
"ds_write_addtid_b32 v85 offset:52480\n"
MF "v[132:135],a[176:179],a[240:243],v[132:135]\n"
"s_nop 0\n"
TR "v[178:179],v2 offset:2048\n"
"ds_write_addtid_b32 v86 offset:52736\n"
MF "v[132:135],a[136:139],v[188:191],v[132:135]\n"
LD "v199,s[48:51],s89 offen offset:3072 lds\n"
MF "v[124:127],a[132:135],v[188:191],v[124:127]\n"
"s_nop 0\n"
TR "v[180:181],v2 offset:4096\n"
"ds_write_addtid_b32 v87 offset:52992\n"
MF "v[124:127],a[172:175],a[240:243],v[124:127]\n"
"s_nop 0\n"
TR "v[182:183],v2 offset:6144\n"
"ds_write_addtid_b32 v88 offset:53248\n"
MF "v[124:127],a[212:215],a[244:247],v[124:127]\n"
"s_nop 0\n"
"ds_write_addtid_b32 v89 offset:53504\n"
"ds_write_addtid_b32 v90 offset:53760\n"
MF "v[116:119],a[208:211],a[244:247],v[116:119]\n"
"s_nop 0\n"
TR "v[184:185],v2 offset:8192\n"
"ds_write_addtid_b32 v91 offset:54016\n"
MF "v[116:119],a[168:171],a[240:243],v[116:119]\n"
"s_add_u32 m0,s79,32768\n"
TR "v[186:187],v2 offset:10240\n"
"ds_write_addtid_b32 v92 offset:50176\n"
MF "v[116:119],a[128:131],v[188:191],v[116:119]\n"
LD "v200,s[48:51],s89 offen lds\n"
MF "v[108:111],a[124:127],v[188:191],v[108:111]\n"
"s_nop 0\n"
"ds_write_addtid_b32 v93 offset:50432\n"
"ds_write_addtid_b32 v94 offset:50688\n"
MF "v[108:111],a[164:167],a[240:243],v[108:111]\n"
"ds_write_addtid_b32 v95 offset:50944\n"
MF "v[108:111],a[204:207],a[244:247],v[108:111]\n"
MF "v[100:103],a[200:203],a[244:247],v[100:103]\n"
LD "v201,s[48:51],s89 offen offset:1024 lds\n"
MF "v[100:103],a[160:163],a[240:243],v[100:103]\n"
MF "v[100:103],a[120:123],v[188:191],v[100:103]\n"
"s_waitcnt lgkmcnt(4)\n"
MF "v[136:139],a[120:123],v[176:179],v[136:139]\n"
TR "v[188:189],v3\n"
MF "v[136:139],a[160:163],v[180:183],v[136:139]\n"
TR "v[190:191],v3 offset:2048\n"
MF "v[136:139],a[200:203],v[184:187],v[136:139]\n"
LD "v202,s[48:51],s89 offen offset:2048 lds\n"
MF "v[144:147],a[204:207],v[184:187],v[144:147]\n"
TR "a[240:241],v3 offset:4096\n"
MF "v[144:147],a[164:167],v[180:183],v[144:147]\n"
TR "a[242:243],v3 offset:6144\n"
MF "v[144:147],a[124:127],v[176:179],v[144:147]\n"
LD "v203,s[48:51],s89 offen offset:3072 lds\n"
MF "v[152:155],a[128:131],v[176:179],v[152:155]\n"
TR "a[244:245],v3 offset:8192\n"
MF "v[152:155],a[168:171],v[180:183],v[152:155]\n"
TR "a[246:247],v3 offset:10240\n"
MF "v[152:155],a[208:211],v[184:187],v[152:155]\n"
MF "v[160:163],a[212:215],v[184:187],v[160:163]\n"
MF "v[160:163],a[172:175],v[180:183],v[160:163]\n"
MF "v[160:163],a[132:135],v[176:179],v[160:163]\n"
MF "v[168:171],a[136:139],v[176:179],v[168:171]\n"
MF "v[168:171],a[176:179],v[180:183],v[168:171]\n"
"s_waitcnt vmcnt(24) lgkmcnt(0)\n"
MF "v[168:171],a[216:219],v[184:187],v[168:171]\n"
"s_barrier\n"
MF "v[172:175],a[216:219],a[244:247],v[172:175]\n"
"s_add_u32 s80,s89,122880\n"
TR "v[176:177],v0 offset:49152\n"
MF "v[172:175],a[176:179],a[240:243],v[172:175]\n"
"s_add_u32 m0,s79,0\n"
TR "v[178:179],v0 offset:51200\n"
MF "v[172:175],a[136:139],v[188:191],v[172:175]\n"
LD "v192,s[48:51],s80 offen lds\n"
MF "v[164:167],a[132:135],v[188:191],v[164:167]\n"
TR "v[180:181],v0 offset:53248\n"
MF "v[164:167],a[172:175],a[240:243],v[164:167]\n"
TR "v[182:183],v0 offset:55296\n"
MF "v[164:167],a[212:215],a[244:247],v[164:167]\n"
LD "v193,s[48:51],s80 offen offset:1024 lds\n"
MF "v[156:159],a[208:211],a[244:247],v[156:159]\n"
TR "v[184:185],v0 offset:57344\n"
MF "v[156:159],a[168:171],a[240:243],v[156:159]\n"
TR "v[186:187],v0 offset:59392\n"
MF "v[156:159],a[128:131],v[188:191],v[156:159]\n"
MF "v[148:151],a[124:127],v[188:191],v[148:151]\n"
MF "v[148:151],a[164:167],a[240:243],v[148:151]\n"
LD "v194,s[48:51],s80 offen offset:2048 lds\n"
MF "v[148:151],a[204:207],a[244:247],v[148:151]\n"
MF "v[140:143],a[200:203],a[244:247],v[140:143]\n"
MF "v[140:143],a[160:163],a[240:243],v[140:143]\n"
MF "v[140:143],a[120:123],v[188:191],v[140:143]\n"
"s_waitcnt lgkmcnt(0)\n"
LD "v195,s[48:51],s80 offen offset:3072 lds\n"
MF "v[96:99],a[20:23],v[176:179],v[96:99]\n"
TR "v[188:189],v1 offset:49152\n"
MF "v[96:99],a[60:63],v[180:183],v[96:99]\n"
TR "v[190:191],v1 offset:51200\n"
MF "v[96:99],a[100:103],v[184:187],v[96:99]\n"
MF "v[104:107],a[104:107],v[184:187],v[104:107]\n"
TR "a[240:241],v1 offset:53248\n"
MF "v[104:107],a[64:67],v[180:183],v[104:107]\n"
"s_add_u32 m0,s79,4096\n"
TR "a[242:243],v1 offset:55296\n"
MF "v[104:107],a[24:27],v[176:179],v[104:107]\n"
LD "v196,s[48:51],s80 offen lds\n"
MF "v[112:115],a[28:31],v[176:179],v[112:115]\n"
TR "a[244:245],v1 offset:57344\n"
MF "v[112:115],a[68:71],v[180:183],v[112:115]\n"
TR "a[246:247],v1 offset:59392\n"
MF "v[112:115],a[108:111],v[184:187],v[112:115]\n"
LD "v197,s[48:51],s80 offen offset:1024 lds\n"
MF "v[120:123],a[112:115],v[184:187],v[120:123]\n"
MF "v[120:123],a[72:75],v[180:183],v[120:123]\n"
MF "v[120:123],a[32:35],v[176:179],v[120:123]\n"
MF "v[128:131],a[36:39],v[176:179],v[128:131]\n"
MF "v[128:131],a[76:79],v[180:183],v[128:131]\n"
LD "v198,s[48:51],s80 offen offset:2048 lds\n"
MF "v[128:131],a[116:119],v[184:187],v[128:131]\n"
"s_waitcnt lgkmcnt(0)\n"
MF "v[132:135],a[116:119],a[244:247],v[132:135]\n"
TR "v[176:177],v2 offset:49152\n"
MF "v[132:135],a[76:79],a[240:243],v[132:135]\n"
TR "v[178:179],v2 offset:51200\n"
MF "v[132:135],a[36:39],v[188:191],v[132:135]\n"
LD "v199,s[48:51],s80 offen offset:3072 lds\n"
MF "v[124:127],a[32:35],v[188:191],v[124:127]\n"
TR "v[180:181],v2 offset:53248\n"
MF "v[124:127],a[72:75],a[240:243],v[124:127]\n"
TR "v[182:183],v2 offset:55296\n"
MF "v[124:127],a[112:115],a[244:247],v[124:127]\n"
MF "v[116:119],a[108:111],a[244:247],v[116:119]\n"
TR "v[184:185],v2 offset:57344\n"
MF "v[116:119],a[68:71],a[240:243],v[116:119]\n"
"s_add_u32 m0,s79,8192\n"
TR "v[186:187],v2 offset:59392\n"
MF "v[116:119],a[28:31],v[188:191],v[116:119]\n"
LD "v200,s[48:51],s80 offen lds\n"
MF "v[108:111],a[24:27],v[188:191],v[108:111]\n"
MF "v[108:111],a[64:67],a[240:243],v[108:111]\n"
MF "v[108:111],a[104:107],a[244:247],v[108:111]\n"
MF "v[100:103],a[100:103],a[244:247],v[100:103]\n"
LD "v201,s[48:51],s80 offen offset:1024 lds\n"
MF "v[100:103],a[60:63],a[240:243],v[100:103]\n"
MF "v[100:103],a[20:23],v[188:191],v[100:103]\n"
"s_waitcnt lgkmcnt(0)\n"
MF "v[136:139],a[20:23],v[176:179],v[136:139]\n"
TR "v[188:189],v3 offset:49152\n"
MF "v[136:139],a[60:63],v[180:183],v[136:139]\n"
TR "v[190:191],v3 offset:51200\n"
MF "v[136:139],a[100:103],v[184:187],v[136:139]\n"
LD "v202,s[48:51],s80 offen offset:2048 lds\n"
MF "v[144:147],a[104:107],v[184:187],v[144:147]\n"
TR "a[240:241],v3 offset:53248\n"
MF "v[144:147],a[64:67],v[180:183],v[144:147]\n"
TR "a[242:243],v3 offset:55296\n"
MF "v[144:147],a[24:27],v[176:179],v[144:147]\n"
LD "v203,s[48:51],s80 offen offset:3072 lds\n"
MF "v[152:155],a[28:31],v[176:179],v[152:155]\n"
TR "a[244:245],v3 offset:57344\n"
MF "v[152:155],a[68:71],v[180:183],v[152:155]\n"
TR "a[246:247],v3 offset:59392\n"
MF "v[152:155],a[108:111],v[184:187],v[152:155]\n"
MF "v[160:163],a[112:115],v[184:187],v[160:163]\n"
MF "v[160:163],a[72:75],v[180:183],v[160:163]\n"
MF "v[160:163],a[32:35],v[176:179],v[160:163]\n"
MF "v[168:171],a[36:39],v[176:179],v[168:171]\n"
MF "v[168:171],a[76:79],v[180:183],v[168:171]\n"
"s_waitcnt vmcnt(24) lgkmcnt(0)\n"
MF "v[168:171],a[116:119],v[184:187],v[168:171]\n"
"s_barrier\n"
MF "v[172:175],a[116:119],a[244:247],v[172:175]\n"
"s_add_u32 s88,s88,s65\n"
TR "v[176:177],v4\n"
MF "v[172:175],a[76:79],a[240:243],v[172:175]\n"
"s_add_u32 m0,s79,49152\n"
TR "v[178:179],v4 offset:2048\n"
MF "v[172:175],a[36:39],v[188:191],v[172:175]\n"
LD "v192,s[48:51],s88 offen lds\n"
MF "v[164:167],a[32:35],v[188:191],v[164:167]\n"
TR "v[180:181],v4 offset:4096\n"
MF "v[164:167],a[72:75],a[240:243],v[164:167]\n"
TR "v[182:183],v4 offset:6144\n"
MF "v[164:167],a[112:115],a[244:247],v[164:167]\n"
LD "v193,s[48:51],s88 offen offset:1024 lds\n"
MF "v[156:159],a[108:111],a[244:247],v[156:159]\n"
TR "v[184:185],v4 offset:8192\n"
MF "v[156:159],a[68:71],a[240:243],v[156:159]\n"
TR "v[186:187],v4 offset:10240\n"
MF "v[156:159],a[28:31],v[188:191],v[156:159]\n"
MF "v[148:151],a[24:27],v[188:191],v[148:151]\n"
MF "v[148:151],a[64:67],a[240:243],v[148:151]\n"
LD "v194,s[48:51],s88 offen offset:2048 lds\n"
MF "v[148:151],a[104:107],a[244:247],v[148:151]\n"
MF "v[140:143],a[100:103],a[244:247],v[140:143]\n"
MF "v[140:143],a[60:63],a[240:243],v[140:143]\n"
MF "v[140:143],a[20:23],v[188:191],v[140:143]\n"
"s_waitcnt lgkmcnt(0)\n"
LD "v195,s[48:51],s88 offen offset:3072 lds\n"
MF "v[96:99],a[140:143],v[176:179],v[96:99]\n"
TR "v[188:189],v5\n"
MF "v[96:99],a[180:183],v[180:183],v[96:99]\n"
TR "v[190:191],v5 offset:2048\n"
MF "v[96:99],a[220:223],v[184:187],v[96:99]\n"
MF "v[104:107],a[224:227],v[184:187],v[104:107]\n"
TR "a[240:241],v5 offset:4096\n"
MF "v[104:107],a[184:187],v[180:183],v[104:107]\n"
"s_add_u32 m0,s79,53248\n"
TR "a[242:243],v5 offset:6144\n"
MF "v[104:107],a[144:147],v[176:179],v[104:107]\n"
LD "v196,s[48:51],s88 offen lds\n"
MF "v[112:115],a[148:151],v[176:179],v[112:115]\n"
TR "a[244:245],v5 offset:8192\n"
MF "v[112:115],a[188:191],v[180:183],v[112:115]\n"
TR "a[246:247],v5 offset:10240\n"
MF "v[112:115],a[228:231],v[184:187],v[112:115]\n"
LD "v197,s[48:51],s88 offen offset:1024 lds\n"
MF "v[120:123],a[232:235],v[184:187],v[120:123]\n"
MF "v[120:123],a[192:195],v[180:183],v[120:123]\n"
MF "v[120:123],a[152:155],v[176:179],v[120:123]\n"
MF "v[128:131],a[156:159],v[176:179],v[128:131]\n"
MF "v[128:131],a[196:199],v[180:183],v[128:131]\n"
LD "v198,s[48:51],s88 offen offset:2048 lds\n"
MF "v[128:131],a[236:239],v[184:187],v[128:131]\n"
"s_waitcnt lgkmcnt(0)\n"
MF "v[132:135],a[236:239],a[244:247],v[132:135]\n"
TR "v[176:177],v6\n"
MF "v[132:135],a[196:199],a[240:243],v[132:135]\n"
TR "v[178:179],v6 offset:2048\n"
MF "v[132:135],a[156:159],v[188:191],v[132:135]\n"
LD "v199,s[48:51],s88 offen offset:3072 lds\n"
MF "v[124:127],a[152:155],v[188:191],v[124:127]\n"
TR "v[180:181],v6 offset:4096\n"
MF "v[124:127],a[192:195],a[240:243],v[124:127]\n"
TR "v[182:183],v6 offset:6144\n"
MF "v[124:127],a[232:235],a[244:247],v[124:127]\n"
MF "v[116:119],a[228:231],a[244:247],v[116:119]\n"
TR "v[184:185],v6 offset:8192\n"
MF "v[116:119],a[188:191],a[240:243],v[116:119]\n"
"s_add_u32 m0,s79,57344\n"
TR "v[186:187],v6 offset:10240\n"
MF "v[116:119],a[148:151],v[188:191],v[116:119]\n"
LD "v200,s[48:51],s88 offen lds\n"
MF "v[108:111],a[144:147],v[188:191],v[108:111]\n"
MF "v[108:111],a[184:187],a[240:243],v[108:111]\n"
MF "v[108:111],a[224:227],a[244:247],v[108:111]\n"
MF "v[100:103],a[220:223],a[244:247],v[100:103]\n"
LD "v201,s[48:51],s88 offen offset:1024 lds\n"
MF "v[100:103],a[180:183],a[240:243],v[100:103]\n"
MF "v[100:103],a[140:143],v[188:191],v[100:103]\n"
"s_waitcnt lgkmcnt(0)\n"
MF "v[136:139],a[140:143],v[176:179],v[136:139]\n"
TR "v[188:189],v7\n"
MF "v[136:139],a[180:183],v[180:183],v[136:139]\n"
TR "v[190:191],v7 offset:2048\n"
MF "v[136:139],a[220:223],v[184:187],v[136:139]\n"
LD "v202,s[48:51],s88 offen offset:2048 lds\n"
MF "v[144:147],a[224:227],v[184:187],v[144:147]\n"
TR "a[240:241],v7 offset:4096\n"
MF "v[144:147],a[184:187],v[180:183],v[144:147]\n"
TR "a[242:243],v7 offset:6144\n"
MF "v[144:147],a[144:147],v[176:179],v[144:147]\n"
LD "v203,s[48:51],s88 offen offset:3072 lds\n"
MF "v[152:155],a[148:151],v[176:179],v[152:155]\n"
TR "a[244:245],v7 offset:8192\n"
MF "v[152:155],a[188:191],v[180:183],v[152:155]\n"
TR "a[246:247],v7 offset:10240\n"
MF "v[152:155],a[228:231],v[184:187],v[152:155]\n"
MF "v[160:163],a[232:235],v[184:187],v[160:163]\n"
MF "v[160:163],a[192:195],v[180:183],v[160:163]\n"
MF "v[160:163],a[152:155],v[176:179],v[160:163]\n"
MF "v[168:171],a[156:159],v[176:179],v[168:171]\n"
MF "v[168:171],a[196:199],v[180:183],v[168:171]\n"
"s_waitcnt vmcnt(24) lgkmcnt(0)\n"
MF "v[168:171],a[236:239],v[184:187],v[168:171]\n"
"s_barrier\n"
MF "v[172:175],a[236:239],a[244:247],v[172:175]\n"
"s_add_u32 s80,s88,122880\n"
TR "v[176:177],v0 offset:24576\n"
MF "v[172:175],a[196:199],a[240:243],v[172:175]\n"
"s_add_u32 m0,s79,98304\n"
"s_add_u32 s85,s85,1\n"
TR "v[178:179],v0 offset:26624\n"
MF "v[172:175],a[156:159],v[188:191],v[172:175]\n"
LD "v192,s[48:51],s80 offen lds\n"
MF "v[164:167],a[152:155],v[188:191],v[164:167]\n"
"s_nop 0\n"
TR "v[180:181],v0 offset:28672\n"
"ds_write_addtid_b32 v96 offset:24576\n"
MF "v[164:167],a[192:195],a[240:243],v[164:167]\n"
"s_nop 0\n"
TR "v[182:183],v0 offset:30720\n"
"ds_write_addtid_b32 v97 offset:24832\n"
MF "v[164:167],a[232:235],a[244:247],v[164:167]\n"
LD "v193,s[48:51],s80 offen offset:1024 lds\n"
MF "v[156:159],a[228:231],a[244:247],v[156:159]\n"
"s_nop 0\n"
TR "v[184:185],v0 offset:32768\n"
"ds_write_addtid_b32 v98 offset:25088\n"
MF "v[156:159],a[188:191],a[240:243],v[156:159]\n"
"s_nop 0\n"
TR "v[186:187],v0 offset:34816\n"
"ds_write_addtid_b32 v99 offset:25344\n"
MF "v[156:159],a[148:151],v[188:191],v[156:159]\n"
"s_nop 0\n"
"ds_write_addtid_b32 v100 offset:25600\n"
"ds_write_addtid_b32 v101 offset:25856\n"
MF "v[148:151],a[144:147],v[188:191],v[148:151]\n"
LD "v194,s[48:51],s80 offen offset:2048 lds\n"
MF "v[148:151],a[184:187],a[240:243],v[148:151]\n"
"s_nop 0\n"
"ds_write_addtid_b32 v102 offset:26112\n"
"ds_write_addtid_b32 v103 offset:26368\n"
MF "v[148:151],a[224:227],a[244:247],v[148:151]\n"
"s_nop 0\n"
"ds_write_addtid_b32 v104 offset:26624\n"
"ds_write_addtid_b32 v105 offset:26880\n"
MF "v[140:143],a[220:223],a[244:247],v[140:143]\n"
"s_nop 0\n"
"ds_write_addtid_b32 v106 offset:27136\n"
"ds_write_addtid_b32 v107 offset:27392\n"
MF "v[140:143],a[180:183],a[240:243],v[140:143]\n"
MF "v[140:143],a[140:143],v[188:191],v[140:143]\n"
"s_waitcnt lgkmcnt(9)\n"
MF "v[16:19],a[0:3],v[176:179],v[252:255]\n"
"s_nop 0\n"
TR "v[188:189],v1 offset:24576\n"
"ds_write_addtid_b32 v108 offset:27648\n"
MF "v[16:19],a[40:43],v[180:183],v[16:19]\n"
"s_nop 0\n"
TR "v[190:191],v1 offset:26624\n"
"ds_write_addtid_b32 v109 offset:27904\n"
MF "v[16:19],a[80:83],v[184:187],v[16:19]\n"
LD "v195,s[48:51],s80 offen offset:3072 lds\n"
MF "v[24:27],a[84:87],v[184:187],v[252:255]\n"
"s_add_u32 s60,s64,1\n"
TR "a[240:241],v1 offset:28672\n"
"ds_write_addtid_b32 v110 offset:28160\n"
MF "v[24:27],a[44:47],v[180:183],v[24:27]\n"
"s_add_u32 m0,s79,102400\n"
TR "a[242:243],v1 offset:30720\n"
"ds_write_addtid_b32 v111 offset:24320\n"
MF "v[24:27],a[4:7],v[176:179],v[24:27]\n"
LD "v196,s[48:51],s80 offen lds\n"
MF "v[32:35],a[8:11],v[176:179],v[252:255]\n"
"s_cmp_eq_u32 s60,8\n"
"s_cselect_b32 s60,0,s60\n"
TR "a[244:245],v1 offset:32768\n"
MF "v[32:35],a[48:51],v[180:183],v[32:35]\n"
"s_cmp_eq_u32 s60,10\n"
"s_cselect_b32 s60,s61,s60\n"
TR "a[246:247],v1 offset:34816\n"
MF "v[32:35],a[88:91],v[184:187],v[32:35]\n"
"s_sub_u32 s65,s60,s64\n"
"ds_write_addtid_b32 v112 offset:24576\n"
"ds_write_addtid_b32 v113 offset:24832\n"
MF "v[40:43],a[92:95],v[184:187],v[252:255]\n"
"s_lshl_b32 s65,s65,7\n"
"ds_write_addtid_b32 v114 offset:25088\n"
"ds_write_addtid_b32 v115 offset:25344\n"
MF "v[40:43],a[52:55],v[180:183],v[40:43]\n"
LD "v197,s[48:51],s80 offen offset:1024 lds\n"
MF "v[40:43],a[12:15],v[176:179],v[40:43]\n"
"s_mov_b32 s64,s60\n"
"ds_write_addtid_b32 v116 offset:25600\n"
"ds_write_addtid_b32 v117 offset:25856\n"
MF "v[48:51],a[16:19],v[176:179],v[252:255]\n"
"s_nop 0\n"
"ds_write_addtid_b32 v118 offset:26112\n"
"ds_write_addtid_b32 v119 offset:26368\n"
MF "v[48:51],a[56:59],v[180:183],v[48:51]\n"
"s_nop 0\n"
"ds_write_addtid_b32 v120 offset:26624\n"
"ds_write_addtid_b32 v121 offset:26880\n"
MF "v[48:51],a[96:99],v[184:187],v[48:51]\n"
"s_waitcnt lgkmcnt(10)\n"
LD "v198,s[48:51],s80 offen offset:2048 lds\n"
MF "v[52:55],a[96:99],a[244:247],v[252:255]\n"
"s_nop 0\n"
TR "v[176:177],v2 offset:24576\n"
"ds_write_addtid_b32 v122 offset:27136\n"
MF "v[52:55],a[56:59],a[240:243],v[52:55]\n"
"s_nop 0\n"
TR "v[178:179],v2 offset:26624\n"
"ds_write_addtid_b32 v123 offset:27392\n"
MF "v[52:55],a[16:19],v[188:191],v[52:55]\n"
"s_nop 0\n"
"ds_write_addtid_b32 v124 offset:27648\n"
"ds_write_addtid_b32 v125 offset:27904\n"
MF "v[44:47],a[12:15],v[188:191],v[252:255]\n"
"s_nop 0\n"
TR "v[180:181],v2 offset:28672\n"
"ds_write_addtid_b32 v126 offset:28160\n"
MF "v[44:47],a[52:55],a[240:243],v[44:47]\n"
"s_nop 0\n"
TR "v[182:183],v2 offset:30720\n"
"ds_write_addtid_b32 v127 offset:28416\n"
MF "v[44:47],a[92:95],a[244:247],v[44:47]\n"
LD "v199,s[48:51],s80 offen offset:3072 lds\n"
MF "v[36:39],a[88:91],a[244:247],v[252:255]\n"
"s_nop 0\n"
TR "v[184:185],v2 offset:32768\n"
"ds_write_addtid_b32 v128 offset:28672\n"
MF "v[36:39],a[48:51],a[240:243],v[36:39]\n"
"s_add_u32 m0,s79,106496\n"
TR "v[186:187],v2 offset:34816\n"
"ds_write_addtid_b32 v129 offset:24832\n"
MF "v[36:39],a[8:11],v[188:191],v[36:39]\n"
LD "v200,s[48:51],s80 offen lds\n"
MF "v[28:31],a[4:7],v[188:191],v[252:255]\n"
"s_nop 0\n"
"ds_write_addtid_b32 v130 offset:25088\n"
"ds_write_addtid_b32 v131 offset:25344\n"
MF "v[28:31],a[44:47],a[240:243],v[28:31]\n"
"s_nop 0\n"
"ds_write_addtid_b32 v132 offset:25600\n"
"ds_write_addtid_b32 v133 offset:25856\n"
MF "v[28:31],a[84:87],a[244:247],v[28:31]\n"
"s_nop 0\n"
"ds_write_addtid_b32 v134 offset:26112\n"
"ds_write_addtid_b32 v135 offset:26368\n"
MF "v[20:23],a[80:83],a[244:247],v[252:255]\n"
MF "v[20:23],a[40:43],a[240:243],v[20:23]\n"
LD "v201,s[48:51],s80 offen offset:1024 lds\n"
MF "v[20:23],a[0:3],v[188:191],v[20:23]\n"
"s_waitcnt lgkmcnt(7)\n"
MF "v[56:59],a[0:3],v[176:179],v[252:255]\n"
TR "v[188:189],v3 offset:24576\n"
MF "v[56:59],a[40:43],v[180:183],v[56:59]\n"
TR "v[190:191],v3 offset:26624\n"
MF "v[56:59],a[80:83],v[184:187],v[56:59]\n"
LD "v202,s[48:51],s80 offen offset:2048 lds\n"
MF "v[64:67],a[84:87],v[184:187],v[252:255]\n"
TR "a[240:241],v3 offset:28672\n"
MF "v[64:67],a[44:47],v[180:183],v[64:67]\n"
TR "a[242:243],v3 offset:30720\n"
MF "v[64:67],a[4:7],v[176:179],v[64:67]\n"
LD "v203,s[48:51],s80 offen offset:3072 lds\n"
MF "v[72:75],a[8:11],v[176:179],v[252:255]\n"
TR "a[244:245],v3 offset:32768\n"
MF "v[72:75],a[48:51],v[180:183],v[72:75]\n"
TR "a[246:247],v3 offset:34816\n"
MF "v[72:75],a[88:91],v[184:187],v[72:75]\n"
MF "v[80:83],a[92:95],v[184:187],v[252:255]\n"
MF "v[80:83],a[52:55],v[180:183],v[80:83]\n"
MF "v[80:83],a[12:15],v[176:179],v[80:83]\n"
MF "v[88:91],a[16:19],v[176:179],v[252:255]\n"
MF "v[88:91],a[56:59],v[180:183],v[88:91]\n"
"s_waitcnt vmcnt(24) lgkmcnt(0)\n"
MF "v[88:91],a[96:99],v[184:187],v[88:91]\n"
"s_barrier\n"
MF "v[92:95],a[96:99],a[244:247],v[252:255]\n"
"s_add_u32 s89,s89,s65\n"
TR "v[176:177],v0\n"
MF "v[92:95],a[56:59],a[240:243],v[92:95]\n"
"s_add_u32 m0,s79,24576\n"
TR "v[178:179],v0 offset:2048\n"
MF "v[92:95],a[16:19],v[188:191],v[92:95]\n"
LD "v192,s[48:51],s89 offen lds\n"
MF "v[84:87],a[12:15],v[188:191],v[252:255]\n"
TR "v[180:181],v0 offset:4096\n"
MF "v[84:87],a[52:55],a[240:243],v[84:87]\n"
TR "v[182:183],v0 offset:6144\n"
MF "v[84:87],a[92:95],a[244:247],v[84:87]\n"
LD "v193,s[48:51],s89 offen offset:1024 lds\n"
MF "v[76:79],a[88:91],a[244:247],v[252:255]\n"
TR "v[184:185],v0 offset:8192\n"
MF "v[76:79],a[48:51],a[240:243],v[76:79]\n"
"s_nop 0\n"
TR "v[186:187],v0 offset:10240\n"
"ds_write_addtid_b32 v136 offset:49152\n"
MF "v[76:79],a[8:11],v[188:191],v[76:79]\n"
"s_nop 0\n"
"ds_write_addtid_b32 v137 offset:49408\n"
"ds_write_addtid_b32 v138 offset:49664\n"
MF "v[68:71],a[4:7],v[188:191],v[252:255]\n"
"s_nop 0\n"
"ds_write_addtid_b32 v139 offset:49920\n"
"ds_write_addtid_b32 v140 offset:50176\n"
MF "v[68:71],a[44:47],a[240:243],v[68:71]\n"
LD "v194,s[48:51],s89 offen offset:2048 lds\n"
MF "v[68:71],a[84:87],a[244:247],v[68:71]\n"
"s_nop 0\n"
"ds_write_addtid_b32 v141 offset:50432\n"
"ds_write_addtid_b32 v142 offset:50688\n"
MF "v[60:63],a[80:83],a[244:247],v[252:255]\n"
"s_nop 0\n"
"ds_write_addtid_b32 v143 offset:50944\n"
"ds_write_addtid_b32 v144 offset:51200\n"
MF "v[60:63],a[40:43],a[240:243],v[60:63]\n"
"s_nop 0\n"
"ds_write_addtid_b32 v145 offset:51456\n"
"ds_write_addtid_b32 v146 offset:51712\n"
MF "v[60:63],a[0:3],v[188:191],v[60:63]\n"
"s_waitcnt lgkmcnt(11)\n"
LD "v195,s[48:51],s89 offen offset:3072 lds\n"
MF "v[16:19],a[120:123],v[176:179],v[16:19]\n"
"s_nop 0\n"
TR "v[188:189],v1\n"
"ds_write_addtid_b32 v147 offset:51968\n"
MF "v[16:19],a[160:163],v[180:183],v[16:19]\n"
"s_nop 0\n"
TR "v[190:191],v1 offset:2048\n"
"ds_write_addtid_b32 v148 offset:52224\n"
MF "v[16:19],a[200:203],v[184:187],v[16:19]\n"
"s_nop 0\n"
"ds_write_addtid_b32 v149 offset:52480\n"
"ds_write_addtid_b32 v150 offset:52736\n"
MF "v[24:27],a[204:207],v[184:187],v[24:27]\n"
"s_nop 0\n"
TR "a[240:241],v1 offset:4096\n"
"ds_write_addtid_b32 v151 offset:52992\n"
MF "v[24:27],a[164:167],v[180:183],v[24:27]\n"
"s_add_u32 m0,s79,28672\n"
TR "a[242:243],v1 offset:6144\n"
"ds_write_addtid_b32 v152 offset:49152\n"
MF "v[24:27],a[124:127],v[176:179],v[24:27]\n"
".Lpg27h:\n"
"s_cbranch_execz .Lpg31h\n"
LD "v196,s[48:51],s89 offen lds\n"
MF "v[32:35],a[128:131],v[176:179],v[32:35]\n"
"s_nop 0\n"
TR "a[244:245],v1 offset:8192\n"
"ds_write_addtid_b32 v153 offset:49408\n"
MF "v[32:35],a[168:171],v[180:183],v[32:35]\n"
"s_nop 0\n"
TR "a[246:247],v1 offset:10240\n"
"ds_write_addtid_b32 v154 offset:49664\n"
MF "v[32:35],a[208:211],v[184:187],v[32:35]\n"
LD "v197,s[48:51],s89 offen offset:1024 lds\n"
MF "v[40:43],a[212:215],v[184:187],v[40:43]\n"
"s_nop 0\n"
"ds_write_addtid_b32 v155 offset:49920\n"
"ds_write_addtid_b32 v156 offset:50176\n"
MF "v[40:43],a[172:175],v[180:183],v[40:43]\n"
"s_nop 0\n"
"ds_write_addtid_b32 v157 offset:50432\n"
"ds_write_addtid_b32 v158 offset:50688\n"
MF "v[40:43],a[132:135],v[176:179],v[40:43]\n"
"s_nop 0\n"
"ds_write_addtid_b32 v159 offset:50944\n"
"ds_write_addtid_b32 v160 offset:51200\n"
MF "v[48:51],a[136:139],v[176:179],v[48:51]\n"
"s_nop 0\n"
"ds_write_addtid_b32 v161 offset:51456\n"
"ds_write_addtid_b32 v162 offset:51712\n"
MF "v[48:51],a[176:179],v[180:183],v[48:51]\n"
LD "v198,s[48:51],s89 offen offset:2048 lds\n"
MF "v[48:51],a[216:219],v[184:187],v[48:51]\n"
"s_waitcnt lgkmcnt(9)\n"
"ds_write_addtid_b32 v163 offset:51968\n"
"ds_write_addtid_b32 v164 offset:52224\n"
MF "v[52:55],a[216:219],a[244:247],v[52:55]\n"
"s_nop 0\n"
TR "v[176:177],v2\n"
"ds_write_addtid_b32 v165 offset:52480\n"
MF "v[52:55],a[176:179],a[240:243],v[52:55]\n"
"s_nop 0\n"
TR "v[178:179],v2 offset:2048\n"
"ds_write_addtid_b32 v166 offset:52736\n"
MF "v[52:55],a[136:139],v[188:191],v[52:55]\n"
LD "v199,s[48:51],s89 offen offset:3072 lds\n"
MF "v[44:47],a[132:135],v[188:191],v[44:47]\n"
"s_nop 0\n"
TR "v[180:181],v2 offset:4096\n"
"ds_write_addtid_b32 v167 offset:52992\n"
MF "v[44:47],a[172:175],a[240:243],v[44:47]\n"
"s_nop 0\n"
TR "v[182:183],v2 offset:6144\n"
"ds_write_addtid_b32 v168 offset:53248\n"
MF "v[44:47],a[212:215],a[244:247],v[44:47]\n"
"s_nop 0\n"
"ds_write_addtid_b32 v169 offset:53504\n"
"ds_write_addtid_b32 v170 offset:53760\n"
MF "v[36:39],a[208:211],a[244:247],v[36:39]\n"
"s_nop 0\n"
TR "v[184:185],v2 offset:8192\n"
"ds_write_addtid_b32 v171 offset:54016\n"
MF "v[36:39],a[168:171],a[240:243],v[36:39]\n"
"s_add_u32 m0,s79,32768\n"
TR "v[186:187],v2 offset:10240\n"
"ds_write_addtid_b32 v172 offset:50176\n"
MF "v[36:39],a[128:131],v[188:191],v[36:39]\n"
LD "v200,s[48:51],s89 offen lds\n"
MF "v[28:31],a[124:127],v[188:191],v[28:31]\n"
"s_nop 0\n"
"ds_write_addtid_b32 v173 offset:50432\n"
"ds_write_addtid_b32 v174 offset:50688\n"
MF "v[28:31],a[164:167],a[240:243],v[28:31]\n"
"ds_write_addtid_b32 v175 offset:50944\n"
MF "v[28:31],a[204:207],a[244:247],v[28:31]\n"
MF "v[20:23],a[200:203],a[244:247],v[20:23]\n"
LD "v201,s[48:51],s89 offen offset:1024 lds\n"
MF "v[20:23],a[160:163],a[240:243],v[20:23]\n"
MF "v[20:23],a[120:123],v[188:191],v[20:23]\n"
"s_waitcnt lgkmcnt(4)\n"
MF "v[56:59],a[120:123],v[176:179],v[56:59]\n"
TR "v[188:189],v3\n"
MF "v[56:59],a[160:163],v[180:183],v[56:59]\n"
TR "v[190:191],v3 offset:2048\n"
MF "v[56:59],a[200:203],v[184:187],v[56:59]\n"
LD "v202,s[48:51],s89 offen offset:2048 lds\n"
MF "v[64:67],a[204:207],v[184:187],v[64:67]\n"
TR "a[240:241],v3 offset:4096\n"
MF "v[64:67],a[164:167],v[180:183],v[64:67]\n"
TR "a[242:243],v3 offset:6144\n"
MF "v[64:67],a[124:127],v[176:179],v[64:67]\n"
LD "v203,s[48:51],s89 offen offset:3072 lds\n"
MF "v[72:75],a[128:131],v[176:179],v[72:75]\n"
TR "a[244:245],v3 offset:8192\n"
MF "v[72:75],a[168:171],v[180:183],v[72:75]\n"
TR "a[246:247],v3 offset:10240\n"
MF "v[72:75],a[208:211],v[184:187],v[72:75]\n"
MF "v[80:83],a[212:215],v[184:187],v[80:83]\n"
MF "v[80:83],a[172:175],v[180:183],v[80:83]\n"
MF "v[80:83],a[132:135],v[176:179],v[80:83]\n"
MF "v[88:91],a[136:139],v[176:179],v[88:91]\n"
MF "v[88:91],a[176:179],v[180:183],v[88:91]\n"
"s_waitcnt vmcnt(24) lgkmcnt(0)\n"
MF "v[88:91],a[216:219],v[184:187],v[88:91]\n"
"s_barrier\n"
MF "v[92:95],a[216:219],a[244:247],v[92:95]\n"
"s_add_u32 s80,s89,122880\n"
TR "v[176:177],v0 offset:49152\n"
MF "v[92:95],a[176:179],a[240:243],v[92:95]\n"
"s_add_u32 m0,s79,0\n"
TR "v[178:179],v0 offset:51200\n"
MF "v[92:95],a[136:139],v[188:191],v[92:95]\n"
LD "v192,s[48:51],s80 offen lds\n"
MF "v[84:87],a[132:135],v[188:191],v[84:87]\n"
TR "v[180:181],v0 offset:53248\n"
MF "v[84:87],a[172:175],a[240:243],v[84:87]\n"
TR "v[182:183],v0 offset:55296\n"
MF "v[84:87],a[212:215],a[244:247],v[84:87]\n"
LD "v193,s[48:51],s80 offen offset:1024 lds\n"
MF "v[76:79],a[208:211],a[244:247],v[76:79]\n"
TR "v[184:185],v0 offset:57344\n"
MF "v[76:79],a[168:171],a[240:243],v[76:79]\n"
TR "v[186:187],v0 offset:59392\n"
MF "v[76:79],a[128:131],v[188:191],v[76:79]\n"
MF "v[68:71],a[124:127],v[188:191],v[68:71]\n"
MF "v[68:71],a[164:167],a[240:243],v[68:71]\n"
LD "v194,s[48:51],s80 offen offset:2048 lds\n"
MF "v[68:71],a[204:207],a[244:247],v[68:71]\n"
MF "v[60:63],a[200:203],a[244:247],v[60:63]\n"
MF "v[60:63],a[160:163],a[240:243],v[60:63]\n"
MF "v[60:63],a[120:123],v[188:191],v[60:63]\n"
"s_waitcnt lgkmcnt(0)\n"
LD "v195,s[48:51],s80 offen offset:3072 lds\n"
MF "v[16:19],a[20:23],v[176:179],v[16:19]\n"
TR "v[188:189],v1 offset:49152\n"
MF "v[16:19],a[60:63],v[180:183],v[16:19]\n"
TR "v[190:191],v1 offset:51200\n"
MF "v[16:19],a[100:103],v[184:187],v[16:19]\n"
MF "v[24:27],a[104:107],v[184:187],v[24:27]\n"
TR "a[240:241],v1 offset:53248\n"
MF "v[24:27],a[64:67],v[180:183],v[24:27]\n"
"s_add_u32 m0,s79,4096\n"
TR "a[242:243],v1 offset:55296\n"
MF "v[24:27],a[24:27],v[176:179],v[24:27]\n"
LD "v196,s[48:51],s80 offen lds\n"
MF "v[32:35],a[28:31],v[176:179],v[32:35]\n"
TR "a[244:245],v1 offset:57344\n"
MF "v[32:35],a[68:71],v[180:183],v[32:35]\n"
TR "a[246:247],v1 offset:59392\n"
MF "v[32:35],a[108:111],v[184:187],v[32:35]\n"
LD "v197,s[48:51],s80 offen offset:1024 lds\n"
MF "v[40:43],a[112:115],v[184:187],v[40:43]\n"
MF "v[40:43],a[72:75],v[180:183],v[40:43]\n"
MF "v[40:43],a[32:35],v[176:179],v[40:43]\n"
MF "v[48:51],a[36:39],v[176:179],v[48:51]\n"
MF "v[48:51],a[76:79],v[180:183],v[48:51]\n"
LD "v198,s[48:51],s80 offen offset:2048 lds\n"
MF "v[48:51],a[116:119],v[184:187],v[48:51]\n"
"s_waitcnt lgkmcnt(0)\n"
MF "v[52:55],a[116:119],a[244:247],v[52:55]\n"
TR "v[176:177],v2 offset:49152\n"
MF "v[52:55],a[76:79],a[240:243],v[52:55]\n"
TR "v[178:179],v2 offset:51200\n"
MF "v[52:55],a[36:39],v[188:191],v[52:55]\n"
LD "v199,s[48:51],s80 offen offset:3072 lds\n"
MF "v[44:47],a[32:35],v[188:191],v[44:47]\n"
TR "v[180:181],v2 offset:53248\n"
MF "v[44:47],a[72:75],a[240:243],v[44:47]\n"
TR "v[182:183],v2 offset:55296\n"
MF "v[44:47],a[112:115],a[244:247],v[44:47]\n"
MF "v[36:39],a[108:111],a[244:247],v[36:39]\n"
TR "v[184:185],v2 offset:57344\n"
MF "v[36:39],a[68:71],a[240:243],v[36:39]\n"
"s_add_u32 m0,s79,8192\n"
TR "v[186:187],v2 offset:59392\n"
MF "v[36:39],a[28:31],v[188:191],v[36:39]\n"
LD "v200,s[48:51],s80 offen lds\n"
MF "v[28:31],a[24:27],v[188:191],v[28:31]\n"
MF "v[28:31],a[64:67],a[240:243],v[28:31]\n"
MF "v[28:31],a[104:107],a[244:247],v[28:31]\n"
MF "v[20:23],a[100:103],a[244:247],v[20:23]\n"
LD "v201,s[48:51],s80 offen offset:1024 lds\n"
MF "v[20:23],a[60:63],a[240:243],v[20:23]\n"
MF "v[20:23],a[20:23],v[188:191],v[20:23]\n"
"s_waitcnt lgkmcnt(0)\n"
MF "v[56:59],a[20:23],v[176:179],v[56:59]\n"
TR "v[188:189],v3 offset:49152\n"
MF "v[56:59],a[60:63],v[180:183],v[56:59]\n"
TR "v[190:191],v3 offset:51200\n"
MF "v[56:59],a[100:103],v[184:187],v[56:59]\n"
LD "v202,s[48:51],s80 offen offset:2048 lds\n"
MF "v[64:67],a[104:107],v[184:187],v[64:67]\n"
TR "a[240:241],v3 offset:53248\n"
MF "v[64:67],a[64:67],v[180:183],v[64:67]\n"
TR "a[242:243],v3 offset:55296\n"
MF "v[64:67],a[24:27],v[176:179],v[64:67]\n"
LD "v203,s[48:51],s80 offen offset:3072 lds\n"
MF "v[72:75],a[28:31],v[176:179],v[72:75]\n"
TR "a[244:245],v3 offset:57344\n"
MF "v[72:75],a[68:71],v[180:183],v[72:75]\n"
TR "a[246:247],v3 offset:59392\n"
MF "v[72:75],a[108:111],v[184:187],v[72:75]\n"
MF "v[80:83],a[112:115],v[184:187],v[80:83]\n"
MF "v[80:83],a[72:75],v[180:183],v[80:83]\n"
MF "v[80:83],a[32:35],v[176:179],v[80:83]\n"
MF "v[88:91],a[36:39],v[176:179],v[88:91]\n"
MF "v[88:91],a[76:79],v[180:183],v[88:91]\n"
"s_waitcnt vmcnt(24) lgkmcnt(0)\n"
MF "v[88:91],a[116:119],v[184:187],v[88:91]\n"
"s_barrier\n"
MF "v[92:95],a[116:119],a[244:247],v[92:95]\n"
"s_add_u32 s88,s88,s65\n"
TR "v[176:177],v4\n"
MF "v[92:95],a[76:79],a[240:243],v[92:95]\n"
"s_add_u32 m0,s79,49152\n"
TR "v[178:179],v4 offset:2048\n"
MF "v[92:95],a[36:39],v[188:191],v[92:95]\n"
LD "v192,s[48:51],s88 offen lds\n"
MF "v[84:87],a[32:35],v[188:191],v[84:87]\n"
TR "v[180:181],v4 offset:4096\n"
MF "v[84:87],a[72:75],a[240:243],v[84:87]\n"
TR "v[182:183],v4 offset:6144\n"
MF "v[84:87],a[112:115],a[244:247],v[84:87]\n"
LD "v193,s[48:51],s88 offen offset:1024 lds\n"
MF "v[76:79],a[108:111],a[244:247],v[76:79]\n"
TR "v[184:185],v4 offset:8192\n"
MF "v[76:79],a[68:71],a[240:243],v[76:79]\n"
TR "v[186:187],v4 offset:10240\n"
MF "v[76:79],a[28:31],v[188:191],v[76:79]\n"
MF "v[68:71],a[24:27],v[188:191],v[68:71]\n"
MF "v[68:71],a[64:67],a[240:243],v[68:71]\n"
LD "v194,s[48:51],s88 offen offset:2048 lds\n"
MF "v[68:71],a[104:107],a[244:247],v[68:71]\n"
MF "v[60:63],a[100:103],a[244:247],v[60:63]\n"
MF "v[60:63],a[60:63],a[240:243],v[60:63]\n"
MF "v[60:63],a[20:23],v[188:191],v[60:63]\n"
"s_waitcnt lgkmcnt(0)\n"
LD "v195,s[48:51],s88 offen offset:3072 lds\n"
MF "v[16:19],a[140:143],v[176:179],v[16:19]\n"
TR "v[188:189],v5\n"
MF "v[16:19],a[180:183],v[180:183],v[16:19]\n"
TR "v[190:191],v5 offset:2048\n"
MF "v[16:19],a[220:223],v[184:187],v[16:19]\n"
MF "v[24:27],a[224:227],v[184:187],v[24:27]\n"
TR "a[240:241],v5 offset:4096\n"
MF "v[24:27],a[184:187],v[180:183],v[24:27]\n"
"s_add_u32 m0,s79,53248\n"
TR "a[242:243],v5 offset:6144\n"
MF "v[24:27],a[144:147],v[176:179],v[24:27]\n"
LD "v196,s[48:51],s88 offen lds\n"
MF "v[32:35],a[148:151],v[176:179],v[32:35]\n"
TR "a[244:245],v5 offset:8192\n"
MF "v[32:35],a[188:191],v[180:183],v[32:35]\n"
TR "a[246:247],v5 offset:10240\n"
MF "v[32:35],a[228:231],v[184:187],v[32:35]\n"
LD "v197,s[48:51],s88 offen offset:1024 lds\n"
MF "v[40:43],a[232:235],v[184:187],v[40:43]\n"
MF "v[40:43],a[192:195],v[180:183],v[40:43]\n"
MF "v[40:43],a[152:155],v[176:179],v[40:43]\n"
MF "v[48:51],a[156:159],v[176:179],v[48:51]\n"
MF "v[48:51],a[196:199],v[180:183],v[48:51]\n"
LD "v198,s[48:51],s88 offen offset:2048 lds\n"
MF "v[48:51],a[236:239],v[184:187],v[48:51]\n"
"s_waitcnt lgkmcnt(0)\n"
MF "v[52:55],a[236:239],a[244:247],v[52:55]\n"
TR "v[176:177],v6\n"
MF "v[52:55],a[196:199],a[240:243],v[52:55]\n"
TR "v[178:179],v6 offset:2048\n"
MF "v[52:55],a[156:159],v[188:191],v[52:55]\n"
LD "v199,s[48:51],s88 offen offset:3072 lds\n"
MF "v[44:47],a[152:155],v[188:191],v[44:47]\n"
TR "v[180:181],v6 offset:4096\n"
MF "v[44:47],a[192:195],a[240:243],v[44:47]\n"
TR "v[182:183],v6 offset:6144\n"
MF "v[44:47],a[232:235],a[244:247],v[44:47]\n"
MF "v[36:39],a[228:231],a[244:247],v[36:39]\n"
TR "v[184:185],v6 offset:8192\n"
MF "v[36:39],a[188:191],a[240:243],v[36:39]\n"
"s_add_u32 m0,s79,57344\n"
TR "v[186:187],v6 offset:10240\n"
MF "v[36:39],a[148:151],v[188:191],v[36:39]\n"
LD "v200,s[48:51],s88 offen lds\n"
MF "v[28:31],a[144:147],v[188:191],v[28:31]\n"
MF "v[28:31],a[184:187],a[240:243],v[28:31]\n"
MF "v[28:31],a[224:227],a[244:247],v[28:31]\n"
MF "v[20:23],a[220:223],a[244:247],v[20:23]\n"
LD "v201,s[48:51],s88 offen offset:1024 lds\n"
MF "v[20:23],a[180:183],a[240:243],v[20:23]\n"
MF "v[20:23],a[140:143],v[188:191],v[20:23]\n"
"s_waitcnt lgkmcnt(0)\n"
MF "v[56:59],a[140:143],v[176:179],v[56:59]\n"
TR "v[188:189],v7\n"
MF "v[56:59],a[180:183],v[180:183],v[56:59]\n"
TR "v[190:191],v7 offset:2048\n"
MF "v[56:59],a[220:223],v[184:187],v[56:59]\n"
LD "v202,s[48:51],s88 offen offset:2048 lds\n"
MF "v[64:67],a[224:227],v[184:187],v[64:67]\n"
TR "a[240:241],v7 offset:4096\n"
"v_mov_b32 v244,0\n"
MF "v[64:67],a[184:187],v[180:183],v[64:67]\n"
TR "a[242:243],v7 offset:6144\n"
"v_mov_b32 v245,0\n"
MF "v[64:67],a[144:147],v[176:179],v[64:67]\n"
LD "v203,s[48:51],s88 offen offset:3072 lds\n"
MF "v[72:75],a[148:151],v[176:179],v[72:75]\n"
TR "a[244:245],v7 offset:8192\n"
"v_mov_b32 v246,0\n"
MF "v[72:75],a[188:191],v[180:183],v[72:75]\n"
TR "a[246:247],v7 offset:10240\n"
"v_mov_b32 v247,0\n"
MF "v[72:75],a[228:231],v[184:187],v[72:75]\n"
"v_mov_b32 v248,0\n"
"v_mov_b32 v249,0\n"
MF "v[80:83],a[232:235],v[184:187],v[80:83]\n"
"v_mov_b32 v250,0\n"
"v_mov_b32 v251,0\n"
MF "v[80:83],a[192:195],v[180:183],v[80:83]\n"
MF "v[80:83],a[152:155],v[176:179],v[80:83]\n"
MF "v[88:91],a[156:159],v[176:179],v[88:91]\n"
MF "v[88:91],a[196:199],v[180:183],v[88:91]\n"
"s_waitcnt vmcnt(24) lgkmcnt(0)\n"
MF "v[88:91],a[236:239],v[184:187],v[88:91]\n"
"s_barrier\n"
MF "v[92:95],a[236:239],a[244:247],v[92:95]\n"
"s_add_u32 s80,s88,122880\n"
TR "v[176:177],v0 offset:24576\n"
MF "v[92:95],a[196:199],a[240:243],v[92:95]\n"
"s_add_u32 m0,s79,98304\n"
"s_add_u32 s85,s85,1\n"
TR "v[178:179],v0 offset:26624\n"
MF "v[92:95],a[156:159],v[188:191],v[92:95]\n"
LD "v192,s[48:51],s80 offen lds\n"
MF "v[84:87],a[152:155],v[188:191],v[84:87]\n"
"s_nop 0\n"
TR "v[180:181],v0 offset:28672\n"
"ds_write_addtid_b32 v16 offset:24576\n"
MF "v[84:87],a[192:195],a[240:243],v[84:87]\n"
"s_nop 0\n"
TR "v[182:183],v0 offset:30720\n"
"ds_write_addtid_b32 v17 offset:24832\n"
MF "v[84:87],a[232:235],a[244:247],v[84:87]\n"
LD "v193,s[48:51],s80 offen offset:1024 lds\n"
MF "v[76:79],a[228:231],a[244:247],v[76:79]\n"
"s_nop 0\n"
TR "v[184:185],v0 offset:32768\n"
"ds_write_addtid_b32 v18 offset:25088\n"
MF "v[76:79],a[188:191],a[240:243],v[76:79]\n"
"s_nop 0\n"
TR "v[186:187],v0 offset:34816\n"
"ds_write_addtid_b32 v19 offset:25344\n"
MF "v[76:79],a[148:151],v[188:191],v[76:79]\n"
"s_nop 0\n"
"ds_write_addtid_b32 v20 offset:25600\n"
"ds_write_addtid_b32 v21 offset:25856\n"
MF "v[68:71],a[144:147],v[188:191],v[68:71]\n"
LD "v194,s[48:51],s80 offen offset:2048 lds\n"
MF "v[68:71],a[184:187],a[240:243],v[68:71]\n"
"s_nop 0\n"
"ds_write_addtid_b32 v22 offset:26112\n"
"ds_write_addtid_b32 v23 offset:26368\n"
MF "v[68:71],a[224:227],a[244:247],v[68:71]\n"
"s_nop 0\n"
"ds_write_addtid_b32 v24 offset:26624\n"
"ds_write_addtid_b32 v25 offset:26880\n"
MF "v[60:63],a[220:223],a[244:247],v[60:63]\n"
"s_nop 0\n"
"ds_write_addtid_b32 v26 offset:27136\n"
"ds_write_addtid_b32 v27 offset:27392\n"
MF "v[60:63],a[180:183],a[240:243],v[60:63]\n"
MF "v[60:63],a[140:143],v[188:191],v[60:63]\n"
"s_waitcnt lgkmcnt(9)\n"
"s_cmp_lg_u32 s85,5\n"
"s_cbranch_scc1 .Lpl05\n"
".Lpl09:\n"
MF "v[96:99],a[0:3],v[176:179],v[252:255]\n"
"s_nop 0\n"
TR "v[188:189],v1 offset:24576\n"
"ds_write_addtid_b32 v28 offset:27648\n"
MF "v[96:99],a[40:43],v[180:183],v[96:99]\n"
"s_nop 0\n"
TR "v[190:191],v1 offset:26624\n"
"ds_write_addtid_b32 v29 offset:27904\n"
MF "v[96:99],a[80:83],v[184:187],v[96:99]\n"
LD "v195,s[48:51],s80 offen offset:3072 lds\n"
MF "v[104:107],a[84:87],v[184:187],v[252:255]\n"
"s_add_u32 s60,s64,1\n"
TR "a[240:241],v1 offset:28672\n"
"ds_write_addtid_b32 v30 offset:28160\n"
MF "v[104:107],a[44:47],v[180:183],v[104:107]\n"
"s_add_u32 m0,s79,102400\n"
TR "a[242:243],v1 offset:30720\n"
"ds_write_addtid_b32 v31 offset:24320\n"
MF "v[104:107],a[4:7],v[176:179],v[104:107]\n"
LD "v196,s[48:51],s80 offen lds\n"
MF "v[112:115],a[8:11],v[176:179],v[252:255]\n"
"s_cmp_eq_u32 s60,8\n"
"s_cselect_b32 s60,0,s60\n"
TR "a[244:245],v1 offset:32768\n"
MF "v[112:115],a[48:51],v[180:183],v[112:115]\n"
"s_cmp_eq_u32 s60,10\n"
"s_cselect_b32 s60,s61,s60\n"
TR "a[246:247],v1 offset:34816\n"
MF "v[112:115],a[88:91],v[184:187],v[112:115]\n"
"s_sub_u32 s65,s60,s64\n"
"ds_write_addtid_b32 v32 offset:24576\n"
"ds_write_addtid_b32 v33 offset:24832\n"
MF "v[120:123],a[92:95],v[184:187],v[252:255]\n"
"s_lshl_b32 s65,s65,7\n"
"ds_write_addtid_b32 v34 offset:25088\n"
"ds_write_addtid_b32 v35 offset:25344\n"
MF "v[120:123],a[52:55],v[180:183],v[120:123]\n"
LD "v197,s[48:51],s80 offen offset:1024 lds\n"
MF "v[120:123],a[12:15],v[176:179],v[120:123]\n"
"s_mov_b32 s64,s60\n"
"ds_write_addtid_b32 v36 offset:25600\n"
"ds_write_addtid_b32 v37 offset:25856\n"
MF "v[128:131],a[16:19],v[176:179],v[252:255]\n"
"s_nop 0\n"
"ds_write_addtid_b32 v38 offset:26112\n"
"ds_write_addtid_b32 v39 offset:26368\n"
MF "v[128:131],a[56:59],v[180:183],v[128:131]\n"
"s_nop 0\n"
"ds_write_addtid_b32 v40 offset:26624\n"
"ds_write_addtid_b32 v41 offset:26880\n"
MF "v[128:131],a[96:99],v[184:187],v[128:131]\n"
"s_waitcnt lgkmcnt(10)\n"
LD "v198,s[48:51],s80 offen offset:2048 lds\n"
MF "v[132:135],a[96:99],a[244:247],v[252:255]\n"
"s_nop 0\n"
TR "v[176:177],v2 offset:24576\n"
"ds_write_addtid_b32 v42 offset:27136\n"
MF "v[132:135],a[56:59],a[240:243],v[132:135]\n"
"s_nop 0\n"
TR "v[178:179],v2 offset:26624\n"
"ds_write_addtid_b32 v43 offset:27392\n"
MF "v[132:135],a[16:19],v[188:191],v[132:135]\n"
"s_nop 0\n"
"ds_write_addtid_b32 v44 offset:27648\n"
"ds_write_addtid_b32 v45 offset:27904\n"
MF "v[124:127],a[12:15],v[188:191],v[252:255]\n"
"s_nop 0\n"
TR "v[180:181],v2 offset:28672\n"
"ds_write_addtid_b32 v46 offset:28160\n"
MF "v[124:127],a[52:55],a[240:243],v[124:127]\n"
"s_nop 0\n"
TR "v[182:183],v2 offset:30720\n"
"ds_write_addtid_b32 v47 offset:28416\n"
MF "v[124:127],a[92:95],a[244:247],v[124:127]\n"
LD "v199,s[48:51],s80 offen offset:3072 lds\n"
MF "v[116:119],a[88:91],a[244:247],v[252:255]\n"
"s_nop 0\n"
TR "v[184:185],v2 offset:32768\n"
"ds_write_addtid_b32 v48 offset:28672\n"
MF "v[116:119],a[48:51],a[240:243],v[116:119]\n"
"s_add_u32 m0,s79,106496\n"
TR "v[186:187],v2 offset:34816\n"
"ds_write_addtid_b32 v49 offset:24832\n"
MF "v[116:119],a[8:11],v[188:191],v[116:119]\n"
LD "v200,s[48:51],s80 offen lds\n"
MF "v[108:111],a[4:7],v[188:191],v[252:255]\n"
"s_nop 0\n"
"ds_write_addtid_b32 v50 offset:25088\n"
"ds_write_addtid_b32 v51 offset:25344\n"
MF "v[108:111],a[44:47],a[240:243],v[108:111]\n"
"s_nop 0\n"
"ds_write_addtid_b32 v52 offset:25600\n"
"ds_write_addtid_b32 v53 offset:25856\n"
MF "v[108:111],a[84:87],a[244:247],v[108:111]\n"
"s_nop 0\n"
"ds_write_addtid_b32 v54 offset:26112\n"
"ds_write_addtid_b32 v55 offset:26368\n"
MF "v[100:103],a[80:83],a[244:247],v[252:255]\n"
MF "v[100:103],a[40:43],a[240:243],v[100:103]\n"
LD "v201,s[48:51],s80 offen offset:1024 lds\n"
MF "v[100:103],a[0:3],v[188:191],v[100:103]\n"
"s_waitcnt lgkmcnt(7)\n"
MF "v[136:139],a[0:3],v[176:179],v[252:255]\n"
TR "v[188:189],v3 offset:24576\n"
MF "v[136:139],a[40:43],v[180:183],v[136:139]\n"
TR "v[190:191],v3 offset:26624\n"
MF "v[136:139],a[80:83],v[184:187],v[136:139]\n"
LD "v202,s[48:51],s80 offen offset:2048 lds\n"
MF "v[144:147],a[84:87],v[184:187],v[252:255]\n"
TR "a[240:241],v3 offset:28672\n"
MF "v[144:147],a[44:47],v[180:183],v[144:147]\n"
TR "a[242:243],v3 offset:30720\n"
MF "v[144:147],a[4:7],v[176:179],v[144:147]\n"
LD "v203,s[48:51],s80 offen offset:3072 lds\n"
MF "v[152:155],a[8:11],v[176:179],v[252:255]\n"
TR "a[244:245],v3 offset:32768\n"
MF "v[152:155],a[48:51],v[180:183],v[152:155]\n"
TR "a[246:247],v3 offset:34816\n"
MF "v[152:155],a[88:91],v[184:187],v[152:155]\n"
MF "v[160:163],a[92:95],v[184:187],v[252:255]\n"
MF "v[160:163],a[52:55],v[180:183],v[160:163]\n"
MF "v[160:163],a[12:15],v[176:179],v[160:163]\n"
MF "v[168:171],a[16:19],v[176:179],v[252:255]\n"
MF "v[168:171],a[56:59],v[180:183],v[168:171]\n"
"s_waitcnt vmcnt(24) lgkmcnt(0)\n"
MF "v[168:171],a[96:99],v[184:187],v[168:171]\n"
"s_barrier\n"
MF "v[172:175],a[96:99],a[244:247],v[252:255]\n"
"s_add_u32 s89,s89,s65\n"
TR "v[176:177],v0\n"
MF "v[172:175],a[56:59],a[240:243],v[172:175]\n"
"s_add_u32 m0,s79,24576\n"
TR "v[178:179],v0 offset:2048\n"
MF "v[172:175],a[16:19],v[188:191],v[172:175]\n"
LD "v192,s[48:51],s89 offen lds\n"
MF "v[164:167],a[12:15],v[188:191],v[252:255]\n"
TR "v[180:181],v0 offset:4096\n"
MF "v[164:167],a[52:55],a[240:243],v[164:167]\n"
TR "v[182:183],v0 offset:6144\n"
MF "v[164:167],a[92:95],a[244:247],v[164:167]\n"
LD "v193,s[48:51],s89 offen offset:1024 lds\n"
MF "v[156:159],a[88:91],a[244:247],v[252:255]\n"
TR "v[184:185],v0 offset:8192\n"
MF "v[156:159],a[48:51],a[240:243],v[156:159]\n"
"s_nop 0\n"
TR "v[186:187],v0 offset:10240\n"
"ds_write_addtid_b32 v56 offset:49152\n"
MF "v[156:159],a[8:11],v[188:191],v[156:159]\n"
"s_nop 0\n"
"ds_write_addtid_b32 v57 offset:49408\n"
"ds_write_addtid_b32 v58 offset:49664\n"
MF "v[148:151],a[4:7],v[188:191],v[252:255]\n"
LD "v194,s[48:51],s89 offen offset:2048 lds\n"
MF "v[148:151],a[44:47],a[240:243],v[148:151]\n"
"buffer_load_dword v10,v12,s[40:43],s86 offen sc1\n"
MF "v[148:151],a[84:87],a[244:247],v[148:151]\n"
"s_nop 0\n"
"ds_write_addtid_b32 v59 offset:49920\n"
"ds_write_addtid_b32 v60 offset:50176\n"
MF "v[140:143],a[80:83],a[244:247],v[252:255]\n"
"s_nop 0\n"
"ds_write_addtid_b32 v61 offset:50432\n"
"ds_write_addtid_b32 v62 offset:50688\n"
MF "v[140:143],a[40:43],a[240:243],v[140:143]\n"
"s_nop 0\n"
"ds_write_addtid_b32 v63 offset:50944\n"
"ds_write_addtid_b32 v64 offset:51200\n"
MF "v[140:143],a[0:3],v[188:191],v[140:143]\n"
"s_waitcnt lgkmcnt(9)\n"
LD "v195,s[48:51],s89 offen offset:3072 lds\n"
MF "v[96:99],a[120:123],v[176:179],v[96:99]\n"
"s_nop 0\n"
TR "v[188:189],v1\n"
"ds_write_addtid_b32 v65 offset:51456\n"
MF "v[96:99],a[160:163],v[180:183],v[96:99]\n"
"s_nop 0\n"
TR "v[190:191],v1 offset:2048\n"
"ds_write_addtid_b32 v66 offset:51712\n"
MF "v[96:99],a[200:203],v[184:187],v[96:99]\n"
"s_nop 0\n"
"ds_write_addtid_b32 v67 offset:51968\n"
"ds_write_addtid_b32 v68 offset:52224\n"
MF "v[104:107],a[204:207],v[184:187],v[104:107]\n"
".Lpg28h:\n"
"s_cbranch_execz .Lpgrole1_h\n"
"s_nop 0\n"
TR "a[240:241],v1 offset:4096\n"
"ds_write_addtid_b32 v69 offset:52480\n"
MF "v[104:107],a[164:167],v[180:183],v[104:107]\n"
"s_add_u32 m0,s79,28672\n"
TR "a[242:243],v1 offset:6144\n"
"ds_write_addtid_b32 v70 offset:48640\n"
MF "v[104:107],a[124:127],v[176:179],v[104:107]\n"
LD "v196,s[48:51],s89 offen lds\n"
MF "v[112:115],a[128:131],v[176:179],v[112:115]\n"
"s_nop 0\n"
TR "a[244:245],v1 offset:8192\n"
"ds_write_addtid_b32 v71 offset:48896\n"
MF "v[112:115],a[168:171],v[180:183],v[112:115]\n"
"s_nop 0\n"
TR "a[246:247],v1 offset:10240\n"
"ds_write_addtid_b32 v72 offset:49152\n"
MF "v[112:115],a[208:211],v[184:187],v[112:115]\n"
LD "v197,s[48:51],s89 offen offset:1024 lds\n"
MF "v[120:123],a[212:215],v[184:187],v[120:123]\n"
"s_nop 0\n"
"ds_write_addtid_b32 v73 offset:49408\n"
"ds_write_addtid_b32 v74 offset:49664\n"
MF "v[120:123],a[172:175],v[180:183],v[120:123]\n"
"s_nop 0\n"
"ds_write_addtid_b32 v75 offset:49920\n"
"ds_write_addtid_b32 v76 offset:50176\n"
MF "v[120:123],a[132:135],v[176:179],v[120:123]\n"
"s_nop 0\n"
"ds_write_addtid_b32 v77 offset:50432\n"
"ds_write_addtid_b32 v78 offset:50688\n"
MF "v[128:131],a[136:139],v[176:179],v[128:131]\n"
"s_nop 0\n"
"ds_write_addtid_b32 v79 offset:50944\n"
"ds_write_addtid_b32 v80 offset:51200\n"
MF "v[128:131],a[176:179],v[180:183],v[128:131]\n"
LD "v198,s[48:51],s89 offen offset:2048 lds\n"
MF "v[128:131],a[216:219],v[184:187],v[128:131]\n"
"s_waitcnt lgkmcnt(9)\n"
"ds_write_addtid_b32 v81 offset:51456\n"
"ds_write_addtid_b32 v82 offset:51712\n"
MF "v[132:135],a[216:219],a[244:247],v[132:135]\n"
"s_nop 0\n"
TR "v[176:177],v2\n"
"ds_write_addtid_b32 v83 offset:51968\n"
MF "v[132:135],a[176:179],a[240:243],v[132:135]\n"
"s_nop 0\n"
TR "v[178:179],v2 offset:2048\n"
"ds_write_addtid_b32 v84 offset:52224\n"
MF "v[132:135],a[136:139],v[188:191],v[132:135]\n"
LD "v199,s[48:51],s89 offen offset:3072 lds\n"
MF "v[124:127],a[132:135],v[188:191],v[124:127]\n"
"s_nop 0\n"
TR "v[180:181],v2 offset:4096\n"
"ds_write_addtid_b32 v85 offset:52480\n"
MF "v[124:127],a[172:175],a[240:243],v[124:127]\n"
"s_nop 0\n"
TR "v[182:183],v2 offset:6144\n"
"ds_write_addtid_b32 v86 offset:52736\n"
MF "v[124:127],a[212:215],a[244:247],v[124:127]\n"
"s_nop 0\n"
"ds_write_addtid_b32 v87 offset:52992\n"
"ds_write_addtid_b32 v88 offset:53248\n"
MF "v[116:119],a[208:211],a[244:247],v[116:119]\n"
"s_nop 0\n"
TR "v[184:185],v2 offset:8192\n"
"ds_write_addtid_b32 v89 offset:53504\n"
MF "v[116:119],a[168:171],a[240:243],v[116:119]\n"
"s_add_u32 m0,s79,32768\n"
TR "v[186:187],v2 offset:10240\n"
"ds_write_addtid_b32 v90 offset:49664\n"
MF "v[116:119],a[128:131],v[188:191],v[116:119]\n"
LD "v200,s[48:51],s89 offen lds\n"
MF "v[108:111],a[124:127],v[188:191],v[108:111]\n"
"s_nop 0\n"
"ds_write_addtid_b32 v91 offset:49920\n"
"ds_write_addtid_b32 v92 offset:50176\n"
MF "v[108:111],a[164:167],a[240:243],v[108:111]\n"
"s_nop 0\n"
"ds_write_addtid_b32 v93 offset:50432\n"
"ds_write_addtid_b32 v94 offset:50688\n"
MF "v[108:111],a[204:207],a[244:247],v[108:111]\n"
"ds_write_addtid_b32 v95 offset:50944\n"
MF "v[100:103],a[200:203],a[244:247],v[100:103]\n"
LD "v201,s[48:51],s89 offen offset:1024 lds\n"
MF "v[100:103],a[160:163],a[240:243],v[100:103]\n"
MF "v[100:103],a[120:123],v[188:191],v[100:103]\n"
"s_waitcnt lgkmcnt(6)\n"
MF "v[136:139],a[120:123],v[176:179],v[136:139]\n"
TR "v[188:189],v3\n"
MF "v[136:139],a[160:163],v[180:183],v[136:139]\n"
TR "v[190:191],v3 offset:2048\n"
MF "v[136:139],a[200:203],v[184:187],v[136:139]\n"
LD "v202,s[48:51],s89 offen offset:2048 lds\n"
MF "v[144:147],a[204:207],v[184:187],v[144:147]\n"
TR "a[240:241],v3 offset:4096\n"
MF "v[144:147],a[164:167],v[180:183],v[144:147]\n"
TR "a[242:243],v3 offset:6144\n"
MF "v[144:147],a[124:127],v[176:179],v[144:147]\n"
LD "v203,s[48:51],s89 offen offset:3072 lds\n"
MF "v[152:155],a[128:131],v[176:179],v[152:155]\n"
TR "a[244:245],v3 offset:8192\n"
MF "v[152:155],a[168:171],v[180:183],v[152:155]\n"
TR "a[246:247],v3 offset:10240\n"
MF "v[152:155],a[208:211],v[184:187],v[152:155]\n"
MF "v[160:163],a[212:215],v[184:187],v[160:163]\n"
MF "v[160:163],a[172:175],v[180:183],v[160:163]\n"
MF "v[160:163],a[132:135],v[176:179],v[160:163]\n"
MF "v[168:171],a[136:139],v[176:179],v[168:171]\n"
MF "v[168:171],a[176:179],v[180:183],v[168:171]\n"
"s_waitcnt vmcnt(25) lgkmcnt(0)\n"
MF "v[168:171],a[216:219],v[184:187],v[168:171]\n"
"s_barrier\n"
MF "v[172:175],a[216:219],a[244:247],v[172:175]\n"
"s_add_u32 s80,s89,122880\n"
TR "v[176:177],v0 offset:49152\n"
MF "v[172:175],a[176:179],a[240:243],v[172:175]\n"
"s_add_u32 m0,s79,0\n"
"s_mov_b32 s82,s70\n"
TR "v[178:179],v0 offset:51200\n"
MF "v[172:175],a[136:139],v[188:191],v[172:175]\n"
LD "v192,s[48:51],s80 offen lds\n"
MF "v[164:167],a[132:135],v[188:191],v[164:167]\n"
TR "v[180:181],v0 offset:53248\n"
MF "v[164:167],a[172:175],a[240:243],v[164:167]\n"
TR "v[182:183],v0 offset:55296\n"
MF "v[164:167],a[212:215],a[244:247],v[164:167]\n"
LD "v193,s[48:51],s80 offen offset:1024 lds\n"
MF "v[156:159],a[208:211],a[244:247],v[156:159]\n"
TR "v[184:185],v0 offset:57344\n"
MF "v[156:159],a[168:171],a[240:243],v[156:159]\n"
TR "v[186:187],v0 offset:59392\n"
MF "v[156:159],a[128:131],v[188:191],v[156:159]\n"
MF "v[148:151],a[124:127],v[188:191],v[148:151]\n"
MF "v[148:151],a[164:167],a[240:243],v[148:151]\n"
LD "v194,s[48:51],s80 offen offset:2048 lds\n"
MF "v[148:151],a[204:207],a[244:247],v[148:151]\n"
MF "v[140:143],a[200:203],a[244:247],v[140:143]\n"
MF "v[140:143],a[160:163],a[240:243],v[140:143]\n"
MF "v[140:143],a[120:123],v[188:191],v[140:143]\n"
"s_waitcnt lgkmcnt(0)\n"
LD "v195,s[48:51],s80 offen offset:3072 lds\n"
MF "v[96:99],a[20:23],v[176:179],v[96:99]\n"
TR "v[188:189],v1 offset:49152\n"
MF "v[96:99],a[60:63],v[180:183],v[96:99]\n"
TR "v[190:191],v1 offset:51200\n"
MF "v[96:99],a[100:103],v[184:187],v[96:99]\n"
MF "v[104:107],a[104:107],v[184:187],v[104:107]\n"
TR "a[240:241],v1 offset:53248\n"
MF "v[104:107],a[64:67],v[180:183],v[104:107]\n"
"s_add_u32 m0,s79,4096\n"
TR "a[242:243],v1 offset:55296\n"
MF "v[104:107],a[24:27],v[176:179],v[104:107]\n"
LD "v196,s[48:51],s80 offen lds\n"
MF "v[112:115],a[28:31],v[176:179],v[112:115]\n"
TR "a[244:245],v1 offset:57344\n"
MF "v[112:115],a[68:71],v[180:183],v[112:115]\n"
TR "a[246:247],v1 offset:59392\n"
MF "v[112:115],a[108:111],v[184:187],v[112:115]\n"
LD "v197,s[48:51],s80 offen offset:1024 lds\n"
MF "v[120:123],a[112:115],v[184:187],v[120:123]\n"
MF "v[120:123],a[72:75],v[180:183],v[120:123]\n"
MF "v[120:123],a[32:35],v[176:179],v[120:123]\n"
MF "v[128:131],a[36:39],v[176:179],v[128:131]\n"
MF "v[128:131],a[76:79],v[180:183],v[128:131]\n"
LD "v198,s[48:51],s80 offen offset:2048 lds\n"
MF "v[128:131],a[116:119],v[184:187],v[128:131]\n"
"s_waitcnt lgkmcnt(0)\n"
MF "v[132:135],a[116:119],a[244:247],v[132:135]\n"
TR "v[176:177],v2 offset:49152\n"
MF "v[132:135],a[76:79],a[240:243],v[132:135]\n"
TR "v[178:179],v2 offset:51200\n"
MF "v[132:135],a[36:39],v[188:191],v[132:135]\n"
LD "v199,s[48:51],s80 offen offset:3072 lds\n"
MF "v[124:127],a[32:35],v[188:191],v[124:127]\n"
TR "v[180:181],v2 offset:53248\n"
MF "v[124:127],a[72:75],a[240:243],v[124:127]\n"
TR "v[182:183],v2 offset:55296\n"
MF "v[124:127],a[112:115],a[244:247],v[124:127]\n"
MF "v[116:119],a[108:111],a[244:247],v[116:119]\n"
TR "v[184:185],v2 offset:57344\n"
MF "v[116:119],a[68:71],a[240:243],v[116:119]\n"
"s_add_u32 m0,s79,8192\n"
TR "v[186:187],v2 offset:59392\n"
MF "v[116:119],a[28:31],v[188:191],v[116:119]\n"
LD "v200,s[48:51],s80 offen lds\n"
MF "v[108:111],a[24:27],v[188:191],v[108:111]\n"
MF "v[108:111],a[64:67],a[240:243],v[108:111]\n"
MF "v[108:111],a[104:107],a[244:247],v[108:111]\n"
MF "v[100:103],a[100:103],a[244:247],v[100:103]\n"
LD "v201,s[48:51],s80 offen offset:1024 lds\n"
MF "v[100:103],a[60:63],a[240:243],v[100:103]\n"
"s_waitcnt vmcnt(19)\n"
"s_cmp_lt_u32 s85,6\n"
MF "v[100:103],a[20:23],v[188:191],v[100:103]\n"
"s_cbranch_scc1 .Ljs4\n"
"v_mov_b32 v236,v228\n"
"v_mov_b32 v237,v229\n"
MF "v[96:99],a[252:255],v[208:211],v[96:99]\n"
MF "v[104:107],a[252:255],v[212:215],v[104:107]\n"
MF "v[112:115],a[252:255],v[216:219],v[112:115]\n"
MF "v[120:123],a[252:255],v[220:223],v[120:123]\n"
MF "v[128:131],a[252:255],v[224:227],v[128:131]\n"
MF "v[100:103],a[248:251],v[208:211],v[100:103]\n"
MF "v[108:111],a[248:251],v[212:215],v[108:111]\n"
MF "v[116:119],a[248:251],v[216:219],v[116:119]\n"
MF "v[124:127],a[248:251],v[220:223],v[124:127]\n"
MF "v[132:135],a[248:251],v[224:227],v[132:135]\n"
MF "v[248:251],v[230:233],a[250:253],v[248:251]\n"
MF "v[244:247],v[234:237],a[250:253],v[244:247]\n"
".Ljs4:\n"
"s_waitcnt lgkmcnt(0)\n"
MF "v[136:139],a[20:23],v[176:179],v[136:139]\n"
TR "v[188:189],v3 offset:49152\n"
"v_sub_u32 v10,v10,v11\n"
MF "v[136:139],a[60:63],v[180:183],v[136:139]\n"
TR "v[190:191],v3 offset:51200\n"
MF "v[136:139],a[100:103],v[184:187],v[136:139]\n"
LD "v202,s[48:51],s80 offen offset:2048 lds\n"
MF "v[144:147],a[104:107],v[184:187],v[144:147]\n"
TR "a[240:241],v3 offset:53248\n"
MF "v[144:147],a[64:67],v[180:183],v[144:147]\n"
TR "a[242:243],v3 offset:55296\n"
MF "v[144:147],a[24:27],v[176:179],v[144:147]\n"
LD "v203,s[48:51],s80 offen offset:3072 lds\n"
MF "v[152:155],a[28:31],v[176:179],v[152:155]\n"
TR "a[244:245],v3 offset:57344\n"
MF "v[152:155],a[68:71],v[180:183],v[152:155]\n"
TR "a[246:247],v3 offset:59392\n"
MF "v[152:155],a[108:111],v[184:187],v[152:155]\n"
".Lnf0:\n"
"v_readlane_b32 s58,v10,s67\n"
"s_cmp_lt_u32 s58,s85\n"
"s_cbranch_scc1 .Lsp0\n"
".Lnb0:\n"
MF "v[160:163],a[112:115],v[184:187],v[160:163]\n"
"s_add_u32 s70,s70,20480\n"
".Lnf1:\n"
"v_cmp_gt_u32 vcc,4,v10\n"
"s_cbranch_vccnz .Lsp1\n"
".Lnb1:\n"
MF "v[160:163],a[72:75],v[180:183],v[160:163]\n"
MF "v[160:163],a[32:35],v[176:179],v[160:163]\n"
MF "v[168:171],a[36:39],v[176:179],v[168:171]\n"
MF "v[168:171],a[76:79],v[180:183],v[168:171]\n"
"s_waitcnt vmcnt(25) lgkmcnt(0)\n"
MF "v[168:171],a[116:119],v[184:187],v[168:171]\n"
"s_barrier\n"
MF "v[172:175],a[116:119],a[244:247],v[172:175]\n"
"s_add_u32 s88,s88,s65\n"
TR "v[176:177],v4\n"
MF "v[172:175],a[76:79],a[240:243],v[172:175]\n"
"s_add_u32 m0,s79,49152\n"
TR "v[178:179],v4 offset:2048\n"
MF "v[172:175],a[36:39],v[188:191],v[172:175]\n"
LD "v192,s[48:51],s88 offen lds\n"
MF "v[164:167],a[32:35],v[188:191],v[164:167]\n"
TR "v[180:181],v4 offset:4096\n"
MF "v[164:167],a[72:75],a[240:243],v[164:167]\n"
TR "v[182:183],v4 offset:6144\n"
MF "v[164:167],a[112:115],a[244:247],v[164:167]\n"
LD "v193,s[48:51],s88 offen offset:1024 lds\n"
MF "v[156:159],a[108:111],a[244:247],v[156:159]\n"
TR "v[184:185],v4 offset:8192\n"
MF "v[156:159],a[68:71],a[240:243],v[156:159]\n"
"s_add_u32 s2,s2,204800\n"
TR "v[186:187],v4 offset:10240\n"
MF "v[156:159],a[28:31],v[188:191],v[156:159]\n"
LD "v[228:231],v9,s[40:43],s2 offen sc1\n"
MF "v[148:151],a[24:27],v[188:191],v[148:151]\n"
MF "v[148:151],a[64:67],a[240:243],v[148:151]\n"
LD "v194,s[48:51],s88 offen offset:2048 lds\n"
MF "v[148:151],a[104:107],a[244:247],v[148:151]\n"
MF "v[140:143],a[100:103],a[244:247],v[140:143]\n"
MF "v[140:143],a[60:63],a[240:243],v[140:143]\n"
MF "v[140:143],a[20:23],v[188:191],v[140:143]\n"
"s_waitcnt lgkmcnt(0)\n"
LD "v195,s[48:51],s88 offen offset:3072 lds\n"
MF "v[96:99],a[140:143],v[176:179],v[96:99]\n"
TR "v[188:189],v5\n"
MF "v[96:99],a[180:183],v[180:183],v[96:99]\n"
"s_add_u32 s2,s2,204800\n"
TR "v[190:191],v5 offset:2048\n"
MF "v[96:99],a[220:223],v[184:187],v[96:99]\n"
LD "v[232:235],v9,s[40:43],s2 offen sc1\n"
MF "v[104:107],a[224:227],v[184:187],v[104:107]\n"
TR "a[240:241],v5 offset:4096\n"
MF "v[104:107],a[184:187],v[180:183],v[104:107]\n"
"s_add_u32 m0,s79,53248\n"
TR "a[242:243],v5 offset:6144\n"
MF "v[104:107],a[144:147],v[176:179],v[104:107]\n"
LD "v196,s[48:51],s88 offen lds\n"
MF "v[112:115],a[148:151],v[176:179],v[112:115]\n"
TR "a[244:245],v5 offset:8192\n"
MF "v[112:115],a[188:191],v[180:183],v[112:115]\n"
TR "a[246:247],v5 offset:10240\n"
MF "v[112:115],a[228:231],v[184:187],v[112:115]\n"
LD "v197,s[48:51],s88 offen offset:1024 lds\n"
MF "v[120:123],a[232:235],v[184:187],v[120:123]\n"
MF "v[120:123],a[192:195],v[180:183],v[120:123]\n"
MF "v[120:123],a[152:155],v[176:179],v[120:123]\n"
LD "v[208:211],v9,s[40:43],s82 offen sc1\n"
MF "v[128:131],a[156:159],v[176:179],v[128:131]\n"
MF "v[128:131],a[196:199],v[180:183],v[128:131]\n"
LD "v198,s[48:51],s88 offen offset:2048 lds\n"
MF "v[128:131],a[236:239],v[184:187],v[128:131]\n"
"s_waitcnt lgkmcnt(0)\n"
MF "v[132:135],a[236:239],a[244:247],v[132:135]\n"
TR "v[176:177],v6\n"
MF "v[132:135],a[196:199],a[240:243],v[132:135]\n"
TR "v[178:179],v6 offset:2048\n"
MF "v[132:135],a[156:159],v[188:191],v[132:135]\n"
LD "v199,s[48:51],s88 offen offset:3072 lds\n"
MF "v[124:127],a[152:155],v[188:191],v[124:127]\n"
TR "v[180:181],v6 offset:4096\n"
MF "v[124:127],a[192:195],a[240:243],v[124:127]\n"
TR "v[182:183],v6 offset:6144\n"
MF "v[124:127],a[232:235],a[244:247],v[124:127]\n"
LD "v[212:215],v9,s[40:43],s82 offen offset:1024 sc1\n"
MF "v[116:119],a[228:231],a[244:247],v[116:119]\n"
TR "v[184:185],v6 offset:8192\n"
MF "v[116:119],a[188:191],a[240:243],v[116:119]\n"
"s_add_u32 m0,s79,57344\n"
TR "v[186:187],v6 offset:10240\n"
MF "v[116:119],a[148:151],v[188:191],v[116:119]\n"
LD "v200,s[48:51],s88 offen lds\n"
MF "v[108:111],a[144:147],v[188:191],v[108:111]\n"
MF "v[108:111],a[184:187],a[240:243],v[108:111]\n"
MF "v[108:111],a[224:227],a[244:247],v[108:111]\n"
MF "v[100:103],a[220:223],a[244:247],v[100:103]\n"
LD "v201,s[48:51],s88 offen offset:1024 lds\n"
MF "v[100:103],a[180:183],a[240:243],v[100:103]\n"
MF "v[100:103],a[140:143],v[188:191],v[100:103]\n"
"s_waitcnt lgkmcnt(0)\n"
LD "v[216:219],v9,s[40:43],s82 offen offset:2048 sc1\n"
MF "v[136:139],a[140:143],v[176:179],v[136:139]\n"
TR "v[188:189],v7\n"
MF "v[136:139],a[180:183],v[180:183],v[136:139]\n"
TR "v[190:191],v7 offset:2048\n"
MF "v[136:139],a[220:223],v[184:187],v[136:139]\n"
LD "v202,s[48:51],s88 offen offset:2048 lds\n"
MF "v[144:147],a[224:227],v[184:187],v[144:147]\n"
TR "a[240:241],v7 offset:4096\n"
MF "v[144:147],a[184:187],v[180:183],v[144:147]\n"
TR "a[242:243],v7 offset:6144\n"
MF "v[144:147],a[144:147],v[176:179],v[144:147]\n"
LD "v203,s[48:51],s88 offen offset:3072 lds\n"
MF "v[152:155],a[148:151],v[176:179],v[152:155]\n"
TR "a[244:245],v7 offset:8192\n"
MF "v[152:155],a[188:191],v[180:183],v[152:155]\n"
TR "a[246:247],v7 offset:10240\n"
MF "v[152:155],a[228:231],v[184:187],v[152:155]\n"
LD "v[220:223],v9,s[40:43],s82 offen offset:3072 sc1\n"
MF "v[160:163],a[232:235],v[184:187],v[160:163]\n"
MF "v[160:163],a[192:195],v[180:183],v[160:163]\n"
MF "v[160:163],a[152:155],v[176:179],v[160:163]\n"
MF "v[168:171],a[156:159],v[176:179],v[168:171]\n"
"s_add_u32 s82,s82,4096\n"
MF "v[168:171],a[196:199],v[180:183],v[168:171]\n"
"s_waitcnt vmcnt(30) lgkmcnt(0)\n"
LD "v[224:227],v9,s[40:43],s82 offen sc1\n"
MF "v[168:171],a[236:239],v[184:187],v[168:171]\n"
"s_barrier\n"
MF "v[172:175],a[236:239],a[244:247],v[172:175]\n"
"s_add_u32 s80,s88,122880\n"
TR "v[176:177],v0 offset:24576\n"
MF "v[172:175],a[196:199],a[240:243],v[172:175]\n"
"s_add_u32 m0,s79,98304\n"
"s_add_u32 s85,s85,1\n"
TR "v[178:179],v0 offset:26624\n"
MF "v[172:175],a[156:159],v[188:191],v[172:175]\n"
LD "v192,s[48:51],s80 offen lds\n"
MF "v[164:167],a[152:155],v[188:191],v[164:167]\n"
"s_nop 0\n"
TR "v[180:181],v0 offset:28672\n"
"ds_write_addtid_b32 v96 offset:24576\n"
MF "v[164:167],a[192:195],a[240:243],v[164:167]\n"
"s_nop 0\n"
TR "v[182:183],v0 offset:30720\n"
"ds_write_addtid_b32 v97 offset:24832\n"
MF "v[164:167],a[232:235],a[244:247],v[164:167]\n"
LD "v193,s[48:51],s80 offen offset:1024 lds\n"
MF "v[156:159],a[228:231],a[244:247],v[156:159]\n"
"s_nop 0\n"
TR "v[184:185],v0 offset:32768\n"
"ds_write_addtid_b32 v98 offset:25088\n"
MF "v[156:159],a[188:191],a[240:243],v[156:159]\n"
"s_nop 0\n"
TR "v[186:187],v0 offset:34816\n"
"ds_write_addtid_b32 v99 offset:25344\n"
MF "v[156:159],a[148:151],v[188:191],v[156:159]\n"
"s_nop 0\n"
"ds_write_addtid_b32 v100 offset:25600\n"
"ds_write_addtid_b32 v101 offset:25856\n"
MF "v[148:151],a[144:147],v[188:191],v[148:151]\n"
LD "v194,s[48:51],s80 offen offset:2048 lds\n"
MF "v[148:151],a[184:187],a[240:243],v[148:151]\n"
"s_nop 0\n"
"ds_write_addtid_b32 v102 offset:26112\n"
"ds_write_addtid_b32 v103 offset:26368\n"
MF "v[148:151],a[224:227],a[244:247],v[148:151]\n"
"s_nop 0\n"
"ds_write_addtid_b32 v104 offset:26624\n"
"ds_write_addtid_b32 v105 offset:26880\n"
MF "v[140:143],a[220:223],a[244:247],v[140:143]\n"
"s_nop 0\n"
"ds_write_addtid_b32 v106 offset:27136\n"
"ds_write_addtid_b32 v107 offset:27392\n"
MF "v[140:143],a[180:183],a[240:243],v[140:143]\n"
MF "v[140:143],a[140:143],v[188:191],v[140:143]\n"
"s_waitcnt lgkmcnt(9)\n"
MF "v[16:19],a[0:3],v[176:179],v[252:255]\n"
"s_nop 0\n"
TR "v[188:189],v1 offset:24576\n"
"ds_write_addtid_b32 v108 offset:27648\n"
MF "v[16:19],a[40:43],v[180:183],v[16:19]\n"
"s_nop 0\n"
TR "v[190:191],v1 offset:26624\n"
"ds_write_addtid_b32 v109 offset:27904\n"
MF "v[16:19],a[80:83],v[184:187],v[16:19]\n"
LD "v195,s[48:51],s80 offen offset:3072 lds\n"
MF "v[24:27],a[84:87],v[184:187],v[252:255]\n"
"s_add_u32 s60,s64,1\n"
TR "a[240:241],v1 offset:28672\n"
"ds_write_addtid_b32 v110 offset:28160\n"
MF "v[24:27],a[44:47],v[180:183],v[24:27]\n"
"s_add_u32 m0,s79,102400\n"
TR "a[242:243],v1 offset:30720\n"
"ds_write_addtid_b32 v111 offset:24320\n"
MF "v[24:27],a[4:7],v[176:179],v[24:27]\n"
LD "v196,s[48:51],s80 offen lds\n"
MF "v[32:35],a[8:11],v[176:179],v[252:255]\n"
"s_cmp_eq_u32 s60,8\n"
"s_cselect_b32 s60,0,s60\n"
TR "a[244:245],v1 offset:32768\n"
MF "v[32:35],a[48:51],v[180:183],v[32:35]\n"
"s_cmp_eq_u32 s60,10\n"
"s_cselect_b32 s60,s61,s60\n"
TR "a[246:247],v1 offset:34816\n"
MF "v[32:35],a[88:91],v[184:187],v[32:35]\n"
"s_sub_u32 s65,s60,s64\n"
"ds_write_addtid_b32 v112 offset:24576\n"
"ds_write_addtid_b32 v113 offset:24832\n"
MF "v[40:43],a[92:95],v[184:187],v[252:255]\n"
"s_lshl_b32 s65,s65,7\n"
"ds_write_addtid_b32 v114 offset:25088\n"
"ds_write_addtid_b32 v115 offset:25344\n"
MF "v[40:43],a[52:55],v[180:183],v[40:43]\n"
LD "v197,s[48:51],s80 offen offset:1024 lds\n"
MF "v[40:43],a[12:15],v[176:179],v[40:43]\n"
"s_mov_b32 s64,s60\n"
"ds_write_addtid_b32 v116 offset:25600\n"
"ds_write_addtid_b32 v117 offset:25856\n"
MF "v[48:51],a[16:19],v[176:179],v[252:255]\n"
"s_nop 0\n"
"ds_write_addtid_b32 v118 offset:26112\n"
"ds_write_addtid_b32 v119 offset:26368\n"
MF "v[48:51],a[56:59],v[180:183],v[48:51]\n"
"s_nop 0\n"
"ds_write_addtid_b32 v120 offset:26624\n"
"ds_write_addtid_b32 v121 offset:26880\n"
MF "v[48:51],a[96:99],v[184:187],v[48:51]\n"
"s_waitcnt lgkmcnt(10)\n"
LD "v198,s[48:51],s80 offen offset:2048 lds\n"
MF "v[52:55],a[96:99],a[244:247],v[252:255]\n"
"s_nop 0\n"
TR "v[176:177],v2 offset:24576\n"
"ds_write_addtid_b32 v122 offset:27136\n"
MF "v[52:55],a[56:59],a[240:243],v[52:55]\n"
"s_nop 0\n"
TR "v[178:179],v2 offset:26624\n"
"ds_write_addtid_b32 v123 offset:27392\n"
MF "v[52:55],a[16:19],v[188:191],v[52:55]\n"
"s_nop 0\n"
"ds_write_addtid_b32 v124 offset:27648\n"
"ds_write_addtid_b32 v125 offset:27904\n"
MF "v[44:47],a[12:15],v[188:191],v[252:255]\n"
"s_nop 0\n"
TR "v[180:181],v2 offset:28672\n"
"ds_write_addtid_b32 v126 offset:28160\n"
MF "v[44:47],a[52:55],a[240:243],v[44:47]\n"
"s_nop 0\n"
TR "v[182:183],v2 offset:30720\n"
"ds_write_addtid_b32 v127 offset:28416\n"
MF "v[44:47],a[92:95],a[244:247],v[44:47]\n"
LD "v199,s[48:51],s80 offen offset:3072 lds\n"
MF "v[36:39],a[88:91],a[244:247],v[252:255]\n"
"s_nop 0\n"
TR "v[184:185],v2 offset:32768\n"
"ds_write_addtid_b32 v128 offset:28672\n"
MF "v[36:39],a[48:51],a[240:243],v[36:39]\n"
"s_add_u32 m0,s79,106496\n"
TR "v[186:187],v2 offset:34816\n"
"ds_write_addtid_b32 v129 offset:24832\n"
MF "v[36:39],a[8:11],v[188:191],v[36:39]\n"
LD "v200,s[48:51],s80 offen lds\n"
MF "v[28:31],a[4:7],v[188:191],v[252:255]\n"
"s_nop 0\n"
"ds_write_addtid_b32 v130 offset:25088\n"
"ds_write_addtid_b32 v131 offset:25344\n"
MF "v[28:31],a[44:47],a[240:243],v[28:31]\n"
"s_nop 0\n"
"ds_write_addtid_b32 v132 offset:25600\n"
"ds_write_addtid_b32 v133 offset:25856\n"
MF "v[28:31],a[84:87],a[244:247],v[28:31]\n"
"s_nop 0\n"
"ds_write_addtid_b32 v134 offset:26112\n"
"ds_write_addtid_b32 v135 offset:26368\n"
MF "v[20:23],a[80:83],a[244:247],v[252:255]\n"
MF "v[20:23],a[40:43],a[240:243],v[20:23]\n"
LD "v201,s[48:51],s80 offen offset:1024 lds\n"
MF "v[20:23],a[0:3],v[188:191],v[20:23]\n"
"s_waitcnt lgkmcnt(7)\n"
MF "v[56:59],a[0:3],v[176:179],v[252:255]\n"
TR "v[188:189],v3 offset:24576\n"
MF "v[56:59],a[40:43],v[180:183],v[56:59]\n"
TR "v[190:191],v3 offset:26624\n"
MF "v[56:59],a[80:83],v[184:187],v[56:59]\n"
LD "v202,s[48:51],s80 offen offset:2048 lds\n"
MF "v[64:67],a[84:87],v[184:187],v[252:255]\n"
TR "a[240:241],v3 offset:28672\n"
MF "v[64:67],a[44:47],v[180:183],v[64:67]\n"
TR "a[242:243],v3 offset:30720\n"
MF "v[64:67],a[4:7],v[176:179],v[64:67]\n"
LD "v203,s[48:51],s80 offen offset:3072 lds\n"
MF "v[72:75],a[8:11],v[176:179],v[252:255]\n"
TR "a[244:245],v3 offset:32768\n"
MF "v[72:75],a[48:51],v[180:183],v[72:75]\n"
TR "a[246:247],v3 offset:34816\n"
MF "v[72:75],a[88:91],v[184:187],v[72:75]\n"
MF "v[80:83],a[92:95],v[184:187],v[252:255]\n"
MF "v[80:83],a[52:55],v[180:183],v[80:83]\n"
MF "v[80:83],a[12:15],v[176:179],v[80:83]\n"
MF "v[88:91],a[16:19],v[176:179],v[252:255]\n"
MF "v[88:91],a[56:59],v[180:183],v[88:91]\n"
"s_waitcnt vmcnt(31) lgkmcnt(0)\n"
MF "v[88:91],a[96:99],v[184:187],v[88:91]\n"
"s_barrier\n"
MF "v[92:95],a[96:99],a[244:247],v[252:255]\n"
"s_add_u32 s89,s89,s65\n"
TR "v[176:177],v0\n"
MF "v[92:95],a[56:59],a[240:243],v[92:95]\n"
"s_add_u32 m0,s79,24576\n"
TR "v[178:179],v0 offset:2048\n"
MF "v[92:95],a[16:19],v[188:191],v[92:95]\n"
LD "v192,s[48:51],s89 offen lds\n"
MF "v[84:87],a[12:15],v[188:191],v[252:255]\n"
TR "v[180:181],v0 offset:4096\n"
MF "v[84:87],a[52:55],a[240:243],v[84:87]\n"
TR "v[182:183],v0 offset:6144\n"
MF "v[84:87],a[92:95],a[244:247],v[84:87]\n"
LD "v193,s[48:51],s89 offen offset:1024 lds\n"
MF "v[76:79],a[88:91],a[244:247],v[252:255]\n"
TR "v[184:185],v0 offset:8192\n"
MF "v[76:79],a[48:51],a[240:243],v[76:79]\n"
".Lpg29h:\n"
"s_cbranch_execz .Lpg32h\n"
"s_nop 0\n"
TR "v[186:187],v0 offset:10240\n"
"ds_write_addtid_b32 v136 offset:49152\n"
MF "v[76:79],a[8:11],v[188:191],v[76:79]\n"
"s_nop 0\n"
"ds_write_addtid_b32 v137 offset:49408\n"
"ds_write_addtid_b32 v138 offset:49664\n"
MF "v[68:71],a[4:7],v[188:191],v[252:255]\n"
LD "v194,s[48:51],s89 offen offset:2048 lds\n"
MF "v[68:71],a[44:47],a[240:243],v[68:71]\n"
"buffer_load_dword v10,v12,s[40:43],s86 offen sc1\n"
MF "v[68:71],a[84:87],a[244:247],v[68:71]\n"
"s_nop 0\n"
"ds_write_addtid_b32 v139 offset:49920\n"
"ds_write_addtid_b32 v140 offset:50176\n"
MF "v[60:63],a[80:83],a[244:247],v[252:255]\n"
"s_nop 0\n"
"ds_write_addtid_b32 v141 offset:50432\n"
"ds_write_addtid_b32 v142 offset:50688\n"
MF "v[60:63],a[40:43],a[240:243],v[60:63]\n"
"s_nop 0\n"
"ds_write_addtid_b32 v143 offset:50944\n"
"ds_write_addtid_b32 v144 offset:51200\n"
MF "v[60:63],a[0:3],v[188:191],v[60:63]\n"
"s_waitcnt lgkmcnt(9)\n"
LD "v195,s[48:51],s89 offen offset:3072 lds\n"
MF "v[16:19],a[120:123],v[176:179],v[16:19]\n"
"s_nop 0\n"
TR "v[188:189],v1\n"
"ds_write_addtid_b32 v145 offset:51456\n"
MF "v[16:19],a[160:163],v[180:183],v[16:19]\n"
"s_nop 0\n"
TR "v[190:191],v1 offset:2048\n"
"ds_write_addtid_b32 v146 offset:51712\n"
MF "v[16:19],a[200:203],v[184:187],v[16:19]\n"
"s_nop 0\n"
"ds_write_addtid_b32 v147 offset:51968\n"
"ds_write_addtid_b32 v148 offset:52224\n"
MF "v[24:27],a[204:207],v[184:187],v[24:27]\n"
"s_nop 0\n"
TR "a[240:241],v1 offset:4096\n"
"ds_write_addtid_b32 v149 offset:52480\n"
MF "v[24:27],a[164:167],v[180:183],v[24:27]\n"
"s_add_u32 m0,s79,28672\n"
TR "a[242:243],v1 offset:6144\n"
"ds_write_addtid_b32 v150 offset:48640\n"
MF "v[24:27],a[124:127],v[176:179],v[24:27]\n"
LD "v196,s[48:51],s89 offen lds\n"
MF "v[32:35],a[128:131],v[176:179],v[32:35]\n"
"s_nop 0\n"
TR "a[244:245],v1 offset:8192\n"
"ds_write_addtid_b32 v151 offset:48896\n"
MF "v[32:35],a[168:171],v[180:183],v[32:35]\n"
"s_nop 0\n"
TR "a[246:247],v1 offset:10240\n"
"ds_write_addtid_b32 v152 offset:49152\n"
MF "v[32:35],a[208:211],v[184:187],v[32:35]\n"
LD "v197,s[48:51],s89 offen offset:1024 lds\n"
MF "v[40:43],a[212:215],v[184:187],v[40:43]\n"
"s_nop 0\n"
"ds_write_addtid_b32 v153 offset:49408\n"
"ds_write_addtid_b32 v154 offset:49664\n"
MF "v[40:43],a[172:175],v[180:183],v[40:43]\n"
"s_nop 0\n"
"ds_write_addtid_b32 v155 offset:49920\n"
"ds_write_addtid_b32 v156 offset:50176\n"
MF "v[40:43],a[132:135],v[176:179],v[40:43]\n"
"s_nop 0\n"
"ds_write_addtid_b32 v157 offset:50432\n"
"ds_write_addtid_b32 v158 offset:50688\n"
MF "v[48:51],a[136:139],v[176:179],v[48:51]\n"
"s_nop 0\n"
"ds_write_addtid_b32 v159 offset:50944\n"
"ds_write_addtid_b32 v160 offset:51200\n"
MF "v[48:51],a[176:179],v[180:183],v[48:51]\n"
LD "v198,s[48:51],s89 offen offset:2048 lds\n"
MF "v[48:51],a[216:219],v[184:187],v[48:51]\n"
"s_waitcnt lgkmcnt(9)\n"
"ds_write_addtid_b32 v161 offset:51456\n"
"ds_write_addtid_b32 v162 offset:51712\n"
MF "v[52:55],a[216:219],a[244:247],v[52:55]\n"
"s_nop 0\n"
TR "v[176:177],v2\n"
"ds_write_addtid_b32 v163 offset:51968\n"
MF "v[52:55],a[176:179],a[240:243],v[52:55]\n"
"s_nop 0\n"
TR "v[178:179],v2 offset:2048\n"
"ds_write_addtid_b32 v164 offset:52224\n"
MF "v[52:55],a[136:139],v[188:191],v[52:55]\n"
LD "v199,s[48:51],s89 offen offset:3072 lds\n"
MF "v[44:47],a[132:135],v[188:191],v[44:47]\n"
"s_nop 0\n"
TR "v[180:181],v2 offset:4096\n"
"ds_write_addtid_b32 v165 offset:52480\n"
MF "v[44:47],a[172:175],a[240:243],v[44:47]\n"
"s_nop 0\n"
TR "v[182:183],v2 offset:6144\n"
"ds_write_addtid_b32 v166 offset:52736\n"
MF "v[44:47],a[212:215],a[244:247],v[44:47]\n"
"s_nop 0\n"
"ds_write_addtid_b32 v167 offset:52992\n"
"ds_write_addtid_b32 v168 offset:53248\n"
MF "v[36:39],a[208:211],a[244:247],v[36:39]\n"
"s_nop 0\n"
TR "v[184:185],v2 offset:8192\n"
"ds_write_addtid_b32 v169 offset:53504\n"
MF "v[36:39],a[168:171],a[240:243],v[36:39]\n"
"s_add_u32 m0,s79,32768\n"
TR "v[186:187],v2 offset:10240\n"
"ds_write_addtid_b32 v170 offset:49664\n"
MF "v[36:39],a[128:131],v[188:191],v[36:39]\n"
LD "v200,s[48:51],s89 offen lds\n"
MF "v[28:31],a[124:127],v[188:191],v[28:31]\n"
"s_nop 0\n"
"ds_write_addtid_b32 v171 offset:49920\n"
"ds_write_addtid_b32 v172 offset:50176\n"
MF "v[28:31],a[164:167],a[240:243],v[28:31]\n"
"s_nop 0\n"
"ds_write_addtid_b32 v173 offset:50432\n"
"ds_write_addtid_b32 v174 offset:50688\n"
MF "v[28:31],a[204:207],a[244:247],v[28:31]\n"
"ds_write_addtid_b32 v175 offset:50944\n"
MF "v[20:23],a[200:203],a[244:247],v[20:23]\n"
LD "v201,s[48:51],s89 offen offset:1024 lds\n"
MF "v[20:23],a[160:163],a[240:243],v[20:23]\n"
MF "v[20:23],a[120:123],v[188:191],v[20:23]\n"
"s_waitcnt lgkmcnt(6)\n"
MF "v[56:59],a[120:123],v[176:179],v[56:59]\n"
TR "v[188:189],v3\n"
MF "v[56:59],a[160:163],v[180:183],v[56:59]\n"
TR "v[190:191],v3 offset:2048\n"
MF "v[56:59],a[200:203],v[184:187],v[56:59]\n"
LD "v202,s[48:51],s89 offen offset:2048 lds\n"
MF "v[64:67],a[204:207],v[184:187],v[64:67]\n"
TR "a[240:241],v3 offset:4096\n"
MF "v[64:67],a[164:167],v[180:183],v[64:67]\n"
TR "a[242:243],v3 offset:6144\n"
MF "v[64:67],a[124:127],v[176:179],v[64:67]\n"
LD "v203,s[48:51],s89 offen offset:3072 lds\n"
MF "v[72:75],a[128:131],v[176:179],v[72:75]\n"
TR "a[244:245],v3 offset:8192\n"
MF "v[72:75],a[168:171],v[180:183],v[72:75]\n"
TR "a[246:247],v3 offset:10240\n"
MF "v[72:75],a[208:211],v[184:187],v[72:75]\n"
MF "v[80:83],a[212:215],v[184:187],v[80:83]\n"
MF "v[80:83],a[172:175],v[180:183],v[80:83]\n"
MF "v[80:83],a[132:135],v[176:179],v[80:83]\n"
MF "v[88:91],a[136:139],v[176:179],v[88:91]\n"
MF "v[88:91],a[176:179],v[180:183],v[88:91]\n"
"s_waitcnt vmcnt(27) lgkmcnt(0)\n"
MF "v[88:91],a[216:219],v[184:187],v[88:91]\n"
"s_barrier\n"
MF "v[92:95],a[216:219],a[244:247],v[92:95]\n"
"s_add_u32 s80,s89,122880\n"
TR "v[176:177],v0 offset:49152\n"
MF "v[92:95],a[176:179],a[240:243],v[92:95]\n"
"s_add_u32 m0,s79,0\n"
"s_mov_b32 s82,s70\n"
TR "v[178:179],v0 offset:51200\n"
MF "v[92:95],a[136:139],v[188:191],v[92:95]\n"
LD "v192,s[48:51],s80 offen lds\n"
MF "v[84:87],a[132:135],v[188:191],v[84:87]\n"
TR "v[180:181],v0 offset:53248\n"
MF "v[84:87],a[172:175],a[240:243],v[84:87]\n"
TR "v[182:183],v0 offset:55296\n"
MF "v[84:87],a[212:215],a[244:247],v[84:87]\n"
LD "v193,s[48:51],s80 offen offset:1024 lds\n"
MF "v[76:79],a[208:211],a[244:247],v[76:79]\n"
TR "v[184:185],v0 offset:57344\n"
MF "v[76:79],a[168:171],a[240:243],v[76:79]\n"
TR "v[186:187],v0 offset:59392\n"
MF "v[76:79],a[128:131],v[188:191],v[76:79]\n"
MF "v[68:71],a[124:127],v[188:191],v[68:71]\n"
MF "v[68:71],a[164:167],a[240:243],v[68:71]\n"
LD "v194,s[48:51],s80 offen offset:2048 lds\n"
MF "v[68:71],a[204:207],a[244:247],v[68:71]\n"
MF "v[60:63],a[200:203],a[244:247],v[60:63]\n"
MF "v[60:63],a[160:163],a[240:243],v[60:63]\n"
MF "v[60:63],a[120:123],v[188:191],v[60:63]\n"
"s_waitcnt lgkmcnt(0)\n"
LD "v195,s[48:51],s80 offen offset:3072 lds\n"
MF "v[16:19],a[20:23],v[176:179],v[16:19]\n"
TR "v[188:189],v1 offset:49152\n"
MF "v[16:19],a[60:63],v[180:183],v[16:19]\n"
TR "v[190:191],v1 offset:51200\n"
MF "v[16:19],a[100:103],v[184:187],v[16:19]\n"
MF "v[24:27],a[104:107],v[184:187],v[24:27]\n"
TR "a[240:241],v1 offset:53248\n"
MF "v[24:27],a[64:67],v[180:183],v[24:27]\n"
"s_add_u32 m0,s79,4096\n"
TR "a[242:243],v1 offset:55296\n"
MF "v[24:27],a[24:27],v[176:179],v[24:27]\n"
LD "v196,s[48:51],s80 offen lds\n"
MF "v[32:35],a[28:31],v[176:179],v[32:35]\n"
TR "a[244:245],v1 offset:57344\n"
MF "v[32:35],a[68:71],v[180:183],v[32:35]\n"
TR "a[246:247],v1 offset:59392\n"
MF "v[32:35],a[108:111],v[184:187],v[32:35]\n"
LD "v197,s[48:51],s80 offen offset:1024 lds\n"
MF "v[40:43],a[112:115],v[184:187],v[40:43]\n"
MF "v[40:43],a[72:75],v[180:183],v[40:43]\n"
MF "v[40:43],a[32:35],v[176:179],v[40:43]\n"
MF "v[48:51],a[36:39],v[176:179],v[48:51]\n"
MF "v[48:51],a[76:79],v[180:183],v[48:51]\n"
LD "v198,s[48:51],s80 offen offset:2048 lds\n"
MF "v[48:51],a[116:119],v[184:187],v[48:51]\n"
"s_waitcnt lgkmcnt(0)\n"
MF "v[52:55],a[116:119],a[244:247],v[52:55]\n"
TR "v[176:177],v2 offset:49152\n"
MF "v[52:55],a[76:79],a[240:243],v[52:55]\n"
TR "v[178:179],v2 offset:51200\n"
MF "v[52:55],a[36:39],v[188:191],v[52:55]\n"
LD "v199,s[48:51],s80 offen offset:3072 lds\n"
MF "v[44:47],a[32:35],v[188:191],v[44:47]\n"
TR "v[180:181],v2 offset:53248\n"
MF "v[44:47],a[72:75],a[240:243],v[44:47]\n"
TR "v[182:183],v2 offset:55296\n"
MF "v[44:47],a[112:115],a[244:247],v[44:47]\n"
MF "v[36:39],a[108:111],a[244:247],v[36:39]\n"
TR "v[184:185],v2 offset:57344\n"
MF "v[36:39],a[68:71],a[240:243],v[36:39]\n"
"s_add_u32 m0,s79,8192\n"
TR "v[186:187],v2 offset:59392\n"
MF "v[36:39],a[28:31],v[188:191],v[36:39]\n"
LD "v200,s[48:51],s80 offen lds\n"
MF "v[28:31],a[24:27],v[188:191],v[28:31]\n"
MF "v[28:31],a[64:67],a[240:243],v[28:31]\n"
MF "v[28:31],a[104:107],a[244:247],v[28:31]\n"
MF "v[20:23],a[100:103],a[244:247],v[20:23]\n"
LD "v201,s[48:51],s80 offen offset:1024 lds\n"
MF "v[20:23],a[60:63],a[240:243],v[20:23]\n"
"s_waitcnt vmcnt(19)\n"
MF "v[20:23],a[20:23],v[188:191],v[20:23]\n"
"v_mov_b32 v236,v228\n"
"v_mov_b32 v237,v229\n"
MF "v[16:19],a[252:255],v[208:211],v[16:19]\n"
MF "v[24:27],a[252:255],v[212:215],v[24:27]\n"
MF "v[32:35],a[252:255],v[216:219],v[32:35]\n"
MF "v[40:43],a[252:255],v[220:223],v[40:43]\n"
MF "v[48:51],a[252:255],v[224:227],v[48:51]\n"
MF "v[20:23],a[248:251],v[208:211],v[20:23]\n"
MF "v[28:31],a[248:251],v[212:215],v[28:31]\n"
MF "v[36:39],a[248:251],v[216:219],v[36:39]\n"
MF "v[44:47],a[248:251],v[220:223],v[44:47]\n"
MF "v[52:55],a[248:251],v[224:227],v[52:55]\n"
MF "v[248:251],v[230:233],a[250:253],v[248:251]\n"
MF "v[244:247],v[234:237],a[250:253],v[244:247]\n"
"s_waitcnt lgkmcnt(0)\n"
MF "v[56:59],a[20:23],v[176:179],v[56:59]\n"
TR "v[188:189],v3 offset:49152\n"
"v_sub_u32 v10,v10,v11\n"
MF "v[56:59],a[60:63],v[180:183],v[56:59]\n"
TR "v[190:191],v3 offset:51200\n"
MF "v[56:59],a[100:103],v[184:187],v[56:59]\n"
LD "v202,s[48:51],s80 offen offset:2048 lds\n"
MF "v[64:67],a[104:107],v[184:187],v[64:67]\n"
TR "a[240:241],v3 offset:53248\n"
MF "v[64:67],a[64:67],v[180:183],v[64:67]\n"
TR "a[242:243],v3 offset:55296\n"
MF "v[64:67],a[24:27],v[176:179],v[64:67]\n"
LD "v203,s[48:51],s80 offen offset:3072 lds\n"
MF "v[72:75],a[28:31],v[176:179],v[72:75]\n"
TR "a[244:245],v3 offset:57344\n"
MF "v[72:75],a[68:71],v[180:183],v[72:75]\n"
TR "a[246:247],v3 offset:59392\n"
MF "v[72:75],a[108:111],v[184:187],v[72:75]\n"
".Lnf2:\n"
"v_readlane_b32 s58,v10,s67\n"
"s_cmp_lt_u32 s58,s85\n"
"s_cbranch_scc1 .Lsp2\n"
".Lnb2:\n"
MF "v[80:83],a[112:115],v[184:187],v[80:83]\n"
"s_add_u32 s70,s70,20480\n"
MF "v[80:83],a[72:75],v[180:183],v[80:83]\n"
MF "v[80:83],a[32:35],v[176:179],v[80:83]\n"
MF "v[88:91],a[36:39],v[176:179],v[88:91]\n"
"s_add_u32 s2,s2,204800\n"
MF "v[88:91],a[76:79],v[180:183],v[88:91]\n"
"s_waitcnt vmcnt(25) lgkmcnt(0)\n"
LD "v[228:231],v9,s[40:43],s2 offen sc1\n"
MF "v[88:91],a[116:119],v[184:187],v[88:91]\n"
"s_barrier\n"
MF "v[92:95],a[116:119],a[244:247],v[92:95]\n"
"s_add_u32 s88,s88,s65\n"
TR "v[176:177],v4\n"
MF "v[92:95],a[76:79],a[240:243],v[92:95]\n"
"s_add_u32 m0,s79,49152\n"
TR "v[178:179],v4 offset:2048\n"
MF "v[92:95],a[36:39],v[188:191],v[92:95]\n"
LD "v192,s[48:51],s88 offen lds\n"
MF "v[84:87],a[32:35],v[188:191],v[84:87]\n"
TR "v[180:181],v4 offset:4096\n"
MF "v[84:87],a[72:75],a[240:243],v[84:87]\n"
TR "v[182:183],v4 offset:6144\n"
MF "v[84:87],a[112:115],a[244:247],v[84:87]\n"
LD "v193,s[48:51],s88 offen offset:1024 lds\n"
MF "v[76:79],a[108:111],a[244:247],v[76:79]\n"
TR "v[184:185],v4 offset:8192\n"
MF "v[76:79],a[68:71],a[240:243],v[76:79]\n"
"s_add_u32 s2,s2,204800\n"
TR "v[186:187],v4 offset:10240\n"
MF "v[76:79],a[28:31],v[188:191],v[76:79]\n"
LD "v[232:235],v9,s[40:43],s2 offen sc1\n"
MF "v[68:71],a[24:27],v[188:191],v[68:71]\n"
MF "v[68:71],a[64:67],a[240:243],v[68:71]\n"
LD "v194,s[48:51],s88 offen offset:2048 lds\n"
MF "v[68:71],a[104:107],a[244:247],v[68:71]\n"
MF "v[60:63],a[100:103],a[244:247],v[60:63]\n"
MF "v[60:63],a[60:63],a[240:243],v[60:63]\n"
MF "v[60:63],a[20:23],v[188:191],v[60:63]\n"
"s_waitcnt lgkmcnt(0)\n"
LD "v195,s[48:51],s88 offen offset:3072 lds\n"
MF "v[16:19],a[140:143],v[176:179],v[16:19]\n"
TR "v[188:189],v5\n"
MF "v[16:19],a[180:183],v[180:183],v[16:19]\n"
TR "v[190:191],v5 offset:2048\n"
MF "v[16:19],a[220:223],v[184:187],v[16:19]\n"
LD "v[208:211],v9,s[40:43],s82 offen sc1\n"
MF "v[24:27],a[224:227],v[184:187],v[24:27]\n"
TR "a[240:241],v5 offset:4096\n"
MF "v[24:27],a[184:187],v[180:183],v[24:27]\n"
"s_add_u32 m0,s79,53248\n"
TR "a[242:243],v5 offset:6144\n"
MF "v[24:27],a[144:147],v[176:179],v[24:27]\n"
LD "v196,s[48:51],s88 offen lds\n"
MF "v[32:35],a[148:151],v[176:179],v[32:35]\n"
TR "a[244:245],v5 offset:8192\n"
MF "v[32:35],a[188:191],v[180:183],v[32:35]\n"
TR "a[246:247],v5 offset:10240\n"
MF "v[32:35],a[228:231],v[184:187],v[32:35]\n"
LD "v197,s[48:51],s88 offen offset:1024 lds\n"
MF "v[40:43],a[232:235],v[184:187],v[40:43]\n"
MF "v[40:43],a[192:195],v[180:183],v[40:43]\n"
MF "v[40:43],a[152:155],v[176:179],v[40:43]\n"
MF "v[48:51],a[156:159],v[176:179],v[48:51]\n"
LD "v[212:215],v9,s[40:43],s82 offen offset:1024 sc1\n"
MF "v[48:51],a[196:199],v[180:183],v[48:51]\n"
LD "v198,s[48:51],s88 offen offset:2048 lds\n"
MF "v[48:51],a[236:239],v[184:187],v[48:51]\n"
"s_waitcnt lgkmcnt(0)\n"
MF "v[52:55],a[236:239],a[244:247],v[52:55]\n"
TR "v[176:177],v6\n"
MF "v[52:55],a[196:199],a[240:243],v[52:55]\n"
TR "v[178:179],v6 offset:2048\n"
MF "v[52:55],a[156:159],v[188:191],v[52:55]\n"
LD "v199,s[48:51],s88 offen offset:3072 lds\n"
MF "v[44:47],a[152:155],v[188:191],v[44:47]\n"
TR "v[180:181],v6 offset:4096\n"
MF "v[44:47],a[192:195],a[240:243],v[44:47]\n"
TR "v[182:183],v6 offset:6144\n"
MF "v[44:47],a[232:235],a[244:247],v[44:47]\n"
MF "v[36:39],a[228:231],a[244:247],v[36:39]\n"
TR "v[184:185],v6 offset:8192\n"
MF "v[36:39],a[188:191],a[240:243],v[36:39]\n"
"s_add_u32 m0,s79,57344\n"
TR "v[186:187],v6 offset:10240\n"
MF "v[36:39],a[148:151],v[188:191],v[36:39]\n"
LD "v200,s[48:51],s88 offen lds\n"
MF "v[28:31],a[144:147],v[188:191],v[28:31]\n"
LD "v[216:219],v9,s[40:43],s82 offen offset:2048 sc1\n"
MF "v[28:31],a[184:187],a[240:243],v[28:31]\n"
MF "v[28:31],a[224:227],a[244:247],v[28:31]\n"
MF "v[20:23],a[220:223],a[244:247],v[20:23]\n"
LD "v201,s[48:51],s88 offen offset:1024 lds\n"
MF "v[20:23],a[180:183],a[240:243],v[20:23]\n"
MF "v[20:23],a[140:143],v[188:191],v[20:23]\n"
"s_waitcnt lgkmcnt(0)\n"
MF "v[56:59],a[140:143],v[176:179],v[56:59]\n"
TR "v[188:189],v7\n"
MF "v[56:59],a[180:183],v[180:183],v[56:59]\n"
TR "v[190:191],v7 offset:2048\n"
MF "v[56:59],a[220:223],v[184:187],v[56:59]\n"
LD "v202,s[48:51],s88 offen offset:2048 lds\n"
MF "v[64:67],a[224:227],v[184:187],v[64:67]\n"
TR "a[240:241],v7 offset:4096\n"
MF "v[64:67],a[184:187],v[180:183],v[64:67]\n"
TR "a[242:243],v7 offset:6144\n"
MF "v[64:67],a[144:147],v[176:179],v[64:67]\n"
LD "v203,s[48:51],s88 offen offset:3072 lds\n"
MF "v[72:75],a[148:151],v[176:179],v[72:75]\n"
TR "a[244:245],v7 offset:8192\n"
MF "v[72:75],a[188:191],v[180:183],v[72:75]\n"
TR "a[246:247],v7 offset:10240\n"
MF "v[72:75],a[228:231],v[184:187],v[72:75]\n"
LD "v[220:223],v9,s[40:43],s82 offen offset:3072 sc1\n"
MF "v[80:83],a[232:235],v[184:187],v[80:83]\n"
MF "v[80:83],a[192:195],v[180:183],v[80:83]\n"
MF "v[80:83],a[152:155],v[176:179],v[80:83]\n"
MF "v[88:91],a[156:159],v[176:179],v[88:91]\n"
"s_add_u32 s82,s82,4096\n"
MF "v[88:91],a[196:199],v[180:183],v[88:91]\n"
"s_waitcnt vmcnt(30) lgkmcnt(0)\n"
LD "v[224:227],v9,s[40:43],s82 offen sc1\n"
MF "v[88:91],a[236:239],v[184:187],v[88:91]\n"
"s_barrier\n"
MF "v[92:95],a[236:239],a[244:247],v[92:95]\n"
"s_add_u32 s80,s88,122880\n"
TR "v[176:177],v0 offset:24576\n"
MF "v[92:95],a[196:199],a[240:243],v[92:95]\n"
"s_add_u32 m0,s79,98304\n"
"s_add_u32 s85,s85,1\n"
TR "v[178:179],v0 offset:26624\n"
MF "v[92:95],a[156:159],v[188:191],v[92:95]\n"
LD "v192,s[48:51],s80 offen lds\n"
MF "v[84:87],a[152:155],v[188:191],v[84:87]\n"
"s_nop 0\n"
TR "v[180:181],v0 offset:28672\n"
"ds_write_addtid_b32 v16 offset:24576\n"
MF "v[84:87],a[192:195],a[240:243],v[84:87]\n"
"s_nop 0\n"
TR "v[182:183],v0 offset:30720\n"
"ds_write_addtid_b32 v17 offset:24832\n"
MF "v[84:87],a[232:235],a[244:247],v[84:87]\n"
LD "v193,s[48:51],s80 offen offset:1024 lds\n"
MF "v[76:79],a[228:231],a[244:247],v[76:79]\n"
"s_nop 0\n"
TR "v[184:185],v0 offset:32768\n"
"ds_write_addtid_b32 v18 offset:25088\n"
MF "v[76:79],a[188:191],a[240:243],v[76:79]\n"
"s_nop 0\n"
TR "v[186:187],v0 offset:34816\n"
"ds_write_addtid_b32 v19 offset:25344\n"
MF "v[76:79],a[148:151],v[188:191],v[76:79]\n"
"s_nop 0\n"
"ds_write_addtid_b32 v20 offset:25600\n"
"ds_write_addtid_b32 v21 offset:25856\n"
MF "v[68:71],a[144:147],v[188:191],v[68:71]\n"
LD "v194,s[48:51],s80 offen offset:2048 lds\n"
MF "v[68:71],a[184:187],a[240:243],v[68:71]\n"
"s_nop 0\n"
"ds_write_addtid_b32 v22 offset:26112\n"
"ds_write_addtid_b32 v23 offset:26368\n"
MF "v[68:71],a[224:227],a[244:247],v[68:71]\n"
"s_nop 0\n"
"ds_write_addtid_b32 v24 offset:26624\n"
"ds_write_addtid_b32 v25 offset:26880\n"
MF "v[60:63],a[220:223],a[244:247],v[60:63]\n"
"s_nop 0\n"
"ds_write_addtid_b32 v26 offset:27136\n"
"ds_write_addtid_b32 v27 offset:27392\n"
MF "v[60:63],a[180:183],a[240:243],v[60:63]\n"
MF "v[60:63],a[140:143],v[188:191],v[60:63]\n"
"s_waitcnt lgkmcnt(9)\n"
"s_cmp_lg_u32 s85,9\n"
"s_cbranch_scc1 .Lpl09\n"
".Lplast0:\n"
MF "v[96:99],v[176:179],a[0:3],v[252:255]\n"
"s_nop 0\n"
TR "v[188:189],v1 offset:24576\n"
TR "v[190:191],v1 offset:26624\n"
MF "v[96:99],v[180:183],a[40:43],v[96:99]\n"
"s_nop 0\n"
TR "a[240:241],v1 offset:28672\n"
TR "a[242:243],v1 offset:30720\n"
MF "v[96:99],v[184:187],a[80:83],v[96:99]\n"
"s_add_u32 s90,s91,163840\n"
TR "a[244:245],v1 offset:32768\n"
TR "a[246:247],v1 offset:34816\n"
MF "v[104:107],v[184:187],a[84:87],v[252:255]\n"
LD "v195,s[48:51],s80 offen offset:3072 lds\n"
MF "v[104:107],v[180:183],a[44:47],v[104:107]\n"
"s_nop 0\n"
"ds_write_addtid_b32 v28 offset:27648\n"
"ds_write_addtid_b32 v29 offset:27904\n"
MF "v[104:107],v[176:179],a[4:7],v[104:107]\n"
"s_nop 0\n"
"ds_write_addtid_b32 v30 offset:28160\n"
"ds_write_addtid_b32 v31 offset:28416\n"
MF "v[112:115],v[176:179],a[8:11],v[252:255]\n"
"s_nop 0\n"
"ds_write_addtid_b32 v32 offset:28672\n"
"ds_write_addtid_b32 v33 offset:28928\n"
MF "v[112:115],v[180:183],a[48:51],v[112:115]\n"
"s_add_u32 m0,s79,102400\n"
"s_nop 0\n"
"ds_write_addtid_b32 v34 offset:25088\n"
"ds_write_addtid_b32 v35 offset:25344\n"
MF "v[112:115],v[184:187],a[88:91],v[112:115]\n"
LD "v196,s[48:51],s80 offen lds\n"
MF "v[120:123],v[184:187],a[92:95],v[252:255]\n"
"s_nop 0\n"
"ds_write_addtid_b32 v36 offset:25600\n"
"ds_write_addtid_b32 v37 offset:25856\n"
MF "v[120:123],v[180:183],a[52:55],v[120:123]\n"
"ds_write_addtid_b32 v38 offset:26112\n"
MF "v[120:123],v[176:179],a[12:15],v[120:123]\n"
"ds_write_addtid_b32 v39 offset:26368\n"
MF "v[128:131],v[176:179],a[16:19],v[252:255]\n"
"ds_write_addtid_b32 v40 offset:26624\n"
MF "v[128:131],v[180:183],a[56:59],v[128:131]\n"
"ds_write_addtid_b32 v41 offset:26880\n"
MF "v[128:131],v[184:187],a[96:99],v[128:131]\n"
"s_waitcnt lgkmcnt(14)\n"
LD "v197,s[48:51],s80 offen offset:1024 lds\n"
MF "v[132:135],a[244:247],a[96:99],v[252:255]\n"
"s_nop 0\n"
TR "v[176:177],v2 offset:24576\n"
TR "v[178:179],v2 offset:26624\n"
MF "v[132:135],a[240:243],a[56:59],v[132:135]\n"
"s_nop 0\n"
TR "v[180:181],v2 offset:28672\n"
TR "v[182:183],v2 offset:30720\n"
MF "v[132:135],v[188:191],a[16:19],v[132:135]\n"
"s_nop 0\n"
TR "v[184:185],v2 offset:32768\n"
TR "v[186:187],v2 offset:34816\n"
MF "v[124:127],v[188:191],a[12:15],v[252:255]\n"
"s_nop 0\n"
"ds_write_addtid_b32 v42 offset:27136\n"
"ds_write_addtid_b32 v43 offset:27392\n"
MF "v[124:127],a[240:243],a[52:55],v[124:127]\n"
"s_nop 0\n"
"ds_write_addtid_b32 v44 offset:27648\n"
"ds_write_addtid_b32 v45 offset:27904\n"
MF "v[124:127],a[244:247],a[92:95],v[124:127]\n"
"s_nop 0\n"
"ds_write_addtid_b32 v46 offset:28160\n"
"ds_write_addtid_b32 v47 offset:28416\n"
MF "v[116:119],a[244:247],a[88:91],v[252:255]\n"
LD "v198,s[48:51],s80 offen offset:2048 lds\n"
MF "v[116:119],a[240:243],a[48:51],v[116:119]\n"
"s_nop 0\n"
"ds_write_addtid_b32 v48 offset:28672\n"
"ds_write_addtid_b32 v49 offset:28928\n"
MF "v[116:119],v[188:191],a[8:11],v[116:119]\n"
"s_nop 0\n"
"ds_write_addtid_b32 v50 offset:29184\n"
"ds_write_addtid_b32 v51 offset:29440\n"
MF "v[108:111],v[188:191],a[4:7],v[252:255]\n"
"ds_write_addtid_b32 v52 offset:29696\n"
MF "v[108:111],a[240:243],a[44:47],v[108:111]\n"
"ds_write_addtid_b32 v53 offset:29952\n"
MF "v[108:111],a[244:247],a[84:87],v[108:111]\n"
"ds_write_addtid_b32 v54 offset:30208\n"
MF "v[100:103],a[244:247],a[80:83],v[252:255]\n"
"ds_write_addtid_b32 v55 offset:30464\n"
MF "v[100:103],a[240:243],a[40:43],v[100:103]\n"
LD "v199,s[48:51],s80 offen offset:3072 lds\n"
MF "v[100:103],v[188:191],a[0:3],v[100:103]\n"
"s_waitcnt lgkmcnt(14)\n"
MF "v[136:139],v[176:179],a[0:3],v[252:255]\n"
"s_nop 0\n"
TR "v[188:189],v3 offset:24576\n"
TR "v[190:191],v3 offset:26624\n"
MF "v[136:139],v[180:183],a[40:43],v[136:139]\n"
"s_add_u32 m0,s79,106496\n"
TR "a[240:241],v3 offset:28672\n"
TR "a[242:243],v3 offset:30720\n"
MF "v[136:139],v[184:187],a[80:83],v[136:139]\n"
"s_nop 0\n"
TR "a[244:245],v3 offset:32768\n"
TR "a[246:247],v3 offset:34816\n"
"s_waitcnt lgkmcnt(0)\n"
MF "v[144:147],v[184:187],a[84:87],v[252:255]\n"
".Lpg30h:\n"
"s_cbranch_execz .Lpg33h\n"
"s_barrier\n"
MF "v[144:147],v[180:183],a[44:47],v[144:147]\n"
LD "v200,s[48:51],s80 offen lds\n"
MF "v[144:147],v[176:179],a[4:7],v[144:147]\n"
"ds_read2st64_b32 v[16:17],v13 offset0:0 offset1:1\n"
MF "v[152:155],v[176:179],a[8:11],v[252:255]\n"
"ds_read2st64_b32 v[18:19],v13 offset0:2 offset1:3\n"
MF "v[152:155],v[180:183],a[48:51],v[152:155]\n"
"ds_read2st64_b32 v[20:21],v13 offset0:4 offset1:5\n"
MF "v[152:155],v[184:187],a[88:91],v[152:155]\n"
"ds_read2st64_b32 v[22:23],v13 offset0:6 offset1:7\n"
MF "v[160:163],v[184:187],a[92:95],v[252:255]\n"
"ds_read2st64_b32 v[24:25],v13 offset0:8 offset1:9\n"
MF "v[160:163],v[180:183],a[52:55],v[160:163]\n"
"ds_read2st64_b32 v[26:27],v13 offset0:10 offset1:11\n"
MF "v[160:163],v[176:179],a[12:15],v[160:163]\n"
LD "v201,s[48:51],s80 offen offset:1024 lds\n"
MF "v[168:171],v[176:179],a[16:19],v[252:255]\n"
"ds_read2st64_b32 v[28:29],v13 offset0:12 offset1:13\n"
MF "v[168:171],v[180:183],a[56:59],v[168:171]\n"
"ds_read2st64_b32 v[30:31],v13 offset0:14 offset1:15\n"
"s_waitcnt vmcnt(29) lgkmcnt(0)\n"
MF "v[168:171],v[184:187],a[96:99],v[168:171]\n"
"s_barrier\n"
MF "v[172:175],a[244:247],a[96:99],v[252:255]\n"
"s_nop 0\n"
TR "v[176:177],v0\n"
TR "v[178:179],v0 offset:2048\n"
MF "v[172:175],a[240:243],a[56:59],v[172:175]\n"
"s_nop 0\n"
TR "v[180:181],v0 offset:4096\n"
TR "v[182:183],v0 offset:6144\n"
MF "v[172:175],v[188:191],a[16:19],v[172:175]\n"
"s_nop 0\n"
TR "v[184:185],v0 offset:8192\n"
TR "v[186:187],v0 offset:10240\n"
MF "v[164:167],v[188:191],a[12:15],v[252:255]\n"
LD "v202,s[48:51],s80 offen offset:2048 lds\n"
MF "v[164:167],a[240:243],a[52:55],v[164:167]\n"
"ds_read2st64_b32 v[32:33],v13 offset0:16 offset1:17\n"
"v_add_f32 v56,v56,v16\n"
MF "v[164:167],a[244:247],a[92:95],v[164:167]\n"
"ds_read2st64_b32 v[34:35],v13 offset0:18 offset1:19\n"
"v_add_f32 v57,v57,v17\n"
MF "v[156:159],a[244:247],a[88:91],v[252:255]\n"
"ds_read2st64_b32 v[36:37],v13 offset0:20 offset1:21\n"
"v_add_f32 v58,v58,v18\n"
MF "v[156:159],a[240:243],a[48:51],v[156:159]\n"
"ds_read2st64_b32 v[38:39],v13 offset0:22 offset1:23\n"
"v_add_f32 v59,v59,v19\n"
MF "v[156:159],v[188:191],a[8:11],v[156:159]\n"
"ds_read2st64_b32 v[40:41],v13 offset0:24 offset1:25\n"
"v_add_f32 v60,v60,v20\n"
MF "v[148:151],v[188:191],a[4:7],v[252:255]\n"
LD "v203,s[48:51],s80 offen offset:3072 lds\n"
MF "v[148:151],a[240:243],a[44:47],v[148:151]\n"
"ds_read2st64_b32 v[42:43],v13 offset0:26 offset1:27\n"
"v_add_f32 v61,v61,v21\n"
MF "v[148:151],a[244:247],a[84:87],v[148:151]\n"
"ds_read2st64_b32 v[44:45],v13 offset0:28 offset1:29\n"
"v_add_f32 v62,v62,v22\n"
MF "v[140:143],a[244:247],a[80:83],v[252:255]\n"
"ds_read2st64_b32 v[46:47],v13 offset0:30 offset1:31\n"
"v_add_f32 v63,v63,v23\n"
MF "v[140:143],a[240:243],a[40:43],v[140:143]\n"
"ds_read2st64_b32 v[48:49],v13 offset0:32 offset1:33\n"
CV "v56,v56,v57\n"
MF "v[140:143],v[188:191],a[0:3],v[140:143]\n"
"s_waitcnt lgkmcnt(9)\n"
"ds_read2st64_b32 v[50:51],v13 offset0:34 offset1:35\n"
CV "v57,v58,v59\n"
MF "v[96:99],v[176:179],a[120:123],v[96:99]\n"
"s_nop 0\n"
TR "v[188:189],v1\n"
TR "v[190:191],v1 offset:2048\n"
MF "v[96:99],v[180:183],a[160:163],v[96:99]\n"
"s_nop 0\n"
TR "a[240:241],v1 offset:4096\n"
TR "a[242:243],v1 offset:6144\n"
MF "v[96:99],v[184:187],a[200:203],v[96:99]\n"
"s_nop 0\n"
TR "a[244:245],v1 offset:8192\n"
TR "a[246:247],v1 offset:10240\n"
MF "v[104:107],v[184:187],a[204:207],v[104:107]\n"
"ds_read2st64_b32 v[52:53],v13 offset0:36 offset1:37\n"
CV "v58,v60,v61\n"
MF "v[104:107],v[180:183],a[164:167],v[104:107]\n"
"s_add_u32 s87,s90,10240\n"
"ds_read2st64_b32 v[54:55],v13 offset0:38 offset1:39\n"
CV "v59,v62,v63\n"
MF "v[104:107],v[176:179],a[124:127],v[104:107]\n"
"buffer_store_dwordx4 v[56:59],v9,s[40:43],s87 offen sc1\n"
MF "v[112:115],v[176:179],a[128:131],v[112:115]\n"
"v_add_f32 v64,v64,v24\n"
"v_add_f32 v65,v65,v25\n"
MF "v[112:115],v[180:183],a[168:171],v[112:115]\n"
"v_add_f32 v66,v66,v26\n"
"v_add_f32 v67,v67,v27\n"
MF "v[112:115],v[184:187],a[208:211],v[112:115]\n"
"v_add_f32 v68,v68,v28\n"
"v_add_f32 v69,v69,v29\n"
MF "v[120:123],v[184:187],a[212:215],v[120:123]\n"
"v_add_f32 v70,v70,v30\n"
"v_add_f32 v71,v71,v31\n"
MF "v[120:123],v[180:183],a[172:175],v[120:123]\n"
CV "v64,v64,v65\n"
CV "v65,v66,v67\n"
MF "v[120:123],v[176:179],a[132:135],v[120:123]\n"
"s_add_u32 s87,s90,11264\n"
CV "v66,v68,v69\n"
CV "v67,v70,v71\n"
MF "v[128:131],v[176:179],a[136:139],v[128:131]\n"
"s_waitcnt lgkmcnt(14)\n"
"buffer_store_dwordx4 v[64:67],v9,s[40:43],s87 offen sc1\n"
MF "v[128:131],v[180:183],a[176:179],v[128:131]\n"
"v_add_f32 v72,v72,v32\n"
"v_add_f32 v73,v73,v33\n"
MF "v[128:131],v[184:187],a[216:219],v[128:131]\n"
"s_waitcnt lgkmcnt(2)\n"
"v_add_f32 v74,v74,v34\n"
"v_add_f32 v75,v75,v35\n"
MF "v[132:135],a[244:247],a[216:219],v[132:135]\n"
"s_nop 0\n"
TR "v[176:177],v2\n"
TR "v[178:179],v2 offset:2048\n"
MF "v[132:135],a[240:243],a[176:179],v[132:135]\n"
"s_nop 0\n"
TR "v[180:181],v2 offset:4096\n"
TR "v[182:183],v2 offset:6144\n"
MF "v[132:135],v[188:191],a[136:139],v[132:135]\n"
"s_nop 0\n"
TR "v[184:185],v2 offset:8192\n"
TR "v[186:187],v2 offset:10240\n"
MF "v[124:127],v[188:191],a[132:135],v[124:127]\n"
"v_add_f32 v76,v76,v36\n"
"v_add_f32 v77,v77,v37\n"
MF "v[124:127],a[240:243],a[172:175],v[124:127]\n"
"v_add_f32 v78,v78,v38\n"
"v_add_f32 v79,v79,v39\n"
MF "v[124:127],a[244:247],a[212:215],v[124:127]\n"
CV "v72,v72,v73\n"
CV "v73,v74,v75\n"
MF "v[116:119],a[244:247],a[208:211],v[116:119]\n"
"s_add_u32 s87,s90,12288\n"
CV "v74,v76,v77\n"
CV "v75,v78,v79\n"
MF "v[116:119],a[240:243],a[168:171],v[116:119]\n"
"buffer_store_dwordx4 v[72:75],v9,s[40:43],s87 offen sc1\n"
MF "v[116:119],v[188:191],a[128:131],v[116:119]\n"
"v_add_f32 v80,v80,v40\n"
"v_add_f32 v81,v81,v41\n"
MF "v[108:111],v[188:191],a[124:127],v[108:111]\n"
"v_add_f32 v82,v82,v42\n"
"v_add_f32 v83,v83,v43\n"
MF "v[108:111],a[240:243],a[164:167],v[108:111]\n"
"v_add_f32 v84,v84,v44\n"
"v_add_f32 v85,v85,v45\n"
MF "v[108:111],a[244:247],a[204:207],v[108:111]\n"
"v_add_f32 v86,v86,v46\n"
"v_add_f32 v87,v87,v47\n"
MF "v[100:103],a[244:247],a[200:203],v[100:103]\n"
CV "v80,v80,v81\n"
CV "v81,v82,v83\n"
MF "v[100:103],a[240:243],a[160:163],v[100:103]\n"
"s_add_u32 s87,s90,13312\n"
CV "v82,v84,v85\n"
CV "v83,v86,v87\n"
MF "v[100:103],v[188:191],a[120:123],v[100:103]\n"
"s_waitcnt lgkmcnt(0)\n"
"buffer_store_dwordx4 v[80:83],v9,s[40:43],s87 offen sc1\n"
MF "v[136:139],v[176:179],a[120:123],v[136:139]\n"
"s_nop 0\n"
TR "v[188:189],v3\n"
TR "v[190:191],v3 offset:2048\n"
MF "v[136:139],v[180:183],a[160:163],v[136:139]\n"
"s_nop 0\n"
TR "a[240:241],v3 offset:4096\n"
TR "a[242:243],v3 offset:6144\n"
MF "v[136:139],v[184:187],a[200:203],v[136:139]\n"
"s_nop 0\n"
TR "a[244:245],v3 offset:8192\n"
TR "a[246:247],v3 offset:10240\n"
MF "v[144:147],v[184:187],a[204:207],v[144:147]\n"
"v_add_f32 v88,v88,v48\n"
"v_add_f32 v89,v89,v49\n"
MF "v[144:147],v[180:183],a[164:167],v[144:147]\n"
"v_add_f32 v90,v90,v50\n"
"v_add_f32 v91,v91,v51\n"
MF "v[144:147],v[176:179],a[124:127],v[144:147]\n"
"v_add_f32 v92,v92,v52\n"
"v_add_f32 v93,v93,v53\n"
MF "v[152:155],v[176:179],a[128:131],v[152:155]\n"
"v_add_f32 v94,v94,v54\n"
"v_add_f32 v95,v95,v55\n"
MF "v[152:155],v[180:183],a[168:171],v[152:155]\n"
CV "v88,v88,v89\n"
CV "v89,v90,v91\n"
MF "v[152:155],v[184:187],a[208:211],v[152:155]\n"
"s_add_u32 s87,s90,14336\n"
CV "v90,v92,v93\n"
CV "v91,v94,v95\n"
MF "v[160:163],v[184:187],a[212:215],v[160:163]\n"
"buffer_store_dwordx4 v[88:91],v9,s[40:43],s87 offen sc1\n"
MF "v[160:163],v[180:183],a[172:175],v[160:163]\n"
MF "v[160:163],v[176:179],a[132:135],v[160:163]\n"
MF "v[168:171],v[176:179],a[136:139],v[168:171]\n"
MF "v[168:171],v[180:183],a[176:179],v[168:171]\n"
"s_waitcnt vmcnt(19) lgkmcnt(0)\n"
MF "v[168:171],v[184:187],a[216:219],v[168:171]\n"
"s_barrier\n"
MF "v[172:175],a[244:247],a[216:219],v[172:175]\n"
"s_nop 0\n"
TR "v[176:177],v0 offset:49152\n"
TR "v[178:179],v0 offset:51200\n"
MF "v[172:175],a[240:243],a[176:179],v[172:175]\n"
"s_nop 0\n"
TR "v[180:181],v0 offset:53248\n"
TR "v[182:183],v0 offset:55296\n"
MF "v[172:175],v[188:191],a[136:139],v[172:175]\n"
"s_nop 0\n"
TR "v[184:185],v0 offset:57344\n"
TR "v[186:187],v0 offset:59392\n"
MF "v[164:167],v[188:191],a[132:135],v[164:167]\n"
"buffer_load_dword v10,v12,s[40:43],s86 offen sc1\n"
MF "v[164:167],a[240:243],a[172:175],v[164:167]\n"
MF "v[164:167],a[244:247],a[212:215],v[164:167]\n"
MF "v[156:159],a[244:247],a[208:211],v[156:159]\n"
MF "v[156:159],a[240:243],a[168:171],v[156:159]\n"
MF "v[156:159],v[188:191],a[128:131],v[156:159]\n"
MF "v[148:151],v[188:191],a[124:127],v[148:151]\n"
MF "v[148:151],a[240:243],a[164:167],v[148:151]\n"
MF "v[148:151],a[244:247],a[204:207],v[148:151]\n"
MF "v[140:143],a[244:247],a[200:203],v[140:143]\n"
MF "v[140:143],a[240:243],a[160:163],v[140:143]\n"
MF "v[140:143],v[188:191],a[120:123],v[140:143]\n"
"s_waitcnt lgkmcnt(0)\n"
MF "v[96:99],v[176:179],a[20:23],v[96:99]\n"
"s_nop 0\n"
TR "v[188:189],v1 offset:49152\n"
TR "v[190:191],v1 offset:51200\n"
MF "v[96:99],v[180:183],a[60:63],v[96:99]\n"
"s_nop 0\n"
TR "a[240:241],v1 offset:53248\n"
TR "a[242:243],v1 offset:55296\n"
MF "v[96:99],v[184:187],a[100:103],v[96:99]\n"
"s_nop 0\n"
TR "a[244:245],v1 offset:57344\n"
TR "a[246:247],v1 offset:59392\n"
MF "v[104:107],v[184:187],a[104:107],v[104:107]\n"
MF "v[104:107],v[180:183],a[64:67],v[104:107]\n"
MF "v[104:107],v[176:179],a[24:27],v[104:107]\n"
"s_waitcnt vmcnt(18)\n"
"v_mov_b32 v236,v228\n"
"v_mov_b32 v237,v229\n"
MF "v[96:99],v[208:211],a[252:255],v[96:99]\n"
MF "v[104:107],v[212:215],a[252:255],v[104:107]\n"
MF "v[112:115],v[216:219],a[252:255],v[112:115]\n"
MF "v[120:123],v[220:223],a[252:255],v[120:123]\n"
MF "v[128:131],v[224:227],a[252:255],v[128:131]\n"
MF "v[100:103],v[208:211],a[248:251],v[100:103]\n"
MF "v[108:111],v[212:215],a[248:251],v[108:111]\n"
MF "v[116:119],v[216:219],a[248:251],v[116:119]\n"
MF "v[124:127],v[220:223],a[248:251],v[124:127]\n"
MF "v[132:135],v[224:227],a[248:251],v[132:135]\n"
MF "v[248:251],v[230:233],a[250:253],v[248:251]\n"
MF "v[244:247],v[234:237],a[250:253],v[244:247]\n"
MF "v[112:115],v[176:179],a[28:31],v[112:115]\n"
MF "v[112:115],v[180:183],a[68:71],v[112:115]\n"
MF "v[112:115],v[184:187],a[108:111],v[112:115]\n"
MF "v[120:123],v[184:187],a[112:115],v[120:123]\n"
"s_nop 3\n"
CV "v244,v244,v245\n"
CV "v245,v246,v247\n"
MF "v[120:123],v[180:183],a[72:75],v[120:123]\n"
"s_bfe_u32 s87,s22,0x30014\n"
CV "v246,v248,v249\n"
CV "v247,v250,v251\n"
MF "v[120:123],v[176:179],a[32:35],v[120:123]\n"
"s_lshl_b32 s87,s87,7\n"
"v_permlane16_swap_b32 v244,v246\n"
"v_permlane16_swap_b32 v245,v247\n"
MF "v[128:131],v[176:179],a[36:39],v[128:131]\n"
"buffer_store_dwordx4 v[244:247],v8,s[44:47],s0 offen sc1\n"
MF "v[128:131],v[180:183],a[76:79],v[128:131]\n"
"s_and_b32 s60,s56,1\n"
MF "v[128:131],v[184:187],a[116:119],v[128:131]\n"
"s_waitcnt lgkmcnt(0)\n"
MF "v[132:135],a[244:247],a[116:119],v[132:135]\n"
"s_mul_i32 s60,s60,204800\n"
TR "v[176:177],v2 offset:49152\n"
TR "v[178:179],v2 offset:51200\n"
MF "v[132:135],a[240:243],a[76:79],v[132:135]\n"
"s_add_u32 s87,s87,s60\n"
TR "v[180:181],v2 offset:53248\n"
TR "v[182:183],v2 offset:55296\n"
MF "v[132:135],v[188:191],a[36:39],v[132:135]\n"
"s_mov_b32 s71,204800\n"
TR "v[184:185],v2 offset:57344\n"
TR "v[186:187],v2 offset:59392\n"
MF "v[124:127],v[188:191],a[32:35],v[124:127]\n"
"s_mov_b32 s66,204800\n"
MF "v[124:127],a[240:243],a[72:75],v[124:127]\n"
"s_add_u32 s72,s78,2\n"
MF "v[124:127],a[244:247],a[112:115],v[124:127]\n"
"s_and_b32 s72,s72,15\n"
MF "v[116:119],a[244:247],a[108:111],v[116:119]\n"
"s_bfe_u32 s60,s22,0x10014\n"
MF "v[116:119],a[240:243],a[68:71],v[116:119]\n"
"s_lshl_b32 s60,s60,5\n"
MF "v[116:119],v[188:191],a[28:31],v[116:119]\n"
"v_xor_b32 v242,s60,v8\n"
MF "v[108:111],v[188:191],a[24:27],v[108:111]\n"
"s_add_u32 s16,s78,6\n"
MF "v[108:111],a[240:243],a[64:67],v[108:111]\n"
"s_and_b32 s16,s16,15\n"
MF "v[108:111],a[244:247],a[104:107],v[108:111]\n"
"s_add_u32 s17,s78,4\n"
MF "v[100:103],a[244:247],a[100:103],v[100:103]\n"
"s_and_b32 s17,s17,15\n"
MF "v[100:103],a[240:243],a[60:63],v[100:103]\n"
MF "v[100:103],v[188:191],a[20:23],v[100:103]\n"
"s_waitcnt lgkmcnt(0)\n"
MF "v[136:139],v[176:179],a[20:23],v[136:139]\n"
"s_nop 0\n"
TR "v[188:189],v3 offset:49152\n"
TR "v[190:191],v3 offset:51200\n"
MF "v[136:139],v[180:183],a[60:63],v[136:139]\n"
"s_nop 0\n"
TR "a[240:241],v3 offset:53248\n"
TR "a[242:243],v3 offset:55296\n"
MF "v[136:139],v[184:187],a[100:103],v[136:139]\n"
"s_nop 0\n"
TR "a[244:245],v3 offset:57344\n"
TR "a[246:247],v3 offset:59392\n"
MF "v[144:147],v[184:187],a[104:107],v[144:147]\n"
MF "v[144:147],v[180:183],a[64:67],v[144:147]\n"
MF "v[144:147],v[176:179],a[24:27],v[144:147]\n"
MF "v[152:155],v[176:179],a[28:31],v[152:155]\n"
MF "v[152:155],v[180:183],a[68:71],v[152:155]\n"
MF "v[152:155],v[184:187],a[108:111],v[152:155]\n"
MF "v[160:163],v[184:187],a[112:115],v[160:163]\n"
MF "v[160:163],v[180:183],a[72:75],v[160:163]\n"
MF "v[160:163],v[176:179],a[32:35],v[160:163]\n"
MF "v[168:171],v[176:179],a[36:39],v[168:171]\n"
MF "v[168:171],v[180:183],a[76:79],v[168:171]\n"
"s_waitcnt vmcnt(1) lgkmcnt(0)\n"
MF "v[168:171],v[184:187],a[116:119],v[168:171]\n"
"s_barrier\n"
MF "v[172:175],a[244:247],a[116:119],v[172:175]\n"
"v_sub_u32 v10,v10,v11\n"
MF "v[172:175],a[240:243],a[76:79],v[172:175]\n"
"s_nop 0\n"
TR "v[176:177],v4\n"
TR "v[178:179],v4 offset:2048\n"
MF "v[172:175],v[188:191],a[36:39],v[172:175]\n"
"s_nop 0\n"
TR "v[180:181],v4 offset:4096\n"
TR "v[182:183],v4 offset:6144\n"
MF "v[164:167],v[188:191],a[32:35],v[164:167]\n"
"s_nop 0\n"
TR "v[184:185],v4 offset:8192\n"
TR "v[186:187],v4 offset:10240\n"
MF "v[164:167],a[240:243],a[72:75],v[164:167]\n"
"v_readlane_b32 s16,v10,s16\n"
MF "v[164:167],a[244:247],a[112:115],v[164:167]\n"
"s_cmp_lt_u32 s16,9\n"
"s_cselect_b32 s16,0xf0000000,0\n"
MF "v[156:159],a[244:247],a[108:111],v[156:159]\n"
"v_readlane_b32 s17,v10,s17\n"
MF "v[156:159],a[240:243],a[68:71],v[156:159]\n"
"s_cmp_lt_u32 s17,10\n"
"s_cselect_b32 s17,0xf0000000,0\n"
MF "v[156:159],v[188:191],a[28:31],v[156:159]\n"
"s_bfe_u32 s62,s22,0x30014\n"
MF "v[148:151],v[188:191],a[24:27],v[148:151]\n"
"s_add_u32 s62,s62,3\n"
"s_and_b32 s62,s62,7\n"
"s_mul_i32 s62,s62,204800\n"
MF "v[148:151],a[240:243],a[64:67],v[148:151]\n"
"s_add_u32 s62,s62,s73\n"
"s_add_u32 s62,s62,133120\n"
"s_or_b32 s62,s62,s16\n"
MF "v[148:151],a[244:247],a[104:107],v[148:151]\n"
LD "v[16:19],v9,s[40:43],s62 offen sc1\n"
MF "v[140:143],a[244:247],a[100:103],v[140:143]\n"
LD "v[24:27],v9,s[40:43],s62 offen offset:1024 sc1\n"
MF "v[140:143],a[240:243],a[60:63],v[140:143]\n"
MF "v[140:143],v[188:191],a[20:23],v[140:143]\n"
"s_waitcnt lgkmcnt(0)\n"
LD "v[32:35],v9,s[40:43],s62 offen offset:2048 sc1\n"
MF "v[96:99],v[176:179],a[140:143],v[96:99]\n"
"s_nop 0\n"
TR "v[188:189],v5\n"
TR "v[190:191],v5 offset:2048\n"
MF "v[96:99],v[180:183],a[180:183],v[96:99]\n"
"s_nop 0\n"
TR "a[240:241],v5 offset:4096\n"
TR "a[242:243],v5 offset:6144\n"
MF "v[96:99],v[184:187],a[220:223],v[96:99]\n"
"s_nop 0\n"
TR "a[244:245],v5 offset:8192\n"
TR "a[246:247],v5 offset:10240\n"
MF "v[104:107],v[184:187],a[224:227],v[104:107]\n"
LD "v[40:43],v9,s[40:43],s62 offen offset:3072 sc1\n"
MF "v[104:107],v[180:183],a[184:187],v[104:107]\n"
MF "v[104:107],v[176:179],a[144:147],v[104:107]\n"
"s_add_u32 s62,s62,4096\n"
MF "v[112:115],v[176:179],a[148:151],v[112:115]\n"
LD "v[48:51],v9,s[40:43],s62 offen sc1\n"
MF "v[112:115],v[180:183],a[188:191],v[112:115]\n"
"s_bfe_u32 s62,s22,0x30014\n"
"ds_write_addtid_b32 v96 offset:16384\n"
"ds_write_addtid_b32 v97 offset:16640\n"
MF "v[112:115],v[184:187],a[228:231],v[112:115]\n"
"s_add_u32 s62,s62,2\n"
"s_and_b32 s62,s62,7\n"
"s_mul_i32 s62,s62,204800\n"
MF "v[120:123],v[184:187],a[232:235],v[120:123]\n"
"s_add_u32 s62,s62,s73\n"
"s_add_u32 s62,s62,153600\n"
"s_or_b32 s62,s62,s17\n"
MF "v[120:123],v[180:183],a[192:195],v[120:123]\n"
LD "v[20:23],v9,s[40:43],s62 offen sc1\n"
MF "v[120:123],v[176:179],a[152:155],v[120:123]\n"
"s_nop 0\n"
"ds_write_addtid_b32 v98 offset:16896\n"
"ds_write_addtid_b32 v99 offset:17152\n"
MF "v[128:131],v[176:179],a[156:159],v[128:131]\n"
LD "v[28:31],v9,s[40:43],s62 offen offset:1024 sc1\n"
MF "v[128:131],v[180:183],a[196:199],v[128:131]\n"
"s_nop 0\n"
"ds_write_addtid_b32 v104 offset:18432\n"
"ds_write_addtid_b32 v105 offset:18688\n"
MF "v[128:131],v[184:187],a[236:239],v[128:131]\n"
"s_waitcnt lgkmcnt(6)\n"
LD "v[36:39],v9,s[40:43],s62 offen offset:2048 sc1\n"
MF "v[100:103],v[188:191],a[140:143],v[100:103]\n"
"s_nop 0\n"
TR "v[176:177],v6\n"
TR "v[178:179],v6 offset:2048\n"
MF "v[100:103],a[240:243],a[180:183],v[100:103]\n"
"s_nop 0\n"
TR "v[180:181],v6 offset:4096\n"
TR "v[182:183],v6 offset:6144\n"
MF "v[100:103],a[244:247],a[220:223],v[100:103]\n"
"s_nop 0\n"
TR "v[184:185],v6 offset:8192\n"
TR "v[186:187],v6 offset:10240\n"
MF "v[108:111],a[244:247],a[224:227],v[108:111]\n"
LD "v[44:47],v9,s[40:43],s62 offen offset:3072 sc1\n"
MF "v[108:111],a[240:243],a[184:187],v[108:111]\n"
"s_nop 0\n"
"ds_write_addtid_b32 v106 offset:18944\n"
"ds_write_addtid_b32 v107 offset:19200\n"
MF "v[108:111],v[188:191],a[144:147],v[108:111]\n"
"s_add_u32 s62,s62,4096\n"
"ds_write_addtid_b32 v112 offset:20480\n"
"ds_write_addtid_b32 v113 offset:20736\n"
MF "v[116:119],v[188:191],a[148:151],v[116:119]\n"
LD "v[52:55],v9,s[40:43],s62 offen sc1\n"
MF "v[116:119],a[240:243],a[188:191],v[116:119]\n"
"s_nop 0\n"
"ds_write_addtid_b32 v114 offset:20992\n"
"ds_write_addtid_b32 v115 offset:21248\n"
MF "v[116:119],a[244:247],a[228:231],v[116:119]\n"
"s_nop 0\n"
"ds_write_addtid_b32 v120 offset:22528\n"
"ds_write_addtid_b32 v121 offset:22784\n"
MF "v[124:127],a[244:247],a[232:235],v[124:127]\n"
"s_nop 0\n"
"ds_write_addtid_b32 v122 offset:23040\n"
"ds_write_addtid_b32 v123 offset:23296\n"
MF "v[124:127],a[240:243],a[192:195],v[124:127]\n"
"s_nop 0\n"
"ds_write_addtid_b32 v128 offset:24576\n"
"ds_write_addtid_b32 v129 offset:24832\n"
MF "v[124:127],v[188:191],a[152:155],v[124:127]\n"
"buffer_load_dword v10,v12,s[40:43],s86 offen sc1\n"
MF "v[132:135],v[188:191],a[156:159],v[132:135]\n"
"s_nop 0\n"
"ds_write_addtid_b32 v130 offset:25088\n"
"ds_write_addtid_b32 v131 offset:25344\n"
MF "v[132:135],a[240:243],a[196:199],v[132:135]\n"
"s_nop 0\n"
"ds_write_addtid_b32 v100 offset:17408\n"
"ds_write_addtid_b32 v101 offset:17664\n"
MF "v[132:135],a[244:247],a[236:239],v[132:135]\n"
"s_waitcnt lgkmcnt(15)\n"
"ds_write_addtid_b32 v102 offset:17920\n"
"ds_write_addtid_b32 v103 offset:18176\n"
MF "v[136:139],v[176:179],a[140:143],v[136:139]\n"
"s_nop 0\n"
TR "v[188:189],v7\n"
TR "v[190:191],v7 offset:2048\n"
MF "v[136:139],v[180:183],a[180:183],v[136:139]\n"
"s_nop 0\n"
TR "a[240:241],v7 offset:4096\n"
TR "a[242:243],v7 offset:6144\n"
MF "v[136:139],v[184:187],a[220:223],v[136:139]\n"
"s_nop 0\n"
TR "a[244:245],v7 offset:8192\n"
TR "a[246:247],v7 offset:10240\n"
MF "v[144:147],v[184:187],a[224:227],v[144:147]\n"
"s_nop 0\n"
"ds_write_addtid_b32 v108 offset:19456\n"
"ds_write_addtid_b32 v109 offset:19712\n"
MF "v[144:147],v[180:183],a[184:187],v[144:147]\n"
"s_nop 0\n"
"ds_write_addtid_b32 v110 offset:19968\n"
"ds_write_addtid_b32 v111 offset:20224\n"
MF "v[144:147],v[176:179],a[144:147],v[144:147]\n"
"s_nop 0\n"
"ds_write_addtid_b32 v116 offset:21504\n"
"ds_write_addtid_b32 v117 offset:21760\n"
MF "v[152:155],v[176:179],a[148:151],v[152:155]\n"
"s_nop 0\n"
"ds_write_addtid_b32 v118 offset:22016\n"
"ds_write_addtid_b32 v119 offset:22272\n"
MF "v[152:155],v[180:183],a[188:191],v[152:155]\n"
"ds_write_addtid_b32 v124 offset:23552\n"
MF "v[152:155],v[184:187],a[228:231],v[152:155]\n"
"ds_write_addtid_b32 v125 offset:23808\n"
MF "v[160:163],v[184:187],a[232:235],v[160:163]\n"
"ds_write_addtid_b32 v126 offset:24064\n"
MF "v[160:163],v[180:183],a[192:195],v[160:163]\n"
"ds_write_addtid_b32 v127 offset:24320\n"
MF "v[160:163],v[176:179],a[152:155],v[160:163]\n"
"ds_write_addtid_b32 v132 offset:25600\n"
MF "v[168:171],v[176:179],a[156:159],v[168:171]\n"
"ds_write_addtid_b32 v133 offset:25856\n"
MF "v[168:171],v[180:183],a[196:199],v[168:171]\n"
"ds_write_addtid_b32 v134 offset:26112\n"
MF "v[168:171],v[184:187],a[236:239],v[168:171]\n"
"s_waitcnt lgkmcnt(15)\n"
"ds_write_addtid_b32 v135 offset:26368\n"
MF "v[140:143],v[188:191],a[140:143],v[140:143]\n"
MF "v[140:143],a[240:243],a[180:183],v[140:143]\n"
MF "v[140:143],a[244:247],a[220:223],v[140:143]\n"
MF "v[148:151],a[244:247],a[224:227],v[148:151]\n"
"s_waitcnt lgkmcnt(0)\n"
MF "v[148:151],a[240:243],a[184:187],v[148:151]\n"
"s_barrier\n"
MF "v[148:151],v[188:191],a[144:147],v[148:151]\n"
"ds_read2st64_b32 v[56:57],v13 offset0:0 offset1:1\n"
MF "v[156:159],v[188:191],a[148:151],v[156:159]\n"
"ds_read2st64_b32 v[58:59],v13 offset0:2 offset1:3\n"
MF "v[156:159],a[240:243],a[188:191],v[156:159]\n"
"ds_read2st64_b32 v[60:61],v13 offset0:4 offset1:5\n"
MF "v[156:159],a[244:247],a[228:231],v[156:159]\n"
"ds_read2st64_b32 v[62:63],v13 offset0:6 offset1:7\n"
MF "v[164:167],a[244:247],a[232:235],v[164:167]\n"
"ds_read2st64_b32 v[64:65],v13 offset0:8 offset1:9\n"
MF "v[164:167],a[240:243],a[192:195],v[164:167]\n"
"ds_read2st64_b32 v[66:67],v13 offset0:10 offset1:11\n"
MF "v[164:167],v[188:191],a[152:155],v[164:167]\n"
"ds_read2st64_b32 v[68:69],v13 offset0:12 offset1:13\n"
MF "v[172:175],v[188:191],a[156:159],v[172:175]\n"
"ds_read2st64_b32 v[70:71],v13 offset0:14 offset1:15\n"
MF "v[172:175],a[240:243],a[196:199],v[172:175]\n"
"ds_read2st64_b32 v[72:73],v13 offset0:16 offset1:17\n"
MF "v[172:175],a[244:247],a[236:239],v[172:175]\n"
"ds_read2st64_b32 v[74:75],v13 offset0:18 offset1:19\n"
"ds_read2st64_b32 v[76:77],v13 offset0:20 offset1:21\n"
"ds_read2st64_b32 v[78:79],v13 offset0:22 offset1:23\n"
"s_nop 0\n"
"ds_read2st64_b32 v[80:81],v13 offset0:24 offset1:25\n"
"ds_read2st64_b32 v[82:83],v13 offset0:26 offset1:27\n"
"ds_read2st64_b32 v[84:85],v13 offset0:28 offset1:29\n"
"ds_read2st64_b32 v[86:87],v13 offset0:30 offset1:31\n"
"ds_read2st64_b32 v[88:89],v13 offset0:32 offset1:33\n"
"ds_read2st64_b32 v[90:91],v13 offset0:34 offset1:35\n"
"ds_read2st64_b32 v[92:93],v13 offset0:36 offset1:37\n"
"ds_read2st64_b32 v[94:95],v13 offset0:38 offset1:39\n"
"s_waitcnt vmcnt(0)\n"
"v_sub_u32 v10,v10,v11\n"
"s_cmp_eq_u32 s16,0\n"
"s_cbranch_scc1 .Lhave03\n"
"s_add_u32 s59,s78,6\n"
"s_and_b32 s59,s59,15\n"
"s_nop 3\n"
".Lnf3:\n"
"v_readlane_b32 s58,v10,s59\n"
"s_cmp_lt_u32 s58,9\n"
"s_cbranch_scc1 .Lsp3\n"
".Lnb3:\n"
"s_bfe_u32 s62,s22,0x30014\n"
".Lpg31h:\n"
"s_cbranch_execz .Lpg34h\n"
"s_nop 0\n"
"s_add_u32 s62,s62,3\n"
"s_and_b32 s62,s62,7\n"
"s_mul_i32 s62,s62,204800\n"
"s_add_u32 s62,s62,s73\n"
"s_add_u32 s62,s62,133120\n"
LD "v[16:19],v9,s[40:43],s62 offen sc1\n"
LD "v[24:27],v9,s[40:43],s62 offen offset:1024 sc1\n"
LD "v[32:35],v9,s[40:43],s62 offen offset:2048 sc1\n"
LD "v[40:43],v9,s[40:43],s62 offen offset:3072 sc1\n"
"s_add_u32 s62,s62,4096\n"
LD "v[48:51],v9,s[40:43],s62 offen sc1\n"
".Lhave03:\n"
"s_cmp_eq_u32 s17,0\n"
"s_cbranch_scc1 .Lhave02\n"
"s_add_u32 s59,s78,4\n"
"s_and_b32 s59,s59,15\n"
"s_nop 3\n"
".Lnf4:\n"
"v_readlane_b32 s58,v10,s59\n"
"s_cmp_lt_u32 s58,10\n"
"s_cbranch_scc1 .Lsp4\n"
".Lnb4:\n"
"s_bfe_u32 s62,s22,0x30014\n"
"s_add_u32 s62,s62,2\n"
"s_and_b32 s62,s62,7\n"
"s_mul_i32 s62,s62,204800\n"
"s_add_u32 s62,s62,s73\n"
"s_add_u32 s62,s62,153600\n"
LD "v[20:23],v9,s[40:43],s62 offen sc1\n"
LD "v[28:31],v9,s[40:43],s62 offen offset:1024 sc1\n"
LD "v[36:39],v9,s[40:43],s62 offen offset:2048 sc1\n"
LD "v[44:47],v9,s[40:43],s62 offen offset:3072 sc1\n"
"s_add_u32 s62,s62,4096\n"
LD "v[52:55],v9,s[40:43],s62 offen sc1\n"
".Lhave02:\n"
".Lnf5:\n"
"v_readlane_b32 s58,v10,s72\n"
"s_cmp_lt_u32 s58,11\n"
"s_cbranch_scc1 .Lsp5\n"
".Lnb5:\n"
"s_bfe_u32 s62,s22,0x30014\n"
"s_add_u32 s62,s62,1\n"
"s_and_b32 s62,s62,7\n"
"s_mul_i32 s62,s62,204800\n"
"s_add_u32 s62,s62,s73\n"
"s_add_u32 s62,s62,174080\n"
LD "v[208:211],v9,s[40:43],s62 offen sc1\n"
LD "v[212:215],v9,s[40:43],s62 offen offset:1024 sc1\n"
LD "v[216:219],v9,s[40:43],s62 offen offset:2048 sc1\n"
LD "v[220:223],v9,s[40:43],s62 offen offset:3072 sc1\n"
"s_add_u32 s62,s62,4096\n"
LD "v[224:227],v9,s[40:43],s62 offen sc1\n"
"s_waitcnt lgkmcnt(15)\n"
"v_pk_add_f32 v[136:137],v[136:137],v[56:57]\n"
"v_pk_add_f32 v[138:139],v[138:139],v[58:59]\n"
"v_pk_add_f32 v[140:141],v[140:141],v[60:61]\n"
"v_pk_add_f32 v[142:143],v[142:143],v[62:63]\n"
"v_pk_add_f32 v[144:145],v[144:145],v[64:65]\n"
"s_waitcnt lgkmcnt(14)\n"
"v_pk_add_f32 v[146:147],v[146:147],v[66:67]\n"
"s_waitcnt lgkmcnt(13)\n"
"v_pk_add_f32 v[148:149],v[148:149],v[68:69]\n"
"s_waitcnt lgkmcnt(12)\n"
"v_pk_add_f32 v[150:151],v[150:151],v[70:71]\n"
"s_waitcnt lgkmcnt(11)\n"
"v_pk_add_f32 v[152:153],v[152:153],v[72:73]\n"
"s_waitcnt lgkmcnt(10)\n"
"v_pk_add_f32 v[154:155],v[154:155],v[74:75]\n"
"s_waitcnt lgkmcnt(9)\n"
"v_pk_add_f32 v[156:157],v[156:157],v[76:77]\n"
"s_waitcnt lgkmcnt(8)\n"
"v_pk_add_f32 v[158:159],v[158:159],v[78:79]\n"
"s_waitcnt lgkmcnt(7)\n"
"v_pk_add_f32 v[160:161],v[160:161],v[80:81]\n"
"s_waitcnt lgkmcnt(6)\n"
"v_pk_add_f32 v[162:163],v[162:163],v[82:83]\n"
"s_waitcnt lgkmcnt(5)\n"
"v_pk_add_f32 v[164:165],v[164:165],v[84:85]\n"
"s_waitcnt lgkmcnt(4)\n"
"v_pk_add_f32 v[166:167],v[166:167],v[86:87]\n"
"s_waitcnt lgkmcnt(3)\n"
"v_pk_add_f32 v[168:169],v[168:169],v[88:89]\n"
"s_waitcnt lgkmcnt(2)\n"
"v_pk_add_f32 v[170:171],v[170:171],v[90:91]\n"
"s_waitcnt lgkmcnt(1)\n"
"v_pk_add_f32 v[172:173],v[172:173],v[92:93]\n"
"s_waitcnt lgkmcnt(0)\n"
"v_pk_add_f32 v[174:175],v[174:175],v[94:95]\n"
"s_waitcnt vmcnt(5)\n"
"s_nop 0\n"
MF "v[140:143],v[16:19],a[252:255],v[140:143]\n"
MF "v[148:151],v[24:27],a[252:255],v[148:151]\n"
MF "v[156:159],v[32:35],a[252:255],v[156:159]\n"
MF "v[164:167],v[40:43],a[252:255],v[164:167]\n"
MF "v[172:175],v[48:51],a[252:255],v[172:175]\n"
MF "v[136:139],v[16:19],a[248:251],v[136:139]\n"
MF "v[144:147],v[24:27],a[248:251],v[144:147]\n"
MF "v[152:155],v[32:35],a[248:251],v[152:155]\n"
MF "v[160:163],v[40:43],a[248:251],v[160:163]\n"
MF "v[168:171],v[48:51],a[248:251],v[168:171]\n"
MF "v[136:139],v[20:23],a[252:255],v[136:139]\n"
MF "v[144:147],v[28:31],a[252:255],v[144:147]\n"
MF "v[152:155],v[36:39],a[252:255],v[152:155]\n"
MF "v[160:163],v[44:47],a[252:255],v[160:163]\n"
MF "v[168:171],v[52:55],a[252:255],v[168:171]\n"
MF "v[140:143],v[20:23],a[248:251],v[140:143]\n"
MF "v[148:151],v[28:31],a[248:251],v[148:151]\n"
MF "v[156:159],v[36:39],a[248:251],v[156:159]\n"
MF "v[164:167],v[44:47],a[248:251],v[164:167]\n"
MF "v[172:175],v[52:55],a[248:251],v[172:175]\n"
"s_waitcnt vmcnt(0)\n"
MF "v[140:143],v[208:211],a[252:255],v[140:143]\n"
MF "v[136:139],v[208:211],a[248:251],v[136:139]\n"
MF "v[148:151],v[212:215],a[252:255],v[148:151]\n"
MF "v[144:147],v[212:215],a[248:251],v[144:147]\n"
MF "v[156:159],v[216:219],a[252:255],v[156:159]\n"
"s_nop 4\n"
CV "v136,v136,v137\n"
CV "v137,v138,v139\n"
MF "v[152:155],v[216:219],a[248:251],v[152:155]\n"
CV "v138,v140,v141\n"
CV "v139,v142,v143\n"
MF "v[164:167],v[220:223],a[252:255],v[164:167]\n"
CV "v144,v144,v145\n"
CV "v145,v146,v147\n"
MF "v[160:163],v[220:223],a[248:251],v[160:163]\n"
CV "v146,v148,v149\n"
CV "v147,v150,v151\n"
"v_permlane16_swap_b32 v136,v138\n"
"v_permlane16_swap_b32 v137,v139\n"
"buffer_store_dwordx4 v[136:139],v242,s[44:47],s87 offen offset:64 sc1\n"
"s_add_u32 s87,s87,40960\n"
MF "v[172:175],v[224:227],a[252:255],v[172:175]\n"
CV "v152,v152,v153\n"
CV "v153,v154,v155\n"
MF "v[168:171],v[224:227],a[248:251],v[168:171]\n"
CV "v154,v156,v157\n"
CV "v155,v158,v159\n"
"v_permlane16_swap_b32 v144,v146\n"
"v_permlane16_swap_b32 v145,v147\n"
"buffer_store_dwordx4 v[144:147],v242,s[44:47],s87 offen offset:64 sc1\n"
"s_add_u32 s87,s87,40960\n"
CV "v160,v160,v161\n"
CV "v161,v162,v163\n"
CV "v162,v164,v165\n"
CV "v163,v166,v167\n"
"v_permlane16_swap_b32 v152,v154\n"
"v_permlane16_swap_b32 v153,v155\n"
"buffer_store_dwordx4 v[152:155],v242,s[44:47],s87 offen offset:64 sc1\n"
"s_add_u32 s87,s87,40960\n"
CV "v168,v168,v169\n"
CV "v169,v170,v171\n"
CV "v170,v172,v173\n"
CV "v171,v174,v175\n"
"v_permlane16_swap_b32 v160,v162\n"
"v_permlane16_swap_b32 v161,v163\n"
"buffer_store_dwordx4 v[160:163],v242,s[44:47],s87 offen offset:64 sc1\n"
"s_add_u32 s87,s87,40960\n"
"v_permlane16_swap_b32 v168,v170\n"
"v_permlane16_swap_b32 v169,v171\n"
"buffer_store_dwordx4 v[168:171],v242,s[44:47],s87 offen offset:64 sc1\n"
"s_add_u32 s87,s87,40960\n"
"s_endpgm\n"
".Lpgrole1_h:\n"
"s_cbranch_execz .Lpg35h\n"
".Lrole1:\n"
"s_waitcnt lgkmcnt(4)\n"
MF "v[16:19],a[120:123],v[176:179],v[252:255]\n"
"s_mov_b32 s65,128\n"
TR "v[188:189],v1 offset:24576\n"
AR "v220,a44\n"
"s_waitcnt lgkmcnt(3)\n"
MF "v[16:19],a[160:163],v[180:183],v[16:19]\n"
"s_mov_b32 s9,0x3f80\n"
TR "v[190:191],v1 offset:26624\n"
AR "v221,a45\n"
"s_waitcnt lgkmcnt(2)\n"
MF "v[16:19],a[200:203],v[184:187],v[16:19]\n"
AR "v222,a46\n"
AR "v223,a47\n"
MF "v[24:27],a[204:207],v[184:187],v[252:255]\n"
"s_bfe_u32 s58,s22,0x30014\n"
TR "a[240:241],v1 offset:28672\n"
AR "v224,a64\n"
MF "v[24:27],a[164:167],v[180:183],v[24:27]\n"
"s_add_u32 s61,s58,1\n"
TR "a[242:243],v1 offset:30720\n"
AR "v225,a65\n"
MF "v[24:27],a[124:127],v[176:179],v[24:27]\n"
"s_and_b32 s61,s61,7\n"
AR "v226,a66\n"
AR "v227,a67\n"
MF "v[32:35],a[128:131],v[176:179],v[252:255]\n"
"s_mov_b32 s64,9\n"
TR "a[244:245],v1 offset:32768\n"
"ds_read_b128 a[64:67],v236 offset:49152\n"
MF "v[32:35],a[168:171],v[180:183],v[32:35]\n"
"s_xor_b32 s67,s78,8\n"
TR "a[246:247],v1 offset:34816\n"
"v_fma_f32 v220,v220,v209,v215\n"
MF "v[32:35],a[208:211],v[184:187],v[32:35]\n"
"s_mov_b32 s71,204800\n"
"v_fma_f32 v221,v221,v209,v215\n"
"v_fma_f32 v222,v222,v209,v215\n"
MF "v[40:43],a[212:215],v[184:187],v[252:255]\n"
"s_mov_b32 s66,204800\n"
"v_fma_f32 v223,v223,v209,v215\n"
"v_fma_f32 v224,v224,v209,v215\n"
MF "v[40:43],a[172:175],v[180:183],v[40:43]\n"
"s_and_b32 s73,s56,1\n"
"v_fma_f32 v225,v225,v209,v215\n"
"v_fma_f32 v226,v226,v209,v215\n"
MF "v[40:43],a[132:135],v[176:179],v[40:43]\n"
"s_mul_i32 s73,s73,5120\n"
"v_fma_f32 v227,v227,v209,v215\n"
CV "v220,v220,v221\n"
MF "v[48:51],a[136:139],v[176:179],v[252:255]\n"
"s_add_u32 s73,s73,s93\n"
CV "v221,v222,v223\n"
CV "v222,v224,v225\n"
MF "v[48:51],a[176:179],v[180:183],v[48:51]\n"
"s_xor_b32 s58,s58,4\n"
CV "v223,v226,v227\n"
AW "a44,v220\n"
MF "v[48:51],a[216:219],v[184:187],v[48:51]\n"
"s_waitcnt lgkmcnt(0)\n"
AW "a45,v221\n"
AW "a46,v222\n"
MF "v[52:55],a[216:219],a[244:247],v[252:255]\n"
"s_mul_i32 s70,s58,204800\n"
TR "v[176:177],v2 offset:24576\n"
AW "a47,v223\n"
MF "v[52:55],a[176:179],a[240:243],v[52:55]\n"
"s_add_u32 s70,s70,s73\n"
TR "v[178:179],v2 offset:26624\n"
AR "v228,a84\n"
MF "v[52:55],a[136:139],v[188:191],v[52:55]\n"
"s_add_u32 s70,s70,0xc800\n"
AR "v229,a85\n"
AR "v230,a86\n"
MF "v[44:47],a[132:135],v[188:191],v[252:255]\n"
"s_mov_b32 s85,1\n"
TR "v[180:181],v2 offset:28672\n"
AR "v231,a87\n"
MF "v[44:47],a[172:175],a[240:243],v[44:47]\n"
"s_mov_b32 s83,0xf0000000\n"
TR "v[182:183],v2 offset:30720\n"
AR "v232,a104\n"
MF "v[44:47],a[212:215],a[244:247],v[44:47]\n"
"s_mov_b32 s82,0xf0000000\n"
AR "v233,a105\n"
AR "v234,a106\n"
MF "v[36:39],a[208:211],a[244:247],v[252:255]\n"
"s_add_u32 s58,s79,73728\n"
TR "v[184:185],v2 offset:32768\n"
AR "v235,a107\n"
MF "v[36:39],a[168:171],a[240:243],v[36:39]\n"
"s_mov_b32 s90,s91\n"
TR "v[186:187],v2 offset:34816\n"
"ds_read_b128 a[104:107],v237 offset:4096\n"
MF "v[36:39],a[128:131],v[188:191],v[36:39]\n"
"s_add_u32 s91,s91,20480\n"
"v_fma_f32 v228,v228,v209,v215\n"
"v_fma_f32 v229,v229,v209,v215\n"
MF "v[28:31],a[124:127],v[188:191],v[252:255]\n"
"s_bfe_u32 s74,s23,0x70000\n"
"v_fma_f32 v230,v230,v209,v215\n"
"v_fma_f32 v231,v231,v209,v215\n"
MF "v[28:31],a[164:167],a[240:243],v[28:31]\n"
"s_lshl_b32 s74,s74,1\n"
"v_fma_f32 v232,v232,v209,v215\n"
"v_fma_f32 v233,v233,v209,v215\n"
MF "v[28:31],a[204:207],a[244:247],v[28:31]\n"
"s_add_u32 s74,s74,s56\n"
"v_fma_f32 v234,v234,v209,v215\n"
"v_fma_f32 v235,v235,v209,v215\n"
MF "v[20:23],a[200:203],a[244:247],v[252:255]\n"
"s_sub_u32 s74,s74,2\n"
CV "v228,v228,v229\n"
CV "v229,v230,v231\n"
MF "v[20:23],a[160:163],a[240:243],v[20:23]\n"
"s_mul_i32 s74,s74,30720\n"
CV "v230,v232,v233\n"
CV "v231,v234,v235\n"
MF "v[20:23],a[120:123],v[188:191],v[20:23]\n"
"s_waitcnt lgkmcnt(1)\n"
AW "a84,v228\n"
AW "a85,v229\n"
MF "v[56:59],a[120:123],v[176:179],v[252:255]\n"
"s_bfe_u32 s68,s23,0x30003\n"
TR "v[188:189],v3 offset:24576\n"
"v_add_u32 v198,59392,v192\n"
MF "v[56:59],a[160:163],v[180:183],v[56:59]\n"
"s_add_u32 s68,s68,7\n"
TR "v[190:191],v3 offset:26624\n"
"v_add_u32 v199,68608,v192\n"
MF "v[56:59],a[200:203],v[184:187],v[56:59]\n"
"s_lshl_b32 s68,s68,7\n"
"v_add_u32 v200,81920,v192\n"
"v_add_u32 v201,91136,v192\n"
MF "v[64:67],a[204:207],v[184:187],v[252:255]\n"
"s_and_b32 s68,s68,768\n"
TR "a[240:241],v3 offset:28672\n"
"v_add_u32 v202,100352,v192\n"
MF "v[64:67],a[164:167],v[180:183],v[64:67]\n"
"s_bfe_u32 s4,s22,0x30014\n"
TR "a[242:243],v3 offset:30720\n"
"v_add_u32 v203,109568,v192\n"
MF "v[64:67],a[124:127],v[176:179],v[64:67]\n"
"s_add_u32 s4,s4,32\n"
AW "a86,v230\n"
AW "a87,v231\n"
MF "v[72:75],a[128:131],v[176:179],v[252:255]\n"
"s_sub_u32 s4,s4,s92\n"
TR "a[244:245],v3 offset:32768\n"
AR "v220,a8\n"
MF "v[72:75],a[168:171],v[180:183],v[72:75]\n"
"s_cmp_eq_u32 s56,2\n"
"s_cselect_b32 s4,s4,64\n"
TR "a[246:247],v3 offset:34816\n"
MF "v[72:75],a[208:211],v[184:187],v[72:75]\n"
"s_mul_i32 s95,s92,3277\n"
AR "v221,a9\n"
AR "v222,a10\n"
MF "v[80:83],a[212:215],v[184:187],v[252:255]\n"
"s_lshr_b32 s95,s95,16\n"
AR "v223,a11\n"
AR "v224,a28\n"
MF "v[80:83],a[172:175],v[180:183],v[80:83]\n"
"s_mul_i32 s59,s95,20\n"
AR "v225,a29\n"
AR "v226,a30\n"
MF "v[80:83],a[132:135],v[176:179],v[80:83]\n"
"s_sub_u32 s59,s92,s59\n"
AR "v227,a31\n"
"v_fma_f32 v220,v220,v210,v216\n"
MF "v[88:91],a[136:139],v[176:179],v[252:255]\n"
"s_cmp_ge_u32 s59,10\n"
"s_cselect_b32 s96,4294557760,0\n"
"ds_read_b128 a[28:31],v236 offset:32768\n"
MF "v[88:91],a[176:179],v[180:183],v[88:91]\n"
"s_waitcnt vmcnt(0) lgkmcnt(0)\n"
"v_fma_f32 v221,v221,v210,v216\n"
"v_fma_f32 v222,v222,v210,v216\n"
MF "v[88:91],a[216:219],v[184:187],v[88:91]\n"
"s_barrier\n"
MF "v[92:95],a[216:219],a[244:247],v[252:255]\n"
"s_add_u32 s89,s89,s65\n"
TR "v[176:177],v0\n"
"v_fma_f32 v223,v223,v210,v216\n"
MF "v[92:95],a[176:179],a[240:243],v[92:95]\n"
"s_add_u32 m0,s79,24576\n"
TR "v[178:179],v0 offset:2048\n"
"v_fma_f32 v224,v224,v210,v216\n"
MF "v[92:95],a[136:139],v[188:191],v[92:95]\n"
LD "v192,s[48:51],s89 offen lds\n"
MF "v[84:87],a[132:135],v[188:191],v[252:255]\n"
"s_add_u32 s96,s96,1024\n"
TR "v[180:181],v0 offset:4096\n"
"v_fma_f32 v225,v225,v210,v216\n"
MF "v[84:87],a[172:175],a[240:243],v[84:87]\n"
"s_mul_i32 s59,s59,40960\n"
TR "v[182:183],v0 offset:6144\n"
"v_fma_f32 v226,v226,v210,v216\n"
MF "v[84:87],a[212:215],a[244:247],v[84:87]\n"
LD "v193,s[48:51],s89 offen offset:1024 lds\n"
MF "v[76:79],a[208:211],a[244:247],v[252:255]\n"
"s_add_u32 s96,s96,s59\n"
TR "v[184:185],v0 offset:8192\n"
"v_fma_f32 v227,v227,v210,v216\n"
MF "v[76:79],a[168:171],a[240:243],v[76:79]\n"
"s_lshl_b32 s59,s95,7\n"
TR "v[186:187],v0 offset:10240\n"
CV "v220,v220,v221\n"
MF "v[76:79],a[128:131],v[188:191],v[76:79]\n"
"s_add_u32 s96,s96,s59\n"
TR "v[96:97],v0 offset:49152\n"
CV "v221,v222,v223\n"
MF "v[68:71],a[124:127],v[188:191],v[252:255]\n"
"s_lshl_b32 s2,s92,10\n"
TR "v[98:99],v0 offset:51200\n"
CV "v222,v224,v225\n"
MF "v[68:71],a[164:167],a[240:243],v[68:71]\n"
LD "v194,s[48:51],s89 offen offset:2048 lds\n"
MF "v[68:71],a[204:207],a[244:247],v[68:71]\n"
"s_add_u32 s2,s2,s93\n"
TR "v[100:101],v0 offset:53248\n"
CV "v223,v226,v227\n"
MF "v[60:63],a[200:203],a[244:247],v[252:255]\n"
"s_mov_b32 s0,s96\n"
TR "v[102:103],v0 offset:55296\n"
AW "a8,v220\n"
MF "v[60:63],a[160:163],a[240:243],v[60:63]\n"
"s_add_u32 s92,s92,s4\n"
TR "v[104:105],v0 offset:57344\n"
AW "a9,v221\n"
MF "v[60:63],a[120:123],v[188:191],v[60:63]\n"
"s_waitcnt lgkmcnt(5)\n"
LD "v195,s[48:51],s89 offen offset:3072 lds\n"
MF "v[16:19],a[140:143],v[176:179],v[16:19]\n"
"s_mul_i32 s95,s92,3277\n"
TR "v[188:189],v1\n"
AW "a10,v222\n"
MF "v[16:19],a[180:183],v[180:183],v[16:19]\n"
"s_lshr_b32 s95,s95,16\n"
TR "v[190:191],v1 offset:2048\n"
AW "a11,v223\n"
MF "v[16:19],a[220:223],v[184:187],v[16:19]\n"
"s_mul_i32 s59,s95,20\n"
TR "v[106:107],v0 offset:59392\n"
AR "v228,a48\n"
MF "v[24:27],a[224:227],v[184:187],v[24:27]\n"
"s_sub_u32 s59,s92,s59\n"
TR "a[240:241],v1 offset:4096\n"
AR "v229,a49\n"
MF "v[24:27],a[184:187],v[180:183],v[24:27]\n"
"s_add_u32 m0,s79,28672\n"
TR "a[242:243],v1 offset:6144\n"
AR "v230,a50\n"
MF "v[24:27],a[144:147],v[176:179],v[24:27]\n"
LD "v196,s[48:51],s89 offen lds\n"
MF "v[32:35],a[148:151],v[176:179],v[32:35]\n"
"s_cmp_ge_u32 s59,10\n"
"s_cselect_b32 s96,4294557760,0\n"
TR "a[244:245],v1 offset:8192\n"
MF "v[32:35],a[188:191],v[180:183],v[32:35]\n"
"s_add_u32 s96,s96,1024\n"
TR "a[246:247],v1 offset:10240\n"
AR "v231,a51\n"
MF "v[32:35],a[228:231],v[184:187],v[32:35]\n"
LD "v197,s[48:51],s89 offen offset:1024 lds\n"
MF "v[40:43],a[232:235],v[184:187],v[40:43]\n"
"s_mul_i32 s59,s59,40960\n"
TR "v[108:109],v4\n"
AR "v232,a68\n"
MF "v[40:43],a[192:195],v[180:183],v[40:43]\n"
"s_add_u32 s96,s96,s59\n"
TR "v[110:111],v4 offset:2048\n"
AR "v233,a69\n"
MF "v[40:43],a[152:155],v[176:179],v[40:43]\n"
"s_lshl_b32 s59,s95,7\n"
TR "v[112:113],v4 offset:4096\n"
AR "v234,a70\n"
MF "v[48:51],a[156:159],v[176:179],v[48:51]\n"
"s_add_u32 s96,s96,s59\n"
TR "v[114:115],v4 offset:6144\n"
AR "v235,a71\n"
MF "v[48:51],a[196:199],v[180:183],v[48:51]\n"
LD "v198,s[48:51],s89 offen offset:2048 lds\n"
MF "v[48:51],a[236:239],v[184:187],v[48:51]\n"
"s_waitcnt lgkmcnt(4)\n"
TR "v[116:117],v4 offset:8192\n"
"ds_read_b128 a[68:71],v236 offset:53248\n"
MF "v[52:55],a[236:239],a[244:247],v[52:55]\n"
"s_lshl_b32 s3,s92,10\n"
TR "v[176:177],v2\n"
"v_fma_f32 v228,v228,v210,v216\n"
MF "v[52:55],a[196:199],a[240:243],v[52:55]\n"
"s_add_u32 s3,s3,s93\n"
TR "v[178:179],v2 offset:2048\n"
"v_fma_f32 v229,v229,v210,v216\n"
MF "v[52:55],a[156:159],v[188:191],v[52:55]\n"
LD "v199,s[48:51],s89 offen offset:3072 lds\n"
MF "v[44:47],a[152:155],v[188:191],v[44:47]\n"
"s_cmp_lt_u32 s92,40\n"
"s_cselect_b32 s3,s3,0xf0000000\n"
TR "v[180:181],v2 offset:4096\n"
MF "v[44:47],a[192:195],a[240:243],v[44:47]\n"
"s_cmp_lt_u32 s92,40\n"
"s_cselect_b32 s1,s96,0xf0000000\n"
TR "v[182:183],v2 offset:6144\n"
MF "v[44:47],a[232:235],a[244:247],v[44:47]\n"
"s_sub_u32 s92,s92,s4\n"
TR "v[118:119],v4 offset:10240\n"
"v_fma_f32 v230,v230,v210,v216\n"
MF "v[36:39],a[228:231],a[244:247],v[36:39]\n"
"s_sub_u32 s2,s2,204800\n"
TR "v[184:185],v2 offset:8192\n"
"v_fma_f32 v231,v231,v210,v216\n"
MF "v[36:39],a[188:191],a[240:243],v[36:39]\n"
"s_add_u32 m0,s79,32768\n"
TR "v[186:187],v2 offset:10240\n"
"v_fma_f32 v232,v232,v210,v216\n"
MF "v[36:39],a[148:151],v[188:191],v[36:39]\n"
LD "v200,s[48:51],s89 offen lds\n"
MF "v[28:31],a[144:147],v[188:191],v[28:31]\n"
"s_mov_b32 s92,64\n"
TR "v[120:121],v1 offset:49152\n"
"v_fma_f32 v233,v233,v210,v216\n"
MF "v[28:31],a[184:187],a[240:243],v[28:31]\n"
TR "v[122:123],v1 offset:51200\n"
"v_fma_f32 v234,v234,v210,v216\n"
MF "v[28:31],a[224:227],a[244:247],v[28:31]\n"
TR "v[124:125],v1 offset:53248\n"
"v_fma_f32 v235,v235,v210,v216\n"
MF "v[20:23],a[220:223],a[244:247],v[20:23]\n"
LD "v201,s[48:51],s89 offen offset:1024 lds\n"
MF "v[20:23],a[180:183],a[240:243],v[20:23]\n"
TR "v[126:127],v1 offset:55296\n"
CV "v228,v228,v229\n"
MF "v[20:23],a[140:143],v[188:191],v[20:23]\n"
"s_waitcnt lgkmcnt(4)\n"
TR "v[128:129],v1 offset:57344\n"
CV "v229,v230,v231\n"
MF "v[56:59],a[140:143],v[176:179],v[56:59]\n"
TR "v[188:189],v3\n"
CV "v230,v232,v233\n"
MF "v[56:59],a[180:183],v[180:183],v[56:59]\n"
TR "v[190:191],v3 offset:2048\n"
CV "v231,v234,v235\n"
MF "v[56:59],a[220:223],v[184:187],v[56:59]\n"
LD "v202,s[48:51],s89 offen offset:2048 lds\n"
MF "v[64:67],a[224:227],v[184:187],v[64:67]\n"
TR "a[240:241],v3 offset:4096\n"
AW "a48,v228\n"
MF "v[64:67],a[184:187],v[180:183],v[64:67]\n"
TR "a[242:243],v3 offset:6144\n"
AW "a49,v229\n"
MF "v[64:67],a[144:147],v[176:179],v[64:67]\n"
LD "v203,s[48:51],s89 offen offset:3072 lds\n"
MF "v[72:75],a[148:151],v[176:179],v[72:75]\n"
TR "a[244:245],v3 offset:8192\n"
AW "a50,v230\n"
MF "v[72:75],a[188:191],v[180:183],v[72:75]\n"
TR "a[246:247],v3 offset:10240\n"
AW "a51,v231\n"
MF "v[72:75],a[228:231],v[184:187],v[72:75]\n"
TR "v[130:131],v1 offset:59392\n"
AR "v220,a88\n"
MF "v[80:83],a[232:235],v[184:187],v[80:83]\n"
TR "v[132:133],v5\n"
AR "v221,a89\n"
MF "v[80:83],a[192:195],v[180:183],v[80:83]\n"
TR "v[134:135],v5 offset:2048\n"
AR "v222,a90\n"
MF "v[80:83],a[152:155],v[176:179],v[80:83]\n"
TR "v[136:137],v5 offset:4096\n"
AR "v223,a91\n"
MF "v[88:91],a[156:159],v[176:179],v[88:91]\n"
TR "v[138:139],v5 offset:6144\n"
AR "v224,a108\n"
MF "v[88:91],a[196:199],v[180:183],v[88:91]\n"
TR "v[140:141],v5 offset:8192\n"
"s_waitcnt lgkmcnt(0)\n"
MF "v[88:91],a[236:239],v[184:187],v[88:91]\n"
"s_barrier\n"
MF "v[92:95],a[236:239],a[244:247],v[92:95]\n"
TR "v[142:143],v5 offset:10240\n"
AR "v225,a109\n"
MF "v[92:95],a[196:199],a[240:243],v[92:95]\n"
TR "v[144:145],v2 offset:49152\n"
AR "v226,a110\n"
MF "v[92:95],a[156:159],v[188:191],v[92:95]\n"
TR "v[146:147],v2 offset:51200\n"
AR "v227,a111\n"
MF "v[84:87],a[152:155],v[188:191],v[84:87]\n"
"s_nop 0\n"
TR "v[148:149],v2 offset:53248\n"
"ds_read_b128 a[108:111],v237 offset:8192\n"
MF "v[84:87],a[192:195],a[240:243],v[84:87]\n"
TR "v[150:151],v2 offset:55296\n"
"v_fma_f32 v220,v220,v210,v216\n"
MF "v[84:87],a[232:235],a[244:247],v[84:87]\n"
TR "v[152:153],v2 offset:57344\n"
"v_fma_f32 v221,v221,v210,v216\n"
MF "v[76:79],a[228:231],a[244:247],v[76:79]\n"
TR "v[154:155],v2 offset:59392\n"
"v_fma_f32 v222,v222,v210,v216\n"
MF "v[76:79],a[188:191],a[240:243],v[76:79]\n"
TR "v[156:157],v6\n"
"v_fma_f32 v223,v223,v210,v216\n"
MF "v[76:79],a[148:151],v[188:191],v[76:79]\n"
".Lpg32h:\n"
"s_cbranch_execz .Lpg36h\n"
TR "v[158:159],v6 offset:2048\n"
"v_fma_f32 v224,v224,v210,v216\n"
MF "v[68:71],a[144:147],v[188:191],v[68:71]\n"
TR "v[160:161],v6 offset:4096\n"
"v_fma_f32 v225,v225,v210,v216\n"
MF "v[68:71],a[184:187],a[240:243],v[68:71]\n"
TR "v[162:163],v6 offset:6144\n"
"v_fma_f32 v226,v226,v210,v216\n"
MF "v[68:71],a[224:227],a[244:247],v[68:71]\n"
TR "v[164:165],v6 offset:8192\n"
"v_fma_f32 v227,v227,v210,v216\n"
MF "v[60:63],a[220:223],a[244:247],v[60:63]\n"
TR "v[166:167],v6 offset:10240\n"
CV "v220,v220,v221\n"
MF "v[60:63],a[180:183],a[240:243],v[60:63]\n"
TR "v[168:169],v3 offset:49152\n"
CV "v221,v222,v223\n"
MF "v[60:63],a[140:143],v[188:191],v[60:63]\n"
TR "v[170:171],v3 offset:51200\n"
CV "v222,v224,v225\n"
MF "v[16:19],a[0:3],v[96:99],v[16:19]\n"
TR "v[172:173],v3 offset:53248\n"
CV "v223,v226,v227\n"
MF "v[16:19],a[40:43],v[100:103],v[16:19]\n"
TR "v[174:175],v3 offset:55296\n"
AW "a88,v220\n"
MF "v[16:19],a[80:83],v[104:107],v[16:19]\n"
TR "v[204:205],v3 offset:57344\n"
AW "a89,v221\n"
MF "v[16:19],a[20:23],v[108:111],v[16:19]\n"
TR "v[206:207],v3 offset:59392\n"
AW "a90,v222\n"
MF "v[16:19],a[60:63],v[112:115],v[16:19]\n"
TR "v[244:245],v7\n"
AW "a91,v223\n"
MF "v[16:19],a[100:103],v[116:119],v[16:19]\n"
"s_waitcnt lgkmcnt(15)\n"
TR "v[246:247],v7 offset:2048\n"
AR "v228,a12\n"
MF "v[20:23],a[100:103],v[140:143],v[20:23]\n"
TR "v[248:249],v7 offset:4096\n"
AR "v229,a13\n"
MF "v[20:23],a[60:63],v[136:139],v[20:23]\n"
TR "v[250:251],v7 offset:6144\n"
AR "v230,a14\n"
MF "v[20:23],a[20:23],v[132:135],v[20:23]\n"
TR "v[238:239],v7 offset:8192\n"
AR "v231,a15\n"
MF "v[20:23],a[80:83],v[128:131],v[20:23]\n"
TR "v[240:241],v7 offset:10240\n"
AR "v232,a32\n"
MF "v[20:23],a[40:43],v[124:127],v[20:23]\n"
AR "v233,a33\n"
AR "v234,a34\n"
MF "v[20:23],a[0:3],v[120:123],v[20:23]\n"
"s_waitcnt lgkmcnt(12)\n"
AR "v235,a35\n"
"v_fma_f32 v228,v228,v211,v217\n"
MF "v[56:59],a[0:3],v[144:147],v[56:59]\n"
"ds_read_b128 a[32:35],v236 offset:36864\n"
"v_fma_f32 v229,v229,v211,v217\n"
MF "v[56:59],a[40:43],v[148:151],v[56:59]\n"
"v_fma_f32 v230,v230,v211,v217\n"
"v_fma_f32 v231,v231,v211,v217\n"
MF "v[56:59],a[80:83],v[152:155],v[56:59]\n"
"v_fma_f32 v232,v232,v211,v217\n"
"v_fma_f32 v233,v233,v211,v217\n"
MF "v[56:59],a[20:23],v[156:159],v[56:59]\n"
"v_fma_f32 v234,v234,v211,v217\n"
"v_fma_f32 v235,v235,v211,v217\n"
MF "v[56:59],a[60:63],v[160:163],v[56:59]\n"
CV "v228,v228,v229\n"
CV "v229,v230,v231\n"
MF "v[56:59],a[100:103],v[164:167],v[56:59]\n"
"s_waitcnt lgkmcnt(1)\n"
CV "v230,v232,v233\n"
CV "v231,v234,v235\n"
MF "v[60:63],a[100:103],v[238:241],v[60:63]\n"
AW "a12,v228\n"
AW "a13,v229\n"
MF "v[60:63],a[60:63],v[248:251],v[60:63]\n"
AW "a14,v230\n"
AW "a15,v231\n"
MF "v[60:63],a[20:23],v[244:247],v[60:63]\n"
AR "v220,a52\n"
AR "v221,a53\n"
MF "v[60:63],a[80:83],v[204:207],v[60:63]\n"
AR "v222,a54\n"
AR "v223,a55\n"
MF "v[60:63],a[40:43],v[172:175],v[60:63]\n"
AR "v224,a72\n"
AR "v225,a73\n"
MF "v[60:63],a[0:3],v[168:171],v[60:63]\n"
AR "v226,a74\n"
AR "v227,a75\n"
MF "v[68:71],a[4:7],v[168:171],v[68:71]\n"
"ds_read_b128 a[72:75],v236 offset:57344\n"
"v_fma_f32 v220,v220,v211,v217\n"
MF "v[68:71],a[44:47],v[172:175],v[68:71]\n"
"v_fma_f32 v221,v221,v211,v217\n"
"v_fma_f32 v222,v222,v211,v217\n"
MF "v[68:71],a[84:87],v[204:207],v[68:71]\n"
"v_fma_f32 v223,v223,v211,v217\n"
"v_fma_f32 v224,v224,v211,v217\n"
MF "v[68:71],a[24:27],v[244:247],v[68:71]\n"
"v_fma_f32 v225,v225,v211,v217\n"
"v_fma_f32 v226,v226,v211,v217\n"
MF "v[68:71],a[64:67],v[248:251],v[68:71]\n"
"v_fma_f32 v227,v227,v211,v217\n"
CV "v220,v220,v221\n"
MF "v[68:71],a[104:107],v[238:241],v[68:71]\n"
"s_waitcnt lgkmcnt(0)\n"
CV "v221,v222,v223\n"
CV "v222,v224,v225\n"
MF "v[64:67],a[104:107],v[164:167],v[64:67]\n"
"s_barrier\n"
MF "v[64:67],a[64:67],v[160:163],v[64:67]\n"
CV "v223,v226,v227\n"
AW "a52,v220\n"
MF "v[64:67],a[24:27],v[156:159],v[64:67]\n"
AW "a53,v221\n"
AW "a54,v222\n"
MF "v[64:67],a[84:87],v[152:155],v[64:67]\n"
AW "a55,v223\n"
AR "v228,a92\n"
MF "v[64:67],a[44:47],v[148:151],v[64:67]\n"
AR "v229,a93\n"
AR "v230,a94\n"
MF "v[64:67],a[4:7],v[144:147],v[64:67]\n"
AR "v231,a95\n"
AR "v232,a112\n"
MF "v[28:31],a[4:7],v[120:123],v[28:31]\n"
AR "v233,a113\n"
AR "v234,a114\n"
MF "v[28:31],a[44:47],v[124:127],v[28:31]\n"
AR "v235,a115\n"
"v_fma_f32 v228,v228,v211,v217\n"
MF "v[28:31],a[84:87],v[128:131],v[28:31]\n"
"ds_read_b128 a[112:115],v237 offset:12288\n"
"v_fma_f32 v229,v229,v211,v217\n"
MF "v[28:31],a[24:27],v[132:135],v[28:31]\n"
"v_fma_f32 v230,v230,v211,v217\n"
"v_fma_f32 v231,v231,v211,v217\n"
MF "v[28:31],a[64:67],v[136:139],v[28:31]\n"
"v_fma_f32 v232,v232,v211,v217\n"
"v_fma_f32 v233,v233,v211,v217\n"
MF "v[28:31],a[104:107],v[140:143],v[28:31]\n"
"v_fma_f32 v234,v234,v211,v217\n"
"v_fma_f32 v235,v235,v211,v217\n"
MF "v[24:27],a[104:107],v[116:119],v[24:27]\n"
CV "v228,v228,v229\n"
CV "v229,v230,v231\n"
MF "v[24:27],a[64:67],v[112:115],v[24:27]\n"
CV "v230,v232,v233\n"
CV "v231,v234,v235\n"
MF "v[24:27],a[24:27],v[108:111],v[24:27]\n"
AW "a92,v228\n"
AW "a93,v229\n"
MF "v[24:27],a[84:87],v[104:107],v[24:27]\n"
AW "a94,v230\n"
AW "a95,v231\n"
MF "v[24:27],a[44:47],v[100:103],v[24:27]\n"
AR "v220,a16\n"
AR "v221,a17\n"
MF "v[24:27],a[4:7],v[96:99],v[24:27]\n"
AR "v222,a18\n"
AR "v223,a19\n"
MF "v[32:35],a[8:11],v[96:99],v[32:35]\n"
AR "v224,a36\n"
AR "v225,a37\n"
MF "v[32:35],a[48:51],v[100:103],v[32:35]\n"
AR "v226,a38\n"
AR "v227,a39\n"
MF "v[32:35],a[88:91],v[104:107],v[32:35]\n"
"ds_read_b128 a[36:39],v236 offset:40960\n"
"v_fma_f32 v220,v220,v212,v218\n"
MF "v[32:35],a[28:31],v[108:111],v[32:35]\n"
"v_fma_f32 v221,v221,v212,v218\n"
"v_fma_f32 v222,v222,v212,v218\n"
MF "v[32:35],a[68:71],v[112:115],v[32:35]\n"
"v_fma_f32 v223,v223,v212,v218\n"
"v_fma_f32 v224,v224,v212,v218\n"
MF "v[32:35],a[108:111],v[116:119],v[32:35]\n"
"v_fma_f32 v225,v225,v212,v218\n"
"v_fma_f32 v226,v226,v212,v218\n"
MF "v[36:39],a[108:111],v[140:143],v[36:39]\n"
"v_fma_f32 v227,v227,v212,v218\n"
CV "v220,v220,v221\n"
MF "v[36:39],a[68:71],v[136:139],v[36:39]\n"
CV "v221,v222,v223\n"
CV "v222,v224,v225\n"
MF "v[36:39],a[28:31],v[132:135],v[36:39]\n"
CV "v223,v226,v227\n"
AW "a16,v220\n"
MF "v[36:39],a[88:91],v[128:131],v[36:39]\n"
AW "a17,v221\n"
AW "a18,v222\n"
MF "v[36:39],a[48:51],v[124:127],v[36:39]\n"
AW "a19,v223\n"
AR "v228,a56\n"
MF "v[36:39],a[8:11],v[120:123],v[36:39]\n"
AR "v229,a57\n"
AR "v230,a58\n"
MF "v[72:75],a[8:11],v[144:147],v[72:75]\n"
AR "v231,a59\n"
AR "v232,a76\n"
MF "v[72:75],a[48:51],v[148:151],v[72:75]\n"
AR "v233,a77\n"
AR "v234,a78\n"
MF "v[72:75],a[88:91],v[152:155],v[72:75]\n"
AR "v235,a79\n"
"v_fma_f32 v228,v228,v212,v218\n"
MF "v[72:75],a[28:31],v[156:159],v[72:75]\n"
"ds_read_b128 a[76:79],v236 offset:61440\n"
"v_fma_f32 v229,v229,v212,v218\n"
MF "v[72:75],a[68:71],v[160:163],v[72:75]\n"
"v_fma_f32 v230,v230,v212,v218\n"
"v_fma_f32 v231,v231,v212,v218\n"
MF "v[72:75],a[108:111],v[164:167],v[72:75]\n"
"v_fma_f32 v232,v232,v212,v218\n"
"v_fma_f32 v233,v233,v212,v218\n"
MF "v[76:79],a[108:111],v[238:241],v[76:79]\n"
"v_fma_f32 v234,v234,v212,v218\n"
"v_fma_f32 v235,v235,v212,v218\n"
MF "v[76:79],a[68:71],v[248:251],v[76:79]\n"
CV "v228,v228,v229\n"
CV "v229,v230,v231\n"
MF "v[76:79],a[28:31],v[244:247],v[76:79]\n"
CV "v230,v232,v233\n"
CV "v231,v234,v235\n"
MF "v[76:79],a[88:91],v[204:207],v[76:79]\n"
AW "a56,v228\n"
AW "a57,v229\n"
MF "v[76:79],a[48:51],v[172:175],v[76:79]\n"
AW "a58,v230\n"
AW "a59,v231\n"
MF "v[76:79],a[8:11],v[168:171],v[76:79]\n"
"s_waitcnt lgkmcnt(2)\n"
AR "v220,a96\n"
AR "v221,a97\n"
MF "v[84:87],a[12:15],v[168:171],v[84:87]\n"
AR "v222,a98\n"
AR "v223,a99\n"
MF "v[84:87],a[52:55],v[172:175],v[84:87]\n"
AR "v224,a116\n"
AR "v225,a117\n"
MF "v[84:87],a[92:95],v[204:207],v[84:87]\n"
AR "v226,a118\n"
AR "v227,a119\n"
MF "v[84:87],a[32:35],v[244:247],v[84:87]\n"
"ds_read_b128 a[116:119],v237 offset:16384\n"
"v_fma_f32 v220,v220,v212,v218\n"
MF "v[84:87],a[72:75],v[248:251],v[84:87]\n"
"v_fma_f32 v221,v221,v212,v218\n"
"v_fma_f32 v222,v222,v212,v218\n"
MF "v[84:87],a[112:115],v[238:241],v[84:87]\n"
"v_fma_f32 v223,v223,v212,v218\n"
"v_fma_f32 v224,v224,v212,v218\n"
MF "v[80:83],a[112:115],v[164:167],v[80:83]\n"
"v_fma_f32 v225,v225,v212,v218\n"
"v_fma_f32 v226,v226,v212,v218\n"
MF "v[80:83],a[72:75],v[160:163],v[80:83]\n"
"v_fma_f32 v227,v227,v212,v218\n"
CV "v220,v220,v221\n"
MF "v[80:83],a[32:35],v[156:159],v[80:83]\n"
CV "v221,v222,v223\n"
CV "v222,v224,v225\n"
MF "v[80:83],a[92:95],v[152:155],v[80:83]\n"
CV "v223,v226,v227\n"
AW "a96,v220\n"
MF "v[80:83],a[52:55],v[148:151],v[80:83]\n"
AW "a97,v221\n"
AW "a98,v222\n"
MF "v[80:83],a[12:15],v[144:147],v[80:83]\n"
AW "a99,v223\n"
"v_mov_b32 v15,0\n"
MF "v[44:47],a[12:15],v[120:123],v[44:47]\n"
"v_lshrrev_b32 v242,5,v9\n"
"v_min_u32 v242,23,v242\n"
MF "v[44:47],a[52:55],v[124:127],v[44:47]\n"
"v_mul_u32_u24 v242,160,v242\n"
"v_and_or_b32 v242,v9,16,v242\n"
MF "v[44:47],a[92:95],v[128:131],v[44:47]\n"
"v_lshlrev_b32 v242,3,v242\n"
AR "v8,a252\n"
MF "v[44:47],a[32:35],v[132:135],v[44:47]\n"
AR "v11,a253\n"
"v_bfe_u32 v224,v9,5,3\n"
MF "v[44:47],a[72:75],v[136:139],v[44:47]\n"
"s_add_u32 s62,s74,s68\n"
"s_add_u32 s68,s68,256\n"
"s_and_b32 s68,s68,768\n"
MF "v[44:47],a[112:115],v[140:143],v[44:47]\n"
"buffer_load_dword v243,v242,s[48:51],s62 offen\n"
MF "v[40:43],a[112:115],v[116:119],v[40:43]\n"
"v_bfe_u32 v225,v9,8,2\n"
"v_lshlrev_b32 v225,1,v225\n"
MF "v[40:43],a[72:75],v[112:115],v[40:43]\n"
"v_mov_b32 v14,s76\n"
"s_add_u32 s76,s76,1\n"
MF "v[40:43],a[32:35],v[108:111],v[40:43]\n"
"buffer_store_dword v14,v15,s[40:43],s94 offen sc1\n"
MF "v[40:43],a[92:95],v[104:107],v[40:43]\n"
"v_and_b32 v226,16,v9\n"
"v_lshlrev_b32_e64 v226,v226,s9\n"
MF "v[40:43],a[52:55],v[100:103],v[40:43]\n"
"v_sub_u32 v224,v224,v225\n"
"v_cmp_eq_u32 vcc,0,v224\n"
MF "v[40:43],a[12:15],v[96:99],v[40:43]\n"
"s_waitcnt lgkmcnt(0)\n"
"v_cndmask_b32 v220,0,v226,vcc\n"
"v_cmp_eq_u32 vcc,1,v224\n"
MF "v[48:51],a[16:19],v[96:99],v[48:51]\n"
"s_nop 0\n"
"v_cndmask_b32 v221,0,v226,vcc\n"
AW "a248,0\n"
MF "v[48:51],a[56:59],v[100:103],v[48:51]\n"
AW "a249,0\n"
AW "a250,v220\n"
MF "v[48:51],a[96:99],v[104:107],v[48:51]\n"
AW "a251,v221\n"
AW "a252,v220\n"
MF "v[48:51],a[36:39],v[108:111],v[48:51]\n"
AW "a253,v221\n"
AW "a254,0\n"
MF "v[48:51],a[76:79],v[112:115],v[48:51]\n"
AW "a255,0\n"
"v_bfe_u32 v12,v9,4,4\n"
MF "v[48:51],a[116:119],v[116:119],v[48:51]\n"
"v_lshlrev_b32 v12,2,v12\n"
"v_lshrrev_b32 v13,2,v9\n"
MF "v[52:55],a[116:119],v[140:143],v[52:55]\n"
"v_add_u32 v13,s58,v13\n"
"v_add_u32 v4,24576,v4\n"
MF "v[52:55],a[76:79],v[136:139],v[52:55]\n"
"s_waitcnt vmcnt(2) lgkmcnt(0)\n"
"v_add_u32 v5,24576,v5\n"
"v_add_u32 v6,24576,v6\n"
MF "v[52:55],a[36:39],v[132:135],v[52:55]\n"
"s_barrier\n"
MF "v[52:55],a[96:99],v[128:131],v[52:55]\n"
TR "v[176:177],v0 offset:24576\n"
"v_add_u32 v7,24576,v7\n"
MF "v[52:55],a[56:59],v[124:127],v[52:55]\n"
TR "v[178:179],v0 offset:26624\n"
MF "v[52:55],a[16:19],v[120:123],v[52:55]\n"
MF "v[88:91],a[16:19],v[144:147],v[88:91]\n"
TR "v[180:181],v0 offset:28672\n"
MF "v[88:91],a[56:59],v[148:151],v[88:91]\n"
TR "v[182:183],v0 offset:30720\n"
MF "v[88:91],a[96:99],v[152:155],v[88:91]\n"
MF "v[88:91],a[36:39],v[156:159],v[88:91]\n"
TR "v[184:185],v0 offset:32768\n"
MF "v[88:91],a[76:79],v[160:163],v[88:91]\n"
TR "v[186:187],v0 offset:34816\n"
MF "v[88:91],a[116:119],v[164:167],v[88:91]\n"
MF "v[92:95],a[116:119],v[238:241],v[92:95]\n"
MF "v[92:95],a[76:79],v[248:251],v[92:95]\n"
MF "v[92:95],a[36:39],v[244:247],v[92:95]\n"
MF "v[92:95],a[96:99],v[204:207],v[92:95]\n"
MF "v[92:95],a[56:59],v[172:175],v[92:95]\n"
MF "v[92:95],a[16:19],v[168:171],v[92:95]\n"
"s_waitcnt lgkmcnt(0)\n"
".Lpl15:\n"
MF "v[96:99],a[0:3],v[176:179],v[252:255]\n"
TR "v[188:189],v1 offset:24576\n"
MF "v[96:99],a[40:43],v[180:183],v[96:99]\n"
TR "v[190:191],v1 offset:26624\n"
MF "v[96:99],a[80:83],v[184:187],v[96:99]\n"
MF "v[104:107],a[84:87],v[184:187],v[252:255]\n"
TR "a[240:241],v1 offset:28672\n"
MF "v[104:107],a[44:47],v[180:183],v[104:107]\n"
TR "a[242:243],v1 offset:30720\n"
MF "v[104:107],a[4:7],v[176:179],v[104:107]\n"
MF "v[112:115],a[8:11],v[176:179],v[252:255]\n"
TR "a[244:245],v1 offset:32768\n"
MF "v[112:115],a[48:51],v[180:183],v[112:115]\n"
TR "a[246:247],v1 offset:34816\n"
MF "v[112:115],a[88:91],v[184:187],v[112:115]\n"
MF "v[120:123],a[92:95],v[184:187],v[252:255]\n"
MF "v[120:123],a[52:55],v[180:183],v[120:123]\n"
MF "v[120:123],a[12:15],v[176:179],v[120:123]\n"
MF "v[128:131],a[16:19],v[176:179],v[252:255]\n"
MF "v[128:131],a[56:59],v[180:183],v[128:131]\n"
MF "v[128:131],a[96:99],v[184:187],v[128:131]\n"
"s_waitcnt lgkmcnt(0)\n"
MF "v[132:135],a[96:99],a[244:247],v[252:255]\n"
TR "v[176:177],v2 offset:24576\n"
MF "v[132:135],a[56:59],a[240:243],v[132:135]\n"
TR "v[178:179],v2 offset:26624\n"
MF "v[132:135],a[16:19],v[188:191],v[132:135]\n"
MF "v[124:127],a[12:15],v[188:191],v[252:255]\n"
TR "v[180:181],v2 offset:28672\n"
MF "v[124:127],a[52:55],a[240:243],v[124:127]\n"
TR "v[182:183],v2 offset:30720\n"
MF "v[124:127],a[92:95],a[244:247],v[124:127]\n"
MF "v[116:119],a[88:91],a[244:247],v[252:255]\n"
TR "v[184:185],v2 offset:32768\n"
MF "v[116:119],a[48:51],a[240:243],v[116:119]\n"
TR "v[186:187],v2 offset:34816\n"
MF "v[116:119],a[8:11],v[188:191],v[116:119]\n"
MF "v[108:111],a[4:7],v[188:191],v[252:255]\n"
MF "v[108:111],a[44:47],a[240:243],v[108:111]\n"
MF "v[108:111],a[84:87],a[244:247],v[108:111]\n"
MF "v[100:103],a[80:83],a[244:247],v[252:255]\n"
MF "v[100:103],a[40:43],a[240:243],v[100:103]\n"
MF "v[100:103],a[0:3],v[188:191],v[100:103]\n"
"s_waitcnt lgkmcnt(0)\n"
MF "v[136:139],a[0:3],v[176:179],v[252:255]\n"
TR "v[188:189],v3 offset:24576\n"
MF "v[136:139],a[40:43],v[180:183],v[136:139]\n"
TR "v[190:191],v3 offset:26624\n"
MF "v[136:139],a[80:83],v[184:187],v[136:139]\n"
MF "v[144:147],a[84:87],v[184:187],v[252:255]\n"
TR "a[240:241],v3 offset:28672\n"
MF "v[144:147],a[44:47],v[180:183],v[144:147]\n"
TR "a[242:243],v3 offset:30720\n"
MF "v[144:147],a[4:7],v[176:179],v[144:147]\n"
MF "v[152:155],a[8:11],v[176:179],v[252:255]\n"
TR "a[244:245],v3 offset:32768\n"
MF "v[152:155],a[48:51],v[180:183],v[152:155]\n"
TR "a[246:247],v3 offset:34816\n"
MF "v[152:155],a[88:91],v[184:187],v[152:155]\n"
MF "v[160:163],a[92:95],v[184:187],v[252:255]\n"
MF "v[160:163],a[52:55],v[180:183],v[160:163]\n"
MF "v[160:163],a[12:15],v[176:179],v[160:163]\n"
MF "v[168:171],a[16:19],v[176:179],v[252:255]\n"
MF "v[168:171],a[56:59],v[180:183],v[168:171]\n"
"s_waitcnt lgkmcnt(0)\n"
MF "v[168:171],a[96:99],v[184:187],v[168:171]\n"
"s_barrier\n"
MF "v[172:175],a[96:99],a[244:247],v[252:255]\n"
TR "v[176:177],v0\n"
MF "v[172:175],a[56:59],a[240:243],v[172:175]\n"
TR "v[178:179],v0 offset:2048\n"
MF "v[172:175],a[16:19],v[188:191],v[172:175]\n"
"ds_read2st64_b32 v[192:193],v13 offset0:192 offset1:193\n"
MF "v[164:167],a[12:15],v[188:191],v[252:255]\n"
TR "v[180:181],v0 offset:4096\n"
MF "v[164:167],a[52:55],a[240:243],v[164:167]\n"
TR "v[182:183],v0 offset:6144\n"
MF "v[164:167],a[92:95],a[244:247],v[164:167]\n"
"ds_read2st64_b32 v[194:195],v13 offset0:194 offset1:195\n"
MF "v[156:159],a[88:91],a[244:247],v[252:255]\n"
TR "v[184:185],v0 offset:8192\n"
MF "v[156:159],a[48:51],a[240:243],v[156:159]\n"
TR "v[186:187],v0 offset:10240\n"
MF "v[156:159],a[8:11],v[188:191],v[156:159]\n"
"ds_read2st64_b32 v[196:197],v13 offset0:196 offset1:197\n"
MF "v[148:151],a[4:7],v[188:191],v[252:255]\n"
"ds_read2st64_b32 v[198:199],v13 offset0:198 offset1:199\n"
MF "v[148:151],a[44:47],a[240:243],v[148:151]\n"
MF "v[148:151],a[84:87],a[244:247],v[148:151]\n"
MF "v[140:143],a[80:83],a[244:247],v[252:255]\n"
MF "v[140:143],a[40:43],a[240:243],v[140:143]\n"
MF "v[140:143],a[0:3],v[188:191],v[140:143]\n"
"s_waitcnt lgkmcnt(2)\n"
MF "v[96:99],a[120:123],v[176:179],v[96:99]\n"
"s_waitcnt lgkmcnt(0)\n"
TR "v[188:189],v1\n"
"v_add_f32 v16,v16,v192\n"
MF "v[96:99],a[160:163],v[180:183],v[96:99]\n"
TR "v[190:191],v1 offset:2048\n"
"v_add_f32 v17,v17,v193\n"
MF "v[96:99],a[200:203],v[184:187],v[96:99]\n"
"ds_read2st64_b32 v[200:201],v13 offset0:200 offset1:201\n"
"v_add_f32 v18,v18,v194\n"
"s_waitcnt vmcnt(0)\n"
"v_mov_b32 v14,s76\n"
"s_add_u32 s76,s76,1\n"
MF "v[104:107],a[204:207],v[184:187],v[104:107]\n"
TR "a[240:241],v1 offset:4096\n"
"v_add_f32 v19,v19,v195\n"
"buffer_store_dword v14,v15,s[40:43],s94 offen sc1\n"
MF "v[104:107],a[164:167],v[180:183],v[104:107]\n"
TR "a[242:243],v1 offset:6144\n"
"v_add_f32 v20,v20,v196\n"
MF "v[104:107],a[124:127],v[176:179],v[104:107]\n"
"ds_read2st64_b32 v[202:203],v13 offset0:202 offset1:203\n"
"v_add_f32 v21,v21,v197\n"
MF "v[112:115],a[128:131],v[176:179],v[112:115]\n"
TR "a[244:245],v1 offset:8192\n"
"v_add_f32 v22,v22,v198\n"
MF "v[112:115],a[168:171],v[180:183],v[112:115]\n"
TR "a[246:247],v1 offset:10240\n"
"v_add_f32 v23,v23,v199\n"
MF "v[112:115],a[208:211],v[184:187],v[112:115]\n"
"ds_read2st64_b32 v[204:205],v13 offset0:204 offset1:205\n"
CV "v16,v16,v17\n"
MF "v[120:123],a[212:215],v[184:187],v[120:123]\n"
"ds_read2st64_b32 v[206:207],v13 offset0:206 offset1:207\n"
CV "v17,v18,v19\n"
MF "v[120:123],a[172:175],v[180:183],v[120:123]\n"
"s_add_u32 s87,s90,0\n"
CV "v18,v20,v21\n"
CV "v19,v22,v23\n"
MF "v[120:123],a[132:135],v[176:179],v[120:123]\n"
MF "v[128:131],a[136:139],v[176:179],v[128:131]\n"
"buffer_store_dwordx4 v[16:19],v9,s[40:43],s87 offen sc1\n"
MF "v[128:131],a[176:179],v[180:183],v[128:131]\n"
MF "v[128:131],a[216:219],v[184:187],v[128:131]\n"
"s_waitcnt lgkmcnt(2)\n"
MF "v[132:135],a[216:219],a[244:247],v[132:135]\n"
"s_waitcnt lgkmcnt(0)\n"
TR "v[176:177],v2\n"
"v_add_f32 v24,v24,v200\n"
MF "v[132:135],a[176:179],a[240:243],v[132:135]\n"
TR "v[178:179],v2 offset:2048\n"
"v_add_f32 v25,v25,v201\n"
MF "v[132:135],a[136:139],v[188:191],v[132:135]\n"
"ds_read2st64_b32 v[192:193],v13 offset0:208 offset1:209\n"
"v_add_f32 v26,v26,v202\n"
MF "v[124:127],a[132:135],v[188:191],v[124:127]\n"
TR "v[180:181],v2 offset:4096\n"
"v_add_f32 v27,v27,v203\n"
MF "v[124:127],a[172:175],a[240:243],v[124:127]\n"
TR "v[182:183],v2 offset:6144\n"
"v_add_f32 v28,v28,v204\n"
MF "v[124:127],a[212:215],a[244:247],v[124:127]\n"
"ds_read2st64_b32 v[194:195],v13 offset0:210 offset1:211\n"
"v_add_f32 v29,v29,v205\n"
MF "v[116:119],a[208:211],a[244:247],v[116:119]\n"
TR "v[184:185],v2 offset:8192\n"
"v_add_f32 v30,v30,v206\n"
MF "v[116:119],a[168:171],a[240:243],v[116:119]\n"
TR "v[186:187],v2 offset:10240\n"
"v_add_f32 v31,v31,v207\n"
MF "v[116:119],a[128:131],v[188:191],v[116:119]\n"
"ds_read2st64_b32 v[196:197],v13 offset0:212 offset1:213\n"
CV "v24,v24,v25\n"
MF "v[108:111],a[124:127],v[188:191],v[108:111]\n"
"ds_read2st64_b32 v[198:199],v13 offset0:214 offset1:215\n"
CV "v25,v26,v27\n"
MF "v[108:111],a[164:167],a[240:243],v[108:111]\n"
"s_add_u32 s87,s90,1024\n"
CV "v26,v28,v29\n"
CV "v27,v30,v31\n"
MF "v[108:111],a[204:207],a[244:247],v[108:111]\n"
MF "v[100:103],a[200:203],a[244:247],v[100:103]\n"
"buffer_store_dwordx4 v[24:27],v9,s[40:43],s87 offen sc1\n"
MF "v[100:103],a[160:163],a[240:243],v[100:103]\n"
".Lpg33h:\n"
"s_cbranch_execz .Lpg37h\n"
MF "v[100:103],a[120:123],v[188:191],v[100:103]\n"
"s_waitcnt lgkmcnt(2)\n"
MF "v[136:139],a[120:123],v[176:179],v[136:139]\n"
"s_waitcnt lgkmcnt(0)\n"
TR "v[188:189],v3\n"
"v_add_f32 v32,v32,v192\n"
MF "v[136:139],a[160:163],v[180:183],v[136:139]\n"
TR "v[190:191],v3 offset:2048\n"
"v_add_f32 v33,v33,v193\n"
MF "v[136:139],a[200:203],v[184:187],v[136:139]\n"
"ds_read2st64_b32 v[200:201],v13 offset0:216 offset1:217\n"
"v_add_f32 v34,v34,v194\n"
MF "v[144:147],a[204:207],v[184:187],v[144:147]\n"
TR "a[240:241],v3 offset:4096\n"
"v_add_f32 v35,v35,v195\n"
MF "v[144:147],a[164:167],v[180:183],v[144:147]\n"
TR "a[242:243],v3 offset:6144\n"
"v_add_f32 v36,v36,v196\n"
MF "v[144:147],a[124:127],v[176:179],v[144:147]\n"
"ds_read2st64_b32 v[202:203],v13 offset0:218 offset1:219\n"
"v_add_f32 v37,v37,v197\n"
MF "v[152:155],a[128:131],v[176:179],v[152:155]\n"
TR "a[244:245],v3 offset:8192\n"
"v_add_f32 v38,v38,v198\n"
MF "v[152:155],a[168:171],v[180:183],v[152:155]\n"
TR "a[246:247],v3 offset:10240\n"
"v_add_f32 v39,v39,v199\n"
MF "v[152:155],a[208:211],v[184:187],v[152:155]\n"
"ds_read2st64_b32 v[204:205],v13 offset0:220 offset1:221\n"
CV "v32,v32,v33\n"
MF "v[160:163],a[212:215],v[184:187],v[160:163]\n"
"ds_read2st64_b32 v[206:207],v13 offset0:222 offset1:223\n"
CV "v33,v34,v35\n"
MF "v[160:163],a[172:175],v[180:183],v[160:163]\n"
"s_add_u32 s87,s90,2048\n"
CV "v34,v36,v37\n"
CV "v35,v38,v39\n"
MF "v[160:163],a[132:135],v[176:179],v[160:163]\n"
MF "v[168:171],a[136:139],v[176:179],v[168:171]\n"
"buffer_store_dwordx4 v[32:35],v9,s[40:43],s87 offen sc1\n"
MF "v[168:171],a[176:179],v[180:183],v[168:171]\n"
"s_waitcnt lgkmcnt(0)\n"
MF "v[168:171],a[216:219],v[184:187],v[168:171]\n"
"s_barrier\n"
MF "v[172:175],a[216:219],a[244:247],v[172:175]\n"
TR "v[176:177],v0 offset:49152\n"
"v_add_f32 v40,v40,v200\n"
MF "v[172:175],a[176:179],a[240:243],v[172:175]\n"
TR "v[178:179],v0 offset:51200\n"
"v_add_f32 v41,v41,v201\n"
MF "v[172:175],a[136:139],v[188:191],v[172:175]\n"
"ds_read2st64_b32 v[192:193],v13 offset0:224 offset1:225\n"
"v_add_f32 v42,v42,v202\n"
MF "v[164:167],a[132:135],v[188:191],v[164:167]\n"
TR "v[180:181],v0 offset:53248\n"
"v_add_f32 v43,v43,v203\n"
MF "v[164:167],a[172:175],a[240:243],v[164:167]\n"
TR "v[182:183],v0 offset:55296\n"
"v_add_f32 v44,v44,v204\n"
MF "v[164:167],a[212:215],a[244:247],v[164:167]\n"
"ds_read2st64_b32 v[194:195],v13 offset0:226 offset1:227\n"
"v_add_f32 v45,v45,v205\n"
MF "v[156:159],a[208:211],a[244:247],v[156:159]\n"
TR "v[184:185],v0 offset:57344\n"
"v_add_f32 v46,v46,v206\n"
MF "v[156:159],a[168:171],a[240:243],v[156:159]\n"
TR "v[186:187],v0 offset:59392\n"
"v_add_f32 v47,v47,v207\n"
MF "v[156:159],a[128:131],v[188:191],v[156:159]\n"
"ds_read2st64_b32 v[196:197],v13 offset0:228 offset1:229\n"
CV "v40,v40,v41\n"
MF "v[148:151],a[124:127],v[188:191],v[148:151]\n"
"ds_read2st64_b32 v[198:199],v13 offset0:230 offset1:231\n"
CV "v41,v42,v43\n"
MF "v[148:151],a[164:167],a[240:243],v[148:151]\n"
"s_add_u32 s87,s90,3072\n"
CV "v42,v44,v45\n"
CV "v43,v46,v47\n"
MF "v[148:151],a[204:207],a[244:247],v[148:151]\n"
MF "v[140:143],a[200:203],a[244:247],v[140:143]\n"
"buffer_store_dwordx4 v[40:43],v9,s[40:43],s87 offen sc1\n"
MF "v[140:143],a[160:163],a[240:243],v[140:143]\n"
MF "v[140:143],a[120:123],v[188:191],v[140:143]\n"
"s_waitcnt lgkmcnt(2)\n"
MF "v[96:99],a[20:23],v[176:179],v[96:99]\n"
"s_waitcnt lgkmcnt(0)\n"
TR "v[188:189],v1 offset:49152\n"
"v_add_f32 v48,v48,v192\n"
MF "v[96:99],a[60:63],v[180:183],v[96:99]\n"
TR "v[190:191],v1 offset:51200\n"
"v_add_f32 v49,v49,v193\n"
MF "v[96:99],a[100:103],v[184:187],v[96:99]\n"
"ds_read2st64_b32 v[200:201],v13 offset0:0 offset1:1\n"
"v_add_f32 v50,v50,v194\n"
MF "v[104:107],a[104:107],v[184:187],v[104:107]\n"
TR "a[240:241],v1 offset:53248\n"
"v_add_f32 v51,v51,v195\n"
MF "v[104:107],a[64:67],v[180:183],v[104:107]\n"
TR "a[242:243],v1 offset:55296\n"
"v_add_f32 v52,v52,v196\n"
MF "v[104:107],a[24:27],v[176:179],v[104:107]\n"
"ds_read2st64_b32 v[202:203],v13 offset0:2 offset1:3\n"
"v_add_f32 v53,v53,v197\n"
MF "v[112:115],a[28:31],v[176:179],v[112:115]\n"
TR "a[244:245],v1 offset:57344\n"
"v_add_f32 v54,v54,v198\n"
MF "v[112:115],a[68:71],v[180:183],v[112:115]\n"
TR "a[246:247],v1 offset:59392\n"
"v_add_f32 v55,v55,v199\n"
MF "v[112:115],a[108:111],v[184:187],v[112:115]\n"
"ds_read2st64_b32 v[204:205],v13 offset0:4 offset1:5\n"
CV "v48,v48,v49\n"
MF "v[120:123],a[112:115],v[184:187],v[120:123]\n"
"ds_read2st64_b32 v[206:207],v13 offset0:6 offset1:7\n"
CV "v49,v50,v51\n"
MF "v[120:123],a[72:75],v[180:183],v[120:123]\n"
"s_add_u32 s87,s90,4096\n"
CV "v50,v52,v53\n"
CV "v51,v54,v55\n"
MF "v[120:123],a[32:35],v[176:179],v[120:123]\n"
MF "v[128:131],a[36:39],v[176:179],v[128:131]\n"
"buffer_store_dwordx4 v[48:51],v9,s[40:43],s87 offen sc1\n"
MF "v[128:131],a[76:79],v[180:183],v[128:131]\n"
MF "v[128:131],a[116:119],v[184:187],v[128:131]\n"
"s_waitcnt lgkmcnt(2)\n"
MF "v[132:135],a[116:119],a[244:247],v[132:135]\n"
"s_waitcnt lgkmcnt(0)\n"
TR "v[176:177],v2 offset:49152\n"
"v_add_f32 v56,v56,v200\n"
MF "v[132:135],a[76:79],a[240:243],v[132:135]\n"
TR "v[178:179],v2 offset:51200\n"
"v_add_f32 v57,v57,v201\n"
MF "v[132:135],a[36:39],v[188:191],v[132:135]\n"
"ds_read2st64_b32 v[192:193],v13 offset0:8 offset1:9\n"
"v_add_f32 v58,v58,v202\n"
MF "v[124:127],a[32:35],v[188:191],v[124:127]\n"
TR "v[180:181],v2 offset:53248\n"
"v_add_f32 v59,v59,v203\n"
MF "v[124:127],a[72:75],a[240:243],v[124:127]\n"
TR "v[182:183],v2 offset:55296\n"
"v_add_f32 v60,v60,v204\n"
MF "v[124:127],a[112:115],a[244:247],v[124:127]\n"
"ds_read2st64_b32 v[194:195],v13 offset0:10 offset1:11\n"
"v_add_f32 v61,v61,v205\n"
MF "v[116:119],a[108:111],a[244:247],v[116:119]\n"
TR "v[184:185],v2 offset:57344\n"
"v_add_f32 v62,v62,v206\n"
MF "v[116:119],a[68:71],a[240:243],v[116:119]\n"
TR "v[186:187],v2 offset:59392\n"
"v_add_f32 v63,v63,v207\n"
MF "v[116:119],a[28:31],v[188:191],v[116:119]\n"
"ds_read2st64_b32 v[196:197],v13 offset0:12 offset1:13\n"
CV "v56,v56,v57\n"
MF "v[108:111],a[24:27],v[188:191],v[108:111]\n"
"ds_read2st64_b32 v[198:199],v13 offset0:14 offset1:15\n"
CV "v57,v58,v59\n"
MF "v[108:111],a[64:67],a[240:243],v[108:111]\n"
"s_add_u32 s87,s90,10240\n"
CV "v58,v60,v61\n"
CV "v59,v62,v63\n"
MF "v[108:111],a[104:107],a[244:247],v[108:111]\n"
MF "v[100:103],a[100:103],a[244:247],v[100:103]\n"
"buffer_store_dwordx4 v[56:59],v9,s[40:43],s87 offen sc1\n"
MF "v[100:103],a[60:63],a[240:243],v[100:103]\n"
MF "v[100:103],a[20:23],v[188:191],v[100:103]\n"
"s_waitcnt lgkmcnt(2)\n"
MF "v[136:139],a[20:23],v[176:179],v[136:139]\n"
"s_waitcnt lgkmcnt(0)\n"
TR "v[188:189],v3 offset:49152\n"
"v_add_f32 v64,v64,v192\n"
MF "v[136:139],a[60:63],v[180:183],v[136:139]\n"
TR "v[190:191],v3 offset:51200\n"
"v_add_f32 v65,v65,v193\n"
MF "v[136:139],a[100:103],v[184:187],v[136:139]\n"
"ds_read2st64_b32 v[200:201],v13 offset0:16 offset1:17\n"
"v_add_f32 v66,v66,v194\n"
MF "v[144:147],a[104:107],v[184:187],v[144:147]\n"
TR "a[240:241],v3 offset:53248\n"
"v_add_f32 v67,v67,v195\n"
MF "v[144:147],a[64:67],v[180:183],v[144:147]\n"
TR "a[242:243],v3 offset:55296\n"
"v_add_f32 v68,v68,v196\n"
MF "v[144:147],a[24:27],v[176:179],v[144:147]\n"
"ds_read2st64_b32 v[202:203],v13 offset0:18 offset1:19\n"
"v_add_f32 v69,v69,v197\n"
MF "v[152:155],a[28:31],v[176:179],v[152:155]\n"
TR "a[244:245],v3 offset:57344\n"
"v_add_f32 v70,v70,v198\n"
MF "v[152:155],a[68:71],v[180:183],v[152:155]\n"
TR "a[246:247],v3 offset:59392\n"
"v_add_f32 v71,v71,v199\n"
MF "v[152:155],a[108:111],v[184:187],v[152:155]\n"
"ds_read2st64_b32 v[204:205],v13 offset0:20 offset1:21\n"
CV "v64,v64,v65\n"
MF "v[160:163],a[112:115],v[184:187],v[160:163]\n"
"ds_read2st64_b32 v[206:207],v13 offset0:22 offset1:23\n"
CV "v65,v66,v67\n"
MF "v[160:163],a[72:75],v[180:183],v[160:163]\n"
"s_add_u32 s87,s90,11264\n"
CV "v66,v68,v69\n"
CV "v67,v70,v71\n"
MF "v[160:163],a[32:35],v[176:179],v[160:163]\n"
MF "v[168:171],a[36:39],v[176:179],v[168:171]\n"
"buffer_store_dwordx4 v[64:67],v9,s[40:43],s87 offen sc1\n"
MF "v[168:171],a[76:79],v[180:183],v[168:171]\n"
"s_waitcnt lgkmcnt(0)\n"
MF "v[168:171],a[116:119],v[184:187],v[168:171]\n"
"s_barrier\n"
MF "v[172:175],a[116:119],a[244:247],v[172:175]\n"
TR "v[176:177],v4\n"
"v_add_f32 v72,v72,v200\n"
MF "v[172:175],a[76:79],a[240:243],v[172:175]\n"
TR "v[178:179],v4 offset:2048\n"
"v_add_f32 v73,v73,v201\n"
MF "v[172:175],a[36:39],v[188:191],v[172:175]\n"
"ds_read2st64_b32 v[192:193],v13 offset0:24 offset1:25\n"
"v_add_f32 v74,v74,v202\n"
MF "v[164:167],a[32:35],v[188:191],v[164:167]\n"
TR "v[180:181],v4 offset:4096\n"
"v_add_f32 v75,v75,v203\n"
MF "v[164:167],a[72:75],a[240:243],v[164:167]\n"
TR "v[182:183],v4 offset:6144\n"
"v_add_f32 v76,v76,v204\n"
MF "v[164:167],a[112:115],a[244:247],v[164:167]\n"
"ds_read2st64_b32 v[194:195],v13 offset0:26 offset1:27\n"
"v_add_f32 v77,v77,v205\n"
MF "v[156:159],a[108:111],a[244:247],v[156:159]\n"
TR "v[184:185],v4 offset:8192\n"
"v_add_f32 v78,v78,v206\n"
MF "v[156:159],a[68:71],a[240:243],v[156:159]\n"
TR "v[186:187],v4 offset:10240\n"
"v_add_f32 v79,v79,v207\n"
MF "v[156:159],a[28:31],v[188:191],v[156:159]\n"
"ds_read2st64_b32 v[196:197],v13 offset0:28 offset1:29\n"
CV "v72,v72,v73\n"
MF "v[148:151],a[24:27],v[188:191],v[148:151]\n"
"ds_read2st64_b32 v[198:199],v13 offset0:30 offset1:31\n"
CV "v73,v74,v75\n"
MF "v[148:151],a[64:67],a[240:243],v[148:151]\n"
"s_add_u32 s87,s90,12288\n"
CV "v74,v76,v77\n"
CV "v75,v78,v79\n"
MF "v[148:151],a[104:107],a[244:247],v[148:151]\n"
MF "v[140:143],a[100:103],a[244:247],v[140:143]\n"
"buffer_store_dwordx4 v[72:75],v9,s[40:43],s87 offen sc1\n"
MF "v[140:143],a[60:63],a[240:243],v[140:143]\n"
MF "v[140:143],a[20:23],v[188:191],v[140:143]\n"
"s_waitcnt lgkmcnt(2)\n"
MF "v[96:99],a[140:143],v[176:179],v[96:99]\n"
"s_waitcnt lgkmcnt(0)\n"
TR "v[188:189],v5\n"
"v_add_f32 v80,v80,v192\n"
MF "v[96:99],a[180:183],v[180:183],v[96:99]\n"
TR "v[190:191],v5 offset:2048\n"
"v_add_f32 v81,v81,v193\n"
MF "v[96:99],a[220:223],v[184:187],v[96:99]\n"
"ds_read2st64_b32 v[200:201],v13 offset0:32 offset1:33\n"
"v_add_f32 v82,v82,v194\n"
MF "v[104:107],a[224:227],v[184:187],v[104:107]\n"
TR "a[240:241],v5 offset:4096\n"
"v_add_f32 v83,v83,v195\n"
MF "v[104:107],a[184:187],v[180:183],v[104:107]\n"
TR "a[242:243],v5 offset:6144\n"
"v_add_f32 v84,v84,v196\n"
MF "v[104:107],a[144:147],v[176:179],v[104:107]\n"
"ds_read2st64_b32 v[202:203],v13 offset0:34 offset1:35\n"
"v_add_f32 v85,v85,v197\n"
MF "v[112:115],a[148:151],v[176:179],v[112:115]\n"
TR "a[244:245],v5 offset:8192\n"
"v_add_f32 v86,v86,v198\n"
MF "v[112:115],a[188:191],v[180:183],v[112:115]\n"
TR "a[246:247],v5 offset:10240\n"
"v_add_f32 v87,v87,v199\n"
MF "v[112:115],a[228:231],v[184:187],v[112:115]\n"
"ds_read2st64_b32 v[204:205],v13 offset0:36 offset1:37\n"
CV "v80,v80,v81\n"
MF "v[120:123],a[232:235],v[184:187],v[120:123]\n"
"ds_read2st64_b32 v[206:207],v13 offset0:38 offset1:39\n"
CV "v81,v82,v83\n"
MF "v[120:123],a[192:195],v[180:183],v[120:123]\n"
"s_add_u32 s87,s90,13312\n"
CV "v82,v84,v85\n"
CV "v83,v86,v87\n"
MF "v[120:123],a[152:155],v[176:179],v[120:123]\n"
MF "v[128:131],a[156:159],v[176:179],v[128:131]\n"
"buffer_store_dwordx4 v[80:83],v9,s[40:43],s87 offen sc1\n"
MF "v[128:131],a[196:199],v[180:183],v[128:131]\n"
MF "v[128:131],a[236:239],v[184:187],v[128:131]\n"
"s_waitcnt lgkmcnt(2)\n"
MF "v[132:135],a[236:239],a[244:247],v[132:135]\n"
"s_waitcnt lgkmcnt(0)\n"
TR "v[176:177],v6\n"
"v_add_f32 v88,v88,v200\n"
MF "v[132:135],a[196:199],a[240:243],v[132:135]\n"
TR "v[178:179],v6 offset:2048\n"
"v_add_f32 v89,v89,v201\n"
MF "v[132:135],a[156:159],v[188:191],v[132:135]\n"
"v_add_f32 v90,v90,v202\n"
"v_add_f32 v91,v91,v203\n"
MF "v[124:127],a[152:155],v[188:191],v[124:127]\n"
TR "v[180:181],v6 offset:4096\n"
"v_add_f32 v92,v92,v204\n"
MF "v[124:127],a[192:195],a[240:243],v[124:127]\n"
TR "v[182:183],v6 offset:6144\n"
"v_add_f32 v93,v93,v205\n"
MF "v[124:127],a[232:235],a[244:247],v[124:127]\n"
"v_add_f32 v94,v94,v206\n"
"v_add_f32 v95,v95,v207\n"
MF "v[116:119],a[228:231],a[244:247],v[116:119]\n"
TR "v[184:185],v6 offset:8192\n"
CV "v88,v88,v89\n"
MF "v[116:119],a[188:191],a[240:243],v[116:119]\n"
TR "v[186:187],v6 offset:10240\n"
CV "v89,v90,v91\n"
MF "v[116:119],a[148:151],v[188:191],v[116:119]\n"
"s_add_u32 s87,s90,14336\n"
CV "v90,v92,v93\n"
CV "v91,v94,v95\n"
MF "v[108:111],a[144:147],v[188:191],v[108:111]\n"
MF "v[108:111],a[184:187],a[240:243],v[108:111]\n"
MF "v[108:111],a[224:227],a[244:247],v[108:111]\n"
MF "v[100:103],a[220:223],a[244:247],v[100:103]\n"
"buffer_store_dwordx4 v[88:91],v9,s[40:43],s87 offen sc1\n"
MF "v[100:103],a[180:183],a[240:243],v[100:103]\n"
"s_mov_b32 s90,s91\n"
"s_add_u32 s91,s91,20480\n"
MF "v[100:103],a[140:143],v[188:191],v[100:103]\n"
"s_waitcnt lgkmcnt(0)\n"
MF "v[136:139],a[140:143],v[176:179],v[136:139]\n"
TR "v[188:189],v7\n"
MF "v[136:139],a[180:183],v[180:183],v[136:139]\n"
TR "v[190:191],v7 offset:2048\n"
MF "v[136:139],a[220:223],v[184:187],v[136:139]\n"
MF "v[144:147],a[224:227],v[184:187],v[144:147]\n"
TR "a[240:241],v7 offset:4096\n"
MF "v[144:147],a[184:187],v[180:183],v[144:147]\n"
TR "a[242:243],v7 offset:6144\n"
MF "v[144:147],a[144:147],v[176:179],v[144:147]\n"
MF "v[152:155],a[148:151],v[176:179],v[152:155]\n"
TR "a[244:245],v7 offset:8192\n"
MF "v[152:155],a[188:191],v[180:183],v[152:155]\n"
TR "a[246:247],v7 offset:10240\n"
MF "v[152:155],a[228:231],v[184:187],v[152:155]\n"
MF "v[160:163],a[232:235],v[184:187],v[160:163]\n"
MF "v[160:163],a[192:195],v[180:183],v[160:163]\n"
MF "v[160:163],a[152:155],v[176:179],v[160:163]\n"
MF "v[168:171],a[156:159],v[176:179],v[168:171]\n"
MF "v[168:171],a[196:199],v[180:183],v[168:171]\n"
"s_waitcnt lgkmcnt(0)\n"
MF "v[168:171],a[236:239],v[184:187],v[168:171]\n"
"s_barrier\n"
MF "v[172:175],a[236:239],a[244:247],v[172:175]\n"
TR "v[176:177],v0 offset:24576\n"
MF "v[172:175],a[196:199],a[240:243],v[172:175]\n"
"s_add_u32 s85,s85,1\n"
TR "v[178:179],v0 offset:26624\n"
MF "v[172:175],a[156:159],v[188:191],v[172:175]\n"
MF "v[164:167],a[152:155],v[188:191],v[164:167]\n"
TR "v[180:181],v0 offset:28672\n"
MF "v[164:167],a[192:195],a[240:243],v[164:167]\n"
TR "v[182:183],v0 offset:30720\n"
MF "v[164:167],a[232:235],a[244:247],v[164:167]\n"
MF "v[156:159],a[228:231],a[244:247],v[156:159]\n"
TR "v[184:185],v0 offset:32768\n"
MF "v[156:159],a[188:191],a[240:243],v[156:159]\n"
TR "v[186:187],v0 offset:34816\n"
MF "v[156:159],a[148:151],v[188:191],v[156:159]\n"
MF "v[148:151],a[144:147],v[188:191],v[148:151]\n"
MF "v[148:151],a[184:187],a[240:243],v[148:151]\n"
MF "v[148:151],a[224:227],a[244:247],v[148:151]\n"
MF "v[140:143],a[220:223],a[244:247],v[140:143]\n"
MF "v[140:143],a[180:183],a[240:243],v[140:143]\n"
MF "v[140:143],a[140:143],v[188:191],v[140:143]\n"
"s_waitcnt lgkmcnt(0)\n"
MF "v[16:19],a[0:3],v[176:179],v[252:255]\n"
TR "v[188:189],v1 offset:24576\n"
MF "v[16:19],a[40:43],v[180:183],v[16:19]\n"
TR "v[190:191],v1 offset:26624\n"
MF "v[16:19],a[80:83],v[184:187],v[16:19]\n"
MF "v[24:27],a[84:87],v[184:187],v[252:255]\n"
TR "a[240:241],v1 offset:28672\n"
MF "v[24:27],a[44:47],v[180:183],v[24:27]\n"
TR "a[242:243],v1 offset:30720\n"
MF "v[24:27],a[4:7],v[176:179],v[24:27]\n"
MF "v[32:35],a[8:11],v[176:179],v[252:255]\n"
TR "a[244:245],v1 offset:32768\n"
MF "v[32:35],a[48:51],v[180:183],v[32:35]\n"
TR "a[246:247],v1 offset:34816\n"
MF "v[32:35],a[88:91],v[184:187],v[32:35]\n"
MF "v[40:43],a[92:95],v[184:187],v[252:255]\n"
MF "v[40:43],a[52:55],v[180:183],v[40:43]\n"
MF "v[40:43],a[12:15],v[176:179],v[40:43]\n"
MF "v[48:51],a[16:19],v[176:179],v[252:255]\n"
MF "v[48:51],a[56:59],v[180:183],v[48:51]\n"
MF "v[48:51],a[96:99],v[184:187],v[48:51]\n"
"s_waitcnt lgkmcnt(0)\n"
MF "v[52:55],a[96:99],a[244:247],v[252:255]\n"
TR "v[176:177],v2 offset:24576\n"
MF "v[52:55],a[56:59],a[240:243],v[52:55]\n"
TR "v[178:179],v2 offset:26624\n"
MF "v[52:55],a[16:19],v[188:191],v[52:55]\n"
MF "v[44:47],a[12:15],v[188:191],v[252:255]\n"
TR "v[180:181],v2 offset:28672\n"
MF "v[44:47],a[52:55],a[240:243],v[44:47]\n"
TR "v[182:183],v2 offset:30720\n"
MF "v[44:47],a[92:95],a[244:247],v[44:47]\n"
MF "v[36:39],a[88:91],a[244:247],v[252:255]\n"
TR "v[184:185],v2 offset:32768\n"
MF "v[36:39],a[48:51],a[240:243],v[36:39]\n"
TR "v[186:187],v2 offset:34816\n"
MF "v[36:39],a[8:11],v[188:191],v[36:39]\n"
MF "v[28:31],a[4:7],v[188:191],v[252:255]\n"
MF "v[28:31],a[44:47],a[240:243],v[28:31]\n"
MF "v[28:31],a[84:87],a[244:247],v[28:31]\n"
MF "v[20:23],a[80:83],a[244:247],v[252:255]\n"
MF "v[20:23],a[40:43],a[240:243],v[20:23]\n"
MF "v[20:23],a[0:3],v[188:191],v[20:23]\n"
"s_waitcnt lgkmcnt(0)\n"
MF "v[56:59],a[0:3],v[176:179],v[252:255]\n"
TR "v[188:189],v3 offset:24576\n"
MF "v[56:59],a[40:43],v[180:183],v[56:59]\n"
TR "v[190:191],v3 offset:26624\n"
MF "v[56:59],a[80:83],v[184:187],v[56:59]\n"
MF "v[64:67],a[84:87],v[184:187],v[252:255]\n"
TR "a[240:241],v3 offset:28672\n"
MF "v[64:67],a[44:47],v[180:183],v[64:67]\n"
TR "a[242:243],v3 offset:30720\n"
MF "v[64:67],a[4:7],v[176:179],v[64:67]\n"
MF "v[72:75],a[8:11],v[176:179],v[252:255]\n"
TR "a[244:245],v3 offset:32768\n"
MF "v[72:75],a[48:51],v[180:183],v[72:75]\n"
TR "a[246:247],v3 offset:34816\n"
MF "v[72:75],a[88:91],v[184:187],v[72:75]\n"
MF "v[80:83],a[92:95],v[184:187],v[252:255]\n"
MF "v[80:83],a[52:55],v[180:183],v[80:83]\n"
MF "v[80:83],a[12:15],v[176:179],v[80:83]\n"
MF "v[88:91],a[16:19],v[176:179],v[252:255]\n"
MF "v[88:91],a[56:59],v[180:183],v[88:91]\n"
"s_waitcnt lgkmcnt(0)\n"
MF "v[88:91],a[96:99],v[184:187],v[88:91]\n"
"s_barrier\n"
MF "v[92:95],a[96:99],a[244:247],v[252:255]\n"
TR "v[176:177],v0\n"
MF "v[92:95],a[56:59],a[240:243],v[92:95]\n"
TR "v[178:179],v0 offset:2048\n"
MF "v[92:95],a[16:19],v[188:191],v[92:95]\n"
"ds_read2st64_b32 v[192:193],v13 offset0:192 offset1:193\n"
MF "v[84:87],a[12:15],v[188:191],v[252:255]\n"
TR "v[180:181],v0 offset:4096\n"
MF "v[84:87],a[52:55],a[240:243],v[84:87]\n"
TR "v[182:183],v0 offset:6144\n"
MF "v[84:87],a[92:95],a[244:247],v[84:87]\n"
"ds_read2st64_b32 v[194:195],v13 offset0:194 offset1:195\n"
MF "v[76:79],a[88:91],a[244:247],v[252:255]\n"
TR "v[184:185],v0 offset:8192\n"
MF "v[76:79],a[48:51],a[240:243],v[76:79]\n"
TR "v[186:187],v0 offset:10240\n"
MF "v[76:79],a[8:11],v[188:191],v[76:79]\n"
"ds_read2st64_b32 v[196:197],v13 offset0:196 offset1:197\n"
MF "v[68:71],a[4:7],v[188:191],v[252:255]\n"
"ds_read2st64_b32 v[198:199],v13 offset0:198 offset1:199\n"
MF "v[68:71],a[44:47],a[240:243],v[68:71]\n"
MF "v[68:71],a[84:87],a[244:247],v[68:71]\n"
MF "v[60:63],a[80:83],a[244:247],v[252:255]\n"
MF "v[60:63],a[40:43],a[240:243],v[60:63]\n"
MF "v[60:63],a[0:3],v[188:191],v[60:63]\n"
"s_waitcnt lgkmcnt(2)\n"
MF "v[16:19],a[120:123],v[176:179],v[16:19]\n"
"s_waitcnt lgkmcnt(0)\n"
TR "v[188:189],v1\n"
"v_add_f32 v96,v96,v192\n"
MF "v[16:19],a[160:163],v[180:183],v[16:19]\n"
TR "v[190:191],v1 offset:2048\n"
"v_add_f32 v97,v97,v193\n"
MF "v[16:19],a[200:203],v[184:187],v[16:19]\n"
"ds_read2st64_b32 v[200:201],v13 offset0:200 offset1:201\n"
"v_add_f32 v98,v98,v194\n"
"s_waitcnt vmcnt(0)\n"
"v_mov_b32 v14,s76\n"
"s_add_u32 s76,s76,1\n"
MF "v[24:27],a[204:207],v[184:187],v[24:27]\n"
TR "a[240:241],v1 offset:4096\n"
"v_add_f32 v99,v99,v195\n"
"buffer_store_dword v14,v15,s[40:43],s94 offen sc1\n"
MF "v[24:27],a[164:167],v[180:183],v[24:27]\n"
TR "a[242:243],v1 offset:6144\n"
"v_add_f32 v100,v100,v196\n"
MF "v[24:27],a[124:127],v[176:179],v[24:27]\n"
"ds_read2st64_b32 v[202:203],v13 offset0:202 offset1:203\n"
"v_add_f32 v101,v101,v197\n"
MF "v[32:35],a[128:131],v[176:179],v[32:35]\n"
TR "a[244:245],v1 offset:8192\n"
"v_add_f32 v102,v102,v198\n"
MF "v[32:35],a[168:171],v[180:183],v[32:35]\n"
TR "a[246:247],v1 offset:10240\n"
"v_add_f32 v103,v103,v199\n"
MF "v[32:35],a[208:211],v[184:187],v[32:35]\n"
"ds_read2st64_b32 v[204:205],v13 offset0:204 offset1:205\n"
CV "v96,v96,v97\n"
MF "v[40:43],a[212:215],v[184:187],v[40:43]\n"
"ds_read2st64_b32 v[206:207],v13 offset0:206 offset1:207\n"
CV "v97,v98,v99\n"
MF "v[40:43],a[172:175],v[180:183],v[40:43]\n"
"s_add_u32 s87,s90,0\n"
CV "v98,v100,v101\n"
CV "v99,v102,v103\n"
MF "v[40:43],a[132:135],v[176:179],v[40:43]\n"
MF "v[48:51],a[136:139],v[176:179],v[48:51]\n"
"buffer_store_dwordx4 v[96:99],v9,s[40:43],s87 offen sc1\n"
MF "v[48:51],a[176:179],v[180:183],v[48:51]\n"
MF "v[48:51],a[216:219],v[184:187],v[48:51]\n"
"s_waitcnt lgkmcnt(2)\n"
MF "v[52:55],a[216:219],a[244:247],v[52:55]\n"
"s_waitcnt lgkmcnt(0)\n"
TR "v[176:177],v2\n"
"v_add_f32 v104,v104,v200\n"
MF "v[52:55],a[176:179],a[240:243],v[52:55]\n"
TR "v[178:179],v2 offset:2048\n"
"v_add_f32 v105,v105,v201\n"
MF "v[52:55],a[136:139],v[188:191],v[52:55]\n"
"ds_read2st64_b32 v[192:193],v13 offset0:208 offset1:209\n"
"v_add_f32 v106,v106,v202\n"
MF "v[44:47],a[132:135],v[188:191],v[44:47]\n"
TR "v[180:181],v2 offset:4096\n"
"v_add_f32 v107,v107,v203\n"
MF "v[44:47],a[172:175],a[240:243],v[44:47]\n"
TR "v[182:183],v2 offset:6144\n"
"v_add_f32 v108,v108,v204\n"
MF "v[44:47],a[212:215],a[244:247],v[44:47]\n"
"ds_read2st64_b32 v[194:195],v13 offset0:210 offset1:211\n"
"v_add_f32 v109,v109,v205\n"
MF "v[36:39],a[208:211],a[244:247],v[36:39]\n"
TR "v[184:185],v2 offset:8192\n"
"v_add_f32 v110,v110,v206\n"
MF "v[36:39],a[168:171],a[240:243],v[36:39]\n"
TR "v[186:187],v2 offset:10240\n"
"v_add_f32 v111,v111,v207\n"
MF "v[36:39],a[128:131],v[188:191],v[36:39]\n"
"ds_read2st64_b32 v[196:197],v13 offset0:212 offset1:213\n"
CV "v104,v104,v105\n"
MF "v[28:31],a[124:127],v[188:191],v[28:31]\n"
"ds_read2st64_b32 v[198:199],v13 offset0:214 offset1:215\n"
CV "v105,v106,v107\n"
MF "v[28:31],a[164:167],a[240:243],v[28:31]\n"
"s_add_u32 s87,s90,1024\n"
CV "v106,v108,v109\n"
CV "v107,v110,v111\n"
MF "v[28:31],a[204:207],a[244:247],v[28:31]\n"
MF "v[20:23],a[200:203],a[244:247],v[20:23]\n"
"buffer_store_dwordx4 v[104:107],v9,s[40:43],s87 offen sc1\n"
MF "v[20:23],a[160:163],a[240:243],v[20:23]\n"
MF "v[20:23],a[120:123],v[188:191],v[20:23]\n"
"s_waitcnt lgkmcnt(2)\n"
MF "v[56:59],a[120:123],v[176:179],v[56:59]\n"
"s_waitcnt lgkmcnt(0)\n"
TR "v[188:189],v3\n"
"v_add_f32 v112,v112,v192\n"
MF "v[56:59],a[160:163],v[180:183],v[56:59]\n"
TR "v[190:191],v3 offset:2048\n"
"v_add_f32 v113,v113,v193\n"
MF "v[56:59],a[200:203],v[184:187],v[56:59]\n"
"ds_read2st64_b32 v[200:201],v13 offset0:216 offset1:217\n"
"v_add_f32 v114,v114,v194\n"
MF "v[64:67],a[204:207],v[184:187],v[64:67]\n"
TR "a[240:241],v3 offset:4096\n"
"v_add_f32 v115,v115,v195\n"
MF "v[64:67],a[164:167],v[180:183],v[64:67]\n"
TR "a[242:243],v3 offset:6144\n"
"v_add_f32 v116,v116,v196\n"
MF "v[64:67],a[124:127],v[176:179],v[64:67]\n"
"ds_read2st64_b32 v[202:203],v13 offset0:218 offset1:219\n"
"v_add_f32 v117,v117,v197\n"
MF "v[72:75],a[128:131],v[176:179],v[72:75]\n"
".Lpg34h:\n"
"s_cbranch_execz .Lpg38h\n"
TR "a[244:245],v3 offset:8192\n"
"v_add_f32 v118,v118,v198\n"
MF "v[72:75],a[168:171],v[180:183],v[72:75]\n"
TR "a[246:247],v3 offset:10240\n"
"v_add_f32 v119,v119,v199\n"
MF "v[72:75],a[208:211],v[184:187],v[72:75]\n"
"ds_read2st64_b32 v[204:205],v13 offset0:220 offset1:221\n"
CV "v112,v112,v113\n"
MF "v[80:83],a[212:215],v[184:187],v[80:83]\n"
"ds_read2st64_b32 v[206:207],v13 offset0:222 offset1:223\n"
CV "v113,v114,v115\n"
MF "v[80:83],a[172:175],v[180:183],v[80:83]\n"
"s_add_u32 s87,s90,2048\n"
CV "v114,v116,v117\n"
CV "v115,v118,v119\n"
MF "v[80:83],a[132:135],v[176:179],v[80:83]\n"
MF "v[88:91],a[136:139],v[176:179],v[88:91]\n"
"buffer_store_dwordx4 v[112:115],v9,s[40:43],s87 offen sc1\n"
MF "v[88:91],a[176:179],v[180:183],v[88:91]\n"
"s_waitcnt lgkmcnt(0)\n"
MF "v[88:91],a[216:219],v[184:187],v[88:91]\n"
"s_barrier\n"
MF "v[92:95],a[216:219],a[244:247],v[92:95]\n"
TR "v[176:177],v0 offset:49152\n"
"v_add_f32 v120,v120,v200\n"
MF "v[92:95],a[176:179],a[240:243],v[92:95]\n"
TR "v[178:179],v0 offset:51200\n"
"v_add_f32 v121,v121,v201\n"
MF "v[92:95],a[136:139],v[188:191],v[92:95]\n"
"ds_read2st64_b32 v[192:193],v13 offset0:224 offset1:225\n"
"v_add_f32 v122,v122,v202\n"
MF "v[84:87],a[132:135],v[188:191],v[84:87]\n"
TR "v[180:181],v0 offset:53248\n"
"v_add_f32 v123,v123,v203\n"
MF "v[84:87],a[172:175],a[240:243],v[84:87]\n"
TR "v[182:183],v0 offset:55296\n"
"v_add_f32 v124,v124,v204\n"
MF "v[84:87],a[212:215],a[244:247],v[84:87]\n"
"ds_read2st64_b32 v[194:195],v13 offset0:226 offset1:227\n"
"v_add_f32 v125,v125,v205\n"
MF "v[76:79],a[208:211],a[244:247],v[76:79]\n"
TR "v[184:185],v0 offset:57344\n"
"v_add_f32 v126,v126,v206\n"
MF "v[76:79],a[168:171],a[240:243],v[76:79]\n"
TR "v[186:187],v0 offset:59392\n"
"v_add_f32 v127,v127,v207\n"
MF "v[76:79],a[128:131],v[188:191],v[76:79]\n"
"ds_read2st64_b32 v[196:197],v13 offset0:228 offset1:229\n"
CV "v120,v120,v121\n"
MF "v[68:71],a[124:127],v[188:191],v[68:71]\n"
"ds_read2st64_b32 v[198:199],v13 offset0:230 offset1:231\n"
CV "v121,v122,v123\n"
MF "v[68:71],a[164:167],a[240:243],v[68:71]\n"
"s_add_u32 s87,s90,3072\n"
CV "v122,v124,v125\n"
CV "v123,v126,v127\n"
MF "v[68:71],a[204:207],a[244:247],v[68:71]\n"
MF "v[60:63],a[200:203],a[244:247],v[60:63]\n"
"buffer_store_dwordx4 v[120:123],v9,s[40:43],s87 offen sc1\n"
MF "v[60:63],a[160:163],a[240:243],v[60:63]\n"
MF "v[60:63],a[120:123],v[188:191],v[60:63]\n"
"s_waitcnt lgkmcnt(2)\n"
MF "v[16:19],a[20:23],v[176:179],v[16:19]\n"
"s_waitcnt lgkmcnt(0)\n"
TR "v[188:189],v1 offset:49152\n"
"v_add_f32 v128,v128,v192\n"
MF "v[16:19],a[60:63],v[180:183],v[16:19]\n"
TR "v[190:191],v1 offset:51200\n"
"v_add_f32 v129,v129,v193\n"
MF "v[16:19],a[100:103],v[184:187],v[16:19]\n"
"ds_read2st64_b32 v[200:201],v13 offset0:0 offset1:1\n"
"v_add_f32 v130,v130,v194\n"
MF "v[24:27],a[104:107],v[184:187],v[24:27]\n"
TR "a[240:241],v1 offset:53248\n"
"v_add_f32 v131,v131,v195\n"
MF "v[24:27],a[64:67],v[180:183],v[24:27]\n"
TR "a[242:243],v1 offset:55296\n"
"v_add_f32 v132,v132,v196\n"
MF "v[24:27],a[24:27],v[176:179],v[24:27]\n"
"ds_read2st64_b32 v[202:203],v13 offset0:2 offset1:3\n"
"v_add_f32 v133,v133,v197\n"
MF "v[32:35],a[28:31],v[176:179],v[32:35]\n"
TR "a[244:245],v1 offset:57344\n"
"v_add_f32 v134,v134,v198\n"
MF "v[32:35],a[68:71],v[180:183],v[32:35]\n"
TR "a[246:247],v1 offset:59392\n"
"v_add_f32 v135,v135,v199\n"
MF "v[32:35],a[108:111],v[184:187],v[32:35]\n"
"ds_read2st64_b32 v[204:205],v13 offset0:4 offset1:5\n"
CV "v128,v128,v129\n"
MF "v[40:43],a[112:115],v[184:187],v[40:43]\n"
"ds_read2st64_b32 v[206:207],v13 offset0:6 offset1:7\n"
CV "v129,v130,v131\n"
MF "v[40:43],a[72:75],v[180:183],v[40:43]\n"
"s_add_u32 s87,s90,4096\n"
CV "v130,v132,v133\n"
CV "v131,v134,v135\n"
MF "v[40:43],a[32:35],v[176:179],v[40:43]\n"
MF "v[48:51],a[36:39],v[176:179],v[48:51]\n"
"buffer_store_dwordx4 v[128:131],v9,s[40:43],s87 offen sc1\n"
MF "v[48:51],a[76:79],v[180:183],v[48:51]\n"
MF "v[48:51],a[116:119],v[184:187],v[48:51]\n"
"s_waitcnt lgkmcnt(2)\n"
MF "v[52:55],a[116:119],a[244:247],v[52:55]\n"
"s_waitcnt lgkmcnt(0)\n"
TR "v[176:177],v2 offset:49152\n"
"v_add_f32 v136,v136,v200\n"
MF "v[52:55],a[76:79],a[240:243],v[52:55]\n"
TR "v[178:179],v2 offset:51200\n"
"v_add_f32 v137,v137,v201\n"
MF "v[52:55],a[36:39],v[188:191],v[52:55]\n"
"ds_read2st64_b32 v[192:193],v13 offset0:8 offset1:9\n"
"v_add_f32 v138,v138,v202\n"
MF "v[44:47],a[32:35],v[188:191],v[44:47]\n"
TR "v[180:181],v2 offset:53248\n"
"v_add_f32 v139,v139,v203\n"
MF "v[44:47],a[72:75],a[240:243],v[44:47]\n"
TR "v[182:183],v2 offset:55296\n"
"v_add_f32 v140,v140,v204\n"
MF "v[44:47],a[112:115],a[244:247],v[44:47]\n"
"ds_read2st64_b32 v[194:195],v13 offset0:10 offset1:11\n"
"v_add_f32 v141,v141,v205\n"
MF "v[36:39],a[108:111],a[244:247],v[36:39]\n"
TR "v[184:185],v2 offset:57344\n"
"v_add_f32 v142,v142,v206\n"
MF "v[36:39],a[68:71],a[240:243],v[36:39]\n"
TR "v[186:187],v2 offset:59392\n"
"v_add_f32 v143,v143,v207\n"
MF "v[36:39],a[28:31],v[188:191],v[36:39]\n"
"ds_read2st64_b32 v[196:197],v13 offset0:12 offset1:13\n"
CV "v136,v136,v137\n"
MF "v[28:31],a[24:27],v[188:191],v[28:31]\n"
"ds_read2st64_b32 v[198:199],v13 offset0:14 offset1:15\n"
CV "v137,v138,v139\n"
MF "v[28:31],a[64:67],a[240:243],v[28:31]\n"
"s_add_u32 s87,s90,10240\n"
CV "v138,v140,v141\n"
CV "v139,v142,v143\n"
MF "v[28:31],a[104:107],a[244:247],v[28:31]\n"
MF "v[20:23],a[100:103],a[244:247],v[20:23]\n"
"buffer_store_dwordx4 v[136:139],v9,s[40:43],s87 offen sc1\n"
MF "v[20:23],a[60:63],a[240:243],v[20:23]\n"
MF "v[20:23],a[20:23],v[188:191],v[20:23]\n"
"s_waitcnt lgkmcnt(2)\n"
MF "v[56:59],a[20:23],v[176:179],v[56:59]\n"
"s_waitcnt lgkmcnt(0)\n"
TR "v[188:189],v3 offset:49152\n"
"v_add_f32 v144,v144,v192\n"
MF "v[56:59],a[60:63],v[180:183],v[56:59]\n"
TR "v[190:191],v3 offset:51200\n"
"v_add_f32 v145,v145,v193\n"
MF "v[56:59],a[100:103],v[184:187],v[56:59]\n"
"ds_read2st64_b32 v[200:201],v13 offset0:16 offset1:17\n"
"v_add_f32 v146,v146,v194\n"
MF "v[64:67],a[104:107],v[184:187],v[64:67]\n"
TR "a[240:241],v3 offset:53248\n"
"v_add_f32 v147,v147,v195\n"
MF "v[64:67],a[64:67],v[180:183],v[64:67]\n"
TR "a[242:243],v3 offset:55296\n"
"v_add_f32 v148,v148,v196\n"
MF "v[64:67],a[24:27],v[176:179],v[64:67]\n"
"ds_read2st64_b32 v[202:203],v13 offset0:18 offset1:19\n"
"v_add_f32 v149,v149,v197\n"
MF "v[72:75],a[28:31],v[176:179],v[72:75]\n"
TR "a[244:245],v3 offset:57344\n"
"v_add_f32 v150,v150,v198\n"
MF "v[72:75],a[68:71],v[180:183],v[72:75]\n"
TR "a[246:247],v3 offset:59392\n"
"v_add_f32 v151,v151,v199\n"
MF "v[72:75],a[108:111],v[184:187],v[72:75]\n"
"ds_read2st64_b32 v[204:205],v13 offset0:20 offset1:21\n"
CV "v144,v144,v145\n"
MF "v[80:83],a[112:115],v[184:187],v[80:83]\n"
"ds_read2st64_b32 v[206:207],v13 offset0:22 offset1:23\n"
CV "v145,v146,v147\n"
MF "v[80:83],a[72:75],v[180:183],v[80:83]\n"
"s_add_u32 s87,s90,11264\n"
CV "v146,v148,v149\n"
CV "v147,v150,v151\n"
MF "v[80:83],a[32:35],v[176:179],v[80:83]\n"
MF "v[88:91],a[36:39],v[176:179],v[88:91]\n"
"buffer_store_dwordx4 v[144:147],v9,s[40:43],s87 offen sc1\n"
MF "v[88:91],a[76:79],v[180:183],v[88:91]\n"
"s_waitcnt lgkmcnt(0)\n"
MF "v[88:91],a[116:119],v[184:187],v[88:91]\n"
"s_barrier\n"
MF "v[92:95],a[116:119],a[244:247],v[92:95]\n"
TR "v[176:177],v4\n"
"v_add_f32 v152,v152,v200\n"
MF "v[92:95],a[76:79],a[240:243],v[92:95]\n"
TR "v[178:179],v4 offset:2048\n"
"v_add_f32 v153,v153,v201\n"
MF "v[92:95],a[36:39],v[188:191],v[92:95]\n"
"ds_read2st64_b32 v[192:193],v13 offset0:24 offset1:25\n"
"v_add_f32 v154,v154,v202\n"
MF "v[84:87],a[32:35],v[188:191],v[84:87]\n"
TR "v[180:181],v4 offset:4096\n"
"v_add_f32 v155,v155,v203\n"
MF "v[84:87],a[72:75],a[240:243],v[84:87]\n"
TR "v[182:183],v4 offset:6144\n"
"v_add_f32 v156,v156,v204\n"
MF "v[84:87],a[112:115],a[244:247],v[84:87]\n"
"ds_read2st64_b32 v[194:195],v13 offset0:26 offset1:27\n"
"v_add_f32 v157,v157,v205\n"
MF "v[76:79],a[108:111],a[244:247],v[76:79]\n"
TR "v[184:185],v4 offset:8192\n"
"v_add_f32 v158,v158,v206\n"
MF "v[76:79],a[68:71],a[240:243],v[76:79]\n"
TR "v[186:187],v4 offset:10240\n"
"v_add_f32 v159,v159,v207\n"
MF "v[76:79],a[28:31],v[188:191],v[76:79]\n"
"ds_read2st64_b32 v[196:197],v13 offset0:28 offset1:29\n"
CV "v152,v152,v153\n"
MF "v[68:71],a[24:27],v[188:191],v[68:71]\n"
"ds_read2st64_b32 v[198:199],v13 offset0:30 offset1:31\n"
CV "v153,v154,v155\n"
MF "v[68:71],a[64:67],a[240:243],v[68:71]\n"
"s_add_u32 s87,s90,12288\n"
CV "v154,v156,v157\n"
CV "v155,v158,v159\n"
MF "v[68:71],a[104:107],a[244:247],v[68:71]\n"
MF "v[60:63],a[100:103],a[244:247],v[60:63]\n"
"buffer_store_dwordx4 v[152:155],v9,s[40:43],s87 offen sc1\n"
MF "v[60:63],a[60:63],a[240:243],v[60:63]\n"
MF "v[60:63],a[20:23],v[188:191],v[60:63]\n"
"s_waitcnt lgkmcnt(2)\n"
MF "v[16:19],a[140:143],v[176:179],v[16:19]\n"
"s_waitcnt lgkmcnt(0)\n"
TR "v[188:189],v5\n"
"v_add_f32 v160,v160,v192\n"
MF "v[16:19],a[180:183],v[180:183],v[16:19]\n"
TR "v[190:191],v5 offset:2048\n"
"v_add_f32 v161,v161,v193\n"
MF "v[16:19],a[220:223],v[184:187],v[16:19]\n"
"ds_read2st64_b32 v[200:201],v13 offset0:32 offset1:33\n"
"v_add_f32 v162,v162,v194\n"
MF "v[24:27],a[224:227],v[184:187],v[24:27]\n"
TR "a[240:241],v5 offset:4096\n"
"v_add_f32 v163,v163,v195\n"
MF "v[24:27],a[184:187],v[180:183],v[24:27]\n"
TR "a[242:243],v5 offset:6144\n"
"v_add_f32 v164,v164,v196\n"
MF "v[24:27],a[144:147],v[176:179],v[24:27]\n"
"ds_read2st64_b32 v[202:203],v13 offset0:34 offset1:35\n"
"v_add_f32 v165,v165,v197\n"
MF "v[32:35],a[148:151],v[176:179],v[32:35]\n"
TR "a[244:245],v5 offset:8192\n"
"v_add_f32 v166,v166,v198\n"
MF "v[32:35],a[188:191],v[180:183],v[32:35]\n"
TR "a[246:247],v5 offset:10240\n"
"v_add_f32 v167,v167,v199\n"
MF "v[32:35],a[228:231],v[184:187],v[32:35]\n"
"ds_read2st64_b32 v[204:205],v13 offset0:36 offset1:37\n"
CV "v160,v160,v161\n"
MF "v[40:43],a[232:235],v[184:187],v[40:43]\n"
"ds_read2st64_b32 v[206:207],v13 offset0:38 offset1:39\n"
CV "v161,v162,v163\n"
MF "v[40:43],a[192:195],v[180:183],v[40:43]\n"
"s_add_u32 s87,s90,13312\n"
CV "v162,v164,v165\n"
CV "v163,v166,v167\n"
MF "v[40:43],a[152:155],v[176:179],v[40:43]\n"
MF "v[48:51],a[156:159],v[176:179],v[48:51]\n"
"buffer_store_dwordx4 v[160:163],v9,s[40:43],s87 offen sc1\n"
MF "v[48:51],a[196:199],v[180:183],v[48:51]\n"
MF "v[48:51],a[236:239],v[184:187],v[48:51]\n"
"s_waitcnt lgkmcnt(2)\n"
MF "v[52:55],a[236:239],a[244:247],v[52:55]\n"
"s_waitcnt lgkmcnt(0)\n"
TR "v[176:177],v6\n"
"v_add_f32 v168,v168,v200\n"
MF "v[52:55],a[196:199],a[240:243],v[52:55]\n"
TR "v[178:179],v6 offset:2048\n"
"v_add_f32 v169,v169,v201\n"
MF "v[52:55],a[156:159],v[188:191],v[52:55]\n"
"v_add_f32 v170,v170,v202\n"
"v_add_f32 v171,v171,v203\n"
MF "v[44:47],a[152:155],v[188:191],v[44:47]\n"
TR "v[180:181],v6 offset:4096\n"
"v_add_f32 v172,v172,v204\n"
MF "v[44:47],a[192:195],a[240:243],v[44:47]\n"
TR "v[182:183],v6 offset:6144\n"
"v_add_f32 v173,v173,v205\n"
MF "v[44:47],a[232:235],a[244:247],v[44:47]\n"
"v_add_f32 v174,v174,v206\n"
"v_add_f32 v175,v175,v207\n"
MF "v[36:39],a[228:231],a[244:247],v[36:39]\n"
TR "v[184:185],v6 offset:8192\n"
CV "v168,v168,v169\n"
MF "v[36:39],a[188:191],a[240:243],v[36:39]\n"
TR "v[186:187],v6 offset:10240\n"
CV "v169,v170,v171\n"
MF "v[36:39],a[148:151],v[188:191],v[36:39]\n"
"s_add_u32 s87,s90,14336\n"
CV "v170,v172,v173\n"
CV "v171,v174,v175\n"
MF "v[28:31],a[144:147],v[188:191],v[28:31]\n"
MF "v[28:31],a[184:187],a[240:243],v[28:31]\n"
MF "v[28:31],a[224:227],a[244:247],v[28:31]\n"
MF "v[20:23],a[220:223],a[244:247],v[20:23]\n"
"buffer_store_dwordx4 v[168:171],v9,s[40:43],s87 offen sc1\n"
MF "v[20:23],a[180:183],a[240:243],v[20:23]\n"
"s_mov_b32 s90,s91\n"
"s_add_u32 s91,s91,20480\n"
MF "v[20:23],a[140:143],v[188:191],v[20:23]\n"
"s_waitcnt lgkmcnt(0)\n"
MF "v[56:59],a[140:143],v[176:179],v[56:59]\n"
TR "v[188:189],v7\n"
MF "v[56:59],a[180:183],v[180:183],v[56:59]\n"
TR "v[190:191],v7 offset:2048\n"
MF "v[56:59],a[220:223],v[184:187],v[56:59]\n"
"v_mov_b32 v244,0\n"
"v_mov_b32 v245,0\n"
MF "v[64:67],a[224:227],v[184:187],v[64:67]\n"
TR "a[240:241],v7 offset:4096\n"
"v_mov_b32 v246,0\n"
MF "v[64:67],a[184:187],v[180:183],v[64:67]\n"
TR "a[242:243],v7 offset:6144\n"
"v_mov_b32 v247,0\n"
MF "v[64:67],a[144:147],v[176:179],v[64:67]\n"
"v_mov_b32 v248,0\n"
"v_mov_b32 v249,0\n"
MF "v[72:75],a[148:151],v[176:179],v[72:75]\n"
TR "a[244:245],v7 offset:8192\n"
"v_mov_b32 v250,0\n"
MF "v[72:75],a[188:191],v[180:183],v[72:75]\n"
TR "a[246:247],v7 offset:10240\n"
"v_mov_b32 v251,0\n"
MF "v[72:75],a[228:231],v[184:187],v[72:75]\n"
MF "v[80:83],a[232:235],v[184:187],v[80:83]\n"
MF "v[80:83],a[192:195],v[180:183],v[80:83]\n"
MF "v[80:83],a[152:155],v[176:179],v[80:83]\n"
MF "v[88:91],a[156:159],v[176:179],v[88:91]\n"
MF "v[88:91],a[196:199],v[180:183],v[88:91]\n"
"s_waitcnt lgkmcnt(0)\n"
MF "v[88:91],a[236:239],v[184:187],v[88:91]\n"
"s_barrier\n"
MF "v[92:95],a[236:239],a[244:247],v[92:95]\n"
TR "v[176:177],v0 offset:24576\n"
MF "v[92:95],a[196:199],a[240:243],v[92:95]\n"
"s_add_u32 s85,s85,1\n"
TR "v[178:179],v0 offset:26624\n"
MF "v[92:95],a[156:159],v[188:191],v[92:95]\n"
MF "v[84:87],a[152:155],v[188:191],v[84:87]\n"
TR "v[180:181],v0 offset:28672\n"
MF "v[84:87],a[192:195],a[240:243],v[84:87]\n"
TR "v[182:183],v0 offset:30720\n"
MF "v[84:87],a[232:235],a[244:247],v[84:87]\n"
MF "v[76:79],a[228:231],a[244:247],v[76:79]\n"
TR "v[184:185],v0 offset:32768\n"
MF "v[76:79],a[188:191],a[240:243],v[76:79]\n"
TR "v[186:187],v0 offset:34816\n"
MF "v[76:79],a[148:151],v[188:191],v[76:79]\n"
MF "v[68:71],a[144:147],v[188:191],v[68:71]\n"
MF "v[68:71],a[184:187],a[240:243],v[68:71]\n"
MF "v[68:71],a[224:227],a[244:247],v[68:71]\n"
MF "v[60:63],a[220:223],a[244:247],v[60:63]\n"
MF "v[60:63],a[180:183],a[240:243],v[60:63]\n"
MF "v[60:63],a[140:143],v[188:191],v[60:63]\n"
"s_waitcnt lgkmcnt(0)\n"
"s_cmp_lg_u32 s85,5\n"
"s_cbranch_scc1 .Lpl15\n"
".Lpl19:\n"
MF "v[96:99],a[0:3],v[176:179],v[252:255]\n"
TR "v[188:189],v1 offset:24576\n"
MF "v[96:99],a[40:43],v[180:183],v[96:99]\n"
"s_add_u32 s82,s82,4096\n"
TR "v[190:191],v1 offset:26624\n"
MF "v[96:99],a[80:83],v[184:187],v[96:99]\n"
LD "v[224:227],v9,s[40:43],s82 offen sc1\n"
MF "v[104:107],a[84:87],v[184:187],v[252:255]\n"
TR "a[240:241],v1 offset:28672\n"
MF "v[104:107],a[44:47],v[180:183],v[104:107]\n"
TR "a[242:243],v1 offset:30720\n"
MF "v[104:107],a[4:7],v[176:179],v[104:107]\n"
MF "v[112:115],a[8:11],v[176:179],v[252:255]\n"
TR "a[244:245],v1 offset:32768\n"
MF "v[112:115],a[48:51],v[180:183],v[112:115]\n"
TR "a[246:247],v1 offset:34816\n"
MF "v[112:115],a[88:91],v[184:187],v[112:115]\n"
MF "v[120:123],a[92:95],v[184:187],v[252:255]\n"
MF "v[120:123],a[52:55],v[180:183],v[120:123]\n"
MF "v[120:123],a[12:15],v[176:179],v[120:123]\n"
MF "v[128:131],a[16:19],v[176:179],v[252:255]\n"
MF "v[128:131],a[56:59],v[180:183],v[128:131]\n"
MF "v[128:131],a[96:99],v[184:187],v[128:131]\n"
"s_waitcnt lgkmcnt(0)\n"
MF "v[132:135],a[96:99],a[244:247],v[252:255]\n"
TR "v[176:177],v2 offset:24576\n"
MF "v[132:135],a[56:59],a[240:243],v[132:135]\n"
TR "v[178:179],v2 offset:26624\n"
MF "v[132:135],a[16:19],v[188:191],v[132:135]\n"
MF "v[124:127],a[12:15],v[188:191],v[252:255]\n"
TR "v[180:181],v2 offset:28672\n"
MF "v[124:127],a[52:55],a[240:243],v[124:127]\n"
TR "v[182:183],v2 offset:30720\n"
MF "v[124:127],a[92:95],a[244:247],v[124:127]\n"
MF "v[116:119],a[88:91],a[244:247],v[252:255]\n"
TR "v[184:185],v2 offset:32768\n"
MF "v[116:119],a[48:51],a[240:243],v[116:119]\n"
TR "v[186:187],v2 offset:34816\n"
MF "v[116:119],a[8:11],v[188:191],v[116:119]\n"
MF "v[108:111],a[4:7],v[188:191],v[252:255]\n"
MF "v[108:111],a[44:47],a[240:243],v[108:111]\n"
MF "v[108:111],a[84:87],a[244:247],v[108:111]\n"
MF "v[100:103],a[80:83],a[244:247],v[252:255]\n"
MF "v[100:103],a[40:43],a[240:243],v[100:103]\n"
MF "v[100:103],a[0:3],v[188:191],v[100:103]\n"
"s_waitcnt lgkmcnt(0)\n"
MF "v[136:139],a[0:3],v[176:179],v[252:255]\n"
TR "v[188:189],v3 offset:24576\n"
MF "v[136:139],a[40:43],v[180:183],v[136:139]\n"
TR "v[190:191],v3 offset:26624\n"
MF "v[136:139],a[80:83],v[184:187],v[136:139]\n"
MF "v[144:147],a[84:87],v[184:187],v[252:255]\n"
TR "a[240:241],v3 offset:28672\n"
MF "v[144:147],a[44:47],v[180:183],v[144:147]\n"
TR "a[242:243],v3 offset:30720\n"
MF "v[144:147],a[4:7],v[176:179],v[144:147]\n"
MF "v[152:155],a[8:11],v[176:179],v[252:255]\n"
TR "a[244:245],v3 offset:32768\n"
MF "v[152:155],a[48:51],v[180:183],v[152:155]\n"
TR "a[246:247],v3 offset:34816\n"
MF "v[152:155],a[88:91],v[184:187],v[152:155]\n"
MF "v[160:163],a[92:95],v[184:187],v[252:255]\n"
MF "v[160:163],a[52:55],v[180:183],v[160:163]\n"
MF "v[160:163],a[12:15],v[176:179],v[160:163]\n"
MF "v[168:171],a[16:19],v[176:179],v[252:255]\n"
MF "v[168:171],a[56:59],v[180:183],v[168:171]\n"
"s_waitcnt lgkmcnt(0)\n"
MF "v[168:171],a[96:99],v[184:187],v[168:171]\n"
"s_barrier\n"
MF "v[172:175],a[96:99],a[244:247],v[252:255]\n"
TR "v[176:177],v0\n"
MF "v[172:175],a[56:59],a[240:243],v[172:175]\n"
TR "v[178:179],v0 offset:2048\n"
MF "v[172:175],a[16:19],v[188:191],v[172:175]\n"
"ds_read2st64_b32 v[192:193],v13 offset0:192 offset1:193\n"
MF "v[164:167],a[12:15],v[188:191],v[252:255]\n"
TR "v[180:181],v0 offset:4096\n"
MF "v[164:167],a[52:55],a[240:243],v[164:167]\n"
TR "v[182:183],v0 offset:6144\n"
MF "v[164:167],a[92:95],a[244:247],v[164:167]\n"
"ds_read2st64_b32 v[194:195],v13 offset0:194 offset1:195\n"
MF "v[156:159],a[88:91],a[244:247],v[252:255]\n"
TR "v[184:185],v0 offset:8192\n"
MF "v[156:159],a[48:51],a[240:243],v[156:159]\n"
TR "v[186:187],v0 offset:10240\n"
MF "v[156:159],a[8:11],v[188:191],v[156:159]\n"
"ds_read2st64_b32 v[196:197],v13 offset0:196 offset1:197\n"
MF "v[148:151],a[4:7],v[188:191],v[252:255]\n"
"ds_read2st64_b32 v[198:199],v13 offset0:198 offset1:199\n"
MF "v[148:151],a[44:47],a[240:243],v[148:151]\n"
"buffer_load_dword v10,v12,s[40:43],s86 offen sc1\n"
MF "v[148:151],a[84:87],a[244:247],v[148:151]\n"
MF "v[140:143],a[80:83],a[244:247],v[252:255]\n"
MF "v[140:143],a[40:43],a[240:243],v[140:143]\n"
MF "v[140:143],a[0:3],v[188:191],v[140:143]\n"
"s_waitcnt lgkmcnt(2)\n"
MF "v[96:99],a[120:123],v[176:179],v[96:99]\n"
"s_waitcnt lgkmcnt(0)\n"
TR "v[188:189],v1\n"
"v_add_f32 v16,v16,v192\n"
MF "v[96:99],a[160:163],v[180:183],v[96:99]\n"
TR "v[190:191],v1 offset:2048\n"
"v_add_f32 v17,v17,v193\n"
MF "v[96:99],a[200:203],v[184:187],v[96:99]\n"
"ds_read2st64_b32 v[200:201],v13 offset0:200 offset1:201\n"
"v_add_f32 v18,v18,v194\n"
"s_waitcnt vmcnt(2)\n"
"v_mov_b32 v14,s76\n"
"s_add_u32 s76,s76,1\n"
MF "v[104:107],a[204:207],v[184:187],v[104:107]\n"
TR "a[240:241],v1 offset:4096\n"
"v_add_f32 v19,v19,v195\n"
"buffer_store_dword v14,v15,s[40:43],s94 offen sc1\n"
MF "v[104:107],a[164:167],v[180:183],v[104:107]\n"
TR "a[242:243],v1 offset:6144\n"
"v_add_f32 v20,v20,v196\n"
MF "v[104:107],a[124:127],v[176:179],v[104:107]\n"
"ds_read2st64_b32 v[202:203],v13 offset0:202 offset1:203\n"
"v_add_f32 v21,v21,v197\n"
MF "v[112:115],a[128:131],v[176:179],v[112:115]\n"
TR "a[244:245],v1 offset:8192\n"
"v_add_f32 v22,v22,v198\n"
MF "v[112:115],a[168:171],v[180:183],v[112:115]\n"
TR "a[246:247],v1 offset:10240\n"
"v_add_f32 v23,v23,v199\n"
MF "v[112:115],a[208:211],v[184:187],v[112:115]\n"
"ds_read2st64_b32 v[204:205],v13 offset0:204 offset1:205\n"
CV "v16,v16,v17\n"
MF "v[120:123],a[212:215],v[184:187],v[120:123]\n"
"ds_read2st64_b32 v[206:207],v13 offset0:206 offset1:207\n"
CV "v17,v18,v19\n"
MF "v[120:123],a[172:175],v[180:183],v[120:123]\n"
"s_add_u32 s87,s90,0\n"
CV "v18,v20,v21\n"
CV "v19,v22,v23\n"
MF "v[120:123],a[132:135],v[176:179],v[120:123]\n"
MF "v[128:131],a[136:139],v[176:179],v[128:131]\n"
"buffer_store_dwordx4 v[16:19],v9,s[40:43],s87 offen sc1\n"
MF "v[128:131],a[176:179],v[180:183],v[128:131]\n"
MF "v[128:131],a[216:219],v[184:187],v[128:131]\n"
"s_waitcnt lgkmcnt(2)\n"
MF "v[132:135],a[216:219],a[244:247],v[132:135]\n"
"s_waitcnt lgkmcnt(0)\n"
TR "v[176:177],v2\n"
"v_add_f32 v24,v24,v200\n"
MF "v[132:135],a[176:179],a[240:243],v[132:135]\n"
TR "v[178:179],v2 offset:2048\n"
"v_add_f32 v25,v25,v201\n"
MF "v[132:135],a[136:139],v[188:191],v[132:135]\n"
"ds_read2st64_b32 v[192:193],v13 offset0:208 offset1:209\n"
"v_add_f32 v26,v26,v202\n"
MF "v[124:127],a[132:135],v[188:191],v[124:127]\n"
TR "v[180:181],v2 offset:4096\n"
"v_add_f32 v27,v27,v203\n"
MF "v[124:127],a[172:175],a[240:243],v[124:127]\n"
TR "v[182:183],v2 offset:6144\n"
"v_add_f32 v28,v28,v204\n"
MF "v[124:127],a[212:215],a[244:247],v[124:127]\n"
"ds_read2st64_b32 v[194:195],v13 offset0:210 offset1:211\n"
"v_add_f32 v29,v29,v205\n"
MF "v[116:119],a[208:211],a[244:247],v[116:119]\n"
TR "v[184:185],v2 offset:8192\n"
"v_add_f32 v30,v30,v206\n"
MF "v[116:119],a[168:171],a[240:243],v[116:119]\n"
TR "v[186:187],v2 offset:10240\n"
"v_add_f32 v31,v31,v207\n"
MF "v[116:119],a[128:131],v[188:191],v[116:119]\n"
"ds_read2st64_b32 v[196:197],v13 offset0:212 offset1:213\n"
CV "v24,v24,v25\n"
MF "v[108:111],a[124:127],v[188:191],v[108:111]\n"
"ds_read2st64_b32 v[198:199],v13 offset0:214 offset1:215\n"
CV "v25,v26,v27\n"
MF "v[108:111],a[164:167],a[240:243],v[108:111]\n"
"s_add_u32 s87,s90,1024\n"
CV "v26,v28,v29\n"
CV "v27,v30,v31\n"
MF "v[108:111],a[204:207],a[244:247],v[108:111]\n"
MF "v[100:103],a[200:203],a[244:247],v[100:103]\n"
"buffer_store_dwordx4 v[24:27],v9,s[40:43],s87 offen sc1\n"
MF "v[100:103],a[160:163],a[240:243],v[100:103]\n"
MF "v[100:103],a[120:123],v[188:191],v[100:103]\n"
"s_waitcnt lgkmcnt(2)\n"
MF "v[136:139],a[120:123],v[176:179],v[136:139]\n"
"s_waitcnt lgkmcnt(0)\n"
TR "v[188:189],v3\n"
"v_add_f32 v32,v32,v192\n"
MF "v[136:139],a[160:163],v[180:183],v[136:139]\n"
TR "v[190:191],v3 offset:2048\n"
"v_add_f32 v33,v33,v193\n"
MF "v[136:139],a[200:203],v[184:187],v[136:139]\n"
"ds_read2st64_b32 v[200:201],v13 offset0:216 offset1:217\n"
"v_add_f32 v34,v34,v194\n"
MF "v[144:147],a[204:207],v[184:187],v[144:147]\n"
TR "a[240:241],v3 offset:4096\n"
"v_add_f32 v35,v35,v195\n"
MF "v[144:147],a[164:167],v[180:183],v[144:147]\n"
TR "a[242:243],v3 offset:6144\n"
"v_add_f32 v36,v36,v196\n"
MF "v[144:147],a[124:127],v[176:179],v[144:147]\n"
"ds_read2st64_b32 v[202:203],v13 offset0:218 offset1:219\n"
"v_add_f32 v37,v37,v197\n"
MF "v[152:155],a[128:131],v[176:179],v[152:155]\n"
TR "a[244:245],v3 offset:8192\n"
"v_add_f32 v38,v38,v198\n"
MF "v[152:155],a[168:171],v[180:183],v[152:155]\n"
TR "a[246:247],v3 offset:10240\n"
"v_add_f32 v39,v39,v199\n"
MF "v[152:155],a[208:211],v[184:187],v[152:155]\n"
"ds_read2st64_b32 v[204:205],v13 offset0:220 offset1:221\n"
CV "v32,v32,v33\n"
MF "v[160:163],a[212:215],v[184:187],v[160:163]\n"
"ds_read2st64_b32 v[206:207],v13 offset0:222 offset1:223\n"
CV "v33,v34,v35\n"
MF "v[160:163],a[172:175],v[180:183],v[160:163]\n"
".Lpg35h:\n"
"s_cbranch_execz .Lisr4\n"
"s_add_u32 s87,s90,2048\n"
CV "v34,v36,v37\n"
CV "v35,v38,v39\n"
MF "v[160:163],a[132:135],v[176:179],v[160:163]\n"
MF "v[168:171],a[136:139],v[176:179],v[168:171]\n"
"buffer_store_dwordx4 v[32:35],v9,s[40:43],s87 offen sc1\n"
MF "v[168:171],a[176:179],v[180:183],v[168:171]\n"
"s_waitcnt lgkmcnt(0)\n"
MF "v[168:171],a[216:219],v[184:187],v[168:171]\n"
"s_barrier\n"
MF "v[172:175],a[216:219],a[244:247],v[172:175]\n"
TR "v[176:177],v0 offset:49152\n"
"v_add_f32 v40,v40,v200\n"
MF "v[172:175],a[176:179],a[240:243],v[172:175]\n"
"s_mov_b32 s82,s70\n"
TR "v[178:179],v0 offset:51200\n"
"v_add_f32 v41,v41,v201\n"
MF "v[172:175],a[136:139],v[188:191],v[172:175]\n"
"ds_read2st64_b32 v[192:193],v13 offset0:224 offset1:225\n"
"v_add_f32 v42,v42,v202\n"
MF "v[164:167],a[132:135],v[188:191],v[164:167]\n"
TR "v[180:181],v0 offset:53248\n"
"v_add_f32 v43,v43,v203\n"
MF "v[164:167],a[172:175],a[240:243],v[164:167]\n"
TR "v[182:183],v0 offset:55296\n"
"v_add_f32 v44,v44,v204\n"
MF "v[164:167],a[212:215],a[244:247],v[164:167]\n"
"ds_read2st64_b32 v[194:195],v13 offset0:226 offset1:227\n"
"v_add_f32 v45,v45,v205\n"
MF "v[156:159],a[208:211],a[244:247],v[156:159]\n"
TR "v[184:185],v0 offset:57344\n"
"v_add_f32 v46,v46,v206\n"
MF "v[156:159],a[168:171],a[240:243],v[156:159]\n"
TR "v[186:187],v0 offset:59392\n"
"v_add_f32 v47,v47,v207\n"
MF "v[156:159],a[128:131],v[188:191],v[156:159]\n"
"ds_read2st64_b32 v[196:197],v13 offset0:228 offset1:229\n"
CV "v40,v40,v41\n"
MF "v[148:151],a[124:127],v[188:191],v[148:151]\n"
"ds_read2st64_b32 v[198:199],v13 offset0:230 offset1:231\n"
CV "v41,v42,v43\n"
MF "v[148:151],a[164:167],a[240:243],v[148:151]\n"
"s_add_u32 s87,s90,3072\n"
CV "v42,v44,v45\n"
CV "v43,v46,v47\n"
MF "v[148:151],a[204:207],a[244:247],v[148:151]\n"
MF "v[140:143],a[200:203],a[244:247],v[140:143]\n"
"buffer_store_dwordx4 v[40:43],v9,s[40:43],s87 offen sc1\n"
MF "v[140:143],a[160:163],a[240:243],v[140:143]\n"
MF "v[140:143],a[120:123],v[188:191],v[140:143]\n"
"s_waitcnt lgkmcnt(2)\n"
MF "v[96:99],a[20:23],v[176:179],v[96:99]\n"
"s_waitcnt lgkmcnt(0)\n"
TR "v[188:189],v1 offset:49152\n"
"v_add_f32 v48,v48,v192\n"
MF "v[96:99],a[60:63],v[180:183],v[96:99]\n"
TR "v[190:191],v1 offset:51200\n"
"v_add_f32 v49,v49,v193\n"
MF "v[96:99],a[100:103],v[184:187],v[96:99]\n"
"ds_read2st64_b32 v[200:201],v13 offset0:0 offset1:1\n"
"v_add_f32 v50,v50,v194\n"
MF "v[104:107],a[104:107],v[184:187],v[104:107]\n"
TR "a[240:241],v1 offset:53248\n"
"v_add_f32 v51,v51,v195\n"
MF "v[104:107],a[64:67],v[180:183],v[104:107]\n"
TR "a[242:243],v1 offset:55296\n"
"v_add_f32 v52,v52,v196\n"
MF "v[104:107],a[24:27],v[176:179],v[104:107]\n"
"ds_read2st64_b32 v[202:203],v13 offset0:2 offset1:3\n"
"v_add_f32 v53,v53,v197\n"
MF "v[112:115],a[28:31],v[176:179],v[112:115]\n"
TR "a[244:245],v1 offset:57344\n"
"v_add_f32 v54,v54,v198\n"
MF "v[112:115],a[68:71],v[180:183],v[112:115]\n"
TR "a[246:247],v1 offset:59392\n"
"v_add_f32 v55,v55,v199\n"
MF "v[112:115],a[108:111],v[184:187],v[112:115]\n"
"ds_read2st64_b32 v[204:205],v13 offset0:4 offset1:5\n"
CV "v48,v48,v49\n"
MF "v[120:123],a[112:115],v[184:187],v[120:123]\n"
"ds_read2st64_b32 v[206:207],v13 offset0:6 offset1:7\n"
CV "v49,v50,v51\n"
MF "v[120:123],a[72:75],v[180:183],v[120:123]\n"
"s_add_u32 s87,s90,4096\n"
CV "v50,v52,v53\n"
CV "v51,v54,v55\n"
MF "v[120:123],a[32:35],v[176:179],v[120:123]\n"
MF "v[128:131],a[36:39],v[176:179],v[128:131]\n"
"buffer_store_dwordx4 v[48:51],v9,s[40:43],s87 offen sc1\n"
MF "v[128:131],a[76:79],v[180:183],v[128:131]\n"
MF "v[128:131],a[116:119],v[184:187],v[128:131]\n"
"s_waitcnt lgkmcnt(2)\n"
MF "v[132:135],a[116:119],a[244:247],v[132:135]\n"
"s_waitcnt lgkmcnt(0)\n"
TR "v[176:177],v2 offset:49152\n"
"v_add_f32 v56,v56,v200\n"
MF "v[132:135],a[76:79],a[240:243],v[132:135]\n"
TR "v[178:179],v2 offset:51200\n"
"v_add_f32 v57,v57,v201\n"
MF "v[132:135],a[36:39],v[188:191],v[132:135]\n"
"ds_read2st64_b32 v[192:193],v13 offset0:8 offset1:9\n"
"v_add_f32 v58,v58,v202\n"
MF "v[124:127],a[32:35],v[188:191],v[124:127]\n"
TR "v[180:181],v2 offset:53248\n"
"v_add_f32 v59,v59,v203\n"
MF "v[124:127],a[72:75],a[240:243],v[124:127]\n"
TR "v[182:183],v2 offset:55296\n"
"v_add_f32 v60,v60,v204\n"
MF "v[124:127],a[112:115],a[244:247],v[124:127]\n"
"ds_read2st64_b32 v[194:195],v13 offset0:10 offset1:11\n"
"v_add_f32 v61,v61,v205\n"
MF "v[116:119],a[108:111],a[244:247],v[116:119]\n"
TR "v[184:185],v2 offset:57344\n"
"v_add_f32 v62,v62,v206\n"
MF "v[116:119],a[68:71],a[240:243],v[116:119]\n"
TR "v[186:187],v2 offset:59392\n"
"v_add_f32 v63,v63,v207\n"
MF "v[116:119],a[28:31],v[188:191],v[116:119]\n"
"ds_read2st64_b32 v[196:197],v13 offset0:12 offset1:13\n"
CV "v56,v56,v57\n"
MF "v[108:111],a[24:27],v[188:191],v[108:111]\n"
"ds_read2st64_b32 v[198:199],v13 offset0:14 offset1:15\n"
CV "v57,v58,v59\n"
MF "v[108:111],a[64:67],a[240:243],v[108:111]\n"
"s_add_u32 s87,s90,10240\n"
CV "v58,v60,v61\n"
CV "v59,v62,v63\n"
MF "v[108:111],a[104:107],a[244:247],v[108:111]\n"
MF "v[100:103],a[100:103],a[244:247],v[100:103]\n"
"buffer_store_dwordx4 v[56:59],v9,s[40:43],s87 offen sc1\n"
MF "v[100:103],a[60:63],a[240:243],v[100:103]\n"
"s_waitcnt vmcnt(7)\n"
"s_cmp_lt_u32 s85,6\n"
MF "v[100:103],a[20:23],v[188:191],v[100:103]\n"
"s_cbranch_scc1 .Ljs10\n"
"v_mov_b32 v236,v228\n"
"v_mov_b32 v237,v229\n"
MF "v[136:139],a[252:255],v[208:211],v[136:139]\n"
MF "v[144:147],a[252:255],v[212:215],v[144:147]\n"
MF "v[152:155],a[252:255],v[216:219],v[152:155]\n"
MF "v[160:163],a[252:255],v[220:223],v[160:163]\n"
MF "v[168:171],a[252:255],v[224:227],v[168:171]\n"
MF "v[140:143],a[248:251],v[208:211],v[140:143]\n"
MF "v[148:151],a[248:251],v[212:215],v[148:151]\n"
MF "v[156:159],a[248:251],v[216:219],v[156:159]\n"
MF "v[164:167],a[248:251],v[220:223],v[164:167]\n"
MF "v[172:175],a[248:251],v[224:227],v[172:175]\n"
MF "v[248:251],v[230:233],a[250:253],v[248:251]\n"
MF "v[244:247],v[234:237],a[250:253],v[244:247]\n"
".Ljs10:\n"
"s_waitcnt lgkmcnt(2)\n"
MF "v[136:139],a[20:23],v[176:179],v[136:139]\n"
"s_waitcnt lgkmcnt(0)\n"
TR "v[188:189],v3 offset:49152\n"
"v_add_f32 v64,v64,v192\n"
MF "v[136:139],a[60:63],v[180:183],v[136:139]\n"
TR "v[190:191],v3 offset:51200\n"
"v_add_f32 v65,v65,v193\n"
MF "v[136:139],a[100:103],v[184:187],v[136:139]\n"
"ds_read2st64_b32 v[200:201],v13 offset0:16 offset1:17\n"
"v_add_f32 v66,v66,v194\n"
MF "v[144:147],a[104:107],v[184:187],v[144:147]\n"
TR "a[240:241],v3 offset:53248\n"
"v_add_f32 v67,v67,v195\n"
MF "v[144:147],a[64:67],v[180:183],v[144:147]\n"
TR "a[242:243],v3 offset:55296\n"
"v_add_f32 v68,v68,v196\n"
MF "v[144:147],a[24:27],v[176:179],v[144:147]\n"
"ds_read2st64_b32 v[202:203],v13 offset0:18 offset1:19\n"
"v_add_f32 v69,v69,v197\n"
MF "v[152:155],a[28:31],v[176:179],v[152:155]\n"
TR "a[244:245],v3 offset:57344\n"
"v_add_f32 v70,v70,v198\n"
MF "v[152:155],a[68:71],v[180:183],v[152:155]\n"
TR "a[246:247],v3 offset:59392\n"
"v_add_f32 v71,v71,v199\n"
MF "v[152:155],a[108:111],v[184:187],v[152:155]\n"
"ds_read2st64_b32 v[204:205],v13 offset0:20 offset1:21\n"
CV "v64,v64,v65\n"
MF "v[160:163],a[112:115],v[184:187],v[160:163]\n"
"ds_read2st64_b32 v[206:207],v13 offset0:22 offset1:23\n"
CV "v65,v66,v67\n"
MF "v[160:163],a[72:75],v[180:183],v[160:163]\n"
"s_add_u32 s87,s90,11264\n"
CV "v66,v68,v69\n"
CV "v67,v70,v71\n"
MF "v[160:163],a[32:35],v[176:179],v[160:163]\n"
"v_sub_u32 v10,v10,v11\n"
MF "v[168:171],a[36:39],v[176:179],v[168:171]\n"
"buffer_store_dwordx4 v[64:67],v9,s[40:43],s87 offen sc1\n"
MF "v[168:171],a[76:79],v[180:183],v[168:171]\n"
"s_waitcnt lgkmcnt(0)\n"
MF "v[168:171],a[116:119],v[184:187],v[168:171]\n"
"s_barrier\n"
MF "v[172:175],a[116:119],a[244:247],v[172:175]\n"
TR "v[176:177],v4\n"
"v_add_f32 v72,v72,v200\n"
MF "v[172:175],a[76:79],a[240:243],v[172:175]\n"
TR "v[178:179],v4 offset:2048\n"
"v_add_f32 v73,v73,v201\n"
MF "v[172:175],a[36:39],v[188:191],v[172:175]\n"
"ds_read2st64_b32 v[192:193],v13 offset0:24 offset1:25\n"
"v_add_f32 v74,v74,v202\n"
MF "v[164:167],a[32:35],v[188:191],v[164:167]\n"
TR "v[180:181],v4 offset:4096\n"
"v_add_f32 v75,v75,v203\n"
MF "v[164:167],a[72:75],a[240:243],v[164:167]\n"
TR "v[182:183],v4 offset:6144\n"
"v_add_f32 v76,v76,v204\n"
MF "v[164:167],a[112:115],a[244:247],v[164:167]\n"
"ds_read2st64_b32 v[194:195],v13 offset0:26 offset1:27\n"
"v_add_f32 v77,v77,v205\n"
MF "v[156:159],a[108:111],a[244:247],v[156:159]\n"
TR "v[184:185],v4 offset:8192\n"
"v_add_f32 v78,v78,v206\n"
MF "v[156:159],a[68:71],a[240:243],v[156:159]\n"
TR "v[186:187],v4 offset:10240\n"
"v_add_f32 v79,v79,v207\n"
MF "v[156:159],a[28:31],v[188:191],v[156:159]\n"
"ds_read2st64_b32 v[196:197],v13 offset0:28 offset1:29\n"
CV "v72,v72,v73\n"
MF "v[148:151],a[24:27],v[188:191],v[148:151]\n"
"ds_read2st64_b32 v[198:199],v13 offset0:30 offset1:31\n"
CV "v73,v74,v75\n"
MF "v[148:151],a[64:67],a[240:243],v[148:151]\n"
"s_add_u32 s87,s90,12288\n"
CV "v74,v76,v77\n"
CV "v75,v78,v79\n"
MF "v[148:151],a[104:107],a[244:247],v[148:151]\n"
".Lnf6:\n"
"v_readlane_b32 s58,v10,s67\n"
"s_cmp_lt_u32 s58,s85\n"
"s_cbranch_scc1 .Lsp6\n"
".Lnb6:\n"
MF "v[140:143],a[100:103],a[244:247],v[140:143]\n"
"buffer_store_dwordx4 v[72:75],v9,s[40:43],s87 offen sc1\n"
MF "v[140:143],a[60:63],a[240:243],v[140:143]\n"
"s_add_u32 s70,s70,20480\n"
".Lnf7:\n"
"v_cmp_gt_u32 vcc,4,v10\n"
"s_cbranch_vccnz .Lsp7\n"
".Lnb7:\n"
MF "v[140:143],a[20:23],v[188:191],v[140:143]\n"
"s_waitcnt lgkmcnt(2)\n"
MF "v[96:99],a[140:143],v[176:179],v[96:99]\n"
"s_waitcnt lgkmcnt(0)\n"
TR "v[188:189],v5\n"
"v_add_f32 v80,v80,v192\n"
MF "v[96:99],a[180:183],v[180:183],v[96:99]\n"
TR "v[190:191],v5 offset:2048\n"
"v_add_f32 v81,v81,v193\n"
MF "v[96:99],a[220:223],v[184:187],v[96:99]\n"
"ds_read2st64_b32 v[200:201],v13 offset0:32 offset1:33\n"
"v_add_f32 v82,v82,v194\n"
MF "v[104:107],a[224:227],v[184:187],v[104:107]\n"
TR "a[240:241],v5 offset:4096\n"
"v_add_f32 v83,v83,v195\n"
MF "v[104:107],a[184:187],v[180:183],v[104:107]\n"
TR "a[242:243],v5 offset:6144\n"
"v_add_f32 v84,v84,v196\n"
MF "v[104:107],a[144:147],v[176:179],v[104:107]\n"
"ds_read2st64_b32 v[202:203],v13 offset0:34 offset1:35\n"
"v_add_f32 v85,v85,v197\n"
MF "v[112:115],a[148:151],v[176:179],v[112:115]\n"
TR "a[244:245],v5 offset:8192\n"
"v_add_f32 v86,v86,v198\n"
MF "v[112:115],a[188:191],v[180:183],v[112:115]\n"
TR "a[246:247],v5 offset:10240\n"
"v_add_f32 v87,v87,v199\n"
MF "v[112:115],a[228:231],v[184:187],v[112:115]\n"
"ds_read2st64_b32 v[204:205],v13 offset0:36 offset1:37\n"
CV "v80,v80,v81\n"
MF "v[120:123],a[232:235],v[184:187],v[120:123]\n"
"s_add_u32 s2,s2,204800\n"
"ds_read2st64_b32 v[206:207],v13 offset0:38 offset1:39\n"
CV "v81,v82,v83\n"
MF "v[120:123],a[192:195],v[180:183],v[120:123]\n"
"s_add_u32 s87,s90,13312\n"
CV "v82,v84,v85\n"
CV "v83,v86,v87\n"
MF "v[120:123],a[152:155],v[176:179],v[120:123]\n"
LD "v[228:231],v9,s[40:43],s2 offen sc1\n"
MF "v[128:131],a[156:159],v[176:179],v[128:131]\n"
"buffer_store_dwordx4 v[80:83],v9,s[40:43],s87 offen sc1\n"
MF "v[128:131],a[196:199],v[180:183],v[128:131]\n"
"s_add_u32 s2,s2,204800\n"
MF "v[128:131],a[236:239],v[184:187],v[128:131]\n"
"s_waitcnt lgkmcnt(2)\n"
LD "v[232:235],v9,s[40:43],s2 offen sc1\n"
MF "v[132:135],a[236:239],a[244:247],v[132:135]\n"
"s_waitcnt lgkmcnt(0)\n"
TR "v[176:177],v6\n"
"v_add_f32 v88,v88,v200\n"
MF "v[132:135],a[196:199],a[240:243],v[132:135]\n"
TR "v[178:179],v6 offset:2048\n"
"v_add_f32 v89,v89,v201\n"
MF "v[132:135],a[156:159],v[188:191],v[132:135]\n"
"v_add_f32 v90,v90,v202\n"
"v_add_f32 v91,v91,v203\n"
MF "v[124:127],a[152:155],v[188:191],v[124:127]\n"
TR "v[180:181],v6 offset:4096\n"
"v_add_f32 v92,v92,v204\n"
MF "v[124:127],a[192:195],a[240:243],v[124:127]\n"
TR "v[182:183],v6 offset:6144\n"
"v_add_f32 v93,v93,v205\n"
MF "v[124:127],a[232:235],a[244:247],v[124:127]\n"
"v_add_f32 v94,v94,v206\n"
"v_add_f32 v95,v95,v207\n"
MF "v[116:119],a[228:231],a[244:247],v[116:119]\n"
TR "v[184:185],v6 offset:8192\n"
CV "v88,v88,v89\n"
MF "v[116:119],a[188:191],a[240:243],v[116:119]\n"
TR "v[186:187],v6 offset:10240\n"
CV "v89,v90,v91\n"
MF "v[116:119],a[148:151],v[188:191],v[116:119]\n"
"s_add_u32 s87,s90,14336\n"
CV "v90,v92,v93\n"
CV "v91,v94,v95\n"
MF "v[108:111],a[144:147],v[188:191],v[108:111]\n"
LD "v[208:211],v9,s[40:43],s82 offen sc1\n"
MF "v[108:111],a[184:187],a[240:243],v[108:111]\n"
MF "v[108:111],a[224:227],a[244:247],v[108:111]\n"
MF "v[100:103],a[220:223],a[244:247],v[100:103]\n"
"buffer_store_dwordx4 v[88:91],v9,s[40:43],s87 offen sc1\n"
MF "v[100:103],a[180:183],a[240:243],v[100:103]\n"
"s_mov_b32 s90,s91\n"
"s_add_u32 s91,s91,20480\n"
MF "v[100:103],a[140:143],v[188:191],v[100:103]\n"
"s_waitcnt lgkmcnt(0)\n"
MF "v[136:139],a[140:143],v[176:179],v[136:139]\n"
TR "v[188:189],v7\n"
MF "v[136:139],a[180:183],v[180:183],v[136:139]\n"
TR "v[190:191],v7 offset:2048\n"
MF "v[136:139],a[220:223],v[184:187],v[136:139]\n"
LD "v[212:215],v9,s[40:43],s82 offen offset:1024 sc1\n"
MF "v[144:147],a[224:227],v[184:187],v[144:147]\n"
TR "a[240:241],v7 offset:4096\n"
MF "v[144:147],a[184:187],v[180:183],v[144:147]\n"
TR "a[242:243],v7 offset:6144\n"
MF "v[144:147],a[144:147],v[176:179],v[144:147]\n"
MF "v[152:155],a[148:151],v[176:179],v[152:155]\n"
TR "a[244:245],v7 offset:8192\n"
MF "v[152:155],a[188:191],v[180:183],v[152:155]\n"
TR "a[246:247],v7 offset:10240\n"
MF "v[152:155],a[228:231],v[184:187],v[152:155]\n"
MF "v[160:163],a[232:235],v[184:187],v[160:163]\n"
MF "v[160:163],a[192:195],v[180:183],v[160:163]\n"
LD "v[216:219],v9,s[40:43],s82 offen offset:2048 sc1\n"
MF "v[160:163],a[152:155],v[176:179],v[160:163]\n"
MF "v[168:171],a[156:159],v[176:179],v[168:171]\n"
MF "v[168:171],a[196:199],v[180:183],v[168:171]\n"
"s_waitcnt lgkmcnt(0)\n"
MF "v[168:171],a[236:239],v[184:187],v[168:171]\n"
"s_barrier\n"
MF "v[172:175],a[236:239],a[244:247],v[172:175]\n"
TR "v[176:177],v0 offset:24576\n"
MF "v[172:175],a[196:199],a[240:243],v[172:175]\n"
"s_add_u32 s85,s85,1\n"
TR "v[178:179],v0 offset:26624\n"
MF "v[172:175],a[156:159],v[188:191],v[172:175]\n"
MF "v[164:167],a[152:155],v[188:191],v[164:167]\n"
TR "v[180:181],v0 offset:28672\n"
MF "v[164:167],a[192:195],a[240:243],v[164:167]\n"
TR "v[182:183],v0 offset:30720\n"
MF "v[164:167],a[232:235],a[244:247],v[164:167]\n"
LD "v[220:223],v9,s[40:43],s82 offen offset:3072 sc1\n"
MF "v[156:159],a[228:231],a[244:247],v[156:159]\n"
TR "v[184:185],v0 offset:32768\n"
MF "v[156:159],a[188:191],a[240:243],v[156:159]\n"
TR "v[186:187],v0 offset:34816\n"
MF "v[156:159],a[148:151],v[188:191],v[156:159]\n"
MF "v[148:151],a[144:147],v[188:191],v[148:151]\n"
MF "v[148:151],a[184:187],a[240:243],v[148:151]\n"
MF "v[148:151],a[224:227],a[244:247],v[148:151]\n"
MF "v[140:143],a[220:223],a[244:247],v[140:143]\n"
MF "v[140:143],a[180:183],a[240:243],v[140:143]\n"
MF "v[140:143],a[140:143],v[188:191],v[140:143]\n"
"s_waitcnt lgkmcnt(0)\n"
MF "v[16:19],a[0:3],v[176:179],v[252:255]\n"
TR "v[188:189],v1 offset:24576\n"
MF "v[16:19],a[40:43],v[180:183],v[16:19]\n"
"s_add_u32 s82,s82,4096\n"
TR "v[190:191],v1 offset:26624\n"
MF "v[16:19],a[80:83],v[184:187],v[16:19]\n"
LD "v[224:227],v9,s[40:43],s82 offen sc1\n"
MF "v[24:27],a[84:87],v[184:187],v[252:255]\n"
TR "a[240:241],v1 offset:28672\n"
MF "v[24:27],a[44:47],v[180:183],v[24:27]\n"
TR "a[242:243],v1 offset:30720\n"
MF "v[24:27],a[4:7],v[176:179],v[24:27]\n"
MF "v[32:35],a[8:11],v[176:179],v[252:255]\n"
TR "a[244:245],v1 offset:32768\n"
MF "v[32:35],a[48:51],v[180:183],v[32:35]\n"
TR "a[246:247],v1 offset:34816\n"
MF "v[32:35],a[88:91],v[184:187],v[32:35]\n"
MF "v[40:43],a[92:95],v[184:187],v[252:255]\n"
MF "v[40:43],a[52:55],v[180:183],v[40:43]\n"
MF "v[40:43],a[12:15],v[176:179],v[40:43]\n"
MF "v[48:51],a[16:19],v[176:179],v[252:255]\n"
MF "v[48:51],a[56:59],v[180:183],v[48:51]\n"
MF "v[48:51],a[96:99],v[184:187],v[48:51]\n"
"s_waitcnt lgkmcnt(0)\n"
MF "v[52:55],a[96:99],a[244:247],v[252:255]\n"
TR "v[176:177],v2 offset:24576\n"
MF "v[52:55],a[56:59],a[240:243],v[52:55]\n"
TR "v[178:179],v2 offset:26624\n"
MF "v[52:55],a[16:19],v[188:191],v[52:55]\n"
MF "v[44:47],a[12:15],v[188:191],v[252:255]\n"
TR "v[180:181],v2 offset:28672\n"
MF "v[44:47],a[52:55],a[240:243],v[44:47]\n"
TR "v[182:183],v2 offset:30720\n"
MF "v[44:47],a[92:95],a[244:247],v[44:47]\n"
MF "v[36:39],a[88:91],a[244:247],v[252:255]\n"
TR "v[184:185],v2 offset:32768\n"
MF "v[36:39],a[48:51],a[240:243],v[36:39]\n"
TR "v[186:187],v2 offset:34816\n"
MF "v[36:39],a[8:11],v[188:191],v[36:39]\n"
MF "v[28:31],a[4:7],v[188:191],v[252:255]\n"
MF "v[28:31],a[44:47],a[240:243],v[28:31]\n"
MF "v[28:31],a[84:87],a[244:247],v[28:31]\n"
MF "v[20:23],a[80:83],a[244:247],v[252:255]\n"
MF "v[20:23],a[40:43],a[240:243],v[20:23]\n"
MF "v[20:23],a[0:3],v[188:191],v[20:23]\n"
"s_waitcnt lgkmcnt(0)\n"
MF "v[56:59],a[0:3],v[176:179],v[252:255]\n"
TR "v[188:189],v3 offset:24576\n"
MF "v[56:59],a[40:43],v[180:183],v[56:59]\n"
TR "v[190:191],v3 offset:26624\n"
MF "v[56:59],a[80:83],v[184:187],v[56:59]\n"
MF "v[64:67],a[84:87],v[184:187],v[252:255]\n"
TR "a[240:241],v3 offset:28672\n"
MF "v[64:67],a[44:47],v[180:183],v[64:67]\n"
TR "a[242:243],v3 offset:30720\n"
MF "v[64:67],a[4:7],v[176:179],v[64:67]\n"
MF "v[72:75],a[8:11],v[176:179],v[252:255]\n"
TR "a[244:245],v3 offset:32768\n"
MF "v[72:75],a[48:51],v[180:183],v[72:75]\n"
TR "a[246:247],v3 offset:34816\n"
MF "v[72:75],a[88:91],v[184:187],v[72:75]\n"
MF "v[80:83],a[92:95],v[184:187],v[252:255]\n"
MF "v[80:83],a[52:55],v[180:183],v[80:83]\n"
MF "v[80:83],a[12:15],v[176:179],v[80:83]\n"
MF "v[88:91],a[16:19],v[176:179],v[252:255]\n"
MF "v[88:91],a[56:59],v[180:183],v[88:91]\n"
"s_waitcnt lgkmcnt(0)\n"
MF "v[88:91],a[96:99],v[184:187],v[88:91]\n"
"s_barrier\n"
MF "v[92:95],a[96:99],a[244:247],v[252:255]\n"
TR "v[176:177],v0\n"
MF "v[92:95],a[56:59],a[240:243],v[92:95]\n"
TR "v[178:179],v0 offset:2048\n"
MF "v[92:95],a[16:19],v[188:191],v[92:95]\n"
"ds_read2st64_b32 v[192:193],v13 offset0:192 offset1:193\n"
MF "v[84:87],a[12:15],v[188:191],v[252:255]\n"
TR "v[180:181],v0 offset:4096\n"
MF "v[84:87],a[52:55],a[240:243],v[84:87]\n"
TR "v[182:183],v0 offset:6144\n"
MF "v[84:87],a[92:95],a[244:247],v[84:87]\n"
"ds_read2st64_b32 v[194:195],v13 offset0:194 offset1:195\n"
MF "v[76:79],a[88:91],a[244:247],v[252:255]\n"
TR "v[184:185],v0 offset:8192\n"
MF "v[76:79],a[48:51],a[240:243],v[76:79]\n"
TR "v[186:187],v0 offset:10240\n"
MF "v[76:79],a[8:11],v[188:191],v[76:79]\n"
"ds_read2st64_b32 v[196:197],v13 offset0:196 offset1:197\n"
MF "v[68:71],a[4:7],v[188:191],v[252:255]\n"
"ds_read2st64_b32 v[198:199],v13 offset0:198 offset1:199\n"
MF "v[68:71],a[44:47],a[240:243],v[68:71]\n"
"buffer_load_dword v10,v12,s[40:43],s86 offen sc1\n"
MF "v[68:71],a[84:87],a[244:247],v[68:71]\n"
MF "v[60:63],a[80:83],a[244:247],v[252:255]\n"
MF "v[60:63],a[40:43],a[240:243],v[60:63]\n"
MF "v[60:63],a[0:3],v[188:191],v[60:63]\n"
"s_waitcnt lgkmcnt(2)\n"
MF "v[16:19],a[120:123],v[176:179],v[16:19]\n"
"s_waitcnt lgkmcnt(0)\n"
TR "v[188:189],v1\n"
"v_add_f32 v96,v96,v192\n"
MF "v[16:19],a[160:163],v[180:183],v[16:19]\n"
TR "v[190:191],v1 offset:2048\n"
"v_add_f32 v97,v97,v193\n"
MF "v[16:19],a[200:203],v[184:187],v[16:19]\n"
"ds_read2st64_b32 v[200:201],v13 offset0:200 offset1:201\n"
"v_add_f32 v98,v98,v194\n"
"s_waitcnt vmcnt(5)\n"
"v_mov_b32 v14,s76\n"
"s_add_u32 s76,s76,1\n"
MF "v[24:27],a[204:207],v[184:187],v[24:27]\n"
TR "a[240:241],v1 offset:4096\n"
"v_add_f32 v99,v99,v195\n"
"buffer_store_dword v14,v15,s[40:43],s94 offen sc1\n"
MF "v[24:27],a[164:167],v[180:183],v[24:27]\n"
TR "a[242:243],v1 offset:6144\n"
"v_add_f32 v100,v100,v196\n"
MF "v[24:27],a[124:127],v[176:179],v[24:27]\n"
"ds_read2st64_b32 v[202:203],v13 offset0:202 offset1:203\n"
"v_add_f32 v101,v101,v197\n"
MF "v[32:35],a[128:131],v[176:179],v[32:35]\n"
TR "a[244:245],v1 offset:8192\n"
"v_add_f32 v102,v102,v198\n"
MF "v[32:35],a[168:171],v[180:183],v[32:35]\n"
TR "a[246:247],v1 offset:10240\n"
"v_add_f32 v103,v103,v199\n"
MF "v[32:35],a[208:211],v[184:187],v[32:35]\n"
"ds_read2st64_b32 v[204:205],v13 offset0:204 offset1:205\n"
CV "v96,v96,v97\n"
MF "v[40:43],a[212:215],v[184:187],v[40:43]\n"
"ds_read2st64_b32 v[206:207],v13 offset0:206 offset1:207\n"
CV "v97,v98,v99\n"
MF "v[40:43],a[172:175],v[180:183],v[40:43]\n"
"s_add_u32 s87,s90,0\n"
CV "v98,v100,v101\n"
CV "v99,v102,v103\n"
MF "v[40:43],a[132:135],v[176:179],v[40:43]\n"
MF "v[48:51],a[136:139],v[176:179],v[48:51]\n"
"buffer_store_dwordx4 v[96:99],v9,s[40:43],s87 offen sc1\n"
MF "v[48:51],a[176:179],v[180:183],v[48:51]\n"
MF "v[48:51],a[216:219],v[184:187],v[48:51]\n"
"s_waitcnt lgkmcnt(2)\n"
MF "v[52:55],a[216:219],a[244:247],v[52:55]\n"
"s_waitcnt lgkmcnt(0)\n"
TR "v[176:177],v2\n"
"v_add_f32 v104,v104,v200\n"
MF "v[52:55],a[176:179],a[240:243],v[52:55]\n"
TR "v[178:179],v2 offset:2048\n"
"v_add_f32 v105,v105,v201\n"
MF "v[52:55],a[136:139],v[188:191],v[52:55]\n"
"ds_read2st64_b32 v[192:193],v13 offset0:208 offset1:209\n"
"v_add_f32 v106,v106,v202\n"
MF "v[44:47],a[132:135],v[188:191],v[44:47]\n"
TR "v[180:181],v2 offset:4096\n"
"v_add_f32 v107,v107,v203\n"
MF "v[44:47],a[172:175],a[240:243],v[44:47]\n"
TR "v[182:183],v2 offset:6144\n"
"v_add_f32 v108,v108,v204\n"
MF "v[44:47],a[212:215],a[244:247],v[44:47]\n"
"ds_read2st64_b32 v[194:195],v13 offset0:210 offset1:211\n"
"v_add_f32 v109,v109,v205\n"
MF "v[36:39],a[208:211],a[244:247],v[36:39]\n"
TR "v[184:185],v2 offset:8192\n"
"v_add_f32 v110,v110,v206\n"
MF "v[36:39],a[168:171],a[240:243],v[36:39]\n"
TR "v[186:187],v2 offset:10240\n"
"v_add_f32 v111,v111,v207\n"
MF "v[36:39],a[128:131],v[188:191],v[36:39]\n"
"ds_read2st64_b32 v[196:197],v13 offset0:212 offset1:213\n"
CV "v104,v104,v105\n"
MF "v[28:31],a[124:127],v[188:191],v[28:31]\n"
"ds_read2st64_b32 v[198:199],v13 offset0:214 offset1:215\n"
CV "v105,v106,v107\n"
MF "v[28:31],a[164:167],a[240:243],v[28:31]\n"
"s_add_u32 s87,s90,1024\n"
CV "v106,v108,v109\n"
CV "v107,v110,v111\n"
MF "v[28:31],a[204:207],a[244:247],v[28:31]\n"
MF "v[20:23],a[200:203],a[244:247],v[20:23]\n"
"buffer_store_dwordx4 v[104:107],v9,s[40:43],s87 offen sc1\n"
MF "v[20:23],a[160:163],a[240:243],v[20:23]\n"
MF "v[20:23],a[120:123],v[188:191],v[20:23]\n"
"s_waitcnt lgkmcnt(2)\n"
MF "v[56:59],a[120:123],v[176:179],v[56:59]\n"
"s_waitcnt lgkmcnt(0)\n"
TR "v[188:189],v3\n"
"v_add_f32 v112,v112,v192\n"
MF "v[56:59],a[160:163],v[180:183],v[56:59]\n"
TR "v[190:191],v3 offset:2048\n"
"v_add_f32 v113,v113,v193\n"
MF "v[56:59],a[200:203],v[184:187],v[56:59]\n"
"ds_read2st64_b32 v[200:201],v13 offset0:216 offset1:217\n"
"v_add_f32 v114,v114,v194\n"
MF "v[64:67],a[204:207],v[184:187],v[64:67]\n"
TR "a[240:241],v3 offset:4096\n"
"v_add_f32 v115,v115,v195\n"
MF "v[64:67],a[164:167],v[180:183],v[64:67]\n"
TR "a[242:243],v3 offset:6144\n"
"v_add_f32 v116,v116,v196\n"
MF "v[64:67],a[124:127],v[176:179],v[64:67]\n"
".Lpg36h:\n"
"s_cbranch_execz .Lisr4\n"
"ds_read2st64_b32 v[202:203],v13 offset0:218 offset1:219\n"
"v_add_f32 v117,v117,v197\n"
MF "v[72:75],a[128:131],v[176:179],v[72:75]\n"
TR "a[244:245],v3 offset:8192\n"
"v_add_f32 v118,v118,v198\n"
MF "v[72:75],a[168:171],v[180:183],v[72:75]\n"
TR "a[246:247],v3 offset:10240\n"
"v_add_f32 v119,v119,v199\n"
MF "v[72:75],a[208:211],v[184:187],v[72:75]\n"
"ds_read2st64_b32 v[204:205],v13 offset0:220 offset1:221\n"
CV "v112,v112,v113\n"
MF "v[80:83],a[212:215],v[184:187],v[80:83]\n"
"ds_read2st64_b32 v[206:207],v13 offset0:222 offset1:223\n"
CV "v113,v114,v115\n"
MF "v[80:83],a[172:175],v[180:183],v[80:83]\n"
"s_add_u32 s87,s90,2048\n"
CV "v114,v116,v117\n"
CV "v115,v118,v119\n"
MF "v[80:83],a[132:135],v[176:179],v[80:83]\n"
MF "v[88:91],a[136:139],v[176:179],v[88:91]\n"
"buffer_store_dwordx4 v[112:115],v9,s[40:43],s87 offen sc1\n"
MF "v[88:91],a[176:179],v[180:183],v[88:91]\n"
"s_waitcnt lgkmcnt(0)\n"
MF "v[88:91],a[216:219],v[184:187],v[88:91]\n"
"s_barrier\n"
MF "v[92:95],a[216:219],a[244:247],v[92:95]\n"
TR "v[176:177],v0 offset:49152\n"
"v_add_f32 v120,v120,v200\n"
MF "v[92:95],a[176:179],a[240:243],v[92:95]\n"
"s_mov_b32 s82,s70\n"
TR "v[178:179],v0 offset:51200\n"
"v_add_f32 v121,v121,v201\n"
MF "v[92:95],a[136:139],v[188:191],v[92:95]\n"
"ds_read2st64_b32 v[192:193],v13 offset0:224 offset1:225\n"
"v_add_f32 v122,v122,v202\n"
MF "v[84:87],a[132:135],v[188:191],v[84:87]\n"
TR "v[180:181],v0 offset:53248\n"
"v_add_f32 v123,v123,v203\n"
MF "v[84:87],a[172:175],a[240:243],v[84:87]\n"
TR "v[182:183],v0 offset:55296\n"
"v_add_f32 v124,v124,v204\n"
MF "v[84:87],a[212:215],a[244:247],v[84:87]\n"
"ds_read2st64_b32 v[194:195],v13 offset0:226 offset1:227\n"
"v_add_f32 v125,v125,v205\n"
MF "v[76:79],a[208:211],a[244:247],v[76:79]\n"
TR "v[184:185],v0 offset:57344\n"
"v_add_f32 v126,v126,v206\n"
MF "v[76:79],a[168:171],a[240:243],v[76:79]\n"
TR "v[186:187],v0 offset:59392\n"
"v_add_f32 v127,v127,v207\n"
MF "v[76:79],a[128:131],v[188:191],v[76:79]\n"
"ds_read2st64_b32 v[196:197],v13 offset0:228 offset1:229\n"
CV "v120,v120,v121\n"
MF "v[68:71],a[124:127],v[188:191],v[68:71]\n"
"ds_read2st64_b32 v[198:199],v13 offset0:230 offset1:231\n"
CV "v121,v122,v123\n"
MF "v[68:71],a[164:167],a[240:243],v[68:71]\n"
"s_add_u32 s87,s90,3072\n"
CV "v122,v124,v125\n"
CV "v123,v126,v127\n"
MF "v[68:71],a[204:207],a[244:247],v[68:71]\n"
MF "v[60:63],a[200:203],a[244:247],v[60:63]\n"
"buffer_store_dwordx4 v[120:123],v9,s[40:43],s87 offen sc1\n"
MF "v[60:63],a[160:163],a[240:243],v[60:63]\n"
MF "v[60:63],a[120:123],v[188:191],v[60:63]\n"
"s_waitcnt lgkmcnt(2)\n"
MF "v[16:19],a[20:23],v[176:179],v[16:19]\n"
"s_waitcnt lgkmcnt(0)\n"
TR "v[188:189],v1 offset:49152\n"
"v_add_f32 v128,v128,v192\n"
MF "v[16:19],a[60:63],v[180:183],v[16:19]\n"
TR "v[190:191],v1 offset:51200\n"
"v_add_f32 v129,v129,v193\n"
MF "v[16:19],a[100:103],v[184:187],v[16:19]\n"
"ds_read2st64_b32 v[200:201],v13 offset0:0 offset1:1\n"
"v_add_f32 v130,v130,v194\n"
MF "v[24:27],a[104:107],v[184:187],v[24:27]\n"
TR "a[240:241],v1 offset:53248\n"
"v_add_f32 v131,v131,v195\n"
MF "v[24:27],a[64:67],v[180:183],v[24:27]\n"
TR "a[242:243],v1 offset:55296\n"
"v_add_f32 v132,v132,v196\n"
MF "v[24:27],a[24:27],v[176:179],v[24:27]\n"
"ds_read2st64_b32 v[202:203],v13 offset0:2 offset1:3\n"
"v_add_f32 v133,v133,v197\n"
MF "v[32:35],a[28:31],v[176:179],v[32:35]\n"
TR "a[244:245],v1 offset:57344\n"
"v_add_f32 v134,v134,v198\n"
MF "v[32:35],a[68:71],v[180:183],v[32:35]\n"
TR "a[246:247],v1 offset:59392\n"
"v_add_f32 v135,v135,v199\n"
MF "v[32:35],a[108:111],v[184:187],v[32:35]\n"
"ds_read2st64_b32 v[204:205],v13 offset0:4 offset1:5\n"
CV "v128,v128,v129\n"
MF "v[40:43],a[112:115],v[184:187],v[40:43]\n"
"ds_read2st64_b32 v[206:207],v13 offset0:6 offset1:7\n"
CV "v129,v130,v131\n"
MF "v[40:43],a[72:75],v[180:183],v[40:43]\n"
"s_add_u32 s87,s90,4096\n"
CV "v130,v132,v133\n"
CV "v131,v134,v135\n"
MF "v[40:43],a[32:35],v[176:179],v[40:43]\n"
MF "v[48:51],a[36:39],v[176:179],v[48:51]\n"
"buffer_store_dwordx4 v[128:131],v9,s[40:43],s87 offen sc1\n"
MF "v[48:51],a[76:79],v[180:183],v[48:51]\n"
MF "v[48:51],a[116:119],v[184:187],v[48:51]\n"
"s_waitcnt lgkmcnt(2)\n"
MF "v[52:55],a[116:119],a[244:247],v[52:55]\n"
"s_waitcnt lgkmcnt(0)\n"
TR "v[176:177],v2 offset:49152\n"
"v_add_f32 v136,v136,v200\n"
MF "v[52:55],a[76:79],a[240:243],v[52:55]\n"
TR "v[178:179],v2 offset:51200\n"
"v_add_f32 v137,v137,v201\n"
MF "v[52:55],a[36:39],v[188:191],v[52:55]\n"
"ds_read2st64_b32 v[192:193],v13 offset0:8 offset1:9\n"
"v_add_f32 v138,v138,v202\n"
MF "v[44:47],a[32:35],v[188:191],v[44:47]\n"
TR "v[180:181],v2 offset:53248\n"
"v_add_f32 v139,v139,v203\n"
MF "v[44:47],a[72:75],a[240:243],v[44:47]\n"
TR "v[182:183],v2 offset:55296\n"
"v_add_f32 v140,v140,v204\n"
MF "v[44:47],a[112:115],a[244:247],v[44:47]\n"
"ds_read2st64_b32 v[194:195],v13 offset0:10 offset1:11\n"
"v_add_f32 v141,v141,v205\n"
MF "v[36:39],a[108:111],a[244:247],v[36:39]\n"
TR "v[184:185],v2 offset:57344\n"
"v_add_f32 v142,v142,v206\n"
MF "v[36:39],a[68:71],a[240:243],v[36:39]\n"
TR "v[186:187],v2 offset:59392\n"
"v_add_f32 v143,v143,v207\n"
MF "v[36:39],a[28:31],v[188:191],v[36:39]\n"
"ds_read2st64_b32 v[196:197],v13 offset0:12 offset1:13\n"
CV "v136,v136,v137\n"
MF "v[28:31],a[24:27],v[188:191],v[28:31]\n"
"ds_read2st64_b32 v[198:199],v13 offset0:14 offset1:15\n"
CV "v137,v138,v139\n"
MF "v[28:31],a[64:67],a[240:243],v[28:31]\n"
"s_add_u32 s87,s90,10240\n"
CV "v138,v140,v141\n"
CV "v139,v142,v143\n"
MF "v[28:31],a[104:107],a[244:247],v[28:31]\n"
MF "v[20:23],a[100:103],a[244:247],v[20:23]\n"
"buffer_store_dwordx4 v[136:139],v9,s[40:43],s87 offen sc1\n"
MF "v[20:23],a[60:63],a[240:243],v[20:23]\n"
"s_waitcnt vmcnt(7)\n"
MF "v[20:23],a[20:23],v[188:191],v[20:23]\n"
"v_mov_b32 v236,v228\n"
"v_mov_b32 v237,v229\n"
MF "v[56:59],a[252:255],v[208:211],v[56:59]\n"
MF "v[64:67],a[252:255],v[212:215],v[64:67]\n"
MF "v[72:75],a[252:255],v[216:219],v[72:75]\n"
MF "v[80:83],a[252:255],v[220:223],v[80:83]\n"
MF "v[88:91],a[252:255],v[224:227],v[88:91]\n"
MF "v[60:63],a[248:251],v[208:211],v[60:63]\n"
MF "v[68:71],a[248:251],v[212:215],v[68:71]\n"
MF "v[76:79],a[248:251],v[216:219],v[76:79]\n"
MF "v[84:87],a[248:251],v[220:223],v[84:87]\n"
MF "v[92:95],a[248:251],v[224:227],v[92:95]\n"
MF "v[248:251],v[230:233],a[250:253],v[248:251]\n"
MF "v[244:247],v[234:237],a[250:253],v[244:247]\n"
"s_waitcnt lgkmcnt(2)\n"
MF "v[56:59],a[20:23],v[176:179],v[56:59]\n"
"s_waitcnt lgkmcnt(0)\n"
TR "v[188:189],v3 offset:49152\n"
"v_add_f32 v144,v144,v192\n"
MF "v[56:59],a[60:63],v[180:183],v[56:59]\n"
TR "v[190:191],v3 offset:51200\n"
"v_add_f32 v145,v145,v193\n"
MF "v[56:59],a[100:103],v[184:187],v[56:59]\n"
"ds_read2st64_b32 v[200:201],v13 offset0:16 offset1:17\n"
"v_add_f32 v146,v146,v194\n"
MF "v[64:67],a[104:107],v[184:187],v[64:67]\n"
TR "a[240:241],v3 offset:53248\n"
"v_add_f32 v147,v147,v195\n"
MF "v[64:67],a[64:67],v[180:183],v[64:67]\n"
TR "a[242:243],v3 offset:55296\n"
"v_add_f32 v148,v148,v196\n"
MF "v[64:67],a[24:27],v[176:179],v[64:67]\n"
"ds_read2st64_b32 v[202:203],v13 offset0:18 offset1:19\n"
"v_add_f32 v149,v149,v197\n"
MF "v[72:75],a[28:31],v[176:179],v[72:75]\n"
TR "a[244:245],v3 offset:57344\n"
"v_add_f32 v150,v150,v198\n"
MF "v[72:75],a[68:71],v[180:183],v[72:75]\n"
TR "a[246:247],v3 offset:59392\n"
"v_add_f32 v151,v151,v199\n"
MF "v[72:75],a[108:111],v[184:187],v[72:75]\n"
"ds_read2st64_b32 v[204:205],v13 offset0:20 offset1:21\n"
CV "v144,v144,v145\n"
MF "v[80:83],a[112:115],v[184:187],v[80:83]\n"
"ds_read2st64_b32 v[206:207],v13 offset0:22 offset1:23\n"
CV "v145,v146,v147\n"
MF "v[80:83],a[72:75],v[180:183],v[80:83]\n"
"s_add_u32 s87,s90,11264\n"
CV "v146,v148,v149\n"
CV "v147,v150,v151\n"
MF "v[80:83],a[32:35],v[176:179],v[80:83]\n"
"v_sub_u32 v10,v10,v11\n"
MF "v[88:91],a[36:39],v[176:179],v[88:91]\n"
"buffer_store_dwordx4 v[144:147],v9,s[40:43],s87 offen sc1\n"
MF "v[88:91],a[76:79],v[180:183],v[88:91]\n"
"s_waitcnt lgkmcnt(0)\n"
MF "v[88:91],a[116:119],v[184:187],v[88:91]\n"
"s_barrier\n"
MF "v[92:95],a[116:119],a[244:247],v[92:95]\n"
TR "v[176:177],v4\n"
"v_add_f32 v152,v152,v200\n"
MF "v[92:95],a[76:79],a[240:243],v[92:95]\n"
TR "v[178:179],v4 offset:2048\n"
"v_add_f32 v153,v153,v201\n"
MF "v[92:95],a[36:39],v[188:191],v[92:95]\n"
"ds_read2st64_b32 v[192:193],v13 offset0:24 offset1:25\n"
"v_add_f32 v154,v154,v202\n"
MF "v[84:87],a[32:35],v[188:191],v[84:87]\n"
TR "v[180:181],v4 offset:4096\n"
"v_add_f32 v155,v155,v203\n"
MF "v[84:87],a[72:75],a[240:243],v[84:87]\n"
TR "v[182:183],v4 offset:6144\n"
"v_add_f32 v156,v156,v204\n"
MF "v[84:87],a[112:115],a[244:247],v[84:87]\n"
"ds_read2st64_b32 v[194:195],v13 offset0:26 offset1:27\n"
"v_add_f32 v157,v157,v205\n"
MF "v[76:79],a[108:111],a[244:247],v[76:79]\n"
TR "v[184:185],v4 offset:8192\n"
"v_add_f32 v158,v158,v206\n"
MF "v[76:79],a[68:71],a[240:243],v[76:79]\n"
TR "v[186:187],v4 offset:10240\n"
"v_add_f32 v159,v159,v207\n"
MF "v[76:79],a[28:31],v[188:191],v[76:79]\n"
"ds_read2st64_b32 v[196:197],v13 offset0:28 offset1:29\n"
CV "v152,v152,v153\n"
MF "v[68:71],a[24:27],v[188:191],v[68:71]\n"
"ds_read2st64_b32 v[198:199],v13 offset0:30 offset1:31\n"
CV "v153,v154,v155\n"
MF "v[68:71],a[64:67],a[240:243],v[68:71]\n"
"s_add_u32 s87,s90,12288\n"
CV "v154,v156,v157\n"
CV "v155,v158,v159\n"
MF "v[68:71],a[104:107],a[244:247],v[68:71]\n"
".Lnf8:\n"
"v_readlane_b32 s58,v10,s67\n"
"s_cmp_lt_u32 s58,s85\n"
"s_cbranch_scc1 .Lsp8\n"
".Lnb8:\n"
MF "v[60:63],a[100:103],a[244:247],v[60:63]\n"
"buffer_store_dwordx4 v[152:155],v9,s[40:43],s87 offen sc1\n"
MF "v[60:63],a[60:63],a[240:243],v[60:63]\n"
"s_add_u32 s70,s70,20480\n"
"s_add_u32 s2,s2,204800\n"
MF "v[60:63],a[20:23],v[188:191],v[60:63]\n"
"s_waitcnt lgkmcnt(2)\n"
LD "v[228:231],v9,s[40:43],s2 offen sc1\n"
MF "v[16:19],a[140:143],v[176:179],v[16:19]\n"
"s_waitcnt lgkmcnt(0)\n"
TR "v[188:189],v5\n"
"v_add_f32 v160,v160,v192\n"
MF "v[16:19],a[180:183],v[180:183],v[16:19]\n"
TR "v[190:191],v5 offset:2048\n"
"v_add_f32 v161,v161,v193\n"
MF "v[16:19],a[220:223],v[184:187],v[16:19]\n"
"ds_read2st64_b32 v[200:201],v13 offset0:32 offset1:33\n"
"v_add_f32 v162,v162,v194\n"
MF "v[24:27],a[224:227],v[184:187],v[24:27]\n"
TR "a[240:241],v5 offset:4096\n"
"v_add_f32 v163,v163,v195\n"
MF "v[24:27],a[184:187],v[180:183],v[24:27]\n"
TR "a[242:243],v5 offset:6144\n"
"v_add_f32 v164,v164,v196\n"
MF "v[24:27],a[144:147],v[176:179],v[24:27]\n"
"ds_read2st64_b32 v[202:203],v13 offset0:34 offset1:35\n"
"v_add_f32 v165,v165,v197\n"
MF "v[32:35],a[148:151],v[176:179],v[32:35]\n"
TR "a[244:245],v5 offset:8192\n"
"v_add_f32 v166,v166,v198\n"
MF "v[32:35],a[188:191],v[180:183],v[32:35]\n"
TR "a[246:247],v5 offset:10240\n"
"v_add_f32 v167,v167,v199\n"
MF "v[32:35],a[228:231],v[184:187],v[32:35]\n"
"ds_read2st64_b32 v[204:205],v13 offset0:36 offset1:37\n"
CV "v160,v160,v161\n"
MF "v[40:43],a[232:235],v[184:187],v[40:43]\n"
"s_add_u32 s2,s2,204800\n"
"ds_read2st64_b32 v[206:207],v13 offset0:38 offset1:39\n"
CV "v161,v162,v163\n"
MF "v[40:43],a[192:195],v[180:183],v[40:43]\n"
"s_add_u32 s87,s90,13312\n"
CV "v162,v164,v165\n"
CV "v163,v166,v167\n"
MF "v[40:43],a[152:155],v[176:179],v[40:43]\n"
LD "v[232:235],v9,s[40:43],s2 offen sc1\n"
MF "v[48:51],a[156:159],v[176:179],v[48:51]\n"
"buffer_store_dwordx4 v[160:163],v9,s[40:43],s87 offen sc1\n"
MF "v[48:51],a[196:199],v[180:183],v[48:51]\n"
MF "v[48:51],a[236:239],v[184:187],v[48:51]\n"
"s_waitcnt lgkmcnt(2)\n"
MF "v[52:55],a[236:239],a[244:247],v[52:55]\n"
"s_waitcnt lgkmcnt(0)\n"
TR "v[176:177],v6\n"
"v_add_f32 v168,v168,v200\n"
MF "v[52:55],a[196:199],a[240:243],v[52:55]\n"
TR "v[178:179],v6 offset:2048\n"
"v_add_f32 v169,v169,v201\n"
MF "v[52:55],a[156:159],v[188:191],v[52:55]\n"
"v_add_f32 v170,v170,v202\n"
"v_add_f32 v171,v171,v203\n"
MF "v[44:47],a[152:155],v[188:191],v[44:47]\n"
TR "v[180:181],v6 offset:4096\n"
"v_add_f32 v172,v172,v204\n"
MF "v[44:47],a[192:195],a[240:243],v[44:47]\n"
TR "v[182:183],v6 offset:6144\n"
"v_add_f32 v173,v173,v205\n"
MF "v[44:47],a[232:235],a[244:247],v[44:47]\n"
"v_add_f32 v174,v174,v206\n"
"v_add_f32 v175,v175,v207\n"
MF "v[36:39],a[228:231],a[244:247],v[36:39]\n"
TR "v[184:185],v6 offset:8192\n"
CV "v168,v168,v169\n"
MF "v[36:39],a[188:191],a[240:243],v[36:39]\n"
TR "v[186:187],v6 offset:10240\n"
CV "v169,v170,v171\n"
MF "v[36:39],a[148:151],v[188:191],v[36:39]\n"
"s_add_u32 s87,s90,14336\n"
CV "v170,v172,v173\n"
CV "v171,v174,v175\n"
MF "v[28:31],a[144:147],v[188:191],v[28:31]\n"
LD "v[208:211],v9,s[40:43],s82 offen sc1\n"
MF "v[28:31],a[184:187],a[240:243],v[28:31]\n"
MF "v[28:31],a[224:227],a[244:247],v[28:31]\n"
MF "v[20:23],a[220:223],a[244:247],v[20:23]\n"
"buffer_store_dwordx4 v[168:171],v9,s[40:43],s87 offen sc1\n"
MF "v[20:23],a[180:183],a[240:243],v[20:23]\n"
"s_mov_b32 s90,s91\n"
"s_add_u32 s91,s91,20480\n"
MF "v[20:23],a[140:143],v[188:191],v[20:23]\n"
"s_waitcnt lgkmcnt(0)\n"
MF "v[56:59],a[140:143],v[176:179],v[56:59]\n"
TR "v[188:189],v7\n"
MF "v[56:59],a[180:183],v[180:183],v[56:59]\n"
TR "v[190:191],v7 offset:2048\n"
MF "v[56:59],a[220:223],v[184:187],v[56:59]\n"
LD "v[212:215],v9,s[40:43],s82 offen offset:1024 sc1\n"
MF "v[64:67],a[224:227],v[184:187],v[64:67]\n"
TR "a[240:241],v7 offset:4096\n"
MF "v[64:67],a[184:187],v[180:183],v[64:67]\n"
TR "a[242:243],v7 offset:6144\n"
MF "v[64:67],a[144:147],v[176:179],v[64:67]\n"
MF "v[72:75],a[148:151],v[176:179],v[72:75]\n"
TR "a[244:245],v7 offset:8192\n"
MF "v[72:75],a[188:191],v[180:183],v[72:75]\n"
TR "a[246:247],v7 offset:10240\n"
MF "v[72:75],a[228:231],v[184:187],v[72:75]\n"
MF "v[80:83],a[232:235],v[184:187],v[80:83]\n"
MF "v[80:83],a[192:195],v[180:183],v[80:83]\n"
LD "v[216:219],v9,s[40:43],s82 offen offset:2048 sc1\n"
MF "v[80:83],a[152:155],v[176:179],v[80:83]\n"
MF "v[88:91],a[156:159],v[176:179],v[88:91]\n"
MF "v[88:91],a[196:199],v[180:183],v[88:91]\n"
"s_waitcnt lgkmcnt(0)\n"
MF "v[88:91],a[236:239],v[184:187],v[88:91]\n"
"s_barrier\n"
MF "v[92:95],a[236:239],a[244:247],v[92:95]\n"
TR "v[176:177],v0 offset:24576\n"
MF "v[92:95],a[196:199],a[240:243],v[92:95]\n"
"s_add_u32 s85,s85,1\n"
TR "v[178:179],v0 offset:26624\n"
MF "v[92:95],a[156:159],v[188:191],v[92:95]\n"
MF "v[84:87],a[152:155],v[188:191],v[84:87]\n"
TR "v[180:181],v0 offset:28672\n"
MF "v[84:87],a[192:195],a[240:243],v[84:87]\n"
TR "v[182:183],v0 offset:30720\n"
MF "v[84:87],a[232:235],a[244:247],v[84:87]\n"
LD "v[220:223],v9,s[40:43],s82 offen offset:3072 sc1\n"
MF "v[76:79],a[228:231],a[244:247],v[76:79]\n"
TR "v[184:185],v0 offset:32768\n"
MF "v[76:79],a[188:191],a[240:243],v[76:79]\n"
TR "v[186:187],v0 offset:34816\n"
MF "v[76:79],a[148:151],v[188:191],v[76:79]\n"
MF "v[68:71],a[144:147],v[188:191],v[68:71]\n"
MF "v[68:71],a[184:187],a[240:243],v[68:71]\n"
MF "v[68:71],a[224:227],a[244:247],v[68:71]\n"
MF "v[60:63],a[220:223],a[244:247],v[60:63]\n"
MF "v[60:63],a[180:183],a[240:243],v[60:63]\n"
MF "v[60:63],a[140:143],v[188:191],v[60:63]\n"
"s_waitcnt lgkmcnt(0)\n"
"s_cmp_lg_u32 s85,9\n"
"s_cbranch_scc1 .Lpl19\n"
".Lplast1:\n"
MF "v[96:99],v[176:179],a[0:3],v[252:255]\n"
"s_add_u32 m0,s79,73728\n"
TR "v[188:189],v1 offset:24576\n"
TR "v[190:191],v1 offset:26624\n"
MF "v[96:99],v[180:183],a[40:43],v[96:99]\n"
"s_nop 0\n"
TR "a[240:241],v1 offset:28672\n"
TR "a[242:243],v1 offset:30720\n"
MF "v[96:99],v[184:187],a[80:83],v[96:99]\n"
"s_add_u32 s82,s82,4096\n"
TR "a[244:245],v1 offset:32768\n"
TR "a[246:247],v1 offset:34816\n"
MF "v[104:107],v[184:187],a[84:87],v[252:255]\n"
LD "v[224:227],v9,s[40:43],s82 offen sc1\n"
MF "v[104:107],v[180:183],a[44:47],v[104:107]\n"
"s_nop 0\n"
"ds_write_addtid_b32 v56\n"
"ds_write_addtid_b32 v57 offset:256\n"
MF "v[104:107],v[176:179],a[4:7],v[104:107]\n"
"s_nop 0\n"
"ds_write_addtid_b32 v58 offset:512\n"
"ds_write_addtid_b32 v59 offset:768\n"
MF "v[112:115],v[176:179],a[8:11],v[252:255]\n"
"s_nop 0\n"
"ds_write_addtid_b32 v60 offset:1024\n"
"ds_write_addtid_b32 v61 offset:1280\n"
MF "v[112:115],v[180:183],a[48:51],v[112:115]\n"
"s_nop 0\n"
"ds_write_addtid_b32 v62 offset:1536\n"
"ds_write_addtid_b32 v63 offset:1792\n"
MF "v[112:115],v[184:187],a[88:91],v[112:115]\n"
"s_nop 0\n"
"ds_write_addtid_b32 v64 offset:2048\n"
"ds_write_addtid_b32 v65 offset:2304\n"
MF "v[120:123],v[184:187],a[92:95],v[252:255]\n"
"s_nop 0\n"
"ds_write_addtid_b32 v66 offset:2560\n"
"ds_write_addtid_b32 v67 offset:2816\n"
MF "v[120:123],v[180:183],a[52:55],v[120:123]\n"
"s_nop 0\n"
"ds_write_addtid_b32 v68 offset:3072\n"
"ds_write_addtid_b32 v69 offset:3328\n"
MF "v[120:123],v[176:179],a[12:15],v[120:123]\n"
"s_nop 0\n"
"ds_write_addtid_b32 v70 offset:3584\n"
"ds_write_addtid_b32 v71 offset:3840\n"
MF "v[128:131],v[176:179],a[16:19],v[252:255]\n"
"s_nop 0\n"
"ds_write_addtid_b32 v72 offset:4096\n"
"ds_write_addtid_b32 v73 offset:4352\n"
MF "v[128:131],v[180:183],a[56:59],v[128:131]\n"
"s_nop 0\n"
"ds_write_addtid_b32 v74 offset:4608\n"
"ds_write_addtid_b32 v75 offset:4864\n"
MF "v[128:131],v[184:187],a[96:99],v[128:131]\n"
"s_waitcnt lgkmcnt(15)\n"
"ds_write_addtid_b32 v76 offset:5120\n"
MF "v[132:135],a[244:247],a[96:99],v[252:255]\n"
"s_nop 0\n"
TR "v[176:177],v2 offset:24576\n"
TR "v[178:179],v2 offset:26624\n"
MF "v[132:135],a[240:243],a[56:59],v[132:135]\n"
"s_nop 0\n"
TR "v[180:181],v2 offset:28672\n"
TR "v[182:183],v2 offset:30720\n"
MF "v[132:135],v[188:191],a[16:19],v[132:135]\n"
"s_nop 0\n"
TR "v[184:185],v2 offset:32768\n"
TR "v[186:187],v2 offset:34816\n"
MF "v[124:127],v[188:191],a[12:15],v[252:255]\n"
"s_nop 0\n"
"ds_write_addtid_b32 v77 offset:5376\n"
"ds_write_addtid_b32 v78 offset:5632\n"
MF "v[124:127],a[240:243],a[52:55],v[124:127]\n"
"s_nop 0\n"
"ds_write_addtid_b32 v79 offset:5888\n"
"ds_write_addtid_b32 v80 offset:6144\n"
MF "v[124:127],a[244:247],a[92:95],v[124:127]\n"
"s_nop 0\n"
"ds_write_addtid_b32 v81 offset:6400\n"
"ds_write_addtid_b32 v82 offset:6656\n"
MF "v[116:119],a[244:247],a[88:91],v[252:255]\n"
"s_nop 0\n"
"ds_write_addtid_b32 v83 offset:6912\n"
"ds_write_addtid_b32 v84 offset:7168\n"
MF "v[116:119],a[240:243],a[48:51],v[116:119]\n"
"s_nop 0\n"
"ds_write_addtid_b32 v85 offset:7424\n"
"ds_write_addtid_b32 v86 offset:7680\n"
MF "v[116:119],v[188:191],a[8:11],v[116:119]\n"
"s_nop 0\n"
"ds_write_addtid_b32 v87 offset:7936\n"
"ds_write_addtid_b32 v88 offset:8192\n"
MF "v[108:111],v[188:191],a[4:7],v[252:255]\n"
"s_nop 0\n"
"ds_write_addtid_b32 v89 offset:8448\n"
"ds_write_addtid_b32 v90 offset:8704\n"
MF "v[108:111],a[240:243],a[44:47],v[108:111]\n"
"s_nop 0\n"
"ds_write_addtid_b32 v91 offset:8960\n"
"ds_write_addtid_b32 v92 offset:9216\n"
MF "v[108:111],a[244:247],a[84:87],v[108:111]\n"
"s_nop 0\n"
"ds_write_addtid_b32 v93 offset:9472\n"
"ds_write_addtid_b32 v94 offset:9728\n"
MF "v[100:103],a[244:247],a[80:83],v[252:255]\n"
"ds_write_addtid_b32 v95 offset:9984\n"
MF "v[100:103],a[240:243],a[40:43],v[100:103]\n"
MF "v[100:103],v[188:191],a[0:3],v[100:103]\n"
"s_waitcnt lgkmcnt(15)\n"
MF "v[136:139],v[176:179],a[0:3],v[252:255]\n"
"s_nop 0\n"
TR "v[188:189],v3 offset:24576\n"
TR "v[190:191],v3 offset:26624\n"
MF "v[136:139],v[180:183],a[40:43],v[136:139]\n"
"s_nop 0\n"
TR "a[240:241],v3 offset:28672\n"
TR "a[242:243],v3 offset:30720\n"
MF "v[136:139],v[184:187],a[80:83],v[136:139]\n"
"s_nop 0\n"
TR "a[244:245],v3 offset:32768\n"
TR "a[246:247],v3 offset:34816\n"
"s_waitcnt lgkmcnt(0)\n"
MF "v[144:147],v[184:187],a[84:87],v[252:255]\n"
"s_barrier\n"
MF "v[144:147],v[180:183],a[44:47],v[144:147]\n"
"ds_read2st64_b32 v[56:57],v13 offset0:192 offset1:193\n"
MF "v[144:147],v[176:179],a[4:7],v[144:147]\n"
"ds_read2st64_b32 v[58:59],v13 offset0:194 offset1:195\n"
MF "v[152:155],v[176:179],a[8:11],v[252:255]\n"
"ds_read2st64_b32 v[60:61],v13 offset0:196 offset1:197\n"
MF "v[152:155],v[180:183],a[48:51],v[152:155]\n"
"ds_read2st64_b32 v[62:63],v13 offset0:198 offset1:199\n"
MF "v[152:155],v[184:187],a[88:91],v[152:155]\n"
"ds_read2st64_b32 v[64:65],v13 offset0:200 offset1:201\n"
MF "v[160:163],v[184:187],a[92:95],v[252:255]\n"
"ds_read2st64_b32 v[66:67],v13 offset0:202 offset1:203\n"
MF "v[160:163],v[180:183],a[52:55],v[160:163]\n"
"ds_read2st64_b32 v[68:69],v13 offset0:204 offset1:205\n"
MF "v[160:163],v[176:179],a[12:15],v[160:163]\n"
"ds_read2st64_b32 v[70:71],v13 offset0:206 offset1:207\n"
MF "v[168:171],v[176:179],a[16:19],v[252:255]\n"
"s_waitcnt lgkmcnt(4)\n"
"ds_read2st64_b32 v[72:73],v13 offset0:208 offset1:209\n"
"v_add_f32 v16,v16,v56\n"
MF "v[168:171],v[180:183],a[56:59],v[168:171]\n"
"ds_read2st64_b32 v[74:75],v13 offset0:210 offset1:211\n"
"s_waitcnt lgkmcnt(0)\n"
MF "v[168:171],v[184:187],a[96:99],v[168:171]\n"
"s_barrier\n"
MF "v[172:175],a[244:247],a[96:99],v[252:255]\n"
"s_nop 0\n"
TR "v[176:177],v0\n"
TR "v[178:179],v0 offset:2048\n"
MF "v[172:175],a[240:243],a[56:59],v[172:175]\n"
"s_nop 0\n"
TR "v[180:181],v0 offset:4096\n"
TR "v[182:183],v0 offset:6144\n"
MF "v[172:175],v[188:191],a[16:19],v[172:175]\n"
"s_nop 0\n"
TR "v[184:185],v0 offset:8192\n"
TR "v[186:187],v0 offset:10240\n"
MF "v[164:167],v[188:191],a[12:15],v[252:255]\n"
"ds_read2st64_b32 v[76:77],v13 offset0:212 offset1:213\n"
"v_add_f32 v17,v17,v57\n"
MF "v[164:167],a[240:243],a[52:55],v[164:167]\n"
"ds_read2st64_b32 v[78:79],v13 offset0:214 offset1:215\n"
"v_add_f32 v18,v18,v58\n"
MF "v[164:167],a[244:247],a[92:95],v[164:167]\n"
"ds_read2st64_b32 v[80:81],v13 offset0:216 offset1:217\n"
"v_add_f32 v19,v19,v59\n"
MF "v[156:159],a[244:247],a[88:91],v[252:255]\n"
"ds_read2st64_b32 v[82:83],v13 offset0:218 offset1:219\n"
"v_add_f32 v20,v20,v60\n"
MF "v[156:159],a[240:243],a[48:51],v[156:159]\n"
"ds_read2st64_b32 v[84:85],v13 offset0:220 offset1:221\n"
"v_add_f32 v21,v21,v61\n"
MF "v[156:159],v[188:191],a[8:11],v[156:159]\n"
"ds_read2st64_b32 v[86:87],v13 offset0:222 offset1:223\n"
"v_add_f32 v22,v22,v62\n"
MF "v[148:151],v[188:191],a[4:7],v[252:255]\n"
"s_waitcnt vmcnt(4)\n"
"v_mov_b32 v14,s76\n"
"s_add_u32 s76,s76,1\n"
MF "v[148:151],a[240:243],a[44:47],v[148:151]\n"
"buffer_store_dword v14,v15,s[40:43],s94 offen sc1\n"
MF "v[148:151],a[244:247],a[84:87],v[148:151]\n"
"ds_read2st64_b32 v[88:89],v13 offset0:224 offset1:225\n"
"v_add_f32 v23,v23,v63\n"
MF "v[140:143],a[244:247],a[80:83],v[252:255]\n"
"ds_read2st64_b32 v[90:91],v13 offset0:226 offset1:227\n"
CV "v16,v16,v17\n"
MF "v[140:143],a[240:243],a[40:43],v[140:143]\n"
".Lpg37h:\n"
"s_cbranch_execz .Lisr4\n"
"ds_read2st64_b32 v[92:93],v13 offset0:228 offset1:229\n"
CV "v17,v18,v19\n"
MF "v[140:143],v[188:191],a[0:3],v[140:143]\n"
"s_waitcnt lgkmcnt(9)\n"
"ds_read2st64_b32 v[94:95],v13 offset0:230 offset1:231\n"
CV "v18,v20,v21\n"
MF "v[96:99],v[176:179],a[120:123],v[96:99]\n"
"s_nop 0\n"
TR "v[188:189],v1\n"
TR "v[190:191],v1 offset:2048\n"
MF "v[96:99],v[180:183],a[160:163],v[96:99]\n"
"s_nop 0\n"
TR "a[240:241],v1 offset:4096\n"
TR "a[242:243],v1 offset:6144\n"
MF "v[96:99],v[184:187],a[200:203],v[96:99]\n"
"s_nop 0\n"
TR "a[244:245],v1 offset:8192\n"
TR "a[246:247],v1 offset:10240\n"
MF "v[104:107],v[184:187],a[204:207],v[104:107]\n"
"s_add_u32 s87,s90,0\n"
CV "v19,v22,v23\n"
MF "v[104:107],v[180:183],a[164:167],v[104:107]\n"
"buffer_store_dwordx4 v[16:19],v9,s[40:43],s87 offen sc1\n"
MF "v[104:107],v[176:179],a[124:127],v[104:107]\n"
"v_add_f32 v24,v24,v64\n"
"v_add_f32 v25,v25,v65\n"
MF "v[112:115],v[176:179],a[128:131],v[112:115]\n"
"v_add_f32 v26,v26,v66\n"
"v_add_f32 v27,v27,v67\n"
MF "v[112:115],v[180:183],a[168:171],v[112:115]\n"
"v_add_f32 v28,v28,v68\n"
"v_add_f32 v29,v29,v69\n"
MF "v[112:115],v[184:187],a[208:211],v[112:115]\n"
"v_add_f32 v30,v30,v70\n"
"v_add_f32 v31,v31,v71\n"
MF "v[120:123],v[184:187],a[212:215],v[120:123]\n"
CV "v24,v24,v25\n"
CV "v25,v26,v27\n"
MF "v[120:123],v[180:183],a[172:175],v[120:123]\n"
"s_add_u32 s87,s90,1024\n"
CV "v26,v28,v29\n"
CV "v27,v30,v31\n"
MF "v[120:123],v[176:179],a[132:135],v[120:123]\n"
"s_waitcnt lgkmcnt(14)\n"
"buffer_store_dwordx4 v[24:27],v9,s[40:43],s87 offen sc1\n"
MF "v[128:131],v[176:179],a[136:139],v[128:131]\n"
"v_add_f32 v32,v32,v72\n"
"v_add_f32 v33,v33,v73\n"
MF "v[128:131],v[180:183],a[176:179],v[128:131]\n"
"v_add_f32 v34,v34,v74\n"
"v_add_f32 v35,v35,v75\n"
MF "v[128:131],v[184:187],a[216:219],v[128:131]\n"
"s_waitcnt lgkmcnt(0)\n"
"v_add_f32 v36,v36,v76\n"
"v_add_f32 v37,v37,v77\n"
MF "v[132:135],a[244:247],a[216:219],v[132:135]\n"
"s_nop 0\n"
TR "v[176:177],v2\n"
TR "v[178:179],v2 offset:2048\n"
MF "v[132:135],a[240:243],a[176:179],v[132:135]\n"
"s_nop 0\n"
TR "v[180:181],v2 offset:4096\n"
TR "v[182:183],v2 offset:6144\n"
MF "v[132:135],v[188:191],a[136:139],v[132:135]\n"
"s_nop 0\n"
TR "v[184:185],v2 offset:8192\n"
TR "v[186:187],v2 offset:10240\n"
MF "v[124:127],v[188:191],a[132:135],v[124:127]\n"
"v_add_f32 v38,v38,v78\n"
"v_add_f32 v39,v39,v79\n"
MF "v[124:127],a[240:243],a[172:175],v[124:127]\n"
CV "v32,v32,v33\n"
CV "v33,v34,v35\n"
MF "v[124:127],a[244:247],a[212:215],v[124:127]\n"
"s_add_u32 s87,s90,2048\n"
CV "v34,v36,v37\n"
CV "v35,v38,v39\n"
MF "v[116:119],a[244:247],a[208:211],v[116:119]\n"
"buffer_store_dwordx4 v[32:35],v9,s[40:43],s87 offen sc1\n"
MF "v[116:119],a[240:243],a[168:171],v[116:119]\n"
"v_add_f32 v40,v40,v80\n"
"v_add_f32 v41,v41,v81\n"
MF "v[116:119],v[188:191],a[128:131],v[116:119]\n"
"v_add_f32 v42,v42,v82\n"
"v_add_f32 v43,v43,v83\n"
MF "v[108:111],v[188:191],a[124:127],v[108:111]\n"
"v_add_f32 v44,v44,v84\n"
"v_add_f32 v45,v45,v85\n"
MF "v[108:111],a[240:243],a[164:167],v[108:111]\n"
"v_add_f32 v46,v46,v86\n"
"v_add_f32 v47,v47,v87\n"
MF "v[108:111],a[244:247],a[204:207],v[108:111]\n"
CV "v40,v40,v41\n"
CV "v41,v42,v43\n"
MF "v[100:103],a[244:247],a[200:203],v[100:103]\n"
"s_add_u32 s87,s90,3072\n"
CV "v42,v44,v45\n"
CV "v43,v46,v47\n"
MF "v[100:103],a[240:243],a[160:163],v[100:103]\n"
"buffer_store_dwordx4 v[40:43],v9,s[40:43],s87 offen sc1\n"
MF "v[100:103],v[188:191],a[120:123],v[100:103]\n"
"s_waitcnt lgkmcnt(0)\n"
"v_add_f32 v48,v48,v88\n"
"v_add_f32 v49,v49,v89\n"
MF "v[136:139],v[176:179],a[120:123],v[136:139]\n"
"s_nop 0\n"
TR "v[188:189],v3\n"
TR "v[190:191],v3 offset:2048\n"
MF "v[136:139],v[180:183],a[160:163],v[136:139]\n"
"s_nop 0\n"
TR "a[240:241],v3 offset:4096\n"
TR "a[242:243],v3 offset:6144\n"
MF "v[136:139],v[184:187],a[200:203],v[136:139]\n"
"s_nop 0\n"
TR "a[244:245],v3 offset:8192\n"
TR "a[246:247],v3 offset:10240\n"
MF "v[144:147],v[184:187],a[204:207],v[144:147]\n"
"v_add_f32 v50,v50,v90\n"
"v_add_f32 v51,v51,v91\n"
MF "v[144:147],v[180:183],a[164:167],v[144:147]\n"
"v_add_f32 v52,v52,v92\n"
"v_add_f32 v53,v53,v93\n"
MF "v[144:147],v[176:179],a[124:127],v[144:147]\n"
"v_add_f32 v54,v54,v94\n"
"v_add_f32 v55,v55,v95\n"
MF "v[152:155],v[176:179],a[128:131],v[152:155]\n"
CV "v48,v48,v49\n"
CV "v49,v50,v51\n"
MF "v[152:155],v[180:183],a[168:171],v[152:155]\n"
"s_add_u32 s87,s90,4096\n"
CV "v50,v52,v53\n"
CV "v51,v54,v55\n"
MF "v[152:155],v[184:187],a[208:211],v[152:155]\n"
"buffer_store_dwordx4 v[48:51],v9,s[40:43],s87 offen sc1\n"
MF "v[160:163],v[184:187],a[212:215],v[160:163]\n"
MF "v[160:163],v[180:183],a[172:175],v[160:163]\n"
MF "v[160:163],v[176:179],a[132:135],v[160:163]\n"
MF "v[168:171],v[176:179],a[136:139],v[168:171]\n"
MF "v[168:171],v[180:183],a[176:179],v[168:171]\n"
"s_waitcnt lgkmcnt(0)\n"
MF "v[168:171],v[184:187],a[216:219],v[168:171]\n"
"s_barrier\n"
MF "v[172:175],a[244:247],a[216:219],v[172:175]\n"
"s_nop 0\n"
TR "v[176:177],v0 offset:49152\n"
TR "v[178:179],v0 offset:51200\n"
MF "v[172:175],a[240:243],a[176:179],v[172:175]\n"
"s_nop 0\n"
TR "v[180:181],v0 offset:53248\n"
TR "v[182:183],v0 offset:55296\n"
MF "v[172:175],v[188:191],a[136:139],v[172:175]\n"
"s_nop 0\n"
TR "v[184:185],v0 offset:57344\n"
TR "v[186:187],v0 offset:59392\n"
MF "v[164:167],v[188:191],a[132:135],v[164:167]\n"
"buffer_load_dword v10,v12,s[40:43],s86 offen sc1\n"
MF "v[164:167],a[240:243],a[172:175],v[164:167]\n"
MF "v[164:167],a[244:247],a[212:215],v[164:167]\n"
LD "a[0:3],v9,s[40:43],s3 offen sc1\n"
MF "v[156:159],a[244:247],a[208:211],v[156:159]\n"
MF "v[156:159],a[240:243],a[168:171],v[156:159]\n"
MF "v[156:159],v[188:191],a[128:131],v[156:159]\n"
MF "v[148:151],v[188:191],a[124:127],v[148:151]\n"
MF "v[148:151],a[240:243],a[164:167],v[148:151]\n"
"s_add_u32 s3,s3,204800\n"
MF "v[148:151],a[244:247],a[204:207],v[148:151]\n"
LD "a[4:7],v9,s[40:43],s3 offen sc1\n"
MF "v[140:143],a[244:247],a[200:203],v[140:143]\n"
MF "v[140:143],a[240:243],a[160:163],v[140:143]\n"
MF "v[140:143],v[188:191],a[120:123],v[140:143]\n"
"s_waitcnt lgkmcnt(0)\n"
MF "v[96:99],v[176:179],a[20:23],v[96:99]\n"
"s_nop 0\n"
TR "v[188:189],v1 offset:49152\n"
TR "v[190:191],v1 offset:51200\n"
MF "v[96:99],v[180:183],a[60:63],v[96:99]\n"
"s_nop 0\n"
TR "a[240:241],v1 offset:53248\n"
TR "a[242:243],v1 offset:55296\n"
MF "v[96:99],v[184:187],a[100:103],v[96:99]\n"
"s_add_u32 s3,s3,204800\n"
TR "a[244:245],v1 offset:57344\n"
TR "a[246:247],v1 offset:59392\n"
MF "v[104:107],v[184:187],a[104:107],v[104:107]\n"
LD "a[8:11],v9,s[40:43],s3 offen sc1\n"
MF "v[104:107],v[180:183],a[64:67],v[104:107]\n"
MF "v[104:107],v[176:179],a[24:27],v[104:107]\n"
"s_waitcnt vmcnt(10)\n"
"v_mov_b32 v236,v228\n"
"v_mov_b32 v237,v229\n"
MF "v[136:139],v[208:211],a[252:255],v[136:139]\n"
MF "v[144:147],v[212:215],a[252:255],v[144:147]\n"
MF "v[152:155],v[216:219],a[252:255],v[152:155]\n"
MF "v[160:163],v[220:223],a[252:255],v[160:163]\n"
MF "v[168:171],v[224:227],a[252:255],v[168:171]\n"
MF "v[140:143],v[208:211],a[248:251],v[140:143]\n"
MF "v[148:151],v[212:215],a[248:251],v[148:151]\n"
MF "v[156:159],v[216:219],a[248:251],v[156:159]\n"
MF "v[164:167],v[220:223],a[248:251],v[164:167]\n"
MF "v[172:175],v[224:227],a[248:251],v[172:175]\n"
MF "v[248:251],v[230:233],a[250:253],v[248:251]\n"
MF "v[244:247],v[234:237],a[250:253],v[244:247]\n"
MF "v[112:115],v[176:179],a[28:31],v[112:115]\n"
MF "v[112:115],v[180:183],a[68:71],v[112:115]\n"
"s_add_u32 s3,s3,204800\n"
MF "v[112:115],v[184:187],a[108:111],v[112:115]\n"
LD "a[12:15],v9,s[40:43],s3 offen sc1\n"
MF "v[120:123],v[184:187],a[112:115],v[120:123]\n"
"s_nop 1\n"
CV "v244,v244,v245\n"
CV "v245,v246,v247\n"
MF "v[120:123],v[180:183],a[72:75],v[120:123]\n"
"s_bfe_u32 s87,s22,0x30014\n"
CV "v246,v248,v249\n"
CV "v247,v250,v251\n"
MF "v[120:123],v[176:179],a[32:35],v[120:123]\n"
"s_lshl_b32 s87,s87,7\n"
"v_permlane16_swap_b32 v244,v246\n"
"v_permlane16_swap_b32 v245,v247\n"
MF "v[128:131],v[176:179],a[36:39],v[128:131]\n"
"buffer_store_dwordx4 v[244:247],v8,s[44:47],s0 offen sc1\n"
MF "v[128:131],v[180:183],a[76:79],v[128:131]\n"
"s_add_u32 s3,s3,204800\n"
MF "v[128:131],v[184:187],a[116:119],v[128:131]\n"
"s_waitcnt lgkmcnt(0)\n"
LD "a[40:43],v9,s[40:43],s3 offen sc1\n"
MF "v[132:135],a[244:247],a[116:119],v[132:135]\n"
"s_and_b32 s60,s56,1\n"
TR "v[176:177],v2 offset:49152\n"
TR "v[178:179],v2 offset:51200\n"
MF "v[132:135],a[240:243],a[76:79],v[132:135]\n"
"s_mul_i32 s60,s60,204800\n"
TR "v[180:181],v2 offset:53248\n"
TR "v[182:183],v2 offset:55296\n"
MF "v[132:135],v[188:191],a[36:39],v[132:135]\n"
"s_add_u32 s87,s87,s60\n"
TR "v[184:185],v2 offset:57344\n"
TR "v[186:187],v2 offset:59392\n"
MF "v[124:127],v[188:191],a[32:35],v[124:127]\n"
"s_mov_b32 s71,204800\n"
MF "v[124:127],a[240:243],a[72:75],v[124:127]\n"
"s_mov_b32 s66,204800\n"
MF "v[124:127],a[244:247],a[112:115],v[124:127]\n"
"s_add_u32 s72,s78,2\n"
MF "v[116:119],a[244:247],a[108:111],v[116:119]\n"
"s_add_u32 s3,s3,204800\n"
MF "v[116:119],a[240:243],a[68:71],v[116:119]\n"
LD "a[44:47],v9,s[40:43],s3 offen sc1\n"
MF "v[116:119],v[188:191],a[28:31],v[116:119]\n"
"s_and_b32 s72,s72,15\n"
MF "v[108:111],v[188:191],a[24:27],v[108:111]\n"
"s_add_u32 s16,s78,6\n"
MF "v[108:111],a[240:243],a[64:67],v[108:111]\n"
"s_and_b32 s16,s16,15\n"
MF "v[108:111],a[244:247],a[104:107],v[108:111]\n"
"s_add_u32 s17,s78,4\n"
MF "v[100:103],a[244:247],a[100:103],v[100:103]\n"
"s_and_b32 s17,s17,15\n"
MF "v[100:103],a[240:243],a[60:63],v[100:103]\n"
"s_add_u32 s3,s3,204800\n"
MF "v[100:103],v[188:191],a[20:23],v[100:103]\n"
"s_waitcnt lgkmcnt(0)\n"
LD "a[48:51],v9,s[40:43],s3 offen sc1\n"
MF "v[136:139],v[176:179],a[20:23],v[136:139]\n"
"s_bfe_u32 s60,s22,0x10014\n"
TR "v[188:189],v3 offset:49152\n"
TR "v[190:191],v3 offset:51200\n"
MF "v[136:139],v[180:183],a[60:63],v[136:139]\n"
"s_lshl_b32 s60,s60,5\n"
TR "a[240:241],v3 offset:53248\n"
TR "a[242:243],v3 offset:55296\n"
MF "v[136:139],v[184:187],a[100:103],v[136:139]\n"
"s_nop 0\n"
TR "a[244:245],v3 offset:57344\n"
TR "a[246:247],v3 offset:59392\n"
MF "v[144:147],v[184:187],a[104:107],v[144:147]\n"
"v_xor_b32 v242,s60,v8\n"
MF "v[144:147],v[180:183],a[64:67],v[144:147]\n"
"s_add_u32 s3,s3,204800\n"
"v_mov_b32 v14,s76\n"
"s_add_u32 s76,s76,1\n"
MF "v[144:147],v[176:179],a[24:27],v[144:147]\n"
LD "a[52:55],v9,s[40:43],s3 offen sc1\n"
MF "v[152:155],v[176:179],a[28:31],v[152:155]\n"
MF "v[152:155],v[180:183],a[68:71],v[152:155]\n"
MF "v[152:155],v[184:187],a[108:111],v[152:155]\n"
MF "v[160:163],v[184:187],a[112:115],v[160:163]\n"
MF "v[160:163],v[180:183],a[72:75],v[160:163]\n"
MF "v[160:163],v[176:179],a[32:35],v[160:163]\n"
MF "v[168:171],v[176:179],a[36:39],v[168:171]\n"
MF "v[168:171],v[180:183],a[76:79],v[168:171]\n"
"s_waitcnt vmcnt(9) lgkmcnt(0)\n"
MF "v[168:171],v[184:187],a[116:119],v[168:171]\n"
"s_barrier\n"
MF "v[172:175],a[244:247],a[116:119],v[172:175]\n"
"buffer_store_dword v14,v15,s[40:43],s94 offen sc1\n"
MF "v[172:175],a[240:243],a[76:79],v[172:175]\n"
"s_nop 0\n"
TR "v[176:177],v6\n"
TR "v[178:179],v6 offset:2048\n"
MF "v[172:175],v[188:191],a[36:39],v[172:175]\n"
"s_nop 0\n"
TR "v[180:181],v6 offset:4096\n"
TR "v[182:183],v6 offset:6144\n"
MF "v[164:167],v[188:191],a[32:35],v[164:167]\n"
"s_nop 0\n"
TR "v[184:185],v6 offset:8192\n"
TR "v[186:187],v6 offset:10240\n"
MF "v[164:167],a[240:243],a[72:75],v[164:167]\n"
"v_sub_u32 v10,v10,v11\n"
MF "v[164:167],a[244:247],a[112:115],v[164:167]\n"
"v_readlane_b32 s16,v10,s16\n"
MF "v[156:159],a[244:247],a[108:111],v[156:159]\n"
"s_cmp_lt_u32 s16,9\n"
"s_cselect_b32 s16,0xf0000000,0\n"
MF "v[156:159],a[240:243],a[68:71],v[156:159]\n"
"v_readlane_b32 s17,v10,s17\n"
MF "v[156:159],v[188:191],a[28:31],v[156:159]\n"
"s_cmp_lt_u32 s17,10\n"
"s_cselect_b32 s17,0xf0000000,0\n"
MF "v[148:151],v[188:191],a[24:27],v[148:151]\n"
"s_bfe_u32 s62,s22,0x30014\n"
MF "v[148:151],a[240:243],a[64:67],v[148:151]\n"
"s_add_u32 s62,s62,3\n"
"s_and_b32 s62,s62,7\n"
"s_mul_i32 s62,s62,204800\n"
MF "v[148:151],a[244:247],a[104:107],v[148:151]\n"
"s_add_u32 s62,s62,s73\n"
"s_add_u32 s62,s62,122880\n"
"s_or_b32 s62,s62,s16\n"
MF "v[140:143],a[244:247],a[100:103],v[140:143]\n"
LD "v[56:59],v9,s[40:43],s62 offen sc1\n"
MF "v[140:143],a[240:243],a[60:63],v[140:143]\n"
LD "v[64:67],v9,s[40:43],s62 offen offset:1024 sc1\n"
MF "v[140:143],v[188:191],a[20:23],v[140:143]\n"
"s_waitcnt lgkmcnt(0)\n"
LD "v[72:75],v9,s[40:43],s62 offen offset:2048 sc1\n"
MF "v[136:139],v[176:179],a[140:143],v[136:139]\n"
"s_nop 0\n"
TR "v[188:189],v7\n"
TR "v[190:191],v7 offset:2048\n"
MF "v[136:139],v[180:183],a[180:183],v[136:139]\n"
"s_nop 0\n"
TR "a[240:241],v7 offset:4096\n"
TR "a[242:243],v7 offset:6144\n"
MF "v[136:139],v[184:187],a[220:223],v[136:139]\n"
"s_nop 0\n"
TR "a[244:245],v7 offset:8192\n"
TR "a[246:247],v7 offset:10240\n"
MF "v[144:147],v[184:187],a[224:227],v[144:147]\n"
LD "v[80:83],v9,s[40:43],s62 offen offset:3072 sc1\n"
MF "v[144:147],v[180:183],a[184:187],v[144:147]\n"
MF "v[144:147],v[176:179],a[144:147],v[144:147]\n"
"s_add_u32 s62,s62,4096\n"
MF "v[152:155],v[176:179],a[148:151],v[152:155]\n"
LD "v[88:91],v9,s[40:43],s62 offen sc1\n"
MF "v[152:155],v[180:183],a[188:191],v[152:155]\n"
"s_bfe_u32 s62,s22,0x30014\n"
"ds_write_addtid_b32 v136\n"
"ds_write_addtid_b32 v137 offset:256\n"
MF "v[152:155],v[184:187],a[228:231],v[152:155]\n"
"s_add_u32 s62,s62,2\n"
"s_and_b32 s62,s62,7\n"
"s_mul_i32 s62,s62,204800\n"
MF "v[160:163],v[184:187],a[232:235],v[160:163]\n"
"s_add_u32 s62,s62,s73\n"
"s_add_u32 s62,s62,143360\n"
"s_or_b32 s62,s62,s17\n"
MF "v[160:163],v[180:183],a[192:195],v[160:163]\n"
LD "v[60:63],v9,s[40:43],s62 offen sc1\n"
MF "v[160:163],v[176:179],a[152:155],v[160:163]\n"
"s_nop 0\n"
"ds_write_addtid_b32 v138 offset:512\n"
"ds_write_addtid_b32 v139 offset:768\n"
MF "v[168:171],v[176:179],a[156:159],v[168:171]\n"
LD "v[68:71],v9,s[40:43],s62 offen offset:1024 sc1\n"
MF "v[168:171],v[180:183],a[196:199],v[168:171]\n"
"s_nop 0\n"
"ds_write_addtid_b32 v144 offset:2048\n"
"ds_write_addtid_b32 v145 offset:2304\n"
MF "v[168:171],v[184:187],a[236:239],v[168:171]\n"
"s_waitcnt lgkmcnt(6)\n"
LD "v[76:79],v9,s[40:43],s62 offen offset:2048 sc1\n"
MF "v[140:143],v[188:191],a[140:143],v[140:143]\n"
"s_nop 0\n"
TR "v[176:177],v4\n"
TR "v[178:179],v4 offset:2048\n"
MF "v[140:143],a[240:243],a[180:183],v[140:143]\n"
"s_nop 0\n"
TR "v[180:181],v4 offset:4096\n"
TR "v[182:183],v4 offset:6144\n"
MF "v[140:143],a[244:247],a[220:223],v[140:143]\n"
"s_nop 0\n"
TR "v[184:185],v4 offset:8192\n"
TR "v[186:187],v4 offset:10240\n"
MF "v[148:151],a[244:247],a[224:227],v[148:151]\n"
LD "v[84:87],v9,s[40:43],s62 offen offset:3072 sc1\n"
MF "v[148:151],a[240:243],a[184:187],v[148:151]\n"
"s_nop 0\n"
"ds_write_addtid_b32 v146 offset:2560\n"
"ds_write_addtid_b32 v147 offset:2816\n"
MF "v[148:151],v[188:191],a[144:147],v[148:151]\n"
"s_add_u32 s62,s62,4096\n"
"ds_write_addtid_b32 v152 offset:4096\n"
"ds_write_addtid_b32 v153 offset:4352\n"
MF "v[156:159],v[188:191],a[148:151],v[156:159]\n"
LD "v[92:95],v9,s[40:43],s62 offen sc1\n"
MF "v[156:159],a[240:243],a[188:191],v[156:159]\n"
"s_nop 0\n"
"ds_write_addtid_b32 v154 offset:4608\n"
"ds_write_addtid_b32 v155 offset:4864\n"
MF "v[156:159],a[244:247],a[228:231],v[156:159]\n"
"s_nop 0\n"
"ds_write_addtid_b32 v160 offset:6144\n"
"ds_write_addtid_b32 v161 offset:6400\n"
MF "v[164:167],a[244:247],a[232:235],v[164:167]\n"
"s_nop 0\n"
"ds_write_addtid_b32 v162 offset:6656\n"
"ds_write_addtid_b32 v163 offset:6912\n"
MF "v[164:167],a[240:243],a[192:195],v[164:167]\n"
"s_nop 0\n"
"ds_write_addtid_b32 v168 offset:8192\n"
"ds_write_addtid_b32 v169 offset:8448\n"
MF "v[164:167],v[188:191],a[152:155],v[164:167]\n"
"buffer_load_dword v10,v12,s[40:43],s86 offen sc1\n"
MF "v[172:175],v[188:191],a[156:159],v[172:175]\n"
"s_nop 0\n"
"ds_write_addtid_b32 v170 offset:8704\n"
"ds_write_addtid_b32 v171 offset:8960\n"
MF "v[172:175],a[240:243],a[196:199],v[172:175]\n"
"s_nop 0\n"
"ds_write_addtid_b32 v140 offset:1024\n"
"ds_write_addtid_b32 v141 offset:1280\n"
MF "v[172:175],a[244:247],a[236:239],v[172:175]\n"
"s_waitcnt lgkmcnt(15)\n"
"ds_write_addtid_b32 v142 offset:1536\n"
"ds_write_addtid_b32 v143 offset:1792\n"
MF "v[96:99],v[176:179],a[140:143],v[96:99]\n"
"s_nop 0\n"
TR "v[188:189],v5\n"
TR "v[190:191],v5 offset:2048\n"
MF "v[96:99],v[180:183],a[180:183],v[96:99]\n"
"s_nop 0\n"
TR "a[240:241],v5 offset:4096\n"
TR "a[242:243],v5 offset:6144\n"
MF "v[96:99],v[184:187],a[220:223],v[96:99]\n"
"s_nop 0\n"
TR "a[244:245],v5 offset:8192\n"
TR "a[246:247],v5 offset:10240\n"
MF "v[104:107],v[184:187],a[224:227],v[104:107]\n"
"s_nop 0\n"
"ds_write_addtid_b32 v148 offset:3072\n"
"ds_write_addtid_b32 v149 offset:3328\n"
MF "v[104:107],v[180:183],a[184:187],v[104:107]\n"
"s_nop 0\n"
"ds_write_addtid_b32 v150 offset:3584\n"
"ds_write_addtid_b32 v151 offset:3840\n"
MF "v[104:107],v[176:179],a[144:147],v[104:107]\n"
"s_nop 0\n"
"ds_write_addtid_b32 v156 offset:5120\n"
"ds_write_addtid_b32 v157 offset:5376\n"
MF "v[112:115],v[176:179],a[148:151],v[112:115]\n"
"s_nop 0\n"
"ds_write_addtid_b32 v158 offset:5632\n"
"ds_write_addtid_b32 v159 offset:5888\n"
MF "v[112:115],v[180:183],a[188:191],v[112:115]\n"
"ds_write_addtid_b32 v164 offset:7168\n"
MF "v[112:115],v[184:187],a[228:231],v[112:115]\n"
"ds_write_addtid_b32 v165 offset:7424\n"
MF "v[120:123],v[184:187],a[232:235],v[120:123]\n"
"ds_write_addtid_b32 v166 offset:7680\n"
MF "v[120:123],v[180:183],a[192:195],v[120:123]\n"
"ds_write_addtid_b32 v167 offset:7936\n"
MF "v[120:123],v[176:179],a[152:155],v[120:123]\n"
"ds_write_addtid_b32 v172 offset:9216\n"
MF "v[128:131],v[176:179],a[156:159],v[128:131]\n"
"ds_write_addtid_b32 v173 offset:9472\n"
MF "v[128:131],v[180:183],a[196:199],v[128:131]\n"
"ds_write_addtid_b32 v174 offset:9728\n"
MF "v[128:131],v[184:187],a[236:239],v[128:131]\n"
"s_waitcnt lgkmcnt(15)\n"
"ds_write_addtid_b32 v175 offset:9984\n"
MF "v[100:103],v[188:191],a[140:143],v[100:103]\n"
MF "v[100:103],a[240:243],a[180:183],v[100:103]\n"
MF "v[100:103],a[244:247],a[220:223],v[100:103]\n"
MF "v[108:111],a[244:247],a[224:227],v[108:111]\n"
"s_waitcnt lgkmcnt(0)\n"
MF "v[108:111],a[240:243],a[184:187],v[108:111]\n"
"s_barrier\n"
MF "v[108:111],v[188:191],a[144:147],v[108:111]\n"
"ds_read2st64_b32 v[16:17],v13 offset0:192 offset1:193\n"
MF "v[116:119],v[188:191],a[148:151],v[116:119]\n"
"ds_read2st64_b32 v[18:19],v13 offset0:194 offset1:195\n"
MF "v[116:119],a[240:243],a[188:191],v[116:119]\n"
"ds_read2st64_b32 v[20:21],v13 offset0:196 offset1:197\n"
MF "v[116:119],a[244:247],a[228:231],v[116:119]\n"
"ds_read2st64_b32 v[22:23],v13 offset0:198 offset1:199\n"
MF "v[124:127],a[244:247],a[232:235],v[124:127]\n"
"ds_read2st64_b32 v[24:25],v13 offset0:200 offset1:201\n"
MF "v[124:127],a[240:243],a[192:195],v[124:127]\n"
"ds_read2st64_b32 v[26:27],v13 offset0:202 offset1:203\n"
MF "v[124:127],v[188:191],a[152:155],v[124:127]\n"
"ds_read2st64_b32 v[28:29],v13 offset0:204 offset1:205\n"
MF "v[132:135],v[188:191],a[156:159],v[132:135]\n"
"ds_read2st64_b32 v[30:31],v13 offset0:206 offset1:207\n"
MF "v[132:135],a[240:243],a[196:199],v[132:135]\n"
"ds_read2st64_b32 v[32:33],v13 offset0:208 offset1:209\n"
MF "v[132:135],a[244:247],a[236:239],v[132:135]\n"
"ds_read2st64_b32 v[34:35],v13 offset0:210 offset1:211\n"
"ds_read2st64_b32 v[36:37],v13 offset0:212 offset1:213\n"
"ds_read2st64_b32 v[38:39],v13 offset0:214 offset1:215\n"
"s_nop 0\n"
"ds_read2st64_b32 v[40:41],v13 offset0:216 offset1:217\n"
"ds_read2st64_b32 v[42:43],v13 offset0:218 offset1:219\n"
"ds_read2st64_b32 v[44:45],v13 offset0:220 offset1:221\n"
"ds_read2st64_b32 v[46:47],v13 offset0:222 offset1:223\n"
"ds_read2st64_b32 v[48:49],v13 offset0:224 offset1:225\n"
"ds_read2st64_b32 v[50:51],v13 offset0:226 offset1:227\n"
"ds_read2st64_b32 v[52:53],v13 offset0:228 offset1:229\n"
"ds_read2st64_b32 v[54:55],v13 offset0:230 offset1:231\n"
"s_waitcnt vmcnt(0)\n"
"v_sub_u32 v10,v10,v11\n"
"s_cmp_eq_u32 s16,0\n"
"s_cbranch_scc1 .Lhave13\n"
"s_add_u32 s59,s78,6\n"
"s_and_b32 s59,s59,15\n"
"s_nop 3\n"
".Lnf9:\n"
"v_readlane_b32 s58,v10,s59\n"
"s_cmp_lt_u32 s58,9\n"
"s_cbranch_scc1 .Lsp9\n"
".Lnb9:\n"
"s_bfe_u32 s62,s22,0x30014\n"
"s_add_u32 s62,s62,3\n"
"s_and_b32 s62,s62,7\n"
"s_mul_i32 s62,s62,204800\n"
"s_add_u32 s62,s62,s73\n"
"s_add_u32 s62,s62,122880\n"
LD "v[56:59],v9,s[40:43],s62 offen sc1\n"
LD "v[64:67],v9,s[40:43],s62 offen offset:1024 sc1\n"
LD "v[72:75],v9,s[40:43],s62 offen offset:2048 sc1\n"
LD "v[80:83],v9,s[40:43],s62 offen offset:3072 sc1\n"
"s_add_u32 s62,s62,4096\n"
LD "v[88:91],v9,s[40:43],s62 offen sc1\n"
".Lhave13:\n"
"s_cmp_eq_u32 s17,0\n"
"s_cbranch_scc1 .Lhave12\n"
"s_add_u32 s59,s78,4\n"
"s_and_b32 s59,s59,15\n"
"s_nop 3\n"
".Lnf10:\n"
"v_readlane_b32 s58,v10,s59\n"
"s_cmp_lt_u32 s58,10\n"
"s_cbranch_scc1 .Lsp10\n"
".Lnb10:\n"
"s_bfe_u32 s62,s22,0x30014\n"
"s_add_u32 s62,s62,2\n"
"s_and_b32 s62,s62,7\n"
"s_mul_i32 s62,s62,204800\n"
"s_add_u32 s62,s62,s73\n"
"s_add_u32 s62,s62,143360\n"
LD "v[60:63],v9,s[40:43],s62 offen sc1\n"
LD "v[68:71],v9,s[40:43],s62 offen offset:1024 sc1\n"
LD "v[76:79],v9,s[40:43],s62 offen offset:2048 sc1\n"
LD "v[84:87],v9,s[40:43],s62 offen offset:3072 sc1\n"
"s_add_u32 s62,s62,4096\n"
LD "v[92:95],v9,s[40:43],s62 offen sc1\n"
".Lhave12:\n"
".Lnf11:\n"
"v_readlane_b32 s58,v10,s72\n"
"s_cmp_lt_u32 s58,11\n"
"s_cbranch_scc1 .Lsp11\n"
".Lnb11:\n"
"s_bfe_u32 s62,s22,0x30014\n"
"s_add_u32 s62,s62,1\n"
"s_and_b32 s62,s62,7\n"
"s_mul_i32 s62,s62,204800\n"
"s_add_u32 s62,s62,s73\n"
"s_nop 0\n"
"s_add_u32 s62,s62,163840\n"
LD "v[208:211],v9,s[40:43],s62 offen sc1\n"
LD "v[212:215],v9,s[40:43],s62 offen offset:1024 sc1\n"
LD "v[216:219],v9,s[40:43],s62 offen offset:2048 sc1\n"
LD "v[220:223],v9,s[40:43],s62 offen offset:3072 sc1\n"
"s_add_u32 s62,s62,4096\n"
LD "v[224:227],v9,s[40:43],s62 offen sc1\n"
"s_cmp_eq_u32 s1,0xf0000000\n"
"s_cbranch_scc1 .Ljx1\n"
"v_accvgpr_mov_b32 a16,a0\n"
"v_accvgpr_mov_b32 a17,a1\n"
"v_accvgpr_mov_b32 a56,a40\n"
"v_accvgpr_mov_b32 a57,a41\n"
MF "v[196:199],a[2:5],a[250:253],0\n"
MF "v[192:195],a[6:9],a[250:253],0\n"
MF "v[196:199],a[10:13],a[250:253],v[196:199]\n"
".Lpg38h:\n"
"s_cbranch_execz .Lisr4\n"
MF "v[192:195],a[14:17],a[250:253],v[192:195]\n"
MF "v[196:199],a[42:45],a[250:253],v[196:199]\n"
MF "v[192:195],a[46:49],a[250:253],v[192:195]\n"
MF "v[196:199],a[50:53],a[250:253],v[196:199]\n"
MF "v[192:195],a[54:57],a[250:253],v[192:195]\n"
"s_nop 7\n"
CV "v192,v192,v193\n"
CV "v193,v194,v195\n"
CV "v194,v196,v197\n"
CV "v195,v198,v199\n"
"s_nop 0\n"
"v_permlane16_swap_b32 v192,v194\n"
"v_permlane16_swap_b32 v193,v195\n"
"buffer_store_dwordx4 v[192:195],v8,s[44:47],s1 offen sc1\n"
".Ljx1:\n"
"s_waitcnt lgkmcnt(15)\n"
"v_pk_add_f32 v[96:97],v[96:97],v[16:17]\n"
"v_pk_add_f32 v[98:99],v[98:99],v[18:19]\n"
"v_pk_add_f32 v[100:101],v[100:101],v[20:21]\n"
"v_pk_add_f32 v[102:103],v[102:103],v[22:23]\n"
"v_pk_add_f32 v[104:105],v[104:105],v[24:25]\n"
"s_waitcnt lgkmcnt(14)\n"
"v_pk_add_f32 v[106:107],v[106:107],v[26:27]\n"
"s_waitcnt lgkmcnt(13)\n"
"v_pk_add_f32 v[108:109],v[108:109],v[28:29]\n"
"s_waitcnt lgkmcnt(12)\n"
"v_pk_add_f32 v[110:111],v[110:111],v[30:31]\n"
"s_waitcnt lgkmcnt(11)\n"
"v_pk_add_f32 v[112:113],v[112:113],v[32:33]\n"
"s_waitcnt lgkmcnt(10)\n"
"v_pk_add_f32 v[114:115],v[114:115],v[34:35]\n"
"s_waitcnt lgkmcnt(9)\n"
"v_pk_add_f32 v[116:117],v[116:117],v[36:37]\n"
"s_waitcnt lgkmcnt(8)\n"
"v_pk_add_f32 v[118:119],v[118:119],v[38:39]\n"
"s_waitcnt lgkmcnt(7)\n"
"v_pk_add_f32 v[120:121],v[120:121],v[40:41]\n"
"s_waitcnt lgkmcnt(6)\n"
"v_pk_add_f32 v[122:123],v[122:123],v[42:43]\n"
"s_waitcnt lgkmcnt(5)\n"
"v_pk_add_f32 v[124:125],v[124:125],v[44:45]\n"
"s_waitcnt lgkmcnt(4)\n"
"v_pk_add_f32 v[126:127],v[126:127],v[46:47]\n"
"s_waitcnt lgkmcnt(3)\n"
"v_pk_add_f32 v[128:129],v[128:129],v[48:49]\n"
"s_waitcnt lgkmcnt(2)\n"
"v_pk_add_f32 v[130:131],v[130:131],v[50:51]\n"
"s_waitcnt lgkmcnt(1)\n"
"v_pk_add_f32 v[132:133],v[132:133],v[52:53]\n"
"s_waitcnt lgkmcnt(0)\n"
"v_pk_add_f32 v[134:135],v[134:135],v[54:55]\n"
"s_waitcnt vmcnt(5)\n"
"s_nop 0\n"
MF "v[100:103],v[56:59],a[252:255],v[100:103]\n"
MF "v[108:111],v[64:67],a[252:255],v[108:111]\n"
MF "v[116:119],v[72:75],a[252:255],v[116:119]\n"
MF "v[124:127],v[80:83],a[252:255],v[124:127]\n"
MF "v[132:135],v[88:91],a[252:255],v[132:135]\n"
MF "v[96:99],v[56:59],a[248:251],v[96:99]\n"
MF "v[104:107],v[64:67],a[248:251],v[104:107]\n"
MF "v[112:115],v[72:75],a[248:251],v[112:115]\n"
MF "v[120:123],v[80:83],a[248:251],v[120:123]\n"
MF "v[128:131],v[88:91],a[248:251],v[128:131]\n"
MF "v[96:99],v[60:63],a[252:255],v[96:99]\n"
MF "v[104:107],v[68:71],a[252:255],v[104:107]\n"
MF "v[112:115],v[76:79],a[252:255],v[112:115]\n"
MF "v[120:123],v[84:87],a[252:255],v[120:123]\n"
MF "v[128:131],v[92:95],a[252:255],v[128:131]\n"
MF "v[100:103],v[60:63],a[248:251],v[100:103]\n"
MF "v[108:111],v[68:71],a[248:251],v[108:111]\n"
MF "v[116:119],v[76:79],a[248:251],v[116:119]\n"
MF "v[124:127],v[84:87],a[248:251],v[124:127]\n"
MF "v[132:135],v[92:95],a[248:251],v[132:135]\n"
"s_waitcnt vmcnt(0)\n"
MF "v[100:103],v[208:211],a[252:255],v[100:103]\n"
MF "v[96:99],v[208:211],a[248:251],v[96:99]\n"
MF "v[108:111],v[212:215],a[252:255],v[108:111]\n"
MF "v[104:107],v[212:215],a[248:251],v[104:107]\n"
MF "v[116:119],v[216:219],a[252:255],v[116:119]\n"
"s_nop 4\n"
CV "v96,v96,v97\n"
CV "v97,v98,v99\n"
MF "v[112:115],v[216:219],a[248:251],v[112:115]\n"
CV "v98,v100,v101\n"
CV "v99,v102,v103\n"
MF "v[124:127],v[220:223],a[252:255],v[124:127]\n"
CV "v104,v104,v105\n"
CV "v105,v106,v107\n"
MF "v[120:123],v[220:223],a[248:251],v[120:123]\n"
CV "v106,v108,v109\n"
CV "v107,v110,v111\n"
"v_permlane16_swap_b32 v96,v98\n"
"v_permlane16_swap_b32 v97,v99\n"
"buffer_store_dwordx4 v[96:99],v242,s[44:47],s87 offen sc1\n"
"s_add_u32 s87,s87,40960\n"
MF "v[132:135],v[224:227],a[252:255],v[132:135]\n"
CV "v112,v112,v113\n"
CV "v113,v114,v115\n"
MF "v[128:131],v[224:227],a[248:251],v[128:131]\n"
CV "v114,v116,v117\n"
CV "v115,v118,v119\n"
"v_permlane16_swap_b32 v104,v106\n"
"v_permlane16_swap_b32 v105,v107\n"
"buffer_store_dwordx4 v[104:107],v242,s[44:47],s87 offen sc1\n"
"s_add_u32 s87,s87,40960\n"
CV "v120,v120,v121\n"
CV "v121,v122,v123\n"
CV "v122,v124,v125\n"
CV "v123,v126,v127\n"
"v_permlane16_swap_b32 v112,v114\n"
"v_permlane16_swap_b32 v113,v115\n"
"buffer_store_dwordx4 v[112:115],v242,s[44:47],s87 offen sc1\n"
"s_add_u32 s87,s87,40960\n"
CV "v128,v128,v129\n"
CV "v129,v130,v131\n"
CV "v130,v132,v133\n"
CV "v131,v134,v135\n"
"v_permlane16_swap_b32 v120,v122\n"
"v_permlane16_swap_b32 v121,v123\n"
"buffer_store_dwordx4 v[120:123],v242,s[44:47],s87 offen sc1\n"
"s_add_u32 s87,s87,40960\n"
"v_permlane16_swap_b32 v128,v130\n"
"v_permlane16_swap_b32 v129,v131\n"
"buffer_store_dwordx4 v[128:131],v242,s[44:47],s87 offen sc1\n"
"s_add_u32 s87,s87,40960\n"
"s_endpgm\n"
".Ljobs:\n"
"s_cmp_ge_u32 s92,40\n"
"s_cbranch_scc1 .Ldone\n"
"buffer_load_dword v10,v12,s[40:43],s86 offen sc1\n"
"s_mul_i32 s95,s92,3277\n"
"s_lshr_b32 s95,s95,16\n"
"s_mul_i32 s59,s95,20\n"
"s_sub_u32 s59,s92,s59\n"
"s_cmp_ge_u32 s59,10\n"
"s_cselect_b32 s96,4294557760,0\n"
"s_add_u32 s96,s96,1024\n"
"s_mul_i32 s59,s59,40960\n"
"s_add_u32 s96,s96,s59\n"
"s_lshl_b32 s59,s95,7\n"
"s_add_u32 s96,s96,s59\n"
"s_add_u32 s95,s95,3\n"
"s_waitcnt vmcnt(0)\n"
".Ljtry:\n"
"s_cmp_eq_u32 s98,0\n"
"s_cselect_b32 s95,0,s95\n"
"v_sub_u32 v10,v10,v11\n"
"v_cmp_gt_u32 vcc,s95,v10\n"
"s_lshl_b32 s82,s92,10\n"
"s_add_u32 s82,s82,s93\n"
"s_cmp_eq_u64 vcc,0\n"
"s_cselect_b32 s82,s82,0xf0000000\n"
"s_cselect_b32 s84,s96,0xf0000000\n"
"s_cselect_b32 s59,s4,0\n"
"s_add_u32 s92,s92,s59\n"
"s_cmp_eq_u32 s59,0\n"
"s_cbranch_scc0 .Ljgo\n"
"s_sub_u32 s98,s98,1\n"
"s_sleep 2\n"
"s_branch .Ljobs\n"
".Ljgo:\n"
"s_mov_b32 s83,s84\n"
LD "v[208:211],v9,s[40:43],s82 offen sc1\n"
"s_add_u32 s82,s82,s71\n"
LD "v[212:215],v9,s[40:43],s82 offen sc1\n"
"s_add_u32 s82,s82,s71\n"
LD "v[216:219],v9,s[40:43],s82 offen sc1\n"
"s_add_u32 s82,s82,s71\n"
LD "v[220:223],v9,s[40:43],s82 offen sc1\n"
"s_add_u32 s82,s82,s71\n"
LD "v[224:227],v9,s[40:43],s82 offen sc1\n"
"s_add_u32 s82,s82,s66\n"
LD "v[228:231],v9,s[40:43],s82 offen sc1\n"
"s_add_u32 s82,s82,s71\n"
LD "v[232:235],v9,s[40:43],s82 offen sc1\n"
"s_add_u32 s82,s82,s71\n"
LD "v[236:239],v9,s[40:43],s82 offen sc1\n"
"buffer_load_dword v10,v12,s[40:43],s86 offen sc1\n"
"s_waitcnt vmcnt(0)\n"
".Ljfin:\n"
"s_nop 1\n"
"v_mov_b32 v240,v208\n"
"v_mov_b32 v241,v209\n"
MF "v[248:251],v[210:213],a[250:253],0\n"
MF "v[244:247],v[214:217],a[250:253],0\n"
MF "v[248:251],v[218:221],a[250:253],v[248:251]\n"
MF "v[244:247],v[222:225],a[250:253],v[244:247]\n"
MF "v[248:251],v[226:229],a[250:253],v[248:251]\n"
MF "v[244:247],v[230:233],a[250:253],v[244:247]\n"
MF "v[248:251],v[234:237],a[250:253],v[248:251]\n"
MF "v[244:247],v[238:241],a[250:253],v[244:247]\n"
"s_nop 7\n"
CV "v244,v244,v245\n"
CV "v245,v246,v247\n"
CV "v246,v248,v249\n"
CV "v247,v250,v251\n"
"s_nop 0\n"
"v_permlane16_swap_b32 v244,v246\n"
"v_permlane16_swap_b32 v245,v247\n"
"buffer_store_dwordx4 v[244:247],v8,s[44:47],s83 offen sc1\n"
"s_cmp_ge_u32 s92,40\n"
"s_cbranch_scc1 .Ldone\n"
"s_mul_i32 s95,s92,3277\n"
"s_lshr_b32 s95,s95,16\n"
"s_mul_i32 s59,s95,20\n"
"s_sub_u32 s59,s92,s59\n"
"s_cmp_ge_u32 s59,10\n"
"s_cselect_b32 s96,4294557760,0\n"
"s_add_u32 s96,s96,1024\n"
"s_mul_i32 s59,s59,40960\n"
"s_add_u32 s96,s96,s59\n"
"s_lshl_b32 s59,s95,7\n"
"s_add_u32 s96,s96,s59\n"
"s_add_u32 s95,s95,3\n"
"s_branch .Ljtry\n"
".Lool:\n"
".Lsp0:\n"
"s_cmp_eq_u32 s98,0\n"
"s_cbranch_scc1 .Lnb0\n"
"s_sub_u32 s98,s98,1\n"
"s_sleep 2\n"
"buffer_load_dword v10,v12,s[40:43],s86 offen sc1\n"
"s_waitcnt vmcnt(0)\n"
"v_sub_u32 v10,v10,v11\n"
"s_nop 1\n"
"s_branch .Lnf0\n"
".Lsp1:\n"
"s_cmp_eq_u32 s98,0\n"
"s_cbranch_scc1 .Lnb1\n"
"s_sub_u32 s98,s98,1\n"
"s_sleep 2\n"
"buffer_load_dword v10,v12,s[40:43],s86 offen sc1\n"
"s_waitcnt vmcnt(0)\n"
"v_sub_u32 v10,v10,v11\n"
"s_nop 1\n"
"s_branch .Lnf1\n"
".Lsp2:\n"
"s_cmp_eq_u32 s98,0\n"
"s_cbranch_scc1 .Lnb2\n"
"s_sub_u32 s98,s98,1\n"
"s_sleep 2\n"
"buffer_load_dword v10,v12,s[40:43],s86 offen sc1\n"
"s_waitcnt vmcnt(0)\n"
"v_sub_u32 v10,v10,v11\n"
"s_nop 1\n"
"s_branch .Lnf2\n"
".Lsp3:\n"
"s_cmp_eq_u32 s98,0\n"
"s_cbranch_scc1 .Lnb3\n"
"s_sub_u32 s98,s98,1\n"
"s_sleep 2\n"
"buffer_load_dword v10,v12,s[40:43],s86 offen sc1\n"
"s_waitcnt vmcnt(0)\n"
"v_sub_u32 v10,v10,v11\n"
"s_nop 1\n"
"s_branch .Lnf3\n"
".Lsp4:\n"
"s_cmp_eq_u32 s98,0\n"
"s_cbranch_scc1 .Lnb4\n"
"s_sub_u32 s98,s98,1\n"
"s_sleep 2\n"
"buffer_load_dword v10,v12,s[40:43],s86 offen sc1\n"
"s_waitcnt vmcnt(0)\n"
"v_sub_u32 v10,v10,v11\n"
"s_nop 1\n"
"s_branch .Lnf4\n"
".Lsp5:\n"
"s_cmp_eq_u32 s98,0\n"
"s_cbranch_scc1 .Lnb5\n"
"s_sub_u32 s98,s98,1\n"
"s_sleep 2\n"
"buffer_load_dword v10,v12,s[40:43],s86 offen sc1\n"
"s_waitcnt vmcnt(0)\n"
"v_sub_u32 v10,v10,v11\n"
"s_nop 1\n"
"s_branch .Lnf5\n"
".Lsp6:\n"
"s_cmp_eq_u32 s98,0\n"
"s_cbranch_scc1 .Lnb6\n"
"s_sub_u32 s98,s98,1\n"
"s_sleep 2\n"
"buffer_load_dword v10,v12,s[40:43],s86 offen sc1\n"
"s_waitcnt vmcnt(0)\n"
"v_sub_u32 v10,v10,v11\n"
"s_nop 1\n"
"s_branch .Lnf6\n"
".Lsp7:\n"
"s_cmp_eq_u32 s98,0\n"
"s_cbranch_scc1 .Lnb7\n"
"s_sub_u32 s98,s98,1\n"
"s_sleep 2\n"
"buffer_load_dword v10,v12,s[40:43],s86 offen sc1\n"
"s_waitcnt vmcnt(0)\n"
"v_sub_u32 v10,v10,v11\n"
"s_nop 1\n"
"s_branch .Lnf7\n"
".Lsp8:\n"
"s_cmp_eq_u32 s98,0\n"
"s_cbranch_scc1 .Lnb8\n"
"s_sub_u32 s98,s98,1\n"
"s_sleep 2\n"
"buffer_load_dword v10,v12,s[40:43],s86 offen sc1\n"
"s_waitcnt vmcnt(0)\n"
"v_sub_u32 v10,v10,v11\n"
"s_nop 1\n"
"s_branch .Lnf8\n"
".Lsp9:\n"
"s_cmp_eq_u32 s98,0\n"
"s_cbranch_scc1 .Lnb9\n"
"s_sub_u32 s98,s98,1\n"
"s_sleep 2\n"
"buffer_load_dword v10,v12,s[40:43],s86 offen sc1\n"
"s_waitcnt vmcnt(0)\n"
"v_sub_u32 v10,v10,v11\n"
"s_nop 1\n"
"s_branch .Lnf9\n"
".Lsp10:\n"
"s_cmp_eq_u32 s98,0\n"
"s_cbranch_scc1 .Lnb10\n"
"s_sub_u32 s98,s98,1\n"
"s_sleep 2\n"
"buffer_load_dword v10,v12,s[40:43],s86 offen sc1\n"
"s_waitcnt vmcnt(0)\n"
"v_sub_u32 v10,v10,v11\n"
"s_nop 1\n"
"s_branch .Lnf10\n"
".Lsp11:\n"
"s_cmp_eq_u32 s98,0\n"
"s_cbranch_scc1 .Lnb11\n"
"s_sub_u32 s98,s98,1\n"
"s_sleep 2\n"
"buffer_load_dword v10,v12,s[40:43],s86 offen sc1\n"
"s_waitcnt vmcnt(0)\n"
"v_sub_u32 v10,v10,v11\n"
"s_nop 1\n"
"s_branch .Lnf11\n"
".Lcode1:\n"
".Lslow_0:\n"
"s_nop 7\n"
"s_nop 7\n"
"v_and_b32 v10,240,v9\n"
"v_mul_u32_u24 v10,24,v10\n"
"v_lshrrev_b32 v11,4,v9\n"
"v_and_or_b32 v10,v11,48,v10\n"
"s_mov_b32 s59,s34\n"
"s_mov_b32 s34,s20\n"
"s_add_u32 s58,s74,0\n"
"s_nop 3\n"
LD "v[52:55],v10,s[32:35],s58 offen\n"
"s_waitcnt vmcnt(0)\n"
MF "v[72:75],v[52:55],v[136:139],v[32:35]\n"
MF "v[76:79],v[52:55],v[140:143],v[36:39]\n"
MF "v[80:83],v[52:55],v[144:147],v[40:43]\n"
MF "v[84:87],v[52:55],v[148:151],v[44:47]\n"
MF "v[88:91],v[52:55],v[152:155],v[48:51]\n"
"s_add_u32 s58,s74,0\n"
"s_nop 3\n"
LD "v[52:55],v10,s[32:35],s58 offen offset:64\n"
"s_waitcnt vmcnt(0)\n"
MF "v[72:75],v[52:55],v[156:159],v[72:75]\n"
MF "v[76:79],v[52:55],v[160:163],v[76:79]\n"
MF "v[80:83],v[52:55],v[164:167],v[80:83]\n"
MF "v[84:87],v[52:55],v[168:171],v[84:87]\n"
MF "v[88:91],v[52:55],v[172:175],v[88:91]\n"
"s_add_u32 s58,s74,0\n"
"s_nop 3\n"
LD "v[52:55],v10,s[32:35],s58 offen offset:128\n"
"s_waitcnt vmcnt(0)\n"
MF "v[72:75],v[52:55],v[176:179],v[72:75]\n"
MF "v[76:79],v[52:55],v[180:183],v[76:79]\n"
MF "v[80:83],v[52:55],v[184:187],v[80:83]\n"
MF "v[84:87],v[52:55],v[188:191],v[84:87]\n"
MF "v[88:91],v[52:55],v[192:195],v[88:91]\n"
"s_add_u32 s58,s74,0\n"
"s_nop 3\n"
LD "v[52:55],v10,s[32:35],s58 offen offset:192\n"
"s_waitcnt vmcnt(0)\n"
MF "v[72:75],v[52:55],v[196:199],v[72:75]\n"
MF "v[76:79],v[52:55],v[200:203],v[76:79]\n"
MF "v[80:83],v[52:55],v[204:207],v[80:83]\n"
MF "v[84:87],v[52:55],v[208:211],v[84:87]\n"
MF "v[88:91],v[52:55],v[212:215],v[88:91]\n"
"s_add_u32 s58,s74,0\n"
"s_nop 3\n"
LD "v[52:55],v10,s[32:35],s58 offen offset:256\n"
"s_waitcnt vmcnt(0)\n"
MF "v[72:75],v[52:55],v[216:219],v[72:75]\n"
MF "v[76:79],v[52:55],v[220:223],v[76:79]\n"
MF "v[80:83],v[52:55],v[224:227],v[80:83]\n"
MF "v[84:87],v[52:55],v[228:231],v[84:87]\n"
MF "v[88:91],v[52:55],v[232:235],v[88:91]\n"
"s_add_u32 s58,s74,0\n"
"s_nop 3\n"
LD "v[52:55],v10,s[32:35],s58 offen offset:320\n"
"s_waitcnt vmcnt(0)\n"
MF "v[72:75],v[52:55],v[236:239],v[72:75]\n"
MF "v[76:79],v[52:55],v[240:243],v[76:79]\n"
MF "v[80:83],v[52:55],v[244:247],v[80:83]\n"
MF "v[84:87],v[52:55],v[248:251],v[84:87]\n"
MF "v[88:91],v[52:55],v[252:255],v[88:91]\n"
"s_add_u32 s58,s74,6144\n"
"s_nop 3\n"
LD "v[52:55],v10,s[32:35],s58 offen\n"
"s_waitcnt vmcnt(0)\n"
MF "v[112:115],v[52:55],v[136:139],v[32:35]\n"
MF "v[116:119],v[52:55],v[140:143],v[36:39]\n"
MF "v[120:123],v[52:55],v[144:147],v[40:43]\n"
MF "v[124:127],v[52:55],v[148:151],v[44:47]\n"
MF "v[128:131],v[52:55],v[152:155],v[48:51]\n"
"s_add_u32 s58,s74,6144\n"
"s_nop 3\n"
LD "v[52:55],v10,s[32:35],s58 offen offset:64\n"
"s_waitcnt vmcnt(0)\n"
MF "v[112:115],v[52:55],v[156:159],v[112:115]\n"
MF "v[116:119],v[52:55],v[160:163],v[116:119]\n"
MF "v[120:123],v[52:55],v[164:167],v[120:123]\n"
MF "v[124:127],v[52:55],v[168:171],v[124:127]\n"
MF "v[128:131],v[52:55],v[172:175],v[128:131]\n"
"s_add_u32 s58,s74,6144\n"
"s_nop 3\n"
LD "v[52:55],v10,s[32:35],s58 offen offset:128\n"
"s_waitcnt vmcnt(0)\n"
MF "v[112:115],v[52:55],v[176:179],v[112:115]\n"
MF "v[116:119],v[52:55],v[180:183],v[116:119]\n"
MF "v[120:123],v[52:55],v[184:187],v[120:123]\n"
MF "v[124:127],v[52:55],v[188:191],v[124:127]\n"
MF "v[128:131],v[52:55],v[192:195],v[128:131]\n"
"s_add_u32 s58,s74,6144\n"
"s_nop 3\n"
LD "v[52:55],v10,s[32:35],s58 offen offset:192\n"
"s_waitcnt vmcnt(0)\n"
MF "v[112:115],v[52:55],v[196:199],v[112:115]\n"
MF "v[116:119],v[52:55],v[200:203],v[116:119]\n"
MF "v[120:123],v[52:55],v[204:207],v[120:123]\n"
MF "v[124:127],v[52:55],v[208:211],v[124:127]\n"
MF "v[128:131],v[52:55],v[212:215],v[128:131]\n"
"s_add_u32 s58,s74,6144\n"
"s_nop 3\n"
LD "v[52:55],v10,s[32:35],s58 offen offset:256\n"
"s_waitcnt vmcnt(0)\n"
MF "v[112:115],v[52:55],v[216:219],v[112:115]\n"
MF "v[116:119],v[52:55],v[220:223],v[116:119]\n"
MF "v[120:123],v[52:55],v[224:227],v[120:123]\n"
MF "v[124:127],v[52:55],v[228:231],v[124:127]\n"
MF "v[128:131],v[52:55],v[232:235],v[128:131]\n"
"s_add_u32 s58,s74,6144\n"
"s_nop 3\n"
LD "v[52:55],v10,s[32:35],s58 offen offset:320\n"
"s_waitcnt vmcnt(0)\n"
MF "v[112:115],v[52:55],v[236:239],v[112:115]\n"
MF "v[116:119],v[52:55],v[240:243],v[116:119]\n"
MF "v[120:123],v[52:55],v[244:247],v[120:123]\n"
MF "v[124:127],v[52:55],v[248:251],v[124:127]\n"
MF "v[128:131],v[52:55],v[252:255],v[128:131]\n"
"s_nop 7\n"
"s_mov_b32 s34,s59\n"
"v_max3_f32 v10,v72,v73,v74\n"
"s_nop 0\n"
"v_max3_f32 v11,v75,v112,v113\n"
"v_max3_f32 v10,v10,v114,v115\n"
"v_max_f32 v10,v10,v11\n"
"v_mov_b32 v11,v10\n"
"s_nop 1\n"
"v_permlane16_swap_b32 v10,v11\n"
"v_max_f32 v10,v10,v11\n"
"v_mov_b32 v11,v10\n"
"s_nop 1\n"
"v_permlane32_swap_b32 v10,v11\n"
"v_max_f32 v10,v10,v11\n"
"v_add_f32 v10,0x41300000,v10\n"
"v_max_f32 v10,0,v10\n"
"v_exp_f32 v11,-v10\n"
AR "v12,a0\n"
"v_subrev_f32 v12,s13,v12\n"
"v_fma_f32 v12,v12,v11,s13\n"
AW "a0,v12\n"
AR "v12,a20\n"
"v_subrev_f32 v12,s13,v12\n"
"v_fma_f32 v12,v12,v11,s13\n"
AW "a20,v12\n"
AR "v12,a40\n"
"v_subrev_f32 v12,s13,v12\n"
"v_fma_f32 v12,v12,v11,s13\n"
AW "a40,v12\n"
AR "v12,a60\n"
"v_subrev_f32 v12,s13,v12\n"
"v_fma_f32 v12,v12,v11,s13\n"
AW "a60,v12\n"
AR "v12,a80\n"
"v_subrev_f32 v12,s13,v12\n"
"v_fma_f32 v12,v12,v11,s13\n"
AW "a80,v12\n"
AR "v12,a100\n"
"v_subrev_f32 v12,s13,v12\n"
"v_fma_f32 v12,v12,v11,s13\n"
AW "a100,v12\n"
AR "v12,a120\n"
"v_subrev_f32 v12,s13,v12\n"
"v_fma_f32 v12,v12,v11,s13\n"
AW "a120,v12\n"
AR "v12,a140\n"
"v_subrev_f32 v12,s13,v12\n"
"v_fma_f32 v12,v12,v11,s13\n"
AW "a140,v12\n"
AR "v12,a160\n"
"v_subrev_f32 v12,s13,v12\n"
"v_fma_f32 v12,v12,v11,s13\n"
AW "a160,v12\n"
AR "v12,a180\n"
"v_subrev_f32 v12,s13,v12\n"
"v_fma_f32 v12,v12,v11,s13\n"
AW "a180,v12\n"
AR "v12,a200\n"
"v_subrev_f32 v12,s13,v12\n"
"v_fma_f32 v12,v12,v11,s13\n"
AW "a200,v12\n"
AR "v12,a220\n"
"v_subrev_f32 v12,s13,v12\n"
"v_fma_f32 v12,v12,v11,s13\n"
AW "a220,v12\n"
AR "v12,a1\n"
"v_subrev_f32 v12,s13,v12\n"
"v_fma_f32 v12,v12,v11,s13\n"
AW "a1,v12\n"
AR "v12,a21\n"
"v_subrev_f32 v12,s13,v12\n"
"v_fma_f32 v12,v12,v11,s13\n"
AW "a21,v12\n"
AR "v12,a41\n"
"v_subrev_f32 v12,s13,v12\n"
"v_fma_f32 v12,v12,v11,s13\n"
AW "a41,v12\n"
AR "v12,a61\n"
"v_subrev_f32 v12,s13,v12\n"
"v_fma_f32 v12,v12,v11,s13\n"
AW "a61,v12\n"
AR "v12,a81\n"
"v_subrev_f32 v12,s13,v12\n"
"v_fma_f32 v12,v12,v11,s13\n"
AW "a81,v12\n"
AR "v12,a101\n"
"v_subrev_f32 v12,s13,v12\n"
"v_fma_f32 v12,v12,v11,s13\n"
AW "a101,v12\n"
AR "v12,a121\n"
"v_subrev_f32 v12,s13,v12\n"
"v_fma_f32 v12,v12,v11,s13\n"
AW "a121,v12\n"
AR "v12,a141\n"
"v_subrev_f32 v12,s13,v12\n"
"v_fma_f32 v12,v12,v11,s13\n"
AW "a141,v12\n"
AR "v12,a161\n"
"v_subrev_f32 v12,s13,v12\n"
"v_fma_f32 v12,v12,v11,s13\n"
AW "a161,v12\n"
AR "v12,a181\n"
"v_subrev_f32 v12,s13,v12\n"
"v_fma_f32 v12,v12,v11,s13\n"
AW "a181,v12\n"
AR "v12,a201\n"
"v_subrev_f32 v12,s13,v12\n"
"v_fma_f32 v12,v12,v11,s13\n"
AW "a201,v12\n"
AR "v12,a221\n"
"v_subrev_f32 v12,s13,v12\n"
"v_fma_f32 v12,v12,v11,s13\n"
AW "a221,v12\n"
AR "v12,a2\n"
"v_subrev_f32 v12,s13,v12\n"
"v_fma_f32 v12,v12,v11,s13\n"
AW "a2,v12\n"
AR "v12,a22\n"
"v_subrev_f32 v12,s13,v12\n"
"v_fma_f32 v12,v12,v11,s13\n"
AW "a22,v12\n"
AR "v12,a42\n"
"v_subrev_f32 v12,s13,v12\n"
"v_fma_f32 v12,v12,v11,s13\n"
AW "a42,v12\n"
AR "v12,a62\n"
"v_subrev_f32 v12,s13,v12\n"
"v_fma_f32 v12,v12,v11,s13\n"
AW "a62,v12\n"
AR "v12,a82\n"
"v_subrev_f32 v12,s13,v12\n"
"v_fma_f32 v12,v12,v11,s13\n"
AW "a82,v12\n"
AR "v12,a102\n"
"v_subrev_f32 v12,s13,v12\n"
"v_fma_f32 v12,v12,v11,s13\n"
AW "a102,v12\n"
AR "v12,a122\n"
"v_subrev_f32 v12,s13,v12\n"
"v_fma_f32 v12,v12,v11,s13\n"
AW "a122,v12\n"
AR "v12,a142\n"
"v_subrev_f32 v12,s13,v12\n"
"v_fma_f32 v12,v12,v11,s13\n"
AW "a142,v12\n"
AR "v12,a162\n"
"v_subrev_f32 v12,s13,v12\n"
"v_fma_f32 v12,v12,v11,s13\n"
AW "a162,v12\n"
AR "v12,a182\n"
"v_subrev_f32 v12,s13,v12\n"
"v_fma_f32 v12,v12,v11,s13\n"
AW "a182,v12\n"
AR "v12,a202\n"
"v_subrev_f32 v12,s13,v12\n"
"v_fma_f32 v12,v12,v11,s13\n"
AW "a202,v12\n"
AR "v12,a222\n"
"v_subrev_f32 v12,s13,v12\n"
"v_fma_f32 v12,v12,v11,s13\n"
AW "a222,v12\n"
AR "v12,a3\n"
"v_subrev_f32 v12,s13,v12\n"
"v_fma_f32 v12,v12,v11,s13\n"
AW "a3,v12\n"
AR "v12,a23\n"
"v_subrev_f32 v12,s13,v12\n"
"v_fma_f32 v12,v12,v11,s13\n"
AW "a23,v12\n"
AR "v12,a43\n"
"v_subrev_f32 v12,s13,v12\n"
"v_fma_f32 v12,v12,v11,s13\n"
AW "a43,v12\n"
AR "v12,a63\n"
"v_subrev_f32 v12,s13,v12\n"
"v_fma_f32 v12,v12,v11,s13\n"
AW "a63,v12\n"
AR "v12,a83\n"
"v_subrev_f32 v12,s13,v12\n"
"v_fma_f32 v12,v12,v11,s13\n"
AW "a83,v12\n"
AR "v12,a103\n"
"v_subrev_f32 v12,s13,v12\n"
"v_fma_f32 v12,v12,v11,s13\n"
AW "a103,v12\n"
AR "v12,a123\n"
"v_subrev_f32 v12,s13,v12\n"
"v_fma_f32 v12,v12,v11,s13\n"
AW "a123,v12\n"
AR "v12,a143\n"
"v_subrev_f32 v12,s13,v12\n"
"v_fma_f32 v12,v12,v11,s13\n"
AW "a143,v12\n"
AR "v12,a163\n"
"v_subrev_f32 v12,s13,v12\n"
"v_fma_f32 v12,v12,v11,s13\n"
AW "a163,v12\n"
AR "v12,a183\n"
"v_subrev_f32 v12,s13,v12\n"
"v_fma_f32 v12,v12,v11,s13\n"
AW "a183,v12\n"
AR "v12,a203\n"
"v_subrev_f32 v12,s13,v12\n"
"v_fma_f32 v12,v12,v11,s13\n"
AW "a203,v12\n"
AR "v12,a223\n"
"v_subrev_f32 v12,s13,v12\n"
"v_fma_f32 v12,v12,v11,s13\n"
AW "a223,v12\n"
"v_mul_f32 v52,v57,v11\n"
"v_sub_f32 v32,v32,v10\n"
"v_sub_f32 v33,v33,v10\n"
"v_sub_f32 v34,v34,v10\n"
"v_sub_f32 v35,v35,v10\n"
"v_sub_f32 v92,v92,v10\n"
"v_sub_f32 v93,v93,v10\n"
"v_sub_f32 v94,v94,v10\n"
"v_sub_f32 v95,v95,v10\n"
"v_sub_f32 v72,v72,v10\n"
"v_sub_f32 v73,v73,v10\n"
"v_sub_f32 v74,v74,v10\n"
"v_sub_f32 v75,v75,v10\n"
"v_sub_f32 v112,v112,v10\n"
"v_sub_f32 v113,v113,v10\n"
"v_sub_f32 v114,v114,v10\n"
"v_sub_f32 v115,v115,v10\n"
"v_exp_f32 v72,v72\n"
"v_exp_f32 v73,v73\n"
"v_exp_f32 v74,v74\n"
"v_exp_f32 v75,v75\n"
"v_exp_f32 v112,v112\n"
"v_exp_f32 v113,v113\n"
"v_exp_f32 v114,v114\n"
"v_exp_f32 v115,v115\n"
"v_add_f32 v52,v52,v72\n"
"v_add_f32 v52,v52,v73\n"
"v_add_f32 v52,v52,v74\n"
"v_add_f32 v52,v52,v75\n"
"v_add_f32 v52,v52,v112\n"
"v_add_f32 v52,v52,v113\n"
"v_add_f32 v52,v52,v114\n"
"v_add_f32_e64 v52,v52,v115\n"
CV "v72,v72,v73\n"
CV "v73,v74,v75\n"
CV "v74,v112,v113\n"
CV "v75,v114,v115\n"
"v_max3_f32 v10,v76,v77,v78\n"
"v_max3_f32 v11,v79,v116,v117\n"
"v_max3_f32 v10,v10,v118,v119\n"
"v_max_f32 v10,v10,v11\n"
"v_mov_b32 v11,v10\n"
"s_nop 1\n"
"v_permlane16_swap_b32 v10,v11\n"
"v_max_f32 v10,v10,v11\n"
"v_mov_b32 v11,v10\n"
"s_nop 1\n"
"v_permlane32_swap_b32 v10,v11\n"
"v_max_f32 v10,v10,v11\n"
"v_add_f32 v10,0x41300000,v10\n"
"v_max_f32 v10,0,v10\n"
"v_exp_f32 v11,-v10\n"
AR "v12,a4\n"
"v_subrev_f32 v12,s13,v12\n"
"v_fma_f32 v12,v12,v11,s13\n"
AW "a4,v12\n"
AR "v12,a24\n"
"v_subrev_f32 v12,s13,v12\n"
"v_fma_f32 v12,v12,v11,s13\n"
AW "a24,v12\n"
AR "v12,a44\n"
"v_subrev_f32 v12,s13,v12\n"
"v_fma_f32 v12,v12,v11,s13\n"
AW "a44,v12\n"
AR "v12,a64\n"
"v_subrev_f32 v12,s13,v12\n"
"v_fma_f32 v12,v12,v11,s13\n"
AW "a64,v12\n"
AR "v12,a84\n"
"v_subrev_f32 v12,s13,v12\n"
"v_fma_f32 v12,v12,v11,s13\n"
AW "a84,v12\n"
AR "v12,a104\n"
"v_subrev_f32 v12,s13,v12\n"
"v_fma_f32 v12,v12,v11,s13\n"
AW "a104,v12\n"
AR "v12,a124\n"
"v_subrev_f32 v12,s13,v12\n"
"v_fma_f32 v12,v12,v11,s13\n"
AW "a124,v12\n"
AR "v12,a144\n"
"v_subrev_f32 v12,s13,v12\n"
"v_fma_f32 v12,v12,v11,s13\n"
AW "a144,v12\n"
AR "v12,a164\n"
"v_subrev_f32 v12,s13,v12\n"
"v_fma_f32 v12,v12,v11,s13\n"
AW "a164,v12\n"
AR "v12,a184\n"
"v_subrev_f32 v12,s13,v12\n"
"v_fma_f32 v12,v12,v11,s13\n"
AW "a184,v12\n"
AR "v12,a204\n"
"v_subrev_f32 v12,s13,v12\n"
"v_fma_f32 v12,v12,v11,s13\n"
AW "a204,v12\n"
AR "v12,a224\n"
"v_subrev_f32 v12,s13,v12\n"
"v_fma_f32 v12,v12,v11,s13\n"
AW "a224,v12\n"
AR "v12,a5\n"
"v_subrev_f32 v12,s13,v12\n"
"v_fma_f32 v12,v12,v11,s13\n"
AW "a5,v12\n"
AR "v12,a25\n"
"v_subrev_f32 v12,s13,v12\n"
"v_fma_f32 v12,v12,v11,s13\n"
AW "a25,v12\n"
AR "v12,a45\n"
"v_subrev_f32 v12,s13,v12\n"
"v_fma_f32 v12,v12,v11,s13\n"
AW "a45,v12\n"
AR "v12,a65\n"
"v_subrev_f32 v12,s13,v12\n"
"v_fma_f32 v12,v12,v11,s13\n"
AW "a65,v12\n"
AR "v12,a85\n"
"v_subrev_f32 v12,s13,v12\n"
"v_fma_f32 v12,v12,v11,s13\n"
AW "a85,v12\n"
AR "v12,a105\n"
"v_subrev_f32 v12,s13,v12\n"
"v_fma_f32 v12,v12,v11,s13\n"
AW "a105,v12\n"
AR "v12,a125\n"
"v_subrev_f32 v12,s13,v12\n"
"v_fma_f32 v12,v12,v11,s13\n"
AW "a125,v12\n"
AR "v12,a145\n"
"v_subrev_f32 v12,s13,v12\n"
"v_fma_f32 v12,v12,v11,s13\n"
AW "a145,v12\n"
AR "v12,a165\n"
"v_subrev_f32 v12,s13,v12\n"
"v_fma_f32 v12,v12,v11,s13\n"
AW "a165,v12\n"
AR "v12,a185\n"
"v_subrev_f32 v12,s13,v12\n"
"v_fma_f32 v12,v12,v11,s13\n"
AW "a185,v12\n"
AR "v12,a205\n"
"v_subrev_f32 v12,s13,v12\n"
"v_fma_f32 v12,v12,v11,s13\n"
AW "a205,v12\n"
AR "v12,a225\n"
"v_subrev_f32 v12,s13,v12\n"
"v_fma_f32 v12,v12,v11,s13\n"
AW "a225,v12\n"
AR "v12,a6\n"
"v_subrev_f32 v12,s13,v12\n"
"v_fma_f32 v12,v12,v11,s13\n"
AW "a6,v12\n"
AR "v12,a26\n"
"v_subrev_f32 v12,s13,v12\n"
"v_fma_f32 v12,v12,v11,s13\n"
AW "a26,v12\n"
AR "v12,a46\n"
"v_subrev_f32 v12,s13,v12\n"
"v_fma_f32 v12,v12,v11,s13\n"
AW "a46,v12\n"
AR "v12,a66\n"
"v_subrev_f32 v12,s13,v12\n"
"v_fma_f32 v12,v12,v11,s13\n"
AW "a66,v12\n"
AR "v12,a86\n"
"v_subrev_f32 v12,s13,v12\n"
"v_fma_f32 v12,v12,v11,s13\n"
AW "a86,v12\n"
AR "v12,a106\n"
"v_subrev_f32 v12,s13,v12\n"
"v_fma_f32 v12,v12,v11,s13\n"
AW "a106,v12\n"
AR "v12,a126\n"
"v_subrev_f32 v12,s13,v12\n"
"v_fma_f32 v12,v12,v11,s13\n"
AW "a126,v12\n"
AR "v12,a146\n"
"v_subrev_f32 v12,s13,v12\n"
"v_fma_f32 v12,v12,v11,s13\n"
AW "a146,v12\n"
AR "v12,a166\n"
"v_subrev_f32 v12,s13,v12\n"
"v_fma_f32 v12,v12,v11,s13\n"
AW "a166,v12\n"
AR "v12,a186\n"
"v_subrev_f32 v12,s13,v12\n"
"v_fma_f32 v12,v12,v11,s13\n"
AW "a186,v12\n"
AR "v12,a206\n"
"v_subrev_f32 v12,s13,v12\n"
"v_fma_f32 v12,v12,v11,s13\n"
AW "a206,v12\n"
AR "v12,a226\n"
"v_subrev_f32 v12,s13,v12\n"
"v_fma_f32 v12,v12,v11,s13\n"
AW "a226,v12\n"
AR "v12,a7\n"
"v_subrev_f32 v12,s13,v12\n"
"v_fma_f32 v12,v12,v11,s13\n"
AW "a7,v12\n"
AR "v12,a27\n"
"v_subrev_f32 v12,s13,v12\n"
"v_fma_f32 v12,v12,v11,s13\n"
AW "a27,v12\n"
AR "v12,a47\n"
"v_subrev_f32 v12,s13,v12\n"
"v_fma_f32 v12,v12,v11,s13\n"
AW "a47,v12\n"
AR "v12,a67\n"
"v_subrev_f32 v12,s13,v12\n"
"v_fma_f32 v12,v12,v11,s13\n"
AW "a67,v12\n"
AR "v12,a87\n"
"v_subrev_f32 v12,s13,v12\n"
"v_fma_f32 v12,v12,v11,s13\n"
AW "a87,v12\n"
AR "v12,a107\n"
"v_subrev_f32 v12,s13,v12\n"
"v_fma_f32 v12,v12,v11,s13\n"
AW "a107,v12\n"
AR "v12,a127\n"
"v_subrev_f32 v12,s13,v12\n"
"v_fma_f32 v12,v12,v11,s13\n"
AW "a127,v12\n"
AR "v12,a147\n"
"v_subrev_f32 v12,s13,v12\n"
"v_fma_f32 v12,v12,v11,s13\n"
AW "a147,v12\n"
AR "v12,a167\n"
"v_subrev_f32 v12,s13,v12\n"
"v_fma_f32 v12,v12,v11,s13\n"
AW "a167,v12\n"
AR "v12,a187\n"
"v_subrev_f32 v12,s13,v12\n"
"v_fma_f32 v12,v12,v11,s13\n"
AW "a187,v12\n"
AR "v12,a207\n"
"v_subrev_f32 v12,s13,v12\n"
"v_fma_f32 v12,v12,v11,s13\n"
AW "a207,v12\n"
AR "v12,a227\n"
"v_subrev_f32 v12,s13,v12\n"
"v_fma_f32 v12,v12,v11,s13\n"
AW "a227,v12\n"
"v_mul_f32 v53,v58,v11\n"
"v_sub_f32 v36,v36,v10\n"
"v_sub_f32 v37,v37,v10\n"
"v_sub_f32 v38,v38,v10\n"
"v_sub_f32 v39,v39,v10\n"
"v_sub_f32 v96,v96,v10\n"
"v_sub_f32 v97,v97,v10\n"
"v_sub_f32 v98,v98,v10\n"
"v_sub_f32 v99,v99,v10\n"
"v_sub_f32 v76,v76,v10\n"
"v_sub_f32 v77,v77,v10\n"
"v_sub_f32 v78,v78,v10\n"
"v_sub_f32 v79,v79,v10\n"
"v_sub_f32 v116,v116,v10\n"
"v_sub_f32 v117,v117,v10\n"
"v_sub_f32 v118,v118,v10\n"
"v_sub_f32 v119,v119,v10\n"
"v_exp_f32 v76,v76\n"
"v_exp_f32 v77,v77\n"
"v_exp_f32 v78,v78\n"
"v_exp_f32 v79,v79\n"
"v_exp_f32 v116,v116\n"
"v_exp_f32 v117,v117\n"
"v_exp_f32 v118,v118\n"
"v_exp_f32 v119,v119\n"
"v_add_f32 v53,v53,v76\n"
"v_add_f32 v53,v53,v77\n"
"v_add_f32 v53,v53,v78\n"
"v_add_f32 v53,v53,v79\n"
"v_add_f32 v53,v53,v116\n"
"v_add_f32 v53,v53,v117\n"
"v_add_f32 v53,v53,v118\n"
"v_add_f32_e64 v53,v53,v119\n"
CV "v76,v76,v77\n"
CV "v77,v78,v79\n"
CV "v78,v116,v117\n"
CV "v79,v118,v119\n"
"v_max3_f32 v10,v80,v81,v82\n"
"v_max3_f32 v11,v83,v120,v121\n"
"v_max3_f32 v10,v10,v122,v123\n"
"v_max_f32 v10,v10,v11\n"
"v_mov_b32 v11,v10\n"
"s_nop 1\n"
"v_permlane16_swap_b32 v10,v11\n"
"v_max_f32 v10,v10,v11\n"
"v_mov_b32 v11,v10\n"
"s_nop 1\n"
"v_permlane32_swap_b32 v10,v11\n"
"v_max_f32 v10,v10,v11\n"
"v_add_f32 v10,0x41300000,v10\n"
"v_max_f32 v10,0,v10\n"
"v_exp_f32 v11,-v10\n"
AR "v12,a8\n"
"v_subrev_f32 v12,s13,v12\n"
"v_fma_f32 v12,v12,v11,s13\n"
AW "a8,v12\n"
AR "v12,a28\n"
"v_subrev_f32 v12,s13,v12\n"
"v_fma_f32 v12,v12,v11,s13\n"
AW "a28,v12\n"
AR "v12,a48\n"
"v_subrev_f32 v12,s13,v12\n"
"v_fma_f32 v12,v12,v11,s13\n"
AW "a48,v12\n"
AR "v12,a68\n"
"v_subrev_f32 v12,s13,v12\n"
"v_fma_f32 v12,v12,v11,s13\n"
AW "a68,v12\n"
AR "v12,a88\n"
"v_subrev_f32 v12,s13,v12\n"
"v_fma_f32 v12,v12,v11,s13\n"
AW "a88,v12\n"
AR "v12,a108\n"
"v_subrev_f32 v12,s13,v12\n"
"v_fma_f32 v12,v12,v11,s13\n"
AW "a108,v12\n"
AR "v12,a128\n"
"v_subrev_f32 v12,s13,v12\n"
"v_fma_f32 v12,v12,v11,s13\n"
AW "a128,v12\n"
AR "v12,a148\n"
"v_subrev_f32 v12,s13,v12\n"
"v_fma_f32 v12,v12,v11,s13\n"
AW "a148,v12\n"
AR "v12,a168\n"
"v_subrev_f32 v12,s13,v12\n"
"v_fma_f32 v12,v12,v11,s13\n"
AW "a168,v12\n"
AR "v12,a188\n"
"v_subrev_f32 v12,s13,v12\n"
"v_fma_f32 v12,v12,v11,s13\n"
AW "a188,v12\n"
AR "v12,a208\n"
"v_subrev_f32 v12,s13,v12\n"
"v_fma_f32 v12,v12,v11,s13\n"
AW "a208,v12\n"
AR "v12,a228\n"
"v_subrev_f32 v12,s13,v12\n"
"v_fma_f32 v12,v12,v11,s13\n"
AW "a228,v12\n"
AR "v12,a9\n"
"v_subrev_f32 v12,s13,v12\n"
"v_fma_f32 v12,v12,v11,s13\n"
AW "a9,v12\n"
AR "v12,a29\n"
"v_subrev_f32 v12,s13,v12\n"
"v_fma_f32 v12,v12,v11,s13\n"
AW "a29,v12\n"
AR "v12,a49\n"
"v_subrev_f32 v12,s13,v12\n"
"v_fma_f32 v12,v12,v11,s13\n"
AW "a49,v12\n"
AR "v12,a69\n"
"v_subrev_f32 v12,s13,v12\n"
"v_fma_f32 v12,v12,v11,s13\n"
AW "a69,v12\n"
AR "v12,a89\n"
"v_subrev_f32 v12,s13,v12\n"
"v_fma_f32 v12,v12,v11,s13\n"
AW "a89,v12\n"
AR "v12,a109\n"
"v_subrev_f32 v12,s13,v12\n"
"v_fma_f32 v12,v12,v11,s13\n"
AW "a109,v12\n"
AR "v12,a129\n"
"v_subrev_f32 v12,s13,v12\n"
"v_fma_f32 v12,v12,v11,s13\n"
AW "a129,v12\n"
AR "v12,a149\n"
"v_subrev_f32 v12,s13,v12\n"
"v_fma_f32 v12,v12,v11,s13\n"
AW "a149,v12\n"
AR "v12,a169\n"
"v_subrev_f32 v12,s13,v12\n"
"v_fma_f32 v12,v12,v11,s13\n"
AW "a169,v12\n"
AR "v12,a189\n"
"v_subrev_f32 v12,s13,v12\n"
"v_fma_f32 v12,v12,v11,s13\n"
AW "a189,v12\n"
AR "v12,a209\n"
"v_subrev_f32 v12,s13,v12\n"
"v_fma_f32 v12,v12,v11,s13\n"
AW "a209,v12\n"
AR "v12,a229\n"
"v_subrev_f32 v12,s13,v12\n"
"v_fma_f32 v12,v12,v11,s13\n"
AW "a229,v12\n"
AR "v12,a10\n"
"v_subrev_f32 v12,s13,v12\n"
"v_fma_f32 v12,v12,v11,s13\n"
AW "a10,v12\n"
AR "v12,a30\n"
"v_subrev_f32 v12,s13,v12\n"
"v_fma_f32 v12,v12,v11,s13\n"
AW "a30,v12\n"
AR "v12,a50\n"
"v_subrev_f32 v12,s13,v12\n"
"v_fma_f32 v12,v12,v11,s13\n"
AW "a50,v12\n"
AR "v12,a70\n"
"v_subrev_f32 v12,s13,v12\n"
"v_fma_f32 v12,v12,v11,s13\n"
AW "a70,v12\n"
AR "v12,a90\n"
"v_subrev_f32 v12,s13,v12\n"
"v_fma_f32 v12,v12,v11,s13\n"
AW "a90,v12\n"
AR "v12,a110\n"
"v_subrev_f32 v12,s13,v12\n"
"v_fma_f32 v12,v12,v11,s13\n"
AW "a110,v12\n"
AR "v12,a130\n"
"v_subrev_f32 v12,s13,v12\n"
"v_fma_f32 v12,v12,v11,s13\n"
AW "a130,v12\n"
AR "v12,a150\n"
"v_subrev_f32 v12,s13,v12\n"
"v_fma_f32 v12,v12,v11,s13\n"
AW "a150,v12\n"
AR "v12,a170\n"
"v_subrev_f32 v12,s13,v12\n"
"v_fma_f32 v12,v12,v11,s13\n"
AW "a170,v12\n"
AR "v12,a190\n"
"v_subrev_f32 v12,s13,v12\n"
"v_fma_f32 v12,v12,v11,s13\n"
AW "a190,v12\n"
AR "v12,a210\n"
"v_subrev_f32 v12,s13,v12\n"
"v_fma_f32 v12,v12,v11,s13\n"
AW "a210,v12\n"
AR "v12,a230\n"
"v_subrev_f32 v12,s13,v12\n"
"v_fma_f32 v12,v12,v11,s13\n"
AW "a230,v12\n"
AR "v12,a11\n"
"v_subrev_f32 v12,s13,v12\n"
"v_fma_f32 v12,v12,v11,s13\n"
AW "a11,v12\n"
AR "v12,a31\n"
"v_subrev_f32 v12,s13,v12\n"
"v_fma_f32 v12,v12,v11,s13\n"
AW "a31,v12\n"
AR "v12,a51\n"
"v_subrev_f32 v12,s13,v12\n"
"v_fma_f32 v12,v12,v11,s13\n"
AW "a51,v12\n"
AR "v12,a71\n"
"v_subrev_f32 v12,s13,v12\n"
"v_fma_f32 v12,v12,v11,s13\n"
AW "a71,v12\n"
AR "v12,a91\n"
"v_subrev_f32 v12,s13,v12\n"
"v_fma_f32 v12,v12,v11,s13\n"
AW "a91,v12\n"
AR "v12,a111\n"
"v_subrev_f32 v12,s13,v12\n"
"v_fma_f32 v12,v12,v11,s13\n"
AW "a111,v12\n"
AR "v12,a131\n"
"v_subrev_f32 v12,s13,v12\n"
"v_fma_f32 v12,v12,v11,s13\n"
AW "a131,v12\n"
AR "v12,a151\n"
"v_subrev_f32 v12,s13,v12\n"
"v_fma_f32 v12,v12,v11,s13\n"
AW "a151,v12\n"
AR "v12,a171\n"
"v_subrev_f32 v12,s13,v12\n"
"v_fma_f32 v12,v12,v11,s13\n"
AW "a171,v12\n"
AR "v12,a191\n"
"v_subrev_f32 v12,s13,v12\n"
"v_fma_f32 v12,v12,v11,s13\n"
AW "a191,v12\n"
AR "v12,a211\n"
"v_subrev_f32 v12,s13,v12\n"
"v_fma_f32 v12,v12,v11,s13\n"
AW "a211,v12\n"
AR "v12,a231\n"
"v_subrev_f32 v12,s13,v12\n"
"v_fma_f32 v12,v12,v11,s13\n"
AW "a231,v12\n"
"v_mul_f32 v54,v59,v11\n"
"v_sub_f32 v40,v40,v10\n"
"v_sub_f32 v41,v41,v10\n"
"v_sub_f32 v42,v42,v10\n"
"v_sub_f32 v43,v43,v10\n"
"v_sub_f32 v100,v100,v10\n"
"v_sub_f32 v101,v101,v10\n"
"v_sub_f32 v102,v102,v10\n"
"v_sub_f32 v103,v103,v10\n"
"v_sub_f32 v80,v80,v10\n"
"v_sub_f32 v81,v81,v10\n"
"v_sub_f32 v82,v82,v10\n"
"v_sub_f32 v83,v83,v10\n"
"v_sub_f32 v120,v120,v10\n"
"v_sub_f32 v121,v121,v10\n"
"v_sub_f32 v122,v122,v10\n"
"v_sub_f32 v123,v123,v10\n"
"v_exp_f32 v80,v80\n"
"v_exp_f32 v81,v81\n"
"v_exp_f32 v82,v82\n"
"v_exp_f32 v83,v83\n"
"v_exp_f32 v120,v120\n"
"v_exp_f32 v121,v121\n"
"v_exp_f32 v122,v122\n"
"v_exp_f32 v123,v123\n"
"v_add_f32 v54,v54,v80\n"
"v_add_f32 v54,v54,v81\n"
"v_add_f32 v54,v54,v82\n"
"v_add_f32 v54,v54,v83\n"
"v_add_f32 v54,v54,v120\n"
"v_add_f32 v54,v54,v121\n"
"v_add_f32 v54,v54,v122\n"
"v_add_f32_e64 v54,v54,v123\n"
CV "v80,v80,v81\n"
CV "v81,v82,v83\n"
CV "v82,v120,v121\n"
CV "v83,v122,v123\n"
"v_max3_f32 v10,v84,v85,v86\n"
"v_max3_f32 v11,v87,v124,v125\n"
"v_max3_f32 v10,v10,v126,v127\n"
"v_max_f32 v10,v10,v11\n"
"v_mov_b32 v11,v10\n"
"s_nop 1\n"
"v_permlane16_swap_b32 v10,v11\n"
"v_max_f32 v10,v10,v11\n"
"v_mov_b32 v11,v10\n"
"s_nop 1\n"
"v_permlane32_swap_b32 v10,v11\n"
"v_max_f32_e64 v10,v10,v11\n"
"v_add_f32 v10,0x41300000,v10\n"
"v_max_f32 v10,0,v10\n"
"v_exp_f32 v11,-v10\n"
AR "v12,a12\n"
"v_subrev_f32 v12,s13,v12\n"
"v_fma_f32 v12,v12,v11,s13\n"
AW "a12,v12\n"
AR "v12,a32\n"
"v_subrev_f32 v12,s13,v12\n"
"v_fma_f32 v12,v12,v11,s13\n"
AW "a32,v12\n"
AR "v12,a52\n"
"v_subrev_f32 v12,s13,v12\n"
"v_fma_f32 v12,v12,v11,s13\n"
AW "a52,v12\n"
AR "v12,a72\n"
"v_subrev_f32 v12,s13,v12\n"
"v_fma_f32 v12,v12,v11,s13\n"
AW "a72,v12\n"
AR "v12,a92\n"
"v_subrev_f32 v12,s13,v12\n"
"v_fma_f32 v12,v12,v11,s13\n"
AW "a92,v12\n"
AR "v12,a112\n"
"v_subrev_f32 v12,s13,v12\n"
"v_fma_f32 v12,v12,v11,s13\n"
AW "a112,v12\n"
AR "v12,a132\n"
"v_subrev_f32 v12,s13,v12\n"
"v_fma_f32 v12,v12,v11,s13\n"
AW "a132,v12\n"
AR "v12,a152\n"
"v_subrev_f32 v12,s13,v12\n"
"v_fma_f32 v12,v12,v11,s13\n"
AW "a152,v12\n"
AR "v12,a172\n"
"v_subrev_f32 v12,s13,v12\n"
"v_fma_f32 v12,v12,v11,s13\n"
AW "a172,v12\n"
AR "v12,a192\n"
"v_subrev_f32 v12,s13,v12\n"
"v_fma_f32 v12,v12,v11,s13\n"
AW "a192,v12\n"
AR "v12,a212\n"
"v_subrev_f32 v12,s13,v12\n"
"v_fma_f32 v12,v12,v11,s13\n"
AW "a212,v12\n"
AR "v12,a232\n"
"v_subrev_f32 v12,s13,v12\n"
"v_fma_f32 v12,v12,v11,s13\n"
AW "a232,v12\n"
AR "v12,a13\n"
"v_subrev_f32 v12,s13,v12\n"
"v_fma_f32 v12,v12,v11,s13\n"
AW "a13,v12\n"
AR "v12,a33\n"
"v_subrev_f32 v12,s13,v12\n"
"v_fma_f32 v12,v12,v11,s13\n"
AW "a33,v12\n"
AR "v12,a53\n"
"v_subrev_f32 v12,s13,v12\n"
"v_fma_f32 v12,v12,v11,s13\n"
AW "a53,v12\n"
AR "v12,a73\n"
"v_subrev_f32 v12,s13,v12\n"
"v_fma_f32 v12,v12,v11,s13\n"
AW "a73,v12\n"
AR "v12,a93\n"
"v_subrev_f32 v12,s13,v12\n"
"v_fma_f32 v12,v12,v11,s13\n"
AW "a93,v12\n"
AR "v12,a113\n"
"v_subrev_f32 v12,s13,v12\n"
"v_fma_f32 v12,v12,v11,s13\n"
AW "a113,v12\n"
AR "v12,a133\n"
"v_subrev_f32 v12,s13,v12\n"
"v_fma_f32 v12,v12,v11,s13\n"
AW "a133,v12\n"
AR "v12,a153\n"
"v_subrev_f32 v12,s13,v12\n"
"v_fma_f32 v12,v12,v11,s13\n"
AW "a153,v12\n"
AR "v12,a173\n"
"v_subrev_f32 v12,s13,v12\n"
"v_fma_f32 v12,v12,v11,s13\n"
AW "a173,v12\n"
AR "v12,a193\n"
"v_subrev_f32 v12,s13,v12\n"
"v_fma_f32 v12,v12,v11,s13\n"
AW "a193,v12\n"
AR "v12,a213\n"
"v_subrev_f32 v12,s13,v12\n"
"v_fma_f32 v12,v12,v11,s13\n"
AW "a213,v12\n"
AR "v12,a233\n"
"v_subrev_f32 v12,s13,v12\n"
"v_fma_f32 v12,v12,v11,s13\n"
AW "a233,v12\n"
AR "v12,a14\n"
"v_subrev_f32 v12,s13,v12\n"
"v_fma_f32 v12,v12,v11,s13\n"
AW "a14,v12\n"
AR "v12,a34\n"
"v_subrev_f32 v12,s13,v12\n"
"v_fma_f32 v12,v12,v11,s13\n"
AW "a34,v12\n"
AR "v12,a54\n"
"v_subrev_f32 v12,s13,v12\n"
"v_fma_f32 v12,v12,v11,s13\n"
AW "a54,v12\n"
AR "v12,a74\n"
"v_subrev_f32 v12,s13,v12\n"
"v_fma_f32 v12,v12,v11,s13\n"
AW "a74,v12\n"
AR "v12,a94\n"
"v_subrev_f32 v12,s13,v12\n"
"v_fma_f32 v12,v12,v11,s13\n"
AW "a94,v12\n"
AR "v12,a114\n"
"v_subrev_f32 v12,s13,v12\n"
"v_fma_f32 v12,v12,v11,s13\n"
AW "a114,v12\n"
AR "v12,a134\n"
"v_subrev_f32 v12,s13,v12\n"
"v_fma_f32 v12,v12,v11,s13\n"
AW "a134,v12\n"
AR "v12,a154\n"
"v_subrev_f32 v12,s13,v12\n"
"v_fma_f32 v12,v12,v11,s13\n"
AW "a154,v12\n"
AR "v12,a174\n"
"v_subrev_f32 v12,s13,v12\n"
"v_fma_f32 v12,v12,v11,s13\n"
AW "a174,v12\n"
AR "v12,a194\n"
"v_subrev_f32 v12,s13,v12\n"
"v_fma_f32 v12,v12,v11,s13\n"
AW "a194,v12\n"
AR "v12,a214\n"
"v_subrev_f32 v12,s13,v12\n"
"v_fma_f32 v12,v12,v11,s13\n"
AW "a214,v12\n"
AR "v12,a234\n"
"v_subrev_f32 v12,s13,v12\n"
"v_fma_f32 v12,v12,v11,s13\n"
AW "a234,v12\n"
AR "v12,a15\n"
"v_subrev_f32 v12,s13,v12\n"
"v_fma_f32 v12,v12,v11,s13\n"
AW "a15,v12\n"
AR "v12,a35\n"
"v_subrev_f32 v12,s13,v12\n"
"v_fma_f32 v12,v12,v11,s13\n"
AW "a35,v12\n"
AR "v12,a55\n"
"v_subrev_f32 v12,s13,v12\n"
"v_fma_f32 v12,v12,v11,s13\n"
AW "a55,v12\n"
AR "v12,a75\n"
"v_subrev_f32 v12,s13,v12\n"
"v_fma_f32 v12,v12,v11,s13\n"
AW "a75,v12\n"
AR "v12,a95\n"
"v_subrev_f32 v12,s13,v12\n"
"v_fma_f32 v12,v12,v11,s13\n"
AW "a95,v12\n"
AR "v12,a115\n"
"v_subrev_f32 v12,s13,v12\n"
"v_fma_f32 v12,v12,v11,s13\n"
AW "a115,v12\n"
AR "v12,a135\n"
"v_subrev_f32 v12,s13,v12\n"
"v_fma_f32 v12,v12,v11,s13\n"
AW "a135,v12\n"
AR "v12,a155\n"
"v_subrev_f32 v12,s13,v12\n"
"v_fma_f32 v12,v12,v11,s13\n"
AW "a155,v12\n"
AR "v12,a175\n"
"v_subrev_f32 v12,s13,v12\n"
"v_fma_f32 v12,v12,v11,s13\n"
AW "a175,v12\n"
AR "v12,a195\n"
"v_subrev_f32 v12,s13,v12\n"
"v_fma_f32 v12,v12,v11,s13\n"
AW "a195,v12\n"
AR "v12,a215\n"
"v_subrev_f32 v12,s13,v12\n"
"v_fma_f32 v12,v12,v11,s13\n"
AW "a215,v12\n"
AR "v12,a235\n"
"v_subrev_f32 v12,s13,v12\n"
"v_fma_f32 v12,v12,v11,s13\n"
AW "a235,v12\n"
"v_mul_f32 v55,v60,v11\n"
"v_sub_f32 v44,v44,v10\n"
"v_sub_f32 v45,v45,v10\n"
"v_sub_f32 v46,v46,v10\n"
"v_sub_f32 v47,v47,v10\n"
"v_sub_f32 v104,v104,v10\n"
"v_sub_f32 v105,v105,v10\n"
"v_sub_f32 v106,v106,v10\n"
"v_sub_f32 v107,v107,v10\n"
"v_sub_f32 v84,v84,v10\n"
"v_sub_f32 v85,v85,v10\n"
"v_sub_f32 v86,v86,v10\n"
"v_sub_f32 v87,v87,v10\n"
"v_sub_f32 v124,v124,v10\n"
"v_sub_f32 v125,v125,v10\n"
"v_sub_f32 v126,v126,v10\n"
"v_sub_f32 v127,v127,v10\n"
"v_exp_f32 v84,v84\n"
"v_exp_f32 v85,v85\n"
"v_exp_f32 v86,v86\n"
"v_exp_f32 v87,v87\n"
"v_exp_f32 v124,v124\n"
"v_exp_f32 v125,v125\n"
"v_exp_f32 v126,v126\n"
"v_exp_f32 v127,v127\n"
"v_add_f32 v55,v55,v84\n"
"v_add_f32 v55,v55,v85\n"
"v_add_f32 v55,v55,v86\n"
"v_add_f32 v55,v55,v87\n"
"v_add_f32 v55,v55,v124\n"
"v_add_f32 v55,v55,v125\n"
"v_add_f32 v55,v55,v126\n"
"v_add_f32 v55,v55,v127\n"
CV "v84,v84,v85\n"
CV "v85,v86,v87\n"
CV "v86,v124,v125\n"
CV "v87,v126,v127\n"
"v_max3_f32 v10,v88,v89,v90\n"
"v_max3_f32 v11,v91,v128,v129\n"
"v_max3_f32 v10,v10,v130,v131\n"
"v_max_f32 v10,v10,v11\n"
"v_mov_b32 v11,v10\n"
"s_nop 1\n"
"v_permlane16_swap_b32 v10,v11\n"
"v_max_f32 v10,v10,v11\n"
"v_mov_b32 v11,v10\n"
"s_nop 1\n"
"v_permlane32_swap_b32 v10,v11\n"
"v_max_f32 v10,v10,v11\n"
"v_add_f32 v10,0x41300000,v10\n"
"v_max_f32 v10,0,v10\n"
"v_exp_f32 v11,-v10\n"
AR "v12,a16\n"
"v_subrev_f32 v12,s13,v12\n"
"v_fma_f32 v12,v12,v11,s13\n"
AW "a16,v12\n"
AR "v12,a36\n"
"v_subrev_f32 v12,s13,v12\n"
"v_fma_f32 v12,v12,v11,s13\n"
AW "a36,v12\n"
AR "v12,a56\n"
"v_subrev_f32 v12,s13,v12\n"
"v_fma_f32 v12,v12,v11,s13\n"
AW "a56,v12\n"
AR "v12,a76\n"
"v_subrev_f32 v12,s13,v12\n"
"v_fma_f32 v12,v12,v11,s13\n"
AW "a76,v12\n"
AR "v12,a96\n"
"v_subrev_f32 v12,s13,v12\n"
"v_fma_f32 v12,v12,v11,s13\n"
AW "a96,v12\n"
AR "v12,a116\n"
"v_subrev_f32 v12,s13,v12\n"
"v_fma_f32 v12,v12,v11,s13\n"
AW "a116,v12\n"
AR "v12,a136\n"
"v_subrev_f32 v12,s13,v12\n"
"v_fma_f32 v12,v12,v11,s13\n"
AW "a136,v12\n"
AR "v12,a156\n"
"v_subrev_f32 v12,s13,v12\n"
"v_fma_f32 v12,v12,v11,s13\n"
AW "a156,v12\n"
AR "v12,a176\n"
"v_subrev_f32 v12,s13,v12\n"
"v_fma_f32 v12,v12,v11,s13\n"
AW "a176,v12\n"
AR "v12,a196\n"
"v_subrev_f32 v12,s13,v12\n"
"v_fma_f32 v12,v12,v11,s13\n"
AW "a196,v12\n"
AR "v12,a216\n"
"v_subrev_f32 v12,s13,v12\n"
"v_fma_f32 v12,v12,v11,s13\n"
AW "a216,v12\n"
AR "v12,a236\n"
"v_subrev_f32 v12,s13,v12\n"
"v_fma_f32 v12,v12,v11,s13\n"
AW "a236,v12\n"
AR "v12,a17\n"
"v_subrev_f32 v12,s13,v12\n"
"v_fma_f32 v12,v12,v11,s13\n"
AW "a17,v12\n"
AR "v12,a37\n"
"v_subrev_f32 v12,s13,v12\n"
"v_fma_f32 v12,v12,v11,s13\n"
AW "a37,v12\n"
AR "v12,a57\n"
"v_subrev_f32 v12,s13,v12\n"
"v_fma_f32 v12,v12,v11,s13\n"
AW "a57,v12\n"
AR "v12,a77\n"
"v_subrev_f32 v12,s13,v12\n"
"v_fma_f32 v12,v12,v11,s13\n"
AW "a77,v12\n"
AR "v12,a97\n"
"v_subrev_f32 v12,s13,v12\n"
"v_fma_f32 v12,v12,v11,s13\n"
AW "a97,v12\n"
AR "v12,a117\n"
"v_subrev_f32 v12,s13,v12\n"
"v_fma_f32 v12,v12,v11,s13\n"
AW "a117,v12\n"
AR "v12,a137\n"
"v_subrev_f32 v12,s13,v12\n"
"v_fma_f32 v12,v12,v11,s13\n"
AW "a137,v12\n"
AR "v12,a157\n"
"v_subrev_f32 v12,s13,v12\n"
"v_fma_f32 v12,v12,v11,s13\n"
AW "a157,v12\n"
AR "v12,a177\n"
"v_subrev_f32 v12,s13,v12\n"
"v_fma_f32 v12,v12,v11,s13\n"
AW "a177,v12\n"
AR "v12,a197\n"
"v_subrev_f32 v12,s13,v12\n"
"v_fma_f32 v12,v12,v11,s13\n"
AW "a197,v12\n"
AR "v12,a217\n"
"v_subrev_f32 v12,s13,v12\n"
"v_fma_f32 v12,v12,v11,s13\n"
AW "a217,v12\n"
AR "v12,a237\n"
"v_subrev_f32 v12,s13,v12\n"
"v_fma_f32 v12,v12,v11,s13\n"
AW "a237,v12\n"
AR "v12,a18\n"
"v_subrev_f32 v12,s13,v12\n"
"v_fma_f32 v12,v12,v11,s13\n"
AW "a18,v12\n"
AR "v12,a38\n"
"v_subrev_f32 v12,s13,v12\n"
"v_fma_f32 v12,v12,v11,s13\n"
AW "a38,v12\n"
AR "v12,a58\n"
"v_subrev_f32 v12,s13,v12\n"
"v_fma_f32 v12,v12,v11,s13\n"
AW "a58,v12\n"
AR "v12,a78\n"
"v_subrev_f32 v12,s13,v12\n"
"v_fma_f32 v12,v12,v11,s13\n"
AW "a78,v12\n"
AR "v12,a98\n"
"v_subrev_f32 v12,s13,v12\n"
"v_fma_f32 v12,v12,v11,s13\n"
AW "a98,v12\n"
AR "v12,a118\n"
"v_subrev_f32 v12,s13,v12\n"
"v_fma_f32 v12,v12,v11,s13\n"
AW "a118,v12\n"
AR "v12,a138\n"
"v_subrev_f32 v12,s13,v12\n"
"v_fma_f32 v12,v12,v11,s13\n"
AW "a138,v12\n"
AR "v12,a158\n"
"v_subrev_f32 v12,s13,v12\n"
"v_fma_f32 v12,v12,v11,s13\n"
AW "a158,v12\n"
AR "v12,a178\n"
"v_subrev_f32 v12,s13,v12\n"
"v_fma_f32 v12,v12,v11,s13\n"
AW "a178,v12\n"
AR "v12,a198\n"
"v_subrev_f32 v12,s13,v12\n"
"v_fma_f32 v12,v12,v11,s13\n"
AW "a198,v12\n"
AR "v12,a218\n"
"v_subrev_f32 v12,s13,v12\n"
"v_fma_f32 v12,v12,v11,s13\n"
AW "a218,v12\n"
AR "v12,a238\n"
"v_subrev_f32 v12,s13,v12\n"
"v_fma_f32 v12,v12,v11,s13\n"
AW "a238,v12\n"
AR "v12,a19\n"
"v_subrev_f32 v12,s13,v12\n"
"v_fma_f32 v12,v12,v11,s13\n"
AW "a19,v12\n"
AR "v12,a39\n"
"v_subrev_f32 v12,s13,v12\n"
"v_fma_f32 v12,v12,v11,s13\n"
AW "a39,v12\n"
AR "v12,a59\n"
"v_subrev_f32 v12,s13,v12\n"
"v_fma_f32 v12,v12,v11,s13\n"
AW "a59,v12\n"
AR "v12,a79\n"
"v_subrev_f32 v12,s13,v12\n"
"v_fma_f32 v12,v12,v11,s13\n"
AW "a79,v12\n"
AR "v12,a99\n"
"v_subrev_f32 v12,s13,v12\n"
"v_fma_f32 v12,v12,v11,s13\n"
AW "a99,v12\n"
AR "v12,a119\n"
"v_subrev_f32 v12,s13,v12\n"
"v_fma_f32 v12,v12,v11,s13\n"
AW "a119,v12\n"
AR "v12,a139\n"
"v_subrev_f32 v12,s13,v12\n"
"v_fma_f32 v12,v12,v11,s13\n"
AW "a139,v12\n"
AR "v12,a159\n"
"v_subrev_f32 v12,s13,v12\n"
"v_fma_f32 v12,v12,v11,s13\n"
AW "a159,v12\n"
AR "v12,a179\n"
"v_subrev_f32 v12,s13,v12\n"
"v_fma_f32 v12,v12,v11,s13\n"
AW "a179,v12\n"
AR "v12,a199\n"
"v_subrev_f32 v12,s13,v12\n"
"v_fma_f32 v12,v12,v11,s13\n"
AW "a199,v12\n"
AR "v12,a219\n"
"v_subrev_f32 v12,s13,v12\n"
"v_fma_f32 v12,v12,v11,s13\n"
AW "a219,v12\n"
AR "v12,a239\n"
"v_subrev_f32 v12,s13,v12\n"
"v_fma_f32 v12,v12,v11,s13\n"
AW "a239,v12\n"
"v_mul_f32 v56,v61,v11\n"
"v_sub_f32 v48,v48,v10\n"
"v_sub_f32 v49,v49,v10\n"
"v_sub_f32 v50,v50,v10\n"
"v_sub_f32 v51,v51,v10\n"
"v_sub_f32 v108,v108,v10\n"
"v_sub_f32 v109,v109,v10\n"
"v_sub_f32 v110,v110,v10\n"
"v_sub_f32 v111,v111,v10\n"
"v_sub_f32 v88,v88,v10\n"
"v_sub_f32 v89,v89,v10\n"
"v_sub_f32 v90,v90,v10\n"
"v_sub_f32 v91,v91,v10\n"
"v_sub_f32 v128,v128,v10\n"
"v_sub_f32 v129,v129,v10\n"
"v_sub_f32 v130,v130,v10\n"
"v_sub_f32 v131,v131,v10\n"
"v_exp_f32 v88,v88\n"
"v_exp_f32 v89,v89\n"
"v_exp_f32 v90,v90\n"
"v_exp_f32 v91,v91\n"
"v_exp_f32 v128,v128\n"
"v_exp_f32 v129,v129\n"
"v_exp_f32 v130,v130\n"
"v_exp_f32 v131,v131\n"
"v_add_f32 v56,v56,v88\n"
"v_add_f32 v56,v56,v89\n"
"v_add_f32 v56,v56,v90\n"
"v_add_f32 v56,v56,v91\n"
"v_add_f32 v56,v56,v128\n"
"v_add_f32 v56,v56,v129\n"
"v_add_f32 v56,v56,v130\n"
"v_add_f32_e64 v56,v56,v131\n"
CV "v88,v88,v89\n"
CV "v89,v90,v91\n"
CV "v90,v128,v129\n"
CV "v91,v130,v131\n"
"s_nop 7\n"
"s_setpc_b64 s[52:53]\n"
".Lslow_1:\n"
"s_nop 7\n"
"s_nop 7\n"
"v_and_b32 v10,240,v9\n"
"v_mul_u32_u24 v10,24,v10\n"
"v_lshrrev_b32 v11,4,v9\n"
"v_and_or_b32 v10,v11,48,v10\n"
"s_mov_b32 s59,s34\n"
"s_mov_b32 s34,s20\n"
"s_add_u32 s58,s74,0\n"
"s_nop 3\n"
LD "v[58:61],v10,s[32:35],s58 offen\n"
"s_waitcnt vmcnt(0)\n"
MF "v[92:95],v[58:61],v[136:139],v[32:35]\n"
MF "v[96:99],v[58:61],v[140:143],v[36:39]\n"
MF "v[100:103],v[58:61],v[144:147],v[40:43]\n"
MF "v[104:107],v[58:61],v[148:151],v[44:47]\n"
MF "v[108:111],v[58:61],v[152:155],v[48:51]\n"
"s_add_u32 s58,s74,0\n"
"s_nop 3\n"
LD "v[58:61],v10,s[32:35],s58 offen offset:64\n"
"s_waitcnt vmcnt(0)\n"
MF "v[92:95],v[58:61],v[156:159],v[92:95]\n"
MF "v[96:99],v[58:61],v[160:163],v[96:99]\n"
MF "v[100:103],v[58:61],v[164:167],v[100:103]\n"
MF "v[104:107],v[58:61],v[168:171],v[104:107]\n"
MF "v[108:111],v[58:61],v[172:175],v[108:111]\n"
"s_add_u32 s58,s74,0\n"
"s_nop 3\n"
LD "v[58:61],v10,s[32:35],s58 offen offset:128\n"
"s_waitcnt vmcnt(0)\n"
MF "v[92:95],v[58:61],v[176:179],v[92:95]\n"
MF "v[96:99],v[58:61],v[180:183],v[96:99]\n"
MF "v[100:103],v[58:61],v[184:187],v[100:103]\n"
MF "v[104:107],v[58:61],v[188:191],v[104:107]\n"
MF "v[108:111],v[58:61],v[192:195],v[108:111]\n"
"s_add_u32 s58,s74,0\n"
"s_nop 3\n"
LD "v[58:61],v10,s[32:35],s58 offen offset:192\n"
"s_waitcnt vmcnt(0)\n"
MF "v[92:95],v[58:61],v[196:199],v[92:95]\n"
MF "v[96:99],v[58:61],v[200:203],v[96:99]\n"
MF "v[100:103],v[58:61],v[204:207],v[100:103]\n"
MF "v[104:107],v[58:61],v[208:211],v[104:107]\n"
MF "v[108:111],v[58:61],v[212:215],v[108:111]\n"
"s_add_u32 s58,s74,0\n"
"s_nop 3\n"
LD "v[58:61],v10,s[32:35],s58 offen offset:256\n"
"s_waitcnt vmcnt(0)\n"
MF "v[92:95],v[58:61],v[216:219],v[92:95]\n"
MF "v[96:99],v[58:61],v[220:223],v[96:99]\n"
MF "v[100:103],v[58:61],v[224:227],v[100:103]\n"
MF "v[104:107],v[58:61],v[228:231],v[104:107]\n"
MF "v[108:111],v[58:61],v[232:235],v[108:111]\n"
"s_add_u32 s58,s74,0\n"
"s_nop 3\n"
LD "v[58:61],v10,s[32:35],s58 offen offset:320\n"
"s_waitcnt vmcnt(0)\n"
MF "v[92:95],v[58:61],v[236:239],v[92:95]\n"
MF "v[96:99],v[58:61],v[240:243],v[96:99]\n"
MF "v[100:103],v[58:61],v[244:247],v[100:103]\n"
MF "v[104:107],v[58:61],v[248:251],v[104:107]\n"
MF "v[108:111],v[58:61],v[252:255],v[108:111]\n"
"s_add_u32 s58,s74,6144\n"
"s_nop 3\n"
LD "v[58:61],v10,s[32:35],s58 offen\n"
"s_waitcnt vmcnt(0)\n"
MF "v[112:115],v[58:61],v[136:139],v[32:35]\n"
MF "v[116:119],v[58:61],v[140:143],v[36:39]\n"
MF "v[120:123],v[58:61],v[144:147],v[40:43]\n"
MF "v[124:127],v[58:61],v[148:151],v[44:47]\n"
MF "v[128:131],v[58:61],v[152:155],v[48:51]\n"
"s_add_u32 s58,s74,6144\n"
"s_nop 3\n"
LD "v[58:61],v10,s[32:35],s58 offen offset:64\n"
"s_waitcnt vmcnt(0)\n"
MF "v[112:115],v[58:61],v[156:159],v[112:115]\n"
MF "v[116:119],v[58:61],v[160:163],v[116:119]\n"
MF "v[120:123],v[58:61],v[164:167],v[120:123]\n"
MF "v[124:127],v[58:61],v[168:171],v[124:127]\n"
MF "v[128:131],v[58:61],v[172:175],v[128:131]\n"
"s_add_u32 s58,s74,6144\n"
"s_nop 3\n"
LD "v[58:61],v10,s[32:35],s58 offen offset:128\n"
"s_waitcnt vmcnt(0)\n"
MF "v[112:115],v[58:61],v[176:179],v[112:115]\n"
MF "v[116:119],v[58:61],v[180:183],v[116:119]\n"
MF "v[120:123],v[58:61],v[184:187],v[120:123]\n"
MF "v[124:127],v[58:61],v[188:191],v[124:127]\n"
MF "v[128:131],v[58:61],v[192:195],v[128:131]\n"
"s_add_u32 s58,s74,6144\n"
"s_nop 3\n"
LD "v[58:61],v10,s[32:35],s58 offen offset:192\n"
"s_waitcnt vmcnt(0)\n"
MF "v[112:115],v[58:61],v[196:199],v[112:115]\n"
MF "v[116:119],v[58:61],v[200:203],v[116:119]\n"
MF "v[120:123],v[58:61],v[204:207],v[120:123]\n"
MF "v[124:127],v[58:61],v[208:211],v[124:127]\n"
MF "v[128:131],v[58:61],v[212:215],v[128:131]\n"
"s_add_u32 s58,s74,6144\n"
"s_nop 3\n"
LD "v[58:61],v10,s[32:35],s58 offen offset:256\n"
"s_waitcnt vmcnt(0)\n"
MF "v[112:115],v[58:61],v[216:219],v[112:115]\n"
MF "v[116:119],v[58:61],v[220:223],v[116:119]\n"
MF "v[120:123],v[58:61],v[224:227],v[120:123]\n"
MF "v[124:127],v[58:61],v[228:231],v[124:127]\n"
MF "v[128:131],v[58:61],v[232:235],v[128:131]\n"
"s_add_u32 s58,s74,6144\n"
"s_nop 3\n"
LD "v[58:61],v10,s[32:35],s58 offen offset:320\n"
"s_waitcnt vmcnt(0)\n"
MF "v[112:115],v[58:61],v[236:239],v[112:115]\n"
MF "v[116:119],v[58:61],v[240:243],v[116:119]\n"
MF "v[120:123],v[58:61],v[244:247],v[120:123]\n"
MF "v[124:127],v[58:61],v[248:251],v[124:127]\n"
MF "v[128:131],v[58:61],v[252:255],v[128:131]\n"
"s_nop 7\n"
"s_mov_b32 s34,s59\n"
"v_max3_f32 v10,v92,v93,v94\n"
"s_nop 0\n"
"v_max3_f32 v11,v95,v112,v113\n"
"v_max3_f32 v10,v10,v114,v115\n"
"v_max_f32 v10,v10,v11\n"
"v_mov_b32 v11,v10\n"
"s_nop 1\n"
"v_permlane16_swap_b32 v10,v11\n"
"v_max_f32 v10,v10,v11\n"
"v_mov_b32 v11,v10\n"
"s_nop 1\n"
"v_permlane32_swap_b32 v10,v11\n"
"v_max_f32 v10,v10,v11\n"
"v_add_f32 v10,0x41300000,v10\n"
"v_max_f32_e64 v10,0,v10\n"
"v_exp_f32 v11,-v10\n"
AR "v12,a0\n"
"v_subrev_f32 v12,s13,v12\n"
"v_fma_f32 v12,v12,v11,s13\n"
AW "a0,v12\n"
AR "v12,a20\n"
"v_subrev_f32 v12,s13,v12\n"
"v_fma_f32 v12,v12,v11,s13\n"
AW "a20,v12\n"
AR "v12,a40\n"
"v_subrev_f32 v12,s13,v12\n"
"v_fma_f32 v12,v12,v11,s13\n"
AW "a40,v12\n"
AR "v12,a60\n"
"v_subrev_f32 v12,s13,v12\n"
"v_fma_f32 v12,v12,v11,s13\n"
AW "a60,v12\n"
AR "v12,a80\n"
"v_subrev_f32 v12,s13,v12\n"
"v_fma_f32 v12,v12,v11,s13\n"
AW "a80,v12\n"
AR "v12,a100\n"
"v_subrev_f32 v12,s13,v12\n"
"v_fma_f32 v12,v12,v11,s13\n"
AW "a100,v12\n"
AR "v12,a120\n"
"v_subrev_f32 v12,s13,v12\n"
"v_fma_f32 v12,v12,v11,s13\n"
AW "a120,v12\n"
AR "v12,a140\n"
"v_subrev_f32 v12,s13,v12\n"
"v_fma_f32 v12,v12,v11,s13\n"
AW "a140,v12\n"
AR "v12,a160\n"
"v_subrev_f32 v12,s13,v12\n"
"v_fma_f32 v12,v12,v11,s13\n"
AW "a160,v12\n"
AR "v12,a180\n"
"v_subrev_f32 v12,s13,v12\n"
"v_fma_f32 v12,v12,v11,s13\n"
AW "a180,v12\n"
AR "v12,a200\n"
"v_subrev_f32 v12,s13,v12\n"
"v_fma_f32 v12,v12,v11,s13\n"
AW "a200,v12\n"
AR "v12,a220\n"
"v_subrev_f32 v12,s13,v12\n"
"v_fma_f32 v12,v12,v11,s13\n"
AW "a220,v12\n"
AR "v12,a1\n"
"v_subrev_f32 v12,s13,v12\n"
"v_fma_f32 v12,v12,v11,s13\n"
AW "a1,v12\n"
AR "v12,a21\n"
"v_subrev_f32 v12,s13,v12\n"
"v_fma_f32 v12,v12,v11,s13\n"
AW "a21,v12\n"
AR "v12,a41\n"
"v_subrev_f32 v12,s13,v12\n"
"v_fma_f32 v12,v12,v11,s13\n"
AW "a41,v12\n"
AR "v12,a61\n"
"v_subrev_f32 v12,s13,v12\n"
"v_fma_f32 v12,v12,v11,s13\n"
AW "a61,v12\n"
AR "v12,a81\n"
"v_subrev_f32 v12,s13,v12\n"
"v_fma_f32 v12,v12,v11,s13\n"
AW "a81,v12\n"
AR "v12,a101\n"
"v_subrev_f32 v12,s13,v12\n"
"v_fma_f32 v12,v12,v11,s13\n"
AW "a101,v12\n"
AR "v12,a121\n"
"v_subrev_f32 v12,s13,v12\n"
"v_fma_f32 v12,v12,v11,s13\n"
AW "a121,v12\n"
AR "v12,a141\n"
"v_subrev_f32 v12,s13,v12\n"
"v_fma_f32 v12,v12,v11,s13\n"
AW "a141,v12\n"
AR "v12,a161\n"
"v_subrev_f32 v12,s13,v12\n"
"v_fma_f32 v12,v12,v11,s13\n"
AW "a161,v12\n"
AR "v12,a181\n"
"v_subrev_f32 v12,s13,v12\n"
"v_fma_f32 v12,v12,v11,s13\n"
AW "a181,v12\n"
AR "v12,a201\n"
"v_subrev_f32 v12,s13,v12\n"
"v_fma_f32 v12,v12,v11,s13\n"
AW "a201,v12\n"
AR "v12,a221\n"
"v_subrev_f32 v12,s13,v12\n"
"v_fma_f32 v12,v12,v11,s13\n"
AW "a221,v12\n"
AR "v12,a2\n"
"v_subrev_f32 v12,s13,v12\n"
"v_fma_f32 v12,v12,v11,s13\n"
AW "a2,v12\n"
AR "v12,a22\n"
"v_subrev_f32 v12,s13,v12\n"
"v_fma_f32 v12,v12,v11,s13\n"
AW "a22,v12\n"
AR "v12,a42\n"
"v_subrev_f32 v12,s13,v12\n"
"v_fma_f32 v12,v12,v11,s13\n"
AW "a42,v12\n"
AR "v12,a62\n"
"v_subrev_f32 v12,s13,v12\n"
"v_fma_f32 v12,v12,v11,s13\n"
AW "a62,v12\n"
AR "v12,a82\n"
"v_subrev_f32 v12,s13,v12\n"
"v_fma_f32 v12,v12,v11,s13\n"
AW "a82,v12\n"
AR "v12,a102\n"
"v_subrev_f32 v12,s13,v12\n"
"v_fma_f32 v12,v12,v11,s13\n"
AW "a102,v12\n"
AR "v12,a122\n"
"v_subrev_f32 v12,s13,v12\n"
"v_fma_f32 v12,v12,v11,s13\n"
AW "a122,v12\n"
AR "v12,a142\n"
"v_subrev_f32 v12,s13,v12\n"
"v_fma_f32 v12,v12,v11,s13\n"
AW "a142,v12\n"
AR "v12,a162\n"
"v_subrev_f32 v12,s13,v12\n"
"v_fma_f32 v12,v12,v11,s13\n"
AW "a162,v12\n"
AR "v12,a182\n"
"v_subrev_f32 v12,s13,v12\n"
"v_fma_f32 v12,v12,v11,s13\n"
AW "a182,v12\n"
AR "v12,a202\n"
"v_subrev_f32 v12,s13,v12\n"
"v_fma_f32 v12,v12,v11,s13\n"
AW "a202,v12\n"
AR "v12,a222\n"
"v_subrev_f32 v12,s13,v12\n"
"v_fma_f32 v12,v12,v11,s13\n"
AW "a222,v12\n"
AR "v12,a3\n"
"v_subrev_f32 v12,s13,v12\n"
"v_fma_f32 v12,v12,v11,s13\n"
AW "a3,v12\n"
AR "v12,a23\n"
"v_subrev_f32 v12,s13,v12\n"
"v_fma_f32 v12,v12,v11,s13\n"
AW "a23,v12\n"
AR "v12,a43\n"
"v_subrev_f32 v12,s13,v12\n"
"v_fma_f32 v12,v12,v11,s13\n"
AW "a43,v12\n"
AR "v12,a63\n"
"v_subrev_f32 v12,s13,v12\n"
"v_fma_f32 v12,v12,v11,s13\n"
AW "a63,v12\n"
AR "v12,a83\n"
"v_subrev_f32 v12,s13,v12\n"
"v_fma_f32 v12,v12,v11,s13\n"
AW "a83,v12\n"
AR "v12,a103\n"
"v_subrev_f32 v12,s13,v12\n"
"v_fma_f32 v12,v12,v11,s13\n"
AW "a103,v12\n"
AR "v12,a123\n"
"v_subrev_f32 v12,s13,v12\n"
"v_fma_f32 v12,v12,v11,s13\n"
AW "a123,v12\n"
AR "v12,a143\n"
"v_subrev_f32 v12,s13,v12\n"
"v_fma_f32 v12,v12,v11,s13\n"
AW "a143,v12\n"
AR "v12,a163\n"
"v_subrev_f32 v12,s13,v12\n"
"v_fma_f32 v12,v12,v11,s13\n"
AW "a163,v12\n"
AR "v12,a183\n"
"v_subrev_f32 v12,s13,v12\n"
"v_fma_f32 v12,v12,v11,s13\n"
AW "a183,v12\n"
AR "v12,a203\n"
"v_subrev_f32 v12,s13,v12\n"
"v_fma_f32 v12,v12,v11,s13\n"
AW "a203,v12\n"
AR "v12,a223\n"
"v_subrev_f32 v12,s13,v12\n"
"v_fma_f32 v12,v12,v11,s13\n"
AW "a223,v12\n"
"v_mul_f32 v57,v52,v11\n"
"v_sub_f32 v32,v32,v10\n"
"v_sub_f32 v33,v33,v10\n"
"v_sub_f32 v34,v34,v10\n"
"v_sub_f32 v35,v35,v10\n"
"v_sub_f32 v72,v72,v10\n"
"v_sub_f32 v73,v73,v10\n"
"v_sub_f32 v74,v74,v10\n"
"v_sub_f32 v75,v75,v10\n"
"v_sub_f32 v92,v92,v10\n"
"v_sub_f32 v93,v93,v10\n"
"v_sub_f32 v94,v94,v10\n"
"v_sub_f32 v95,v95,v10\n"
"v_sub_f32 v112,v112,v10\n"
"v_sub_f32 v113,v113,v10\n"
"v_sub_f32 v114,v114,v10\n"
"v_sub_f32 v115,v115,v10\n"
"v_exp_f32 v92,v92\n"
"v_exp_f32 v93,v93\n"
"v_exp_f32 v94,v94\n"
"v_exp_f32 v95,v95\n"
"v_exp_f32 v112,v112\n"
"v_exp_f32 v113,v113\n"
"v_exp_f32 v114,v114\n"
"v_exp_f32 v115,v115\n"
"v_add_f32 v57,v57,v92\n"
"v_add_f32 v57,v57,v93\n"
"v_add_f32 v57,v57,v94\n"
"v_add_f32 v57,v57,v95\n"
"v_add_f32 v57,v57,v112\n"
"v_add_f32 v57,v57,v113\n"
"v_add_f32 v57,v57,v114\n"
"v_add_f32_e64 v57,v57,v115\n"
CV "v92,v92,v93\n"
CV "v93,v94,v95\n"
CV "v94,v112,v113\n"
CV "v95,v114,v115\n"
"v_max3_f32 v10,v96,v97,v98\n"
"v_max3_f32 v11,v99,v116,v117\n"
"v_max3_f32 v10,v10,v118,v119\n"
"v_max_f32 v10,v10,v11\n"
"v_mov_b32 v11,v10\n"
"s_nop 1\n"
"v_permlane16_swap_b32 v10,v11\n"
"v_max_f32 v10,v10,v11\n"
"v_mov_b32 v11,v10\n"
"s_nop 1\n"
"v_permlane32_swap_b32 v10,v11\n"
"v_max_f32 v10,v10,v11\n"
"v_add_f32 v10,0x41300000,v10\n"
"v_max_f32 v10,0,v10\n"
"v_exp_f32 v11,-v10\n"
AR "v12,a4\n"
"v_subrev_f32 v12,s13,v12\n"
"v_fma_f32 v12,v12,v11,s13\n"
AW "a4,v12\n"
AR "v12,a24\n"
"v_subrev_f32 v12,s13,v12\n"
"v_fma_f32 v12,v12,v11,s13\n"
AW "a24,v12\n"
AR "v12,a44\n"
"v_subrev_f32 v12,s13,v12\n"
"v_fma_f32 v12,v12,v11,s13\n"
AW "a44,v12\n"
AR "v12,a64\n"
"v_subrev_f32 v12,s13,v12\n"
"v_fma_f32 v12,v12,v11,s13\n"
AW "a64,v12\n"
AR "v12,a84\n"
"v_subrev_f32 v12,s13,v12\n"
"v_fma_f32 v12,v12,v11,s13\n"
AW "a84,v12\n"
AR "v12,a104\n"
"v_subrev_f32 v12,s13,v12\n"
"v_fma_f32 v12,v12,v11,s13\n"
AW "a104,v12\n"
AR "v12,a124\n"
"v_subrev_f32 v12,s13,v12\n"
"v_fma_f32 v12,v12,v11,s13\n"
AW "a124,v12\n"
AR "v12,a144\n"
"v_subrev_f32 v12,s13,v12\n"
"v_fma_f32 v12,v12,v11,s13\n"
AW "a144,v12\n"
AR "v12,a164\n"
"v_subrev_f32 v12,s13,v12\n"
"v_fma_f32 v12,v12,v11,s13\n"
AW "a164,v12\n"
AR "v12,a184\n"
"v_subrev_f32 v12,s13,v12\n"
"v_fma_f32 v12,v12,v11,s13\n"
AW "a184,v12\n"
AR "v12,a204\n"
"v_subrev_f32 v12,s13,v12\n"
"v_fma_f32 v12,v12,v11,s13\n"
AW "a204,v12\n"
AR "v12,a224\n"
"v_subrev_f32 v12,s13,v12\n"
"v_fma_f32 v12,v12,v11,s13\n"
AW "a224,v12\n"
AR "v12,a5\n"
"v_subrev_f32 v12,s13,v12\n"
"v_fma_f32 v12,v12,v11,s13\n"
AW "a5,v12\n"
AR "v12,a25\n"
"v_subrev_f32 v12,s13,v12\n"
"v_fma_f32 v12,v12,v11,s13\n"
AW "a25,v12\n"
AR "v12,a45\n"
"v_subrev_f32 v12,s13,v12\n"
"v_fma_f32 v12,v12,v11,s13\n"
AW "a45,v12\n"
AR "v12,a65\n"
"v_subrev_f32 v12,s13,v12\n"
"v_fma_f32 v12,v12,v11,s13\n"
AW "a65,v12\n"
AR "v12,a85\n"
"v_subrev_f32 v12,s13,v12\n"
"v_fma_f32 v12,v12,v11,s13\n"
AW "a85,v12\n"
AR "v12,a105\n"
"v_subrev_f32 v12,s13,v12\n"
"v_fma_f32 v12,v12,v11,s13\n"
AW "a105,v12\n"
AR "v12,a125\n"
"v_subrev_f32 v12,s13,v12\n"
"v_fma_f32 v12,v12,v11,s13\n"
AW "a125,v12\n"
AR "v12,a145\n"
"v_subrev_f32 v12,s13,v12\n"
"v_fma_f32 v12,v12,v11,s13\n"
AW "a145,v12\n"
AR "v12,a165\n"
"v_subrev_f32 v12,s13,v12\n"
"v_fma_f32 v12,v12,v11,s13\n"
AW "a165,v12\n"
AR "v12,a185\n"
"v_subrev_f32 v12,s13,v12\n"
"v_fma_f32 v12,v12,v11,s13\n"
AW "a185,v12\n"
AR "v12,a205\n"
"v_subrev_f32 v12,s13,v12\n"
"v_fma_f32 v12,v12,v11,s13\n"
AW "a205,v12\n"
AR "v12,a225\n"
"v_subrev_f32 v12,s13,v12\n"
"v_fma_f32 v12,v12,v11,s13\n"
AW "a225,v12\n"
AR "v12,a6\n"
"v_subrev_f32 v12,s13,v12\n"
"v_fma_f32 v12,v12,v11,s13\n"
AW "a6,v12\n"
AR "v12,a26\n"
"v_subrev_f32 v12,s13,v12\n"
"v_fma_f32 v12,v12,v11,s13\n"
AW "a26,v12\n"
AR "v12,a46\n"
"v_subrev_f32 v12,s13,v12\n"
"v_fma_f32 v12,v12,v11,s13\n"
AW "a46,v12\n"
AR "v12,a66\n"
"v_subrev_f32 v12,s13,v12\n"
"v_fma_f32 v12,v12,v11,s13\n"
AW "a66,v12\n"
AR "v12,a86\n"
"v_subrev_f32 v12,s13,v12\n"
"v_fma_f32 v12,v12,v11,s13\n"
AW "a86,v12\n"
AR "v12,a106\n"
"v_subrev_f32 v12,s13,v12\n"
"v_fma_f32 v12,v12,v11,s13\n"
AW "a106,v12\n"
AR "v12,a126\n"
"v_subrev_f32 v12,s13,v12\n"
"v_fma_f32 v12,v12,v11,s13\n"
AW "a126,v12\n"
AR "v12,a146\n"
"v_subrev_f32 v12,s13,v12\n"
"v_fma_f32 v12,v12,v11,s13\n"
AW "a146,v12\n"
AR "v12,a166\n"
"v_subrev_f32 v12,s13,v12\n"
"v_fma_f32 v12,v12,v11,s13\n"
AW "a166,v12\n"
AR "v12,a186\n"
"v_subrev_f32 v12,s13,v12\n"
"v_fma_f32 v12,v12,v11,s13\n"
AW "a186,v12\n"
AR "v12,a206\n"
"v_subrev_f32 v12,s13,v12\n"
"v_fma_f32 v12,v12,v11,s13\n"
AW "a206,v12\n"
AR "v12,a226\n"
"v_subrev_f32 v12,s13,v12\n"
"v_fma_f32 v12,v12,v11,s13\n"
AW "a226,v12\n"
AR "v12,a7\n"
"v_subrev_f32 v12,s13,v12\n"
"v_fma_f32 v12,v12,v11,s13\n"
AW "a7,v12\n"
AR "v12,a27\n"
"v_subrev_f32 v12,s13,v12\n"
"v_fma_f32 v12,v12,v11,s13\n"
AW "a27,v12\n"
AR "v12,a47\n"
"v_subrev_f32 v12,s13,v12\n"
"v_fma_f32 v12,v12,v11,s13\n"
AW "a47,v12\n"
AR "v12,a67\n"
"v_subrev_f32 v12,s13,v12\n"
"v_fma_f32 v12,v12,v11,s13\n"
AW "a67,v12\n"
AR "v12,a87\n"
"v_subrev_f32 v12,s13,v12\n"
"v_fma_f32 v12,v12,v11,s13\n"
AW "a87,v12\n"
AR "v12,a107\n"
"v_subrev_f32 v12,s13,v12\n"
"v_fma_f32 v12,v12,v11,s13\n"
AW "a107,v12\n"
AR "v12,a127\n"
"v_subrev_f32 v12,s13,v12\n"
"v_fma_f32 v12,v12,v11,s13\n"
AW "a127,v12\n"
AR "v12,a147\n"
"v_subrev_f32 v12,s13,v12\n"
"v_fma_f32 v12,v12,v11,s13\n"
AW "a147,v12\n"
AR "v12,a167\n"
"v_subrev_f32 v12,s13,v12\n"
"v_fma_f32 v12,v12,v11,s13\n"
AW "a167,v12\n"
AR "v12,a187\n"
"v_subrev_f32 v12,s13,v12\n"
"v_fma_f32 v12,v12,v11,s13\n"
AW "a187,v12\n"
AR "v12,a207\n"
"v_subrev_f32 v12,s13,v12\n"
"v_fma_f32 v12,v12,v11,s13\n"
AW "a207,v12\n"
AR "v12,a227\n"
"v_subrev_f32 v12,s13,v12\n"
"v_fma_f32 v12,v12,v11,s13\n"
AW "a227,v12\n"
"v_mul_f32 v58,v53,v11\n"
"v_sub_f32 v36,v36,v10\n"
"v_sub_f32 v37,v37,v10\n"
"v_sub_f32 v38,v38,v10\n"
"v_sub_f32 v39,v39,v10\n"
"v_sub_f32 v76,v76,v10\n"
"v_sub_f32 v77,v77,v10\n"
"v_sub_f32 v78,v78,v10\n"
"v_sub_f32 v79,v79,v10\n"
"v_sub_f32 v96,v96,v10\n"
"v_sub_f32 v97,v97,v10\n"
"v_sub_f32 v98,v98,v10\n"
"v_sub_f32 v99,v99,v10\n"
"v_sub_f32 v116,v116,v10\n"
"v_sub_f32 v117,v117,v10\n"
"v_sub_f32 v118,v118,v10\n"
"v_sub_f32 v119,v119,v10\n"
"v_exp_f32 v96,v96\n"
"v_exp_f32 v97,v97\n"
"v_exp_f32 v98,v98\n"
"v_exp_f32 v99,v99\n"
"v_exp_f32 v116,v116\n"
"v_exp_f32 v117,v117\n"
"v_exp_f32 v118,v118\n"
"v_exp_f32 v119,v119\n"
"v_add_f32 v58,v58,v96\n"
"v_add_f32 v58,v58,v97\n"
"v_add_f32 v58,v58,v98\n"
"v_add_f32 v58,v58,v99\n"
"v_add_f32 v58,v58,v116\n"
"v_add_f32 v58,v58,v117\n"
"v_add_f32 v58,v58,v118\n"
"v_add_f32_e64 v58,v58,v119\n"
CV "v96,v96,v97\n"
CV "v97,v98,v99\n"
CV "v98,v116,v117\n"
CV "v99,v118,v119\n"
"v_max3_f32 v10,v100,v101,v102\n"
"v_max3_f32 v11,v103,v120,v121\n"
"v_max3_f32 v10,v10,v122,v123\n"
"v_max_f32 v10,v10,v11\n"
"v_mov_b32 v11,v10\n"
"s_nop 1\n"
"v_permlane16_swap_b32 v10,v11\n"
"v_max_f32 v10,v10,v11\n"
"v_mov_b32 v11,v10\n"
"s_nop 1\n"
"v_permlane32_swap_b32 v10,v11\n"
"v_max_f32 v10,v10,v11\n"
"v_add_f32 v10,0x41300000,v10\n"
"v_max_f32 v10,0,v10\n"
"v_exp_f32 v11,-v10\n"
AR "v12,a8\n"
"v_subrev_f32 v12,s13,v12\n"
"v_fma_f32 v12,v12,v11,s13\n"
AW "a8,v12\n"
AR "v12,a28\n"
"v_subrev_f32 v12,s13,v12\n"
"v_fma_f32 v12,v12,v11,s13\n"
AW "a28,v12\n"
AR "v12,a48\n"
"v_subrev_f32 v12,s13,v12\n"
"v_fma_f32 v12,v12,v11,s13\n"
AW "a48,v12\n"
AR "v12,a68\n"
"v_subrev_f32 v12,s13,v12\n"
"v_fma_f32 v12,v12,v11,s13\n"
AW "a68,v12\n"
AR "v12,a88\n"
"v_subrev_f32 v12,s13,v12\n"
"v_fma_f32 v12,v12,v11,s13\n"
AW "a88,v12\n"
AR "v12,a108\n"
"v_subrev_f32 v12,s13,v12\n"
"v_fma_f32 v12,v12,v11,s13\n"
AW "a108,v12\n"
AR "v12,a128\n"
"v_subrev_f32 v12,s13,v12\n"
"v_fma_f32 v12,v12,v11,s13\n"
AW "a128,v12\n"
AR "v12,a148\n"
"v_subrev_f32 v12,s13,v12\n"
"v_fma_f32 v12,v12,v11,s13\n"
AW "a148,v12\n"
AR "v12,a168\n"
"v_subrev_f32 v12,s13,v12\n"
"v_fma_f32 v12,v12,v11,s13\n"
AW "a168,v12\n"
AR "v12,a188\n"
"v_subrev_f32 v12,s13,v12\n"
"v_fma_f32 v12,v12,v11,s13\n"
AW "a188,v12\n"
AR "v12,a208\n"
"v_subrev_f32 v12,s13,v12\n"
"v_fma_f32 v12,v12,v11,s13\n"
AW "a208,v12\n"
AR "v12,a228\n"
"v_subrev_f32 v12,s13,v12\n"
"v_fma_f32 v12,v12,v11,s13\n"
AW "a228,v12\n"
AR "v12,a9\n"
"v_subrev_f32 v12,s13,v12\n"
"v_fma_f32 v12,v12,v11,s13\n"
AW "a9,v12\n"
AR "v12,a29\n"
"v_subrev_f32 v12,s13,v12\n"
"v_fma_f32 v12,v12,v11,s13\n"
AW "a29,v12\n"
AR "v12,a49\n"
"v_subrev_f32 v12,s13,v12\n"
"v_fma_f32 v12,v12,v11,s13\n"
AW "a49,v12\n"
AR "v12,a69\n"
"v_subrev_f32 v12,s13,v12\n"
"v_fma_f32 v12,v12,v11,s13\n"
AW "a69,v12\n"
AR "v12,a89\n"
"v_subrev_f32 v12,s13,v12\n"
"v_fma_f32 v12,v12,v11,s13\n"
AW "a89,v12\n"
AR "v12,a109\n"
"v_subrev_f32 v12,s13,v12\n"
"v_fma_f32 v12,v12,v11,s13\n"
AW "a109,v12\n"
AR "v12,a129\n"
"v_subrev_f32 v12,s13,v12\n"
"v_fma_f32 v12,v12,v11,s13\n"
AW "a129,v12\n"
AR "v12,a149\n"
"v_subrev_f32 v12,s13,v12\n"
"v_fma_f32 v12,v12,v11,s13\n"
AW "a149,v12\n"
AR "v12,a169\n"
"v_subrev_f32 v12,s13,v12\n"
"v_fma_f32 v12,v12,v11,s13\n"
AW "a169,v12\n"
AR "v12,a189\n"
"v_subrev_f32 v12,s13,v12\n"
"v_fma_f32 v12,v12,v11,s13\n"
AW "a189,v12\n"
AR "v12,a209\n"
"v_subrev_f32 v12,s13,v12\n"
"v_fma_f32 v12,v12,v11,s13\n"
AW "a209,v12\n"
AR "v12,a229\n"
"v_subrev_f32 v12,s13,v12\n"
"v_fma_f32 v12,v12,v11,s13\n"
AW "a229,v12\n"
AR "v12,a10\n"
"v_subrev_f32 v12,s13,v12\n"
"v_fma_f32 v12,v12,v11,s13\n"
AW "a10,v12\n"
AR "v12,a30\n"
"v_subrev_f32 v12,s13,v12\n"
"v_fma_f32 v12,v12,v11,s13\n"
AW "a30,v12\n"
AR "v12,a50\n"
"v_subrev_f32 v12,s13,v12\n"
"v_fma_f32 v12,v12,v11,s13\n"
AW "a50,v12\n"
AR "v12,a70\n"
"v_subrev_f32 v12,s13,v12\n"
"v_fma_f32 v12,v12,v11,s13\n"
AW "a70,v12\n"
AR "v12,a90\n"
"v_subrev_f32 v12,s13,v12\n"
"v_fma_f32 v12,v12,v11,s13\n"
AW "a90,v12\n"
AR "v12,a110\n"
"v_subrev_f32 v12,s13,v12\n"
"v_fma_f32 v12,v12,v11,s13\n"
AW "a110,v12\n"
AR "v12,a130\n"
"v_subrev_f32 v12,s13,v12\n"
"v_fma_f32 v12,v12,v11,s13\n"
AW "a130,v12\n"
AR "v12,a150\n"
"v_subrev_f32 v12,s13,v12\n"
"v_fma_f32 v12,v12,v11,s13\n"
AW "a150,v12\n"
AR "v12,a170\n"
"v_subrev_f32 v12,s13,v12\n"
"v_fma_f32 v12,v12,v11,s13\n"
AW "a170,v12\n"
AR "v12,a190\n"
"v_subrev_f32 v12,s13,v12\n"
"v_fma_f32 v12,v12,v11,s13\n"
AW "a190,v12\n"
AR "v12,a210\n"
"v_subrev_f32 v12,s13,v12\n"
"v_fma_f32 v12,v12,v11,s13\n"
AW "a210,v12\n"
AR "v12,a230\n"
"v_subrev_f32 v12,s13,v12\n"
"v_fma_f32 v12,v12,v11,s13\n"
AW "a230,v12\n"
AR "v12,a11\n"
"v_subrev_f32 v12,s13,v12\n"
"v_fma_f32 v12,v12,v11,s13\n"
AW "a11,v12\n"
AR "v12,a31\n"
"v_subrev_f32 v12,s13,v12\n"
"v_fma_f32 v12,v12,v11,s13\n"
AW "a31,v12\n"
AR "v12,a51\n"
"v_subrev_f32 v12,s13,v12\n"
"v_fma_f32 v12,v12,v11,s13\n"
AW "a51,v12\n"
AR "v12,a71\n"
"v_subrev_f32 v12,s13,v12\n"
"v_fma_f32 v12,v12,v11,s13\n"
AW "a71,v12\n"
AR "v12,a91\n"
"v_subrev_f32 v12,s13,v12\n"
"v_fma_f32 v12,v12,v11,s13\n"
AW "a91,v12\n"
AR "v12,a111\n"
"v_subrev_f32 v12,s13,v12\n"
"v_fma_f32 v12,v12,v11,s13\n"
AW "a111,v12\n"
AR "v12,a131\n"
"v_subrev_f32 v12,s13,v12\n"
"v_fma_f32 v12,v12,v11,s13\n"
AW "a131,v12\n"
AR "v12,a151\n"
"v_subrev_f32 v12,s13,v12\n"
"v_fma_f32 v12,v12,v11,s13\n"
AW "a151,v12\n"
AR "v12,a171\n"
"v_subrev_f32 v12,s13,v12\n"
"v_fma_f32 v12,v12,v11,s13\n"
AW "a171,v12\n"
AR "v12,a191\n"
"v_subrev_f32 v12,s13,v12\n"
"v_fma_f32 v12,v12,v11,s13\n"
AW "a191,v12\n"
AR "v12,a211\n"
"v_subrev_f32 v12,s13,v12\n"
"v_fma_f32 v12,v12,v11,s13\n"
AW "a211,v12\n"
AR "v12,a231\n"
"v_subrev_f32 v12,s13,v12\n"
"v_fma_f32 v12,v12,v11,s13\n"
AW "a231,v12\n"
"v_mul_f32 v59,v54,v11\n"
"v_sub_f32 v40,v40,v10\n"
"v_sub_f32 v41,v41,v10\n"
"v_sub_f32 v42,v42,v10\n"
"v_sub_f32 v43,v43,v10\n"
"v_sub_f32 v80,v80,v10\n"
"v_sub_f32 v81,v81,v10\n"
"v_sub_f32 v82,v82,v10\n"
"v_sub_f32 v83,v83,v10\n"
"v_sub_f32 v100,v100,v10\n"
"v_sub_f32 v101,v101,v10\n"
"v_sub_f32 v102,v102,v10\n"
"v_sub_f32 v103,v103,v10\n"
"v_sub_f32 v120,v120,v10\n"
"v_sub_f32 v121,v121,v10\n"
"v_sub_f32 v122,v122,v10\n"
"v_sub_f32 v123,v123,v10\n"
"v_exp_f32 v100,v100\n"
"v_exp_f32 v101,v101\n"
"v_exp_f32 v102,v102\n"
"v_exp_f32 v103,v103\n"
"v_exp_f32 v120,v120\n"
"v_exp_f32 v121,v121\n"
"v_exp_f32 v122,v122\n"
"v_exp_f32 v123,v123\n"
"v_add_f32 v59,v59,v100\n"
"v_add_f32 v59,v59,v101\n"
"v_add_f32 v59,v59,v102\n"
"v_add_f32 v59,v59,v103\n"
"v_add_f32 v59,v59,v120\n"
"v_add_f32 v59,v59,v121\n"
"v_add_f32 v59,v59,v122\n"
"v_add_f32_e64 v59,v59,v123\n"
CV "v100,v100,v101\n"
CV "v101,v102,v103\n"
CV "v102,v120,v121\n"
CV "v103,v122,v123\n"
"v_max3_f32 v10,v104,v105,v106\n"
"v_max3_f32 v11,v107,v124,v125\n"
"v_max3_f32 v10,v10,v126,v127\n"
"v_max_f32 v10,v10,v11\n"
"v_mov_b32 v11,v10\n"
"s_nop 1\n"
"v_permlane16_swap_b32 v10,v11\n"
"v_max_f32 v10,v10,v11\n"
"v_mov_b32 v11,v10\n"
"s_nop 1\n"
"v_permlane32_swap_b32 v10,v11\n"
"v_max_f32_e64 v10,v10,v11\n"
"v_add_f32 v10,0x41300000,v10\n"
"v_max_f32 v10,0,v10\n"
"v_exp_f32 v11,-v10\n"
AR "v12,a12\n"
"v_subrev_f32 v12,s13,v12\n"
"v_fma_f32 v12,v12,v11,s13\n"
AW "a12,v12\n"
AR "v12,a32\n"
"v_subrev_f32 v12,s13,v12\n"
"v_fma_f32 v12,v12,v11,s13\n"
AW "a32,v12\n"
AR "v12,a52\n"
"v_subrev_f32 v12,s13,v12\n"
"v_fma_f32 v12,v12,v11,s13\n"
AW "a52,v12\n"
AR "v12,a72\n"
"v_subrev_f32 v12,s13,v12\n"
"v_fma_f32 v12,v12,v11,s13\n"
AW "a72,v12\n"
AR "v12,a92\n"
"v_subrev_f32 v12,s13,v12\n"
"v_fma_f32 v12,v12,v11,s13\n"
AW "a92,v12\n"
AR "v12,a112\n"
"v_subrev_f32 v12,s13,v12\n"
"v_fma_f32 v12,v12,v11,s13\n"
AW "a112,v12\n"
AR "v12,a132\n"
"v_subrev_f32 v12,s13,v12\n"
"v_fma_f32 v12,v12,v11,s13\n"
AW "a132,v12\n"
AR "v12,a152\n"
"v_subrev_f32 v12,s13,v12\n"
"v_fma_f32 v12,v12,v11,s13\n"
AW "a152,v12\n"
AR "v12,a172\n"
"v_subrev_f32 v12,s13,v12\n"
"v_fma_f32 v12,v12,v11,s13\n"
AW "a172,v12\n"
AR "v12,a192\n"
"v_subrev_f32 v12,s13,v12\n"
"v_fma_f32 v12,v12,v11,s13\n"
AW "a192,v12\n"
AR "v12,a212\n"
"v_subrev_f32 v12,s13,v12\n"
"v_fma_f32 v12,v12,v11,s13\n"
AW "a212,v12\n"
AR "v12,a232\n"
"v_subrev_f32 v12,s13,v12\n"
"v_fma_f32 v12,v12,v11,s13\n"
AW "a232,v12\n"
AR "v12,a13\n"
"v_subrev_f32 v12,s13,v12\n"
"v_fma_f32 v12,v12,v11,s13\n"
AW "a13,v12\n"
AR "v12,a33\n"
"v_subrev_f32 v12,s13,v12\n"
"v_fma_f32 v12,v12,v11,s13\n"
AW "a33,v12\n"
AR "v12,a53\n"
"v_subrev_f32 v12,s13,v12\n"
"v_fma_f32 v12,v12,v11,s13\n"
AW "a53,v12\n"
AR "v12,a73\n"
"v_subrev_f32 v12,s13,v12\n"
"v_fma_f32 v12,v12,v11,s13\n"
AW "a73,v12\n"
AR "v12,a93\n"
"v_subrev_f32 v12,s13,v12\n"
"v_fma_f32 v12,v12,v11,s13\n"
AW "a93,v12\n"
AR "v12,a113\n"
"v_subrev_f32 v12,s13,v12\n"
"v_fma_f32 v12,v12,v11,s13\n"
AW "a113,v12\n"
AR "v12,a133\n"
"v_subrev_f32 v12,s13,v12\n"
"v_fma_f32 v12,v12,v11,s13\n"
AW "a133,v12\n"
AR "v12,a153\n"
"v_subrev_f32 v12,s13,v12\n"
"v_fma_f32 v12,v12,v11,s13\n"
AW "a153,v12\n"
AR "v12,a173\n"
"v_subrev_f32 v12,s13,v12\n"
"v_fma_f32 v12,v12,v11,s13\n"
AW "a173,v12\n"
AR "v12,a193\n"
"v_subrev_f32 v12,s13,v12\n"
"v_fma_f32 v12,v12,v11,s13\n"
AW "a193,v12\n"
AR "v12,a213\n"
"v_subrev_f32 v12,s13,v12\n"
"v_fma_f32 v12,v12,v11,s13\n"
AW "a213,v12\n"
AR "v12,a233\n"
"v_subrev_f32 v12,s13,v12\n"
"v_fma_f32 v12,v12,v11,s13\n"
AW "a233,v12\n"
AR "v12,a14\n"
"v_subrev_f32 v12,s13,v12\n"
"v_fma_f32 v12,v12,v11,s13\n"
AW "a14,v12\n"
AR "v12,a34\n"
"v_subrev_f32 v12,s13,v12\n"
"v_fma_f32 v12,v12,v11,s13\n"
AW "a34,v12\n"
AR "v12,a54\n"
"v_subrev_f32 v12,s13,v12\n"
"v_fma_f32 v12,v12,v11,s13\n"
AW "a54,v12\n"
AR "v12,a74\n"
"v_subrev_f32 v12,s13,v12\n"
"v_fma_f32 v12,v12,v11,s13\n"
AW "a74,v12\n"
AR "v12,a94\n"
"v_subrev_f32 v12,s13,v12\n"
"v_fma_f32 v12,v12,v11,s13\n"
AW "a94,v12\n"
AR "v12,a114\n"
"v_subrev_f32 v12,s13,v12\n"
"v_fma_f32 v12,v12,v11,s13\n"
AW "a114,v12\n"
AR "v12,a134\n"
"v_subrev_f32 v12,s13,v12\n"
"v_fma_f32 v12,v12,v11,s13\n"
AW "a134,v12\n"
AR "v12,a154\n"
"v_subrev_f32 v12,s13,v12\n"
"v_fma_f32 v12,v12,v11,s13\n"
AW "a154,v12\n"
AR "v12,a174\n"
"v_subrev_f32 v12,s13,v12\n"
"v_fma_f32 v12,v12,v11,s13\n"
AW "a174,v12\n"
AR "v12,a194\n"
"v_subrev_f32 v12,s13,v12\n"
"v_fma_f32 v12,v12,v11,s13\n"
AW "a194,v12\n"
AR "v12,a214\n"
"v_subrev_f32 v12,s13,v12\n"
"v_fma_f32 v12,v12,v11,s13\n"
AW "a214,v12\n"
AR "v12,a234\n"
"v_subrev_f32 v12,s13,v12\n"
"v_fma_f32 v12,v12,v11,s13\n"
AW "a234,v12\n"
AR "v12,a15\n"
"v_subrev_f32 v12,s13,v12\n"
"v_fma_f32 v12,v12,v11,s13\n"
AW "a15,v12\n"
AR "v12,a35\n"
"v_subrev_f32 v12,s13,v12\n"
"v_fma_f32 v12,v12,v11,s13\n"
AW "a35,v12\n"
AR "v12,a55\n"
"v_subrev_f32 v12,s13,v12\n"
"v_fma_f32 v12,v12,v11,s13\n"
AW "a55,v12\n"
AR "v12,a75\n"
"v_subrev_f32 v12,s13,v12\n"
"v_fma_f32 v12,v12,v11,s13\n"
AW "a75,v12\n"
AR "v12,a95\n"
"v_subrev_f32 v12,s13,v12\n"
"v_fma_f32 v12,v12,v11,s13\n"
AW "a95,v12\n"
AR "v12,a115\n"
"v_subrev_f32 v12,s13,v12\n"
"v_fma_f32 v12,v12,v11,s13\n"
AW "a115,v12\n"
AR "v12,a135\n"
"v_subrev_f32 v12,s13,v12\n"
"v_fma_f32 v12,v12,v11,s13\n"
AW "a135,v12\n"
AR "v12,a155\n"
"v_subrev_f32 v12,s13,v12\n"
"v_fma_f32 v12,v12,v11,s13\n"
AW "a155,v12\n"
AR "v12,a175\n"
"v_subrev_f32 v12,s13,v12\n"
"v_fma_f32 v12,v12,v11,s13\n"
AW "a175,v12\n"
AR "v12,a195\n"
"v_subrev_f32 v12,s13,v12\n"
"v_fma_f32 v12,v12,v11,s13\n"
AW "a195,v12\n"
AR "v12,a215\n"
"v_subrev_f32 v12,s13,v12\n"
"v_fma_f32 v12,v12,v11,s13\n"
AW "a215,v12\n"
AR "v12,a235\n"
"v_subrev_f32 v12,s13,v12\n"
"v_fma_f32 v12,v12,v11,s13\n"
AW "a235,v12\n"
"v_mul_f32 v60,v55,v11\n"
"v_sub_f32 v44,v44,v10\n"
"v_sub_f32 v45,v45,v10\n"
"v_sub_f32 v46,v46,v10\n"
"v_sub_f32 v47,v47,v10\n"
"v_sub_f32 v84,v84,v10\n"
"v_sub_f32 v85,v85,v10\n"
"v_sub_f32 v86,v86,v10\n"
"v_sub_f32 v87,v87,v10\n"
"v_sub_f32 v104,v104,v10\n"
"v_sub_f32 v105,v105,v10\n"
"v_sub_f32 v106,v106,v10\n"
"v_sub_f32 v107,v107,v10\n"
"v_sub_f32 v124,v124,v10\n"
"v_sub_f32 v125,v125,v10\n"
"v_sub_f32 v126,v126,v10\n"
"v_sub_f32 v127,v127,v10\n"
"v_exp_f32 v104,v104\n"
"v_exp_f32 v105,v105\n"
"v_exp_f32 v106,v106\n"
"v_exp_f32 v107,v107\n"
"v_exp_f32 v124,v124\n"
"v_exp_f32 v125,v125\n"
"v_exp_f32 v126,v126\n"
"v_exp_f32 v127,v127\n"
"v_add_f32 v60,v60,v104\n"
"v_add_f32 v60,v60,v105\n"
"v_add_f32 v60,v60,v106\n"
"v_add_f32 v60,v60,v107\n"
"v_add_f32 v60,v60,v124\n"
"v_add_f32 v60,v60,v125\n"
"v_add_f32 v60,v60,v126\n"
"v_add_f32 v60,v60,v127\n"
CV "v104,v104,v105\n"
CV "v105,v106,v107\n"
CV "v106,v124,v125\n"
CV "v107,v126,v127\n"
"v_max3_f32 v10,v108,v109,v110\n"
"v_max3_f32 v11,v111,v128,v129\n"
"v_max3_f32 v10,v10,v130,v131\n"
"v_max_f32 v10,v10,v11\n"
"v_mov_b32 v11,v10\n"
"s_nop 1\n"
"v_permlane16_swap_b32 v10,v11\n"
"v_max_f32 v10,v10,v11\n"
"v_mov_b32 v11,v10\n"
"s_nop 1\n"
"v_permlane32_swap_b32 v10,v11\n"
"v_max_f32 v10,v10,v11\n"
"v_add_f32 v10,0x41300000,v10\n"
"v_max_f32 v10,0,v10\n"
"v_exp_f32 v11,-v10\n"
AR "v12,a16\n"
"v_subrev_f32 v12,s13,v12\n"
"v_fma_f32 v12,v12,v11,s13\n"
AW "a16,v12\n"
AR "v12,a36\n"
"v_subrev_f32 v12,s13,v12\n"
"v_fma_f32 v12,v12,v11,s13\n"
AW "a36,v12\n"
AR "v12,a56\n"
"v_subrev_f32 v12,s13,v12\n"
"v_fma_f32 v12,v12,v11,s13\n"
AW "a56,v12\n"
AR "v12,a76\n"
"v_subrev_f32 v12,s13,v12\n"
"v_fma_f32 v12,v12,v11,s13\n"
AW "a76,v12\n"
AR "v12,a96\n"
"v_subrev_f32 v12,s13,v12\n"
"v_fma_f32 v12,v12,v11,s13\n"
AW "a96,v12\n"
AR "v12,a116\n"
"v_subrev_f32 v12,s13,v12\n"
"v_fma_f32 v12,v12,v11,s13\n"
AW "a116,v12\n"
AR "v12,a136\n"
"v_subrev_f32 v12,s13,v12\n"
"v_fma_f32 v12,v12,v11,s13\n"
AW "a136,v12\n"
AR "v12,a156\n"
"v_subrev_f32 v12,s13,v12\n"
"v_fma_f32 v12,v12,v11,s13\n"
AW "a156,v12\n"
AR "v12,a176\n"
"v_subrev_f32 v12,s13,v12\n"
"v_fma_f32 v12,v12,v11,s13\n"
AW "a176,v12\n"
AR "v12,a196\n"
"v_subrev_f32 v12,s13,v12\n"
"v_fma_f32 v12,v12,v11,s13\n"
AW "a196,v12\n"
AR "v12,a216\n"
"v_subrev_f32 v12,s13,v12\n"
"v_fma_f32 v12,v12,v11,s13\n"
AW "a216,v12\n"
AR "v12,a236\n"
"v_subrev_f32 v12,s13,v12\n"
"v_fma_f32 v12,v12,v11,s13\n"
AW "a236,v12\n"
AR "v12,a17\n"
"v_subrev_f32 v12,s13,v12\n"
"v_fma_f32 v12,v12,v11,s13\n"
AW "a17,v12\n"
AR "v12,a37\n"
"v_subrev_f32 v12,s13,v12\n"
"v_fma_f32 v12,v12,v11,s13\n"
AW "a37,v12\n"
AR "v12,a57\n"
"v_subrev_f32 v12,s13,v12\n"
"v_fma_f32 v12,v12,v11,s13\n"
AW "a57,v12\n"
AR "v12,a77\n"
"v_subrev_f32 v12,s13,v12\n"
"v_fma_f32 v12,v12,v11,s13\n"
AW "a77,v12\n"
AR "v12,a97\n"
"v_subrev_f32 v12,s13,v12\n"
"v_fma_f32 v12,v12,v11,s13\n"
AW "a97,v12\n"
AR "v12,a117\n"
"v_subrev_f32 v12,s13,v12\n"
"v_fma_f32 v12,v12,v11,s13\n"
AW "a117,v12\n"
AR "v12,a137\n"
"v_subrev_f32 v12,s13,v12\n"
"v_fma_f32 v12,v12,v11,s13\n"
AW "a137,v12\n"
AR "v12,a157\n"
"v_subrev_f32 v12,s13,v12\n"
"v_fma_f32 v12,v12,v11,s13\n"
AW "a157,v12\n"
AR "v12,a177\n"
"v_subrev_f32 v12,s13,v12\n"
"v_fma_f32 v12,v12,v11,s13\n"
AW "a177,v12\n"
AR "v12,a197\n"
"v_subrev_f32 v12,s13,v12\n"
"v_fma_f32 v12,v12,v11,s13\n"
AW "a197,v12\n"
AR "v12,a217\n"
"v_subrev_f32 v12,s13,v12\n"
"v_fma_f32 v12,v12,v11,s13\n"
AW "a217,v12\n"
AR "v12,a237\n"
"v_subrev_f32 v12,s13,v12\n"
"v_fma_f32 v12,v12,v11,s13\n"
AW "a237,v12\n"
AR "v12,a18\n"
"v_subrev_f32 v12,s13,v12\n"
"v_fma_f32 v12,v12,v11,s13\n"
AW "a18,v12\n"
AR "v12,a38\n"
"v_subrev_f32 v12,s13,v12\n"
"v_fma_f32 v12,v12,v11,s13\n"
AW "a38,v12\n"
AR "v12,a58\n"
"v_subrev_f32 v12,s13,v12\n"
"v_fma_f32 v12,v12,v11,s13\n"
AW "a58,v12\n"
AR "v12,a78\n"
"v_subrev_f32 v12,s13,v12\n"
"v_fma_f32 v12,v12,v11,s13\n"
AW "a78,v12\n"
AR "v12,a98\n"
"v_subrev_f32 v12,s13,v12\n"
"v_fma_f32 v12,v12,v11,s13\n"
AW "a98,v12\n"
AR "v12,a118\n"
"v_subrev_f32 v12,s13,v12\n"
"v_fma_f32 v12,v12,v11,s13\n"
AW "a118,v12\n"
AR "v12,a138\n"
"v_subrev_f32 v12,s13,v12\n"
"v_fma_f32 v12,v12,v11,s13\n"
AW "a138,v12\n"
AR "v12,a158\n"
"v_subrev_f32 v12,s13,v12\n"
"v_fma_f32 v12,v12,v11,s13\n"
AW "a158,v12\n"
AR "v12,a178\n"
"v_subrev_f32 v12,s13,v12\n"
"v_fma_f32 v12,v12,v11,s13\n"
AW "a178,v12\n"
AR "v12,a198\n"
"v_subrev_f32 v12,s13,v12\n"
"v_fma_f32 v12,v12,v11,s13\n"
AW "a198,v12\n"
AR "v12,a218\n"
"v_subrev_f32 v12,s13,v12\n"
"v_fma_f32 v12,v12,v11,s13\n"
AW "a218,v12\n"
AR "v12,a238\n"
"v_subrev_f32 v12,s13,v12\n"
"v_fma_f32 v12,v12,v11,s13\n"
AW "a238,v12\n"
AR "v12,a19\n"
"v_subrev_f32 v12,s13,v12\n"
"v_fma_f32 v12,v12,v11,s13\n"
AW "a19,v12\n"
AR "v12,a39\n"
"v_subrev_f32 v12,s13,v12\n"
"v_fma_f32 v12,v12,v11,s13\n"
AW "a39,v12\n"
AR "v12,a59\n"
"v_subrev_f32 v12,s13,v12\n"
"v_fma_f32 v12,v12,v11,s13\n"
AW "a59,v12\n"
AR "v12,a79\n"
"v_subrev_f32 v12,s13,v12\n"
"v_fma_f32 v12,v12,v11,s13\n"
AW "a79,v12\n"
AR "v12,a99\n"
"v_subrev_f32 v12,s13,v12\n"
"v_fma_f32 v12,v12,v11,s13\n"
AW "a99,v12\n"
AR "v12,a119\n"
"v_subrev_f32 v12,s13,v12\n"
"v_fma_f32 v12,v12,v11,s13\n"
AW "a119,v12\n"
AR "v12,a139\n"
"v_subrev_f32 v12,s13,v12\n"
"v_fma_f32 v12,v12,v11,s13\n"
AW "a139,v12\n"
AR "v12,a159\n"
"v_subrev_f32 v12,s13,v12\n"
"v_fma_f32 v12,v12,v11,s13\n"
AW "a159,v12\n"
AR "v12,a179\n"
"v_subrev_f32 v12,s13,v12\n"
"v_fma_f32 v12,v12,v11,s13\n"
AW "a179,v12\n"
AR "v12,a199\n"
"v_subrev_f32 v12,s13,v12\n"
"v_fma_f32 v12,v12,v11,s13\n"
AW "a199,v12\n"
AR "v12,a219\n"
"v_subrev_f32 v12,s13,v12\n"
"v_fma_f32 v12,v12,v11,s13\n"
AW "a219,v12\n"
AR "v12,a239\n"
"v_subrev_f32 v12,s13,v12\n"
"v_fma_f32 v12,v12,v11,s13\n"
AW "a239,v12\n"
"v_mul_f32 v61,v56,v11\n"
"v_sub_f32 v48,v48,v10\n"
"v_sub_f32 v49,v49,v10\n"
"v_sub_f32 v50,v50,v10\n"
"v_sub_f32 v51,v51,v10\n"
"v_sub_f32 v88,v88,v10\n"
"v_sub_f32 v89,v89,v10\n"
"v_sub_f32 v90,v90,v10\n"
"v_sub_f32 v91,v91,v10\n"
"v_sub_f32 v108,v108,v10\n"
"v_sub_f32 v109,v109,v10\n"
"v_sub_f32 v110,v110,v10\n"
"v_sub_f32 v111,v111,v10\n"
"v_sub_f32 v128,v128,v10\n"
"v_sub_f32 v129,v129,v10\n"
"v_sub_f32 v130,v130,v10\n"
"v_sub_f32 v131,v131,v10\n"
"v_exp_f32 v108,v108\n"
"v_exp_f32 v109,v109\n"
"v_exp_f32 v110,v110\n"
"v_exp_f32 v111,v111\n"
"v_exp_f32 v128,v128\n"
"v_exp_f32 v129,v129\n"
"v_exp_f32 v130,v130\n"
"v_exp_f32 v131,v131\n"
"v_add_f32 v61,v61,v108\n"
"v_add_f32 v61,v61,v109\n"
"v_add_f32 v61,v61,v110\n"
"v_add_f32 v61,v61,v111\n"
"v_add_f32 v61,v61,v128\n"
"v_add_f32 v61,v61,v129\n"
"v_add_f32 v61,v61,v130\n"
"v_add_f32_e64 v61,v61,v131\n"
CV "v108,v108,v109\n"
CV "v109,v110,v111\n"
CV "v110,v128,v129\n"
CV "v111,v130,v131\n"
"s_nop 7\n"
"s_setpc_b64 s[52:53]\n"
".Ldone:\n"
    : "+{v0}"(tid), "+{s23}"(bid), "+{s[20:21]}"(ka)
    :
    : "v1", "v2", "v3", "v4", "v5", "v6", "v7", "v8", "v9", "v10", "v11", "v12", "v13", "v14", "v15", "v16", "v17", "v18", "v19", "v20", "v21", "v22", "v23", "v24", "v25", "v26", "v27", "v28", "v29", "v30", "v31", "v32", "v33", "v34", "v35", "v36", "v37", "v38", "v39", "v40", "v41", "v42", "v43", "v44", "v45", "v46", "v47", "v48", "v49", "v50", "v51", "v52", "v53", "v54", "v55", "v56", "v57", "v58", "v59", "v60", "v61", "v62", "v63", "v64", "v65", "v66", "v67", "v68", "v69", "v70", "v71", "v72", "v73", "v74", "v75", "v76", "v77", "v78", "v79", "v80", "v81", "v82", "v83", "v84", "v85", "v86", "v87", "v88", "v89", "v90", "v91", "v92", "v93", "v94", "v95", "v96", "v97", "v98", "v99", "v100", "v101", "v102", "v103", "v104", "v105", "v106", "v107", "v108", "v109", "v110", "v111", "v112", "v113", "v114", "v115", "v116", "v117", "v118", "v119", "v120", "v121", "v122", "v123", "v124", "v125", "v126", "v127", "v128", "v129", "v130", "v131", "v132", "v133", "v134", "v135", "v136", "v137", "v138", "v139", "v140", "v141", "v142", "v143", "v144", "v145", "v146", "v147", "v148", "v149", "v150", "v151", "v152", "v153", "v154", "v155", "v156", "v157", "v158", "v159", "v160", "v161", "v162", "v163", "v164", "v165", "v166", "v167", "v168", "v169", "v170", "v171", "v172", "v173", "v174", "v175", "v176", "v177", "v178", "v179", "v180", "v181", "v182", "v183", "v184", "v185", "v186", "v187", "v188", "v189", "v190", "v191", "v192", "v193", "v194", "v195", "v196", "v197", "v198", "v199", "v200", "v201", "v202", "v203", "v204", "v205", "v206", "v207", "v208", "v209", "v210", "v211", "v212", "v213", "v214", "v215", "v216", "v217", "v218", "v219", "v220", "v221", "v222", "v223", "v224", "v225", "v226", "v227", "v228", "v229", "v230", "v231", "v232", "v233", "v234", "v235", "v236", "v237", "v238", "v239", "v240", "v241", "v242", "v243", "v244", "v245", "v246", "v247", "v248", "v249", "v250", "v251", "v252", "v253", "v254", "v255", "a0", "a1", "a2", "a3", "a4", "a5", "a6", "a7", "a8", "a9", "a10", "a11", "a12", "a13", "a14", "a15", "a16", "a17", "a18", "a19", "a20", "a21", "a22", "a23", "a24", "a25", "a26", "a27", "a28", "a29", "a30", "a31", "a32", "a33", "a34", "a35", "a36", "a37", "a38", "a39", "a40", "a41", "a42", "a43", "a44", "a45", "a46", "a47", "a48", "a49", "a50", "a51", "a52", "a53", "a54", "a55", "a56", "a57", "a58", "a59", "a60", "a61", "a62", "a63", "a64", "a65", "a66", "a67", "a68", "a69", "a70", "a71", "a72", "a73", "a74", "a75", "a76", "a77", "a78", "a79", "a80", "a81", "a82", "a83", "a84", "a85", "a86", "a87", "a88", "a89", "a90", "a91", "a92", "a93", "a94", "a95", "a96", "a97", "a98", "a99", "a100", "a101", "a102", "a103", "a104", "a105", "a106", "a107", "a108", "a109", "a110", "a111", "a112", "a113", "a114", "a115", "a116", "a117", "a118", "a119", "a120", "a121", "a122", "a123", "a124", "a125", "a126", "a127", "a128", "a129", "a130", "a131", "a132", "a133", "a134", "a135", "a136", "a137", "a138", "a139", "a140", "a141", "a142", "a143", "a144", "a145", "a146", "a147", "a148", "a149", "a150", "a151", "a152", "a153", "a154", "a155", "a156", "a157", "a158", "a159", "a160", "a161", "a162", "a163", "a164", "a165", "a166", "a167", "a168", "a169", "a170", "a171", "a172", "a173", "a174", "a175", "a176", "a177", "a178", "a179", "a180", "a181", "a182", "a183", "a184", "a185", "a186", "a187", "a188", "a189", "a190", "a191", "a192", "a193", "a194", "a195", "a196", "a197", "a198", "a199", "a200", "a201", "a202", "a203", "a204", "a205", "a206", "a207", "a208", "a209", "a210", "a211", "a212", "a213", "a214", "a215", "a216", "a217", "a218", "a219", "a220", "a221", "a222", "a223", "a224", "a225", "a226", "a227", "a228", "a229", "a230", "a231", "a232", "a233", "a234", "a235", "a236", "a237", "a238", "a239", "a240", "a241", "a242", "a243", "a244", "a245", "a246", "a247", "a248", "a249", "a250", "a251", "a252", "a253", "a254", "a255", "s0", "s1", "s2", "s3", "s4", "s5", "s6", "s7", "s8", "s9", "s10", "s11", "s12", "s13", "s14", "s15", "s16", "s17", "s18", "s19", "s22", "s24", "s25", "s26", "s27", "s28", "s29", "s30", "s31", "s32", "s33", "s34", "s35", "s36", "s37", "s38", "s39", "s40", "s41", "s42", "s43", "s44", "s45", "s46", "s47", "s48", "s49", "s50", "s51", "s52", "s53", "s54", "s55", "s56", "s57", "s58", "s59", "s60", "s61", "s62", "s63", "s64", "s65", "s66", "s67", "s68", "s69", "s70", "s71", "s72", "s73", "s74", "s75", "s76", "s77", "s78", "s79", "s80", "s81", "s82", "s83", "s84", "s85", "s86", "s87", "s88", "s89", "s90", "s91", "s92", "s93", "s94", "s95", "s96", "s97", "s98", "s99", "s100", "s101", "vcc", "scc", "memory");
}

extern "C" void kernel_launch(void* const* d_in, const int* in_sizes, int n_in, void* d_out, int out_size, void* d_ws, size_t ws_size, hipStream_t stream) {
  Ptrs P;
  for (int br = 0; br < 2; ++br) { P.x[br] = d_in[5 * br]; P.k[br] = d_in[5 * br + 1]; P.v[br] = d_in[5 * br + 2]; P.wq[br] = d_in[5 * br + 3]; P.wo[br] = d_in[5 * br + 4]; }
  (void)hipFuncSetAttribute((const void*)fused_kernel, hipFuncAttributeMaxDynamicSharedMemorySize, LDS1);
  fused_kernel<<<256, 256, LDS1, stream>>>(P, (char*)d_ws, (char*)d_out);
}
```

```cpp
#include <hip/hip_runtime.h>
#include <hip/hip_bf16.h>

#define MF "v_mfma_f32_16x16x32_bf16 "
#define TR "ds_read_b64_tr_b16 "
#define AR "v_accvgpr_read_b32 "
#define AW "v_accvgpr_write_b32 "
#define CV "v_cvt_pk_bf16_f32 "
#define LD "buffer_load_dwordx4 "
constexpr unsigned LDS1 = 160 * 1024;

struct Ptrs { const void* x[2]; const void* k[2]; const void* v[2]; const void* wq[2]; const void* wo[2]; };

__global__ __launch_bounds__(256, 1) __attribute__((aligned(4096))) void fused_kernel(Ptrs P, char* __restrict__ ws, char* __restrict__ out) {
  unsigned tid = threadIdx.x, bid = blockIdx.x;
  unsigned long long ka = (unsigned long long)__builtin_amdgcn_kernarg_segment_ptr();
  asm volatile(
".p2align 5\n"
".Lcode0:\n"
"s_and_b32 s58,s23,4\n"
"s_lshl_b32 s58,s58,1\n"
"s_load_dwordx2 s[28:29],s[20:21],s58 offset:48\n"
"s_load_dwordx2 s[24:25],s[20:21],s58\n"
"s_load_dwordx2 s[32:33],s[20:21],s58 offset:16\n"
"s_load_dwordx2 s[36:37],s[20:21],s58 offset:32\n"
"s_load_dwordx2 s[48:49],s[20:21],s58 offset:64\n"
"s_memtime s[100:101]\n"
"s_load_dwordx2 s[40:41],s[20:21],0x50\n"
"s_load_dwordx2 s[44:45],s[20:21],0x58\n"
"v_readfirstlane_b32 s22,v0\n"
"v_and_b32 v9,63,v0\n"
"v_bfe_u32 v10,v0,3,3\n"
"v_and_b32 v11,7,v0\n"
"s_lshr_b32 s22,s22,6\n"
"s_or_b32 s22,s22,s58\n"
"s_lshr_b32 s59,s23,4\n"
"s_and_b32 s58,s59,8\n"
"s_mul_i32 s59,s59,4112\n"
"s_or_b32 s22,s22,s59\n"
"s_and_b32 s59,s23,3\n"
"s_lshl_b32 s59,s59,1\n"
"s_bfe_u32 s60,s23,0x10003\n"
"s_or_b32 s59,s59,s60\n"
"s_lshl_b32 s60,s59,20\n"
"s_or_b32 s22,s22,s60\n"
"s_or_b32 s59,s59,s58\n"
"s_mul_i32 s59,s59,65792\n"
"s_xor_b32 s59,s59,524288\n"
"s_or_b32 s22,s22,s59\n"
"v_lshrrev_b32 v6,2,v0\n"
"v_and_b32 v6,4,v6\n"
"v_xor_b32 v6,v6,v11\n"
"v_lshlrev_b32_e64 v6,4,v6\n"
"s_mov_b32 s58,12288\n"
"v_mad_u32_u24 v6,v10,s58,v6\n"
"v_bfe_u32 v4,v0,4,3\n"
"v_xor_b32 v4,v4,v11\n"
"v_lshlrev_b32 v4,4,v4\n"
"s_mov_b32 s58,1280\n"
"v_mad_u32_u24 v4,v10,s58,v4\n"
"v_lshlrev_b32_e64 v9,4,v9\n"
"s_mov_b32 s26,3276800\n"
"s_mov_b32 s27,0x00020000\n"
"s_mov_b32 s30,7864320\n"
"s_mov_b32 s31,0x00020000\n"
"s_mov_b32 s34,0\n"
"s_mov_b32 s35,0x00020000\n"
"s_mov_b32 s38,0\n"
"s_mov_b32 s39,0x00020000\n"
"s_mov_b32 s42,268435456\n"
"s_mov_b32 s43,0x00020000\n"
"s_mov_b32 s46,6553600\n"
"s_mov_b32 s47,0x00020000\n"
"s_mov_b32 s50,7864320\n"
"s_mov_b32 s51,0x00020000\n"
"s_mov_b32 s98,20000\n"
"s_bfe_u32 s56,s22,0x20000\n"
"s_bfe_u32 s72,s22,0x10003\n"
"s_lshr_b32 s73,s56,1\n"
"s_and_b32 s74,s56,1\n"
"s_mul_i32 s62,s56,4096\n"
"s_mul_i32 s63,s56,5120\n"
"s_add_u32 s63,s63,98304\n"
"s_bfe_u32 s58,s22,0x40004\n"
"s_bfe_u32 s59,s22,0x40008\n"
"s_mul_i32 s68,s58,204800\n"
"s_mul_i32 s60,s56,51200\n"
"s_add_u32 s68,s68,s60\n"
"s_lshl_b32 s59,s59,1\n"
"s_add_u32 s59,s59,s73\n"
"s_mul_i32 s66,s59,384\n"
"s_mul_i32 s60,s74,393216\n"
"s_add_u32 s66,s66,s60\n"
"s_lshr_b32 s58,s22,6\n"
"s_xor_b32 s58,s58,s22\n"
"s_bfe_u32 s58,s58,0x10004\n"
"s_mul_i32 s81,s58,640\n"
"s_mul_i32 s60,s81,6144\n"
"s_add_u32 s66,s66,s60\n"
"v_xor_b32 v19,64,v4\n"
"v_add_u32 v16,97280,v6\n"
"v_add_u32 v17,194560,v6\n"
"v_add_u32 v18,291840,v6\n"
"s_waitcnt lgkmcnt(0)\n"
"s_mov_b32 s80,s100\n"
"s_mov_b32 s87,2\n"
"s_add_u32 m0,s62,0\n"
"s_add_u32 s82,s66,0\n"
"s_sub_u32 s15,s82,7864320\n"
"s_min_u32 s82,s82,s15\n"
LD "v6,s[28:31],s82 offen lds\n"
LD "v16,s[28:31],s82 offen offset:1024 lds\n"
LD "v17,s[28:31],s82 offen offset:2048 lds\n"
LD "v18,s[28:31],s82 offen offset:3072 lds\n"
"s_add_u32 s14,s81,0\n"
"s_sub_u32 s15,s14,1280\n"
"s_min_u32 s14,s14,s15\n"
"s_add_u32 s14,s14,s68\n"
"s_add_u32 s84,s14,0\n"
"s_add_u32 m0,s63,0\n"
"s_nop 0\n"
LD "v4,s[24:27],s84 offen lds\n"
"s_add_u32 s84,s14,9216\n"
LD "v19,s[24:27],s84 offen offset:1024 lds\n"
"s_add_u32 s84,s14,18432\n"
LD "v4,s[24:27],s84 offen offset:2048 lds\n"
"s_add_u32 s84,s14,27648\n"
LD "v19,s[24:27],s84 offen offset:3072 lds\n"
"s_add_u32 s84,s14,40960\n"
"s_add_u32 m0,s63,4096\n"
"s_nop 0\n"
LD "v4,s[24:27],s84 offen lds\n"
"s_lshr_b32 s58,s23,3\n"
"s_mul_i32 s59,s58,7\n"
"s_and_b32 s60,s23,7\n"
"s_mul_i32 s60,s60,3\n"
"s_add_u32 s59,s59,s60\n"
"s_and_b32 s59,s59,15\n"
"s_lshl_b32 s59,s59,8\n"
"s_or_b32 s58,s58,s59\n"
"s_and_b32 s59,s23,3\n"
"s_lshl_b32 s59,s59,5\n"
"s_or_b32 s58,s58,s59\n"
"s_bfe_u32 s59,s23,0x10000\n"
"s_lshl_b32 s59,s59,12\n"
"s_or_b32 s58,s58,s59\n"
"s_lshl_b32 s23,s23,16\n"
"s_or_b32 s23,s23,s58\n"
"v_bfe_u32 v20,v0,4,2\n"
"v_mul_u32_u24 v21,384,v10\n"
"v_xor_b32 v22,v11,v20\n"
"s_add_u32 m0,s62,16384\n"
"s_add_u32 s82,s66,128\n"
"s_sub_u32 s15,s82,7864320\n"
"s_min_u32 s82,s82,s15\n"
LD "v6,s[28:31],s82 offen lds\n"
"v_lshl_add_u32 v7,v22,4,v21\n"
"s_lshl_b32 s58,s74,6\n"
"v_xor_b32 v7,s58,v7\n"
"v_lshlrev_b32 v22,1,v20\n"
"v_xor_b32 v22,v11,v22\n"
"v_lshl_add_u32 v8,v22,4,v21\n"
"v_bfe_u32 v21,v0,1,3\n"
"v_xor_b32 v21,v21,v20\n"
"v_lshlrev_b32 v21,4,v21\n"
"v_lshl_add_u32 v21,v11,7,v21\n"
"v_bfe_u32 v22,v0,3,1\n"
"s_mul_i32 s58,s73,12288\n"
"v_mul_u32_u24 v23,3072,v22\n"
"v_add3_u32 v1,v23,v21,s58\n"
"s_mul_i32 s59,s74,10240\n"
"s_add_u32 s59,s59,98304\n"
"v_lshl_add_u32 v2,v22,10,v21\n"
"v_add_u32_e64 v2,s59,v2\n"
"v_bfe_u32 v21,v0,2,3\n"
LD "v16,s[28:31],s82 offen offset:1024 lds\n"
"v_lshlrev_b32_e64 v21,7,v21\n"
"v_bfe_u32 v23,v0,3,2\n"
"v_lshl_add_u32 v21,v23,5,v21\n"
"v_and_b32_e64 v23,3,v0\n"
"v_lshl_add_u32 v21,v23,3,v21\n"
"v_bfe_u32 v23,v0,5,1\n"
"v_mul_u32_u24 v3,3072,v23\n"
"v_add3_u32 v3,v3,v21,s58\n"
"s_bfe_u32 s58,s22,0x40004\n"
"s_mul_i32 s58,s58,409600\n"
"s_mul_i32 s59,s72,1280\n"
"s_add_u32 s58,s58,s59\n"
"v_and_b32 v21,15,v0\n"
"v_mul_u32_u24 v21,2560,v21\n"
"v_lshl_add_u32 v21,v23,4,v21\n"
"v_bfe_u32 v23,v0,4,1\n"
"v_lshl_add_u32 v21,v23,5,v21\n"
"v_add_u32_e64 v12,s58,v21\n"
"v_bfe_u32 v21,v0,2,2\n"
LD "v17,s[28:31],s82 offen offset:2048 lds\n"
"v_lshlrev_b32 v21,7,v21\n"
"v_lshl_add_u32 v21,v20,10,v21\n"
"v_lshl_add_u32 v21,v22,6,v21\n"
"v_and_b32 v23,3,v0\n"
"v_lshl_add_u32 v21,v23,4,v21\n"
"s_lshl_b32 s58,s73,13\n"
"v_add_u32 v0,s58,v21\n"
"s_getpc_b64 s[52:53]\n"
".Lcts:\n"
"s_sub_u32 s52,s52,.Lcts-.Lcode0\n"
"s_subb_u32 s53,s53,0\n"
"s_bfe_u32 s58,s23,0x50000\n"
"s_lshl_b32 s58,s58,2\n"
"s_add_u32 s58,s58,s56\n"
"s_mul_i32 s58,s58,1280\n"
"v_min_u32 v15,144,v9\n"
"v_lshl_add_u32 v15,v15,3,s58\n"
"v_min_u32 v15,.Lcode1-.Lcode0,v15\n"
LD "v18,s[28:31],s82 offen offset:3072 lds\n"
"s_bfe_u32 s58,s22,0x4000c\n"
"s_bfe_u32 s59,s22,0x40010\n"
"s_mul_i32 s69,s58,204800\n"
"s_mul_i32 s60,s56,51200\n"
"s_add_u32 s69,s69,s60\n"
"s_lshl_b32 s59,s59,1\n"
"s_add_u32 s59,s59,s73\n"
"s_mul_i32 s67,s59,384\n"
"s_mul_i32 s60,s74,393216\n"
"s_add_u32 s67,s67,s60\n"
"s_mul_i32 s60,s81,6144\n"
"s_add_u32 s67,s67,s60\n"
"s_bfe_u32 s58,s22,0x40004\n"
"s_bfe_u32 s59,s22,0x40008\n"
"s_lshl_b32 s59,s59,1\n"
"s_add_u32 s88,s59,s74\n"
"s_mul_i32 s88,s88,245760\n"
"s_add_u32 s59,s59,s73\n"
"s_lshl_b32 s70,s58,5\n"
"s_add_u32 m0,s62,32768\n"
"s_add_u32 s82,s66,256\n"
"s_sub_u32 s15,s82,7864320\n"
"s_min_u32 s82,s82,s15\n"
LD "v6,s[28:31],s82 offen lds\n"
"s_add_u32 s70,s70,s59\n"
"s_mul_i32 s70,s70,196608\n"
"s_bfe_u32 s58,s22,0x4000c\n"
"s_bfe_u32 s59,s22,0x40010\n"
"s_lshl_b32 s59,s59,1\n"
"s_add_u32 s89,s59,s74\n"
"s_mul_i32 s89,s89,245760\n"
"s_add_u32 s59,s59,s73\n"
"s_lshl_b32 s71,s58,5\n"
"s_add_u32 s71,s71,s59\n"
"s_mul_i32 s71,s71,196608\n"
"s_bitcmp1_b32 s23,12\n"
"s_cselect_b32 s58,s71,s70\n"
"s_cselect_b32 s71,s70,s71\n"
"s_mov_b32 s70,s58\n"
"s_cselect_b32 s58,s89,s88\n"
"s_cselect_b32 s89,s88,s89\n"
"s_mov_b32 s88,s58\n"
"s_cselect_b32 s69,98,1\n"
LD "v16,s[28:31],s82 offen offset:1024 lds\n"
"s_mov_b32 s54,1037384597\n"
"s_mov_b32 s55,1037384597\n"
"s_bfe_u32 s58,s22,0x40004\n"
"s_lshl_b32 s59,s72,4\n"
"s_add_u32 s59,s59,s58\n"
"s_lshl_b32 s86,s59,12\n"
"s_add_u32 s86,s86,100663296\n"
"s_mul_i32 s93,s59,1638400\n"
"s_add_u32 s93,s93,33554432\n"
"s_bfe_u32 s58,s22,0x30014\n"
"s_lshl_b32 s78,s58,1\n"
"s_add_u32 s78,s78,s74\n"
"s_lshl_b32 s94,s78,2\n"
"s_add_u32 s94,s94,s86\n"
"s_lshl_b32 s92,s58,2\n"
"s_add_u32 s92,s92,s56\n"
"s_mul_i32 s91,s58,204800\n"
"s_add_u32 s91,s91,s93\n"
"s_mul_i32 s75,s56,30720\n"
LD "v17,s[28:31],s82 offen offset:2048 lds\n"
"s_add_u32 s75,s75,s91\n"
"s_add_u32 s75,s75,0\n"
"s_mul_i32 s58,s74,5120\n"
"s_add_u32 s91,s91,s58\n"
"s_mov_b32 s90,0xf0000000\n"
"s_mov_b32 s77,0xf0000000\n"
"s_mov_b32 s85,0xf0000000\n"
AW "a240,v0\n"
AW "a244,v1\n"
AW "a241,v2\n"
AW "a245,v3\n"
AW "a242,v4\n"
AW "a243,v6\n"
AW "a252,v12\n"
"s_mov_b32 s57,0\n"
"s_mov_b32 s83,0\n"
LD "v18,s[28:31],s82 offen offset:3072 lds\n"
"global_load_dword a249,v15,s[52:53]\n"
AR "v0,a240\n"
AR "v2,a241\n"
AR "v4,a242\n"
AR "v6,a243\n"
"v_xor_b32 v1,64,v0\n"
"v_xor_b32 v3,64,v2\n"
"v_xor_b32_e64 v5,64,v4\n"
"v_add_u32 v11,97280,v6\n"
"v_add_u32 v12,194560,v6\n"
"v_add_u32 v13,291840,v6\n"
"s_mov_b64 vcc,0\n"
"s_waitcnt vmcnt(9) lgkmcnt(0)\n"
"s_barrier\n"
"s_nop 0\n"
TR "a[200:201],v0\n"
TR "a[202:203],v0 offset:512\n"
TR "a[204:205],v0 offset:4096\n"
TR "a[206:207],v0 offset:4608\n"
"ds_read_b128 a[60:63],v2\n"
"ds_read_b128 a[100:103],v3\n"
"ds_read_b128 a[64:67],v2 offset:2048\n"
"ds_read_b128 a[104:107],v3 offset:2048\n"
"ds_read_b128 a[68:71],v2 offset:4096\n"
"ds_read_b128 a[108:111],v3 offset:4096\n"
"ds_read_b128 a[72:75],v2 offset:6144\n"
"ds_read_b128 a[112:115],v3 offset:6144\n"
"ds_read_b128 a[76:79],v2 offset:8192\n"
"ds_read_b128 a[116:119],v3 offset:8192\n"
TR "a[208:209],v0 offset:8\n"
TR "a[210:211],v0 offset:520\n"
TR "a[212:213],v0 offset:4104\n"
TR "a[214:215],v0 offset:4616\n"
"s_waitcnt lgkmcnt(0)\n"
MF "v[16:19],a[200:203],a[60:63],0\n"
TR "a[216:217],v1\n"
MF "v[16:19],a[204:207],a[100:103],v[16:19]\n"
"s_add_u32 s14,s81,128\n"
TR "a[218:219],v1 offset:512\n"
MF "v[24:27],a[204:207],a[104:107],0\n"
"s_sub_u32 s15,s14,1280\n"
"v_lshrrev_b32 v15,5,v9\n"
"v_min_u32 v15,11,v15\n"
MF "v[24:27],a[200:203],a[64:67],v[24:27]\n"
"s_min_u32 s14,s14,s15\n"
TR "a[220:221],v1 offset:4096\n"
"v_mul_u32_u24 v15,160,v15\n"
MF "v[32:35],a[200:203],a[68:71],0\n"
"s_add_u32 s14,s14,s68\n"
TR "a[222:223],v1 offset:4608\n"
"v_and_or_b32 v15,v9,16,v15\n"
MF "v[32:35],a[204:207],a[108:111],v[32:35]\n"
"s_add_u32 s84,s14,0\n"
"v_max3_f32 v220,|v16|,|v17|,|v18|\n"
"v_max_f32_e64 v220,v220,|v19|\n"
MF "v[40:43],a[204:207],a[112:115],0\n"
"s_add_u32 m0,s63,20480\n"
"v_readlane_b32 s12,v220,3\n"
"v_readlane_b32 s6,v220,22\n"
MF "v[40:43],a[200:203],a[72:75],v[40:43]\n"
"s_min_u32 s12,s12,s6\n"
"v_readlane_b32 s7,v220,41\n"
"v_readlane_b32 s8,v220,60\n"
MF "v[48:51],a[200:203],a[76:79],0\n"
"s_min_u32 s7,s7,s8\n"
"v_lshlrev_b32 v15,3,v15\n"
MF "v[48:51],a[204:207],a[116:119],v[48:51]\n"
LD "v4,s[24:27],s84 offen lds\n"
MF "v[52:55],a[212:215],a[116:119],0\n"
"s_min_u32 s12,s12,s7\n"
TR "a[200:201],v1 offset:8\n"
MF "v[52:55],a[208:211],a[76:79],v[52:55]\n"
"s_and_b32 s12,s12,0x7f800000\n"
TR "a[202:203],v1 offset:520\n"
MF "v[44:47],a[208:211],a[72:75],0\n"
"s_sub_u32 s12,s12,0x14000000\n"
MF "v[44:47],a[212:215],a[112:115],v[44:47]\n"
"s_cmp_lt_u32 s12,0x50000000\n"
"s_cselect_b32 s12,s12,0xe8400000\n"
TR "a[204:205],v1 offset:4104\n"
MF "v[36:39],a[212:215],a[108:111],0\n"
"s_add_u32 s12,s12,0x17c00000\n"
TR "a[206:207],v1 offset:4616\n"
MF "v[36:39],a[208:211],a[68:71],v[36:39]\n"
"s_bfe_u32 s60,s23,0x70000\n"
"v_mov_b32 v220,s12\n"
"v_mov_b32 v221,s12\n"
MF "v[28:31],a[208:211],a[64:67],0\n"
"s_add_u32 s84,s14,9216\n"
"v_mov_b32 v222,s12\n"
"v_mov_b32 v223,s12\n"
MF "v[28:31],a[212:215],a[104:107],v[28:31]\n"
LD "v5,s[24:27],s84 offen offset:1024 lds\n"
MF "v[20:23],a[212:215],a[100:103],0\n"
"s_waitcnt vmcnt(7) lgkmcnt(0)\n"
MF "v[20:23],a[208:211],a[60:63],v[20:23]\n"
"s_barrier\n"
MF "v[56:59],a[216:219],a[60:63],v[220:223]\n"
"s_lshl_b32 s60,s60,2\n"
TR "a[208:209],v0 offset:16384\n"
MF "v[56:59],a[220:223],a[100:103],v[56:59]\n"
"s_add_u32 s84,s14,18432\n"
TR "a[210:211],v0 offset:16896\n"
MF "v[64:67],a[220:223],a[104:107],v[220:223]\n"
LD "v4,s[24:27],s84 offen offset:2048 lds\n"
MF "v[64:67],a[216:219],a[64:67],v[64:67]\n"
"s_add_u32 s60,s60,s56\n"
TR "a[212:213],v0 offset:20480\n"
MF "v[72:75],a[216:219],a[68:71],v[220:223]\n"
"s_mul_i32 s60,s60,15360\n"
TR "a[214:215],v0 offset:20992\n"
MF "v[72:75],a[220:223],a[108:111],v[72:75]\n"
MF "v[80:83],a[220:223],a[112:115],v[220:223]\n"
"s_add_u32 s84,s14,27648\n"
MF "v[80:83],a[216:219],a[72:75],v[80:83]\n"
LD "v5,s[24:27],s84 offen offset:3072 lds\n"
MF "v[88:91],a[216:219],a[76:79],v[220:223]\n"
MF "v[88:91],a[220:223],a[116:119],v[88:91]\n"
MF "v[92:95],a[204:207],a[116:119],v[220:223]\n"
"s_add_u32 s84,s14,40960\n"
TR "a[216:217],v0 offset:16392\n"
MF "v[92:95],a[200:203],a[76:79],v[92:95]\n"
"s_add_u32 m0,s63,24576\n"
TR "a[218:219],v0 offset:16904\n"
MF "v[84:87],a[200:203],a[72:75],v[220:223]\n"
LD "v4,s[24:27],s84 offen lds\n"
MF "v[84:87],a[204:207],a[112:115],v[84:87]\n"
"s_add_u32 m0,s62,49152\n"
TR "a[220:221],v0 offset:20488\n"
MF "v[76:79],a[204:207],a[108:111],v[220:223]\n"
"s_add_u32 s82,s66,786432\n"
TR "a[222:223],v0 offset:21000\n"
MF "v[76:79],a[200:203],a[68:71],v[76:79]\n"
"s_sub_u32 s15,s82,7864320\n"
MF "v[68:71],a[200:203],a[64:67],v[220:223]\n"
"s_min_u32 s82,s82,s15\n"
MF "v[68:71],a[204:207],a[104:107],v[68:71]\n"
LD "v6,s[28:31],s82 offen lds\n"
MF "v[60:63],a[204:207],a[100:103],v[220:223]\n"
MF "v[60:63],a[200:203],a[60:63],v[60:63]\n"
"s_waitcnt lgkmcnt(4)\n"
MF "v[96:99],a[208:211],a[60:63],v[220:223]\n"
TR "a[200:201],v1 offset:16384\n"
MF "v[96:99],a[212:215],a[100:103],v[96:99]\n"
TR "a[202:203],v1 offset:16896\n"
MF "v[104:107],a[212:215],a[104:107],v[220:223]\n"
LD "v11,s[28:31],s82 offen offset:1024 lds\n"
MF "v[104:107],a[208:211],a[64:67],v[104:107]\n"
TR "a[204:205],v1 offset:20480\n"
MF "v[112:115],a[208:211],a[68:71],v[220:223]\n"
TR "a[206:207],v1 offset:20992\n"
MF "v[112:115],a[212:215],a[108:111],v[112:115]\n"
MF "v[120:123],a[212:215],a[112:115],v[220:223]\n"
MF "v[120:123],a[208:211],a[72:75],v[120:123]\n"
LD "v12,s[28:31],s82 offen offset:2048 lds\n"
MF "v[128:131],a[208:211],a[76:79],v[220:223]\n"
MF "v[128:131],a[212:215],a[116:119],v[128:131]\n"
"s_waitcnt lgkmcnt(4)\n"
MF "v[132:135],a[220:223],a[116:119],v[220:223]\n"
TR "a[208:209],v1 offset:16392\n"
MF "v[132:135],a[216:219],a[76:79],v[132:135]\n"
TR "a[210:211],v1 offset:16904\n"
MF "v[124:127],a[216:219],a[72:75],v[220:223]\n"
LD "v13,s[28:31],s82 offen offset:3072 lds\n"
MF "v[124:127],a[220:223],a[112:115],v[124:127]\n"
"s_add_u32 m0,s62,65536\n"
TR "a[212:213],v1 offset:20488\n"
MF "v[116:119],a[220:223],a[108:111],v[220:223]\n"
"s_add_u32 s82,s66,786560\n"
TR "a[214:215],v1 offset:21000\n"
MF "v[116:119],a[216:219],a[68:71],v[116:119]\n"
"s_sub_u32 s15,s82,7864320\n"
MF "v[108:111],a[216:219],a[64:67],v[220:223]\n"
"s_min_u32 s82,s82,s15\n"
MF "v[108:111],a[220:223],a[104:107],v[108:111]\n"
LD "v6,s[28:31],s82 offen lds\n"
MF "v[100:103],a[220:223],a[100:103],v[220:223]\n"
"s_waitcnt vmcnt(5) lgkmcnt(0)\n"
MF "v[100:103],a[216:219],a[60:63],v[100:103]\n"
"s_barrier\n"
MF "v[136:139],a[200:203],a[60:63],v[220:223]\n"
TR "a[216:217],v0 offset:32768\n"
MF "v[136:139],a[204:207],a[100:103],v[136:139]\n"
TR "a[218:219],v0 offset:33280\n"
MF "v[144:147],a[204:207],a[104:107],v[220:223]\n"
"ds_read_b128 a[140:143],v2 offset:20480\n"
MF "v[144:147],a[200:203],a[64:67],v[144:147]\n"
TR "a[220:221],v0 offset:36864\n"
MF "v[152:155],a[200:203],a[68:71],v[220:223]\n"
TR "a[222:223],v0 offset:37376\n"
MF "v[152:155],a[204:207],a[108:111],v[152:155]\n"
LD "v11,s[28:31],s82 offen offset:1024 lds\n"
MF "v[160:163],a[204:207],a[112:115],v[220:223]\n"
"ds_read_b128 a[180:183],v3 offset:20480\n"
MF "v[160:163],a[200:203],a[72:75],v[160:163]\n"
MF "v[168:171],a[200:203],a[76:79],v[220:223]\n"
MF "v[168:171],a[204:207],a[116:119],v[168:171]\n"
LD "v12,s[28:31],s82 offen offset:2048 lds\n"
MF "v[172:175],a[212:215],a[116:119],v[220:223]\n"
TR "a[200:201],v0 offset:32776\n"
MF "v[172:175],a[208:211],a[76:79],v[172:175]\n"
TR "a[202:203],v0 offset:33288\n"
MF "v[164:167],a[208:211],a[72:75],v[220:223]\n"
"ds_read_b128 a[144:147],v2 offset:22528\n"
MF "v[164:167],a[212:215],a[112:115],v[164:167]\n"
TR "a[204:205],v0 offset:36872\n"
MF "v[156:159],a[212:215],a[108:111],v[220:223]\n"
TR "a[206:207],v0 offset:37384\n"
MF "v[156:159],a[208:211],a[68:71],v[156:159]\n"
LD "v13,s[28:31],s82 offen offset:3072 lds\n"
MF "v[148:151],a[208:211],a[64:67],v[220:223]\n"
"ds_read_b128 a[184:187],v3 offset:22528\n"
MF "v[148:151],a[212:215],a[104:107],v[148:151]\n"
"s_add_u32 m0,s62,81920\n"
MF "v[140:143],a[212:215],a[100:103],v[220:223]\n"
"s_add_u32 s82,s66,786688\n"
"s_sub_u32 s15,s82,7864320\n"
"s_min_u32 s82,s82,s15\n"
MF "v[140:143],a[208:211],a[60:63],v[140:143]\n"
"s_waitcnt lgkmcnt(7)\n"
LD "v6,s[28:31],s82 offen lds\n"
MF "v[176:179],a[216:219],a[60:63],v[220:223]\n"
TR "a[208:209],v1 offset:32768\n"
MF "v[176:179],a[220:223],a[100:103],v[176:179]\n"
TR "a[210:211],v1 offset:33280\n"
MF "v[184:187],a[220:223],a[104:107],v[220:223]\n"
"ds_read_b128 a[148:151],v2 offset:24576\n"
MF "v[184:187],a[216:219],a[64:67],v[184:187]\n"
TR "a[212:213],v1 offset:36864\n"
MF "v[192:195],a[216:219],a[68:71],v[220:223]\n"
TR "a[214:215],v1 offset:37376\n"
MF "v[192:195],a[220:223],a[108:111],v[192:195]\n"
LD "v11,s[28:31],s82 offen offset:1024 lds\n"
MF "v[200:203],a[220:223],a[112:115],v[220:223]\n"
"ds_read_b128 a[188:191],v3 offset:24576\n"
MF "v[200:203],a[216:219],a[72:75],v[200:203]\n"
MF "v[208:211],a[216:219],a[76:79],v[220:223]\n"
MF "v[208:211],a[220:223],a[116:119],v[208:211]\n"
"s_waitcnt lgkmcnt(7)\n"
LD "v12,s[28:31],s82 offen offset:2048 lds\n"
MF "v[212:215],a[204:207],a[116:119],v[220:223]\n"
TR "a[216:217],v1 offset:32776\n"
MF "v[212:215],a[200:203],a[76:79],v[212:215]\n"
TR "a[218:219],v1 offset:33288\n"
MF "v[204:207],a[200:203],a[72:75],v[220:223]\n"
"ds_read_b128 a[152:155],v2 offset:26624\n"
MF "v[204:207],a[204:207],a[112:115],v[204:207]\n"
TR "a[220:221],v1 offset:36872\n"
MF "v[196:199],a[204:207],a[108:111],v[220:223]\n"
TR "a[222:223],v1 offset:37384\n"
MF "v[196:199],a[200:203],a[68:71],v[196:199]\n"
LD "v13,s[28:31],s82 offen offset:3072 lds\n"
MF "v[188:191],a[200:203],a[64:67],v[220:223]\n"
"v_add_u32 v0,0xc000,v0\n"
MF "v[188:191],a[204:207],a[104:107],v[188:191]\n"
"s_add_u32 s14,s81,256\n"
"v_add_u32 v1,0xc000,v1\n"
MF "v[180:183],a[204:207],a[100:103],v[220:223]\n"
"s_waitcnt vmcnt(8) lgkmcnt(0)\n"
"s_mov_b64 exec,0\n"
"s_bitcmp1_b32 s23,12\n"
"s_cbranch_scc1 .Lisy1\n"
"s_cmp_eq_u32 s56,0\n"
"s_cbranch_scc1 .Lpg8b\n"
"s_cmp_eq_u32 s56,1\n"
"s_cbranch_scc1 .Lpg9b\n"
"s_cmp_eq_u32 s56,2\n"
"s_cbranch_scc1 .Lpgt0ex_b\n"
"s_branch .Lpg10b\n"
".Lisy1:\n"
"s_cmp_eq_u32 s56,0\n"
"s_cbranch_scc1 .Lpgq0_c\n"
"s_cmp_eq_u32 s56,1\n"
"s_cbranch_scc1 .Lpg4c\n"
"s_cmp_eq_u32 s56,2\n"
"s_cbranch_scc1 .Lpg5c\n"
"s_branch .Lpg6c\n"
".Lisr1:\n"
"s_mov_b64 exec,-1\n"
".Lisx1:\n"
MF "v[180:183],a[200:203],a[60:63],v[180:183]\n"
"s_barrier\n"
MF "v[216:219],a[208:211],a[60:63],v[220:223]\n"
"s_sub_u32 s15,s14,1280\n"
TR "a[200:201],v0\n"
MF "v[216:219],a[212:215],a[100:103],v[216:219]\n"
"s_min_u32 s14,s14,s15\n"
TR "a[202:203],v0 offset:512\n"
MF "v[224:227],a[212:215],a[104:107],v[220:223]\n"
"s_add_u32 s14,s14,s68\n"
"ds_read_b128 a[192:195],v3 offset:26624\n"
MF "v[224:227],a[208:211],a[64:67],v[224:227]\n"
"s_add_u32 s84,s14,0\n"
TR "a[204:205],v0 offset:4096\n"
MF "v[232:235],a[208:211],a[68:71],v[220:223]\n"
"s_add_u32 m0,s63,40960\n"
TR "a[206:207],v0 offset:4608\n"
MF "v[232:235],a[212:215],a[108:111],v[232:235]\n"
LD "v4,s[24:27],s84 offen lds\n"
MF "v[240:243],a[212:215],a[112:115],v[220:223]\n"
"ds_read_b128 a[156:159],v2 offset:28672\n"
MF "v[240:243],a[208:211],a[72:75],v[240:243]\n"
"ds_read_b128 a[196:199],v3 offset:28672\n"
MF "v[248:251],a[208:211],a[76:79],v[220:223]\n"
"s_add_u32 s84,s14,9216\n"
MF "v[248:251],a[212:215],a[116:119],v[248:251]\n"
LD "v5,s[24:27],s84 offen offset:1024 lds\n"
MF "v[252:255],a[220:223],a[116:119],v[220:223]\n"
TR "a[208:209],v0 offset:8\n"
MF "v[252:255],a[216:219],a[76:79],v[252:255]\n"
TR "a[210:211],v0 offset:520\n"
MF "v[244:247],a[216:219],a[72:75],v[220:223]\n"
MF "v[244:247],a[220:223],a[112:115],v[244:247]\n"
TR "a[212:213],v0 offset:4104\n"
MF "v[236:239],a[220:223],a[108:111],v[220:223]\n"
"s_add_u32 s84,s14,18432\n"
TR "a[214:215],v0 offset:4616\n"
MF "v[236:239],a[216:219],a[68:71],v[236:239]\n"
LD "v4,s[24:27],s84 offen offset:2048 lds\n"
MF "v[228:231],a[216:219],a[64:67],v[220:223]\n"
MF "v[228:231],a[220:223],a[104:107],v[228:231]\n"
MF "v[220:223],a[220:223],a[100:103],v[220:223]\n"
".Lpg1a:\n"
"s_cbranch_execz .Lpg7a\n"
"s_add_u32 s84,s14,27648\n"
MF "v[220:223],a[216:219],a[60:63],v[220:223]\n"
"s_waitcnt lgkmcnt(4)\n"
LD "v5,s[24:27],s84 offen offset:3072 lds\n"
MF "v[16:19],a[200:203],a[140:143],v[16:19]\n"
TR "a[216:217],v1\n"
MF "v[16:19],a[204:207],a[180:183],v[16:19]\n"
TR "a[218:219],v1 offset:512\n"
MF "v[24:27],a[204:207],a[184:187],v[24:27]\n"
MF "v[24:27],a[200:203],a[144:147],v[24:27]\n"
"s_add_u32 s84,s14,40960\n"
TR "a[220:221],v1 offset:4096\n"
MF "v[32:35],a[200:203],a[148:151],v[32:35]\n"
"s_add_u32 m0,s63,45056\n"
TR "a[222:223],v1 offset:4608\n"
MF "v[32:35],a[204:207],a[188:191],v[32:35]\n"
LD "v4,s[24:27],s84 offen lds\n"
MF "v[40:43],a[204:207],a[192:195],v[40:43]\n"
MF "v[40:43],a[200:203],a[152:155],v[40:43]\n"
"s_add_u32 m0,s62,0\n"
MF "v[48:51],a[200:203],a[156:159],v[48:51]\n"
"s_add_u32 s82,s66,1572864\n"
"s_sub_u32 s15,s82,7864320\n"
"s_min_u32 s82,s82,s15\n"
MF "v[48:51],a[204:207],a[196:199],v[48:51]\n"
"s_waitcnt lgkmcnt(4)\n"
LD "v6,s[28:31],s82 offen lds\n"
MF "v[52:55],a[212:215],a[196:199],v[52:55]\n"
TR "a[200:201],v1 offset:8\n"
MF "v[52:55],a[208:211],a[156:159],v[52:55]\n"
TR "a[202:203],v1 offset:520\n"
MF "v[44:47],a[208:211],a[152:155],v[44:47]\n"
MF "v[44:47],a[212:215],a[192:195],v[44:47]\n"
TR "a[204:205],v1 offset:4104\n"
MF "v[36:39],a[212:215],a[188:191],v[36:39]\n"
TR "a[206:207],v1 offset:4616\n"
MF "v[36:39],a[208:211],a[148:151],v[36:39]\n"
LD "v11,s[28:31],s82 offen offset:1024 lds\n"
MF "v[28:31],a[208:211],a[144:147],v[28:31]\n"
MF "v[28:31],a[212:215],a[184:187],v[28:31]\n"
MF "v[20:23],a[212:215],a[180:183],v[20:23]\n"
"s_waitcnt vmcnt(11) lgkmcnt(0)\n"
LD "v12,s[28:31],s82 offen offset:2048 lds\n"
MF "v[20:23],a[208:211],a[140:143],v[20:23]\n"
"s_barrier\n"
"v_bfe_u32 v14,v9,4,4\n"
"v_lshlrev_b32 v14,2,v14\n"
"buffer_load_dword v10,v14,s[40:43],s86 offen sc1\n"
".Lhs2:\n"
MF "v[56:59],a[216:219],a[140:143],v[56:59]\n"
TR "a[208:209],v0 offset:16384\n"
MF "v[56:59],a[220:223],a[180:183],v[56:59]\n"
TR "a[210:211],v0 offset:16896\n"
MF "v[64:67],a[220:223],a[184:187],v[64:67]\n"
LD "v13,s[28:31],s82 offen offset:3072 lds\n"
MF "v[64:67],a[216:219],a[144:147],v[64:67]\n"
TR "a[212:213],v0 offset:20480\n"
MF "v[72:75],a[216:219],a[148:151],v[72:75]\n"
TR "a[214:215],v0 offset:20992\n"
MF "v[72:75],a[220:223],a[188:191],v[72:75]\n"
"s_add_u32 s14,s81,384\n"
"s_sub_u32 s15,s14,1280\n"
"s_min_u32 s14,s14,s15\n"
MF "v[80:83],a[220:223],a[192:195],v[80:83]\n"
"s_add_u32 s14,s14,s68\n"
"s_add_u32 s84,s14,0\n"
"s_add_u32 m0,s63,0\n"
MF "v[80:83],a[216:219],a[152:155],v[80:83]\n"
LD "v4,s[24:27],s84 offen lds\n"
MF "v[88:91],a[216:219],a[156:159],v[88:91]\n"
MF "v[88:91],a[220:223],a[196:199],v[88:91]\n"
MF "v[92:95],a[204:207],a[196:199],v[92:95]\n"
TR "a[216:217],v0 offset:16392\n"
MF "v[92:95],a[200:203],a[156:159],v[92:95]\n"
"s_add_u32 s84,s14,9216\n"
TR "a[218:219],v0 offset:16904\n"
MF "v[84:87],a[200:203],a[152:155],v[84:87]\n"
LD "v5,s[24:27],s84 offen offset:1024 lds\n"
MF "v[84:87],a[204:207],a[192:195],v[84:87]\n"
"s_add_u32 m0,s62,16384\n"
TR "a[220:221],v0 offset:20488\n"
MF "v[76:79],a[204:207],a[188:191],v[76:79]\n"
"s_add_u32 s82,s66,1572992\n"
TR "a[222:223],v0 offset:21000\n"
MF "v[76:79],a[200:203],a[148:151],v[76:79]\n"
"s_sub_u32 s15,s82,7864320\n"
MF "v[68:71],a[200:203],a[144:147],v[68:71]\n"
"s_min_u32 s82,s82,s15\n"
MF "v[68:71],a[204:207],a[184:187],v[68:71]\n"
LD "v6,s[28:31],s82 offen lds\n"
MF "v[60:63],a[204:207],a[180:183],v[60:63]\n"
MF "v[60:63],a[200:203],a[140:143],v[60:63]\n"
"s_waitcnt lgkmcnt(4)\n"
MF "v[96:99],a[208:211],a[140:143],v[96:99]\n"
TR "a[200:201],v1 offset:16384\n"
MF "v[96:99],a[212:215],a[180:183],v[96:99]\n"
TR "a[202:203],v1 offset:16896\n"
MF "v[104:107],a[212:215],a[184:187],v[104:107]\n"
LD "v11,s[28:31],s82 offen offset:1024 lds\n"
MF "v[104:107],a[208:211],a[144:147],v[104:107]\n"
TR "a[204:205],v1 offset:20480\n"
MF "v[112:115],a[208:211],a[148:151],v[112:115]\n"
TR "a[206:207],v1 offset:20992\n"
MF "v[112:115],a[212:215],a[188:191],v[112:115]\n"
MF "v[120:123],a[212:215],a[192:195],v[120:123]\n"
MF "v[120:123],a[208:211],a[152:155],v[120:123]\n"
LD "v12,s[28:31],s82 offen offset:2048 lds\n"
MF "v[128:131],a[208:211],a[156:159],v[128:131]\n"
MF "v[128:131],a[212:215],a[196:199],v[128:131]\n"
"s_waitcnt lgkmcnt(4)\n"
MF "v[132:135],a[220:223],a[196:199],v[132:135]\n"
TR "a[208:209],v1 offset:16392\n"
MF "v[132:135],a[216:219],a[156:159],v[132:135]\n"
TR "a[210:211],v1 offset:16904\n"
MF "v[124:127],a[216:219],a[152:155],v[124:127]\n"
LD "v13,s[28:31],s82 offen offset:3072 lds\n"
MF "v[124:127],a[220:223],a[192:195],v[124:127]\n"
TR "a[212:213],v1 offset:20488\n"
MF "v[116:119],a[220:223],a[188:191],v[116:119]\n"
TR "a[214:215],v1 offset:21000\n"
MF "v[116:119],a[216:219],a[148:151],v[116:119]\n"
"s_add_u32 s84,s14,20480\n"
MF "v[108:111],a[216:219],a[144:147],v[108:111]\n"
"s_add_u32 m0,s63,2048\n"
MF "v[108:111],a[220:223],a[184:187],v[108:111]\n"
LD "v4,s[24:27],s84 offen lds\n"
MF "v[100:103],a[220:223],a[180:183],v[100:103]\n"
"s_waitcnt vmcnt(11) lgkmcnt(0)\n"
MF "v[100:103],a[216:219],a[140:143],v[100:103]\n"
"s_barrier\n"
MF "v[136:139],a[200:203],a[140:143],v[136:139]\n"
TR "a[216:217],v0 offset:32768\n"
MF "v[136:139],a[204:207],a[180:183],v[136:139]\n"
TR "a[218:219],v0 offset:33280\n"
MF "v[144:147],a[204:207],a[184:187],v[144:147]\n"
"ds_read_b128 a[60:63],v2 offset:40960\n"
MF "v[144:147],a[200:203],a[144:147],v[144:147]\n"
TR "a[220:221],v0 offset:36864\n"
MF "v[152:155],a[200:203],a[148:151],v[152:155]\n"
"s_add_u32 s84,s14,29696\n"
TR "a[222:223],v0 offset:37376\n"
MF "v[152:155],a[204:207],a[188:191],v[152:155]\n"
LD "v5,s[24:27],s84 offen offset:1024 lds\n"
MF "v[160:163],a[204:207],a[192:195],v[160:163]\n"
"ds_read_b128 a[100:103],v3 offset:40960\n"
MF "v[160:163],a[200:203],a[152:155],v[160:163]\n"
"s_add_u32 m0,s62,32768\n"
MF "v[168:171],a[200:203],a[156:159],v[168:171]\n"
"s_add_u32 s82,s66,1573120\n"
"s_sub_u32 s15,s82,7864320\n"
"s_min_u32 s82,s82,s15\n"
MF "v[168:171],a[204:207],a[196:199],v[168:171]\n"
LD "v6,s[28:31],s82 offen lds\n"
MF "v[172:175],a[212:215],a[196:199],v[172:175]\n"
TR "a[200:201],v0 offset:32776\n"
MF "v[172:175],a[208:211],a[156:159],v[172:175]\n"
TR "a[202:203],v0 offset:33288\n"
MF "v[164:167],a[208:211],a[152:155],v[164:167]\n"
"ds_read_b128 a[64:67],v2 offset:43008\n"
MF "v[164:167],a[212:215],a[192:195],v[164:167]\n"
TR "a[204:205],v0 offset:36872\n"
MF "v[156:159],a[212:215],a[188:191],v[156:159]\n"
TR "a[206:207],v0 offset:37384\n"
MF "v[156:159],a[208:211],a[148:151],v[156:159]\n"
LD "v11,s[28:31],s82 offen offset:1024 lds\n"
MF "v[148:151],a[208:211],a[144:147],v[148:151]\n"
"ds_read_b128 a[104:107],v3 offset:43008\n"
MF "v[148:151],a[212:215],a[184:187],v[148:151]\n"
MF "v[140:143],a[212:215],a[180:183],v[140:143]\n"
MF "v[140:143],a[208:211],a[140:143],v[140:143]\n"
"s_waitcnt lgkmcnt(7)\n"
LD "v12,s[28:31],s82 offen offset:2048 lds\n"
MF "v[176:179],a[216:219],a[140:143],v[176:179]\n"
TR "a[208:209],v1 offset:32768\n"
MF "v[176:179],a[220:223],a[180:183],v[176:179]\n"
TR "a[210:211],v1 offset:33280\n"
MF "v[184:187],a[220:223],a[184:187],v[184:187]\n"
"ds_read_b128 a[68:71],v2 offset:45056\n"
MF "v[184:187],a[216:219],a[144:147],v[184:187]\n"
TR "a[212:213],v1 offset:36864\n"
MF "v[192:195],a[216:219],a[148:151],v[192:195]\n"
TR "a[214:215],v1 offset:37376\n"
MF "v[192:195],a[220:223],a[188:191],v[192:195]\n"
LD "v13,s[28:31],s82 offen offset:3072 lds\n"
MF "v[200:203],a[220:223],a[192:195],v[200:203]\n"
"ds_read_b128 a[108:111],v3 offset:45056\n"
MF "v[200:203],a[216:219],a[152:155],v[200:203]\n"
"s_add_u32 s84,s14,40960\n"
MF "v[208:211],a[216:219],a[156:159],v[208:211]\n"
"s_add_u32 m0,s63,4096\n"
MF "v[208:211],a[220:223],a[196:199],v[208:211]\n"
"s_waitcnt lgkmcnt(7)\n"
LD "v4,s[24:27],s84 offen lds\n"
MF "v[212:215],a[204:207],a[196:199],v[212:215]\n"
"s_sub_u32 s66,s66,s83\n"
TR "a[216:217],v1 offset:32776\n"
MF "v[212:215],a[200:203],a[156:159],v[212:215]\n"
"s_add_u32 m0,s62,49152\n"
TR "a[218:219],v1 offset:33288\n"
MF "v[204:207],a[200:203],a[152:155],v[204:207]\n"
"s_add_u32 s82,s66,2359296\n"
"ds_read_b128 a[72:75],v2 offset:47104\n"
MF "v[204:207],a[204:207],a[192:195],v[204:207]\n"
"s_sub_u32 s15,s82,7864320\n"
TR "a[220:221],v1 offset:36872\n"
MF "v[196:199],a[204:207],a[188:191],v[196:199]\n"
"s_min_u32 s82,s82,s15\n"
TR "a[222:223],v1 offset:37384\n"
MF "v[196:199],a[200:203],a[148:151],v[196:199]\n"
LD "v6,s[28:31],s82 offen lds\n"
MF "v[188:191],a[200:203],a[144:147],v[188:191]\n"
"v_add_u32 v0,0xffff4000,v0\n"
MF "v[188:191],a[204:207],a[184:187],v[188:191]\n"
"v_add_u32 v1,0xffff4000,v1\n"
MF "v[180:183],a[204:207],a[180:183],v[180:183]\n"
"s_waitcnt vmcnt(14) lgkmcnt(0)\n"
LD "v11,s[28:31],s82 offen offset:1024 lds\n"
MF "v[180:183],a[200:203],a[140:143],v[180:183]\n"
"s_barrier\n"
MF "v[216:219],a[208:211],a[140:143],v[216:219]\n"
TR "a[200:201],v0\n"
MF "v[216:219],a[212:215],a[180:183],v[216:219]\n"
TR "a[202:203],v0 offset:512\n"
MF "v[224:227],a[212:215],a[184:187],v[224:227]\n"
"ds_read_b128 a[112:115],v3 offset:47104\n"
MF "v[224:227],a[208:211],a[144:147],v[224:227]\n"
TR "a[204:205],v0 offset:4096\n"
MF "v[232:235],a[208:211],a[148:151],v[232:235]\n"
TR "a[206:207],v0 offset:4608\n"
MF "v[232:235],a[212:215],a[188:191],v[232:235]\n"
LD "v12,s[28:31],s82 offen offset:2048 lds\n"
MF "v[240:243],a[212:215],a[192:195],v[240:243]\n"
"ds_read_b128 a[76:79],v2 offset:49152\n"
MF "v[240:243],a[208:211],a[152:155],v[240:243]\n"
"ds_read_b128 a[116:119],v3 offset:49152\n"
MF "v[248:251],a[208:211],a[156:159],v[248:251]\n"
MF "v[248:251],a[212:215],a[196:199],v[248:251]\n"
LD "v13,s[28:31],s82 offen offset:3072 lds\n"
MF "v[252:255],a[220:223],a[196:199],v[252:255]\n"
TR "a[208:209],v0 offset:8\n"
MF "v[252:255],a[216:219],a[156:159],v[252:255]\n"
TR "a[210:211],v0 offset:520\n"
MF "v[244:247],a[216:219],a[152:155],v[244:247]\n"
"s_add_u32 s14,s81,512\n"
"s_sub_u32 s15,s14,1280\n"
MF "v[244:247],a[220:223],a[192:195],v[244:247]\n"
"s_min_u32 s14,s14,s15\n"
"s_add_u32 s14,s14,s68\n"
TR "a[212:213],v0 offset:4104\n"
MF "v[236:239],a[220:223],a[188:191],v[236:239]\n"
"s_add_u32 s84,s14,0\n"
"s_add_u32 m0,s63,20480\n"
TR "a[214:215],v0 offset:4616\n"
MF "v[236:239],a[216:219],a[148:151],v[236:239]\n"
LD "v4,s[24:27],s84 offen lds\n"
MF "v[228:231],a[216:219],a[144:147],v[228:231]\n"
MF "v[228:231],a[220:223],a[184:187],v[228:231]\n"
MF "v[220:223],a[220:223],a[180:183],v[220:223]\n"
"s_add_u32 s84,s14,9216\n"
MF "v[220:223],a[216:219],a[140:143],v[220:223]\n"
"s_waitcnt lgkmcnt(4)\n"
LD "v5,s[24:27],s84 offen offset:1024 lds\n"
MF "v[16:19],a[200:203],a[60:63],v[16:19]\n"
TR "a[216:217],v1\n"
MF "v[16:19],a[204:207],a[100:103],v[16:19]\n"
"s_add_u32 m0,s62,65536\n"
TR "a[218:219],v1 offset:512\n"
MF "v[24:27],a[204:207],a[104:107],v[24:27]\n"
"s_add_u32 s82,s66,2359424\n"
MF "v[24:27],a[200:203],a[64:67],v[24:27]\n"
"s_sub_u32 s15,s82,7864320\n"
TR "a[220:221],v1 offset:4096\n"
MF "v[32:35],a[200:203],a[68:71],v[32:35]\n"
"s_min_u32 s82,s82,s15\n"
TR "a[222:223],v1 offset:4608\n"
MF "v[32:35],a[204:207],a[108:111],v[32:35]\n"
LD "v6,s[28:31],s82 offen lds\n"
MF "v[40:43],a[204:207],a[112:115],v[40:43]\n"
MF "v[40:43],a[200:203],a[72:75],v[40:43]\n"
MF "v[48:51],a[200:203],a[76:79],v[48:51]\n"
MF "v[48:51],a[204:207],a[116:119],v[48:51]\n"
"s_waitcnt lgkmcnt(4)\n"
LD "v11,s[28:31],s82 offen offset:1024 lds\n"
MF "v[52:55],a[212:215],a[116:119],v[52:55]\n"
TR "a[200:201],v1 offset:8\n"
MF "v[52:55],a[208:211],a[76:79],v[52:55]\n"
TR "a[202:203],v1 offset:520\n"
MF "v[44:47],a[208:211],a[72:75],v[44:47]\n"
MF "v[44:47],a[212:215],a[112:115],v[44:47]\n"
TR "a[204:205],v1 offset:4104\n"
MF "v[36:39],a[212:215],a[108:111],v[36:39]\n"
TR "a[206:207],v1 offset:4616\n"
MF "v[36:39],a[208:211],a[68:71],v[36:39]\n"
LD "v12,s[28:31],s82 offen offset:2048 lds\n"
MF "v[28:31],a[208:211],a[64:67],v[28:31]\n"
MF "v[28:31],a[212:215],a[104:107],v[28:31]\n"
MF "v[20:23],a[212:215],a[100:103],v[20:23]\n"
"s_waitcnt vmcnt(16) lgkmcnt(0)\n"
LD "v13,s[28:31],s82 offen offset:3072 lds\n"
MF "v[20:23],a[208:211],a[60:63],v[20:23]\n"
"s_barrier\n"
MF "v[56:59],a[216:219],a[60:63],v[56:59]\n"
"s_add_u32 s84,s14,20480\n"
TR "a[208:209],v0 offset:16384\n"
MF "v[56:59],a[220:223],a[100:103],v[56:59]\n"
"s_add_u32 m0,s63,22528\n"
TR "a[210:211],v0 offset:16896\n"
MF "v[64:67],a[220:223],a[104:107],v[64:67]\n"
LD "v4,s[24:27],s84 offen lds\n"
MF "v[64:67],a[216:219],a[64:67],v[64:67]\n"
TR "a[212:213],v0 offset:20480\n"
MF "v[72:75],a[216:219],a[68:71],v[72:75]\n"
TR "a[214:215],v0 offset:20992\n"
MF "v[72:75],a[220:223],a[108:111],v[72:75]\n"
MF "v[80:83],a[220:223],a[112:115],v[80:83]\n"
"s_add_u32 s84,s14,29696\n"
MF "v[80:83],a[216:219],a[72:75],v[80:83]\n"
LD "v5,s[24:27],s84 offen offset:1024 lds\n"
MF "v[88:91],a[216:219],a[76:79],v[88:91]\n"
"s_add_u32 m0,s62,81920\n"
MF "v[88:91],a[220:223],a[116:119],v[88:91]\n"
"s_add_u32 s82,s66,2359552\n"
MF "v[92:95],a[204:207],a[116:119],v[92:95]\n"
"s_sub_u32 s15,s82,7864320\n"
TR "a[216:217],v0 offset:16392\n"
MF "v[92:95],a[200:203],a[76:79],v[92:95]\n"
"s_min_u32 s82,s82,s15\n"
TR "a[218:219],v0 offset:16904\n"
MF "v[84:87],a[200:203],a[72:75],v[84:87]\n"
LD "v6,s[28:31],s82 offen lds\n"
MF "v[84:87],a[204:207],a[112:115],v[84:87]\n"
TR "a[220:221],v0 offset:20488\n"
MF "v[76:79],a[204:207],a[108:111],v[76:79]\n"
TR "a[222:223],v0 offset:21000\n"
MF "v[76:79],a[200:203],a[68:71],v[76:79]\n"
MF "v[68:71],a[200:203],a[64:67],v[68:71]\n"
MF "v[68:71],a[204:207],a[104:107],v[68:71]\n"
LD "v11,s[28:31],s82 offen offset:1024 lds\n"
MF "v[60:63],a[204:207],a[100:103],v[60:63]\n"
MF "v[60:63],a[200:203],a[60:63],v[60:63]\n"
"s_waitcnt lgkmcnt(4)\n"
MF "v[96:99],a[208:211],a[60:63],v[96:99]\n"
TR "a[200:201],v1 offset:16384\n"
MF "v[96:99],a[212:215],a[100:103],v[96:99]\n"
TR "a[202:203],v1 offset:16896\n"
MF "v[104:107],a[212:215],a[104:107],v[104:107]\n"
LD "v12,s[28:31],s82 offen offset:2048 lds\n"
MF "v[104:107],a[208:211],a[64:67],v[104:107]\n"
TR "a[204:205],v1 offset:20480\n"
MF "v[112:115],a[208:211],a[68:71],v[112:115]\n"
TR "a[206:207],v1 offset:20992\n"
MF "v[112:115],a[212:215],a[108:111],v[112:115]\n"
MF "v[120:123],a[212:215],a[112:115],v[120:123]\n"
MF "v[120:123],a[208:211],a[72:75],v[120:123]\n"
LD "v13,s[28:31],s82 offen offset:3072 lds\n"
MF "v[128:131],a[208:211],a[76:79],v[128:131]\n"
MF "v[128:131],a[212:215],a[116:119],v[128:131]\n"
"s_waitcnt lgkmcnt(4)\n"
MF "v[132:135],a[220:223],a[116:119],v[132:135]\n"
"s_add_u32 s84,s14,40960\n"
TR "a[208:209],v1 offset:16392\n"
MF "v[132:135],a[216:219],a[76:79],v[132:135]\n"
"s_add_u32 m0,s63,24576\n"
TR "a[210:211],v1 offset:16904\n"
MF "v[124:127],a[216:219],a[72:75],v[124:127]\n"
LD "v4,s[24:27],s84 offen lds\n"
MF "v[124:127],a[220:223],a[112:115],v[124:127]\n"
"s_add_u32 m0,s62,0\n"
TR "a[212:213],v1 offset:20488\n"
MF "v[116:119],a[220:223],a[108:111],v[116:119]\n"
"s_add_u32 s82,s66,3145728\n"
TR "a[214:215],v1 offset:21000\n"
MF "v[116:119],a[216:219],a[68:71],v[116:119]\n"
"s_sub_u32 s15,s82,7864320\n"
MF "v[108:111],a[216:219],a[64:67],v[108:111]\n"
"s_min_u32 s82,s82,s15\n"
MF "v[108:111],a[220:223],a[104:107],v[108:111]\n"
LD "v6,s[28:31],s82 offen lds\n"
MF "v[100:103],a[220:223],a[100:103],v[100:103]\n"
"s_waitcnt vmcnt(18) lgkmcnt(0)\n"
MF "v[100:103],a[216:219],a[60:63],v[100:103]\n"
"s_barrier\n"
MF "v[136:139],a[200:203],a[60:63],v[136:139]\n"
TR "a[216:217],v0 offset:32768\n"
MF "v[136:139],a[204:207],a[100:103],v[136:139]\n"
TR "a[218:219],v0 offset:33280\n"
MF "v[144:147],a[204:207],a[104:107],v[144:147]\n"
"ds_read_b128 a[140:143],v2\n"
MF "v[144:147],a[200:203],a[64:67],v[144:147]\n"
TR "a[220:221],v0 offset:36864\n"
MF "v[152:155],a[200:203],a[68:71],v[152:155]\n"
TR "a[222:223],v0 offset:37376\n"
MF "v[152:155],a[204:207],a[108:111],v[152:155]\n"
LD "v11,s[28:31],s82 offen offset:1024 lds\n"
MF "v[160:163],a[204:207],a[112:115],v[160:163]\n"
"ds_read_b128 a[180:183],v3\n"
MF "v[160:163],a[200:203],a[72:75],v[160:163]\n"
MF "v[168:171],a[200:203],a[76:79],v[168:171]\n"
MF "v[168:171],a[204:207],a[116:119],v[168:171]\n"
LD "v12,s[28:31],s82 offen offset:2048 lds\n"
MF "v[172:175],a[212:215],a[116:119],v[172:175]\n"
TR "a[200:201],v0 offset:32776\n"
MF "v[172:175],a[208:211],a[76:79],v[172:175]\n"
TR "a[202:203],v0 offset:33288\n"
MF "v[164:167],a[208:211],a[72:75],v[164:167]\n"
"ds_read_b128 a[144:147],v2 offset:2048\n"
MF "v[164:167],a[212:215],a[112:115],v[164:167]\n"
TR "a[204:205],v0 offset:36872\n"
MF "v[156:159],a[212:215],a[108:111],v[156:159]\n"
TR "a[206:207],v0 offset:37384\n"
MF "v[156:159],a[208:211],a[68:71],v[156:159]\n"
LD "v13,s[28:31],s82 offen offset:3072 lds\n"
MF "v[148:151],a[208:211],a[64:67],v[148:151]\n"
"ds_read_b128 a[184:187],v3 offset:2048\n"
MF "v[148:151],a[212:215],a[104:107],v[148:151]\n"
"s_add_u32 s14,s81,640\n"
"s_sub_u32 s15,s14,1280\n"
"s_min_u32 s14,s14,s15\n"
MF "v[140:143],a[212:215],a[100:103],v[140:143]\n"
"s_add_u32 s14,s14,s68\n"
"s_add_u32 s84,s14,0\n"
"s_add_u32 m0,s63,40960\n"
MF "v[140:143],a[208:211],a[60:63],v[140:143]\n"
"s_waitcnt lgkmcnt(7)\n"
LD "v4,s[24:27],s84 offen lds\n"
MF "v[176:179],a[216:219],a[60:63],v[176:179]\n"
TR "a[208:209],v1 offset:32768\n"
MF "v[176:179],a[220:223],a[100:103],v[176:179]\n"
TR "a[210:211],v1 offset:33280\n"
MF "v[184:187],a[220:223],a[104:107],v[184:187]\n"
"ds_read_b128 a[148:151],v2 offset:4096\n"
MF "v[184:187],a[216:219],a[64:67],v[184:187]\n"
TR "a[212:213],v1 offset:36864\n"
MF "v[192:195],a[216:219],a[68:71],v[192:195]\n"
"s_add_u32 s84,s14,9216\n"
TR "a[214:215],v1 offset:37376\n"
MF "v[192:195],a[220:223],a[108:111],v[192:195]\n"
LD "v5,s[24:27],s84 offen offset:1024 lds\n"
MF "v[200:203],a[220:223],a[112:115],v[200:203]\n"
"ds_read_b128 a[188:191],v3 offset:4096\n"
MF "v[200:203],a[216:219],a[72:75],v[200:203]\n"
"s_add_u32 m0,s62,16384\n"
MF "v[208:211],a[216:219],a[76:79],v[208:211]\n"
"s_add_u32 s82,s66,3145856\n"
"s_sub_u32 s15,s82,7864320\n"
"s_min_u32 s82,s82,s15\n"
MF "v[208:211],a[220:223],a[116:119],v[208:211]\n"
"s_waitcnt lgkmcnt(7)\n"
LD "v6,s[28:31],s82 offen lds\n"
MF "v[212:215],a[204:207],a[116:119],v[212:215]\n"
TR "a[216:217],v1 offset:32776\n"
MF "v[212:215],a[200:203],a[76:79],v[212:215]\n"
TR "a[218:219],v1 offset:33288\n"
MF "v[204:207],a[200:203],a[72:75],v[204:207]\n"
"ds_read_b128 a[152:155],v2 offset:6144\n"
MF "v[204:207],a[204:207],a[112:115],v[204:207]\n"
TR "a[220:221],v1 offset:36872\n"
MF "v[196:199],a[204:207],a[108:111],v[196:199]\n"
TR "a[222:223],v1 offset:37384\n"
MF "v[196:199],a[200:203],a[68:71],v[196:199]\n"
LD "v11,s[28:31],s82 offen offset:1024 lds\n"
MF "v[188:191],a[200:203],a[64:67],v[188:191]\n"
"v_add_u32 v0,0xc000,v0\n"
MF "v[188:191],a[204:207],a[104:107],v[188:191]\n"
"v_add_u32 v1,0xc000,v1\n"
MF "v[180:183],a[204:207],a[100:103],v[180:183]\n"
"s_waitcnt vmcnt(21) lgkmcnt(0)\n"
LD "v12,s[28:31],s82 offen offset:2048 lds\n"
MF "v[180:183],a[200:203],a[60:63],v[180:183]\n"
"s_barrier\n"
MF "v[216:219],a[208:211],a[60:63],v[216:219]\n"
TR "a[200:201],v0\n"
MF "v[216:219],a[212:215],a[100:103],v[216:219]\n"
TR "a[202:203],v0 offset:512\n"
MF "v[224:227],a[212:215],a[104:107],v[224:227]\n"
"ds_read_b128 a[192:195],v3 offset:6144\n"
MF "v[224:227],a[208:211],a[64:67],v[224:227]\n"
TR "a[204:205],v0 offset:4096\n"
MF "v[232:235],a[208:211],a[68:71],v[232:235]\n"
TR "a[206:207],v0 offset:4608\n"
MF "v[232:235],a[212:215],a[108:111],v[232:235]\n"
LD "v13,s[28:31],s82 offen offset:3072 lds\n"
MF "v[240:243],a[212:215],a[112:115],v[240:243]\n"
"ds_read_b128 a[156:159],v2 offset:8192\n"
MF "v[240:243],a[208:211],a[72:75],v[240:243]\n"
"s_add_u32 s84,s14,20480\n"
"ds_read_b128 a[196:199],v3 offset:8192\n"
MF "v[248:251],a[208:211],a[76:79],v[248:251]\n"
"s_add_u32 m0,s63,43008\n"
MF "v[248:251],a[212:215],a[116:119],v[248:251]\n"
LD "v4,s[24:27],s84 offen lds\n"
MF "v[252:255],a[220:223],a[116:119],v[252:255]\n"
TR "a[208:209],v0 offset:8\n"
MF "v[252:255],a[216:219],a[76:79],v[252:255]\n"
TR "a[210:211],v0 offset:520\n"
MF "v[244:247],a[216:219],a[72:75],v[244:247]\n"
MF "v[244:247],a[220:223],a[112:115],v[244:247]\n"
TR "a[212:213],v0 offset:4104\n"
MF "v[236:239],a[220:223],a[108:111],v[236:239]\n"
"s_add_u32 s84,s14,29696\n"
TR "a[214:215],v0 offset:4616\n"
MF "v[236:239],a[216:219],a[68:71],v[236:239]\n"
LD "v5,s[24:27],s84 offen offset:1024 lds\n"
MF "v[228:231],a[216:219],a[64:67],v[228:231]\n"
MF "v[228:231],a[220:223],a[104:107],v[228:231]\n"
"s_add_u32 m0,s62,32768\n"
MF "v[220:223],a[220:223],a[100:103],v[220:223]\n"
"s_add_u32 s82,s66,3145984\n"
"s_sub_u32 s15,s82,7864320\n"
"s_min_u32 s82,s82,s15\n"
MF "v[220:223],a[216:219],a[60:63],v[220:223]\n"
"s_waitcnt lgkmcnt(4)\n"
LD "v6,s[28:31],s82 offen lds\n"
MF "v[16:19],a[200:203],a[140:143],v[16:19]\n"
TR "a[216:217],v1\n"
MF "v[16:19],a[204:207],a[180:183],v[16:19]\n"
TR "a[218:219],v1 offset:512\n"
MF "v[24:27],a[204:207],a[184:187],v[24:27]\n"
MF "v[24:27],a[200:203],a[144:147],v[24:27]\n"
TR "a[220:221],v1 offset:4096\n"
MF "v[32:35],a[200:203],a[148:151],v[32:35]\n"
TR "a[222:223],v1 offset:4608\n"
MF "v[32:35],a[204:207],a[188:191],v[32:35]\n"
LD "v11,s[28:31],s82 offen offset:1024 lds\n"
MF "v[40:43],a[204:207],a[192:195],v[40:43]\n"
MF "v[40:43],a[200:203],a[152:155],v[40:43]\n"
MF "v[48:51],a[200:203],a[156:159],v[48:51]\n"
MF "v[48:51],a[204:207],a[196:199],v[48:51]\n"
"s_waitcnt lgkmcnt(4)\n"
LD "v12,s[28:31],s82 offen offset:2048 lds\n"
MF "v[52:55],a[212:215],a[196:199],v[52:55]\n"
TR "a[200:201],v1 offset:8\n"
MF "v[52:55],a[208:211],a[156:159],v[52:55]\n"
TR "a[202:203],v1 offset:520\n"
MF "v[44:47],a[208:211],a[152:155],v[44:47]\n"
MF "v[44:47],a[212:215],a[192:195],v[44:47]\n"
TR "a[204:205],v1 offset:4104\n"
MF "v[36:39],a[212:215],a[188:191],v[36:39]\n"
".Lpg2a:\n"
"s_cbranch_execz .Lisr1\n"
TR "a[206:207],v1 offset:4616\n"
MF "v[36:39],a[208:211],a[148:151],v[36:39]\n"
LD "v13,s[28:31],s82 offen offset:3072 lds\n"
MF "v[28:31],a[208:211],a[144:147],v[28:31]\n"
"s_add_u32 s84,s14,40960\n"
MF "v[28:31],a[212:215],a[184:187],v[28:31]\n"
"s_add_u32 m0,s63,45056\n"
MF "v[20:23],a[212:215],a[180:183],v[20:23]\n"
"s_waitcnt vmcnt(23) lgkmcnt(0)\n"
LD "v4,s[24:27],s84 offen lds\n"
MF "v[20:23],a[208:211],a[140:143],v[20:23]\n"
"s_barrier\n"
AW "a253,v10\n"
"v_add_u32 v10,1,v10\n"
"s_bfe_u32 s58,s22,0x30014\n"
"s_lshl_b32 s58,s58,2\n"
"s_add_u32 s58,s58,s56\n"
"s_lshl_b32 s58,s58,6\n"
"s_add_u32 s58,s58,s86\n"
"v_readlane_b32 s76,v10,s78\n"
"buffer_store_dword v10,v14,s[40:43],s58 offen offset:128 sc1\n"
".Lhs5:\n"
MF "v[56:59],a[216:219],a[140:143],v[56:59]\n"
"s_add_u32 m0,s62,49152\n"
"s_add_u32 s82,s66,3932160\n"
TR "a[208:209],v0 offset:16384\n"
MF "v[56:59],a[220:223],a[180:183],v[56:59]\n"
"s_sub_u32 s15,s82,7864320\n"
"s_min_u32 s82,s82,s15\n"
TR "a[210:211],v0 offset:16896\n"
MF "v[64:67],a[220:223],a[184:187],v[64:67]\n"
LD "v6,s[28:31],s82 offen lds\n"
MF "v[64:67],a[216:219],a[144:147],v[64:67]\n"
TR "a[212:213],v0 offset:20480\n"
MF "v[72:75],a[216:219],a[148:151],v[72:75]\n"
TR "a[214:215],v0 offset:20992\n"
MF "v[72:75],a[220:223],a[188:191],v[72:75]\n"
"buffer_load_dword a249,v15,s[48:51],s60 offen offset:1024\n"
MF "v[80:83],a[220:223],a[192:195],v[80:83]\n"
"s_bfe_u32 s59,s23,0x30003\n"
MF "v[80:83],a[216:219],a[152:155],v[80:83]\n"
LD "v11,s[28:31],s82 offen offset:1024 lds\n"
MF "v[88:91],a[216:219],a[156:159],v[88:91]\n"
"s_add_u32 s59,s59,1\n"
MF "v[88:91],a[220:223],a[196:199],v[88:91]\n"
"s_lshl_b32 s59,s59,7\n"
MF "v[92:95],a[204:207],a[196:199],v[92:95]\n"
"s_and_b32 s59,s59,768\n"
TR "a[216:217],v0 offset:16392\n"
MF "v[92:95],a[200:203],a[156:159],v[92:95]\n"
"s_add_u32 s59,s59,s60\n"
TR "a[218:219],v0 offset:16904\n"
MF "v[84:87],a[200:203],a[152:155],v[84:87]\n"
LD "v12,s[28:31],s82 offen offset:2048 lds\n"
MF "v[84:87],a[204:207],a[192:195],v[84:87]\n"
TR "a[220:221],v0 offset:20488\n"
MF "v[76:79],a[204:207],a[188:191],v[76:79]\n"
TR "a[222:223],v0 offset:21000\n"
MF "v[76:79],a[200:203],a[148:151],v[76:79]\n"
MF "v[68:71],a[200:203],a[144:147],v[68:71]\n"
MF "v[68:71],a[204:207],a[184:187],v[68:71]\n"
LD "v13,s[28:31],s82 offen offset:3072 lds\n"
MF "v[60:63],a[204:207],a[180:183],v[60:63]\n"
MF "v[60:63],a[200:203],a[140:143],v[60:63]\n"
"s_add_u32 s14,s81,768\n"
"s_sub_u32 s15,s14,1280\n"
"s_waitcnt lgkmcnt(4)\n"
MF "v[96:99],a[208:211],a[140:143],v[96:99]\n"
"s_min_u32 s14,s14,s15\n"
"s_add_u32 s14,s14,s68\n"
TR "a[200:201],v1 offset:16384\n"
MF "v[96:99],a[212:215],a[180:183],v[96:99]\n"
"s_add_u32 s84,s14,0\n"
"s_add_u32 m0,s63,0\n"
TR "a[202:203],v1 offset:16896\n"
MF "v[104:107],a[212:215],a[184:187],v[104:107]\n"
LD "v4,s[24:27],s84 offen lds\n"
MF "v[104:107],a[208:211],a[144:147],v[104:107]\n"
TR "a[204:205],v1 offset:20480\n"
MF "v[112:115],a[208:211],a[148:151],v[112:115]\n"
TR "a[206:207],v1 offset:20992\n"
MF "v[112:115],a[212:215],a[188:191],v[112:115]\n"
MF "v[120:123],a[212:215],a[192:195],v[120:123]\n"
"s_add_u32 s84,s14,9216\n"
MF "v[120:123],a[208:211],a[152:155],v[120:123]\n"
LD "v5,s[24:27],s84 offen offset:1024 lds\n"
MF "v[128:131],a[208:211],a[156:159],v[128:131]\n"
MF "v[128:131],a[212:215],a[196:199],v[128:131]\n"
"s_waitcnt lgkmcnt(4)\n"
MF "v[132:135],a[220:223],a[196:199],v[132:135]\n"
TR "a[208:209],v1 offset:16392\n"
MF "v[132:135],a[216:219],a[156:159],v[132:135]\n"
TR "a[210:211],v1 offset:16904\n"
MF "v[124:127],a[216:219],a[152:155],v[124:127]\n"
MF "v[124:127],a[220:223],a[192:195],v[124:127]\n"
TR "a[212:213],v1 offset:20488\n"
MF "v[116:119],a[220:223],a[188:191],v[116:119]\n"
TR "a[214:215],v1 offset:21000\n"
MF "v[116:119],a[216:219],a[148:151],v[116:119]\n"
MF "v[108:111],a[216:219],a[144:147],v[108:111]\n"
MF "v[108:111],a[220:223],a[184:187],v[108:111]\n"
MF "v[100:103],a[220:223],a[180:183],v[100:103]\n"
"s_waitcnt vmcnt(24) lgkmcnt(0)\n"
MF "v[100:103],a[216:219],a[140:143],v[100:103]\n"
"s_barrier\n"
MF "v[136:139],a[200:203],a[140:143],v[136:139]\n"
TR "a[216:217],v0 offset:32768\n"
MF "v[136:139],a[204:207],a[180:183],v[136:139]\n"
"s_add_u32 m0,s62,65536\n"
TR "a[218:219],v0 offset:33280\n"
MF "v[144:147],a[204:207],a[184:187],v[144:147]\n"
"s_add_u32 s82,s66,3932288\n"
"ds_read_b128 a[60:63],v2 offset:20480\n"
MF "v[144:147],a[200:203],a[144:147],v[144:147]\n"
"s_sub_u32 s15,s82,7864320\n"
TR "a[220:221],v0 offset:36864\n"
MF "v[152:155],a[200:203],a[148:151],v[152:155]\n"
"s_min_u32 s82,s82,s15\n"
TR "a[222:223],v0 offset:37376\n"
MF "v[152:155],a[204:207],a[188:191],v[152:155]\n"
LD "v6,s[28:31],s82 offen lds\n"
MF "v[160:163],a[204:207],a[192:195],v[160:163]\n"
"ds_read_b128 a[100:103],v3 offset:20480\n"
MF "v[160:163],a[200:203],a[152:155],v[160:163]\n"
MF "v[168:171],a[200:203],a[156:159],v[168:171]\n"
MF "v[168:171],a[204:207],a[196:199],v[168:171]\n"
LD "v11,s[28:31],s82 offen offset:1024 lds\n"
MF "v[172:175],a[212:215],a[196:199],v[172:175]\n"
TR "a[200:201],v0 offset:32776\n"
MF "v[172:175],a[208:211],a[156:159],v[172:175]\n"
TR "a[202:203],v0 offset:33288\n"
MF "v[164:167],a[208:211],a[152:155],v[164:167]\n"
"ds_read_b128 a[64:67],v2 offset:22528\n"
MF "v[164:167],a[212:215],a[192:195],v[164:167]\n"
TR "a[204:205],v0 offset:36872\n"
MF "v[156:159],a[212:215],a[188:191],v[156:159]\n"
TR "a[206:207],v0 offset:37384\n"
MF "v[156:159],a[208:211],a[148:151],v[156:159]\n"
LD "v12,s[28:31],s82 offen offset:2048 lds\n"
MF "v[148:151],a[208:211],a[144:147],v[148:151]\n"
"ds_read_b128 a[104:107],v3 offset:22528\n"
MF "v[148:151],a[212:215],a[184:187],v[148:151]\n"
MF "v[140:143],a[212:215],a[180:183],v[140:143]\n"
MF "v[140:143],a[208:211],a[140:143],v[140:143]\n"
"s_waitcnt lgkmcnt(7)\n"
LD "v13,s[28:31],s82 offen offset:3072 lds\n"
MF "v[176:179],a[216:219],a[140:143],v[176:179]\n"
TR "a[208:209],v1 offset:32768\n"
MF "v[176:179],a[220:223],a[180:183],v[176:179]\n"
TR "a[210:211],v1 offset:33280\n"
MF "v[184:187],a[220:223],a[184:187],v[184:187]\n"
"ds_read_b128 a[68:71],v2 offset:24576\n"
MF "v[184:187],a[216:219],a[144:147],v[184:187]\n"
"s_add_u32 s84,s14,20480\n"
TR "a[212:213],v1 offset:36864\n"
MF "v[192:195],a[216:219],a[148:151],v[192:195]\n"
"s_add_u32 m0,s63,2048\n"
TR "a[214:215],v1 offset:37376\n"
MF "v[192:195],a[220:223],a[188:191],v[192:195]\n"
LD "v4,s[24:27],s84 offen lds\n"
MF "v[200:203],a[220:223],a[192:195],v[200:203]\n"
"ds_read_b128 a[108:111],v3 offset:24576\n"
MF "v[200:203],a[216:219],a[152:155],v[200:203]\n"
MF "v[208:211],a[216:219],a[156:159],v[208:211]\n"
"s_add_u32 s84,s14,29696\n"
MF "v[208:211],a[220:223],a[196:199],v[208:211]\n"
"s_waitcnt lgkmcnt(7)\n"
LD "v5,s[24:27],s84 offen offset:1024 lds\n"
MF "v[212:215],a[204:207],a[196:199],v[212:215]\n"
TR "a[216:217],v1 offset:32776\n"
MF "v[212:215],a[200:203],a[156:159],v[212:215]\n"
TR "a[218:219],v1 offset:33288\n"
MF "v[204:207],a[200:203],a[152:155],v[204:207]\n"
"ds_read_b128 a[72:75],v2 offset:26624\n"
MF "v[204:207],a[204:207],a[192:195],v[204:207]\n"
TR "a[220:221],v1 offset:36872\n"
MF "v[196:199],a[204:207],a[188:191],v[196:199]\n"
TR "a[222:223],v1 offset:37384\n"
MF "v[196:199],a[200:203],a[148:151],v[196:199]\n"
MF "v[188:191],a[200:203],a[144:147],v[188:191]\n"
"v_add_u32 v0,0xffff4000,v0\n"
MF "v[188:191],a[204:207],a[184:187],v[188:191]\n"
"v_add_u32 v1,0xffff4000,v1\n"
MF "v[180:183],a[204:207],a[180:183],v[180:183]\n"
"s_waitcnt vmcnt(26) lgkmcnt(0)\n"
MF "v[180:183],a[200:203],a[140:143],v[180:183]\n"
"s_barrier\n"
MF "v[216:219],a[208:211],a[140:143],v[216:219]\n"
TR "a[200:201],v0\n"
MF "v[216:219],a[212:215],a[180:183],v[216:219]\n"
"s_add_u32 m0,s62,81920\n"
TR "a[202:203],v0 offset:512\n"
MF "v[224:227],a[212:215],a[184:187],v[224:227]\n"
"s_add_u32 s82,s66,3932416\n"
"ds_read_b128 a[112:115],v3 offset:26624\n"
MF "v[224:227],a[208:211],a[144:147],v[224:227]\n"
"s_sub_u32 s15,s82,7864320\n"
TR "a[204:205],v0 offset:4096\n"
MF "v[232:235],a[208:211],a[148:151],v[232:235]\n"
"s_min_u32 s82,s82,s15\n"
TR "a[206:207],v0 offset:4608\n"
MF "v[232:235],a[212:215],a[188:191],v[232:235]\n"
LD "v6,s[28:31],s82 offen lds\n"
MF "v[240:243],a[212:215],a[192:195],v[240:243]\n"
"ds_read_b128 a[76:79],v2 offset:28672\n"
MF "v[240:243],a[208:211],a[152:155],v[240:243]\n"
"ds_read_b128 a[116:119],v3 offset:28672\n"
MF "v[248:251],a[208:211],a[156:159],v[248:251]\n"
MF "v[248:251],a[212:215],a[196:199],v[248:251]\n"
LD "v11,s[28:31],s82 offen offset:1024 lds\n"
MF "v[252:255],a[220:223],a[196:199],v[252:255]\n"
TR "a[208:209],v0 offset:8\n"
MF "v[252:255],a[216:219],a[156:159],v[252:255]\n"
TR "a[210:211],v0 offset:520\n"
MF "v[244:247],a[216:219],a[152:155],v[244:247]\n"
MF "v[244:247],a[220:223],a[192:195],v[244:247]\n"
TR "a[212:213],v0 offset:4104\n"
MF "v[236:239],a[220:223],a[188:191],v[236:239]\n"
TR "a[214:215],v0 offset:4616\n"
MF "v[236:239],a[216:219],a[148:151],v[236:239]\n"
LD "v12,s[28:31],s82 offen offset:2048 lds\n"
MF "v[228:231],a[216:219],a[144:147],v[228:231]\n"
MF "v[228:231],a[220:223],a[184:187],v[228:231]\n"
MF "v[220:223],a[220:223],a[180:183],v[220:223]\n"
MF "v[220:223],a[216:219],a[140:143],v[220:223]\n"
"s_waitcnt lgkmcnt(4)\n"
LD "v13,s[28:31],s82 offen offset:3072 lds\n"
MF "v[16:19],a[200:203],a[60:63],v[16:19]\n"
TR "a[216:217],v1\n"
MF "v[16:19],a[204:207],a[100:103],v[16:19]\n"
TR "a[218:219],v1 offset:512\n"
MF "v[24:27],a[204:207],a[104:107],v[24:27]\n"
MF "v[24:27],a[200:203],a[64:67],v[24:27]\n"
"s_add_u32 s84,s14,40960\n"
TR "a[220:221],v1 offset:4096\n"
MF "v[32:35],a[200:203],a[68:71],v[32:35]\n"
"s_add_u32 m0,s63,4096\n"
TR "a[222:223],v1 offset:4608\n"
MF "v[32:35],a[204:207],a[108:111],v[32:35]\n"
LD "v4,s[24:27],s84 offen lds\n"
MF "v[40:43],a[204:207],a[112:115],v[40:43]\n"
MF "v[40:43],a[200:203],a[72:75],v[40:43]\n"
MF "v[48:51],a[200:203],a[76:79],v[48:51]\n"
MF "v[48:51],a[204:207],a[116:119],v[48:51]\n"
"s_waitcnt lgkmcnt(4)\n"
MF "v[52:55],a[212:215],a[116:119],v[52:55]\n"
TR "a[200:201],v1 offset:8\n"
MF "v[52:55],a[208:211],a[76:79],v[52:55]\n"
TR "a[202:203],v1 offset:520\n"
MF "v[44:47],a[208:211],a[72:75],v[44:47]\n"
MF "v[44:47],a[212:215],a[112:115],v[44:47]\n"
TR "a[204:205],v1 offset:4104\n"
MF "v[36:39],a[212:215],a[108:111],v[36:39]\n"
TR "a[206:207],v1 offset:4616\n"
MF "v[36:39],a[208:211],a[68:71],v[36:39]\n"
MF "v[28:31],a[208:211],a[64:67],v[28:31]\n"
MF "v[28:31],a[212:215],a[104:107],v[28:31]\n"
MF "v[20:23],a[212:215],a[100:103],v[20:23]\n"
"s_waitcnt vmcnt(25) lgkmcnt(0)\n"
MF "v[20:23],a[208:211],a[60:63],v[20:23]\n"
"s_barrier\n"
MF "v[56:59],a[216:219],a[60:63],v[56:59]\n"
"s_add_u32 m0,s62,0\n"
"s_add_u32 s82,s66,4718592\n"
TR "a[208:209],v0 offset:16384\n"
MF "v[56:59],a[220:223],a[100:103],v[56:59]\n"
"s_sub_u32 s15,s82,7864320\n"
"s_min_u32 s82,s82,s15\n"
TR "a[210:211],v0 offset:16896\n"
MF "v[64:67],a[220:223],a[104:107],v[64:67]\n"
LD "v6,s[28:31],s82 offen lds\n"
MF "v[64:67],a[216:219],a[64:67],v[64:67]\n"
TR "a[212:213],v0 offset:20480\n"
MF "v[72:75],a[216:219],a[68:71],v[72:75]\n"
TR "a[214:215],v0 offset:20992\n"
MF "v[72:75],a[220:223],a[108:111],v[72:75]\n"
"buffer_load_dword a249,v15,s[48:51],s59 offen\n"
MF "v[80:83],a[220:223],a[112:115],v[80:83]\n"
"s_bfe_u32 s59,s23,0x30003\n"
MF "v[80:83],a[216:219],a[72:75],v[80:83]\n"
LD "v11,s[28:31],s82 offen offset:1024 lds\n"
MF "v[88:91],a[216:219],a[76:79],v[88:91]\n"
"s_add_u32 s59,s59,3\n"
MF "v[88:91],a[220:223],a[116:119],v[88:91]\n"
"s_lshl_b32 s59,s59,7\n"
MF "v[92:95],a[204:207],a[116:119],v[92:95]\n"
"s_and_b32 s59,s59,768\n"
TR "a[216:217],v0 offset:16392\n"
MF "v[92:95],a[200:203],a[76:79],v[92:95]\n"
"s_add_u32 s59,s59,s60\n"
TR "a[218:219],v0 offset:16904\n"
MF "v[84:87],a[200:203],a[72:75],v[84:87]\n"
LD "v12,s[28:31],s82 offen offset:2048 lds\n"
MF "v[84:87],a[204:207],a[112:115],v[84:87]\n"
TR "a[220:221],v0 offset:20488\n"
MF "v[76:79],a[204:207],a[108:111],v[76:79]\n"
TR "a[222:223],v0 offset:21000\n"
MF "v[76:79],a[200:203],a[68:71],v[76:79]\n"
MF "v[68:71],a[200:203],a[64:67],v[68:71]\n"
MF "v[68:71],a[204:207],a[104:107],v[68:71]\n"
LD "v13,s[28:31],s82 offen offset:3072 lds\n"
MF "v[60:63],a[204:207],a[100:103],v[60:63]\n"
MF "v[60:63],a[200:203],a[60:63],v[60:63]\n"
"s_add_u32 s14,s81,896\n"
"s_sub_u32 s15,s14,1280\n"
"s_waitcnt lgkmcnt(4)\n"
MF "v[96:99],a[208:211],a[60:63],v[96:99]\n"
"s_min_u32 s14,s14,s15\n"
"s_add_u32 s14,s14,s68\n"
TR "a[200:201],v1 offset:16384\n"
MF "v[96:99],a[212:215],a[100:103],v[96:99]\n"
"s_add_u32 s84,s14,0\n"
"s_add_u32 m0,s63,20480\n"
TR "a[202:203],v1 offset:16896\n"
MF "v[104:107],a[212:215],a[104:107],v[104:107]\n"
LD "v4,s[24:27],s84 offen lds\n"
MF "v[104:107],a[208:211],a[64:67],v[104:107]\n"
TR "a[204:205],v1 offset:20480\n"
MF "v[112:115],a[208:211],a[68:71],v[112:115]\n"
TR "a[206:207],v1 offset:20992\n"
MF "v[112:115],a[212:215],a[108:111],v[112:115]\n"
MF "v[120:123],a[212:215],a[112:115],v[120:123]\n"
"s_add_u32 s84,s14,9216\n"
MF "v[120:123],a[208:211],a[72:75],v[120:123]\n"
LD "v5,s[24:27],s84 offen offset:1024 lds\n"
MF "v[128:131],a[208:211],a[76:79],v[128:131]\n"
MF "v[128:131],a[212:215],a[116:119],v[128:131]\n"
"s_waitcnt lgkmcnt(4)\n"
MF "v[132:135],a[220:223],a[116:119],v[132:135]\n"
TR "a[208:209],v1 offset:16392\n"
MF "v[132:135],a[216:219],a[76:79],v[132:135]\n"
TR "a[210:211],v1 offset:16904\n"
MF "v[124:127],a[216:219],a[72:75],v[124:127]\n"
MF "v[124:127],a[220:223],a[112:115],v[124:127]\n"
TR "a[212:213],v1 offset:20488\n"
MF "v[116:119],a[220:223],a[108:111],v[116:119]\n"
TR "a[214:215],v1 offset:21000\n"
MF "v[116:119],a[216:219],a[68:71],v[116:119]\n"
MF "v[108:111],a[216:219],a[64:67],v[108:111]\n"
MF "v[108:111],a[220:223],a[104:107],v[108:111]\n"
MF "v[100:103],a[220:223],a[100:103],v[100:103]\n"
"s_waitcnt vmcnt(25) lgkmcnt(0)\n"
MF "v[100:103],a[216:219],a[60:63],v[100:103]\n"
"s_barrier\n"
MF "v[136:139],a[200:203],a[60:63],v[136:139]\n"
TR "a[216:217],v0 offset:32768\n"
MF "v[136:139],a[204:207],a[100:103],v[136:139]\n"
"s_add_u32 m0,s62,16384\n"
TR "a[218:219],v0 offset:33280\n"
MF "v[144:147],a[204:207],a[104:107],v[144:147]\n"
"s_add_u32 s82,s66,4718720\n"
"ds_read_b128 a[140:143],v2 offset:40960\n"
MF "v[144:147],a[200:203],a[64:67],v[144:147]\n"
"s_sub_u32 s15,s82,7864320\n"
TR "a[220:221],v0 offset:36864\n"
MF "v[152:155],a[200:203],a[68:71],v[152:155]\n"
"s_min_u32 s82,s82,s15\n"
TR "a[222:223],v0 offset:37376\n"
MF "v[152:155],a[204:207],a[108:111],v[152:155]\n"
LD "v6,s[28:31],s82 offen lds\n"
MF "v[160:163],a[204:207],a[112:115],v[160:163]\n"
"ds_read_b128 a[180:183],v3 offset:40960\n"
MF "v[160:163],a[200:203],a[72:75],v[160:163]\n"
MF "v[168:171],a[200:203],a[76:79],v[168:171]\n"
MF "v[168:171],a[204:207],a[116:119],v[168:171]\n"
LD "v11,s[28:31],s82 offen offset:1024 lds\n"
MF "v[172:175],a[212:215],a[116:119],v[172:175]\n"
TR "a[200:201],v0 offset:32776\n"
MF "v[172:175],a[208:211],a[76:79],v[172:175]\n"
TR "a[202:203],v0 offset:33288\n"
MF "v[164:167],a[208:211],a[72:75],v[164:167]\n"
"ds_read_b128 a[144:147],v2 offset:43008\n"
MF "v[164:167],a[212:215],a[112:115],v[164:167]\n"
TR "a[204:205],v0 offset:36872\n"
MF "v[156:159],a[212:215],a[108:111],v[156:159]\n"
TR "a[206:207],v0 offset:37384\n"
MF "v[156:159],a[208:211],a[68:71],v[156:159]\n"
LD "v12,s[28:31],s82 offen offset:2048 lds\n"
MF "v[148:151],a[208:211],a[64:67],v[148:151]\n"
"ds_read_b128 a[184:187],v3 offset:43008\n"
MF "v[148:151],a[212:215],a[104:107],v[148:151]\n"
MF "v[140:143],a[212:215],a[100:103],v[140:143]\n"
MF "v[140:143],a[208:211],a[60:63],v[140:143]\n"
"s_waitcnt lgkmcnt(7)\n"
LD "v13,s[28:31],s82 offen offset:3072 lds\n"
MF "v[176:179],a[216:219],a[60:63],v[176:179]\n"
TR "a[208:209],v1 offset:32768\n"
MF "v[176:179],a[220:223],a[100:103],v[176:179]\n"
TR "a[210:211],v1 offset:33280\n"
MF "v[184:187],a[220:223],a[104:107],v[184:187]\n"
"ds_read_b128 a[148:151],v2 offset:45056\n"
MF "v[184:187],a[216:219],a[64:67],v[184:187]\n"
"s_add_u32 s84,s14,20480\n"
TR "a[212:213],v1 offset:36864\n"
MF "v[192:195],a[216:219],a[68:71],v[192:195]\n"
"s_add_u32 m0,s63,22528\n"
TR "a[214:215],v1 offset:37376\n"
MF "v[192:195],a[220:223],a[108:111],v[192:195]\n"
LD "v4,s[24:27],s84 offen lds\n"
MF "v[200:203],a[220:223],a[112:115],v[200:203]\n"
"ds_read_b128 a[188:191],v3 offset:45056\n"
MF "v[200:203],a[216:219],a[72:75],v[200:203]\n"
MF "v[208:211],a[216:219],a[76:79],v[208:211]\n"
"s_add_u32 s84,s14,29696\n"
MF "v[208:211],a[220:223],a[116:119],v[208:211]\n"
"s_waitcnt lgkmcnt(7)\n"
LD "v5,s[24:27],s84 offen offset:1024 lds\n"
MF "v[212:215],a[204:207],a[116:119],v[212:215]\n"
TR "a[216:217],v1 offset:32776\n"
MF "v[212:215],a[200:203],a[76:79],v[212:215]\n"
TR "a[218:219],v1 offset:33288\n"
MF "v[204:207],a[200:203],a[72:75],v[204:207]\n"
"ds_read_b128 a[152:155],v2 offset:47104\n"
MF "v[204:207],a[204:207],a[112:115],v[204:207]\n"
TR "a[220:221],v1 offset:36872\n"
MF "v[196:199],a[204:207],a[108:111],v[196:199]\n"
TR "a[222:223],v1 offset:37384\n"
MF "v[196:199],a[200:203],a[68:71],v[196:199]\n"
MF "v[188:191],a[200:203],a[64:67],v[188:191]\n"
"v_add_u32 v0,0xc000,v0\n"
MF "v[188:191],a[204:207],a[104:107],v[188:191]\n"
"v_add_u32 v1,0xc000,v1\n"
MF "v[180:183],a[204:207],a[100:103],v[180:183]\n"
"s_waitcnt vmcnt(26) lgkmcnt(0)\n"
MF "v[180:183],a[200:203],a[60:63],v[180:183]\n"
"s_barrier\n"
MF "v[216:219],a[208:211],a[60:63],v[216:219]\n"
TR "a[200:201],v0\n"
MF "v[216:219],a[212:215],a[100:103],v[216:219]\n"
"s_add_u32 m0,s62,32768\n"
TR "a[202:203],v0 offset:512\n"
MF "v[224:227],a[212:215],a[104:107],v[224:227]\n"
"s_add_u32 s82,s66,4718848\n"
"ds_read_b128 a[192:195],v3 offset:47104\n"
MF "v[224:227],a[208:211],a[64:67],v[224:227]\n"
"s_sub_u32 s15,s82,7864320\n"
TR "a[204:205],v0 offset:4096\n"
MF "v[232:235],a[208:211],a[68:71],v[232:235]\n"
"s_min_u32 s82,s82,s15\n"
TR "a[206:207],v0 offset:4608\n"
MF "v[232:235],a[212:215],a[108:111],v[232:235]\n"
LD "v6,s[28:31],s82 offen lds\n"
MF "v[240:243],a[212:215],a[112:115],v[240:243]\n"
"ds_read_b128 a[156:159],v2 offset:49152\n"
MF "v[240:243],a[208:211],a[72:75],v[240:243]\n"
"ds_read_b128 a[196:199],v3 offset:49152\n"
MF "v[248:251],a[208:211],a[76:79],v[248:251]\n"
MF "v[248:251],a[212:215],a[116:119],v[248:251]\n"
LD "v11,s[28:31],s82 offen offset:1024 lds\n"
MF "v[252:255],a[220:223],a[116:119],v[252:255]\n"
TR "a[208:209],v0 offset:8\n"
MF "v[252:255],a[216:219],a[76:79],v[252:255]\n"
TR "a[210:211],v0 offset:520\n"
MF "v[244:247],a[216:219],a[72:75],v[244:247]\n"
MF "v[244:247],a[220:223],a[112:115],v[244:247]\n"
TR "a[212:213],v0 offset:4104\n"
MF "v[236:239],a[220:223],a[108:111],v[236:239]\n"
TR "a[214:215],v0 offset:4616\n"
MF "v[236:239],a[216:219],a[68:71],v[236:239]\n"
LD "v12,s[28:31],s82 offen offset:2048 lds\n"
MF "v[228:231],a[216:219],a[64:67],v[228:231]\n"
MF "v[228:231],a[220:223],a[104:107],v[228:231]\n"
MF "v[220:223],a[220:223],a[100:103],v[220:223]\n"
MF "v[220:223],a[216:219],a[60:63],v[220:223]\n"
"s_waitcnt lgkmcnt(4)\n"
LD "v13,s[28:31],s82 offen offset:3072 lds\n"
MF "v[16:19],a[200:203],a[140:143],v[16:19]\n"
TR "a[216:217],v1\n"
MF "v[16:19],a[204:207],a[180:183],v[16:19]\n"
TR "a[218:219],v1 offset:512\n"
MF "v[24:27],a[204:207],a[184:187],v[24:27]\n"
MF "v[24:27],a[200:203],a[144:147],v[24:27]\n"
"s_add_u32 s84,s14,40960\n"
TR "a[220:221],v1 offset:4096\n"
MF "v[32:35],a[200:203],a[148:151],v[32:35]\n"
"s_add_u32 m0,s63,24576\n"
TR "a[222:223],v1 offset:4608\n"
MF "v[32:35],a[204:207],a[188:191],v[32:35]\n"
LD "v4,s[24:27],s84 offen lds\n"
MF "v[40:43],a[204:207],a[192:195],v[40:43]\n"
MF "v[40:43],a[200:203],a[152:155],v[40:43]\n"
MF "v[48:51],a[200:203],a[156:159],v[48:51]\n"
MF "v[48:51],a[204:207],a[196:199],v[48:51]\n"
"s_waitcnt lgkmcnt(4)\n"
MF "v[52:55],a[212:215],a[196:199],v[52:55]\n"
TR "a[200:201],v1 offset:8\n"
MF "v[52:55],a[208:211],a[156:159],v[52:55]\n"
TR "a[202:203],v1 offset:520\n"
MF "v[44:47],a[208:211],a[152:155],v[44:47]\n"
MF "v[44:47],a[212:215],a[192:195],v[44:47]\n"
TR "a[204:205],v1 offset:4104\n"
MF "v[36:39],a[212:215],a[188:191],v[36:39]\n"
TR "a[206:207],v1 offset:4616\n"
MF "v[36:39],a[208:211],a[148:151],v[36:39]\n"
MF "v[28:31],a[208:211],a[144:147],v[28:31]\n"
MF "v[28:31],a[212:215],a[184:187],v[28:31]\n"
MF "v[20:23],a[212:215],a[180:183],v[20:23]\n"
"s_waitcnt vmcnt(25) lgkmcnt(0)\n"
MF "v[20:23],a[208:211],a[140:143],v[20:23]\n"
"s_barrier\n"
MF "v[56:59],a[216:219],a[140:143],v[56:59]\n"
"s_add_u32 m0,s62,49152\n"
"s_add_u32 s82,s66,5505024\n"
TR "a[208:209],v0 offset:16384\n"
MF "v[56:59],a[220:223],a[180:183],v[56:59]\n"
"s_sub_u32 s15,s82,7864320\n"
"s_min_u32 s82,s82,s15\n"
TR "a[210:211],v0 offset:16896\n"
MF "v[64:67],a[220:223],a[184:187],v[64:67]\n"
LD "v6,s[28:31],s82 offen lds\n"
MF "v[64:67],a[216:219],a[144:147],v[64:67]\n"
TR "a[212:213],v0 offset:20480\n"
MF "v[72:75],a[216:219],a[148:151],v[72:75]\n"
TR "a[214:215],v0 offset:20992\n"
MF "v[72:75],a[220:223],a[188:191],v[72:75]\n"
"buffer_load_dword a249,v15,s[48:51],s59 offen\n"
MF "v[80:83],a[220:223],a[192:195],v[80:83]\n"
"s_bfe_u32 s59,s23,0x30003\n"
MF "v[80:83],a[216:219],a[152:155],v[80:83]\n"
LD "v11,s[28:31],s82 offen offset:1024 lds\n"
MF "v[88:91],a[216:219],a[156:159],v[88:91]\n"
"s_add_u32 s59,s59,5\n"
MF "v[88:91],a[220:223],a[196:199],v[88:91]\n"
"s_lshl_b32 s59,s59,7\n"
MF "v[92:95],a[204:207],a[196:199],v[92:95]\n"
"s_and_b32 s59,s59,768\n"
TR "a[216:217],v0 offset:16392\n"
MF "v[92:95],a[200:203],a[156:159],v[92:95]\n"
"s_add_u32 s59,s59,s60\n"
TR "a[218:219],v0 offset:16904\n"
MF "v[84:87],a[200:203],a[152:155],v[84:87]\n"
LD "v12,s[28:31],s82 offen offset:2048 lds\n"
MF "v[84:87],a[204:207],a[192:195],v[84:87]\n"
TR "a[220:221],v0 offset:20488\n"
MF "v[76:79],a[204:207],a[188:191],v[76:79]\n"
TR "a[222:223],v0 offset:21000\n"
MF "v[76:79],a[200:203],a[148:151],v[76:79]\n"
MF "v[68:71],a[200:203],a[144:147],v[68:71]\n"
MF "v[68:71],a[204:207],a[184:187],v[68:71]\n"
".Lpg3a:\n"
"s_cbranch_execz .Lisr1\n"
LD "v13,s[28:31],s82 offen offset:3072 lds\n"
MF "v[60:63],a[204:207],a[180:183],v[60:63]\n"
MF "v[60:63],a[200:203],a[140:143],v[60:63]\n"
"s_add_u32 s14,s81,1024\n"
"s_sub_u32 s15,s14,1280\n"
"s_waitcnt lgkmcnt(4)\n"
MF "v[96:99],a[208:211],a[140:143],v[96:99]\n"
"s_min_u32 s14,s14,s15\n"
"s_add_u32 s14,s14,s68\n"
TR "a[200:201],v1 offset:16384\n"
MF "v[96:99],a[212:215],a[180:183],v[96:99]\n"
"s_add_u32 s84,s14,0\n"
"s_add_u32 m0,s63,40960\n"
TR "a[202:203],v1 offset:16896\n"
MF "v[104:107],a[212:215],a[184:187],v[104:107]\n"
LD "v4,s[24:27],s84 offen lds\n"
MF "v[104:107],a[208:211],a[144:147],v[104:107]\n"
TR "a[204:205],v1 offset:20480\n"
MF "v[112:115],a[208:211],a[148:151],v[112:115]\n"
TR "a[206:207],v1 offset:20992\n"
MF "v[112:115],a[212:215],a[188:191],v[112:115]\n"
MF "v[120:123],a[212:215],a[192:195],v[120:123]\n"
"s_add_u32 s84,s14,9216\n"
MF "v[120:123],a[208:211],a[152:155],v[120:123]\n"
LD "v5,s[24:27],s84 offen offset:1024 lds\n"
MF "v[128:131],a[208:211],a[156:159],v[128:131]\n"
MF "v[128:131],a[212:215],a[196:199],v[128:131]\n"
"s_waitcnt lgkmcnt(4)\n"
MF "v[132:135],a[220:223],a[196:199],v[132:135]\n"
TR "a[208:209],v1 offset:16392\n"
MF "v[132:135],a[216:219],a[156:159],v[132:135]\n"
TR "a[210:211],v1 offset:16904\n"
MF "v[124:127],a[216:219],a[152:155],v[124:127]\n"
MF "v[124:127],a[220:223],a[192:195],v[124:127]\n"
TR "a[212:213],v1 offset:20488\n"
MF "v[116:119],a[220:223],a[188:191],v[116:119]\n"
TR "a[214:215],v1 offset:21000\n"
MF "v[116:119],a[216:219],a[148:151],v[116:119]\n"
MF "v[108:111],a[216:219],a[144:147],v[108:111]\n"
MF "v[108:111],a[220:223],a[184:187],v[108:111]\n"
MF "v[100:103],a[220:223],a[180:183],v[100:103]\n"
"s_waitcnt vmcnt(25) lgkmcnt(0)\n"
MF "v[100:103],a[216:219],a[140:143],v[100:103]\n"
"s_barrier\n"
MF "v[136:139],a[200:203],a[140:143],v[136:139]\n"
TR "a[216:217],v0 offset:32768\n"
MF "v[136:139],a[204:207],a[180:183],v[136:139]\n"
"s_add_u32 m0,s62,65536\n"
TR "a[218:219],v0 offset:33280\n"
MF "v[144:147],a[204:207],a[184:187],v[144:147]\n"
"s_add_u32 s82,s66,5505152\n"
"ds_read_b128 a[60:63],v2\n"
MF "v[144:147],a[200:203],a[144:147],v[144:147]\n"
"s_sub_u32 s15,s82,7864320\n"
TR "a[220:221],v0 offset:36864\n"
MF "v[152:155],a[200:203],a[148:151],v[152:155]\n"
"s_min_u32 s82,s82,s15\n"
TR "a[222:223],v0 offset:37376\n"
MF "v[152:155],a[204:207],a[188:191],v[152:155]\n"
LD "v6,s[28:31],s82 offen lds\n"
MF "v[160:163],a[204:207],a[192:195],v[160:163]\n"
"ds_read_b128 a[100:103],v3\n"
MF "v[160:163],a[200:203],a[152:155],v[160:163]\n"
MF "v[168:171],a[200:203],a[156:159],v[168:171]\n"
MF "v[168:171],a[204:207],a[196:199],v[168:171]\n"
LD "v11,s[28:31],s82 offen offset:1024 lds\n"
MF "v[172:175],a[212:215],a[196:199],v[172:175]\n"
TR "a[200:201],v0 offset:32776\n"
MF "v[172:175],a[208:211],a[156:159],v[172:175]\n"
TR "a[202:203],v0 offset:33288\n"
MF "v[164:167],a[208:211],a[152:155],v[164:167]\n"
"ds_read_b128 a[64:67],v2 offset:2048\n"
MF "v[164:167],a[212:215],a[192:195],v[164:167]\n"
TR "a[204:205],v0 offset:36872\n"
MF "v[156:159],a[212:215],a[188:191],v[156:159]\n"
TR "a[206:207],v0 offset:37384\n"
MF "v[156:159],a[208:211],a[148:151],v[156:159]\n"
LD "v12,s[28:31],s82 offen offset:2048 lds\n"
MF "v[148:151],a[208:211],a[144:147],v[148:151]\n"
"ds_read_b128 a[104:107],v3 offset:2048\n"
MF "v[148:151],a[212:215],a[184:187],v[148:151]\n"
MF "v[140:143],a[212:215],a[180:183],v[140:143]\n"
MF "v[140:143],a[208:211],a[140:143],v[140:143]\n"
"s_waitcnt lgkmcnt(7)\n"
LD "v13,s[28:31],s82 offen offset:3072 lds\n"
MF "v[176:179],a[216:219],a[140:143],v[176:179]\n"
TR "a[208:209],v1 offset:32768\n"
MF "v[176:179],a[220:223],a[180:183],v[176:179]\n"
TR "a[210:211],v1 offset:33280\n"
MF "v[184:187],a[220:223],a[184:187],v[184:187]\n"
"ds_read_b128 a[68:71],v2 offset:4096\n"
MF "v[184:187],a[216:219],a[144:147],v[184:187]\n"
"s_add_u32 s84,s14,20480\n"
TR "a[212:213],v1 offset:36864\n"
MF "v[192:195],a[216:219],a[148:151],v[192:195]\n"
"s_add_u32 m0,s63,43008\n"
TR "a[214:215],v1 offset:37376\n"
MF "v[192:195],a[220:223],a[188:191],v[192:195]\n"
LD "v4,s[24:27],s84 offen lds\n"
MF "v[200:203],a[220:223],a[192:195],v[200:203]\n"
"ds_read_b128 a[108:111],v3 offset:4096\n"
MF "v[200:203],a[216:219],a[152:155],v[200:203]\n"
MF "v[208:211],a[216:219],a[156:159],v[208:211]\n"
"s_add_u32 s84,s14,29696\n"
MF "v[208:211],a[220:223],a[196:199],v[208:211]\n"
"s_waitcnt lgkmcnt(7)\n"
LD "v5,s[24:27],s84 offen offset:1024 lds\n"
MF "v[212:215],a[204:207],a[196:199],v[212:215]\n"
TR "a[216:217],v1 offset:32776\n"
MF "v[212:215],a[200:203],a[156:159],v[212:215]\n"
TR "a[218:219],v1 offset:33288\n"
MF "v[204:207],a[200:203],a[152:155],v[204:207]\n"
"ds_read_b128 a[72:75],v2 offset:6144\n"
MF "v[204:207],a[204:207],a[192:195],v[204:207]\n"
TR "a[220:221],v1 offset:36872\n"
MF "v[196:199],a[204:207],a[188:191],v[196:199]\n"
TR "a[222:223],v1 offset:37384\n"
MF "v[196:199],a[200:203],a[148:151],v[196:199]\n"
MF "v[188:191],a[200:203],a[144:147],v[188:191]\n"
"v_add_u32 v0,0xffff4000,v0\n"
MF "v[188:191],a[204:207],a[184:187],v[188:191]\n"
"v_add_u32 v1,0xffff4000,v1\n"
MF "v[180:183],a[204:207],a[180:183],v[180:183]\n"
"s_waitcnt vmcnt(26) lgkmcnt(0)\n"
MF "v[180:183],a[200:203],a[140:143],v[180:183]\n"
"s_barrier\n"
MF "v[216:219],a[208:211],a[140:143],v[216:219]\n"
TR "a[200:201],v0\n"
MF "v[216:219],a[212:215],a[180:183],v[216:219]\n"
"s_add_u32 m0,s62,81920\n"
TR "a[202:203],v0 offset:512\n"
MF "v[224:227],a[212:215],a[184:187],v[224:227]\n"
"s_add_u32 s82,s66,5505280\n"
"ds_read_b128 a[112:115],v3 offset:6144\n"
MF "v[224:227],a[208:211],a[144:147],v[224:227]\n"
"s_sub_u32 s15,s82,7864320\n"
TR "a[204:205],v0 offset:4096\n"
MF "v[232:235],a[208:211],a[148:151],v[232:235]\n"
"s_min_u32 s82,s82,s15\n"
TR "a[206:207],v0 offset:4608\n"
MF "v[232:235],a[212:215],a[188:191],v[232:235]\n"
LD "v6,s[28:31],s82 offen lds\n"
MF "v[240:243],a[212:215],a[192:195],v[240:243]\n"
"ds_read_b128 a[76:79],v2 offset:8192\n"
MF "v[240:243],a[208:211],a[152:155],v[240:243]\n"
"ds_read_b128 a[116:119],v3 offset:8192\n"
MF "v[248:251],a[208:211],a[156:159],v[248:251]\n"
MF "v[248:251],a[212:215],a[196:199],v[248:251]\n"
LD "v11,s[28:31],s82 offen offset:1024 lds\n"
MF "v[252:255],a[220:223],a[196:199],v[252:255]\n"
TR "a[208:209],v0 offset:8\n"
MF "v[252:255],a[216:219],a[156:159],v[252:255]\n"
TR "a[210:211],v0 offset:520\n"
MF "v[244:247],a[216:219],a[152:155],v[244:247]\n"
MF "v[244:247],a[220:223],a[192:195],v[244:247]\n"
TR "a[212:213],v0 offset:4104\n"
MF "v[236:239],a[220:223],a[188:191],v[236:239]\n"
TR "a[214:215],v0 offset:4616\n"
MF "v[236:239],a[216:219],a[148:151],v[236:239]\n"
LD "v12,s[28:31],s82 offen offset:2048 lds\n"
MF "v[228:231],a[216:219],a[144:147],v[228:231]\n"
MF "v[228:231],a[220:223],a[184:187],v[228:231]\n"
MF "v[220:223],a[220:223],a[180:183],v[220:223]\n"
MF "v[220:223],a[216:219],a[140:143],v[220:223]\n"
"s_waitcnt lgkmcnt(4)\n"
LD "v13,s[28:31],s82 offen offset:3072 lds\n"
MF "v[16:19],a[200:203],a[60:63],v[16:19]\n"
TR "a[216:217],v1\n"
MF "v[16:19],a[204:207],a[100:103],v[16:19]\n"
TR "a[218:219],v1 offset:512\n"
MF "v[24:27],a[204:207],a[104:107],v[24:27]\n"
MF "v[24:27],a[200:203],a[64:67],v[24:27]\n"
"s_add_u32 s84,s14,40960\n"
TR "a[220:221],v1 offset:4096\n"
MF "v[32:35],a[200:203],a[68:71],v[32:35]\n"
"s_add_u32 m0,s63,45056\n"
TR "a[222:223],v1 offset:4608\n"
MF "v[32:35],a[204:207],a[108:111],v[32:35]\n"
LD "v4,s[24:27],s84 offen lds\n"
MF "v[40:43],a[204:207],a[112:115],v[40:43]\n"
MF "v[40:43],a[200:203],a[72:75],v[40:43]\n"
MF "v[48:51],a[200:203],a[76:79],v[48:51]\n"
MF "v[48:51],a[204:207],a[116:119],v[48:51]\n"
"s_waitcnt lgkmcnt(4)\n"
MF "v[52:55],a[212:215],a[116:119],v[52:55]\n"
TR "a[200:201],v1 offset:8\n"
MF "v[52:55],a[208:211],a[76:79],v[52:55]\n"
TR "a[202:203],v1 offset:520\n"
MF "v[44:47],a[208:211],a[72:75],v[44:47]\n"
MF "v[44:47],a[212:215],a[112:115],v[44:47]\n"
TR "a[204:205],v1 offset:4104\n"
MF "v[36:39],a[212:215],a[108:111],v[36:39]\n"
TR "a[206:207],v1 offset:4616\n"
MF "v[36:39],a[208:211],a[68:71],v[36:39]\n"
MF "v[28:31],a[208:211],a[64:67],v[28:31]\n"
MF "v[28:31],a[212:215],a[104:107],v[28:31]\n"
MF "v[20:23],a[212:215],a[100:103],v[20:23]\n"
"s_waitcnt vmcnt(25) lgkmcnt(0)\n"
MF "v[20:23],a[208:211],a[60:63],v[20:23]\n"
"s_barrier\n"
MF "v[56:59],a[216:219],a[60:63],v[56:59]\n"
"s_add_u32 m0,s62,0\n"
"s_add_u32 s82,s66,6291712\n"
TR "a[208:209],v0 offset:16384\n"
MF "v[56:59],a[220:223],a[100:103],v[56:59]\n"
"s_sub_u32 s15,s82,7864320\n"
"s_min_u32 s82,s82,s15\n"
TR "a[210:211],v0 offset:16896\n"
MF "v[64:67],a[220:223],a[104:107],v[64:67]\n"
LD "v6,s[28:31],s82 offen lds\n"
MF "v[64:67],a[216:219],a[64:67],v[64:67]\n"
TR "a[212:213],v0 offset:20480\n"
MF "v[72:75],a[216:219],a[68:71],v[72:75]\n"
TR "a[214:215],v0 offset:20992\n"
MF "v[72:75],a[220:223],a[108:111],v[72:75]\n"
"buffer_load_dword a249,v15,s[48:51],s59 offen\n"
MF "v[80:83],a[220:223],a[112:115],v[80:83]\n"
MF "v[80:83],a[216:219],a[72:75],v[80:83]\n"
LD "v11,s[28:31],s82 offen offset:1024 lds\n"
MF "v[88:91],a[216:219],a[76:79],v[88:91]\n"
MF "v[88:91],a[220:223],a[116:119],v[88:91]\n"
MF "v[92:95],a[204:207],a[116:119],v[92:95]\n"
TR "a[216:217],v0 offset:16392\n"
MF "v[92:95],a[200:203],a[76:79],v[92:95]\n"
TR "a[218:219],v0 offset:16904\n"
MF "v[84:87],a[200:203],a[72:75],v[84:87]\n"
LD "v12,s[28:31],s82 offen offset:2048 lds\n"
MF "v[84:87],a[204:207],a[112:115],v[84:87]\n"
TR "a[220:221],v0 offset:20488\n"
MF "v[76:79],a[204:207],a[108:111],v[76:79]\n"
TR "a[222:223],v0 offset:21000\n"
MF "v[76:79],a[200:203],a[68:71],v[76:79]\n"
MF "v[68:71],a[200:203],a[64:67],v[68:71]\n"
MF "v[68:71],a[204:207],a[104:107],v[68:71]\n"
LD "v13,s[28:31],s82 offen offset:3072 lds\n"
MF "v[60:63],a[204:207],a[100:103],v[60:63]\n"
MF "v[60:63],a[200:203],a[60:63],v[60:63]\n"
"s_add_u32 s14,s81,1152\n"
"s_sub_u32 s15,s14,1280\n"
"s_waitcnt lgkmcnt(4)\n"
MF "v[96:99],a[208:211],a[60:63],v[96:99]\n"
"s_min_u32 s14,s14,s15\n"
"s_add_u32 s14,s14,s68\n"
TR "a[200:201],v1 offset:16384\n"
MF "v[96:99],a[212:215],a[100:103],v[96:99]\n"
"s_add_u32 s84,s14,0\n"
"s_add_u32 m0,s63,0\n"
TR "a[202:203],v1 offset:16896\n"
MF "v[104:107],a[212:215],a[104:107],v[104:107]\n"
LD "v4,s[24:27],s84 offen lds\n"
MF "v[104:107],a[208:211],a[64:67],v[104:107]\n"
TR "a[204:205],v1 offset:20480\n"
MF "v[112:115],a[208:211],a[68:71],v[112:115]\n"
TR "a[206:207],v1 offset:20992\n"
MF "v[112:115],a[212:215],a[108:111],v[112:115]\n"
MF "v[120:123],a[212:215],a[112:115],v[120:123]\n"
"s_add_u32 s84,s14,9216\n"
MF "v[120:123],a[208:211],a[72:75],v[120:123]\n"
LD "v5,s[24:27],s84 offen offset:1024 lds\n"
MF "v[128:131],a[208:211],a[76:79],v[128:131]\n"
MF "v[128:131],a[212:215],a[116:119],v[128:131]\n"
"s_waitcnt lgkmcnt(4)\n"
MF "v[132:135],a[220:223],a[116:119],v[132:135]\n"
TR "a[208:209],v1 offset:16392\n"
MF "v[132:135],a[216:219],a[76:79],v[132:135]\n"
"s_add_u32 s84,s14,18432\n"
TR "a[210:211],v1 offset:16904\n"
MF "v[124:127],a[216:219],a[72:75],v[124:127]\n"
LD "v4,s[24:27],s84 offen offset:2048 lds\n"
MF "v[124:127],a[220:223],a[112:115],v[124:127]\n"
TR "a[212:213],v1 offset:20488\n"
MF "v[116:119],a[220:223],a[108:111],v[116:119]\n"
TR "a[214:215],v1 offset:21000\n"
MF "v[116:119],a[216:219],a[68:71],v[116:119]\n"
MF "v[108:111],a[216:219],a[64:67],v[108:111]\n"
MF "v[108:111],a[220:223],a[104:107],v[108:111]\n"
MF "v[100:103],a[220:223],a[100:103],v[100:103]\n"
"s_waitcnt vmcnt(26) lgkmcnt(0)\n"
MF "v[100:103],a[216:219],a[60:63],v[100:103]\n"
"s_barrier\n"
MF "v[136:139],a[200:203],a[60:63],v[136:139]\n"
TR "a[216:217],v0 offset:32768\n"
MF "v[136:139],a[204:207],a[100:103],v[136:139]\n"
"s_add_u32 m0,s62,16384\n"
TR "a[218:219],v0 offset:33280\n"
MF "v[144:147],a[204:207],a[104:107],v[144:147]\n"
"s_add_u32 s82,s66,7078144\n"
"ds_read_b128 a[140:143],v2 offset:20480\n"
MF "v[144:147],a[200:203],a[64:67],v[144:147]\n"
"s_sub_u32 s15,s82,7864320\n"
TR "a[220:221],v0 offset:36864\n"
MF "v[152:155],a[200:203],a[68:71],v[152:155]\n"
"s_min_u32 s82,s82,s15\n"
TR "a[222:223],v0 offset:37376\n"
MF "v[152:155],a[204:207],a[108:111],v[152:155]\n"
LD "v6,s[28:31],s82 offen lds\n"
MF "v[160:163],a[204:207],a[112:115],v[160:163]\n"
"ds_read_b128 a[180:183],v3 offset:20480\n"
MF "v[160:163],a[200:203],a[72:75],v[160:163]\n"
MF "v[168:171],a[200:203],a[76:79],v[168:171]\n"
MF "v[168:171],a[204:207],a[116:119],v[168:171]\n"
LD "v11,s[28:31],s82 offen offset:1024 lds\n"
MF "v[172:175],a[212:215],a[116:119],v[172:175]\n"
TR "a[200:201],v0 offset:32776\n"
MF "v[172:175],a[208:211],a[76:79],v[172:175]\n"
TR "a[202:203],v0 offset:33288\n"
MF "v[164:167],a[208:211],a[72:75],v[164:167]\n"
"ds_read_b128 a[144:147],v2 offset:22528\n"
MF "v[164:167],a[212:215],a[112:115],v[164:167]\n"
TR "a[204:205],v0 offset:36872\n"
MF "v[156:159],a[212:215],a[108:111],v[156:159]\n"
TR "a[206:207],v0 offset:37384\n"
MF "v[156:159],a[208:211],a[68:71],v[156:159]\n"
LD "v12,s[28:31],s82 offen offset:2048 lds\n"
MF "v[148:151],a[208:211],a[64:67],v[148:151]\n"
"ds_read_b128 a[184:187],v3 offset:22528\n"
MF "v[148:151],a[212:215],a[104:107],v[148:151]\n"
MF "v[140:143],a[212:215],a[100:103],v[140:143]\n"
MF "v[140:143],a[208:211],a[60:63],v[140:143]\n"
"s_waitcnt lgkmcnt(7)\n"
LD "v13,s[28:31],s82 offen offset:3072 lds\n"
MF "v[176:179],a[216:219],a[60:63],v[176:179]\n"
TR "a[208:209],v1 offset:32768\n"
MF "v[176:179],a[220:223],a[100:103],v[176:179]\n"
TR "a[210:211],v1 offset:33280\n"
MF "v[184:187],a[220:223],a[104:107],v[184:187]\n"
"ds_read_b128 a[148:151],v2 offset:24576\n"
MF "v[184:187],a[216:219],a[64:67],v[184:187]\n"
"s_add_u32 s84,s14,30720\n"
TR "a[212:213],v1 offset:36864\n"
MF "v[192:195],a[216:219],a[68:71],v[192:195]\n"
"s_add_u32 m0,s63,3072\n"
TR "a[214:215],v1 offset:37376\n"
MF "v[192:195],a[220:223],a[108:111],v[192:195]\n"
LD "v5,s[24:27],s84 offen lds\n"
MF "v[200:203],a[220:223],a[112:115],v[200:203]\n"
"ds_read_b128 a[188:191],v3 offset:24576\n"
MF "v[200:203],a[216:219],a[72:75],v[200:203]\n"
MF "v[208:211],a[216:219],a[76:79],v[208:211]\n"
"s_add_u32 s84,s14,39936\n"
MF "v[208:211],a[220:223],a[116:119],v[208:211]\n"
"s_waitcnt lgkmcnt(7)\n"
LD "v4,s[24:27],s84 offen offset:1024 lds\n"
MF "v[212:215],a[204:207],a[116:119],v[212:215]\n"
TR "a[216:217],v1 offset:32776\n"
MF "v[212:215],a[200:203],a[76:79],v[212:215]\n"
TR "a[218:219],v1 offset:33288\n"
MF "v[204:207],a[200:203],a[72:75],v[204:207]\n"
"ds_read_b128 a[152:155],v2 offset:26624\n"
MF "v[204:207],a[204:207],a[112:115],v[204:207]\n"
TR "a[220:221],v1 offset:36872\n"
MF "v[196:199],a[204:207],a[108:111],v[196:199]\n"
TR "a[222:223],v1 offset:37384\n"
MF "v[196:199],a[200:203],a[68:71],v[196:199]\n"
MF "v[188:191],a[200:203],a[64:67],v[188:191]\n"
"v_add_u32 v0,0xc000,v0\n"
MF "v[188:191],a[204:207],a[104:107],v[188:191]\n"
"v_add_u32 v1,0xc000,v1\n"
MF "v[180:183],a[204:207],a[100:103],v[180:183]\n"
"s_waitcnt vmcnt(27) lgkmcnt(0)\n"
"s_branch .Lqj\n"
".Lpgq0_c:\n"
"s_cbranch_execz .Lpgqy_c\n"
".Lpgq0_f:\n"
"s_cbranch_execz .Lpg12f\n"
".Lq0:\n"
MF "v[16:19],a[200:203],a[60:63],0\n"
"s_add_u32 m0,s62,49152\n"
"s_add_u32 s82,s66,786432\n"
TR "a[216:217],v1\n"
MF "v[16:19],a[204:207],a[100:103],v[16:19]\n"
"s_sub_u32 s15,s82,7864320\n"
"s_min_u32 s82,s82,s15\n"
TR "a[218:219],v1 offset:512\n"
MF "v[24:27],a[204:207],a[104:107],0\n"
LD "v6,s[28:31],s82 offen lds\n"
MF "v[24:27],a[200:203],a[64:67],v[24:27]\n"
TR "a[220:221],v1 offset:4096\n"
MF "v[32:35],a[200:203],a[68:71],0\n"
TR "a[222:223],v1 offset:4608\n"
MF "v[32:35],a[204:207],a[108:111],v[32:35]\n"
"v_max3_f32 v220,|v16|,|v17|,|v18|\n"
"v_max_f32_e64 v220,v220,|v19|\n"
MF "v[40:43],a[204:207],a[112:115],0\n"
"v_readlane_b32 s12,v220,3\n"
"v_readlane_b32 s6,v220,22\n"
MF "v[40:43],a[200:203],a[72:75],v[40:43]\n"
"s_min_u32 s12,s12,s6\n"
"v_readlane_b32 s7,v220,41\n"
"v_readlane_b32 s8,v220,60\n"
MF "v[48:51],a[200:203],a[76:79],0\n"
"s_min_u32 s7,s7,s8\n"
MF "v[48:51],a[204:207],a[116:119],v[48:51]\n"
LD "v11,s[28:31],s82 offen offset:1024 lds\n"
MF "v[52:55],a[212:215],a[116:119],0\n"
"s_min_u32 s12,s12,s7\n"
TR "a[200:201],v1 offset:8\n"
MF "v[52:55],a[208:211],a[76:79],v[52:55]\n"
"s_and_b32 s12,s12,0x7f800000\n"
TR "a[202:203],v1 offset:520\n"
MF "v[44:47],a[208:211],a[72:75],0\n"
"s_sub_u32 s12,s12,0x14000000\n"
MF "v[44:47],a[212:215],a[112:115],v[44:47]\n"
"s_cmp_lt_u32 s12,0x50000000\n"
"s_cselect_b32 s12,s12,0xe8400000\n"
TR "a[204:205],v1 offset:4104\n"
MF "v[36:39],a[212:215],a[108:111],0\n"
"s_add_u32 s12,s12,0x17c00000\n"
TR "a[206:207],v1 offset:4616\n"
MF "v[36:39],a[208:211],a[68:71],v[36:39]\n"
"v_mov_b32 v220,s12\n"
"v_mov_b32 v221,s12\n"
MF "v[28:31],a[208:211],a[64:67],0\n"
"v_mov_b32 v222,s12\n"
"v_mov_b32 v223,s12\n"
MF "v[28:31],a[212:215],a[104:107],v[28:31]\n"
LD "v12,s[28:31],s82 offen offset:2048 lds\n"
MF "v[20:23],a[212:215],a[100:103],0\n"
"s_waitcnt vmcnt(7) lgkmcnt(0)\n"
MF "v[20:23],a[208:211],a[60:63],v[20:23]\n"
"s_barrier\n"
MF "v[56:59],a[216:219],a[60:63],v[220:223]\n"
TR "a[208:209],v0 offset:16384\n"
MF "v[56:59],a[220:223],a[100:103],v[56:59]\n"
TR "a[210:211],v0 offset:16896\n"
MF "v[64:67],a[220:223],a[104:107],v[220:223]\n"
LD "v13,s[28:31],s82 offen offset:3072 lds\n"
MF "v[64:67],a[216:219],a[64:67],v[64:67]\n"
"s_add_u32 m0,s62,65536\n"
TR "a[212:213],v0 offset:20480\n"
MF "v[72:75],a[216:219],a[68:71],v[220:223]\n"
"s_add_u32 s82,s66,786560\n"
TR "a[214:215],v0 offset:20992\n"
MF "v[72:75],a[220:223],a[108:111],v[72:75]\n"
"s_sub_u32 s15,s82,7864320\n"
MF "v[80:83],a[220:223],a[112:115],v[220:223]\n"
"s_min_u32 s82,s82,s15\n"
MF "v[80:83],a[216:219],a[72:75],v[80:83]\n"
LD "v6,s[28:31],s82 offen lds\n"
MF "v[88:91],a[216:219],a[76:79],v[220:223]\n"
MF "v[88:91],a[220:223],a[116:119],v[88:91]\n"
MF "v[92:95],a[204:207],a[116:119],v[220:223]\n"
TR "a[216:217],v0 offset:16392\n"
MF "v[92:95],a[200:203],a[76:79],v[92:95]\n"
TR "a[218:219],v0 offset:16904\n"
MF "v[84:87],a[200:203],a[72:75],v[220:223]\n"
LD "v11,s[28:31],s82 offen offset:1024 lds\n"
MF "v[84:87],a[204:207],a[112:115],v[84:87]\n"
TR "a[220:221],v0 offset:20488\n"
MF "v[76:79],a[204:207],a[108:111],v[220:223]\n"
TR "a[222:223],v0 offset:21000\n"
MF "v[76:79],a[200:203],a[68:71],v[76:79]\n"
MF "v[68:71],a[200:203],a[64:67],v[220:223]\n"
MF "v[68:71],a[204:207],a[104:107],v[68:71]\n"
LD "v12,s[28:31],s82 offen offset:2048 lds\n"
MF "v[60:63],a[204:207],a[100:103],v[220:223]\n"
MF "v[60:63],a[200:203],a[60:63],v[60:63]\n"
"s_waitcnt lgkmcnt(4)\n"
MF "v[96:99],a[208:211],a[60:63],v[220:223]\n"
TR "a[200:201],v1 offset:16384\n"
MF "v[96:99],a[212:215],a[100:103],v[96:99]\n"
TR "a[202:203],v1 offset:16896\n"
MF "v[104:107],a[212:215],a[104:107],v[220:223]\n"
LD "v13,s[28:31],s82 offen offset:3072 lds\n"
MF "v[104:107],a[208:211],a[64:67],v[104:107]\n"
"s_add_u32 m0,s62,81920\n"
TR "a[204:205],v1 offset:20480\n"
MF "v[112:115],a[208:211],a[68:71],v[220:223]\n"
"s_add_u32 s82,s66,786688\n"
TR "a[206:207],v1 offset:20992\n"
MF "v[112:115],a[212:215],a[108:111],v[112:115]\n"
"s_sub_u32 s15,s82,7864320\n"
MF "v[120:123],a[212:215],a[112:115],v[220:223]\n"
"s_min_u32 s82,s82,s15\n"
MF "v[120:123],a[208:211],a[72:75],v[120:123]\n"
LD "v6,s[28:31],s82 offen lds\n"
MF "v[128:131],a[208:211],a[76:79],v[220:223]\n"
MF "v[128:131],a[212:215],a[116:119],v[128:131]\n"
"s_waitcnt lgkmcnt(4)\n"
MF "v[132:135],a[220:223],a[116:119],v[220:223]\n"
TR "a[208:209],v1 offset:16392\n"
MF "v[132:135],a[216:219],a[76:79],v[132:135]\n"
TR "a[210:211],v1 offset:16904\n"
MF "v[124:127],a[216:219],a[72:75],v[220:223]\n"
LD "v11,s[28:31],s82 offen offset:1024 lds\n"
MF "v[124:127],a[220:223],a[112:115],v[124:127]\n"
TR "a[212:213],v1 offset:20488\n"
MF "v[116:119],a[220:223],a[108:111],v[220:223]\n"
TR "a[214:215],v1 offset:21000\n"
MF "v[116:119],a[216:219],a[68:71],v[116:119]\n"
MF "v[108:111],a[216:219],a[64:67],v[220:223]\n"
MF "v[108:111],a[220:223],a[104:107],v[108:111]\n"
LD "v12,s[28:31],s82 offen offset:2048 lds\n"
MF "v[100:103],a[220:223],a[100:103],v[220:223]\n"
"s_waitcnt vmcnt(11) lgkmcnt(0)\n"
MF "v[100:103],a[216:219],a[60:63],v[100:103]\n"
"s_barrier\n"
MF "v[136:139],a[200:203],a[60:63],v[220:223]\n"
TR "a[216:217],v0 offset:32768\n"
MF "v[136:139],a[204:207],a[100:103],v[136:139]\n"
TR "a[218:219],v0 offset:33280\n"
MF "v[144:147],a[204:207],a[104:107],v[220:223]\n"
"ds_read_b128 a[140:143],v2 offset:20480\n"
MF "v[144:147],a[200:203],a[64:67],v[144:147]\n"
TR "a[220:221],v0 offset:36864\n"
MF "v[152:155],a[200:203],a[68:71],v[220:223]\n"
TR "a[222:223],v0 offset:37376\n"
MF "v[152:155],a[204:207],a[108:111],v[152:155]\n"
LD "v13,s[28:31],s82 offen offset:3072 lds\n"
MF "v[160:163],a[204:207],a[112:115],v[220:223]\n"
"ds_read_b128 a[180:183],v3 offset:20480\n"
MF "v[160:163],a[200:203],a[72:75],v[160:163]\n"
"s_add_u32 m0,s62,0\n"
MF "v[168:171],a[200:203],a[76:79],v[220:223]\n"
"s_add_u32 s82,s66,1572864\n"
"s_sub_u32 s15,s82,7864320\n"
"s_min_u32 s82,s82,s15\n"
MF "v[168:171],a[204:207],a[116:119],v[168:171]\n"
LD "v6,s[28:31],s82 offen lds\n"
MF "v[172:175],a[212:215],a[116:119],v[220:223]\n"
TR "a[200:201],v0 offset:32776\n"
MF "v[172:175],a[208:211],a[76:79],v[172:175]\n"
TR "a[202:203],v0 offset:33288\n"
MF "v[164:167],a[208:211],a[72:75],v[220:223]\n"
"ds_read_b128 a[144:147],v2 offset:22528\n"
MF "v[164:167],a[212:215],a[112:115],v[164:167]\n"
TR "a[204:205],v0 offset:36872\n"
MF "v[156:159],a[212:215],a[108:111],v[220:223]\n"
TR "a[206:207],v0 offset:37384\n"
MF "v[156:159],a[208:211],a[68:71],v[156:159]\n"
LD "v11,s[28:31],s82 offen offset:1024 lds\n"
MF "v[148:151],a[208:211],a[64:67],v[220:223]\n"
"ds_read_b128 a[184:187],v3 offset:22528\n"
MF "v[148:151],a[212:215],a[104:107],v[148:151]\n"
MF "v[140:143],a[212:215],a[100:103],v[220:223]\n"
MF "v[140:143],a[208:211],a[60:63],v[140:143]\n"
"s_waitcnt lgkmcnt(7)\n"
LD "v12,s[28:31],s82 offen offset:2048 lds\n"
MF "v[176:179],a[216:219],a[60:63],v[220:223]\n"
TR "a[208:209],v1 offset:32768\n"
MF "v[176:179],a[220:223],a[100:103],v[176:179]\n"
".Lpg4c:\n"
"s_cbranch_execz .Lpg14c\n"
".Lpg4f:\n"
"s_cbranch_execz .Lpg13f\n"
TR "a[210:211],v1 offset:33280\n"
MF "v[184:187],a[220:223],a[104:107],v[220:223]\n"
"ds_read_b128 a[148:151],v2 offset:24576\n"
MF "v[184:187],a[216:219],a[64:67],v[184:187]\n"
TR "a[212:213],v1 offset:36864\n"
MF "v[192:195],a[216:219],a[68:71],v[220:223]\n"
TR "a[214:215],v1 offset:37376\n"
MF "v[192:195],a[220:223],a[108:111],v[192:195]\n"
LD "v13,s[28:31],s82 offen offset:3072 lds\n"
MF "v[200:203],a[220:223],a[112:115],v[220:223]\n"
"ds_read_b128 a[188:191],v3 offset:24576\n"
MF "v[200:203],a[216:219],a[72:75],v[200:203]\n"
"s_add_u32 s14,s81,384\n"
"s_sub_u32 s15,s14,1280\n"
"s_min_u32 s14,s14,s15\n"
MF "v[208:211],a[216:219],a[76:79],v[220:223]\n"
"s_add_u32 s14,s14,s68\n"
"s_add_u32 s84,s14,0\n"
"s_add_u32 m0,s63,0\n"
MF "v[208:211],a[220:223],a[116:119],v[208:211]\n"
"s_waitcnt lgkmcnt(7)\n"
LD "v4,s[24:27],s84 offen lds\n"
MF "v[212:215],a[204:207],a[116:119],v[220:223]\n"
TR "a[216:217],v1 offset:32776\n"
MF "v[212:215],a[200:203],a[76:79],v[212:215]\n"
TR "a[218:219],v1 offset:33288\n"
MF "v[204:207],a[200:203],a[72:75],v[220:223]\n"
"ds_read_b128 a[152:155],v2 offset:26624\n"
MF "v[204:207],a[204:207],a[112:115],v[204:207]\n"
TR "a[220:221],v1 offset:36872\n"
MF "v[196:199],a[204:207],a[108:111],v[220:223]\n"
"s_add_u32 s84,s14,9216\n"
TR "a[222:223],v1 offset:37384\n"
MF "v[196:199],a[200:203],a[68:71],v[196:199]\n"
LD "v5,s[24:27],s84 offen offset:1024 lds\n"
MF "v[188:191],a[200:203],a[64:67],v[220:223]\n"
"s_add_u32 m0,s62,16384\n"
"s_add_u32 s82,s66,1572992\n"
"v_add_u32 v0,0xc000,v0\n"
MF "v[188:191],a[204:207],a[104:107],v[188:191]\n"
"s_sub_u32 s15,s82,7864320\n"
"s_min_u32 s82,s82,s15\n"
"v_add_u32 v1,0xc000,v1\n"
MF "v[180:183],a[204:207],a[100:103],v[220:223]\n"
"s_waitcnt vmcnt(14) lgkmcnt(0)\n"
LD "v6,s[28:31],s82 offen lds\n"
MF "v[180:183],a[200:203],a[60:63],v[180:183]\n"
"s_barrier\n"
MF "v[216:219],a[208:211],a[60:63],v[220:223]\n"
TR "a[200:201],v0\n"
MF "v[216:219],a[212:215],a[100:103],v[216:219]\n"
TR "a[202:203],v0 offset:512\n"
MF "v[224:227],a[212:215],a[104:107],v[220:223]\n"
"ds_read_b128 a[192:195],v3 offset:26624\n"
MF "v[224:227],a[208:211],a[64:67],v[224:227]\n"
TR "a[204:205],v0 offset:4096\n"
MF "v[232:235],a[208:211],a[68:71],v[220:223]\n"
TR "a[206:207],v0 offset:4608\n"
MF "v[232:235],a[212:215],a[108:111],v[232:235]\n"
LD "v11,s[28:31],s82 offen offset:1024 lds\n"
MF "v[240:243],a[212:215],a[112:115],v[220:223]\n"
"ds_read_b128 a[156:159],v2 offset:28672\n"
MF "v[240:243],a[208:211],a[72:75],v[240:243]\n"
"ds_read_b128 a[196:199],v3 offset:28672\n"
MF "v[248:251],a[208:211],a[76:79],v[220:223]\n"
MF "v[248:251],a[212:215],a[116:119],v[248:251]\n"
LD "v12,s[28:31],s82 offen offset:2048 lds\n"
MF "v[252:255],a[220:223],a[116:119],v[220:223]\n"
TR "a[208:209],v0 offset:8\n"
MF "v[252:255],a[216:219],a[76:79],v[252:255]\n"
TR "a[210:211],v0 offset:520\n"
MF "v[244:247],a[216:219],a[72:75],v[220:223]\n"
MF "v[244:247],a[220:223],a[112:115],v[244:247]\n"
TR "a[212:213],v0 offset:4104\n"
MF "v[236:239],a[220:223],a[108:111],v[220:223]\n"
TR "a[214:215],v0 offset:4616\n"
MF "v[236:239],a[216:219],a[68:71],v[236:239]\n"
LD "v13,s[28:31],s82 offen offset:3072 lds\n"
MF "v[228:231],a[216:219],a[64:67],v[220:223]\n"
MF "v[228:231],a[220:223],a[104:107],v[228:231]\n"
"s_add_u32 s84,s14,20480\n"
MF "v[220:223],a[220:223],a[100:103],v[220:223]\n"
"s_add_u32 m0,s63,2048\n"
MF "v[220:223],a[216:219],a[60:63],v[220:223]\n"
"s_waitcnt lgkmcnt(4)\n"
LD "v4,s[24:27],s84 offen lds\n"
MF "v[16:19],a[200:203],a[140:143],v[16:19]\n"
TR "a[216:217],v1\n"
MF "v[16:19],a[204:207],a[180:183],v[16:19]\n"
TR "a[218:219],v1 offset:512\n"
MF "v[24:27],a[204:207],a[184:187],v[24:27]\n"
MF "v[24:27],a[200:203],a[144:147],v[24:27]\n"
TR "a[220:221],v1 offset:4096\n"
MF "v[32:35],a[200:203],a[148:151],v[32:35]\n"
"s_add_u32 s84,s14,29696\n"
TR "a[222:223],v1 offset:4608\n"
MF "v[32:35],a[204:207],a[188:191],v[32:35]\n"
LD "v5,s[24:27],s84 offen offset:1024 lds\n"
MF "v[40:43],a[204:207],a[192:195],v[40:43]\n"
MF "v[40:43],a[200:203],a[152:155],v[40:43]\n"
"s_add_u32 m0,s62,32768\n"
MF "v[48:51],a[200:203],a[156:159],v[48:51]\n"
"s_add_u32 s82,s66,1573120\n"
"s_sub_u32 s15,s82,7864320\n"
"s_min_u32 s82,s82,s15\n"
MF "v[48:51],a[204:207],a[196:199],v[48:51]\n"
"s_waitcnt lgkmcnt(4)\n"
LD "v6,s[28:31],s82 offen lds\n"
MF "v[52:55],a[212:215],a[196:199],v[52:55]\n"
TR "a[200:201],v1 offset:8\n"
MF "v[52:55],a[208:211],a[156:159],v[52:55]\n"
TR "a[202:203],v1 offset:520\n"
MF "v[44:47],a[208:211],a[152:155],v[44:47]\n"
MF "v[44:47],a[212:215],a[192:195],v[44:47]\n"
TR "a[204:205],v1 offset:4104\n"
MF "v[36:39],a[212:215],a[188:191],v[36:39]\n"
TR "a[206:207],v1 offset:4616\n"
MF "v[36:39],a[208:211],a[148:151],v[36:39]\n"
LD "v11,s[28:31],s82 offen offset:1024 lds\n"
MF "v[28:31],a[208:211],a[144:147],v[28:31]\n"
MF "v[28:31],a[212:215],a[184:187],v[28:31]\n"
MF "v[20:23],a[212:215],a[180:183],v[20:23]\n"
"s_waitcnt vmcnt(18) lgkmcnt(0)\n"
LD "v12,s[28:31],s82 offen offset:2048 lds\n"
"s_bitcmp0_b32 s23,12\n"
"s_cbranch_scc1 .Lisx2\n"
"s_mov_b64 exec,0\n"
"s_cmp_eq_u32 s56,0\n"
"s_cbranch_scc1 .Lpg11d\n"
"s_cmp_eq_u32 s56,1\n"
"s_cbranch_scc1 .Lpg10d\n"
"s_cmp_eq_u32 s56,2\n"
"s_cbranch_scc1 .Lpgt0kx_d\n"
"s_branch .Lpg8d\n"
".Lisr2:\n"
"s_mov_b64 exec,-1\n"
".Lisx2:\n"
MF "v[20:23],a[208:211],a[140:143],v[20:23]\n"
"s_barrier\n"
MF "v[56:59],a[216:219],a[140:143],v[56:59]\n"
TR "a[208:209],v0 offset:16384\n"
MF "v[56:59],a[220:223],a[180:183],v[56:59]\n"
TR "a[210:211],v0 offset:16896\n"
MF "v[64:67],a[220:223],a[184:187],v[64:67]\n"
LD "v13,s[28:31],s82 offen offset:3072 lds\n"
MF "v[64:67],a[216:219],a[144:147],v[64:67]\n"
TR "a[212:213],v0 offset:20480\n"
MF "v[72:75],a[216:219],a[148:151],v[72:75]\n"
TR "a[214:215],v0 offset:20992\n"
MF "v[72:75],a[220:223],a[188:191],v[72:75]\n"
"s_add_u32 s84,s14,40960\n"
MF "v[80:83],a[220:223],a[192:195],v[80:83]\n"
"s_add_u32 m0,s63,4096\n"
MF "v[80:83],a[216:219],a[152:155],v[80:83]\n"
LD "v4,s[24:27],s84 offen lds\n"
MF "v[88:91],a[216:219],a[156:159],v[88:91]\n"
MF "v[88:91],a[220:223],a[196:199],v[88:91]\n"
"s_sub_u32 s66,s66,s83\n"
MF "v[92:95],a[204:207],a[196:199],v[92:95]\n"
"s_add_u32 m0,s62,49152\n"
"s_add_u32 s82,s66,2359296\n"
TR "a[216:217],v0 offset:16392\n"
MF "v[92:95],a[200:203],a[156:159],v[92:95]\n"
"s_sub_u32 s15,s82,7864320\n"
"s_min_u32 s82,s82,s15\n"
TR "a[218:219],v0 offset:16904\n"
MF "v[84:87],a[200:203],a[152:155],v[84:87]\n"
LD "v6,s[28:31],s82 offen lds\n"
MF "v[84:87],a[204:207],a[192:195],v[84:87]\n"
TR "a[220:221],v0 offset:20488\n"
MF "v[76:79],a[204:207],a[188:191],v[76:79]\n"
TR "a[222:223],v0 offset:21000\n"
MF "v[76:79],a[200:203],a[148:151],v[76:79]\n"
MF "v[68:71],a[200:203],a[144:147],v[68:71]\n"
MF "v[68:71],a[204:207],a[184:187],v[68:71]\n"
LD "v11,s[28:31],s82 offen offset:1024 lds\n"
MF "v[60:63],a[204:207],a[180:183],v[60:63]\n"
MF "v[60:63],a[200:203],a[140:143],v[60:63]\n"
"s_waitcnt lgkmcnt(4)\n"
MF "v[96:99],a[208:211],a[140:143],v[96:99]\n"
TR "a[200:201],v1 offset:16384\n"
MF "v[96:99],a[212:215],a[180:183],v[96:99]\n"
TR "a[202:203],v1 offset:16896\n"
MF "v[104:107],a[212:215],a[184:187],v[104:107]\n"
LD "v12,s[28:31],s82 offen offset:2048 lds\n"
MF "v[104:107],a[208:211],a[144:147],v[104:107]\n"
TR "a[204:205],v1 offset:20480\n"
MF "v[112:115],a[208:211],a[148:151],v[112:115]\n"
TR "a[206:207],v1 offset:20992\n"
MF "v[112:115],a[212:215],a[188:191],v[112:115]\n"
MF "v[120:123],a[212:215],a[192:195],v[120:123]\n"
MF "v[120:123],a[208:211],a[152:155],v[120:123]\n"
LD "v13,s[28:31],s82 offen offset:3072 lds\n"
MF "v[128:131],a[208:211],a[156:159],v[128:131]\n"
MF "v[128:131],a[212:215],a[196:199],v[128:131]\n"
"s_add_u32 s14,s81,512\n"
"s_sub_u32 s15,s14,1280\n"
"s_waitcnt lgkmcnt(4)\n"
MF "v[132:135],a[220:223],a[196:199],v[132:135]\n"
"s_min_u32 s14,s14,s15\n"
"s_add_u32 s14,s14,s68\n"
TR "a[208:209],v1 offset:16392\n"
MF "v[132:135],a[216:219],a[156:159],v[132:135]\n"
"s_add_u32 s84,s14,0\n"
"s_add_u32 m0,s63,20480\n"
TR "a[210:211],v1 offset:16904\n"
MF "v[124:127],a[216:219],a[152:155],v[124:127]\n"
LD "v4,s[24:27],s84 offen lds\n"
MF "v[124:127],a[220:223],a[192:195],v[124:127]\n"
TR "a[212:213],v1 offset:20488\n"
MF "v[116:119],a[220:223],a[188:191],v[116:119]\n"
TR "a[214:215],v1 offset:21000\n"
MF "v[116:119],a[216:219],a[148:151],v[116:119]\n"
MF "v[108:111],a[216:219],a[144:147],v[108:111]\n"
"s_add_u32 s84,s14,9216\n"
MF "v[108:111],a[220:223],a[184:187],v[108:111]\n"
LD "v5,s[24:27],s84 offen offset:1024 lds\n"
MF "v[100:103],a[220:223],a[180:183],v[100:103]\n"
"s_waitcnt vmcnt(23) lgkmcnt(0)\n"
MF "v[100:103],a[216:219],a[140:143],v[100:103]\n"
"s_barrier\n"
MF "v[136:139],a[200:203],a[140:143],v[136:139]\n"
TR "a[216:217],v0 offset:32768\n"
MF "v[136:139],a[204:207],a[180:183],v[136:139]\n"
"s_add_u32 m0,s62,65536\n"
TR "a[218:219],v0 offset:33280\n"
MF "v[144:147],a[204:207],a[184:187],v[144:147]\n"
"s_add_u32 s82,s66,2359424\n"
"ds_read_b128 a[60:63],v2 offset:40960\n"
MF "v[144:147],a[200:203],a[144:147],v[144:147]\n"
"s_sub_u32 s15,s82,7864320\n"
TR "a[220:221],v0 offset:36864\n"
MF "v[152:155],a[200:203],a[148:151],v[152:155]\n"
"s_min_u32 s82,s82,s15\n"
TR "a[222:223],v0 offset:37376\n"
MF "v[152:155],a[204:207],a[188:191],v[152:155]\n"
LD "v6,s[28:31],s82 offen lds\n"
MF "v[160:163],a[204:207],a[192:195],v[160:163]\n"
"ds_read_b128 a[100:103],v3 offset:40960\n"
MF "v[160:163],a[200:203],a[152:155],v[160:163]\n"
LD "v11,s[28:31],s82 offen offset:1024 lds\n"
MF "v[168:171],a[200:203],a[156:159],v[168:171]\n"
MF "v[168:171],a[204:207],a[196:199],v[168:171]\n"
LD "v12,s[28:31],s82 offen offset:2048 lds\n"
MF "v[172:175],a[212:215],a[196:199],v[172:175]\n"
TR "a[200:201],v0 offset:32776\n"
MF "v[172:175],a[208:211],a[156:159],v[172:175]\n"
TR "a[202:203],v0 offset:33288\n"
MF "v[164:167],a[208:211],a[152:155],v[164:167]\n"
"ds_read_b128 a[64:67],v2 offset:43008\n"
MF "v[164:167],a[212:215],a[192:195],v[164:167]\n"
TR "a[204:205],v0 offset:36872\n"
MF "v[156:159],a[212:215],a[188:191],v[156:159]\n"
TR "a[206:207],v0 offset:37384\n"
MF "v[156:159],a[208:211],a[148:151],v[156:159]\n"
MF "v[148:151],a[208:211],a[144:147],v[148:151]\n"
"ds_read_b128 a[104:107],v3 offset:43008\n"
MF "v[148:151],a[212:215],a[184:187],v[148:151]\n"
MF "v[140:143],a[212:215],a[180:183],v[140:143]\n"
LD "v13,s[28:31],s82 offen offset:3072 lds\n"
MF "v[140:143],a[208:211],a[140:143],v[140:143]\n"
"s_waitcnt lgkmcnt(7)\n"
MF "v[176:179],a[216:219],a[140:143],v[176:179]\n"
TR "a[208:209],v1 offset:32768\n"
MF "v[176:179],a[220:223],a[180:183],v[176:179]\n"
TR "a[210:211],v1 offset:33280\n"
MF "v[184:187],a[220:223],a[184:187],v[184:187]\n"
"ds_read_b128 a[68:71],v2 offset:45056\n"
MF "v[184:187],a[216:219],a[144:147],v[184:187]\n"
"s_add_u32 s84,s14,20480\n"
TR "a[212:213],v1 offset:36864\n"
MF "v[192:195],a[216:219],a[148:151],v[192:195]\n"
"s_add_u32 m0,s63,22528\n"
TR "a[214:215],v1 offset:37376\n"
MF "v[192:195],a[220:223],a[188:191],v[192:195]\n"
LD "v4,s[24:27],s84 offen lds\n"
MF "v[200:203],a[220:223],a[192:195],v[200:203]\n"
"ds_read_b128 a[108:111],v3 offset:45056\n"
MF "v[200:203],a[216:219],a[152:155],v[200:203]\n"
MF "v[208:211],a[216:219],a[156:159],v[208:211]\n"
"s_add_u32 s84,s14,29696\n"
MF "v[208:211],a[220:223],a[196:199],v[208:211]\n"
"s_waitcnt lgkmcnt(7)\n"
LD "v5,s[24:27],s84 offen offset:1024 lds\n"
MF "v[212:215],a[204:207],a[196:199],v[212:215]\n"
TR "a[216:217],v1 offset:32776\n"
MF "v[212:215],a[200:203],a[156:159],v[212:215]\n"
TR "a[218:219],v1 offset:33288\n"
MF "v[204:207],a[200:203],a[152:155],v[204:207]\n"
"ds_read_b128 a[72:75],v2 offset:47104\n"
MF "v[204:207],a[204:207],a[192:195],v[204:207]\n"
TR "a[220:221],v1 offset:36872\n"
MF "v[196:199],a[204:207],a[188:191],v[196:199]\n"
TR "a[222:223],v1 offset:37384\n"
MF "v[196:199],a[200:203],a[148:151],v[196:199]\n"
MF "v[188:191],a[200:203],a[144:147],v[188:191]\n"
"v_add_u32 v0,0xffff4000,v0\n"
MF "v[188:191],a[204:207],a[184:187],v[188:191]\n"
"v_add_u32 v1,0xffff4000,v1\n"
MF "v[180:183],a[204:207],a[180:183],v[180:183]\n"
"s_waitcnt vmcnt(25) lgkmcnt(0)\n"
MF "v[180:183],a[200:203],a[140:143],v[180:183]\n"
"s_barrier\n"
MF "v[216:219],a[208:211],a[140:143],v[216:219]\n"
TR "a[200:201],v0\n"
MF "v[216:219],a[212:215],a[180:183],v[216:219]\n"
"s_add_u32 m0,s62,81920\n"
TR "a[202:203],v0 offset:512\n"
MF "v[224:227],a[212:215],a[184:187],v[224:227]\n"
"s_add_u32 s82,s66,2359552\n"
"ds_read_b128 a[112:115],v3 offset:47104\n"
MF "v[224:227],a[208:211],a[144:147],v[224:227]\n"
"s_sub_u32 s15,s82,7864320\n"
TR "a[204:205],v0 offset:4096\n"
MF "v[232:235],a[208:211],a[148:151],v[232:235]\n"
"s_min_u32 s82,s82,s15\n"
TR "a[206:207],v0 offset:4608\n"
MF "v[232:235],a[212:215],a[188:191],v[232:235]\n"
LD "v6,s[28:31],s82 offen lds\n"
MF "v[240:243],a[212:215],a[192:195],v[240:243]\n"
"ds_read_b128 a[76:79],v2 offset:49152\n"
MF "v[240:243],a[208:211],a[152:155],v[240:243]\n"
"ds_read_b128 a[116:119],v3 offset:49152\n"
MF "v[248:251],a[208:211],a[156:159],v[248:251]\n"
LD "v11,s[28:31],s82 offen offset:1024 lds\n"
MF "v[248:251],a[212:215],a[196:199],v[248:251]\n"
MF "v[252:255],a[220:223],a[196:199],v[252:255]\n"
TR "a[208:209],v0 offset:8\n"
MF "v[252:255],a[216:219],a[156:159],v[252:255]\n"
TR "a[210:211],v0 offset:520\n"
MF "v[244:247],a[216:219],a[152:155],v[244:247]\n"
MF "v[244:247],a[220:223],a[192:195],v[244:247]\n"
TR "a[212:213],v0 offset:4104\n"
MF "v[236:239],a[220:223],a[188:191],v[236:239]\n"
TR "a[214:215],v0 offset:4616\n"
MF "v[236:239],a[216:219],a[148:151],v[236:239]\n"
LD "v12,s[28:31],s82 offen offset:2048 lds\n"
MF "v[228:231],a[216:219],a[144:147],v[228:231]\n"
MF "v[228:231],a[220:223],a[184:187],v[228:231]\n"
MF "v[220:223],a[220:223],a[180:183],v[220:223]\n"
MF "v[220:223],a[216:219],a[140:143],v[220:223]\n"
"s_waitcnt lgkmcnt(4)\n"
MF "v[16:19],a[200:203],a[60:63],v[16:19]\n"
TR "a[216:217],v1\n"
MF "v[16:19],a[204:207],a[100:103],v[16:19]\n"
TR "a[218:219],v1 offset:512\n"
MF "v[24:27],a[204:207],a[104:107],v[24:27]\n"
LD "v13,s[28:31],s82 offen offset:3072 lds\n"
MF "v[24:27],a[200:203],a[64:67],v[24:27]\n"
TR "a[220:221],v1 offset:4096\n"
MF "v[32:35],a[200:203],a[68:71],v[32:35]\n"
TR "a[222:223],v1 offset:4608\n"
MF "v[32:35],a[204:207],a[108:111],v[32:35]\n"
MF "v[40:43],a[204:207],a[112:115],v[40:43]\n"
MF "v[40:43],a[200:203],a[72:75],v[40:43]\n"
"s_add_u32 s84,s14,40960\n"
MF "v[48:51],a[200:203],a[76:79],v[48:51]\n"
"s_add_u32 m0,s63,24576\n"
MF "v[48:51],a[204:207],a[116:119],v[48:51]\n"
"s_waitcnt lgkmcnt(4)\n"
LD "v4,s[24:27],s84 offen lds\n"
MF "v[52:55],a[212:215],a[116:119],v[52:55]\n"
TR "a[200:201],v1 offset:8\n"
MF "v[52:55],a[208:211],a[76:79],v[52:55]\n"
TR "a[202:203],v1 offset:520\n"
MF "v[44:47],a[208:211],a[72:75],v[44:47]\n"
MF "v[44:47],a[212:215],a[112:115],v[44:47]\n"
TR "a[204:205],v1 offset:4104\n"
MF "v[36:39],a[212:215],a[108:111],v[36:39]\n"
TR "a[206:207],v1 offset:4616\n"
MF "v[36:39],a[208:211],a[68:71],v[36:39]\n"
MF "v[28:31],a[208:211],a[64:67],v[28:31]\n"
MF "v[28:31],a[212:215],a[104:107],v[28:31]\n"
MF "v[20:23],a[212:215],a[100:103],v[20:23]\n"
"s_waitcnt vmcnt(24) lgkmcnt(0)\n"
MF "v[20:23],a[208:211],a[60:63],v[20:23]\n"
"s_barrier\n"
MF "v[56:59],a[216:219],a[60:63],v[56:59]\n"
"s_add_u32 m0,s62,0\n"
"s_add_u32 s82,s66,3145728\n"
TR "a[208:209],v0 offset:16384\n"
MF "v[56:59],a[220:223],a[100:103],v[56:59]\n"
"s_sub_u32 s15,s82,7864320\n"
"s_min_u32 s82,s82,s15\n"
TR "a[210:211],v0 offset:16896\n"
MF "v[64:67],a[220:223],a[104:107],v[64:67]\n"
LD "v6,s[28:31],s82 offen lds\n"
MF "v[64:67],a[216:219],a[64:67],v[64:67]\n"
TR "a[212:213],v0 offset:20480\n"
MF "v[72:75],a[216:219],a[68:71],v[72:75]\n"
TR "a[214:215],v0 offset:20992\n"
MF "v[72:75],a[220:223],a[108:111],v[72:75]\n"
MF "v[80:83],a[220:223],a[112:115],v[80:83]\n"
LD "v11,s[28:31],s82 offen offset:1024 lds\n"
MF "v[80:83],a[216:219],a[72:75],v[80:83]\n"
MF "v[88:91],a[216:219],a[76:79],v[88:91]\n"
MF "v[88:91],a[220:223],a[116:119],v[88:91]\n"
MF "v[92:95],a[204:207],a[116:119],v[92:95]\n"
TR "a[216:217],v0 offset:16392\n"
MF "v[92:95],a[200:203],a[76:79],v[92:95]\n"
TR "a[218:219],v0 offset:16904\n"
MF "v[84:87],a[200:203],a[72:75],v[84:87]\n"
LD "v12,s[28:31],s82 offen offset:2048 lds\n"
MF "v[84:87],a[204:207],a[112:115],v[84:87]\n"
TR "a[220:221],v0 offset:20488\n"
MF "v[76:79],a[204:207],a[108:111],v[76:79]\n"
TR "a[222:223],v0 offset:21000\n"
MF "v[76:79],a[200:203],a[68:71],v[76:79]\n"
MF "v[68:71],a[200:203],a[64:67],v[68:71]\n"
MF "v[68:71],a[204:207],a[104:107],v[68:71]\n"
MF "v[60:63],a[204:207],a[100:103],v[60:63]\n"
LD "v13,s[28:31],s82 offen offset:3072 lds\n"
MF "v[60:63],a[200:203],a[60:63],v[60:63]\n"
"s_waitcnt lgkmcnt(4)\n"
MF "v[96:99],a[208:211],a[60:63],v[96:99]\n"
TR "a[200:201],v1 offset:16384\n"
MF "v[96:99],a[212:215],a[100:103],v[96:99]\n"
TR "a[202:203],v1 offset:16896\n"
MF "v[104:107],a[212:215],a[104:107],v[104:107]\n"
"s_add_u32 s14,s81,640\n"
"s_sub_u32 s15,s14,1280\n"
MF "v[104:107],a[208:211],a[64:67],v[104:107]\n"
"s_min_u32 s14,s14,s15\n"
"s_add_u32 s14,s14,s68\n"
TR "a[204:205],v1 offset:20480\n"
MF "v[112:115],a[208:211],a[68:71],v[112:115]\n"
"s_add_u32 s84,s14,0\n"
"s_add_u32 m0,s63,40960\n"
TR "a[206:207],v1 offset:20992\n"
MF "v[112:115],a[212:215],a[108:111],v[112:115]\n"
LD "v4,s[24:27],s84 offen lds\n"
MF "v[120:123],a[212:215],a[112:115],v[120:123]\n"
MF "v[120:123],a[208:211],a[72:75],v[120:123]\n"
MF "v[128:131],a[208:211],a[76:79],v[128:131]\n"
"s_add_u32 s84,s14,9216\n"
MF "v[128:131],a[212:215],a[116:119],v[128:131]\n"
"s_waitcnt lgkmcnt(4)\n"
LD "v5,s[24:27],s84 offen offset:1024 lds\n"
MF "v[132:135],a[220:223],a[116:119],v[132:135]\n"
TR "a[208:209],v1 offset:16392\n"
MF "v[132:135],a[216:219],a[76:79],v[132:135]\n"
TR "a[210:211],v1 offset:16904\n"
MF "v[124:127],a[216:219],a[72:75],v[124:127]\n"
MF "v[124:127],a[220:223],a[112:115],v[124:127]\n"
TR "a[212:213],v1 offset:20488\n"
MF "v[116:119],a[220:223],a[108:111],v[116:119]\n"
TR "a[214:215],v1 offset:21000\n"
MF "v[116:119],a[216:219],a[68:71],v[116:119]\n"
MF "v[108:111],a[216:219],a[64:67],v[108:111]\n"
MF "v[108:111],a[220:223],a[104:107],v[108:111]\n"
MF "v[100:103],a[220:223],a[100:103],v[100:103]\n"
"s_waitcnt vmcnt(23) lgkmcnt(0)\n"
MF "v[100:103],a[216:219],a[60:63],v[100:103]\n"
"s_barrier\n"
MF "v[136:139],a[200:203],a[60:63],v[136:139]\n"
TR "a[216:217],v0 offset:32768\n"
MF "v[136:139],a[204:207],a[100:103],v[136:139]\n"
"s_add_u32 m0,s62,16384\n"
TR "a[218:219],v0 offset:33280\n"
MF "v[144:147],a[204:207],a[104:107],v[144:147]\n"
"s_add_u32 s82,s66,3145856\n"
"ds_read_b128 a[140:143],v2\n"
MF "v[144:147],a[200:203],a[64:67],v[144:147]\n"
"s_sub_u32 s15,s82,7864320\n"
TR "a[220:221],v0 offset:36864\n"
MF "v[152:155],a[200:203],a[68:71],v[152:155]\n"
"s_min_u32 s82,s82,s15\n"
TR "a[222:223],v0 offset:37376\n"
MF "v[152:155],a[204:207],a[108:111],v[152:155]\n"
LD "v6,s[28:31],s82 offen lds\n"
MF "v[160:163],a[204:207],a[112:115],v[160:163]\n"
"ds_read_b128 a[180:183],v3\n"
MF "v[160:163],a[200:203],a[72:75],v[160:163]\n"
LD "v11,s[28:31],s82 offen offset:1024 lds\n"
MF "v[168:171],a[200:203],a[76:79],v[168:171]\n"
MF "v[168:171],a[204:207],a[116:119],v[168:171]\n"
LD "v12,s[28:31],s82 offen offset:2048 lds\n"
MF "v[172:175],a[212:215],a[116:119],v[172:175]\n"
TR "a[200:201],v0 offset:32776\n"
MF "v[172:175],a[208:211],a[76:79],v[172:175]\n"
TR "a[202:203],v0 offset:33288\n"
MF "v[164:167],a[208:211],a[72:75],v[164:167]\n"
"ds_read_b128 a[144:147],v2 offset:2048\n"
MF "v[164:167],a[212:215],a[112:115],v[164:167]\n"
TR "a[204:205],v0 offset:36872\n"
MF "v[156:159],a[212:215],a[108:111],v[156:159]\n"
TR "a[206:207],v0 offset:37384\n"
MF "v[156:159],a[208:211],a[68:71],v[156:159]\n"
MF "v[148:151],a[208:211],a[64:67],v[148:151]\n"
"ds_read_b128 a[184:187],v3 offset:2048\n"
MF "v[148:151],a[212:215],a[104:107],v[148:151]\n"
MF "v[140:143],a[212:215],a[100:103],v[140:143]\n"
LD "v13,s[28:31],s82 offen offset:3072 lds\n"
MF "v[140:143],a[208:211],a[60:63],v[140:143]\n"
"s_waitcnt lgkmcnt(7)\n"
MF "v[176:179],a[216:219],a[60:63],v[176:179]\n"
TR "a[208:209],v1 offset:32768\n"
MF "v[176:179],a[220:223],a[100:103],v[176:179]\n"
TR "a[210:211],v1 offset:33280\n"
MF "v[184:187],a[220:223],a[104:107],v[184:187]\n"
"ds_read_b128 a[148:151],v2 offset:4096\n"
MF "v[184:187],a[216:219],a[64:67],v[184:187]\n"
"s_add_u32 s84,s14,20480\n"
TR "a[212:213],v1 offset:36864\n"
MF "v[192:195],a[216:219],a[68:71],v[192:195]\n"
"s_add_u32 m0,s63,43008\n"
TR "a[214:215],v1 offset:37376\n"
MF "v[192:195],a[220:223],a[108:111],v[192:195]\n"
LD "v4,s[24:27],s84 offen lds\n"
MF "v[200:203],a[220:223],a[112:115],v[200:203]\n"
"ds_read_b128 a[188:191],v3 offset:4096\n"
MF "v[200:203],a[216:219],a[72:75],v[200:203]\n"
MF "v[208:211],a[216:219],a[76:79],v[208:211]\n"
"s_add_u32 s84,s14,29696\n"
MF "v[208:211],a[220:223],a[116:119],v[208:211]\n"
"s_waitcnt lgkmcnt(7)\n"
LD "v5,s[24:27],s84 offen offset:1024 lds\n"
MF "v[212:215],a[204:207],a[116:119],v[212:215]\n"
TR "a[216:217],v1 offset:32776\n"
MF "v[212:215],a[200:203],a[76:79],v[212:215]\n"
TR "a[218:219],v1 offset:33288\n"
MF "v[204:207],a[200:203],a[72:75],v[204:207]\n"
"ds_read_b128 a[152:155],v2 offset:6144\n"
MF "v[204:207],a[204:207],a[112:115],v[204:207]\n"
TR "a[220:221],v1 offset:36872\n"
MF "v[196:199],a[204:207],a[108:111],v[196:199]\n"
TR "a[222:223],v1 offset:37384\n"
MF "v[196:199],a[200:203],a[68:71],v[196:199]\n"
MF "v[188:191],a[200:203],a[64:67],v[188:191]\n"
"v_add_u32 v0,0xc000,v0\n"
MF "v[188:191],a[204:207],a[104:107],v[188:191]\n"
"v_add_u32 v1,0xc000,v1\n"
MF "v[180:183],a[204:207],a[100:103],v[180:183]\n"
"s_waitcnt vmcnt(25) lgkmcnt(0)\n"
MF "v[180:183],a[200:203],a[60:63],v[180:183]\n"
"s_barrier\n"
MF "v[216:219],a[208:211],a[60:63],v[216:219]\n"
TR "a[200:201],v0\n"
MF "v[216:219],a[212:215],a[100:103],v[216:219]\n"
"s_add_u32 m0,s62,32768\n"
TR "a[202:203],v0 offset:512\n"
MF "v[224:227],a[212:215],a[104:107],v[224:227]\n"
".Lpg5c:\n"
"s_cbranch_execz .Lpg15c\n"
".Lpg5f:\n"
"s_cbranch_execz .LpgattB_f\n"
"s_add_u32 s82,s66,3145984\n"
"ds_read_b128 a[192:195],v3 offset:6144\n"
MF "v[224:227],a[208:211],a[64:67],v[224:227]\n"
"s_sub_u32 s15,s82,7864320\n"
TR "a[204:205],v0 offset:4096\n"
MF "v[232:235],a[208:211],a[68:71],v[232:235]\n"
"s_min_u32 s82,s82,s15\n"
TR "a[206:207],v0 offset:4608\n"
MF "v[232:235],a[212:215],a[108:111],v[232:235]\n"
LD "v6,s[28:31],s82 offen lds\n"
MF "v[240:243],a[212:215],a[112:115],v[240:243]\n"
"ds_read_b128 a[156:159],v2 offset:8192\n"
MF "v[240:243],a[208:211],a[72:75],v[240:243]\n"
"ds_read_b128 a[196:199],v3 offset:8192\n"
MF "v[248:251],a[208:211],a[76:79],v[248:251]\n"
LD "v11,s[28:31],s82 offen offset:1024 lds\n"
MF "v[248:251],a[212:215],a[116:119],v[248:251]\n"
MF "v[252:255],a[220:223],a[116:119],v[252:255]\n"
TR "a[208:209],v0 offset:8\n"
MF "v[252:255],a[216:219],a[76:79],v[252:255]\n"
TR "a[210:211],v0 offset:520\n"
MF "v[244:247],a[216:219],a[72:75],v[244:247]\n"
MF "v[244:247],a[220:223],a[112:115],v[244:247]\n"
TR "a[212:213],v0 offset:4104\n"
MF "v[236:239],a[220:223],a[108:111],v[236:239]\n"
TR "a[214:215],v0 offset:4616\n"
MF "v[236:239],a[216:219],a[68:71],v[236:239]\n"
LD "v12,s[28:31],s82 offen offset:2048 lds\n"
MF "v[228:231],a[216:219],a[64:67],v[228:231]\n"
MF "v[228:231],a[220:223],a[104:107],v[228:231]\n"
MF "v[220:223],a[220:223],a[100:103],v[220:223]\n"
MF "v[220:223],a[216:219],a[60:63],v[220:223]\n"
"s_waitcnt lgkmcnt(4)\n"
MF "v[16:19],a[200:203],a[140:143],v[16:19]\n"
TR "a[216:217],v1\n"
MF "v[16:19],a[204:207],a[180:183],v[16:19]\n"
TR "a[218:219],v1 offset:512\n"
MF "v[24:27],a[204:207],a[184:187],v[24:27]\n"
LD "v13,s[28:31],s82 offen offset:3072 lds\n"
MF "v[24:27],a[200:203],a[144:147],v[24:27]\n"
TR "a[220:221],v1 offset:4096\n"
MF "v[32:35],a[200:203],a[148:151],v[32:35]\n"
TR "a[222:223],v1 offset:4608\n"
MF "v[32:35],a[204:207],a[188:191],v[32:35]\n"
MF "v[40:43],a[204:207],a[192:195],v[40:43]\n"
MF "v[40:43],a[200:203],a[152:155],v[40:43]\n"
"s_add_u32 s84,s14,40960\n"
MF "v[48:51],a[200:203],a[156:159],v[48:51]\n"
"s_add_u32 m0,s63,45056\n"
MF "v[48:51],a[204:207],a[196:199],v[48:51]\n"
"s_waitcnt lgkmcnt(4)\n"
LD "v4,s[24:27],s84 offen lds\n"
MF "v[52:55],a[212:215],a[196:199],v[52:55]\n"
TR "a[200:201],v1 offset:8\n"
MF "v[52:55],a[208:211],a[156:159],v[52:55]\n"
TR "a[202:203],v1 offset:520\n"
MF "v[44:47],a[208:211],a[152:155],v[44:47]\n"
MF "v[44:47],a[212:215],a[192:195],v[44:47]\n"
TR "a[204:205],v1 offset:4104\n"
MF "v[36:39],a[212:215],a[188:191],v[36:39]\n"
TR "a[206:207],v1 offset:4616\n"
MF "v[36:39],a[208:211],a[148:151],v[36:39]\n"
MF "v[28:31],a[208:211],a[144:147],v[28:31]\n"
MF "v[28:31],a[212:215],a[184:187],v[28:31]\n"
MF "v[20:23],a[212:215],a[180:183],v[20:23]\n"
"s_waitcnt vmcnt(24) lgkmcnt(0)\n"
MF "v[20:23],a[208:211],a[140:143],v[20:23]\n"
"s_barrier\n"
MF "v[56:59],a[216:219],a[140:143],v[56:59]\n"
"s_add_u32 m0,s62,49152\n"
"s_add_u32 s82,s66,3932160\n"
TR "a[208:209],v0 offset:16384\n"
MF "v[56:59],a[220:223],a[180:183],v[56:59]\n"
"s_sub_u32 s15,s82,7864320\n"
"s_min_u32 s82,s82,s15\n"
TR "a[210:211],v0 offset:16896\n"
MF "v[64:67],a[220:223],a[184:187],v[64:67]\n"
LD "v6,s[28:31],s82 offen lds\n"
MF "v[64:67],a[216:219],a[144:147],v[64:67]\n"
TR "a[212:213],v0 offset:20480\n"
MF "v[72:75],a[216:219],a[148:151],v[72:75]\n"
TR "a[214:215],v0 offset:20992\n"
MF "v[72:75],a[220:223],a[188:191],v[72:75]\n"
MF "v[80:83],a[220:223],a[192:195],v[80:83]\n"
LD "v11,s[28:31],s82 offen offset:1024 lds\n"
MF "v[80:83],a[216:219],a[152:155],v[80:83]\n"
MF "v[88:91],a[216:219],a[156:159],v[88:91]\n"
MF "v[88:91],a[220:223],a[196:199],v[88:91]\n"
MF "v[92:95],a[204:207],a[196:199],v[92:95]\n"
TR "a[216:217],v0 offset:16392\n"
MF "v[92:95],a[200:203],a[156:159],v[92:95]\n"
TR "a[218:219],v0 offset:16904\n"
MF "v[84:87],a[200:203],a[152:155],v[84:87]\n"
LD "v12,s[28:31],s82 offen offset:2048 lds\n"
MF "v[84:87],a[204:207],a[192:195],v[84:87]\n"
TR "a[220:221],v0 offset:20488\n"
MF "v[76:79],a[204:207],a[188:191],v[76:79]\n"
TR "a[222:223],v0 offset:21000\n"
MF "v[76:79],a[200:203],a[148:151],v[76:79]\n"
MF "v[68:71],a[200:203],a[144:147],v[68:71]\n"
MF "v[68:71],a[204:207],a[184:187],v[68:71]\n"
MF "v[60:63],a[204:207],a[180:183],v[60:63]\n"
LD "v13,s[28:31],s82 offen offset:3072 lds\n"
MF "v[60:63],a[200:203],a[140:143],v[60:63]\n"
"s_waitcnt lgkmcnt(4)\n"
MF "v[96:99],a[208:211],a[140:143],v[96:99]\n"
TR "a[200:201],v1 offset:16384\n"
MF "v[96:99],a[212:215],a[180:183],v[96:99]\n"
TR "a[202:203],v1 offset:16896\n"
MF "v[104:107],a[212:215],a[184:187],v[104:107]\n"
"s_add_u32 s14,s81,768\n"
"s_sub_u32 s15,s14,1280\n"
MF "v[104:107],a[208:211],a[144:147],v[104:107]\n"
"s_min_u32 s14,s14,s15\n"
"s_add_u32 s14,s14,s68\n"
TR "a[204:205],v1 offset:20480\n"
MF "v[112:115],a[208:211],a[148:151],v[112:115]\n"
"s_add_u32 s84,s14,0\n"
"s_add_u32 m0,s63,0\n"
TR "a[206:207],v1 offset:20992\n"
MF "v[112:115],a[212:215],a[188:191],v[112:115]\n"
LD "v4,s[24:27],s84 offen lds\n"
MF "v[120:123],a[212:215],a[192:195],v[120:123]\n"
MF "v[120:123],a[208:211],a[152:155],v[120:123]\n"
MF "v[128:131],a[208:211],a[156:159],v[128:131]\n"
"s_add_u32 s84,s14,9216\n"
MF "v[128:131],a[212:215],a[196:199],v[128:131]\n"
"s_waitcnt lgkmcnt(4)\n"
LD "v5,s[24:27],s84 offen offset:1024 lds\n"
MF "v[132:135],a[220:223],a[196:199],v[132:135]\n"
TR "a[208:209],v1 offset:16392\n"
MF "v[132:135],a[216:219],a[156:159],v[132:135]\n"
TR "a[210:211],v1 offset:16904\n"
MF "v[124:127],a[216:219],a[152:155],v[124:127]\n"
MF "v[124:127],a[220:223],a[192:195],v[124:127]\n"
TR "a[212:213],v1 offset:20488\n"
MF "v[116:119],a[220:223],a[188:191],v[116:119]\n"
TR "a[214:215],v1 offset:21000\n"
MF "v[116:119],a[216:219],a[148:151],v[116:119]\n"
MF "v[108:111],a[216:219],a[144:147],v[108:111]\n"
MF "v[108:111],a[220:223],a[184:187],v[108:111]\n"
MF "v[100:103],a[220:223],a[180:183],v[100:103]\n"
"s_waitcnt vmcnt(23) lgkmcnt(0)\n"
MF "v[100:103],a[216:219],a[140:143],v[100:103]\n"
"s_barrier\n"
MF "v[136:139],a[200:203],a[140:143],v[136:139]\n"
TR "a[216:217],v0 offset:32768\n"
MF "v[136:139],a[204:207],a[180:183],v[136:139]\n"
"s_add_u32 m0,s62,65536\n"
TR "a[218:219],v0 offset:33280\n"
MF "v[144:147],a[204:207],a[184:187],v[144:147]\n"
"s_add_u32 s82,s66,3932288\n"
"ds_read_b128 a[60:63],v2 offset:20480\n"
MF "v[144:147],a[200:203],a[144:147],v[144:147]\n"
"s_sub_u32 s15,s82,7864320\n"
TR "a[220:221],v0 offset:36864\n"
MF "v[152:155],a[200:203],a[148:151],v[152:155]\n"
"s_min_u32 s82,s82,s15\n"
TR "a[222:223],v0 offset:37376\n"
MF "v[152:155],a[204:207],a[188:191],v[152:155]\n"
LD "v6,s[28:31],s82 offen lds\n"
MF "v[160:163],a[204:207],a[192:195],v[160:163]\n"
"ds_read_b128 a[100:103],v3 offset:20480\n"
MF "v[160:163],a[200:203],a[152:155],v[160:163]\n"
LD "v11,s[28:31],s82 offen offset:1024 lds\n"
MF "v[168:171],a[200:203],a[156:159],v[168:171]\n"
MF "v[168:171],a[204:207],a[196:199],v[168:171]\n"
LD "v12,s[28:31],s82 offen offset:2048 lds\n"
MF "v[172:175],a[212:215],a[196:199],v[172:175]\n"
TR "a[200:201],v0 offset:32776\n"
MF "v[172:175],a[208:211],a[156:159],v[172:175]\n"
TR "a[202:203],v0 offset:33288\n"
MF "v[164:167],a[208:211],a[152:155],v[164:167]\n"
"ds_read_b128 a[64:67],v2 offset:22528\n"
MF "v[164:167],a[212:215],a[192:195],v[164:167]\n"
TR "a[204:205],v0 offset:36872\n"
MF "v[156:159],a[212:215],a[188:191],v[156:159]\n"
TR "a[206:207],v0 offset:37384\n"
MF "v[156:159],a[208:211],a[148:151],v[156:159]\n"
MF "v[148:151],a[208:211],a[144:147],v[148:151]\n"
"ds_read_b128 a[104:107],v3 offset:22528\n"
MF "v[148:151],a[212:215],a[184:187],v[148:151]\n"
MF "v[140:143],a[212:215],a[180:183],v[140:143]\n"
LD "v13,s[28:31],s82 offen offset:3072 lds\n"
MF "v[140:143],a[208:211],a[140:143],v[140:143]\n"
"s_waitcnt lgkmcnt(7)\n"
MF "v[176:179],a[216:219],a[140:143],v[176:179]\n"
TR "a[208:209],v1 offset:32768\n"
MF "v[176:179],a[220:223],a[180:183],v[176:179]\n"
TR "a[210:211],v1 offset:33280\n"
MF "v[184:187],a[220:223],a[184:187],v[184:187]\n"
"ds_read_b128 a[68:71],v2 offset:24576\n"
MF "v[184:187],a[216:219],a[144:147],v[184:187]\n"
"s_add_u32 s84,s14,20480\n"
TR "a[212:213],v1 offset:36864\n"
MF "v[192:195],a[216:219],a[148:151],v[192:195]\n"
"s_add_u32 m0,s63,2048\n"
TR "a[214:215],v1 offset:37376\n"
MF "v[192:195],a[220:223],a[188:191],v[192:195]\n"
LD "v4,s[24:27],s84 offen lds\n"
MF "v[200:203],a[220:223],a[192:195],v[200:203]\n"
"ds_read_b128 a[108:111],v3 offset:24576\n"
MF "v[200:203],a[216:219],a[152:155],v[200:203]\n"
MF "v[208:211],a[216:219],a[156:159],v[208:211]\n"
"s_add_u32 s84,s14,29696\n"
MF "v[208:211],a[220:223],a[196:199],v[208:211]\n"
"s_waitcnt lgkmcnt(7)\n"
LD "v5,s[24:27],s84 offen offset:1024 lds\n"
MF "v[212:215],a[204:207],a[196:199],v[212:215]\n"
TR "a[216:217],v1 offset:32776\n"
MF "v[212:215],a[200:203],a[156:159],v[212:215]\n"
TR "a[218:219],v1 offset:33288\n"
MF "v[204:207],a[200:203],a[152:155],v[204:207]\n"
"ds_read_b128 a[72:75],v2 offset:26624\n"
MF "v[204:207],a[204:207],a[192:195],v[204:207]\n"
TR "a[220:221],v1 offset:36872\n"
MF "v[196:199],a[204:207],a[188:191],v[196:199]\n"
TR "a[222:223],v1 offset:37384\n"
MF "v[196:199],a[200:203],a[148:151],v[196:199]\n"
MF "v[188:191],a[200:203],a[144:147],v[188:191]\n"
"v_add_u32 v0,0xffff4000,v0\n"
MF "v[188:191],a[204:207],a[184:187],v[188:191]\n"
"v_add_u32 v1,0xffff4000,v1\n"
MF "v[180:183],a[204:207],a[180:183],v[180:183]\n"
"s_waitcnt vmcnt(25) lgkmcnt(0)\n"
MF "v[180:183],a[200:203],a[140:143],v[180:183]\n"
"s_barrier\n"
MF "v[216:219],a[208:211],a[140:143],v[216:219]\n"
TR "a[200:201],v0\n"
MF "v[216:219],a[212:215],a[180:183],v[216:219]\n"
"s_add_u32 m0,s62,81920\n"
TR "a[202:203],v0 offset:512\n"
MF "v[224:227],a[212:215],a[184:187],v[224:227]\n"
"s_add_u32 s82,s66,3932416\n"
"ds_read_b128 a[112:115],v3 offset:26624\n"
MF "v[224:227],a[208:211],a[144:147],v[224:227]\n"
"s_sub_u32 s15,s82,7864320\n"
TR "a[204:205],v0 offset:4096\n"
MF "v[232:235],a[208:211],a[148:151],v[232:235]\n"
"s_min_u32 s82,s82,s15\n"
TR "a[206:207],v0 offset:4608\n"
MF "v[232:235],a[212:215],a[188:191],v[232:235]\n"
LD "v6,s[28:31],s82 offen lds\n"
MF "v[240:243],a[212:215],a[192:195],v[240:243]\n"
"ds_read_b128 a[76:79],v2 offset:28672\n"
MF "v[240:243],a[208:211],a[152:155],v[240:243]\n"
"ds_read_b128 a[116:119],v3 offset:28672\n"
MF "v[248:251],a[208:211],a[156:159],v[248:251]\n"
LD "v11,s[28:31],s82 offen offset:1024 lds\n"
MF "v[248:251],a[212:215],a[196:199],v[248:251]\n"
MF "v[252:255],a[220:223],a[196:199],v[252:255]\n"
TR "a[208:209],v0 offset:8\n"
MF "v[252:255],a[216:219],a[156:159],v[252:255]\n"
TR "a[210:211],v0 offset:520\n"
MF "v[244:247],a[216:219],a[152:155],v[244:247]\n"
MF "v[244:247],a[220:223],a[192:195],v[244:247]\n"
TR "a[212:213],v0 offset:4104\n"
MF "v[236:239],a[220:223],a[188:191],v[236:239]\n"
TR "a[214:215],v0 offset:4616\n"
MF "v[236:239],a[216:219],a[148:151],v[236:239]\n"
LD "v12,s[28:31],s82 offen offset:2048 lds\n"
MF "v[228:231],a[216:219],a[144:147],v[228:231]\n"
MF "v[228:231],a[220:223],a[184:187],v[228:231]\n"
MF "v[220:223],a[220:223],a[180:183],v[220:223]\n"
MF "v[220:223],a[216:219],a[140:143],v[220:223]\n"
"s_waitcnt lgkmcnt(4)\n"
MF "v[16:19],a[200:203],a[60:63],v[16:19]\n"
TR "a[216:217],v1\n"
MF "v[16:19],a[204:207],a[100:103],v[16:19]\n"
TR "a[218:219],v1 offset:512\n"
MF "v[24:27],a[204:207],a[104:107],v[24:27]\n"
LD "v13,s[28:31],s82 offen offset:3072 lds\n"
MF "v[24:27],a[200:203],a[64:67],v[24:27]\n"
TR "a[220:221],v1 offset:4096\n"
MF "v[32:35],a[200:203],a[68:71],v[32:35]\n"
TR "a[222:223],v1 offset:4608\n"
MF "v[32:35],a[204:207],a[108:111],v[32:35]\n"
MF "v[40:43],a[204:207],a[112:115],v[40:43]\n"
MF "v[40:43],a[200:203],a[72:75],v[40:43]\n"
"s_add_u32 s84,s14,40960\n"
MF "v[48:51],a[200:203],a[76:79],v[48:51]\n"
"s_add_u32 m0,s63,4096\n"
MF "v[48:51],a[204:207],a[116:119],v[48:51]\n"
"s_waitcnt lgkmcnt(4)\n"
LD "v4,s[24:27],s84 offen lds\n"
MF "v[52:55],a[212:215],a[116:119],v[52:55]\n"
TR "a[200:201],v1 offset:8\n"
MF "v[52:55],a[208:211],a[76:79],v[52:55]\n"
TR "a[202:203],v1 offset:520\n"
MF "v[44:47],a[208:211],a[72:75],v[44:47]\n"
MF "v[44:47],a[212:215],a[112:115],v[44:47]\n"
TR "a[204:205],v1 offset:4104\n"
MF "v[36:39],a[212:215],a[108:111],v[36:39]\n"
TR "a[206:207],v1 offset:4616\n"
MF "v[36:39],a[208:211],a[68:71],v[36:39]\n"
MF "v[28:31],a[208:211],a[64:67],v[28:31]\n"
MF "v[28:31],a[212:215],a[104:107],v[28:31]\n"
MF "v[20:23],a[212:215],a[100:103],v[20:23]\n"
"s_waitcnt vmcnt(24) lgkmcnt(0)\n"
MF "v[20:23],a[208:211],a[60:63],v[20:23]\n"
"s_barrier\n"
MF "v[56:59],a[216:219],a[60:63],v[56:59]\n"
"s_add_u32 m0,s62,0\n"
"s_add_u32 s82,s66,4718592\n"
TR "a[208:209],v0 offset:16384\n"
MF "v[56:59],a[220:223],a[100:103],v[56:59]\n"
"s_sub_u32 s15,s82,7864320\n"
"s_min_u32 s82,s82,s15\n"
TR "a[210:211],v0 offset:16896\n"
MF "v[64:67],a[220:223],a[104:107],v[64:67]\n"
LD "v6,s[28:31],s82 offen lds\n"
MF "v[64:67],a[216:219],a[64:67],v[64:67]\n"
TR "a[212:213],v0 offset:20480\n"
MF "v[72:75],a[216:219],a[68:71],v[72:75]\n"
TR "a[214:215],v0 offset:20992\n"
MF "v[72:75],a[220:223],a[108:111],v[72:75]\n"
MF "v[80:83],a[220:223],a[112:115],v[80:83]\n"
LD "v11,s[28:31],s82 offen offset:1024 lds\n"
MF "v[80:83],a[216:219],a[72:75],v[80:83]\n"
MF "v[88:91],a[216:219],a[76:79],v[88:91]\n"
MF "v[88:91],a[220:223],a[116:119],v[88:91]\n"
MF "v[92:95],a[204:207],a[116:119],v[92:95]\n"
TR "a[216:217],v0 offset:16392\n"
MF "v[92:95],a[200:203],a[76:79],v[92:95]\n"
TR "a[218:219],v0 offset:16904\n"
MF "v[84:87],a[200:203],a[72:75],v[84:87]\n"
LD "v12,s[28:31],s82 offen offset:2048 lds\n"
MF "v[84:87],a[204:207],a[112:115],v[84:87]\n"
TR "a[220:221],v0 offset:20488\n"
MF "v[76:79],a[204:207],a[108:111],v[76:79]\n"
TR "a[222:223],v0 offset:21000\n"
MF "v[76:79],a[200:203],a[68:71],v[76:79]\n"
MF "v[68:71],a[200:203],a[64:67],v[68:71]\n"
MF "v[68:71],a[204:207],a[104:107],v[68:71]\n"
MF "v[60:63],a[204:207],a[100:103],v[60:63]\n"
LD "v13,s[28:31],s82 offen offset:3072 lds\n"
MF "v[60:63],a[200:203],a[60:63],v[60:63]\n"
"s_waitcnt lgkmcnt(4)\n"
MF "v[96:99],a[208:211],a[60:63],v[96:99]\n"
TR "a[200:201],v1 offset:16384\n"
MF "v[96:99],a[212:215],a[100:103],v[96:99]\n"
TR "a[202:203],v1 offset:16896\n"
MF "v[104:107],a[212:215],a[104:107],v[104:107]\n"
"s_add_u32 s14,s81,896\n"
"s_sub_u32 s15,s14,1280\n"
MF "v[104:107],a[208:211],a[64:67],v[104:107]\n"
"s_min_u32 s14,s14,s15\n"
"s_add_u32 s14,s14,s68\n"
TR "a[204:205],v1 offset:20480\n"
MF "v[112:115],a[208:211],a[68:71],v[112:115]\n"
"s_add_u32 s84,s14,0\n"
"s_add_u32 m0,s63,20480\n"
TR "a[206:207],v1 offset:20992\n"
MF "v[112:115],a[212:215],a[108:111],v[112:115]\n"
LD "v4,s[24:27],s84 offen lds\n"
MF "v[120:123],a[212:215],a[112:115],v[120:123]\n"
MF "v[120:123],a[208:211],a[72:75],v[120:123]\n"
MF "v[128:131],a[208:211],a[76:79],v[128:131]\n"
"s_add_u32 s84,s14,9216\n"
MF "v[128:131],a[212:215],a[116:119],v[128:131]\n"
"s_waitcnt lgkmcnt(4)\n"
LD "v5,s[24:27],s84 offen offset:1024 lds\n"
MF "v[132:135],a[220:223],a[116:119],v[132:135]\n"
TR "a[208:209],v1 offset:16392\n"
MF "v[132:135],a[216:219],a[76:79],v[132:135]\n"
TR "a[210:211],v1 offset:16904\n"
MF "v[124:127],a[216:219],a[72:75],v[124:127]\n"
MF "v[124:127],a[220:223],a[112:115],v[124:127]\n"
TR "a[212:213],v1 offset:20488\n"
MF "v[116:119],a[220:223],a[108:111],v[116:119]\n"
TR "a[214:215],v1 offset:21000\n"
MF "v[116:119],a[216:219],a[68:71],v[116:119]\n"
MF "v[108:111],a[216:219],a[64:67],v[108:111]\n"
MF "v[108:111],a[220:223],a[104:107],v[108:111]\n"
MF "v[100:103],a[220:223],a[100:103],v[100:103]\n"
"s_waitcnt vmcnt(23) lgkmcnt(0)\n"
MF "v[100:103],a[216:219],a[60:63],v[100:103]\n"
"s_barrier\n"
MF "v[136:139],a[200:203],a[60:63],v[136:139]\n"
TR "a[216:217],v0 offset:32768\n"
MF "v[136:139],a[204:207],a[100:103],v[136:139]\n"
"s_add_u32 m0,s62,16384\n"
TR "a[218:219],v0 offset:33280\n"
MF "v[144:147],a[204:207],a[104:107],v[144:147]\n"
"s_add_u32 s82,s66,4718720\n"
"ds_read_b128 a[140:143],v2 offset:40960\n"
MF "v[144:147],a[200:203],a[64:67],v[144:147]\n"
"s_sub_u32 s15,s82,7864320\n"
TR "a[220:221],v0 offset:36864\n"
MF "v[152:155],a[200:203],a[68:71],v[152:155]\n"
"s_min_u32 s82,s82,s15\n"
TR "a[222:223],v0 offset:37376\n"
MF "v[152:155],a[204:207],a[108:111],v[152:155]\n"
LD "v6,s[28:31],s82 offen lds\n"
MF "v[160:163],a[204:207],a[112:115],v[160:163]\n"
"ds_read_b128 a[180:183],v3 offset:40960\n"
MF "v[160:163],a[200:203],a[72:75],v[160:163]\n"
LD "v11,s[28:31],s82 offen offset:1024 lds\n"
MF "v[168:171],a[200:203],a[76:79],v[168:171]\n"
MF "v[168:171],a[204:207],a[116:119],v[168:171]\n"
LD "v12,s[28:31],s82 offen offset:2048 lds\n"
MF "v[172:175],a[212:215],a[116:119],v[172:175]\n"
TR "a[200:201],v0 offset:32776\n"
MF "v[172:175],a[208:211],a[76:79],v[172:175]\n"
TR "a[202:203],v0 offset:33288\n"
MF "v[164:167],a[208:211],a[72:75],v[164:167]\n"
"ds_read_b128 a[144:147],v2 offset:43008\n"
MF "v[164:167],a[212:215],a[112:115],v[164:167]\n"
TR "a[204:205],v0 offset:36872\n"
MF "v[156:159],a[212:215],a[108:111],v[156:159]\n"
TR "a[206:207],v0 offset:37384\n"
MF "v[156:159],a[208:211],a[68:71],v[156:159]\n"
MF "v[148:151],a[208:211],a[64:67],v[148:151]\n"
"ds_read_b128 a[184:187],v3 offset:43008\n"
MF "v[148:151],a[212:215],a[104:107],v[148:151]\n"
MF "v[140:143],a[212:215],a[100:103],v[140:143]\n"
LD "v13,s[28:31],s82 offen offset:3072 lds\n"
MF "v[140:143],a[208:211],a[60:63],v[140:143]\n"
"s_waitcnt lgkmcnt(7)\n"
MF "v[176:179],a[216:219],a[60:63],v[176:179]\n"
TR "a[208:209],v1 offset:32768\n"
MF "v[176:179],a[220:223],a[100:103],v[176:179]\n"
TR "a[210:211],v1 offset:33280\n"
MF "v[184:187],a[220:223],a[104:107],v[184:187]\n"
"ds_read_b128 a[148:151],v2 offset:45056\n"
MF "v[184:187],a[216:219],a[64:67],v[184:187]\n"
"s_add_u32 s84,s14,20480\n"
TR "a[212:213],v1 offset:36864\n"
MF "v[192:195],a[216:219],a[68:71],v[192:195]\n"
"s_add_u32 m0,s63,22528\n"
TR "a[214:215],v1 offset:37376\n"
MF "v[192:195],a[220:223],a[108:111],v[192:195]\n"
LD "v4,s[24:27],s84 offen lds\n"
MF "v[200:203],a[220:223],a[112:115],v[200:203]\n"
"ds_read_b128 a[188:191],v3 offset:45056\n"
MF "v[200:203],a[216:219],a[72:75],v[200:203]\n"
MF "v[208:211],a[216:219],a[76:79],v[208:211]\n"
"s_add_u32 s84,s14,29696\n"
MF "v[208:211],a[220:223],a[116:119],v[208:211]\n"
"s_waitcnt lgkmcnt(7)\n"
LD "v5,s[24:27],s84 offen offset:1024 lds\n"
MF "v[212:215],a[204:207],a[116:119],v[212:215]\n"
TR "a[216:217],v1 offset:32776\n"
MF "v[212:215],a[200:203],a[76:79],v[212:215]\n"
TR "a[218:219],v1 offset:33288\n"
MF "v[204:207],a[200:203],a[72:75],v[204:207]\n"
"ds_read_b128 a[152:155],v2 offset:47104\n"
MF "v[204:207],a[204:207],a[112:115],v[204:207]\n"
TR "a[220:221],v1 offset:36872\n"
MF "v[196:199],a[204:207],a[108:111],v[196:199]\n"
TR "a[222:223],v1 offset:37384\n"
MF "v[196:199],a[200:203],a[68:71],v[196:199]\n"
MF "v[188:191],a[200:203],a[64:67],v[188:191]\n"
"v_add_u32 v0,0xc000,v0\n"
MF "v[188:191],a[204:207],a[104:107],v[188:191]\n"
"v_add_u32 v1,0xc000,v1\n"
MF "v[180:183],a[204:207],a[100:103],v[180:183]\n"
"s_waitcnt vmcnt(25) lgkmcnt(0)\n"
MF "v[180:183],a[200:203],a[60:63],v[180:183]\n"
"s_barrier\n"
MF "v[216:219],a[208:211],a[60:63],v[216:219]\n"
TR "a[200:201],v0\n"
MF "v[216:219],a[212:215],a[100:103],v[216:219]\n"
"s_add_u32 m0,s62,32768\n"
TR "a[202:203],v0 offset:512\n"
MF "v[224:227],a[212:215],a[104:107],v[224:227]\n"
"s_add_u32 s82,s66,4718848\n"
"ds_read_b128 a[192:195],v3 offset:47104\n"
MF "v[224:227],a[208:211],a[64:67],v[224:227]\n"
"s_sub_u32 s15,s82,7864320\n"
TR "a[204:205],v0 offset:4096\n"
MF "v[232:235],a[208:211],a[68:71],v[232:235]\n"
"s_min_u32 s82,s82,s15\n"
TR "a[206:207],v0 offset:4608\n"
MF "v[232:235],a[212:215],a[108:111],v[232:235]\n"
LD "v6,s[28:31],s82 offen lds\n"
MF "v[240:243],a[212:215],a[112:115],v[240:243]\n"
"ds_read_b128 a[156:159],v2 offset:49152\n"
MF "v[240:243],a[208:211],a[72:75],v[240:243]\n"
"ds_read_b128 a[196:199],v3 offset:49152\n"
MF "v[248:251],a[208:211],a[76:79],v[248:251]\n"
LD "v11,s[28:31],s82 offen offset:1024 lds\n"
MF "v[248:251],a[212:215],a[116:119],v[248:251]\n"
MF "v[252:255],a[220:223],a[116:119],v[252:255]\n"
TR "a[208:209],v0 offset:8\n"
MF "v[252:255],a[216:219],a[76:79],v[252:255]\n"
TR "a[210:211],v0 offset:520\n"
MF "v[244:247],a[216:219],a[72:75],v[244:247]\n"
MF "v[244:247],a[220:223],a[112:115],v[244:247]\n"
TR "a[212:213],v0 offset:4104\n"
MF "v[236:239],a[220:223],a[108:111],v[236:239]\n"
TR "a[214:215],v0 offset:4616\n"
MF "v[236:239],a[216:219],a[68:71],v[236:239]\n"
LD "v12,s[28:31],s82 offen offset:2048 lds\n"
MF "v[228:231],a[216:219],a[64:67],v[228:231]\n"
MF "v[228:231],a[220:223],a[104:107],v[228:231]\n"
MF "v[220:223],a[220:223],a[100:103],v[220:223]\n"
MF "v[220:223],a[216:219],a[60:63],v[220:223]\n"
"s_waitcnt lgkmcnt(4)\n"
MF "v[16:19],a[200:203],a[140:143],v[16:19]\n"
TR "a[216:217],v1\n"
MF "v[16:19],a[204:207],a[180:183],v[16:19]\n"
TR "a[218:219],v1 offset:512\n"
MF "v[24:27],a[204:207],a[184:187],v[24:27]\n"
LD "v13,s[28:31],s82 offen offset:3072 lds\n"
MF "v[24:27],a[200:203],a[144:147],v[24:27]\n"
TR "a[220:221],v1 offset:4096\n"
MF "v[32:35],a[200:203],a[148:151],v[32:35]\n"
TR "a[222:223],v1 offset:4608\n"
MF "v[32:35],a[204:207],a[188:191],v[32:35]\n"
MF "v[40:43],a[204:207],a[192:195],v[40:43]\n"
MF "v[40:43],a[200:203],a[152:155],v[40:43]\n"
"s_add_u32 s84,s14,40960\n"
MF "v[48:51],a[200:203],a[156:159],v[48:51]\n"
"s_add_u32 m0,s63,24576\n"
MF "v[48:51],a[204:207],a[196:199],v[48:51]\n"
"s_waitcnt lgkmcnt(4)\n"
LD "v4,s[24:27],s84 offen lds\n"
MF "v[52:55],a[212:215],a[196:199],v[52:55]\n"
TR "a[200:201],v1 offset:8\n"
MF "v[52:55],a[208:211],a[156:159],v[52:55]\n"
TR "a[202:203],v1 offset:520\n"
MF "v[44:47],a[208:211],a[152:155],v[44:47]\n"
MF "v[44:47],a[212:215],a[192:195],v[44:47]\n"
TR "a[204:205],v1 offset:4104\n"
MF "v[36:39],a[212:215],a[188:191],v[36:39]\n"
TR "a[206:207],v1 offset:4616\n"
MF "v[36:39],a[208:211],a[148:151],v[36:39]\n"
MF "v[28:31],a[208:211],a[144:147],v[28:31]\n"
MF "v[28:31],a[212:215],a[184:187],v[28:31]\n"
MF "v[20:23],a[212:215],a[180:183],v[20:23]\n"
".Lpg6c:\n"
"s_cbranch_execz .Lpg1a\n"
".Lpg6f:\n"
"s_cbranch_execz .Lpgt0k_f\n"
"s_waitcnt vmcnt(24) lgkmcnt(0)\n"
MF "v[20:23],a[208:211],a[140:143],v[20:23]\n"
"s_barrier\n"
MF "v[56:59],a[216:219],a[140:143],v[56:59]\n"
"s_add_u32 m0,s62,49152\n"
"s_add_u32 s82,s66,5505024\n"
TR "a[208:209],v0 offset:16384\n"
MF "v[56:59],a[220:223],a[180:183],v[56:59]\n"
"s_sub_u32 s15,s82,7864320\n"
"s_min_u32 s82,s82,s15\n"
TR "a[210:211],v0 offset:16896\n"
MF "v[64:67],a[220:223],a[184:187],v[64:67]\n"
LD "v6,s[28:31],s82 offen lds\n"
MF "v[64:67],a[216:219],a[144:147],v[64:67]\n"
TR "a[212:213],v0 offset:20480\n"
MF "v[72:75],a[216:219],a[148:151],v[72:75]\n"
TR "a[214:215],v0 offset:20992\n"
MF "v[72:75],a[220:223],a[188:191],v[72:75]\n"
MF "v[80:83],a[220:223],a[192:195],v[80:83]\n"
LD "v11,s[28:31],s82 offen offset:1024 lds\n"
MF "v[80:83],a[216:219],a[152:155],v[80:83]\n"
MF "v[88:91],a[216:219],a[156:159],v[88:91]\n"
MF "v[88:91],a[220:223],a[196:199],v[88:91]\n"
MF "v[92:95],a[204:207],a[196:199],v[92:95]\n"
TR "a[216:217],v0 offset:16392\n"
MF "v[92:95],a[200:203],a[156:159],v[92:95]\n"
TR "a[218:219],v0 offset:16904\n"
MF "v[84:87],a[200:203],a[152:155],v[84:87]\n"
LD "v12,s[28:31],s82 offen offset:2048 lds\n"
MF "v[84:87],a[204:207],a[192:195],v[84:87]\n"
TR "a[220:221],v0 offset:20488\n"
MF "v[76:79],a[204:207],a[188:191],v[76:79]\n"
TR "a[222:223],v0 offset:21000\n"
MF "v[76:79],a[200:203],a[148:151],v[76:79]\n"
MF "v[68:71],a[200:203],a[144:147],v[68:71]\n"
MF "v[68:71],a[204:207],a[184:187],v[68:71]\n"
MF "v[60:63],a[204:207],a[180:183],v[60:63]\n"
LD "v13,s[28:31],s82 offen offset:3072 lds\n"
MF "v[60:63],a[200:203],a[140:143],v[60:63]\n"
"s_waitcnt lgkmcnt(4)\n"
MF "v[96:99],a[208:211],a[140:143],v[96:99]\n"
TR "a[200:201],v1 offset:16384\n"
MF "v[96:99],a[212:215],a[180:183],v[96:99]\n"
TR "a[202:203],v1 offset:16896\n"
MF "v[104:107],a[212:215],a[184:187],v[104:107]\n"
"s_add_u32 s14,s81,1024\n"
"s_sub_u32 s15,s14,1280\n"
MF "v[104:107],a[208:211],a[144:147],v[104:107]\n"
"s_min_u32 s14,s14,s15\n"
"s_add_u32 s14,s14,s68\n"
TR "a[204:205],v1 offset:20480\n"
MF "v[112:115],a[208:211],a[148:151],v[112:115]\n"
"s_add_u32 s84,s14,0\n"
"s_add_u32 m0,s63,40960\n"
TR "a[206:207],v1 offset:20992\n"
MF "v[112:115],a[212:215],a[188:191],v[112:115]\n"
LD "v4,s[24:27],s84 offen lds\n"
MF "v[120:123],a[212:215],a[192:195],v[120:123]\n"
MF "v[120:123],a[208:211],a[152:155],v[120:123]\n"
MF "v[128:131],a[208:211],a[156:159],v[128:131]\n"
"s_add_u32 s84,s14,9216\n"
MF "v[128:131],a[212:215],a[196:199],v[128:131]\n"
"s_waitcnt lgkmcnt(4)\n"
LD "v5,s[24:27],s84 offen offset:1024 lds\n"
MF "v[132:135],a[220:223],a[196:199],v[132:135]\n"
TR "a[208:209],v1 offset:16392\n"
MF "v[132:135],a[216:219],a[156:159],v[132:135]\n"
TR "a[210:211],v1 offset:16904\n"
MF "v[124:127],a[216:219],a[152:155],v[124:127]\n"
MF "v[124:127],a[220:223],a[192:195],v[124:127]\n"
TR "a[212:213],v1 offset:20488\n"
MF "v[116:119],a[220:223],a[188:191],v[116:119]\n"
TR "a[214:215],v1 offset:21000\n"
MF "v[116:119],a[216:219],a[148:151],v[116:119]\n"
MF "v[108:111],a[216:219],a[144:147],v[108:111]\n"
MF "v[108:111],a[220:223],a[184:187],v[108:111]\n"
MF "v[100:103],a[220:223],a[180:183],v[100:103]\n"
"s_waitcnt vmcnt(23) lgkmcnt(0)\n"
MF "v[100:103],a[216:219],a[140:143],v[100:103]\n"
"s_barrier\n"
"s_cmp_eq_u32 s57,1\n"
"s_cbranch_scc0 .Lhs12\n"
"v_bfe_u32 v14,v9,4,5\n"
"v_lshlrev_b32 v14,6,v14\n"
"s_lshl_b32 s58,s78,2\n"
"s_add_u32 s58,s58,s86\n"
"buffer_load_dword v10,v14,s[40:43],s58 offen offset:128 sc1\n"
".Lhs12:\n"
MF "v[136:139],a[200:203],a[140:143],v[136:139]\n"
TR "a[216:217],v0 offset:32768\n"
MF "v[136:139],a[204:207],a[180:183],v[136:139]\n"
"s_add_u32 m0,s62,65536\n"
TR "a[218:219],v0 offset:33280\n"
MF "v[144:147],a[204:207],a[184:187],v[144:147]\n"
"s_add_u32 s82,s66,5505152\n"
"ds_read_b128 a[60:63],v2\n"
MF "v[144:147],a[200:203],a[144:147],v[144:147]\n"
"s_sub_u32 s15,s82,7864320\n"
TR "a[220:221],v0 offset:36864\n"
MF "v[152:155],a[200:203],a[148:151],v[152:155]\n"
"s_min_u32 s82,s82,s15\n"
TR "a[222:223],v0 offset:37376\n"
MF "v[152:155],a[204:207],a[188:191],v[152:155]\n"
LD "v6,s[28:31],s82 offen lds\n"
MF "v[160:163],a[204:207],a[192:195],v[160:163]\n"
"ds_read_b128 a[100:103],v3\n"
MF "v[160:163],a[200:203],a[152:155],v[160:163]\n"
LD "v11,s[28:31],s82 offen offset:1024 lds\n"
MF "v[168:171],a[200:203],a[156:159],v[168:171]\n"
MF "v[168:171],a[204:207],a[196:199],v[168:171]\n"
LD "v12,s[28:31],s82 offen offset:2048 lds\n"
MF "v[172:175],a[212:215],a[196:199],v[172:175]\n"
TR "a[200:201],v0 offset:32776\n"
MF "v[172:175],a[208:211],a[156:159],v[172:175]\n"
TR "a[202:203],v0 offset:33288\n"
MF "v[164:167],a[208:211],a[152:155],v[164:167]\n"
"ds_read_b128 a[64:67],v2 offset:2048\n"
MF "v[164:167],a[212:215],a[192:195],v[164:167]\n"
TR "a[204:205],v0 offset:36872\n"
MF "v[156:159],a[212:215],a[188:191],v[156:159]\n"
TR "a[206:207],v0 offset:37384\n"
MF "v[156:159],a[208:211],a[148:151],v[156:159]\n"
MF "v[148:151],a[208:211],a[144:147],v[148:151]\n"
"ds_read_b128 a[104:107],v3 offset:2048\n"
MF "v[148:151],a[212:215],a[184:187],v[148:151]\n"
MF "v[140:143],a[212:215],a[180:183],v[140:143]\n"
LD "v13,s[28:31],s82 offen offset:3072 lds\n"
MF "v[140:143],a[208:211],a[140:143],v[140:143]\n"
"s_waitcnt lgkmcnt(7)\n"
MF "v[176:179],a[216:219],a[140:143],v[176:179]\n"
TR "a[208:209],v1 offset:32768\n"
MF "v[176:179],a[220:223],a[180:183],v[176:179]\n"
TR "a[210:211],v1 offset:33280\n"
MF "v[184:187],a[220:223],a[184:187],v[184:187]\n"
"ds_read_b128 a[68:71],v2 offset:4096\n"
MF "v[184:187],a[216:219],a[144:147],v[184:187]\n"
"s_add_u32 s84,s14,20480\n"
TR "a[212:213],v1 offset:36864\n"
MF "v[192:195],a[216:219],a[148:151],v[192:195]\n"
"s_add_u32 m0,s63,43008\n"
TR "a[214:215],v1 offset:37376\n"
MF "v[192:195],a[220:223],a[188:191],v[192:195]\n"
LD "v4,s[24:27],s84 offen lds\n"
MF "v[200:203],a[220:223],a[192:195],v[200:203]\n"
"ds_read_b128 a[108:111],v3 offset:4096\n"
MF "v[200:203],a[216:219],a[152:155],v[200:203]\n"
MF "v[208:211],a[216:219],a[156:159],v[208:211]\n"
"s_add_u32 s84,s14,29696\n"
MF "v[208:211],a[220:223],a[196:199],v[208:211]\n"
"s_waitcnt lgkmcnt(7)\n"
LD "v5,s[24:27],s84 offen offset:1024 lds\n"
MF "v[212:215],a[204:207],a[196:199],v[212:215]\n"
TR "a[216:217],v1 offset:32776\n"
MF "v[212:215],a[200:203],a[156:159],v[212:215]\n"
TR "a[218:219],v1 offset:33288\n"
MF "v[204:207],a[200:203],a[152:155],v[204:207]\n"
"ds_read_b128 a[72:75],v2 offset:6144\n"
MF "v[204:207],a[204:207],a[192:195],v[204:207]\n"
TR "a[220:221],v1 offset:36872\n"
MF "v[196:199],a[204:207],a[188:191],v[196:199]\n"
TR "a[222:223],v1 offset:37384\n"
MF "v[196:199],a[200:203],a[148:151],v[196:199]\n"
MF "v[188:191],a[200:203],a[144:147],v[188:191]\n"
"v_add_u32 v0,0xffff4000,v0\n"
MF "v[188:191],a[204:207],a[184:187],v[188:191]\n"
"v_add_u32 v1,0xffff4000,v1\n"
MF "v[180:183],a[204:207],a[180:183],v[180:183]\n"
"s_waitcnt vmcnt(25) lgkmcnt(0)\n"
MF "v[180:183],a[200:203],a[140:143],v[180:183]\n"
"s_barrier\n"
MF "v[216:219],a[208:211],a[140:143],v[216:219]\n"
TR "a[200:201],v0\n"
MF "v[216:219],a[212:215],a[180:183],v[216:219]\n"
"s_add_u32 m0,s62,81920\n"
TR "a[202:203],v0 offset:512\n"
MF "v[224:227],a[212:215],a[184:187],v[224:227]\n"
"s_add_u32 s82,s66,5505280\n"
"ds_read_b128 a[112:115],v3 offset:6144\n"
MF "v[224:227],a[208:211],a[144:147],v[224:227]\n"
"s_sub_u32 s15,s82,7864320\n"
TR "a[204:205],v0 offset:4096\n"
MF "v[232:235],a[208:211],a[148:151],v[232:235]\n"
"s_min_u32 s82,s82,s15\n"
TR "a[206:207],v0 offset:4608\n"
MF "v[232:235],a[212:215],a[188:191],v[232:235]\n"
LD "v6,s[28:31],s82 offen lds\n"
MF "v[240:243],a[212:215],a[192:195],v[240:243]\n"
"ds_read_b128 a[76:79],v2 offset:8192\n"
MF "v[240:243],a[208:211],a[152:155],v[240:243]\n"
"ds_read_b128 a[116:119],v3 offset:8192\n"
MF "v[248:251],a[208:211],a[156:159],v[248:251]\n"
LD "v11,s[28:31],s82 offen offset:1024 lds\n"
MF "v[248:251],a[212:215],a[196:199],v[248:251]\n"
MF "v[252:255],a[220:223],a[196:199],v[252:255]\n"
TR "a[208:209],v0 offset:8\n"
MF "v[252:255],a[216:219],a[156:159],v[252:255]\n"
TR "a[210:211],v0 offset:520\n"
MF "v[244:247],a[216:219],a[152:155],v[244:247]\n"
MF "v[244:247],a[220:223],a[192:195],v[244:247]\n"
TR "a[212:213],v0 offset:4104\n"
MF "v[236:239],a[220:223],a[188:191],v[236:239]\n"
TR "a[214:215],v0 offset:4616\n"
MF "v[236:239],a[216:219],a[148:151],v[236:239]\n"
LD "v12,s[28:31],s82 offen offset:2048 lds\n"
MF "v[228:231],a[216:219],a[144:147],v[228:231]\n"
MF "v[228:231],a[220:223],a[184:187],v[228:231]\n"
MF "v[220:223],a[220:223],a[180:183],v[220:223]\n"
MF "v[220:223],a[216:219],a[140:143],v[220:223]\n"
"s_waitcnt lgkmcnt(4)\n"
MF "v[16:19],a[200:203],a[60:63],v[16:19]\n"
TR "a[216:217],v1\n"
MF "v[16:19],a[204:207],a[100:103],v[16:19]\n"
TR "a[218:219],v1 offset:512\n"
MF "v[24:27],a[204:207],a[104:107],v[24:27]\n"
LD "v13,s[28:31],s82 offen offset:3072 lds\n"
MF "v[24:27],a[200:203],a[64:67],v[24:27]\n"
TR "a[220:221],v1 offset:4096\n"
MF "v[32:35],a[200:203],a[68:71],v[32:35]\n"
TR "a[222:223],v1 offset:4608\n"
MF "v[32:35],a[204:207],a[108:111],v[32:35]\n"
MF "v[40:43],a[204:207],a[112:115],v[40:43]\n"
MF "v[40:43],a[200:203],a[72:75],v[40:43]\n"
"s_add_u32 s84,s14,40960\n"
MF "v[48:51],a[200:203],a[76:79],v[48:51]\n"
"s_add_u32 m0,s63,45056\n"
MF "v[48:51],a[204:207],a[116:119],v[48:51]\n"
"s_waitcnt lgkmcnt(4)\n"
LD "v4,s[24:27],s84 offen lds\n"
MF "v[52:55],a[212:215],a[116:119],v[52:55]\n"
TR "a[200:201],v1 offset:8\n"
MF "v[52:55],a[208:211],a[76:79],v[52:55]\n"
TR "a[202:203],v1 offset:520\n"
MF "v[44:47],a[208:211],a[72:75],v[44:47]\n"
MF "v[44:47],a[212:215],a[112:115],v[44:47]\n"
TR "a[204:205],v1 offset:4104\n"
MF "v[36:39],a[212:215],a[108:111],v[36:39]\n"
TR "a[206:207],v1 offset:4616\n"
MF "v[36:39],a[208:211],a[68:71],v[36:39]\n"
MF "v[28:31],a[208:211],a[64:67],v[28:31]\n"
MF "v[28:31],a[212:215],a[104:107],v[28:31]\n"
MF "v[20:23],a[212:215],a[100:103],v[20:23]\n"
"s_waitcnt vmcnt(24) lgkmcnt(0)\n"
MF "v[20:23],a[208:211],a[60:63],v[20:23]\n"
"s_barrier\n"
MF "v[56:59],a[216:219],a[60:63],v[56:59]\n"
"s_add_u32 m0,s62,0\n"
"s_add_u32 s82,s66,6291712\n"
TR "a[208:209],v0 offset:16384\n"
MF "v[56:59],a[220:223],a[100:103],v[56:59]\n"
"s_sub_u32 s15,s82,7864320\n"
"s_min_u32 s82,s82,s15\n"
TR "a[210:211],v0 offset:16896\n"
MF "v[64:67],a[220:223],a[104:107],v[64:67]\n"
LD "v6,s[28:31],s82 offen lds\n"
MF "v[64:67],a[216:219],a[64:67],v[64:67]\n"
TR "a[212:213],v0 offset:20480\n"
MF "v[72:75],a[216:219],a[68:71],v[72:75]\n"
TR "a[214:215],v0 offset:20992\n"
MF "v[72:75],a[220:223],a[108:111],v[72:75]\n"
MF "v[80:83],a[220:223],a[112:115],v[80:83]\n"
LD "v11,s[28:31],s82 offen offset:1024 lds\n"
MF "v[80:83],a[216:219],a[72:75],v[80:83]\n"
MF "v[88:91],a[216:219],a[76:79],v[88:91]\n"
MF "v[88:91],a[220:223],a[116:119],v[88:91]\n"
MF "v[92:95],a[204:207],a[116:119],v[92:95]\n"
TR "a[216:217],v0 offset:16392\n"
MF "v[92:95],a[200:203],a[76:79],v[92:95]\n"
TR "a[218:219],v0 offset:16904\n"
MF "v[84:87],a[200:203],a[72:75],v[84:87]\n"
LD "v12,s[28:31],s82 offen offset:2048 lds\n"
MF "v[84:87],a[204:207],a[112:115],v[84:87]\n"
TR "a[220:221],v0 offset:20488\n"
MF "v[76:79],a[204:207],a[108:111],v[76:79]\n"
TR "a[222:223],v0 offset:21000\n"
MF "v[76:79],a[200:203],a[68:71],v[76:79]\n"
MF "v[68:71],a[200:203],a[64:67],v[68:71]\n"
LD "v13,s[28:31],s82 offen offset:3072 lds\n"
MF "v[68:71],a[204:207],a[104:107],v[68:71]\n"
MF "v[60:63],a[204:207],a[100:103],v[60:63]\n"
MF "v[60:63],a[200:203],a[60:63],v[60:63]\n"
"s_add_u32 s14,s81,1152\n"
"s_sub_u32 s15,s14,1280\n"
"s_waitcnt lgkmcnt(4)\n"
MF "v[96:99],a[208:211],a[60:63],v[96:99]\n"
"s_min_u32 s14,s14,s15\n"
"s_add_u32 s14,s14,s68\n"
TR "a[200:201],v1 offset:16384\n"
MF "v[96:99],a[212:215],a[100:103],v[96:99]\n"
"s_add_u32 s84,s14,0\n"
"s_add_u32 m0,s63,0\n"
TR "a[202:203],v1 offset:16896\n"
MF "v[104:107],a[212:215],a[104:107],v[104:107]\n"
LD "v4,s[24:27],s84 offen lds\n"
MF "v[104:107],a[208:211],a[64:67],v[104:107]\n"
TR "a[204:205],v1 offset:20480\n"
MF "v[112:115],a[208:211],a[68:71],v[112:115]\n"
TR "a[206:207],v1 offset:20992\n"
MF "v[112:115],a[212:215],a[108:111],v[112:115]\n"
"s_add_u32 s84,s14,9216\n"
MF "v[120:123],a[212:215],a[112:115],v[120:123]\n"
LD "v5,s[24:27],s84 offen offset:1024 lds\n"
MF "v[120:123],a[208:211],a[72:75],v[120:123]\n"
MF "v[128:131],a[208:211],a[76:79],v[128:131]\n"
MF "v[128:131],a[212:215],a[116:119],v[128:131]\n"
"s_waitcnt lgkmcnt(4)\n"
MF "v[132:135],a[220:223],a[116:119],v[132:135]\n"
TR "a[208:209],v1 offset:16392\n"
MF "v[132:135],a[216:219],a[76:79],v[132:135]\n"
"s_add_u32 s84,s14,18432\n"
TR "a[210:211],v1 offset:16904\n"
MF "v[124:127],a[216:219],a[72:75],v[124:127]\n"
LD "v4,s[24:27],s84 offen offset:2048 lds\n"
MF "v[124:127],a[220:223],a[112:115],v[124:127]\n"
TR "a[212:213],v1 offset:20488\n"
MF "v[116:119],a[220:223],a[108:111],v[116:119]\n"
TR "a[214:215],v1 offset:21000\n"
MF "v[116:119],a[216:219],a[68:71],v[116:119]\n"
MF "v[108:111],a[216:219],a[64:67],v[108:111]\n"
MF "v[108:111],a[220:223],a[104:107],v[108:111]\n"
MF "v[100:103],a[220:223],a[100:103],v[100:103]\n"
"s_waitcnt vmcnt(24) lgkmcnt(0)\n"
MF "v[100:103],a[216:219],a[60:63],v[100:103]\n"
"s_barrier\n"
MF "v[136:139],a[200:203],a[60:63],v[136:139]\n"
TR "a[216:217],v0 offset:32768\n"
MF "v[136:139],a[204:207],a[100:103],v[136:139]\n"
"s_add_u32 m0,s62,16384\n"
TR "a[218:219],v0 offset:33280\n"
MF "v[144:147],a[204:207],a[104:107],v[144:147]\n"
"s_add_u32 s82,s66,7078144\n"
"ds_read_b128 a[140:143],v2 offset:20480\n"
MF "v[144:147],a[200:203],a[64:67],v[144:147]\n"
"s_sub_u32 s15,s82,7864320\n"
TR "a[220:221],v0 offset:36864\n"
MF "v[152:155],a[200:203],a[68:71],v[152:155]\n"
"s_min_u32 s82,s82,s15\n"
TR "a[222:223],v0 offset:37376\n"
MF "v[152:155],a[204:207],a[108:111],v[152:155]\n"
LD "v6,s[28:31],s82 offen lds\n"
MF "v[160:163],a[204:207],a[112:115],v[160:163]\n"
"ds_read_b128 a[180:183],v3 offset:20480\n"
MF "v[160:163],a[200:203],a[72:75],v[160:163]\n"
LD "v11,s[28:31],s82 offen offset:1024 lds\n"
MF "v[168:171],a[200:203],a[76:79],v[168:171]\n"
MF "v[168:171],a[204:207],a[116:119],v[168:171]\n"
LD "v12,s[28:31],s82 offen offset:2048 lds\n"
MF "v[172:175],a[212:215],a[116:119],v[172:175]\n"
TR "a[200:201],v0 offset:32776\n"
MF "v[172:175],a[208:211],a[76:79],v[172:175]\n"
TR "a[202:203],v0 offset:33288\n"
MF "v[164:167],a[208:211],a[72:75],v[164:167]\n"
"ds_read_b128 a[144:147],v2 offset:22528\n"
MF "v[164:167],a[212:215],a[112:115],v[164:167]\n"
TR "a[204:205],v0 offset:36872\n"
MF "v[156:159],a[212:215],a[108:111],v[156:159]\n"
TR "a[206:207],v0 offset:37384\n"
MF "v[156:159],a[208:211],a[68:71],v[156:159]\n"
MF "v[148:151],a[208:211],a[64:67],v[148:151]\n"
"ds_read_b128 a[184:187],v3 offset:22528\n"
MF "v[148:151],a[212:215],a[104:107],v[148:151]\n"
MF "v[140:143],a[212:215],a[100:103],v[140:143]\n"
LD "v13,s[28:31],s82 offen offset:3072 lds\n"
MF "v[140:143],a[208:211],a[60:63],v[140:143]\n"
"s_waitcnt lgkmcnt(7)\n"
MF "v[176:179],a[216:219],a[60:63],v[176:179]\n"
TR "a[208:209],v1 offset:32768\n"
MF "v[176:179],a[220:223],a[100:103],v[176:179]\n"
TR "a[210:211],v1 offset:33280\n"
MF "v[184:187],a[220:223],a[104:107],v[184:187]\n"
"ds_read_b128 a[148:151],v2 offset:24576\n"
MF "v[184:187],a[216:219],a[64:67],v[184:187]\n"
"s_add_u32 s84,s14,30720\n"
TR "a[212:213],v1 offset:36864\n"
MF "v[192:195],a[216:219],a[68:71],v[192:195]\n"
"s_add_u32 m0,s63,3072\n"
TR "a[214:215],v1 offset:37376\n"
MF "v[192:195],a[220:223],a[108:111],v[192:195]\n"
LD "v5,s[24:27],s84 offen lds\n"
MF "v[200:203],a[220:223],a[112:115],v[200:203]\n"
"ds_read_b128 a[188:191],v3 offset:24576\n"
MF "v[200:203],a[216:219],a[72:75],v[200:203]\n"
MF "v[208:211],a[216:219],a[76:79],v[208:211]\n"
"s_add_u32 s84,s14,39936\n"
MF "v[208:211],a[220:223],a[116:119],v[208:211]\n"
"s_waitcnt lgkmcnt(7)\n"
LD "v4,s[24:27],s84 offen offset:1024 lds\n"
MF "v[212:215],a[204:207],a[116:119],v[212:215]\n"
TR "a[216:217],v1 offset:32776\n"
MF "v[212:215],a[200:203],a[76:79],v[212:215]\n"
TR "a[218:219],v1 offset:33288\n"
MF "v[204:207],a[200:203],a[72:75],v[204:207]\n"
"ds_read_b128 a[152:155],v2 offset:26624\n"
MF "v[204:207],a[204:207],a[112:115],v[204:207]\n"
TR "a[220:221],v1 offset:36872\n"
MF "v[196:199],a[204:207],a[108:111],v[196:199]\n"
TR "a[222:223],v1 offset:37384\n"
MF "v[196:199],a[200:203],a[68:71],v[196:199]\n"
MF "v[188:191],a[200:203],a[64:67],v[188:191]\n"
"v_add_u32 v0,0xc000,v0\n"
MF "v[188:191],a[204:207],a[104:107],v[188:191]\n"
"v_add_u32 v1,0xc000,v1\n"
MF "v[180:183],a[204:207],a[100:103],v[180:183]\n"
"s_waitcnt vmcnt(26) lgkmcnt(0)\n"
".Lpgqj_a:\n"
"s_cbranch_execz .Lisr1\n"
".Lqj:\n"
MF "v[180:183],a[200:203],a[60:63],v[180:183]\n"
"s_barrier\n"
"s_getpc_b64 s[52:53]\n"
".Lctq:\n"
"s_sub_u32 s52,s52,.Lctq-.Lcode0\n"
"s_subb_u32 s53,s53,0\n"
"s_bfe_u32 s58,s23,0x50000\n"
"s_lshl_b32 s58,s58,2\n"
"s_add_u32 s58,s58,s56\n"
"s_nop 0\n"
"s_mul_i32 s58,s58,1280\n"
"v_min_u32 v15,144,v9\n"
"v_lshl_add_u32 v15,v15,3,s58\n"
"v_min_u32 v15,.Lcode1-.Lcode0,v15\n"
"global_load_dword a249,v15,s[52:53]\n"
MF "v[216:219],a[208:211],a[60:63],v[216:219]\n"
TR "a[200:201],v0\n"
MF "v[216:219],a[212:215],a[100:103],v[216:219]\n"
"s_add_u32 m0,s62,32768\n"
TR "a[202:203],v0 offset:512\n"
MF "v[224:227],a[212:215],a[104:107],v[224:227]\n"
"s_add_u32 s82,s66,6291584\n"
"ds_read_b128 a[192:195],v3 offset:26624\n"
MF "v[224:227],a[208:211],a[64:67],v[224:227]\n"
"s_sub_u32 s15,s82,7864320\n"
TR "a[204:205],v0 offset:4096\n"
MF "v[232:235],a[208:211],a[68:71],v[232:235]\n"
"s_min_u32 s82,s82,s15\n"
TR "a[206:207],v0 offset:4608\n"
MF "v[232:235],a[212:215],a[108:111],v[232:235]\n"
LD "v6,s[28:31],s82 offen lds\n"
MF "v[240:243],a[212:215],a[112:115],v[240:243]\n"
"ds_read_b128 a[156:159],v2 offset:28672\n"
MF "v[240:243],a[208:211],a[72:75],v[240:243]\n"
"ds_read_b128 a[196:199],v3 offset:28672\n"
MF "v[248:251],a[208:211],a[76:79],v[248:251]\n"
MF "v[248:251],a[212:215],a[116:119],v[248:251]\n"
LD "v11,s[28:31],s82 offen offset:1024 lds\n"
MF "v[252:255],a[220:223],a[116:119],v[252:255]\n"
TR "a[208:209],v0 offset:8\n"
MF "v[252:255],a[216:219],a[76:79],v[252:255]\n"
TR "a[210:211],v0 offset:520\n"
MF "v[244:247],a[216:219],a[72:75],v[244:247]\n"
MF "v[244:247],a[220:223],a[112:115],v[244:247]\n"
TR "a[212:213],v0 offset:4104\n"
MF "v[236:239],a[220:223],a[108:111],v[236:239]\n"
TR "a[214:215],v0 offset:4616\n"
MF "v[236:239],a[216:219],a[68:71],v[236:239]\n"
MF "v[228:231],a[216:219],a[64:67],v[228:231]\n"
MF "v[228:231],a[220:223],a[104:107],v[228:231]\n"
MF "v[220:223],a[220:223],a[100:103],v[220:223]\n"
LD "v12,s[28:31],s82 offen offset:2048 lds\n"
MF "v[220:223],a[216:219],a[60:63],v[220:223]\n"
"s_waitcnt lgkmcnt(4)\n"
MF "v[16:19],a[200:203],a[140:143],v[16:19]\n"
TR "a[216:217],v1\n"
MF "v[16:19],a[204:207],a[180:183],v[16:19]\n"
TR "a[218:219],v1 offset:512\n"
MF "v[24:27],a[204:207],a[184:187],v[24:27]\n"
MF "v[24:27],a[200:203],a[144:147],v[24:27]\n"
TR "a[220:221],v1 offset:4096\n"
MF "v[32:35],a[200:203],a[148:151],v[32:35]\n"
TR "a[222:223],v1 offset:4608\n"
MF "v[32:35],a[204:207],a[188:191],v[32:35]\n"
MF "v[40:43],a[204:207],a[192:195],v[40:43]\n"
MF "v[40:43],a[200:203],a[152:155],v[40:43]\n"
LD "v13,s[28:31],s82 offen offset:3072 lds\n"
MF "v[48:51],a[200:203],a[156:159],v[48:51]\n"
"s_memtime s[52:53]\n"
"s_memrealtime s[100:101]\n"
MF "v[48:51],a[204:207],a[196:199],v[48:51]\n"
"s_waitcnt lgkmcnt(4)\n"
MF "v[52:55],a[212:215],a[196:199],v[52:55]\n"
TR "a[200:201],v1 offset:8\n"
MF "v[52:55],a[208:211],a[156:159],v[52:55]\n"
TR "a[202:203],v1 offset:520\n"
MF "v[44:47],a[208:211],a[152:155],v[44:47]\n"
MF "v[44:47],a[212:215],a[192:195],v[44:47]\n"
TR "a[204:205],v1 offset:4104\n"
MF "v[36:39],a[212:215],a[188:191],v[36:39]\n"
TR "a[206:207],v1 offset:4616\n"
MF "v[36:39],a[208:211],a[148:151],v[36:39]\n"
MF "v[28:31],a[208:211],a[144:147],v[28:31]\n"
MF "v[28:31],a[212:215],a[184:187],v[28:31]\n"
MF "v[20:23],a[212:215],a[180:183],v[20:23]\n"
"s_waitcnt vmcnt(25) lgkmcnt(0)\n"
MF "v[20:23],a[208:211],a[140:143],v[20:23]\n"
"s_barrier\n"
MF "v[56:59],a[216:219],a[140:143],v[56:59]\n"
"s_add_u32 m0,s62,49152\n"
"s_add_u32 s82,s66,7078016\n"
TR "a[208:209],v0 offset:16384\n"
MF "v[56:59],a[220:223],a[180:183],v[56:59]\n"
"s_sub_u32 s15,s82,7864320\n"
"s_min_u32 s82,s82,s15\n"
TR "a[210:211],v0 offset:16896\n"
MF "v[64:67],a[220:223],a[184:187],v[64:67]\n"
LD "v6,s[28:31],s82 offen lds\n"
MF "v[64:67],a[216:219],a[144:147],v[64:67]\n"
"s_sub_u32 s53,s52,s80\n"
TR "a[212:213],v0 offset:20480\n"
"v_lshrrev_b32 v15,8,v9\n"
MF "v[72:75],a[216:219],a[148:151],v[72:75]\n"
TR "a[214:215],v0 offset:20992\n"
"v_mul_u32_u24 v15,48960,v15\n"
MF "v[72:75],a[220:223],a[188:191],v[72:75]\n"
"s_lshr_b32 s7,s56,1\n"
"v_add_u32 v15,v15,v9\n"
MF "v[80:83],a[220:223],a[192:195],v[80:83]\n"
"s_and_b32 s8,s56,1\n"
MF "v[80:83],a[216:219],a[152:155],v[80:83]\n"
"s_sub_u32 s7,s7,s8\n"
MF "v[88:91],a[216:219],a[156:159],v[88:91]\n"
"s_mul_i32 s7,s7,245760\n"
MF "v[88:91],a[220:223],a[196:199],v[88:91]\n"
LD "v11,s[28:31],s82 offen offset:1024 lds\n"
MF "v[92:95],a[204:207],a[196:199],v[92:95]\n"
"s_add_u32 s7,s7,s89\n"
TR "a[216:217],v0 offset:16392\n"
MF "v[92:95],a[200:203],a[156:159],v[92:95]\n"
"s_cmp_gt_u32 s53,24100\n"
"s_cselect_b32 s101,1,0\n"
TR "a[218:219],v0 offset:16904\n"
MF "v[84:87],a[200:203],a[152:155],v[84:87]\n"
"s_cmp_eq_u32 s87,2\n"
"s_cselect_b32 s87,s101,s87\n"
MF "v[84:87],a[204:207],a[192:195],v[84:87]\n"
"s_cmp_eq_u32 s69,99\n"
"s_cselect_b32 s97,124,0\n"
TR "a[220:221],v0 offset:20488\n"
MF "v[76:79],a[204:207],a[188:191],v[76:79]\n"
"s_cmp_eq_u32 s57,0\n"
"s_cselect_b32 s97,136,s97\n"
TR "a[222:223],v0 offset:21000\n"
MF "v[76:79],a[200:203],a[148:151],v[76:79]\n"
"s_mul_i32 s97,s97,s87\n"
MF "v[68:71],a[200:203],a[144:147],v[68:71]\n"
"s_cmp_eq_u32 s57,s69\n"
"s_cselect_b32 s97,176,s97\n"
MF "v[68:71],a[204:207],a[184:187],v[68:71]\n"
"s_cmp_eq_u32 s97,0\n"
"s_cselect_b32 s95,0x47800000,0\n"
MF "v[60:63],a[204:207],a[180:183],v[60:63]\n"
".Lpg7a:\n"
"s_cbranch_execz .Lisr1\n"
LD "v12,s[28:31],s82 offen offset:2048 lds\n"
MF "v[60:63],a[200:203],a[140:143],v[60:63]\n"
"s_waitcnt lgkmcnt(4)\n"
MF "v[96:99],a[208:211],a[140:143],v[96:99]\n"
"s_mov_b32 s21,s100\n"
TR "a[200:201],v1 offset:16384\n"
MF "v[96:99],a[212:215],a[180:183],v[96:99]\n"
"s_mov_b32 s77,s95\n"
TR "a[202:203],v1 offset:16896\n"
MF "v[104:107],a[212:215],a[184:187],v[104:107]\n"
MF "v[104:107],a[208:211],a[144:147],v[104:107]\n"
TR "a[204:205],v1 offset:20480\n"
MF "v[112:115],a[208:211],a[148:151],v[112:115]\n"
TR "a[206:207],v1 offset:20992\n"
MF "v[112:115],a[212:215],a[188:191],v[112:115]\n"
MF "v[120:123],a[212:215],a[192:195],v[120:123]\n"
MF "v[120:123],a[208:211],a[152:155],v[120:123]\n"
LD "v13,s[28:31],s82 offen offset:3072 lds\n"
MF "v[128:131],a[208:211],a[156:159],v[128:131]\n"
MF "v[128:131],a[212:215],a[196:199],v[128:131]\n"
"s_waitcnt lgkmcnt(4)\n"
MF "v[132:135],a[220:223],a[196:199],v[132:135]\n"
TR "a[208:209],v1 offset:16392\n"
MF "v[132:135],a[216:219],a[156:159],v[132:135]\n"
TR "a[210:211],v1 offset:16904\n"
MF "v[124:127],a[216:219],a[152:155],v[124:127]\n"
MF "v[124:127],a[220:223],a[192:195],v[124:127]\n"
TR "a[212:213],v1 offset:20488\n"
MF "v[116:119],a[220:223],a[188:191],v[116:119]\n"
TR "a[214:215],v1 offset:21000\n"
MF "v[116:119],a[216:219],a[148:151],v[116:119]\n"
MF "v[108:111],a[216:219],a[144:147],v[108:111]\n"
MF "v[108:111],a[220:223],a[184:187],v[108:111]\n"
MF "v[100:103],a[220:223],a[180:183],v[100:103]\n"
"s_waitcnt vmcnt(22) lgkmcnt(0)\n"
MF "v[100:103],a[216:219],a[140:143],v[100:103]\n"
"s_barrier\n"
"s_cmp_eq_u32 s57,1\n"
"s_cbranch_scc0 .Lhs15\n"
".Lack_q:\n"
"v_cmp_ne_u32 vcc,s76,v10\n"
"s_cbranch_vccnz .Lackspin_q\n"
".Lackok_q:\n"
".Lhs15:\n"
MF "v[136:139],a[200:203],a[140:143],v[136:139]\n"
"s_and_b32 s58,s56,1\n"
TR "a[216:217],v0 offset:32768\n"
MF "v[136:139],a[204:207],a[180:183],v[136:139]\n"
"s_add_u32 m0,s62,65536\n"
TR "a[218:219],v0 offset:33280\n"
MF "v[144:147],a[204:207],a[184:187],v[144:147]\n"
"s_add_u32 s82,s66,6291456\n"
"ds_read_b128 a[60:63],v2 offset:40960\n"
MF "v[144:147],a[200:203],a[144:147],v[144:147]\n"
"s_sub_u32 s15,s82,7864320\n"
TR "a[220:221],v0 offset:36864\n"
MF "v[152:155],a[200:203],a[148:151],v[152:155]\n"
"s_min_u32 s82,s82,s15\n"
TR "a[222:223],v0 offset:37376\n"
MF "v[152:155],a[204:207],a[188:191],v[152:155]\n"
LD "v6,s[28:31],s82 offen lds\n"
MF "v[160:163],a[204:207],a[192:195],v[160:163]\n"
"s_mul_i32 s59,s58,3072\n"
"ds_read_b128 a[100:103],v3 offset:40960\n"
MF "v[160:163],a[200:203],a[152:155],v[160:163]\n"
"s_add_u32 s16,s70,s59\n"
MF "v[168:171],a[200:203],a[156:159],v[168:171]\n"
"s_mul_i32 s59,s58,6144\n"
MF "v[168:171],a[204:207],a[196:199],v[168:171]\n"
LD "v11,s[28:31],s82 offen offset:1024 lds\n"
MF "v[172:175],a[212:215],a[196:199],v[172:175]\n"
"s_add_u32 s18,s70,s59\n"
TR "a[200:201],v0 offset:32776\n"
MF "v[172:175],a[208:211],a[156:159],v[172:175]\n"
"s_add_u32 s17,s16,196608\n"
TR "a[202:203],v0 offset:33288\n"
MF "v[164:167],a[208:211],a[152:155],v[164:167]\n"
"s_add_u32 s19,s18,196608\n"
"ds_read_b128 a[64:67],v2 offset:43008\n"
MF "v[164:167],a[212:215],a[192:195],v[164:167]\n"
"s_add_u32 s20,s70,196608\n"
TR "a[204:205],v0 offset:36872\n"
MF "v[156:159],a[212:215],a[188:191],v[156:159]\n"
"s_bfe_u32 s58,s23,0x40008\n"
TR "a[206:207],v0 offset:37384\n"
MF "v[156:159],a[208:211],a[148:151],v[156:159]\n"
"s_lshr_b32 s59,s56,1\n"
MF "v[148:151],a[208:211],a[144:147],v[148:151]\n"
"s_mul_i32 s59,s59,5\n"
"ds_read_b128 a[104:107],v3 offset:43008\n"
MF "v[148:151],a[212:215],a[184:187],v[148:151]\n"
"s_add_u32 s58,s58,s59\n"
MF "v[140:143],a[212:215],a[180:183],v[140:143]\n"
LD "v12,s[28:31],s82 offen offset:2048 lds\n"
MF "v[140:143],a[208:211],a[140:143],v[140:143]\n"
"s_waitcnt lgkmcnt(7)\n"
MF "v[176:179],a[216:219],a[140:143],v[176:179]\n"
"s_and_b32 s58,s58,15\n"
TR "a[208:209],v1 offset:32768\n"
MF "v[176:179],a[220:223],a[180:183],v[176:179]\n"
"s_mul_i32 s58,s58,12288\n"
TR "a[210:211],v1 offset:33280\n"
MF "v[184:187],a[220:223],a[184:187],v[184:187]\n"
"s_add_u32 s72,s16,s58\n"
"ds_read_b128 a[68:71],v2 offset:45056\n"
MF "v[184:187],a[216:219],a[144:147],v[184:187]\n"
"s_add_u32 s73,s18,s58\n"
TR "a[212:213],v1 offset:36864\n"
MF "v[192:195],a[216:219],a[148:151],v[192:195]\n"
"s_add_u32 s74,s70,s58\n"
TR "a[214:215],v1 offset:37376\n"
MF "v[192:195],a[220:223],a[188:191],v[192:195]\n"
"s_add_u32 s74,s74,12288\n"
MF "v[200:203],a[220:223],a[192:195],v[200:203]\n"
"s_cmp_eq_u32 s74,s20\n"
"s_cselect_b32 s74,s70,s74\n"
"ds_read_b128 a[108:111],v3 offset:45056\n"
MF "v[200:203],a[216:219],a[152:155],v[200:203]\n"
LD "v13,s[28:31],s82 offen offset:3072 lds\n"
MF "v[208:211],a[216:219],a[156:159],v[208:211]\n"
"s_lshr_b32 s58,s56,1\n"
MF "v[208:211],a[220:223],a[196:199],v[208:211]\n"
"s_waitcnt lgkmcnt(7)\n"
MF "v[212:215],a[204:207],a[196:199],v[212:215]\n"
"s_mul_i32 s58,s58,6144\n"
TR "a[216:217],v1 offset:32776\n"
MF "v[212:215],a[200:203],a[156:159],v[212:215]\n"
"s_mul_i32 s64,s56,3072\n"
TR "a[218:219],v1 offset:33288\n"
MF "v[204:207],a[200:203],a[152:155],v[204:207]\n"
"s_add_u32 s64,s64,s58\n"
"ds_read_b128 a[72:75],v2 offset:47104\n"
MF "v[204:207],a[204:207],a[192:195],v[204:207]\n"
"s_mul_i32 s65,s56,6144\n"
TR "a[220:221],v1 offset:36872\n"
MF "v[196:199],a[204:207],a[188:191],v[196:199]\n"
"s_sub_u32 s58,s71,s70\n"
TR "a[222:223],v1 offset:37384\n"
MF "v[196:199],a[200:203],a[148:151],v[196:199]\n"
"s_add_u32 s96,s74,s58\n"
MF "v[188:191],a[200:203],a[144:147],v[188:191]\n"
"s_sub_u32 s58,s16,s70\n"
"v_add_u32 v0,0xffff4000,v0\n"
MF "v[188:191],a[204:207],a[184:187],v[188:191]\n"
"s_add_u32 s96,s96,s58\n"
"v_add_u32 v1,0xffff4000,v1\n"
MF "v[180:183],a[204:207],a[180:183],v[180:183]\n"
"s_waitcnt vmcnt(13) lgkmcnt(0)\n"
MF "v[180:183],a[200:203],a[140:143],v[180:183]\n"
"s_barrier\n"
MF "v[216:219],a[208:211],a[140:143],v[216:219]\n"
"s_max_u32 s60,s70,s71\n"
TR "a[200:201],v1 offset:8\n"
MF "v[216:219],a[212:215],a[180:183],v[216:219]\n"
"s_add_u32 m0,s62,81920\n"
TR "a[202:203],v1 offset:520\n"
"v_mov_b32 v14,s12\n"
MF "v[224:227],a[212:215],a[184:187],v[224:227]\n"
"s_add_u32 s82,s66,7077888\n"
"ds_read_b128 a[112:115],v3 offset:47104\n"
"v_mul_f32 v14,0xbdd53b95,v14\n"
MF "v[224:227],a[208:211],a[144:147],v[224:227]\n"
"s_sub_u32 s15,s82,7864320\n"
TR "a[204:205],v1 offset:4104\n"
MF "v[232:235],a[208:211],a[148:151],v[232:235]\n"
"s_min_u32 s82,s82,s15\n"
TR "a[206:207],v1 offset:4616\n"
MF "v[232:235],a[212:215],a[188:191],v[232:235]\n"
LD "v6,s[28:31],s82 offen lds\n"
MF "v[240:243],a[212:215],a[192:195],v[240:243]\n"
"s_add_u32 s34,s60,196608\n"
"ds_read_b128 a[76:79],v2 offset:49152\n"
MF "v[240:243],a[208:211],a[152:155],v[240:243]\n"
"s_add_u32 s38,s60,196608\n"
"ds_read_b128 a[116:119],v3 offset:49152\n"
MF "v[248:251],a[208:211],a[156:159],v[248:251]\n"
MF "v[248:251],a[212:215],a[196:199],v[248:251]\n"
LD "v11,s[28:31],s82 offen offset:1024 lds\n"
MF "v[220:223],a[216:219],a[140:143],v[220:223]\n"
TR "a[208:209],v1 offset:16392\n"
MF "v[220:223],a[220:223],a[180:183],v[220:223]\n"
TR "a[210:211],v1 offset:16904\n"
MF "v[228:231],a[220:223],a[184:187],v[228:231]\n"
"ds_read_b128 a[140:143],v2\n"
MF "v[228:231],a[216:219],a[144:147],v[228:231]\n"
TR "a[212:213],v1 offset:20488\n"
MF "v[236:239],a[216:219],a[148:151],v[236:239]\n"
TR "a[214:215],v1 offset:21000\n"
MF "v[236:239],a[220:223],a[188:191],v[236:239]\n"
"ds_read_b128 a[180:183],v3\n"
MF "v[244:247],a[220:223],a[192:195],v[244:247]\n"
"ds_read_b128 a[144:147],v2 offset:2048\n"
MF "v[244:247],a[216:219],a[152:155],v[244:247]\n"
"ds_read_b128 a[184:187],v3 offset:2048\n"
MF "v[252:255],a[216:219],a[156:159],v[252:255]\n"
"ds_read_b128 a[148:151],v2 offset:4096\n"
MF "v[252:255],a[220:223],a[196:199],v[252:255]\n"
"s_waitcnt lgkmcnt(9)\n"
"ds_read_b128 a[188:191],v3 offset:4096\n"
MF "v[220:223],a[200:203],a[60:63],v[220:223]\n"
TR "a[216:217],v1\n"
MF "v[220:223],a[204:207],a[100:103],v[220:223]\n"
TR "a[218:219],v1 offset:512\n"
MF "v[228:231],a[204:207],a[104:107],v[228:231]\n"
"ds_read_b128 a[152:155],v2 offset:6144\n"
MF "v[228:231],a[200:203],a[64:67],v[228:231]\n"
TR "a[220:221],v1 offset:4096\n"
MF "v[236:239],a[200:203],a[68:71],v[236:239]\n"
TR "a[222:223],v1 offset:4608\n"
MF "v[236:239],a[204:207],a[108:111],v[236:239]\n"
"ds_read_b128 a[192:195],v3 offset:6144\n"
MF "v[244:247],a[204:207],a[112:115],v[244:247]\n"
"ds_read_b128 a[156:159],v2 offset:8192\n"
MF "v[244:247],a[200:203],a[72:75],v[244:247]\n"
"ds_read_b128 a[196:199],v3 offset:8192\n"
MF "v[252:255],a[200:203],a[76:79],v[252:255]\n"
LD "v12,s[28:31],s82 offen offset:2048 lds\n"
MF "v[252:255],a[204:207],a[116:119],v[252:255]\n"
"s_waitcnt lgkmcnt(8)\n"
LD "v13,s[28:31],s82 offen offset:3072 lds\n"
MF "v[220:223],a[208:211],a[140:143],v[220:223]\n"
TR "a[200:201],v1 offset:16384\n"
MF "v[220:223],a[212:215],a[180:183],v[220:223]\n"
TR "a[202:203],v1 offset:16896\n"
MF "v[228:231],a[212:215],a[184:187],v[228:231]\n"
MF "v[228:231],a[208:211],a[144:147],v[228:231]\n"
TR "a[204:205],v1 offset:20480\n"
MF "v[236:239],a[208:211],a[148:151],v[236:239]\n"
TR "a[206:207],v1 offset:20992\n"
MF "v[236:239],a[212:215],a[188:191],v[236:239]\n"
"s_waitcnt lgkmcnt(4)\n"
MF "v[244:247],a[212:215],a[192:195],v[244:247]\n"
MF "v[244:247],a[208:211],a[152:155],v[244:247]\n"
MF "v[252:255],a[208:211],a[156:159],v[252:255]\n"
MF "v[252:255],a[212:215],a[196:199],v[252:255]\n"
"s_waitcnt lgkmcnt(0)\n"
"s_cmp_eq_u32 s69,98\n"
"s_cbranch_scc1 .Lqy\n"
".Lqf:\n"
MF "v[216:219],a[216:219],a[60:63],v[216:219]\n"
TR "a[208:209],v0 offset:8\n"
MF "v[216:219],a[220:223],a[100:103],v[216:219]\n"
TR "a[210:211],v0 offset:520\n"
MF "v[224:227],a[220:223],a[104:107],v[224:227]\n"
"buffer_load_dword a224,v15,s[36:39],s70 offen offset:2688 nt\n"
MF "v[224:227],a[216:219],a[64:67],v[224:227]\n"
TR "a[212:213],v0 offset:4104\n"
MF "v[232:235],a[216:219],a[68:71],v[232:235]\n"
TR "a[214:215],v0 offset:4616\n"
MF "v[232:235],a[220:223],a[108:111],v[232:235]\n"
"buffer_load_dword a225,v15,s[36:39],s71 offen offset:2688 nt\n"
MF "v[240:243],a[220:223],a[112:115],v[240:243]\n"
MF "v[240:243],a[216:219],a[72:75],v[240:243]\n"
"buffer_load_dword a226,v15,s[48:51],s7 offen offset:2688\n"
MF "v[248:251],a[216:219],a[76:79],v[248:251]\n"
MF "v[248:251],a[220:223],a[116:119],v[248:251]\n"
MF "v[248:251],a[200:203],a[156:159],v[248:251]\n"
TR "a[216:217],v0 offset:16392\n"
MF "v[248:251],a[204:207],a[196:199],v[248:251]\n"
TR "a[218:219],v0 offset:16904\n"
MF "v[240:243],a[204:207],a[192:195],v[240:243]\n"
MF "v[240:243],a[200:203],a[152:155],v[240:243]\n"
TR "a[220:221],v0 offset:20488\n"
MF "v[232:235],a[200:203],a[148:151],v[232:235]\n"
TR "a[222:223],v0 offset:21000\n"
MF "v[232:235],a[204:207],a[188:191],v[232:235]\n"
MF "v[224:227],a[204:207],a[184:187],v[224:227]\n"
MF "v[224:227],a[200:203],a[144:147],v[224:227]\n"
"v_fma_f32 v248,v248,s54,v14\n"
"v_fma_f32 v249,v249,s54,v14\n"
MF "v[216:219],a[200:203],a[140:143],v[216:219]\n"
"v_fma_f32 v250,v250,s54,v14\n"
"v_fma_f32 v251,v251,s54,v14\n"
MF "v[216:219],a[204:207],a[180:183],v[216:219]\n"
"s_waitcnt lgkmcnt(4)\n"
"v_fma_f32 v252,v252,s54,v14\n"
"v_fma_f32 v253,v253,s54,v14\n"
MF "v[180:183],a[208:211],a[60:63],v[180:183]\n"
TR "a[200:201],v0\n"
"v_fma_f32 v254,v254,s54,v14\n"
MF "v[180:183],a[212:215],a[100:103],v[180:183]\n"
TR "a[202:203],v0 offset:512\n"
"v_fma_f32 v255,v255,s54,v14\n"
MF "v[188:191],a[212:215],a[104:107],v[188:191]\n"
CV "v255,v254,v255\n"
CV "v254,v252,v253\n"
MF "v[188:191],a[208:211],a[64:67],v[188:191]\n"
TR "a[204:205],v0 offset:4096\n"
CV "v253,v250,v251\n"
MF "v[196:199],a[208:211],a[68:71],v[196:199]\n"
TR "a[206:207],v0 offset:4608\n"
CV "v252,v248,v249\n"
MF "v[196:199],a[212:215],a[108:111],v[196:199]\n"
"v_fma_f32 v240,v240,s54,v14\n"
"v_fma_f32 v241,v241,s54,v14\n"
MF "v[204:207],a[212:215],a[112:115],v[204:207]\n"
"v_fma_f32 v242,v242,s54,v14\n"
"v_fma_f32 v243,v243,s54,v14\n"
MF "v[204:207],a[208:211],a[72:75],v[204:207]\n"
"v_fma_f32 v244,v244,s54,v14\n"
"v_fma_f32 v245,v245,s54,v14\n"
MF "v[212:215],a[208:211],a[76:79],v[212:215]\n"
"v_fma_f32 v246,v246,s54,v14\n"
"v_fma_f32 v247,v247,s54,v14\n"
MF "v[212:215],a[212:215],a[116:119],v[212:215]\n"
"s_waitcnt lgkmcnt(4)\n"
CV "v251,v246,v247\n"
CV "v250,v244,v245\n"
MF "v[180:183],a[216:219],a[140:143],v[180:183]\n"
TR "a[208:209],v0 offset:16384\n"
CV "v249,v242,v243\n"
MF "v[180:183],a[220:223],a[180:183],v[180:183]\n"
TR "a[210:211],v0 offset:16896\n"
CV "v248,v240,v241\n"
MF "v[188:191],a[220:223],a[184:187],v[188:191]\n"
"v_fma_f32 v232,v232,s54,v14\n"
"v_fma_f32 v233,v233,s54,v14\n"
MF "v[188:191],a[216:219],a[144:147],v[188:191]\n"
TR "a[212:213],v0 offset:20480\n"
"v_fma_f32 v234,v234,s54,v14\n"
MF "v[196:199],a[216:219],a[148:151],v[196:199]\n"
TR "a[214:215],v0 offset:20992\n"
"v_fma_f32 v235,v235,s54,v14\n"
MF "v[196:199],a[220:223],a[188:191],v[196:199]\n"
"v_fma_f32 v236,v236,s54,v14\n"
"v_fma_f32 v237,v237,s54,v14\n"
MF "v[204:207],a[220:223],a[192:195],v[204:207]\n"
"v_fma_f32 v238,v238,s54,v14\n"
"v_fma_f32 v239,v239,s54,v14\n"
MF "v[204:207],a[216:219],a[152:155],v[204:207]\n"
CV "v247,v238,v239\n"
CV "v246,v236,v237\n"
MF "v[212:215],a[216:219],a[156:159],v[212:215]\n"
"s_waitcnt vmcnt(11) lgkmcnt(0)\n"
CV "v245,v234,v235\n"
CV "v244,v232,v233\n"
MF "v[212:215],a[220:223],a[196:199],v[212:215]\n"
"s_barrier\n"
MF "v[176:179],a[200:203],a[60:63],v[176:179]\n"
TR "a[216:217],v1 offset:32776\n"
"v_fma_f32 v224,v224,s54,v14\n"
MF "v[176:179],a[204:207],a[100:103],v[176:179]\n"
"s_add_u32 m0,s64,0\n"
TR "a[218:219],v1 offset:33288\n"
"v_fma_f32 v225,v225,s54,v14\n"
MF "v[184:187],a[204:207],a[104:107],v[184:187]\n"
LD "v7,s[32:35],s72 offen nt lds\n"
MF "v[184:187],a[200:203],a[64:67],v[184:187]\n"
TR "a[220:221],v1 offset:36872\n"
"v_fma_f32 v226,v226,s54,v14\n"
MF "v[192:195],a[200:203],a[68:71],v[192:195]\n"
TR "a[222:223],v1 offset:37384\n"
"v_fma_f32 v227,v227,s54,v14\n"
MF "v[192:195],a[204:207],a[108:111],v[192:195]\n"
"s_add_u32 m0,s64,896\n"
"v_fma_f32 v228,v228,s54,v14\n"
"v_fma_f32 v229,v229,s54,v14\n"
MF "v[200:203],a[204:207],a[112:115],v[200:203]\n"
LD "v7,s[32:35],s72 offen offset:128 nt lds\n"
MF "v[200:203],a[200:203],a[72:75],v[200:203]\n"
"v_fma_f32 v230,v230,s54,v14\n"
"v_fma_f32 v231,v231,s54,v14\n"
MF "v[208:211],a[200:203],a[76:79],v[208:211]\n"
CV "v243,v230,v231\n"
CV "v242,v228,v229\n"
MF "v[208:211],a[204:207],a[116:119],v[208:211]\n"
CV "v241,v226,v227\n"
CV "v240,v224,v225\n"
MF "v[208:211],a[208:211],a[156:159],v[208:211]\n"
TR "a[200:201],v1 offset:49160\n"
"v_fma_f32 v216,v216,s54,v14\n"
MF "v[208:211],a[212:215],a[196:199],v[208:211]\n"
"s_add_u32 m0,s64,1792\n"
TR "a[202:203],v1 offset:49672\n"
"v_fma_f32 v217,v217,s54,v14\n"
MF "v[200:203],a[212:215],a[192:195],v[200:203]\n"
LD "v7,s[32:35],s72 offen offset:256 nt lds\n"
MF "v[200:203],a[208:211],a[152:155],v[200:203]\n"
TR "a[204:205],v1 offset:53256\n"
"v_fma_f32 v218,v218,s54,v14\n"
MF "v[192:195],a[208:211],a[148:151],v[192:195]\n"
TR "a[206:207],v1 offset:53768\n"
"v_fma_f32 v219,v219,s54,v14\n"
MF "v[192:195],a[212:215],a[188:191],v[192:195]\n"
"v_fma_f32 v220,v220,s54,v14\n"
"v_fma_f32 v221,v221,s54,v14\n"
MF "v[184:187],a[212:215],a[184:187],v[184:187]\n"
"s_add_u32 s72,s72,6144\n"
"v_fma_f32 v222,v222,s54,v14\n"
"v_fma_f32 v223,v223,s54,v14\n"
MF "v[184:187],a[208:211],a[144:147],v[184:187]\n"
"s_add_u32 m0,s64,6144\n"
CV "v239,v222,v223\n"
CV "v238,v220,v221\n"
MF "v[176:179],a[208:211],a[140:143],v[176:179]\n"
LD "v7,s[32:35],s72 offen nt lds\n"
MF "v[176:179],a[212:215],a[180:183],v[176:179]\n"
"s_waitcnt lgkmcnt(4)\n"
CV "v237,v218,v219\n"
CV "v236,v216,v217\n"
MF "v[140:143],a[216:219],a[60:63],v[140:143]\n"
TR "a[208:209],v1 offset:32768\n"
"v_fma_f32 v208,v208,s54,v14\n"
MF "v[140:143],a[220:223],a[100:103],v[140:143]\n"
TR "a[210:211],v1 offset:33280\n"
"v_fma_f32 v209,v209,s54,v14\n"
MF "v[148:151],a[220:223],a[104:107],v[148:151]\n"
"v_fma_f32 v210,v210,s54,v14\n"
"v_fma_f32 v211,v211,s54,v14\n"
MF "v[148:151],a[216:219],a[64:67],v[148:151]\n"
TR "a[212:213],v1 offset:36864\n"
"v_fma_f32 v212,v212,s54,v14\n"
MF "v[156:159],a[216:219],a[68:71],v[156:159]\n"
"s_add_u32 m0,s64,7040\n"
TR "a[214:215],v1 offset:37376\n"
"v_fma_f32 v213,v213,s54,v14\n"
MF "v[156:159],a[220:223],a[108:111],v[156:159]\n"
LD "v7,s[32:35],s72 offen offset:128 nt lds\n"
MF "v[164:167],a[220:223],a[112:115],v[164:167]\n"
"v_fma_f32 v214,v214,s54,v14\n"
"v_fma_f32 v215,v215,s54,v14\n"
MF "v[164:167],a[216:219],a[72:75],v[164:167]\n"
CV "v235,v214,v215\n"
CV "v234,v212,v213\n"
MF "v[172:175],a[216:219],a[76:79],v[172:175]\n"
"s_add_u32 m0,s64,7936\n"
CV "v233,v210,v211\n"
CV "v232,v208,v209\n"
MF "v[172:175],a[220:223],a[116:119],v[172:175]\n"
"s_waitcnt lgkmcnt(4)\n"
LD "v7,s[32:35],s72 offen offset:256 nt lds\n"
MF "v[140:143],a[200:203],a[140:143],v[140:143]\n"
TR "a[216:217],v1 offset:49152\n"
"v_fma_f32 v200,v200,s54,v14\n"
MF "v[140:143],a[204:207],a[180:183],v[140:143]\n"
TR "a[218:219],v1 offset:49664\n"
"v_fma_f32 v201,v201,s54,v14\n"
MF "v[148:151],a[204:207],a[184:187],v[148:151]\n"
"v_fma_f32 v202,v202,s54,v14\n"
"v_fma_f32 v203,v203,s54,v14\n"
MF "v[148:151],a[200:203],a[144:147],v[148:151]\n"
TR "a[220:221],v1 offset:53248\n"
"v_fma_f32 v204,v204,s54,v14\n"
MF "v[156:159],a[200:203],a[148:151],v[156:159]\n"
TR "a[222:223],v1 offset:53760\n"
"v_fma_f32 v205,v205,s54,v14\n"
MF "v[156:159],a[204:207],a[188:191],v[156:159]\n"
"v_fma_f32 v206,v206,s54,v14\n"
"v_fma_f32 v207,v207,s54,v14\n"
MF "v[164:167],a[204:207],a[192:195],v[164:167]\n"
CV "v231,v206,v207\n"
CV "v230,v204,v205\n"
MF "v[164:167],a[200:203],a[152:155],v[164:167]\n"
CV "v229,v202,v203\n"
CV "v228,v200,v201\n"
MF "v[172:175],a[200:203],a[156:159],v[172:175]\n"
"v_fma_f32 v192,v192,s54,v14\n"
"v_fma_f32 v193,v193,s54,v14\n"
MF "v[172:175],a[204:207],a[196:199],v[172:175]\n"
"s_waitcnt lgkmcnt(0)\n"
"v_fma_f32 v194,v194,s54,v14\n"
"v_fma_f32 v195,v195,s54,v14\n"
MF "v[136:139],a[208:211],a[60:63],v[136:139]\n"
TR "a[200:201],v0 offset:32776\n"
"v_fma_f32 v196,v196,s54,v14\n"
MF "v[136:139],a[212:215],a[100:103],v[136:139]\n"
TR "a[202:203],v0 offset:33288\n"
"v_fma_f32 v197,v197,s54,v14\n"
MF "v[144:147],a[212:215],a[104:107],v[144:147]\n"
"v_fma_f32 v198,v198,s54,v14\n"
"v_fma_f32 v199,v199,s54,v14\n"
MF "v[144:147],a[208:211],a[64:67],v[144:147]\n"
TR "a[204:205],v0 offset:36872\n"
CV "v227,v198,v199\n"
MF "v[152:155],a[208:211],a[68:71],v[152:155]\n"
TR "a[206:207],v0 offset:37384\n"
CV "v226,v196,v197\n"
MF "v[152:155],a[212:215],a[108:111],v[152:155]\n"
CV "v225,v194,v195\n"
CV "v224,v192,v193\n"
MF "v[160:163],a[212:215],a[112:115],v[160:163]\n"
"v_fma_f32 v184,v184,s54,v14\n"
"v_fma_f32 v185,v185,s54,v14\n"
MF "v[160:163],a[208:211],a[72:75],v[160:163]\n"
"v_fma_f32 v186,v186,s54,v14\n"
"v_fma_f32 v187,v187,s54,v14\n"
MF "v[168:171],a[208:211],a[76:79],v[168:171]\n"
"v_fma_f32 v188,v188,s54,v14\n"
"v_fma_f32 v189,v189,s54,v14\n"
MF "v[168:171],a[212:215],a[116:119],v[168:171]\n"
"v_fma_f32 v190,v190,s54,v14\n"
"v_fma_f32 v191,v191,s54,v14\n"
MF "v[168:171],a[216:219],a[156:159],v[168:171]\n"
TR "a[208:209],v0 offset:49160\n"
CV "v223,v190,v191\n"
MF "v[168:171],a[220:223],a[196:199],v[168:171]\n"
TR "a[210:211],v0 offset:49672\n"
CV "v222,v188,v189\n"
MF "v[160:163],a[220:223],a[192:195],v[160:163]\n"
CV "v221,v186,v187\n"
CV "v220,v184,v185\n"
MF "v[160:163],a[216:219],a[152:155],v[160:163]\n"
TR "a[212:213],v0 offset:53256\n"
"v_fma_f32 v176,v176,s54,v14\n"
MF "v[152:155],a[216:219],a[148:151],v[152:155]\n"
TR "a[214:215],v0 offset:53768\n"
"v_fma_f32 v177,v177,s54,v14\n"
MF "v[152:155],a[220:223],a[188:191],v[152:155]\n"
"v_fma_f32 v178,v178,s54,v14\n"
"v_fma_f32 v179,v179,s54,v14\n"
MF "v[144:147],a[220:223],a[184:187],v[144:147]\n"
"v_fma_f32 v180,v180,s54,v14\n"
"v_fma_f32 v181,v181,s54,v14\n"
MF "v[144:147],a[216:219],a[144:147],v[144:147]\n"
"v_fma_f32 v182,v182,s54,v14\n"
"v_fma_f32 v183,v183,s54,v14\n"
MF "v[136:139],a[216:219],a[140:143],v[136:139]\n"
CV "v219,v182,v183\n"
CV "v218,v180,v181\n"
MF "v[136:139],a[220:223],a[180:183],v[136:139]\n"
"s_waitcnt lgkmcnt(4)\n"
CV "v217,v178,v179\n"
CV "v216,v176,v177\n"
MF "v[100:103],a[200:203],a[60:63],v[100:103]\n"
TR "a[216:217],v0 offset:32768\n"
"v_fma_f32 v168,v168,s54,v14\n"
MF "v[100:103],a[204:207],a[100:103],v[100:103]\n"
TR "a[218:219],v0 offset:33280\n"
"v_fma_f32 v169,v169,s54,v14\n"
MF "v[108:111],a[204:207],a[104:107],v[108:111]\n"
"v_fma_f32 v170,v170,s54,v14\n"
"v_fma_f32 v171,v171,s54,v14\n"
MF "v[108:111],a[200:203],a[64:67],v[108:111]\n"
TR "a[220:221],v0 offset:36864\n"
"v_fma_f32 v172,v172,s54,v14\n"
MF "v[116:119],a[200:203],a[68:71],v[116:119]\n"
TR "a[222:223],v0 offset:37376\n"
"v_fma_f32 v173,v173,s54,v14\n"
MF "v[116:119],a[204:207],a[108:111],v[116:119]\n"
"v_fma_f32 v174,v174,s54,v14\n"
"v_fma_f32 v175,v175,s54,v14\n"
MF "v[124:127],a[204:207],a[112:115],v[124:127]\n"
".Lpg8b:\n"
"s_cbranch_execz .Lpg11b\n"
".Lpg8d:\n"
"s_cbranch_execz .Lisr2\n"
CV "v215,v174,v175\n"
CV "v214,v172,v173\n"
MF "v[124:127],a[200:203],a[72:75],v[124:127]\n"
CV "v213,v170,v171\n"
CV "v212,v168,v169\n"
MF "v[132:135],a[200:203],a[76:79],v[132:135]\n"
"v_fma_f32 v160,v160,s54,v14\n"
"v_fma_f32 v161,v161,s54,v14\n"
MF "v[132:135],a[204:207],a[116:119],v[132:135]\n"
"s_waitcnt lgkmcnt(4)\n"
"v_fma_f32 v162,v162,s54,v14\n"
"v_fma_f32 v163,v163,s54,v14\n"
MF "v[100:103],a[208:211],a[140:143],v[100:103]\n"
TR "a[200:201],v0 offset:49152\n"
"v_fma_f32 v164,v164,s54,v14\n"
MF "v[100:103],a[212:215],a[180:183],v[100:103]\n"
TR "a[202:203],v0 offset:49664\n"
"v_fma_f32 v165,v165,s54,v14\n"
MF "v[108:111],a[212:215],a[184:187],v[108:111]\n"
"v_fma_f32 v166,v166,s54,v14\n"
"v_fma_f32 v167,v167,s54,v14\n"
MF "v[108:111],a[208:211],a[144:147],v[108:111]\n"
TR "a[204:205],v0 offset:53248\n"
CV "v211,v166,v167\n"
MF "v[116:119],a[208:211],a[148:151],v[116:119]\n"
TR "a[206:207],v0 offset:53760\n"
CV "v210,v164,v165\n"
MF "v[116:119],a[212:215],a[188:191],v[116:119]\n"
CV "v209,v162,v163\n"
CV "v208,v160,v161\n"
MF "v[124:127],a[212:215],a[192:195],v[124:127]\n"
"v_add_u32 v0,0xc000,v0\n"
"v_fma_f32 v152,v152,s54,v14\n"
MF "v[124:127],a[208:211],a[152:155],v[124:127]\n"
"v_add_u32 v1,0xc000,v1\n"
"v_fma_f32 v153,v153,s54,v14\n"
MF "v[132:135],a[208:211],a[156:159],v[132:135]\n"
"s_waitcnt vmcnt(9) lgkmcnt(0)\n"
"v_fma_f32 v154,v154,s54,v14\n"
"v_fma_f32 v155,v155,s54,v14\n"
MF "v[132:135],a[212:215],a[196:199],v[132:135]\n"
"s_barrier\n"
MF "v[96:99],a[216:219],a[60:63],v[96:99]\n"
TR "a[208:209],v1 offset:16392\n"
"v_fma_f32 v156,v156,s54,v14\n"
MF "v[96:99],a[220:223],a[100:103],v[96:99]\n"
TR "a[210:211],v1 offset:16904\n"
"v_fma_f32 v157,v157,s54,v14\n"
MF "v[104:107],a[220:223],a[104:107],v[104:107]\n"
"s_add_u32 s72,s72,6144\n"
"v_fma_f32 v158,v158,s54,v14\n"
"v_fma_f32 v159,v159,s54,v14\n"
MF "v[104:107],a[216:219],a[64:67],v[104:107]\n"
"s_cmp_eq_u32 s72,s17\n"
"s_cselect_b32 s72,s16,s72\n"
TR "a[212:213],v1 offset:20488\n"
MF "v[112:115],a[216:219],a[68:71],v[112:115]\n"
"s_add_u32 m0,s64,24576\n"
TR "a[214:215],v1 offset:21000\n"
CV "v207,v158,v159\n"
MF "v[112:115],a[220:223],a[108:111],v[112:115]\n"
LD "v7,s[32:35],s72 offen nt lds\n"
MF "v[120:123],a[220:223],a[112:115],v[120:123]\n"
"s_add_u32 m0,s64,25472\n"
CV "v206,v156,v157\n"
CV "v205,v154,v155\n"
MF "v[120:123],a[216:219],a[72:75],v[120:123]\n"
LD "v7,s[32:35],s72 offen offset:128 nt lds\n"
MF "v[128:131],a[216:219],a[76:79],v[128:131]\n"
CV "v204,v152,v153\n"
"v_fma_f32 v144,v144,s54,v14\n"
MF "v[128:131],a[220:223],a[116:119],v[128:131]\n"
"v_fma_f32 v145,v145,s54,v14\n"
"v_fma_f32 v146,v146,s54,v14\n"
MF "v[128:131],a[200:203],a[156:159],v[128:131]\n"
TR "a[216:217],v1 offset:32776\n"
"v_fma_f32 v147,v147,s54,v14\n"
MF "v[128:131],a[204:207],a[196:199],v[128:131]\n"
"s_add_u32 m0,s64,26368\n"
TR "a[218:219],v1 offset:33288\n"
"v_fma_f32 v148,v148,s54,v14\n"
MF "v[120:123],a[204:207],a[192:195],v[120:123]\n"
LD "v7,s[32:35],s72 offen offset:256 nt lds\n"
MF "v[120:123],a[200:203],a[152:155],v[120:123]\n"
TR "a[220:221],v1 offset:36872\n"
"v_fma_f32 v149,v149,s54,v14\n"
MF "v[112:115],a[200:203],a[148:151],v[112:115]\n"
TR "a[222:223],v1 offset:37384\n"
"v_fma_f32 v150,v150,s54,v14\n"
MF "v[112:115],a[204:207],a[188:191],v[112:115]\n"
"v_fma_f32 v151,v151,s54,v14\n"
CV "v203,v150,v151\n"
MF "v[104:107],a[204:207],a[184:187],v[104:107]\n"
"s_add_u32 s72,s72,6144\n"
CV "v202,v148,v149\n"
CV "v201,v146,v147\n"
MF "v[104:107],a[200:203],a[144:147],v[104:107]\n"
"s_add_u32 m0,s64,30720\n"
CV "v200,v144,v145\n"
"v_fma_f32 v136,v136,s54,v14\n"
MF "v[96:99],a[200:203],a[140:143],v[96:99]\n"
LD "v7,s[32:35],s72 offen nt lds\n"
MF "v[96:99],a[204:207],a[180:183],v[96:99]\n"
"s_waitcnt lgkmcnt(4)\n"
"v_fma_f32 v137,v137,s54,v14\n"
"v_fma_f32 v138,v138,s54,v14\n"
MF "v[60:63],a[208:211],a[60:63],v[60:63]\n"
TR "a[200:201],v1 offset:16384\n"
"v_fma_f32 v139,v139,s54,v14\n"
MF "v[60:63],a[212:215],a[100:103],v[60:63]\n"
TR "a[202:203],v1 offset:16896\n"
"v_fma_f32 v140,v140,s54,v14\n"
MF "v[68:71],a[212:215],a[104:107],v[68:71]\n"
"v_fma_f32 v141,v141,s54,v14\n"
"v_fma_f32 v142,v142,s54,v14\n"
MF "v[68:71],a[208:211],a[64:67],v[68:71]\n"
TR "a[204:205],v1 offset:20480\n"
"v_fma_f32 v143,v143,s54,v14\n"
MF "v[76:79],a[208:211],a[68:71],v[76:79]\n"
"s_add_u32 m0,s64,31616\n"
TR "a[206:207],v1 offset:20992\n"
CV "v199,v142,v143\n"
MF "v[76:79],a[212:215],a[108:111],v[76:79]\n"
LD "v7,s[32:35],s72 offen offset:128 nt lds\n"
MF "v[84:87],a[212:215],a[112:115],v[84:87]\n"
CV "v198,v140,v141\n"
CV "v197,v138,v139\n"
MF "v[84:87],a[208:211],a[72:75],v[84:87]\n"
CV "v196,v136,v137\n"
"v_fma_f32 v128,v128,s54,v14\n"
MF "v[92:95],a[208:211],a[76:79],v[92:95]\n"
"s_add_u32 m0,s64,32512\n"
"v_fma_f32 v129,v129,s54,v14\n"
"v_fma_f32 v130,v130,s54,v14\n"
MF "v[92:95],a[212:215],a[116:119],v[92:95]\n"
"s_waitcnt lgkmcnt(4)\n"
LD "v7,s[32:35],s72 offen offset:256 nt lds\n"
MF "v[60:63],a[216:219],a[140:143],v[60:63]\n"
TR "a[208:209],v1 offset:32768\n"
"v_fma_f32 v131,v131,s54,v14\n"
MF "v[60:63],a[220:223],a[180:183],v[60:63]\n"
TR "a[210:211],v1 offset:33280\n"
"v_fma_f32 v132,v132,s54,v14\n"
MF "v[68:71],a[220:223],a[184:187],v[68:71]\n"
"v_fma_f32 v133,v133,s54,v14\n"
"v_fma_f32 v134,v134,s54,v14\n"
MF "v[68:71],a[216:219],a[144:147],v[68:71]\n"
TR "a[212:213],v1 offset:36864\n"
"v_fma_f32 v135,v135,s54,v14\n"
MF "v[76:79],a[216:219],a[148:151],v[76:79]\n"
TR "a[214:215],v1 offset:37376\n"
CV "v195,v134,v135\n"
MF "v[76:79],a[220:223],a[188:191],v[76:79]\n"
CV "v194,v132,v133\n"
CV "v193,v130,v131\n"
MF "v[84:87],a[220:223],a[192:195],v[84:87]\n"
CV "v192,v128,v129\n"
"v_fma_f32 v120,v120,s54,v14\n"
MF "v[84:87],a[216:219],a[152:155],v[84:87]\n"
"v_fma_f32 v121,v121,s54,v14\n"
"v_fma_f32 v122,v122,s54,v14\n"
MF "v[92:95],a[216:219],a[156:159],v[92:95]\n"
"v_fma_f32 v123,v123,s54,v14\n"
"v_fma_f32 v124,v124,s54,v14\n"
MF "v[92:95],a[220:223],a[196:199],v[92:95]\n"
"s_waitcnt lgkmcnt(0)\n"
"v_fma_f32 v125,v125,s54,v14\n"
"v_fma_f32 v126,v126,s54,v14\n"
MF "v[56:59],a[200:203],a[60:63],v[56:59]\n"
TR "a[216:217],v0 offset:16392\n"
"v_fma_f32 v127,v127,s54,v14\n"
MF "v[56:59],a[204:207],a[100:103],v[56:59]\n"
TR "a[218:219],v0 offset:16904\n"
CV "v191,v126,v127\n"
MF "v[64:67],a[204:207],a[104:107],v[64:67]\n"
CV "v190,v124,v125\n"
CV "v189,v122,v123\n"
MF "v[64:67],a[200:203],a[64:67],v[64:67]\n"
TR "a[220:221],v0 offset:20488\n"
CV "v188,v120,v121\n"
MF "v[72:75],a[200:203],a[68:71],v[72:75]\n"
TR "a[222:223],v0 offset:21000\n"
"v_fma_f32 v112,v112,s54,v14\n"
MF "v[72:75],a[204:207],a[108:111],v[72:75]\n"
"v_fma_f32 v113,v113,s54,v14\n"
"v_fma_f32 v114,v114,s54,v14\n"
MF "v[80:83],a[204:207],a[112:115],v[80:83]\n"
"v_fma_f32 v115,v115,s54,v14\n"
"v_fma_f32 v116,v116,s54,v14\n"
MF "v[80:83],a[200:203],a[72:75],v[80:83]\n"
"v_fma_f32 v117,v117,s54,v14\n"
"v_fma_f32 v118,v118,s54,v14\n"
MF "v[88:91],a[200:203],a[76:79],v[88:91]\n"
"v_fma_f32 v119,v119,s54,v14\n"
CV "v187,v118,v119\n"
MF "v[88:91],a[204:207],a[116:119],v[88:91]\n"
CV "v186,v116,v117\n"
CV "v185,v114,v115\n"
MF "v[88:91],a[208:211],a[156:159],v[88:91]\n"
TR "a[200:201],v0 offset:32776\n"
CV "v184,v112,v113\n"
MF "v[88:91],a[212:215],a[196:199],v[88:91]\n"
TR "a[202:203],v0 offset:33288\n"
"v_fma_f32 v104,v104,s54,v14\n"
MF "v[80:83],a[212:215],a[192:195],v[80:83]\n"
"v_fma_f32 v105,v105,s54,v14\n"
"v_fma_f32 v106,v106,s54,v14\n"
MF "v[80:83],a[208:211],a[152:155],v[80:83]\n"
TR "a[204:205],v0 offset:36872\n"
"v_fma_f32 v107,v107,s54,v14\n"
MF "v[72:75],a[208:211],a[148:151],v[72:75]\n"
TR "a[206:207],v0 offset:37384\n"
"v_fma_f32 v108,v108,s54,v14\n"
MF "v[72:75],a[212:215],a[188:191],v[72:75]\n"
"v_fma_f32 v109,v109,s54,v14\n"
"v_fma_f32 v110,v110,s54,v14\n"
MF "v[64:67],a[212:215],a[184:187],v[64:67]\n"
"v_fma_f32 v111,v111,s54,v14\n"
CV "v183,v110,v111\n"
MF "v[64:67],a[208:211],a[144:147],v[64:67]\n"
CV "v182,v108,v109\n"
CV "v181,v106,v107\n"
MF "v[56:59],a[208:211],a[140:143],v[56:59]\n"
CV "v180,v104,v105\n"
"v_fma_f32 v96,v96,s54,v14\n"
MF "v[56:59],a[212:215],a[180:183],v[56:59]\n"
"s_waitcnt lgkmcnt(4)\n"
"v_fma_f32 v97,v97,s54,v14\n"
"v_fma_f32 v98,v98,s54,v14\n"
MF "v[20:23],a[216:219],a[60:63],v[20:23]\n"
TR "a[208:209],v0 offset:16384\n"
"v_fma_f32 v99,v99,s54,v14\n"
MF "v[20:23],a[220:223],a[100:103],v[20:23]\n"
TR "a[210:211],v0 offset:16896\n"
"v_fma_f32 v100,v100,s54,v14\n"
MF "v[28:31],a[220:223],a[104:107],v[28:31]\n"
"v_fma_f32 v101,v101,s54,v14\n"
"v_fma_f32 v102,v102,s54,v14\n"
MF "v[28:31],a[216:219],a[64:67],v[28:31]\n"
TR "a[212:213],v0 offset:20480\n"
"v_fma_f32 v103,v103,s54,v14\n"
MF "v[36:39],a[216:219],a[68:71],v[36:39]\n"
TR "a[214:215],v0 offset:20992\n"
CV "v179,v102,v103\n"
MF "v[36:39],a[220:223],a[108:111],v[36:39]\n"
CV "v178,v100,v101\n"
CV "v177,v98,v99\n"
MF "v[44:47],a[220:223],a[112:115],v[44:47]\n"
CV "v176,v96,v97\n"
"v_fma_f32 v88,v88,s54,v14\n"
MF "v[44:47],a[216:219],a[72:75],v[44:47]\n"
"v_fma_f32 v89,v89,s54,v14\n"
"v_fma_f32 v90,v90,s54,v14\n"
MF "v[52:55],a[216:219],a[76:79],v[52:55]\n"
"v_fma_f32 v91,v91,s54,v14\n"
"v_fma_f32 v92,v92,s54,v14\n"
MF "v[52:55],a[220:223],a[116:119],v[52:55]\n"
"s_waitcnt lgkmcnt(4)\n"
"v_fma_f32 v93,v93,s54,v14\n"
"v_fma_f32 v94,v94,s54,v14\n"
MF "v[20:23],a[200:203],a[140:143],v[20:23]\n"
TR "a[216:217],v0 offset:32768\n"
"v_fma_f32 v95,v95,s54,v14\n"
MF "v[20:23],a[204:207],a[180:183],v[20:23]\n"
TR "a[218:219],v0 offset:33280\n"
CV "v175,v94,v95\n"
MF "v[28:31],a[204:207],a[184:187],v[28:31]\n"
CV "v174,v92,v93\n"
CV "v173,v90,v91\n"
MF "v[28:31],a[200:203],a[144:147],v[28:31]\n"
TR "a[220:221],v0 offset:36864\n"
CV "v172,v88,v89\n"
MF "v[36:39],a[200:203],a[148:151],v[36:39]\n"
TR "a[222:223],v0 offset:37376\n"
"v_fma_f32 v80,v80,s54,v14\n"
MF "v[36:39],a[204:207],a[188:191],v[36:39]\n"
"v_fma_f32 v81,v81,s54,v14\n"
"v_fma_f32 v82,v82,s54,v14\n"
MF "v[44:47],a[204:207],a[192:195],v[44:47]\n"
"v_fma_f32 v83,v83,s54,v14\n"
"v_fma_f32 v84,v84,s54,v14\n"
MF "v[44:47],a[200:203],a[152:155],v[44:47]\n"
"v_fma_f32 v85,v85,s54,v14\n"
"v_fma_f32 v86,v86,s54,v14\n"
MF "v[52:55],a[200:203],a[156:159],v[52:55]\n"
"s_waitcnt lgkmcnt(0)\n"
"v_fma_f32 v87,v87,s54,v14\n"
CV "v171,v86,v87\n"
MF "v[52:55],a[204:207],a[196:199],v[52:55]\n"
"s_barrier\n"
MF "v[16:19],a[208:211],a[60:63],v[16:19]\n"
"s_add_u32 m0,s65,49152\n"
CV "v170,v84,v85\n"
CV "v169,v82,v83\n"
MF "v[16:19],a[212:215],a[100:103],v[16:19]\n"
LD "v8,s[36:39],s73 offen nt lds\n"
MF "v[24:27],a[212:215],a[104:107],v[24:27]\n"
"s_add_u32 m0,s65,50048\n"
CV "v168,v80,v81\n"
"v_fma_f32 v72,v72,s54,v14\n"
MF "v[24:27],a[208:211],a[64:67],v[24:27]\n"
LD "v8,s[36:39],s73 offen offset:128 nt lds\n"
MF "v[32:35],a[208:211],a[68:71],v[32:35]\n"
"v_fma_f32 v73,v73,s54,v14\n"
"v_fma_f32 v74,v74,s54,v14\n"
MF "v[32:35],a[212:215],a[108:111],v[32:35]\n"
"s_add_u32 m0,s65,50944\n"
"v_fma_f32 v75,v75,s54,v14\n"
"v_fma_f32 v76,v76,s54,v14\n"
MF "v[40:43],a[212:215],a[112:115],v[40:43]\n"
LD "v8,s[36:39],s73 offen offset:256 nt lds\n"
MF "v[40:43],a[208:211],a[72:75],v[40:43]\n"
"v_fma_f32 v77,v77,s54,v14\n"
"v_fma_f32 v78,v78,s54,v14\n"
MF "v[48:51],a[208:211],a[76:79],v[48:51]\n"
"s_add_u32 m0,s65,49152\n"
"v_fma_f32 v79,v79,s54,v14\n"
CV "v167,v78,v79\n"
MF "v[48:51],a[212:215],a[116:119],v[48:51]\n"
LD "v8,s[36:39],s73 offen offset:3072 nt lds\n"
MF "v[48:51],a[216:219],a[156:159],v[48:51]\n"
"s_add_u32 m0,s65,50048\n"
CV "v166,v76,v77\n"
CV "v165,v74,v75\n"
MF "v[48:51],a[220:223],a[196:199],v[48:51]\n"
LD "v8,s[36:39],s73 offen offset:3200 nt lds\n"
MF "v[40:43],a[220:223],a[192:195],v[40:43]\n"
CV "v164,v72,v73\n"
"v_fma_f32 v64,v64,s54,v14\n"
MF "v[40:43],a[216:219],a[152:155],v[40:43]\n"
"s_add_u32 m0,s65,50944\n"
"v_fma_f32 v65,v65,s54,v14\n"
"v_fma_f32 v66,v66,s54,v14\n"
MF "v[32:35],a[216:219],a[148:151],v[32:35]\n"
LD "v8,s[36:39],s73 offen offset:3328 nt lds\n"
MF "v[32:35],a[220:223],a[188:191],v[32:35]\n"
"v_fma_f32 v67,v67,s54,v14\n"
"v_fma_f32 v68,v68,s54,v14\n"
MF "v[24:27],a[220:223],a[184:187],v[24:27]\n"
"v_fma_f32 v69,v69,s54,v14\n"
"v_fma_f32 v70,v70,s54,v14\n"
MF "v[24:27],a[216:219],a[144:147],v[24:27]\n"
"v_fma_f32 v71,v71,s54,v14\n"
CV "v163,v70,v71\n"
MF "v[16:19],a[216:219],a[140:143],v[16:19]\n"
CV "v162,v68,v69\n"
CV "v161,v66,v67\n"
MF "v[16:19],a[220:223],a[180:183],v[16:19]\n"
CV "v160,v64,v65\n"
AR "v0,a244\n"
"v_xor_b32 v1,64,v0\n"
"s_waitcnt vmcnt(12) lgkmcnt(0)\n"
"s_barrier\n"
"ds_read_b128 v[92:95],v0\n"
"ds_read_b128 v[96:99],v1\n"
"ds_read_b128 v[100:103],v0 offset:1024\n"
"v_pk_fma_f32 v[56:57],v[56:57],s[54:55],v[14:15] op_sel_hi:[1,1,0]\n"
"v_pk_fma_f32 v[58:59],v[58:59],s[54:55],v[14:15] op_sel_hi:[1,1,0]\n"
"v_pk_fma_f32 v[60:61],v[60:61],s[54:55],v[14:15] op_sel_hi:[1,1,0]\n"
"v_pk_fma_f32 v[62:63],v[62:63],s[54:55],v[14:15] op_sel_hi:[1,1,0]\n"
CV "v159,v62,v63\n"
CV "v158,v60,v61\n"
CV "v157,v58,v59\n"
CV "v156,v56,v57\n"
"v_pk_mul_f32 v[48:49],v[48:49],s[54:55]\n"
"v_pk_mul_f32 v[50:51],v[50:51],s[54:55]\n"
"v_pk_mul_f32 v[52:53],v[52:53],s[54:55]\n"
"v_pk_mul_f32 v[54:55],v[54:55],s[54:55]\n"
CV "v155,v54,v55\n"
CV "v154,v52,v53\n"
CV "v153,v50,v51\n"
CV "v152,v48,v49\n"
"v_pk_mul_f32 v[40:41],v[40:41],s[54:55]\n"
"v_pk_mul_f32 v[42:43],v[42:43],s[54:55]\n"
"v_pk_mul_f32 v[44:45],v[44:45],s[54:55]\n"
"v_pk_mul_f32 v[46:47],v[46:47],s[54:55]\n"
CV "v151,v46,v47\n"
CV "v150,v44,v45\n"
CV "v149,v42,v43\n"
CV "v148,v40,v41\n"
"v_pk_mul_f32 v[32:33],v[32:33],s[54:55]\n"
"v_pk_mul_f32 v[34:35],v[34:35],s[54:55]\n"
"v_pk_mul_f32 v[36:37],v[36:37],s[54:55]\n"
"v_pk_mul_f32 v[38:39],v[38:39],s[54:55]\n"
CV "v147,v38,v39\n"
CV "v146,v36,v37\n"
CV "v145,v34,v35\n"
CV "v144,v32,v33\n"
"v_pk_mul_f32 v[24:25],v[24:25],s[54:55]\n"
"v_pk_mul_f32 v[26:27],v[26:27],s[54:55]\n"
"v_pk_mul_f32 v[28:29],v[28:29],s[54:55]\n"
"v_pk_mul_f32 v[30:31],v[30:31],s[54:55]\n"
CV "v143,v30,v31\n"
CV "v142,v28,v29\n"
CV "v141,v26,v27\n"
CV "v140,v24,v25\n"
"v_pk_mul_f32 v[16:17],v[16:17],s[54:55]\n"
"v_pk_mul_f32 v[18:19],v[18:19],s[54:55]\n"
"v_pk_mul_f32 v[20:21],v[20:21],s[54:55]\n"
"v_pk_mul_f32 v[22:23],v[22:23],s[54:55]\n"
CV "v139,v22,v23\n"
CV "v138,v20,v21\n"
CV "v137,v18,v19\n"
CV "v136,v16,v17\n"
"s_cmp_eq_u32 s57,s69\n"
"s_cbranch_scc1 .LattB\n"
AR "v2,a245\n"
"v_add_u32 v2,49152,v2\n"
"v_xor_b32 v3,32,v2\n"
"v_xor_b32 v4,64,v2\n"
"v_xor_b32 v5,96,v2\n"
"s_mov_b32 s61,7\n"
".Latt2x:\n"
"s_cmp_eq_u32 s57,1\n"
"s_cbranch_scc1 .Lt0kx\n"
".Lt0nx:\n"
"s_waitcnt lgkmcnt(2)\n"
MF "v[72:75],v[92:95],v[136:139],0\n"
"s_add_u32 s73,s73,12288\n"
"ds_read_b128 v[104:107],v1 offset:1024\n"
AR "v10,a224\n"
MF "v[76:79],v[92:95],v[140:143],0\n"
"s_cmp_eq_u32 s73,s19\n"
"s_cselect_b32 s73,s18,s73\n"
"s_add_u32 m0,s65,73728\n"
MF "v[80:83],v[92:95],v[144:147],0\n"
LD "v8,s[36:39],s73 offen nt lds\n"
MF "v[84:87],v[92:95],v[148:151],0\n"
"v_max_f32_dpp v10,|v10|,|v10| quad_perm:[1,0,3,2] row_mask:0xf bank_mask:0xf\n"
AR "v11,a225\n"
MF "v[88:91],v[92:95],v[152:155],0\n"
"s_waitcnt lgkmcnt(2)\n"
"ds_read_b128 v[92:95],v0 offset:2048\n"
AR "v12,a226\n"
MF "v[72:75],v[96:99],v[156:159],v[72:75]\n"
"s_add_u32 m0,s65,74624\n"
"v_max_f32_dpp v10,v10,v10 quad_perm:[2,3,0,1] row_mask:0xf bank_mask:0xf\n"
"v_max_f32_dpp v11,|v11|,|v11| quad_perm:[1,0,3,2] row_mask:0xf bank_mask:0xf\n"
MF "v[76:79],v[96:99],v[160:163],v[76:79]\n"
LD "v8,s[36:39],s73 offen offset:128 nt lds\n"
MF "v[80:83],v[96:99],v[164:167],v[80:83]\n"
"v_max_f32_dpp v10,v10,v10 row_half_mirror row_mask:0xf bank_mask:0xf\n"
"v_max_f32_dpp v11,v11,v11 quad_perm:[2,3,0,1] row_mask:0xf bank_mask:0xf\n"
MF "v[84:87],v[96:99],v[168:171],v[84:87]\n"
"v_readlane_b32 s13,v10,0\n"
"v_readlane_b32 s0,v10,24\n"
MF "v[88:91],v[96:99],v[172:175],v[88:91]\n"
"s_waitcnt lgkmcnt(2)\n"
"ds_read_b128 v[96:99],v1 offset:2048\n"
"v_readlane_b32 s1,v10,32\n"
MF "v[72:75],v[100:103],v[176:179],v[72:75]\n"
"s_add_u32 m0,s65,75520\n"
"v_readlane_b32 s2,v10,56\n"
"v_max_f32_dpp v11,v11,v11 row_half_mirror row_mask:0xf bank_mask:0xf\n"
MF "v[76:79],v[100:103],v[180:183],v[76:79]\n"
LD "v8,s[36:39],s73 offen offset:256 nt lds\n"
MF "v[80:83],v[100:103],v[184:187],v[80:83]\n"
"s_min_u32 s13,s13,s0\n"
"v_readlane_b32 s11,v11,0\n"
"v_readlane_b32 s3,v11,24\n"
MF "v[84:87],v[100:103],v[188:191],v[84:87]\n"
"s_min_u32 s1,s1,s2\n"
"v_readlane_b32 s4,v11,32\n"
"v_readlane_b32 s5,v11,56\n"
MF "v[88:91],v[100:103],v[192:195],v[88:91]\n"
"s_waitcnt lgkmcnt(2)\n"
"ds_read_b128 v[100:103],v0 offset:6144\n"
"v_max_f32_dpp v12,|v12|,|v12| quad_perm:[1,0,3,2] row_mask:0xf bank_mask:0xf\n"
MF "v[72:75],v[104:107],v[196:199],v[72:75]\n"
"s_add_u32 m0,s65,73728\n"
MF "v[76:79],v[104:107],v[200:203],v[76:79]\n"
LD "v8,s[36:39],s73 offen offset:3072 nt lds\n"
MF "v[80:83],v[104:107],v[204:207],v[80:83]\n"
"s_min_u32 s13,s13,s1\n"
"v_max_f32_dpp v12,v12,v12 quad_perm:[2,3,0,1] row_mask:0xf bank_mask:0xf\n"
MF "v[84:87],v[104:107],v[208:211],v[84:87]\n"
"s_and_b32 s13,s13,0x7f800000\n"
MF "v[88:91],v[104:107],v[212:215],v[88:91]\n"
"s_waitcnt lgkmcnt(2)\n"
"ds_read_b128 v[104:107],v1 offset:6144\n"
"v_max_f32_dpp v12,v12,v12 row_half_mirror row_mask:0xf bank_mask:0xf\n"
MF "v[72:75],v[92:95],v[216:219],v[72:75]\n"
"s_add_u32 m0,s65,74624\n"
"v_readlane_b32 s10,v12,0\n"
"v_readlane_b32 s6,v12,24\n"
MF "v[76:79],v[92:95],v[220:223],v[76:79]\n"
LD "v8,s[36:39],s73 offen offset:3200 nt lds\n"
MF "v[80:83],v[92:95],v[224:227],v[80:83]\n"
"s_sub_u32 s13,s13,0x14000000\n"
"v_readlane_b32 s7,v12,32\n"
"v_readlane_b32 s8,v12,56\n"
MF "v[84:87],v[92:95],v[228:231],v[84:87]\n"
"s_cmp_lt_u32 s13,0x50000000\n"
"s_cselect_b32 s13,s13,0xed400000\n"
MF "v[88:91],v[92:95],v[232:235],v[88:91]\n"
"s_waitcnt lgkmcnt(2)\n"
"ds_read_b128 v[92:95],v0 offset:7168\n"
MF "v[72:75],v[96:99],v[236:239],v[72:75]\n"
"s_add_u32 m0,s65,75520\n"
MF "v[76:79],v[96:99],v[240:243],v[76:79]\n"
LD "v8,s[36:39],s73 offen offset:3328 nt lds\n"
MF "v[80:83],v[96:99],v[244:247],v[80:83]\n"
"s_add_u32 s13,s13,0x12c00000\n"
MF "v[84:87],v[96:99],v[248:251],v[84:87]\n"
"s_min_u32 s11,s11,s3\n"
MF "v[88:91],v[96:99],v[252:255],v[88:91]\n"
"s_waitcnt lgkmcnt(2)\n"
"ds_read_b128 v[96:99],v1 offset:7168\n"
MF "v[112:115],v[100:103],v[136:139],0\n"
"s_min_u32 s4,s4,s5\n"
MF "v[116:119],v[100:103],v[140:143],0\n"
"s_min_u32 s11,s11,s4\n"
MF "v[120:123],v[100:103],v[144:147],0\n"
"s_and_b32 s11,s11,0x7f800000\n"
MF "v[124:127],v[100:103],v[148:151],0\n"
"s_sub_u32 s11,s11,0x14000000\n"
MF "v[128:131],v[100:103],v[152:155],0\n"
"s_waitcnt lgkmcnt(2)\n"
"ds_read_b128 v[100:103],v0 offset:8192\n"
MF "v[112:115],v[104:107],v[156:159],v[112:115]\n"
"s_min_u32 s10,s10,s6\n"
"v_max3_f32 v52,v72,v73,v74\n"
"v_max_f32 v52,v52,v75\n"
MF "v[116:119],v[104:107],v[160:163],v[116:119]\n"
"s_min_u32 s7,s7,s8\n"
"v_max3_f32 v53,v76,v77,v78\n"
"v_max_f32 v53,v53,v79\n"
MF "v[120:123],v[104:107],v[164:167],v[120:123]\n"
"s_min_u32 s10,s10,s7\n"
"v_max3_f32 v54,v80,v81,v82\n"
"v_max_f32 v54,v54,v83\n"
MF "v[124:127],v[104:107],v[168:171],v[124:127]\n"
"v_max3_f32 v55,v84,v85,v86\n"
"v_max_f32 v55,v55,v87\n"
MF "v[128:131],v[104:107],v[172:175],v[128:131]\n"
".Lpg9b:\n"
"s_cbranch_execz .Lpgpsl_b\n"
"s_waitcnt lgkmcnt(2)\n"
"ds_read_b128 v[104:107],v1 offset:8192\n"
"v_max3_f32 v56,v88,v89,v90\n"
MF "v[112:115],v[92:95],v[176:179],v[112:115]\n"
"s_cmp_lt_u32 s11,0x50000000\n"
"s_cselect_b32 s11,s11,0xed400000\n"
"v_max_f32 v56,v56,v91\n"
MF "v[116:119],v[92:95],v[180:183],v[116:119]\n"
"s_add_u32 s11,s11,0x12c00000\n"
MF "v[120:123],v[92:95],v[184:187],v[120:123]\n"
MF "v[124:127],v[92:95],v[188:191],v[124:127]\n"
MF "v[128:131],v[92:95],v[192:195],v[128:131]\n"
"s_waitcnt lgkmcnt(2)\n"
MF "v[112:115],v[96:99],v[196:199],v[112:115]\n"
"s_waitcnt lgkmcnt(0)\n"
MF "v[116:119],v[96:99],v[200:203],v[116:119]\n"
"s_barrier\n"
MF "v[120:123],v[96:99],v[204:207],v[120:123]\n"
"s_add_u32 s72,s72,6144\n"
MF "v[124:127],v[96:99],v[208:211],v[124:127]\n"
"s_cmp_eq_u32 s72,s17\n"
"s_cselect_b32 s72,s16,s72\n"
"s_add_u32 m0,s64,0\n"
MF "v[128:131],v[96:99],v[212:215],v[128:131]\n"
LD "v7,s[32:35],s72 offen nt lds\n"
MF "v[112:115],v[100:103],v[216:219],v[112:115]\n"
"s_add_u32 m0,s64,896\n"
MF "v[116:119],v[100:103],v[220:223],v[116:119]\n"
LD "v7,s[32:35],s72 offen offset:128 nt lds\n"
MF "v[120:123],v[100:103],v[224:227],v[120:123]\n"
"s_add_u32 m0,s64,1792\n"
MF "v[124:127],v[100:103],v[228:231],v[124:127]\n"
LD "v7,s[32:35],s72 offen offset:256 nt lds\n"
MF "v[128:131],v[100:103],v[232:235],v[128:131]\n"
"s_add_u32 s72,s72,6144\n"
"s_add_u32 m0,s64,6144\n"
MF "v[112:115],v[104:107],v[236:239],v[112:115]\n"
LD "v7,s[32:35],s72 offen nt lds\n"
MF "v[116:119],v[104:107],v[240:243],v[116:119]\n"
"s_add_u32 m0,s64,7040\n"
MF "v[120:123],v[104:107],v[244:247],v[120:123]\n"
LD "v7,s[32:35],s72 offen offset:128 nt lds\n"
MF "v[124:127],v[104:107],v[248:251],v[124:127]\n"
"s_add_u32 m0,s64,7936\n"
MF "v[128:131],v[104:107],v[252:255],v[128:131]\n"
LD "v7,s[32:35],s72 offen offset:256 nt lds\n"
"s_branch .Lt0ex\n"
".Lpgt0kx_d:\n"
"s_cbranch_execz .Lisr2\n"
".Lt0kx:\n"
"s_lshl_b32 s58,s56,10\n"
"s_add_u32 s58,s58,73728\n"
"v_add_u32 v32,s58,v9\n"
"v_add_u32 v33,65536,v32\n"
"s_waitcnt lgkmcnt(2)\n"
MF "v[72:75],v[92:95],v[136:139],0\n"
"s_add_u32 s73,s73,12288\n"
"ds_read_b128 v[104:107],v1 offset:1024\n"
AR "v10,a224\n"
MF "v[76:79],v[92:95],v[140:143],0\n"
"s_cmp_eq_u32 s73,s19\n"
"s_cselect_b32 s73,s18,s73\n"
"s_add_u32 m0,s65,73728\n"
MF "v[80:83],v[92:95],v[144:147],0\n"
LD "v8,s[36:39],s73 offen nt lds\n"
MF "v[84:87],v[92:95],v[148:151],0\n"
"v_max_f32_dpp v10,|v10|,|v10| quad_perm:[1,0,3,2] row_mask:0xf bank_mask:0xf\n"
AR "v11,a225\n"
MF "v[88:91],v[92:95],v[152:155],0\n"
"s_waitcnt lgkmcnt(2)\n"
"ds_read_b128 v[92:95],v0 offset:2048\n"
AR "v12,a226\n"
MF "v[72:75],v[96:99],v[156:159],v[72:75]\n"
"s_add_u32 m0,s65,74624\n"
"v_max_f32_dpp v10,v10,v10 quad_perm:[2,3,0,1] row_mask:0xf bank_mask:0xf\n"
"v_max_f32_dpp v11,|v11|,|v11| quad_perm:[1,0,3,2] row_mask:0xf bank_mask:0xf\n"
MF "v[76:79],v[96:99],v[160:163],v[76:79]\n"
LD "v8,s[36:39],s73 offen offset:128 nt lds\n"
MF "v[80:83],v[96:99],v[164:167],v[80:83]\n"
"v_max_f32_dpp v10,v10,v10 row_half_mirror row_mask:0xf bank_mask:0xf\n"
"v_max_f32_dpp v11,v11,v11 quad_perm:[2,3,0,1] row_mask:0xf bank_mask:0xf\n"
MF "v[84:87],v[96:99],v[168:171],v[84:87]\n"
"v_readlane_b32 s13,v10,0\n"
"v_readlane_b32 s0,v10,24\n"
MF "v[88:91],v[96:99],v[172:175],v[88:91]\n"
"s_waitcnt lgkmcnt(2)\n"
"ds_read_b128 v[96:99],v1 offset:2048\n"
"v_readlane_b32 s1,v10,32\n"
MF "v[72:75],v[100:103],v[176:179],v[72:75]\n"
"s_add_u32 m0,s65,75520\n"
"v_readlane_b32 s2,v10,56\n"
"v_max_f32_dpp v11,v11,v11 row_half_mirror row_mask:0xf bank_mask:0xf\n"
MF "v[76:79],v[100:103],v[180:183],v[76:79]\n"
LD "v8,s[36:39],s73 offen offset:256 nt lds\n"
MF "v[80:83],v[100:103],v[184:187],v[80:83]\n"
"s_min_u32 s13,s13,s0\n"
"v_readlane_b32 s11,v11,0\n"
"v_readlane_b32 s3,v11,24\n"
MF "v[84:87],v[100:103],v[188:191],v[84:87]\n"
"s_min_u32 s1,s1,s2\n"
"v_readlane_b32 s4,v11,32\n"
"v_readlane_b32 s5,v11,56\n"
MF "v[88:91],v[100:103],v[192:195],v[88:91]\n"
"s_waitcnt lgkmcnt(2)\n"
"ds_read_b128 v[100:103],v0 offset:6144\n"
"v_max_f32_dpp v12,|v12|,|v12| quad_perm:[1,0,3,2] row_mask:0xf bank_mask:0xf\n"
MF "v[72:75],v[104:107],v[196:199],v[72:75]\n"
"s_add_u32 m0,s65,73728\n"
"ds_write_b128 v32,a[0:3] offset:24576\n"
MF "v[76:79],v[104:107],v[200:203],v[76:79]\n"
LD "v8,s[36:39],s73 offen offset:3072 nt lds\n"
MF "v[80:83],v[104:107],v[204:207],v[80:83]\n"
"s_min_u32 s13,s13,s1\n"
"v_max_f32_dpp v12,v12,v12 quad_perm:[2,3,0,1] row_mask:0xf bank_mask:0xf\n"
AR "v62,a20\n"
MF "v[84:87],v[104:107],v[208:211],v[84:87]\n"
"s_and_b32 s13,s13,0x7f800000\n"
"ds_write_b128 v32,a[4:7] offset:28672\n"
MF "v[88:91],v[104:107],v[212:215],v[88:91]\n"
"s_waitcnt lgkmcnt(4)\n"
"ds_read_b128 v[104:107],v1 offset:6144\n"
"v_max_f32_dpp v12,v12,v12 row_half_mirror row_mask:0xf bank_mask:0xf\n"
MF "v[72:75],v[92:95],v[216:219],v[72:75]\n"
"s_add_u32 m0,s65,74624\n"
"v_readlane_b32 s10,v12,0\n"
"v_readlane_b32 s6,v12,24\n"
MF "v[76:79],v[92:95],v[220:223],v[76:79]\n"
LD "v8,s[36:39],s73 offen offset:3200 nt lds\n"
MF "v[80:83],v[92:95],v[224:227],v[80:83]\n"
"s_sub_u32 s13,s13,0x14000000\n"
"v_readlane_b32 s7,v12,32\n"
"v_readlane_b32 s8,v12,56\n"
MF "v[84:87],v[92:95],v[228:231],v[84:87]\n"
"s_cmp_lt_u32 s13,0x50000000\n"
"s_cselect_b32 s13,s13,0xed400000\n"
AR "v63,a21\n"
MF "v[88:91],v[92:95],v[232:235],v[88:91]\n"
"s_waitcnt lgkmcnt(4)\n"
"ds_read_b128 v[92:95],v0 offset:7168\n"
AR "v64,a22\n"
MF "v[72:75],v[96:99],v[236:239],v[72:75]\n"
"s_add_u32 m0,s65,75520\n"
"ds_write_b128 v32,a[8:11] offset:32768\n"
MF "v[76:79],v[96:99],v[240:243],v[76:79]\n"
LD "v8,s[36:39],s73 offen offset:3328 nt lds\n"
MF "v[80:83],v[96:99],v[244:247],v[80:83]\n"
"s_add_u32 s13,s13,0x12c00000\n"
"ds_write_b128 v32,a[12:15] offset:36864\n"
MF "v[84:87],v[96:99],v[248:251],v[84:87]\n"
"s_min_u32 s11,s11,s3\n"
"ds_write_b128 v32,a[16:19] offset:40960\n"
MF "v[88:91],v[96:99],v[252:255],v[88:91]\n"
"s_waitcnt lgkmcnt(7)\n"
"ds_read_b128 v[96:99],v1 offset:7168\n"
AR "v65,a23\n"
MF "v[112:115],v[100:103],v[136:139],0\n"
"s_min_u32 s4,s4,s5\n"
"ds_write_b128 v32,a[40:43] offset:45056\n"
MF "v[116:119],v[100:103],v[140:143],0\n"
"s_min_u32 s11,s11,s4\n"
"ds_write_b128 v32,a[44:47] offset:49152\n"
MF "v[120:123],v[100:103],v[144:147],0\n"
"s_and_b32 s11,s11,0x7f800000\n"
"ds_write_b128 v32,a[48:51] offset:53248\n"
MF "v[124:127],v[100:103],v[148:151],0\n"
"s_sub_u32 s11,s11,0x14000000\n"
"ds_write_b128 v32,a[52:55] offset:57344\n"
MF "v[128:131],v[100:103],v[152:155],0\n"
"s_waitcnt lgkmcnt(9)\n"
"ds_read_b128 v[100:103],v0 offset:8192\n"
AR "v66,a24\n"
MF "v[112:115],v[104:107],v[156:159],v[112:115]\n"
"s_min_u32 s10,s10,s6\n"
"v_max3_f32 v52,v72,v73,v74\n"
"v_max_f32 v52,v52,v75\n"
MF "v[116:119],v[104:107],v[160:163],v[116:119]\n"
"s_min_u32 s7,s7,s8\n"
"v_max3_f32 v53,v76,v77,v78\n"
"v_max_f32 v53,v53,v79\n"
MF "v[120:123],v[104:107],v[164:167],v[120:123]\n"
"s_min_u32 s10,s10,s7\n"
"v_max3_f32 v54,v80,v81,v82\n"
"v_max_f32 v54,v54,v83\n"
MF "v[124:127],v[104:107],v[168:171],v[124:127]\n"
"v_max3_f32 v55,v84,v85,v86\n"
"v_max_f32 v55,v55,v87\n"
MF "v[128:131],v[104:107],v[172:175],v[128:131]\n"
"s_waitcnt lgkmcnt(9)\n"
"ds_read_b128 v[104:107],v1 offset:8192\n"
"v_max3_f32 v56,v88,v89,v90\n"
MF "v[112:115],v[92:95],v[176:179],v[112:115]\n"
"s_cmp_lt_u32 s11,0x50000000\n"
"s_cselect_b32 s11,s11,0xed400000\n"
"v_max_f32 v56,v56,v91\n"
MF "v[116:119],v[92:95],v[180:183],v[116:119]\n"
"s_add_u32 s11,s11,0x12c00000\n"
"ds_write_b128 v32,a[56:59] offset:61440\n"
MF "v[120:123],v[92:95],v[184:187],v[120:123]\n"
"ds_write_b128 v33,a[80:83]\n"
MF "v[124:127],v[92:95],v[188:191],v[124:127]\n"
"ds_write_b128 v33,a[84:87] offset:4096\n"
MF "v[128:131],v[92:95],v[192:195],v[128:131]\n"
"s_waitcnt lgkmcnt(9)\n"
"ds_write_b128 v33,a[88:91] offset:8192\n"
MF "v[112:115],v[96:99],v[196:199],v[112:115]\n"
"s_waitcnt lgkmcnt(0)\n"
AR "v67,a25\n"
AR "v68,a26\n"
MF "v[116:119],v[96:99],v[200:203],v[116:119]\n"
"s_barrier\n"
MF "v[120:123],v[96:99],v[204:207],v[120:123]\n"
"s_add_u32 s72,s72,6144\n"
"ds_write_b128 v33,a[92:95] offset:12288\n"
MF "v[124:127],v[96:99],v[208:211],v[124:127]\n"
"s_cmp_eq_u32 s72,s17\n"
"s_cselect_b32 s72,s16,s72\n"
"s_add_u32 m0,s64,0\n"
MF "v[128:131],v[96:99],v[212:215],v[128:131]\n"
LD "v7,s[32:35],s72 offen nt lds\n"
MF "v[112:115],v[100:103],v[216:219],v[112:115]\n"
"s_add_u32 m0,s64,896\n"
"ds_write_b128 v33,a[96:99] offset:16384\n"
MF "v[116:119],v[100:103],v[220:223],v[116:119]\n"
LD "v7,s[32:35],s72 offen offset:128 nt lds\n"
MF "v[120:123],v[100:103],v[224:227],v[120:123]\n"
"s_add_u32 m0,s64,1792\n"
AR "v69,a27\n"
AR "v70,a28\n"
MF "v[124:127],v[100:103],v[228:231],v[124:127]\n"
LD "v7,s[32:35],s72 offen offset:256 nt lds\n"
MF "v[128:131],v[100:103],v[232:235],v[128:131]\n"
"s_add_u32 s72,s72,6144\n"
"s_add_u32 m0,s64,6144\n"
AR "v71,a29\n"
MF "v[112:115],v[104:107],v[236:239],v[112:115]\n"
LD "v7,s[32:35],s72 offen nt lds\n"
MF "v[116:119],v[104:107],v[240:243],v[116:119]\n"
"s_add_u32 m0,s64,7040\n"
"ds_write_b128 v33,a[120:123] offset:20480\n"
MF "v[120:123],v[104:107],v[244:247],v[120:123]\n"
LD "v7,s[32:35],s72 offen offset:128 nt lds\n"
MF "v[124:127],v[104:107],v[248:251],v[124:127]\n"
"s_add_u32 m0,s64,7936\n"
AR "v132,a30\n"
AR "v133,a31\n"
MF "v[128:131],v[104:107],v[252:255],v[128:131]\n"
LD "v7,s[32:35],s72 offen offset:256 nt lds\n"
AR "v134,a32\n"
AR "v135,a33\n"
AR "v6,a34\n"
AR "v13,a35\n"
AR "v14,a36\n"
AR "v15,a37\n"
"v_accvgpr_mov_b32 a254,a38\n"
"v_accvgpr_mov_b32 a255,a39\n"
"v_accvgpr_mov_b32 a240,a168\n"
"v_accvgpr_mov_b32 a241,a169\n"
"v_accvgpr_mov_b32 a242,a170\n"
"v_accvgpr_mov_b32 a243,a171\n"
"v_accvgpr_mov_b32 a244,a172\n"
"v_accvgpr_mov_b32 a245,a173\n"
"v_accvgpr_mov_b32 a246,a174\n"
"v_accvgpr_mov_b32 a247,a175\n"
"v_accvgpr_mov_b32 a248,a176\n"
"v_accvgpr_mov_b32 a249,a177\n"
"v_accvgpr_mov_b32 a250,a178\n"
"v_accvgpr_mov_b32 a251,a179\n"
".Lpgt0ex_b:\n"
"s_cbranch_execz .Lpg2a\n"
".Lt0ex:\n"
"s_nop 1\n"
"v_max3_f32 v52,v52,v112,v113\n"
"v_max3_f32 v53,v53,v116,v117\n"
"v_max3_f32 v54,v54,v120,v121\n"
"v_max3_f32 v55,v55,v124,v125\n"
"v_max3_f32 v56,v56,v128,v129\n"
"v_max3_f32 v52,v52,v114,v115\n"
"v_max3_f32 v53,v53,v118,v119\n"
"v_max3_f32 v54,v54,v122,v123\n"
"v_max3_f32 v55,v55,v126,v127\n"
"v_max3_f32 v56,v56,v130,v131\n"
"v_mov_b32 v57,v52\n"
"v_mov_b32 v58,v53\n"
"v_mov_b32 v59,v54\n"
"v_mov_b32 v60,v55\n"
"v_mov_b32 v61,v56\n"
"v_permlane16_swap_b32 v52,v57\n"
"v_permlane16_swap_b32 v53,v58\n"
"v_permlane16_swap_b32 v54,v59\n"
"v_permlane16_swap_b32 v55,v60\n"
"v_permlane16_swap_b32 v56,v61\n"
"v_max_f32 v52,v52,v57\n"
"v_max_f32 v53,v53,v58\n"
"v_max_f32 v54,v54,v59\n"
"v_max_f32 v55,v55,v60\n"
"v_max_f32 v56,v56,v61\n"
"v_mov_b32 v57,v52\n"
"v_mov_b32 v58,v53\n"
"v_mov_b32 v59,v54\n"
"v_mov_b32 v60,v55\n"
"v_mov_b32 v61,v56\n"
"v_permlane32_swap_b32 v52,v57\n"
"v_permlane32_swap_b32 v53,v58\n"
"v_permlane32_swap_b32 v54,v59\n"
"v_permlane32_swap_b32 v55,v60\n"
"v_permlane32_swap_b32 v56,v61\n"
"v_max_f32 v52,v52,v57\n"
"v_max_f32 v53,v53,v58\n"
"v_max_f32 v54,v54,v59\n"
"v_max_f32 v55,v55,v60\n"
"v_max_f32 v56,v56,v61\n"
"v_sub_f32 v32,0xc1300000,v52\n"
"v_sub_f32 v36,0xc1300000,v53\n"
"v_sub_f32 v40,0xc1300000,v54\n"
"v_sub_f32 v44,0xc1300000,v55\n"
"v_sub_f32 v48,0xc1300000,v56\n"
"v_mov_b32 v33,v32\n"
"v_pk_mov_b32 v[34:35],v[32:33],v[32:33]\n"
"v_mov_b32 v37,v36\n"
"v_pk_mov_b32 v[38:39],v[36:37],v[36:37]\n"
"v_mov_b32 v41,v40\n"
"v_pk_mov_b32 v[42:43],v[40:41],v[40:41]\n"
"v_mov_b32 v45,v44\n"
"v_pk_mov_b32 v[46:47],v[44:45],v[44:45]\n"
"v_mov_b32_e64 v49,v48\n"
"v_pk_mov_b32 v[50:51],v[48:49],v[48:49]\n"
"v_pk_add_f32 v[72:73],v[72:73],v[32:33]\n"
"v_pk_add_f32 v[74:75],v[74:75],v[32:33]\n"
"v_pk_add_f32 v[112:113],v[112:113],v[32:33]\n"
"v_pk_add_f32 v[114:115],v[114:115],v[32:33]\n"
"v_exp_f32 v72,v72\n"
"v_exp_f32 v73,v73\n"
"v_exp_f32 v74,v74\n"
"v_exp_f32 v75,v75\n"
"v_exp_f32 v112,v112\n"
"v_exp_f32 v113,v113\n"
"v_exp_f32 v114,v114\n"
"v_exp_f32 v115,v115\n"
"v_pk_add_f32 v[10:11],v[72:73],v[74:75]\n"
"v_pk_add_f32 v[28:29],v[112:113],v[114:115]\n"
"v_pk_add_f32 v[10:11],v[10:11],v[28:29]\n"
"v_add_f32_e64 v52,v10,v11\n"
CV "v72,v72,v73\n"
CV "v73,v74,v75\n"
CV "v74,v112,v113\n"
CV "v75,v114,v115\n"
"v_pk_add_f32 v[76:77],v[76:77],v[36:37]\n"
"v_pk_add_f32 v[78:79],v[78:79],v[36:37]\n"
"v_pk_add_f32 v[116:117],v[116:117],v[36:37]\n"
"v_pk_add_f32 v[118:119],v[118:119],v[36:37]\n"
"v_exp_f32 v76,v76\n"
"v_exp_f32 v77,v77\n"
"v_exp_f32 v78,v78\n"
"v_exp_f32 v79,v79\n"
"v_exp_f32 v116,v116\n"
"v_exp_f32 v117,v117\n"
"v_exp_f32 v118,v118\n"
"v_exp_f32 v119,v119\n"
"v_pk_add_f32 v[10:11],v[76:77],v[78:79]\n"
"v_pk_add_f32 v[28:29],v[116:117],v[118:119]\n"
"v_pk_add_f32 v[10:11],v[10:11],v[28:29]\n"
"v_add_f32_e64 v53,v10,v11\n"
CV "v76,v76,v77\n"
CV "v77,v78,v79\n"
CV "v78,v116,v117\n"
CV "v79,v118,v119\n"
"v_pk_add_f32 v[80:81],v[80:81],v[40:41]\n"
"v_pk_add_f32 v[82:83],v[82:83],v[40:41]\n"
"v_pk_add_f32 v[120:121],v[120:121],v[40:41]\n"
"v_pk_add_f32 v[122:123],v[122:123],v[40:41]\n"
"v_exp_f32 v80,v80\n"
"v_exp_f32 v81,v81\n"
"v_exp_f32 v82,v82\n"
"v_exp_f32 v83,v83\n"
"v_exp_f32 v120,v120\n"
"v_exp_f32 v121,v121\n"
"v_exp_f32 v122,v122\n"
"v_exp_f32 v123,v123\n"
"v_pk_add_f32 v[10:11],v[80:81],v[82:83]\n"
"v_pk_add_f32 v[28:29],v[120:121],v[122:123]\n"
"v_pk_add_f32 v[10:11],v[10:11],v[28:29]\n"
"v_add_f32_e64 v54,v10,v11\n"
CV "v80,v80,v81\n"
CV "v81,v82,v83\n"
CV "v82,v120,v121\n"
CV "v83,v122,v123\n"
"v_pk_add_f32 v[84:85],v[84:85],v[44:45]\n"
"v_pk_add_f32 v[86:87],v[86:87],v[44:45]\n"
"v_pk_add_f32 v[124:125],v[124:125],v[44:45]\n"
"v_pk_add_f32 v[126:127],v[126:127],v[44:45]\n"
"v_mov_b32_e64 v55,0\n"
"v_pk_add_f32 v[88:89],v[88:89],v[48:49]\n"
"v_pk_add_f32 v[90:91],v[90:91],v[48:49]\n"
"v_pk_add_f32 v[128:129],v[128:129],v[48:49]\n"
"v_pk_add_f32 v[130:131],v[130:131],v[48:49]\n"
"v_mov_b32 v56,0\n"
"s_waitcnt vmcnt(18) lgkmcnt(0)\n"
"s_barrier\n"
"ds_read_b128 v[16:19],v0 offset:24576\n"
"ds_read_b128 v[20:23],v1 offset:24576\n"
AW "a220,s13\n"
AW "a221,s13\n"
AW "a222,s13\n"
AW "a223,s13\n"
"s_waitcnt lgkmcnt(1)\n"
MF "v[92:95],v[16:19],v[136:139],v[32:35]\n"
"s_nop 0\n"
"ds_read_b128 v[24:27],v0 offset:25600\n"
"ds_read_b128 v[28:31],v1 offset:25600\n"
"s_waitcnt lgkmcnt(2)\n"
MF "v[92:95],v[20:23],v[156:159],v[92:95]\n"
"v_exp_f32 v84,v84\n"
MF "v[96:99],v[20:23],v[160:163],v[36:39]\n"
"v_exp_f32 v85,v85\n"
MF "v[96:99],v[16:19],v[140:143],v[96:99]\n"
"v_exp_f32 v86,v86\n"
MF "v[100:103],v[16:19],v[144:147],v[40:43]\n"
"v_exp_f32 v87,v87\n"
MF "v[100:103],v[20:23],v[164:167],v[100:103]\n"
"v_add_f32 v55,v55,v84\n"
"v_add_f32 v55,v55,v85\n"
MF "v[104:107],v[20:23],v[168:171],v[44:47]\n"
CV "v84,v84,v85\n"
"v_add_f32 v55,v55,v86\n"
MF "v[104:107],v[16:19],v[148:151],v[104:107]\n"
"v_add_f32 v55,v55,v87\n"
CV "v85,v86,v87\n"
MF "v[108:111],v[16:19],v[152:155],v[48:51]\n"
"v_exp_f32 v124,v124\n"
MF "v[108:111],v[20:23],v[172:175],v[108:111]\n"
"s_waitcnt lgkmcnt(0)\n"
"ds_read_b128 v[16:19],v0 offset:26624\n"
"ds_read_b128 v[20:23],v1 offset:26624\n"
MF "v[108:111],v[24:27],v[192:195],v[108:111]\n"
"v_exp_f32 v125,v125\n"
MF "v[108:111],v[28:31],v[212:215],v[108:111]\n"
"v_exp_f32 v126,v126\n"
MF "v[104:107],v[28:31],v[208:211],v[104:107]\n"
"v_exp_f32 v127,v127\n"
MF "v[104:107],v[24:27],v[188:191],v[104:107]\n"
"v_add_f32 v55,v55,v124\n"
"v_add_f32 v55,v55,v125\n"
MF "v[100:103],v[24:27],v[184:187],v[100:103]\n"
CV "v86,v124,v125\n"
"v_add_f32 v55,v55,v126\n"
MF "v[100:103],v[28:31],v[204:207],v[100:103]\n"
"v_add_f32 v55,v55,v127\n"
CV "v87,v126,v127\n"
MF "v[96:99],v[28:31],v[200:203],v[96:99]\n"
"v_exp_f32 v88,v88\n"
MF "v[96:99],v[24:27],v[180:183],v[96:99]\n"
"v_exp_f32 v89,v89\n"
MF "v[92:95],v[24:27],v[176:179],v[92:95]\n"
"v_exp_f32 v90,v90\n"
MF "v[92:95],v[28:31],v[196:199],v[92:95]\n"
"s_waitcnt lgkmcnt(0)\n"
"ds_read_b128 v[24:27],v0 offset:30720\n"
"ds_read_b128 v[28:31],v1 offset:30720\n"
MF "v[92:95],v[16:19],v[216:219],v[92:95]\n"
"v_exp_f32 v91,v91\n"
MF "v[92:95],v[20:23],v[236:239],v[92:95]\n"
"v_add_f32 v56,v56,v88\n"
"v_add_f32 v56,v56,v89\n"
MF "v[96:99],v[20:23],v[240:243],v[96:99]\n"
CV "v88,v88,v89\n"
"v_add_f32 v56,v56,v90\n"
MF "v[96:99],v[16:19],v[220:223],v[96:99]\n"
"v_add_f32 v56,v56,v91\n"
CV "v89,v90,v91\n"
MF "v[100:103],v[16:19],v[224:227],v[100:103]\n"
"v_exp_f32 v128,v128\n"
MF "v[100:103],v[20:23],v[244:247],v[100:103]\n"
"v_exp_f32 v129,v129\n"
MF "v[104:107],v[20:23],v[248:251],v[104:107]\n"
"v_exp_f32 v130,v130\n"
MF "v[104:107],v[16:19],v[228:231],v[104:107]\n"
"v_exp_f32 v131,v131\n"
MF "v[108:111],v[16:19],v[232:235],v[108:111]\n"
"v_add_f32 v56,v56,v128\n"
"v_add_f32 v56,v56,v129\n"
MF "v[108:111],v[20:23],v[252:255],v[108:111]\n"
"s_waitcnt lgkmcnt(0)\n"
"ds_read_b128 v[16:19],v0 offset:31744\n"
"ds_read_b128 v[20:23],v1 offset:31744\n"
MF "v[112:115],v[24:27],v[136:139],v[32:35]\n"
CV "v90,v128,v129\n"
"v_add_f32 v56,v56,v130\n"
MF "v[112:115],v[28:31],v[156:159],v[112:115]\n"
"v_add_f32 v56,v56,v131\n"
CV "v91,v130,v131\n"
MF "v[116:119],v[28:31],v[160:163],v[36:39]\n"
"v_exp_f32 v92,v92\n"
MF "v[116:119],v[24:27],v[140:143],v[116:119]\n"
"v_exp_f32 v93,v93\n"
MF "v[120:123],v[24:27],v[144:147],v[40:43]\n"
"v_exp_f32 v94,v94\n"
MF "v[120:123],v[28:31],v[164:167],v[120:123]\n"
"v_exp_f32 v95,v95\n"
MF "v[124:127],v[28:31],v[168:171],v[44:47]\n"
"v_add_f32 v57,v52,v92\n"
"v_add_f32 v57,v57,v93\n"
MF "v[124:127],v[24:27],v[148:151],v[124:127]\n"
CV "v92,v92,v93\n"
"v_add_f32 v57,v57,v94\n"
MF "v[128:131],v[24:27],v[152:155],v[48:51]\n"
"v_add_f32 v57,v57,v95\n"
CV "v93,v94,v95\n"
MF "v[128:131],v[28:31],v[172:175],v[128:131]\n"
"s_waitcnt lgkmcnt(0)\n"
"ds_read_b128 v[24:27],v0 offset:32768\n"
"ds_read_b128 v[28:31],v1 offset:32768\n"
MF "v[128:131],v[16:19],v[192:195],v[128:131]\n"
"v_exp_f32 v96,v96\n"
MF "v[128:131],v[20:23],v[212:215],v[128:131]\n"
"v_exp_f32 v97,v97\n"
MF "v[124:127],v[20:23],v[208:211],v[124:127]\n"
"v_exp_f32 v98,v98\n"
MF "v[124:127],v[16:19],v[188:191],v[124:127]\n"
"v_exp_f32 v99,v99\n"
MF "v[120:123],v[16:19],v[184:187],v[120:123]\n"
"v_add_f32 v58,v53,v96\n"
"v_add_f32 v58,v58,v97\n"
MF "v[120:123],v[20:23],v[204:207],v[120:123]\n"
CV "v96,v96,v97\n"
"v_add_f32 v58,v58,v98\n"
MF "v[116:119],v[20:23],v[200:203],v[116:119]\n"
"s_waitcnt vmcnt(12) lgkmcnt(0)\n"
"v_add_f32 v58,v58,v99\n"
CV "v97,v98,v99\n"
MF "v[116:119],v[16:19],v[180:183],v[116:119]\n"
"s_barrier\n"
MF "v[112:115],v[16:19],v[176:179],v[112:115]\n"
"v_exp_f32 v100,v100\n"
MF "v[112:115],v[20:23],v[196:199],v[112:115]\n"
"s_nop 0\n"
TR "v[16:17],v2\n"
TR "v[18:19],v2 offset:6144\n"
MF "v[112:115],v[24:27],v[216:219],v[112:115]\n"
"s_add_u32 s72,s72,6144\n"
TR "v[20:21],v3\n"
TR "v[22:23],v3 offset:6144\n"
MF "v[112:115],v[28:31],v[236:239],v[112:115]\n"
"s_cmp_eq_u32 s72,s17\n"
"s_cselect_b32 s72,s16,s72\n"
"s_add_u32 m0,s64,24576\n"
MF "v[116:119],v[28:31],v[240:243],v[116:119]\n"
LD "v7,s[32:35],s72 offen nt lds\n"
MF "v[116:119],v[24:27],v[220:223],v[116:119]\n"
"s_add_u32 m0,s64,25472\n"
"v_exp_f32 v101,v101\n"
MF "v[120:123],v[24:27],v[224:227],v[120:123]\n"
LD "v7,s[32:35],s72 offen offset:128 nt lds\n"
MF "v[120:123],v[28:31],v[244:247],v[120:123]\n"
"v_exp_f32 v102,v102\n"
MF "v[124:127],v[28:31],v[248:251],v[124:127]\n"
"s_add_u32 m0,s64,26368\n"
"v_exp_f32 v103,v103\n"
MF "v[124:127],v[24:27],v[228:231],v[124:127]\n"
LD "v7,s[32:35],s72 offen offset:256 nt lds\n"
MF "v[128:131],v[24:27],v[232:235],v[128:131]\n"
"s_add_u32 s72,s72,6144\n"
"v_add_f32 v59,v54,v100\n"
"v_add_f32 v59,v59,v101\n"
MF "v[128:131],v[28:31],v[252:255],v[128:131]\n"
"s_waitcnt lgkmcnt(0)\n"
TR "v[24:25],v4\n"
TR "v[26:27],v4 offset:6144\n"
MF "a[0:3],v[16:19],v[72:75],a[220:223]\n"
"s_add_u32 m0,s64,30720\n"
TR "v[28:29],v5\n"
TR "v[30:31],v5 offset:6144\n"
MF "a[20:23],v[20:23],v[72:75],a[220:223]\n"
LD "v7,s[32:35],s72 offen nt lds\n"
MF "a[24:27],v[20:23],v[76:79],a[220:223]\n"
CV "v100,v100,v101\n"
"v_add_f32 v59,v59,v102\n"
MF "a[4:7],v[16:19],v[76:79],a[220:223]\n"
"s_add_u32 m0,s64,31616\n"
"v_add_f32 v59,v59,v103\n"
CV "v101,v102,v103\n"
MF "a[8:11],v[16:19],v[80:83],a[220:223]\n"
LD "v7,s[32:35],s72 offen offset:128 nt lds\n"
MF "a[28:31],v[20:23],v[80:83],a[220:223]\n"
"v_exp_f32 v104,v104\n"
MF "a[32:35],v[20:23],v[84:87],a[220:223]\n"
"s_add_u32 m0,s64,32512\n"
"v_exp_f32 v105,v105\n"
MF "a[12:15],v[16:19],v[84:87],a[220:223]\n"
LD "v7,s[32:35],s72 offen offset:256 nt lds\n"
MF "a[16:19],v[16:19],v[88:91],a[220:223]\n"
"v_exp_f32 v106,v106\n"
MF "a[36:39],v[20:23],v[88:91],a[220:223]\n"
"s_waitcnt lgkmcnt(0)\n"
TR "v[16:17],v2 offset:1024\n"
TR "v[18:19],v2 offset:7168\n"
MF "a[56:59],v[24:27],v[88:91],a[220:223]\n"
"s_nop 0\n"
TR "v[20:21],v3 offset:1024\n"
TR "v[22:23],v3 offset:7168\n"
MF "a[76:79],v[28:31],v[88:91],a[220:223]\n"
"v_exp_f32 v107,v107\n"
MF "a[72:75],v[28:31],v[84:87],a[220:223]\n"
"v_add_f32 v60,v55,v104\n"
"v_add_f32 v60,v60,v105\n"
MF "a[52:55],v[24:27],v[84:87],a[220:223]\n"
CV "v104,v104,v105\n"
"v_add_f32 v60,v60,v106\n"
MF "a[48:51],v[24:27],v[80:83],a[220:223]\n"
"v_add_f32 v60,v60,v107\n"
CV "v105,v106,v107\n"
MF "a[68:71],v[28:31],v[80:83],a[220:223]\n"
"v_exp_f32 v108,v108\n"
MF "a[64:67],v[28:31],v[76:79],a[220:223]\n"
"v_exp_f32 v109,v109\n"
MF "a[44:47],v[24:27],v[76:79],a[220:223]\n"
".Lpg10b:\n"
"s_cbranch_execz .Lpg1a\n"
".Lpg10d:\n"
"s_cbranch_execz .Lisr2\n"
"v_exp_f32 v110,v110\n"
MF "a[40:43],v[24:27],v[72:75],a[220:223]\n"
"v_exp_f32 v111,v111\n"
MF "a[60:63],v[28:31],v[72:75],a[220:223]\n"
"s_waitcnt lgkmcnt(0)\n"
TR "v[24:25],v4 offset:1024\n"
TR "v[26:27],v4 offset:7168\n"
MF "a[80:83],v[16:19],v[72:75],a[220:223]\n"
"s_nop 0\n"
TR "v[28:29],v5 offset:1024\n"
TR "v[30:31],v5 offset:7168\n"
MF "a[100:103],v[20:23],v[72:75],a[220:223]\n"
"v_add_f32 v61,v56,v108\n"
"v_add_f32 v61,v61,v109\n"
MF "a[104:107],v[20:23],v[76:79],a[220:223]\n"
CV "v108,v108,v109\n"
"v_add_f32 v61,v61,v110\n"
MF "a[84:87],v[16:19],v[76:79],a[220:223]\n"
"v_add_f32 v61,v61,v111\n"
CV "v109,v110,v111\n"
MF "a[88:91],v[16:19],v[80:83],a[220:223]\n"
"v_exp_f32 v112,v112\n"
MF "a[108:111],v[20:23],v[80:83],a[220:223]\n"
"v_exp_f32 v113,v113\n"
MF "a[112:115],v[20:23],v[84:87],a[220:223]\n"
"v_exp_f32 v114,v114\n"
MF "a[92:95],v[16:19],v[84:87],a[220:223]\n"
"v_exp_f32 v115,v115\n"
MF "a[96:99],v[16:19],v[88:91],a[220:223]\n"
"v_add_f32 v57,v57,v112\n"
"v_add_f32 v57,v57,v113\n"
MF "a[116:119],v[20:23],v[88:91],a[220:223]\n"
"s_waitcnt lgkmcnt(0)\n"
TR "v[16:17],v2 offset:2048\n"
TR "v[18:19],v2 offset:8192\n"
MF "a[136:139],v[24:27],v[88:91],a[220:223]\n"
"s_nop 0\n"
TR "v[20:21],v3 offset:2048\n"
TR "v[22:23],v3 offset:8192\n"
MF "a[156:159],v[28:31],v[88:91],a[220:223]\n"
CV "v94,v112,v113\n"
"v_add_f32 v57,v57,v114\n"
MF "a[152:155],v[28:31],v[84:87],a[220:223]\n"
"v_add_f32 v57,v57,v115\n"
CV "v95,v114,v115\n"
MF "a[132:135],v[24:27],v[84:87],a[220:223]\n"
"v_exp_f32 v116,v116\n"
MF "a[128:131],v[24:27],v[80:83],a[220:223]\n"
"v_exp_f32 v117,v117\n"
MF "a[148:151],v[28:31],v[80:83],a[220:223]\n"
"v_exp_f32 v118,v118\n"
MF "a[144:147],v[28:31],v[76:79],a[220:223]\n"
"v_exp_f32 v119,v119\n"
MF "a[124:127],v[24:27],v[76:79],a[220:223]\n"
"v_add_f32 v58,v58,v116\n"
"v_add_f32 v58,v58,v117\n"
MF "a[120:123],v[24:27],v[72:75],a[220:223]\n"
CV "v98,v116,v117\n"
"v_add_f32 v58,v58,v118\n"
MF "a[140:143],v[28:31],v[72:75],a[220:223]\n"
"s_waitcnt lgkmcnt(0)\n"
TR "v[24:25],v4 offset:2048\n"
TR "v[26:27],v4 offset:8192\n"
MF "a[160:163],v[16:19],v[72:75],a[220:223]\n"
"s_nop 0\n"
TR "v[28:29],v5 offset:2048\n"
TR "v[30:31],v5 offset:8192\n"
MF "a[180:183],v[20:23],v[72:75],a[220:223]\n"
"v_add_f32 v58,v58,v119\n"
CV "v99,v118,v119\n"
MF "a[184:187],v[20:23],v[76:79],a[220:223]\n"
"v_exp_f32 v120,v120\n"
MF "a[164:167],v[16:19],v[76:79],a[220:223]\n"
"v_exp_f32 v121,v121\n"
MF "a[168:171],v[16:19],v[80:83],a[220:223]\n"
"v_exp_f32 v122,v122\n"
MF "a[188:191],v[20:23],v[80:83],a[220:223]\n"
"v_exp_f32 v123,v123\n"
MF "a[192:195],v[20:23],v[84:87],a[220:223]\n"
"v_add_f32 v59,v59,v120\n"
"v_add_f32 v59,v59,v121\n"
"s_cmp_eq_u32 s57,0\n"
"s_cbranch_scc1 .Lisx6\n"
"s_mov_b64 exec,0\n"
"s_cmp_eq_u32 s56,0\n"
"s_cbranch_scc1 .Lpgt0e_g\n"
"s_cmp_eq_u32 s56,1\n"
"s_cbranch_scc1 .Lpg17g\n"
"s_cmp_eq_u32 s56,2\n"
"s_cbranch_scc1 .Lpg16g\n"
"s_branch .Lpgep_g\n"
".Lisr6:\n"
"s_mov_b64 exec,-1\n"
".Lisx6:\n"
MF "a[172:175],v[16:19],v[84:87],a[220:223]\n"
"s_waitcnt vmcnt(6) lgkmcnt(0)\n"
CV "v102,v120,v121\n"
"v_add_f32 v59,v59,v122\n"
MF "a[176:179],v[16:19],v[88:91],a[220:223]\n"
"s_barrier\n"
MF "a[196:199],v[20:23],v[88:91],a[220:223]\n"
"s_add_u32 s73,s73,12288\n"
"ds_read_b128 v[16:19],v0\n"
"ds_read_b128 v[20:23],v1\n"
MF "a[216:219],v[24:27],v[88:91],a[220:223]\n"
"s_cmp_eq_u32 s73,s19\n"
"s_cselect_b32 s73,s18,s73\n"
"s_add_u32 m0,s65,49152\n"
MF "a[236:239],v[28:31],v[88:91],a[220:223]\n"
LD "v8,s[36:39],s73 offen nt lds\n"
MF "a[232:235],v[28:31],v[84:87],a[220:223]\n"
"s_add_u32 m0,s65,50048\n"
"v_add_f32 v59,v59,v123\n"
CV "v103,v122,v123\n"
MF "a[212:215],v[24:27],v[84:87],a[220:223]\n"
LD "v8,s[36:39],s73 offen offset:128 nt lds\n"
MF "a[208:211],v[24:27],v[80:83],a[220:223]\n"
"s_add_u32 m0,s65,50944\n"
MF "a[228:231],v[28:31],v[80:83],a[220:223]\n"
LD "v8,s[36:39],s73 offen offset:256 nt lds\n"
MF "a[224:227],v[28:31],v[76:79],a[220:223]\n"
MF "a[204:207],v[24:27],v[76:79],a[220:223]\n"
MF "a[200:203],v[24:27],v[72:75],a[220:223]\n"
MF "a[220:223],v[28:31],v[72:75],a[220:223]\n"
"s_waitcnt lgkmcnt(0)\n"
".Laloopx:\n"
MF "v[72:75],v[16:19],v[136:139],v[32:35]\n"
"s_nop 0\n"
"ds_read_b128 v[24:27],v0 offset:1024\n"
"ds_read_b128 v[28:31],v1 offset:1024\n"
MF "v[72:75],v[20:23],v[156:159],v[72:75]\n"
MF "v[76:79],v[20:23],v[160:163],v[36:39]\n"
MF "v[76:79],v[16:19],v[140:143],v[76:79]\n"
MF "v[80:83],v[16:19],v[144:147],v[40:43]\n"
MF "v[80:83],v[20:23],v[164:167],v[80:83]\n"
MF "v[84:87],v[20:23],v[168:171],v[44:47]\n"
MF "v[84:87],v[16:19],v[148:151],v[84:87]\n"
"s_add_u32 m0,s65,49152\n"
MF "v[88:91],v[16:19],v[152:155],v[48:51]\n"
LD "v8,s[36:39],s73 offen offset:3072 nt lds\n"
MF "v[88:91],v[20:23],v[172:175],v[88:91]\n"
"s_waitcnt lgkmcnt(0)\n"
"ds_read_b128 v[16:19],v0 offset:2048\n"
"ds_read_b128 v[20:23],v1 offset:2048\n"
MF "v[88:91],v[24:27],v[192:195],v[88:91]\n"
"v_exp_f32 v124,v124\n"
MF "v[88:91],v[28:31],v[212:215],v[88:91]\n"
"s_add_u32 m0,s65,50048\n"
"v_exp_f32 v125,v125\n"
MF "v[84:87],v[28:31],v[208:211],v[84:87]\n"
LD "v8,s[36:39],s73 offen offset:3200 nt lds\n"
MF "v[84:87],v[24:27],v[188:191],v[84:87]\n"
"v_exp_f32 v126,v126\n"
MF "v[80:83],v[24:27],v[184:187],v[80:83]\n"
"v_exp_f32 v127,v127\n"
MF "v[80:83],v[28:31],v[204:207],v[80:83]\n"
"s_add_u32 m0,s65,50944\n"
"v_add_f32 v60,v60,v124\n"
"v_add_f32 v60,v60,v125\n"
MF "v[76:79],v[28:31],v[200:203],v[76:79]\n"
LD "v8,s[36:39],s73 offen offset:3328 nt lds\n"
MF "v[76:79],v[24:27],v[180:183],v[76:79]\n"
CV "v106,v124,v125\n"
"v_add_f32 v60,v60,v126\n"
MF "v[72:75],v[24:27],v[176:179],v[72:75]\n"
"v_add_f32 v60,v60,v127\n"
CV "v107,v126,v127\n"
MF "v[72:75],v[28:31],v[196:199],v[72:75]\n"
"s_waitcnt lgkmcnt(0)\n"
"ds_read_b128 v[24:27],v0 offset:6144\n"
"ds_read_b128 v[28:31],v1 offset:6144\n"
MF "v[72:75],v[16:19],v[216:219],v[72:75]\n"
"v_exp_f32 v128,v128\n"
MF "v[72:75],v[20:23],v[236:239],v[72:75]\n"
"v_exp_f32 v129,v129\n"
MF "v[76:79],v[20:23],v[240:243],v[76:79]\n"
"v_exp_f32 v130,v130\n"
MF "v[76:79],v[16:19],v[220:223],v[76:79]\n"
"v_exp_f32 v131,v131\n"
MF "v[80:83],v[16:19],v[224:227],v[80:83]\n"
"v_add_f32 v61,v61,v128\n"
"v_add_f32 v61,v61,v129\n"
MF "v[80:83],v[20:23],v[244:247],v[80:83]\n"
CV "v110,v128,v129\n"
"v_add_f32 v61,v61,v130\n"
MF "v[84:87],v[20:23],v[248:251],v[84:87]\n"
"v_add_f32 v61,v61,v131\n"
CV "v111,v130,v131\n"
MF "v[84:87],v[16:19],v[228:231],v[84:87]\n"
"v_max3_f32 v10,v57,v58,v59\n"
"v_max3_f32 v10,v10,v60,v61\n"
MF "v[88:91],v[16:19],v[232:235],v[88:91]\n"
"v_cmp_ngt_f32 vcc,s77,v10\n"
MF "v[88:91],v[20:23],v[252:255],v[88:91]\n"
"s_waitcnt lgkmcnt(0)\n"
"s_cbranch_vccnz .Lstub_6\n"
".Lback_6:\n"
MF "v[112:115],v[24:27],v[136:139],v[32:35]\n"
"s_add_u32 s74,s74,12288\n"
"ds_read_b128 v[16:19],v0 offset:7168\n"
"ds_read_b128 v[20:23],v1 offset:7168\n"
MF "v[112:115],v[28:31],v[156:159],v[112:115]\n"
"s_cmp_eq_u32 s74,s20\n"
"s_cselect_b32 s74,s70,s74\n"
MF "v[116:119],v[28:31],v[160:163],v[36:39]\n"
"v_exp_f32 v72,v72\n"
MF "v[116:119],v[24:27],v[140:143],v[116:119]\n"
"v_exp_f32 v73,v73\n"
MF "v[120:123],v[24:27],v[144:147],v[40:43]\n"
"v_exp_f32 v74,v74\n"
MF "v[120:123],v[28:31],v[164:167],v[120:123]\n"
"v_exp_f32 v75,v75\n"
MF "v[124:127],v[28:31],v[168:171],v[44:47]\n"
"v_add_f32 v52,v57,v72\n"
"v_add_f32 v52,v52,v73\n"
MF "v[124:127],v[24:27],v[148:151],v[124:127]\n"
CV "v72,v72,v73\n"
"v_add_f32 v52,v52,v74\n"
MF "v[128:131],v[24:27],v[152:155],v[48:51]\n"
"v_add_f32 v52,v52,v75\n"
CV "v73,v74,v75\n"
MF "v[128:131],v[28:31],v[172:175],v[128:131]\n"
"s_waitcnt lgkmcnt(0)\n"
"ds_read_b128 v[24:27],v0 offset:8192\n"
"ds_read_b128 v[28:31],v1 offset:8192\n"
MF "v[128:131],v[16:19],v[192:195],v[128:131]\n"
"v_exp_f32 v76,v76\n"
MF "v[128:131],v[20:23],v[212:215],v[128:131]\n"
"v_exp_f32 v77,v77\n"
MF "v[124:127],v[20:23],v[208:211],v[124:127]\n"
"v_exp_f32 v78,v78\n"
MF "v[124:127],v[16:19],v[188:191],v[124:127]\n"
"v_exp_f32 v79,v79\n"
MF "v[120:123],v[16:19],v[184:187],v[120:123]\n"
"v_add_f32 v53,v58,v76\n"
"v_add_f32 v53,v53,v77\n"
MF "v[120:123],v[20:23],v[204:207],v[120:123]\n"
CV "v76,v76,v77\n"
"v_add_f32 v53,v53,v78\n"
MF "v[116:119],v[20:23],v[200:203],v[116:119]\n"
"s_waitcnt vmcnt(12) lgkmcnt(0)\n"
"v_add_f32 v53,v53,v79\n"
CV "v77,v78,v79\n"
MF "v[116:119],v[16:19],v[180:183],v[116:119]\n"
"s_barrier\n"
MF "v[112:115],v[16:19],v[176:179],v[112:115]\n"
"v_exp_f32 v80,v80\n"
MF "v[112:115],v[20:23],v[196:199],v[112:115]\n"
"s_nop 0\n"
TR "v[16:17],v2 offset:24576\n"
TR "v[18:19],v2 offset:30720\n"
MF "v[112:115],v[24:27],v[216:219],v[112:115]\n"
"s_add_u32 s72,s72,6144\n"
TR "v[20:21],v3 offset:24576\n"
TR "v[22:23],v3 offset:30720\n"
MF "v[112:115],v[28:31],v[236:239],v[112:115]\n"
"s_cmp_eq_u32 s72,s17\n"
"s_cselect_b32 s72,s16,s72\n"
"s_add_u32 m0,s64,0\n"
MF "v[116:119],v[28:31],v[240:243],v[116:119]\n"
LD "v7,s[32:35],s72 offen nt lds\n"
MF "v[116:119],v[24:27],v[220:223],v[116:119]\n"
"s_add_u32 m0,s64,896\n"
"v_exp_f32 v81,v81\n"
MF "v[120:123],v[24:27],v[224:227],v[120:123]\n"
LD "v7,s[32:35],s72 offen offset:128 nt lds\n"
MF "v[120:123],v[28:31],v[244:247],v[120:123]\n"
"v_exp_f32 v82,v82\n"
MF "v[124:127],v[28:31],v[248:251],v[124:127]\n"
"s_add_u32 m0,s64,1792\n"
"v_exp_f32 v83,v83\n"
MF "v[124:127],v[24:27],v[228:231],v[124:127]\n"
LD "v7,s[32:35],s72 offen offset:256 nt lds\n"
MF "v[128:131],v[24:27],v[232:235],v[128:131]\n"
"s_add_u32 s72,s72,6144\n"
"v_add_f32 v54,v59,v80\n"
"v_add_f32 v54,v54,v81\n"
MF "v[128:131],v[28:31],v[252:255],v[128:131]\n"
"s_waitcnt lgkmcnt(0)\n"
TR "v[24:25],v4 offset:24576\n"
TR "v[26:27],v4 offset:30720\n"
MF "a[0:3],v[16:19],v[92:95],a[0:3]\n"
"s_add_u32 m0,s64,6144\n"
TR "v[28:29],v5 offset:24576\n"
TR "v[30:31],v5 offset:30720\n"
MF "a[20:23],v[20:23],v[92:95],a[20:23]\n"
LD "v7,s[32:35],s72 offen nt lds\n"
MF "a[24:27],v[20:23],v[96:99],a[24:27]\n"
CV "v80,v80,v81\n"
"v_add_f32 v54,v54,v82\n"
MF "a[4:7],v[16:19],v[96:99],a[4:7]\n"
"s_add_u32 m0,s64,7040\n"
"v_add_f32 v54,v54,v83\n"
CV "v81,v82,v83\n"
MF "a[8:11],v[16:19],v[100:103],a[8:11]\n"
LD "v7,s[32:35],s72 offen offset:128 nt lds\n"
MF "a[28:31],v[20:23],v[100:103],a[28:31]\n"
"v_exp_f32 v84,v84\n"
MF "a[32:35],v[20:23],v[104:107],a[32:35]\n"
"s_add_u32 m0,s64,7936\n"
"v_exp_f32 v85,v85\n"
MF "a[12:15],v[16:19],v[104:107],a[12:15]\n"
LD "v7,s[32:35],s72 offen offset:256 nt lds\n"
MF "a[16:19],v[16:19],v[108:111],a[16:19]\n"
"v_exp_f32 v86,v86\n"
MF "a[36:39],v[20:23],v[108:111],a[36:39]\n"
"s_waitcnt lgkmcnt(0)\n"
TR "v[16:17],v2 offset:25600\n"
TR "v[18:19],v2 offset:31744\n"
MF "a[56:59],v[24:27],v[108:111],a[56:59]\n"
"s_nop 0\n"
TR "v[20:21],v3 offset:25600\n"
TR "v[22:23],v3 offset:31744\n"
MF "a[76:79],v[28:31],v[108:111],a[76:79]\n"
"v_exp_f32 v87,v87\n"
MF "a[72:75],v[28:31],v[104:107],a[72:75]\n"
"v_add_f32 v55,v60,v84\n"
"v_add_f32 v55,v55,v85\n"
MF "a[52:55],v[24:27],v[104:107],a[52:55]\n"
CV "v84,v84,v85\n"
"v_add_f32 v55,v55,v86\n"
MF "a[48:51],v[24:27],v[100:103],a[48:51]\n"
"v_add_f32 v55,v55,v87\n"
CV "v85,v86,v87\n"
MF "a[68:71],v[28:31],v[100:103],a[68:71]\n"
"v_exp_f32 v88,v88\n"
MF "a[64:67],v[28:31],v[96:99],a[64:67]\n"
"v_exp_f32 v89,v89\n"
MF "a[44:47],v[24:27],v[96:99],a[44:47]\n"
"v_exp_f32 v90,v90\n"
MF "a[40:43],v[24:27],v[92:95],a[40:43]\n"
"v_exp_f32 v91,v91\n"
MF "a[60:63],v[28:31],v[92:95],a[60:63]\n"
"s_waitcnt lgkmcnt(0)\n"
TR "v[24:25],v4 offset:25600\n"
TR "v[26:27],v4 offset:31744\n"
MF "a[80:83],v[16:19],v[92:95],a[80:83]\n"
"s_nop 0\n"
TR "v[28:29],v5 offset:25600\n"
TR "v[30:31],v5 offset:31744\n"
MF "a[100:103],v[20:23],v[92:95],a[100:103]\n"
"v_add_f32 v56,v61,v88\n"
"v_add_f32 v56,v56,v89\n"
MF "a[104:107],v[20:23],v[96:99],a[104:107]\n"
CV "v88,v88,v89\n"
"v_add_f32 v56,v56,v90\n"
MF "a[84:87],v[16:19],v[96:99],a[84:87]\n"
"v_add_f32 v56,v56,v91\n"
CV "v89,v90,v91\n"
MF "a[88:91],v[16:19],v[100:103],a[88:91]\n"
"v_exp_f32 v112,v112\n"
MF "a[108:111],v[20:23],v[100:103],a[108:111]\n"
"v_exp_f32 v113,v113\n"
MF "a[112:115],v[20:23],v[104:107],a[112:115]\n"
"v_exp_f32 v114,v114\n"
MF "a[92:95],v[16:19],v[104:107],a[92:95]\n"
"v_exp_f32 v115,v115\n"
MF "a[96:99],v[16:19],v[108:111],a[96:99]\n"
"v_add_f32 v52,v52,v112\n"
"v_add_f32 v52,v52,v113\n"
MF "a[116:119],v[20:23],v[108:111],a[116:119]\n"
"s_waitcnt lgkmcnt(0)\n"
TR "v[16:17],v2 offset:26624\n"
TR "v[18:19],v2 offset:32768\n"
MF "a[136:139],v[24:27],v[108:111],a[136:139]\n"
"s_nop 0\n"
TR "v[20:21],v3 offset:26624\n"
TR "v[22:23],v3 offset:32768\n"
MF "a[156:159],v[28:31],v[108:111],a[156:159]\n"
CV "v74,v112,v113\n"
"v_add_f32 v52,v52,v114\n"
MF "a[152:155],v[28:31],v[104:107],a[152:155]\n"
"v_add_f32 v52,v52,v115\n"
CV "v75,v114,v115\n"
MF "a[132:135],v[24:27],v[104:107],a[132:135]\n"
"v_exp_f32 v116,v116\n"
MF "a[128:131],v[24:27],v[100:103],a[128:131]\n"
"v_exp_f32 v117,v117\n"
MF "a[148:151],v[28:31],v[100:103],a[148:151]\n"
"v_exp_f32 v118,v118\n"
MF "a[144:147],v[28:31],v[96:99],a[144:147]\n"
"v_exp_f32 v119,v119\n"
MF "a[124:127],v[24:27],v[96:99],a[124:127]\n"
"v_add_f32 v53,v53,v116\n"
"v_add_f32 v53,v53,v117\n"
MF "a[120:123],v[24:27],v[92:95],a[120:123]\n"
CV "v78,v116,v117\n"
"v_add_f32 v53,v53,v118\n"
MF "a[140:143],v[28:31],v[92:95],a[140:143]\n"
"s_waitcnt lgkmcnt(0)\n"
TR "v[24:25],v4 offset:26624\n"
TR "v[26:27],v4 offset:32768\n"
MF "a[160:163],v[16:19],v[92:95],a[160:163]\n"
"s_nop 0\n"
TR "v[28:29],v5 offset:26624\n"
TR "v[30:31],v5 offset:32768\n"
MF "a[180:183],v[20:23],v[92:95],a[180:183]\n"
"v_add_f32 v53,v53,v119\n"
CV "v79,v118,v119\n"
MF "a[184:187],v[20:23],v[96:99],a[184:187]\n"
"v_exp_f32 v120,v120\n"
MF "a[164:167],v[16:19],v[96:99],a[164:167]\n"
"v_exp_f32 v121,v121\n"
MF "a[168:171],v[16:19],v[100:103],a[168:171]\n"
"v_exp_f32 v122,v122\n"
MF "a[188:191],v[20:23],v[100:103],a[188:191]\n"
"v_exp_f32 v123,v123\n"
MF "a[192:195],v[20:23],v[104:107],a[192:195]\n"
"v_add_f32 v54,v54,v120\n"
"v_add_f32 v54,v54,v121\n"
MF "a[172:175],v[16:19],v[104:107],a[172:175]\n"
"s_waitcnt vmcnt(12) lgkmcnt(0)\n"
CV "v82,v120,v121\n"
"v_add_f32 v54,v54,v122\n"
MF "a[176:179],v[16:19],v[108:111],a[176:179]\n"
"s_barrier\n"
MF "a[196:199],v[20:23],v[108:111],a[196:199]\n"
"s_add_u32 s73,s73,12288\n"
"ds_read_b128 v[16:19],v0 offset:24576\n"
"ds_read_b128 v[20:23],v1 offset:24576\n"
MF "a[216:219],v[24:27],v[108:111],a[216:219]\n"
"s_cmp_eq_u32 s73,s19\n"
"s_cselect_b32 s73,s18,s73\n"
"s_add_u32 m0,s65,73728\n"
MF "a[236:239],v[28:31],v[108:111],a[236:239]\n"
LD "v8,s[36:39],s73 offen nt lds\n"
MF "a[232:235],v[28:31],v[104:107],a[232:235]\n"
"s_add_u32 m0,s65,74624\n"
"v_add_f32 v54,v54,v123\n"
CV "v83,v122,v123\n"
MF "a[212:215],v[24:27],v[104:107],a[212:215]\n"
LD "v8,s[36:39],s73 offen offset:128 nt lds\n"
MF "a[208:211],v[24:27],v[100:103],a[208:211]\n"
"s_add_u32 m0,s65,75520\n"
MF "a[228:231],v[28:31],v[100:103],a[228:231]\n"
LD "v8,s[36:39],s73 offen offset:256 nt lds\n"
MF "a[224:227],v[28:31],v[96:99],a[224:227]\n"
MF "a[204:207],v[24:27],v[96:99],a[204:207]\n"
MF "a[200:203],v[24:27],v[92:95],a[200:203]\n"
MF "a[220:223],v[28:31],v[92:95],a[220:223]\n"
"s_waitcnt lgkmcnt(0)\n"
MF "v[92:95],v[16:19],v[136:139],v[32:35]\n"
"s_add_u32 m0,s65,73728\n"
"ds_read_b128 v[24:27],v0 offset:25600\n"
"ds_read_b128 v[28:31],v1 offset:25600\n"
MF "v[92:95],v[20:23],v[156:159],v[92:95]\n"
LD "v8,s[36:39],s73 offen offset:3072 nt lds\n"
MF "v[96:99],v[20:23],v[160:163],v[36:39]\n"
"v_exp_f32 v124,v124\n"
MF "v[96:99],v[16:19],v[140:143],v[96:99]\n"
"v_exp_f32 v125,v125\n"
MF "v[100:103],v[16:19],v[144:147],v[40:43]\n"
"s_add_u32 m0,s65,74624\n"
"v_exp_f32 v126,v126\n"
MF "v[100:103],v[20:23],v[164:167],v[100:103]\n"
LD "v8,s[36:39],s73 offen offset:3200 nt lds\n"
MF "v[104:107],v[20:23],v[168:171],v[44:47]\n"
"v_exp_f32 v127,v127\n"
MF "v[104:107],v[16:19],v[148:151],v[104:107]\n"
"s_add_u32 m0,s65,75520\n"
"v_add_f32 v55,v55,v124\n"
"v_add_f32 v55,v55,v125\n"
MF "v[108:111],v[16:19],v[152:155],v[48:51]\n"
LD "v8,s[36:39],s73 offen offset:3328 nt lds\n"
MF "v[108:111],v[20:23],v[172:175],v[108:111]\n"
"s_waitcnt lgkmcnt(0)\n"
"ds_read_b128 v[16:19],v0 offset:26624\n"
"ds_read_b128 v[20:23],v1 offset:26624\n"
MF "v[108:111],v[24:27],v[192:195],v[108:111]\n"
CV "v86,v124,v125\n"
"v_add_f32 v55,v55,v126\n"
MF "v[108:111],v[28:31],v[212:215],v[108:111]\n"
"v_add_f32 v55,v55,v127\n"
CV "v87,v126,v127\n"
MF "v[104:107],v[28:31],v[208:211],v[104:107]\n"
"v_exp_f32 v128,v128\n"
MF "v[104:107],v[24:27],v[188:191],v[104:107]\n"
"v_exp_f32 v129,v129\n"
MF "v[100:103],v[24:27],v[184:187],v[100:103]\n"
"v_exp_f32 v130,v130\n"
MF "v[100:103],v[28:31],v[204:207],v[100:103]\n"
"v_exp_f32 v131,v131\n"
MF "v[96:99],v[28:31],v[200:203],v[96:99]\n"
"v_add_f32 v56,v56,v128\n"
"v_add_f32 v56,v56,v129\n"
MF "v[96:99],v[24:27],v[180:183],v[96:99]\n"
CV "v90,v128,v129\n"
"v_add_f32 v56,v56,v130\n"
MF "v[92:95],v[24:27],v[176:179],v[92:95]\n"
"v_add_f32 v56,v56,v131\n"
CV "v91,v130,v131\n"
MF "v[92:95],v[28:31],v[196:199],v[92:95]\n"
"s_waitcnt lgkmcnt(0)\n"
"ds_read_b128 v[24:27],v0 offset:30720\n"
"ds_read_b128 v[28:31],v1 offset:30720\n"
MF "v[92:95],v[16:19],v[216:219],v[92:95]\n"
MF "v[92:95],v[20:23],v[236:239],v[92:95]\n"
MF "v[96:99],v[20:23],v[240:243],v[96:99]\n"
MF "v[96:99],v[16:19],v[220:223],v[96:99]\n"
MF "v[100:103],v[16:19],v[224:227],v[100:103]\n"
MF "v[100:103],v[20:23],v[244:247],v[100:103]\n"
MF "v[104:107],v[20:23],v[248:251],v[104:107]\n"
MF "v[104:107],v[16:19],v[228:231],v[104:107]\n"
"v_max3_f32 v10,v52,v53,v54\n"
"v_max3_f32 v10,v10,v55,v56\n"
MF "v[108:111],v[16:19],v[232:235],v[108:111]\n"
"v_cmp_ngt_f32 vcc,s77,v10\n"
MF "v[108:111],v[20:23],v[252:255],v[108:111]\n"
"s_waitcnt lgkmcnt(0)\n"
"s_cbranch_vccnz .Lstub_7\n"
".Lback_7:\n"
MF "v[112:115],v[24:27],v[136:139],v[32:35]\n"
"s_add_u32 s74,s74,12288\n"
"ds_read_b128 v[16:19],v0 offset:31744\n"
"ds_read_b128 v[20:23],v1 offset:31744\n"
MF "v[112:115],v[28:31],v[156:159],v[112:115]\n"
"s_cmp_eq_u32 s74,s20\n"
"s_cselect_b32 s74,s70,s74\n"
MF "v[116:119],v[28:31],v[160:163],v[36:39]\n"
"v_exp_f32 v92,v92\n"
MF "v[116:119],v[24:27],v[140:143],v[116:119]\n"
"v_exp_f32 v93,v93\n"
MF "v[120:123],v[24:27],v[144:147],v[40:43]\n"
"v_exp_f32 v94,v94\n"
MF "v[120:123],v[28:31],v[164:167],v[120:123]\n"
"v_exp_f32 v95,v95\n"
MF "v[124:127],v[28:31],v[168:171],v[44:47]\n"
"v_add_f32 v57,v52,v92\n"
"v_add_f32 v57,v57,v93\n"
MF "v[124:127],v[24:27],v[148:151],v[124:127]\n"
CV "v92,v92,v93\n"
"v_add_f32 v57,v57,v94\n"
MF "v[128:131],v[24:27],v[152:155],v[48:51]\n"
"v_add_f32 v57,v57,v95\n"
CV "v93,v94,v95\n"
MF "v[128:131],v[28:31],v[172:175],v[128:131]\n"
"s_waitcnt lgkmcnt(0)\n"
"ds_read_b128 v[24:27],v0 offset:32768\n"
"ds_read_b128 v[28:31],v1 offset:32768\n"
MF "v[128:131],v[16:19],v[192:195],v[128:131]\n"
"s_sub_u32 s61,s61,1\n"
"v_exp_f32 v96,v96\n"
MF "v[128:131],v[20:23],v[212:215],v[128:131]\n"
"v_exp_f32 v97,v97\n"
MF "v[124:127],v[20:23],v[208:211],v[124:127]\n"
"v_exp_f32 v98,v98\n"
MF "v[124:127],v[16:19],v[188:191],v[124:127]\n"
"v_exp_f32 v99,v99\n"
MF "v[120:123],v[16:19],v[184:187],v[120:123]\n"
"v_add_f32 v58,v53,v96\n"
"v_add_f32 v58,v58,v97\n"
MF "v[120:123],v[20:23],v[204:207],v[120:123]\n"
CV "v96,v96,v97\n"
"v_add_f32 v58,v58,v98\n"
MF "v[116:119],v[20:23],v[200:203],v[116:119]\n"
"s_waitcnt vmcnt(12) lgkmcnt(0)\n"
"v_add_f32 v58,v58,v99\n"
CV "v97,v98,v99\n"
MF "v[116:119],v[16:19],v[180:183],v[116:119]\n"
"s_barrier\n"
MF "v[112:115],v[16:19],v[176:179],v[112:115]\n"
"v_exp_f32 v100,v100\n"
MF "v[112:115],v[20:23],v[196:199],v[112:115]\n"
"s_nop 0\n"
TR "v[16:17],v2\n"
TR "v[18:19],v2 offset:6144\n"
MF "v[112:115],v[24:27],v[216:219],v[112:115]\n"
"s_add_u32 s72,s72,6144\n"
TR "v[20:21],v3\n"
TR "v[22:23],v3 offset:6144\n"
MF "v[112:115],v[28:31],v[236:239],v[112:115]\n"
"s_cmp_eq_u32 s72,s17\n"
"s_cselect_b32 s72,s16,s72\n"
"s_add_u32 m0,s64,24576\n"
MF "v[116:119],v[28:31],v[240:243],v[116:119]\n"
LD "v7,s[32:35],s72 offen nt lds\n"
MF "v[116:119],v[24:27],v[220:223],v[116:119]\n"
"s_add_u32 m0,s64,25472\n"
"v_exp_f32 v101,v101\n"
MF "v[120:123],v[24:27],v[224:227],v[120:123]\n"
LD "v7,s[32:35],s72 offen offset:128 nt lds\n"
MF "v[120:123],v[28:31],v[244:247],v[120:123]\n"
"v_exp_f32 v102,v102\n"
MF "v[124:127],v[28:31],v[248:251],v[124:127]\n"
"s_add_u32 m0,s64,26368\n"
"v_exp_f32 v103,v103\n"
MF "v[124:127],v[24:27],v[228:231],v[124:127]\n"
LD "v7,s[32:35],s72 offen offset:256 nt lds\n"
MF "v[128:131],v[24:27],v[232:235],v[128:131]\n"
"s_add_u32 s72,s72,6144\n"
"v_add_f32 v59,v54,v100\n"
"v_add_f32 v59,v59,v101\n"
MF "v[128:131],v[28:31],v[252:255],v[128:131]\n"
"s_waitcnt lgkmcnt(0)\n"
TR "v[24:25],v4\n"
TR "v[26:27],v4 offset:6144\n"
MF "a[0:3],v[16:19],v[72:75],a[0:3]\n"
"s_add_u32 m0,s64,30720\n"
TR "v[28:29],v5\n"
TR "v[30:31],v5 offset:6144\n"
MF "a[20:23],v[20:23],v[72:75],a[20:23]\n"
LD "v7,s[32:35],s72 offen nt lds\n"
MF "a[24:27],v[20:23],v[76:79],a[24:27]\n"
CV "v100,v100,v101\n"
"v_add_f32 v59,v59,v102\n"
MF "a[4:7],v[16:19],v[76:79],a[4:7]\n"
"s_add_u32 m0,s64,31616\n"
"v_add_f32 v59,v59,v103\n"
CV "v101,v102,v103\n"
MF "a[8:11],v[16:19],v[80:83],a[8:11]\n"
LD "v7,s[32:35],s72 offen offset:128 nt lds\n"
MF "a[28:31],v[20:23],v[80:83],a[28:31]\n"
"v_exp_f32 v104,v104\n"
MF "a[32:35],v[20:23],v[84:87],a[32:35]\n"
"s_add_u32 m0,s64,32512\n"
"v_exp_f32 v105,v105\n"
MF "a[12:15],v[16:19],v[84:87],a[12:15]\n"
LD "v7,s[32:35],s72 offen offset:256 nt lds\n"
MF "a[16:19],v[16:19],v[88:91],a[16:19]\n"
"v_exp_f32 v106,v106\n"
MF "a[36:39],v[20:23],v[88:91],a[36:39]\n"
"s_waitcnt lgkmcnt(0)\n"
TR "v[16:17],v2 offset:1024\n"
TR "v[18:19],v2 offset:7168\n"
MF "a[56:59],v[24:27],v[88:91],a[56:59]\n"
".Lpg11b:\n"
"s_cbranch_execz .Lpgqj_a\n"
".Lpg11d:\n"
"s_cbranch_execz .Lisr2\n"
"s_nop 0\n"
TR "v[20:21],v3 offset:1024\n"
TR "v[22:23],v3 offset:7168\n"
MF "a[76:79],v[28:31],v[88:91],a[76:79]\n"
"v_exp_f32 v107,v107\n"
MF "a[72:75],v[28:31],v[84:87],a[72:75]\n"
"v_add_f32 v60,v55,v104\n"
"v_add_f32 v60,v60,v105\n"
MF "a[52:55],v[24:27],v[84:87],a[52:55]\n"
CV "v104,v104,v105\n"
"v_add_f32 v60,v60,v106\n"
MF "a[48:51],v[24:27],v[80:83],a[48:51]\n"
"v_add_f32 v60,v60,v107\n"
CV "v105,v106,v107\n"
MF "a[68:71],v[28:31],v[80:83],a[68:71]\n"
"v_exp_f32 v108,v108\n"
MF "a[64:67],v[28:31],v[76:79],a[64:67]\n"
"v_exp_f32 v109,v109\n"
MF "a[44:47],v[24:27],v[76:79],a[44:47]\n"
"v_exp_f32 v110,v110\n"
MF "a[40:43],v[24:27],v[72:75],a[40:43]\n"
"v_exp_f32 v111,v111\n"
MF "a[60:63],v[28:31],v[72:75],a[60:63]\n"
"s_waitcnt lgkmcnt(0)\n"
TR "v[24:25],v4 offset:1024\n"
TR "v[26:27],v4 offset:7168\n"
MF "a[80:83],v[16:19],v[72:75],a[80:83]\n"
"s_nop 0\n"
TR "v[28:29],v5 offset:1024\n"
TR "v[30:31],v5 offset:7168\n"
MF "a[100:103],v[20:23],v[72:75],a[100:103]\n"
"v_add_f32 v61,v56,v108\n"
"v_add_f32 v61,v61,v109\n"
MF "a[104:107],v[20:23],v[76:79],a[104:107]\n"
CV "v108,v108,v109\n"
"v_add_f32 v61,v61,v110\n"
MF "a[84:87],v[16:19],v[76:79],a[84:87]\n"
"v_add_f32 v61,v61,v111\n"
CV "v109,v110,v111\n"
MF "a[88:91],v[16:19],v[80:83],a[88:91]\n"
"v_exp_f32 v112,v112\n"
MF "a[108:111],v[20:23],v[80:83],a[108:111]\n"
"v_exp_f32 v113,v113\n"
MF "a[112:115],v[20:23],v[84:87],a[112:115]\n"
"v_exp_f32 v114,v114\n"
MF "a[92:95],v[16:19],v[84:87],a[92:95]\n"
"v_exp_f32 v115,v115\n"
MF "a[96:99],v[16:19],v[88:91],a[96:99]\n"
"v_add_f32 v57,v57,v112\n"
"v_add_f32 v57,v57,v113\n"
MF "a[116:119],v[20:23],v[88:91],a[116:119]\n"
"s_waitcnt lgkmcnt(0)\n"
TR "v[16:17],v2 offset:2048\n"
TR "v[18:19],v2 offset:8192\n"
MF "a[136:139],v[24:27],v[88:91],a[136:139]\n"
"s_nop 0\n"
TR "v[20:21],v3 offset:2048\n"
TR "v[22:23],v3 offset:8192\n"
MF "a[156:159],v[28:31],v[88:91],a[156:159]\n"
CV "v94,v112,v113\n"
"v_add_f32 v57,v57,v114\n"
MF "a[152:155],v[28:31],v[84:87],a[152:155]\n"
"v_add_f32 v57,v57,v115\n"
CV "v95,v114,v115\n"
MF "a[132:135],v[24:27],v[84:87],a[132:135]\n"
"v_exp_f32 v116,v116\n"
MF "a[128:131],v[24:27],v[80:83],a[128:131]\n"
"v_exp_f32 v117,v117\n"
MF "a[148:151],v[28:31],v[80:83],a[148:151]\n"
"v_exp_f32 v118,v118\n"
MF "a[144:147],v[28:31],v[76:79],a[144:147]\n"
"v_exp_f32 v119,v119\n"
MF "a[124:127],v[24:27],v[76:79],a[124:127]\n"
"v_add_f32 v58,v58,v116\n"
"v_add_f32 v58,v58,v117\n"
MF "a[120:123],v[24:27],v[72:75],a[120:123]\n"
CV "v98,v116,v117\n"
"v_add_f32 v58,v58,v118\n"
MF "a[140:143],v[28:31],v[72:75],a[140:143]\n"
"s_waitcnt lgkmcnt(0)\n"
TR "v[24:25],v4 offset:2048\n"
TR "v[26:27],v4 offset:8192\n"
MF "a[160:163],v[16:19],v[72:75],a[160:163]\n"
"s_nop 0\n"
TR "v[28:29],v5 offset:2048\n"
TR "v[30:31],v5 offset:8192\n"
MF "a[180:183],v[20:23],v[72:75],a[180:183]\n"
"v_add_f32 v58,v58,v119\n"
CV "v99,v118,v119\n"
MF "a[184:187],v[20:23],v[76:79],a[184:187]\n"
"v_exp_f32 v120,v120\n"
MF "a[164:167],v[16:19],v[76:79],a[164:167]\n"
"v_exp_f32 v121,v121\n"
MF "a[168:171],v[16:19],v[80:83],a[168:171]\n"
"v_exp_f32 v122,v122\n"
MF "a[188:191],v[20:23],v[80:83],a[188:191]\n"
"v_exp_f32 v123,v123\n"
MF "a[192:195],v[20:23],v[84:87],a[192:195]\n"
"v_add_f32 v59,v59,v120\n"
"v_add_f32 v59,v59,v121\n"
MF "a[172:175],v[16:19],v[84:87],a[172:175]\n"
"s_waitcnt vmcnt(12) lgkmcnt(0)\n"
CV "v102,v120,v121\n"
"v_add_f32 v59,v59,v122\n"
MF "a[176:179],v[16:19],v[88:91],a[176:179]\n"
"s_barrier\n"
MF "a[196:199],v[20:23],v[88:91],a[196:199]\n"
"s_add_u32 s73,s73,12288\n"
"ds_read_b128 v[16:19],v0\n"
"ds_read_b128 v[20:23],v1\n"
MF "a[216:219],v[24:27],v[88:91],a[216:219]\n"
"s_cmp_eq_u32 s73,s19\n"
"s_cselect_b32 s73,s18,s73\n"
"s_add_u32 m0,s65,49152\n"
MF "a[236:239],v[28:31],v[88:91],a[236:239]\n"
LD "v8,s[36:39],s73 offen nt lds\n"
MF "a[232:235],v[28:31],v[84:87],a[232:235]\n"
"s_add_u32 m0,s65,50048\n"
"v_add_f32 v59,v59,v123\n"
CV "v103,v122,v123\n"
MF "a[212:215],v[24:27],v[84:87],a[212:215]\n"
LD "v8,s[36:39],s73 offen offset:128 nt lds\n"
MF "a[208:211],v[24:27],v[80:83],a[208:211]\n"
"s_add_u32 m0,s65,50944\n"
MF "a[228:231],v[28:31],v[80:83],a[228:231]\n"
LD "v8,s[36:39],s73 offen offset:256 nt lds\n"
MF "a[224:227],v[28:31],v[76:79],a[224:227]\n"
MF "a[204:207],v[24:27],v[76:79],a[204:207]\n"
"s_waitcnt lgkmcnt(0)\n"
MF "a[200:203],v[24:27],v[72:75],a[200:203]\n"
"s_cmp_lg_u32 s61,1\n"
MF "a[220:223],v[28:31],v[72:75],a[220:223]\n"
"s_cbranch_scc1 .Laloopx\n"
"s_cmp_eq_u32 s57,0\n"
"s_cbranch_scc0 .Lep\n"
".Lepx:\n"
MF "v[72:75],v[16:19],v[136:139],v[32:35]\n"
"s_add_u32 m0,s65,49152\n"
"ds_read_b128 v[24:27],v0 offset:1024\n"
"ds_read_b128 v[28:31],v1 offset:1024\n"
MF "v[72:75],v[20:23],v[156:159],v[72:75]\n"
LD "v8,s[36:39],s73 offen offset:3072 nt lds\n"
MF "v[76:79],v[20:23],v[160:163],v[36:39]\n"
"v_exp_f32 v124,v124\n"
MF "v[76:79],v[16:19],v[140:143],v[76:79]\n"
"v_exp_f32 v125,v125\n"
MF "v[80:83],v[16:19],v[144:147],v[40:43]\n"
"s_add_u32 m0,s65,50048\n"
"v_exp_f32 v126,v126\n"
MF "v[80:83],v[20:23],v[164:167],v[80:83]\n"
LD "v8,s[36:39],s73 offen offset:3200 nt lds\n"
MF "v[84:87],v[20:23],v[168:171],v[44:47]\n"
"v_exp_f32 v127,v127\n"
MF "v[84:87],v[16:19],v[148:151],v[84:87]\n"
"s_add_u32 m0,s65,50944\n"
"v_add_f32 v60,v60,v124\n"
"v_add_f32 v60,v60,v125\n"
MF "v[88:91],v[16:19],v[152:155],v[48:51]\n"
LD "v8,s[36:39],s73 offen offset:3328 nt lds\n"
MF "v[88:91],v[20:23],v[172:175],v[88:91]\n"
"s_waitcnt lgkmcnt(0)\n"
"ds_read_b128 v[16:19],v0 offset:2048\n"
"ds_read_b128 v[20:23],v1 offset:2048\n"
MF "v[88:91],v[24:27],v[192:195],v[88:91]\n"
CV "v106,v124,v125\n"
"v_add_f32 v60,v60,v126\n"
MF "v[88:91],v[28:31],v[212:215],v[88:91]\n"
"v_add_f32 v60,v60,v127\n"
CV "v107,v126,v127\n"
MF "v[84:87],v[28:31],v[208:211],v[84:87]\n"
"v_exp_f32 v128,v128\n"
MF "v[84:87],v[24:27],v[188:191],v[84:87]\n"
"v_exp_f32 v129,v129\n"
MF "v[80:83],v[24:27],v[184:187],v[80:83]\n"
"v_exp_f32 v130,v130\n"
MF "v[80:83],v[28:31],v[204:207],v[80:83]\n"
"v_exp_f32 v131,v131\n"
MF "v[76:79],v[28:31],v[200:203],v[76:79]\n"
"v_add_f32 v61,v61,v128\n"
"v_add_f32 v61,v61,v129\n"
MF "v[76:79],v[24:27],v[180:183],v[76:79]\n"
CV "v110,v128,v129\n"
"v_add_f32 v61,v61,v130\n"
MF "v[72:75],v[24:27],v[176:179],v[72:75]\n"
"v_add_f32 v61,v61,v131\n"
CV "v111,v130,v131\n"
MF "v[72:75],v[28:31],v[196:199],v[72:75]\n"
"s_waitcnt lgkmcnt(0)\n"
"ds_read_b128 v[24:27],v0 offset:6144\n"
"ds_read_b128 v[28:31],v1 offset:6144\n"
MF "v[72:75],v[16:19],v[216:219],v[72:75]\n"
MF "v[72:75],v[20:23],v[236:239],v[72:75]\n"
MF "v[76:79],v[20:23],v[240:243],v[76:79]\n"
MF "v[76:79],v[16:19],v[220:223],v[76:79]\n"
MF "v[80:83],v[16:19],v[224:227],v[80:83]\n"
MF "v[80:83],v[20:23],v[244:247],v[80:83]\n"
MF "v[84:87],v[20:23],v[248:251],v[84:87]\n"
MF "v[84:87],v[16:19],v[228:231],v[84:87]\n"
"v_max3_f32 v10,v57,v58,v59\n"
"v_max3_f32 v10,v10,v60,v61\n"
MF "v[88:91],v[16:19],v[232:235],v[88:91]\n"
"v_cmp_le_f32 vcc,0x47800000,v10\n"
MF "v[88:91],v[20:23],v[252:255],v[88:91]\n"
"s_waitcnt lgkmcnt(0)\n"
"s_cbranch_vccnz .Lstub_8\n"
".Lback_8:\n"
MF "v[112:115],v[24:27],v[136:139],v[32:35]\n"
"s_add_u32 s74,s74,12288\n"
"ds_read_b128 v[16:19],v0 offset:7168\n"
"ds_read_b128 v[20:23],v1 offset:7168\n"
MF "v[112:115],v[28:31],v[156:159],v[112:115]\n"
"s_cmp_eq_u32 s74,s20\n"
"s_cselect_b32 s74,s70,s74\n"
MF "v[116:119],v[28:31],v[160:163],v[36:39]\n"
"v_exp_f32 v72,v72\n"
MF "v[116:119],v[24:27],v[140:143],v[116:119]\n"
"v_exp_f32 v73,v73\n"
MF "v[120:123],v[24:27],v[144:147],v[40:43]\n"
"v_exp_f32 v74,v74\n"
MF "v[120:123],v[28:31],v[164:167],v[120:123]\n"
"v_exp_f32 v75,v75\n"
MF "v[124:127],v[28:31],v[168:171],v[44:47]\n"
"v_add_f32 v52,v57,v72\n"
"v_add_f32 v52,v52,v73\n"
MF "v[124:127],v[24:27],v[148:151],v[124:127]\n"
CV "v72,v72,v73\n"
"v_add_f32 v52,v52,v74\n"
MF "v[128:131],v[24:27],v[152:155],v[48:51]\n"
"v_add_f32 v52,v52,v75\n"
CV "v73,v74,v75\n"
MF "v[128:131],v[28:31],v[172:175],v[128:131]\n"
"s_waitcnt lgkmcnt(0)\n"
"ds_read_b128 v[24:27],v0 offset:8192\n"
"ds_read_b128 v[28:31],v1 offset:8192\n"
MF "v[128:131],v[16:19],v[192:195],v[128:131]\n"
"v_exp_f32 v76,v76\n"
MF "v[128:131],v[20:23],v[212:215],v[128:131]\n"
"v_exp_f32 v77,v77\n"
MF "v[124:127],v[20:23],v[208:211],v[124:127]\n"
"v_exp_f32 v78,v78\n"
MF "v[124:127],v[16:19],v[188:191],v[124:127]\n"
"v_exp_f32 v79,v79\n"
MF "v[120:123],v[16:19],v[184:187],v[120:123]\n"
"v_add_f32 v53,v58,v76\n"
"v_add_f32 v53,v53,v77\n"
MF "v[120:123],v[20:23],v[204:207],v[120:123]\n"
CV "v76,v76,v77\n"
"v_add_f32 v53,v53,v78\n"
MF "v[116:119],v[20:23],v[200:203],v[116:119]\n"
"s_waitcnt vmcnt(12) lgkmcnt(0)\n"
"v_add_f32 v53,v53,v79\n"
CV "v77,v78,v79\n"
"s_mov_b64 exec,0\n"
"s_cmp_eq_u32 s56,0\n"
"s_cbranch_scc1 .Lpg6f\n"
"s_cmp_eq_u32 s56,1\n"
"s_cbranch_scc1 .Lpg5f\n"
"s_cmp_eq_u32 s56,2\n"
"s_cbranch_scc1 .Lpg4f\n"
"s_branch .Lpgq0_f\n"
".Lisr3:\n"
"s_mov_b64 exec,-1\n"
".Lisx3:\n"
MF "v[116:119],v[16:19],v[180:183],v[116:119]\n"
"s_barrier\n"
MF "v[112:115],v[16:19],v[176:179],v[112:115]\n"
"v_exp_f32 v80,v80\n"
MF "v[112:115],v[20:23],v[196:199],v[112:115]\n"
"s_nop 0\n"
TR "v[16:17],v2 offset:24576\n"
TR "v[18:19],v2 offset:30720\n"
MF "v[112:115],v[24:27],v[216:219],v[112:115]\n"
"s_add_u32 s66,s67,4718592\n"
TR "v[20:21],v3 offset:24576\n"
TR "v[22:23],v3 offset:30720\n"
MF "v[112:115],v[28:31],v[236:239],v[112:115]\n"
"s_sub_u32 s81,s81,384\n"
"s_mov_b32 s83,7077888\n"
"s_add_u32 m0,s62,0\n"
MF "v[116:119],v[28:31],v[240:243],v[116:119]\n"
"s_add_u32 s82,s66,2359296\n"
"s_sub_u32 s15,s82,7864320\n"
"s_min_u32 s82,s82,s15\n"
MF "v[116:119],v[24:27],v[220:223],v[116:119]\n"
LD "v6,s[28:31],s82 offen lds\n"
MF "v[120:123],v[24:27],v[224:227],v[120:123]\n"
"s_add_u32 s82,s82,97280\n"
"v_exp_f32 v81,v81\n"
MF "v[120:123],v[28:31],v[244:247],v[120:123]\n"
LD "v6,s[28:31],s82 offen offset:1024 lds\n"
MF "v[124:127],v[28:31],v[248:251],v[124:127]\n"
"v_exp_f32 v82,v82\n"
MF "v[124:127],v[24:27],v[228:231],v[124:127]\n"
"v_exp_f32 v83,v83\n"
MF "v[128:131],v[24:27],v[232:235],v[128:131]\n"
"v_add_f32 v54,v59,v80\n"
"v_add_f32 v54,v54,v81\n"
MF "v[128:131],v[28:31],v[252:255],v[128:131]\n"
"s_waitcnt lgkmcnt(0)\n"
TR "v[24:25],v4 offset:24576\n"
TR "v[26:27],v4 offset:30720\n"
MF "a[0:3],v[16:19],v[92:95],a[0:3]\n"
"s_add_u32 s82,s82,97280\n"
TR "v[28:29],v5 offset:24576\n"
TR "v[30:31],v5 offset:30720\n"
MF "a[20:23],v[20:23],v[92:95],a[20:23]\n"
LD "v6,s[28:31],s82 offen offset:2048 lds\n"
MF "a[24:27],v[20:23],v[96:99],a[24:27]\n"
CV "v80,v80,v81\n"
"v_add_f32 v54,v54,v82\n"
MF "a[4:7],v[16:19],v[96:99],a[4:7]\n"
"v_add_f32 v54,v54,v83\n"
CV "v81,v82,v83\n"
MF "a[8:11],v[16:19],v[100:103],a[8:11]\n"
"s_add_u32 s82,s82,97280\n"
"v_exp_f32 v84,v84\n"
MF "a[28:31],v[20:23],v[100:103],a[28:31]\n"
LD "v6,s[28:31],s82 offen offset:3072 lds\n"
MF "a[32:35],v[20:23],v[104:107],a[32:35]\n"
"v_exp_f32 v85,v85\n"
MF "a[12:15],v[16:19],v[104:107],a[12:15]\n"
"v_exp_f32 v86,v86\n"
MF "a[16:19],v[16:19],v[108:111],a[16:19]\n"
"v_exp_f32 v87,v87\n"
MF "a[36:39],v[20:23],v[108:111],a[36:39]\n"
"s_waitcnt lgkmcnt(0)\n"
TR "v[16:17],v2 offset:25600\n"
TR "v[18:19],v2 offset:31744\n"
MF "a[56:59],v[24:27],v[108:111],a[56:59]\n"
"s_nop 0\n"
TR "v[20:21],v3 offset:25600\n"
TR "v[22:23],v3 offset:31744\n"
MF "a[76:79],v[28:31],v[108:111],a[76:79]\n"
"v_add_f32 v55,v60,v84\n"
"v_add_f32 v55,v55,v85\n"
MF "a[72:75],v[28:31],v[104:107],a[72:75]\n"
CV "v84,v84,v85\n"
"v_add_f32 v55,v55,v86\n"
MF "a[52:55],v[24:27],v[104:107],a[52:55]\n"
"v_add_f32 v55,v55,v87\n"
CV "v85,v86,v87\n"
MF "a[48:51],v[24:27],v[100:103],a[48:51]\n"
"v_exp_f32 v88,v88\n"
MF "a[68:71],v[28:31],v[100:103],a[68:71]\n"
"v_exp_f32 v89,v89\n"
MF "a[64:67],v[28:31],v[96:99],a[64:67]\n"
"v_exp_f32 v90,v90\n"
MF "a[44:47],v[24:27],v[96:99],a[44:47]\n"
"v_exp_f32 v91,v91\n"
MF "a[40:43],v[24:27],v[92:95],a[40:43]\n"
"v_add_f32 v56,v61,v88\n"
"v_add_f32 v56,v56,v89\n"
MF "a[60:63],v[28:31],v[92:95],a[60:63]\n"
"s_waitcnt lgkmcnt(0)\n"
TR "v[24:25],v4 offset:25600\n"
TR "v[26:27],v4 offset:31744\n"
MF "a[80:83],v[16:19],v[92:95],a[80:83]\n"
"s_nop 0\n"
TR "v[28:29],v5 offset:25600\n"
TR "v[30:31],v5 offset:31744\n"
MF "a[100:103],v[20:23],v[92:95],a[100:103]\n"
CV "v88,v88,v89\n"
"v_add_f32 v56,v56,v90\n"
MF "a[104:107],v[20:23],v[96:99],a[104:107]\n"
"v_add_f32 v56,v56,v91\n"
CV "v89,v90,v91\n"
MF "a[84:87],v[16:19],v[96:99],a[84:87]\n"
"v_exp_f32 v112,v112\n"
MF "a[88:91],v[16:19],v[100:103],a[88:91]\n"
"v_exp_f32 v113,v113\n"
MF "a[108:111],v[20:23],v[100:103],a[108:111]\n"
"v_exp_f32 v114,v114\n"
MF "a[112:115],v[20:23],v[104:107],a[112:115]\n"
"v_exp_f32 v115,v115\n"
MF "a[92:95],v[16:19],v[104:107],a[92:95]\n"
"v_add_f32 v52,v52,v112\n"
"v_add_f32 v52,v52,v113\n"
MF "a[96:99],v[16:19],v[108:111],a[96:99]\n"
CV "v74,v112,v113\n"
"v_add_f32 v52,v52,v114\n"
MF "a[116:119],v[20:23],v[108:111],a[116:119]\n"
"s_waitcnt lgkmcnt(0)\n"
TR "v[16:17],v2 offset:26624\n"
TR "v[18:19],v2 offset:32768\n"
MF "a[136:139],v[24:27],v[108:111],a[136:139]\n"
"s_nop 0\n"
TR "v[20:21],v3 offset:26624\n"
TR "v[22:23],v3 offset:32768\n"
MF "a[156:159],v[28:31],v[108:111],a[156:159]\n"
"v_add_f32 v52,v52,v115\n"
CV "v75,v114,v115\n"
MF "a[152:155],v[28:31],v[104:107],a[152:155]\n"
"v_exp_f32 v116,v116\n"
MF "a[132:135],v[24:27],v[104:107],a[132:135]\n"
"v_exp_f32 v117,v117\n"
MF "a[128:131],v[24:27],v[100:103],a[128:131]\n"
"v_exp_f32 v118,v118\n"
MF "a[148:151],v[28:31],v[100:103],a[148:151]\n"
"v_exp_f32 v119,v119\n"
MF "a[144:147],v[28:31],v[96:99],a[144:147]\n"
"v_add_f32 v53,v53,v116\n"
"v_add_f32 v53,v53,v117\n"
MF "a[124:127],v[24:27],v[96:99],a[124:127]\n"
CV "v78,v116,v117\n"
"v_add_f32 v53,v53,v118\n"
MF "a[120:123],v[24:27],v[92:95],a[120:123]\n"
"v_add_f32 v53,v53,v119\n"
CV "v79,v118,v119\n"
MF "a[140:143],v[28:31],v[92:95],a[140:143]\n"
"s_waitcnt lgkmcnt(0)\n"
TR "v[24:25],v4 offset:26624\n"
TR "v[26:27],v4 offset:32768\n"
MF "a[160:163],v[16:19],v[92:95],a[160:163]\n"
"s_nop 0\n"
TR "v[28:29],v5 offset:26624\n"
TR "v[30:31],v5 offset:32768\n"
MF "a[180:183],v[20:23],v[92:95],a[180:183]\n"
"v_exp_f32 v120,v120\n"
MF "a[184:187],v[20:23],v[96:99],a[184:187]\n"
"v_exp_f32 v121,v121\n"
MF "a[164:167],v[16:19],v[96:99],a[164:167]\n"
"v_exp_f32 v122,v122\n"
MF "a[168:171],v[16:19],v[100:103],a[168:171]\n"
"v_exp_f32 v123,v123\n"
MF "a[188:191],v[20:23],v[100:103],a[188:191]\n"
"v_add_f32 v54,v54,v120\n"
"v_add_f32 v54,v54,v121\n"
MF "a[192:195],v[20:23],v[104:107],a[192:195]\n"
CV "v82,v120,v121\n"
"v_add_f32 v54,v54,v122\n"
MF "a[172:175],v[16:19],v[104:107],a[172:175]\n"
"s_waitcnt vmcnt(10) lgkmcnt(0)\n"
"v_add_f32 v54,v54,v123\n"
CV "v83,v122,v123\n"
MF "a[176:179],v[16:19],v[108:111],a[176:179]\n"
"s_barrier\n"
MF "a[196:199],v[20:23],v[108:111],a[196:199]\n"
"s_add_u32 s73,s73,12288\n"
"ds_read_b128 v[16:19],v0 offset:24576\n"
"ds_read_b128 v[20:23],v1 offset:24576\n"
MF "a[216:219],v[24:27],v[108:111],a[216:219]\n"
"s_cmp_eq_u32 s73,s19\n"
"s_cselect_b32 s73,s18,s73\n"
"s_add_u32 m0,s65,73728\n"
MF "a[236:239],v[28:31],v[108:111],a[236:239]\n"
LD "v8,s[36:39],s73 offen nt lds\n"
MF "a[232:235],v[28:31],v[104:107],a[232:235]\n"
"s_add_u32 m0,s65,74624\n"
MF "a[212:215],v[24:27],v[104:107],a[212:215]\n"
LD "v8,s[36:39],s73 offen offset:128 nt lds\n"
MF "a[208:211],v[24:27],v[100:103],a[208:211]\n"
"s_add_u32 m0,s65,75520\n"
MF "a[228:231],v[28:31],v[100:103],a[228:231]\n"
LD "v8,s[36:39],s73 offen offset:256 nt lds\n"
MF "a[224:227],v[28:31],v[96:99],a[224:227]\n"
MF "a[204:207],v[24:27],v[96:99],a[204:207]\n"
MF "a[200:203],v[24:27],v[92:95],a[200:203]\n"
MF "a[220:223],v[28:31],v[92:95],a[220:223]\n"
"s_waitcnt lgkmcnt(0)\n"
MF "v[92:95],v[16:19],v[136:139],v[32:35]\n"
"s_add_u32 m0,s65,73728\n"
"ds_read_b128 v[24:27],v0 offset:25600\n"
"ds_read_b128 v[28:31],v1 offset:25600\n"
MF "v[92:95],v[20:23],v[156:159],v[92:95]\n"
LD "v8,s[36:39],s73 offen offset:3072 nt lds\n"
MF "v[96:99],v[20:23],v[160:163],v[36:39]\n"
"v_exp_f32 v124,v124\n"
MF "v[96:99],v[16:19],v[140:143],v[96:99]\n"
"v_exp_f32 v125,v125\n"
MF "v[100:103],v[16:19],v[144:147],v[40:43]\n"
"s_add_u32 m0,s65,74624\n"
"v_exp_f32 v126,v126\n"
MF "v[100:103],v[20:23],v[164:167],v[100:103]\n"
LD "v8,s[36:39],s73 offen offset:3200 nt lds\n"
MF "v[104:107],v[20:23],v[168:171],v[44:47]\n"
"v_exp_f32 v127,v127\n"
MF "v[104:107],v[16:19],v[148:151],v[104:107]\n"
"s_add_u32 m0,s65,75520\n"
"v_add_f32 v55,v55,v124\n"
"v_add_f32 v55,v55,v125\n"
MF "v[108:111],v[16:19],v[152:155],v[48:51]\n"
LD "v8,s[36:39],s73 offen offset:3328 nt lds\n"
MF "v[108:111],v[20:23],v[172:175],v[108:111]\n"
"s_waitcnt lgkmcnt(0)\n"
"ds_read_b128 v[16:19],v0 offset:26624\n"
"ds_read_b128 v[20:23],v1 offset:26624\n"
MF "v[108:111],v[24:27],v[192:195],v[108:111]\n"
CV "v86,v124,v125\n"
"v_add_f32 v55,v55,v126\n"
MF "v[108:111],v[28:31],v[212:215],v[108:111]\n"
"v_add_f32 v55,v55,v127\n"
CV "v87,v126,v127\n"
MF "v[104:107],v[28:31],v[208:211],v[104:107]\n"
"v_exp_f32 v128,v128\n"
MF "v[104:107],v[24:27],v[188:191],v[104:107]\n"
"v_exp_f32 v129,v129\n"
MF "v[100:103],v[24:27],v[184:187],v[100:103]\n"
"v_exp_f32 v130,v130\n"
MF "v[100:103],v[28:31],v[204:207],v[100:103]\n"
"v_exp_f32 v131,v131\n"
MF "v[96:99],v[28:31],v[200:203],v[96:99]\n"
"v_add_f32 v56,v56,v128\n"
"v_add_f32 v56,v56,v129\n"
MF "v[96:99],v[24:27],v[180:183],v[96:99]\n"
CV "v90,v128,v129\n"
"v_add_f32 v56,v56,v130\n"
MF "v[92:95],v[24:27],v[176:179],v[92:95]\n"
"v_add_f32 v56,v56,v131\n"
CV "v91,v130,v131\n"
MF "v[92:95],v[28:31],v[196:199],v[92:95]\n"
"s_waitcnt lgkmcnt(0)\n"
"ds_read_b128 v[24:27],v0 offset:30720\n"
"ds_read_b128 v[28:31],v1 offset:30720\n"
MF "v[92:95],v[16:19],v[216:219],v[92:95]\n"
MF "v[92:95],v[20:23],v[236:239],v[92:95]\n"
MF "v[96:99],v[20:23],v[240:243],v[96:99]\n"
MF "v[96:99],v[16:19],v[220:223],v[96:99]\n"
MF "v[100:103],v[16:19],v[224:227],v[100:103]\n"
MF "v[100:103],v[20:23],v[244:247],v[100:103]\n"
MF "v[104:107],v[20:23],v[248:251],v[104:107]\n"
MF "v[104:107],v[16:19],v[228:231],v[104:107]\n"
"v_max3_f32 v10,v52,v53,v54\n"
"v_max3_f32 v10,v10,v55,v56\n"
MF "v[108:111],v[16:19],v[232:235],v[108:111]\n"
"v_cmp_le_f32 vcc,0x47800000,v10\n"
MF "v[108:111],v[20:23],v[252:255],v[108:111]\n"
"s_waitcnt lgkmcnt(0)\n"
"s_cbranch_vccnz .Lstub_9\n"
".Lback_9:\n"
MF "v[112:115],v[24:27],v[136:139],v[32:35]\n"
"s_add_u32 s74,s74,12288\n"
"ds_read_b128 v[16:19],v0 offset:31744\n"
"ds_read_b128 v[20:23],v1 offset:31744\n"
MF "v[112:115],v[28:31],v[156:159],v[112:115]\n"
"s_cmp_eq_u32 s74,s20\n"
"s_cselect_b32 s74,s70,s74\n"
MF "v[116:119],v[28:31],v[160:163],v[36:39]\n"
"v_exp_f32 v92,v92\n"
MF "v[116:119],v[24:27],v[140:143],v[116:119]\n"
"v_exp_f32 v93,v93\n"
MF "v[120:123],v[24:27],v[144:147],v[40:43]\n"
"v_exp_f32 v94,v94\n"
MF "v[120:123],v[28:31],v[164:167],v[120:123]\n"
"v_exp_f32 v95,v95\n"
MF "v[124:127],v[28:31],v[168:171],v[44:47]\n"
"v_add_f32 v57,v52,v92\n"
"v_add_f32 v57,v57,v93\n"
MF "v[124:127],v[24:27],v[148:151],v[124:127]\n"
CV "v92,v92,v93\n"
"v_add_f32 v57,v57,v94\n"
MF "v[128:131],v[24:27],v[152:155],v[48:51]\n"
"v_add_f32 v57,v57,v95\n"
CV "v93,v94,v95\n"
MF "v[128:131],v[28:31],v[172:175],v[128:131]\n"
"s_waitcnt lgkmcnt(0)\n"
"ds_read_b128 v[24:27],v0 offset:32768\n"
"ds_read_b128 v[28:31],v1 offset:32768\n"
MF "v[128:131],v[16:19],v[192:195],v[128:131]\n"
"v_exp_f32 v96,v96\n"
MF "v[128:131],v[20:23],v[212:215],v[128:131]\n"
"v_exp_f32 v97,v97\n"
MF "v[124:127],v[20:23],v[208:211],v[124:127]\n"
"v_exp_f32 v98,v98\n"
MF "v[124:127],v[16:19],v[188:191],v[124:127]\n"
"v_exp_f32 v99,v99\n"
MF "v[120:123],v[16:19],v[184:187],v[120:123]\n"
"v_add_f32 v58,v53,v96\n"
"v_add_f32 v58,v58,v97\n"
MF "v[120:123],v[20:23],v[204:207],v[120:123]\n"
CV "v96,v96,v97\n"
"v_add_f32 v58,v58,v98\n"
MF "v[116:119],v[20:23],v[200:203],v[116:119]\n"
"s_waitcnt vmcnt(10) lgkmcnt(0)\n"
"v_add_f32 v58,v58,v99\n"
CV "v97,v98,v99\n"
MF "v[116:119],v[16:19],v[180:183],v[116:119]\n"
"s_barrier\n"
MF "v[112:115],v[16:19],v[176:179],v[112:115]\n"
"v_exp_f32 v100,v100\n"
MF "v[112:115],v[20:23],v[196:199],v[112:115]\n"
"s_nop 0\n"
TR "v[16:17],v2\n"
TR "v[18:19],v2 offset:6144\n"
MF "v[112:115],v[24:27],v[216:219],v[112:115]\n"
"s_add_u32 m0,s62,16384\n"
TR "v[20:21],v3\n"
TR "v[22:23],v3 offset:6144\n"
MF "v[112:115],v[28:31],v[236:239],v[112:115]\n"
"s_add_u32 s82,s66,2359424\n"
"s_sub_u32 s15,s82,7864320\n"
"s_min_u32 s82,s82,s15\n"
MF "v[116:119],v[28:31],v[240:243],v[116:119]\n"
LD "v6,s[28:31],s82 offen lds\n"
MF "v[116:119],v[24:27],v[220:223],v[116:119]\n"
"s_add_u32 s82,s82,97280\n"
"v_exp_f32 v101,v101\n"
MF "v[120:123],v[24:27],v[224:227],v[120:123]\n"
LD "v6,s[28:31],s82 offen offset:1024 lds\n"
MF "v[120:123],v[28:31],v[244:247],v[120:123]\n"
"v_exp_f32 v102,v102\n"
MF "v[124:127],v[28:31],v[248:251],v[124:127]\n"
"s_add_u32 s82,s82,97280\n"
"v_exp_f32 v103,v103\n"
MF "v[124:127],v[24:27],v[228:231],v[124:127]\n"
LD "v6,s[28:31],s82 offen offset:2048 lds\n"
MF "v[128:131],v[24:27],v[232:235],v[128:131]\n"
"v_add_f32 v59,v54,v100\n"
"v_add_f32 v59,v59,v101\n"
MF "v[128:131],v[28:31],v[252:255],v[128:131]\n"
"s_waitcnt lgkmcnt(0)\n"
TR "v[24:25],v4\n"
TR "v[26:27],v4 offset:6144\n"
MF "a[0:3],v[16:19],v[72:75],a[0:3]\n"
"s_add_u32 s82,s82,97280\n"
TR "v[28:29],v5\n"
TR "v[30:31],v5 offset:6144\n"
MF "a[20:23],v[20:23],v[72:75],a[20:23]\n"
LD "v6,s[28:31],s82 offen offset:3072 lds\n"
MF "a[24:27],v[20:23],v[76:79],a[24:27]\n"
"s_add_u32 m0,s62,32768\n"
CV "v100,v100,v101\n"
"v_add_f32 v59,v59,v102\n"
MF "a[4:7],v[16:19],v[76:79],a[4:7]\n"
"s_add_u32 s82,s66,2359552\n"
"s_sub_u32 s15,s82,7864320\n"
"s_min_u32 s82,s82,s15\n"
MF "a[8:11],v[16:19],v[80:83],a[8:11]\n"
".Lpg12f:\n"
"s_cbranch_execz .Lpg18f\n"
LD "v6,s[28:31],s82 offen lds\n"
MF "a[28:31],v[20:23],v[80:83],a[28:31]\n"
"v_add_f32 v59,v59,v103\n"
CV "v101,v102,v103\n"
MF "a[32:35],v[20:23],v[84:87],a[32:35]\n"
"s_add_u32 s82,s82,97280\n"
"v_exp_f32 v104,v104\n"
MF "a[12:15],v[16:19],v[84:87],a[12:15]\n"
LD "v6,s[28:31],s82 offen offset:1024 lds\n"
MF "a[16:19],v[16:19],v[88:91],a[16:19]\n"
"v_exp_f32 v105,v105\n"
MF "a[36:39],v[20:23],v[88:91],a[36:39]\n"
"s_waitcnt lgkmcnt(0)\n"
TR "v[16:17],v2 offset:1024\n"
TR "v[18:19],v2 offset:7168\n"
MF "a[56:59],v[24:27],v[88:91],a[56:59]\n"
"s_nop 0\n"
TR "v[20:21],v3 offset:1024\n"
TR "v[22:23],v3 offset:7168\n"
MF "a[76:79],v[28:31],v[88:91],a[76:79]\n"
"v_exp_f32 v106,v106\n"
MF "a[72:75],v[28:31],v[84:87],a[72:75]\n"
"v_exp_f32 v107,v107\n"
MF "a[52:55],v[24:27],v[84:87],a[52:55]\n"
"v_add_f32 v60,v55,v104\n"
"v_add_f32 v60,v60,v105\n"
MF "a[48:51],v[24:27],v[80:83],a[48:51]\n"
CV "v104,v104,v105\n"
"v_add_f32 v60,v60,v106\n"
MF "a[68:71],v[28:31],v[80:83],a[68:71]\n"
"v_add_f32 v60,v60,v107\n"
CV "v105,v106,v107\n"
MF "a[64:67],v[28:31],v[76:79],a[64:67]\n"
"v_exp_f32 v108,v108\n"
MF "a[44:47],v[24:27],v[76:79],a[44:47]\n"
"v_exp_f32 v109,v109\n"
MF "a[40:43],v[24:27],v[72:75],a[40:43]\n"
"v_exp_f32 v110,v110\n"
MF "a[60:63],v[28:31],v[72:75],a[60:63]\n"
"s_waitcnt lgkmcnt(0)\n"
TR "v[24:25],v4 offset:1024\n"
TR "v[26:27],v4 offset:7168\n"
MF "a[80:83],v[16:19],v[72:75],a[80:83]\n"
"s_nop 0\n"
TR "v[28:29],v5 offset:1024\n"
TR "v[30:31],v5 offset:7168\n"
MF "a[100:103],v[20:23],v[72:75],a[100:103]\n"
"v_exp_f32 v111,v111\n"
MF "a[104:107],v[20:23],v[76:79],a[104:107]\n"
"v_add_f32 v61,v56,v108\n"
"v_add_f32 v61,v61,v109\n"
MF "a[84:87],v[16:19],v[76:79],a[84:87]\n"
CV "v108,v108,v109\n"
"v_add_f32 v61,v61,v110\n"
MF "a[88:91],v[16:19],v[80:83],a[88:91]\n"
"v_add_f32 v61,v61,v111\n"
CV "v109,v110,v111\n"
MF "a[108:111],v[20:23],v[80:83],a[108:111]\n"
"v_exp_f32 v112,v112\n"
MF "a[112:115],v[20:23],v[84:87],a[112:115]\n"
"v_exp_f32 v113,v113\n"
MF "a[92:95],v[16:19],v[84:87],a[92:95]\n"
"v_exp_f32 v114,v114\n"
MF "a[96:99],v[16:19],v[88:91],a[96:99]\n"
"v_exp_f32 v115,v115\n"
MF "a[116:119],v[20:23],v[88:91],a[116:119]\n"
"s_waitcnt lgkmcnt(0)\n"
TR "v[16:17],v2 offset:2048\n"
TR "v[18:19],v2 offset:8192\n"
MF "a[136:139],v[24:27],v[88:91],a[136:139]\n"
"s_nop 0\n"
TR "v[20:21],v3 offset:2048\n"
TR "v[22:23],v3 offset:8192\n"
MF "a[156:159],v[28:31],v[88:91],a[156:159]\n"
"v_add_f32 v57,v57,v112\n"
"v_add_f32 v57,v57,v113\n"
MF "a[152:155],v[28:31],v[84:87],a[152:155]\n"
CV "v94,v112,v113\n"
"v_add_f32 v57,v57,v114\n"
MF "a[132:135],v[24:27],v[84:87],a[132:135]\n"
"v_add_f32 v57,v57,v115\n"
CV "v95,v114,v115\n"
MF "a[128:131],v[24:27],v[80:83],a[128:131]\n"
"v_exp_f32 v116,v116\n"
MF "a[148:151],v[28:31],v[80:83],a[148:151]\n"
"v_exp_f32 v117,v117\n"
MF "a[144:147],v[28:31],v[76:79],a[144:147]\n"
"v_exp_f32 v118,v118\n"
MF "a[124:127],v[24:27],v[76:79],a[124:127]\n"
"v_exp_f32 v119,v119\n"
MF "a[120:123],v[24:27],v[72:75],a[120:123]\n"
"v_add_f32 v58,v58,v116\n"
"v_add_f32 v58,v58,v117\n"
MF "a[140:143],v[28:31],v[72:75],a[140:143]\n"
"s_waitcnt lgkmcnt(0)\n"
TR "v[24:25],v4 offset:2048\n"
TR "v[26:27],v4 offset:8192\n"
MF "a[160:163],v[16:19],v[72:75],a[160:163]\n"
"s_nop 0\n"
TR "v[28:29],v5 offset:2048\n"
TR "v[30:31],v5 offset:8192\n"
MF "a[180:183],v[20:23],v[72:75],a[180:183]\n"
CV "v98,v116,v117\n"
"v_add_f32 v58,v58,v118\n"
MF "a[184:187],v[20:23],v[76:79],a[184:187]\n"
"v_add_f32 v58,v58,v119\n"
CV "v99,v118,v119\n"
MF "a[164:167],v[16:19],v[76:79],a[164:167]\n"
"v_exp_f32 v120,v120\n"
MF "a[168:171],v[16:19],v[80:83],a[168:171]\n"
"v_exp_f32 v121,v121\n"
MF "a[188:191],v[20:23],v[80:83],a[188:191]\n"
"v_exp_f32 v122,v122\n"
MF "a[192:195],v[20:23],v[84:87],a[192:195]\n"
"v_exp_f32 v123,v123\n"
MF "a[172:175],v[16:19],v[84:87],a[172:175]\n"
"v_add_f32 v59,v59,v120\n"
"v_add_f32 v59,v59,v121\n"
MF "a[176:179],v[16:19],v[88:91],a[176:179]\n"
"s_waitcnt lgkmcnt(0)\n"
CV "v102,v120,v121\n"
"v_add_f32 v59,v59,v122\n"
MF "a[196:199],v[20:23],v[88:91],a[196:199]\n"
"s_add_u32 s82,s82,97280\n"
"v_add_f32 v59,v59,v123\n"
CV "v103,v122,v123\n"
MF "a[216:219],v[24:27],v[88:91],a[216:219]\n"
LD "v6,s[28:31],s82 offen offset:2048 lds\n"
MF "a[236:239],v[28:31],v[88:91],a[236:239]\n"
"v_exp_f32 v124,v124\n"
MF "a[232:235],v[28:31],v[84:87],a[232:235]\n"
"s_add_u32 s82,s82,97280\n"
"v_exp_f32 v125,v125\n"
MF "a[212:215],v[24:27],v[84:87],a[212:215]\n"
LD "v6,s[28:31],s82 offen offset:3072 lds\n"
MF "a[208:211],v[24:27],v[80:83],a[208:211]\n"
"v_exp_f32 v126,v126\n"
MF "a[228:231],v[28:31],v[80:83],a[228:231]\n"
"v_exp_f32 v127,v127\n"
MF "a[224:227],v[28:31],v[76:79],a[224:227]\n"
"v_add_f32 v60,v60,v124\n"
"v_add_f32 v60,v60,v125\n"
MF "a[204:207],v[24:27],v[76:79],a[204:207]\n"
CV "v106,v124,v125\n"
"v_add_f32 v60,v60,v126\n"
MF "a[200:203],v[24:27],v[72:75],a[200:203]\n"
"v_add_f32 v60,v60,v127\n"
CV "v107,v126,v127\n"
MF "a[220:223],v[28:31],v[72:75],a[220:223]\n"
"v_exp_f32 v128,v128\n"
"v_exp_f32 v129,v129\n"
"v_exp_f32 v130,v130\n"
"v_exp_f32 v131,v131\n"
"v_add_f32 v61,v61,v128\n"
"v_add_f32 v61,v61,v129\n"
CV "v110,v128,v129\n"
"v_add_f32 v61,v61,v130\n"
"v_add_f32_e64 v61,v61,v131\n"
CV "v111,v130,v131\n"
"v_max3_f32 v10,v57,v58,v59\n"
"v_max3_f32 v10,v10,v60,v61\n"
"v_cmp_le_f32 vcc,0x47800000,v10\n"
"s_cbranch_vccnz .Lstub_10\n"
".Lback_10:\n"
"s_waitcnt vmcnt(8) lgkmcnt(0)\n"
"s_barrier\n"
"v_add_u32 v11,97280,v6\n"
"v_add_u32 v12,194560,v6\n"
"v_add_u32 v13,291840,v6\n"
"v_mov_b32 v10,1.0\n"
TR "v[136:137],v2 offset:24576\n"
TR "v[138:139],v2 offset:30720\n"
"v_mfma_f32_16x16x4_f32 v[16:19],v10,v57,0\n"
TR "v[140:141],v3 offset:24576\n"
TR "v[142:143],v3 offset:30720\n"
TR "v[144:145],v4 offset:24576\n"
TR "v[146:147],v4 offset:30720\n"
TR "v[148:149],v5 offset:24576\n"
TR "v[150:151],v5 offset:30720\n"
"v_mfma_f32_16x16x4_f32 v[20:23],v10,v58,0\n"
TR "v[152:153],v2 offset:25600\n"
TR "v[154:155],v2 offset:31744\n"
TR "v[156:157],v3 offset:25600\n"
TR "v[158:159],v3 offset:31744\n"
TR "v[160:161],v4 offset:25600\n"
TR "v[162:163],v4 offset:31744\n"
"v_mfma_f32_16x16x4_f32 v[24:27],v10,v59,0\n"
TR "v[164:165],v5 offset:25600\n"
TR "v[166:167],v5 offset:31744\n"
TR "v[168:169],v2 offset:26624\n"
TR "v[170:171],v2 offset:32768\n"
TR "v[172:173],v3 offset:26624\n"
TR "v[174:175],v3 offset:32768\n"
"v_mfma_f32_16x16x4_f32 v[28:31],v10,v60,0\n"
TR "v[176:177],v4 offset:26624\n"
TR "v[178:179],v4 offset:32768\n"
TR "v[180:181],v5 offset:26624\n"
TR "v[182:183],v5 offset:32768\n"
"v_mfma_f32_16x16x4_f32 v[32:35],v10,v61,0\n"
"s_waitcnt lgkmcnt(15)\n"
MF "a[0:3],v[136:139],v[92:95],a[0:3]\n"
MF "a[20:23],v[140:143],v[92:95],a[20:23]\n"
"v_rcp_f32 v57,v16\n"
"v_rcp_f32 v58,v20\n"
"v_rcp_f32 v59,v24\n"
"v_rcp_f32 v60,v28\n"
"s_nop 2\n"
"v_rcp_f32_e64 v61,v32\n"
MF "a[4:7],v[136:139],v[96:99],a[4:7]\n"
"v_mul_f32_e64 v52,-s13,v57\n"
"v_mul_f32_e64 v53,-s13,v58\n"
"v_mul_f32_e64 v54,-s13,v59\n"
"v_mul_f32_e64 v55,-s13,v60\n"
"v_mul_f32_e64 v56,-s13,v61\n"
AR "v72,a0\n"
AR "v73,a1\n"
AR "v74,a2\n"
MF "a[24:27],v[140:143],v[96:99],a[24:27]\n"
AR "v75,a3\n"
AR "v76,a20\n"
AR "v77,a21\n"
AR "v78,a22\n"
AR "v79,a23\n"
"v_pk_fma_f32 v[72:73],v[72:73],v[56:57],v[52:53] op_sel:[0,1,0] op_sel_hi:[1,1,0]\n"
"v_pk_fma_f32 v[74:75],v[74:75],v[56:57],v[52:53] op_sel:[0,1,0] op_sel_hi:[1,1,0]\n"
"v_pk_fma_f32 v[76:77],v[76:77],v[56:57],v[52:53] op_sel:[0,1,0] op_sel_hi:[1,1,0]\n"
"v_pk_fma_f32 v[78:79],v[78:79],v[56:57],v[52:53] op_sel:[0,1,0] op_sel_hi:[1,1,0]\n"
CV "v112,v72,v73\n"
CV "v113,v74,v75\n"
CV "v114,v76,v77\n"
CV "v115,v78,v79\n"
MF "a[8:11],v[136:139],v[100:103],a[8:11]\n"
AW "a0,v112\n"
AW "a1,v113\n"
AW "a2,v114\n"
AW "a3,v115\n"
AR "v80,a4\n"
AR "v81,a5\n"
AR "v82,a6\n"
MF "a[28:31],v[140:143],v[100:103],a[28:31]\n"
AR "v83,a7\n"
AR "v84,a24\n"
AR "v85,a25\n"
AR "v86,a26\n"
AR "v87,a27\n"
"v_pk_fma_f32 v[80:81],v[80:81],v[58:59],v[52:53] op_sel:[0,0,1] op_sel_hi:[1,0,1]\n"
"v_pk_fma_f32 v[82:83],v[82:83],v[58:59],v[52:53] op_sel:[0,0,1] op_sel_hi:[1,0,1]\n"
"v_pk_fma_f32 v[84:85],v[84:85],v[58:59],v[52:53] op_sel:[0,0,1] op_sel_hi:[1,0,1]\n"
"v_pk_fma_f32 v[86:87],v[86:87],v[58:59],v[52:53] op_sel:[0,0,1] op_sel_hi:[1,0,1]\n"
CV "v116,v80,v81\n"
CV "v117,v82,v83\n"
CV "v118,v84,v85\n"
CV "v119,v86,v87\n"
MF "a[12:15],v[136:139],v[104:107],a[12:15]\n"
AW "a4,v116\n"
AW "a5,v117\n"
AW "a6,v118\n"
AW "a7,v119\n"
AR "v72,a8\n"
AR "v73,a9\n"
AR "v74,a10\n"
MF "a[32:35],v[140:143],v[104:107],a[32:35]\n"
AR "v75,a11\n"
AR "v76,a28\n"
AR "v77,a29\n"
AR "v78,a30\n"
AR "v79,a31\n"
"v_pk_fma_f32 v[72:73],v[72:73],v[58:59],v[54:55] op_sel:[0,1,0] op_sel_hi:[1,1,0]\n"
"v_pk_fma_f32 v[74:75],v[74:75],v[58:59],v[54:55] op_sel:[0,1,0] op_sel_hi:[1,1,0]\n"
"v_pk_fma_f32 v[76:77],v[76:77],v[58:59],v[54:55] op_sel:[0,1,0] op_sel_hi:[1,1,0]\n"
"v_pk_fma_f32 v[78:79],v[78:79],v[58:59],v[54:55] op_sel:[0,1,0] op_sel_hi:[1,1,0]\n"
CV "v112,v72,v73\n"
CV "v113,v74,v75\n"
CV "v114,v76,v77\n"
CV "v115,v78,v79\n"
MF "a[16:19],v[136:139],v[108:111],a[16:19]\n"
AW "a8,v112\n"
AW "a9,v113\n"
AW "a10,v114\n"
AW "a11,v115\n"
AR "v80,a12\n"
AR "v81,a13\n"
AR "v82,a14\n"
MF "a[36:39],v[140:143],v[108:111],a[36:39]\n"
AR "v83,a15\n"
AR "v84,a32\n"
AR "v85,a33\n"
AR "v86,a34\n"
AR "v87,a35\n"
"v_pk_fma_f32 v[80:81],v[80:81],v[60:61],v[54:55] op_sel:[0,0,1] op_sel_hi:[1,0,1]\n"
"v_pk_fma_f32 v[82:83],v[82:83],v[60:61],v[54:55] op_sel:[0,0,1] op_sel_hi:[1,0,1]\n"
"v_pk_fma_f32 v[84:85],v[84:85],v[60:61],v[54:55] op_sel:[0,0,1] op_sel_hi:[1,0,1]\n"
"v_pk_fma_f32 v[86:87],v[86:87],v[60:61],v[54:55] op_sel:[0,0,1] op_sel_hi:[1,0,1]\n"
CV "v116,v80,v81\n"
CV "v117,v82,v83\n"
CV "v118,v84,v85\n"
CV "v119,v86,v87\n"
MF "a[40:43],v[144:147],v[92:95],a[40:43]\n"
AW "a12,v116\n"
AW "a13,v117\n"
AW "a14,v118\n"
AW "a15,v119\n"
AR "v72,a16\n"
AR "v73,a17\n"
AR "v74,a18\n"
MF "a[60:63],v[148:151],v[92:95],a[60:63]\n"
AR "v75,a19\n"
AR "v76,a36\n"
AR "v77,a37\n"
AR "v78,a38\n"
AR "v79,a39\n"
"v_pk_fma_f32 v[72:73],v[72:73],v[60:61],v[56:57] op_sel:[0,1,0] op_sel_hi:[1,1,0]\n"
"v_pk_fma_f32 v[74:75],v[74:75],v[60:61],v[56:57] op_sel:[0,1,0] op_sel_hi:[1,1,0]\n"
"v_pk_fma_f32 v[76:77],v[76:77],v[60:61],v[56:57] op_sel:[0,1,0] op_sel_hi:[1,1,0]\n"
"v_pk_fma_f32 v[78:79],v[78:79],v[60:61],v[56:57] op_sel:[0,1,0] op_sel_hi:[1,1,0]\n"
CV "v112,v72,v73\n"
CV "v113,v74,v75\n"
CV "v114,v76,v77\n"
CV "v115,v78,v79\n"
MF "a[44:47],v[144:147],v[96:99],a[44:47]\n"
AW "a16,v112\n"
AW "a17,v113\n"
AW "a18,v114\n"
AW "a19,v115\n"
AR "v80,a40\n"
AR "v81,a41\n"
AR "v82,a42\n"
MF "a[64:67],v[148:151],v[96:99],a[64:67]\n"
AR "v83,a43\n"
AR "v84,a60\n"
AR "v85,a61\n"
AR "v86,a62\n"
AR "v87,a63\n"
"v_pk_fma_f32 v[80:81],v[80:81],v[56:57],v[52:53] op_sel:[0,1,0] op_sel_hi:[1,1,0]\n"
"v_pk_fma_f32 v[82:83],v[82:83],v[56:57],v[52:53] op_sel:[0,1,0] op_sel_hi:[1,1,0]\n"
"v_pk_fma_f32 v[84:85],v[84:85],v[56:57],v[52:53] op_sel:[0,1,0] op_sel_hi:[1,1,0]\n"
"v_pk_fma_f32 v[86:87],v[86:87],v[56:57],v[52:53] op_sel:[0,1,0] op_sel_hi:[1,1,0]\n"
CV "v116,v80,v81\n"
CV "v117,v82,v83\n"
CV "v118,v84,v85\n"
CV "v119,v86,v87\n"
MF "a[48:51],v[144:147],v[100:103],a[48:51]\n"
AW "a40,v116\n"
AW "a41,v117\n"
AW "a42,v118\n"
AW "a43,v119\n"
AR "v72,a44\n"
AR "v73,a45\n"
AR "v74,a46\n"
MF "a[68:71],v[148:151],v[100:103],a[68:71]\n"
AR "v75,a47\n"
AR "v76,a64\n"
AR "v77,a65\n"
AR "v78,a66\n"
AR "v79,a67\n"
"v_pk_fma_f32 v[72:73],v[72:73],v[58:59],v[52:53] op_sel:[0,0,1] op_sel_hi:[1,0,1]\n"
"v_pk_fma_f32 v[74:75],v[74:75],v[58:59],v[52:53] op_sel:[0,0,1] op_sel_hi:[1,0,1]\n"
"v_pk_fma_f32 v[76:77],v[76:77],v[58:59],v[52:53] op_sel:[0,0,1] op_sel_hi:[1,0,1]\n"
"v_pk_fma_f32 v[78:79],v[78:79],v[58:59],v[52:53] op_sel:[0,0,1] op_sel_hi:[1,0,1]\n"
CV "v112,v72,v73\n"
CV "v113,v74,v75\n"
CV "v114,v76,v77\n"
CV "v115,v78,v79\n"
MF "a[52:55],v[144:147],v[104:107],a[52:55]\n"
AW "a44,v112\n"
AW "a45,v113\n"
AW "a46,v114\n"
AW "a47,v115\n"
AR "v80,a48\n"
AR "v81,a49\n"
AR "v82,a50\n"
MF "a[72:75],v[148:151],v[104:107],a[72:75]\n"
AR "v83,a51\n"
AR "v84,a68\n"
AR "v85,a69\n"
AR "v86,a70\n"
AR "v87,a71\n"
"v_pk_fma_f32 v[80:81],v[80:81],v[58:59],v[54:55] op_sel:[0,1,0] op_sel_hi:[1,1,0]\n"
"v_pk_fma_f32 v[82:83],v[82:83],v[58:59],v[54:55] op_sel:[0,1,0] op_sel_hi:[1,1,0]\n"
"v_pk_fma_f32 v[84:85],v[84:85],v[58:59],v[54:55] op_sel:[0,1,0] op_sel_hi:[1,1,0]\n"
"v_pk_fma_f32 v[86:87],v[86:87],v[58:59],v[54:55] op_sel:[0,1,0] op_sel_hi:[1,1,0]\n"
CV "v116,v80,v81\n"
CV "v117,v82,v83\n"
CV "v118,v84,v85\n"
CV "v119,v86,v87\n"
MF "a[56:59],v[144:147],v[108:111],a[56:59]\n"
AW "a48,v116\n"
AW "a49,v117\n"
AW "a50,v118\n"
AW "a51,v119\n"
AR "v72,a52\n"
AR "v73,a53\n"
AR "v74,a54\n"
MF "a[76:79],v[148:151],v[108:111],a[76:79]\n"
AR "v75,a55\n"
AR "v76,a72\n"
AR "v77,a73\n"
AR "v78,a74\n"
AR "v79,a75\n"
"v_pk_fma_f32 v[72:73],v[72:73],v[60:61],v[54:55] op_sel:[0,0,1] op_sel_hi:[1,0,1]\n"
"v_pk_fma_f32 v[74:75],v[74:75],v[60:61],v[54:55] op_sel:[0,0,1] op_sel_hi:[1,0,1]\n"
"v_pk_fma_f32 v[76:77],v[76:77],v[60:61],v[54:55] op_sel:[0,0,1] op_sel_hi:[1,0,1]\n"
"v_pk_fma_f32 v[78:79],v[78:79],v[60:61],v[54:55] op_sel:[0,0,1] op_sel_hi:[1,0,1]\n"
CV "v112,v72,v73\n"
CV "v113,v74,v75\n"
CV "v114,v76,v77\n"
CV "v115,v78,v79\n"
"s_waitcnt lgkmcnt(14)\n"
MF "a[80:83],v[152:155],v[92:95],a[80:83]\n"
AW "a52,v112\n"
AW "a53,v113\n"
AW "a54,v114\n"
AW "a55,v115\n"
AR "v80,a56\n"
AR "v81,a57\n"
AR "v82,a58\n"
"s_waitcnt lgkmcnt(12)\n"
MF "a[100:103],v[156:159],v[92:95],a[100:103]\n"
AR "v83,a59\n"
AR "v84,a76\n"
AR "v85,a77\n"
AR "v86,a78\n"
AR "v87,a79\n"
"v_pk_fma_f32 v[80:81],v[80:81],v[60:61],v[56:57] op_sel:[0,1,0] op_sel_hi:[1,1,0]\n"
"v_pk_fma_f32 v[82:83],v[82:83],v[60:61],v[56:57] op_sel:[0,1,0] op_sel_hi:[1,1,0]\n"
"v_pk_fma_f32 v[84:85],v[84:85],v[60:61],v[56:57] op_sel:[0,1,0] op_sel_hi:[1,1,0]\n"
"v_pk_fma_f32 v[86:87],v[86:87],v[60:61],v[56:57] op_sel:[0,1,0] op_sel_hi:[1,1,0]\n"
CV "v116,v80,v81\n"
CV "v117,v82,v83\n"
CV "v118,v84,v85\n"
CV "v119,v86,v87\n"
MF "a[84:87],v[152:155],v[96:99],a[84:87]\n"
AW "a56,v116\n"
AW "a57,v117\n"
AW "a58,v118\n"
AW "a59,v119\n"
AR "v72,a80\n"
AR "v73,a81\n"
AR "v74,a82\n"
MF "a[104:107],v[156:159],v[96:99],a[104:107]\n"
AR "v75,a83\n"
AR "v76,a100\n"
AR "v77,a101\n"
AR "v78,a102\n"
AR "v79,a103\n"
"v_pk_fma_f32 v[72:73],v[72:73],v[56:57],v[52:53] op_sel:[0,1,0] op_sel_hi:[1,1,0]\n"
"v_pk_fma_f32 v[74:75],v[74:75],v[56:57],v[52:53] op_sel:[0,1,0] op_sel_hi:[1,1,0]\n"
"v_pk_fma_f32 v[76:77],v[76:77],v[56:57],v[52:53] op_sel:[0,1,0] op_sel_hi:[1,1,0]\n"
"v_pk_fma_f32 v[78:79],v[78:79],v[56:57],v[52:53] op_sel:[0,1,0] op_sel_hi:[1,1,0]\n"
CV "v112,v72,v73\n"
CV "v113,v74,v75\n"
CV "v114,v76,v77\n"
CV "v115,v78,v79\n"
MF "a[88:91],v[152:155],v[100:103],a[88:91]\n"
AW "a80,v112\n"
AW "a81,v113\n"
AW "a82,v114\n"
AW "a83,v115\n"
AR "v80,a84\n"
AR "v81,a85\n"
AR "v82,a86\n"
MF "a[108:111],v[156:159],v[100:103],a[108:111]\n"
AR "v83,a87\n"
AR "v84,a104\n"
AR "v85,a105\n"
AR "v86,a106\n"
AR "v87,a107\n"
"v_pk_fma_f32 v[80:81],v[80:81],v[58:59],v[52:53] op_sel:[0,0,1] op_sel_hi:[1,0,1]\n"
"v_pk_fma_f32 v[82:83],v[82:83],v[58:59],v[52:53] op_sel:[0,0,1] op_sel_hi:[1,0,1]\n"
"v_pk_fma_f32 v[84:85],v[84:85],v[58:59],v[52:53] op_sel:[0,0,1] op_sel_hi:[1,0,1]\n"
"v_pk_fma_f32 v[86:87],v[86:87],v[58:59],v[52:53] op_sel:[0,0,1] op_sel_hi:[1,0,1]\n"
CV "v116,v80,v81\n"
CV "v117,v82,v83\n"
CV "v118,v84,v85\n"
CV "v119,v86,v87\n"
MF "a[92:95],v[152:155],v[104:107],a[92:95]\n"
AW "a84,v116\n"
AW "a85,v117\n"
AW "a86,v118\n"
AW "a87,v119\n"
AR "v72,a88\n"
AR "v73,a89\n"
AR "v74,a90\n"
MF "a[112:115],v[156:159],v[104:107],a[112:115]\n"
AR "v75,a91\n"
AR "v76,a108\n"
AR "v77,a109\n"
AR "v78,a110\n"
AR "v79,a111\n"
"v_pk_fma_f32 v[72:73],v[72:73],v[58:59],v[54:55] op_sel:[0,1,0] op_sel_hi:[1,1,0]\n"
"v_pk_fma_f32 v[74:75],v[74:75],v[58:59],v[54:55] op_sel:[0,1,0] op_sel_hi:[1,1,0]\n"
"v_pk_fma_f32 v[76:77],v[76:77],v[58:59],v[54:55] op_sel:[0,1,0] op_sel_hi:[1,1,0]\n"
"v_pk_fma_f32 v[78:79],v[78:79],v[58:59],v[54:55] op_sel:[0,1,0] op_sel_hi:[1,1,0]\n"
CV "v112,v72,v73\n"
CV "v113,v74,v75\n"
CV "v114,v76,v77\n"
CV "v115,v78,v79\n"
MF "a[96:99],v[152:155],v[108:111],a[96:99]\n"
AW "a88,v112\n"
AW "a89,v113\n"
AW "a90,v114\n"
AW "a91,v115\n"
AR "v80,a92\n"
AR "v81,a93\n"
AR "v82,a94\n"
MF "a[116:119],v[156:159],v[108:111],a[116:119]\n"
AR "v83,a95\n"
AR "v84,a112\n"
AR "v85,a113\n"
AR "v86,a114\n"
AR "v87,a115\n"
"v_pk_fma_f32 v[80:81],v[80:81],v[60:61],v[54:55] op_sel:[0,0,1] op_sel_hi:[1,0,1]\n"
"v_pk_fma_f32 v[82:83],v[82:83],v[60:61],v[54:55] op_sel:[0,0,1] op_sel_hi:[1,0,1]\n"
"v_pk_fma_f32 v[84:85],v[84:85],v[60:61],v[54:55] op_sel:[0,0,1] op_sel_hi:[1,0,1]\n"
"v_pk_fma_f32 v[86:87],v[86:87],v[60:61],v[54:55] op_sel:[0,0,1] op_sel_hi:[1,0,1]\n"
CV "v116,v80,v81\n"
CV "v117,v82,v83\n"
CV "v118,v84,v85\n"
CV "v119,v86,v87\n"
"s_waitcnt lgkmcnt(10)\n"
MF "a[120:123],v[160:163],v[92:95],a[120:123]\n"
AW "a92,v116\n"
AW "a93,v117\n"
AW "a94,v118\n"
AW "a95,v119\n"
AR "v72,a96\n"
AR "v73,a97\n"
AR "v74,a98\n"
"s_waitcnt lgkmcnt(8)\n"
MF "a[140:143],v[164:167],v[92:95],a[140:143]\n"
AR "v75,a99\n"
AR "v76,a116\n"
AR "v77,a117\n"
AR "v78,a118\n"
AR "v79,a119\n"
"v_pk_fma_f32 v[72:73],v[72:73],v[60:61],v[56:57] op_sel:[0,1,0] op_sel_hi:[1,1,0]\n"
"v_pk_fma_f32 v[74:75],v[74:75],v[60:61],v[56:57] op_sel:[0,1,0] op_sel_hi:[1,1,0]\n"
"v_pk_fma_f32 v[76:77],v[76:77],v[60:61],v[56:57] op_sel:[0,1,0] op_sel_hi:[1,1,0]\n"
"v_pk_fma_f32 v[78:79],v[78:79],v[60:61],v[56:57] op_sel:[0,1,0] op_sel_hi:[1,1,0]\n"
CV "v112,v72,v73\n"
CV "v113,v74,v75\n"
CV "v114,v76,v77\n"
CV "v115,v78,v79\n"
MF "a[124:127],v[160:163],v[96:99],a[124:127]\n"
AW "a96,v112\n"
AW "a97,v113\n"
AW "a98,v114\n"
AW "a99,v115\n"
AR "v0,a240\n"
AR "v2,a241\n"
AR "v4,a242\n"
".Lpg13f:\n"
"s_cbranch_execz .Lisr3\n"
"s_nop 0\n"
"v_xor_b32 v1,64,v0\n"
"v_xor_b32 v3,64,v2\n"
"v_xor_b32_e64 v5,64,v4\n"
AR "v80,a120\n"
AR "v81,a121\n"
AR "v82,a122\n"
MF "a[144:147],v[164:167],v[96:99],a[144:147]\n"
AR "v83,a123\n"
AR "v84,a140\n"
AR "v85,a141\n"
AR "v86,a142\n"
AR "v87,a143\n"
"v_pk_fma_f32 v[80:81],v[80:81],v[56:57],v[52:53] op_sel:[0,1,0] op_sel_hi:[1,1,0]\n"
"v_pk_fma_f32 v[82:83],v[82:83],v[56:57],v[52:53] op_sel:[0,1,0] op_sel_hi:[1,1,0]\n"
"v_pk_fma_f32 v[84:85],v[84:85],v[56:57],v[52:53] op_sel:[0,1,0] op_sel_hi:[1,1,0]\n"
"v_pk_fma_f32 v[86:87],v[86:87],v[56:57],v[52:53] op_sel:[0,1,0] op_sel_hi:[1,1,0]\n"
CV "v116,v80,v81\n"
CV "v117,v82,v83\n"
CV "v118,v84,v85\n"
CV "v119,v86,v87\n"
MF "a[128:131],v[160:163],v[100:103],a[128:131]\n"
AW "a120,v116\n"
AW "a121,v117\n"
AW "a122,v118\n"
AW "a123,v119\n"
AR "v72,a124\n"
AR "v73,a125\n"
AR "v74,a126\n"
MF "a[148:151],v[164:167],v[100:103],a[148:151]\n"
AR "v75,a127\n"
AR "v76,a144\n"
AR "v77,a145\n"
AR "v78,a146\n"
AR "v79,a147\n"
"v_pk_fma_f32 v[72:73],v[72:73],v[58:59],v[52:53] op_sel:[0,0,1] op_sel_hi:[1,0,1]\n"
"v_pk_fma_f32 v[74:75],v[74:75],v[58:59],v[52:53] op_sel:[0,0,1] op_sel_hi:[1,0,1]\n"
"v_pk_fma_f32 v[76:77],v[76:77],v[58:59],v[52:53] op_sel:[0,0,1] op_sel_hi:[1,0,1]\n"
"v_pk_fma_f32 v[78:79],v[78:79],v[58:59],v[52:53] op_sel:[0,0,1] op_sel_hi:[1,0,1]\n"
CV "v112,v72,v73\n"
CV "v113,v74,v75\n"
CV "v114,v76,v77\n"
CV "v115,v78,v79\n"
MF "a[132:135],v[160:163],v[104:107],a[132:135]\n"
"ds_read_b128 a[60:63],v2\n"
AW "a124,v112\n"
AW "a125,v113\n"
AW "a126,v114\n"
AW "a127,v115\n"
AR "v80,a128\n"
AR "v81,a129\n"
AR "v82,a130\n"
MF "a[152:155],v[164:167],v[104:107],a[152:155]\n"
AR "v83,a131\n"
AR "v84,a148\n"
AR "v85,a149\n"
AR "v86,a150\n"
AR "v87,a151\n"
"v_pk_fma_f32 v[80:81],v[80:81],v[58:59],v[54:55] op_sel:[0,1,0] op_sel_hi:[1,1,0]\n"
"v_pk_fma_f32 v[82:83],v[82:83],v[58:59],v[54:55] op_sel:[0,1,0] op_sel_hi:[1,1,0]\n"
"v_pk_fma_f32 v[84:85],v[84:85],v[58:59],v[54:55] op_sel:[0,1,0] op_sel_hi:[1,1,0]\n"
"v_pk_fma_f32 v[86:87],v[86:87],v[58:59],v[54:55] op_sel:[0,1,0] op_sel_hi:[1,1,0]\n"
CV "v116,v80,v81\n"
CV "v117,v82,v83\n"
CV "v118,v84,v85\n"
CV "v119,v86,v87\n"
MF "a[136:139],v[160:163],v[108:111],a[136:139]\n"
"ds_read_b128 a[100:103],v3\n"
AW "a128,v116\n"
AW "a129,v117\n"
AW "a130,v118\n"
AW "a131,v119\n"
AR "v72,a132\n"
AR "v73,a133\n"
AR "v74,a134\n"
MF "a[156:159],v[164:167],v[108:111],a[156:159]\n"
AR "v75,a135\n"
AR "v76,a152\n"
AR "v77,a153\n"
AR "v78,a154\n"
AR "v79,a155\n"
"v_pk_fma_f32 v[72:73],v[72:73],v[60:61],v[54:55] op_sel:[0,0,1] op_sel_hi:[1,0,1]\n"
"v_pk_fma_f32 v[74:75],v[74:75],v[60:61],v[54:55] op_sel:[0,0,1] op_sel_hi:[1,0,1]\n"
"v_pk_fma_f32 v[76:77],v[76:77],v[60:61],v[54:55] op_sel:[0,0,1] op_sel_hi:[1,0,1]\n"
"v_pk_fma_f32 v[78:79],v[78:79],v[60:61],v[54:55] op_sel:[0,0,1] op_sel_hi:[1,0,1]\n"
CV "v112,v72,v73\n"
CV "v113,v74,v75\n"
CV "v114,v76,v77\n"
CV "v115,v78,v79\n"
"s_waitcnt lgkmcnt(8)\n"
MF "a[160:163],v[168:171],v[92:95],a[160:163]\n"
"ds_read_b128 a[64:67],v2 offset:2048\n"
AW "a132,v112\n"
AW "a133,v113\n"
AW "a134,v114\n"
AW "a135,v115\n"
AR "v80,a136\n"
AR "v81,a137\n"
AR "v82,a138\n"
"s_waitcnt lgkmcnt(7)\n"
MF "a[180:183],v[172:175],v[92:95],a[180:183]\n"
AR "v83,a139\n"
AR "v84,a156\n"
AR "v85,a157\n"
AR "v86,a158\n"
AR "v87,a159\n"
"v_pk_fma_f32 v[80:81],v[80:81],v[60:61],v[56:57] op_sel:[0,1,0] op_sel_hi:[1,1,0]\n"
"v_pk_fma_f32 v[82:83],v[82:83],v[60:61],v[56:57] op_sel:[0,1,0] op_sel_hi:[1,1,0]\n"
"v_pk_fma_f32 v[84:85],v[84:85],v[60:61],v[56:57] op_sel:[0,1,0] op_sel_hi:[1,1,0]\n"
"v_pk_fma_f32 v[86:87],v[86:87],v[60:61],v[56:57] op_sel:[0,1,0] op_sel_hi:[1,1,0]\n"
CV "v116,v80,v81\n"
CV "v117,v82,v83\n"
CV "v118,v84,v85\n"
CV "v119,v86,v87\n"
MF "a[164:167],v[168:171],v[96:99],a[164:167]\n"
"ds_read_b128 a[104:107],v3 offset:2048\n"
AW "a136,v116\n"
AW "a137,v117\n"
AW "a138,v118\n"
AW "a139,v119\n"
AR "v72,a160\n"
AR "v73,a161\n"
AR "v74,a162\n"
MF "a[184:187],v[172:175],v[96:99],a[184:187]\n"
AR "v75,a163\n"
AR "v76,a180\n"
AR "v77,a181\n"
AR "v78,a182\n"
AR "v79,a183\n"
"v_pk_fma_f32 v[72:73],v[72:73],v[56:57],v[52:53] op_sel:[0,1,0] op_sel_hi:[1,1,0]\n"
"v_pk_fma_f32 v[74:75],v[74:75],v[56:57],v[52:53] op_sel:[0,1,0] op_sel_hi:[1,1,0]\n"
"v_pk_fma_f32 v[76:77],v[76:77],v[56:57],v[52:53] op_sel:[0,1,0] op_sel_hi:[1,1,0]\n"
"v_pk_fma_f32 v[78:79],v[78:79],v[56:57],v[52:53] op_sel:[0,1,0] op_sel_hi:[1,1,0]\n"
CV "v112,v72,v73\n"
CV "v113,v74,v75\n"
CV "v114,v76,v77\n"
CV "v115,v78,v79\n"
MF "a[168:171],v[168:171],v[100:103],a[168:171]\n"
"ds_read_b128 a[68:71],v2 offset:4096\n"
AW "a160,v112\n"
AW "a161,v113\n"
AW "a162,v114\n"
AW "a163,v115\n"
AR "v80,a164\n"
AR "v81,a165\n"
AR "v82,a166\n"
MF "a[188:191],v[172:175],v[100:103],a[188:191]\n"
AR "v83,a167\n"
AR "v84,a184\n"
AR "v85,a185\n"
AR "v86,a186\n"
AR "v87,a187\n"
"v_pk_fma_f32 v[80:81],v[80:81],v[58:59],v[52:53] op_sel:[0,0,1] op_sel_hi:[1,0,1]\n"
"v_pk_fma_f32 v[82:83],v[82:83],v[58:59],v[52:53] op_sel:[0,0,1] op_sel_hi:[1,0,1]\n"
"v_pk_fma_f32 v[84:85],v[84:85],v[58:59],v[52:53] op_sel:[0,0,1] op_sel_hi:[1,0,1]\n"
"v_pk_fma_f32 v[86:87],v[86:87],v[58:59],v[52:53] op_sel:[0,0,1] op_sel_hi:[1,0,1]\n"
CV "v116,v80,v81\n"
CV "v117,v82,v83\n"
CV "v118,v84,v85\n"
CV "v119,v86,v87\n"
MF "a[172:175],v[168:171],v[104:107],a[172:175]\n"
"ds_read_b128 a[108:111],v3 offset:4096\n"
AW "a164,v116\n"
AW "a165,v117\n"
AW "a166,v118\n"
AW "a167,v119\n"
AR "v72,a168\n"
AR "v73,a169\n"
AR "v74,a170\n"
MF "a[192:195],v[172:175],v[104:107],a[192:195]\n"
AR "v75,a171\n"
AR "v76,a188\n"
AR "v77,a189\n"
AR "v78,a190\n"
AR "v79,a191\n"
"v_pk_fma_f32 v[72:73],v[72:73],v[58:59],v[54:55] op_sel:[0,1,0] op_sel_hi:[1,1,0]\n"
"v_pk_fma_f32 v[74:75],v[74:75],v[58:59],v[54:55] op_sel:[0,1,0] op_sel_hi:[1,1,0]\n"
"v_pk_fma_f32 v[76:77],v[76:77],v[58:59],v[54:55] op_sel:[0,1,0] op_sel_hi:[1,1,0]\n"
"v_pk_fma_f32 v[78:79],v[78:79],v[58:59],v[54:55] op_sel:[0,1,0] op_sel_hi:[1,1,0]\n"
CV "v112,v72,v73\n"
CV "v113,v74,v75\n"
CV "v114,v76,v77\n"
CV "v115,v78,v79\n"
MF "a[176:179],v[168:171],v[108:111],a[176:179]\n"
"ds_read_b128 a[72:75],v2 offset:6144\n"
AW "a168,v112\n"
AW "a169,v113\n"
AW "a170,v114\n"
AW "a171,v115\n"
AR "v80,a172\n"
AR "v81,a173\n"
AR "v82,a174\n"
MF "a[196:199],v[172:175],v[108:111],a[196:199]\n"
AR "v83,a175\n"
AR "v84,a192\n"
AR "v85,a193\n"
AR "v86,a194\n"
AR "v87,a195\n"
"v_pk_fma_f32 v[80:81],v[80:81],v[60:61],v[54:55] op_sel:[0,0,1] op_sel_hi:[1,0,1]\n"
"v_pk_fma_f32 v[82:83],v[82:83],v[60:61],v[54:55] op_sel:[0,0,1] op_sel_hi:[1,0,1]\n"
"v_pk_fma_f32 v[84:85],v[84:85],v[60:61],v[54:55] op_sel:[0,0,1] op_sel_hi:[1,0,1]\n"
"v_pk_fma_f32 v[86:87],v[86:87],v[60:61],v[54:55] op_sel:[0,0,1] op_sel_hi:[1,0,1]\n"
CV "v116,v80,v81\n"
CV "v117,v82,v83\n"
CV "v118,v84,v85\n"
CV "v119,v86,v87\n"
"s_waitcnt lgkmcnt(9)\n"
MF "a[200:203],v[176:179],v[92:95],a[200:203]\n"
"ds_read_b128 a[112:115],v3 offset:6144\n"
AW "a172,v116\n"
AW "a173,v117\n"
AW "a174,v118\n"
AW "a175,v119\n"
AR "v72,a176\n"
AR "v73,a177\n"
AR "v74,a178\n"
"s_waitcnt lgkmcnt(8)\n"
MF "a[220:223],v[180:183],v[92:95],a[220:223]\n"
AR "v75,a179\n"
AR "v76,a196\n"
AR "v77,a197\n"
AR "v78,a198\n"
AR "v79,a199\n"
"v_pk_fma_f32 v[72:73],v[72:73],v[60:61],v[56:57] op_sel:[0,1,0] op_sel_hi:[1,1,0]\n"
"v_pk_fma_f32 v[74:75],v[74:75],v[60:61],v[56:57] op_sel:[0,1,0] op_sel_hi:[1,1,0]\n"
"v_pk_fma_f32 v[76:77],v[76:77],v[60:61],v[56:57] op_sel:[0,1,0] op_sel_hi:[1,1,0]\n"
"v_pk_fma_f32 v[78:79],v[78:79],v[60:61],v[56:57] op_sel:[0,1,0] op_sel_hi:[1,1,0]\n"
CV "v112,v72,v73\n"
CV "v113,v74,v75\n"
CV "v114,v76,v77\n"
CV "v115,v78,v79\n"
MF "a[204:207],v[176:179],v[96:99],a[204:207]\n"
"ds_read_b128 a[76:79],v2 offset:8192\n"
AW "a176,v112\n"
AW "a177,v113\n"
AW "a178,v114\n"
AW "a179,v115\n"
AR "v80,a200\n"
AR "v81,a201\n"
AR "v82,a202\n"
MF "a[224:227],v[180:183],v[96:99],a[224:227]\n"
AR "v83,a203\n"
AR "v84,a220\n"
AR "v85,a221\n"
AR "v86,a222\n"
AR "v87,a223\n"
"v_pk_fma_f32 v[80:81],v[80:81],v[56:57],v[52:53] op_sel:[0,1,0] op_sel_hi:[1,1,0]\n"
"v_pk_fma_f32 v[82:83],v[82:83],v[56:57],v[52:53] op_sel:[0,1,0] op_sel_hi:[1,1,0]\n"
"v_pk_fma_f32 v[84:85],v[84:85],v[56:57],v[52:53] op_sel:[0,1,0] op_sel_hi:[1,1,0]\n"
"v_pk_fma_f32 v[86:87],v[86:87],v[56:57],v[52:53] op_sel:[0,1,0] op_sel_hi:[1,1,0]\n"
CV "v116,v80,v81\n"
CV "v117,v82,v83\n"
CV "v118,v84,v85\n"
CV "v119,v86,v87\n"
MF "a[208:211],v[176:179],v[100:103],a[208:211]\n"
"ds_read_b128 a[116:119],v3 offset:8192\n"
AW "a20,v116\n"
AW "a21,v117\n"
AW "a22,v118\n"
AW "a23,v119\n"
AR "v72,a204\n"
AR "v73,a205\n"
AR "v74,a206\n"
MF "a[228:231],v[180:183],v[100:103],a[228:231]\n"
AR "v75,a207\n"
AR "v76,a224\n"
AR "v77,a225\n"
AR "v78,a226\n"
AR "v79,a227\n"
"v_pk_fma_f32 v[72:73],v[72:73],v[58:59],v[52:53] op_sel:[0,0,1] op_sel_hi:[1,0,1]\n"
"v_pk_fma_f32 v[74:75],v[74:75],v[58:59],v[52:53] op_sel:[0,0,1] op_sel_hi:[1,0,1]\n"
"v_pk_fma_f32 v[76:77],v[76:77],v[58:59],v[52:53] op_sel:[0,0,1] op_sel_hi:[1,0,1]\n"
"v_pk_fma_f32 v[78:79],v[78:79],v[58:59],v[52:53] op_sel:[0,0,1] op_sel_hi:[1,0,1]\n"
CV "v112,v72,v73\n"
CV "v113,v74,v75\n"
CV "v114,v76,v77\n"
CV "v115,v78,v79\n"
MF "a[212:215],v[176:179],v[104:107],a[212:215]\n"
AW "a24,v112\n"
AW "a25,v113\n"
AW "a26,v114\n"
AW "a27,v115\n"
AR "v80,a208\n"
AR "v81,a209\n"
AR "v82,a210\n"
MF "a[232:235],v[180:183],v[104:107],a[232:235]\n"
AR "v83,a211\n"
AR "v84,a228\n"
AR "v85,a229\n"
AR "v86,a230\n"
AR "v87,a231\n"
"v_pk_fma_f32 v[80:81],v[80:81],v[58:59],v[54:55] op_sel:[0,1,0] op_sel_hi:[1,1,0]\n"
"v_pk_fma_f32 v[82:83],v[82:83],v[58:59],v[54:55] op_sel:[0,1,0] op_sel_hi:[1,1,0]\n"
"v_pk_fma_f32 v[84:85],v[84:85],v[58:59],v[54:55] op_sel:[0,1,0] op_sel_hi:[1,1,0]\n"
"v_pk_fma_f32 v[86:87],v[86:87],v[58:59],v[54:55] op_sel:[0,1,0] op_sel_hi:[1,1,0]\n"
CV "v116,v80,v81\n"
CV "v117,v82,v83\n"
CV "v118,v84,v85\n"
CV "v119,v86,v87\n"
MF "a[216:219],v[176:179],v[108:111],a[216:219]\n"
"s_waitcnt vmcnt(14) lgkmcnt(0)\n"
"s_barrier\n"
TR "a[200:201],v0\n"
TR "a[202:203],v0 offset:512\n"
TR "a[204:205],v0 offset:4096\n"
TR "a[206:207],v0 offset:4608\n"
AW "a28,v116\n"
AW "a29,v117\n"
AW "a30,v118\n"
AW "a31,v119\n"
AR "v72,a212\n"
AR "v73,a213\n"
AR "v74,a214\n"
MF "a[236:239],v[180:183],v[108:111],a[236:239]\n"
AR "v75,a215\n"
AR "v76,a232\n"
AR "v77,a233\n"
AR "v78,a234\n"
AR "v79,a235\n"
"v_pk_fma_f32 v[72:73],v[72:73],v[60:61],v[54:55] op_sel:[0,0,1] op_sel_hi:[1,0,1]\n"
"v_pk_fma_f32 v[74:75],v[74:75],v[60:61],v[54:55] op_sel:[0,0,1] op_sel_hi:[1,0,1]\n"
"v_pk_fma_f32 v[76:77],v[76:77],v[60:61],v[54:55] op_sel:[0,0,1] op_sel_hi:[1,0,1]\n"
"v_pk_fma_f32 v[78:79],v[78:79],v[60:61],v[54:55] op_sel:[0,0,1] op_sel_hi:[1,0,1]\n"
CV "v112,v72,v73\n"
CV "v113,v74,v75\n"
CV "v114,v76,v77\n"
CV "v115,v78,v79\n"
TR "a[208:209],v0 offset:8\n"
TR "a[210:211],v0 offset:520\n"
TR "a[212:213],v0 offset:4104\n"
TR "a[214:215],v0 offset:4616\n"
AW "a32,v112\n"
AW "a33,v113\n"
AW "a34,v114\n"
AW "a35,v115\n"
AR "v80,a216\n"
AR "v81,a217\n"
AR "v82,a218\n"
AR "v83,a219\n"
AR "v84,a236\n"
AR "v85,a237\n"
AR "v86,a238\n"
AR "v87,a239\n"
"v_pk_fma_f32 v[80:81],v[80:81],v[60:61],v[56:57] op_sel:[0,1,0] op_sel_hi:[1,1,0]\n"
"v_pk_fma_f32 v[82:83],v[82:83],v[60:61],v[56:57] op_sel:[0,1,0] op_sel_hi:[1,1,0]\n"
"v_pk_fma_f32 v[84:85],v[84:85],v[60:61],v[56:57] op_sel:[0,1,0] op_sel_hi:[1,1,0]\n"
"v_pk_fma_f32 v[86:87],v[86:87],v[60:61],v[56:57] op_sel:[0,1,0] op_sel_hi:[1,1,0]\n"
CV "v116,v80,v81\n"
CV "v117,v82,v83\n"
CV "v118,v84,v85\n"
CV "v119,v86,v87\n"
AW "a36,v116\n"
AW "a37,v117\n"
AW "a38,v118\n"
AW "a39,v119\n"
"s_mov_b32 s13,s11\n"
"s_mov_b32 s70,s71\n"
"s_mov_b32 s57,1\n"
"s_waitcnt lgkmcnt(0)\n"
"s_branch .Lq0\n"
".Lpgqy_c:\n"
"s_cbranch_execz .Lpgqj_a\n"
".Lqy:\n"
MF "v[216:219],a[216:219],a[60:63],v[216:219]\n"
TR "a[208:209],v0 offset:8\n"
MF "v[216:219],a[220:223],a[100:103],v[216:219]\n"
TR "a[210:211],v0 offset:520\n"
MF "v[224:227],a[220:223],a[104:107],v[224:227]\n"
MF "v[224:227],a[216:219],a[64:67],v[224:227]\n"
TR "a[212:213],v0 offset:4104\n"
MF "v[232:235],a[216:219],a[68:71],v[232:235]\n"
TR "a[214:215],v0 offset:4616\n"
MF "v[232:235],a[220:223],a[108:111],v[232:235]\n"
MF "v[240:243],a[220:223],a[112:115],v[240:243]\n"
MF "v[240:243],a[216:219],a[72:75],v[240:243]\n"
MF "v[248:251],a[216:219],a[76:79],v[248:251]\n"
MF "v[248:251],a[220:223],a[116:119],v[248:251]\n"
MF "v[248:251],a[200:203],a[156:159],v[248:251]\n"
TR "a[216:217],v0 offset:16392\n"
MF "v[248:251],a[204:207],a[196:199],v[248:251]\n"
TR "a[218:219],v0 offset:16904\n"
MF "v[240:243],a[204:207],a[192:195],v[240:243]\n"
MF "v[240:243],a[200:203],a[152:155],v[240:243]\n"
TR "a[220:221],v0 offset:20488\n"
MF "v[232:235],a[200:203],a[148:151],v[232:235]\n"
TR "a[222:223],v0 offset:21000\n"
MF "v[232:235],a[204:207],a[188:191],v[232:235]\n"
MF "v[224:227],a[204:207],a[184:187],v[224:227]\n"
MF "v[224:227],a[200:203],a[144:147],v[224:227]\n"
"v_fma_f32 v248,v248,s54,v14\n"
"v_fma_f32 v249,v249,s54,v14\n"
MF "v[216:219],a[200:203],a[140:143],v[216:219]\n"
"v_fma_f32 v250,v250,s54,v14\n"
"v_fma_f32 v251,v251,s54,v14\n"
MF "v[216:219],a[204:207],a[180:183],v[216:219]\n"
"s_waitcnt lgkmcnt(4)\n"
"v_fma_f32 v252,v252,s54,v14\n"
"v_fma_f32 v253,v253,s54,v14\n"
MF "v[180:183],a[208:211],a[60:63],v[180:183]\n"
TR "a[200:201],v0\n"
"v_fma_f32 v254,v254,s54,v14\n"
MF "v[180:183],a[212:215],a[100:103],v[180:183]\n"
TR "a[202:203],v0 offset:512\n"
"v_fma_f32 v255,v255,s54,v14\n"
MF "v[188:191],a[212:215],a[104:107],v[188:191]\n"
CV "v255,v254,v255\n"
CV "v254,v252,v253\n"
MF "v[188:191],a[208:211],a[64:67],v[188:191]\n"
TR "a[204:205],v0 offset:4096\n"
CV "v253,v250,v251\n"
MF "v[196:199],a[208:211],a[68:71],v[196:199]\n"
TR "a[206:207],v0 offset:4608\n"
CV "v252,v248,v249\n"
MF "v[196:199],a[212:215],a[108:111],v[196:199]\n"
"s_add_u32 s58,s75,4096\n"
"v_fma_f32 v240,v240,s54,v14\n"
"v_fma_f32 v241,v241,s54,v14\n"
MF "v[204:207],a[212:215],a[112:115],v[204:207]\n"
"buffer_store_dwordx4 v[252:255],v9,s[40:43],s58 offen offset:1024 sc1\n"
MF "v[204:207],a[208:211],a[72:75],v[204:207]\n"
"v_fma_f32 v242,v242,s54,v14\n"
"v_fma_f32 v243,v243,s54,v14\n"
MF "v[212:215],a[208:211],a[76:79],v[212:215]\n"
"v_fma_f32 v244,v244,s54,v14\n"
"v_fma_f32 v245,v245,s54,v14\n"
MF "v[212:215],a[212:215],a[116:119],v[212:215]\n"
"s_waitcnt lgkmcnt(4)\n"
"v_fma_f32 v246,v246,s54,v14\n"
"v_fma_f32 v247,v247,s54,v14\n"
MF "v[180:183],a[216:219],a[140:143],v[180:183]\n"
TR "a[208:209],v0 offset:16384\n"
CV "v251,v246,v247\n"
MF "v[180:183],a[220:223],a[180:183],v[180:183]\n"
TR "a[210:211],v0 offset:16896\n"
CV "v250,v244,v245\n"
MF "v[188:191],a[220:223],a[184:187],v[188:191]\n"
CV "v249,v242,v243\n"
CV "v248,v240,v241\n"
MF "v[188:191],a[216:219],a[144:147],v[188:191]\n"
TR "a[212:213],v0 offset:20480\n"
"v_fma_f32 v232,v232,s54,v14\n"
MF "v[196:199],a[216:219],a[148:151],v[196:199]\n"
TR "a[214:215],v0 offset:20992\n"
"v_fma_f32 v233,v233,s54,v14\n"
MF "v[196:199],a[220:223],a[188:191],v[196:199]\n"
"buffer_store_dwordx4 v[248:251],v9,s[40:43],s58 offen sc1\n"
MF "v[204:207],a[220:223],a[192:195],v[204:207]\n"
"v_fma_f32 v234,v234,s54,v14\n"
"v_fma_f32 v235,v235,s54,v14\n"
MF "v[204:207],a[216:219],a[152:155],v[204:207]\n"
"v_fma_f32 v236,v236,s54,v14\n"
"v_fma_f32 v237,v237,s54,v14\n"
MF "v[212:215],a[216:219],a[156:159],v[212:215]\n"
"s_waitcnt vmcnt(10) lgkmcnt(0)\n"
"v_fma_f32 v238,v238,s54,v14\n"
"v_fma_f32 v239,v239,s54,v14\n"
MF "v[212:215],a[220:223],a[196:199],v[212:215]\n"
"s_barrier\n"
MF "v[176:179],a[200:203],a[60:63],v[176:179]\n"
TR "a[216:217],v1 offset:32776\n"
CV "v247,v238,v239\n"
MF "v[176:179],a[204:207],a[100:103],v[176:179]\n"
TR "a[218:219],v1 offset:33288\n"
CV "v246,v236,v237\n"
MF "v[184:187],a[204:207],a[104:107],v[184:187]\n"
"s_add_u32 s66,s67,4718592\n"
"s_sub_u32 s81,s81,384\n"
"s_mov_b32 s83,7077888\n"
MF "v[184:187],a[200:203],a[64:67],v[184:187]\n"
"s_add_u32 m0,s62,0\n"
"s_add_u32 s82,s66,2359296\n"
TR "a[220:221],v1 offset:36872\n"
MF "v[192:195],a[200:203],a[68:71],v[192:195]\n"
"s_sub_u32 s15,s82,7864320\n"
"s_min_u32 s82,s82,s15\n"
TR "a[222:223],v1 offset:37384\n"
MF "v[192:195],a[204:207],a[108:111],v[192:195]\n"
LD "v6,s[28:31],s82 offen lds\n"
MF "v[200:203],a[204:207],a[112:115],v[200:203]\n"
CV "v245,v234,v235\n"
CV "v244,v232,v233\n"
MF "v[200:203],a[200:203],a[72:75],v[200:203]\n"
"v_fma_f32 v224,v224,s54,v14\n"
"v_fma_f32 v225,v225,s54,v14\n"
MF "v[208:211],a[200:203],a[76:79],v[208:211]\n"
"buffer_store_dwordx4 v[244:247],v9,s[40:43],s75 offen offset:3072 sc1\n"
MF "v[208:211],a[204:207],a[116:119],v[208:211]\n"
LD "v11,s[28:31],s82 offen offset:1024 lds\n"
MF "v[208:211],a[208:211],a[156:159],v[208:211]\n"
TR "a[200:201],v1 offset:49160\n"
"v_fma_f32 v226,v226,s54,v14\n"
MF "v[208:211],a[212:215],a[196:199],v[208:211]\n"
TR "a[202:203],v1 offset:49672\n"
"v_fma_f32 v227,v227,s54,v14\n"
MF "v[200:203],a[212:215],a[192:195],v[200:203]\n"
"v_fma_f32 v228,v228,s54,v14\n"
"v_fma_f32 v229,v229,s54,v14\n"
MF "v[200:203],a[208:211],a[152:155],v[200:203]\n"
TR "a[204:205],v1 offset:53256\n"
"v_fma_f32 v230,v230,s54,v14\n"
MF "v[192:195],a[208:211],a[148:151],v[192:195]\n"
TR "a[206:207],v1 offset:53768\n"
"v_fma_f32 v231,v231,s54,v14\n"
MF "v[192:195],a[212:215],a[188:191],v[192:195]\n"
CV "v243,v230,v231\n"
CV "v242,v228,v229\n"
MF "v[184:187],a[212:215],a[184:187],v[184:187]\n"
CV "v241,v226,v227\n"
CV "v240,v224,v225\n"
MF "v[184:187],a[208:211],a[144:147],v[184:187]\n"
"buffer_store_dwordx4 v[240:243],v9,s[40:43],s75 offen offset:2048 sc1\n"
MF "v[176:179],a[208:211],a[140:143],v[176:179]\n"
LD "v12,s[28:31],s82 offen offset:2048 lds\n"
MF "v[176:179],a[212:215],a[180:183],v[176:179]\n"
"s_waitcnt lgkmcnt(4)\n"
"v_fma_f32 v216,v216,s54,v14\n"
"v_fma_f32 v217,v217,s54,v14\n"
MF "v[140:143],a[216:219],a[60:63],v[140:143]\n"
TR "a[208:209],v1 offset:32768\n"
"v_fma_f32 v218,v218,s54,v14\n"
MF "v[140:143],a[220:223],a[100:103],v[140:143]\n"
TR "a[210:211],v1 offset:33280\n"
"v_fma_f32 v219,v219,s54,v14\n"
MF "v[148:151],a[220:223],a[104:107],v[148:151]\n"
"v_fma_f32 v220,v220,s54,v14\n"
"v_fma_f32 v221,v221,s54,v14\n"
MF "v[148:151],a[216:219],a[64:67],v[148:151]\n"
".Lpg14c:\n"
"s_cbranch_execz .Lpg3a\n"
TR "a[212:213],v1 offset:36864\n"
"v_fma_f32 v222,v222,s54,v14\n"
MF "v[156:159],a[216:219],a[68:71],v[156:159]\n"
TR "a[214:215],v1 offset:37376\n"
"v_fma_f32 v223,v223,s54,v14\n"
MF "v[156:159],a[220:223],a[108:111],v[156:159]\n"
CV "v239,v222,v223\n"
CV "v238,v220,v221\n"
MF "v[164:167],a[220:223],a[112:115],v[164:167]\n"
CV "v237,v218,v219\n"
CV "v236,v216,v217\n"
MF "v[164:167],a[216:219],a[72:75],v[164:167]\n"
LD "v13,s[28:31],s82 offen offset:3072 lds\n"
MF "v[172:175],a[216:219],a[76:79],v[172:175]\n"
"buffer_store_dwordx4 v[236:239],v9,s[40:43],s75 offen offset:1024 sc1\n"
MF "v[172:175],a[220:223],a[116:119],v[172:175]\n"
"s_waitcnt lgkmcnt(4)\n"
"v_fma_f32 v208,v208,s54,v14\n"
"v_fma_f32 v209,v209,s54,v14\n"
MF "v[140:143],a[200:203],a[140:143],v[140:143]\n"
TR "a[216:217],v1 offset:49152\n"
"v_fma_f32 v210,v210,s54,v14\n"
MF "v[140:143],a[204:207],a[180:183],v[140:143]\n"
TR "a[218:219],v1 offset:49664\n"
"v_fma_f32 v211,v211,s54,v14\n"
MF "v[148:151],a[204:207],a[184:187],v[148:151]\n"
"v_fma_f32 v212,v212,s54,v14\n"
"v_fma_f32 v213,v213,s54,v14\n"
MF "v[148:151],a[200:203],a[144:147],v[148:151]\n"
TR "a[220:221],v1 offset:53248\n"
"v_fma_f32 v214,v214,s54,v14\n"
MF "v[156:159],a[200:203],a[148:151],v[156:159]\n"
TR "a[222:223],v1 offset:53760\n"
"v_fma_f32 v215,v215,s54,v14\n"
MF "v[156:159],a[204:207],a[188:191],v[156:159]\n"
CV "v235,v214,v215\n"
CV "v234,v212,v213\n"
MF "v[164:167],a[204:207],a[192:195],v[164:167]\n"
CV "v233,v210,v211\n"
CV "v232,v208,v209\n"
MF "v[164:167],a[200:203],a[152:155],v[164:167]\n"
"v_fma_f32 v200,v200,s54,v14\n"
"v_fma_f32 v201,v201,s54,v14\n"
MF "v[172:175],a[200:203],a[156:159],v[172:175]\n"
"buffer_store_dwordx4 v[232:235],v9,s[40:43],s75 offen sc1\n"
MF "v[172:175],a[204:207],a[196:199],v[172:175]\n"
"s_waitcnt lgkmcnt(0)\n"
"v_fma_f32 v202,v202,s54,v14\n"
"v_fma_f32 v203,v203,s54,v14\n"
MF "v[136:139],a[208:211],a[60:63],v[136:139]\n"
"s_add_u32 m0,s62,16384\n"
"s_add_u32 s82,s66,2359424\n"
TR "a[200:201],v0 offset:32776\n"
MF "v[136:139],a[212:215],a[100:103],v[136:139]\n"
"s_sub_u32 s15,s82,7864320\n"
"s_min_u32 s82,s82,s15\n"
TR "a[202:203],v0 offset:33288\n"
MF "v[144:147],a[212:215],a[104:107],v[144:147]\n"
LD "v6,s[28:31],s82 offen lds\n"
MF "v[144:147],a[208:211],a[64:67],v[144:147]\n"
TR "a[204:205],v0 offset:36872\n"
"v_fma_f32 v204,v204,s54,v14\n"
MF "v[152:155],a[208:211],a[68:71],v[152:155]\n"
TR "a[206:207],v0 offset:37384\n"
"v_fma_f32 v205,v205,s54,v14\n"
MF "v[152:155],a[212:215],a[108:111],v[152:155]\n"
"v_fma_f32 v206,v206,s54,v14\n"
"v_fma_f32 v207,v207,s54,v14\n"
MF "v[160:163],a[212:215],a[112:115],v[160:163]\n"
CV "v231,v206,v207\n"
CV "v230,v204,v205\n"
MF "v[160:163],a[208:211],a[72:75],v[160:163]\n"
CV "v229,v202,v203\n"
CV "v228,v200,v201\n"
MF "v[168:171],a[208:211],a[76:79],v[168:171]\n"
AW "a84,v228\n"
AW "a85,v229\n"
MF "v[168:171],a[212:215],a[116:119],v[168:171]\n"
LD "v11,s[28:31],s82 offen offset:1024 lds\n"
MF "v[168:171],a[216:219],a[156:159],v[168:171]\n"
TR "a[208:209],v0 offset:49160\n"
AW "a86,v230\n"
MF "v[168:171],a[220:223],a[196:199],v[168:171]\n"
TR "a[210:211],v0 offset:49672\n"
AW "a87,v231\n"
MF "v[160:163],a[220:223],a[192:195],v[160:163]\n"
"v_fma_f32 v192,v192,s54,v14\n"
"v_fma_f32 v193,v193,s54,v14\n"
MF "v[160:163],a[216:219],a[152:155],v[160:163]\n"
TR "a[212:213],v0 offset:53256\n"
"v_fma_f32 v194,v194,s54,v14\n"
MF "v[152:155],a[216:219],a[148:151],v[152:155]\n"
TR "a[214:215],v0 offset:53768\n"
"v_fma_f32 v195,v195,s54,v14\n"
MF "v[152:155],a[220:223],a[188:191],v[152:155]\n"
"v_fma_f32 v196,v196,s54,v14\n"
"v_fma_f32 v197,v197,s54,v14\n"
MF "v[144:147],a[220:223],a[184:187],v[144:147]\n"
"v_fma_f32 v198,v198,s54,v14\n"
"v_fma_f32 v199,v199,s54,v14\n"
MF "v[144:147],a[216:219],a[144:147],v[144:147]\n"
CV "v227,v198,v199\n"
CV "v226,v196,v197\n"
MF "v[136:139],a[216:219],a[140:143],v[136:139]\n"
LD "v12,s[28:31],s82 offen offset:2048 lds\n"
MF "v[136:139],a[220:223],a[180:183],v[136:139]\n"
"s_waitcnt lgkmcnt(4)\n"
CV "v225,v194,v195\n"
CV "v224,v192,v193\n"
MF "v[100:103],a[200:203],a[60:63],v[100:103]\n"
TR "a[216:217],v0 offset:32768\n"
AW "a80,v224\n"
MF "v[100:103],a[204:207],a[100:103],v[100:103]\n"
TR "a[218:219],v0 offset:33280\n"
AW "a81,v225\n"
MF "v[108:111],a[204:207],a[104:107],v[108:111]\n"
AW "a82,v226\n"
AW "a83,v227\n"
MF "v[108:111],a[200:203],a[64:67],v[108:111]\n"
TR "a[220:221],v0 offset:36864\n"
"v_fma_f32 v184,v184,s54,v14\n"
MF "v[116:119],a[200:203],a[68:71],v[116:119]\n"
TR "a[222:223],v0 offset:37376\n"
"v_fma_f32 v185,v185,s54,v14\n"
MF "v[116:119],a[204:207],a[108:111],v[116:119]\n"
"v_fma_f32 v186,v186,s54,v14\n"
"v_fma_f32 v187,v187,s54,v14\n"
MF "v[124:127],a[204:207],a[112:115],v[124:127]\n"
"v_fma_f32 v188,v188,s54,v14\n"
"v_fma_f32 v189,v189,s54,v14\n"
MF "v[124:127],a[200:203],a[72:75],v[124:127]\n"
LD "v13,s[28:31],s82 offen offset:3072 lds\n"
MF "v[132:135],a[200:203],a[76:79],v[132:135]\n"
"v_fma_f32 v190,v190,s54,v14\n"
"v_fma_f32 v191,v191,s54,v14\n"
MF "v[132:135],a[204:207],a[116:119],v[132:135]\n"
"s_waitcnt lgkmcnt(4)\n"
CV "v223,v190,v191\n"
CV "v222,v188,v189\n"
MF "v[100:103],a[208:211],a[140:143],v[100:103]\n"
TR "a[200:201],v0 offset:49152\n"
CV "v221,v186,v187\n"
MF "v[100:103],a[212:215],a[180:183],v[100:103]\n"
TR "a[202:203],v0 offset:49664\n"
CV "v220,v184,v185\n"
MF "v[108:111],a[212:215],a[184:187],v[108:111]\n"
AW "a56,v220\n"
AW "a57,v221\n"
MF "v[108:111],a[208:211],a[144:147],v[108:111]\n"
TR "a[204:205],v0 offset:53248\n"
AW "a58,v222\n"
MF "v[116:119],a[208:211],a[148:151],v[116:119]\n"
TR "a[206:207],v0 offset:53760\n"
AW "a59,v223\n"
MF "v[116:119],a[212:215],a[188:191],v[116:119]\n"
"v_fma_f32 v176,v176,s54,v14\n"
"v_fma_f32 v177,v177,s54,v14\n"
MF "v[124:127],a[212:215],a[192:195],v[124:127]\n"
"v_add_u32 v0,0xc000,v0\n"
"v_fma_f32 v178,v178,s54,v14\n"
MF "v[124:127],a[208:211],a[152:155],v[124:127]\n"
"v_add_u32 v1,0xc000,v1\n"
"v_fma_f32 v179,v179,s54,v14\n"
MF "v[132:135],a[208:211],a[156:159],v[132:135]\n"
"s_waitcnt vmcnt(14) lgkmcnt(0)\n"
"v_fma_f32 v180,v180,s54,v14\n"
"v_fma_f32 v181,v181,s54,v14\n"
MF "v[132:135],a[212:215],a[196:199],v[132:135]\n"
"s_barrier\n"
MF "v[96:99],a[216:219],a[60:63],v[96:99]\n"
"s_add_u32 m0,s62,32768\n"
"s_add_u32 s82,s66,2359552\n"
TR "a[208:209],v1 offset:16392\n"
MF "v[96:99],a[220:223],a[100:103],v[96:99]\n"
"s_sub_u32 s15,s82,7864320\n"
"s_min_u32 s82,s82,s15\n"
TR "a[210:211],v1 offset:16904\n"
MF "v[104:107],a[220:223],a[104:107],v[104:107]\n"
LD "v6,s[28:31],s82 offen lds\n"
MF "v[104:107],a[216:219],a[64:67],v[104:107]\n"
TR "a[212:213],v1 offset:20488\n"
"v_fma_f32 v182,v182,s54,v14\n"
MF "v[112:115],a[216:219],a[68:71],v[112:115]\n"
TR "a[214:215],v1 offset:21000\n"
"v_fma_f32 v183,v183,s54,v14\n"
MF "v[112:115],a[220:223],a[108:111],v[112:115]\n"
CV "v219,v182,v183\n"
CV "v218,v180,v181\n"
MF "v[120:123],a[220:223],a[112:115],v[120:123]\n"
CV "v217,v178,v179\n"
CV "v216,v176,v177\n"
MF "v[120:123],a[216:219],a[72:75],v[120:123]\n"
AW "a52,v216\n"
AW "a53,v217\n"
MF "v[128:131],a[216:219],a[76:79],v[128:131]\n"
AW "a54,v218\n"
AW "a55,v219\n"
MF "v[128:131],a[220:223],a[116:119],v[128:131]\n"
LD "v11,s[28:31],s82 offen offset:1024 lds\n"
MF "v[128:131],a[200:203],a[156:159],v[128:131]\n"
TR "a[216:217],v1 offset:32776\n"
"v_fma_f32 v168,v168,s54,v14\n"
MF "v[128:131],a[204:207],a[196:199],v[128:131]\n"
TR "a[218:219],v1 offset:33288\n"
"v_fma_f32 v169,v169,s54,v14\n"
MF "v[120:123],a[204:207],a[192:195],v[120:123]\n"
"v_fma_f32 v170,v170,s54,v14\n"
"v_fma_f32 v171,v171,s54,v14\n"
MF "v[120:123],a[200:203],a[152:155],v[120:123]\n"
TR "a[220:221],v1 offset:36872\n"
"v_fma_f32 v172,v172,s54,v14\n"
MF "v[112:115],a[200:203],a[148:151],v[112:115]\n"
TR "a[222:223],v1 offset:37384\n"
"v_fma_f32 v173,v173,s54,v14\n"
MF "v[112:115],a[204:207],a[188:191],v[112:115]\n"
"v_fma_f32 v174,v174,s54,v14\n"
"v_fma_f32 v175,v175,s54,v14\n"
MF "v[104:107],a[204:207],a[184:187],v[104:107]\n"
CV "v215,v174,v175\n"
CV "v214,v172,v173\n"
MF "v[104:107],a[200:203],a[144:147],v[104:107]\n"
CV "v213,v170,v171\n"
CV "v212,v168,v169\n"
MF "v[96:99],a[200:203],a[140:143],v[96:99]\n"
LD "v12,s[28:31],s82 offen offset:2048 lds\n"
MF "v[96:99],a[204:207],a[180:183],v[96:99]\n"
"s_waitcnt lgkmcnt(4)\n"
AW "a48,v212\n"
AW "a49,v213\n"
MF "v[60:63],a[208:211],a[60:63],v[60:63]\n"
TR "a[200:201],v1 offset:16384\n"
AW "a50,v214\n"
MF "v[60:63],a[212:215],a[100:103],v[60:63]\n"
TR "a[202:203],v1 offset:16896\n"
AW "a51,v215\n"
MF "v[68:71],a[212:215],a[104:107],v[68:71]\n"
"v_fma_f32 v160,v160,s54,v14\n"
"v_fma_f32 v161,v161,s54,v14\n"
MF "v[68:71],a[208:211],a[64:67],v[68:71]\n"
TR "a[204:205],v1 offset:20480\n"
"v_fma_f32 v162,v162,s54,v14\n"
MF "v[76:79],a[208:211],a[68:71],v[76:79]\n"
TR "a[206:207],v1 offset:20992\n"
"v_fma_f32 v163,v163,s54,v14\n"
MF "v[76:79],a[212:215],a[108:111],v[76:79]\n"
"v_fma_f32 v164,v164,s54,v14\n"
"v_fma_f32 v165,v165,s54,v14\n"
MF "v[84:87],a[212:215],a[112:115],v[84:87]\n"
"v_fma_f32 v166,v166,s54,v14\n"
"v_fma_f32 v167,v167,s54,v14\n"
MF "v[84:87],a[208:211],a[72:75],v[84:87]\n"
LD "v13,s[28:31],s82 offen offset:3072 lds\n"
MF "v[92:95],a[208:211],a[76:79],v[92:95]\n"
CV "v211,v166,v167\n"
CV "v210,v164,v165\n"
MF "v[92:95],a[212:215],a[116:119],v[92:95]\n"
"s_waitcnt lgkmcnt(4)\n"
CV "v209,v162,v163\n"
CV "v208,v160,v161\n"
MF "v[60:63],a[216:219],a[140:143],v[60:63]\n"
TR "a[208:209],v1 offset:32768\n"
AW "a44,v208\n"
MF "v[60:63],a[220:223],a[180:183],v[60:63]\n"
TR "a[210:211],v1 offset:33280\n"
AW "a45,v209\n"
MF "v[68:71],a[220:223],a[184:187],v[68:71]\n"
AW "a46,v210\n"
AW "a47,v211\n"
MF "v[68:71],a[216:219],a[144:147],v[68:71]\n"
TR "a[212:213],v1 offset:36864\n"
"v_fma_f32 v152,v152,s54,v14\n"
MF "v[76:79],a[216:219],a[148:151],v[76:79]\n"
TR "a[214:215],v1 offset:37376\n"
"v_fma_f32 v153,v153,s54,v14\n"
MF "v[76:79],a[220:223],a[188:191],v[76:79]\n"
"v_fma_f32 v154,v154,s54,v14\n"
"v_fma_f32 v155,v155,s54,v14\n"
MF "v[84:87],a[220:223],a[192:195],v[84:87]\n"
"v_fma_f32 v156,v156,s54,v14\n"
"v_fma_f32 v157,v157,s54,v14\n"
MF "v[84:87],a[216:219],a[152:155],v[84:87]\n"
"v_fma_f32 v158,v158,s54,v14\n"
"v_fma_f32 v159,v159,s54,v14\n"
MF "v[92:95],a[216:219],a[156:159],v[92:95]\n"
CV "v207,v158,v159\n"
CV "v206,v156,v157\n"
MF "v[92:95],a[220:223],a[196:199],v[92:95]\n"
"s_waitcnt lgkmcnt(0)\n"
CV "v205,v154,v155\n"
CV "v204,v152,v153\n"
MF "v[56:59],a[200:203],a[60:63],v[56:59]\n"
TR "a[216:217],v0 offset:16392\n"
AW "a40,v204\n"
MF "v[56:59],a[204:207],a[100:103],v[56:59]\n"
TR "a[218:219],v0 offset:16904\n"
AW "a41,v205\n"
MF "v[64:67],a[204:207],a[104:107],v[64:67]\n"
AW "a42,v206\n"
AW "a43,v207\n"
MF "v[64:67],a[200:203],a[64:67],v[64:67]\n"
TR "a[220:221],v0 offset:20488\n"
"v_fma_f32 v144,v144,s54,v14\n"
MF "v[72:75],a[200:203],a[68:71],v[72:75]\n"
TR "a[222:223],v0 offset:21000\n"
"v_fma_f32 v145,v145,s54,v14\n"
MF "v[72:75],a[204:207],a[108:111],v[72:75]\n"
"v_fma_f32 v146,v146,s54,v14\n"
"v_fma_f32 v147,v147,s54,v14\n"
MF "v[80:83],a[204:207],a[112:115],v[80:83]\n"
"v_fma_f32 v148,v148,s54,v14\n"
"v_fma_f32 v149,v149,s54,v14\n"
MF "v[80:83],a[200:203],a[72:75],v[80:83]\n"
"v_fma_f32 v150,v150,s54,v14\n"
"v_fma_f32 v151,v151,s54,v14\n"
MF "v[88:91],a[200:203],a[76:79],v[88:91]\n"
CV "v203,v150,v151\n"
CV "v202,v148,v149\n"
MF "v[88:91],a[204:207],a[116:119],v[88:91]\n"
CV "v201,v146,v147\n"
CV "v200,v144,v145\n"
MF "v[88:91],a[208:211],a[156:159],v[88:91]\n"
TR "a[200:201],v0 offset:32776\n"
AW "a16,v200\n"
MF "v[88:91],a[212:215],a[196:199],v[88:91]\n"
TR "a[202:203],v0 offset:33288\n"
AW "a17,v201\n"
MF "v[80:83],a[212:215],a[192:195],v[80:83]\n"
AW "a18,v202\n"
AW "a19,v203\n"
MF "v[80:83],a[208:211],a[152:155],v[80:83]\n"
TR "a[204:205],v0 offset:36872\n"
"v_fma_f32 v136,v136,s54,v14\n"
MF "v[72:75],a[208:211],a[148:151],v[72:75]\n"
TR "a[206:207],v0 offset:37384\n"
"v_fma_f32 v137,v137,s54,v14\n"
MF "v[72:75],a[212:215],a[188:191],v[72:75]\n"
"v_fma_f32 v138,v138,s54,v14\n"
"v_fma_f32 v139,v139,s54,v14\n"
MF "v[64:67],a[212:215],a[184:187],v[64:67]\n"
"v_fma_f32 v140,v140,s54,v14\n"
"v_fma_f32 v141,v141,s54,v14\n"
MF "v[64:67],a[208:211],a[144:147],v[64:67]\n"
"v_fma_f32 v142,v142,s54,v14\n"
"v_fma_f32 v143,v143,s54,v14\n"
MF "v[56:59],a[208:211],a[140:143],v[56:59]\n"
CV "v199,v142,v143\n"
CV "v198,v140,v141\n"
MF "v[56:59],a[212:215],a[180:183],v[56:59]\n"
"s_waitcnt lgkmcnt(4)\n"
CV "v197,v138,v139\n"
CV "v196,v136,v137\n"
MF "v[20:23],a[216:219],a[60:63],v[20:23]\n"
TR "a[208:209],v0 offset:16384\n"
AW "a12,v196\n"
MF "v[20:23],a[220:223],a[100:103],v[20:23]\n"
TR "a[210:211],v0 offset:16896\n"
AW "a13,v197\n"
MF "v[28:31],a[220:223],a[104:107],v[28:31]\n"
AW "a14,v198\n"
AW "a15,v199\n"
MF "v[28:31],a[216:219],a[64:67],v[28:31]\n"
TR "a[212:213],v0 offset:20480\n"
"v_fma_f32 v128,v128,s54,v14\n"
MF "v[36:39],a[216:219],a[68:71],v[36:39]\n"
TR "a[214:215],v0 offset:20992\n"
"v_fma_f32 v129,v129,s54,v14\n"
MF "v[36:39],a[220:223],a[108:111],v[36:39]\n"
"v_fma_f32 v130,v130,s54,v14\n"
"v_fma_f32 v131,v131,s54,v14\n"
MF "v[44:47],a[220:223],a[112:115],v[44:47]\n"
"v_fma_f32 v132,v132,s54,v14\n"
"v_fma_f32 v133,v133,s54,v14\n"
MF "v[44:47],a[216:219],a[72:75],v[44:47]\n"
"v_fma_f32 v134,v134,s54,v14\n"
"v_fma_f32 v135,v135,s54,v14\n"
MF "v[52:55],a[216:219],a[76:79],v[52:55]\n"
CV "v195,v134,v135\n"
CV "v194,v132,v133\n"
MF "v[52:55],a[220:223],a[116:119],v[52:55]\n"
"s_waitcnt lgkmcnt(4)\n"
CV "v193,v130,v131\n"
CV "v192,v128,v129\n"
MF "v[20:23],a[200:203],a[140:143],v[20:23]\n"
TR "a[216:217],v0 offset:32768\n"
AW "a8,v192\n"
MF "v[20:23],a[204:207],a[180:183],v[20:23]\n"
TR "a[218:219],v0 offset:33280\n"
AW "a9,v193\n"
MF "v[28:31],a[204:207],a[184:187],v[28:31]\n"
AW "a10,v194\n"
AW "a11,v195\n"
MF "v[28:31],a[200:203],a[144:147],v[28:31]\n"
TR "a[220:221],v0 offset:36864\n"
"v_fma_f32 v120,v120,s54,v14\n"
MF "v[36:39],a[200:203],a[148:151],v[36:39]\n"
TR "a[222:223],v0 offset:37376\n"
"v_fma_f32 v121,v121,s54,v14\n"
MF "v[36:39],a[204:207],a[188:191],v[36:39]\n"
"v_fma_f32 v122,v122,s54,v14\n"
"v_fma_f32 v123,v123,s54,v14\n"
MF "v[44:47],a[204:207],a[192:195],v[44:47]\n"
"v_fma_f32 v124,v124,s54,v14\n"
"v_fma_f32 v125,v125,s54,v14\n"
MF "v[44:47],a[200:203],a[152:155],v[44:47]\n"
"v_fma_f32 v126,v126,s54,v14\n"
"v_fma_f32 v127,v127,s54,v14\n"
MF "v[52:55],a[200:203],a[156:159],v[52:55]\n"
"s_waitcnt lgkmcnt(0)\n"
CV "v191,v126,v127\n"
CV "v190,v124,v125\n"
MF "v[52:55],a[204:207],a[196:199],v[52:55]\n"
"s_barrier\n"
MF "v[16:19],a[208:211],a[60:63],v[16:19]\n"
CV "v189,v122,v123\n"
CV "v188,v120,v121\n"
MF "v[16:19],a[212:215],a[100:103],v[16:19]\n"
AW "a4,v188\n"
AW "a5,v189\n"
MF "v[24:27],a[212:215],a[104:107],v[24:27]\n"
AW "a6,v190\n"
AW "a7,v191\n"
MF "v[24:27],a[208:211],a[64:67],v[24:27]\n"
"v_fma_f32 v112,v112,s54,v14\n"
"v_fma_f32 v113,v113,s54,v14\n"
MF "v[32:35],a[208:211],a[68:71],v[32:35]\n"
"v_fma_f32 v114,v114,s54,v14\n"
"v_fma_f32 v115,v115,s54,v14\n"
MF "v[32:35],a[212:215],a[108:111],v[32:35]\n"
"v_fma_f32 v116,v116,s54,v14\n"
"v_fma_f32 v117,v117,s54,v14\n"
MF "v[40:43],a[212:215],a[112:115],v[40:43]\n"
"v_fma_f32 v118,v118,s54,v14\n"
"v_fma_f32 v119,v119,s54,v14\n"
MF "v[40:43],a[208:211],a[72:75],v[40:43]\n"
CV "v187,v118,v119\n"
CV "v186,v116,v117\n"
MF "v[48:51],a[208:211],a[76:79],v[48:51]\n"
CV "v185,v114,v115\n"
CV "v184,v112,v113\n"
MF "v[48:51],a[212:215],a[116:119],v[48:51]\n"
AW "a0,v184\n"
AW "a1,v185\n"
MF "v[48:51],a[216:219],a[156:159],v[48:51]\n"
AW "a2,v186\n"
AW "a3,v187\n"
MF "v[48:51],a[220:223],a[196:199],v[48:51]\n"
"v_fma_f32 v104,v104,s54,v14\n"
"v_fma_f32 v105,v105,s54,v14\n"
MF "v[40:43],a[220:223],a[192:195],v[40:43]\n"
"v_fma_f32 v106,v106,s54,v14\n"
"v_fma_f32 v107,v107,s54,v14\n"
MF "v[40:43],a[216:219],a[152:155],v[40:43]\n"
"v_fma_f32 v108,v108,s54,v14\n"
"v_fma_f32 v109,v109,s54,v14\n"
MF "v[32:35],a[216:219],a[148:151],v[32:35]\n"
"v_fma_f32 v110,v110,s54,v14\n"
"v_fma_f32 v111,v111,s54,v14\n"
MF "v[32:35],a[220:223],a[188:191],v[32:35]\n"
CV "v183,v110,v111\n"
CV "v182,v108,v109\n"
MF "v[24:27],a[220:223],a[184:187],v[24:27]\n"
CV "v181,v106,v107\n"
CV "v180,v104,v105\n"
MF "v[24:27],a[216:219],a[144:147],v[24:27]\n"
AW "a36,v180\n"
AW "a37,v181\n"
MF "v[16:19],a[216:219],a[140:143],v[16:19]\n"
AW "a38,v182\n"
AW "a39,v183\n"
MF "v[16:19],a[220:223],a[180:183],v[16:19]\n"
"s_mov_b32 s57,1\n"
"s_mov_b32 s69,99\n"
AR "v0,a240\n"
AR "v2,a241\n"
AR "v4,a242\n"
AR "v6,a243\n"
"v_xor_b32 v1,64,v0\n"
"v_xor_b32 v3,64,v2\n"
"v_xor_b32 v5,64,v4\n"
"v_add_u32 v11,97280,v6\n"
"v_add_u32 v12,194560,v6\n"
"v_add_u32 v13,291840,v6\n"
"s_mov_b64 vcc,-1\n"
"s_waitcnt vmcnt(10) lgkmcnt(0)\n"
"s_barrier\n"
TR "a[200:201],v0\n"
TR "a[202:203],v0 offset:512\n"
TR "a[204:205],v0 offset:4096\n"
TR "a[206:207],v0 offset:4608\n"
"ds_read_b128 a[60:63],v2\n"
"ds_read_b128 a[100:103],v3\n"
"ds_read_b128 a[64:67],v2 offset:2048\n"
"ds_read_b128 a[104:107],v3 offset:2048\n"
"ds_read_b128 a[68:71],v2 offset:4096\n"
"ds_read_b128 a[108:111],v3 offset:4096\n"
"ds_read_b128 a[72:75],v2 offset:6144\n"
"ds_read_b128 a[112:115],v3 offset:6144\n"
"ds_read_b128 a[76:79],v2 offset:8192\n"
"ds_read_b128 a[116:119],v3 offset:8192\n"
TR "a[208:209],v0 offset:8\n"
TR "a[210:211],v0 offset:520\n"
TR "a[212:213],v0 offset:4104\n"
TR "a[214:215],v0 offset:4616\n"
"v_pk_fma_f32 v[96:97],v[96:97],s[54:55],v[14:15] op_sel_hi:[1,1,0]\n"
"v_pk_fma_f32 v[98:99],v[98:99],s[54:55],v[14:15] op_sel_hi:[1,1,0]\n"
"v_pk_fma_f32 v[100:101],v[100:101],s[54:55],v[14:15] op_sel_hi:[1,1,0]\n"
"v_pk_fma_f32 v[102:103],v[102:103],s[54:55],v[14:15] op_sel_hi:[1,1,0]\n"
CV "v179,v102,v103\n"
CV "v178,v100,v101\n"
CV "v177,v98,v99\n"
CV "v176,v96,v97\n"
AW "a32,v176\n"
AW "a33,v177\n"
AW "a34,v178\n"
AW "a35,v179\n"
"v_pk_fma_f32 v[88:89],v[88:89],s[54:55],v[14:15] op_sel_hi:[1,1,0]\n"
"v_pk_fma_f32 v[90:91],v[90:91],s[54:55],v[14:15] op_sel_hi:[1,1,0]\n"
"v_pk_fma_f32 v[92:93],v[92:93],s[54:55],v[14:15] op_sel_hi:[1,1,0]\n"
"v_pk_fma_f32 v[94:95],v[94:95],s[54:55],v[14:15] op_sel_hi:[1,1,0]\n"
CV "v175,v94,v95\n"
".Lpg15c:\n"
"s_cbranch_execz .Lpg2a\n"
"s_nop 0\n"
CV "v174,v92,v93\n"
CV "v173,v90,v91\n"
CV "v172,v88,v89\n"
AW "a28,v172\n"
AW "a29,v173\n"
AW "a30,v174\n"
AW "a31,v175\n"
"v_pk_fma_f32 v[80:81],v[80:81],s[54:55],v[14:15] op_sel_hi:[1,1,0]\n"
"v_pk_fma_f32 v[82:83],v[82:83],s[54:55],v[14:15] op_sel_hi:[1,1,0]\n"
"v_pk_fma_f32 v[84:85],v[84:85],s[54:55],v[14:15] op_sel_hi:[1,1,0]\n"
"v_pk_fma_f32 v[86:87],v[86:87],s[54:55],v[14:15] op_sel_hi:[1,1,0]\n"
CV "v171,v86,v87\n"
CV "v170,v84,v85\n"
CV "v169,v82,v83\n"
CV "v168,v80,v81\n"
AW "a24,v168\n"
AW "a25,v169\n"
AW "a26,v170\n"
AW "a27,v171\n"
"v_pk_fma_f32 v[72:73],v[72:73],s[54:55],v[14:15] op_sel_hi:[1,1,0]\n"
"v_pk_fma_f32 v[74:75],v[74:75],s[54:55],v[14:15] op_sel_hi:[1,1,0]\n"
"v_pk_fma_f32 v[76:77],v[76:77],s[54:55],v[14:15] op_sel_hi:[1,1,0]\n"
"v_pk_fma_f32 v[78:79],v[78:79],s[54:55],v[14:15] op_sel_hi:[1,1,0]\n"
CV "v167,v78,v79\n"
CV "v166,v76,v77\n"
CV "v165,v74,v75\n"
CV "v164,v72,v73\n"
AW "a20,v164\n"
AW "a21,v165\n"
AW "a22,v166\n"
AW "a23,v167\n"
"v_pk_fma_f32 v[64:65],v[64:65],s[54:55],v[14:15] op_sel_hi:[1,1,0]\n"
"v_pk_fma_f32 v[66:67],v[66:67],s[54:55],v[14:15] op_sel_hi:[1,1,0]\n"
"v_pk_fma_f32 v[68:69],v[68:69],s[54:55],v[14:15] op_sel_hi:[1,1,0]\n"
"v_pk_fma_f32 v[70:71],v[70:71],s[54:55],v[14:15] op_sel_hi:[1,1,0]\n"
CV "v163,v70,v71\n"
CV "v162,v68,v69\n"
CV "v161,v66,v67\n"
CV "v160,v64,v65\n"
AW "a176,v160\n"
AW "a177,v161\n"
AW "a178,v162\n"
AW "a179,v163\n"
"v_pk_fma_f32 v[56:57],v[56:57],s[54:55],v[14:15] op_sel_hi:[1,1,0]\n"
"v_pk_fma_f32 v[58:59],v[58:59],s[54:55],v[14:15] op_sel_hi:[1,1,0]\n"
"v_pk_fma_f32 v[60:61],v[60:61],s[54:55],v[14:15] op_sel_hi:[1,1,0]\n"
"v_pk_fma_f32 v[62:63],v[62:63],s[54:55],v[14:15] op_sel_hi:[1,1,0]\n"
CV "v159,v62,v63\n"
CV "v158,v60,v61\n"
CV "v157,v58,v59\n"
CV "v156,v56,v57\n"
AW "a172,v156\n"
AW "a173,v157\n"
AW "a174,v158\n"
AW "a175,v159\n"
"v_pk_mul_f32 v[48:49],v[48:49],s[54:55]\n"
"v_pk_mul_f32 v[50:51],v[50:51],s[54:55]\n"
"v_pk_mul_f32 v[52:53],v[52:53],s[54:55]\n"
"v_pk_mul_f32 v[54:55],v[54:55],s[54:55]\n"
CV "v155,v54,v55\n"
CV "v154,v52,v53\n"
CV "v153,v50,v51\n"
CV "v152,v48,v49\n"
AW "a168,v152\n"
AW "a169,v153\n"
AW "a170,v154\n"
AW "a171,v155\n"
"v_pk_mul_f32 v[40:41],v[40:41],s[54:55]\n"
"v_pk_mul_f32 v[42:43],v[42:43],s[54:55]\n"
"v_pk_mul_f32 v[44:45],v[44:45],s[54:55]\n"
"v_pk_mul_f32 v[46:47],v[46:47],s[54:55]\n"
CV "v151,v46,v47\n"
CV "v150,v44,v45\n"
CV "v149,v42,v43\n"
CV "v148,v40,v41\n"
AW "a120,v148\n"
AW "a121,v149\n"
AW "a122,v150\n"
AW "a123,v151\n"
"v_pk_mul_f32 v[32:33],v[32:33],s[54:55]\n"
"v_pk_mul_f32 v[34:35],v[34:35],s[54:55]\n"
"v_pk_mul_f32 v[36:37],v[36:37],s[54:55]\n"
"v_pk_mul_f32 v[38:39],v[38:39],s[54:55]\n"
CV "v147,v38,v39\n"
CV "v146,v36,v37\n"
CV "v145,v34,v35\n"
CV "v144,v32,v33\n"
AW "a96,v144\n"
AW "a97,v145\n"
AW "a98,v146\n"
AW "a99,v147\n"
"v_pk_mul_f32 v[24:25],v[24:25],s[54:55]\n"
"v_pk_mul_f32 v[26:27],v[26:27],s[54:55]\n"
"v_pk_mul_f32 v[28:29],v[28:29],s[54:55]\n"
"v_pk_mul_f32 v[30:31],v[30:31],s[54:55]\n"
CV "v143,v30,v31\n"
CV "v142,v28,v29\n"
CV "v141,v26,v27\n"
CV "v140,v24,v25\n"
AW "a92,v140\n"
AW "a93,v141\n"
AW "a94,v142\n"
AW "a95,v143\n"
"v_pk_mul_f32 v[16:17],v[16:17],s[54:55]\n"
"v_pk_mul_f32 v[18:19],v[18:19],s[54:55]\n"
"v_pk_mul_f32 v[20:21],v[20:21],s[54:55]\n"
"v_pk_mul_f32 v[22:23],v[22:23],s[54:55]\n"
CV "v139,v22,v23\n"
CV "v138,v20,v21\n"
CV "v137,v18,v19\n"
CV "v136,v16,v17\n"
AW "a88,v136\n"
AW "a89,v137\n"
AW "a90,v138\n"
AW "a91,v139\n"
"s_waitcnt lgkmcnt(0)\n"
"s_branch .Lq0\n"
".Lpgpsl_b:\n"
"s_cbranch_execz .Lpg3a\n"
".Lpsl:\n"
"s_sleep 1\n"
".Lpsub:\n"
"s_memrealtime s[100:101]\n"
"s_waitcnt lgkmcnt(0)\n"
"s_sub_u32 s101,s21,s100\n"
"s_cmp_gt_i32 s101,0\n"
"s_cbranch_scc1 .Lpsl\n"
"s_add_u32 s21,s100,s97\n"
"s_setpc_b64 s[52:53]\n"
".Lstub_6:\n"
"v_cmp_le_f32 vcc,0x47800000,v10\n"
"s_cmp_eq_u32 s77,0\n"
"s_cbranch_scc0 .Lnop_6\n"
"s_call_b64 s[52:53],.Lpsub\n"
".Lnop_6:\n"
"s_cbranch_vccz .Lback_6\n"
"s_call_b64 s[52:53],.Lslow_1\n"
"s_branch .Lback_6\n"
".Lstub_7:\n"
"v_cmp_le_f32 vcc,0x47800000,v10\n"
"s_cmp_eq_u32 s77,0\n"
"s_cbranch_scc0 .Lnop_7\n"
"s_call_b64 s[52:53],.Lpsub\n"
"s_mul_i32 s58,s57,4\n"
"s_add_u32 s58,s58,s61\n"
"s_cmp_eq_u32 s58,4\n"
"s_cselect_b32 s77,0x47800000,s77\n"
"s_cselect_b32 s95,0x47800000,s95\n"
".Lnop_7:\n"
"s_cbranch_vccz .Lback_7\n"
"s_call_b64 s[52:53],.Lslow_0\n"
"s_branch .Lback_7\n"
".Lstub_8:\n"
"s_call_b64 s[52:53],.Lslow_1\n"
"s_branch .Lback_8\n"
".Lstub_9:\n"
"s_call_b64 s[52:53],.Lslow_0\n"
"s_branch .Lback_9\n"
".Lstub_10:\n"
"s_call_b64 s[52:53],.Lslow_1\n"
"s_branch .Lback_10\n"
".Lstub_11:\n"
"s_call_b64 s[52:53],.Lslow_1\n"
"s_branch .Lback_11\n"
".Lstub_12:\n"
"s_call_b64 s[52:53],.Lslow_0\n"
"s_branch .Lback_12\n"
".Lstub_13:\n"
"s_call_b64 s[52:53],.Lslow_1\n"
"s_branch .Lback_13\n"
".Lstub_20:\n"
"v_cmp_le_f32 vcc,0x47800000,v10\n"
"s_cmp_eq_u32 s77,0\n"
"s_cbranch_scc0 .Lnop_20\n"
"s_call_b64 s[52:53],.Lpsub\n"
".Lnop_20:\n"
"s_cbranch_vccz .Lback_20\n"
"s_call_b64 s[52:53],.Lslow_1\n"
"s_branch .Lback_20\n"
".Lstub_21:\n"
"v_cmp_le_f32 vcc,0x47800000,v10\n"
"s_cmp_eq_u32 s77,0\n"
"s_cbranch_scc0 .Lnop_21\n"
"s_call_b64 s[52:53],.Lpsub\n"
".Lnop_21:\n"
"s_cbranch_vccz .Lback_21\n"
"s_call_b64 s[52:53],.Lslow_0\n"
"s_branch .Lback_21\n"
".Lstub_22:\n"
"v_cmp_le_f32 vcc,0x47800000,v10\n"
"s_cmp_eq_u32 s77,0\n"
"s_cbranch_scc0 .Lnop_22\n"
"s_call_b64 s[52:53],.Lpsub\n"
".Lnop_22:\n"
"s_cbranch_vccz .Lback_22\n"
"s_call_b64 s[52:53],.Lslow_1\n"
"s_branch .Lback_22\n"
".Lstub_23:\n"
"v_cmp_le_f32 vcc,0x47800000,v10\n"
"s_cmp_eq_u32 s77,0\n"
"s_cbranch_scc0 .Lnop_23\n"
"s_call_b64 s[52:53],.Lpsub\n"
".Lnop_23:\n"
"s_cbranch_vccz .Lback_23\n"
"s_call_b64 s[52:53],.Lslow_0\n"
"s_branch .Lback_23\n"
".Lstub_24:\n"
"v_cmp_le_f32 vcc,0x47800000,v10\n"
"s_cmp_eq_u32 s61,1\n"
"s_cbranch_scc0 .Lhook_24\n"
"s_mov_b64 exec,0\n"
"s_cmp_eq_u32 s56,0\n"
"s_cbranch_scc1 .Lpg26h\n"
"s_cmp_eq_u32 s56,1\n"
"s_cbranch_scc1 .Lpg25h\n"
"s_cmp_eq_u32 s56,2\n"
"s_cbranch_scc1 .Lpg24h\n"
"s_branch .Lpg23h\n"
".Lisr4:\n"
"s_mov_b64 exec,-1\n"
".Lisx4:\n"
"s_and_b32 s58,s56,1\n"
"s_mul_i32 s79,s58,12288\n"
"s_lshl_b32 s15,s58,10\n"
"s_lshr_b32 s58,s56,1\n"
"s_mul_i32 s5,s58,6144\n"
"s_add_u32 s5,s5,s79\n"
"s_lshl_b32 s59,s58,8\n"
"s_add_u32 s15,s15,s59\n"
"s_add_u32 s15,s15,159744\n"
"s_add_u32 s88,s88,1024\n"
"s_add_u32 s89,s89,1024\n"
"s_mul_i32 s58,s58,61440\n"
"s_add_u32 s2,s89,s58\n"
"s_add_u32 s3,s88,s58\n"
"s_add_u32 s0,s2,122880\n"
"s_add_u32 s1,s3,122880\n"
"s_lshl_b32 s58,s56,10\n"
"s_add_u32 s9,s58,73728\n"
"s_add_u32 s14,s58,159744\n"
"s_bfe_u32 s4,s22,0x10014\n"
"s_lshl_b32 s4,s4,5\n"
"s_mov_b32 s6,896\n"
".Lhook_24:\n"
"s_cmp_eq_u32 s77,0\n"
"s_cbranch_scc0 .Lnop_24\n"
"s_call_b64 s[52:53],.Lpsub\n"
".Lnop_24:\n"
"s_cbranch_vccz .Lback_24\n"
"s_call_b64 s[52:53],.Lslow_1\n"
"s_branch .Lback_24\n"
".Lstub_25:\n"
"v_cmp_le_f32 vcc,0x47800000,v10\n"
"s_cmp_eq_u32 s77,0\n"
"s_cbranch_scc0 .Lnop_25\n"
"s_call_b64 s[52:53],.Lpsub\n"
".Lnop_25:\n"
"s_cbranch_vccz .Lback_25\n"
"s_call_b64 s[52:53],.Lslow_0\n"
"s_branch .Lback_25\n"
".Lstub_26:\n"
"s_call_b64 s[52:53],.Lslow_1\n"
"s_branch .Lback_26\n"
".Lstub_27:\n"
"s_call_b64 s[52:53],.Lslow_0\n"
"s_branch .Lback_27\n"
".Lstub_28:\n"
"s_call_b64 s[52:53],.Lslow_1\n"
"s_branch .Lback_28\n"
".Lackspin_q:\n"
"s_cmp_eq_u32 s98,0\n"
"s_cbranch_scc1 .Lackok_q\n"
"s_sub_u32 s98,s98,1\n"
"s_sleep 8\n"
"v_bfe_u32 v14,v9,4,5\n"
"v_lshlrev_b32 v14,6,v14\n"
"s_lshl_b32 s58,s78,2\n"
"s_add_u32 s58,s58,s86\n"
"buffer_load_dword v10,v14,s[40:43],s58 offen offset:128 sc1\n"
"s_waitcnt vmcnt(0)\n"
"s_branch .Lack_q\n"
".Lpgep_g:\n"
"s_cbranch_execz .Lisr6\n"
".Lep:\n"
MF "v[72:75],v[16:19],v[136:139],v[32:35]\n"
"s_nop 0\n"
"ds_read_b128 v[24:27],v0 offset:1024\n"
"ds_read_b128 v[28:31],v1 offset:1024\n"
MF "v[72:75],v[20:23],v[156:159],v[72:75]\n"
MF "v[76:79],v[20:23],v[160:163],v[36:39]\n"
MF "v[76:79],v[16:19],v[140:143],v[76:79]\n"
MF "v[80:83],v[16:19],v[144:147],v[40:43]\n"
MF "v[80:83],v[20:23],v[164:167],v[80:83]\n"
MF "v[84:87],v[20:23],v[168:171],v[44:47]\n"
MF "v[84:87],v[16:19],v[148:151],v[84:87]\n"
"s_add_u32 m0,s65,49152\n"
MF "v[88:91],v[16:19],v[152:155],v[48:51]\n"
LD "v8,s[36:39],s73 offen offset:3072 nt lds\n"
MF "v[88:91],v[20:23],v[172:175],v[88:91]\n"
"s_waitcnt lgkmcnt(0)\n"
"ds_read_b128 v[16:19],v0 offset:2048\n"
"ds_read_b128 v[20:23],v1 offset:2048\n"
MF "v[88:91],v[24:27],v[192:195],v[88:91]\n"
"v_exp_f32 v124,v124\n"
MF "v[88:91],v[28:31],v[212:215],v[88:91]\n"
"s_add_u32 m0,s65,50048\n"
"v_exp_f32 v125,v125\n"
MF "v[84:87],v[28:31],v[208:211],v[84:87]\n"
LD "v8,s[36:39],s73 offen offset:3200 nt lds\n"
MF "v[84:87],v[24:27],v[188:191],v[84:87]\n"
"v_exp_f32 v126,v126\n"
MF "v[80:83],v[24:27],v[184:187],v[80:83]\n"
"v_exp_f32 v127,v127\n"
MF "v[80:83],v[28:31],v[204:207],v[80:83]\n"
"s_add_u32 m0,s65,50944\n"
"v_add_f32 v60,v60,v124\n"
"v_add_f32 v60,v60,v125\n"
MF "v[76:79],v[28:31],v[200:203],v[76:79]\n"
LD "v8,s[36:39],s73 offen offset:3328 nt lds\n"
MF "v[76:79],v[24:27],v[180:183],v[76:79]\n"
CV "v106,v124,v125\n"
"v_add_f32 v60,v60,v126\n"
MF "v[72:75],v[24:27],v[176:179],v[72:75]\n"
"v_add_f32 v60,v60,v127\n"
CV "v107,v126,v127\n"
MF "v[72:75],v[28:31],v[196:199],v[72:75]\n"
"s_waitcnt lgkmcnt(0)\n"
"ds_read_b128 v[24:27],v0 offset:6144\n"
"ds_read_b128 v[28:31],v1 offset:6144\n"
MF "v[72:75],v[16:19],v[216:219],v[72:75]\n"
"v_exp_f32 v128,v128\n"
MF "v[72:75],v[20:23],v[236:239],v[72:75]\n"
"v_exp_f32 v129,v129\n"
MF "v[76:79],v[20:23],v[240:243],v[76:79]\n"
"v_exp_f32 v130,v130\n"
MF "v[76:79],v[16:19],v[220:223],v[76:79]\n"
"v_exp_f32 v131,v131\n"
MF "v[80:83],v[16:19],v[224:227],v[80:83]\n"
"v_add_f32 v61,v61,v128\n"
"v_add_f32 v61,v61,v129\n"
MF "v[80:83],v[20:23],v[244:247],v[80:83]\n"
CV "v110,v128,v129\n"
"v_add_f32 v61,v61,v130\n"
MF "v[84:87],v[20:23],v[248:251],v[84:87]\n"
"v_add_f32 v61,v61,v131\n"
CV "v111,v130,v131\n"
MF "v[84:87],v[16:19],v[228:231],v[84:87]\n"
"v_max3_f32 v10,v57,v58,v59\n"
"v_max3_f32 v10,v10,v60,v61\n"
MF "v[88:91],v[16:19],v[232:235],v[88:91]\n"
"v_cmp_le_f32 vcc,0x47800000,v10\n"
MF "v[88:91],v[20:23],v[252:255],v[88:91]\n"
"s_waitcnt lgkmcnt(0)\n"
"s_cbranch_vccnz .Lstub_11\n"
".Lback_11:\n"
MF "v[112:115],v[24:27],v[136:139],v[32:35]\n"
"s_add_u32 s74,s74,12288\n"
"ds_read_b128 v[16:19],v0 offset:7168\n"
"ds_read_b128 v[20:23],v1 offset:7168\n"
MF "v[112:115],v[28:31],v[156:159],v[112:115]\n"
"s_cmp_eq_u32 s74,s20\n"
"s_cselect_b32 s74,s70,s74\n"
MF "v[116:119],v[28:31],v[160:163],v[36:39]\n"
"v_exp_f32 v72,v72\n"
MF "v[116:119],v[24:27],v[140:143],v[116:119]\n"
"v_exp_f32 v73,v73\n"
MF "v[120:123],v[24:27],v[144:147],v[40:43]\n"
"v_exp_f32 v74,v74\n"
MF "v[120:123],v[28:31],v[164:167],v[120:123]\n"
"v_exp_f32 v75,v75\n"
MF "v[124:127],v[28:31],v[168:171],v[44:47]\n"
"v_add_f32 v52,v57,v72\n"
"v_add_f32 v52,v52,v73\n"
MF "v[124:127],v[24:27],v[148:151],v[124:127]\n"
CV "v72,v72,v73\n"
"v_add_f32 v52,v52,v74\n"
MF "v[128:131],v[24:27],v[152:155],v[48:51]\n"
"v_add_f32 v52,v52,v75\n"
CV "v73,v74,v75\n"
MF "v[128:131],v[28:31],v[172:175],v[128:131]\n"
"s_waitcnt lgkmcnt(0)\n"
"ds_read_b128 v[24:27],v0 offset:8192\n"
"ds_read_b128 v[28:31],v1 offset:8192\n"
MF "v[128:131],v[16:19],v[192:195],v[128:131]\n"
"v_exp_f32 v76,v76\n"
MF "v[128:131],v[20:23],v[212:215],v[128:131]\n"
"v_exp_f32 v77,v77\n"
MF "v[124:127],v[20:23],v[208:211],v[124:127]\n"
"v_exp_f32 v78,v78\n"
MF "v[124:127],v[16:19],v[188:191],v[124:127]\n"
"v_exp_f32 v79,v79\n"
MF "v[120:123],v[16:19],v[184:187],v[120:123]\n"
"v_add_f32 v53,v58,v76\n"
"v_add_f32 v53,v53,v77\n"
MF "v[120:123],v[20:23],v[204:207],v[120:123]\n"
CV "v76,v76,v77\n"
"v_add_f32 v53,v53,v78\n"
MF "v[116:119],v[20:23],v[200:203],v[116:119]\n"
"s_waitcnt vmcnt(12) lgkmcnt(0)\n"
"v_add_f32 v53,v53,v79\n"
CV "v77,v78,v79\n"
MF "v[116:119],v[16:19],v[180:183],v[116:119]\n"
"s_barrier\n"
MF "v[112:115],v[16:19],v[176:179],v[112:115]\n"
"v_exp_f32 v80,v80\n"
MF "v[112:115],v[20:23],v[196:199],v[112:115]\n"
"s_sub_u32 s58,s71,s70\n"
TR "v[16:17],v2 offset:24576\n"
TR "v[18:19],v2 offset:30720\n"
MF "v[112:115],v[24:27],v[216:219],v[112:115]\n"
"s_add_u32 s72,s72,s58\n"
TR "v[20:21],v3 offset:24576\n"
TR "v[22:23],v3 offset:30720\n"
MF "v[112:115],v[28:31],v[236:239],v[112:115]\n"
"s_add_u32 s16,s16,s58\n"
"s_add_u32 s17,s17,s58\n"
"s_add_u32 s72,s72,6144\n"
MF "v[116:119],v[28:31],v[240:243],v[116:119]\n"
"s_cmp_eq_u32 s72,s17\n"
"s_cselect_b32 s72,s16,s72\n"
"s_add_u32 m0,s64,0\n"
MF "v[116:119],v[24:27],v[220:223],v[116:119]\n"
LD "v7,s[32:35],s72 offen nt lds\n"
MF "v[120:123],v[24:27],v[224:227],v[120:123]\n"
"s_add_u32 m0,s64,896\n"
"v_exp_f32 v81,v81\n"
MF "v[120:123],v[28:31],v[244:247],v[120:123]\n"
LD "v7,s[32:35],s72 offen offset:128 nt lds\n"
MF "v[124:127],v[28:31],v[248:251],v[124:127]\n"
"s_add_u32 m0,s64,1792\n"
"v_exp_f32 v82,v82\n"
MF "v[124:127],v[24:27],v[228:231],v[124:127]\n"
LD "v7,s[32:35],s72 offen offset:256 nt lds\n"
MF "v[128:131],v[24:27],v[232:235],v[128:131]\n"
"s_add_u32 s72,s72,6144\n"
"v_exp_f32 v83,v83\n"
MF "v[128:131],v[28:31],v[252:255],v[128:131]\n"
"s_waitcnt lgkmcnt(0)\n"
TR "v[24:25],v4 offset:24576\n"
TR "v[26:27],v4 offset:30720\n"
MF "a[0:3],v[16:19],v[92:95],a[0:3]\n"
"s_add_u32 m0,s64,6144\n"
TR "v[28:29],v5 offset:24576\n"
TR "v[30:31],v5 offset:30720\n"
MF "a[20:23],v[20:23],v[92:95],a[20:23]\n"
LD "v7,s[32:35],s72 offen nt lds\n"
MF "a[24:27],v[20:23],v[96:99],a[24:27]\n"
"v_add_f32 v54,v59,v80\n"
"v_add_f32 v54,v54,v81\n"
MF "a[4:7],v[16:19],v[96:99],a[4:7]\n"
"s_add_u32 m0,s64,7040\n"
CV "v80,v80,v81\n"
"v_add_f32 v54,v54,v82\n"
MF "a[8:11],v[16:19],v[100:103],a[8:11]\n"
LD "v7,s[32:35],s72 offen offset:128 nt lds\n"
MF "a[28:31],v[20:23],v[100:103],a[28:31]\n"
"v_add_f32 v54,v54,v83\n"
CV "v81,v82,v83\n"
MF "a[32:35],v[20:23],v[104:107],a[32:35]\n"
"s_add_u32 m0,s64,7936\n"
"v_exp_f32 v84,v84\n"
MF "a[12:15],v[16:19],v[104:107],a[12:15]\n"
LD "v7,s[32:35],s72 offen offset:256 nt lds\n"
MF "a[16:19],v[16:19],v[108:111],a[16:19]\n"
"v_exp_f32 v85,v85\n"
MF "a[36:39],v[20:23],v[108:111],a[36:39]\n"
"s_waitcnt lgkmcnt(0)\n"
TR "v[16:17],v2 offset:25600\n"
TR "v[18:19],v2 offset:31744\n"
MF "a[56:59],v[24:27],v[108:111],a[56:59]\n"
"s_nop 0\n"
TR "v[20:21],v3 offset:25600\n"
TR "v[22:23],v3 offset:31744\n"
MF "a[76:79],v[28:31],v[108:111],a[76:79]\n"
"v_exp_f32 v86,v86\n"
MF "a[72:75],v[28:31],v[104:107],a[72:75]\n"
"v_exp_f32 v87,v87\n"
MF "a[52:55],v[24:27],v[104:107],a[52:55]\n"
"v_add_f32 v55,v60,v84\n"
"v_add_f32 v55,v55,v85\n"
MF "a[48:51],v[24:27],v[100:103],a[48:51]\n"
CV "v84,v84,v85\n"
"v_add_f32 v55,v55,v86\n"
MF "a[68:71],v[28:31],v[100:103],a[68:71]\n"
"v_add_f32 v55,v55,v87\n"
CV "v85,v86,v87\n"
MF "a[64:67],v[28:31],v[96:99],a[64:67]\n"
"v_exp_f32 v88,v88\n"
MF "a[44:47],v[24:27],v[96:99],a[44:47]\n"
"v_exp_f32 v89,v89\n"
MF "a[40:43],v[24:27],v[92:95],a[40:43]\n"
"v_exp_f32 v90,v90\n"
MF "a[60:63],v[28:31],v[92:95],a[60:63]\n"
"s_waitcnt lgkmcnt(0)\n"
TR "v[24:25],v4 offset:25600\n"
TR "v[26:27],v4 offset:31744\n"
MF "a[80:83],v[16:19],v[92:95],a[80:83]\n"
"s_nop 0\n"
TR "v[28:29],v5 offset:25600\n"
TR "v[30:31],v5 offset:31744\n"
MF "a[100:103],v[20:23],v[92:95],a[100:103]\n"
"v_exp_f32 v91,v91\n"
MF "a[104:107],v[20:23],v[96:99],a[104:107]\n"
"v_add_f32 v56,v61,v88\n"
"v_add_f32 v56,v56,v89\n"
MF "a[84:87],v[16:19],v[96:99],a[84:87]\n"
CV "v88,v88,v89\n"
"v_add_f32 v56,v56,v90\n"
MF "a[88:91],v[16:19],v[100:103],a[88:91]\n"
"v_add_f32 v56,v56,v91\n"
CV "v89,v90,v91\n"
MF "a[108:111],v[20:23],v[100:103],a[108:111]\n"
"v_exp_f32 v112,v112\n"
MF "a[112:115],v[20:23],v[104:107],a[112:115]\n"
"v_exp_f32 v113,v113\n"
MF "a[92:95],v[16:19],v[104:107],a[92:95]\n"
"v_exp_f32 v114,v114\n"
MF "a[96:99],v[16:19],v[108:111],a[96:99]\n"
"v_exp_f32 v115,v115\n"
MF "a[116:119],v[20:23],v[108:111],a[116:119]\n"
"s_waitcnt lgkmcnt(0)\n"
TR "v[16:17],v2 offset:26624\n"
TR "v[18:19],v2 offset:32768\n"
MF "a[136:139],v[24:27],v[108:111],a[136:139]\n"
"s_nop 0\n"
TR "v[20:21],v3 offset:26624\n"
TR "v[22:23],v3 offset:32768\n"
MF "a[156:159],v[28:31],v[108:111],a[156:159]\n"
"v_add_f32 v52,v52,v112\n"
"v_add_f32 v52,v52,v113\n"
MF "a[152:155],v[28:31],v[104:107],a[152:155]\n"
CV "v74,v112,v113\n"
"v_add_f32 v52,v52,v114\n"
MF "a[132:135],v[24:27],v[104:107],a[132:135]\n"
"v_add_f32 v52,v52,v115\n"
CV "v75,v114,v115\n"
MF "a[128:131],v[24:27],v[100:103],a[128:131]\n"
"v_exp_f32 v116,v116\n"
MF "a[148:151],v[28:31],v[100:103],a[148:151]\n"
"v_exp_f32 v117,v117\n"
MF "a[144:147],v[28:31],v[96:99],a[144:147]\n"
"v_exp_f32 v118,v118\n"
MF "a[124:127],v[24:27],v[96:99],a[124:127]\n"
"v_exp_f32 v119,v119\n"
MF "a[120:123],v[24:27],v[92:95],a[120:123]\n"
"v_add_f32 v53,v53,v116\n"
"v_add_f32 v53,v53,v117\n"
MF "a[140:143],v[28:31],v[92:95],a[140:143]\n"
"s_waitcnt lgkmcnt(0)\n"
TR "v[24:25],v4 offset:26624\n"
TR "v[26:27],v4 offset:32768\n"
MF "a[160:163],v[16:19],v[92:95],a[160:163]\n"
"s_nop 0\n"
TR "v[28:29],v5 offset:26624\n"
TR "v[30:31],v5 offset:32768\n"
MF "a[180:183],v[20:23],v[92:95],a[180:183]\n"
CV "v78,v116,v117\n"
"v_add_f32 v53,v53,v118\n"
MF "a[184:187],v[20:23],v[96:99],a[184:187]\n"
"v_add_f32 v53,v53,v119\n"
CV "v79,v118,v119\n"
MF "a[164:167],v[16:19],v[96:99],a[164:167]\n"
"v_exp_f32 v120,v120\n"
MF "a[168:171],v[16:19],v[100:103],a[168:171]\n"
"v_exp_f32 v121,v121\n"
MF "a[188:191],v[20:23],v[100:103],a[188:191]\n"
"v_exp_f32 v122,v122\n"
MF "a[192:195],v[20:23],v[104:107],a[192:195]\n"
"v_exp_f32 v123,v123\n"
MF "a[172:175],v[16:19],v[104:107],a[172:175]\n"
"s_waitcnt vmcnt(12) lgkmcnt(0)\n"
"v_add_f32 v54,v54,v120\n"
"v_add_f32 v54,v54,v121\n"
MF "a[176:179],v[16:19],v[108:111],a[176:179]\n"
"s_barrier\n"
MF "a[196:199],v[20:23],v[108:111],a[196:199]\n"
"s_add_u32 s73,s73,12288\n"
"ds_read_b128 v[16:19],v0 offset:24576\n"
"ds_read_b128 v[20:23],v1 offset:24576\n"
MF "a[216:219],v[24:27],v[108:111],a[216:219]\n"
"s_cmp_eq_u32 s73,s19\n"
"s_cselect_b32 s73,s18,s73\n"
"s_add_u32 m0,s65,73728\n"
MF "a[236:239],v[28:31],v[108:111],a[236:239]\n"
LD "v8,s[36:39],s73 offen nt lds\n"
MF "a[232:235],v[28:31],v[104:107],a[232:235]\n"
"s_add_u32 m0,s65,74624\n"
CV "v82,v120,v121\n"
"v_add_f32 v54,v54,v122\n"
MF "a[212:215],v[24:27],v[104:107],a[212:215]\n"
LD "v8,s[36:39],s73 offen offset:128 nt lds\n"
MF "a[208:211],v[24:27],v[100:103],a[208:211]\n"
"s_add_u32 m0,s65,75520\n"
"v_add_f32 v54,v54,v123\n"
CV "v83,v122,v123\n"
MF "a[228:231],v[28:31],v[100:103],a[228:231]\n"
LD "v8,s[36:39],s73 offen offset:256 nt lds\n"
MF "a[224:227],v[28:31],v[96:99],a[224:227]\n"
MF "a[204:207],v[24:27],v[96:99],a[204:207]\n"
MF "a[200:203],v[24:27],v[92:95],a[200:203]\n"
MF "a[220:223],v[28:31],v[92:95],a[220:223]\n"
"s_waitcnt lgkmcnt(0)\n"
MF "v[92:95],v[16:19],v[136:139],v[32:35]\n"
"s_add_u32 m0,s65,73728\n"
"ds_read_b128 v[24:27],v0 offset:25600\n"
"ds_read_b128 v[28:31],v1 offset:25600\n"
MF "v[92:95],v[20:23],v[156:159],v[92:95]\n"
LD "v8,s[36:39],s73 offen offset:3072 nt lds\n"
MF "v[96:99],v[20:23],v[160:163],v[36:39]\n"
"v_exp_f32 v124,v124\n"
MF "v[96:99],v[16:19],v[140:143],v[96:99]\n"
"v_exp_f32 v125,v125\n"
MF "v[100:103],v[16:19],v[144:147],v[40:43]\n"
"s_add_u32 m0,s65,74624\n"
"v_exp_f32 v126,v126\n"
MF "v[100:103],v[20:23],v[164:167],v[100:103]\n"
LD "v8,s[36:39],s73 offen offset:3200 nt lds\n"
MF "v[104:107],v[20:23],v[168:171],v[44:47]\n"
"v_exp_f32 v127,v127\n"
MF "v[104:107],v[16:19],v[148:151],v[104:107]\n"
"s_add_u32 m0,s65,75520\n"
"v_add_f32 v55,v55,v124\n"
"v_add_f32 v55,v55,v125\n"
MF "v[108:111],v[16:19],v[152:155],v[48:51]\n"
LD "v8,s[36:39],s73 offen offset:3328 nt lds\n"
MF "v[108:111],v[20:23],v[172:175],v[108:111]\n"
"s_waitcnt lgkmcnt(0)\n"
"ds_read_b128 v[16:19],v0 offset:26624\n"
"ds_read_b128 v[20:23],v1 offset:26624\n"
MF "v[108:111],v[24:27],v[192:195],v[108:111]\n"
CV "v86,v124,v125\n"
"v_add_f32 v55,v55,v126\n"
MF "v[108:111],v[28:31],v[212:215],v[108:111]\n"
"v_add_f32 v55,v55,v127\n"
CV "v87,v126,v127\n"
MF "v[104:107],v[28:31],v[208:211],v[104:107]\n"
"v_exp_f32 v128,v128\n"
MF "v[104:107],v[24:27],v[188:191],v[104:107]\n"
"v_exp_f32 v129,v129\n"
MF "v[100:103],v[24:27],v[184:187],v[100:103]\n"
"v_exp_f32 v130,v130\n"
MF "v[100:103],v[28:31],v[204:207],v[100:103]\n"
"v_exp_f32 v131,v131\n"
MF "v[96:99],v[28:31],v[200:203],v[96:99]\n"
"v_add_f32 v56,v56,v128\n"
"v_add_f32 v56,v56,v129\n"
MF "v[96:99],v[24:27],v[180:183],v[96:99]\n"
CV "v90,v128,v129\n"
"v_add_f32 v56,v56,v130\n"
MF "v[92:95],v[24:27],v[176:179],v[92:95]\n"
"v_add_f32 v56,v56,v131\n"
CV "v91,v130,v131\n"
MF "v[92:95],v[28:31],v[196:199],v[92:95]\n"
"s_waitcnt lgkmcnt(0)\n"
"ds_read_b128 v[24:27],v0 offset:30720\n"
"ds_read_b128 v[28:31],v1 offset:30720\n"
MF "v[92:95],v[16:19],v[216:219],v[92:95]\n"
MF "v[92:95],v[20:23],v[236:239],v[92:95]\n"
MF "v[96:99],v[20:23],v[240:243],v[96:99]\n"
MF "v[96:99],v[16:19],v[220:223],v[96:99]\n"
MF "v[100:103],v[16:19],v[224:227],v[100:103]\n"
MF "v[100:103],v[20:23],v[244:247],v[100:103]\n"
MF "v[104:107],v[20:23],v[248:251],v[104:107]\n"
MF "v[104:107],v[16:19],v[228:231],v[104:107]\n"
"v_max3_f32 v10,v52,v53,v54\n"
"v_max3_f32 v10,v10,v55,v56\n"
MF "v[108:111],v[16:19],v[232:235],v[108:111]\n"
".Lpg16g:\n"
"s_cbranch_execz .Lisr6\n"
"v_cmp_le_f32 vcc,0x47800000,v10\n"
MF "v[108:111],v[20:23],v[252:255],v[108:111]\n"
"s_waitcnt lgkmcnt(0)\n"
"s_cbranch_vccnz .Lstub_12\n"
".Lback_12:\n"
MF "v[112:115],v[24:27],v[136:139],v[32:35]\n"
"s_add_u32 s74,s74,12288\n"
"ds_read_b128 v[16:19],v0 offset:31744\n"
"ds_read_b128 v[20:23],v1 offset:31744\n"
MF "v[112:115],v[28:31],v[156:159],v[112:115]\n"
"s_cmp_eq_u32 s74,s20\n"
"s_cselect_b32 s74,s70,s74\n"
MF "v[116:119],v[28:31],v[160:163],v[36:39]\n"
"v_exp_f32 v92,v92\n"
MF "v[116:119],v[24:27],v[140:143],v[116:119]\n"
"v_exp_f32 v93,v93\n"
MF "v[120:123],v[24:27],v[144:147],v[40:43]\n"
"v_exp_f32 v94,v94\n"
MF "v[120:123],v[28:31],v[164:167],v[120:123]\n"
"v_exp_f32 v95,v95\n"
MF "v[124:127],v[28:31],v[168:171],v[44:47]\n"
"v_add_f32 v57,v52,v92\n"
"v_add_f32 v57,v57,v93\n"
MF "v[124:127],v[24:27],v[148:151],v[124:127]\n"
CV "v92,v92,v93\n"
"v_add_f32 v57,v57,v94\n"
MF "v[128:131],v[24:27],v[152:155],v[48:51]\n"
"v_add_f32 v57,v57,v95\n"
CV "v93,v94,v95\n"
MF "v[128:131],v[28:31],v[172:175],v[128:131]\n"
"s_waitcnt lgkmcnt(0)\n"
"ds_read_b128 v[24:27],v0 offset:32768\n"
"ds_read_b128 v[28:31],v1 offset:32768\n"
MF "v[128:131],v[16:19],v[192:195],v[128:131]\n"
"v_exp_f32 v96,v96\n"
MF "v[128:131],v[20:23],v[212:215],v[128:131]\n"
"v_exp_f32 v97,v97\n"
MF "v[124:127],v[20:23],v[208:211],v[124:127]\n"
"v_exp_f32 v98,v98\n"
MF "v[124:127],v[16:19],v[188:191],v[124:127]\n"
"v_exp_f32 v99,v99\n"
MF "v[120:123],v[16:19],v[184:187],v[120:123]\n"
"v_add_f32 v58,v53,v96\n"
"v_add_f32 v58,v58,v97\n"
MF "v[120:123],v[20:23],v[204:207],v[120:123]\n"
CV "v96,v96,v97\n"
"v_add_f32 v58,v58,v98\n"
MF "v[116:119],v[20:23],v[200:203],v[116:119]\n"
"s_waitcnt vmcnt(12) lgkmcnt(0)\n"
"v_add_f32 v58,v58,v99\n"
CV "v97,v98,v99\n"
MF "v[116:119],v[16:19],v[180:183],v[116:119]\n"
"s_barrier\n"
MF "v[112:115],v[16:19],v[176:179],v[112:115]\n"
"v_exp_f32 v100,v100\n"
MF "v[112:115],v[20:23],v[196:199],v[112:115]\n"
"s_nop 0\n"
TR "v[16:17],v2\n"
TR "v[18:19],v2 offset:6144\n"
MF "v[112:115],v[24:27],v[216:219],v[112:115]\n"
"s_add_u32 s72,s72,6144\n"
TR "v[20:21],v3\n"
TR "v[22:23],v3 offset:6144\n"
MF "v[112:115],v[28:31],v[236:239],v[112:115]\n"
"s_cmp_eq_u32 s72,s17\n"
"s_cselect_b32 s72,s16,s72\n"
"s_add_u32 m0,s64,24576\n"
MF "v[116:119],v[28:31],v[240:243],v[116:119]\n"
LD "v7,s[32:35],s72 offen nt lds\n"
MF "v[116:119],v[24:27],v[220:223],v[116:119]\n"
"s_add_u32 m0,s64,25472\n"
"v_exp_f32 v101,v101\n"
MF "v[120:123],v[24:27],v[224:227],v[120:123]\n"
LD "v7,s[32:35],s72 offen offset:128 nt lds\n"
MF "v[120:123],v[28:31],v[244:247],v[120:123]\n"
"v_exp_f32 v102,v102\n"
MF "v[124:127],v[28:31],v[248:251],v[124:127]\n"
"s_add_u32 m0,s64,26368\n"
"v_exp_f32 v103,v103\n"
MF "v[124:127],v[24:27],v[228:231],v[124:127]\n"
LD "v7,s[32:35],s72 offen offset:256 nt lds\n"
MF "v[128:131],v[24:27],v[232:235],v[128:131]\n"
"s_add_u32 s72,s72,6144\n"
"v_add_f32 v59,v54,v100\n"
"v_add_f32 v59,v59,v101\n"
MF "v[128:131],v[28:31],v[252:255],v[128:131]\n"
"s_waitcnt lgkmcnt(0)\n"
TR "v[24:25],v4\n"
TR "v[26:27],v4 offset:6144\n"
MF "a[0:3],v[16:19],v[72:75],a[0:3]\n"
"s_add_u32 m0,s64,30720\n"
TR "v[28:29],v5\n"
TR "v[30:31],v5 offset:6144\n"
MF "a[20:23],v[20:23],v[72:75],a[20:23]\n"
LD "v7,s[32:35],s72 offen nt lds\n"
MF "a[24:27],v[20:23],v[76:79],a[24:27]\n"
CV "v100,v100,v101\n"
"v_add_f32 v59,v59,v102\n"
MF "a[4:7],v[16:19],v[76:79],a[4:7]\n"
"s_add_u32 m0,s64,31616\n"
"v_add_f32 v59,v59,v103\n"
CV "v101,v102,v103\n"
MF "a[8:11],v[16:19],v[80:83],a[8:11]\n"
LD "v7,s[32:35],s72 offen offset:128 nt lds\n"
MF "a[28:31],v[20:23],v[80:83],a[28:31]\n"
"v_exp_f32 v104,v104\n"
MF "a[32:35],v[20:23],v[84:87],a[32:35]\n"
"s_add_u32 m0,s64,32512\n"
"v_exp_f32 v105,v105\n"
MF "a[12:15],v[16:19],v[84:87],a[12:15]\n"
LD "v7,s[32:35],s72 offen offset:256 nt lds\n"
MF "a[16:19],v[16:19],v[88:91],a[16:19]\n"
"v_exp_f32 v106,v106\n"
MF "a[36:39],v[20:23],v[88:91],a[36:39]\n"
"s_waitcnt lgkmcnt(0)\n"
TR "v[16:17],v2 offset:1024\n"
TR "v[18:19],v2 offset:7168\n"
MF "a[56:59],v[24:27],v[88:91],a[56:59]\n"
"s_nop 0\n"
TR "v[20:21],v3 offset:1024\n"
TR "v[22:23],v3 offset:7168\n"
MF "a[76:79],v[28:31],v[88:91],a[76:79]\n"
"v_exp_f32 v107,v107\n"
MF "a[72:75],v[28:31],v[84:87],a[72:75]\n"
"v_add_f32 v60,v55,v104\n"
"v_add_f32 v60,v60,v105\n"
MF "a[52:55],v[24:27],v[84:87],a[52:55]\n"
CV "v104,v104,v105\n"
"v_add_f32 v60,v60,v106\n"
MF "a[48:51],v[24:27],v[80:83],a[48:51]\n"
"v_add_f32 v60,v60,v107\n"
CV "v105,v106,v107\n"
MF "a[68:71],v[28:31],v[80:83],a[68:71]\n"
"v_exp_f32 v108,v108\n"
MF "a[64:67],v[28:31],v[76:79],a[64:67]\n"
"v_exp_f32 v109,v109\n"
MF "a[44:47],v[24:27],v[76:79],a[44:47]\n"
"v_exp_f32 v110,v110\n"
MF "a[40:43],v[24:27],v[72:75],a[40:43]\n"
"v_exp_f32 v111,v111\n"
MF "a[60:63],v[28:31],v[72:75],a[60:63]\n"
"s_waitcnt lgkmcnt(0)\n"
TR "v[24:25],v4 offset:1024\n"
TR "v[26:27],v4 offset:7168\n"
MF "a[80:83],v[16:19],v[72:75],a[80:83]\n"
"s_nop 0\n"
TR "v[28:29],v5 offset:1024\n"
TR "v[30:31],v5 offset:7168\n"
MF "a[100:103],v[20:23],v[72:75],a[100:103]\n"
"v_add_f32 v61,v56,v108\n"
"v_add_f32 v61,v61,v109\n"
MF "a[104:107],v[20:23],v[76:79],a[104:107]\n"
CV "v108,v108,v109\n"
"v_add_f32 v61,v61,v110\n"
MF "a[84:87],v[16:19],v[76:79],a[84:87]\n"
"v_add_f32 v61,v61,v111\n"
CV "v109,v110,v111\n"
MF "a[88:91],v[16:19],v[80:83],a[88:91]\n"
"v_exp_f32 v112,v112\n"
MF "a[108:111],v[20:23],v[80:83],a[108:111]\n"
"v_exp_f32 v113,v113\n"
MF "a[112:115],v[20:23],v[84:87],a[112:115]\n"
"v_exp_f32 v114,v114\n"
MF "a[92:95],v[16:19],v[84:87],a[92:95]\n"
"v_exp_f32 v115,v115\n"
MF "a[96:99],v[16:19],v[88:91],a[96:99]\n"
"v_add_f32 v57,v57,v112\n"
"v_add_f32 v57,v57,v113\n"
MF "a[116:119],v[20:23],v[88:91],a[116:119]\n"
"s_waitcnt lgkmcnt(0)\n"
TR "v[16:17],v2 offset:2048\n"
TR "v[18:19],v2 offset:8192\n"
MF "a[136:139],v[24:27],v[88:91],a[136:139]\n"
"s_nop 0\n"
TR "v[20:21],v3 offset:2048\n"
TR "v[22:23],v3 offset:8192\n"
MF "a[156:159],v[28:31],v[88:91],a[156:159]\n"
CV "v94,v112,v113\n"
"v_add_f32 v57,v57,v114\n"
MF "a[152:155],v[28:31],v[84:87],a[152:155]\n"
"v_add_f32 v57,v57,v115\n"
CV "v95,v114,v115\n"
MF "a[132:135],v[24:27],v[84:87],a[132:135]\n"
"v_exp_f32 v116,v116\n"
MF "a[128:131],v[24:27],v[80:83],a[128:131]\n"
"v_exp_f32 v117,v117\n"
MF "a[148:151],v[28:31],v[80:83],a[148:151]\n"
"v_exp_f32 v118,v118\n"
MF "a[144:147],v[28:31],v[76:79],a[144:147]\n"
"v_exp_f32 v119,v119\n"
MF "a[124:127],v[24:27],v[76:79],a[124:127]\n"
"v_add_f32 v58,v58,v116\n"
"v_add_f32 v58,v58,v117\n"
MF "a[120:123],v[24:27],v[72:75],a[120:123]\n"
CV "v98,v116,v117\n"
"v_add_f32 v58,v58,v118\n"
MF "a[140:143],v[28:31],v[72:75],a[140:143]\n"
"s_waitcnt lgkmcnt(0)\n"
TR "v[24:25],v4 offset:2048\n"
TR "v[26:27],v4 offset:8192\n"
MF "a[160:163],v[16:19],v[72:75],a[160:163]\n"
"s_nop 0\n"
TR "v[28:29],v5 offset:2048\n"
TR "v[30:31],v5 offset:8192\n"
MF "a[180:183],v[20:23],v[72:75],a[180:183]\n"
"v_add_f32 v58,v58,v119\n"
CV "v99,v118,v119\n"
MF "a[184:187],v[20:23],v[76:79],a[184:187]\n"
"v_exp_f32 v120,v120\n"
MF "a[164:167],v[16:19],v[76:79],a[164:167]\n"
"v_exp_f32 v121,v121\n"
MF "a[168:171],v[16:19],v[80:83],a[168:171]\n"
"v_exp_f32 v122,v122\n"
MF "a[188:191],v[20:23],v[80:83],a[188:191]\n"
"v_exp_f32 v123,v123\n"
MF "a[192:195],v[20:23],v[84:87],a[192:195]\n"
"v_add_f32 v59,v59,v120\n"
"v_add_f32 v59,v59,v121\n"
MF "a[172:175],v[16:19],v[84:87],a[172:175]\n"
"s_waitcnt lgkmcnt(0)\n"
CV "v102,v120,v121\n"
"v_add_f32 v59,v59,v122\n"
MF "a[176:179],v[16:19],v[88:91],a[176:179]\n"
"s_barrier\n"
MF "a[196:199],v[20:23],v[88:91],a[196:199]\n"
"s_sub_u32 s58,s71,s70\n"
"s_add_u32 s73,s73,s58\n"
"v_add_f32 v59,v59,v123\n"
MF "a[216:219],v[24:27],v[88:91],a[216:219]\n"
"s_add_u32 s18,s18,s58\n"
"s_add_u32 s19,s19,s58\n"
"s_add_u32 s73,s73,12288\n"
MF "a[236:239],v[28:31],v[88:91],a[236:239]\n"
"s_cmp_eq_u32 s73,s19\n"
"s_cselect_b32 s73,s18,s73\n"
"s_add_u32 m0,s65,49152\n"
MF "a[232:235],v[28:31],v[84:87],a[232:235]\n"
LD "v8,s[36:39],s73 offen nt lds\n"
MF "a[212:215],v[24:27],v[84:87],a[212:215]\n"
"s_add_u32 m0,s65,50048\n"
CV "v103,v122,v123\n"
MF "a[208:211],v[24:27],v[80:83],a[208:211]\n"
LD "v8,s[36:39],s73 offen offset:128 nt lds\n"
MF "a[228:231],v[28:31],v[80:83],a[228:231]\n"
"s_add_u32 m0,s65,50944\n"
"v_exp_f32 v124,v124\n"
MF "a[224:227],v[28:31],v[76:79],a[224:227]\n"
LD "v8,s[36:39],s73 offen offset:256 nt lds\n"
MF "a[204:207],v[24:27],v[76:79],a[204:207]\n"
"v_exp_f32 v125,v125\n"
MF "a[200:203],v[24:27],v[72:75],a[200:203]\n"
"v_exp_f32 v126,v126\n"
MF "a[220:223],v[28:31],v[72:75],a[220:223]\n"
"v_exp_f32 v127,v127\n"
"v_add_f32 v60,v60,v124\n"
"v_add_f32 v60,v60,v125\n"
CV "v106,v124,v125\n"
"v_add_f32 v60,v60,v126\n"
"v_add_f32 v60,v60,v127\n"
CV "v107,v126,v127\n"
"v_exp_f32 v128,v128\n"
"v_exp_f32 v129,v129\n"
"v_exp_f32 v130,v130\n"
"v_exp_f32 v131,v131\n"
"v_add_f32 v61,v61,v128\n"
"v_add_f32 v61,v61,v129\n"
CV "v110,v128,v129\n"
"v_add_f32 v61,v61,v130\n"
"v_add_f32_e64 v61,v61,v131\n"
CV "v111,v130,v131\n"
"v_max3_f32 v10,v57,v58,v59\n"
"v_max3_f32 v10,v10,v60,v61\n"
"v_cmp_le_f32 vcc,0x47800000,v10\n"
"s_cbranch_vccnz .Lstub_13\n"
".Lback_13:\n"
"s_waitcnt vmcnt(9) lgkmcnt(0)\n"
"s_barrier\n"
"s_lshl_b32 s58,s56,10\n"
"s_add_u32 s58,s58,73728\n"
"v_add_u32 v120,s58,v9\n"
"v_add_u32 v121,65536,v120\n"
"s_sub_u32 s59,s71,s70\n"
"v_mov_b32 v10,1.0\n"
TR "v[136:137],v2 offset:24576\n"
TR "v[138:139],v2 offset:30720\n"
"v_mfma_f32_16x16x4_f32 v[16:19],v10,v57,0\n"
TR "v[140:141],v3 offset:24576\n"
TR "v[142:143],v3 offset:30720\n"
TR "v[144:145],v4 offset:24576\n"
TR "v[146:147],v4 offset:30720\n"
TR "v[148:149],v5 offset:24576\n"
TR "v[150:151],v5 offset:30720\n"
"v_mfma_f32_16x16x4_f32 v[20:23],v10,v58,0\n"
TR "v[152:153],v2 offset:25600\n"
TR "v[154:155],v2 offset:31744\n"
TR "v[156:157],v3 offset:25600\n"
TR "v[158:159],v3 offset:31744\n"
TR "v[160:161],v4 offset:25600\n"
TR "v[162:163],v4 offset:31744\n"
"v_mfma_f32_16x16x4_f32 v[24:27],v10,v59,0\n"
TR "v[164:165],v5 offset:25600\n"
TR "v[166:167],v5 offset:31744\n"
TR "v[168:169],v2 offset:26624\n"
TR "v[170:171],v2 offset:32768\n"
TR "v[172:173],v3 offset:26624\n"
TR "v[174:175],v3 offset:32768\n"
"v_mfma_f32_16x16x4_f32 v[28:31],v10,v60,0\n"
TR "v[176:177],v4 offset:26624\n"
TR "v[178:179],v4 offset:32768\n"
TR "v[180:181],v5 offset:26624\n"
TR "v[182:183],v5 offset:32768\n"
"v_mfma_f32_16x16x4_f32 v[32:35],v10,v61,0\n"
"s_add_u32 m0,s65,49152\n"
"s_nop 0\n"
LD "v8,s[36:39],s73 offen offset:3072 nt lds\n"
"s_add_u32 m0,s65,50048\n"
"s_nop 0\n"
LD "v8,s[36:39],s73 offen offset:3200 nt lds\n"
"s_waitcnt lgkmcnt(15)\n"
MF "a[0:3],v[136:139],v[92:95],a[0:3]\n"
MF "a[20:23],v[140:143],v[92:95],a[20:23]\n"
"s_add_u32 m0,s65,50944\n"
"s_nop 0\n"
LD "v8,s[36:39],s73 offen offset:3328 nt lds\n"
"s_sub_u32 s74,s96,s16\n"
"s_add_u32 s74,s74,s71\n"
"s_add_u32 s20,s20,s59\n"
"s_mov_b32 s70,s71\n"
LD "v[232:235],v9,s[40:43],s75 offen sc1\n"
"v_rcp_f32 v57,v16\n"
"v_rcp_f32 v58,v20\n"
"v_rcp_f32 v59,v24\n"
"v_rcp_f32 v60,v28\n"
"v_rcp_f32 v61,v32\n"
MF "a[4:7],v[136:139],v[96:99],a[4:7]\n"
"v_mul_f32_e64 v52,-s13,v57\n"
"v_mul_f32_e64 v53,-s13,v58\n"
"v_mul_f32_e64 v54,-s13,v59\n"
"v_mul_f32_e64 v55,-s13,v60\n"
"v_mul_f32_e64 v56,-s13,v61\n"
AR "v72,a0\n"
AR "v73,a1\n"
AR "v74,a2\n"
MF "a[24:27],v[140:143],v[96:99],a[24:27]\n"
AR "v75,a3\n"
AR "v76,a20\n"
AR "v77,a21\n"
AR "v78,a22\n"
AR "v79,a23\n"
"v_pk_fma_f32 v[72:73],v[72:73],v[56:57],v[52:53] op_sel:[0,1,0] op_sel_hi:[1,1,0]\n"
"v_pk_fma_f32 v[74:75],v[74:75],v[56:57],v[52:53] op_sel:[0,1,0] op_sel_hi:[1,1,0]\n"
"v_pk_fma_f32 v[76:77],v[76:77],v[56:57],v[52:53] op_sel:[0,1,0] op_sel_hi:[1,1,0]\n"
"v_pk_fma_f32 v[78:79],v[78:79],v[56:57],v[52:53] op_sel:[0,1,0] op_sel_hi:[1,1,0]\n"
CV "v112,v72,v73\n"
CV "v113,v74,v75\n"
CV "v114,v76,v77\n"
CV "v115,v78,v79\n"
MF "a[8:11],v[136:139],v[100:103],a[8:11]\n"
"ds_read_b128 v[184:187],v120 offset:24576\n"
"ds_write_b128 v120,v[112:115] offset:24576\n"
LD "v[236:239],v9,s[40:43],s75 offen offset:1024 sc1\n"
AR "v80,a4\n"
AR "v81,a5\n"
AR "v82,a6\n"
MF "a[28:31],v[140:143],v[100:103],a[28:31]\n"
AR "v83,a7\n"
AR "v84,a24\n"
AR "v85,a25\n"
AR "v86,a26\n"
AR "v87,a27\n"
"v_pk_fma_f32 v[80:81],v[80:81],v[58:59],v[52:53] op_sel:[0,0,1] op_sel_hi:[1,0,1]\n"
"v_pk_fma_f32 v[82:83],v[82:83],v[58:59],v[52:53] op_sel:[0,0,1] op_sel_hi:[1,0,1]\n"
"v_pk_fma_f32 v[84:85],v[84:85],v[58:59],v[52:53] op_sel:[0,0,1] op_sel_hi:[1,0,1]\n"
"v_pk_fma_f32 v[86:87],v[86:87],v[58:59],v[52:53] op_sel:[0,0,1] op_sel_hi:[1,0,1]\n"
CV "v116,v80,v81\n"
CV "v117,v82,v83\n"
CV "v118,v84,v85\n"
CV "v119,v86,v87\n"
MF "a[12:15],v[136:139],v[104:107],a[12:15]\n"
"ds_read_b128 v[188:191],v120 offset:28672\n"
"ds_write_b128 v120,v[116:119] offset:28672\n"
AR "v72,a8\n"
AR "v73,a9\n"
AR "v74,a10\n"
MF "a[32:35],v[140:143],v[104:107],a[32:35]\n"
AR "v75,a11\n"
AR "v76,a28\n"
AR "v77,a29\n"
AR "v78,a30\n"
AR "v79,a31\n"
"v_pk_fma_f32 v[72:73],v[72:73],v[58:59],v[54:55] op_sel:[0,1,0] op_sel_hi:[1,1,0]\n"
"v_pk_fma_f32 v[74:75],v[74:75],v[58:59],v[54:55] op_sel:[0,1,0] op_sel_hi:[1,1,0]\n"
"v_pk_fma_f32 v[76:77],v[76:77],v[58:59],v[54:55] op_sel:[0,1,0] op_sel_hi:[1,1,0]\n"
"v_pk_fma_f32 v[78:79],v[78:79],v[58:59],v[54:55] op_sel:[0,1,0] op_sel_hi:[1,1,0]\n"
CV "v112,v72,v73\n"
CV "v113,v74,v75\n"
CV "v114,v76,v77\n"
CV "v115,v78,v79\n"
MF "a[16:19],v[136:139],v[108:111],a[16:19]\n"
"ds_read_b128 v[192:195],v120 offset:32768\n"
"ds_write_b128 v120,v[112:115] offset:32768\n"
"s_waitcnt lgkmcnt(0)\n"
"s_barrier\n"
LD "v[240:243],v9,s[40:43],s75 offen offset:2048 sc1\n"
AR "v80,a12\n"
AR "v81,a13\n"
AR "v82,a14\n"
MF "a[36:39],v[140:143],v[108:111],a[36:39]\n"
AR "v83,a15\n"
AR "v84,a32\n"
AR "v85,a33\n"
AR "v86,a34\n"
AR "v87,a35\n"
"v_pk_fma_f32 v[80:81],v[80:81],v[60:61],v[54:55] op_sel:[0,0,1] op_sel_hi:[1,0,1]\n"
"v_pk_fma_f32 v[82:83],v[82:83],v[60:61],v[54:55] op_sel:[0,0,1] op_sel_hi:[1,0,1]\n"
"v_pk_fma_f32 v[84:85],v[84:85],v[60:61],v[54:55] op_sel:[0,0,1] op_sel_hi:[1,0,1]\n"
"v_pk_fma_f32 v[86:87],v[86:87],v[60:61],v[54:55] op_sel:[0,0,1] op_sel_hi:[1,0,1]\n"
CV "v116,v80,v81\n"
CV "v117,v82,v83\n"
CV "v118,v84,v85\n"
CV "v119,v86,v87\n"
MF "a[40:43],v[144:147],v[92:95],a[40:43]\n"
"ds_read_b128 v[196:199],v120 offset:36864\n"
"ds_write_b128 v120,v[116:119] offset:36864\n"
LD "v[244:247],v9,s[40:43],s75 offen offset:3072 sc1\n"
AR "v72,a16\n"
AR "v73,a17\n"
AR "v74,a18\n"
MF "a[60:63],v[148:151],v[92:95],a[60:63]\n"
AR "v75,a19\n"
AR "v76,a36\n"
AR "v77,a37\n"
AR "v78,a38\n"
AR "v79,a39\n"
"v_pk_fma_f32 v[72:73],v[72:73],v[60:61],v[56:57] op_sel:[0,1,0] op_sel_hi:[1,1,0]\n"
"v_pk_fma_f32 v[74:75],v[74:75],v[60:61],v[56:57] op_sel:[0,1,0] op_sel_hi:[1,1,0]\n"
"v_pk_fma_f32 v[76:77],v[76:77],v[60:61],v[56:57] op_sel:[0,1,0] op_sel_hi:[1,1,0]\n"
"v_pk_fma_f32 v[78:79],v[78:79],v[60:61],v[56:57] op_sel:[0,1,0] op_sel_hi:[1,1,0]\n"
CV "v112,v72,v73\n"
CV "v113,v74,v75\n"
CV "v114,v76,v77\n"
CV "v115,v78,v79\n"
MF "a[44:47],v[144:147],v[96:99],a[44:47]\n"
"ds_read_b128 v[200:203],v120 offset:40960\n"
"ds_write_b128 v120,v[112:115] offset:40960\n"
AR "v80,a40\n"
AR "v81,a41\n"
AR "v82,a42\n"
MF "a[64:67],v[148:151],v[96:99],a[64:67]\n"
AR "v83,a43\n"
AR "v84,a60\n"
AR "v85,a61\n"
AR "v86,a62\n"
AR "v87,a63\n"
"v_pk_fma_f32 v[80:81],v[80:81],v[56:57],v[52:53] op_sel:[0,1,0] op_sel_hi:[1,1,0]\n"
"v_pk_fma_f32 v[82:83],v[82:83],v[56:57],v[52:53] op_sel:[0,1,0] op_sel_hi:[1,1,0]\n"
"v_pk_fma_f32 v[84:85],v[84:85],v[56:57],v[52:53] op_sel:[0,1,0] op_sel_hi:[1,1,0]\n"
"v_pk_fma_f32 v[86:87],v[86:87],v[56:57],v[52:53] op_sel:[0,1,0] op_sel_hi:[1,1,0]\n"
CV "v116,v80,v81\n"
CV "v117,v82,v83\n"
CV "v118,v84,v85\n"
CV "v119,v86,v87\n"
MF "a[48:51],v[144:147],v[100:103],a[48:51]\n"
"ds_read_b128 v[204:207],v120 offset:45056\n"
"ds_write_b128 v120,v[116:119] offset:45056\n"
"s_add_u32 s58,s75,4096\n"
LD "v[248:251],v9,s[40:43],s58 offen sc1\n"
AR "v72,a44\n"
AR "v73,a45\n"
AR "v74,a46\n"
MF "a[68:71],v[148:151],v[100:103],a[68:71]\n"
AR "v75,a47\n"
AR "v76,a64\n"
AR "v77,a65\n"
AR "v78,a66\n"
AR "v79,a67\n"
"v_pk_fma_f32 v[72:73],v[72:73],v[58:59],v[52:53] op_sel:[0,0,1] op_sel_hi:[1,0,1]\n"
"v_pk_fma_f32 v[74:75],v[74:75],v[58:59],v[52:53] op_sel:[0,0,1] op_sel_hi:[1,0,1]\n"
"v_pk_fma_f32 v[76:77],v[76:77],v[58:59],v[52:53] op_sel:[0,0,1] op_sel_hi:[1,0,1]\n"
"v_pk_fma_f32 v[78:79],v[78:79],v[58:59],v[52:53] op_sel:[0,0,1] op_sel_hi:[1,0,1]\n"
CV "v112,v72,v73\n"
CV "v113,v74,v75\n"
CV "v114,v76,v77\n"
CV "v115,v78,v79\n"
MF "a[52:55],v[144:147],v[104:107],a[52:55]\n"
"ds_read_b128 v[208:211],v120 offset:49152\n"
"ds_write_b128 v120,v[112:115] offset:49152\n"
LD "v[252:255],v9,s[40:43],s58 offen offset:1024 sc1\n"
AR "v80,a48\n"
AR "v81,a49\n"
AR "v82,a50\n"
MF "a[72:75],v[148:151],v[104:107],a[72:75]\n"
AR "v83,a51\n"
AR "v84,a68\n"
AR "v85,a69\n"
AR "v86,a70\n"
AR "v87,a71\n"
"v_pk_fma_f32 v[80:81],v[80:81],v[58:59],v[54:55] op_sel:[0,1,0] op_sel_hi:[1,1,0]\n"
"v_pk_fma_f32 v[82:83],v[82:83],v[58:59],v[54:55] op_sel:[0,1,0] op_sel_hi:[1,1,0]\n"
"v_pk_fma_f32 v[84:85],v[84:85],v[58:59],v[54:55] op_sel:[0,1,0] op_sel_hi:[1,1,0]\n"
"v_pk_fma_f32 v[86:87],v[86:87],v[58:59],v[54:55] op_sel:[0,1,0] op_sel_hi:[1,1,0]\n"
CV "v116,v80,v81\n"
CV "v117,v82,v83\n"
CV "v118,v84,v85\n"
CV "v119,v86,v87\n"
MF "a[56:59],v[144:147],v[108:111],a[56:59]\n"
"ds_read_b128 v[212:215],v120 offset:53248\n"
"ds_write_b128 v120,v[116:119] offset:53248\n"
AR "v72,a52\n"
AR "v73,a53\n"
AR "v74,a54\n"
MF "a[76:79],v[148:151],v[108:111],a[76:79]\n"
AR "v75,a55\n"
AR "v76,a72\n"
AR "v77,a73\n"
AR "v78,a74\n"
AR "v79,a75\n"
"v_pk_fma_f32 v[72:73],v[72:73],v[60:61],v[54:55] op_sel:[0,0,1] op_sel_hi:[1,0,1]\n"
"v_pk_fma_f32 v[74:75],v[74:75],v[60:61],v[54:55] op_sel:[0,0,1] op_sel_hi:[1,0,1]\n"
"v_pk_fma_f32 v[76:77],v[76:77],v[60:61],v[54:55] op_sel:[0,0,1] op_sel_hi:[1,0,1]\n"
"v_pk_fma_f32 v[78:79],v[78:79],v[60:61],v[54:55] op_sel:[0,0,1] op_sel_hi:[1,0,1]\n"
CV "v112,v72,v73\n"
CV "v113,v74,v75\n"
CV "v114,v76,v77\n"
CV "v115,v78,v79\n"
MF "a[80:83],v[152:155],v[92:95],a[80:83]\n"
"ds_read_b128 v[216:219],v120 offset:57344\n"
"ds_write_b128 v120,v[112:115] offset:57344\n"
AR "v80,a56\n"
AR "v81,a57\n"
AR "v82,a58\n"
MF "a[100:103],v[156:159],v[92:95],a[100:103]\n"
AR "v83,a59\n"
AR "v84,a76\n"
AR "v85,a77\n"
AR "v86,a78\n"
AR "v87,a79\n"
"v_pk_fma_f32 v[80:81],v[80:81],v[60:61],v[56:57] op_sel:[0,1,0] op_sel_hi:[1,1,0]\n"
"v_pk_fma_f32 v[82:83],v[82:83],v[60:61],v[56:57] op_sel:[0,1,0] op_sel_hi:[1,1,0]\n"
"v_pk_fma_f32 v[84:85],v[84:85],v[60:61],v[56:57] op_sel:[0,1,0] op_sel_hi:[1,1,0]\n"
"v_pk_fma_f32 v[86:87],v[86:87],v[60:61],v[56:57] op_sel:[0,1,0] op_sel_hi:[1,1,0]\n"
CV "v116,v80,v81\n"
CV "v117,v82,v83\n"
CV "v118,v84,v85\n"
CV "v119,v86,v87\n"
MF "a[84:87],v[152:155],v[96:99],a[84:87]\n"
"ds_read_b128 v[220:223],v120 offset:61440\n"
"ds_write_b128 v120,v[116:119] offset:61440\n"
AR "v72,a80\n"
AR "v73,a81\n"
AR "v74,a82\n"
MF "a[104:107],v[156:159],v[96:99],a[104:107]\n"
AR "v75,a83\n"
AR "v76,a100\n"
AR "v77,a101\n"
AR "v78,a102\n"
AR "v79,a103\n"
"v_pk_fma_f32 v[72:73],v[72:73],v[56:57],v[52:53] op_sel:[0,1,0] op_sel_hi:[1,1,0]\n"
"v_pk_fma_f32 v[74:75],v[74:75],v[56:57],v[52:53] op_sel:[0,1,0] op_sel_hi:[1,1,0]\n"
"v_pk_fma_f32 v[76:77],v[76:77],v[56:57],v[52:53] op_sel:[0,1,0] op_sel_hi:[1,1,0]\n"
"v_pk_fma_f32 v[78:79],v[78:79],v[56:57],v[52:53] op_sel:[0,1,0] op_sel_hi:[1,1,0]\n"
CV "v112,v72,v73\n"
CV "v113,v74,v75\n"
CV "v114,v76,v77\n"
CV "v115,v78,v79\n"
MF "a[88:91],v[152:155],v[100:103],a[88:91]\n"
"ds_read_b128 v[224:227],v121\n"
"ds_write_b128 v121,v[112:115]\n"
AR "v80,a84\n"
AR "v81,a85\n"
AR "v82,a86\n"
MF "a[108:111],v[156:159],v[100:103],a[108:111]\n"
AR "v83,a87\n"
AR "v84,a104\n"
AR "v85,a105\n"
AR "v86,a106\n"
".Lpg17g:\n"
"s_cbranch_execz .Lisr6\n"
"s_nop 0\n"
AR "v87,a107\n"
"v_pk_fma_f32 v[80:81],v[80:81],v[58:59],v[52:53] op_sel:[0,0,1] op_sel_hi:[1,0,1]\n"
"v_pk_fma_f32 v[82:83],v[82:83],v[58:59],v[52:53] op_sel:[0,0,1] op_sel_hi:[1,0,1]\n"
"v_pk_fma_f32 v[84:85],v[84:85],v[58:59],v[52:53] op_sel:[0,0,1] op_sel_hi:[1,0,1]\n"
"v_pk_fma_f32 v[86:87],v[86:87],v[58:59],v[52:53] op_sel:[0,0,1] op_sel_hi:[1,0,1]\n"
CV "v116,v80,v81\n"
CV "v117,v82,v83\n"
CV "v118,v84,v85\n"
CV "v119,v86,v87\n"
MF "a[92:95],v[152:155],v[104:107],a[92:95]\n"
"ds_read_b128 v[228:231],v121 offset:4096\n"
"ds_write_b128 v121,v[116:119] offset:4096\n"
AR "v72,a88\n"
AR "v73,a89\n"
AR "v74,a90\n"
MF "a[112:115],v[156:159],v[104:107],a[112:115]\n"
AR "v75,a91\n"
AR "v76,a108\n"
AR "v77,a109\n"
AR "v78,a110\n"
AR "v79,a111\n"
"v_pk_fma_f32 v[72:73],v[72:73],v[58:59],v[54:55] op_sel:[0,1,0] op_sel_hi:[1,1,0]\n"
"v_pk_fma_f32 v[74:75],v[74:75],v[58:59],v[54:55] op_sel:[0,1,0] op_sel_hi:[1,1,0]\n"
"v_pk_fma_f32 v[76:77],v[76:77],v[58:59],v[54:55] op_sel:[0,1,0] op_sel_hi:[1,1,0]\n"
"v_pk_fma_f32 v[78:79],v[78:79],v[58:59],v[54:55] op_sel:[0,1,0] op_sel_hi:[1,1,0]\n"
CV "v112,v72,v73\n"
CV "v113,v74,v75\n"
CV "v114,v76,v77\n"
CV "v115,v78,v79\n"
MF "a[96:99],v[152:155],v[108:111],a[96:99]\n"
"ds_read_b128 v[136:139],v121 offset:8192\n"
"ds_write_b128 v121,v[112:115] offset:8192\n"
AR "v80,a92\n"
AR "v81,a93\n"
AR "v82,a94\n"
MF "a[116:119],v[156:159],v[108:111],a[116:119]\n"
AR "v83,a95\n"
AR "v84,a112\n"
AR "v85,a113\n"
AR "v86,a114\n"
AR "v87,a115\n"
"v_pk_fma_f32 v[80:81],v[80:81],v[60:61],v[54:55] op_sel:[0,0,1] op_sel_hi:[1,0,1]\n"
"v_pk_fma_f32 v[82:83],v[82:83],v[60:61],v[54:55] op_sel:[0,0,1] op_sel_hi:[1,0,1]\n"
"v_pk_fma_f32 v[84:85],v[84:85],v[60:61],v[54:55] op_sel:[0,0,1] op_sel_hi:[1,0,1]\n"
"v_pk_fma_f32 v[86:87],v[86:87],v[60:61],v[54:55] op_sel:[0,0,1] op_sel_hi:[1,0,1]\n"
CV "v116,v80,v81\n"
CV "v117,v82,v83\n"
CV "v118,v84,v85\n"
CV "v119,v86,v87\n"
MF "a[120:123],v[160:163],v[92:95],a[120:123]\n"
"ds_read_b128 v[140:143],v121 offset:12288\n"
"ds_write_b128 v121,v[116:119] offset:12288\n"
AR "v72,a96\n"
AR "v73,a97\n"
AR "v74,a98\n"
MF "a[140:143],v[164:167],v[92:95],a[140:143]\n"
AR "v75,a99\n"
AR "v76,a116\n"
AR "v77,a117\n"
AR "v78,a118\n"
AR "v79,a119\n"
"v_pk_fma_f32 v[72:73],v[72:73],v[60:61],v[56:57] op_sel:[0,1,0] op_sel_hi:[1,1,0]\n"
"v_pk_fma_f32 v[74:75],v[74:75],v[60:61],v[56:57] op_sel:[0,1,0] op_sel_hi:[1,1,0]\n"
"v_pk_fma_f32 v[76:77],v[76:77],v[60:61],v[56:57] op_sel:[0,1,0] op_sel_hi:[1,1,0]\n"
"v_pk_fma_f32 v[78:79],v[78:79],v[60:61],v[56:57] op_sel:[0,1,0] op_sel_hi:[1,1,0]\n"
CV "v112,v72,v73\n"
CV "v113,v74,v75\n"
CV "v114,v76,v77\n"
CV "v115,v78,v79\n"
MF "a[124:127],v[160:163],v[96:99],a[124:127]\n"
"ds_read_b128 v[144:147],v121 offset:16384\n"
"ds_write_b128 v121,v[112:115] offset:16384\n"
AR "v80,a120\n"
AR "v81,a121\n"
AR "v82,a122\n"
MF "a[144:147],v[164:167],v[96:99],a[144:147]\n"
AR "v83,a123\n"
AR "v84,a140\n"
AR "v85,a141\n"
AR "v86,a142\n"
AR "v87,a143\n"
"v_pk_fma_f32 v[80:81],v[80:81],v[56:57],v[52:53] op_sel:[0,1,0] op_sel_hi:[1,1,0]\n"
"v_pk_fma_f32 v[82:83],v[82:83],v[56:57],v[52:53] op_sel:[0,1,0] op_sel_hi:[1,1,0]\n"
"v_pk_fma_f32 v[84:85],v[84:85],v[56:57],v[52:53] op_sel:[0,1,0] op_sel_hi:[1,1,0]\n"
"v_pk_fma_f32 v[86:87],v[86:87],v[56:57],v[52:53] op_sel:[0,1,0] op_sel_hi:[1,1,0]\n"
CV "v116,v80,v81\n"
CV "v117,v82,v83\n"
CV "v118,v84,v85\n"
CV "v119,v86,v87\n"
MF "a[128:131],v[160:163],v[100:103],a[128:131]\n"
"ds_read_b128 v[148:151],v121 offset:20480\n"
"ds_write_b128 v121,v[116:119] offset:20480\n"
AR "v72,a124\n"
AR "v73,a125\n"
AR "v74,a126\n"
MF "a[148:151],v[164:167],v[100:103],a[148:151]\n"
AR "v75,a127\n"
AR "v76,a144\n"
AR "v77,a145\n"
AR "v78,a146\n"
AR "v79,a147\n"
"v_pk_fma_f32 v[72:73],v[72:73],v[58:59],v[52:53] op_sel:[0,0,1] op_sel_hi:[1,0,1]\n"
"v_pk_fma_f32 v[74:75],v[74:75],v[58:59],v[52:53] op_sel:[0,0,1] op_sel_hi:[1,0,1]\n"
"v_pk_fma_f32 v[76:77],v[76:77],v[58:59],v[52:53] op_sel:[0,0,1] op_sel_hi:[1,0,1]\n"
"v_pk_fma_f32 v[78:79],v[78:79],v[58:59],v[52:53] op_sel:[0,0,1] op_sel_hi:[1,0,1]\n"
CV "v112,v72,v73\n"
CV "v113,v74,v75\n"
CV "v114,v76,v77\n"
CV "v115,v78,v79\n"
MF "a[132:135],v[160:163],v[104:107],a[132:135]\n"
"ds_write_b128 v120,v[112:115]\n"
AR "v80,a128\n"
AR "v81,a129\n"
AR "v82,a130\n"
MF "a[152:155],v[164:167],v[104:107],a[152:155]\n"
AR "v83,a131\n"
AR "v84,a148\n"
AR "v85,a149\n"
AR "v86,a150\n"
AR "v87,a151\n"
"v_pk_fma_f32 v[80:81],v[80:81],v[58:59],v[54:55] op_sel:[0,1,0] op_sel_hi:[1,1,0]\n"
"v_pk_fma_f32 v[82:83],v[82:83],v[58:59],v[54:55] op_sel:[0,1,0] op_sel_hi:[1,1,0]\n"
"v_pk_fma_f32 v[84:85],v[84:85],v[58:59],v[54:55] op_sel:[0,1,0] op_sel_hi:[1,1,0]\n"
"v_pk_fma_f32 v[86:87],v[86:87],v[58:59],v[54:55] op_sel:[0,1,0] op_sel_hi:[1,1,0]\n"
CV "v116,v80,v81\n"
CV "v117,v82,v83\n"
CV "v118,v84,v85\n"
CV "v119,v86,v87\n"
MF "a[136:139],v[160:163],v[108:111],a[136:139]\n"
"ds_write_b128 v120,v[116:119] offset:4096\n"
AR "v72,a132\n"
AR "v73,a133\n"
AR "v74,a134\n"
MF "a[156:159],v[164:167],v[108:111],a[156:159]\n"
AR "v75,a135\n"
AR "v76,a152\n"
AR "v77,a153\n"
AR "v78,a154\n"
AR "v79,a155\n"
"v_pk_fma_f32 v[72:73],v[72:73],v[60:61],v[54:55] op_sel:[0,0,1] op_sel_hi:[1,0,1]\n"
"v_pk_fma_f32 v[74:75],v[74:75],v[60:61],v[54:55] op_sel:[0,0,1] op_sel_hi:[1,0,1]\n"
"v_pk_fma_f32 v[76:77],v[76:77],v[60:61],v[54:55] op_sel:[0,0,1] op_sel_hi:[1,0,1]\n"
"v_pk_fma_f32 v[78:79],v[78:79],v[60:61],v[54:55] op_sel:[0,0,1] op_sel_hi:[1,0,1]\n"
CV "v112,v72,v73\n"
CV "v113,v74,v75\n"
CV "v114,v76,v77\n"
CV "v115,v78,v79\n"
MF "a[160:163],v[168:171],v[92:95],a[160:163]\n"
"ds_write_b128 v120,v[112:115] offset:8192\n"
AR "v80,a136\n"
AR "v81,a137\n"
AR "v82,a138\n"
MF "a[180:183],v[172:175],v[92:95],a[180:183]\n"
AR "v83,a139\n"
AR "v84,a156\n"
AR "v85,a157\n"
AR "v86,a158\n"
AR "v87,a159\n"
"v_pk_fma_f32 v[80:81],v[80:81],v[60:61],v[56:57] op_sel:[0,1,0] op_sel_hi:[1,1,0]\n"
"v_pk_fma_f32 v[82:83],v[82:83],v[60:61],v[56:57] op_sel:[0,1,0] op_sel_hi:[1,1,0]\n"
"v_pk_fma_f32 v[84:85],v[84:85],v[60:61],v[56:57] op_sel:[0,1,0] op_sel_hi:[1,1,0]\n"
"v_pk_fma_f32 v[86:87],v[86:87],v[60:61],v[56:57] op_sel:[0,1,0] op_sel_hi:[1,1,0]\n"
CV "v116,v80,v81\n"
CV "v117,v82,v83\n"
CV "v118,v84,v85\n"
CV "v119,v86,v87\n"
MF "a[164:167],v[168:171],v[96:99],a[164:167]\n"
"ds_write_b128 v120,v[116:119] offset:12288\n"
AR "v72,a160\n"
AR "v73,a161\n"
AR "v74,a162\n"
MF "a[184:187],v[172:175],v[96:99],a[184:187]\n"
AR "v75,a163\n"
AR "v76,a180\n"
AR "v77,a181\n"
AR "v78,a182\n"
AR "v79,a183\n"
"v_pk_fma_f32 v[72:73],v[72:73],v[56:57],v[52:53] op_sel:[0,1,0] op_sel_hi:[1,1,0]\n"
"v_pk_fma_f32 v[74:75],v[74:75],v[56:57],v[52:53] op_sel:[0,1,0] op_sel_hi:[1,1,0]\n"
"v_pk_fma_f32 v[76:77],v[76:77],v[56:57],v[52:53] op_sel:[0,1,0] op_sel_hi:[1,1,0]\n"
"v_pk_fma_f32 v[78:79],v[78:79],v[56:57],v[52:53] op_sel:[0,1,0] op_sel_hi:[1,1,0]\n"
CV "v112,v72,v73\n"
CV "v113,v74,v75\n"
CV "v114,v76,v77\n"
CV "v115,v78,v79\n"
MF "a[168:171],v[168:171],v[100:103],a[168:171]\n"
"ds_write_b128 v120,v[112:115] offset:16384\n"
AR "v80,a164\n"
AR "v81,a165\n"
AR "v82,a166\n"
MF "a[188:191],v[172:175],v[100:103],a[188:191]\n"
AR "v83,a167\n"
AR "v84,a184\n"
AR "v85,a185\n"
AR "v86,a186\n"
AR "v87,a187\n"
"v_pk_fma_f32 v[80:81],v[80:81],v[58:59],v[52:53] op_sel:[0,0,1] op_sel_hi:[1,0,1]\n"
"v_pk_fma_f32 v[82:83],v[82:83],v[58:59],v[52:53] op_sel:[0,0,1] op_sel_hi:[1,0,1]\n"
"v_pk_fma_f32 v[84:85],v[84:85],v[58:59],v[52:53] op_sel:[0,0,1] op_sel_hi:[1,0,1]\n"
"v_pk_fma_f32 v[86:87],v[86:87],v[58:59],v[52:53] op_sel:[0,0,1] op_sel_hi:[1,0,1]\n"
CV "v116,v80,v81\n"
CV "v117,v82,v83\n"
CV "v118,v84,v85\n"
CV "v119,v86,v87\n"
MF "a[172:175],v[168:171],v[104:107],a[172:175]\n"
"ds_write_b128 v120,v[116:119] offset:20480\n"
AR "v72,a168\n"
AR "v73,a169\n"
AR "v74,a170\n"
MF "a[192:195],v[172:175],v[104:107],a[192:195]\n"
AR "v75,a171\n"
AR "v76,a188\n"
AR "v77,a189\n"
AR "v78,a190\n"
AR "v79,a191\n"
"v_pk_fma_f32 v[72:73],v[72:73],v[58:59],v[54:55] op_sel:[0,1,0] op_sel_hi:[1,1,0]\n"
"v_pk_fma_f32 v[74:75],v[74:75],v[58:59],v[54:55] op_sel:[0,1,0] op_sel_hi:[1,1,0]\n"
"v_pk_fma_f32 v[76:77],v[76:77],v[58:59],v[54:55] op_sel:[0,1,0] op_sel_hi:[1,1,0]\n"
"v_pk_fma_f32 v[78:79],v[78:79],v[58:59],v[54:55] op_sel:[0,1,0] op_sel_hi:[1,1,0]\n"
CV "v112,v72,v73\n"
CV "v113,v74,v75\n"
CV "v114,v76,v77\n"
CV "v115,v78,v79\n"
MF "a[176:179],v[168:171],v[108:111],a[176:179]\n"
AR "v152,a240\n"
AW "a240,v112\n"
AR "v153,a241\n"
AW "a241,v113\n"
AR "v154,a242\n"
AW "a242,v114\n"
AR "v155,a243\n"
AW "a243,v115\n"
AR "v80,a172\n"
AR "v81,a173\n"
AR "v82,a174\n"
MF "a[196:199],v[172:175],v[108:111],a[196:199]\n"
AR "v83,a175\n"
AR "v84,a192\n"
AR "v85,a193\n"
AR "v86,a194\n"
AR "v87,a195\n"
"v_pk_fma_f32 v[80:81],v[80:81],v[60:61],v[54:55] op_sel:[0,0,1] op_sel_hi:[1,0,1]\n"
"v_pk_fma_f32 v[82:83],v[82:83],v[60:61],v[54:55] op_sel:[0,0,1] op_sel_hi:[1,0,1]\n"
"v_pk_fma_f32 v[84:85],v[84:85],v[60:61],v[54:55] op_sel:[0,0,1] op_sel_hi:[1,0,1]\n"
"v_pk_fma_f32 v[86:87],v[86:87],v[60:61],v[54:55] op_sel:[0,0,1] op_sel_hi:[1,0,1]\n"
CV "v116,v80,v81\n"
CV "v117,v82,v83\n"
CV "v118,v84,v85\n"
CV "v119,v86,v87\n"
MF "a[200:203],v[176:179],v[92:95],a[200:203]\n"
AR "v156,a244\n"
AW "a244,v116\n"
AR "v157,a245\n"
AW "a245,v117\n"
AR "v158,a246\n"
AW "a246,v118\n"
AR "v159,a247\n"
AW "a247,v119\n"
AR "v72,a176\n"
AR "v73,a177\n"
AR "v74,a178\n"
MF "a[220:223],v[180:183],v[92:95],a[220:223]\n"
AR "v75,a179\n"
AR "v76,a196\n"
AR "v77,a197\n"
AR "v78,a198\n"
AR "v79,a199\n"
"v_pk_fma_f32 v[72:73],v[72:73],v[60:61],v[56:57] op_sel:[0,1,0] op_sel_hi:[1,1,0]\n"
"v_pk_fma_f32 v[74:75],v[74:75],v[60:61],v[56:57] op_sel:[0,1,0] op_sel_hi:[1,1,0]\n"
"v_pk_fma_f32 v[76:77],v[76:77],v[60:61],v[56:57] op_sel:[0,1,0] op_sel_hi:[1,1,0]\n"
"v_pk_fma_f32 v[78:79],v[78:79],v[60:61],v[56:57] op_sel:[0,1,0] op_sel_hi:[1,1,0]\n"
CV "v112,v72,v73\n"
CV "v113,v74,v75\n"
CV "v114,v76,v77\n"
CV "v115,v78,v79\n"
MF "a[204:207],v[176:179],v[96:99],a[204:207]\n"
AR "v160,a248\n"
AW "a248,v112\n"
AR "v161,a249\n"
AW "a249,v113\n"
AR "v162,a250\n"
AW "a250,v114\n"
AR "v163,a251\n"
AW "a251,v115\n"
AR "v80,a200\n"
AR "v81,a201\n"
AR "v82,a202\n"
MF "a[224:227],v[180:183],v[96:99],a[224:227]\n"
AR "v83,a203\n"
AR "v84,a220\n"
AR "v85,a221\n"
AR "v86,a222\n"
AR "v87,a223\n"
"v_pk_fma_f32 v[80:81],v[80:81],v[56:57],v[52:53] op_sel:[0,1,0] op_sel_hi:[1,1,0]\n"
"v_pk_fma_f32 v[82:83],v[82:83],v[56:57],v[52:53] op_sel:[0,1,0] op_sel_hi:[1,1,0]\n"
"v_pk_fma_f32 v[84:85],v[84:85],v[56:57],v[52:53] op_sel:[0,1,0] op_sel_hi:[1,1,0]\n"
"v_pk_fma_f32 v[86:87],v[86:87],v[56:57],v[52:53] op_sel:[0,1,0] op_sel_hi:[1,1,0]\n"
CV "v116,v80,v81\n"
CV "v117,v82,v83\n"
CV "v118,v84,v85\n"
CV "v119,v86,v87\n"
MF "a[208:211],v[176:179],v[100:103],a[208:211]\n"
"v_mov_b32 v164,v62\n"
"v_mov_b32 v62,v116\n"
"v_mov_b32 v165,v63\n"
"v_mov_b32 v63,v117\n"
"v_mov_b32 v166,v64\n"
"v_mov_b32 v64,v118\n"
"v_mov_b32 v167,v65\n"
"v_mov_b32 v65,v119\n"
AR "v72,a204\n"
AR "v73,a205\n"
AR "v74,a206\n"
MF "a[228:231],v[180:183],v[100:103],a[228:231]\n"
AR "v75,a207\n"
AR "v76,a224\n"
AR "v77,a225\n"
AR "v78,a226\n"
AR "v79,a227\n"
"v_pk_fma_f32 v[72:73],v[72:73],v[58:59],v[52:53] op_sel:[0,0,1] op_sel_hi:[1,0,1]\n"
"v_pk_fma_f32 v[74:75],v[74:75],v[58:59],v[52:53] op_sel:[0,0,1] op_sel_hi:[1,0,1]\n"
"v_pk_fma_f32 v[76:77],v[76:77],v[58:59],v[52:53] op_sel:[0,0,1] op_sel_hi:[1,0,1]\n"
"v_pk_fma_f32 v[78:79],v[78:79],v[58:59],v[52:53] op_sel:[0,0,1] op_sel_hi:[1,0,1]\n"
CV "v112,v72,v73\n"
CV "v113,v74,v75\n"
CV "v114,v76,v77\n"
CV "v115,v78,v79\n"
MF "a[212:215],v[176:179],v[104:107],a[212:215]\n"
"v_mov_b32 v168,v66\n"
"v_mov_b32 v66,v112\n"
"v_mov_b32 v169,v67\n"
"v_mov_b32 v67,v113\n"
"v_mov_b32 v170,v68\n"
"v_mov_b32 v68,v114\n"
"v_mov_b32 v171,v69\n"
"v_mov_b32 v69,v115\n"
AR "v80,a208\n"
AR "v81,a209\n"
AR "v82,a210\n"
MF "a[232:235],v[180:183],v[104:107],a[232:235]\n"
AR "v83,a211\n"
AR "v84,a228\n"
AR "v85,a229\n"
AR "v86,a230\n"
AR "v87,a231\n"
"v_pk_fma_f32 v[80:81],v[80:81],v[58:59],v[54:55] op_sel:[0,1,0] op_sel_hi:[1,1,0]\n"
"v_pk_fma_f32 v[82:83],v[82:83],v[58:59],v[54:55] op_sel:[0,1,0] op_sel_hi:[1,1,0]\n"
"v_pk_fma_f32 v[84:85],v[84:85],v[58:59],v[54:55] op_sel:[0,1,0] op_sel_hi:[1,1,0]\n"
"v_pk_fma_f32 v[86:87],v[86:87],v[58:59],v[54:55] op_sel:[0,1,0] op_sel_hi:[1,1,0]\n"
CV "v116,v80,v81\n"
CV "v117,v82,v83\n"
CV "v118,v84,v85\n"
CV "v119,v86,v87\n"
MF "a[216:219],v[176:179],v[108:111],a[216:219]\n"
"v_mov_b32 v172,v70\n"
"v_mov_b32 v70,v116\n"
"v_mov_b32 v173,v71\n"
"v_mov_b32 v71,v117\n"
"v_mov_b32 v174,v132\n"
"v_mov_b32 v132,v118\n"
"v_mov_b32 v175,v133\n"
"v_mov_b32 v133,v119\n"
AR "v72,a212\n"
AR "v73,a213\n"
AR "v74,a214\n"
MF "a[236:239],v[180:183],v[108:111],a[236:239]\n"
AR "v75,a215\n"
AR "v76,a232\n"
AR "v77,a233\n"
AR "v78,a234\n"
AR "v79,a235\n"
"v_pk_fma_f32 v[72:73],v[72:73],v[60:61],v[54:55] op_sel:[0,0,1] op_sel_hi:[1,0,1]\n"
"v_pk_fma_f32 v[74:75],v[74:75],v[60:61],v[54:55] op_sel:[0,0,1] op_sel_hi:[1,0,1]\n"
"v_pk_fma_f32 v[76:77],v[76:77],v[60:61],v[54:55] op_sel:[0,0,1] op_sel_hi:[1,0,1]\n"
"v_pk_fma_f32 v[78:79],v[78:79],v[60:61],v[54:55] op_sel:[0,0,1] op_sel_hi:[1,0,1]\n"
CV "v112,v72,v73\n"
CV "v113,v74,v75\n"
CV "v114,v76,v77\n"
CV "v115,v78,v79\n"
"s_waitcnt vmcnt(0) lgkmcnt(0)\n"
"s_barrier\n"
"ds_read_b128 v[92:95],v0\n"
"ds_read_b128 v[96:99],v1\n"
"ds_read_b128 v[100:103],v0 offset:1024\n"
"v_mov_b32 v176,v134\n"
"v_mov_b32 v134,v112\n"
"v_mov_b32 v177,v135\n"
"v_mov_b32 v135,v113\n"
"v_mov_b32 v178,v6\n"
"v_mov_b32 v6,v114\n"
"v_mov_b32 v179,v13\n"
"v_mov_b32 v13,v115\n"
AR "v80,a216\n"
AR "v81,a217\n"
AR "v82,a218\n"
AR "v83,a219\n"
AR "v84,a236\n"
AR "v85,a237\n"
AR "v86,a238\n"
AR "v87,a239\n"
"v_pk_fma_f32 v[80:81],v[80:81],v[60:61],v[56:57] op_sel:[0,1,0] op_sel_hi:[1,1,0]\n"
"v_pk_fma_f32 v[82:83],v[82:83],v[60:61],v[56:57] op_sel:[0,1,0] op_sel_hi:[1,1,0]\n"
"v_pk_fma_f32 v[84:85],v[84:85],v[60:61],v[56:57] op_sel:[0,1,0] op_sel_hi:[1,1,0]\n"
"v_pk_fma_f32 v[86:87],v[86:87],v[60:61],v[56:57] op_sel:[0,1,0] op_sel_hi:[1,1,0]\n"
CV "v116,v80,v81\n"
CV "v117,v82,v83\n"
CV "v118,v84,v85\n"
CV "v119,v86,v87\n"
"v_mov_b32 v180,v14\n"
"v_mov_b32 v14,v116\n"
"v_mov_b32 v181,v15\n"
"v_mov_b32 v15,v117\n"
AR "v182,a254\n"
AW "a254,v118\n"
AR "v183,a255\n"
AW "a255,v119\n"
"s_mov_b32 s13,s11\n"
"v_subrev_u32 v2,49152,v2\n"
"v_subrev_u32 v3,49152,v3\n"
"v_subrev_u32 v4,49152,v4\n"
"v_subrev_u32 v5,49152,v5\n"
"s_mov_b32 s57,2\n"
"s_mov_b32 s69,1\n"
"s_mov_b32 s61,2\n"
"s_mov_b32 s97,0\n"
"s_mov_b32 s95,0x47800000\n"
"s_mov_b32 s77,0x47800000\n"
"s_branch .Latt2\n"
".LpgattB_f:\n"
"s_cbranch_execz .Lisr3\n"
".LattB:\n"
AR "v2,a245\n"
"v_xor_b32 v3,32,v2\n"
"v_xor_b32 v4,64,v2\n"
"v_xor_b32 v5,96,v2\n"
"s_mov_b32 s61,2\n"
".Latt2:\n"
"s_cmp_eq_u32 s57,1\n"
"s_cbranch_scc1 .Lt0k\n"
".Lt0n:\n"
"s_waitcnt lgkmcnt(2)\n"
MF "v[72:75],v[92:95],v[136:139],0\n"
"ds_read_b128 v[104:107],v1 offset:1024\n"
MF "v[76:79],v[92:95],v[140:143],0\n"
MF "v[80:83],v[92:95],v[144:147],0\n"
MF "v[84:87],v[92:95],v[148:151],0\n"
MF "v[88:91],v[92:95],v[152:155],0\n"
"s_waitcnt lgkmcnt(2)\n"
"ds_read_b128 v[92:95],v0 offset:2048\n"
MF "v[72:75],v[96:99],v[156:159],v[72:75]\n"
MF "v[76:79],v[96:99],v[160:163],v[76:79]\n"
MF "v[80:83],v[96:99],v[164:167],v[80:83]\n"
MF "v[84:87],v[96:99],v[168:171],v[84:87]\n"
MF "v[88:91],v[96:99],v[172:175],v[88:91]\n"
"s_waitcnt lgkmcnt(2)\n"
"ds_read_b128 v[96:99],v1 offset:2048\n"
MF "v[72:75],v[100:103],v[176:179],v[72:75]\n"
MF "v[76:79],v[100:103],v[180:183],v[76:79]\n"
MF "v[80:83],v[100:103],v[184:187],v[80:83]\n"
MF "v[84:87],v[100:103],v[188:191],v[84:87]\n"
MF "v[88:91],v[100:103],v[192:195],v[88:91]\n"
"s_waitcnt lgkmcnt(2)\n"
"ds_read_b128 v[100:103],v0 offset:6144\n"
MF "v[72:75],v[104:107],v[196:199],v[72:75]\n"
MF "v[76:79],v[104:107],v[200:203],v[76:79]\n"
MF "v[80:83],v[104:107],v[204:207],v[80:83]\n"
MF "v[84:87],v[104:107],v[208:211],v[84:87]\n"
MF "v[88:91],v[104:107],v[212:215],v[88:91]\n"
"s_waitcnt lgkmcnt(2)\n"
"ds_read_b128 v[104:107],v1 offset:6144\n"
MF "v[72:75],v[92:95],v[216:219],v[72:75]\n"
MF "v[76:79],v[92:95],v[220:223],v[76:79]\n"
MF "v[80:83],v[92:95],v[224:227],v[80:83]\n"
MF "v[84:87],v[92:95],v[228:231],v[84:87]\n"
MF "v[88:91],v[92:95],v[232:235],v[88:91]\n"
"s_waitcnt lgkmcnt(2)\n"
"ds_read_b128 v[92:95],v0 offset:7168\n"
MF "v[72:75],v[96:99],v[236:239],v[72:75]\n"
MF "v[76:79],v[96:99],v[240:243],v[76:79]\n"
MF "v[80:83],v[96:99],v[244:247],v[80:83]\n"
MF "v[84:87],v[96:99],v[248:251],v[84:87]\n"
MF "v[88:91],v[96:99],v[252:255],v[88:91]\n"
"s_waitcnt lgkmcnt(2)\n"
"ds_read_b128 v[96:99],v1 offset:7168\n"
MF "v[112:115],v[100:103],v[136:139],0\n"
MF "v[116:119],v[100:103],v[140:143],0\n"
MF "v[120:123],v[100:103],v[144:147],0\n"
MF "v[124:127],v[100:103],v[148:151],0\n"
MF "v[128:131],v[100:103],v[152:155],0\n"
"s_waitcnt lgkmcnt(2)\n"
"ds_read_b128 v[100:103],v0 offset:8192\n"
MF "v[112:115],v[104:107],v[156:159],v[112:115]\n"
"v_max3_f32 v52,v72,v73,v74\n"
"v_max_f32 v52,v52,v75\n"
MF "v[116:119],v[104:107],v[160:163],v[116:119]\n"
"v_max3_f32 v53,v76,v77,v78\n"
"v_max_f32 v53,v53,v79\n"
MF "v[120:123],v[104:107],v[164:167],v[120:123]\n"
"v_max3_f32 v54,v80,v81,v82\n"
"v_max_f32 v54,v54,v83\n"
MF "v[124:127],v[104:107],v[168:171],v[124:127]\n"
"v_max3_f32 v55,v84,v85,v86\n"
"v_max_f32 v55,v55,v87\n"
MF "v[128:131],v[104:107],v[172:175],v[128:131]\n"
"s_waitcnt lgkmcnt(2)\n"
"ds_read_b128 v[104:107],v1 offset:8192\n"
"v_max3_f32 v56,v88,v89,v90\n"
MF "v[112:115],v[92:95],v[176:179],v[112:115]\n"
"v_max_f32 v56,v56,v91\n"
MF "v[116:119],v[92:95],v[180:183],v[116:119]\n"
MF "v[120:123],v[92:95],v[184:187],v[120:123]\n"
MF "v[124:127],v[92:95],v[188:191],v[124:127]\n"
MF "v[128:131],v[92:95],v[192:195],v[128:131]\n"
"s_waitcnt lgkmcnt(2)\n"
MF "v[112:115],v[96:99],v[196:199],v[112:115]\n"
"s_waitcnt lgkmcnt(0)\n"
MF "v[116:119],v[96:99],v[200:203],v[116:119]\n"
"s_barrier\n"
MF "v[120:123],v[96:99],v[204:207],v[120:123]\n"
"s_add_u32 s72,s72,6144\n"
MF "v[124:127],v[96:99],v[208:211],v[124:127]\n"
"s_cmp_eq_u32 s72,s17\n"
"s_cselect_b32 s72,s16,s72\n"
"s_add_u32 m0,s64,0\n"
MF "v[128:131],v[96:99],v[212:215],v[128:131]\n"
LD "v7,s[32:35],s72 offen nt lds\n"
MF "v[112:115],v[100:103],v[216:219],v[112:115]\n"
"s_add_u32 m0,s64,896\n"
MF "v[116:119],v[100:103],v[220:223],v[116:119]\n"
LD "v7,s[32:35],s72 offen offset:128 nt lds\n"
MF "v[120:123],v[100:103],v[224:227],v[120:123]\n"
"s_add_u32 m0,s64,1792\n"
MF "v[124:127],v[100:103],v[228:231],v[124:127]\n"
LD "v7,s[32:35],s72 offen offset:256 nt lds\n"
MF "v[128:131],v[100:103],v[232:235],v[128:131]\n"
"s_add_u32 s72,s72,6144\n"
"s_add_u32 m0,s64,6144\n"
MF "v[112:115],v[104:107],v[236:239],v[112:115]\n"
LD "v7,s[32:35],s72 offen nt lds\n"
MF "v[116:119],v[104:107],v[240:243],v[116:119]\n"
"s_add_u32 m0,s64,7040\n"
MF "v[120:123],v[104:107],v[244:247],v[120:123]\n"
LD "v7,s[32:35],s72 offen offset:128 nt lds\n"
MF "v[124:127],v[104:107],v[248:251],v[124:127]\n"
"s_add_u32 m0,s64,7936\n"
MF "v[128:131],v[104:107],v[252:255],v[128:131]\n"
LD "v7,s[32:35],s72 offen offset:256 nt lds\n"
"s_branch .Lt0e\n"
".Lpgt0k_f:\n"
"s_cbranch_execz .Lisr3\n"
".Lt0k:\n"
"s_lshl_b32 s58,s56,10\n"
"s_add_u32 s58,s58,73728\n"
"v_add_u32 v32,s58,v9\n"
"v_add_u32 v33,65536,v32\n"
"s_waitcnt lgkmcnt(2)\n"
MF "v[72:75],v[92:95],v[136:139],0\n"
".Lpg18f:\n"
"s_cbranch_execz .Lisr3\n"
"ds_read_b128 v[104:107],v1 offset:1024\n"
AR "v62,a20\n"
MF "v[76:79],v[92:95],v[140:143],0\n"
"ds_write_b128 v32,a[0:3] offset:24576\n"
MF "v[80:83],v[92:95],v[144:147],0\n"
"ds_write_b128 v32,a[4:7] offset:28672\n"
MF "v[84:87],v[92:95],v[148:151],0\n"
AR "v63,a21\n"
AR "v64,a22\n"
MF "v[88:91],v[92:95],v[152:155],0\n"
"s_waitcnt lgkmcnt(4)\n"
"ds_read_b128 v[92:95],v0 offset:2048\n"
AR "v65,a23\n"
MF "v[72:75],v[96:99],v[156:159],v[72:75]\n"
"ds_write_b128 v32,a[8:11] offset:32768\n"
MF "v[76:79],v[96:99],v[160:163],v[76:79]\n"
AR "v66,a24\n"
AR "v67,a25\n"
MF "v[80:83],v[96:99],v[164:167],v[80:83]\n"
"ds_write_b128 v32,a[12:15] offset:36864\n"
MF "v[84:87],v[96:99],v[168:171],v[84:87]\n"
AR "v68,a26\n"
AR "v69,a27\n"
MF "v[88:91],v[96:99],v[172:175],v[88:91]\n"
"s_waitcnt lgkmcnt(6)\n"
"ds_read_b128 v[96:99],v1 offset:2048\n"
AR "v70,a28\n"
MF "v[72:75],v[100:103],v[176:179],v[72:75]\n"
"ds_write_b128 v32,a[16:19] offset:40960\n"
MF "v[76:79],v[100:103],v[180:183],v[76:79]\n"
AR "v71,a29\n"
AR "v132,a30\n"
MF "v[80:83],v[100:103],v[184:187],v[80:83]\n"
AR "v133,a31\n"
AR "v134,a32\n"
MF "v[84:87],v[100:103],v[188:191],v[84:87]\n"
"ds_write_b128 v32,a[40:43] offset:45056\n"
MF "v[88:91],v[100:103],v[192:195],v[88:91]\n"
"s_waitcnt lgkmcnt(8)\n"
"ds_read_b128 v[100:103],v0 offset:6144\n"
AR "v135,a33\n"
MF "v[72:75],v[104:107],v[196:199],v[72:75]\n"
"ds_write_b128 v32,a[44:47] offset:49152\n"
MF "v[76:79],v[104:107],v[200:203],v[76:79]\n"
AR "v6,a34\n"
AR "v13,a35\n"
MF "v[80:83],v[104:107],v[204:207],v[80:83]\n"
AR "v14,a36\n"
AR "v15,a37\n"
MF "v[84:87],v[104:107],v[208:211],v[84:87]\n"
"ds_write_b128 v32,a[48:51] offset:53248\n"
MF "v[88:91],v[104:107],v[212:215],v[88:91]\n"
"s_waitcnt lgkmcnt(8)\n"
"ds_read_b128 v[104:107],v1 offset:6144\n"
"v_accvgpr_mov_b32 a254,a38\n"
MF "v[72:75],v[92:95],v[216:219],v[72:75]\n"
"v_accvgpr_mov_b32 a255,a39\n"
"v_accvgpr_mov_b32 a240,a168\n"
MF "v[76:79],v[92:95],v[220:223],v[76:79]\n"
"ds_write_b128 v32,a[52:55] offset:57344\n"
MF "v[80:83],v[92:95],v[224:227],v[80:83]\n"
"v_accvgpr_mov_b32 a241,a169\n"
"v_accvgpr_mov_b32 a242,a170\n"
MF "v[84:87],v[92:95],v[228:231],v[84:87]\n"
"ds_write_b128 v32,a[56:59] offset:61440\n"
MF "v[88:91],v[92:95],v[232:235],v[88:91]\n"
"s_waitcnt lgkmcnt(8)\n"
"ds_read_b128 v[92:95],v0 offset:7168\n"
"v_accvgpr_mov_b32 a243,a171\n"
MF "v[72:75],v[96:99],v[236:239],v[72:75]\n"
"v_accvgpr_mov_b32 a244,a172\n"
"v_accvgpr_mov_b32 a245,a173\n"
MF "v[76:79],v[96:99],v[240:243],v[76:79]\n"
"ds_write_b128 v33,a[80:83]\n"
MF "v[80:83],v[96:99],v[244:247],v[80:83]\n"
"v_accvgpr_mov_b32 a246,a174\n"
"v_accvgpr_mov_b32 a247,a175\n"
MF "v[84:87],v[96:99],v[248:251],v[84:87]\n"
"v_accvgpr_mov_b32 a248,a176\n"
"v_accvgpr_mov_b32 a249,a177\n"
MF "v[88:91],v[96:99],v[252:255],v[88:91]\n"
"s_waitcnt lgkmcnt(7)\n"
"ds_read_b128 v[96:99],v1 offset:7168\n"
"v_accvgpr_mov_b32 a250,a178\n"
MF "v[112:115],v[100:103],v[136:139],0\n"
"ds_write_b128 v33,a[84:87] offset:4096\n"
MF "v[116:119],v[100:103],v[140:143],0\n"
"ds_write_b128 v33,a[88:91] offset:8192\n"
MF "v[120:123],v[100:103],v[144:147],0\n"
"v_accvgpr_mov_b32 a251,a179\n"
MF "v[124:127],v[100:103],v[148:151],0\n"
MF "v[128:131],v[100:103],v[152:155],0\n"
"s_waitcnt lgkmcnt(7)\n"
"ds_read_b128 v[100:103],v0 offset:8192\n"
MF "v[112:115],v[104:107],v[156:159],v[112:115]\n"
"v_max3_f32 v52,v72,v73,v74\n"
"v_max_f32 v52,v52,v75\n"
MF "v[116:119],v[104:107],v[160:163],v[116:119]\n"
"v_max3_f32 v53,v76,v77,v78\n"
"v_max_f32 v53,v53,v79\n"
MF "v[120:123],v[104:107],v[164:167],v[120:123]\n"
"v_max3_f32 v54,v80,v81,v82\n"
"v_max_f32 v54,v54,v83\n"
MF "v[124:127],v[104:107],v[168:171],v[124:127]\n"
"v_max3_f32 v55,v84,v85,v86\n"
"v_max_f32 v55,v55,v87\n"
MF "v[128:131],v[104:107],v[172:175],v[128:131]\n"
"s_waitcnt lgkmcnt(5)\n"
"ds_read_b128 v[104:107],v1 offset:8192\n"
"v_max3_f32 v56,v88,v89,v90\n"
MF "v[112:115],v[92:95],v[176:179],v[112:115]\n"
"v_max_f32 v56,v56,v91\n"
MF "v[116:119],v[92:95],v[180:183],v[116:119]\n"
"ds_write_b128 v33,a[92:95] offset:12288\n"
MF "v[120:123],v[92:95],v[184:187],v[120:123]\n"
"ds_write_b128 v33,a[96:99] offset:16384\n"
MF "v[124:127],v[92:95],v[188:191],v[124:127]\n"
"ds_write_b128 v33,a[120:123] offset:20480\n"
MF "v[128:131],v[92:95],v[192:195],v[128:131]\n"
"s_waitcnt lgkmcnt(7)\n"
"ds_write_b128 v32,a[124:127]\n"
MF "v[112:115],v[96:99],v[196:199],v[112:115]\n"
"s_waitcnt lgkmcnt(0)\n"
MF "v[116:119],v[96:99],v[200:203],v[116:119]\n"
"s_barrier\n"
MF "v[120:123],v[96:99],v[204:207],v[120:123]\n"
"s_add_u32 s72,s72,6144\n"
"ds_write_b128 v32,a[128:131] offset:4096\n"
MF "v[124:127],v[96:99],v[208:211],v[124:127]\n"
"s_cmp_eq_u32 s72,s17\n"
"s_cselect_b32 s72,s16,s72\n"
"s_add_u32 m0,s64,0\n"
MF "v[128:131],v[96:99],v[212:215],v[128:131]\n"
LD "v7,s[32:35],s72 offen nt lds\n"
MF "v[112:115],v[100:103],v[216:219],v[112:115]\n"
"s_add_u32 m0,s64,896\n"
"ds_write_b128 v32,a[132:135] offset:8192\n"
MF "v[116:119],v[100:103],v[220:223],v[116:119]\n"
LD "v7,s[32:35],s72 offen offset:128 nt lds\n"
MF "v[120:123],v[100:103],v[224:227],v[120:123]\n"
"s_add_u32 m0,s64,1792\n"
"ds_write_b128 v32,a[136:139] offset:12288\n"
MF "v[124:127],v[100:103],v[228:231],v[124:127]\n"
LD "v7,s[32:35],s72 offen offset:256 nt lds\n"
MF "v[128:131],v[100:103],v[232:235],v[128:131]\n"
"s_add_u32 s72,s72,6144\n"
"s_add_u32 m0,s64,6144\n"
MF "v[112:115],v[104:107],v[236:239],v[112:115]\n"
LD "v7,s[32:35],s72 offen nt lds\n"
MF "v[116:119],v[104:107],v[240:243],v[116:119]\n"
"s_add_u32 m0,s64,7040\n"
"ds_write_b128 v32,a[160:163] offset:16384\n"
MF "v[120:123],v[104:107],v[244:247],v[120:123]\n"
LD "v7,s[32:35],s72 offen offset:128 nt lds\n"
MF "v[124:127],v[104:107],v[248:251],v[124:127]\n"
"s_add_u32 m0,s64,7936\n"
"ds_write_b128 v32,a[164:167] offset:20480\n"
MF "v[128:131],v[104:107],v[252:255],v[128:131]\n"
LD "v7,s[32:35],s72 offen offset:256 nt lds\n"
".Lpgt0e_g:\n"
"s_cbranch_execz .Lisr6\n"
".Lt0e:\n"
"s_nop 1\n"
"v_max3_f32 v52,v52,v112,v113\n"
"v_max3_f32 v53,v53,v116,v117\n"
"v_max3_f32 v54,v54,v120,v121\n"
"v_max3_f32 v55,v55,v124,v125\n"
"s_nop 1\n"
"v_max3_f32 v56,v56,v128,v129\n"
"v_max3_f32 v52,v52,v114,v115\n"
"v_max3_f32 v53,v53,v118,v119\n"
"v_max3_f32 v54,v54,v122,v123\n"
"v_max3_f32 v55,v55,v126,v127\n"
"v_max3_f32 v56,v56,v130,v131\n"
"v_mov_b32 v57,v52\n"
"v_mov_b32 v58,v53\n"
"v_mov_b32 v59,v54\n"
"v_mov_b32 v60,v55\n"
"v_mov_b32 v61,v56\n"
"v_permlane16_swap_b32 v52,v57\n"
"v_permlane16_swap_b32 v53,v58\n"
"v_permlane16_swap_b32 v54,v59\n"
"v_permlane16_swap_b32 v55,v60\n"
"v_permlane16_swap_b32 v56,v61\n"
"v_max_f32 v52,v52,v57\n"
"v_max_f32 v53,v53,v58\n"
"v_max_f32 v54,v54,v59\n"
"v_max_f32 v55,v55,v60\n"
"v_max_f32 v56,v56,v61\n"
"v_mov_b32 v57,v52\n"
"v_mov_b32 v58,v53\n"
"v_mov_b32 v59,v54\n"
"v_mov_b32 v60,v55\n"
"v_mov_b32 v61,v56\n"
"v_permlane32_swap_b32 v52,v57\n"
"v_permlane32_swap_b32 v53,v58\n"
"v_permlane32_swap_b32 v54,v59\n"
"v_permlane32_swap_b32 v55,v60\n"
"v_permlane32_swap_b32 v56,v61\n"
"v_max_f32 v52,v52,v57\n"
"v_max_f32 v53,v53,v58\n"
"v_max_f32 v54,v54,v59\n"
"v_max_f32 v55,v55,v60\n"
"v_max_f32 v56,v56,v61\n"
"v_sub_f32 v32,0xc1300000,v52\n"
"v_sub_f32 v36,0xc1300000,v53\n"
"v_sub_f32 v40,0xc1300000,v54\n"
"v_sub_f32 v44,0xc1300000,v55\n"
"v_sub_f32 v48,0xc1300000,v56\n"
"v_mov_b32 v33,v32\n"
"v_pk_mov_b32 v[34:35],v[32:33],v[32:33]\n"
"v_mov_b32 v37,v36\n"
"v_pk_mov_b32 v[38:39],v[36:37],v[36:37]\n"
"v_mov_b32 v41,v40\n"
"v_pk_mov_b32 v[42:43],v[40:41],v[40:41]\n"
"v_mov_b32 v45,v44\n"
"v_pk_mov_b32 v[46:47],v[44:45],v[44:45]\n"
"v_mov_b32_e64 v49,v48\n"
"v_pk_mov_b32 v[50:51],v[48:49],v[48:49]\n"
"v_pk_add_f32 v[72:73],v[72:73],v[32:33]\n"
"v_pk_add_f32 v[74:75],v[74:75],v[32:33]\n"
"v_pk_add_f32 v[112:113],v[112:113],v[32:33]\n"
"v_pk_add_f32 v[114:115],v[114:115],v[32:33]\n"
"v_exp_f32 v72,v72\n"
"v_exp_f32 v73,v73\n"
"v_exp_f32 v74,v74\n"
"v_exp_f32 v75,v75\n"
"v_exp_f32 v112,v112\n"
"v_exp_f32 v113,v113\n"
"v_exp_f32 v114,v114\n"
"v_exp_f32 v115,v115\n"
"v_pk_add_f32 v[10:11],v[72:73],v[74:75]\n"
"v_pk_add_f32 v[28:29],v[112:113],v[114:115]\n"
"v_pk_add_f32 v[10:11],v[10:11],v[28:29]\n"
"v_add_f32_e64 v52,v10,v11\n"
CV "v72,v72,v73\n"
CV "v73,v74,v75\n"
CV "v74,v112,v113\n"
CV "v75,v114,v115\n"
"v_pk_add_f32 v[76:77],v[76:77],v[36:37]\n"
"v_pk_add_f32 v[78:79],v[78:79],v[36:37]\n"
"v_pk_add_f32 v[116:117],v[116:117],v[36:37]\n"
"v_pk_add_f32 v[118:119],v[118:119],v[36:37]\n"
"v_exp_f32 v76,v76\n"
"v_exp_f32 v77,v77\n"
"v_exp_f32 v78,v78\n"
"v_exp_f32 v79,v79\n"
"v_exp_f32 v116,v116\n"
"v_exp_f32 v117,v117\n"
"v_exp_f32 v118,v118\n"
"v_exp_f32 v119,v119\n"
"v_pk_add_f32 v[10:11],v[76:77],v[78:79]\n"
"v_pk_add_f32 v[28:29],v[116:117],v[118:119]\n"
"v_pk_add_f32 v[10:11],v[10:11],v[28:29]\n"
"v_add_f32_e64 v53,v10,v11\n"
CV "v76,v76,v77\n"
CV "v77,v78,v79\n"
CV "v78,v116,v117\n"
CV "v79,v118,v119\n"
"v_pk_add_f32 v[80:81],v[80:81],v[40:41]\n"
"v_pk_add_f32 v[82:83],v[82:83],v[40:41]\n"
"v_pk_add_f32 v[120:121],v[120:121],v[40:41]\n"
"v_pk_add_f32 v[122:123],v[122:123],v[40:41]\n"
"v_exp_f32 v80,v80\n"
"v_exp_f32 v81,v81\n"
"v_exp_f32 v82,v82\n"
"v_exp_f32 v83,v83\n"
"v_exp_f32 v120,v120\n"
"v_exp_f32 v121,v121\n"
"v_exp_f32 v122,v122\n"
"v_exp_f32 v123,v123\n"
"v_pk_add_f32 v[10:11],v[80:81],v[82:83]\n"
"v_pk_add_f32 v[28:29],v[120:121],v[122:123]\n"
"v_pk_add_f32 v[10:11],v[10:11],v[28:29]\n"
"v_add_f32_e64 v54,v10,v11\n"
CV "v80,v80,v81\n"
CV "v81,v82,v83\n"
CV "v82,v120,v121\n"
CV "v83,v122,v123\n"
"v_pk_add_f32 v[84:85],v[84:85],v[44:45]\n"
"v_pk_add_f32 v[86:87],v[86:87],v[44:45]\n"
"v_pk_add_f32 v[124:125],v[124:125],v[44:45]\n"
"v_pk_add_f32 v[126:127],v[126:127],v[44:45]\n"
"v_mov_b32_e64 v55,0\n"
"v_pk_add_f32 v[88:89],v[88:89],v[48:49]\n"
"v_pk_add_f32 v[90:91],v[90:91],v[48:49]\n"
"v_pk_add_f32 v[128:129],v[128:129],v[48:49]\n"
"v_pk_add_f32 v[130:131],v[130:131],v[48:49]\n"
"v_mov_b32 v56,0\n"
"s_waitcnt vmcnt(12) lgkmcnt(0)\n"
"s_barrier\n"
"ds_read_b128 v[16:19],v0 offset:24576\n"
"ds_read_b128 v[20:23],v1 offset:24576\n"
AW "a220,s13\n"
AW "a221,s13\n"
AW "a222,s13\n"
AW "a223,s13\n"
"s_waitcnt lgkmcnt(1)\n"
MF "v[92:95],v[16:19],v[136:139],v[32:35]\n"
"s_nop 0\n"
"ds_read_b128 v[24:27],v0 offset:25600\n"
"ds_read_b128 v[28:31],v1 offset:25600\n"
"s_waitcnt lgkmcnt(2)\n"
MF "v[92:95],v[20:23],v[156:159],v[92:95]\n"
"v_exp_f32 v84,v84\n"
MF "v[96:99],v[20:23],v[160:163],v[36:39]\n"
"v_exp_f32 v85,v85\n"
MF "v[96:99],v[16:19],v[140:143],v[96:99]\n"
"v_exp_f32 v86,v86\n"
MF "v[100:103],v[16:19],v[144:147],v[40:43]\n"
"v_exp_f32 v87,v87\n"
MF "v[100:103],v[20:23],v[164:167],v[100:103]\n"
"v_add_f32 v55,v55,v84\n"
"v_add_f32 v55,v55,v85\n"
MF "v[104:107],v[20:23],v[168:171],v[44:47]\n"
CV "v84,v84,v85\n"
"v_add_f32 v55,v55,v86\n"
MF "v[104:107],v[16:19],v[148:151],v[104:107]\n"
"v_add_f32 v55,v55,v87\n"
CV "v85,v86,v87\n"
MF "v[108:111],v[16:19],v[152:155],v[48:51]\n"
"v_exp_f32 v124,v124\n"
MF "v[108:111],v[20:23],v[172:175],v[108:111]\n"
"s_waitcnt lgkmcnt(0)\n"
"ds_read_b128 v[16:19],v0 offset:26624\n"
"ds_read_b128 v[20:23],v1 offset:26624\n"
MF "v[108:111],v[24:27],v[192:195],v[108:111]\n"
"v_exp_f32 v125,v125\n"
MF "v[108:111],v[28:31],v[212:215],v[108:111]\n"
"v_exp_f32 v126,v126\n"
MF "v[104:107],v[28:31],v[208:211],v[104:107]\n"
"v_exp_f32 v127,v127\n"
MF "v[104:107],v[24:27],v[188:191],v[104:107]\n"
"v_add_f32 v55,v55,v124\n"
"v_add_f32 v55,v55,v125\n"
MF "v[100:103],v[24:27],v[184:187],v[100:103]\n"
CV "v86,v124,v125\n"
"v_add_f32 v55,v55,v126\n"
MF "v[100:103],v[28:31],v[204:207],v[100:103]\n"
"v_add_f32 v55,v55,v127\n"
CV "v87,v126,v127\n"
MF "v[96:99],v[28:31],v[200:203],v[96:99]\n"
"v_exp_f32 v88,v88\n"
MF "v[96:99],v[24:27],v[180:183],v[96:99]\n"
"v_exp_f32 v89,v89\n"
MF "v[92:95],v[24:27],v[176:179],v[92:95]\n"
"v_exp_f32 v90,v90\n"
MF "v[92:95],v[28:31],v[196:199],v[92:95]\n"
"s_waitcnt lgkmcnt(0)\n"
"ds_read_b128 v[24:27],v0 offset:30720\n"
"ds_read_b128 v[28:31],v1 offset:30720\n"
MF "v[92:95],v[16:19],v[216:219],v[92:95]\n"
"v_exp_f32 v91,v91\n"
MF "v[92:95],v[20:23],v[236:239],v[92:95]\n"
"v_add_f32 v56,v56,v88\n"
"v_add_f32 v56,v56,v89\n"
MF "v[96:99],v[20:23],v[240:243],v[96:99]\n"
CV "v88,v88,v89\n"
"v_add_f32 v56,v56,v90\n"
MF "v[96:99],v[16:19],v[220:223],v[96:99]\n"
"v_add_f32 v56,v56,v91\n"
CV "v89,v90,v91\n"
MF "v[100:103],v[16:19],v[224:227],v[100:103]\n"
"v_exp_f32 v128,v128\n"
MF "v[100:103],v[20:23],v[244:247],v[100:103]\n"
"v_exp_f32 v129,v129\n"
MF "v[104:107],v[20:23],v[248:251],v[104:107]\n"
"v_exp_f32 v130,v130\n"
MF "v[104:107],v[16:19],v[228:231],v[104:107]\n"
"v_exp_f32 v131,v131\n"
MF "v[108:111],v[16:19],v[232:235],v[108:111]\n"
"v_add_f32 v56,v56,v128\n"
"v_add_f32 v56,v56,v129\n"
MF "v[108:111],v[20:23],v[252:255],v[108:111]\n"
"s_waitcnt lgkmcnt(0)\n"
"ds_read_b128 v[16:19],v0 offset:31744\n"
"ds_read_b128 v[20:23],v1 offset:31744\n"
MF "v[112:115],v[24:27],v[136:139],v[32:35]\n"
CV "v90,v128,v129\n"
"v_add_f32 v56,v56,v130\n"
MF "v[112:115],v[28:31],v[156:159],v[112:115]\n"
"v_add_f32 v56,v56,v131\n"
CV "v91,v130,v131\n"
MF "v[116:119],v[28:31],v[160:163],v[36:39]\n"
"v_exp_f32 v92,v92\n"
MF "v[116:119],v[24:27],v[140:143],v[116:119]\n"
"v_exp_f32 v93,v93\n"
MF "v[120:123],v[24:27],v[144:147],v[40:43]\n"
"v_exp_f32 v94,v94\n"
MF "v[120:123],v[28:31],v[164:167],v[120:123]\n"
"v_exp_f32 v95,v95\n"
MF "v[124:127],v[28:31],v[168:171],v[44:47]\n"
"v_add_f32 v57,v52,v92\n"
"v_add_f32 v57,v57,v93\n"
MF "v[124:127],v[24:27],v[148:151],v[124:127]\n"
CV "v92,v92,v93\n"
"v_add_f32 v57,v57,v94\n"
MF "v[128:131],v[24:27],v[152:155],v[48:51]\n"
"v_add_f32 v57,v57,v95\n"
CV "v93,v94,v95\n"
MF "v[128:131],v[28:31],v[172:175],v[128:131]\n"
"s_waitcnt lgkmcnt(0)\n"
"ds_read_b128 v[24:27],v0 offset:32768\n"
"ds_read_b128 v[28:31],v1 offset:32768\n"
MF "v[128:131],v[16:19],v[192:195],v[128:131]\n"
"v_exp_f32 v96,v96\n"
MF "v[128:131],v[20:23],v[212:215],v[128:131]\n"
"v_exp_f32 v97,v97\n"
MF "v[124:127],v[20:23],v[208:211],v[124:127]\n"
"v_exp_f32 v98,v98\n"
MF "v[124:127],v[16:19],v[188:191],v[124:127]\n"
"v_exp_f32 v99,v99\n"
MF "v[120:123],v[16:19],v[184:187],v[120:123]\n"
"v_add_f32 v58,v53,v96\n"
"v_add_f32 v58,v58,v97\n"
MF "v[120:123],v[20:23],v[204:207],v[120:123]\n"
CV "v96,v96,v97\n"
"v_add_f32 v58,v58,v98\n"
MF "v[116:119],v[20:23],v[200:203],v[116:119]\n"
"s_waitcnt vmcnt(6) lgkmcnt(0)\n"
"v_add_f32 v58,v58,v99\n"
CV "v97,v98,v99\n"
MF "v[116:119],v[16:19],v[180:183],v[116:119]\n"
"s_barrier\n"
MF "v[112:115],v[16:19],v[176:179],v[112:115]\n"
"v_exp_f32 v100,v100\n"
MF "v[112:115],v[20:23],v[196:199],v[112:115]\n"
"s_nop 0\n"
TR "v[16:17],v2 offset:49152\n"
TR "v[18:19],v2 offset:55296\n"
MF "v[112:115],v[24:27],v[216:219],v[112:115]\n"
"s_add_u32 s73,s73,12288\n"
TR "v[20:21],v3 offset:49152\n"
TR "v[22:23],v3 offset:55296\n"
MF "v[112:115],v[28:31],v[236:239],v[112:115]\n"
"s_cmp_eq_u32 s73,s19\n"
"s_cselect_b32 s73,s18,s73\n"
"s_add_u32 m0,s65,24576\n"
MF "v[116:119],v[28:31],v[240:243],v[116:119]\n"
LD "v8,s[36:39],s73 offen nt lds\n"
MF "v[116:119],v[24:27],v[220:223],v[116:119]\n"
"s_add_u32 m0,s65,25472\n"
"v_exp_f32 v101,v101\n"
MF "v[120:123],v[24:27],v[224:227],v[120:123]\n"
LD "v8,s[36:39],s73 offen offset:128 nt lds\n"
MF "v[120:123],v[28:31],v[244:247],v[120:123]\n"
"v_exp_f32 v102,v102\n"
MF "v[124:127],v[28:31],v[248:251],v[124:127]\n"
"s_add_u32 m0,s65,26368\n"
"v_exp_f32 v103,v103\n"
MF "v[124:127],v[24:27],v[228:231],v[124:127]\n"
LD "v8,s[36:39],s73 offen offset:256 nt lds\n"
MF "v[128:131],v[24:27],v[232:235],v[128:131]\n"
"v_add_f32 v59,v54,v100\n"
"v_add_f32 v59,v59,v101\n"
MF "v[128:131],v[28:31],v[252:255],v[128:131]\n"
"s_waitcnt lgkmcnt(0)\n"
TR "v[24:25],v4 offset:49152\n"
TR "v[26:27],v4 offset:55296\n"
MF "a[0:3],v[16:19],v[72:75],a[220:223]\n"
"s_add_u32 m0,s65,24576\n"
TR "v[28:29],v5 offset:49152\n"
TR "v[30:31],v5 offset:55296\n"
MF "a[20:23],v[20:23],v[72:75],a[220:223]\n"
LD "v8,s[36:39],s73 offen offset:3072 nt lds\n"
MF "a[24:27],v[20:23],v[76:79],a[220:223]\n"
CV "v100,v100,v101\n"
"v_add_f32 v59,v59,v102\n"
MF "a[4:7],v[16:19],v[76:79],a[220:223]\n"
"s_add_u32 m0,s65,25472\n"
"v_add_f32 v59,v59,v103\n"
CV "v101,v102,v103\n"
MF "a[8:11],v[16:19],v[80:83],a[220:223]\n"
LD "v8,s[36:39],s73 offen offset:3200 nt lds\n"
MF "a[28:31],v[20:23],v[80:83],a[220:223]\n"
"v_exp_f32 v104,v104\n"
MF "a[32:35],v[20:23],v[84:87],a[220:223]\n"
"s_add_u32 m0,s65,26368\n"
"v_exp_f32 v105,v105\n"
MF "a[12:15],v[16:19],v[84:87],a[220:223]\n"
LD "v8,s[36:39],s73 offen offset:3328 nt lds\n"
MF "a[16:19],v[16:19],v[88:91],a[220:223]\n"
"v_exp_f32 v106,v106\n"
MF "a[36:39],v[20:23],v[88:91],a[220:223]\n"
"s_waitcnt lgkmcnt(0)\n"
TR "v[16:17],v2 offset:50176\n"
TR "v[18:19],v2 offset:56320\n"
MF "a[56:59],v[24:27],v[88:91],a[220:223]\n"
"s_nop 0\n"
TR "v[20:21],v3 offset:50176\n"
TR "v[22:23],v3 offset:56320\n"
MF "a[76:79],v[28:31],v[88:91],a[220:223]\n"
"v_exp_f32 v107,v107\n"
MF "a[72:75],v[28:31],v[84:87],a[220:223]\n"
"v_add_f32 v60,v55,v104\n"
"v_add_f32 v60,v60,v105\n"
MF "a[52:55],v[24:27],v[84:87],a[220:223]\n"
CV "v104,v104,v105\n"
"v_add_f32 v60,v60,v106\n"
MF "a[48:51],v[24:27],v[80:83],a[220:223]\n"
"v_add_f32 v60,v60,v107\n"
CV "v105,v106,v107\n"
MF "a[68:71],v[28:31],v[80:83],a[220:223]\n"
"v_exp_f32 v108,v108\n"
MF "a[64:67],v[28:31],v[76:79],a[220:223]\n"
"v_exp_f32 v109,v109\n"
MF "a[44:47],v[24:27],v[76:79],a[220:223]\n"
"v_exp_f32 v110,v110\n"
MF "a[40:43],v[24:27],v[72:75],a[220:223]\n"
"v_exp_f32 v111,v111\n"
MF "a[60:63],v[28:31],v[72:75],a[220:223]\n"
"s_waitcnt lgkmcnt(0)\n"
TR "v[24:25],v4 offset:50176\n"
TR "v[26:27],v4 offset:56320\n"
MF "a[80:83],v[16:19],v[72:75],a[220:223]\n"
"s_nop 0\n"
TR "v[28:29],v5 offset:50176\n"
TR "v[30:31],v5 offset:56320\n"
MF "a[100:103],v[20:23],v[72:75],a[220:223]\n"
"v_add_f32 v61,v56,v108\n"
"v_add_f32 v61,v61,v109\n"
MF "a[104:107],v[20:23],v[76:79],a[220:223]\n"
CV "v108,v108,v109\n"
"v_add_f32 v61,v61,v110\n"
MF "a[84:87],v[16:19],v[76:79],a[220:223]\n"
"v_add_f32 v61,v61,v111\n"
CV "v109,v110,v111\n"
MF "a[88:91],v[16:19],v[80:83],a[220:223]\n"
"v_exp_f32 v112,v112\n"
MF "a[108:111],v[20:23],v[80:83],a[220:223]\n"
"v_exp_f32 v113,v113\n"
MF "a[112:115],v[20:23],v[84:87],a[220:223]\n"
"v_exp_f32 v114,v114\n"
MF "a[92:95],v[16:19],v[84:87],a[220:223]\n"
"v_exp_f32 v115,v115\n"
MF "a[96:99],v[16:19],v[88:91],a[220:223]\n"
"v_add_f32 v57,v57,v112\n"
"v_add_f32 v57,v57,v113\n"
MF "a[116:119],v[20:23],v[88:91],a[220:223]\n"
"s_waitcnt lgkmcnt(0)\n"
TR "v[16:17],v2 offset:51200\n"
TR "v[18:19],v2 offset:57344\n"
MF "a[136:139],v[24:27],v[88:91],a[220:223]\n"
"s_nop 0\n"
TR "v[20:21],v3 offset:51200\n"
TR "v[22:23],v3 offset:57344\n"
MF "a[156:159],v[28:31],v[88:91],a[220:223]\n"
CV "v94,v112,v113\n"
"v_add_f32 v57,v57,v114\n"
MF "a[152:155],v[28:31],v[84:87],a[220:223]\n"
"v_add_f32 v57,v57,v115\n"
CV "v95,v114,v115\n"
MF "a[132:135],v[24:27],v[84:87],a[220:223]\n"
"v_exp_f32 v116,v116\n"
MF "a[128:131],v[24:27],v[80:83],a[220:223]\n"
"v_exp_f32 v117,v117\n"
MF "a[148:151],v[28:31],v[80:83],a[220:223]\n"
"v_exp_f32 v118,v118\n"
MF "a[144:147],v[28:31],v[76:79],a[220:223]\n"
"v_exp_f32 v119,v119\n"
MF "a[124:127],v[24:27],v[76:79],a[220:223]\n"
"v_add_f32 v58,v58,v116\n"
"v_add_f32 v58,v58,v117\n"
MF "a[120:123],v[24:27],v[72:75],a[220:223]\n"
CV "v98,v116,v117\n"
"v_add_f32 v58,v58,v118\n"
MF "a[140:143],v[28:31],v[72:75],a[220:223]\n"
"s_waitcnt lgkmcnt(0)\n"
TR "v[24:25],v4 offset:51200\n"
TR "v[26:27],v4 offset:57344\n"
MF "a[160:163],v[16:19],v[72:75],a[220:223]\n"
"s_nop 0\n"
TR "v[28:29],v5 offset:51200\n"
TR "v[30:31],v5 offset:57344\n"
MF "a[180:183],v[20:23],v[72:75],a[220:223]\n"
"v_add_f32 v58,v58,v119\n"
CV "v99,v118,v119\n"
MF "a[184:187],v[20:23],v[76:79],a[220:223]\n"
"v_exp_f32 v120,v120\n"
MF "a[164:167],v[16:19],v[76:79],a[220:223]\n"
"v_exp_f32 v121,v121\n"
MF "a[168:171],v[16:19],v[80:83],a[220:223]\n"
"v_exp_f32 v122,v122\n"
MF "a[188:191],v[20:23],v[80:83],a[220:223]\n"
"v_exp_f32 v123,v123\n"
MF "a[192:195],v[20:23],v[84:87],a[220:223]\n"
"v_add_f32 v59,v59,v120\n"
"v_add_f32 v59,v59,v121\n"
MF "a[172:175],v[16:19],v[84:87],a[220:223]\n"
"s_waitcnt vmcnt(6) lgkmcnt(0)\n"
CV "v102,v120,v121\n"
"v_add_f32 v59,v59,v122\n"
MF "a[176:179],v[16:19],v[88:91],a[220:223]\n"
"s_barrier\n"
MF "a[196:199],v[20:23],v[88:91],a[220:223]\n"
"s_add_u32 s72,s72,6144\n"
"ds_read_b128 v[16:19],v0\n"
"ds_read_b128 v[20:23],v1\n"
MF "a[216:219],v[24:27],v[88:91],a[220:223]\n"
"s_cmp_eq_u32 s72,s17\n"
"s_cselect_b32 s72,s16,s72\n"
"s_add_u32 m0,s64,49152\n"
MF "a[236:239],v[28:31],v[88:91],a[220:223]\n"
LD "v7,s[32:35],s72 offen nt lds\n"
MF "a[232:235],v[28:31],v[84:87],a[220:223]\n"
"s_add_u32 m0,s64,50048\n"
"v_add_f32 v59,v59,v123\n"
CV "v103,v122,v123\n"
MF "a[212:215],v[24:27],v[84:87],a[220:223]\n"
LD "v7,s[32:35],s72 offen offset:128 nt lds\n"
MF "a[208:211],v[24:27],v[80:83],a[220:223]\n"
"s_add_u32 m0,s64,50944\n"
MF "a[228:231],v[28:31],v[80:83],a[220:223]\n"
LD "v7,s[32:35],s72 offen offset:256 nt lds\n"
MF "a[224:227],v[28:31],v[76:79],a[220:223]\n"
MF "a[204:207],v[24:27],v[76:79],a[220:223]\n"
MF "a[200:203],v[24:27],v[72:75],a[220:223]\n"
MF "a[220:223],v[28:31],v[72:75],a[220:223]\n"
"s_waitcnt lgkmcnt(0)\n"
".Laloop:\n"
MF "v[72:75],v[16:19],v[136:139],v[32:35]\n"
"s_nop 0\n"
"ds_read_b128 v[24:27],v0 offset:1024\n"
"ds_read_b128 v[28:31],v1 offset:1024\n"
MF "v[72:75],v[20:23],v[156:159],v[72:75]\n"
MF "v[76:79],v[20:23],v[160:163],v[36:39]\n"
MF "v[76:79],v[16:19],v[140:143],v[76:79]\n"
MF "v[80:83],v[16:19],v[144:147],v[40:43]\n"
MF "v[80:83],v[20:23],v[164:167],v[80:83]\n"
MF "v[84:87],v[20:23],v[168:171],v[44:47]\n"
MF "v[84:87],v[16:19],v[148:151],v[84:87]\n"
"s_add_u32 s72,s72,6144\n"
"s_add_u32 m0,s64,55296\n"
MF "v[88:91],v[16:19],v[152:155],v[48:51]\n"
LD "v7,s[32:35],s72 offen nt lds\n"
MF "v[88:91],v[20:23],v[172:175],v[88:91]\n"
"s_waitcnt lgkmcnt(0)\n"
"ds_read_b128 v[16:19],v0 offset:2048\n"
"ds_read_b128 v[20:23],v1 offset:2048\n"
MF "v[88:91],v[24:27],v[192:195],v[88:91]\n"
"v_exp_f32 v124,v124\n"
MF "v[88:91],v[28:31],v[212:215],v[88:91]\n"
"s_add_u32 m0,s64,56192\n"
"v_exp_f32 v125,v125\n"
MF "v[84:87],v[28:31],v[208:211],v[84:87]\n"
LD "v7,s[32:35],s72 offen offset:128 nt lds\n"
MF "v[84:87],v[24:27],v[188:191],v[84:87]\n"
"v_exp_f32 v126,v126\n"
MF "v[80:83],v[24:27],v[184:187],v[80:83]\n"
"v_exp_f32 v127,v127\n"
MF "v[80:83],v[28:31],v[204:207],v[80:83]\n"
"s_add_u32 m0,s64,57088\n"
"v_add_f32 v60,v60,v124\n"
"v_add_f32 v60,v60,v125\n"
MF "v[76:79],v[28:31],v[200:203],v[76:79]\n"
LD "v7,s[32:35],s72 offen offset:256 nt lds\n"
MF "v[76:79],v[24:27],v[180:183],v[76:79]\n"
CV "v106,v124,v125\n"
"v_add_f32 v60,v60,v126\n"
MF "v[72:75],v[24:27],v[176:179],v[72:75]\n"
"v_add_f32 v60,v60,v127\n"
CV "v107,v126,v127\n"
MF "v[72:75],v[28:31],v[196:199],v[72:75]\n"
"s_waitcnt lgkmcnt(0)\n"
"ds_read_b128 v[24:27],v0 offset:6144\n"
"ds_read_b128 v[28:31],v1 offset:6144\n"
MF "v[72:75],v[16:19],v[216:219],v[72:75]\n"
"v_exp_f32 v128,v128\n"
MF "v[72:75],v[20:23],v[236:239],v[72:75]\n"
"v_exp_f32 v129,v129\n"
MF "v[76:79],v[20:23],v[240:243],v[76:79]\n"
"v_exp_f32 v130,v130\n"
MF "v[76:79],v[16:19],v[220:223],v[76:79]\n"
"v_exp_f32 v131,v131\n"
MF "v[80:83],v[16:19],v[224:227],v[80:83]\n"
"v_add_f32 v61,v61,v128\n"
"v_add_f32 v61,v61,v129\n"
MF "v[80:83],v[20:23],v[244:247],v[80:83]\n"
CV "v110,v128,v129\n"
"v_add_f32 v61,v61,v130\n"
MF "v[84:87],v[20:23],v[248:251],v[84:87]\n"
"v_add_f32 v61,v61,v131\n"
CV "v111,v130,v131\n"
MF "v[84:87],v[16:19],v[228:231],v[84:87]\n"
"v_max3_f32 v10,v57,v58,v59\n"
"v_max3_f32 v10,v10,v60,v61\n"
MF "v[88:91],v[16:19],v[232:235],v[88:91]\n"
"v_cmp_ngt_f32 vcc,s77,v10\n"
MF "v[88:91],v[20:23],v[252:255],v[88:91]\n"
"s_waitcnt lgkmcnt(0)\n"
"s_cbranch_vccnz .Lstub_20\n"
".Lback_20:\n"
MF "v[112:115],v[24:27],v[136:139],v[32:35]\n"
"s_add_u32 s74,s74,12288\n"
"ds_read_b128 v[16:19],v0 offset:7168\n"
"ds_read_b128 v[20:23],v1 offset:7168\n"
MF "v[112:115],v[28:31],v[156:159],v[112:115]\n"
"s_cmp_eq_u32 s74,s20\n"
"s_cselect_b32 s74,s70,s74\n"
MF "v[116:119],v[28:31],v[160:163],v[36:39]\n"
"v_exp_f32 v72,v72\n"
MF "v[116:119],v[24:27],v[140:143],v[116:119]\n"
"v_exp_f32 v73,v73\n"
MF "v[120:123],v[24:27],v[144:147],v[40:43]\n"
"v_exp_f32 v74,v74\n"
MF "v[120:123],v[28:31],v[164:167],v[120:123]\n"
"v_exp_f32 v75,v75\n"
MF "v[124:127],v[28:31],v[168:171],v[44:47]\n"
"v_add_f32 v52,v57,v72\n"
"v_add_f32 v52,v52,v73\n"
MF "v[124:127],v[24:27],v[148:151],v[124:127]\n"
CV "v72,v72,v73\n"
"v_add_f32 v52,v52,v74\n"
MF "v[128:131],v[24:27],v[152:155],v[48:51]\n"
"v_add_f32 v52,v52,v75\n"
CV "v73,v74,v75\n"
MF "v[128:131],v[28:31],v[172:175],v[128:131]\n"
"s_waitcnt lgkmcnt(0)\n"
"ds_read_b128 v[24:27],v0 offset:8192\n"
"ds_read_b128 v[28:31],v1 offset:8192\n"
MF "v[128:131],v[16:19],v[192:195],v[128:131]\n"
"v_exp_f32 v76,v76\n"
MF "v[128:131],v[20:23],v[212:215],v[128:131]\n"
"v_exp_f32 v77,v77\n"
MF "v[124:127],v[20:23],v[208:211],v[124:127]\n"
"v_exp_f32 v78,v78\n"
MF "v[124:127],v[16:19],v[188:191],v[124:127]\n"
"v_exp_f32 v79,v79\n"
MF "v[120:123],v[16:19],v[184:187],v[120:123]\n"
"v_add_f32 v53,v58,v76\n"
"v_add_f32 v53,v53,v77\n"
MF "v[120:123],v[20:23],v[204:207],v[120:123]\n"
CV "v76,v76,v77\n"
"v_add_f32 v53,v53,v78\n"
MF "v[116:119],v[20:23],v[200:203],v[116:119]\n"
"s_waitcnt vmcnt(6) lgkmcnt(0)\n"
"v_add_f32 v53,v53,v79\n"
CV "v77,v78,v79\n"
MF "v[116:119],v[16:19],v[180:183],v[116:119]\n"
"s_barrier\n"
MF "v[112:115],v[16:19],v[176:179],v[112:115]\n"
"v_exp_f32 v80,v80\n"
MF "v[112:115],v[20:23],v[196:199],v[112:115]\n"
"s_nop 0\n"
TR "v[16:17],v2 offset:24576\n"
TR "v[18:19],v2 offset:30720\n"
MF "v[112:115],v[24:27],v[216:219],v[112:115]\n"
"s_add_u32 s73,s73,12288\n"
TR "v[20:21],v3 offset:24576\n"
TR "v[22:23],v3 offset:30720\n"
MF "v[112:115],v[28:31],v[236:239],v[112:115]\n"
"s_cmp_eq_u32 s73,s19\n"
"s_cselect_b32 s73,s18,s73\n"
"s_add_u32 m0,s65,0\n"
MF "v[116:119],v[28:31],v[240:243],v[116:119]\n"
LD "v8,s[36:39],s73 offen nt lds\n"
MF "v[116:119],v[24:27],v[220:223],v[116:119]\n"
"s_add_u32 m0,s65,896\n"
"v_exp_f32 v81,v81\n"
MF "v[120:123],v[24:27],v[224:227],v[120:123]\n"
LD "v8,s[36:39],s73 offen offset:128 nt lds\n"
MF "v[120:123],v[28:31],v[244:247],v[120:123]\n"
"v_exp_f32 v82,v82\n"
MF "v[124:127],v[28:31],v[248:251],v[124:127]\n"
"s_add_u32 m0,s65,1792\n"
"v_exp_f32 v83,v83\n"
MF "v[124:127],v[24:27],v[228:231],v[124:127]\n"
LD "v8,s[36:39],s73 offen offset:256 nt lds\n"
MF "v[128:131],v[24:27],v[232:235],v[128:131]\n"
"v_add_f32 v54,v59,v80\n"
"v_add_f32 v54,v54,v81\n"
MF "v[128:131],v[28:31],v[252:255],v[128:131]\n"
"s_waitcnt lgkmcnt(0)\n"
TR "v[24:25],v4 offset:24576\n"
TR "v[26:27],v4 offset:30720\n"
MF "a[0:3],v[16:19],v[92:95],a[0:3]\n"
"s_add_u32 m0,s65,0\n"
TR "v[28:29],v5 offset:24576\n"
TR "v[30:31],v5 offset:30720\n"
MF "a[20:23],v[20:23],v[92:95],a[20:23]\n"
LD "v8,s[36:39],s73 offen offset:3072 nt lds\n"
MF "a[24:27],v[20:23],v[96:99],a[24:27]\n"
CV "v80,v80,v81\n"
"v_add_f32 v54,v54,v82\n"
MF "a[4:7],v[16:19],v[96:99],a[4:7]\n"
"s_add_u32 m0,s65,896\n"
"v_add_f32 v54,v54,v83\n"
CV "v81,v82,v83\n"
MF "a[8:11],v[16:19],v[100:103],a[8:11]\n"
LD "v8,s[36:39],s73 offen offset:3200 nt lds\n"
MF "a[28:31],v[20:23],v[100:103],a[28:31]\n"
"v_exp_f32 v84,v84\n"
MF "a[32:35],v[20:23],v[104:107],a[32:35]\n"
"s_add_u32 m0,s65,1792\n"
"v_exp_f32 v85,v85\n"
MF "a[12:15],v[16:19],v[104:107],a[12:15]\n"
LD "v8,s[36:39],s73 offen offset:3328 nt lds\n"
MF "a[16:19],v[16:19],v[108:111],a[16:19]\n"
"v_exp_f32 v86,v86\n"
MF "a[36:39],v[20:23],v[108:111],a[36:39]\n"
"s_waitcnt lgkmcnt(0)\n"
TR "v[16:17],v2 offset:25600\n"
TR "v[18:19],v2 offset:31744\n"
MF "a[56:59],v[24:27],v[108:111],a[56:59]\n"
"s_nop 0\n"
TR "v[20:21],v3 offset:25600\n"
TR "v[22:23],v3 offset:31744\n"
MF "a[76:79],v[28:31],v[108:111],a[76:79]\n"
"v_exp_f32 v87,v87\n"
MF "a[72:75],v[28:31],v[104:107],a[72:75]\n"
"v_add_f32 v55,v60,v84\n"
"v_add_f32 v55,v55,v85\n"
MF "a[52:55],v[24:27],v[104:107],a[52:55]\n"
CV "v84,v84,v85\n"
"v_add_f32 v55,v55,v86\n"
MF "a[48:51],v[24:27],v[100:103],a[48:51]\n"
"v_add_f32 v55,v55,v87\n"
CV "v85,v86,v87\n"
MF "a[68:71],v[28:31],v[100:103],a[68:71]\n"
"v_exp_f32 v88,v88\n"
MF "a[64:67],v[28:31],v[96:99],a[64:67]\n"
"v_exp_f32 v89,v89\n"
MF "a[44:47],v[24:27],v[96:99],a[44:47]\n"
"v_exp_f32 v90,v90\n"
MF "a[40:43],v[24:27],v[92:95],a[40:43]\n"
"v_exp_f32 v91,v91\n"
MF "a[60:63],v[28:31],v[92:95],a[60:63]\n"
"s_waitcnt lgkmcnt(0)\n"
TR "v[24:25],v4 offset:25600\n"
TR "v[26:27],v4 offset:31744\n"
MF "a[80:83],v[16:19],v[92:95],a[80:83]\n"
"s_nop 0\n"
TR "v[28:29],v5 offset:25600\n"
TR "v[30:31],v5 offset:31744\n"
MF "a[100:103],v[20:23],v[92:95],a[100:103]\n"
"v_add_f32 v56,v61,v88\n"
"v_add_f32 v56,v56,v89\n"
MF "a[104:107],v[20:23],v[96:99],a[104:107]\n"
CV "v88,v88,v89\n"
"v_add_f32 v56,v56,v90\n"
MF "a[84:87],v[16:19],v[96:99],a[84:87]\n"
"v_add_f32 v56,v56,v91\n"
CV "v89,v90,v91\n"
MF "a[88:91],v[16:19],v[100:103],a[88:91]\n"
"v_exp_f32 v112,v112\n"
MF "a[108:111],v[20:23],v[100:103],a[108:111]\n"
"v_exp_f32 v113,v113\n"
MF "a[112:115],v[20:23],v[104:107],a[112:115]\n"
"v_exp_f32 v114,v114\n"
MF "a[92:95],v[16:19],v[104:107],a[92:95]\n"
"v_exp_f32 v115,v115\n"
MF "a[96:99],v[16:19],v[108:111],a[96:99]\n"
"v_add_f32 v52,v52,v112\n"
"v_add_f32 v52,v52,v113\n"
MF "a[116:119],v[20:23],v[108:111],a[116:119]\n"
"s_waitcnt lgkmcnt(0)\n"
TR "v[16:17],v2 offset:26624\n"
TR "v[18:19],v2 offset:32768\n"
MF "a[136:139],v[24:27],v[108:111],a[136:139]\n"
"s_nop 0\n"
TR "v[20:21],v3 offset:26624\n"
TR "v[22:23],v3 offset:32768\n"
MF "a[156:159],v[28:31],v[108:111],a[156:159]\n"
CV "v74,v112,v113\n"
"v_add_f32 v52,v52,v114\n"
MF "a[152:155],v[28:31],v[104:107],a[152:155]\n"
"v_add_f32 v52,v52,v115\n"
CV "v75,v114,v115\n"
MF "a[132:135],v[24:27],v[104:107],a[132:135]\n"
"v_exp_f32 v116,v116\n"
MF "a[128:131],v[24:27],v[100:103],a[128:131]\n"
"v_exp_f32 v117,v117\n"
MF "a[148:151],v[28:31],v[100:103],a[148:151]\n"
"v_exp_f32 v118,v118\n"
MF "a[144:147],v[28:31],v[96:99],a[144:147]\n"
"v_exp_f32 v119,v119\n"
MF "a[124:127],v[24:27],v[96:99],a[124:127]\n"
"v_add_f32 v53,v53,v116\n"
"v_add_f32 v53,v53,v117\n"
MF "a[120:123],v[24:27],v[92:95],a[120:123]\n"
CV "v78,v116,v117\n"
"v_add_f32 v53,v53,v118\n"
MF "a[140:143],v[28:31],v[92:95],a[140:143]\n"
"s_waitcnt lgkmcnt(0)\n"
TR "v[24:25],v4 offset:26624\n"
TR "v[26:27],v4 offset:32768\n"
MF "a[160:163],v[16:19],v[92:95],a[160:163]\n"
"s_nop 0\n"
TR "v[28:29],v5 offset:26624\n"
TR "v[30:31],v5 offset:32768\n"
MF "a[180:183],v[20:23],v[92:95],a[180:183]\n"
"v_add_f32 v53,v53,v119\n"
CV "v79,v118,v119\n"
MF "a[184:187],v[20:23],v[96:99],a[184:187]\n"
"v_exp_f32 v120,v120\n"
MF "a[164:167],v[16:19],v[96:99],a[164:167]\n"
"v_exp_f32 v121,v121\n"
MF "a[168:171],v[16:19],v[100:103],a[168:171]\n"
"v_exp_f32 v122,v122\n"
MF "a[188:191],v[20:23],v[100:103],a[188:191]\n"
"v_exp_f32 v123,v123\n"
MF "a[192:195],v[20:23],v[104:107],a[192:195]\n"
"v_add_f32 v54,v54,v120\n"
"v_add_f32 v54,v54,v121\n"
MF "a[172:175],v[16:19],v[104:107],a[172:175]\n"
"s_waitcnt vmcnt(6) lgkmcnt(0)\n"
CV "v82,v120,v121\n"
"v_add_f32 v54,v54,v122\n"
MF "a[176:179],v[16:19],v[108:111],a[176:179]\n"
"s_barrier\n"
MF "a[196:199],v[20:23],v[108:111],a[196:199]\n"
"s_add_u32 s72,s72,6144\n"
"ds_read_b128 v[16:19],v0 offset:49152\n"
"ds_read_b128 v[20:23],v1 offset:49152\n"
MF "a[216:219],v[24:27],v[108:111],a[216:219]\n"
"s_cmp_eq_u32 s72,s17\n"
"s_cselect_b32 s72,s16,s72\n"
"s_add_u32 m0,s64,24576\n"
MF "a[236:239],v[28:31],v[108:111],a[236:239]\n"
LD "v7,s[32:35],s72 offen nt lds\n"
MF "a[232:235],v[28:31],v[104:107],a[232:235]\n"
"s_add_u32 m0,s64,25472\n"
"v_add_f32 v54,v54,v123\n"
CV "v83,v122,v123\n"
MF "a[212:215],v[24:27],v[104:107],a[212:215]\n"
LD "v7,s[32:35],s72 offen offset:128 nt lds\n"
MF "a[208:211],v[24:27],v[100:103],a[208:211]\n"
"s_add_u32 m0,s64,26368\n"
MF "a[228:231],v[28:31],v[100:103],a[228:231]\n"
LD "v7,s[32:35],s72 offen offset:256 nt lds\n"
MF "a[224:227],v[28:31],v[96:99],a[224:227]\n"
MF "a[204:207],v[24:27],v[96:99],a[204:207]\n"
MF "a[200:203],v[24:27],v[92:95],a[200:203]\n"
MF "a[220:223],v[28:31],v[92:95],a[220:223]\n"
"s_waitcnt lgkmcnt(0)\n"
MF "v[92:95],v[16:19],v[136:139],v[32:35]\n"
"s_add_u32 s72,s72,6144\n"
"ds_read_b128 v[24:27],v0 offset:50176\n"
"ds_read_b128 v[28:31],v1 offset:50176\n"
MF "v[92:95],v[20:23],v[156:159],v[92:95]\n"
"s_add_u32 m0,s64,30720\n"
"v_exp_f32 v124,v124\n"
MF "v[96:99],v[20:23],v[160:163],v[36:39]\n"
LD "v7,s[32:35],s72 offen nt lds\n"
MF "v[96:99],v[16:19],v[140:143],v[96:99]\n"
"v_exp_f32 v125,v125\n"
MF "v[100:103],v[16:19],v[144:147],v[40:43]\n"
"s_add_u32 m0,s64,31616\n"
"v_exp_f32 v126,v126\n"
MF "v[100:103],v[20:23],v[164:167],v[100:103]\n"
LD "v7,s[32:35],s72 offen offset:128 nt lds\n"
MF "v[104:107],v[20:23],v[168:171],v[44:47]\n"
"v_exp_f32 v127,v127\n"
MF "v[104:107],v[16:19],v[148:151],v[104:107]\n"
"s_add_u32 m0,s64,32512\n"
"v_add_f32 v55,v55,v124\n"
"v_add_f32 v55,v55,v125\n"
MF "v[108:111],v[16:19],v[152:155],v[48:51]\n"
LD "v7,s[32:35],s72 offen offset:256 nt lds\n"
MF "v[108:111],v[20:23],v[172:175],v[108:111]\n"
"s_waitcnt lgkmcnt(0)\n"
"ds_read_b128 v[16:19],v0 offset:51200\n"
"ds_read_b128 v[20:23],v1 offset:51200\n"
MF "v[108:111],v[24:27],v[192:195],v[108:111]\n"
CV "v86,v124,v125\n"
"v_add_f32 v55,v55,v126\n"
MF "v[108:111],v[28:31],v[212:215],v[108:111]\n"
"v_add_f32 v55,v55,v127\n"
CV "v87,v126,v127\n"
MF "v[104:107],v[28:31],v[208:211],v[104:107]\n"
"v_exp_f32 v128,v128\n"
MF "v[104:107],v[24:27],v[188:191],v[104:107]\n"
"v_exp_f32 v129,v129\n"
MF "v[100:103],v[24:27],v[184:187],v[100:103]\n"
"v_exp_f32 v130,v130\n"
MF "v[100:103],v[28:31],v[204:207],v[100:103]\n"
"v_exp_f32 v131,v131\n"
MF "v[96:99],v[28:31],v[200:203],v[96:99]\n"
"v_add_f32 v56,v56,v128\n"
"v_add_f32 v56,v56,v129\n"
MF "v[96:99],v[24:27],v[180:183],v[96:99]\n"
CV "v90,v128,v129\n"
"v_add_f32 v56,v56,v130\n"
MF "v[92:95],v[24:27],v[176:179],v[92:95]\n"
"v_add_f32 v56,v56,v131\n"
CV "v91,v130,v131\n"
MF "v[92:95],v[28:31],v[196:199],v[92:95]\n"
"s_waitcnt lgkmcnt(0)\n"
"ds_read_b128 v[24:27],v0 offset:55296\n"
"ds_read_b128 v[28:31],v1 offset:55296\n"
MF "v[92:95],v[16:19],v[216:219],v[92:95]\n"
MF "v[92:95],v[20:23],v[236:239],v[92:95]\n"
MF "v[96:99],v[20:23],v[240:243],v[96:99]\n"
MF "v[96:99],v[16:19],v[220:223],v[96:99]\n"
MF "v[100:103],v[16:19],v[224:227],v[100:103]\n"
MF "v[100:103],v[20:23],v[244:247],v[100:103]\n"
MF "v[104:107],v[20:23],v[248:251],v[104:107]\n"
MF "v[104:107],v[16:19],v[228:231],v[104:107]\n"
"v_max3_f32 v10,v52,v53,v54\n"
"v_max3_f32 v10,v10,v55,v56\n"
MF "v[108:111],v[16:19],v[232:235],v[108:111]\n"
"v_cmp_ngt_f32 vcc,s77,v10\n"
MF "v[108:111],v[20:23],v[252:255],v[108:111]\n"
"s_waitcnt lgkmcnt(0)\n"
"s_cbranch_vccnz .Lstub_21\n"
".Lback_21:\n"
MF "v[112:115],v[24:27],v[136:139],v[32:35]\n"
"s_add_u32 s74,s74,12288\n"
"ds_read_b128 v[16:19],v0 offset:56320\n"
"ds_read_b128 v[20:23],v1 offset:56320\n"
MF "v[112:115],v[28:31],v[156:159],v[112:115]\n"
"s_cmp_eq_u32 s74,s20\n"
"s_cselect_b32 s74,s70,s74\n"
MF "v[116:119],v[28:31],v[160:163],v[36:39]\n"
"v_exp_f32 v92,v92\n"
MF "v[116:119],v[24:27],v[140:143],v[116:119]\n"
"v_exp_f32 v93,v93\n"
MF "v[120:123],v[24:27],v[144:147],v[40:43]\n"
"v_exp_f32 v94,v94\n"
MF "v[120:123],v[28:31],v[164:167],v[120:123]\n"
"v_exp_f32 v95,v95\n"
MF "v[124:127],v[28:31],v[168:171],v[44:47]\n"
"v_add_f32 v57,v52,v92\n"
"v_add_f32 v57,v57,v93\n"
MF "v[124:127],v[24:27],v[148:151],v[124:127]\n"
CV "v92,v92,v93\n"
"v_add_f32 v57,v57,v94\n"
MF "v[128:131],v[24:27],v[152:155],v[48:51]\n"
"v_add_f32 v57,v57,v95\n"
CV "v93,v94,v95\n"
MF "v[128:131],v[28:31],v[172:175],v[128:131]\n"
"s_waitcnt lgkmcnt(0)\n"
"ds_read_b128 v[24:27],v0 offset:57344\n"
"ds_read_b128 v[28:31],v1 offset:57344\n"
MF "v[128:131],v[16:19],v[192:195],v[128:131]\n"
"v_exp_f32 v96,v96\n"
MF "v[128:131],v[20:23],v[212:215],v[128:131]\n"
"v_exp_f32 v97,v97\n"
MF "v[124:127],v[20:23],v[208:211],v[124:127]\n"
"v_exp_f32 v98,v98\n"
MF "v[124:127],v[16:19],v[188:191],v[124:127]\n"
"v_exp_f32 v99,v99\n"
MF "v[120:123],v[16:19],v[184:187],v[120:123]\n"
"v_add_f32 v58,v53,v96\n"
"v_add_f32 v58,v58,v97\n"
MF "v[120:123],v[20:23],v[204:207],v[120:123]\n"
CV "v96,v96,v97\n"
"v_add_f32 v58,v58,v98\n"
MF "v[116:119],v[20:23],v[200:203],v[116:119]\n"
"s_waitcnt vmcnt(6) lgkmcnt(0)\n"
"v_add_f32 v58,v58,v99\n"
CV "v97,v98,v99\n"
MF "v[116:119],v[16:19],v[180:183],v[116:119]\n"
"s_barrier\n"
MF "v[112:115],v[16:19],v[176:179],v[112:115]\n"
"v_exp_f32 v100,v100\n"
MF "v[112:115],v[20:23],v[196:199],v[112:115]\n"
"s_nop 0\n"
TR "v[16:17],v2\n"
TR "v[18:19],v2 offset:6144\n"
MF "v[112:115],v[24:27],v[216:219],v[112:115]\n"
"s_add_u32 s73,s73,12288\n"
TR "v[20:21],v3\n"
TR "v[22:23],v3 offset:6144\n"
MF "v[112:115],v[28:31],v[236:239],v[112:115]\n"
"s_cmp_eq_u32 s73,s19\n"
"s_cselect_b32 s73,s18,s73\n"
"s_add_u32 m0,s65,49152\n"
MF "v[116:119],v[28:31],v[240:243],v[116:119]\n"
LD "v8,s[36:39],s73 offen nt lds\n"
MF "v[116:119],v[24:27],v[220:223],v[116:119]\n"
"s_add_u32 m0,s65,50048\n"
"v_exp_f32 v101,v101\n"
MF "v[120:123],v[24:27],v[224:227],v[120:123]\n"
LD "v8,s[36:39],s73 offen offset:128 nt lds\n"
MF "v[120:123],v[28:31],v[244:247],v[120:123]\n"
"v_exp_f32 v102,v102\n"
MF "v[124:127],v[28:31],v[248:251],v[124:127]\n"
"s_add_u32 m0,s65,50944\n"
"v_exp_f32 v103,v103\n"
MF "v[124:127],v[24:27],v[228:231],v[124:127]\n"
LD "v8,s[36:39],s73 offen offset:256 nt lds\n"
MF "v[128:131],v[24:27],v[232:235],v[128:131]\n"
"v_add_f32 v59,v54,v100\n"
"v_add_f32 v59,v59,v101\n"
MF "v[128:131],v[28:31],v[252:255],v[128:131]\n"
"s_waitcnt lgkmcnt(0)\n"
TR "v[24:25],v4\n"
TR "v[26:27],v4 offset:6144\n"
MF "a[0:3],v[16:19],v[72:75],a[0:3]\n"
"s_add_u32 m0,s65,49152\n"
TR "v[28:29],v5\n"
TR "v[30:31],v5 offset:6144\n"
MF "a[20:23],v[20:23],v[72:75],a[20:23]\n"
LD "v8,s[36:39],s73 offen offset:3072 nt lds\n"
MF "a[24:27],v[20:23],v[76:79],a[24:27]\n"
CV "v100,v100,v101\n"
"v_add_f32 v59,v59,v102\n"
MF "a[4:7],v[16:19],v[76:79],a[4:7]\n"
"s_add_u32 m0,s65,50048\n"
"v_add_f32 v59,v59,v103\n"
CV "v101,v102,v103\n"
MF "a[8:11],v[16:19],v[80:83],a[8:11]\n"
LD "v8,s[36:39],s73 offen offset:3200 nt lds\n"
MF "a[28:31],v[20:23],v[80:83],a[28:31]\n"
"v_exp_f32 v104,v104\n"
MF "a[32:35],v[20:23],v[84:87],a[32:35]\n"
"s_add_u32 m0,s65,50944\n"
"v_exp_f32 v105,v105\n"
MF "a[12:15],v[16:19],v[84:87],a[12:15]\n"
LD "v8,s[36:39],s73 offen offset:3328 nt lds\n"
MF "a[16:19],v[16:19],v[88:91],a[16:19]\n"
"v_exp_f32 v106,v106\n"
MF "a[36:39],v[20:23],v[88:91],a[36:39]\n"
"s_waitcnt lgkmcnt(0)\n"
TR "v[16:17],v2 offset:1024\n"
TR "v[18:19],v2 offset:7168\n"
MF "a[56:59],v[24:27],v[88:91],a[56:59]\n"
"s_nop 0\n"
TR "v[20:21],v3 offset:1024\n"
TR "v[22:23],v3 offset:7168\n"
MF "a[76:79],v[28:31],v[88:91],a[76:79]\n"
"v_exp_f32 v107,v107\n"
MF "a[72:75],v[28:31],v[84:87],a[72:75]\n"
"v_add_f32 v60,v55,v104\n"
"v_add_f32 v60,v60,v105\n"
MF "a[52:55],v[24:27],v[84:87],a[52:55]\n"
CV "v104,v104,v105\n"
"v_add_f32 v60,v60,v106\n"
MF "a[48:51],v[24:27],v[80:83],a[48:51]\n"
"v_add_f32 v60,v60,v107\n"
CV "v105,v106,v107\n"
MF "a[68:71],v[28:31],v[80:83],a[68:71]\n"
"v_exp_f32 v108,v108\n"
MF "a[64:67],v[28:31],v[76:79],a[64:67]\n"
"v_exp_f32 v109,v109\n"
MF "a[44:47],v[24:27],v[76:79],a[44:47]\n"
"v_exp_f32 v110,v110\n"
MF "a[40:43],v[24:27],v[72:75],a[40:43]\n"
"v_exp_f32 v111,v111\n"
MF "a[60:63],v[28:31],v[72:75],a[60:63]\n"
"s_waitcnt lgkmcnt(0)\n"
TR "v[24:25],v4 offset:1024\n"
TR "v[26:27],v4 offset:7168\n"
MF "a[80:83],v[16:19],v[72:75],a[80:83]\n"
"s_nop 0\n"
TR "v[28:29],v5 offset:1024\n"
TR "v[30:31],v5 offset:7168\n"
MF "a[100:103],v[20:23],v[72:75],a[100:103]\n"
"v_add_f32 v61,v56,v108\n"
"v_add_f32 v61,v61,v109\n"
MF "a[104:107],v[20:23],v[76:79],a[104:107]\n"
CV "v108,v108,v109\n"
"v_add_f32 v61,v61,v110\n"
MF "a[84:87],v[16:19],v[76:79],a[84:87]\n"
"v_add_f32 v61,v61,v111\n"
CV "v109,v110,v111\n"
MF "a[88:91],v[16:19],v[80:83],a[88:91]\n"
"v_exp_f32 v112,v112\n"
MF "a[108:111],v[20:23],v[80:83],a[108:111]\n"
"v_exp_f32 v113,v113\n"
MF "a[112:115],v[20:23],v[84:87],a[112:115]\n"
"v_exp_f32 v114,v114\n"
MF "a[92:95],v[16:19],v[84:87],a[92:95]\n"
"v_exp_f32 v115,v115\n"
MF "a[96:99],v[16:19],v[88:91],a[96:99]\n"
"v_add_f32 v57,v57,v112\n"
"v_add_f32 v57,v57,v113\n"
MF "a[116:119],v[20:23],v[88:91],a[116:119]\n"
"s_waitcnt lgkmcnt(0)\n"
TR "v[16:17],v2 offset:2048\n"
TR "v[18:19],v2 offset:8192\n"
MF "a[136:139],v[24:27],v[88:91],a[136:139]\n"
"s_nop 0\n"
TR "v[20:21],v3 offset:2048\n"
TR "v[22:23],v3 offset:8192\n"
MF "a[156:159],v[28:31],v[88:91],a[156:159]\n"
CV "v94,v112,v113\n"
"v_add_f32 v57,v57,v114\n"
MF "a[152:155],v[28:31],v[84:87],a[152:155]\n"
"v_add_f32 v57,v57,v115\n"
CV "v95,v114,v115\n"
MF "a[132:135],v[24:27],v[84:87],a[132:135]\n"
"v_exp_f32 v116,v116\n"
MF "a[128:131],v[24:27],v[80:83],a[128:131]\n"
"v_exp_f32 v117,v117\n"
MF "a[148:151],v[28:31],v[80:83],a[148:151]\n"
"v_exp_f32 v118,v118\n"
MF "a[144:147],v[28:31],v[76:79],a[144:147]\n"
"v_exp_f32 v119,v119\n"
MF "a[124:127],v[24:27],v[76:79],a[124:127]\n"
"v_add_f32 v58,v58,v116\n"
"v_add_f32 v58,v58,v117\n"
MF "a[120:123],v[24:27],v[72:75],a[120:123]\n"
CV "v98,v116,v117\n"
"v_add_f32 v58,v58,v118\n"
MF "a[140:143],v[28:31],v[72:75],a[140:143]\n"
"s_waitcnt lgkmcnt(0)\n"
TR "v[24:25],v4 offset:2048\n"
TR "v[26:27],v4 offset:8192\n"
MF "a[160:163],v[16:19],v[72:75],a[160:163]\n"
"s_nop 0\n"
TR "v[28:29],v5 offset:2048\n"
TR "v[30:31],v5 offset:8192\n"
MF "a[180:183],v[20:23],v[72:75],a[180:183]\n"
"v_add_f32 v58,v58,v119\n"
CV "v99,v118,v119\n"
MF "a[184:187],v[20:23],v[76:79],a[184:187]\n"
"v_exp_f32 v120,v120\n"
MF "a[164:167],v[16:19],v[76:79],a[164:167]\n"
"v_exp_f32 v121,v121\n"
MF "a[168:171],v[16:19],v[80:83],a[168:171]\n"
"v_exp_f32 v122,v122\n"
MF "a[188:191],v[20:23],v[80:83],a[188:191]\n"
"v_exp_f32 v123,v123\n"
MF "a[192:195],v[20:23],v[84:87],a[192:195]\n"
"v_add_f32 v59,v59,v120\n"
"v_add_f32 v59,v59,v121\n"
MF "a[172:175],v[16:19],v[84:87],a[172:175]\n"
"s_waitcnt vmcnt(6) lgkmcnt(0)\n"
CV "v102,v120,v121\n"
"v_add_f32 v59,v59,v122\n"
MF "a[176:179],v[16:19],v[88:91],a[176:179]\n"
"s_barrier\n"
MF "a[196:199],v[20:23],v[88:91],a[196:199]\n"
"s_add_u32 s72,s72,6144\n"
"ds_read_b128 v[16:19],v0 offset:24576\n"
"ds_read_b128 v[20:23],v1 offset:24576\n"
MF "a[216:219],v[24:27],v[88:91],a[216:219]\n"
"s_cmp_eq_u32 s72,s17\n"
"s_cselect_b32 s72,s16,s72\n"
"s_add_u32 m0,s64,0\n"
MF "a[236:239],v[28:31],v[88:91],a[236:239]\n"
LD "v7,s[32:35],s72 offen nt lds\n"
MF "a[232:235],v[28:31],v[84:87],a[232:235]\n"
"s_add_u32 m0,s64,896\n"
"v_add_f32 v59,v59,v123\n"
CV "v103,v122,v123\n"
MF "a[212:215],v[24:27],v[84:87],a[212:215]\n"
LD "v7,s[32:35],s72 offen offset:128 nt lds\n"
MF "a[208:211],v[24:27],v[80:83],a[208:211]\n"
"s_add_u32 m0,s64,1792\n"
MF "a[228:231],v[28:31],v[80:83],a[228:231]\n"
LD "v7,s[32:35],s72 offen offset:256 nt lds\n"
MF "a[224:227],v[28:31],v[76:79],a[224:227]\n"
MF "a[204:207],v[24:27],v[76:79],a[204:207]\n"
MF "a[200:203],v[24:27],v[72:75],a[200:203]\n"
MF "a[220:223],v[28:31],v[72:75],a[220:223]\n"
"s_waitcnt lgkmcnt(0)\n"
MF "v[72:75],v[16:19],v[136:139],v[32:35]\n"
"s_add_u32 s72,s72,6144\n"
"ds_read_b128 v[24:27],v0 offset:25600\n"
"ds_read_b128 v[28:31],v1 offset:25600\n"
MF "v[72:75],v[20:23],v[156:159],v[72:75]\n"
"s_add_u32 m0,s64,6144\n"
"v_exp_f32 v124,v124\n"
MF "v[76:79],v[20:23],v[160:163],v[36:39]\n"
LD "v7,s[32:35],s72 offen nt lds\n"
MF "v[76:79],v[16:19],v[140:143],v[76:79]\n"
"v_exp_f32 v125,v125\n"
MF "v[80:83],v[16:19],v[144:147],v[40:43]\n"
"s_add_u32 m0,s64,7040\n"
"v_exp_f32 v126,v126\n"
MF "v[80:83],v[20:23],v[164:167],v[80:83]\n"
LD "v7,s[32:35],s72 offen offset:128 nt lds\n"
MF "v[84:87],v[20:23],v[168:171],v[44:47]\n"
"v_exp_f32 v127,v127\n"
MF "v[84:87],v[16:19],v[148:151],v[84:87]\n"
"s_add_u32 m0,s64,7936\n"
"v_add_f32 v60,v60,v124\n"
"v_add_f32 v60,v60,v125\n"
MF "v[88:91],v[16:19],v[152:155],v[48:51]\n"
LD "v7,s[32:35],s72 offen offset:256 nt lds\n"
MF "v[88:91],v[20:23],v[172:175],v[88:91]\n"
"s_waitcnt lgkmcnt(0)\n"
"ds_read_b128 v[16:19],v0 offset:26624\n"
"ds_read_b128 v[20:23],v1 offset:26624\n"
MF "v[88:91],v[24:27],v[192:195],v[88:91]\n"
CV "v106,v124,v125\n"
"v_add_f32 v60,v60,v126\n"
MF "v[88:91],v[28:31],v[212:215],v[88:91]\n"
"v_add_f32 v60,v60,v127\n"
CV "v107,v126,v127\n"
MF "v[84:87],v[28:31],v[208:211],v[84:87]\n"
"v_exp_f32 v128,v128\n"
MF "v[84:87],v[24:27],v[188:191],v[84:87]\n"
"v_exp_f32 v129,v129\n"
MF "v[80:83],v[24:27],v[184:187],v[80:83]\n"
"v_exp_f32 v130,v130\n"
MF "v[80:83],v[28:31],v[204:207],v[80:83]\n"
"v_exp_f32 v131,v131\n"
MF "v[76:79],v[28:31],v[200:203],v[76:79]\n"
"v_add_f32 v61,v61,v128\n"
"v_add_f32 v61,v61,v129\n"
MF "v[76:79],v[24:27],v[180:183],v[76:79]\n"
CV "v110,v128,v129\n"
"v_add_f32 v61,v61,v130\n"
MF "v[72:75],v[24:27],v[176:179],v[72:75]\n"
"v_add_f32 v61,v61,v131\n"
CV "v111,v130,v131\n"
MF "v[72:75],v[28:31],v[196:199],v[72:75]\n"
"s_waitcnt lgkmcnt(0)\n"
"ds_read_b128 v[24:27],v0 offset:30720\n"
"ds_read_b128 v[28:31],v1 offset:30720\n"
MF "v[72:75],v[16:19],v[216:219],v[72:75]\n"
MF "v[72:75],v[20:23],v[236:239],v[72:75]\n"
MF "v[76:79],v[20:23],v[240:243],v[76:79]\n"
MF "v[76:79],v[16:19],v[220:223],v[76:79]\n"
MF "v[80:83],v[16:19],v[224:227],v[80:83]\n"
MF "v[80:83],v[20:23],v[244:247],v[80:83]\n"
MF "v[84:87],v[20:23],v[248:251],v[84:87]\n"
MF "v[84:87],v[16:19],v[228:231],v[84:87]\n"
"v_max3_f32 v10,v57,v58,v59\n"
"v_max3_f32 v10,v10,v60,v61\n"
MF "v[88:91],v[16:19],v[232:235],v[88:91]\n"
"v_cmp_ngt_f32 vcc,s77,v10\n"
MF "v[88:91],v[20:23],v[252:255],v[88:91]\n"
"s_waitcnt lgkmcnt(0)\n"
"s_cbranch_vccnz .Lstub_22\n"
".Lback_22:\n"
MF "v[112:115],v[24:27],v[136:139],v[32:35]\n"
"s_add_u32 s74,s74,12288\n"
"ds_read_b128 v[16:19],v0 offset:31744\n"
"ds_read_b128 v[20:23],v1 offset:31744\n"
MF "v[112:115],v[28:31],v[156:159],v[112:115]\n"
"s_cmp_eq_u32 s74,s20\n"
"s_cselect_b32 s74,s70,s74\n"
MF "v[116:119],v[28:31],v[160:163],v[36:39]\n"
"v_exp_f32 v72,v72\n"
MF "v[116:119],v[24:27],v[140:143],v[116:119]\n"
"v_exp_f32 v73,v73\n"
MF "v[120:123],v[24:27],v[144:147],v[40:43]\n"
"v_exp_f32 v74,v74\n"
MF "v[120:123],v[28:31],v[164:167],v[120:123]\n"
"v_exp_f32 v75,v75\n"
MF "v[124:127],v[28:31],v[168:171],v[44:47]\n"
"v_add_f32 v52,v57,v72\n"
"v_add_f32 v52,v52,v73\n"
MF "v[124:127],v[24:27],v[148:151],v[124:127]\n"
CV "v72,v72,v73\n"
"v_add_f32 v52,v52,v74\n"
MF "v[128:131],v[24:27],v[152:155],v[48:51]\n"
"v_add_f32 v52,v52,v75\n"
CV "v73,v74,v75\n"
MF "v[128:131],v[28:31],v[172:175],v[128:131]\n"
"s_waitcnt lgkmcnt(0)\n"
"ds_read_b128 v[24:27],v0 offset:32768\n"
"ds_read_b128 v[28:31],v1 offset:32768\n"
MF "v[128:131],v[16:19],v[192:195],v[128:131]\n"
"v_exp_f32 v76,v76\n"
MF "v[128:131],v[20:23],v[212:215],v[128:131]\n"
"v_exp_f32 v77,v77\n"
MF "v[124:127],v[20:23],v[208:211],v[124:127]\n"
"v_exp_f32 v78,v78\n"
MF "v[124:127],v[16:19],v[188:191],v[124:127]\n"
"v_exp_f32 v79,v79\n"
MF "v[120:123],v[16:19],v[184:187],v[120:123]\n"
"v_add_f32 v53,v58,v76\n"
"v_add_f32 v53,v53,v77\n"
MF "v[120:123],v[20:23],v[204:207],v[120:123]\n"
CV "v76,v76,v77\n"
"v_add_f32 v53,v53,v78\n"
MF "v[116:119],v[20:23],v[200:203],v[116:119]\n"
"s_waitcnt vmcnt(6) lgkmcnt(0)\n"
"v_add_f32 v53,v53,v79\n"
CV "v77,v78,v79\n"
MF "v[116:119],v[16:19],v[180:183],v[116:119]\n"
"s_barrier\n"
MF "v[112:115],v[16:19],v[176:179],v[112:115]\n"
"v_exp_f32 v80,v80\n"
MF "v[112:115],v[20:23],v[196:199],v[112:115]\n"
"s_nop 0\n"
TR "v[16:17],v2 offset:49152\n"
TR "v[18:19],v2 offset:55296\n"
MF "v[112:115],v[24:27],v[216:219],v[112:115]\n"
"s_add_u32 s73,s73,12288\n"
TR "v[20:21],v3 offset:49152\n"
TR "v[22:23],v3 offset:55296\n"
MF "v[112:115],v[28:31],v[236:239],v[112:115]\n"
"s_cmp_eq_u32 s73,s19\n"
"s_cselect_b32 s73,s18,s73\n"
"s_add_u32 m0,s65,24576\n"
MF "v[116:119],v[28:31],v[240:243],v[116:119]\n"
LD "v8,s[36:39],s73 offen nt lds\n"
MF "v[116:119],v[24:27],v[220:223],v[116:119]\n"
"s_add_u32 m0,s65,25472\n"
"v_exp_f32 v81,v81\n"
MF "v[120:123],v[24:27],v[224:227],v[120:123]\n"
LD "v8,s[36:39],s73 offen offset:128 nt lds\n"
MF "v[120:123],v[28:31],v[244:247],v[120:123]\n"
"v_exp_f32 v82,v82\n"
MF "v[124:127],v[28:31],v[248:251],v[124:127]\n"
"s_add_u32 m0,s65,26368\n"
"v_exp_f32 v83,v83\n"
MF "v[124:127],v[24:27],v[228:231],v[124:127]\n"
LD "v8,s[36:39],s73 offen offset:256 nt lds\n"
MF "v[128:131],v[24:27],v[232:235],v[128:131]\n"
"v_add_f32 v54,v59,v80\n"
"v_add_f32 v54,v54,v81\n"
MF "v[128:131],v[28:31],v[252:255],v[128:131]\n"
"s_waitcnt lgkmcnt(0)\n"
TR "v[24:25],v4 offset:49152\n"
TR "v[26:27],v4 offset:55296\n"
MF "a[0:3],v[16:19],v[92:95],a[0:3]\n"
"s_add_u32 m0,s65,24576\n"
TR "v[28:29],v5 offset:49152\n"
TR "v[30:31],v5 offset:55296\n"
MF "a[20:23],v[20:23],v[92:95],a[20:23]\n"
LD "v8,s[36:39],s73 offen offset:3072 nt lds\n"
MF "a[24:27],v[20:23],v[96:99],a[24:27]\n"
CV "v80,v80,v81\n"
"v_add_f32 v54,v54,v82\n"
MF "a[4:7],v[16:19],v[96:99],a[4:7]\n"
"s_add_u32 m0,s65,25472\n"
"v_add_f32 v54,v54,v83\n"
CV "v81,v82,v83\n"
MF "a[8:11],v[16:19],v[100:103],a[8:11]\n"
LD "v8,s[36:39],s73 offen offset:3200 nt lds\n"
MF "a[28:31],v[20:23],v[100:103],a[28:31]\n"
"v_exp_f32 v84,v84\n"
MF "a[32:35],v[20:23],v[104:107],a[32:35]\n"
"s_add_u32 m0,s65,26368\n"
"v_exp_f32 v85,v85\n"
MF "a[12:15],v[16:19],v[104:107],a[12:15]\n"
LD "v8,s[36:39],s73 offen offset:3328 nt lds\n"
MF "a[16:19],v[16:19],v[108:111],a[16:19]\n"
"v_exp_f32 v86,v86\n"
MF "a[36:39],v[20:23],v[108:111],a[36:39]\n"
"s_waitcnt lgkmcnt(0)\n"
TR "v[16:17],v2 offset:50176\n"
TR "v[18:19],v2 offset:56320\n"
MF "a[56:59],v[24:27],v[108:111],a[56:59]\n"
"s_nop 0\n"
TR "v[20:21],v3 offset:50176\n"
TR "v[22:23],v3 offset:56320\n"
MF "a[76:79],v[28:31],v[108:111],a[76:79]\n"
"v_exp_f32 v87,v87\n"
MF "a[72:75],v[28:31],v[104:107],a[72:75]\n"
"v_add_f32 v55,v60,v84\n"
"v_add_f32 v55,v55,v85\n"
MF "a[52:55],v[24:27],v[104:107],a[52:55]\n"
CV "v84,v84,v85\n"
"v_add_f32 v55,v55,v86\n"
MF "a[48:51],v[24:27],v[100:103],a[48:51]\n"
"v_add_f32 v55,v55,v87\n"
CV "v85,v86,v87\n"
MF "a[68:71],v[28:31],v[100:103],a[68:71]\n"
"v_exp_f32 v88,v88\n"
MF "a[64:67],v[28:31],v[96:99],a[64:67]\n"
"v_exp_f32 v89,v89\n"
MF "a[44:47],v[24:27],v[96:99],a[44:47]\n"
"v_exp_f32 v90,v90\n"
MF "a[40:43],v[24:27],v[92:95],a[40:43]\n"
"v_exp_f32 v91,v91\n"
MF "a[60:63],v[28:31],v[92:95],a[60:63]\n"
"s_waitcnt lgkmcnt(0)\n"
TR "v[24:25],v4 offset:50176\n"
TR "v[26:27],v4 offset:56320\n"
MF "a[80:83],v[16:19],v[92:95],a[80:83]\n"
"s_nop 0\n"
TR "v[28:29],v5 offset:50176\n"
TR "v[30:31],v5 offset:56320\n"
MF "a[100:103],v[20:23],v[92:95],a[100:103]\n"
"v_add_f32 v56,v61,v88\n"
"v_add_f32 v56,v56,v89\n"
MF "a[104:107],v[20:23],v[96:99],a[104:107]\n"
CV "v88,v88,v89\n"
"v_add_f32 v56,v56,v90\n"
MF "a[84:87],v[16:19],v[96:99],a[84:87]\n"
"v_add_f32 v56,v56,v91\n"
CV "v89,v90,v91\n"
MF "a[88:91],v[16:19],v[100:103],a[88:91]\n"
"v_exp_f32 v112,v112\n"
MF "a[108:111],v[20:23],v[100:103],a[108:111]\n"
"v_exp_f32 v113,v113\n"
MF "a[112:115],v[20:23],v[104:107],a[112:115]\n"
"v_exp_f32 v114,v114\n"
MF "a[92:95],v[16:19],v[104:107],a[92:95]\n"
"v_exp_f32 v115,v115\n"
MF "a[96:99],v[16:19],v[108:111],a[96:99]\n"
"v_add_f32 v52,v52,v112\n"
"v_add_f32 v52,v52,v113\n"
MF "a[116:119],v[20:23],v[108:111],a[116:119]\n"
"s_waitcnt lgkmcnt(0)\n"
TR "v[16:17],v2 offset:51200\n"
TR "v[18:19],v2 offset:57344\n"
MF "a[136:139],v[24:27],v[108:111],a[136:139]\n"
"s_nop 0\n"
TR "v[20:21],v3 offset:51200\n"
TR "v[22:23],v3 offset:57344\n"
MF "a[156:159],v[28:31],v[108:111],a[156:159]\n"
CV "v74,v112,v113\n"
"v_add_f32 v52,v52,v114\n"
MF "a[152:155],v[28:31],v[104:107],a[152:155]\n"
"v_add_f32 v52,v52,v115\n"
CV "v75,v114,v115\n"
MF "a[132:135],v[24:27],v[104:107],a[132:135]\n"
"v_exp_f32 v116,v116\n"
MF "a[128:131],v[24:27],v[100:103],a[128:131]\n"
"v_exp_f32 v117,v117\n"
MF "a[148:151],v[28:31],v[100:103],a[148:151]\n"
"v_exp_f32 v118,v118\n"
MF "a[144:147],v[28:31],v[96:99],a[144:147]\n"
"v_exp_f32 v119,v119\n"
MF "a[124:127],v[24:27],v[96:99],a[124:127]\n"
"v_add_f32 v53,v53,v116\n"
"v_add_f32 v53,v53,v117\n"
MF "a[120:123],v[24:27],v[92:95],a[120:123]\n"
CV "v78,v116,v117\n"
"v_add_f32 v53,v53,v118\n"
MF "a[140:143],v[28:31],v[92:95],a[140:143]\n"
"s_waitcnt lgkmcnt(0)\n"
TR "v[24:25],v4 offset:51200\n"
TR "v[26:27],v4 offset:57344\n"
MF "a[160:163],v[16:19],v[92:95],a[160:163]\n"
"s_nop 0\n"
TR "v[28:29],v5 offset:51200\n"
TR "v[30:31],v5 offset:57344\n"
MF "a[180:183],v[20:23],v[92:95],a[180:183]\n"
"v_add_f32 v53,v53,v119\n"
CV "v79,v118,v119\n"
MF "a[184:187],v[20:23],v[96:99],a[184:187]\n"
"v_exp_f32 v120,v120\n"
MF "a[164:167],v[16:19],v[96:99],a[164:167]\n"
"v_exp_f32 v121,v121\n"
MF "a[168:171],v[16:19],v[100:103],a[168:171]\n"
"v_exp_f32 v122,v122\n"
MF "a[188:191],v[20:23],v[100:103],a[188:191]\n"
"v_exp_f32 v123,v123\n"
MF "a[192:195],v[20:23],v[104:107],a[192:195]\n"
"v_add_f32 v54,v54,v120\n"
"v_add_f32 v54,v54,v121\n"
MF "a[172:175],v[16:19],v[104:107],a[172:175]\n"
"s_waitcnt vmcnt(6) lgkmcnt(0)\n"
CV "v82,v120,v121\n"
"v_add_f32 v54,v54,v122\n"
MF "a[176:179],v[16:19],v[108:111],a[176:179]\n"
"s_barrier\n"
MF "a[196:199],v[20:23],v[108:111],a[196:199]\n"
"s_add_u32 s72,s72,6144\n"
"ds_read_b128 v[16:19],v0\n"
"ds_read_b128 v[20:23],v1\n"
MF "a[216:219],v[24:27],v[108:111],a[216:219]\n"
"s_cmp_eq_u32 s72,s17\n"
"s_cselect_b32 s72,s16,s72\n"
"s_add_u32 m0,s64,49152\n"
MF "a[236:239],v[28:31],v[108:111],a[236:239]\n"
LD "v7,s[32:35],s72 offen nt lds\n"
MF "a[232:235],v[28:31],v[104:107],a[232:235]\n"
"s_add_u32 m0,s64,50048\n"
"v_add_f32 v54,v54,v123\n"
CV "v83,v122,v123\n"
MF "a[212:215],v[24:27],v[104:107],a[212:215]\n"
LD "v7,s[32:35],s72 offen offset:128 nt lds\n"
MF "a[208:211],v[24:27],v[100:103],a[208:211]\n"
"s_add_u32 m0,s64,50944\n"
MF "a[228:231],v[28:31],v[100:103],a[228:231]\n"
LD "v7,s[32:35],s72 offen offset:256 nt lds\n"
MF "a[224:227],v[28:31],v[96:99],a[224:227]\n"
MF "a[204:207],v[24:27],v[96:99],a[204:207]\n"
MF "a[200:203],v[24:27],v[92:95],a[200:203]\n"
MF "a[220:223],v[28:31],v[92:95],a[220:223]\n"
"s_waitcnt lgkmcnt(0)\n"
MF "v[92:95],v[16:19],v[136:139],v[32:35]\n"
"s_add_u32 s72,s72,6144\n"
"ds_read_b128 v[24:27],v0 offset:1024\n"
"ds_read_b128 v[28:31],v1 offset:1024\n"
MF "v[92:95],v[20:23],v[156:159],v[92:95]\n"
"s_add_u32 m0,s64,55296\n"
"v_exp_f32 v124,v124\n"
MF "v[96:99],v[20:23],v[160:163],v[36:39]\n"
LD "v7,s[32:35],s72 offen nt lds\n"
MF "v[96:99],v[16:19],v[140:143],v[96:99]\n"
"v_exp_f32 v125,v125\n"
MF "v[100:103],v[16:19],v[144:147],v[40:43]\n"
"s_add_u32 m0,s64,56192\n"
"v_exp_f32 v126,v126\n"
MF "v[100:103],v[20:23],v[164:167],v[100:103]\n"
LD "v7,s[32:35],s72 offen offset:128 nt lds\n"
MF "v[104:107],v[20:23],v[168:171],v[44:47]\n"
"v_exp_f32 v127,v127\n"
MF "v[104:107],v[16:19],v[148:151],v[104:107]\n"
"s_add_u32 m0,s64,57088\n"
"v_add_f32 v55,v55,v124\n"
"v_add_f32 v55,v55,v125\n"
MF "v[108:111],v[16:19],v[152:155],v[48:51]\n"
LD "v7,s[32:35],s72 offen offset:256 nt lds\n"
MF "v[108:111],v[20:23],v[172:175],v[108:111]\n"
"s_waitcnt lgkmcnt(0)\n"
"ds_read_b128 v[16:19],v0 offset:2048\n"
"ds_read_b128 v[20:23],v1 offset:2048\n"
MF "v[108:111],v[24:27],v[192:195],v[108:111]\n"
CV "v86,v124,v125\n"
"v_add_f32 v55,v55,v126\n"
MF "v[108:111],v[28:31],v[212:215],v[108:111]\n"
"v_add_f32 v55,v55,v127\n"
CV "v87,v126,v127\n"
MF "v[104:107],v[28:31],v[208:211],v[104:107]\n"
"v_exp_f32 v128,v128\n"
MF "v[104:107],v[24:27],v[188:191],v[104:107]\n"
"v_exp_f32 v129,v129\n"
MF "v[100:103],v[24:27],v[184:187],v[100:103]\n"
"v_exp_f32 v130,v130\n"
MF "v[100:103],v[28:31],v[204:207],v[100:103]\n"
"v_exp_f32 v131,v131\n"
MF "v[96:99],v[28:31],v[200:203],v[96:99]\n"
"v_add_f32 v56,v56,v128\n"
"v_add_f32 v56,v56,v129\n"
MF "v[96:99],v[24:27],v[180:183],v[96:99]\n"
CV "v90,v128,v129\n"
"v_add_f32 v56,v56,v130\n"
MF "v[92:95],v[24:27],v[176:179],v[92:95]\n"
"v_add_f32 v56,v56,v131\n"
CV "v91,v130,v131\n"
MF "v[92:95],v[28:31],v[196:199],v[92:95]\n"
"s_waitcnt lgkmcnt(0)\n"
"ds_read_b128 v[24:27],v0 offset:6144\n"
"ds_read_b128 v[28:31],v1 offset:6144\n"
MF "v[92:95],v[16:19],v[216:219],v[92:95]\n"
MF "v[92:95],v[20:23],v[236:239],v[92:95]\n"
MF "v[96:99],v[20:23],v[240:243],v[96:99]\n"
MF "v[96:99],v[16:19],v[220:223],v[96:99]\n"
MF "v[100:103],v[16:19],v[224:227],v[100:103]\n"
MF "v[100:103],v[20:23],v[244:247],v[100:103]\n"
MF "v[104:107],v[20:23],v[248:251],v[104:107]\n"
MF "v[104:107],v[16:19],v[228:231],v[104:107]\n"
"v_max3_f32 v10,v52,v53,v54\n"
"v_max3_f32 v10,v10,v55,v56\n"
MF "v[108:111],v[16:19],v[232:235],v[108:111]\n"
"v_cmp_ngt_f32 vcc,s77,v10\n"
MF "v[108:111],v[20:23],v[252:255],v[108:111]\n"
"s_waitcnt lgkmcnt(0)\n"
"s_cbranch_vccnz .Lstub_23\n"
".Lback_23:\n"
MF "v[112:115],v[24:27],v[136:139],v[32:35]\n"
"s_add_u32 s74,s74,12288\n"
"ds_read_b128 v[16:19],v0 offset:7168\n"
"ds_read_b128 v[20:23],v1 offset:7168\n"
MF "v[112:115],v[28:31],v[156:159],v[112:115]\n"
"s_cmp_eq_u32 s74,s20\n"
"s_cselect_b32 s74,s70,s74\n"
MF "v[116:119],v[28:31],v[160:163],v[36:39]\n"
"v_exp_f32 v92,v92\n"
MF "v[116:119],v[24:27],v[140:143],v[116:119]\n"
"v_exp_f32 v93,v93\n"
MF "v[120:123],v[24:27],v[144:147],v[40:43]\n"
"v_exp_f32 v94,v94\n"
MF "v[120:123],v[28:31],v[164:167],v[120:123]\n"
"v_exp_f32 v95,v95\n"
MF "v[124:127],v[28:31],v[168:171],v[44:47]\n"
"v_add_f32 v57,v52,v92\n"
"v_add_f32 v57,v57,v93\n"
MF "v[124:127],v[24:27],v[148:151],v[124:127]\n"
CV "v92,v92,v93\n"
"v_add_f32 v57,v57,v94\n"
MF "v[128:131],v[24:27],v[152:155],v[48:51]\n"
"v_add_f32 v57,v57,v95\n"
CV "v93,v94,v95\n"
MF "v[128:131],v[28:31],v[172:175],v[128:131]\n"
"s_waitcnt lgkmcnt(0)\n"
"ds_read_b128 v[24:27],v0 offset:8192\n"
"ds_read_b128 v[28:31],v1 offset:8192\n"
MF "v[128:131],v[16:19],v[192:195],v[128:131]\n"
"v_exp_f32 v96,v96\n"
MF "v[128:131],v[20:23],v[212:215],v[128:131]\n"
"v_exp_f32 v97,v97\n"
MF "v[124:127],v[20:23],v[208:211],v[124:127]\n"
"v_exp_f32 v98,v98\n"
MF "v[124:127],v[16:19],v[188:191],v[124:127]\n"
"v_exp_f32 v99,v99\n"
MF "v[120:123],v[16:19],v[184:187],v[120:123]\n"
"v_add_f32 v58,v53,v96\n"
"v_add_f32 v58,v58,v97\n"
MF "v[120:123],v[20:23],v[204:207],v[120:123]\n"
CV "v96,v96,v97\n"
"v_add_f32 v58,v58,v98\n"
MF "v[116:119],v[20:23],v[200:203],v[116:119]\n"
"s_waitcnt vmcnt(6) lgkmcnt(0)\n"
"v_add_f32 v58,v58,v99\n"
CV "v97,v98,v99\n"
MF "v[116:119],v[16:19],v[180:183],v[116:119]\n"
"s_barrier\n"
MF "v[112:115],v[16:19],v[176:179],v[112:115]\n"
"v_exp_f32 v100,v100\n"
MF "v[112:115],v[20:23],v[196:199],v[112:115]\n"
"s_nop 0\n"
TR "v[16:17],v2 offset:24576\n"
TR "v[18:19],v2 offset:30720\n"
MF "v[112:115],v[24:27],v[216:219],v[112:115]\n"
"s_add_u32 s73,s73,12288\n"
TR "v[20:21],v3 offset:24576\n"
TR "v[22:23],v3 offset:30720\n"
MF "v[112:115],v[28:31],v[236:239],v[112:115]\n"
"s_cmp_eq_u32 s73,s19\n"
"s_cselect_b32 s73,s18,s73\n"
"s_add_u32 m0,s65,0\n"
MF "v[116:119],v[28:31],v[240:243],v[116:119]\n"
LD "v8,s[36:39],s73 offen nt lds\n"
MF "v[116:119],v[24:27],v[220:223],v[116:119]\n"
"s_add_u32 m0,s65,896\n"
"v_exp_f32 v101,v101\n"
MF "v[120:123],v[24:27],v[224:227],v[120:123]\n"
LD "v8,s[36:39],s73 offen offset:128 nt lds\n"
MF "v[120:123],v[28:31],v[244:247],v[120:123]\n"
"v_exp_f32 v102,v102\n"
MF "v[124:127],v[28:31],v[248:251],v[124:127]\n"
"s_add_u32 m0,s65,1792\n"
"v_exp_f32 v103,v103\n"
MF "v[124:127],v[24:27],v[228:231],v[124:127]\n"
LD "v8,s[36:39],s73 offen offset:256 nt lds\n"
MF "v[128:131],v[24:27],v[232:235],v[128:131]\n"
"v_add_f32 v59,v54,v100\n"
"v_add_f32 v59,v59,v101\n"
MF "v[128:131],v[28:31],v[252:255],v[128:131]\n"
"s_waitcnt lgkmcnt(0)\n"
TR "v[24:25],v4 offset:24576\n"
TR "v[26:27],v4 offset:30720\n"
MF "a[0:3],v[16:19],v[72:75],a[0:3]\n"
"s_add_u32 m0,s65,0\n"
TR "v[28:29],v5 offset:24576\n"
TR "v[30:31],v5 offset:30720\n"
MF "a[20:23],v[20:23],v[72:75],a[20:23]\n"
LD "v8,s[36:39],s73 offen offset:3072 nt lds\n"
MF "a[24:27],v[20:23],v[76:79],a[24:27]\n"
CV "v100,v100,v101\n"
"v_add_f32 v59,v59,v102\n"
MF "a[4:7],v[16:19],v[76:79],a[4:7]\n"
"s_add_u32 m0,s65,896\n"
"v_add_f32 v59,v59,v103\n"
CV "v101,v102,v103\n"
MF "a[8:11],v[16:19],v[80:83],a[8:11]\n"
LD "v8,s[36:39],s73 offen offset:3200 nt lds\n"
MF "a[28:31],v[20:23],v[80:83],a[28:31]\n"
"v_exp_f32 v104,v104\n"
MF "a[32:35],v[20:23],v[84:87],a[32:35]\n"
"s_add_u32 m0,s65,1792\n"
"v_exp_f32 v105,v105\n"
MF "a[12:15],v[16:19],v[84:87],a[12:15]\n"
LD "v8,s[36:39],s73 offen offset:3328 nt lds\n"
MF "a[16:19],v[16:19],v[88:91],a[16:19]\n"
"v_exp_f32 v106,v106\n"
MF "a[36:39],v[20:23],v[88:91],a[36:39]\n"
"s_waitcnt lgkmcnt(0)\n"
TR "v[16:17],v2 offset:25600\n"
TR "v[18:19],v2 offset:31744\n"
MF "a[56:59],v[24:27],v[88:91],a[56:59]\n"
"s_nop 0\n"
TR "v[20:21],v3 offset:25600\n"
TR "v[22:23],v3 offset:31744\n"
MF "a[76:79],v[28:31],v[88:91],a[76:79]\n"
"v_exp_f32 v107,v107\n"
MF "a[72:75],v[28:31],v[84:87],a[72:75]\n"
"v_add_f32 v60,v55,v104\n"
"v_add_f32 v60,v60,v105\n"
MF "a[52:55],v[24:27],v[84:87],a[52:55]\n"
CV "v104,v104,v105\n"
"v_add_f32 v60,v60,v106\n"
MF "a[48:51],v[24:27],v[80:83],a[48:51]\n"
"v_add_f32 v60,v60,v107\n"
CV "v105,v106,v107\n"
MF "a[68:71],v[28:31],v[80:83],a[68:71]\n"
"v_exp_f32 v108,v108\n"
MF "a[64:67],v[28:31],v[76:79],a[64:67]\n"
"v_exp_f32 v109,v109\n"
MF "a[44:47],v[24:27],v[76:79],a[44:47]\n"
"v_exp_f32 v110,v110\n"
MF "a[40:43],v[24:27],v[72:75],a[40:43]\n"
"v_exp_f32 v111,v111\n"
MF "a[60:63],v[28:31],v[72:75],a[60:63]\n"
"s_waitcnt lgkmcnt(0)\n"
TR "v[24:25],v4 offset:25600\n"
TR "v[26:27],v4 offset:31744\n"
MF "a[80:83],v[16:19],v[72:75],a[80:83]\n"
"s_nop 0\n"
TR "v[28:29],v5 offset:25600\n"
TR "v[30:31],v5 offset:31744\n"
MF "a[100:103],v[20:23],v[72:75],a[100:103]\n"
"v_add_f32 v61,v56,v108\n"
"v_add_f32 v61,v61,v109\n"
MF "a[104:107],v[20:23],v[76:79],a[104:107]\n"
CV "v108,v108,v109\n"
"v_add_f32 v61,v61,v110\n"
MF "a[84:87],v[16:19],v[76:79],a[84:87]\n"
"v_add_f32 v61,v61,v111\n"
CV "v109,v110,v111\n"
MF "a[88:91],v[16:19],v[80:83],a[88:91]\n"
"v_exp_f32 v112,v112\n"
MF "a[108:111],v[20:23],v[80:83],a[108:111]\n"
"v_exp_f32 v113,v113\n"
MF "a[112:115],v[20:23],v[84:87],a[112:115]\n"
"v_exp_f32 v114,v114\n"
MF "a[92:95],v[16:19],v[84:87],a[92:95]\n"
"v_exp_f32 v115,v115\n"
MF "a[96:99],v[16:19],v[88:91],a[96:99]\n"
"v_add_f32 v57,v57,v112\n"
"v_add_f32 v57,v57,v113\n"
MF "a[116:119],v[20:23],v[88:91],a[116:119]\n"
"s_waitcnt lgkmcnt(0)\n"
TR "v[16:17],v2 offset:26624\n"
TR "v[18:19],v2 offset:32768\n"
MF "a[136:139],v[24:27],v[88:91],a[136:139]\n"
"s_nop 0\n"
TR "v[20:21],v3 offset:26624\n"
TR "v[22:23],v3 offset:32768\n"
MF "a[156:159],v[28:31],v[88:91],a[156:159]\n"
CV "v94,v112,v113\n"
"v_add_f32 v57,v57,v114\n"
MF "a[152:155],v[28:31],v[84:87],a[152:155]\n"
"v_add_f32 v57,v57,v115\n"
CV "v95,v114,v115\n"
MF "a[132:135],v[24:27],v[84:87],a[132:135]\n"
"v_exp_f32 v116,v116\n"
MF "a[128:131],v[24:27],v[80:83],a[128:131]\n"
"v_exp_f32 v117,v117\n"
MF "a[148:151],v[28:31],v[80:83],a[148:151]\n"
"v_exp_f32 v118,v118\n"
MF "a[144:147],v[28:31],v[76:79],a[144:147]\n"
"v_exp_f32 v119,v119\n"
MF "a[124:127],v[24:27],v[76:79],a[124:127]\n"
"v_add_f32 v58,v58,v116\n"
"v_add_f32 v58,v58,v117\n"
MF "a[120:123],v[24:27],v[72:75],a[120:123]\n"
CV "v98,v116,v117\n"
"v_add_f32 v58,v58,v118\n"
MF "a[140:143],v[28:31],v[72:75],a[140:143]\n"
"s_waitcnt lgkmcnt(0)\n"
TR "v[24:25],v4 offset:26624\n"
TR "v[26:27],v4 offset:32768\n"
MF "a[160:163],v[16:19],v[72:75],a[160:163]\n"
"s_nop 0\n"
TR "v[28:29],v5 offset:26624\n"
TR "v[30:31],v5 offset:32768\n"
MF "a[180:183],v[20:23],v[72:75],a[180:183]\n"
"v_add_f32 v58,v58,v119\n"
CV "v99,v118,v119\n"
MF "a[184:187],v[20:23],v[76:79],a[184:187]\n"
"v_exp_f32 v120,v120\n"
MF "a[164:167],v[16:19],v[76:79],a[164:167]\n"
"v_exp_f32 v121,v121\n"
MF "a[168:171],v[16:19],v[80:83],a[168:171]\n"
"v_exp_f32 v122,v122\n"
MF "a[188:191],v[20:23],v[80:83],a[188:191]\n"
"v_exp_f32 v123,v123\n"
MF "a[192:195],v[20:23],v[84:87],a[192:195]\n"
"v_add_f32 v59,v59,v120\n"
"v_add_f32 v59,v59,v121\n"
MF "a[172:175],v[16:19],v[84:87],a[172:175]\n"
"s_waitcnt vmcnt(6) lgkmcnt(0)\n"
CV "v102,v120,v121\n"
"v_add_f32 v59,v59,v122\n"
MF "a[176:179],v[16:19],v[88:91],a[176:179]\n"
"s_barrier\n"
MF "a[196:199],v[20:23],v[88:91],a[196:199]\n"
"s_add_u32 s72,s72,6144\n"
"ds_read_b128 v[16:19],v0 offset:49152\n"
"ds_read_b128 v[20:23],v1 offset:49152\n"
MF "a[216:219],v[24:27],v[88:91],a[216:219]\n"
"s_cmp_eq_u32 s72,s17\n"
"s_cselect_b32 s72,s16,s72\n"
"s_add_u32 m0,s64,24576\n"
MF "a[236:239],v[28:31],v[88:91],a[236:239]\n"
LD "v7,s[32:35],s72 offen nt lds\n"
MF "a[232:235],v[28:31],v[84:87],a[232:235]\n"
"s_add_u32 m0,s64,25472\n"
"v_add_f32 v59,v59,v123\n"
CV "v103,v122,v123\n"
MF "a[212:215],v[24:27],v[84:87],a[212:215]\n"
LD "v7,s[32:35],s72 offen offset:128 nt lds\n"
MF "a[208:211],v[24:27],v[80:83],a[208:211]\n"
"s_add_u32 m0,s64,26368\n"
MF "a[228:231],v[28:31],v[80:83],a[228:231]\n"
LD "v7,s[32:35],s72 offen offset:256 nt lds\n"
MF "a[224:227],v[28:31],v[76:79],a[224:227]\n"
MF "a[204:207],v[24:27],v[76:79],a[204:207]\n"
MF "a[200:203],v[24:27],v[72:75],a[200:203]\n"
MF "a[220:223],v[28:31],v[72:75],a[220:223]\n"
"s_waitcnt lgkmcnt(0)\n"
MF "v[72:75],v[16:19],v[136:139],v[32:35]\n"
"s_add_u32 s72,s72,6144\n"
"ds_read_b128 v[24:27],v0 offset:50176\n"
"ds_read_b128 v[28:31],v1 offset:50176\n"
MF "v[72:75],v[20:23],v[156:159],v[72:75]\n"
"s_add_u32 m0,s64,30720\n"
"v_exp_f32 v124,v124\n"
MF "v[76:79],v[20:23],v[160:163],v[36:39]\n"
LD "v7,s[32:35],s72 offen nt lds\n"
MF "v[76:79],v[16:19],v[140:143],v[76:79]\n"
"v_exp_f32 v125,v125\n"
MF "v[80:83],v[16:19],v[144:147],v[40:43]\n"
"s_add_u32 m0,s64,31616\n"
"v_exp_f32 v126,v126\n"
MF "v[80:83],v[20:23],v[164:167],v[80:83]\n"
LD "v7,s[32:35],s72 offen offset:128 nt lds\n"
MF "v[84:87],v[20:23],v[168:171],v[44:47]\n"
"v_exp_f32 v127,v127\n"
MF "v[84:87],v[16:19],v[148:151],v[84:87]\n"
"s_add_u32 m0,s64,32512\n"
"v_add_f32 v60,v60,v124\n"
"v_add_f32 v60,v60,v125\n"
MF "v[88:91],v[16:19],v[152:155],v[48:51]\n"
LD "v7,s[32:35],s72 offen offset:256 nt lds\n"
MF "v[88:91],v[20:23],v[172:175],v[88:91]\n"
"s_waitcnt lgkmcnt(0)\n"
"ds_read_b128 v[16:19],v0 offset:51200\n"
"ds_read_b128 v[20:23],v1 offset:51200\n"
MF "v[88:91],v[24:27],v[192:195],v[88:91]\n"
CV "v106,v124,v125\n"
"v_add_f32 v60,v60,v126\n"
MF "v[88:91],v[28:31],v[212:215],v[88:91]\n"
"v_add_f32 v60,v60,v127\n"
CV "v107,v126,v127\n"
MF "v[84:87],v[28:31],v[208:211],v[84:87]\n"
"v_exp_f32 v128,v128\n"
MF "v[84:87],v[24:27],v[188:191],v[84:87]\n"
"v_exp_f32 v129,v129\n"
MF "v[80:83],v[24:27],v[184:187],v[80:83]\n"
"v_exp_f32 v130,v130\n"
MF "v[80:83],v[28:31],v[204:207],v[80:83]\n"
"v_exp_f32 v131,v131\n"
MF "v[76:79],v[28:31],v[200:203],v[76:79]\n"
"v_add_f32 v61,v61,v128\n"
"v_add_f32 v61,v61,v129\n"
MF "v[76:79],v[24:27],v[180:183],v[76:79]\n"
CV "v110,v128,v129\n"
"v_add_f32 v61,v61,v130\n"
MF "v[72:75],v[24:27],v[176:179],v[72:75]\n"
"v_add_f32 v61,v61,v131\n"
CV "v111,v130,v131\n"
MF "v[72:75],v[28:31],v[196:199],v[72:75]\n"
"s_waitcnt lgkmcnt(0)\n"
"ds_read_b128 v[24:27],v0 offset:55296\n"
"ds_read_b128 v[28:31],v1 offset:55296\n"
MF "v[72:75],v[16:19],v[216:219],v[72:75]\n"
MF "v[72:75],v[20:23],v[236:239],v[72:75]\n"
MF "v[76:79],v[20:23],v[240:243],v[76:79]\n"
MF "v[76:79],v[16:19],v[220:223],v[76:79]\n"
MF "v[80:83],v[16:19],v[224:227],v[80:83]\n"
MF "v[80:83],v[20:23],v[244:247],v[80:83]\n"
MF "v[84:87],v[20:23],v[248:251],v[84:87]\n"
MF "v[84:87],v[16:19],v[228:231],v[84:87]\n"
"v_max3_f32 v10,v57,v58,v59\n"
"v_max3_f32 v10,v10,v60,v61\n"
MF "v[88:91],v[16:19],v[232:235],v[88:91]\n"
"v_cmp_ngt_f32 vcc,s95,v10\n"
MF "v[88:91],v[20:23],v[252:255],v[88:91]\n"
"s_waitcnt lgkmcnt(0)\n"
"s_cbranch_vccnz .Lstub_24\n"
".Lback_24:\n"
MF "v[112:115],v[24:27],v[136:139],v[32:35]\n"
"s_add_u32 s74,s74,12288\n"
"ds_read_b128 v[16:19],v0 offset:56320\n"
"ds_read_b128 v[20:23],v1 offset:56320\n"
MF "v[112:115],v[28:31],v[156:159],v[112:115]\n"
"s_cmp_eq_u32 s74,s20\n"
"s_cselect_b32 s74,s70,s74\n"
MF "v[116:119],v[28:31],v[160:163],v[36:39]\n"
"v_exp_f32 v72,v72\n"
MF "v[116:119],v[24:27],v[140:143],v[116:119]\n"
"v_exp_f32 v73,v73\n"
MF "v[120:123],v[24:27],v[144:147],v[40:43]\n"
"v_exp_f32 v74,v74\n"
MF "v[120:123],v[28:31],v[164:167],v[120:123]\n"
"v_exp_f32 v75,v75\n"
MF "v[124:127],v[28:31],v[168:171],v[44:47]\n"
"v_add_f32 v52,v57,v72\n"
"v_add_f32 v52,v52,v73\n"
MF "v[124:127],v[24:27],v[148:151],v[124:127]\n"
CV "v72,v72,v73\n"
"v_add_f32 v52,v52,v74\n"
MF "v[128:131],v[24:27],v[152:155],v[48:51]\n"
"v_add_f32 v52,v52,v75\n"
CV "v73,v74,v75\n"
MF "v[128:131],v[28:31],v[172:175],v[128:131]\n"
"s_waitcnt lgkmcnt(0)\n"
"ds_read_b128 v[24:27],v0 offset:57344\n"
"ds_read_b128 v[28:31],v1 offset:57344\n"
MF "v[128:131],v[16:19],v[192:195],v[128:131]\n"
"v_exp_f32 v76,v76\n"
MF "v[128:131],v[20:23],v[212:215],v[128:131]\n"
"v_exp_f32 v77,v77\n"
MF "v[124:127],v[20:23],v[208:211],v[124:127]\n"
"v_exp_f32 v78,v78\n"
MF "v[124:127],v[16:19],v[188:191],v[124:127]\n"
"v_exp_f32 v79,v79\n"
MF "v[120:123],v[16:19],v[184:187],v[120:123]\n"
"v_add_f32 v53,v58,v76\n"
"v_add_f32 v53,v53,v77\n"
MF "v[120:123],v[20:23],v[204:207],v[120:123]\n"
CV "v76,v76,v77\n"
"v_add_f32 v53,v53,v78\n"
MF "v[116:119],v[20:23],v[200:203],v[116:119]\n"
"s_waitcnt vmcnt(6) lgkmcnt(0)\n"
"v_add_f32 v53,v53,v79\n"
CV "v77,v78,v79\n"
MF "v[116:119],v[16:19],v[180:183],v[116:119]\n"
"s_barrier\n"
MF "v[112:115],v[16:19],v[176:179],v[112:115]\n"
"v_exp_f32 v80,v80\n"
MF "v[112:115],v[20:23],v[196:199],v[112:115]\n"
"s_nop 0\n"
TR "v[16:17],v2\n"
TR "v[18:19],v2 offset:6144\n"
MF "v[112:115],v[24:27],v[216:219],v[112:115]\n"
"s_add_u32 s73,s73,12288\n"
TR "v[20:21],v3\n"
TR "v[22:23],v3 offset:6144\n"
MF "v[112:115],v[28:31],v[236:239],v[112:115]\n"
"s_cmp_eq_u32 s73,s19\n"
"s_cselect_b32 s73,s18,s73\n"
"s_add_u32 m0,s65,49152\n"
MF "v[116:119],v[28:31],v[240:243],v[116:119]\n"
LD "v8,s[36:39],s73 offen nt lds\n"
MF "v[116:119],v[24:27],v[220:223],v[116:119]\n"
"s_add_u32 m0,s65,50048\n"
"v_exp_f32 v81,v81\n"
MF "v[120:123],v[24:27],v[224:227],v[120:123]\n"
LD "v8,s[36:39],s73 offen offset:128 nt lds\n"
MF "v[120:123],v[28:31],v[244:247],v[120:123]\n"
"v_exp_f32 v82,v82\n"
MF "v[124:127],v[28:31],v[248:251],v[124:127]\n"
"s_add_u32 m0,s65,50944\n"
"v_exp_f32 v83,v83\n"
MF "v[124:127],v[24:27],v[228:231],v[124:127]\n"
LD "v8,s[36:39],s73 offen offset:256 nt lds\n"
MF "v[128:131],v[24:27],v[232:235],v[128:131]\n"
"v_add_f32 v54,v59,v80\n"
"v_add_f32 v54,v54,v81\n"
MF "v[128:131],v[28:31],v[252:255],v[128:131]\n"
"s_waitcnt lgkmcnt(0)\n"
TR "v[24:25],v4\n"
TR "v[26:27],v4 offset:6144\n"
MF "a[0:3],v[16:19],v[92:95],a[0:3]\n"
"s_add_u32 m0,s65,49152\n"
TR "v[28:29],v5\n"
TR "v[30:31],v5 offset:6144\n"
MF "a[20:23],v[20:23],v[92:95],a[20:23]\n"
LD "v8,s[36:39],s73 offen offset:3072 nt lds\n"
MF "a[24:27],v[20:23],v[96:99],a[24:27]\n"
CV "v80,v80,v81\n"
"v_add_f32 v54,v54,v82\n"
MF "a[4:7],v[16:19],v[96:99],a[4:7]\n"
"s_add_u32 m0,s65,50048\n"
"v_add_f32 v54,v54,v83\n"
CV "v81,v82,v83\n"
MF "a[8:11],v[16:19],v[100:103],a[8:11]\n"
LD "v8,s[36:39],s73 offen offset:3200 nt lds\n"
MF "a[28:31],v[20:23],v[100:103],a[28:31]\n"
"v_exp_f32 v84,v84\n"
MF "a[32:35],v[20:23],v[104:107],a[32:35]\n"
"s_add_u32 m0,s65,50944\n"
"v_exp_f32 v85,v85\n"
MF "a[12:15],v[16:19],v[104:107],a[12:15]\n"
LD "v8,s[36:39],s73 offen offset:3328 nt lds\n"
MF "a[16:19],v[16:19],v[108:111],a[16:19]\n"
"v_exp_f32 v86,v86\n"
MF "a[36:39],v[20:23],v[108:111],a[36:39]\n"
"s_waitcnt lgkmcnt(0)\n"
TR "v[16:17],v2 offset:1024\n"
TR "v[18:19],v2 offset:7168\n"
MF "a[56:59],v[24:27],v[108:111],a[56:59]\n"
"s_nop 0\n"
TR "v[20:21],v3 offset:1024\n"
TR "v[22:23],v3 offset:7168\n"
MF "a[76:79],v[28:31],v[108:111],a[76:79]\n"
"v_exp_f32 v87,v87\n"
MF "a[72:75],v[28:31],v[104:107],a[72:75]\n"
"v_add_f32 v55,v60,v84\n"
"v_add_f32 v55,v55,v85\n"
MF "a[52:55],v[24:27],v[104:107],a[52:55]\n"
CV "v84,v84,v85\n"
"v_add_f32 v55,v55,v86\n"
MF "a[48:51],v[24:27],v[100:103],a[48:51]\n"
"v_add_f32 v55,v55,v87\n"
CV "v85,v86,v87\n"
MF "a[68:71],v[28:31],v[100:103],a[68:71]\n"
"v_exp_f32 v88,v88\n"
MF "a[64:67],v[28:31],v[96:99],a[64:67]\n"
"v_exp_f32 v89,v89\n"
MF "a[44:47],v[24:27],v[96:99],a[44:47]\n"
"v_exp_f32 v90,v90\n"
MF "a[40:43],v[24:27],v[92:95],a[40:43]\n"
"v_exp_f32 v91,v91\n"
MF "a[60:63],v[28:31],v[92:95],a[60:63]\n"
"s_waitcnt lgkmcnt(0)\n"
TR "v[24:25],v4 offset:1024\n"
TR "v[26:27],v4 offset:7168\n"
MF "a[80:83],v[16:19],v[92:95],a[80:83]\n"
"s_nop 0\n"
TR "v[28:29],v5 offset:1024\n"
TR "v[30:31],v5 offset:7168\n"
MF "a[100:103],v[20:23],v[92:95],a[100:103]\n"
"v_add_f32 v56,v61,v88\n"
"v_add_f32 v56,v56,v89\n"
MF "a[104:107],v[20:23],v[96:99],a[104:107]\n"
CV "v88,v88,v89\n"
"v_add_f32 v56,v56,v90\n"
MF "a[84:87],v[16:19],v[96:99],a[84:87]\n"
"v_add_f32 v56,v56,v91\n"
CV "v89,v90,v91\n"
MF "a[88:91],v[16:19],v[100:103],a[88:91]\n"
"v_exp_f32 v112,v112\n"
MF "a[108:111],v[20:23],v[100:103],a[108:111]\n"
"v_exp_f32 v113,v113\n"
MF "a[112:115],v[20:23],v[104:107],a[112:115]\n"
"v_exp_f32 v114,v114\n"
MF "a[92:95],v[16:19],v[104:107],a[92:95]\n"
"v_exp_f32 v115,v115\n"
MF "a[96:99],v[16:19],v[108:111],a[96:99]\n"
"v_add_f32 v52,v52,v112\n"
"v_add_f32 v52,v52,v113\n"
MF "a[116:119],v[20:23],v[108:111],a[116:119]\n"
"s_waitcnt lgkmcnt(0)\n"
TR "v[16:17],v2 offset:2048\n"
TR "v[18:19],v2 offset:8192\n"
MF "a[136:139],v[24:27],v[108:111],a[136:139]\n"
"s_nop 0\n"
TR "v[20:21],v3 offset:2048\n"
TR "v[22:23],v3 offset:8192\n"
MF "a[156:159],v[28:31],v[108:111],a[156:159]\n"
CV "v74,v112,v113\n"
"v_add_f32 v52,v52,v114\n"
MF "a[152:155],v[28:31],v[104:107],a[152:155]\n"
"v_add_f32 v52,v52,v115\n"
CV "v75,v114,v115\n"
MF "a[132:135],v[24:27],v[104:107],a[132:135]\n"
"v_exp_f32 v116,v116\n"
MF "a[128:131],v[24:27],v[100:103],a[128:131]\n"
"v_exp_f32 v117,v117\n"
MF "a[148:151],v[28:31],v[100:103],a[148:151]\n"
"v_exp_f32 v118,v118\n"
MF "a[144:147],v[28:31],v[96:99],a[144:147]\n"
"v_exp_f32 v119,v119\n"
MF "a[124:127],v[24:27],v[96:99],a[124:127]\n"
"v_add_f32 v53,v53,v116\n"
"v_add_f32 v53,v53,v117\n"
MF "a[120:123],v[24:27],v[92:95],a[120:123]\n"
CV "v78,v116,v117\n"
"v_add_f32 v53,v53,v118\n"
MF "a[140:143],v[28:31],v[92:95],a[140:143]\n"
"s_waitcnt lgkmcnt(0)\n"
TR "v[24:25],v4 offset:2048\n"
TR "v[26:27],v4 offset:8192\n"
MF "a[160:163],v[16:19],v[92:95],a[160:163]\n"
"s_nop 0\n"
TR "v[28:29],v5 offset:2048\n"
TR "v[30:31],v5 offset:8192\n"
MF "a[180:183],v[20:23],v[92:95],a[180:183]\n"
"v_add_f32 v53,v53,v119\n"
CV "v79,v118,v119\n"
MF "a[184:187],v[20:23],v[96:99],a[184:187]\n"
"v_exp_f32 v120,v120\n"
MF "a[164:167],v[16:19],v[96:99],a[164:167]\n"
"v_exp_f32 v121,v121\n"
MF "a[168:171],v[16:19],v[100:103],a[168:171]\n"
"v_exp_f32 v122,v122\n"
MF "a[188:191],v[20:23],v[100:103],a[188:191]\n"
"v_exp_f32 v123,v123\n"
MF "a[192:195],v[20:23],v[104:107],a[192:195]\n"
"v_add_f32 v54,v54,v120\n"
"v_add_f32 v54,v54,v121\n"
MF "a[172:175],v[16:19],v[104:107],a[172:175]\n"
"s_waitcnt vmcnt(6) lgkmcnt(0)\n"
CV "v82,v120,v121\n"
"v_add_f32 v54,v54,v122\n"
MF "a[176:179],v[16:19],v[108:111],a[176:179]\n"
"s_barrier\n"
MF "a[196:199],v[20:23],v[108:111],a[196:199]\n"
"s_add_u32 s72,s72,6144\n"
"ds_read_b128 v[16:19],v0 offset:24576\n"
"ds_read_b128 v[20:23],v1 offset:24576\n"
MF "a[216:219],v[24:27],v[108:111],a[216:219]\n"
"s_cmp_eq_u32 s72,s17\n"
"s_cselect_b32 s72,s16,s72\n"
"s_add_u32 m0,s64,0\n"
MF "a[236:239],v[28:31],v[108:111],a[236:239]\n"
LD "v7,s[32:35],s72 offen nt lds\n"
MF "a[232:235],v[28:31],v[104:107],a[232:235]\n"
"s_add_u32 m0,s64,896\n"
"v_add_f32 v54,v54,v123\n"
CV "v83,v122,v123\n"
MF "a[212:215],v[24:27],v[104:107],a[212:215]\n"
LD "v7,s[32:35],s72 offen offset:128 nt lds\n"
MF "a[208:211],v[24:27],v[100:103],a[208:211]\n"
"s_add_u32 m0,s64,1792\n"
MF "a[228:231],v[28:31],v[100:103],a[228:231]\n"
LD "v7,s[32:35],s72 offen offset:256 nt lds\n"
MF "a[224:227],v[28:31],v[96:99],a[224:227]\n"
MF "a[204:207],v[24:27],v[96:99],a[204:207]\n"
MF "a[200:203],v[24:27],v[92:95],a[200:203]\n"
MF "a[220:223],v[28:31],v[92:95],a[220:223]\n"
"s_waitcnt lgkmcnt(0)\n"
MF "v[92:95],v[16:19],v[136:139],v[32:35]\n"
"s_add_u32 s72,s72,6144\n"
"ds_read_b128 v[24:27],v0 offset:25600\n"
"ds_read_b128 v[28:31],v1 offset:25600\n"
MF "v[92:95],v[20:23],v[156:159],v[92:95]\n"
"s_add_u32 m0,s64,6144\n"
"v_exp_f32 v124,v124\n"
MF "v[96:99],v[20:23],v[160:163],v[36:39]\n"
LD "v7,s[32:35],s72 offen nt lds\n"
MF "v[96:99],v[16:19],v[140:143],v[96:99]\n"
"v_exp_f32 v125,v125\n"
MF "v[100:103],v[16:19],v[144:147],v[40:43]\n"
"s_add_u32 m0,s64,7040\n"
"v_exp_f32 v126,v126\n"
MF "v[100:103],v[20:23],v[164:167],v[100:103]\n"
LD "v7,s[32:35],s72 offen offset:128 nt lds\n"
MF "v[104:107],v[20:23],v[168:171],v[44:47]\n"
"v_exp_f32 v127,v127\n"
MF "v[104:107],v[16:19],v[148:151],v[104:107]\n"
"s_add_u32 m0,s64,7936\n"
"v_add_f32 v55,v55,v124\n"
"v_add_f32 v55,v55,v125\n"
MF "v[108:111],v[16:19],v[152:155],v[48:51]\n"
LD "v7,s[32:35],s72 offen offset:256 nt lds\n"
MF "v[108:111],v[20:23],v[172:175],v[108:111]\n"
"s_waitcnt lgkmcnt(0)\n"
"ds_read_b128 v[16:19],v0 offset:26624\n"
"ds_read_b128 v[20:23],v1 offset:26624\n"
MF "v[108:111],v[24:27],v[192:195],v[108:111]\n"
CV "v86,v124,v125\n"
"v_add_f32 v55,v55,v126\n"
MF "v[108:111],v[28:31],v[212:215],v[108:111]\n"
"v_add_f32 v55,v55,v127\n"
CV "v87,v126,v127\n"
MF "v[104:107],v[28:31],v[208:211],v[104:107]\n"
"v_exp_f32 v128,v128\n"
MF "v[104:107],v[24:27],v[188:191],v[104:107]\n"
"v_exp_f32 v129,v129\n"
MF "v[100:103],v[24:27],v[184:187],v[100:103]\n"
"v_exp_f32 v130,v130\n"
MF "v[100:103],v[28:31],v[204:207],v[100:103]\n"
"v_exp_f32 v131,v131\n"
MF "v[96:99],v[28:31],v[200:203],v[96:99]\n"
"v_add_f32 v56,v56,v128\n"
"v_add_f32 v56,v56,v129\n"
MF "v[96:99],v[24:27],v[180:183],v[96:99]\n"
CV "v90,v128,v129\n"
"v_add_f32 v56,v56,v130\n"
MF "v[92:95],v[24:27],v[176:179],v[92:95]\n"
"v_add_f32 v56,v56,v131\n"
CV "v91,v130,v131\n"
MF "v[92:95],v[28:31],v[196:199],v[92:95]\n"
"s_waitcnt lgkmcnt(0)\n"
"ds_read_b128 v[24:27],v0 offset:30720\n"
"ds_read_b128 v[28:31],v1 offset:30720\n"
MF "v[92:95],v[16:19],v[216:219],v[92:95]\n"
MF "v[92:95],v[20:23],v[236:239],v[92:95]\n"
MF "v[96:99],v[20:23],v[240:243],v[96:99]\n"
MF "v[96:99],v[16:19],v[220:223],v[96:99]\n"
MF "v[100:103],v[16:19],v[224:227],v[100:103]\n"
MF "v[100:103],v[20:23],v[244:247],v[100:103]\n"
MF "v[104:107],v[20:23],v[248:251],v[104:107]\n"
MF "v[104:107],v[16:19],v[228:231],v[104:107]\n"
"v_max3_f32 v10,v52,v53,v54\n"
"v_max3_f32 v10,v10,v55,v56\n"
MF "v[108:111],v[16:19],v[232:235],v[108:111]\n"
"v_cmp_ngt_f32 vcc,s77,v10\n"
MF "v[108:111],v[20:23],v[252:255],v[108:111]\n"
"s_waitcnt lgkmcnt(0)\n"
"s_cbranch_vccnz .Lstub_25\n"
".Lback_25:\n"
MF "v[112:115],v[24:27],v[136:139],v[32:35]\n"
"s_add_u32 s74,s74,12288\n"
"ds_read_b128 v[16:19],v0 offset:31744\n"
"ds_read_b128 v[20:23],v1 offset:31744\n"
MF "v[112:115],v[28:31],v[156:159],v[112:115]\n"
"s_cmp_eq_u32 s74,s20\n"
"s_cselect_b32 s74,s70,s74\n"
MF "v[116:119],v[28:31],v[160:163],v[36:39]\n"
"v_exp_f32 v92,v92\n"
MF "v[116:119],v[24:27],v[140:143],v[116:119]\n"
"v_exp_f32 v93,v93\n"
MF "v[120:123],v[24:27],v[144:147],v[40:43]\n"
"v_exp_f32 v94,v94\n"
MF "v[120:123],v[28:31],v[164:167],v[120:123]\n"
"v_exp_f32 v95,v95\n"
MF "v[124:127],v[28:31],v[168:171],v[44:47]\n"
"v_add_f32 v57,v52,v92\n"
"v_add_f32 v57,v57,v93\n"
MF "v[124:127],v[24:27],v[148:151],v[124:127]\n"
CV "v92,v92,v93\n"
"v_add_f32 v57,v57,v94\n"
MF "v[128:131],v[24:27],v[152:155],v[48:51]\n"
"v_add_f32 v57,v57,v95\n"
CV "v93,v94,v95\n"
MF "v[128:131],v[28:31],v[172:175],v[128:131]\n"
"s_waitcnt lgkmcnt(0)\n"
"ds_read_b128 v[24:27],v0 offset:32768\n"
"ds_read_b128 v[28:31],v1 offset:32768\n"
MF "v[128:131],v[16:19],v[192:195],v[128:131]\n"
"s_sub_u32 s61,s61,1\n"
"v_exp_f32 v96,v96\n"
MF "v[128:131],v[20:23],v[212:215],v[128:131]\n"
"s_mov_b32 s95,0\n"
"v_exp_f32 v97,v97\n"
MF "v[124:127],v[20:23],v[208:211],v[124:127]\n"
"v_exp_f32 v98,v98\n"
MF "v[124:127],v[16:19],v[188:191],v[124:127]\n"
"v_exp_f32 v99,v99\n"
MF "v[120:123],v[16:19],v[184:187],v[120:123]\n"
"v_add_f32 v58,v53,v96\n"
"v_add_f32 v58,v58,v97\n"
MF "v[120:123],v[20:23],v[204:207],v[120:123]\n"
CV "v96,v96,v97\n"
"v_add_f32 v58,v58,v98\n"
MF "v[116:119],v[20:23],v[200:203],v[116:119]\n"
"s_waitcnt vmcnt(6) lgkmcnt(0)\n"
"v_add_f32 v58,v58,v99\n"
CV "v97,v98,v99\n"
MF "v[116:119],v[16:19],v[180:183],v[116:119]\n"
"s_barrier\n"
MF "v[112:115],v[16:19],v[176:179],v[112:115]\n"
"v_exp_f32 v100,v100\n"
MF "v[112:115],v[20:23],v[196:199],v[112:115]\n"
"s_nop 0\n"
TR "v[16:17],v2 offset:49152\n"
TR "v[18:19],v2 offset:55296\n"
MF "v[112:115],v[24:27],v[216:219],v[112:115]\n"
"s_add_u32 s73,s73,12288\n"
TR "v[20:21],v3 offset:49152\n"
TR "v[22:23],v3 offset:55296\n"
MF "v[112:115],v[28:31],v[236:239],v[112:115]\n"
"s_cmp_eq_u32 s73,s19\n"
"s_cselect_b32 s73,s18,s73\n"
"s_add_u32 m0,s65,24576\n"
MF "v[116:119],v[28:31],v[240:243],v[116:119]\n"
LD "v8,s[36:39],s73 offen nt lds\n"
MF "v[116:119],v[24:27],v[220:223],v[116:119]\n"
"s_add_u32 m0,s65,25472\n"
"v_exp_f32 v101,v101\n"
MF "v[120:123],v[24:27],v[224:227],v[120:123]\n"
LD "v8,s[36:39],s73 offen offset:128 nt lds\n"
MF "v[120:123],v[28:31],v[244:247],v[120:123]\n"
"v_exp_f32 v102,v102\n"
MF "v[124:127],v[28:31],v[248:251],v[124:127]\n"
"s_add_u32 m0,s65,26368\n"
"v_exp_f32 v103,v103\n"
MF "v[124:127],v[24:27],v[228:231],v[124:127]\n"
LD "v8,s[36:39],s73 offen offset:256 nt lds\n"
MF "v[128:131],v[24:27],v[232:235],v[128:131]\n"
"v_add_f32 v59,v54,v100\n"
"v_add_f32 v59,v59,v101\n"
MF "v[128:131],v[28:31],v[252:255],v[128:131]\n"
"s_waitcnt lgkmcnt(0)\n"
TR "v[24:25],v4 offset:49152\n"
TR "v[26:27],v4 offset:55296\n"
MF "a[0:3],v[16:19],v[72:75],a[0:3]\n"
"s_add_u32 m0,s65,24576\n"
TR "v[28:29],v5 offset:49152\n"
TR "v[30:31],v5 offset:55296\n"
MF "a[20:23],v[20:23],v[72:75],a[20:23]\n"
LD "v8,s[36:39],s73 offen offset:3072 nt lds\n"
MF "a[24:27],v[20:23],v[76:79],a[24:27]\n"
CV "v100,v100,v101\n"
"v_add_f32 v59,v59,v102\n"
MF "a[4:7],v[16:19],v[76:79],a[4:7]\n"
"s_add_u32 m0,s65,25472\n"
"v_add_f32 v59,v59,v103\n"
CV "v101,v102,v103\n"
MF "a[8:11],v[16:19],v[80:83],a[8:11]\n"
LD "v8,s[36:39],s73 offen offset:3200 nt lds\n"
MF "a[28:31],v[20:23],v[80:83],a[28:31]\n"
"v_exp_f32 v104,v104\n"
MF "a[32:35],v[20:23],v[84:87],a[32:35]\n"
"s_add_u32 m0,s65,26368\n"
"v_exp_f32 v105,v105\n"
MF "a[12:15],v[16:19],v[84:87],a[12:15]\n"
LD "v8,s[36:39],s73 offen offset:3328 nt lds\n"
MF "a[16:19],v[16:19],v[88:91],a[16:19]\n"
"v_exp_f32 v106,v106\n"
MF "a[36:39],v[20:23],v[88:91],a[36:39]\n"
"s_waitcnt lgkmcnt(0)\n"
TR "v[16:17],v2 offset:50176\n"
TR "v[18:19],v2 offset:56320\n"
MF "a[56:59],v[24:27],v[88:91],a[56:59]\n"
"s_nop 0\n"
TR "v[20:21],v3 offset:50176\n"
TR "v[22:23],v3 offset:56320\n"
MF "a[76:79],v[28:31],v[88:91],a[76:79]\n"
"v_exp_f32 v107,v107\n"
MF "a[72:75],v[28:31],v[84:87],a[72:75]\n"
"v_add_f32 v60,v55,v104\n"
"v_add_f32 v60,v60,v105\n"
MF "a[52:55],v[24:27],v[84:87],a[52:55]\n"
CV "v104,v104,v105\n"
"v_add_f32 v60,v60,v106\n"
MF "a[48:51],v[24:27],v[80:83],a[48:51]\n"
"v_add_f32 v60,v60,v107\n"
CV "v105,v106,v107\n"
MF "a[68:71],v[28:31],v[80:83],a[68:71]\n"
"v_exp_f32 v108,v108\n"
MF "a[64:67],v[28:31],v[76:79],a[64:67]\n"
"v_exp_f32 v109,v109\n"
MF "a[44:47],v[24:27],v[76:79],a[44:47]\n"
"v_exp_f32 v110,v110\n"
MF "a[40:43],v[24:27],v[72:75],a[40:43]\n"
"v_exp_f32 v111,v111\n"
MF "a[60:63],v[28:31],v[72:75],a[60:63]\n"
"s_waitcnt lgkmcnt(0)\n"
TR "v[24:25],v4 offset:50176\n"
TR "v[26:27],v4 offset:56320\n"
MF "a[80:83],v[16:19],v[72:75],a[80:83]\n"
"s_nop 0\n"
TR "v[28:29],v5 offset:50176\n"
TR "v[30:31],v5 offset:56320\n"
MF "a[100:103],v[20:23],v[72:75],a[100:103]\n"
"v_add_f32 v61,v56,v108\n"
"v_add_f32 v61,v61,v109\n"
MF "a[104:107],v[20:23],v[76:79],a[104:107]\n"
CV "v108,v108,v109\n"
"v_add_f32 v61,v61,v110\n"
MF "a[84:87],v[16:19],v[76:79],a[84:87]\n"
"v_add_f32 v61,v61,v111\n"
CV "v109,v110,v111\n"
MF "a[88:91],v[16:19],v[80:83],a[88:91]\n"
"v_exp_f32 v112,v112\n"
MF "a[108:111],v[20:23],v[80:83],a[108:111]\n"
"v_exp_f32 v113,v113\n"
MF "a[112:115],v[20:23],v[84:87],a[112:115]\n"
"v_exp_f32 v114,v114\n"
MF "a[92:95],v[16:19],v[84:87],a[92:95]\n"
"v_exp_f32 v115,v115\n"
MF "a[96:99],v[16:19],v[88:91],a[96:99]\n"
"v_add_f32 v57,v57,v112\n"
"v_add_f32 v57,v57,v113\n"
MF "a[116:119],v[20:23],v[88:91],a[116:119]\n"
"s_waitcnt lgkmcnt(0)\n"
TR "v[16:17],v2 offset:51200\n"
TR "v[18:19],v2 offset:57344\n"
MF "a[136:139],v[24:27],v[88:91],a[136:139]\n"
"s_nop 0\n"
TR "v[20:21],v3 offset:51200\n"
TR "v[22:23],v3 offset:57344\n"
MF "a[156:159],v[28:31],v[88:91],a[156:159]\n"
CV "v94,v112,v113\n"
"v_add_f32 v57,v57,v114\n"
MF "a[152:155],v[28:31],v[84:87],a[152:155]\n"
"v_add_f32 v57,v57,v115\n"
CV "v95,v114,v115\n"
MF "a[132:135],v[24:27],v[84:87],a[132:135]\n"
"v_exp_f32 v116,v116\n"
MF "a[128:131],v[24:27],v[80:83],a[128:131]\n"
"v_exp_f32 v117,v117\n"
MF "a[148:151],v[28:31],v[80:83],a[148:151]\n"
"v_exp_f32 v118,v118\n"
MF "a[144:147],v[28:31],v[76:79],a[144:147]\n"
"v_exp_f32 v119,v119\n"
MF "a[124:127],v[24:27],v[76:79],a[124:127]\n"
"v_add_f32 v58,v58,v116\n"
"v_add_f32 v58,v58,v117\n"
MF "a[120:123],v[24:27],v[72:75],a[120:123]\n"
CV "v98,v116,v117\n"
"v_add_f32 v58,v58,v118\n"
MF "a[140:143],v[28:31],v[72:75],a[140:143]\n"
"s_waitcnt lgkmcnt(0)\n"
TR "v[24:25],v4 offset:51200\n"
TR "v[26:27],v4 offset:57344\n"
MF "a[160:163],v[16:19],v[72:75],a[160:163]\n"
"s_nop 0\n"
TR "v[28:29],v5 offset:51200\n"
TR "v[30:31],v5 offset:57344\n"
MF "a[180:183],v[20:23],v[72:75],a[180:183]\n"
"v_add_f32 v58,v58,v119\n"
CV "v99,v118,v119\n"
MF "a[184:187],v[20:23],v[76:79],a[184:187]\n"
"v_exp_f32 v120,v120\n"
MF "a[164:167],v[16:19],v[76:79],a[164:167]\n"
"v_exp_f32 v121,v121\n"
MF "a[168:171],v[16:19],v[80:83],a[168:171]\n"
"v_exp_f32 v122,v122\n"
MF "a[188:191],v[20:23],v[80:83],a[188:191]\n"
"v_exp_f32 v123,v123\n"
MF "a[192:195],v[20:23],v[84:87],a[192:195]\n"
"v_add_f32 v59,v59,v120\n"
"v_add_f32 v59,v59,v121\n"
MF "a[172:175],v[16:19],v[84:87],a[172:175]\n"
"s_waitcnt vmcnt(6) lgkmcnt(0)\n"
CV "v102,v120,v121\n"
"v_add_f32 v59,v59,v122\n"
MF "a[176:179],v[16:19],v[88:91],a[176:179]\n"
"s_barrier\n"
MF "a[196:199],v[20:23],v[88:91],a[196:199]\n"
"s_add_u32 s72,s72,6144\n"
"ds_read_b128 v[16:19],v0\n"
"ds_read_b128 v[20:23],v1\n"
MF "a[216:219],v[24:27],v[88:91],a[216:219]\n"
"s_cmp_eq_u32 s72,s17\n"
"s_cselect_b32 s72,s16,s72\n"
"s_add_u32 m0,s64,49152\n"
MF "a[236:239],v[28:31],v[88:91],a[236:239]\n"
LD "v7,s[32:35],s72 offen nt lds\n"
MF "a[232:235],v[28:31],v[84:87],a[232:235]\n"
"s_add_u32 m0,s64,50048\n"
"v_add_f32 v59,v59,v123\n"
CV "v103,v122,v123\n"
MF "a[212:215],v[24:27],v[84:87],a[212:215]\n"
LD "v7,s[32:35],s72 offen offset:128 nt lds\n"
MF "a[208:211],v[24:27],v[80:83],a[208:211]\n"
"s_add_u32 m0,s64,50944\n"
MF "a[228:231],v[28:31],v[80:83],a[228:231]\n"
LD "v7,s[32:35],s72 offen offset:256 nt lds\n"
MF "a[224:227],v[28:31],v[76:79],a[224:227]\n"
MF "a[204:207],v[24:27],v[76:79],a[204:207]\n"
"s_waitcnt lgkmcnt(0)\n"
MF "a[200:203],v[24:27],v[72:75],a[200:203]\n"
"s_cmp_lg_u32 s61,0\n"
MF "a[220:223],v[28:31],v[72:75],a[220:223]\n"
"s_cbranch_scc1 .Laloop\n"
".Lepi2:\n"
MF "v[72:75],v[16:19],v[136:139],v[32:35]\n"
"s_add_u32 s72,s72,6144\n"
"ds_read_b128 v[24:27],v0 offset:1024\n"
"ds_read_b128 v[28:31],v1 offset:1024\n"
MF "v[72:75],v[20:23],v[156:159],v[72:75]\n"
"s_add_u32 m0,s64,55296\n"
"v_exp_f32 v124,v124\n"
MF "v[76:79],v[20:23],v[160:163],v[36:39]\n"
LD "v7,s[32:35],s72 offen nt lds\n"
MF "v[76:79],v[16:19],v[140:143],v[76:79]\n"
"v_exp_f32 v125,v125\n"
MF "v[80:83],v[16:19],v[144:147],v[40:43]\n"
"s_add_u32 m0,s64,56192\n"
"v_exp_f32 v126,v126\n"
MF "v[80:83],v[20:23],v[164:167],v[80:83]\n"
LD "v7,s[32:35],s72 offen offset:128 nt lds\n"
MF "v[84:87],v[20:23],v[168:171],v[44:47]\n"
"v_exp_f32 v127,v127\n"
MF "v[84:87],v[16:19],v[148:151],v[84:87]\n"
"s_add_u32 m0,s64,57088\n"
"v_add_f32 v60,v60,v124\n"
"v_add_f32 v60,v60,v125\n"
MF "v[88:91],v[16:19],v[152:155],v[48:51]\n"
LD "v7,s[32:35],s72 offen offset:256 nt lds\n"
MF "v[88:91],v[20:23],v[172:175],v[88:91]\n"
"s_waitcnt lgkmcnt(0)\n"
"ds_read_b128 v[16:19],v0 offset:2048\n"
"ds_read_b128 v[20:23],v1 offset:2048\n"
MF "v[88:91],v[24:27],v[192:195],v[88:91]\n"
CV "v106,v124,v125\n"
"v_add_f32 v60,v60,v126\n"
MF "v[88:91],v[28:31],v[212:215],v[88:91]\n"
"v_add_f32 v60,v60,v127\n"
CV "v107,v126,v127\n"
MF "v[84:87],v[28:31],v[208:211],v[84:87]\n"
"v_exp_f32 v128,v128\n"
MF "v[84:87],v[24:27],v[188:191],v[84:87]\n"
"v_exp_f32 v129,v129\n"
MF "v[80:83],v[24:27],v[184:187],v[80:83]\n"
"v_exp_f32 v130,v130\n"
MF "v[80:83],v[28:31],v[204:207],v[80:83]\n"
"v_exp_f32 v131,v131\n"
MF "v[76:79],v[28:31],v[200:203],v[76:79]\n"
"v_add_f32 v61,v61,v128\n"
"v_add_f32 v61,v61,v129\n"
MF "v[76:79],v[24:27],v[180:183],v[76:79]\n"
CV "v110,v128,v129\n"
"v_add_f32 v61,v61,v130\n"
MF "v[72:75],v[24:27],v[176:179],v[72:75]\n"
"v_add_f32 v61,v61,v131\n"
CV "v111,v130,v131\n"
MF "v[72:75],v[28:31],v[196:199],v[72:75]\n"
"s_waitcnt lgkmcnt(0)\n"
"ds_read_b128 v[24:27],v0 offset:6144\n"
"ds_read_b128 v[28:31],v1 offset:6144\n"
MF "v[72:75],v[16:19],v[216:219],v[72:75]\n"
MF "v[72:75],v[20:23],v[236:239],v[72:75]\n"
MF "v[76:79],v[20:23],v[240:243],v[76:79]\n"
MF "v[76:79],v[16:19],v[220:223],v[76:79]\n"
MF "v[80:83],v[16:19],v[224:227],v[80:83]\n"
MF "v[80:83],v[20:23],v[244:247],v[80:83]\n"
MF "v[84:87],v[20:23],v[248:251],v[84:87]\n"
MF "v[84:87],v[16:19],v[228:231],v[84:87]\n"
"v_max3_f32 v10,v57,v58,v59\n"
"v_max3_f32 v10,v10,v60,v61\n"
MF "v[88:91],v[16:19],v[232:235],v[88:91]\n"
"v_cmp_le_f32 vcc,0x47800000,v10\n"
MF "v[88:91],v[20:23],v[252:255],v[88:91]\n"
"s_waitcnt lgkmcnt(0)\n"
"s_cbranch_vccnz .Lstub_26\n"
".Lback_26:\n"
MF "v[112:115],v[24:27],v[136:139],v[32:35]\n"
"s_add_u32 s74,s74,12288\n"
"ds_read_b128 v[16:19],v0 offset:7168\n"
"ds_read_b128 v[20:23],v1 offset:7168\n"
MF "v[112:115],v[28:31],v[156:159],v[112:115]\n"
"s_cmp_eq_u32 s74,s20\n"
"s_cselect_b32 s74,s70,s74\n"
MF "v[116:119],v[28:31],v[160:163],v[36:39]\n"
"v_exp_f32 v72,v72\n"
MF "v[116:119],v[24:27],v[140:143],v[116:119]\n"
"v_exp_f32 v73,v73\n"
MF "v[120:123],v[24:27],v[144:147],v[40:43]\n"
"v_exp_f32 v74,v74\n"
MF "v[120:123],v[28:31],v[164:167],v[120:123]\n"
"v_exp_f32 v75,v75\n"
MF "v[124:127],v[28:31],v[168:171],v[44:47]\n"
"v_add_f32 v52,v57,v72\n"
"v_add_f32 v52,v52,v73\n"
MF "v[124:127],v[24:27],v[148:151],v[124:127]\n"
CV "v72,v72,v73\n"
"v_add_f32 v52,v52,v74\n"
MF "v[128:131],v[24:27],v[152:155],v[48:51]\n"
"v_add_f32 v52,v52,v75\n"
CV "v73,v74,v75\n"
MF "v[128:131],v[28:31],v[172:175],v[128:131]\n"
"s_waitcnt lgkmcnt(0)\n"
"ds_read_b128 v[24:27],v0 offset:8192\n"
"ds_read_b128 v[28:31],v1 offset:8192\n"
MF "v[128:131],v[16:19],v[192:195],v[128:131]\n"
"v_exp_f32 v76,v76\n"
MF "v[128:131],v[20:23],v[212:215],v[128:131]\n"
"v_exp_f32 v77,v77\n"
MF "v[124:127],v[20:23],v[208:211],v[124:127]\n"
"v_exp_f32 v78,v78\n"
MF "v[124:127],v[16:19],v[188:191],v[124:127]\n"
"v_exp_f32 v79,v79\n"
MF "v[120:123],v[16:19],v[184:187],v[120:123]\n"
"v_add_f32 v53,v58,v76\n"
"v_add_f32 v53,v53,v77\n"
MF "v[120:123],v[20:23],v[204:207],v[120:123]\n"
CV "v76,v76,v77\n"
"v_add_f32 v53,v53,v78\n"
MF "v[116:119],v[20:23],v[200:203],v[116:119]\n"
"s_waitcnt vmcnt(6) lgkmcnt(0)\n"
"v_add_f32 v53,v53,v79\n"
CV "v77,v78,v79\n"
MF "v[116:119],v[16:19],v[180:183],v[116:119]\n"
"s_barrier\n"
MF "v[112:115],v[16:19],v[176:179],v[112:115]\n"
"v_lshrrev_b32 v7,7,v9\n"
"v_mad_u32_u24 v7,v7,s6,v8\n"
MF "v[112:115],v[20:23],v[196:199],v[112:115]\n"
"s_nop 0\n"
TR "v[16:17],v2 offset:24576\n"
TR "v[18:19],v2 offset:30720\n"
MF "v[112:115],v[24:27],v[216:219],v[112:115]\n"
"s_add_u32 s73,s73,12288\n"
TR "v[20:21],v3 offset:24576\n"
TR "v[22:23],v3 offset:30720\n"
MF "v[112:115],v[28:31],v[236:239],v[112:115]\n"
"s_cmp_eq_u32 s73,s19\n"
"s_cselect_b32 s73,s18,s73\n"
"s_add_u32 m0,s65,0\n"
MF "v[116:119],v[28:31],v[240:243],v[116:119]\n"
LD "v8,s[36:39],s73 offen nt lds\n"
MF "v[116:119],v[24:27],v[220:223],v[116:119]\n"
"s_add_u32 m0,s65,896\n"
"v_exp_f32 v80,v80\n"
MF "v[120:123],v[24:27],v[224:227],v[120:123]\n"
LD "v8,s[36:39],s73 offen offset:128 nt lds\n"
MF "v[120:123],v[28:31],v[244:247],v[120:123]\n"
"v_exp_f32 v81,v81\n"
MF "v[124:127],v[28:31],v[248:251],v[124:127]\n"
"s_add_u32 m0,s65,1792\n"
"v_exp_f32 v82,v82\n"
MF "v[124:127],v[24:27],v[228:231],v[124:127]\n"
LD "v8,s[36:39],s73 offen offset:256 nt lds\n"
MF "v[128:131],v[24:27],v[232:235],v[128:131]\n"
"v_exp_f32 v83,v83\n"
MF "v[128:131],v[28:31],v[252:255],v[128:131]\n"
"s_waitcnt lgkmcnt(0)\n"
TR "v[24:25],v4 offset:24576\n"
TR "v[26:27],v4 offset:30720\n"
MF "a[0:3],v[16:19],v[92:95],a[0:3]\n"
"s_add_u32 m0,s65,0\n"
TR "v[28:29],v5 offset:24576\n"
TR "v[30:31],v5 offset:30720\n"
MF "a[20:23],v[20:23],v[92:95],a[20:23]\n"
LD "v8,s[36:39],s73 offen offset:3072 nt lds\n"
MF "a[24:27],v[20:23],v[96:99],a[24:27]\n"
"v_add_f32 v54,v59,v80\n"
"v_add_f32 v54,v54,v81\n"
MF "a[4:7],v[16:19],v[96:99],a[4:7]\n"
"s_add_u32 m0,s65,896\n"
CV "v80,v80,v81\n"
"v_add_f32 v54,v54,v82\n"
MF "a[8:11],v[16:19],v[100:103],a[8:11]\n"
LD "v8,s[36:39],s73 offen offset:3200 nt lds\n"
MF "a[28:31],v[20:23],v[100:103],a[28:31]\n"
"v_add_f32 v54,v54,v83\n"
CV "v81,v82,v83\n"
MF "a[32:35],v[20:23],v[104:107],a[32:35]\n"
"s_add_u32 m0,s65,1792\n"
"v_exp_f32 v84,v84\n"
MF "a[12:15],v[16:19],v[104:107],a[12:15]\n"
LD "v8,s[36:39],s73 offen offset:3328 nt lds\n"
MF "a[16:19],v[16:19],v[108:111],a[16:19]\n"
"v_exp_f32 v85,v85\n"
MF "a[36:39],v[20:23],v[108:111],a[36:39]\n"
"s_waitcnt lgkmcnt(0)\n"
TR "v[16:17],v2 offset:25600\n"
TR "v[18:19],v2 offset:31744\n"
MF "a[56:59],v[24:27],v[108:111],a[56:59]\n"
"s_nop 0\n"
TR "v[20:21],v3 offset:25600\n"
TR "v[22:23],v3 offset:31744\n"
MF "a[76:79],v[28:31],v[108:111],a[76:79]\n"
"v_exp_f32 v86,v86\n"
MF "a[72:75],v[28:31],v[104:107],a[72:75]\n"
"v_exp_f32 v87,v87\n"
MF "a[52:55],v[24:27],v[104:107],a[52:55]\n"
"v_add_f32 v55,v60,v84\n"
"v_add_f32 v55,v55,v85\n"
MF "a[48:51],v[24:27],v[100:103],a[48:51]\n"
CV "v84,v84,v85\n"
"v_add_f32 v55,v55,v86\n"
MF "a[68:71],v[28:31],v[100:103],a[68:71]\n"
"v_add_f32 v55,v55,v87\n"
CV "v85,v86,v87\n"
MF "a[64:67],v[28:31],v[96:99],a[64:67]\n"
"v_exp_f32 v88,v88\n"
MF "a[44:47],v[24:27],v[96:99],a[44:47]\n"
"v_exp_f32 v89,v89\n"
MF "a[40:43],v[24:27],v[92:95],a[40:43]\n"
"v_exp_f32 v90,v90\n"
MF "a[60:63],v[28:31],v[92:95],a[60:63]\n"
"s_waitcnt lgkmcnt(0)\n"
TR "v[24:25],v4 offset:25600\n"
TR "v[26:27],v4 offset:31744\n"
MF "a[80:83],v[16:19],v[92:95],a[80:83]\n"
"s_nop 0\n"
TR "v[28:29],v5 offset:25600\n"
TR "v[30:31],v5 offset:31744\n"
MF "a[100:103],v[20:23],v[92:95],a[100:103]\n"
"v_exp_f32 v91,v91\n"
MF "a[104:107],v[20:23],v[96:99],a[104:107]\n"
"v_add_f32 v56,v61,v88\n"
"v_add_f32 v56,v56,v89\n"
MF "a[84:87],v[16:19],v[96:99],a[84:87]\n"
CV "v88,v88,v89\n"
"v_add_f32 v56,v56,v90\n"
MF "a[88:91],v[16:19],v[100:103],a[88:91]\n"
"v_add_f32 v56,v56,v91\n"
CV "v89,v90,v91\n"
MF "a[108:111],v[20:23],v[100:103],a[108:111]\n"
"v_exp_f32 v112,v112\n"
MF "a[112:115],v[20:23],v[104:107],a[112:115]\n"
"v_exp_f32 v113,v113\n"
MF "a[92:95],v[16:19],v[104:107],a[92:95]\n"
"v_exp_f32 v114,v114\n"
MF "a[96:99],v[16:19],v[108:111],a[96:99]\n"
"v_exp_f32 v115,v115\n"
MF "a[116:119],v[20:23],v[108:111],a[116:119]\n"
"s_waitcnt lgkmcnt(0)\n"
TR "v[16:17],v2 offset:26624\n"
TR "v[18:19],v2 offset:32768\n"
MF "a[136:139],v[24:27],v[108:111],a[136:139]\n"
"s_nop 0\n"
TR "v[20:21],v3 offset:26624\n"
TR "v[22:23],v3 offset:32768\n"
MF "a[156:159],v[28:31],v[108:111],a[156:159]\n"
"v_add_f32 v52,v52,v112\n"
"v_add_f32 v52,v52,v113\n"
MF "a[152:155],v[28:31],v[104:107],a[152:155]\n"
CV "v74,v112,v113\n"
"v_add_f32 v52,v52,v114\n"
MF "a[132:135],v[24:27],v[104:107],a[132:135]\n"
"v_add_f32 v52,v52,v115\n"
CV "v75,v114,v115\n"
MF "a[128:131],v[24:27],v[100:103],a[128:131]\n"
"v_exp_f32 v116,v116\n"
MF "a[148:151],v[28:31],v[100:103],a[148:151]\n"
"v_exp_f32 v117,v117\n"
MF "a[144:147],v[28:31],v[96:99],a[144:147]\n"
"v_exp_f32 v118,v118\n"
MF "a[124:127],v[24:27],v[96:99],a[124:127]\n"
"v_exp_f32 v119,v119\n"
MF "a[120:123],v[24:27],v[92:95],a[120:123]\n"
"v_add_f32 v53,v53,v116\n"
"v_add_f32 v53,v53,v117\n"
MF "a[140:143],v[28:31],v[92:95],a[140:143]\n"
"s_waitcnt lgkmcnt(0)\n"
TR "v[24:25],v4 offset:26624\n"
TR "v[26:27],v4 offset:32768\n"
MF "a[160:163],v[16:19],v[92:95],a[160:163]\n"
"s_nop 0\n"
TR "v[28:29],v5 offset:26624\n"
TR "v[30:31],v5 offset:32768\n"
MF "a[180:183],v[20:23],v[92:95],a[180:183]\n"
CV "v78,v116,v117\n"
"v_add_f32 v53,v53,v118\n"
MF "a[184:187],v[20:23],v[96:99],a[184:187]\n"
"v_add_f32 v53,v53,v119\n"
CV "v79,v118,v119\n"
MF "a[164:167],v[16:19],v[96:99],a[164:167]\n"
"v_exp_f32 v120,v120\n"
MF "a[168:171],v[16:19],v[100:103],a[168:171]\n"
"v_exp_f32 v121,v121\n"
MF "a[188:191],v[20:23],v[100:103],a[188:191]\n"
"v_exp_f32 v122,v122\n"
MF "a[192:195],v[20:23],v[104:107],a[192:195]\n"
"v_exp_f32 v123,v123\n"
MF "a[172:175],v[16:19],v[104:107],a[172:175]\n"
"s_waitcnt vmcnt(6) lgkmcnt(0)\n"
"v_add_f32 v54,v54,v120\n"
"v_add_f32 v54,v54,v121\n"
MF "a[176:179],v[16:19],v[108:111],a[176:179]\n"
"s_barrier\n"
MF "a[196:199],v[20:23],v[108:111],a[196:199]\n"
"s_add_u32 s60,s0,0\n"
"ds_read_b128 v[16:19],v0 offset:49152\n"
"ds_read_b128 v[20:23],v1 offset:49152\n"
MF "a[216:219],v[24:27],v[108:111],a[216:219]\n"
"s_add_u32 m0,s5,24576\n"
CV "v82,v120,v121\n"
"v_add_f32 v54,v54,v122\n"
MF "a[236:239],v[28:31],v[108:111],a[236:239]\n"
LD "v7,s[48:51],s60 offen lds\n"
MF "a[232:235],v[28:31],v[104:107],a[232:235]\n"
"s_add_u32 s60,s0,10240\n"
"s_add_u32 m0,s5,25600\n"
"v_add_f32 v54,v54,v123\n"
MF "a[212:215],v[24:27],v[104:107],a[212:215]\n"
LD "v7,s[48:51],s60 offen lds\n"
MF "a[208:211],v[24:27],v[100:103],a[208:211]\n"
"s_add_u32 s60,s0,20480\n"
"s_add_u32 m0,s5,26624\n"
CV "v83,v122,v123\n"
MF "a[228:231],v[28:31],v[100:103],a[228:231]\n"
LD "v7,s[48:51],s60 offen lds\n"
MF "a[224:227],v[28:31],v[96:99],a[224:227]\n"
MF "a[204:207],v[24:27],v[96:99],a[204:207]\n"
MF "a[200:203],v[24:27],v[92:95],a[200:203]\n"
MF "a[220:223],v[28:31],v[92:95],a[220:223]\n"
"s_waitcnt lgkmcnt(0)\n"
MF "v[92:95],v[16:19],v[136:139],v[32:35]\n"
"s_add_u32 s60,s0,30720\n"
"ds_read_b128 v[24:27],v0 offset:50176\n"
"ds_read_b128 v[28:31],v1 offset:50176\n"
MF "v[92:95],v[20:23],v[156:159],v[92:95]\n"
"s_add_u32 m0,s5,27648\n"
"v_exp_f32 v124,v124\n"
MF "v[96:99],v[20:23],v[160:163],v[36:39]\n"
LD "v7,s[48:51],s60 offen lds\n"
MF "v[96:99],v[16:19],v[140:143],v[96:99]\n"
"s_add_u32 s60,s0,40960\n"
"v_exp_f32 v125,v125\n"
MF "v[100:103],v[16:19],v[144:147],v[40:43]\n"
"s_add_u32 m0,s5,28672\n"
"v_exp_f32 v126,v126\n"
MF "v[100:103],v[20:23],v[164:167],v[100:103]\n"
LD "v7,s[48:51],s60 offen lds\n"
MF "v[104:107],v[20:23],v[168:171],v[44:47]\n"
"s_add_u32 s60,s0,51200\n"
"v_exp_f32 v127,v127\n"
MF "v[104:107],v[16:19],v[148:151],v[104:107]\n"
"s_add_u32 m0,s5,29696\n"
"v_add_f32 v55,v55,v124\n"
"v_add_f32 v55,v55,v125\n"
MF "v[108:111],v[16:19],v[152:155],v[48:51]\n"
LD "v7,s[48:51],s60 offen lds\n"
MF "v[108:111],v[20:23],v[172:175],v[108:111]\n"
"s_waitcnt lgkmcnt(0)\n"
"ds_read_b128 v[16:19],v0 offset:51200\n"
"ds_read_b128 v[20:23],v1 offset:51200\n"
MF "v[108:111],v[24:27],v[192:195],v[108:111]\n"
CV "v86,v124,v125\n"
"v_add_f32 v55,v55,v126\n"
MF "v[108:111],v[28:31],v[212:215],v[108:111]\n"
"v_add_f32 v55,v55,v127\n"
CV "v87,v126,v127\n"
MF "v[104:107],v[28:31],v[208:211],v[104:107]\n"
"v_exp_f32 v128,v128\n"
MF "v[104:107],v[24:27],v[188:191],v[104:107]\n"
"v_exp_f32 v129,v129\n"
MF "v[100:103],v[24:27],v[184:187],v[100:103]\n"
"v_exp_f32 v130,v130\n"
MF "v[100:103],v[28:31],v[204:207],v[100:103]\n"
"v_exp_f32 v131,v131\n"
MF "v[96:99],v[28:31],v[200:203],v[96:99]\n"
"v_add_f32 v56,v56,v128\n"
"v_add_f32 v56,v56,v129\n"
MF "v[96:99],v[24:27],v[180:183],v[96:99]\n"
CV "v90,v128,v129\n"
"v_add_f32 v56,v56,v130\n"
MF "v[92:95],v[24:27],v[176:179],v[92:95]\n"
"v_add_f32 v56,v56,v131\n"
CV "v91,v130,v131\n"
MF "v[92:95],v[28:31],v[196:199],v[92:95]\n"
"s_waitcnt lgkmcnt(0)\n"
"ds_read_b128 v[24:27],v0 offset:55296\n"
"ds_read_b128 v[28:31],v1 offset:55296\n"
MF "v[92:95],v[16:19],v[216:219],v[92:95]\n"
MF "v[92:95],v[20:23],v[236:239],v[92:95]\n"
MF "v[96:99],v[20:23],v[240:243],v[96:99]\n"
MF "v[96:99],v[16:19],v[220:223],v[96:99]\n"
MF "v[100:103],v[16:19],v[224:227],v[100:103]\n"
MF "v[100:103],v[20:23],v[244:247],v[100:103]\n"
MF "v[104:107],v[20:23],v[248:251],v[104:107]\n"
MF "v[104:107],v[16:19],v[228:231],v[104:107]\n"
"v_max3_f32 v10,v52,v53,v54\n"
"v_max3_f32 v10,v10,v55,v56\n"
MF "v[108:111],v[16:19],v[232:235],v[108:111]\n"
"v_cmp_le_f32 vcc,0x47800000,v10\n"
MF "v[108:111],v[20:23],v[252:255],v[108:111]\n"
"s_waitcnt lgkmcnt(0)\n"
"s_cbranch_vccnz .Lstub_27\n"
".Lback_27:\n"
MF "v[112:115],v[24:27],v[136:139],v[32:35]\n"
"s_add_u32 s74,s74,12288\n"
"ds_read_b128 v[16:19],v0 offset:56320\n"
"ds_read_b128 v[20:23],v1 offset:56320\n"
MF "v[112:115],v[28:31],v[156:159],v[112:115]\n"
"s_cmp_eq_u32 s74,s20\n"
"s_cselect_b32 s74,s70,s74\n"
MF "v[116:119],v[28:31],v[160:163],v[36:39]\n"
"v_exp_f32 v92,v92\n"
MF "v[116:119],v[24:27],v[140:143],v[116:119]\n"
"v_exp_f32 v93,v93\n"
MF "v[120:123],v[24:27],v[144:147],v[40:43]\n"
"v_exp_f32 v94,v94\n"
MF "v[120:123],v[28:31],v[164:167],v[120:123]\n"
"v_exp_f32 v95,v95\n"
MF "v[124:127],v[28:31],v[168:171],v[44:47]\n"
"v_add_f32 v57,v52,v92\n"
"v_add_f32 v57,v57,v93\n"
MF "v[124:127],v[24:27],v[148:151],v[124:127]\n"
CV "v92,v92,v93\n"
"v_add_f32 v57,v57,v94\n"
MF "v[128:131],v[24:27],v[152:155],v[48:51]\n"
"v_add_f32 v57,v57,v95\n"
CV "v93,v94,v95\n"
MF "v[128:131],v[28:31],v[172:175],v[128:131]\n"
"s_waitcnt lgkmcnt(0)\n"
"ds_read_b128 v[24:27],v0 offset:57344\n"
"ds_read_b128 v[28:31],v1 offset:57344\n"
MF "v[128:131],v[16:19],v[192:195],v[128:131]\n"
"v_exp_f32 v96,v96\n"
MF "v[128:131],v[20:23],v[212:215],v[128:131]\n"
"v_exp_f32 v97,v97\n"
MF "v[124:127],v[20:23],v[208:211],v[124:127]\n"
"v_exp_f32 v98,v98\n"
MF "v[124:127],v[16:19],v[188:191],v[124:127]\n"
"v_exp_f32 v99,v99\n"
MF "v[120:123],v[16:19],v[184:187],v[120:123]\n"
"v_add_f32 v58,v53,v96\n"
"v_add_f32 v58,v58,v97\n"
MF "v[120:123],v[20:23],v[204:207],v[120:123]\n"
CV "v96,v96,v97\n"
"v_add_f32 v58,v58,v98\n"
MF "v[116:119],v[20:23],v[200:203],v[116:119]\n"
"s_waitcnt vmcnt(6) lgkmcnt(0)\n"
"v_add_f32 v58,v58,v99\n"
CV "v97,v98,v99\n"
MF "v[116:119],v[16:19],v[180:183],v[116:119]\n"
"s_barrier\n"
MF "v[112:115],v[16:19],v[176:179],v[112:115]\n"
"v_exp_f32 v100,v100\n"
MF "v[112:115],v[20:23],v[196:199],v[112:115]\n"
"s_nop 0\n"
TR "v[16:17],v2\n"
TR "v[18:19],v2 offset:6144\n"
MF "v[112:115],v[24:27],v[216:219],v[112:115]\n"
"s_add_u32 s73,s73,12288\n"
TR "v[20:21],v3\n"
TR "v[22:23],v3 offset:6144\n"
MF "v[112:115],v[28:31],v[236:239],v[112:115]\n"
"s_cmp_eq_u32 s73,s19\n"
"s_cselect_b32 s73,s18,s73\n"
"s_add_u32 m0,s65,49152\n"
MF "v[116:119],v[28:31],v[240:243],v[116:119]\n"
LD "v8,s[36:39],s73 offen nt lds\n"
MF "v[116:119],v[24:27],v[220:223],v[116:119]\n"
"s_add_u32 m0,s65,50048\n"
"v_exp_f32 v101,v101\n"
MF "v[120:123],v[24:27],v[224:227],v[120:123]\n"
LD "v8,s[36:39],s73 offen offset:128 nt lds\n"
MF "v[120:123],v[28:31],v[244:247],v[120:123]\n"
"v_exp_f32 v102,v102\n"
MF "v[124:127],v[28:31],v[248:251],v[124:127]\n"
"s_add_u32 m0,s65,50944\n"
"v_exp_f32 v103,v103\n"
MF "v[124:127],v[24:27],v[228:231],v[124:127]\n"
LD "v8,s[36:39],s73 offen offset:256 nt lds\n"
MF "v[128:131],v[24:27],v[232:235],v[128:131]\n"
"v_add_f32 v59,v54,v100\n"
"v_add_f32 v59,v59,v101\n"
MF "v[128:131],v[28:31],v[252:255],v[128:131]\n"
"s_waitcnt lgkmcnt(0)\n"
TR "v[24:25],v4\n"
TR "v[26:27],v4 offset:6144\n"
MF "a[0:3],v[16:19],v[72:75],a[0:3]\n"
"s_add_u32 m0,s65,49152\n"
TR "v[28:29],v5\n"
TR "v[30:31],v5 offset:6144\n"
MF "a[20:23],v[20:23],v[72:75],a[20:23]\n"
LD "v8,s[36:39],s73 offen offset:3072 nt lds\n"
MF "a[24:27],v[20:23],v[76:79],a[24:27]\n"
CV "v100,v100,v101\n"
"v_add_f32 v59,v59,v102\n"
MF "a[4:7],v[16:19],v[76:79],a[4:7]\n"
"s_add_u32 m0,s65,50048\n"
"v_add_f32 v59,v59,v103\n"
CV "v101,v102,v103\n"
MF "a[8:11],v[16:19],v[80:83],a[8:11]\n"
LD "v8,s[36:39],s73 offen offset:3200 nt lds\n"
MF "a[28:31],v[20:23],v[80:83],a[28:31]\n"
"v_exp_f32 v104,v104\n"
MF "a[32:35],v[20:23],v[84:87],a[32:35]\n"
"s_add_u32 m0,s65,50944\n"
"v_exp_f32 v105,v105\n"
MF "a[12:15],v[16:19],v[84:87],a[12:15]\n"
LD "v8,s[36:39],s73 offen offset:3328 nt lds\n"
MF "a[16:19],v[16:19],v[88:91],a[16:19]\n"
"v_exp_f32 v106,v106\n"
MF "a[36:39],v[20:23],v[88:91],a[36:39]\n"
"s_waitcnt lgkmcnt(0)\n"
TR "v[16:17],v2 offset:1024\n"
TR "v[18:19],v2 offset:7168\n"
MF "a[56:59],v[24:27],v[88:91],a[56:59]\n"
"s_nop 0\n"
TR "v[20:21],v3 offset:1024\n"
TR "v[22:23],v3 offset:7168\n"
MF "a[76:79],v[28:31],v[88:91],a[76:79]\n"
"v_exp_f32 v107,v107\n"
MF "a[72:75],v[28:31],v[84:87],a[72:75]\n"
"v_add_f32 v60,v55,v104\n"
"v_add_f32 v60,v60,v105\n"
MF "a[52:55],v[24:27],v[84:87],a[52:55]\n"
CV "v104,v104,v105\n"
"v_add_f32 v60,v60,v106\n"
MF "a[48:51],v[24:27],v[80:83],a[48:51]\n"
"v_add_f32 v60,v60,v107\n"
CV "v105,v106,v107\n"
MF "a[68:71],v[28:31],v[80:83],a[68:71]\n"
"v_exp_f32 v108,v108\n"
MF "a[64:67],v[28:31],v[76:79],a[64:67]\n"
"v_exp_f32 v109,v109\n"
MF "a[44:47],v[24:27],v[76:79],a[44:47]\n"
"v_exp_f32 v110,v110\n"
MF "a[40:43],v[24:27],v[72:75],a[40:43]\n"
"v_exp_f32 v111,v111\n"
MF "a[60:63],v[28:31],v[72:75],a[60:63]\n"
"s_waitcnt lgkmcnt(0)\n"
TR "v[24:25],v4 offset:1024\n"
TR "v[26:27],v4 offset:7168\n"
MF "a[80:83],v[16:19],v[72:75],a[80:83]\n"
"s_nop 0\n"
TR "v[28:29],v5 offset:1024\n"
TR "v[30:31],v5 offset:7168\n"
MF "a[100:103],v[20:23],v[72:75],a[100:103]\n"
"v_add_f32 v61,v56,v108\n"
"v_add_f32 v61,v61,v109\n"
MF "a[104:107],v[20:23],v[76:79],a[104:107]\n"
CV "v108,v108,v109\n"
"v_add_f32 v61,v61,v110\n"
MF "a[84:87],v[16:19],v[76:79],a[84:87]\n"
"v_add_f32 v61,v61,v111\n"
CV "v109,v110,v111\n"
MF "a[88:91],v[16:19],v[80:83],a[88:91]\n"
"v_exp_f32 v112,v112\n"
MF "a[108:111],v[20:23],v[80:83],a[108:111]\n"
"v_exp_f32 v113,v113\n"
MF "a[112:115],v[20:23],v[84:87],a[112:115]\n"
"v_exp_f32 v114,v114\n"
MF "a[92:95],v[16:19],v[84:87],a[92:95]\n"
"v_exp_f32 v115,v115\n"
MF "a[96:99],v[16:19],v[88:91],a[96:99]\n"
"v_add_f32 v57,v57,v112\n"
"v_add_f32 v57,v57,v113\n"
MF "a[116:119],v[20:23],v[88:91],a[116:119]\n"
"s_waitcnt lgkmcnt(0)\n"
TR "v[16:17],v2 offset:2048\n"
TR "v[18:19],v2 offset:8192\n"
MF "a[136:139],v[24:27],v[88:91],a[136:139]\n"
"s_nop 0\n"
TR "v[20:21],v3 offset:2048\n"
TR "v[22:23],v3 offset:8192\n"
MF "a[156:159],v[28:31],v[88:91],a[156:159]\n"
CV "v94,v112,v113\n"
"v_add_f32 v57,v57,v114\n"
MF "a[152:155],v[28:31],v[84:87],a[152:155]\n"
"v_add_f32 v57,v57,v115\n"
CV "v95,v114,v115\n"
MF "a[132:135],v[24:27],v[84:87],a[132:135]\n"
"v_exp_f32 v116,v116\n"
MF "a[128:131],v[24:27],v[80:83],a[128:131]\n"
"v_exp_f32 v117,v117\n"
MF "a[148:151],v[28:31],v[80:83],a[148:151]\n"
"v_exp_f32 v118,v118\n"
MF "a[144:147],v[28:31],v[76:79],a[144:147]\n"
"v_exp_f32 v119,v119\n"
MF "a[124:127],v[24:27],v[76:79],a[124:127]\n"
"v_add_f32 v58,v58,v116\n"
"v_add_f32 v58,v58,v117\n"
MF "a[120:123],v[24:27],v[72:75],a[120:123]\n"
CV "v98,v116,v117\n"
"v_add_f32 v58,v58,v118\n"
MF "a[140:143],v[28:31],v[72:75],a[140:143]\n"
"s_waitcnt lgkmcnt(0)\n"
TR "v[24:25],v4 offset:2048\n"
TR "v[26:27],v4 offset:8192\n"
MF "a[160:163],v[16:19],v[72:75],a[160:163]\n"
"s_nop 0\n"
TR "v[28:29],v5 offset:2048\n"
TR "v[30:31],v5 offset:8192\n"
MF "a[180:183],v[20:23],v[72:75],a[180:183]\n"
"v_add_f32 v58,v58,v119\n"
CV "v99,v118,v119\n"
MF "a[184:187],v[20:23],v[76:79],a[184:187]\n"
"v_exp_f32 v120,v120\n"
MF "a[164:167],v[16:19],v[76:79],a[164:167]\n"
"v_exp_f32 v121,v121\n"
MF "a[168:171],v[16:19],v[80:83],a[168:171]\n"
"v_exp_f32 v122,v122\n"
MF "a[188:191],v[20:23],v[80:83],a[188:191]\n"
".Lpg23h:\n"
"s_cbranch_execz .Lpg27h\n"
"v_exp_f32 v123,v123\n"
MF "a[192:195],v[20:23],v[84:87],a[192:195]\n"
"v_add_f32 v59,v59,v120\n"
"v_add_f32 v59,v59,v121\n"
MF "a[172:175],v[16:19],v[84:87],a[172:175]\n"
"s_waitcnt lgkmcnt(0)\n"
CV "v102,v120,v121\n"
"v_add_f32 v59,v59,v122\n"
MF "a[176:179],v[16:19],v[88:91],a[176:179]\n"
"s_barrier\n"
MF "a[196:199],v[20:23],v[88:91],a[196:199]\n"
"s_add_u32 s60,s1,0\n"
"s_add_u32 m0,s5,0\n"
"v_add_f32 v59,v59,v123\n"
MF "a[216:219],v[24:27],v[88:91],a[216:219]\n"
LD "v7,s[48:51],s60 offen lds\n"
MF "a[236:239],v[28:31],v[88:91],a[236:239]\n"
"s_add_u32 s60,s1,10240\n"
"s_add_u32 m0,s5,1024\n"
CV "v103,v122,v123\n"
MF "a[232:235],v[28:31],v[84:87],a[232:235]\n"
LD "v7,s[48:51],s60 offen lds\n"
MF "a[212:215],v[24:27],v[84:87],a[212:215]\n"
"s_add_u32 s60,s1,20480\n"
"s_add_u32 m0,s5,2048\n"
MF "a[208:211],v[24:27],v[80:83],a[208:211]\n"
LD "v7,s[48:51],s60 offen lds\n"
MF "a[228:231],v[28:31],v[80:83],a[228:231]\n"
"v_exp_f32 v124,v124\n"
MF "a[224:227],v[28:31],v[76:79],a[224:227]\n"
"v_exp_f32 v125,v125\n"
MF "a[204:207],v[24:27],v[76:79],a[204:207]\n"
"v_exp_f32 v126,v126\n"
MF "a[200:203],v[24:27],v[72:75],a[200:203]\n"
"v_exp_f32 v127,v127\n"
MF "a[220:223],v[28:31],v[72:75],a[220:223]\n"
"v_add_f32 v60,v60,v124\n"
"v_add_f32 v60,v60,v125\n"
CV "v106,v124,v125\n"
"v_add_f32 v60,v60,v126\n"
"v_add_f32 v60,v60,v127\n"
CV "v107,v126,v127\n"
"v_exp_f32 v128,v128\n"
"v_exp_f32 v129,v129\n"
"v_exp_f32 v130,v130\n"
"v_exp_f32 v131,v131\n"
"v_add_f32 v61,v61,v128\n"
"v_add_f32 v61,v61,v129\n"
CV "v110,v128,v129\n"
"v_add_f32 v61,v61,v130\n"
"v_add_f32 v61,v61,v131\n"
CV "v111,v130,v131\n"
"v_max3_f32 v10,v57,v58,v59\n"
"v_max3_f32 v10,v10,v60,v61\n"
"v_cmp_le_f32 vcc,0x47800000,v10\n"
"s_cbranch_vccnz .Lstub_28\n"
".Lback_28:\n"
"s_waitcnt vmcnt(3) lgkmcnt(0)\n"
"s_barrier\n"
"v_add_u32 v236,s9,v9\n"
"v_add_u32 v237,65536,v236\n"
"v_mov_b32 v10,1.0\n"
TR "v[160:161],v4 offset:50176\n"
TR "v[162:163],v4 offset:56320\n"
"v_mfma_f32_16x16x4_f32 v[16:19],v10,v57,0\n"
TR "v[164:165],v5 offset:50176\n"
TR "v[166:167],v5 offset:56320\n"
TR "v[168:169],v2 offset:51200\n"
TR "v[170:171],v2 offset:57344\n"
TR "v[172:173],v3 offset:51200\n"
TR "v[174:175],v3 offset:57344\n"
"v_mfma_f32_16x16x4_f32 v[20:23],v10,v58,0\n"
TR "v[176:177],v4 offset:51200\n"
TR "v[178:179],v4 offset:57344\n"
TR "v[180:181],v5 offset:51200\n"
TR "v[182:183],v5 offset:57344\n"
TR "v[136:137],v2 offset:49152\n"
TR "v[138:139],v2 offset:55296\n"
"v_mfma_f32_16x16x4_f32 v[24:27],v10,v59,0\n"
TR "v[140:141],v3 offset:49152\n"
TR "v[142:143],v3 offset:55296\n"
TR "v[144:145],v4 offset:49152\n"
TR "v[146:147],v4 offset:55296\n"
TR "v[148:149],v5 offset:49152\n"
TR "v[150:151],v5 offset:55296\n"
"v_mfma_f32_16x16x4_f32 v[28:31],v10,v60,0\n"
TR "v[152:153],v2 offset:50176\n"
TR "v[154:155],v2 offset:56320\n"
TR "v[156:157],v3 offset:50176\n"
TR "v[158:159],v3 offset:56320\n"
"v_mfma_f32_16x16x4_f32 v[32:35],v10,v61,0\n"
"s_add_u32 m0,s5,0\n"
"v_add_u32 v195,27648,v7\n"
LD "v195,s[48:51],s1 offen offset:3072 lds\n"
"s_add_u32 m0,s5,4096\n"
"v_add_u32 v196,40960,v7\n"
LD "v196,s[48:51],s1 offen lds\n"
"s_waitcnt lgkmcnt(15)\n"
MF "a[120:123],v[160:163],v[92:95],a[120:123]\n"
MF "a[140:143],v[164:167],v[92:95],a[140:143]\n"
"v_add_u32 v197,50176,v7\n"
LD "v197,s[48:51],s1 offen offset:1024 lds\n"
"v_rcp_f32 v208,v16\n"
"v_rcp_f32 v209,v20\n"
"v_rcp_f32 v210,v24\n"
"v_rcp_f32 v211,v28\n"
"v_rcp_f32 v212,v32\n"
MF "a[124:127],v[160:163],v[96:99],a[124:127]\n"
"v_mul_f32_e64 v214,-s13,v208\n"
"v_mul_f32_e64 v215,-s13,v209\n"
"v_mul_f32_e64 v216,-s13,v210\n"
"v_mul_f32_e64 v217,-s13,v211\n"
"v_mul_f32_e64 v218,-s13,v212\n"
AR "v72,a120\n"
AR "v73,a121\n"
AR "v74,a122\n"
MF "a[144:147],v[164:167],v[96:99],a[144:147]\n"
AR "v75,a123\n"
AR "v76,a140\n"
AR "v77,a141\n"
AR "v78,a142\n"
AR "v79,a143\n"
"v_pk_fma_f32 v[72:73],v[72:73],v[208:209],v[214:215] op_sel:[0,0,0] op_sel_hi:[1,0,0]\n"
"v_pk_fma_f32 v[74:75],v[74:75],v[208:209],v[214:215] op_sel:[0,0,0] op_sel_hi:[1,0,0]\n"
"v_pk_fma_f32 v[76:77],v[76:77],v[208:209],v[214:215] op_sel:[0,0,0] op_sel_hi:[1,0,0]\n"
"v_pk_fma_f32 v[78:79],v[78:79],v[208:209],v[214:215] op_sel:[0,0,0] op_sel_hi:[1,0,0]\n"
CV "v112,v72,v73\n"
CV "v113,v74,v75\n"
CV "v114,v76,v77\n"
CV "v115,v78,v79\n"
MF "a[128:131],v[160:163],v[100:103],a[128:131]\n"
"ds_read_b128 a[140:143],v237 offset:20480\n"
AW "a120,v112\n"
AW "a121,v113\n"
AW "a122,v114\n"
AW "a123,v115\n"
"s_waitcnt lgkmcnt(7)\n"
MF "a[44:47],v[144:147],v[96:99],a[44:47]\n"
AR "v80,a124\n"
AR "v81,a125\n"
AR "v82,a126\n"
MF "a[148:151],v[164:167],v[100:103],a[148:151]\n"
AR "v83,a127\n"
AR "v84,a144\n"
AR "v85,a145\n"
"s_nop 0\n"
AR "v86,a146\n"
AR "v87,a147\n"
"v_pk_fma_f32 v[80:81],v[80:81],v[208:209],v[214:215] op_sel:[0,1,1] op_sel_hi:[1,1,1]\n"
"v_pk_fma_f32 v[82:83],v[82:83],v[208:209],v[214:215] op_sel:[0,1,1] op_sel_hi:[1,1,1]\n"
"v_pk_fma_f32 v[84:85],v[84:85],v[208:209],v[214:215] op_sel:[0,1,1] op_sel_hi:[1,1,1]\n"
"v_pk_fma_f32 v[86:87],v[86:87],v[208:209],v[214:215] op_sel:[0,1,1] op_sel_hi:[1,1,1]\n"
CV "v116,v80,v81\n"
CV "v117,v82,v83\n"
CV "v118,v84,v85\n"
CV "v119,v86,v87\n"
MF "a[132:135],v[160:163],v[104:107],a[132:135]\n"
"ds_read_b128 a[144:147],v236\n"
AW "a124,v116\n"
AW "a125,v117\n"
AW "a126,v118\n"
AW "a127,v119\n"
"s_waitcnt lgkmcnt(6)\n"
MF "a[64:67],v[148:151],v[96:99],a[64:67]\n"
AR "v72,a128\n"
AR "v73,a129\n"
AR "v74,a130\n"
MF "a[152:155],v[164:167],v[104:107],a[152:155]\n"
AR "v75,a131\n"
AR "v76,a148\n"
AR "v77,a149\n"
"s_nop 0\n"
AR "v78,a150\n"
AR "v79,a151\n"
"v_pk_fma_f32 v[72:73],v[72:73],v[210:211],v[216:217] op_sel:[0,0,0] op_sel_hi:[1,0,0]\n"
"v_pk_fma_f32 v[74:75],v[74:75],v[210:211],v[216:217] op_sel:[0,0,0] op_sel_hi:[1,0,0]\n"
"v_pk_fma_f32 v[76:77],v[76:77],v[210:211],v[216:217] op_sel:[0,0,0] op_sel_hi:[1,0,0]\n"
"v_pk_fma_f32 v[78:79],v[78:79],v[210:211],v[216:217] op_sel:[0,0,0] op_sel_hi:[1,0,0]\n"
CV "v112,v72,v73\n"
CV "v113,v74,v75\n"
CV "v114,v76,v77\n"
CV "v115,v78,v79\n"
MF "a[136:139],v[160:163],v[108:111],a[136:139]\n"
"ds_read_b128 a[148:151],v236 offset:4096\n"
AW "a128,v112\n"
AW "a129,v113\n"
AW "a130,v114\n"
AW "a131,v115\n"
"s_waitcnt lgkmcnt(5)\n"
MF "a[84:87],v[152:155],v[96:99],a[84:87]\n"
AR "v80,a132\n"
AR "v81,a133\n"
AR "v82,a134\n"
MF "a[156:159],v[164:167],v[108:111],a[156:159]\n"
AR "v83,a135\n"
AR "v84,a152\n"
AR "v85,a153\n"
"s_nop 0\n"
AR "v86,a154\n"
AR "v87,a155\n"
"v_pk_fma_f32 v[80:81],v[80:81],v[210:211],v[216:217] op_sel:[0,1,1] op_sel_hi:[1,1,1]\n"
"v_pk_fma_f32 v[82:83],v[82:83],v[210:211],v[216:217] op_sel:[0,1,1] op_sel_hi:[1,1,1]\n"
"v_pk_fma_f32 v[84:85],v[84:85],v[210:211],v[216:217] op_sel:[0,1,1] op_sel_hi:[1,1,1]\n"
"v_pk_fma_f32 v[86:87],v[86:87],v[210:211],v[216:217] op_sel:[0,1,1] op_sel_hi:[1,1,1]\n"
CV "v116,v80,v81\n"
CV "v117,v82,v83\n"
CV "v118,v84,v85\n"
CV "v119,v86,v87\n"
MF "a[160:163],v[168:171],v[92:95],a[160:163]\n"
"ds_read_b128 a[152:155],v236 offset:8192\n"
AW "a132,v116\n"
AW "a133,v117\n"
AW "a134,v118\n"
AW "a135,v119\n"
"s_waitcnt lgkmcnt(4)\n"
MF "a[104:107],v[156:159],v[96:99],a[104:107]\n"
AR "v72,a136\n"
AR "v73,a137\n"
AR "v74,a138\n"
MF "a[180:183],v[172:175],v[92:95],a[180:183]\n"
AR "v75,a139\n"
AR "v76,a156\n"
AR "v77,a157\n"
"s_nop 0\n"
AR "v78,a158\n"
AR "v79,a159\n"
"v_pk_fma_f32 v[72:73],v[72:73],v[212:213],v[218:219] op_sel:[0,0,0] op_sel_hi:[1,0,0]\n"
"v_pk_fma_f32 v[74:75],v[74:75],v[212:213],v[218:219] op_sel:[0,0,0] op_sel_hi:[1,0,0]\n"
"v_pk_fma_f32 v[76:77],v[76:77],v[212:213],v[218:219] op_sel:[0,0,0] op_sel_hi:[1,0,0]\n"
"v_pk_fma_f32 v[78:79],v[78:79],v[212:213],v[218:219] op_sel:[0,0,0] op_sel_hi:[1,0,0]\n"
CV "v112,v72,v73\n"
CV "v113,v74,v75\n"
CV "v114,v76,v77\n"
CV "v115,v78,v79\n"
MF "a[164:167],v[168:171],v[96:99],a[164:167]\n"
"ds_read_b128 a[156:159],v236 offset:12288\n"
AW "a136,v112\n"
AW "a137,v113\n"
AW "a138,v114\n"
AW "a139,v115\n"
"v_max3_f32 v10,|v72|,|v73|,|v74|\n"
"v_max3_f32 v10,v10,|v75|,|v76|\n"
"v_max3_f32 v10,v10,|v77|,|v78|\n"
"v_max3_f32 v10,v10,|v78|,|v79|\n"
"v_mul_f32 v10,s10,v10\n"
"v_lshrrev_b32 v11,2,v9\n"
"v_readlane_b32 s12,v10,3\n"
"v_readlane_b32 s6,v10,22\n"
"v_readlane_b32 s7,v10,41\n"
"v_readlane_b32 s8,v10,60\n"
"v_add_u32 v11,s14,v11\n"
"s_min_u32 s12,s12,s6\n"
"s_min_u32 s7,s7,s8\n"
"s_min_u32 s12,s12,s7\n"
"s_and_b32 s12,s12,0x7f800000\n"
"s_sub_u32 s12,s12,0x14000000\n"
"s_cmp_lt_u32 s12,0x50000000\n"
"s_cselect_b32 s12,s12,0xe7400000\n"
"s_add_u32 s12,s12,0x18c00000\n"
"v_mov_b32_e64 v10,s12\n"
"v_or_b32 v12,0x80000000,v10\n"
"ds_write2st64_b32 v11,v10,v12 offset1:1\n"
MF "a[8:11],v[136:139],v[100:103],a[8:11]\n"
AR "v80,a160\n"
AR "v81,a161\n"
AR "v82,a162\n"
MF "a[184:187],v[172:175],v[96:99],a[184:187]\n"
AR "v83,a163\n"
AR "v84,a180\n"
AR "v85,a181\n"
AR "v86,a182\n"
AR "v87,a183\n"
"v_pk_fma_f32 v[80:81],v[80:81],v[208:209],v[214:215] op_sel:[0,0,0] op_sel_hi:[1,0,0]\n"
"v_pk_fma_f32 v[82:83],v[82:83],v[208:209],v[214:215] op_sel:[0,0,0] op_sel_hi:[1,0,0]\n"
"v_pk_fma_f32 v[84:85],v[84:85],v[208:209],v[214:215] op_sel:[0,0,0] op_sel_hi:[1,0,0]\n"
"v_pk_fma_f32 v[86:87],v[86:87],v[208:209],v[214:215] op_sel:[0,0,0] op_sel_hi:[1,0,0]\n"
CV "v116,v80,v81\n"
CV "v117,v82,v83\n"
CV "v118,v84,v85\n"
CV "v119,v86,v87\n"
MF "a[168:171],v[168:171],v[100:103],a[168:171]\n"
"ds_read_b128 a[180:183],v236 offset:16384\n"
AW "a160,v116\n"
AW "a161,v117\n"
AW "a162,v118\n"
AW "a163,v119\n"
MF "a[28:31],v[140:143],v[100:103],a[28:31]\n"
AR "v72,a164\n"
AR "v73,a165\n"
AR "v74,a166\n"
MF "a[188:191],v[172:175],v[100:103],a[188:191]\n"
AR "v75,a167\n"
AR "v76,a184\n"
AR "v77,a185\n"
AR "v78,a186\n"
AR "v79,a187\n"
"v_pk_fma_f32 v[72:73],v[72:73],v[208:209],v[214:215] op_sel:[0,1,1] op_sel_hi:[1,1,1]\n"
"v_pk_fma_f32 v[74:75],v[74:75],v[208:209],v[214:215] op_sel:[0,1,1] op_sel_hi:[1,1,1]\n"
"v_pk_fma_f32 v[76:77],v[76:77],v[208:209],v[214:215] op_sel:[0,1,1] op_sel_hi:[1,1,1]\n"
"v_pk_fma_f32 v[78:79],v[78:79],v[208:209],v[214:215] op_sel:[0,1,1] op_sel_hi:[1,1,1]\n"
CV "v112,v72,v73\n"
CV "v113,v74,v75\n"
CV "v114,v76,v77\n"
CV "v115,v78,v79\n"
MF "a[172:175],v[168:171],v[104:107],a[172:175]\n"
"ds_read_b128 a[184:187],v236 offset:20480\n"
AW "a164,v112\n"
AW "a165,v113\n"
AW "a166,v114\n"
AW "a167,v115\n"
MF "a[48:51],v[144:147],v[100:103],a[48:51]\n"
"s_waitcnt lgkmcnt(0)\n"
"s_barrier\n"
"s_add_u32 m0,s5,49152\n"
"v_add_u32_e64 v192,0,v7\n"
LD "v192,s[48:51],s2 offen lds\n"
AR "v80,a168\n"
AR "v81,a169\n"
AR "v82,a170\n"
MF "a[192:195],v[172:175],v[104:107],a[192:195]\n"
AR "v83,a171\n"
AR "v84,a188\n"
AR "v85,a189\n"
AR "v86,a190\n"
AR "v87,a191\n"
"v_pk_fma_f32 v[80:81],v[80:81],v[210:211],v[216:217] op_sel:[0,0,0] op_sel_hi:[1,0,0]\n"
"v_pk_fma_f32 v[82:83],v[82:83],v[210:211],v[216:217] op_sel:[0,0,0] op_sel_hi:[1,0,0]\n"
"v_pk_fma_f32 v[84:85],v[84:85],v[210:211],v[216:217] op_sel:[0,0,0] op_sel_hi:[1,0,0]\n"
"v_pk_fma_f32 v[86:87],v[86:87],v[210:211],v[216:217] op_sel:[0,0,0] op_sel_hi:[1,0,0]\n"
"v_add_u32 v193,9216,v7\n"
LD "v193,s[48:51],s2 offen offset:1024 lds\n"
"v_add_u32 v194,18432,v7\n"
LD "v194,s[48:51],s2 offen offset:2048 lds\n"
LD "v195,s[48:51],s2 offen offset:3072 lds\n"
"s_add_u32 m0,s5,53248\n"
"s_nop 0\n"
LD "v196,s[48:51],s2 offen lds\n"
LD "v197,s[48:51],s2 offen offset:1024 lds\n"
CV "v116,v80,v81\n"
CV "v117,v82,v83\n"
CV "v118,v84,v85\n"
CV "v119,v86,v87\n"
MF "a[176:179],v[168:171],v[108:111],a[176:179]\n"
"v_accvgpr_mov_b32 a188,a240\n"
"v_accvgpr_mov_b32 a189,a241\n"
"v_accvgpr_mov_b32 a190,a242\n"
"v_accvgpr_mov_b32 a191,a243\n"
AW "a168,v116\n"
AW "a169,v117\n"
AW "a170,v118\n"
AW "a171,v119\n"
MF "a[68:71],v[148:151],v[100:103],a[68:71]\n"
"s_add_u32 m0,s5,73728\n"
"s_nop 0\n"
LD "v192,s[48:51],s3 offen lds\n"
AR "v72,a172\n"
AR "v73,a173\n"
AR "v74,a174\n"
MF "a[196:199],v[172:175],v[108:111],a[196:199]\n"
AR "v75,a175\n"
AR "v76,a192\n"
AR "v77,a193\n"
AR "v78,a194\n"
AR "v79,a195\n"
"v_pk_fma_f32 v[72:73],v[72:73],v[210:211],v[216:217] op_sel:[0,1,1] op_sel_hi:[1,1,1]\n"
"v_pk_fma_f32 v[74:75],v[74:75],v[210:211],v[216:217] op_sel:[0,1,1] op_sel_hi:[1,1,1]\n"
"v_pk_fma_f32 v[76:77],v[76:77],v[210:211],v[216:217] op_sel:[0,1,1] op_sel_hi:[1,1,1]\n"
"v_pk_fma_f32 v[78:79],v[78:79],v[210:211],v[216:217] op_sel:[0,1,1] op_sel_hi:[1,1,1]\n"
CV "v112,v72,v73\n"
CV "v113,v74,v75\n"
CV "v114,v76,v77\n"
CV "v115,v78,v79\n"
MF "a[200:203],v[176:179],v[92:95],a[200:203]\n"
"v_accvgpr_mov_b32 a192,a244\n"
"v_accvgpr_mov_b32 a193,a245\n"
"v_accvgpr_mov_b32 a194,a246\n"
"v_accvgpr_mov_b32 a195,a247\n"
AW "a172,v112\n"
AW "a173,v113\n"
AW "a174,v114\n"
AW "a175,v115\n"
MF "a[88:91],v[152:155],v[100:103],a[88:91]\n"
LD "v193,s[48:51],s3 offen offset:1024 lds\n"
AR "v80,a176\n"
AR "v81,a177\n"
AR "v82,a178\n"
MF "a[220:223],v[180:183],v[92:95],a[220:223]\n"
AR "v83,a179\n"
AR "v84,a196\n"
AR "v85,a197\n"
AR "v86,a198\n"
AR "v87,a199\n"
"v_pk_fma_f32 v[80:81],v[80:81],v[212:213],v[218:219] op_sel:[0,0,0] op_sel_hi:[1,0,0]\n"
"v_pk_fma_f32 v[82:83],v[82:83],v[212:213],v[218:219] op_sel:[0,0,0] op_sel_hi:[1,0,0]\n"
"v_pk_fma_f32 v[84:85],v[84:85],v[212:213],v[218:219] op_sel:[0,0,0] op_sel_hi:[1,0,0]\n"
"v_pk_fma_f32 v[86:87],v[86:87],v[212:213],v[218:219] op_sel:[0,0,0] op_sel_hi:[1,0,0]\n"
CV "v116,v80,v81\n"
CV "v117,v82,v83\n"
CV "v118,v84,v85\n"
CV "v119,v86,v87\n"
MF "a[204:207],v[176:179],v[96:99],a[204:207]\n"
"v_accvgpr_mov_b32 a196,a248\n"
"v_accvgpr_mov_b32 a197,a249\n"
"v_accvgpr_mov_b32 a198,a250\n"
"v_accvgpr_mov_b32 a199,a251\n"
AW "a176,v116\n"
AW "a177,v117\n"
AW "a178,v118\n"
AW "a179,v119\n"
MF "a[108:111],v[156:159],v[100:103],a[108:111]\n"
AR "v72,a200\n"
AR "v73,a201\n"
AR "v74,a202\n"
MF "a[224:227],v[180:183],v[96:99],a[224:227]\n"
AR "v75,a203\n"
AR "v76,a220\n"
AR "v77,a221\n"
AR "v78,a222\n"
AR "v79,a223\n"
"v_pk_fma_f32 v[72:73],v[72:73],v[208:209],v[214:215] op_sel:[0,0,0] op_sel_hi:[1,0,0]\n"
"v_pk_fma_f32 v[74:75],v[74:75],v[208:209],v[214:215] op_sel:[0,0,0] op_sel_hi:[1,0,0]\n"
"v_pk_fma_f32 v[76:77],v[76:77],v[208:209],v[214:215] op_sel:[0,0,0] op_sel_hi:[1,0,0]\n"
"v_pk_fma_f32 v[78:79],v[78:79],v[208:209],v[214:215] op_sel:[0,0,0] op_sel_hi:[1,0,0]\n"
CV "v112,v72,v73\n"
CV "v113,v74,v75\n"
CV "v114,v76,v77\n"
CV "v115,v78,v79\n"
MF "a[208:211],v[176:179],v[100:103],a[208:211]\n"
AW "a220,v62\n"
AW "a221,v63\n"
AW "a222,v64\n"
AW "a223,v65\n"
AW "a200,v112\n"
AW "a201,v113\n"
AW "a202,v114\n"
AW "a203,v115\n"
MF "a[12:15],v[136:139],v[104:107],a[12:15]\n"
LD "v194,s[48:51],s3 offen offset:2048 lds\n"
AR "v80,a204\n"
AR "v81,a205\n"
AR "v82,a206\n"
MF "a[228:231],v[180:183],v[100:103],a[228:231]\n"
AR "v83,a207\n"
AR "v84,a224\n"
AR "v85,a225\n"
AR "v86,a226\n"
AR "v87,a227\n"
"v_pk_fma_f32 v[80:81],v[80:81],v[208:209],v[214:215] op_sel:[0,1,1] op_sel_hi:[1,1,1]\n"
"v_pk_fma_f32 v[82:83],v[82:83],v[208:209],v[214:215] op_sel:[0,1,1] op_sel_hi:[1,1,1]\n"
"v_pk_fma_f32 v[84:85],v[84:85],v[208:209],v[214:215] op_sel:[0,1,1] op_sel_hi:[1,1,1]\n"
"v_pk_fma_f32 v[86:87],v[86:87],v[208:209],v[214:215] op_sel:[0,1,1] op_sel_hi:[1,1,1]\n"
CV "v116,v80,v81\n"
CV "v117,v82,v83\n"
CV "v118,v84,v85\n"
CV "v119,v86,v87\n"
MF "a[212:215],v[176:179],v[104:107],a[212:215]\n"
AW "a224,v66\n"
AW "a225,v67\n"
AW "a226,v68\n"
AW "a227,v69\n"
AW "a204,v116\n"
AW "a205,v117\n"
AW "a206,v118\n"
AW "a207,v119\n"
MF "a[32:35],v[140:143],v[104:107],a[32:35]\n"
LD "v195,s[48:51],s3 offen offset:3072 lds\n"
AR "v72,a208\n"
AR "v73,a209\n"
AR "v74,a210\n"
MF "a[232:235],v[180:183],v[104:107],a[232:235]\n"
AR "v75,a211\n"
AR "v76,a228\n"
AR "v77,a229\n"
AR "v78,a230\n"
AR "v79,a231\n"
"v_pk_fma_f32 v[72:73],v[72:73],v[210:211],v[216:217] op_sel:[0,0,0] op_sel_hi:[1,0,0]\n"
"v_pk_fma_f32 v[74:75],v[74:75],v[210:211],v[216:217] op_sel:[0,0,0] op_sel_hi:[1,0,0]\n"
"v_pk_fma_f32 v[76:77],v[76:77],v[210:211],v[216:217] op_sel:[0,0,0] op_sel_hi:[1,0,0]\n"
"v_pk_fma_f32 v[78:79],v[78:79],v[210:211],v[216:217] op_sel:[0,0,0] op_sel_hi:[1,0,0]\n"
CV "v112,v72,v73\n"
CV "v113,v74,v75\n"
CV "v114,v76,v77\n"
CV "v115,v78,v79\n"
MF "a[216:219],v[176:179],v[108:111],a[216:219]\n"
AW "a228,v70\n"
AW "a229,v71\n"
AW "a230,v132\n"
AW "a231,v133\n"
AW "a208,v112\n"
AW "a209,v113\n"
AW "a210,v114\n"
AW "a211,v115\n"
MF "a[52:55],v[144:147],v[104:107],a[52:55]\n"
"s_add_u32 m0,s5,77824\n"
"s_nop 0\n"
LD "v196,s[48:51],s3 offen lds\n"
AR "v80,a212\n"
AR "v81,a213\n"
AR "v82,a214\n"
MF "a[236:239],v[180:183],v[108:111],a[236:239]\n"
AR "v83,a215\n"
AR "v84,a232\n"
AR "v85,a233\n"
"s_nop 0\n"
AR "v86,a234\n"
AR "v87,a235\n"
"v_pk_fma_f32 v[80:81],v[80:81],v[210:211],v[216:217] op_sel:[0,1,1] op_sel_hi:[1,1,1]\n"
"v_pk_fma_f32 v[82:83],v[82:83],v[210:211],v[216:217] op_sel:[0,1,1] op_sel_hi:[1,1,1]\n"
"v_pk_fma_f32 v[84:85],v[84:85],v[210:211],v[216:217] op_sel:[0,1,1] op_sel_hi:[1,1,1]\n"
"v_pk_fma_f32 v[86:87],v[86:87],v[210:211],v[216:217] op_sel:[0,1,1] op_sel_hi:[1,1,1]\n"
CV "v116,v80,v81\n"
CV "v117,v82,v83\n"
CV "v118,v84,v85\n"
CV "v119,v86,v87\n"
MF "a[0:3],v[136:139],v[92:95],a[0:3]\n"
AW "a232,v134\n"
AW "a233,v135\n"
AW "a234,v6\n"
AW "a235,v13\n"
AW "a212,v116\n"
AW "a213,v117\n"
AW "a214,v118\n"
AW "a215,v119\n"
MF "a[72:75],v[148:151],v[104:107],a[72:75]\n"
LD "v197,s[48:51],s3 offen offset:1024 lds\n"
AR "v72,a216\n"
AR "v73,a217\n"
AR "v74,a218\n"
MF "a[20:23],v[140:143],v[92:95],a[20:23]\n"
AR "v75,a219\n"
AR "v76,a236\n"
AR "v77,a237\n"
AR "v78,a238\n"
AR "v79,a239\n"
"v_pk_fma_f32 v[72:73],v[72:73],v[212:213],v[218:219] op_sel:[0,0,0] op_sel_hi:[1,0,0]\n"
"v_pk_fma_f32 v[74:75],v[74:75],v[212:213],v[218:219] op_sel:[0,0,0] op_sel_hi:[1,0,0]\n"
"v_pk_fma_f32 v[76:77],v[76:77],v[212:213],v[218:219] op_sel:[0,0,0] op_sel_hi:[1,0,0]\n"
"v_pk_fma_f32 v[78:79],v[78:79],v[212:213],v[218:219] op_sel:[0,0,0] op_sel_hi:[1,0,0]\n"
CV "v112,v72,v73\n"
CV "v113,v74,v75\n"
CV "v114,v76,v77\n"
CV "v115,v78,v79\n"
MF "a[40:43],v[144:147],v[92:95],a[40:43]\n"
AW "a236,v14\n"
AW "a237,v15\n"
"v_accvgpr_mov_b32 a238,a254\n"
"v_accvgpr_mov_b32 a239,a255\n"
AW "a216,v112\n"
AW "a217,v113\n"
AW "a218,v114\n"
AW "a219,v115\n"
MF "a[92:95],v[152:155],v[104:107],a[92:95]\n"
".Lpg24h:\n"
"s_cbranch_execz .Lpg28h\n"
AR "v80,a0\n"
AR "v81,a1\n"
AR "v82,a2\n"
MF "a[60:63],v[148:151],v[92:95],a[60:63]\n"
AR "v83,a3\n"
AR "v84,a20\n"
AR "v85,a21\n"
"s_nop 0\n"
AR "v86,a22\n"
AR "v87,a23\n"
"v_pk_fma_f32 v[80:81],v[80:81],v[208:209],v[214:215] op_sel:[0,0,0] op_sel_hi:[1,0,0]\n"
"v_pk_fma_f32 v[82:83],v[82:83],v[208:209],v[214:215] op_sel:[0,0,0] op_sel_hi:[1,0,0]\n"
"v_pk_fma_f32 v[84:85],v[84:85],v[208:209],v[214:215] op_sel:[0,0,0] op_sel_hi:[1,0,0]\n"
"v_pk_fma_f32 v[86:87],v[86:87],v[208:209],v[214:215] op_sel:[0,0,0] op_sel_hi:[1,0,0]\n"
CV "v116,v80,v81\n"
CV "v117,v82,v83\n"
CV "v118,v84,v85\n"
CV "v119,v86,v87\n"
MF "a[80:83],v[152:155],v[92:95],a[80:83]\n"
"ds_read_b128 a[20:23],v236 offset:24576\n"
AW "a0,v116\n"
AW "a1,v117\n"
AW "a2,v118\n"
AW "a3,v119\n"
MF "a[112:115],v[156:159],v[104:107],a[112:115]\n"
AR "v72,a40\n"
AR "v73,a41\n"
AR "v74,a42\n"
MF "a[100:103],v[156:159],v[92:95],a[100:103]\n"
AR "v75,a43\n"
AR "v76,a60\n"
AR "v77,a61\n"
AR "v78,a62\n"
AR "v79,a63\n"
"v_pk_fma_f32 v[72:73],v[72:73],v[208:209],v[214:215] op_sel:[0,0,0] op_sel_hi:[1,0,0]\n"
"v_pk_fma_f32 v[74:75],v[74:75],v[208:209],v[214:215] op_sel:[0,0,0] op_sel_hi:[1,0,0]\n"
"v_pk_fma_f32 v[76:77],v[76:77],v[208:209],v[214:215] op_sel:[0,0,0] op_sel_hi:[1,0,0]\n"
"v_pk_fma_f32 v[78:79],v[78:79],v[208:209],v[214:215] op_sel:[0,0,0] op_sel_hi:[1,0,0]\n"
CV "v112,v72,v73\n"
CV "v113,v74,v75\n"
CV "v114,v76,v77\n"
CV "v115,v78,v79\n"
MF "a[4:7],v[136:139],v[96:99],a[4:7]\n"
"ds_read_b128 a[60:63],v236 offset:45056\n"
AW "a40,v112\n"
AW "a41,v113\n"
AW "a42,v114\n"
AW "a43,v115\n"
MF "a[16:19],v[136:139],v[108:111],a[16:19]\n"
AR "v80,a80\n"
AR "v81,a81\n"
AR "v82,a82\n"
MF "a[24:27],v[140:143],v[96:99],a[24:27]\n"
AR "v83,a83\n"
AR "v84,a100\n"
AR "v85,a101\n"
AR "v86,a102\n"
AR "v87,a103\n"
"v_pk_fma_f32 v[80:81],v[80:81],v[208:209],v[214:215] op_sel:[0,0,0] op_sel_hi:[1,0,0]\n"
"v_pk_fma_f32 v[82:83],v[82:83],v[208:209],v[214:215] op_sel:[0,0,0] op_sel_hi:[1,0,0]\n"
"v_pk_fma_f32 v[84:85],v[84:85],v[208:209],v[214:215] op_sel:[0,0,0] op_sel_hi:[1,0,0]\n"
"v_pk_fma_f32 v[86:87],v[86:87],v[208:209],v[214:215] op_sel:[0,0,0] op_sel_hi:[1,0,0]\n"
CV "v116,v80,v81\n"
CV "v117,v82,v83\n"
CV "v118,v84,v85\n"
CV "v119,v86,v87\n"
MF "a[36:39],v[140:143],v[108:111],a[36:39]\n"
"ds_read_b128 a[100:103],v237\n"
AW "a80,v116\n"
AW "a81,v117\n"
AW "a82,v118\n"
AW "a83,v119\n"
MF "a[56:59],v[144:147],v[108:111],a[56:59]\n"
AR "v72,a4\n"
AR "v73,a5\n"
AR "v74,a6\n"
MF "a[76:79],v[148:151],v[108:111],a[76:79]\n"
AR "v75,a7\n"
AR "v76,a24\n"
AR "v77,a25\n"
AR "v78,a26\n"
AR "v79,a27\n"
"v_pk_fma_f32 v[72:73],v[72:73],v[208:209],v[214:215] op_sel:[0,1,1] op_sel_hi:[1,1,1]\n"
"v_pk_fma_f32 v[74:75],v[74:75],v[208:209],v[214:215] op_sel:[0,1,1] op_sel_hi:[1,1,1]\n"
"v_pk_fma_f32 v[76:77],v[76:77],v[208:209],v[214:215] op_sel:[0,1,1] op_sel_hi:[1,1,1]\n"
"v_pk_fma_f32 v[78:79],v[78:79],v[208:209],v[214:215] op_sel:[0,1,1] op_sel_hi:[1,1,1]\n"
CV "v112,v72,v73\n"
CV "v113,v74,v75\n"
CV "v114,v76,v77\n"
CV "v115,v78,v79\n"
MF "a[96:99],v[152:155],v[108:111],a[96:99]\n"
"ds_read_b128 a[24:27],v236 offset:28672\n"
AW "a4,v112\n"
AW "a5,v113\n"
AW "a6,v114\n"
AW "a7,v115\n"
MF "a[116:119],v[156:159],v[108:111],a[116:119]\n"
"v_and_b32 v0,512,v9\n"
"v_lshlrev_b32 v0,2,v0\n"
"v_sub_u32 v0,v2,v0\n"
"v_xor_b32 v0,s4,v0\n"
"v_mov_b32 v252,s15\n"
"s_waitcnt vmcnt(12) lgkmcnt(0)\n"
"s_barrier\n"
"ds_read_b128 v[252:255],v252\n"
TR "v[176:177],v0 offset:24576\n"
TR "v[178:179],v0 offset:26624\n"
TR "v[180:181],v0 offset:28672\n"
TR "v[182:183],v0 offset:30720\n"
TR "v[184:185],v0 offset:32768\n"
TR "v[186:187],v0 offset:34816\n"
"v_xor_b32 v1,32,v0\n"
"v_xor_b32 v2,64,v0\n"
"v_xor_b32 v3,96,v0\n"
"v_add_u32 v4,73728,v0\n"
"v_add_u32 v5,73728,v1\n"
"v_add_u32 v6,73728,v2\n"
"v_add_u32 v7,73728,v3\n"
"s_cmp_ge_u32 s56,2\n"
"s_cbranch_scc1 .Lrole1\n"
"s_waitcnt lgkmcnt(4)\n"
MF "v[16:19],a[120:123],v[176:179],v[252:255]\n"
"s_mov_b32 s65,128\n"
TR "v[188:189],v1 offset:24576\n"
AR "v220,a44\n"
"s_waitcnt lgkmcnt(3)\n"
MF "v[16:19],a[160:163],v[180:183],v[16:19]\n"
"s_mov_b32 s9,0x3f80\n"
TR "v[190:191],v1 offset:26624\n"
AR "v221,a45\n"
"s_waitcnt lgkmcnt(2)\n"
MF "v[16:19],a[200:203],v[184:187],v[16:19]\n"
"s_add_u32 s89,s89,s65\n"
AR "v222,a46\n"
AR "v223,a47\n"
MF "v[24:27],a[204:207],v[184:187],v[252:255]\n"
"s_bfe_u32 s58,s22,0x30014\n"
TR "a[240:241],v1 offset:28672\n"
AR "v224,a64\n"
MF "v[24:27],a[164:167],v[180:183],v[24:27]\n"
"s_add_u32 s61,s58,1\n"
TR "a[242:243],v1 offset:30720\n"
AR "v225,a65\n"
MF "v[24:27],a[124:127],v[176:179],v[24:27]\n"
"s_and_b32 s61,s61,7\n"
AR "v226,a66\n"
AR "v227,a67\n"
MF "v[32:35],a[128:131],v[176:179],v[252:255]\n"
"s_mov_b32 s64,9\n"
TR "a[244:245],v1 offset:32768\n"
"ds_read_b128 a[64:67],v236 offset:49152\n"
MF "v[32:35],a[168:171],v[180:183],v[32:35]\n"
"s_xor_b32 s67,s78,8\n"
TR "a[246:247],v1 offset:34816\n"
"v_fma_f32 v220,v220,v209,v215\n"
MF "v[32:35],a[208:211],v[184:187],v[32:35]\n"
"s_mov_b32 s71,204800\n"
"v_fma_f32 v221,v221,v209,v215\n"
"v_fma_f32 v222,v222,v209,v215\n"
MF "v[40:43],a[212:215],v[184:187],v[252:255]\n"
"s_mov_b32 s66,204800\n"
"v_fma_f32 v223,v223,v209,v215\n"
"v_fma_f32 v224,v224,v209,v215\n"
MF "v[40:43],a[172:175],v[180:183],v[40:43]\n"
"s_and_b32 s73,s56,1\n"
"v_fma_f32 v225,v225,v209,v215\n"
"v_fma_f32 v226,v226,v209,v215\n"
MF "v[40:43],a[132:135],v[176:179],v[40:43]\n"
"s_mul_i32 s73,s73,5120\n"
"v_fma_f32 v227,v227,v209,v215\n"
CV "v220,v220,v221\n"
MF "v[48:51],a[136:139],v[176:179],v[252:255]\n"
"s_add_u32 s73,s73,s93\n"
CV "v221,v222,v223\n"
CV "v222,v224,v225\n"
MF "v[48:51],a[176:179],v[180:183],v[48:51]\n"
"s_xor_b32 s58,s58,4\n"
CV "v223,v226,v227\n"
AW "a44,v220\n"
MF "v[48:51],a[216:219],v[184:187],v[48:51]\n"
"s_waitcnt lgkmcnt(0)\n"
AW "a45,v221\n"
AW "a46,v222\n"
MF "v[52:55],a[216:219],a[244:247],v[252:255]\n"
"s_mul_i32 s70,s58,204800\n"
TR "v[176:177],v2 offset:24576\n"
AW "a47,v223\n"
MF "v[52:55],a[176:179],a[240:243],v[52:55]\n"
"s_add_u32 s70,s70,s73\n"
TR "v[178:179],v2 offset:26624\n"
AR "v228,a84\n"
MF "v[52:55],a[136:139],v[188:191],v[52:55]\n"
"s_add_u32 s70,s70,0xa000\n"
AR "v229,a85\n"
AR "v230,a86\n"
MF "v[44:47],a[132:135],v[188:191],v[252:255]\n"
"s_mov_b32 s85,1\n"
TR "v[180:181],v2 offset:28672\n"
AR "v231,a87\n"
MF "v[44:47],a[172:175],a[240:243],v[44:47]\n"
"s_mov_b32 s83,0xf0000000\n"
TR "v[182:183],v2 offset:30720\n"
AR "v232,a104\n"
MF "v[44:47],a[212:215],a[244:247],v[44:47]\n"
"s_mov_b32 s82,0xf0000000\n"
AR "v233,a105\n"
AR "v234,a106\n"
MF "v[36:39],a[208:211],a[244:247],v[252:255]\n"
"s_add_u32 s58,s79,73728\n"
TR "v[184:185],v2 offset:32768\n"
AR "v235,a107\n"
MF "v[36:39],a[168:171],a[240:243],v[36:39]\n"
"s_mov_b32 s4,64\n"
TR "v[186:187],v2 offset:34816\n"
"ds_read_b128 a[104:107],v237 offset:4096\n"
MF "v[36:39],a[128:131],v[188:191],v[36:39]\n"
"s_mul_i32 s95,s92,3277\n"
"v_fma_f32 v228,v228,v209,v215\n"
"v_fma_f32 v229,v229,v209,v215\n"
MF "v[28:31],a[124:127],v[188:191],v[252:255]\n"
"s_lshr_b32 s95,s95,16\n"
"v_fma_f32 v230,v230,v209,v215\n"
"v_fma_f32 v231,v231,v209,v215\n"
MF "v[28:31],a[164:167],a[240:243],v[28:31]\n"
"s_mul_i32 s59,s95,20\n"
"v_fma_f32 v232,v232,v209,v215\n"
"v_fma_f32 v233,v233,v209,v215\n"
MF "v[28:31],a[204:207],a[244:247],v[28:31]\n"
"s_sub_u32 s59,s92,s59\n"
"v_fma_f32 v234,v234,v209,v215\n"
"v_fma_f32 v235,v235,v209,v215\n"
MF "v[20:23],a[200:203],a[244:247],v[252:255]\n"
"s_cmp_ge_u32 s59,10\n"
"s_cselect_b32 s96,4294557760,0\n"
CV "v228,v228,v229\n"
MF "v[20:23],a[160:163],a[240:243],v[20:23]\n"
"s_add_u32 s96,s96,1024\n"
CV "v229,v230,v231\n"
CV "v230,v232,v233\n"
MF "v[20:23],a[120:123],v[188:191],v[20:23]\n"
"s_waitcnt lgkmcnt(1)\n"
CV "v231,v234,v235\n"
AW "a84,v228\n"
MF "v[56:59],a[120:123],v[176:179],v[252:255]\n"
"s_mul_i32 s59,s59,40960\n"
TR "v[188:189],v3 offset:24576\n"
AW "a85,v229\n"
MF "v[56:59],a[160:163],v[180:183],v[56:59]\n"
"s_add_u32 s96,s96,s59\n"
TR "v[190:191],v3 offset:26624\n"
AW "a86,v230\n"
MF "v[56:59],a[200:203],v[184:187],v[56:59]\n"
"s_lshl_b32 s59,s95,7\n"
AW "a87,v231\n"
AR "v220,a8\n"
MF "v[64:67],a[204:207],v[184:187],v[252:255]\n"
"s_add_u32 s96,s96,s59\n"
TR "a[240:241],v3 offset:28672\n"
AR "v221,a9\n"
MF "v[64:67],a[164:167],v[180:183],v[64:67]\n"
"s_lshl_b32 s2,s92,10\n"
TR "a[242:243],v3 offset:30720\n"
AR "v222,a10\n"
MF "v[64:67],a[124:127],v[176:179],v[64:67]\n"
"s_add_u32 s2,s2,s93\n"
AR "v223,a11\n"
AR "v224,a28\n"
MF "v[72:75],a[128:131],v[176:179],v[252:255]\n"
"s_mov_b32 s0,s96\n"
TR "a[244:245],v3 offset:32768\n"
AR "v225,a29\n"
MF "v[72:75],a[168:171],v[180:183],v[72:75]\n"
"s_sub_u32 s2,s2,204800\n"
TR "a[246:247],v3 offset:34816\n"
AR "v226,a30\n"
MF "v[72:75],a[208:211],v[184:187],v[72:75]\n"
"s_mov_b32 s92,64\n"
AR "v227,a31\n"
"v_fma_f32 v220,v220,v210,v216\n"
MF "v[80:83],a[212:215],v[184:187],v[252:255]\n"
"ds_read_b128 a[28:31],v236 offset:32768\n"
"v_fma_f32 v221,v221,v210,v216\n"
MF "v[80:83],a[172:175],v[180:183],v[80:83]\n"
"v_fma_f32 v222,v222,v210,v216\n"
"v_fma_f32 v223,v223,v210,v216\n"
MF "v[80:83],a[132:135],v[176:179],v[80:83]\n"
"v_fma_f32 v224,v224,v210,v216\n"
"v_fma_f32 v225,v225,v210,v216\n"
MF "v[88:91],a[136:139],v[176:179],v[252:255]\n"
"v_fma_f32 v226,v226,v210,v216\n"
"v_fma_f32 v227,v227,v210,v216\n"
MF "v[88:91],a[176:179],v[180:183],v[88:91]\n"
"s_waitcnt vmcnt(0) lgkmcnt(0)\n"
CV "v220,v220,v221\n"
CV "v221,v222,v223\n"
MF "v[88:91],a[216:219],v[184:187],v[88:91]\n"
"s_barrier\n"
MF "v[92:95],a[216:219],a[244:247],v[252:255]\n"
TR "v[176:177],v0\n"
CV "v222,v224,v225\n"
MF "v[92:95],a[176:179],a[240:243],v[92:95]\n"
TR "v[178:179],v0 offset:2048\n"
CV "v223,v226,v227\n"
MF "v[92:95],a[136:139],v[188:191],v[92:95]\n"
TR "v[96:97],v0 offset:49152\n"
AW "a8,v220\n"
MF "v[84:87],a[132:135],v[188:191],v[252:255]\n"
TR "v[180:181],v0 offset:4096\n"
AW "a9,v221\n"
MF "v[84:87],a[172:175],a[240:243],v[84:87]\n"
TR "v[182:183],v0 offset:6144\n"
AW "a10,v222\n"
MF "v[84:87],a[212:215],a[244:247],v[84:87]\n"
TR "v[98:99],v0 offset:51200\n"
AW "a11,v223\n"
MF "v[76:79],a[208:211],a[244:247],v[252:255]\n"
TR "v[184:185],v0 offset:8192\n"
AR "v228,a48\n"
MF "v[76:79],a[168:171],a[240:243],v[76:79]\n"
TR "v[186:187],v0 offset:10240\n"
AR "v229,a49\n"
MF "v[76:79],a[128:131],v[188:191],v[76:79]\n"
TR "v[100:101],v0 offset:53248\n"
AR "v230,a50\n"
MF "v[68:71],a[124:127],v[188:191],v[252:255]\n"
TR "v[102:103],v0 offset:55296\n"
AR "v231,a51\n"
MF "v[68:71],a[164:167],a[240:243],v[68:71]\n"
TR "v[104:105],v0 offset:57344\n"
AR "v232,a68\n"
MF "v[68:71],a[204:207],a[244:247],v[68:71]\n"
TR "v[106:107],v0 offset:59392\n"
AR "v233,a69\n"
MF "v[60:63],a[200:203],a[244:247],v[252:255]\n"
TR "v[108:109],v4\n"
AR "v234,a70\n"
MF "v[60:63],a[160:163],a[240:243],v[60:63]\n"
TR "v[110:111],v4 offset:2048\n"
AR "v235,a71\n"
MF "v[60:63],a[120:123],v[188:191],v[60:63]\n"
"s_waitcnt lgkmcnt(6)\n"
TR "v[112:113],v4 offset:4096\n"
"ds_read_b128 a[68:71],v236 offset:53248\n"
MF "v[16:19],a[140:143],v[176:179],v[16:19]\n"
TR "v[188:189],v1\n"
"v_fma_f32 v228,v228,v210,v216\n"
MF "v[16:19],a[180:183],v[180:183],v[16:19]\n"
TR "v[190:191],v1 offset:2048\n"
"v_fma_f32 v229,v229,v210,v216\n"
MF "v[16:19],a[220:223],v[184:187],v[16:19]\n"
TR "v[114:115],v4 offset:6144\n"
"v_fma_f32 v230,v230,v210,v216\n"
MF "v[24:27],a[224:227],v[184:187],v[24:27]\n"
TR "a[240:241],v1 offset:4096\n"
"v_fma_f32 v231,v231,v210,v216\n"
MF "v[24:27],a[184:187],v[180:183],v[24:27]\n"
TR "a[242:243],v1 offset:6144\n"
"v_fma_f32 v232,v232,v210,v216\n"
MF "v[24:27],a[144:147],v[176:179],v[24:27]\n"
TR "v[116:117],v4 offset:8192\n"
"v_fma_f32 v233,v233,v210,v216\n"
MF "v[32:35],a[148:151],v[176:179],v[32:35]\n"
TR "a[244:245],v1 offset:8192\n"
"v_fma_f32 v234,v234,v210,v216\n"
MF "v[32:35],a[188:191],v[180:183],v[32:35]\n"
TR "a[246:247],v1 offset:10240\n"
"v_fma_f32 v235,v235,v210,v216\n"
MF "v[32:35],a[228:231],v[184:187],v[32:35]\n"
TR "v[118:119],v4 offset:10240\n"
CV "v228,v228,v229\n"
MF "v[40:43],a[232:235],v[184:187],v[40:43]\n"
TR "v[120:121],v1 offset:49152\n"
CV "v229,v230,v231\n"
MF "v[40:43],a[192:195],v[180:183],v[40:43]\n"
TR "v[122:123],v1 offset:51200\n"
CV "v230,v232,v233\n"
MF "v[40:43],a[152:155],v[176:179],v[40:43]\n"
TR "v[124:125],v1 offset:53248\n"
CV "v231,v234,v235\n"
MF "v[48:51],a[156:159],v[176:179],v[48:51]\n"
TR "v[126:127],v1 offset:55296\n"
AW "a48,v228\n"
MF "v[48:51],a[196:199],v[180:183],v[48:51]\n"
TR "v[128:129],v1 offset:57344\n"
AW "a49,v229\n"
MF "v[48:51],a[236:239],v[184:187],v[48:51]\n"
"s_waitcnt lgkmcnt(6)\n"
TR "v[130:131],v1 offset:59392\n"
AW "a50,v230\n"
MF "v[52:55],a[236:239],a[244:247],v[52:55]\n"
TR "v[176:177],v2\n"
AW "a51,v231\n"
MF "v[52:55],a[196:199],a[240:243],v[52:55]\n"
TR "v[178:179],v2 offset:2048\n"
AR "v220,a88\n"
MF "v[52:55],a[156:159],v[188:191],v[52:55]\n"
TR "v[132:133],v5\n"
AR "v221,a89\n"
MF "v[44:47],a[152:155],v[188:191],v[44:47]\n"
TR "v[180:181],v2 offset:4096\n"
AR "v222,a90\n"
MF "v[44:47],a[192:195],a[240:243],v[44:47]\n"
TR "v[182:183],v2 offset:6144\n"
AR "v223,a91\n"
MF "v[44:47],a[232:235],a[244:247],v[44:47]\n"
TR "v[134:135],v5 offset:2048\n"
AR "v224,a108\n"
MF "v[36:39],a[228:231],a[244:247],v[36:39]\n"
TR "v[184:185],v2 offset:8192\n"
AR "v225,a109\n"
MF "v[36:39],a[188:191],a[240:243],v[36:39]\n"
TR "v[186:187],v2 offset:10240\n"
AR "v226,a110\n"
MF "v[36:39],a[148:151],v[188:191],v[36:39]\n"
TR "v[136:137],v5 offset:4096\n"
AR "v227,a111\n"
MF "v[28:31],a[144:147],v[188:191],v[28:31]\n"
"s_nop 0\n"
TR "v[138:139],v5 offset:6144\n"
"ds_read_b128 a[108:111],v237 offset:8192\n"
MF "v[28:31],a[184:187],a[240:243],v[28:31]\n"
TR "v[140:141],v5 offset:8192\n"
"v_fma_f32 v220,v220,v210,v216\n"
MF "v[28:31],a[224:227],a[244:247],v[28:31]\n"
TR "v[142:143],v5 offset:10240\n"
"v_fma_f32 v221,v221,v210,v216\n"
MF "v[20:23],a[220:223],a[244:247],v[20:23]\n"
TR "v[144:145],v2 offset:49152\n"
"v_fma_f32 v222,v222,v210,v216\n"
MF "v[20:23],a[180:183],a[240:243],v[20:23]\n"
TR "v[146:147],v2 offset:51200\n"
"v_fma_f32 v223,v223,v210,v216\n"
MF "v[20:23],a[140:143],v[188:191],v[20:23]\n"
"s_waitcnt lgkmcnt(7)\n"
TR "v[148:149],v2 offset:53248\n"
"v_fma_f32 v224,v224,v210,v216\n"
MF "v[56:59],a[140:143],v[176:179],v[56:59]\n"
TR "v[188:189],v3\n"
"v_add_u32 v198,59392,v192\n"
MF "v[56:59],a[180:183],v[180:183],v[56:59]\n"
TR "v[190:191],v3 offset:2048\n"
"v_add_u32 v199,68608,v192\n"
MF "v[56:59],a[220:223],v[184:187],v[56:59]\n"
TR "v[150:151],v2 offset:55296\n"
"v_add_u32 v200,81920,v192\n"
"v_add_u32 v201,91136,v192\n"
MF "v[64:67],a[224:227],v[184:187],v[64:67]\n"
TR "a[240:241],v3 offset:4096\n"
"v_add_u32 v202,100352,v192\n"
MF "v[64:67],a[184:187],v[180:183],v[64:67]\n"
TR "a[242:243],v3 offset:6144\n"
"v_add_u32 v203,109568,v192\n"
MF "v[64:67],a[144:147],v[176:179],v[64:67]\n"
TR "v[152:153],v2 offset:57344\n"
"v_fma_f32 v225,v225,v210,v216\n"
MF "v[72:75],a[148:151],v[176:179],v[72:75]\n"
TR "a[244:245],v3 offset:8192\n"
"v_fma_f32 v226,v226,v210,v216\n"
MF "v[72:75],a[188:191],v[180:183],v[72:75]\n"
TR "a[246:247],v3 offset:10240\n"
"v_fma_f32 v227,v227,v210,v216\n"
MF "v[72:75],a[228:231],v[184:187],v[72:75]\n"
TR "v[154:155],v2 offset:59392\n"
CV "v220,v220,v221\n"
MF "v[80:83],a[232:235],v[184:187],v[80:83]\n"
TR "v[156:157],v6\n"
CV "v221,v222,v223\n"
MF "v[80:83],a[192:195],v[180:183],v[80:83]\n"
TR "v[158:159],v6 offset:2048\n"
CV "v222,v224,v225\n"
MF "v[80:83],a[152:155],v[176:179],v[80:83]\n"
TR "v[160:161],v6 offset:4096\n"
CV "v223,v226,v227\n"
MF "v[88:91],a[156:159],v[176:179],v[88:91]\n"
TR "v[162:163],v6 offset:6144\n"
AW "a88,v220\n"
MF "v[88:91],a[196:199],v[180:183],v[88:91]\n"
TR "v[164:165],v6 offset:8192\n"
"s_waitcnt lgkmcnt(0)\n"
MF "v[88:91],a[236:239],v[184:187],v[88:91]\n"
"s_barrier\n"
MF "v[92:95],a[236:239],a[244:247],v[92:95]\n"
"s_add_u32 s80,s89,122880\n"
"s_add_u32 m0,s79,0\n"
TR "v[166:167],v6 offset:10240\n"
MF "v[92:95],a[196:199],a[240:243],v[92:95]\n"
LD "v192,s[48:51],s80 offen lds\n"
MF "v[92:95],a[156:159],v[188:191],v[92:95]\n"
TR "v[168:169],v3 offset:49152\n"
AW "a89,v221\n"
MF "v[84:87],a[152:155],v[188:191],v[84:87]\n"
TR "v[170:171],v3 offset:51200\n"
AW "a90,v222\n"
MF "v[84:87],a[192:195],a[240:243],v[84:87]\n"
TR "v[172:173],v3 offset:53248\n"
AW "a91,v223\n"
MF "v[84:87],a[232:235],a[244:247],v[84:87]\n"
LD "v193,s[48:51],s80 offen offset:1024 lds\n"
MF "v[76:79],a[228:231],a[244:247],v[76:79]\n"
TR "v[174:175],v3 offset:55296\n"
AR "v228,a12\n"
MF "v[76:79],a[188:191],a[240:243],v[76:79]\n"
TR "v[204:205],v3 offset:57344\n"
AR "v229,a13\n"
MF "v[76:79],a[148:151],v[188:191],v[76:79]\n"
TR "v[206:207],v3 offset:59392\n"
AR "v230,a14\n"
MF "v[68:71],a[144:147],v[188:191],v[68:71]\n"
TR "v[244:245],v7\n"
AR "v231,a15\n"
MF "v[68:71],a[184:187],a[240:243],v[68:71]\n"
LD "v194,s[48:51],s80 offen offset:2048 lds\n"
MF "v[68:71],a[224:227],a[244:247],v[68:71]\n"
TR "v[246:247],v7 offset:2048\n"
AR "v232,a32\n"
MF "v[60:63],a[220:223],a[244:247],v[60:63]\n"
TR "v[248:249],v7 offset:4096\n"
AR "v233,a33\n"
MF "v[60:63],a[180:183],a[240:243],v[60:63]\n"
TR "v[250:251],v7 offset:6144\n"
AR "v234,a34\n"
MF "v[60:63],a[140:143],v[188:191],v[60:63]\n"
LD "v195,s[48:51],s80 offen offset:3072 lds\n"
MF "v[16:19],a[0:3],v[96:99],v[16:19]\n"
TR "v[238:239],v7 offset:8192\n"
AR "v235,a35\n"
MF "v[16:19],a[40:43],v[100:103],v[16:19]\n"
"s_nop 0\n"
TR "v[240:241],v7 offset:10240\n"
"ds_read_b128 a[32:35],v236 offset:36864\n"
MF "v[16:19],a[80:83],v[104:107],v[16:19]\n"
"v_fma_f32 v228,v228,v211,v217\n"
"v_fma_f32 v229,v229,v211,v217\n"
MF "v[16:19],a[20:23],v[108:111],v[16:19]\n"
".Lpg25h:\n"
"s_cbranch_execz .Lpg29h\n"
"s_add_u32 m0,s79,4096\n"
"v_fma_f32 v230,v230,v211,v217\n"
"v_fma_f32 v231,v231,v211,v217\n"
MF "v[16:19],a[60:63],v[112:115],v[16:19]\n"
LD "v196,s[48:51],s80 offen lds\n"
MF "v[16:19],a[100:103],v[116:119],v[16:19]\n"
"v_fma_f32 v232,v232,v211,v217\n"
"v_fma_f32 v233,v233,v211,v217\n"
MF "v[20:23],a[100:103],v[140:143],v[20:23]\n"
"v_fma_f32 v234,v234,v211,v217\n"
"v_fma_f32 v235,v235,v211,v217\n"
MF "v[20:23],a[60:63],v[136:139],v[20:23]\n"
CV "v228,v228,v229\n"
CV "v229,v230,v231\n"
MF "v[20:23],a[20:23],v[132:135],v[20:23]\n"
LD "v197,s[48:51],s80 offen offset:1024 lds\n"
MF "v[20:23],a[80:83],v[128:131],v[20:23]\n"
CV "v230,v232,v233\n"
CV "v231,v234,v235\n"
MF "v[20:23],a[40:43],v[124:127],v[20:23]\n"
AW "a12,v228\n"
AW "a13,v229\n"
MF "v[20:23],a[0:3],v[120:123],v[20:23]\n"
"s_waitcnt lgkmcnt(13)\n"
AW "a14,v230\n"
AW "a15,v231\n"
MF "v[56:59],a[0:3],v[144:147],v[56:59]\n"
AR "v220,a52\n"
AR "v221,a53\n"
MF "v[56:59],a[40:43],v[148:151],v[56:59]\n"
LD "v198,s[48:51],s80 offen offset:2048 lds\n"
MF "v[56:59],a[80:83],v[152:155],v[56:59]\n"
AR "v222,a54\n"
AR "v223,a55\n"
MF "v[56:59],a[20:23],v[156:159],v[56:59]\n"
AR "v224,a72\n"
AR "v225,a73\n"
MF "v[56:59],a[60:63],v[160:163],v[56:59]\n"
AR "v226,a74\n"
AR "v227,a75\n"
MF "v[56:59],a[100:103],v[164:167],v[56:59]\n"
"s_waitcnt lgkmcnt(1)\n"
"ds_read_b128 a[72:75],v236 offset:57344\n"
"v_fma_f32 v220,v220,v211,v217\n"
MF "v[60:63],a[100:103],v[238:241],v[60:63]\n"
LD "v199,s[48:51],s80 offen offset:3072 lds\n"
MF "v[60:63],a[60:63],v[248:251],v[60:63]\n"
"v_fma_f32 v221,v221,v211,v217\n"
"v_fma_f32 v222,v222,v211,v217\n"
MF "v[60:63],a[20:23],v[244:247],v[60:63]\n"
"v_fma_f32 v223,v223,v211,v217\n"
"v_fma_f32 v224,v224,v211,v217\n"
MF "v[60:63],a[80:83],v[204:207],v[60:63]\n"
"s_add_u32 m0,s79,8192\n"
"v_fma_f32 v225,v225,v211,v217\n"
"v_fma_f32 v226,v226,v211,v217\n"
MF "v[60:63],a[40:43],v[172:175],v[60:63]\n"
LD "v200,s[48:51],s80 offen lds\n"
MF "v[60:63],a[0:3],v[168:171],v[60:63]\n"
"v_fma_f32 v227,v227,v211,v217\n"
CV "v220,v220,v221\n"
MF "v[68:71],a[4:7],v[168:171],v[68:71]\n"
CV "v221,v222,v223\n"
CV "v222,v224,v225\n"
MF "v[68:71],a[44:47],v[172:175],v[68:71]\n"
CV "v223,v226,v227\n"
AW "a52,v220\n"
MF "v[68:71],a[84:87],v[204:207],v[68:71]\n"
AW "a53,v221\n"
AW "a54,v222\n"
MF "v[68:71],a[24:27],v[244:247],v[68:71]\n"
LD "v201,s[48:51],s80 offen offset:1024 lds\n"
MF "v[68:71],a[64:67],v[248:251],v[68:71]\n"
AW "a55,v223\n"
AR "v228,a92\n"
MF "v[68:71],a[104:107],v[238:241],v[68:71]\n"
"s_waitcnt lgkmcnt(0)\n"
AR "v229,a93\n"
AR "v230,a94\n"
MF "v[64:67],a[104:107],v[164:167],v[64:67]\n"
"s_barrier\n"
MF "v[64:67],a[64:67],v[160:163],v[64:67]\n"
LD "v202,s[48:51],s80 offen offset:2048 lds\n"
MF "v[64:67],a[24:27],v[156:159],v[64:67]\n"
AR "v231,a95\n"
AR "v232,a112\n"
MF "v[64:67],a[84:87],v[152:155],v[64:67]\n"
AR "v233,a113\n"
AR "v234,a114\n"
MF "v[64:67],a[44:47],v[148:151],v[64:67]\n"
AR "v235,a115\n"
"v_fma_f32 v228,v228,v211,v217\n"
MF "v[64:67],a[4:7],v[144:147],v[64:67]\n"
"ds_read_b128 a[112:115],v237 offset:12288\n"
"v_fma_f32 v229,v229,v211,v217\n"
MF "v[28:31],a[4:7],v[120:123],v[28:31]\n"
LD "v203,s[48:51],s80 offen offset:3072 lds\n"
MF "v[28:31],a[44:47],v[124:127],v[28:31]\n"
"v_fma_f32 v230,v230,v211,v217\n"
"v_fma_f32 v231,v231,v211,v217\n"
MF "v[28:31],a[84:87],v[128:131],v[28:31]\n"
"v_fma_f32 v232,v232,v211,v217\n"
"v_fma_f32 v233,v233,v211,v217\n"
MF "v[28:31],a[24:27],v[132:135],v[28:31]\n"
"v_fma_f32 v234,v234,v211,v217\n"
"v_fma_f32 v235,v235,v211,v217\n"
MF "v[28:31],a[64:67],v[136:139],v[28:31]\n"
CV "v228,v228,v229\n"
CV "v229,v230,v231\n"
MF "v[28:31],a[104:107],v[140:143],v[28:31]\n"
"s_add_u32 s88,s88,s65\n"
"s_add_u32 m0,s79,49152\n"
CV "v230,v232,v233\n"
MF "v[24:27],a[104:107],v[116:119],v[24:27]\n"
LD "v192,s[48:51],s88 offen lds\n"
MF "v[24:27],a[64:67],v[112:115],v[24:27]\n"
CV "v231,v234,v235\n"
AW "a92,v228\n"
MF "v[24:27],a[24:27],v[108:111],v[24:27]\n"
AW "a93,v229\n"
AW "a94,v230\n"
MF "v[24:27],a[84:87],v[104:107],v[24:27]\n"
AW "a95,v231\n"
AR "v220,a16\n"
MF "v[24:27],a[44:47],v[100:103],v[24:27]\n"
LD "v193,s[48:51],s88 offen offset:1024 lds\n"
MF "v[24:27],a[4:7],v[96:99],v[24:27]\n"
AR "v221,a17\n"
AR "v222,a18\n"
MF "v[32:35],a[8:11],v[96:99],v[32:35]\n"
AR "v223,a19\n"
AR "v224,a36\n"
MF "v[32:35],a[48:51],v[100:103],v[32:35]\n"
AR "v225,a37\n"
AR "v226,a38\n"
MF "v[32:35],a[88:91],v[104:107],v[32:35]\n"
AR "v227,a39\n"
"v_fma_f32 v220,v220,v212,v218\n"
MF "v[32:35],a[28:31],v[108:111],v[32:35]\n"
LD "v194,s[48:51],s88 offen offset:2048 lds\n"
MF "v[32:35],a[68:71],v[112:115],v[32:35]\n"
"ds_read_b128 a[36:39],v236 offset:40960\n"
"v_fma_f32 v221,v221,v212,v218\n"
MF "v[32:35],a[108:111],v[116:119],v[32:35]\n"
"v_fma_f32 v222,v222,v212,v218\n"
"v_fma_f32 v223,v223,v212,v218\n"
MF "v[36:39],a[108:111],v[140:143],v[36:39]\n"
"v_fma_f32 v224,v224,v212,v218\n"
"v_fma_f32 v225,v225,v212,v218\n"
MF "v[36:39],a[68:71],v[136:139],v[36:39]\n"
"v_fma_f32 v226,v226,v212,v218\n"
"v_fma_f32 v227,v227,v212,v218\n"
MF "v[36:39],a[28:31],v[132:135],v[36:39]\n"
LD "v195,s[48:51],s88 offen offset:3072 lds\n"
MF "v[36:39],a[88:91],v[128:131],v[36:39]\n"
CV "v220,v220,v221\n"
CV "v221,v222,v223\n"
MF "v[36:39],a[48:51],v[124:127],v[36:39]\n"
CV "v222,v224,v225\n"
CV "v223,v226,v227\n"
MF "v[36:39],a[8:11],v[120:123],v[36:39]\n"
AW "a16,v220\n"
AW "a17,v221\n"
MF "v[72:75],a[8:11],v[144:147],v[72:75]\n"
"s_add_u32 m0,s79,53248\n"
AW "a18,v222\n"
AW "a19,v223\n"
MF "v[72:75],a[48:51],v[148:151],v[72:75]\n"
LD "v196,s[48:51],s88 offen lds\n"
MF "v[72:75],a[88:91],v[152:155],v[72:75]\n"
AR "v228,a56\n"
AR "v229,a57\n"
MF "v[72:75],a[28:31],v[156:159],v[72:75]\n"
AR "v230,a58\n"
AR "v231,a59\n"
MF "v[72:75],a[68:71],v[160:163],v[72:75]\n"
AR "v232,a76\n"
AR "v233,a77\n"
MF "v[72:75],a[108:111],v[164:167],v[72:75]\n"
AR "v234,a78\n"
AR "v235,a79\n"
MF "v[76:79],a[108:111],v[238:241],v[76:79]\n"
LD "v197,s[48:51],s88 offen offset:1024 lds\n"
MF "v[76:79],a[68:71],v[248:251],v[76:79]\n"
"ds_read_b128 a[76:79],v236 offset:61440\n"
"v_fma_f32 v228,v228,v212,v218\n"
MF "v[76:79],a[28:31],v[244:247],v[76:79]\n"
"v_fma_f32 v229,v229,v212,v218\n"
"v_fma_f32 v230,v230,v212,v218\n"
MF "v[76:79],a[88:91],v[204:207],v[76:79]\n"
"v_fma_f32 v231,v231,v212,v218\n"
"v_fma_f32 v232,v232,v212,v218\n"
MF "v[76:79],a[48:51],v[172:175],v[76:79]\n"
"v_fma_f32 v233,v233,v212,v218\n"
"v_fma_f32 v234,v234,v212,v218\n"
MF "v[76:79],a[8:11],v[168:171],v[76:79]\n"
"s_waitcnt lgkmcnt(2)\n"
LD "v198,s[48:51],s88 offen offset:2048 lds\n"
MF "v[84:87],a[12:15],v[168:171],v[84:87]\n"
"v_fma_f32 v235,v235,v212,v218\n"
CV "v228,v228,v229\n"
MF "v[84:87],a[52:55],v[172:175],v[84:87]\n"
CV "v229,v230,v231\n"
CV "v230,v232,v233\n"
MF "v[84:87],a[92:95],v[204:207],v[84:87]\n"
CV "v231,v234,v235\n"
AW "a56,v228\n"
MF "v[84:87],a[32:35],v[244:247],v[84:87]\n"
AW "a57,v229\n"
AW "a58,v230\n"
MF "v[84:87],a[72:75],v[248:251],v[84:87]\n"
LD "v199,s[48:51],s88 offen offset:3072 lds\n"
MF "v[84:87],a[112:115],v[238:241],v[84:87]\n"
AW "a59,v231\n"
AR "v220,a96\n"
MF "v[80:83],a[112:115],v[164:167],v[80:83]\n"
AR "v221,a97\n"
AR "v222,a98\n"
MF "v[80:83],a[72:75],v[160:163],v[80:83]\n"
AR "v223,a99\n"
AR "v224,a116\n"
MF "v[80:83],a[32:35],v[156:159],v[80:83]\n"
"s_add_u32 m0,s79,57344\n"
AR "v225,a117\n"
AR "v226,a118\n"
MF "v[80:83],a[92:95],v[152:155],v[80:83]\n"
LD "v200,s[48:51],s88 offen lds\n"
MF "v[80:83],a[52:55],v[148:151],v[80:83]\n"
AR "v227,a119\n"
"v_fma_f32 v220,v220,v212,v218\n"
MF "v[80:83],a[12:15],v[144:147],v[80:83]\n"
"ds_read_b128 a[116:119],v237 offset:16384\n"
"v_fma_f32 v221,v221,v212,v218\n"
MF "v[44:47],a[12:15],v[120:123],v[44:47]\n"
"v_fma_f32 v222,v222,v212,v218\n"
"v_fma_f32 v223,v223,v212,v218\n"
MF "v[44:47],a[52:55],v[124:127],v[44:47]\n"
"v_fma_f32 v224,v224,v212,v218\n"
"v_fma_f32 v225,v225,v212,v218\n"
MF "v[44:47],a[92:95],v[128:131],v[44:47]\n"
LD "v201,s[48:51],s88 offen offset:1024 lds\n"
MF "v[44:47],a[32:35],v[132:135],v[44:47]\n"
"v_fma_f32 v226,v226,v212,v218\n"
"v_fma_f32 v227,v227,v212,v218\n"
MF "v[44:47],a[72:75],v[136:139],v[44:47]\n"
CV "v220,v220,v221\n"
CV "v221,v222,v223\n"
MF "v[44:47],a[112:115],v[140:143],v[44:47]\n"
CV "v222,v224,v225\n"
CV "v223,v226,v227\n"
MF "v[40:43],a[112:115],v[116:119],v[40:43]\n"
AW "a96,v220\n"
AW "a97,v221\n"
MF "v[40:43],a[72:75],v[112:115],v[40:43]\n"
LD "v202,s[48:51],s88 offen offset:2048 lds\n"
MF "v[40:43],a[32:35],v[108:111],v[40:43]\n"
AW "a98,v222\n"
AW "a99,v223\n"
MF "v[40:43],a[92:95],v[104:107],v[40:43]\n"
AR "v8,a252\n"
AR "v11,a253\n"
MF "v[40:43],a[52:55],v[100:103],v[40:43]\n"
"v_bfe_u32 v224,v9,5,3\n"
"v_bfe_u32 v225,v9,8,2\n"
MF "v[40:43],a[12:15],v[96:99],v[40:43]\n"
"s_waitcnt lgkmcnt(0)\n"
"v_lshlrev_b32 v225,1,v225\n"
"v_and_b32 v226,16,v9\n"
MF "v[48:51],a[16:19],v[96:99],v[48:51]\n"
LD "v203,s[48:51],s88 offen offset:3072 lds\n"
MF "v[48:51],a[56:59],v[100:103],v[48:51]\n"
"v_lshlrev_b32_e64 v226,v226,s9\n"
"v_sub_u32 v224,v224,v225\n"
MF "v[48:51],a[96:99],v[104:107],v[48:51]\n"
"v_cmp_eq_u32 vcc,0,v224\n"
MF "v[48:51],a[36:39],v[108:111],v[48:51]\n"
"s_nop 0\n"
"v_cndmask_b32 v220,0,v226,vcc\n"
"v_cmp_eq_u32 vcc,1,v224\n"
MF "v[48:51],a[76:79],v[112:115],v[48:51]\n"
"s_nop 0\n"
"v_cndmask_b32 v221,0,v226,vcc\n"
AW "a248,0\n"
MF "v[48:51],a[116:119],v[116:119],v[48:51]\n"
AW "a249,0\n"
AW "a250,v220\n"
MF "v[52:55],a[116:119],v[140:143],v[52:55]\n"
AW "a251,v221\n"
AW "a252,v220\n"
MF "v[52:55],a[76:79],v[136:139],v[52:55]\n"
"s_waitcnt lgkmcnt(0)\n"
AW "a253,v221\n"
AW "a254,0\n"
MF "v[52:55],a[36:39],v[132:135],v[52:55]\n"
"s_barrier\n"
MF "v[52:55],a[96:99],v[128:131],v[52:55]\n"
"s_add_u32 s80,s88,122880\n"
TR "v[176:177],v0 offset:24576\n"
AW "a255,0\n"
MF "v[52:55],a[56:59],v[124:127],v[52:55]\n"
"s_add_u32 m0,s79,98304\n"
TR "v[178:179],v0 offset:26624\n"
"v_bfe_u32 v12,v9,4,4\n"
MF "v[52:55],a[16:19],v[120:123],v[52:55]\n"
LD "v192,s[48:51],s80 offen lds\n"
MF "v[88:91],a[16:19],v[144:147],v[88:91]\n"
"s_nop 0\n"
TR "v[180:181],v0 offset:28672\n"
"ds_write_addtid_b32 v16 offset:24576\n"
MF "v[88:91],a[56:59],v[148:151],v[88:91]\n"
"s_nop 0\n"
TR "v[182:183],v0 offset:30720\n"
"ds_write_addtid_b32 v17 offset:24832\n"
MF "v[88:91],a[96:99],v[152:155],v[88:91]\n"
LD "v193,s[48:51],s80 offen offset:1024 lds\n"
MF "v[88:91],a[36:39],v[156:159],v[88:91]\n"
"s_nop 0\n"
TR "v[184:185],v0 offset:32768\n"
"ds_write_addtid_b32 v18 offset:25088\n"
MF "v[88:91],a[76:79],v[160:163],v[88:91]\n"
"s_nop 0\n"
TR "v[186:187],v0 offset:34816\n"
"ds_write_addtid_b32 v19 offset:25344\n"
MF "v[88:91],a[116:119],v[164:167],v[88:91]\n"
"s_nop 0\n"
"ds_write_addtid_b32 v20 offset:25600\n"
"ds_write_addtid_b32 v21 offset:25856\n"
MF "v[92:95],a[116:119],v[238:241],v[92:95]\n"
LD "v194,s[48:51],s80 offen offset:2048 lds\n"
MF "v[92:95],a[76:79],v[248:251],v[92:95]\n"
"s_nop 0\n"
"ds_write_addtid_b32 v22 offset:26112\n"
"ds_write_addtid_b32 v23 offset:26368\n"
MF "v[92:95],a[36:39],v[244:247],v[92:95]\n"
"s_nop 0\n"
"ds_write_addtid_b32 v24 offset:26624\n"
"ds_write_addtid_b32 v25 offset:26880\n"
MF "v[92:95],a[96:99],v[204:207],v[92:95]\n"
"s_nop 0\n"
"ds_write_addtid_b32 v26 offset:27136\n"
"ds_write_addtid_b32 v27 offset:27392\n"
MF "v[92:95],a[56:59],v[172:175],v[92:95]\n"
"v_lshlrev_b32 v12,2,v12\n"
"v_lshrrev_b32 v13,2,v9\n"
MF "v[92:95],a[16:19],v[168:171],v[92:95]\n"
"s_waitcnt lgkmcnt(9)\n"
"v_add_u32 v13,s58,v13\n"
"v_add_u32 v4,24576,v4\n"
"v_add_u32 v5,24576,v5\n"
"v_add_u32 v6,24576,v6\n"
"v_add_u32 v7,24576,v7\n"
".Lpl05:\n"
MF "v[96:99],a[0:3],v[176:179],v[252:255]\n"
"s_nop 0\n"
TR "v[188:189],v1 offset:24576\n"
"ds_write_addtid_b32 v28 offset:27648\n"
MF "v[96:99],a[40:43],v[180:183],v[96:99]\n"
"s_nop 0\n"
TR "v[190:191],v1 offset:26624\n"
"ds_write_addtid_b32 v29 offset:27904\n"
MF "v[96:99],a[80:83],v[184:187],v[96:99]\n"
LD "v195,s[48:51],s80 offen offset:3072 lds\n"
MF "v[104:107],a[84:87],v[184:187],v[252:255]\n"
"s_add_u32 s60,s64,1\n"
TR "a[240:241],v1 offset:28672\n"
"ds_write_addtid_b32 v30 offset:28160\n"
MF "v[104:107],a[44:47],v[180:183],v[104:107]\n"
"s_add_u32 m0,s79,102400\n"
TR "a[242:243],v1 offset:30720\n"
"ds_write_addtid_b32 v31 offset:24320\n"
MF "v[104:107],a[4:7],v[176:179],v[104:107]\n"
LD "v196,s[48:51],s80 offen lds\n"
MF "v[112:115],a[8:11],v[176:179],v[252:255]\n"
"s_cmp_eq_u32 s60,8\n"
"s_cselect_b32 s60,0,s60\n"
TR "a[244:245],v1 offset:32768\n"
MF "v[112:115],a[48:51],v[180:183],v[112:115]\n"
"s_cmp_eq_u32 s60,10\n"
"s_cselect_b32 s60,s61,s60\n"
TR "a[246:247],v1 offset:34816\n"
MF "v[112:115],a[88:91],v[184:187],v[112:115]\n"
"s_sub_u32 s65,s60,s64\n"
"ds_write_addtid_b32 v32 offset:24576\n"
"ds_write_addtid_b32 v33 offset:24832\n"
MF "v[120:123],a[92:95],v[184:187],v[252:255]\n"
"s_lshl_b32 s65,s65,7\n"
"ds_write_addtid_b32 v34 offset:25088\n"
"ds_write_addtid_b32 v35 offset:25344\n"
MF "v[120:123],a[52:55],v[180:183],v[120:123]\n"
LD "v197,s[48:51],s80 offen offset:1024 lds\n"
MF "v[120:123],a[12:15],v[176:179],v[120:123]\n"
"s_mov_b32 s64,s60\n"
"ds_write_addtid_b32 v36 offset:25600\n"
"ds_write_addtid_b32 v37 offset:25856\n"
MF "v[128:131],a[16:19],v[176:179],v[252:255]\n"
"s_nop 0\n"
"ds_write_addtid_b32 v38 offset:26112\n"
"ds_write_addtid_b32 v39 offset:26368\n"
MF "v[128:131],a[56:59],v[180:183],v[128:131]\n"
"s_nop 0\n"
"ds_write_addtid_b32 v40 offset:26624\n"
"ds_write_addtid_b32 v41 offset:26880\n"
MF "v[128:131],a[96:99],v[184:187],v[128:131]\n"
"s_waitcnt lgkmcnt(10)\n"
LD "v198,s[48:51],s80 offen offset:2048 lds\n"
MF "v[132:135],a[96:99],a[244:247],v[252:255]\n"
"s_nop 0\n"
TR "v[176:177],v2 offset:24576\n"
"ds_write_addtid_b32 v42 offset:27136\n"
MF "v[132:135],a[56:59],a[240:243],v[132:135]\n"
"s_nop 0\n"
TR "v[178:179],v2 offset:26624\n"
"ds_write_addtid_b32 v43 offset:27392\n"
MF "v[132:135],a[16:19],v[188:191],v[132:135]\n"
"s_nop 0\n"
"ds_write_addtid_b32 v44 offset:27648\n"
"ds_write_addtid_b32 v45 offset:27904\n"
MF "v[124:127],a[12:15],v[188:191],v[252:255]\n"
"s_nop 0\n"
TR "v[180:181],v2 offset:28672\n"
"ds_write_addtid_b32 v46 offset:28160\n"
MF "v[124:127],a[52:55],a[240:243],v[124:127]\n"
"s_nop 0\n"
TR "v[182:183],v2 offset:30720\n"
"ds_write_addtid_b32 v47 offset:28416\n"
MF "v[124:127],a[92:95],a[244:247],v[124:127]\n"
LD "v199,s[48:51],s80 offen offset:3072 lds\n"
MF "v[116:119],a[88:91],a[244:247],v[252:255]\n"
"s_nop 0\n"
TR "v[184:185],v2 offset:32768\n"
"ds_write_addtid_b32 v48 offset:28672\n"
MF "v[116:119],a[48:51],a[240:243],v[116:119]\n"
"s_add_u32 m0,s79,106496\n"
TR "v[186:187],v2 offset:34816\n"
"ds_write_addtid_b32 v49 offset:24832\n"
MF "v[116:119],a[8:11],v[188:191],v[116:119]\n"
LD "v200,s[48:51],s80 offen lds\n"
MF "v[108:111],a[4:7],v[188:191],v[252:255]\n"
"s_nop 0\n"
"ds_write_addtid_b32 v50 offset:25088\n"
"ds_write_addtid_b32 v51 offset:25344\n"
MF "v[108:111],a[44:47],a[240:243],v[108:111]\n"
"s_nop 0\n"
"ds_write_addtid_b32 v52 offset:25600\n"
"ds_write_addtid_b32 v53 offset:25856\n"
MF "v[108:111],a[84:87],a[244:247],v[108:111]\n"
"s_nop 0\n"
"ds_write_addtid_b32 v54 offset:26112\n"
"ds_write_addtid_b32 v55 offset:26368\n"
MF "v[100:103],a[80:83],a[244:247],v[252:255]\n"
MF "v[100:103],a[40:43],a[240:243],v[100:103]\n"
LD "v201,s[48:51],s80 offen offset:1024 lds\n"
MF "v[100:103],a[0:3],v[188:191],v[100:103]\n"
"s_waitcnt lgkmcnt(7)\n"
MF "v[136:139],a[0:3],v[176:179],v[252:255]\n"
TR "v[188:189],v3 offset:24576\n"
MF "v[136:139],a[40:43],v[180:183],v[136:139]\n"
TR "v[190:191],v3 offset:26624\n"
MF "v[136:139],a[80:83],v[184:187],v[136:139]\n"
LD "v202,s[48:51],s80 offen offset:2048 lds\n"
MF "v[144:147],a[84:87],v[184:187],v[252:255]\n"
TR "a[240:241],v3 offset:28672\n"
MF "v[144:147],a[44:47],v[180:183],v[144:147]\n"
TR "a[242:243],v3 offset:30720\n"
MF "v[144:147],a[4:7],v[176:179],v[144:147]\n"
LD "v203,s[48:51],s80 offen offset:3072 lds\n"
MF "v[152:155],a[8:11],v[176:179],v[252:255]\n"
TR "a[244:245],v3 offset:32768\n"
MF "v[152:155],a[48:51],v[180:183],v[152:155]\n"
TR "a[246:247],v3 offset:34816\n"
MF "v[152:155],a[88:91],v[184:187],v[152:155]\n"
MF "v[160:163],a[92:95],v[184:187],v[252:255]\n"
MF "v[160:163],a[52:55],v[180:183],v[160:163]\n"
MF "v[160:163],a[12:15],v[176:179],v[160:163]\n"
MF "v[168:171],a[16:19],v[176:179],v[252:255]\n"
MF "v[168:171],a[56:59],v[180:183],v[168:171]\n"
"s_waitcnt vmcnt(24) lgkmcnt(0)\n"
MF "v[168:171],a[96:99],v[184:187],v[168:171]\n"
"s_barrier\n"
MF "v[172:175],a[96:99],a[244:247],v[252:255]\n"
"s_add_u32 s89,s89,s65\n"
TR "v[176:177],v0\n"
MF "v[172:175],a[56:59],a[240:243],v[172:175]\n"
"s_add_u32 m0,s79,24576\n"
TR "v[178:179],v0 offset:2048\n"
MF "v[172:175],a[16:19],v[188:191],v[172:175]\n"
LD "v192,s[48:51],s89 offen lds\n"
MF "v[164:167],a[12:15],v[188:191],v[252:255]\n"
TR "v[180:181],v0 offset:4096\n"
MF "v[164:167],a[52:55],a[240:243],v[164:167]\n"
TR "v[182:183],v0 offset:6144\n"
MF "v[164:167],a[92:95],a[244:247],v[164:167]\n"
LD "v193,s[48:51],s89 offen offset:1024 lds\n"
MF "v[156:159],a[88:91],a[244:247],v[252:255]\n"
TR "v[184:185],v0 offset:8192\n"
MF "v[156:159],a[48:51],a[240:243],v[156:159]\n"
"s_nop 0\n"
TR "v[186:187],v0 offset:10240\n"
"ds_write_addtid_b32 v56 offset:49152\n"
MF "v[156:159],a[8:11],v[188:191],v[156:159]\n"
"s_nop 0\n"
"ds_write_addtid_b32 v57 offset:49408\n"
"ds_write_addtid_b32 v58 offset:49664\n"
MF "v[148:151],a[4:7],v[188:191],v[252:255]\n"
"s_nop 0\n"
"ds_write_addtid_b32 v59 offset:49920\n"
"ds_write_addtid_b32 v60 offset:50176\n"
MF "v[148:151],a[44:47],a[240:243],v[148:151]\n"
LD "v194,s[48:51],s89 offen offset:2048 lds\n"
MF "v[148:151],a[84:87],a[244:247],v[148:151]\n"
"s_nop 0\n"
"ds_write_addtid_b32 v61 offset:50432\n"
"ds_write_addtid_b32 v62 offset:50688\n"
MF "v[140:143],a[80:83],a[244:247],v[252:255]\n"
"s_nop 0\n"
"ds_write_addtid_b32 v63 offset:50944\n"
"ds_write_addtid_b32 v64 offset:51200\n"
MF "v[140:143],a[40:43],a[240:243],v[140:143]\n"
"s_nop 0\n"
"ds_write_addtid_b32 v65 offset:51456\n"
"ds_write_addtid_b32 v66 offset:51712\n"
MF "v[140:143],a[0:3],v[188:191],v[140:143]\n"
"s_waitcnt lgkmcnt(11)\n"
LD "v195,s[48:51],s89 offen offset:3072 lds\n"
MF "v[96:99],a[120:123],v[176:179],v[96:99]\n"
"s_nop 0\n"
TR "v[188:189],v1\n"
"ds_write_addtid_b32 v67 offset:51968\n"
MF "v[96:99],a[160:163],v[180:183],v[96:99]\n"
"s_nop 0\n"
TR "v[190:191],v1 offset:2048\n"
"ds_write_addtid_b32 v68 offset:52224\n"
MF "v[96:99],a[200:203],v[184:187],v[96:99]\n"
"s_nop 0\n"
"ds_write_addtid_b32 v69 offset:52480\n"
"ds_write_addtid_b32 v70 offset:52736\n"
MF "v[104:107],a[204:207],v[184:187],v[104:107]\n"
"s_nop 0\n"
TR "a[240:241],v1 offset:4096\n"
"ds_write_addtid_b32 v71 offset:52992\n"
MF "v[104:107],a[164:167],v[180:183],v[104:107]\n"
"s_add_u32 m0,s79,28672\n"
TR "a[242:243],v1 offset:6144\n"
"ds_write_addtid_b32 v72 offset:49152\n"
MF "v[104:107],a[124:127],v[176:179],v[104:107]\n"
".Lpg26h:\n"
"s_cbranch_execz .Lpg30h\n"
LD "v196,s[48:51],s89 offen lds\n"
MF "v[112:115],a[128:131],v[176:179],v[112:115]\n"
"s_nop 0\n"
TR "a[244:245],v1 offset:8192\n"
"ds_write_addtid_b32 v73 offset:49408\n"
MF "v[112:115],a[168:171],v[180:183],v[112:115]\n"
"s_nop 0\n"
TR "a[246:247],v1 offset:10240\n"
"ds_write_addtid_b32 v74 offset:49664\n"
MF "v[112:115],a[208:211],v[184:187],v[112:115]\n"
LD "v197,s[48:51],s89 offen offset:1024 lds\n"
MF "v[120:123],a[212:215],v[184:187],v[120:123]\n"
"s_nop 0\n"
"ds_write_addtid_b32 v75 offset:49920\n"
"ds_write_addtid_b32 v76 offset:50176\n"
MF "v[120:123],a[172:175],v[180:183],v[120:123]\n"
"s_nop 0\n"
"ds_write_addtid_b32 v77 offset:50432\n"
"ds_write_addtid_b32 v78 offset:50688\n"
MF "v[120:123],a[132:135],v[176:179],v[120:123]\n"
"s_nop 0\n"
"ds_write_addtid_b32 v79 offset:50944\n"
"ds_write_addtid_b32 v80 offset:51200\n"
MF "v[128:131],a[136:139],v[176:179],v[128:131]\n"
"s_nop 0\n"
"ds_write_addtid_b32 v81 offset:51456\n"
"ds_write_addtid_b32 v82 offset:51712\n"
MF "v[128:131],a[176:179],v[180:183],v[128:131]\n"
LD "v198,s[48:51],s89 offen offset:2048 lds\n"
MF "v[128:131],a[216:219],v[184:187],v[128:131]\n"
"s_waitcnt lgkmcnt(9)\n"
"ds_write_addtid_b32 v83 offset:51968\n"
"ds_write_addtid_b32 v84 offset:52224\n"
MF "v[132:135],a[216:219],a[244:247],v[132:135]\n"
"s_nop 0\n"
TR "v[176:177],v2\n"
"ds_write_addtid_b32 v85 offset:52480\n"
MF "v[132:135],a[176:179],a[240:243],v[132:135]\n"
"s_nop 0\n"
TR "v[178:179],v2 offset:2048\n"
"ds_write_addtid_b32 v86 offset:52736\n"
MF "v[132:135],a[136:139],v[188:191],v[132:135]\n"
LD "v199,s[48:51],s89 offen offset:3072 lds\n"
MF "v[124:127],a[132:135],v[188:191],v[124:127]\n"
"s_nop 0\n"
TR "v[180:181],v2 offset:4096\n"
"ds_write_addtid_b32 v87 offset:52992\n"
MF "v[124:127],a[172:175],a[240:243],v[124:127]\n"
"s_nop 0\n"
TR "v[182:183],v2 offset:6144\n"
"ds_write_addtid_b32 v88 offset:53248\n"
MF "v[124:127],a[212:215],a[244:247],v[124:127]\n"
"s_nop 0\n"
"ds_write_addtid_b32 v89 offset:53504\n"
"ds_write_addtid_b32 v90 offset:53760\n"
MF "v[116:119],a[208:211],a[244:247],v[116:119]\n"
"s_nop 0\n"
TR "v[184:185],v2 offset:8192\n"
"ds_write_addtid_b32 v91 offset:54016\n"
MF "v[116:119],a[168:171],a[240:243],v[116:119]\n"
"s_add_u32 m0,s79,32768\n"
TR "v[186:187],v2 offset:10240\n"
"ds_write_addtid_b32 v92 offset:50176\n"
MF "v[116:119],a[128:131],v[188:191],v[116:119]\n"
LD "v200,s[48:51],s89 offen lds\n"
MF "v[108:111],a[124:127],v[188:191],v[108:111]\n"
"s_nop 0\n"
"ds_write_addtid_b32 v93 offset:50432\n"
"ds_write_addtid_b32 v94 offset:50688\n"
MF "v[108:111],a[164:167],a[240:243],v[108:111]\n"
"ds_write_addtid_b32 v95 offset:50944\n"
MF "v[108:111],a[204:207],a[244:247],v[108:111]\n"
MF "v[100:103],a[200:203],a[244:247],v[100:103]\n"
LD "v201,s[48:51],s89 offen offset:1024 lds\n"
MF "v[100:103],a[160:163],a[240:243],v[100:103]\n"
MF "v[100:103],a[120:123],v[188:191],v[100:103]\n"
"s_waitcnt lgkmcnt(4)\n"
MF "v[136:139],a[120:123],v[176:179],v[136:139]\n"
TR "v[188:189],v3\n"
MF "v[136:139],a[160:163],v[180:183],v[136:139]\n"
TR "v[190:191],v3 offset:2048\n"
MF "v[136:139],a[200:203],v[184:187],v[136:139]\n"
LD "v202,s[48:51],s89 offen offset:2048 lds\n"
MF "v[144:147],a[204:207],v[184:187],v[144:147]\n"
TR "a[240:241],v3 offset:4096\n"
MF "v[144:147],a[164:167],v[180:183],v[144:147]\n"
TR "a[242:243],v3 offset:6144\n"
MF "v[144:147],a[124:127],v[176:179],v[144:147]\n"
LD "v203,s[48:51],s89 offen offset:3072 lds\n"
MF "v[152:155],a[128:131],v[176:179],v[152:155]\n"
TR "a[244:245],v3 offset:8192\n"
MF "v[152:155],a[168:171],v[180:183],v[152:155]\n"
TR "a[246:247],v3 offset:10240\n"
MF "v[152:155],a[208:211],v[184:187],v[152:155]\n"
MF "v[160:163],a[212:215],v[184:187],v[160:163]\n"
MF "v[160:163],a[172:175],v[180:183],v[160:163]\n"
MF "v[160:163],a[132:135],v[176:179],v[160:163]\n"
MF "v[168:171],a[136:139],v[176:179],v[168:171]\n"
MF "v[168:171],a[176:179],v[180:183],v[168:171]\n"
"s_waitcnt vmcnt(24) lgkmcnt(0)\n"
MF "v[168:171],a[216:219],v[184:187],v[168:171]\n"
"s_barrier\n"
MF "v[172:175],a[216:219],a[244:247],v[172:175]\n"
"s_add_u32 s80,s89,122880\n"
TR "v[176:177],v0 offset:49152\n"
MF "v[172:175],a[176:179],a[240:243],v[172:175]\n"
"s_add_u32 m0,s79,0\n"
TR "v[178:179],v0 offset:51200\n"
MF "v[172:175],a[136:139],v[188:191],v[172:175]\n"
LD "v192,s[48:51],s80 offen lds\n"
MF "v[164:167],a[132:135],v[188:191],v[164:167]\n"
TR "v[180:181],v0 offset:53248\n"
MF "v[164:167],a[172:175],a[240:243],v[164:167]\n"
TR "v[182:183],v0 offset:55296\n"
MF "v[164:167],a[212:215],a[244:247],v[164:167]\n"
LD "v193,s[48:51],s80 offen offset:1024 lds\n"
MF "v[156:159],a[208:211],a[244:247],v[156:159]\n"
TR "v[184:185],v0 offset:57344\n"
MF "v[156:159],a[168:171],a[240:243],v[156:159]\n"
TR "v[186:187],v0 offset:59392\n"
MF "v[156:159],a[128:131],v[188:191],v[156:159]\n"
MF "v[148:151],a[124:127],v[188:191],v[148:151]\n"
MF "v[148:151],a[164:167],a[240:243],v[148:151]\n"
LD "v194,s[48:51],s80 offen offset:2048 lds\n"
MF "v[148:151],a[204:207],a[244:247],v[148:151]\n"
MF "v[140:143],a[200:203],a[244:247],v[140:143]\n"
MF "v[140:143],a[160:163],a[240:243],v[140:143]\n"
MF "v[140:143],a[120:123],v[188:191],v[140:143]\n"
"s_waitcnt lgkmcnt(0)\n"
LD "v195,s[48:51],s80 offen offset:3072 lds\n"
MF "v[96:99],a[20:23],v[176:179],v[96:99]\n"
TR "v[188:189],v1 offset:49152\n"
MF "v[96:99],a[60:63],v[180:183],v[96:99]\n"
TR "v[190:191],v1 offset:51200\n"
MF "v[96:99],a[100:103],v[184:187],v[96:99]\n"
MF "v[104:107],a[104:107],v[184:187],v[104:107]\n"
TR "a[240:241],v1 offset:53248\n"
MF "v[104:107],a[64:67],v[180:183],v[104:107]\n"
"s_add_u32 m0,s79,4096\n"
TR "a[242:243],v1 offset:55296\n"
MF "v[104:107],a[24:27],v[176:179],v[104:107]\n"
LD "v196,s[48:51],s80 offen lds\n"
MF "v[112:115],a[28:31],v[176:179],v[112:115]\n"
TR "a[244:245],v1 offset:57344\n"
MF "v[112:115],a[68:71],v[180:183],v[112:115]\n"
TR "a[246:247],v1 offset:59392\n"
MF "v[112:115],a[108:111],v[184:187],v[112:115]\n"
LD "v197,s[48:51],s80 offen offset:1024 lds\n"
MF "v[120:123],a[112:115],v[184:187],v[120:123]\n"
MF "v[120:123],a[72:75],v[180:183],v[120:123]\n"
MF "v[120:123],a[32:35],v[176:179],v[120:123]\n"
MF "v[128:131],a[36:39],v[176:179],v[128:131]\n"
MF "v[128:131],a[76:79],v[180:183],v[128:131]\n"
LD "v198,s[48:51],s80 offen offset:2048 lds\n"
MF "v[128:131],a[116:119],v[184:187],v[128:131]\n"
"s_waitcnt lgkmcnt(0)\n"
MF "v[132:135],a[116:119],a[244:247],v[132:135]\n"
TR "v[176:177],v2 offset:49152\n"
MF "v[132:135],a[76:79],a[240:243],v[132:135]\n"
TR "v[178:179],v2 offset:51200\n"
MF "v[132:135],a[36:39],v[188:191],v[132:135]\n"
LD "v199,s[48:51],s80 offen offset:3072 lds\n"
MF "v[124:127],a[32:35],v[188:191],v[124:127]\n"
TR "v[180:181],v2 offset:53248\n"
MF "v[124:127],a[72:75],a[240:243],v[124:127]\n"
TR "v[182:183],v2 offset:55296\n"
MF "v[124:127],a[112:115],a[244:247],v[124:127]\n"
MF "v[116:119],a[108:111],a[244:247],v[116:119]\n"
TR "v[184:185],v2 offset:57344\n"
MF "v[116:119],a[68:71],a[240:243],v[116:119]\n"
"s_add_u32 m0,s79,8192\n"
TR "v[186:187],v2 offset:59392\n"
MF "v[116:119],a[28:31],v[188:191],v[116:119]\n"
LD "v200,s[48:51],s80 offen lds\n"
MF "v[108:111],a[24:27],v[188:191],v[108:111]\n"
MF "v[108:111],a[64:67],a[240:243],v[108:111]\n"
MF "v[108:111],a[104:107],a[244:247],v[108:111]\n"
MF "v[100:103],a[100:103],a[244:247],v[100:103]\n"
LD "v201,s[48:51],s80 offen offset:1024 lds\n"
MF "v[100:103],a[60:63],a[240:243],v[100:103]\n"
MF "v[100:103],a[20:23],v[188:191],v[100:103]\n"
"s_waitcnt lgkmcnt(0)\n"
MF "v[136:139],a[20:23],v[176:179],v[136:139]\n"
TR "v[188:189],v3 offset:49152\n"
MF "v[136:139],a[60:63],v[180:183],v[136:139]\n"
TR "v[190:191],v3 offset:51200\n"
MF "v[136:139],a[100:103],v[184:187],v[136:139]\n"
LD "v202,s[48:51],s80 offen offset:2048 lds\n"
MF "v[144:147],a[104:107],v[184:187],v[144:147]\n"
TR "a[240:241],v3 offset:53248\n"
MF "v[144:147],a[64:67],v[180:183],v[144:147]\n"
TR "a[242:243],v3 offset:55296\n"
MF "v[144:147],a[24:27],v[176:179],v[144:147]\n"
LD "v203,s[48:51],s80 offen offset:3072 lds\n"
MF "v[152:155],a[28:31],v[176:179],v[152:155]\n"
TR "a[244:245],v3 offset:57344\n"
MF "v[152:155],a[68:71],v[180:183],v[152:155]\n"
TR "a[246:247],v3 offset:59392\n"
MF "v[152:155],a[108:111],v[184:187],v[152:155]\n"
MF "v[160:163],a[112:115],v[184:187],v[160:163]\n"
MF "v[160:163],a[72:75],v[180:183],v[160:163]\n"
MF "v[160:163],a[32:35],v[176:179],v[160:163]\n"
MF "v[168:171],a[36:39],v[176:179],v[168:171]\n"
MF "v[168:171],a[76:79],v[180:183],v[168:171]\n"
"s_waitcnt vmcnt(24) lgkmcnt(0)\n"
MF "v[168:171],a[116:119],v[184:187],v[168:171]\n"
"s_barrier\n"
MF "v[172:175],a[116:119],a[244:247],v[172:175]\n"
"s_add_u32 s88,s88,s65\n"
TR "v[176:177],v4\n"
MF "v[172:175],a[76:79],a[240:243],v[172:175]\n"
"s_add_u32 m0,s79,49152\n"
TR "v[178:179],v4 offset:2048\n"
MF "v[172:175],a[36:39],v[188:191],v[172:175]\n"
LD "v192,s[48:51],s88 offen lds\n"
MF "v[164:167],a[32:35],v[188:191],v[164:167]\n"
TR "v[180:181],v4 offset:4096\n"
MF "v[164:167],a[72:75],a[240:243],v[164:167]\n"
TR "v[182:183],v4 offset:6144\n"
MF "v[164:167],a[112:115],a[244:247],v[164:167]\n"
LD "v193,s[48:51],s88 offen offset:1024 lds\n"
MF "v[156:159],a[108:111],a[244:247],v[156:159]\n"
TR "v[184:185],v4 offset:8192\n"
MF "v[156:159],a[68:71],a[240:243],v[156:159]\n"
TR "v[186:187],v4 offset:10240\n"
MF "v[156:159],a[28:31],v[188:191],v[156:159]\n"
MF "v[148:151],a[24:27],v[188:191],v[148:151]\n"
MF "v[148:151],a[64:67],a[240:243],v[148:151]\n"
LD "v194,s[48:51],s88 offen offset:2048 lds\n"
MF "v[148:151],a[104:107],a[244:247],v[148:151]\n"
MF "v[140:143],a[100:103],a[244:247],v[140:143]\n"
MF "v[140:143],a[60:63],a[240:243],v[140:143]\n"
MF "v[140:143],a[20:23],v[188:191],v[140:143]\n"
"s_waitcnt lgkmcnt(0)\n"
LD "v195,s[48:51],s88 offen offset:3072 lds\n"
MF "v[96:99],a[140:143],v[176:179],v[96:99]\n"
TR "v[188:189],v5\n"
MF "v[96:99],a[180:183],v[180:183],v[96:99]\n"
TR "v[190:191],v5 offset:2048\n"
MF "v[96:99],a[220:223],v[184:187],v[96:99]\n"
MF "v[104:107],a[224:227],v[184:187],v[104:107]\n"
TR "a[240:241],v5 offset:4096\n"
MF "v[104:107],a[184:187],v[180:183],v[104:107]\n"
"s_add_u32 m0,s79,53248\n"
TR "a[242:243],v5 offset:6144\n"
MF "v[104:107],a[144:147],v[176:179],v[104:107]\n"
LD "v196,s[48:51],s88 offen lds\n"
MF "v[112:115],a[148:151],v[176:179],v[112:115]\n"
TR "a[244:245],v5 offset:8192\n"
MF "v[112:115],a[188:191],v[180:183],v[112:115]\n"
TR "a[246:247],v5 offset:10240\n"
MF "v[112:115],a[228:231],v[184:187],v[112:115]\n"
LD "v197,s[48:51],s88 offen offset:1024 lds\n"
MF "v[120:123],a[232:235],v[184:187],v[120:123]\n"
MF "v[120:123],a[192:195],v[180:183],v[120:123]\n"
MF "v[120:123],a[152:155],v[176:179],v[120:123]\n"
MF "v[128:131],a[156:159],v[176:179],v[128:131]\n"
MF "v[128:131],a[196:199],v[180:183],v[128:131]\n"
LD "v198,s[48:51],s88 offen offset:2048 lds\n"
MF "v[128:131],a[236:239],v[184:187],v[128:131]\n"
"s_waitcnt lgkmcnt(0)\n"
MF "v[132:135],a[236:239],a[244:247],v[132:135]\n"
TR "v[176:177],v6\n"
MF "v[132:135],a[196:199],a[240:243],v[132:135]\n"
TR "v[178:179],v6 offset:2048\n"
MF "v[132:135],a[156:159],v[188:191],v[132:135]\n"
LD "v199,s[48:51],s88 offen offset:3072 lds\n"
MF "v[124:127],a[152:155],v[188:191],v[124:127]\n"
TR "v[180:181],v6 offset:4096\n"
MF "v[124:127],a[192:195],a[240:243],v[124:127]\n"
TR "v[182:183],v6 offset:6144\n"
MF "v[124:127],a[232:235],a[244:247],v[124:127]\n"
MF "v[116:119],a[228:231],a[244:247],v[116:119]\n"
TR "v[184:185],v6 offset:8192\n"
MF "v[116:119],a[188:191],a[240:243],v[116:119]\n"
"s_add_u32 m0,s79,57344\n"
TR "v[186:187],v6 offset:10240\n"
MF "v[116:119],a[148:151],v[188:191],v[116:119]\n"
LD "v200,s[48:51],s88 offen lds\n"
MF "v[108:111],a[144:147],v[188:191],v[108:111]\n"
MF "v[108:111],a[184:187],a[240:243],v[108:111]\n"
MF "v[108:111],a[224:227],a[244:247],v[108:111]\n"
MF "v[100:103],a[220:223],a[244:247],v[100:103]\n"
LD "v201,s[48:51],s88 offen offset:1024 lds\n"
MF "v[100:103],a[180:183],a[240:243],v[100:103]\n"
MF "v[100:103],a[140:143],v[188:191],v[100:103]\n"
"s_waitcnt lgkmcnt(0)\n"
MF "v[136:139],a[140:143],v[176:179],v[136:139]\n"
TR "v[188:189],v7\n"
MF "v[136:139],a[180:183],v[180:183],v[136:139]\n"
TR "v[190:191],v7 offset:2048\n"
MF "v[136:139],a[220:223],v[184:187],v[136:139]\n"
LD "v202,s[48:51],s88 offen offset:2048 lds\n"
MF "v[144:147],a[224:227],v[184:187],v[144:147]\n"
TR "a[240:241],v7 offset:4096\n"
MF "v[144:147],a[184:187],v[180:183],v[144:147]\n"
TR "a[242:243],v7 offset:6144\n"
MF "v[144:147],a[144:147],v[176:179],v[144:147]\n"
LD "v203,s[48:51],s88 offen offset:3072 lds\n"
MF "v[152:155],a[148:151],v[176:179],v[152:155]\n"
TR "a[244:245],v7 offset:8192\n"
MF "v[152:155],a[188:191],v[180:183],v[152:155]\n"
TR "a[246:247],v7 offset:10240\n"
MF "v[152:155],a[228:231],v[184:187],v[152:155]\n"
MF "v[160:163],a[232:235],v[184:187],v[160:163]\n"
MF "v[160:163],a[192:195],v[180:183],v[160:163]\n"
MF "v[160:163],a[152:155],v[176:179],v[160:163]\n"
MF "v[168:171],a[156:159],v[176:179],v[168:171]\n"
MF "v[168:171],a[196:199],v[180:183],v[168:171]\n"
"s_waitcnt vmcnt(24) lgkmcnt(0)\n"
MF "v[168:171],a[236:239],v[184:187],v[168:171]\n"
"s_barrier\n"
MF "v[172:175],a[236:239],a[244:247],v[172:175]\n"
"s_add_u32 s80,s88,122880\n"
TR "v[176:177],v0 offset:24576\n"
MF "v[172:175],a[196:199],a[240:243],v[172:175]\n"
"s_add_u32 m0,s79,98304\n"
"s_add_u32 s85,s85,1\n"
TR "v[178:179],v0 offset:26624\n"
MF "v[172:175],a[156:159],v[188:191],v[172:175]\n"
LD "v192,s[48:51],s80 offen lds\n"
MF "v[164:167],a[152:155],v[188:191],v[164:167]\n"
"s_nop 0\n"
TR "v[180:181],v0 offset:28672\n"
"ds_write_addtid_b32 v96 offset:24576\n"
MF "v[164:167],a[192:195],a[240:243],v[164:167]\n"
"s_nop 0\n"
TR "v[182:183],v0 offset:30720\n"
"ds_write_addtid_b32 v97 offset:24832\n"
MF "v[164:167],a[232:235],a[244:247],v[164:167]\n"
LD "v193,s[48:51],s80 offen offset:1024 lds\n"
MF "v[156:159],a[228:231],a[244:247],v[156:159]\n"
"s_nop 0\n"
TR "v[184:185],v0 offset:32768\n"
"ds_write_addtid_b32 v98 offset:25088\n"
MF "v[156:159],a[188:191],a[240:243],v[156:159]\n"
"s_nop 0\n"
TR "v[186:187],v0 offset:34816\n"
"ds_write_addtid_b32 v99 offset:25344\n"
MF "v[156:159],a[148:151],v[188:191],v[156:159]\n"
"s_nop 0\n"
"ds_write_addtid_b32 v100 offset:25600\n"
"ds_write_addtid_b32 v101 offset:25856\n"
MF "v[148:151],a[144:147],v[188:191],v[148:151]\n"
LD "v194,s[48:51],s80 offen offset:2048 lds\n"
MF "v[148:151],a[184:187],a[240:243],v[148:151]\n"
"s_nop 0\n"
"ds_write_addtid_b32 v102 offset:26112\n"
"ds_write_addtid_b32 v103 offset:26368\n"
MF "v[148:151],a[224:227],a[244:247],v[148:151]\n"
"s_nop 0\n"
"ds_write_addtid_b32 v104 offset:26624\n"
"ds_write_addtid_b32 v105 offset:26880\n"
MF "v[140:143],a[220:223],a[244:247],v[140:143]\n"
"s_nop 0\n"
"ds_write_addtid_b32 v106 offset:27136\n"
"ds_write_addtid_b32 v107 offset:27392\n"
MF "v[140:143],a[180:183],a[240:243],v[140:143]\n"
MF "v[140:143],a[140:143],v[188:191],v[140:143]\n"
"s_waitcnt lgkmcnt(9)\n"
MF "v[16:19],a[0:3],v[176:179],v[252:255]\n"
"s_nop 0\n"
TR "v[188:189],v1 offset:24576\n"
"ds_write_addtid_b32 v108 offset:27648\n"
MF "v[16:19],a[40:43],v[180:183],v[16:19]\n"
"s_nop 0\n"
TR "v[190:191],v1 offset:26624\n"
"ds_write_addtid_b32 v109 offset:27904\n"
MF "v[16:19],a[80:83],v[184:187],v[16:19]\n"
LD "v195,s[48:51],s80 offen offset:3072 lds\n"
MF "v[24:27],a[84:87],v[184:187],v[252:255]\n"
"s_add_u32 s60,s64,1\n"
TR "a[240:241],v1 offset:28672\n"
"ds_write_addtid_b32 v110 offset:28160\n"
MF "v[24:27],a[44:47],v[180:183],v[24:27]\n"
"s_add_u32 m0,s79,102400\n"
TR "a[242:243],v1 offset:30720\n"
"ds_write_addtid_b32 v111 offset:24320\n"
MF "v[24:27],a[4:7],v[176:179],v[24:27]\n"
LD "v196,s[48:51],s80 offen lds\n"
MF "v[32:35],a[8:11],v[176:179],v[252:255]\n"
"s_cmp_eq_u32 s60,8\n"
"s_cselect_b32 s60,0,s60\n"
TR "a[244:245],v1 offset:32768\n"
MF "v[32:35],a[48:51],v[180:183],v[32:35]\n"
"s_cmp_eq_u32 s60,10\n"
"s_cselect_b32 s60,s61,s60\n"
TR "a[246:247],v1 offset:34816\n"
MF "v[32:35],a[88:91],v[184:187],v[32:35]\n"
"s_sub_u32 s65,s60,s64\n"
"ds_write_addtid_b32 v112 offset:24576\n"
"ds_write_addtid_b32 v113 offset:24832\n"
MF "v[40:43],a[92:95],v[184:187],v[252:255]\n"
"s_lshl_b32 s65,s65,7\n"
"ds_write_addtid_b32 v114 offset:25088\n"
"ds_write_addtid_b32 v115 offset:25344\n"
MF "v[40:43],a[52:55],v[180:183],v[40:43]\n"
LD "v197,s[48:51],s80 offen offset:1024 lds\n"
MF "v[40:43],a[12:15],v[176:179],v[40:43]\n"
"s_mov_b32 s64,s60\n"
"ds_write_addtid_b32 v116 offset:25600\n"
"ds_write_addtid_b32 v117 offset:25856\n"
MF "v[48:51],a[16:19],v[176:179],v[252:255]\n"
"s_nop 0\n"
"ds_write_addtid_b32 v118 offset:26112\n"
"ds_write_addtid_b32 v119 offset:26368\n"
MF "v[48:51],a[56:59],v[180:183],v[48:51]\n"
"s_nop 0\n"
"ds_write_addtid_b32 v120 offset:26624\n"
"ds_write_addtid_b32 v121 offset:26880\n"
MF "v[48:51],a[96:99],v[184:187],v[48:51]\n"
"s_waitcnt lgkmcnt(10)\n"
LD "v198,s[48:51],s80 offen offset:2048 lds\n"
MF "v[52:55],a[96:99],a[244:247],v[252:255]\n"
"s_nop 0\n"
TR "v[176:177],v2 offset:24576\n"
"ds_write_addtid_b32 v122 offset:27136\n"
MF "v[52:55],a[56:59],a[240:243],v[52:55]\n"
"s_nop 0\n"
TR "v[178:179],v2 offset:26624\n"
"ds_write_addtid_b32 v123 offset:27392\n"
MF "v[52:55],a[16:19],v[188:191],v[52:55]\n"
"s_nop 0\n"
"ds_write_addtid_b32 v124 offset:27648\n"
"ds_write_addtid_b32 v125 offset:27904\n"
MF "v[44:47],a[12:15],v[188:191],v[252:255]\n"
"s_nop 0\n"
TR "v[180:181],v2 offset:28672\n"
"ds_write_addtid_b32 v126 offset:28160\n"
MF "v[44:47],a[52:55],a[240:243],v[44:47]\n"
"s_nop 0\n"
TR "v[182:183],v2 offset:30720\n"
"ds_write_addtid_b32 v127 offset:28416\n"
MF "v[44:47],a[92:95],a[244:247],v[44:47]\n"
LD "v199,s[48:51],s80 offen offset:3072 lds\n"
MF "v[36:39],a[88:91],a[244:247],v[252:255]\n"
"s_nop 0\n"
TR "v[184:185],v2 offset:32768\n"
"ds_write_addtid_b32 v128 offset:28672\n"
MF "v[36:39],a[48:51],a[240:243],v[36:39]\n"
"s_add_u32 m0,s79,106496\n"
TR "v[186:187],v2 offset:34816\n"
"ds_write_addtid_b32 v129 offset:24832\n"
MF "v[36:39],a[8:11],v[188:191],v[36:39]\n"
LD "v200,s[48:51],s80 offen lds\n"
MF "v[28:31],a[4:7],v[188:191],v[252:255]\n"
"s_nop 0\n"
"ds_write_addtid_b32 v130 offset:25088\n"
"ds_write_addtid_b32 v131 offset:25344\n"
MF "v[28:31],a[44:47],a[240:243],v[28:31]\n"
"s_nop 0\n"
"ds_write_addtid_b32 v132 offset:25600\n"
"ds_write_addtid_b32 v133 offset:25856\n"
MF "v[28:31],a[84:87],a[244:247],v[28:31]\n"
"s_nop 0\n"
"ds_write_addtid_b32 v134 offset:26112\n"
"ds_write_addtid_b32 v135 offset:26368\n"
MF "v[20:23],a[80:83],a[244:247],v[252:255]\n"
MF "v[20:23],a[40:43],a[240:243],v[20:23]\n"
LD "v201,s[48:51],s80 offen offset:1024 lds\n"
MF "v[20:23],a[0:3],v[188:191],v[20:23]\n"
"s_waitcnt lgkmcnt(7)\n"
MF "v[56:59],a[0:3],v[176:179],v[252:255]\n"
TR "v[188:189],v3 offset:24576\n"
MF "v[56:59],a[40:43],v[180:183],v[56:59]\n"
TR "v[190:191],v3 offset:26624\n"
MF "v[56:59],a[80:83],v[184:187],v[56:59]\n"
LD "v202,s[48:51],s80 offen offset:2048 lds\n"
MF "v[64:67],a[84:87],v[184:187],v[252:255]\n"
TR "a[240:241],v3 offset:28672\n"
MF "v[64:67],a[44:47],v[180:183],v[64:67]\n"
TR "a[242:243],v3 offset:30720\n"
MF "v[64:67],a[4:7],v[176:179],v[64:67]\n"
LD "v203,s[48:51],s80 offen offset:3072 lds\n"
MF "v[72:75],a[8:11],v[176:179],v[252:255]\n"
TR "a[244:245],v3 offset:32768\n"
MF "v[72:75],a[48:51],v[180:183],v[72:75]\n"
TR "a[246:247],v3 offset:34816\n"
MF "v[72:75],a[88:91],v[184:187],v[72:75]\n"
MF "v[80:83],a[92:95],v[184:187],v[252:255]\n"
MF "v[80:83],a[52:55],v[180:183],v[80:83]\n"
MF "v[80:83],a[12:15],v[176:179],v[80:83]\n"
MF "v[88:91],a[16:19],v[176:179],v[252:255]\n"
MF "v[88:91],a[56:59],v[180:183],v[88:91]\n"
"s_waitcnt vmcnt(24) lgkmcnt(0)\n"
MF "v[88:91],a[96:99],v[184:187],v[88:91]\n"
"s_barrier\n"
MF "v[92:95],a[96:99],a[244:247],v[252:255]\n"
"s_add_u32 s89,s89,s65\n"
TR "v[176:177],v0\n"
MF "v[92:95],a[56:59],a[240:243],v[92:95]\n"
"s_add_u32 m0,s79,24576\n"
TR "v[178:179],v0 offset:2048\n"
MF "v[92:95],a[16:19],v[188:191],v[92:95]\n"
LD "v192,s[48:51],s89 offen lds\n"
MF "v[84:87],a[12:15],v[188:191],v[252:255]\n"
TR "v[180:181],v0 offset:4096\n"
MF "v[84:87],a[52:55],a[240:243],v[84:87]\n"
TR "v[182:183],v0 offset:6144\n"
MF "v[84:87],a[92:95],a[244:247],v[84:87]\n"
LD "v193,s[48:51],s89 offen offset:1024 lds\n"
MF "v[76:79],a[88:91],a[244:247],v[252:255]\n"
TR "v[184:185],v0 offset:8192\n"
MF "v[76:79],a[48:51],a[240:243],v[76:79]\n"
"s_nop 0\n"
TR "v[186:187],v0 offset:10240\n"
"ds_write_addtid_b32 v136 offset:49152\n"
MF "v[76:79],a[8:11],v[188:191],v[76:79]\n"
"s_nop 0\n"
"ds_write_addtid_b32 v137 offset:49408\n"
"ds_write_addtid_b32 v138 offset:49664\n"
MF "v[68:71],a[4:7],v[188:191],v[252:255]\n"
"s_nop 0\n"
"ds_write_addtid_b32 v139 offset:49920\n"
"ds_write_addtid_b32 v140 offset:50176\n"
MF "v[68:71],a[44:47],a[240:243],v[68:71]\n"
LD "v194,s[48:51],s89 offen offset:2048 lds\n"
MF "v[68:71],a[84:87],a[244:247],v[68:71]\n"
"s_nop 0\n"
"ds_write_addtid_b32 v141 offset:50432\n"
"ds_write_addtid_b32 v142 offset:50688\n"
MF "v[60:63],a[80:83],a[244:247],v[252:255]\n"
"s_nop 0\n"
"ds_write_addtid_b32 v143 offset:50944\n"
"ds_write_addtid_b32 v144 offset:51200\n"
MF "v[60:63],a[40:43],a[240:243],v[60:63]\n"
"s_nop 0\n"
"ds_write_addtid_b32 v145 offset:51456\n"
"ds_write_addtid_b32 v146 offset:51712\n"
MF "v[60:63],a[0:3],v[188:191],v[60:63]\n"
"s_waitcnt lgkmcnt(11)\n"
LD "v195,s[48:51],s89 offen offset:3072 lds\n"
MF "v[16:19],a[120:123],v[176:179],v[16:19]\n"
"s_nop 0\n"
TR "v[188:189],v1\n"
"ds_write_addtid_b32 v147 offset:51968\n"
MF "v[16:19],a[160:163],v[180:183],v[16:19]\n"
"s_nop 0\n"
TR "v[190:191],v1 offset:2048\n"
"ds_write_addtid_b32 v148 offset:52224\n"
MF "v[16:19],a[200:203],v[184:187],v[16:19]\n"
"s_nop 0\n"
"ds_write_addtid_b32 v149 offset:52480\n"
"ds_write_addtid_b32 v150 offset:52736\n"
MF "v[24:27],a[204:207],v[184:187],v[24:27]\n"
"s_nop 0\n"
TR "a[240:241],v1 offset:4096\n"
"ds_write_addtid_b32 v151 offset:52992\n"
MF "v[24:27],a[164:167],v[180:183],v[24:27]\n"
".Lpg27h:\n"
"s_cbranch_execz .Lpg31h\n"
"s_add_u32 m0,s79,28672\n"
TR "a[242:243],v1 offset:6144\n"
"ds_write_addtid_b32 v152 offset:49152\n"
MF "v[24:27],a[124:127],v[176:179],v[24:27]\n"
LD "v196,s[48:51],s89 offen lds\n"
MF "v[32:35],a[128:131],v[176:179],v[32:35]\n"
"s_nop 0\n"
TR "a[244:245],v1 offset:8192\n"
"ds_write_addtid_b32 v153 offset:49408\n"
MF "v[32:35],a[168:171],v[180:183],v[32:35]\n"
"s_nop 0\n"
TR "a[246:247],v1 offset:10240\n"
"ds_write_addtid_b32 v154 offset:49664\n"
MF "v[32:35],a[208:211],v[184:187],v[32:35]\n"
LD "v197,s[48:51],s89 offen offset:1024 lds\n"
MF "v[40:43],a[212:215],v[184:187],v[40:43]\n"
"s_nop 0\n"
"ds_write_addtid_b32 v155 offset:49920\n"
"ds_write_addtid_b32 v156 offset:50176\n"
MF "v[40:43],a[172:175],v[180:183],v[40:43]\n"
"s_nop 0\n"
"ds_write_addtid_b32 v157 offset:50432\n"
"ds_write_addtid_b32 v158 offset:50688\n"
MF "v[40:43],a[132:135],v[176:179],v[40:43]\n"
"s_nop 0\n"
"ds_write_addtid_b32 v159 offset:50944\n"
"ds_write_addtid_b32 v160 offset:51200\n"
MF "v[48:51],a[136:139],v[176:179],v[48:51]\n"
"s_nop 0\n"
"ds_write_addtid_b32 v161 offset:51456\n"
"ds_write_addtid_b32 v162 offset:51712\n"
MF "v[48:51],a[176:179],v[180:183],v[48:51]\n"
LD "v198,s[48:51],s89 offen offset:2048 lds\n"
MF "v[48:51],a[216:219],v[184:187],v[48:51]\n"
"s_waitcnt lgkmcnt(9)\n"
"ds_write_addtid_b32 v163 offset:51968\n"
"ds_write_addtid_b32 v164 offset:52224\n"
MF "v[52:55],a[216:219],a[244:247],v[52:55]\n"
"s_nop 0\n"
TR "v[176:177],v2\n"
"ds_write_addtid_b32 v165 offset:52480\n"
MF "v[52:55],a[176:179],a[240:243],v[52:55]\n"
"s_nop 0\n"
TR "v[178:179],v2 offset:2048\n"
"ds_write_addtid_b32 v166 offset:52736\n"
MF "v[52:55],a[136:139],v[188:191],v[52:55]\n"
LD "v199,s[48:51],s89 offen offset:3072 lds\n"
MF "v[44:47],a[132:135],v[188:191],v[44:47]\n"
"s_nop 0\n"
TR "v[180:181],v2 offset:4096\n"
"ds_write_addtid_b32 v167 offset:52992\n"
MF "v[44:47],a[172:175],a[240:243],v[44:47]\n"
"s_nop 0\n"
TR "v[182:183],v2 offset:6144\n"
"ds_write_addtid_b32 v168 offset:53248\n"
MF "v[44:47],a[212:215],a[244:247],v[44:47]\n"
"s_nop 0\n"
"ds_write_addtid_b32 v169 offset:53504\n"
"ds_write_addtid_b32 v170 offset:53760\n"
MF "v[36:39],a[208:211],a[244:247],v[36:39]\n"
"s_nop 0\n"
TR "v[184:185],v2 offset:8192\n"
"ds_write_addtid_b32 v171 offset:54016\n"
MF "v[36:39],a[168:171],a[240:243],v[36:39]\n"
"s_add_u32 m0,s79,32768\n"
TR "v[186:187],v2 offset:10240\n"
"ds_write_addtid_b32 v172 offset:50176\n"
MF "v[36:39],a[128:131],v[188:191],v[36:39]\n"
LD "v200,s[48:51],s89 offen lds\n"
MF "v[28:31],a[124:127],v[188:191],v[28:31]\n"
"s_nop 0\n"
"ds_write_addtid_b32 v173 offset:50432\n"
"ds_write_addtid_b32 v174 offset:50688\n"
MF "v[28:31],a[164:167],a[240:243],v[28:31]\n"
"ds_write_addtid_b32 v175 offset:50944\n"
MF "v[28:31],a[204:207],a[244:247],v[28:31]\n"
MF "v[20:23],a[200:203],a[244:247],v[20:23]\n"
LD "v201,s[48:51],s89 offen offset:1024 lds\n"
MF "v[20:23],a[160:163],a[240:243],v[20:23]\n"
MF "v[20:23],a[120:123],v[188:191],v[20:23]\n"
"s_waitcnt lgkmcnt(4)\n"
MF "v[56:59],a[120:123],v[176:179],v[56:59]\n"
TR "v[188:189],v3\n"
MF "v[56:59],a[160:163],v[180:183],v[56:59]\n"
TR "v[190:191],v3 offset:2048\n"
MF "v[56:59],a[200:203],v[184:187],v[56:59]\n"
LD "v202,s[48:51],s89 offen offset:2048 lds\n"
MF "v[64:67],a[204:207],v[184:187],v[64:67]\n"
TR "a[240:241],v3 offset:4096\n"
MF "v[64:67],a[164:167],v[180:183],v[64:67]\n"
TR "a[242:243],v3 offset:6144\n"
MF "v[64:67],a[124:127],v[176:179],v[64:67]\n"
LD "v203,s[48:51],s89 offen offset:3072 lds\n"
MF "v[72:75],a[128:131],v[176:179],v[72:75]\n"
TR "a[244:245],v3 offset:8192\n"
MF "v[72:75],a[168:171],v[180:183],v[72:75]\n"
TR "a[246:247],v3 offset:10240\n"
MF "v[72:75],a[208:211],v[184:187],v[72:75]\n"
MF "v[80:83],a[212:215],v[184:187],v[80:83]\n"
MF "v[80:83],a[172:175],v[180:183],v[80:83]\n"
MF "v[80:83],a[132:135],v[176:179],v[80:83]\n"
MF "v[88:91],a[136:139],v[176:179],v[88:91]\n"
MF "v[88:91],a[176:179],v[180:183],v[88:91]\n"
"s_waitcnt vmcnt(24) lgkmcnt(0)\n"
MF "v[88:91],a[216:219],v[184:187],v[88:91]\n"
"s_barrier\n"
MF "v[92:95],a[216:219],a[244:247],v[92:95]\n"
"s_add_u32 s80,s89,122880\n"
TR "v[176:177],v0 offset:49152\n"
MF "v[92:95],a[176:179],a[240:243],v[92:95]\n"
"s_add_u32 m0,s79,0\n"
TR "v[178:179],v0 offset:51200\n"
MF "v[92:95],a[136:139],v[188:191],v[92:95]\n"
LD "v192,s[48:51],s80 offen lds\n"
MF "v[84:87],a[132:135],v[188:191],v[84:87]\n"
TR "v[180:181],v0 offset:53248\n"
MF "v[84:87],a[172:175],a[240:243],v[84:87]\n"
TR "v[182:183],v0 offset:55296\n"
MF "v[84:87],a[212:215],a[244:247],v[84:87]\n"
LD "v193,s[48:51],s80 offen offset:1024 lds\n"
MF "v[76:79],a[208:211],a[244:247],v[76:79]\n"
TR "v[184:185],v0 offset:57344\n"
MF "v[76:79],a[168:171],a[240:243],v[76:79]\n"
TR "v[186:187],v0 offset:59392\n"
MF "v[76:79],a[128:131],v[188:191],v[76:79]\n"
MF "v[68:71],a[124:127],v[188:191],v[68:71]\n"
MF "v[68:71],a[164:167],a[240:243],v[68:71]\n"
LD "v194,s[48:51],s80 offen offset:2048 lds\n"
MF "v[68:71],a[204:207],a[244:247],v[68:71]\n"
MF "v[60:63],a[200:203],a[244:247],v[60:63]\n"
MF "v[60:63],a[160:163],a[240:243],v[60:63]\n"
MF "v[60:63],a[120:123],v[188:191],v[60:63]\n"
"s_waitcnt lgkmcnt(0)\n"
LD "v195,s[48:51],s80 offen offset:3072 lds\n"
MF "v[16:19],a[20:23],v[176:179],v[16:19]\n"
TR "v[188:189],v1 offset:49152\n"
MF "v[16:19],a[60:63],v[180:183],v[16:19]\n"
TR "v[190:191],v1 offset:51200\n"
MF "v[16:19],a[100:103],v[184:187],v[16:19]\n"
MF "v[24:27],a[104:107],v[184:187],v[24:27]\n"
TR "a[240:241],v1 offset:53248\n"
MF "v[24:27],a[64:67],v[180:183],v[24:27]\n"
"s_add_u32 m0,s79,4096\n"
TR "a[242:243],v1 offset:55296\n"
MF "v[24:27],a[24:27],v[176:179],v[24:27]\n"
LD "v196,s[48:51],s80 offen lds\n"
MF "v[32:35],a[28:31],v[176:179],v[32:35]\n"
TR "a[244:245],v1 offset:57344\n"
MF "v[32:35],a[68:71],v[180:183],v[32:35]\n"
TR "a[246:247],v1 offset:59392\n"
MF "v[32:35],a[108:111],v[184:187],v[32:35]\n"
LD "v197,s[48:51],s80 offen offset:1024 lds\n"
MF "v[40:43],a[112:115],v[184:187],v[40:43]\n"
MF "v[40:43],a[72:75],v[180:183],v[40:43]\n"
MF "v[40:43],a[32:35],v[176:179],v[40:43]\n"
MF "v[48:51],a[36:39],v[176:179],v[48:51]\n"
MF "v[48:51],a[76:79],v[180:183],v[48:51]\n"
LD "v198,s[48:51],s80 offen offset:2048 lds\n"
MF "v[48:51],a[116:119],v[184:187],v[48:51]\n"
"s_waitcnt lgkmcnt(0)\n"
MF "v[52:55],a[116:119],a[244:247],v[52:55]\n"
TR "v[176:177],v2 offset:49152\n"
MF "v[52:55],a[76:79],a[240:243],v[52:55]\n"
TR "v[178:179],v2 offset:51200\n"
MF "v[52:55],a[36:39],v[188:191],v[52:55]\n"
LD "v199,s[48:51],s80 offen offset:3072 lds\n"
MF "v[44:47],a[32:35],v[188:191],v[44:47]\n"
TR "v[180:181],v2 offset:53248\n"
MF "v[44:47],a[72:75],a[240:243],v[44:47]\n"
TR "v[182:183],v2 offset:55296\n"
MF "v[44:47],a[112:115],a[244:247],v[44:47]\n"
MF "v[36:39],a[108:111],a[244:247],v[36:39]\n"
TR "v[184:185],v2 offset:57344\n"
MF "v[36:39],a[68:71],a[240:243],v[36:39]\n"
"s_add_u32 m0,s79,8192\n"
TR "v[186:187],v2 offset:59392\n"
MF "v[36:39],a[28:31],v[188:191],v[36:39]\n"
LD "v200,s[48:51],s80 offen lds\n"
MF "v[28:31],a[24:27],v[188:191],v[28:31]\n"
MF "v[28:31],a[64:67],a[240:243],v[28:31]\n"
MF "v[28:31],a[104:107],a[244:247],v[28:31]\n"
MF "v[20:23],a[100:103],a[244:247],v[20:23]\n"
LD "v201,s[48:51],s80 offen offset:1024 lds\n"
MF "v[20:23],a[60:63],a[240:243],v[20:23]\n"
MF "v[20:23],a[20:23],v[188:191],v[20:23]\n"
"s_waitcnt lgkmcnt(0)\n"
MF "v[56:59],a[20:23],v[176:179],v[56:59]\n"
TR "v[188:189],v3 offset:49152\n"
MF "v[56:59],a[60:63],v[180:183],v[56:59]\n"
TR "v[190:191],v3 offset:51200\n"
MF "v[56:59],a[100:103],v[184:187],v[56:59]\n"
LD "v202,s[48:51],s80 offen offset:2048 lds\n"
MF "v[64:67],a[104:107],v[184:187],v[64:67]\n"
TR "a[240:241],v3 offset:53248\n"
MF "v[64:67],a[64:67],v[180:183],v[64:67]\n"
TR "a[242:243],v3 offset:55296\n"
MF "v[64:67],a[24:27],v[176:179],v[64:67]\n"
LD "v203,s[48:51],s80 offen offset:3072 lds\n"
MF "v[72:75],a[28:31],v[176:179],v[72:75]\n"
TR "a[244:245],v3 offset:57344\n"
MF "v[72:75],a[68:71],v[180:183],v[72:75]\n"
TR "a[246:247],v3 offset:59392\n"
MF "v[72:75],a[108:111],v[184:187],v[72:75]\n"
MF "v[80:83],a[112:115],v[184:187],v[80:83]\n"
MF "v[80:83],a[72:75],v[180:183],v[80:83]\n"
MF "v[80:83],a[32:35],v[176:179],v[80:83]\n"
MF "v[88:91],a[36:39],v[176:179],v[88:91]\n"
MF "v[88:91],a[76:79],v[180:183],v[88:91]\n"
"s_waitcnt vmcnt(24) lgkmcnt(0)\n"
MF "v[88:91],a[116:119],v[184:187],v[88:91]\n"
"s_barrier\n"
MF "v[92:95],a[116:119],a[244:247],v[92:95]\n"
"s_add_u32 s88,s88,s65\n"
TR "v[176:177],v4\n"
MF "v[92:95],a[76:79],a[240:243],v[92:95]\n"
"s_add_u32 m0,s79,49152\n"
TR "v[178:179],v4 offset:2048\n"
MF "v[92:95],a[36:39],v[188:191],v[92:95]\n"
LD "v192,s[48:51],s88 offen lds\n"
MF "v[84:87],a[32:35],v[188:191],v[84:87]\n"
TR "v[180:181],v4 offset:4096\n"
MF "v[84:87],a[72:75],a[240:243],v[84:87]\n"
TR "v[182:183],v4 offset:6144\n"
MF "v[84:87],a[112:115],a[244:247],v[84:87]\n"
LD "v193,s[48:51],s88 offen offset:1024 lds\n"
MF "v[76:79],a[108:111],a[244:247],v[76:79]\n"
TR "v[184:185],v4 offset:8192\n"
MF "v[76:79],a[68:71],a[240:243],v[76:79]\n"
TR "v[186:187],v4 offset:10240\n"
MF "v[76:79],a[28:31],v[188:191],v[76:79]\n"
MF "v[68:71],a[24:27],v[188:191],v[68:71]\n"
MF "v[68:71],a[64:67],a[240:243],v[68:71]\n"
LD "v194,s[48:51],s88 offen offset:2048 lds\n"
MF "v[68:71],a[104:107],a[244:247],v[68:71]\n"
MF "v[60:63],a[100:103],a[244:247],v[60:63]\n"
MF "v[60:63],a[60:63],a[240:243],v[60:63]\n"
MF "v[60:63],a[20:23],v[188:191],v[60:63]\n"
"s_waitcnt lgkmcnt(0)\n"
LD "v195,s[48:51],s88 offen offset:3072 lds\n"
MF "v[16:19],a[140:143],v[176:179],v[16:19]\n"
TR "v[188:189],v5\n"
MF "v[16:19],a[180:183],v[180:183],v[16:19]\n"
TR "v[190:191],v5 offset:2048\n"
MF "v[16:19],a[220:223],v[184:187],v[16:19]\n"
MF "v[24:27],a[224:227],v[184:187],v[24:27]\n"
TR "a[240:241],v5 offset:4096\n"
MF "v[24:27],a[184:187],v[180:183],v[24:27]\n"
"s_add_u32 m0,s79,53248\n"
TR "a[242:243],v5 offset:6144\n"
MF "v[24:27],a[144:147],v[176:179],v[24:27]\n"
LD "v196,s[48:51],s88 offen lds\n"
MF "v[32:35],a[148:151],v[176:179],v[32:35]\n"
TR "a[244:245],v5 offset:8192\n"
MF "v[32:35],a[188:191],v[180:183],v[32:35]\n"
TR "a[246:247],v5 offset:10240\n"
MF "v[32:35],a[228:231],v[184:187],v[32:35]\n"
LD "v197,s[48:51],s88 offen offset:1024 lds\n"
MF "v[40:43],a[232:235],v[184:187],v[40:43]\n"
MF "v[40:43],a[192:195],v[180:183],v[40:43]\n"
MF "v[40:43],a[152:155],v[176:179],v[40:43]\n"
MF "v[48:51],a[156:159],v[176:179],v[48:51]\n"
MF "v[48:51],a[196:199],v[180:183],v[48:51]\n"
LD "v198,s[48:51],s88 offen offset:2048 lds\n"
MF "v[48:51],a[236:239],v[184:187],v[48:51]\n"
"s_waitcnt lgkmcnt(0)\n"
MF "v[52:55],a[236:239],a[244:247],v[52:55]\n"
TR "v[176:177],v6\n"
MF "v[52:55],a[196:199],a[240:243],v[52:55]\n"
TR "v[178:179],v6 offset:2048\n"
MF "v[52:55],a[156:159],v[188:191],v[52:55]\n"
LD "v199,s[48:51],s88 offen offset:3072 lds\n"
MF "v[44:47],a[152:155],v[188:191],v[44:47]\n"
TR "v[180:181],v6 offset:4096\n"
MF "v[44:47],a[192:195],a[240:243],v[44:47]\n"
TR "v[182:183],v6 offset:6144\n"
MF "v[44:47],a[232:235],a[244:247],v[44:47]\n"
MF "v[36:39],a[228:231],a[244:247],v[36:39]\n"
TR "v[184:185],v6 offset:8192\n"
MF "v[36:39],a[188:191],a[240:243],v[36:39]\n"
"s_add_u32 m0,s79,57344\n"
TR "v[186:187],v6 offset:10240\n"
MF "v[36:39],a[148:151],v[188:191],v[36:39]\n"
LD "v200,s[48:51],s88 offen lds\n"
MF "v[28:31],a[144:147],v[188:191],v[28:31]\n"
MF "v[28:31],a[184:187],a[240:243],v[28:31]\n"
MF "v[28:31],a[224:227],a[244:247],v[28:31]\n"
MF "v[20:23],a[220:223],a[244:247],v[20:23]\n"
LD "v201,s[48:51],s88 offen offset:1024 lds\n"
MF "v[20:23],a[180:183],a[240:243],v[20:23]\n"
MF "v[20:23],a[140:143],v[188:191],v[20:23]\n"
"s_waitcnt lgkmcnt(0)\n"
MF "v[56:59],a[140:143],v[176:179],v[56:59]\n"
TR "v[188:189],v7\n"
MF "v[56:59],a[180:183],v[180:183],v[56:59]\n"
TR "v[190:191],v7 offset:2048\n"
MF "v[56:59],a[220:223],v[184:187],v[56:59]\n"
LD "v202,s[48:51],s88 offen offset:2048 lds\n"
MF "v[64:67],a[224:227],v[184:187],v[64:67]\n"
TR "a[240:241],v7 offset:4096\n"
"v_mov_b32 v244,0\n"
MF "v[64:67],a[184:187],v[180:183],v[64:67]\n"
TR "a[242:243],v7 offset:6144\n"
"v_mov_b32 v245,0\n"
MF "v[64:67],a[144:147],v[176:179],v[64:67]\n"
LD "v203,s[48:51],s88 offen offset:3072 lds\n"
MF "v[72:75],a[148:151],v[176:179],v[72:75]\n"
TR "a[244:245],v7 offset:8192\n"
"v_mov_b32 v246,0\n"
MF "v[72:75],a[188:191],v[180:183],v[72:75]\n"
TR "a[246:247],v7 offset:10240\n"
"v_mov_b32 v247,0\n"
MF "v[72:75],a[228:231],v[184:187],v[72:75]\n"
"v_mov_b32 v248,0\n"
"v_mov_b32 v249,0\n"
MF "v[80:83],a[232:235],v[184:187],v[80:83]\n"
"v_mov_b32 v250,0\n"
"v_mov_b32 v251,0\n"
MF "v[80:83],a[192:195],v[180:183],v[80:83]\n"
MF "v[80:83],a[152:155],v[176:179],v[80:83]\n"
MF "v[88:91],a[156:159],v[176:179],v[88:91]\n"
MF "v[88:91],a[196:199],v[180:183],v[88:91]\n"
"s_waitcnt vmcnt(24) lgkmcnt(0)\n"
MF "v[88:91],a[236:239],v[184:187],v[88:91]\n"
"s_barrier\n"
MF "v[92:95],a[236:239],a[244:247],v[92:95]\n"
"s_add_u32 s80,s88,122880\n"
TR "v[176:177],v0 offset:24576\n"
MF "v[92:95],a[196:199],a[240:243],v[92:95]\n"
"s_add_u32 m0,s79,98304\n"
"s_add_u32 s85,s85,1\n"
TR "v[178:179],v0 offset:26624\n"
MF "v[92:95],a[156:159],v[188:191],v[92:95]\n"
LD "v192,s[48:51],s80 offen lds\n"
MF "v[84:87],a[152:155],v[188:191],v[84:87]\n"
"s_nop 0\n"
TR "v[180:181],v0 offset:28672\n"
"ds_write_addtid_b32 v16 offset:24576\n"
MF "v[84:87],a[192:195],a[240:243],v[84:87]\n"
"s_nop 0\n"
TR "v[182:183],v0 offset:30720\n"
"ds_write_addtid_b32 v17 offset:24832\n"
MF "v[84:87],a[232:235],a[244:247],v[84:87]\n"
LD "v193,s[48:51],s80 offen offset:1024 lds\n"
MF "v[76:79],a[228:231],a[244:247],v[76:79]\n"
"s_nop 0\n"
TR "v[184:185],v0 offset:32768\n"
"ds_write_addtid_b32 v18 offset:25088\n"
MF "v[76:79],a[188:191],a[240:243],v[76:79]\n"
"s_nop 0\n"
TR "v[186:187],v0 offset:34816\n"
"ds_write_addtid_b32 v19 offset:25344\n"
MF "v[76:79],a[148:151],v[188:191],v[76:79]\n"
"s_nop 0\n"
"ds_write_addtid_b32 v20 offset:25600\n"
"ds_write_addtid_b32 v21 offset:25856\n"
MF "v[68:71],a[144:147],v[188:191],v[68:71]\n"
LD "v194,s[48:51],s80 offen offset:2048 lds\n"
MF "v[68:71],a[184:187],a[240:243],v[68:71]\n"
"s_nop 0\n"
"ds_write_addtid_b32 v22 offset:26112\n"
"ds_write_addtid_b32 v23 offset:26368\n"
MF "v[68:71],a[224:227],a[244:247],v[68:71]\n"
"s_nop 0\n"
"ds_write_addtid_b32 v24 offset:26624\n"
"ds_write_addtid_b32 v25 offset:26880\n"
MF "v[60:63],a[220:223],a[244:247],v[60:63]\n"
"s_nop 0\n"
"ds_write_addtid_b32 v26 offset:27136\n"
"ds_write_addtid_b32 v27 offset:27392\n"
MF "v[60:63],a[180:183],a[240:243],v[60:63]\n"
MF "v[60:63],a[140:143],v[188:191],v[60:63]\n"
"s_waitcnt lgkmcnt(9)\n"
"s_cmp_lg_u32 s85,5\n"
"s_cbranch_scc1 .Lpl05\n"
".Lpl09:\n"
MF "v[96:99],a[0:3],v[176:179],v[252:255]\n"
"s_nop 0\n"
TR "v[188:189],v1 offset:24576\n"
"ds_write_addtid_b32 v28 offset:27648\n"
MF "v[96:99],a[40:43],v[180:183],v[96:99]\n"
"s_nop 0\n"
TR "v[190:191],v1 offset:26624\n"
"ds_write_addtid_b32 v29 offset:27904\n"
MF "v[96:99],a[80:83],v[184:187],v[96:99]\n"
LD "v195,s[48:51],s80 offen offset:3072 lds\n"
MF "v[104:107],a[84:87],v[184:187],v[252:255]\n"
"s_add_u32 s60,s64,1\n"
TR "a[240:241],v1 offset:28672\n"
"ds_write_addtid_b32 v30 offset:28160\n"
MF "v[104:107],a[44:47],v[180:183],v[104:107]\n"
"s_add_u32 m0,s79,102400\n"
TR "a[242:243],v1 offset:30720\n"
"ds_write_addtid_b32 v31 offset:24320\n"
MF "v[104:107],a[4:7],v[176:179],v[104:107]\n"
LD "v196,s[48:51],s80 offen lds\n"
MF "v[112:115],a[8:11],v[176:179],v[252:255]\n"
"s_cmp_eq_u32 s60,8\n"
"s_cselect_b32 s60,0,s60\n"
TR "a[244:245],v1 offset:32768\n"
MF "v[112:115],a[48:51],v[180:183],v[112:115]\n"
"s_cmp_eq_u32 s60,10\n"
"s_cselect_b32 s60,s61,s60\n"
TR "a[246:247],v1 offset:34816\n"
MF "v[112:115],a[88:91],v[184:187],v[112:115]\n"
"s_sub_u32 s65,s60,s64\n"
"ds_write_addtid_b32 v32 offset:24576\n"
"ds_write_addtid_b32 v33 offset:24832\n"
MF "v[120:123],a[92:95],v[184:187],v[252:255]\n"
"s_lshl_b32 s65,s65,7\n"
"ds_write_addtid_b32 v34 offset:25088\n"
"ds_write_addtid_b32 v35 offset:25344\n"
MF "v[120:123],a[52:55],v[180:183],v[120:123]\n"
LD "v197,s[48:51],s80 offen offset:1024 lds\n"
MF "v[120:123],a[12:15],v[176:179],v[120:123]\n"
"s_mov_b32 s64,s60\n"
"ds_write_addtid_b32 v36 offset:25600\n"
"ds_write_addtid_b32 v37 offset:25856\n"
MF "v[128:131],a[16:19],v[176:179],v[252:255]\n"
"s_nop 0\n"
"ds_write_addtid_b32 v38 offset:26112\n"
"ds_write_addtid_b32 v39 offset:26368\n"
MF "v[128:131],a[56:59],v[180:183],v[128:131]\n"
"s_nop 0\n"
"ds_write_addtid_b32 v40 offset:26624\n"
"ds_write_addtid_b32 v41 offset:26880\n"
MF "v[128:131],a[96:99],v[184:187],v[128:131]\n"
"s_waitcnt lgkmcnt(10)\n"
LD "v198,s[48:51],s80 offen offset:2048 lds\n"
MF "v[132:135],a[96:99],a[244:247],v[252:255]\n"
"s_nop 0\n"
TR "v[176:177],v2 offset:24576\n"
"ds_write_addtid_b32 v42 offset:27136\n"
MF "v[132:135],a[56:59],a[240:243],v[132:135]\n"
"s_nop 0\n"
TR "v[178:179],v2 offset:26624\n"
"ds_write_addtid_b32 v43 offset:27392\n"
MF "v[132:135],a[16:19],v[188:191],v[132:135]\n"
"s_nop 0\n"
"ds_write_addtid_b32 v44 offset:27648\n"
"ds_write_addtid_b32 v45 offset:27904\n"
MF "v[124:127],a[12:15],v[188:191],v[252:255]\n"
"s_nop 0\n"
TR "v[180:181],v2 offset:28672\n"
"ds_write_addtid_b32 v46 offset:28160\n"
MF "v[124:127],a[52:55],a[240:243],v[124:127]\n"
"s_nop 0\n"
TR "v[182:183],v2 offset:30720\n"
"ds_write_addtid_b32 v47 offset:28416\n"
MF "v[124:127],a[92:95],a[244:247],v[124:127]\n"
LD "v199,s[48:51],s80 offen offset:3072 lds\n"
MF "v[116:119],a[88:91],a[244:247],v[252:255]\n"
"s_nop 0\n"
TR "v[184:185],v2 offset:32768\n"
"ds_write_addtid_b32 v48 offset:28672\n"
MF "v[116:119],a[48:51],a[240:243],v[116:119]\n"
"s_add_u32 m0,s79,106496\n"
TR "v[186:187],v2 offset:34816\n"
"ds_write_addtid_b32 v49 offset:24832\n"
MF "v[116:119],a[8:11],v[188:191],v[116:119]\n"
LD "v200,s[48:51],s80 offen lds\n"
MF "v[108:111],a[4:7],v[188:191],v[252:255]\n"
"s_nop 0\n"
"ds_write_addtid_b32 v50 offset:25088\n"
"ds_write_addtid_b32 v51 offset:25344\n"
MF "v[108:111],a[44:47],a[240:243],v[108:111]\n"
"s_nop 0\n"
"ds_write_addtid_b32 v52 offset:25600\n"
"ds_write_addtid_b32 v53 offset:25856\n"
MF "v[108:111],a[84:87],a[244:247],v[108:111]\n"
"s_nop 0\n"
"ds_write_addtid_b32 v54 offset:26112\n"
"ds_write_addtid_b32 v55 offset:26368\n"
MF "v[100:103],a[80:83],a[244:247],v[252:255]\n"
MF "v[100:103],a[40:43],a[240:243],v[100:103]\n"
LD "v201,s[48:51],s80 offen offset:1024 lds\n"
MF "v[100:103],a[0:3],v[188:191],v[100:103]\n"
"s_waitcnt lgkmcnt(7)\n"
MF "v[136:139],a[0:3],v[176:179],v[252:255]\n"
TR "v[188:189],v3 offset:24576\n"
MF "v[136:139],a[40:43],v[180:183],v[136:139]\n"
TR "v[190:191],v3 offset:26624\n"
MF "v[136:139],a[80:83],v[184:187],v[136:139]\n"
LD "v202,s[48:51],s80 offen offset:2048 lds\n"
MF "v[144:147],a[84:87],v[184:187],v[252:255]\n"
TR "a[240:241],v3 offset:28672\n"
MF "v[144:147],a[44:47],v[180:183],v[144:147]\n"
TR "a[242:243],v3 offset:30720\n"
MF "v[144:147],a[4:7],v[176:179],v[144:147]\n"
LD "v203,s[48:51],s80 offen offset:3072 lds\n"
MF "v[152:155],a[8:11],v[176:179],v[252:255]\n"
TR "a[244:245],v3 offset:32768\n"
MF "v[152:155],a[48:51],v[180:183],v[152:155]\n"
TR "a[246:247],v3 offset:34816\n"
MF "v[152:155],a[88:91],v[184:187],v[152:155]\n"
MF "v[160:163],a[92:95],v[184:187],v[252:255]\n"
MF "v[160:163],a[52:55],v[180:183],v[160:163]\n"
MF "v[160:163],a[12:15],v[176:179],v[160:163]\n"
MF "v[168:171],a[16:19],v[176:179],v[252:255]\n"
MF "v[168:171],a[56:59],v[180:183],v[168:171]\n"
"s_waitcnt vmcnt(24) lgkmcnt(0)\n"
MF "v[168:171],a[96:99],v[184:187],v[168:171]\n"
"s_barrier\n"
MF "v[172:175],a[96:99],a[244:247],v[252:255]\n"
"s_add_u32 s89,s89,s65\n"
TR "v[176:177],v0\n"
MF "v[172:175],a[56:59],a[240:243],v[172:175]\n"
"s_add_u32 m0,s79,24576\n"
TR "v[178:179],v0 offset:2048\n"
MF "v[172:175],a[16:19],v[188:191],v[172:175]\n"
LD "v192,s[48:51],s89 offen lds\n"
MF "v[164:167],a[12:15],v[188:191],v[252:255]\n"
TR "v[180:181],v0 offset:4096\n"
MF "v[164:167],a[52:55],a[240:243],v[164:167]\n"
TR "v[182:183],v0 offset:6144\n"
MF "v[164:167],a[92:95],a[244:247],v[164:167]\n"
LD "v193,s[48:51],s89 offen offset:1024 lds\n"
MF "v[156:159],a[88:91],a[244:247],v[252:255]\n"
TR "v[184:185],v0 offset:8192\n"
MF "v[156:159],a[48:51],a[240:243],v[156:159]\n"
"s_nop 0\n"
TR "v[186:187],v0 offset:10240\n"
"ds_write_addtid_b32 v56 offset:49152\n"
MF "v[156:159],a[8:11],v[188:191],v[156:159]\n"
"s_nop 0\n"
"ds_write_addtid_b32 v57 offset:49408\n"
"ds_write_addtid_b32 v58 offset:49664\n"
MF "v[148:151],a[4:7],v[188:191],v[252:255]\n"
LD "v194,s[48:51],s89 offen offset:2048 lds\n"
MF "v[148:151],a[44:47],a[240:243],v[148:151]\n"
"buffer_load_dword v10,v12,s[40:43],s86 offen sc1\n"
MF "v[148:151],a[84:87],a[244:247],v[148:151]\n"
"s_nop 0\n"
"ds_write_addtid_b32 v59 offset:49920\n"
"ds_write_addtid_b32 v60 offset:50176\n"
MF "v[140:143],a[80:83],a[244:247],v[252:255]\n"
"s_nop 0\n"
"ds_write_addtid_b32 v61 offset:50432\n"
"ds_write_addtid_b32 v62 offset:50688\n"
MF "v[140:143],a[40:43],a[240:243],v[140:143]\n"
"s_nop 0\n"
"ds_write_addtid_b32 v63 offset:50944\n"
"ds_write_addtid_b32 v64 offset:51200\n"
MF "v[140:143],a[0:3],v[188:191],v[140:143]\n"
"s_waitcnt lgkmcnt(9)\n"
LD "v195,s[48:51],s89 offen offset:3072 lds\n"
MF "v[96:99],a[120:123],v[176:179],v[96:99]\n"
"s_nop 0\n"
TR "v[188:189],v1\n"
"ds_write_addtid_b32 v65 offset:51456\n"
MF "v[96:99],a[160:163],v[180:183],v[96:99]\n"
"s_nop 0\n"
TR "v[190:191],v1 offset:2048\n"
"ds_write_addtid_b32 v66 offset:51712\n"
MF "v[96:99],a[200:203],v[184:187],v[96:99]\n"
".Lpg28h:\n"
"s_cbranch_execz .Lpgrole1_h\n"
"s_nop 0\n"
"ds_write_addtid_b32 v67 offset:51968\n"
"ds_write_addtid_b32 v68 offset:52224\n"
MF "v[104:107],a[204:207],v[184:187],v[104:107]\n"
"s_nop 0\n"
TR "a[240:241],v1 offset:4096\n"
"ds_write_addtid_b32 v69 offset:52480\n"
MF "v[104:107],a[164:167],v[180:183],v[104:107]\n"
"s_add_u32 m0,s79,28672\n"
TR "a[242:243],v1 offset:6144\n"
"ds_write_addtid_b32 v70 offset:48640\n"
MF "v[104:107],a[124:127],v[176:179],v[104:107]\n"
LD "v196,s[48:51],s89 offen lds\n"
MF "v[112:115],a[128:131],v[176:179],v[112:115]\n"
"s_nop 0\n"
TR "a[244:245],v1 offset:8192\n"
"ds_write_addtid_b32 v71 offset:48896\n"
MF "v[112:115],a[168:171],v[180:183],v[112:115]\n"
"s_nop 0\n"
TR "a[246:247],v1 offset:10240\n"
"ds_write_addtid_b32 v72 offset:49152\n"
MF "v[112:115],a[208:211],v[184:187],v[112:115]\n"
LD "v197,s[48:51],s89 offen offset:1024 lds\n"
MF "v[120:123],a[212:215],v[184:187],v[120:123]\n"
"s_nop 0\n"
"ds_write_addtid_b32 v73 offset:49408\n"
"ds_write_addtid_b32 v74 offset:49664\n"
MF "v[120:123],a[172:175],v[180:183],v[120:123]\n"
"s_nop 0\n"
"ds_write_addtid_b32 v75 offset:49920\n"
"ds_write_addtid_b32 v76 offset:50176\n"
MF "v[120:123],a[132:135],v[176:179],v[120:123]\n"
"s_nop 0\n"
"ds_write_addtid_b32 v77 offset:50432\n"
"ds_write_addtid_b32 v78 offset:50688\n"
MF "v[128:131],a[136:139],v[176:179],v[128:131]\n"
"s_nop 0\n"
"ds_write_addtid_b32 v79 offset:50944\n"
"ds_write_addtid_b32 v80 offset:51200\n"
MF "v[128:131],a[176:179],v[180:183],v[128:131]\n"
LD "v198,s[48:51],s89 offen offset:2048 lds\n"
MF "v[128:131],a[216:219],v[184:187],v[128:131]\n"
"s_waitcnt lgkmcnt(9)\n"
"ds_write_addtid_b32 v81 offset:51456\n"
"ds_write_addtid_b32 v82 offset:51712\n"
MF "v[132:135],a[216:219],a[244:247],v[132:135]\n"
"s_nop 0\n"
TR "v[176:177],v2\n"
"ds_write_addtid_b32 v83 offset:51968\n"
MF "v[132:135],a[176:179],a[240:243],v[132:135]\n"
"s_nop 0\n"
TR "v[178:179],v2 offset:2048\n"
"ds_write_addtid_b32 v84 offset:52224\n"
MF "v[132:135],a[136:139],v[188:191],v[132:135]\n"
LD "v199,s[48:51],s89 offen offset:3072 lds\n"
MF "v[124:127],a[132:135],v[188:191],v[124:127]\n"
"s_nop 0\n"
TR "v[180:181],v2 offset:4096\n"
"ds_write_addtid_b32 v85 offset:52480\n"
MF "v[124:127],a[172:175],a[240:243],v[124:127]\n"
"s_nop 0\n"
TR "v[182:183],v2 offset:6144\n"
"ds_write_addtid_b32 v86 offset:52736\n"
MF "v[124:127],a[212:215],a[244:247],v[124:127]\n"
"s_nop 0\n"
"ds_write_addtid_b32 v87 offset:52992\n"
"ds_write_addtid_b32 v88 offset:53248\n"
MF "v[116:119],a[208:211],a[244:247],v[116:119]\n"
"s_nop 0\n"
TR "v[184:185],v2 offset:8192\n"
"ds_write_addtid_b32 v89 offset:53504\n"
MF "v[116:119],a[168:171],a[240:243],v[116:119]\n"
"s_add_u32 m0,s79,32768\n"
TR "v[186:187],v2 offset:10240\n"
"ds_write_addtid_b32 v90 offset:49664\n"
MF "v[116:119],a[128:131],v[188:191],v[116:119]\n"
LD "v200,s[48:51],s89 offen lds\n"
MF "v[108:111],a[124:127],v[188:191],v[108:111]\n"
"s_nop 0\n"
"ds_write_addtid_b32 v91 offset:49920\n"
"ds_write_addtid_b32 v92 offset:50176\n"
MF "v[108:111],a[164:167],a[240:243],v[108:111]\n"
"s_nop 0\n"
"ds_write_addtid_b32 v93 offset:50432\n"
"ds_write_addtid_b32 v94 offset:50688\n"
MF "v[108:111],a[204:207],a[244:247],v[108:111]\n"
"ds_write_addtid_b32 v95 offset:50944\n"
MF "v[100:103],a[200:203],a[244:247],v[100:103]\n"
LD "v201,s[48:51],s89 offen offset:1024 lds\n"
MF "v[100:103],a[160:163],a[240:243],v[100:103]\n"
MF "v[100:103],a[120:123],v[188:191],v[100:103]\n"
"s_waitcnt lgkmcnt(6)\n"
MF "v[136:139],a[120:123],v[176:179],v[136:139]\n"
TR "v[188:189],v3\n"
MF "v[136:139],a[160:163],v[180:183],v[136:139]\n"
TR "v[190:191],v3 offset:2048\n"
MF "v[136:139],a[200:203],v[184:187],v[136:139]\n"
LD "v202,s[48:51],s89 offen offset:2048 lds\n"
MF "v[144:147],a[204:207],v[184:187],v[144:147]\n"
TR "a[240:241],v3 offset:4096\n"
MF "v[144:147],a[164:167],v[180:183],v[144:147]\n"
TR "a[242:243],v3 offset:6144\n"
MF "v[144:147],a[124:127],v[176:179],v[144:147]\n"
LD "v203,s[48:51],s89 offen offset:3072 lds\n"
MF "v[152:155],a[128:131],v[176:179],v[152:155]\n"
TR "a[244:245],v3 offset:8192\n"
MF "v[152:155],a[168:171],v[180:183],v[152:155]\n"
TR "a[246:247],v3 offset:10240\n"
MF "v[152:155],a[208:211],v[184:187],v[152:155]\n"
MF "v[160:163],a[212:215],v[184:187],v[160:163]\n"
MF "v[160:163],a[172:175],v[180:183],v[160:163]\n"
MF "v[160:163],a[132:135],v[176:179],v[160:163]\n"
MF "v[168:171],a[136:139],v[176:179],v[168:171]\n"
MF "v[168:171],a[176:179],v[180:183],v[168:171]\n"
"s_waitcnt vmcnt(25) lgkmcnt(0)\n"
MF "v[168:171],a[216:219],v[184:187],v[168:171]\n"
"s_barrier\n"
MF "v[172:175],a[216:219],a[244:247],v[172:175]\n"
"s_add_u32 s80,s89,122880\n"
TR "v[176:177],v0 offset:49152\n"
MF "v[172:175],a[176:179],a[240:243],v[172:175]\n"
"s_add_u32 m0,s79,0\n"
"s_mov_b32 s82,s70\n"
TR "v[178:179],v0 offset:51200\n"
MF "v[172:175],a[136:139],v[188:191],v[172:175]\n"
LD "v192,s[48:51],s80 offen lds\n"
MF "v[164:167],a[132:135],v[188:191],v[164:167]\n"
TR "v[180:181],v0 offset:53248\n"
MF "v[164:167],a[172:175],a[240:243],v[164:167]\n"
TR "v[182:183],v0 offset:55296\n"
MF "v[164:167],a[212:215],a[244:247],v[164:167]\n"
LD "v193,s[48:51],s80 offen offset:1024 lds\n"
MF "v[156:159],a[208:211],a[244:247],v[156:159]\n"
TR "v[184:185],v0 offset:57344\n"
MF "v[156:159],a[168:171],a[240:243],v[156:159]\n"
TR "v[186:187],v0 offset:59392\n"
MF "v[156:159],a[128:131],v[188:191],v[156:159]\n"
MF "v[148:151],a[124:127],v[188:191],v[148:151]\n"
MF "v[148:151],a[164:167],a[240:243],v[148:151]\n"
LD "v194,s[48:51],s80 offen offset:2048 lds\n"
MF "v[148:151],a[204:207],a[244:247],v[148:151]\n"
MF "v[140:143],a[200:203],a[244:247],v[140:143]\n"
MF "v[140:143],a[160:163],a[240:243],v[140:143]\n"
MF "v[140:143],a[120:123],v[188:191],v[140:143]\n"
"s_waitcnt lgkmcnt(0)\n"
LD "v195,s[48:51],s80 offen offset:3072 lds\n"
MF "v[96:99],a[20:23],v[176:179],v[96:99]\n"
TR "v[188:189],v1 offset:49152\n"
MF "v[96:99],a[60:63],v[180:183],v[96:99]\n"
TR "v[190:191],v1 offset:51200\n"
MF "v[96:99],a[100:103],v[184:187],v[96:99]\n"
MF "v[104:107],a[104:107],v[184:187],v[104:107]\n"
TR "a[240:241],v1 offset:53248\n"
MF "v[104:107],a[64:67],v[180:183],v[104:107]\n"
"s_add_u32 m0,s79,4096\n"
TR "a[242:243],v1 offset:55296\n"
MF "v[104:107],a[24:27],v[176:179],v[104:107]\n"
LD "v196,s[48:51],s80 offen lds\n"
MF "v[112:115],a[28:31],v[176:179],v[112:115]\n"
TR "a[244:245],v1 offset:57344\n"
MF "v[112:115],a[68:71],v[180:183],v[112:115]\n"
TR "a[246:247],v1 offset:59392\n"
MF "v[112:115],a[108:111],v[184:187],v[112:115]\n"
LD "v197,s[48:51],s80 offen offset:1024 lds\n"
MF "v[120:123],a[112:115],v[184:187],v[120:123]\n"
MF "v[120:123],a[72:75],v[180:183],v[120:123]\n"
MF "v[120:123],a[32:35],v[176:179],v[120:123]\n"
MF "v[128:131],a[36:39],v[176:179],v[128:131]\n"
MF "v[128:131],a[76:79],v[180:183],v[128:131]\n"
LD "v198,s[48:51],s80 offen offset:2048 lds\n"
MF "v[128:131],a[116:119],v[184:187],v[128:131]\n"
"s_waitcnt lgkmcnt(0)\n"
MF "v[132:135],a[116:119],a[244:247],v[132:135]\n"
TR "v[176:177],v2 offset:49152\n"
MF "v[132:135],a[76:79],a[240:243],v[132:135]\n"
TR "v[178:179],v2 offset:51200\n"
MF "v[132:135],a[36:39],v[188:191],v[132:135]\n"
LD "v199,s[48:51],s80 offen offset:3072 lds\n"
MF "v[124:127],a[32:35],v[188:191],v[124:127]\n"
TR "v[180:181],v2 offset:53248\n"
MF "v[124:127],a[72:75],a[240:243],v[124:127]\n"
TR "v[182:183],v2 offset:55296\n"
MF "v[124:127],a[112:115],a[244:247],v[124:127]\n"
MF "v[116:119],a[108:111],a[244:247],v[116:119]\n"
TR "v[184:185],v2 offset:57344\n"
MF "v[116:119],a[68:71],a[240:243],v[116:119]\n"
"s_add_u32 m0,s79,8192\n"
TR "v[186:187],v2 offset:59392\n"
MF "v[116:119],a[28:31],v[188:191],v[116:119]\n"
LD "v200,s[48:51],s80 offen lds\n"
MF "v[108:111],a[24:27],v[188:191],v[108:111]\n"
MF "v[108:111],a[64:67],a[240:243],v[108:111]\n"
MF "v[108:111],a[104:107],a[244:247],v[108:111]\n"
MF "v[100:103],a[100:103],a[244:247],v[100:103]\n"
LD "v201,s[48:51],s80 offen offset:1024 lds\n"
MF "v[100:103],a[60:63],a[240:243],v[100:103]\n"
"s_waitcnt vmcnt(19)\n"
"s_cmp_lt_u32 s85,6\n"
MF "v[100:103],a[20:23],v[188:191],v[100:103]\n"
"s_cbranch_scc1 .Ljs4\n"
"v_mov_b32 v236,v228\n"
"v_mov_b32 v237,v229\n"
MF "v[96:99],a[252:255],v[208:211],v[96:99]\n"
MF "v[104:107],a[252:255],v[212:215],v[104:107]\n"
MF "v[112:115],a[252:255],v[216:219],v[112:115]\n"
MF "v[120:123],a[252:255],v[220:223],v[120:123]\n"
MF "v[128:131],a[252:255],v[224:227],v[128:131]\n"
MF "v[100:103],a[248:251],v[208:211],v[100:103]\n"
MF "v[108:111],a[248:251],v[212:215],v[108:111]\n"
MF "v[116:119],a[248:251],v[216:219],v[116:119]\n"
MF "v[124:127],a[248:251],v[220:223],v[124:127]\n"
MF "v[132:135],a[248:251],v[224:227],v[132:135]\n"
MF "v[248:251],v[230:233],a[250:253],v[248:251]\n"
MF "v[244:247],v[234:237],a[250:253],v[244:247]\n"
".Ljs4:\n"
"s_waitcnt lgkmcnt(0)\n"
MF "v[136:139],a[20:23],v[176:179],v[136:139]\n"
TR "v[188:189],v3 offset:49152\n"
"v_sub_u32 v10,v10,v11\n"
MF "v[136:139],a[60:63],v[180:183],v[136:139]\n"
TR "v[190:191],v3 offset:51200\n"
MF "v[136:139],a[100:103],v[184:187],v[136:139]\n"
LD "v202,s[48:51],s80 offen offset:2048 lds\n"
MF "v[144:147],a[104:107],v[184:187],v[144:147]\n"
TR "a[240:241],v3 offset:53248\n"
MF "v[144:147],a[64:67],v[180:183],v[144:147]\n"
TR "a[242:243],v3 offset:55296\n"
MF "v[144:147],a[24:27],v[176:179],v[144:147]\n"
LD "v203,s[48:51],s80 offen offset:3072 lds\n"
MF "v[152:155],a[28:31],v[176:179],v[152:155]\n"
TR "a[244:245],v3 offset:57344\n"
MF "v[152:155],a[68:71],v[180:183],v[152:155]\n"
TR "a[246:247],v3 offset:59392\n"
MF "v[152:155],a[108:111],v[184:187],v[152:155]\n"
".Lnf0:\n"
"v_readlane_b32 s58,v10,s67\n"
"s_cmp_lt_u32 s58,s85\n"
"s_cbranch_scc1 .Lsp0\n"
".Lnb0:\n"
MF "v[160:163],a[112:115],v[184:187],v[160:163]\n"
"s_add_u32 s70,s70,20480\n"
".Lnf1:\n"
"v_cmp_gt_u32 vcc,4,v10\n"
"s_cbranch_vccnz .Lsp1\n"
".Lnb1:\n"
MF "v[160:163],a[72:75],v[180:183],v[160:163]\n"
MF "v[160:163],a[32:35],v[176:179],v[160:163]\n"
MF "v[168:171],a[36:39],v[176:179],v[168:171]\n"
MF "v[168:171],a[76:79],v[180:183],v[168:171]\n"
"s_waitcnt vmcnt(25) lgkmcnt(0)\n"
MF "v[168:171],a[116:119],v[184:187],v[168:171]\n"
"s_barrier\n"
MF "v[172:175],a[116:119],a[244:247],v[172:175]\n"
"s_add_u32 s88,s88,s65\n"
TR "v[176:177],v4\n"
MF "v[172:175],a[76:79],a[240:243],v[172:175]\n"
"s_add_u32 m0,s79,49152\n"
TR "v[178:179],v4 offset:2048\n"
MF "v[172:175],a[36:39],v[188:191],v[172:175]\n"
LD "v192,s[48:51],s88 offen lds\n"
MF "v[164:167],a[32:35],v[188:191],v[164:167]\n"
TR "v[180:181],v4 offset:4096\n"
MF "v[164:167],a[72:75],a[240:243],v[164:167]\n"
TR "v[182:183],v4 offset:6144\n"
MF "v[164:167],a[112:115],a[244:247],v[164:167]\n"
LD "v193,s[48:51],s88 offen offset:1024 lds\n"
MF "v[156:159],a[108:111],a[244:247],v[156:159]\n"
TR "v[184:185],v4 offset:8192\n"
MF "v[156:159],a[68:71],a[240:243],v[156:159]\n"
"s_add_u32 s2,s2,204800\n"
TR "v[186:187],v4 offset:10240\n"
MF "v[156:159],a[28:31],v[188:191],v[156:159]\n"
LD "v[228:231],v9,s[40:43],s2 offen sc1\n"
MF "v[148:151],a[24:27],v[188:191],v[148:151]\n"
MF "v[148:151],a[64:67],a[240:243],v[148:151]\n"
LD "v194,s[48:51],s88 offen offset:2048 lds\n"
MF "v[148:151],a[104:107],a[244:247],v[148:151]\n"
MF "v[140:143],a[100:103],a[244:247],v[140:143]\n"
MF "v[140:143],a[60:63],a[240:243],v[140:143]\n"
MF "v[140:143],a[20:23],v[188:191],v[140:143]\n"
"s_waitcnt lgkmcnt(0)\n"
LD "v195,s[48:51],s88 offen offset:3072 lds\n"
MF "v[96:99],a[140:143],v[176:179],v[96:99]\n"
TR "v[188:189],v5\n"
MF "v[96:99],a[180:183],v[180:183],v[96:99]\n"
"s_add_u32 s2,s2,204800\n"
TR "v[190:191],v5 offset:2048\n"
MF "v[96:99],a[220:223],v[184:187],v[96:99]\n"
LD "v[232:235],v9,s[40:43],s2 offen sc1\n"
MF "v[104:107],a[224:227],v[184:187],v[104:107]\n"
TR "a[240:241],v5 offset:4096\n"
MF "v[104:107],a[184:187],v[180:183],v[104:107]\n"
"s_add_u32 m0,s79,53248\n"
TR "a[242:243],v5 offset:6144\n"
MF "v[104:107],a[144:147],v[176:179],v[104:107]\n"
LD "v196,s[48:51],s88 offen lds\n"
MF "v[112:115],a[148:151],v[176:179],v[112:115]\n"
TR "a[244:245],v5 offset:8192\n"
MF "v[112:115],a[188:191],v[180:183],v[112:115]\n"
TR "a[246:247],v5 offset:10240\n"
MF "v[112:115],a[228:231],v[184:187],v[112:115]\n"
LD "v197,s[48:51],s88 offen offset:1024 lds\n"
MF "v[120:123],a[232:235],v[184:187],v[120:123]\n"
MF "v[120:123],a[192:195],v[180:183],v[120:123]\n"
MF "v[120:123],a[152:155],v[176:179],v[120:123]\n"
LD "v[208:211],v9,s[40:43],s82 offen sc1\n"
MF "v[128:131],a[156:159],v[176:179],v[128:131]\n"
MF "v[128:131],a[196:199],v[180:183],v[128:131]\n"
LD "v198,s[48:51],s88 offen offset:2048 lds\n"
MF "v[128:131],a[236:239],v[184:187],v[128:131]\n"
"s_waitcnt lgkmcnt(0)\n"
MF "v[132:135],a[236:239],a[244:247],v[132:135]\n"
TR "v[176:177],v6\n"
MF "v[132:135],a[196:199],a[240:243],v[132:135]\n"
TR "v[178:179],v6 offset:2048\n"
MF "v[132:135],a[156:159],v[188:191],v[132:135]\n"
LD "v199,s[48:51],s88 offen offset:3072 lds\n"
MF "v[124:127],a[152:155],v[188:191],v[124:127]\n"
TR "v[180:181],v6 offset:4096\n"
MF "v[124:127],a[192:195],a[240:243],v[124:127]\n"
TR "v[182:183],v6 offset:6144\n"
MF "v[124:127],a[232:235],a[244:247],v[124:127]\n"
LD "v[212:215],v9,s[40:43],s82 offen offset:1024 sc1\n"
MF "v[116:119],a[228:231],a[244:247],v[116:119]\n"
TR "v[184:185],v6 offset:8192\n"
MF "v[116:119],a[188:191],a[240:243],v[116:119]\n"
"s_add_u32 m0,s79,57344\n"
TR "v[186:187],v6 offset:10240\n"
MF "v[116:119],a[148:151],v[188:191],v[116:119]\n"
LD "v200,s[48:51],s88 offen lds\n"
MF "v[108:111],a[144:147],v[188:191],v[108:111]\n"
MF "v[108:111],a[184:187],a[240:243],v[108:111]\n"
MF "v[108:111],a[224:227],a[244:247],v[108:111]\n"
MF "v[100:103],a[220:223],a[244:247],v[100:103]\n"
LD "v201,s[48:51],s88 offen offset:1024 lds\n"
MF "v[100:103],a[180:183],a[240:243],v[100:103]\n"
MF "v[100:103],a[140:143],v[188:191],v[100:103]\n"
"s_waitcnt lgkmcnt(0)\n"
LD "v[216:219],v9,s[40:43],s82 offen offset:2048 sc1\n"
MF "v[136:139],a[140:143],v[176:179],v[136:139]\n"
TR "v[188:189],v7\n"
MF "v[136:139],a[180:183],v[180:183],v[136:139]\n"
TR "v[190:191],v7 offset:2048\n"
MF "v[136:139],a[220:223],v[184:187],v[136:139]\n"
LD "v202,s[48:51],s88 offen offset:2048 lds\n"
MF "v[144:147],a[224:227],v[184:187],v[144:147]\n"
TR "a[240:241],v7 offset:4096\n"
MF "v[144:147],a[184:187],v[180:183],v[144:147]\n"
TR "a[242:243],v7 offset:6144\n"
MF "v[144:147],a[144:147],v[176:179],v[144:147]\n"
LD "v203,s[48:51],s88 offen offset:3072 lds\n"
MF "v[152:155],a[148:151],v[176:179],v[152:155]\n"
TR "a[244:245],v7 offset:8192\n"
MF "v[152:155],a[188:191],v[180:183],v[152:155]\n"
TR "a[246:247],v7 offset:10240\n"
MF "v[152:155],a[228:231],v[184:187],v[152:155]\n"
LD "v[220:223],v9,s[40:43],s82 offen offset:3072 sc1\n"
MF "v[160:163],a[232:235],v[184:187],v[160:163]\n"
MF "v[160:163],a[192:195],v[180:183],v[160:163]\n"
MF "v[160:163],a[152:155],v[176:179],v[160:163]\n"
MF "v[168:171],a[156:159],v[176:179],v[168:171]\n"
"s_add_u32 s82,s82,4096\n"
MF "v[168:171],a[196:199],v[180:183],v[168:171]\n"
"s_waitcnt vmcnt(30) lgkmcnt(0)\n"
LD "v[224:227],v9,s[40:43],s82 offen sc1\n"
MF "v[168:171],a[236:239],v[184:187],v[168:171]\n"
"s_barrier\n"
MF "v[172:175],a[236:239],a[244:247],v[172:175]\n"
"s_add_u32 s80,s88,122880\n"
TR "v[176:177],v0 offset:24576\n"
MF "v[172:175],a[196:199],a[240:243],v[172:175]\n"
"s_add_u32 m0,s79,98304\n"
"s_add_u32 s85,s85,1\n"
TR "v[178:179],v0 offset:26624\n"
MF "v[172:175],a[156:159],v[188:191],v[172:175]\n"
LD "v192,s[48:51],s80 offen lds\n"
MF "v[164:167],a[152:155],v[188:191],v[164:167]\n"
"s_nop 0\n"
TR "v[180:181],v0 offset:28672\n"
"ds_write_addtid_b32 v96 offset:24576\n"
MF "v[164:167],a[192:195],a[240:243],v[164:167]\n"
"s_nop 0\n"
TR "v[182:183],v0 offset:30720\n"
"ds_write_addtid_b32 v97 offset:24832\n"
MF "v[164:167],a[232:235],a[244:247],v[164:167]\n"
LD "v193,s[48:51],s80 offen offset:1024 lds\n"
MF "v[156:159],a[228:231],a[244:247],v[156:159]\n"
"s_nop 0\n"
TR "v[184:185],v0 offset:32768\n"
"ds_write_addtid_b32 v98 offset:25088\n"
MF "v[156:159],a[188:191],a[240:243],v[156:159]\n"
"s_nop 0\n"
TR "v[186:187],v0 offset:34816\n"
"ds_write_addtid_b32 v99 offset:25344\n"
MF "v[156:159],a[148:151],v[188:191],v[156:159]\n"
"s_nop 0\n"
"ds_write_addtid_b32 v100 offset:25600\n"
"ds_write_addtid_b32 v101 offset:25856\n"
MF "v[148:151],a[144:147],v[188:191],v[148:151]\n"
LD "v194,s[48:51],s80 offen offset:2048 lds\n"
MF "v[148:151],a[184:187],a[240:243],v[148:151]\n"
"s_nop 0\n"
"ds_write_addtid_b32 v102 offset:26112\n"
"ds_write_addtid_b32 v103 offset:26368\n"
MF "v[148:151],a[224:227],a[244:247],v[148:151]\n"
"s_nop 0\n"
"ds_write_addtid_b32 v104 offset:26624\n"
"ds_write_addtid_b32 v105 offset:26880\n"
MF "v[140:143],a[220:223],a[244:247],v[140:143]\n"
"s_nop 0\n"
"ds_write_addtid_b32 v106 offset:27136\n"
"ds_write_addtid_b32 v107 offset:27392\n"
MF "v[140:143],a[180:183],a[240:243],v[140:143]\n"
MF "v[140:143],a[140:143],v[188:191],v[140:143]\n"
"s_waitcnt lgkmcnt(9)\n"
MF "v[16:19],a[0:3],v[176:179],v[252:255]\n"
"s_nop 0\n"
TR "v[188:189],v1 offset:24576\n"
"ds_write_addtid_b32 v108 offset:27648\n"
MF "v[16:19],a[40:43],v[180:183],v[16:19]\n"
"s_nop 0\n"
TR "v[190:191],v1 offset:26624\n"
"ds_write_addtid_b32 v109 offset:27904\n"
MF "v[16:19],a[80:83],v[184:187],v[16:19]\n"
LD "v195,s[48:51],s80 offen offset:3072 lds\n"
MF "v[24:27],a[84:87],v[184:187],v[252:255]\n"
"s_add_u32 s60,s64,1\n"
TR "a[240:241],v1 offset:28672\n"
"ds_write_addtid_b32 v110 offset:28160\n"
MF "v[24:27],a[44:47],v[180:183],v[24:27]\n"
"s_add_u32 m0,s79,102400\n"
TR "a[242:243],v1 offset:30720\n"
"ds_write_addtid_b32 v111 offset:24320\n"
MF "v[24:27],a[4:7],v[176:179],v[24:27]\n"
LD "v196,s[48:51],s80 offen lds\n"
MF "v[32:35],a[8:11],v[176:179],v[252:255]\n"
"s_cmp_eq_u32 s60,8\n"
"s_cselect_b32 s60,0,s60\n"
TR "a[244:245],v1 offset:32768\n"
MF "v[32:35],a[48:51],v[180:183],v[32:35]\n"
"s_cmp_eq_u32 s60,10\n"
"s_cselect_b32 s60,s61,s60\n"
TR "a[246:247],v1 offset:34816\n"
MF "v[32:35],a[88:91],v[184:187],v[32:35]\n"
"s_sub_u32 s65,s60,s64\n"
"ds_write_addtid_b32 v112 offset:24576\n"
"ds_write_addtid_b32 v113 offset:24832\n"
MF "v[40:43],a[92:95],v[184:187],v[252:255]\n"
"s_lshl_b32 s65,s65,7\n"
"ds_write_addtid_b32 v114 offset:25088\n"
"ds_write_addtid_b32 v115 offset:25344\n"
MF "v[40:43],a[52:55],v[180:183],v[40:43]\n"
LD "v197,s[48:51],s80 offen offset:1024 lds\n"
MF "v[40:43],a[12:15],v[176:179],v[40:43]\n"
"s_mov_b32 s64,s60\n"
"ds_write_addtid_b32 v116 offset:25600\n"
"ds_write_addtid_b32 v117 offset:25856\n"
MF "v[48:51],a[16:19],v[176:179],v[252:255]\n"
"s_nop 0\n"
"ds_write_addtid_b32 v118 offset:26112\n"
"ds_write_addtid_b32 v119 offset:26368\n"
MF "v[48:51],a[56:59],v[180:183],v[48:51]\n"
"s_nop 0\n"
"ds_write_addtid_b32 v120 offset:26624\n"
"ds_write_addtid_b32 v121 offset:26880\n"
MF "v[48:51],a[96:99],v[184:187],v[48:51]\n"
"s_waitcnt lgkmcnt(10)\n"
LD "v198,s[48:51],s80 offen offset:2048 lds\n"
MF "v[52:55],a[96:99],a[244:247],v[252:255]\n"
"s_nop 0\n"
TR "v[176:177],v2 offset:24576\n"
"ds_write_addtid_b32 v122 offset:27136\n"
MF "v[52:55],a[56:59],a[240:243],v[52:55]\n"
"s_nop 0\n"
TR "v[178:179],v2 offset:26624\n"
"ds_write_addtid_b32 v123 offset:27392\n"
MF "v[52:55],a[16:19],v[188:191],v[52:55]\n"
"s_nop 0\n"
"ds_write_addtid_b32 v124 offset:27648\n"
"ds_write_addtid_b32 v125 offset:27904\n"
MF "v[44:47],a[12:15],v[188:191],v[252:255]\n"
"s_nop 0\n"
TR "v[180:181],v2 offset:28672\n"
"ds_write_addtid_b32 v126 offset:28160\n"
MF "v[44:47],a[52:55],a[240:243],v[44:47]\n"
"s_nop 0\n"
TR "v[182:183],v2 offset:30720\n"
"ds_write_addtid_b32 v127 offset:28416\n"
MF "v[44:47],a[92:95],a[244:247],v[44:47]\n"
LD "v199,s[48:51],s80 offen offset:3072 lds\n"
MF "v[36:39],a[88:91],a[244:247],v[252:255]\n"
"s_nop 0\n"
TR "v[184:185],v2 offset:32768\n"
"ds_write_addtid_b32 v128 offset:28672\n"
MF "v[36:39],a[48:51],a[240:243],v[36:39]\n"
"s_add_u32 m0,s79,106496\n"
TR "v[186:187],v2 offset:34816\n"
"ds_write_addtid_b32 v129 offset:24832\n"
MF "v[36:39],a[8:11],v[188:191],v[36:39]\n"
LD "v200,s[48:51],s80 offen lds\n"
MF "v[28:31],a[4:7],v[188:191],v[252:255]\n"
"s_nop 0\n"
"ds_write_addtid_b32 v130 offset:25088\n"
"ds_write_addtid_b32 v131 offset:25344\n"
MF "v[28:31],a[44:47],a[240:243],v[28:31]\n"
"s_nop 0\n"
"ds_write_addtid_b32 v132 offset:25600\n"
"ds_write_addtid_b32 v133 offset:25856\n"
MF "v[28:31],a[84:87],a[244:247],v[28:31]\n"
"s_nop 0\n"
"ds_write_addtid_b32 v134 offset:26112\n"
"ds_write_addtid_b32 v135 offset:26368\n"
MF "v[20:23],a[80:83],a[244:247],v[252:255]\n"
MF "v[20:23],a[40:43],a[240:243],v[20:23]\n"
LD "v201,s[48:51],s80 offen offset:1024 lds\n"
MF "v[20:23],a[0:3],v[188:191],v[20:23]\n"
"s_waitcnt lgkmcnt(7)\n"
MF "v[56:59],a[0:3],v[176:179],v[252:255]\n"
TR "v[188:189],v3 offset:24576\n"
MF "v[56:59],a[40:43],v[180:183],v[56:59]\n"
TR "v[190:191],v3 offset:26624\n"
MF "v[56:59],a[80:83],v[184:187],v[56:59]\n"
LD "v202,s[48:51],s80 offen offset:2048 lds\n"
MF "v[64:67],a[84:87],v[184:187],v[252:255]\n"
TR "a[240:241],v3 offset:28672\n"
MF "v[64:67],a[44:47],v[180:183],v[64:67]\n"
TR "a[242:243],v3 offset:30720\n"
MF "v[64:67],a[4:7],v[176:179],v[64:67]\n"
LD "v203,s[48:51],s80 offen offset:3072 lds\n"
MF "v[72:75],a[8:11],v[176:179],v[252:255]\n"
TR "a[244:245],v3 offset:32768\n"
MF "v[72:75],a[48:51],v[180:183],v[72:75]\n"
TR "a[246:247],v3 offset:34816\n"
MF "v[72:75],a[88:91],v[184:187],v[72:75]\n"
MF "v[80:83],a[92:95],v[184:187],v[252:255]\n"
MF "v[80:83],a[52:55],v[180:183],v[80:83]\n"
MF "v[80:83],a[12:15],v[176:179],v[80:83]\n"
MF "v[88:91],a[16:19],v[176:179],v[252:255]\n"
MF "v[88:91],a[56:59],v[180:183],v[88:91]\n"
"s_waitcnt vmcnt(31) lgkmcnt(0)\n"
MF "v[88:91],a[96:99],v[184:187],v[88:91]\n"
"s_barrier\n"
MF "v[92:95],a[96:99],a[244:247],v[252:255]\n"
"s_add_u32 s89,s89,s65\n"
TR "v[176:177],v0\n"
MF "v[92:95],a[56:59],a[240:243],v[92:95]\n"
"s_add_u32 m0,s79,24576\n"
TR "v[178:179],v0 offset:2048\n"
MF "v[92:95],a[16:19],v[188:191],v[92:95]\n"
LD "v192,s[48:51],s89 offen lds\n"
MF "v[84:87],a[12:15],v[188:191],v[252:255]\n"
TR "v[180:181],v0 offset:4096\n"
MF "v[84:87],a[52:55],a[240:243],v[84:87]\n"
TR "v[182:183],v0 offset:6144\n"
MF "v[84:87],a[92:95],a[244:247],v[84:87]\n"
".Lpg29h:\n"
"s_cbranch_execz .Lpg32h\n"
LD "v193,s[48:51],s89 offen offset:1024 lds\n"
MF "v[76:79],a[88:91],a[244:247],v[252:255]\n"
TR "v[184:185],v0 offset:8192\n"
MF "v[76:79],a[48:51],a[240:243],v[76:79]\n"
"s_nop 0\n"
TR "v[186:187],v0 offset:10240\n"
"ds_write_addtid_b32 v136 offset:49152\n"
MF "v[76:79],a[8:11],v[188:191],v[76:79]\n"
"s_nop 0\n"
"ds_write_addtid_b32 v137 offset:49408\n"
"ds_write_addtid_b32 v138 offset:49664\n"
MF "v[68:71],a[4:7],v[188:191],v[252:255]\n"
LD "v194,s[48:51],s89 offen offset:2048 lds\n"
MF "v[68:71],a[44:47],a[240:243],v[68:71]\n"
"buffer_load_dword v10,v12,s[40:43],s86 offen sc1\n"
MF "v[68:71],a[84:87],a[244:247],v[68:71]\n"
"s_nop 0\n"
"ds_write_addtid_b32 v139 offset:49920\n"
"ds_write_addtid_b32 v140 offset:50176\n"
MF "v[60:63],a[80:83],a[244:247],v[252:255]\n"
"s_nop 0\n"
"ds_write_addtid_b32 v141 offset:50432\n"
"ds_write_addtid_b32 v142 offset:50688\n"
MF "v[60:63],a[40:43],a[240:243],v[60:63]\n"
"s_nop 0\n"
"ds_write_addtid_b32 v143 offset:50944\n"
"ds_write_addtid_b32 v144 offset:51200\n"
MF "v[60:63],a[0:3],v[188:191],v[60:63]\n"
"s_waitcnt lgkmcnt(9)\n"
LD "v195,s[48:51],s89 offen offset:3072 lds\n"
MF "v[16:19],a[120:123],v[176:179],v[16:19]\n"
"s_nop 0\n"
TR "v[188:189],v1\n"
"ds_write_addtid_b32 v145 offset:51456\n"
MF "v[16:19],a[160:163],v[180:183],v[16:19]\n"
"s_nop 0\n"
TR "v[190:191],v1 offset:2048\n"
"ds_write_addtid_b32 v146 offset:51712\n"
MF "v[16:19],a[200:203],v[184:187],v[16:19]\n"
"s_nop 0\n"
"ds_write_addtid_b32 v147 offset:51968\n"
"ds_write_addtid_b32 v148 offset:52224\n"
MF "v[24:27],a[204:207],v[184:187],v[24:27]\n"
"s_nop 0\n"
TR "a[240:241],v1 offset:4096\n"
"ds_write_addtid_b32 v149 offset:52480\n"
MF "v[24:27],a[164:167],v[180:183],v[24:27]\n"
"s_add_u32 m0,s79,28672\n"
TR "a[242:243],v1 offset:6144\n"
"ds_write_addtid_b32 v150 offset:48640\n"
MF "v[24:27],a[124:127],v[176:179],v[24:27]\n"
LD "v196,s[48:51],s89 offen lds\n"
MF "v[32:35],a[128:131],v[176:179],v[32:35]\n"
"s_nop 0\n"
TR "a[244:245],v1 offset:8192\n"
"ds_write_addtid_b32 v151 offset:48896\n"
MF "v[32:35],a[168:171],v[180:183],v[32:35]\n"
"s_nop 0\n"
TR "a[246:247],v1 offset:10240\n"
"ds_write_addtid_b32 v152 offset:49152\n"
MF "v[32:35],a[208:211],v[184:187],v[32:35]\n"
LD "v197,s[48:51],s89 offen offset:1024 lds\n"
MF "v[40:43],a[212:215],v[184:187],v[40:43]\n"
"s_nop 0\n"
"ds_write_addtid_b32 v153 offset:49408\n"
"ds_write_addtid_b32 v154 offset:49664\n"
MF "v[40:43],a[172:175],v[180:183],v[40:43]\n"
"s_nop 0\n"
"ds_write_addtid_b32 v155 offset:49920\n"
"ds_write_addtid_b32 v156 offset:50176\n"
MF "v[40:43],a[132:135],v[176:179],v[40:43]\n"
"s_nop 0\n"
"ds_write_addtid_b32 v157 offset:50432\n"
"ds_write_addtid_b32 v158 offset:50688\n"
MF "v[48:51],a[136:139],v[176:179],v[48:51]\n"
"s_nop 0\n"
"ds_write_addtid_b32 v159 offset:50944\n"
"ds_write_addtid_b32 v160 offset:51200\n"
MF "v[48:51],a[176:179],v[180:183],v[48:51]\n"
LD "v198,s[48:51],s89 offen offset:2048 lds\n"
MF "v[48:51],a[216:219],v[184:187],v[48:51]\n"
"s_waitcnt lgkmcnt(9)\n"
"ds_write_addtid_b32 v161 offset:51456\n"
"ds_write_addtid_b32 v162 offset:51712\n"
MF "v[52:55],a[216:219],a[244:247],v[52:55]\n"
"s_nop 0\n"
TR "v[176:177],v2\n"
"ds_write_addtid_b32 v163 offset:51968\n"
MF "v[52:55],a[176:179],a[240:243],v[52:55]\n"
"s_nop 0\n"
TR "v[178:179],v2 offset:2048\n"
"ds_write_addtid_b32 v164 offset:52224\n"
MF "v[52:55],a[136:139],v[188:191],v[52:55]\n"
LD "v199,s[48:51],s89 offen offset:3072 lds\n"
MF "v[44:47],a[132:135],v[188:191],v[44:47]\n"
"s_nop 0\n"
TR "v[180:181],v2 offset:4096\n"
"ds_write_addtid_b32 v165 offset:52480\n"
MF "v[44:47],a[172:175],a[240:243],v[44:47]\n"
"s_nop 0\n"
TR "v[182:183],v2 offset:6144\n"
"ds_write_addtid_b32 v166 offset:52736\n"
MF "v[44:47],a[212:215],a[244:247],v[44:47]\n"
"s_nop 0\n"
"ds_write_addtid_b32 v167 offset:52992\n"
"ds_write_addtid_b32 v168 offset:53248\n"
MF "v[36:39],a[208:211],a[244:247],v[36:39]\n"
"s_nop 0\n"
TR "v[184:185],v2 offset:8192\n"
"ds_write_addtid_b32 v169 offset:53504\n"
MF "v[36:39],a[168:171],a[240:243],v[36:39]\n"
"s_add_u32 m0,s79,32768\n"
TR "v[186:187],v2 offset:10240\n"
"ds_write_addtid_b32 v170 offset:49664\n"
MF "v[36:39],a[128:131],v[188:191],v[36:39]\n"
LD "v200,s[48:51],s89 offen lds\n"
MF "v[28:31],a[124:127],v[188:191],v[28:31]\n"
"s_nop 0\n"
"ds_write_addtid_b32 v171 offset:49920\n"
"ds_write_addtid_b32 v172 offset:50176\n"
MF "v[28:31],a[164:167],a[240:243],v[28:31]\n"
"s_nop 0\n"
"ds_write_addtid_b32 v173 offset:50432\n"
"ds_write_addtid_b32 v174 offset:50688\n"
MF "v[28:31],a[204:207],a[244:247],v[28:31]\n"
"ds_write_addtid_b32 v175 offset:50944\n"
MF "v[20:23],a[200:203],a[244:247],v[20:23]\n"
LD "v201,s[48:51],s89 offen offset:1024 lds\n"
MF "v[20:23],a[160:163],a[240:243],v[20:23]\n"
MF "v[20:23],a[120:123],v[188:191],v[20:23]\n"
"s_waitcnt lgkmcnt(6)\n"
MF "v[56:59],a[120:123],v[176:179],v[56:59]\n"
TR "v[188:189],v3\n"
MF "v[56:59],a[160:163],v[180:183],v[56:59]\n"
TR "v[190:191],v3 offset:2048\n"
MF "v[56:59],a[200:203],v[184:187],v[56:59]\n"
LD "v202,s[48:51],s89 offen offset:2048 lds\n"
MF "v[64:67],a[204:207],v[184:187],v[64:67]\n"
TR "a[240:241],v3 offset:4096\n"
MF "v[64:67],a[164:167],v[180:183],v[64:67]\n"
TR "a[242:243],v3 offset:6144\n"
MF "v[64:67],a[124:127],v[176:179],v[64:67]\n"
LD "v203,s[48:51],s89 offen offset:3072 lds\n"
MF "v[72:75],a[128:131],v[176:179],v[72:75]\n"
TR "a[244:245],v3 offset:8192\n"
MF "v[72:75],a[168:171],v[180:183],v[72:75]\n"
TR "a[246:247],v3 offset:10240\n"
MF "v[72:75],a[208:211],v[184:187],v[72:75]\n"
MF "v[80:83],a[212:215],v[184:187],v[80:83]\n"
MF "v[80:83],a[172:175],v[180:183],v[80:83]\n"
MF "v[80:83],a[132:135],v[176:179],v[80:83]\n"
MF "v[88:91],a[136:139],v[176:179],v[88:91]\n"
MF "v[88:91],a[176:179],v[180:183],v[88:91]\n"
"s_waitcnt vmcnt(27) lgkmcnt(0)\n"
MF "v[88:91],a[216:219],v[184:187],v[88:91]\n"
"s_barrier\n"
MF "v[92:95],a[216:219],a[244:247],v[92:95]\n"
"s_add_u32 s80,s89,122880\n"
TR "v[176:177],v0 offset:49152\n"
MF "v[92:95],a[176:179],a[240:243],v[92:95]\n"
"s_add_u32 m0,s79,0\n"
"s_mov_b32 s82,s70\n"
TR "v[178:179],v0 offset:51200\n"
MF "v[92:95],a[136:139],v[188:191],v[92:95]\n"
LD "v192,s[48:51],s80 offen lds\n"
MF "v[84:87],a[132:135],v[188:191],v[84:87]\n"
TR "v[180:181],v0 offset:53248\n"
MF "v[84:87],a[172:175],a[240:243],v[84:87]\n"
TR "v[182:183],v0 offset:55296\n"
MF "v[84:87],a[212:215],a[244:247],v[84:87]\n"
LD "v193,s[48:51],s80 offen offset:1024 lds\n"
MF "v[76:79],a[208:211],a[244:247],v[76:79]\n"
TR "v[184:185],v0 offset:57344\n"
MF "v[76:79],a[168:171],a[240:243],v[76:79]\n"
TR "v[186:187],v0 offset:59392\n"
MF "v[76:79],a[128:131],v[188:191],v[76:79]\n"
MF "v[68:71],a[124:127],v[188:191],v[68:71]\n"
MF "v[68:71],a[164:167],a[240:243],v[68:71]\n"
LD "v194,s[48:51],s80 offen offset:2048 lds\n"
MF "v[68:71],a[204:207],a[244:247],v[68:71]\n"
MF "v[60:63],a[200:203],a[244:247],v[60:63]\n"
MF "v[60:63],a[160:163],a[240:243],v[60:63]\n"
MF "v[60:63],a[120:123],v[188:191],v[60:63]\n"
"s_waitcnt lgkmcnt(0)\n"
LD "v195,s[48:51],s80 offen offset:3072 lds\n"
MF "v[16:19],a[20:23],v[176:179],v[16:19]\n"
TR "v[188:189],v1 offset:49152\n"
MF "v[16:19],a[60:63],v[180:183],v[16:19]\n"
TR "v[190:191],v1 offset:51200\n"
MF "v[16:19],a[100:103],v[184:187],v[16:19]\n"
MF "v[24:27],a[104:107],v[184:187],v[24:27]\n"
TR "a[240:241],v1 offset:53248\n"
MF "v[24:27],a[64:67],v[180:183],v[24:27]\n"
"s_add_u32 m0,s79,4096\n"
TR "a[242:243],v1 offset:55296\n"
MF "v[24:27],a[24:27],v[176:179],v[24:27]\n"
LD "v196,s[48:51],s80 offen lds\n"
MF "v[32:35],a[28:31],v[176:179],v[32:35]\n"
TR "a[244:245],v1 offset:57344\n"
MF "v[32:35],a[68:71],v[180:183],v[32:35]\n"
TR "a[246:247],v1 offset:59392\n"
MF "v[32:35],a[108:111],v[184:187],v[32:35]\n"
LD "v197,s[48:51],s80 offen offset:1024 lds\n"
MF "v[40:43],a[112:115],v[184:187],v[40:43]\n"
MF "v[40:43],a[72:75],v[180:183],v[40:43]\n"
MF "v[40:43],a[32:35],v[176:179],v[40:43]\n"
MF "v[48:51],a[36:39],v[176:179],v[48:51]\n"
MF "v[48:51],a[76:79],v[180:183],v[48:51]\n"
LD "v198,s[48:51],s80 offen offset:2048 lds\n"
MF "v[48:51],a[116:119],v[184:187],v[48:51]\n"
"s_waitcnt lgkmcnt(0)\n"
MF "v[52:55],a[116:119],a[244:247],v[52:55]\n"
TR "v[176:177],v2 offset:49152\n"
MF "v[52:55],a[76:79],a[240:243],v[52:55]\n"
TR "v[178:179],v2 offset:51200\n"
MF "v[52:55],a[36:39],v[188:191],v[52:55]\n"
LD "v199,s[48:51],s80 offen offset:3072 lds\n"
MF "v[44:47],a[32:35],v[188:191],v[44:47]\n"
TR "v[180:181],v2 offset:53248\n"
MF "v[44:47],a[72:75],a[240:243],v[44:47]\n"
TR "v[182:183],v2 offset:55296\n"
MF "v[44:47],a[112:115],a[244:247],v[44:47]\n"
MF "v[36:39],a[108:111],a[244:247],v[36:39]\n"
TR "v[184:185],v2 offset:57344\n"
MF "v[36:39],a[68:71],a[240:243],v[36:39]\n"
"s_add_u32 m0,s79,8192\n"
TR "v[186:187],v2 offset:59392\n"
MF "v[36:39],a[28:31],v[188:191],v[36:39]\n"
LD "v200,s[48:51],s80 offen lds\n"
MF "v[28:31],a[24:27],v[188:191],v[28:31]\n"
MF "v[28:31],a[64:67],a[240:243],v[28:31]\n"
MF "v[28:31],a[104:107],a[244:247],v[28:31]\n"
MF "v[20:23],a[100:103],a[244:247],v[20:23]\n"
LD "v201,s[48:51],s80 offen offset:1024 lds\n"
MF "v[20:23],a[60:63],a[240:243],v[20:23]\n"
"s_waitcnt vmcnt(19)\n"
MF "v[20:23],a[20:23],v[188:191],v[20:23]\n"
"v_mov_b32 v236,v228\n"
"v_mov_b32 v237,v229\n"
MF "v[16:19],a[252:255],v[208:211],v[16:19]\n"
MF "v[24:27],a[252:255],v[212:215],v[24:27]\n"
MF "v[32:35],a[252:255],v[216:219],v[32:35]\n"
MF "v[40:43],a[252:255],v[220:223],v[40:43]\n"
MF "v[48:51],a[252:255],v[224:227],v[48:51]\n"
MF "v[20:23],a[248:251],v[208:211],v[20:23]\n"
MF "v[28:31],a[248:251],v[212:215],v[28:31]\n"
MF "v[36:39],a[248:251],v[216:219],v[36:39]\n"
MF "v[44:47],a[248:251],v[220:223],v[44:47]\n"
MF "v[52:55],a[248:251],v[224:227],v[52:55]\n"
MF "v[248:251],v[230:233],a[250:253],v[248:251]\n"
MF "v[244:247],v[234:237],a[250:253],v[244:247]\n"
"s_waitcnt lgkmcnt(0)\n"
MF "v[56:59],a[20:23],v[176:179],v[56:59]\n"
TR "v[188:189],v3 offset:49152\n"
"v_sub_u32 v10,v10,v11\n"
MF "v[56:59],a[60:63],v[180:183],v[56:59]\n"
TR "v[190:191],v3 offset:51200\n"
MF "v[56:59],a[100:103],v[184:187],v[56:59]\n"
LD "v202,s[48:51],s80 offen offset:2048 lds\n"
MF "v[64:67],a[104:107],v[184:187],v[64:67]\n"
TR "a[240:241],v3 offset:53248\n"
MF "v[64:67],a[64:67],v[180:183],v[64:67]\n"
TR "a[242:243],v3 offset:55296\n"
MF "v[64:67],a[24:27],v[176:179],v[64:67]\n"
LD "v203,s[48:51],s80 offen offset:3072 lds\n"
MF "v[72:75],a[28:31],v[176:179],v[72:75]\n"
TR "a[244:245],v3 offset:57344\n"
MF "v[72:75],a[68:71],v[180:183],v[72:75]\n"
TR "a[246:247],v3 offset:59392\n"
MF "v[72:75],a[108:111],v[184:187],v[72:75]\n"
".Lnf2:\n"
"v_readlane_b32 s58,v10,s67\n"
"s_cmp_lt_u32 s58,s85\n"
"s_cbranch_scc1 .Lsp2\n"
".Lnb2:\n"
MF "v[80:83],a[112:115],v[184:187],v[80:83]\n"
"s_add_u32 s70,s70,20480\n"
MF "v[80:83],a[72:75],v[180:183],v[80:83]\n"
MF "v[80:83],a[32:35],v[176:179],v[80:83]\n"
MF "v[88:91],a[36:39],v[176:179],v[88:91]\n"
"s_add_u32 s2,s2,204800\n"
MF "v[88:91],a[76:79],v[180:183],v[88:91]\n"
"s_waitcnt vmcnt(25) lgkmcnt(0)\n"
LD "v[228:231],v9,s[40:43],s2 offen sc1\n"
MF "v[88:91],a[116:119],v[184:187],v[88:91]\n"
"s_barrier\n"
MF "v[92:95],a[116:119],a[244:247],v[92:95]\n"
"s_add_u32 s88,s88,s65\n"
TR "v[176:177],v4\n"
MF "v[92:95],a[76:79],a[240:243],v[92:95]\n"
"s_add_u32 m0,s79,49152\n"
TR "v[178:179],v4 offset:2048\n"
MF "v[92:95],a[36:39],v[188:191],v[92:95]\n"
LD "v192,s[48:51],s88 offen lds\n"
MF "v[84:87],a[32:35],v[188:191],v[84:87]\n"
TR "v[180:181],v4 offset:4096\n"
MF "v[84:87],a[72:75],a[240:243],v[84:87]\n"
TR "v[182:183],v4 offset:6144\n"
MF "v[84:87],a[112:115],a[244:247],v[84:87]\n"
LD "v193,s[48:51],s88 offen offset:1024 lds\n"
MF "v[76:79],a[108:111],a[244:247],v[76:79]\n"
TR "v[184:185],v4 offset:8192\n"
MF "v[76:79],a[68:71],a[240:243],v[76:79]\n"
"s_add_u32 s2,s2,204800\n"
TR "v[186:187],v4 offset:10240\n"
MF "v[76:79],a[28:31],v[188:191],v[76:79]\n"
LD "v[232:235],v9,s[40:43],s2 offen sc1\n"
MF "v[68:71],a[24:27],v[188:191],v[68:71]\n"
MF "v[68:71],a[64:67],a[240:243],v[68:71]\n"
LD "v194,s[48:51],s88 offen offset:2048 lds\n"
MF "v[68:71],a[104:107],a[244:247],v[68:71]\n"
MF "v[60:63],a[100:103],a[244:247],v[60:63]\n"
MF "v[60:63],a[60:63],a[240:243],v[60:63]\n"
MF "v[60:63],a[20:23],v[188:191],v[60:63]\n"
"s_waitcnt lgkmcnt(0)\n"
LD "v195,s[48:51],s88 offen offset:3072 lds\n"
MF "v[16:19],a[140:143],v[176:179],v[16:19]\n"
TR "v[188:189],v5\n"
MF "v[16:19],a[180:183],v[180:183],v[16:19]\n"
TR "v[190:191],v5 offset:2048\n"
MF "v[16:19],a[220:223],v[184:187],v[16:19]\n"
LD "v[208:211],v9,s[40:43],s82 offen sc1\n"
MF "v[24:27],a[224:227],v[184:187],v[24:27]\n"
TR "a[240:241],v5 offset:4096\n"
MF "v[24:27],a[184:187],v[180:183],v[24:27]\n"
"s_add_u32 m0,s79,53248\n"
TR "a[242:243],v5 offset:6144\n"
MF "v[24:27],a[144:147],v[176:179],v[24:27]\n"
LD "v196,s[48:51],s88 offen lds\n"
MF "v[32:35],a[148:151],v[176:179],v[32:35]\n"
TR "a[244:245],v5 offset:8192\n"
MF "v[32:35],a[188:191],v[180:183],v[32:35]\n"
TR "a[246:247],v5 offset:10240\n"
MF "v[32:35],a[228:231],v[184:187],v[32:35]\n"
LD "v197,s[48:51],s88 offen offset:1024 lds\n"
MF "v[40:43],a[232:235],v[184:187],v[40:43]\n"
MF "v[40:43],a[192:195],v[180:183],v[40:43]\n"
MF "v[40:43],a[152:155],v[176:179],v[40:43]\n"
MF "v[48:51],a[156:159],v[176:179],v[48:51]\n"
LD "v[212:215],v9,s[40:43],s82 offen offset:1024 sc1\n"
MF "v[48:51],a[196:199],v[180:183],v[48:51]\n"
LD "v198,s[48:51],s88 offen offset:2048 lds\n"
MF "v[48:51],a[236:239],v[184:187],v[48:51]\n"
"s_waitcnt lgkmcnt(0)\n"
MF "v[52:55],a[236:239],a[244:247],v[52:55]\n"
TR "v[176:177],v6\n"
MF "v[52:55],a[196:199],a[240:243],v[52:55]\n"
TR "v[178:179],v6 offset:2048\n"
MF "v[52:55],a[156:159],v[188:191],v[52:55]\n"
LD "v199,s[48:51],s88 offen offset:3072 lds\n"
MF "v[44:47],a[152:155],v[188:191],v[44:47]\n"
TR "v[180:181],v6 offset:4096\n"
MF "v[44:47],a[192:195],a[240:243],v[44:47]\n"
TR "v[182:183],v6 offset:6144\n"
MF "v[44:47],a[232:235],a[244:247],v[44:47]\n"
MF "v[36:39],a[228:231],a[244:247],v[36:39]\n"
TR "v[184:185],v6 offset:8192\n"
MF "v[36:39],a[188:191],a[240:243],v[36:39]\n"
"s_add_u32 m0,s79,57344\n"
TR "v[186:187],v6 offset:10240\n"
MF "v[36:39],a[148:151],v[188:191],v[36:39]\n"
LD "v200,s[48:51],s88 offen lds\n"
MF "v[28:31],a[144:147],v[188:191],v[28:31]\n"
LD "v[216:219],v9,s[40:43],s82 offen offset:2048 sc1\n"
MF "v[28:31],a[184:187],a[240:243],v[28:31]\n"
MF "v[28:31],a[224:227],a[244:247],v[28:31]\n"
MF "v[20:23],a[220:223],a[244:247],v[20:23]\n"
LD "v201,s[48:51],s88 offen offset:1024 lds\n"
MF "v[20:23],a[180:183],a[240:243],v[20:23]\n"
MF "v[20:23],a[140:143],v[188:191],v[20:23]\n"
"s_waitcnt lgkmcnt(0)\n"
MF "v[56:59],a[140:143],v[176:179],v[56:59]\n"
TR "v[188:189],v7\n"
MF "v[56:59],a[180:183],v[180:183],v[56:59]\n"
TR "v[190:191],v7 offset:2048\n"
MF "v[56:59],a[220:223],v[184:187],v[56:59]\n"
LD "v202,s[48:51],s88 offen offset:2048 lds\n"
MF "v[64:67],a[224:227],v[184:187],v[64:67]\n"
TR "a[240:241],v7 offset:4096\n"
MF "v[64:67],a[184:187],v[180:183],v[64:67]\n"
TR "a[242:243],v7 offset:6144\n"
MF "v[64:67],a[144:147],v[176:179],v[64:67]\n"
LD "v203,s[48:51],s88 offen offset:3072 lds\n"
MF "v[72:75],a[148:151],v[176:179],v[72:75]\n"
TR "a[244:245],v7 offset:8192\n"
MF "v[72:75],a[188:191],v[180:183],v[72:75]\n"
TR "a[246:247],v7 offset:10240\n"
MF "v[72:75],a[228:231],v[184:187],v[72:75]\n"
LD "v[220:223],v9,s[40:43],s82 offen offset:3072 sc1\n"
MF "v[80:83],a[232:235],v[184:187],v[80:83]\n"
MF "v[80:83],a[192:195],v[180:183],v[80:83]\n"
MF "v[80:83],a[152:155],v[176:179],v[80:83]\n"
MF "v[88:91],a[156:159],v[176:179],v[88:91]\n"
"s_add_u32 s82,s82,4096\n"
MF "v[88:91],a[196:199],v[180:183],v[88:91]\n"
"s_waitcnt vmcnt(30) lgkmcnt(0)\n"
LD "v[224:227],v9,s[40:43],s82 offen sc1\n"
MF "v[88:91],a[236:239],v[184:187],v[88:91]\n"
"s_barrier\n"
MF "v[92:95],a[236:239],a[244:247],v[92:95]\n"
"s_add_u32 s80,s88,122880\n"
TR "v[176:177],v0 offset:24576\n"
MF "v[92:95],a[196:199],a[240:243],v[92:95]\n"
"s_add_u32 m0,s79,98304\n"
"s_add_u32 s85,s85,1\n"
TR "v[178:179],v0 offset:26624\n"
MF "v[92:95],a[156:159],v[188:191],v[92:95]\n"
LD "v192,s[48:51],s80 offen lds\n"
MF "v[84:87],a[152:155],v[188:191],v[84:87]\n"
"s_nop 0\n"
TR "v[180:181],v0 offset:28672\n"
"ds_write_addtid_b32 v16 offset:24576\n"
MF "v[84:87],a[192:195],a[240:243],v[84:87]\n"
"s_nop 0\n"
TR "v[182:183],v0 offset:30720\n"
"ds_write_addtid_b32 v17 offset:24832\n"
MF "v[84:87],a[232:235],a[244:247],v[84:87]\n"
LD "v193,s[48:51],s80 offen offset:1024 lds\n"
MF "v[76:79],a[228:231],a[244:247],v[76:79]\n"
"s_nop 0\n"
TR "v[184:185],v0 offset:32768\n"
"ds_write_addtid_b32 v18 offset:25088\n"
MF "v[76:79],a[188:191],a[240:243],v[76:79]\n"
"s_nop 0\n"
TR "v[186:187],v0 offset:34816\n"
"ds_write_addtid_b32 v19 offset:25344\n"
MF "v[76:79],a[148:151],v[188:191],v[76:79]\n"
"s_nop 0\n"
"ds_write_addtid_b32 v20 offset:25600\n"
"ds_write_addtid_b32 v21 offset:25856\n"
MF "v[68:71],a[144:147],v[188:191],v[68:71]\n"
LD "v194,s[48:51],s80 offen offset:2048 lds\n"
MF "v[68:71],a[184:187],a[240:243],v[68:71]\n"
"s_nop 0\n"
"ds_write_addtid_b32 v22 offset:26112\n"
"ds_write_addtid_b32 v23 offset:26368\n"
MF "v[68:71],a[224:227],a[244:247],v[68:71]\n"
"s_nop 0\n"
"ds_write_addtid_b32 v24 offset:26624\n"
"ds_write_addtid_b32 v25 offset:26880\n"
MF "v[60:63],a[220:223],a[244:247],v[60:63]\n"
"s_nop 0\n"
"ds_write_addtid_b32 v26 offset:27136\n"
"ds_write_addtid_b32 v27 offset:27392\n"
MF "v[60:63],a[180:183],a[240:243],v[60:63]\n"
MF "v[60:63],a[140:143],v[188:191],v[60:63]\n"
"s_waitcnt lgkmcnt(9)\n"
"s_cmp_lg_u32 s85,9\n"
"s_cbranch_scc1 .Lpl09\n"
".Lplast0:\n"
MF "v[96:99],v[176:179],a[0:3],v[252:255]\n"
"s_nop 0\n"
TR "v[188:189],v1 offset:24576\n"
TR "v[190:191],v1 offset:26624\n"
MF "v[96:99],v[180:183],a[40:43],v[96:99]\n"
"s_nop 0\n"
TR "a[240:241],v1 offset:28672\n"
TR "a[242:243],v1 offset:30720\n"
MF "v[96:99],v[184:187],a[80:83],v[96:99]\n"
"s_add_u32 s90,s91,163840\n"
TR "a[244:245],v1 offset:32768\n"
TR "a[246:247],v1 offset:34816\n"
MF "v[104:107],v[184:187],a[84:87],v[252:255]\n"
LD "v195,s[48:51],s80 offen offset:3072 lds\n"
MF "v[104:107],v[180:183],a[44:47],v[104:107]\n"
"s_nop 0\n"
"ds_write_addtid_b32 v28 offset:27648\n"
"ds_write_addtid_b32 v29 offset:27904\n"
MF "v[104:107],v[176:179],a[4:7],v[104:107]\n"
"s_nop 0\n"
"ds_write_addtid_b32 v30 offset:28160\n"
"ds_write_addtid_b32 v31 offset:28416\n"
MF "v[112:115],v[176:179],a[8:11],v[252:255]\n"
"s_nop 0\n"
"ds_write_addtid_b32 v32 offset:28672\n"
"ds_write_addtid_b32 v33 offset:28928\n"
MF "v[112:115],v[180:183],a[48:51],v[112:115]\n"
"s_add_u32 m0,s79,102400\n"
"s_nop 0\n"
"ds_write_addtid_b32 v34 offset:25088\n"
"ds_write_addtid_b32 v35 offset:25344\n"
MF "v[112:115],v[184:187],a[88:91],v[112:115]\n"
LD "v196,s[48:51],s80 offen lds\n"
MF "v[120:123],v[184:187],a[92:95],v[252:255]\n"
"s_nop 0\n"
"ds_write_addtid_b32 v36 offset:25600\n"
"ds_write_addtid_b32 v37 offset:25856\n"
MF "v[120:123],v[180:183],a[52:55],v[120:123]\n"
"ds_write_addtid_b32 v38 offset:26112\n"
MF "v[120:123],v[176:179],a[12:15],v[120:123]\n"
"ds_write_addtid_b32 v39 offset:26368\n"
MF "v[128:131],v[176:179],a[16:19],v[252:255]\n"
"ds_write_addtid_b32 v40 offset:26624\n"
MF "v[128:131],v[180:183],a[56:59],v[128:131]\n"
"ds_write_addtid_b32 v41 offset:26880\n"
MF "v[128:131],v[184:187],a[96:99],v[128:131]\n"
"s_waitcnt lgkmcnt(14)\n"
LD "v197,s[48:51],s80 offen offset:1024 lds\n"
MF "v[132:135],a[244:247],a[96:99],v[252:255]\n"
"s_nop 0\n"
TR "v[176:177],v2 offset:24576\n"
TR "v[178:179],v2 offset:26624\n"
MF "v[132:135],a[240:243],a[56:59],v[132:135]\n"
"s_nop 0\n"
TR "v[180:181],v2 offset:28672\n"
TR "v[182:183],v2 offset:30720\n"
MF "v[132:135],v[188:191],a[16:19],v[132:135]\n"
"s_nop 0\n"
TR "v[184:185],v2 offset:32768\n"
TR "v[186:187],v2 offset:34816\n"
MF "v[124:127],v[188:191],a[12:15],v[252:255]\n"
"s_nop 0\n"
"ds_write_addtid_b32 v42 offset:27136\n"
"ds_write_addtid_b32 v43 offset:27392\n"
MF "v[124:127],a[240:243],a[52:55],v[124:127]\n"
"s_nop 0\n"
"ds_write_addtid_b32 v44 offset:27648\n"
"ds_write_addtid_b32 v45 offset:27904\n"
MF "v[124:127],a[244:247],a[92:95],v[124:127]\n"
"s_nop 0\n"
"ds_write_addtid_b32 v46 offset:28160\n"
"ds_write_addtid_b32 v47 offset:28416\n"
MF "v[116:119],a[244:247],a[88:91],v[252:255]\n"
LD "v198,s[48:51],s80 offen offset:2048 lds\n"
MF "v[116:119],a[240:243],a[48:51],v[116:119]\n"
"s_nop 0\n"
"ds_write_addtid_b32 v48 offset:28672\n"
"ds_write_addtid_b32 v49 offset:28928\n"
MF "v[116:119],v[188:191],a[8:11],v[116:119]\n"
"s_nop 0\n"
"ds_write_addtid_b32 v50 offset:29184\n"
"ds_write_addtid_b32 v51 offset:29440\n"
MF "v[108:111],v[188:191],a[4:7],v[252:255]\n"
"ds_write_addtid_b32 v52 offset:29696\n"
MF "v[108:111],a[240:243],a[44:47],v[108:111]\n"
"ds_write_addtid_b32 v53 offset:29952\n"
MF "v[108:111],a[244:247],a[84:87],v[108:111]\n"
"ds_write_addtid_b32 v54 offset:30208\n"
MF "v[100:103],a[244:247],a[80:83],v[252:255]\n"
"ds_write_addtid_b32 v55 offset:30464\n"
MF "v[100:103],a[240:243],a[40:43],v[100:103]\n"
LD "v199,s[48:51],s80 offen offset:3072 lds\n"
MF "v[100:103],v[188:191],a[0:3],v[100:103]\n"
"s_waitcnt lgkmcnt(14)\n"
MF "v[136:139],v[176:179],a[0:3],v[252:255]\n"
"s_nop 0\n"
TR "v[188:189],v3 offset:24576\n"
TR "v[190:191],v3 offset:26624\n"
MF "v[136:139],v[180:183],a[40:43],v[136:139]\n"
"s_add_u32 m0,s79,106496\n"
TR "a[240:241],v3 offset:28672\n"
TR "a[242:243],v3 offset:30720\n"
MF "v[136:139],v[184:187],a[80:83],v[136:139]\n"
".Lpg30h:\n"
"s_cbranch_execz .Lpg33h\n"
"s_nop 0\n"
TR "a[244:245],v3 offset:32768\n"
TR "a[246:247],v3 offset:34816\n"
"s_waitcnt lgkmcnt(0)\n"
MF "v[144:147],v[184:187],a[84:87],v[252:255]\n"
"s_barrier\n"
MF "v[144:147],v[180:183],a[44:47],v[144:147]\n"
LD "v200,s[48:51],s80 offen lds\n"
MF "v[144:147],v[176:179],a[4:7],v[144:147]\n"
"ds_read2st64_b32 v[16:17],v13 offset0:0 offset1:1\n"
MF "v[152:155],v[176:179],a[8:11],v[252:255]\n"
"ds_read2st64_b32 v[18:19],v13 offset0:2 offset1:3\n"
MF "v[152:155],v[180:183],a[48:51],v[152:155]\n"
"ds_read2st64_b32 v[20:21],v13 offset0:4 offset1:5\n"
MF "v[152:155],v[184:187],a[88:91],v[152:155]\n"
"ds_read2st64_b32 v[22:23],v13 offset0:6 offset1:7\n"
MF "v[160:163],v[184:187],a[92:95],v[252:255]\n"
"ds_read2st64_b32 v[24:25],v13 offset0:8 offset1:9\n"
MF "v[160:163],v[180:183],a[52:55],v[160:163]\n"
"ds_read2st64_b32 v[26:27],v13 offset0:10 offset1:11\n"
MF "v[160:163],v[176:179],a[12:15],v[160:163]\n"
LD "v201,s[48:51],s80 offen offset:1024 lds\n"
MF "v[168:171],v[176:179],a[16:19],v[252:255]\n"
"ds_read2st64_b32 v[28:29],v13 offset0:12 offset1:13\n"
MF "v[168:171],v[180:183],a[56:59],v[168:171]\n"
"ds_read2st64_b32 v[30:31],v13 offset0:14 offset1:15\n"
"s_waitcnt vmcnt(29) lgkmcnt(0)\n"
MF "v[168:171],v[184:187],a[96:99],v[168:171]\n"
"s_barrier\n"
MF "v[172:175],a[244:247],a[96:99],v[252:255]\n"
"s_nop 0\n"
TR "v[176:177],v0\n"
TR "v[178:179],v0 offset:2048\n"
MF "v[172:175],a[240:243],a[56:59],v[172:175]\n"
"s_nop 0\n"
TR "v[180:181],v0 offset:4096\n"
TR "v[182:183],v0 offset:6144\n"
MF "v[172:175],v[188:191],a[16:19],v[172:175]\n"
"s_nop 0\n"
TR "v[184:185],v0 offset:8192\n"
TR "v[186:187],v0 offset:10240\n"
MF "v[164:167],v[188:191],a[12:15],v[252:255]\n"
LD "v202,s[48:51],s80 offen offset:2048 lds\n"
MF "v[164:167],a[240:243],a[52:55],v[164:167]\n"
"ds_read2st64_b32 v[32:33],v13 offset0:16 offset1:17\n"
"v_add_f32 v56,v56,v16\n"
MF "v[164:167],a[244:247],a[92:95],v[164:167]\n"
"ds_read2st64_b32 v[34:35],v13 offset0:18 offset1:19\n"
"v_add_f32 v57,v57,v17\n"
MF "v[156:159],a[244:247],a[88:91],v[252:255]\n"
"ds_read2st64_b32 v[36:37],v13 offset0:20 offset1:21\n"
"v_add_f32 v58,v58,v18\n"
MF "v[156:159],a[240:243],a[48:51],v[156:159]\n"
"ds_read2st64_b32 v[38:39],v13 offset0:22 offset1:23\n"
"v_add_f32 v59,v59,v19\n"
MF "v[156:159],v[188:191],a[8:11],v[156:159]\n"
"ds_read2st64_b32 v[40:41],v13 offset0:24 offset1:25\n"
"v_add_f32 v60,v60,v20\n"
MF "v[148:151],v[188:191],a[4:7],v[252:255]\n"
LD "v203,s[48:51],s80 offen offset:3072 lds\n"
MF "v[148:151],a[240:243],a[44:47],v[148:151]\n"
"ds_read2st64_b32 v[42:43],v13 offset0:26 offset1:27\n"
"v_add_f32 v61,v61,v21\n"
MF "v[148:151],a[244:247],a[84:87],v[148:151]\n"
"ds_read2st64_b32 v[44:45],v13 offset0:28 offset1:29\n"
"v_add_f32 v62,v62,v22\n"
MF "v[140:143],a[244:247],a[80:83],v[252:255]\n"
"ds_read2st64_b32 v[46:47],v13 offset0:30 offset1:31\n"
"v_add_f32 v63,v63,v23\n"
MF "v[140:143],a[240:243],a[40:43],v[140:143]\n"
"ds_read2st64_b32 v[48:49],v13 offset0:32 offset1:33\n"
CV "v56,v56,v57\n"
MF "v[140:143],v[188:191],a[0:3],v[140:143]\n"
"s_waitcnt lgkmcnt(9)\n"
"ds_read2st64_b32 v[50:51],v13 offset0:34 offset1:35\n"
CV "v57,v58,v59\n"
MF "v[96:99],v[176:179],a[120:123],v[96:99]\n"
"s_nop 0\n"
TR "v[188:189],v1\n"
TR "v[190:191],v1 offset:2048\n"
MF "v[96:99],v[180:183],a[160:163],v[96:99]\n"
"s_nop 0\n"
TR "a[240:241],v1 offset:4096\n"
TR "a[242:243],v1 offset:6144\n"
MF "v[96:99],v[184:187],a[200:203],v[96:99]\n"
"s_nop 0\n"
TR "a[244:245],v1 offset:8192\n"
TR "a[246:247],v1 offset:10240\n"
MF "v[104:107],v[184:187],a[204:207],v[104:107]\n"
"ds_read2st64_b32 v[52:53],v13 offset0:36 offset1:37\n"
CV "v58,v60,v61\n"
MF "v[104:107],v[180:183],a[164:167],v[104:107]\n"
"s_add_u32 s87,s90,10240\n"
"ds_read2st64_b32 v[54:55],v13 offset0:38 offset1:39\n"
CV "v59,v62,v63\n"
MF "v[104:107],v[176:179],a[124:127],v[104:107]\n"
"buffer_store_dwordx4 v[56:59],v9,s[40:43],s87 offen sc1\n"
MF "v[112:115],v[176:179],a[128:131],v[112:115]\n"
"v_add_f32 v64,v64,v24\n"
"v_add_f32 v65,v65,v25\n"
MF "v[112:115],v[180:183],a[168:171],v[112:115]\n"
"v_add_f32 v66,v66,v26\n"
"v_add_f32 v67,v67,v27\n"
MF "v[112:115],v[184:187],a[208:211],v[112:115]\n"
"v_add_f32 v68,v68,v28\n"
"v_add_f32 v69,v69,v29\n"
MF "v[120:123],v[184:187],a[212:215],v[120:123]\n"
"v_add_f32 v70,v70,v30\n"
"v_add_f32 v71,v71,v31\n"
MF "v[120:123],v[180:183],a[172:175],v[120:123]\n"
CV "v64,v64,v65\n"
CV "v65,v66,v67\n"
MF "v[120:123],v[176:179],a[132:135],v[120:123]\n"
"s_add_u32 s87,s90,11264\n"
CV "v66,v68,v69\n"
CV "v67,v70,v71\n"
MF "v[128:131],v[176:179],a[136:139],v[128:131]\n"
"s_waitcnt lgkmcnt(14)\n"
"buffer_store_dwordx4 v[64:67],v9,s[40:43],s87 offen sc1\n"
MF "v[128:131],v[180:183],a[176:179],v[128:131]\n"
"v_add_f32 v72,v72,v32\n"
"v_add_f32 v73,v73,v33\n"
MF "v[128:131],v[184:187],a[216:219],v[128:131]\n"
"s_waitcnt lgkmcnt(2)\n"
"v_add_f32 v74,v74,v34\n"
"v_add_f32 v75,v75,v35\n"
MF "v[132:135],a[244:247],a[216:219],v[132:135]\n"
"s_nop 0\n"
TR "v[176:177],v2\n"
TR "v[178:179],v2 offset:2048\n"
MF "v[132:135],a[240:243],a[176:179],v[132:135]\n"
"s_nop 0\n"
TR "v[180:181],v2 offset:4096\n"
TR "v[182:183],v2 offset:6144\n"
MF "v[132:135],v[188:191],a[136:139],v[132:135]\n"
"s_nop 0\n"
TR "v[184:185],v2 offset:8192\n"
TR "v[186:187],v2 offset:10240\n"
MF "v[124:127],v[188:191],a[132:135],v[124:127]\n"
"v_add_f32 v76,v76,v36\n"
"v_add_f32 v77,v77,v37\n"
MF "v[124:127],a[240:243],a[172:175],v[124:127]\n"
"v_add_f32 v78,v78,v38\n"
"v_add_f32 v79,v79,v39\n"
MF "v[124:127],a[244:247],a[212:215],v[124:127]\n"
CV "v72,v72,v73\n"
CV "v73,v74,v75\n"
MF "v[116:119],a[244:247],a[208:211],v[116:119]\n"
"s_add_u32 s87,s90,12288\n"
CV "v74,v76,v77\n"
CV "v75,v78,v79\n"
MF "v[116:119],a[240:243],a[168:171],v[116:119]\n"
"buffer_store_dwordx4 v[72:75],v9,s[40:43],s87 offen sc1\n"
MF "v[116:119],v[188:191],a[128:131],v[116:119]\n"
"v_add_f32 v80,v80,v40\n"
"v_add_f32 v81,v81,v41\n"
MF "v[108:111],v[188:191],a[124:127],v[108:111]\n"
"v_add_f32 v82,v82,v42\n"
"v_add_f32 v83,v83,v43\n"
MF "v[108:111],a[240:243],a[164:167],v[108:111]\n"
"v_add_f32 v84,v84,v44\n"
"v_add_f32 v85,v85,v45\n"
MF "v[108:111],a[244:247],a[204:207],v[108:111]\n"
"v_add_f32 v86,v86,v46\n"
"v_add_f32 v87,v87,v47\n"
MF "v[100:103],a[244:247],a[200:203],v[100:103]\n"
CV "v80,v80,v81\n"
CV "v81,v82,v83\n"
MF "v[100:103],a[240:243],a[160:163],v[100:103]\n"
"s_add_u32 s87,s90,13312\n"
CV "v82,v84,v85\n"
CV "v83,v86,v87\n"
MF "v[100:103],v[188:191],a[120:123],v[100:103]\n"
"s_waitcnt lgkmcnt(0)\n"
"buffer_store_dwordx4 v[80:83],v9,s[40:43],s87 offen sc1\n"
MF "v[136:139],v[176:179],a[120:123],v[136:139]\n"
"s_nop 0\n"
TR "v[188:189],v3\n"
TR "v[190:191],v3 offset:2048\n"
MF "v[136:139],v[180:183],a[160:163],v[136:139]\n"
"s_nop 0\n"
TR "a[240:241],v3 offset:4096\n"
TR "a[242:243],v3 offset:6144\n"
MF "v[136:139],v[184:187],a[200:203],v[136:139]\n"
"s_nop 0\n"
TR "a[244:245],v3 offset:8192\n"
TR "a[246:247],v3 offset:10240\n"
MF "v[144:147],v[184:187],a[204:207],v[144:147]\n"
"v_add_f32 v88,v88,v48\n"
"v_add_f32 v89,v89,v49\n"
MF "v[144:147],v[180:183],a[164:167],v[144:147]\n"
"v_add_f32 v90,v90,v50\n"
"v_add_f32 v91,v91,v51\n"
MF "v[144:147],v[176:179],a[124:127],v[144:147]\n"
"v_add_f32 v92,v92,v52\n"
"v_add_f32 v93,v93,v53\n"
MF "v[152:155],v[176:179],a[128:131],v[152:155]\n"
"v_add_f32 v94,v94,v54\n"
"v_add_f32 v95,v95,v55\n"
MF "v[152:155],v[180:183],a[168:171],v[152:155]\n"
CV "v88,v88,v89\n"
CV "v89,v90,v91\n"
MF "v[152:155],v[184:187],a[208:211],v[152:155]\n"
"s_add_u32 s87,s90,14336\n"
CV "v90,v92,v93\n"
CV "v91,v94,v95\n"
MF "v[160:163],v[184:187],a[212:215],v[160:163]\n"
"buffer_store_dwordx4 v[88:91],v9,s[40:43],s87 offen sc1\n"
MF "v[160:163],v[180:183],a[172:175],v[160:163]\n"
MF "v[160:163],v[176:179],a[132:135],v[160:163]\n"
MF "v[168:171],v[176:179],a[136:139],v[168:171]\n"
MF "v[168:171],v[180:183],a[176:179],v[168:171]\n"
"s_waitcnt vmcnt(19) lgkmcnt(0)\n"
MF "v[168:171],v[184:187],a[216:219],v[168:171]\n"
"s_barrier\n"
MF "v[172:175],a[244:247],a[216:219],v[172:175]\n"
"s_nop 0\n"
TR "v[176:177],v0 offset:49152\n"
TR "v[178:179],v0 offset:51200\n"
MF "v[172:175],a[240:243],a[176:179],v[172:175]\n"
"s_nop 0\n"
TR "v[180:181],v0 offset:53248\n"
TR "v[182:183],v0 offset:55296\n"
MF "v[172:175],v[188:191],a[136:139],v[172:175]\n"
"s_nop 0\n"
TR "v[184:185],v0 offset:57344\n"
TR "v[186:187],v0 offset:59392\n"
MF "v[164:167],v[188:191],a[132:135],v[164:167]\n"
"buffer_load_dword v10,v12,s[40:43],s86 offen sc1\n"
MF "v[164:167],a[240:243],a[172:175],v[164:167]\n"
MF "v[164:167],a[244:247],a[212:215],v[164:167]\n"
MF "v[156:159],a[244:247],a[208:211],v[156:159]\n"
MF "v[156:159],a[240:243],a[168:171],v[156:159]\n"
MF "v[156:159],v[188:191],a[128:131],v[156:159]\n"
MF "v[148:151],v[188:191],a[124:127],v[148:151]\n"
MF "v[148:151],a[240:243],a[164:167],v[148:151]\n"
MF "v[148:151],a[244:247],a[204:207],v[148:151]\n"
MF "v[140:143],a[244:247],a[200:203],v[140:143]\n"
MF "v[140:143],a[240:243],a[160:163],v[140:143]\n"
MF "v[140:143],v[188:191],a[120:123],v[140:143]\n"
"s_waitcnt lgkmcnt(0)\n"
MF "v[96:99],v[176:179],a[20:23],v[96:99]\n"
"s_nop 0\n"
TR "v[188:189],v1 offset:49152\n"
TR "v[190:191],v1 offset:51200\n"
MF "v[96:99],v[180:183],a[60:63],v[96:99]\n"
"s_nop 0\n"
TR "a[240:241],v1 offset:53248\n"
TR "a[242:243],v1 offset:55296\n"
MF "v[96:99],v[184:187],a[100:103],v[96:99]\n"
"s_nop 0\n"
TR "a[244:245],v1 offset:57344\n"
TR "a[246:247],v1 offset:59392\n"
MF "v[104:107],v[184:187],a[104:107],v[104:107]\n"
MF "v[104:107],v[180:183],a[64:67],v[104:107]\n"
MF "v[104:107],v[176:179],a[24:27],v[104:107]\n"
"s_waitcnt vmcnt(18)\n"
"v_mov_b32 v236,v228\n"
"v_mov_b32 v237,v229\n"
MF "v[96:99],v[208:211],a[252:255],v[96:99]\n"
MF "v[104:107],v[212:215],a[252:255],v[104:107]\n"
MF "v[112:115],v[216:219],a[252:255],v[112:115]\n"
MF "v[120:123],v[220:223],a[252:255],v[120:123]\n"
MF "v[128:131],v[224:227],a[252:255],v[128:131]\n"
MF "v[100:103],v[208:211],a[248:251],v[100:103]\n"
MF "v[108:111],v[212:215],a[248:251],v[108:111]\n"
MF "v[116:119],v[216:219],a[248:251],v[116:119]\n"
MF "v[124:127],v[220:223],a[248:251],v[124:127]\n"
MF "v[132:135],v[224:227],a[248:251],v[132:135]\n"
MF "v[248:251],v[230:233],a[250:253],v[248:251]\n"
MF "v[244:247],v[234:237],a[250:253],v[244:247]\n"
MF "v[112:115],v[176:179],a[28:31],v[112:115]\n"
MF "v[112:115],v[180:183],a[68:71],v[112:115]\n"
MF "v[112:115],v[184:187],a[108:111],v[112:115]\n"
MF "v[120:123],v[184:187],a[112:115],v[120:123]\n"
"s_nop 3\n"
CV "v244,v244,v245\n"
CV "v245,v246,v247\n"
MF "v[120:123],v[180:183],a[72:75],v[120:123]\n"
"s_bfe_u32 s87,s22,0x30014\n"
CV "v246,v248,v249\n"
CV "v247,v250,v251\n"
MF "v[120:123],v[176:179],a[32:35],v[120:123]\n"
"s_lshl_b32 s87,s87,7\n"
"v_permlane16_swap_b32 v244,v246\n"
"v_permlane16_swap_b32 v245,v247\n"
MF "v[128:131],v[176:179],a[36:39],v[128:131]\n"
"buffer_store_dwordx4 v[244:247],v8,s[44:47],s0 offen sc1\n"
MF "v[128:131],v[180:183],a[76:79],v[128:131]\n"
"s_and_b32 s60,s56,1\n"
MF "v[128:131],v[184:187],a[116:119],v[128:131]\n"
"s_waitcnt lgkmcnt(0)\n"
MF "v[132:135],a[244:247],a[116:119],v[132:135]\n"
"s_mul_i32 s60,s60,204800\n"
TR "v[176:177],v2 offset:49152\n"
TR "v[178:179],v2 offset:51200\n"
MF "v[132:135],a[240:243],a[76:79],v[132:135]\n"
"s_add_u32 s87,s87,s60\n"
TR "v[180:181],v2 offset:53248\n"
TR "v[182:183],v2 offset:55296\n"
MF "v[132:135],v[188:191],a[36:39],v[132:135]\n"
"s_mov_b32 s71,204800\n"
TR "v[184:185],v2 offset:57344\n"
TR "v[186:187],v2 offset:59392\n"
MF "v[124:127],v[188:191],a[32:35],v[124:127]\n"
"s_mov_b32 s66,204800\n"
MF "v[124:127],a[240:243],a[72:75],v[124:127]\n"
"s_add_u32 s72,s78,2\n"
MF "v[124:127],a[244:247],a[112:115],v[124:127]\n"
"s_and_b32 s72,s72,15\n"
MF "v[116:119],a[244:247],a[108:111],v[116:119]\n"
"s_bfe_u32 s60,s22,0x10014\n"
MF "v[116:119],a[240:243],a[68:71],v[116:119]\n"
"s_lshl_b32 s60,s60,5\n"
MF "v[116:119],v[188:191],a[28:31],v[116:119]\n"
"v_xor_b32 v242,s60,v8\n"
MF "v[108:111],v[188:191],a[24:27],v[108:111]\n"
"s_add_u32 s16,s78,6\n"
MF "v[108:111],a[240:243],a[64:67],v[108:111]\n"
"s_and_b32 s16,s16,15\n"
MF "v[108:111],a[244:247],a[104:107],v[108:111]\n"
"s_add_u32 s17,s78,4\n"
MF "v[100:103],a[244:247],a[100:103],v[100:103]\n"
"s_and_b32 s17,s17,15\n"
MF "v[100:103],a[240:243],a[60:63],v[100:103]\n"
MF "v[100:103],v[188:191],a[20:23],v[100:103]\n"
"s_waitcnt lgkmcnt(0)\n"
MF "v[136:139],v[176:179],a[20:23],v[136:139]\n"
"s_nop 0\n"
TR "v[188:189],v3 offset:49152\n"
TR "v[190:191],v3 offset:51200\n"
MF "v[136:139],v[180:183],a[60:63],v[136:139]\n"
"s_nop 0\n"
TR "a[240:241],v3 offset:53248\n"
TR "a[242:243],v3 offset:55296\n"
MF "v[136:139],v[184:187],a[100:103],v[136:139]\n"
"s_nop 0\n"
TR "a[244:245],v3 offset:57344\n"
TR "a[246:247],v3 offset:59392\n"
MF "v[144:147],v[184:187],a[104:107],v[144:147]\n"
MF "v[144:147],v[180:183],a[64:67],v[144:147]\n"
MF "v[144:147],v[176:179],a[24:27],v[144:147]\n"
MF "v[152:155],v[176:179],a[28:31],v[152:155]\n"
MF "v[152:155],v[180:183],a[68:71],v[152:155]\n"
MF "v[152:155],v[184:187],a[108:111],v[152:155]\n"
MF "v[160:163],v[184:187],a[112:115],v[160:163]\n"
MF "v[160:163],v[180:183],a[72:75],v[160:163]\n"
MF "v[160:163],v[176:179],a[32:35],v[160:163]\n"
MF "v[168:171],v[176:179],a[36:39],v[168:171]\n"
MF "v[168:171],v[180:183],a[76:79],v[168:171]\n"
"s_waitcnt vmcnt(1) lgkmcnt(0)\n"
MF "v[168:171],v[184:187],a[116:119],v[168:171]\n"
"s_barrier\n"
MF "v[172:175],a[244:247],a[116:119],v[172:175]\n"
"v_sub_u32 v10,v10,v11\n"
MF "v[172:175],a[240:243],a[76:79],v[172:175]\n"
"s_nop 0\n"
TR "v[176:177],v4\n"
TR "v[178:179],v4 offset:2048\n"
MF "v[172:175],v[188:191],a[36:39],v[172:175]\n"
"s_nop 0\n"
TR "v[180:181],v4 offset:4096\n"
TR "v[182:183],v4 offset:6144\n"
MF "v[164:167],v[188:191],a[32:35],v[164:167]\n"
"s_nop 0\n"
TR "v[184:185],v4 offset:8192\n"
TR "v[186:187],v4 offset:10240\n"
MF "v[164:167],a[240:243],a[72:75],v[164:167]\n"
"v_readlane_b32 s16,v10,s16\n"
MF "v[164:167],a[244:247],a[112:115],v[164:167]\n"
"s_cmp_lt_u32 s16,9\n"
"s_cselect_b32 s16,0xf0000000,0\n"
MF "v[156:159],a[244:247],a[108:111],v[156:159]\n"
"v_readlane_b32 s17,v10,s17\n"
MF "v[156:159],a[240:243],a[68:71],v[156:159]\n"
"s_cmp_lt_u32 s17,10\n"
"s_cselect_b32 s17,0xf0000000,0\n"
MF "v[156:159],v[188:191],a[28:31],v[156:159]\n"
"s_bfe_u32 s62,s22,0x30014\n"
MF "v[148:151],v[188:191],a[24:27],v[148:151]\n"
"s_add_u32 s62,s62,3\n"
"s_and_b32 s62,s62,7\n"
"s_mul_i32 s62,s62,204800\n"
MF "v[148:151],a[240:243],a[64:67],v[148:151]\n"
"s_add_u32 s62,s62,s73\n"
"s_add_u32 s62,s62,133120\n"
"s_or_b32 s62,s62,s16\n"
MF "v[148:151],a[244:247],a[104:107],v[148:151]\n"
LD "v[16:19],v9,s[40:43],s62 offen sc1\n"
MF "v[140:143],a[244:247],a[100:103],v[140:143]\n"
LD "v[24:27],v9,s[40:43],s62 offen offset:1024 sc1\n"
MF "v[140:143],a[240:243],a[60:63],v[140:143]\n"
MF "v[140:143],v[188:191],a[20:23],v[140:143]\n"
"s_waitcnt lgkmcnt(0)\n"
LD "v[32:35],v9,s[40:43],s62 offen offset:2048 sc1\n"
MF "v[96:99],v[176:179],a[140:143],v[96:99]\n"
"s_nop 0\n"
TR "v[188:189],v5\n"
TR "v[190:191],v5 offset:2048\n"
MF "v[96:99],v[180:183],a[180:183],v[96:99]\n"
"s_nop 0\n"
TR "a[240:241],v5 offset:4096\n"
TR "a[242:243],v5 offset:6144\n"
MF "v[96:99],v[184:187],a[220:223],v[96:99]\n"
"s_nop 0\n"
TR "a[244:245],v5 offset:8192\n"
TR "a[246:247],v5 offset:10240\n"
MF "v[104:107],v[184:187],a[224:227],v[104:107]\n"
LD "v[40:43],v9,s[40:43],s62 offen offset:3072 sc1\n"
MF "v[104:107],v[180:183],a[184:187],v[104:107]\n"
MF "v[104:107],v[176:179],a[144:147],v[104:107]\n"
"s_add_u32 s62,s62,4096\n"
MF "v[112:115],v[176:179],a[148:151],v[112:115]\n"
LD "v[48:51],v9,s[40:43],s62 offen sc1\n"
MF "v[112:115],v[180:183],a[188:191],v[112:115]\n"
"s_bfe_u32 s62,s22,0x30014\n"
"ds_write_addtid_b32 v96 offset:16384\n"
"ds_write_addtid_b32 v97 offset:16640\n"
MF "v[112:115],v[184:187],a[228:231],v[112:115]\n"
"s_add_u32 s62,s62,2\n"
"s_and_b32 s62,s62,7\n"
"s_mul_i32 s62,s62,204800\n"
MF "v[120:123],v[184:187],a[232:235],v[120:123]\n"
"s_add_u32 s62,s62,s73\n"
"s_add_u32 s62,s62,153600\n"
"s_or_b32 s62,s62,s17\n"
MF "v[120:123],v[180:183],a[192:195],v[120:123]\n"
LD "v[20:23],v9,s[40:43],s62 offen sc1\n"
MF "v[120:123],v[176:179],a[152:155],v[120:123]\n"
"s_nop 0\n"
"ds_write_addtid_b32 v98 offset:16896\n"
"ds_write_addtid_b32 v99 offset:17152\n"
MF "v[128:131],v[176:179],a[156:159],v[128:131]\n"
LD "v[28:31],v9,s[40:43],s62 offen offset:1024 sc1\n"
MF "v[128:131],v[180:183],a[196:199],v[128:131]\n"
"s_nop 0\n"
"ds_write_addtid_b32 v104 offset:18432\n"
"ds_write_addtid_b32 v105 offset:18688\n"
MF "v[128:131],v[184:187],a[236:239],v[128:131]\n"
"s_waitcnt lgkmcnt(6)\n"
LD "v[36:39],v9,s[40:43],s62 offen offset:2048 sc1\n"
MF "v[100:103],v[188:191],a[140:143],v[100:103]\n"
"s_nop 0\n"
TR "v[176:177],v6\n"
TR "v[178:179],v6 offset:2048\n"
MF "v[100:103],a[240:243],a[180:183],v[100:103]\n"
"s_nop 0\n"
TR "v[180:181],v6 offset:4096\n"
TR "v[182:183],v6 offset:6144\n"
MF "v[100:103],a[244:247],a[220:223],v[100:103]\n"
"s_nop 0\n"
TR "v[184:185],v6 offset:8192\n"
TR "v[186:187],v6 offset:10240\n"
MF "v[108:111],a[244:247],a[224:227],v[108:111]\n"
LD "v[44:47],v9,s[40:43],s62 offen offset:3072 sc1\n"
MF "v[108:111],a[240:243],a[184:187],v[108:111]\n"
"s_nop 0\n"
"ds_write_addtid_b32 v106 offset:18944\n"
"ds_write_addtid_b32 v107 offset:19200\n"
MF "v[108:111],v[188:191],a[144:147],v[108:111]\n"
"s_add_u32 s62,s62,4096\n"
"ds_write_addtid_b32 v112 offset:20480\n"
"ds_write_addtid_b32 v113 offset:20736\n"
MF "v[116:119],v[188:191],a[148:151],v[116:119]\n"
LD "v[52:55],v9,s[40:43],s62 offen sc1\n"
MF "v[116:119],a[240:243],a[188:191],v[116:119]\n"
"s_nop 0\n"
"ds_write_addtid_b32 v114 offset:20992\n"
"ds_write_addtid_b32 v115 offset:21248\n"
MF "v[116:119],a[244:247],a[228:231],v[116:119]\n"
"s_nop 0\n"
"ds_write_addtid_b32 v120 offset:22528\n"
"ds_write_addtid_b32 v121 offset:22784\n"
MF "v[124:127],a[244:247],a[232:235],v[124:127]\n"
"s_nop 0\n"
"ds_write_addtid_b32 v122 offset:23040\n"
"ds_write_addtid_b32 v123 offset:23296\n"
MF "v[124:127],a[240:243],a[192:195],v[124:127]\n"
"s_nop 0\n"
"ds_write_addtid_b32 v128 offset:24576\n"
"ds_write_addtid_b32 v129 offset:24832\n"
MF "v[124:127],v[188:191],a[152:155],v[124:127]\n"
"buffer_load_dword v10,v12,s[40:43],s86 offen sc1\n"
MF "v[132:135],v[188:191],a[156:159],v[132:135]\n"
"s_nop 0\n"
"ds_write_addtid_b32 v130 offset:25088\n"
"ds_write_addtid_b32 v131 offset:25344\n"
MF "v[132:135],a[240:243],a[196:199],v[132:135]\n"
"s_nop 0\n"
"ds_write_addtid_b32 v100 offset:17408\n"
"ds_write_addtid_b32 v101 offset:17664\n"
MF "v[132:135],a[244:247],a[236:239],v[132:135]\n"
"s_waitcnt lgkmcnt(15)\n"
"ds_write_addtid_b32 v102 offset:17920\n"
"ds_write_addtid_b32 v103 offset:18176\n"
MF "v[136:139],v[176:179],a[140:143],v[136:139]\n"
"s_nop 0\n"
TR "v[188:189],v7\n"
TR "v[190:191],v7 offset:2048\n"
MF "v[136:139],v[180:183],a[180:183],v[136:139]\n"
"s_nop 0\n"
TR "a[240:241],v7 offset:4096\n"
TR "a[242:243],v7 offset:6144\n"
MF "v[136:139],v[184:187],a[220:223],v[136:139]\n"
"s_nop 0\n"
TR "a[244:245],v7 offset:8192\n"
TR "a[246:247],v7 offset:10240\n"
MF "v[144:147],v[184:187],a[224:227],v[144:147]\n"
"s_nop 0\n"
"ds_write_addtid_b32 v108 offset:19456\n"
"ds_write_addtid_b32 v109 offset:19712\n"
MF "v[144:147],v[180:183],a[184:187],v[144:147]\n"
"s_nop 0\n"
"ds_write_addtid_b32 v110 offset:19968\n"
"ds_write_addtid_b32 v111 offset:20224\n"
MF "v[144:147],v[176:179],a[144:147],v[144:147]\n"
"s_nop 0\n"
"ds_write_addtid_b32 v116 offset:21504\n"
"ds_write_addtid_b32 v117 offset:21760\n"
MF "v[152:155],v[176:179],a[148:151],v[152:155]\n"
"s_nop 0\n"
"ds_write_addtid_b32 v118 offset:22016\n"
"ds_write_addtid_b32 v119 offset:22272\n"
MF "v[152:155],v[180:183],a[188:191],v[152:155]\n"
"ds_write_addtid_b32 v124 offset:23552\n"
MF "v[152:155],v[184:187],a[228:231],v[152:155]\n"
"ds_write_addtid_b32 v125 offset:23808\n"
MF "v[160:163],v[184:187],a[232:235],v[160:163]\n"
"ds_write_addtid_b32 v126 offset:24064\n"
MF "v[160:163],v[180:183],a[192:195],v[160:163]\n"
"ds_write_addtid_b32 v127 offset:24320\n"
MF "v[160:163],v[176:179],a[152:155],v[160:163]\n"
"ds_write_addtid_b32 v132 offset:25600\n"
MF "v[168:171],v[176:179],a[156:159],v[168:171]\n"
"ds_write_addtid_b32 v133 offset:25856\n"
MF "v[168:171],v[180:183],a[196:199],v[168:171]\n"
"ds_write_addtid_b32 v134 offset:26112\n"
MF "v[168:171],v[184:187],a[236:239],v[168:171]\n"
"s_waitcnt lgkmcnt(15)\n"
"ds_write_addtid_b32 v135 offset:26368\n"
MF "v[140:143],v[188:191],a[140:143],v[140:143]\n"
MF "v[140:143],a[240:243],a[180:183],v[140:143]\n"
MF "v[140:143],a[244:247],a[220:223],v[140:143]\n"
MF "v[148:151],a[244:247],a[224:227],v[148:151]\n"
"s_waitcnt lgkmcnt(0)\n"
MF "v[148:151],a[240:243],a[184:187],v[148:151]\n"
"s_barrier\n"
MF "v[148:151],v[188:191],a[144:147],v[148:151]\n"
"ds_read2st64_b32 v[56:57],v13 offset0:0 offset1:1\n"
MF "v[156:159],v[188:191],a[148:151],v[156:159]\n"
"ds_read2st64_b32 v[58:59],v13 offset0:2 offset1:3\n"
MF "v[156:159],a[240:243],a[188:191],v[156:159]\n"
"ds_read2st64_b32 v[60:61],v13 offset0:4 offset1:5\n"
MF "v[156:159],a[244:247],a[228:231],v[156:159]\n"
"ds_read2st64_b32 v[62:63],v13 offset0:6 offset1:7\n"
MF "v[164:167],a[244:247],a[232:235],v[164:167]\n"
"ds_read2st64_b32 v[64:65],v13 offset0:8 offset1:9\n"
MF "v[164:167],a[240:243],a[192:195],v[164:167]\n"
"ds_read2st64_b32 v[66:67],v13 offset0:10 offset1:11\n"
MF "v[164:167],v[188:191],a[152:155],v[164:167]\n"
"ds_read2st64_b32 v[68:69],v13 offset0:12 offset1:13\n"
MF "v[172:175],v[188:191],a[156:159],v[172:175]\n"
"ds_read2st64_b32 v[70:71],v13 offset0:14 offset1:15\n"
MF "v[172:175],a[240:243],a[196:199],v[172:175]\n"
"ds_read2st64_b32 v[72:73],v13 offset0:16 offset1:17\n"
MF "v[172:175],a[244:247],a[236:239],v[172:175]\n"
"ds_read2st64_b32 v[74:75],v13 offset0:18 offset1:19\n"
"ds_read2st64_b32 v[76:77],v13 offset0:20 offset1:21\n"
"ds_read2st64_b32 v[78:79],v13 offset0:22 offset1:23\n"
"s_nop 0\n"
"ds_read2st64_b32 v[80:81],v13 offset0:24 offset1:25\n"
"ds_read2st64_b32 v[82:83],v13 offset0:26 offset1:27\n"
"ds_read2st64_b32 v[84:85],v13 offset0:28 offset1:29\n"
"ds_read2st64_b32 v[86:87],v13 offset0:30 offset1:31\n"
"ds_read2st64_b32 v[88:89],v13 offset0:32 offset1:33\n"
"ds_read2st64_b32 v[90:91],v13 offset0:34 offset1:35\n"
"ds_read2st64_b32 v[92:93],v13 offset0:36 offset1:37\n"
"ds_read2st64_b32 v[94:95],v13 offset0:38 offset1:39\n"
"s_waitcnt vmcnt(0)\n"
"v_sub_u32 v10,v10,v11\n"
"s_cmp_eq_u32 s16,0\n"
"s_cbranch_scc1 .Lhave03\n"
".Lpg31h:\n"
"s_cbranch_execz .Lpg34h\n"
"s_nop 0\n"
"s_add_u32 s59,s78,6\n"
"s_and_b32 s59,s59,15\n"
"s_nop 3\n"
".Lnf3:\n"
"v_readlane_b32 s58,v10,s59\n"
"s_cmp_lt_u32 s58,9\n"
"s_cbranch_scc1 .Lsp3\n"
".Lnb3:\n"
"s_bfe_u32 s62,s22,0x30014\n"
"s_add_u32 s62,s62,3\n"
"s_and_b32 s62,s62,7\n"
"s_mul_i32 s62,s62,204800\n"
"s_add_u32 s62,s62,s73\n"
"s_add_u32 s62,s62,133120\n"
LD "v[16:19],v9,s[40:43],s62 offen sc1\n"
LD "v[24:27],v9,s[40:43],s62 offen offset:1024 sc1\n"
LD "v[32:35],v9,s[40:43],s62 offen offset:2048 sc1\n"
LD "v[40:43],v9,s[40:43],s62 offen offset:3072 sc1\n"
"s_add_u32 s62,s62,4096\n"
LD "v[48:51],v9,s[40:43],s62 offen sc1\n"
".Lhave03:\n"
"s_cmp_eq_u32 s17,0\n"
"s_cbranch_scc1 .Lhave02\n"
"s_add_u32 s59,s78,4\n"
"s_and_b32 s59,s59,15\n"
"s_nop 3\n"
".Lnf4:\n"
"v_readlane_b32 s58,v10,s59\n"
"s_cmp_lt_u32 s58,10\n"
"s_cbranch_scc1 .Lsp4\n"
".Lnb4:\n"
"s_bfe_u32 s62,s22,0x30014\n"
"s_add_u32 s62,s62,2\n"
"s_and_b32 s62,s62,7\n"
"s_mul_i32 s62,s62,204800\n"
"s_add_u32 s62,s62,s73\n"
"s_add_u32 s62,s62,153600\n"
LD "v[20:23],v9,s[40:43],s62 offen sc1\n"
LD "v[28:31],v9,s[40:43],s62 offen offset:1024 sc1\n"
LD "v[36:39],v9,s[40:43],s62 offen offset:2048 sc1\n"
LD "v[44:47],v9,s[40:43],s62 offen offset:3072 sc1\n"
"s_add_u32 s62,s62,4096\n"
LD "v[52:55],v9,s[40:43],s62 offen sc1\n"
".Lhave02:\n"
".Lnf5:\n"
"v_readlane_b32 s58,v10,s72\n"
"s_cmp_lt_u32 s58,11\n"
"s_cbranch_scc1 .Lsp5\n"
".Lnb5:\n"
"s_bfe_u32 s62,s22,0x30014\n"
"s_add_u32 s62,s62,1\n"
"s_and_b32 s62,s62,7\n"
"s_mul_i32 s62,s62,204800\n"
"s_add_u32 s62,s62,s73\n"
"s_add_u32 s62,s62,174080\n"
LD "v[208:211],v9,s[40:43],s62 offen sc1\n"
LD "v[212:215],v9,s[40:43],s62 offen offset:1024 sc1\n"
LD "v[216:219],v9,s[40:43],s62 offen offset:2048 sc1\n"
LD "v[220:223],v9,s[40:43],s62 offen offset:3072 sc1\n"
"s_add_u32 s62,s62,4096\n"
LD "v[224:227],v9,s[40:43],s62 offen sc1\n"
"s_waitcnt lgkmcnt(15)\n"
"v_pk_add_f32 v[136:137],v[136:137],v[56:57]\n"
"v_pk_add_f32 v[138:139],v[138:139],v[58:59]\n"
"v_pk_add_f32 v[140:141],v[140:141],v[60:61]\n"
"v_pk_add_f32 v[142:143],v[142:143],v[62:63]\n"
"v_pk_add_f32 v[144:145],v[144:145],v[64:65]\n"
"s_waitcnt lgkmcnt(14)\n"
"v_pk_add_f32 v[146:147],v[146:147],v[66:67]\n"
"s_waitcnt lgkmcnt(13)\n"
"v_pk_add_f32 v[148:149],v[148:149],v[68:69]\n"
"s_waitcnt lgkmcnt(12)\n"
"v_pk_add_f32 v[150:151],v[150:151],v[70:71]\n"
"s_waitcnt lgkmcnt(11)\n"
"v_pk_add_f32 v[152:153],v[152:153],v[72:73]\n"
"s_waitcnt lgkmcnt(10)\n"
"v_pk_add_f32 v[154:155],v[154:155],v[74:75]\n"
"s_waitcnt lgkmcnt(9)\n"
"v_pk_add_f32 v[156:157],v[156:157],v[76:77]\n"
"s_waitcnt lgkmcnt(8)\n"
"v_pk_add_f32 v[158:159],v[158:159],v[78:79]\n"
"s_waitcnt lgkmcnt(7)\n"
"v_pk_add_f32 v[160:161],v[160:161],v[80:81]\n"
"s_waitcnt lgkmcnt(6)\n"
"v_pk_add_f32 v[162:163],v[162:163],v[82:83]\n"
"s_waitcnt lgkmcnt(5)\n"
"v_pk_add_f32 v[164:165],v[164:165],v[84:85]\n"
"s_waitcnt lgkmcnt(4)\n"
"v_pk_add_f32 v[166:167],v[166:167],v[86:87]\n"
"s_waitcnt lgkmcnt(3)\n"
"v_pk_add_f32 v[168:169],v[168:169],v[88:89]\n"
"s_waitcnt lgkmcnt(2)\n"
"v_pk_add_f32 v[170:171],v[170:171],v[90:91]\n"
"s_waitcnt lgkmcnt(1)\n"
"v_pk_add_f32 v[172:173],v[172:173],v[92:93]\n"
"s_waitcnt lgkmcnt(0)\n"
"v_pk_add_f32 v[174:175],v[174:175],v[94:95]\n"
"s_waitcnt vmcnt(5)\n"
"s_nop 0\n"
MF "v[140:143],v[16:19],a[252:255],v[140:143]\n"
MF "v[148:151],v[24:27],a[252:255],v[148:151]\n"
MF "v[156:159],v[32:35],a[252:255],v[156:159]\n"
MF "v[164:167],v[40:43],a[252:255],v[164:167]\n"
MF "v[172:175],v[48:51],a[252:255],v[172:175]\n"
MF "v[136:139],v[16:19],a[248:251],v[136:139]\n"
MF "v[144:147],v[24:27],a[248:251],v[144:147]\n"
MF "v[152:155],v[32:35],a[248:251],v[152:155]\n"
MF "v[160:163],v[40:43],a[248:251],v[160:163]\n"
MF "v[168:171],v[48:51],a[248:251],v[168:171]\n"
MF "v[136:139],v[20:23],a[252:255],v[136:139]\n"
MF "v[144:147],v[28:31],a[252:255],v[144:147]\n"
MF "v[152:155],v[36:39],a[252:255],v[152:155]\n"
MF "v[160:163],v[44:47],a[252:255],v[160:163]\n"
MF "v[168:171],v[52:55],a[252:255],v[168:171]\n"
MF "v[140:143],v[20:23],a[248:251],v[140:143]\n"
MF "v[148:151],v[28:31],a[248:251],v[148:151]\n"
MF "v[156:159],v[36:39],a[248:251],v[156:159]\n"
MF "v[164:167],v[44:47],a[248:251],v[164:167]\n"
MF "v[172:175],v[52:55],a[248:251],v[172:175]\n"
"s_waitcnt vmcnt(0)\n"
MF "v[140:143],v[208:211],a[252:255],v[140:143]\n"
MF "v[136:139],v[208:211],a[248:251],v[136:139]\n"
MF "v[148:151],v[212:215],a[252:255],v[148:151]\n"
MF "v[144:147],v[212:215],a[248:251],v[144:147]\n"
MF "v[156:159],v[216:219],a[252:255],v[156:159]\n"
"s_nop 4\n"
CV "v136,v136,v137\n"
CV "v137,v138,v139\n"
MF "v[152:155],v[216:219],a[248:251],v[152:155]\n"
CV "v138,v140,v141\n"
CV "v139,v142,v143\n"
MF "v[164:167],v[220:223],a[252:255],v[164:167]\n"
CV "v144,v144,v145\n"
CV "v145,v146,v147\n"
MF "v[160:163],v[220:223],a[248:251],v[160:163]\n"
CV "v146,v148,v149\n"
CV "v147,v150,v151\n"
"v_permlane16_swap_b32 v136,v138\n"
"v_permlane16_swap_b32 v137,v139\n"
"buffer_store_dwordx4 v[136:139],v242,s[44:47],s87 offen offset:64 sc1\n"
"s_add_u32 s87,s87,40960\n"
MF "v[172:175],v[224:227],a[252:255],v[172:175]\n"
CV "v152,v152,v153\n"
CV "v153,v154,v155\n"
MF "v[168:171],v[224:227],a[248:251],v[168:171]\n"
CV "v154,v156,v157\n"
CV "v155,v158,v159\n"
"v_permlane16_swap_b32 v144,v146\n"
"v_permlane16_swap_b32 v145,v147\n"
"buffer_store_dwordx4 v[144:147],v242,s[44:47],s87 offen offset:64 sc1\n"
"s_add_u32 s87,s87,40960\n"
CV "v160,v160,v161\n"
CV "v161,v162,v163\n"
CV "v162,v164,v165\n"
CV "v163,v166,v167\n"
"v_permlane16_swap_b32 v152,v154\n"
"v_permlane16_swap_b32 v153,v155\n"
"buffer_store_dwordx4 v[152:155],v242,s[44:47],s87 offen offset:64 sc1\n"
"s_add_u32 s87,s87,40960\n"
CV "v168,v168,v169\n"
CV "v169,v170,v171\n"
CV "v170,v172,v173\n"
CV "v171,v174,v175\n"
"v_permlane16_swap_b32 v160,v162\n"
"v_permlane16_swap_b32 v161,v163\n"
"buffer_store_dwordx4 v[160:163],v242,s[44:47],s87 offen offset:64 sc1\n"
"s_add_u32 s87,s87,40960\n"
"v_permlane16_swap_b32 v168,v170\n"
"v_permlane16_swap_b32 v169,v171\n"
"buffer_store_dwordx4 v[168:171],v242,s[44:47],s87 offen offset:64 sc1\n"
"s_add_u32 s87,s87,40960\n"
"s_endpgm\n"
".Lpgrole1_h:\n"
"s_cbranch_execz .Lpg35h\n"
".Lrole1:\n"
"s_waitcnt lgkmcnt(4)\n"
MF "v[16:19],a[120:123],v[176:179],v[252:255]\n"
"s_mov_b32 s65,128\n"
TR "v[188:189],v1 offset:24576\n"
AR "v220,a44\n"
"s_waitcnt lgkmcnt(3)\n"
MF "v[16:19],a[160:163],v[180:183],v[16:19]\n"
"s_mov_b32 s9,0x3f80\n"
TR "v[190:191],v1 offset:26624\n"
AR "v221,a45\n"
"s_waitcnt lgkmcnt(2)\n"
MF "v[16:19],a[200:203],v[184:187],v[16:19]\n"
AR "v222,a46\n"
AR "v223,a47\n"
MF "v[24:27],a[204:207],v[184:187],v[252:255]\n"
"s_bfe_u32 s58,s22,0x30014\n"
TR "a[240:241],v1 offset:28672\n"
AR "v224,a64\n"
MF "v[24:27],a[164:167],v[180:183],v[24:27]\n"
"s_add_u32 s61,s58,1\n"
TR "a[242:243],v1 offset:30720\n"
AR "v225,a65\n"
MF "v[24:27],a[124:127],v[176:179],v[24:27]\n"
"s_and_b32 s61,s61,7\n"
AR "v226,a66\n"
AR "v227,a67\n"
MF "v[32:35],a[128:131],v[176:179],v[252:255]\n"
"s_mov_b32 s64,9\n"
TR "a[244:245],v1 offset:32768\n"
"ds_read_b128 a[64:67],v236 offset:49152\n"
MF "v[32:35],a[168:171],v[180:183],v[32:35]\n"
"s_xor_b32 s67,s78,8\n"
TR "a[246:247],v1 offset:34816\n"
"v_fma_f32 v220,v220,v209,v215\n"
MF "v[32:35],a[208:211],v[184:187],v[32:35]\n"
"s_mov_b32 s71,204800\n"
"v_fma_f32 v221,v221,v209,v215\n"
"v_fma_f32 v222,v222,v209,v215\n"
MF "v[40:43],a[212:215],v[184:187],v[252:255]\n"
"s_mov_b32 s66,204800\n"
"v_fma_f32 v223,v223,v209,v215\n"
"v_fma_f32 v224,v224,v209,v215\n"
MF "v[40:43],a[172:175],v[180:183],v[40:43]\n"
"s_and_b32 s73,s56,1\n"
"v_fma_f32 v225,v225,v209,v215\n"
"v_fma_f32 v226,v226,v209,v215\n"
MF "v[40:43],a[132:135],v[176:179],v[40:43]\n"
"s_mul_i32 s73,s73,5120\n"
"v_fma_f32 v227,v227,v209,v215\n"
CV "v220,v220,v221\n"
MF "v[48:51],a[136:139],v[176:179],v[252:255]\n"
"s_add_u32 s73,s73,s93\n"
CV "v221,v222,v223\n"
CV "v222,v224,v225\n"
MF "v[48:51],a[176:179],v[180:183],v[48:51]\n"
"s_xor_b32 s58,s58,4\n"
CV "v223,v226,v227\n"
AW "a44,v220\n"
MF "v[48:51],a[216:219],v[184:187],v[48:51]\n"
"s_waitcnt lgkmcnt(0)\n"
AW "a45,v221\n"
AW "a46,v222\n"
MF "v[52:55],a[216:219],a[244:247],v[252:255]\n"
"s_mul_i32 s70,s58,204800\n"
TR "v[176:177],v2 offset:24576\n"
AW "a47,v223\n"
MF "v[52:55],a[176:179],a[240:243],v[52:55]\n"
"s_add_u32 s70,s70,s73\n"
TR "v[178:179],v2 offset:26624\n"
AR "v228,a84\n"
MF "v[52:55],a[136:139],v[188:191],v[52:55]\n"
"s_add_u32 s70,s70,0xc800\n"
AR "v229,a85\n"
AR "v230,a86\n"
MF "v[44:47],a[132:135],v[188:191],v[252:255]\n"
"s_mov_b32 s85,1\n"
TR "v[180:181],v2 offset:28672\n"
AR "v231,a87\n"
MF "v[44:47],a[172:175],a[240:243],v[44:47]\n"
"s_mov_b32 s83,0xf0000000\n"
TR "v[182:183],v2 offset:30720\n"
AR "v232,a104\n"
MF "v[44:47],a[212:215],a[244:247],v[44:47]\n"
"s_mov_b32 s82,0xf0000000\n"
AR "v233,a105\n"
AR "v234,a106\n"
MF "v[36:39],a[208:211],a[244:247],v[252:255]\n"
"s_add_u32 s58,s79,73728\n"
TR "v[184:185],v2 offset:32768\n"
AR "v235,a107\n"
MF "v[36:39],a[168:171],a[240:243],v[36:39]\n"
"s_mov_b32 s90,s91\n"
TR "v[186:187],v2 offset:34816\n"
"ds_read_b128 a[104:107],v237 offset:4096\n"
MF "v[36:39],a[128:131],v[188:191],v[36:39]\n"
"s_add_u32 s91,s91,20480\n"
"v_fma_f32 v228,v228,v209,v215\n"
"v_fma_f32 v229,v229,v209,v215\n"
MF "v[28:31],a[124:127],v[188:191],v[252:255]\n"
"s_bfe_u32 s74,s22,0x30004\n"
"v_fma_f32 v230,v230,v209,v215\n"
"v_fma_f32 v231,v231,v209,v215\n"
MF "v[28:31],a[164:167],a[240:243],v[28:31]\n"
"s_mul_i32 s74,s74,2\n"
"v_fma_f32 v232,v232,v209,v215\n"
"v_fma_f32 v233,v233,v209,v215\n"
MF "v[28:31],a[204:207],a[244:247],v[28:31]\n"
"s_add_u32 s74,s74,s56\n"
"v_fma_f32 v234,v234,v209,v215\n"
"v_fma_f32 v235,v235,v209,v215\n"
MF "v[20:23],a[200:203],a[244:247],v[252:255]\n"
"s_sub_u32 s74,s74,2\n"
CV "v228,v228,v229\n"
CV "v229,v230,v231\n"
MF "v[20:23],a[160:163],a[240:243],v[20:23]\n"
"s_mul_i32 s74,s74,30720\n"
CV "v230,v232,v233\n"
CV "v231,v234,v235\n"
MF "v[20:23],a[120:123],v[188:191],v[20:23]\n"
"s_waitcnt lgkmcnt(1)\n"
AW "a84,v228\n"
AW "a85,v229\n"
MF "v[56:59],a[120:123],v[176:179],v[252:255]\n"
"s_bfe_u32 s68,s22,0x10007\n"
TR "v[188:189],v3 offset:24576\n"
"v_add_u32 v198,59392,v192\n"
MF "v[56:59],a[160:163],v[180:183],v[56:59]\n"
"s_lshl_b32 s68,s68,3\n"
TR "v[190:191],v3 offset:26624\n"
"v_add_u32 v199,68608,v192\n"
MF "v[56:59],a[200:203],v[184:187],v[56:59]\n"
"s_mul_i32 s68,s68,491520\n"
"v_add_u32 v200,81920,v192\n"
"v_add_u32 v201,91136,v192\n"
MF "v[64:67],a[204:207],v[184:187],v[252:255]\n"
"s_add_u32 s74,s74,s68\n"
TR "a[240:241],v3 offset:28672\n"
"v_add_u32 v202,100352,v192\n"
MF "v[64:67],a[164:167],v[180:183],v[64:67]\n"
"s_bfe_u32 s68,s22,0x30014\n"
TR "a[242:243],v3 offset:30720\n"
"v_add_u32 v203,109568,v192\n"
MF "v[64:67],a[124:127],v[176:179],v[64:67]\n"
"s_mul_i32 s68,s68,491520\n"
AW "a86,v230\n"
AW "a87,v231\n"
MF "v[72:75],a[128:131],v[176:179],v[252:255]\n"
"s_add_u32 s74,s74,s68\n"
TR "a[244:245],v3 offset:32768\n"
AR "v220,a8\n"
MF "v[72:75],a[168:171],v[180:183],v[72:75]\n"
"s_bfe_u32 s68,s22,0x30014\n"
TR "a[246:247],v3 offset:34816\n"
AR "v221,a9\n"
MF "v[72:75],a[208:211],v[184:187],v[72:75]\n"
"s_add_u32 s68,s68,7\n"
AR "v222,a10\n"
AR "v223,a11\n"
MF "v[80:83],a[212:215],v[184:187],v[252:255]\n"
"s_lshl_b32 s68,s68,7\n"
AR "v224,a28\n"
AR "v225,a29\n"
MF "v[80:83],a[172:175],v[180:183],v[80:83]\n"
"s_and_b32 s68,s68,768\n"
AR "v226,a30\n"
AR "v227,a31\n"
MF "v[80:83],a[132:135],v[176:179],v[80:83]\n"
"s_bfe_u32 s4,s22,0x30014\n"
"ds_read_b128 a[28:31],v236 offset:32768\n"
"v_fma_f32 v220,v220,v210,v216\n"
MF "v[88:91],a[136:139],v[176:179],v[252:255]\n"
"s_add_u32 s4,s4,32\n"
"v_fma_f32 v221,v221,v210,v216\n"
"v_fma_f32 v222,v222,v210,v216\n"
MF "v[88:91],a[176:179],v[180:183],v[88:91]\n"
"s_waitcnt vmcnt(0) lgkmcnt(0)\n"
"v_fma_f32 v223,v223,v210,v216\n"
"v_fma_f32 v224,v224,v210,v216\n"
MF "v[88:91],a[216:219],v[184:187],v[88:91]\n"
"s_barrier\n"
MF "v[92:95],a[216:219],a[244:247],v[252:255]\n"
"s_add_u32 s89,s89,s65\n"
TR "v[176:177],v0\n"
"v_fma_f32 v225,v225,v210,v216\n"
MF "v[92:95],a[176:179],a[240:243],v[92:95]\n"
"s_add_u32 m0,s79,24576\n"
TR "v[178:179],v0 offset:2048\n"
"v_fma_f32 v226,v226,v210,v216\n"
MF "v[92:95],a[136:139],v[188:191],v[92:95]\n"
LD "v192,s[48:51],s89 offen lds\n"
MF "v[84:87],a[132:135],v[188:191],v[252:255]\n"
"s_sub_u32 s4,s4,s92\n"
TR "v[180:181],v0 offset:4096\n"
"v_fma_f32 v227,v227,v210,v216\n"
MF "v[84:87],a[172:175],a[240:243],v[84:87]\n"
"s_cmp_eq_u32 s56,2\n"
"s_cselect_b32 s4,s4,64\n"
TR "v[182:183],v0 offset:6144\n"
MF "v[84:87],a[212:215],a[244:247],v[84:87]\n"
LD "v193,s[48:51],s89 offen offset:1024 lds\n"
MF "v[76:79],a[208:211],a[244:247],v[252:255]\n"
"s_mul_i32 s95,s92,3277\n"
TR "v[184:185],v0 offset:8192\n"
CV "v220,v220,v221\n"
MF "v[76:79],a[168:171],a[240:243],v[76:79]\n"
"s_lshr_b32 s95,s95,16\n"
TR "v[186:187],v0 offset:10240\n"
CV "v221,v222,v223\n"
MF "v[76:79],a[128:131],v[188:191],v[76:79]\n"
"s_mul_i32 s59,s95,20\n"
TR "v[96:97],v0 offset:49152\n"
CV "v222,v224,v225\n"
MF "v[68:71],a[124:127],v[188:191],v[252:255]\n"
"s_sub_u32 s59,s92,s59\n"
TR "v[98:99],v0 offset:51200\n"
CV "v223,v226,v227\n"
MF "v[68:71],a[164:167],a[240:243],v[68:71]\n"
LD "v194,s[48:51],s89 offen offset:2048 lds\n"
MF "v[68:71],a[204:207],a[244:247],v[68:71]\n"
"s_cmp_ge_u32 s59,10\n"
"s_cselect_b32 s96,4294557760,0\n"
TR "v[100:101],v0 offset:53248\n"
MF "v[60:63],a[200:203],a[244:247],v[252:255]\n"
"s_add_u32 s96,s96,1024\n"
TR "v[102:103],v0 offset:55296\n"
AW "a8,v220\n"
MF "v[60:63],a[160:163],a[240:243],v[60:63]\n"
"s_mul_i32 s59,s59,40960\n"
TR "v[104:105],v0 offset:57344\n"
AW "a9,v221\n"
MF "v[60:63],a[120:123],v[188:191],v[60:63]\n"
"s_waitcnt lgkmcnt(5)\n"
LD "v195,s[48:51],s89 offen offset:3072 lds\n"
MF "v[16:19],a[140:143],v[176:179],v[16:19]\n"
"s_add_u32 s96,s96,s59\n"
TR "v[188:189],v1\n"
AW "a10,v222\n"
MF "v[16:19],a[180:183],v[180:183],v[16:19]\n"
"s_lshl_b32 s59,s95,7\n"
TR "v[190:191],v1 offset:2048\n"
AW "a11,v223\n"
MF "v[16:19],a[220:223],v[184:187],v[16:19]\n"
"s_add_u32 s96,s96,s59\n"
TR "v[106:107],v0 offset:59392\n"
AR "v228,a48\n"
MF "v[24:27],a[224:227],v[184:187],v[24:27]\n"
"s_lshl_b32 s2,s92,10\n"
TR "a[240:241],v1 offset:4096\n"
AR "v229,a49\n"
MF "v[24:27],a[184:187],v[180:183],v[24:27]\n"
"s_add_u32 m0,s79,28672\n"
TR "a[242:243],v1 offset:6144\n"
AR "v230,a50\n"
MF "v[24:27],a[144:147],v[176:179],v[24:27]\n"
LD "v196,s[48:51],s89 offen lds\n"
MF "v[32:35],a[148:151],v[176:179],v[32:35]\n"
"s_add_u32 s2,s2,s93\n"
TR "a[244:245],v1 offset:8192\n"
AR "v231,a51\n"
MF "v[32:35],a[188:191],v[180:183],v[32:35]\n"
"s_mov_b32 s0,s96\n"
TR "a[246:247],v1 offset:10240\n"
AR "v232,a68\n"
MF "v[32:35],a[228:231],v[184:187],v[32:35]\n"
LD "v197,s[48:51],s89 offen offset:1024 lds\n"
MF "v[40:43],a[232:235],v[184:187],v[40:43]\n"
"s_add_u32 s92,s92,s4\n"
TR "v[108:109],v4\n"
AR "v233,a69\n"
MF "v[40:43],a[192:195],v[180:183],v[40:43]\n"
"s_mul_i32 s95,s92,3277\n"
TR "v[110:111],v4 offset:2048\n"
AR "v234,a70\n"
MF "v[40:43],a[152:155],v[176:179],v[40:43]\n"
"s_lshr_b32 s95,s95,16\n"
TR "v[112:113],v4 offset:4096\n"
AR "v235,a71\n"
MF "v[48:51],a[156:159],v[176:179],v[48:51]\n"
"s_mul_i32 s59,s95,20\n"
TR "v[114:115],v4 offset:6144\n"
"ds_read_b128 a[68:71],v236 offset:53248\n"
MF "v[48:51],a[196:199],v[180:183],v[48:51]\n"
LD "v198,s[48:51],s89 offen offset:2048 lds\n"
MF "v[48:51],a[236:239],v[184:187],v[48:51]\n"
"s_waitcnt lgkmcnt(5)\n"
TR "v[116:117],v4 offset:8192\n"
"v_fma_f32 v228,v228,v210,v216\n"
MF "v[52:55],a[236:239],a[244:247],v[52:55]\n"
"s_sub_u32 s59,s92,s59\n"
TR "v[176:177],v2\n"
"v_fma_f32 v229,v229,v210,v216\n"
MF "v[52:55],a[196:199],a[240:243],v[52:55]\n"
"s_cmp_ge_u32 s59,10\n"
"s_cselect_b32 s96,4294557760,0\n"
TR "v[178:179],v2 offset:2048\n"
MF "v[52:55],a[156:159],v[188:191],v[52:55]\n"
LD "v199,s[48:51],s89 offen offset:3072 lds\n"
MF "v[44:47],a[152:155],v[188:191],v[44:47]\n"
"s_add_u32 s96,s96,1024\n"
TR "v[180:181],v2 offset:4096\n"
"v_fma_f32 v230,v230,v210,v216\n"
MF "v[44:47],a[192:195],a[240:243],v[44:47]\n"
"s_mul_i32 s59,s59,40960\n"
TR "v[182:183],v2 offset:6144\n"
"v_fma_f32 v231,v231,v210,v216\n"
MF "v[44:47],a[232:235],a[244:247],v[44:47]\n"
"s_add_u32 s96,s96,s59\n"
TR "v[118:119],v4 offset:10240\n"
"v_fma_f32 v232,v232,v210,v216\n"
MF "v[36:39],a[228:231],a[244:247],v[36:39]\n"
"s_lshl_b32 s59,s95,7\n"
TR "v[184:185],v2 offset:8192\n"
"v_fma_f32 v233,v233,v210,v216\n"
MF "v[36:39],a[188:191],a[240:243],v[36:39]\n"
"s_add_u32 m0,s79,32768\n"
TR "v[186:187],v2 offset:10240\n"
"v_fma_f32 v234,v234,v210,v216\n"
MF "v[36:39],a[148:151],v[188:191],v[36:39]\n"
LD "v200,s[48:51],s89 offen lds\n"
MF "v[28:31],a[144:147],v[188:191],v[28:31]\n"
"s_add_u32 s96,s96,s59\n"
TR "v[120:121],v1 offset:49152\n"
"v_fma_f32 v235,v235,v210,v216\n"
MF "v[28:31],a[184:187],a[240:243],v[28:31]\n"
"s_lshl_b32 s3,s92,10\n"
TR "v[122:123],v1 offset:51200\n"
CV "v228,v228,v229\n"
MF "v[28:31],a[224:227],a[244:247],v[28:31]\n"
"s_add_u32 s3,s3,s93\n"
TR "v[124:125],v1 offset:53248\n"
CV "v229,v230,v231\n"
MF "v[20:23],a[220:223],a[244:247],v[20:23]\n"
LD "v201,s[48:51],s89 offen offset:1024 lds\n"
MF "v[20:23],a[180:183],a[240:243],v[20:23]\n"
"s_cmp_lt_u32 s92,40\n"
"s_cselect_b32 s3,s3,0xf0000000\n"
TR "v[126:127],v1 offset:55296\n"
MF "v[20:23],a[140:143],v[188:191],v[20:23]\n"
"s_waitcnt lgkmcnt(4)\n"
TR "v[128:129],v1 offset:57344\n"
CV "v230,v232,v233\n"
MF "v[56:59],a[140:143],v[176:179],v[56:59]\n"
"s_cmp_lt_u32 s92,40\n"
"s_cselect_b32 s1,s96,0xf0000000\n"
TR "v[188:189],v3\n"
MF "v[56:59],a[180:183],v[180:183],v[56:59]\n"
"s_sub_u32 s92,s92,s4\n"
TR "v[190:191],v3 offset:2048\n"
CV "v231,v234,v235\n"
MF "v[56:59],a[220:223],v[184:187],v[56:59]\n"
LD "v202,s[48:51],s89 offen offset:2048 lds\n"
MF "v[64:67],a[224:227],v[184:187],v[64:67]\n"
"s_sub_u32 s2,s2,204800\n"
TR "a[240:241],v3 offset:4096\n"
AW "a48,v228\n"
MF "v[64:67],a[184:187],v[180:183],v[64:67]\n"
"s_mov_b32 s92,64\n"
TR "a[242:243],v3 offset:6144\n"
AW "a49,v229\n"
MF "v[64:67],a[144:147],v[176:179],v[64:67]\n"
LD "v203,s[48:51],s89 offen offset:3072 lds\n"
MF "v[72:75],a[148:151],v[176:179],v[72:75]\n"
TR "a[244:245],v3 offset:8192\n"
AW "a50,v230\n"
MF "v[72:75],a[188:191],v[180:183],v[72:75]\n"
TR "a[246:247],v3 offset:10240\n"
AW "a51,v231\n"
MF "v[72:75],a[228:231],v[184:187],v[72:75]\n"
TR "v[130:131],v1 offset:59392\n"
AR "v220,a88\n"
MF "v[80:83],a[232:235],v[184:187],v[80:83]\n"
TR "v[132:133],v5\n"
AR "v221,a89\n"
MF "v[80:83],a[192:195],v[180:183],v[80:83]\n"
TR "v[134:135],v5 offset:2048\n"
AR "v222,a90\n"
MF "v[80:83],a[152:155],v[176:179],v[80:83]\n"
TR "v[136:137],v5 offset:4096\n"
AR "v223,a91\n"
MF "v[88:91],a[156:159],v[176:179],v[88:91]\n"
TR "v[138:139],v5 offset:6144\n"
AR "v224,a108\n"
MF "v[88:91],a[196:199],v[180:183],v[88:91]\n"
TR "v[140:141],v5 offset:8192\n"
"s_waitcnt lgkmcnt(0)\n"
MF "v[88:91],a[236:239],v[184:187],v[88:91]\n"
"s_barrier\n"
MF "v[92:95],a[236:239],a[244:247],v[92:95]\n"
TR "v[142:143],v5 offset:10240\n"
AR "v225,a109\n"
MF "v[92:95],a[196:199],a[240:243],v[92:95]\n"
TR "v[144:145],v2 offset:49152\n"
AR "v226,a110\n"
MF "v[92:95],a[156:159],v[188:191],v[92:95]\n"
TR "v[146:147],v2 offset:51200\n"
AR "v227,a111\n"
MF "v[84:87],a[152:155],v[188:191],v[84:87]\n"
"s_nop 0\n"
TR "v[148:149],v2 offset:53248\n"
"ds_read_b128 a[108:111],v237 offset:8192\n"
MF "v[84:87],a[192:195],a[240:243],v[84:87]\n"
TR "v[150:151],v2 offset:55296\n"
"v_fma_f32 v220,v220,v210,v216\n"
MF "v[84:87],a[232:235],a[244:247],v[84:87]\n"
".Lpg32h:\n"
"s_cbranch_execz .Lpg36h\n"
TR "v[152:153],v2 offset:57344\n"
"v_fma_f32 v221,v221,v210,v216\n"
MF "v[76:79],a[228:231],a[244:247],v[76:79]\n"
TR "v[154:155],v2 offset:59392\n"
"v_fma_f32 v222,v222,v210,v216\n"
MF "v[76:79],a[188:191],a[240:243],v[76:79]\n"
TR "v[156:157],v6\n"
"v_fma_f32 v223,v223,v210,v216\n"
MF "v[76:79],a[148:151],v[188:191],v[76:79]\n"
TR "v[158:159],v6 offset:2048\n"
"v_fma_f32 v224,v224,v210,v216\n"
MF "v[68:71],a[144:147],v[188:191],v[68:71]\n"
TR "v[160:161],v6 offset:4096\n"
"v_fma_f32 v225,v225,v210,v216\n"
MF "v[68:71],a[184:187],a[240:243],v[68:71]\n"
TR "v[162:163],v6 offset:6144\n"
"v_fma_f32 v226,v226,v210,v216\n"
MF "v[68:71],a[224:227],a[244:247],v[68:71]\n"
TR "v[164:165],v6 offset:8192\n"
"v_fma_f32 v227,v227,v210,v216\n"
MF "v[60:63],a[220:223],a[244:247],v[60:63]\n"
TR "v[166:167],v6 offset:10240\n"
CV "v220,v220,v221\n"
MF "v[60:63],a[180:183],a[240:243],v[60:63]\n"
TR "v[168:169],v3 offset:49152\n"
CV "v221,v222,v223\n"
MF "v[60:63],a[140:143],v[188:191],v[60:63]\n"
TR "v[170:171],v3 offset:51200\n"
CV "v222,v224,v225\n"
MF "v[16:19],a[0:3],v[96:99],v[16:19]\n"
TR "v[172:173],v3 offset:53248\n"
CV "v223,v226,v227\n"
MF "v[16:19],a[40:43],v[100:103],v[16:19]\n"
TR "v[174:175],v3 offset:55296\n"
AW "a88,v220\n"
MF "v[16:19],a[80:83],v[104:107],v[16:19]\n"
TR "v[204:205],v3 offset:57344\n"
AW "a89,v221\n"
MF "v[16:19],a[20:23],v[108:111],v[16:19]\n"
TR "v[206:207],v3 offset:59392\n"
AW "a90,v222\n"
MF "v[16:19],a[60:63],v[112:115],v[16:19]\n"
TR "v[244:245],v7\n"
AW "a91,v223\n"
MF "v[16:19],a[100:103],v[116:119],v[16:19]\n"
"s_waitcnt lgkmcnt(15)\n"
TR "v[246:247],v7 offset:2048\n"
AR "v228,a12\n"
MF "v[20:23],a[100:103],v[140:143],v[20:23]\n"
TR "v[248:249],v7 offset:4096\n"
AR "v229,a13\n"
MF "v[20:23],a[60:63],v[136:139],v[20:23]\n"
TR "v[250:251],v7 offset:6144\n"
AR "v230,a14\n"
MF "v[20:23],a[20:23],v[132:135],v[20:23]\n"
TR "v[238:239],v7 offset:8192\n"
AR "v231,a15\n"
MF "v[20:23],a[80:83],v[128:131],v[20:23]\n"
TR "v[240:241],v7 offset:10240\n"
AR "v232,a32\n"
MF "v[20:23],a[40:43],v[124:127],v[20:23]\n"
AR "v233,a33\n"
AR "v234,a34\n"
MF "v[20:23],a[0:3],v[120:123],v[20:23]\n"
"s_waitcnt lgkmcnt(12)\n"
AR "v235,a35\n"
"v_fma_f32 v228,v228,v211,v217\n"
MF "v[56:59],a[0:3],v[144:147],v[56:59]\n"
"ds_read_b128 a[32:35],v236 offset:36864\n"
"v_fma_f32 v229,v229,v211,v217\n"
MF "v[56:59],a[40:43],v[148:151],v[56:59]\n"
"v_fma_f32 v230,v230,v211,v217\n"
"v_fma_f32 v231,v231,v211,v217\n"
MF "v[56:59],a[80:83],v[152:155],v[56:59]\n"
"v_fma_f32 v232,v232,v211,v217\n"
"v_fma_f32 v233,v233,v211,v217\n"
MF "v[56:59],a[20:23],v[156:159],v[56:59]\n"
"v_fma_f32 v234,v234,v211,v217\n"
"v_fma_f32 v235,v235,v211,v217\n"
MF "v[56:59],a[60:63],v[160:163],v[56:59]\n"
CV "v228,v228,v229\n"
CV "v229,v230,v231\n"
MF "v[56:59],a[100:103],v[164:167],v[56:59]\n"
"s_waitcnt lgkmcnt(1)\n"
CV "v230,v232,v233\n"
CV "v231,v234,v235\n"
MF "v[60:63],a[100:103],v[238:241],v[60:63]\n"
AW "a12,v228\n"
AW "a13,v229\n"
MF "v[60:63],a[60:63],v[248:251],v[60:63]\n"
AW "a14,v230\n"
AW "a15,v231\n"
MF "v[60:63],a[20:23],v[244:247],v[60:63]\n"
AR "v220,a52\n"
AR "v221,a53\n"
MF "v[60:63],a[80:83],v[204:207],v[60:63]\n"
AR "v222,a54\n"
AR "v223,a55\n"
MF "v[60:63],a[40:43],v[172:175],v[60:63]\n"
AR "v224,a72\n"
AR "v225,a73\n"
MF "v[60:63],a[0:3],v[168:171],v[60:63]\n"
AR "v226,a74\n"
AR "v227,a75\n"
MF "v[68:71],a[4:7],v[168:171],v[68:71]\n"
"ds_read_b128 a[72:75],v236 offset:57344\n"
"v_fma_f32 v220,v220,v211,v217\n"
MF "v[68:71],a[44:47],v[172:175],v[68:71]\n"
"v_fma_f32 v221,v221,v211,v217\n"
"v_fma_f32 v222,v222,v211,v217\n"
MF "v[68:71],a[84:87],v[204:207],v[68:71]\n"
"v_fma_f32 v223,v223,v211,v217\n"
"v_fma_f32 v224,v224,v211,v217\n"
MF "v[68:71],a[24:27],v[244:247],v[68:71]\n"
"v_fma_f32 v225,v225,v211,v217\n"
"v_fma_f32 v226,v226,v211,v217\n"
MF "v[68:71],a[64:67],v[248:251],v[68:71]\n"
"v_fma_f32 v227,v227,v211,v217\n"
CV "v220,v220,v221\n"
MF "v[68:71],a[104:107],v[238:241],v[68:71]\n"
"s_waitcnt lgkmcnt(0)\n"
CV "v221,v222,v223\n"
CV "v222,v224,v225\n"
MF "v[64:67],a[104:107],v[164:167],v[64:67]\n"
"s_barrier\n"
MF "v[64:67],a[64:67],v[160:163],v[64:67]\n"
CV "v223,v226,v227\n"
AW "a52,v220\n"
MF "v[64:67],a[24:27],v[156:159],v[64:67]\n"
AW "a53,v221\n"
AW "a54,v222\n"
MF "v[64:67],a[84:87],v[152:155],v[64:67]\n"
AW "a55,v223\n"
AR "v228,a92\n"
MF "v[64:67],a[44:47],v[148:151],v[64:67]\n"
AR "v229,a93\n"
AR "v230,a94\n"
MF "v[64:67],a[4:7],v[144:147],v[64:67]\n"
AR "v231,a95\n"
AR "v232,a112\n"
MF "v[28:31],a[4:7],v[120:123],v[28:31]\n"
AR "v233,a113\n"
AR "v234,a114\n"
MF "v[28:31],a[44:47],v[124:127],v[28:31]\n"
AR "v235,a115\n"
"v_fma_f32 v228,v228,v211,v217\n"
MF "v[28:31],a[84:87],v[128:131],v[28:31]\n"
"ds_read_b128 a[112:115],v237 offset:12288\n"
"v_fma_f32 v229,v229,v211,v217\n"
MF "v[28:31],a[24:27],v[132:135],v[28:31]\n"
"v_fma_f32 v230,v230,v211,v217\n"
"v_fma_f32 v231,v231,v211,v217\n"
MF "v[28:31],a[64:67],v[136:139],v[28:31]\n"
"v_fma_f32 v232,v232,v211,v217\n"
"v_fma_f32 v233,v233,v211,v217\n"
MF "v[28:31],a[104:107],v[140:143],v[28:31]\n"
"v_fma_f32 v234,v234,v211,v217\n"
"v_fma_f32 v235,v235,v211,v217\n"
MF "v[24:27],a[104:107],v[116:119],v[24:27]\n"
CV "v228,v228,v229\n"
CV "v229,v230,v231\n"
MF "v[24:27],a[64:67],v[112:115],v[24:27]\n"
CV "v230,v232,v233\n"
CV "v231,v234,v235\n"
MF "v[24:27],a[24:27],v[108:111],v[24:27]\n"
AW "a92,v228\n"
AW "a93,v229\n"
MF "v[24:27],a[84:87],v[104:107],v[24:27]\n"
AW "a94,v230\n"
AW "a95,v231\n"
MF "v[24:27],a[44:47],v[100:103],v[24:27]\n"
AR "v220,a16\n"
AR "v221,a17\n"
MF "v[24:27],a[4:7],v[96:99],v[24:27]\n"
AR "v222,a18\n"
AR "v223,a19\n"
MF "v[32:35],a[8:11],v[96:99],v[32:35]\n"
AR "v224,a36\n"
AR "v225,a37\n"
MF "v[32:35],a[48:51],v[100:103],v[32:35]\n"
AR "v226,a38\n"
AR "v227,a39\n"
MF "v[32:35],a[88:91],v[104:107],v[32:35]\n"
"ds_read_b128 a[36:39],v236 offset:40960\n"
"v_fma_f32 v220,v220,v212,v218\n"
MF "v[32:35],a[28:31],v[108:111],v[32:35]\n"
"v_fma_f32 v221,v221,v212,v218\n"
"v_fma_f32 v222,v222,v212,v218\n"
MF "v[32:35],a[68:71],v[112:115],v[32:35]\n"
"v_fma_f32 v223,v223,v212,v218\n"
"v_fma_f32 v224,v224,v212,v218\n"
MF "v[32:35],a[108:111],v[116:119],v[32:35]\n"
"v_fma_f32 v225,v225,v212,v218\n"
"v_fma_f32 v226,v226,v212,v218\n"
MF "v[36:39],a[108:111],v[140:143],v[36:39]\n"
"v_fma_f32 v227,v227,v212,v218\n"
CV "v220,v220,v221\n"
MF "v[36:39],a[68:71],v[136:139],v[36:39]\n"
CV "v221,v222,v223\n"
CV "v222,v224,v225\n"
MF "v[36:39],a[28:31],v[132:135],v[36:39]\n"
CV "v223,v226,v227\n"
AW "a16,v220\n"
MF "v[36:39],a[88:91],v[128:131],v[36:39]\n"
AW "a17,v221\n"
AW "a18,v222\n"
MF "v[36:39],a[48:51],v[124:127],v[36:39]\n"
AW "a19,v223\n"
AR "v228,a56\n"
MF "v[36:39],a[8:11],v[120:123],v[36:39]\n"
AR "v229,a57\n"
AR "v230,a58\n"
MF "v[72:75],a[8:11],v[144:147],v[72:75]\n"
AR "v231,a59\n"
AR "v232,a76\n"
MF "v[72:75],a[48:51],v[148:151],v[72:75]\n"
AR "v233,a77\n"
AR "v234,a78\n"
MF "v[72:75],a[88:91],v[152:155],v[72:75]\n"
AR "v235,a79\n"
"v_fma_f32 v228,v228,v212,v218\n"
MF "v[72:75],a[28:31],v[156:159],v[72:75]\n"
"ds_read_b128 a[76:79],v236 offset:61440\n"
"v_fma_f32 v229,v229,v212,v218\n"
MF "v[72:75],a[68:71],v[160:163],v[72:75]\n"
"v_fma_f32 v230,v230,v212,v218\n"
"v_fma_f32 v231,v231,v212,v218\n"
MF "v[72:75],a[108:111],v[164:167],v[72:75]\n"
"v_fma_f32 v232,v232,v212,v218\n"
"v_fma_f32 v233,v233,v212,v218\n"
MF "v[76:79],a[108:111],v[238:241],v[76:79]\n"
"v_fma_f32 v234,v234,v212,v218\n"
"v_fma_f32 v235,v235,v212,v218\n"
MF "v[76:79],a[68:71],v[248:251],v[76:79]\n"
CV "v228,v228,v229\n"
CV "v229,v230,v231\n"
MF "v[76:79],a[28:31],v[244:247],v[76:79]\n"
CV "v230,v232,v233\n"
CV "v231,v234,v235\n"
MF "v[76:79],a[88:91],v[204:207],v[76:79]\n"
AW "a56,v228\n"
AW "a57,v229\n"
MF "v[76:79],a[48:51],v[172:175],v[76:79]\n"
AW "a58,v230\n"
AW "a59,v231\n"
MF "v[76:79],a[8:11],v[168:171],v[76:79]\n"
"s_waitcnt lgkmcnt(2)\n"
AR "v220,a96\n"
AR "v221,a97\n"
MF "v[84:87],a[12:15],v[168:171],v[84:87]\n"
AR "v222,a98\n"
AR "v223,a99\n"
MF "v[84:87],a[52:55],v[172:175],v[84:87]\n"
AR "v224,a116\n"
AR "v225,a117\n"
MF "v[84:87],a[92:95],v[204:207],v[84:87]\n"
AR "v226,a118\n"
AR "v227,a119\n"
MF "v[84:87],a[32:35],v[244:247],v[84:87]\n"
"ds_read_b128 a[116:119],v237 offset:16384\n"
"v_fma_f32 v220,v220,v212,v218\n"
MF "v[84:87],a[72:75],v[248:251],v[84:87]\n"
"v_fma_f32 v221,v221,v212,v218\n"
"v_fma_f32 v222,v222,v212,v218\n"
MF "v[84:87],a[112:115],v[238:241],v[84:87]\n"
"v_fma_f32 v223,v223,v212,v218\n"
"v_fma_f32 v224,v224,v212,v218\n"
MF "v[80:83],a[112:115],v[164:167],v[80:83]\n"
"v_fma_f32 v225,v225,v212,v218\n"
"v_fma_f32 v226,v226,v212,v218\n"
MF "v[80:83],a[72:75],v[160:163],v[80:83]\n"
"v_fma_f32 v227,v227,v212,v218\n"
CV "v220,v220,v221\n"
MF "v[80:83],a[32:35],v[156:159],v[80:83]\n"
CV "v221,v222,v223\n"
CV "v222,v224,v225\n"
MF "v[80:83],a[92:95],v[152:155],v[80:83]\n"
CV "v223,v226,v227\n"
AW "a96,v220\n"
MF "v[80:83],a[52:55],v[148:151],v[80:83]\n"
AW "a97,v221\n"
AW "a98,v222\n"
MF "v[80:83],a[12:15],v[144:147],v[80:83]\n"
AW "a99,v223\n"
"v_mov_b32 v15,0\n"
MF "v[44:47],a[12:15],v[120:123],v[44:47]\n"
"v_lshrrev_b32 v242,5,v9\n"
"v_min_u32 v242,23,v242\n"
MF "v[44:47],a[52:55],v[124:127],v[44:47]\n"
"v_mul_u32_u24 v242,160,v242\n"
"v_and_or_b32 v242,v9,16,v242\n"
MF "v[44:47],a[92:95],v[128:131],v[44:47]\n"
"v_lshlrev_b32 v242,3,v242\n"
AR "v8,a252\n"
MF "v[44:47],a[32:35],v[132:135],v[44:47]\n"
AR "v11,a253\n"
"v_bfe_u32 v224,v9,5,3\n"
MF "v[44:47],a[72:75],v[136:139],v[44:47]\n"
"s_add_u32 s62,s74,s68\n"
"s_add_u32 s68,s68,256\n"
"s_and_b32 s68,s68,768\n"
MF "v[44:47],a[112:115],v[140:143],v[44:47]\n"
"buffer_load_dword v243,v242,s[48:51],s62 offen\n"
MF "v[40:43],a[112:115],v[116:119],v[40:43]\n"
"v_bfe_u32 v225,v9,8,2\n"
"v_lshlrev_b32 v225,1,v225\n"
MF "v[40:43],a[72:75],v[112:115],v[40:43]\n"
"v_mov_b32 v14,s76\n"
"s_add_u32 s76,s76,1\n"
MF "v[40:43],a[32:35],v[108:111],v[40:43]\n"
"buffer_store_dword v14,v15,s[40:43],s94 offen sc1\n"
MF "v[40:43],a[92:95],v[104:107],v[40:43]\n"
"v_and_b32 v226,16,v9\n"
"v_lshlrev_b32_e64 v226,v226,s9\n"
MF "v[40:43],a[52:55],v[100:103],v[40:43]\n"
"v_sub_u32 v224,v224,v225\n"
"v_cmp_eq_u32 vcc,0,v224\n"
MF "v[40:43],a[12:15],v[96:99],v[40:43]\n"
"s_waitcnt lgkmcnt(0)\n"
"v_cndmask_b32 v220,0,v226,vcc\n"
"v_cmp_eq_u32 vcc,1,v224\n"
MF "v[48:51],a[16:19],v[96:99],v[48:51]\n"
"s_nop 0\n"
"v_cndmask_b32 v221,0,v226,vcc\n"
AW "a248,0\n"
MF "v[48:51],a[56:59],v[100:103],v[48:51]\n"
AW "a249,0\n"
AW "a250,v220\n"
MF "v[48:51],a[96:99],v[104:107],v[48:51]\n"
AW "a251,v221\n"
AW "a252,v220\n"
MF "v[48:51],a[36:39],v[108:111],v[48:51]\n"
AW "a253,v221\n"
AW "a254,0\n"
MF "v[48:51],a[76:79],v[112:115],v[48:51]\n"
AW "a255,0\n"
"v_bfe_u32 v12,v9,4,4\n"
MF "v[48:51],a[116:119],v[116:119],v[48:51]\n"
"v_lshlrev_b32 v12,2,v12\n"
"v_lshrrev_b32 v13,2,v9\n"
MF "v[52:55],a[116:119],v[140:143],v[52:55]\n"
"v_add_u32 v13,s58,v13\n"
"v_add_u32 v4,24576,v4\n"
MF "v[52:55],a[76:79],v[136:139],v[52:55]\n"
"s_waitcnt vmcnt(2) lgkmcnt(0)\n"
"v_add_u32 v5,24576,v5\n"
"v_add_u32 v6,24576,v6\n"
MF "v[52:55],a[36:39],v[132:135],v[52:55]\n"
"s_barrier\n"
MF "v[52:55],a[96:99],v[128:131],v[52:55]\n"
TR "v[176:177],v0 offset:24576\n"
"v_add_u32 v7,24576,v7\n"
MF "v[52:55],a[56:59],v[124:127],v[52:55]\n"
TR "v[178:179],v0 offset:26624\n"
MF "v[52:55],a[16:19],v[120:123],v[52:55]\n"
MF "v[88:91],a[16:19],v[144:147],v[88:91]\n"
TR "v[180:181],v0 offset:28672\n"
MF "v[88:91],a[56:59],v[148:151],v[88:91]\n"
TR "v[182:183],v0 offset:30720\n"
MF "v[88:91],a[96:99],v[152:155],v[88:91]\n"
MF "v[88:91],a[36:39],v[156:159],v[88:91]\n"
TR "v[184:185],v0 offset:32768\n"
MF "v[88:91],a[76:79],v[160:163],v[88:91]\n"
TR "v[186:187],v0 offset:34816\n"
MF "v[88:91],a[116:119],v[164:167],v[88:91]\n"
MF "v[92:95],a[116:119],v[238:241],v[92:95]\n"
MF "v[92:95],a[76:79],v[248:251],v[92:95]\n"
MF "v[92:95],a[36:39],v[244:247],v[92:95]\n"
MF "v[92:95],a[96:99],v[204:207],v[92:95]\n"
MF "v[92:95],a[56:59],v[172:175],v[92:95]\n"
MF "v[92:95],a[16:19],v[168:171],v[92:95]\n"
"s_waitcnt lgkmcnt(0)\n"
".Lpl15:\n"
MF "v[96:99],a[0:3],v[176:179],v[252:255]\n"
TR "v[188:189],v1 offset:24576\n"
MF "v[96:99],a[40:43],v[180:183],v[96:99]\n"
TR "v[190:191],v1 offset:26624\n"
MF "v[96:99],a[80:83],v[184:187],v[96:99]\n"
MF "v[104:107],a[84:87],v[184:187],v[252:255]\n"
TR "a[240:241],v1 offset:28672\n"
MF "v[104:107],a[44:47],v[180:183],v[104:107]\n"
TR "a[242:243],v1 offset:30720\n"
MF "v[104:107],a[4:7],v[176:179],v[104:107]\n"
MF "v[112:115],a[8:11],v[176:179],v[252:255]\n"
TR "a[244:245],v1 offset:32768\n"
MF "v[112:115],a[48:51],v[180:183],v[112:115]\n"
TR "a[246:247],v1 offset:34816\n"
MF "v[112:115],a[88:91],v[184:187],v[112:115]\n"
MF "v[120:123],a[92:95],v[184:187],v[252:255]\n"
MF "v[120:123],a[52:55],v[180:183],v[120:123]\n"
MF "v[120:123],a[12:15],v[176:179],v[120:123]\n"
MF "v[128:131],a[16:19],v[176:179],v[252:255]\n"
MF "v[128:131],a[56:59],v[180:183],v[128:131]\n"
MF "v[128:131],a[96:99],v[184:187],v[128:131]\n"
"s_waitcnt lgkmcnt(0)\n"
MF "v[132:135],a[96:99],a[244:247],v[252:255]\n"
TR "v[176:177],v2 offset:24576\n"
MF "v[132:135],a[56:59],a[240:243],v[132:135]\n"
TR "v[178:179],v2 offset:26624\n"
MF "v[132:135],a[16:19],v[188:191],v[132:135]\n"
MF "v[124:127],a[12:15],v[188:191],v[252:255]\n"
TR "v[180:181],v2 offset:28672\n"
MF "v[124:127],a[52:55],a[240:243],v[124:127]\n"
TR "v[182:183],v2 offset:30720\n"
MF "v[124:127],a[92:95],a[244:247],v[124:127]\n"
MF "v[116:119],a[88:91],a[244:247],v[252:255]\n"
TR "v[184:185],v2 offset:32768\n"
MF "v[116:119],a[48:51],a[240:243],v[116:119]\n"
TR "v[186:187],v2 offset:34816\n"
MF "v[116:119],a[8:11],v[188:191],v[116:119]\n"
MF "v[108:111],a[4:7],v[188:191],v[252:255]\n"
MF "v[108:111],a[44:47],a[240:243],v[108:111]\n"
MF "v[108:111],a[84:87],a[244:247],v[108:111]\n"
MF "v[100:103],a[80:83],a[244:247],v[252:255]\n"
MF "v[100:103],a[40:43],a[240:243],v[100:103]\n"
MF "v[100:103],a[0:3],v[188:191],v[100:103]\n"
"s_waitcnt lgkmcnt(0)\n"
MF "v[136:139],a[0:3],v[176:179],v[252:255]\n"
TR "v[188:189],v3 offset:24576\n"
MF "v[136:139],a[40:43],v[180:183],v[136:139]\n"
TR "v[190:191],v3 offset:26624\n"
MF "v[136:139],a[80:83],v[184:187],v[136:139]\n"
MF "v[144:147],a[84:87],v[184:187],v[252:255]\n"
TR "a[240:241],v3 offset:28672\n"
MF "v[144:147],a[44:47],v[180:183],v[144:147]\n"
TR "a[242:243],v3 offset:30720\n"
MF "v[144:147],a[4:7],v[176:179],v[144:147]\n"
MF "v[152:155],a[8:11],v[176:179],v[252:255]\n"
TR "a[244:245],v3 offset:32768\n"
MF "v[152:155],a[48:51],v[180:183],v[152:155]\n"
TR "a[246:247],v3 offset:34816\n"
MF "v[152:155],a[88:91],v[184:187],v[152:155]\n"
MF "v[160:163],a[92:95],v[184:187],v[252:255]\n"
MF "v[160:163],a[52:55],v[180:183],v[160:163]\n"
MF "v[160:163],a[12:15],v[176:179],v[160:163]\n"
MF "v[168:171],a[16:19],v[176:179],v[252:255]\n"
MF "v[168:171],a[56:59],v[180:183],v[168:171]\n"
"s_waitcnt lgkmcnt(0)\n"
MF "v[168:171],a[96:99],v[184:187],v[168:171]\n"
"s_barrier\n"
MF "v[172:175],a[96:99],a[244:247],v[252:255]\n"
TR "v[176:177],v0\n"
MF "v[172:175],a[56:59],a[240:243],v[172:175]\n"
TR "v[178:179],v0 offset:2048\n"
MF "v[172:175],a[16:19],v[188:191],v[172:175]\n"
"ds_read2st64_b32 v[192:193],v13 offset0:192 offset1:193\n"
MF "v[164:167],a[12:15],v[188:191],v[252:255]\n"
TR "v[180:181],v0 offset:4096\n"
MF "v[164:167],a[52:55],a[240:243],v[164:167]\n"
TR "v[182:183],v0 offset:6144\n"
MF "v[164:167],a[92:95],a[244:247],v[164:167]\n"
"ds_read2st64_b32 v[194:195],v13 offset0:194 offset1:195\n"
MF "v[156:159],a[88:91],a[244:247],v[252:255]\n"
TR "v[184:185],v0 offset:8192\n"
MF "v[156:159],a[48:51],a[240:243],v[156:159]\n"
TR "v[186:187],v0 offset:10240\n"
MF "v[156:159],a[8:11],v[188:191],v[156:159]\n"
"ds_read2st64_b32 v[196:197],v13 offset0:196 offset1:197\n"
MF "v[148:151],a[4:7],v[188:191],v[252:255]\n"
"ds_read2st64_b32 v[198:199],v13 offset0:198 offset1:199\n"
MF "v[148:151],a[44:47],a[240:243],v[148:151]\n"
MF "v[148:151],a[84:87],a[244:247],v[148:151]\n"
MF "v[140:143],a[80:83],a[244:247],v[252:255]\n"
MF "v[140:143],a[40:43],a[240:243],v[140:143]\n"
MF "v[140:143],a[0:3],v[188:191],v[140:143]\n"
"s_waitcnt lgkmcnt(2)\n"
MF "v[96:99],a[120:123],v[176:179],v[96:99]\n"
"s_waitcnt lgkmcnt(0)\n"
TR "v[188:189],v1\n"
"v_add_f32 v16,v16,v192\n"
MF "v[96:99],a[160:163],v[180:183],v[96:99]\n"
TR "v[190:191],v1 offset:2048\n"
"v_add_f32 v17,v17,v193\n"
MF "v[96:99],a[200:203],v[184:187],v[96:99]\n"
"ds_read2st64_b32 v[200:201],v13 offset0:200 offset1:201\n"
"v_add_f32 v18,v18,v194\n"
"s_waitcnt vmcnt(0)\n"
"v_mov_b32 v14,s76\n"
"s_add_u32 s76,s76,1\n"
MF "v[104:107],a[204:207],v[184:187],v[104:107]\n"
TR "a[240:241],v1 offset:4096\n"
"v_add_f32 v19,v19,v195\n"
"buffer_store_dword v14,v15,s[40:43],s94 offen sc1\n"
MF "v[104:107],a[164:167],v[180:183],v[104:107]\n"
TR "a[242:243],v1 offset:6144\n"
"v_add_f32 v20,v20,v196\n"
MF "v[104:107],a[124:127],v[176:179],v[104:107]\n"
"ds_read2st64_b32 v[202:203],v13 offset0:202 offset1:203\n"
"v_add_f32 v21,v21,v197\n"
MF "v[112:115],a[128:131],v[176:179],v[112:115]\n"
TR "a[244:245],v1 offset:8192\n"
"v_add_f32 v22,v22,v198\n"
MF "v[112:115],a[168:171],v[180:183],v[112:115]\n"
TR "a[246:247],v1 offset:10240\n"
"v_add_f32 v23,v23,v199\n"
MF "v[112:115],a[208:211],v[184:187],v[112:115]\n"
"ds_read2st64_b32 v[204:205],v13 offset0:204 offset1:205\n"
CV "v16,v16,v17\n"
MF "v[120:123],a[212:215],v[184:187],v[120:123]\n"
"ds_read2st64_b32 v[206:207],v13 offset0:206 offset1:207\n"
CV "v17,v18,v19\n"
MF "v[120:123],a[172:175],v[180:183],v[120:123]\n"
"s_add_u32 s87,s90,0\n"
CV "v18,v20,v21\n"
CV "v19,v22,v23\n"
MF "v[120:123],a[132:135],v[176:179],v[120:123]\n"
MF "v[128:131],a[136:139],v[176:179],v[128:131]\n"
"buffer_store_dwordx4 v[16:19],v9,s[40:43],s87 offen sc1\n"
MF "v[128:131],a[176:179],v[180:183],v[128:131]\n"
MF "v[128:131],a[216:219],v[184:187],v[128:131]\n"
"s_waitcnt lgkmcnt(2)\n"
MF "v[132:135],a[216:219],a[244:247],v[132:135]\n"
"s_waitcnt lgkmcnt(0)\n"
TR "v[176:177],v2\n"
"v_add_f32 v24,v24,v200\n"
MF "v[132:135],a[176:179],a[240:243],v[132:135]\n"
TR "v[178:179],v2 offset:2048\n"
"v_add_f32 v25,v25,v201\n"
MF "v[132:135],a[136:139],v[188:191],v[132:135]\n"
"ds_read2st64_b32 v[192:193],v13 offset0:208 offset1:209\n"
"v_add_f32 v26,v26,v202\n"
MF "v[124:127],a[132:135],v[188:191],v[124:127]\n"
TR "v[180:181],v2 offset:4096\n"
"v_add_f32 v27,v27,v203\n"
MF "v[124:127],a[172:175],a[240:243],v[124:127]\n"
TR "v[182:183],v2 offset:6144\n"
"v_add_f32 v28,v28,v204\n"
MF "v[124:127],a[212:215],a[244:247],v[124:127]\n"
"ds_read2st64_b32 v[194:195],v13 offset0:210 offset1:211\n"
"v_add_f32 v29,v29,v205\n"
MF "v[116:119],a[208:211],a[244:247],v[116:119]\n"
TR "v[184:185],v2 offset:8192\n"
"v_add_f32 v30,v30,v206\n"
MF "v[116:119],a[168:171],a[240:243],v[116:119]\n"
TR "v[186:187],v2 offset:10240\n"
"v_add_f32 v31,v31,v207\n"
MF "v[116:119],a[128:131],v[188:191],v[116:119]\n"
"ds_read2st64_b32 v[196:197],v13 offset0:212 offset1:213\n"
CV "v24,v24,v25\n"
MF "v[108:111],a[124:127],v[188:191],v[108:111]\n"
".Lpg33h:\n"
"s_cbranch_execz .Lpg37h\n"
"ds_read2st64_b32 v[198:199],v13 offset0:214 offset1:215\n"
CV "v25,v26,v27\n"
MF "v[108:111],a[164:167],a[240:243],v[108:111]\n"
"s_add_u32 s87,s90,1024\n"
CV "v26,v28,v29\n"
CV "v27,v30,v31\n"
MF "v[108:111],a[204:207],a[244:247],v[108:111]\n"
MF "v[100:103],a[200:203],a[244:247],v[100:103]\n"
"buffer_store_dwordx4 v[24:27],v9,s[40:43],s87 offen sc1\n"
MF "v[100:103],a[160:163],a[240:243],v[100:103]\n"
MF "v[100:103],a[120:123],v[188:191],v[100:103]\n"
"s_waitcnt lgkmcnt(2)\n"
MF "v[136:139],a[120:123],v[176:179],v[136:139]\n"
"s_waitcnt lgkmcnt(0)\n"
TR "v[188:189],v3\n"
"v_add_f32 v32,v32,v192\n"
MF "v[136:139],a[160:163],v[180:183],v[136:139]\n"
TR "v[190:191],v3 offset:2048\n"
"v_add_f32 v33,v33,v193\n"
MF "v[136:139],a[200:203],v[184:187],v[136:139]\n"
"ds_read2st64_b32 v[200:201],v13 offset0:216 offset1:217\n"
"v_add_f32 v34,v34,v194\n"
MF "v[144:147],a[204:207],v[184:187],v[144:147]\n"
TR "a[240:241],v3 offset:4096\n"
"v_add_f32 v35,v35,v195\n"
MF "v[144:147],a[164:167],v[180:183],v[144:147]\n"
TR "a[242:243],v3 offset:6144\n"
"v_add_f32 v36,v36,v196\n"
MF "v[144:147],a[124:127],v[176:179],v[144:147]\n"
"ds_read2st64_b32 v[202:203],v13 offset0:218 offset1:219\n"
"v_add_f32 v37,v37,v197\n"
MF "v[152:155],a[128:131],v[176:179],v[152:155]\n"
TR "a[244:245],v3 offset:8192\n"
"v_add_f32 v38,v38,v198\n"
MF "v[152:155],a[168:171],v[180:183],v[152:155]\n"
TR "a[246:247],v3 offset:10240\n"
"v_add_f32 v39,v39,v199\n"
MF "v[152:155],a[208:211],v[184:187],v[152:155]\n"
"ds_read2st64_b32 v[204:205],v13 offset0:220 offset1:221\n"
CV "v32,v32,v33\n"
MF "v[160:163],a[212:215],v[184:187],v[160:163]\n"
"ds_read2st64_b32 v[206:207],v13 offset0:222 offset1:223\n"
CV "v33,v34,v35\n"
MF "v[160:163],a[172:175],v[180:183],v[160:163]\n"
"s_add_u32 s87,s90,2048\n"
CV "v34,v36,v37\n"
CV "v35,v38,v39\n"
MF "v[160:163],a[132:135],v[176:179],v[160:163]\n"
MF "v[168:171],a[136:139],v[176:179],v[168:171]\n"
"buffer_store_dwordx4 v[32:35],v9,s[40:43],s87 offen sc1\n"
MF "v[168:171],a[176:179],v[180:183],v[168:171]\n"
"s_waitcnt lgkmcnt(0)\n"
MF "v[168:171],a[216:219],v[184:187],v[168:171]\n"
"s_barrier\n"
MF "v[172:175],a[216:219],a[244:247],v[172:175]\n"
TR "v[176:177],v0 offset:49152\n"
"v_add_f32 v40,v40,v200\n"
MF "v[172:175],a[176:179],a[240:243],v[172:175]\n"
TR "v[178:179],v0 offset:51200\n"
"v_add_f32 v41,v41,v201\n"
MF "v[172:175],a[136:139],v[188:191],v[172:175]\n"
"ds_read2st64_b32 v[192:193],v13 offset0:224 offset1:225\n"
"v_add_f32 v42,v42,v202\n"
MF "v[164:167],a[132:135],v[188:191],v[164:167]\n"
TR "v[180:181],v0 offset:53248\n"
"v_add_f32 v43,v43,v203\n"
MF "v[164:167],a[172:175],a[240:243],v[164:167]\n"
TR "v[182:183],v0 offset:55296\n"
"v_add_f32 v44,v44,v204\n"
MF "v[164:167],a[212:215],a[244:247],v[164:167]\n"
"ds_read2st64_b32 v[194:195],v13 offset0:226 offset1:227\n"
"v_add_f32 v45,v45,v205\n"
MF "v[156:159],a[208:211],a[244:247],v[156:159]\n"
TR "v[184:185],v0 offset:57344\n"
"v_add_f32 v46,v46,v206\n"
MF "v[156:159],a[168:171],a[240:243],v[156:159]\n"
TR "v[186:187],v0 offset:59392\n"
"v_add_f32 v47,v47,v207\n"
MF "v[156:159],a[128:131],v[188:191],v[156:159]\n"
"ds_read2st64_b32 v[196:197],v13 offset0:228 offset1:229\n"
CV "v40,v40,v41\n"
MF "v[148:151],a[124:127],v[188:191],v[148:151]\n"
"ds_read2st64_b32 v[198:199],v13 offset0:230 offset1:231\n"
CV "v41,v42,v43\n"
MF "v[148:151],a[164:167],a[240:243],v[148:151]\n"
"s_add_u32 s87,s90,3072\n"
CV "v42,v44,v45\n"
CV "v43,v46,v47\n"
MF "v[148:151],a[204:207],a[244:247],v[148:151]\n"
MF "v[140:143],a[200:203],a[244:247],v[140:143]\n"
"buffer_store_dwordx4 v[40:43],v9,s[40:43],s87 offen sc1\n"
MF "v[140:143],a[160:163],a[240:243],v[140:143]\n"
MF "v[140:143],a[120:123],v[188:191],v[140:143]\n"
"s_waitcnt lgkmcnt(2)\n"
MF "v[96:99],a[20:23],v[176:179],v[96:99]\n"
"s_waitcnt lgkmcnt(0)\n"
TR "v[188:189],v1 offset:49152\n"
"v_add_f32 v48,v48,v192\n"
MF "v[96:99],a[60:63],v[180:183],v[96:99]\n"
TR "v[190:191],v1 offset:51200\n"
"v_add_f32 v49,v49,v193\n"
MF "v[96:99],a[100:103],v[184:187],v[96:99]\n"
"ds_read2st64_b32 v[200:201],v13 offset0:0 offset1:1\n"
"v_add_f32 v50,v50,v194\n"
MF "v[104:107],a[104:107],v[184:187],v[104:107]\n"
TR "a[240:241],v1 offset:53248\n"
"v_add_f32 v51,v51,v195\n"
MF "v[104:107],a[64:67],v[180:183],v[104:107]\n"
TR "a[242:243],v1 offset:55296\n"
"v_add_f32 v52,v52,v196\n"
MF "v[104:107],a[24:27],v[176:179],v[104:107]\n"
"ds_read2st64_b32 v[202:203],v13 offset0:2 offset1:3\n"
"v_add_f32 v53,v53,v197\n"
MF "v[112:115],a[28:31],v[176:179],v[112:115]\n"
TR "a[244:245],v1 offset:57344\n"
"v_add_f32 v54,v54,v198\n"
MF "v[112:115],a[68:71],v[180:183],v[112:115]\n"
TR "a[246:247],v1 offset:59392\n"
"v_add_f32 v55,v55,v199\n"
MF "v[112:115],a[108:111],v[184:187],v[112:115]\n"
"ds_read2st64_b32 v[204:205],v13 offset0:4 offset1:5\n"
CV "v48,v48,v49\n"
MF "v[120:123],a[112:115],v[184:187],v[120:123]\n"
"ds_read2st64_b32 v[206:207],v13 offset0:6 offset1:7\n"
CV "v49,v50,v51\n"
MF "v[120:123],a[72:75],v[180:183],v[120:123]\n"
"s_add_u32 s87,s90,4096\n"
CV "v50,v52,v53\n"
CV "v51,v54,v55\n"
MF "v[120:123],a[32:35],v[176:179],v[120:123]\n"
MF "v[128:131],a[36:39],v[176:179],v[128:131]\n"
"buffer_store_dwordx4 v[48:51],v9,s[40:43],s87 offen sc1\n"
MF "v[128:131],a[76:79],v[180:183],v[128:131]\n"
MF "v[128:131],a[116:119],v[184:187],v[128:131]\n"
"s_waitcnt lgkmcnt(2)\n"
MF "v[132:135],a[116:119],a[244:247],v[132:135]\n"
"s_waitcnt lgkmcnt(0)\n"
TR "v[176:177],v2 offset:49152\n"
"v_add_f32 v56,v56,v200\n"
MF "v[132:135],a[76:79],a[240:243],v[132:135]\n"
TR "v[178:179],v2 offset:51200\n"
"v_add_f32 v57,v57,v201\n"
MF "v[132:135],a[36:39],v[188:191],v[132:135]\n"
"ds_read2st64_b32 v[192:193],v13 offset0:8 offset1:9\n"
"v_add_f32 v58,v58,v202\n"
MF "v[124:127],a[32:35],v[188:191],v[124:127]\n"
TR "v[180:181],v2 offset:53248\n"
"v_add_f32 v59,v59,v203\n"
MF "v[124:127],a[72:75],a[240:243],v[124:127]\n"
TR "v[182:183],v2 offset:55296\n"
"v_add_f32 v60,v60,v204\n"
MF "v[124:127],a[112:115],a[244:247],v[124:127]\n"
"ds_read2st64_b32 v[194:195],v13 offset0:10 offset1:11\n"
"v_add_f32 v61,v61,v205\n"
MF "v[116:119],a[108:111],a[244:247],v[116:119]\n"
TR "v[184:185],v2 offset:57344\n"
"v_add_f32 v62,v62,v206\n"
MF "v[116:119],a[68:71],a[240:243],v[116:119]\n"
TR "v[186:187],v2 offset:59392\n"
"v_add_f32 v63,v63,v207\n"
MF "v[116:119],a[28:31],v[188:191],v[116:119]\n"
"ds_read2st64_b32 v[196:197],v13 offset0:12 offset1:13\n"
CV "v56,v56,v57\n"
MF "v[108:111],a[24:27],v[188:191],v[108:111]\n"
"ds_read2st64_b32 v[198:199],v13 offset0:14 offset1:15\n"
CV "v57,v58,v59\n"
MF "v[108:111],a[64:67],a[240:243],v[108:111]\n"
"s_add_u32 s87,s90,10240\n"
CV "v58,v60,v61\n"
CV "v59,v62,v63\n"
MF "v[108:111],a[104:107],a[244:247],v[108:111]\n"
MF "v[100:103],a[100:103],a[244:247],v[100:103]\n"
"buffer_store_dwordx4 v[56:59],v9,s[40:43],s87 offen sc1\n"
MF "v[100:103],a[60:63],a[240:243],v[100:103]\n"
MF "v[100:103],a[20:23],v[188:191],v[100:103]\n"
"s_waitcnt lgkmcnt(2)\n"
MF "v[136:139],a[20:23],v[176:179],v[136:139]\n"
"s_waitcnt lgkmcnt(0)\n"
TR "v[188:189],v3 offset:49152\n"
"v_add_f32 v64,v64,v192\n"
MF "v[136:139],a[60:63],v[180:183],v[136:139]\n"
TR "v[190:191],v3 offset:51200\n"
"v_add_f32 v65,v65,v193\n"
MF "v[136:139],a[100:103],v[184:187],v[136:139]\n"
"ds_read2st64_b32 v[200:201],v13 offset0:16 offset1:17\n"
"v_add_f32 v66,v66,v194\n"
MF "v[144:147],a[104:107],v[184:187],v[144:147]\n"
TR "a[240:241],v3 offset:53248\n"
"v_add_f32 v67,v67,v195\n"
MF "v[144:147],a[64:67],v[180:183],v[144:147]\n"
TR "a[242:243],v3 offset:55296\n"
"v_add_f32 v68,v68,v196\n"
MF "v[144:147],a[24:27],v[176:179],v[144:147]\n"
"ds_read2st64_b32 v[202:203],v13 offset0:18 offset1:19\n"
"v_add_f32 v69,v69,v197\n"
MF "v[152:155],a[28:31],v[176:179],v[152:155]\n"
TR "a[244:245],v3 offset:57344\n"
"v_add_f32 v70,v70,v198\n"
MF "v[152:155],a[68:71],v[180:183],v[152:155]\n"
TR "a[246:247],v3 offset:59392\n"
"v_add_f32 v71,v71,v199\n"
MF "v[152:155],a[108:111],v[184:187],v[152:155]\n"
"ds_read2st64_b32 v[204:205],v13 offset0:20 offset1:21\n"
CV "v64,v64,v65\n"
MF "v[160:163],a[112:115],v[184:187],v[160:163]\n"
"ds_read2st64_b32 v[206:207],v13 offset0:22 offset1:23\n"
CV "v65,v66,v67\n"
MF "v[160:163],a[72:75],v[180:183],v[160:163]\n"
"s_add_u32 s87,s90,11264\n"
CV "v66,v68,v69\n"
CV "v67,v70,v71\n"
MF "v[160:163],a[32:35],v[176:179],v[160:163]\n"
MF "v[168:171],a[36:39],v[176:179],v[168:171]\n"
"buffer_store_dwordx4 v[64:67],v9,s[40:43],s87 offen sc1\n"
MF "v[168:171],a[76:79],v[180:183],v[168:171]\n"
"s_waitcnt lgkmcnt(0)\n"
MF "v[168:171],a[116:119],v[184:187],v[168:171]\n"
"s_barrier\n"
MF "v[172:175],a[116:119],a[244:247],v[172:175]\n"
TR "v[176:177],v4\n"
"v_add_f32 v72,v72,v200\n"
MF "v[172:175],a[76:79],a[240:243],v[172:175]\n"
TR "v[178:179],v4 offset:2048\n"
"v_add_f32 v73,v73,v201\n"
MF "v[172:175],a[36:39],v[188:191],v[172:175]\n"
"ds_read2st64_b32 v[192:193],v13 offset0:24 offset1:25\n"
"v_add_f32 v74,v74,v202\n"
MF "v[164:167],a[32:35],v[188:191],v[164:167]\n"
TR "v[180:181],v4 offset:4096\n"
"v_add_f32 v75,v75,v203\n"
MF "v[164:167],a[72:75],a[240:243],v[164:167]\n"
TR "v[182:183],v4 offset:6144\n"
"v_add_f32 v76,v76,v204\n"
MF "v[164:167],a[112:115],a[244:247],v[164:167]\n"
"ds_read2st64_b32 v[194:195],v13 offset0:26 offset1:27\n"
"v_add_f32 v77,v77,v205\n"
MF "v[156:159],a[108:111],a[244:247],v[156:159]\n"
TR "v[184:185],v4 offset:8192\n"
"v_add_f32 v78,v78,v206\n"
MF "v[156:159],a[68:71],a[240:243],v[156:159]\n"
TR "v[186:187],v4 offset:10240\n"
"v_add_f32 v79,v79,v207\n"
MF "v[156:159],a[28:31],v[188:191],v[156:159]\n"
"ds_read2st64_b32 v[196:197],v13 offset0:28 offset1:29\n"
CV "v72,v72,v73\n"
MF "v[148:151],a[24:27],v[188:191],v[148:151]\n"
"ds_read2st64_b32 v[198:199],v13 offset0:30 offset1:31\n"
CV "v73,v74,v75\n"
MF "v[148:151],a[64:67],a[240:243],v[148:151]\n"
"s_add_u32 s87,s90,12288\n"
CV "v74,v76,v77\n"
CV "v75,v78,v79\n"
MF "v[148:151],a[104:107],a[244:247],v[148:151]\n"
MF "v[140:143],a[100:103],a[244:247],v[140:143]\n"
"buffer_store_dwordx4 v[72:75],v9,s[40:43],s87 offen sc1\n"
MF "v[140:143],a[60:63],a[240:243],v[140:143]\n"
MF "v[140:143],a[20:23],v[188:191],v[140:143]\n"
"s_waitcnt lgkmcnt(2)\n"
MF "v[96:99],a[140:143],v[176:179],v[96:99]\n"
"s_waitcnt lgkmcnt(0)\n"
TR "v[188:189],v5\n"
"v_add_f32 v80,v80,v192\n"
MF "v[96:99],a[180:183],v[180:183],v[96:99]\n"
TR "v[190:191],v5 offset:2048\n"
"v_add_f32 v81,v81,v193\n"
MF "v[96:99],a[220:223],v[184:187],v[96:99]\n"
"ds_read2st64_b32 v[200:201],v13 offset0:32 offset1:33\n"
"v_add_f32 v82,v82,v194\n"
MF "v[104:107],a[224:227],v[184:187],v[104:107]\n"
TR "a[240:241],v5 offset:4096\n"
"v_add_f32 v83,v83,v195\n"
MF "v[104:107],a[184:187],v[180:183],v[104:107]\n"
TR "a[242:243],v5 offset:6144\n"
"v_add_f32 v84,v84,v196\n"
MF "v[104:107],a[144:147],v[176:179],v[104:107]\n"
"ds_read2st64_b32 v[202:203],v13 offset0:34 offset1:35\n"
"v_add_f32 v85,v85,v197\n"
MF "v[112:115],a[148:151],v[176:179],v[112:115]\n"
TR "a[244:245],v5 offset:8192\n"
"v_add_f32 v86,v86,v198\n"
MF "v[112:115],a[188:191],v[180:183],v[112:115]\n"
TR "a[246:247],v5 offset:10240\n"
"v_add_f32 v87,v87,v199\n"
MF "v[112:115],a[228:231],v[184:187],v[112:115]\n"
"ds_read2st64_b32 v[204:205],v13 offset0:36 offset1:37\n"
CV "v80,v80,v81\n"
MF "v[120:123],a[232:235],v[184:187],v[120:123]\n"
"ds_read2st64_b32 v[206:207],v13 offset0:38 offset1:39\n"
CV "v81,v82,v83\n"
MF "v[120:123],a[192:195],v[180:183],v[120:123]\n"
"s_add_u32 s87,s90,13312\n"
CV "v82,v84,v85\n"
CV "v83,v86,v87\n"
MF "v[120:123],a[152:155],v[176:179],v[120:123]\n"
MF "v[128:131],a[156:159],v[176:179],v[128:131]\n"
"buffer_store_dwordx4 v[80:83],v9,s[40:43],s87 offen sc1\n"
MF "v[128:131],a[196:199],v[180:183],v[128:131]\n"
MF "v[128:131],a[236:239],v[184:187],v[128:131]\n"
"s_waitcnt lgkmcnt(2)\n"
MF "v[132:135],a[236:239],a[244:247],v[132:135]\n"
"s_waitcnt lgkmcnt(0)\n"
TR "v[176:177],v6\n"
"v_add_f32 v88,v88,v200\n"
MF "v[132:135],a[196:199],a[240:243],v[132:135]\n"
TR "v[178:179],v6 offset:2048\n"
"v_add_f32 v89,v89,v201\n"
MF "v[132:135],a[156:159],v[188:191],v[132:135]\n"
"v_add_f32 v90,v90,v202\n"
"v_add_f32 v91,v91,v203\n"
MF "v[124:127],a[152:155],v[188:191],v[124:127]\n"
TR "v[180:181],v6 offset:4096\n"
"v_add_f32 v92,v92,v204\n"
MF "v[124:127],a[192:195],a[240:243],v[124:127]\n"
TR "v[182:183],v6 offset:6144\n"
"v_add_f32 v93,v93,v205\n"
MF "v[124:127],a[232:235],a[244:247],v[124:127]\n"
"v_add_f32 v94,v94,v206\n"
"v_add_f32 v95,v95,v207\n"
MF "v[116:119],a[228:231],a[244:247],v[116:119]\n"
TR "v[184:185],v6 offset:8192\n"
CV "v88,v88,v89\n"
MF "v[116:119],a[188:191],a[240:243],v[116:119]\n"
TR "v[186:187],v6 offset:10240\n"
CV "v89,v90,v91\n"
MF "v[116:119],a[148:151],v[188:191],v[116:119]\n"
"s_add_u32 s87,s90,14336\n"
CV "v90,v92,v93\n"
CV "v91,v94,v95\n"
MF "v[108:111],a[144:147],v[188:191],v[108:111]\n"
MF "v[108:111],a[184:187],a[240:243],v[108:111]\n"
MF "v[108:111],a[224:227],a[244:247],v[108:111]\n"
MF "v[100:103],a[220:223],a[244:247],v[100:103]\n"
"buffer_store_dwordx4 v[88:91],v9,s[40:43],s87 offen sc1\n"
MF "v[100:103],a[180:183],a[240:243],v[100:103]\n"
"s_mov_b32 s90,s91\n"
"s_add_u32 s91,s91,20480\n"
MF "v[100:103],a[140:143],v[188:191],v[100:103]\n"
"s_waitcnt lgkmcnt(0)\n"
MF "v[136:139],a[140:143],v[176:179],v[136:139]\n"
TR "v[188:189],v7\n"
MF "v[136:139],a[180:183],v[180:183],v[136:139]\n"
TR "v[190:191],v7 offset:2048\n"
MF "v[136:139],a[220:223],v[184:187],v[136:139]\n"
MF "v[144:147],a[224:227],v[184:187],v[144:147]\n"
TR "a[240:241],v7 offset:4096\n"
MF "v[144:147],a[184:187],v[180:183],v[144:147]\n"
TR "a[242:243],v7 offset:6144\n"
MF "v[144:147],a[144:147],v[176:179],v[144:147]\n"
MF "v[152:155],a[148:151],v[176:179],v[152:155]\n"
TR "a[244:245],v7 offset:8192\n"
MF "v[152:155],a[188:191],v[180:183],v[152:155]\n"
TR "a[246:247],v7 offset:10240\n"
MF "v[152:155],a[228:231],v[184:187],v[152:155]\n"
MF "v[160:163],a[232:235],v[184:187],v[160:163]\n"
MF "v[160:163],a[192:195],v[180:183],v[160:163]\n"
MF "v[160:163],a[152:155],v[176:179],v[160:163]\n"
MF "v[168:171],a[156:159],v[176:179],v[168:171]\n"
MF "v[168:171],a[196:199],v[180:183],v[168:171]\n"
"s_waitcnt lgkmcnt(0)\n"
MF "v[168:171],a[236:239],v[184:187],v[168:171]\n"
"s_barrier\n"
MF "v[172:175],a[236:239],a[244:247],v[172:175]\n"
TR "v[176:177],v0 offset:24576\n"
MF "v[172:175],a[196:199],a[240:243],v[172:175]\n"
"s_add_u32 s85,s85,1\n"
TR "v[178:179],v0 offset:26624\n"
MF "v[172:175],a[156:159],v[188:191],v[172:175]\n"
MF "v[164:167],a[152:155],v[188:191],v[164:167]\n"
TR "v[180:181],v0 offset:28672\n"
MF "v[164:167],a[192:195],a[240:243],v[164:167]\n"
TR "v[182:183],v0 offset:30720\n"
MF "v[164:167],a[232:235],a[244:247],v[164:167]\n"
MF "v[156:159],a[228:231],a[244:247],v[156:159]\n"
TR "v[184:185],v0 offset:32768\n"
MF "v[156:159],a[188:191],a[240:243],v[156:159]\n"
TR "v[186:187],v0 offset:34816\n"
MF "v[156:159],a[148:151],v[188:191],v[156:159]\n"
MF "v[148:151],a[144:147],v[188:191],v[148:151]\n"
MF "v[148:151],a[184:187],a[240:243],v[148:151]\n"
MF "v[148:151],a[224:227],a[244:247],v[148:151]\n"
MF "v[140:143],a[220:223],a[244:247],v[140:143]\n"
MF "v[140:143],a[180:183],a[240:243],v[140:143]\n"
MF "v[140:143],a[140:143],v[188:191],v[140:143]\n"
"s_waitcnt lgkmcnt(0)\n"
MF "v[16:19],a[0:3],v[176:179],v[252:255]\n"
TR "v[188:189],v1 offset:24576\n"
MF "v[16:19],a[40:43],v[180:183],v[16:19]\n"
TR "v[190:191],v1 offset:26624\n"
MF "v[16:19],a[80:83],v[184:187],v[16:19]\n"
MF "v[24:27],a[84:87],v[184:187],v[252:255]\n"
TR "a[240:241],v1 offset:28672\n"
MF "v[24:27],a[44:47],v[180:183],v[24:27]\n"
TR "a[242:243],v1 offset:30720\n"
MF "v[24:27],a[4:7],v[176:179],v[24:27]\n"
MF "v[32:35],a[8:11],v[176:179],v[252:255]\n"
TR "a[244:245],v1 offset:32768\n"
MF "v[32:35],a[48:51],v[180:183],v[32:35]\n"
TR "a[246:247],v1 offset:34816\n"
MF "v[32:35],a[88:91],v[184:187],v[32:35]\n"
MF "v[40:43],a[92:95],v[184:187],v[252:255]\n"
MF "v[40:43],a[52:55],v[180:183],v[40:43]\n"
MF "v[40:43],a[12:15],v[176:179],v[40:43]\n"
MF "v[48:51],a[16:19],v[176:179],v[252:255]\n"
MF "v[48:51],a[56:59],v[180:183],v[48:51]\n"
MF "v[48:51],a[96:99],v[184:187],v[48:51]\n"
"s_waitcnt lgkmcnt(0)\n"
MF "v[52:55],a[96:99],a[244:247],v[252:255]\n"
TR "v[176:177],v2 offset:24576\n"
MF "v[52:55],a[56:59],a[240:243],v[52:55]\n"
TR "v[178:179],v2 offset:26624\n"
MF "v[52:55],a[16:19],v[188:191],v[52:55]\n"
MF "v[44:47],a[12:15],v[188:191],v[252:255]\n"
TR "v[180:181],v2 offset:28672\n"
MF "v[44:47],a[52:55],a[240:243],v[44:47]\n"
TR "v[182:183],v2 offset:30720\n"
MF "v[44:47],a[92:95],a[244:247],v[44:47]\n"
MF "v[36:39],a[88:91],a[244:247],v[252:255]\n"
TR "v[184:185],v2 offset:32768\n"
MF "v[36:39],a[48:51],a[240:243],v[36:39]\n"
TR "v[186:187],v2 offset:34816\n"
MF "v[36:39],a[8:11],v[188:191],v[36:39]\n"
MF "v[28:31],a[4:7],v[188:191],v[252:255]\n"
MF "v[28:31],a[44:47],a[240:243],v[28:31]\n"
MF "v[28:31],a[84:87],a[244:247],v[28:31]\n"
MF "v[20:23],a[80:83],a[244:247],v[252:255]\n"
MF "v[20:23],a[40:43],a[240:243],v[20:23]\n"
MF "v[20:23],a[0:3],v[188:191],v[20:23]\n"
"s_waitcnt lgkmcnt(0)\n"
MF "v[56:59],a[0:3],v[176:179],v[252:255]\n"
TR "v[188:189],v3 offset:24576\n"
MF "v[56:59],a[40:43],v[180:183],v[56:59]\n"
TR "v[190:191],v3 offset:26624\n"
MF "v[56:59],a[80:83],v[184:187],v[56:59]\n"
MF "v[64:67],a[84:87],v[184:187],v[252:255]\n"
TR "a[240:241],v3 offset:28672\n"
MF "v[64:67],a[44:47],v[180:183],v[64:67]\n"
TR "a[242:243],v3 offset:30720\n"
MF "v[64:67],a[4:7],v[176:179],v[64:67]\n"
MF "v[72:75],a[8:11],v[176:179],v[252:255]\n"
TR "a[244:245],v3 offset:32768\n"
MF "v[72:75],a[48:51],v[180:183],v[72:75]\n"
TR "a[246:247],v3 offset:34816\n"
MF "v[72:75],a[88:91],v[184:187],v[72:75]\n"
MF "v[80:83],a[92:95],v[184:187],v[252:255]\n"
MF "v[80:83],a[52:55],v[180:183],v[80:83]\n"
MF "v[80:83],a[12:15],v[176:179],v[80:83]\n"
MF "v[88:91],a[16:19],v[176:179],v[252:255]\n"
MF "v[88:91],a[56:59],v[180:183],v[88:91]\n"
"s_waitcnt lgkmcnt(0)\n"
MF "v[88:91],a[96:99],v[184:187],v[88:91]\n"
"s_barrier\n"
MF "v[92:95],a[96:99],a[244:247],v[252:255]\n"
TR "v[176:177],v0\n"
MF "v[92:95],a[56:59],a[240:243],v[92:95]\n"
TR "v[178:179],v0 offset:2048\n"
MF "v[92:95],a[16:19],v[188:191],v[92:95]\n"
"ds_read2st64_b32 v[192:193],v13 offset0:192 offset1:193\n"
MF "v[84:87],a[12:15],v[188:191],v[252:255]\n"
TR "v[180:181],v0 offset:4096\n"
MF "v[84:87],a[52:55],a[240:243],v[84:87]\n"
TR "v[182:183],v0 offset:6144\n"
MF "v[84:87],a[92:95],a[244:247],v[84:87]\n"
"ds_read2st64_b32 v[194:195],v13 offset0:194 offset1:195\n"
MF "v[76:79],a[88:91],a[244:247],v[252:255]\n"
TR "v[184:185],v0 offset:8192\n"
MF "v[76:79],a[48:51],a[240:243],v[76:79]\n"
TR "v[186:187],v0 offset:10240\n"
MF "v[76:79],a[8:11],v[188:191],v[76:79]\n"
"ds_read2st64_b32 v[196:197],v13 offset0:196 offset1:197\n"
MF "v[68:71],a[4:7],v[188:191],v[252:255]\n"
"ds_read2st64_b32 v[198:199],v13 offset0:198 offset1:199\n"
MF "v[68:71],a[44:47],a[240:243],v[68:71]\n"
MF "v[68:71],a[84:87],a[244:247],v[68:71]\n"
MF "v[60:63],a[80:83],a[244:247],v[252:255]\n"
MF "v[60:63],a[40:43],a[240:243],v[60:63]\n"
MF "v[60:63],a[0:3],v[188:191],v[60:63]\n"
"s_waitcnt lgkmcnt(2)\n"
MF "v[16:19],a[120:123],v[176:179],v[16:19]\n"
"s_waitcnt lgkmcnt(0)\n"
TR "v[188:189],v1\n"
"v_add_f32 v96,v96,v192\n"
MF "v[16:19],a[160:163],v[180:183],v[16:19]\n"
TR "v[190:191],v1 offset:2048\n"
"v_add_f32 v97,v97,v193\n"
MF "v[16:19],a[200:203],v[184:187],v[16:19]\n"
"ds_read2st64_b32 v[200:201],v13 offset0:200 offset1:201\n"
"v_add_f32 v98,v98,v194\n"
"s_waitcnt vmcnt(0)\n"
"v_mov_b32 v14,s76\n"
"s_add_u32 s76,s76,1\n"
MF "v[24:27],a[204:207],v[184:187],v[24:27]\n"
TR "a[240:241],v1 offset:4096\n"
"v_add_f32 v99,v99,v195\n"
"buffer_store_dword v14,v15,s[40:43],s94 offen sc1\n"
MF "v[24:27],a[164:167],v[180:183],v[24:27]\n"
TR "a[242:243],v1 offset:6144\n"
"v_add_f32 v100,v100,v196\n"
MF "v[24:27],a[124:127],v[176:179],v[24:27]\n"
"ds_read2st64_b32 v[202:203],v13 offset0:202 offset1:203\n"
"v_add_f32 v101,v101,v197\n"
MF "v[32:35],a[128:131],v[176:179],v[32:35]\n"
TR "a[244:245],v1 offset:8192\n"
"v_add_f32 v102,v102,v198\n"
MF "v[32:35],a[168:171],v[180:183],v[32:35]\n"
TR "a[246:247],v1 offset:10240\n"
"v_add_f32 v103,v103,v199\n"
MF "v[32:35],a[208:211],v[184:187],v[32:35]\n"
"ds_read2st64_b32 v[204:205],v13 offset0:204 offset1:205\n"
CV "v96,v96,v97\n"
MF "v[40:43],a[212:215],v[184:187],v[40:43]\n"
"ds_read2st64_b32 v[206:207],v13 offset0:206 offset1:207\n"
CV "v97,v98,v99\n"
MF "v[40:43],a[172:175],v[180:183],v[40:43]\n"
"s_add_u32 s87,s90,0\n"
CV "v98,v100,v101\n"
CV "v99,v102,v103\n"
MF "v[40:43],a[132:135],v[176:179],v[40:43]\n"
MF "v[48:51],a[136:139],v[176:179],v[48:51]\n"
"buffer_store_dwordx4 v[96:99],v9,s[40:43],s87 offen sc1\n"
MF "v[48:51],a[176:179],v[180:183],v[48:51]\n"
MF "v[48:51],a[216:219],v[184:187],v[48:51]\n"
"s_waitcnt lgkmcnt(2)\n"
MF "v[52:55],a[216:219],a[244:247],v[52:55]\n"
"s_waitcnt lgkmcnt(0)\n"
TR "v[176:177],v2\n"
"v_add_f32 v104,v104,v200\n"
MF "v[52:55],a[176:179],a[240:243],v[52:55]\n"
TR "v[178:179],v2 offset:2048\n"
"v_add_f32 v105,v105,v201\n"
MF "v[52:55],a[136:139],v[188:191],v[52:55]\n"
"ds_read2st64_b32 v[192:193],v13 offset0:208 offset1:209\n"
"v_add_f32 v106,v106,v202\n"
MF "v[44:47],a[132:135],v[188:191],v[44:47]\n"
TR "v[180:181],v2 offset:4096\n"
"v_add_f32 v107,v107,v203\n"
MF "v[44:47],a[172:175],a[240:243],v[44:47]\n"
TR "v[182:183],v2 offset:6144\n"
"v_add_f32 v108,v108,v204\n"
MF "v[44:47],a[212:215],a[244:247],v[44:47]\n"
"ds_read2st64_b32 v[194:195],v13 offset0:210 offset1:211\n"
"v_add_f32 v109,v109,v205\n"
MF "v[36:39],a[208:211],a[244:247],v[36:39]\n"
TR "v[184:185],v2 offset:8192\n"
"v_add_f32 v110,v110,v206\n"
MF "v[36:39],a[168:171],a[240:243],v[36:39]\n"
TR "v[186:187],v2 offset:10240\n"
"v_add_f32 v111,v111,v207\n"
MF "v[36:39],a[128:131],v[188:191],v[36:39]\n"
"ds_read2st64_b32 v[196:197],v13 offset0:212 offset1:213\n"
CV "v104,v104,v105\n"
MF "v[28:31],a[124:127],v[188:191],v[28:31]\n"
"ds_read2st64_b32 v[198:199],v13 offset0:214 offset1:215\n"
CV "v105,v106,v107\n"
MF "v[28:31],a[164:167],a[240:243],v[28:31]\n"
"s_add_u32 s87,s90,1024\n"
CV "v106,v108,v109\n"
CV "v107,v110,v111\n"
MF "v[28:31],a[204:207],a[244:247],v[28:31]\n"
MF "v[20:23],a[200:203],a[244:247],v[20:23]\n"
"buffer_store_dwordx4 v[104:107],v9,s[40:43],s87 offen sc1\n"
MF "v[20:23],a[160:163],a[240:243],v[20:23]\n"
MF "v[20:23],a[120:123],v[188:191],v[20:23]\n"
"s_waitcnt lgkmcnt(2)\n"
MF "v[56:59],a[120:123],v[176:179],v[56:59]\n"
"s_waitcnt lgkmcnt(0)\n"
TR "v[188:189],v3\n"
"v_add_f32 v112,v112,v192\n"
MF "v[56:59],a[160:163],v[180:183],v[56:59]\n"
TR "v[190:191],v3 offset:2048\n"
"v_add_f32 v113,v113,v193\n"
MF "v[56:59],a[200:203],v[184:187],v[56:59]\n"
".Lpg34h:\n"
"s_cbranch_execz .Lpg38h\n"
"ds_read2st64_b32 v[200:201],v13 offset0:216 offset1:217\n"
"v_add_f32 v114,v114,v194\n"
MF "v[64:67],a[204:207],v[184:187],v[64:67]\n"
TR "a[240:241],v3 offset:4096\n"
"v_add_f32 v115,v115,v195\n"
MF "v[64:67],a[164:167],v[180:183],v[64:67]\n"
TR "a[242:243],v3 offset:6144\n"
"v_add_f32 v116,v116,v196\n"
MF "v[64:67],a[124:127],v[176:179],v[64:67]\n"
"ds_read2st64_b32 v[202:203],v13 offset0:218 offset1:219\n"
"v_add_f32 v117,v117,v197\n"
MF "v[72:75],a[128:131],v[176:179],v[72:75]\n"
TR "a[244:245],v3 offset:8192\n"
"v_add_f32 v118,v118,v198\n"
MF "v[72:75],a[168:171],v[180:183],v[72:75]\n"
TR "a[246:247],v3 offset:10240\n"
"v_add_f32 v119,v119,v199\n"
MF "v[72:75],a[208:211],v[184:187],v[72:75]\n"
"ds_read2st64_b32 v[204:205],v13 offset0:220 offset1:221\n"
CV "v112,v112,v113\n"
MF "v[80:83],a[212:215],v[184:187],v[80:83]\n"
"ds_read2st64_b32 v[206:207],v13 offset0:222 offset1:223\n"
CV "v113,v114,v115\n"
MF "v[80:83],a[172:175],v[180:183],v[80:83]\n"
"s_add_u32 s87,s90,2048\n"
CV "v114,v116,v117\n"
CV "v115,v118,v119\n"
MF "v[80:83],a[132:135],v[176:179],v[80:83]\n"
MF "v[88:91],a[136:139],v[176:179],v[88:91]\n"
"buffer_store_dwordx4 v[112:115],v9,s[40:43],s87 offen sc1\n"
MF "v[88:91],a[176:179],v[180:183],v[88:91]\n"
"s_waitcnt lgkmcnt(0)\n"
MF "v[88:91],a[216:219],v[184:187],v[88:91]\n"
"s_barrier\n"
MF "v[92:95],a[216:219],a[244:247],v[92:95]\n"
TR "v[176:177],v0 offset:49152\n"
"v_add_f32 v120,v120,v200\n"
MF "v[92:95],a[176:179],a[240:243],v[92:95]\n"
TR "v[178:179],v0 offset:51200\n"
"v_add_f32 v121,v121,v201\n"
MF "v[92:95],a[136:139],v[188:191],v[92:95]\n"
"ds_read2st64_b32 v[192:193],v13 offset0:224 offset1:225\n"
"v_add_f32 v122,v122,v202\n"
MF "v[84:87],a[132:135],v[188:191],v[84:87]\n"
TR "v[180:181],v0 offset:53248\n"
"v_add_f32 v123,v123,v203\n"
MF "v[84:87],a[172:175],a[240:243],v[84:87]\n"
TR "v[182:183],v0 offset:55296\n"
"v_add_f32 v124,v124,v204\n"
MF "v[84:87],a[212:215],a[244:247],v[84:87]\n"
"ds_read2st64_b32 v[194:195],v13 offset0:226 offset1:227\n"
"v_add_f32 v125,v125,v205\n"
MF "v[76:79],a[208:211],a[244:247],v[76:79]\n"
TR "v[184:185],v0 offset:57344\n"
"v_add_f32 v126,v126,v206\n"
MF "v[76:79],a[168:171],a[240:243],v[76:79]\n"
TR "v[186:187],v0 offset:59392\n"
"v_add_f32 v127,v127,v207\n"
MF "v[76:79],a[128:131],v[188:191],v[76:79]\n"
"ds_read2st64_b32 v[196:197],v13 offset0:228 offset1:229\n"
CV "v120,v120,v121\n"
MF "v[68:71],a[124:127],v[188:191],v[68:71]\n"
"ds_read2st64_b32 v[198:199],v13 offset0:230 offset1:231\n"
CV "v121,v122,v123\n"
MF "v[68:71],a[164:167],a[240:243],v[68:71]\n"
"s_add_u32 s87,s90,3072\n"
CV "v122,v124,v125\n"
CV "v123,v126,v127\n"
MF "v[68:71],a[204:207],a[244:247],v[68:71]\n"
MF "v[60:63],a[200:203],a[244:247],v[60:63]\n"
"buffer_store_dwordx4 v[120:123],v9,s[40:43],s87 offen sc1\n"
MF "v[60:63],a[160:163],a[240:243],v[60:63]\n"
MF "v[60:63],a[120:123],v[188:191],v[60:63]\n"
"s_waitcnt lgkmcnt(2)\n"
MF "v[16:19],a[20:23],v[176:179],v[16:19]\n"
"s_waitcnt lgkmcnt(0)\n"
TR "v[188:189],v1 offset:49152\n"
"v_add_f32 v128,v128,v192\n"
MF "v[16:19],a[60:63],v[180:183],v[16:19]\n"
TR "v[190:191],v1 offset:51200\n"
"v_add_f32 v129,v129,v193\n"
MF "v[16:19],a[100:103],v[184:187],v[16:19]\n"
"ds_read2st64_b32 v[200:201],v13 offset0:0 offset1:1\n"
"v_add_f32 v130,v130,v194\n"
MF "v[24:27],a[104:107],v[184:187],v[24:27]\n"
TR "a[240:241],v1 offset:53248\n"
"v_add_f32 v131,v131,v195\n"
MF "v[24:27],a[64:67],v[180:183],v[24:27]\n"
TR "a[242:243],v1 offset:55296\n"
"v_add_f32 v132,v132,v196\n"
MF "v[24:27],a[24:27],v[176:179],v[24:27]\n"
"ds_read2st64_b32 v[202:203],v13 offset0:2 offset1:3\n"
"v_add_f32 v133,v133,v197\n"
MF "v[32:35],a[28:31],v[176:179],v[32:35]\n"
TR "a[244:245],v1 offset:57344\n"
"v_add_f32 v134,v134,v198\n"
MF "v[32:35],a[68:71],v[180:183],v[32:35]\n"
TR "a[246:247],v1 offset:59392\n"
"v_add_f32 v135,v135,v199\n"
MF "v[32:35],a[108:111],v[184:187],v[32:35]\n"
"ds_read2st64_b32 v[204:205],v13 offset0:4 offset1:5\n"
CV "v128,v128,v129\n"
MF "v[40:43],a[112:115],v[184:187],v[40:43]\n"
"ds_read2st64_b32 v[206:207],v13 offset0:6 offset1:7\n"
CV "v129,v130,v131\n"
MF "v[40:43],a[72:75],v[180:183],v[40:43]\n"
"s_add_u32 s87,s90,4096\n"
CV "v130,v132,v133\n"
CV "v131,v134,v135\n"
MF "v[40:43],a[32:35],v[176:179],v[40:43]\n"
MF "v[48:51],a[36:39],v[176:179],v[48:51]\n"
"buffer_store_dwordx4 v[128:131],v9,s[40:43],s87 offen sc1\n"
MF "v[48:51],a[76:79],v[180:183],v[48:51]\n"
MF "v[48:51],a[116:119],v[184:187],v[48:51]\n"
"s_waitcnt lgkmcnt(2)\n"
MF "v[52:55],a[116:119],a[244:247],v[52:55]\n"
"s_waitcnt lgkmcnt(0)\n"
TR "v[176:177],v2 offset:49152\n"
"v_add_f32 v136,v136,v200\n"
MF "v[52:55],a[76:79],a[240:243],v[52:55]\n"
TR "v[178:179],v2 offset:51200\n"
"v_add_f32 v137,v137,v201\n"
MF "v[52:55],a[36:39],v[188:191],v[52:55]\n"
"ds_read2st64_b32 v[192:193],v13 offset0:8 offset1:9\n"
"v_add_f32 v138,v138,v202\n"
MF "v[44:47],a[32:35],v[188:191],v[44:47]\n"
TR "v[180:181],v2 offset:53248\n"
"v_add_f32 v139,v139,v203\n"
MF "v[44:47],a[72:75],a[240:243],v[44:47]\n"
TR "v[182:183],v2 offset:55296\n"
"v_add_f32 v140,v140,v204\n"
MF "v[44:47],a[112:115],a[244:247],v[44:47]\n"
"ds_read2st64_b32 v[194:195],v13 offset0:10 offset1:11\n"
"v_add_f32 v141,v141,v205\n"
MF "v[36:39],a[108:111],a[244:247],v[36:39]\n"
TR "v[184:185],v2 offset:57344\n"
"v_add_f32 v142,v142,v206\n"
MF "v[36:39],a[68:71],a[240:243],v[36:39]\n"
TR "v[186:187],v2 offset:59392\n"
"v_add_f32 v143,v143,v207\n"
MF "v[36:39],a[28:31],v[188:191],v[36:39]\n"
"ds_read2st64_b32 v[196:197],v13 offset0:12 offset1:13\n"
CV "v136,v136,v137\n"
MF "v[28:31],a[24:27],v[188:191],v[28:31]\n"
"ds_read2st64_b32 v[198:199],v13 offset0:14 offset1:15\n"
CV "v137,v138,v139\n"
MF "v[28:31],a[64:67],a[240:243],v[28:31]\n"
"s_add_u32 s87,s90,10240\n"
CV "v138,v140,v141\n"
CV "v139,v142,v143\n"
MF "v[28:31],a[104:107],a[244:247],v[28:31]\n"
MF "v[20:23],a[100:103],a[244:247],v[20:23]\n"
"buffer_store_dwordx4 v[136:139],v9,s[40:43],s87 offen sc1\n"
MF "v[20:23],a[60:63],a[240:243],v[20:23]\n"
MF "v[20:23],a[20:23],v[188:191],v[20:23]\n"
"s_waitcnt lgkmcnt(2)\n"
MF "v[56:59],a[20:23],v[176:179],v[56:59]\n"
"s_waitcnt lgkmcnt(0)\n"
TR "v[188:189],v3 offset:49152\n"
"v_add_f32 v144,v144,v192\n"
MF "v[56:59],a[60:63],v[180:183],v[56:59]\n"
TR "v[190:191],v3 offset:51200\n"
"v_add_f32 v145,v145,v193\n"
MF "v[56:59],a[100:103],v[184:187],v[56:59]\n"
"ds_read2st64_b32 v[200:201],v13 offset0:16 offset1:17\n"
"v_add_f32 v146,v146,v194\n"
MF "v[64:67],a[104:107],v[184:187],v[64:67]\n"
TR "a[240:241],v3 offset:53248\n"
"v_add_f32 v147,v147,v195\n"
MF "v[64:67],a[64:67],v[180:183],v[64:67]\n"
TR "a[242:243],v3 offset:55296\n"
"v_add_f32 v148,v148,v196\n"
MF "v[64:67],a[24:27],v[176:179],v[64:67]\n"
"ds_read2st64_b32 v[202:203],v13 offset0:18 offset1:19\n"
"v_add_f32 v149,v149,v197\n"
MF "v[72:75],a[28:31],v[176:179],v[72:75]\n"
TR "a[244:245],v3 offset:57344\n"
"v_add_f32 v150,v150,v198\n"
MF "v[72:75],a[68:71],v[180:183],v[72:75]\n"
TR "a[246:247],v3 offset:59392\n"
"v_add_f32 v151,v151,v199\n"
MF "v[72:75],a[108:111],v[184:187],v[72:75]\n"
"ds_read2st64_b32 v[204:205],v13 offset0:20 offset1:21\n"
CV "v144,v144,v145\n"
MF "v[80:83],a[112:115],v[184:187],v[80:83]\n"
"ds_read2st64_b32 v[206:207],v13 offset0:22 offset1:23\n"
CV "v145,v146,v147\n"
MF "v[80:83],a[72:75],v[180:183],v[80:83]\n"
"s_add_u32 s87,s90,11264\n"
CV "v146,v148,v149\n"
CV "v147,v150,v151\n"
MF "v[80:83],a[32:35],v[176:179],v[80:83]\n"
MF "v[88:91],a[36:39],v[176:179],v[88:91]\n"
"buffer_store_dwordx4 v[144:147],v9,s[40:43],s87 offen sc1\n"
MF "v[88:91],a[76:79],v[180:183],v[88:91]\n"
"s_waitcnt lgkmcnt(0)\n"
MF "v[88:91],a[116:119],v[184:187],v[88:91]\n"
"s_barrier\n"
MF "v[92:95],a[116:119],a[244:247],v[92:95]\n"
TR "v[176:177],v4\n"
"v_add_f32 v152,v152,v200\n"
MF "v[92:95],a[76:79],a[240:243],v[92:95]\n"
TR "v[178:179],v4 offset:2048\n"
"v_add_f32 v153,v153,v201\n"
MF "v[92:95],a[36:39],v[188:191],v[92:95]\n"
"ds_read2st64_b32 v[192:193],v13 offset0:24 offset1:25\n"
"v_add_f32 v154,v154,v202\n"
MF "v[84:87],a[32:35],v[188:191],v[84:87]\n"
TR "v[180:181],v4 offset:4096\n"
"v_add_f32 v155,v155,v203\n"
MF "v[84:87],a[72:75],a[240:243],v[84:87]\n"
TR "v[182:183],v4 offset:6144\n"
"v_add_f32 v156,v156,v204\n"
MF "v[84:87],a[112:115],a[244:247],v[84:87]\n"
"ds_read2st64_b32 v[194:195],v13 offset0:26 offset1:27\n"
"v_add_f32 v157,v157,v205\n"
MF "v[76:79],a[108:111],a[244:247],v[76:79]\n"
TR "v[184:185],v4 offset:8192\n"
"v_add_f32 v158,v158,v206\n"
MF "v[76:79],a[68:71],a[240:243],v[76:79]\n"
TR "v[186:187],v4 offset:10240\n"
"v_add_f32 v159,v159,v207\n"
MF "v[76:79],a[28:31],v[188:191],v[76:79]\n"
"ds_read2st64_b32 v[196:197],v13 offset0:28 offset1:29\n"
CV "v152,v152,v153\n"
MF "v[68:71],a[24:27],v[188:191],v[68:71]\n"
"ds_read2st64_b32 v[198:199],v13 offset0:30 offset1:31\n"
CV "v153,v154,v155\n"
MF "v[68:71],a[64:67],a[240:243],v[68:71]\n"
"s_add_u32 s87,s90,12288\n"
CV "v154,v156,v157\n"
CV "v155,v158,v159\n"
MF "v[68:71],a[104:107],a[244:247],v[68:71]\n"
MF "v[60:63],a[100:103],a[244:247],v[60:63]\n"
"buffer_store_dwordx4 v[152:155],v9,s[40:43],s87 offen sc1\n"
MF "v[60:63],a[60:63],a[240:243],v[60:63]\n"
MF "v[60:63],a[20:23],v[188:191],v[60:63]\n"
"s_waitcnt lgkmcnt(2)\n"
MF "v[16:19],a[140:143],v[176:179],v[16:19]\n"
"s_waitcnt lgkmcnt(0)\n"
TR "v[188:189],v5\n"
"v_add_f32 v160,v160,v192\n"
MF "v[16:19],a[180:183],v[180:183],v[16:19]\n"
TR "v[190:191],v5 offset:2048\n"
"v_add_f32 v161,v161,v193\n"
MF "v[16:19],a[220:223],v[184:187],v[16:19]\n"
"ds_read2st64_b32 v[200:201],v13 offset0:32 offset1:33\n"
"v_add_f32 v162,v162,v194\n"
MF "v[24:27],a[224:227],v[184:187],v[24:27]\n"
TR "a[240:241],v5 offset:4096\n"
"v_add_f32 v163,v163,v195\n"
MF "v[24:27],a[184:187],v[180:183],v[24:27]\n"
TR "a[242:243],v5 offset:6144\n"
"v_add_f32 v164,v164,v196\n"
MF "v[24:27],a[144:147],v[176:179],v[24:27]\n"
"ds_read2st64_b32 v[202:203],v13 offset0:34 offset1:35\n"
"v_add_f32 v165,v165,v197\n"
MF "v[32:35],a[148:151],v[176:179],v[32:35]\n"
TR "a[244:245],v5 offset:8192\n"
"v_add_f32 v166,v166,v198\n"
MF "v[32:35],a[188:191],v[180:183],v[32:35]\n"
TR "a[246:247],v5 offset:10240\n"
"v_add_f32 v167,v167,v199\n"
MF "v[32:35],a[228:231],v[184:187],v[32:35]\n"
"ds_read2st64_b32 v[204:205],v13 offset0:36 offset1:37\n"
CV "v160,v160,v161\n"
MF "v[40:43],a[232:235],v[184:187],v[40:43]\n"
"ds_read2st64_b32 v[206:207],v13 offset0:38 offset1:39\n"
CV "v161,v162,v163\n"
MF "v[40:43],a[192:195],v[180:183],v[40:43]\n"
"s_add_u32 s87,s90,13312\n"
CV "v162,v164,v165\n"
CV "v163,v166,v167\n"
MF "v[40:43],a[152:155],v[176:179],v[40:43]\n"
MF "v[48:51],a[156:159],v[176:179],v[48:51]\n"
"buffer_store_dwordx4 v[160:163],v9,s[40:43],s87 offen sc1\n"
MF "v[48:51],a[196:199],v[180:183],v[48:51]\n"
MF "v[48:51],a[236:239],v[184:187],v[48:51]\n"
"s_waitcnt lgkmcnt(2)\n"
MF "v[52:55],a[236:239],a[244:247],v[52:55]\n"
"s_waitcnt lgkmcnt(0)\n"
TR "v[176:177],v6\n"
"v_add_f32 v168,v168,v200\n"
MF "v[52:55],a[196:199],a[240:243],v[52:55]\n"
TR "v[178:179],v6 offset:2048\n"
"v_add_f32 v169,v169,v201\n"
MF "v[52:55],a[156:159],v[188:191],v[52:55]\n"
"v_add_f32 v170,v170,v202\n"
"v_add_f32 v171,v171,v203\n"
MF "v[44:47],a[152:155],v[188:191],v[44:47]\n"
TR "v[180:181],v6 offset:4096\n"
"v_add_f32 v172,v172,v204\n"
MF "v[44:47],a[192:195],a[240:243],v[44:47]\n"
TR "v[182:183],v6 offset:6144\n"
"v_add_f32 v173,v173,v205\n"
MF "v[44:47],a[232:235],a[244:247],v[44:47]\n"
"v_add_f32 v174,v174,v206\n"
"v_add_f32 v175,v175,v207\n"
MF "v[36:39],a[228:231],a[244:247],v[36:39]\n"
TR "v[184:185],v6 offset:8192\n"
CV "v168,v168,v169\n"
MF "v[36:39],a[188:191],a[240:243],v[36:39]\n"
TR "v[186:187],v6 offset:10240\n"
CV "v169,v170,v171\n"
MF "v[36:39],a[148:151],v[188:191],v[36:39]\n"
"s_add_u32 s87,s90,14336\n"
CV "v170,v172,v173\n"
CV "v171,v174,v175\n"
MF "v[28:31],a[144:147],v[188:191],v[28:31]\n"
MF "v[28:31],a[184:187],a[240:243],v[28:31]\n"
MF "v[28:31],a[224:227],a[244:247],v[28:31]\n"
MF "v[20:23],a[220:223],a[244:247],v[20:23]\n"
"buffer_store_dwordx4 v[168:171],v9,s[40:43],s87 offen sc1\n"
MF "v[20:23],a[180:183],a[240:243],v[20:23]\n"
"s_mov_b32 s90,s91\n"
"s_add_u32 s91,s91,20480\n"
MF "v[20:23],a[140:143],v[188:191],v[20:23]\n"
"s_waitcnt lgkmcnt(0)\n"
MF "v[56:59],a[140:143],v[176:179],v[56:59]\n"
TR "v[188:189],v7\n"
MF "v[56:59],a[180:183],v[180:183],v[56:59]\n"
TR "v[190:191],v7 offset:2048\n"
MF "v[56:59],a[220:223],v[184:187],v[56:59]\n"
"v_mov_b32 v244,0\n"
"v_mov_b32 v245,0\n"
MF "v[64:67],a[224:227],v[184:187],v[64:67]\n"
TR "a[240:241],v7 offset:4096\n"
"v_mov_b32 v246,0\n"
MF "v[64:67],a[184:187],v[180:183],v[64:67]\n"
TR "a[242:243],v7 offset:6144\n"
"v_mov_b32 v247,0\n"
MF "v[64:67],a[144:147],v[176:179],v[64:67]\n"
"v_mov_b32 v248,0\n"
"v_mov_b32 v249,0\n"
MF "v[72:75],a[148:151],v[176:179],v[72:75]\n"
TR "a[244:245],v7 offset:8192\n"
"v_mov_b32 v250,0\n"
MF "v[72:75],a[188:191],v[180:183],v[72:75]\n"
TR "a[246:247],v7 offset:10240\n"
"v_mov_b32 v251,0\n"
MF "v[72:75],a[228:231],v[184:187],v[72:75]\n"
MF "v[80:83],a[232:235],v[184:187],v[80:83]\n"
MF "v[80:83],a[192:195],v[180:183],v[80:83]\n"
MF "v[80:83],a[152:155],v[176:179],v[80:83]\n"
MF "v[88:91],a[156:159],v[176:179],v[88:91]\n"
MF "v[88:91],a[196:199],v[180:183],v[88:91]\n"
"s_waitcnt lgkmcnt(0)\n"
MF "v[88:91],a[236:239],v[184:187],v[88:91]\n"
"s_barrier\n"
MF "v[92:95],a[236:239],a[244:247],v[92:95]\n"
TR "v[176:177],v0 offset:24576\n"
MF "v[92:95],a[196:199],a[240:243],v[92:95]\n"
"s_add_u32 s85,s85,1\n"
TR "v[178:179],v0 offset:26624\n"
MF "v[92:95],a[156:159],v[188:191],v[92:95]\n"
MF "v[84:87],a[152:155],v[188:191],v[84:87]\n"
TR "v[180:181],v0 offset:28672\n"
MF "v[84:87],a[192:195],a[240:243],v[84:87]\n"
TR "v[182:183],v0 offset:30720\n"
MF "v[84:87],a[232:235],a[244:247],v[84:87]\n"
MF "v[76:79],a[228:231],a[244:247],v[76:79]\n"
TR "v[184:185],v0 offset:32768\n"
MF "v[76:79],a[188:191],a[240:243],v[76:79]\n"
TR "v[186:187],v0 offset:34816\n"
MF "v[76:79],a[148:151],v[188:191],v[76:79]\n"
MF "v[68:71],a[144:147],v[188:191],v[68:71]\n"
MF "v[68:71],a[184:187],a[240:243],v[68:71]\n"
MF "v[68:71],a[224:227],a[244:247],v[68:71]\n"
MF "v[60:63],a[220:223],a[244:247],v[60:63]\n"
MF "v[60:63],a[180:183],a[240:243],v[60:63]\n"
MF "v[60:63],a[140:143],v[188:191],v[60:63]\n"
"s_waitcnt lgkmcnt(0)\n"
"s_cmp_lg_u32 s85,5\n"
"s_cbranch_scc1 .Lpl15\n"
".Lpl19:\n"
MF "v[96:99],a[0:3],v[176:179],v[252:255]\n"
TR "v[188:189],v1 offset:24576\n"
MF "v[96:99],a[40:43],v[180:183],v[96:99]\n"
"s_add_u32 s82,s82,4096\n"
TR "v[190:191],v1 offset:26624\n"
MF "v[96:99],a[80:83],v[184:187],v[96:99]\n"
LD "v[224:227],v9,s[40:43],s82 offen sc1\n"
MF "v[104:107],a[84:87],v[184:187],v[252:255]\n"
TR "a[240:241],v1 offset:28672\n"
MF "v[104:107],a[44:47],v[180:183],v[104:107]\n"
TR "a[242:243],v1 offset:30720\n"
MF "v[104:107],a[4:7],v[176:179],v[104:107]\n"
MF "v[112:115],a[8:11],v[176:179],v[252:255]\n"
TR "a[244:245],v1 offset:32768\n"
MF "v[112:115],a[48:51],v[180:183],v[112:115]\n"
TR "a[246:247],v1 offset:34816\n"
MF "v[112:115],a[88:91],v[184:187],v[112:115]\n"
MF "v[120:123],a[92:95],v[184:187],v[252:255]\n"
MF "v[120:123],a[52:55],v[180:183],v[120:123]\n"
MF "v[120:123],a[12:15],v[176:179],v[120:123]\n"
MF "v[128:131],a[16:19],v[176:179],v[252:255]\n"
MF "v[128:131],a[56:59],v[180:183],v[128:131]\n"
MF "v[128:131],a[96:99],v[184:187],v[128:131]\n"
"s_waitcnt lgkmcnt(0)\n"
MF "v[132:135],a[96:99],a[244:247],v[252:255]\n"
TR "v[176:177],v2 offset:24576\n"
MF "v[132:135],a[56:59],a[240:243],v[132:135]\n"
TR "v[178:179],v2 offset:26624\n"
MF "v[132:135],a[16:19],v[188:191],v[132:135]\n"
MF "v[124:127],a[12:15],v[188:191],v[252:255]\n"
TR "v[180:181],v2 offset:28672\n"
MF "v[124:127],a[52:55],a[240:243],v[124:127]\n"
TR "v[182:183],v2 offset:30720\n"
MF "v[124:127],a[92:95],a[244:247],v[124:127]\n"
MF "v[116:119],a[88:91],a[244:247],v[252:255]\n"
TR "v[184:185],v2 offset:32768\n"
MF "v[116:119],a[48:51],a[240:243],v[116:119]\n"
TR "v[186:187],v2 offset:34816\n"
MF "v[116:119],a[8:11],v[188:191],v[116:119]\n"
MF "v[108:111],a[4:7],v[188:191],v[252:255]\n"
MF "v[108:111],a[44:47],a[240:243],v[108:111]\n"
MF "v[108:111],a[84:87],a[244:247],v[108:111]\n"
MF "v[100:103],a[80:83],a[244:247],v[252:255]\n"
MF "v[100:103],a[40:43],a[240:243],v[100:103]\n"
MF "v[100:103],a[0:3],v[188:191],v[100:103]\n"
"s_waitcnt lgkmcnt(0)\n"
MF "v[136:139],a[0:3],v[176:179],v[252:255]\n"
TR "v[188:189],v3 offset:24576\n"
MF "v[136:139],a[40:43],v[180:183],v[136:139]\n"
TR "v[190:191],v3 offset:26624\n"
MF "v[136:139],a[80:83],v[184:187],v[136:139]\n"
MF "v[144:147],a[84:87],v[184:187],v[252:255]\n"
TR "a[240:241],v3 offset:28672\n"
MF "v[144:147],a[44:47],v[180:183],v[144:147]\n"
TR "a[242:243],v3 offset:30720\n"
MF "v[144:147],a[4:7],v[176:179],v[144:147]\n"
MF "v[152:155],a[8:11],v[176:179],v[252:255]\n"
TR "a[244:245],v3 offset:32768\n"
MF "v[152:155],a[48:51],v[180:183],v[152:155]\n"
TR "a[246:247],v3 offset:34816\n"
MF "v[152:155],a[88:91],v[184:187],v[152:155]\n"
MF "v[160:163],a[92:95],v[184:187],v[252:255]\n"
MF "v[160:163],a[52:55],v[180:183],v[160:163]\n"
MF "v[160:163],a[12:15],v[176:179],v[160:163]\n"
MF "v[168:171],a[16:19],v[176:179],v[252:255]\n"
MF "v[168:171],a[56:59],v[180:183],v[168:171]\n"
"s_waitcnt lgkmcnt(0)\n"
MF "v[168:171],a[96:99],v[184:187],v[168:171]\n"
"s_barrier\n"
MF "v[172:175],a[96:99],a[244:247],v[252:255]\n"
TR "v[176:177],v0\n"
MF "v[172:175],a[56:59],a[240:243],v[172:175]\n"
TR "v[178:179],v0 offset:2048\n"
MF "v[172:175],a[16:19],v[188:191],v[172:175]\n"
"ds_read2st64_b32 v[192:193],v13 offset0:192 offset1:193\n"
MF "v[164:167],a[12:15],v[188:191],v[252:255]\n"
TR "v[180:181],v0 offset:4096\n"
MF "v[164:167],a[52:55],a[240:243],v[164:167]\n"
TR "v[182:183],v0 offset:6144\n"
MF "v[164:167],a[92:95],a[244:247],v[164:167]\n"
"ds_read2st64_b32 v[194:195],v13 offset0:194 offset1:195\n"
MF "v[156:159],a[88:91],a[244:247],v[252:255]\n"
TR "v[184:185],v0 offset:8192\n"
MF "v[156:159],a[48:51],a[240:243],v[156:159]\n"
TR "v[186:187],v0 offset:10240\n"
MF "v[156:159],a[8:11],v[188:191],v[156:159]\n"
"ds_read2st64_b32 v[196:197],v13 offset0:196 offset1:197\n"
MF "v[148:151],a[4:7],v[188:191],v[252:255]\n"
"ds_read2st64_b32 v[198:199],v13 offset0:198 offset1:199\n"
MF "v[148:151],a[44:47],a[240:243],v[148:151]\n"
"buffer_load_dword v10,v12,s[40:43],s86 offen sc1\n"
MF "v[148:151],a[84:87],a[244:247],v[148:151]\n"
MF "v[140:143],a[80:83],a[244:247],v[252:255]\n"
MF "v[140:143],a[40:43],a[240:243],v[140:143]\n"
MF "v[140:143],a[0:3],v[188:191],v[140:143]\n"
"s_waitcnt lgkmcnt(2)\n"
MF "v[96:99],a[120:123],v[176:179],v[96:99]\n"
"s_waitcnt lgkmcnt(0)\n"
TR "v[188:189],v1\n"
"v_add_f32 v16,v16,v192\n"
MF "v[96:99],a[160:163],v[180:183],v[96:99]\n"
TR "v[190:191],v1 offset:2048\n"
"v_add_f32 v17,v17,v193\n"
MF "v[96:99],a[200:203],v[184:187],v[96:99]\n"
"ds_read2st64_b32 v[200:201],v13 offset0:200 offset1:201\n"
"v_add_f32 v18,v18,v194\n"
"s_waitcnt vmcnt(2)\n"
"v_mov_b32 v14,s76\n"
"s_add_u32 s76,s76,1\n"
MF "v[104:107],a[204:207],v[184:187],v[104:107]\n"
TR "a[240:241],v1 offset:4096\n"
"v_add_f32 v19,v19,v195\n"
"buffer_store_dword v14,v15,s[40:43],s94 offen sc1\n"
MF "v[104:107],a[164:167],v[180:183],v[104:107]\n"
TR "a[242:243],v1 offset:6144\n"
"v_add_f32 v20,v20,v196\n"
MF "v[104:107],a[124:127],v[176:179],v[104:107]\n"
"ds_read2st64_b32 v[202:203],v13 offset0:202 offset1:203\n"
"v_add_f32 v21,v21,v197\n"
MF "v[112:115],a[128:131],v[176:179],v[112:115]\n"
TR "a[244:245],v1 offset:8192\n"
"v_add_f32 v22,v22,v198\n"
MF "v[112:115],a[168:171],v[180:183],v[112:115]\n"
TR "a[246:247],v1 offset:10240\n"
"v_add_f32 v23,v23,v199\n"
MF "v[112:115],a[208:211],v[184:187],v[112:115]\n"
"ds_read2st64_b32 v[204:205],v13 offset0:204 offset1:205\n"
CV "v16,v16,v17\n"
MF "v[120:123],a[212:215],v[184:187],v[120:123]\n"
"ds_read2st64_b32 v[206:207],v13 offset0:206 offset1:207\n"
CV "v17,v18,v19\n"
MF "v[120:123],a[172:175],v[180:183],v[120:123]\n"
"s_add_u32 s87,s90,0\n"
CV "v18,v20,v21\n"
CV "v19,v22,v23\n"
MF "v[120:123],a[132:135],v[176:179],v[120:123]\n"
MF "v[128:131],a[136:139],v[176:179],v[128:131]\n"
"buffer_store_dwordx4 v[16:19],v9,s[40:43],s87 offen sc1\n"
MF "v[128:131],a[176:179],v[180:183],v[128:131]\n"
MF "v[128:131],a[216:219],v[184:187],v[128:131]\n"
"s_waitcnt lgkmcnt(2)\n"
MF "v[132:135],a[216:219],a[244:247],v[132:135]\n"
"s_waitcnt lgkmcnt(0)\n"
TR "v[176:177],v2\n"
"v_add_f32 v24,v24,v200\n"
MF "v[132:135],a[176:179],a[240:243],v[132:135]\n"
TR "v[178:179],v2 offset:2048\n"
"v_add_f32 v25,v25,v201\n"
MF "v[132:135],a[136:139],v[188:191],v[132:135]\n"
"ds_read2st64_b32 v[192:193],v13 offset0:208 offset1:209\n"
"v_add_f32 v26,v26,v202\n"
MF "v[124:127],a[132:135],v[188:191],v[124:127]\n"
TR "v[180:181],v2 offset:4096\n"
"v_add_f32 v27,v27,v203\n"
MF "v[124:127],a[172:175],a[240:243],v[124:127]\n"
TR "v[182:183],v2 offset:6144\n"
"v_add_f32 v28,v28,v204\n"
MF "v[124:127],a[212:215],a[244:247],v[124:127]\n"
"ds_read2st64_b32 v[194:195],v13 offset0:210 offset1:211\n"
"v_add_f32 v29,v29,v205\n"
MF "v[116:119],a[208:211],a[244:247],v[116:119]\n"
TR "v[184:185],v2 offset:8192\n"
"v_add_f32 v30,v30,v206\n"
MF "v[116:119],a[168:171],a[240:243],v[116:119]\n"
TR "v[186:187],v2 offset:10240\n"
"v_add_f32 v31,v31,v207\n"
MF "v[116:119],a[128:131],v[188:191],v[116:119]\n"
"ds_read2st64_b32 v[196:197],v13 offset0:212 offset1:213\n"
CV "v24,v24,v25\n"
MF "v[108:111],a[124:127],v[188:191],v[108:111]\n"
"ds_read2st64_b32 v[198:199],v13 offset0:214 offset1:215\n"
CV "v25,v26,v27\n"
MF "v[108:111],a[164:167],a[240:243],v[108:111]\n"
"s_add_u32 s87,s90,1024\n"
CV "v26,v28,v29\n"
CV "v27,v30,v31\n"
MF "v[108:111],a[204:207],a[244:247],v[108:111]\n"
MF "v[100:103],a[200:203],a[244:247],v[100:103]\n"
"buffer_store_dwordx4 v[24:27],v9,s[40:43],s87 offen sc1\n"
MF "v[100:103],a[160:163],a[240:243],v[100:103]\n"
MF "v[100:103],a[120:123],v[188:191],v[100:103]\n"
"s_waitcnt lgkmcnt(2)\n"
MF "v[136:139],a[120:123],v[176:179],v[136:139]\n"
"s_waitcnt lgkmcnt(0)\n"
TR "v[188:189],v3\n"
"v_add_f32 v32,v32,v192\n"
MF "v[136:139],a[160:163],v[180:183],v[136:139]\n"
TR "v[190:191],v3 offset:2048\n"
"v_add_f32 v33,v33,v193\n"
MF "v[136:139],a[200:203],v[184:187],v[136:139]\n"
"ds_read2st64_b32 v[200:201],v13 offset0:216 offset1:217\n"
"v_add_f32 v34,v34,v194\n"
MF "v[144:147],a[204:207],v[184:187],v[144:147]\n"
TR "a[240:241],v3 offset:4096\n"
"v_add_f32 v35,v35,v195\n"
MF "v[144:147],a[164:167],v[180:183],v[144:147]\n"
TR "a[242:243],v3 offset:6144\n"
"v_add_f32 v36,v36,v196\n"
MF "v[144:147],a[124:127],v[176:179],v[144:147]\n"
"ds_read2st64_b32 v[202:203],v13 offset0:218 offset1:219\n"
"v_add_f32 v37,v37,v197\n"
MF "v[152:155],a[128:131],v[176:179],v[152:155]\n"
".Lpg35h:\n"
"s_cbranch_execz .Lisr4\n"
TR "a[244:245],v3 offset:8192\n"
"v_add_f32 v38,v38,v198\n"
MF "v[152:155],a[168:171],v[180:183],v[152:155]\n"
TR "a[246:247],v3 offset:10240\n"
"v_add_f32 v39,v39,v199\n"
MF "v[152:155],a[208:211],v[184:187],v[152:155]\n"
"ds_read2st64_b32 v[204:205],v13 offset0:220 offset1:221\n"
CV "v32,v32,v33\n"
MF "v[160:163],a[212:215],v[184:187],v[160:163]\n"
"ds_read2st64_b32 v[206:207],v13 offset0:222 offset1:223\n"
CV "v33,v34,v35\n"
MF "v[160:163],a[172:175],v[180:183],v[160:163]\n"
"s_add_u32 s87,s90,2048\n"
CV "v34,v36,v37\n"
CV "v35,v38,v39\n"
MF "v[160:163],a[132:135],v[176:179],v[160:163]\n"
MF "v[168:171],a[136:139],v[176:179],v[168:171]\n"
"buffer_store_dwordx4 v[32:35],v9,s[40:43],s87 offen sc1\n"
MF "v[168:171],a[176:179],v[180:183],v[168:171]\n"
"s_waitcnt lgkmcnt(0)\n"
MF "v[168:171],a[216:219],v[184:187],v[168:171]\n"
"s_barrier\n"
MF "v[172:175],a[216:219],a[244:247],v[172:175]\n"
TR "v[176:177],v0 offset:49152\n"
"v_add_f32 v40,v40,v200\n"
MF "v[172:175],a[176:179],a[240:243],v[172:175]\n"
"s_mov_b32 s82,s70\n"
TR "v[178:179],v0 offset:51200\n"
"v_add_f32 v41,v41,v201\n"
MF "v[172:175],a[136:139],v[188:191],v[172:175]\n"
"ds_read2st64_b32 v[192:193],v13 offset0:224 offset1:225\n"
"v_add_f32 v42,v42,v202\n"
MF "v[164:167],a[132:135],v[188:191],v[164:167]\n"
TR "v[180:181],v0 offset:53248\n"
"v_add_f32 v43,v43,v203\n"
MF "v[164:167],a[172:175],a[240:243],v[164:167]\n"
TR "v[182:183],v0 offset:55296\n"
"v_add_f32 v44,v44,v204\n"
MF "v[164:167],a[212:215],a[244:247],v[164:167]\n"
"ds_read2st64_b32 v[194:195],v13 offset0:226 offset1:227\n"
"v_add_f32 v45,v45,v205\n"
MF "v[156:159],a[208:211],a[244:247],v[156:159]\n"
TR "v[184:185],v0 offset:57344\n"
"v_add_f32 v46,v46,v206\n"
MF "v[156:159],a[168:171],a[240:243],v[156:159]\n"
TR "v[186:187],v0 offset:59392\n"
"v_add_f32 v47,v47,v207\n"
MF "v[156:159],a[128:131],v[188:191],v[156:159]\n"
"ds_read2st64_b32 v[196:197],v13 offset0:228 offset1:229\n"
CV "v40,v40,v41\n"
MF "v[148:151],a[124:127],v[188:191],v[148:151]\n"
"ds_read2st64_b32 v[198:199],v13 offset0:230 offset1:231\n"
CV "v41,v42,v43\n"
MF "v[148:151],a[164:167],a[240:243],v[148:151]\n"
"s_add_u32 s87,s90,3072\n"
CV "v42,v44,v45\n"
CV "v43,v46,v47\n"
MF "v[148:151],a[204:207],a[244:247],v[148:151]\n"
MF "v[140:143],a[200:203],a[244:247],v[140:143]\n"
"buffer_store_dwordx4 v[40:43],v9,s[40:43],s87 offen sc1\n"
MF "v[140:143],a[160:163],a[240:243],v[140:143]\n"
MF "v[140:143],a[120:123],v[188:191],v[140:143]\n"
"s_waitcnt lgkmcnt(2)\n"
MF "v[96:99],a[20:23],v[176:179],v[96:99]\n"
"s_waitcnt lgkmcnt(0)\n"
TR "v[188:189],v1 offset:49152\n"
"v_add_f32 v48,v48,v192\n"
MF "v[96:99],a[60:63],v[180:183],v[96:99]\n"
TR "v[190:191],v1 offset:51200\n"
"v_add_f32 v49,v49,v193\n"
MF "v[96:99],a[100:103],v[184:187],v[96:99]\n"
"ds_read2st64_b32 v[200:201],v13 offset0:0 offset1:1\n"
"v_add_f32 v50,v50,v194\n"
MF "v[104:107],a[104:107],v[184:187],v[104:107]\n"
TR "a[240:241],v1 offset:53248\n"
"v_add_f32 v51,v51,v195\n"
MF "v[104:107],a[64:67],v[180:183],v[104:107]\n"
TR "a[242:243],v1 offset:55296\n"
"v_add_f32 v52,v52,v196\n"
MF "v[104:107],a[24:27],v[176:179],v[104:107]\n"
"ds_read2st64_b32 v[202:203],v13 offset0:2 offset1:3\n"
"v_add_f32 v53,v53,v197\n"
MF "v[112:115],a[28:31],v[176:179],v[112:115]\n"
TR "a[244:245],v1 offset:57344\n"
"v_add_f32 v54,v54,v198\n"
MF "v[112:115],a[68:71],v[180:183],v[112:115]\n"
TR "a[246:247],v1 offset:59392\n"
"v_add_f32 v55,v55,v199\n"
MF "v[112:115],a[108:111],v[184:187],v[112:115]\n"
"ds_read2st64_b32 v[204:205],v13 offset0:4 offset1:5\n"
CV "v48,v48,v49\n"
MF "v[120:123],a[112:115],v[184:187],v[120:123]\n"
"ds_read2st64_b32 v[206:207],v13 offset0:6 offset1:7\n"
CV "v49,v50,v51\n"
MF "v[120:123],a[72:75],v[180:183],v[120:123]\n"
"s_add_u32 s87,s90,4096\n"
CV "v50,v52,v53\n"
CV "v51,v54,v55\n"
MF "v[120:123],a[32:35],v[176:179],v[120:123]\n"
MF "v[128:131],a[36:39],v[176:179],v[128:131]\n"
"buffer_store_dwordx4 v[48:51],v9,s[40:43],s87 offen sc1\n"
MF "v[128:131],a[76:79],v[180:183],v[128:131]\n"
MF "v[128:131],a[116:119],v[184:187],v[128:131]\n"
"s_waitcnt lgkmcnt(2)\n"
MF "v[132:135],a[116:119],a[244:247],v[132:135]\n"
"s_waitcnt lgkmcnt(0)\n"
TR "v[176:177],v2 offset:49152\n"
"v_add_f32 v56,v56,v200\n"
MF "v[132:135],a[76:79],a[240:243],v[132:135]\n"
TR "v[178:179],v2 offset:51200\n"
"v_add_f32 v57,v57,v201\n"
MF "v[132:135],a[36:39],v[188:191],v[132:135]\n"
"ds_read2st64_b32 v[192:193],v13 offset0:8 offset1:9\n"
"v_add_f32 v58,v58,v202\n"
MF "v[124:127],a[32:35],v[188:191],v[124:127]\n"
TR "v[180:181],v2 offset:53248\n"
"v_add_f32 v59,v59,v203\n"
MF "v[124:127],a[72:75],a[240:243],v[124:127]\n"
TR "v[182:183],v2 offset:55296\n"
"v_add_f32 v60,v60,v204\n"
MF "v[124:127],a[112:115],a[244:247],v[124:127]\n"
"ds_read2st64_b32 v[194:195],v13 offset0:10 offset1:11\n"
"v_add_f32 v61,v61,v205\n"
MF "v[116:119],a[108:111],a[244:247],v[116:119]\n"
TR "v[184:185],v2 offset:57344\n"
"v_add_f32 v62,v62,v206\n"
MF "v[116:119],a[68:71],a[240:243],v[116:119]\n"
TR "v[186:187],v2 offset:59392\n"
"v_add_f32 v63,v63,v207\n"
MF "v[116:119],a[28:31],v[188:191],v[116:119]\n"
"ds_read2st64_b32 v[196:197],v13 offset0:12 offset1:13\n"
CV "v56,v56,v57\n"
MF "v[108:111],a[24:27],v[188:191],v[108:111]\n"
"ds_read2st64_b32 v[198:199],v13 offset0:14 offset1:15\n"
CV "v57,v58,v59\n"
MF "v[108:111],a[64:67],a[240:243],v[108:111]\n"
"s_add_u32 s87,s90,10240\n"
CV "v58,v60,v61\n"
CV "v59,v62,v63\n"
MF "v[108:111],a[104:107],a[244:247],v[108:111]\n"
MF "v[100:103],a[100:103],a[244:247],v[100:103]\n"
"buffer_store_dwordx4 v[56:59],v9,s[40:43],s87 offen sc1\n"
MF "v[100:103],a[60:63],a[240:243],v[100:103]\n"
"s_waitcnt vmcnt(7)\n"
"s_cmp_lt_u32 s85,6\n"
MF "v[100:103],a[20:23],v[188:191],v[100:103]\n"
"s_cbranch_scc1 .Ljs10\n"
"v_mov_b32 v236,v228\n"
"v_mov_b32 v237,v229\n"
MF "v[136:139],a[252:255],v[208:211],v[136:139]\n"
MF "v[144:147],a[252:255],v[212:215],v[144:147]\n"
MF "v[152:155],a[252:255],v[216:219],v[152:155]\n"
MF "v[160:163],a[252:255],v[220:223],v[160:163]\n"
MF "v[168:171],a[252:255],v[224:227],v[168:171]\n"
MF "v[140:143],a[248:251],v[208:211],v[140:143]\n"
MF "v[148:151],a[248:251],v[212:215],v[148:151]\n"
MF "v[156:159],a[248:251],v[216:219],v[156:159]\n"
MF "v[164:167],a[248:251],v[220:223],v[164:167]\n"
MF "v[172:175],a[248:251],v[224:227],v[172:175]\n"
MF "v[248:251],v[230:233],a[250:253],v[248:251]\n"
MF "v[244:247],v[234:237],a[250:253],v[244:247]\n"
".Ljs10:\n"
"s_waitcnt lgkmcnt(2)\n"
MF "v[136:139],a[20:23],v[176:179],v[136:139]\n"
"s_waitcnt lgkmcnt(0)\n"
TR "v[188:189],v3 offset:49152\n"
"v_add_f32 v64,v64,v192\n"
MF "v[136:139],a[60:63],v[180:183],v[136:139]\n"
TR "v[190:191],v3 offset:51200\n"
"v_add_f32 v65,v65,v193\n"
MF "v[136:139],a[100:103],v[184:187],v[136:139]\n"
"ds_read2st64_b32 v[200:201],v13 offset0:16 offset1:17\n"
"v_add_f32 v66,v66,v194\n"
MF "v[144:147],a[104:107],v[184:187],v[144:147]\n"
TR "a[240:241],v3 offset:53248\n"
"v_add_f32 v67,v67,v195\n"
MF "v[144:147],a[64:67],v[180:183],v[144:147]\n"
TR "a[242:243],v3 offset:55296\n"
"v_add_f32 v68,v68,v196\n"
MF "v[144:147],a[24:27],v[176:179],v[144:147]\n"
"ds_read2st64_b32 v[202:203],v13 offset0:18 offset1:19\n"
"v_add_f32 v69,v69,v197\n"
MF "v[152:155],a[28:31],v[176:179],v[152:155]\n"
TR "a[244:245],v3 offset:57344\n"
"v_add_f32 v70,v70,v198\n"
MF "v[152:155],a[68:71],v[180:183],v[152:155]\n"
TR "a[246:247],v3 offset:59392\n"
"v_add_f32 v71,v71,v199\n"
MF "v[152:155],a[108:111],v[184:187],v[152:155]\n"
"ds_read2st64_b32 v[204:205],v13 offset0:20 offset1:21\n"
CV "v64,v64,v65\n"
MF "v[160:163],a[112:115],v[184:187],v[160:163]\n"
"ds_read2st64_b32 v[206:207],v13 offset0:22 offset1:23\n"
CV "v65,v66,v67\n"
MF "v[160:163],a[72:75],v[180:183],v[160:163]\n"
"s_add_u32 s87,s90,11264\n"
CV "v66,v68,v69\n"
CV "v67,v70,v71\n"
MF "v[160:163],a[32:35],v[176:179],v[160:163]\n"
"v_sub_u32 v10,v10,v11\n"
MF "v[168:171],a[36:39],v[176:179],v[168:171]\n"
"buffer_store_dwordx4 v[64:67],v9,s[40:43],s87 offen sc1\n"
MF "v[168:171],a[76:79],v[180:183],v[168:171]\n"
"s_waitcnt lgkmcnt(0)\n"
MF "v[168:171],a[116:119],v[184:187],v[168:171]\n"
"s_barrier\n"
MF "v[172:175],a[116:119],a[244:247],v[172:175]\n"
TR "v[176:177],v4\n"
"v_add_f32 v72,v72,v200\n"
MF "v[172:175],a[76:79],a[240:243],v[172:175]\n"
TR "v[178:179],v4 offset:2048\n"
"v_add_f32 v73,v73,v201\n"
MF "v[172:175],a[36:39],v[188:191],v[172:175]\n"
"ds_read2st64_b32 v[192:193],v13 offset0:24 offset1:25\n"
"v_add_f32 v74,v74,v202\n"
MF "v[164:167],a[32:35],v[188:191],v[164:167]\n"
TR "v[180:181],v4 offset:4096\n"
"v_add_f32 v75,v75,v203\n"
MF "v[164:167],a[72:75],a[240:243],v[164:167]\n"
TR "v[182:183],v4 offset:6144\n"
"v_add_f32 v76,v76,v204\n"
MF "v[164:167],a[112:115],a[244:247],v[164:167]\n"
"ds_read2st64_b32 v[194:195],v13 offset0:26 offset1:27\n"
"v_add_f32 v77,v77,v205\n"
MF "v[156:159],a[108:111],a[244:247],v[156:159]\n"
TR "v[184:185],v4 offset:8192\n"
"v_add_f32 v78,v78,v206\n"
MF "v[156:159],a[68:71],a[240:243],v[156:159]\n"
TR "v[186:187],v4 offset:10240\n"
"v_add_f32 v79,v79,v207\n"
MF "v[156:159],a[28:31],v[188:191],v[156:159]\n"
"ds_read2st64_b32 v[196:197],v13 offset0:28 offset1:29\n"
CV "v72,v72,v73\n"
MF "v[148:151],a[24:27],v[188:191],v[148:151]\n"
"ds_read2st64_b32 v[198:199],v13 offset0:30 offset1:31\n"
CV "v73,v74,v75\n"
MF "v[148:151],a[64:67],a[240:243],v[148:151]\n"
"s_add_u32 s87,s90,12288\n"
CV "v74,v76,v77\n"
CV "v75,v78,v79\n"
MF "v[148:151],a[104:107],a[244:247],v[148:151]\n"
".Lnf6:\n"
"v_readlane_b32 s58,v10,s67\n"
"s_cmp_lt_u32 s58,s85\n"
"s_cbranch_scc1 .Lsp6\n"
".Lnb6:\n"
MF "v[140:143],a[100:103],a[244:247],v[140:143]\n"
"buffer_store_dwordx4 v[72:75],v9,s[40:43],s87 offen sc1\n"
MF "v[140:143],a[60:63],a[240:243],v[140:143]\n"
"s_add_u32 s70,s70,20480\n"
".Lnf7:\n"
"v_cmp_gt_u32 vcc,4,v10\n"
"s_cbranch_vccnz .Lsp7\n"
".Lnb7:\n"
MF "v[140:143],a[20:23],v[188:191],v[140:143]\n"
"s_waitcnt lgkmcnt(2)\n"
MF "v[96:99],a[140:143],v[176:179],v[96:99]\n"
"s_waitcnt lgkmcnt(0)\n"
TR "v[188:189],v5\n"
"v_add_f32 v80,v80,v192\n"
MF "v[96:99],a[180:183],v[180:183],v[96:99]\n"
TR "v[190:191],v5 offset:2048\n"
"v_add_f32 v81,v81,v193\n"
MF "v[96:99],a[220:223],v[184:187],v[96:99]\n"
"ds_read2st64_b32 v[200:201],v13 offset0:32 offset1:33\n"
"v_add_f32 v82,v82,v194\n"
MF "v[104:107],a[224:227],v[184:187],v[104:107]\n"
TR "a[240:241],v5 offset:4096\n"
"v_add_f32 v83,v83,v195\n"
MF "v[104:107],a[184:187],v[180:183],v[104:107]\n"
TR "a[242:243],v5 offset:6144\n"
"v_add_f32 v84,v84,v196\n"
MF "v[104:107],a[144:147],v[176:179],v[104:107]\n"
"ds_read2st64_b32 v[202:203],v13 offset0:34 offset1:35\n"
"v_add_f32 v85,v85,v197\n"
MF "v[112:115],a[148:151],v[176:179],v[112:115]\n"
TR "a[244:245],v5 offset:8192\n"
"v_add_f32 v86,v86,v198\n"
MF "v[112:115],a[188:191],v[180:183],v[112:115]\n"
TR "a[246:247],v5 offset:10240\n"
"v_add_f32 v87,v87,v199\n"
MF "v[112:115],a[228:231],v[184:187],v[112:115]\n"
"ds_read2st64_b32 v[204:205],v13 offset0:36 offset1:37\n"
CV "v80,v80,v81\n"
MF "v[120:123],a[232:235],v[184:187],v[120:123]\n"
"s_add_u32 s2,s2,204800\n"
"ds_read2st64_b32 v[206:207],v13 offset0:38 offset1:39\n"
CV "v81,v82,v83\n"
MF "v[120:123],a[192:195],v[180:183],v[120:123]\n"
"s_add_u32 s87,s90,13312\n"
CV "v82,v84,v85\n"
CV "v83,v86,v87\n"
MF "v[120:123],a[152:155],v[176:179],v[120:123]\n"
LD "v[228:231],v9,s[40:43],s2 offen sc1\n"
MF "v[128:131],a[156:159],v[176:179],v[128:131]\n"
"buffer_store_dwordx4 v[80:83],v9,s[40:43],s87 offen sc1\n"
MF "v[128:131],a[196:199],v[180:183],v[128:131]\n"
"s_add_u32 s2,s2,204800\n"
MF "v[128:131],a[236:239],v[184:187],v[128:131]\n"
"s_waitcnt lgkmcnt(2)\n"
LD "v[232:235],v9,s[40:43],s2 offen sc1\n"
MF "v[132:135],a[236:239],a[244:247],v[132:135]\n"
"s_waitcnt lgkmcnt(0)\n"
TR "v[176:177],v6\n"
"v_add_f32 v88,v88,v200\n"
MF "v[132:135],a[196:199],a[240:243],v[132:135]\n"
TR "v[178:179],v6 offset:2048\n"
"v_add_f32 v89,v89,v201\n"
MF "v[132:135],a[156:159],v[188:191],v[132:135]\n"
"v_add_f32 v90,v90,v202\n"
"v_add_f32 v91,v91,v203\n"
MF "v[124:127],a[152:155],v[188:191],v[124:127]\n"
TR "v[180:181],v6 offset:4096\n"
"v_add_f32 v92,v92,v204\n"
MF "v[124:127],a[192:195],a[240:243],v[124:127]\n"
TR "v[182:183],v6 offset:6144\n"
"v_add_f32 v93,v93,v205\n"
MF "v[124:127],a[232:235],a[244:247],v[124:127]\n"
"v_add_f32 v94,v94,v206\n"
"v_add_f32 v95,v95,v207\n"
MF "v[116:119],a[228:231],a[244:247],v[116:119]\n"
TR "v[184:185],v6 offset:8192\n"
CV "v88,v88,v89\n"
MF "v[116:119],a[188:191],a[240:243],v[116:119]\n"
TR "v[186:187],v6 offset:10240\n"
CV "v89,v90,v91\n"
MF "v[116:119],a[148:151],v[188:191],v[116:119]\n"
"s_add_u32 s87,s90,14336\n"
CV "v90,v92,v93\n"
CV "v91,v94,v95\n"
MF "v[108:111],a[144:147],v[188:191],v[108:111]\n"
LD "v[208:211],v9,s[40:43],s82 offen sc1\n"
MF "v[108:111],a[184:187],a[240:243],v[108:111]\n"
MF "v[108:111],a[224:227],a[244:247],v[108:111]\n"
MF "v[100:103],a[220:223],a[244:247],v[100:103]\n"
"buffer_store_dwordx4 v[88:91],v9,s[40:43],s87 offen sc1\n"
MF "v[100:103],a[180:183],a[240:243],v[100:103]\n"
"s_mov_b32 s90,s91\n"
"s_add_u32 s91,s91,20480\n"
MF "v[100:103],a[140:143],v[188:191],v[100:103]\n"
"s_waitcnt lgkmcnt(0)\n"
MF "v[136:139],a[140:143],v[176:179],v[136:139]\n"
TR "v[188:189],v7\n"
MF "v[136:139],a[180:183],v[180:183],v[136:139]\n"
TR "v[190:191],v7 offset:2048\n"
MF "v[136:139],a[220:223],v[184:187],v[136:139]\n"
LD "v[212:215],v9,s[40:43],s82 offen offset:1024 sc1\n"
MF "v[144:147],a[224:227],v[184:187],v[144:147]\n"
TR "a[240:241],v7 offset:4096\n"
MF "v[144:147],a[184:187],v[180:183],v[144:147]\n"
TR "a[242:243],v7 offset:6144\n"
MF "v[144:147],a[144:147],v[176:179],v[144:147]\n"
MF "v[152:155],a[148:151],v[176:179],v[152:155]\n"
TR "a[244:245],v7 offset:8192\n"
MF "v[152:155],a[188:191],v[180:183],v[152:155]\n"
TR "a[246:247],v7 offset:10240\n"
MF "v[152:155],a[228:231],v[184:187],v[152:155]\n"
MF "v[160:163],a[232:235],v[184:187],v[160:163]\n"
MF "v[160:163],a[192:195],v[180:183],v[160:163]\n"
LD "v[216:219],v9,s[40:43],s82 offen offset:2048 sc1\n"
MF "v[160:163],a[152:155],v[176:179],v[160:163]\n"
MF "v[168:171],a[156:159],v[176:179],v[168:171]\n"
MF "v[168:171],a[196:199],v[180:183],v[168:171]\n"
"s_waitcnt lgkmcnt(0)\n"
MF "v[168:171],a[236:239],v[184:187],v[168:171]\n"
"s_barrier\n"
MF "v[172:175],a[236:239],a[244:247],v[172:175]\n"
TR "v[176:177],v0 offset:24576\n"
MF "v[172:175],a[196:199],a[240:243],v[172:175]\n"
"s_add_u32 s85,s85,1\n"
TR "v[178:179],v0 offset:26624\n"
MF "v[172:175],a[156:159],v[188:191],v[172:175]\n"
MF "v[164:167],a[152:155],v[188:191],v[164:167]\n"
TR "v[180:181],v0 offset:28672\n"
MF "v[164:167],a[192:195],a[240:243],v[164:167]\n"
TR "v[182:183],v0 offset:30720\n"
MF "v[164:167],a[232:235],a[244:247],v[164:167]\n"
LD "v[220:223],v9,s[40:43],s82 offen offset:3072 sc1\n"
MF "v[156:159],a[228:231],a[244:247],v[156:159]\n"
TR "v[184:185],v0 offset:32768\n"
MF "v[156:159],a[188:191],a[240:243],v[156:159]\n"
TR "v[186:187],v0 offset:34816\n"
MF "v[156:159],a[148:151],v[188:191],v[156:159]\n"
MF "v[148:151],a[144:147],v[188:191],v[148:151]\n"
MF "v[148:151],a[184:187],a[240:243],v[148:151]\n"
MF "v[148:151],a[224:227],a[244:247],v[148:151]\n"
MF "v[140:143],a[220:223],a[244:247],v[140:143]\n"
MF "v[140:143],a[180:183],a[240:243],v[140:143]\n"
MF "v[140:143],a[140:143],v[188:191],v[140:143]\n"
"s_waitcnt lgkmcnt(0)\n"
MF "v[16:19],a[0:3],v[176:179],v[252:255]\n"
TR "v[188:189],v1 offset:24576\n"
MF "v[16:19],a[40:43],v[180:183],v[16:19]\n"
"s_add_u32 s82,s82,4096\n"
TR "v[190:191],v1 offset:26624\n"
MF "v[16:19],a[80:83],v[184:187],v[16:19]\n"
LD "v[224:227],v9,s[40:43],s82 offen sc1\n"
MF "v[24:27],a[84:87],v[184:187],v[252:255]\n"
TR "a[240:241],v1 offset:28672\n"
MF "v[24:27],a[44:47],v[180:183],v[24:27]\n"
TR "a[242:243],v1 offset:30720\n"
MF "v[24:27],a[4:7],v[176:179],v[24:27]\n"
MF "v[32:35],a[8:11],v[176:179],v[252:255]\n"
TR "a[244:245],v1 offset:32768\n"
MF "v[32:35],a[48:51],v[180:183],v[32:35]\n"
TR "a[246:247],v1 offset:34816\n"
MF "v[32:35],a[88:91],v[184:187],v[32:35]\n"
MF "v[40:43],a[92:95],v[184:187],v[252:255]\n"
MF "v[40:43],a[52:55],v[180:183],v[40:43]\n"
MF "v[40:43],a[12:15],v[176:179],v[40:43]\n"
MF "v[48:51],a[16:19],v[176:179],v[252:255]\n"
MF "v[48:51],a[56:59],v[180:183],v[48:51]\n"
MF "v[48:51],a[96:99],v[184:187],v[48:51]\n"
"s_waitcnt lgkmcnt(0)\n"
MF "v[52:55],a[96:99],a[244:247],v[252:255]\n"
TR "v[176:177],v2 offset:24576\n"
MF "v[52:55],a[56:59],a[240:243],v[52:55]\n"
TR "v[178:179],v2 offset:26624\n"
MF "v[52:55],a[16:19],v[188:191],v[52:55]\n"
MF "v[44:47],a[12:15],v[188:191],v[252:255]\n"
TR "v[180:181],v2 offset:28672\n"
MF "v[44:47],a[52:55],a[240:243],v[44:47]\n"
TR "v[182:183],v2 offset:30720\n"
MF "v[44:47],a[92:95],a[244:247],v[44:47]\n"
MF "v[36:39],a[88:91],a[244:247],v[252:255]\n"
TR "v[184:185],v2 offset:32768\n"
MF "v[36:39],a[48:51],a[240:243],v[36:39]\n"
TR "v[186:187],v2 offset:34816\n"
MF "v[36:39],a[8:11],v[188:191],v[36:39]\n"
MF "v[28:31],a[4:7],v[188:191],v[252:255]\n"
MF "v[28:31],a[44:47],a[240:243],v[28:31]\n"
MF "v[28:31],a[84:87],a[244:247],v[28:31]\n"
MF "v[20:23],a[80:83],a[244:247],v[252:255]\n"
MF "v[20:23],a[40:43],a[240:243],v[20:23]\n"
MF "v[20:23],a[0:3],v[188:191],v[20:23]\n"
"s_waitcnt lgkmcnt(0)\n"
MF "v[56:59],a[0:3],v[176:179],v[252:255]\n"
TR "v[188:189],v3 offset:24576\n"
MF "v[56:59],a[40:43],v[180:183],v[56:59]\n"
TR "v[190:191],v3 offset:26624\n"
MF "v[56:59],a[80:83],v[184:187],v[56:59]\n"
MF "v[64:67],a[84:87],v[184:187],v[252:255]\n"
TR "a[240:241],v3 offset:28672\n"
MF "v[64:67],a[44:47],v[180:183],v[64:67]\n"
TR "a[242:243],v3 offset:30720\n"
MF "v[64:67],a[4:7],v[176:179],v[64:67]\n"
MF "v[72:75],a[8:11],v[176:179],v[252:255]\n"
TR "a[244:245],v3 offset:32768\n"
MF "v[72:75],a[48:51],v[180:183],v[72:75]\n"
TR "a[246:247],v3 offset:34816\n"
MF "v[72:75],a[88:91],v[184:187],v[72:75]\n"
MF "v[80:83],a[92:95],v[184:187],v[252:255]\n"
MF "v[80:83],a[52:55],v[180:183],v[80:83]\n"
MF "v[80:83],a[12:15],v[176:179],v[80:83]\n"
MF "v[88:91],a[16:19],v[176:179],v[252:255]\n"
MF "v[88:91],a[56:59],v[180:183],v[88:91]\n"
"s_waitcnt lgkmcnt(0)\n"
MF "v[88:91],a[96:99],v[184:187],v[88:91]\n"
"s_barrier\n"
MF "v[92:95],a[96:99],a[244:247],v[252:255]\n"
TR "v[176:177],v0\n"
MF "v[92:95],a[56:59],a[240:243],v[92:95]\n"
TR "v[178:179],v0 offset:2048\n"
MF "v[92:95],a[16:19],v[188:191],v[92:95]\n"
"ds_read2st64_b32 v[192:193],v13 offset0:192 offset1:193\n"
MF "v[84:87],a[12:15],v[188:191],v[252:255]\n"
TR "v[180:181],v0 offset:4096\n"
MF "v[84:87],a[52:55],a[240:243],v[84:87]\n"
TR "v[182:183],v0 offset:6144\n"
MF "v[84:87],a[92:95],a[244:247],v[84:87]\n"
"ds_read2st64_b32 v[194:195],v13 offset0:194 offset1:195\n"
MF "v[76:79],a[88:91],a[244:247],v[252:255]\n"
TR "v[184:185],v0 offset:8192\n"
MF "v[76:79],a[48:51],a[240:243],v[76:79]\n"
TR "v[186:187],v0 offset:10240\n"
MF "v[76:79],a[8:11],v[188:191],v[76:79]\n"
"ds_read2st64_b32 v[196:197],v13 offset0:196 offset1:197\n"
MF "v[68:71],a[4:7],v[188:191],v[252:255]\n"
"ds_read2st64_b32 v[198:199],v13 offset0:198 offset1:199\n"
MF "v[68:71],a[44:47],a[240:243],v[68:71]\n"
"buffer_load_dword v10,v12,s[40:43],s86 offen sc1\n"
MF "v[68:71],a[84:87],a[244:247],v[68:71]\n"
MF "v[60:63],a[80:83],a[244:247],v[252:255]\n"
MF "v[60:63],a[40:43],a[240:243],v[60:63]\n"
MF "v[60:63],a[0:3],v[188:191],v[60:63]\n"
"s_waitcnt lgkmcnt(2)\n"
MF "v[16:19],a[120:123],v[176:179],v[16:19]\n"
"s_waitcnt lgkmcnt(0)\n"
TR "v[188:189],v1\n"
"v_add_f32 v96,v96,v192\n"
MF "v[16:19],a[160:163],v[180:183],v[16:19]\n"
TR "v[190:191],v1 offset:2048\n"
"v_add_f32 v97,v97,v193\n"
MF "v[16:19],a[200:203],v[184:187],v[16:19]\n"
"ds_read2st64_b32 v[200:201],v13 offset0:200 offset1:201\n"
"v_add_f32 v98,v98,v194\n"
"s_waitcnt vmcnt(5)\n"
"v_mov_b32 v14,s76\n"
"s_add_u32 s76,s76,1\n"
MF "v[24:27],a[204:207],v[184:187],v[24:27]\n"
TR "a[240:241],v1 offset:4096\n"
"v_add_f32 v99,v99,v195\n"
"buffer_store_dword v14,v15,s[40:43],s94 offen sc1\n"
MF "v[24:27],a[164:167],v[180:183],v[24:27]\n"
TR "a[242:243],v1 offset:6144\n"
"v_add_f32 v100,v100,v196\n"
MF "v[24:27],a[124:127],v[176:179],v[24:27]\n"
"ds_read2st64_b32 v[202:203],v13 offset0:202 offset1:203\n"
"v_add_f32 v101,v101,v197\n"
MF "v[32:35],a[128:131],v[176:179],v[32:35]\n"
TR "a[244:245],v1 offset:8192\n"
"v_add_f32 v102,v102,v198\n"
MF "v[32:35],a[168:171],v[180:183],v[32:35]\n"
TR "a[246:247],v1 offset:10240\n"
"v_add_f32 v103,v103,v199\n"
MF "v[32:35],a[208:211],v[184:187],v[32:35]\n"
"ds_read2st64_b32 v[204:205],v13 offset0:204 offset1:205\n"
CV "v96,v96,v97\n"
MF "v[40:43],a[212:215],v[184:187],v[40:43]\n"
"ds_read2st64_b32 v[206:207],v13 offset0:206 offset1:207\n"
CV "v97,v98,v99\n"
MF "v[40:43],a[172:175],v[180:183],v[40:43]\n"
"s_add_u32 s87,s90,0\n"
CV "v98,v100,v101\n"
CV "v99,v102,v103\n"
MF "v[40:43],a[132:135],v[176:179],v[40:43]\n"
MF "v[48:51],a[136:139],v[176:179],v[48:51]\n"
"buffer_store_dwordx4 v[96:99],v9,s[40:43],s87 offen sc1\n"
MF "v[48:51],a[176:179],v[180:183],v[48:51]\n"
MF "v[48:51],a[216:219],v[184:187],v[48:51]\n"
"s_waitcnt lgkmcnt(2)\n"
MF "v[52:55],a[216:219],a[244:247],v[52:55]\n"
"s_waitcnt lgkmcnt(0)\n"
TR "v[176:177],v2\n"
"v_add_f32 v104,v104,v200\n"
MF "v[52:55],a[176:179],a[240:243],v[52:55]\n"
TR "v[178:179],v2 offset:2048\n"
"v_add_f32 v105,v105,v201\n"
MF "v[52:55],a[136:139],v[188:191],v[52:55]\n"
"ds_read2st64_b32 v[192:193],v13 offset0:208 offset1:209\n"
"v_add_f32 v106,v106,v202\n"
MF "v[44:47],a[132:135],v[188:191],v[44:47]\n"
TR "v[180:181],v2 offset:4096\n"
"v_add_f32 v107,v107,v203\n"
MF "v[44:47],a[172:175],a[240:243],v[44:47]\n"
TR "v[182:183],v2 offset:6144\n"
"v_add_f32 v108,v108,v204\n"
MF "v[44:47],a[212:215],a[244:247],v[44:47]\n"
"ds_read2st64_b32 v[194:195],v13 offset0:210 offset1:211\n"
"v_add_f32 v109,v109,v205\n"
MF "v[36:39],a[208:211],a[244:247],v[36:39]\n"
TR "v[184:185],v2 offset:8192\n"
"v_add_f32 v110,v110,v206\n"
MF "v[36:39],a[168:171],a[240:243],v[36:39]\n"
TR "v[186:187],v2 offset:10240\n"
"v_add_f32 v111,v111,v207\n"
MF "v[36:39],a[128:131],v[188:191],v[36:39]\n"
"ds_read2st64_b32 v[196:197],v13 offset0:212 offset1:213\n"
CV "v104,v104,v105\n"
MF "v[28:31],a[124:127],v[188:191],v[28:31]\n"
"ds_read2st64_b32 v[198:199],v13 offset0:214 offset1:215\n"
CV "v105,v106,v107\n"
MF "v[28:31],a[164:167],a[240:243],v[28:31]\n"
"s_add_u32 s87,s90,1024\n"
CV "v106,v108,v109\n"
CV "v107,v110,v111\n"
MF "v[28:31],a[204:207],a[244:247],v[28:31]\n"
MF "v[20:23],a[200:203],a[244:247],v[20:23]\n"
"buffer_store_dwordx4 v[104:107],v9,s[40:43],s87 offen sc1\n"
MF "v[20:23],a[160:163],a[240:243],v[20:23]\n"
MF "v[20:23],a[120:123],v[188:191],v[20:23]\n"
"s_waitcnt lgkmcnt(2)\n"
MF "v[56:59],a[120:123],v[176:179],v[56:59]\n"
"s_waitcnt lgkmcnt(0)\n"
TR "v[188:189],v3\n"
"v_add_f32 v112,v112,v192\n"
MF "v[56:59],a[160:163],v[180:183],v[56:59]\n"
".Lpg36h:\n"
"s_cbranch_execz .Lisr4\n"
TR "v[190:191],v3 offset:2048\n"
"v_add_f32 v113,v113,v193\n"
MF "v[56:59],a[200:203],v[184:187],v[56:59]\n"
"ds_read2st64_b32 v[200:201],v13 offset0:216 offset1:217\n"
"v_add_f32 v114,v114,v194\n"
MF "v[64:67],a[204:207],v[184:187],v[64:67]\n"
TR "a[240:241],v3 offset:4096\n"
"v_add_f32 v115,v115,v195\n"
MF "v[64:67],a[164:167],v[180:183],v[64:67]\n"
TR "a[242:243],v3 offset:6144\n"
"v_add_f32 v116,v116,v196\n"
MF "v[64:67],a[124:127],v[176:179],v[64:67]\n"
"ds_read2st64_b32 v[202:203],v13 offset0:218 offset1:219\n"
"v_add_f32 v117,v117,v197\n"
MF "v[72:75],a[128:131],v[176:179],v[72:75]\n"
TR "a[244:245],v3 offset:8192\n"
"v_add_f32 v118,v118,v198\n"
MF "v[72:75],a[168:171],v[180:183],v[72:75]\n"
TR "a[246:247],v3 offset:10240\n"
"v_add_f32 v119,v119,v199\n"
MF "v[72:75],a[208:211],v[184:187],v[72:75]\n"
"ds_read2st64_b32 v[204:205],v13 offset0:220 offset1:221\n"
CV "v112,v112,v113\n"
MF "v[80:83],a[212:215],v[184:187],v[80:83]\n"
"ds_read2st64_b32 v[206:207],v13 offset0:222 offset1:223\n"
CV "v113,v114,v115\n"
MF "v[80:83],a[172:175],v[180:183],v[80:83]\n"
"s_add_u32 s87,s90,2048\n"
CV "v114,v116,v117\n"
CV "v115,v118,v119\n"
MF "v[80:83],a[132:135],v[176:179],v[80:83]\n"
MF "v[88:91],a[136:139],v[176:179],v[88:91]\n"
"buffer_store_dwordx4 v[112:115],v9,s[40:43],s87 offen sc1\n"
MF "v[88:91],a[176:179],v[180:183],v[88:91]\n"
"s_waitcnt lgkmcnt(0)\n"
MF "v[88:91],a[216:219],v[184:187],v[88:91]\n"
"s_barrier\n"
MF "v[92:95],a[216:219],a[244:247],v[92:95]\n"
TR "v[176:177],v0 offset:49152\n"
"v_add_f32 v120,v120,v200\n"
MF "v[92:95],a[176:179],a[240:243],v[92:95]\n"
"s_mov_b32 s82,s70\n"
TR "v[178:179],v0 offset:51200\n"
"v_add_f32 v121,v121,v201\n"
MF "v[92:95],a[136:139],v[188:191],v[92:95]\n"
"ds_read2st64_b32 v[192:193],v13 offset0:224 offset1:225\n"
"v_add_f32 v122,v122,v202\n"
MF "v[84:87],a[132:135],v[188:191],v[84:87]\n"
TR "v[180:181],v0 offset:53248\n"
"v_add_f32 v123,v123,v203\n"
MF "v[84:87],a[172:175],a[240:243],v[84:87]\n"
TR "v[182:183],v0 offset:55296\n"
"v_add_f32 v124,v124,v204\n"
MF "v[84:87],a[212:215],a[244:247],v[84:87]\n"
"ds_read2st64_b32 v[194:195],v13 offset0:226 offset1:227\n"
"v_add_f32 v125,v125,v205\n"
MF "v[76:79],a[208:211],a[244:247],v[76:79]\n"
TR "v[184:185],v0 offset:57344\n"
"v_add_f32 v126,v126,v206\n"
MF "v[76:79],a[168:171],a[240:243],v[76:79]\n"
TR "v[186:187],v0 offset:59392\n"
"v_add_f32 v127,v127,v207\n"
MF "v[76:79],a[128:131],v[188:191],v[76:79]\n"
"ds_read2st64_b32 v[196:197],v13 offset0:228 offset1:229\n"
CV "v120,v120,v121\n"
MF "v[68:71],a[124:127],v[188:191],v[68:71]\n"
"ds_read2st64_b32 v[198:199],v13 offset0:230 offset1:231\n"
CV "v121,v122,v123\n"
MF "v[68:71],a[164:167],a[240:243],v[68:71]\n"
"s_add_u32 s87,s90,3072\n"
CV "v122,v124,v125\n"
CV "v123,v126,v127\n"
MF "v[68:71],a[204:207],a[244:247],v[68:71]\n"
MF "v[60:63],a[200:203],a[244:247],v[60:63]\n"
"buffer_store_dwordx4 v[120:123],v9,s[40:43],s87 offen sc1\n"
MF "v[60:63],a[160:163],a[240:243],v[60:63]\n"
MF "v[60:63],a[120:123],v[188:191],v[60:63]\n"
"s_waitcnt lgkmcnt(2)\n"
MF "v[16:19],a[20:23],v[176:179],v[16:19]\n"
"s_waitcnt lgkmcnt(0)\n"
TR "v[188:189],v1 offset:49152\n"
"v_add_f32 v128,v128,v192\n"
MF "v[16:19],a[60:63],v[180:183],v[16:19]\n"
TR "v[190:191],v1 offset:51200\n"
"v_add_f32 v129,v129,v193\n"
MF "v[16:19],a[100:103],v[184:187],v[16:19]\n"
"ds_read2st64_b32 v[200:201],v13 offset0:0 offset1:1\n"
"v_add_f32 v130,v130,v194\n"
MF "v[24:27],a[104:107],v[184:187],v[24:27]\n"
TR "a[240:241],v1 offset:53248\n"
"v_add_f32 v131,v131,v195\n"
MF "v[24:27],a[64:67],v[180:183],v[24:27]\n"
TR "a[242:243],v1 offset:55296\n"
"v_add_f32 v132,v132,v196\n"
MF "v[24:27],a[24:27],v[176:179],v[24:27]\n"
"ds_read2st64_b32 v[202:203],v13 offset0:2 offset1:3\n"
"v_add_f32 v133,v133,v197\n"
MF "v[32:35],a[28:31],v[176:179],v[32:35]\n"
TR "a[244:245],v1 offset:57344\n"
"v_add_f32 v134,v134,v198\n"
MF "v[32:35],a[68:71],v[180:183],v[32:35]\n"
TR "a[246:247],v1 offset:59392\n"
"v_add_f32 v135,v135,v199\n"
MF "v[32:35],a[108:111],v[184:187],v[32:35]\n"
"ds_read2st64_b32 v[204:205],v13 offset0:4 offset1:5\n"
CV "v128,v128,v129\n"
MF "v[40:43],a[112:115],v[184:187],v[40:43]\n"
"ds_read2st64_b32 v[206:207],v13 offset0:6 offset1:7\n"
CV "v129,v130,v131\n"
MF "v[40:43],a[72:75],v[180:183],v[40:43]\n"
"s_add_u32 s87,s90,4096\n"
CV "v130,v132,v133\n"
CV "v131,v134,v135\n"
MF "v[40:43],a[32:35],v[176:179],v[40:43]\n"
MF "v[48:51],a[36:39],v[176:179],v[48:51]\n"
"buffer_store_dwordx4 v[128:131],v9,s[40:43],s87 offen sc1\n"
MF "v[48:51],a[76:79],v[180:183],v[48:51]\n"
MF "v[48:51],a[116:119],v[184:187],v[48:51]\n"
"s_waitcnt lgkmcnt(2)\n"
MF "v[52:55],a[116:119],a[244:247],v[52:55]\n"
"s_waitcnt lgkmcnt(0)\n"
TR "v[176:177],v2 offset:49152\n"
"v_add_f32 v136,v136,v200\n"
MF "v[52:55],a[76:79],a[240:243],v[52:55]\n"
TR "v[178:179],v2 offset:51200\n"
"v_add_f32 v137,v137,v201\n"
MF "v[52:55],a[36:39],v[188:191],v[52:55]\n"
"ds_read2st64_b32 v[192:193],v13 offset0:8 offset1:9\n"
"v_add_f32 v138,v138,v202\n"
MF "v[44:47],a[32:35],v[188:191],v[44:47]\n"
TR "v[180:181],v2 offset:53248\n"
"v_add_f32 v139,v139,v203\n"
MF "v[44:47],a[72:75],a[240:243],v[44:47]\n"
TR "v[182:183],v2 offset:55296\n"
"v_add_f32 v140,v140,v204\n"
MF "v[44:47],a[112:115],a[244:247],v[44:47]\n"
"ds_read2st64_b32 v[194:195],v13 offset0:10 offset1:11\n"
"v_add_f32 v141,v141,v205\n"
MF "v[36:39],a[108:111],a[244:247],v[36:39]\n"
TR "v[184:185],v2 offset:57344\n"
"v_add_f32 v142,v142,v206\n"
MF "v[36:39],a[68:71],a[240:243],v[36:39]\n"
TR "v[186:187],v2 offset:59392\n"
"v_add_f32 v143,v143,v207\n"
MF "v[36:39],a[28:31],v[188:191],v[36:39]\n"
"ds_read2st64_b32 v[196:197],v13 offset0:12 offset1:13\n"
CV "v136,v136,v137\n"
MF "v[28:31],a[24:27],v[188:191],v[28:31]\n"
"ds_read2st64_b32 v[198:199],v13 offset0:14 offset1:15\n"
CV "v137,v138,v139\n"
MF "v[28:31],a[64:67],a[240:243],v[28:31]\n"
"s_add_u32 s87,s90,10240\n"
CV "v138,v140,v141\n"
CV "v139,v142,v143\n"
MF "v[28:31],a[104:107],a[244:247],v[28:31]\n"
MF "v[20:23],a[100:103],a[244:247],v[20:23]\n"
"buffer_store_dwordx4 v[136:139],v9,s[40:43],s87 offen sc1\n"
MF "v[20:23],a[60:63],a[240:243],v[20:23]\n"
"s_waitcnt vmcnt(7)\n"
MF "v[20:23],a[20:23],v[188:191],v[20:23]\n"
"v_mov_b32 v236,v228\n"
"v_mov_b32 v237,v229\n"
MF "v[56:59],a[252:255],v[208:211],v[56:59]\n"
MF "v[64:67],a[252:255],v[212:215],v[64:67]\n"
MF "v[72:75],a[252:255],v[216:219],v[72:75]\n"
MF "v[80:83],a[252:255],v[220:223],v[80:83]\n"
MF "v[88:91],a[252:255],v[224:227],v[88:91]\n"
MF "v[60:63],a[248:251],v[208:211],v[60:63]\n"
MF "v[68:71],a[248:251],v[212:215],v[68:71]\n"
MF "v[76:79],a[248:251],v[216:219],v[76:79]\n"
MF "v[84:87],a[248:251],v[220:223],v[84:87]\n"
MF "v[92:95],a[248:251],v[224:227],v[92:95]\n"
MF "v[248:251],v[230:233],a[250:253],v[248:251]\n"
MF "v[244:247],v[234:237],a[250:253],v[244:247]\n"
"s_waitcnt lgkmcnt(2)\n"
MF "v[56:59],a[20:23],v[176:179],v[56:59]\n"
"s_waitcnt lgkmcnt(0)\n"
TR "v[188:189],v3 offset:49152\n"
"v_add_f32 v144,v144,v192\n"
MF "v[56:59],a[60:63],v[180:183],v[56:59]\n"
TR "v[190:191],v3 offset:51200\n"
"v_add_f32 v145,v145,v193\n"
MF "v[56:59],a[100:103],v[184:187],v[56:59]\n"
"ds_read2st64_b32 v[200:201],v13 offset0:16 offset1:17\n"
"v_add_f32 v146,v146,v194\n"
MF "v[64:67],a[104:107],v[184:187],v[64:67]\n"
TR "a[240:241],v3 offset:53248\n"
"v_add_f32 v147,v147,v195\n"
MF "v[64:67],a[64:67],v[180:183],v[64:67]\n"
TR "a[242:243],v3 offset:55296\n"
"v_add_f32 v148,v148,v196\n"
MF "v[64:67],a[24:27],v[176:179],v[64:67]\n"
"ds_read2st64_b32 v[202:203],v13 offset0:18 offset1:19\n"
"v_add_f32 v149,v149,v197\n"
MF "v[72:75],a[28:31],v[176:179],v[72:75]\n"
TR "a[244:245],v3 offset:57344\n"
"v_add_f32 v150,v150,v198\n"
MF "v[72:75],a[68:71],v[180:183],v[72:75]\n"
TR "a[246:247],v3 offset:59392\n"
"v_add_f32 v151,v151,v199\n"
MF "v[72:75],a[108:111],v[184:187],v[72:75]\n"
"ds_read2st64_b32 v[204:205],v13 offset0:20 offset1:21\n"
CV "v144,v144,v145\n"
MF "v[80:83],a[112:115],v[184:187],v[80:83]\n"
"ds_read2st64_b32 v[206:207],v13 offset0:22 offset1:23\n"
CV "v145,v146,v147\n"
MF "v[80:83],a[72:75],v[180:183],v[80:83]\n"
"s_add_u32 s87,s90,11264\n"
CV "v146,v148,v149\n"
CV "v147,v150,v151\n"
MF "v[80:83],a[32:35],v[176:179],v[80:83]\n"
"v_sub_u32 v10,v10,v11\n"
MF "v[88:91],a[36:39],v[176:179],v[88:91]\n"
"buffer_store_dwordx4 v[144:147],v9,s[40:43],s87 offen sc1\n"
MF "v[88:91],a[76:79],v[180:183],v[88:91]\n"
"s_waitcnt lgkmcnt(0)\n"
MF "v[88:91],a[116:119],v[184:187],v[88:91]\n"
"s_barrier\n"
MF "v[92:95],a[116:119],a[244:247],v[92:95]\n"
TR "v[176:177],v4\n"
"v_add_f32 v152,v152,v200\n"
MF "v[92:95],a[76:79],a[240:243],v[92:95]\n"
TR "v[178:179],v4 offset:2048\n"
"v_add_f32 v153,v153,v201\n"
MF "v[92:95],a[36:39],v[188:191],v[92:95]\n"
"ds_read2st64_b32 v[192:193],v13 offset0:24 offset1:25\n"
"v_add_f32 v154,v154,v202\n"
MF "v[84:87],a[32:35],v[188:191],v[84:87]\n"
TR "v[180:181],v4 offset:4096\n"
"v_add_f32 v155,v155,v203\n"
MF "v[84:87],a[72:75],a[240:243],v[84:87]\n"
TR "v[182:183],v4 offset:6144\n"
"v_add_f32 v156,v156,v204\n"
MF "v[84:87],a[112:115],a[244:247],v[84:87]\n"
"ds_read2st64_b32 v[194:195],v13 offset0:26 offset1:27\n"
"v_add_f32 v157,v157,v205\n"
MF "v[76:79],a[108:111],a[244:247],v[76:79]\n"
TR "v[184:185],v4 offset:8192\n"
"v_add_f32 v158,v158,v206\n"
MF "v[76:79],a[68:71],a[240:243],v[76:79]\n"
TR "v[186:187],v4 offset:10240\n"
"v_add_f32 v159,v159,v207\n"
MF "v[76:79],a[28:31],v[188:191],v[76:79]\n"
"ds_read2st64_b32 v[196:197],v13 offset0:28 offset1:29\n"
CV "v152,v152,v153\n"
MF "v[68:71],a[24:27],v[188:191],v[68:71]\n"
"ds_read2st64_b32 v[198:199],v13 offset0:30 offset1:31\n"
CV "v153,v154,v155\n"
MF "v[68:71],a[64:67],a[240:243],v[68:71]\n"
"s_add_u32 s87,s90,12288\n"
CV "v154,v156,v157\n"
CV "v155,v158,v159\n"
MF "v[68:71],a[104:107],a[244:247],v[68:71]\n"
".Lnf8:\n"
"v_readlane_b32 s58,v10,s67\n"
"s_cmp_lt_u32 s58,s85\n"
"s_cbranch_scc1 .Lsp8\n"
".Lnb8:\n"
MF "v[60:63],a[100:103],a[244:247],v[60:63]\n"
"buffer_store_dwordx4 v[152:155],v9,s[40:43],s87 offen sc1\n"
MF "v[60:63],a[60:63],a[240:243],v[60:63]\n"
"s_add_u32 s70,s70,20480\n"
"s_add_u32 s2,s2,204800\n"
MF "v[60:63],a[20:23],v[188:191],v[60:63]\n"
"s_waitcnt lgkmcnt(2)\n"
LD "v[228:231],v9,s[40:43],s2 offen sc1\n"
MF "v[16:19],a[140:143],v[176:179],v[16:19]\n"
"s_waitcnt lgkmcnt(0)\n"
TR "v[188:189],v5\n"
"v_add_f32 v160,v160,v192\n"
MF "v[16:19],a[180:183],v[180:183],v[16:19]\n"
TR "v[190:191],v5 offset:2048\n"
"v_add_f32 v161,v161,v193\n"
MF "v[16:19],a[220:223],v[184:187],v[16:19]\n"
"ds_read2st64_b32 v[200:201],v13 offset0:32 offset1:33\n"
"v_add_f32 v162,v162,v194\n"
MF "v[24:27],a[224:227],v[184:187],v[24:27]\n"
TR "a[240:241],v5 offset:4096\n"
"v_add_f32 v163,v163,v195\n"
MF "v[24:27],a[184:187],v[180:183],v[24:27]\n"
TR "a[242:243],v5 offset:6144\n"
"v_add_f32 v164,v164,v196\n"
MF "v[24:27],a[144:147],v[176:179],v[24:27]\n"
"ds_read2st64_b32 v[202:203],v13 offset0:34 offset1:35\n"
"v_add_f32 v165,v165,v197\n"
MF "v[32:35],a[148:151],v[176:179],v[32:35]\n"
TR "a[244:245],v5 offset:8192\n"
"v_add_f32 v166,v166,v198\n"
MF "v[32:35],a[188:191],v[180:183],v[32:35]\n"
TR "a[246:247],v5 offset:10240\n"
"v_add_f32 v167,v167,v199\n"
MF "v[32:35],a[228:231],v[184:187],v[32:35]\n"
"ds_read2st64_b32 v[204:205],v13 offset0:36 offset1:37\n"
CV "v160,v160,v161\n"
MF "v[40:43],a[232:235],v[184:187],v[40:43]\n"
"s_add_u32 s2,s2,204800\n"
"ds_read2st64_b32 v[206:207],v13 offset0:38 offset1:39\n"
CV "v161,v162,v163\n"
MF "v[40:43],a[192:195],v[180:183],v[40:43]\n"
"s_add_u32 s87,s90,13312\n"
CV "v162,v164,v165\n"
CV "v163,v166,v167\n"
MF "v[40:43],a[152:155],v[176:179],v[40:43]\n"
LD "v[232:235],v9,s[40:43],s2 offen sc1\n"
MF "v[48:51],a[156:159],v[176:179],v[48:51]\n"
"buffer_store_dwordx4 v[160:163],v9,s[40:43],s87 offen sc1\n"
MF "v[48:51],a[196:199],v[180:183],v[48:51]\n"
MF "v[48:51],a[236:239],v[184:187],v[48:51]\n"
"s_waitcnt lgkmcnt(2)\n"
MF "v[52:55],a[236:239],a[244:247],v[52:55]\n"
"s_waitcnt lgkmcnt(0)\n"
TR "v[176:177],v6\n"
"v_add_f32 v168,v168,v200\n"
MF "v[52:55],a[196:199],a[240:243],v[52:55]\n"
TR "v[178:179],v6 offset:2048\n"
"v_add_f32 v169,v169,v201\n"
MF "v[52:55],a[156:159],v[188:191],v[52:55]\n"
"v_add_f32 v170,v170,v202\n"
"v_add_f32 v171,v171,v203\n"
MF "v[44:47],a[152:155],v[188:191],v[44:47]\n"
TR "v[180:181],v6 offset:4096\n"
"v_add_f32 v172,v172,v204\n"
MF "v[44:47],a[192:195],a[240:243],v[44:47]\n"
TR "v[182:183],v6 offset:6144\n"
"v_add_f32 v173,v173,v205\n"
MF "v[44:47],a[232:235],a[244:247],v[44:47]\n"
"v_add_f32 v174,v174,v206\n"
"v_add_f32 v175,v175,v207\n"
MF "v[36:39],a[228:231],a[244:247],v[36:39]\n"
TR "v[184:185],v6 offset:8192\n"
CV "v168,v168,v169\n"
MF "v[36:39],a[188:191],a[240:243],v[36:39]\n"
TR "v[186:187],v6 offset:10240\n"
CV "v169,v170,v171\n"
MF "v[36:39],a[148:151],v[188:191],v[36:39]\n"
"s_add_u32 s87,s90,14336\n"
CV "v170,v172,v173\n"
CV "v171,v174,v175\n"
MF "v[28:31],a[144:147],v[188:191],v[28:31]\n"
LD "v[208:211],v9,s[40:43],s82 offen sc1\n"
MF "v[28:31],a[184:187],a[240:243],v[28:31]\n"
MF "v[28:31],a[224:227],a[244:247],v[28:31]\n"
MF "v[20:23],a[220:223],a[244:247],v[20:23]\n"
"buffer_store_dwordx4 v[168:171],v9,s[40:43],s87 offen sc1\n"
MF "v[20:23],a[180:183],a[240:243],v[20:23]\n"
"s_mov_b32 s90,s91\n"
"s_add_u32 s91,s91,20480\n"
MF "v[20:23],a[140:143],v[188:191],v[20:23]\n"
"s_waitcnt lgkmcnt(0)\n"
MF "v[56:59],a[140:143],v[176:179],v[56:59]\n"
TR "v[188:189],v7\n"
MF "v[56:59],a[180:183],v[180:183],v[56:59]\n"
TR "v[190:191],v7 offset:2048\n"
MF "v[56:59],a[220:223],v[184:187],v[56:59]\n"
LD "v[212:215],v9,s[40:43],s82 offen offset:1024 sc1\n"
MF "v[64:67],a[224:227],v[184:187],v[64:67]\n"
TR "a[240:241],v7 offset:4096\n"
MF "v[64:67],a[184:187],v[180:183],v[64:67]\n"
TR "a[242:243],v7 offset:6144\n"
MF "v[64:67],a[144:147],v[176:179],v[64:67]\n"
MF "v[72:75],a[148:151],v[176:179],v[72:75]\n"
TR "a[244:245],v7 offset:8192\n"
MF "v[72:75],a[188:191],v[180:183],v[72:75]\n"
TR "a[246:247],v7 offset:10240\n"
MF "v[72:75],a[228:231],v[184:187],v[72:75]\n"
MF "v[80:83],a[232:235],v[184:187],v[80:83]\n"
MF "v[80:83],a[192:195],v[180:183],v[80:83]\n"
LD "v[216:219],v9,s[40:43],s82 offen offset:2048 sc1\n"
MF "v[80:83],a[152:155],v[176:179],v[80:83]\n"
MF "v[88:91],a[156:159],v[176:179],v[88:91]\n"
MF "v[88:91],a[196:199],v[180:183],v[88:91]\n"
"s_waitcnt lgkmcnt(0)\n"
MF "v[88:91],a[236:239],v[184:187],v[88:91]\n"
"s_barrier\n"
MF "v[92:95],a[236:239],a[244:247],v[92:95]\n"
TR "v[176:177],v0 offset:24576\n"
MF "v[92:95],a[196:199],a[240:243],v[92:95]\n"
"s_add_u32 s85,s85,1\n"
TR "v[178:179],v0 offset:26624\n"
MF "v[92:95],a[156:159],v[188:191],v[92:95]\n"
MF "v[84:87],a[152:155],v[188:191],v[84:87]\n"
TR "v[180:181],v0 offset:28672\n"
MF "v[84:87],a[192:195],a[240:243],v[84:87]\n"
TR "v[182:183],v0 offset:30720\n"
MF "v[84:87],a[232:235],a[244:247],v[84:87]\n"
LD "v[220:223],v9,s[40:43],s82 offen offset:3072 sc1\n"
MF "v[76:79],a[228:231],a[244:247],v[76:79]\n"
TR "v[184:185],v0 offset:32768\n"
MF "v[76:79],a[188:191],a[240:243],v[76:79]\n"
TR "v[186:187],v0 offset:34816\n"
MF "v[76:79],a[148:151],v[188:191],v[76:79]\n"
MF "v[68:71],a[144:147],v[188:191],v[68:71]\n"
MF "v[68:71],a[184:187],a[240:243],v[68:71]\n"
MF "v[68:71],a[224:227],a[244:247],v[68:71]\n"
MF "v[60:63],a[220:223],a[244:247],v[60:63]\n"
MF "v[60:63],a[180:183],a[240:243],v[60:63]\n"
MF "v[60:63],a[140:143],v[188:191],v[60:63]\n"
"s_waitcnt lgkmcnt(0)\n"
"s_cmp_lg_u32 s85,9\n"
"s_cbranch_scc1 .Lpl19\n"
".Lplast1:\n"
MF "v[96:99],v[176:179],a[0:3],v[252:255]\n"
"s_add_u32 m0,s79,73728\n"
TR "v[188:189],v1 offset:24576\n"
TR "v[190:191],v1 offset:26624\n"
MF "v[96:99],v[180:183],a[40:43],v[96:99]\n"
"s_nop 0\n"
TR "a[240:241],v1 offset:28672\n"
TR "a[242:243],v1 offset:30720\n"
MF "v[96:99],v[184:187],a[80:83],v[96:99]\n"
"s_add_u32 s82,s82,4096\n"
TR "a[244:245],v1 offset:32768\n"
TR "a[246:247],v1 offset:34816\n"
MF "v[104:107],v[184:187],a[84:87],v[252:255]\n"
LD "v[224:227],v9,s[40:43],s82 offen sc1\n"
MF "v[104:107],v[180:183],a[44:47],v[104:107]\n"
"s_nop 0\n"
"ds_write_addtid_b32 v56\n"
"ds_write_addtid_b32 v57 offset:256\n"
MF "v[104:107],v[176:179],a[4:7],v[104:107]\n"
"s_nop 0\n"
"ds_write_addtid_b32 v58 offset:512\n"
"ds_write_addtid_b32 v59 offset:768\n"
MF "v[112:115],v[176:179],a[8:11],v[252:255]\n"
"s_nop 0\n"
"ds_write_addtid_b32 v60 offset:1024\n"
"ds_write_addtid_b32 v61 offset:1280\n"
MF "v[112:115],v[180:183],a[48:51],v[112:115]\n"
"s_nop 0\n"
"ds_write_addtid_b32 v62 offset:1536\n"
"ds_write_addtid_b32 v63 offset:1792\n"
MF "v[112:115],v[184:187],a[88:91],v[112:115]\n"
"s_nop 0\n"
"ds_write_addtid_b32 v64 offset:2048\n"
"ds_write_addtid_b32 v65 offset:2304\n"
MF "v[120:123],v[184:187],a[92:95],v[252:255]\n"
"s_nop 0\n"
"ds_write_addtid_b32 v66 offset:2560\n"
"ds_write_addtid_b32 v67 offset:2816\n"
MF "v[120:123],v[180:183],a[52:55],v[120:123]\n"
"s_nop 0\n"
"ds_write_addtid_b32 v68 offset:3072\n"
"ds_write_addtid_b32 v69 offset:3328\n"
MF "v[120:123],v[176:179],a[12:15],v[120:123]\n"
"s_nop 0\n"
"ds_write_addtid_b32 v70 offset:3584\n"
"ds_write_addtid_b32 v71 offset:3840\n"
MF "v[128:131],v[176:179],a[16:19],v[252:255]\n"
"s_nop 0\n"
"ds_write_addtid_b32 v72 offset:4096\n"
"ds_write_addtid_b32 v73 offset:4352\n"
MF "v[128:131],v[180:183],a[56:59],v[128:131]\n"
"s_nop 0\n"
"ds_write_addtid_b32 v74 offset:4608\n"
"ds_write_addtid_b32 v75 offset:4864\n"
MF "v[128:131],v[184:187],a[96:99],v[128:131]\n"
"s_waitcnt lgkmcnt(15)\n"
"ds_write_addtid_b32 v76 offset:5120\n"
MF "v[132:135],a[244:247],a[96:99],v[252:255]\n"
"s_nop 0\n"
TR "v[176:177],v2 offset:24576\n"
TR "v[178:179],v2 offset:26624\n"
MF "v[132:135],a[240:243],a[56:59],v[132:135]\n"
"s_nop 0\n"
TR "v[180:181],v2 offset:28672\n"
TR "v[182:183],v2 offset:30720\n"
MF "v[132:135],v[188:191],a[16:19],v[132:135]\n"
"s_nop 0\n"
TR "v[184:185],v2 offset:32768\n"
TR "v[186:187],v2 offset:34816\n"
MF "v[124:127],v[188:191],a[12:15],v[252:255]\n"
"s_nop 0\n"
"ds_write_addtid_b32 v77 offset:5376\n"
"ds_write_addtid_b32 v78 offset:5632\n"
MF "v[124:127],a[240:243],a[52:55],v[124:127]\n"
"s_nop 0\n"
"ds_write_addtid_b32 v79 offset:5888\n"
"ds_write_addtid_b32 v80 offset:6144\n"
MF "v[124:127],a[244:247],a[92:95],v[124:127]\n"
"s_nop 0\n"
"ds_write_addtid_b32 v81 offset:6400\n"
"ds_write_addtid_b32 v82 offset:6656\n"
MF "v[116:119],a[244:247],a[88:91],v[252:255]\n"
"s_nop 0\n"
"ds_write_addtid_b32 v83 offset:6912\n"
"ds_write_addtid_b32 v84 offset:7168\n"
MF "v[116:119],a[240:243],a[48:51],v[116:119]\n"
"s_nop 0\n"
"ds_write_addtid_b32 v85 offset:7424\n"
"ds_write_addtid_b32 v86 offset:7680\n"
MF "v[116:119],v[188:191],a[8:11],v[116:119]\n"
"s_nop 0\n"
"ds_write_addtid_b32 v87 offset:7936\n"
"ds_write_addtid_b32 v88 offset:8192\n"
MF "v[108:111],v[188:191],a[4:7],v[252:255]\n"
"s_nop 0\n"
"ds_write_addtid_b32 v89 offset:8448\n"
"ds_write_addtid_b32 v90 offset:8704\n"
MF "v[108:111],a[240:243],a[44:47],v[108:111]\n"
"s_nop 0\n"
"ds_write_addtid_b32 v91 offset:8960\n"
"ds_write_addtid_b32 v92 offset:9216\n"
MF "v[108:111],a[244:247],a[84:87],v[108:111]\n"
"s_nop 0\n"
"ds_write_addtid_b32 v93 offset:9472\n"
"ds_write_addtid_b32 v94 offset:9728\n"
MF "v[100:103],a[244:247],a[80:83],v[252:255]\n"
"ds_write_addtid_b32 v95 offset:9984\n"
MF "v[100:103],a[240:243],a[40:43],v[100:103]\n"
MF "v[100:103],v[188:191],a[0:3],v[100:103]\n"
"s_waitcnt lgkmcnt(15)\n"
MF "v[136:139],v[176:179],a[0:3],v[252:255]\n"
"s_nop 0\n"
TR "v[188:189],v3 offset:24576\n"
TR "v[190:191],v3 offset:26624\n"
MF "v[136:139],v[180:183],a[40:43],v[136:139]\n"
"s_nop 0\n"
TR "a[240:241],v3 offset:28672\n"
TR "a[242:243],v3 offset:30720\n"
MF "v[136:139],v[184:187],a[80:83],v[136:139]\n"
"s_nop 0\n"
TR "a[244:245],v3 offset:32768\n"
TR "a[246:247],v3 offset:34816\n"
"s_waitcnt lgkmcnt(0)\n"
MF "v[144:147],v[184:187],a[84:87],v[252:255]\n"
"s_barrier\n"
MF "v[144:147],v[180:183],a[44:47],v[144:147]\n"
"ds_read2st64_b32 v[56:57],v13 offset0:192 offset1:193\n"
MF "v[144:147],v[176:179],a[4:7],v[144:147]\n"
"ds_read2st64_b32 v[58:59],v13 offset0:194 offset1:195\n"
MF "v[152:155],v[176:179],a[8:11],v[252:255]\n"
"ds_read2st64_b32 v[60:61],v13 offset0:196 offset1:197\n"
MF "v[152:155],v[180:183],a[48:51],v[152:155]\n"
"ds_read2st64_b32 v[62:63],v13 offset0:198 offset1:199\n"
MF "v[152:155],v[184:187],a[88:91],v[152:155]\n"
"ds_read2st64_b32 v[64:65],v13 offset0:200 offset1:201\n"
MF "v[160:163],v[184:187],a[92:95],v[252:255]\n"
"ds_read2st64_b32 v[66:67],v13 offset0:202 offset1:203\n"
MF "v[160:163],v[180:183],a[52:55],v[160:163]\n"
"ds_read2st64_b32 v[68:69],v13 offset0:204 offset1:205\n"
MF "v[160:163],v[176:179],a[12:15],v[160:163]\n"
"ds_read2st64_b32 v[70:71],v13 offset0:206 offset1:207\n"
MF "v[168:171],v[176:179],a[16:19],v[252:255]\n"
"s_waitcnt lgkmcnt(4)\n"
"ds_read2st64_b32 v[72:73],v13 offset0:208 offset1:209\n"
"v_add_f32 v16,v16,v56\n"
MF "v[168:171],v[180:183],a[56:59],v[168:171]\n"
"ds_read2st64_b32 v[74:75],v13 offset0:210 offset1:211\n"
"s_waitcnt lgkmcnt(0)\n"
MF "v[168:171],v[184:187],a[96:99],v[168:171]\n"
"s_barrier\n"
MF "v[172:175],a[244:247],a[96:99],v[252:255]\n"
"s_nop 0\n"
TR "v[176:177],v0\n"
TR "v[178:179],v0 offset:2048\n"
MF "v[172:175],a[240:243],a[56:59],v[172:175]\n"
"s_nop 0\n"
TR "v[180:181],v0 offset:4096\n"
TR "v[182:183],v0 offset:6144\n"
MF "v[172:175],v[188:191],a[16:19],v[172:175]\n"
"s_nop 0\n"
TR "v[184:185],v0 offset:8192\n"
TR "v[186:187],v0 offset:10240\n"
MF "v[164:167],v[188:191],a[12:15],v[252:255]\n"
"ds_read2st64_b32 v[76:77],v13 offset0:212 offset1:213\n"
"v_add_f32 v17,v17,v57\n"
MF "v[164:167],a[240:243],a[52:55],v[164:167]\n"
"ds_read2st64_b32 v[78:79],v13 offset0:214 offset1:215\n"
"v_add_f32 v18,v18,v58\n"
MF "v[164:167],a[244:247],a[92:95],v[164:167]\n"
"ds_read2st64_b32 v[80:81],v13 offset0:216 offset1:217\n"
"v_add_f32 v19,v19,v59\n"
MF "v[156:159],a[244:247],a[88:91],v[252:255]\n"
"ds_read2st64_b32 v[82:83],v13 offset0:218 offset1:219\n"
"v_add_f32 v20,v20,v60\n"
MF "v[156:159],a[240:243],a[48:51],v[156:159]\n"
"ds_read2st64_b32 v[84:85],v13 offset0:220 offset1:221\n"
"v_add_f32 v21,v21,v61\n"
MF "v[156:159],v[188:191],a[8:11],v[156:159]\n"
"ds_read2st64_b32 v[86:87],v13 offset0:222 offset1:223\n"
"v_add_f32 v22,v22,v62\n"
MF "v[148:151],v[188:191],a[4:7],v[252:255]\n"
".Lpg37h:\n"
"s_cbranch_execz .Lisr4\n"
"s_waitcnt vmcnt(4)\n"
"v_mov_b32 v14,s76\n"
"s_add_u32 s76,s76,1\n"
MF "v[148:151],a[240:243],a[44:47],v[148:151]\n"
"buffer_store_dword v14,v15,s[40:43],s94 offen sc1\n"
MF "v[148:151],a[244:247],a[84:87],v[148:151]\n"
"ds_read2st64_b32 v[88:89],v13 offset0:224 offset1:225\n"
"v_add_f32 v23,v23,v63\n"
MF "v[140:143],a[244:247],a[80:83],v[252:255]\n"
"ds_read2st64_b32 v[90:91],v13 offset0:226 offset1:227\n"
CV "v16,v16,v17\n"
MF "v[140:143],a[240:243],a[40:43],v[140:143]\n"
"ds_read2st64_b32 v[92:93],v13 offset0:228 offset1:229\n"
CV "v17,v18,v19\n"
MF "v[140:143],v[188:191],a[0:3],v[140:143]\n"
"s_waitcnt lgkmcnt(9)\n"
"ds_read2st64_b32 v[94:95],v13 offset0:230 offset1:231\n"
CV "v18,v20,v21\n"
MF "v[96:99],v[176:179],a[120:123],v[96:99]\n"
"s_nop 0\n"
TR "v[188:189],v1\n"
TR "v[190:191],v1 offset:2048\n"
MF "v[96:99],v[180:183],a[160:163],v[96:99]\n"
"s_nop 0\n"
TR "a[240:241],v1 offset:4096\n"
TR "a[242:243],v1 offset:6144\n"
MF "v[96:99],v[184:187],a[200:203],v[96:99]\n"
"s_nop 0\n"
TR "a[244:245],v1 offset:8192\n"
TR "a[246:247],v1 offset:10240\n"
MF "v[104:107],v[184:187],a[204:207],v[104:107]\n"
"s_add_u32 s87,s90,0\n"
CV "v19,v22,v23\n"
MF "v[104:107],v[180:183],a[164:167],v[104:107]\n"
"buffer_store_dwordx4 v[16:19],v9,s[40:43],s87 offen sc1\n"
MF "v[104:107],v[176:179],a[124:127],v[104:107]\n"
"v_add_f32 v24,v24,v64\n"
"v_add_f32 v25,v25,v65\n"
MF "v[112:115],v[176:179],a[128:131],v[112:115]\n"
"v_add_f32 v26,v26,v66\n"
"v_add_f32 v27,v27,v67\n"
MF "v[112:115],v[180:183],a[168:171],v[112:115]\n"
"v_add_f32 v28,v28,v68\n"
"v_add_f32 v29,v29,v69\n"
MF "v[112:115],v[184:187],a[208:211],v[112:115]\n"
"v_add_f32 v30,v30,v70\n"
"v_add_f32 v31,v31,v71\n"
MF "v[120:123],v[184:187],a[212:215],v[120:123]\n"
CV "v24,v24,v25\n"
CV "v25,v26,v27\n"
MF "v[120:123],v[180:183],a[172:175],v[120:123]\n"
"s_add_u32 s87,s90,1024\n"
CV "v26,v28,v29\n"
CV "v27,v30,v31\n"
MF "v[120:123],v[176:179],a[132:135],v[120:123]\n"
"s_waitcnt lgkmcnt(14)\n"
"buffer_store_dwordx4 v[24:27],v9,s[40:43],s87 offen sc1\n"
MF "v[128:131],v[176:179],a[136:139],v[128:131]\n"
"v_add_f32 v32,v32,v72\n"
"v_add_f32 v33,v33,v73\n"
MF "v[128:131],v[180:183],a[176:179],v[128:131]\n"
"v_add_f32 v34,v34,v74\n"
"v_add_f32 v35,v35,v75\n"
MF "v[128:131],v[184:187],a[216:219],v[128:131]\n"
"s_waitcnt lgkmcnt(0)\n"
"v_add_f32 v36,v36,v76\n"
"v_add_f32 v37,v37,v77\n"
MF "v[132:135],a[244:247],a[216:219],v[132:135]\n"
"s_nop 0\n"
TR "v[176:177],v2\n"
TR "v[178:179],v2 offset:2048\n"
MF "v[132:135],a[240:243],a[176:179],v[132:135]\n"
"s_nop 0\n"
TR "v[180:181],v2 offset:4096\n"
TR "v[182:183],v2 offset:6144\n"
MF "v[132:135],v[188:191],a[136:139],v[132:135]\n"
"s_nop 0\n"
TR "v[184:185],v2 offset:8192\n"
TR "v[186:187],v2 offset:10240\n"
MF "v[124:127],v[188:191],a[132:135],v[124:127]\n"
"v_add_f32 v38,v38,v78\n"
"v_add_f32 v39,v39,v79\n"
MF "v[124:127],a[240:243],a[172:175],v[124:127]\n"
CV "v32,v32,v33\n"
CV "v33,v34,v35\n"
MF "v[124:127],a[244:247],a[212:215],v[124:127]\n"
"s_add_u32 s87,s90,2048\n"
CV "v34,v36,v37\n"
CV "v35,v38,v39\n"
MF "v[116:119],a[244:247],a[208:211],v[116:119]\n"
"buffer_store_dwordx4 v[32:35],v9,s[40:43],s87 offen sc1\n"
MF "v[116:119],a[240:243],a[168:171],v[116:119]\n"
"v_add_f32 v40,v40,v80\n"
"v_add_f32 v41,v41,v81\n"
MF "v[116:119],v[188:191],a[128:131],v[116:119]\n"
"v_add_f32 v42,v42,v82\n"
"v_add_f32 v43,v43,v83\n"
MF "v[108:111],v[188:191],a[124:127],v[108:111]\n"
"v_add_f32 v44,v44,v84\n"
"v_add_f32 v45,v45,v85\n"
MF "v[108:111],a[240:243],a[164:167],v[108:111]\n"
"v_add_f32 v46,v46,v86\n"
"v_add_f32 v47,v47,v87\n"
MF "v[108:111],a[244:247],a[204:207],v[108:111]\n"
CV "v40,v40,v41\n"
CV "v41,v42,v43\n"
MF "v[100:103],a[244:247],a[200:203],v[100:103]\n"
"s_add_u32 s87,s90,3072\n"
CV "v42,v44,v45\n"
CV "v43,v46,v47\n"
MF "v[100:103],a[240:243],a[160:163],v[100:103]\n"
"buffer_store_dwordx4 v[40:43],v9,s[40:43],s87 offen sc1\n"
MF "v[100:103],v[188:191],a[120:123],v[100:103]\n"
"s_waitcnt lgkmcnt(0)\n"
"v_add_f32 v48,v48,v88\n"
"v_add_f32 v49,v49,v89\n"
MF "v[136:139],v[176:179],a[120:123],v[136:139]\n"
"s_nop 0\n"
TR "v[188:189],v3\n"
TR "v[190:191],v3 offset:2048\n"
MF "v[136:139],v[180:183],a[160:163],v[136:139]\n"
"s_nop 0\n"
TR "a[240:241],v3 offset:4096\n"
TR "a[242:243],v3 offset:6144\n"
MF "v[136:139],v[184:187],a[200:203],v[136:139]\n"
"s_nop 0\n"
TR "a[244:245],v3 offset:8192\n"
TR "a[246:247],v3 offset:10240\n"
MF "v[144:147],v[184:187],a[204:207],v[144:147]\n"
"v_add_f32 v50,v50,v90\n"
"v_add_f32 v51,v51,v91\n"
MF "v[144:147],v[180:183],a[164:167],v[144:147]\n"
"v_add_f32 v52,v52,v92\n"
"v_add_f32 v53,v53,v93\n"
MF "v[144:147],v[176:179],a[124:127],v[144:147]\n"
"v_add_f32 v54,v54,v94\n"
"v_add_f32 v55,v55,v95\n"
MF "v[152:155],v[176:179],a[128:131],v[152:155]\n"
CV "v48,v48,v49\n"
CV "v49,v50,v51\n"
MF "v[152:155],v[180:183],a[168:171],v[152:155]\n"
"s_add_u32 s87,s90,4096\n"
CV "v50,v52,v53\n"
CV "v51,v54,v55\n"
MF "v[152:155],v[184:187],a[208:211],v[152:155]\n"
"buffer_store_dwordx4 v[48:51],v9,s[40:43],s87 offen sc1\n"
MF "v[160:163],v[184:187],a[212:215],v[160:163]\n"
MF "v[160:163],v[180:183],a[172:175],v[160:163]\n"
MF "v[160:163],v[176:179],a[132:135],v[160:163]\n"
MF "v[168:171],v[176:179],a[136:139],v[168:171]\n"
MF "v[168:171],v[180:183],a[176:179],v[168:171]\n"
"s_waitcnt lgkmcnt(0)\n"
MF "v[168:171],v[184:187],a[216:219],v[168:171]\n"
"s_barrier\n"
MF "v[172:175],a[244:247],a[216:219],v[172:175]\n"
"s_nop 0\n"
TR "v[176:177],v0 offset:49152\n"
TR "v[178:179],v0 offset:51200\n"
MF "v[172:175],a[240:243],a[176:179],v[172:175]\n"
"s_nop 0\n"
TR "v[180:181],v0 offset:53248\n"
TR "v[182:183],v0 offset:55296\n"
MF "v[172:175],v[188:191],a[136:139],v[172:175]\n"
"s_nop 0\n"
TR "v[184:185],v0 offset:57344\n"
TR "v[186:187],v0 offset:59392\n"
MF "v[164:167],v[188:191],a[132:135],v[164:167]\n"
"buffer_load_dword v10,v12,s[40:43],s86 offen sc1\n"
MF "v[164:167],a[240:243],a[172:175],v[164:167]\n"
MF "v[164:167],a[244:247],a[212:215],v[164:167]\n"
LD "a[0:3],v9,s[40:43],s3 offen sc1\n"
MF "v[156:159],a[244:247],a[208:211],v[156:159]\n"
MF "v[156:159],a[240:243],a[168:171],v[156:159]\n"
MF "v[156:159],v[188:191],a[128:131],v[156:159]\n"
MF "v[148:151],v[188:191],a[124:127],v[148:151]\n"
MF "v[148:151],a[240:243],a[164:167],v[148:151]\n"
"s_add_u32 s3,s3,204800\n"
MF "v[148:151],a[244:247],a[204:207],v[148:151]\n"
LD "a[4:7],v9,s[40:43],s3 offen sc1\n"
MF "v[140:143],a[244:247],a[200:203],v[140:143]\n"
MF "v[140:143],a[240:243],a[160:163],v[140:143]\n"
MF "v[140:143],v[188:191],a[120:123],v[140:143]\n"
"s_waitcnt lgkmcnt(0)\n"
MF "v[96:99],v[176:179],a[20:23],v[96:99]\n"
"s_nop 0\n"
TR "v[188:189],v1 offset:49152\n"
TR "v[190:191],v1 offset:51200\n"
MF "v[96:99],v[180:183],a[60:63],v[96:99]\n"
"s_nop 0\n"
TR "a[240:241],v1 offset:53248\n"
TR "a[242:243],v1 offset:55296\n"
MF "v[96:99],v[184:187],a[100:103],v[96:99]\n"
"s_add_u32 s3,s3,204800\n"
TR "a[244:245],v1 offset:57344\n"
TR "a[246:247],v1 offset:59392\n"
MF "v[104:107],v[184:187],a[104:107],v[104:107]\n"
LD "a[8:11],v9,s[40:43],s3 offen sc1\n"
MF "v[104:107],v[180:183],a[64:67],v[104:107]\n"
MF "v[104:107],v[176:179],a[24:27],v[104:107]\n"
"s_waitcnt vmcnt(10)\n"
"v_mov_b32 v236,v228\n"
"v_mov_b32 v237,v229\n"
MF "v[136:139],v[208:211],a[252:255],v[136:139]\n"
MF "v[144:147],v[212:215],a[252:255],v[144:147]\n"
MF "v[152:155],v[216:219],a[252:255],v[152:155]\n"
MF "v[160:163],v[220:223],a[252:255],v[160:163]\n"
MF "v[168:171],v[224:227],a[252:255],v[168:171]\n"
MF "v[140:143],v[208:211],a[248:251],v[140:143]\n"
MF "v[148:151],v[212:215],a[248:251],v[148:151]\n"
MF "v[156:159],v[216:219],a[248:251],v[156:159]\n"
MF "v[164:167],v[220:223],a[248:251],v[164:167]\n"
MF "v[172:175],v[224:227],a[248:251],v[172:175]\n"
MF "v[248:251],v[230:233],a[250:253],v[248:251]\n"
MF "v[244:247],v[234:237],a[250:253],v[244:247]\n"
MF "v[112:115],v[176:179],a[28:31],v[112:115]\n"
MF "v[112:115],v[180:183],a[68:71],v[112:115]\n"
"s_add_u32 s3,s3,204800\n"
MF "v[112:115],v[184:187],a[108:111],v[112:115]\n"
LD "a[12:15],v9,s[40:43],s3 offen sc1\n"
MF "v[120:123],v[184:187],a[112:115],v[120:123]\n"
"s_nop 1\n"
CV "v244,v244,v245\n"
CV "v245,v246,v247\n"
MF "v[120:123],v[180:183],a[72:75],v[120:123]\n"
"s_bfe_u32 s87,s22,0x30014\n"
CV "v246,v248,v249\n"
CV "v247,v250,v251\n"
MF "v[120:123],v[176:179],a[32:35],v[120:123]\n"
"s_lshl_b32 s87,s87,7\n"
"v_permlane16_swap_b32 v244,v246\n"
"v_permlane16_swap_b32 v245,v247\n"
MF "v[128:131],v[176:179],a[36:39],v[128:131]\n"
"buffer_store_dwordx4 v[244:247],v8,s[44:47],s0 offen sc1\n"
MF "v[128:131],v[180:183],a[76:79],v[128:131]\n"
"s_add_u32 s3,s3,204800\n"
MF "v[128:131],v[184:187],a[116:119],v[128:131]\n"
"s_waitcnt lgkmcnt(0)\n"
LD "a[40:43],v9,s[40:43],s3 offen sc1\n"
MF "v[132:135],a[244:247],a[116:119],v[132:135]\n"
"s_and_b32 s60,s56,1\n"
TR "v[176:177],v2 offset:49152\n"
TR "v[178:179],v2 offset:51200\n"
MF "v[132:135],a[240:243],a[76:79],v[132:135]\n"
"s_mul_i32 s60,s60,204800\n"
TR "v[180:181],v2 offset:53248\n"
TR "v[182:183],v2 offset:55296\n"
MF "v[132:135],v[188:191],a[36:39],v[132:135]\n"
"s_add_u32 s87,s87,s60\n"
TR "v[184:185],v2 offset:57344\n"
TR "v[186:187],v2 offset:59392\n"
MF "v[124:127],v[188:191],a[32:35],v[124:127]\n"
"s_mov_b32 s71,204800\n"
MF "v[124:127],a[240:243],a[72:75],v[124:127]\n"
"s_mov_b32 s66,204800\n"
MF "v[124:127],a[244:247],a[112:115],v[124:127]\n"
"s_add_u32 s72,s78,2\n"
MF "v[116:119],a[244:247],a[108:111],v[116:119]\n"
"s_add_u32 s3,s3,204800\n"
MF "v[116:119],a[240:243],a[68:71],v[116:119]\n"
LD "a[44:47],v9,s[40:43],s3 offen sc1\n"
MF "v[116:119],v[188:191],a[28:31],v[116:119]\n"
"s_and_b32 s72,s72,15\n"
MF "v[108:111],v[188:191],a[24:27],v[108:111]\n"
"s_add_u32 s16,s78,6\n"
MF "v[108:111],a[240:243],a[64:67],v[108:111]\n"
"s_and_b32 s16,s16,15\n"
MF "v[108:111],a[244:247],a[104:107],v[108:111]\n"
"s_add_u32 s17,s78,4\n"
MF "v[100:103],a[244:247],a[100:103],v[100:103]\n"
"s_and_b32 s17,s17,15\n"
MF "v[100:103],a[240:243],a[60:63],v[100:103]\n"
"s_add_u32 s3,s3,204800\n"
MF "v[100:103],v[188:191],a[20:23],v[100:103]\n"
"s_waitcnt lgkmcnt(0)\n"
LD "a[48:51],v9,s[40:43],s3 offen sc1\n"
MF "v[136:139],v[176:179],a[20:23],v[136:139]\n"
"s_bfe_u32 s60,s22,0x10014\n"
TR "v[188:189],v3 offset:49152\n"
TR "v[190:191],v3 offset:51200\n"
MF "v[136:139],v[180:183],a[60:63],v[136:139]\n"
"s_lshl_b32 s60,s60,5\n"
TR "a[240:241],v3 offset:53248\n"
TR "a[242:243],v3 offset:55296\n"
MF "v[136:139],v[184:187],a[100:103],v[136:139]\n"
"s_nop 0\n"
TR "a[244:245],v3 offset:57344\n"
TR "a[246:247],v3 offset:59392\n"
MF "v[144:147],v[184:187],a[104:107],v[144:147]\n"
"v_xor_b32 v242,s60,v8\n"
MF "v[144:147],v[180:183],a[64:67],v[144:147]\n"
"s_add_u32 s3,s3,204800\n"
"v_mov_b32 v14,s76\n"
"s_add_u32 s76,s76,1\n"
MF "v[144:147],v[176:179],a[24:27],v[144:147]\n"
LD "a[52:55],v9,s[40:43],s3 offen sc1\n"
MF "v[152:155],v[176:179],a[28:31],v[152:155]\n"
MF "v[152:155],v[180:183],a[68:71],v[152:155]\n"
MF "v[152:155],v[184:187],a[108:111],v[152:155]\n"
MF "v[160:163],v[184:187],a[112:115],v[160:163]\n"
MF "v[160:163],v[180:183],a[72:75],v[160:163]\n"
MF "v[160:163],v[176:179],a[32:35],v[160:163]\n"
MF "v[168:171],v[176:179],a[36:39],v[168:171]\n"
MF "v[168:171],v[180:183],a[76:79],v[168:171]\n"
"s_waitcnt vmcnt(9) lgkmcnt(0)\n"
MF "v[168:171],v[184:187],a[116:119],v[168:171]\n"
"s_barrier\n"
MF "v[172:175],a[244:247],a[116:119],v[172:175]\n"
"buffer_store_dword v14,v15,s[40:43],s94 offen sc1\n"
MF "v[172:175],a[240:243],a[76:79],v[172:175]\n"
"s_nop 0\n"
TR "v[176:177],v6\n"
TR "v[178:179],v6 offset:2048\n"
MF "v[172:175],v[188:191],a[36:39],v[172:175]\n"
"s_nop 0\n"
TR "v[180:181],v6 offset:4096\n"
TR "v[182:183],v6 offset:6144\n"
MF "v[164:167],v[188:191],a[32:35],v[164:167]\n"
"s_nop 0\n"
TR "v[184:185],v6 offset:8192\n"
TR "v[186:187],v6 offset:10240\n"
MF "v[164:167],a[240:243],a[72:75],v[164:167]\n"
"v_sub_u32 v10,v10,v11\n"
MF "v[164:167],a[244:247],a[112:115],v[164:167]\n"
"v_readlane_b32 s16,v10,s16\n"
MF "v[156:159],a[244:247],a[108:111],v[156:159]\n"
"s_cmp_lt_u32 s16,9\n"
"s_cselect_b32 s16,0xf0000000,0\n"
MF "v[156:159],a[240:243],a[68:71],v[156:159]\n"
"v_readlane_b32 s17,v10,s17\n"
MF "v[156:159],v[188:191],a[28:31],v[156:159]\n"
"s_cmp_lt_u32 s17,10\n"
"s_cselect_b32 s17,0xf0000000,0\n"
MF "v[148:151],v[188:191],a[24:27],v[148:151]\n"
"s_bfe_u32 s62,s22,0x30014\n"
MF "v[148:151],a[240:243],a[64:67],v[148:151]\n"
"s_add_u32 s62,s62,3\n"
"s_and_b32 s62,s62,7\n"
"s_mul_i32 s62,s62,204800\n"
MF "v[148:151],a[244:247],a[104:107],v[148:151]\n"
"s_add_u32 s62,s62,s73\n"
"s_add_u32 s62,s62,122880\n"
"s_or_b32 s62,s62,s16\n"
MF "v[140:143],a[244:247],a[100:103],v[140:143]\n"
LD "v[56:59],v9,s[40:43],s62 offen sc1\n"
MF "v[140:143],a[240:243],a[60:63],v[140:143]\n"
LD "v[64:67],v9,s[40:43],s62 offen offset:1024 sc1\n"
MF "v[140:143],v[188:191],a[20:23],v[140:143]\n"
"s_waitcnt lgkmcnt(0)\n"
LD "v[72:75],v9,s[40:43],s62 offen offset:2048 sc1\n"
MF "v[136:139],v[176:179],a[140:143],v[136:139]\n"
"s_nop 0\n"
TR "v[188:189],v7\n"
TR "v[190:191],v7 offset:2048\n"
MF "v[136:139],v[180:183],a[180:183],v[136:139]\n"
"s_nop 0\n"
TR "a[240:241],v7 offset:4096\n"
TR "a[242:243],v7 offset:6144\n"
MF "v[136:139],v[184:187],a[220:223],v[136:139]\n"
"s_nop 0\n"
TR "a[244:245],v7 offset:8192\n"
TR "a[246:247],v7 offset:10240\n"
MF "v[144:147],v[184:187],a[224:227],v[144:147]\n"
LD "v[80:83],v9,s[40:43],s62 offen offset:3072 sc1\n"
MF "v[144:147],v[180:183],a[184:187],v[144:147]\n"
MF "v[144:147],v[176:179],a[144:147],v[144:147]\n"
"s_add_u32 s62,s62,4096\n"
MF "v[152:155],v[176:179],a[148:151],v[152:155]\n"
LD "v[88:91],v9,s[40:43],s62 offen sc1\n"
MF "v[152:155],v[180:183],a[188:191],v[152:155]\n"
"s_bfe_u32 s62,s22,0x30014\n"
"ds_write_addtid_b32 v136\n"
"ds_write_addtid_b32 v137 offset:256\n"
MF "v[152:155],v[184:187],a[228:231],v[152:155]\n"
"s_add_u32 s62,s62,2\n"
"s_and_b32 s62,s62,7\n"
"s_mul_i32 s62,s62,204800\n"
MF "v[160:163],v[184:187],a[232:235],v[160:163]\n"
"s_add_u32 s62,s62,s73\n"
"s_add_u32 s62,s62,143360\n"
"s_or_b32 s62,s62,s17\n"
MF "v[160:163],v[180:183],a[192:195],v[160:163]\n"
LD "v[60:63],v9,s[40:43],s62 offen sc1\n"
MF "v[160:163],v[176:179],a[152:155],v[160:163]\n"
"s_nop 0\n"
"ds_write_addtid_b32 v138 offset:512\n"
"ds_write_addtid_b32 v139 offset:768\n"
MF "v[168:171],v[176:179],a[156:159],v[168:171]\n"
LD "v[68:71],v9,s[40:43],s62 offen offset:1024 sc1\n"
MF "v[168:171],v[180:183],a[196:199],v[168:171]\n"
"s_nop 0\n"
"ds_write_addtid_b32 v144 offset:2048\n"
"ds_write_addtid_b32 v145 offset:2304\n"
MF "v[168:171],v[184:187],a[236:239],v[168:171]\n"
"s_waitcnt lgkmcnt(6)\n"
LD "v[76:79],v9,s[40:43],s62 offen offset:2048 sc1\n"
MF "v[140:143],v[188:191],a[140:143],v[140:143]\n"
"s_nop 0\n"
TR "v[176:177],v4\n"
TR "v[178:179],v4 offset:2048\n"
MF "v[140:143],a[240:243],a[180:183],v[140:143]\n"
"s_nop 0\n"
TR "v[180:181],v4 offset:4096\n"
TR "v[182:183],v4 offset:6144\n"
MF "v[140:143],a[244:247],a[220:223],v[140:143]\n"
"s_nop 0\n"
TR "v[184:185],v4 offset:8192\n"
TR "v[186:187],v4 offset:10240\n"
MF "v[148:151],a[244:247],a[224:227],v[148:151]\n"
LD "v[84:87],v9,s[40:43],s62 offen offset:3072 sc1\n"
MF "v[148:151],a[240:243],a[184:187],v[148:151]\n"
"s_nop 0\n"
"ds_write_addtid_b32 v146 offset:2560\n"
"ds_write_addtid_b32 v147 offset:2816\n"
MF "v[148:151],v[188:191],a[144:147],v[148:151]\n"
"s_add_u32 s62,s62,4096\n"
"ds_write_addtid_b32 v152 offset:4096\n"
"ds_write_addtid_b32 v153 offset:4352\n"
MF "v[156:159],v[188:191],a[148:151],v[156:159]\n"
LD "v[92:95],v9,s[40:43],s62 offen sc1\n"
MF "v[156:159],a[240:243],a[188:191],v[156:159]\n"
"s_nop 0\n"
"ds_write_addtid_b32 v154 offset:4608\n"
"ds_write_addtid_b32 v155 offset:4864\n"
MF "v[156:159],a[244:247],a[228:231],v[156:159]\n"
"s_nop 0\n"
"ds_write_addtid_b32 v160 offset:6144\n"
"ds_write_addtid_b32 v161 offset:6400\n"
MF "v[164:167],a[244:247],a[232:235],v[164:167]\n"
"s_nop 0\n"
"ds_write_addtid_b32 v162 offset:6656\n"
"ds_write_addtid_b32 v163 offset:6912\n"
MF "v[164:167],a[240:243],a[192:195],v[164:167]\n"
"s_nop 0\n"
"ds_write_addtid_b32 v168 offset:8192\n"
"ds_write_addtid_b32 v169 offset:8448\n"
MF "v[164:167],v[188:191],a[152:155],v[164:167]\n"
"buffer_load_dword v10,v12,s[40:43],s86 offen sc1\n"
MF "v[172:175],v[188:191],a[156:159],v[172:175]\n"
"s_nop 0\n"
"ds_write_addtid_b32 v170 offset:8704\n"
"ds_write_addtid_b32 v171 offset:8960\n"
MF "v[172:175],a[240:243],a[196:199],v[172:175]\n"
"s_nop 0\n"
"ds_write_addtid_b32 v140 offset:1024\n"
"ds_write_addtid_b32 v141 offset:1280\n"
MF "v[172:175],a[244:247],a[236:239],v[172:175]\n"
"s_waitcnt lgkmcnt(15)\n"
"ds_write_addtid_b32 v142 offset:1536\n"
"ds_write_addtid_b32 v143 offset:1792\n"
MF "v[96:99],v[176:179],a[140:143],v[96:99]\n"
"s_nop 0\n"
TR "v[188:189],v5\n"
TR "v[190:191],v5 offset:2048\n"
MF "v[96:99],v[180:183],a[180:183],v[96:99]\n"
"s_nop 0\n"
TR "a[240:241],v5 offset:4096\n"
TR "a[242:243],v5 offset:6144\n"
MF "v[96:99],v[184:187],a[220:223],v[96:99]\n"
"s_nop 0\n"
TR "a[244:245],v5 offset:8192\n"
TR "a[246:247],v5 offset:10240\n"
MF "v[104:107],v[184:187],a[224:227],v[104:107]\n"
"s_nop 0\n"
"ds_write_addtid_b32 v148 offset:3072\n"
"ds_write_addtid_b32 v149 offset:3328\n"
MF "v[104:107],v[180:183],a[184:187],v[104:107]\n"
"s_nop 0\n"
"ds_write_addtid_b32 v150 offset:3584\n"
"ds_write_addtid_b32 v151 offset:3840\n"
MF "v[104:107],v[176:179],a[144:147],v[104:107]\n"
"s_nop 0\n"
"ds_write_addtid_b32 v156 offset:5120\n"
"ds_write_addtid_b32 v157 offset:5376\n"
MF "v[112:115],v[176:179],a[148:151],v[112:115]\n"
"s_nop 0\n"
"ds_write_addtid_b32 v158 offset:5632\n"
"ds_write_addtid_b32 v159 offset:5888\n"
MF "v[112:115],v[180:183],a[188:191],v[112:115]\n"
"ds_write_addtid_b32 v164 offset:7168\n"
MF "v[112:115],v[184:187],a[228:231],v[112:115]\n"
"ds_write_addtid_b32 v165 offset:7424\n"
MF "v[120:123],v[184:187],a[232:235],v[120:123]\n"
"ds_write_addtid_b32 v166 offset:7680\n"
MF "v[120:123],v[180:183],a[192:195],v[120:123]\n"
"ds_write_addtid_b32 v167 offset:7936\n"
MF "v[120:123],v[176:179],a[152:155],v[120:123]\n"
"ds_write_addtid_b32 v172 offset:9216\n"
MF "v[128:131],v[176:179],a[156:159],v[128:131]\n"
"ds_write_addtid_b32 v173 offset:9472\n"
MF "v[128:131],v[180:183],a[196:199],v[128:131]\n"
"ds_write_addtid_b32 v174 offset:9728\n"
MF "v[128:131],v[184:187],a[236:239],v[128:131]\n"
"s_waitcnt lgkmcnt(15)\n"
"ds_write_addtid_b32 v175 offset:9984\n"
MF "v[100:103],v[188:191],a[140:143],v[100:103]\n"
MF "v[100:103],a[240:243],a[180:183],v[100:103]\n"
MF "v[100:103],a[244:247],a[220:223],v[100:103]\n"
MF "v[108:111],a[244:247],a[224:227],v[108:111]\n"
"s_waitcnt lgkmcnt(0)\n"
MF "v[108:111],a[240:243],a[184:187],v[108:111]\n"
"s_barrier\n"
MF "v[108:111],v[188:191],a[144:147],v[108:111]\n"
"ds_read2st64_b32 v[16:17],v13 offset0:192 offset1:193\n"
MF "v[116:119],v[188:191],a[148:151],v[116:119]\n"
"ds_read2st64_b32 v[18:19],v13 offset0:194 offset1:195\n"
MF "v[116:119],a[240:243],a[188:191],v[116:119]\n"
"ds_read2st64_b32 v[20:21],v13 offset0:196 offset1:197\n"
MF "v[116:119],a[244:247],a[228:231],v[116:119]\n"
"ds_read2st64_b32 v[22:23],v13 offset0:198 offset1:199\n"
MF "v[124:127],a[244:247],a[232:235],v[124:127]\n"
"ds_read2st64_b32 v[24:25],v13 offset0:200 offset1:201\n"
MF "v[124:127],a[240:243],a[192:195],v[124:127]\n"
"ds_read2st64_b32 v[26:27],v13 offset0:202 offset1:203\n"
MF "v[124:127],v[188:191],a[152:155],v[124:127]\n"
"ds_read2st64_b32 v[28:29],v13 offset0:204 offset1:205\n"
MF "v[132:135],v[188:191],a[156:159],v[132:135]\n"
"ds_read2st64_b32 v[30:31],v13 offset0:206 offset1:207\n"
MF "v[132:135],a[240:243],a[196:199],v[132:135]\n"
"ds_read2st64_b32 v[32:33],v13 offset0:208 offset1:209\n"
MF "v[132:135],a[244:247],a[236:239],v[132:135]\n"
"ds_read2st64_b32 v[34:35],v13 offset0:210 offset1:211\n"
"ds_read2st64_b32 v[36:37],v13 offset0:212 offset1:213\n"
"ds_read2st64_b32 v[38:39],v13 offset0:214 offset1:215\n"
"ds_read2st64_b32 v[40:41],v13 offset0:216 offset1:217\n"
"ds_read2st64_b32 v[42:43],v13 offset0:218 offset1:219\n"
"ds_read2st64_b32 v[44:45],v13 offset0:220 offset1:221\n"
"ds_read2st64_b32 v[46:47],v13 offset0:222 offset1:223\n"
"ds_read2st64_b32 v[48:49],v13 offset0:224 offset1:225\n"
"ds_read2st64_b32 v[50:51],v13 offset0:226 offset1:227\n"
"ds_read2st64_b32 v[52:53],v13 offset0:228 offset1:229\n"
"ds_read2st64_b32 v[54:55],v13 offset0:230 offset1:231\n"
"s_waitcnt vmcnt(0)\n"
"v_sub_u32 v10,v10,v11\n"
"s_cmp_eq_u32 s16,0\n"
"s_cbranch_scc1 .Lhave13\n"
"s_add_u32 s59,s78,6\n"
"s_and_b32 s59,s59,15\n"
"s_nop 3\n"
".Lnf9:\n"
"v_readlane_b32 s58,v10,s59\n"
"s_cmp_lt_u32 s58,9\n"
"s_cbranch_scc1 .Lsp9\n"
".Lnb9:\n"
"s_bfe_u32 s62,s22,0x30014\n"
"s_add_u32 s62,s62,3\n"
"s_and_b32 s62,s62,7\n"
"s_mul_i32 s62,s62,204800\n"
"s_add_u32 s62,s62,s73\n"
"s_add_u32 s62,s62,122880\n"
LD "v[56:59],v9,s[40:43],s62 offen sc1\n"
LD "v[64:67],v9,s[40:43],s62 offen offset:1024 sc1\n"
LD "v[72:75],v9,s[40:43],s62 offen offset:2048 sc1\n"
LD "v[80:83],v9,s[40:43],s62 offen offset:3072 sc1\n"
"s_add_u32 s62,s62,4096\n"
LD "v[88:91],v9,s[40:43],s62 offen sc1\n"
".Lhave13:\n"
"s_cmp_eq_u32 s17,0\n"
"s_cbranch_scc1 .Lhave12\n"
"s_add_u32 s59,s78,4\n"
"s_and_b32 s59,s59,15\n"
"s_nop 3\n"
".Lnf10:\n"
"v_readlane_b32 s58,v10,s59\n"
"s_cmp_lt_u32 s58,10\n"
"s_cbranch_scc1 .Lsp10\n"
".Lnb10:\n"
"s_bfe_u32 s62,s22,0x30014\n"
"s_add_u32 s62,s62,2\n"
"s_and_b32 s62,s62,7\n"
"s_mul_i32 s62,s62,204800\n"
"s_add_u32 s62,s62,s73\n"
"s_add_u32 s62,s62,143360\n"
LD "v[60:63],v9,s[40:43],s62 offen sc1\n"
LD "v[68:71],v9,s[40:43],s62 offen offset:1024 sc1\n"
LD "v[76:79],v9,s[40:43],s62 offen offset:2048 sc1\n"
LD "v[84:87],v9,s[40:43],s62 offen offset:3072 sc1\n"
"s_add_u32 s62,s62,4096\n"
LD "v[92:95],v9,s[40:43],s62 offen sc1\n"
".Lhave12:\n"
".Lnf11:\n"
"v_readlane_b32 s58,v10,s72\n"
"s_cmp_lt_u32 s58,11\n"
"s_cbranch_scc1 .Lsp11\n"
".Lnb11:\n"
"s_bfe_u32 s62,s22,0x30014\n"
"s_add_u32 s62,s62,1\n"
"s_and_b32 s62,s62,7\n"
"s_mul_i32 s62,s62,204800\n"
"s_add_u32 s62,s62,s73\n"
"s_nop 0\n"
"s_add_u32 s62,s62,163840\n"
LD "v[208:211],v9,s[40:43],s62 offen sc1\n"
LD "v[212:215],v9,s[40:43],s62 offen offset:1024 sc1\n"
LD "v[216:219],v9,s[40:43],s62 offen offset:2048 sc1\n"
".Lpg38h:\n"
"s_cbranch_execz .Lisr4\n"
"s_nop 0\n"
LD "v[220:223],v9,s[40:43],s62 offen offset:3072 sc1\n"
"s_add_u32 s62,s62,4096\n"
LD "v[224:227],v9,s[40:43],s62 offen sc1\n"
"s_cmp_eq_u32 s1,0xf0000000\n"
"s_cbranch_scc1 .Ljx1\n"
"v_accvgpr_mov_b32 a16,a0\n"
"v_accvgpr_mov_b32 a17,a1\n"
"v_accvgpr_mov_b32 a56,a40\n"
"v_accvgpr_mov_b32 a57,a41\n"
MF "v[196:199],a[2:5],a[250:253],0\n"
MF "v[192:195],a[6:9],a[250:253],0\n"
MF "v[196:199],a[10:13],a[250:253],v[196:199]\n"
MF "v[192:195],a[14:17],a[250:253],v[192:195]\n"
MF "v[196:199],a[42:45],a[250:253],v[196:199]\n"
MF "v[192:195],a[46:49],a[250:253],v[192:195]\n"
MF "v[196:199],a[50:53],a[250:253],v[196:199]\n"
MF "v[192:195],a[54:57],a[250:253],v[192:195]\n"
"s_nop 7\n"
CV "v192,v192,v193\n"
CV "v193,v194,v195\n"
CV "v194,v196,v197\n"
CV "v195,v198,v199\n"
"s_nop 0\n"
"v_permlane16_swap_b32 v192,v194\n"
"v_permlane16_swap_b32 v193,v195\n"
"buffer_store_dwordx4 v[192:195],v8,s[44:47],s1 offen sc1\n"
".Ljx1:\n"
"s_waitcnt lgkmcnt(15)\n"
"v_pk_add_f32 v[96:97],v[96:97],v[16:17]\n"
"v_pk_add_f32 v[98:99],v[98:99],v[18:19]\n"
"v_pk_add_f32 v[100:101],v[100:101],v[20:21]\n"
"v_pk_add_f32 v[102:103],v[102:103],v[22:23]\n"
"v_pk_add_f32 v[104:105],v[104:105],v[24:25]\n"
"s_waitcnt lgkmcnt(14)\n"
"v_pk_add_f32 v[106:107],v[106:107],v[26:27]\n"
"s_waitcnt lgkmcnt(13)\n"
"v_pk_add_f32 v[108:109],v[108:109],v[28:29]\n"
"s_waitcnt lgkmcnt(12)\n"
"v_pk_add_f32 v[110:111],v[110:111],v[30:31]\n"
"s_waitcnt lgkmcnt(11)\n"
"v_pk_add_f32 v[112:113],v[112:113],v[32:33]\n"
"s_waitcnt lgkmcnt(10)\n"
"v_pk_add_f32 v[114:115],v[114:115],v[34:35]\n"
"s_waitcnt lgkmcnt(9)\n"
"v_pk_add_f32 v[116:117],v[116:117],v[36:37]\n"
"s_waitcnt lgkmcnt(8)\n"
"v_pk_add_f32 v[118:119],v[118:119],v[38:39]\n"
"s_waitcnt lgkmcnt(7)\n"
"v_pk_add_f32 v[120:121],v[120:121],v[40:41]\n"
"s_waitcnt lgkmcnt(6)\n"
"v_pk_add_f32 v[122:123],v[122:123],v[42:43]\n"
"s_waitcnt lgkmcnt(5)\n"
"v_pk_add_f32 v[124:125],v[124:125],v[44:45]\n"
"s_waitcnt lgkmcnt(4)\n"
"v_pk_add_f32 v[126:127],v[126:127],v[46:47]\n"
"s_waitcnt lgkmcnt(3)\n"
"v_pk_add_f32 v[128:129],v[128:129],v[48:49]\n"
"s_waitcnt lgkmcnt(2)\n"
"v_pk_add_f32 v[130:131],v[130:131],v[50:51]\n"
"s_waitcnt lgkmcnt(1)\n"
"v_pk_add_f32 v[132:133],v[132:133],v[52:53]\n"
"s_waitcnt lgkmcnt(0)\n"
"v_pk_add_f32 v[134:135],v[134:135],v[54:55]\n"
"s_waitcnt vmcnt(5)\n"
"s_nop 0\n"
MF "v[100:103],v[56:59],a[252:255],v[100:103]\n"
MF "v[108:111],v[64:67],a[252:255],v[108:111]\n"
MF "v[116:119],v[72:75],a[252:255],v[116:119]\n"
MF "v[124:127],v[80:83],a[252:255],v[124:127]\n"
MF "v[132:135],v[88:91],a[252:255],v[132:135]\n"
MF "v[96:99],v[56:59],a[248:251],v[96:99]\n"
MF "v[104:107],v[64:67],a[248:251],v[104:107]\n"
MF "v[112:115],v[72:75],a[248:251],v[112:115]\n"
MF "v[120:123],v[80:83],a[248:251],v[120:123]\n"
MF "v[128:131],v[88:91],a[248:251],v[128:131]\n"
MF "v[96:99],v[60:63],a[252:255],v[96:99]\n"
MF "v[104:107],v[68:71],a[252:255],v[104:107]\n"
MF "v[112:115],v[76:79],a[252:255],v[112:115]\n"
MF "v[120:123],v[84:87],a[252:255],v[120:123]\n"
MF "v[128:131],v[92:95],a[252:255],v[128:131]\n"
MF "v[100:103],v[60:63],a[248:251],v[100:103]\n"
MF "v[108:111],v[68:71],a[248:251],v[108:111]\n"
MF "v[116:119],v[76:79],a[248:251],v[116:119]\n"
MF "v[124:127],v[84:87],a[248:251],v[124:127]\n"
MF "v[132:135],v[92:95],a[248:251],v[132:135]\n"
"s_waitcnt vmcnt(0)\n"
MF "v[100:103],v[208:211],a[252:255],v[100:103]\n"
MF "v[96:99],v[208:211],a[248:251],v[96:99]\n"
MF "v[108:111],v[212:215],a[252:255],v[108:111]\n"
MF "v[104:107],v[212:215],a[248:251],v[104:107]\n"
MF "v[116:119],v[216:219],a[252:255],v[116:119]\n"
"s_nop 4\n"
CV "v96,v96,v97\n"
CV "v97,v98,v99\n"
MF "v[112:115],v[216:219],a[248:251],v[112:115]\n"
CV "v98,v100,v101\n"
CV "v99,v102,v103\n"
MF "v[124:127],v[220:223],a[252:255],v[124:127]\n"
CV "v104,v104,v105\n"
CV "v105,v106,v107\n"
MF "v[120:123],v[220:223],a[248:251],v[120:123]\n"
CV "v106,v108,v109\n"
CV "v107,v110,v111\n"
"v_permlane16_swap_b32 v96,v98\n"
"v_permlane16_swap_b32 v97,v99\n"
"buffer_store_dwordx4 v[96:99],v242,s[44:47],s87 offen sc1\n"
"s_add_u32 s87,s87,40960\n"
MF "v[132:135],v[224:227],a[252:255],v[132:135]\n"
CV "v112,v112,v113\n"
CV "v113,v114,v115\n"
MF "v[128:131],v[224:227],a[248:251],v[128:131]\n"
CV "v114,v116,v117\n"
CV "v115,v118,v119\n"
"v_permlane16_swap_b32 v104,v106\n"
"v_permlane16_swap_b32 v105,v107\n"
"buffer_store_dwordx4 v[104:107],v242,s[44:47],s87 offen sc1\n"
"s_add_u32 s87,s87,40960\n"
CV "v120,v120,v121\n"
CV "v121,v122,v123\n"
CV "v122,v124,v125\n"
CV "v123,v126,v127\n"
"v_permlane16_swap_b32 v112,v114\n"
"v_permlane16_swap_b32 v113,v115\n"
"buffer_store_dwordx4 v[112:115],v242,s[44:47],s87 offen sc1\n"
"s_add_u32 s87,s87,40960\n"
CV "v128,v128,v129\n"
CV "v129,v130,v131\n"
CV "v130,v132,v133\n"
CV "v131,v134,v135\n"
"v_permlane16_swap_b32 v120,v122\n"
"v_permlane16_swap_b32 v121,v123\n"
"buffer_store_dwordx4 v[120:123],v242,s[44:47],s87 offen sc1\n"
"s_add_u32 s87,s87,40960\n"
"v_permlane16_swap_b32 v128,v130\n"
"v_permlane16_swap_b32 v129,v131\n"
"buffer_store_dwordx4 v[128:131],v242,s[44:47],s87 offen sc1\n"
"s_add_u32 s87,s87,40960\n"
"s_endpgm\n"
".Ljobs:\n"
"s_cmp_ge_u32 s92,40\n"
"s_cbranch_scc1 .Ldone\n"
"buffer_load_dword v10,v12,s[40:43],s86 offen sc1\n"
"s_mul_i32 s95,s92,3277\n"
"s_lshr_b32 s95,s95,16\n"
"s_mul_i32 s59,s95,20\n"
"s_sub_u32 s59,s92,s59\n"
"s_cmp_ge_u32 s59,10\n"
"s_cselect_b32 s96,4294557760,0\n"
"s_add_u32 s96,s96,1024\n"
"s_mul_i32 s59,s59,40960\n"
"s_add_u32 s96,s96,s59\n"
"s_lshl_b32 s59,s95,7\n"
"s_add_u32 s96,s96,s59\n"
"s_add_u32 s95,s95,3\n"
"s_waitcnt vmcnt(0)\n"
".Ljtry:\n"
"s_cmp_eq_u32 s98,0\n"
"s_cselect_b32 s95,0,s95\n"
"v_sub_u32 v10,v10,v11\n"
"v_cmp_gt_u32 vcc,s95,v10\n"
"s_lshl_b32 s82,s92,10\n"
"s_add_u32 s82,s82,s93\n"
"s_cmp_eq_u64 vcc,0\n"
"s_cselect_b32 s82,s82,0xf0000000\n"
"s_cselect_b32 s84,s96,0xf0000000\n"
"s_cselect_b32 s59,s4,0\n"
"s_add_u32 s92,s92,s59\n"
"s_cmp_eq_u32 s59,0\n"
"s_cbranch_scc0 .Ljgo\n"
"s_sub_u32 s98,s98,1\n"
"s_sleep 2\n"
"s_branch .Ljobs\n"
".Ljgo:\n"
"s_mov_b32 s83,s84\n"
LD "v[208:211],v9,s[40:43],s82 offen sc1\n"
"s_add_u32 s82,s82,s71\n"
LD "v[212:215],v9,s[40:43],s82 offen sc1\n"
"s_add_u32 s82,s82,s71\n"
LD "v[216:219],v9,s[40:43],s82 offen sc1\n"
"s_add_u32 s82,s82,s71\n"
LD "v[220:223],v9,s[40:43],s82 offen sc1\n"
"s_add_u32 s82,s82,s71\n"
LD "v[224:227],v9,s[40:43],s82 offen sc1\n"
"s_add_u32 s82,s82,s66\n"
LD "v[228:231],v9,s[40:43],s82 offen sc1\n"
"s_add_u32 s82,s82,s71\n"
LD "v[232:235],v9,s[40:43],s82 offen sc1\n"
"s_add_u32 s82,s82,s71\n"
LD "v[236:239],v9,s[40:43],s82 offen sc1\n"
"buffer_load_dword v10,v12,s[40:43],s86 offen sc1\n"
"s_waitcnt vmcnt(0)\n"
".Ljfin:\n"
"s_nop 1\n"
"v_mov_b32 v240,v208\n"
"v_mov_b32 v241,v209\n"
MF "v[248:251],v[210:213],a[250:253],0\n"
MF "v[244:247],v[214:217],a[250:253],0\n"
MF "v[248:251],v[218:221],a[250:253],v[248:251]\n"
MF "v[244:247],v[222:225],a[250:253],v[244:247]\n"
MF "v[248:251],v[226:229],a[250:253],v[248:251]\n"
MF "v[244:247],v[230:233],a[250:253],v[244:247]\n"
MF "v[248:251],v[234:237],a[250:253],v[248:251]\n"
MF "v[244:247],v[238:241],a[250:253],v[244:247]\n"
"s_nop 7\n"
CV "v244,v244,v245\n"
CV "v245,v246,v247\n"
CV "v246,v248,v249\n"
CV "v247,v250,v251\n"
"s_nop 0\n"
"v_permlane16_swap_b32 v244,v246\n"
"v_permlane16_swap_b32 v245,v247\n"
"buffer_store_dwordx4 v[244:247],v8,s[44:47],s83 offen sc1\n"
"s_cmp_ge_u32 s92,40\n"
"s_cbranch_scc1 .Ldone\n"
"s_mul_i32 s95,s92,3277\n"
"s_lshr_b32 s95,s95,16\n"
"s_mul_i32 s59,s95,20\n"
"s_sub_u32 s59,s92,s59\n"
"s_cmp_ge_u32 s59,10\n"
"s_cselect_b32 s96,4294557760,0\n"
"s_add_u32 s96,s96,1024\n"
"s_mul_i32 s59,s59,40960\n"
"s_add_u32 s96,s96,s59\n"
"s_lshl_b32 s59,s95,7\n"
"s_add_u32 s96,s96,s59\n"
"s_add_u32 s95,s95,3\n"
"s_branch .Ljtry\n"
".Lool:\n"
".Lsp0:\n"
"s_cmp_eq_u32 s98,0\n"
"s_cbranch_scc1 .Lnb0\n"
"s_sub_u32 s98,s98,1\n"
"s_sleep 2\n"
"buffer_load_dword v10,v12,s[40:43],s86 offen sc1\n"
"s_waitcnt vmcnt(0)\n"
"v_sub_u32 v10,v10,v11\n"
"s_nop 1\n"
"s_branch .Lnf0\n"
".Lsp1:\n"
"s_cmp_eq_u32 s98,0\n"
"s_cbranch_scc1 .Lnb1\n"
"s_sub_u32 s98,s98,1\n"
"s_sleep 2\n"
"buffer_load_dword v10,v12,s[40:43],s86 offen sc1\n"
"s_waitcnt vmcnt(0)\n"
"v_sub_u32 v10,v10,v11\n"
"s_nop 1\n"
"s_branch .Lnf1\n"
".Lsp2:\n"
"s_cmp_eq_u32 s98,0\n"
"s_cbranch_scc1 .Lnb2\n"
"s_sub_u32 s98,s98,1\n"
"s_sleep 2\n"
"buffer_load_dword v10,v12,s[40:43],s86 offen sc1\n"
"s_waitcnt vmcnt(0)\n"
"v_sub_u32 v10,v10,v11\n"
"s_nop 1\n"
"s_branch .Lnf2\n"
".Lsp3:\n"
"s_cmp_eq_u32 s98,0\n"
"s_cbranch_scc1 .Lnb3\n"
"s_sub_u32 s98,s98,1\n"
"s_sleep 2\n"
"buffer_load_dword v10,v12,s[40:43],s86 offen sc1\n"
"s_waitcnt vmcnt(0)\n"
"v_sub_u32 v10,v10,v11\n"
"s_nop 1\n"
"s_branch .Lnf3\n"
".Lsp4:\n"
"s_cmp_eq_u32 s98,0\n"
"s_cbranch_scc1 .Lnb4\n"
"s_sub_u32 s98,s98,1\n"
"s_sleep 2\n"
"buffer_load_dword v10,v12,s[40:43],s86 offen sc1\n"
"s_waitcnt vmcnt(0)\n"
"v_sub_u32 v10,v10,v11\n"
"s_nop 1\n"
"s_branch .Lnf4\n"
".Lsp5:\n"
"s_cmp_eq_u32 s98,0\n"
"s_cbranch_scc1 .Lnb5\n"
"s_sub_u32 s98,s98,1\n"
"s_sleep 2\n"
"buffer_load_dword v10,v12,s[40:43],s86 offen sc1\n"
"s_waitcnt vmcnt(0)\n"
"v_sub_u32 v10,v10,v11\n"
"s_nop 1\n"
"s_branch .Lnf5\n"
".Lsp6:\n"
"s_cmp_eq_u32 s98,0\n"
"s_cbranch_scc1 .Lnb6\n"
"s_sub_u32 s98,s98,1\n"
"s_sleep 2\n"
"buffer_load_dword v10,v12,s[40:43],s86 offen sc1\n"
"s_waitcnt vmcnt(0)\n"
"v_sub_u32 v10,v10,v11\n"
"s_nop 1\n"
"s_branch .Lnf6\n"
".Lsp7:\n"
"s_cmp_eq_u32 s98,0\n"
"s_cbranch_scc1 .Lnb7\n"
"s_sub_u32 s98,s98,1\n"
"s_sleep 2\n"
"buffer_load_dword v10,v12,s[40:43],s86 offen sc1\n"
"s_waitcnt vmcnt(0)\n"
"v_sub_u32 v10,v10,v11\n"
"s_nop 1\n"
"s_branch .Lnf7\n"
".Lsp8:\n"
"s_cmp_eq_u32 s98,0\n"
"s_cbranch_scc1 .Lnb8\n"
"s_sub_u32 s98,s98,1\n"
"s_sleep 2\n"
"buffer_load_dword v10,v12,s[40:43],s86 offen sc1\n"
"s_waitcnt vmcnt(0)\n"
"v_sub_u32 v10,v10,v11\n"
"s_nop 1\n"
"s_branch .Lnf8\n"
".Lsp9:\n"
"s_cmp_eq_u32 s98,0\n"
"s_cbranch_scc1 .Lnb9\n"
"s_sub_u32 s98,s98,1\n"
"s_sleep 2\n"
"buffer_load_dword v10,v12,s[40:43],s86 offen sc1\n"
"s_waitcnt vmcnt(0)\n"
"v_sub_u32 v10,v10,v11\n"
"s_nop 1\n"
"s_branch .Lnf9\n"
".Lsp10:\n"
"s_cmp_eq_u32 s98,0\n"
"s_cbranch_scc1 .Lnb10\n"
"s_sub_u32 s98,s98,1\n"
"s_sleep 2\n"
"buffer_load_dword v10,v12,s[40:43],s86 offen sc1\n"
"s_waitcnt vmcnt(0)\n"
"v_sub_u32 v10,v10,v11\n"
"s_nop 1\n"
"s_branch .Lnf10\n"
".Lsp11:\n"
"s_cmp_eq_u32 s98,0\n"
"s_cbranch_scc1 .Lnb11\n"
"s_sub_u32 s98,s98,1\n"
"s_sleep 2\n"
"buffer_load_dword v10,v12,s[40:43],s86 offen sc1\n"
"s_waitcnt vmcnt(0)\n"
"v_sub_u32 v10,v10,v11\n"
"s_nop 1\n"
"s_branch .Lnf11\n"
".Lcode1:\n"
".Lslow_0:\n"
"s_nop 7\n"
"s_nop 7\n"
"v_and_b32 v10,240,v9\n"
"v_mul_u32_u24 v10,24,v10\n"
"v_lshrrev_b32 v11,4,v9\n"
"v_and_or_b32 v10,v11,48,v10\n"
"s_mov_b32 s59,s34\n"
"s_mov_b32 s34,s20\n"
"s_add_u32 s58,s74,0\n"
"s_nop 3\n"
LD "v[52:55],v10,s[32:35],s58 offen\n"
"s_waitcnt vmcnt(0)\n"
MF "v[72:75],v[52:55],v[136:139],v[32:35]\n"
MF "v[76:79],v[52:55],v[140:143],v[36:39]\n"
MF "v[80:83],v[52:55],v[144:147],v[40:43]\n"
MF "v[84:87],v[52:55],v[148:151],v[44:47]\n"
MF "v[88:91],v[52:55],v[152:155],v[48:51]\n"
"s_add_u32 s58,s74,0\n"
"s_nop 3\n"
LD "v[52:55],v10,s[32:35],s58 offen offset:64\n"
"s_waitcnt vmcnt(0)\n"
MF "v[72:75],v[52:55],v[156:159],v[72:75]\n"
MF "v[76:79],v[52:55],v[160:163],v[76:79]\n"
MF "v[80:83],v[52:55],v[164:167],v[80:83]\n"
MF "v[84:87],v[52:55],v[168:171],v[84:87]\n"
MF "v[88:91],v[52:55],v[172:175],v[88:91]\n"
"s_add_u32 s58,s74,0\n"
"s_nop 3\n"
LD "v[52:55],v10,s[32:35],s58 offen offset:128\n"
"s_waitcnt vmcnt(0)\n"
MF "v[72:75],v[52:55],v[176:179],v[72:75]\n"
MF "v[76:79],v[52:55],v[180:183],v[76:79]\n"
MF "v[80:83],v[52:55],v[184:187],v[80:83]\n"
MF "v[84:87],v[52:55],v[188:191],v[84:87]\n"
MF "v[88:91],v[52:55],v[192:195],v[88:91]\n"
"s_add_u32 s58,s74,0\n"
"s_nop 3\n"
LD "v[52:55],v10,s[32:35],s58 offen offset:192\n"
"s_waitcnt vmcnt(0)\n"
MF "v[72:75],v[52:55],v[196:199],v[72:75]\n"
MF "v[76:79],v[52:55],v[200:203],v[76:79]\n"
MF "v[80:83],v[52:55],v[204:207],v[80:83]\n"
MF "v[84:87],v[52:55],v[208:211],v[84:87]\n"
MF "v[88:91],v[52:55],v[212:215],v[88:91]\n"
"s_add_u32 s58,s74,0\n"
"s_nop 3\n"
LD "v[52:55],v10,s[32:35],s58 offen offset:256\n"
"s_waitcnt vmcnt(0)\n"
MF "v[72:75],v[52:55],v[216:219],v[72:75]\n"
MF "v[76:79],v[52:55],v[220:223],v[76:79]\n"
MF "v[80:83],v[52:55],v[224:227],v[80:83]\n"
MF "v[84:87],v[52:55],v[228:231],v[84:87]\n"
MF "v[88:91],v[52:55],v[232:235],v[88:91]\n"
"s_add_u32 s58,s74,0\n"
"s_nop 3\n"
LD "v[52:55],v10,s[32:35],s58 offen offset:320\n"
"s_waitcnt vmcnt(0)\n"
MF "v[72:75],v[52:55],v[236:239],v[72:75]\n"
MF "v[76:79],v[52:55],v[240:243],v[76:79]\n"
MF "v[80:83],v[52:55],v[244:247],v[80:83]\n"
MF "v[84:87],v[52:55],v[248:251],v[84:87]\n"
MF "v[88:91],v[52:55],v[252:255],v[88:91]\n"
"s_add_u32 s58,s74,6144\n"
"s_nop 3\n"
LD "v[52:55],v10,s[32:35],s58 offen\n"
"s_waitcnt vmcnt(0)\n"
MF "v[112:115],v[52:55],v[136:139],v[32:35]\n"
MF "v[116:119],v[52:55],v[140:143],v[36:39]\n"
MF "v[120:123],v[52:55],v[144:147],v[40:43]\n"
MF "v[124:127],v[52:55],v[148:151],v[44:47]\n"
MF "v[128:131],v[52:55],v[152:155],v[48:51]\n"
"s_add_u32 s58,s74,6144\n"
"s_nop 3\n"
LD "v[52:55],v10,s[32:35],s58 offen offset:64\n"
"s_waitcnt vmcnt(0)\n"
MF "v[112:115],v[52:55],v[156:159],v[112:115]\n"
MF "v[116:119],v[52:55],v[160:163],v[116:119]\n"
MF "v[120:123],v[52:55],v[164:167],v[120:123]\n"
MF "v[124:127],v[52:55],v[168:171],v[124:127]\n"
MF "v[128:131],v[52:55],v[172:175],v[128:131]\n"
"s_add_u32 s58,s74,6144\n"
"s_nop 3\n"
LD "v[52:55],v10,s[32:35],s58 offen offset:128\n"
"s_waitcnt vmcnt(0)\n"
MF "v[112:115],v[52:55],v[176:179],v[112:115]\n"
MF "v[116:119],v[52:55],v[180:183],v[116:119]\n"
MF "v[120:123],v[52:55],v[184:187],v[120:123]\n"
MF "v[124:127],v[52:55],v[188:191],v[124:127]\n"
MF "v[128:131],v[52:55],v[192:195],v[128:131]\n"
"s_add_u32 s58,s74,6144\n"
"s_nop 3\n"
LD "v[52:55],v10,s[32:35],s58 offen offset:192\n"
"s_waitcnt vmcnt(0)\n"
MF "v[112:115],v[52:55],v[196:199],v[112:115]\n"
MF "v[116:119],v[52:55],v[200:203],v[116:119]\n"
MF "v[120:123],v[52:55],v[204:207],v[120:123]\n"
MF "v[124:127],v[52:55],v[208:211],v[124:127]\n"
MF "v[128:131],v[52:55],v[212:215],v[128:131]\n"
"s_add_u32 s58,s74,6144\n"
"s_nop 3\n"
LD "v[52:55],v10,s[32:35],s58 offen offset:256\n"
"s_waitcnt vmcnt(0)\n"
MF "v[112:115],v[52:55],v[216:219],v[112:115]\n"
MF "v[116:119],v[52:55],v[220:223],v[116:119]\n"
MF "v[120:123],v[52:55],v[224:227],v[120:123]\n"
MF "v[124:127],v[52:55],v[228:231],v[124:127]\n"
MF "v[128:131],v[52:55],v[232:235],v[128:131]\n"
"s_add_u32 s58,s74,6144\n"
"s_nop 3\n"
LD "v[52:55],v10,s[32:35],s58 offen offset:320\n"
"s_waitcnt vmcnt(0)\n"
MF "v[112:115],v[52:55],v[236:239],v[112:115]\n"
MF "v[116:119],v[52:55],v[240:243],v[116:119]\n"
MF "v[120:123],v[52:55],v[244:247],v[120:123]\n"
MF "v[124:127],v[52:55],v[248:251],v[124:127]\n"
MF "v[128:131],v[52:55],v[252:255],v[128:131]\n"
"s_nop 7\n"
"s_mov_b32 s34,s59\n"
"v_max3_f32 v10,v72,v73,v74\n"
"s_nop 0\n"
"v_max3_f32 v11,v75,v112,v113\n"
"v_max3_f32 v10,v10,v114,v115\n"
"v_max_f32 v10,v10,v11\n"
"v_mov_b32 v11,v10\n"
"s_nop 1\n"
"v_permlane16_swap_b32 v10,v11\n"
"v_max_f32 v10,v10,v11\n"
"v_mov_b32 v11,v10\n"
"s_nop 1\n"
"v_permlane32_swap_b32 v10,v11\n"
"v_max_f32 v10,v10,v11\n"
"v_add_f32 v10,0x41300000,v10\n"
"v_max_f32 v10,0,v10\n"
"v_exp_f32 v11,-v10\n"
AR "v12,a0\n"
"v_subrev_f32 v12,s13,v12\n"
"v_fma_f32 v12,v12,v11,s13\n"
AW "a0,v12\n"
AR "v12,a20\n"
"v_subrev_f32 v12,s13,v12\n"
"v_fma_f32 v12,v12,v11,s13\n"
AW "a20,v12\n"
AR "v12,a40\n"
"v_subrev_f32 v12,s13,v12\n"
"v_fma_f32 v12,v12,v11,s13\n"
AW "a40,v12\n"
AR "v12,a60\n"
"v_subrev_f32 v12,s13,v12\n"
"v_fma_f32 v12,v12,v11,s13\n"
AW "a60,v12\n"
AR "v12,a80\n"
"v_subrev_f32 v12,s13,v12\n"
"v_fma_f32 v12,v12,v11,s13\n"
AW "a80,v12\n"
AR "v12,a100\n"
"v_subrev_f32 v12,s13,v12\n"
"v_fma_f32 v12,v12,v11,s13\n"
AW "a100,v12\n"
AR "v12,a120\n"
"v_subrev_f32 v12,s13,v12\n"
"v_fma_f32 v12,v12,v11,s13\n"
AW "a120,v12\n"
AR "v12,a140\n"
"v_subrev_f32 v12,s13,v12\n"
"v_fma_f32 v12,v12,v11,s13\n"
AW "a140,v12\n"
AR "v12,a160\n"
"v_subrev_f32 v12,s13,v12\n"
"v_fma_f32 v12,v12,v11,s13\n"
AW "a160,v12\n"
AR "v12,a180\n"
"v_subrev_f32 v12,s13,v12\n"
"v_fma_f32 v12,v12,v11,s13\n"
AW "a180,v12\n"
AR "v12,a200\n"
"v_subrev_f32 v12,s13,v12\n"
"v_fma_f32 v12,v12,v11,s13\n"
AW "a200,v12\n"
AR "v12,a220\n"
"v_subrev_f32 v12,s13,v12\n"
"v_fma_f32 v12,v12,v11,s13\n"
AW "a220,v12\n"
AR "v12,a1\n"
"v_subrev_f32 v12,s13,v12\n"
"v_fma_f32 v12,v12,v11,s13\n"
AW "a1,v12\n"
AR "v12,a21\n"
"v_subrev_f32 v12,s13,v12\n"
"v_fma_f32 v12,v12,v11,s13\n"
AW "a21,v12\n"
AR "v12,a41\n"
"v_subrev_f32 v12,s13,v12\n"
"v_fma_f32 v12,v12,v11,s13\n"
AW "a41,v12\n"
AR "v12,a61\n"
"v_subrev_f32 v12,s13,v12\n"
"v_fma_f32 v12,v12,v11,s13\n"
AW "a61,v12\n"
AR "v12,a81\n"
"v_subrev_f32 v12,s13,v12\n"
"v_fma_f32 v12,v12,v11,s13\n"
AW "a81,v12\n"
AR "v12,a101\n"
"v_subrev_f32 v12,s13,v12\n"
"v_fma_f32 v12,v12,v11,s13\n"
AW "a101,v12\n"
AR "v12,a121\n"
"v_subrev_f32 v12,s13,v12\n"
"v_fma_f32 v12,v12,v11,s13\n"
AW "a121,v12\n"
AR "v12,a141\n"
"v_subrev_f32 v12,s13,v12\n"
"v_fma_f32 v12,v12,v11,s13\n"
AW "a141,v12\n"
AR "v12,a161\n"
"v_subrev_f32 v12,s13,v12\n"
"v_fma_f32 v12,v12,v11,s13\n"
AW "a161,v12\n"
AR "v12,a181\n"
"v_subrev_f32 v12,s13,v12\n"
"v_fma_f32 v12,v12,v11,s13\n"
AW "a181,v12\n"
AR "v12,a201\n"
"v_subrev_f32 v12,s13,v12\n"
"v_fma_f32 v12,v12,v11,s13\n"
AW "a201,v12\n"
AR "v12,a221\n"
"v_subrev_f32 v12,s13,v12\n"
"v_fma_f32 v12,v12,v11,s13\n"
AW "a221,v12\n"
AR "v12,a2\n"
"v_subrev_f32 v12,s13,v12\n"
"v_fma_f32 v12,v12,v11,s13\n"
AW "a2,v12\n"
AR "v12,a22\n"
"v_subrev_f32 v12,s13,v12\n"
"v_fma_f32 v12,v12,v11,s13\n"
AW "a22,v12\n"
AR "v12,a42\n"
"v_subrev_f32 v12,s13,v12\n"
"v_fma_f32 v12,v12,v11,s13\n"
AW "a42,v12\n"
AR "v12,a62\n"
"v_subrev_f32 v12,s13,v12\n"
"v_fma_f32 v12,v12,v11,s13\n"
AW "a62,v12\n"
AR "v12,a82\n"
"v_subrev_f32 v12,s13,v12\n"
"v_fma_f32 v12,v12,v11,s13\n"
AW "a82,v12\n"
AR "v12,a102\n"
"v_subrev_f32 v12,s13,v12\n"
"v_fma_f32 v12,v12,v11,s13\n"
AW "a102,v12\n"
AR "v12,a122\n"
"v_subrev_f32 v12,s13,v12\n"
"v_fma_f32 v12,v12,v11,s13\n"
AW "a122,v12\n"
AR "v12,a142\n"
"v_subrev_f32 v12,s13,v12\n"
"v_fma_f32 v12,v12,v11,s13\n"
AW "a142,v12\n"
AR "v12,a162\n"
"v_subrev_f32 v12,s13,v12\n"
"v_fma_f32 v12,v12,v11,s13\n"
AW "a162,v12\n"
AR "v12,a182\n"
"v_subrev_f32 v12,s13,v12\n"
"v_fma_f32 v12,v12,v11,s13\n"
AW "a182,v12\n"
AR "v12,a202\n"
"v_subrev_f32 v12,s13,v12\n"
"v_fma_f32 v12,v12,v11,s13\n"
AW "a202,v12\n"
AR "v12,a222\n"
"v_subrev_f32 v12,s13,v12\n"
"v_fma_f32 v12,v12,v11,s13\n"
AW "a222,v12\n"
AR "v12,a3\n"
"v_subrev_f32 v12,s13,v12\n"
"v_fma_f32 v12,v12,v11,s13\n"
AW "a3,v12\n"
AR "v12,a23\n"
"v_subrev_f32 v12,s13,v12\n"
"v_fma_f32 v12,v12,v11,s13\n"
AW "a23,v12\n"
AR "v12,a43\n"
"v_subrev_f32 v12,s13,v12\n"
"v_fma_f32 v12,v12,v11,s13\n"
AW "a43,v12\n"
AR "v12,a63\n"
"v_subrev_f32 v12,s13,v12\n"
"v_fma_f32 v12,v12,v11,s13\n"
AW "a63,v12\n"
AR "v12,a83\n"
"v_subrev_f32 v12,s13,v12\n"
"v_fma_f32 v12,v12,v11,s13\n"
AW "a83,v12\n"
AR "v12,a103\n"
"v_subrev_f32 v12,s13,v12\n"
"v_fma_f32 v12,v12,v11,s13\n"
AW "a103,v12\n"
AR "v12,a123\n"
"v_subrev_f32 v12,s13,v12\n"
"v_fma_f32 v12,v12,v11,s13\n"
AW "a123,v12\n"
AR "v12,a143\n"
"v_subrev_f32 v12,s13,v12\n"
"v_fma_f32 v12,v12,v11,s13\n"
AW "a143,v12\n"
AR "v12,a163\n"
"v_subrev_f32 v12,s13,v12\n"
"v_fma_f32 v12,v12,v11,s13\n"
AW "a163,v12\n"
AR "v12,a183\n"
"v_subrev_f32 v12,s13,v12\n"
"v_fma_f32 v12,v12,v11,s13\n"
AW "a183,v12\n"
AR "v12,a203\n"
"v_subrev_f32 v12,s13,v12\n"
"v_fma_f32 v12,v12,v11,s13\n"
AW "a203,v12\n"
AR "v12,a223\n"
"v_subrev_f32 v12,s13,v12\n"
"v_fma_f32 v12,v12,v11,s13\n"
AW "a223,v12\n"
"v_mul_f32 v52,v57,v11\n"
"v_sub_f32 v32,v32,v10\n"
"v_sub_f32 v33,v33,v10\n"
"v_sub_f32 v34,v34,v10\n"
"v_sub_f32 v35,v35,v10\n"
"v_sub_f32 v92,v92,v10\n"
"v_sub_f32 v93,v93,v10\n"
"v_sub_f32 v94,v94,v10\n"
"v_sub_f32 v95,v95,v10\n"
"v_sub_f32 v72,v72,v10\n"
"v_sub_f32 v73,v73,v10\n"
"v_sub_f32 v74,v74,v10\n"
"v_sub_f32 v75,v75,v10\n"
"v_sub_f32 v112,v112,v10\n"
"v_sub_f32 v113,v113,v10\n"
"v_sub_f32 v114,v114,v10\n"
"v_sub_f32 v115,v115,v10\n"
"v_exp_f32 v72,v72\n"
"v_exp_f32 v73,v73\n"
"v_exp_f32 v74,v74\n"
"v_exp_f32 v75,v75\n"
"v_exp_f32 v112,v112\n"
"v_exp_f32 v113,v113\n"
"v_exp_f32 v114,v114\n"
"v_exp_f32 v115,v115\n"
"v_add_f32 v52,v52,v72\n"
"v_add_f32 v52,v52,v73\n"
"v_add_f32 v52,v52,v74\n"
"v_add_f32 v52,v52,v75\n"
"v_add_f32 v52,v52,v112\n"
"v_add_f32 v52,v52,v113\n"
"v_add_f32 v52,v52,v114\n"
"v_add_f32 v52,v52,v115\n"
CV "v72,v72,v73\n"
CV "v73,v74,v75\n"
CV "v74,v112,v113\n"
CV "v75,v114,v115\n"
"v_max3_f32 v10,v76,v77,v78\n"
"v_max3_f32 v11,v79,v116,v117\n"
"v_max3_f32 v10,v10,v118,v119\n"
"v_max_f32 v10,v10,v11\n"
"v_mov_b32 v11,v10\n"
"s_nop 1\n"
"v_permlane16_swap_b32 v10,v11\n"
"v_max_f32 v10,v10,v11\n"
"v_mov_b32 v11,v10\n"
"s_nop 1\n"
"v_permlane32_swap_b32 v10,v11\n"
"v_max_f32 v10,v10,v11\n"
"v_add_f32 v10,0x41300000,v10\n"
"v_max_f32 v10,0,v10\n"
"v_exp_f32 v11,-v10\n"
AR "v12,a4\n"
"v_subrev_f32 v12,s13,v12\n"
"v_fma_f32 v12,v12,v11,s13\n"
AW "a4,v12\n"
AR "v12,a24\n"
"v_subrev_f32 v12,s13,v12\n"
"v_fma_f32 v12,v12,v11,s13\n"
AW "a24,v12\n"
AR "v12,a44\n"
"v_subrev_f32 v12,s13,v12\n"
"v_fma_f32 v12,v12,v11,s13\n"
AW "a44,v12\n"
AR "v12,a64\n"
"v_subrev_f32 v12,s13,v12\n"
"v_fma_f32 v12,v12,v11,s13\n"
AW "a64,v12\n"
AR "v12,a84\n"
"v_subrev_f32 v12,s13,v12\n"
"v_fma_f32 v12,v12,v11,s13\n"
AW "a84,v12\n"
AR "v12,a104\n"
"v_subrev_f32 v12,s13,v12\n"
"v_fma_f32 v12,v12,v11,s13\n"
AW "a104,v12\n"
AR "v12,a124\n"
"v_subrev_f32 v12,s13,v12\n"
"v_fma_f32 v12,v12,v11,s13\n"
AW "a124,v12\n"
AR "v12,a144\n"
"v_subrev_f32 v12,s13,v12\n"
"v_fma_f32 v12,v12,v11,s13\n"
AW "a144,v12\n"
AR "v12,a164\n"
"v_subrev_f32 v12,s13,v12\n"
"v_fma_f32 v12,v12,v11,s13\n"
AW "a164,v12\n"
AR "v12,a184\n"
"v_subrev_f32 v12,s13,v12\n"
"v_fma_f32 v12,v12,v11,s13\n"
AW "a184,v12\n"
AR "v12,a204\n"
"v_subrev_f32 v12,s13,v12\n"
"v_fma_f32 v12,v12,v11,s13\n"
AW "a204,v12\n"
AR "v12,a224\n"
"v_subrev_f32 v12,s13,v12\n"
"v_fma_f32 v12,v12,v11,s13\n"
AW "a224,v12\n"
AR "v12,a5\n"
"v_subrev_f32 v12,s13,v12\n"
"v_fma_f32 v12,v12,v11,s13\n"
AW "a5,v12\n"
AR "v12,a25\n"
"v_subrev_f32 v12,s13,v12\n"
"v_fma_f32 v12,v12,v11,s13\n"
AW "a25,v12\n"
AR "v12,a45\n"
"v_subrev_f32 v12,s13,v12\n"
"v_fma_f32 v12,v12,v11,s13\n"
AW "a45,v12\n"
AR "v12,a65\n"
"v_subrev_f32 v12,s13,v12\n"
"v_fma_f32 v12,v12,v11,s13\n"
AW "a65,v12\n"
AR "v12,a85\n"
"v_subrev_f32 v12,s13,v12\n"
"v_fma_f32 v12,v12,v11,s13\n"
AW "a85,v12\n"
AR "v12,a105\n"
"v_subrev_f32 v12,s13,v12\n"
"v_fma_f32 v12,v12,v11,s13\n"
AW "a105,v12\n"
AR "v12,a125\n"
"v_subrev_f32 v12,s13,v12\n"
"v_fma_f32 v12,v12,v11,s13\n"
AW "a125,v12\n"
AR "v12,a145\n"
"v_subrev_f32 v12,s13,v12\n"
"v_fma_f32 v12,v12,v11,s13\n"
AW "a145,v12\n"
AR "v12,a165\n"
"v_subrev_f32 v12,s13,v12\n"
"v_fma_f32 v12,v12,v11,s13\n"
AW "a165,v12\n"
AR "v12,a185\n"
"v_subrev_f32 v12,s13,v12\n"
"v_fma_f32 v12,v12,v11,s13\n"
AW "a185,v12\n"
AR "v12,a205\n"
"v_subrev_f32 v12,s13,v12\n"
"v_fma_f32 v12,v12,v11,s13\n"
AW "a205,v12\n"
AR "v12,a225\n"
"v_subrev_f32 v12,s13,v12\n"
"v_fma_f32 v12,v12,v11,s13\n"
AW "a225,v12\n"
AR "v12,a6\n"
"v_subrev_f32 v12,s13,v12\n"
"v_fma_f32 v12,v12,v11,s13\n"
AW "a6,v12\n"
AR "v12,a26\n"
"v_subrev_f32 v12,s13,v12\n"
"v_fma_f32 v12,v12,v11,s13\n"
AW "a26,v12\n"
AR "v12,a46\n"
"v_subrev_f32 v12,s13,v12\n"
"v_fma_f32 v12,v12,v11,s13\n"
AW "a46,v12\n"
AR "v12,a66\n"
"v_subrev_f32 v12,s13,v12\n"
"v_fma_f32 v12,v12,v11,s13\n"
AW "a66,v12\n"
AR "v12,a86\n"
"v_subrev_f32 v12,s13,v12\n"
"v_fma_f32 v12,v12,v11,s13\n"
AW "a86,v12\n"
AR "v12,a106\n"
"v_subrev_f32 v12,s13,v12\n"
"v_fma_f32 v12,v12,v11,s13\n"
AW "a106,v12\n"
AR "v12,a126\n"
"v_subrev_f32 v12,s13,v12\n"
"v_fma_f32 v12,v12,v11,s13\n"
AW "a126,v12\n"
AR "v12,a146\n"
"v_subrev_f32 v12,s13,v12\n"
"v_fma_f32 v12,v12,v11,s13\n"
AW "a146,v12\n"
AR "v12,a166\n"
"v_subrev_f32 v12,s13,v12\n"
"v_fma_f32 v12,v12,v11,s13\n"
AW "a166,v12\n"
AR "v12,a186\n"
"v_subrev_f32 v12,s13,v12\n"
"v_fma_f32 v12,v12,v11,s13\n"
AW "a186,v12\n"
AR "v12,a206\n"
"v_subrev_f32 v12,s13,v12\n"
"v_fma_f32 v12,v12,v11,s13\n"
AW "a206,v12\n"
AR "v12,a226\n"
"v_subrev_f32 v12,s13,v12\n"
"v_fma_f32 v12,v12,v11,s13\n"
AW "a226,v12\n"
AR "v12,a7\n"
"v_subrev_f32 v12,s13,v12\n"
"v_fma_f32 v12,v12,v11,s13\n"
AW "a7,v12\n"
AR "v12,a27\n"
"v_subrev_f32 v12,s13,v12\n"
"v_fma_f32 v12,v12,v11,s13\n"
AW "a27,v12\n"
AR "v12,a47\n"
"v_subrev_f32 v12,s13,v12\n"
"v_fma_f32 v12,v12,v11,s13\n"
AW "a47,v12\n"
AR "v12,a67\n"
"v_subrev_f32 v12,s13,v12\n"
"v_fma_f32 v12,v12,v11,s13\n"
AW "a67,v12\n"
AR "v12,a87\n"
"v_subrev_f32 v12,s13,v12\n"
"v_fma_f32 v12,v12,v11,s13\n"
AW "a87,v12\n"
AR "v12,a107\n"
"v_subrev_f32 v12,s13,v12\n"
"v_fma_f32 v12,v12,v11,s13\n"
AW "a107,v12\n"
AR "v12,a127\n"
"v_subrev_f32 v12,s13,v12\n"
"v_fma_f32 v12,v12,v11,s13\n"
AW "a127,v12\n"
AR "v12,a147\n"
"v_subrev_f32 v12,s13,v12\n"
"v_fma_f32 v12,v12,v11,s13\n"
AW "a147,v12\n"
AR "v12,a167\n"
"v_subrev_f32 v12,s13,v12\n"
"v_fma_f32 v12,v12,v11,s13\n"
AW "a167,v12\n"
AR "v12,a187\n"
"v_subrev_f32 v12,s13,v12\n"
"v_fma_f32 v12,v12,v11,s13\n"
AW "a187,v12\n"
AR "v12,a207\n"
"v_subrev_f32 v12,s13,v12\n"
"v_fma_f32 v12,v12,v11,s13\n"
AW "a207,v12\n"
AR "v12,a227\n"
"v_subrev_f32 v12,s13,v12\n"
"v_fma_f32 v12,v12,v11,s13\n"
AW "a227,v12\n"
"v_mul_f32 v53,v58,v11\n"
"v_sub_f32 v36,v36,v10\n"
"v_sub_f32 v37,v37,v10\n"
"v_sub_f32 v38,v38,v10\n"
"v_sub_f32 v39,v39,v10\n"
"v_sub_f32 v96,v96,v10\n"
"v_sub_f32 v97,v97,v10\n"
"v_sub_f32 v98,v98,v10\n"
"v_sub_f32 v99,v99,v10\n"
"v_sub_f32 v76,v76,v10\n"
"v_sub_f32 v77,v77,v10\n"
"v_sub_f32 v78,v78,v10\n"
"v_sub_f32 v79,v79,v10\n"
"v_sub_f32 v116,v116,v10\n"
"v_sub_f32 v117,v117,v10\n"
"v_sub_f32 v118,v118,v10\n"
"v_sub_f32 v119,v119,v10\n"
"v_exp_f32 v76,v76\n"
"v_exp_f32 v77,v77\n"
"v_exp_f32 v78,v78\n"
"v_exp_f32 v79,v79\n"
"v_exp_f32 v116,v116\n"
"v_exp_f32 v117,v117\n"
"v_exp_f32 v118,v118\n"
"v_exp_f32 v119,v119\n"
"v_add_f32 v53,v53,v76\n"
"v_add_f32 v53,v53,v77\n"
"v_add_f32 v53,v53,v78\n"
"v_add_f32 v53,v53,v79\n"
"v_add_f32 v53,v53,v116\n"
"v_add_f32 v53,v53,v117\n"
"v_add_f32 v53,v53,v118\n"
"v_add_f32_e64 v53,v53,v119\n"
CV "v76,v76,v77\n"
CV "v77,v78,v79\n"
CV "v78,v116,v117\n"
CV "v79,v118,v119\n"
"v_max3_f32 v10,v80,v81,v82\n"
"v_max3_f32 v11,v83,v120,v121\n"
"v_max3_f32 v10,v10,v122,v123\n"
"v_max_f32 v10,v10,v11\n"
"v_mov_b32 v11,v10\n"
"s_nop 1\n"
"v_permlane16_swap_b32 v10,v11\n"
"v_max_f32 v10,v10,v11\n"
"v_mov_b32 v11,v10\n"
"s_nop 1\n"
"v_permlane32_swap_b32 v10,v11\n"
"v_max_f32 v10,v10,v11\n"
"v_add_f32 v10,0x41300000,v10\n"
"v_max_f32 v10,0,v10\n"
"v_exp_f32 v11,-v10\n"
AR "v12,a8\n"
"v_subrev_f32 v12,s13,v12\n"
"v_fma_f32 v12,v12,v11,s13\n"
AW "a8,v12\n"
AR "v12,a28\n"
"v_subrev_f32 v12,s13,v12\n"
"v_fma_f32 v12,v12,v11,s13\n"
AW "a28,v12\n"
AR "v12,a48\n"
"v_subrev_f32 v12,s13,v12\n"
"v_fma_f32 v12,v12,v11,s13\n"
AW "a48,v12\n"
AR "v12,a68\n"
"v_subrev_f32 v12,s13,v12\n"
"v_fma_f32 v12,v12,v11,s13\n"
AW "a68,v12\n"
AR "v12,a88\n"
"v_subrev_f32 v12,s13,v12\n"
"v_fma_f32 v12,v12,v11,s13\n"
AW "a88,v12\n"
AR "v12,a108\n"
"v_subrev_f32 v12,s13,v12\n"
"v_fma_f32 v12,v12,v11,s13\n"
AW "a108,v12\n"
AR "v12,a128\n"
"v_subrev_f32 v12,s13,v12\n"
"v_fma_f32 v12,v12,v11,s13\n"
AW "a128,v12\n"
AR "v12,a148\n"
"v_subrev_f32 v12,s13,v12\n"
"v_fma_f32 v12,v12,v11,s13\n"
AW "a148,v12\n"
AR "v12,a168\n"
"v_subrev_f32 v12,s13,v12\n"
"v_fma_f32 v12,v12,v11,s13\n"
AW "a168,v12\n"
AR "v12,a188\n"
"v_subrev_f32 v12,s13,v12\n"
"v_fma_f32 v12,v12,v11,s13\n"
AW "a188,v12\n"
AR "v12,a208\n"
"v_subrev_f32 v12,s13,v12\n"
"v_fma_f32 v12,v12,v11,s13\n"
AW "a208,v12\n"
AR "v12,a228\n"
"v_subrev_f32 v12,s13,v12\n"
"v_fma_f32 v12,v12,v11,s13\n"
AW "a228,v12\n"
AR "v12,a9\n"
"v_subrev_f32 v12,s13,v12\n"
"v_fma_f32 v12,v12,v11,s13\n"
AW "a9,v12\n"
AR "v12,a29\n"
"v_subrev_f32 v12,s13,v12\n"
"v_fma_f32 v12,v12,v11,s13\n"
AW "a29,v12\n"
AR "v12,a49\n"
"v_subrev_f32 v12,s13,v12\n"
"v_fma_f32 v12,v12,v11,s13\n"
AW "a49,v12\n"
AR "v12,a69\n"
"v_subrev_f32 v12,s13,v12\n"
"v_fma_f32 v12,v12,v11,s13\n"
AW "a69,v12\n"
AR "v12,a89\n"
"v_subrev_f32 v12,s13,v12\n"
"v_fma_f32 v12,v12,v11,s13\n"
AW "a89,v12\n"
AR "v12,a109\n"
"v_subrev_f32 v12,s13,v12\n"
"v_fma_f32 v12,v12,v11,s13\n"
AW "a109,v12\n"
AR "v12,a129\n"
"v_subrev_f32 v12,s13,v12\n"
"v_fma_f32 v12,v12,v11,s13\n"
AW "a129,v12\n"
AR "v12,a149\n"
"v_subrev_f32 v12,s13,v12\n"
"v_fma_f32 v12,v12,v11,s13\n"
AW "a149,v12\n"
AR "v12,a169\n"
"v_subrev_f32 v12,s13,v12\n"
"v_fma_f32 v12,v12,v11,s13\n"
AW "a169,v12\n"
AR "v12,a189\n"
"v_subrev_f32 v12,s13,v12\n"
"v_fma_f32 v12,v12,v11,s13\n"
AW "a189,v12\n"
AR "v12,a209\n"
"v_subrev_f32 v12,s13,v12\n"
"v_fma_f32 v12,v12,v11,s13\n"
AW "a209,v12\n"
AR "v12,a229\n"
"v_subrev_f32 v12,s13,v12\n"
"v_fma_f32 v12,v12,v11,s13\n"
AW "a229,v12\n"
AR "v12,a10\n"
"v_subrev_f32 v12,s13,v12\n"
"v_fma_f32 v12,v12,v11,s13\n"
AW "a10,v12\n"
AR "v12,a30\n"
"v_subrev_f32 v12,s13,v12\n"
"v_fma_f32 v12,v12,v11,s13\n"
AW "a30,v12\n"
AR "v12,a50\n"
"v_subrev_f32 v12,s13,v12\n"
"v_fma_f32 v12,v12,v11,s13\n"
AW "a50,v12\n"
AR "v12,a70\n"
"v_subrev_f32 v12,s13,v12\n"
"v_fma_f32 v12,v12,v11,s13\n"
AW "a70,v12\n"
AR "v12,a90\n"
"v_subrev_f32 v12,s13,v12\n"
"v_fma_f32 v12,v12,v11,s13\n"
AW "a90,v12\n"
AR "v12,a110\n"
"v_subrev_f32 v12,s13,v12\n"
"v_fma_f32 v12,v12,v11,s13\n"
AW "a110,v12\n"
AR "v12,a130\n"
"v_subrev_f32 v12,s13,v12\n"
"v_fma_f32 v12,v12,v11,s13\n"
AW "a130,v12\n"
AR "v12,a150\n"
"v_subrev_f32 v12,s13,v12\n"
"v_fma_f32 v12,v12,v11,s13\n"
AW "a150,v12\n"
AR "v12,a170\n"
"v_subrev_f32 v12,s13,v12\n"
"v_fma_f32 v12,v12,v11,s13\n"
AW "a170,v12\n"
AR "v12,a190\n"
"v_subrev_f32 v12,s13,v12\n"
"v_fma_f32 v12,v12,v11,s13\n"
AW "a190,v12\n"
AR "v12,a210\n"
"v_subrev_f32 v12,s13,v12\n"
"v_fma_f32 v12,v12,v11,s13\n"
AW "a210,v12\n"
AR "v12,a230\n"
"v_subrev_f32 v12,s13,v12\n"
"v_fma_f32 v12,v12,v11,s13\n"
AW "a230,v12\n"
AR "v12,a11\n"
"v_subrev_f32 v12,s13,v12\n"
"v_fma_f32 v12,v12,v11,s13\n"
AW "a11,v12\n"
AR "v12,a31\n"
"v_subrev_f32 v12,s13,v12\n"
"v_fma_f32 v12,v12,v11,s13\n"
AW "a31,v12\n"
AR "v12,a51\n"
"v_subrev_f32 v12,s13,v12\n"
"v_fma_f32 v12,v12,v11,s13\n"
AW "a51,v12\n"
AR "v12,a71\n"
"v_subrev_f32 v12,s13,v12\n"
"v_fma_f32 v12,v12,v11,s13\n"
AW "a71,v12\n"
AR "v12,a91\n"
"v_subrev_f32 v12,s13,v12\n"
"v_fma_f32 v12,v12,v11,s13\n"
AW "a91,v12\n"
AR "v12,a111\n"
"v_subrev_f32 v12,s13,v12\n"
"v_fma_f32 v12,v12,v11,s13\n"
AW "a111,v12\n"
AR "v12,a131\n"
"v_subrev_f32 v12,s13,v12\n"
"v_fma_f32 v12,v12,v11,s13\n"
AW "a131,v12\n"
AR "v12,a151\n"
"v_subrev_f32 v12,s13,v12\n"
"v_fma_f32 v12,v12,v11,s13\n"
AW "a151,v12\n"
AR "v12,a171\n"
"v_subrev_f32 v12,s13,v12\n"
"v_fma_f32 v12,v12,v11,s13\n"
AW "a171,v12\n"
AR "v12,a191\n"
"v_subrev_f32 v12,s13,v12\n"
"v_fma_f32 v12,v12,v11,s13\n"
AW "a191,v12\n"
AR "v12,a211\n"
"v_subrev_f32 v12,s13,v12\n"
"v_fma_f32 v12,v12,v11,s13\n"
AW "a211,v12\n"
AR "v12,a231\n"
"v_subrev_f32 v12,s13,v12\n"
"v_fma_f32 v12,v12,v11,s13\n"
AW "a231,v12\n"
"v_mul_f32 v54,v59,v11\n"
"v_sub_f32 v40,v40,v10\n"
"v_sub_f32 v41,v41,v10\n"
"v_sub_f32 v42,v42,v10\n"
"v_sub_f32 v43,v43,v10\n"
"v_sub_f32 v100,v100,v10\n"
"v_sub_f32 v101,v101,v10\n"
"v_sub_f32 v102,v102,v10\n"
"v_sub_f32 v103,v103,v10\n"
"v_sub_f32 v80,v80,v10\n"
"v_sub_f32 v81,v81,v10\n"
"v_sub_f32 v82,v82,v10\n"
"v_sub_f32 v83,v83,v10\n"
"v_sub_f32 v120,v120,v10\n"
"v_sub_f32 v121,v121,v10\n"
"v_sub_f32 v122,v122,v10\n"
"v_sub_f32 v123,v123,v10\n"
"v_exp_f32 v80,v80\n"
"v_exp_f32 v81,v81\n"
"v_exp_f32 v82,v82\n"
"v_exp_f32 v83,v83\n"
"v_exp_f32 v120,v120\n"
"v_exp_f32 v121,v121\n"
"v_exp_f32 v122,v122\n"
"v_exp_f32 v123,v123\n"
"v_add_f32 v54,v54,v80\n"
"v_add_f32 v54,v54,v81\n"
"v_add_f32 v54,v54,v82\n"
"v_add_f32 v54,v54,v83\n"
"v_add_f32 v54,v54,v120\n"
"v_add_f32 v54,v54,v121\n"
"v_add_f32 v54,v54,v122\n"
"v_add_f32_e64 v54,v54,v123\n"
CV "v80,v80,v81\n"
CV "v81,v82,v83\n"
CV "v82,v120,v121\n"
CV "v83,v122,v123\n"
"v_max3_f32 v10,v84,v85,v86\n"
"v_max3_f32 v11,v87,v124,v125\n"
"v_max3_f32 v10,v10,v126,v127\n"
"v_max_f32 v10,v10,v11\n"
"v_mov_b32 v11,v10\n"
"s_nop 1\n"
"v_permlane16_swap_b32 v10,v11\n"
"v_max_f32 v10,v10,v11\n"
"v_mov_b32 v11,v10\n"
"s_nop 1\n"
"v_permlane32_swap_b32 v10,v11\n"
"v_max_f32 v10,v10,v11\n"
"v_add_f32 v10,0x41300000,v10\n"
"v_max_f32 v10,0,v10\n"
"v_exp_f32 v11,-v10\n"
AR "v12,a12\n"
"v_subrev_f32 v12,s13,v12\n"
"v_fma_f32 v12,v12,v11,s13\n"
AW "a12,v12\n"
AR "v12,a32\n"
"v_subrev_f32 v12,s13,v12\n"
"v_fma_f32 v12,v12,v11,s13\n"
AW "a32,v12\n"
AR "v12,a52\n"
"v_subrev_f32 v12,s13,v12\n"
"v_fma_f32 v12,v12,v11,s13\n"
AW "a52,v12\n"
AR "v12,a72\n"
"v_subrev_f32 v12,s13,v12\n"
"v_fma_f32 v12,v12,v11,s13\n"
AW "a72,v12\n"
AR "v12,a92\n"
"v_subrev_f32 v12,s13,v12\n"
"v_fma_f32 v12,v12,v11,s13\n"
AW "a92,v12\n"
AR "v12,a112\n"
"v_subrev_f32 v12,s13,v12\n"
"v_fma_f32 v12,v12,v11,s13\n"
AW "a112,v12\n"
AR "v12,a132\n"
"v_subrev_f32 v12,s13,v12\n"
"v_fma_f32 v12,v12,v11,s13\n"
AW "a132,v12\n"
AR "v12,a152\n"
"v_subrev_f32 v12,s13,v12\n"
"v_fma_f32 v12,v12,v11,s13\n"
AW "a152,v12\n"
AR "v12,a172\n"
"v_subrev_f32 v12,s13,v12\n"
"v_fma_f32 v12,v12,v11,s13\n"
AW "a172,v12\n"
AR "v12,a192\n"
"v_subrev_f32 v12,s13,v12\n"
"v_fma_f32 v12,v12,v11,s13\n"
AW "a192,v12\n"
AR "v12,a212\n"
"v_subrev_f32 v12,s13,v12\n"
"v_fma_f32 v12,v12,v11,s13\n"
AW "a212,v12\n"
AR "v12,a232\n"
"v_subrev_f32 v12,s13,v12\n"
"v_fma_f32 v12,v12,v11,s13\n"
AW "a232,v12\n"
AR "v12,a13\n"
"v_subrev_f32 v12,s13,v12\n"
"v_fma_f32 v12,v12,v11,s13\n"
AW "a13,v12\n"
AR "v12,a33\n"
"v_subrev_f32 v12,s13,v12\n"
"v_fma_f32 v12,v12,v11,s13\n"
AW "a33,v12\n"
AR "v12,a53\n"
"v_subrev_f32 v12,s13,v12\n"
"v_fma_f32 v12,v12,v11,s13\n"
AW "a53,v12\n"
AR "v12,a73\n"
"v_subrev_f32 v12,s13,v12\n"
"v_fma_f32 v12,v12,v11,s13\n"
AW "a73,v12\n"
AR "v12,a93\n"
"v_subrev_f32 v12,s13,v12\n"
"v_fma_f32 v12,v12,v11,s13\n"
AW "a93,v12\n"
AR "v12,a113\n"
"v_subrev_f32 v12,s13,v12\n"
"v_fma_f32 v12,v12,v11,s13\n"
AW "a113,v12\n"
AR "v12,a133\n"
"v_subrev_f32 v12,s13,v12\n"
"v_fma_f32 v12,v12,v11,s13\n"
AW "a133,v12\n"
AR "v12,a153\n"
"v_subrev_f32 v12,s13,v12\n"
"v_fma_f32 v12,v12,v11,s13\n"
AW "a153,v12\n"
AR "v12,a173\n"
"v_subrev_f32 v12,s13,v12\n"
"v_fma_f32 v12,v12,v11,s13\n"
AW "a173,v12\n"
AR "v12,a193\n"
"v_subrev_f32 v12,s13,v12\n"
"v_fma_f32 v12,v12,v11,s13\n"
AW "a193,v12\n"
AR "v12,a213\n"
"v_subrev_f32 v12,s13,v12\n"
"v_fma_f32 v12,v12,v11,s13\n"
AW "a213,v12\n"
AR "v12,a233\n"
"v_subrev_f32 v12,s13,v12\n"
"v_fma_f32 v12,v12,v11,s13\n"
AW "a233,v12\n"
AR "v12,a14\n"
"v_subrev_f32 v12,s13,v12\n"
"v_fma_f32 v12,v12,v11,s13\n"
AW "a14,v12\n"
AR "v12,a34\n"
"v_subrev_f32 v12,s13,v12\n"
"v_fma_f32 v12,v12,v11,s13\n"
AW "a34,v12\n"
AR "v12,a54\n"
"v_subrev_f32 v12,s13,v12\n"
"v_fma_f32 v12,v12,v11,s13\n"
AW "a54,v12\n"
AR "v12,a74\n"
"v_subrev_f32 v12,s13,v12\n"
"v_fma_f32 v12,v12,v11,s13\n"
AW "a74,v12\n"
AR "v12,a94\n"
"v_subrev_f32 v12,s13,v12\n"
"v_fma_f32 v12,v12,v11,s13\n"
AW "a94,v12\n"
AR "v12,a114\n"
"v_subrev_f32 v12,s13,v12\n"
"v_fma_f32 v12,v12,v11,s13\n"
AW "a114,v12\n"
AR "v12,a134\n"
"v_subrev_f32 v12,s13,v12\n"
"v_fma_f32 v12,v12,v11,s13\n"
AW "a134,v12\n"
AR "v12,a154\n"
"v_subrev_f32 v12,s13,v12\n"
"v_fma_f32 v12,v12,v11,s13\n"
AW "a154,v12\n"
AR "v12,a174\n"
"v_subrev_f32 v12,s13,v12\n"
"v_fma_f32 v12,v12,v11,s13\n"
AW "a174,v12\n"
AR "v12,a194\n"
"v_subrev_f32 v12,s13,v12\n"
"v_fma_f32 v12,v12,v11,s13\n"
AW "a194,v12\n"
AR "v12,a214\n"
"v_subrev_f32 v12,s13,v12\n"
"v_fma_f32 v12,v12,v11,s13\n"
AW "a214,v12\n"
AR "v12,a234\n"
"v_subrev_f32 v12,s13,v12\n"
"v_fma_f32 v12,v12,v11,s13\n"
AW "a234,v12\n"
AR "v12,a15\n"
"v_subrev_f32 v12,s13,v12\n"
"v_fma_f32 v12,v12,v11,s13\n"
AW "a15,v12\n"
AR "v12,a35\n"
"v_subrev_f32 v12,s13,v12\n"
"v_fma_f32 v12,v12,v11,s13\n"
AW "a35,v12\n"
AR "v12,a55\n"
"v_subrev_f32 v12,s13,v12\n"
"v_fma_f32 v12,v12,v11,s13\n"
AW "a55,v12\n"
AR "v12,a75\n"
"v_subrev_f32 v12,s13,v12\n"
"v_fma_f32 v12,v12,v11,s13\n"
AW "a75,v12\n"
AR "v12,a95\n"
"v_subrev_f32 v12,s13,v12\n"
"v_fma_f32 v12,v12,v11,s13\n"
AW "a95,v12\n"
AR "v12,a115\n"
"v_subrev_f32 v12,s13,v12\n"
"v_fma_f32 v12,v12,v11,s13\n"
AW "a115,v12\n"
AR "v12,a135\n"
"v_subrev_f32 v12,s13,v12\n"
"v_fma_f32 v12,v12,v11,s13\n"
AW "a135,v12\n"
AR "v12,a155\n"
"v_subrev_f32 v12,s13,v12\n"
"v_fma_f32 v12,v12,v11,s13\n"
AW "a155,v12\n"
AR "v12,a175\n"
"v_subrev_f32 v12,s13,v12\n"
"v_fma_f32 v12,v12,v11,s13\n"
AW "a175,v12\n"
AR "v12,a195\n"
"v_subrev_f32 v12,s13,v12\n"
"v_fma_f32 v12,v12,v11,s13\n"
AW "a195,v12\n"
AR "v12,a215\n"
"v_subrev_f32 v12,s13,v12\n"
"v_fma_f32 v12,v12,v11,s13\n"
AW "a215,v12\n"
AR "v12,a235\n"
"v_subrev_f32 v12,s13,v12\n"
"v_fma_f32 v12,v12,v11,s13\n"
AW "a235,v12\n"
"v_mul_f32 v55,v60,v11\n"
"v_sub_f32 v44,v44,v10\n"
"v_sub_f32 v45,v45,v10\n"
"v_sub_f32 v46,v46,v10\n"
"v_sub_f32 v47,v47,v10\n"
"v_sub_f32 v104,v104,v10\n"
"v_sub_f32 v105,v105,v10\n"
"v_sub_f32 v106,v106,v10\n"
"v_sub_f32 v107,v107,v10\n"
"v_sub_f32 v84,v84,v10\n"
"v_sub_f32 v85,v85,v10\n"
"v_sub_f32 v86,v86,v10\n"
"v_sub_f32 v87,v87,v10\n"
"v_sub_f32 v124,v124,v10\n"
"v_sub_f32 v125,v125,v10\n"
"v_sub_f32 v126,v126,v10\n"
"v_sub_f32 v127,v127,v10\n"
"v_exp_f32 v84,v84\n"
"v_exp_f32 v85,v85\n"
"v_exp_f32 v86,v86\n"
"v_exp_f32 v87,v87\n"
"v_exp_f32 v124,v124\n"
"v_exp_f32 v125,v125\n"
"v_exp_f32 v126,v126\n"
"v_exp_f32 v127,v127\n"
"v_add_f32 v55,v55,v84\n"
"v_add_f32 v55,v55,v85\n"
"v_add_f32 v55,v55,v86\n"
"v_add_f32 v55,v55,v87\n"
"v_add_f32 v55,v55,v124\n"
"v_add_f32 v55,v55,v125\n"
"v_add_f32 v55,v55,v126\n"
"v_add_f32_e64 v55,v55,v127\n"
CV "v84,v84,v85\n"
CV "v85,v86,v87\n"
CV "v86,v124,v125\n"
CV "v87,v126,v127\n"
"v_max3_f32 v10,v88,v89,v90\n"
"v_max3_f32 v11,v91,v128,v129\n"
"v_max3_f32 v10,v10,v130,v131\n"
"v_max_f32 v10,v10,v11\n"
"v_mov_b32 v11,v10\n"
"s_nop 1\n"
"v_permlane16_swap_b32 v10,v11\n"
"v_max_f32 v10,v10,v11\n"
"v_mov_b32 v11,v10\n"
"s_nop 1\n"
"v_permlane32_swap_b32 v10,v11\n"
"v_max_f32_e64 v10,v10,v11\n"
"v_add_f32 v10,0x41300000,v10\n"
"v_max_f32 v10,0,v10\n"
"v_exp_f32 v11,-v10\n"
AR "v12,a16\n"
"v_subrev_f32 v12,s13,v12\n"
"v_fma_f32 v12,v12,v11,s13\n"
AW "a16,v12\n"
AR "v12,a36\n"
"v_subrev_f32 v12,s13,v12\n"
"v_fma_f32 v12,v12,v11,s13\n"
AW "a36,v12\n"
AR "v12,a56\n"
"v_subrev_f32 v12,s13,v12\n"
"v_fma_f32 v12,v12,v11,s13\n"
AW "a56,v12\n"
AR "v12,a76\n"
"v_subrev_f32 v12,s13,v12\n"
"v_fma_f32 v12,v12,v11,s13\n"
AW "a76,v12\n"
AR "v12,a96\n"
"v_subrev_f32 v12,s13,v12\n"
"v_fma_f32 v12,v12,v11,s13\n"
AW "a96,v12\n"
AR "v12,a116\n"
"v_subrev_f32 v12,s13,v12\n"
"v_fma_f32 v12,v12,v11,s13\n"
AW "a116,v12\n"
AR "v12,a136\n"
"v_subrev_f32 v12,s13,v12\n"
"v_fma_f32 v12,v12,v11,s13\n"
AW "a136,v12\n"
AR "v12,a156\n"
"v_subrev_f32 v12,s13,v12\n"
"v_fma_f32 v12,v12,v11,s13\n"
AW "a156,v12\n"
AR "v12,a176\n"
"v_subrev_f32 v12,s13,v12\n"
"v_fma_f32 v12,v12,v11,s13\n"
AW "a176,v12\n"
AR "v12,a196\n"
"v_subrev_f32 v12,s13,v12\n"
"v_fma_f32 v12,v12,v11,s13\n"
AW "a196,v12\n"
AR "v12,a216\n"
"v_subrev_f32 v12,s13,v12\n"
"v_fma_f32 v12,v12,v11,s13\n"
AW "a216,v12\n"
AR "v12,a236\n"
"v_subrev_f32 v12,s13,v12\n"
"v_fma_f32 v12,v12,v11,s13\n"
AW "a236,v12\n"
AR "v12,a17\n"
"v_subrev_f32 v12,s13,v12\n"
"v_fma_f32 v12,v12,v11,s13\n"
AW "a17,v12\n"
AR "v12,a37\n"
"v_subrev_f32 v12,s13,v12\n"
"v_fma_f32 v12,v12,v11,s13\n"
AW "a37,v12\n"
AR "v12,a57\n"
"v_subrev_f32 v12,s13,v12\n"
"v_fma_f32 v12,v12,v11,s13\n"
AW "a57,v12\n"
AR "v12,a77\n"
"v_subrev_f32 v12,s13,v12\n"
"v_fma_f32 v12,v12,v11,s13\n"
AW "a77,v12\n"
AR "v12,a97\n"
"v_subrev_f32 v12,s13,v12\n"
"v_fma_f32 v12,v12,v11,s13\n"
AW "a97,v12\n"
AR "v12,a117\n"
"v_subrev_f32 v12,s13,v12\n"
"v_fma_f32 v12,v12,v11,s13\n"
AW "a117,v12\n"
AR "v12,a137\n"
"v_subrev_f32 v12,s13,v12\n"
"v_fma_f32 v12,v12,v11,s13\n"
AW "a137,v12\n"
AR "v12,a157\n"
"v_subrev_f32 v12,s13,v12\n"
"v_fma_f32 v12,v12,v11,s13\n"
AW "a157,v12\n"
AR "v12,a177\n"
"v_subrev_f32 v12,s13,v12\n"
"v_fma_f32 v12,v12,v11,s13\n"
AW "a177,v12\n"
AR "v12,a197\n"
"v_subrev_f32 v12,s13,v12\n"
"v_fma_f32 v12,v12,v11,s13\n"
AW "a197,v12\n"
AR "v12,a217\n"
"v_subrev_f32 v12,s13,v12\n"
"v_fma_f32 v12,v12,v11,s13\n"
AW "a217,v12\n"
AR "v12,a237\n"
"v_subrev_f32 v12,s13,v12\n"
"v_fma_f32 v12,v12,v11,s13\n"
AW "a237,v12\n"
AR "v12,a18\n"
"v_subrev_f32 v12,s13,v12\n"
"v_fma_f32 v12,v12,v11,s13\n"
AW "a18,v12\n"
AR "v12,a38\n"
"v_subrev_f32 v12,s13,v12\n"
"v_fma_f32 v12,v12,v11,s13\n"
AW "a38,v12\n"
AR "v12,a58\n"
"v_subrev_f32 v12,s13,v12\n"
"v_fma_f32 v12,v12,v11,s13\n"
AW "a58,v12\n"
AR "v12,a78\n"
"v_subrev_f32 v12,s13,v12\n"
"v_fma_f32 v12,v12,v11,s13\n"
AW "a78,v12\n"
AR "v12,a98\n"
"v_subrev_f32 v12,s13,v12\n"
"v_fma_f32 v12,v12,v11,s13\n"
AW "a98,v12\n"
AR "v12,a118\n"
"v_subrev_f32 v12,s13,v12\n"
"v_fma_f32 v12,v12,v11,s13\n"
AW "a118,v12\n"
AR "v12,a138\n"
"v_subrev_f32 v12,s13,v12\n"
"v_fma_f32 v12,v12,v11,s13\n"
AW "a138,v12\n"
AR "v12,a158\n"
"v_subrev_f32 v12,s13,v12\n"
"v_fma_f32 v12,v12,v11,s13\n"
AW "a158,v12\n"
AR "v12,a178\n"
"v_subrev_f32 v12,s13,v12\n"
"v_fma_f32 v12,v12,v11,s13\n"
AW "a178,v12\n"
AR "v12,a198\n"
"v_subrev_f32 v12,s13,v12\n"
"v_fma_f32 v12,v12,v11,s13\n"
AW "a198,v12\n"
AR "v12,a218\n"
"v_subrev_f32 v12,s13,v12\n"
"v_fma_f32 v12,v12,v11,s13\n"
AW "a218,v12\n"
AR "v12,a238\n"
"v_subrev_f32 v12,s13,v12\n"
"v_fma_f32 v12,v12,v11,s13\n"
AW "a238,v12\n"
AR "v12,a19\n"
"v_subrev_f32 v12,s13,v12\n"
"v_fma_f32 v12,v12,v11,s13\n"
AW "a19,v12\n"
AR "v12,a39\n"
"v_subrev_f32 v12,s13,v12\n"
"v_fma_f32 v12,v12,v11,s13\n"
AW "a39,v12\n"
AR "v12,a59\n"
"v_subrev_f32 v12,s13,v12\n"
"v_fma_f32 v12,v12,v11,s13\n"
AW "a59,v12\n"
AR "v12,a79\n"
"v_subrev_f32 v12,s13,v12\n"
"v_fma_f32 v12,v12,v11,s13\n"
AW "a79,v12\n"
AR "v12,a99\n"
"v_subrev_f32 v12,s13,v12\n"
"v_fma_f32 v12,v12,v11,s13\n"
AW "a99,v12\n"
AR "v12,a119\n"
"v_subrev_f32 v12,s13,v12\n"
"v_fma_f32 v12,v12,v11,s13\n"
AW "a119,v12\n"
AR "v12,a139\n"
"v_subrev_f32 v12,s13,v12\n"
"v_fma_f32 v12,v12,v11,s13\n"
AW "a139,v12\n"
AR "v12,a159\n"
"v_subrev_f32 v12,s13,v12\n"
"v_fma_f32 v12,v12,v11,s13\n"
AW "a159,v12\n"
AR "v12,a179\n"
"v_subrev_f32 v12,s13,v12\n"
"v_fma_f32 v12,v12,v11,s13\n"
AW "a179,v12\n"
AR "v12,a199\n"
"v_subrev_f32 v12,s13,v12\n"
"v_fma_f32 v12,v12,v11,s13\n"
AW "a199,v12\n"
AR "v12,a219\n"
"v_subrev_f32 v12,s13,v12\n"
"v_fma_f32 v12,v12,v11,s13\n"
AW "a219,v12\n"
AR "v12,a239\n"
"v_subrev_f32 v12,s13,v12\n"
"v_fma_f32 v12,v12,v11,s13\n"
AW "a239,v12\n"
"v_mul_f32 v56,v61,v11\n"
"v_sub_f32 v48,v48,v10\n"
"v_sub_f32 v49,v49,v10\n"
"v_sub_f32 v50,v50,v10\n"
"v_sub_f32 v51,v51,v10\n"
"v_sub_f32 v108,v108,v10\n"
"v_sub_f32 v109,v109,v10\n"
"v_sub_f32 v110,v110,v10\n"
"v_sub_f32 v111,v111,v10\n"
"v_sub_f32 v88,v88,v10\n"
"v_sub_f32 v89,v89,v10\n"
"v_sub_f32 v90,v90,v10\n"
"v_sub_f32 v91,v91,v10\n"
"v_sub_f32 v128,v128,v10\n"
"v_sub_f32 v129,v129,v10\n"
"v_sub_f32 v130,v130,v10\n"
"v_sub_f32 v131,v131,v10\n"
"v_exp_f32 v88,v88\n"
"v_exp_f32 v89,v89\n"
"v_exp_f32 v90,v90\n"
"v_exp_f32 v91,v91\n"
"v_exp_f32 v128,v128\n"
"v_exp_f32 v129,v129\n"
"v_exp_f32 v130,v130\n"
"v_exp_f32 v131,v131\n"
"v_add_f32 v56,v56,v88\n"
"v_add_f32 v56,v56,v89\n"
"v_add_f32 v56,v56,v90\n"
"v_add_f32 v56,v56,v91\n"
"v_add_f32 v56,v56,v128\n"
"v_add_f32 v56,v56,v129\n"
"v_add_f32 v56,v56,v130\n"
"v_add_f32 v56,v56,v131\n"
CV "v88,v88,v89\n"
CV "v89,v90,v91\n"
CV "v90,v128,v129\n"
CV "v91,v130,v131\n"
"s_nop 7\n"
"s_setpc_b64 s[52:53]\n"
".Lslow_1:\n"
"s_nop 7\n"
"s_nop 7\n"
"v_and_b32 v10,240,v9\n"
"v_mul_u32_u24 v10,24,v10\n"
"v_lshrrev_b32 v11,4,v9\n"
"v_and_or_b32 v10,v11,48,v10\n"
"s_mov_b32 s59,s34\n"
"s_mov_b32 s34,s20\n"
"s_add_u32 s58,s74,0\n"
"s_nop 3\n"
LD "v[58:61],v10,s[32:35],s58 offen\n"
"s_waitcnt vmcnt(0)\n"
MF "v[92:95],v[58:61],v[136:139],v[32:35]\n"
MF "v[96:99],v[58:61],v[140:143],v[36:39]\n"
MF "v[100:103],v[58:61],v[144:147],v[40:43]\n"
MF "v[104:107],v[58:61],v[148:151],v[44:47]\n"
MF "v[108:111],v[58:61],v[152:155],v[48:51]\n"
"s_add_u32 s58,s74,0\n"
"s_nop 3\n"
LD "v[58:61],v10,s[32:35],s58 offen offset:64\n"
"s_waitcnt vmcnt(0)\n"
MF "v[92:95],v[58:61],v[156:159],v[92:95]\n"
MF "v[96:99],v[58:61],v[160:163],v[96:99]\n"
MF "v[100:103],v[58:61],v[164:167],v[100:103]\n"
MF "v[104:107],v[58:61],v[168:171],v[104:107]\n"
MF "v[108:111],v[58:61],v[172:175],v[108:111]\n"
"s_add_u32 s58,s74,0\n"
"s_nop 3\n"
LD "v[58:61],v10,s[32:35],s58 offen offset:128\n"
"s_waitcnt vmcnt(0)\n"
MF "v[92:95],v[58:61],v[176:179],v[92:95]\n"
MF "v[96:99],v[58:61],v[180:183],v[96:99]\n"
MF "v[100:103],v[58:61],v[184:187],v[100:103]\n"
MF "v[104:107],v[58:61],v[188:191],v[104:107]\n"
MF "v[108:111],v[58:61],v[192:195],v[108:111]\n"
"s_add_u32 s58,s74,0\n"
"s_nop 3\n"
LD "v[58:61],v10,s[32:35],s58 offen offset:192\n"
"s_waitcnt vmcnt(0)\n"
MF "v[92:95],v[58:61],v[196:199],v[92:95]\n"
MF "v[96:99],v[58:61],v[200:203],v[96:99]\n"
MF "v[100:103],v[58:61],v[204:207],v[100:103]\n"
MF "v[104:107],v[58:61],v[208:211],v[104:107]\n"
MF "v[108:111],v[58:61],v[212:215],v[108:111]\n"
"s_add_u32 s58,s74,0\n"
"s_nop 3\n"
LD "v[58:61],v10,s[32:35],s58 offen offset:256\n"
"s_waitcnt vmcnt(0)\n"
MF "v[92:95],v[58:61],v[216:219],v[92:95]\n"
MF "v[96:99],v[58:61],v[220:223],v[96:99]\n"
MF "v[100:103],v[58:61],v[224:227],v[100:103]\n"
MF "v[104:107],v[58:61],v[228:231],v[104:107]\n"
MF "v[108:111],v[58:61],v[232:235],v[108:111]\n"
"s_add_u32 s58,s74,0\n"
"s_nop 3\n"
LD "v[58:61],v10,s[32:35],s58 offen offset:320\n"
"s_waitcnt vmcnt(0)\n"
MF "v[92:95],v[58:61],v[236:239],v[92:95]\n"
MF "v[96:99],v[58:61],v[240:243],v[96:99]\n"
MF "v[100:103],v[58:61],v[244:247],v[100:103]\n"
MF "v[104:107],v[58:61],v[248:251],v[104:107]\n"
MF "v[108:111],v[58:61],v[252:255],v[108:111]\n"
"s_add_u32 s58,s74,6144\n"
"s_nop 3\n"
LD "v[58:61],v10,s[32:35],s58 offen\n"
"s_waitcnt vmcnt(0)\n"
MF "v[112:115],v[58:61],v[136:139],v[32:35]\n"
MF "v[116:119],v[58:61],v[140:143],v[36:39]\n"
MF "v[120:123],v[58:61],v[144:147],v[40:43]\n"
MF "v[124:127],v[58:61],v[148:151],v[44:47]\n"
MF "v[128:131],v[58:61],v[152:155],v[48:51]\n"
"s_add_u32 s58,s74,6144\n"
"s_nop 3\n"
LD "v[58:61],v10,s[32:35],s58 offen offset:64\n"
"s_waitcnt vmcnt(0)\n"
MF "v[112:115],v[58:61],v[156:159],v[112:115]\n"
MF "v[116:119],v[58:61],v[160:163],v[116:119]\n"
MF "v[120:123],v[58:61],v[164:167],v[120:123]\n"
MF "v[124:127],v[58:61],v[168:171],v[124:127]\n"
MF "v[128:131],v[58:61],v[172:175],v[128:131]\n"
"s_add_u32 s58,s74,6144\n"
"s_nop 3\n"
LD "v[58:61],v10,s[32:35],s58 offen offset:128\n"
"s_waitcnt vmcnt(0)\n"
MF "v[112:115],v[58:61],v[176:179],v[112:115]\n"
MF "v[116:119],v[58:61],v[180:183],v[116:119]\n"
MF "v[120:123],v[58:61],v[184:187],v[120:123]\n"
MF "v[124:127],v[58:61],v[188:191],v[124:127]\n"
MF "v[128:131],v[58:61],v[192:195],v[128:131]\n"
"s_add_u32 s58,s74,6144\n"
"s_nop 3\n"
LD "v[58:61],v10,s[32:35],s58 offen offset:192\n"
"s_waitcnt vmcnt(0)\n"
MF "v[112:115],v[58:61],v[196:199],v[112:115]\n"
MF "v[116:119],v[58:61],v[200:203],v[116:119]\n"
MF "v[120:123],v[58:61],v[204:207],v[120:123]\n"
MF "v[124:127],v[58:61],v[208:211],v[124:127]\n"
MF "v[128:131],v[58:61],v[212:215],v[128:131]\n"
"s_add_u32 s58,s74,6144\n"
"s_nop 3\n"
LD "v[58:61],v10,s[32:35],s58 offen offset:256\n"
"s_waitcnt vmcnt(0)\n"
MF "v[112:115],v[58:61],v[216:219],v[112:115]\n"
MF "v[116:119],v[58:61],v[220:223],v[116:119]\n"
MF "v[120:123],v[58:61],v[224:227],v[120:123]\n"
MF "v[124:127],v[58:61],v[228:231],v[124:127]\n"
MF "v[128:131],v[58:61],v[232:235],v[128:131]\n"
"s_add_u32 s58,s74,6144\n"
"s_nop 3\n"
LD "v[58:61],v10,s[32:35],s58 offen offset:320\n"
"s_waitcnt vmcnt(0)\n"
MF "v[112:115],v[58:61],v[236:239],v[112:115]\n"
MF "v[116:119],v[58:61],v[240:243],v[116:119]\n"
MF "v[120:123],v[58:61],v[244:247],v[120:123]\n"
MF "v[124:127],v[58:61],v[248:251],v[124:127]\n"
MF "v[128:131],v[58:61],v[252:255],v[128:131]\n"
"s_nop 7\n"
"s_mov_b32 s34,s59\n"
"v_max3_f32 v10,v92,v93,v94\n"
"s_nop 0\n"
"v_max3_f32 v11,v95,v112,v113\n"
"v_max3_f32 v10,v10,v114,v115\n"
"v_max_f32 v10,v10,v11\n"
"v_mov_b32 v11,v10\n"
"s_nop 1\n"
"v_permlane16_swap_b32 v10,v11\n"
"v_max_f32 v10,v10,v11\n"
"v_mov_b32 v11,v10\n"
"s_nop 1\n"
"v_permlane32_swap_b32 v10,v11\n"
"v_max_f32 v10,v10,v11\n"
"v_add_f32 v10,0x41300000,v10\n"
"v_max_f32 v10,0,v10\n"
"v_exp_f32 v11,-v10\n"
AR "v12,a0\n"
"v_subrev_f32 v12,s13,v12\n"
"v_fma_f32 v12,v12,v11,s13\n"
AW "a0,v12\n"
AR "v12,a20\n"
"v_subrev_f32 v12,s13,v12\n"
"v_fma_f32 v12,v12,v11,s13\n"
AW "a20,v12\n"
AR "v12,a40\n"
"v_subrev_f32 v12,s13,v12\n"
"v_fma_f32 v12,v12,v11,s13\n"
AW "a40,v12\n"
AR "v12,a60\n"
"v_subrev_f32 v12,s13,v12\n"
"v_fma_f32 v12,v12,v11,s13\n"
AW "a60,v12\n"
AR "v12,a80\n"
"v_subrev_f32 v12,s13,v12\n"
"v_fma_f32 v12,v12,v11,s13\n"
AW "a80,v12\n"
AR "v12,a100\n"
"v_subrev_f32 v12,s13,v12\n"
"v_fma_f32 v12,v12,v11,s13\n"
AW "a100,v12\n"
AR "v12,a120\n"
"v_subrev_f32 v12,s13,v12\n"
"v_fma_f32 v12,v12,v11,s13\n"
AW "a120,v12\n"
AR "v12,a140\n"
"v_subrev_f32 v12,s13,v12\n"
"v_fma_f32 v12,v12,v11,s13\n"
AW "a140,v12\n"
AR "v12,a160\n"
"v_subrev_f32 v12,s13,v12\n"
"v_fma_f32 v12,v12,v11,s13\n"
AW "a160,v12\n"
AR "v12,a180\n"
"v_subrev_f32 v12,s13,v12\n"
"v_fma_f32 v12,v12,v11,s13\n"
AW "a180,v12\n"
AR "v12,a200\n"
"v_subrev_f32 v12,s13,v12\n"
"v_fma_f32 v12,v12,v11,s13\n"
AW "a200,v12\n"
AR "v12,a220\n"
"v_subrev_f32 v12,s13,v12\n"
"v_fma_f32 v12,v12,v11,s13\n"
AW "a220,v12\n"
AR "v12,a1\n"
"v_subrev_f32 v12,s13,v12\n"
"v_fma_f32 v12,v12,v11,s13\n"
AW "a1,v12\n"
AR "v12,a21\n"
"v_subrev_f32 v12,s13,v12\n"
"v_fma_f32 v12,v12,v11,s13\n"
AW "a21,v12\n"
AR "v12,a41\n"
"v_subrev_f32 v12,s13,v12\n"
"v_fma_f32 v12,v12,v11,s13\n"
AW "a41,v12\n"
AR "v12,a61\n"
"v_subrev_f32 v12,s13,v12\n"
"v_fma_f32 v12,v12,v11,s13\n"
AW "a61,v12\n"
AR "v12,a81\n"
"v_subrev_f32 v12,s13,v12\n"
"v_fma_f32 v12,v12,v11,s13\n"
AW "a81,v12\n"
AR "v12,a101\n"
"v_subrev_f32 v12,s13,v12\n"
"v_fma_f32 v12,v12,v11,s13\n"
AW "a101,v12\n"
AR "v12,a121\n"
"v_subrev_f32 v12,s13,v12\n"
"v_fma_f32 v12,v12,v11,s13\n"
AW "a121,v12\n"
AR "v12,a141\n"
"v_subrev_f32 v12,s13,v12\n"
"v_fma_f32 v12,v12,v11,s13\n"
AW "a141,v12\n"
AR "v12,a161\n"
"v_subrev_f32 v12,s13,v12\n"
"v_fma_f32 v12,v12,v11,s13\n"
AW "a161,v12\n"
AR "v12,a181\n"
"v_subrev_f32 v12,s13,v12\n"
"v_fma_f32 v12,v12,v11,s13\n"
AW "a181,v12\n"
AR "v12,a201\n"
"v_subrev_f32 v12,s13,v12\n"
"v_fma_f32 v12,v12,v11,s13\n"
AW "a201,v12\n"
AR "v12,a221\n"
"v_subrev_f32 v12,s13,v12\n"
"v_fma_f32 v12,v12,v11,s13\n"
AW "a221,v12\n"
AR "v12,a2\n"
"v_subrev_f32 v12,s13,v12\n"
"v_fma_f32 v12,v12,v11,s13\n"
AW "a2,v12\n"
AR "v12,a22\n"
"v_subrev_f32 v12,s13,v12\n"
"v_fma_f32 v12,v12,v11,s13\n"
AW "a22,v12\n"
AR "v12,a42\n"
"v_subrev_f32 v12,s13,v12\n"
"v_fma_f32 v12,v12,v11,s13\n"
AW "a42,v12\n"
AR "v12,a62\n"
"v_subrev_f32 v12,s13,v12\n"
"v_fma_f32 v12,v12,v11,s13\n"
AW "a62,v12\n"
AR "v12,a82\n"
"v_subrev_f32 v12,s13,v12\n"
"v_fma_f32 v12,v12,v11,s13\n"
AW "a82,v12\n"
AR "v12,a102\n"
"v_subrev_f32 v12,s13,v12\n"
"v_fma_f32 v12,v12,v11,s13\n"
AW "a102,v12\n"
AR "v12,a122\n"
"v_subrev_f32 v12,s13,v12\n"
"v_fma_f32 v12,v12,v11,s13\n"
AW "a122,v12\n"
AR "v12,a142\n"
"v_subrev_f32 v12,s13,v12\n"
"v_fma_f32 v12,v12,v11,s13\n"
AW "a142,v12\n"
AR "v12,a162\n"
"v_subrev_f32 v12,s13,v12\n"
"v_fma_f32 v12,v12,v11,s13\n"
AW "a162,v12\n"
AR "v12,a182\n"
"v_subrev_f32 v12,s13,v12\n"
"v_fma_f32 v12,v12,v11,s13\n"
AW "a182,v12\n"
AR "v12,a202\n"
"v_subrev_f32 v12,s13,v12\n"
"v_fma_f32 v12,v12,v11,s13\n"
AW "a202,v12\n"
AR "v12,a222\n"
"v_subrev_f32 v12,s13,v12\n"
"v_fma_f32 v12,v12,v11,s13\n"
AW "a222,v12\n"
AR "v12,a3\n"
"v_subrev_f32 v12,s13,v12\n"
"v_fma_f32 v12,v12,v11,s13\n"
AW "a3,v12\n"
AR "v12,a23\n"
"v_subrev_f32 v12,s13,v12\n"
"v_fma_f32 v12,v12,v11,s13\n"
AW "a23,v12\n"
AR "v12,a43\n"
"v_subrev_f32 v12,s13,v12\n"
"v_fma_f32 v12,v12,v11,s13\n"
AW "a43,v12\n"
AR "v12,a63\n"
"v_subrev_f32 v12,s13,v12\n"
"v_fma_f32 v12,v12,v11,s13\n"
AW "a63,v12\n"
AR "v12,a83\n"
"v_subrev_f32 v12,s13,v12\n"
"v_fma_f32 v12,v12,v11,s13\n"
AW "a83,v12\n"
AR "v12,a103\n"
"v_subrev_f32 v12,s13,v12\n"
"v_fma_f32 v12,v12,v11,s13\n"
AW "a103,v12\n"
AR "v12,a123\n"
"v_subrev_f32 v12,s13,v12\n"
"v_fma_f32 v12,v12,v11,s13\n"
AW "a123,v12\n"
AR "v12,a143\n"
"v_subrev_f32 v12,s13,v12\n"
"v_fma_f32 v12,v12,v11,s13\n"
AW "a143,v12\n"
AR "v12,a163\n"
"v_subrev_f32 v12,s13,v12\n"
"v_fma_f32 v12,v12,v11,s13\n"
AW "a163,v12\n"
AR "v12,a183\n"
"v_subrev_f32 v12,s13,v12\n"
"v_fma_f32 v12,v12,v11,s13\n"
AW "a183,v12\n"
AR "v12,a203\n"
"v_subrev_f32 v12,s13,v12\n"
"v_fma_f32 v12,v12,v11,s13\n"
AW "a203,v12\n"
AR "v12,a223\n"
"v_subrev_f32 v12,s13,v12\n"
"v_fma_f32 v12,v12,v11,s13\n"
AW "a223,v12\n"
"v_mul_f32 v57,v52,v11\n"
"v_sub_f32 v32,v32,v10\n"
"v_sub_f32 v33,v33,v10\n"
"v_sub_f32 v34,v34,v10\n"
"v_sub_f32 v35,v35,v10\n"
"v_sub_f32 v72,v72,v10\n"
"v_sub_f32 v73,v73,v10\n"
"v_sub_f32 v74,v74,v10\n"
"v_sub_f32 v75,v75,v10\n"
"v_sub_f32 v92,v92,v10\n"
"v_sub_f32 v93,v93,v10\n"
"v_sub_f32 v94,v94,v10\n"
"v_sub_f32 v95,v95,v10\n"
"v_sub_f32 v112,v112,v10\n"
"v_sub_f32 v113,v113,v10\n"
"v_sub_f32 v114,v114,v10\n"
"v_sub_f32 v115,v115,v10\n"
"v_exp_f32 v92,v92\n"
"v_exp_f32 v93,v93\n"
"v_exp_f32 v94,v94\n"
"v_exp_f32 v95,v95\n"
"v_exp_f32 v112,v112\n"
"v_exp_f32 v113,v113\n"
"v_exp_f32 v114,v114\n"
"v_exp_f32 v115,v115\n"
"v_add_f32 v57,v57,v92\n"
"v_add_f32 v57,v57,v93\n"
"v_add_f32 v57,v57,v94\n"
"v_add_f32 v57,v57,v95\n"
"v_add_f32 v57,v57,v112\n"
"v_add_f32 v57,v57,v113\n"
"v_add_f32 v57,v57,v114\n"
"v_add_f32 v57,v57,v115\n"
CV "v92,v92,v93\n"
CV "v93,v94,v95\n"
CV "v94,v112,v113\n"
CV "v95,v114,v115\n"
"v_max3_f32 v10,v96,v97,v98\n"
"v_max3_f32 v11,v99,v116,v117\n"
"v_max3_f32 v10,v10,v118,v119\n"
"v_max_f32 v10,v10,v11\n"
"v_mov_b32 v11,v10\n"
"s_nop 1\n"
"v_permlane16_swap_b32 v10,v11\n"
"v_max_f32 v10,v10,v11\n"
"v_mov_b32 v11,v10\n"
"s_nop 1\n"
"v_permlane32_swap_b32 v10,v11\n"
"v_max_f32 v10,v10,v11\n"
"v_add_f32 v10,0x41300000,v10\n"
"v_max_f32 v10,0,v10\n"
"v_exp_f32 v11,-v10\n"
AR "v12,a4\n"
"v_subrev_f32 v12,s13,v12\n"
"v_fma_f32 v12,v12,v11,s13\n"
AW "a4,v12\n"
AR "v12,a24\n"
"v_subrev_f32 v12,s13,v12\n"
"v_fma_f32 v12,v12,v11,s13\n"
AW "a24,v12\n"
AR "v12,a44\n"
"v_subrev_f32 v12,s13,v12\n"
"v_fma_f32 v12,v12,v11,s13\n"
AW "a44,v12\n"
AR "v12,a64\n"
"v_subrev_f32 v12,s13,v12\n"
"v_fma_f32 v12,v12,v11,s13\n"
AW "a64,v12\n"
AR "v12,a84\n"
"v_subrev_f32 v12,s13,v12\n"
"v_fma_f32 v12,v12,v11,s13\n"
AW "a84,v12\n"
AR "v12,a104\n"
"v_subrev_f32 v12,s13,v12\n"
"v_fma_f32 v12,v12,v11,s13\n"
AW "a104,v12\n"
AR "v12,a124\n"
"v_subrev_f32 v12,s13,v12\n"
"v_fma_f32 v12,v12,v11,s13\n"
AW "a124,v12\n"
AR "v12,a144\n"
"v_subrev_f32 v12,s13,v12\n"
"v_fma_f32 v12,v12,v11,s13\n"
AW "a144,v12\n"
AR "v12,a164\n"
"v_subrev_f32 v12,s13,v12\n"
"v_fma_f32 v12,v12,v11,s13\n"
AW "a164,v12\n"
AR "v12,a184\n"
"v_subrev_f32 v12,s13,v12\n"
"v_fma_f32 v12,v12,v11,s13\n"
AW "a184,v12\n"
AR "v12,a204\n"
"v_subrev_f32 v12,s13,v12\n"
"v_fma_f32 v12,v12,v11,s13\n"
AW "a204,v12\n"
AR "v12,a224\n"
"v_subrev_f32 v12,s13,v12\n"
"v_fma_f32 v12,v12,v11,s13\n"
AW "a224,v12\n"
AR "v12,a5\n"
"v_subrev_f32 v12,s13,v12\n"
"v_fma_f32 v12,v12,v11,s13\n"
AW "a5,v12\n"
AR "v12,a25\n"
"v_subrev_f32 v12,s13,v12\n"
"v_fma_f32 v12,v12,v11,s13\n"
AW "a25,v12\n"
AR "v12,a45\n"
"v_subrev_f32 v12,s13,v12\n"
"v_fma_f32 v12,v12,v11,s13\n"
AW "a45,v12\n"
AR "v12,a65\n"
"v_subrev_f32 v12,s13,v12\n"
"v_fma_f32 v12,v12,v11,s13\n"
AW "a65,v12\n"
AR "v12,a85\n"
"v_subrev_f32 v12,s13,v12\n"
"v_fma_f32 v12,v12,v11,s13\n"
AW "a85,v12\n"
AR "v12,a105\n"
"v_subrev_f32 v12,s13,v12\n"
"v_fma_f32 v12,v12,v11,s13\n"
AW "a105,v12\n"
AR "v12,a125\n"
"v_subrev_f32 v12,s13,v12\n"
"v_fma_f32 v12,v12,v11,s13\n"
AW "a125,v12\n"
AR "v12,a145\n"
"v_subrev_f32 v12,s13,v12\n"
"v_fma_f32 v12,v12,v11,s13\n"
AW "a145,v12\n"
AR "v12,a165\n"
"v_subrev_f32 v12,s13,v12\n"
"v_fma_f32 v12,v12,v11,s13\n"
AW "a165,v12\n"
AR "v12,a185\n"
"v_subrev_f32 v12,s13,v12\n"
"v_fma_f32 v12,v12,v11,s13\n"
AW "a185,v12\n"
AR "v12,a205\n"
"v_subrev_f32 v12,s13,v12\n"
"v_fma_f32 v12,v12,v11,s13\n"
AW "a205,v12\n"
AR "v12,a225\n"
"v_subrev_f32 v12,s13,v12\n"
"v_fma_f32 v12,v12,v11,s13\n"
AW "a225,v12\n"
AR "v12,a6\n"
"v_subrev_f32 v12,s13,v12\n"
"v_fma_f32 v12,v12,v11,s13\n"
AW "a6,v12\n"
AR "v12,a26\n"
"v_subrev_f32 v12,s13,v12\n"
"v_fma_f32 v12,v12,v11,s13\n"
AW "a26,v12\n"
AR "v12,a46\n"
"v_subrev_f32 v12,s13,v12\n"
"v_fma_f32 v12,v12,v11,s13\n"
AW "a46,v12\n"
AR "v12,a66\n"
"v_subrev_f32 v12,s13,v12\n"
"v_fma_f32 v12,v12,v11,s13\n"
AW "a66,v12\n"
AR "v12,a86\n"
"v_subrev_f32 v12,s13,v12\n"
"v_fma_f32 v12,v12,v11,s13\n"
AW "a86,v12\n"
AR "v12,a106\n"
"v_subrev_f32 v12,s13,v12\n"
"v_fma_f32 v12,v12,v11,s13\n"
AW "a106,v12\n"
AR "v12,a126\n"
"v_subrev_f32 v12,s13,v12\n"
"v_fma_f32 v12,v12,v11,s13\n"
AW "a126,v12\n"
AR "v12,a146\n"
"v_subrev_f32 v12,s13,v12\n"
"v_fma_f32 v12,v12,v11,s13\n"
AW "a146,v12\n"
AR "v12,a166\n"
"v_subrev_f32 v12,s13,v12\n"
"v_fma_f32 v12,v12,v11,s13\n"
AW "a166,v12\n"
AR "v12,a186\n"
"v_subrev_f32 v12,s13,v12\n"
"v_fma_f32 v12,v12,v11,s13\n"
AW "a186,v12\n"
AR "v12,a206\n"
"v_subrev_f32 v12,s13,v12\n"
"v_fma_f32 v12,v12,v11,s13\n"
AW "a206,v12\n"
AR "v12,a226\n"
"v_subrev_f32 v12,s13,v12\n"
"v_fma_f32 v12,v12,v11,s13\n"
AW "a226,v12\n"
AR "v12,a7\n"
"v_subrev_f32 v12,s13,v12\n"
"v_fma_f32 v12,v12,v11,s13\n"
AW "a7,v12\n"
AR "v12,a27\n"
"v_subrev_f32 v12,s13,v12\n"
"v_fma_f32 v12,v12,v11,s13\n"
AW "a27,v12\n"
AR "v12,a47\n"
"v_subrev_f32 v12,s13,v12\n"
"v_fma_f32 v12,v12,v11,s13\n"
AW "a47,v12\n"
AR "v12,a67\n"
"v_subrev_f32 v12,s13,v12\n"
"v_fma_f32 v12,v12,v11,s13\n"
AW "a67,v12\n"
AR "v12,a87\n"
"v_subrev_f32 v12,s13,v12\n"
"v_fma_f32 v12,v12,v11,s13\n"
AW "a87,v12\n"
AR "v12,a107\n"
"v_subrev_f32 v12,s13,v12\n"
"v_fma_f32 v12,v12,v11,s13\n"
AW "a107,v12\n"
AR "v12,a127\n"
"v_subrev_f32 v12,s13,v12\n"
"v_fma_f32 v12,v12,v11,s13\n"
AW "a127,v12\n"
AR "v12,a147\n"
"v_subrev_f32 v12,s13,v12\n"
"v_fma_f32 v12,v12,v11,s13\n"
AW "a147,v12\n"
AR "v12,a167\n"
"v_subrev_f32 v12,s13,v12\n"
"v_fma_f32 v12,v12,v11,s13\n"
AW "a167,v12\n"
AR "v12,a187\n"
"v_subrev_f32 v12,s13,v12\n"
"v_fma_f32 v12,v12,v11,s13\n"
AW "a187,v12\n"
AR "v12,a207\n"
"v_subrev_f32 v12,s13,v12\n"
"v_fma_f32 v12,v12,v11,s13\n"
AW "a207,v12\n"
AR "v12,a227\n"
"v_subrev_f32 v12,s13,v12\n"
"v_fma_f32 v12,v12,v11,s13\n"
AW "a227,v12\n"
"v_mul_f32 v58,v53,v11\n"
"v_sub_f32 v36,v36,v10\n"
"v_sub_f32 v37,v37,v10\n"
"v_sub_f32 v38,v38,v10\n"
"v_sub_f32 v39,v39,v10\n"
"v_sub_f32 v76,v76,v10\n"
"v_sub_f32 v77,v77,v10\n"
"v_sub_f32 v78,v78,v10\n"
"v_sub_f32 v79,v79,v10\n"
"v_sub_f32 v96,v96,v10\n"
"v_sub_f32 v97,v97,v10\n"
"v_sub_f32 v98,v98,v10\n"
"v_sub_f32 v99,v99,v10\n"
"v_sub_f32 v116,v116,v10\n"
"v_sub_f32 v117,v117,v10\n"
"v_sub_f32 v118,v118,v10\n"
"v_sub_f32 v119,v119,v10\n"
"v_exp_f32 v96,v96\n"
"v_exp_f32 v97,v97\n"
"v_exp_f32 v98,v98\n"
"v_exp_f32 v99,v99\n"
"v_exp_f32 v116,v116\n"
"v_exp_f32 v117,v117\n"
"v_exp_f32 v118,v118\n"
"v_exp_f32 v119,v119\n"
"v_add_f32 v58,v58,v96\n"
"v_add_f32 v58,v58,v97\n"
"v_add_f32 v58,v58,v98\n"
"v_add_f32 v58,v58,v99\n"
"v_add_f32 v58,v58,v116\n"
"v_add_f32 v58,v58,v117\n"
"v_add_f32 v58,v58,v118\n"
"v_add_f32_e64 v58,v58,v119\n"
CV "v96,v96,v97\n"
CV "v97,v98,v99\n"
CV "v98,v116,v117\n"
CV "v99,v118,v119\n"
"v_max3_f32 v10,v100,v101,v102\n"
"v_max3_f32 v11,v103,v120,v121\n"
"v_max3_f32 v10,v10,v122,v123\n"
"v_max_f32 v10,v10,v11\n"
"v_mov_b32 v11,v10\n"
"s_nop 1\n"
"v_permlane16_swap_b32 v10,v11\n"
"v_max_f32 v10,v10,v11\n"
"v_mov_b32 v11,v10\n"
"s_nop 1\n"
"v_permlane32_swap_b32 v10,v11\n"
"v_max_f32 v10,v10,v11\n"
"v_add_f32 v10,0x41300000,v10\n"
"v_max_f32 v10,0,v10\n"
"v_exp_f32 v11,-v10\n"
AR "v12,a8\n"
"v_subrev_f32 v12,s13,v12\n"
"v_fma_f32 v12,v12,v11,s13\n"
AW "a8,v12\n"
AR "v12,a28\n"
"v_subrev_f32 v12,s13,v12\n"
"v_fma_f32 v12,v12,v11,s13\n"
AW "a28,v12\n"
AR "v12,a48\n"
"v_subrev_f32 v12,s13,v12\n"
"v_fma_f32 v12,v12,v11,s13\n"
AW "a48,v12\n"
AR "v12,a68\n"
"v_subrev_f32 v12,s13,v12\n"
"v_fma_f32 v12,v12,v11,s13\n"
AW "a68,v12\n"
AR "v12,a88\n"
"v_subrev_f32 v12,s13,v12\n"
"v_fma_f32 v12,v12,v11,s13\n"
AW "a88,v12\n"
AR "v12,a108\n"
"v_subrev_f32 v12,s13,v12\n"
"v_fma_f32 v12,v12,v11,s13\n"
AW "a108,v12\n"
AR "v12,a128\n"
"v_subrev_f32 v12,s13,v12\n"
"v_fma_f32 v12,v12,v11,s13\n"
AW "a128,v12\n"
AR "v12,a148\n"
"v_subrev_f32 v12,s13,v12\n"
"v_fma_f32 v12,v12,v11,s13\n"
AW "a148,v12\n"
AR "v12,a168\n"
"v_subrev_f32 v12,s13,v12\n"
"v_fma_f32 v12,v12,v11,s13\n"
AW "a168,v12\n"
AR "v12,a188\n"
"v_subrev_f32 v12,s13,v12\n"
"v_fma_f32 v12,v12,v11,s13\n"
AW "a188,v12\n"
AR "v12,a208\n"
"v_subrev_f32 v12,s13,v12\n"
"v_fma_f32 v12,v12,v11,s13\n"
AW "a208,v12\n"
AR "v12,a228\n"
"v_subrev_f32 v12,s13,v12\n"
"v_fma_f32 v12,v12,v11,s13\n"
AW "a228,v12\n"
AR "v12,a9\n"
"v_subrev_f32 v12,s13,v12\n"
"v_fma_f32 v12,v12,v11,s13\n"
AW "a9,v12\n"
AR "v12,a29\n"
"v_subrev_f32 v12,s13,v12\n"
"v_fma_f32 v12,v12,v11,s13\n"
AW "a29,v12\n"
AR "v12,a49\n"
"v_subrev_f32 v12,s13,v12\n"
"v_fma_f32 v12,v12,v11,s13\n"
AW "a49,v12\n"
AR "v12,a69\n"
"v_subrev_f32 v12,s13,v12\n"
"v_fma_f32 v12,v12,v11,s13\n"
AW "a69,v12\n"
AR "v12,a89\n"
"v_subrev_f32 v12,s13,v12\n"
"v_fma_f32 v12,v12,v11,s13\n"
AW "a89,v12\n"
AR "v12,a109\n"
"v_subrev_f32 v12,s13,v12\n"
"v_fma_f32 v12,v12,v11,s13\n"
AW "a109,v12\n"
AR "v12,a129\n"
"v_subrev_f32 v12,s13,v12\n"
"v_fma_f32 v12,v12,v11,s13\n"
AW "a129,v12\n"
AR "v12,a149\n"
"v_subrev_f32 v12,s13,v12\n"
"v_fma_f32 v12,v12,v11,s13\n"
AW "a149,v12\n"
AR "v12,a169\n"
"v_subrev_f32 v12,s13,v12\n"
"v_fma_f32 v12,v12,v11,s13\n"
AW "a169,v12\n"
AR "v12,a189\n"
"v_subrev_f32 v12,s13,v12\n"
"v_fma_f32 v12,v12,v11,s13\n"
AW "a189,v12\n"
AR "v12,a209\n"
"v_subrev_f32 v12,s13,v12\n"
"v_fma_f32 v12,v12,v11,s13\n"
AW "a209,v12\n"
AR "v12,a229\n"
"v_subrev_f32 v12,s13,v12\n"
"v_fma_f32 v12,v12,v11,s13\n"
AW "a229,v12\n"
AR "v12,a10\n"
"v_subrev_f32 v12,s13,v12\n"
"v_fma_f32 v12,v12,v11,s13\n"
AW "a10,v12\n"
AR "v12,a30\n"
"v_subrev_f32 v12,s13,v12\n"
"v_fma_f32 v12,v12,v11,s13\n"
AW "a30,v12\n"
AR "v12,a50\n"
"v_subrev_f32 v12,s13,v12\n"
"v_fma_f32 v12,v12,v11,s13\n"
AW "a50,v12\n"
AR "v12,a70\n"
"v_subrev_f32 v12,s13,v12\n"
"v_fma_f32 v12,v12,v11,s13\n"
AW "a70,v12\n"
AR "v12,a90\n"
"v_subrev_f32 v12,s13,v12\n"
"v_fma_f32 v12,v12,v11,s13\n"
AW "a90,v12\n"
AR "v12,a110\n"
"v_subrev_f32 v12,s13,v12\n"
"v_fma_f32 v12,v12,v11,s13\n"
AW "a110,v12\n"
AR "v12,a130\n"
"v_subrev_f32 v12,s13,v12\n"
"v_fma_f32 v12,v12,v11,s13\n"
AW "a130,v12\n"
AR "v12,a150\n"
"v_subrev_f32 v12,s13,v12\n"
"v_fma_f32 v12,v12,v11,s13\n"
AW "a150,v12\n"
AR "v12,a170\n"
"v_subrev_f32 v12,s13,v12\n"
"v_fma_f32 v12,v12,v11,s13\n"
AW "a170,v12\n"
AR "v12,a190\n"
"v_subrev_f32 v12,s13,v12\n"
"v_fma_f32 v12,v12,v11,s13\n"
AW "a190,v12\n"
AR "v12,a210\n"
"v_subrev_f32 v12,s13,v12\n"
"v_fma_f32 v12,v12,v11,s13\n"
AW "a210,v12\n"
AR "v12,a230\n"
"v_subrev_f32 v12,s13,v12\n"
"v_fma_f32 v12,v12,v11,s13\n"
AW "a230,v12\n"
AR "v12,a11\n"
"v_subrev_f32 v12,s13,v12\n"
"v_fma_f32 v12,v12,v11,s13\n"
AW "a11,v12\n"
AR "v12,a31\n"
"v_subrev_f32 v12,s13,v12\n"
"v_fma_f32 v12,v12,v11,s13\n"
AW "a31,v12\n"
AR "v12,a51\n"
"v_subrev_f32 v12,s13,v12\n"
"v_fma_f32 v12,v12,v11,s13\n"
AW "a51,v12\n"
AR "v12,a71\n"
"v_subrev_f32 v12,s13,v12\n"
"v_fma_f32 v12,v12,v11,s13\n"
AW "a71,v12\n"
AR "v12,a91\n"
"v_subrev_f32 v12,s13,v12\n"
"v_fma_f32 v12,v12,v11,s13\n"
AW "a91,v12\n"
AR "v12,a111\n"
"v_subrev_f32 v12,s13,v12\n"
"v_fma_f32 v12,v12,v11,s13\n"
AW "a111,v12\n"
AR "v12,a131\n"
"v_subrev_f32 v12,s13,v12\n"
"v_fma_f32 v12,v12,v11,s13\n"
AW "a131,v12\n"
AR "v12,a151\n"
"v_subrev_f32 v12,s13,v12\n"
"v_fma_f32 v12,v12,v11,s13\n"
AW "a151,v12\n"
AR "v12,a171\n"
"v_subrev_f32 v12,s13,v12\n"
"v_fma_f32 v12,v12,v11,s13\n"
AW "a171,v12\n"
AR "v12,a191\n"
"v_subrev_f32 v12,s13,v12\n"
"v_fma_f32 v12,v12,v11,s13\n"
AW "a191,v12\n"
AR "v12,a211\n"
"v_subrev_f32 v12,s13,v12\n"
"v_fma_f32 v12,v12,v11,s13\n"
AW "a211,v12\n"
AR "v12,a231\n"
"v_subrev_f32 v12,s13,v12\n"
"v_fma_f32 v12,v12,v11,s13\n"
AW "a231,v12\n"
"v_mul_f32 v59,v54,v11\n"
"v_sub_f32 v40,v40,v10\n"
"v_sub_f32 v41,v41,v10\n"
"v_sub_f32 v42,v42,v10\n"
"v_sub_f32 v43,v43,v10\n"
"v_sub_f32 v80,v80,v10\n"
"v_sub_f32 v81,v81,v10\n"
"v_sub_f32 v82,v82,v10\n"
"v_sub_f32 v83,v83,v10\n"
"v_sub_f32 v100,v100,v10\n"
"v_sub_f32 v101,v101,v10\n"
"v_sub_f32 v102,v102,v10\n"
"v_sub_f32 v103,v103,v10\n"
"v_sub_f32 v120,v120,v10\n"
"v_sub_f32 v121,v121,v10\n"
"v_sub_f32 v122,v122,v10\n"
"v_sub_f32 v123,v123,v10\n"
"v_exp_f32 v100,v100\n"
"v_exp_f32 v101,v101\n"
"v_exp_f32 v102,v102\n"
"v_exp_f32 v103,v103\n"
"v_exp_f32 v120,v120\n"
"v_exp_f32 v121,v121\n"
"v_exp_f32 v122,v122\n"
"v_exp_f32 v123,v123\n"
"v_add_f32 v59,v59,v100\n"
"v_add_f32 v59,v59,v101\n"
"v_add_f32 v59,v59,v102\n"
"v_add_f32 v59,v59,v103\n"
"v_add_f32 v59,v59,v120\n"
"v_add_f32 v59,v59,v121\n"
"v_add_f32 v59,v59,v122\n"
"v_add_f32_e64 v59,v59,v123\n"
CV "v100,v100,v101\n"
CV "v101,v102,v103\n"
CV "v102,v120,v121\n"
CV "v103,v122,v123\n"
"v_max3_f32 v10,v104,v105,v106\n"
"v_max3_f32 v11,v107,v124,v125\n"
"v_max3_f32 v10,v10,v126,v127\n"
"v_max_f32 v10,v10,v11\n"
"v_mov_b32 v11,v10\n"
"s_nop 1\n"
"v_permlane16_swap_b32 v10,v11\n"
"v_max_f32 v10,v10,v11\n"
"v_mov_b32 v11,v10\n"
"s_nop 1\n"
"v_permlane32_swap_b32 v10,v11\n"
"v_max_f32_e64 v10,v10,v11\n"
"v_add_f32 v10,0x41300000,v10\n"
"v_max_f32 v10,0,v10\n"
"v_exp_f32 v11,-v10\n"
AR "v12,a12\n"
"v_subrev_f32 v12,s13,v12\n"
"v_fma_f32 v12,v12,v11,s13\n"
AW "a12,v12\n"
AR "v12,a32\n"
"v_subrev_f32 v12,s13,v12\n"
"v_fma_f32 v12,v12,v11,s13\n"
AW "a32,v12\n"
AR "v12,a52\n"
"v_subrev_f32 v12,s13,v12\n"
"v_fma_f32 v12,v12,v11,s13\n"
AW "a52,v12\n"
AR "v12,a72\n"
"v_subrev_f32 v12,s13,v12\n"
"v_fma_f32 v12,v12,v11,s13\n"
AW "a72,v12\n"
AR "v12,a92\n"
"v_subrev_f32 v12,s13,v12\n"
"v_fma_f32 v12,v12,v11,s13\n"
AW "a92,v12\n"
AR "v12,a112\n"
"v_subrev_f32 v12,s13,v12\n"
"v_fma_f32 v12,v12,v11,s13\n"
AW "a112,v12\n"
AR "v12,a132\n"
"v_subrev_f32 v12,s13,v12\n"
"v_fma_f32 v12,v12,v11,s13\n"
AW "a132,v12\n"
AR "v12,a152\n"
"v_subrev_f32 v12,s13,v12\n"
"v_fma_f32 v12,v12,v11,s13\n"
AW "a152,v12\n"
AR "v12,a172\n"
"v_subrev_f32 v12,s13,v12\n"
"v_fma_f32 v12,v12,v11,s13\n"
AW "a172,v12\n"
AR "v12,a192\n"
"v_subrev_f32 v12,s13,v12\n"
"v_fma_f32 v12,v12,v11,s13\n"
AW "a192,v12\n"
AR "v12,a212\n"
"v_subrev_f32 v12,s13,v12\n"
"v_fma_f32 v12,v12,v11,s13\n"
AW "a212,v12\n"
AR "v12,a232\n"
"v_subrev_f32 v12,s13,v12\n"
"v_fma_f32 v12,v12,v11,s13\n"
AW "a232,v12\n"
AR "v12,a13\n"
"v_subrev_f32 v12,s13,v12\n"
"v_fma_f32 v12,v12,v11,s13\n"
AW "a13,v12\n"
AR "v12,a33\n"
"v_subrev_f32 v12,s13,v12\n"
"v_fma_f32 v12,v12,v11,s13\n"
AW "a33,v12\n"
AR "v12,a53\n"
"v_subrev_f32 v12,s13,v12\n"
"v_fma_f32 v12,v12,v11,s13\n"
AW "a53,v12\n"
AR "v12,a73\n"
"v_subrev_f32 v12,s13,v12\n"
"v_fma_f32 v12,v12,v11,s13\n"
AW "a73,v12\n"
AR "v12,a93\n"
"v_subrev_f32 v12,s13,v12\n"
"v_fma_f32 v12,v12,v11,s13\n"
AW "a93,v12\n"
AR "v12,a113\n"
"v_subrev_f32 v12,s13,v12\n"
"v_fma_f32 v12,v12,v11,s13\n"
AW "a113,v12\n"
AR "v12,a133\n"
"v_subrev_f32 v12,s13,v12\n"
"v_fma_f32 v12,v12,v11,s13\n"
AW "a133,v12\n"
AR "v12,a153\n"
"v_subrev_f32 v12,s13,v12\n"
"v_fma_f32 v12,v12,v11,s13\n"
AW "a153,v12\n"
AR "v12,a173\n"
"v_subrev_f32 v12,s13,v12\n"
"v_fma_f32 v12,v12,v11,s13\n"
AW "a173,v12\n"
AR "v12,a193\n"
"v_subrev_f32 v12,s13,v12\n"
"v_fma_f32 v12,v12,v11,s13\n"
AW "a193,v12\n"
AR "v12,a213\n"
"v_subrev_f32 v12,s13,v12\n"
"v_fma_f32 v12,v12,v11,s13\n"
AW "a213,v12\n"
AR "v12,a233\n"
"v_subrev_f32 v12,s13,v12\n"
"v_fma_f32 v12,v12,v11,s13\n"
AW "a233,v12\n"
AR "v12,a14\n"
"v_subrev_f32 v12,s13,v12\n"
"v_fma_f32 v12,v12,v11,s13\n"
AW "a14,v12\n"
AR "v12,a34\n"
"v_subrev_f32 v12,s13,v12\n"
"v_fma_f32 v12,v12,v11,s13\n"
AW "a34,v12\n"
AR "v12,a54\n"
"v_subrev_f32 v12,s13,v12\n"
"v_fma_f32 v12,v12,v11,s13\n"
AW "a54,v12\n"
AR "v12,a74\n"
"v_subrev_f32 v12,s13,v12\n"
"v_fma_f32 v12,v12,v11,s13\n"
AW "a74,v12\n"
AR "v12,a94\n"
"v_subrev_f32 v12,s13,v12\n"
"v_fma_f32 v12,v12,v11,s13\n"
AW "a94,v12\n"
AR "v12,a114\n"
"v_subrev_f32 v12,s13,v12\n"
"v_fma_f32 v12,v12,v11,s13\n"
AW "a114,v12\n"
AR "v12,a134\n"
"v_subrev_f32 v12,s13,v12\n"
"v_fma_f32 v12,v12,v11,s13\n"
AW "a134,v12\n"
AR "v12,a154\n"
"v_subrev_f32 v12,s13,v12\n"
"v_fma_f32 v12,v12,v11,s13\n"
AW "a154,v12\n"
AR "v12,a174\n"
"v_subrev_f32 v12,s13,v12\n"
"v_fma_f32 v12,v12,v11,s13\n"
AW "a174,v12\n"
AR "v12,a194\n"
"v_subrev_f32 v12,s13,v12\n"
"v_fma_f32 v12,v12,v11,s13\n"
AW "a194,v12\n"
AR "v12,a214\n"
"v_subrev_f32 v12,s13,v12\n"
"v_fma_f32 v12,v12,v11,s13\n"
AW "a214,v12\n"
AR "v12,a234\n"
"v_subrev_f32 v12,s13,v12\n"
"v_fma_f32 v12,v12,v11,s13\n"
AW "a234,v12\n"
AR "v12,a15\n"
"v_subrev_f32 v12,s13,v12\n"
"v_fma_f32 v12,v12,v11,s13\n"
AW "a15,v12\n"
AR "v12,a35\n"
"v_subrev_f32 v12,s13,v12\n"
"v_fma_f32 v12,v12,v11,s13\n"
AW "a35,v12\n"
AR "v12,a55\n"
"v_subrev_f32 v12,s13,v12\n"
"v_fma_f32 v12,v12,v11,s13\n"
AW "a55,v12\n"
AR "v12,a75\n"
"v_subrev_f32 v12,s13,v12\n"
"v_fma_f32 v12,v12,v11,s13\n"
AW "a75,v12\n"
AR "v12,a95\n"
"v_subrev_f32 v12,s13,v12\n"
"v_fma_f32 v12,v12,v11,s13\n"
AW "a95,v12\n"
AR "v12,a115\n"
"v_subrev_f32 v12,s13,v12\n"
"v_fma_f32 v12,v12,v11,s13\n"
AW "a115,v12\n"
AR "v12,a135\n"
"v_subrev_f32 v12,s13,v12\n"
"v_fma_f32 v12,v12,v11,s13\n"
AW "a135,v12\n"
AR "v12,a155\n"
"v_subrev_f32 v12,s13,v12\n"
"v_fma_f32 v12,v12,v11,s13\n"
AW "a155,v12\n"
AR "v12,a175\n"
"v_subrev_f32 v12,s13,v12\n"
"v_fma_f32 v12,v12,v11,s13\n"
AW "a175,v12\n"
AR "v12,a195\n"
"v_subrev_f32 v12,s13,v12\n"
"v_fma_f32 v12,v12,v11,s13\n"
AW "a195,v12\n"
AR "v12,a215\n"
"v_subrev_f32 v12,s13,v12\n"
"v_fma_f32 v12,v12,v11,s13\n"
AW "a215,v12\n"
AR "v12,a235\n"
"v_subrev_f32 v12,s13,v12\n"
"v_fma_f32 v12,v12,v11,s13\n"
AW "a235,v12\n"
"v_mul_f32 v60,v55,v11\n"
"v_sub_f32 v44,v44,v10\n"
"v_sub_f32 v45,v45,v10\n"
"v_sub_f32 v46,v46,v10\n"
"v_sub_f32 v47,v47,v10\n"
"v_sub_f32 v84,v84,v10\n"
"v_sub_f32 v85,v85,v10\n"
"v_sub_f32 v86,v86,v10\n"
"v_sub_f32 v87,v87,v10\n"
"v_sub_f32 v104,v104,v10\n"
"v_sub_f32 v105,v105,v10\n"
"v_sub_f32 v106,v106,v10\n"
"v_sub_f32 v107,v107,v10\n"
"v_sub_f32 v124,v124,v10\n"
"v_sub_f32 v125,v125,v10\n"
"v_sub_f32 v126,v126,v10\n"
"v_sub_f32 v127,v127,v10\n"
"v_exp_f32 v104,v104\n"
"v_exp_f32 v105,v105\n"
"v_exp_f32 v106,v106\n"
"v_exp_f32 v107,v107\n"
"v_exp_f32 v124,v124\n"
"v_exp_f32 v125,v125\n"
"v_exp_f32 v126,v126\n"
"v_exp_f32 v127,v127\n"
"v_add_f32 v60,v60,v104\n"
"v_add_f32 v60,v60,v105\n"
"v_add_f32 v60,v60,v106\n"
"v_add_f32 v60,v60,v107\n"
"v_add_f32 v60,v60,v124\n"
"v_add_f32 v60,v60,v125\n"
"v_add_f32 v60,v60,v126\n"
"v_add_f32 v60,v60,v127\n"
CV "v104,v104,v105\n"
CV "v105,v106,v107\n"
CV "v106,v124,v125\n"
CV "v107,v126,v127\n"
"v_max3_f32 v10,v108,v109,v110\n"
"v_max3_f32 v11,v111,v128,v129\n"
"v_max3_f32 v10,v10,v130,v131\n"
"v_max_f32 v10,v10,v11\n"
"v_mov_b32 v11,v10\n"
"s_nop 1\n"
"v_permlane16_swap_b32 v10,v11\n"
"v_max_f32 v10,v10,v11\n"
"v_mov_b32 v11,v10\n"
"s_nop 1\n"
"v_permlane32_swap_b32 v10,v11\n"
"v_max_f32 v10,v10,v11\n"
"v_add_f32 v10,0x41300000,v10\n"
"v_max_f32 v10,0,v10\n"
"v_exp_f32 v11,-v10\n"
AR "v12,a16\n"
"v_subrev_f32 v12,s13,v12\n"
"v_fma_f32 v12,v12,v11,s13\n"
AW "a16,v12\n"
AR "v12,a36\n"
"v_subrev_f32 v12,s13,v12\n"
"v_fma_f32 v12,v12,v11,s13\n"
AW "a36,v12\n"
AR "v12,a56\n"
"v_subrev_f32 v12,s13,v12\n"
"v_fma_f32 v12,v12,v11,s13\n"
AW "a56,v12\n"
AR "v12,a76\n"
"v_subrev_f32 v12,s13,v12\n"
"v_fma_f32 v12,v12,v11,s13\n"
AW "a76,v12\n"
AR "v12,a96\n"
"v_subrev_f32 v12,s13,v12\n"
"v_fma_f32 v12,v12,v11,s13\n"
AW "a96,v12\n"
AR "v12,a116\n"
"v_subrev_f32 v12,s13,v12\n"
"v_fma_f32 v12,v12,v11,s13\n"
AW "a116,v12\n"
AR "v12,a136\n"
"v_subrev_f32 v12,s13,v12\n"
"v_fma_f32 v12,v12,v11,s13\n"
AW "a136,v12\n"
AR "v12,a156\n"
"v_subrev_f32 v12,s13,v12\n"
"v_fma_f32 v12,v12,v11,s13\n"
AW "a156,v12\n"
AR "v12,a176\n"
"v_subrev_f32 v12,s13,v12\n"
"v_fma_f32 v12,v12,v11,s13\n"
AW "a176,v12\n"
AR "v12,a196\n"
"v_subrev_f32 v12,s13,v12\n"
"v_fma_f32 v12,v12,v11,s13\n"
AW "a196,v12\n"
AR "v12,a216\n"
"v_subrev_f32 v12,s13,v12\n"
"v_fma_f32 v12,v12,v11,s13\n"
AW "a216,v12\n"
AR "v12,a236\n"
"v_subrev_f32 v12,s13,v12\n"
"v_fma_f32 v12,v12,v11,s13\n"
AW "a236,v12\n"
AR "v12,a17\n"
"v_subrev_f32 v12,s13,v12\n"
"v_fma_f32 v12,v12,v11,s13\n"
AW "a17,v12\n"
AR "v12,a37\n"
"v_subrev_f32 v12,s13,v12\n"
"v_fma_f32 v12,v12,v11,s13\n"
AW "a37,v12\n"
AR "v12,a57\n"
"v_subrev_f32 v12,s13,v12\n"
"v_fma_f32 v12,v12,v11,s13\n"
AW "a57,v12\n"
AR "v12,a77\n"
"v_subrev_f32 v12,s13,v12\n"
"v_fma_f32 v12,v12,v11,s13\n"
AW "a77,v12\n"
AR "v12,a97\n"
"v_subrev_f32 v12,s13,v12\n"
"v_fma_f32 v12,v12,v11,s13\n"
AW "a97,v12\n"
AR "v12,a117\n"
"v_subrev_f32 v12,s13,v12\n"
"v_fma_f32 v12,v12,v11,s13\n"
AW "a117,v12\n"
AR "v12,a137\n"
"v_subrev_f32 v12,s13,v12\n"
"v_fma_f32 v12,v12,v11,s13\n"
AW "a137,v12\n"
AR "v12,a157\n"
"v_subrev_f32 v12,s13,v12\n"
"v_fma_f32 v12,v12,v11,s13\n"
AW "a157,v12\n"
AR "v12,a177\n"
"v_subrev_f32 v12,s13,v12\n"
"v_fma_f32 v12,v12,v11,s13\n"
AW "a177,v12\n"
AR "v12,a197\n"
"v_subrev_f32 v12,s13,v12\n"
"v_fma_f32 v12,v12,v11,s13\n"
AW "a197,v12\n"
AR "v12,a217\n"
"v_subrev_f32 v12,s13,v12\n"
"v_fma_f32 v12,v12,v11,s13\n"
AW "a217,v12\n"
AR "v12,a237\n"
"v_subrev_f32 v12,s13,v12\n"
"v_fma_f32 v12,v12,v11,s13\n"
AW "a237,v12\n"
AR "v12,a18\n"
"v_subrev_f32 v12,s13,v12\n"
"v_fma_f32 v12,v12,v11,s13\n"
AW "a18,v12\n"
AR "v12,a38\n"
"v_subrev_f32 v12,s13,v12\n"
"v_fma_f32 v12,v12,v11,s13\n"
AW "a38,v12\n"
AR "v12,a58\n"
"v_subrev_f32 v12,s13,v12\n"
"v_fma_f32 v12,v12,v11,s13\n"
AW "a58,v12\n"
AR "v12,a78\n"
"v_subrev_f32 v12,s13,v12\n"
"v_fma_f32 v12,v12,v11,s13\n"
AW "a78,v12\n"
AR "v12,a98\n"
"v_subrev_f32 v12,s13,v12\n"
"v_fma_f32 v12,v12,v11,s13\n"
AW "a98,v12\n"
AR "v12,a118\n"
"v_subrev_f32 v12,s13,v12\n"
"v_fma_f32 v12,v12,v11,s13\n"
AW "a118,v12\n"
AR "v12,a138\n"
"v_subrev_f32 v12,s13,v12\n"
"v_fma_f32 v12,v12,v11,s13\n"
AW "a138,v12\n"
AR "v12,a158\n"
"v_subrev_f32 v12,s13,v12\n"
"v_fma_f32 v12,v12,v11,s13\n"
AW "a158,v12\n"
AR "v12,a178\n"
"v_subrev_f32 v12,s13,v12\n"
"v_fma_f32 v12,v12,v11,s13\n"
AW "a178,v12\n"
AR "v12,a198\n"
"v_subrev_f32 v12,s13,v12\n"
"v_fma_f32 v12,v12,v11,s13\n"
AW "a198,v12\n"
AR "v12,a218\n"
"v_subrev_f32 v12,s13,v12\n"
"v_fma_f32 v12,v12,v11,s13\n"
AW "a218,v12\n"
AR "v12,a238\n"
"v_subrev_f32 v12,s13,v12\n"
"v_fma_f32 v12,v12,v11,s13\n"
AW "a238,v12\n"
AR "v12,a19\n"
"v_subrev_f32 v12,s13,v12\n"
"v_fma_f32 v12,v12,v11,s13\n"
AW "a19,v12\n"
AR "v12,a39\n"
"v_subrev_f32 v12,s13,v12\n"
"v_fma_f32 v12,v12,v11,s13\n"
AW "a39,v12\n"
AR "v12,a59\n"
"v_subrev_f32 v12,s13,v12\n"
"v_fma_f32 v12,v12,v11,s13\n"
AW "a59,v12\n"
AR "v12,a79\n"
"v_subrev_f32 v12,s13,v12\n"
"v_fma_f32 v12,v12,v11,s13\n"
AW "a79,v12\n"
AR "v12,a99\n"
"v_subrev_f32 v12,s13,v12\n"
"v_fma_f32 v12,v12,v11,s13\n"
AW "a99,v12\n"
AR "v12,a119\n"
"v_subrev_f32 v12,s13,v12\n"
"v_fma_f32 v12,v12,v11,s13\n"
AW "a119,v12\n"
AR "v12,a139\n"
"v_subrev_f32 v12,s13,v12\n"
"v_fma_f32 v12,v12,v11,s13\n"
AW "a139,v12\n"
AR "v12,a159\n"
"v_subrev_f32 v12,s13,v12\n"
"v_fma_f32 v12,v12,v11,s13\n"
AW "a159,v12\n"
AR "v12,a179\n"
"v_subrev_f32 v12,s13,v12\n"
"v_fma_f32 v12,v12,v11,s13\n"
AW "a179,v12\n"
AR "v12,a199\n"
"v_subrev_f32 v12,s13,v12\n"
"v_fma_f32 v12,v12,v11,s13\n"
AW "a199,v12\n"
AR "v12,a219\n"
"v_subrev_f32 v12,s13,v12\n"
"v_fma_f32 v12,v12,v11,s13\n"
AW "a219,v12\n"
AR "v12,a239\n"
"v_subrev_f32 v12,s13,v12\n"
"v_fma_f32 v12,v12,v11,s13\n"
AW "a239,v12\n"
"v_mul_f32 v61,v56,v11\n"
"v_sub_f32 v48,v48,v10\n"
"v_sub_f32 v49,v49,v10\n"
"v_sub_f32 v50,v50,v10\n"
"v_sub_f32 v51,v51,v10\n"
"v_sub_f32 v88,v88,v10\n"
"v_sub_f32 v89,v89,v10\n"
"v_sub_f32 v90,v90,v10\n"
"v_sub_f32 v91,v91,v10\n"
"v_sub_f32 v108,v108,v10\n"
"v_sub_f32 v109,v109,v10\n"
"v_sub_f32 v110,v110,v10\n"
"v_sub_f32 v111,v111,v10\n"
"v_sub_f32 v128,v128,v10\n"
"v_sub_f32 v129,v129,v10\n"
"v_sub_f32 v130,v130,v10\n"
"v_sub_f32 v131,v131,v10\n"
"v_exp_f32 v108,v108\n"
"v_exp_f32 v109,v109\n"
"v_exp_f32 v110,v110\n"
"v_exp_f32 v111,v111\n"
"v_exp_f32 v128,v128\n"
"v_exp_f32 v129,v129\n"
"v_exp_f32 v130,v130\n"
"v_exp_f32 v131,v131\n"
"v_add_f32 v61,v61,v108\n"
"v_add_f32 v61,v61,v109\n"
"v_add_f32 v61,v61,v110\n"
"v_add_f32 v61,v61,v111\n"
"v_add_f32 v61,v61,v128\n"
"v_add_f32 v61,v61,v129\n"
"v_add_f32 v61,v61,v130\n"
"v_add_f32_e64 v61,v61,v131\n"
CV "v108,v108,v109\n"
CV "v109,v110,v111\n"
CV "v110,v128,v129\n"
CV "v111,v130,v131\n"
"s_nop 7\n"
"s_setpc_b64 s[52:53]\n"
".Ldone:\n"
    : "+{v0}"(tid), "+{s23}"(bid), "+{s[20:21]}"(ka)
    :
    : "v1", "v2", "v3", "v4", "v5", "v6", "v7", "v8", "v9", "v10", "v11", "v12", "v13", "v14", "v15", "v16", "v17", "v18", "v19", "v20", "v21", "v22", "v23", "v24", "v25", "v26", "v27", "v28", "v29", "v30", "v31", "v32", "v33", "v34", "v35", "v36", "v37", "v38", "v39", "v40", "v41", "v42", "v43", "v44", "v45", "v46", "v47", "v48", "v49", "v50", "v51", "v52", "v53", "v54", "v55", "v56", "v57", "v58", "v59", "v60", "v61", "v62", "v63", "v64", "v65", "v66", "v67", "v68", "v69", "v70", "v71", "v72", "v73", "v74", "v75", "v76", "v77", "v78", "v79", "v80", "v81", "v82", "v83", "v84", "v85", "v86", "v87", "v88", "v89", "v90", "v91", "v92", "v93", "v94", "v95", "v96", "v97", "v98", "v99", "v100", "v101", "v102", "v103", "v104", "v105", "v106", "v107", "v108", "v109", "v110", "v111", "v112", "v113", "v114", "v115", "v116", "v117", "v118", "v119", "v120", "v121", "v122", "v123", "v124", "v125", "v126", "v127", "v128", "v129", "v130", "v131", "v132", "v133", "v134", "v135", "v136", "v137", "v138", "v139", "v140", "v141", "v142", "v143", "v144", "v145", "v146", "v147", "v148", "v149", "v150", "v151", "v152", "v153", "v154", "v155", "v156", "v157", "v158", "v159", "v160", "v161", "v162", "v163", "v164", "v165", "v166", "v167", "v168", "v169", "v170", "v171", "v172", "v173", "v174", "v175", "v176", "v177", "v178", "v179", "v180", "v181", "v182", "v183", "v184", "v185", "v186", "v187", "v188", "v189", "v190", "v191", "v192", "v193", "v194", "v195", "v196", "v197", "v198", "v199", "v200", "v201", "v202", "v203", "v204", "v205", "v206", "v207", "v208", "v209", "v210", "v211", "v212", "v213", "v214", "v215", "v216", "v217", "v218", "v219", "v220", "v221", "v222", "v223", "v224", "v225", "v226", "v227", "v228", "v229", "v230", "v231", "v232", "v233", "v234", "v235", "v236", "v237", "v238", "v239", "v240", "v241", "v242", "v243", "v244", "v245", "v246", "v247", "v248", "v249", "v250", "v251", "v252", "v253", "v254", "v255", "a0", "a1", "a2", "a3", "a4", "a5", "a6", "a7", "a8", "a9", "a10", "a11", "a12", "a13", "a14", "a15", "a16", "a17", "a18", "a19", "a20", "a21", "a22", "a23", "a24", "a25", "a26", "a27", "a28", "a29", "a30", "a31", "a32", "a33", "a34", "a35", "a36", "a37", "a38", "a39", "a40", "a41", "a42", "a43", "a44", "a45", "a46", "a47", "a48", "a49", "a50", "a51", "a52", "a53", "a54", "a55", "a56", "a57", "a58", "a59", "a60", "a61", "a62", "a63", "a64", "a65", "a66", "a67", "a68", "a69", "a70", "a71", "a72", "a73", "a74", "a75", "a76", "a77", "a78", "a79", "a80", "a81", "a82", "a83", "a84", "a85", "a86", "a87", "a88", "a89", "a90", "a91", "a92", "a93", "a94", "a95", "a96", "a97", "a98", "a99", "a100", "a101", "a102", "a103", "a104", "a105", "a106", "a107", "a108", "a109", "a110", "a111", "a112", "a113", "a114", "a115", "a116", "a117", "a118", "a119", "a120", "a121", "a122", "a123", "a124", "a125", "a126", "a127", "a128", "a129", "a130", "a131", "a132", "a133", "a134", "a135", "a136", "a137", "a138", "a139", "a140", "a141", "a142", "a143", "a144", "a145", "a146", "a147", "a148", "a149", "a150", "a151", "a152", "a153", "a154", "a155", "a156", "a157", "a158", "a159", "a160", "a161", "a162", "a163", "a164", "a165", "a166", "a167", "a168", "a169", "a170", "a171", "a172", "a173", "a174", "a175", "a176", "a177", "a178", "a179", "a180", "a181", "a182", "a183", "a184", "a185", "a186", "a187", "a188", "a189", "a190", "a191", "a192", "a193", "a194", "a195", "a196", "a197", "a198", "a199", "a200", "a201", "a202", "a203", "a204", "a205", "a206", "a207", "a208", "a209", "a210", "a211", "a212", "a213", "a214", "a215", "a216", "a217", "a218", "a219", "a220", "a221", "a222", "a223", "a224", "a225", "a226", "a227", "a228", "a229", "a230", "a231", "a232", "a233", "a234", "a235", "a236", "a237", "a238", "a239", "a240", "a241", "a242", "a243", "a244", "a245", "a246", "a247", "a248", "a249", "a250", "a251", "a252", "a253", "a254", "a255", "s0", "s1", "s2", "s3", "s4", "s5", "s6", "s7", "s8", "s9", "s10", "s11", "s12", "s13", "s14", "s15", "s16", "s17", "s18", "s19", "s22", "s24", "s25", "s26", "s27", "s28", "s29", "s30", "s31", "s32", "s33", "s34", "s35", "s36", "s37", "s38", "s39", "s40", "s41", "s42", "s43", "s44", "s45", "s46", "s47", "s48", "s49", "s50", "s51", "s52", "s53", "s54", "s55", "s56", "s57", "s58", "s59", "s60", "s61", "s62", "s63", "s64", "s65", "s66", "s67", "s68", "s69", "s70", "s71", "s72", "s73", "s74", "s75", "s76", "s77", "s78", "s79", "s80", "s81", "s82", "s83", "s84", "s85", "s86", "s87", "s88", "s89", "s90", "s91", "s92", "s93", "s94", "s95", "s96", "s97", "s98", "s99", "s100", "s101", "vcc", "scc", "memory");
}

extern "C" void kernel_launch(void* const* d_in, const int* in_sizes, int n_in, void* d_out, int out_size, void* d_ws, size_t ws_size, hipStream_t stream) {
  Ptrs P;
  for (int br = 0; br < 2; ++br) { P.x[br] = d_in[5 * br]; P.k[br] = d_in[5 * br + 1]; P.v[br] = d_in[5 * br + 2]; P.wq[br] = d_in[5 * br + 3]; P.wo[br] = d_in[5 * br + 4]; }
  (void)hipFuncSetAttribute((const void*)fused_kernel, hipFuncAttributeMaxDynamicSharedMemorySize, LDS1);
  fused_kernel<<<256, 256, LDS1, stream>>>(P, (char*)d_ws, (char*)d_out);
}
```

```cpp
#include <hip/hip_runtime.h>
#include <hip/hip_bf16.h>

#define MF "v_mfma_f32_16x16x32_bf16 "
#define TR "ds_read_b64_tr_b16 "
#define AR "v_accvgpr_read_b32 "
#define AW "v_accvgpr_write_b32 "
#define CV "v_cvt_pk_bf16_f32 "
#define LD "buffer_load_dwordx4 "
constexpr unsigned LDS1 = 160 * 1024;

struct Ptrs { const void* x[2]; const void* k[2]; const void* v[2]; const void* wq[2]; const void* wo[2]; };

__global__ __launch_bounds__(256, 1) __attribute__((aligned(4096))) void fused_kernel(Ptrs P, char* __restrict__ ws, char* __restrict__ out) {
  unsigned tid = threadIdx.x, bid = blockIdx.x;
  unsigned long long ka = (unsigned long long)__builtin_amdgcn_kernarg_segment_ptr();
  asm volatile(
".p2align 5\n"
".Lcode0:\n"
"s_and_b32 s58,s23,4\n"
"s_lshl_b32 s58,s58,1\n"
"s_load_dwordx2 s[28:29],s[20:21],s58 offset:48\n"
"s_load_dwordx2 s[24:25],s[20:21],s58\n"
"s_load_dwordx2 s[32:33],s[20:21],s58 offset:16\n"
"s_load_dwordx2 s[36:37],s[20:21],s58 offset:32\n"
"s_load_dwordx2 s[48:49],s[20:21],s58 offset:64\n"
"s_memtime s[100:101]\n"
"s_load_dwordx2 s[40:41],s[20:21],0x50\n"
"s_load_dwordx2 s[44:45],s[20:21],0x58\n"
"v_readfirstlane_b32 s22,v0\n"
"v_and_b32 v9,63,v0\n"
"v_bfe_u32 v10,v0,3,3\n"
"v_and_b32 v11,7,v0\n"
"s_lshr_b32 s22,s22,6\n"
"s_or_b32 s22,s22,s58\n"
"s_lshr_b32 s59,s23,4\n"
"s_and_b32 s58,s59,8\n"
"s_mul_i32 s59,s59,4112\n"
"s_or_b32 s22,s22,s59\n"
"s_and_b32 s59,s23,3\n"
"s_lshl_b32 s59,s59,1\n"
"s_bfe_u32 s60,s23,0x10003\n"
"s_or_b32 s59,s59,s60\n"
"s_lshl_b32 s60,s59,20\n"
"s_or_b32 s22,s22,s60\n"
"s_or_b32 s59,s59,s58\n"
"s_mul_i32 s59,s59,65792\n"
"s_xor_b32 s59,s59,524288\n"
"s_or_b32 s22,s22,s59\n"
"v_lshrrev_b32 v6,2,v0\n"
"v_and_b32 v6,4,v6\n"
"v_xor_b32 v6,v6,v11\n"
"v_lshlrev_b32_e64 v6,4,v6\n"
"s_mov_b32 s58,12288\n"
"v_mad_u32_u24 v6,v10,s58,v6\n"
"v_bfe_u32 v4,v0,4,3\n"
"v_xor_b32 v4,v4,v11\n"
"v_lshlrev_b32 v4,4,v4\n"
"s_mov_b32 s58,1280\n"
"v_mad_u32_u24 v4,v10,s58,v4\n"
"v_lshlrev_b32_e64 v9,4,v9\n"
"s_mov_b32 s26,3276800\n"
"s_mov_b32 s27,0x00020000\n"
"s_mov_b32 s30,7864320\n"
"s_mov_b32 s31,0x00020000\n"
"s_mov_b32 s34,0\n"
"s_mov_b32 s35,0x00020000\n"
"s_mov_b32 s38,0\n"
"s_mov_b32 s39,0x00020000\n"
"s_mov_b32 s42,268435456\n"
"s_mov_b32 s43,0x00020000\n"
"s_mov_b32 s46,6553600\n"
"s_mov_b32 s47,0x00020000\n"
"s_mov_b32 s50,7864320\n"
"s_mov_b32 s51,0x00020000\n"
"s_mov_b32 s98,20000\n"
"s_bfe_u32 s56,s22,0x20000\n"
"s_bfe_u32 s72,s22,0x10003\n"
"s_lshr_b32 s73,s56,1\n"
"s_and_b32 s74,s56,1\n"
"s_mul_i32 s62,s56,4096\n"
"s_mul_i32 s63,s56,5120\n"
"s_add_u32 s63,s63,98304\n"
"s_bfe_u32 s58,s22,0x40004\n"
"s_bfe_u32 s59,s22,0x40008\n"
"s_mul_i32 s68,s58,204800\n"
"s_mul_i32 s60,s56,51200\n"
"s_add_u32 s68,s68,s60\n"
"s_lshl_b32 s59,s59,1\n"
"s_add_u32 s59,s59,s73\n"
"s_mul_i32 s66,s59,384\n"
"s_mul_i32 s60,s74,393216\n"
"s_add_u32 s66,s66,s60\n"
"s_lshr_b32 s58,s22,6\n"
"s_xor_b32 s58,s58,s22\n"
"s_bfe_u32 s58,s58,0x10004\n"
"s_mul_i32 s81,s58,640\n"
"s_mul_i32 s60,s81,6144\n"
"s_add_u32 s66,s66,s60\n"
"v_xor_b32 v19,64,v4\n"
"v_add_u32 v16,97280,v6\n"
"v_add_u32 v17,194560,v6\n"
"v_add_u32 v18,291840,v6\n"
"s_waitcnt lgkmcnt(0)\n"
"s_mov_b32 s80,s100\n"
"s_mov_b32 s87,2\n"
"s_add_u32 m0,s62,0\n"
"s_add_u32 s82,s66,0\n"
"s_sub_u32 s15,s82,7864320\n"
"s_min_u32 s82,s82,s15\n"
LD "v6,s[28:31],s82 offen lds\n"
LD "v16,s[28:31],s82 offen offset:1024 lds\n"
LD "v17,s[28:31],s82 offen offset:2048 lds\n"
LD "v18,s[28:31],s82 offen offset:3072 lds\n"
"s_add_u32 s14,s81,0\n"
"s_sub_u32 s15,s14,1280\n"
"s_min_u32 s14,s14,s15\n"
"s_add_u32 s14,s14,s68\n"
"s_add_u32 s84,s14,0\n"
"s_add_u32 m0,s63,0\n"
"s_nop 0\n"
LD "v4,s[24:27],s84 offen lds\n"
"s_add_u32 s84,s14,9216\n"
LD "v19,s[24:27],s84 offen offset:1024 lds\n"
"s_add_u32 s84,s14,18432\n"
LD "v4,s[24:27],s84 offen offset:2048 lds\n"
"s_add_u32 s84,s14,27648\n"
LD "v19,s[24:27],s84 offen offset:3072 lds\n"
"s_add_u32 s84,s14,40960\n"
"s_add_u32 m0,s63,4096\n"
"s_nop 0\n"
LD "v4,s[24:27],s84 offen lds\n"
"s_lshr_b32 s58,s23,3\n"
"s_mul_i32 s59,s58,7\n"
"s_and_b32 s60,s23,7\n"
"s_mul_i32 s60,s60,3\n"
"s_add_u32 s59,s59,s60\n"
"s_and_b32 s59,s59,15\n"
"s_lshl_b32 s59,s59,8\n"
"s_or_b32 s58,s58,s59\n"
"s_and_b32 s59,s23,3\n"
"s_lshl_b32 s59,s59,5\n"
"s_or_b32 s58,s58,s59\n"
"s_bfe_u32 s59,s23,0x10000\n"
"s_lshl_b32 s59,s59,12\n"
"s_or_b32 s58,s58,s59\n"
"s_lshl_b32 s23,s23,16\n"
"s_or_b32 s23,s23,s58\n"
"v_bfe_u32 v20,v0,4,2\n"
"v_mul_u32_u24 v21,384,v10\n"
"v_xor_b32 v22,v11,v20\n"
"s_add_u32 m0,s62,16384\n"
"s_add_u32 s82,s66,128\n"
"s_sub_u32 s15,s82,7864320\n"
"s_min_u32 s82,s82,s15\n"
LD "v6,s[28:31],s82 offen lds\n"
"v_lshl_add_u32 v7,v22,4,v21\n"
"s_lshl_b32 s58,s74,6\n"
"v_xor_b32 v7,s58,v7\n"
"v_lshlrev_b32 v22,1,v20\n"
"v_xor_b32 v22,v11,v22\n"
"v_lshl_add_u32 v8,v22,4,v21\n"
"v_bfe_u32 v21,v0,1,3\n"
"v_xor_b32 v21,v21,v20\n"
"v_lshlrev_b32 v21,4,v21\n"
"v_lshl_add_u32 v21,v11,7,v21\n"
"v_bfe_u32 v22,v0,3,1\n"
"s_mul_i32 s58,s73,12288\n"
"v_mul_u32_u24 v23,3072,v22\n"
"v_add3_u32 v1,v23,v21,s58\n"
"s_mul_i32 s59,s74,10240\n"
"s_add_u32 s59,s59,98304\n"
"v_lshl_add_u32 v2,v22,10,v21\n"
"v_add_u32_e64 v2,s59,v2\n"
"v_bfe_u32 v21,v0,2,3\n"
LD "v16,s[28:31],s82 offen offset:1024 lds\n"
"v_lshlrev_b32_e64 v21,7,v21\n"
"v_bfe_u32 v23,v0,3,2\n"
"v_lshl_add_u32 v21,v23,5,v21\n"
"v_and_b32_e64 v23,3,v0\n"
"v_lshl_add_u32 v21,v23,3,v21\n"
"v_bfe_u32 v23,v0,5,1\n"
"v_mul_u32_u24 v3,3072,v23\n"
"v_add3_u32 v3,v3,v21,s58\n"
"s_bfe_u32 s58,s22,0x40004\n"
"s_mul_i32 s58,s58,409600\n"
"s_mul_i32 s59,s72,1280\n"
"s_add_u32 s58,s58,s59\n"
"v_and_b32 v21,15,v0\n"
"v_mul_u32_u24 v21,2560,v21\n"
"v_lshl_add_u32 v21,v23,4,v21\n"
"v_bfe_u32 v23,v0,4,1\n"
"v_lshl_add_u32 v21,v23,5,v21\n"
"v_add_u32_e64 v12,s58,v21\n"
"v_bfe_u32 v21,v0,2,2\n"
LD "v17,s[28:31],s82 offen offset:2048 lds\n"
"v_lshlrev_b32 v21,7,v21\n"
"v_lshl_add_u32 v21,v20,10,v21\n"
"v_lshl_add_u32 v21,v22,6,v21\n"
"v_and_b32 v23,3,v0\n"
"v_lshl_add_u32 v21,v23,4,v21\n"
"s_lshl_b32 s58,s73,13\n"
"v_add_u32 v0,s58,v21\n"
"s_getpc_b64 s[52:53]\n"
".Lcts:\n"
"s_sub_u32 s52,s52,.Lcts-.Lcode0\n"
"s_subb_u32 s53,s53,0\n"
"s_bfe_u32 s58,s23,0x50000\n"
"s_lshl_b32 s58,s58,2\n"
"s_add_u32 s58,s58,s56\n"
"s_mul_i32 s58,s58,1280\n"
"v_min_u32 v15,144,v9\n"
"v_lshl_add_u32 v15,v15,3,s58\n"
"v_min_u32 v15,.Lcode1-.Lcode0,v15\n"
LD "v18,s[28:31],s82 offen offset:3072 lds\n"
"s_bfe_u32 s58,s22,0x4000c\n"
"s_bfe_u32 s59,s22,0x40010\n"
"s_mul_i32 s69,s58,204800\n"
"s_mul_i32 s60,s56,51200\n"
"s_add_u32 s69,s69,s60\n"
"s_lshl_b32 s59,s59,1\n"
"s_add_u32 s59,s59,s73\n"
"s_mul_i32 s67,s59,384\n"
"s_mul_i32 s60,s74,393216\n"
"s_add_u32 s67,s67,s60\n"
"s_mul_i32 s60,s81,6144\n"
"s_add_u32 s67,s67,s60\n"
"s_bfe_u32 s58,s22,0x40004\n"
"s_bfe_u32 s59,s22,0x40008\n"
"s_lshl_b32 s59,s59,1\n"
"s_add_u32 s88,s59,s74\n"
"s_mul_i32 s88,s88,245760\n"
"s_add_u32 s59,s59,s73\n"
"s_lshl_b32 s70,s58,5\n"
"s_add_u32 m0,s62,32768\n"
"s_add_u32 s82,s66,256\n"
"s_sub_u32 s15,s82,7864320\n"
"s_min_u32 s82,s82,s15\n"
LD "v6,s[28:31],s82 offen lds\n"
"s_add_u32 s70,s70,s59\n"
"s_mul_i32 s70,s70,196608\n"
"s_bfe_u32 s58,s22,0x4000c\n"
"s_bfe_u32 s59,s22,0x40010\n"
"s_lshl_b32 s59,s59,1\n"
"s_add_u32 s89,s59,s74\n"
"s_mul_i32 s89,s89,245760\n"
"s_add_u32 s59,s59,s73\n"
"s_lshl_b32 s71,s58,5\n"
"s_add_u32 s71,s71,s59\n"
"s_mul_i32 s71,s71,196608\n"
"s_bitcmp1_b32 s23,12\n"
"s_cselect_b32 s58,s71,s70\n"
"s_cselect_b32 s71,s70,s71\n"
"s_mov_b32 s70,s58\n"
"s_cselect_b32 s58,s89,s88\n"
"s_cselect_b32 s89,s88,s89\n"
"s_mov_b32 s88,s58\n"
"s_cselect_b32 s69,98,1\n"
LD "v16,s[28:31],s82 offen offset:1024 lds\n"
"s_mov_b32 s54,1037384597\n"
"s_mov_b32 s55,1037384597\n"
"s_bfe_u32 s58,s22,0x40004\n"
"s_lshl_b32 s59,s72,4\n"
"s_add_u32 s59,s59,s58\n"
"s_lshl_b32 s86,s59,12\n"
"s_add_u32 s86,s86,100663296\n"
"s_mul_i32 s93,s59,1638400\n"
"s_add_u32 s93,s93,33554432\n"
"s_bfe_u32 s58,s22,0x30014\n"
"s_lshl_b32 s78,s58,1\n"
"s_add_u32 s78,s78,s74\n"
"s_lshl_b32 s94,s78,2\n"
"s_add_u32 s94,s94,s86\n"
"s_lshl_b32 s92,s58,2\n"
"s_add_u32 s92,s92,s56\n"
"s_mul_i32 s91,s58,204800\n"
"s_add_u32 s91,s91,s93\n"
"s_mul_i32 s75,s56,30720\n"
LD "v17,s[28:31],s82 offen offset:2048 lds\n"
"s_add_u32 s75,s75,s91\n"
"s_add_u32 s75,s75,0\n"
"s_mul_i32 s58,s74,5120\n"
"s_add_u32 s91,s91,s58\n"
"s_mov_b32 s90,0xf0000000\n"
"s_mov_b32 s77,0xf0000000\n"
"s_mov_b32 s85,0xf0000000\n"
AW "a240,v0\n"
AW "a244,v1\n"
AW "a241,v2\n"
AW "a245,v3\n"
AW "a242,v4\n"
AW "a243,v6\n"
AW "a252,v12\n"
"s_mov_b32 s57,0\n"
"s_mov_b32 s83,0\n"
LD "v18,s[28:31],s82 offen offset:3072 lds\n"
"global_load_dword a249,v15,s[52:53]\n"
AR "v0,a240\n"
AR "v2,a241\n"
AR "v4,a242\n"
AR "v6,a243\n"
"v_xor_b32 v1,64,v0\n"
"v_xor_b32 v3,64,v2\n"
"v_xor_b32_e64 v5,64,v4\n"
"v_add_u32 v11,97280,v6\n"
"v_add_u32 v12,194560,v6\n"
"v_add_u32 v13,291840,v6\n"
"s_mov_b64 vcc,0\n"
"s_waitcnt vmcnt(9) lgkmcnt(0)\n"
"s_barrier\n"
"s_nop 0\n"
TR "a[200:201],v0\n"
TR "a[202:203],v0 offset:512\n"
TR "a[204:205],v0 offset:4096\n"
TR "a[206:207],v0 offset:4608\n"
"ds_read_b128 a[60:63],v2\n"
"ds_read_b128 a[100:103],v3\n"
"ds_read_b128 a[64:67],v2 offset:2048\n"
"ds_read_b128 a[104:107],v3 offset:2048\n"
"ds_read_b128 a[68:71],v2 offset:4096\n"
"ds_read_b128 a[108:111],v3 offset:4096\n"
"ds_read_b128 a[72:75],v2 offset:6144\n"
"ds_read_b128 a[112:115],v3 offset:6144\n"
"ds_read_b128 a[76:79],v2 offset:8192\n"
"ds_read_b128 a[116:119],v3 offset:8192\n"
TR "a[208:209],v0 offset:8\n"
TR "a[210:211],v0 offset:520\n"
TR "a[212:213],v0 offset:4104\n"
TR "a[214:215],v0 offset:4616\n"
"s_waitcnt lgkmcnt(0)\n"
MF "v[16:19],a[200:203],a[60:63],0\n"
TR "a[216:217],v1\n"
MF "v[16:19],a[204:207],a[100:103],v[16:19]\n"
"s_add_u32 s14,s81,128\n"
TR "a[218:219],v1 offset:512\n"
MF "v[24:27],a[204:207],a[104:107],0\n"
"s_sub_u32 s15,s14,1280\n"
"v_lshrrev_b32 v15,5,v9\n"
"v_min_u32 v15,11,v15\n"
MF "v[24:27],a[200:203],a[64:67],v[24:27]\n"
"s_min_u32 s14,s14,s15\n"
TR "a[220:221],v1 offset:4096\n"
"v_mul_u32_u24 v15,160,v15\n"
MF "v[32:35],a[200:203],a[68:71],0\n"
"s_add_u32 s14,s14,s68\n"
TR "a[222:223],v1 offset:4608\n"
"v_and_or_b32 v15,v9,16,v15\n"
MF "v[32:35],a[204:207],a[108:111],v[32:35]\n"
"s_add_u32 s84,s14,0\n"
"v_max3_f32 v220,|v16|,|v17|,|v18|\n"
"v_max_f32_e64 v220,v220,|v19|\n"
MF "v[40:43],a[204:207],a[112:115],0\n"
"s_add_u32 m0,s63,20480\n"
"v_readlane_b32 s12,v220,3\n"
"v_readlane_b32 s6,v220,22\n"
MF "v[40:43],a[200:203],a[72:75],v[40:43]\n"
"s_min_u32 s12,s12,s6\n"
"v_readlane_b32 s7,v220,41\n"
"v_readlane_b32 s8,v220,60\n"
MF "v[48:51],a[200:203],a[76:79],0\n"
"s_min_u32 s7,s7,s8\n"
"v_lshlrev_b32 v15,3,v15\n"
MF "v[48:51],a[204:207],a[116:119],v[48:51]\n"
LD "v4,s[24:27],s84 offen lds\n"
MF "v[52:55],a[212:215],a[116:119],0\n"
"s_min_u32 s12,s12,s7\n"
TR "a[200:201],v1 offset:8\n"
MF "v[52:55],a[208:211],a[76:79],v[52:55]\n"
"s_and_b32 s12,s12,0x7f800000\n"
TR "a[202:203],v1 offset:520\n"
MF "v[44:47],a[208:211],a[72:75],0\n"
"s_sub_u32 s12,s12,0x14000000\n"
MF "v[44:47],a[212:215],a[112:115],v[44:47]\n"
"s_cmp_lt_u32 s12,0x50000000\n"
"s_cselect_b32 s12,s12,0xe8400000\n"
TR "a[204:205],v1 offset:4104\n"
MF "v[36:39],a[212:215],a[108:111],0\n"
"s_add_u32 s12,s12,0x17c00000\n"
TR "a[206:207],v1 offset:4616\n"
MF "v[36:39],a[208:211],a[68:71],v[36:39]\n"
"s_bfe_u32 s60,s23,0x70000\n"
"v_mov_b32 v220,s12\n"
"v_mov_b32 v221,s12\n"
MF "v[28:31],a[208:211],a[64:67],0\n"
"s_add_u32 s84,s14,9216\n"
"v_mov_b32 v222,s12\n"
"v_mov_b32 v223,s12\n"
MF "v[28:31],a[212:215],a[104:107],v[28:31]\n"
LD "v5,s[24:27],s84 offen offset:1024 lds\n"
MF "v[20:23],a[212:215],a[100:103],0\n"
"s_waitcnt vmcnt(7) lgkmcnt(0)\n"
MF "v[20:23],a[208:211],a[60:63],v[20:23]\n"
"s_barrier\n"
MF "v[56:59],a[216:219],a[60:63],v[220:223]\n"
"s_lshl_b32 s60,s60,2\n"
TR "a[208:209],v0 offset:16384\n"
MF "v[56:59],a[220:223],a[100:103],v[56:59]\n"
"s_add_u32 s84,s14,18432\n"
TR "a[210:211],v0 offset:16896\n"
MF "v[64:67],a[220:223],a[104:107],v[220:223]\n"
LD "v4,s[24:27],s84 offen offset:2048 lds\n"
MF "v[64:67],a[216:219],a[64:67],v[64:67]\n"
"s_add_u32 s60,s60,s56\n"
TR "a[212:213],v0 offset:20480\n"
MF "v[72:75],a[216:219],a[68:71],v[220:223]\n"
"s_mul_i32 s60,s60,15360\n"
TR "a[214:215],v0 offset:20992\n"
MF "v[72:75],a[220:223],a[108:111],v[72:75]\n"
MF "v[80:83],a[220:223],a[112:115],v[220:223]\n"
"s_add_u32 s84,s14,27648\n"
MF "v[80:83],a[216:219],a[72:75],v[80:83]\n"
LD "v5,s[24:27],s84 offen offset:3072 lds\n"
MF "v[88:91],a[216:219],a[76:79],v[220:223]\n"
MF "v[88:91],a[220:223],a[116:119],v[88:91]\n"
MF "v[92:95],a[204:207],a[116:119],v[220:223]\n"
"s_add_u32 s84,s14,40960\n"
TR "a[216:217],v0 offset:16392\n"
MF "v[92:95],a[200:203],a[76:79],v[92:95]\n"
"s_add_u32 m0,s63,24576\n"
TR "a[218:219],v0 offset:16904\n"
MF "v[84:87],a[200:203],a[72:75],v[220:223]\n"
LD "v4,s[24:27],s84 offen lds\n"
MF "v[84:87],a[204:207],a[112:115],v[84:87]\n"
"s_add_u32 m0,s62,49152\n"
TR "a[220:221],v0 offset:20488\n"
MF "v[76:79],a[204:207],a[108:111],v[220:223]\n"
"s_add_u32 s82,s66,786432\n"
TR "a[222:223],v0 offset:21000\n"
MF "v[76:79],a[200:203],a[68:71],v[76:79]\n"
"s_sub_u32 s15,s82,7864320\n"
MF "v[68:71],a[200:203],a[64:67],v[220:223]\n"
"s_min_u32 s82,s82,s15\n"
MF "v[68:71],a[204:207],a[104:107],v[68:71]\n"
LD "v6,s[28:31],s82 offen lds\n"
MF "v[60:63],a[204:207],a[100:103],v[220:223]\n"
MF "v[60:63],a[200:203],a[60:63],v[60:63]\n"
"s_waitcnt lgkmcnt(4)\n"
MF "v[96:99],a[208:211],a[60:63],v[220:223]\n"
TR "a[200:201],v1 offset:16384\n"
MF "v[96:99],a[212:215],a[100:103],v[96:99]\n"
TR "a[202:203],v1 offset:16896\n"
MF "v[104:107],a[212:215],a[104:107],v[220:223]\n"
LD "v11,s[28:31],s82 offen offset:1024 lds\n"
MF "v[104:107],a[208:211],a[64:67],v[104:107]\n"
TR "a[204:205],v1 offset:20480\n"
MF "v[112:115],a[208:211],a[68:71],v[220:223]\n"
TR "a[206:207],v1 offset:20992\n"
MF "v[112:115],a[212:215],a[108:111],v[112:115]\n"
MF "v[120:123],a[212:215],a[112:115],v[220:223]\n"
MF "v[120:123],a[208:211],a[72:75],v[120:123]\n"
LD "v12,s[28:31],s82 offen offset:2048 lds\n"
MF "v[128:131],a[208:211],a[76:79],v[220:223]\n"
MF "v[128:131],a[212:215],a[116:119],v[128:131]\n"
"s_waitcnt lgkmcnt(4)\n"
MF "v[132:135],a[220:223],a[116:119],v[220:223]\n"
TR "a[208:209],v1 offset:16392\n"
MF "v[132:135],a[216:219],a[76:79],v[132:135]\n"
TR "a[210:211],v1 offset:16904\n"
MF "v[124:127],a[216:219],a[72:75],v[220:223]\n"
LD "v13,s[28:31],s82 offen offset:3072 lds\n"
MF "v[124:127],a[220:223],a[112:115],v[124:127]\n"
"s_add_u32 m0,s62,65536\n"
TR "a[212:213],v1 offset:20488\n"
MF "v[116:119],a[220:223],a[108:111],v[220:223]\n"
"s_add_u32 s82,s66,786560\n"
TR "a[214:215],v1 offset:21000\n"
MF "v[116:119],a[216:219],a[68:71],v[116:119]\n"
"s_sub_u32 s15,s82,7864320\n"
MF "v[108:111],a[216:219],a[64:67],v[220:223]\n"
"s_min_u32 s82,s82,s15\n"
MF "v[108:111],a[220:223],a[104:107],v[108:111]\n"
LD "v6,s[28:31],s82 offen lds\n"
MF "v[100:103],a[220:223],a[100:103],v[220:223]\n"
"s_waitcnt vmcnt(5) lgkmcnt(0)\n"
MF "v[100:103],a[216:219],a[60:63],v[100:103]\n"
"s_barrier\n"
MF "v[136:139],a[200:203],a[60:63],v[220:223]\n"
TR "a[216:217],v0 offset:32768\n"
MF "v[136:139],a[204:207],a[100:103],v[136:139]\n"
TR "a[218:219],v0 offset:33280\n"
MF "v[144:147],a[204:207],a[104:107],v[220:223]\n"
"ds_read_b128 a[140:143],v2 offset:20480\n"
MF "v[144:147],a[200:203],a[64:67],v[144:147]\n"
TR "a[220:221],v0 offset:36864\n"
MF "v[152:155],a[200:203],a[68:71],v[220:223]\n"
TR "a[222:223],v0 offset:37376\n"
MF "v[152:155],a[204:207],a[108:111],v[152:155]\n"
LD "v11,s[28:31],s82 offen offset:1024 lds\n"
MF "v[160:163],a[204:207],a[112:115],v[220:223]\n"
"ds_read_b128 a[180:183],v3 offset:20480\n"
MF "v[160:163],a[200:203],a[72:75],v[160:163]\n"
MF "v[168:171],a[200:203],a[76:79],v[220:223]\n"
MF "v[168:171],a[204:207],a[116:119],v[168:171]\n"
LD "v12,s[28:31],s82 offen offset:2048 lds\n"
MF "v[172:175],a[212:215],a[116:119],v[220:223]\n"
TR "a[200:201],v0 offset:32776\n"
MF "v[172:175],a[208:211],a[76:79],v[172:175]\n"
TR "a[202:203],v0 offset:33288\n"
MF "v[164:167],a[208:211],a[72:75],v[220:223]\n"
"ds_read_b128 a[144:147],v2 offset:22528\n"
MF "v[164:167],a[212:215],a[112:115],v[164:167]\n"
TR "a[204:205],v0 offset:36872\n"
MF "v[156:159],a[212:215],a[108:111],v[220:223]\n"
TR "a[206:207],v0 offset:37384\n"
MF "v[156:159],a[208:211],a[68:71],v[156:159]\n"
LD "v13,s[28:31],s82 offen offset:3072 lds\n"
MF "v[148:151],a[208:211],a[64:67],v[220:223]\n"
"ds_read_b128 a[184:187],v3 offset:22528\n"
MF "v[148:151],a[212:215],a[104:107],v[148:151]\n"
"s_add_u32 m0,s62,81920\n"
MF "v[140:143],a[212:215],a[100:103],v[220:223]\n"
"s_add_u32 s82,s66,786688\n"
"s_sub_u32 s15,s82,7864320\n"
"s_min_u32 s82,s82,s15\n"
MF "v[140:143],a[208:211],a[60:63],v[140:143]\n"
"s_waitcnt lgkmcnt(7)\n"
LD "v6,s[28:31],s82 offen lds\n"
MF "v[176:179],a[216:219],a[60:63],v[220:223]\n"
TR "a[208:209],v1 offset:32768\n"
MF "v[176:179],a[220:223],a[100:103],v[176:179]\n"
TR "a[210:211],v1 offset:33280\n"
MF "v[184:187],a[220:223],a[104:107],v[220:223]\n"
"ds_read_b128 a[148:151],v2 offset:24576\n"
MF "v[184:187],a[216:219],a[64:67],v[184:187]\n"
TR "a[212:213],v1 offset:36864\n"
MF "v[192:195],a[216:219],a[68:71],v[220:223]\n"
TR "a[214:215],v1 offset:37376\n"
MF "v[192:195],a[220:223],a[108:111],v[192:195]\n"
LD "v11,s[28:31],s82 offen offset:1024 lds\n"
MF "v[200:203],a[220:223],a[112:115],v[220:223]\n"
"ds_read_b128 a[188:191],v3 offset:24576\n"
MF "v[200:203],a[216:219],a[72:75],v[200:203]\n"
MF "v[208:211],a[216:219],a[76:79],v[220:223]\n"
MF "v[208:211],a[220:223],a[116:119],v[208:211]\n"
"s_waitcnt lgkmcnt(7)\n"
LD "v12,s[28:31],s82 offen offset:2048 lds\n"
MF "v[212:215],a[204:207],a[116:119],v[220:223]\n"
TR "a[216:217],v1 offset:32776\n"
MF "v[212:215],a[200:203],a[76:79],v[212:215]\n"
TR "a[218:219],v1 offset:33288\n"
MF "v[204:207],a[200:203],a[72:75],v[220:223]\n"
"ds_read_b128 a[152:155],v2 offset:26624\n"
MF "v[204:207],a[204:207],a[112:115],v[204:207]\n"
TR "a[220:221],v1 offset:36872\n"
MF "v[196:199],a[204:207],a[108:111],v[220:223]\n"
TR "a[222:223],v1 offset:37384\n"
MF "v[196:199],a[200:203],a[68:71],v[196:199]\n"
LD "v13,s[28:31],s82 offen offset:3072 lds\n"
MF "v[188:191],a[200:203],a[64:67],v[220:223]\n"
"v_add_u32 v0,0xc000,v0\n"
MF "v[188:191],a[204:207],a[104:107],v[188:191]\n"
"s_add_u32 s14,s81,256\n"
"v_add_u32 v1,0xc000,v1\n"
MF "v[180:183],a[204:207],a[100:103],v[220:223]\n"
"s_waitcnt vmcnt(8) lgkmcnt(0)\n"
"s_mov_b64 exec,0\n"
"s_bitcmp1_b32 s23,12\n"
"s_cbranch_scc1 .Lisy1\n"
"s_cmp_eq_u32 s56,0\n"
"s_cbranch_scc1 .Lpg8b\n"
"s_cmp_eq_u32 s56,1\n"
"s_cbranch_scc1 .Lpg9b\n"
"s_cmp_eq_u32 s56,2\n"
"s_cbranch_scc1 .Lpgt0ex_b\n"
"s_branch .Lpg10b\n"
".Lisy1:\n"
"s_cmp_eq_u32 s56,0\n"
"s_cbranch_scc1 .Lpgq0_c\n"
"s_cmp_eq_u32 s56,1\n"
"s_cbranch_scc1 .Lpg4c\n"
"s_cmp_eq_u32 s56,2\n"
"s_cbranch_scc1 .Lpg5c\n"
"s_branch .Lpg6c\n"
".Lisr1:\n"
"s_mov_b64 exec,-1\n"
".Lisx1:\n"
MF "v[180:183],a[200:203],a[60:63],v[180:183]\n"
"s_barrier\n"
MF "v[216:219],a[208:211],a[60:63],v[220:223]\n"
"s_sub_u32 s15,s14,1280\n"
TR "a[200:201],v0\n"
MF "v[216:219],a[212:215],a[100:103],v[216:219]\n"
"s_min_u32 s14,s14,s15\n"
TR "a[202:203],v0 offset:512\n"
MF "v[224:227],a[212:215],a[104:107],v[220:223]\n"
"s_add_u32 s14,s14,s68\n"
"ds_read_b128 a[192:195],v3 offset:26624\n"
MF "v[224:227],a[208:211],a[64:67],v[224:227]\n"
"s_add_u32 s84,s14,0\n"
TR "a[204:205],v0 offset:4096\n"
MF "v[232:235],a[208:211],a[68:71],v[220:223]\n"
"s_add_u32 m0,s63,40960\n"
TR "a[206:207],v0 offset:4608\n"
MF "v[232:235],a[212:215],a[108:111],v[232:235]\n"
LD "v4,s[24:27],s84 offen lds\n"
MF "v[240:243],a[212:215],a[112:115],v[220:223]\n"
"ds_read_b128 a[156:159],v2 offset:28672\n"
MF "v[240:243],a[208:211],a[72:75],v[240:243]\n"
"ds_read_b128 a[196:199],v3 offset:28672\n"
MF "v[248:251],a[208:211],a[76:79],v[220:223]\n"
"s_add_u32 s84,s14,9216\n"
MF "v[248:251],a[212:215],a[116:119],v[248:251]\n"
LD "v5,s[24:27],s84 offen offset:1024 lds\n"
MF "v[252:255],a[220:223],a[116:119],v[220:223]\n"
TR "a[208:209],v0 offset:8\n"
MF "v[252:255],a[216:219],a[76:79],v[252:255]\n"
TR "a[210:211],v0 offset:520\n"
MF "v[244:247],a[216:219],a[72:75],v[220:223]\n"
MF "v[244:247],a[220:223],a[112:115],v[244:247]\n"
TR "a[212:213],v0 offset:4104\n"
MF "v[236:239],a[220:223],a[108:111],v[220:223]\n"
"s_add_u32 s84,s14,18432\n"
TR "a[214:215],v0 offset:4616\n"
MF "v[236:239],a[216:219],a[68:71],v[236:239]\n"
LD "v4,s[24:27],s84 offen offset:2048 lds\n"
MF "v[228:231],a[216:219],a[64:67],v[220:223]\n"
MF "v[228:231],a[220:223],a[104:107],v[228:231]\n"
MF "v[220:223],a[220:223],a[100:103],v[220:223]\n"
".Lpg1a:\n"
"s_cbranch_execz .Lpg7a\n"
"s_add_u32 s84,s14,27648\n"
MF "v[220:223],a[216:219],a[60:63],v[220:223]\n"
"s_waitcnt lgkmcnt(4)\n"
LD "v5,s[24:27],s84 offen offset:3072 lds\n"
MF "v[16:19],a[200:203],a[140:143],v[16:19]\n"
TR "a[216:217],v1\n"
MF "v[16:19],a[204:207],a[180:183],v[16:19]\n"
TR "a[218:219],v1 offset:512\n"
MF "v[24:27],a[204:207],a[184:187],v[24:27]\n"
MF "v[24:27],a[200:203],a[144:147],v[24:27]\n"
"s_add_u32 s84,s14,40960\n"
TR "a[220:221],v1 offset:4096\n"
MF "v[32:35],a[200:203],a[148:151],v[32:35]\n"
"s_add_u32 m0,s63,45056\n"
TR "a[222:223],v1 offset:4608\n"
MF "v[32:35],a[204:207],a[188:191],v[32:35]\n"
LD "v4,s[24:27],s84 offen lds\n"
MF "v[40:43],a[204:207],a[192:195],v[40:43]\n"
MF "v[40:43],a[200:203],a[152:155],v[40:43]\n"
"s_add_u32 m0,s62,0\n"
MF "v[48:51],a[200:203],a[156:159],v[48:51]\n"
"s_add_u32 s82,s66,1572864\n"
"s_sub_u32 s15,s82,7864320\n"
"s_min_u32 s82,s82,s15\n"
MF "v[48:51],a[204:207],a[196:199],v[48:51]\n"
"s_waitcnt lgkmcnt(4)\n"
LD "v6,s[28:31],s82 offen lds\n"
MF "v[52:55],a[212:215],a[196:199],v[52:55]\n"
TR "a[200:201],v1 offset:8\n"
MF "v[52:55],a[208:211],a[156:159],v[52:55]\n"
TR "a[202:203],v1 offset:520\n"
MF "v[44:47],a[208:211],a[152:155],v[44:47]\n"
MF "v[44:47],a[212:215],a[192:195],v[44:47]\n"
TR "a[204:205],v1 offset:4104\n"
MF "v[36:39],a[212:215],a[188:191],v[36:39]\n"
TR "a[206:207],v1 offset:4616\n"
MF "v[36:39],a[208:211],a[148:151],v[36:39]\n"
LD "v11,s[28:31],s82 offen offset:1024 lds\n"
MF "v[28:31],a[208:211],a[144:147],v[28:31]\n"
MF "v[28:31],a[212:215],a[184:187],v[28:31]\n"
MF "v[20:23],a[212:215],a[180:183],v[20:23]\n"
"s_waitcnt vmcnt(11) lgkmcnt(0)\n"
LD "v12,s[28:31],s82 offen offset:2048 lds\n"
MF "v[20:23],a[208:211],a[140:143],v[20:23]\n"
"s_barrier\n"
"v_bfe_u32 v14,v9,4,4\n"
"v_lshlrev_b32 v14,2,v14\n"
"buffer_load_dword v10,v14,s[40:43],s86 offen sc1\n"
".Lhs2:\n"
MF "v[56:59],a[216:219],a[140:143],v[56:59]\n"
TR "a[208:209],v0 offset:16384\n"
MF "v[56:59],a[220:223],a[180:183],v[56:59]\n"
TR "a[210:211],v0 offset:16896\n"
MF "v[64:67],a[220:223],a[184:187],v[64:67]\n"
LD "v13,s[28:31],s82 offen offset:3072 lds\n"
MF "v[64:67],a[216:219],a[144:147],v[64:67]\n"
TR "a[212:213],v0 offset:20480\n"
MF "v[72:75],a[216:219],a[148:151],v[72:75]\n"
TR "a[214:215],v0 offset:20992\n"
MF "v[72:75],a[220:223],a[188:191],v[72:75]\n"
"s_add_u32 s14,s81,384\n"
"s_sub_u32 s15,s14,1280\n"
"s_min_u32 s14,s14,s15\n"
MF "v[80:83],a[220:223],a[192:195],v[80:83]\n"
"s_add_u32 s14,s14,s68\n"
"s_add_u32 s84,s14,0\n"
"s_add_u32 m0,s63,0\n"
MF "v[80:83],a[216:219],a[152:155],v[80:83]\n"
LD "v4,s[24:27],s84 offen lds\n"
MF "v[88:91],a[216:219],a[156:159],v[88:91]\n"
MF "v[88:91],a[220:223],a[196:199],v[88:91]\n"
MF "v[92:95],a[204:207],a[196:199],v[92:95]\n"
TR "a[216:217],v0 offset:16392\n"
MF "v[92:95],a[200:203],a[156:159],v[92:95]\n"
"s_add_u32 s84,s14,9216\n"
TR "a[218:219],v0 offset:16904\n"
MF "v[84:87],a[200:203],a[152:155],v[84:87]\n"
LD "v5,s[24:27],s84 offen offset:1024 lds\n"
MF "v[84:87],a[204:207],a[192:195],v[84:87]\n"
"s_add_u32 m0,s62,16384\n"
TR "a[220:221],v0 offset:20488\n"
MF "v[76:79],a[204:207],a[188:191],v[76:79]\n"
"s_add_u32 s82,s66,1572992\n"
TR "a[222:223],v0 offset:21000\n"
MF "v[76:79],a[200:203],a[148:151],v[76:79]\n"
"s_sub_u32 s15,s82,7864320\n"
MF "v[68:71],a[200:203],a[144:147],v[68:71]\n"
"s_min_u32 s82,s82,s15\n"
MF "v[68:71],a[204:207],a[184:187],v[68:71]\n"
LD "v6,s[28:31],s82 offen lds\n"
MF "v[60:63],a[204:207],a[180:183],v[60:63]\n"
MF "v[60:63],a[200:203],a[140:143],v[60:63]\n"
"s_waitcnt lgkmcnt(4)\n"
MF "v[96:99],a[208:211],a[140:143],v[96:99]\n"
TR "a[200:201],v1 offset:16384\n"
MF "v[96:99],a[212:215],a[180:183],v[96:99]\n"
TR "a[202:203],v1 offset:16896\n"
MF "v[104:107],a[212:215],a[184:187],v[104:107]\n"
LD "v11,s[28:31],s82 offen offset:1024 lds\n"
MF "v[104:107],a[208:211],a[144:147],v[104:107]\n"
TR "a[204:205],v1 offset:20480\n"
MF "v[112:115],a[208:211],a[148:151],v[112:115]\n"
TR "a[206:207],v1 offset:20992\n"
MF "v[112:115],a[212:215],a[188:191],v[112:115]\n"
MF "v[120:123],a[212:215],a[192:195],v[120:123]\n"
MF "v[120:123],a[208:211],a[152:155],v[120:123]\n"
LD "v12,s[28:31],s82 offen offset:2048 lds\n"
MF "v[128:131],a[208:211],a[156:159],v[128:131]\n"
MF "v[128:131],a[212:215],a[196:199],v[128:131]\n"
"s_waitcnt lgkmcnt(4)\n"
MF "v[132:135],a[220:223],a[196:199],v[132:135]\n"
TR "a[208:209],v1 offset:16392\n"
MF "v[132:135],a[216:219],a[156:159],v[132:135]\n"
TR "a[210:211],v1 offset:16904\n"
MF "v[124:127],a[216:219],a[152:155],v[124:127]\n"
LD "v13,s[28:31],s82 offen offset:3072 lds\n"
MF "v[124:127],a[220:223],a[192:195],v[124:127]\n"
TR "a[212:213],v1 offset:20488\n"
MF "v[116:119],a[220:223],a[188:191],v[116:119]\n"
TR "a[214:215],v1 offset:21000\n"
MF "v[116:119],a[216:219],a[148:151],v[116:119]\n"
"s_add_u32 s84,s14,20480\n"
MF "v[108:111],a[216:219],a[144:147],v[108:111]\n"
"s_add_u32 m0,s63,2048\n"
MF "v[108:111],a[220:223],a[184:187],v[108:111]\n"
LD "v4,s[24:27],s84 offen lds\n"
MF "v[100:103],a[220:223],a[180:183],v[100:103]\n"
"s_waitcnt vmcnt(11) lgkmcnt(0)\n"
MF "v[100:103],a[216:219],a[140:143],v[100:103]\n"
"s_barrier\n"
MF "v[136:139],a[200:203],a[140:143],v[136:139]\n"
TR "a[216:217],v0 offset:32768\n"
MF "v[136:139],a[204:207],a[180:183],v[136:139]\n"
TR "a[218:219],v0 offset:33280\n"
MF "v[144:147],a[204:207],a[184:187],v[144:147]\n"
"ds_read_b128 a[60:63],v2 offset:40960\n"
MF "v[144:147],a[200:203],a[144:147],v[144:147]\n"
TR "a[220:221],v0 offset:36864\n"
MF "v[152:155],a[200:203],a[148:151],v[152:155]\n"
"s_add_u32 s84,s14,29696\n"
TR "a[222:223],v0 offset:37376\n"
MF "v[152:155],a[204:207],a[188:191],v[152:155]\n"
LD "v5,s[24:27],s84 offen offset:1024 lds\n"
MF "v[160:163],a[204:207],a[192:195],v[160:163]\n"
"ds_read_b128 a[100:103],v3 offset:40960\n"
MF "v[160:163],a[200:203],a[152:155],v[160:163]\n"
"s_add_u32 m0,s62,32768\n"
MF "v[168:171],a[200:203],a[156:159],v[168:171]\n"
"s_add_u32 s82,s66,1573120\n"
"s_sub_u32 s15,s82,7864320\n"
"s_min_u32 s82,s82,s15\n"
MF "v[168:171],a[204:207],a[196:199],v[168:171]\n"
LD "v6,s[28:31],s82 offen lds\n"
MF "v[172:175],a[212:215],a[196:199],v[172:175]\n"
TR "a[200:201],v0 offset:32776\n"
MF "v[172:175],a[208:211],a[156:159],v[172:175]\n"
TR "a[202:203],v0 offset:33288\n"
MF "v[164:167],a[208:211],a[152:155],v[164:167]\n"
"ds_read_b128 a[64:67],v2 offset:43008\n"
MF "v[164:167],a[212:215],a[192:195],v[164:167]\n"
TR "a[204:205],v0 offset:36872\n"
MF "v[156:159],a[212:215],a[188:191],v[156:159]\n"
TR "a[206:207],v0 offset:37384\n"
MF "v[156:159],a[208:211],a[148:151],v[156:159]\n"
LD "v11,s[28:31],s82 offen offset:1024 lds\n"
MF "v[148:151],a[208:211],a[144:147],v[148:151]\n"
"ds_read_b128 a[104:107],v3 offset:43008\n"
MF "v[148:151],a[212:215],a[184:187],v[148:151]\n"
MF "v[140:143],a[212:215],a[180:183],v[140:143]\n"
MF "v[140:143],a[208:211],a[140:143],v[140:143]\n"
"s_waitcnt lgkmcnt(7)\n"
LD "v12,s[28:31],s82 offen offset:2048 lds\n"
MF "v[176:179],a[216:219],a[140:143],v[176:179]\n"
TR "a[208:209],v1 offset:32768\n"
MF "v[176:179],a[220:223],a[180:183],v[176:179]\n"
TR "a[210:211],v1 offset:33280\n"
MF "v[184:187],a[220:223],a[184:187],v[184:187]\n"
"ds_read_b128 a[68:71],v2 offset:45056\n"
MF "v[184:187],a[216:219],a[144:147],v[184:187]\n"
TR "a[212:213],v1 offset:36864\n"
MF "v[192:195],a[216:219],a[148:151],v[192:195]\n"
TR "a[214:215],v1 offset:37376\n"
MF "v[192:195],a[220:223],a[188:191],v[192:195]\n"
LD "v13,s[28:31],s82 offen offset:3072 lds\n"
MF "v[200:203],a[220:223],a[192:195],v[200:203]\n"
"ds_read_b128 a[108:111],v3 offset:45056\n"
MF "v[200:203],a[216:219],a[152:155],v[200:203]\n"
"s_add_u32 s84,s14,40960\n"
MF "v[208:211],a[216:219],a[156:159],v[208:211]\n"
"s_add_u32 m0,s63,4096\n"
MF "v[208:211],a[220:223],a[196:199],v[208:211]\n"
"s_waitcnt lgkmcnt(7)\n"
LD "v4,s[24:27],s84 offen lds\n"
MF "v[212:215],a[204:207],a[196:199],v[212:215]\n"
"s_sub_u32 s66,s66,s83\n"
TR "a[216:217],v1 offset:32776\n"
MF "v[212:215],a[200:203],a[156:159],v[212:215]\n"
"s_add_u32 m0,s62,49152\n"
TR "a[218:219],v1 offset:33288\n"
MF "v[204:207],a[200:203],a[152:155],v[204:207]\n"
"s_add_u32 s82,s66,2359296\n"
"ds_read_b128 a[72:75],v2 offset:47104\n"
MF "v[204:207],a[204:207],a[192:195],v[204:207]\n"
"s_sub_u32 s15,s82,7864320\n"
TR "a[220:221],v1 offset:36872\n"
MF "v[196:199],a[204:207],a[188:191],v[196:199]\n"
"s_min_u32 s82,s82,s15\n"
TR "a[222:223],v1 offset:37384\n"
MF "v[196:199],a[200:203],a[148:151],v[196:199]\n"
LD "v6,s[28:31],s82 offen lds\n"
MF "v[188:191],a[200:203],a[144:147],v[188:191]\n"
"v_add_u32 v0,0xffff4000,v0\n"
MF "v[188:191],a[204:207],a[184:187],v[188:191]\n"
"v_add_u32 v1,0xffff4000,v1\n"
MF "v[180:183],a[204:207],a[180:183],v[180:183]\n"
"s_waitcnt vmcnt(14) lgkmcnt(0)\n"
LD "v11,s[28:31],s82 offen offset:1024 lds\n"
MF "v[180:183],a[200:203],a[140:143],v[180:183]\n"
"s_barrier\n"
MF "v[216:219],a[208:211],a[140:143],v[216:219]\n"
TR "a[200:201],v0\n"
MF "v[216:219],a[212:215],a[180:183],v[216:219]\n"
TR "a[202:203],v0 offset:512\n"
MF "v[224:227],a[212:215],a[184:187],v[224:227]\n"
"ds_read_b128 a[112:115],v3 offset:47104\n"
MF "v[224:227],a[208:211],a[144:147],v[224:227]\n"
TR "a[204:205],v0 offset:4096\n"
MF "v[232:235],a[208:211],a[148:151],v[232:235]\n"
TR "a[206:207],v0 offset:4608\n"
MF "v[232:235],a[212:215],a[188:191],v[232:235]\n"
LD "v12,s[28:31],s82 offen offset:2048 lds\n"
MF "v[240:243],a[212:215],a[192:195],v[240:243]\n"
"ds_read_b128 a[76:79],v2 offset:49152\n"
MF "v[240:243],a[208:211],a[152:155],v[240:243]\n"
"ds_read_b128 a[116:119],v3 offset:49152\n"
MF "v[248:251],a[208:211],a[156:159],v[248:251]\n"
MF "v[248:251],a[212:215],a[196:199],v[248:251]\n"
LD "v13,s[28:31],s82 offen offset:3072 lds\n"
MF "v[252:255],a[220:223],a[196:199],v[252:255]\n"
TR "a[208:209],v0 offset:8\n"
MF "v[252:255],a[216:219],a[156:159],v[252:255]\n"
TR "a[210:211],v0 offset:520\n"
MF "v[244:247],a[216:219],a[152:155],v[244:247]\n"
"s_add_u32 s14,s81,512\n"
"s_sub_u32 s15,s14,1280\n"
MF "v[244:247],a[220:223],a[192:195],v[244:247]\n"
"s_min_u32 s14,s14,s15\n"
"s_add_u32 s14,s14,s68\n"
TR "a[212:213],v0 offset:4104\n"
MF "v[236:239],a[220:223],a[188:191],v[236:239]\n"
"s_add_u32 s84,s14,0\n"
"s_add_u32 m0,s63,20480\n"
TR "a[214:215],v0 offset:4616\n"
MF "v[236:239],a[216:219],a[148:151],v[236:239]\n"
LD "v4,s[24:27],s84 offen lds\n"
MF "v[228:231],a[216:219],a[144:147],v[228:231]\n"
MF "v[228:231],a[220:223],a[184:187],v[228:231]\n"
MF "v[220:223],a[220:223],a[180:183],v[220:223]\n"
"s_add_u32 s84,s14,9216\n"
MF "v[220:223],a[216:219],a[140:143],v[220:223]\n"
"s_waitcnt lgkmcnt(4)\n"
LD "v5,s[24:27],s84 offen offset:1024 lds\n"
MF "v[16:19],a[200:203],a[60:63],v[16:19]\n"
TR "a[216:217],v1\n"
MF "v[16:19],a[204:207],a[100:103],v[16:19]\n"
"s_add_u32 m0,s62,65536\n"
TR "a[218:219],v1 offset:512\n"
MF "v[24:27],a[204:207],a[104:107],v[24:27]\n"
"s_add_u32 s82,s66,2359424\n"
MF "v[24:27],a[200:203],a[64:67],v[24:27]\n"
"s_sub_u32 s15,s82,7864320\n"
TR "a[220:221],v1 offset:4096\n"
MF "v[32:35],a[200:203],a[68:71],v[32:35]\n"
"s_min_u32 s82,s82,s15\n"
TR "a[222:223],v1 offset:4608\n"
MF "v[32:35],a[204:207],a[108:111],v[32:35]\n"
LD "v6,s[28:31],s82 offen lds\n"
MF "v[40:43],a[204:207],a[112:115],v[40:43]\n"
MF "v[40:43],a[200:203],a[72:75],v[40:43]\n"
MF "v[48:51],a[200:203],a[76:79],v[48:51]\n"
MF "v[48:51],a[204:207],a[116:119],v[48:51]\n"
"s_waitcnt lgkmcnt(4)\n"
LD "v11,s[28:31],s82 offen offset:1024 lds\n"
MF "v[52:55],a[212:215],a[116:119],v[52:55]\n"
TR "a[200:201],v1 offset:8\n"
MF "v[52:55],a[208:211],a[76:79],v[52:55]\n"
TR "a[202:203],v1 offset:520\n"
MF "v[44:47],a[208:211],a[72:75],v[44:47]\n"
MF "v[44:47],a[212:215],a[112:115],v[44:47]\n"
TR "a[204:205],v1 offset:4104\n"
MF "v[36:39],a[212:215],a[108:111],v[36:39]\n"
TR "a[206:207],v1 offset:4616\n"
MF "v[36:39],a[208:211],a[68:71],v[36:39]\n"
LD "v12,s[28:31],s82 offen offset:2048 lds\n"
MF "v[28:31],a[208:211],a[64:67],v[28:31]\n"
MF "v[28:31],a[212:215],a[104:107],v[28:31]\n"
MF "v[20:23],a[212:215],a[100:103],v[20:23]\n"
"s_waitcnt vmcnt(16) lgkmcnt(0)\n"
LD "v13,s[28:31],s82 offen offset:3072 lds\n"
MF "v[20:23],a[208:211],a[60:63],v[20:23]\n"
"s_barrier\n"
MF "v[56:59],a[216:219],a[60:63],v[56:59]\n"
"s_add_u32 s84,s14,20480\n"
TR "a[208:209],v0 offset:16384\n"
MF "v[56:59],a[220:223],a[100:103],v[56:59]\n"
"s_add_u32 m0,s63,22528\n"
TR "a[210:211],v0 offset:16896\n"
MF "v[64:67],a[220:223],a[104:107],v[64:67]\n"
LD "v4,s[24:27],s84 offen lds\n"
MF "v[64:67],a[216:219],a[64:67],v[64:67]\n"
TR "a[212:213],v0 offset:20480\n"
MF "v[72:75],a[216:219],a[68:71],v[72:75]\n"
TR "a[214:215],v0 offset:20992\n"
MF "v[72:75],a[220:223],a[108:111],v[72:75]\n"
MF "v[80:83],a[220:223],a[112:115],v[80:83]\n"
"s_add_u32 s84,s14,29696\n"
MF "v[80:83],a[216:219],a[72:75],v[80:83]\n"
LD "v5,s[24:27],s84 offen offset:1024 lds\n"
MF "v[88:91],a[216:219],a[76:79],v[88:91]\n"
"s_add_u32 m0,s62,81920\n"
MF "v[88:91],a[220:223],a[116:119],v[88:91]\n"
"s_add_u32 s82,s66,2359552\n"
MF "v[92:95],a[204:207],a[116:119],v[92:95]\n"
"s_sub_u32 s15,s82,7864320\n"
TR "a[216:217],v0 offset:16392\n"
MF "v[92:95],a[200:203],a[76:79],v[92:95]\n"
"s_min_u32 s82,s82,s15\n"
TR "a[218:219],v0 offset:16904\n"
MF "v[84:87],a[200:203],a[72:75],v[84:87]\n"
LD "v6,s[28:31],s82 offen lds\n"
MF "v[84:87],a[204:207],a[112:115],v[84:87]\n"
TR "a[220:221],v0 offset:20488\n"
MF "v[76:79],a[204:207],a[108:111],v[76:79]\n"
TR "a[222:223],v0 offset:21000\n"
MF "v[76:79],a[200:203],a[68:71],v[76:79]\n"
MF "v[68:71],a[200:203],a[64:67],v[68:71]\n"
MF "v[68:71],a[204:207],a[104:107],v[68:71]\n"
LD "v11,s[28:31],s82 offen offset:1024 lds\n"
MF "v[60:63],a[204:207],a[100:103],v[60:63]\n"
MF "v[60:63],a[200:203],a[60:63],v[60:63]\n"
"s_waitcnt lgkmcnt(4)\n"
MF "v[96:99],a[208:211],a[60:63],v[96:99]\n"
TR "a[200:201],v1 offset:16384\n"
MF "v[96:99],a[212:215],a[100:103],v[96:99]\n"
TR "a[202:203],v1 offset:16896\n"
MF "v[104:107],a[212:215],a[104:107],v[104:107]\n"
LD "v12,s[28:31],s82 offen offset:2048 lds\n"
MF "v[104:107],a[208:211],a[64:67],v[104:107]\n"
TR "a[204:205],v1 offset:20480\n"
MF "v[112:115],a[208:211],a[68:71],v[112:115]\n"
TR "a[206:207],v1 offset:20992\n"
MF "v[112:115],a[212:215],a[108:111],v[112:115]\n"
MF "v[120:123],a[212:215],a[112:115],v[120:123]\n"
MF "v[120:123],a[208:211],a[72:75],v[120:123]\n"
LD "v13,s[28:31],s82 offen offset:3072 lds\n"
MF "v[128:131],a[208:211],a[76:79],v[128:131]\n"
MF "v[128:131],a[212:215],a[116:119],v[128:131]\n"
"s_waitcnt lgkmcnt(4)\n"
MF "v[132:135],a[220:223],a[116:119],v[132:135]\n"
"s_add_u32 s84,s14,40960\n"
TR "a[208:209],v1 offset:16392\n"
MF "v[132:135],a[216:219],a[76:79],v[132:135]\n"
"s_add_u32 m0,s63,24576\n"
TR "a[210:211],v1 offset:16904\n"
MF "v[124:127],a[216:219],a[72:75],v[124:127]\n"
LD "v4,s[24:27],s84 offen lds\n"
MF "v[124:127],a[220:223],a[112:115],v[124:127]\n"
"s_add_u32 m0,s62,0\n"
TR "a[212:213],v1 offset:20488\n"
MF "v[116:119],a[220:223],a[108:111],v[116:119]\n"
"s_add_u32 s82,s66,3145728\n"
TR "a[214:215],v1 offset:21000\n"
MF "v[116:119],a[216:219],a[68:71],v[116:119]\n"
"s_sub_u32 s15,s82,7864320\n"
MF "v[108:111],a[216:219],a[64:67],v[108:111]\n"
"s_min_u32 s82,s82,s15\n"
MF "v[108:111],a[220:223],a[104:107],v[108:111]\n"
LD "v6,s[28:31],s82 offen lds\n"
MF "v[100:103],a[220:223],a[100:103],v[100:103]\n"
"s_waitcnt vmcnt(18) lgkmcnt(0)\n"
MF "v[100:103],a[216:219],a[60:63],v[100:103]\n"
"s_barrier\n"
MF "v[136:139],a[200:203],a[60:63],v[136:139]\n"
TR "a[216:217],v0 offset:32768\n"
MF "v[136:139],a[204:207],a[100:103],v[136:139]\n"
TR "a[218:219],v0 offset:33280\n"
MF "v[144:147],a[204:207],a[104:107],v[144:147]\n"
"ds_read_b128 a[140:143],v2\n"
MF "v[144:147],a[200:203],a[64:67],v[144:147]\n"
TR "a[220:221],v0 offset:36864\n"
MF "v[152:155],a[200:203],a[68:71],v[152:155]\n"
TR "a[222:223],v0 offset:37376\n"
MF "v[152:155],a[204:207],a[108:111],v[152:155]\n"
LD "v11,s[28:31],s82 offen offset:1024 lds\n"
MF "v[160:163],a[204:207],a[112:115],v[160:163]\n"
"ds_read_b128 a[180:183],v3\n"
MF "v[160:163],a[200:203],a[72:75],v[160:163]\n"
MF "v[168:171],a[200:203],a[76:79],v[168:171]\n"
MF "v[168:171],a[204:207],a[116:119],v[168:171]\n"
LD "v12,s[28:31],s82 offen offset:2048 lds\n"
MF "v[172:175],a[212:215],a[116:119],v[172:175]\n"
TR "a[200:201],v0 offset:32776\n"
MF "v[172:175],a[208:211],a[76:79],v[172:175]\n"
TR "a[202:203],v0 offset:33288\n"
MF "v[164:167],a[208:211],a[72:75],v[164:167]\n"
"ds_read_b128 a[144:147],v2 offset:2048\n"
MF "v[164:167],a[212:215],a[112:115],v[164:167]\n"
TR "a[204:205],v0 offset:36872\n"
MF "v[156:159],a[212:215],a[108:111],v[156:159]\n"
TR "a[206:207],v0 offset:37384\n"
MF "v[156:159],a[208:211],a[68:71],v[156:159]\n"
LD "v13,s[28:31],s82 offen offset:3072 lds\n"
MF "v[148:151],a[208:211],a[64:67],v[148:151]\n"
"ds_read_b128 a[184:187],v3 offset:2048\n"
MF "v[148:151],a[212:215],a[104:107],v[148:151]\n"
"s_add_u32 s14,s81,640\n"
"s_sub_u32 s15,s14,1280\n"
"s_min_u32 s14,s14,s15\n"
MF "v[140:143],a[212:215],a[100:103],v[140:143]\n"
"s_add_u32 s14,s14,s68\n"
"s_add_u32 s84,s14,0\n"
"s_add_u32 m0,s63,40960\n"
MF "v[140:143],a[208:211],a[60:63],v[140:143]\n"
"s_waitcnt lgkmcnt(7)\n"
LD "v4,s[24:27],s84 offen lds\n"
MF "v[176:179],a[216:219],a[60:63],v[176:179]\n"
TR "a[208:209],v1 offset:32768\n"
MF "v[176:179],a[220:223],a[100:103],v[176:179]\n"
TR "a[210:211],v1 offset:33280\n"
MF "v[184:187],a[220:223],a[104:107],v[184:187]\n"
"ds_read_b128 a[148:151],v2 offset:4096\n"
MF "v[184:187],a[216:219],a[64:67],v[184:187]\n"
TR "a[212:213],v1 offset:36864\n"
MF "v[192:195],a[216:219],a[68:71],v[192:195]\n"
"s_add_u32 s84,s14,9216\n"
TR "a[214:215],v1 offset:37376\n"
MF "v[192:195],a[220:223],a[108:111],v[192:195]\n"
LD "v5,s[24:27],s84 offen offset:1024 lds\n"
MF "v[200:203],a[220:223],a[112:115],v[200:203]\n"
"ds_read_b128 a[188:191],v3 offset:4096\n"
MF "v[200:203],a[216:219],a[72:75],v[200:203]\n"
"s_add_u32 m0,s62,16384\n"
MF "v[208:211],a[216:219],a[76:79],v[208:211]\n"
"s_add_u32 s82,s66,3145856\n"
"s_sub_u32 s15,s82,7864320\n"
"s_min_u32 s82,s82,s15\n"
MF "v[208:211],a[220:223],a[116:119],v[208:211]\n"
"s_waitcnt lgkmcnt(7)\n"
LD "v6,s[28:31],s82 offen lds\n"
MF "v[212:215],a[204:207],a[116:119],v[212:215]\n"
TR "a[216:217],v1 offset:32776\n"
MF "v[212:215],a[200:203],a[76:79],v[212:215]\n"
TR "a[218:219],v1 offset:33288\n"
MF "v[204:207],a[200:203],a[72:75],v[204:207]\n"
"ds_read_b128 a[152:155],v2 offset:6144\n"
MF "v[204:207],a[204:207],a[112:115],v[204:207]\n"
TR "a[220:221],v1 offset:36872\n"
MF "v[196:199],a[204:207],a[108:111],v[196:199]\n"
TR "a[222:223],v1 offset:37384\n"
MF "v[196:199],a[200:203],a[68:71],v[196:199]\n"
LD "v11,s[28:31],s82 offen offset:1024 lds\n"
MF "v[188:191],a[200:203],a[64:67],v[188:191]\n"
"v_add_u32 v0,0xc000,v0\n"
MF "v[188:191],a[204:207],a[104:107],v[188:191]\n"
"v_add_u32 v1,0xc000,v1\n"
MF "v[180:183],a[204:207],a[100:103],v[180:183]\n"
"s_waitcnt vmcnt(21) lgkmcnt(0)\n"
LD "v12,s[28:31],s82 offen offset:2048 lds\n"
MF "v[180:183],a[200:203],a[60:63],v[180:183]\n"
"s_barrier\n"
MF "v[216:219],a[208:211],a[60:63],v[216:219]\n"
TR "a[200:201],v0\n"
MF "v[216:219],a[212:215],a[100:103],v[216:219]\n"
TR "a[202:203],v0 offset:512\n"
MF "v[224:227],a[212:215],a[104:107],v[224:227]\n"
"ds_read_b128 a[192:195],v3 offset:6144\n"
MF "v[224:227],a[208:211],a[64:67],v[224:227]\n"
TR "a[204:205],v0 offset:4096\n"
MF "v[232:235],a[208:211],a[68:71],v[232:235]\n"
TR "a[206:207],v0 offset:4608\n"
MF "v[232:235],a[212:215],a[108:111],v[232:235]\n"
LD "v13,s[28:31],s82 offen offset:3072 lds\n"
MF "v[240:243],a[212:215],a[112:115],v[240:243]\n"
"ds_read_b128 a[156:159],v2 offset:8192\n"
MF "v[240:243],a[208:211],a[72:75],v[240:243]\n"
"s_add_u32 s84,s14,20480\n"
"ds_read_b128 a[196:199],v3 offset:8192\n"
MF "v[248:251],a[208:211],a[76:79],v[248:251]\n"
"s_add_u32 m0,s63,43008\n"
MF "v[248:251],a[212:215],a[116:119],v[248:251]\n"
LD "v4,s[24:27],s84 offen lds\n"
MF "v[252:255],a[220:223],a[116:119],v[252:255]\n"
TR "a[208:209],v0 offset:8\n"
MF "v[252:255],a[216:219],a[76:79],v[252:255]\n"
TR "a[210:211],v0 offset:520\n"
MF "v[244:247],a[216:219],a[72:75],v[244:247]\n"
MF "v[244:247],a[220:223],a[112:115],v[244:247]\n"
TR "a[212:213],v0 offset:4104\n"
MF "v[236:239],a[220:223],a[108:111],v[236:239]\n"
"s_add_u32 s84,s14,29696\n"
TR "a[214:215],v0 offset:4616\n"
MF "v[236:239],a[216:219],a[68:71],v[236:239]\n"
LD "v5,s[24:27],s84 offen offset:1024 lds\n"
MF "v[228:231],a[216:219],a[64:67],v[228:231]\n"
MF "v[228:231],a[220:223],a[104:107],v[228:231]\n"
"s_add_u32 m0,s62,32768\n"
MF "v[220:223],a[220:223],a[100:103],v[220:223]\n"
"s_add_u32 s82,s66,3145984\n"
"s_sub_u32 s15,s82,7864320\n"
"s_min_u32 s82,s82,s15\n"
MF "v[220:223],a[216:219],a[60:63],v[220:223]\n"
"s_waitcnt lgkmcnt(4)\n"
LD "v6,s[28:31],s82 offen lds\n"
MF "v[16:19],a[200:203],a[140:143],v[16:19]\n"
TR "a[216:217],v1\n"
MF "v[16:19],a[204:207],a[180:183],v[16:19]\n"
TR "a[218:219],v1 offset:512\n"
MF "v[24:27],a[204:207],a[184:187],v[24:27]\n"
MF "v[24:27],a[200:203],a[144:147],v[24:27]\n"
TR "a[220:221],v1 offset:4096\n"
MF "v[32:35],a[200:203],a[148:151],v[32:35]\n"
TR "a[222:223],v1 offset:4608\n"
MF "v[32:35],a[204:207],a[188:191],v[32:35]\n"
LD "v11,s[28:31],s82 offen offset:1024 lds\n"
MF "v[40:43],a[204:207],a[192:195],v[40:43]\n"
MF "v[40:43],a[200:203],a[152:155],v[40:43]\n"
MF "v[48:51],a[200:203],a[156:159],v[48:51]\n"
MF "v[48:51],a[204:207],a[196:199],v[48:51]\n"
"s_waitcnt lgkmcnt(4)\n"
LD "v12,s[28:31],s82 offen offset:2048 lds\n"
MF "v[52:55],a[212:215],a[196:199],v[52:55]\n"
TR "a[200:201],v1 offset:8\n"
MF "v[52:55],a[208:211],a[156:159],v[52:55]\n"
TR "a[202:203],v1 offset:520\n"
MF "v[44:47],a[208:211],a[152:155],v[44:47]\n"
MF "v[44:47],a[212:215],a[192:195],v[44:47]\n"
TR "a[204:205],v1 offset:4104\n"
MF "v[36:39],a[212:215],a[188:191],v[36:39]\n"
".Lpg2a:\n"
"s_cbranch_execz .Lisr1\n"
TR "a[206:207],v1 offset:4616\n"
MF "v[36:39],a[208:211],a[148:151],v[36:39]\n"
LD "v13,s[28:31],s82 offen offset:3072 lds\n"
MF "v[28:31],a[208:211],a[144:147],v[28:31]\n"
"s_add_u32 s84,s14,40960\n"
MF "v[28:31],a[212:215],a[184:187],v[28:31]\n"
"s_add_u32 m0,s63,45056\n"
MF "v[20:23],a[212:215],a[180:183],v[20:23]\n"
"s_waitcnt vmcnt(23) lgkmcnt(0)\n"
LD "v4,s[24:27],s84 offen lds\n"
MF "v[20:23],a[208:211],a[140:143],v[20:23]\n"
"s_barrier\n"
AW "a253,v10\n"
"v_add_u32 v10,1,v10\n"
"s_bfe_u32 s58,s22,0x30014\n"
"s_lshl_b32 s58,s58,2\n"
"s_add_u32 s58,s58,s56\n"
"s_lshl_b32 s58,s58,6\n"
"s_add_u32 s58,s58,s86\n"
"v_readlane_b32 s76,v10,s78\n"
"buffer_store_dword v10,v14,s[40:43],s58 offen offset:128 sc1\n"
".Lhs5:\n"
MF "v[56:59],a[216:219],a[140:143],v[56:59]\n"
"s_add_u32 m0,s62,49152\n"
"s_add_u32 s82,s66,3932160\n"
TR "a[208:209],v0 offset:16384\n"
MF "v[56:59],a[220:223],a[180:183],v[56:59]\n"
"s_sub_u32 s15,s82,7864320\n"
"s_min_u32 s82,s82,s15\n"
TR "a[210:211],v0 offset:16896\n"
MF "v[64:67],a[220:223],a[184:187],v[64:67]\n"
LD "v6,s[28:31],s82 offen lds\n"
MF "v[64:67],a[216:219],a[144:147],v[64:67]\n"
TR "a[212:213],v0 offset:20480\n"
MF "v[72:75],a[216:219],a[148:151],v[72:75]\n"
TR "a[214:215],v0 offset:20992\n"
MF "v[72:75],a[220:223],a[188:191],v[72:75]\n"
"buffer_load_dword a249,v15,s[48:51],s60 offen offset:1024\n"
MF "v[80:83],a[220:223],a[192:195],v[80:83]\n"
"s_bfe_u32 s59,s23,0x30003\n"
MF "v[80:83],a[216:219],a[152:155],v[80:83]\n"
LD "v11,s[28:31],s82 offen offset:1024 lds\n"
MF "v[88:91],a[216:219],a[156:159],v[88:91]\n"
"s_add_u32 s59,s59,1\n"
MF "v[88:91],a[220:223],a[196:199],v[88:91]\n"
"s_lshl_b32 s59,s59,7\n"
MF "v[92:95],a[204:207],a[196:199],v[92:95]\n"
"s_and_b32 s59,s59,768\n"
TR "a[216:217],v0 offset:16392\n"
MF "v[92:95],a[200:203],a[156:159],v[92:95]\n"
"s_add_u32 s59,s59,s60\n"
TR "a[218:219],v0 offset:16904\n"
MF "v[84:87],a[200:203],a[152:155],v[84:87]\n"
LD "v12,s[28:31],s82 offen offset:2048 lds\n"
MF "v[84:87],a[204:207],a[192:195],v[84:87]\n"
TR "a[220:221],v0 offset:20488\n"
MF "v[76:79],a[204:207],a[188:191],v[76:79]\n"
TR "a[222:223],v0 offset:21000\n"
MF "v[76:79],a[200:203],a[148:151],v[76:79]\n"
MF "v[68:71],a[200:203],a[144:147],v[68:71]\n"
MF "v[68:71],a[204:207],a[184:187],v[68:71]\n"
LD "v13,s[28:31],s82 offen offset:3072 lds\n"
MF "v[60:63],a[204:207],a[180:183],v[60:63]\n"
MF "v[60:63],a[200:203],a[140:143],v[60:63]\n"
"s_add_u32 s14,s81,768\n"
"s_sub_u32 s15,s14,1280\n"
"s_waitcnt lgkmcnt(4)\n"
MF "v[96:99],a[208:211],a[140:143],v[96:99]\n"
"s_min_u32 s14,s14,s15\n"
"s_add_u32 s14,s14,s68\n"
TR "a[200:201],v1 offset:16384\n"
MF "v[96:99],a[212:215],a[180:183],v[96:99]\n"
"s_add_u32 s84,s14,0\n"
"s_add_u32 m0,s63,0\n"
TR "a[202:203],v1 offset:16896\n"
MF "v[104:107],a[212:215],a[184:187],v[104:107]\n"
LD "v4,s[24:27],s84 offen lds\n"
MF "v[104:107],a[208:211],a[144:147],v[104:107]\n"
TR "a[204:205],v1 offset:20480\n"
MF "v[112:115],a[208:211],a[148:151],v[112:115]\n"
TR "a[206:207],v1 offset:20992\n"
MF "v[112:115],a[212:215],a[188:191],v[112:115]\n"
MF "v[120:123],a[212:215],a[192:195],v[120:123]\n"
"s_add_u32 s84,s14,9216\n"
MF "v[120:123],a[208:211],a[152:155],v[120:123]\n"
LD "v5,s[24:27],s84 offen offset:1024 lds\n"
MF "v[128:131],a[208:211],a[156:159],v[128:131]\n"
MF "v[128:131],a[212:215],a[196:199],v[128:131]\n"
"s_waitcnt lgkmcnt(4)\n"
MF "v[132:135],a[220:223],a[196:199],v[132:135]\n"
TR "a[208:209],v1 offset:16392\n"
MF "v[132:135],a[216:219],a[156:159],v[132:135]\n"
TR "a[210:211],v1 offset:16904\n"
MF "v[124:127],a[216:219],a[152:155],v[124:127]\n"
MF "v[124:127],a[220:223],a[192:195],v[124:127]\n"
TR "a[212:213],v1 offset:20488\n"
MF "v[116:119],a[220:223],a[188:191],v[116:119]\n"
TR "a[214:215],v1 offset:21000\n"
MF "v[116:119],a[216:219],a[148:151],v[116:119]\n"
MF "v[108:111],a[216:219],a[144:147],v[108:111]\n"
MF "v[108:111],a[220:223],a[184:187],v[108:111]\n"
MF "v[100:103],a[220:223],a[180:183],v[100:103]\n"
"s_waitcnt vmcnt(24) lgkmcnt(0)\n"
MF "v[100:103],a[216:219],a[140:143],v[100:103]\n"
"s_barrier\n"
MF "v[136:139],a[200:203],a[140:143],v[136:139]\n"
TR "a[216:217],v0 offset:32768\n"
MF "v[136:139],a[204:207],a[180:183],v[136:139]\n"
"s_add_u32 m0,s62,65536\n"
TR "a[218:219],v0 offset:33280\n"
MF "v[144:147],a[204:207],a[184:187],v[144:147]\n"
"s_add_u32 s82,s66,3932288\n"
"ds_read_b128 a[60:63],v2 offset:20480\n"
MF "v[144:147],a[200:203],a[144:147],v[144:147]\n"
"s_sub_u32 s15,s82,7864320\n"
TR "a[220:221],v0 offset:36864\n"
MF "v[152:155],a[200:203],a[148:151],v[152:155]\n"
"s_min_u32 s82,s82,s15\n"
TR "a[222:223],v0 offset:37376\n"
MF "v[152:155],a[204:207],a[188:191],v[152:155]\n"
LD "v6,s[28:31],s82 offen lds\n"
MF "v[160:163],a[204:207],a[192:195],v[160:163]\n"
"ds_read_b128 a[100:103],v3 offset:20480\n"
MF "v[160:163],a[200:203],a[152:155],v[160:163]\n"
MF "v[168:171],a[200:203],a[156:159],v[168:171]\n"
MF "v[168:171],a[204:207],a[196:199],v[168:171]\n"
LD "v11,s[28:31],s82 offen offset:1024 lds\n"
MF "v[172:175],a[212:215],a[196:199],v[172:175]\n"
TR "a[200:201],v0 offset:32776\n"
MF "v[172:175],a[208:211],a[156:159],v[172:175]\n"
TR "a[202:203],v0 offset:33288\n"
MF "v[164:167],a[208:211],a[152:155],v[164:167]\n"
"ds_read_b128 a[64:67],v2 offset:22528\n"
MF "v[164:167],a[212:215],a[192:195],v[164:167]\n"
TR "a[204:205],v0 offset:36872\n"
MF "v[156:159],a[212:215],a[188:191],v[156:159]\n"
TR "a[206:207],v0 offset:37384\n"
MF "v[156:159],a[208:211],a[148:151],v[156:159]\n"
LD "v12,s[28:31],s82 offen offset:2048 lds\n"
MF "v[148:151],a[208:211],a[144:147],v[148:151]\n"
"ds_read_b128 a[104:107],v3 offset:22528\n"
MF "v[148:151],a[212:215],a[184:187],v[148:151]\n"
MF "v[140:143],a[212:215],a[180:183],v[140:143]\n"
MF "v[140:143],a[208:211],a[140:143],v[140:143]\n"
"s_waitcnt lgkmcnt(7)\n"
LD "v13,s[28:31],s82 offen offset:3072 lds\n"
MF "v[176:179],a[216:219],a[140:143],v[176:179]\n"
TR "a[208:209],v1 offset:32768\n"
MF "v[176:179],a[220:223],a[180:183],v[176:179]\n"
TR "a[210:211],v1 offset:33280\n"
MF "v[184:187],a[220:223],a[184:187],v[184:187]\n"
"ds_read_b128 a[68:71],v2 offset:24576\n"
MF "v[184:187],a[216:219],a[144:147],v[184:187]\n"
"s_add_u32 s84,s14,20480\n"
TR "a[212:213],v1 offset:36864\n"
MF "v[192:195],a[216:219],a[148:151],v[192:195]\n"
"s_add_u32 m0,s63,2048\n"
TR "a[214:215],v1 offset:37376\n"
MF "v[192:195],a[220:223],a[188:191],v[192:195]\n"
LD "v4,s[24:27],s84 offen lds\n"
MF "v[200:203],a[220:223],a[192:195],v[200:203]\n"
"ds_read_b128 a[108:111],v3 offset:24576\n"
MF "v[200:203],a[216:219],a[152:155],v[200:203]\n"
MF "v[208:211],a[216:219],a[156:159],v[208:211]\n"
"s_add_u32 s84,s14,29696\n"
MF "v[208:211],a[220:223],a[196:199],v[208:211]\n"
"s_waitcnt lgkmcnt(7)\n"
LD "v5,s[24:27],s84 offen offset:1024 lds\n"
MF "v[212:215],a[204:207],a[196:199],v[212:215]\n"
TR "a[216:217],v1 offset:32776\n"
MF "v[212:215],a[200:203],a[156:159],v[212:215]\n"
TR "a[218:219],v1 offset:33288\n"
MF "v[204:207],a[200:203],a[152:155],v[204:207]\n"
"ds_read_b128 a[72:75],v2 offset:26624\n"
MF "v[204:207],a[204:207],a[192:195],v[204:207]\n"
TR "a[220:221],v1 offset:36872\n"
MF "v[196:199],a[204:207],a[188:191],v[196:199]\n"
TR "a[222:223],v1 offset:37384\n"
MF "v[196:199],a[200:203],a[148:151],v[196:199]\n"
MF "v[188:191],a[200:203],a[144:147],v[188:191]\n"
"v_add_u32 v0,0xffff4000,v0\n"
MF "v[188:191],a[204:207],a[184:187],v[188:191]\n"
"v_add_u32 v1,0xffff4000,v1\n"
MF "v[180:183],a[204:207],a[180:183],v[180:183]\n"
"s_waitcnt vmcnt(26) lgkmcnt(0)\n"
MF "v[180:183],a[200:203],a[140:143],v[180:183]\n"
"s_barrier\n"
MF "v[216:219],a[208:211],a[140:143],v[216:219]\n"
TR "a[200:201],v0\n"
MF "v[216:219],a[212:215],a[180:183],v[216:219]\n"
"s_add_u32 m0,s62,81920\n"
TR "a[202:203],v0 offset:512\n"
MF "v[224:227],a[212:215],a[184:187],v[224:227]\n"
"s_add_u32 s82,s66,3932416\n"
"ds_read_b128 a[112:115],v3 offset:26624\n"
MF "v[224:227],a[208:211],a[144:147],v[224:227]\n"
"s_sub_u32 s15,s82,7864320\n"
TR "a[204:205],v0 offset:4096\n"
MF "v[232:235],a[208:211],a[148:151],v[232:235]\n"
"s_min_u32 s82,s82,s15\n"
TR "a[206:207],v0 offset:4608\n"
MF "v[232:235],a[212:215],a[188:191],v[232:235]\n"
LD "v6,s[28:31],s82 offen lds\n"
MF "v[240:243],a[212:215],a[192:195],v[240:243]\n"
"ds_read_b128 a[76:79],v2 offset:28672\n"
MF "v[240:243],a[208:211],a[152:155],v[240:243]\n"
"ds_read_b128 a[116:119],v3 offset:28672\n"
MF "v[248:251],a[208:211],a[156:159],v[248:251]\n"
MF "v[248:251],a[212:215],a[196:199],v[248:251]\n"
LD "v11,s[28:31],s82 offen offset:1024 lds\n"
MF "v[252:255],a[220:223],a[196:199],v[252:255]\n"
TR "a[208:209],v0 offset:8\n"
MF "v[252:255],a[216:219],a[156:159],v[252:255]\n"
TR "a[210:211],v0 offset:520\n"
MF "v[244:247],a[216:219],a[152:155],v[244:247]\n"
MF "v[244:247],a[220:223],a[192:195],v[244:247]\n"
TR "a[212:213],v0 offset:4104\n"
MF "v[236:239],a[220:223],a[188:191],v[236:239]\n"
TR "a[214:215],v0 offset:4616\n"
MF "v[236:239],a[216:219],a[148:151],v[236:239]\n"
LD "v12,s[28:31],s82 offen offset:2048 lds\n"
MF "v[228:231],a[216:219],a[144:147],v[228:231]\n"
MF "v[228:231],a[220:223],a[184:187],v[228:231]\n"
MF "v[220:223],a[220:223],a[180:183],v[220:223]\n"
MF "v[220:223],a[216:219],a[140:143],v[220:223]\n"
"s_waitcnt lgkmcnt(4)\n"
LD "v13,s[28:31],s82 offen offset:3072 lds\n"
MF "v[16:19],a[200:203],a[60:63],v[16:19]\n"
TR "a[216:217],v1\n"
MF "v[16:19],a[204:207],a[100:103],v[16:19]\n"
TR "a[218:219],v1 offset:512\n"
MF "v[24:27],a[204:207],a[104:107],v[24:27]\n"
MF "v[24:27],a[200:203],a[64:67],v[24:27]\n"
"s_add_u32 s84,s14,40960\n"
TR "a[220:221],v1 offset:4096\n"
MF "v[32:35],a[200:203],a[68:71],v[32:35]\n"
"s_add_u32 m0,s63,4096\n"
TR "a[222:223],v1 offset:4608\n"
MF "v[32:35],a[204:207],a[108:111],v[32:35]\n"
LD "v4,s[24:27],s84 offen lds\n"
MF "v[40:43],a[204:207],a[112:115],v[40:43]\n"
MF "v[40:43],a[200:203],a[72:75],v[40:43]\n"
MF "v[48:51],a[200:203],a[76:79],v[48:51]\n"
MF "v[48:51],a[204:207],a[116:119],v[48:51]\n"
"s_waitcnt lgkmcnt(4)\n"
MF "v[52:55],a[212:215],a[116:119],v[52:55]\n"
TR "a[200:201],v1 offset:8\n"
MF "v[52:55],a[208:211],a[76:79],v[52:55]\n"
TR "a[202:203],v1 offset:520\n"
MF "v[44:47],a[208:211],a[72:75],v[44:47]\n"
MF "v[44:47],a[212:215],a[112:115],v[44:47]\n"
TR "a[204:205],v1 offset:4104\n"
MF "v[36:39],a[212:215],a[108:111],v[36:39]\n"
TR "a[206:207],v1 offset:4616\n"
MF "v[36:39],a[208:211],a[68:71],v[36:39]\n"
MF "v[28:31],a[208:211],a[64:67],v[28:31]\n"
MF "v[28:31],a[212:215],a[104:107],v[28:31]\n"
MF "v[20:23],a[212:215],a[100:103],v[20:23]\n"
"s_waitcnt vmcnt(25) lgkmcnt(0)\n"
MF "v[20:23],a[208:211],a[60:63],v[20:23]\n"
"s_barrier\n"
MF "v[56:59],a[216:219],a[60:63],v[56:59]\n"
"s_add_u32 m0,s62,0\n"
"s_add_u32 s82,s66,4718592\n"
TR "a[208:209],v0 offset:16384\n"
MF "v[56:59],a[220:223],a[100:103],v[56:59]\n"
"s_sub_u32 s15,s82,7864320\n"
"s_min_u32 s82,s82,s15\n"
TR "a[210:211],v0 offset:16896\n"
MF "v[64:67],a[220:223],a[104:107],v[64:67]\n"
LD "v6,s[28:31],s82 offen lds\n"
MF "v[64:67],a[216:219],a[64:67],v[64:67]\n"
TR "a[212:213],v0 offset:20480\n"
MF "v[72:75],a[216:219],a[68:71],v[72:75]\n"
TR "a[214:215],v0 offset:20992\n"
MF "v[72:75],a[220:223],a[108:111],v[72:75]\n"
"buffer_load_dword a249,v15,s[48:51],s59 offen\n"
MF "v[80:83],a[220:223],a[112:115],v[80:83]\n"
"s_bfe_u32 s59,s23,0x30003\n"
MF "v[80:83],a[216:219],a[72:75],v[80:83]\n"
LD "v11,s[28:31],s82 offen offset:1024 lds\n"
MF "v[88:91],a[216:219],a[76:79],v[88:91]\n"
"s_add_u32 s59,s59,3\n"
MF "v[88:91],a[220:223],a[116:119],v[88:91]\n"
"s_lshl_b32 s59,s59,7\n"
MF "v[92:95],a[204:207],a[116:119],v[92:95]\n"
"s_and_b32 s59,s59,768\n"
TR "a[216:217],v0 offset:16392\n"
MF "v[92:95],a[200:203],a[76:79],v[92:95]\n"
"s_add_u32 s59,s59,s60\n"
TR "a[218:219],v0 offset:16904\n"
MF "v[84:87],a[200:203],a[72:75],v[84:87]\n"
LD "v12,s[28:31],s82 offen offset:2048 lds\n"
MF "v[84:87],a[204:207],a[112:115],v[84:87]\n"
TR "a[220:221],v0 offset:20488\n"
MF "v[76:79],a[204:207],a[108:111],v[76:79]\n"
TR "a[222:223],v0 offset:21000\n"
MF "v[76:79],a[200:203],a[68:71],v[76:79]\n"
MF "v[68:71],a[200:203],a[64:67],v[68:71]\n"
MF "v[68:71],a[204:207],a[104:107],v[68:71]\n"
LD "v13,s[28:31],s82 offen offset:3072 lds\n"
MF "v[60:63],a[204:207],a[100:103],v[60:63]\n"
MF "v[60:63],a[200:203],a[60:63],v[60:63]\n"
"s_add_u32 s14,s81,896\n"
"s_sub_u32 s15,s14,1280\n"
"s_waitcnt lgkmcnt(4)\n"
MF "v[96:99],a[208:211],a[60:63],v[96:99]\n"
"s_min_u32 s14,s14,s15\n"
"s_add_u32 s14,s14,s68\n"
TR "a[200:201],v1 offset:16384\n"
MF "v[96:99],a[212:215],a[100:103],v[96:99]\n"
"s_add_u32 s84,s14,0\n"
"s_add_u32 m0,s63,20480\n"
TR "a[202:203],v1 offset:16896\n"
MF "v[104:107],a[212:215],a[104:107],v[104:107]\n"
LD "v4,s[24:27],s84 offen lds\n"
MF "v[104:107],a[208:211],a[64:67],v[104:107]\n"
TR "a[204:205],v1 offset:20480\n"
MF "v[112:115],a[208:211],a[68:71],v[112:115]\n"
TR "a[206:207],v1 offset:20992\n"
MF "v[112:115],a[212:215],a[108:111],v[112:115]\n"
MF "v[120:123],a[212:215],a[112:115],v[120:123]\n"
"s_add_u32 s84,s14,9216\n"
MF "v[120:123],a[208:211],a[72:75],v[120:123]\n"
LD "v5,s[24:27],s84 offen offset:1024 lds\n"
MF "v[128:131],a[208:211],a[76:79],v[128:131]\n"
MF "v[128:131],a[212:215],a[116:119],v[128:131]\n"
"s_waitcnt lgkmcnt(4)\n"
MF "v[132:135],a[220:223],a[116:119],v[132:135]\n"
TR "a[208:209],v1 offset:16392\n"
MF "v[132:135],a[216:219],a[76:79],v[132:135]\n"
TR "a[210:211],v1 offset:16904\n"
MF "v[124:127],a[216:219],a[72:75],v[124:127]\n"
MF "v[124:127],a[220:223],a[112:115],v[124:127]\n"
TR "a[212:213],v1 offset:20488\n"
MF "v[116:119],a[220:223],a[108:111],v[116:119]\n"
TR "a[214:215],v1 offset:21000\n"
MF "v[116:119],a[216:219],a[68:71],v[116:119]\n"
MF "v[108:111],a[216:219],a[64:67],v[108:111]\n"
MF "v[108:111],a[220:223],a[104:107],v[108:111]\n"
MF "v[100:103],a[220:223],a[100:103],v[100:103]\n"
"s_waitcnt vmcnt(25) lgkmcnt(0)\n"
MF "v[100:103],a[216:219],a[60:63],v[100:103]\n"
"s_barrier\n"
MF "v[136:139],a[200:203],a[60:63],v[136:139]\n"
TR "a[216:217],v0 offset:32768\n"
MF "v[136:139],a[204:207],a[100:103],v[136:139]\n"
"s_add_u32 m0,s62,16384\n"
TR "a[218:219],v0 offset:33280\n"
MF "v[144:147],a[204:207],a[104:107],v[144:147]\n"
"s_add_u32 s82,s66,4718720\n"
"ds_read_b128 a[140:143],v2 offset:40960\n"
MF "v[144:147],a[200:203],a[64:67],v[144:147]\n"
"s_sub_u32 s15,s82,7864320\n"
TR "a[220:221],v0 offset:36864\n"
MF "v[152:155],a[200:203],a[68:71],v[152:155]\n"
"s_min_u32 s82,s82,s15\n"
TR "a[222:223],v0 offset:37376\n"
MF "v[152:155],a[204:207],a[108:111],v[152:155]\n"
LD "v6,s[28:31],s82 offen lds\n"
MF "v[160:163],a[204:207],a[112:115],v[160:163]\n"
"ds_read_b128 a[180:183],v3 offset:40960\n"
MF "v[160:163],a[200:203],a[72:75],v[160:163]\n"
MF "v[168:171],a[200:203],a[76:79],v[168:171]\n"
MF "v[168:171],a[204:207],a[116:119],v[168:171]\n"
LD "v11,s[28:31],s82 offen offset:1024 lds\n"
MF "v[172:175],a[212:215],a[116:119],v[172:175]\n"
TR "a[200:201],v0 offset:32776\n"
MF "v[172:175],a[208:211],a[76:79],v[172:175]\n"
TR "a[202:203],v0 offset:33288\n"
MF "v[164:167],a[208:211],a[72:75],v[164:167]\n"
"ds_read_b128 a[144:147],v2 offset:43008\n"
MF "v[164:167],a[212:215],a[112:115],v[164:167]\n"
TR "a[204:205],v0 offset:36872\n"
MF "v[156:159],a[212:215],a[108:111],v[156:159]\n"
TR "a[206:207],v0 offset:37384\n"
MF "v[156:159],a[208:211],a[68:71],v[156:159]\n"
LD "v12,s[28:31],s82 offen offset:2048 lds\n"
MF "v[148:151],a[208:211],a[64:67],v[148:151]\n"
"ds_read_b128 a[184:187],v3 offset:43008\n"
MF "v[148:151],a[212:215],a[104:107],v[148:151]\n"
MF "v[140:143],a[212:215],a[100:103],v[140:143]\n"
MF "v[140:143],a[208:211],a[60:63],v[140:143]\n"
"s_waitcnt lgkmcnt(7)\n"
LD "v13,s[28:31],s82 offen offset:3072 lds\n"
MF "v[176:179],a[216:219],a[60:63],v[176:179]\n"
TR "a[208:209],v1 offset:32768\n"
MF "v[176:179],a[220:223],a[100:103],v[176:179]\n"
TR "a[210:211],v1 offset:33280\n"
MF "v[184:187],a[220:223],a[104:107],v[184:187]\n"
"ds_read_b128 a[148:151],v2 offset:45056\n"
MF "v[184:187],a[216:219],a[64:67],v[184:187]\n"
"s_add_u32 s84,s14,20480\n"
TR "a[212:213],v1 offset:36864\n"
MF "v[192:195],a[216:219],a[68:71],v[192:195]\n"
"s_add_u32 m0,s63,22528\n"
TR "a[214:215],v1 offset:37376\n"
MF "v[192:195],a[220:223],a[108:111],v[192:195]\n"
LD "v4,s[24:27],s84 offen lds\n"
MF "v[200:203],a[220:223],a[112:115],v[200:203]\n"
"ds_read_b128 a[188:191],v3 offset:45056\n"
MF "v[200:203],a[216:219],a[72:75],v[200:203]\n"
MF "v[208:211],a[216:219],a[76:79],v[208:211]\n"
"s_add_u32 s84,s14,29696\n"
MF "v[208:211],a[220:223],a[116:119],v[208:211]\n"
"s_waitcnt lgkmcnt(7)\n"
LD "v5,s[24:27],s84 offen offset:1024 lds\n"
MF "v[212:215],a[204:207],a[116:119],v[212:215]\n"
TR "a[216:217],v1 offset:32776\n"
MF "v[212:215],a[200:203],a[76:79],v[212:215]\n"
TR "a[218:219],v1 offset:33288\n"
MF "v[204:207],a[200:203],a[72:75],v[204:207]\n"
"ds_read_b128 a[152:155],v2 offset:47104\n"
MF "v[204:207],a[204:207],a[112:115],v[204:207]\n"
TR "a[220:221],v1 offset:36872\n"
MF "v[196:199],a[204:207],a[108:111],v[196:199]\n"
TR "a[222:223],v1 offset:37384\n"
MF "v[196:199],a[200:203],a[68:71],v[196:199]\n"
MF "v[188:191],a[200:203],a[64:67],v[188:191]\n"
"v_add_u32 v0,0xc000,v0\n"
MF "v[188:191],a[204:207],a[104:107],v[188:191]\n"
"v_add_u32 v1,0xc000,v1\n"
MF "v[180:183],a[204:207],a[100:103],v[180:183]\n"
"s_waitcnt vmcnt(26) lgkmcnt(0)\n"
MF "v[180:183],a[200:203],a[60:63],v[180:183]\n"
"s_barrier\n"
MF "v[216:219],a[208:211],a[60:63],v[216:219]\n"
TR "a[200:201],v0\n"
MF "v[216:219],a[212:215],a[100:103],v[216:219]\n"
"s_add_u32 m0,s62,32768\n"
TR "a[202:203],v0 offset:512\n"
MF "v[224:227],a[212:215],a[104:107],v[224:227]\n"
"s_add_u32 s82,s66,4718848\n"
"ds_read_b128 a[192:195],v3 offset:47104\n"
MF "v[224:227],a[208:211],a[64:67],v[224:227]\n"
"s_sub_u32 s15,s82,7864320\n"
TR "a[204:205],v0 offset:4096\n"
MF "v[232:235],a[208:211],a[68:71],v[232:235]\n"
"s_min_u32 s82,s82,s15\n"
TR "a[206:207],v0 offset:4608\n"
MF "v[232:235],a[212:215],a[108:111],v[232:235]\n"
LD "v6,s[28:31],s82 offen lds\n"
MF "v[240:243],a[212:215],a[112:115],v[240:243]\n"
"ds_read_b128 a[156:159],v2 offset:49152\n"
MF "v[240:243],a[208:211],a[72:75],v[240:243]\n"
"ds_read_b128 a[196:199],v3 offset:49152\n"
MF "v[248:251],a[208:211],a[76:79],v[248:251]\n"
MF "v[248:251],a[212:215],a[116:119],v[248:251]\n"
LD "v11,s[28:31],s82 offen offset:1024 lds\n"
MF "v[252:255],a[220:223],a[116:119],v[252:255]\n"
TR "a[208:209],v0 offset:8\n"
MF "v[252:255],a[216:219],a[76:79],v[252:255]\n"
TR "a[210:211],v0 offset:520\n"
MF "v[244:247],a[216:219],a[72:75],v[244:247]\n"
MF "v[244:247],a[220:223],a[112:115],v[244:247]\n"
TR "a[212:213],v0 offset:4104\n"
MF "v[236:239],a[220:223],a[108:111],v[236:239]\n"
TR "a[214:215],v0 offset:4616\n"
MF "v[236:239],a[216:219],a[68:71],v[236:239]\n"
LD "v12,s[28:31],s82 offen offset:2048 lds\n"
MF "v[228:231],a[216:219],a[64:67],v[228:231]\n"
MF "v[228:231],a[220:223],a[104:107],v[228:231]\n"
MF "v[220:223],a[220:223],a[100:103],v[220:223]\n"
MF "v[220:223],a[216:219],a[60:63],v[220:223]\n"
"s_waitcnt lgkmcnt(4)\n"
LD "v13,s[28:31],s82 offen offset:3072 lds\n"
MF "v[16:19],a[200:203],a[140:143],v[16:19]\n"
TR "a[216:217],v1\n"
MF "v[16:19],a[204:207],a[180:183],v[16:19]\n"
TR "a[218:219],v1 offset:512\n"
MF "v[24:27],a[204:207],a[184:187],v[24:27]\n"
MF "v[24:27],a[200:203],a[144:147],v[24:27]\n"
"s_add_u32 s84,s14,40960\n"
TR "a[220:221],v1 offset:4096\n"
MF "v[32:35],a[200:203],a[148:151],v[32:35]\n"
"s_add_u32 m0,s63,24576\n"
TR "a[222:223],v1 offset:4608\n"
MF "v[32:35],a[204:207],a[188:191],v[32:35]\n"
LD "v4,s[24:27],s84 offen lds\n"
MF "v[40:43],a[204:207],a[192:195],v[40:43]\n"
MF "v[40:43],a[200:203],a[152:155],v[40:43]\n"
MF "v[48:51],a[200:203],a[156:159],v[48:51]\n"
MF "v[48:51],a[204:207],a[196:199],v[48:51]\n"
"s_waitcnt lgkmcnt(4)\n"
MF "v[52:55],a[212:215],a[196:199],v[52:55]\n"
TR "a[200:201],v1 offset:8\n"
MF "v[52:55],a[208:211],a[156:159],v[52:55]\n"
TR "a[202:203],v1 offset:520\n"
MF "v[44:47],a[208:211],a[152:155],v[44:47]\n"
MF "v[44:47],a[212:215],a[192:195],v[44:47]\n"
TR "a[204:205],v1 offset:4104\n"
MF "v[36:39],a[212:215],a[188:191],v[36:39]\n"
TR "a[206:207],v1 offset:4616\n"
MF "v[36:39],a[208:211],a[148:151],v[36:39]\n"
MF "v[28:31],a[208:211],a[144:147],v[28:31]\n"
MF "v[28:31],a[212:215],a[184:187],v[28:31]\n"
MF "v[20:23],a[212:215],a[180:183],v[20:23]\n"
"s_waitcnt vmcnt(25) lgkmcnt(0)\n"
MF "v[20:23],a[208:211],a[140:143],v[20:23]\n"
"s_barrier\n"
MF "v[56:59],a[216:219],a[140:143],v[56:59]\n"
"s_add_u32 m0,s62,49152\n"
"s_add_u32 s82,s66,5505024\n"
TR "a[208:209],v0 offset:16384\n"
MF "v[56:59],a[220:223],a[180:183],v[56:59]\n"
"s_sub_u32 s15,s82,7864320\n"
"s_min_u32 s82,s82,s15\n"
TR "a[210:211],v0 offset:16896\n"
MF "v[64:67],a[220:223],a[184:187],v[64:67]\n"
LD "v6,s[28:31],s82 offen lds\n"
MF "v[64:67],a[216:219],a[144:147],v[64:67]\n"
TR "a[212:213],v0 offset:20480\n"
MF "v[72:75],a[216:219],a[148:151],v[72:75]\n"
TR "a[214:215],v0 offset:20992\n"
MF "v[72:75],a[220:223],a[188:191],v[72:75]\n"
"buffer_load_dword a249,v15,s[48:51],s59 offen\n"
MF "v[80:83],a[220:223],a[192:195],v[80:83]\n"
"s_bfe_u32 s59,s23,0x30003\n"
MF "v[80:83],a[216:219],a[152:155],v[80:83]\n"
LD "v11,s[28:31],s82 offen offset:1024 lds\n"
MF "v[88:91],a[216:219],a[156:159],v[88:91]\n"
"s_add_u32 s59,s59,5\n"
MF "v[88:91],a[220:223],a[196:199],v[88:91]\n"
"s_lshl_b32 s59,s59,7\n"
MF "v[92:95],a[204:207],a[196:199],v[92:95]\n"
"s_and_b32 s59,s59,768\n"
TR "a[216:217],v0 offset:16392\n"
MF "v[92:95],a[200:203],a[156:159],v[92:95]\n"
"s_add_u32 s59,s59,s60\n"
TR "a[218:219],v0 offset:16904\n"
MF "v[84:87],a[200:203],a[152:155],v[84:87]\n"
LD "v12,s[28:31],s82 offen offset:2048 lds\n"
MF "v[84:87],a[204:207],a[192:195],v[84:87]\n"
TR "a[220:221],v0 offset:20488\n"
MF "v[76:79],a[204:207],a[188:191],v[76:79]\n"
TR "a[222:223],v0 offset:21000\n"
MF "v[76:79],a[200:203],a[148:151],v[76:79]\n"
MF "v[68:71],a[200:203],a[144:147],v[68:71]\n"
MF "v[68:71],a[204:207],a[184:187],v[68:71]\n"
".Lpg3a:\n"
"s_cbranch_execz .Lisr1\n"
LD "v13,s[28:31],s82 offen offset:3072 lds\n"
MF "v[60:63],a[204:207],a[180:183],v[60:63]\n"
MF "v[60:63],a[200:203],a[140:143],v[60:63]\n"
"s_add_u32 s14,s81,1024\n"
"s_sub_u32 s15,s14,1280\n"
"s_waitcnt lgkmcnt(4)\n"
MF "v[96:99],a[208:211],a[140:143],v[96:99]\n"
"s_min_u32 s14,s14,s15\n"
"s_add_u32 s14,s14,s68\n"
TR "a[200:201],v1 offset:16384\n"
MF "v[96:99],a[212:215],a[180:183],v[96:99]\n"
"s_add_u32 s84,s14,0\n"
"s_add_u32 m0,s63,40960\n"
TR "a[202:203],v1 offset:16896\n"
MF "v[104:107],a[212:215],a[184:187],v[104:107]\n"
LD "v4,s[24:27],s84 offen lds\n"
MF "v[104:107],a[208:211],a[144:147],v[104:107]\n"
TR "a[204:205],v1 offset:20480\n"
MF "v[112:115],a[208:211],a[148:151],v[112:115]\n"
TR "a[206:207],v1 offset:20992\n"
MF "v[112:115],a[212:215],a[188:191],v[112:115]\n"
MF "v[120:123],a[212:215],a[192:195],v[120:123]\n"
"s_add_u32 s84,s14,9216\n"
MF "v[120:123],a[208:211],a[152:155],v[120:123]\n"
LD "v5,s[24:27],s84 offen offset:1024 lds\n"
MF "v[128:131],a[208:211],a[156:159],v[128:131]\n"
MF "v[128:131],a[212:215],a[196:199],v[128:131]\n"
"s_waitcnt lgkmcnt(4)\n"
MF "v[132:135],a[220:223],a[196:199],v[132:135]\n"
TR "a[208:209],v1 offset:16392\n"
MF "v[132:135],a[216:219],a[156:159],v[132:135]\n"
TR "a[210:211],v1 offset:16904\n"
MF "v[124:127],a[216:219],a[152:155],v[124:127]\n"
MF "v[124:127],a[220:223],a[192:195],v[124:127]\n"
TR "a[212:213],v1 offset:20488\n"
MF "v[116:119],a[220:223],a[188:191],v[116:119]\n"
TR "a[214:215],v1 offset:21000\n"
MF "v[116:119],a[216:219],a[148:151],v[116:119]\n"
MF "v[108:111],a[216:219],a[144:147],v[108:111]\n"
MF "v[108:111],a[220:223],a[184:187],v[108:111]\n"
MF "v[100:103],a[220:223],a[180:183],v[100:103]\n"
"s_waitcnt vmcnt(25) lgkmcnt(0)\n"
MF "v[100:103],a[216:219],a[140:143],v[100:103]\n"
"s_barrier\n"
MF "v[136:139],a[200:203],a[140:143],v[136:139]\n"
TR "a[216:217],v0 offset:32768\n"
MF "v[136:139],a[204:207],a[180:183],v[136:139]\n"
"s_add_u32 m0,s62,65536\n"
TR "a[218:219],v0 offset:33280\n"
MF "v[144:147],a[204:207],a[184:187],v[144:147]\n"
"s_add_u32 s82,s66,5505152\n"
"ds_read_b128 a[60:63],v2\n"
MF "v[144:147],a[200:203],a[144:147],v[144:147]\n"
"s_sub_u32 s15,s82,7864320\n"
TR "a[220:221],v0 offset:36864\n"
MF "v[152:155],a[200:203],a[148:151],v[152:155]\n"
"s_min_u32 s82,s82,s15\n"
TR "a[222:223],v0 offset:37376\n"
MF "v[152:155],a[204:207],a[188:191],v[152:155]\n"
LD "v6,s[28:31],s82 offen lds\n"
MF "v[160:163],a[204:207],a[192:195],v[160:163]\n"
"ds_read_b128 a[100:103],v3\n"
MF "v[160:163],a[200:203],a[152:155],v[160:163]\n"
MF "v[168:171],a[200:203],a[156:159],v[168:171]\n"
MF "v[168:171],a[204:207],a[196:199],v[168:171]\n"
LD "v11,s[28:31],s82 offen offset:1024 lds\n"
MF "v[172:175],a[212:215],a[196:199],v[172:175]\n"
TR "a[200:201],v0 offset:32776\n"
MF "v[172:175],a[208:211],a[156:159],v[172:175]\n"
TR "a[202:203],v0 offset:33288\n"
MF "v[164:167],a[208:211],a[152:155],v[164:167]\n"
"ds_read_b128 a[64:67],v2 offset:2048\n"
MF "v[164:167],a[212:215],a[192:195],v[164:167]\n"
TR "a[204:205],v0 offset:36872\n"
MF "v[156:159],a[212:215],a[188:191],v[156:159]\n"
TR "a[206:207],v0 offset:37384\n"
MF "v[156:159],a[208:211],a[148:151],v[156:159]\n"
LD "v12,s[28:31],s82 offen offset:2048 lds\n"
MF "v[148:151],a[208:211],a[144:147],v[148:151]\n"
"ds_read_b128 a[104:107],v3 offset:2048\n"
MF "v[148:151],a[212:215],a[184:187],v[148:151]\n"
MF "v[140:143],a[212:215],a[180:183],v[140:143]\n"
MF "v[140:143],a[208:211],a[140:143],v[140:143]\n"
"s_waitcnt lgkmcnt(7)\n"
LD "v13,s[28:31],s82 offen offset:3072 lds\n"
MF "v[176:179],a[216:219],a[140:143],v[176:179]\n"
TR "a[208:209],v1 offset:32768\n"
MF "v[176:179],a[220:223],a[180:183],v[176:179]\n"
TR "a[210:211],v1 offset:33280\n"
MF "v[184:187],a[220:223],a[184:187],v[184:187]\n"
"ds_read_b128 a[68:71],v2 offset:4096\n"
MF "v[184:187],a[216:219],a[144:147],v[184:187]\n"
"s_add_u32 s84,s14,20480\n"
TR "a[212:213],v1 offset:36864\n"
MF "v[192:195],a[216:219],a[148:151],v[192:195]\n"
"s_add_u32 m0,s63,43008\n"
TR "a[214:215],v1 offset:37376\n"
MF "v[192:195],a[220:223],a[188:191],v[192:195]\n"
LD "v4,s[24:27],s84 offen lds\n"
MF "v[200:203],a[220:223],a[192:195],v[200:203]\n"
"ds_read_b128 a[108:111],v3 offset:4096\n"
MF "v[200:203],a[216:219],a[152:155],v[200:203]\n"
MF "v[208:211],a[216:219],a[156:159],v[208:211]\n"
"s_add_u32 s84,s14,29696\n"
MF "v[208:211],a[220:223],a[196:199],v[208:211]\n"
"s_waitcnt lgkmcnt(7)\n"
LD "v5,s[24:27],s84 offen offset:1024 lds\n"
MF "v[212:215],a[204:207],a[196:199],v[212:215]\n"
TR "a[216:217],v1 offset:32776\n"
MF "v[212:215],a[200:203],a[156:159],v[212:215]\n"
TR "a[218:219],v1 offset:33288\n"
MF "v[204:207],a[200:203],a[152:155],v[204:207]\n"
"ds_read_b128 a[72:75],v2 offset:6144\n"
MF "v[204:207],a[204:207],a[192:195],v[204:207]\n"
TR "a[220:221],v1 offset:36872\n"
MF "v[196:199],a[204:207],a[188:191],v[196:199]\n"
TR "a[222:223],v1 offset:37384\n"
MF "v[196:199],a[200:203],a[148:151],v[196:199]\n"
MF "v[188:191],a[200:203],a[144:147],v[188:191]\n"
"v_add_u32 v0,0xffff4000,v0\n"
MF "v[188:191],a[204:207],a[184:187],v[188:191]\n"
"v_add_u32 v1,0xffff4000,v1\n"
MF "v[180:183],a[204:207],a[180:183],v[180:183]\n"
"s_waitcnt vmcnt(26) lgkmcnt(0)\n"
MF "v[180:183],a[200:203],a[140:143],v[180:183]\n"
"s_barrier\n"
MF "v[216:219],a[208:211],a[140:143],v[216:219]\n"
TR "a[200:201],v0\n"
MF "v[216:219],a[212:215],a[180:183],v[216:219]\n"
"s_add_u32 m0,s62,81920\n"
TR "a[202:203],v0 offset:512\n"
MF "v[224:227],a[212:215],a[184:187],v[224:227]\n"
"s_add_u32 s82,s66,5505280\n"
"ds_read_b128 a[112:115],v3 offset:6144\n"
MF "v[224:227],a[208:211],a[144:147],v[224:227]\n"
"s_sub_u32 s15,s82,7864320\n"
TR "a[204:205],v0 offset:4096\n"
MF "v[232:235],a[208:211],a[148:151],v[232:235]\n"
"s_min_u32 s82,s82,s15\n"
TR "a[206:207],v0 offset:4608\n"
MF "v[232:235],a[212:215],a[188:191],v[232:235]\n"
LD "v6,s[28:31],s82 offen lds\n"
MF "v[240:243],a[212:215],a[192:195],v[240:243]\n"
"ds_read_b128 a[76:79],v2 offset:8192\n"
MF "v[240:243],a[208:211],a[152:155],v[240:243]\n"
"ds_read_b128 a[116:119],v3 offset:8192\n"
MF "v[248:251],a[208:211],a[156:159],v[248:251]\n"
MF "v[248:251],a[212:215],a[196:199],v[248:251]\n"
LD "v11,s[28:31],s82 offen offset:1024 lds\n"
MF "v[252:255],a[220:223],a[196:199],v[252:255]\n"
TR "a[208:209],v0 offset:8\n"
MF "v[252:255],a[216:219],a[156:159],v[252:255]\n"
TR "a[210:211],v0 offset:520\n"
MF "v[244:247],a[216:219],a[152:155],v[244:247]\n"
MF "v[244:247],a[220:223],a[192:195],v[244:247]\n"
TR "a[212:213],v0 offset:4104\n"
MF "v[236:239],a[220:223],a[188:191],v[236:239]\n"
TR "a[214:215],v0 offset:4616\n"
MF "v[236:239],a[216:219],a[148:151],v[236:239]\n"
LD "v12,s[28:31],s82 offen offset:2048 lds\n"
MF "v[228:231],a[216:219],a[144:147],v[228:231]\n"
MF "v[228:231],a[220:223],a[184:187],v[228:231]\n"
MF "v[220:223],a[220:223],a[180:183],v[220:223]\n"
MF "v[220:223],a[216:219],a[140:143],v[220:223]\n"
"s_waitcnt lgkmcnt(4)\n"
LD "v13,s[28:31],s82 offen offset:3072 lds\n"
MF "v[16:19],a[200:203],a[60:63],v[16:19]\n"
TR "a[216:217],v1\n"
MF "v[16:19],a[204:207],a[100:103],v[16:19]\n"
TR "a[218:219],v1 offset:512\n"
MF "v[24:27],a[204:207],a[104:107],v[24:27]\n"
MF "v[24:27],a[200:203],a[64:67],v[24:27]\n"
"s_add_u32 s84,s14,40960\n"
TR "a[220:221],v1 offset:4096\n"
MF "v[32:35],a[200:203],a[68:71],v[32:35]\n"
"s_add_u32 m0,s63,45056\n"
TR "a[222:223],v1 offset:4608\n"
MF "v[32:35],a[204:207],a[108:111],v[32:35]\n"
LD "v4,s[24:27],s84 offen lds\n"
MF "v[40:43],a[204:207],a[112:115],v[40:43]\n"
MF "v[40:43],a[200:203],a[72:75],v[40:43]\n"
MF "v[48:51],a[200:203],a[76:79],v[48:51]\n"
MF "v[48:51],a[204:207],a[116:119],v[48:51]\n"
"s_waitcnt lgkmcnt(4)\n"
MF "v[52:55],a[212:215],a[116:119],v[52:55]\n"
TR "a[200:201],v1 offset:8\n"
MF "v[52:55],a[208:211],a[76:79],v[52:55]\n"
TR "a[202:203],v1 offset:520\n"
MF "v[44:47],a[208:211],a[72:75],v[44:47]\n"
MF "v[44:47],a[212:215],a[112:115],v[44:47]\n"
TR "a[204:205],v1 offset:4104\n"
MF "v[36:39],a[212:215],a[108:111],v[36:39]\n"
TR "a[206:207],v1 offset:4616\n"
MF "v[36:39],a[208:211],a[68:71],v[36:39]\n"
MF "v[28:31],a[208:211],a[64:67],v[28:31]\n"
MF "v[28:31],a[212:215],a[104:107],v[28:31]\n"
MF "v[20:23],a[212:215],a[100:103],v[20:23]\n"
"s_waitcnt vmcnt(25) lgkmcnt(0)\n"
MF "v[20:23],a[208:211],a[60:63],v[20:23]\n"
"s_barrier\n"
MF "v[56:59],a[216:219],a[60:63],v[56:59]\n"
"s_add_u32 m0,s62,0\n"
"s_add_u32 s82,s66,6291712\n"
TR "a[208:209],v0 offset:16384\n"
MF "v[56:59],a[220:223],a[100:103],v[56:59]\n"
"s_sub_u32 s15,s82,7864320\n"
"s_min_u32 s82,s82,s15\n"
TR "a[210:211],v0 offset:16896\n"
MF "v[64:67],a[220:223],a[104:107],v[64:67]\n"
LD "v6,s[28:31],s82 offen lds\n"
MF "v[64:67],a[216:219],a[64:67],v[64:67]\n"
TR "a[212:213],v0 offset:20480\n"
MF "v[72:75],a[216:219],a[68:71],v[72:75]\n"
TR "a[214:215],v0 offset:20992\n"
MF "v[72:75],a[220:223],a[108:111],v[72:75]\n"
"buffer_load_dword a249,v15,s[48:51],s59 offen\n"
MF "v[80:83],a[220:223],a[112:115],v[80:83]\n"
MF "v[80:83],a[216:219],a[72:75],v[80:83]\n"
LD "v11,s[28:31],s82 offen offset:1024 lds\n"
MF "v[88:91],a[216:219],a[76:79],v[88:91]\n"
MF "v[88:91],a[220:223],a[116:119],v[88:91]\n"
MF "v[92:95],a[204:207],a[116:119],v[92:95]\n"
TR "a[216:217],v0 offset:16392\n"
MF "v[92:95],a[200:203],a[76:79],v[92:95]\n"
TR "a[218:219],v0 offset:16904\n"
MF "v[84:87],a[200:203],a[72:75],v[84:87]\n"
LD "v12,s[28:31],s82 offen offset:2048 lds\n"
MF "v[84:87],a[204:207],a[112:115],v[84:87]\n"
TR "a[220:221],v0 offset:20488\n"
MF "v[76:79],a[204:207],a[108:111],v[76:79]\n"
TR "a[222:223],v0 offset:21000\n"
MF "v[76:79],a[200:203],a[68:71],v[76:79]\n"
MF "v[68:71],a[200:203],a[64:67],v[68:71]\n"
MF "v[68:71],a[204:207],a[104:107],v[68:71]\n"
LD "v13,s[28:31],s82 offen offset:3072 lds\n"
MF "v[60:63],a[204:207],a[100:103],v[60:63]\n"
MF "v[60:63],a[200:203],a[60:63],v[60:63]\n"
"s_add_u32 s14,s81,1152\n"
"s_sub_u32 s15,s14,1280\n"
"s_waitcnt lgkmcnt(4)\n"
MF "v[96:99],a[208:211],a[60:63],v[96:99]\n"
"s_min_u32 s14,s14,s15\n"
"s_add_u32 s14,s14,s68\n"
TR "a[200:201],v1 offset:16384\n"
MF "v[96:99],a[212:215],a[100:103],v[96:99]\n"
"s_add_u32 s84,s14,0\n"
"s_add_u32 m0,s63,0\n"
TR "a[202:203],v1 offset:16896\n"
MF "v[104:107],a[212:215],a[104:107],v[104:107]\n"
LD "v4,s[24:27],s84 offen lds\n"
MF "v[104:107],a[208:211],a[64:67],v[104:107]\n"
TR "a[204:205],v1 offset:20480\n"
MF "v[112:115],a[208:211],a[68:71],v[112:115]\n"
TR "a[206:207],v1 offset:20992\n"
MF "v[112:115],a[212:215],a[108:111],v[112:115]\n"
MF "v[120:123],a[212:215],a[112:115],v[120:123]\n"
"s_add_u32 s84,s14,9216\n"
MF "v[120:123],a[208:211],a[72:75],v[120:123]\n"
LD "v5,s[24:27],s84 offen offset:1024 lds\n"
MF "v[128:131],a[208:211],a[76:79],v[128:131]\n"
MF "v[128:131],a[212:215],a[116:119],v[128:131]\n"
"s_waitcnt lgkmcnt(4)\n"
MF "v[132:135],a[220:223],a[116:119],v[132:135]\n"
TR "a[208:209],v1 offset:16392\n"
MF "v[132:135],a[216:219],a[76:79],v[132:135]\n"
"s_add_u32 s84,s14,18432\n"
TR "a[210:211],v1 offset:16904\n"
MF "v[124:127],a[216:219],a[72:75],v[124:127]\n"
LD "v4,s[24:27],s84 offen offset:2048 lds\n"
MF "v[124:127],a[220:223],a[112:115],v[124:127]\n"
TR "a[212:213],v1 offset:20488\n"
MF "v[116:119],a[220:223],a[108:111],v[116:119]\n"
TR "a[214:215],v1 offset:21000\n"
MF "v[116:119],a[216:219],a[68:71],v[116:119]\n"
MF "v[108:111],a[216:219],a[64:67],v[108:111]\n"
MF "v[108:111],a[220:223],a[104:107],v[108:111]\n"
MF "v[100:103],a[220:223],a[100:103],v[100:103]\n"
"s_waitcnt vmcnt(26) lgkmcnt(0)\n"
MF "v[100:103],a[216:219],a[60:63],v[100:103]\n"
"s_barrier\n"
MF "v[136:139],a[200:203],a[60:63],v[136:139]\n"
TR "a[216:217],v0 offset:32768\n"
MF "v[136:139],a[204:207],a[100:103],v[136:139]\n"
"s_add_u32 m0,s62,16384\n"
TR "a[218:219],v0 offset:33280\n"
MF "v[144:147],a[204:207],a[104:107],v[144:147]\n"
"s_add_u32 s82,s66,7078144\n"
"ds_read_b128 a[140:143],v2 offset:20480\n"
MF "v[144:147],a[200:203],a[64:67],v[144:147]\n"
"s_sub_u32 s15,s82,7864320\n"
TR "a[220:221],v0 offset:36864\n"
MF "v[152:155],a[200:203],a[68:71],v[152:155]\n"
"s_min_u32 s82,s82,s15\n"
TR "a[222:223],v0 offset:37376\n"
MF "v[152:155],a[204:207],a[108:111],v[152:155]\n"
LD "v6,s[28:31],s82 offen lds\n"
MF "v[160:163],a[204:207],a[112:115],v[160:163]\n"
"ds_read_b128 a[180:183],v3 offset:20480\n"
MF "v[160:163],a[200:203],a[72:75],v[160:163]\n"
MF "v[168:171],a[200:203],a[76:79],v[168:171]\n"
MF "v[168:171],a[204:207],a[116:119],v[168:171]\n"
LD "v11,s[28:31],s82 offen offset:1024 lds\n"
MF "v[172:175],a[212:215],a[116:119],v[172:175]\n"
TR "a[200:201],v0 offset:32776\n"
MF "v[172:175],a[208:211],a[76:79],v[172:175]\n"
TR "a[202:203],v0 offset:33288\n"
MF "v[164:167],a[208:211],a[72:75],v[164:167]\n"
"ds_read_b128 a[144:147],v2 offset:22528\n"
MF "v[164:167],a[212:215],a[112:115],v[164:167]\n"
TR "a[204:205],v0 offset:36872\n"
MF "v[156:159],a[212:215],a[108:111],v[156:159]\n"
TR "a[206:207],v0 offset:37384\n"
MF "v[156:159],a[208:211],a[68:71],v[156:159]\n"
LD "v12,s[28:31],s82 offen offset:2048 lds\n"
MF "v[148:151],a[208:211],a[64:67],v[148:151]\n"
"ds_read_b128 a[184:187],v3 offset:22528\n"
MF "v[148:151],a[212:215],a[104:107],v[148:151]\n"
MF "v[140:143],a[212:215],a[100:103],v[140:143]\n"
MF "v[140:143],a[208:211],a[60:63],v[140:143]\n"
"s_waitcnt lgkmcnt(7)\n"
LD "v13,s[28:31],s82 offen offset:3072 lds\n"
MF "v[176:179],a[216:219],a[60:63],v[176:179]\n"
TR "a[208:209],v1 offset:32768\n"
MF "v[176:179],a[220:223],a[100:103],v[176:179]\n"
TR "a[210:211],v1 offset:33280\n"
MF "v[184:187],a[220:223],a[104:107],v[184:187]\n"
"ds_read_b128 a[148:151],v2 offset:24576\n"
MF "v[184:187],a[216:219],a[64:67],v[184:187]\n"
"s_add_u32 s84,s14,30720\n"
TR "a[212:213],v1 offset:36864\n"
MF "v[192:195],a[216:219],a[68:71],v[192:195]\n"
"s_add_u32 m0,s63,3072\n"
TR "a[214:215],v1 offset:37376\n"
MF "v[192:195],a[220:223],a[108:111],v[192:195]\n"
LD "v5,s[24:27],s84 offen lds\n"
MF "v[200:203],a[220:223],a[112:115],v[200:203]\n"
"ds_read_b128 a[188:191],v3 offset:24576\n"
MF "v[200:203],a[216:219],a[72:75],v[200:203]\n"
MF "v[208:211],a[216:219],a[76:79],v[208:211]\n"
"s_add_u32 s84,s14,39936\n"
MF "v[208:211],a[220:223],a[116:119],v[208:211]\n"
"s_waitcnt lgkmcnt(7)\n"
LD "v4,s[24:27],s84 offen offset:1024 lds\n"
MF "v[212:215],a[204:207],a[116:119],v[212:215]\n"
TR "a[216:217],v1 offset:32776\n"
MF "v[212:215],a[200:203],a[76:79],v[212:215]\n"
TR "a[218:219],v1 offset:33288\n"
MF "v[204:207],a[200:203],a[72:75],v[204:207]\n"
"ds_read_b128 a[152:155],v2 offset:26624\n"
MF "v[204:207],a[204:207],a[112:115],v[204:207]\n"
TR "a[220:221],v1 offset:36872\n"
MF "v[196:199],a[204:207],a[108:111],v[196:199]\n"
TR "a[222:223],v1 offset:37384\n"
MF "v[196:199],a[200:203],a[68:71],v[196:199]\n"
MF "v[188:191],a[200:203],a[64:67],v[188:191]\n"
"v_add_u32 v0,0xc000,v0\n"
MF "v[188:191],a[204:207],a[104:107],v[188:191]\n"
"v_add_u32 v1,0xc000,v1\n"
MF "v[180:183],a[204:207],a[100:103],v[180:183]\n"
"s_waitcnt vmcnt(27) lgkmcnt(0)\n"
"s_branch .Lqj\n"
".Lpgq0_c:\n"
"s_cbranch_execz .Lpgqy_c\n"
".Lpgq0_f:\n"
"s_cbranch_execz .Lpg12f\n"
".Lq0:\n"
MF "v[16:19],a[200:203],a[60:63],0\n"
"s_add_u32 m0,s62,49152\n"
"s_add_u32 s82,s66,786432\n"
TR "a[216:217],v1\n"
MF "v[16:19],a[204:207],a[100:103],v[16:19]\n"
"s_sub_u32 s15,s82,7864320\n"
"s_min_u32 s82,s82,s15\n"
TR "a[218:219],v1 offset:512\n"
MF "v[24:27],a[204:207],a[104:107],0\n"
LD "v6,s[28:31],s82 offen lds\n"
MF "v[24:27],a[200:203],a[64:67],v[24:27]\n"
TR "a[220:221],v1 offset:4096\n"
MF "v[32:35],a[200:203],a[68:71],0\n"
TR "a[222:223],v1 offset:4608\n"
MF "v[32:35],a[204:207],a[108:111],v[32:35]\n"
"v_max3_f32 v220,|v16|,|v17|,|v18|\n"
"v_max_f32_e64 v220,v220,|v19|\n"
MF "v[40:43],a[204:207],a[112:115],0\n"
"v_readlane_b32 s12,v220,3\n"
"v_readlane_b32 s6,v220,22\n"
MF "v[40:43],a[200:203],a[72:75],v[40:43]\n"
"s_min_u32 s12,s12,s6\n"
"v_readlane_b32 s7,v220,41\n"
"v_readlane_b32 s8,v220,60\n"
MF "v[48:51],a[200:203],a[76:79],0\n"
"s_min_u32 s7,s7,s8\n"
MF "v[48:51],a[204:207],a[116:119],v[48:51]\n"
LD "v11,s[28:31],s82 offen offset:1024 lds\n"
MF "v[52:55],a[212:215],a[116:119],0\n"
"s_min_u32 s12,s12,s7\n"
TR "a[200:201],v1 offset:8\n"
MF "v[52:55],a[208:211],a[76:79],v[52:55]\n"
"s_and_b32 s12,s12,0x7f800000\n"
TR "a[202:203],v1 offset:520\n"
MF "v[44:47],a[208:211],a[72:75],0\n"
"s_sub_u32 s12,s12,0x14000000\n"
MF "v[44:47],a[212:215],a[112:115],v[44:47]\n"
"s_cmp_lt_u32 s12,0x50000000\n"
"s_cselect_b32 s12,s12,0xe8400000\n"
TR "a[204:205],v1 offset:4104\n"
MF "v[36:39],a[212:215],a[108:111],0\n"
"s_add_u32 s12,s12,0x17c00000\n"
TR "a[206:207],v1 offset:4616\n"
MF "v[36:39],a[208:211],a[68:71],v[36:39]\n"
"v_mov_b32 v220,s12\n"
"v_mov_b32 v221,s12\n"
MF "v[28:31],a[208:211],a[64:67],0\n"
"v_mov_b32 v222,s12\n"
"v_mov_b32 v223,s12\n"
MF "v[28:31],a[212:215],a[104:107],v[28:31]\n"
LD "v12,s[28:31],s82 offen offset:2048 lds\n"
MF "v[20:23],a[212:215],a[100:103],0\n"
"s_waitcnt vmcnt(7) lgkmcnt(0)\n"
MF "v[20:23],a[208:211],a[60:63],v[20:23]\n"
"s_barrier\n"
MF "v[56:59],a[216:219],a[60:63],v[220:223]\n"
TR "a[208:209],v0 offset:16384\n"
MF "v[56:59],a[220:223],a[100:103],v[56:59]\n"
TR "a[210:211],v0 offset:16896\n"
MF "v[64:67],a[220:223],a[104:107],v[220:223]\n"
LD "v13,s[28:31],s82 offen offset:3072 lds\n"
MF "v[64:67],a[216:219],a[64:67],v[64:67]\n"
"s_add_u32 m0,s62,65536\n"
TR "a[212:213],v0 offset:20480\n"
MF "v[72:75],a[216:219],a[68:71],v[220:223]\n"
"s_add_u32 s82,s66,786560\n"
TR "a[214:215],v0 offset:20992\n"
MF "v[72:75],a[220:223],a[108:111],v[72:75]\n"
"s_sub_u32 s15,s82,7864320\n"
MF "v[80:83],a[220:223],a[112:115],v[220:223]\n"
"s_min_u32 s82,s82,s15\n"
MF "v[80:83],a[216:219],a[72:75],v[80:83]\n"
LD "v6,s[28:31],s82 offen lds\n"
MF "v[88:91],a[216:219],a[76:79],v[220:223]\n"
MF "v[88:91],a[220:223],a[116:119],v[88:91]\n"
MF "v[92:95],a[204:207],a[116:119],v[220:223]\n"
TR "a[216:217],v0 offset:16392\n"
MF "v[92:95],a[200:203],a[76:79],v[92:95]\n"
TR "a[218:219],v0 offset:16904\n"
MF "v[84:87],a[200:203],a[72:75],v[220:223]\n"
LD "v11,s[28:31],s82 offen offset:1024 lds\n"
MF "v[84:87],a[204:207],a[112:115],v[84:87]\n"
TR "a[220:221],v0 offset:20488\n"
MF "v[76:79],a[204:207],a[108:111],v[220:223]\n"
TR "a[222:223],v0 offset:21000\n"
MF "v[76:79],a[200:203],a[68:71],v[76:79]\n"
MF "v[68:71],a[200:203],a[64:67],v[220:223]\n"
MF "v[68:71],a[204:207],a[104:107],v[68:71]\n"
LD "v12,s[28:31],s82 offen offset:2048 lds\n"
MF "v[60:63],a[204:207],a[100:103],v[220:223]\n"
MF "v[60:63],a[200:203],a[60:63],v[60:63]\n"
"s_waitcnt lgkmcnt(4)\n"
MF "v[96:99],a[208:211],a[60:63],v[220:223]\n"
TR "a[200:201],v1 offset:16384\n"
MF "v[96:99],a[212:215],a[100:103],v[96:99]\n"
TR "a[202:203],v1 offset:16896\n"
MF "v[104:107],a[212:215],a[104:107],v[220:223]\n"
LD "v13,s[28:31],s82 offen offset:3072 lds\n"
MF "v[104:107],a[208:211],a[64:67],v[104:107]\n"
"s_add_u32 m0,s62,81920\n"
TR "a[204:205],v1 offset:20480\n"
MF "v[112:115],a[208:211],a[68:71],v[220:223]\n"
"s_add_u32 s82,s66,786688\n"
TR "a[206:207],v1 offset:20992\n"
MF "v[112:115],a[212:215],a[108:111],v[112:115]\n"
"s_sub_u32 s15,s82,7864320\n"
MF "v[120:123],a[212:215],a[112:115],v[220:223]\n"
"s_min_u32 s82,s82,s15\n"
MF "v[120:123],a[208:211],a[72:75],v[120:123]\n"
LD "v6,s[28:31],s82 offen lds\n"
MF "v[128:131],a[208:211],a[76:79],v[220:223]\n"
MF "v[128:131],a[212:215],a[116:119],v[128:131]\n"
"s_waitcnt lgkmcnt(4)\n"
MF "v[132:135],a[220:223],a[116:119],v[220:223]\n"
TR "a[208:209],v1 offset:16392\n"
MF "v[132:135],a[216:219],a[76:79],v[132:135]\n"
TR "a[210:211],v1 offset:16904\n"
MF "v[124:127],a[216:219],a[72:75],v[220:223]\n"
LD "v11,s[28:31],s82 offen offset:1024 lds\n"
MF "v[124:127],a[220:223],a[112:115],v[124:127]\n"
TR "a[212:213],v1 offset:20488\n"
MF "v[116:119],a[220:223],a[108:111],v[220:223]\n"
TR "a[214:215],v1 offset:21000\n"
MF "v[116:119],a[216:219],a[68:71],v[116:119]\n"
MF "v[108:111],a[216:219],a[64:67],v[220:223]\n"
MF "v[108:111],a[220:223],a[104:107],v[108:111]\n"
LD "v12,s[28:31],s82 offen offset:2048 lds\n"
MF "v[100:103],a[220:223],a[100:103],v[220:223]\n"
"s_waitcnt vmcnt(11) lgkmcnt(0)\n"
MF "v[100:103],a[216:219],a[60:63],v[100:103]\n"
"s_barrier\n"
MF "v[136:139],a[200:203],a[60:63],v[220:223]\n"
TR "a[216:217],v0 offset:32768\n"
MF "v[136:139],a[204:207],a[100:103],v[136:139]\n"
TR "a[218:219],v0 offset:33280\n"
MF "v[144:147],a[204:207],a[104:107],v[220:223]\n"
"ds_read_b128 a[140:143],v2 offset:20480\n"
MF "v[144:147],a[200:203],a[64:67],v[144:147]\n"
TR "a[220:221],v0 offset:36864\n"
MF "v[152:155],a[200:203],a[68:71],v[220:223]\n"
TR "a[222:223],v0 offset:37376\n"
MF "v[152:155],a[204:207],a[108:111],v[152:155]\n"
LD "v13,s[28:31],s82 offen offset:3072 lds\n"
MF "v[160:163],a[204:207],a[112:115],v[220:223]\n"
"ds_read_b128 a[180:183],v3 offset:20480\n"
MF "v[160:163],a[200:203],a[72:75],v[160:163]\n"
"s_add_u32 m0,s62,0\n"
MF "v[168:171],a[200:203],a[76:79],v[220:223]\n"
"s_add_u32 s82,s66,1572864\n"
"s_sub_u32 s15,s82,7864320\n"
"s_min_u32 s82,s82,s15\n"
MF "v[168:171],a[204:207],a[116:119],v[168:171]\n"
LD "v6,s[28:31],s82 offen lds\n"
MF "v[172:175],a[212:215],a[116:119],v[220:223]\n"
TR "a[200:201],v0 offset:32776\n"
MF "v[172:175],a[208:211],a[76:79],v[172:175]\n"
TR "a[202:203],v0 offset:33288\n"
MF "v[164:167],a[208:211],a[72:75],v[220:223]\n"
"ds_read_b128 a[144:147],v2 offset:22528\n"
MF "v[164:167],a[212:215],a[112:115],v[164:167]\n"
TR "a[204:205],v0 offset:36872\n"
MF "v[156:159],a[212:215],a[108:111],v[220:223]\n"
TR "a[206:207],v0 offset:37384\n"
MF "v[156:159],a[208:211],a[68:71],v[156:159]\n"
LD "v11,s[28:31],s82 offen offset:1024 lds\n"
MF "v[148:151],a[208:211],a[64:67],v[220:223]\n"
"ds_read_b128 a[184:187],v3 offset:22528\n"
MF "v[148:151],a[212:215],a[104:107],v[148:151]\n"
MF "v[140:143],a[212:215],a[100:103],v[220:223]\n"
MF "v[140:143],a[208:211],a[60:63],v[140:143]\n"
"s_waitcnt lgkmcnt(7)\n"
LD "v12,s[28:31],s82 offen offset:2048 lds\n"
MF "v[176:179],a[216:219],a[60:63],v[220:223]\n"
TR "a[208:209],v1 offset:32768\n"
MF "v[176:179],a[220:223],a[100:103],v[176:179]\n"
".Lpg4c:\n"
"s_cbranch_execz .Lpg14c\n"
".Lpg4f:\n"
"s_cbranch_execz .Lpg13f\n"
TR "a[210:211],v1 offset:33280\n"
MF "v[184:187],a[220:223],a[104:107],v[220:223]\n"
"ds_read_b128 a[148:151],v2 offset:24576\n"
MF "v[184:187],a[216:219],a[64:67],v[184:187]\n"
TR "a[212:213],v1 offset:36864\n"
MF "v[192:195],a[216:219],a[68:71],v[220:223]\n"
TR "a[214:215],v1 offset:37376\n"
MF "v[192:195],a[220:223],a[108:111],v[192:195]\n"
LD "v13,s[28:31],s82 offen offset:3072 lds\n"
MF "v[200:203],a[220:223],a[112:115],v[220:223]\n"
"ds_read_b128 a[188:191],v3 offset:24576\n"
MF "v[200:203],a[216:219],a[72:75],v[200:203]\n"
"s_add_u32 s14,s81,384\n"
"s_sub_u32 s15,s14,1280\n"
"s_min_u32 s14,s14,s15\n"
MF "v[208:211],a[216:219],a[76:79],v[220:223]\n"
"s_add_u32 s14,s14,s68\n"
"s_add_u32 s84,s14,0\n"
"s_add_u32 m0,s63,0\n"
MF "v[208:211],a[220:223],a[116:119],v[208:211]\n"
"s_waitcnt lgkmcnt(7)\n"
LD "v4,s[24:27],s84 offen lds\n"
MF "v[212:215],a[204:207],a[116:119],v[220:223]\n"
TR "a[216:217],v1 offset:32776\n"
MF "v[212:215],a[200:203],a[76:79],v[212:215]\n"
TR "a[218:219],v1 offset:33288\n"
MF "v[204:207],a[200:203],a[72:75],v[220:223]\n"
"ds_read_b128 a[152:155],v2 offset:26624\n"
MF "v[204:207],a[204:207],a[112:115],v[204:207]\n"
TR "a[220:221],v1 offset:36872\n"
MF "v[196:199],a[204:207],a[108:111],v[220:223]\n"
"s_add_u32 s84,s14,9216\n"
TR "a[222:223],v1 offset:37384\n"
MF "v[196:199],a[200:203],a[68:71],v[196:199]\n"
LD "v5,s[24:27],s84 offen offset:1024 lds\n"
MF "v[188:191],a[200:203],a[64:67],v[220:223]\n"
"s_add_u32 m0,s62,16384\n"
"s_add_u32 s82,s66,1572992\n"
"v_add_u32 v0,0xc000,v0\n"
MF "v[188:191],a[204:207],a[104:107],v[188:191]\n"
"s_sub_u32 s15,s82,7864320\n"
"s_min_u32 s82,s82,s15\n"
"v_add_u32 v1,0xc000,v1\n"
MF "v[180:183],a[204:207],a[100:103],v[220:223]\n"
"s_waitcnt vmcnt(14) lgkmcnt(0)\n"
LD "v6,s[28:31],s82 offen lds\n"
MF "v[180:183],a[200:203],a[60:63],v[180:183]\n"
"s_barrier\n"
MF "v[216:219],a[208:211],a[60:63],v[220:223]\n"
TR "a[200:201],v0\n"
MF "v[216:219],a[212:215],a[100:103],v[216:219]\n"
TR "a[202:203],v0 offset:512\n"
MF "v[224:227],a[212:215],a[104:107],v[220:223]\n"
"ds_read_b128 a[192:195],v3 offset:26624\n"
MF "v[224:227],a[208:211],a[64:67],v[224:227]\n"
TR "a[204:205],v0 offset:4096\n"
MF "v[232:235],a[208:211],a[68:71],v[220:223]\n"
TR "a[206:207],v0 offset:4608\n"
MF "v[232:235],a[212:215],a[108:111],v[232:235]\n"
LD "v11,s[28:31],s82 offen offset:1024 lds\n"
MF "v[240:243],a[212:215],a[112:115],v[220:223]\n"
"ds_read_b128 a[156:159],v2 offset:28672\n"
MF "v[240:243],a[208:211],a[72:75],v[240:243]\n"
"ds_read_b128 a[196:199],v3 offset:28672\n"
MF "v[248:251],a[208:211],a[76:79],v[220:223]\n"
MF "v[248:251],a[212:215],a[116:119],v[248:251]\n"
LD "v12,s[28:31],s82 offen offset:2048 lds\n"
MF "v[252:255],a[220:223],a[116:119],v[220:223]\n"
TR "a[208:209],v0 offset:8\n"
MF "v[252:255],a[216:219],a[76:79],v[252:255]\n"
TR "a[210:211],v0 offset:520\n"
MF "v[244:247],a[216:219],a[72:75],v[220:223]\n"
MF "v[244:247],a[220:223],a[112:115],v[244:247]\n"
TR "a[212:213],v0 offset:4104\n"
MF "v[236:239],a[220:223],a[108:111],v[220:223]\n"
TR "a[214:215],v0 offset:4616\n"
MF "v[236:239],a[216:219],a[68:71],v[236:239]\n"
LD "v13,s[28:31],s82 offen offset:3072 lds\n"
MF "v[228:231],a[216:219],a[64:67],v[220:223]\n"
MF "v[228:231],a[220:223],a[104:107],v[228:231]\n"
"s_add_u32 s84,s14,20480\n"
MF "v[220:223],a[220:223],a[100:103],v[220:223]\n"
"s_add_u32 m0,s63,2048\n"
MF "v[220:223],a[216:219],a[60:63],v[220:223]\n"
"s_waitcnt lgkmcnt(4)\n"
LD "v4,s[24:27],s84 offen lds\n"
MF "v[16:19],a[200:203],a[140:143],v[16:19]\n"
TR "a[216:217],v1\n"
MF "v[16:19],a[204:207],a[180:183],v[16:19]\n"
TR "a[218:219],v1 offset:512\n"
MF "v[24:27],a[204:207],a[184:187],v[24:27]\n"
MF "v[24:27],a[200:203],a[144:147],v[24:27]\n"
TR "a[220:221],v1 offset:4096\n"
MF "v[32:35],a[200:203],a[148:151],v[32:35]\n"
"s_add_u32 s84,s14,29696\n"
TR "a[222:223],v1 offset:4608\n"
MF "v[32:35],a[204:207],a[188:191],v[32:35]\n"
LD "v5,s[24:27],s84 offen offset:1024 lds\n"
MF "v[40:43],a[204:207],a[192:195],v[40:43]\n"
MF "v[40:43],a[200:203],a[152:155],v[40:43]\n"
"s_add_u32 m0,s62,32768\n"
MF "v[48:51],a[200:203],a[156:159],v[48:51]\n"
"s_add_u32 s82,s66,1573120\n"
"s_sub_u32 s15,s82,7864320\n"
"s_min_u32 s82,s82,s15\n"
MF "v[48:51],a[204:207],a[196:199],v[48:51]\n"
"s_waitcnt lgkmcnt(4)\n"
LD "v6,s[28:31],s82 offen lds\n"
MF "v[52:55],a[212:215],a[196:199],v[52:55]\n"
TR "a[200:201],v1 offset:8\n"
MF "v[52:55],a[208:211],a[156:159],v[52:55]\n"
TR "a[202:203],v1 offset:520\n"
MF "v[44:47],a[208:211],a[152:155],v[44:47]\n"
MF "v[44:47],a[212:215],a[192:195],v[44:47]\n"
TR "a[204:205],v1 offset:4104\n"
MF "v[36:39],a[212:215],a[188:191],v[36:39]\n"
TR "a[206:207],v1 offset:4616\n"
MF "v[36:39],a[208:211],a[148:151],v[36:39]\n"
LD "v11,s[28:31],s82 offen offset:1024 lds\n"
MF "v[28:31],a[208:211],a[144:147],v[28:31]\n"
MF "v[28:31],a[212:215],a[184:187],v[28:31]\n"
MF "v[20:23],a[212:215],a[180:183],v[20:23]\n"
"s_waitcnt vmcnt(18) lgkmcnt(0)\n"
LD "v12,s[28:31],s82 offen offset:2048 lds\n"
"s_bitcmp0_b32 s23,12\n"
"s_cbranch_scc1 .Lisx2\n"
"s_mov_b64 exec,0\n"
"s_cmp_eq_u32 s56,0\n"
"s_cbranch_scc1 .Lpg11d\n"
"s_cmp_eq_u32 s56,1\n"
"s_cbranch_scc1 .Lpg10d\n"
"s_cmp_eq_u32 s56,2\n"
"s_cbranch_scc1 .Lpgt0kx_d\n"
"s_branch .Lpg8d\n"
".Lisr2:\n"
"s_mov_b64 exec,-1\n"
".Lisx2:\n"
MF "v[20:23],a[208:211],a[140:143],v[20:23]\n"
"s_barrier\n"
MF "v[56:59],a[216:219],a[140:143],v[56:59]\n"
TR "a[208:209],v0 offset:16384\n"
MF "v[56:59],a[220:223],a[180:183],v[56:59]\n"
TR "a[210:211],v0 offset:16896\n"
MF "v[64:67],a[220:223],a[184:187],v[64:67]\n"
LD "v13,s[28:31],s82 offen offset:3072 lds\n"
MF "v[64:67],a[216:219],a[144:147],v[64:67]\n"
TR "a[212:213],v0 offset:20480\n"
MF "v[72:75],a[216:219],a[148:151],v[72:75]\n"
TR "a[214:215],v0 offset:20992\n"
MF "v[72:75],a[220:223],a[188:191],v[72:75]\n"
"s_add_u32 s84,s14,40960\n"
MF "v[80:83],a[220:223],a[192:195],v[80:83]\n"
"s_add_u32 m0,s63,4096\n"
MF "v[80:83],a[216:219],a[152:155],v[80:83]\n"
LD "v4,s[24:27],s84 offen lds\n"
MF "v[88:91],a[216:219],a[156:159],v[88:91]\n"
MF "v[88:91],a[220:223],a[196:199],v[88:91]\n"
"s_sub_u32 s66,s66,s83\n"
MF "v[92:95],a[204:207],a[196:199],v[92:95]\n"
"s_add_u32 m0,s62,49152\n"
"s_add_u32 s82,s66,2359296\n"
TR "a[216:217],v0 offset:16392\n"
MF "v[92:95],a[200:203],a[156:159],v[92:95]\n"
"s_sub_u32 s15,s82,7864320\n"
"s_min_u32 s82,s82,s15\n"
TR "a[218:219],v0 offset:16904\n"
MF "v[84:87],a[200:203],a[152:155],v[84:87]\n"
LD "v6,s[28:31],s82 offen lds\n"
MF "v[84:87],a[204:207],a[192:195],v[84:87]\n"
TR "a[220:221],v0 offset:20488\n"
MF "v[76:79],a[204:207],a[188:191],v[76:79]\n"
TR "a[222:223],v0 offset:21000\n"
MF "v[76:79],a[200:203],a[148:151],v[76:79]\n"
MF "v[68:71],a[200:203],a[144:147],v[68:71]\n"
MF "v[68:71],a[204:207],a[184:187],v[68:71]\n"
LD "v11,s[28:31],s82 offen offset:1024 lds\n"
MF "v[60:63],a[204:207],a[180:183],v[60:63]\n"
MF "v[60:63],a[200:203],a[140:143],v[60:63]\n"
"s_waitcnt lgkmcnt(4)\n"
MF "v[96:99],a[208:211],a[140:143],v[96:99]\n"
TR "a[200:201],v1 offset:16384\n"
MF "v[96:99],a[212:215],a[180:183],v[96:99]\n"
TR "a[202:203],v1 offset:16896\n"
MF "v[104:107],a[212:215],a[184:187],v[104:107]\n"
LD "v12,s[28:31],s82 offen offset:2048 lds\n"
MF "v[104:107],a[208:211],a[144:147],v[104:107]\n"
TR "a[204:205],v1 offset:20480\n"
MF "v[112:115],a[208:211],a[148:151],v[112:115]\n"
TR "a[206:207],v1 offset:20992\n"
MF "v[112:115],a[212:215],a[188:191],v[112:115]\n"
MF "v[120:123],a[212:215],a[192:195],v[120:123]\n"
MF "v[120:123],a[208:211],a[152:155],v[120:123]\n"
LD "v13,s[28:31],s82 offen offset:3072 lds\n"
MF "v[128:131],a[208:211],a[156:159],v[128:131]\n"
MF "v[128:131],a[212:215],a[196:199],v[128:131]\n"
"s_add_u32 s14,s81,512\n"
"s_sub_u32 s15,s14,1280\n"
"s_waitcnt lgkmcnt(4)\n"
MF "v[132:135],a[220:223],a[196:199],v[132:135]\n"
"s_min_u32 s14,s14,s15\n"
"s_add_u32 s14,s14,s68\n"
TR "a[208:209],v1 offset:16392\n"
MF "v[132:135],a[216:219],a[156:159],v[132:135]\n"
"s_add_u32 s84,s14,0\n"
"s_add_u32 m0,s63,20480\n"
TR "a[210:211],v1 offset:16904\n"
MF "v[124:127],a[216:219],a[152:155],v[124:127]\n"
LD "v4,s[24:27],s84 offen lds\n"
MF "v[124:127],a[220:223],a[192:195],v[124:127]\n"
TR "a[212:213],v1 offset:20488\n"
MF "v[116:119],a[220:223],a[188:191],v[116:119]\n"
TR "a[214:215],v1 offset:21000\n"
MF "v[116:119],a[216:219],a[148:151],v[116:119]\n"
MF "v[108:111],a[216:219],a[144:147],v[108:111]\n"
"s_add_u32 s84,s14,9216\n"
MF "v[108:111],a[220:223],a[184:187],v[108:111]\n"
LD "v5,s[24:27],s84 offen offset:1024 lds\n"
MF "v[100:103],a[220:223],a[180:183],v[100:103]\n"
"s_waitcnt vmcnt(23) lgkmcnt(0)\n"
MF "v[100:103],a[216:219],a[140:143],v[100:103]\n"
"s_barrier\n"
MF "v[136:139],a[200:203],a[140:143],v[136:139]\n"
TR "a[216:217],v0 offset:32768\n"
MF "v[136:139],a[204:207],a[180:183],v[136:139]\n"
"s_add_u32 m0,s62,65536\n"
TR "a[218:219],v0 offset:33280\n"
MF "v[144:147],a[204:207],a[184:187],v[144:147]\n"
"s_add_u32 s82,s66,2359424\n"
"ds_read_b128 a[60:63],v2 offset:40960\n"
MF "v[144:147],a[200:203],a[144:147],v[144:147]\n"
"s_sub_u32 s15,s82,7864320\n"
TR "a[220:221],v0 offset:36864\n"
MF "v[152:155],a[200:203],a[148:151],v[152:155]\n"
"s_min_u32 s82,s82,s15\n"
TR "a[222:223],v0 offset:37376\n"
MF "v[152:155],a[204:207],a[188:191],v[152:155]\n"
LD "v6,s[28:31],s82 offen lds\n"
MF "v[160:163],a[204:207],a[192:195],v[160:163]\n"
"ds_read_b128 a[100:103],v3 offset:40960\n"
MF "v[160:163],a[200:203],a[152:155],v[160:163]\n"
LD "v11,s[28:31],s82 offen offset:1024 lds\n"
MF "v[168:171],a[200:203],a[156:159],v[168:171]\n"
MF "v[168:171],a[204:207],a[196:199],v[168:171]\n"
LD "v12,s[28:31],s82 offen offset:2048 lds\n"
MF "v[172:175],a[212:215],a[196:199],v[172:175]\n"
TR "a[200:201],v0 offset:32776\n"
MF "v[172:175],a[208:211],a[156:159],v[172:175]\n"
TR "a[202:203],v0 offset:33288\n"
MF "v[164:167],a[208:211],a[152:155],v[164:167]\n"
"ds_read_b128 a[64:67],v2 offset:43008\n"
MF "v[164:167],a[212:215],a[192:195],v[164:167]\n"
TR "a[204:205],v0 offset:36872\n"
MF "v[156:159],a[212:215],a[188:191],v[156:159]\n"
TR "a[206:207],v0 offset:37384\n"
MF "v[156:159],a[208:211],a[148:151],v[156:159]\n"
MF "v[148:151],a[208:211],a[144:147],v[148:151]\n"
"ds_read_b128 a[104:107],v3 offset:43008\n"
MF "v[148:151],a[212:215],a[184:187],v[148:151]\n"
MF "v[140:143],a[212:215],a[180:183],v[140:143]\n"
LD "v13,s[28:31],s82 offen offset:3072 lds\n"
MF "v[140:143],a[208:211],a[140:143],v[140:143]\n"
"s_waitcnt lgkmcnt(7)\n"
MF "v[176:179],a[216:219],a[140:143],v[176:179]\n"
TR "a[208:209],v1 offset:32768\n"
MF "v[176:179],a[220:223],a[180:183],v[176:179]\n"
TR "a[210:211],v1 offset:33280\n"
MF "v[184:187],a[220:223],a[184:187],v[184:187]\n"
"ds_read_b128 a[68:71],v2 offset:45056\n"
MF "v[184:187],a[216:219],a[144:147],v[184:187]\n"
"s_add_u32 s84,s14,20480\n"
TR "a[212:213],v1 offset:36864\n"
MF "v[192:195],a[216:219],a[148:151],v[192:195]\n"
"s_add_u32 m0,s63,22528\n"
TR "a[214:215],v1 offset:37376\n"
MF "v[192:195],a[220:223],a[188:191],v[192:195]\n"
LD "v4,s[24:27],s84 offen lds\n"
MF "v[200:203],a[220:223],a[192:195],v[200:203]\n"
"ds_read_b128 a[108:111],v3 offset:45056\n"
MF "v[200:203],a[216:219],a[152:155],v[200:203]\n"
MF "v[208:211],a[216:219],a[156:159],v[208:211]\n"
"s_add_u32 s84,s14,29696\n"
MF "v[208:211],a[220:223],a[196:199],v[208:211]\n"
"s_waitcnt lgkmcnt(7)\n"
LD "v5,s[24:27],s84 offen offset:1024 lds\n"
MF "v[212:215],a[204:207],a[196:199],v[212:215]\n"
TR "a[216:217],v1 offset:32776\n"
MF "v[212:215],a[200:203],a[156:159],v[212:215]\n"
TR "a[218:219],v1 offset:33288\n"
MF "v[204:207],a[200:203],a[152:155],v[204:207]\n"
"ds_read_b128 a[72:75],v2 offset:47104\n"
MF "v[204:207],a[204:207],a[192:195],v[204:207]\n"
TR "a[220:221],v1 offset:36872\n"
MF "v[196:199],a[204:207],a[188:191],v[196:199]\n"
TR "a[222:223],v1 offset:37384\n"
MF "v[196:199],a[200:203],a[148:151],v[196:199]\n"
MF "v[188:191],a[200:203],a[144:147],v[188:191]\n"
"v_add_u32 v0,0xffff4000,v0\n"
MF "v[188:191],a[204:207],a[184:187],v[188:191]\n"
"v_add_u32 v1,0xffff4000,v1\n"
MF "v[180:183],a[204:207],a[180:183],v[180:183]\n"
"s_waitcnt vmcnt(25) lgkmcnt(0)\n"
MF "v[180:183],a[200:203],a[140:143],v[180:183]\n"
"s_barrier\n"
MF "v[216:219],a[208:211],a[140:143],v[216:219]\n"
TR "a[200:201],v0\n"
MF "v[216:219],a[212:215],a[180:183],v[216:219]\n"
"s_add_u32 m0,s62,81920\n"
TR "a[202:203],v0 offset:512\n"
MF "v[224:227],a[212:215],a[184:187],v[224:227]\n"
"s_add_u32 s82,s66,2359552\n"
"ds_read_b128 a[112:115],v3 offset:47104\n"
MF "v[224:227],a[208:211],a[144:147],v[224:227]\n"
"s_sub_u32 s15,s82,7864320\n"
TR "a[204:205],v0 offset:4096\n"
MF "v[232:235],a[208:211],a[148:151],v[232:235]\n"
"s_min_u32 s82,s82,s15\n"
TR "a[206:207],v0 offset:4608\n"
MF "v[232:235],a[212:215],a[188:191],v[232:235]\n"
LD "v6,s[28:31],s82 offen lds\n"
MF "v[240:243],a[212:215],a[192:195],v[240:243]\n"
"ds_read_b128 a[76:79],v2 offset:49152\n"
MF "v[240:243],a[208:211],a[152:155],v[240:243]\n"
"ds_read_b128 a[116:119],v3 offset:49152\n"
MF "v[248:251],a[208:211],a[156:159],v[248:251]\n"
LD "v11,s[28:31],s82 offen offset:1024 lds\n"
MF "v[248:251],a[212:215],a[196:199],v[248:251]\n"
MF "v[252:255],a[220:223],a[196:199],v[252:255]\n"
TR "a[208:209],v0 offset:8\n"
MF "v[252:255],a[216:219],a[156:159],v[252:255]\n"
TR "a[210:211],v0 offset:520\n"
MF "v[244:247],a[216:219],a[152:155],v[244:247]\n"
MF "v[244:247],a[220:223],a[192:195],v[244:247]\n"
TR "a[212:213],v0 offset:4104\n"
MF "v[236:239],a[220:223],a[188:191],v[236:239]\n"
TR "a[214:215],v0 offset:4616\n"
MF "v[236:239],a[216:219],a[148:151],v[236:239]\n"
LD "v12,s[28:31],s82 offen offset:2048 lds\n"
MF "v[228:231],a[216:219],a[144:147],v[228:231]\n"
MF "v[228:231],a[220:223],a[184:187],v[228:231]\n"
MF "v[220:223],a[220:223],a[180:183],v[220:223]\n"
MF "v[220:223],a[216:219],a[140:143],v[220:223]\n"
"s_waitcnt lgkmcnt(4)\n"
MF "v[16:19],a[200:203],a[60:63],v[16:19]\n"
TR "a[216:217],v1\n"
MF "v[16:19],a[204:207],a[100:103],v[16:19]\n"
TR "a[218:219],v1 offset:512\n"
MF "v[24:27],a[204:207],a[104:107],v[24:27]\n"
LD "v13,s[28:31],s82 offen offset:3072 lds\n"
MF "v[24:27],a[200:203],a[64:67],v[24:27]\n"
TR "a[220:221],v1 offset:4096\n"
MF "v[32:35],a[200:203],a[68:71],v[32:35]\n"
TR "a[222:223],v1 offset:4608\n"
MF "v[32:35],a[204:207],a[108:111],v[32:35]\n"
MF "v[40:43],a[204:207],a[112:115],v[40:43]\n"
MF "v[40:43],a[200:203],a[72:75],v[40:43]\n"
"s_add_u32 s84,s14,40960\n"
MF "v[48:51],a[200:203],a[76:79],v[48:51]\n"
"s_add_u32 m0,s63,24576\n"
MF "v[48:51],a[204:207],a[116:119],v[48:51]\n"
"s_waitcnt lgkmcnt(4)\n"
LD "v4,s[24:27],s84 offen lds\n"
MF "v[52:55],a[212:215],a[116:119],v[52:55]\n"
TR "a[200:201],v1 offset:8\n"
MF "v[52:55],a[208:211],a[76:79],v[52:55]\n"
TR "a[202:203],v1 offset:520\n"
MF "v[44:47],a[208:211],a[72:75],v[44:47]\n"
MF "v[44:47],a[212:215],a[112:115],v[44:47]\n"
TR "a[204:205],v1 offset:4104\n"
MF "v[36:39],a[212:215],a[108:111],v[36:39]\n"
TR "a[206:207],v1 offset:4616\n"
MF "v[36:39],a[208:211],a[68:71],v[36:39]\n"
MF "v[28:31],a[208:211],a[64:67],v[28:31]\n"
MF "v[28:31],a[212:215],a[104:107],v[28:31]\n"
MF "v[20:23],a[212:215],a[100:103],v[20:23]\n"
"s_waitcnt vmcnt(24) lgkmcnt(0)\n"
MF "v[20:23],a[208:211],a[60:63],v[20:23]\n"
"s_barrier\n"
MF "v[56:59],a[216:219],a[60:63],v[56:59]\n"
"s_add_u32 m0,s62,0\n"
"s_add_u32 s82,s66,3145728\n"
TR "a[208:209],v0 offset:16384\n"
MF "v[56:59],a[220:223],a[100:103],v[56:59]\n"
"s_sub_u32 s15,s82,7864320\n"
"s_min_u32 s82,s82,s15\n"
TR "a[210:211],v0 offset:16896\n"
MF "v[64:67],a[220:223],a[104:107],v[64:67]\n"
LD "v6,s[28:31],s82 offen lds\n"
MF "v[64:67],a[216:219],a[64:67],v[64:67]\n"
TR "a[212:213],v0 offset:20480\n"
MF "v[72:75],a[216:219],a[68:71],v[72:75]\n"
TR "a[214:215],v0 offset:20992\n"
MF "v[72:75],a[220:223],a[108:111],v[72:75]\n"
MF "v[80:83],a[220:223],a[112:115],v[80:83]\n"
LD "v11,s[28:31],s82 offen offset:1024 lds\n"
MF "v[80:83],a[216:219],a[72:75],v[80:83]\n"
MF "v[88:91],a[216:219],a[76:79],v[88:91]\n"
MF "v[88:91],a[220:223],a[116:119],v[88:91]\n"
MF "v[92:95],a[204:207],a[116:119],v[92:95]\n"
TR "a[216:217],v0 offset:16392\n"
MF "v[92:95],a[200:203],a[76:79],v[92:95]\n"
TR "a[218:219],v0 offset:16904\n"
MF "v[84:87],a[200:203],a[72:75],v[84:87]\n"
LD "v12,s[28:31],s82 offen offset:2048 lds\n"
MF "v[84:87],a[204:207],a[112:115],v[84:87]\n"
TR "a[220:221],v0 offset:20488\n"
MF "v[76:79],a[204:207],a[108:111],v[76:79]\n"
TR "a[222:223],v0 offset:21000\n"
MF "v[76:79],a[200:203],a[68:71],v[76:79]\n"
MF "v[68:71],a[200:203],a[64:67],v[68:71]\n"
MF "v[68:71],a[204:207],a[104:107],v[68:71]\n"
MF "v[60:63],a[204:207],a[100:103],v[60:63]\n"
LD "v13,s[28:31],s82 offen offset:3072 lds\n"
MF "v[60:63],a[200:203],a[60:63],v[60:63]\n"
"s_waitcnt lgkmcnt(4)\n"
MF "v[96:99],a[208:211],a[60:63],v[96:99]\n"
TR "a[200:201],v1 offset:16384\n"
MF "v[96:99],a[212:215],a[100:103],v[96:99]\n"
TR "a[202:203],v1 offset:16896\n"
MF "v[104:107],a[212:215],a[104:107],v[104:107]\n"
"s_add_u32 s14,s81,640\n"
"s_sub_u32 s15,s14,1280\n"
MF "v[104:107],a[208:211],a[64:67],v[104:107]\n"
"s_min_u32 s14,s14,s15\n"
"s_add_u32 s14,s14,s68\n"
TR "a[204:205],v1 offset:20480\n"
MF "v[112:115],a[208:211],a[68:71],v[112:115]\n"
"s_add_u32 s84,s14,0\n"
"s_add_u32 m0,s63,40960\n"
TR "a[206:207],v1 offset:20992\n"
MF "v[112:115],a[212:215],a[108:111],v[112:115]\n"
LD "v4,s[24:27],s84 offen lds\n"
MF "v[120:123],a[212:215],a[112:115],v[120:123]\n"
MF "v[120:123],a[208:211],a[72:75],v[120:123]\n"
MF "v[128:131],a[208:211],a[76:79],v[128:131]\n"
"s_add_u32 s84,s14,9216\n"
MF "v[128:131],a[212:215],a[116:119],v[128:131]\n"
"s_waitcnt lgkmcnt(4)\n"
LD "v5,s[24:27],s84 offen offset:1024 lds\n"
MF "v[132:135],a[220:223],a[116:119],v[132:135]\n"
TR "a[208:209],v1 offset:16392\n"
MF "v[132:135],a[216:219],a[76:79],v[132:135]\n"
TR "a[210:211],v1 offset:16904\n"
MF "v[124:127],a[216:219],a[72:75],v[124:127]\n"
MF "v[124:127],a[220:223],a[112:115],v[124:127]\n"
TR "a[212:213],v1 offset:20488\n"
MF "v[116:119],a[220:223],a[108:111],v[116:119]\n"
TR "a[214:215],v1 offset:21000\n"
MF "v[116:119],a[216:219],a[68:71],v[116:119]\n"
MF "v[108:111],a[216:219],a[64:67],v[108:111]\n"
MF "v[108:111],a[220:223],a[104:107],v[108:111]\n"
MF "v[100:103],a[220:223],a[100:103],v[100:103]\n"
"s_waitcnt vmcnt(23) lgkmcnt(0)\n"
MF "v[100:103],a[216:219],a[60:63],v[100:103]\n"
"s_barrier\n"
MF "v[136:139],a[200:203],a[60:63],v[136:139]\n"
TR "a[216:217],v0 offset:32768\n"
MF "v[136:139],a[204:207],a[100:103],v[136:139]\n"
"s_add_u32 m0,s62,16384\n"
TR "a[218:219],v0 offset:33280\n"
MF "v[144:147],a[204:207],a[104:107],v[144:147]\n"
"s_add_u32 s82,s66,3145856\n"
"ds_read_b128 a[140:143],v2\n"
MF "v[144:147],a[200:203],a[64:67],v[144:147]\n"
"s_sub_u32 s15,s82,7864320\n"
TR "a[220:221],v0 offset:36864\n"
MF "v[152:155],a[200:203],a[68:71],v[152:155]\n"
"s_min_u32 s82,s82,s15\n"
TR "a[222:223],v0 offset:37376\n"
MF "v[152:155],a[204:207],a[108:111],v[152:155]\n"
LD "v6,s[28:31],s82 offen lds\n"
MF "v[160:163],a[204:207],a[112:115],v[160:163]\n"
"ds_read_b128 a[180:183],v3\n"
MF "v[160:163],a[200:203],a[72:75],v[160:163]\n"
LD "v11,s[28:31],s82 offen offset:1024 lds\n"
MF "v[168:171],a[200:203],a[76:79],v[168:171]\n"
MF "v[168:171],a[204:207],a[116:119],v[168:171]\n"
LD "v12,s[28:31],s82 offen offset:2048 lds\n"
MF "v[172:175],a[212:215],a[116:119],v[172:175]\n"
TR "a[200:201],v0 offset:32776\n"
MF "v[172:175],a[208:211],a[76:79],v[172:175]\n"
TR "a[202:203],v0 offset:33288\n"
MF "v[164:167],a[208:211],a[72:75],v[164:167]\n"
"ds_read_b128 a[144:147],v2 offset:2048\n"
MF "v[164:167],a[212:215],a[112:115],v[164:167]\n"
TR "a[204:205],v0 offset:36872\n"
MF "v[156:159],a[212:215],a[108:111],v[156:159]\n"
TR "a[206:207],v0 offset:37384\n"
MF "v[156:159],a[208:211],a[68:71],v[156:159]\n"
MF "v[148:151],a[208:211],a[64:67],v[148:151]\n"
"ds_read_b128 a[184:187],v3 offset:2048\n"
MF "v[148:151],a[212:215],a[104:107],v[148:151]\n"
MF "v[140:143],a[212:215],a[100:103],v[140:143]\n"
LD "v13,s[28:31],s82 offen offset:3072 lds\n"
MF "v[140:143],a[208:211],a[60:63],v[140:143]\n"
"s_waitcnt lgkmcnt(7)\n"
MF "v[176:179],a[216:219],a[60:63],v[176:179]\n"
TR "a[208:209],v1 offset:32768\n"
MF "v[176:179],a[220:223],a[100:103],v[176:179]\n"
TR "a[210:211],v1 offset:33280\n"
MF "v[184:187],a[220:223],a[104:107],v[184:187]\n"
"ds_read_b128 a[148:151],v2 offset:4096\n"
MF "v[184:187],a[216:219],a[64:67],v[184:187]\n"
"s_add_u32 s84,s14,20480\n"
TR "a[212:213],v1 offset:36864\n"
MF "v[192:195],a[216:219],a[68:71],v[192:195]\n"
"s_add_u32 m0,s63,43008\n"
TR "a[214:215],v1 offset:37376\n"
MF "v[192:195],a[220:223],a[108:111],v[192:195]\n"
LD "v4,s[24:27],s84 offen lds\n"
MF "v[200:203],a[220:223],a[112:115],v[200:203]\n"
"ds_read_b128 a[188:191],v3 offset:4096\n"
MF "v[200:203],a[216:219],a[72:75],v[200:203]\n"
MF "v[208:211],a[216:219],a[76:79],v[208:211]\n"
"s_add_u32 s84,s14,29696\n"
MF "v[208:211],a[220:223],a[116:119],v[208:211]\n"
"s_waitcnt lgkmcnt(7)\n"
LD "v5,s[24:27],s84 offen offset:1024 lds\n"
MF "v[212:215],a[204:207],a[116:119],v[212:215]\n"
TR "a[216:217],v1 offset:32776\n"
MF "v[212:215],a[200:203],a[76:79],v[212:215]\n"
TR "a[218:219],v1 offset:33288\n"
MF "v[204:207],a[200:203],a[72:75],v[204:207]\n"
"ds_read_b128 a[152:155],v2 offset:6144\n"
MF "v[204:207],a[204:207],a[112:115],v[204:207]\n"
TR "a[220:221],v1 offset:36872\n"
MF "v[196:199],a[204:207],a[108:111],v[196:199]\n"
TR "a[222:223],v1 offset:37384\n"
MF "v[196:199],a[200:203],a[68:71],v[196:199]\n"
MF "v[188:191],a[200:203],a[64:67],v[188:191]\n"
"v_add_u32 v0,0xc000,v0\n"
MF "v[188:191],a[204:207],a[104:107],v[188:191]\n"
"v_add_u32 v1,0xc000,v1\n"
MF "v[180:183],a[204:207],a[100:103],v[180:183]\n"
"s_waitcnt vmcnt(25) lgkmcnt(0)\n"
MF "v[180:183],a[200:203],a[60:63],v[180:183]\n"
"s_barrier\n"
MF "v[216:219],a[208:211],a[60:63],v[216:219]\n"
TR "a[200:201],v0\n"
MF "v[216:219],a[212:215],a[100:103],v[216:219]\n"
"s_add_u32 m0,s62,32768\n"
TR "a[202:203],v0 offset:512\n"
MF "v[224:227],a[212:215],a[104:107],v[224:227]\n"
".Lpg5c:\n"
"s_cbranch_execz .Lpg15c\n"
".Lpg5f:\n"
"s_cbranch_execz .LpgattB_f\n"
"s_add_u32 s82,s66,3145984\n"
"ds_read_b128 a[192:195],v3 offset:6144\n"
MF "v[224:227],a[208:211],a[64:67],v[224:227]\n"
"s_sub_u32 s15,s82,7864320\n"
TR "a[204:205],v0 offset:4096\n"
MF "v[232:235],a[208:211],a[68:71],v[232:235]\n"
"s_min_u32 s82,s82,s15\n"
TR "a[206:207],v0 offset:4608\n"
MF "v[232:235],a[212:215],a[108:111],v[232:235]\n"
LD "v6,s[28:31],s82 offen lds\n"
MF "v[240:243],a[212:215],a[112:115],v[240:243]\n"
"ds_read_b128 a[156:159],v2 offset:8192\n"
MF "v[240:243],a[208:211],a[72:75],v[240:243]\n"
"ds_read_b128 a[196:199],v3 offset:8192\n"
MF "v[248:251],a[208:211],a[76:79],v[248:251]\n"
LD "v11,s[28:31],s82 offen offset:1024 lds\n"
MF "v[248:251],a[212:215],a[116:119],v[248:251]\n"
MF "v[252:255],a[220:223],a[116:119],v[252:255]\n"
TR "a[208:209],v0 offset:8\n"
MF "v[252:255],a[216:219],a[76:79],v[252:255]\n"
TR "a[210:211],v0 offset:520\n"
MF "v[244:247],a[216:219],a[72:75],v[244:247]\n"
MF "v[244:247],a[220:223],a[112:115],v[244:247]\n"
TR "a[212:213],v0 offset:4104\n"
MF "v[236:239],a[220:223],a[108:111],v[236:239]\n"
TR "a[214:215],v0 offset:4616\n"
MF "v[236:239],a[216:219],a[68:71],v[236:239]\n"
LD "v12,s[28:31],s82 offen offset:2048 lds\n"
MF "v[228:231],a[216:219],a[64:67],v[228:231]\n"
MF "v[228:231],a[220:223],a[104:107],v[228:231]\n"
MF "v[220:223],a[220:223],a[100:103],v[220:223]\n"
MF "v[220:223],a[216:219],a[60:63],v[220:223]\n"
"s_waitcnt lgkmcnt(4)\n"
MF "v[16:19],a[200:203],a[140:143],v[16:19]\n"
TR "a[216:217],v1\n"
MF "v[16:19],a[204:207],a[180:183],v[16:19]\n"
TR "a[218:219],v1 offset:512\n"
MF "v[24:27],a[204:207],a[184:187],v[24:27]\n"
LD "v13,s[28:31],s82 offen offset:3072 lds\n"
MF "v[24:27],a[200:203],a[144:147],v[24:27]\n"
TR "a[220:221],v1 offset:4096\n"
MF "v[32:35],a[200:203],a[148:151],v[32:35]\n"
TR "a[222:223],v1 offset:4608\n"
MF "v[32:35],a[204:207],a[188:191],v[32:35]\n"
MF "v[40:43],a[204:207],a[192:195],v[40:43]\n"
MF "v[40:43],a[200:203],a[152:155],v[40:43]\n"
"s_add_u32 s84,s14,40960\n"
MF "v[48:51],a[200:203],a[156:159],v[48:51]\n"
"s_add_u32 m0,s63,45056\n"
MF "v[48:51],a[204:207],a[196:199],v[48:51]\n"
"s_waitcnt lgkmcnt(4)\n"
LD "v4,s[24:27],s84 offen lds\n"
MF "v[52:55],a[212:215],a[196:199],v[52:55]\n"
TR "a[200:201],v1 offset:8\n"
MF "v[52:55],a[208:211],a[156:159],v[52:55]\n"
TR "a[202:203],v1 offset:520\n"
MF "v[44:47],a[208:211],a[152:155],v[44:47]\n"
MF "v[44:47],a[212:215],a[192:195],v[44:47]\n"
TR "a[204:205],v1 offset:4104\n"
MF "v[36:39],a[212:215],a[188:191],v[36:39]\n"
TR "a[206:207],v1 offset:4616\n"
MF "v[36:39],a[208:211],a[148:151],v[36:39]\n"
MF "v[28:31],a[208:211],a[144:147],v[28:31]\n"
MF "v[28:31],a[212:215],a[184:187],v[28:31]\n"
MF "v[20:23],a[212:215],a[180:183],v[20:23]\n"
"s_waitcnt vmcnt(24) lgkmcnt(0)\n"
MF "v[20:23],a[208:211],a[140:143],v[20:23]\n"
"s_barrier\n"
MF "v[56:59],a[216:219],a[140:143],v[56:59]\n"
"s_add_u32 m0,s62,49152\n"
"s_add_u32 s82,s66,3932160\n"
TR "a[208:209],v0 offset:16384\n"
MF "v[56:59],a[220:223],a[180:183],v[56:59]\n"
"s_sub_u32 s15,s82,7864320\n"
"s_min_u32 s82,s82,s15\n"
TR "a[210:211],v0 offset:16896\n"
MF "v[64:67],a[220:223],a[184:187],v[64:67]\n"
LD "v6,s[28:31],s82 offen lds\n"
MF "v[64:67],a[216:219],a[144:147],v[64:67]\n"
TR "a[212:213],v0 offset:20480\n"
MF "v[72:75],a[216:219],a[148:151],v[72:75]\n"
TR "a[214:215],v0 offset:20992\n"
MF "v[72:75],a[220:223],a[188:191],v[72:75]\n"
MF "v[80:83],a[220:223],a[192:195],v[80:83]\n"
LD "v11,s[28:31],s82 offen offset:1024 lds\n"
MF "v[80:83],a[216:219],a[152:155],v[80:83]\n"
MF "v[88:91],a[216:219],a[156:159],v[88:91]\n"
MF "v[88:91],a[220:223],a[196:199],v[88:91]\n"
MF "v[92:95],a[204:207],a[196:199],v[92:95]\n"
TR "a[216:217],v0 offset:16392\n"
MF "v[92:95],a[200:203],a[156:159],v[92:95]\n"
TR "a[218:219],v0 offset:16904\n"
MF "v[84:87],a[200:203],a[152:155],v[84:87]\n"
LD "v12,s[28:31],s82 offen offset:2048 lds\n"
MF "v[84:87],a[204:207],a[192:195],v[84:87]\n"
TR "a[220:221],v0 offset:20488\n"
MF "v[76:79],a[204:207],a[188:191],v[76:79]\n"
TR "a[222:223],v0 offset:21000\n"
MF "v[76:79],a[200:203],a[148:151],v[76:79]\n"
MF "v[68:71],a[200:203],a[144:147],v[68:71]\n"
MF "v[68:71],a[204:207],a[184:187],v[68:71]\n"
MF "v[60:63],a[204:207],a[180:183],v[60:63]\n"
LD "v13,s[28:31],s82 offen offset:3072 lds\n"
MF "v[60:63],a[200:203],a[140:143],v[60:63]\n"
"s_waitcnt lgkmcnt(4)\n"
MF "v[96:99],a[208:211],a[140:143],v[96:99]\n"
TR "a[200:201],v1 offset:16384\n"
MF "v[96:99],a[212:215],a[180:183],v[96:99]\n"
TR "a[202:203],v1 offset:16896\n"
MF "v[104:107],a[212:215],a[184:187],v[104:107]\n"
"s_add_u32 s14,s81,768\n"
"s_sub_u32 s15,s14,1280\n"
MF "v[104:107],a[208:211],a[144:147],v[104:107]\n"
"s_min_u32 s14,s14,s15\n"
"s_add_u32 s14,s14,s68\n"
TR "a[204:205],v1 offset:20480\n"
MF "v[112:115],a[208:211],a[148:151],v[112:115]\n"
"s_add_u32 s84,s14,0\n"
"s_add_u32 m0,s63,0\n"
TR "a[206:207],v1 offset:20992\n"
MF "v[112:115],a[212:215],a[188:191],v[112:115]\n"
LD "v4,s[24:27],s84 offen lds\n"
MF "v[120:123],a[212:215],a[192:195],v[120:123]\n"
MF "v[120:123],a[208:211],a[152:155],v[120:123]\n"
MF "v[128:131],a[208:211],a[156:159],v[128:131]\n"
"s_add_u32 s84,s14,9216\n"
MF "v[128:131],a[212:215],a[196:199],v[128:131]\n"
"s_waitcnt lgkmcnt(4)\n"
LD "v5,s[24:27],s84 offen offset:1024 lds\n"
MF "v[132:135],a[220:223],a[196:199],v[132:135]\n"
TR "a[208:209],v1 offset:16392\n"
MF "v[132:135],a[216:219],a[156:159],v[132:135]\n"
TR "a[210:211],v1 offset:16904\n"
MF "v[124:127],a[216:219],a[152:155],v[124:127]\n"
MF "v[124:127],a[220:223],a[192:195],v[124:127]\n"
TR "a[212:213],v1 offset:20488\n"
MF "v[116:119],a[220:223],a[188:191],v[116:119]\n"
TR "a[214:215],v1 offset:21000\n"
MF "v[116:119],a[216:219],a[148:151],v[116:119]\n"
MF "v[108:111],a[216:219],a[144:147],v[108:111]\n"
MF "v[108:111],a[220:223],a[184:187],v[108:111]\n"
MF "v[100:103],a[220:223],a[180:183],v[100:103]\n"
"s_waitcnt vmcnt(23) lgkmcnt(0)\n"
MF "v[100:103],a[216:219],a[140:143],v[100:103]\n"
"s_barrier\n"
MF "v[136:139],a[200:203],a[140:143],v[136:139]\n"
TR "a[216:217],v0 offset:32768\n"
MF "v[136:139],a[204:207],a[180:183],v[136:139]\n"
"s_add_u32 m0,s62,65536\n"
TR "a[218:219],v0 offset:33280\n"
MF "v[144:147],a[204:207],a[184:187],v[144:147]\n"
"s_add_u32 s82,s66,3932288\n"
"ds_read_b128 a[60:63],v2 offset:20480\n"
MF "v[144:147],a[200:203],a[144:147],v[144:147]\n"
"s_sub_u32 s15,s82,7864320\n"
TR "a[220:221],v0 offset:36864\n"
MF "v[152:155],a[200:203],a[148:151],v[152:155]\n"
"s_min_u32 s82,s82,s15\n"
TR "a[222:223],v0 offset:37376\n"
MF "v[152:155],a[204:207],a[188:191],v[152:155]\n"
LD "v6,s[28:31],s82 offen lds\n"
MF "v[160:163],a[204:207],a[192:195],v[160:163]\n"
"ds_read_b128 a[100:103],v3 offset:20480\n"
MF "v[160:163],a[200:203],a[152:155],v[160:163]\n"
LD "v11,s[28:31],s82 offen offset:1024 lds\n"
MF "v[168:171],a[200:203],a[156:159],v[168:171]\n"
MF "v[168:171],a[204:207],a[196:199],v[168:171]\n"
LD "v12,s[28:31],s82 offen offset:2048 lds\n"
MF "v[172:175],a[212:215],a[196:199],v[172:175]\n"
TR "a[200:201],v0 offset:32776\n"
MF "v[172:175],a[208:211],a[156:159],v[172:175]\n"
TR "a[202:203],v0 offset:33288\n"
MF "v[164:167],a[208:211],a[152:155],v[164:167]\n"
"ds_read_b128 a[64:67],v2 offset:22528\n"
MF "v[164:167],a[212:215],a[192:195],v[164:167]\n"
TR "a[204:205],v0 offset:36872\n"
MF "v[156:159],a[212:215],a[188:191],v[156:159]\n"
TR "a[206:207],v0 offset:37384\n"
MF "v[156:159],a[208:211],a[148:151],v[156:159]\n"
MF "v[148:151],a[208:211],a[144:147],v[148:151]\n"
"ds_read_b128 a[104:107],v3 offset:22528\n"
MF "v[148:151],a[212:215],a[184:187],v[148:151]\n"
MF "v[140:143],a[212:215],a[180:183],v[140:143]\n"
LD "v13,s[28:31],s82 offen offset:3072 lds\n"
MF "v[140:143],a[208:211],a[140:143],v[140:143]\n"
"s_waitcnt lgkmcnt(7)\n"
MF "v[176:179],a[216:219],a[140:143],v[176:179]\n"
TR "a[208:209],v1 offset:32768\n"
MF "v[176:179],a[220:223],a[180:183],v[176:179]\n"
TR "a[210:211],v1 offset:33280\n"
MF "v[184:187],a[220:223],a[184:187],v[184:187]\n"
"ds_read_b128 a[68:71],v2 offset:24576\n"
MF "v[184:187],a[216:219],a[144:147],v[184:187]\n"
"s_add_u32 s84,s14,20480\n"
TR "a[212:213],v1 offset:36864\n"
MF "v[192:195],a[216:219],a[148:151],v[192:195]\n"
"s_add_u32 m0,s63,2048\n"
TR "a[214:215],v1 offset:37376\n"
MF "v[192:195],a[220:223],a[188:191],v[192:195]\n"
LD "v4,s[24:27],s84 offen lds\n"
MF "v[200:203],a[220:223],a[192:195],v[200:203]\n"
"ds_read_b128 a[108:111],v3 offset:24576\n"
MF "v[200:203],a[216:219],a[152:155],v[200:203]\n"
MF "v[208:211],a[216:219],a[156:159],v[208:211]\n"
"s_add_u32 s84,s14,29696\n"
MF "v[208:211],a[220:223],a[196:199],v[208:211]\n"
"s_waitcnt lgkmcnt(7)\n"
LD "v5,s[24:27],s84 offen offset:1024 lds\n"
MF "v[212:215],a[204:207],a[196:199],v[212:215]\n"
TR "a[216:217],v1 offset:32776\n"
MF "v[212:215],a[200:203],a[156:159],v[212:215]\n"
TR "a[218:219],v1 offset:33288\n"
MF "v[204:207],a[200:203],a[152:155],v[204:207]\n"
"ds_read_b128 a[72:75],v2 offset:26624\n"
MF "v[204:207],a[204:207],a[192:195],v[204:207]\n"
TR "a[220:221],v1 offset:36872\n"
MF "v[196:199],a[204:207],a[188:191],v[196:199]\n"
TR "a[222:223],v1 offset:37384\n"
MF "v[196:199],a[200:203],a[148:151],v[196:199]\n"
MF "v[188:191],a[200:203],a[144:147],v[188:191]\n"
"v_add_u32 v0,0xffff4000,v0\n"
MF "v[188:191],a[204:207],a[184:187],v[188:191]\n"
"v_add_u32 v1,0xffff4000,v1\n"
MF "v[180:183],a[204:207],a[180:183],v[180:183]\n"
"s_waitcnt vmcnt(25) lgkmcnt(0)\n"
MF "v[180:183],a[200:203],a[140:143],v[180:183]\n"
"s_barrier\n"
MF "v[216:219],a[208:211],a[140:143],v[216:219]\n"
TR "a[200:201],v0\n"
MF "v[216:219],a[212:215],a[180:183],v[216:219]\n"
"s_add_u32 m0,s62,81920\n"
TR "a[202:203],v0 offset:512\n"
MF "v[224:227],a[212:215],a[184:187],v[224:227]\n"
"s_add_u32 s82,s66,3932416\n"
"ds_read_b128 a[112:115],v3 offset:26624\n"
MF "v[224:227],a[208:211],a[144:147],v[224:227]\n"
"s_sub_u32 s15,s82,7864320\n"
TR "a[204:205],v0 offset:4096\n"
MF "v[232:235],a[208:211],a[148:151],v[232:235]\n"
"s_min_u32 s82,s82,s15\n"
TR "a[206:207],v0 offset:4608\n"
MF "v[232:235],a[212:215],a[188:191],v[232:235]\n"
LD "v6,s[28:31],s82 offen lds\n"
MF "v[240:243],a[212:215],a[192:195],v[240:243]\n"
"ds_read_b128 a[76:79],v2 offset:28672\n"
MF "v[240:243],a[208:211],a[152:155],v[240:243]\n"
"ds_read_b128 a[116:119],v3 offset:28672\n"
MF "v[248:251],a[208:211],a[156:159],v[248:251]\n"
LD "v11,s[28:31],s82 offen offset:1024 lds\n"
MF "v[248:251],a[212:215],a[196:199],v[248:251]\n"
MF "v[252:255],a[220:223],a[196:199],v[252:255]\n"
TR "a[208:209],v0 offset:8\n"
MF "v[252:255],a[216:219],a[156:159],v[252:255]\n"
TR "a[210:211],v0 offset:520\n"
MF "v[244:247],a[216:219],a[152:155],v[244:247]\n"
MF "v[244:247],a[220:223],a[192:195],v[244:247]\n"
TR "a[212:213],v0 offset:4104\n"
MF "v[236:239],a[220:223],a[188:191],v[236:239]\n"
TR "a[214:215],v0 offset:4616\n"
MF "v[236:239],a[216:219],a[148:151],v[236:239]\n"
LD "v12,s[28:31],s82 offen offset:2048 lds\n"
MF "v[228:231],a[216:219],a[144:147],v[228:231]\n"
MF "v[228:231],a[220:223],a[184:187],v[228:231]\n"
MF "v[220:223],a[220:223],a[180:183],v[220:223]\n"
MF "v[220:223],a[216:219],a[140:143],v[220:223]\n"
"s_waitcnt lgkmcnt(4)\n"
MF "v[16:19],a[200:203],a[60:63],v[16:19]\n"
TR "a[216:217],v1\n"
MF "v[16:19],a[204:207],a[100:103],v[16:19]\n"
TR "a[218:219],v1 offset:512\n"
MF "v[24:27],a[204:207],a[104:107],v[24:27]\n"
LD "v13,s[28:31],s82 offen offset:3072 lds\n"
MF "v[24:27],a[200:203],a[64:67],v[24:27]\n"
TR "a[220:221],v1 offset:4096\n"
MF "v[32:35],a[200:203],a[68:71],v[32:35]\n"
TR "a[222:223],v1 offset:4608\n"
MF "v[32:35],a[204:207],a[108:111],v[32:35]\n"
MF "v[40:43],a[204:207],a[112:115],v[40:43]\n"
MF "v[40:43],a[200:203],a[72:75],v[40:43]\n"
"s_add_u32 s84,s14,40960\n"
MF "v[48:51],a[200:203],a[76:79],v[48:51]\n"
"s_add_u32 m0,s63,4096\n"
MF "v[48:51],a[204:207],a[116:119],v[48:51]\n"
"s_waitcnt lgkmcnt(4)\n"
LD "v4,s[24:27],s84 offen lds\n"
MF "v[52:55],a[212:215],a[116:119],v[52:55]\n"
TR "a[200:201],v1 offset:8\n"
MF "v[52:55],a[208:211],a[76:79],v[52:55]\n"
TR "a[202:203],v1 offset:520\n"
MF "v[44:47],a[208:211],a[72:75],v[44:47]\n"
MF "v[44:47],a[212:215],a[112:115],v[44:47]\n"
TR "a[204:205],v1 offset:4104\n"
MF "v[36:39],a[212:215],a[108:111],v[36:39]\n"
TR "a[206:207],v1 offset:4616\n"
MF "v[36:39],a[208:211],a[68:71],v[36:39]\n"
MF "v[28:31],a[208:211],a[64:67],v[28:31]\n"
MF "v[28:31],a[212:215],a[104:107],v[28:31]\n"
MF "v[20:23],a[212:215],a[100:103],v[20:23]\n"
"s_waitcnt vmcnt(24) lgkmcnt(0)\n"
MF "v[20:23],a[208:211],a[60:63],v[20:23]\n"
"s_barrier\n"
MF "v[56:59],a[216:219],a[60:63],v[56:59]\n"
"s_add_u32 m0,s62,0\n"
"s_add_u32 s82,s66,4718592\n"
TR "a[208:209],v0 offset:16384\n"
MF "v[56:59],a[220:223],a[100:103],v[56:59]\n"
"s_sub_u32 s15,s82,7864320\n"
"s_min_u32 s82,s82,s15\n"
TR "a[210:211],v0 offset:16896\n"
MF "v[64:67],a[220:223],a[104:107],v[64:67]\n"
LD "v6,s[28:31],s82 offen lds\n"
MF "v[64:67],a[216:219],a[64:67],v[64:67]\n"
TR "a[212:213],v0 offset:20480\n"
MF "v[72:75],a[216:219],a[68:71],v[72:75]\n"
TR "a[214:215],v0 offset:20992\n"
MF "v[72:75],a[220:223],a[108:111],v[72:75]\n"
MF "v[80:83],a[220:223],a[112:115],v[80:83]\n"
LD "v11,s[28:31],s82 offen offset:1024 lds\n"
MF "v[80:83],a[216:219],a[72:75],v[80:83]\n"
MF "v[88:91],a[216:219],a[76:79],v[88:91]\n"
MF "v[88:91],a[220:223],a[116:119],v[88:91]\n"
MF "v[92:95],a[204:207],a[116:119],v[92:95]\n"
TR "a[216:217],v0 offset:16392\n"
MF "v[92:95],a[200:203],a[76:79],v[92:95]\n"
TR "a[218:219],v0 offset:16904\n"
MF "v[84:87],a[200:203],a[72:75],v[84:87]\n"
LD "v12,s[28:31],s82 offen offset:2048 lds\n"
MF "v[84:87],a[204:207],a[112:115],v[84:87]\n"
TR "a[220:221],v0 offset:20488\n"
MF "v[76:79],a[204:207],a[108:111],v[76:79]\n"
TR "a[222:223],v0 offset:21000\n"
MF "v[76:79],a[200:203],a[68:71],v[76:79]\n"
MF "v[68:71],a[200:203],a[64:67],v[68:71]\n"
MF "v[68:71],a[204:207],a[104:107],v[68:71]\n"
MF "v[60:63],a[204:207],a[100:103],v[60:63]\n"
LD "v13,s[28:31],s82 offen offset:3072 lds\n"
MF "v[60:63],a[200:203],a[60:63],v[60:63]\n"
"s_waitcnt lgkmcnt(4)\n"
MF "v[96:99],a[208:211],a[60:63],v[96:99]\n"
TR "a[200:201],v1 offset:16384\n"
MF "v[96:99],a[212:215],a[100:103],v[96:99]\n"
TR "a[202:203],v1 offset:16896\n"
MF "v[104:107],a[212:215],a[104:107],v[104:107]\n"
"s_add_u32 s14,s81,896\n"
"s_sub_u32 s15,s14,1280\n"
MF "v[104:107],a[208:211],a[64:67],v[104:107]\n"
"s_min_u32 s14,s14,s15\n"
"s_add_u32 s14,s14,s68\n"
TR "a[204:205],v1 offset:20480\n"
MF "v[112:115],a[208:211],a[68:71],v[112:115]\n"
"s_add_u32 s84,s14,0\n"
"s_add_u32 m0,s63,20480\n"
TR "a[206:207],v1 offset:20992\n"
MF "v[112:115],a[212:215],a[108:111],v[112:115]\n"
LD "v4,s[24:27],s84 offen lds\n"
MF "v[120:123],a[212:215],a[112:115],v[120:123]\n"
MF "v[120:123],a[208:211],a[72:75],v[120:123]\n"
MF "v[128:131],a[208:211],a[76:79],v[128:131]\n"
"s_add_u32 s84,s14,9216\n"
MF "v[128:131],a[212:215],a[116:119],v[128:131]\n"
"s_waitcnt lgkmcnt(4)\n"
LD "v5,s[24:27],s84 offen offset:1024 lds\n"
MF "v[132:135],a[220:223],a[116:119],v[132:135]\n"
TR "a[208:209],v1 offset:16392\n"
MF "v[132:135],a[216:219],a[76:79],v[132:135]\n"
TR "a[210:211],v1 offset:16904\n"
MF "v[124:127],a[216:219],a[72:75],v[124:127]\n"
MF "v[124:127],a[220:223],a[112:115],v[124:127]\n"
TR "a[212:213],v1 offset:20488\n"
MF "v[116:119],a[220:223],a[108:111],v[116:119]\n"
TR "a[214:215],v1 offset:21000\n"
MF "v[116:119],a[216:219],a[68:71],v[116:119]\n"
MF "v[108:111],a[216:219],a[64:67],v[108:111]\n"
MF "v[108:111],a[220:223],a[104:107],v[108:111]\n"
MF "v[100:103],a[220:223],a[100:103],v[100:103]\n"
"s_waitcnt vmcnt(23) lgkmcnt(0)\n"
MF "v[100:103],a[216:219],a[60:63],v[100:103]\n"
"s_barrier\n"
MF "v[136:139],a[200:203],a[60:63],v[136:139]\n"
TR "a[216:217],v0 offset:32768\n"
MF "v[136:139],a[204:207],a[100:103],v[136:139]\n"
"s_add_u32 m0,s62,16384\n"
TR "a[218:219],v0 offset:33280\n"
MF "v[144:147],a[204:207],a[104:107],v[144:147]\n"
"s_add_u32 s82,s66,4718720\n"
"ds_read_b128 a[140:143],v2 offset:40960\n"
MF "v[144:147],a[200:203],a[64:67],v[144:147]\n"
"s_sub_u32 s15,s82,7864320\n"
TR "a[220:221],v0 offset:36864\n"
MF "v[152:155],a[200:203],a[68:71],v[152:155]\n"
"s_min_u32 s82,s82,s15\n"
TR "a[222:223],v0 offset:37376\n"
MF "v[152:155],a[204:207],a[108:111],v[152:155]\n"
LD "v6,s[28:31],s82 offen lds\n"
MF "v[160:163],a[204:207],a[112:115],v[160:163]\n"
"ds_read_b128 a[180:183],v3 offset:40960\n"
MF "v[160:163],a[200:203],a[72:75],v[160:163]\n"
LD "v11,s[28:31],s82 offen offset:1024 lds\n"
MF "v[168:171],a[200:203],a[76:79],v[168:171]\n"
MF "v[168:171],a[204:207],a[116:119],v[168:171]\n"
LD "v12,s[28:31],s82 offen offset:2048 lds\n"
MF "v[172:175],a[212:215],a[116:119],v[172:175]\n"
TR "a[200:201],v0 offset:32776\n"
MF "v[172:175],a[208:211],a[76:79],v[172:175]\n"
TR "a[202:203],v0 offset:33288\n"
MF "v[164:167],a[208:211],a[72:75],v[164:167]\n"
"ds_read_b128 a[144:147],v2 offset:43008\n"
MF "v[164:167],a[212:215],a[112:115],v[164:167]\n"
TR "a[204:205],v0 offset:36872\n"
MF "v[156:159],a[212:215],a[108:111],v[156:159]\n"
TR "a[206:207],v0 offset:37384\n"
MF "v[156:159],a[208:211],a[68:71],v[156:159]\n"
MF "v[148:151],a[208:211],a[64:67],v[148:151]\n"
"ds_read_b128 a[184:187],v3 offset:43008\n"
MF "v[148:151],a[212:215],a[104:107],v[148:151]\n"
MF "v[140:143],a[212:215],a[100:103],v[140:143]\n"
LD "v13,s[28:31],s82 offen offset:3072 lds\n"
MF "v[140:143],a[208:211],a[60:63],v[140:143]\n"
"s_waitcnt lgkmcnt(7)\n"
MF "v[176:179],a[216:219],a[60:63],v[176:179]\n"
TR "a[208:209],v1 offset:32768\n"
MF "v[176:179],a[220:223],a[100:103],v[176:179]\n"
TR "a[210:211],v1 offset:33280\n"
MF "v[184:187],a[220:223],a[104:107],v[184:187]\n"
"ds_read_b128 a[148:151],v2 offset:45056\n"
MF "v[184:187],a[216:219],a[64:67],v[184:187]\n"
"s_add_u32 s84,s14,20480\n"
TR "a[212:213],v1 offset:36864\n"
MF "v[192:195],a[216:219],a[68:71],v[192:195]\n"
"s_add_u32 m0,s63,22528\n"
TR "a[214:215],v1 offset:37376\n"
MF "v[192:195],a[220:223],a[108:111],v[192:195]\n"
LD "v4,s[24:27],s84 offen lds\n"
MF "v[200:203],a[220:223],a[112:115],v[200:203]\n"
"ds_read_b128 a[188:191],v3 offset:45056\n"
MF "v[200:203],a[216:219],a[72:75],v[200:203]\n"
MF "v[208:211],a[216:219],a[76:79],v[208:211]\n"
"s_add_u32 s84,s14,29696\n"
MF "v[208:211],a[220:223],a[116:119],v[208:211]\n"
"s_waitcnt lgkmcnt(7)\n"
LD "v5,s[24:27],s84 offen offset:1024 lds\n"
MF "v[212:215],a[204:207],a[116:119],v[212:215]\n"
TR "a[216:217],v1 offset:32776\n"
MF "v[212:215],a[200:203],a[76:79],v[212:215]\n"
TR "a[218:219],v1 offset:33288\n"
MF "v[204:207],a[200:203],a[72:75],v[204:207]\n"
"ds_read_b128 a[152:155],v2 offset:47104\n"
MF "v[204:207],a[204:207],a[112:115],v[204:207]\n"
TR "a[220:221],v1 offset:36872\n"
MF "v[196:199],a[204:207],a[108:111],v[196:199]\n"
TR "a[222:223],v1 offset:37384\n"
MF "v[196:199],a[200:203],a[68:71],v[196:199]\n"
MF "v[188:191],a[200:203],a[64:67],v[188:191]\n"
"v_add_u32 v0,0xc000,v0\n"
MF "v[188:191],a[204:207],a[104:107],v[188:191]\n"
"v_add_u32 v1,0xc000,v1\n"
MF "v[180:183],a[204:207],a[100:103],v[180:183]\n"
"s_waitcnt vmcnt(25) lgkmcnt(0)\n"
MF "v[180:183],a[200:203],a[60:63],v[180:183]\n"
"s_barrier\n"
MF "v[216:219],a[208:211],a[60:63],v[216:219]\n"
TR "a[200:201],v0\n"
MF "v[216:219],a[212:215],a[100:103],v[216:219]\n"
"s_add_u32 m0,s62,32768\n"
TR "a[202:203],v0 offset:512\n"
MF "v[224:227],a[212:215],a[104:107],v[224:227]\n"
"s_add_u32 s82,s66,4718848\n"
"ds_read_b128 a[192:195],v3 offset:47104\n"
MF "v[224:227],a[208:211],a[64:67],v[224:227]\n"
"s_sub_u32 s15,s82,7864320\n"
TR "a[204:205],v0 offset:4096\n"
MF "v[232:235],a[208:211],a[68:71],v[232:235]\n"
"s_min_u32 s82,s82,s15\n"
TR "a[206:207],v0 offset:4608\n"
MF "v[232:235],a[212:215],a[108:111],v[232:235]\n"
LD "v6,s[28:31],s82 offen lds\n"
MF "v[240:243],a[212:215],a[112:115],v[240:243]\n"
"ds_read_b128 a[156:159],v2 offset:49152\n"
MF "v[240:243],a[208:211],a[72:75],v[240:243]\n"
"ds_read_b128 a[196:199],v3 offset:49152\n"
MF "v[248:251],a[208:211],a[76:79],v[248:251]\n"
LD "v11,s[28:31],s82 offen offset:1024 lds\n"
MF "v[248:251],a[212:215],a[116:119],v[248:251]\n"
MF "v[252:255],a[220:223],a[116:119],v[252:255]\n"
TR "a[208:209],v0 offset:8\n"
MF "v[252:255],a[216:219],a[76:79],v[252:255]\n"
TR "a[210:211],v0 offset:520\n"
MF "v[244:247],a[216:219],a[72:75],v[244:247]\n"
MF "v[244:247],a[220:223],a[112:115],v[244:247]\n"
TR "a[212:213],v0 offset:4104\n"
MF "v[236:239],a[220:223],a[108:111],v[236:239]\n"
TR "a[214:215],v0 offset:4616\n"
MF "v[236:239],a[216:219],a[68:71],v[236:239]\n"
LD "v12,s[28:31],s82 offen offset:2048 lds\n"
MF "v[228:231],a[216:219],a[64:67],v[228:231]\n"
MF "v[228:231],a[220:223],a[104:107],v[228:231]\n"
MF "v[220:223],a[220:223],a[100:103],v[220:223]\n"
MF "v[220:223],a[216:219],a[60:63],v[220:223]\n"
"s_waitcnt lgkmcnt(4)\n"
MF "v[16:19],a[200:203],a[140:143],v[16:19]\n"
TR "a[216:217],v1\n"
MF "v[16:19],a[204:207],a[180:183],v[16:19]\n"
TR "a[218:219],v1 offset:512\n"
MF "v[24:27],a[204:207],a[184:187],v[24:27]\n"
LD "v13,s[28:31],s82 offen offset:3072 lds\n"
MF "v[24:27],a[200:203],a[144:147],v[24:27]\n"
TR "a[220:221],v1 offset:4096\n"
MF "v[32:35],a[200:203],a[148:151],v[32:35]\n"
TR "a[222:223],v1 offset:4608\n"
MF "v[32:35],a[204:207],a[188:191],v[32:35]\n"
MF "v[40:43],a[204:207],a[192:195],v[40:43]\n"
MF "v[40:43],a[200:203],a[152:155],v[40:43]\n"
"s_add_u32 s84,s14,40960\n"
MF "v[48:51],a[200:203],a[156:159],v[48:51]\n"
"s_add_u32 m0,s63,24576\n"
MF "v[48:51],a[204:207],a[196:199],v[48:51]\n"
"s_waitcnt lgkmcnt(4)\n"
LD "v4,s[24:27],s84 offen lds\n"
MF "v[52:55],a[212:215],a[196:199],v[52:55]\n"
TR "a[200:201],v1 offset:8\n"
MF "v[52:55],a[208:211],a[156:159],v[52:55]\n"
TR "a[202:203],v1 offset:520\n"
MF "v[44:47],a[208:211],a[152:155],v[44:47]\n"
MF "v[44:47],a[212:215],a[192:195],v[44:47]\n"
TR "a[204:205],v1 offset:4104\n"
MF "v[36:39],a[212:215],a[188:191],v[36:39]\n"
TR "a[206:207],v1 offset:4616\n"
MF "v[36:39],a[208:211],a[148:151],v[36:39]\n"
MF "v[28:31],a[208:211],a[144:147],v[28:31]\n"
MF "v[28:31],a[212:215],a[184:187],v[28:31]\n"
MF "v[20:23],a[212:215],a[180:183],v[20:23]\n"
".Lpg6c:\n"
"s_cbranch_execz .Lpg1a\n"
".Lpg6f:\n"
"s_cbranch_execz .Lpgt0k_f\n"
"s_waitcnt vmcnt(24) lgkmcnt(0)\n"
MF "v[20:23],a[208:211],a[140:143],v[20:23]\n"
"s_barrier\n"
MF "v[56:59],a[216:219],a[140:143],v[56:59]\n"
"s_add_u32 m0,s62,49152\n"
"s_add_u32 s82,s66,5505024\n"
TR "a[208:209],v0 offset:16384\n"
MF "v[56:59],a[220:223],a[180:183],v[56:59]\n"
"s_sub_u32 s15,s82,7864320\n"
"s_min_u32 s82,s82,s15\n"
TR "a[210:211],v0 offset:16896\n"
MF "v[64:67],a[220:223],a[184:187],v[64:67]\n"
LD "v6,s[28:31],s82 offen lds\n"
MF "v[64:67],a[216:219],a[144:147],v[64:67]\n"
TR "a[212:213],v0 offset:20480\n"
MF "v[72:75],a[216:219],a[148:151],v[72:75]\n"
TR "a[214:215],v0 offset:20992\n"
MF "v[72:75],a[220:223],a[188:191],v[72:75]\n"
MF "v[80:83],a[220:223],a[192:195],v[80:83]\n"
LD "v11,s[28:31],s82 offen offset:1024 lds\n"
MF "v[80:83],a[216:219],a[152:155],v[80:83]\n"
MF "v[88:91],a[216:219],a[156:159],v[88:91]\n"
MF "v[88:91],a[220:223],a[196:199],v[88:91]\n"
MF "v[92:95],a[204:207],a[196:199],v[92:95]\n"
TR "a[216:217],v0 offset:16392\n"
MF "v[92:95],a[200:203],a[156:159],v[92:95]\n"
TR "a[218:219],v0 offset:16904\n"
MF "v[84:87],a[200:203],a[152:155],v[84:87]\n"
LD "v12,s[28:31],s82 offen offset:2048 lds\n"
MF "v[84:87],a[204:207],a[192:195],v[84:87]\n"
TR "a[220:221],v0 offset:20488\n"
MF "v[76:79],a[204:207],a[188:191],v[76:79]\n"
TR "a[222:223],v0 offset:21000\n"
MF "v[76:79],a[200:203],a[148:151],v[76:79]\n"
MF "v[68:71],a[200:203],a[144:147],v[68:71]\n"
MF "v[68:71],a[204:207],a[184:187],v[68:71]\n"
MF "v[60:63],a[204:207],a[180:183],v[60:63]\n"
LD "v13,s[28:31],s82 offen offset:3072 lds\n"
MF "v[60:63],a[200:203],a[140:143],v[60:63]\n"
"s_waitcnt lgkmcnt(4)\n"
MF "v[96:99],a[208:211],a[140:143],v[96:99]\n"
TR "a[200:201],v1 offset:16384\n"
MF "v[96:99],a[212:215],a[180:183],v[96:99]\n"
TR "a[202:203],v1 offset:16896\n"
MF "v[104:107],a[212:215],a[184:187],v[104:107]\n"
"s_add_u32 s14,s81,1024\n"
"s_sub_u32 s15,s14,1280\n"
MF "v[104:107],a[208:211],a[144:147],v[104:107]\n"
"s_min_u32 s14,s14,s15\n"
"s_add_u32 s14,s14,s68\n"
TR "a[204:205],v1 offset:20480\n"
MF "v[112:115],a[208:211],a[148:151],v[112:115]\n"
"s_add_u32 s84,s14,0\n"
"s_add_u32 m0,s63,40960\n"
TR "a[206:207],v1 offset:20992\n"
MF "v[112:115],a[212:215],a[188:191],v[112:115]\n"
LD "v4,s[24:27],s84 offen lds\n"
MF "v[120:123],a[212:215],a[192:195],v[120:123]\n"
MF "v[120:123],a[208:211],a[152:155],v[120:123]\n"
MF "v[128:131],a[208:211],a[156:159],v[128:131]\n"
"s_add_u32 s84,s14,9216\n"
MF "v[128:131],a[212:215],a[196:199],v[128:131]\n"
"s_waitcnt lgkmcnt(4)\n"
LD "v5,s[24:27],s84 offen offset:1024 lds\n"
MF "v[132:135],a[220:223],a[196:199],v[132:135]\n"
TR "a[208:209],v1 offset:16392\n"
MF "v[132:135],a[216:219],a[156:159],v[132:135]\n"
TR "a[210:211],v1 offset:16904\n"
MF "v[124:127],a[216:219],a[152:155],v[124:127]\n"
MF "v[124:127],a[220:223],a[192:195],v[124:127]\n"
TR "a[212:213],v1 offset:20488\n"
MF "v[116:119],a[220:223],a[188:191],v[116:119]\n"
TR "a[214:215],v1 offset:21000\n"
MF "v[116:119],a[216:219],a[148:151],v[116:119]\n"
MF "v[108:111],a[216:219],a[144:147],v[108:111]\n"
MF "v[108:111],a[220:223],a[184:187],v[108:111]\n"
MF "v[100:103],a[220:223],a[180:183],v[100:103]\n"
"s_waitcnt vmcnt(23) lgkmcnt(0)\n"
MF "v[100:103],a[216:219],a[140:143],v[100:103]\n"
"s_barrier\n"
"s_cmp_eq_u32 s57,1\n"
"s_cbranch_scc0 .Lhs12\n"
"v_bfe_u32 v14,v9,4,5\n"
"v_lshlrev_b32 v14,6,v14\n"
"s_lshl_b32 s58,s78,2\n"
"s_add_u32 s58,s58,s86\n"
"buffer_load_dword v10,v14,s[40:43],s58 offen offset:128 sc1\n"
".Lhs12:\n"
MF "v[136:139],a[200:203],a[140:143],v[136:139]\n"
TR "a[216:217],v0 offset:32768\n"
MF "v[136:139],a[204:207],a[180:183],v[136:139]\n"
"s_add_u32 m0,s62,65536\n"
TR "a[218:219],v0 offset:33280\n"
MF "v[144:147],a[204:207],a[184:187],v[144:147]\n"
"s_add_u32 s82,s66,5505152\n"
"ds_read_b128 a[60:63],v2\n"
MF "v[144:147],a[200:203],a[144:147],v[144:147]\n"
"s_sub_u32 s15,s82,7864320\n"
TR "a[220:221],v0 offset:36864\n"
MF "v[152:155],a[200:203],a[148:151],v[152:155]\n"
"s_min_u32 s82,s82,s15\n"
TR "a[222:223],v0 offset:37376\n"
MF "v[152:155],a[204:207],a[188:191],v[152:155]\n"
LD "v6,s[28:31],s82 offen lds\n"
MF "v[160:163],a[204:207],a[192:195],v[160:163]\n"
"ds_read_b128 a[100:103],v3\n"
MF "v[160:163],a[200:203],a[152:155],v[160:163]\n"
LD "v11,s[28:31],s82 offen offset:1024 lds\n"
MF "v[168:171],a[200:203],a[156:159],v[168:171]\n"
MF "v[168:171],a[204:207],a[196:199],v[168:171]\n"
LD "v12,s[28:31],s82 offen offset:2048 lds\n"
MF "v[172:175],a[212:215],a[196:199],v[172:175]\n"
TR "a[200:201],v0 offset:32776\n"
MF "v[172:175],a[208:211],a[156:159],v[172:175]\n"
TR "a[202:203],v0 offset:33288\n"
MF "v[164:167],a[208:211],a[152:155],v[164:167]\n"
"ds_read_b128 a[64:67],v2 offset:2048\n"
MF "v[164:167],a[212:215],a[192:195],v[164:167]\n"
TR "a[204:205],v0 offset:36872\n"
MF "v[156:159],a[212:215],a[188:191],v[156:159]\n"
TR "a[206:207],v0 offset:37384\n"
MF "v[156:159],a[208:211],a[148:151],v[156:159]\n"
MF "v[148:151],a[208:211],a[144:147],v[148:151]\n"
"ds_read_b128 a[104:107],v3 offset:2048\n"
MF "v[148:151],a[212:215],a[184:187],v[148:151]\n"
MF "v[140:143],a[212:215],a[180:183],v[140:143]\n"
LD "v13,s[28:31],s82 offen offset:3072 lds\n"
MF "v[140:143],a[208:211],a[140:143],v[140:143]\n"
"s_waitcnt lgkmcnt(7)\n"
MF "v[176:179],a[216:219],a[140:143],v[176:179]\n"
TR "a[208:209],v1 offset:32768\n"
MF "v[176:179],a[220:223],a[180:183],v[176:179]\n"
TR "a[210:211],v1 offset:33280\n"
MF "v[184:187],a[220:223],a[184:187],v[184:187]\n"
"ds_read_b128 a[68:71],v2 offset:4096\n"
MF "v[184:187],a[216:219],a[144:147],v[184:187]\n"
"s_add_u32 s84,s14,20480\n"
TR "a[212:213],v1 offset:36864\n"
MF "v[192:195],a[216:219],a[148:151],v[192:195]\n"
"s_add_u32 m0,s63,43008\n"
TR "a[214:215],v1 offset:37376\n"
MF "v[192:195],a[220:223],a[188:191],v[192:195]\n"
LD "v4,s[24:27],s84 offen lds\n"
MF "v[200:203],a[220:223],a[192:195],v[200:203]\n"
"ds_read_b128 a[108:111],v3 offset:4096\n"
MF "v[200:203],a[216:219],a[152:155],v[200:203]\n"
MF "v[208:211],a[216:219],a[156:159],v[208:211]\n"
"s_add_u32 s84,s14,29696\n"
MF "v[208:211],a[220:223],a[196:199],v[208:211]\n"
"s_waitcnt lgkmcnt(7)\n"
LD "v5,s[24:27],s84 offen offset:1024 lds\n"
MF "v[212:215],a[204:207],a[196:199],v[212:215]\n"
TR "a[216:217],v1 offset:32776\n"
MF "v[212:215],a[200:203],a[156:159],v[212:215]\n"
TR "a[218:219],v1 offset:33288\n"
MF "v[204:207],a[200:203],a[152:155],v[204:207]\n"
"ds_read_b128 a[72:75],v2 offset:6144\n"
MF "v[204:207],a[204:207],a[192:195],v[204:207]\n"
TR "a[220:221],v1 offset:36872\n"
MF "v[196:199],a[204:207],a[188:191],v[196:199]\n"
TR "a[222:223],v1 offset:37384\n"
MF "v[196:199],a[200:203],a[148:151],v[196:199]\n"
MF "v[188:191],a[200:203],a[144:147],v[188:191]\n"
"v_add_u32 v0,0xffff4000,v0\n"
MF "v[188:191],a[204:207],a[184:187],v[188:191]\n"
"v_add_u32 v1,0xffff4000,v1\n"
MF "v[180:183],a[204:207],a[180:183],v[180:183]\n"
"s_waitcnt vmcnt(25) lgkmcnt(0)\n"
MF "v[180:183],a[200:203],a[140:143],v[180:183]\n"
"s_barrier\n"
MF "v[216:219],a[208:211],a[140:143],v[216:219]\n"
TR "a[200:201],v0\n"
MF "v[216:219],a[212:215],a[180:183],v[216:219]\n"
"s_add_u32 m0,s62,81920\n"
TR "a[202:203],v0 offset:512\n"
MF "v[224:227],a[212:215],a[184:187],v[224:227]\n"
"s_add_u32 s82,s66,5505280\n"
"ds_read_b128 a[112:115],v3 offset:6144\n"
MF "v[224:227],a[208:211],a[144:147],v[224:227]\n"
"s_sub_u32 s15,s82,7864320\n"
TR "a[204:205],v0 offset:4096\n"
MF "v[232:235],a[208:211],a[148:151],v[232:235]\n"
"s_min_u32 s82,s82,s15\n"
TR "a[206:207],v0 offset:4608\n"
MF "v[232:235],a[212:215],a[188:191],v[232:235]\n"
LD "v6,s[28:31],s82 offen lds\n"
MF "v[240:243],a[212:215],a[192:195],v[240:243]\n"
"ds_read_b128 a[76:79],v2 offset:8192\n"
MF "v[240:243],a[208:211],a[152:155],v[240:243]\n"
"ds_read_b128 a[116:119],v3 offset:8192\n"
MF "v[248:251],a[208:211],a[156:159],v[248:251]\n"
LD "v11,s[28:31],s82 offen offset:1024 lds\n"
MF "v[248:251],a[212:215],a[196:199],v[248:251]\n"
MF "v[252:255],a[220:223],a[196:199],v[252:255]\n"
TR "a[208:209],v0 offset:8\n"
MF "v[252:255],a[216:219],a[156:159],v[252:255]\n"
TR "a[210:211],v0 offset:520\n"
MF "v[244:247],a[216:219],a[152:155],v[244:247]\n"
MF "v[244:247],a[220:223],a[192:195],v[244:247]\n"
TR "a[212:213],v0 offset:4104\n"
MF "v[236:239],a[220:223],a[188:191],v[236:239]\n"
TR "a[214:215],v0 offset:4616\n"
MF "v[236:239],a[216:219],a[148:151],v[236:239]\n"
LD "v12,s[28:31],s82 offen offset:2048 lds\n"
MF "v[228:231],a[216:219],a[144:147],v[228:231]\n"
MF "v[228:231],a[220:223],a[184:187],v[228:231]\n"
MF "v[220:223],a[220:223],a[180:183],v[220:223]\n"
MF "v[220:223],a[216:219],a[140:143],v[220:223]\n"
"s_waitcnt lgkmcnt(4)\n"
MF "v[16:19],a[200:203],a[60:63],v[16:19]\n"
TR "a[216:217],v1\n"
MF "v[16:19],a[204:207],a[100:103],v[16:19]\n"
TR "a[218:219],v1 offset:512\n"
MF "v[24:27],a[204:207],a[104:107],v[24:27]\n"
LD "v13,s[28:31],s82 offen offset:3072 lds\n"
MF "v[24:27],a[200:203],a[64:67],v[24:27]\n"
TR "a[220:221],v1 offset:4096\n"
MF "v[32:35],a[200:203],a[68:71],v[32:35]\n"
TR "a[222:223],v1 offset:4608\n"
MF "v[32:35],a[204:207],a[108:111],v[32:35]\n"
MF "v[40:43],a[204:207],a[112:115],v[40:43]\n"
MF "v[40:43],a[200:203],a[72:75],v[40:43]\n"
"s_add_u32 s84,s14,40960\n"
MF "v[48:51],a[200:203],a[76:79],v[48:51]\n"
"s_add_u32 m0,s63,45056\n"
MF "v[48:51],a[204:207],a[116:119],v[48:51]\n"
"s_waitcnt lgkmcnt(4)\n"
LD "v4,s[24:27],s84 offen lds\n"
MF "v[52:55],a[212:215],a[116:119],v[52:55]\n"
TR "a[200:201],v1 offset:8\n"
MF "v[52:55],a[208:211],a[76:79],v[52:55]\n"
TR "a[202:203],v1 offset:520\n"
MF "v[44:47],a[208:211],a[72:75],v[44:47]\n"
MF "v[44:47],a[212:215],a[112:115],v[44:47]\n"
TR "a[204:205],v1 offset:4104\n"
MF "v[36:39],a[212:215],a[108:111],v[36:39]\n"
TR "a[206:207],v1 offset:4616\n"
MF "v[36:39],a[208:211],a[68:71],v[36:39]\n"
MF "v[28:31],a[208:211],a[64:67],v[28:31]\n"
MF "v[28:31],a[212:215],a[104:107],v[28:31]\n"
MF "v[20:23],a[212:215],a[100:103],v[20:23]\n"
"s_waitcnt vmcnt(24) lgkmcnt(0)\n"
MF "v[20:23],a[208:211],a[60:63],v[20:23]\n"
"s_barrier\n"
MF "v[56:59],a[216:219],a[60:63],v[56:59]\n"
"s_add_u32 m0,s62,0\n"
"s_add_u32 s82,s66,6291712\n"
TR "a[208:209],v0 offset:16384\n"
MF "v[56:59],a[220:223],a[100:103],v[56:59]\n"
"s_sub_u32 s15,s82,7864320\n"
"s_min_u32 s82,s82,s15\n"
TR "a[210:211],v0 offset:16896\n"
MF "v[64:67],a[220:223],a[104:107],v[64:67]\n"
LD "v6,s[28:31],s82 offen lds\n"
MF "v[64:67],a[216:219],a[64:67],v[64:67]\n"
TR "a[212:213],v0 offset:20480\n"
MF "v[72:75],a[216:219],a[68:71],v[72:75]\n"
TR "a[214:215],v0 offset:20992\n"
MF "v[72:75],a[220:223],a[108:111],v[72:75]\n"
MF "v[80:83],a[220:223],a[112:115],v[80:83]\n"
LD "v11,s[28:31],s82 offen offset:1024 lds\n"
MF "v[80:83],a[216:219],a[72:75],v[80:83]\n"
MF "v[88:91],a[216:219],a[76:79],v[88:91]\n"
MF "v[88:91],a[220:223],a[116:119],v[88:91]\n"
MF "v[92:95],a[204:207],a[116:119],v[92:95]\n"
TR "a[216:217],v0 offset:16392\n"
MF "v[92:95],a[200:203],a[76:79],v[92:95]\n"
TR "a[218:219],v0 offset:16904\n"
MF "v[84:87],a[200:203],a[72:75],v[84:87]\n"
LD "v12,s[28:31],s82 offen offset:2048 lds\n"
MF "v[84:87],a[204:207],a[112:115],v[84:87]\n"
TR "a[220:221],v0 offset:20488\n"
MF "v[76:79],a[204:207],a[108:111],v[76:79]\n"
TR "a[222:223],v0 offset:21000\n"
MF "v[76:79],a[200:203],a[68:71],v[76:79]\n"
MF "v[68:71],a[200:203],a[64:67],v[68:71]\n"
LD "v13,s[28:31],s82 offen offset:3072 lds\n"
MF "v[68:71],a[204:207],a[104:107],v[68:71]\n"
MF "v[60:63],a[204:207],a[100:103],v[60:63]\n"
MF "v[60:63],a[200:203],a[60:63],v[60:63]\n"
"s_add_u32 s14,s81,1152\n"
"s_sub_u32 s15,s14,1280\n"
"s_waitcnt lgkmcnt(4)\n"
MF "v[96:99],a[208:211],a[60:63],v[96:99]\n"
"s_min_u32 s14,s14,s15\n"
"s_add_u32 s14,s14,s68\n"
TR "a[200:201],v1 offset:16384\n"
MF "v[96:99],a[212:215],a[100:103],v[96:99]\n"
"s_add_u32 s84,s14,0\n"
"s_add_u32 m0,s63,0\n"
TR "a[202:203],v1 offset:16896\n"
MF "v[104:107],a[212:215],a[104:107],v[104:107]\n"
LD "v4,s[24:27],s84 offen lds\n"
MF "v[104:107],a[208:211],a[64:67],v[104:107]\n"
TR "a[204:205],v1 offset:20480\n"
MF "v[112:115],a[208:211],a[68:71],v[112:115]\n"
TR "a[206:207],v1 offset:20992\n"
MF "v[112:115],a[212:215],a[108:111],v[112:115]\n"
"s_add_u32 s84,s14,9216\n"
MF "v[120:123],a[212:215],a[112:115],v[120:123]\n"
LD "v5,s[24:27],s84 offen offset:1024 lds\n"
MF "v[120:123],a[208:211],a[72:75],v[120:123]\n"
MF "v[128:131],a[208:211],a[76:79],v[128:131]\n"
MF "v[128:131],a[212:215],a[116:119],v[128:131]\n"
"s_waitcnt lgkmcnt(4)\n"
MF "v[132:135],a[220:223],a[116:119],v[132:135]\n"
TR "a[208:209],v1 offset:16392\n"
MF "v[132:135],a[216:219],a[76:79],v[132:135]\n"
"s_add_u32 s84,s14,18432\n"
TR "a[210:211],v1 offset:16904\n"
MF "v[124:127],a[216:219],a[72:75],v[124:127]\n"
LD "v4,s[24:27],s84 offen offset:2048 lds\n"
MF "v[124:127],a[220:223],a[112:115],v[124:127]\n"
TR "a[212:213],v1 offset:20488\n"
MF "v[116:119],a[220:223],a[108:111],v[116:119]\n"
TR "a[214:215],v1 offset:21000\n"
MF "v[116:119],a[216:219],a[68:71],v[116:119]\n"
MF "v[108:111],a[216:219],a[64:67],v[108:111]\n"
MF "v[108:111],a[220:223],a[104:107],v[108:111]\n"
MF "v[100:103],a[220:223],a[100:103],v[100:103]\n"
"s_waitcnt vmcnt(24) lgkmcnt(0)\n"
MF "v[100:103],a[216:219],a[60:63],v[100:103]\n"
"s_barrier\n"
MF "v[136:139],a[200:203],a[60:63],v[136:139]\n"
TR "a[216:217],v0 offset:32768\n"
MF "v[136:139],a[204:207],a[100:103],v[136:139]\n"
"s_add_u32 m0,s62,16384\n"
TR "a[218:219],v0 offset:33280\n"
MF "v[144:147],a[204:207],a[104:107],v[144:147]\n"
"s_add_u32 s82,s66,7078144\n"
"ds_read_b128 a[140:143],v2 offset:20480\n"
MF "v[144:147],a[200:203],a[64:67],v[144:147]\n"
"s_sub_u32 s15,s82,7864320\n"
TR "a[220:221],v0 offset:36864\n"
MF "v[152:155],a[200:203],a[68:71],v[152:155]\n"
"s_min_u32 s82,s82,s15\n"
TR "a[222:223],v0 offset:37376\n"
MF "v[152:155],a[204:207],a[108:111],v[152:155]\n"
LD "v6,s[28:31],s82 offen lds\n"
MF "v[160:163],a[204:207],a[112:115],v[160:163]\n"
"ds_read_b128 a[180:183],v3 offset:20480\n"
MF "v[160:163],a[200:203],a[72:75],v[160:163]\n"
LD "v11,s[28:31],s82 offen offset:1024 lds\n"
MF "v[168:171],a[200:203],a[76:79],v[168:171]\n"
MF "v[168:171],a[204:207],a[116:119],v[168:171]\n"
LD "v12,s[28:31],s82 offen offset:2048 lds\n"
MF "v[172:175],a[212:215],a[116:119],v[172:175]\n"
TR "a[200:201],v0 offset:32776\n"
MF "v[172:175],a[208:211],a[76:79],v[172:175]\n"
TR "a[202:203],v0 offset:33288\n"
MF "v[164:167],a[208:211],a[72:75],v[164:167]\n"
"ds_read_b128 a[144:147],v2 offset:22528\n"
MF "v[164:167],a[212:215],a[112:115],v[164:167]\n"
TR "a[204:205],v0 offset:36872\n"
MF "v[156:159],a[212:215],a[108:111],v[156:159]\n"
TR "a[206:207],v0 offset:37384\n"
MF "v[156:159],a[208:211],a[68:71],v[156:159]\n"
MF "v[148:151],a[208:211],a[64:67],v[148:151]\n"
"ds_read_b128 a[184:187],v3 offset:22528\n"
MF "v[148:151],a[212:215],a[104:107],v[148:151]\n"
MF "v[140:143],a[212:215],a[100:103],v[140:143]\n"
LD "v13,s[28:31],s82 offen offset:3072 lds\n"
MF "v[140:143],a[208:211],a[60:63],v[140:143]\n"
"s_waitcnt lgkmcnt(7)\n"
MF "v[176:179],a[216:219],a[60:63],v[176:179]\n"
TR "a[208:209],v1 offset:32768\n"
MF "v[176:179],a[220:223],a[100:103],v[176:179]\n"
TR "a[210:211],v1 offset:33280\n"
MF "v[184:187],a[220:223],a[104:107],v[184:187]\n"
"ds_read_b128 a[148:151],v2 offset:24576\n"
MF "v[184:187],a[216:219],a[64:67],v[184:187]\n"
"s_add_u32 s84,s14,30720\n"
TR "a[212:213],v1 offset:36864\n"
MF "v[192:195],a[216:219],a[68:71],v[192:195]\n"
"s_add_u32 m0,s63,3072\n"
TR "a[214:215],v1 offset:37376\n"
MF "v[192:195],a[220:223],a[108:111],v[192:195]\n"
LD "v5,s[24:27],s84 offen lds\n"
MF "v[200:203],a[220:223],a[112:115],v[200:203]\n"
"ds_read_b128 a[188:191],v3 offset:24576\n"
MF "v[200:203],a[216:219],a[72:75],v[200:203]\n"
MF "v[208:211],a[216:219],a[76:79],v[208:211]\n"
"s_add_u32 s84,s14,39936\n"
MF "v[208:211],a[220:223],a[116:119],v[208:211]\n"
"s_waitcnt lgkmcnt(7)\n"
LD "v4,s[24:27],s84 offen offset:1024 lds\n"
MF "v[212:215],a[204:207],a[116:119],v[212:215]\n"
TR "a[216:217],v1 offset:32776\n"
MF "v[212:215],a[200:203],a[76:79],v[212:215]\n"
TR "a[218:219],v1 offset:33288\n"
MF "v[204:207],a[200:203],a[72:75],v[204:207]\n"
"ds_read_b128 a[152:155],v2 offset:26624\n"
MF "v[204:207],a[204:207],a[112:115],v[204:207]\n"
TR "a[220:221],v1 offset:36872\n"
MF "v[196:199],a[204:207],a[108:111],v[196:199]\n"
TR "a[222:223],v1 offset:37384\n"
MF "v[196:199],a[200:203],a[68:71],v[196:199]\n"
MF "v[188:191],a[200:203],a[64:67],v[188:191]\n"
"v_add_u32 v0,0xc000,v0\n"
MF "v[188:191],a[204:207],a[104:107],v[188:191]\n"
"v_add_u32 v1,0xc000,v1\n"
MF "v[180:183],a[204:207],a[100:103],v[180:183]\n"
"s_waitcnt vmcnt(26) lgkmcnt(0)\n"
".Lpgqj_a:\n"
"s_cbranch_execz .Lisr1\n"
".Lqj:\n"
MF "v[180:183],a[200:203],a[60:63],v[180:183]\n"
"s_barrier\n"
"s_getpc_b64 s[52:53]\n"
".Lctq:\n"
"s_sub_u32 s52,s52,.Lctq-.Lcode0\n"
"s_subb_u32 s53,s53,0\n"
"s_bfe_u32 s58,s23,0x50000\n"
"s_lshl_b32 s58,s58,2\n"
"s_add_u32 s58,s58,s56\n"
"s_nop 0\n"
"s_mul_i32 s58,s58,1280\n"
"v_min_u32 v15,144,v9\n"
"v_lshl_add_u32 v15,v15,3,s58\n"
"v_min_u32 v15,.Lcode1-.Lcode0,v15\n"
"global_load_dword a249,v15,s[52:53]\n"
MF "v[216:219],a[208:211],a[60:63],v[216:219]\n"
TR "a[200:201],v0\n"
MF "v[216:219],a[212:215],a[100:103],v[216:219]\n"
"s_add_u32 m0,s62,32768\n"
TR "a[202:203],v0 offset:512\n"
MF "v[224:227],a[212:215],a[104:107],v[224:227]\n"
"s_add_u32 s82,s66,6291584\n"
"ds_read_b128 a[192:195],v3 offset:26624\n"
MF "v[224:227],a[208:211],a[64:67],v[224:227]\n"
"s_sub_u32 s15,s82,7864320\n"
TR "a[204:205],v0 offset:4096\n"
MF "v[232:235],a[208:211],a[68:71],v[232:235]\n"
"s_min_u32 s82,s82,s15\n"
TR "a[206:207],v0 offset:4608\n"
MF "v[232:235],a[212:215],a[108:111],v[232:235]\n"
LD "v6,s[28:31],s82 offen lds\n"
MF "v[240:243],a[212:215],a[112:115],v[240:243]\n"
"ds_read_b128 a[156:159],v2 offset:28672\n"
MF "v[240:243],a[208:211],a[72:75],v[240:243]\n"
"ds_read_b128 a[196:199],v3 offset:28672\n"
MF "v[248:251],a[208:211],a[76:79],v[248:251]\n"
MF "v[248:251],a[212:215],a[116:119],v[248:251]\n"
LD "v11,s[28:31],s82 offen offset:1024 lds\n"
MF "v[252:255],a[220:223],a[116:119],v[252:255]\n"
TR "a[208:209],v0 offset:8\n"
MF "v[252:255],a[216:219],a[76:79],v[252:255]\n"
TR "a[210:211],v0 offset:520\n"
MF "v[244:247],a[216:219],a[72:75],v[244:247]\n"
MF "v[244:247],a[220:223],a[112:115],v[244:247]\n"
TR "a[212:213],v0 offset:4104\n"
MF "v[236:239],a[220:223],a[108:111],v[236:239]\n"
TR "a[214:215],v0 offset:4616\n"
MF "v[236:239],a[216:219],a[68:71],v[236:239]\n"
MF "v[228:231],a[216:219],a[64:67],v[228:231]\n"
MF "v[228:231],a[220:223],a[104:107],v[228:231]\n"
MF "v[220:223],a[220:223],a[100:103],v[220:223]\n"
LD "v12,s[28:31],s82 offen offset:2048 lds\n"
MF "v[220:223],a[216:219],a[60:63],v[220:223]\n"
"s_waitcnt lgkmcnt(4)\n"
MF "v[16:19],a[200:203],a[140:143],v[16:19]\n"
TR "a[216:217],v1\n"
MF "v[16:19],a[204:207],a[180:183],v[16:19]\n"
TR "a[218:219],v1 offset:512\n"
MF "v[24:27],a[204:207],a[184:187],v[24:27]\n"
MF "v[24:27],a[200:203],a[144:147],v[24:27]\n"
TR "a[220:221],v1 offset:4096\n"
MF "v[32:35],a[200:203],a[148:151],v[32:35]\n"
TR "a[222:223],v1 offset:4608\n"
MF "v[32:35],a[204:207],a[188:191],v[32:35]\n"
MF "v[40:43],a[204:207],a[192:195],v[40:43]\n"
MF "v[40:43],a[200:203],a[152:155],v[40:43]\n"
LD "v13,s[28:31],s82 offen offset:3072 lds\n"
MF "v[48:51],a[200:203],a[156:159],v[48:51]\n"
"s_memtime s[52:53]\n"
"s_memrealtime s[100:101]\n"
MF "v[48:51],a[204:207],a[196:199],v[48:51]\n"
"s_waitcnt lgkmcnt(4)\n"
MF "v[52:55],a[212:215],a[196:199],v[52:55]\n"
TR "a[200:201],v1 offset:8\n"
MF "v[52:55],a[208:211],a[156:159],v[52:55]\n"
TR "a[202:203],v1 offset:520\n"
MF "v[44:47],a[208:211],a[152:155],v[44:47]\n"
MF "v[44:47],a[212:215],a[192:195],v[44:47]\n"
TR "a[204:205],v1 offset:4104\n"
MF "v[36:39],a[212:215],a[188:191],v[36:39]\n"
TR "a[206:207],v1 offset:4616\n"
MF "v[36:39],a[208:211],a[148:151],v[36:39]\n"
MF "v[28:31],a[208:211],a[144:147],v[28:31]\n"
MF "v[28:31],a[212:215],a[184:187],v[28:31]\n"
MF "v[20:23],a[212:215],a[180:183],v[20:23]\n"
"s_waitcnt vmcnt(25) lgkmcnt(0)\n"
MF "v[20:23],a[208:211],a[140:143],v[20:23]\n"
"s_barrier\n"
MF "v[56:59],a[216:219],a[140:143],v[56:59]\n"
"s_add_u32 m0,s62,49152\n"
"s_add_u32 s82,s66,7078016\n"
TR "a[208:209],v0 offset:16384\n"
MF "v[56:59],a[220:223],a[180:183],v[56:59]\n"
"s_sub_u32 s15,s82,7864320\n"
"s_min_u32 s82,s82,s15\n"
TR "a[210:211],v0 offset:16896\n"
MF "v[64:67],a[220:223],a[184:187],v[64:67]\n"
LD "v6,s[28:31],s82 offen lds\n"
MF "v[64:67],a[216:219],a[144:147],v[64:67]\n"
"s_sub_u32 s53,s52,s80\n"
TR "a[212:213],v0 offset:20480\n"
"v_lshrrev_b32 v15,8,v9\n"
MF "v[72:75],a[216:219],a[148:151],v[72:75]\n"
TR "a[214:215],v0 offset:20992\n"
"v_mul_u32_u24 v15,48960,v15\n"
MF "v[72:75],a[220:223],a[188:191],v[72:75]\n"
"s_lshr_b32 s7,s56,1\n"
"v_add_u32 v15,v15,v9\n"
MF "v[80:83],a[220:223],a[192:195],v[80:83]\n"
"s_and_b32 s8,s56,1\n"
MF "v[80:83],a[216:219],a[152:155],v[80:83]\n"
"s_sub_u32 s7,s7,s8\n"
MF "v[88:91],a[216:219],a[156:159],v[88:91]\n"
"s_mul_i32 s7,s7,245760\n"
MF "v[88:91],a[220:223],a[196:199],v[88:91]\n"
LD "v11,s[28:31],s82 offen offset:1024 lds\n"
MF "v[92:95],a[204:207],a[196:199],v[92:95]\n"
"s_add_u32 s7,s7,s89\n"
TR "a[216:217],v0 offset:16392\n"
MF "v[92:95],a[200:203],a[156:159],v[92:95]\n"
"s_cmp_gt_u32 s53,24100\n"
"s_cselect_b32 s101,1,0\n"
TR "a[218:219],v0 offset:16904\n"
MF "v[84:87],a[200:203],a[152:155],v[84:87]\n"
"s_cmp_eq_u32 s87,2\n"
"s_cselect_b32 s87,s101,s87\n"
MF "v[84:87],a[204:207],a[192:195],v[84:87]\n"
"s_cmp_eq_u32 s69,99\n"
"s_cselect_b32 s97,124,0\n"
TR "a[220:221],v0 offset:20488\n"
MF "v[76:79],a[204:207],a[188:191],v[76:79]\n"
"s_cmp_eq_u32 s57,0\n"
"s_cselect_b32 s97,136,s97\n"
TR "a[222:223],v0 offset:21000\n"
MF "v[76:79],a[200:203],a[148:151],v[76:79]\n"
"s_mul_i32 s97,s97,s87\n"
MF "v[68:71],a[200:203],a[144:147],v[68:71]\n"
"s_getreg_b32 s101,hwreg(HW_REG_XCC_ID,0,1)\n"
MF "v[68:71],a[204:207],a[184:187],v[68:71]\n"
"s_add_u32 s101,s101,s69\n"
MF "v[60:63],a[204:207],a[180:183],v[60:63]\n"
LD "v12,s[28:31],s82 offen offset:2048 lds\n"
MF "v[60:63],a[200:203],a[140:143],v[60:63]\n"
".Lpg7a:\n"
"s_cbranch_execz .Lisr1\n"
"s_waitcnt lgkmcnt(4)\n"
MF "v[96:99],a[208:211],a[140:143],v[96:99]\n"
"s_cmp_eq_u32 s57,s101\n"
"s_cselect_b32 s97,176,s97\n"
TR "a[200:201],v1 offset:16384\n"
MF "v[96:99],a[212:215],a[180:183],v[96:99]\n"
"s_cmp_eq_u32 s97,0\n"
"s_cselect_b32 s95,0x47800000,0\n"
TR "a[202:203],v1 offset:16896\n"
MF "v[104:107],a[212:215],a[184:187],v[104:107]\n"
"s_mov_b32 s21,s100\n"
MF "v[104:107],a[208:211],a[144:147],v[104:107]\n"
"s_mov_b32 s77,s95\n"
TR "a[204:205],v1 offset:20480\n"
MF "v[112:115],a[208:211],a[148:151],v[112:115]\n"
TR "a[206:207],v1 offset:20992\n"
MF "v[112:115],a[212:215],a[188:191],v[112:115]\n"
MF "v[120:123],a[212:215],a[192:195],v[120:123]\n"
MF "v[120:123],a[208:211],a[152:155],v[120:123]\n"
LD "v13,s[28:31],s82 offen offset:3072 lds\n"
MF "v[128:131],a[208:211],a[156:159],v[128:131]\n"
MF "v[128:131],a[212:215],a[196:199],v[128:131]\n"
"s_waitcnt lgkmcnt(4)\n"
MF "v[132:135],a[220:223],a[196:199],v[132:135]\n"
TR "a[208:209],v1 offset:16392\n"
MF "v[132:135],a[216:219],a[156:159],v[132:135]\n"
TR "a[210:211],v1 offset:16904\n"
MF "v[124:127],a[216:219],a[152:155],v[124:127]\n"
MF "v[124:127],a[220:223],a[192:195],v[124:127]\n"
TR "a[212:213],v1 offset:20488\n"
MF "v[116:119],a[220:223],a[188:191],v[116:119]\n"
TR "a[214:215],v1 offset:21000\n"
MF "v[116:119],a[216:219],a[148:151],v[116:119]\n"
MF "v[108:111],a[216:219],a[144:147],v[108:111]\n"
MF "v[108:111],a[220:223],a[184:187],v[108:111]\n"
MF "v[100:103],a[220:223],a[180:183],v[100:103]\n"
"s_waitcnt vmcnt(22) lgkmcnt(0)\n"
MF "v[100:103],a[216:219],a[140:143],v[100:103]\n"
"s_barrier\n"
"s_cmp_eq_u32 s57,1\n"
"s_cbranch_scc0 .Lhs15\n"
".Lack_q:\n"
"v_cmp_ne_u32 vcc,s76,v10\n"
"s_cbranch_vccnz .Lackspin_q\n"
".Lackok_q:\n"
".Lhs15:\n"
MF "v[136:139],a[200:203],a[140:143],v[136:139]\n"
"s_and_b32 s58,s56,1\n"
TR "a[216:217],v0 offset:32768\n"
MF "v[136:139],a[204:207],a[180:183],v[136:139]\n"
"s_add_u32 m0,s62,65536\n"
TR "a[218:219],v0 offset:33280\n"
MF "v[144:147],a[204:207],a[184:187],v[144:147]\n"
"s_add_u32 s82,s66,6291456\n"
"ds_read_b128 a[60:63],v2 offset:40960\n"
MF "v[144:147],a[200:203],a[144:147],v[144:147]\n"
"s_sub_u32 s15,s82,7864320\n"
TR "a[220:221],v0 offset:36864\n"
MF "v[152:155],a[200:203],a[148:151],v[152:155]\n"
"s_min_u32 s82,s82,s15\n"
TR "a[222:223],v0 offset:37376\n"
MF "v[152:155],a[204:207],a[188:191],v[152:155]\n"
LD "v6,s[28:31],s82 offen lds\n"
MF "v[160:163],a[204:207],a[192:195],v[160:163]\n"
"s_mul_i32 s59,s58,3072\n"
"ds_read_b128 a[100:103],v3 offset:40960\n"
MF "v[160:163],a[200:203],a[152:155],v[160:163]\n"
"s_add_u32 s16,s70,s59\n"
MF "v[168:171],a[200:203],a[156:159],v[168:171]\n"
"s_mul_i32 s59,s58,6144\n"
MF "v[168:171],a[204:207],a[196:199],v[168:171]\n"
LD "v11,s[28:31],s82 offen offset:1024 lds\n"
MF "v[172:175],a[212:215],a[196:199],v[172:175]\n"
"s_add_u32 s18,s70,s59\n"
TR "a[200:201],v0 offset:32776\n"
MF "v[172:175],a[208:211],a[156:159],v[172:175]\n"
"s_add_u32 s17,s16,196608\n"
TR "a[202:203],v0 offset:33288\n"
MF "v[164:167],a[208:211],a[152:155],v[164:167]\n"
"s_add_u32 s19,s18,196608\n"
"ds_read_b128 a[64:67],v2 offset:43008\n"
MF "v[164:167],a[212:215],a[192:195],v[164:167]\n"
"s_add_u32 s20,s70,196608\n"
TR "a[204:205],v0 offset:36872\n"
MF "v[156:159],a[212:215],a[188:191],v[156:159]\n"
"s_bfe_u32 s58,s23,0x40008\n"
TR "a[206:207],v0 offset:37384\n"
MF "v[156:159],a[208:211],a[148:151],v[156:159]\n"
"s_lshr_b32 s59,s56,1\n"
MF "v[148:151],a[208:211],a[144:147],v[148:151]\n"
"s_mul_i32 s59,s59,5\n"
"ds_read_b128 a[104:107],v3 offset:43008\n"
MF "v[148:151],a[212:215],a[184:187],v[148:151]\n"
"s_add_u32 s58,s58,s59\n"
MF "v[140:143],a[212:215],a[180:183],v[140:143]\n"
LD "v12,s[28:31],s82 offen offset:2048 lds\n"
MF "v[140:143],a[208:211],a[140:143],v[140:143]\n"
"s_waitcnt lgkmcnt(7)\n"
MF "v[176:179],a[216:219],a[140:143],v[176:179]\n"
"s_and_b32 s58,s58,15\n"
TR "a[208:209],v1 offset:32768\n"
MF "v[176:179],a[220:223],a[180:183],v[176:179]\n"
"s_mul_i32 s58,s58,12288\n"
TR "a[210:211],v1 offset:33280\n"
MF "v[184:187],a[220:223],a[184:187],v[184:187]\n"
"s_add_u32 s72,s16,s58\n"
"ds_read_b128 a[68:71],v2 offset:45056\n"
MF "v[184:187],a[216:219],a[144:147],v[184:187]\n"
"s_add_u32 s73,s18,s58\n"
TR "a[212:213],v1 offset:36864\n"
MF "v[192:195],a[216:219],a[148:151],v[192:195]\n"
"s_add_u32 s74,s70,s58\n"
TR "a[214:215],v1 offset:37376\n"
MF "v[192:195],a[220:223],a[188:191],v[192:195]\n"
"s_add_u32 s74,s74,12288\n"
MF "v[200:203],a[220:223],a[192:195],v[200:203]\n"
"s_cmp_eq_u32 s74,s20\n"
"s_cselect_b32 s74,s70,s74\n"
"ds_read_b128 a[108:111],v3 offset:45056\n"
MF "v[200:203],a[216:219],a[152:155],v[200:203]\n"
LD "v13,s[28:31],s82 offen offset:3072 lds\n"
MF "v[208:211],a[216:219],a[156:159],v[208:211]\n"
"s_lshr_b32 s58,s56,1\n"
MF "v[208:211],a[220:223],a[196:199],v[208:211]\n"
"s_waitcnt lgkmcnt(7)\n"
MF "v[212:215],a[204:207],a[196:199],v[212:215]\n"
"s_mul_i32 s58,s58,6144\n"
TR "a[216:217],v1 offset:32776\n"
MF "v[212:215],a[200:203],a[156:159],v[212:215]\n"
"s_mul_i32 s64,s56,3072\n"
TR "a[218:219],v1 offset:33288\n"
MF "v[204:207],a[200:203],a[152:155],v[204:207]\n"
"s_add_u32 s64,s64,s58\n"
"ds_read_b128 a[72:75],v2 offset:47104\n"
MF "v[204:207],a[204:207],a[192:195],v[204:207]\n"
"s_mul_i32 s65,s56,6144\n"
TR "a[220:221],v1 offset:36872\n"
MF "v[196:199],a[204:207],a[188:191],v[196:199]\n"
"s_sub_u32 s58,s71,s70\n"
TR "a[222:223],v1 offset:37384\n"
MF "v[196:199],a[200:203],a[148:151],v[196:199]\n"
"s_add_u32 s96,s74,s58\n"
MF "v[188:191],a[200:203],a[144:147],v[188:191]\n"
"s_sub_u32 s58,s16,s70\n"
"v_add_u32 v0,0xffff4000,v0\n"
MF "v[188:191],a[204:207],a[184:187],v[188:191]\n"
"s_add_u32 s96,s96,s58\n"
"v_add_u32 v1,0xffff4000,v1\n"
MF "v[180:183],a[204:207],a[180:183],v[180:183]\n"
"s_waitcnt vmcnt(13) lgkmcnt(0)\n"
MF "v[180:183],a[200:203],a[140:143],v[180:183]\n"
"s_barrier\n"
MF "v[216:219],a[208:211],a[140:143],v[216:219]\n"
"s_max_u32 s60,s70,s71\n"
TR "a[200:201],v1 offset:8\n"
MF "v[216:219],a[212:215],a[180:183],v[216:219]\n"
"s_add_u32 m0,s62,81920\n"
TR "a[202:203],v1 offset:520\n"
"v_mov_b32 v14,s12\n"
MF "v[224:227],a[212:215],a[184:187],v[224:227]\n"
"s_add_u32 s82,s66,7077888\n"
"ds_read_b128 a[112:115],v3 offset:47104\n"
"v_mul_f32 v14,0xbdd53b95,v14\n"
MF "v[224:227],a[208:211],a[144:147],v[224:227]\n"
"s_sub_u32 s15,s82,7864320\n"
TR "a[204:205],v1 offset:4104\n"
MF "v[232:235],a[208:211],a[148:151],v[232:235]\n"
"s_min_u32 s82,s82,s15\n"
TR "a[206:207],v1 offset:4616\n"
MF "v[232:235],a[212:215],a[188:191],v[232:235]\n"
LD "v6,s[28:31],s82 offen lds\n"
MF "v[240:243],a[212:215],a[192:195],v[240:243]\n"
"s_add_u32 s34,s60,196608\n"
"ds_read_b128 a[76:79],v2 offset:49152\n"
MF "v[240:243],a[208:211],a[152:155],v[240:243]\n"
"s_add_u32 s38,s60,196608\n"
"ds_read_b128 a[116:119],v3 offset:49152\n"
MF "v[248:251],a[208:211],a[156:159],v[248:251]\n"
MF "v[248:251],a[212:215],a[196:199],v[248:251]\n"
LD "v11,s[28:31],s82 offen offset:1024 lds\n"
MF "v[220:223],a[216:219],a[140:143],v[220:223]\n"
TR "a[208:209],v1 offset:16392\n"
MF "v[220:223],a[220:223],a[180:183],v[220:223]\n"
TR "a[210:211],v1 offset:16904\n"
MF "v[228:231],a[220:223],a[184:187],v[228:231]\n"
"ds_read_b128 a[140:143],v2\n"
MF "v[228:231],a[216:219],a[144:147],v[228:231]\n"
TR "a[212:213],v1 offset:20488\n"
MF "v[236:239],a[216:219],a[148:151],v[236:239]\n"
TR "a[214:215],v1 offset:21000\n"
MF "v[236:239],a[220:223],a[188:191],v[236:239]\n"
"ds_read_b128 a[180:183],v3\n"
MF "v[244:247],a[220:223],a[192:195],v[244:247]\n"
"ds_read_b128 a[144:147],v2 offset:2048\n"
MF "v[244:247],a[216:219],a[152:155],v[244:247]\n"
"ds_read_b128 a[184:187],v3 offset:2048\n"
MF "v[252:255],a[216:219],a[156:159],v[252:255]\n"
"ds_read_b128 a[148:151],v2 offset:4096\n"
MF "v[252:255],a[220:223],a[196:199],v[252:255]\n"
"s_waitcnt lgkmcnt(9)\n"
"ds_read_b128 a[188:191],v3 offset:4096\n"
MF "v[220:223],a[200:203],a[60:63],v[220:223]\n"
TR "a[216:217],v1\n"
MF "v[220:223],a[204:207],a[100:103],v[220:223]\n"
TR "a[218:219],v1 offset:512\n"
MF "v[228:231],a[204:207],a[104:107],v[228:231]\n"
"ds_read_b128 a[152:155],v2 offset:6144\n"
MF "v[228:231],a[200:203],a[64:67],v[228:231]\n"
TR "a[220:221],v1 offset:4096\n"
MF "v[236:239],a[200:203],a[68:71],v[236:239]\n"
TR "a[222:223],v1 offset:4608\n"
MF "v[236:239],a[204:207],a[108:111],v[236:239]\n"
"ds_read_b128 a[192:195],v3 offset:6144\n"
MF "v[244:247],a[204:207],a[112:115],v[244:247]\n"
"ds_read_b128 a[156:159],v2 offset:8192\n"
MF "v[244:247],a[200:203],a[72:75],v[244:247]\n"
"ds_read_b128 a[196:199],v3 offset:8192\n"
MF "v[252:255],a[200:203],a[76:79],v[252:255]\n"
LD "v12,s[28:31],s82 offen offset:2048 lds\n"
MF "v[252:255],a[204:207],a[116:119],v[252:255]\n"
"s_waitcnt lgkmcnt(8)\n"
LD "v13,s[28:31],s82 offen offset:3072 lds\n"
MF "v[220:223],a[208:211],a[140:143],v[220:223]\n"
TR "a[200:201],v1 offset:16384\n"
MF "v[220:223],a[212:215],a[180:183],v[220:223]\n"
TR "a[202:203],v1 offset:16896\n"
MF "v[228:231],a[212:215],a[184:187],v[228:231]\n"
MF "v[228:231],a[208:211],a[144:147],v[228:231]\n"
TR "a[204:205],v1 offset:20480\n"
MF "v[236:239],a[208:211],a[148:151],v[236:239]\n"
TR "a[206:207],v1 offset:20992\n"
MF "v[236:239],a[212:215],a[188:191],v[236:239]\n"
"s_waitcnt lgkmcnt(4)\n"
MF "v[244:247],a[212:215],a[192:195],v[244:247]\n"
MF "v[244:247],a[208:211],a[152:155],v[244:247]\n"
MF "v[252:255],a[208:211],a[156:159],v[252:255]\n"
MF "v[252:255],a[212:215],a[196:199],v[252:255]\n"
"s_waitcnt lgkmcnt(0)\n"
"s_cmp_eq_u32 s69,98\n"
"s_cbranch_scc1 .Lqy\n"
".Lqf:\n"
MF "v[216:219],a[216:219],a[60:63],v[216:219]\n"
TR "a[208:209],v0 offset:8\n"
MF "v[216:219],a[220:223],a[100:103],v[216:219]\n"
TR "a[210:211],v0 offset:520\n"
MF "v[224:227],a[220:223],a[104:107],v[224:227]\n"
"buffer_load_dword a224,v15,s[36:39],s70 offen offset:2688 nt\n"
MF "v[224:227],a[216:219],a[64:67],v[224:227]\n"
TR "a[212:213],v0 offset:4104\n"
MF "v[232:235],a[216:219],a[68:71],v[232:235]\n"
TR "a[214:215],v0 offset:4616\n"
MF "v[232:235],a[220:223],a[108:111],v[232:235]\n"
"buffer_load_dword a225,v15,s[36:39],s71 offen offset:2688 nt\n"
MF "v[240:243],a[220:223],a[112:115],v[240:243]\n"
MF "v[240:243],a[216:219],a[72:75],v[240:243]\n"
"buffer_load_dword a226,v15,s[48:51],s7 offen offset:2688\n"
MF "v[248:251],a[216:219],a[76:79],v[248:251]\n"
MF "v[248:251],a[220:223],a[116:119],v[248:251]\n"
MF "v[248:251],a[200:203],a[156:159],v[248:251]\n"
TR "a[216:217],v0 offset:16392\n"
MF "v[248:251],a[204:207],a[196:199],v[248:251]\n"
TR "a[218:219],v0 offset:16904\n"
MF "v[240:243],a[204:207],a[192:195],v[240:243]\n"
MF "v[240:243],a[200:203],a[152:155],v[240:243]\n"
TR "a[220:221],v0 offset:20488\n"
MF "v[232:235],a[200:203],a[148:151],v[232:235]\n"
TR "a[222:223],v0 offset:21000\n"
MF "v[232:235],a[204:207],a[188:191],v[232:235]\n"
MF "v[224:227],a[204:207],a[184:187],v[224:227]\n"
MF "v[224:227],a[200:203],a[144:147],v[224:227]\n"
"v_fma_f32 v248,v248,s54,v14\n"
"v_fma_f32 v249,v249,s54,v14\n"
MF "v[216:219],a[200:203],a[140:143],v[216:219]\n"
"v_fma_f32 v250,v250,s54,v14\n"
"v_fma_f32 v251,v251,s54,v14\n"
MF "v[216:219],a[204:207],a[180:183],v[216:219]\n"
"s_waitcnt lgkmcnt(4)\n"
"v_fma_f32 v252,v252,s54,v14\n"
"v_fma_f32 v253,v253,s54,v14\n"
MF "v[180:183],a[208:211],a[60:63],v[180:183]\n"
TR "a[200:201],v0\n"
"v_fma_f32 v254,v254,s54,v14\n"
MF "v[180:183],a[212:215],a[100:103],v[180:183]\n"
TR "a[202:203],v0 offset:512\n"
"v_fma_f32 v255,v255,s54,v14\n"
MF "v[188:191],a[212:215],a[104:107],v[188:191]\n"
CV "v255,v254,v255\n"
CV "v254,v252,v253\n"
MF "v[188:191],a[208:211],a[64:67],v[188:191]\n"
TR "a[204:205],v0 offset:4096\n"
CV "v253,v250,v251\n"
MF "v[196:199],a[208:211],a[68:71],v[196:199]\n"
TR "a[206:207],v0 offset:4608\n"
CV "v252,v248,v249\n"
MF "v[196:199],a[212:215],a[108:111],v[196:199]\n"
"v_fma_f32 v240,v240,s54,v14\n"
"v_fma_f32 v241,v241,s54,v14\n"
MF "v[204:207],a[212:215],a[112:115],v[204:207]\n"
"v_fma_f32 v242,v242,s54,v14\n"
"v_fma_f32 v243,v243,s54,v14\n"
MF "v[204:207],a[208:211],a[72:75],v[204:207]\n"
"v_fma_f32 v244,v244,s54,v14\n"
"v_fma_f32 v245,v245,s54,v14\n"
MF "v[212:215],a[208:211],a[76:79],v[212:215]\n"
"v_fma_f32 v246,v246,s54,v14\n"
"v_fma_f32 v247,v247,s54,v14\n"
MF "v[212:215],a[212:215],a[116:119],v[212:215]\n"
"s_waitcnt lgkmcnt(4)\n"
CV "v251,v246,v247\n"
CV "v250,v244,v245\n"
MF "v[180:183],a[216:219],a[140:143],v[180:183]\n"
TR "a[208:209],v0 offset:16384\n"
CV "v249,v242,v243\n"
MF "v[180:183],a[220:223],a[180:183],v[180:183]\n"
TR "a[210:211],v0 offset:16896\n"
CV "v248,v240,v241\n"
MF "v[188:191],a[220:223],a[184:187],v[188:191]\n"
"v_fma_f32 v232,v232,s54,v14\n"
"v_fma_f32 v233,v233,s54,v14\n"
MF "v[188:191],a[216:219],a[144:147],v[188:191]\n"
TR "a[212:213],v0 offset:20480\n"
"v_fma_f32 v234,v234,s54,v14\n"
MF "v[196:199],a[216:219],a[148:151],v[196:199]\n"
TR "a[214:215],v0 offset:20992\n"
"v_fma_f32 v235,v235,s54,v14\n"
MF "v[196:199],a[220:223],a[188:191],v[196:199]\n"
"v_fma_f32 v236,v236,s54,v14\n"
"v_fma_f32 v237,v237,s54,v14\n"
MF "v[204:207],a[220:223],a[192:195],v[204:207]\n"
"v_fma_f32 v238,v238,s54,v14\n"
"v_fma_f32 v239,v239,s54,v14\n"
MF "v[204:207],a[216:219],a[152:155],v[204:207]\n"
CV "v247,v238,v239\n"
CV "v246,v236,v237\n"
MF "v[212:215],a[216:219],a[156:159],v[212:215]\n"
"s_waitcnt vmcnt(11) lgkmcnt(0)\n"
CV "v245,v234,v235\n"
CV "v244,v232,v233\n"
MF "v[212:215],a[220:223],a[196:199],v[212:215]\n"
"s_barrier\n"
MF "v[176:179],a[200:203],a[60:63],v[176:179]\n"
TR "a[216:217],v1 offset:32776\n"
"v_fma_f32 v224,v224,s54,v14\n"
MF "v[176:179],a[204:207],a[100:103],v[176:179]\n"
"s_add_u32 m0,s64,0\n"
TR "a[218:219],v1 offset:33288\n"
"v_fma_f32 v225,v225,s54,v14\n"
MF "v[184:187],a[204:207],a[104:107],v[184:187]\n"
LD "v7,s[32:35],s72 offen nt lds\n"
MF "v[184:187],a[200:203],a[64:67],v[184:187]\n"
TR "a[220:221],v1 offset:36872\n"
"v_fma_f32 v226,v226,s54,v14\n"
MF "v[192:195],a[200:203],a[68:71],v[192:195]\n"
TR "a[222:223],v1 offset:37384\n"
"v_fma_f32 v227,v227,s54,v14\n"
MF "v[192:195],a[204:207],a[108:111],v[192:195]\n"
"s_add_u32 m0,s64,896\n"
"v_fma_f32 v228,v228,s54,v14\n"
"v_fma_f32 v229,v229,s54,v14\n"
MF "v[200:203],a[204:207],a[112:115],v[200:203]\n"
LD "v7,s[32:35],s72 offen offset:128 nt lds\n"
MF "v[200:203],a[200:203],a[72:75],v[200:203]\n"
"v_fma_f32 v230,v230,s54,v14\n"
"v_fma_f32 v231,v231,s54,v14\n"
MF "v[208:211],a[200:203],a[76:79],v[208:211]\n"
CV "v243,v230,v231\n"
CV "v242,v228,v229\n"
MF "v[208:211],a[204:207],a[116:119],v[208:211]\n"
CV "v241,v226,v227\n"
CV "v240,v224,v225\n"
MF "v[208:211],a[208:211],a[156:159],v[208:211]\n"
TR "a[200:201],v1 offset:49160\n"
"v_fma_f32 v216,v216,s54,v14\n"
MF "v[208:211],a[212:215],a[196:199],v[208:211]\n"
"s_add_u32 m0,s64,1792\n"
TR "a[202:203],v1 offset:49672\n"
"v_fma_f32 v217,v217,s54,v14\n"
MF "v[200:203],a[212:215],a[192:195],v[200:203]\n"
LD "v7,s[32:35],s72 offen offset:256 nt lds\n"
MF "v[200:203],a[208:211],a[152:155],v[200:203]\n"
TR "a[204:205],v1 offset:53256\n"
"v_fma_f32 v218,v218,s54,v14\n"
MF "v[192:195],a[208:211],a[148:151],v[192:195]\n"
TR "a[206:207],v1 offset:53768\n"
"v_fma_f32 v219,v219,s54,v14\n"
MF "v[192:195],a[212:215],a[188:191],v[192:195]\n"
"v_fma_f32 v220,v220,s54,v14\n"
"v_fma_f32 v221,v221,s54,v14\n"
MF "v[184:187],a[212:215],a[184:187],v[184:187]\n"
"s_add_u32 s72,s72,6144\n"
"v_fma_f32 v222,v222,s54,v14\n"
"v_fma_f32 v223,v223,s54,v14\n"
MF "v[184:187],a[208:211],a[144:147],v[184:187]\n"
"s_add_u32 m0,s64,6144\n"
CV "v239,v222,v223\n"
CV "v238,v220,v221\n"
MF "v[176:179],a[208:211],a[140:143],v[176:179]\n"
LD "v7,s[32:35],s72 offen nt lds\n"
MF "v[176:179],a[212:215],a[180:183],v[176:179]\n"
"s_waitcnt lgkmcnt(4)\n"
CV "v237,v218,v219\n"
CV "v236,v216,v217\n"
MF "v[140:143],a[216:219],a[60:63],v[140:143]\n"
TR "a[208:209],v1 offset:32768\n"
"v_fma_f32 v208,v208,s54,v14\n"
MF "v[140:143],a[220:223],a[100:103],v[140:143]\n"
TR "a[210:211],v1 offset:33280\n"
"v_fma_f32 v209,v209,s54,v14\n"
MF "v[148:151],a[220:223],a[104:107],v[148:151]\n"
"v_fma_f32 v210,v210,s54,v14\n"
"v_fma_f32 v211,v211,s54,v14\n"
MF "v[148:151],a[216:219],a[64:67],v[148:151]\n"
TR "a[212:213],v1 offset:36864\n"
"v_fma_f32 v212,v212,s54,v14\n"
MF "v[156:159],a[216:219],a[68:71],v[156:159]\n"
"s_add_u32 m0,s64,7040\n"
TR "a[214:215],v1 offset:37376\n"
"v_fma_f32 v213,v213,s54,v14\n"
MF "v[156:159],a[220:223],a[108:111],v[156:159]\n"
LD "v7,s[32:35],s72 offen offset:128 nt lds\n"
MF "v[164:167],a[220:223],a[112:115],v[164:167]\n"
"v_fma_f32 v214,v214,s54,v14\n"
"v_fma_f32 v215,v215,s54,v14\n"
MF "v[164:167],a[216:219],a[72:75],v[164:167]\n"
CV "v235,v214,v215\n"
CV "v234,v212,v213\n"
MF "v[172:175],a[216:219],a[76:79],v[172:175]\n"
"s_add_u32 m0,s64,7936\n"
CV "v233,v210,v211\n"
CV "v232,v208,v209\n"
MF "v[172:175],a[220:223],a[116:119],v[172:175]\n"
"s_waitcnt lgkmcnt(4)\n"
LD "v7,s[32:35],s72 offen offset:256 nt lds\n"
MF "v[140:143],a[200:203],a[140:143],v[140:143]\n"
TR "a[216:217],v1 offset:49152\n"
"v_fma_f32 v200,v200,s54,v14\n"
MF "v[140:143],a[204:207],a[180:183],v[140:143]\n"
TR "a[218:219],v1 offset:49664\n"
"v_fma_f32 v201,v201,s54,v14\n"
MF "v[148:151],a[204:207],a[184:187],v[148:151]\n"
"v_fma_f32 v202,v202,s54,v14\n"
"v_fma_f32 v203,v203,s54,v14\n"
MF "v[148:151],a[200:203],a[144:147],v[148:151]\n"
TR "a[220:221],v1 offset:53248\n"
"v_fma_f32 v204,v204,s54,v14\n"
MF "v[156:159],a[200:203],a[148:151],v[156:159]\n"
TR "a[222:223],v1 offset:53760\n"
"v_fma_f32 v205,v205,s54,v14\n"
MF "v[156:159],a[204:207],a[188:191],v[156:159]\n"
"v_fma_f32 v206,v206,s54,v14\n"
"v_fma_f32 v207,v207,s54,v14\n"
MF "v[164:167],a[204:207],a[192:195],v[164:167]\n"
CV "v231,v206,v207\n"
CV "v230,v204,v205\n"
MF "v[164:167],a[200:203],a[152:155],v[164:167]\n"
CV "v229,v202,v203\n"
CV "v228,v200,v201\n"
MF "v[172:175],a[200:203],a[156:159],v[172:175]\n"
"v_fma_f32 v192,v192,s54,v14\n"
"v_fma_f32 v193,v193,s54,v14\n"
MF "v[172:175],a[204:207],a[196:199],v[172:175]\n"
"s_waitcnt lgkmcnt(0)\n"
"v_fma_f32 v194,v194,s54,v14\n"
"v_fma_f32 v195,v195,s54,v14\n"
MF "v[136:139],a[208:211],a[60:63],v[136:139]\n"
TR "a[200:201],v0 offset:32776\n"
"v_fma_f32 v196,v196,s54,v14\n"
MF "v[136:139],a[212:215],a[100:103],v[136:139]\n"
TR "a[202:203],v0 offset:33288\n"
"v_fma_f32 v197,v197,s54,v14\n"
MF "v[144:147],a[212:215],a[104:107],v[144:147]\n"
"v_fma_f32 v198,v198,s54,v14\n"
"v_fma_f32 v199,v199,s54,v14\n"
MF "v[144:147],a[208:211],a[64:67],v[144:147]\n"
TR "a[204:205],v0 offset:36872\n"
CV "v227,v198,v199\n"
MF "v[152:155],a[208:211],a[68:71],v[152:155]\n"
TR "a[206:207],v0 offset:37384\n"
CV "v226,v196,v197\n"
MF "v[152:155],a[212:215],a[108:111],v[152:155]\n"
CV "v225,v194,v195\n"
CV "v224,v192,v193\n"
MF "v[160:163],a[212:215],a[112:115],v[160:163]\n"
"v_fma_f32 v184,v184,s54,v14\n"
"v_fma_f32 v185,v185,s54,v14\n"
MF "v[160:163],a[208:211],a[72:75],v[160:163]\n"
"v_fma_f32 v186,v186,s54,v14\n"
"v_fma_f32 v187,v187,s54,v14\n"
MF "v[168:171],a[208:211],a[76:79],v[168:171]\n"
"v_fma_f32 v188,v188,s54,v14\n"
"v_fma_f32 v189,v189,s54,v14\n"
MF "v[168:171],a[212:215],a[116:119],v[168:171]\n"
"v_fma_f32 v190,v190,s54,v14\n"
"v_fma_f32 v191,v191,s54,v14\n"
MF "v[168:171],a[216:219],a[156:159],v[168:171]\n"
TR "a[208:209],v0 offset:49160\n"
CV "v223,v190,v191\n"
MF "v[168:171],a[220:223],a[196:199],v[168:171]\n"
TR "a[210:211],v0 offset:49672\n"
CV "v222,v188,v189\n"
MF "v[160:163],a[220:223],a[192:195],v[160:163]\n"
CV "v221,v186,v187\n"
CV "v220,v184,v185\n"
MF "v[160:163],a[216:219],a[152:155],v[160:163]\n"
TR "a[212:213],v0 offset:53256\n"
"v_fma_f32 v176,v176,s54,v14\n"
MF "v[152:155],a[216:219],a[148:151],v[152:155]\n"
TR "a[214:215],v0 offset:53768\n"
"v_fma_f32 v177,v177,s54,v14\n"
MF "v[152:155],a[220:223],a[188:191],v[152:155]\n"
"v_fma_f32 v178,v178,s54,v14\n"
"v_fma_f32 v179,v179,s54,v14\n"
MF "v[144:147],a[220:223],a[184:187],v[144:147]\n"
"v_fma_f32 v180,v180,s54,v14\n"
"v_fma_f32 v181,v181,s54,v14\n"
MF "v[144:147],a[216:219],a[144:147],v[144:147]\n"
"v_fma_f32 v182,v182,s54,v14\n"
"v_fma_f32 v183,v183,s54,v14\n"
MF "v[136:139],a[216:219],a[140:143],v[136:139]\n"
CV "v219,v182,v183\n"
CV "v218,v180,v181\n"
MF "v[136:139],a[220:223],a[180:183],v[136:139]\n"
"s_waitcnt lgkmcnt(4)\n"
CV "v217,v178,v179\n"
CV "v216,v176,v177\n"
MF "v[100:103],a[200:203],a[60:63],v[100:103]\n"
TR "a[216:217],v0 offset:32768\n"
"v_fma_f32 v168,v168,s54,v14\n"
MF "v[100:103],a[204:207],a[100:103],v[100:103]\n"
TR "a[218:219],v0 offset:33280\n"
"v_fma_f32 v169,v169,s54,v14\n"
MF "v[108:111],a[204:207],a[104:107],v[108:111]\n"
"v_fma_f32 v170,v170,s54,v14\n"
"v_fma_f32 v171,v171,s54,v14\n"
MF "v[108:111],a[200:203],a[64:67],v[108:111]\n"
TR "a[220:221],v0 offset:36864\n"
"v_fma_f32 v172,v172,s54,v14\n"
MF "v[116:119],a[200:203],a[68:71],v[116:119]\n"
TR "a[222:223],v0 offset:37376\n"
"v_fma_f32 v173,v173,s54,v14\n"
MF "v[116:119],a[204:207],a[108:111],v[116:119]\n"
".Lpg8b:\n"
"s_cbranch_execz .Lpg11b\n"
".Lpg8d:\n"
"s_cbranch_execz .Lisr2\n"
"v_fma_f32 v174,v174,s54,v14\n"
"v_fma_f32 v175,v175,s54,v14\n"
MF "v[124:127],a[204:207],a[112:115],v[124:127]\n"
CV "v215,v174,v175\n"
CV "v214,v172,v173\n"
MF "v[124:127],a[200:203],a[72:75],v[124:127]\n"
CV "v213,v170,v171\n"
CV "v212,v168,v169\n"
MF "v[132:135],a[200:203],a[76:79],v[132:135]\n"
"v_fma_f32 v160,v160,s54,v14\n"
"v_fma_f32 v161,v161,s54,v14\n"
MF "v[132:135],a[204:207],a[116:119],v[132:135]\n"
"s_waitcnt lgkmcnt(4)\n"
"v_fma_f32 v162,v162,s54,v14\n"
"v_fma_f32 v163,v163,s54,v14\n"
MF "v[100:103],a[208:211],a[140:143],v[100:103]\n"
TR "a[200:201],v0 offset:49152\n"
"v_fma_f32 v164,v164,s54,v14\n"
MF "v[100:103],a[212:215],a[180:183],v[100:103]\n"
TR "a[202:203],v0 offset:49664\n"
"v_fma_f32 v165,v165,s54,v14\n"
MF "v[108:111],a[212:215],a[184:187],v[108:111]\n"
"v_fma_f32 v166,v166,s54,v14\n"
"v_fma_f32 v167,v167,s54,v14\n"
MF "v[108:111],a[208:211],a[144:147],v[108:111]\n"
TR "a[204:205],v0 offset:53248\n"
CV "v211,v166,v167\n"
MF "v[116:119],a[208:211],a[148:151],v[116:119]\n"
TR "a[206:207],v0 offset:53760\n"
CV "v210,v164,v165\n"
MF "v[116:119],a[212:215],a[188:191],v[116:119]\n"
CV "v209,v162,v163\n"
CV "v208,v160,v161\n"
MF "v[124:127],a[212:215],a[192:195],v[124:127]\n"
"v_add_u32 v0,0xc000,v0\n"
"v_fma_f32 v152,v152,s54,v14\n"
MF "v[124:127],a[208:211],a[152:155],v[124:127]\n"
"v_add_u32 v1,0xc000,v1\n"
"v_fma_f32 v153,v153,s54,v14\n"
MF "v[132:135],a[208:211],a[156:159],v[132:135]\n"
"s_waitcnt vmcnt(9) lgkmcnt(0)\n"
"v_fma_f32 v154,v154,s54,v14\n"
"v_fma_f32 v155,v155,s54,v14\n"
MF "v[132:135],a[212:215],a[196:199],v[132:135]\n"
"s_barrier\n"
MF "v[96:99],a[216:219],a[60:63],v[96:99]\n"
TR "a[208:209],v1 offset:16392\n"
"v_fma_f32 v156,v156,s54,v14\n"
MF "v[96:99],a[220:223],a[100:103],v[96:99]\n"
TR "a[210:211],v1 offset:16904\n"
"v_fma_f32 v157,v157,s54,v14\n"
MF "v[104:107],a[220:223],a[104:107],v[104:107]\n"
"s_add_u32 s72,s72,6144\n"
"v_fma_f32 v158,v158,s54,v14\n"
"v_fma_f32 v159,v159,s54,v14\n"
MF "v[104:107],a[216:219],a[64:67],v[104:107]\n"
"s_cmp_eq_u32 s72,s17\n"
"s_cselect_b32 s72,s16,s72\n"
TR "a[212:213],v1 offset:20488\n"
MF "v[112:115],a[216:219],a[68:71],v[112:115]\n"
"s_add_u32 m0,s64,24576\n"
TR "a[214:215],v1 offset:21000\n"
CV "v207,v158,v159\n"
MF "v[112:115],a[220:223],a[108:111],v[112:115]\n"
LD "v7,s[32:35],s72 offen nt lds\n"
MF "v[120:123],a[220:223],a[112:115],v[120:123]\n"
"s_add_u32 m0,s64,25472\n"
CV "v206,v156,v157\n"
CV "v205,v154,v155\n"
MF "v[120:123],a[216:219],a[72:75],v[120:123]\n"
LD "v7,s[32:35],s72 offen offset:128 nt lds\n"
MF "v[128:131],a[216:219],a[76:79],v[128:131]\n"
CV "v204,v152,v153\n"
"v_fma_f32 v144,v144,s54,v14\n"
MF "v[128:131],a[220:223],a[116:119],v[128:131]\n"
"v_fma_f32 v145,v145,s54,v14\n"
"v_fma_f32 v146,v146,s54,v14\n"
MF "v[128:131],a[200:203],a[156:159],v[128:131]\n"
TR "a[216:217],v1 offset:32776\n"
"v_fma_f32 v147,v147,s54,v14\n"
MF "v[128:131],a[204:207],a[196:199],v[128:131]\n"
"s_add_u32 m0,s64,26368\n"
TR "a[218:219],v1 offset:33288\n"
"v_fma_f32 v148,v148,s54,v14\n"
MF "v[120:123],a[204:207],a[192:195],v[120:123]\n"
LD "v7,s[32:35],s72 offen offset:256 nt lds\n"
MF "v[120:123],a[200:203],a[152:155],v[120:123]\n"
TR "a[220:221],v1 offset:36872\n"
"v_fma_f32 v149,v149,s54,v14\n"
MF "v[112:115],a[200:203],a[148:151],v[112:115]\n"
TR "a[222:223],v1 offset:37384\n"
"v_fma_f32 v150,v150,s54,v14\n"
MF "v[112:115],a[204:207],a[188:191],v[112:115]\n"
"v_fma_f32 v151,v151,s54,v14\n"
CV "v203,v150,v151\n"
MF "v[104:107],a[204:207],a[184:187],v[104:107]\n"
"s_add_u32 s72,s72,6144\n"
CV "v202,v148,v149\n"
CV "v201,v146,v147\n"
MF "v[104:107],a[200:203],a[144:147],v[104:107]\n"
"s_add_u32 m0,s64,30720\n"
CV "v200,v144,v145\n"
"v_fma_f32 v136,v136,s54,v14\n"
MF "v[96:99],a[200:203],a[140:143],v[96:99]\n"
LD "v7,s[32:35],s72 offen nt lds\n"
MF "v[96:99],a[204:207],a[180:183],v[96:99]\n"
"s_waitcnt lgkmcnt(4)\n"
"v_fma_f32 v137,v137,s54,v14\n"
"v_fma_f32 v138,v138,s54,v14\n"
MF "v[60:63],a[208:211],a[60:63],v[60:63]\n"
TR "a[200:201],v1 offset:16384\n"
"v_fma_f32 v139,v139,s54,v14\n"
MF "v[60:63],a[212:215],a[100:103],v[60:63]\n"
TR "a[202:203],v1 offset:16896\n"
"v_fma_f32 v140,v140,s54,v14\n"
MF "v[68:71],a[212:215],a[104:107],v[68:71]\n"
"v_fma_f32 v141,v141,s54,v14\n"
"v_fma_f32 v142,v142,s54,v14\n"
MF "v[68:71],a[208:211],a[64:67],v[68:71]\n"
TR "a[204:205],v1 offset:20480\n"
"v_fma_f32 v143,v143,s54,v14\n"
MF "v[76:79],a[208:211],a[68:71],v[76:79]\n"
"s_add_u32 m0,s64,31616\n"
TR "a[206:207],v1 offset:20992\n"
CV "v199,v142,v143\n"
MF "v[76:79],a[212:215],a[108:111],v[76:79]\n"
LD "v7,s[32:35],s72 offen offset:128 nt lds\n"
MF "v[84:87],a[212:215],a[112:115],v[84:87]\n"
CV "v198,v140,v141\n"
CV "v197,v138,v139\n"
MF "v[84:87],a[208:211],a[72:75],v[84:87]\n"
CV "v196,v136,v137\n"
"v_fma_f32 v128,v128,s54,v14\n"
MF "v[92:95],a[208:211],a[76:79],v[92:95]\n"
"s_add_u32 m0,s64,32512\n"
"v_fma_f32 v129,v129,s54,v14\n"
"v_fma_f32 v130,v130,s54,v14\n"
MF "v[92:95],a[212:215],a[116:119],v[92:95]\n"
"s_waitcnt lgkmcnt(4)\n"
LD "v7,s[32:35],s72 offen offset:256 nt lds\n"
MF "v[60:63],a[216:219],a[140:143],v[60:63]\n"
TR "a[208:209],v1 offset:32768\n"
"v_fma_f32 v131,v131,s54,v14\n"
MF "v[60:63],a[220:223],a[180:183],v[60:63]\n"
TR "a[210:211],v1 offset:33280\n"
"v_fma_f32 v132,v132,s54,v14\n"
MF "v[68:71],a[220:223],a[184:187],v[68:71]\n"
"v_fma_f32 v133,v133,s54,v14\n"
"v_fma_f32 v134,v134,s54,v14\n"
MF "v[68:71],a[216:219],a[144:147],v[68:71]\n"
TR "a[212:213],v1 offset:36864\n"
"v_fma_f32 v135,v135,s54,v14\n"
MF "v[76:79],a[216:219],a[148:151],v[76:79]\n"
TR "a[214:215],v1 offset:37376\n"
CV "v195,v134,v135\n"
MF "v[76:79],a[220:223],a[188:191],v[76:79]\n"
CV "v194,v132,v133\n"
CV "v193,v130,v131\n"
MF "v[84:87],a[220:223],a[192:195],v[84:87]\n"
CV "v192,v128,v129\n"
"v_fma_f32 v120,v120,s54,v14\n"
MF "v[84:87],a[216:219],a[152:155],v[84:87]\n"
"v_fma_f32 v121,v121,s54,v14\n"
"v_fma_f32 v122,v122,s54,v14\n"
MF "v[92:95],a[216:219],a[156:159],v[92:95]\n"
"v_fma_f32 v123,v123,s54,v14\n"
"v_fma_f32 v124,v124,s54,v14\n"
MF "v[92:95],a[220:223],a[196:199],v[92:95]\n"
"s_waitcnt lgkmcnt(0)\n"
"v_fma_f32 v125,v125,s54,v14\n"
"v_fma_f32 v126,v126,s54,v14\n"
MF "v[56:59],a[200:203],a[60:63],v[56:59]\n"
TR "a[216:217],v0 offset:16392\n"
"v_fma_f32 v127,v127,s54,v14\n"
MF "v[56:59],a[204:207],a[100:103],v[56:59]\n"
TR "a[218:219],v0 offset:16904\n"
CV "v191,v126,v127\n"
MF "v[64:67],a[204:207],a[104:107],v[64:67]\n"
CV "v190,v124,v125\n"
CV "v189,v122,v123\n"
MF "v[64:67],a[200:203],a[64:67],v[64:67]\n"
TR "a[220:221],v0 offset:20488\n"
CV "v188,v120,v121\n"
MF "v[72:75],a[200:203],a[68:71],v[72:75]\n"
TR "a[222:223],v0 offset:21000\n"
"v_fma_f32 v112,v112,s54,v14\n"
MF "v[72:75],a[204:207],a[108:111],v[72:75]\n"
"v_fma_f32 v113,v113,s54,v14\n"
"v_fma_f32 v114,v114,s54,v14\n"
MF "v[80:83],a[204:207],a[112:115],v[80:83]\n"
"v_fma_f32 v115,v115,s54,v14\n"
"v_fma_f32 v116,v116,s54,v14\n"
MF "v[80:83],a[200:203],a[72:75],v[80:83]\n"
"v_fma_f32 v117,v117,s54,v14\n"
"v_fma_f32 v118,v118,s54,v14\n"
MF "v[88:91],a[200:203],a[76:79],v[88:91]\n"
"v_fma_f32 v119,v119,s54,v14\n"
CV "v187,v118,v119\n"
MF "v[88:91],a[204:207],a[116:119],v[88:91]\n"
CV "v186,v116,v117\n"
CV "v185,v114,v115\n"
MF "v[88:91],a[208:211],a[156:159],v[88:91]\n"
TR "a[200:201],v0 offset:32776\n"
CV "v184,v112,v113\n"
MF "v[88:91],a[212:215],a[196:199],v[88:91]\n"
TR "a[202:203],v0 offset:33288\n"
"v_fma_f32 v104,v104,s54,v14\n"
MF "v[80:83],a[212:215],a[192:195],v[80:83]\n"
"v_fma_f32 v105,v105,s54,v14\n"
"v_fma_f32 v106,v106,s54,v14\n"
MF "v[80:83],a[208:211],a[152:155],v[80:83]\n"
TR "a[204:205],v0 offset:36872\n"
"v_fma_f32 v107,v107,s54,v14\n"
MF "v[72:75],a[208:211],a[148:151],v[72:75]\n"
TR "a[206:207],v0 offset:37384\n"
"v_fma_f32 v108,v108,s54,v14\n"
MF "v[72:75],a[212:215],a[188:191],v[72:75]\n"
"v_fma_f32 v109,v109,s54,v14\n"
"v_fma_f32 v110,v110,s54,v14\n"
MF "v[64:67],a[212:215],a[184:187],v[64:67]\n"
"v_fma_f32 v111,v111,s54,v14\n"
CV "v183,v110,v111\n"
MF "v[64:67],a[208:211],a[144:147],v[64:67]\n"
CV "v182,v108,v109\n"
CV "v181,v106,v107\n"
MF "v[56:59],a[208:211],a[140:143],v[56:59]\n"
CV "v180,v104,v105\n"
"v_fma_f32 v96,v96,s54,v14\n"
MF "v[56:59],a[212:215],a[180:183],v[56:59]\n"
"s_waitcnt lgkmcnt(4)\n"
"v_fma_f32 v97,v97,s54,v14\n"
"v_fma_f32 v98,v98,s54,v14\n"
MF "v[20:23],a[216:219],a[60:63],v[20:23]\n"
TR "a[208:209],v0 offset:16384\n"
"v_fma_f32 v99,v99,s54,v14\n"
MF "v[20:23],a[220:223],a[100:103],v[20:23]\n"
TR "a[210:211],v0 offset:16896\n"
"v_fma_f32 v100,v100,s54,v14\n"
MF "v[28:31],a[220:223],a[104:107],v[28:31]\n"
"v_fma_f32 v101,v101,s54,v14\n"
"v_fma_f32 v102,v102,s54,v14\n"
MF "v[28:31],a[216:219],a[64:67],v[28:31]\n"
TR "a[212:213],v0 offset:20480\n"
"v_fma_f32 v103,v103,s54,v14\n"
MF "v[36:39],a[216:219],a[68:71],v[36:39]\n"
TR "a[214:215],v0 offset:20992\n"
CV "v179,v102,v103\n"
MF "v[36:39],a[220:223],a[108:111],v[36:39]\n"
CV "v178,v100,v101\n"
CV "v177,v98,v99\n"
MF "v[44:47],a[220:223],a[112:115],v[44:47]\n"
CV "v176,v96,v97\n"
"v_fma_f32 v88,v88,s54,v14\n"
MF "v[44:47],a[216:219],a[72:75],v[44:47]\n"
"v_fma_f32 v89,v89,s54,v14\n"
"v_fma_f32 v90,v90,s54,v14\n"
MF "v[52:55],a[216:219],a[76:79],v[52:55]\n"
"v_fma_f32 v91,v91,s54,v14\n"
"v_fma_f32 v92,v92,s54,v14\n"
MF "v[52:55],a[220:223],a[116:119],v[52:55]\n"
"s_waitcnt lgkmcnt(4)\n"
"v_fma_f32 v93,v93,s54,v14\n"
"v_fma_f32 v94,v94,s54,v14\n"
MF "v[20:23],a[200:203],a[140:143],v[20:23]\n"
TR "a[216:217],v0 offset:32768\n"
"v_fma_f32 v95,v95,s54,v14\n"
MF "v[20:23],a[204:207],a[180:183],v[20:23]\n"
TR "a[218:219],v0 offset:33280\n"
CV "v175,v94,v95\n"
MF "v[28:31],a[204:207],a[184:187],v[28:31]\n"
CV "v174,v92,v93\n"
CV "v173,v90,v91\n"
MF "v[28:31],a[200:203],a[144:147],v[28:31]\n"
TR "a[220:221],v0 offset:36864\n"
CV "v172,v88,v89\n"
MF "v[36:39],a[200:203],a[148:151],v[36:39]\n"
TR "a[222:223],v0 offset:37376\n"
"v_fma_f32 v80,v80,s54,v14\n"
MF "v[36:39],a[204:207],a[188:191],v[36:39]\n"
"v_fma_f32 v81,v81,s54,v14\n"
"v_fma_f32 v82,v82,s54,v14\n"
MF "v[44:47],a[204:207],a[192:195],v[44:47]\n"
"v_fma_f32 v83,v83,s54,v14\n"
"v_fma_f32 v84,v84,s54,v14\n"
MF "v[44:47],a[200:203],a[152:155],v[44:47]\n"
"v_fma_f32 v85,v85,s54,v14\n"
"v_fma_f32 v86,v86,s54,v14\n"
MF "v[52:55],a[200:203],a[156:159],v[52:55]\n"
"s_waitcnt lgkmcnt(0)\n"
"v_fma_f32 v87,v87,s54,v14\n"
CV "v171,v86,v87\n"
MF "v[52:55],a[204:207],a[196:199],v[52:55]\n"
"s_barrier\n"
MF "v[16:19],a[208:211],a[60:63],v[16:19]\n"
"s_add_u32 m0,s65,49152\n"
CV "v170,v84,v85\n"
CV "v169,v82,v83\n"
MF "v[16:19],a[212:215],a[100:103],v[16:19]\n"
LD "v8,s[36:39],s73 offen nt lds\n"
MF "v[24:27],a[212:215],a[104:107],v[24:27]\n"
"s_add_u32 m0,s65,50048\n"
CV "v168,v80,v81\n"
"v_fma_f32 v72,v72,s54,v14\n"
MF "v[24:27],a[208:211],a[64:67],v[24:27]\n"
LD "v8,s[36:39],s73 offen offset:128 nt lds\n"
MF "v[32:35],a[208:211],a[68:71],v[32:35]\n"
"v_fma_f32 v73,v73,s54,v14\n"
"v_fma_f32 v74,v74,s54,v14\n"
MF "v[32:35],a[212:215],a[108:111],v[32:35]\n"
"s_add_u32 m0,s65,50944\n"
"v_fma_f32 v75,v75,s54,v14\n"
"v_fma_f32 v76,v76,s54,v14\n"
MF "v[40:43],a[212:215],a[112:115],v[40:43]\n"
LD "v8,s[36:39],s73 offen offset:256 nt lds\n"
MF "v[40:43],a[208:211],a[72:75],v[40:43]\n"
"v_fma_f32 v77,v77,s54,v14\n"
"v_fma_f32 v78,v78,s54,v14\n"
MF "v[48:51],a[208:211],a[76:79],v[48:51]\n"
"s_add_u32 m0,s65,49152\n"
"v_fma_f32 v79,v79,s54,v14\n"
CV "v167,v78,v79\n"
MF "v[48:51],a[212:215],a[116:119],v[48:51]\n"
LD "v8,s[36:39],s73 offen offset:3072 nt lds\n"
MF "v[48:51],a[216:219],a[156:159],v[48:51]\n"
"s_add_u32 m0,s65,50048\n"
CV "v166,v76,v77\n"
CV "v165,v74,v75\n"
MF "v[48:51],a[220:223],a[196:199],v[48:51]\n"
LD "v8,s[36:39],s73 offen offset:3200 nt lds\n"
MF "v[40:43],a[220:223],a[192:195],v[40:43]\n"
CV "v164,v72,v73\n"
"v_fma_f32 v64,v64,s54,v14\n"
MF "v[40:43],a[216:219],a[152:155],v[40:43]\n"
"s_add_u32 m0,s65,50944\n"
"v_fma_f32 v65,v65,s54,v14\n"
"v_fma_f32 v66,v66,s54,v14\n"
MF "v[32:35],a[216:219],a[148:151],v[32:35]\n"
LD "v8,s[36:39],s73 offen offset:3328 nt lds\n"
MF "v[32:35],a[220:223],a[188:191],v[32:35]\n"
"v_fma_f32 v67,v67,s54,v14\n"
"v_fma_f32 v68,v68,s54,v14\n"
MF "v[24:27],a[220:223],a[184:187],v[24:27]\n"
"v_fma_f32 v69,v69,s54,v14\n"
"v_fma_f32 v70,v70,s54,v14\n"
MF "v[24:27],a[216:219],a[144:147],v[24:27]\n"
"v_fma_f32 v71,v71,s54,v14\n"
CV "v163,v70,v71\n"
MF "v[16:19],a[216:219],a[140:143],v[16:19]\n"
CV "v162,v68,v69\n"
CV "v161,v66,v67\n"
MF "v[16:19],a[220:223],a[180:183],v[16:19]\n"
CV "v160,v64,v65\n"
AR "v0,a244\n"
"v_xor_b32 v1,64,v0\n"
"s_waitcnt vmcnt(12) lgkmcnt(0)\n"
"s_barrier\n"
"ds_read_b128 v[92:95],v0\n"
"ds_read_b128 v[96:99],v1\n"
"ds_read_b128 v[100:103],v0 offset:1024\n"
"v_pk_fma_f32 v[56:57],v[56:57],s[54:55],v[14:15] op_sel_hi:[1,1,0]\n"
"v_pk_fma_f32 v[58:59],v[58:59],s[54:55],v[14:15] op_sel_hi:[1,1,0]\n"
"v_pk_fma_f32 v[60:61],v[60:61],s[54:55],v[14:15] op_sel_hi:[1,1,0]\n"
"v_pk_fma_f32 v[62:63],v[62:63],s[54:55],v[14:15] op_sel_hi:[1,1,0]\n"
CV "v159,v62,v63\n"
CV "v158,v60,v61\n"
CV "v157,v58,v59\n"
CV "v156,v56,v57\n"
"v_pk_mul_f32 v[48:49],v[48:49],s[54:55]\n"
"v_pk_mul_f32 v[50:51],v[50:51],s[54:55]\n"
"v_pk_mul_f32 v[52:53],v[52:53],s[54:55]\n"
"v_pk_mul_f32 v[54:55],v[54:55],s[54:55]\n"
CV "v155,v54,v55\n"
CV "v154,v52,v53\n"
CV "v153,v50,v51\n"
CV "v152,v48,v49\n"
"v_pk_mul_f32 v[40:41],v[40:41],s[54:55]\n"
"v_pk_mul_f32 v[42:43],v[42:43],s[54:55]\n"
"v_pk_mul_f32 v[44:45],v[44:45],s[54:55]\n"
"v_pk_mul_f32 v[46:47],v[46:47],s[54:55]\n"
CV "v151,v46,v47\n"
CV "v150,v44,v45\n"
CV "v149,v42,v43\n"
CV "v148,v40,v41\n"
"v_pk_mul_f32 v[32:33],v[32:33],s[54:55]\n"
"v_pk_mul_f32 v[34:35],v[34:35],s[54:55]\n"
"v_pk_mul_f32 v[36:37],v[36:37],s[54:55]\n"
"v_pk_mul_f32 v[38:39],v[38:39],s[54:55]\n"
CV "v147,v38,v39\n"
CV "v146,v36,v37\n"
CV "v145,v34,v35\n"
CV "v144,v32,v33\n"
"v_pk_mul_f32 v[24:25],v[24:25],s[54:55]\n"
"v_pk_mul_f32 v[26:27],v[26:27],s[54:55]\n"
"v_pk_mul_f32 v[28:29],v[28:29],s[54:55]\n"
"v_pk_mul_f32 v[30:31],v[30:31],s[54:55]\n"
CV "v143,v30,v31\n"
CV "v142,v28,v29\n"
CV "v141,v26,v27\n"
CV "v140,v24,v25\n"
"v_pk_mul_f32 v[16:17],v[16:17],s[54:55]\n"
"v_pk_mul_f32 v[18:19],v[18:19],s[54:55]\n"
"v_pk_mul_f32 v[20:21],v[20:21],s[54:55]\n"
"v_pk_mul_f32 v[22:23],v[22:23],s[54:55]\n"
CV "v139,v22,v23\n"
CV "v138,v20,v21\n"
CV "v137,v18,v19\n"
CV "v136,v16,v17\n"
"s_cmp_eq_u32 s57,s69\n"
"s_cbranch_scc1 .LattB\n"
AR "v2,a245\n"
"v_add_u32 v2,49152,v2\n"
"v_xor_b32 v3,32,v2\n"
"v_xor_b32 v4,64,v2\n"
"v_xor_b32 v5,96,v2\n"
"s_mov_b32 s61,7\n"
".Latt2x:\n"
"s_cmp_eq_u32 s57,1\n"
"s_cbranch_scc1 .Lt0kx\n"
".Lt0nx:\n"
"s_waitcnt lgkmcnt(2)\n"
MF "v[72:75],v[92:95],v[136:139],0\n"
"s_add_u32 s73,s73,12288\n"
"ds_read_b128 v[104:107],v1 offset:1024\n"
AR "v10,a224\n"
MF "v[76:79],v[92:95],v[140:143],0\n"
"s_cmp_eq_u32 s73,s19\n"
"s_cselect_b32 s73,s18,s73\n"
"s_add_u32 m0,s65,73728\n"
MF "v[80:83],v[92:95],v[144:147],0\n"
LD "v8,s[36:39],s73 offen nt lds\n"
MF "v[84:87],v[92:95],v[148:151],0\n"
"v_max_f32_dpp v10,|v10|,|v10| quad_perm:[1,0,3,2] row_mask:0xf bank_mask:0xf\n"
AR "v11,a225\n"
MF "v[88:91],v[92:95],v[152:155],0\n"
"s_waitcnt lgkmcnt(2)\n"
"ds_read_b128 v[92:95],v0 offset:2048\n"
AR "v12,a226\n"
MF "v[72:75],v[96:99],v[156:159],v[72:75]\n"
"s_add_u32 m0,s65,74624\n"
"v_max_f32_dpp v10,v10,v10 quad_perm:[2,3,0,1] row_mask:0xf bank_mask:0xf\n"
"v_max_f32_dpp v11,|v11|,|v11| quad_perm:[1,0,3,2] row_mask:0xf bank_mask:0xf\n"
MF "v[76:79],v[96:99],v[160:163],v[76:79]\n"
LD "v8,s[36:39],s73 offen offset:128 nt lds\n"
MF "v[80:83],v[96:99],v[164:167],v[80:83]\n"
"v_max_f32_dpp v10,v10,v10 row_half_mirror row_mask:0xf bank_mask:0xf\n"
"v_max_f32_dpp v11,v11,v11 quad_perm:[2,3,0,1] row_mask:0xf bank_mask:0xf\n"
MF "v[84:87],v[96:99],v[168:171],v[84:87]\n"
"v_readlane_b32 s13,v10,0\n"
"v_readlane_b32 s0,v10,24\n"
MF "v[88:91],v[96:99],v[172:175],v[88:91]\n"
"s_waitcnt lgkmcnt(2)\n"
"ds_read_b128 v[96:99],v1 offset:2048\n"
"v_readlane_b32 s1,v10,32\n"
MF "v[72:75],v[100:103],v[176:179],v[72:75]\n"
"s_add_u32 m0,s65,75520\n"
"v_readlane_b32 s2,v10,56\n"
"v_max_f32_dpp v11,v11,v11 row_half_mirror row_mask:0xf bank_mask:0xf\n"
MF "v[76:79],v[100:103],v[180:183],v[76:79]\n"
LD "v8,s[36:39],s73 offen offset:256 nt lds\n"
MF "v[80:83],v[100:103],v[184:187],v[80:83]\n"
"s_min_u32 s13,s13,s0\n"
"v_readlane_b32 s11,v11,0\n"
"v_readlane_b32 s3,v11,24\n"
MF "v[84:87],v[100:103],v[188:191],v[84:87]\n"
"s_min_u32 s1,s1,s2\n"
"v_readlane_b32 s4,v11,32\n"
"v_readlane_b32 s5,v11,56\n"
MF "v[88:91],v[100:103],v[192:195],v[88:91]\n"
"s_waitcnt lgkmcnt(2)\n"
"ds_read_b128 v[100:103],v0 offset:6144\n"
"v_max_f32_dpp v12,|v12|,|v12| quad_perm:[1,0,3,2] row_mask:0xf bank_mask:0xf\n"
MF "v[72:75],v[104:107],v[196:199],v[72:75]\n"
"s_add_u32 m0,s65,73728\n"
MF "v[76:79],v[104:107],v[200:203],v[76:79]\n"
LD "v8,s[36:39],s73 offen offset:3072 nt lds\n"
MF "v[80:83],v[104:107],v[204:207],v[80:83]\n"
"s_min_u32 s13,s13,s1\n"
"v_max_f32_dpp v12,v12,v12 quad_perm:[2,3,0,1] row_mask:0xf bank_mask:0xf\n"
MF "v[84:87],v[104:107],v[208:211],v[84:87]\n"
"s_and_b32 s13,s13,0x7f800000\n"
MF "v[88:91],v[104:107],v[212:215],v[88:91]\n"
"s_waitcnt lgkmcnt(2)\n"
"ds_read_b128 v[104:107],v1 offset:6144\n"
"v_max_f32_dpp v12,v12,v12 row_half_mirror row_mask:0xf bank_mask:0xf\n"
MF "v[72:75],v[92:95],v[216:219],v[72:75]\n"
"s_add_u32 m0,s65,74624\n"
"v_readlane_b32 s10,v12,0\n"
"v_readlane_b32 s6,v12,24\n"
MF "v[76:79],v[92:95],v[220:223],v[76:79]\n"
LD "v8,s[36:39],s73 offen offset:3200 nt lds\n"
MF "v[80:83],v[92:95],v[224:227],v[80:83]\n"
"s_sub_u32 s13,s13,0x14000000\n"
"v_readlane_b32 s7,v12,32\n"
"v_readlane_b32 s8,v12,56\n"
MF "v[84:87],v[92:95],v[228:231],v[84:87]\n"
"s_cmp_lt_u32 s13,0x50000000\n"
"s_cselect_b32 s13,s13,0xed400000\n"
MF "v[88:91],v[92:95],v[232:235],v[88:91]\n"
"s_waitcnt lgkmcnt(2)\n"
"ds_read_b128 v[92:95],v0 offset:7168\n"
MF "v[72:75],v[96:99],v[236:239],v[72:75]\n"
"s_add_u32 m0,s65,75520\n"
MF "v[76:79],v[96:99],v[240:243],v[76:79]\n"
LD "v8,s[36:39],s73 offen offset:3328 nt lds\n"
MF "v[80:83],v[96:99],v[244:247],v[80:83]\n"
"s_add_u32 s13,s13,0x12c00000\n"
MF "v[84:87],v[96:99],v[248:251],v[84:87]\n"
"s_min_u32 s11,s11,s3\n"
MF "v[88:91],v[96:99],v[252:255],v[88:91]\n"
"s_waitcnt lgkmcnt(2)\n"
"ds_read_b128 v[96:99],v1 offset:7168\n"
MF "v[112:115],v[100:103],v[136:139],0\n"
"s_min_u32 s4,s4,s5\n"
MF "v[116:119],v[100:103],v[140:143],0\n"
"s_min_u32 s11,s11,s4\n"
MF "v[120:123],v[100:103],v[144:147],0\n"
"s_and_b32 s11,s11,0x7f800000\n"
MF "v[124:127],v[100:103],v[148:151],0\n"
"s_sub_u32 s11,s11,0x14000000\n"
MF "v[128:131],v[100:103],v[152:155],0\n"
"s_waitcnt lgkmcnt(2)\n"
"ds_read_b128 v[100:103],v0 offset:8192\n"
MF "v[112:115],v[104:107],v[156:159],v[112:115]\n"
"s_min_u32 s10,s10,s6\n"
"v_max3_f32 v52,v72,v73,v74\n"
"v_max_f32 v52,v52,v75\n"
MF "v[116:119],v[104:107],v[160:163],v[116:119]\n"
"s_min_u32 s7,s7,s8\n"
"v_max3_f32 v53,v76,v77,v78\n"
"v_max_f32 v53,v53,v79\n"
MF "v[120:123],v[104:107],v[164:167],v[120:123]\n"
"s_min_u32 s10,s10,s7\n"
"v_max3_f32 v54,v80,v81,v82\n"
"v_max_f32 v54,v54,v83\n"
MF "v[124:127],v[104:107],v[168:171],v[124:127]\n"
".Lpg9b:\n"
"s_cbranch_execz .Lpgpsl_b\n"
"v_max3_f32 v55,v84,v85,v86\n"
"v_max_f32 v55,v55,v87\n"
MF "v[128:131],v[104:107],v[172:175],v[128:131]\n"
"s_waitcnt lgkmcnt(2)\n"
"ds_read_b128 v[104:107],v1 offset:8192\n"
"v_max3_f32 v56,v88,v89,v90\n"
MF "v[112:115],v[92:95],v[176:179],v[112:115]\n"
"s_cmp_lt_u32 s11,0x50000000\n"
"s_cselect_b32 s11,s11,0xed400000\n"
"v_max_f32 v56,v56,v91\n"
MF "v[116:119],v[92:95],v[180:183],v[116:119]\n"
"s_add_u32 s11,s11,0x12c00000\n"
MF "v[120:123],v[92:95],v[184:187],v[120:123]\n"
MF "v[124:127],v[92:95],v[188:191],v[124:127]\n"
MF "v[128:131],v[92:95],v[192:195],v[128:131]\n"
"s_waitcnt lgkmcnt(2)\n"
MF "v[112:115],v[96:99],v[196:199],v[112:115]\n"
"s_waitcnt lgkmcnt(0)\n"
MF "v[116:119],v[96:99],v[200:203],v[116:119]\n"
"s_barrier\n"
MF "v[120:123],v[96:99],v[204:207],v[120:123]\n"
"s_add_u32 s72,s72,6144\n"
MF "v[124:127],v[96:99],v[208:211],v[124:127]\n"
"s_cmp_eq_u32 s72,s17\n"
"s_cselect_b32 s72,s16,s72\n"
"s_add_u32 m0,s64,0\n"
MF "v[128:131],v[96:99],v[212:215],v[128:131]\n"
LD "v7,s[32:35],s72 offen nt lds\n"
MF "v[112:115],v[100:103],v[216:219],v[112:115]\n"
"s_add_u32 m0,s64,896\n"
MF "v[116:119],v[100:103],v[220:223],v[116:119]\n"
LD "v7,s[32:35],s72 offen offset:128 nt lds\n"
MF "v[120:123],v[100:103],v[224:227],v[120:123]\n"
"s_add_u32 m0,s64,1792\n"
MF "v[124:127],v[100:103],v[228:231],v[124:127]\n"
LD "v7,s[32:35],s72 offen offset:256 nt lds\n"
MF "v[128:131],v[100:103],v[232:235],v[128:131]\n"
"s_add_u32 s72,s72,6144\n"
"s_add_u32 m0,s64,6144\n"
MF "v[112:115],v[104:107],v[236:239],v[112:115]\n"
LD "v7,s[32:35],s72 offen nt lds\n"
MF "v[116:119],v[104:107],v[240:243],v[116:119]\n"
"s_add_u32 m0,s64,7040\n"
MF "v[120:123],v[104:107],v[244:247],v[120:123]\n"
LD "v7,s[32:35],s72 offen offset:128 nt lds\n"
MF "v[124:127],v[104:107],v[248:251],v[124:127]\n"
"s_add_u32 m0,s64,7936\n"
MF "v[128:131],v[104:107],v[252:255],v[128:131]\n"
LD "v7,s[32:35],s72 offen offset:256 nt lds\n"
"s_branch .Lt0ex\n"
".Lpgt0kx_d:\n"
"s_cbranch_execz .Lisr2\n"
".Lt0kx:\n"
"s_lshl_b32 s58,s56,10\n"
"s_add_u32 s58,s58,73728\n"
"v_add_u32 v32,s58,v9\n"
"v_add_u32 v33,65536,v32\n"
"s_waitcnt lgkmcnt(2)\n"
MF "v[72:75],v[92:95],v[136:139],0\n"
"s_add_u32 s73,s73,12288\n"
"ds_read_b128 v[104:107],v1 offset:1024\n"
AR "v10,a224\n"
MF "v[76:79],v[92:95],v[140:143],0\n"
"s_cmp_eq_u32 s73,s19\n"
"s_cselect_b32 s73,s18,s73\n"
"s_add_u32 m0,s65,73728\n"
MF "v[80:83],v[92:95],v[144:147],0\n"
LD "v8,s[36:39],s73 offen nt lds\n"
MF "v[84:87],v[92:95],v[148:151],0\n"
"v_max_f32_dpp v10,|v10|,|v10| quad_perm:[1,0,3,2] row_mask:0xf bank_mask:0xf\n"
AR "v11,a225\n"
MF "v[88:91],v[92:95],v[152:155],0\n"
"s_waitcnt lgkmcnt(2)\n"
"ds_read_b128 v[92:95],v0 offset:2048\n"
AR "v12,a226\n"
MF "v[72:75],v[96:99],v[156:159],v[72:75]\n"
"s_add_u32 m0,s65,74624\n"
"v_max_f32_dpp v10,v10,v10 quad_perm:[2,3,0,1] row_mask:0xf bank_mask:0xf\n"
"v_max_f32_dpp v11,|v11|,|v11| quad_perm:[1,0,3,2] row_mask:0xf bank_mask:0xf\n"
MF "v[76:79],v[96:99],v[160:163],v[76:79]\n"
LD "v8,s[36:39],s73 offen offset:128 nt lds\n"
MF "v[80:83],v[96:99],v[164:167],v[80:83]\n"
"v_max_f32_dpp v10,v10,v10 row_half_mirror row_mask:0xf bank_mask:0xf\n"
"v_max_f32_dpp v11,v11,v11 quad_perm:[2,3,0,1] row_mask:0xf bank_mask:0xf\n"
MF "v[84:87],v[96:99],v[168:171],v[84:87]\n"
"v_readlane_b32 s13,v10,0\n"
"v_readlane_b32 s0,v10,24\n"
MF "v[88:91],v[96:99],v[172:175],v[88:91]\n"
"s_waitcnt lgkmcnt(2)\n"
"ds_read_b128 v[96:99],v1 offset:2048\n"
"v_readlane_b32 s1,v10,32\n"
MF "v[72:75],v[100:103],v[176:179],v[72:75]\n"
"s_add_u32 m0,s65,75520\n"
"v_readlane_b32 s2,v10,56\n"
"v_max_f32_dpp v11,v11,v11 row_half_mirror row_mask:0xf bank_mask:0xf\n"
MF "v[76:79],v[100:103],v[180:183],v[76:79]\n"
LD "v8,s[36:39],s73 offen offset:256 nt lds\n"
MF "v[80:83],v[100:103],v[184:187],v[80:83]\n"
"s_min_u32 s13,s13,s0\n"
"v_readlane_b32 s11,v11,0\n"
"v_readlane_b32 s3,v11,24\n"
MF "v[84:87],v[100:103],v[188:191],v[84:87]\n"
"s_min_u32 s1,s1,s2\n"
"v_readlane_b32 s4,v11,32\n"
"v_readlane_b32 s5,v11,56\n"
MF "v[88:91],v[100:103],v[192:195],v[88:91]\n"
"s_waitcnt lgkmcnt(2)\n"
"ds_read_b128 v[100:103],v0 offset:6144\n"
"v_max_f32_dpp v12,|v12|,|v12| quad_perm:[1,0,3,2] row_mask:0xf bank_mask:0xf\n"
MF "v[72:75],v[104:107],v[196:199],v[72:75]\n"
"s_add_u32 m0,s65,73728\n"
"ds_write_b128 v32,a[0:3] offset:24576\n"
MF "v[76:79],v[104:107],v[200:203],v[76:79]\n"
LD "v8,s[36:39],s73 offen offset:3072 nt lds\n"
MF "v[80:83],v[104:107],v[204:207],v[80:83]\n"
"s_min_u32 s13,s13,s1\n"
"v_max_f32_dpp v12,v12,v12 quad_perm:[2,3,0,1] row_mask:0xf bank_mask:0xf\n"
AR "v62,a20\n"
MF "v[84:87],v[104:107],v[208:211],v[84:87]\n"
"s_and_b32 s13,s13,0x7f800000\n"
"ds_write_b128 v32,a[4:7] offset:28672\n"
MF "v[88:91],v[104:107],v[212:215],v[88:91]\n"
"s_waitcnt lgkmcnt(4)\n"
"ds_read_b128 v[104:107],v1 offset:6144\n"
"v_max_f32_dpp v12,v12,v12 row_half_mirror row_mask:0xf bank_mask:0xf\n"
MF "v[72:75],v[92:95],v[216:219],v[72:75]\n"
"s_add_u32 m0,s65,74624\n"
"v_readlane_b32 s10,v12,0\n"
"v_readlane_b32 s6,v12,24\n"
MF "v[76:79],v[92:95],v[220:223],v[76:79]\n"
LD "v8,s[36:39],s73 offen offset:3200 nt lds\n"
MF "v[80:83],v[92:95],v[224:227],v[80:83]\n"
"s_sub_u32 s13,s13,0x14000000\n"
"v_readlane_b32 s7,v12,32\n"
"v_readlane_b32 s8,v12,56\n"
MF "v[84:87],v[92:95],v[228:231],v[84:87]\n"
"s_cmp_lt_u32 s13,0x50000000\n"
"s_cselect_b32 s13,s13,0xed400000\n"
AR "v63,a21\n"
MF "v[88:91],v[92:95],v[232:235],v[88:91]\n"
"s_waitcnt lgkmcnt(4)\n"
"ds_read_b128 v[92:95],v0 offset:7168\n"
AR "v64,a22\n"
MF "v[72:75],v[96:99],v[236:239],v[72:75]\n"
"s_add_u32 m0,s65,75520\n"
"ds_write_b128 v32,a[8:11] offset:32768\n"
MF "v[76:79],v[96:99],v[240:243],v[76:79]\n"
LD "v8,s[36:39],s73 offen offset:3328 nt lds\n"
MF "v[80:83],v[96:99],v[244:247],v[80:83]\n"
"s_add_u32 s13,s13,0x12c00000\n"
"ds_write_b128 v32,a[12:15] offset:36864\n"
MF "v[84:87],v[96:99],v[248:251],v[84:87]\n"
"s_min_u32 s11,s11,s3\n"
"ds_write_b128 v32,a[16:19] offset:40960\n"
MF "v[88:91],v[96:99],v[252:255],v[88:91]\n"
"s_waitcnt lgkmcnt(7)\n"
"ds_read_b128 v[96:99],v1 offset:7168\n"
AR "v65,a23\n"
MF "v[112:115],v[100:103],v[136:139],0\n"
"s_min_u32 s4,s4,s5\n"
"ds_write_b128 v32,a[40:43] offset:45056\n"
MF "v[116:119],v[100:103],v[140:143],0\n"
"s_min_u32 s11,s11,s4\n"
"ds_write_b128 v32,a[44:47] offset:49152\n"
MF "v[120:123],v[100:103],v[144:147],0\n"
"s_and_b32 s11,s11,0x7f800000\n"
"ds_write_b128 v32,a[48:51] offset:53248\n"
MF "v[124:127],v[100:103],v[148:151],0\n"
"s_sub_u32 s11,s11,0x14000000\n"
"ds_write_b128 v32,a[52:55] offset:57344\n"
MF "v[128:131],v[100:103],v[152:155],0\n"
"s_waitcnt lgkmcnt(9)\n"
"ds_read_b128 v[100:103],v0 offset:8192\n"
AR "v66,a24\n"
MF "v[112:115],v[104:107],v[156:159],v[112:115]\n"
"s_min_u32 s10,s10,s6\n"
"v_max3_f32 v52,v72,v73,v74\n"
"v_max_f32 v52,v52,v75\n"
MF "v[116:119],v[104:107],v[160:163],v[116:119]\n"
"s_min_u32 s7,s7,s8\n"
"v_max3_f32 v53,v76,v77,v78\n"
"v_max_f32 v53,v53,v79\n"
MF "v[120:123],v[104:107],v[164:167],v[120:123]\n"
"s_min_u32 s10,s10,s7\n"
"v_max3_f32 v54,v80,v81,v82\n"
"v_max_f32 v54,v54,v83\n"
MF "v[124:127],v[104:107],v[168:171],v[124:127]\n"
"v_max3_f32 v55,v84,v85,v86\n"
"v_max_f32 v55,v55,v87\n"
MF "v[128:131],v[104:107],v[172:175],v[128:131]\n"
"s_waitcnt lgkmcnt(9)\n"
"ds_read_b128 v[104:107],v1 offset:8192\n"
"v_max3_f32 v56,v88,v89,v90\n"
MF "v[112:115],v[92:95],v[176:179],v[112:115]\n"
"s_cmp_lt_u32 s11,0x50000000\n"
"s_cselect_b32 s11,s11,0xed400000\n"
"v_max_f32 v56,v56,v91\n"
MF "v[116:119],v[92:95],v[180:183],v[116:119]\n"
"s_add_u32 s11,s11,0x12c00000\n"
"ds_write_b128 v32,a[56:59] offset:61440\n"
MF "v[120:123],v[92:95],v[184:187],v[120:123]\n"
"ds_write_b128 v33,a[80:83]\n"
MF "v[124:127],v[92:95],v[188:191],v[124:127]\n"
"ds_write_b128 v33,a[84:87] offset:4096\n"
MF "v[128:131],v[92:95],v[192:195],v[128:131]\n"
"s_waitcnt lgkmcnt(9)\n"
"ds_write_b128 v33,a[88:91] offset:8192\n"
MF "v[112:115],v[96:99],v[196:199],v[112:115]\n"
"s_waitcnt lgkmcnt(0)\n"
AR "v67,a25\n"
AR "v68,a26\n"
MF "v[116:119],v[96:99],v[200:203],v[116:119]\n"
"s_barrier\n"
MF "v[120:123],v[96:99],v[204:207],v[120:123]\n"
"s_add_u32 s72,s72,6144\n"
"ds_write_b128 v33,a[92:95] offset:12288\n"
MF "v[124:127],v[96:99],v[208:211],v[124:127]\n"
"s_cmp_eq_u32 s72,s17\n"
"s_cselect_b32 s72,s16,s72\n"
"s_add_u32 m0,s64,0\n"
MF "v[128:131],v[96:99],v[212:215],v[128:131]\n"
LD "v7,s[32:35],s72 offen nt lds\n"
MF "v[112:115],v[100:103],v[216:219],v[112:115]\n"
"s_add_u32 m0,s64,896\n"
"ds_write_b128 v33,a[96:99] offset:16384\n"
MF "v[116:119],v[100:103],v[220:223],v[116:119]\n"
LD "v7,s[32:35],s72 offen offset:128 nt lds\n"
MF "v[120:123],v[100:103],v[224:227],v[120:123]\n"
"s_add_u32 m0,s64,1792\n"
AR "v69,a27\n"
AR "v70,a28\n"
MF "v[124:127],v[100:103],v[228:231],v[124:127]\n"
LD "v7,s[32:35],s72 offen offset:256 nt lds\n"
MF "v[128:131],v[100:103],v[232:235],v[128:131]\n"
"s_add_u32 s72,s72,6144\n"
"s_add_u32 m0,s64,6144\n"
AR "v71,a29\n"
MF "v[112:115],v[104:107],v[236:239],v[112:115]\n"
LD "v7,s[32:35],s72 offen nt lds\n"
MF "v[116:119],v[104:107],v[240:243],v[116:119]\n"
"s_add_u32 m0,s64,7040\n"
"ds_write_b128 v33,a[120:123] offset:20480\n"
MF "v[120:123],v[104:107],v[244:247],v[120:123]\n"
LD "v7,s[32:35],s72 offen offset:128 nt lds\n"
MF "v[124:127],v[104:107],v[248:251],v[124:127]\n"
"s_add_u32 m0,s64,7936\n"
AR "v132,a30\n"
AR "v133,a31\n"
MF "v[128:131],v[104:107],v[252:255],v[128:131]\n"
LD "v7,s[32:35],s72 offen offset:256 nt lds\n"
AR "v134,a32\n"
AR "v135,a33\n"
AR "v6,a34\n"
AR "v13,a35\n"
AR "v14,a36\n"
AR "v15,a37\n"
"v_accvgpr_mov_b32 a254,a38\n"
"v_accvgpr_mov_b32 a255,a39\n"
"v_accvgpr_mov_b32 a240,a168\n"
"v_accvgpr_mov_b32 a241,a169\n"
"v_accvgpr_mov_b32 a242,a170\n"
"v_accvgpr_mov_b32 a243,a171\n"
"v_accvgpr_mov_b32 a244,a172\n"
"v_accvgpr_mov_b32 a245,a173\n"
"v_accvgpr_mov_b32 a246,a174\n"
"v_accvgpr_mov_b32 a247,a175\n"
"v_accvgpr_mov_b32 a248,a176\n"
"v_accvgpr_mov_b32 a249,a177\n"
"v_accvgpr_mov_b32 a250,a178\n"
"v_accvgpr_mov_b32 a251,a179\n"
".Lpgt0ex_b:\n"
"s_cbranch_execz .Lpg2a\n"
".Lt0ex:\n"
"s_nop 1\n"
"v_max3_f32 v52,v52,v112,v113\n"
"v_max3_f32 v53,v53,v116,v117\n"
"v_max3_f32 v54,v54,v120,v121\n"
"v_max3_f32 v55,v55,v124,v125\n"
"v_max3_f32 v56,v56,v128,v129\n"
"v_max3_f32 v52,v52,v114,v115\n"
"v_max3_f32 v53,v53,v118,v119\n"
"v_max3_f32 v54,v54,v122,v123\n"
"v_max3_f32 v55,v55,v126,v127\n"
"v_max3_f32 v56,v56,v130,v131\n"
"v_mov_b32 v57,v52\n"
"v_mov_b32 v58,v53\n"
"v_mov_b32 v59,v54\n"
"v_mov_b32 v60,v55\n"
"v_mov_b32 v61,v56\n"
"v_permlane16_swap_b32 v52,v57\n"
"v_permlane16_swap_b32 v53,v58\n"
"v_permlane16_swap_b32 v54,v59\n"
"v_permlane16_swap_b32 v55,v60\n"
"v_permlane16_swap_b32 v56,v61\n"
"v_max_f32 v52,v52,v57\n"
"v_max_f32 v53,v53,v58\n"
"v_max_f32 v54,v54,v59\n"
"v_max_f32 v55,v55,v60\n"
"v_max_f32 v56,v56,v61\n"
"v_mov_b32 v57,v52\n"
"v_mov_b32 v58,v53\n"
"v_mov_b32 v59,v54\n"
"v_mov_b32 v60,v55\n"
"v_mov_b32 v61,v56\n"
"v_permlane32_swap_b32 v52,v57\n"
"v_permlane32_swap_b32 v53,v58\n"
"v_permlane32_swap_b32 v54,v59\n"
"v_permlane32_swap_b32 v55,v60\n"
"v_permlane32_swap_b32 v56,v61\n"
"v_max_f32 v52,v52,v57\n"
"v_max_f32 v53,v53,v58\n"
"v_max_f32 v54,v54,v59\n"
"v_max_f32 v55,v55,v60\n"
"v_max_f32 v56,v56,v61\n"
"v_sub_f32 v32,0xc1300000,v52\n"
"v_sub_f32 v36,0xc1300000,v53\n"
"v_sub_f32 v40,0xc1300000,v54\n"
"v_sub_f32 v44,0xc1300000,v55\n"
"v_sub_f32 v48,0xc1300000,v56\n"
"v_mov_b32_e64 v33,v32\n"
"v_pk_mov_b32 v[34:35],v[32:33],v[32:33]\n"
"v_mov_b32 v37,v36\n"
"v_pk_mov_b32 v[38:39],v[36:37],v[36:37]\n"
"v_mov_b32 v41,v40\n"
"v_pk_mov_b32 v[42:43],v[40:41],v[40:41]\n"
"v_mov_b32 v45,v44\n"
"v_pk_mov_b32 v[46:47],v[44:45],v[44:45]\n"
"v_mov_b32 v49,v48\n"
"v_pk_mov_b32 v[50:51],v[48:49],v[48:49]\n"
"v_pk_add_f32 v[72:73],v[72:73],v[32:33]\n"
"v_pk_add_f32 v[74:75],v[74:75],v[32:33]\n"
"v_pk_add_f32 v[112:113],v[112:113],v[32:33]\n"
"v_pk_add_f32 v[114:115],v[114:115],v[32:33]\n"
"v_exp_f32 v72,v72\n"
"v_exp_f32 v73,v73\n"
"v_exp_f32 v74,v74\n"
"v_exp_f32 v75,v75\n"
"v_exp_f32 v112,v112\n"
"v_exp_f32 v113,v113\n"
"v_exp_f32 v114,v114\n"
"v_exp_f32 v115,v115\n"
"v_pk_add_f32 v[10:11],v[72:73],v[74:75]\n"
"v_pk_add_f32 v[28:29],v[112:113],v[114:115]\n"
"v_pk_add_f32 v[10:11],v[10:11],v[28:29]\n"
"v_add_f32_e64 v52,v10,v11\n"
CV "v72,v72,v73\n"
CV "v73,v74,v75\n"
CV "v74,v112,v113\n"
CV "v75,v114,v115\n"
"v_pk_add_f32 v[76:77],v[76:77],v[36:37]\n"
"v_pk_add_f32 v[78:79],v[78:79],v[36:37]\n"
"v_pk_add_f32 v[116:117],v[116:117],v[36:37]\n"
"v_pk_add_f32 v[118:119],v[118:119],v[36:37]\n"
"v_exp_f32 v76,v76\n"
"v_exp_f32 v77,v77\n"
"v_exp_f32 v78,v78\n"
"v_exp_f32 v79,v79\n"
"v_exp_f32 v116,v116\n"
"v_exp_f32 v117,v117\n"
"v_exp_f32 v118,v118\n"
"v_exp_f32 v119,v119\n"
"v_pk_add_f32 v[10:11],v[76:77],v[78:79]\n"
"v_pk_add_f32 v[28:29],v[116:117],v[118:119]\n"
"v_pk_add_f32 v[10:11],v[10:11],v[28:29]\n"
"v_add_f32_e64 v53,v10,v11\n"
CV "v76,v76,v77\n"
CV "v77,v78,v79\n"
CV "v78,v116,v117\n"
CV "v79,v118,v119\n"
"v_pk_add_f32 v[80:81],v[80:81],v[40:41]\n"
"v_pk_add_f32 v[82:83],v[82:83],v[40:41]\n"
"v_pk_add_f32 v[120:121],v[120:121],v[40:41]\n"
"v_pk_add_f32 v[122:123],v[122:123],v[40:41]\n"
"v_exp_f32 v80,v80\n"
"v_exp_f32 v81,v81\n"
"v_exp_f32 v82,v82\n"
"v_exp_f32 v83,v83\n"
"v_exp_f32 v120,v120\n"
"v_exp_f32 v121,v121\n"
"v_exp_f32 v122,v122\n"
"v_exp_f32 v123,v123\n"
"v_pk_add_f32 v[10:11],v[80:81],v[82:83]\n"
"v_pk_add_f32 v[28:29],v[120:121],v[122:123]\n"
"v_pk_add_f32 v[10:11],v[10:11],v[28:29]\n"
"v_add_f32_e64 v54,v10,v11\n"
CV "v80,v80,v81\n"
CV "v81,v82,v83\n"
CV "v82,v120,v121\n"
CV "v83,v122,v123\n"
"v_pk_add_f32 v[84:85],v[84:85],v[44:45]\n"
"v_pk_add_f32 v[86:87],v[86:87],v[44:45]\n"
"v_pk_add_f32 v[124:125],v[124:125],v[44:45]\n"
"v_pk_add_f32 v[126:127],v[126:127],v[44:45]\n"
"v_mov_b32_e64 v55,0\n"
"v_pk_add_f32 v[88:89],v[88:89],v[48:49]\n"
"v_pk_add_f32 v[90:91],v[90:91],v[48:49]\n"
"v_pk_add_f32 v[128:129],v[128:129],v[48:49]\n"
"v_pk_add_f32 v[130:131],v[130:131],v[48:49]\n"
"v_mov_b32 v56,0\n"
"s_waitcnt vmcnt(18) lgkmcnt(0)\n"
"s_barrier\n"
"ds_read_b128 v[16:19],v0 offset:24576\n"
"ds_read_b128 v[20:23],v1 offset:24576\n"
AW "a220,s13\n"
AW "a221,s13\n"
AW "a222,s13\n"
AW "a223,s13\n"
"s_waitcnt lgkmcnt(1)\n"
MF "v[92:95],v[16:19],v[136:139],v[32:35]\n"
"s_nop 0\n"
"ds_read_b128 v[24:27],v0 offset:25600\n"
"ds_read_b128 v[28:31],v1 offset:25600\n"
"s_waitcnt lgkmcnt(2)\n"
MF "v[92:95],v[20:23],v[156:159],v[92:95]\n"
"v_exp_f32 v84,v84\n"
MF "v[96:99],v[20:23],v[160:163],v[36:39]\n"
"v_exp_f32 v85,v85\n"
MF "v[96:99],v[16:19],v[140:143],v[96:99]\n"
"v_exp_f32 v86,v86\n"
MF "v[100:103],v[16:19],v[144:147],v[40:43]\n"
"v_exp_f32 v87,v87\n"
MF "v[100:103],v[20:23],v[164:167],v[100:103]\n"
"v_add_f32 v55,v55,v84\n"
"v_add_f32 v55,v55,v85\n"
MF "v[104:107],v[20:23],v[168:171],v[44:47]\n"
CV "v84,v84,v85\n"
"v_add_f32 v55,v55,v86\n"
MF "v[104:107],v[16:19],v[148:151],v[104:107]\n"
"v_add_f32 v55,v55,v87\n"
CV "v85,v86,v87\n"
MF "v[108:111],v[16:19],v[152:155],v[48:51]\n"
"v_exp_f32 v124,v124\n"
MF "v[108:111],v[20:23],v[172:175],v[108:111]\n"
"s_waitcnt lgkmcnt(0)\n"
"ds_read_b128 v[16:19],v0 offset:26624\n"
"ds_read_b128 v[20:23],v1 offset:26624\n"
MF "v[108:111],v[24:27],v[192:195],v[108:111]\n"
"v_exp_f32 v125,v125\n"
MF "v[108:111],v[28:31],v[212:215],v[108:111]\n"
"v_exp_f32 v126,v126\n"
MF "v[104:107],v[28:31],v[208:211],v[104:107]\n"
"v_exp_f32 v127,v127\n"
MF "v[104:107],v[24:27],v[188:191],v[104:107]\n"
"v_add_f32 v55,v55,v124\n"
"v_add_f32 v55,v55,v125\n"
MF "v[100:103],v[24:27],v[184:187],v[100:103]\n"
CV "v86,v124,v125\n"
"v_add_f32 v55,v55,v126\n"
MF "v[100:103],v[28:31],v[204:207],v[100:103]\n"
"v_add_f32 v55,v55,v127\n"
CV "v87,v126,v127\n"
MF "v[96:99],v[28:31],v[200:203],v[96:99]\n"
"v_exp_f32 v88,v88\n"
MF "v[96:99],v[24:27],v[180:183],v[96:99]\n"
"v_exp_f32 v89,v89\n"
MF "v[92:95],v[24:27],v[176:179],v[92:95]\n"
"v_exp_f32 v90,v90\n"
MF "v[92:95],v[28:31],v[196:199],v[92:95]\n"
"s_waitcnt lgkmcnt(0)\n"
"ds_read_b128 v[24:27],v0 offset:30720\n"
"ds_read_b128 v[28:31],v1 offset:30720\n"
MF "v[92:95],v[16:19],v[216:219],v[92:95]\n"
"v_exp_f32 v91,v91\n"
MF "v[92:95],v[20:23],v[236:239],v[92:95]\n"
"v_add_f32 v56,v56,v88\n"
"v_add_f32 v56,v56,v89\n"
MF "v[96:99],v[20:23],v[240:243],v[96:99]\n"
CV "v88,v88,v89\n"
"v_add_f32 v56,v56,v90\n"
MF "v[96:99],v[16:19],v[220:223],v[96:99]\n"
"v_add_f32 v56,v56,v91\n"
CV "v89,v90,v91\n"
MF "v[100:103],v[16:19],v[224:227],v[100:103]\n"
"v_exp_f32 v128,v128\n"
MF "v[100:103],v[20:23],v[244:247],v[100:103]\n"
"v_exp_f32 v129,v129\n"
MF "v[104:107],v[20:23],v[248:251],v[104:107]\n"
"v_exp_f32 v130,v130\n"
MF "v[104:107],v[16:19],v[228:231],v[104:107]\n"
"v_exp_f32 v131,v131\n"
MF "v[108:111],v[16:19],v[232:235],v[108:111]\n"
"v_add_f32 v56,v56,v128\n"
"v_add_f32 v56,v56,v129\n"
MF "v[108:111],v[20:23],v[252:255],v[108:111]\n"
"s_waitcnt lgkmcnt(0)\n"
"ds_read_b128 v[16:19],v0 offset:31744\n"
"ds_read_b128 v[20:23],v1 offset:31744\n"
MF "v[112:115],v[24:27],v[136:139],v[32:35]\n"
CV "v90,v128,v129\n"
"v_add_f32 v56,v56,v130\n"
MF "v[112:115],v[28:31],v[156:159],v[112:115]\n"
"v_add_f32 v56,v56,v131\n"
CV "v91,v130,v131\n"
MF "v[116:119],v[28:31],v[160:163],v[36:39]\n"
"v_exp_f32 v92,v92\n"
MF "v[116:119],v[24:27],v[140:143],v[116:119]\n"
"v_exp_f32 v93,v93\n"
MF "v[120:123],v[24:27],v[144:147],v[40:43]\n"
"v_exp_f32 v94,v94\n"
MF "v[120:123],v[28:31],v[164:167],v[120:123]\n"
"v_exp_f32 v95,v95\n"
MF "v[124:127],v[28:31],v[168:171],v[44:47]\n"
"v_add_f32 v57,v52,v92\n"
"v_add_f32 v57,v57,v93\n"
MF "v[124:127],v[24:27],v[148:151],v[124:127]\n"
CV "v92,v92,v93\n"
"v_add_f32 v57,v57,v94\n"
MF "v[128:131],v[24:27],v[152:155],v[48:51]\n"
"v_add_f32 v57,v57,v95\n"
CV "v93,v94,v95\n"
MF "v[128:131],v[28:31],v[172:175],v[128:131]\n"
"s_waitcnt lgkmcnt(0)\n"
"ds_read_b128 v[24:27],v0 offset:32768\n"
"ds_read_b128 v[28:31],v1 offset:32768\n"
MF "v[128:131],v[16:19],v[192:195],v[128:131]\n"
"v_exp_f32 v96,v96\n"
MF "v[128:131],v[20:23],v[212:215],v[128:131]\n"
"v_exp_f32 v97,v97\n"
MF "v[124:127],v[20:23],v[208:211],v[124:127]\n"
"v_exp_f32 v98,v98\n"
MF "v[124:127],v[16:19],v[188:191],v[124:127]\n"
"v_exp_f32 v99,v99\n"
MF "v[120:123],v[16:19],v[184:187],v[120:123]\n"
"v_add_f32 v58,v53,v96\n"
"v_add_f32 v58,v58,v97\n"
MF "v[120:123],v[20:23],v[204:207],v[120:123]\n"
CV "v96,v96,v97\n"
"v_add_f32 v58,v58,v98\n"
MF "v[116:119],v[20:23],v[200:203],v[116:119]\n"
"s_waitcnt vmcnt(12) lgkmcnt(0)\n"
"v_add_f32 v58,v58,v99\n"
CV "v97,v98,v99\n"
MF "v[116:119],v[16:19],v[180:183],v[116:119]\n"
"s_barrier\n"
MF "v[112:115],v[16:19],v[176:179],v[112:115]\n"
"v_exp_f32 v100,v100\n"
MF "v[112:115],v[20:23],v[196:199],v[112:115]\n"
"s_nop 0\n"
TR "v[16:17],v2\n"
TR "v[18:19],v2 offset:6144\n"
MF "v[112:115],v[24:27],v[216:219],v[112:115]\n"
"s_add_u32 s72,s72,6144\n"
TR "v[20:21],v3\n"
TR "v[22:23],v3 offset:6144\n"
MF "v[112:115],v[28:31],v[236:239],v[112:115]\n"
"s_cmp_eq_u32 s72,s17\n"
"s_cselect_b32 s72,s16,s72\n"
"s_add_u32 m0,s64,24576\n"
MF "v[116:119],v[28:31],v[240:243],v[116:119]\n"
LD "v7,s[32:35],s72 offen nt lds\n"
MF "v[116:119],v[24:27],v[220:223],v[116:119]\n"
"s_add_u32 m0,s64,25472\n"
"v_exp_f32 v101,v101\n"
MF "v[120:123],v[24:27],v[224:227],v[120:123]\n"
LD "v7,s[32:35],s72 offen offset:128 nt lds\n"
MF "v[120:123],v[28:31],v[244:247],v[120:123]\n"
"v_exp_f32 v102,v102\n"
MF "v[124:127],v[28:31],v[248:251],v[124:127]\n"
"s_add_u32 m0,s64,26368\n"
"v_exp_f32 v103,v103\n"
MF "v[124:127],v[24:27],v[228:231],v[124:127]\n"
LD "v7,s[32:35],s72 offen offset:256 nt lds\n"
MF "v[128:131],v[24:27],v[232:235],v[128:131]\n"
"s_add_u32 s72,s72,6144\n"
"v_add_f32 v59,v54,v100\n"
"v_add_f32 v59,v59,v101\n"
MF "v[128:131],v[28:31],v[252:255],v[128:131]\n"
"s_waitcnt lgkmcnt(0)\n"
TR "v[24:25],v4\n"
TR "v[26:27],v4 offset:6144\n"
MF "a[0:3],v[16:19],v[72:75],a[220:223]\n"
"s_add_u32 m0,s64,30720\n"
TR "v[28:29],v5\n"
TR "v[30:31],v5 offset:6144\n"
MF "a[20:23],v[20:23],v[72:75],a[220:223]\n"
LD "v7,s[32:35],s72 offen nt lds\n"
MF "a[24:27],v[20:23],v[76:79],a[220:223]\n"
CV "v100,v100,v101\n"
"v_add_f32 v59,v59,v102\n"
MF "a[4:7],v[16:19],v[76:79],a[220:223]\n"
"s_add_u32 m0,s64,31616\n"
"v_add_f32 v59,v59,v103\n"
CV "v101,v102,v103\n"
MF "a[8:11],v[16:19],v[80:83],a[220:223]\n"
LD "v7,s[32:35],s72 offen offset:128 nt lds\n"
MF "a[28:31],v[20:23],v[80:83],a[220:223]\n"
"v_exp_f32 v104,v104\n"
MF "a[32:35],v[20:23],v[84:87],a[220:223]\n"
"s_add_u32 m0,s64,32512\n"
"v_exp_f32 v105,v105\n"
MF "a[12:15],v[16:19],v[84:87],a[220:223]\n"
LD "v7,s[32:35],s72 offen offset:256 nt lds\n"
MF "a[16:19],v[16:19],v[88:91],a[220:223]\n"
"v_exp_f32 v106,v106\n"
MF "a[36:39],v[20:23],v[88:91],a[220:223]\n"
"s_waitcnt lgkmcnt(0)\n"
TR "v[16:17],v2 offset:1024\n"
TR "v[18:19],v2 offset:7168\n"
MF "a[56:59],v[24:27],v[88:91],a[220:223]\n"
"s_nop 0\n"
TR "v[20:21],v3 offset:1024\n"
TR "v[22:23],v3 offset:7168\n"
MF "a[76:79],v[28:31],v[88:91],a[220:223]\n"
"v_exp_f32 v107,v107\n"
MF "a[72:75],v[28:31],v[84:87],a[220:223]\n"
"v_add_f32 v60,v55,v104\n"
"v_add_f32 v60,v60,v105\n"
MF "a[52:55],v[24:27],v[84:87],a[220:223]\n"
CV "v104,v104,v105\n"
"v_add_f32 v60,v60,v106\n"
MF "a[48:51],v[24:27],v[80:83],a[220:223]\n"
"v_add_f32 v60,v60,v107\n"
CV "v105,v106,v107\n"
MF "a[68:71],v[28:31],v[80:83],a[220:223]\n"
"v_exp_f32 v108,v108\n"
MF "a[64:67],v[28:31],v[76:79],a[220:223]\n"
".Lpg10b:\n"
"s_cbranch_execz .Lpg1a\n"
".Lpg10d:\n"
"s_cbranch_execz .Lisr2\n"
"v_exp_f32 v109,v109\n"
MF "a[44:47],v[24:27],v[76:79],a[220:223]\n"
"v_exp_f32 v110,v110\n"
MF "a[40:43],v[24:27],v[72:75],a[220:223]\n"
"v_exp_f32 v111,v111\n"
MF "a[60:63],v[28:31],v[72:75],a[220:223]\n"
"s_waitcnt lgkmcnt(0)\n"
TR "v[24:25],v4 offset:1024\n"
TR "v[26:27],v4 offset:7168\n"
MF "a[80:83],v[16:19],v[72:75],a[220:223]\n"
"s_nop 0\n"
TR "v[28:29],v5 offset:1024\n"
TR "v[30:31],v5 offset:7168\n"
MF "a[100:103],v[20:23],v[72:75],a[220:223]\n"
"v_add_f32 v61,v56,v108\n"
"v_add_f32 v61,v61,v109\n"
MF "a[104:107],v[20:23],v[76:79],a[220:223]\n"
CV "v108,v108,v109\n"
"v_add_f32 v61,v61,v110\n"
MF "a[84:87],v[16:19],v[76:79],a[220:223]\n"
"v_add_f32 v61,v61,v111\n"
CV "v109,v110,v111\n"
MF "a[88:91],v[16:19],v[80:83],a[220:223]\n"
"v_exp_f32 v112,v112\n"
MF "a[108:111],v[20:23],v[80:83],a[220:223]\n"
"v_exp_f32 v113,v113\n"
MF "a[112:115],v[20:23],v[84:87],a[220:223]\n"
"v_exp_f32 v114,v114\n"
MF "a[92:95],v[16:19],v[84:87],a[220:223]\n"
"v_exp_f32 v115,v115\n"
MF "a[96:99],v[16:19],v[88:91],a[220:223]\n"
"v_add_f32 v57,v57,v112\n"
"v_add_f32 v57,v57,v113\n"
MF "a[116:119],v[20:23],v[88:91],a[220:223]\n"
"s_waitcnt lgkmcnt(0)\n"
TR "v[16:17],v2 offset:2048\n"
TR "v[18:19],v2 offset:8192\n"
MF "a[136:139],v[24:27],v[88:91],a[220:223]\n"
"s_nop 0\n"
TR "v[20:21],v3 offset:2048\n"
TR "v[22:23],v3 offset:8192\n"
MF "a[156:159],v[28:31],v[88:91],a[220:223]\n"
CV "v94,v112,v113\n"
"v_add_f32 v57,v57,v114\n"
MF "a[152:155],v[28:31],v[84:87],a[220:223]\n"
"v_add_f32 v57,v57,v115\n"
CV "v95,v114,v115\n"
MF "a[132:135],v[24:27],v[84:87],a[220:223]\n"
"v_exp_f32 v116,v116\n"
MF "a[128:131],v[24:27],v[80:83],a[220:223]\n"
"v_exp_f32 v117,v117\n"
MF "a[148:151],v[28:31],v[80:83],a[220:223]\n"
"v_exp_f32 v118,v118\n"
MF "a[144:147],v[28:31],v[76:79],a[220:223]\n"
"v_exp_f32 v119,v119\n"
MF "a[124:127],v[24:27],v[76:79],a[220:223]\n"
"v_add_f32 v58,v58,v116\n"
"v_add_f32 v58,v58,v117\n"
MF "a[120:123],v[24:27],v[72:75],a[220:223]\n"
CV "v98,v116,v117\n"
"v_add_f32 v58,v58,v118\n"
MF "a[140:143],v[28:31],v[72:75],a[220:223]\n"
"s_waitcnt lgkmcnt(0)\n"
TR "v[24:25],v4 offset:2048\n"
TR "v[26:27],v4 offset:8192\n"
MF "a[160:163],v[16:19],v[72:75],a[220:223]\n"
"s_nop 0\n"
TR "v[28:29],v5 offset:2048\n"
TR "v[30:31],v5 offset:8192\n"
MF "a[180:183],v[20:23],v[72:75],a[220:223]\n"
"v_add_f32 v58,v58,v119\n"
CV "v99,v118,v119\n"
MF "a[184:187],v[20:23],v[76:79],a[220:223]\n"
"v_exp_f32 v120,v120\n"
MF "a[164:167],v[16:19],v[76:79],a[220:223]\n"
"v_exp_f32 v121,v121\n"
MF "a[168:171],v[16:19],v[80:83],a[220:223]\n"
"v_exp_f32 v122,v122\n"
MF "a[188:191],v[20:23],v[80:83],a[220:223]\n"
"v_exp_f32 v123,v123\n"
MF "a[192:195],v[20:23],v[84:87],a[220:223]\n"
"v_add_f32 v59,v59,v120\n"
"v_add_f32 v59,v59,v121\n"
"s_cmp_eq_u32 s57,0\n"
"s_cbranch_scc1 .Lisx6\n"
"s_mov_b64 exec,0\n"
"s_cmp_eq_u32 s56,0\n"
"s_cbranch_scc1 .Lpgt0e_g\n"
"s_cmp_eq_u32 s56,1\n"
"s_cbranch_scc1 .Lpg17g\n"
"s_cmp_eq_u32 s56,2\n"
"s_cbranch_scc1 .Lpg16g\n"
"s_branch .Lpgep_g\n"
".Lisr6:\n"
"s_mov_b64 exec,-1\n"
".Lisx6:\n"
MF "a[172:175],v[16:19],v[84:87],a[220:223]\n"
"s_waitcnt vmcnt(6) lgkmcnt(0)\n"
CV "v102,v120,v121\n"
"v_add_f32 v59,v59,v122\n"
MF "a[176:179],v[16:19],v[88:91],a[220:223]\n"
"s_barrier\n"
MF "a[196:199],v[20:23],v[88:91],a[220:223]\n"
"s_add_u32 s73,s73,12288\n"
"ds_read_b128 v[16:19],v0\n"
"ds_read_b128 v[20:23],v1\n"
MF "a[216:219],v[24:27],v[88:91],a[220:223]\n"
"s_cmp_eq_u32 s73,s19\n"
"s_cselect_b32 s73,s18,s73\n"
"s_add_u32 m0,s65,49152\n"
MF "a[236:239],v[28:31],v[88:91],a[220:223]\n"
LD "v8,s[36:39],s73 offen nt lds\n"
MF "a[232:235],v[28:31],v[84:87],a[220:223]\n"
"s_add_u32 m0,s65,50048\n"
"v_add_f32 v59,v59,v123\n"
CV "v103,v122,v123\n"
MF "a[212:215],v[24:27],v[84:87],a[220:223]\n"
LD "v8,s[36:39],s73 offen offset:128 nt lds\n"
MF "a[208:211],v[24:27],v[80:83],a[220:223]\n"
"s_add_u32 m0,s65,50944\n"
MF "a[228:231],v[28:31],v[80:83],a[220:223]\n"
LD "v8,s[36:39],s73 offen offset:256 nt lds\n"
MF "a[224:227],v[28:31],v[76:79],a[220:223]\n"
MF "a[204:207],v[24:27],v[76:79],a[220:223]\n"
MF "a[200:203],v[24:27],v[72:75],a[220:223]\n"
MF "a[220:223],v[28:31],v[72:75],a[220:223]\n"
"s_waitcnt lgkmcnt(0)\n"
".Laloopx:\n"
MF "v[72:75],v[16:19],v[136:139],v[32:35]\n"
"s_nop 0\n"
"ds_read_b128 v[24:27],v0 offset:1024\n"
"ds_read_b128 v[28:31],v1 offset:1024\n"
MF "v[72:75],v[20:23],v[156:159],v[72:75]\n"
MF "v[76:79],v[20:23],v[160:163],v[36:39]\n"
MF "v[76:79],v[16:19],v[140:143],v[76:79]\n"
MF "v[80:83],v[16:19],v[144:147],v[40:43]\n"
MF "v[80:83],v[20:23],v[164:167],v[80:83]\n"
MF "v[84:87],v[20:23],v[168:171],v[44:47]\n"
MF "v[84:87],v[16:19],v[148:151],v[84:87]\n"
"s_add_u32 m0,s65,49152\n"
MF "v[88:91],v[16:19],v[152:155],v[48:51]\n"
LD "v8,s[36:39],s73 offen offset:3072 nt lds\n"
MF "v[88:91],v[20:23],v[172:175],v[88:91]\n"
"s_waitcnt lgkmcnt(0)\n"
"ds_read_b128 v[16:19],v0 offset:2048\n"
"ds_read_b128 v[20:23],v1 offset:2048\n"
MF "v[88:91],v[24:27],v[192:195],v[88:91]\n"
"v_exp_f32 v124,v124\n"
MF "v[88:91],v[28:31],v[212:215],v[88:91]\n"
"s_add_u32 m0,s65,50048\n"
"v_exp_f32 v125,v125\n"
MF "v[84:87],v[28:31],v[208:211],v[84:87]\n"
LD "v8,s[36:39],s73 offen offset:3200 nt lds\n"
MF "v[84:87],v[24:27],v[188:191],v[84:87]\n"
"v_exp_f32 v126,v126\n"
MF "v[80:83],v[24:27],v[184:187],v[80:83]\n"
"v_exp_f32 v127,v127\n"
MF "v[80:83],v[28:31],v[204:207],v[80:83]\n"
"s_add_u32 m0,s65,50944\n"
"v_add_f32 v60,v60,v124\n"
"v_add_f32 v60,v60,v125\n"
MF "v[76:79],v[28:31],v[200:203],v[76:79]\n"
LD "v8,s[36:39],s73 offen offset:3328 nt lds\n"
MF "v[76:79],v[24:27],v[180:183],v[76:79]\n"
CV "v106,v124,v125\n"
"v_add_f32 v60,v60,v126\n"
MF "v[72:75],v[24:27],v[176:179],v[72:75]\n"
"v_add_f32 v60,v60,v127\n"
CV "v107,v126,v127\n"
MF "v[72:75],v[28:31],v[196:199],v[72:75]\n"
"s_waitcnt lgkmcnt(0)\n"
"ds_read_b128 v[24:27],v0 offset:6144\n"
"ds_read_b128 v[28:31],v1 offset:6144\n"
MF "v[72:75],v[16:19],v[216:219],v[72:75]\n"
"v_exp_f32 v128,v128\n"
MF "v[72:75],v[20:23],v[236:239],v[72:75]\n"
"v_exp_f32 v129,v129\n"
MF "v[76:79],v[20:23],v[240:243],v[76:79]\n"
"v_exp_f32 v130,v130\n"
MF "v[76:79],v[16:19],v[220:223],v[76:79]\n"
"v_exp_f32 v131,v131\n"
MF "v[80:83],v[16:19],v[224:227],v[80:83]\n"
"v_add_f32 v61,v61,v128\n"
"v_add_f32 v61,v61,v129\n"
MF "v[80:83],v[20:23],v[244:247],v[80:83]\n"
CV "v110,v128,v129\n"
"v_add_f32 v61,v61,v130\n"
MF "v[84:87],v[20:23],v[248:251],v[84:87]\n"
"v_add_f32 v61,v61,v131\n"
CV "v111,v130,v131\n"
MF "v[84:87],v[16:19],v[228:231],v[84:87]\n"
"v_max3_f32 v10,v57,v58,v59\n"
"v_max3_f32 v10,v10,v60,v61\n"
MF "v[88:91],v[16:19],v[232:235],v[88:91]\n"
"v_cmp_ngt_f32 vcc,s77,v10\n"
MF "v[88:91],v[20:23],v[252:255],v[88:91]\n"
"s_waitcnt lgkmcnt(0)\n"
"s_cbranch_vccnz .Lstub_6\n"
".Lback_6:\n"
MF "v[112:115],v[24:27],v[136:139],v[32:35]\n"
"s_add_u32 s74,s74,12288\n"
"ds_read_b128 v[16:19],v0 offset:7168\n"
"ds_read_b128 v[20:23],v1 offset:7168\n"
MF "v[112:115],v[28:31],v[156:159],v[112:115]\n"
"s_cmp_eq_u32 s74,s20\n"
"s_cselect_b32 s74,s70,s74\n"
MF "v[116:119],v[28:31],v[160:163],v[36:39]\n"
"v_exp_f32 v72,v72\n"
MF "v[116:119],v[24:27],v[140:143],v[116:119]\n"
"v_exp_f32 v73,v73\n"
MF "v[120:123],v[24:27],v[144:147],v[40:43]\n"
"v_exp_f32 v74,v74\n"
MF "v[120:123],v[28:31],v[164:167],v[120:123]\n"
"v_exp_f32 v75,v75\n"
MF "v[124:127],v[28:31],v[168:171],v[44:47]\n"
"v_add_f32 v52,v57,v72\n"
"v_add_f32 v52,v52,v73\n"
MF "v[124:127],v[24:27],v[148:151],v[124:127]\n"
CV "v72,v72,v73\n"
"v_add_f32 v52,v52,v74\n"
MF "v[128:131],v[24:27],v[152:155],v[48:51]\n"
"v_add_f32 v52,v52,v75\n"
CV "v73,v74,v75\n"
MF "v[128:131],v[28:31],v[172:175],v[128:131]\n"
"s_waitcnt lgkmcnt(0)\n"
"ds_read_b128 v[24:27],v0 offset:8192\n"
"ds_read_b128 v[28:31],v1 offset:8192\n"
MF "v[128:131],v[16:19],v[192:195],v[128:131]\n"
"v_exp_f32 v76,v76\n"
MF "v[128:131],v[20:23],v[212:215],v[128:131]\n"
"v_exp_f32 v77,v77\n"
MF "v[124:127],v[20:23],v[208:211],v[124:127]\n"
"v_exp_f32 v78,v78\n"
MF "v[124:127],v[16:19],v[188:191],v[124:127]\n"
"v_exp_f32 v79,v79\n"
MF "v[120:123],v[16:19],v[184:187],v[120:123]\n"
"v_add_f32 v53,v58,v76\n"
"v_add_f32 v53,v53,v77\n"
MF "v[120:123],v[20:23],v[204:207],v[120:123]\n"
CV "v76,v76,v77\n"
"v_add_f32 v53,v53,v78\n"
MF "v[116:119],v[20:23],v[200:203],v[116:119]\n"
"s_waitcnt vmcnt(12) lgkmcnt(0)\n"
"v_add_f32 v53,v53,v79\n"
CV "v77,v78,v79\n"
MF "v[116:119],v[16:19],v[180:183],v[116:119]\n"
"s_barrier\n"
MF "v[112:115],v[16:19],v[176:179],v[112:115]\n"
"v_exp_f32 v80,v80\n"
MF "v[112:115],v[20:23],v[196:199],v[112:115]\n"
"s_nop 0\n"
TR "v[16:17],v2 offset:24576\n"
TR "v[18:19],v2 offset:30720\n"
MF "v[112:115],v[24:27],v[216:219],v[112:115]\n"
"s_add_u32 s72,s72,6144\n"
TR "v[20:21],v3 offset:24576\n"
TR "v[22:23],v3 offset:30720\n"
MF "v[112:115],v[28:31],v[236:239],v[112:115]\n"
"s_cmp_eq_u32 s72,s17\n"
"s_cselect_b32 s72,s16,s72\n"
"s_add_u32 m0,s64,0\n"
MF "v[116:119],v[28:31],v[240:243],v[116:119]\n"
LD "v7,s[32:35],s72 offen nt lds\n"
MF "v[116:119],v[24:27],v[220:223],v[116:119]\n"
"s_add_u32 m0,s64,896\n"
"v_exp_f32 v81,v81\n"
MF "v[120:123],v[24:27],v[224:227],v[120:123]\n"
LD "v7,s[32:35],s72 offen offset:128 nt lds\n"
MF "v[120:123],v[28:31],v[244:247],v[120:123]\n"
"v_exp_f32 v82,v82\n"
MF "v[124:127],v[28:31],v[248:251],v[124:127]\n"
"s_add_u32 m0,s64,1792\n"
"v_exp_f32 v83,v83\n"
MF "v[124:127],v[24:27],v[228:231],v[124:127]\n"
LD "v7,s[32:35],s72 offen offset:256 nt lds\n"
MF "v[128:131],v[24:27],v[232:235],v[128:131]\n"
"s_add_u32 s72,s72,6144\n"
"v_add_f32 v54,v59,v80\n"
"v_add_f32 v54,v54,v81\n"
MF "v[128:131],v[28:31],v[252:255],v[128:131]\n"
"s_waitcnt lgkmcnt(0)\n"
TR "v[24:25],v4 offset:24576\n"
TR "v[26:27],v4 offset:30720\n"
MF "a[0:3],v[16:19],v[92:95],a[0:3]\n"
"s_add_u32 m0,s64,6144\n"
TR "v[28:29],v5 offset:24576\n"
TR "v[30:31],v5 offset:30720\n"
MF "a[20:23],v[20:23],v[92:95],a[20:23]\n"
LD "v7,s[32:35],s72 offen nt lds\n"
MF "a[24:27],v[20:23],v[96:99],a[24:27]\n"
CV "v80,v80,v81\n"
"v_add_f32 v54,v54,v82\n"
MF "a[4:7],v[16:19],v[96:99],a[4:7]\n"
"s_add_u32 m0,s64,7040\n"
"v_add_f32 v54,v54,v83\n"
CV "v81,v82,v83\n"
MF "a[8:11],v[16:19],v[100:103],a[8:11]\n"
LD "v7,s[32:35],s72 offen offset:128 nt lds\n"
MF "a[28:31],v[20:23],v[100:103],a[28:31]\n"
"v_exp_f32 v84,v84\n"
MF "a[32:35],v[20:23],v[104:107],a[32:35]\n"
"s_add_u32 m0,s64,7936\n"
"v_exp_f32 v85,v85\n"
MF "a[12:15],v[16:19],v[104:107],a[12:15]\n"
LD "v7,s[32:35],s72 offen offset:256 nt lds\n"
MF "a[16:19],v[16:19],v[108:111],a[16:19]\n"
"v_exp_f32 v86,v86\n"
MF "a[36:39],v[20:23],v[108:111],a[36:39]\n"
"s_waitcnt lgkmcnt(0)\n"
TR "v[16:17],v2 offset:25600\n"
TR "v[18:19],v2 offset:31744\n"
MF "a[56:59],v[24:27],v[108:111],a[56:59]\n"
"s_nop 0\n"
TR "v[20:21],v3 offset:25600\n"
TR "v[22:23],v3 offset:31744\n"
MF "a[76:79],v[28:31],v[108:111],a[76:79]\n"
"v_exp_f32 v87,v87\n"
MF "a[72:75],v[28:31],v[104:107],a[72:75]\n"
"v_add_f32 v55,v60,v84\n"
"v_add_f32 v55,v55,v85\n"
MF "a[52:55],v[24:27],v[104:107],a[52:55]\n"
CV "v84,v84,v85\n"
"v_add_f32 v55,v55,v86\n"
MF "a[48:51],v[24:27],v[100:103],a[48:51]\n"
"v_add_f32 v55,v55,v87\n"
CV "v85,v86,v87\n"
MF "a[68:71],v[28:31],v[100:103],a[68:71]\n"
"v_exp_f32 v88,v88\n"
MF "a[64:67],v[28:31],v[96:99],a[64:67]\n"
"v_exp_f32 v89,v89\n"
MF "a[44:47],v[24:27],v[96:99],a[44:47]\n"
"v_exp_f32 v90,v90\n"
MF "a[40:43],v[24:27],v[92:95],a[40:43]\n"
"v_exp_f32 v91,v91\n"
MF "a[60:63],v[28:31],v[92:95],a[60:63]\n"
"s_waitcnt lgkmcnt(0)\n"
TR "v[24:25],v4 offset:25600\n"
TR "v[26:27],v4 offset:31744\n"
MF "a[80:83],v[16:19],v[92:95],a[80:83]\n"
"s_nop 0\n"
TR "v[28:29],v5 offset:25600\n"
TR "v[30:31],v5 offset:31744\n"
MF "a[100:103],v[20:23],v[92:95],a[100:103]\n"
"v_add_f32 v56,v61,v88\n"
"v_add_f32 v56,v56,v89\n"
MF "a[104:107],v[20:23],v[96:99],a[104:107]\n"
CV "v88,v88,v89\n"
"v_add_f32 v56,v56,v90\n"
MF "a[84:87],v[16:19],v[96:99],a[84:87]\n"
"v_add_f32 v56,v56,v91\n"
CV "v89,v90,v91\n"
MF "a[88:91],v[16:19],v[100:103],a[88:91]\n"
"v_exp_f32 v112,v112\n"
MF "a[108:111],v[20:23],v[100:103],a[108:111]\n"
"v_exp_f32 v113,v113\n"
MF "a[112:115],v[20:23],v[104:107],a[112:115]\n"
"v_exp_f32 v114,v114\n"
MF "a[92:95],v[16:19],v[104:107],a[92:95]\n"
"v_exp_f32 v115,v115\n"
MF "a[96:99],v[16:19],v[108:111],a[96:99]\n"
"v_add_f32 v52,v52,v112\n"
"v_add_f32 v52,v52,v113\n"
MF "a[116:119],v[20:23],v[108:111],a[116:119]\n"
"s_waitcnt lgkmcnt(0)\n"
TR "v[16:17],v2 offset:26624\n"
TR "v[18:19],v2 offset:32768\n"
MF "a[136:139],v[24:27],v[108:111],a[136:139]\n"
"s_nop 0\n"
TR "v[20:21],v3 offset:26624\n"
TR "v[22:23],v3 offset:32768\n"
MF "a[156:159],v[28:31],v[108:111],a[156:159]\n"
CV "v74,v112,v113\n"
"v_add_f32 v52,v52,v114\n"
MF "a[152:155],v[28:31],v[104:107],a[152:155]\n"
"v_add_f32 v52,v52,v115\n"
CV "v75,v114,v115\n"
MF "a[132:135],v[24:27],v[104:107],a[132:135]\n"
"v_exp_f32 v116,v116\n"
MF "a[128:131],v[24:27],v[100:103],a[128:131]\n"
"v_exp_f32 v117,v117\n"
MF "a[148:151],v[28:31],v[100:103],a[148:151]\n"
"v_exp_f32 v118,v118\n"
MF "a[144:147],v[28:31],v[96:99],a[144:147]\n"
"v_exp_f32 v119,v119\n"
MF "a[124:127],v[24:27],v[96:99],a[124:127]\n"
"v_add_f32 v53,v53,v116\n"
"v_add_f32 v53,v53,v117\n"
MF "a[120:123],v[24:27],v[92:95],a[120:123]\n"
CV "v78,v116,v117\n"
"v_add_f32 v53,v53,v118\n"
MF "a[140:143],v[28:31],v[92:95],a[140:143]\n"
"s_waitcnt lgkmcnt(0)\n"
TR "v[24:25],v4 offset:26624\n"
TR "v[26:27],v4 offset:32768\n"
MF "a[160:163],v[16:19],v[92:95],a[160:163]\n"
"s_nop 0\n"
TR "v[28:29],v5 offset:26624\n"
TR "v[30:31],v5 offset:32768\n"
MF "a[180:183],v[20:23],v[92:95],a[180:183]\n"
"v_add_f32 v53,v53,v119\n"
CV "v79,v118,v119\n"
MF "a[184:187],v[20:23],v[96:99],a[184:187]\n"
"v_exp_f32 v120,v120\n"
MF "a[164:167],v[16:19],v[96:99],a[164:167]\n"
"v_exp_f32 v121,v121\n"
MF "a[168:171],v[16:19],v[100:103],a[168:171]\n"
"v_exp_f32 v122,v122\n"
MF "a[188:191],v[20:23],v[100:103],a[188:191]\n"
"v_exp_f32 v123,v123\n"
MF "a[192:195],v[20:23],v[104:107],a[192:195]\n"
"v_add_f32 v54,v54,v120\n"
"v_add_f32 v54,v54,v121\n"
MF "a[172:175],v[16:19],v[104:107],a[172:175]\n"
"s_waitcnt vmcnt(12) lgkmcnt(0)\n"
CV "v82,v120,v121\n"
"v_add_f32 v54,v54,v122\n"
MF "a[176:179],v[16:19],v[108:111],a[176:179]\n"
"s_barrier\n"
MF "a[196:199],v[20:23],v[108:111],a[196:199]\n"
"s_add_u32 s73,s73,12288\n"
"ds_read_b128 v[16:19],v0 offset:24576\n"
"ds_read_b128 v[20:23],v1 offset:24576\n"
MF "a[216:219],v[24:27],v[108:111],a[216:219]\n"
"s_cmp_eq_u32 s73,s19\n"
"s_cselect_b32 s73,s18,s73\n"
"s_add_u32 m0,s65,73728\n"
MF "a[236:239],v[28:31],v[108:111],a[236:239]\n"
LD "v8,s[36:39],s73 offen nt lds\n"
MF "a[232:235],v[28:31],v[104:107],a[232:235]\n"
"s_add_u32 m0,s65,74624\n"
"v_add_f32 v54,v54,v123\n"
CV "v83,v122,v123\n"
MF "a[212:215],v[24:27],v[104:107],a[212:215]\n"
LD "v8,s[36:39],s73 offen offset:128 nt lds\n"
MF "a[208:211],v[24:27],v[100:103],a[208:211]\n"
"s_add_u32 m0,s65,75520\n"
MF "a[228:231],v[28:31],v[100:103],a[228:231]\n"
LD "v8,s[36:39],s73 offen offset:256 nt lds\n"
MF "a[224:227],v[28:31],v[96:99],a[224:227]\n"
MF "a[204:207],v[24:27],v[96:99],a[204:207]\n"
MF "a[200:203],v[24:27],v[92:95],a[200:203]\n"
MF "a[220:223],v[28:31],v[92:95],a[220:223]\n"
"s_waitcnt lgkmcnt(0)\n"
MF "v[92:95],v[16:19],v[136:139],v[32:35]\n"
"s_add_u32 m0,s65,73728\n"
"ds_read_b128 v[24:27],v0 offset:25600\n"
"ds_read_b128 v[28:31],v1 offset:25600\n"
MF "v[92:95],v[20:23],v[156:159],v[92:95]\n"
LD "v8,s[36:39],s73 offen offset:3072 nt lds\n"
MF "v[96:99],v[20:23],v[160:163],v[36:39]\n"
"v_exp_f32 v124,v124\n"
MF "v[96:99],v[16:19],v[140:143],v[96:99]\n"
"v_exp_f32 v125,v125\n"
MF "v[100:103],v[16:19],v[144:147],v[40:43]\n"
"s_add_u32 m0,s65,74624\n"
"v_exp_f32 v126,v126\n"
MF "v[100:103],v[20:23],v[164:167],v[100:103]\n"
LD "v8,s[36:39],s73 offen offset:3200 nt lds\n"
MF "v[104:107],v[20:23],v[168:171],v[44:47]\n"
"v_exp_f32 v127,v127\n"
MF "v[104:107],v[16:19],v[148:151],v[104:107]\n"
"s_add_u32 m0,s65,75520\n"
"v_add_f32 v55,v55,v124\n"
"v_add_f32 v55,v55,v125\n"
MF "v[108:111],v[16:19],v[152:155],v[48:51]\n"
LD "v8,s[36:39],s73 offen offset:3328 nt lds\n"
MF "v[108:111],v[20:23],v[172:175],v[108:111]\n"
"s_waitcnt lgkmcnt(0)\n"
"ds_read_b128 v[16:19],v0 offset:26624\n"
"ds_read_b128 v[20:23],v1 offset:26624\n"
MF "v[108:111],v[24:27],v[192:195],v[108:111]\n"
CV "v86,v124,v125\n"
"v_add_f32 v55,v55,v126\n"
MF "v[108:111],v[28:31],v[212:215],v[108:111]\n"
"v_add_f32 v55,v55,v127\n"
CV "v87,v126,v127\n"
MF "v[104:107],v[28:31],v[208:211],v[104:107]\n"
"v_exp_f32 v128,v128\n"
MF "v[104:107],v[24:27],v[188:191],v[104:107]\n"
"v_exp_f32 v129,v129\n"
MF "v[100:103],v[24:27],v[184:187],v[100:103]\n"
"v_exp_f32 v130,v130\n"
MF "v[100:103],v[28:31],v[204:207],v[100:103]\n"
"v_exp_f32 v131,v131\n"
MF "v[96:99],v[28:31],v[200:203],v[96:99]\n"
"v_add_f32 v56,v56,v128\n"
"v_add_f32 v56,v56,v129\n"
MF "v[96:99],v[24:27],v[180:183],v[96:99]\n"
CV "v90,v128,v129\n"
"v_add_f32 v56,v56,v130\n"
MF "v[92:95],v[24:27],v[176:179],v[92:95]\n"
"v_add_f32 v56,v56,v131\n"
CV "v91,v130,v131\n"
MF "v[92:95],v[28:31],v[196:199],v[92:95]\n"
"s_waitcnt lgkmcnt(0)\n"
"ds_read_b128 v[24:27],v0 offset:30720\n"
"ds_read_b128 v[28:31],v1 offset:30720\n"
MF "v[92:95],v[16:19],v[216:219],v[92:95]\n"
MF "v[92:95],v[20:23],v[236:239],v[92:95]\n"
MF "v[96:99],v[20:23],v[240:243],v[96:99]\n"
MF "v[96:99],v[16:19],v[220:223],v[96:99]\n"
MF "v[100:103],v[16:19],v[224:227],v[100:103]\n"
MF "v[100:103],v[20:23],v[244:247],v[100:103]\n"
MF "v[104:107],v[20:23],v[248:251],v[104:107]\n"
MF "v[104:107],v[16:19],v[228:231],v[104:107]\n"
"v_max3_f32 v10,v52,v53,v54\n"
"v_max3_f32 v10,v10,v55,v56\n"
MF "v[108:111],v[16:19],v[232:235],v[108:111]\n"
"v_cmp_ngt_f32 vcc,s77,v10\n"
MF "v[108:111],v[20:23],v[252:255],v[108:111]\n"
"s_waitcnt lgkmcnt(0)\n"
"s_cbranch_vccnz .Lstub_7\n"
".Lback_7:\n"
MF "v[112:115],v[24:27],v[136:139],v[32:35]\n"
"s_add_u32 s74,s74,12288\n"
"ds_read_b128 v[16:19],v0 offset:31744\n"
"ds_read_b128 v[20:23],v1 offset:31744\n"
MF "v[112:115],v[28:31],v[156:159],v[112:115]\n"
"s_cmp_eq_u32 s74,s20\n"
"s_cselect_b32 s74,s70,s74\n"
MF "v[116:119],v[28:31],v[160:163],v[36:39]\n"
"v_exp_f32 v92,v92\n"
MF "v[116:119],v[24:27],v[140:143],v[116:119]\n"
"v_exp_f32 v93,v93\n"
MF "v[120:123],v[24:27],v[144:147],v[40:43]\n"
"v_exp_f32 v94,v94\n"
MF "v[120:123],v[28:31],v[164:167],v[120:123]\n"
"v_exp_f32 v95,v95\n"
MF "v[124:127],v[28:31],v[168:171],v[44:47]\n"
"v_add_f32 v57,v52,v92\n"
"v_add_f32 v57,v57,v93\n"
MF "v[124:127],v[24:27],v[148:151],v[124:127]\n"
CV "v92,v92,v93\n"
"v_add_f32 v57,v57,v94\n"
MF "v[128:131],v[24:27],v[152:155],v[48:51]\n"
"v_add_f32 v57,v57,v95\n"
CV "v93,v94,v95\n"
MF "v[128:131],v[28:31],v[172:175],v[128:131]\n"
"s_waitcnt lgkmcnt(0)\n"
"ds_read_b128 v[24:27],v0 offset:32768\n"
"ds_read_b128 v[28:31],v1 offset:32768\n"
MF "v[128:131],v[16:19],v[192:195],v[128:131]\n"
"s_sub_u32 s61,s61,1\n"
"v_exp_f32 v96,v96\n"
MF "v[128:131],v[20:23],v[212:215],v[128:131]\n"
"v_exp_f32 v97,v97\n"
MF "v[124:127],v[20:23],v[208:211],v[124:127]\n"
"v_exp_f32 v98,v98\n"
MF "v[124:127],v[16:19],v[188:191],v[124:127]\n"
"v_exp_f32 v99,v99\n"
MF "v[120:123],v[16:19],v[184:187],v[120:123]\n"
"v_add_f32 v58,v53,v96\n"
"v_add_f32 v58,v58,v97\n"
MF "v[120:123],v[20:23],v[204:207],v[120:123]\n"
CV "v96,v96,v97\n"
"v_add_f32 v58,v58,v98\n"
MF "v[116:119],v[20:23],v[200:203],v[116:119]\n"
"s_waitcnt vmcnt(12) lgkmcnt(0)\n"
"v_add_f32 v58,v58,v99\n"
CV "v97,v98,v99\n"
MF "v[116:119],v[16:19],v[180:183],v[116:119]\n"
"s_barrier\n"
MF "v[112:115],v[16:19],v[176:179],v[112:115]\n"
"v_exp_f32 v100,v100\n"
MF "v[112:115],v[20:23],v[196:199],v[112:115]\n"
"s_nop 0\n"
TR "v[16:17],v2\n"
TR "v[18:19],v2 offset:6144\n"
MF "v[112:115],v[24:27],v[216:219],v[112:115]\n"
"s_add_u32 s72,s72,6144\n"
TR "v[20:21],v3\n"
TR "v[22:23],v3 offset:6144\n"
MF "v[112:115],v[28:31],v[236:239],v[112:115]\n"
"s_cmp_eq_u32 s72,s17\n"
"s_cselect_b32 s72,s16,s72\n"
"s_add_u32 m0,s64,24576\n"
MF "v[116:119],v[28:31],v[240:243],v[116:119]\n"
LD "v7,s[32:35],s72 offen nt lds\n"
MF "v[116:119],v[24:27],v[220:223],v[116:119]\n"
"s_add_u32 m0,s64,25472\n"
"v_exp_f32 v101,v101\n"
MF "v[120:123],v[24:27],v[224:227],v[120:123]\n"
LD "v7,s[32:35],s72 offen offset:128 nt lds\n"
MF "v[120:123],v[28:31],v[244:247],v[120:123]\n"
"v_exp_f32 v102,v102\n"
MF "v[124:127],v[28:31],v[248:251],v[124:127]\n"
"s_add_u32 m0,s64,26368\n"
"v_exp_f32 v103,v103\n"
MF "v[124:127],v[24:27],v[228:231],v[124:127]\n"
LD "v7,s[32:35],s72 offen offset:256 nt lds\n"
MF "v[128:131],v[24:27],v[232:235],v[128:131]\n"
"s_add_u32 s72,s72,6144\n"
"v_add_f32 v59,v54,v100\n"
"v_add_f32 v59,v59,v101\n"
MF "v[128:131],v[28:31],v[252:255],v[128:131]\n"
"s_waitcnt lgkmcnt(0)\n"
TR "v[24:25],v4\n"
TR "v[26:27],v4 offset:6144\n"
MF "a[0:3],v[16:19],v[72:75],a[0:3]\n"
"s_add_u32 m0,s64,30720\n"
TR "v[28:29],v5\n"
TR "v[30:31],v5 offset:6144\n"
MF "a[20:23],v[20:23],v[72:75],a[20:23]\n"
LD "v7,s[32:35],s72 offen nt lds\n"
MF "a[24:27],v[20:23],v[76:79],a[24:27]\n"
CV "v100,v100,v101\n"
"v_add_f32 v59,v59,v102\n"
MF "a[4:7],v[16:19],v[76:79],a[4:7]\n"
"s_add_u32 m0,s64,31616\n"
"v_add_f32 v59,v59,v103\n"
CV "v101,v102,v103\n"
MF "a[8:11],v[16:19],v[80:83],a[8:11]\n"
LD "v7,s[32:35],s72 offen offset:128 nt lds\n"
MF "a[28:31],v[20:23],v[80:83],a[28:31]\n"
"v_exp_f32 v104,v104\n"
MF "a[32:35],v[20:23],v[84:87],a[32:35]\n"
"s_add_u32 m0,s64,32512\n"
"v_exp_f32 v105,v105\n"
MF "a[12:15],v[16:19],v[84:87],a[12:15]\n"
LD "v7,s[32:35],s72 offen offset:256 nt lds\n"
MF "a[16:19],v[16:19],v[88:91],a[16:19]\n"
"v_exp_f32 v106,v106\n"
MF "a[36:39],v[20:23],v[88:91],a[36:39]\n"
"s_waitcnt lgkmcnt(0)\n"
TR "v[16:17],v2 offset:1024\n"
TR "v[18:19],v2 offset:7168\n"
MF "a[56:59],v[24:27],v[88:91],a[56:59]\n"
".Lpg11b:\n"
"s_cbranch_execz .Lpgqj_a\n"
".Lpg11d:\n"
"s_cbranch_execz .Lisr2\n"
"s_nop 0\n"
TR "v[20:21],v3 offset:1024\n"
TR "v[22:23],v3 offset:7168\n"
MF "a[76:79],v[28:31],v[88:91],a[76:79]\n"
"v_exp_f32 v107,v107\n"
MF "a[72:75],v[28:31],v[84:87],a[72:75]\n"
"v_add_f32 v60,v55,v104\n"
"v_add_f32 v60,v60,v105\n"
MF "a[52:55],v[24:27],v[84:87],a[52:55]\n"
CV "v104,v104,v105\n"
"v_add_f32 v60,v60,v106\n"
MF "a[48:51],v[24:27],v[80:83],a[48:51]\n"
"v_add_f32 v60,v60,v107\n"
CV "v105,v106,v107\n"
MF "a[68:71],v[28:31],v[80:83],a[68:71]\n"
"v_exp_f32 v108,v108\n"
MF "a[64:67],v[28:31],v[76:79],a[64:67]\n"
"v_exp_f32 v109,v109\n"
MF "a[44:47],v[24:27],v[76:79],a[44:47]\n"
"v_exp_f32 v110,v110\n"
MF "a[40:43],v[24:27],v[72:75],a[40:43]\n"
"v_exp_f32 v111,v111\n"
MF "a[60:63],v[28:31],v[72:75],a[60:63]\n"
"s_waitcnt lgkmcnt(0)\n"
TR "v[24:25],v4 offset:1024\n"
TR "v[26:27],v4 offset:7168\n"
MF "a[80:83],v[16:19],v[72:75],a[80:83]\n"
"s_nop 0\n"
TR "v[28:29],v5 offset:1024\n"
TR "v[30:31],v5 offset:7168\n"
MF "a[100:103],v[20:23],v[72:75],a[100:103]\n"
"v_add_f32 v61,v56,v108\n"
"v_add_f32 v61,v61,v109\n"
MF "a[104:107],v[20:23],v[76:79],a[104:107]\n"
CV "v108,v108,v109\n"
"v_add_f32 v61,v61,v110\n"
MF "a[84:87],v[16:19],v[76:79],a[84:87]\n"
"v_add_f32 v61,v61,v111\n"
CV "v109,v110,v111\n"
MF "a[88:91],v[16:19],v[80:83],a[88:91]\n"
"v_exp_f32 v112,v112\n"
MF "a[108:111],v[20:23],v[80:83],a[108:111]\n"
"v_exp_f32 v113,v113\n"
MF "a[112:115],v[20:23],v[84:87],a[112:115]\n"
"v_exp_f32 v114,v114\n"
MF "a[92:95],v[16:19],v[84:87],a[92:95]\n"
"v_exp_f32 v115,v115\n"
MF "a[96:99],v[16:19],v[88:91],a[96:99]\n"
"v_add_f32 v57,v57,v112\n"
"v_add_f32 v57,v57,v113\n"
MF "a[116:119],v[20:23],v[88:91],a[116:119]\n"
"s_waitcnt lgkmcnt(0)\n"
TR "v[16:17],v2 offset:2048\n"
TR "v[18:19],v2 offset:8192\n"
MF "a[136:139],v[24:27],v[88:91],a[136:139]\n"
"s_nop 0\n"
TR "v[20:21],v3 offset:2048\n"
TR "v[22:23],v3 offset:8192\n"
MF "a[156:159],v[28:31],v[88:91],a[156:159]\n"
CV "v94,v112,v113\n"
"v_add_f32 v57,v57,v114\n"
MF "a[152:155],v[28:31],v[84:87],a[152:155]\n"
"v_add_f32 v57,v57,v115\n"
CV "v95,v114,v115\n"
MF "a[132:135],v[24:27],v[84:87],a[132:135]\n"
"v_exp_f32 v116,v116\n"
MF "a[128:131],v[24:27],v[80:83],a[128:131]\n"
"v_exp_f32 v117,v117\n"
MF "a[148:151],v[28:31],v[80:83],a[148:151]\n"
"v_exp_f32 v118,v118\n"
MF "a[144:147],v[28:31],v[76:79],a[144:147]\n"
"v_exp_f32 v119,v119\n"
MF "a[124:127],v[24:27],v[76:79],a[124:127]\n"
"v_add_f32 v58,v58,v116\n"
"v_add_f32 v58,v58,v117\n"
MF "a[120:123],v[24:27],v[72:75],a[120:123]\n"
CV "v98,v116,v117\n"
"v_add_f32 v58,v58,v118\n"
MF "a[140:143],v[28:31],v[72:75],a[140:143]\n"
"s_waitcnt lgkmcnt(0)\n"
TR "v[24:25],v4 offset:2048\n"
TR "v[26:27],v4 offset:8192\n"
MF "a[160:163],v[16:19],v[72:75],a[160:163]\n"
"s_nop 0\n"
TR "v[28:29],v5 offset:2048\n"
TR "v[30:31],v5 offset:8192\n"
MF "a[180:183],v[20:23],v[72:75],a[180:183]\n"
"v_add_f32 v58,v58,v119\n"
CV "v99,v118,v119\n"
MF "a[184:187],v[20:23],v[76:79],a[184:187]\n"
"v_exp_f32 v120,v120\n"
MF "a[164:167],v[16:19],v[76:79],a[164:167]\n"
"v_exp_f32 v121,v121\n"
MF "a[168:171],v[16:19],v[80:83],a[168:171]\n"
"v_exp_f32 v122,v122\n"
MF "a[188:191],v[20:23],v[80:83],a[188:191]\n"
"v_exp_f32 v123,v123\n"
MF "a[192:195],v[20:23],v[84:87],a[192:195]\n"
"v_add_f32 v59,v59,v120\n"
"v_add_f32 v59,v59,v121\n"
MF "a[172:175],v[16:19],v[84:87],a[172:175]\n"
"s_waitcnt vmcnt(12) lgkmcnt(0)\n"
CV "v102,v120,v121\n"
"v_add_f32 v59,v59,v122\n"
MF "a[176:179],v[16:19],v[88:91],a[176:179]\n"
"s_barrier\n"
MF "a[196:199],v[20:23],v[88:91],a[196:199]\n"
"s_add_u32 s73,s73,12288\n"
"ds_read_b128 v[16:19],v0\n"
"ds_read_b128 v[20:23],v1\n"
MF "a[216:219],v[24:27],v[88:91],a[216:219]\n"
"s_cmp_eq_u32 s73,s19\n"
"s_cselect_b32 s73,s18,s73\n"
"s_add_u32 m0,s65,49152\n"
MF "a[236:239],v[28:31],v[88:91],a[236:239]\n"
LD "v8,s[36:39],s73 offen nt lds\n"
MF "a[232:235],v[28:31],v[84:87],a[232:235]\n"
"s_add_u32 m0,s65,50048\n"
"v_add_f32 v59,v59,v123\n"
CV "v103,v122,v123\n"
MF "a[212:215],v[24:27],v[84:87],a[212:215]\n"
LD "v8,s[36:39],s73 offen offset:128 nt lds\n"
MF "a[208:211],v[24:27],v[80:83],a[208:211]\n"
"s_add_u32 m0,s65,50944\n"
MF "a[228:231],v[28:31],v[80:83],a[228:231]\n"
LD "v8,s[36:39],s73 offen offset:256 nt lds\n"
MF "a[224:227],v[28:31],v[76:79],a[224:227]\n"
MF "a[204:207],v[24:27],v[76:79],a[204:207]\n"
"s_waitcnt lgkmcnt(0)\n"
MF "a[200:203],v[24:27],v[72:75],a[200:203]\n"
"s_cmp_lg_u32 s61,1\n"
MF "a[220:223],v[28:31],v[72:75],a[220:223]\n"
"s_cbranch_scc1 .Laloopx\n"
"s_cmp_eq_u32 s57,0\n"
"s_cbranch_scc0 .Lep\n"
".Lepx:\n"
MF "v[72:75],v[16:19],v[136:139],v[32:35]\n"
"s_add_u32 m0,s65,49152\n"
"ds_read_b128 v[24:27],v0 offset:1024\n"
"ds_read_b128 v[28:31],v1 offset:1024\n"
MF "v[72:75],v[20:23],v[156:159],v[72:75]\n"
LD "v8,s[36:39],s73 offen offset:3072 nt lds\n"
MF "v[76:79],v[20:23],v[160:163],v[36:39]\n"
"v_exp_f32 v124,v124\n"
MF "v[76:79],v[16:19],v[140:143],v[76:79]\n"
"v_exp_f32 v125,v125\n"
MF "v[80:83],v[16:19],v[144:147],v[40:43]\n"
"s_add_u32 m0,s65,50048\n"
"v_exp_f32 v126,v126\n"
MF "v[80:83],v[20:23],v[164:167],v[80:83]\n"
LD "v8,s[36:39],s73 offen offset:3200 nt lds\n"
MF "v[84:87],v[20:23],v[168:171],v[44:47]\n"
"v_exp_f32 v127,v127\n"
MF "v[84:87],v[16:19],v[148:151],v[84:87]\n"
"s_add_u32 m0,s65,50944\n"
"v_add_f32 v60,v60,v124\n"
"v_add_f32 v60,v60,v125\n"
MF "v[88:91],v[16:19],v[152:155],v[48:51]\n"
LD "v8,s[36:39],s73 offen offset:3328 nt lds\n"
MF "v[88:91],v[20:23],v[172:175],v[88:91]\n"
"s_waitcnt lgkmcnt(0)\n"
"ds_read_b128 v[16:19],v0 offset:2048\n"
"ds_read_b128 v[20:23],v1 offset:2048\n"
MF "v[88:91],v[24:27],v[192:195],v[88:91]\n"
CV "v106,v124,v125\n"
"v_add_f32 v60,v60,v126\n"
MF "v[88:91],v[28:31],v[212:215],v[88:91]\n"
"v_add_f32 v60,v60,v127\n"
CV "v107,v126,v127\n"
MF "v[84:87],v[28:31],v[208:211],v[84:87]\n"
"v_exp_f32 v128,v128\n"
MF "v[84:87],v[24:27],v[188:191],v[84:87]\n"
"v_exp_f32 v129,v129\n"
MF "v[80:83],v[24:27],v[184:187],v[80:83]\n"
"v_exp_f32 v130,v130\n"
MF "v[80:83],v[28:31],v[204:207],v[80:83]\n"
"v_exp_f32 v131,v131\n"
MF "v[76:79],v[28:31],v[200:203],v[76:79]\n"
"v_add_f32 v61,v61,v128\n"
"v_add_f32 v61,v61,v129\n"
MF "v[76:79],v[24:27],v[180:183],v[76:79]\n"
CV "v110,v128,v129\n"
"v_add_f32 v61,v61,v130\n"
MF "v[72:75],v[24:27],v[176:179],v[72:75]\n"
"v_add_f32 v61,v61,v131\n"
CV "v111,v130,v131\n"
MF "v[72:75],v[28:31],v[196:199],v[72:75]\n"
"s_waitcnt lgkmcnt(0)\n"
"ds_read_b128 v[24:27],v0 offset:6144\n"
"ds_read_b128 v[28:31],v1 offset:6144\n"
MF "v[72:75],v[16:19],v[216:219],v[72:75]\n"
MF "v[72:75],v[20:23],v[236:239],v[72:75]\n"
MF "v[76:79],v[20:23],v[240:243],v[76:79]\n"
MF "v[76:79],v[16:19],v[220:223],v[76:79]\n"
MF "v[80:83],v[16:19],v[224:227],v[80:83]\n"
MF "v[80:83],v[20:23],v[244:247],v[80:83]\n"
MF "v[84:87],v[20:23],v[248:251],v[84:87]\n"
MF "v[84:87],v[16:19],v[228:231],v[84:87]\n"
"v_max3_f32 v10,v57,v58,v59\n"
"v_max3_f32 v10,v10,v60,v61\n"
MF "v[88:91],v[16:19],v[232:235],v[88:91]\n"
"v_cmp_le_f32 vcc,0x47800000,v10\n"
MF "v[88:91],v[20:23],v[252:255],v[88:91]\n"
"s_waitcnt lgkmcnt(0)\n"
"s_cbranch_vccnz .Lstub_8\n"
".Lback_8:\n"
MF "v[112:115],v[24:27],v[136:139],v[32:35]\n"
"s_add_u32 s74,s74,12288\n"
"ds_read_b128 v[16:19],v0 offset:7168\n"
"ds_read_b128 v[20:23],v1 offset:7168\n"
MF "v[112:115],v[28:31],v[156:159],v[112:115]\n"
"s_cmp_eq_u32 s74,s20\n"
"s_cselect_b32 s74,s70,s74\n"
MF "v[116:119],v[28:31],v[160:163],v[36:39]\n"
"v_exp_f32 v72,v72\n"
MF "v[116:119],v[24:27],v[140:143],v[116:119]\n"
"v_exp_f32 v73,v73\n"
MF "v[120:123],v[24:27],v[144:147],v[40:43]\n"
"v_exp_f32 v74,v74\n"
MF "v[120:123],v[28:31],v[164:167],v[120:123]\n"
"v_exp_f32 v75,v75\n"
MF "v[124:127],v[28:31],v[168:171],v[44:47]\n"
"v_add_f32 v52,v57,v72\n"
"v_add_f32 v52,v52,v73\n"
MF "v[124:127],v[24:27],v[148:151],v[124:127]\n"
CV "v72,v72,v73\n"
"v_add_f32 v52,v52,v74\n"
MF "v[128:131],v[24:27],v[152:155],v[48:51]\n"
"v_add_f32 v52,v52,v75\n"
CV "v73,v74,v75\n"
MF "v[128:131],v[28:31],v[172:175],v[128:131]\n"
"s_waitcnt lgkmcnt(0)\n"
"ds_read_b128 v[24:27],v0 offset:8192\n"
"ds_read_b128 v[28:31],v1 offset:8192\n"
MF "v[128:131],v[16:19],v[192:195],v[128:131]\n"
"v_exp_f32 v76,v76\n"
MF "v[128:131],v[20:23],v[212:215],v[128:131]\n"
"v_exp_f32 v77,v77\n"
MF "v[124:127],v[20:23],v[208:211],v[124:127]\n"
"v_exp_f32 v78,v78\n"
MF "v[124:127],v[16:19],v[188:191],v[124:127]\n"
"v_exp_f32 v79,v79\n"
MF "v[120:123],v[16:19],v[184:187],v[120:123]\n"
"v_add_f32 v53,v58,v76\n"
"v_add_f32 v53,v53,v77\n"
MF "v[120:123],v[20:23],v[204:207],v[120:123]\n"
CV "v76,v76,v77\n"
"v_add_f32 v53,v53,v78\n"
MF "v[116:119],v[20:23],v[200:203],v[116:119]\n"
"s_waitcnt vmcnt(12) lgkmcnt(0)\n"
"v_add_f32 v53,v53,v79\n"
CV "v77,v78,v79\n"
"s_mov_b64 exec,0\n"
"s_cmp_eq_u32 s56,0\n"
"s_cbranch_scc1 .Lpg6f\n"
"s_cmp_eq_u32 s56,1\n"
"s_cbranch_scc1 .Lpg5f\n"
"s_cmp_eq_u32 s56,2\n"
"s_cbranch_scc1 .Lpg4f\n"
"s_branch .Lpgq0_f\n"
".Lisr3:\n"
"s_mov_b64 exec,-1\n"
".Lisx3:\n"
MF "v[116:119],v[16:19],v[180:183],v[116:119]\n"
"s_barrier\n"
MF "v[112:115],v[16:19],v[176:179],v[112:115]\n"
"v_exp_f32 v80,v80\n"
MF "v[112:115],v[20:23],v[196:199],v[112:115]\n"
"s_nop 0\n"
TR "v[16:17],v2 offset:24576\n"
TR "v[18:19],v2 offset:30720\n"
MF "v[112:115],v[24:27],v[216:219],v[112:115]\n"
"s_add_u32 s66,s67,4718592\n"
TR "v[20:21],v3 offset:24576\n"
TR "v[22:23],v3 offset:30720\n"
MF "v[112:115],v[28:31],v[236:239],v[112:115]\n"
"s_sub_u32 s81,s81,384\n"
"s_mov_b32 s83,7077888\n"
"s_add_u32 m0,s62,0\n"
MF "v[116:119],v[28:31],v[240:243],v[116:119]\n"
"s_add_u32 s82,s66,2359296\n"
"s_sub_u32 s15,s82,7864320\n"
"s_min_u32 s82,s82,s15\n"
MF "v[116:119],v[24:27],v[220:223],v[116:119]\n"
LD "v6,s[28:31],s82 offen lds\n"
MF "v[120:123],v[24:27],v[224:227],v[120:123]\n"
"s_add_u32 s82,s82,97280\n"
"v_exp_f32 v81,v81\n"
MF "v[120:123],v[28:31],v[244:247],v[120:123]\n"
LD "v6,s[28:31],s82 offen offset:1024 lds\n"
MF "v[124:127],v[28:31],v[248:251],v[124:127]\n"
"v_exp_f32 v82,v82\n"
MF "v[124:127],v[24:27],v[228:231],v[124:127]\n"
"v_exp_f32 v83,v83\n"
MF "v[128:131],v[24:27],v[232:235],v[128:131]\n"
"v_add_f32 v54,v59,v80\n"
"v_add_f32 v54,v54,v81\n"
MF "v[128:131],v[28:31],v[252:255],v[128:131]\n"
"s_waitcnt lgkmcnt(0)\n"
TR "v[24:25],v4 offset:24576\n"
TR "v[26:27],v4 offset:30720\n"
MF "a[0:3],v[16:19],v[92:95],a[0:3]\n"
"s_add_u32 s82,s82,97280\n"
TR "v[28:29],v5 offset:24576\n"
TR "v[30:31],v5 offset:30720\n"
MF "a[20:23],v[20:23],v[92:95],a[20:23]\n"
LD "v6,s[28:31],s82 offen offset:2048 lds\n"
MF "a[24:27],v[20:23],v[96:99],a[24:27]\n"
CV "v80,v80,v81\n"
"v_add_f32 v54,v54,v82\n"
MF "a[4:7],v[16:19],v[96:99],a[4:7]\n"
"v_add_f32 v54,v54,v83\n"
CV "v81,v82,v83\n"
MF "a[8:11],v[16:19],v[100:103],a[8:11]\n"
"s_add_u32 s82,s82,97280\n"
"v_exp_f32 v84,v84\n"
MF "a[28:31],v[20:23],v[100:103],a[28:31]\n"
LD "v6,s[28:31],s82 offen offset:3072 lds\n"
MF "a[32:35],v[20:23],v[104:107],a[32:35]\n"
"v_exp_f32 v85,v85\n"
MF "a[12:15],v[16:19],v[104:107],a[12:15]\n"
"v_exp_f32 v86,v86\n"
MF "a[16:19],v[16:19],v[108:111],a[16:19]\n"
"v_exp_f32 v87,v87\n"
MF "a[36:39],v[20:23],v[108:111],a[36:39]\n"
"s_waitcnt lgkmcnt(0)\n"
TR "v[16:17],v2 offset:25600\n"
TR "v[18:19],v2 offset:31744\n"
MF "a[56:59],v[24:27],v[108:111],a[56:59]\n"
"s_nop 0\n"
TR "v[20:21],v3 offset:25600\n"
TR "v[22:23],v3 offset:31744\n"
MF "a[76:79],v[28:31],v[108:111],a[76:79]\n"
"v_add_f32 v55,v60,v84\n"
"v_add_f32 v55,v55,v85\n"
MF "a[72:75],v[28:31],v[104:107],a[72:75]\n"
CV "v84,v84,v85\n"
"v_add_f32 v55,v55,v86\n"
MF "a[52:55],v[24:27],v[104:107],a[52:55]\n"
"v_add_f32 v55,v55,v87\n"
CV "v85,v86,v87\n"
MF "a[48:51],v[24:27],v[100:103],a[48:51]\n"
"v_exp_f32 v88,v88\n"
MF "a[68:71],v[28:31],v[100:103],a[68:71]\n"
"v_exp_f32 v89,v89\n"
MF "a[64:67],v[28:31],v[96:99],a[64:67]\n"
"v_exp_f32 v90,v90\n"
MF "a[44:47],v[24:27],v[96:99],a[44:47]\n"
"v_exp_f32 v91,v91\n"
MF "a[40:43],v[24:27],v[92:95],a[40:43]\n"
"v_add_f32 v56,v61,v88\n"
"v_add_f32 v56,v56,v89\n"
MF "a[60:63],v[28:31],v[92:95],a[60:63]\n"
"s_waitcnt lgkmcnt(0)\n"
TR "v[24:25],v4 offset:25600\n"
TR "v[26:27],v4 offset:31744\n"
MF "a[80:83],v[16:19],v[92:95],a[80:83]\n"
"s_nop 0\n"
TR "v[28:29],v5 offset:25600\n"
TR "v[30:31],v5 offset:31744\n"
MF "a[100:103],v[20:23],v[92:95],a[100:103]\n"
CV "v88,v88,v89\n"
"v_add_f32 v56,v56,v90\n"
MF "a[104:107],v[20:23],v[96:99],a[104:107]\n"
"v_add_f32 v56,v56,v91\n"
CV "v89,v90,v91\n"
MF "a[84:87],v[16:19],v[96:99],a[84:87]\n"
"v_exp_f32 v112,v112\n"
MF "a[88:91],v[16:19],v[100:103],a[88:91]\n"
"v_exp_f32 v113,v113\n"
MF "a[108:111],v[20:23],v[100:103],a[108:111]\n"
"v_exp_f32 v114,v114\n"
MF "a[112:115],v[20:23],v[104:107],a[112:115]\n"
"v_exp_f32 v115,v115\n"
MF "a[92:95],v[16:19],v[104:107],a[92:95]\n"
"v_add_f32 v52,v52,v112\n"
"v_add_f32 v52,v52,v113\n"
MF "a[96:99],v[16:19],v[108:111],a[96:99]\n"
CV "v74,v112,v113\n"
"v_add_f32 v52,v52,v114\n"
MF "a[116:119],v[20:23],v[108:111],a[116:119]\n"
"s_waitcnt lgkmcnt(0)\n"
TR "v[16:17],v2 offset:26624\n"
TR "v[18:19],v2 offset:32768\n"
MF "a[136:139],v[24:27],v[108:111],a[136:139]\n"
"s_nop 0\n"
TR "v[20:21],v3 offset:26624\n"
TR "v[22:23],v3 offset:32768\n"
MF "a[156:159],v[28:31],v[108:111],a[156:159]\n"
"v_add_f32 v52,v52,v115\n"
CV "v75,v114,v115\n"
MF "a[152:155],v[28:31],v[104:107],a[152:155]\n"
"v_exp_f32 v116,v116\n"
MF "a[132:135],v[24:27],v[104:107],a[132:135]\n"
"v_exp_f32 v117,v117\n"
MF "a[128:131],v[24:27],v[100:103],a[128:131]\n"
"v_exp_f32 v118,v118\n"
MF "a[148:151],v[28:31],v[100:103],a[148:151]\n"
"v_exp_f32 v119,v119\n"
MF "a[144:147],v[28:31],v[96:99],a[144:147]\n"
"v_add_f32 v53,v53,v116\n"
"v_add_f32 v53,v53,v117\n"
MF "a[124:127],v[24:27],v[96:99],a[124:127]\n"
CV "v78,v116,v117\n"
"v_add_f32 v53,v53,v118\n"
MF "a[120:123],v[24:27],v[92:95],a[120:123]\n"
"v_add_f32 v53,v53,v119\n"
CV "v79,v118,v119\n"
MF "a[140:143],v[28:31],v[92:95],a[140:143]\n"
"s_waitcnt lgkmcnt(0)\n"
TR "v[24:25],v4 offset:26624\n"
TR "v[26:27],v4 offset:32768\n"
MF "a[160:163],v[16:19],v[92:95],a[160:163]\n"
"s_nop 0\n"
TR "v[28:29],v5 offset:26624\n"
TR "v[30:31],v5 offset:32768\n"
MF "a[180:183],v[20:23],v[92:95],a[180:183]\n"
"v_exp_f32 v120,v120\n"
MF "a[184:187],v[20:23],v[96:99],a[184:187]\n"
"v_exp_f32 v121,v121\n"
MF "a[164:167],v[16:19],v[96:99],a[164:167]\n"
"v_exp_f32 v122,v122\n"
MF "a[168:171],v[16:19],v[100:103],a[168:171]\n"
"v_exp_f32 v123,v123\n"
MF "a[188:191],v[20:23],v[100:103],a[188:191]\n"
"v_add_f32 v54,v54,v120\n"
"v_add_f32 v54,v54,v121\n"
MF "a[192:195],v[20:23],v[104:107],a[192:195]\n"
CV "v82,v120,v121\n"
"v_add_f32 v54,v54,v122\n"
MF "a[172:175],v[16:19],v[104:107],a[172:175]\n"
"s_waitcnt vmcnt(10) lgkmcnt(0)\n"
"v_add_f32 v54,v54,v123\n"
CV "v83,v122,v123\n"
MF "a[176:179],v[16:19],v[108:111],a[176:179]\n"
"s_barrier\n"
MF "a[196:199],v[20:23],v[108:111],a[196:199]\n"
"s_add_u32 s73,s73,12288\n"
"ds_read_b128 v[16:19],v0 offset:24576\n"
"ds_read_b128 v[20:23],v1 offset:24576\n"
MF "a[216:219],v[24:27],v[108:111],a[216:219]\n"
"s_cmp_eq_u32 s73,s19\n"
"s_cselect_b32 s73,s18,s73\n"
"s_add_u32 m0,s65,73728\n"
MF "a[236:239],v[28:31],v[108:111],a[236:239]\n"
LD "v8,s[36:39],s73 offen nt lds\n"
MF "a[232:235],v[28:31],v[104:107],a[232:235]\n"
"s_add_u32 m0,s65,74624\n"
MF "a[212:215],v[24:27],v[104:107],a[212:215]\n"
LD "v8,s[36:39],s73 offen offset:128 nt lds\n"
MF "a[208:211],v[24:27],v[100:103],a[208:211]\n"
"s_add_u32 m0,s65,75520\n"
MF "a[228:231],v[28:31],v[100:103],a[228:231]\n"
LD "v8,s[36:39],s73 offen offset:256 nt lds\n"
MF "a[224:227],v[28:31],v[96:99],a[224:227]\n"
MF "a[204:207],v[24:27],v[96:99],a[204:207]\n"
MF "a[200:203],v[24:27],v[92:95],a[200:203]\n"
MF "a[220:223],v[28:31],v[92:95],a[220:223]\n"
"s_waitcnt lgkmcnt(0)\n"
MF "v[92:95],v[16:19],v[136:139],v[32:35]\n"
"s_add_u32 m0,s65,73728\n"
"ds_read_b128 v[24:27],v0 offset:25600\n"
"ds_read_b128 v[28:31],v1 offset:25600\n"
MF "v[92:95],v[20:23],v[156:159],v[92:95]\n"
LD "v8,s[36:39],s73 offen offset:3072 nt lds\n"
MF "v[96:99],v[20:23],v[160:163],v[36:39]\n"
"v_exp_f32 v124,v124\n"
MF "v[96:99],v[16:19],v[140:143],v[96:99]\n"
"v_exp_f32 v125,v125\n"
MF "v[100:103],v[16:19],v[144:147],v[40:43]\n"
"s_add_u32 m0,s65,74624\n"
"v_exp_f32 v126,v126\n"
MF "v[100:103],v[20:23],v[164:167],v[100:103]\n"
LD "v8,s[36:39],s73 offen offset:3200 nt lds\n"
MF "v[104:107],v[20:23],v[168:171],v[44:47]\n"
"v_exp_f32 v127,v127\n"
MF "v[104:107],v[16:19],v[148:151],v[104:107]\n"
"s_add_u32 m0,s65,75520\n"
"v_add_f32 v55,v55,v124\n"
"v_add_f32 v55,v55,v125\n"
MF "v[108:111],v[16:19],v[152:155],v[48:51]\n"
LD "v8,s[36:39],s73 offen offset:3328 nt lds\n"
MF "v[108:111],v[20:23],v[172:175],v[108:111]\n"
"s_waitcnt lgkmcnt(0)\n"
"ds_read_b128 v[16:19],v0 offset:26624\n"
"ds_read_b128 v[20:23],v1 offset:26624\n"
MF "v[108:111],v[24:27],v[192:195],v[108:111]\n"
CV "v86,v124,v125\n"
"v_add_f32 v55,v55,v126\n"
MF "v[108:111],v[28:31],v[212:215],v[108:111]\n"
"v_add_f32 v55,v55,v127\n"
CV "v87,v126,v127\n"
MF "v[104:107],v[28:31],v[208:211],v[104:107]\n"
"v_exp_f32 v128,v128\n"
MF "v[104:107],v[24:27],v[188:191],v[104:107]\n"
"v_exp_f32 v129,v129\n"
MF "v[100:103],v[24:27],v[184:187],v[100:103]\n"
"v_exp_f32 v130,v130\n"
MF "v[100:103],v[28:31],v[204:207],v[100:103]\n"
"v_exp_f32 v131,v131\n"
MF "v[96:99],v[28:31],v[200:203],v[96:99]\n"
"v_add_f32 v56,v56,v128\n"
"v_add_f32 v56,v56,v129\n"
MF "v[96:99],v[24:27],v[180:183],v[96:99]\n"
CV "v90,v128,v129\n"
"v_add_f32 v56,v56,v130\n"
MF "v[92:95],v[24:27],v[176:179],v[92:95]\n"
"v_add_f32 v56,v56,v131\n"
CV "v91,v130,v131\n"
MF "v[92:95],v[28:31],v[196:199],v[92:95]\n"
"s_waitcnt lgkmcnt(0)\n"
"ds_read_b128 v[24:27],v0 offset:30720\n"
"ds_read_b128 v[28:31],v1 offset:30720\n"
MF "v[92:95],v[16:19],v[216:219],v[92:95]\n"
MF "v[92:95],v[20:23],v[236:239],v[92:95]\n"
MF "v[96:99],v[20:23],v[240:243],v[96:99]\n"
MF "v[96:99],v[16:19],v[220:223],v[96:99]\n"
MF "v[100:103],v[16:19],v[224:227],v[100:103]\n"
MF "v[100:103],v[20:23],v[244:247],v[100:103]\n"
MF "v[104:107],v[20:23],v[248:251],v[104:107]\n"
MF "v[104:107],v[16:19],v[228:231],v[104:107]\n"
"v_max3_f32 v10,v52,v53,v54\n"
"v_max3_f32 v10,v10,v55,v56\n"
MF "v[108:111],v[16:19],v[232:235],v[108:111]\n"
"v_cmp_le_f32 vcc,0x47800000,v10\n"
MF "v[108:111],v[20:23],v[252:255],v[108:111]\n"
"s_waitcnt lgkmcnt(0)\n"
"s_cbranch_vccnz .Lstub_9\n"
".Lback_9:\n"
MF "v[112:115],v[24:27],v[136:139],v[32:35]\n"
"s_add_u32 s74,s74,12288\n"
"ds_read_b128 v[16:19],v0 offset:31744\n"
"ds_read_b128 v[20:23],v1 offset:31744\n"
MF "v[112:115],v[28:31],v[156:159],v[112:115]\n"
"s_cmp_eq_u32 s74,s20\n"
"s_cselect_b32 s74,s70,s74\n"
MF "v[116:119],v[28:31],v[160:163],v[36:39]\n"
"v_exp_f32 v92,v92\n"
MF "v[116:119],v[24:27],v[140:143],v[116:119]\n"
"v_exp_f32 v93,v93\n"
MF "v[120:123],v[24:27],v[144:147],v[40:43]\n"
"v_exp_f32 v94,v94\n"
MF "v[120:123],v[28:31],v[164:167],v[120:123]\n"
"v_exp_f32 v95,v95\n"
MF "v[124:127],v[28:31],v[168:171],v[44:47]\n"
"v_add_f32 v57,v52,v92\n"
"v_add_f32 v57,v57,v93\n"
MF "v[124:127],v[24:27],v[148:151],v[124:127]\n"
CV "v92,v92,v93\n"
"v_add_f32 v57,v57,v94\n"
MF "v[128:131],v[24:27],v[152:155],v[48:51]\n"
"v_add_f32 v57,v57,v95\n"
CV "v93,v94,v95\n"
MF "v[128:131],v[28:31],v[172:175],v[128:131]\n"
"s_waitcnt lgkmcnt(0)\n"
"ds_read_b128 v[24:27],v0 offset:32768\n"
"ds_read_b128 v[28:31],v1 offset:32768\n"
MF "v[128:131],v[16:19],v[192:195],v[128:131]\n"
"v_exp_f32 v96,v96\n"
MF "v[128:131],v[20:23],v[212:215],v[128:131]\n"
"v_exp_f32 v97,v97\n"
MF "v[124:127],v[20:23],v[208:211],v[124:127]\n"
"v_exp_f32 v98,v98\n"
MF "v[124:127],v[16:19],v[188:191],v[124:127]\n"
"v_exp_f32 v99,v99\n"
MF "v[120:123],v[16:19],v[184:187],v[120:123]\n"
"v_add_f32 v58,v53,v96\n"
"v_add_f32 v58,v58,v97\n"
MF "v[120:123],v[20:23],v[204:207],v[120:123]\n"
CV "v96,v96,v97\n"
"v_add_f32 v58,v58,v98\n"
MF "v[116:119],v[20:23],v[200:203],v[116:119]\n"
"s_waitcnt vmcnt(10) lgkmcnt(0)\n"
"v_add_f32 v58,v58,v99\n"
CV "v97,v98,v99\n"
MF "v[116:119],v[16:19],v[180:183],v[116:119]\n"
"s_barrier\n"
MF "v[112:115],v[16:19],v[176:179],v[112:115]\n"
"v_exp_f32 v100,v100\n"
MF "v[112:115],v[20:23],v[196:199],v[112:115]\n"
"s_nop 0\n"
TR "v[16:17],v2\n"
TR "v[18:19],v2 offset:6144\n"
MF "v[112:115],v[24:27],v[216:219],v[112:115]\n"
"s_add_u32 m0,s62,16384\n"
TR "v[20:21],v3\n"
TR "v[22:23],v3 offset:6144\n"
MF "v[112:115],v[28:31],v[236:239],v[112:115]\n"
"s_add_u32 s82,s66,2359424\n"
"s_sub_u32 s15,s82,7864320\n"
"s_min_u32 s82,s82,s15\n"
MF "v[116:119],v[28:31],v[240:243],v[116:119]\n"
LD "v6,s[28:31],s82 offen lds\n"
MF "v[116:119],v[24:27],v[220:223],v[116:119]\n"
"s_add_u32 s82,s82,97280\n"
"v_exp_f32 v101,v101\n"
MF "v[120:123],v[24:27],v[224:227],v[120:123]\n"
LD "v6,s[28:31],s82 offen offset:1024 lds\n"
MF "v[120:123],v[28:31],v[244:247],v[120:123]\n"
"v_exp_f32 v102,v102\n"
MF "v[124:127],v[28:31],v[248:251],v[124:127]\n"
"s_add_u32 s82,s82,97280\n"
"v_exp_f32 v103,v103\n"
MF "v[124:127],v[24:27],v[228:231],v[124:127]\n"
LD "v6,s[28:31],s82 offen offset:2048 lds\n"
MF "v[128:131],v[24:27],v[232:235],v[128:131]\n"
"v_add_f32 v59,v54,v100\n"
"v_add_f32 v59,v59,v101\n"
MF "v[128:131],v[28:31],v[252:255],v[128:131]\n"
"s_waitcnt lgkmcnt(0)\n"
TR "v[24:25],v4\n"
TR "v[26:27],v4 offset:6144\n"
MF "a[0:3],v[16:19],v[72:75],a[0:3]\n"
"s_add_u32 s82,s82,97280\n"
TR "v[28:29],v5\n"
TR "v[30:31],v5 offset:6144\n"
MF "a[20:23],v[20:23],v[72:75],a[20:23]\n"
LD "v6,s[28:31],s82 offen offset:3072 lds\n"
MF "a[24:27],v[20:23],v[76:79],a[24:27]\n"
"s_add_u32 m0,s62,32768\n"
CV "v100,v100,v101\n"
"v_add_f32 v59,v59,v102\n"
MF "a[4:7],v[16:19],v[76:79],a[4:7]\n"
"s_add_u32 s82,s66,2359552\n"
"s_sub_u32 s15,s82,7864320\n"
"s_min_u32 s82,s82,s15\n"
MF "a[8:11],v[16:19],v[80:83],a[8:11]\n"
".Lpg12f:\n"
"s_cbranch_execz .Lpg18f\n"
LD "v6,s[28:31],s82 offen lds\n"
MF "a[28:31],v[20:23],v[80:83],a[28:31]\n"
"v_add_f32 v59,v59,v103\n"
CV "v101,v102,v103\n"
MF "a[32:35],v[20:23],v[84:87],a[32:35]\n"
"s_add_u32 s82,s82,97280\n"
"v_exp_f32 v104,v104\n"
MF "a[12:15],v[16:19],v[84:87],a[12:15]\n"
LD "v6,s[28:31],s82 offen offset:1024 lds\n"
MF "a[16:19],v[16:19],v[88:91],a[16:19]\n"
"v_exp_f32 v105,v105\n"
MF "a[36:39],v[20:23],v[88:91],a[36:39]\n"
"s_waitcnt lgkmcnt(0)\n"
TR "v[16:17],v2 offset:1024\n"
TR "v[18:19],v2 offset:7168\n"
MF "a[56:59],v[24:27],v[88:91],a[56:59]\n"
"s_nop 0\n"
TR "v[20:21],v3 offset:1024\n"
TR "v[22:23],v3 offset:7168\n"
MF "a[76:79],v[28:31],v[88:91],a[76:79]\n"
"v_exp_f32 v106,v106\n"
MF "a[72:75],v[28:31],v[84:87],a[72:75]\n"
"v_exp_f32 v107,v107\n"
MF "a[52:55],v[24:27],v[84:87],a[52:55]\n"
"v_add_f32 v60,v55,v104\n"
"v_add_f32 v60,v60,v105\n"
MF "a[48:51],v[24:27],v[80:83],a[48:51]\n"
CV "v104,v104,v105\n"
"v_add_f32 v60,v60,v106\n"
MF "a[68:71],v[28:31],v[80:83],a[68:71]\n"
"v_add_f32 v60,v60,v107\n"
CV "v105,v106,v107\n"
MF "a[64:67],v[28:31],v[76:79],a[64:67]\n"
"v_exp_f32 v108,v108\n"
MF "a[44:47],v[24:27],v[76:79],a[44:47]\n"
"v_exp_f32 v109,v109\n"
MF "a[40:43],v[24:27],v[72:75],a[40:43]\n"
"v_exp_f32 v110,v110\n"
MF "a[60:63],v[28:31],v[72:75],a[60:63]\n"
"s_waitcnt lgkmcnt(0)\n"
TR "v[24:25],v4 offset:1024\n"
TR "v[26:27],v4 offset:7168\n"
MF "a[80:83],v[16:19],v[72:75],a[80:83]\n"
"s_nop 0\n"
TR "v[28:29],v5 offset:1024\n"
TR "v[30:31],v5 offset:7168\n"
MF "a[100:103],v[20:23],v[72:75],a[100:103]\n"
"v_exp_f32 v111,v111\n"
MF "a[104:107],v[20:23],v[76:79],a[104:107]\n"
"v_add_f32 v61,v56,v108\n"
"v_add_f32 v61,v61,v109\n"
MF "a[84:87],v[16:19],v[76:79],a[84:87]\n"
CV "v108,v108,v109\n"
"v_add_f32 v61,v61,v110\n"
MF "a[88:91],v[16:19],v[80:83],a[88:91]\n"
"v_add_f32 v61,v61,v111\n"
CV "v109,v110,v111\n"
MF "a[108:111],v[20:23],v[80:83],a[108:111]\n"
"v_exp_f32 v112,v112\n"
MF "a[112:115],v[20:23],v[84:87],a[112:115]\n"
"v_exp_f32 v113,v113\n"
MF "a[92:95],v[16:19],v[84:87],a[92:95]\n"
"v_exp_f32 v114,v114\n"
MF "a[96:99],v[16:19],v[88:91],a[96:99]\n"
"v_exp_f32 v115,v115\n"
MF "a[116:119],v[20:23],v[88:91],a[116:119]\n"
"s_waitcnt lgkmcnt(0)\n"
TR "v[16:17],v2 offset:2048\n"
TR "v[18:19],v2 offset:8192\n"
MF "a[136:139],v[24:27],v[88:91],a[136:139]\n"
"s_nop 0\n"
TR "v[20:21],v3 offset:2048\n"
TR "v[22:23],v3 offset:8192\n"
MF "a[156:159],v[28:31],v[88:91],a[156:159]\n"
"v_add_f32 v57,v57,v112\n"
"v_add_f32 v57,v57,v113\n"
MF "a[152:155],v[28:31],v[84:87],a[152:155]\n"
CV "v94,v112,v113\n"
"v_add_f32 v57,v57,v114\n"
MF "a[132:135],v[24:27],v[84:87],a[132:135]\n"
"v_add_f32 v57,v57,v115\n"
CV "v95,v114,v115\n"
MF "a[128:131],v[24:27],v[80:83],a[128:131]\n"
"v_exp_f32 v116,v116\n"
MF "a[148:151],v[28:31],v[80:83],a[148:151]\n"
"v_exp_f32 v117,v117\n"
MF "a[144:147],v[28:31],v[76:79],a[144:147]\n"
"v_exp_f32 v118,v118\n"
MF "a[124:127],v[24:27],v[76:79],a[124:127]\n"
"v_exp_f32 v119,v119\n"
MF "a[120:123],v[24:27],v[72:75],a[120:123]\n"
"v_add_f32 v58,v58,v116\n"
"v_add_f32 v58,v58,v117\n"
MF "a[140:143],v[28:31],v[72:75],a[140:143]\n"
"s_waitcnt lgkmcnt(0)\n"
TR "v[24:25],v4 offset:2048\n"
TR "v[26:27],v4 offset:8192\n"
MF "a[160:163],v[16:19],v[72:75],a[160:163]\n"
"s_nop 0\n"
TR "v[28:29],v5 offset:2048\n"
TR "v[30:31],v5 offset:8192\n"
MF "a[180:183],v[20:23],v[72:75],a[180:183]\n"
CV "v98,v116,v117\n"
"v_add_f32 v58,v58,v118\n"
MF "a[184:187],v[20:23],v[76:79],a[184:187]\n"
"v_add_f32 v58,v58,v119\n"
CV "v99,v118,v119\n"
MF "a[164:167],v[16:19],v[76:79],a[164:167]\n"
"v_exp_f32 v120,v120\n"
MF "a[168:171],v[16:19],v[80:83],a[168:171]\n"
"v_exp_f32 v121,v121\n"
MF "a[188:191],v[20:23],v[80:83],a[188:191]\n"
"v_exp_f32 v122,v122\n"
MF "a[192:195],v[20:23],v[84:87],a[192:195]\n"
"v_exp_f32 v123,v123\n"
MF "a[172:175],v[16:19],v[84:87],a[172:175]\n"
"v_add_f32 v59,v59,v120\n"
"v_add_f32 v59,v59,v121\n"
MF "a[176:179],v[16:19],v[88:91],a[176:179]\n"
"s_waitcnt lgkmcnt(0)\n"
CV "v102,v120,v121\n"
"v_add_f32 v59,v59,v122\n"
MF "a[196:199],v[20:23],v[88:91],a[196:199]\n"
"s_add_u32 s82,s82,97280\n"
"v_add_f32 v59,v59,v123\n"
CV "v103,v122,v123\n"
MF "a[216:219],v[24:27],v[88:91],a[216:219]\n"
LD "v6,s[28:31],s82 offen offset:2048 lds\n"
MF "a[236:239],v[28:31],v[88:91],a[236:239]\n"
"v_exp_f32 v124,v124\n"
MF "a[232:235],v[28:31],v[84:87],a[232:235]\n"
"s_add_u32 s82,s82,97280\n"
"v_exp_f32 v125,v125\n"
MF "a[212:215],v[24:27],v[84:87],a[212:215]\n"
LD "v6,s[28:31],s82 offen offset:3072 lds\n"
MF "a[208:211],v[24:27],v[80:83],a[208:211]\n"
"v_exp_f32 v126,v126\n"
MF "a[228:231],v[28:31],v[80:83],a[228:231]\n"
"v_exp_f32 v127,v127\n"
MF "a[224:227],v[28:31],v[76:79],a[224:227]\n"
"v_add_f32 v60,v60,v124\n"
"v_add_f32 v60,v60,v125\n"
MF "a[204:207],v[24:27],v[76:79],a[204:207]\n"
CV "v106,v124,v125\n"
"v_add_f32 v60,v60,v126\n"
MF "a[200:203],v[24:27],v[72:75],a[200:203]\n"
"v_add_f32 v60,v60,v127\n"
CV "v107,v126,v127\n"
MF "a[220:223],v[28:31],v[72:75],a[220:223]\n"
"v_exp_f32 v128,v128\n"
"v_exp_f32 v129,v129\n"
"v_exp_f32 v130,v130\n"
"v_exp_f32 v131,v131\n"
"v_add_f32 v61,v61,v128\n"
"v_add_f32 v61,v61,v129\n"
CV "v110,v128,v129\n"
"v_add_f32 v61,v61,v130\n"
"v_add_f32_e64 v61,v61,v131\n"
CV "v111,v130,v131\n"
"v_max3_f32 v10,v57,v58,v59\n"
"v_max3_f32 v10,v10,v60,v61\n"
"v_cmp_le_f32 vcc,0x47800000,v10\n"
"s_cbranch_vccnz .Lstub_10\n"
".Lback_10:\n"
"s_waitcnt vmcnt(8) lgkmcnt(0)\n"
"s_barrier\n"
"v_add_u32 v11,97280,v6\n"
"v_add_u32 v12,194560,v6\n"
"v_add_u32 v13,291840,v6\n"
"v_mov_b32 v10,1.0\n"
TR "v[136:137],v2 offset:24576\n"
TR "v[138:139],v2 offset:30720\n"
"v_mfma_f32_16x16x4_f32 v[16:19],v10,v57,0\n"
TR "v[140:141],v3 offset:24576\n"
TR "v[142:143],v3 offset:30720\n"
TR "v[144:145],v4 offset:24576\n"
TR "v[146:147],v4 offset:30720\n"
TR "v[148:149],v5 offset:24576\n"
TR "v[150:151],v5 offset:30720\n"
"v_mfma_f32_16x16x4_f32 v[20:23],v10,v58,0\n"
TR "v[152:153],v2 offset:25600\n"
TR "v[154:155],v2 offset:31744\n"
TR "v[156:157],v3 offset:25600\n"
TR "v[158:159],v3 offset:31744\n"
TR "v[160:161],v4 offset:25600\n"
TR "v[162:163],v4 offset:31744\n"
"v_mfma_f32_16x16x4_f32 v[24:27],v10,v59,0\n"
TR "v[164:165],v5 offset:25600\n"
TR "v[166:167],v5 offset:31744\n"
TR "v[168:169],v2 offset:26624\n"
TR "v[170:171],v2 offset:32768\n"
TR "v[172:173],v3 offset:26624\n"
TR "v[174:175],v3 offset:32768\n"
"v_mfma_f32_16x16x4_f32 v[28:31],v10,v60,0\n"
TR "v[176:177],v4 offset:26624\n"
TR "v[178:179],v4 offset:32768\n"
TR "v[180:181],v5 offset:26624\n"
TR "v[182:183],v5 offset:32768\n"
"v_mfma_f32_16x16x4_f32 v[32:35],v10,v61,0\n"
"s_waitcnt lgkmcnt(15)\n"
MF "a[0:3],v[136:139],v[92:95],a[0:3]\n"
MF "a[20:23],v[140:143],v[92:95],a[20:23]\n"
"v_rcp_f32 v57,v16\n"
"v_rcp_f32 v58,v20\n"
"v_rcp_f32 v59,v24\n"
"v_rcp_f32 v60,v28\n"
"s_nop 2\n"
"v_rcp_f32_e64 v61,v32\n"
MF "a[4:7],v[136:139],v[96:99],a[4:7]\n"
"v_mul_f32_e64 v52,-s13,v57\n"
"v_mul_f32_e64 v53,-s13,v58\n"
"v_mul_f32_e64 v54,-s13,v59\n"
"v_mul_f32_e64 v55,-s13,v60\n"
"v_mul_f32_e64 v56,-s13,v61\n"
AR "v72,a0\n"
AR "v73,a1\n"
AR "v74,a2\n"
MF "a[24:27],v[140:143],v[96:99],a[24:27]\n"
AR "v75,a3\n"
AR "v76,a20\n"
AR "v77,a21\n"
AR "v78,a22\n"
AR "v79,a23\n"
"v_pk_fma_f32 v[72:73],v[72:73],v[56:57],v[52:53] op_sel:[0,1,0] op_sel_hi:[1,1,0]\n"
"v_pk_fma_f32 v[74:75],v[74:75],v[56:57],v[52:53] op_sel:[0,1,0] op_sel_hi:[1,1,0]\n"
"v_pk_fma_f32 v[76:77],v[76:77],v[56:57],v[52:53] op_sel:[0,1,0] op_sel_hi:[1,1,0]\n"
"v_pk_fma_f32 v[78:79],v[78:79],v[56:57],v[52:53] op_sel:[0,1,0] op_sel_hi:[1,1,0]\n"
CV "v112,v72,v73\n"
CV "v113,v74,v75\n"
CV "v114,v76,v77\n"
CV "v115,v78,v79\n"
MF "a[8:11],v[136:139],v[100:103],a[8:11]\n"
AW "a0,v112\n"
AW "a1,v113\n"
AW "a2,v114\n"
AW "a3,v115\n"
AR "v80,a4\n"
AR "v81,a5\n"
AR "v82,a6\n"
MF "a[28:31],v[140:143],v[100:103],a[28:31]\n"
AR "v83,a7\n"
AR "v84,a24\n"
AR "v85,a25\n"
AR "v86,a26\n"
AR "v87,a27\n"
"v_pk_fma_f32 v[80:81],v[80:81],v[58:59],v[52:53] op_sel:[0,0,1] op_sel_hi:[1,0,1]\n"
"v_pk_fma_f32 v[82:83],v[82:83],v[58:59],v[52:53] op_sel:[0,0,1] op_sel_hi:[1,0,1]\n"
"v_pk_fma_f32 v[84:85],v[84:85],v[58:59],v[52:53] op_sel:[0,0,1] op_sel_hi:[1,0,1]\n"
"v_pk_fma_f32 v[86:87],v[86:87],v[58:59],v[52:53] op_sel:[0,0,1] op_sel_hi:[1,0,1]\n"
CV "v116,v80,v81\n"
CV "v117,v82,v83\n"
CV "v118,v84,v85\n"
CV "v119,v86,v87\n"
MF "a[12:15],v[136:139],v[104:107],a[12:15]\n"
AW "a4,v116\n"
AW "a5,v117\n"
AW "a6,v118\n"
AW "a7,v119\n"
AR "v72,a8\n"
AR "v73,a9\n"
AR "v74,a10\n"
MF "a[32:35],v[140:143],v[104:107],a[32:35]\n"
AR "v75,a11\n"
AR "v76,a28\n"
AR "v77,a29\n"
AR "v78,a30\n"
AR "v79,a31\n"
"v_pk_fma_f32 v[72:73],v[72:73],v[58:59],v[54:55] op_sel:[0,1,0] op_sel_hi:[1,1,0]\n"
"v_pk_fma_f32 v[74:75],v[74:75],v[58:59],v[54:55] op_sel:[0,1,0] op_sel_hi:[1,1,0]\n"
"v_pk_fma_f32 v[76:77],v[76:77],v[58:59],v[54:55] op_sel:[0,1,0] op_sel_hi:[1,1,0]\n"
"v_pk_fma_f32 v[78:79],v[78:79],v[58:59],v[54:55] op_sel:[0,1,0] op_sel_hi:[1,1,0]\n"
CV "v112,v72,v73\n"
CV "v113,v74,v75\n"
CV "v114,v76,v77\n"
CV "v115,v78,v79\n"
MF "a[16:19],v[136:139],v[108:111],a[16:19]\n"
AW "a8,v112\n"
AW "a9,v113\n"
AW "a10,v114\n"
AW "a11,v115\n"
AR "v80,a12\n"
AR "v81,a13\n"
AR "v82,a14\n"
MF "a[36:39],v[140:143],v[108:111],a[36:39]\n"
AR "v83,a15\n"
AR "v84,a32\n"
AR "v85,a33\n"
AR "v86,a34\n"
AR "v87,a35\n"
"v_pk_fma_f32 v[80:81],v[80:81],v[60:61],v[54:55] op_sel:[0,0,1] op_sel_hi:[1,0,1]\n"
"v_pk_fma_f32 v[82:83],v[82:83],v[60:61],v[54:55] op_sel:[0,0,1] op_sel_hi:[1,0,1]\n"
"v_pk_fma_f32 v[84:85],v[84:85],v[60:61],v[54:55] op_sel:[0,0,1] op_sel_hi:[1,0,1]\n"
"v_pk_fma_f32 v[86:87],v[86:87],v[60:61],v[54:55] op_sel:[0,0,1] op_sel_hi:[1,0,1]\n"
CV "v116,v80,v81\n"
CV "v117,v82,v83\n"
CV "v118,v84,v85\n"
CV "v119,v86,v87\n"
MF "a[40:43],v[144:147],v[92:95],a[40:43]\n"
AW "a12,v116\n"
AW "a13,v117\n"
AW "a14,v118\n"
AW "a15,v119\n"
AR "v72,a16\n"
AR "v73,a17\n"
AR "v74,a18\n"
MF "a[60:63],v[148:151],v[92:95],a[60:63]\n"
AR "v75,a19\n"
AR "v76,a36\n"
AR "v77,a37\n"
AR "v78,a38\n"
AR "v79,a39\n"
"v_pk_fma_f32 v[72:73],v[72:73],v[60:61],v[56:57] op_sel:[0,1,0] op_sel_hi:[1,1,0]\n"
"v_pk_fma_f32 v[74:75],v[74:75],v[60:61],v[56:57] op_sel:[0,1,0] op_sel_hi:[1,1,0]\n"
"v_pk_fma_f32 v[76:77],v[76:77],v[60:61],v[56:57] op_sel:[0,1,0] op_sel_hi:[1,1,0]\n"
"v_pk_fma_f32 v[78:79],v[78:79],v[60:61],v[56:57] op_sel:[0,1,0] op_sel_hi:[1,1,0]\n"
CV "v112,v72,v73\n"
CV "v113,v74,v75\n"
CV "v114,v76,v77\n"
CV "v115,v78,v79\n"
MF "a[44:47],v[144:147],v[96:99],a[44:47]\n"
AW "a16,v112\n"
AW "a17,v113\n"
AW "a18,v114\n"
AW "a19,v115\n"
AR "v80,a40\n"
AR "v81,a41\n"
AR "v82,a42\n"
MF "a[64:67],v[148:151],v[96:99],a[64:67]\n"
AR "v83,a43\n"
AR "v84,a60\n"
AR "v85,a61\n"
AR "v86,a62\n"
AR "v87,a63\n"
"v_pk_fma_f32 v[80:81],v[80:81],v[56:57],v[52:53] op_sel:[0,1,0] op_sel_hi:[1,1,0]\n"
"v_pk_fma_f32 v[82:83],v[82:83],v[56:57],v[52:53] op_sel:[0,1,0] op_sel_hi:[1,1,0]\n"
"v_pk_fma_f32 v[84:85],v[84:85],v[56:57],v[52:53] op_sel:[0,1,0] op_sel_hi:[1,1,0]\n"
"v_pk_fma_f32 v[86:87],v[86:87],v[56:57],v[52:53] op_sel:[0,1,0] op_sel_hi:[1,1,0]\n"
CV "v116,v80,v81\n"
CV "v117,v82,v83\n"
CV "v118,v84,v85\n"
CV "v119,v86,v87\n"
MF "a[48:51],v[144:147],v[100:103],a[48:51]\n"
AW "a40,v116\n"
AW "a41,v117\n"
AW "a42,v118\n"
AW "a43,v119\n"
AR "v72,a44\n"
AR "v73,a45\n"
AR "v74,a46\n"
MF "a[68:71],v[148:151],v[100:103],a[68:71]\n"
AR "v75,a47\n"
AR "v76,a64\n"
AR "v77,a65\n"
AR "v78,a66\n"
AR "v79,a67\n"
"v_pk_fma_f32 v[72:73],v[72:73],v[58:59],v[52:53] op_sel:[0,0,1] op_sel_hi:[1,0,1]\n"
"v_pk_fma_f32 v[74:75],v[74:75],v[58:59],v[52:53] op_sel:[0,0,1] op_sel_hi:[1,0,1]\n"
"v_pk_fma_f32 v[76:77],v[76:77],v[58:59],v[52:53] op_sel:[0,0,1] op_sel_hi:[1,0,1]\n"
"v_pk_fma_f32 v[78:79],v[78:79],v[58:59],v[52:53] op_sel:[0,0,1] op_sel_hi:[1,0,1]\n"
CV "v112,v72,v73\n"
CV "v113,v74,v75\n"
CV "v114,v76,v77\n"
CV "v115,v78,v79\n"
MF "a[52:55],v[144:147],v[104:107],a[52:55]\n"
AW "a44,v112\n"
AW "a45,v113\n"
AW "a46,v114\n"
AW "a47,v115\n"
AR "v80,a48\n"
AR "v81,a49\n"
AR "v82,a50\n"
MF "a[72:75],v[148:151],v[104:107],a[72:75]\n"
AR "v83,a51\n"
AR "v84,a68\n"
AR "v85,a69\n"
AR "v86,a70\n"
AR "v87,a71\n"
"v_pk_fma_f32 v[80:81],v[80:81],v[58:59],v[54:55] op_sel:[0,1,0] op_sel_hi:[1,1,0]\n"
"v_pk_fma_f32 v[82:83],v[82:83],v[58:59],v[54:55] op_sel:[0,1,0] op_sel_hi:[1,1,0]\n"
"v_pk_fma_f32 v[84:85],v[84:85],v[58:59],v[54:55] op_sel:[0,1,0] op_sel_hi:[1,1,0]\n"
"v_pk_fma_f32 v[86:87],v[86:87],v[58:59],v[54:55] op_sel:[0,1,0] op_sel_hi:[1,1,0]\n"
CV "v116,v80,v81\n"
CV "v117,v82,v83\n"
CV "v118,v84,v85\n"
CV "v119,v86,v87\n"
MF "a[56:59],v[144:147],v[108:111],a[56:59]\n"
AW "a48,v116\n"
AW "a49,v117\n"
AW "a50,v118\n"
AW "a51,v119\n"
AR "v72,a52\n"
AR "v73,a53\n"
AR "v74,a54\n"
MF "a[76:79],v[148:151],v[108:111],a[76:79]\n"
AR "v75,a55\n"
AR "v76,a72\n"
AR "v77,a73\n"
AR "v78,a74\n"
AR "v79,a75\n"
"v_pk_fma_f32 v[72:73],v[72:73],v[60:61],v[54:55] op_sel:[0,0,1] op_sel_hi:[1,0,1]\n"
"v_pk_fma_f32 v[74:75],v[74:75],v[60:61],v[54:55] op_sel:[0,0,1] op_sel_hi:[1,0,1]\n"
"v_pk_fma_f32 v[76:77],v[76:77],v[60:61],v[54:55] op_sel:[0,0,1] op_sel_hi:[1,0,1]\n"
"v_pk_fma_f32 v[78:79],v[78:79],v[60:61],v[54:55] op_sel:[0,0,1] op_sel_hi:[1,0,1]\n"
CV "v112,v72,v73\n"
CV "v113,v74,v75\n"
CV "v114,v76,v77\n"
CV "v115,v78,v79\n"
"s_waitcnt lgkmcnt(14)\n"
MF "a[80:83],v[152:155],v[92:95],a[80:83]\n"
AW "a52,v112\n"
AW "a53,v113\n"
AW "a54,v114\n"
AW "a55,v115\n"
AR "v80,a56\n"
AR "v81,a57\n"
AR "v82,a58\n"
"s_waitcnt lgkmcnt(12)\n"
MF "a[100:103],v[156:159],v[92:95],a[100:103]\n"
AR "v83,a59\n"
AR "v84,a76\n"
AR "v85,a77\n"
AR "v86,a78\n"
AR "v87,a79\n"
"v_pk_fma_f32 v[80:81],v[80:81],v[60:61],v[56:57] op_sel:[0,1,0] op_sel_hi:[1,1,0]\n"
"v_pk_fma_f32 v[82:83],v[82:83],v[60:61],v[56:57] op_sel:[0,1,0] op_sel_hi:[1,1,0]\n"
"v_pk_fma_f32 v[84:85],v[84:85],v[60:61],v[56:57] op_sel:[0,1,0] op_sel_hi:[1,1,0]\n"
"v_pk_fma_f32 v[86:87],v[86:87],v[60:61],v[56:57] op_sel:[0,1,0] op_sel_hi:[1,1,0]\n"
CV "v116,v80,v81\n"
CV "v117,v82,v83\n"
CV "v118,v84,v85\n"
CV "v119,v86,v87\n"
MF "a[84:87],v[152:155],v[96:99],a[84:87]\n"
AW "a56,v116\n"
AW "a57,v117\n"
AW "a58,v118\n"
AW "a59,v119\n"
AR "v72,a80\n"
AR "v73,a81\n"
AR "v74,a82\n"
MF "a[104:107],v[156:159],v[96:99],a[104:107]\n"
AR "v75,a83\n"
AR "v76,a100\n"
AR "v77,a101\n"
AR "v78,a102\n"
AR "v79,a103\n"
"v_pk_fma_f32 v[72:73],v[72:73],v[56:57],v[52:53] op_sel:[0,1,0] op_sel_hi:[1,1,0]\n"
"v_pk_fma_f32 v[74:75],v[74:75],v[56:57],v[52:53] op_sel:[0,1,0] op_sel_hi:[1,1,0]\n"
"v_pk_fma_f32 v[76:77],v[76:77],v[56:57],v[52:53] op_sel:[0,1,0] op_sel_hi:[1,1,0]\n"
"v_pk_fma_f32 v[78:79],v[78:79],v[56:57],v[52:53] op_sel:[0,1,0] op_sel_hi:[1,1,0]\n"
CV "v112,v72,v73\n"
CV "v113,v74,v75\n"
CV "v114,v76,v77\n"
CV "v115,v78,v79\n"
MF "a[88:91],v[152:155],v[100:103],a[88:91]\n"
AW "a80,v112\n"
AW "a81,v113\n"
AW "a82,v114\n"
AW "a83,v115\n"
AR "v80,a84\n"
AR "v81,a85\n"
AR "v82,a86\n"
MF "a[108:111],v[156:159],v[100:103],a[108:111]\n"
AR "v83,a87\n"
AR "v84,a104\n"
AR "v85,a105\n"
AR "v86,a106\n"
AR "v87,a107\n"
"v_pk_fma_f32 v[80:81],v[80:81],v[58:59],v[52:53] op_sel:[0,0,1] op_sel_hi:[1,0,1]\n"
"v_pk_fma_f32 v[82:83],v[82:83],v[58:59],v[52:53] op_sel:[0,0,1] op_sel_hi:[1,0,1]\n"
"v_pk_fma_f32 v[84:85],v[84:85],v[58:59],v[52:53] op_sel:[0,0,1] op_sel_hi:[1,0,1]\n"
"v_pk_fma_f32 v[86:87],v[86:87],v[58:59],v[52:53] op_sel:[0,0,1] op_sel_hi:[1,0,1]\n"
CV "v116,v80,v81\n"
CV "v117,v82,v83\n"
CV "v118,v84,v85\n"
CV "v119,v86,v87\n"
MF "a[92:95],v[152:155],v[104:107],a[92:95]\n"
AW "a84,v116\n"
AW "a85,v117\n"
AW "a86,v118\n"
AW "a87,v119\n"
AR "v72,a88\n"
AR "v73,a89\n"
AR "v74,a90\n"
MF "a[112:115],v[156:159],v[104:107],a[112:115]\n"
AR "v75,a91\n"
AR "v76,a108\n"
AR "v77,a109\n"
AR "v78,a110\n"
AR "v79,a111\n"
"v_pk_fma_f32 v[72:73],v[72:73],v[58:59],v[54:55] op_sel:[0,1,0] op_sel_hi:[1,1,0]\n"
"v_pk_fma_f32 v[74:75],v[74:75],v[58:59],v[54:55] op_sel:[0,1,0] op_sel_hi:[1,1,0]\n"
"v_pk_fma_f32 v[76:77],v[76:77],v[58:59],v[54:55] op_sel:[0,1,0] op_sel_hi:[1,1,0]\n"
"v_pk_fma_f32 v[78:79],v[78:79],v[58:59],v[54:55] op_sel:[0,1,0] op_sel_hi:[1,1,0]\n"
CV "v112,v72,v73\n"
CV "v113,v74,v75\n"
CV "v114,v76,v77\n"
CV "v115,v78,v79\n"
MF "a[96:99],v[152:155],v[108:111],a[96:99]\n"
AW "a88,v112\n"
AW "a89,v113\n"
AW "a90,v114\n"
AW "a91,v115\n"
AR "v80,a92\n"
AR "v81,a93\n"
AR "v82,a94\n"
MF "a[116:119],v[156:159],v[108:111],a[116:119]\n"
AR "v83,a95\n"
AR "v84,a112\n"
AR "v85,a113\n"
AR "v86,a114\n"
AR "v87,a115\n"
"v_pk_fma_f32 v[80:81],v[80:81],v[60:61],v[54:55] op_sel:[0,0,1] op_sel_hi:[1,0,1]\n"
"v_pk_fma_f32 v[82:83],v[82:83],v[60:61],v[54:55] op_sel:[0,0,1] op_sel_hi:[1,0,1]\n"
"v_pk_fma_f32 v[84:85],v[84:85],v[60:61],v[54:55] op_sel:[0,0,1] op_sel_hi:[1,0,1]\n"
"v_pk_fma_f32 v[86:87],v[86:87],v[60:61],v[54:55] op_sel:[0,0,1] op_sel_hi:[1,0,1]\n"
CV "v116,v80,v81\n"
CV "v117,v82,v83\n"
CV "v118,v84,v85\n"
CV "v119,v86,v87\n"
"s_waitcnt lgkmcnt(10)\n"
MF "a[120:123],v[160:163],v[92:95],a[120:123]\n"
AW "a92,v116\n"
AW "a93,v117\n"
AW "a94,v118\n"
AW "a95,v119\n"
AR "v72,a96\n"
AR "v73,a97\n"
AR "v74,a98\n"
"s_waitcnt lgkmcnt(8)\n"
MF "a[140:143],v[164:167],v[92:95],a[140:143]\n"
AR "v75,a99\n"
AR "v76,a116\n"
AR "v77,a117\n"
AR "v78,a118\n"
AR "v79,a119\n"
"v_pk_fma_f32 v[72:73],v[72:73],v[60:61],v[56:57] op_sel:[0,1,0] op_sel_hi:[1,1,0]\n"
"v_pk_fma_f32 v[74:75],v[74:75],v[60:61],v[56:57] op_sel:[0,1,0] op_sel_hi:[1,1,0]\n"
"v_pk_fma_f32 v[76:77],v[76:77],v[60:61],v[56:57] op_sel:[0,1,0] op_sel_hi:[1,1,0]\n"
"v_pk_fma_f32 v[78:79],v[78:79],v[60:61],v[56:57] op_sel:[0,1,0] op_sel_hi:[1,1,0]\n"
CV "v112,v72,v73\n"
CV "v113,v74,v75\n"
CV "v114,v76,v77\n"
CV "v115,v78,v79\n"
MF "a[124:127],v[160:163],v[96:99],a[124:127]\n"
AW "a96,v112\n"
AW "a97,v113\n"
AW "a98,v114\n"
AW "a99,v115\n"
AR "v0,a240\n"
AR "v2,a241\n"
".Lpg13f:\n"
"s_cbranch_execz .Lpg19f\n"
"s_nop 0\n"
AR "v4,a242\n"
"v_xor_b32 v1,64,v0\n"
"v_xor_b32 v3,64,v2\n"
"v_xor_b32_e64 v5,64,v4\n"
AR "v80,a120\n"
AR "v81,a121\n"
AR "v82,a122\n"
MF "a[144:147],v[164:167],v[96:99],a[144:147]\n"
AR "v83,a123\n"
AR "v84,a140\n"
AR "v85,a141\n"
AR "v86,a142\n"
AR "v87,a143\n"
"v_pk_fma_f32 v[80:81],v[80:81],v[56:57],v[52:53] op_sel:[0,1,0] op_sel_hi:[1,1,0]\n"
"v_pk_fma_f32 v[82:83],v[82:83],v[56:57],v[52:53] op_sel:[0,1,0] op_sel_hi:[1,1,0]\n"
"v_pk_fma_f32 v[84:85],v[84:85],v[56:57],v[52:53] op_sel:[0,1,0] op_sel_hi:[1,1,0]\n"
"v_pk_fma_f32 v[86:87],v[86:87],v[56:57],v[52:53] op_sel:[0,1,0] op_sel_hi:[1,1,0]\n"
CV "v116,v80,v81\n"
CV "v117,v82,v83\n"
CV "v118,v84,v85\n"
CV "v119,v86,v87\n"
MF "a[128:131],v[160:163],v[100:103],a[128:131]\n"
AW "a120,v116\n"
AW "a121,v117\n"
AW "a122,v118\n"
AW "a123,v119\n"
AR "v72,a124\n"
AR "v73,a125\n"
AR "v74,a126\n"
MF "a[148:151],v[164:167],v[100:103],a[148:151]\n"
AR "v75,a127\n"
AR "v76,a144\n"
AR "v77,a145\n"
AR "v78,a146\n"
AR "v79,a147\n"
"v_pk_fma_f32 v[72:73],v[72:73],v[58:59],v[52:53] op_sel:[0,0,1] op_sel_hi:[1,0,1]\n"
"v_pk_fma_f32 v[74:75],v[74:75],v[58:59],v[52:53] op_sel:[0,0,1] op_sel_hi:[1,0,1]\n"
"v_pk_fma_f32 v[76:77],v[76:77],v[58:59],v[52:53] op_sel:[0,0,1] op_sel_hi:[1,0,1]\n"
"v_pk_fma_f32 v[78:79],v[78:79],v[58:59],v[52:53] op_sel:[0,0,1] op_sel_hi:[1,0,1]\n"
CV "v112,v72,v73\n"
CV "v113,v74,v75\n"
CV "v114,v76,v77\n"
CV "v115,v78,v79\n"
MF "a[132:135],v[160:163],v[104:107],a[132:135]\n"
"ds_read_b128 a[60:63],v2\n"
AW "a124,v112\n"
AW "a125,v113\n"
AW "a126,v114\n"
AW "a127,v115\n"
AR "v80,a128\n"
AR "v81,a129\n"
AR "v82,a130\n"
MF "a[152:155],v[164:167],v[104:107],a[152:155]\n"
AR "v83,a131\n"
AR "v84,a148\n"
AR "v85,a149\n"
AR "v86,a150\n"
AR "v87,a151\n"
"v_pk_fma_f32 v[80:81],v[80:81],v[58:59],v[54:55] op_sel:[0,1,0] op_sel_hi:[1,1,0]\n"
"v_pk_fma_f32 v[82:83],v[82:83],v[58:59],v[54:55] op_sel:[0,1,0] op_sel_hi:[1,1,0]\n"
"v_pk_fma_f32 v[84:85],v[84:85],v[58:59],v[54:55] op_sel:[0,1,0] op_sel_hi:[1,1,0]\n"
"v_pk_fma_f32 v[86:87],v[86:87],v[58:59],v[54:55] op_sel:[0,1,0] op_sel_hi:[1,1,0]\n"
CV "v116,v80,v81\n"
CV "v117,v82,v83\n"
CV "v118,v84,v85\n"
CV "v119,v86,v87\n"
MF "a[136:139],v[160:163],v[108:111],a[136:139]\n"
"ds_read_b128 a[100:103],v3\n"
AW "a128,v116\n"
AW "a129,v117\n"
AW "a130,v118\n"
AW "a131,v119\n"
AR "v72,a132\n"
AR "v73,a133\n"
AR "v74,a134\n"
MF "a[156:159],v[164:167],v[108:111],a[156:159]\n"
AR "v75,a135\n"
AR "v76,a152\n"
AR "v77,a153\n"
AR "v78,a154\n"
AR "v79,a155\n"
"v_pk_fma_f32 v[72:73],v[72:73],v[60:61],v[54:55] op_sel:[0,0,1] op_sel_hi:[1,0,1]\n"
"v_pk_fma_f32 v[74:75],v[74:75],v[60:61],v[54:55] op_sel:[0,0,1] op_sel_hi:[1,0,1]\n"
"v_pk_fma_f32 v[76:77],v[76:77],v[60:61],v[54:55] op_sel:[0,0,1] op_sel_hi:[1,0,1]\n"
"v_pk_fma_f32 v[78:79],v[78:79],v[60:61],v[54:55] op_sel:[0,0,1] op_sel_hi:[1,0,1]\n"
CV "v112,v72,v73\n"
CV "v113,v74,v75\n"
CV "v114,v76,v77\n"
CV "v115,v78,v79\n"
"s_waitcnt lgkmcnt(8)\n"
MF "a[160:163],v[168:171],v[92:95],a[160:163]\n"
"ds_read_b128 a[64:67],v2 offset:2048\n"
AW "a132,v112\n"
AW "a133,v113\n"
AW "a134,v114\n"
AW "a135,v115\n"
AR "v80,a136\n"
AR "v81,a137\n"
AR "v82,a138\n"
"s_waitcnt lgkmcnt(7)\n"
MF "a[180:183],v[172:175],v[92:95],a[180:183]\n"
AR "v83,a139\n"
AR "v84,a156\n"
AR "v85,a157\n"
AR "v86,a158\n"
AR "v87,a159\n"
"v_pk_fma_f32 v[80:81],v[80:81],v[60:61],v[56:57] op_sel:[0,1,0] op_sel_hi:[1,1,0]\n"
"v_pk_fma_f32 v[82:83],v[82:83],v[60:61],v[56:57] op_sel:[0,1,0] op_sel_hi:[1,1,0]\n"
"v_pk_fma_f32 v[84:85],v[84:85],v[60:61],v[56:57] op_sel:[0,1,0] op_sel_hi:[1,1,0]\n"
"v_pk_fma_f32 v[86:87],v[86:87],v[60:61],v[56:57] op_sel:[0,1,0] op_sel_hi:[1,1,0]\n"
CV "v116,v80,v81\n"
CV "v117,v82,v83\n"
CV "v118,v84,v85\n"
CV "v119,v86,v87\n"
MF "a[164:167],v[168:171],v[96:99],a[164:167]\n"
"ds_read_b128 a[104:107],v3 offset:2048\n"
AW "a136,v116\n"
AW "a137,v117\n"
AW "a138,v118\n"
AW "a139,v119\n"
AR "v72,a160\n"
AR "v73,a161\n"
AR "v74,a162\n"
MF "a[184:187],v[172:175],v[96:99],a[184:187]\n"
AR "v75,a163\n"
AR "v76,a180\n"
AR "v77,a181\n"
AR "v78,a182\n"
AR "v79,a183\n"
"v_pk_fma_f32 v[72:73],v[72:73],v[56:57],v[52:53] op_sel:[0,1,0] op_sel_hi:[1,1,0]\n"
"v_pk_fma_f32 v[74:75],v[74:75],v[56:57],v[52:53] op_sel:[0,1,0] op_sel_hi:[1,1,0]\n"
"v_pk_fma_f32 v[76:77],v[76:77],v[56:57],v[52:53] op_sel:[0,1,0] op_sel_hi:[1,1,0]\n"
"v_pk_fma_f32 v[78:79],v[78:79],v[56:57],v[52:53] op_sel:[0,1,0] op_sel_hi:[1,1,0]\n"
CV "v112,v72,v73\n"
CV "v113,v74,v75\n"
CV "v114,v76,v77\n"
CV "v115,v78,v79\n"
MF "a[168:171],v[168:171],v[100:103],a[168:171]\n"
"ds_read_b128 a[68:71],v2 offset:4096\n"
AW "a160,v112\n"
AW "a161,v113\n"
AW "a162,v114\n"
AW "a163,v115\n"
AR "v80,a164\n"
AR "v81,a165\n"
AR "v82,a166\n"
MF "a[188:191],v[172:175],v[100:103],a[188:191]\n"
AR "v83,a167\n"
AR "v84,a184\n"
AR "v85,a185\n"
AR "v86,a186\n"
AR "v87,a187\n"
"v_pk_fma_f32 v[80:81],v[80:81],v[58:59],v[52:53] op_sel:[0,0,1] op_sel_hi:[1,0,1]\n"
"v_pk_fma_f32 v[82:83],v[82:83],v[58:59],v[52:53] op_sel:[0,0,1] op_sel_hi:[1,0,1]\n"
"v_pk_fma_f32 v[84:85],v[84:85],v[58:59],v[52:53] op_sel:[0,0,1] op_sel_hi:[1,0,1]\n"
"v_pk_fma_f32 v[86:87],v[86:87],v[58:59],v[52:53] op_sel:[0,0,1] op_sel_hi:[1,0,1]\n"
CV "v116,v80,v81\n"
CV "v117,v82,v83\n"
CV "v118,v84,v85\n"
CV "v119,v86,v87\n"
MF "a[172:175],v[168:171],v[104:107],a[172:175]\n"
"ds_read_b128 a[108:111],v3 offset:4096\n"
AW "a164,v116\n"
AW "a165,v117\n"
AW "a166,v118\n"
AW "a167,v119\n"
AR "v72,a168\n"
AR "v73,a169\n"
AR "v74,a170\n"
MF "a[192:195],v[172:175],v[104:107],a[192:195]\n"
AR "v75,a171\n"
AR "v76,a188\n"
AR "v77,a189\n"
AR "v78,a190\n"
AR "v79,a191\n"
"v_pk_fma_f32 v[72:73],v[72:73],v[58:59],v[54:55] op_sel:[0,1,0] op_sel_hi:[1,1,0]\n"
"v_pk_fma_f32 v[74:75],v[74:75],v[58:59],v[54:55] op_sel:[0,1,0] op_sel_hi:[1,1,0]\n"
"v_pk_fma_f32 v[76:77],v[76:77],v[58:59],v[54:55] op_sel:[0,1,0] op_sel_hi:[1,1,0]\n"
"v_pk_fma_f32 v[78:79],v[78:79],v[58:59],v[54:55] op_sel:[0,1,0] op_sel_hi:[1,1,0]\n"
CV "v112,v72,v73\n"
CV "v113,v74,v75\n"
CV "v114,v76,v77\n"
CV "v115,v78,v79\n"
MF "a[176:179],v[168:171],v[108:111],a[176:179]\n"
"ds_read_b128 a[72:75],v2 offset:6144\n"
AW "a168,v112\n"
AW "a169,v113\n"
AW "a170,v114\n"
AW "a171,v115\n"
AR "v80,a172\n"
AR "v81,a173\n"
AR "v82,a174\n"
MF "a[196:199],v[172:175],v[108:111],a[196:199]\n"
AR "v83,a175\n"
AR "v84,a192\n"
AR "v85,a193\n"
AR "v86,a194\n"
AR "v87,a195\n"
"v_pk_fma_f32 v[80:81],v[80:81],v[60:61],v[54:55] op_sel:[0,0,1] op_sel_hi:[1,0,1]\n"
"v_pk_fma_f32 v[82:83],v[82:83],v[60:61],v[54:55] op_sel:[0,0,1] op_sel_hi:[1,0,1]\n"
"v_pk_fma_f32 v[84:85],v[84:85],v[60:61],v[54:55] op_sel:[0,0,1] op_sel_hi:[1,0,1]\n"
"v_pk_fma_f32 v[86:87],v[86:87],v[60:61],v[54:55] op_sel:[0,0,1] op_sel_hi:[1,0,1]\n"
CV "v116,v80,v81\n"
CV "v117,v82,v83\n"
CV "v118,v84,v85\n"
CV "v119,v86,v87\n"
"s_waitcnt lgkmcnt(9)\n"
MF "a[200:203],v[176:179],v[92:95],a[200:203]\n"
"ds_read_b128 a[112:115],v3 offset:6144\n"
AW "a172,v116\n"
AW "a173,v117\n"
AW "a174,v118\n"
AW "a175,v119\n"
AR "v72,a176\n"
AR "v73,a177\n"
AR "v74,a178\n"
"s_waitcnt lgkmcnt(8)\n"
MF "a[220:223],v[180:183],v[92:95],a[220:223]\n"
AR "v75,a179\n"
AR "v76,a196\n"
AR "v77,a197\n"
AR "v78,a198\n"
AR "v79,a199\n"
"v_pk_fma_f32 v[72:73],v[72:73],v[60:61],v[56:57] op_sel:[0,1,0] op_sel_hi:[1,1,0]\n"
"v_pk_fma_f32 v[74:75],v[74:75],v[60:61],v[56:57] op_sel:[0,1,0] op_sel_hi:[1,1,0]\n"
"v_pk_fma_f32 v[76:77],v[76:77],v[60:61],v[56:57] op_sel:[0,1,0] op_sel_hi:[1,1,0]\n"
"v_pk_fma_f32 v[78:79],v[78:79],v[60:61],v[56:57] op_sel:[0,1,0] op_sel_hi:[1,1,0]\n"
CV "v112,v72,v73\n"
CV "v113,v74,v75\n"
CV "v114,v76,v77\n"
CV "v115,v78,v79\n"
MF "a[204:207],v[176:179],v[96:99],a[204:207]\n"
"ds_read_b128 a[76:79],v2 offset:8192\n"
AW "a176,v112\n"
AW "a177,v113\n"
AW "a178,v114\n"
AW "a179,v115\n"
AR "v80,a200\n"
AR "v81,a201\n"
AR "v82,a202\n"
MF "a[224:227],v[180:183],v[96:99],a[224:227]\n"
AR "v83,a203\n"
AR "v84,a220\n"
AR "v85,a221\n"
AR "v86,a222\n"
AR "v87,a223\n"
"v_pk_fma_f32 v[80:81],v[80:81],v[56:57],v[52:53] op_sel:[0,1,0] op_sel_hi:[1,1,0]\n"
"v_pk_fma_f32 v[82:83],v[82:83],v[56:57],v[52:53] op_sel:[0,1,0] op_sel_hi:[1,1,0]\n"
"v_pk_fma_f32 v[84:85],v[84:85],v[56:57],v[52:53] op_sel:[0,1,0] op_sel_hi:[1,1,0]\n"
"v_pk_fma_f32 v[86:87],v[86:87],v[56:57],v[52:53] op_sel:[0,1,0] op_sel_hi:[1,1,0]\n"
CV "v116,v80,v81\n"
CV "v117,v82,v83\n"
CV "v118,v84,v85\n"
CV "v119,v86,v87\n"
MF "a[208:211],v[176:179],v[100:103],a[208:211]\n"
"ds_read_b128 a[116:119],v3 offset:8192\n"
AW "a20,v116\n"
AW "a21,v117\n"
AW "a22,v118\n"
AW "a23,v119\n"
AR "v72,a204\n"
AR "v73,a205\n"
AR "v74,a206\n"
MF "a[228:231],v[180:183],v[100:103],a[228:231]\n"
AR "v75,a207\n"
AR "v76,a224\n"
AR "v77,a225\n"
AR "v78,a226\n"
AR "v79,a227\n"
"v_pk_fma_f32 v[72:73],v[72:73],v[58:59],v[52:53] op_sel:[0,0,1] op_sel_hi:[1,0,1]\n"
"v_pk_fma_f32 v[74:75],v[74:75],v[58:59],v[52:53] op_sel:[0,0,1] op_sel_hi:[1,0,1]\n"
"v_pk_fma_f32 v[76:77],v[76:77],v[58:59],v[52:53] op_sel:[0,0,1] op_sel_hi:[1,0,1]\n"
"v_pk_fma_f32 v[78:79],v[78:79],v[58:59],v[52:53] op_sel:[0,0,1] op_sel_hi:[1,0,1]\n"
CV "v112,v72,v73\n"
CV "v113,v74,v75\n"
CV "v114,v76,v77\n"
CV "v115,v78,v79\n"
MF "a[212:215],v[176:179],v[104:107],a[212:215]\n"
AW "a24,v112\n"
AW "a25,v113\n"
AW "a26,v114\n"
AW "a27,v115\n"
AR "v80,a208\n"
AR "v81,a209\n"
AR "v82,a210\n"
MF "a[232:235],v[180:183],v[104:107],a[232:235]\n"
AR "v83,a211\n"
AR "v84,a228\n"
AR "v85,a229\n"
AR "v86,a230\n"
AR "v87,a231\n"
"v_pk_fma_f32 v[80:81],v[80:81],v[58:59],v[54:55] op_sel:[0,1,0] op_sel_hi:[1,1,0]\n"
"v_pk_fma_f32 v[82:83],v[82:83],v[58:59],v[54:55] op_sel:[0,1,0] op_sel_hi:[1,1,0]\n"
"v_pk_fma_f32 v[84:85],v[84:85],v[58:59],v[54:55] op_sel:[0,1,0] op_sel_hi:[1,1,0]\n"
"v_pk_fma_f32 v[86:87],v[86:87],v[58:59],v[54:55] op_sel:[0,1,0] op_sel_hi:[1,1,0]\n"
CV "v116,v80,v81\n"
CV "v117,v82,v83\n"
CV "v118,v84,v85\n"
CV "v119,v86,v87\n"
MF "a[216:219],v[176:179],v[108:111],a[216:219]\n"
"s_waitcnt vmcnt(14) lgkmcnt(0)\n"
"s_barrier\n"
TR "a[200:201],v0\n"
TR "a[202:203],v0 offset:512\n"
TR "a[204:205],v0 offset:4096\n"
TR "a[206:207],v0 offset:4608\n"
AW "a28,v116\n"
AW "a29,v117\n"
AW "a30,v118\n"
AW "a31,v119\n"
AR "v72,a212\n"
AR "v73,a213\n"
AR "v74,a214\n"
MF "a[236:239],v[180:183],v[108:111],a[236:239]\n"
AR "v75,a215\n"
AR "v76,a232\n"
AR "v77,a233\n"
AR "v78,a234\n"
AR "v79,a235\n"
"v_pk_fma_f32 v[72:73],v[72:73],v[60:61],v[54:55] op_sel:[0,0,1] op_sel_hi:[1,0,1]\n"
"v_pk_fma_f32 v[74:75],v[74:75],v[60:61],v[54:55] op_sel:[0,0,1] op_sel_hi:[1,0,1]\n"
"v_pk_fma_f32 v[76:77],v[76:77],v[60:61],v[54:55] op_sel:[0,0,1] op_sel_hi:[1,0,1]\n"
"v_pk_fma_f32 v[78:79],v[78:79],v[60:61],v[54:55] op_sel:[0,0,1] op_sel_hi:[1,0,1]\n"
CV "v112,v72,v73\n"
CV "v113,v74,v75\n"
CV "v114,v76,v77\n"
CV "v115,v78,v79\n"
TR "a[208:209],v0 offset:8\n"
TR "a[210:211],v0 offset:520\n"
TR "a[212:213],v0 offset:4104\n"
TR "a[214:215],v0 offset:4616\n"
AW "a32,v112\n"
AW "a33,v113\n"
AW "a34,v114\n"
AW "a35,v115\n"
AR "v80,a216\n"
AR "v81,a217\n"
AR "v82,a218\n"
AR "v83,a219\n"
AR "v84,a236\n"
AR "v85,a237\n"
AR "v86,a238\n"
AR "v87,a239\n"
"v_pk_fma_f32 v[80:81],v[80:81],v[60:61],v[56:57] op_sel:[0,1,0] op_sel_hi:[1,1,0]\n"
"v_pk_fma_f32 v[82:83],v[82:83],v[60:61],v[56:57] op_sel:[0,1,0] op_sel_hi:[1,1,0]\n"
"v_pk_fma_f32 v[84:85],v[84:85],v[60:61],v[56:57] op_sel:[0,1,0] op_sel_hi:[1,1,0]\n"
"v_pk_fma_f32 v[86:87],v[86:87],v[60:61],v[56:57] op_sel:[0,1,0] op_sel_hi:[1,1,0]\n"
CV "v116,v80,v81\n"
CV "v117,v82,v83\n"
CV "v118,v84,v85\n"
CV "v119,v86,v87\n"
AW "a36,v116\n"
AW "a37,v117\n"
AW "a38,v118\n"
AW "a39,v119\n"
"s_mov_b32 s13,s11\n"
"s_mov_b32 s70,s71\n"
"s_mov_b32 s57,1\n"
"s_waitcnt lgkmcnt(0)\n"
"s_branch .Lq0\n"
".Lpgqy_c:\n"
"s_cbranch_execz .Lpgqj_a\n"
".Lqy:\n"
MF "v[216:219],a[216:219],a[60:63],v[216:219]\n"
TR "a[208:209],v0 offset:8\n"
MF "v[216:219],a[220:223],a[100:103],v[216:219]\n"
TR "a[210:211],v0 offset:520\n"
MF "v[224:227],a[220:223],a[104:107],v[224:227]\n"
MF "v[224:227],a[216:219],a[64:67],v[224:227]\n"
TR "a[212:213],v0 offset:4104\n"
MF "v[232:235],a[216:219],a[68:71],v[232:235]\n"
TR "a[214:215],v0 offset:4616\n"
MF "v[232:235],a[220:223],a[108:111],v[232:235]\n"
MF "v[240:243],a[220:223],a[112:115],v[240:243]\n"
MF "v[240:243],a[216:219],a[72:75],v[240:243]\n"
MF "v[248:251],a[216:219],a[76:79],v[248:251]\n"
MF "v[248:251],a[220:223],a[116:119],v[248:251]\n"
MF "v[248:251],a[200:203],a[156:159],v[248:251]\n"
TR "a[216:217],v0 offset:16392\n"
MF "v[248:251],a[204:207],a[196:199],v[248:251]\n"
TR "a[218:219],v0 offset:16904\n"
MF "v[240:243],a[204:207],a[192:195],v[240:243]\n"
MF "v[240:243],a[200:203],a[152:155],v[240:243]\n"
TR "a[220:221],v0 offset:20488\n"
MF "v[232:235],a[200:203],a[148:151],v[232:235]\n"
TR "a[222:223],v0 offset:21000\n"
MF "v[232:235],a[204:207],a[188:191],v[232:235]\n"
MF "v[224:227],a[204:207],a[184:187],v[224:227]\n"
MF "v[224:227],a[200:203],a[144:147],v[224:227]\n"
"v_fma_f32 v248,v248,s54,v14\n"
"v_fma_f32 v249,v249,s54,v14\n"
MF "v[216:219],a[200:203],a[140:143],v[216:219]\n"
"v_fma_f32 v250,v250,s54,v14\n"
"v_fma_f32 v251,v251,s54,v14\n"
MF "v[216:219],a[204:207],a[180:183],v[216:219]\n"
"s_waitcnt lgkmcnt(4)\n"
"v_fma_f32 v252,v252,s54,v14\n"
"v_fma_f32 v253,v253,s54,v14\n"
MF "v[180:183],a[208:211],a[60:63],v[180:183]\n"
TR "a[200:201],v0\n"
"v_fma_f32 v254,v254,s54,v14\n"
MF "v[180:183],a[212:215],a[100:103],v[180:183]\n"
TR "a[202:203],v0 offset:512\n"
"v_fma_f32 v255,v255,s54,v14\n"
MF "v[188:191],a[212:215],a[104:107],v[188:191]\n"
CV "v255,v254,v255\n"
CV "v254,v252,v253\n"
MF "v[188:191],a[208:211],a[64:67],v[188:191]\n"
TR "a[204:205],v0 offset:4096\n"
CV "v253,v250,v251\n"
MF "v[196:199],a[208:211],a[68:71],v[196:199]\n"
TR "a[206:207],v0 offset:4608\n"
CV "v252,v248,v249\n"
MF "v[196:199],a[212:215],a[108:111],v[196:199]\n"
"s_add_u32 s58,s75,4096\n"
"v_fma_f32 v240,v240,s54,v14\n"
"v_fma_f32 v241,v241,s54,v14\n"
MF "v[204:207],a[212:215],a[112:115],v[204:207]\n"
"buffer_store_dwordx4 v[252:255],v9,s[40:43],s58 offen offset:1024 sc1\n"
MF "v[204:207],a[208:211],a[72:75],v[204:207]\n"
"v_fma_f32 v242,v242,s54,v14\n"
"v_fma_f32 v243,v243,s54,v14\n"
MF "v[212:215],a[208:211],a[76:79],v[212:215]\n"
"v_fma_f32 v244,v244,s54,v14\n"
"v_fma_f32 v245,v245,s54,v14\n"
MF "v[212:215],a[212:215],a[116:119],v[212:215]\n"
"s_waitcnt lgkmcnt(4)\n"
"v_fma_f32 v246,v246,s54,v14\n"
"v_fma_f32 v247,v247,s54,v14\n"
MF "v[180:183],a[216:219],a[140:143],v[180:183]\n"
TR "a[208:209],v0 offset:16384\n"
CV "v251,v246,v247\n"
MF "v[180:183],a[220:223],a[180:183],v[180:183]\n"
TR "a[210:211],v0 offset:16896\n"
CV "v250,v244,v245\n"
MF "v[188:191],a[220:223],a[184:187],v[188:191]\n"
CV "v249,v242,v243\n"
CV "v248,v240,v241\n"
MF "v[188:191],a[216:219],a[144:147],v[188:191]\n"
TR "a[212:213],v0 offset:20480\n"
"v_fma_f32 v232,v232,s54,v14\n"
MF "v[196:199],a[216:219],a[148:151],v[196:199]\n"
TR "a[214:215],v0 offset:20992\n"
"v_fma_f32 v233,v233,s54,v14\n"
MF "v[196:199],a[220:223],a[188:191],v[196:199]\n"
"buffer_store_dwordx4 v[248:251],v9,s[40:43],s58 offen sc1\n"
MF "v[204:207],a[220:223],a[192:195],v[204:207]\n"
"v_fma_f32 v234,v234,s54,v14\n"
"v_fma_f32 v235,v235,s54,v14\n"
MF "v[204:207],a[216:219],a[152:155],v[204:207]\n"
"v_fma_f32 v236,v236,s54,v14\n"
"v_fma_f32 v237,v237,s54,v14\n"
MF "v[212:215],a[216:219],a[156:159],v[212:215]\n"
"s_waitcnt vmcnt(10) lgkmcnt(0)\n"
"v_fma_f32 v238,v238,s54,v14\n"
"v_fma_f32 v239,v239,s54,v14\n"
MF "v[212:215],a[220:223],a[196:199],v[212:215]\n"
"s_barrier\n"
MF "v[176:179],a[200:203],a[60:63],v[176:179]\n"
TR "a[216:217],v1 offset:32776\n"
CV "v247,v238,v239\n"
MF "v[176:179],a[204:207],a[100:103],v[176:179]\n"
TR "a[218:219],v1 offset:33288\n"
CV "v246,v236,v237\n"
MF "v[184:187],a[204:207],a[104:107],v[184:187]\n"
"s_add_u32 s66,s67,4718592\n"
"s_sub_u32 s81,s81,384\n"
"s_mov_b32 s83,7077888\n"
MF "v[184:187],a[200:203],a[64:67],v[184:187]\n"
"s_add_u32 m0,s62,0\n"
"s_add_u32 s82,s66,2359296\n"
TR "a[220:221],v1 offset:36872\n"
MF "v[192:195],a[200:203],a[68:71],v[192:195]\n"
"s_sub_u32 s15,s82,7864320\n"
"s_min_u32 s82,s82,s15\n"
TR "a[222:223],v1 offset:37384\n"
MF "v[192:195],a[204:207],a[108:111],v[192:195]\n"
LD "v6,s[28:31],s82 offen lds\n"
MF "v[200:203],a[204:207],a[112:115],v[200:203]\n"
CV "v245,v234,v235\n"
CV "v244,v232,v233\n"
MF "v[200:203],a[200:203],a[72:75],v[200:203]\n"
"v_fma_f32 v224,v224,s54,v14\n"
"v_fma_f32 v225,v225,s54,v14\n"
MF "v[208:211],a[200:203],a[76:79],v[208:211]\n"
"buffer_store_dwordx4 v[244:247],v9,s[40:43],s75 offen offset:3072 sc1\n"
MF "v[208:211],a[204:207],a[116:119],v[208:211]\n"
LD "v11,s[28:31],s82 offen offset:1024 lds\n"
MF "v[208:211],a[208:211],a[156:159],v[208:211]\n"
TR "a[200:201],v1 offset:49160\n"
"v_fma_f32 v226,v226,s54,v14\n"
MF "v[208:211],a[212:215],a[196:199],v[208:211]\n"
TR "a[202:203],v1 offset:49672\n"
"v_fma_f32 v227,v227,s54,v14\n"
MF "v[200:203],a[212:215],a[192:195],v[200:203]\n"
"v_fma_f32 v228,v228,s54,v14\n"
"v_fma_f32 v229,v229,s54,v14\n"
MF "v[200:203],a[208:211],a[152:155],v[200:203]\n"
TR "a[204:205],v1 offset:53256\n"
"v_fma_f32 v230,v230,s54,v14\n"
MF "v[192:195],a[208:211],a[148:151],v[192:195]\n"
TR "a[206:207],v1 offset:53768\n"
"v_fma_f32 v231,v231,s54,v14\n"
MF "v[192:195],a[212:215],a[188:191],v[192:195]\n"
CV "v243,v230,v231\n"
CV "v242,v228,v229\n"
MF "v[184:187],a[212:215],a[184:187],v[184:187]\n"
CV "v241,v226,v227\n"
CV "v240,v224,v225\n"
MF "v[184:187],a[208:211],a[144:147],v[184:187]\n"
"buffer_store_dwordx4 v[240:243],v9,s[40:43],s75 offen offset:2048 sc1\n"
MF "v[176:179],a[208:211],a[140:143],v[176:179]\n"
LD "v12,s[28:31],s82 offen offset:2048 lds\n"
MF "v[176:179],a[212:215],a[180:183],v[176:179]\n"
"s_waitcnt lgkmcnt(4)\n"
"v_fma_f32 v216,v216,s54,v14\n"
"v_fma_f32 v217,v217,s54,v14\n"
MF "v[140:143],a[216:219],a[60:63],v[140:143]\n"
TR "a[208:209],v1 offset:32768\n"
"v_fma_f32 v218,v218,s54,v14\n"
MF "v[140:143],a[220:223],a[100:103],v[140:143]\n"
TR "a[210:211],v1 offset:33280\n"
"v_fma_f32 v219,v219,s54,v14\n"
MF "v[148:151],a[220:223],a[104:107],v[148:151]\n"
".Lpg14c:\n"
"s_cbranch_execz .Lpg3a\n"
"v_fma_f32 v220,v220,s54,v14\n"
"v_fma_f32 v221,v221,s54,v14\n"
MF "v[148:151],a[216:219],a[64:67],v[148:151]\n"
TR "a[212:213],v1 offset:36864\n"
"v_fma_f32 v222,v222,s54,v14\n"
MF "v[156:159],a[216:219],a[68:71],v[156:159]\n"
TR "a[214:215],v1 offset:37376\n"
"v_fma_f32 v223,v223,s54,v14\n"
MF "v[156:159],a[220:223],a[108:111],v[156:159]\n"
CV "v239,v222,v223\n"
CV "v238,v220,v221\n"
MF "v[164:167],a[220:223],a[112:115],v[164:167]\n"
CV "v237,v218,v219\n"
CV "v236,v216,v217\n"
MF "v[164:167],a[216:219],a[72:75],v[164:167]\n"
LD "v13,s[28:31],s82 offen offset:3072 lds\n"
MF "v[172:175],a[216:219],a[76:79],v[172:175]\n"
"buffer_store_dwordx4 v[236:239],v9,s[40:43],s75 offen offset:1024 sc1\n"
MF "v[172:175],a[220:223],a[116:119],v[172:175]\n"
"s_waitcnt lgkmcnt(4)\n"
"v_fma_f32 v208,v208,s54,v14\n"
"v_fma_f32 v209,v209,s54,v14\n"
MF "v[140:143],a[200:203],a[140:143],v[140:143]\n"
TR "a[216:217],v1 offset:49152\n"
"v_fma_f32 v210,v210,s54,v14\n"
MF "v[140:143],a[204:207],a[180:183],v[140:143]\n"
TR "a[218:219],v1 offset:49664\n"
"v_fma_f32 v211,v211,s54,v14\n"
MF "v[148:151],a[204:207],a[184:187],v[148:151]\n"
"v_fma_f32 v212,v212,s54,v14\n"
"v_fma_f32 v213,v213,s54,v14\n"
MF "v[148:151],a[200:203],a[144:147],v[148:151]\n"
TR "a[220:221],v1 offset:53248\n"
"v_fma_f32 v214,v214,s54,v14\n"
MF "v[156:159],a[200:203],a[148:151],v[156:159]\n"
TR "a[222:223],v1 offset:53760\n"
"v_fma_f32 v215,v215,s54,v14\n"
MF "v[156:159],a[204:207],a[188:191],v[156:159]\n"
CV "v235,v214,v215\n"
CV "v234,v212,v213\n"
MF "v[164:167],a[204:207],a[192:195],v[164:167]\n"
CV "v233,v210,v211\n"
CV "v232,v208,v209\n"
MF "v[164:167],a[200:203],a[152:155],v[164:167]\n"
"v_fma_f32 v200,v200,s54,v14\n"
"v_fma_f32 v201,v201,s54,v14\n"
MF "v[172:175],a[200:203],a[156:159],v[172:175]\n"
"buffer_store_dwordx4 v[232:235],v9,s[40:43],s75 offen sc1\n"
MF "v[172:175],a[204:207],a[196:199],v[172:175]\n"
"s_waitcnt lgkmcnt(0)\n"
"v_fma_f32 v202,v202,s54,v14\n"
"v_fma_f32 v203,v203,s54,v14\n"
MF "v[136:139],a[208:211],a[60:63],v[136:139]\n"
"s_add_u32 m0,s62,16384\n"
"s_add_u32 s82,s66,2359424\n"
TR "a[200:201],v0 offset:32776\n"
MF "v[136:139],a[212:215],a[100:103],v[136:139]\n"
"s_sub_u32 s15,s82,7864320\n"
"s_min_u32 s82,s82,s15\n"
TR "a[202:203],v0 offset:33288\n"
MF "v[144:147],a[212:215],a[104:107],v[144:147]\n"
LD "v6,s[28:31],s82 offen lds\n"
MF "v[144:147],a[208:211],a[64:67],v[144:147]\n"
TR "a[204:205],v0 offset:36872\n"
"v_fma_f32 v204,v204,s54,v14\n"
MF "v[152:155],a[208:211],a[68:71],v[152:155]\n"
TR "a[206:207],v0 offset:37384\n"
"v_fma_f32 v205,v205,s54,v14\n"
MF "v[152:155],a[212:215],a[108:111],v[152:155]\n"
"v_fma_f32 v206,v206,s54,v14\n"
"v_fma_f32 v207,v207,s54,v14\n"
MF "v[160:163],a[212:215],a[112:115],v[160:163]\n"
CV "v231,v206,v207\n"
CV "v230,v204,v205\n"
MF "v[160:163],a[208:211],a[72:75],v[160:163]\n"
CV "v229,v202,v203\n"
CV "v228,v200,v201\n"
MF "v[168:171],a[208:211],a[76:79],v[168:171]\n"
AW "a84,v228\n"
AW "a85,v229\n"
MF "v[168:171],a[212:215],a[116:119],v[168:171]\n"
LD "v11,s[28:31],s82 offen offset:1024 lds\n"
MF "v[168:171],a[216:219],a[156:159],v[168:171]\n"
TR "a[208:209],v0 offset:49160\n"
AW "a86,v230\n"
MF "v[168:171],a[220:223],a[196:199],v[168:171]\n"
TR "a[210:211],v0 offset:49672\n"
AW "a87,v231\n"
MF "v[160:163],a[220:223],a[192:195],v[160:163]\n"
"v_fma_f32 v192,v192,s54,v14\n"
"v_fma_f32 v193,v193,s54,v14\n"
MF "v[160:163],a[216:219],a[152:155],v[160:163]\n"
TR "a[212:213],v0 offset:53256\n"
"v_fma_f32 v194,v194,s54,v14\n"
MF "v[152:155],a[216:219],a[148:151],v[152:155]\n"
TR "a[214:215],v0 offset:53768\n"
"v_fma_f32 v195,v195,s54,v14\n"
MF "v[152:155],a[220:223],a[188:191],v[152:155]\n"
"v_fma_f32 v196,v196,s54,v14\n"
"v_fma_f32 v197,v197,s54,v14\n"
MF "v[144:147],a[220:223],a[184:187],v[144:147]\n"
"v_fma_f32 v198,v198,s54,v14\n"
"v_fma_f32 v199,v199,s54,v14\n"
MF "v[144:147],a[216:219],a[144:147],v[144:147]\n"
CV "v227,v198,v199\n"
CV "v226,v196,v197\n"
MF "v[136:139],a[216:219],a[140:143],v[136:139]\n"
LD "v12,s[28:31],s82 offen offset:2048 lds\n"
MF "v[136:139],a[220:223],a[180:183],v[136:139]\n"
"s_waitcnt lgkmcnt(4)\n"
CV "v225,v194,v195\n"
CV "v224,v192,v193\n"
MF "v[100:103],a[200:203],a[60:63],v[100:103]\n"
TR "a[216:217],v0 offset:32768\n"
AW "a80,v224\n"
MF "v[100:103],a[204:207],a[100:103],v[100:103]\n"
TR "a[218:219],v0 offset:33280\n"
AW "a81,v225\n"
MF "v[108:111],a[204:207],a[104:107],v[108:111]\n"
AW "a82,v226\n"
AW "a83,v227\n"
MF "v[108:111],a[200:203],a[64:67],v[108:111]\n"
TR "a[220:221],v0 offset:36864\n"
"v_fma_f32 v184,v184,s54,v14\n"
MF "v[116:119],a[200:203],a[68:71],v[116:119]\n"
TR "a[222:223],v0 offset:37376\n"
"v_fma_f32 v185,v185,s54,v14\n"
MF "v[116:119],a[204:207],a[108:111],v[116:119]\n"
"v_fma_f32 v186,v186,s54,v14\n"
"v_fma_f32 v187,v187,s54,v14\n"
MF "v[124:127],a[204:207],a[112:115],v[124:127]\n"
"v_fma_f32 v188,v188,s54,v14\n"
"v_fma_f32 v189,v189,s54,v14\n"
MF "v[124:127],a[200:203],a[72:75],v[124:127]\n"
LD "v13,s[28:31],s82 offen offset:3072 lds\n"
MF "v[132:135],a[200:203],a[76:79],v[132:135]\n"
"v_fma_f32 v190,v190,s54,v14\n"
"v_fma_f32 v191,v191,s54,v14\n"
MF "v[132:135],a[204:207],a[116:119],v[132:135]\n"
"s_waitcnt lgkmcnt(4)\n"
CV "v223,v190,v191\n"
CV "v222,v188,v189\n"
MF "v[100:103],a[208:211],a[140:143],v[100:103]\n"
TR "a[200:201],v0 offset:49152\n"
CV "v221,v186,v187\n"
MF "v[100:103],a[212:215],a[180:183],v[100:103]\n"
TR "a[202:203],v0 offset:49664\n"
CV "v220,v184,v185\n"
MF "v[108:111],a[212:215],a[184:187],v[108:111]\n"
AW "a56,v220\n"
AW "a57,v221\n"
MF "v[108:111],a[208:211],a[144:147],v[108:111]\n"
TR "a[204:205],v0 offset:53248\n"
AW "a58,v222\n"
MF "v[116:119],a[208:211],a[148:151],v[116:119]\n"
TR "a[206:207],v0 offset:53760\n"
AW "a59,v223\n"
MF "v[116:119],a[212:215],a[188:191],v[116:119]\n"
"v_fma_f32 v176,v176,s54,v14\n"
"v_fma_f32 v177,v177,s54,v14\n"
MF "v[124:127],a[212:215],a[192:195],v[124:127]\n"
"v_add_u32 v0,0xc000,v0\n"
"v_fma_f32 v178,v178,s54,v14\n"
MF "v[124:127],a[208:211],a[152:155],v[124:127]\n"
"v_add_u32 v1,0xc000,v1\n"
"v_fma_f32 v179,v179,s54,v14\n"
MF "v[132:135],a[208:211],a[156:159],v[132:135]\n"
"s_waitcnt vmcnt(14) lgkmcnt(0)\n"
"v_fma_f32 v180,v180,s54,v14\n"
"v_fma_f32 v181,v181,s54,v14\n"
MF "v[132:135],a[212:215],a[196:199],v[132:135]\n"
"s_barrier\n"
MF "v[96:99],a[216:219],a[60:63],v[96:99]\n"
"s_add_u32 m0,s62,32768\n"
"s_add_u32 s82,s66,2359552\n"
TR "a[208:209],v1 offset:16392\n"
MF "v[96:99],a[220:223],a[100:103],v[96:99]\n"
"s_sub_u32 s15,s82,7864320\n"
"s_min_u32 s82,s82,s15\n"
TR "a[210:211],v1 offset:16904\n"
MF "v[104:107],a[220:223],a[104:107],v[104:107]\n"
LD "v6,s[28:31],s82 offen lds\n"
MF "v[104:107],a[216:219],a[64:67],v[104:107]\n"
TR "a[212:213],v1 offset:20488\n"
"v_fma_f32 v182,v182,s54,v14\n"
MF "v[112:115],a[216:219],a[68:71],v[112:115]\n"
TR "a[214:215],v1 offset:21000\n"
"v_fma_f32 v183,v183,s54,v14\n"
MF "v[112:115],a[220:223],a[108:111],v[112:115]\n"
CV "v219,v182,v183\n"
CV "v218,v180,v181\n"
MF "v[120:123],a[220:223],a[112:115],v[120:123]\n"
CV "v217,v178,v179\n"
CV "v216,v176,v177\n"
MF "v[120:123],a[216:219],a[72:75],v[120:123]\n"
AW "a52,v216\n"
AW "a53,v217\n"
MF "v[128:131],a[216:219],a[76:79],v[128:131]\n"
AW "a54,v218\n"
AW "a55,v219\n"
MF "v[128:131],a[220:223],a[116:119],v[128:131]\n"
LD "v11,s[28:31],s82 offen offset:1024 lds\n"
MF "v[128:131],a[200:203],a[156:159],v[128:131]\n"
TR "a[216:217],v1 offset:32776\n"
"v_fma_f32 v168,v168,s54,v14\n"
MF "v[128:131],a[204:207],a[196:199],v[128:131]\n"
TR "a[218:219],v1 offset:33288\n"
"v_fma_f32 v169,v169,s54,v14\n"
MF "v[120:123],a[204:207],a[192:195],v[120:123]\n"
"v_fma_f32 v170,v170,s54,v14\n"
"v_fma_f32 v171,v171,s54,v14\n"
MF "v[120:123],a[200:203],a[152:155],v[120:123]\n"
TR "a[220:221],v1 offset:36872\n"
"v_fma_f32 v172,v172,s54,v14\n"
MF "v[112:115],a[200:203],a[148:151],v[112:115]\n"
TR "a[222:223],v1 offset:37384\n"
"v_fma_f32 v173,v173,s54,v14\n"
MF "v[112:115],a[204:207],a[188:191],v[112:115]\n"
"v_fma_f32 v174,v174,s54,v14\n"
"v_fma_f32 v175,v175,s54,v14\n"
MF "v[104:107],a[204:207],a[184:187],v[104:107]\n"
CV "v215,v174,v175\n"
CV "v214,v172,v173\n"
MF "v[104:107],a[200:203],a[144:147],v[104:107]\n"
CV "v213,v170,v171\n"
CV "v212,v168,v169\n"
MF "v[96:99],a[200:203],a[140:143],v[96:99]\n"
LD "v12,s[28:31],s82 offen offset:2048 lds\n"
MF "v[96:99],a[204:207],a[180:183],v[96:99]\n"
"s_waitcnt lgkmcnt(4)\n"
AW "a48,v212\n"
AW "a49,v213\n"
MF "v[60:63],a[208:211],a[60:63],v[60:63]\n"
TR "a[200:201],v1 offset:16384\n"
AW "a50,v214\n"
MF "v[60:63],a[212:215],a[100:103],v[60:63]\n"
TR "a[202:203],v1 offset:16896\n"
AW "a51,v215\n"
MF "v[68:71],a[212:215],a[104:107],v[68:71]\n"
"v_fma_f32 v160,v160,s54,v14\n"
"v_fma_f32 v161,v161,s54,v14\n"
MF "v[68:71],a[208:211],a[64:67],v[68:71]\n"
TR "a[204:205],v1 offset:20480\n"
"v_fma_f32 v162,v162,s54,v14\n"
MF "v[76:79],a[208:211],a[68:71],v[76:79]\n"
TR "a[206:207],v1 offset:20992\n"
"v_fma_f32 v163,v163,s54,v14\n"
MF "v[76:79],a[212:215],a[108:111],v[76:79]\n"
"v_fma_f32 v164,v164,s54,v14\n"
"v_fma_f32 v165,v165,s54,v14\n"
MF "v[84:87],a[212:215],a[112:115],v[84:87]\n"
"v_fma_f32 v166,v166,s54,v14\n"
"v_fma_f32 v167,v167,s54,v14\n"
MF "v[84:87],a[208:211],a[72:75],v[84:87]\n"
LD "v13,s[28:31],s82 offen offset:3072 lds\n"
MF "v[92:95],a[208:211],a[76:79],v[92:95]\n"
CV "v211,v166,v167\n"
CV "v210,v164,v165\n"
MF "v[92:95],a[212:215],a[116:119],v[92:95]\n"
"s_waitcnt lgkmcnt(4)\n"
CV "v209,v162,v163\n"
CV "v208,v160,v161\n"
MF "v[60:63],a[216:219],a[140:143],v[60:63]\n"
TR "a[208:209],v1 offset:32768\n"
AW "a44,v208\n"
MF "v[60:63],a[220:223],a[180:183],v[60:63]\n"
TR "a[210:211],v1 offset:33280\n"
AW "a45,v209\n"
MF "v[68:71],a[220:223],a[184:187],v[68:71]\n"
AW "a46,v210\n"
AW "a47,v211\n"
MF "v[68:71],a[216:219],a[144:147],v[68:71]\n"
TR "a[212:213],v1 offset:36864\n"
"v_fma_f32 v152,v152,s54,v14\n"
MF "v[76:79],a[216:219],a[148:151],v[76:79]\n"
TR "a[214:215],v1 offset:37376\n"
"v_fma_f32 v153,v153,s54,v14\n"
MF "v[76:79],a[220:223],a[188:191],v[76:79]\n"
"v_fma_f32 v154,v154,s54,v14\n"
"v_fma_f32 v155,v155,s54,v14\n"
MF "v[84:87],a[220:223],a[192:195],v[84:87]\n"
"v_fma_f32 v156,v156,s54,v14\n"
"v_fma_f32 v157,v157,s54,v14\n"
MF "v[84:87],a[216:219],a[152:155],v[84:87]\n"
"v_fma_f32 v158,v158,s54,v14\n"
"v_fma_f32 v159,v159,s54,v14\n"
MF "v[92:95],a[216:219],a[156:159],v[92:95]\n"
CV "v207,v158,v159\n"
CV "v206,v156,v157\n"
MF "v[92:95],a[220:223],a[196:199],v[92:95]\n"
"s_waitcnt lgkmcnt(0)\n"
CV "v205,v154,v155\n"
CV "v204,v152,v153\n"
MF "v[56:59],a[200:203],a[60:63],v[56:59]\n"
TR "a[216:217],v0 offset:16392\n"
AW "a40,v204\n"
MF "v[56:59],a[204:207],a[100:103],v[56:59]\n"
TR "a[218:219],v0 offset:16904\n"
AW "a41,v205\n"
MF "v[64:67],a[204:207],a[104:107],v[64:67]\n"
AW "a42,v206\n"
AW "a43,v207\n"
MF "v[64:67],a[200:203],a[64:67],v[64:67]\n"
TR "a[220:221],v0 offset:20488\n"
"v_fma_f32 v144,v144,s54,v14\n"
MF "v[72:75],a[200:203],a[68:71],v[72:75]\n"
TR "a[222:223],v0 offset:21000\n"
"v_fma_f32 v145,v145,s54,v14\n"
MF "v[72:75],a[204:207],a[108:111],v[72:75]\n"
"v_fma_f32 v146,v146,s54,v14\n"
"v_fma_f32 v147,v147,s54,v14\n"
MF "v[80:83],a[204:207],a[112:115],v[80:83]\n"
"v_fma_f32 v148,v148,s54,v14\n"
"v_fma_f32 v149,v149,s54,v14\n"
MF "v[80:83],a[200:203],a[72:75],v[80:83]\n"
"v_fma_f32 v150,v150,s54,v14\n"
"v_fma_f32 v151,v151,s54,v14\n"
MF "v[88:91],a[200:203],a[76:79],v[88:91]\n"
CV "v203,v150,v151\n"
CV "v202,v148,v149\n"
MF "v[88:91],a[204:207],a[116:119],v[88:91]\n"
CV "v201,v146,v147\n"
CV "v200,v144,v145\n"
MF "v[88:91],a[208:211],a[156:159],v[88:91]\n"
TR "a[200:201],v0 offset:32776\n"
AW "a16,v200\n"
MF "v[88:91],a[212:215],a[196:199],v[88:91]\n"
TR "a[202:203],v0 offset:33288\n"
AW "a17,v201\n"
MF "v[80:83],a[212:215],a[192:195],v[80:83]\n"
AW "a18,v202\n"
AW "a19,v203\n"
MF "v[80:83],a[208:211],a[152:155],v[80:83]\n"
TR "a[204:205],v0 offset:36872\n"
"v_fma_f32 v136,v136,s54,v14\n"
MF "v[72:75],a[208:211],a[148:151],v[72:75]\n"
TR "a[206:207],v0 offset:37384\n"
"v_fma_f32 v137,v137,s54,v14\n"
MF "v[72:75],a[212:215],a[188:191],v[72:75]\n"
"v_fma_f32 v138,v138,s54,v14\n"
"v_fma_f32 v139,v139,s54,v14\n"
MF "v[64:67],a[212:215],a[184:187],v[64:67]\n"
"v_fma_f32 v140,v140,s54,v14\n"
"v_fma_f32 v141,v141,s54,v14\n"
MF "v[64:67],a[208:211],a[144:147],v[64:67]\n"
"v_fma_f32 v142,v142,s54,v14\n"
"v_fma_f32 v143,v143,s54,v14\n"
MF "v[56:59],a[208:211],a[140:143],v[56:59]\n"
CV "v199,v142,v143\n"
CV "v198,v140,v141\n"
MF "v[56:59],a[212:215],a[180:183],v[56:59]\n"
"s_waitcnt lgkmcnt(4)\n"
CV "v197,v138,v139\n"
CV "v196,v136,v137\n"
MF "v[20:23],a[216:219],a[60:63],v[20:23]\n"
TR "a[208:209],v0 offset:16384\n"
AW "a12,v196\n"
MF "v[20:23],a[220:223],a[100:103],v[20:23]\n"
TR "a[210:211],v0 offset:16896\n"
AW "a13,v197\n"
MF "v[28:31],a[220:223],a[104:107],v[28:31]\n"
AW "a14,v198\n"
AW "a15,v199\n"
MF "v[28:31],a[216:219],a[64:67],v[28:31]\n"
TR "a[212:213],v0 offset:20480\n"
"v_fma_f32 v128,v128,s54,v14\n"
MF "v[36:39],a[216:219],a[68:71],v[36:39]\n"
TR "a[214:215],v0 offset:20992\n"
"v_fma_f32 v129,v129,s54,v14\n"
MF "v[36:39],a[220:223],a[108:111],v[36:39]\n"
"v_fma_f32 v130,v130,s54,v14\n"
"v_fma_f32 v131,v131,s54,v14\n"
MF "v[44:47],a[220:223],a[112:115],v[44:47]\n"
"v_fma_f32 v132,v132,s54,v14\n"
"v_fma_f32 v133,v133,s54,v14\n"
MF "v[44:47],a[216:219],a[72:75],v[44:47]\n"
"v_fma_f32 v134,v134,s54,v14\n"
"v_fma_f32 v135,v135,s54,v14\n"
MF "v[52:55],a[216:219],a[76:79],v[52:55]\n"
CV "v195,v134,v135\n"
CV "v194,v132,v133\n"
MF "v[52:55],a[220:223],a[116:119],v[52:55]\n"
"s_waitcnt lgkmcnt(4)\n"
CV "v193,v130,v131\n"
CV "v192,v128,v129\n"
MF "v[20:23],a[200:203],a[140:143],v[20:23]\n"
TR "a[216:217],v0 offset:32768\n"
AW "a8,v192\n"
MF "v[20:23],a[204:207],a[180:183],v[20:23]\n"
TR "a[218:219],v0 offset:33280\n"
AW "a9,v193\n"
MF "v[28:31],a[204:207],a[184:187],v[28:31]\n"
AW "a10,v194\n"
AW "a11,v195\n"
MF "v[28:31],a[200:203],a[144:147],v[28:31]\n"
TR "a[220:221],v0 offset:36864\n"
"v_fma_f32 v120,v120,s54,v14\n"
MF "v[36:39],a[200:203],a[148:151],v[36:39]\n"
TR "a[222:223],v0 offset:37376\n"
"v_fma_f32 v121,v121,s54,v14\n"
MF "v[36:39],a[204:207],a[188:191],v[36:39]\n"
"v_fma_f32 v122,v122,s54,v14\n"
"v_fma_f32 v123,v123,s54,v14\n"
MF "v[44:47],a[204:207],a[192:195],v[44:47]\n"
"v_fma_f32 v124,v124,s54,v14\n"
"v_fma_f32 v125,v125,s54,v14\n"
MF "v[44:47],a[200:203],a[152:155],v[44:47]\n"
"v_fma_f32 v126,v126,s54,v14\n"
"v_fma_f32 v127,v127,s54,v14\n"
MF "v[52:55],a[200:203],a[156:159],v[52:55]\n"
"s_waitcnt lgkmcnt(0)\n"
CV "v191,v126,v127\n"
CV "v190,v124,v125\n"
MF "v[52:55],a[204:207],a[196:199],v[52:55]\n"
"s_barrier\n"
MF "v[16:19],a[208:211],a[60:63],v[16:19]\n"
CV "v189,v122,v123\n"
CV "v188,v120,v121\n"
MF "v[16:19],a[212:215],a[100:103],v[16:19]\n"
AW "a4,v188\n"
AW "a5,v189\n"
MF "v[24:27],a[212:215],a[104:107],v[24:27]\n"
AW "a6,v190\n"
AW "a7,v191\n"
MF "v[24:27],a[208:211],a[64:67],v[24:27]\n"
"v_fma_f32 v112,v112,s54,v14\n"
"v_fma_f32 v113,v113,s54,v14\n"
MF "v[32:35],a[208:211],a[68:71],v[32:35]\n"
"v_fma_f32 v114,v114,s54,v14\n"
"v_fma_f32 v115,v115,s54,v14\n"
MF "v[32:35],a[212:215],a[108:111],v[32:35]\n"
"v_fma_f32 v116,v116,s54,v14\n"
"v_fma_f32 v117,v117,s54,v14\n"
MF "v[40:43],a[212:215],a[112:115],v[40:43]\n"
"v_fma_f32 v118,v118,s54,v14\n"
"v_fma_f32 v119,v119,s54,v14\n"
MF "v[40:43],a[208:211],a[72:75],v[40:43]\n"
CV "v187,v118,v119\n"
CV "v186,v116,v117\n"
MF "v[48:51],a[208:211],a[76:79],v[48:51]\n"
CV "v185,v114,v115\n"
CV "v184,v112,v113\n"
MF "v[48:51],a[212:215],a[116:119],v[48:51]\n"
AW "a0,v184\n"
AW "a1,v185\n"
MF "v[48:51],a[216:219],a[156:159],v[48:51]\n"
AW "a2,v186\n"
AW "a3,v187\n"
MF "v[48:51],a[220:223],a[196:199],v[48:51]\n"
"v_fma_f32 v104,v104,s54,v14\n"
"v_fma_f32 v105,v105,s54,v14\n"
MF "v[40:43],a[220:223],a[192:195],v[40:43]\n"
"v_fma_f32 v106,v106,s54,v14\n"
"v_fma_f32 v107,v107,s54,v14\n"
MF "v[40:43],a[216:219],a[152:155],v[40:43]\n"
"v_fma_f32 v108,v108,s54,v14\n"
"v_fma_f32 v109,v109,s54,v14\n"
MF "v[32:35],a[216:219],a[148:151],v[32:35]\n"
"v_fma_f32 v110,v110,s54,v14\n"
"v_fma_f32 v111,v111,s54,v14\n"
MF "v[32:35],a[220:223],a[188:191],v[32:35]\n"
CV "v183,v110,v111\n"
CV "v182,v108,v109\n"
MF "v[24:27],a[220:223],a[184:187],v[24:27]\n"
CV "v181,v106,v107\n"
CV "v180,v104,v105\n"
MF "v[24:27],a[216:219],a[144:147],v[24:27]\n"
AW "a36,v180\n"
AW "a37,v181\n"
MF "v[16:19],a[216:219],a[140:143],v[16:19]\n"
AW "a38,v182\n"
AW "a39,v183\n"
MF "v[16:19],a[220:223],a[180:183],v[16:19]\n"
"s_mov_b32 s57,1\n"
"s_mov_b32 s69,99\n"
AR "v0,a240\n"
AR "v2,a241\n"
AR "v4,a242\n"
AR "v6,a243\n"
"v_xor_b32 v1,64,v0\n"
"v_xor_b32 v3,64,v2\n"
"v_xor_b32_e64 v5,64,v4\n"
"v_add_u32 v11,97280,v6\n"
"v_add_u32 v12,194560,v6\n"
"v_add_u32 v13,291840,v6\n"
"s_mov_b64 vcc,-1\n"
"s_waitcnt vmcnt(10) lgkmcnt(0)\n"
"s_barrier\n"
"s_nop 0\n"
TR "a[200:201],v0\n"
TR "a[202:203],v0 offset:512\n"
TR "a[204:205],v0 offset:4096\n"
TR "a[206:207],v0 offset:4608\n"
"ds_read_b128 a[60:63],v2\n"
"ds_read_b128 a[100:103],v3\n"
"ds_read_b128 a[64:67],v2 offset:2048\n"
"ds_read_b128 a[104:107],v3 offset:2048\n"
"ds_read_b128 a[68:71],v2 offset:4096\n"
"ds_read_b128 a[108:111],v3 offset:4096\n"
"ds_read_b128 a[72:75],v2 offset:6144\n"
"ds_read_b128 a[112:115],v3 offset:6144\n"
"ds_read_b128 a[76:79],v2 offset:8192\n"
"ds_read_b128 a[116:119],v3 offset:8192\n"
TR "a[208:209],v0 offset:8\n"
TR "a[210:211],v0 offset:520\n"
TR "a[212:213],v0 offset:4104\n"
TR "a[214:215],v0 offset:4616\n"
"v_pk_fma_f32 v[96:97],v[96:97],s[54:55],v[14:15] op_sel_hi:[1,1,0]\n"
"v_pk_fma_f32 v[98:99],v[98:99],s[54:55],v[14:15] op_sel_hi:[1,1,0]\n"
"v_pk_fma_f32 v[100:101],v[100:101],s[54:55],v[14:15] op_sel_hi:[1,1,0]\n"
"v_pk_fma_f32 v[102:103],v[102:103],s[54:55],v[14:15] op_sel_hi:[1,1,0]\n"
CV "v179,v102,v103\n"
CV "v178,v100,v101\n"
CV "v177,v98,v99\n"
CV "v176,v96,v97\n"
AW "a32,v176\n"
AW "a33,v177\n"
AW "a34,v178\n"
AW "a35,v179\n"
"v_pk_fma_f32 v[88:89],v[88:89],s[54:55],v[14:15] op_sel_hi:[1,1,0]\n"
"v_pk_fma_f32 v[90:91],v[90:91],s[54:55],v[14:15] op_sel_hi:[1,1,0]\n"
"v_pk_fma_f32 v[92:93],v[92:93],s[54:55],v[14:15] op_sel_hi:[1,1,0]\n"
".Lpg15c:\n"
"s_cbranch_execz .Lpg2a\n"
"s_nop 0\n"
"v_pk_fma_f32 v[94:95],v[94:95],s[54:55],v[14:15] op_sel_hi:[1,1,0]\n"
CV "v175,v94,v95\n"
CV "v174,v92,v93\n"
CV "v173,v90,v91\n"
CV "v172,v88,v89\n"
AW "a28,v172\n"
AW "a29,v173\n"
AW "a30,v174\n"
AW "a31,v175\n"
"v_pk_fma_f32 v[80:81],v[80:81],s[54:55],v[14:15] op_sel_hi:[1,1,0]\n"
"v_pk_fma_f32 v[82:83],v[82:83],s[54:55],v[14:15] op_sel_hi:[1,1,0]\n"
"v_pk_fma_f32 v[84:85],v[84:85],s[54:55],v[14:15] op_sel_hi:[1,1,0]\n"
"v_pk_fma_f32 v[86:87],v[86:87],s[54:55],v[14:15] op_sel_hi:[1,1,0]\n"
CV "v171,v86,v87\n"
CV "v170,v84,v85\n"
CV "v169,v82,v83\n"
CV "v168,v80,v81\n"
AW "a24,v168\n"
AW "a25,v169\n"
AW "a26,v170\n"
AW "a27,v171\n"
"v_pk_fma_f32 v[72:73],v[72:73],s[54:55],v[14:15] op_sel_hi:[1,1,0]\n"
"v_pk_fma_f32 v[74:75],v[74:75],s[54:55],v[14:15] op_sel_hi:[1,1,0]\n"
"v_pk_fma_f32 v[76:77],v[76:77],s[54:55],v[14:15] op_sel_hi:[1,1,0]\n"
"v_pk_fma_f32 v[78:79],v[78:79],s[54:55],v[14:15] op_sel_hi:[1,1,0]\n"
CV "v167,v78,v79\n"
CV "v166,v76,v77\n"
CV "v165,v74,v75\n"
CV "v164,v72,v73\n"
AW "a20,v164\n"
AW "a21,v165\n"
AW "a22,v166\n"
AW "a23,v167\n"
"v_pk_fma_f32 v[64:65],v[64:65],s[54:55],v[14:15] op_sel_hi:[1,1,0]\n"
"v_pk_fma_f32 v[66:67],v[66:67],s[54:55],v[14:15] op_sel_hi:[1,1,0]\n"
"v_pk_fma_f32 v[68:69],v[68:69],s[54:55],v[14:15] op_sel_hi:[1,1,0]\n"
"v_pk_fma_f32 v[70:71],v[70:71],s[54:55],v[14:15] op_sel_hi:[1,1,0]\n"
CV "v163,v70,v71\n"
CV "v162,v68,v69\n"
CV "v161,v66,v67\n"
CV "v160,v64,v65\n"
AW "a176,v160\n"
AW "a177,v161\n"
AW "a178,v162\n"
AW "a179,v163\n"
"v_pk_fma_f32 v[56:57],v[56:57],s[54:55],v[14:15] op_sel_hi:[1,1,0]\n"
"v_pk_fma_f32 v[58:59],v[58:59],s[54:55],v[14:15] op_sel_hi:[1,1,0]\n"
"v_pk_fma_f32 v[60:61],v[60:61],s[54:55],v[14:15] op_sel_hi:[1,1,0]\n"
"v_pk_fma_f32 v[62:63],v[62:63],s[54:55],v[14:15] op_sel_hi:[1,1,0]\n"
CV "v159,v62,v63\n"
CV "v158,v60,v61\n"
CV "v157,v58,v59\n"
CV "v156,v56,v57\n"
AW "a172,v156\n"
AW "a173,v157\n"
AW "a174,v158\n"
AW "a175,v159\n"
"v_pk_mul_f32 v[48:49],v[48:49],s[54:55]\n"
"v_pk_mul_f32 v[50:51],v[50:51],s[54:55]\n"
"v_pk_mul_f32 v[52:53],v[52:53],s[54:55]\n"
"v_pk_mul_f32 v[54:55],v[54:55],s[54:55]\n"
CV "v155,v54,v55\n"
CV "v154,v52,v53\n"
CV "v153,v50,v51\n"
CV "v152,v48,v49\n"
AW "a168,v152\n"
AW "a169,v153\n"
AW "a170,v154\n"
AW "a171,v155\n"
"v_pk_mul_f32 v[40:41],v[40:41],s[54:55]\n"
"v_pk_mul_f32 v[42:43],v[42:43],s[54:55]\n"
"v_pk_mul_f32 v[44:45],v[44:45],s[54:55]\n"
"v_pk_mul_f32 v[46:47],v[46:47],s[54:55]\n"
CV "v151,v46,v47\n"
CV "v150,v44,v45\n"
CV "v149,v42,v43\n"
CV "v148,v40,v41\n"
AW "a120,v148\n"
AW "a121,v149\n"
AW "a122,v150\n"
AW "a123,v151\n"
"v_pk_mul_f32 v[32:33],v[32:33],s[54:55]\n"
"v_pk_mul_f32 v[34:35],v[34:35],s[54:55]\n"
"v_pk_mul_f32 v[36:37],v[36:37],s[54:55]\n"
"v_pk_mul_f32 v[38:39],v[38:39],s[54:55]\n"
CV "v147,v38,v39\n"
CV "v146,v36,v37\n"
CV "v145,v34,v35\n"
CV "v144,v32,v33\n"
AW "a96,v144\n"
AW "a97,v145\n"
AW "a98,v146\n"
AW "a99,v147\n"
"v_pk_mul_f32 v[24:25],v[24:25],s[54:55]\n"
"v_pk_mul_f32 v[26:27],v[26:27],s[54:55]\n"
"v_pk_mul_f32 v[28:29],v[28:29],s[54:55]\n"
"v_pk_mul_f32 v[30:31],v[30:31],s[54:55]\n"
CV "v143,v30,v31\n"
CV "v142,v28,v29\n"
CV "v141,v26,v27\n"
CV "v140,v24,v25\n"
AW "a92,v140\n"
AW "a93,v141\n"
AW "a94,v142\n"
AW "a95,v143\n"
"v_pk_mul_f32 v[16:17],v[16:17],s[54:55]\n"
"v_pk_mul_f32 v[18:19],v[18:19],s[54:55]\n"
"v_pk_mul_f32 v[20:21],v[20:21],s[54:55]\n"
"v_pk_mul_f32 v[22:23],v[22:23],s[54:55]\n"
CV "v139,v22,v23\n"
CV "v138,v20,v21\n"
CV "v137,v18,v19\n"
CV "v136,v16,v17\n"
AW "a88,v136\n"
AW "a89,v137\n"
AW "a90,v138\n"
AW "a91,v139\n"
"s_waitcnt lgkmcnt(0)\n"
"s_branch .Lq0\n"
".Lpgpsl_b:\n"
"s_cbranch_execz .Lpg3a\n"
".Lpsl:\n"
"s_sleep 1\n"
".Lpsub:\n"
"s_memrealtime s[100:101]\n"
"s_waitcnt lgkmcnt(0)\n"
"s_sub_u32 s101,s21,s100\n"
"s_cmp_gt_i32 s101,0\n"
"s_cbranch_scc1 .Lpsl\n"
"s_add_u32 s21,s100,s97\n"
"s_setpc_b64 s[52:53]\n"
".Lstub_6:\n"
"v_cmp_le_f32 vcc,0x47800000,v10\n"
"s_cmp_eq_u32 s77,0\n"
"s_cbranch_scc0 .Lnop_6\n"
"s_call_b64 s[52:53],.Lpsub\n"
".Lnop_6:\n"
"s_cbranch_vccz .Lback_6\n"
"s_call_b64 s[52:53],.Lslow_1\n"
"s_branch .Lback_6\n"
".Lstub_7:\n"
"v_cmp_le_f32 vcc,0x47800000,v10\n"
"s_cmp_eq_u32 s77,0\n"
"s_cbranch_scc0 .Lnop_7\n"
"s_call_b64 s[52:53],.Lpsub\n"
"s_mul_i32 s58,s57,4\n"
"s_add_u32 s58,s58,s61\n"
"s_cmp_eq_u32 s58,4\n"
"s_cselect_b32 s77,0x47800000,s77\n"
"s_cselect_b32 s95,0x47800000,s95\n"
".Lnop_7:\n"
"s_cbranch_vccz .Lback_7\n"
"s_call_b64 s[52:53],.Lslow_0\n"
"s_branch .Lback_7\n"
".Lstub_8:\n"
"s_call_b64 s[52:53],.Lslow_1\n"
"s_branch .Lback_8\n"
".Lstub_9:\n"
"s_call_b64 s[52:53],.Lslow_0\n"
"s_branch .Lback_9\n"
".Lstub_10:\n"
"s_call_b64 s[52:53],.Lslow_1\n"
"s_branch .Lback_10\n"
".Lstub_11:\n"
"s_call_b64 s[52:53],.Lslow_1\n"
"s_branch .Lback_11\n"
".Lstub_12:\n"
"s_call_b64 s[52:53],.Lslow_0\n"
"s_branch .Lback_12\n"
".Lstub_13:\n"
"s_call_b64 s[52:53],.Lslow_1\n"
"s_branch .Lback_13\n"
".Lstub_20:\n"
"v_cmp_le_f32 vcc,0x47800000,v10\n"
"s_cmp_eq_u32 s77,0\n"
"s_cbranch_scc0 .Lnop_20\n"
"s_call_b64 s[52:53],.Lpsub\n"
".Lnop_20:\n"
"s_cbranch_vccz .Lback_20\n"
"s_call_b64 s[52:53],.Lslow_1\n"
"s_branch .Lback_20\n"
".Lstub_21:\n"
"v_cmp_le_f32 vcc,0x47800000,v10\n"
"s_cmp_eq_u32 s77,0\n"
"s_cbranch_scc0 .Lnop_21\n"
"s_call_b64 s[52:53],.Lpsub\n"
".Lnop_21:\n"
"s_cbranch_vccz .Lback_21\n"
"s_call_b64 s[52:53],.Lslow_0\n"
"s_branch .Lback_21\n"
".Lstub_22:\n"
"v_cmp_le_f32 vcc,0x47800000,v10\n"
"s_cmp_eq_u32 s77,0\n"
"s_cbranch_scc0 .Lnop_22\n"
"s_call_b64 s[52:53],.Lpsub\n"
".Lnop_22:\n"
"s_cbranch_vccz .Lback_22\n"
"s_call_b64 s[52:53],.Lslow_1\n"
"s_branch .Lback_22\n"
".Lstub_23:\n"
"v_cmp_le_f32 vcc,0x47800000,v10\n"
"s_cmp_eq_u32 s77,0\n"
"s_cbranch_scc0 .Lnop_23\n"
"s_call_b64 s[52:53],.Lpsub\n"
".Lnop_23:\n"
"s_cbranch_vccz .Lback_23\n"
"s_call_b64 s[52:53],.Lslow_0\n"
"s_branch .Lback_23\n"
".Lstub_24:\n"
"v_cmp_le_f32 vcc,0x47800000,v10\n"
"s_cmp_eq_u32 s61,1\n"
"s_cbranch_scc0 .Lhook_24\n"
"s_mov_b64 exec,0\n"
"s_cmp_eq_u32 s56,0\n"
"s_cbranch_scc1 .Lpg26h\n"
"s_cmp_eq_u32 s56,1\n"
"s_cbranch_scc1 .Lpg25h\n"
"s_cmp_eq_u32 s56,2\n"
"s_cbranch_scc1 .Lpg24h\n"
"s_branch .Lpg23h\n"
".Lisr4:\n"
"s_mov_b64 exec,-1\n"
".Lisx4:\n"
"s_and_b32 s58,s56,1\n"
"s_mul_i32 s79,s58,12288\n"
"s_lshl_b32 s15,s58,10\n"
"s_lshr_b32 s58,s56,1\n"
"s_mul_i32 s5,s58,6144\n"
"s_add_u32 s5,s5,s79\n"
"s_lshl_b32 s59,s58,8\n"
"s_add_u32 s15,s15,s59\n"
"s_add_u32 s15,s15,159744\n"
"s_add_u32 s88,s88,1024\n"
"s_add_u32 s89,s89,1024\n"
"s_mul_i32 s58,s58,61440\n"
"s_add_u32 s2,s89,s58\n"
"s_add_u32 s3,s88,s58\n"
"s_add_u32 s0,s2,122880\n"
"s_add_u32 s1,s3,122880\n"
"s_lshl_b32 s58,s56,10\n"
"s_add_u32 s9,s58,73728\n"
"s_add_u32 s14,s58,159744\n"
"s_bfe_u32 s4,s22,0x10014\n"
"s_lshl_b32 s4,s4,5\n"
"s_mov_b32 s6,896\n"
".Lhook_24:\n"
"s_cmp_eq_u32 s77,0\n"
"s_cbranch_scc0 .Lnop_24\n"
"s_call_b64 s[52:53],.Lpsub\n"
".Lnop_24:\n"
"s_cbranch_vccz .Lback_24\n"
"s_call_b64 s[52:53],.Lslow_1\n"
"s_branch .Lback_24\n"
".Lstub_25:\n"
"v_cmp_le_f32 vcc,0x47800000,v10\n"
"s_cmp_eq_u32 s77,0\n"
"s_cbranch_scc0 .Lnop_25\n"
"s_call_b64 s[52:53],.Lpsub\n"
".Lnop_25:\n"
"s_cbranch_vccz .Lback_25\n"
"s_call_b64 s[52:53],.Lslow_0\n"
"s_branch .Lback_25\n"
".Lstub_26:\n"
"s_call_b64 s[52:53],.Lslow_1\n"
"s_branch .Lback_26\n"
".Lstub_27:\n"
"s_call_b64 s[52:53],.Lslow_0\n"
"s_branch .Lback_27\n"
".Lstub_28:\n"
"s_call_b64 s[52:53],.Lslow_1\n"
"s_branch .Lback_28\n"
".Lackspin_q:\n"
"s_cmp_eq_u32 s98,0\n"
"s_cbranch_scc1 .Lackok_q\n"
"s_sub_u32 s98,s98,1\n"
"s_sleep 8\n"
"v_bfe_u32 v14,v9,4,5\n"
"v_lshlrev_b32 v14,6,v14\n"
"s_lshl_b32 s58,s78,2\n"
"s_add_u32 s58,s58,s86\n"
"buffer_load_dword v10,v14,s[40:43],s58 offen offset:128 sc1\n"
"s_waitcnt vmcnt(0)\n"
"s_branch .Lack_q\n"
".Lpgep_g:\n"
"s_cbranch_execz .Lpg19g\n"
".Lep:\n"
MF "v[72:75],v[16:19],v[136:139],v[32:35]\n"
"s_nop 0\n"
"ds_read_b128 v[24:27],v0 offset:1024\n"
"ds_read_b128 v[28:31],v1 offset:1024\n"
MF "v[72:75],v[20:23],v[156:159],v[72:75]\n"
MF "v[76:79],v[20:23],v[160:163],v[36:39]\n"
MF "v[76:79],v[16:19],v[140:143],v[76:79]\n"
MF "v[80:83],v[16:19],v[144:147],v[40:43]\n"
MF "v[80:83],v[20:23],v[164:167],v[80:83]\n"
MF "v[84:87],v[20:23],v[168:171],v[44:47]\n"
MF "v[84:87],v[16:19],v[148:151],v[84:87]\n"
"s_add_u32 m0,s65,49152\n"
MF "v[88:91],v[16:19],v[152:155],v[48:51]\n"
LD "v8,s[36:39],s73 offen offset:3072 nt lds\n"
MF "v[88:91],v[20:23],v[172:175],v[88:91]\n"
"s_waitcnt lgkmcnt(0)\n"
"ds_read_b128 v[16:19],v0 offset:2048\n"
"ds_read_b128 v[20:23],v1 offset:2048\n"
MF "v[88:91],v[24:27],v[192:195],v[88:91]\n"
"v_exp_f32 v124,v124\n"
MF "v[88:91],v[28:31],v[212:215],v[88:91]\n"
"s_add_u32 m0,s65,50048\n"
"v_exp_f32 v125,v125\n"
MF "v[84:87],v[28:31],v[208:211],v[84:87]\n"
LD "v8,s[36:39],s73 offen offset:3200 nt lds\n"
MF "v[84:87],v[24:27],v[188:191],v[84:87]\n"
"v_exp_f32 v126,v126\n"
MF "v[80:83],v[24:27],v[184:187],v[80:83]\n"
"v_exp_f32 v127,v127\n"
MF "v[80:83],v[28:31],v[204:207],v[80:83]\n"
"s_add_u32 m0,s65,50944\n"
"v_add_f32 v60,v60,v124\n"
"v_add_f32 v60,v60,v125\n"
MF "v[76:79],v[28:31],v[200:203],v[76:79]\n"
LD "v8,s[36:39],s73 offen offset:3328 nt lds\n"
MF "v[76:79],v[24:27],v[180:183],v[76:79]\n"
CV "v106,v124,v125\n"
"v_add_f32 v60,v60,v126\n"
MF "v[72:75],v[24:27],v[176:179],v[72:75]\n"
"v_add_f32 v60,v60,v127\n"
CV "v107,v126,v127\n"
MF "v[72:75],v[28:31],v[196:199],v[72:75]\n"
"s_waitcnt lgkmcnt(0)\n"
"ds_read_b128 v[24:27],v0 offset:6144\n"
"ds_read_b128 v[28:31],v1 offset:6144\n"
MF "v[72:75],v[16:19],v[216:219],v[72:75]\n"
"v_exp_f32 v128,v128\n"
MF "v[72:75],v[20:23],v[236:239],v[72:75]\n"
"v_exp_f32 v129,v129\n"
MF "v[76:79],v[20:23],v[240:243],v[76:79]\n"
"v_exp_f32 v130,v130\n"
MF "v[76:79],v[16:19],v[220:223],v[76:79]\n"
"v_exp_f32 v131,v131\n"
MF "v[80:83],v[16:19],v[224:227],v[80:83]\n"
"v_add_f32 v61,v61,v128\n"
"v_add_f32 v61,v61,v129\n"
MF "v[80:83],v[20:23],v[244:247],v[80:83]\n"
CV "v110,v128,v129\n"
"v_add_f32 v61,v61,v130\n"
MF "v[84:87],v[20:23],v[248:251],v[84:87]\n"
"v_add_f32 v61,v61,v131\n"
CV "v111,v130,v131\n"
MF "v[84:87],v[16:19],v[228:231],v[84:87]\n"
"v_max3_f32 v10,v57,v58,v59\n"
"v_max3_f32 v10,v10,v60,v61\n"
MF "v[88:91],v[16:19],v[232:235],v[88:91]\n"
"v_cmp_le_f32 vcc,0x47800000,v10\n"
MF "v[88:91],v[20:23],v[252:255],v[88:91]\n"
"s_waitcnt lgkmcnt(0)\n"
"s_cbranch_vccnz .Lstub_11\n"
".Lback_11:\n"
MF "v[112:115],v[24:27],v[136:139],v[32:35]\n"
"s_add_u32 s74,s74,12288\n"
"ds_read_b128 v[16:19],v0 offset:7168\n"
"ds_read_b128 v[20:23],v1 offset:7168\n"
MF "v[112:115],v[28:31],v[156:159],v[112:115]\n"
"s_cmp_eq_u32 s74,s20\n"
"s_cselect_b32 s74,s70,s74\n"
MF "v[116:119],v[28:31],v[160:163],v[36:39]\n"
"v_exp_f32 v72,v72\n"
MF "v[116:119],v[24:27],v[140:143],v[116:119]\n"
"v_exp_f32 v73,v73\n"
MF "v[120:123],v[24:27],v[144:147],v[40:43]\n"
"v_exp_f32 v74,v74\n"
MF "v[120:123],v[28:31],v[164:167],v[120:123]\n"
"v_exp_f32 v75,v75\n"
MF "v[124:127],v[28:31],v[168:171],v[44:47]\n"
"v_add_f32 v52,v57,v72\n"
"v_add_f32 v52,v52,v73\n"
MF "v[124:127],v[24:27],v[148:151],v[124:127]\n"
CV "v72,v72,v73\n"
"v_add_f32 v52,v52,v74\n"
MF "v[128:131],v[24:27],v[152:155],v[48:51]\n"
"v_add_f32 v52,v52,v75\n"
CV "v73,v74,v75\n"
MF "v[128:131],v[28:31],v[172:175],v[128:131]\n"
"s_waitcnt lgkmcnt(0)\n"
"ds_read_b128 v[24:27],v0 offset:8192\n"
"ds_read_b128 v[28:31],v1 offset:8192\n"
MF "v[128:131],v[16:19],v[192:195],v[128:131]\n"
"v_exp_f32 v76,v76\n"
MF "v[128:131],v[20:23],v[212:215],v[128:131]\n"
"v_exp_f32 v77,v77\n"
MF "v[124:127],v[20:23],v[208:211],v[124:127]\n"
"v_exp_f32 v78,v78\n"
MF "v[124:127],v[16:19],v[188:191],v[124:127]\n"
"v_exp_f32 v79,v79\n"
MF "v[120:123],v[16:19],v[184:187],v[120:123]\n"
"v_add_f32 v53,v58,v76\n"
"v_add_f32 v53,v53,v77\n"
MF "v[120:123],v[20:23],v[204:207],v[120:123]\n"
CV "v76,v76,v77\n"
"v_add_f32 v53,v53,v78\n"
MF "v[116:119],v[20:23],v[200:203],v[116:119]\n"
"s_waitcnt vmcnt(12) lgkmcnt(0)\n"
"v_add_f32 v53,v53,v79\n"
CV "v77,v78,v79\n"
MF "v[116:119],v[16:19],v[180:183],v[116:119]\n"
"s_barrier\n"
MF "v[112:115],v[16:19],v[176:179],v[112:115]\n"
"v_exp_f32 v80,v80\n"
MF "v[112:115],v[20:23],v[196:199],v[112:115]\n"
"s_sub_u32 s58,s71,s70\n"
TR "v[16:17],v2 offset:24576\n"
TR "v[18:19],v2 offset:30720\n"
MF "v[112:115],v[24:27],v[216:219],v[112:115]\n"
"s_add_u32 s72,s72,s58\n"
TR "v[20:21],v3 offset:24576\n"
TR "v[22:23],v3 offset:30720\n"
MF "v[112:115],v[28:31],v[236:239],v[112:115]\n"
"s_add_u32 s16,s16,s58\n"
"s_add_u32 s17,s17,s58\n"
"s_add_u32 s72,s72,6144\n"
MF "v[116:119],v[28:31],v[240:243],v[116:119]\n"
"s_cmp_eq_u32 s72,s17\n"
"s_cselect_b32 s72,s16,s72\n"
"s_add_u32 m0,s64,0\n"
MF "v[116:119],v[24:27],v[220:223],v[116:119]\n"
LD "v7,s[32:35],s72 offen nt lds\n"
MF "v[120:123],v[24:27],v[224:227],v[120:123]\n"
"s_add_u32 m0,s64,896\n"
"v_exp_f32 v81,v81\n"
MF "v[120:123],v[28:31],v[244:247],v[120:123]\n"
LD "v7,s[32:35],s72 offen offset:128 nt lds\n"
MF "v[124:127],v[28:31],v[248:251],v[124:127]\n"
"s_add_u32 m0,s64,1792\n"
"v_exp_f32 v82,v82\n"
MF "v[124:127],v[24:27],v[228:231],v[124:127]\n"
LD "v7,s[32:35],s72 offen offset:256 nt lds\n"
MF "v[128:131],v[24:27],v[232:235],v[128:131]\n"
"s_add_u32 s72,s72,6144\n"
"v_exp_f32 v83,v83\n"
MF "v[128:131],v[28:31],v[252:255],v[128:131]\n"
"s_waitcnt lgkmcnt(0)\n"
TR "v[24:25],v4 offset:24576\n"
TR "v[26:27],v4 offset:30720\n"
MF "a[0:3],v[16:19],v[92:95],a[0:3]\n"
"s_add_u32 m0,s64,6144\n"
TR "v[28:29],v5 offset:24576\n"
TR "v[30:31],v5 offset:30720\n"
MF "a[20:23],v[20:23],v[92:95],a[20:23]\n"
LD "v7,s[32:35],s72 offen nt lds\n"
MF "a[24:27],v[20:23],v[96:99],a[24:27]\n"
"v_add_f32 v54,v59,v80\n"
"v_add_f32 v54,v54,v81\n"
MF "a[4:7],v[16:19],v[96:99],a[4:7]\n"
"s_add_u32 m0,s64,7040\n"
CV "v80,v80,v81\n"
"v_add_f32 v54,v54,v82\n"
MF "a[8:11],v[16:19],v[100:103],a[8:11]\n"
LD "v7,s[32:35],s72 offen offset:128 nt lds\n"
MF "a[28:31],v[20:23],v[100:103],a[28:31]\n"
"v_add_f32 v54,v54,v83\n"
CV "v81,v82,v83\n"
MF "a[32:35],v[20:23],v[104:107],a[32:35]\n"
"s_add_u32 m0,s64,7936\n"
"v_exp_f32 v84,v84\n"
MF "a[12:15],v[16:19],v[104:107],a[12:15]\n"
LD "v7,s[32:35],s72 offen offset:256 nt lds\n"
MF "a[16:19],v[16:19],v[108:111],a[16:19]\n"
"v_exp_f32 v85,v85\n"
MF "a[36:39],v[20:23],v[108:111],a[36:39]\n"
"s_waitcnt lgkmcnt(0)\n"
TR "v[16:17],v2 offset:25600\n"
TR "v[18:19],v2 offset:31744\n"
MF "a[56:59],v[24:27],v[108:111],a[56:59]\n"
"s_nop 0\n"
TR "v[20:21],v3 offset:25600\n"
TR "v[22:23],v3 offset:31744\n"
MF "a[76:79],v[28:31],v[108:111],a[76:79]\n"
"v_exp_f32 v86,v86\n"
MF "a[72:75],v[28:31],v[104:107],a[72:75]\n"
"v_exp_f32 v87,v87\n"
MF "a[52:55],v[24:27],v[104:107],a[52:55]\n"
"v_add_f32 v55,v60,v84\n"
"v_add_f32 v55,v55,v85\n"
MF "a[48:51],v[24:27],v[100:103],a[48:51]\n"
CV "v84,v84,v85\n"
"v_add_f32 v55,v55,v86\n"
MF "a[68:71],v[28:31],v[100:103],a[68:71]\n"
"v_add_f32 v55,v55,v87\n"
CV "v85,v86,v87\n"
MF "a[64:67],v[28:31],v[96:99],a[64:67]\n"
"v_exp_f32 v88,v88\n"
MF "a[44:47],v[24:27],v[96:99],a[44:47]\n"
"v_exp_f32 v89,v89\n"
MF "a[40:43],v[24:27],v[92:95],a[40:43]\n"
"v_exp_f32 v90,v90\n"
MF "a[60:63],v[28:31],v[92:95],a[60:63]\n"
"s_waitcnt lgkmcnt(0)\n"
TR "v[24:25],v4 offset:25600\n"
TR "v[26:27],v4 offset:31744\n"
MF "a[80:83],v[16:19],v[92:95],a[80:83]\n"
"s_nop 0\n"
TR "v[28:29],v5 offset:25600\n"
TR "v[30:31],v5 offset:31744\n"
MF "a[100:103],v[20:23],v[92:95],a[100:103]\n"
"v_exp_f32 v91,v91\n"
MF "a[104:107],v[20:23],v[96:99],a[104:107]\n"
"v_add_f32 v56,v61,v88\n"
"v_add_f32 v56,v56,v89\n"
MF "a[84:87],v[16:19],v[96:99],a[84:87]\n"
CV "v88,v88,v89\n"
"v_add_f32 v56,v56,v90\n"
MF "a[88:91],v[16:19],v[100:103],a[88:91]\n"
"v_add_f32 v56,v56,v91\n"
CV "v89,v90,v91\n"
MF "a[108:111],v[20:23],v[100:103],a[108:111]\n"
"v_exp_f32 v112,v112\n"
MF "a[112:115],v[20:23],v[104:107],a[112:115]\n"
"v_exp_f32 v113,v113\n"
MF "a[92:95],v[16:19],v[104:107],a[92:95]\n"
"v_exp_f32 v114,v114\n"
MF "a[96:99],v[16:19],v[108:111],a[96:99]\n"
"v_exp_f32 v115,v115\n"
MF "a[116:119],v[20:23],v[108:111],a[116:119]\n"
"s_waitcnt lgkmcnt(0)\n"
TR "v[16:17],v2 offset:26624\n"
TR "v[18:19],v2 offset:32768\n"
MF "a[136:139],v[24:27],v[108:111],a[136:139]\n"
"s_nop 0\n"
TR "v[20:21],v3 offset:26624\n"
TR "v[22:23],v3 offset:32768\n"
MF "a[156:159],v[28:31],v[108:111],a[156:159]\n"
"v_add_f32 v52,v52,v112\n"
"v_add_f32 v52,v52,v113\n"
MF "a[152:155],v[28:31],v[104:107],a[152:155]\n"
CV "v74,v112,v113\n"
"v_add_f32 v52,v52,v114\n"
MF "a[132:135],v[24:27],v[104:107],a[132:135]\n"
"v_add_f32 v52,v52,v115\n"
CV "v75,v114,v115\n"
MF "a[128:131],v[24:27],v[100:103],a[128:131]\n"
"v_exp_f32 v116,v116\n"
MF "a[148:151],v[28:31],v[100:103],a[148:151]\n"
"v_exp_f32 v117,v117\n"
MF "a[144:147],v[28:31],v[96:99],a[144:147]\n"
"v_exp_f32 v118,v118\n"
MF "a[124:127],v[24:27],v[96:99],a[124:127]\n"
"v_exp_f32 v119,v119\n"
MF "a[120:123],v[24:27],v[92:95],a[120:123]\n"
"v_add_f32 v53,v53,v116\n"
"v_add_f32 v53,v53,v117\n"
MF "a[140:143],v[28:31],v[92:95],a[140:143]\n"
"s_waitcnt lgkmcnt(0)\n"
TR "v[24:25],v4 offset:26624\n"
TR "v[26:27],v4 offset:32768\n"
MF "a[160:163],v[16:19],v[92:95],a[160:163]\n"
"s_nop 0\n"
TR "v[28:29],v5 offset:26624\n"
TR "v[30:31],v5 offset:32768\n"
MF "a[180:183],v[20:23],v[92:95],a[180:183]\n"
CV "v78,v116,v117\n"
"v_add_f32 v53,v53,v118\n"
MF "a[184:187],v[20:23],v[96:99],a[184:187]\n"
"v_add_f32 v53,v53,v119\n"
CV "v79,v118,v119\n"
MF "a[164:167],v[16:19],v[96:99],a[164:167]\n"
"v_exp_f32 v120,v120\n"
MF "a[168:171],v[16:19],v[100:103],a[168:171]\n"
"v_exp_f32 v121,v121\n"
MF "a[188:191],v[20:23],v[100:103],a[188:191]\n"
"v_exp_f32 v122,v122\n"
MF "a[192:195],v[20:23],v[104:107],a[192:195]\n"
"v_exp_f32 v123,v123\n"
MF "a[172:175],v[16:19],v[104:107],a[172:175]\n"
"s_waitcnt vmcnt(12) lgkmcnt(0)\n"
"v_add_f32 v54,v54,v120\n"
"v_add_f32 v54,v54,v121\n"
MF "a[176:179],v[16:19],v[108:111],a[176:179]\n"
"s_barrier\n"
MF "a[196:199],v[20:23],v[108:111],a[196:199]\n"
"s_add_u32 s73,s73,12288\n"
"ds_read_b128 v[16:19],v0 offset:24576\n"
"ds_read_b128 v[20:23],v1 offset:24576\n"
MF "a[216:219],v[24:27],v[108:111],a[216:219]\n"
"s_cmp_eq_u32 s73,s19\n"
"s_cselect_b32 s73,s18,s73\n"
"s_add_u32 m0,s65,73728\n"
MF "a[236:239],v[28:31],v[108:111],a[236:239]\n"
LD "v8,s[36:39],s73 offen nt lds\n"
MF "a[232:235],v[28:31],v[104:107],a[232:235]\n"
"s_add_u32 m0,s65,74624\n"
CV "v82,v120,v121\n"
"v_add_f32 v54,v54,v122\n"
MF "a[212:215],v[24:27],v[104:107],a[212:215]\n"
LD "v8,s[36:39],s73 offen offset:128 nt lds\n"
MF "a[208:211],v[24:27],v[100:103],a[208:211]\n"
"s_add_u32 m0,s65,75520\n"
"v_add_f32 v54,v54,v123\n"
CV "v83,v122,v123\n"
MF "a[228:231],v[28:31],v[100:103],a[228:231]\n"
LD "v8,s[36:39],s73 offen offset:256 nt lds\n"
MF "a[224:227],v[28:31],v[96:99],a[224:227]\n"
MF "a[204:207],v[24:27],v[96:99],a[204:207]\n"
MF "a[200:203],v[24:27],v[92:95],a[200:203]\n"
MF "a[220:223],v[28:31],v[92:95],a[220:223]\n"
"s_waitcnt lgkmcnt(0)\n"
MF "v[92:95],v[16:19],v[136:139],v[32:35]\n"
"s_add_u32 m0,s65,73728\n"
"ds_read_b128 v[24:27],v0 offset:25600\n"
"ds_read_b128 v[28:31],v1 offset:25600\n"
MF "v[92:95],v[20:23],v[156:159],v[92:95]\n"
LD "v8,s[36:39],s73 offen offset:3072 nt lds\n"
MF "v[96:99],v[20:23],v[160:163],v[36:39]\n"
"v_exp_f32 v124,v124\n"
MF "v[96:99],v[16:19],v[140:143],v[96:99]\n"
"v_exp_f32 v125,v125\n"
MF "v[100:103],v[16:19],v[144:147],v[40:43]\n"
"s_add_u32 m0,s65,74624\n"
"v_exp_f32 v126,v126\n"
MF "v[100:103],v[20:23],v[164:167],v[100:103]\n"
LD "v8,s[36:39],s73 offen offset:3200 nt lds\n"
MF "v[104:107],v[20:23],v[168:171],v[44:47]\n"
"v_exp_f32 v127,v127\n"
MF "v[104:107],v[16:19],v[148:151],v[104:107]\n"
"s_add_u32 m0,s65,75520\n"
"v_add_f32 v55,v55,v124\n"
"v_add_f32 v55,v55,v125\n"
MF "v[108:111],v[16:19],v[152:155],v[48:51]\n"
LD "v8,s[36:39],s73 offen offset:3328 nt lds\n"
MF "v[108:111],v[20:23],v[172:175],v[108:111]\n"
"s_waitcnt lgkmcnt(0)\n"
"ds_read_b128 v[16:19],v0 offset:26624\n"
"ds_read_b128 v[20:23],v1 offset:26624\n"
MF "v[108:111],v[24:27],v[192:195],v[108:111]\n"
CV "v86,v124,v125\n"
"v_add_f32 v55,v55,v126\n"
MF "v[108:111],v[28:31],v[212:215],v[108:111]\n"
"v_add_f32 v55,v55,v127\n"
CV "v87,v126,v127\n"
MF "v[104:107],v[28:31],v[208:211],v[104:107]\n"
"v_exp_f32 v128,v128\n"
MF "v[104:107],v[24:27],v[188:191],v[104:107]\n"
"v_exp_f32 v129,v129\n"
MF "v[100:103],v[24:27],v[184:187],v[100:103]\n"
"v_exp_f32 v130,v130\n"
MF "v[100:103],v[28:31],v[204:207],v[100:103]\n"
"v_exp_f32 v131,v131\n"
MF "v[96:99],v[28:31],v[200:203],v[96:99]\n"
"v_add_f32 v56,v56,v128\n"
"v_add_f32 v56,v56,v129\n"
MF "v[96:99],v[24:27],v[180:183],v[96:99]\n"
CV "v90,v128,v129\n"
"v_add_f32 v56,v56,v130\n"
MF "v[92:95],v[24:27],v[176:179],v[92:95]\n"
"v_add_f32 v56,v56,v131\n"
CV "v91,v130,v131\n"
MF "v[92:95],v[28:31],v[196:199],v[92:95]\n"
"s_waitcnt lgkmcnt(0)\n"
"ds_read_b128 v[24:27],v0 offset:30720\n"
"ds_read_b128 v[28:31],v1 offset:30720\n"
MF "v[92:95],v[16:19],v[216:219],v[92:95]\n"
MF "v[92:95],v[20:23],v[236:239],v[92:95]\n"
MF "v[96:99],v[20:23],v[240:243],v[96:99]\n"
MF "v[96:99],v[16:19],v[220:223],v[96:99]\n"
MF "v[100:103],v[16:19],v[224:227],v[100:103]\n"
MF "v[100:103],v[20:23],v[244:247],v[100:103]\n"
MF "v[104:107],v[20:23],v[248:251],v[104:107]\n"
MF "v[104:107],v[16:19],v[228:231],v[104:107]\n"
".Lpg16g:\n"
"s_cbranch_execz .Lisr6\n"
"v_max3_f32 v10,v52,v53,v54\n"
"v_max3_f32 v10,v10,v55,v56\n"
MF "v[108:111],v[16:19],v[232:235],v[108:111]\n"
"v_cmp_le_f32 vcc,0x47800000,v10\n"
MF "v[108:111],v[20:23],v[252:255],v[108:111]\n"
"s_waitcnt lgkmcnt(0)\n"
"s_cbranch_vccnz .Lstub_12\n"
".Lback_12:\n"
MF "v[112:115],v[24:27],v[136:139],v[32:35]\n"
"s_add_u32 s74,s74,12288\n"
"ds_read_b128 v[16:19],v0 offset:31744\n"
"ds_read_b128 v[20:23],v1 offset:31744\n"
MF "v[112:115],v[28:31],v[156:159],v[112:115]\n"
"s_cmp_eq_u32 s74,s20\n"
"s_cselect_b32 s74,s70,s74\n"
MF "v[116:119],v[28:31],v[160:163],v[36:39]\n"
"v_exp_f32 v92,v92\n"
MF "v[116:119],v[24:27],v[140:143],v[116:119]\n"
"v_exp_f32 v93,v93\n"
MF "v[120:123],v[24:27],v[144:147],v[40:43]\n"
"v_exp_f32 v94,v94\n"
MF "v[120:123],v[28:31],v[164:167],v[120:123]\n"
"v_exp_f32 v95,v95\n"
MF "v[124:127],v[28:31],v[168:171],v[44:47]\n"
"v_add_f32 v57,v52,v92\n"
"v_add_f32 v57,v57,v93\n"
MF "v[124:127],v[24:27],v[148:151],v[124:127]\n"
CV "v92,v92,v93\n"
"v_add_f32 v57,v57,v94\n"
MF "v[128:131],v[24:27],v[152:155],v[48:51]\n"
"v_add_f32 v57,v57,v95\n"
CV "v93,v94,v95\n"
MF "v[128:131],v[28:31],v[172:175],v[128:131]\n"
"s_waitcnt lgkmcnt(0)\n"
"ds_read_b128 v[24:27],v0 offset:32768\n"
"ds_read_b128 v[28:31],v1 offset:32768\n"
MF "v[128:131],v[16:19],v[192:195],v[128:131]\n"
"v_exp_f32 v96,v96\n"
MF "v[128:131],v[20:23],v[212:215],v[128:131]\n"
"v_exp_f32 v97,v97\n"
MF "v[124:127],v[20:23],v[208:211],v[124:127]\n"
"v_exp_f32 v98,v98\n"
MF "v[124:127],v[16:19],v[188:191],v[124:127]\n"
"v_exp_f32 v99,v99\n"
MF "v[120:123],v[16:19],v[184:187],v[120:123]\n"
"v_add_f32 v58,v53,v96\n"
"v_add_f32 v58,v58,v97\n"
MF "v[120:123],v[20:23],v[204:207],v[120:123]\n"
CV "v96,v96,v97\n"
"v_add_f32 v58,v58,v98\n"
MF "v[116:119],v[20:23],v[200:203],v[116:119]\n"
"s_waitcnt vmcnt(12) lgkmcnt(0)\n"
"v_add_f32 v58,v58,v99\n"
CV "v97,v98,v99\n"
MF "v[116:119],v[16:19],v[180:183],v[116:119]\n"
"s_barrier\n"
MF "v[112:115],v[16:19],v[176:179],v[112:115]\n"
"v_exp_f32 v100,v100\n"
MF "v[112:115],v[20:23],v[196:199],v[112:115]\n"
"s_nop 0\n"
TR "v[16:17],v2\n"
TR "v[18:19],v2 offset:6144\n"
MF "v[112:115],v[24:27],v[216:219],v[112:115]\n"
"s_add_u32 s72,s72,6144\n"
TR "v[20:21],v3\n"
TR "v[22:23],v3 offset:6144\n"
MF "v[112:115],v[28:31],v[236:239],v[112:115]\n"
"s_cmp_eq_u32 s72,s17\n"
"s_cselect_b32 s72,s16,s72\n"
"s_add_u32 m0,s64,24576\n"
MF "v[116:119],v[28:31],v[240:243],v[116:119]\n"
LD "v7,s[32:35],s72 offen nt lds\n"
MF "v[116:119],v[24:27],v[220:223],v[116:119]\n"
"s_add_u32 m0,s64,25472\n"
"v_exp_f32 v101,v101\n"
MF "v[120:123],v[24:27],v[224:227],v[120:123]\n"
LD "v7,s[32:35],s72 offen offset:128 nt lds\n"
MF "v[120:123],v[28:31],v[244:247],v[120:123]\n"
"v_exp_f32 v102,v102\n"
MF "v[124:127],v[28:31],v[248:251],v[124:127]\n"
"s_add_u32 m0,s64,26368\n"
"v_exp_f32 v103,v103\n"
MF "v[124:127],v[24:27],v[228:231],v[124:127]\n"
LD "v7,s[32:35],s72 offen offset:256 nt lds\n"
MF "v[128:131],v[24:27],v[232:235],v[128:131]\n"
"s_add_u32 s72,s72,6144\n"
"v_add_f32 v59,v54,v100\n"
"v_add_f32 v59,v59,v101\n"
MF "v[128:131],v[28:31],v[252:255],v[128:131]\n"
"s_waitcnt lgkmcnt(0)\n"
TR "v[24:25],v4\n"
TR "v[26:27],v4 offset:6144\n"
MF "a[0:3],v[16:19],v[72:75],a[0:3]\n"
"s_add_u32 m0,s64,30720\n"
TR "v[28:29],v5\n"
TR "v[30:31],v5 offset:6144\n"
MF "a[20:23],v[20:23],v[72:75],a[20:23]\n"
LD "v7,s[32:35],s72 offen nt lds\n"
MF "a[24:27],v[20:23],v[76:79],a[24:27]\n"
CV "v100,v100,v101\n"
"v_add_f32 v59,v59,v102\n"
MF "a[4:7],v[16:19],v[76:79],a[4:7]\n"
"s_add_u32 m0,s64,31616\n"
"v_add_f32 v59,v59,v103\n"
CV "v101,v102,v103\n"
MF "a[8:11],v[16:19],v[80:83],a[8:11]\n"
LD "v7,s[32:35],s72 offen offset:128 nt lds\n"
MF "a[28:31],v[20:23],v[80:83],a[28:31]\n"
"v_exp_f32 v104,v104\n"
MF "a[32:35],v[20:23],v[84:87],a[32:35]\n"
"s_add_u32 m0,s64,32512\n"
"v_exp_f32 v105,v105\n"
MF "a[12:15],v[16:19],v[84:87],a[12:15]\n"
LD "v7,s[32:35],s72 offen offset:256 nt lds\n"
MF "a[16:19],v[16:19],v[88:91],a[16:19]\n"
"v_exp_f32 v106,v106\n"
MF "a[36:39],v[20:23],v[88:91],a[36:39]\n"
"s_waitcnt lgkmcnt(0)\n"
TR "v[16:17],v2 offset:1024\n"
TR "v[18:19],v2 offset:7168\n"
MF "a[56:59],v[24:27],v[88:91],a[56:59]\n"
"s_nop 0\n"
TR "v[20:21],v3 offset:1024\n"
TR "v[22:23],v3 offset:7168\n"
MF "a[76:79],v[28:31],v[88:91],a[76:79]\n"
"v_exp_f32 v107,v107\n"
MF "a[72:75],v[28:31],v[84:87],a[72:75]\n"
"v_add_f32 v60,v55,v104\n"
"v_add_f32 v60,v60,v105\n"
MF "a[52:55],v[24:27],v[84:87],a[52:55]\n"
CV "v104,v104,v105\n"
"v_add_f32 v60,v60,v106\n"
MF "a[48:51],v[24:27],v[80:83],a[48:51]\n"
"v_add_f32 v60,v60,v107\n"
CV "v105,v106,v107\n"
MF "a[68:71],v[28:31],v[80:83],a[68:71]\n"
"v_exp_f32 v108,v108\n"
MF "a[64:67],v[28:31],v[76:79],a[64:67]\n"
"v_exp_f32 v109,v109\n"
MF "a[44:47],v[24:27],v[76:79],a[44:47]\n"
"v_exp_f32 v110,v110\n"
MF "a[40:43],v[24:27],v[72:75],a[40:43]\n"
"v_exp_f32 v111,v111\n"
MF "a[60:63],v[28:31],v[72:75],a[60:63]\n"
"s_waitcnt lgkmcnt(0)\n"
TR "v[24:25],v4 offset:1024\n"
TR "v[26:27],v4 offset:7168\n"
MF "a[80:83],v[16:19],v[72:75],a[80:83]\n"
"s_nop 0\n"
TR "v[28:29],v5 offset:1024\n"
TR "v[30:31],v5 offset:7168\n"
MF "a[100:103],v[20:23],v[72:75],a[100:103]\n"
"v_add_f32 v61,v56,v108\n"
"v_add_f32 v61,v61,v109\n"
MF "a[104:107],v[20:23],v[76:79],a[104:107]\n"
CV "v108,v108,v109\n"
"v_add_f32 v61,v61,v110\n"
MF "a[84:87],v[16:19],v[76:79],a[84:87]\n"
"v_add_f32 v61,v61,v111\n"
CV "v109,v110,v111\n"
MF "a[88:91],v[16:19],v[80:83],a[88:91]\n"
"v_exp_f32 v112,v112\n"
MF "a[108:111],v[20:23],v[80:83],a[108:111]\n"
"v_exp_f32 v113,v113\n"
MF "a[112:115],v[20:23],v[84:87],a[112:115]\n"
"v_exp_f32 v114,v114\n"
MF "a[92:95],v[16:19],v[84:87],a[92:95]\n"
"v_exp_f32 v115,v115\n"
MF "a[96:99],v[16:19],v[88:91],a[96:99]\n"
"v_add_f32 v57,v57,v112\n"
"v_add_f32 v57,v57,v113\n"
MF "a[116:119],v[20:23],v[88:91],a[116:119]\n"
"s_waitcnt lgkmcnt(0)\n"
TR "v[16:17],v2 offset:2048\n"
TR "v[18:19],v2 offset:8192\n"
MF "a[136:139],v[24:27],v[88:91],a[136:139]\n"
"s_nop 0\n"
TR "v[20:21],v3 offset:2048\n"
TR "v[22:23],v3 offset:8192\n"
MF "a[156:159],v[28:31],v[88:91],a[156:159]\n"
CV "v94,v112,v113\n"
"v_add_f32 v57,v57,v114\n"
MF "a[152:155],v[28:31],v[84:87],a[152:155]\n"
"v_add_f32 v57,v57,v115\n"
CV "v95,v114,v115\n"
MF "a[132:135],v[24:27],v[84:87],a[132:135]\n"
"v_exp_f32 v116,v116\n"
MF "a[128:131],v[24:27],v[80:83],a[128:131]\n"
"v_exp_f32 v117,v117\n"
MF "a[148:151],v[28:31],v[80:83],a[148:151]\n"
"v_exp_f32 v118,v118\n"
MF "a[144:147],v[28:31],v[76:79],a[144:147]\n"
"v_exp_f32 v119,v119\n"
MF "a[124:127],v[24:27],v[76:79],a[124:127]\n"
"v_add_f32 v58,v58,v116\n"
"v_add_f32 v58,v58,v117\n"
MF "a[120:123],v[24:27],v[72:75],a[120:123]\n"
CV "v98,v116,v117\n"
"v_add_f32 v58,v58,v118\n"
MF "a[140:143],v[28:31],v[72:75],a[140:143]\n"
"s_waitcnt lgkmcnt(0)\n"
TR "v[24:25],v4 offset:2048\n"
TR "v[26:27],v4 offset:8192\n"
MF "a[160:163],v[16:19],v[72:75],a[160:163]\n"
"s_nop 0\n"
TR "v[28:29],v5 offset:2048\n"
TR "v[30:31],v5 offset:8192\n"
MF "a[180:183],v[20:23],v[72:75],a[180:183]\n"
"v_add_f32 v58,v58,v119\n"
CV "v99,v118,v119\n"
MF "a[184:187],v[20:23],v[76:79],a[184:187]\n"
"v_exp_f32 v120,v120\n"
MF "a[164:167],v[16:19],v[76:79],a[164:167]\n"
"v_exp_f32 v121,v121\n"
MF "a[168:171],v[16:19],v[80:83],a[168:171]\n"
"v_exp_f32 v122,v122\n"
MF "a[188:191],v[20:23],v[80:83],a[188:191]\n"
"v_exp_f32 v123,v123\n"
MF "a[192:195],v[20:23],v[84:87],a[192:195]\n"
"v_add_f32 v59,v59,v120\n"
"v_add_f32 v59,v59,v121\n"
MF "a[172:175],v[16:19],v[84:87],a[172:175]\n"
"s_waitcnt lgkmcnt(0)\n"
CV "v102,v120,v121\n"
"v_add_f32 v59,v59,v122\n"
MF "a[176:179],v[16:19],v[88:91],a[176:179]\n"
"s_barrier\n"
MF "a[196:199],v[20:23],v[88:91],a[196:199]\n"
"s_sub_u32 s58,s71,s70\n"
"s_add_u32 s73,s73,s58\n"
"v_add_f32 v59,v59,v123\n"
MF "a[216:219],v[24:27],v[88:91],a[216:219]\n"
"s_add_u32 s18,s18,s58\n"
"s_add_u32 s19,s19,s58\n"
"s_add_u32 s73,s73,12288\n"
MF "a[236:239],v[28:31],v[88:91],a[236:239]\n"
"s_cmp_eq_u32 s73,s19\n"
"s_cselect_b32 s73,s18,s73\n"
"s_add_u32 m0,s65,49152\n"
MF "a[232:235],v[28:31],v[84:87],a[232:235]\n"
LD "v8,s[36:39],s73 offen nt lds\n"
MF "a[212:215],v[24:27],v[84:87],a[212:215]\n"
"s_add_u32 m0,s65,50048\n"
CV "v103,v122,v123\n"
MF "a[208:211],v[24:27],v[80:83],a[208:211]\n"
LD "v8,s[36:39],s73 offen offset:128 nt lds\n"
MF "a[228:231],v[28:31],v[80:83],a[228:231]\n"
"s_add_u32 m0,s65,50944\n"
"v_exp_f32 v124,v124\n"
MF "a[224:227],v[28:31],v[76:79],a[224:227]\n"
LD "v8,s[36:39],s73 offen offset:256 nt lds\n"
MF "a[204:207],v[24:27],v[76:79],a[204:207]\n"
"v_exp_f32 v125,v125\n"
MF "a[200:203],v[24:27],v[72:75],a[200:203]\n"
"v_exp_f32 v126,v126\n"
MF "a[220:223],v[28:31],v[72:75],a[220:223]\n"
"v_exp_f32 v127,v127\n"
"v_add_f32 v60,v60,v124\n"
"v_add_f32 v60,v60,v125\n"
CV "v106,v124,v125\n"
"v_add_f32 v60,v60,v126\n"
"v_add_f32 v60,v60,v127\n"
CV "v107,v126,v127\n"
"v_exp_f32 v128,v128\n"
"v_exp_f32 v129,v129\n"
"v_exp_f32 v130,v130\n"
"v_exp_f32 v131,v131\n"
"v_add_f32 v61,v61,v128\n"
"v_add_f32 v61,v61,v129\n"
CV "v110,v128,v129\n"
"v_add_f32 v61,v61,v130\n"
"v_add_f32_e64 v61,v61,v131\n"
CV "v111,v130,v131\n"
"v_max3_f32 v10,v57,v58,v59\n"
"v_max3_f32 v10,v10,v60,v61\n"
"v_cmp_le_f32 vcc,0x47800000,v10\n"
"s_cbranch_vccnz .Lstub_13\n"
".Lback_13:\n"
"s_waitcnt vmcnt(9) lgkmcnt(0)\n"
"s_barrier\n"
"s_lshl_b32 s58,s56,10\n"
"s_add_u32 s58,s58,73728\n"
"v_add_u32 v120,s58,v9\n"
"v_add_u32 v121,65536,v120\n"
"s_sub_u32 s59,s71,s70\n"
"v_mov_b32 v10,1.0\n"
TR "v[136:137],v2 offset:24576\n"
TR "v[138:139],v2 offset:30720\n"
"v_mfma_f32_16x16x4_f32 v[16:19],v10,v57,0\n"
TR "v[140:141],v3 offset:24576\n"
TR "v[142:143],v3 offset:30720\n"
TR "v[144:145],v4 offset:24576\n"
TR "v[146:147],v4 offset:30720\n"
TR "v[148:149],v5 offset:24576\n"
TR "v[150:151],v5 offset:30720\n"
"v_mfma_f32_16x16x4_f32 v[20:23],v10,v58,0\n"
TR "v[152:153],v2 offset:25600\n"
TR "v[154:155],v2 offset:31744\n"
TR "v[156:157],v3 offset:25600\n"
TR "v[158:159],v3 offset:31744\n"
TR "v[160:161],v4 offset:25600\n"
TR "v[162:163],v4 offset:31744\n"
"v_mfma_f32_16x16x4_f32 v[24:27],v10,v59,0\n"
TR "v[164:165],v5 offset:25600\n"
TR "v[166:167],v5 offset:31744\n"
TR "v[168:169],v2 offset:26624\n"
TR "v[170:171],v2 offset:32768\n"
TR "v[172:173],v3 offset:26624\n"
TR "v[174:175],v3 offset:32768\n"
"v_mfma_f32_16x16x4_f32 v[28:31],v10,v60,0\n"
TR "v[176:177],v4 offset:26624\n"
TR "v[178:179],v4 offset:32768\n"
TR "v[180:181],v5 offset:26624\n"
TR "v[182:183],v5 offset:32768\n"
"v_mfma_f32_16x16x4_f32 v[32:35],v10,v61,0\n"
"s_add_u32 m0,s65,49152\n"
"s_nop 0\n"
LD "v8,s[36:39],s73 offen offset:3072 nt lds\n"
"s_add_u32 m0,s65,50048\n"
"s_nop 0\n"
LD "v8,s[36:39],s73 offen offset:3200 nt lds\n"
"s_waitcnt lgkmcnt(15)\n"
MF "a[0:3],v[136:139],v[92:95],a[0:3]\n"
MF "a[20:23],v[140:143],v[92:95],a[20:23]\n"
"s_add_u32 m0,s65,50944\n"
"s_nop 0\n"
LD "v8,s[36:39],s73 offen offset:3328 nt lds\n"
"s_sub_u32 s74,s96,s16\n"
"s_add_u32 s74,s74,s71\n"
"s_add_u32 s20,s20,s59\n"
"s_mov_b32 s70,s71\n"
LD "v[232:235],v9,s[40:43],s75 offen sc1\n"
"v_rcp_f32 v57,v16\n"
"v_rcp_f32 v58,v20\n"
"v_rcp_f32 v59,v24\n"
"v_rcp_f32 v60,v28\n"
"v_rcp_f32 v61,v32\n"
MF "a[4:7],v[136:139],v[96:99],a[4:7]\n"
"v_mul_f32_e64 v52,-s13,v57\n"
"v_mul_f32_e64 v53,-s13,v58\n"
"v_mul_f32_e64 v54,-s13,v59\n"
"v_mul_f32_e64 v55,-s13,v60\n"
"v_mul_f32_e64 v56,-s13,v61\n"
AR "v72,a0\n"
AR "v73,a1\n"
AR "v74,a2\n"
MF "a[24:27],v[140:143],v[96:99],a[24:27]\n"
AR "v75,a3\n"
AR "v76,a20\n"
AR "v77,a21\n"
AR "v78,a22\n"
AR "v79,a23\n"
"v_pk_fma_f32 v[72:73],v[72:73],v[56:57],v[52:53] op_sel:[0,1,0] op_sel_hi:[1,1,0]\n"
"v_pk_fma_f32 v[74:75],v[74:75],v[56:57],v[52:53] op_sel:[0,1,0] op_sel_hi:[1,1,0]\n"
"v_pk_fma_f32 v[76:77],v[76:77],v[56:57],v[52:53] op_sel:[0,1,0] op_sel_hi:[1,1,0]\n"
"v_pk_fma_f32 v[78:79],v[78:79],v[56:57],v[52:53] op_sel:[0,1,0] op_sel_hi:[1,1,0]\n"
CV "v112,v72,v73\n"
CV "v113,v74,v75\n"
CV "v114,v76,v77\n"
CV "v115,v78,v79\n"
MF "a[8:11],v[136:139],v[100:103],a[8:11]\n"
"ds_read_b128 v[184:187],v120 offset:24576\n"
"ds_write_b128 v120,v[112:115] offset:24576\n"
LD "v[236:239],v9,s[40:43],s75 offen offset:1024 sc1\n"
AR "v80,a4\n"
AR "v81,a5\n"
AR "v82,a6\n"
MF "a[28:31],v[140:143],v[100:103],a[28:31]\n"
AR "v83,a7\n"
AR "v84,a24\n"
AR "v85,a25\n"
AR "v86,a26\n"
AR "v87,a27\n"
"v_pk_fma_f32 v[80:81],v[80:81],v[58:59],v[52:53] op_sel:[0,0,1] op_sel_hi:[1,0,1]\n"
"v_pk_fma_f32 v[82:83],v[82:83],v[58:59],v[52:53] op_sel:[0,0,1] op_sel_hi:[1,0,1]\n"
"v_pk_fma_f32 v[84:85],v[84:85],v[58:59],v[52:53] op_sel:[0,0,1] op_sel_hi:[1,0,1]\n"
"v_pk_fma_f32 v[86:87],v[86:87],v[58:59],v[52:53] op_sel:[0,0,1] op_sel_hi:[1,0,1]\n"
CV "v116,v80,v81\n"
CV "v117,v82,v83\n"
CV "v118,v84,v85\n"
CV "v119,v86,v87\n"
MF "a[12:15],v[136:139],v[104:107],a[12:15]\n"
"ds_read_b128 v[188:191],v120 offset:28672\n"
"ds_write_b128 v120,v[116:119] offset:28672\n"
AR "v72,a8\n"
AR "v73,a9\n"
AR "v74,a10\n"
MF "a[32:35],v[140:143],v[104:107],a[32:35]\n"
AR "v75,a11\n"
AR "v76,a28\n"
AR "v77,a29\n"
AR "v78,a30\n"
AR "v79,a31\n"
"v_pk_fma_f32 v[72:73],v[72:73],v[58:59],v[54:55] op_sel:[0,1,0] op_sel_hi:[1,1,0]\n"
"v_pk_fma_f32 v[74:75],v[74:75],v[58:59],v[54:55] op_sel:[0,1,0] op_sel_hi:[1,1,0]\n"
"v_pk_fma_f32 v[76:77],v[76:77],v[58:59],v[54:55] op_sel:[0,1,0] op_sel_hi:[1,1,0]\n"
"v_pk_fma_f32 v[78:79],v[78:79],v[58:59],v[54:55] op_sel:[0,1,0] op_sel_hi:[1,1,0]\n"
CV "v112,v72,v73\n"
CV "v113,v74,v75\n"
CV "v114,v76,v77\n"
CV "v115,v78,v79\n"
MF "a[16:19],v[136:139],v[108:111],a[16:19]\n"
"ds_read_b128 v[192:195],v120 offset:32768\n"
"ds_write_b128 v120,v[112:115] offset:32768\n"
"s_waitcnt lgkmcnt(0)\n"
"s_barrier\n"
LD "v[240:243],v9,s[40:43],s75 offen offset:2048 sc1\n"
AR "v80,a12\n"
AR "v81,a13\n"
AR "v82,a14\n"
MF "a[36:39],v[140:143],v[108:111],a[36:39]\n"
AR "v83,a15\n"
AR "v84,a32\n"
AR "v85,a33\n"
AR "v86,a34\n"
AR "v87,a35\n"
"v_pk_fma_f32 v[80:81],v[80:81],v[60:61],v[54:55] op_sel:[0,0,1] op_sel_hi:[1,0,1]\n"
"v_pk_fma_f32 v[82:83],v[82:83],v[60:61],v[54:55] op_sel:[0,0,1] op_sel_hi:[1,0,1]\n"
"v_pk_fma_f32 v[84:85],v[84:85],v[60:61],v[54:55] op_sel:[0,0,1] op_sel_hi:[1,0,1]\n"
"v_pk_fma_f32 v[86:87],v[86:87],v[60:61],v[54:55] op_sel:[0,0,1] op_sel_hi:[1,0,1]\n"
CV "v116,v80,v81\n"
CV "v117,v82,v83\n"
CV "v118,v84,v85\n"
CV "v119,v86,v87\n"
MF "a[40:43],v[144:147],v[92:95],a[40:43]\n"
"ds_read_b128 v[196:199],v120 offset:36864\n"
"ds_write_b128 v120,v[116:119] offset:36864\n"
LD "v[244:247],v9,s[40:43],s75 offen offset:3072 sc1\n"
AR "v72,a16\n"
AR "v73,a17\n"
AR "v74,a18\n"
MF "a[60:63],v[148:151],v[92:95],a[60:63]\n"
AR "v75,a19\n"
AR "v76,a36\n"
AR "v77,a37\n"
AR "v78,a38\n"
AR "v79,a39\n"
"v_pk_fma_f32 v[72:73],v[72:73],v[60:61],v[56:57] op_sel:[0,1,0] op_sel_hi:[1,1,0]\n"
"v_pk_fma_f32 v[74:75],v[74:75],v[60:61],v[56:57] op_sel:[0,1,0] op_sel_hi:[1,1,0]\n"
"v_pk_fma_f32 v[76:77],v[76:77],v[60:61],v[56:57] op_sel:[0,1,0] op_sel_hi:[1,1,0]\n"
"v_pk_fma_f32 v[78:79],v[78:79],v[60:61],v[56:57] op_sel:[0,1,0] op_sel_hi:[1,1,0]\n"
CV "v112,v72,v73\n"
CV "v113,v74,v75\n"
CV "v114,v76,v77\n"
CV "v115,v78,v79\n"
MF "a[44:47],v[144:147],v[96:99],a[44:47]\n"
"ds_read_b128 v[200:203],v120 offset:40960\n"
"ds_write_b128 v120,v[112:115] offset:40960\n"
AR "v80,a40\n"
AR "v81,a41\n"
AR "v82,a42\n"
MF "a[64:67],v[148:151],v[96:99],a[64:67]\n"
AR "v83,a43\n"
AR "v84,a60\n"
AR "v85,a61\n"
AR "v86,a62\n"
AR "v87,a63\n"
"v_pk_fma_f32 v[80:81],v[80:81],v[56:57],v[52:53] op_sel:[0,1,0] op_sel_hi:[1,1,0]\n"
"v_pk_fma_f32 v[82:83],v[82:83],v[56:57],v[52:53] op_sel:[0,1,0] op_sel_hi:[1,1,0]\n"
"v_pk_fma_f32 v[84:85],v[84:85],v[56:57],v[52:53] op_sel:[0,1,0] op_sel_hi:[1,1,0]\n"
"v_pk_fma_f32 v[86:87],v[86:87],v[56:57],v[52:53] op_sel:[0,1,0] op_sel_hi:[1,1,0]\n"
CV "v116,v80,v81\n"
CV "v117,v82,v83\n"
CV "v118,v84,v85\n"
CV "v119,v86,v87\n"
MF "a[48:51],v[144:147],v[100:103],a[48:51]\n"
"ds_read_b128 v[204:207],v120 offset:45056\n"
"ds_write_b128 v120,v[116:119] offset:45056\n"
"s_add_u32 s58,s75,4096\n"
LD "v[248:251],v9,s[40:43],s58 offen sc1\n"
AR "v72,a44\n"
AR "v73,a45\n"
AR "v74,a46\n"
MF "a[68:71],v[148:151],v[100:103],a[68:71]\n"
AR "v75,a47\n"
AR "v76,a64\n"
AR "v77,a65\n"
AR "v78,a66\n"
AR "v79,a67\n"
"v_pk_fma_f32 v[72:73],v[72:73],v[58:59],v[52:53] op_sel:[0,0,1] op_sel_hi:[1,0,1]\n"
"v_pk_fma_f32 v[74:75],v[74:75],v[58:59],v[52:53] op_sel:[0,0,1] op_sel_hi:[1,0,1]\n"
"v_pk_fma_f32 v[76:77],v[76:77],v[58:59],v[52:53] op_sel:[0,0,1] op_sel_hi:[1,0,1]\n"
"v_pk_fma_f32 v[78:79],v[78:79],v[58:59],v[52:53] op_sel:[0,0,1] op_sel_hi:[1,0,1]\n"
CV "v112,v72,v73\n"
CV "v113,v74,v75\n"
CV "v114,v76,v77\n"
CV "v115,v78,v79\n"
MF "a[52:55],v[144:147],v[104:107],a[52:55]\n"
"ds_read_b128 v[208:211],v120 offset:49152\n"
"ds_write_b128 v120,v[112:115] offset:49152\n"
LD "v[252:255],v9,s[40:43],s58 offen offset:1024 sc1\n"
AR "v80,a48\n"
AR "v81,a49\n"
AR "v82,a50\n"
MF "a[72:75],v[148:151],v[104:107],a[72:75]\n"
AR "v83,a51\n"
AR "v84,a68\n"
AR "v85,a69\n"
AR "v86,a70\n"
AR "v87,a71\n"
"v_pk_fma_f32 v[80:81],v[80:81],v[58:59],v[54:55] op_sel:[0,1,0] op_sel_hi:[1,1,0]\n"
"v_pk_fma_f32 v[82:83],v[82:83],v[58:59],v[54:55] op_sel:[0,1,0] op_sel_hi:[1,1,0]\n"
"v_pk_fma_f32 v[84:85],v[84:85],v[58:59],v[54:55] op_sel:[0,1,0] op_sel_hi:[1,1,0]\n"
"v_pk_fma_f32 v[86:87],v[86:87],v[58:59],v[54:55] op_sel:[0,1,0] op_sel_hi:[1,1,0]\n"
CV "v116,v80,v81\n"
CV "v117,v82,v83\n"
CV "v118,v84,v85\n"
CV "v119,v86,v87\n"
MF "a[56:59],v[144:147],v[108:111],a[56:59]\n"
"ds_read_b128 v[212:215],v120 offset:53248\n"
"ds_write_b128 v120,v[116:119] offset:53248\n"
AR "v72,a52\n"
AR "v73,a53\n"
AR "v74,a54\n"
MF "a[76:79],v[148:151],v[108:111],a[76:79]\n"
AR "v75,a55\n"
AR "v76,a72\n"
AR "v77,a73\n"
AR "v78,a74\n"
AR "v79,a75\n"
"v_pk_fma_f32 v[72:73],v[72:73],v[60:61],v[54:55] op_sel:[0,0,1] op_sel_hi:[1,0,1]\n"
"v_pk_fma_f32 v[74:75],v[74:75],v[60:61],v[54:55] op_sel:[0,0,1] op_sel_hi:[1,0,1]\n"
"v_pk_fma_f32 v[76:77],v[76:77],v[60:61],v[54:55] op_sel:[0,0,1] op_sel_hi:[1,0,1]\n"
"v_pk_fma_f32 v[78:79],v[78:79],v[60:61],v[54:55] op_sel:[0,0,1] op_sel_hi:[1,0,1]\n"
CV "v112,v72,v73\n"
CV "v113,v74,v75\n"
CV "v114,v76,v77\n"
CV "v115,v78,v79\n"
MF "a[80:83],v[152:155],v[92:95],a[80:83]\n"
"ds_read_b128 v[216:219],v120 offset:57344\n"
"ds_write_b128 v120,v[112:115] offset:57344\n"
AR "v80,a56\n"
AR "v81,a57\n"
AR "v82,a58\n"
MF "a[100:103],v[156:159],v[92:95],a[100:103]\n"
AR "v83,a59\n"
AR "v84,a76\n"
AR "v85,a77\n"
AR "v86,a78\n"
AR "v87,a79\n"
"v_pk_fma_f32 v[80:81],v[80:81],v[60:61],v[56:57] op_sel:[0,1,0] op_sel_hi:[1,1,0]\n"
"v_pk_fma_f32 v[82:83],v[82:83],v[60:61],v[56:57] op_sel:[0,1,0] op_sel_hi:[1,1,0]\n"
"v_pk_fma_f32 v[84:85],v[84:85],v[60:61],v[56:57] op_sel:[0,1,0] op_sel_hi:[1,1,0]\n"
"v_pk_fma_f32 v[86:87],v[86:87],v[60:61],v[56:57] op_sel:[0,1,0] op_sel_hi:[1,1,0]\n"
CV "v116,v80,v81\n"
CV "v117,v82,v83\n"
CV "v118,v84,v85\n"
CV "v119,v86,v87\n"
MF "a[84:87],v[152:155],v[96:99],a[84:87]\n"
"ds_read_b128 v[220:223],v120 offset:61440\n"
"ds_write_b128 v120,v[116:119] offset:61440\n"
AR "v72,a80\n"
AR "v73,a81\n"
AR "v74,a82\n"
MF "a[104:107],v[156:159],v[96:99],a[104:107]\n"
AR "v75,a83\n"
AR "v76,a100\n"
AR "v77,a101\n"
AR "v78,a102\n"
AR "v79,a103\n"
"v_pk_fma_f32 v[72:73],v[72:73],v[56:57],v[52:53] op_sel:[0,1,0] op_sel_hi:[1,1,0]\n"
"v_pk_fma_f32 v[74:75],v[74:75],v[56:57],v[52:53] op_sel:[0,1,0] op_sel_hi:[1,1,0]\n"
"v_pk_fma_f32 v[76:77],v[76:77],v[56:57],v[52:53] op_sel:[0,1,0] op_sel_hi:[1,1,0]\n"
"v_pk_fma_f32 v[78:79],v[78:79],v[56:57],v[52:53] op_sel:[0,1,0] op_sel_hi:[1,1,0]\n"
CV "v112,v72,v73\n"
CV "v113,v74,v75\n"
CV "v114,v76,v77\n"
CV "v115,v78,v79\n"
MF "a[88:91],v[152:155],v[100:103],a[88:91]\n"
"ds_read_b128 v[224:227],v121\n"
"ds_write_b128 v121,v[112:115]\n"
AR "v80,a84\n"
AR "v81,a85\n"
AR "v82,a86\n"
MF "a[108:111],v[156:159],v[100:103],a[108:111]\n"
AR "v83,a87\n"
AR "v84,a104\n"
AR "v85,a105\n"
".Lpg17g:\n"
"s_cbranch_execz .Lisr6\n"
"s_nop 0\n"
AR "v86,a106\n"
AR "v87,a107\n"
"v_pk_fma_f32 v[80:81],v[80:81],v[58:59],v[52:53] op_sel:[0,0,1] op_sel_hi:[1,0,1]\n"
"v_pk_fma_f32 v[82:83],v[82:83],v[58:59],v[52:53] op_sel:[0,0,1] op_sel_hi:[1,0,1]\n"
"v_pk_fma_f32 v[84:85],v[84:85],v[58:59],v[52:53] op_sel:[0,0,1] op_sel_hi:[1,0,1]\n"
"v_pk_fma_f32 v[86:87],v[86:87],v[58:59],v[52:53] op_sel:[0,0,1] op_sel_hi:[1,0,1]\n"
CV "v116,v80,v81\n"
CV "v117,v82,v83\n"
CV "v118,v84,v85\n"
CV "v119,v86,v87\n"
MF "a[92:95],v[152:155],v[104:107],a[92:95]\n"
"ds_read_b128 v[228:231],v121 offset:4096\n"
"ds_write_b128 v121,v[116:119] offset:4096\n"
AR "v72,a88\n"
AR "v73,a89\n"
AR "v74,a90\n"
MF "a[112:115],v[156:159],v[104:107],a[112:115]\n"
AR "v75,a91\n"
AR "v76,a108\n"
AR "v77,a109\n"
AR "v78,a110\n"
AR "v79,a111\n"
"v_pk_fma_f32 v[72:73],v[72:73],v[58:59],v[54:55] op_sel:[0,1,0] op_sel_hi:[1,1,0]\n"
"v_pk_fma_f32 v[74:75],v[74:75],v[58:59],v[54:55] op_sel:[0,1,0] op_sel_hi:[1,1,0]\n"
"v_pk_fma_f32 v[76:77],v[76:77],v[58:59],v[54:55] op_sel:[0,1,0] op_sel_hi:[1,1,0]\n"
"v_pk_fma_f32 v[78:79],v[78:79],v[58:59],v[54:55] op_sel:[0,1,0] op_sel_hi:[1,1,0]\n"
CV "v112,v72,v73\n"
CV "v113,v74,v75\n"
CV "v114,v76,v77\n"
CV "v115,v78,v79\n"
MF "a[96:99],v[152:155],v[108:111],a[96:99]\n"
"ds_read_b128 v[136:139],v121 offset:8192\n"
"ds_write_b128 v121,v[112:115] offset:8192\n"
AR "v80,a92\n"
AR "v81,a93\n"
AR "v82,a94\n"
MF "a[116:119],v[156:159],v[108:111],a[116:119]\n"
AR "v83,a95\n"
AR "v84,a112\n"
AR "v85,a113\n"
AR "v86,a114\n"
AR "v87,a115\n"
"v_pk_fma_f32 v[80:81],v[80:81],v[60:61],v[54:55] op_sel:[0,0,1] op_sel_hi:[1,0,1]\n"
"v_pk_fma_f32 v[82:83],v[82:83],v[60:61],v[54:55] op_sel:[0,0,1] op_sel_hi:[1,0,1]\n"
"v_pk_fma_f32 v[84:85],v[84:85],v[60:61],v[54:55] op_sel:[0,0,1] op_sel_hi:[1,0,1]\n"
"v_pk_fma_f32 v[86:87],v[86:87],v[60:61],v[54:55] op_sel:[0,0,1] op_sel_hi:[1,0,1]\n"
CV "v116,v80,v81\n"
CV "v117,v82,v83\n"
CV "v118,v84,v85\n"
CV "v119,v86,v87\n"
MF "a[120:123],v[160:163],v[92:95],a[120:123]\n"
"ds_read_b128 v[140:143],v121 offset:12288\n"
"ds_write_b128 v121,v[116:119] offset:12288\n"
AR "v72,a96\n"
AR "v73,a97\n"
AR "v74,a98\n"
MF "a[140:143],v[164:167],v[92:95],a[140:143]\n"
AR "v75,a99\n"
AR "v76,a116\n"
AR "v77,a117\n"
AR "v78,a118\n"
AR "v79,a119\n"
"v_pk_fma_f32 v[72:73],v[72:73],v[60:61],v[56:57] op_sel:[0,1,0] op_sel_hi:[1,1,0]\n"
"v_pk_fma_f32 v[74:75],v[74:75],v[60:61],v[56:57] op_sel:[0,1,0] op_sel_hi:[1,1,0]\n"
"v_pk_fma_f32 v[76:77],v[76:77],v[60:61],v[56:57] op_sel:[0,1,0] op_sel_hi:[1,1,0]\n"
"v_pk_fma_f32 v[78:79],v[78:79],v[60:61],v[56:57] op_sel:[0,1,0] op_sel_hi:[1,1,0]\n"
CV "v112,v72,v73\n"
CV "v113,v74,v75\n"
CV "v114,v76,v77\n"
CV "v115,v78,v79\n"
MF "a[124:127],v[160:163],v[96:99],a[124:127]\n"
"ds_read_b128 v[144:147],v121 offset:16384\n"
"ds_write_b128 v121,v[112:115] offset:16384\n"
AR "v80,a120\n"
AR "v81,a121\n"
AR "v82,a122\n"
MF "a[144:147],v[164:167],v[96:99],a[144:147]\n"
AR "v83,a123\n"
AR "v84,a140\n"
AR "v85,a141\n"
AR "v86,a142\n"
AR "v87,a143\n"
"v_pk_fma_f32 v[80:81],v[80:81],v[56:57],v[52:53] op_sel:[0,1,0] op_sel_hi:[1,1,0]\n"
"v_pk_fma_f32 v[82:83],v[82:83],v[56:57],v[52:53] op_sel:[0,1,0] op_sel_hi:[1,1,0]\n"
"v_pk_fma_f32 v[84:85],v[84:85],v[56:57],v[52:53] op_sel:[0,1,0] op_sel_hi:[1,1,0]\n"
"v_pk_fma_f32 v[86:87],v[86:87],v[56:57],v[52:53] op_sel:[0,1,0] op_sel_hi:[1,1,0]\n"
CV "v116,v80,v81\n"
CV "v117,v82,v83\n"
CV "v118,v84,v85\n"
CV "v119,v86,v87\n"
MF "a[128:131],v[160:163],v[100:103],a[128:131]\n"
"ds_read_b128 v[148:151],v121 offset:20480\n"
"ds_write_b128 v121,v[116:119] offset:20480\n"
AR "v72,a124\n"
AR "v73,a125\n"
AR "v74,a126\n"
MF "a[148:151],v[164:167],v[100:103],a[148:151]\n"
AR "v75,a127\n"
AR "v76,a144\n"
AR "v77,a145\n"
AR "v78,a146\n"
AR "v79,a147\n"
"v_pk_fma_f32 v[72:73],v[72:73],v[58:59],v[52:53] op_sel:[0,0,1] op_sel_hi:[1,0,1]\n"
"v_pk_fma_f32 v[74:75],v[74:75],v[58:59],v[52:53] op_sel:[0,0,1] op_sel_hi:[1,0,1]\n"
"v_pk_fma_f32 v[76:77],v[76:77],v[58:59],v[52:53] op_sel:[0,0,1] op_sel_hi:[1,0,1]\n"
"v_pk_fma_f32 v[78:79],v[78:79],v[58:59],v[52:53] op_sel:[0,0,1] op_sel_hi:[1,0,1]\n"
CV "v112,v72,v73\n"
CV "v113,v74,v75\n"
CV "v114,v76,v77\n"
CV "v115,v78,v79\n"
MF "a[132:135],v[160:163],v[104:107],a[132:135]\n"
"ds_write_b128 v120,v[112:115]\n"
AR "v80,a128\n"
AR "v81,a129\n"
AR "v82,a130\n"
MF "a[152:155],v[164:167],v[104:107],a[152:155]\n"
AR "v83,a131\n"
AR "v84,a148\n"
AR "v85,a149\n"
AR "v86,a150\n"
AR "v87,a151\n"
"v_pk_fma_f32 v[80:81],v[80:81],v[58:59],v[54:55] op_sel:[0,1,0] op_sel_hi:[1,1,0]\n"
"v_pk_fma_f32 v[82:83],v[82:83],v[58:59],v[54:55] op_sel:[0,1,0] op_sel_hi:[1,1,0]\n"
"v_pk_fma_f32 v[84:85],v[84:85],v[58:59],v[54:55] op_sel:[0,1,0] op_sel_hi:[1,1,0]\n"
"v_pk_fma_f32 v[86:87],v[86:87],v[58:59],v[54:55] op_sel:[0,1,0] op_sel_hi:[1,1,0]\n"
CV "v116,v80,v81\n"
CV "v117,v82,v83\n"
CV "v118,v84,v85\n"
CV "v119,v86,v87\n"
MF "a[136:139],v[160:163],v[108:111],a[136:139]\n"
"ds_write_b128 v120,v[116:119] offset:4096\n"
AR "v72,a132\n"
AR "v73,a133\n"
AR "v74,a134\n"
MF "a[156:159],v[164:167],v[108:111],a[156:159]\n"
AR "v75,a135\n"
AR "v76,a152\n"
AR "v77,a153\n"
AR "v78,a154\n"
AR "v79,a155\n"
"v_pk_fma_f32 v[72:73],v[72:73],v[60:61],v[54:55] op_sel:[0,0,1] op_sel_hi:[1,0,1]\n"
"v_pk_fma_f32 v[74:75],v[74:75],v[60:61],v[54:55] op_sel:[0,0,1] op_sel_hi:[1,0,1]\n"
"v_pk_fma_f32 v[76:77],v[76:77],v[60:61],v[54:55] op_sel:[0,0,1] op_sel_hi:[1,0,1]\n"
"v_pk_fma_f32 v[78:79],v[78:79],v[60:61],v[54:55] op_sel:[0,0,1] op_sel_hi:[1,0,1]\n"
CV "v112,v72,v73\n"
CV "v113,v74,v75\n"
CV "v114,v76,v77\n"
CV "v115,v78,v79\n"
MF "a[160:163],v[168:171],v[92:95],a[160:163]\n"
"ds_write_b128 v120,v[112:115] offset:8192\n"
AR "v80,a136\n"
AR "v81,a137\n"
AR "v82,a138\n"
MF "a[180:183],v[172:175],v[92:95],a[180:183]\n"
AR "v83,a139\n"
AR "v84,a156\n"
AR "v85,a157\n"
AR "v86,a158\n"
AR "v87,a159\n"
"v_pk_fma_f32 v[80:81],v[80:81],v[60:61],v[56:57] op_sel:[0,1,0] op_sel_hi:[1,1,0]\n"
"v_pk_fma_f32 v[82:83],v[82:83],v[60:61],v[56:57] op_sel:[0,1,0] op_sel_hi:[1,1,0]\n"
"v_pk_fma_f32 v[84:85],v[84:85],v[60:61],v[56:57] op_sel:[0,1,0] op_sel_hi:[1,1,0]\n"
"v_pk_fma_f32 v[86:87],v[86:87],v[60:61],v[56:57] op_sel:[0,1,0] op_sel_hi:[1,1,0]\n"
CV "v116,v80,v81\n"
CV "v117,v82,v83\n"
CV "v118,v84,v85\n"
CV "v119,v86,v87\n"
MF "a[164:167],v[168:171],v[96:99],a[164:167]\n"
"ds_write_b128 v120,v[116:119] offset:12288\n"
AR "v72,a160\n"
AR "v73,a161\n"
AR "v74,a162\n"
MF "a[184:187],v[172:175],v[96:99],a[184:187]\n"
AR "v75,a163\n"
AR "v76,a180\n"
AR "v77,a181\n"
AR "v78,a182\n"
AR "v79,a183\n"
"v_pk_fma_f32 v[72:73],v[72:73],v[56:57],v[52:53] op_sel:[0,1,0] op_sel_hi:[1,1,0]\n"
"v_pk_fma_f32 v[74:75],v[74:75],v[56:57],v[52:53] op_sel:[0,1,0] op_sel_hi:[1,1,0]\n"
"v_pk_fma_f32 v[76:77],v[76:77],v[56:57],v[52:53] op_sel:[0,1,0] op_sel_hi:[1,1,0]\n"
"v_pk_fma_f32 v[78:79],v[78:79],v[56:57],v[52:53] op_sel:[0,1,0] op_sel_hi:[1,1,0]\n"
CV "v112,v72,v73\n"
CV "v113,v74,v75\n"
CV "v114,v76,v77\n"
CV "v115,v78,v79\n"
MF "a[168:171],v[168:171],v[100:103],a[168:171]\n"
"ds_write_b128 v120,v[112:115] offset:16384\n"
AR "v80,a164\n"
AR "v81,a165\n"
AR "v82,a166\n"
MF "a[188:191],v[172:175],v[100:103],a[188:191]\n"
AR "v83,a167\n"
AR "v84,a184\n"
AR "v85,a185\n"
AR "v86,a186\n"
AR "v87,a187\n"
"v_pk_fma_f32 v[80:81],v[80:81],v[58:59],v[52:53] op_sel:[0,0,1] op_sel_hi:[1,0,1]\n"
"v_pk_fma_f32 v[82:83],v[82:83],v[58:59],v[52:53] op_sel:[0,0,1] op_sel_hi:[1,0,1]\n"
"v_pk_fma_f32 v[84:85],v[84:85],v[58:59],v[52:53] op_sel:[0,0,1] op_sel_hi:[1,0,1]\n"
"v_pk_fma_f32 v[86:87],v[86:87],v[58:59],v[52:53] op_sel:[0,0,1] op_sel_hi:[1,0,1]\n"
CV "v116,v80,v81\n"
CV "v117,v82,v83\n"
CV "v118,v84,v85\n"
CV "v119,v86,v87\n"
MF "a[172:175],v[168:171],v[104:107],a[172:175]\n"
"ds_write_b128 v120,v[116:119] offset:20480\n"
AR "v72,a168\n"
AR "v73,a169\n"
AR "v74,a170\n"
MF "a[192:195],v[172:175],v[104:107],a[192:195]\n"
AR "v75,a171\n"
AR "v76,a188\n"
AR "v77,a189\n"
AR "v78,a190\n"
AR "v79,a191\n"
"v_pk_fma_f32 v[72:73],v[72:73],v[58:59],v[54:55] op_sel:[0,1,0] op_sel_hi:[1,1,0]\n"
"v_pk_fma_f32 v[74:75],v[74:75],v[58:59],v[54:55] op_sel:[0,1,0] op_sel_hi:[1,1,0]\n"
"v_pk_fma_f32 v[76:77],v[76:77],v[58:59],v[54:55] op_sel:[0,1,0] op_sel_hi:[1,1,0]\n"
"v_pk_fma_f32 v[78:79],v[78:79],v[58:59],v[54:55] op_sel:[0,1,0] op_sel_hi:[1,1,0]\n"
CV "v112,v72,v73\n"
CV "v113,v74,v75\n"
CV "v114,v76,v77\n"
CV "v115,v78,v79\n"
MF "a[176:179],v[168:171],v[108:111],a[176:179]\n"
AR "v152,a240\n"
AW "a240,v112\n"
AR "v153,a241\n"
AW "a241,v113\n"
AR "v154,a242\n"
AW "a242,v114\n"
AR "v155,a243\n"
AW "a243,v115\n"
AR "v80,a172\n"
AR "v81,a173\n"
AR "v82,a174\n"
MF "a[196:199],v[172:175],v[108:111],a[196:199]\n"
AR "v83,a175\n"
AR "v84,a192\n"
AR "v85,a193\n"
AR "v86,a194\n"
AR "v87,a195\n"
"v_pk_fma_f32 v[80:81],v[80:81],v[60:61],v[54:55] op_sel:[0,0,1] op_sel_hi:[1,0,1]\n"
"v_pk_fma_f32 v[82:83],v[82:83],v[60:61],v[54:55] op_sel:[0,0,1] op_sel_hi:[1,0,1]\n"
"v_pk_fma_f32 v[84:85],v[84:85],v[60:61],v[54:55] op_sel:[0,0,1] op_sel_hi:[1,0,1]\n"
"v_pk_fma_f32 v[86:87],v[86:87],v[60:61],v[54:55] op_sel:[0,0,1] op_sel_hi:[1,0,1]\n"
CV "v116,v80,v81\n"
CV "v117,v82,v83\n"
CV "v118,v84,v85\n"
CV "v119,v86,v87\n"
MF "a[200:203],v[176:179],v[92:95],a[200:203]\n"
AR "v156,a244\n"
AW "a244,v116\n"
AR "v157,a245\n"
AW "a245,v117\n"
AR "v158,a246\n"
AW "a246,v118\n"
AR "v159,a247\n"
AW "a247,v119\n"
AR "v72,a176\n"
AR "v73,a177\n"
AR "v74,a178\n"
MF "a[220:223],v[180:183],v[92:95],a[220:223]\n"
AR "v75,a179\n"
AR "v76,a196\n"
AR "v77,a197\n"
AR "v78,a198\n"
AR "v79,a199\n"
"v_pk_fma_f32 v[72:73],v[72:73],v[60:61],v[56:57] op_sel:[0,1,0] op_sel_hi:[1,1,0]\n"
"v_pk_fma_f32 v[74:75],v[74:75],v[60:61],v[56:57] op_sel:[0,1,0] op_sel_hi:[1,1,0]\n"
"v_pk_fma_f32 v[76:77],v[76:77],v[60:61],v[56:57] op_sel:[0,1,0] op_sel_hi:[1,1,0]\n"
"v_pk_fma_f32 v[78:79],v[78:79],v[60:61],v[56:57] op_sel:[0,1,0] op_sel_hi:[1,1,0]\n"
CV "v112,v72,v73\n"
CV "v113,v74,v75\n"
CV "v114,v76,v77\n"
CV "v115,v78,v79\n"
MF "a[204:207],v[176:179],v[96:99],a[204:207]\n"
AR "v160,a248\n"
AW "a248,v112\n"
AR "v161,a249\n"
AW "a249,v113\n"
AR "v162,a250\n"
AW "a250,v114\n"
AR "v163,a251\n"
AW "a251,v115\n"
AR "v80,a200\n"
AR "v81,a201\n"
AR "v82,a202\n"
MF "a[224:227],v[180:183],v[96:99],a[224:227]\n"
AR "v83,a203\n"
AR "v84,a220\n"
AR "v85,a221\n"
AR "v86,a222\n"
AR "v87,a223\n"
"v_pk_fma_f32 v[80:81],v[80:81],v[56:57],v[52:53] op_sel:[0,1,0] op_sel_hi:[1,1,0]\n"
"v_pk_fma_f32 v[82:83],v[82:83],v[56:57],v[52:53] op_sel:[0,1,0] op_sel_hi:[1,1,0]\n"
"v_pk_fma_f32 v[84:85],v[84:85],v[56:57],v[52:53] op_sel:[0,1,0] op_sel_hi:[1,1,0]\n"
"v_pk_fma_f32 v[86:87],v[86:87],v[56:57],v[52:53] op_sel:[0,1,0] op_sel_hi:[1,1,0]\n"
CV "v116,v80,v81\n"
CV "v117,v82,v83\n"
CV "v118,v84,v85\n"
CV "v119,v86,v87\n"
MF "a[208:211],v[176:179],v[100:103],a[208:211]\n"
"v_mov_b32 v164,v62\n"
"v_mov_b32 v62,v116\n"
"v_mov_b32 v165,v63\n"
"v_mov_b32 v63,v117\n"
"v_mov_b32 v166,v64\n"
"v_mov_b32 v64,v118\n"
"v_mov_b32 v167,v65\n"
"v_mov_b32 v65,v119\n"
AR "v72,a204\n"
AR "v73,a205\n"
AR "v74,a206\n"
MF "a[228:231],v[180:183],v[100:103],a[228:231]\n"
AR "v75,a207\n"
AR "v76,a224\n"
AR "v77,a225\n"
AR "v78,a226\n"
AR "v79,a227\n"
"v_pk_fma_f32 v[72:73],v[72:73],v[58:59],v[52:53] op_sel:[0,0,1] op_sel_hi:[1,0,1]\n"
"v_pk_fma_f32 v[74:75],v[74:75],v[58:59],v[52:53] op_sel:[0,0,1] op_sel_hi:[1,0,1]\n"
"v_pk_fma_f32 v[76:77],v[76:77],v[58:59],v[52:53] op_sel:[0,0,1] op_sel_hi:[1,0,1]\n"
"v_pk_fma_f32 v[78:79],v[78:79],v[58:59],v[52:53] op_sel:[0,0,1] op_sel_hi:[1,0,1]\n"
CV "v112,v72,v73\n"
CV "v113,v74,v75\n"
CV "v114,v76,v77\n"
CV "v115,v78,v79\n"
MF "a[212:215],v[176:179],v[104:107],a[212:215]\n"
"v_mov_b32 v168,v66\n"
"v_mov_b32 v66,v112\n"
"v_mov_b32 v169,v67\n"
"v_mov_b32 v67,v113\n"
"v_mov_b32 v170,v68\n"
"v_mov_b32 v68,v114\n"
"v_mov_b32 v171,v69\n"
"v_mov_b32 v69,v115\n"
AR "v80,a208\n"
AR "v81,a209\n"
AR "v82,a210\n"
MF "a[232:235],v[180:183],v[104:107],a[232:235]\n"
AR "v83,a211\n"
AR "v84,a228\n"
AR "v85,a229\n"
AR "v86,a230\n"
AR "v87,a231\n"
"v_pk_fma_f32 v[80:81],v[80:81],v[58:59],v[54:55] op_sel:[0,1,0] op_sel_hi:[1,1,0]\n"
"v_pk_fma_f32 v[82:83],v[82:83],v[58:59],v[54:55] op_sel:[0,1,0] op_sel_hi:[1,1,0]\n"
"v_pk_fma_f32 v[84:85],v[84:85],v[58:59],v[54:55] op_sel:[0,1,0] op_sel_hi:[1,1,0]\n"
"v_pk_fma_f32 v[86:87],v[86:87],v[58:59],v[54:55] op_sel:[0,1,0] op_sel_hi:[1,1,0]\n"
CV "v116,v80,v81\n"
CV "v117,v82,v83\n"
CV "v118,v84,v85\n"
CV "v119,v86,v87\n"
MF "a[216:219],v[176:179],v[108:111],a[216:219]\n"
"v_mov_b32 v172,v70\n"
"v_mov_b32 v70,v116\n"
"v_mov_b32 v173,v71\n"
"v_mov_b32 v71,v117\n"
"v_mov_b32 v174,v132\n"
"v_mov_b32 v132,v118\n"
"v_mov_b32 v175,v133\n"
"v_mov_b32 v133,v119\n"
AR "v72,a212\n"
AR "v73,a213\n"
AR "v74,a214\n"
MF "a[236:239],v[180:183],v[108:111],a[236:239]\n"
AR "v75,a215\n"
AR "v76,a232\n"
AR "v77,a233\n"
AR "v78,a234\n"
AR "v79,a235\n"
"v_pk_fma_f32 v[72:73],v[72:73],v[60:61],v[54:55] op_sel:[0,0,1] op_sel_hi:[1,0,1]\n"
"v_pk_fma_f32 v[74:75],v[74:75],v[60:61],v[54:55] op_sel:[0,0,1] op_sel_hi:[1,0,1]\n"
"v_pk_fma_f32 v[76:77],v[76:77],v[60:61],v[54:55] op_sel:[0,0,1] op_sel_hi:[1,0,1]\n"
"v_pk_fma_f32 v[78:79],v[78:79],v[60:61],v[54:55] op_sel:[0,0,1] op_sel_hi:[1,0,1]\n"
CV "v112,v72,v73\n"
CV "v113,v74,v75\n"
CV "v114,v76,v77\n"
CV "v115,v78,v79\n"
"s_waitcnt vmcnt(0) lgkmcnt(0)\n"
"s_barrier\n"
"ds_read_b128 v[92:95],v0\n"
"ds_read_b128 v[96:99],v1\n"
"ds_read_b128 v[100:103],v0 offset:1024\n"
"v_mov_b32 v176,v134\n"
"v_mov_b32 v134,v112\n"
"v_mov_b32 v177,v135\n"
"v_mov_b32 v135,v113\n"
"v_mov_b32 v178,v6\n"
"v_mov_b32 v6,v114\n"
"v_mov_b32 v179,v13\n"
"v_mov_b32 v13,v115\n"
AR "v80,a216\n"
AR "v81,a217\n"
AR "v82,a218\n"
AR "v83,a219\n"
AR "v84,a236\n"
AR "v85,a237\n"
AR "v86,a238\n"
AR "v87,a239\n"
"v_pk_fma_f32 v[80:81],v[80:81],v[60:61],v[56:57] op_sel:[0,1,0] op_sel_hi:[1,1,0]\n"
"v_pk_fma_f32 v[82:83],v[82:83],v[60:61],v[56:57] op_sel:[0,1,0] op_sel_hi:[1,1,0]\n"
"v_pk_fma_f32 v[84:85],v[84:85],v[60:61],v[56:57] op_sel:[0,1,0] op_sel_hi:[1,1,0]\n"
"v_pk_fma_f32 v[86:87],v[86:87],v[60:61],v[56:57] op_sel:[0,1,0] op_sel_hi:[1,1,0]\n"
CV "v116,v80,v81\n"
CV "v117,v82,v83\n"
CV "v118,v84,v85\n"
CV "v119,v86,v87\n"
"v_mov_b32 v180,v14\n"
"v_mov_b32 v14,v116\n"
"v_mov_b32 v181,v15\n"
"v_mov_b32 v15,v117\n"
AR "v182,a254\n"
AW "a254,v118\n"
AR "v183,a255\n"
AW "a255,v119\n"
"s_mov_b32 s13,s11\n"
"v_subrev_u32 v2,49152,v2\n"
"v_subrev_u32 v3,49152,v3\n"
"v_subrev_u32 v4,49152,v4\n"
"v_subrev_u32 v5,49152,v5\n"
"s_mov_b32 s57,2\n"
"s_mov_b32 s69,1\n"
"s_mov_b32 s61,2\n"
"s_getreg_b32 s58,hwreg(HW_REG_XCC_ID,0,1)\n"
"s_mov_b32 s97,170\n"
"s_mul_i32 s95,s58,0x47800000\n"
"s_mov_b32 s77,s95\n"
"s_branch .Latt2\n"
".LpgattB_f:\n"
"s_cbranch_execz .Lisr3\n"
".LattB:\n"
AR "v2,a245\n"
"v_xor_b32 v3,32,v2\n"
"v_xor_b32 v4,64,v2\n"
"v_xor_b32 v5,96,v2\n"
"s_mov_b32 s61,2\n"
".Latt2:\n"
"s_cmp_eq_u32 s57,1\n"
"s_cbranch_scc1 .Lt0k\n"
".Lt0n:\n"
"s_waitcnt lgkmcnt(2)\n"
MF "v[72:75],v[92:95],v[136:139],0\n"
"ds_read_b128 v[104:107],v1 offset:1024\n"
MF "v[76:79],v[92:95],v[140:143],0\n"
MF "v[80:83],v[92:95],v[144:147],0\n"
MF "v[84:87],v[92:95],v[148:151],0\n"
MF "v[88:91],v[92:95],v[152:155],0\n"
"s_waitcnt lgkmcnt(2)\n"
"ds_read_b128 v[92:95],v0 offset:2048\n"
MF "v[72:75],v[96:99],v[156:159],v[72:75]\n"
MF "v[76:79],v[96:99],v[160:163],v[76:79]\n"
MF "v[80:83],v[96:99],v[164:167],v[80:83]\n"
MF "v[84:87],v[96:99],v[168:171],v[84:87]\n"
MF "v[88:91],v[96:99],v[172:175],v[88:91]\n"
"s_waitcnt lgkmcnt(2)\n"
"ds_read_b128 v[96:99],v1 offset:2048\n"
MF "v[72:75],v[100:103],v[176:179],v[72:75]\n"
MF "v[76:79],v[100:103],v[180:183],v[76:79]\n"
MF "v[80:83],v[100:103],v[184:187],v[80:83]\n"
MF "v[84:87],v[100:103],v[188:191],v[84:87]\n"
MF "v[88:91],v[100:103],v[192:195],v[88:91]\n"
"s_waitcnt lgkmcnt(2)\n"
"ds_read_b128 v[100:103],v0 offset:6144\n"
MF "v[72:75],v[104:107],v[196:199],v[72:75]\n"
MF "v[76:79],v[104:107],v[200:203],v[76:79]\n"
MF "v[80:83],v[104:107],v[204:207],v[80:83]\n"
MF "v[84:87],v[104:107],v[208:211],v[84:87]\n"
MF "v[88:91],v[104:107],v[212:215],v[88:91]\n"
"s_waitcnt lgkmcnt(2)\n"
"ds_read_b128 v[104:107],v1 offset:6144\n"
MF "v[72:75],v[92:95],v[216:219],v[72:75]\n"
MF "v[76:79],v[92:95],v[220:223],v[76:79]\n"
MF "v[80:83],v[92:95],v[224:227],v[80:83]\n"
MF "v[84:87],v[92:95],v[228:231],v[84:87]\n"
MF "v[88:91],v[92:95],v[232:235],v[88:91]\n"
"s_waitcnt lgkmcnt(2)\n"
"ds_read_b128 v[92:95],v0 offset:7168\n"
MF "v[72:75],v[96:99],v[236:239],v[72:75]\n"
MF "v[76:79],v[96:99],v[240:243],v[76:79]\n"
MF "v[80:83],v[96:99],v[244:247],v[80:83]\n"
MF "v[84:87],v[96:99],v[248:251],v[84:87]\n"
MF "v[88:91],v[96:99],v[252:255],v[88:91]\n"
"s_waitcnt lgkmcnt(2)\n"
"ds_read_b128 v[96:99],v1 offset:7168\n"
MF "v[112:115],v[100:103],v[136:139],0\n"
MF "v[116:119],v[100:103],v[140:143],0\n"
MF "v[120:123],v[100:103],v[144:147],0\n"
MF "v[124:127],v[100:103],v[148:151],0\n"
MF "v[128:131],v[100:103],v[152:155],0\n"
"s_waitcnt lgkmcnt(2)\n"
"ds_read_b128 v[100:103],v0 offset:8192\n"
MF "v[112:115],v[104:107],v[156:159],v[112:115]\n"
"v_max3_f32 v52,v72,v73,v74\n"
"v_max_f32 v52,v52,v75\n"
MF "v[116:119],v[104:107],v[160:163],v[116:119]\n"
"v_max3_f32 v53,v76,v77,v78\n"
"v_max_f32 v53,v53,v79\n"
MF "v[120:123],v[104:107],v[164:167],v[120:123]\n"
"v_max3_f32 v54,v80,v81,v82\n"
"v_max_f32 v54,v54,v83\n"
MF "v[124:127],v[104:107],v[168:171],v[124:127]\n"
"v_max3_f32 v55,v84,v85,v86\n"
"v_max_f32 v55,v55,v87\n"
MF "v[128:131],v[104:107],v[172:175],v[128:131]\n"
"s_waitcnt lgkmcnt(2)\n"
"ds_read_b128 v[104:107],v1 offset:8192\n"
"v_max3_f32 v56,v88,v89,v90\n"
MF "v[112:115],v[92:95],v[176:179],v[112:115]\n"
"v_max_f32 v56,v56,v91\n"
MF "v[116:119],v[92:95],v[180:183],v[116:119]\n"
MF "v[120:123],v[92:95],v[184:187],v[120:123]\n"
MF "v[124:127],v[92:95],v[188:191],v[124:127]\n"
MF "v[128:131],v[92:95],v[192:195],v[128:131]\n"
"s_waitcnt lgkmcnt(2)\n"
MF "v[112:115],v[96:99],v[196:199],v[112:115]\n"
"s_waitcnt lgkmcnt(0)\n"
MF "v[116:119],v[96:99],v[200:203],v[116:119]\n"
"s_barrier\n"
MF "v[120:123],v[96:99],v[204:207],v[120:123]\n"
"s_add_u32 s72,s72,6144\n"
MF "v[124:127],v[96:99],v[208:211],v[124:127]\n"
"s_cmp_eq_u32 s72,s17\n"
"s_cselect_b32 s72,s16,s72\n"
"s_add_u32 m0,s64,0\n"
MF "v[128:131],v[96:99],v[212:215],v[128:131]\n"
LD "v7,s[32:35],s72 offen nt lds\n"
MF "v[112:115],v[100:103],v[216:219],v[112:115]\n"
"s_add_u32 m0,s64,896\n"
MF "v[116:119],v[100:103],v[220:223],v[116:119]\n"
LD "v7,s[32:35],s72 offen offset:128 nt lds\n"
MF "v[120:123],v[100:103],v[224:227],v[120:123]\n"
"s_add_u32 m0,s64,1792\n"
MF "v[124:127],v[100:103],v[228:231],v[124:127]\n"
LD "v7,s[32:35],s72 offen offset:256 nt lds\n"
MF "v[128:131],v[100:103],v[232:235],v[128:131]\n"
"s_add_u32 s72,s72,6144\n"
"s_add_u32 m0,s64,6144\n"
MF "v[112:115],v[104:107],v[236:239],v[112:115]\n"
LD "v7,s[32:35],s72 offen nt lds\n"
MF "v[116:119],v[104:107],v[240:243],v[116:119]\n"
"s_add_u32 m0,s64,7040\n"
MF "v[120:123],v[104:107],v[244:247],v[120:123]\n"
LD "v7,s[32:35],s72 offen offset:128 nt lds\n"
MF "v[124:127],v[104:107],v[248:251],v[124:127]\n"
"s_add_u32 m0,s64,7936\n"
MF "v[128:131],v[104:107],v[252:255],v[128:131]\n"
LD "v7,s[32:35],s72 offen offset:256 nt lds\n"
"s_branch .Lt0e\n"
".Lpgt0k_f:\n"
"s_cbranch_execz .Lisr3\n"
".Lt0k:\n"
".Lpg18f:\n"
"s_cbranch_execz .Lisr3\n"
"s_nop 0\n"
"s_lshl_b32 s58,s56,10\n"
"s_add_u32 s58,s58,73728\n"
"v_add_u32 v32,s58,v9\n"
"v_add_u32 v33,65536,v32\n"
"s_waitcnt lgkmcnt(2)\n"
MF "v[72:75],v[92:95],v[136:139],0\n"
"ds_read_b128 v[104:107],v1 offset:1024\n"
AR "v62,a20\n"
MF "v[76:79],v[92:95],v[140:143],0\n"
"ds_write_b128 v32,a[0:3] offset:24576\n"
MF "v[80:83],v[92:95],v[144:147],0\n"
"ds_write_b128 v32,a[4:7] offset:28672\n"
MF "v[84:87],v[92:95],v[148:151],0\n"
AR "v63,a21\n"
AR "v64,a22\n"
MF "v[88:91],v[92:95],v[152:155],0\n"
"s_waitcnt lgkmcnt(4)\n"
"ds_read_b128 v[92:95],v0 offset:2048\n"
AR "v65,a23\n"
MF "v[72:75],v[96:99],v[156:159],v[72:75]\n"
"ds_write_b128 v32,a[8:11] offset:32768\n"
MF "v[76:79],v[96:99],v[160:163],v[76:79]\n"
AR "v66,a24\n"
AR "v67,a25\n"
MF "v[80:83],v[96:99],v[164:167],v[80:83]\n"
"ds_write_b128 v32,a[12:15] offset:36864\n"
MF "v[84:87],v[96:99],v[168:171],v[84:87]\n"
AR "v68,a26\n"
AR "v69,a27\n"
MF "v[88:91],v[96:99],v[172:175],v[88:91]\n"
"s_waitcnt lgkmcnt(6)\n"
"ds_read_b128 v[96:99],v1 offset:2048\n"
AR "v70,a28\n"
MF "v[72:75],v[100:103],v[176:179],v[72:75]\n"
"ds_write_b128 v32,a[16:19] offset:40960\n"
MF "v[76:79],v[100:103],v[180:183],v[76:79]\n"
AR "v71,a29\n"
AR "v132,a30\n"
MF "v[80:83],v[100:103],v[184:187],v[80:83]\n"
AR "v133,a31\n"
AR "v134,a32\n"
MF "v[84:87],v[100:103],v[188:191],v[84:87]\n"
"ds_write_b128 v32,a[40:43] offset:45056\n"
MF "v[88:91],v[100:103],v[192:195],v[88:91]\n"
"s_waitcnt lgkmcnt(8)\n"
"ds_read_b128 v[100:103],v0 offset:6144\n"
AR "v135,a33\n"
MF "v[72:75],v[104:107],v[196:199],v[72:75]\n"
"ds_write_b128 v32,a[44:47] offset:49152\n"
MF "v[76:79],v[104:107],v[200:203],v[76:79]\n"
AR "v6,a34\n"
AR "v13,a35\n"
MF "v[80:83],v[104:107],v[204:207],v[80:83]\n"
AR "v14,a36\n"
AR "v15,a37\n"
MF "v[84:87],v[104:107],v[208:211],v[84:87]\n"
"ds_write_b128 v32,a[48:51] offset:53248\n"
MF "v[88:91],v[104:107],v[212:215],v[88:91]\n"
"s_waitcnt lgkmcnt(8)\n"
"ds_read_b128 v[104:107],v1 offset:6144\n"
"v_accvgpr_mov_b32 a254,a38\n"
MF "v[72:75],v[92:95],v[216:219],v[72:75]\n"
"v_accvgpr_mov_b32 a255,a39\n"
"v_accvgpr_mov_b32 a240,a168\n"
MF "v[76:79],v[92:95],v[220:223],v[76:79]\n"
"ds_write_b128 v32,a[52:55] offset:57344\n"
MF "v[80:83],v[92:95],v[224:227],v[80:83]\n"
"v_accvgpr_mov_b32 a241,a169\n"
"v_accvgpr_mov_b32 a242,a170\n"
MF "v[84:87],v[92:95],v[228:231],v[84:87]\n"
"ds_write_b128 v32,a[56:59] offset:61440\n"
MF "v[88:91],v[92:95],v[232:235],v[88:91]\n"
"s_waitcnt lgkmcnt(8)\n"
"ds_read_b128 v[92:95],v0 offset:7168\n"
"v_accvgpr_mov_b32 a243,a171\n"
MF "v[72:75],v[96:99],v[236:239],v[72:75]\n"
"v_accvgpr_mov_b32 a244,a172\n"
"v_accvgpr_mov_b32 a245,a173\n"
MF "v[76:79],v[96:99],v[240:243],v[76:79]\n"
"ds_write_b128 v33,a[80:83]\n"
MF "v[80:83],v[96:99],v[244:247],v[80:83]\n"
"v_accvgpr_mov_b32 a246,a174\n"
"v_accvgpr_mov_b32 a247,a175\n"
MF "v[84:87],v[96:99],v[248:251],v[84:87]\n"
"v_accvgpr_mov_b32 a248,a176\n"
"v_accvgpr_mov_b32 a249,a177\n"
MF "v[88:91],v[96:99],v[252:255],v[88:91]\n"
"s_waitcnt lgkmcnt(7)\n"
"ds_read_b128 v[96:99],v1 offset:7168\n"
"v_accvgpr_mov_b32 a250,a178\n"
MF "v[112:115],v[100:103],v[136:139],0\n"
"ds_write_b128 v33,a[84:87] offset:4096\n"
MF "v[116:119],v[100:103],v[140:143],0\n"
"ds_write_b128 v33,a[88:91] offset:8192\n"
MF "v[120:123],v[100:103],v[144:147],0\n"
"v_accvgpr_mov_b32 a251,a179\n"
MF "v[124:127],v[100:103],v[148:151],0\n"
MF "v[128:131],v[100:103],v[152:155],0\n"
"s_waitcnt lgkmcnt(7)\n"
"ds_read_b128 v[100:103],v0 offset:8192\n"
MF "v[112:115],v[104:107],v[156:159],v[112:115]\n"
"v_max3_f32 v52,v72,v73,v74\n"
"v_max_f32 v52,v52,v75\n"
MF "v[116:119],v[104:107],v[160:163],v[116:119]\n"
"v_max3_f32 v53,v76,v77,v78\n"
"v_max_f32 v53,v53,v79\n"
MF "v[120:123],v[104:107],v[164:167],v[120:123]\n"
"v_max3_f32 v54,v80,v81,v82\n"
"v_max_f32 v54,v54,v83\n"
MF "v[124:127],v[104:107],v[168:171],v[124:127]\n"
"v_max3_f32 v55,v84,v85,v86\n"
"v_max_f32 v55,v55,v87\n"
MF "v[128:131],v[104:107],v[172:175],v[128:131]\n"
"s_waitcnt lgkmcnt(5)\n"
"ds_read_b128 v[104:107],v1 offset:8192\n"
"v_max3_f32 v56,v88,v89,v90\n"
MF "v[112:115],v[92:95],v[176:179],v[112:115]\n"
"v_max_f32 v56,v56,v91\n"
MF "v[116:119],v[92:95],v[180:183],v[116:119]\n"
"ds_write_b128 v33,a[92:95] offset:12288\n"
MF "v[120:123],v[92:95],v[184:187],v[120:123]\n"
"ds_write_b128 v33,a[96:99] offset:16384\n"
MF "v[124:127],v[92:95],v[188:191],v[124:127]\n"
"ds_write_b128 v33,a[120:123] offset:20480\n"
MF "v[128:131],v[92:95],v[192:195],v[128:131]\n"
"s_waitcnt lgkmcnt(7)\n"
"ds_write_b128 v32,a[124:127]\n"
MF "v[112:115],v[96:99],v[196:199],v[112:115]\n"
"s_waitcnt lgkmcnt(0)\n"
MF "v[116:119],v[96:99],v[200:203],v[116:119]\n"
"s_barrier\n"
MF "v[120:123],v[96:99],v[204:207],v[120:123]\n"
"s_add_u32 s72,s72,6144\n"
"ds_write_b128 v32,a[128:131] offset:4096\n"
MF "v[124:127],v[96:99],v[208:211],v[124:127]\n"
"s_cmp_eq_u32 s72,s17\n"
"s_cselect_b32 s72,s16,s72\n"
"s_add_u32 m0,s64,0\n"
MF "v[128:131],v[96:99],v[212:215],v[128:131]\n"
LD "v7,s[32:35],s72 offen nt lds\n"
MF "v[112:115],v[100:103],v[216:219],v[112:115]\n"
"s_add_u32 m0,s64,896\n"
"ds_write_b128 v32,a[132:135] offset:8192\n"
MF "v[116:119],v[100:103],v[220:223],v[116:119]\n"
LD "v7,s[32:35],s72 offen offset:128 nt lds\n"
MF "v[120:123],v[100:103],v[224:227],v[120:123]\n"
"s_add_u32 m0,s64,1792\n"
"ds_write_b128 v32,a[136:139] offset:12288\n"
MF "v[124:127],v[100:103],v[228:231],v[124:127]\n"
LD "v7,s[32:35],s72 offen offset:256 nt lds\n"
MF "v[128:131],v[100:103],v[232:235],v[128:131]\n"
"s_add_u32 s72,s72,6144\n"
"s_add_u32 m0,s64,6144\n"
MF "v[112:115],v[104:107],v[236:239],v[112:115]\n"
LD "v7,s[32:35],s72 offen nt lds\n"
MF "v[116:119],v[104:107],v[240:243],v[116:119]\n"
"s_add_u32 m0,s64,7040\n"
"ds_write_b128 v32,a[160:163] offset:16384\n"
MF "v[120:123],v[104:107],v[244:247],v[120:123]\n"
LD "v7,s[32:35],s72 offen offset:128 nt lds\n"
MF "v[124:127],v[104:107],v[248:251],v[124:127]\n"
"s_add_u32 m0,s64,7936\n"
"ds_write_b128 v32,a[164:167] offset:20480\n"
MF "v[128:131],v[104:107],v[252:255],v[128:131]\n"
LD "v7,s[32:35],s72 offen offset:256 nt lds\n"
".Lpgt0e_g:\n"
"s_cbranch_execz .Lisr6\n"
".Lt0e:\n"
"s_nop 1\n"
"v_max3_f32 v52,v52,v112,v113\n"
"v_max3_f32 v53,v53,v116,v117\n"
"v_max3_f32 v54,v54,v120,v121\n"
"v_max3_f32 v55,v55,v124,v125\n"
"s_nop 1\n"
"v_max3_f32 v56,v56,v128,v129\n"
"v_max3_f32 v52,v52,v114,v115\n"
"v_max3_f32 v53,v53,v118,v119\n"
"v_max3_f32 v54,v54,v122,v123\n"
"v_max3_f32 v55,v55,v126,v127\n"
"v_max3_f32 v56,v56,v130,v131\n"
"v_mov_b32 v57,v52\n"
"v_mov_b32 v58,v53\n"
"v_mov_b32 v59,v54\n"
"v_mov_b32 v60,v55\n"
"v_mov_b32 v61,v56\n"
"v_permlane16_swap_b32 v52,v57\n"
"v_permlane16_swap_b32 v53,v58\n"
"v_permlane16_swap_b32 v54,v59\n"
"v_permlane16_swap_b32 v55,v60\n"
"v_permlane16_swap_b32 v56,v61\n"
"v_max_f32 v52,v52,v57\n"
"v_max_f32 v53,v53,v58\n"
"v_max_f32 v54,v54,v59\n"
"v_max_f32 v55,v55,v60\n"
"v_max_f32 v56,v56,v61\n"
"v_mov_b32 v57,v52\n"
"v_mov_b32 v58,v53\n"
"v_mov_b32 v59,v54\n"
"v_mov_b32 v60,v55\n"
"v_mov_b32 v61,v56\n"
"v_permlane32_swap_b32 v52,v57\n"
"v_permlane32_swap_b32 v53,v58\n"
"v_permlane32_swap_b32 v54,v59\n"
"v_permlane32_swap_b32 v55,v60\n"
"v_permlane32_swap_b32 v56,v61\n"
"v_max_f32 v52,v52,v57\n"
"v_max_f32 v53,v53,v58\n"
"v_max_f32 v54,v54,v59\n"
"v_max_f32 v55,v55,v60\n"
"v_max_f32 v56,v56,v61\n"
"v_sub_f32 v32,0xc1300000,v52\n"
"v_sub_f32 v36,0xc1300000,v53\n"
"v_sub_f32 v40,0xc1300000,v54\n"
"v_sub_f32 v44,0xc1300000,v55\n"
"v_sub_f32 v48,0xc1300000,v56\n"
"v_mov_b32 v33,v32\n"
"v_pk_mov_b32 v[34:35],v[32:33],v[32:33]\n"
"v_mov_b32 v37,v36\n"
"v_pk_mov_b32 v[38:39],v[36:37],v[36:37]\n"
"v_mov_b32_e64 v41,v40\n"
"v_pk_mov_b32 v[42:43],v[40:41],v[40:41]\n"
"v_mov_b32 v45,v44\n"
"v_pk_mov_b32 v[46:47],v[44:45],v[44:45]\n"
"v_mov_b32 v49,v48\n"
"v_pk_mov_b32 v[50:51],v[48:49],v[48:49]\n"
"v_pk_add_f32 v[72:73],v[72:73],v[32:33]\n"
"v_pk_add_f32 v[74:75],v[74:75],v[32:33]\n"
"v_pk_add_f32 v[112:113],v[112:113],v[32:33]\n"
"v_pk_add_f32 v[114:115],v[114:115],v[32:33]\n"
"v_exp_f32 v72,v72\n"
"v_exp_f32 v73,v73\n"
"v_exp_f32 v74,v74\n"
"v_exp_f32 v75,v75\n"
"v_exp_f32 v112,v112\n"
"v_exp_f32 v113,v113\n"
"v_exp_f32 v114,v114\n"
"v_exp_f32 v115,v115\n"
"v_pk_add_f32 v[10:11],v[72:73],v[74:75]\n"
"v_pk_add_f32 v[28:29],v[112:113],v[114:115]\n"
"v_pk_add_f32 v[10:11],v[10:11],v[28:29]\n"
"v_add_f32_e64 v52,v10,v11\n"
CV "v72,v72,v73\n"
CV "v73,v74,v75\n"
CV "v74,v112,v113\n"
CV "v75,v114,v115\n"
"v_pk_add_f32 v[76:77],v[76:77],v[36:37]\n"
"v_pk_add_f32 v[78:79],v[78:79],v[36:37]\n"
"v_pk_add_f32 v[116:117],v[116:117],v[36:37]\n"
"v_pk_add_f32 v[118:119],v[118:119],v[36:37]\n"
"v_exp_f32 v76,v76\n"
"v_exp_f32 v77,v77\n"
"v_exp_f32 v78,v78\n"
"v_exp_f32 v79,v79\n"
"v_exp_f32 v116,v116\n"
"v_exp_f32 v117,v117\n"
"v_exp_f32 v118,v118\n"
"v_exp_f32 v119,v119\n"
"v_pk_add_f32 v[10:11],v[76:77],v[78:79]\n"
"v_pk_add_f32 v[28:29],v[116:117],v[118:119]\n"
"v_pk_add_f32 v[10:11],v[10:11],v[28:29]\n"
"v_add_f32_e64 v53,v10,v11\n"
CV "v76,v76,v77\n"
CV "v77,v78,v79\n"
CV "v78,v116,v117\n"
CV "v79,v118,v119\n"
"v_pk_add_f32 v[80:81],v[80:81],v[40:41]\n"
"v_pk_add_f32 v[82:83],v[82:83],v[40:41]\n"
"v_pk_add_f32 v[120:121],v[120:121],v[40:41]\n"
"v_pk_add_f32 v[122:123],v[122:123],v[40:41]\n"
"v_exp_f32 v80,v80\n"
"v_exp_f32 v81,v81\n"
"v_exp_f32 v82,v82\n"
"v_exp_f32 v83,v83\n"
"v_exp_f32 v120,v120\n"
"v_exp_f32 v121,v121\n"
"v_exp_f32 v122,v122\n"
"v_exp_f32 v123,v123\n"
"v_pk_add_f32 v[10:11],v[80:81],v[82:83]\n"
"v_pk_add_f32 v[28:29],v[120:121],v[122:123]\n"
"v_pk_add_f32 v[10:11],v[10:11],v[28:29]\n"
"v_add_f32_e64 v54,v10,v11\n"
CV "v80,v80,v81\n"
CV "v81,v82,v83\n"
CV "v82,v120,v121\n"
CV "v83,v122,v123\n"
"v_pk_add_f32 v[84:85],v[84:85],v[44:45]\n"
"v_pk_add_f32 v[86:87],v[86:87],v[44:45]\n"
"v_pk_add_f32 v[124:125],v[124:125],v[44:45]\n"
"v_pk_add_f32 v[126:127],v[126:127],v[44:45]\n"
"v_mov_b32_e64 v55,0\n"
"v_pk_add_f32 v[88:89],v[88:89],v[48:49]\n"
"v_pk_add_f32 v[90:91],v[90:91],v[48:49]\n"
"v_pk_add_f32 v[128:129],v[128:129],v[48:49]\n"
"v_pk_add_f32 v[130:131],v[130:131],v[48:49]\n"
"v_mov_b32 v56,0\n"
"s_waitcnt vmcnt(12) lgkmcnt(0)\n"
"s_barrier\n"
"ds_read_b128 v[16:19],v0 offset:24576\n"
"ds_read_b128 v[20:23],v1 offset:24576\n"
AW "a220,s13\n"
AW "a221,s13\n"
AW "a222,s13\n"
AW "a223,s13\n"
"s_waitcnt lgkmcnt(1)\n"
MF "v[92:95],v[16:19],v[136:139],v[32:35]\n"
"s_nop 0\n"
"ds_read_b128 v[24:27],v0 offset:25600\n"
"ds_read_b128 v[28:31],v1 offset:25600\n"
"s_waitcnt lgkmcnt(2)\n"
MF "v[92:95],v[20:23],v[156:159],v[92:95]\n"
"v_exp_f32 v84,v84\n"
MF "v[96:99],v[20:23],v[160:163],v[36:39]\n"
"v_exp_f32 v85,v85\n"
MF "v[96:99],v[16:19],v[140:143],v[96:99]\n"
"v_exp_f32 v86,v86\n"
MF "v[100:103],v[16:19],v[144:147],v[40:43]\n"
"v_exp_f32 v87,v87\n"
MF "v[100:103],v[20:23],v[164:167],v[100:103]\n"
"v_add_f32 v55,v55,v84\n"
"v_add_f32 v55,v55,v85\n"
MF "v[104:107],v[20:23],v[168:171],v[44:47]\n"
CV "v84,v84,v85\n"
"v_add_f32 v55,v55,v86\n"
MF "v[104:107],v[16:19],v[148:151],v[104:107]\n"
"v_add_f32 v55,v55,v87\n"
CV "v85,v86,v87\n"
MF "v[108:111],v[16:19],v[152:155],v[48:51]\n"
"v_exp_f32 v124,v124\n"
MF "v[108:111],v[20:23],v[172:175],v[108:111]\n"
"s_waitcnt lgkmcnt(0)\n"
"ds_read_b128 v[16:19],v0 offset:26624\n"
"ds_read_b128 v[20:23],v1 offset:26624\n"
MF "v[108:111],v[24:27],v[192:195],v[108:111]\n"
"v_exp_f32 v125,v125\n"
MF "v[108:111],v[28:31],v[212:215],v[108:111]\n"
"v_exp_f32 v126,v126\n"
MF "v[104:107],v[28:31],v[208:211],v[104:107]\n"
"v_exp_f32 v127,v127\n"
MF "v[104:107],v[24:27],v[188:191],v[104:107]\n"
"v_add_f32 v55,v55,v124\n"
"v_add_f32 v55,v55,v125\n"
MF "v[100:103],v[24:27],v[184:187],v[100:103]\n"
CV "v86,v124,v125\n"
"v_add_f32 v55,v55,v126\n"
MF "v[100:103],v[28:31],v[204:207],v[100:103]\n"
"v_add_f32 v55,v55,v127\n"
CV "v87,v126,v127\n"
MF "v[96:99],v[28:31],v[200:203],v[96:99]\n"
"v_exp_f32 v88,v88\n"
MF "v[96:99],v[24:27],v[180:183],v[96:99]\n"
"v_exp_f32 v89,v89\n"
MF "v[92:95],v[24:27],v[176:179],v[92:95]\n"
"v_exp_f32 v90,v90\n"
MF "v[92:95],v[28:31],v[196:199],v[92:95]\n"
"s_waitcnt lgkmcnt(0)\n"
"ds_read_b128 v[24:27],v0 offset:30720\n"
"ds_read_b128 v[28:31],v1 offset:30720\n"
MF "v[92:95],v[16:19],v[216:219],v[92:95]\n"
"v_exp_f32 v91,v91\n"
MF "v[92:95],v[20:23],v[236:239],v[92:95]\n"
"v_add_f32 v56,v56,v88\n"
"v_add_f32 v56,v56,v89\n"
MF "v[96:99],v[20:23],v[240:243],v[96:99]\n"
CV "v88,v88,v89\n"
"v_add_f32 v56,v56,v90\n"
MF "v[96:99],v[16:19],v[220:223],v[96:99]\n"
"v_add_f32 v56,v56,v91\n"
CV "v89,v90,v91\n"
MF "v[100:103],v[16:19],v[224:227],v[100:103]\n"
"v_exp_f32 v128,v128\n"
MF "v[100:103],v[20:23],v[244:247],v[100:103]\n"
"v_exp_f32 v129,v129\n"
MF "v[104:107],v[20:23],v[248:251],v[104:107]\n"
"v_exp_f32 v130,v130\n"
MF "v[104:107],v[16:19],v[228:231],v[104:107]\n"
"v_exp_f32 v131,v131\n"
MF "v[108:111],v[16:19],v[232:235],v[108:111]\n"
"v_add_f32 v56,v56,v128\n"
"v_add_f32 v56,v56,v129\n"
MF "v[108:111],v[20:23],v[252:255],v[108:111]\n"
"s_waitcnt lgkmcnt(0)\n"
"ds_read_b128 v[16:19],v0 offset:31744\n"
"ds_read_b128 v[20:23],v1 offset:31744\n"
MF "v[112:115],v[24:27],v[136:139],v[32:35]\n"
CV "v90,v128,v129\n"
"v_add_f32 v56,v56,v130\n"
MF "v[112:115],v[28:31],v[156:159],v[112:115]\n"
"v_add_f32 v56,v56,v131\n"
CV "v91,v130,v131\n"
MF "v[116:119],v[28:31],v[160:163],v[36:39]\n"
"v_exp_f32 v92,v92\n"
MF "v[116:119],v[24:27],v[140:143],v[116:119]\n"
"v_exp_f32 v93,v93\n"
MF "v[120:123],v[24:27],v[144:147],v[40:43]\n"
"v_exp_f32 v94,v94\n"
MF "v[120:123],v[28:31],v[164:167],v[120:123]\n"
"v_exp_f32 v95,v95\n"
MF "v[124:127],v[28:31],v[168:171],v[44:47]\n"
"v_add_f32 v57,v52,v92\n"
"v_add_f32 v57,v57,v93\n"
MF "v[124:127],v[24:27],v[148:151],v[124:127]\n"
CV "v92,v92,v93\n"
"v_add_f32 v57,v57,v94\n"
MF "v[128:131],v[24:27],v[152:155],v[48:51]\n"
"v_add_f32 v57,v57,v95\n"
CV "v93,v94,v95\n"
MF "v[128:131],v[28:31],v[172:175],v[128:131]\n"
"s_waitcnt lgkmcnt(0)\n"
"ds_read_b128 v[24:27],v0 offset:32768\n"
"ds_read_b128 v[28:31],v1 offset:32768\n"
MF "v[128:131],v[16:19],v[192:195],v[128:131]\n"
"v_exp_f32 v96,v96\n"
MF "v[128:131],v[20:23],v[212:215],v[128:131]\n"
"v_exp_f32 v97,v97\n"
MF "v[124:127],v[20:23],v[208:211],v[124:127]\n"
"v_exp_f32 v98,v98\n"
MF "v[124:127],v[16:19],v[188:191],v[124:127]\n"
"v_exp_f32 v99,v99\n"
MF "v[120:123],v[16:19],v[184:187],v[120:123]\n"
"v_add_f32 v58,v53,v96\n"
"v_add_f32 v58,v58,v97\n"
MF "v[120:123],v[20:23],v[204:207],v[120:123]\n"
CV "v96,v96,v97\n"
"v_add_f32 v58,v58,v98\n"
MF "v[116:119],v[20:23],v[200:203],v[116:119]\n"
"s_waitcnt vmcnt(6) lgkmcnt(0)\n"
"v_add_f32 v58,v58,v99\n"
CV "v97,v98,v99\n"
MF "v[116:119],v[16:19],v[180:183],v[116:119]\n"
"s_barrier\n"
MF "v[112:115],v[16:19],v[176:179],v[112:115]\n"
"v_exp_f32 v100,v100\n"
MF "v[112:115],v[20:23],v[196:199],v[112:115]\n"
"s_nop 0\n"
TR "v[16:17],v2 offset:49152\n"
TR "v[18:19],v2 offset:55296\n"
MF "v[112:115],v[24:27],v[216:219],v[112:115]\n"
"s_add_u32 s73,s73,12288\n"
TR "v[20:21],v3 offset:49152\n"
TR "v[22:23],v3 offset:55296\n"
MF "v[112:115],v[28:31],v[236:239],v[112:115]\n"
"s_cmp_eq_u32 s73,s19\n"
"s_cselect_b32 s73,s18,s73\n"
"s_add_u32 m0,s65,24576\n"
MF "v[116:119],v[28:31],v[240:243],v[116:119]\n"
LD "v8,s[36:39],s73 offen nt lds\n"
MF "v[116:119],v[24:27],v[220:223],v[116:119]\n"
"s_add_u32 m0,s65,25472\n"
"v_exp_f32 v101,v101\n"
MF "v[120:123],v[24:27],v[224:227],v[120:123]\n"
LD "v8,s[36:39],s73 offen offset:128 nt lds\n"
MF "v[120:123],v[28:31],v[244:247],v[120:123]\n"
"v_exp_f32 v102,v102\n"
MF "v[124:127],v[28:31],v[248:251],v[124:127]\n"
"s_add_u32 m0,s65,26368\n"
"v_exp_f32 v103,v103\n"
MF "v[124:127],v[24:27],v[228:231],v[124:127]\n"
LD "v8,s[36:39],s73 offen offset:256 nt lds\n"
MF "v[128:131],v[24:27],v[232:235],v[128:131]\n"
"v_add_f32 v59,v54,v100\n"
"v_add_f32 v59,v59,v101\n"
MF "v[128:131],v[28:31],v[252:255],v[128:131]\n"
"s_waitcnt lgkmcnt(0)\n"
TR "v[24:25],v4 offset:49152\n"
TR "v[26:27],v4 offset:55296\n"
MF "a[0:3],v[16:19],v[72:75],a[220:223]\n"
"s_add_u32 m0,s65,24576\n"
TR "v[28:29],v5 offset:49152\n"
TR "v[30:31],v5 offset:55296\n"
MF "a[20:23],v[20:23],v[72:75],a[220:223]\n"
LD "v8,s[36:39],s73 offen offset:3072 nt lds\n"
MF "a[24:27],v[20:23],v[76:79],a[220:223]\n"
CV "v100,v100,v101\n"
"v_add_f32 v59,v59,v102\n"
MF "a[4:7],v[16:19],v[76:79],a[220:223]\n"
"s_add_u32 m0,s65,25472\n"
"v_add_f32 v59,v59,v103\n"
CV "v101,v102,v103\n"
MF "a[8:11],v[16:19],v[80:83],a[220:223]\n"
LD "v8,s[36:39],s73 offen offset:3200 nt lds\n"
MF "a[28:31],v[20:23],v[80:83],a[220:223]\n"
"v_exp_f32 v104,v104\n"
MF "a[32:35],v[20:23],v[84:87],a[220:223]\n"
"s_add_u32 m0,s65,26368\n"
"v_exp_f32 v105,v105\n"
MF "a[12:15],v[16:19],v[84:87],a[220:223]\n"
LD "v8,s[36:39],s73 offen offset:3328 nt lds\n"
MF "a[16:19],v[16:19],v[88:91],a[220:223]\n"
"v_exp_f32 v106,v106\n"
MF "a[36:39],v[20:23],v[88:91],a[220:223]\n"
"s_waitcnt lgkmcnt(0)\n"
TR "v[16:17],v2 offset:50176\n"
TR "v[18:19],v2 offset:56320\n"
MF "a[56:59],v[24:27],v[88:91],a[220:223]\n"
"s_nop 0\n"
TR "v[20:21],v3 offset:50176\n"
TR "v[22:23],v3 offset:56320\n"
MF "a[76:79],v[28:31],v[88:91],a[220:223]\n"
"v_exp_f32 v107,v107\n"
MF "a[72:75],v[28:31],v[84:87],a[220:223]\n"
"v_add_f32 v60,v55,v104\n"
"v_add_f32 v60,v60,v105\n"
MF "a[52:55],v[24:27],v[84:87],a[220:223]\n"
CV "v104,v104,v105\n"
"v_add_f32 v60,v60,v106\n"
MF "a[48:51],v[24:27],v[80:83],a[220:223]\n"
"v_add_f32 v60,v60,v107\n"
CV "v105,v106,v107\n"
MF "a[68:71],v[28:31],v[80:83],a[220:223]\n"
"v_exp_f32 v108,v108\n"
MF "a[64:67],v[28:31],v[76:79],a[220:223]\n"
"v_exp_f32 v109,v109\n"
MF "a[44:47],v[24:27],v[76:79],a[220:223]\n"
"v_exp_f32 v110,v110\n"
MF "a[40:43],v[24:27],v[72:75],a[220:223]\n"
"v_exp_f32 v111,v111\n"
MF "a[60:63],v[28:31],v[72:75],a[220:223]\n"
"s_waitcnt lgkmcnt(0)\n"
TR "v[24:25],v4 offset:50176\n"
TR "v[26:27],v4 offset:56320\n"
MF "a[80:83],v[16:19],v[72:75],a[220:223]\n"
"s_nop 0\n"
TR "v[28:29],v5 offset:50176\n"
TR "v[30:31],v5 offset:56320\n"
MF "a[100:103],v[20:23],v[72:75],a[220:223]\n"
"v_add_f32 v61,v56,v108\n"
"v_add_f32 v61,v61,v109\n"
MF "a[104:107],v[20:23],v[76:79],a[220:223]\n"
CV "v108,v108,v109\n"
"v_add_f32 v61,v61,v110\n"
MF "a[84:87],v[16:19],v[76:79],a[220:223]\n"
"v_add_f32 v61,v61,v111\n"
CV "v109,v110,v111\n"
MF "a[88:91],v[16:19],v[80:83],a[220:223]\n"
"v_exp_f32 v112,v112\n"
MF "a[108:111],v[20:23],v[80:83],a[220:223]\n"
"v_exp_f32 v113,v113\n"
MF "a[112:115],v[20:23],v[84:87],a[220:223]\n"
"v_exp_f32 v114,v114\n"
MF "a[92:95],v[16:19],v[84:87],a[220:223]\n"
"v_exp_f32 v115,v115\n"
MF "a[96:99],v[16:19],v[88:91],a[220:223]\n"
"v_add_f32 v57,v57,v112\n"
"v_add_f32 v57,v57,v113\n"
MF "a[116:119],v[20:23],v[88:91],a[220:223]\n"
"s_waitcnt lgkmcnt(0)\n"
TR "v[16:17],v2 offset:51200\n"
TR "v[18:19],v2 offset:57344\n"
MF "a[136:139],v[24:27],v[88:91],a[220:223]\n"
"s_nop 0\n"
TR "v[20:21],v3 offset:51200\n"
TR "v[22:23],v3 offset:57344\n"
MF "a[156:159],v[28:31],v[88:91],a[220:223]\n"
CV "v94,v112,v113\n"
"v_add_f32 v57,v57,v114\n"
MF "a[152:155],v[28:31],v[84:87],a[220:223]\n"
"v_add_f32 v57,v57,v115\n"
CV "v95,v114,v115\n"
MF "a[132:135],v[24:27],v[84:87],a[220:223]\n"
"v_exp_f32 v116,v116\n"
MF "a[128:131],v[24:27],v[80:83],a[220:223]\n"
"v_exp_f32 v117,v117\n"
MF "a[148:151],v[28:31],v[80:83],a[220:223]\n"
"v_exp_f32 v118,v118\n"
MF "a[144:147],v[28:31],v[76:79],a[220:223]\n"
"v_exp_f32 v119,v119\n"
MF "a[124:127],v[24:27],v[76:79],a[220:223]\n"
"v_add_f32 v58,v58,v116\n"
"v_add_f32 v58,v58,v117\n"
MF "a[120:123],v[24:27],v[72:75],a[220:223]\n"
CV "v98,v116,v117\n"
"v_add_f32 v58,v58,v118\n"
MF "a[140:143],v[28:31],v[72:75],a[220:223]\n"
"s_waitcnt lgkmcnt(0)\n"
TR "v[24:25],v4 offset:51200\n"
TR "v[26:27],v4 offset:57344\n"
MF "a[160:163],v[16:19],v[72:75],a[220:223]\n"
"s_nop 0\n"
TR "v[28:29],v5 offset:51200\n"
TR "v[30:31],v5 offset:57344\n"
MF "a[180:183],v[20:23],v[72:75],a[220:223]\n"
"v_add_f32 v58,v58,v119\n"
CV "v99,v118,v119\n"
MF "a[184:187],v[20:23],v[76:79],a[220:223]\n"
"v_exp_f32 v120,v120\n"
MF "a[164:167],v[16:19],v[76:79],a[220:223]\n"
"v_exp_f32 v121,v121\n"
MF "a[168:171],v[16:19],v[80:83],a[220:223]\n"
"v_exp_f32 v122,v122\n"
MF "a[188:191],v[20:23],v[80:83],a[220:223]\n"
"v_exp_f32 v123,v123\n"
MF "a[192:195],v[20:23],v[84:87],a[220:223]\n"
"v_add_f32 v59,v59,v120\n"
"v_add_f32 v59,v59,v121\n"
MF "a[172:175],v[16:19],v[84:87],a[220:223]\n"
"s_waitcnt vmcnt(6) lgkmcnt(0)\n"
CV "v102,v120,v121\n"
"v_add_f32 v59,v59,v122\n"
MF "a[176:179],v[16:19],v[88:91],a[220:223]\n"
"s_barrier\n"
MF "a[196:199],v[20:23],v[88:91],a[220:223]\n"
"s_add_u32 s72,s72,6144\n"
"ds_read_b128 v[16:19],v0\n"
"ds_read_b128 v[20:23],v1\n"
MF "a[216:219],v[24:27],v[88:91],a[220:223]\n"
"s_cmp_eq_u32 s72,s17\n"
"s_cselect_b32 s72,s16,s72\n"
"s_add_u32 m0,s64,49152\n"
MF "a[236:239],v[28:31],v[88:91],a[220:223]\n"
LD "v7,s[32:35],s72 offen nt lds\n"
MF "a[232:235],v[28:31],v[84:87],a[220:223]\n"
"s_add_u32 m0,s64,50048\n"
"v_add_f32 v59,v59,v123\n"
CV "v103,v122,v123\n"
MF "a[212:215],v[24:27],v[84:87],a[220:223]\n"
LD "v7,s[32:35],s72 offen offset:128 nt lds\n"
MF "a[208:211],v[24:27],v[80:83],a[220:223]\n"
"s_add_u32 m0,s64,50944\n"
MF "a[228:231],v[28:31],v[80:83],a[220:223]\n"
LD "v7,s[32:35],s72 offen offset:256 nt lds\n"
MF "a[224:227],v[28:31],v[76:79],a[220:223]\n"
MF "a[204:207],v[24:27],v[76:79],a[220:223]\n"
MF "a[200:203],v[24:27],v[72:75],a[220:223]\n"
".Lpg19f:\n"
"s_cbranch_execz .Lisr3\n"
".Lpg19g:\n"
"s_cbranch_execz .Lisr6\n"
MF "a[220:223],v[28:31],v[72:75],a[220:223]\n"
"s_waitcnt lgkmcnt(0)\n"
".Laloop:\n"
MF "v[72:75],v[16:19],v[136:139],v[32:35]\n"
"s_nop 0\n"
"ds_read_b128 v[24:27],v0 offset:1024\n"
"ds_read_b128 v[28:31],v1 offset:1024\n"
MF "v[72:75],v[20:23],v[156:159],v[72:75]\n"
MF "v[76:79],v[20:23],v[160:163],v[36:39]\n"
MF "v[76:79],v[16:19],v[140:143],v[76:79]\n"
MF "v[80:83],v[16:19],v[144:147],v[40:43]\n"
MF "v[80:83],v[20:23],v[164:167],v[80:83]\n"
MF "v[84:87],v[20:23],v[168:171],v[44:47]\n"
MF "v[84:87],v[16:19],v[148:151],v[84:87]\n"
"s_add_u32 s72,s72,6144\n"
"s_add_u32 m0,s64,55296\n"
MF "v[88:91],v[16:19],v[152:155],v[48:51]\n"
LD "v7,s[32:35],s72 offen nt lds\n"
MF "v[88:91],v[20:23],v[172:175],v[88:91]\n"
"s_waitcnt lgkmcnt(0)\n"
"ds_read_b128 v[16:19],v0 offset:2048\n"
"ds_read_b128 v[20:23],v1 offset:2048\n"
MF "v[88:91],v[24:27],v[192:195],v[88:91]\n"
"v_exp_f32 v124,v124\n"
MF "v[88:91],v[28:31],v[212:215],v[88:91]\n"
"s_add_u32 m0,s64,56192\n"
"v_exp_f32 v125,v125\n"
MF "v[84:87],v[28:31],v[208:211],v[84:87]\n"
LD "v7,s[32:35],s72 offen offset:128 nt lds\n"
MF "v[84:87],v[24:27],v[188:191],v[84:87]\n"
"v_exp_f32 v126,v126\n"
MF "v[80:83],v[24:27],v[184:187],v[80:83]\n"
"v_exp_f32 v127,v127\n"
MF "v[80:83],v[28:31],v[204:207],v[80:83]\n"
"s_add_u32 m0,s64,57088\n"
"v_add_f32 v60,v60,v124\n"
"v_add_f32 v60,v60,v125\n"
MF "v[76:79],v[28:31],v[200:203],v[76:79]\n"
LD "v7,s[32:35],s72 offen offset:256 nt lds\n"
MF "v[76:79],v[24:27],v[180:183],v[76:79]\n"
CV "v106,v124,v125\n"
"v_add_f32 v60,v60,v126\n"
MF "v[72:75],v[24:27],v[176:179],v[72:75]\n"
"v_add_f32 v60,v60,v127\n"
CV "v107,v126,v127\n"
MF "v[72:75],v[28:31],v[196:199],v[72:75]\n"
"s_waitcnt lgkmcnt(0)\n"
"ds_read_b128 v[24:27],v0 offset:6144\n"
"ds_read_b128 v[28:31],v1 offset:6144\n"
MF "v[72:75],v[16:19],v[216:219],v[72:75]\n"
"v_exp_f32 v128,v128\n"
MF "v[72:75],v[20:23],v[236:239],v[72:75]\n"
"v_exp_f32 v129,v129\n"
MF "v[76:79],v[20:23],v[240:243],v[76:79]\n"
"v_exp_f32 v130,v130\n"
MF "v[76:79],v[16:19],v[220:223],v[76:79]\n"
"v_exp_f32 v131,v131\n"
MF "v[80:83],v[16:19],v[224:227],v[80:83]\n"
"v_add_f32 v61,v61,v128\n"
"v_add_f32 v61,v61,v129\n"
MF "v[80:83],v[20:23],v[244:247],v[80:83]\n"
CV "v110,v128,v129\n"
"v_add_f32 v61,v61,v130\n"
MF "v[84:87],v[20:23],v[248:251],v[84:87]\n"
"v_add_f32 v61,v61,v131\n"
CV "v111,v130,v131\n"
MF "v[84:87],v[16:19],v[228:231],v[84:87]\n"
"v_max3_f32 v10,v57,v58,v59\n"
"v_max3_f32 v10,v10,v60,v61\n"
MF "v[88:91],v[16:19],v[232:235],v[88:91]\n"
"v_cmp_ngt_f32 vcc,s77,v10\n"
MF "v[88:91],v[20:23],v[252:255],v[88:91]\n"
"s_waitcnt lgkmcnt(0)\n"
"s_cbranch_vccnz .Lstub_20\n"
".Lback_20:\n"
MF "v[112:115],v[24:27],v[136:139],v[32:35]\n"
"s_add_u32 s74,s74,12288\n"
"ds_read_b128 v[16:19],v0 offset:7168\n"
"ds_read_b128 v[20:23],v1 offset:7168\n"
MF "v[112:115],v[28:31],v[156:159],v[112:115]\n"
"s_cmp_eq_u32 s74,s20\n"
"s_cselect_b32 s74,s70,s74\n"
MF "v[116:119],v[28:31],v[160:163],v[36:39]\n"
"v_exp_f32 v72,v72\n"
MF "v[116:119],v[24:27],v[140:143],v[116:119]\n"
"v_exp_f32 v73,v73\n"
MF "v[120:123],v[24:27],v[144:147],v[40:43]\n"
"v_exp_f32 v74,v74\n"
MF "v[120:123],v[28:31],v[164:167],v[120:123]\n"
"v_exp_f32 v75,v75\n"
MF "v[124:127],v[28:31],v[168:171],v[44:47]\n"
"v_add_f32 v52,v57,v72\n"
"v_add_f32 v52,v52,v73\n"
MF "v[124:127],v[24:27],v[148:151],v[124:127]\n"
CV "v72,v72,v73\n"
"v_add_f32 v52,v52,v74\n"
MF "v[128:131],v[24:27],v[152:155],v[48:51]\n"
"v_add_f32 v52,v52,v75\n"
CV "v73,v74,v75\n"
MF "v[128:131],v[28:31],v[172:175],v[128:131]\n"
"s_waitcnt lgkmcnt(0)\n"
"ds_read_b128 v[24:27],v0 offset:8192\n"
"ds_read_b128 v[28:31],v1 offset:8192\n"
MF "v[128:131],v[16:19],v[192:195],v[128:131]\n"
"v_exp_f32 v76,v76\n"
MF "v[128:131],v[20:23],v[212:215],v[128:131]\n"
"v_exp_f32 v77,v77\n"
MF "v[124:127],v[20:23],v[208:211],v[124:127]\n"
"v_exp_f32 v78,v78\n"
MF "v[124:127],v[16:19],v[188:191],v[124:127]\n"
"v_exp_f32 v79,v79\n"
MF "v[120:123],v[16:19],v[184:187],v[120:123]\n"
"v_add_f32 v53,v58,v76\n"
"v_add_f32 v53,v53,v77\n"
MF "v[120:123],v[20:23],v[204:207],v[120:123]\n"
CV "v76,v76,v77\n"
"v_add_f32 v53,v53,v78\n"
MF "v[116:119],v[20:23],v[200:203],v[116:119]\n"
"s_waitcnt vmcnt(6) lgkmcnt(0)\n"
"v_add_f32 v53,v53,v79\n"
CV "v77,v78,v79\n"
MF "v[116:119],v[16:19],v[180:183],v[116:119]\n"
"s_barrier\n"
MF "v[112:115],v[16:19],v[176:179],v[112:115]\n"
"v_exp_f32 v80,v80\n"
MF "v[112:115],v[20:23],v[196:199],v[112:115]\n"
"s_nop 0\n"
TR "v[16:17],v2 offset:24576\n"
TR "v[18:19],v2 offset:30720\n"
MF "v[112:115],v[24:27],v[216:219],v[112:115]\n"
"s_add_u32 s73,s73,12288\n"
TR "v[20:21],v3 offset:24576\n"
TR "v[22:23],v3 offset:30720\n"
MF "v[112:115],v[28:31],v[236:239],v[112:115]\n"
"s_cmp_eq_u32 s73,s19\n"
"s_cselect_b32 s73,s18,s73\n"
"s_add_u32 m0,s65,0\n"
MF "v[116:119],v[28:31],v[240:243],v[116:119]\n"
LD "v8,s[36:39],s73 offen nt lds\n"
MF "v[116:119],v[24:27],v[220:223],v[116:119]\n"
"s_add_u32 m0,s65,896\n"
"v_exp_f32 v81,v81\n"
MF "v[120:123],v[24:27],v[224:227],v[120:123]\n"
LD "v8,s[36:39],s73 offen offset:128 nt lds\n"
MF "v[120:123],v[28:31],v[244:247],v[120:123]\n"
"v_exp_f32 v82,v82\n"
MF "v[124:127],v[28:31],v[248:251],v[124:127]\n"
"s_add_u32 m0,s65,1792\n"
"v_exp_f32 v83,v83\n"
MF "v[124:127],v[24:27],v[228:231],v[124:127]\n"
LD "v8,s[36:39],s73 offen offset:256 nt lds\n"
MF "v[128:131],v[24:27],v[232:235],v[128:131]\n"
"v_add_f32 v54,v59,v80\n"
"v_add_f32 v54,v54,v81\n"
MF "v[128:131],v[28:31],v[252:255],v[128:131]\n"
"s_waitcnt lgkmcnt(0)\n"
TR "v[24:25],v4 offset:24576\n"
TR "v[26:27],v4 offset:30720\n"
MF "a[0:3],v[16:19],v[92:95],a[0:3]\n"
"s_add_u32 m0,s65,0\n"
TR "v[28:29],v5 offset:24576\n"
TR "v[30:31],v5 offset:30720\n"
MF "a[20:23],v[20:23],v[92:95],a[20:23]\n"
LD "v8,s[36:39],s73 offen offset:3072 nt lds\n"
MF "a[24:27],v[20:23],v[96:99],a[24:27]\n"
CV "v80,v80,v81\n"
"v_add_f32 v54,v54,v82\n"
MF "a[4:7],v[16:19],v[96:99],a[4:7]\n"
"s_add_u32 m0,s65,896\n"
"v_add_f32 v54,v54,v83\n"
CV "v81,v82,v83\n"
MF "a[8:11],v[16:19],v[100:103],a[8:11]\n"
LD "v8,s[36:39],s73 offen offset:3200 nt lds\n"
MF "a[28:31],v[20:23],v[100:103],a[28:31]\n"
"v_exp_f32 v84,v84\n"
MF "a[32:35],v[20:23],v[104:107],a[32:35]\n"
"s_add_u32 m0,s65,1792\n"
"v_exp_f32 v85,v85\n"
MF "a[12:15],v[16:19],v[104:107],a[12:15]\n"
LD "v8,s[36:39],s73 offen offset:3328 nt lds\n"
MF "a[16:19],v[16:19],v[108:111],a[16:19]\n"
"v_exp_f32 v86,v86\n"
MF "a[36:39],v[20:23],v[108:111],a[36:39]\n"
"s_waitcnt lgkmcnt(0)\n"
TR "v[16:17],v2 offset:25600\n"
TR "v[18:19],v2 offset:31744\n"
MF "a[56:59],v[24:27],v[108:111],a[56:59]\n"
"s_nop 0\n"
TR "v[20:21],v3 offset:25600\n"
TR "v[22:23],v3 offset:31744\n"
MF "a[76:79],v[28:31],v[108:111],a[76:79]\n"
"v_exp_f32 v87,v87\n"
MF "a[72:75],v[28:31],v[104:107],a[72:75]\n"
"v_add_f32 v55,v60,v84\n"
"v_add_f32 v55,v55,v85\n"
MF "a[52:55],v[24:27],v[104:107],a[52:55]\n"
CV "v84,v84,v85\n"
"v_add_f32 v55,v55,v86\n"
MF "a[48:51],v[24:27],v[100:103],a[48:51]\n"
"v_add_f32 v55,v55,v87\n"
CV "v85,v86,v87\n"
MF "a[68:71],v[28:31],v[100:103],a[68:71]\n"
"v_exp_f32 v88,v88\n"
MF "a[64:67],v[28:31],v[96:99],a[64:67]\n"
"v_exp_f32 v89,v89\n"
MF "a[44:47],v[24:27],v[96:99],a[44:47]\n"
"v_exp_f32 v90,v90\n"
MF "a[40:43],v[24:27],v[92:95],a[40:43]\n"
"v_exp_f32 v91,v91\n"
MF "a[60:63],v[28:31],v[92:95],a[60:63]\n"
"s_waitcnt lgkmcnt(0)\n"
TR "v[24:25],v4 offset:25600\n"
TR "v[26:27],v4 offset:31744\n"
MF "a[80:83],v[16:19],v[92:95],a[80:83]\n"
"s_nop 0\n"
TR "v[28:29],v5 offset:25600\n"
TR "v[30:31],v5 offset:31744\n"
MF "a[100:103],v[20:23],v[92:95],a[100:103]\n"
"v_add_f32 v56,v61,v88\n"
"v_add_f32 v56,v56,v89\n"
MF "a[104:107],v[20:23],v[96:99],a[104:107]\n"
CV "v88,v88,v89\n"
"v_add_f32 v56,v56,v90\n"
MF "a[84:87],v[16:19],v[96:99],a[84:87]\n"
"v_add_f32 v56,v56,v91\n"
CV "v89,v90,v91\n"
MF "a[88:91],v[16:19],v[100:103],a[88:91]\n"
"v_exp_f32 v112,v112\n"
MF "a[108:111],v[20:23],v[100:103],a[108:111]\n"
"v_exp_f32 v113,v113\n"
MF "a[112:115],v[20:23],v[104:107],a[112:115]\n"
"v_exp_f32 v114,v114\n"
MF "a[92:95],v[16:19],v[104:107],a[92:95]\n"
"v_exp_f32 v115,v115\n"
MF "a[96:99],v[16:19],v[108:111],a[96:99]\n"
"v_add_f32 v52,v52,v112\n"
"v_add_f32 v52,v52,v113\n"
MF "a[116:119],v[20:23],v[108:111],a[116:119]\n"
"s_waitcnt lgkmcnt(0)\n"
TR "v[16:17],v2 offset:26624\n"
TR "v[18:19],v2 offset:32768\n"
MF "a[136:139],v[24:27],v[108:111],a[136:139]\n"
"s_nop 0\n"
TR "v[20:21],v3 offset:26624\n"
TR "v[22:23],v3 offset:32768\n"
MF "a[156:159],v[28:31],v[108:111],a[156:159]\n"
CV "v74,v112,v113\n"
"v_add_f32 v52,v52,v114\n"
MF "a[152:155],v[28:31],v[104:107],a[152:155]\n"
"v_add_f32 v52,v52,v115\n"
CV "v75,v114,v115\n"
MF "a[132:135],v[24:27],v[104:107],a[132:135]\n"
"v_exp_f32 v116,v116\n"
MF "a[128:131],v[24:27],v[100:103],a[128:131]\n"
"v_exp_f32 v117,v117\n"
MF "a[148:151],v[28:31],v[100:103],a[148:151]\n"
"v_exp_f32 v118,v118\n"
MF "a[144:147],v[28:31],v[96:99],a[144:147]\n"
"v_exp_f32 v119,v119\n"
MF "a[124:127],v[24:27],v[96:99],a[124:127]\n"
"v_add_f32 v53,v53,v116\n"
"v_add_f32 v53,v53,v117\n"
MF "a[120:123],v[24:27],v[92:95],a[120:123]\n"
CV "v78,v116,v117\n"
"v_add_f32 v53,v53,v118\n"
MF "a[140:143],v[28:31],v[92:95],a[140:143]\n"
"s_waitcnt lgkmcnt(0)\n"
TR "v[24:25],v4 offset:26624\n"
TR "v[26:27],v4 offset:32768\n"
MF "a[160:163],v[16:19],v[92:95],a[160:163]\n"
"s_nop 0\n"
TR "v[28:29],v5 offset:26624\n"
TR "v[30:31],v5 offset:32768\n"
MF "a[180:183],v[20:23],v[92:95],a[180:183]\n"
"v_add_f32 v53,v53,v119\n"
CV "v79,v118,v119\n"
MF "a[184:187],v[20:23],v[96:99],a[184:187]\n"
"v_exp_f32 v120,v120\n"
MF "a[164:167],v[16:19],v[96:99],a[164:167]\n"
"v_exp_f32 v121,v121\n"
MF "a[168:171],v[16:19],v[100:103],a[168:171]\n"
"v_exp_f32 v122,v122\n"
MF "a[188:191],v[20:23],v[100:103],a[188:191]\n"
"v_exp_f32 v123,v123\n"
MF "a[192:195],v[20:23],v[104:107],a[192:195]\n"
"v_add_f32 v54,v54,v120\n"
"v_add_f32 v54,v54,v121\n"
MF "a[172:175],v[16:19],v[104:107],a[172:175]\n"
"s_waitcnt vmcnt(6) lgkmcnt(0)\n"
CV "v82,v120,v121\n"
"v_add_f32 v54,v54,v122\n"
MF "a[176:179],v[16:19],v[108:111],a[176:179]\n"
"s_barrier\n"
MF "a[196:199],v[20:23],v[108:111],a[196:199]\n"
"s_add_u32 s72,s72,6144\n"
"ds_read_b128 v[16:19],v0 offset:49152\n"
"ds_read_b128 v[20:23],v1 offset:49152\n"
MF "a[216:219],v[24:27],v[108:111],a[216:219]\n"
"s_cmp_eq_u32 s72,s17\n"
"s_cselect_b32 s72,s16,s72\n"
"s_add_u32 m0,s64,24576\n"
MF "a[236:239],v[28:31],v[108:111],a[236:239]\n"
LD "v7,s[32:35],s72 offen nt lds\n"
MF "a[232:235],v[28:31],v[104:107],a[232:235]\n"
"s_add_u32 m0,s64,25472\n"
"v_add_f32 v54,v54,v123\n"
CV "v83,v122,v123\n"
MF "a[212:215],v[24:27],v[104:107],a[212:215]\n"
LD "v7,s[32:35],s72 offen offset:128 nt lds\n"
MF "a[208:211],v[24:27],v[100:103],a[208:211]\n"
"s_add_u32 m0,s64,26368\n"
MF "a[228:231],v[28:31],v[100:103],a[228:231]\n"
LD "v7,s[32:35],s72 offen offset:256 nt lds\n"
MF "a[224:227],v[28:31],v[96:99],a[224:227]\n"
MF "a[204:207],v[24:27],v[96:99],a[204:207]\n"
MF "a[200:203],v[24:27],v[92:95],a[200:203]\n"
MF "a[220:223],v[28:31],v[92:95],a[220:223]\n"
"s_waitcnt lgkmcnt(0)\n"
MF "v[92:95],v[16:19],v[136:139],v[32:35]\n"
"s_add_u32 s72,s72,6144\n"
"ds_read_b128 v[24:27],v0 offset:50176\n"
"ds_read_b128 v[28:31],v1 offset:50176\n"
MF "v[92:95],v[20:23],v[156:159],v[92:95]\n"
"s_add_u32 m0,s64,30720\n"
"v_exp_f32 v124,v124\n"
MF "v[96:99],v[20:23],v[160:163],v[36:39]\n"
LD "v7,s[32:35],s72 offen nt lds\n"
MF "v[96:99],v[16:19],v[140:143],v[96:99]\n"
"v_exp_f32 v125,v125\n"
MF "v[100:103],v[16:19],v[144:147],v[40:43]\n"
"s_add_u32 m0,s64,31616\n"
"v_exp_f32 v126,v126\n"
MF "v[100:103],v[20:23],v[164:167],v[100:103]\n"
LD "v7,s[32:35],s72 offen offset:128 nt lds\n"
MF "v[104:107],v[20:23],v[168:171],v[44:47]\n"
"v_exp_f32 v127,v127\n"
MF "v[104:107],v[16:19],v[148:151],v[104:107]\n"
"s_add_u32 m0,s64,32512\n"
"v_add_f32 v55,v55,v124\n"
"v_add_f32 v55,v55,v125\n"
MF "v[108:111],v[16:19],v[152:155],v[48:51]\n"
LD "v7,s[32:35],s72 offen offset:256 nt lds\n"
MF "v[108:111],v[20:23],v[172:175],v[108:111]\n"
"s_waitcnt lgkmcnt(0)\n"
"ds_read_b128 v[16:19],v0 offset:51200\n"
"ds_read_b128 v[20:23],v1 offset:51200\n"
MF "v[108:111],v[24:27],v[192:195],v[108:111]\n"
CV "v86,v124,v125\n"
"v_add_f32 v55,v55,v126\n"
MF "v[108:111],v[28:31],v[212:215],v[108:111]\n"
"v_add_f32 v55,v55,v127\n"
CV "v87,v126,v127\n"
MF "v[104:107],v[28:31],v[208:211],v[104:107]\n"
"v_exp_f32 v128,v128\n"
MF "v[104:107],v[24:27],v[188:191],v[104:107]\n"
"v_exp_f32 v129,v129\n"
MF "v[100:103],v[24:27],v[184:187],v[100:103]\n"
"v_exp_f32 v130,v130\n"
MF "v[100:103],v[28:31],v[204:207],v[100:103]\n"
"v_exp_f32 v131,v131\n"
MF "v[96:99],v[28:31],v[200:203],v[96:99]\n"
"v_add_f32 v56,v56,v128\n"
"v_add_f32 v56,v56,v129\n"
MF "v[96:99],v[24:27],v[180:183],v[96:99]\n"
CV "v90,v128,v129\n"
"v_add_f32 v56,v56,v130\n"
MF "v[92:95],v[24:27],v[176:179],v[92:95]\n"
"v_add_f32 v56,v56,v131\n"
CV "v91,v130,v131\n"
MF "v[92:95],v[28:31],v[196:199],v[92:95]\n"
"s_waitcnt lgkmcnt(0)\n"
"ds_read_b128 v[24:27],v0 offset:55296\n"
"ds_read_b128 v[28:31],v1 offset:55296\n"
MF "v[92:95],v[16:19],v[216:219],v[92:95]\n"
MF "v[92:95],v[20:23],v[236:239],v[92:95]\n"
MF "v[96:99],v[20:23],v[240:243],v[96:99]\n"
MF "v[96:99],v[16:19],v[220:223],v[96:99]\n"
MF "v[100:103],v[16:19],v[224:227],v[100:103]\n"
MF "v[100:103],v[20:23],v[244:247],v[100:103]\n"
MF "v[104:107],v[20:23],v[248:251],v[104:107]\n"
MF "v[104:107],v[16:19],v[228:231],v[104:107]\n"
"v_max3_f32 v10,v52,v53,v54\n"
"v_max3_f32 v10,v10,v55,v56\n"
MF "v[108:111],v[16:19],v[232:235],v[108:111]\n"
"v_cmp_ngt_f32 vcc,s77,v10\n"
MF "v[108:111],v[20:23],v[252:255],v[108:111]\n"
"s_waitcnt lgkmcnt(0)\n"
"s_cbranch_vccnz .Lstub_21\n"
".Lback_21:\n"
MF "v[112:115],v[24:27],v[136:139],v[32:35]\n"
"s_add_u32 s74,s74,12288\n"
"ds_read_b128 v[16:19],v0 offset:56320\n"
"ds_read_b128 v[20:23],v1 offset:56320\n"
MF "v[112:115],v[28:31],v[156:159],v[112:115]\n"
"s_cmp_eq_u32 s74,s20\n"
"s_cselect_b32 s74,s70,s74\n"
MF "v[116:119],v[28:31],v[160:163],v[36:39]\n"
"v_exp_f32 v92,v92\n"
MF "v[116:119],v[24:27],v[140:143],v[116:119]\n"
"v_exp_f32 v93,v93\n"
MF "v[120:123],v[24:27],v[144:147],v[40:43]\n"
"v_exp_f32 v94,v94\n"
MF "v[120:123],v[28:31],v[164:167],v[120:123]\n"
"v_exp_f32 v95,v95\n"
MF "v[124:127],v[28:31],v[168:171],v[44:47]\n"
"v_add_f32 v57,v52,v92\n"
"v_add_f32 v57,v57,v93\n"
MF "v[124:127],v[24:27],v[148:151],v[124:127]\n"
CV "v92,v92,v93\n"
"v_add_f32 v57,v57,v94\n"
MF "v[128:131],v[24:27],v[152:155],v[48:51]\n"
"v_add_f32 v57,v57,v95\n"
CV "v93,v94,v95\n"
MF "v[128:131],v[28:31],v[172:175],v[128:131]\n"
"s_waitcnt lgkmcnt(0)\n"
"ds_read_b128 v[24:27],v0 offset:57344\n"
"ds_read_b128 v[28:31],v1 offset:57344\n"
MF "v[128:131],v[16:19],v[192:195],v[128:131]\n"
"v_exp_f32 v96,v96\n"
MF "v[128:131],v[20:23],v[212:215],v[128:131]\n"
"v_exp_f32 v97,v97\n"
MF "v[124:127],v[20:23],v[208:211],v[124:127]\n"
"v_exp_f32 v98,v98\n"
MF "v[124:127],v[16:19],v[188:191],v[124:127]\n"
"v_exp_f32 v99,v99\n"
MF "v[120:123],v[16:19],v[184:187],v[120:123]\n"
"v_add_f32 v58,v53,v96\n"
"v_add_f32 v58,v58,v97\n"
MF "v[120:123],v[20:23],v[204:207],v[120:123]\n"
CV "v96,v96,v97\n"
"v_add_f32 v58,v58,v98\n"
MF "v[116:119],v[20:23],v[200:203],v[116:119]\n"
"s_waitcnt vmcnt(6) lgkmcnt(0)\n"
"v_add_f32 v58,v58,v99\n"
CV "v97,v98,v99\n"
MF "v[116:119],v[16:19],v[180:183],v[116:119]\n"
"s_barrier\n"
MF "v[112:115],v[16:19],v[176:179],v[112:115]\n"
"v_exp_f32 v100,v100\n"
MF "v[112:115],v[20:23],v[196:199],v[112:115]\n"
"s_nop 0\n"
TR "v[16:17],v2\n"
TR "v[18:19],v2 offset:6144\n"
MF "v[112:115],v[24:27],v[216:219],v[112:115]\n"
"s_add_u32 s73,s73,12288\n"
TR "v[20:21],v3\n"
TR "v[22:23],v3 offset:6144\n"
MF "v[112:115],v[28:31],v[236:239],v[112:115]\n"
"s_cmp_eq_u32 s73,s19\n"
"s_cselect_b32 s73,s18,s73\n"
"s_add_u32 m0,s65,49152\n"
MF "v[116:119],v[28:31],v[240:243],v[116:119]\n"
LD "v8,s[36:39],s73 offen nt lds\n"
MF "v[116:119],v[24:27],v[220:223],v[116:119]\n"
"s_add_u32 m0,s65,50048\n"
"v_exp_f32 v101,v101\n"
MF "v[120:123],v[24:27],v[224:227],v[120:123]\n"
LD "v8,s[36:39],s73 offen offset:128 nt lds\n"
MF "v[120:123],v[28:31],v[244:247],v[120:123]\n"
"v_exp_f32 v102,v102\n"
MF "v[124:127],v[28:31],v[248:251],v[124:127]\n"
"s_add_u32 m0,s65,50944\n"
"v_exp_f32 v103,v103\n"
MF "v[124:127],v[24:27],v[228:231],v[124:127]\n"
LD "v8,s[36:39],s73 offen offset:256 nt lds\n"
MF "v[128:131],v[24:27],v[232:235],v[128:131]\n"
"v_add_f32 v59,v54,v100\n"
"v_add_f32 v59,v59,v101\n"
MF "v[128:131],v[28:31],v[252:255],v[128:131]\n"
"s_waitcnt lgkmcnt(0)\n"
TR "v[24:25],v4\n"
TR "v[26:27],v4 offset:6144\n"
MF "a[0:3],v[16:19],v[72:75],a[0:3]\n"
"s_add_u32 m0,s65,49152\n"
TR "v[28:29],v5\n"
TR "v[30:31],v5 offset:6144\n"
MF "a[20:23],v[20:23],v[72:75],a[20:23]\n"
LD "v8,s[36:39],s73 offen offset:3072 nt lds\n"
MF "a[24:27],v[20:23],v[76:79],a[24:27]\n"
CV "v100,v100,v101\n"
"v_add_f32 v59,v59,v102\n"
MF "a[4:7],v[16:19],v[76:79],a[4:7]\n"
"s_add_u32 m0,s65,50048\n"
"v_add_f32 v59,v59,v103\n"
CV "v101,v102,v103\n"
MF "a[8:11],v[16:19],v[80:83],a[8:11]\n"
LD "v8,s[36:39],s73 offen offset:3200 nt lds\n"
MF "a[28:31],v[20:23],v[80:83],a[28:31]\n"
"v_exp_f32 v104,v104\n"
MF "a[32:35],v[20:23],v[84:87],a[32:35]\n"
"s_add_u32 m0,s65,50944\n"
"v_exp_f32 v105,v105\n"
MF "a[12:15],v[16:19],v[84:87],a[12:15]\n"
LD "v8,s[36:39],s73 offen offset:3328 nt lds\n"
MF "a[16:19],v[16:19],v[88:91],a[16:19]\n"
"v_exp_f32 v106,v106\n"
MF "a[36:39],v[20:23],v[88:91],a[36:39]\n"
"s_waitcnt lgkmcnt(0)\n"
TR "v[16:17],v2 offset:1024\n"
TR "v[18:19],v2 offset:7168\n"
MF "a[56:59],v[24:27],v[88:91],a[56:59]\n"
"s_nop 0\n"
TR "v[20:21],v3 offset:1024\n"
TR "v[22:23],v3 offset:7168\n"
MF "a[76:79],v[28:31],v[88:91],a[76:79]\n"
"v_exp_f32 v107,v107\n"
MF "a[72:75],v[28:31],v[84:87],a[72:75]\n"
"v_add_f32 v60,v55,v104\n"
"v_add_f32 v60,v60,v105\n"
MF "a[52:55],v[24:27],v[84:87],a[52:55]\n"
CV "v104,v104,v105\n"
"v_add_f32 v60,v60,v106\n"
MF "a[48:51],v[24:27],v[80:83],a[48:51]\n"
"v_add_f32 v60,v60,v107\n"
CV "v105,v106,v107\n"
MF "a[68:71],v[28:31],v[80:83],a[68:71]\n"
"v_exp_f32 v108,v108\n"
MF "a[64:67],v[28:31],v[76:79],a[64:67]\n"
"v_exp_f32 v109,v109\n"
MF "a[44:47],v[24:27],v[76:79],a[44:47]\n"
"v_exp_f32 v110,v110\n"
MF "a[40:43],v[24:27],v[72:75],a[40:43]\n"
"v_exp_f32 v111,v111\n"
MF "a[60:63],v[28:31],v[72:75],a[60:63]\n"
"s_waitcnt lgkmcnt(0)\n"
TR "v[24:25],v4 offset:1024\n"
TR "v[26:27],v4 offset:7168\n"
MF "a[80:83],v[16:19],v[72:75],a[80:83]\n"
"s_nop 0\n"
TR "v[28:29],v5 offset:1024\n"
TR "v[30:31],v5 offset:7168\n"
MF "a[100:103],v[20:23],v[72:75],a[100:103]\n"
"v_add_f32 v61,v56,v108\n"
"v_add_f32 v61,v61,v109\n"
MF "a[104:107],v[20:23],v[76:79],a[104:107]\n"
CV "v108,v108,v109\n"
"v_add_f32 v61,v61,v110\n"
MF "a[84:87],v[16:19],v[76:79],a[84:87]\n"
"v_add_f32 v61,v61,v111\n"
CV "v109,v110,v111\n"
MF "a[88:91],v[16:19],v[80:83],a[88:91]\n"
"v_exp_f32 v112,v112\n"
MF "a[108:111],v[20:23],v[80:83],a[108:111]\n"
"v_exp_f32 v113,v113\n"
MF "a[112:115],v[20:23],v[84:87],a[112:115]\n"
"v_exp_f32 v114,v114\n"
MF "a[92:95],v[16:19],v[84:87],a[92:95]\n"
"v_exp_f32 v115,v115\n"
MF "a[96:99],v[16:19],v[88:91],a[96:99]\n"
"v_add_f32 v57,v57,v112\n"
"v_add_f32 v57,v57,v113\n"
MF "a[116:119],v[20:23],v[88:91],a[116:119]\n"
"s_waitcnt lgkmcnt(0)\n"
TR "v[16:17],v2 offset:2048\n"
TR "v[18:19],v2 offset:8192\n"
MF "a[136:139],v[24:27],v[88:91],a[136:139]\n"
"s_nop 0\n"
TR "v[20:21],v3 offset:2048\n"
TR "v[22:23],v3 offset:8192\n"
MF "a[156:159],v[28:31],v[88:91],a[156:159]\n"
CV "v94,v112,v113\n"
"v_add_f32 v57,v57,v114\n"
MF "a[152:155],v[28:31],v[84:87],a[152:155]\n"
"v_add_f32 v57,v57,v115\n"
CV "v95,v114,v115\n"
MF "a[132:135],v[24:27],v[84:87],a[132:135]\n"
"v_exp_f32 v116,v116\n"
MF "a[128:131],v[24:27],v[80:83],a[128:131]\n"
"v_exp_f32 v117,v117\n"
MF "a[148:151],v[28:31],v[80:83],a[148:151]\n"
"v_exp_f32 v118,v118\n"
MF "a[144:147],v[28:31],v[76:79],a[144:147]\n"
"v_exp_f32 v119,v119\n"
MF "a[124:127],v[24:27],v[76:79],a[124:127]\n"
"v_add_f32 v58,v58,v116\n"
"v_add_f32 v58,v58,v117\n"
MF "a[120:123],v[24:27],v[72:75],a[120:123]\n"
CV "v98,v116,v117\n"
"v_add_f32 v58,v58,v118\n"
MF "a[140:143],v[28:31],v[72:75],a[140:143]\n"
"s_waitcnt lgkmcnt(0)\n"
TR "v[24:25],v4 offset:2048\n"
TR "v[26:27],v4 offset:8192\n"
MF "a[160:163],v[16:19],v[72:75],a[160:163]\n"
"s_nop 0\n"
TR "v[28:29],v5 offset:2048\n"
TR "v[30:31],v5 offset:8192\n"
MF "a[180:183],v[20:23],v[72:75],a[180:183]\n"
"v_add_f32 v58,v58,v119\n"
CV "v99,v118,v119\n"
MF "a[184:187],v[20:23],v[76:79],a[184:187]\n"
"v_exp_f32 v120,v120\n"
MF "a[164:167],v[16:19],v[76:79],a[164:167]\n"
"v_exp_f32 v121,v121\n"
MF "a[168:171],v[16:19],v[80:83],a[168:171]\n"
"v_exp_f32 v122,v122\n"
MF "a[188:191],v[20:23],v[80:83],a[188:191]\n"
"v_exp_f32 v123,v123\n"
MF "a[192:195],v[20:23],v[84:87],a[192:195]\n"
"v_add_f32 v59,v59,v120\n"
"v_add_f32 v59,v59,v121\n"
MF "a[172:175],v[16:19],v[84:87],a[172:175]\n"
"s_waitcnt vmcnt(6) lgkmcnt(0)\n"
CV "v102,v120,v121\n"
"v_add_f32 v59,v59,v122\n"
MF "a[176:179],v[16:19],v[88:91],a[176:179]\n"
"s_barrier\n"
MF "a[196:199],v[20:23],v[88:91],a[196:199]\n"
"s_add_u32 s72,s72,6144\n"
"ds_read_b128 v[16:19],v0 offset:24576\n"
"ds_read_b128 v[20:23],v1 offset:24576\n"
MF "a[216:219],v[24:27],v[88:91],a[216:219]\n"
"s_cmp_eq_u32 s72,s17\n"
"s_cselect_b32 s72,s16,s72\n"
"s_add_u32 m0,s64,0\n"
MF "a[236:239],v[28:31],v[88:91],a[236:239]\n"
LD "v7,s[32:35],s72 offen nt lds\n"
MF "a[232:235],v[28:31],v[84:87],a[232:235]\n"
"s_add_u32 m0,s64,896\n"
"v_add_f32 v59,v59,v123\n"
CV "v103,v122,v123\n"
MF "a[212:215],v[24:27],v[84:87],a[212:215]\n"
LD "v7,s[32:35],s72 offen offset:128 nt lds\n"
MF "a[208:211],v[24:27],v[80:83],a[208:211]\n"
"s_add_u32 m0,s64,1792\n"
MF "a[228:231],v[28:31],v[80:83],a[228:231]\n"
LD "v7,s[32:35],s72 offen offset:256 nt lds\n"
MF "a[224:227],v[28:31],v[76:79],a[224:227]\n"
MF "a[204:207],v[24:27],v[76:79],a[204:207]\n"
MF "a[200:203],v[24:27],v[72:75],a[200:203]\n"
MF "a[220:223],v[28:31],v[72:75],a[220:223]\n"
"s_waitcnt lgkmcnt(0)\n"
MF "v[72:75],v[16:19],v[136:139],v[32:35]\n"
"s_add_u32 s72,s72,6144\n"
"ds_read_b128 v[24:27],v0 offset:25600\n"
"ds_read_b128 v[28:31],v1 offset:25600\n"
MF "v[72:75],v[20:23],v[156:159],v[72:75]\n"
"s_add_u32 m0,s64,6144\n"
"v_exp_f32 v124,v124\n"
MF "v[76:79],v[20:23],v[160:163],v[36:39]\n"
LD "v7,s[32:35],s72 offen nt lds\n"
MF "v[76:79],v[16:19],v[140:143],v[76:79]\n"
"v_exp_f32 v125,v125\n"
MF "v[80:83],v[16:19],v[144:147],v[40:43]\n"
"s_add_u32 m0,s64,7040\n"
"v_exp_f32 v126,v126\n"
MF "v[80:83],v[20:23],v[164:167],v[80:83]\n"
LD "v7,s[32:35],s72 offen offset:128 nt lds\n"
MF "v[84:87],v[20:23],v[168:171],v[44:47]\n"
"v_exp_f32 v127,v127\n"
MF "v[84:87],v[16:19],v[148:151],v[84:87]\n"
"s_add_u32 m0,s64,7936\n"
"v_add_f32 v60,v60,v124\n"
"v_add_f32 v60,v60,v125\n"
MF "v[88:91],v[16:19],v[152:155],v[48:51]\n"
LD "v7,s[32:35],s72 offen offset:256 nt lds\n"
MF "v[88:91],v[20:23],v[172:175],v[88:91]\n"
"s_waitcnt lgkmcnt(0)\n"
"ds_read_b128 v[16:19],v0 offset:26624\n"
"ds_read_b128 v[20:23],v1 offset:26624\n"
MF "v[88:91],v[24:27],v[192:195],v[88:91]\n"
CV "v106,v124,v125\n"
"v_add_f32 v60,v60,v126\n"
MF "v[88:91],v[28:31],v[212:215],v[88:91]\n"
"v_add_f32 v60,v60,v127\n"
CV "v107,v126,v127\n"
MF "v[84:87],v[28:31],v[208:211],v[84:87]\n"
"v_exp_f32 v128,v128\n"
MF "v[84:87],v[24:27],v[188:191],v[84:87]\n"
"v_exp_f32 v129,v129\n"
MF "v[80:83],v[24:27],v[184:187],v[80:83]\n"
"v_exp_f32 v130,v130\n"
MF "v[80:83],v[28:31],v[204:207],v[80:83]\n"
"v_exp_f32 v131,v131\n"
MF "v[76:79],v[28:31],v[200:203],v[76:79]\n"
"v_add_f32 v61,v61,v128\n"
"v_add_f32 v61,v61,v129\n"
MF "v[76:79],v[24:27],v[180:183],v[76:79]\n"
CV "v110,v128,v129\n"
"v_add_f32 v61,v61,v130\n"
MF "v[72:75],v[24:27],v[176:179],v[72:75]\n"
"v_add_f32 v61,v61,v131\n"
CV "v111,v130,v131\n"
MF "v[72:75],v[28:31],v[196:199],v[72:75]\n"
"s_waitcnt lgkmcnt(0)\n"
"ds_read_b128 v[24:27],v0 offset:30720\n"
"ds_read_b128 v[28:31],v1 offset:30720\n"
MF "v[72:75],v[16:19],v[216:219],v[72:75]\n"
MF "v[72:75],v[20:23],v[236:239],v[72:75]\n"
MF "v[76:79],v[20:23],v[240:243],v[76:79]\n"
MF "v[76:79],v[16:19],v[220:223],v[76:79]\n"
MF "v[80:83],v[16:19],v[224:227],v[80:83]\n"
MF "v[80:83],v[20:23],v[244:247],v[80:83]\n"
MF "v[84:87],v[20:23],v[248:251],v[84:87]\n"
MF "v[84:87],v[16:19],v[228:231],v[84:87]\n"
"v_max3_f32 v10,v57,v58,v59\n"
"v_max3_f32 v10,v10,v60,v61\n"
MF "v[88:91],v[16:19],v[232:235],v[88:91]\n"
"v_cmp_ngt_f32 vcc,s77,v10\n"
MF "v[88:91],v[20:23],v[252:255],v[88:91]\n"
"s_waitcnt lgkmcnt(0)\n"
"s_cbranch_vccnz .Lstub_22\n"
".Lback_22:\n"
MF "v[112:115],v[24:27],v[136:139],v[32:35]\n"
"s_add_u32 s74,s74,12288\n"
"ds_read_b128 v[16:19],v0 offset:31744\n"
"ds_read_b128 v[20:23],v1 offset:31744\n"
MF "v[112:115],v[28:31],v[156:159],v[112:115]\n"
"s_cmp_eq_u32 s74,s20\n"
"s_cselect_b32 s74,s70,s74\n"
MF "v[116:119],v[28:31],v[160:163],v[36:39]\n"
"v_exp_f32 v72,v72\n"
MF "v[116:119],v[24:27],v[140:143],v[116:119]\n"
"v_exp_f32 v73,v73\n"
MF "v[120:123],v[24:27],v[144:147],v[40:43]\n"
"v_exp_f32 v74,v74\n"
MF "v[120:123],v[28:31],v[164:167],v[120:123]\n"
"v_exp_f32 v75,v75\n"
MF "v[124:127],v[28:31],v[168:171],v[44:47]\n"
"v_add_f32 v52,v57,v72\n"
"v_add_f32 v52,v52,v73\n"
MF "v[124:127],v[24:27],v[148:151],v[124:127]\n"
CV "v72,v72,v73\n"
"v_add_f32 v52,v52,v74\n"
MF "v[128:131],v[24:27],v[152:155],v[48:51]\n"
"v_add_f32 v52,v52,v75\n"
CV "v73,v74,v75\n"
MF "v[128:131],v[28:31],v[172:175],v[128:131]\n"
"s_waitcnt lgkmcnt(0)\n"
"ds_read_b128 v[24:27],v0 offset:32768\n"
"ds_read_b128 v[28:31],v1 offset:32768\n"
MF "v[128:131],v[16:19],v[192:195],v[128:131]\n"
"v_exp_f32 v76,v76\n"
MF "v[128:131],v[20:23],v[212:215],v[128:131]\n"
"v_exp_f32 v77,v77\n"
MF "v[124:127],v[20:23],v[208:211],v[124:127]\n"
"v_exp_f32 v78,v78\n"
MF "v[124:127],v[16:19],v[188:191],v[124:127]\n"
"v_exp_f32 v79,v79\n"
MF "v[120:123],v[16:19],v[184:187],v[120:123]\n"
"v_add_f32 v53,v58,v76\n"
"v_add_f32 v53,v53,v77\n"
MF "v[120:123],v[20:23],v[204:207],v[120:123]\n"
CV "v76,v76,v77\n"
"v_add_f32 v53,v53,v78\n"
MF "v[116:119],v[20:23],v[200:203],v[116:119]\n"
"s_waitcnt vmcnt(6) lgkmcnt(0)\n"
"v_add_f32 v53,v53,v79\n"
CV "v77,v78,v79\n"
MF "v[116:119],v[16:19],v[180:183],v[116:119]\n"
"s_barrier\n"
MF "v[112:115],v[16:19],v[176:179],v[112:115]\n"
"v_exp_f32 v80,v80\n"
MF "v[112:115],v[20:23],v[196:199],v[112:115]\n"
"s_nop 0\n"
TR "v[16:17],v2 offset:49152\n"
TR "v[18:19],v2 offset:55296\n"
MF "v[112:115],v[24:27],v[216:219],v[112:115]\n"
"s_add_u32 s73,s73,12288\n"
TR "v[20:21],v3 offset:49152\n"
TR "v[22:23],v3 offset:55296\n"
MF "v[112:115],v[28:31],v[236:239],v[112:115]\n"
"s_cmp_eq_u32 s73,s19\n"
"s_cselect_b32 s73,s18,s73\n"
"s_add_u32 m0,s65,24576\n"
MF "v[116:119],v[28:31],v[240:243],v[116:119]\n"
LD "v8,s[36:39],s73 offen nt lds\n"
MF "v[116:119],v[24:27],v[220:223],v[116:119]\n"
"s_add_u32 m0,s65,25472\n"
"v_exp_f32 v81,v81\n"
MF "v[120:123],v[24:27],v[224:227],v[120:123]\n"
LD "v8,s[36:39],s73 offen offset:128 nt lds\n"
MF "v[120:123],v[28:31],v[244:247],v[120:123]\n"
"v_exp_f32 v82,v82\n"
MF "v[124:127],v[28:31],v[248:251],v[124:127]\n"
"s_add_u32 m0,s65,26368\n"
"v_exp_f32 v83,v83\n"
MF "v[124:127],v[24:27],v[228:231],v[124:127]\n"
LD "v8,s[36:39],s73 offen offset:256 nt lds\n"
MF "v[128:131],v[24:27],v[232:235],v[128:131]\n"
"v_add_f32 v54,v59,v80\n"
"v_add_f32 v54,v54,v81\n"
MF "v[128:131],v[28:31],v[252:255],v[128:131]\n"
"s_waitcnt lgkmcnt(0)\n"
TR "v[24:25],v4 offset:49152\n"
TR "v[26:27],v4 offset:55296\n"
MF "a[0:3],v[16:19],v[92:95],a[0:3]\n"
"s_add_u32 m0,s65,24576\n"
TR "v[28:29],v5 offset:49152\n"
TR "v[30:31],v5 offset:55296\n"
MF "a[20:23],v[20:23],v[92:95],a[20:23]\n"
LD "v8,s[36:39],s73 offen offset:3072 nt lds\n"
MF "a[24:27],v[20:23],v[96:99],a[24:27]\n"
CV "v80,v80,v81\n"
"v_add_f32 v54,v54,v82\n"
MF "a[4:7],v[16:19],v[96:99],a[4:7]\n"
"s_add_u32 m0,s65,25472\n"
"v_add_f32 v54,v54,v83\n"
CV "v81,v82,v83\n"
MF "a[8:11],v[16:19],v[100:103],a[8:11]\n"
LD "v8,s[36:39],s73 offen offset:3200 nt lds\n"
MF "a[28:31],v[20:23],v[100:103],a[28:31]\n"
"v_exp_f32 v84,v84\n"
MF "a[32:35],v[20:23],v[104:107],a[32:35]\n"
"s_add_u32 m0,s65,26368\n"
"v_exp_f32 v85,v85\n"
MF "a[12:15],v[16:19],v[104:107],a[12:15]\n"
LD "v8,s[36:39],s73 offen offset:3328 nt lds\n"
MF "a[16:19],v[16:19],v[108:111],a[16:19]\n"
"v_exp_f32 v86,v86\n"
MF "a[36:39],v[20:23],v[108:111],a[36:39]\n"
"s_waitcnt lgkmcnt(0)\n"
TR "v[16:17],v2 offset:50176\n"
TR "v[18:19],v2 offset:56320\n"
MF "a[56:59],v[24:27],v[108:111],a[56:59]\n"
"s_nop 0\n"
TR "v[20:21],v3 offset:50176\n"
TR "v[22:23],v3 offset:56320\n"
MF "a[76:79],v[28:31],v[108:111],a[76:79]\n"
"v_exp_f32 v87,v87\n"
MF "a[72:75],v[28:31],v[104:107],a[72:75]\n"
"v_add_f32 v55,v60,v84\n"
"v_add_f32 v55,v55,v85\n"
MF "a[52:55],v[24:27],v[104:107],a[52:55]\n"
CV "v84,v84,v85\n"
"v_add_f32 v55,v55,v86\n"
MF "a[48:51],v[24:27],v[100:103],a[48:51]\n"
"v_add_f32 v55,v55,v87\n"
CV "v85,v86,v87\n"
MF "a[68:71],v[28:31],v[100:103],a[68:71]\n"
"v_exp_f32 v88,v88\n"
MF "a[64:67],v[28:31],v[96:99],a[64:67]\n"
"v_exp_f32 v89,v89\n"
MF "a[44:47],v[24:27],v[96:99],a[44:47]\n"
"v_exp_f32 v90,v90\n"
MF "a[40:43],v[24:27],v[92:95],a[40:43]\n"
"v_exp_f32 v91,v91\n"
MF "a[60:63],v[28:31],v[92:95],a[60:63]\n"
"s_waitcnt lgkmcnt(0)\n"
TR "v[24:25],v4 offset:50176\n"
TR "v[26:27],v4 offset:56320\n"
MF "a[80:83],v[16:19],v[92:95],a[80:83]\n"
"s_nop 0\n"
TR "v[28:29],v5 offset:50176\n"
TR "v[30:31],v5 offset:56320\n"
MF "a[100:103],v[20:23],v[92:95],a[100:103]\n"
"v_add_f32 v56,v61,v88\n"
"v_add_f32 v56,v56,v89\n"
MF "a[104:107],v[20:23],v[96:99],a[104:107]\n"
CV "v88,v88,v89\n"
"v_add_f32 v56,v56,v90\n"
MF "a[84:87],v[16:19],v[96:99],a[84:87]\n"
"v_add_f32 v56,v56,v91\n"
CV "v89,v90,v91\n"
MF "a[88:91],v[16:19],v[100:103],a[88:91]\n"
"v_exp_f32 v112,v112\n"
MF "a[108:111],v[20:23],v[100:103],a[108:111]\n"
"v_exp_f32 v113,v113\n"
MF "a[112:115],v[20:23],v[104:107],a[112:115]\n"
"v_exp_f32 v114,v114\n"
MF "a[92:95],v[16:19],v[104:107],a[92:95]\n"
"v_exp_f32 v115,v115\n"
MF "a[96:99],v[16:19],v[108:111],a[96:99]\n"
"v_add_f32 v52,v52,v112\n"
"v_add_f32 v52,v52,v113\n"
MF "a[116:119],v[20:23],v[108:111],a[116:119]\n"
"s_waitcnt lgkmcnt(0)\n"
TR "v[16:17],v2 offset:51200\n"
TR "v[18:19],v2 offset:57344\n"
MF "a[136:139],v[24:27],v[108:111],a[136:139]\n"
"s_nop 0\n"
TR "v[20:21],v3 offset:51200\n"
TR "v[22:23],v3 offset:57344\n"
MF "a[156:159],v[28:31],v[108:111],a[156:159]\n"
CV "v74,v112,v113\n"
"v_add_f32 v52,v52,v114\n"
MF "a[152:155],v[28:31],v[104:107],a[152:155]\n"
"v_add_f32 v52,v52,v115\n"
CV "v75,v114,v115\n"
MF "a[132:135],v[24:27],v[104:107],a[132:135]\n"
"v_exp_f32 v116,v116\n"
MF "a[128:131],v[24:27],v[100:103],a[128:131]\n"
"v_exp_f32 v117,v117\n"
MF "a[148:151],v[28:31],v[100:103],a[148:151]\n"
"v_exp_f32 v118,v118\n"
MF "a[144:147],v[28:31],v[96:99],a[144:147]\n"
"v_exp_f32 v119,v119\n"
MF "a[124:127],v[24:27],v[96:99],a[124:127]\n"
"v_add_f32 v53,v53,v116\n"
"v_add_f32 v53,v53,v117\n"
MF "a[120:123],v[24:27],v[92:95],a[120:123]\n"
CV "v78,v116,v117\n"
"v_add_f32 v53,v53,v118\n"
MF "a[140:143],v[28:31],v[92:95],a[140:143]\n"
"s_waitcnt lgkmcnt(0)\n"
TR "v[24:25],v4 offset:51200\n"
TR "v[26:27],v4 offset:57344\n"
MF "a[160:163],v[16:19],v[92:95],a[160:163]\n"
"s_nop 0\n"
TR "v[28:29],v5 offset:51200\n"
TR "v[30:31],v5 offset:57344\n"
MF "a[180:183],v[20:23],v[92:95],a[180:183]\n"
"v_add_f32 v53,v53,v119\n"
CV "v79,v118,v119\n"
MF "a[184:187],v[20:23],v[96:99],a[184:187]\n"
"v_exp_f32 v120,v120\n"
MF "a[164:167],v[16:19],v[96:99],a[164:167]\n"
"v_exp_f32 v121,v121\n"
MF "a[168:171],v[16:19],v[100:103],a[168:171]\n"
"v_exp_f32 v122,v122\n"
MF "a[188:191],v[20:23],v[100:103],a[188:191]\n"
"v_exp_f32 v123,v123\n"
MF "a[192:195],v[20:23],v[104:107],a[192:195]\n"
"v_add_f32 v54,v54,v120\n"
"v_add_f32 v54,v54,v121\n"
MF "a[172:175],v[16:19],v[104:107],a[172:175]\n"
"s_waitcnt vmcnt(6) lgkmcnt(0)\n"
CV "v82,v120,v121\n"
"v_add_f32 v54,v54,v122\n"
MF "a[176:179],v[16:19],v[108:111],a[176:179]\n"
"s_barrier\n"
MF "a[196:199],v[20:23],v[108:111],a[196:199]\n"
"s_add_u32 s72,s72,6144\n"
"ds_read_b128 v[16:19],v0\n"
"ds_read_b128 v[20:23],v1\n"
MF "a[216:219],v[24:27],v[108:111],a[216:219]\n"
"s_cmp_eq_u32 s72,s17\n"
"s_cselect_b32 s72,s16,s72\n"
"s_add_u32 m0,s64,49152\n"
MF "a[236:239],v[28:31],v[108:111],a[236:239]\n"
LD "v7,s[32:35],s72 offen nt lds\n"
MF "a[232:235],v[28:31],v[104:107],a[232:235]\n"
"s_add_u32 m0,s64,50048\n"
"v_add_f32 v54,v54,v123\n"
CV "v83,v122,v123\n"
MF "a[212:215],v[24:27],v[104:107],a[212:215]\n"
LD "v7,s[32:35],s72 offen offset:128 nt lds\n"
MF "a[208:211],v[24:27],v[100:103],a[208:211]\n"
"s_add_u32 m0,s64,50944\n"
MF "a[228:231],v[28:31],v[100:103],a[228:231]\n"
LD "v7,s[32:35],s72 offen offset:256 nt lds\n"
MF "a[224:227],v[28:31],v[96:99],a[224:227]\n"
MF "a[204:207],v[24:27],v[96:99],a[204:207]\n"
MF "a[200:203],v[24:27],v[92:95],a[200:203]\n"
MF "a[220:223],v[28:31],v[92:95],a[220:223]\n"
"s_waitcnt lgkmcnt(0)\n"
MF "v[92:95],v[16:19],v[136:139],v[32:35]\n"
"s_add_u32 s72,s72,6144\n"
"ds_read_b128 v[24:27],v0 offset:1024\n"
"ds_read_b128 v[28:31],v1 offset:1024\n"
MF "v[92:95],v[20:23],v[156:159],v[92:95]\n"
"s_add_u32 m0,s64,55296\n"
"v_exp_f32 v124,v124\n"
MF "v[96:99],v[20:23],v[160:163],v[36:39]\n"
LD "v7,s[32:35],s72 offen nt lds\n"
MF "v[96:99],v[16:19],v[140:143],v[96:99]\n"
"v_exp_f32 v125,v125\n"
MF "v[100:103],v[16:19],v[144:147],v[40:43]\n"
"s_add_u32 m0,s64,56192\n"
"v_exp_f32 v126,v126\n"
MF "v[100:103],v[20:23],v[164:167],v[100:103]\n"
LD "v7,s[32:35],s72 offen offset:128 nt lds\n"
MF "v[104:107],v[20:23],v[168:171],v[44:47]\n"
"v_exp_f32 v127,v127\n"
MF "v[104:107],v[16:19],v[148:151],v[104:107]\n"
"s_add_u32 m0,s64,57088\n"
"v_add_f32 v55,v55,v124\n"
"v_add_f32 v55,v55,v125\n"
MF "v[108:111],v[16:19],v[152:155],v[48:51]\n"
LD "v7,s[32:35],s72 offen offset:256 nt lds\n"
MF "v[108:111],v[20:23],v[172:175],v[108:111]\n"
"s_waitcnt lgkmcnt(0)\n"
"ds_read_b128 v[16:19],v0 offset:2048\n"
"ds_read_b128 v[20:23],v1 offset:2048\n"
MF "v[108:111],v[24:27],v[192:195],v[108:111]\n"
CV "v86,v124,v125\n"
"v_add_f32 v55,v55,v126\n"
MF "v[108:111],v[28:31],v[212:215],v[108:111]\n"
"v_add_f32 v55,v55,v127\n"
CV "v87,v126,v127\n"
MF "v[104:107],v[28:31],v[208:211],v[104:107]\n"
"v_exp_f32 v128,v128\n"
MF "v[104:107],v[24:27],v[188:191],v[104:107]\n"
"v_exp_f32 v129,v129\n"
MF "v[100:103],v[24:27],v[184:187],v[100:103]\n"
"v_exp_f32 v130,v130\n"
MF "v[100:103],v[28:31],v[204:207],v[100:103]\n"
"v_exp_f32 v131,v131\n"
MF "v[96:99],v[28:31],v[200:203],v[96:99]\n"
"v_add_f32 v56,v56,v128\n"
"v_add_f32 v56,v56,v129\n"
MF "v[96:99],v[24:27],v[180:183],v[96:99]\n"
CV "v90,v128,v129\n"
"v_add_f32 v56,v56,v130\n"
MF "v[92:95],v[24:27],v[176:179],v[92:95]\n"
"v_add_f32 v56,v56,v131\n"
CV "v91,v130,v131\n"
MF "v[92:95],v[28:31],v[196:199],v[92:95]\n"
"s_waitcnt lgkmcnt(0)\n"
"ds_read_b128 v[24:27],v0 offset:6144\n"
"ds_read_b128 v[28:31],v1 offset:6144\n"
MF "v[92:95],v[16:19],v[216:219],v[92:95]\n"
MF "v[92:95],v[20:23],v[236:239],v[92:95]\n"
MF "v[96:99],v[20:23],v[240:243],v[96:99]\n"
MF "v[96:99],v[16:19],v[220:223],v[96:99]\n"
MF "v[100:103],v[16:19],v[224:227],v[100:103]\n"
MF "v[100:103],v[20:23],v[244:247],v[100:103]\n"
MF "v[104:107],v[20:23],v[248:251],v[104:107]\n"
MF "v[104:107],v[16:19],v[228:231],v[104:107]\n"
"v_max3_f32 v10,v52,v53,v54\n"
"v_max3_f32 v10,v10,v55,v56\n"
MF "v[108:111],v[16:19],v[232:235],v[108:111]\n"
"v_cmp_ngt_f32 vcc,s77,v10\n"
MF "v[108:111],v[20:23],v[252:255],v[108:111]\n"
"s_waitcnt lgkmcnt(0)\n"
"s_cbranch_vccnz .Lstub_23\n"
".Lback_23:\n"
MF "v[112:115],v[24:27],v[136:139],v[32:35]\n"
"s_add_u32 s74,s74,12288\n"
"ds_read_b128 v[16:19],v0 offset:7168\n"
"ds_read_b128 v[20:23],v1 offset:7168\n"
MF "v[112:115],v[28:31],v[156:159],v[112:115]\n"
"s_cmp_eq_u32 s74,s20\n"
"s_cselect_b32 s74,s70,s74\n"
MF "v[116:119],v[28:31],v[160:163],v[36:39]\n"
"v_exp_f32 v92,v92\n"
MF "v[116:119],v[24:27],v[140:143],v[116:119]\n"
"v_exp_f32 v93,v93\n"
MF "v[120:123],v[24:27],v[144:147],v[40:43]\n"
"v_exp_f32 v94,v94\n"
MF "v[120:123],v[28:31],v[164:167],v[120:123]\n"
"v_exp_f32 v95,v95\n"
MF "v[124:127],v[28:31],v[168:171],v[44:47]\n"
"v_add_f32 v57,v52,v92\n"
"v_add_f32 v57,v57,v93\n"
MF "v[124:127],v[24:27],v[148:151],v[124:127]\n"
CV "v92,v92,v93\n"
"v_add_f32 v57,v57,v94\n"
MF "v[128:131],v[24:27],v[152:155],v[48:51]\n"
"v_add_f32 v57,v57,v95\n"
CV "v93,v94,v95\n"
MF "v[128:131],v[28:31],v[172:175],v[128:131]\n"
"s_waitcnt lgkmcnt(0)\n"
"ds_read_b128 v[24:27],v0 offset:8192\n"
"ds_read_b128 v[28:31],v1 offset:8192\n"
MF "v[128:131],v[16:19],v[192:195],v[128:131]\n"
"v_exp_f32 v96,v96\n"
MF "v[128:131],v[20:23],v[212:215],v[128:131]\n"
"v_exp_f32 v97,v97\n"
MF "v[124:127],v[20:23],v[208:211],v[124:127]\n"
"v_exp_f32 v98,v98\n"
MF "v[124:127],v[16:19],v[188:191],v[124:127]\n"
"v_exp_f32 v99,v99\n"
MF "v[120:123],v[16:19],v[184:187],v[120:123]\n"
"v_add_f32 v58,v53,v96\n"
"v_add_f32 v58,v58,v97\n"
MF "v[120:123],v[20:23],v[204:207],v[120:123]\n"
CV "v96,v96,v97\n"
"v_add_f32 v58,v58,v98\n"
MF "v[116:119],v[20:23],v[200:203],v[116:119]\n"
"s_waitcnt vmcnt(6) lgkmcnt(0)\n"
"v_add_f32 v58,v58,v99\n"
CV "v97,v98,v99\n"
MF "v[116:119],v[16:19],v[180:183],v[116:119]\n"
"s_barrier\n"
MF "v[112:115],v[16:19],v[176:179],v[112:115]\n"
"v_exp_f32 v100,v100\n"
MF "v[112:115],v[20:23],v[196:199],v[112:115]\n"
"s_nop 0\n"
TR "v[16:17],v2 offset:24576\n"
TR "v[18:19],v2 offset:30720\n"
MF "v[112:115],v[24:27],v[216:219],v[112:115]\n"
"s_add_u32 s73,s73,12288\n"
TR "v[20:21],v3 offset:24576\n"
TR "v[22:23],v3 offset:30720\n"
MF "v[112:115],v[28:31],v[236:239],v[112:115]\n"
"s_cmp_eq_u32 s73,s19\n"
"s_cselect_b32 s73,s18,s73\n"
"s_add_u32 m0,s65,0\n"
MF "v[116:119],v[28:31],v[240:243],v[116:119]\n"
LD "v8,s[36:39],s73 offen nt lds\n"
MF "v[116:119],v[24:27],v[220:223],v[116:119]\n"
"s_add_u32 m0,s65,896\n"
"v_exp_f32 v101,v101\n"
MF "v[120:123],v[24:27],v[224:227],v[120:123]\n"
LD "v8,s[36:39],s73 offen offset:128 nt lds\n"
MF "v[120:123],v[28:31],v[244:247],v[120:123]\n"
"v_exp_f32 v102,v102\n"
MF "v[124:127],v[28:31],v[248:251],v[124:127]\n"
"s_add_u32 m0,s65,1792\n"
"v_exp_f32 v103,v103\n"
MF "v[124:127],v[24:27],v[228:231],v[124:127]\n"
LD "v8,s[36:39],s73 offen offset:256 nt lds\n"
MF "v[128:131],v[24:27],v[232:235],v[128:131]\n"
"v_add_f32 v59,v54,v100\n"
"v_add_f32 v59,v59,v101\n"
MF "v[128:131],v[28:31],v[252:255],v[128:131]\n"
"s_waitcnt lgkmcnt(0)\n"
TR "v[24:25],v4 offset:24576\n"
TR "v[26:27],v4 offset:30720\n"
MF "a[0:3],v[16:19],v[72:75],a[0:3]\n"
"s_add_u32 m0,s65,0\n"
TR "v[28:29],v5 offset:24576\n"
TR "v[30:31],v5 offset:30720\n"
MF "a[20:23],v[20:23],v[72:75],a[20:23]\n"
LD "v8,s[36:39],s73 offen offset:3072 nt lds\n"
MF "a[24:27],v[20:23],v[76:79],a[24:27]\n"
CV "v100,v100,v101\n"
"v_add_f32 v59,v59,v102\n"
MF "a[4:7],v[16:19],v[76:79],a[4:7]\n"
"s_add_u32 m0,s65,896\n"
"v_add_f32 v59,v59,v103\n"
CV "v101,v102,v103\n"
MF "a[8:11],v[16:19],v[80:83],a[8:11]\n"
LD "v8,s[36:39],s73 offen offset:3200 nt lds\n"
MF "a[28:31],v[20:23],v[80:83],a[28:31]\n"
"v_exp_f32 v104,v104\n"
MF "a[32:35],v[20:23],v[84:87],a[32:35]\n"
"s_add_u32 m0,s65,1792\n"
"v_exp_f32 v105,v105\n"
MF "a[12:15],v[16:19],v[84:87],a[12:15]\n"
LD "v8,s[36:39],s73 offen offset:3328 nt lds\n"
MF "a[16:19],v[16:19],v[88:91],a[16:19]\n"
"v_exp_f32 v106,v106\n"
MF "a[36:39],v[20:23],v[88:91],a[36:39]\n"
"s_waitcnt lgkmcnt(0)\n"
TR "v[16:17],v2 offset:25600\n"
TR "v[18:19],v2 offset:31744\n"
MF "a[56:59],v[24:27],v[88:91],a[56:59]\n"
"s_nop 0\n"
TR "v[20:21],v3 offset:25600\n"
TR "v[22:23],v3 offset:31744\n"
MF "a[76:79],v[28:31],v[88:91],a[76:79]\n"
"v_exp_f32 v107,v107\n"
MF "a[72:75],v[28:31],v[84:87],a[72:75]\n"
"v_add_f32 v60,v55,v104\n"
"v_add_f32 v60,v60,v105\n"
MF "a[52:55],v[24:27],v[84:87],a[52:55]\n"
CV "v104,v104,v105\n"
"v_add_f32 v60,v60,v106\n"
MF "a[48:51],v[24:27],v[80:83],a[48:51]\n"
"v_add_f32 v60,v60,v107\n"
CV "v105,v106,v107\n"
MF "a[68:71],v[28:31],v[80:83],a[68:71]\n"
"v_exp_f32 v108,v108\n"
MF "a[64:67],v[28:31],v[76:79],a[64:67]\n"
"v_exp_f32 v109,v109\n"
MF "a[44:47],v[24:27],v[76:79],a[44:47]\n"
"v_exp_f32 v110,v110\n"
MF "a[40:43],v[24:27],v[72:75],a[40:43]\n"
"v_exp_f32 v111,v111\n"
MF "a[60:63],v[28:31],v[72:75],a[60:63]\n"
"s_waitcnt lgkmcnt(0)\n"
TR "v[24:25],v4 offset:25600\n"
TR "v[26:27],v4 offset:31744\n"
MF "a[80:83],v[16:19],v[72:75],a[80:83]\n"
"s_nop 0\n"
TR "v[28:29],v5 offset:25600\n"
TR "v[30:31],v5 offset:31744\n"
MF "a[100:103],v[20:23],v[72:75],a[100:103]\n"
"v_add_f32 v61,v56,v108\n"
"v_add_f32 v61,v61,v109\n"
MF "a[104:107],v[20:23],v[76:79],a[104:107]\n"
CV "v108,v108,v109\n"
"v_add_f32 v61,v61,v110\n"
MF "a[84:87],v[16:19],v[76:79],a[84:87]\n"
"v_add_f32 v61,v61,v111\n"
CV "v109,v110,v111\n"
MF "a[88:91],v[16:19],v[80:83],a[88:91]\n"
"v_exp_f32 v112,v112\n"
MF "a[108:111],v[20:23],v[80:83],a[108:111]\n"
"v_exp_f32 v113,v113\n"
MF "a[112:115],v[20:23],v[84:87],a[112:115]\n"
"v_exp_f32 v114,v114\n"
MF "a[92:95],v[16:19],v[84:87],a[92:95]\n"
"v_exp_f32 v115,v115\n"
MF "a[96:99],v[16:19],v[88:91],a[96:99]\n"
"v_add_f32 v57,v57,v112\n"
"v_add_f32 v57,v57,v113\n"
MF "a[116:119],v[20:23],v[88:91],a[116:119]\n"
"s_waitcnt lgkmcnt(0)\n"
TR "v[16:17],v2 offset:26624\n"
TR "v[18:19],v2 offset:32768\n"
MF "a[136:139],v[24:27],v[88:91],a[136:139]\n"
"s_nop 0\n"
TR "v[20:21],v3 offset:26624\n"
TR "v[22:23],v3 offset:32768\n"
MF "a[156:159],v[28:31],v[88:91],a[156:159]\n"
CV "v94,v112,v113\n"
"v_add_f32 v57,v57,v114\n"
MF "a[152:155],v[28:31],v[84:87],a[152:155]\n"
"v_add_f32 v57,v57,v115\n"
CV "v95,v114,v115\n"
MF "a[132:135],v[24:27],v[84:87],a[132:135]\n"
"v_exp_f32 v116,v116\n"
MF "a[128:131],v[24:27],v[80:83],a[128:131]\n"
"v_exp_f32 v117,v117\n"
MF "a[148:151],v[28:31],v[80:83],a[148:151]\n"
"v_exp_f32 v118,v118\n"
MF "a[144:147],v[28:31],v[76:79],a[144:147]\n"
"v_exp_f32 v119,v119\n"
MF "a[124:127],v[24:27],v[76:79],a[124:127]\n"
"v_add_f32 v58,v58,v116\n"
"v_add_f32 v58,v58,v117\n"
MF "a[120:123],v[24:27],v[72:75],a[120:123]\n"
CV "v98,v116,v117\n"
"v_add_f32 v58,v58,v118\n"
MF "a[140:143],v[28:31],v[72:75],a[140:143]\n"
"s_waitcnt lgkmcnt(0)\n"
TR "v[24:25],v4 offset:26624\n"
TR "v[26:27],v4 offset:32768\n"
MF "a[160:163],v[16:19],v[72:75],a[160:163]\n"
"s_nop 0\n"
TR "v[28:29],v5 offset:26624\n"
TR "v[30:31],v5 offset:32768\n"
MF "a[180:183],v[20:23],v[72:75],a[180:183]\n"
"v_add_f32 v58,v58,v119\n"
CV "v99,v118,v119\n"
MF "a[184:187],v[20:23],v[76:79],a[184:187]\n"
"v_exp_f32 v120,v120\n"
MF "a[164:167],v[16:19],v[76:79],a[164:167]\n"
"v_exp_f32 v121,v121\n"
MF "a[168:171],v[16:19],v[80:83],a[168:171]\n"
"v_exp_f32 v122,v122\n"
MF "a[188:191],v[20:23],v[80:83],a[188:191]\n"
"v_exp_f32 v123,v123\n"
MF "a[192:195],v[20:23],v[84:87],a[192:195]\n"
"v_add_f32 v59,v59,v120\n"
"v_add_f32 v59,v59,v121\n"
MF "a[172:175],v[16:19],v[84:87],a[172:175]\n"
"s_waitcnt vmcnt(6) lgkmcnt(0)\n"
CV "v102,v120,v121\n"
"v_add_f32 v59,v59,v122\n"
MF "a[176:179],v[16:19],v[88:91],a[176:179]\n"
"s_barrier\n"
MF "a[196:199],v[20:23],v[88:91],a[196:199]\n"
"s_add_u32 s72,s72,6144\n"
"ds_read_b128 v[16:19],v0 offset:49152\n"
"ds_read_b128 v[20:23],v1 offset:49152\n"
MF "a[216:219],v[24:27],v[88:91],a[216:219]\n"
"s_cmp_eq_u32 s72,s17\n"
"s_cselect_b32 s72,s16,s72\n"
"s_add_u32 m0,s64,24576\n"
MF "a[236:239],v[28:31],v[88:91],a[236:239]\n"
LD "v7,s[32:35],s72 offen nt lds\n"
MF "a[232:235],v[28:31],v[84:87],a[232:235]\n"
"s_add_u32 m0,s64,25472\n"
"v_add_f32 v59,v59,v123\n"
CV "v103,v122,v123\n"
MF "a[212:215],v[24:27],v[84:87],a[212:215]\n"
LD "v7,s[32:35],s72 offen offset:128 nt lds\n"
MF "a[208:211],v[24:27],v[80:83],a[208:211]\n"
"s_add_u32 m0,s64,26368\n"
MF "a[228:231],v[28:31],v[80:83],a[228:231]\n"
LD "v7,s[32:35],s72 offen offset:256 nt lds\n"
MF "a[224:227],v[28:31],v[76:79],a[224:227]\n"
MF "a[204:207],v[24:27],v[76:79],a[204:207]\n"
MF "a[200:203],v[24:27],v[72:75],a[200:203]\n"
MF "a[220:223],v[28:31],v[72:75],a[220:223]\n"
"s_waitcnt lgkmcnt(0)\n"
MF "v[72:75],v[16:19],v[136:139],v[32:35]\n"
"s_add_u32 s72,s72,6144\n"
"ds_read_b128 v[24:27],v0 offset:50176\n"
"ds_read_b128 v[28:31],v1 offset:50176\n"
MF "v[72:75],v[20:23],v[156:159],v[72:75]\n"
"s_add_u32 m0,s64,30720\n"
"v_exp_f32 v124,v124\n"
MF "v[76:79],v[20:23],v[160:163],v[36:39]\n"
LD "v7,s[32:35],s72 offen nt lds\n"
MF "v[76:79],v[16:19],v[140:143],v[76:79]\n"
"v_exp_f32 v125,v125\n"
MF "v[80:83],v[16:19],v[144:147],v[40:43]\n"
"s_add_u32 m0,s64,31616\n"
"v_exp_f32 v126,v126\n"
MF "v[80:83],v[20:23],v[164:167],v[80:83]\n"
LD "v7,s[32:35],s72 offen offset:128 nt lds\n"
MF "v[84:87],v[20:23],v[168:171],v[44:47]\n"
"v_exp_f32 v127,v127\n"
MF "v[84:87],v[16:19],v[148:151],v[84:87]\n"
"s_add_u32 m0,s64,32512\n"
"v_add_f32 v60,v60,v124\n"
"v_add_f32 v60,v60,v125\n"
MF "v[88:91],v[16:19],v[152:155],v[48:51]\n"
LD "v7,s[32:35],s72 offen offset:256 nt lds\n"
MF "v[88:91],v[20:23],v[172:175],v[88:91]\n"
"s_waitcnt lgkmcnt(0)\n"
"ds_read_b128 v[16:19],v0 offset:51200\n"
"ds_read_b128 v[20:23],v1 offset:51200\n"
MF "v[88:91],v[24:27],v[192:195],v[88:91]\n"
CV "v106,v124,v125\n"
"v_add_f32 v60,v60,v126\n"
MF "v[88:91],v[28:31],v[212:215],v[88:91]\n"
"v_add_f32 v60,v60,v127\n"
CV "v107,v126,v127\n"
MF "v[84:87],v[28:31],v[208:211],v[84:87]\n"
"v_exp_f32 v128,v128\n"
MF "v[84:87],v[24:27],v[188:191],v[84:87]\n"
"v_exp_f32 v129,v129\n"
MF "v[80:83],v[24:27],v[184:187],v[80:83]\n"
"v_exp_f32 v130,v130\n"
MF "v[80:83],v[28:31],v[204:207],v[80:83]\n"
"v_exp_f32 v131,v131\n"
MF "v[76:79],v[28:31],v[200:203],v[76:79]\n"
"v_add_f32 v61,v61,v128\n"
"v_add_f32 v61,v61,v129\n"
MF "v[76:79],v[24:27],v[180:183],v[76:79]\n"
CV "v110,v128,v129\n"
"v_add_f32 v61,v61,v130\n"
MF "v[72:75],v[24:27],v[176:179],v[72:75]\n"
"v_add_f32 v61,v61,v131\n"
CV "v111,v130,v131\n"
MF "v[72:75],v[28:31],v[196:199],v[72:75]\n"
"s_waitcnt lgkmcnt(0)\n"
"ds_read_b128 v[24:27],v0 offset:55296\n"
"ds_read_b128 v[28:31],v1 offset:55296\n"
MF "v[72:75],v[16:19],v[216:219],v[72:75]\n"
MF "v[72:75],v[20:23],v[236:239],v[72:75]\n"
MF "v[76:79],v[20:23],v[240:243],v[76:79]\n"
MF "v[76:79],v[16:19],v[220:223],v[76:79]\n"
MF "v[80:83],v[16:19],v[224:227],v[80:83]\n"
MF "v[80:83],v[20:23],v[244:247],v[80:83]\n"
MF "v[84:87],v[20:23],v[248:251],v[84:87]\n"
MF "v[84:87],v[16:19],v[228:231],v[84:87]\n"
"v_max3_f32 v10,v57,v58,v59\n"
"v_max3_f32 v10,v10,v60,v61\n"
MF "v[88:91],v[16:19],v[232:235],v[88:91]\n"
"v_cmp_ngt_f32 vcc,s95,v10\n"
MF "v[88:91],v[20:23],v[252:255],v[88:91]\n"
"s_waitcnt lgkmcnt(0)\n"
"s_cbranch_vccnz .Lstub_24\n"
".Lback_24:\n"
MF "v[112:115],v[24:27],v[136:139],v[32:35]\n"
"s_add_u32 s74,s74,12288\n"
"ds_read_b128 v[16:19],v0 offset:56320\n"
"ds_read_b128 v[20:23],v1 offset:56320\n"
MF "v[112:115],v[28:31],v[156:159],v[112:115]\n"
"s_cmp_eq_u32 s74,s20\n"
"s_cselect_b32 s74,s70,s74\n"
MF "v[116:119],v[28:31],v[160:163],v[36:39]\n"
"v_exp_f32 v72,v72\n"
MF "v[116:119],v[24:27],v[140:143],v[116:119]\n"
"v_exp_f32 v73,v73\n"
MF "v[120:123],v[24:27],v[144:147],v[40:43]\n"
"v_exp_f32 v74,v74\n"
MF "v[120:123],v[28:31],v[164:167],v[120:123]\n"
"v_exp_f32 v75,v75\n"
MF "v[124:127],v[28:31],v[168:171],v[44:47]\n"
"v_add_f32 v52,v57,v72\n"
"v_add_f32 v52,v52,v73\n"
MF "v[124:127],v[24:27],v[148:151],v[124:127]\n"
CV "v72,v72,v73\n"
"v_add_f32 v52,v52,v74\n"
MF "v[128:131],v[24:27],v[152:155],v[48:51]\n"
"v_add_f32 v52,v52,v75\n"
CV "v73,v74,v75\n"
MF "v[128:131],v[28:31],v[172:175],v[128:131]\n"
"s_waitcnt lgkmcnt(0)\n"
"ds_read_b128 v[24:27],v0 offset:57344\n"
"ds_read_b128 v[28:31],v1 offset:57344\n"
MF "v[128:131],v[16:19],v[192:195],v[128:131]\n"
"v_exp_f32 v76,v76\n"
MF "v[128:131],v[20:23],v[212:215],v[128:131]\n"
"v_exp_f32 v77,v77\n"
MF "v[124:127],v[20:23],v[208:211],v[124:127]\n"
"v_exp_f32 v78,v78\n"
MF "v[124:127],v[16:19],v[188:191],v[124:127]\n"
"v_exp_f32 v79,v79\n"
MF "v[120:123],v[16:19],v[184:187],v[120:123]\n"
"v_add_f32 v53,v58,v76\n"
"v_add_f32 v53,v53,v77\n"
MF "v[120:123],v[20:23],v[204:207],v[120:123]\n"
CV "v76,v76,v77\n"
"v_add_f32 v53,v53,v78\n"
MF "v[116:119],v[20:23],v[200:203],v[116:119]\n"
"s_waitcnt vmcnt(6) lgkmcnt(0)\n"
"v_add_f32 v53,v53,v79\n"
CV "v77,v78,v79\n"
MF "v[116:119],v[16:19],v[180:183],v[116:119]\n"
"s_barrier\n"
MF "v[112:115],v[16:19],v[176:179],v[112:115]\n"
"v_exp_f32 v80,v80\n"
MF "v[112:115],v[20:23],v[196:199],v[112:115]\n"
"s_nop 0\n"
TR "v[16:17],v2\n"
TR "v[18:19],v2 offset:6144\n"
MF "v[112:115],v[24:27],v[216:219],v[112:115]\n"
"s_add_u32 s73,s73,12288\n"
TR "v[20:21],v3\n"
TR "v[22:23],v3 offset:6144\n"
MF "v[112:115],v[28:31],v[236:239],v[112:115]\n"
"s_cmp_eq_u32 s73,s19\n"
"s_cselect_b32 s73,s18,s73\n"
"s_add_u32 m0,s65,49152\n"
MF "v[116:119],v[28:31],v[240:243],v[116:119]\n"
LD "v8,s[36:39],s73 offen nt lds\n"
MF "v[116:119],v[24:27],v[220:223],v[116:119]\n"
"s_add_u32 m0,s65,50048\n"
"v_exp_f32 v81,v81\n"
MF "v[120:123],v[24:27],v[224:227],v[120:123]\n"
LD "v8,s[36:39],s73 offen offset:128 nt lds\n"
MF "v[120:123],v[28:31],v[244:247],v[120:123]\n"
"v_exp_f32 v82,v82\n"
MF "v[124:127],v[28:31],v[248:251],v[124:127]\n"
"s_add_u32 m0,s65,50944\n"
"v_exp_f32 v83,v83\n"
MF "v[124:127],v[24:27],v[228:231],v[124:127]\n"
LD "v8,s[36:39],s73 offen offset:256 nt lds\n"
MF "v[128:131],v[24:27],v[232:235],v[128:131]\n"
"v_add_f32 v54,v59,v80\n"
"v_add_f32 v54,v54,v81\n"
MF "v[128:131],v[28:31],v[252:255],v[128:131]\n"
"s_waitcnt lgkmcnt(0)\n"
TR "v[24:25],v4\n"
TR "v[26:27],v4 offset:6144\n"
MF "a[0:3],v[16:19],v[92:95],a[0:3]\n"
"s_add_u32 m0,s65,49152\n"
TR "v[28:29],v5\n"
TR "v[30:31],v5 offset:6144\n"
MF "a[20:23],v[20:23],v[92:95],a[20:23]\n"
LD "v8,s[36:39],s73 offen offset:3072 nt lds\n"
MF "a[24:27],v[20:23],v[96:99],a[24:27]\n"
CV "v80,v80,v81\n"
"v_add_f32 v54,v54,v82\n"
MF "a[4:7],v[16:19],v[96:99],a[4:7]\n"
"s_add_u32 m0,s65,50048\n"
"v_add_f32 v54,v54,v83\n"
CV "v81,v82,v83\n"
MF "a[8:11],v[16:19],v[100:103],a[8:11]\n"
LD "v8,s[36:39],s73 offen offset:3200 nt lds\n"
MF "a[28:31],v[20:23],v[100:103],a[28:31]\n"
"v_exp_f32 v84,v84\n"
MF "a[32:35],v[20:23],v[104:107],a[32:35]\n"
"s_add_u32 m0,s65,50944\n"
"v_exp_f32 v85,v85\n"
MF "a[12:15],v[16:19],v[104:107],a[12:15]\n"
LD "v8,s[36:39],s73 offen offset:3328 nt lds\n"
MF "a[16:19],v[16:19],v[108:111],a[16:19]\n"
"v_exp_f32 v86,v86\n"
MF "a[36:39],v[20:23],v[108:111],a[36:39]\n"
"s_waitcnt lgkmcnt(0)\n"
TR "v[16:17],v2 offset:1024\n"
TR "v[18:19],v2 offset:7168\n"
MF "a[56:59],v[24:27],v[108:111],a[56:59]\n"
"s_nop 0\n"
TR "v[20:21],v3 offset:1024\n"
TR "v[22:23],v3 offset:7168\n"
MF "a[76:79],v[28:31],v[108:111],a[76:79]\n"
"v_exp_f32 v87,v87\n"
MF "a[72:75],v[28:31],v[104:107],a[72:75]\n"
"v_add_f32 v55,v60,v84\n"
"v_add_f32 v55,v55,v85\n"
MF "a[52:55],v[24:27],v[104:107],a[52:55]\n"
CV "v84,v84,v85\n"
"v_add_f32 v55,v55,v86\n"
MF "a[48:51],v[24:27],v[100:103],a[48:51]\n"
"v_add_f32 v55,v55,v87\n"
CV "v85,v86,v87\n"
MF "a[68:71],v[28:31],v[100:103],a[68:71]\n"
"v_exp_f32 v88,v88\n"
MF "a[64:67],v[28:31],v[96:99],a[64:67]\n"
"v_exp_f32 v89,v89\n"
MF "a[44:47],v[24:27],v[96:99],a[44:47]\n"
"v_exp_f32 v90,v90\n"
MF "a[40:43],v[24:27],v[92:95],a[40:43]\n"
"v_exp_f32 v91,v91\n"
MF "a[60:63],v[28:31],v[92:95],a[60:63]\n"
"s_waitcnt lgkmcnt(0)\n"
TR "v[24:25],v4 offset:1024\n"
TR "v[26:27],v4 offset:7168\n"
MF "a[80:83],v[16:19],v[92:95],a[80:83]\n"
"s_nop 0\n"
TR "v[28:29],v5 offset:1024\n"
TR "v[30:31],v5 offset:7168\n"
MF "a[100:103],v[20:23],v[92:95],a[100:103]\n"
"v_add_f32 v56,v61,v88\n"
"v_add_f32 v56,v56,v89\n"
MF "a[104:107],v[20:23],v[96:99],a[104:107]\n"
CV "v88,v88,v89\n"
"v_add_f32 v56,v56,v90\n"
MF "a[84:87],v[16:19],v[96:99],a[84:87]\n"
"v_add_f32 v56,v56,v91\n"
CV "v89,v90,v91\n"
MF "a[88:91],v[16:19],v[100:103],a[88:91]\n"
"v_exp_f32 v112,v112\n"
MF "a[108:111],v[20:23],v[100:103],a[108:111]\n"
"v_exp_f32 v113,v113\n"
MF "a[112:115],v[20:23],v[104:107],a[112:115]\n"
"v_exp_f32 v114,v114\n"
MF "a[92:95],v[16:19],v[104:107],a[92:95]\n"
"v_exp_f32 v115,v115\n"
MF "a[96:99],v[16:19],v[108:111],a[96:99]\n"
"v_add_f32 v52,v52,v112\n"
"v_add_f32 v52,v52,v113\n"
MF "a[116:119],v[20:23],v[108:111],a[116:119]\n"
"s_waitcnt lgkmcnt(0)\n"
TR "v[16:17],v2 offset:2048\n"
TR "v[18:19],v2 offset:8192\n"
MF "a[136:139],v[24:27],v[108:111],a[136:139]\n"
"s_nop 0\n"
TR "v[20:21],v3 offset:2048\n"
TR "v[22:23],v3 offset:8192\n"
MF "a[156:159],v[28:31],v[108:111],a[156:159]\n"
CV "v74,v112,v113\n"
"v_add_f32 v52,v52,v114\n"
MF "a[152:155],v[28:31],v[104:107],a[152:155]\n"
"v_add_f32 v52,v52,v115\n"
CV "v75,v114,v115\n"
MF "a[132:135],v[24:27],v[104:107],a[132:135]\n"
"v_exp_f32 v116,v116\n"
MF "a[128:131],v[24:27],v[100:103],a[128:131]\n"
"v_exp_f32 v117,v117\n"
MF "a[148:151],v[28:31],v[100:103],a[148:151]\n"
"v_exp_f32 v118,v118\n"
MF "a[144:147],v[28:31],v[96:99],a[144:147]\n"
"v_exp_f32 v119,v119\n"
MF "a[124:127],v[24:27],v[96:99],a[124:127]\n"
"v_add_f32 v53,v53,v116\n"
"v_add_f32 v53,v53,v117\n"
MF "a[120:123],v[24:27],v[92:95],a[120:123]\n"
CV "v78,v116,v117\n"
"v_add_f32 v53,v53,v118\n"
MF "a[140:143],v[28:31],v[92:95],a[140:143]\n"
"s_waitcnt lgkmcnt(0)\n"
TR "v[24:25],v4 offset:2048\n"
TR "v[26:27],v4 offset:8192\n"
MF "a[160:163],v[16:19],v[92:95],a[160:163]\n"
"s_nop 0\n"
TR "v[28:29],v5 offset:2048\n"
TR "v[30:31],v5 offset:8192\n"
MF "a[180:183],v[20:23],v[92:95],a[180:183]\n"
"v_add_f32 v53,v53,v119\n"
CV "v79,v118,v119\n"
MF "a[184:187],v[20:23],v[96:99],a[184:187]\n"
"v_exp_f32 v120,v120\n"
MF "a[164:167],v[16:19],v[96:99],a[164:167]\n"
"v_exp_f32 v121,v121\n"
MF "a[168:171],v[16:19],v[100:103],a[168:171]\n"
"v_exp_f32 v122,v122\n"
MF "a[188:191],v[20:23],v[100:103],a[188:191]\n"
"v_exp_f32 v123,v123\n"
MF "a[192:195],v[20:23],v[104:107],a[192:195]\n"
"v_add_f32 v54,v54,v120\n"
"v_add_f32 v54,v54,v121\n"
MF "a[172:175],v[16:19],v[104:107],a[172:175]\n"
"s_waitcnt vmcnt(6) lgkmcnt(0)\n"
CV "v82,v120,v121\n"
"v_add_f32 v54,v54,v122\n"
MF "a[176:179],v[16:19],v[108:111],a[176:179]\n"
"s_barrier\n"
MF "a[196:199],v[20:23],v[108:111],a[196:199]\n"
"s_add_u32 s72,s72,6144\n"
"ds_read_b128 v[16:19],v0 offset:24576\n"
"ds_read_b128 v[20:23],v1 offset:24576\n"
MF "a[216:219],v[24:27],v[108:111],a[216:219]\n"
"s_cmp_eq_u32 s72,s17\n"
"s_cselect_b32 s72,s16,s72\n"
"s_add_u32 m0,s64,0\n"
MF "a[236:239],v[28:31],v[108:111],a[236:239]\n"
LD "v7,s[32:35],s72 offen nt lds\n"
MF "a[232:235],v[28:31],v[104:107],a[232:235]\n"
"s_add_u32 m0,s64,896\n"
"v_add_f32 v54,v54,v123\n"
CV "v83,v122,v123\n"
MF "a[212:215],v[24:27],v[104:107],a[212:215]\n"
LD "v7,s[32:35],s72 offen offset:128 nt lds\n"
MF "a[208:211],v[24:27],v[100:103],a[208:211]\n"
"s_add_u32 m0,s64,1792\n"
MF "a[228:231],v[28:31],v[100:103],a[228:231]\n"
LD "v7,s[32:35],s72 offen offset:256 nt lds\n"
MF "a[224:227],v[28:31],v[96:99],a[224:227]\n"
MF "a[204:207],v[24:27],v[96:99],a[204:207]\n"
MF "a[200:203],v[24:27],v[92:95],a[200:203]\n"
MF "a[220:223],v[28:31],v[92:95],a[220:223]\n"
"s_waitcnt lgkmcnt(0)\n"
MF "v[92:95],v[16:19],v[136:139],v[32:35]\n"
"s_add_u32 s72,s72,6144\n"
"ds_read_b128 v[24:27],v0 offset:25600\n"
"ds_read_b128 v[28:31],v1 offset:25600\n"
MF "v[92:95],v[20:23],v[156:159],v[92:95]\n"
"s_add_u32 m0,s64,6144\n"
"v_exp_f32 v124,v124\n"
MF "v[96:99],v[20:23],v[160:163],v[36:39]\n"
LD "v7,s[32:35],s72 offen nt lds\n"
MF "v[96:99],v[16:19],v[140:143],v[96:99]\n"
"v_exp_f32 v125,v125\n"
MF "v[100:103],v[16:19],v[144:147],v[40:43]\n"
"s_add_u32 m0,s64,7040\n"
"v_exp_f32 v126,v126\n"
MF "v[100:103],v[20:23],v[164:167],v[100:103]\n"
LD "v7,s[32:35],s72 offen offset:128 nt lds\n"
MF "v[104:107],v[20:23],v[168:171],v[44:47]\n"
"v_exp_f32 v127,v127\n"
MF "v[104:107],v[16:19],v[148:151],v[104:107]\n"
"s_add_u32 m0,s64,7936\n"
"v_add_f32 v55,v55,v124\n"
"v_add_f32 v55,v55,v125\n"
MF "v[108:111],v[16:19],v[152:155],v[48:51]\n"
LD "v7,s[32:35],s72 offen offset:256 nt lds\n"
MF "v[108:111],v[20:23],v[172:175],v[108:111]\n"
"s_waitcnt lgkmcnt(0)\n"
"ds_read_b128 v[16:19],v0 offset:26624\n"
"ds_read_b128 v[20:23],v1 offset:26624\n"
MF "v[108:111],v[24:27],v[192:195],v[108:111]\n"
CV "v86,v124,v125\n"
"v_add_f32 v55,v55,v126\n"
MF "v[108:111],v[28:31],v[212:215],v[108:111]\n"
"v_add_f32 v55,v55,v127\n"
CV "v87,v126,v127\n"
MF "v[104:107],v[28:31],v[208:211],v[104:107]\n"
"v_exp_f32 v128,v128\n"
MF "v[104:107],v[24:27],v[188:191],v[104:107]\n"
"v_exp_f32 v129,v129\n"
MF "v[100:103],v[24:27],v[184:187],v[100:103]\n"
"v_exp_f32 v130,v130\n"
MF "v[100:103],v[28:31],v[204:207],v[100:103]\n"
"v_exp_f32 v131,v131\n"
MF "v[96:99],v[28:31],v[200:203],v[96:99]\n"
"v_add_f32 v56,v56,v128\n"
"v_add_f32 v56,v56,v129\n"
MF "v[96:99],v[24:27],v[180:183],v[96:99]\n"
CV "v90,v128,v129\n"
"v_add_f32 v56,v56,v130\n"
MF "v[92:95],v[24:27],v[176:179],v[92:95]\n"
"v_add_f32 v56,v56,v131\n"
CV "v91,v130,v131\n"
MF "v[92:95],v[28:31],v[196:199],v[92:95]\n"
"s_waitcnt lgkmcnt(0)\n"
"ds_read_b128 v[24:27],v0 offset:30720\n"
"ds_read_b128 v[28:31],v1 offset:30720\n"
MF "v[92:95],v[16:19],v[216:219],v[92:95]\n"
MF "v[92:95],v[20:23],v[236:239],v[92:95]\n"
MF "v[96:99],v[20:23],v[240:243],v[96:99]\n"
MF "v[96:99],v[16:19],v[220:223],v[96:99]\n"
MF "v[100:103],v[16:19],v[224:227],v[100:103]\n"
MF "v[100:103],v[20:23],v[244:247],v[100:103]\n"
MF "v[104:107],v[20:23],v[248:251],v[104:107]\n"
MF "v[104:107],v[16:19],v[228:231],v[104:107]\n"
"v_max3_f32 v10,v52,v53,v54\n"
"v_max3_f32 v10,v10,v55,v56\n"
MF "v[108:111],v[16:19],v[232:235],v[108:111]\n"
"v_cmp_ngt_f32 vcc,s77,v10\n"
MF "v[108:111],v[20:23],v[252:255],v[108:111]\n"
"s_waitcnt lgkmcnt(0)\n"
"s_cbranch_vccnz .Lstub_25\n"
".Lback_25:\n"
MF "v[112:115],v[24:27],v[136:139],v[32:35]\n"
"s_add_u32 s74,s74,12288\n"
"ds_read_b128 v[16:19],v0 offset:31744\n"
"ds_read_b128 v[20:23],v1 offset:31744\n"
MF "v[112:115],v[28:31],v[156:159],v[112:115]\n"
"s_cmp_eq_u32 s74,s20\n"
"s_cselect_b32 s74,s70,s74\n"
MF "v[116:119],v[28:31],v[160:163],v[36:39]\n"
"v_exp_f32 v92,v92\n"
MF "v[116:119],v[24:27],v[140:143],v[116:119]\n"
"v_exp_f32 v93,v93\n"
MF "v[120:123],v[24:27],v[144:147],v[40:43]\n"
"v_exp_f32 v94,v94\n"
MF "v[120:123],v[28:31],v[164:167],v[120:123]\n"
"v_exp_f32 v95,v95\n"
MF "v[124:127],v[28:31],v[168:171],v[44:47]\n"
"v_add_f32 v57,v52,v92\n"
"v_add_f32 v57,v57,v93\n"
MF "v[124:127],v[24:27],v[148:151],v[124:127]\n"
CV "v92,v92,v93\n"
"v_add_f32 v57,v57,v94\n"
MF "v[128:131],v[24:27],v[152:155],v[48:51]\n"
"v_add_f32 v57,v57,v95\n"
CV "v93,v94,v95\n"
MF "v[128:131],v[28:31],v[172:175],v[128:131]\n"
"s_waitcnt lgkmcnt(0)\n"
"ds_read_b128 v[24:27],v0 offset:32768\n"
"ds_read_b128 v[28:31],v1 offset:32768\n"
MF "v[128:131],v[16:19],v[192:195],v[128:131]\n"
"s_sub_u32 s61,s61,1\n"
"v_exp_f32 v96,v96\n"
MF "v[128:131],v[20:23],v[212:215],v[128:131]\n"
"s_mov_b32 s95,0\n"
"v_exp_f32 v97,v97\n"
MF "v[124:127],v[20:23],v[208:211],v[124:127]\n"
"v_exp_f32 v98,v98\n"
MF "v[124:127],v[16:19],v[188:191],v[124:127]\n"
"v_exp_f32 v99,v99\n"
MF "v[120:123],v[16:19],v[184:187],v[120:123]\n"
"v_add_f32 v58,v53,v96\n"
"v_add_f32 v58,v58,v97\n"
MF "v[120:123],v[20:23],v[204:207],v[120:123]\n"
CV "v96,v96,v97\n"
"v_add_f32 v58,v58,v98\n"
MF "v[116:119],v[20:23],v[200:203],v[116:119]\n"
"s_waitcnt vmcnt(6) lgkmcnt(0)\n"
"v_add_f32 v58,v58,v99\n"
CV "v97,v98,v99\n"
MF "v[116:119],v[16:19],v[180:183],v[116:119]\n"
"s_barrier\n"
MF "v[112:115],v[16:19],v[176:179],v[112:115]\n"
"v_exp_f32 v100,v100\n"
MF "v[112:115],v[20:23],v[196:199],v[112:115]\n"
"s_nop 0\n"
TR "v[16:17],v2 offset:49152\n"
TR "v[18:19],v2 offset:55296\n"
MF "v[112:115],v[24:27],v[216:219],v[112:115]\n"
"s_add_u32 s73,s73,12288\n"
TR "v[20:21],v3 offset:49152\n"
TR "v[22:23],v3 offset:55296\n"
MF "v[112:115],v[28:31],v[236:239],v[112:115]\n"
"s_cmp_eq_u32 s73,s19\n"
"s_cselect_b32 s73,s18,s73\n"
"s_add_u32 m0,s65,24576\n"
MF "v[116:119],v[28:31],v[240:243],v[116:119]\n"
LD "v8,s[36:39],s73 offen nt lds\n"
MF "v[116:119],v[24:27],v[220:223],v[116:119]\n"
"s_add_u32 m0,s65,25472\n"
"v_exp_f32 v101,v101\n"
MF "v[120:123],v[24:27],v[224:227],v[120:123]\n"
LD "v8,s[36:39],s73 offen offset:128 nt lds\n"
MF "v[120:123],v[28:31],v[244:247],v[120:123]\n"
"v_exp_f32 v102,v102\n"
MF "v[124:127],v[28:31],v[248:251],v[124:127]\n"
"s_add_u32 m0,s65,26368\n"
"v_exp_f32 v103,v103\n"
MF "v[124:127],v[24:27],v[228:231],v[124:127]\n"
LD "v8,s[36:39],s73 offen offset:256 nt lds\n"
MF "v[128:131],v[24:27],v[232:235],v[128:131]\n"
"v_add_f32 v59,v54,v100\n"
"v_add_f32 v59,v59,v101\n"
MF "v[128:131],v[28:31],v[252:255],v[128:131]\n"
"s_waitcnt lgkmcnt(0)\n"
TR "v[24:25],v4 offset:49152\n"
TR "v[26:27],v4 offset:55296\n"
MF "a[0:3],v[16:19],v[72:75],a[0:3]\n"
"s_add_u32 m0,s65,24576\n"
TR "v[28:29],v5 offset:49152\n"
TR "v[30:31],v5 offset:55296\n"
MF "a[20:23],v[20:23],v[72:75],a[20:23]\n"
LD "v8,s[36:39],s73 offen offset:3072 nt lds\n"
MF "a[24:27],v[20:23],v[76:79],a[24:27]\n"
CV "v100,v100,v101\n"
"v_add_f32 v59,v59,v102\n"
MF "a[4:7],v[16:19],v[76:79],a[4:7]\n"
"s_add_u32 m0,s65,25472\n"
"v_add_f32 v59,v59,v103\n"
CV "v101,v102,v103\n"
MF "a[8:11],v[16:19],v[80:83],a[8:11]\n"
LD "v8,s[36:39],s73 offen offset:3200 nt lds\n"
MF "a[28:31],v[20:23],v[80:83],a[28:31]\n"
"v_exp_f32 v104,v104\n"
MF "a[32:35],v[20:23],v[84:87],a[32:35]\n"
"s_add_u32 m0,s65,26368\n"
"v_exp_f32 v105,v105\n"
MF "a[12:15],v[16:19],v[84:87],a[12:15]\n"
LD "v8,s[36:39],s73 offen offset:3328 nt lds\n"
MF "a[16:19],v[16:19],v[88:91],a[16:19]\n"
"v_exp_f32 v106,v106\n"
MF "a[36:39],v[20:23],v[88:91],a[36:39]\n"
"s_waitcnt lgkmcnt(0)\n"
TR "v[16:17],v2 offset:50176\n"
TR "v[18:19],v2 offset:56320\n"
MF "a[56:59],v[24:27],v[88:91],a[56:59]\n"
"s_nop 0\n"
TR "v[20:21],v3 offset:50176\n"
TR "v[22:23],v3 offset:56320\n"
MF "a[76:79],v[28:31],v[88:91],a[76:79]\n"
"v_exp_f32 v107,v107\n"
MF "a[72:75],v[28:31],v[84:87],a[72:75]\n"
"v_add_f32 v60,v55,v104\n"
"v_add_f32 v60,v60,v105\n"
MF "a[52:55],v[24:27],v[84:87],a[52:55]\n"
CV "v104,v104,v105\n"
"v_add_f32 v60,v60,v106\n"
MF "a[48:51],v[24:27],v[80:83],a[48:51]\n"
"v_add_f32 v60,v60,v107\n"
CV "v105,v106,v107\n"
MF "a[68:71],v[28:31],v[80:83],a[68:71]\n"
"v_exp_f32 v108,v108\n"
MF "a[64:67],v[28:31],v[76:79],a[64:67]\n"
"v_exp_f32 v109,v109\n"
MF "a[44:47],v[24:27],v[76:79],a[44:47]\n"
"v_exp_f32 v110,v110\n"
MF "a[40:43],v[24:27],v[72:75],a[40:43]\n"
"v_exp_f32 v111,v111\n"
MF "a[60:63],v[28:31],v[72:75],a[60:63]\n"
"s_waitcnt lgkmcnt(0)\n"
TR "v[24:25],v4 offset:50176\n"
TR "v[26:27],v4 offset:56320\n"
MF "a[80:83],v[16:19],v[72:75],a[80:83]\n"
"s_nop 0\n"
TR "v[28:29],v5 offset:50176\n"
TR "v[30:31],v5 offset:56320\n"
MF "a[100:103],v[20:23],v[72:75],a[100:103]\n"
"v_add_f32 v61,v56,v108\n"
"v_add_f32 v61,v61,v109\n"
MF "a[104:107],v[20:23],v[76:79],a[104:107]\n"
CV "v108,v108,v109\n"
"v_add_f32 v61,v61,v110\n"
MF "a[84:87],v[16:19],v[76:79],a[84:87]\n"
"v_add_f32 v61,v61,v111\n"
CV "v109,v110,v111\n"
MF "a[88:91],v[16:19],v[80:83],a[88:91]\n"
"v_exp_f32 v112,v112\n"
MF "a[108:111],v[20:23],v[80:83],a[108:111]\n"
"v_exp_f32 v113,v113\n"
MF "a[112:115],v[20:23],v[84:87],a[112:115]\n"
"v_exp_f32 v114,v114\n"
MF "a[92:95],v[16:19],v[84:87],a[92:95]\n"
"v_exp_f32 v115,v115\n"
MF "a[96:99],v[16:19],v[88:91],a[96:99]\n"
"v_add_f32 v57,v57,v112\n"
"v_add_f32 v57,v57,v113\n"
MF "a[116:119],v[20:23],v[88:91],a[116:119]\n"
"s_waitcnt lgkmcnt(0)\n"
TR "v[16:17],v2 offset:51200\n"
TR "v[18:19],v2 offset:57344\n"
MF "a[136:139],v[24:27],v[88:91],a[136:139]\n"
"s_nop 0\n"
TR "v[20:21],v3 offset:51200\n"
TR "v[22:23],v3 offset:57344\n"
MF "a[156:159],v[28:31],v[88:91],a[156:159]\n"
CV "v94,v112,v113\n"
"v_add_f32 v57,v57,v114\n"
MF "a[152:155],v[28:31],v[84:87],a[152:155]\n"
"v_add_f32 v57,v57,v115\n"
CV "v95,v114,v115\n"
MF "a[132:135],v[24:27],v[84:87],a[132:135]\n"
"v_exp_f32 v116,v116\n"
MF "a[128:131],v[24:27],v[80:83],a[128:131]\n"
"v_exp_f32 v117,v117\n"
MF "a[148:151],v[28:31],v[80:83],a[148:151]\n"
"v_exp_f32 v118,v118\n"
MF "a[144:147],v[28:31],v[76:79],a[144:147]\n"
"v_exp_f32 v119,v119\n"
MF "a[124:127],v[24:27],v[76:79],a[124:127]\n"
"v_add_f32 v58,v58,v116\n"
"v_add_f32 v58,v58,v117\n"
MF "a[120:123],v[24:27],v[72:75],a[120:123]\n"
CV "v98,v116,v117\n"
"v_add_f32 v58,v58,v118\n"
MF "a[140:143],v[28:31],v[72:75],a[140:143]\n"
"s_waitcnt lgkmcnt(0)\n"
TR "v[24:25],v4 offset:51200\n"
TR "v[26:27],v4 offset:57344\n"
MF "a[160:163],v[16:19],v[72:75],a[160:163]\n"
"s_nop 0\n"
TR "v[28:29],v5 offset:51200\n"
TR "v[30:31],v5 offset:57344\n"
MF "a[180:183],v[20:23],v[72:75],a[180:183]\n"
"v_add_f32 v58,v58,v119\n"
CV "v99,v118,v119\n"
MF "a[184:187],v[20:23],v[76:79],a[184:187]\n"
"v_exp_f32 v120,v120\n"
MF "a[164:167],v[16:19],v[76:79],a[164:167]\n"
"v_exp_f32 v121,v121\n"
MF "a[168:171],v[16:19],v[80:83],a[168:171]\n"
"v_exp_f32 v122,v122\n"
MF "a[188:191],v[20:23],v[80:83],a[188:191]\n"
"v_exp_f32 v123,v123\n"
MF "a[192:195],v[20:23],v[84:87],a[192:195]\n"
"v_add_f32 v59,v59,v120\n"
"v_add_f32 v59,v59,v121\n"
MF "a[172:175],v[16:19],v[84:87],a[172:175]\n"
"s_waitcnt vmcnt(6) lgkmcnt(0)\n"
CV "v102,v120,v121\n"
"v_add_f32 v59,v59,v122\n"
MF "a[176:179],v[16:19],v[88:91],a[176:179]\n"
"s_barrier\n"
MF "a[196:199],v[20:23],v[88:91],a[196:199]\n"
"s_add_u32 s72,s72,6144\n"
"ds_read_b128 v[16:19],v0\n"
"ds_read_b128 v[20:23],v1\n"
MF "a[216:219],v[24:27],v[88:91],a[216:219]\n"
"s_cmp_eq_u32 s72,s17\n"
"s_cselect_b32 s72,s16,s72\n"
"s_add_u32 m0,s64,49152\n"
MF "a[236:239],v[28:31],v[88:91],a[236:239]\n"
LD "v7,s[32:35],s72 offen nt lds\n"
MF "a[232:235],v[28:31],v[84:87],a[232:235]\n"
"s_add_u32 m0,s64,50048\n"
"v_add_f32 v59,v59,v123\n"
CV "v103,v122,v123\n"
MF "a[212:215],v[24:27],v[84:87],a[212:215]\n"
LD "v7,s[32:35],s72 offen offset:128 nt lds\n"
MF "a[208:211],v[24:27],v[80:83],a[208:211]\n"
"s_add_u32 m0,s64,50944\n"
MF "a[228:231],v[28:31],v[80:83],a[228:231]\n"
LD "v7,s[32:35],s72 offen offset:256 nt lds\n"
MF "a[224:227],v[28:31],v[76:79],a[224:227]\n"
MF "a[204:207],v[24:27],v[76:79],a[204:207]\n"
"s_waitcnt lgkmcnt(0)\n"
MF "a[200:203],v[24:27],v[72:75],a[200:203]\n"
"s_cmp_lg_u32 s61,0\n"
MF "a[220:223],v[28:31],v[72:75],a[220:223]\n"
"s_cbranch_scc1 .Laloop\n"
".Lepi2:\n"
MF "v[72:75],v[16:19],v[136:139],v[32:35]\n"
"s_add_u32 s72,s72,6144\n"
"ds_read_b128 v[24:27],v0 offset:1024\n"
"ds_read_b128 v[28:31],v1 offset:1024\n"
MF "v[72:75],v[20:23],v[156:159],v[72:75]\n"
"s_add_u32 m0,s64,55296\n"
"v_exp_f32 v124,v124\n"
MF "v[76:79],v[20:23],v[160:163],v[36:39]\n"
LD "v7,s[32:35],s72 offen nt lds\n"
MF "v[76:79],v[16:19],v[140:143],v[76:79]\n"
"v_exp_f32 v125,v125\n"
MF "v[80:83],v[16:19],v[144:147],v[40:43]\n"
"s_add_u32 m0,s64,56192\n"
"v_exp_f32 v126,v126\n"
MF "v[80:83],v[20:23],v[164:167],v[80:83]\n"
LD "v7,s[32:35],s72 offen offset:128 nt lds\n"
MF "v[84:87],v[20:23],v[168:171],v[44:47]\n"
"v_exp_f32 v127,v127\n"
MF "v[84:87],v[16:19],v[148:151],v[84:87]\n"
"s_add_u32 m0,s64,57088\n"
"v_add_f32 v60,v60,v124\n"
"v_add_f32 v60,v60,v125\n"
MF "v[88:91],v[16:19],v[152:155],v[48:51]\n"
LD "v7,s[32:35],s72 offen offset:256 nt lds\n"
MF "v[88:91],v[20:23],v[172:175],v[88:91]\n"
"s_waitcnt lgkmcnt(0)\n"
"ds_read_b128 v[16:19],v0 offset:2048\n"
"ds_read_b128 v[20:23],v1 offset:2048\n"
MF "v[88:91],v[24:27],v[192:195],v[88:91]\n"
CV "v106,v124,v125\n"
"v_add_f32 v60,v60,v126\n"
MF "v[88:91],v[28:31],v[212:215],v[88:91]\n"
"v_add_f32 v60,v60,v127\n"
CV "v107,v126,v127\n"
MF "v[84:87],v[28:31],v[208:211],v[84:87]\n"
"v_exp_f32 v128,v128\n"
MF "v[84:87],v[24:27],v[188:191],v[84:87]\n"
"v_exp_f32 v129,v129\n"
MF "v[80:83],v[24:27],v[184:187],v[80:83]\n"
"v_exp_f32 v130,v130\n"
MF "v[80:83],v[28:31],v[204:207],v[80:83]\n"
"v_exp_f32 v131,v131\n"
MF "v[76:79],v[28:31],v[200:203],v[76:79]\n"
"v_add_f32 v61,v61,v128\n"
"v_add_f32 v61,v61,v129\n"
MF "v[76:79],v[24:27],v[180:183],v[76:79]\n"
CV "v110,v128,v129\n"
"v_add_f32 v61,v61,v130\n"
MF "v[72:75],v[24:27],v[176:179],v[72:75]\n"
"v_add_f32 v61,v61,v131\n"
CV "v111,v130,v131\n"
MF "v[72:75],v[28:31],v[196:199],v[72:75]\n"
"s_waitcnt lgkmcnt(0)\n"
"ds_read_b128 v[24:27],v0 offset:6144\n"
"ds_read_b128 v[28:31],v1 offset:6144\n"
MF "v[72:75],v[16:19],v[216:219],v[72:75]\n"
MF "v[72:75],v[20:23],v[236:239],v[72:75]\n"
MF "v[76:79],v[20:23],v[240:243],v[76:79]\n"
MF "v[76:79],v[16:19],v[220:223],v[76:79]\n"
MF "v[80:83],v[16:19],v[224:227],v[80:83]\n"
MF "v[80:83],v[20:23],v[244:247],v[80:83]\n"
MF "v[84:87],v[20:23],v[248:251],v[84:87]\n"
MF "v[84:87],v[16:19],v[228:231],v[84:87]\n"
"v_max3_f32 v10,v57,v58,v59\n"
"v_max3_f32 v10,v10,v60,v61\n"
MF "v[88:91],v[16:19],v[232:235],v[88:91]\n"
"v_cmp_le_f32 vcc,0x47800000,v10\n"
MF "v[88:91],v[20:23],v[252:255],v[88:91]\n"
"s_waitcnt lgkmcnt(0)\n"
"s_cbranch_vccnz .Lstub_26\n"
".Lback_26:\n"
MF "v[112:115],v[24:27],v[136:139],v[32:35]\n"
"s_add_u32 s74,s74,12288\n"
"ds_read_b128 v[16:19],v0 offset:7168\n"
"ds_read_b128 v[20:23],v1 offset:7168\n"
MF "v[112:115],v[28:31],v[156:159],v[112:115]\n"
"s_cmp_eq_u32 s74,s20\n"
"s_cselect_b32 s74,s70,s74\n"
MF "v[116:119],v[28:31],v[160:163],v[36:39]\n"
"v_exp_f32 v72,v72\n"
MF "v[116:119],v[24:27],v[140:143],v[116:119]\n"
"v_exp_f32 v73,v73\n"
MF "v[120:123],v[24:27],v[144:147],v[40:43]\n"
"v_exp_f32 v74,v74\n"
MF "v[120:123],v[28:31],v[164:167],v[120:123]\n"
"v_exp_f32 v75,v75\n"
MF "v[124:127],v[28:31],v[168:171],v[44:47]\n"
"v_add_f32 v52,v57,v72\n"
"v_add_f32 v52,v52,v73\n"
MF "v[124:127],v[24:27],v[148:151],v[124:127]\n"
CV "v72,v72,v73\n"
"v_add_f32 v52,v52,v74\n"
MF "v[128:131],v[24:27],v[152:155],v[48:51]\n"
"v_add_f32 v52,v52,v75\n"
CV "v73,v74,v75\n"
MF "v[128:131],v[28:31],v[172:175],v[128:131]\n"
"s_waitcnt lgkmcnt(0)\n"
"ds_read_b128 v[24:27],v0 offset:8192\n"
"ds_read_b128 v[28:31],v1 offset:8192\n"
MF "v[128:131],v[16:19],v[192:195],v[128:131]\n"
"v_exp_f32 v76,v76\n"
MF "v[128:131],v[20:23],v[212:215],v[128:131]\n"
"v_exp_f32 v77,v77\n"
MF "v[124:127],v[20:23],v[208:211],v[124:127]\n"
"v_exp_f32 v78,v78\n"
MF "v[124:127],v[16:19],v[188:191],v[124:127]\n"
"v_exp_f32 v79,v79\n"
MF "v[120:123],v[16:19],v[184:187],v[120:123]\n"
"v_add_f32 v53,v58,v76\n"
"v_add_f32 v53,v53,v77\n"
MF "v[120:123],v[20:23],v[204:207],v[120:123]\n"
CV "v76,v76,v77\n"
"v_add_f32 v53,v53,v78\n"
MF "v[116:119],v[20:23],v[200:203],v[116:119]\n"
"s_waitcnt vmcnt(6) lgkmcnt(0)\n"
"v_add_f32 v53,v53,v79\n"
CV "v77,v78,v79\n"
MF "v[116:119],v[16:19],v[180:183],v[116:119]\n"
"s_barrier\n"
MF "v[112:115],v[16:19],v[176:179],v[112:115]\n"
"v_lshrrev_b32 v7,7,v9\n"
"v_mad_u32_u24 v7,v7,s6,v8\n"
MF "v[112:115],v[20:23],v[196:199],v[112:115]\n"
"s_nop 0\n"
TR "v[16:17],v2 offset:24576\n"
TR "v[18:19],v2 offset:30720\n"
MF "v[112:115],v[24:27],v[216:219],v[112:115]\n"
"s_add_u32 s73,s73,12288\n"
TR "v[20:21],v3 offset:24576\n"
TR "v[22:23],v3 offset:30720\n"
MF "v[112:115],v[28:31],v[236:239],v[112:115]\n"
"s_cmp_eq_u32 s73,s19\n"
"s_cselect_b32 s73,s18,s73\n"
"s_add_u32 m0,s65,0\n"
MF "v[116:119],v[28:31],v[240:243],v[116:119]\n"
LD "v8,s[36:39],s73 offen nt lds\n"
MF "v[116:119],v[24:27],v[220:223],v[116:119]\n"
"s_add_u32 m0,s65,896\n"
"v_exp_f32 v80,v80\n"
MF "v[120:123],v[24:27],v[224:227],v[120:123]\n"
LD "v8,s[36:39],s73 offen offset:128 nt lds\n"
MF "v[120:123],v[28:31],v[244:247],v[120:123]\n"
"v_exp_f32 v81,v81\n"
MF "v[124:127],v[28:31],v[248:251],v[124:127]\n"
"s_add_u32 m0,s65,1792\n"
"v_exp_f32 v82,v82\n"
MF "v[124:127],v[24:27],v[228:231],v[124:127]\n"
LD "v8,s[36:39],s73 offen offset:256 nt lds\n"
MF "v[128:131],v[24:27],v[232:235],v[128:131]\n"
"v_exp_f32 v83,v83\n"
MF "v[128:131],v[28:31],v[252:255],v[128:131]\n"
"s_waitcnt lgkmcnt(0)\n"
TR "v[24:25],v4 offset:24576\n"
TR "v[26:27],v4 offset:30720\n"
MF "a[0:3],v[16:19],v[92:95],a[0:3]\n"
"s_add_u32 m0,s65,0\n"
TR "v[28:29],v5 offset:24576\n"
TR "v[30:31],v5 offset:30720\n"
MF "a[20:23],v[20:23],v[92:95],a[20:23]\n"
LD "v8,s[36:39],s73 offen offset:3072 nt lds\n"
MF "a[24:27],v[20:23],v[96:99],a[24:27]\n"
"v_add_f32 v54,v59,v80\n"
"v_add_f32 v54,v54,v81\n"
MF "a[4:7],v[16:19],v[96:99],a[4:7]\n"
"s_add_u32 m0,s65,896\n"
CV "v80,v80,v81\n"
"v_add_f32 v54,v54,v82\n"
MF "a[8:11],v[16:19],v[100:103],a[8:11]\n"
LD "v8,s[36:39],s73 offen offset:3200 nt lds\n"
MF "a[28:31],v[20:23],v[100:103],a[28:31]\n"
"v_add_f32 v54,v54,v83\n"
CV "v81,v82,v83\n"
MF "a[32:35],v[20:23],v[104:107],a[32:35]\n"
"s_add_u32 m0,s65,1792\n"
"v_exp_f32 v84,v84\n"
MF "a[12:15],v[16:19],v[104:107],a[12:15]\n"
LD "v8,s[36:39],s73 offen offset:3328 nt lds\n"
MF "a[16:19],v[16:19],v[108:111],a[16:19]\n"
"v_exp_f32 v85,v85\n"
MF "a[36:39],v[20:23],v[108:111],a[36:39]\n"
"s_waitcnt lgkmcnt(0)\n"
TR "v[16:17],v2 offset:25600\n"
TR "v[18:19],v2 offset:31744\n"
MF "a[56:59],v[24:27],v[108:111],a[56:59]\n"
"s_nop 0\n"
TR "v[20:21],v3 offset:25600\n"
TR "v[22:23],v3 offset:31744\n"
MF "a[76:79],v[28:31],v[108:111],a[76:79]\n"
"v_exp_f32 v86,v86\n"
MF "a[72:75],v[28:31],v[104:107],a[72:75]\n"
"v_exp_f32 v87,v87\n"
MF "a[52:55],v[24:27],v[104:107],a[52:55]\n"
"v_add_f32 v55,v60,v84\n"
"v_add_f32 v55,v55,v85\n"
MF "a[48:51],v[24:27],v[100:103],a[48:51]\n"
CV "v84,v84,v85\n"
"v_add_f32 v55,v55,v86\n"
MF "a[68:71],v[28:31],v[100:103],a[68:71]\n"
"v_add_f32 v55,v55,v87\n"
CV "v85,v86,v87\n"
MF "a[64:67],v[28:31],v[96:99],a[64:67]\n"
"v_exp_f32 v88,v88\n"
MF "a[44:47],v[24:27],v[96:99],a[44:47]\n"
"v_exp_f32 v89,v89\n"
MF "a[40:43],v[24:27],v[92:95],a[40:43]\n"
"v_exp_f32 v90,v90\n"
MF "a[60:63],v[28:31],v[92:95],a[60:63]\n"
"s_waitcnt lgkmcnt(0)\n"
TR "v[24:25],v4 offset:25600\n"
TR "v[26:27],v4 offset:31744\n"
MF "a[80:83],v[16:19],v[92:95],a[80:83]\n"
"s_nop 0\n"
TR "v[28:29],v5 offset:25600\n"
TR "v[30:31],v5 offset:31744\n"
MF "a[100:103],v[20:23],v[92:95],a[100:103]\n"
"v_exp_f32 v91,v91\n"
MF "a[104:107],v[20:23],v[96:99],a[104:107]\n"
"v_add_f32 v56,v61,v88\n"
"v_add_f32 v56,v56,v89\n"
MF "a[84:87],v[16:19],v[96:99],a[84:87]\n"
CV "v88,v88,v89\n"
"v_add_f32 v56,v56,v90\n"
MF "a[88:91],v[16:19],v[100:103],a[88:91]\n"
"v_add_f32 v56,v56,v91\n"
CV "v89,v90,v91\n"
MF "a[108:111],v[20:23],v[100:103],a[108:111]\n"
"v_exp_f32 v112,v112\n"
MF "a[112:115],v[20:23],v[104:107],a[112:115]\n"
"v_exp_f32 v113,v113\n"
MF "a[92:95],v[16:19],v[104:107],a[92:95]\n"
"v_exp_f32 v114,v114\n"
MF "a[96:99],v[16:19],v[108:111],a[96:99]\n"
"v_exp_f32 v115,v115\n"
MF "a[116:119],v[20:23],v[108:111],a[116:119]\n"
"s_waitcnt lgkmcnt(0)\n"
TR "v[16:17],v2 offset:26624\n"
TR "v[18:19],v2 offset:32768\n"
MF "a[136:139],v[24:27],v[108:111],a[136:139]\n"
"s_nop 0\n"
TR "v[20:21],v3 offset:26624\n"
TR "v[22:23],v3 offset:32768\n"
MF "a[156:159],v[28:31],v[108:111],a[156:159]\n"
"v_add_f32 v52,v52,v112\n"
"v_add_f32 v52,v52,v113\n"
MF "a[152:155],v[28:31],v[104:107],a[152:155]\n"
CV "v74,v112,v113\n"
"v_add_f32 v52,v52,v114\n"
MF "a[132:135],v[24:27],v[104:107],a[132:135]\n"
"v_add_f32 v52,v52,v115\n"
CV "v75,v114,v115\n"
MF "a[128:131],v[24:27],v[100:103],a[128:131]\n"
"v_exp_f32 v116,v116\n"
MF "a[148:151],v[28:31],v[100:103],a[148:151]\n"
"v_exp_f32 v117,v117\n"
MF "a[144:147],v[28:31],v[96:99],a[144:147]\n"
"v_exp_f32 v118,v118\n"
MF "a[124:127],v[24:27],v[96:99],a[124:127]\n"
"v_exp_f32 v119,v119\n"
MF "a[120:123],v[24:27],v[92:95],a[120:123]\n"
"v_add_f32 v53,v53,v116\n"
"v_add_f32 v53,v53,v117\n"
MF "a[140:143],v[28:31],v[92:95],a[140:143]\n"
"s_waitcnt lgkmcnt(0)\n"
TR "v[24:25],v4 offset:26624\n"
TR "v[26:27],v4 offset:32768\n"
MF "a[160:163],v[16:19],v[92:95],a[160:163]\n"
"s_nop 0\n"
TR "v[28:29],v5 offset:26624\n"
TR "v[30:31],v5 offset:32768\n"
MF "a[180:183],v[20:23],v[92:95],a[180:183]\n"
CV "v78,v116,v117\n"
"v_add_f32 v53,v53,v118\n"
MF "a[184:187],v[20:23],v[96:99],a[184:187]\n"
"v_add_f32 v53,v53,v119\n"
CV "v79,v118,v119\n"
MF "a[164:167],v[16:19],v[96:99],a[164:167]\n"
"v_exp_f32 v120,v120\n"
MF "a[168:171],v[16:19],v[100:103],a[168:171]\n"
"v_exp_f32 v121,v121\n"
MF "a[188:191],v[20:23],v[100:103],a[188:191]\n"
"v_exp_f32 v122,v122\n"
MF "a[192:195],v[20:23],v[104:107],a[192:195]\n"
"v_exp_f32 v123,v123\n"
MF "a[172:175],v[16:19],v[104:107],a[172:175]\n"
"s_waitcnt vmcnt(6) lgkmcnt(0)\n"
"v_add_f32 v54,v54,v120\n"
"v_add_f32 v54,v54,v121\n"
MF "a[176:179],v[16:19],v[108:111],a[176:179]\n"
"s_barrier\n"
MF "a[196:199],v[20:23],v[108:111],a[196:199]\n"
"s_add_u32 s60,s0,0\n"
"ds_read_b128 v[16:19],v0 offset:49152\n"
"ds_read_b128 v[20:23],v1 offset:49152\n"
MF "a[216:219],v[24:27],v[108:111],a[216:219]\n"
"s_add_u32 m0,s5,24576\n"
CV "v82,v120,v121\n"
"v_add_f32 v54,v54,v122\n"
MF "a[236:239],v[28:31],v[108:111],a[236:239]\n"
LD "v7,s[48:51],s60 offen lds\n"
MF "a[232:235],v[28:31],v[104:107],a[232:235]\n"
"s_add_u32 s60,s0,10240\n"
"s_add_u32 m0,s5,25600\n"
"v_add_f32 v54,v54,v123\n"
MF "a[212:215],v[24:27],v[104:107],a[212:215]\n"
LD "v7,s[48:51],s60 offen lds\n"
MF "a[208:211],v[24:27],v[100:103],a[208:211]\n"
"s_add_u32 s60,s0,20480\n"
"s_add_u32 m0,s5,26624\n"
CV "v83,v122,v123\n"
MF "a[228:231],v[28:31],v[100:103],a[228:231]\n"
LD "v7,s[48:51],s60 offen lds\n"
MF "a[224:227],v[28:31],v[96:99],a[224:227]\n"
MF "a[204:207],v[24:27],v[96:99],a[204:207]\n"
MF "a[200:203],v[24:27],v[92:95],a[200:203]\n"
MF "a[220:223],v[28:31],v[92:95],a[220:223]\n"
"s_waitcnt lgkmcnt(0)\n"
MF "v[92:95],v[16:19],v[136:139],v[32:35]\n"
"s_add_u32 s60,s0,30720\n"
"ds_read_b128 v[24:27],v0 offset:50176\n"
"ds_read_b128 v[28:31],v1 offset:50176\n"
MF "v[92:95],v[20:23],v[156:159],v[92:95]\n"
"s_add_u32 m0,s5,27648\n"
"v_exp_f32 v124,v124\n"
MF "v[96:99],v[20:23],v[160:163],v[36:39]\n"
LD "v7,s[48:51],s60 offen lds\n"
MF "v[96:99],v[16:19],v[140:143],v[96:99]\n"
"s_add_u32 s60,s0,40960\n"
"v_exp_f32 v125,v125\n"
MF "v[100:103],v[16:19],v[144:147],v[40:43]\n"
"s_add_u32 m0,s5,28672\n"
"v_exp_f32 v126,v126\n"
MF "v[100:103],v[20:23],v[164:167],v[100:103]\n"
LD "v7,s[48:51],s60 offen lds\n"
MF "v[104:107],v[20:23],v[168:171],v[44:47]\n"
"s_add_u32 s60,s0,51200\n"
"v_exp_f32 v127,v127\n"
MF "v[104:107],v[16:19],v[148:151],v[104:107]\n"
"s_add_u32 m0,s5,29696\n"
"v_add_f32 v55,v55,v124\n"
"v_add_f32 v55,v55,v125\n"
MF "v[108:111],v[16:19],v[152:155],v[48:51]\n"
LD "v7,s[48:51],s60 offen lds\n"
MF "v[108:111],v[20:23],v[172:175],v[108:111]\n"
"s_waitcnt lgkmcnt(0)\n"
"ds_read_b128 v[16:19],v0 offset:51200\n"
"ds_read_b128 v[20:23],v1 offset:51200\n"
MF "v[108:111],v[24:27],v[192:195],v[108:111]\n"
CV "v86,v124,v125\n"
"v_add_f32 v55,v55,v126\n"
MF "v[108:111],v[28:31],v[212:215],v[108:111]\n"
"v_add_f32 v55,v55,v127\n"
CV "v87,v126,v127\n"
MF "v[104:107],v[28:31],v[208:211],v[104:107]\n"
"v_exp_f32 v128,v128\n"
MF "v[104:107],v[24:27],v[188:191],v[104:107]\n"
"v_exp_f32 v129,v129\n"
MF "v[100:103],v[24:27],v[184:187],v[100:103]\n"
"v_exp_f32 v130,v130\n"
MF "v[100:103],v[28:31],v[204:207],v[100:103]\n"
"v_exp_f32 v131,v131\n"
MF "v[96:99],v[28:31],v[200:203],v[96:99]\n"
"v_add_f32 v56,v56,v128\n"
"v_add_f32 v56,v56,v129\n"
MF "v[96:99],v[24:27],v[180:183],v[96:99]\n"
CV "v90,v128,v129\n"
"v_add_f32 v56,v56,v130\n"
MF "v[92:95],v[24:27],v[176:179],v[92:95]\n"
"v_add_f32 v56,v56,v131\n"
CV "v91,v130,v131\n"
MF "v[92:95],v[28:31],v[196:199],v[92:95]\n"
"s_waitcnt lgkmcnt(0)\n"
"ds_read_b128 v[24:27],v0 offset:55296\n"
"ds_read_b128 v[28:31],v1 offset:55296\n"
MF "v[92:95],v[16:19],v[216:219],v[92:95]\n"
MF "v[92:95],v[20:23],v[236:239],v[92:95]\n"
MF "v[96:99],v[20:23],v[240:243],v[96:99]\n"
MF "v[96:99],v[16:19],v[220:223],v[96:99]\n"
MF "v[100:103],v[16:19],v[224:227],v[100:103]\n"
MF "v[100:103],v[20:23],v[244:247],v[100:103]\n"
MF "v[104:107],v[20:23],v[248:251],v[104:107]\n"
MF "v[104:107],v[16:19],v[228:231],v[104:107]\n"
"v_max3_f32 v10,v52,v53,v54\n"
"v_max3_f32 v10,v10,v55,v56\n"
MF "v[108:111],v[16:19],v[232:235],v[108:111]\n"
"v_cmp_le_f32 vcc,0x47800000,v10\n"
MF "v[108:111],v[20:23],v[252:255],v[108:111]\n"
"s_waitcnt lgkmcnt(0)\n"
"s_cbranch_vccnz .Lstub_27\n"
".Lback_27:\n"
MF "v[112:115],v[24:27],v[136:139],v[32:35]\n"
"s_add_u32 s74,s74,12288\n"
"ds_read_b128 v[16:19],v0 offset:56320\n"
"ds_read_b128 v[20:23],v1 offset:56320\n"
MF "v[112:115],v[28:31],v[156:159],v[112:115]\n"
"s_cmp_eq_u32 s74,s20\n"
"s_cselect_b32 s74,s70,s74\n"
MF "v[116:119],v[28:31],v[160:163],v[36:39]\n"
"v_exp_f32 v92,v92\n"
MF "v[116:119],v[24:27],v[140:143],v[116:119]\n"
"v_exp_f32 v93,v93\n"
MF "v[120:123],v[24:27],v[144:147],v[40:43]\n"
"v_exp_f32 v94,v94\n"
MF "v[120:123],v[28:31],v[164:167],v[120:123]\n"
"v_exp_f32 v95,v95\n"
MF "v[124:127],v[28:31],v[168:171],v[44:47]\n"
"v_add_f32 v57,v52,v92\n"
"v_add_f32 v57,v57,v93\n"
MF "v[124:127],v[24:27],v[148:151],v[124:127]\n"
CV "v92,v92,v93\n"
"v_add_f32 v57,v57,v94\n"
MF "v[128:131],v[24:27],v[152:155],v[48:51]\n"
"v_add_f32 v57,v57,v95\n"
CV "v93,v94,v95\n"
MF "v[128:131],v[28:31],v[172:175],v[128:131]\n"
"s_waitcnt lgkmcnt(0)\n"
"ds_read_b128 v[24:27],v0 offset:57344\n"
"ds_read_b128 v[28:31],v1 offset:57344\n"
MF "v[128:131],v[16:19],v[192:195],v[128:131]\n"
"v_exp_f32 v96,v96\n"
MF "v[128:131],v[20:23],v[212:215],v[128:131]\n"
"v_exp_f32 v97,v97\n"
MF "v[124:127],v[20:23],v[208:211],v[124:127]\n"
"v_exp_f32 v98,v98\n"
MF "v[124:127],v[16:19],v[188:191],v[124:127]\n"
"v_exp_f32 v99,v99\n"
MF "v[120:123],v[16:19],v[184:187],v[120:123]\n"
"v_add_f32 v58,v53,v96\n"
"v_add_f32 v58,v58,v97\n"
MF "v[120:123],v[20:23],v[204:207],v[120:123]\n"
CV "v96,v96,v97\n"
"v_add_f32 v58,v58,v98\n"
MF "v[116:119],v[20:23],v[200:203],v[116:119]\n"
"s_waitcnt vmcnt(6) lgkmcnt(0)\n"
"v_add_f32 v58,v58,v99\n"
CV "v97,v98,v99\n"
MF "v[116:119],v[16:19],v[180:183],v[116:119]\n"
"s_barrier\n"
MF "v[112:115],v[16:19],v[176:179],v[112:115]\n"
"v_exp_f32 v100,v100\n"
MF "v[112:115],v[20:23],v[196:199],v[112:115]\n"
"s_nop 0\n"
TR "v[16:17],v2\n"
TR "v[18:19],v2 offset:6144\n"
MF "v[112:115],v[24:27],v[216:219],v[112:115]\n"
"s_add_u32 s73,s73,12288\n"
TR "v[20:21],v3\n"
TR "v[22:23],v3 offset:6144\n"
MF "v[112:115],v[28:31],v[236:239],v[112:115]\n"
"s_cmp_eq_u32 s73,s19\n"
"s_cselect_b32 s73,s18,s73\n"
"s_add_u32 m0,s65,49152\n"
MF "v[116:119],v[28:31],v[240:243],v[116:119]\n"
LD "v8,s[36:39],s73 offen nt lds\n"
MF "v[116:119],v[24:27],v[220:223],v[116:119]\n"
"s_add_u32 m0,s65,50048\n"
"v_exp_f32 v101,v101\n"
MF "v[120:123],v[24:27],v[224:227],v[120:123]\n"
LD "v8,s[36:39],s73 offen offset:128 nt lds\n"
MF "v[120:123],v[28:31],v[244:247],v[120:123]\n"
"v_exp_f32 v102,v102\n"
MF "v[124:127],v[28:31],v[248:251],v[124:127]\n"
"s_add_u32 m0,s65,50944\n"
"v_exp_f32 v103,v103\n"
MF "v[124:127],v[24:27],v[228:231],v[124:127]\n"
LD "v8,s[36:39],s73 offen offset:256 nt lds\n"
MF "v[128:131],v[24:27],v[232:235],v[128:131]\n"
"v_add_f32 v59,v54,v100\n"
"v_add_f32 v59,v59,v101\n"
MF "v[128:131],v[28:31],v[252:255],v[128:131]\n"
"s_waitcnt lgkmcnt(0)\n"
TR "v[24:25],v4\n"
TR "v[26:27],v4 offset:6144\n"
MF "a[0:3],v[16:19],v[72:75],a[0:3]\n"
"s_add_u32 m0,s65,49152\n"
TR "v[28:29],v5\n"
TR "v[30:31],v5 offset:6144\n"
MF "a[20:23],v[20:23],v[72:75],a[20:23]\n"
LD "v8,s[36:39],s73 offen offset:3072 nt lds\n"
MF "a[24:27],v[20:23],v[76:79],a[24:27]\n"
CV "v100,v100,v101\n"
"v_add_f32 v59,v59,v102\n"
MF "a[4:7],v[16:19],v[76:79],a[4:7]\n"
"s_add_u32 m0,s65,50048\n"
"v_add_f32 v59,v59,v103\n"
CV "v101,v102,v103\n"
MF "a[8:11],v[16:19],v[80:83],a[8:11]\n"
LD "v8,s[36:39],s73 offen offset:3200 nt lds\n"
MF "a[28:31],v[20:23],v[80:83],a[28:31]\n"
"v_exp_f32 v104,v104\n"
MF "a[32:35],v[20:23],v[84:87],a[32:35]\n"
"s_add_u32 m0,s65,50944\n"
"v_exp_f32 v105,v105\n"
MF "a[12:15],v[16:19],v[84:87],a[12:15]\n"
LD "v8,s[36:39],s73 offen offset:3328 nt lds\n"
MF "a[16:19],v[16:19],v[88:91],a[16:19]\n"
"v_exp_f32 v106,v106\n"
MF "a[36:39],v[20:23],v[88:91],a[36:39]\n"
"s_waitcnt lgkmcnt(0)\n"
TR "v[16:17],v2 offset:1024\n"
TR "v[18:19],v2 offset:7168\n"
MF "a[56:59],v[24:27],v[88:91],a[56:59]\n"
"s_nop 0\n"
TR "v[20:21],v3 offset:1024\n"
TR "v[22:23],v3 offset:7168\n"
MF "a[76:79],v[28:31],v[88:91],a[76:79]\n"
"v_exp_f32 v107,v107\n"
MF "a[72:75],v[28:31],v[84:87],a[72:75]\n"
"v_add_f32 v60,v55,v104\n"
"v_add_f32 v60,v60,v105\n"
MF "a[52:55],v[24:27],v[84:87],a[52:55]\n"
CV "v104,v104,v105\n"
"v_add_f32 v60,v60,v106\n"
MF "a[48:51],v[24:27],v[80:83],a[48:51]\n"
"v_add_f32 v60,v60,v107\n"
CV "v105,v106,v107\n"
MF "a[68:71],v[28:31],v[80:83],a[68:71]\n"
"v_exp_f32 v108,v108\n"
MF "a[64:67],v[28:31],v[76:79],a[64:67]\n"
"v_exp_f32 v109,v109\n"
MF "a[44:47],v[24:27],v[76:79],a[44:47]\n"
"v_exp_f32 v110,v110\n"
MF "a[40:43],v[24:27],v[72:75],a[40:43]\n"
"v_exp_f32 v111,v111\n"
MF "a[60:63],v[28:31],v[72:75],a[60:63]\n"
"s_waitcnt lgkmcnt(0)\n"
TR "v[24:25],v4 offset:1024\n"
TR "v[26:27],v4 offset:7168\n"
MF "a[80:83],v[16:19],v[72:75],a[80:83]\n"
"s_nop 0\n"
TR "v[28:29],v5 offset:1024\n"
TR "v[30:31],v5 offset:7168\n"
MF "a[100:103],v[20:23],v[72:75],a[100:103]\n"
"v_add_f32 v61,v56,v108\n"
"v_add_f32 v61,v61,v109\n"
MF "a[104:107],v[20:23],v[76:79],a[104:107]\n"
CV "v108,v108,v109\n"
"v_add_f32 v61,v61,v110\n"
MF "a[84:87],v[16:19],v[76:79],a[84:87]\n"
"v_add_f32 v61,v61,v111\n"
CV "v109,v110,v111\n"
MF "a[88:91],v[16:19],v[80:83],a[88:91]\n"
"v_exp_f32 v112,v112\n"
MF "a[108:111],v[20:23],v[80:83],a[108:111]\n"
"v_exp_f32 v113,v113\n"
MF "a[112:115],v[20:23],v[84:87],a[112:115]\n"
"v_exp_f32 v114,v114\n"
MF "a[92:95],v[16:19],v[84:87],a[92:95]\n"
"v_exp_f32 v115,v115\n"
MF "a[96:99],v[16:19],v[88:91],a[96:99]\n"
"v_add_f32 v57,v57,v112\n"
"v_add_f32 v57,v57,v113\n"
MF "a[116:119],v[20:23],v[88:91],a[116:119]\n"
"s_waitcnt lgkmcnt(0)\n"
TR "v[16:17],v2 offset:2048\n"
TR "v[18:19],v2 offset:8192\n"
MF "a[136:139],v[24:27],v[88:91],a[136:139]\n"
"s_nop 0\n"
TR "v[20:21],v3 offset:2048\n"
TR "v[22:23],v3 offset:8192\n"
MF "a[156:159],v[28:31],v[88:91],a[156:159]\n"
CV "v94,v112,v113\n"
"v_add_f32 v57,v57,v114\n"
MF "a[152:155],v[28:31],v[84:87],a[152:155]\n"
"v_add_f32 v57,v57,v115\n"
CV "v95,v114,v115\n"
MF "a[132:135],v[24:27],v[84:87],a[132:135]\n"
"v_exp_f32 v116,v116\n"
MF "a[128:131],v[24:27],v[80:83],a[128:131]\n"
"v_exp_f32 v117,v117\n"
MF "a[148:151],v[28:31],v[80:83],a[148:151]\n"
"v_exp_f32 v118,v118\n"
MF "a[144:147],v[28:31],v[76:79],a[144:147]\n"
"v_exp_f32 v119,v119\n"
MF "a[124:127],v[24:27],v[76:79],a[124:127]\n"
"v_add_f32 v58,v58,v116\n"
"v_add_f32 v58,v58,v117\n"
MF "a[120:123],v[24:27],v[72:75],a[120:123]\n"
CV "v98,v116,v117\n"
"v_add_f32 v58,v58,v118\n"
MF "a[140:143],v[28:31],v[72:75],a[140:143]\n"
"s_waitcnt lgkmcnt(0)\n"
TR "v[24:25],v4 offset:2048\n"
TR "v[26:27],v4 offset:8192\n"
MF "a[160:163],v[16:19],v[72:75],a[160:163]\n"
"s_nop 0\n"
TR "v[28:29],v5 offset:2048\n"
TR "v[30:31],v5 offset:8192\n"
MF "a[180:183],v[20:23],v[72:75],a[180:183]\n"
"v_add_f32 v58,v58,v119\n"
CV "v99,v118,v119\n"
MF "a[184:187],v[20:23],v[76:79],a[184:187]\n"
"v_exp_f32 v120,v120\n"
MF "a[164:167],v[16:19],v[76:79],a[164:167]\n"
".Lpg23h:\n"
"s_cbranch_execz .Lpg27h\n"
"v_exp_f32 v121,v121\n"
MF "a[168:171],v[16:19],v[80:83],a[168:171]\n"
"v_exp_f32 v122,v122\n"
MF "a[188:191],v[20:23],v[80:83],a[188:191]\n"
"v_exp_f32 v123,v123\n"
MF "a[192:195],v[20:23],v[84:87],a[192:195]\n"
"v_add_f32 v59,v59,v120\n"
"v_add_f32 v59,v59,v121\n"
MF "a[172:175],v[16:19],v[84:87],a[172:175]\n"
"s_waitcnt lgkmcnt(0)\n"
CV "v102,v120,v121\n"
"v_add_f32 v59,v59,v122\n"
MF "a[176:179],v[16:19],v[88:91],a[176:179]\n"
"s_barrier\n"
MF "a[196:199],v[20:23],v[88:91],a[196:199]\n"
"s_add_u32 s60,s1,0\n"
"s_add_u32 m0,s5,0\n"
"v_add_f32 v59,v59,v123\n"
MF "a[216:219],v[24:27],v[88:91],a[216:219]\n"
LD "v7,s[48:51],s60 offen lds\n"
MF "a[236:239],v[28:31],v[88:91],a[236:239]\n"
"s_add_u32 s60,s1,10240\n"
"s_add_u32 m0,s5,1024\n"
CV "v103,v122,v123\n"
MF "a[232:235],v[28:31],v[84:87],a[232:235]\n"
LD "v7,s[48:51],s60 offen lds\n"
MF "a[212:215],v[24:27],v[84:87],a[212:215]\n"
"s_add_u32 s60,s1,20480\n"
"s_add_u32 m0,s5,2048\n"
MF "a[208:211],v[24:27],v[80:83],a[208:211]\n"
LD "v7,s[48:51],s60 offen lds\n"
MF "a[228:231],v[28:31],v[80:83],a[228:231]\n"
"v_exp_f32 v124,v124\n"
MF "a[224:227],v[28:31],v[76:79],a[224:227]\n"
"v_exp_f32 v125,v125\n"
MF "a[204:207],v[24:27],v[76:79],a[204:207]\n"
"v_exp_f32 v126,v126\n"
MF "a[200:203],v[24:27],v[72:75],a[200:203]\n"
"v_exp_f32 v127,v127\n"
MF "a[220:223],v[28:31],v[72:75],a[220:223]\n"
"v_add_f32 v60,v60,v124\n"
"v_add_f32 v60,v60,v125\n"
CV "v106,v124,v125\n"
"v_add_f32 v60,v60,v126\n"
"v_add_f32 v60,v60,v127\n"
CV "v107,v126,v127\n"
"v_exp_f32 v128,v128\n"
"v_exp_f32 v129,v129\n"
"v_exp_f32 v130,v130\n"
"v_exp_f32 v131,v131\n"
"v_add_f32 v61,v61,v128\n"
"v_add_f32 v61,v61,v129\n"
CV "v110,v128,v129\n"
"v_add_f32 v61,v61,v130\n"
"v_add_f32 v61,v61,v131\n"
CV "v111,v130,v131\n"
"v_max3_f32 v10,v57,v58,v59\n"
"v_max3_f32 v10,v10,v60,v61\n"
"v_cmp_le_f32 vcc,0x47800000,v10\n"
"s_cbranch_vccnz .Lstub_28\n"
".Lback_28:\n"
"s_waitcnt vmcnt(3) lgkmcnt(0)\n"
"s_barrier\n"
"v_add_u32 v236,s9,v9\n"
"v_add_u32 v237,65536,v236\n"
"v_mov_b32 v10,1.0\n"
TR "v[160:161],v4 offset:50176\n"
TR "v[162:163],v4 offset:56320\n"
"v_mfma_f32_16x16x4_f32 v[16:19],v10,v57,0\n"
TR "v[164:165],v5 offset:50176\n"
TR "v[166:167],v5 offset:56320\n"
TR "v[168:169],v2 offset:51200\n"
TR "v[170:171],v2 offset:57344\n"
TR "v[172:173],v3 offset:51200\n"
TR "v[174:175],v3 offset:57344\n"
"v_mfma_f32_16x16x4_f32 v[20:23],v10,v58,0\n"
TR "v[176:177],v4 offset:51200\n"
TR "v[178:179],v4 offset:57344\n"
TR "v[180:181],v5 offset:51200\n"
TR "v[182:183],v5 offset:57344\n"
TR "v[136:137],v2 offset:49152\n"
TR "v[138:139],v2 offset:55296\n"
"v_mfma_f32_16x16x4_f32 v[24:27],v10,v59,0\n"
TR "v[140:141],v3 offset:49152\n"
TR "v[142:143],v3 offset:55296\n"
TR "v[144:145],v4 offset:49152\n"
TR "v[146:147],v4 offset:55296\n"
TR "v[148:149],v5 offset:49152\n"
TR "v[150:151],v5 offset:55296\n"
"v_mfma_f32_16x16x4_f32 v[28:31],v10,v60,0\n"
TR "v[152:153],v2 offset:50176\n"
TR "v[154:155],v2 offset:56320\n"
TR "v[156:157],v3 offset:50176\n"
TR "v[158:159],v3 offset:56320\n"
"v_mfma_f32_16x16x4_f32 v[32:35],v10,v61,0\n"
"s_add_u32 m0,s5,0\n"
"v_add_u32 v195,27648,v7\n"
LD "v195,s[48:51],s1 offen offset:3072 lds\n"
"s_add_u32 m0,s5,4096\n"
"v_add_u32 v196,40960,v7\n"
LD "v196,s[48:51],s1 offen lds\n"
"s_waitcnt lgkmcnt(15)\n"
MF "a[120:123],v[160:163],v[92:95],a[120:123]\n"
MF "a[140:143],v[164:167],v[92:95],a[140:143]\n"
"v_add_u32 v197,50176,v7\n"
LD "v197,s[48:51],s1 offen offset:1024 lds\n"
"v_rcp_f32 v208,v16\n"
"v_rcp_f32 v209,v20\n"
"v_rcp_f32 v210,v24\n"
"v_rcp_f32 v211,v28\n"
"v_rcp_f32 v212,v32\n"
MF "a[124:127],v[160:163],v[96:99],a[124:127]\n"
"v_mul_f32_e64 v214,-s13,v208\n"
"v_mul_f32_e64 v215,-s13,v209\n"
"v_mul_f32_e64 v216,-s13,v210\n"
"v_mul_f32_e64 v217,-s13,v211\n"
"v_mul_f32_e64 v218,-s13,v212\n"
AR "v72,a120\n"
AR "v73,a121\n"
AR "v74,a122\n"
MF "a[144:147],v[164:167],v[96:99],a[144:147]\n"
AR "v75,a123\n"
AR "v76,a140\n"
AR "v77,a141\n"
AR "v78,a142\n"
AR "v79,a143\n"
"v_pk_fma_f32 v[72:73],v[72:73],v[208:209],v[214:215] op_sel:[0,0,0] op_sel_hi:[1,0,0]\n"
"v_pk_fma_f32 v[74:75],v[74:75],v[208:209],v[214:215] op_sel:[0,0,0] op_sel_hi:[1,0,0]\n"
"v_pk_fma_f32 v[76:77],v[76:77],v[208:209],v[214:215] op_sel:[0,0,0] op_sel_hi:[1,0,0]\n"
"v_pk_fma_f32 v[78:79],v[78:79],v[208:209],v[214:215] op_sel:[0,0,0] op_sel_hi:[1,0,0]\n"
CV "v112,v72,v73\n"
CV "v113,v74,v75\n"
CV "v114,v76,v77\n"
CV "v115,v78,v79\n"
"s_waitcnt lgkmcnt(0)\n"
MF "a[128:131],v[160:163],v[100:103],a[128:131]\n"
"ds_read_b128 a[140:143],v237 offset:20480\n"
AW "a120,v112\n"
AW "a121,v113\n"
AW "a122,v114\n"
AW "a123,v115\n"
MF "a[44:47],v[144:147],v[96:99],a[44:47]\n"
AR "v80,a124\n"
AR "v81,a125\n"
AR "v82,a126\n"
MF "a[148:151],v[164:167],v[100:103],a[148:151]\n"
AR "v83,a127\n"
AR "v84,a144\n"
AR "v85,a145\n"
"s_nop 0\n"
AR "v86,a146\n"
AR "v87,a147\n"
"v_pk_fma_f32 v[80:81],v[80:81],v[208:209],v[214:215] op_sel:[0,1,1] op_sel_hi:[1,1,1]\n"
"v_pk_fma_f32 v[82:83],v[82:83],v[208:209],v[214:215] op_sel:[0,1,1] op_sel_hi:[1,1,1]\n"
"v_pk_fma_f32 v[84:85],v[84:85],v[208:209],v[214:215] op_sel:[0,1,1] op_sel_hi:[1,1,1]\n"
"v_pk_fma_f32 v[86:87],v[86:87],v[208:209],v[214:215] op_sel:[0,1,1] op_sel_hi:[1,1,1]\n"
CV "v116,v80,v81\n"
CV "v117,v82,v83\n"
CV "v118,v84,v85\n"
CV "v119,v86,v87\n"
MF "a[132:135],v[160:163],v[104:107],a[132:135]\n"
"ds_read_b128 a[144:147],v236\n"
AW "a124,v116\n"
AW "a125,v117\n"
AW "a126,v118\n"
AW "a127,v119\n"
MF "a[64:67],v[148:151],v[96:99],a[64:67]\n"
AR "v72,a128\n"
AR "v73,a129\n"
AR "v74,a130\n"
MF "a[152:155],v[164:167],v[104:107],a[152:155]\n"
AR "v75,a131\n"
AR "v76,a148\n"
AR "v77,a149\n"
AR "v78,a150\n"
AR "v79,a151\n"
"v_pk_fma_f32 v[72:73],v[72:73],v[210:211],v[216:217] op_sel:[0,0,0] op_sel_hi:[1,0,0]\n"
"v_pk_fma_f32 v[74:75],v[74:75],v[210:211],v[216:217] op_sel:[0,0,0] op_sel_hi:[1,0,0]\n"
"v_pk_fma_f32 v[76:77],v[76:77],v[210:211],v[216:217] op_sel:[0,0,0] op_sel_hi:[1,0,0]\n"
"v_pk_fma_f32 v[78:79],v[78:79],v[210:211],v[216:217] op_sel:[0,0,0] op_sel_hi:[1,0,0]\n"
CV "v112,v72,v73\n"
CV "v113,v74,v75\n"
CV "v114,v76,v77\n"
CV "v115,v78,v79\n"
MF "a[136:139],v[160:163],v[108:111],a[136:139]\n"
"ds_read_b128 a[148:151],v236 offset:4096\n"
AW "a128,v112\n"
AW "a129,v113\n"
AW "a130,v114\n"
AW "a131,v115\n"
MF "a[84:87],v[152:155],v[96:99],a[84:87]\n"
AR "v80,a132\n"
AR "v81,a133\n"
AR "v82,a134\n"
MF "a[156:159],v[164:167],v[108:111],a[156:159]\n"
AR "v83,a135\n"
AR "v84,a152\n"
AR "v85,a153\n"
AR "v86,a154\n"
AR "v87,a155\n"
"v_pk_fma_f32 v[80:81],v[80:81],v[210:211],v[216:217] op_sel:[0,1,1] op_sel_hi:[1,1,1]\n"
"v_pk_fma_f32 v[82:83],v[82:83],v[210:211],v[216:217] op_sel:[0,1,1] op_sel_hi:[1,1,1]\n"
"v_pk_fma_f32 v[84:85],v[84:85],v[210:211],v[216:217] op_sel:[0,1,1] op_sel_hi:[1,1,1]\n"
"v_pk_fma_f32 v[86:87],v[86:87],v[210:211],v[216:217] op_sel:[0,1,1] op_sel_hi:[1,1,1]\n"
CV "v116,v80,v81\n"
CV "v117,v82,v83\n"
CV "v118,v84,v85\n"
CV "v119,v86,v87\n"
MF "a[160:163],v[168:171],v[92:95],a[160:163]\n"
"ds_read_b128 a[152:155],v236 offset:8192\n"
AW "a132,v116\n"
AW "a133,v117\n"
AW "a134,v118\n"
AW "a135,v119\n"
MF "a[104:107],v[156:159],v[96:99],a[104:107]\n"
AR "v72,a136\n"
AR "v73,a137\n"
AR "v74,a138\n"
MF "a[180:183],v[172:175],v[92:95],a[180:183]\n"
AR "v75,a139\n"
AR "v76,a156\n"
AR "v77,a157\n"
AR "v78,a158\n"
AR "v79,a159\n"
"v_pk_fma_f32 v[72:73],v[72:73],v[212:213],v[218:219] op_sel:[0,0,0] op_sel_hi:[1,0,0]\n"
"v_pk_fma_f32 v[74:75],v[74:75],v[212:213],v[218:219] op_sel:[0,0,0] op_sel_hi:[1,0,0]\n"
"v_pk_fma_f32 v[76:77],v[76:77],v[212:213],v[218:219] op_sel:[0,0,0] op_sel_hi:[1,0,0]\n"
"v_pk_fma_f32 v[78:79],v[78:79],v[212:213],v[218:219] op_sel:[0,0,0] op_sel_hi:[1,0,0]\n"
CV "v112,v72,v73\n"
CV "v113,v74,v75\n"
CV "v114,v76,v77\n"
CV "v115,v78,v79\n"
MF "a[164:167],v[168:171],v[96:99],a[164:167]\n"
"ds_read_b128 a[156:159],v236 offset:12288\n"
AW "a136,v112\n"
AW "a137,v113\n"
AW "a138,v114\n"
AW "a139,v115\n"
"v_max3_f32 v10,|v72|,|v73|,|v74|\n"
"v_max3_f32 v10,v10,|v75|,|v76|\n"
"v_max3_f32 v10,v10,|v77|,|v78|\n"
"v_max3_f32 v10,v10,|v78|,|v79|\n"
"v_mul_f32 v10,s10,v10\n"
"v_lshrrev_b32 v11,2,v9\n"
"v_readlane_b32 s12,v10,3\n"
"v_readlane_b32 s6,v10,22\n"
"v_readlane_b32 s7,v10,41\n"
"v_readlane_b32 s8,v10,60\n"
"v_add_u32 v11,s14,v11\n"
"s_min_u32 s12,s12,s6\n"
"s_min_u32 s7,s7,s8\n"
"s_min_u32 s12,s12,s7\n"
"s_and_b32 s12,s12,0x7f800000\n"
"s_sub_u32 s12,s12,0x14000000\n"
"s_cmp_lt_u32 s12,0x50000000\n"
"s_cselect_b32 s12,s12,0xe7400000\n"
"s_add_u32 s12,s12,0x18c00000\n"
"v_mov_b32_e64 v10,s12\n"
"v_or_b32 v12,0x80000000,v10\n"
"ds_write2st64_b32 v11,v10,v12 offset1:1\n"
MF "a[8:11],v[136:139],v[100:103],a[8:11]\n"
AR "v80,a160\n"
AR "v81,a161\n"
AR "v82,a162\n"
MF "a[184:187],v[172:175],v[96:99],a[184:187]\n"
AR "v83,a163\n"
AR "v84,a180\n"
AR "v85,a181\n"
AR "v86,a182\n"
AR "v87,a183\n"
"v_pk_fma_f32 v[80:81],v[80:81],v[208:209],v[214:215] op_sel:[0,0,0] op_sel_hi:[1,0,0]\n"
"v_pk_fma_f32 v[82:83],v[82:83],v[208:209],v[214:215] op_sel:[0,0,0] op_sel_hi:[1,0,0]\n"
"v_pk_fma_f32 v[84:85],v[84:85],v[208:209],v[214:215] op_sel:[0,0,0] op_sel_hi:[1,0,0]\n"
"v_pk_fma_f32 v[86:87],v[86:87],v[208:209],v[214:215] op_sel:[0,0,0] op_sel_hi:[1,0,0]\n"
CV "v116,v80,v81\n"
CV "v117,v82,v83\n"
CV "v118,v84,v85\n"
CV "v119,v86,v87\n"
MF "a[168:171],v[168:171],v[100:103],a[168:171]\n"
"ds_read_b128 a[180:183],v236 offset:16384\n"
AW "a160,v116\n"
AW "a161,v117\n"
AW "a162,v118\n"
AW "a163,v119\n"
MF "a[28:31],v[140:143],v[100:103],a[28:31]\n"
AR "v72,a164\n"
AR "v73,a165\n"
AR "v74,a166\n"
MF "a[188:191],v[172:175],v[100:103],a[188:191]\n"
AR "v75,a167\n"
AR "v76,a184\n"
AR "v77,a185\n"
AR "v78,a186\n"
AR "v79,a187\n"
"v_pk_fma_f32 v[72:73],v[72:73],v[208:209],v[214:215] op_sel:[0,1,1] op_sel_hi:[1,1,1]\n"
"v_pk_fma_f32 v[74:75],v[74:75],v[208:209],v[214:215] op_sel:[0,1,1] op_sel_hi:[1,1,1]\n"
"v_pk_fma_f32 v[76:77],v[76:77],v[208:209],v[214:215] op_sel:[0,1,1] op_sel_hi:[1,1,1]\n"
"v_pk_fma_f32 v[78:79],v[78:79],v[208:209],v[214:215] op_sel:[0,1,1] op_sel_hi:[1,1,1]\n"
CV "v112,v72,v73\n"
CV "v113,v74,v75\n"
CV "v114,v76,v77\n"
CV "v115,v78,v79\n"
MF "a[172:175],v[168:171],v[104:107],a[172:175]\n"
"ds_read_b128 a[184:187],v236 offset:20480\n"
AW "a164,v112\n"
AW "a165,v113\n"
AW "a166,v114\n"
AW "a167,v115\n"
MF "a[48:51],v[144:147],v[100:103],a[48:51]\n"
"s_waitcnt lgkmcnt(0)\n"
"s_barrier\n"
"s_add_u32 m0,s5,49152\n"
"v_add_u32_e64 v192,0,v7\n"
LD "v192,s[48:51],s2 offen lds\n"
AR "v80,a168\n"
AR "v81,a169\n"
AR "v82,a170\n"
MF "a[192:195],v[172:175],v[104:107],a[192:195]\n"
AR "v83,a171\n"
AR "v84,a188\n"
AR "v85,a189\n"
AR "v86,a190\n"
AR "v87,a191\n"
"v_pk_fma_f32 v[80:81],v[80:81],v[210:211],v[216:217] op_sel:[0,0,0] op_sel_hi:[1,0,0]\n"
"v_pk_fma_f32 v[82:83],v[82:83],v[210:211],v[216:217] op_sel:[0,0,0] op_sel_hi:[1,0,0]\n"
"v_pk_fma_f32 v[84:85],v[84:85],v[210:211],v[216:217] op_sel:[0,0,0] op_sel_hi:[1,0,0]\n"
"v_pk_fma_f32 v[86:87],v[86:87],v[210:211],v[216:217] op_sel:[0,0,0] op_sel_hi:[1,0,0]\n"
"v_add_u32 v193,9216,v7\n"
LD "v193,s[48:51],s2 offen offset:1024 lds\n"
CV "v116,v80,v81\n"
CV "v117,v82,v83\n"
CV "v118,v84,v85\n"
CV "v119,v86,v87\n"
MF "a[176:179],v[168:171],v[108:111],a[176:179]\n"
"v_accvgpr_mov_b32 a188,a240\n"
"v_accvgpr_mov_b32 a189,a241\n"
"v_accvgpr_mov_b32 a190,a242\n"
"v_accvgpr_mov_b32 a191,a243\n"
AW "a168,v116\n"
AW "a169,v117\n"
AW "a170,v118\n"
AW "a171,v119\n"
MF "a[68:71],v[148:151],v[100:103],a[68:71]\n"
"v_add_u32 v194,18432,v7\n"
LD "v194,s[48:51],s2 offen offset:2048 lds\n"
AR "v72,a172\n"
AR "v73,a173\n"
AR "v74,a174\n"
MF "a[196:199],v[172:175],v[108:111],a[196:199]\n"
AR "v75,a175\n"
AR "v76,a192\n"
AR "v77,a193\n"
AR "v78,a194\n"
AR "v79,a195\n"
"v_pk_fma_f32 v[72:73],v[72:73],v[210:211],v[216:217] op_sel:[0,1,1] op_sel_hi:[1,1,1]\n"
"v_pk_fma_f32 v[74:75],v[74:75],v[210:211],v[216:217] op_sel:[0,1,1] op_sel_hi:[1,1,1]\n"
"v_pk_fma_f32 v[76:77],v[76:77],v[210:211],v[216:217] op_sel:[0,1,1] op_sel_hi:[1,1,1]\n"
"v_pk_fma_f32 v[78:79],v[78:79],v[210:211],v[216:217] op_sel:[0,1,1] op_sel_hi:[1,1,1]\n"
LD "v195,s[48:51],s2 offen offset:3072 lds\n"
CV "v112,v72,v73\n"
CV "v113,v74,v75\n"
CV "v114,v76,v77\n"
CV "v115,v78,v79\n"
MF "a[200:203],v[176:179],v[92:95],a[200:203]\n"
"s_add_u32 m0,s5,53248\n"
"v_accvgpr_mov_b32 a192,a244\n"
"v_accvgpr_mov_b32 a193,a245\n"
"v_accvgpr_mov_b32 a194,a246\n"
"v_accvgpr_mov_b32 a195,a247\n"
AW "a172,v112\n"
AW "a173,v113\n"
AW "a174,v114\n"
AW "a175,v115\n"
MF "a[88:91],v[152:155],v[100:103],a[88:91]\n"
LD "v196,s[48:51],s2 offen lds\n"
AR "v80,a176\n"
AR "v81,a177\n"
AR "v82,a178\n"
MF "a[220:223],v[180:183],v[92:95],a[220:223]\n"
AR "v83,a179\n"
AR "v84,a196\n"
AR "v85,a197\n"
AR "v86,a198\n"
AR "v87,a199\n"
"v_pk_fma_f32 v[80:81],v[80:81],v[212:213],v[218:219] op_sel:[0,0,0] op_sel_hi:[1,0,0]\n"
"v_pk_fma_f32 v[82:83],v[82:83],v[212:213],v[218:219] op_sel:[0,0,0] op_sel_hi:[1,0,0]\n"
"v_pk_fma_f32 v[84:85],v[84:85],v[212:213],v[218:219] op_sel:[0,0,0] op_sel_hi:[1,0,0]\n"
"v_pk_fma_f32 v[86:87],v[86:87],v[212:213],v[218:219] op_sel:[0,0,0] op_sel_hi:[1,0,0]\n"
CV "v116,v80,v81\n"
CV "v117,v82,v83\n"
CV "v118,v84,v85\n"
CV "v119,v86,v87\n"
MF "a[204:207],v[176:179],v[96:99],a[204:207]\n"
"v_accvgpr_mov_b32 a196,a248\n"
"v_accvgpr_mov_b32 a197,a249\n"
"v_accvgpr_mov_b32 a198,a250\n"
"v_accvgpr_mov_b32 a199,a251\n"
AW "a176,v116\n"
AW "a177,v117\n"
AW "a178,v118\n"
AW "a179,v119\n"
MF "a[108:111],v[156:159],v[100:103],a[108:111]\n"
LD "v197,s[48:51],s2 offen offset:1024 lds\n"
AR "v72,a200\n"
AR "v73,a201\n"
AR "v74,a202\n"
MF "a[224:227],v[180:183],v[96:99],a[224:227]\n"
AR "v75,a203\n"
AR "v76,a220\n"
AR "v77,a221\n"
AR "v78,a222\n"
AR "v79,a223\n"
"v_pk_fma_f32 v[72:73],v[72:73],v[208:209],v[214:215] op_sel:[0,0,0] op_sel_hi:[1,0,0]\n"
"v_pk_fma_f32 v[74:75],v[74:75],v[208:209],v[214:215] op_sel:[0,0,0] op_sel_hi:[1,0,0]\n"
"v_pk_fma_f32 v[76:77],v[76:77],v[208:209],v[214:215] op_sel:[0,0,0] op_sel_hi:[1,0,0]\n"
"v_pk_fma_f32 v[78:79],v[78:79],v[208:209],v[214:215] op_sel:[0,0,0] op_sel_hi:[1,0,0]\n"
CV "v112,v72,v73\n"
CV "v113,v74,v75\n"
CV "v114,v76,v77\n"
CV "v115,v78,v79\n"
MF "a[208:211],v[176:179],v[100:103],a[208:211]\n"
"s_add_u32 m0,s5,73728\n"
AW "a220,v62\n"
AW "a221,v63\n"
AW "a222,v64\n"
AW "a223,v65\n"
AW "a200,v112\n"
AW "a201,v113\n"
AW "a202,v114\n"
AW "a203,v115\n"
MF "a[12:15],v[136:139],v[104:107],a[12:15]\n"
LD "v192,s[48:51],s3 offen lds\n"
AR "v80,a204\n"
AR "v81,a205\n"
AR "v82,a206\n"
MF "a[228:231],v[180:183],v[100:103],a[228:231]\n"
AR "v83,a207\n"
AR "v84,a224\n"
AR "v85,a225\n"
AR "v86,a226\n"
AR "v87,a227\n"
"v_pk_fma_f32 v[80:81],v[80:81],v[208:209],v[214:215] op_sel:[0,1,1] op_sel_hi:[1,1,1]\n"
"v_pk_fma_f32 v[82:83],v[82:83],v[208:209],v[214:215] op_sel:[0,1,1] op_sel_hi:[1,1,1]\n"
"v_pk_fma_f32 v[84:85],v[84:85],v[208:209],v[214:215] op_sel:[0,1,1] op_sel_hi:[1,1,1]\n"
"v_pk_fma_f32 v[86:87],v[86:87],v[208:209],v[214:215] op_sel:[0,1,1] op_sel_hi:[1,1,1]\n"
CV "v116,v80,v81\n"
CV "v117,v82,v83\n"
CV "v118,v84,v85\n"
CV "v119,v86,v87\n"
MF "a[212:215],v[176:179],v[104:107],a[212:215]\n"
AW "a224,v66\n"
AW "a225,v67\n"
AW "a226,v68\n"
AW "a227,v69\n"
AW "a204,v116\n"
AW "a205,v117\n"
AW "a206,v118\n"
AW "a207,v119\n"
MF "a[32:35],v[140:143],v[104:107],a[32:35]\n"
LD "v193,s[48:51],s3 offen offset:1024 lds\n"
AR "v72,a208\n"
AR "v73,a209\n"
AR "v74,a210\n"
MF "a[232:235],v[180:183],v[104:107],a[232:235]\n"
AR "v75,a211\n"
AR "v76,a228\n"
AR "v77,a229\n"
AR "v78,a230\n"
AR "v79,a231\n"
"v_pk_fma_f32 v[72:73],v[72:73],v[210:211],v[216:217] op_sel:[0,0,0] op_sel_hi:[1,0,0]\n"
"v_pk_fma_f32 v[74:75],v[74:75],v[210:211],v[216:217] op_sel:[0,0,0] op_sel_hi:[1,0,0]\n"
"v_pk_fma_f32 v[76:77],v[76:77],v[210:211],v[216:217] op_sel:[0,0,0] op_sel_hi:[1,0,0]\n"
"v_pk_fma_f32 v[78:79],v[78:79],v[210:211],v[216:217] op_sel:[0,0,0] op_sel_hi:[1,0,0]\n"
CV "v112,v72,v73\n"
CV "v113,v74,v75\n"
CV "v114,v76,v77\n"
CV "v115,v78,v79\n"
MF "a[216:219],v[176:179],v[108:111],a[216:219]\n"
AW "a228,v70\n"
AW "a229,v71\n"
AW "a230,v132\n"
AW "a231,v133\n"
AW "a208,v112\n"
AW "a209,v113\n"
AW "a210,v114\n"
AW "a211,v115\n"
MF "a[52:55],v[144:147],v[104:107],a[52:55]\n"
LD "v194,s[48:51],s3 offen offset:2048 lds\n"
AR "v80,a212\n"
AR "v81,a213\n"
AR "v82,a214\n"
MF "a[236:239],v[180:183],v[108:111],a[236:239]\n"
AR "v83,a215\n"
AR "v84,a232\n"
AR "v85,a233\n"
AR "v86,a234\n"
AR "v87,a235\n"
"v_pk_fma_f32 v[80:81],v[80:81],v[210:211],v[216:217] op_sel:[0,1,1] op_sel_hi:[1,1,1]\n"
"v_pk_fma_f32 v[82:83],v[82:83],v[210:211],v[216:217] op_sel:[0,1,1] op_sel_hi:[1,1,1]\n"
"v_pk_fma_f32 v[84:85],v[84:85],v[210:211],v[216:217] op_sel:[0,1,1] op_sel_hi:[1,1,1]\n"
"v_pk_fma_f32 v[86:87],v[86:87],v[210:211],v[216:217] op_sel:[0,1,1] op_sel_hi:[1,1,1]\n"
CV "v116,v80,v81\n"
CV "v117,v82,v83\n"
CV "v118,v84,v85\n"
CV "v119,v86,v87\n"
MF "a[0:3],v[136:139],v[92:95],a[0:3]\n"
AW "a232,v134\n"
AW "a233,v135\n"
AW "a234,v6\n"
AW "a235,v13\n"
AW "a212,v116\n"
AW "a213,v117\n"
AW "a214,v118\n"
AW "a215,v119\n"
MF "a[72:75],v[148:151],v[104:107],a[72:75]\n"
LD "v195,s[48:51],s3 offen offset:3072 lds\n"
AR "v72,a216\n"
AR "v73,a217\n"
AR "v74,a218\n"
MF "a[20:23],v[140:143],v[92:95],a[20:23]\n"
AR "v75,a219\n"
AR "v76,a236\n"
AR "v77,a237\n"
AR "v78,a238\n"
AR "v79,a239\n"
"v_pk_fma_f32 v[72:73],v[72:73],v[212:213],v[218:219] op_sel:[0,0,0] op_sel_hi:[1,0,0]\n"
"v_pk_fma_f32 v[74:75],v[74:75],v[212:213],v[218:219] op_sel:[0,0,0] op_sel_hi:[1,0,0]\n"
"v_pk_fma_f32 v[76:77],v[76:77],v[212:213],v[218:219] op_sel:[0,0,0] op_sel_hi:[1,0,0]\n"
"v_pk_fma_f32 v[78:79],v[78:79],v[212:213],v[218:219] op_sel:[0,0,0] op_sel_hi:[1,0,0]\n"
CV "v112,v72,v73\n"
CV "v113,v74,v75\n"
CV "v114,v76,v77\n"
CV "v115,v78,v79\n"
MF "a[40:43],v[144:147],v[92:95],a[40:43]\n"
"s_add_u32 m0,s5,77824\n"
AW "a236,v14\n"
AW "a237,v15\n"
"v_accvgpr_mov_b32 a238,a254\n"
"v_accvgpr_mov_b32 a239,a255\n"
AW "a216,v112\n"
AW "a217,v113\n"
AW "a218,v114\n"
AW "a219,v115\n"
MF "a[92:95],v[152:155],v[104:107],a[92:95]\n"
".Lpg24h:\n"
"s_cbranch_execz .Lpg28h\n"
LD "v196,s[48:51],s3 offen lds\n"
AR "v80,a0\n"
AR "v81,a1\n"
AR "v82,a2\n"
MF "a[60:63],v[148:151],v[92:95],a[60:63]\n"
AR "v83,a3\n"
AR "v84,a20\n"
AR "v85,a21\n"
"s_nop 0\n"
AR "v86,a22\n"
AR "v87,a23\n"
"v_pk_fma_f32 v[80:81],v[80:81],v[208:209],v[214:215] op_sel:[0,0,0] op_sel_hi:[1,0,0]\n"
"v_pk_fma_f32 v[82:83],v[82:83],v[208:209],v[214:215] op_sel:[0,0,0] op_sel_hi:[1,0,0]\n"
"v_pk_fma_f32 v[84:85],v[84:85],v[208:209],v[214:215] op_sel:[0,0,0] op_sel_hi:[1,0,0]\n"
"v_pk_fma_f32 v[86:87],v[86:87],v[208:209],v[214:215] op_sel:[0,0,0] op_sel_hi:[1,0,0]\n"
CV "v116,v80,v81\n"
CV "v117,v82,v83\n"
CV "v118,v84,v85\n"
CV "v119,v86,v87\n"
MF "a[80:83],v[152:155],v[92:95],a[80:83]\n"
"ds_read_b128 a[20:23],v236 offset:24576\n"
AW "a0,v116\n"
AW "a1,v117\n"
AW "a2,v118\n"
AW "a3,v119\n"
MF "a[112:115],v[156:159],v[104:107],a[112:115]\n"
LD "v197,s[48:51],s3 offen offset:1024 lds\n"
AR "v72,a40\n"
AR "v73,a41\n"
AR "v74,a42\n"
MF "a[100:103],v[156:159],v[92:95],a[100:103]\n"
AR "v75,a43\n"
AR "v76,a60\n"
AR "v77,a61\n"
AR "v78,a62\n"
AR "v79,a63\n"
"v_pk_fma_f32 v[72:73],v[72:73],v[208:209],v[214:215] op_sel:[0,0,0] op_sel_hi:[1,0,0]\n"
"v_pk_fma_f32 v[74:75],v[74:75],v[208:209],v[214:215] op_sel:[0,0,0] op_sel_hi:[1,0,0]\n"
"v_pk_fma_f32 v[76:77],v[76:77],v[208:209],v[214:215] op_sel:[0,0,0] op_sel_hi:[1,0,0]\n"
"v_pk_fma_f32 v[78:79],v[78:79],v[208:209],v[214:215] op_sel:[0,0,0] op_sel_hi:[1,0,0]\n"
CV "v112,v72,v73\n"
CV "v113,v74,v75\n"
CV "v114,v76,v77\n"
CV "v115,v78,v79\n"
MF "a[4:7],v[136:139],v[96:99],a[4:7]\n"
"ds_read_b128 a[60:63],v236 offset:45056\n"
AW "a40,v112\n"
AW "a41,v113\n"
AW "a42,v114\n"
AW "a43,v115\n"
MF "a[16:19],v[136:139],v[108:111],a[16:19]\n"
AR "v80,a80\n"
AR "v81,a81\n"
AR "v82,a82\n"
MF "a[24:27],v[140:143],v[96:99],a[24:27]\n"
AR "v83,a83\n"
AR "v84,a100\n"
AR "v85,a101\n"
AR "v86,a102\n"
AR "v87,a103\n"
"v_pk_fma_f32 v[80:81],v[80:81],v[208:209],v[214:215] op_sel:[0,0,0] op_sel_hi:[1,0,0]\n"
"v_pk_fma_f32 v[82:83],v[82:83],v[208:209],v[214:215] op_sel:[0,0,0] op_sel_hi:[1,0,0]\n"
"v_pk_fma_f32 v[84:85],v[84:85],v[208:209],v[214:215] op_sel:[0,0,0] op_sel_hi:[1,0,0]\n"
"v_pk_fma_f32 v[86:87],v[86:87],v[208:209],v[214:215] op_sel:[0,0,0] op_sel_hi:[1,0,0]\n"
CV "v116,v80,v81\n"
CV "v117,v82,v83\n"
CV "v118,v84,v85\n"
CV "v119,v86,v87\n"
MF "a[36:39],v[140:143],v[108:111],a[36:39]\n"
"ds_read_b128 a[100:103],v237\n"
AW "a80,v116\n"
AW "a81,v117\n"
AW "a82,v118\n"
AW "a83,v119\n"
MF "a[56:59],v[144:147],v[108:111],a[56:59]\n"
AR "v72,a4\n"
AR "v73,a5\n"
AR "v74,a6\n"
MF "a[76:79],v[148:151],v[108:111],a[76:79]\n"
AR "v75,a7\n"
AR "v76,a24\n"
AR "v77,a25\n"
AR "v78,a26\n"
AR "v79,a27\n"
"v_pk_fma_f32 v[72:73],v[72:73],v[208:209],v[214:215] op_sel:[0,1,1] op_sel_hi:[1,1,1]\n"
"v_pk_fma_f32 v[74:75],v[74:75],v[208:209],v[214:215] op_sel:[0,1,1] op_sel_hi:[1,1,1]\n"
"v_pk_fma_f32 v[76:77],v[76:77],v[208:209],v[214:215] op_sel:[0,1,1] op_sel_hi:[1,1,1]\n"
"v_pk_fma_f32 v[78:79],v[78:79],v[208:209],v[214:215] op_sel:[0,1,1] op_sel_hi:[1,1,1]\n"
CV "v112,v72,v73\n"
CV "v113,v74,v75\n"
CV "v114,v76,v77\n"
CV "v115,v78,v79\n"
MF "a[96:99],v[152:155],v[108:111],a[96:99]\n"
"ds_read_b128 a[24:27],v236 offset:28672\n"
AW "a4,v112\n"
AW "a5,v113\n"
AW "a6,v114\n"
AW "a7,v115\n"
MF "a[116:119],v[156:159],v[108:111],a[116:119]\n"
"v_and_b32 v0,512,v9\n"
"v_lshlrev_b32 v0,2,v0\n"
"v_sub_u32 v0,v2,v0\n"
"v_xor_b32 v0,s4,v0\n"
"v_mov_b32 v252,s15\n"
"s_waitcnt vmcnt(12) lgkmcnt(0)\n"
"s_barrier\n"
"ds_read_b128 v[252:255],v252\n"
TR "v[176:177],v0 offset:24576\n"
TR "v[178:179],v0 offset:26624\n"
TR "v[180:181],v0 offset:28672\n"
TR "v[182:183],v0 offset:30720\n"
TR "v[184:185],v0 offset:32768\n"
TR "v[186:187],v0 offset:34816\n"
"v_xor_b32 v1,32,v0\n"
"v_xor_b32 v2,64,v0\n"
"v_xor_b32 v3,96,v0\n"
"v_add_u32 v4,73728,v0\n"
"v_add_u32 v5,73728,v1\n"
"v_add_u32 v6,73728,v2\n"
"v_add_u32 v7,73728,v3\n"
"s_cmp_ge_u32 s56,2\n"
"s_cbranch_scc1 .Lrole1\n"
"s_waitcnt lgkmcnt(4)\n"
MF "v[16:19],a[120:123],v[176:179],v[252:255]\n"
"s_mov_b32 s65,128\n"
TR "v[188:189],v1 offset:24576\n"
AR "v220,a44\n"
"s_waitcnt lgkmcnt(3)\n"
MF "v[16:19],a[160:163],v[180:183],v[16:19]\n"
"s_mov_b32 s9,0x3f80\n"
TR "v[190:191],v1 offset:26624\n"
AR "v221,a45\n"
"s_waitcnt lgkmcnt(2)\n"
MF "v[16:19],a[200:203],v[184:187],v[16:19]\n"
"s_add_u32 s89,s89,s65\n"
AR "v222,a46\n"
AR "v223,a47\n"
MF "v[24:27],a[204:207],v[184:187],v[252:255]\n"
"s_bfe_u32 s58,s22,0x30014\n"
TR "a[240:241],v1 offset:28672\n"
AR "v224,a64\n"
MF "v[24:27],a[164:167],v[180:183],v[24:27]\n"
"s_add_u32 s61,s58,1\n"
TR "a[242:243],v1 offset:30720\n"
AR "v225,a65\n"
MF "v[24:27],a[124:127],v[176:179],v[24:27]\n"
"s_and_b32 s61,s61,7\n"
AR "v226,a66\n"
AR "v227,a67\n"
MF "v[32:35],a[128:131],v[176:179],v[252:255]\n"
"s_mov_b32 s64,9\n"
TR "a[244:245],v1 offset:32768\n"
"ds_read_b128 a[64:67],v236 offset:49152\n"
MF "v[32:35],a[168:171],v[180:183],v[32:35]\n"
"s_xor_b32 s67,s78,8\n"
TR "a[246:247],v1 offset:34816\n"
"v_fma_f32 v220,v220,v209,v215\n"
MF "v[32:35],a[208:211],v[184:187],v[32:35]\n"
"s_mov_b32 s71,204800\n"
"v_fma_f32 v221,v221,v209,v215\n"
"v_fma_f32 v222,v222,v209,v215\n"
MF "v[40:43],a[212:215],v[184:187],v[252:255]\n"
"s_mov_b32 s66,204800\n"
"v_fma_f32 v223,v223,v209,v215\n"
"v_fma_f32 v224,v224,v209,v215\n"
MF "v[40:43],a[172:175],v[180:183],v[40:43]\n"
"s_and_b32 s73,s56,1\n"
"v_fma_f32 v225,v225,v209,v215\n"
"v_fma_f32 v226,v226,v209,v215\n"
MF "v[40:43],a[132:135],v[176:179],v[40:43]\n"
"s_mul_i32 s73,s73,5120\n"
"v_fma_f32 v227,v227,v209,v215\n"
CV "v220,v220,v221\n"
MF "v[48:51],a[136:139],v[176:179],v[252:255]\n"
"s_add_u32 s73,s73,s93\n"
CV "v221,v222,v223\n"
CV "v222,v224,v225\n"
MF "v[48:51],a[176:179],v[180:183],v[48:51]\n"
"s_xor_b32 s58,s58,4\n"
CV "v223,v226,v227\n"
AW "a44,v220\n"
MF "v[48:51],a[216:219],v[184:187],v[48:51]\n"
"s_waitcnt lgkmcnt(0)\n"
AW "a45,v221\n"
AW "a46,v222\n"
MF "v[52:55],a[216:219],a[244:247],v[252:255]\n"
"s_mul_i32 s70,s58,204800\n"
TR "v[176:177],v2 offset:24576\n"
AW "a47,v223\n"
MF "v[52:55],a[176:179],a[240:243],v[52:55]\n"
"s_add_u32 s70,s70,s73\n"
TR "v[178:179],v2 offset:26624\n"
AR "v228,a84\n"
MF "v[52:55],a[136:139],v[188:191],v[52:55]\n"
"s_add_u32 s70,s70,0xa000\n"
AR "v229,a85\n"
AR "v230,a86\n"
MF "v[44:47],a[132:135],v[188:191],v[252:255]\n"
"s_mov_b32 s85,1\n"
TR "v[180:181],v2 offset:28672\n"
AR "v231,a87\n"
MF "v[44:47],a[172:175],a[240:243],v[44:47]\n"
"s_mov_b32 s83,0xf0000000\n"
TR "v[182:183],v2 offset:30720\n"
AR "v232,a104\n"
MF "v[44:47],a[212:215],a[244:247],v[44:47]\n"
"s_mov_b32 s82,0xf0000000\n"
AR "v233,a105\n"
AR "v234,a106\n"
MF "v[36:39],a[208:211],a[244:247],v[252:255]\n"
"s_add_u32 s58,s79,73728\n"
TR "v[184:185],v2 offset:32768\n"
AR "v235,a107\n"
MF "v[36:39],a[168:171],a[240:243],v[36:39]\n"
"s_mov_b32 s4,64\n"
TR "v[186:187],v2 offset:34816\n"
"ds_read_b128 a[104:107],v237 offset:4096\n"
MF "v[36:39],a[128:131],v[188:191],v[36:39]\n"
"s_mul_i32 s95,s92,3277\n"
"v_fma_f32 v228,v228,v209,v215\n"
"v_fma_f32 v229,v229,v209,v215\n"
MF "v[28:31],a[124:127],v[188:191],v[252:255]\n"
"s_lshr_b32 s95,s95,16\n"
"v_fma_f32 v230,v230,v209,v215\n"
"v_fma_f32 v231,v231,v209,v215\n"
MF "v[28:31],a[164:167],a[240:243],v[28:31]\n"
"s_mul_i32 s59,s95,20\n"
"v_fma_f32 v232,v232,v209,v215\n"
"v_fma_f32 v233,v233,v209,v215\n"
MF "v[28:31],a[204:207],a[244:247],v[28:31]\n"
"s_sub_u32 s59,s92,s59\n"
"v_fma_f32 v234,v234,v209,v215\n"
"v_fma_f32 v235,v235,v209,v215\n"
MF "v[20:23],a[200:203],a[244:247],v[252:255]\n"
"s_cmp_ge_u32 s59,10\n"
"s_cselect_b32 s96,4294557760,0\n"
CV "v228,v228,v229\n"
MF "v[20:23],a[160:163],a[240:243],v[20:23]\n"
"s_add_u32 s96,s96,1024\n"
CV "v229,v230,v231\n"
CV "v230,v232,v233\n"
MF "v[20:23],a[120:123],v[188:191],v[20:23]\n"
"s_waitcnt lgkmcnt(1)\n"
CV "v231,v234,v235\n"
AW "a84,v228\n"
MF "v[56:59],a[120:123],v[176:179],v[252:255]\n"
"s_mul_i32 s59,s59,40960\n"
TR "v[188:189],v3 offset:24576\n"
AW "a85,v229\n"
MF "v[56:59],a[160:163],v[180:183],v[56:59]\n"
"s_add_u32 s96,s96,s59\n"
TR "v[190:191],v3 offset:26624\n"
AW "a86,v230\n"
MF "v[56:59],a[200:203],v[184:187],v[56:59]\n"
"s_lshl_b32 s59,s95,7\n"
AW "a87,v231\n"
AR "v220,a8\n"
MF "v[64:67],a[204:207],v[184:187],v[252:255]\n"
"s_add_u32 s96,s96,s59\n"
TR "a[240:241],v3 offset:28672\n"
AR "v221,a9\n"
MF "v[64:67],a[164:167],v[180:183],v[64:67]\n"
"s_lshl_b32 s2,s92,10\n"
TR "a[242:243],v3 offset:30720\n"
AR "v222,a10\n"
MF "v[64:67],a[124:127],v[176:179],v[64:67]\n"
"s_add_u32 s2,s2,s93\n"
AR "v223,a11\n"
AR "v224,a28\n"
MF "v[72:75],a[128:131],v[176:179],v[252:255]\n"
"s_mov_b32 s0,s96\n"
TR "a[244:245],v3 offset:32768\n"
AR "v225,a29\n"
MF "v[72:75],a[168:171],v[180:183],v[72:75]\n"
"s_sub_u32 s2,s2,204800\n"
TR "a[246:247],v3 offset:34816\n"
AR "v226,a30\n"
MF "v[72:75],a[208:211],v[184:187],v[72:75]\n"
"s_mov_b32 s92,64\n"
AR "v227,a31\n"
"v_fma_f32 v220,v220,v210,v216\n"
MF "v[80:83],a[212:215],v[184:187],v[252:255]\n"
"ds_read_b128 a[28:31],v236 offset:32768\n"
"v_fma_f32 v221,v221,v210,v216\n"
MF "v[80:83],a[172:175],v[180:183],v[80:83]\n"
"v_fma_f32 v222,v222,v210,v216\n"
"v_fma_f32 v223,v223,v210,v216\n"
MF "v[80:83],a[132:135],v[176:179],v[80:83]\n"
"v_fma_f32 v224,v224,v210,v216\n"
"v_fma_f32 v225,v225,v210,v216\n"
MF "v[88:91],a[136:139],v[176:179],v[252:255]\n"
"v_fma_f32 v226,v226,v210,v216\n"
"v_fma_f32 v227,v227,v210,v216\n"
MF "v[88:91],a[176:179],v[180:183],v[88:91]\n"
"s_waitcnt vmcnt(0) lgkmcnt(0)\n"
CV "v220,v220,v221\n"
CV "v221,v222,v223\n"
MF "v[88:91],a[216:219],v[184:187],v[88:91]\n"
"s_barrier\n"
MF "v[92:95],a[216:219],a[244:247],v[252:255]\n"
TR "v[176:177],v0\n"
CV "v222,v224,v225\n"
MF "v[92:95],a[176:179],a[240:243],v[92:95]\n"
TR "v[178:179],v0 offset:2048\n"
CV "v223,v226,v227\n"
MF "v[92:95],a[136:139],v[188:191],v[92:95]\n"
TR "v[96:97],v0 offset:49152\n"
AW "a8,v220\n"
MF "v[84:87],a[132:135],v[188:191],v[252:255]\n"
TR "v[180:181],v0 offset:4096\n"
AW "a9,v221\n"
MF "v[84:87],a[172:175],a[240:243],v[84:87]\n"
TR "v[182:183],v0 offset:6144\n"
AW "a10,v222\n"
MF "v[84:87],a[212:215],a[244:247],v[84:87]\n"
TR "v[98:99],v0 offset:51200\n"
AW "a11,v223\n"
MF "v[76:79],a[208:211],a[244:247],v[252:255]\n"
TR "v[184:185],v0 offset:8192\n"
AR "v228,a48\n"
MF "v[76:79],a[168:171],a[240:243],v[76:79]\n"
TR "v[186:187],v0 offset:10240\n"
AR "v229,a49\n"
MF "v[76:79],a[128:131],v[188:191],v[76:79]\n"
TR "v[100:101],v0 offset:53248\n"
AR "v230,a50\n"
MF "v[68:71],a[124:127],v[188:191],v[252:255]\n"
TR "v[102:103],v0 offset:55296\n"
AR "v231,a51\n"
MF "v[68:71],a[164:167],a[240:243],v[68:71]\n"
TR "v[104:105],v0 offset:57344\n"
AR "v232,a68\n"
MF "v[68:71],a[204:207],a[244:247],v[68:71]\n"
TR "v[106:107],v0 offset:59392\n"
AR "v233,a69\n"
MF "v[60:63],a[200:203],a[244:247],v[252:255]\n"
TR "v[108:109],v4\n"
AR "v234,a70\n"
MF "v[60:63],a[160:163],a[240:243],v[60:63]\n"
TR "v[110:111],v4 offset:2048\n"
AR "v235,a71\n"
MF "v[60:63],a[120:123],v[188:191],v[60:63]\n"
"s_waitcnt lgkmcnt(6)\n"
TR "v[112:113],v4 offset:4096\n"
"ds_read_b128 a[68:71],v236 offset:53248\n"
MF "v[16:19],a[140:143],v[176:179],v[16:19]\n"
TR "v[188:189],v1\n"
"v_fma_f32 v228,v228,v210,v216\n"
MF "v[16:19],a[180:183],v[180:183],v[16:19]\n"
TR "v[190:191],v1 offset:2048\n"
"v_fma_f32 v229,v229,v210,v216\n"
MF "v[16:19],a[220:223],v[184:187],v[16:19]\n"
TR "v[114:115],v4 offset:6144\n"
"v_fma_f32 v230,v230,v210,v216\n"
MF "v[24:27],a[224:227],v[184:187],v[24:27]\n"
TR "a[240:241],v1 offset:4096\n"
"v_fma_f32 v231,v231,v210,v216\n"
MF "v[24:27],a[184:187],v[180:183],v[24:27]\n"
TR "a[242:243],v1 offset:6144\n"
"v_fma_f32 v232,v232,v210,v216\n"
MF "v[24:27],a[144:147],v[176:179],v[24:27]\n"
TR "v[116:117],v4 offset:8192\n"
"v_fma_f32 v233,v233,v210,v216\n"
MF "v[32:35],a[148:151],v[176:179],v[32:35]\n"
TR "a[244:245],v1 offset:8192\n"
"v_fma_f32 v234,v234,v210,v216\n"
MF "v[32:35],a[188:191],v[180:183],v[32:35]\n"
TR "a[246:247],v1 offset:10240\n"
"v_fma_f32 v235,v235,v210,v216\n"
MF "v[32:35],a[228:231],v[184:187],v[32:35]\n"
TR "v[118:119],v4 offset:10240\n"
CV "v228,v228,v229\n"
MF "v[40:43],a[232:235],v[184:187],v[40:43]\n"
TR "v[120:121],v1 offset:49152\n"
CV "v229,v230,v231\n"
MF "v[40:43],a[192:195],v[180:183],v[40:43]\n"
TR "v[122:123],v1 offset:51200\n"
CV "v230,v232,v233\n"
MF "v[40:43],a[152:155],v[176:179],v[40:43]\n"
TR "v[124:125],v1 offset:53248\n"
CV "v231,v234,v235\n"
MF "v[48:51],a[156:159],v[176:179],v[48:51]\n"
TR "v[126:127],v1 offset:55296\n"
AW "a48,v228\n"
MF "v[48:51],a[196:199],v[180:183],v[48:51]\n"
TR "v[128:129],v1 offset:57344\n"
AW "a49,v229\n"
MF "v[48:51],a[236:239],v[184:187],v[48:51]\n"
"s_waitcnt lgkmcnt(6)\n"
TR "v[130:131],v1 offset:59392\n"
AW "a50,v230\n"
MF "v[52:55],a[236:239],a[244:247],v[52:55]\n"
TR "v[176:177],v2\n"
AW "a51,v231\n"
MF "v[52:55],a[196:199],a[240:243],v[52:55]\n"
TR "v[178:179],v2 offset:2048\n"
AR "v220,a88\n"
MF "v[52:55],a[156:159],v[188:191],v[52:55]\n"
TR "v[132:133],v5\n"
AR "v221,a89\n"
MF "v[44:47],a[152:155],v[188:191],v[44:47]\n"
TR "v[180:181],v2 offset:4096\n"
AR "v222,a90\n"
MF "v[44:47],a[192:195],a[240:243],v[44:47]\n"
TR "v[182:183],v2 offset:6144\n"
AR "v223,a91\n"
MF "v[44:47],a[232:235],a[244:247],v[44:47]\n"
TR "v[134:135],v5 offset:2048\n"
AR "v224,a108\n"
MF "v[36:39],a[228:231],a[244:247],v[36:39]\n"
TR "v[184:185],v2 offset:8192\n"
AR "v225,a109\n"
MF "v[36:39],a[188:191],a[240:243],v[36:39]\n"
TR "v[186:187],v2 offset:10240\n"
AR "v226,a110\n"
MF "v[36:39],a[148:151],v[188:191],v[36:39]\n"
TR "v[136:137],v5 offset:4096\n"
AR "v227,a111\n"
MF "v[28:31],a[144:147],v[188:191],v[28:31]\n"
"s_nop 0\n"
TR "v[138:139],v5 offset:6144\n"
"ds_read_b128 a[108:111],v237 offset:8192\n"
MF "v[28:31],a[184:187],a[240:243],v[28:31]\n"
TR "v[140:141],v5 offset:8192\n"
"v_fma_f32 v220,v220,v210,v216\n"
MF "v[28:31],a[224:227],a[244:247],v[28:31]\n"
TR "v[142:143],v5 offset:10240\n"
"v_fma_f32 v221,v221,v210,v216\n"
MF "v[20:23],a[220:223],a[244:247],v[20:23]\n"
TR "v[144:145],v2 offset:49152\n"
"v_fma_f32 v222,v222,v210,v216\n"
MF "v[20:23],a[180:183],a[240:243],v[20:23]\n"
TR "v[146:147],v2 offset:51200\n"
"v_fma_f32 v223,v223,v210,v216\n"
MF "v[20:23],a[140:143],v[188:191],v[20:23]\n"
"s_waitcnt lgkmcnt(7)\n"
TR "v[148:149],v2 offset:53248\n"
"v_fma_f32 v224,v224,v210,v216\n"
MF "v[56:59],a[140:143],v[176:179],v[56:59]\n"
TR "v[188:189],v3\n"
"v_add_u32 v198,59392,v192\n"
MF "v[56:59],a[180:183],v[180:183],v[56:59]\n"
TR "v[190:191],v3 offset:2048\n"
"v_add_u32 v199,68608,v192\n"
MF "v[56:59],a[220:223],v[184:187],v[56:59]\n"
TR "v[150:151],v2 offset:55296\n"
"v_add_u32 v200,81920,v192\n"
"v_add_u32 v201,91136,v192\n"
MF "v[64:67],a[224:227],v[184:187],v[64:67]\n"
TR "a[240:241],v3 offset:4096\n"
"v_add_u32 v202,100352,v192\n"
MF "v[64:67],a[184:187],v[180:183],v[64:67]\n"
TR "a[242:243],v3 offset:6144\n"
"v_add_u32 v203,109568,v192\n"
MF "v[64:67],a[144:147],v[176:179],v[64:67]\n"
TR "v[152:153],v2 offset:57344\n"
"v_fma_f32 v225,v225,v210,v216\n"
MF "v[72:75],a[148:151],v[176:179],v[72:75]\n"
TR "a[244:245],v3 offset:8192\n"
"v_fma_f32 v226,v226,v210,v216\n"
MF "v[72:75],a[188:191],v[180:183],v[72:75]\n"
TR "a[246:247],v3 offset:10240\n"
"v_fma_f32 v227,v227,v210,v216\n"
MF "v[72:75],a[228:231],v[184:187],v[72:75]\n"
TR "v[154:155],v2 offset:59392\n"
CV "v220,v220,v221\n"
MF "v[80:83],a[232:235],v[184:187],v[80:83]\n"
TR "v[156:157],v6\n"
CV "v221,v222,v223\n"
MF "v[80:83],a[192:195],v[180:183],v[80:83]\n"
TR "v[158:159],v6 offset:2048\n"
CV "v222,v224,v225\n"
MF "v[80:83],a[152:155],v[176:179],v[80:83]\n"
TR "v[160:161],v6 offset:4096\n"
CV "v223,v226,v227\n"
MF "v[88:91],a[156:159],v[176:179],v[88:91]\n"
TR "v[162:163],v6 offset:6144\n"
AW "a88,v220\n"
MF "v[88:91],a[196:199],v[180:183],v[88:91]\n"
TR "v[164:165],v6 offset:8192\n"
"s_waitcnt lgkmcnt(0)\n"
MF "v[88:91],a[236:239],v[184:187],v[88:91]\n"
"s_barrier\n"
MF "v[92:95],a[236:239],a[244:247],v[92:95]\n"
"s_add_u32 s80,s89,122880\n"
"s_add_u32 m0,s79,0\n"
TR "v[166:167],v6 offset:10240\n"
MF "v[92:95],a[196:199],a[240:243],v[92:95]\n"
LD "v192,s[48:51],s80 offen lds\n"
MF "v[92:95],a[156:159],v[188:191],v[92:95]\n"
TR "v[168:169],v3 offset:49152\n"
AW "a89,v221\n"
MF "v[84:87],a[152:155],v[188:191],v[84:87]\n"
TR "v[170:171],v3 offset:51200\n"
AW "a90,v222\n"
MF "v[84:87],a[192:195],a[240:243],v[84:87]\n"
TR "v[172:173],v3 offset:53248\n"
AW "a91,v223\n"
MF "v[84:87],a[232:235],a[244:247],v[84:87]\n"
LD "v193,s[48:51],s80 offen offset:1024 lds\n"
MF "v[76:79],a[228:231],a[244:247],v[76:79]\n"
TR "v[174:175],v3 offset:55296\n"
AR "v228,a12\n"
MF "v[76:79],a[188:191],a[240:243],v[76:79]\n"
TR "v[204:205],v3 offset:57344\n"
AR "v229,a13\n"
MF "v[76:79],a[148:151],v[188:191],v[76:79]\n"
TR "v[206:207],v3 offset:59392\n"
AR "v230,a14\n"
MF "v[68:71],a[144:147],v[188:191],v[68:71]\n"
TR "v[244:245],v7\n"
AR "v231,a15\n"
MF "v[68:71],a[184:187],a[240:243],v[68:71]\n"
LD "v194,s[48:51],s80 offen offset:2048 lds\n"
MF "v[68:71],a[224:227],a[244:247],v[68:71]\n"
TR "v[246:247],v7 offset:2048\n"
AR "v232,a32\n"
MF "v[60:63],a[220:223],a[244:247],v[60:63]\n"
TR "v[248:249],v7 offset:4096\n"
AR "v233,a33\n"
MF "v[60:63],a[180:183],a[240:243],v[60:63]\n"
TR "v[250:251],v7 offset:6144\n"
AR "v234,a34\n"
MF "v[60:63],a[140:143],v[188:191],v[60:63]\n"
LD "v195,s[48:51],s80 offen offset:3072 lds\n"
MF "v[16:19],a[0:3],v[96:99],v[16:19]\n"
TR "v[238:239],v7 offset:8192\n"
AR "v235,a35\n"
MF "v[16:19],a[40:43],v[100:103],v[16:19]\n"
"s_nop 0\n"
TR "v[240:241],v7 offset:10240\n"
"ds_read_b128 a[32:35],v236 offset:36864\n"
MF "v[16:19],a[80:83],v[104:107],v[16:19]\n"
"v_fma_f32 v228,v228,v211,v217\n"
"v_fma_f32 v229,v229,v211,v217\n"
MF "v[16:19],a[20:23],v[108:111],v[16:19]\n"
".Lpg25h:\n"
"s_cbranch_execz .Lpg29h\n"
"s_add_u32 m0,s79,4096\n"
"v_fma_f32 v230,v230,v211,v217\n"
"v_fma_f32 v231,v231,v211,v217\n"
MF "v[16:19],a[60:63],v[112:115],v[16:19]\n"
LD "v196,s[48:51],s80 offen lds\n"
MF "v[16:19],a[100:103],v[116:119],v[16:19]\n"
"v_fma_f32 v232,v232,v211,v217\n"
"v_fma_f32 v233,v233,v211,v217\n"
MF "v[20:23],a[100:103],v[140:143],v[20:23]\n"
"v_fma_f32 v234,v234,v211,v217\n"
"v_fma_f32 v235,v235,v211,v217\n"
MF "v[20:23],a[60:63],v[136:139],v[20:23]\n"
CV "v228,v228,v229\n"
CV "v229,v230,v231\n"
MF "v[20:23],a[20:23],v[132:135],v[20:23]\n"
LD "v197,s[48:51],s80 offen offset:1024 lds\n"
MF "v[20:23],a[80:83],v[128:131],v[20:23]\n"
CV "v230,v232,v233\n"
CV "v231,v234,v235\n"
MF "v[20:23],a[40:43],v[124:127],v[20:23]\n"
AW "a12,v228\n"
AW "a13,v229\n"
MF "v[20:23],a[0:3],v[120:123],v[20:23]\n"
"s_waitcnt lgkmcnt(13)\n"
AW "a14,v230\n"
AW "a15,v231\n"
MF "v[56:59],a[0:3],v[144:147],v[56:59]\n"
AR "v220,a52\n"
AR "v221,a53\n"
MF "v[56:59],a[40:43],v[148:151],v[56:59]\n"
LD "v198,s[48:51],s80 offen offset:2048 lds\n"
MF "v[56:59],a[80:83],v[152:155],v[56:59]\n"
AR "v222,a54\n"
AR "v223,a55\n"
MF "v[56:59],a[20:23],v[156:159],v[56:59]\n"
AR "v224,a72\n"
AR "v225,a73\n"
MF "v[56:59],a[60:63],v[160:163],v[56:59]\n"
AR "v226,a74\n"
AR "v227,a75\n"
MF "v[56:59],a[100:103],v[164:167],v[56:59]\n"
"s_waitcnt lgkmcnt(1)\n"
"ds_read_b128 a[72:75],v236 offset:57344\n"
"v_fma_f32 v220,v220,v211,v217\n"
MF "v[60:63],a[100:103],v[238:241],v[60:63]\n"
LD "v199,s[48:51],s80 offen offset:3072 lds\n"
MF "v[60:63],a[60:63],v[248:251],v[60:63]\n"
"v_fma_f32 v221,v221,v211,v217\n"
"v_fma_f32 v222,v222,v211,v217\n"
MF "v[60:63],a[20:23],v[244:247],v[60:63]\n"
"v_fma_f32 v223,v223,v211,v217\n"
"v_fma_f32 v224,v224,v211,v217\n"
MF "v[60:63],a[80:83],v[204:207],v[60:63]\n"
"s_add_u32 m0,s79,8192\n"
"v_fma_f32 v225,v225,v211,v217\n"
"v_fma_f32 v226,v226,v211,v217\n"
MF "v[60:63],a[40:43],v[172:175],v[60:63]\n"
LD "v200,s[48:51],s80 offen lds\n"
MF "v[60:63],a[0:3],v[168:171],v[60:63]\n"
"v_fma_f32 v227,v227,v211,v217\n"
CV "v220,v220,v221\n"
MF "v[68:71],a[4:7],v[168:171],v[68:71]\n"
CV "v221,v222,v223\n"
CV "v222,v224,v225\n"
MF "v[68:71],a[44:47],v[172:175],v[68:71]\n"
CV "v223,v226,v227\n"
AW "a52,v220\n"
MF "v[68:71],a[84:87],v[204:207],v[68:71]\n"
AW "a53,v221\n"
AW "a54,v222\n"
MF "v[68:71],a[24:27],v[244:247],v[68:71]\n"
LD "v201,s[48:51],s80 offen offset:1024 lds\n"
MF "v[68:71],a[64:67],v[248:251],v[68:71]\n"
AW "a55,v223\n"
AR "v228,a92\n"
MF "v[68:71],a[104:107],v[238:241],v[68:71]\n"
"s_waitcnt lgkmcnt(0)\n"
AR "v229,a93\n"
AR "v230,a94\n"
MF "v[64:67],a[104:107],v[164:167],v[64:67]\n"
"s_barrier\n"
MF "v[64:67],a[64:67],v[160:163],v[64:67]\n"
LD "v202,s[48:51],s80 offen offset:2048 lds\n"
MF "v[64:67],a[24:27],v[156:159],v[64:67]\n"
AR "v231,a95\n"
AR "v232,a112\n"
MF "v[64:67],a[84:87],v[152:155],v[64:67]\n"
AR "v233,a113\n"
AR "v234,a114\n"
MF "v[64:67],a[44:47],v[148:151],v[64:67]\n"
AR "v235,a115\n"
"v_fma_f32 v228,v228,v211,v217\n"
MF "v[64:67],a[4:7],v[144:147],v[64:67]\n"
"ds_read_b128 a[112:115],v237 offset:12288\n"
"v_fma_f32 v229,v229,v211,v217\n"
MF "v[28:31],a[4:7],v[120:123],v[28:31]\n"
LD "v203,s[48:51],s80 offen offset:3072 lds\n"
MF "v[28:31],a[44:47],v[124:127],v[28:31]\n"
"v_fma_f32 v230,v230,v211,v217\n"
"v_fma_f32 v231,v231,v211,v217\n"
MF "v[28:31],a[84:87],v[128:131],v[28:31]\n"
"v_fma_f32 v232,v232,v211,v217\n"
"v_fma_f32 v233,v233,v211,v217\n"
MF "v[28:31],a[24:27],v[132:135],v[28:31]\n"
"v_fma_f32 v234,v234,v211,v217\n"
"v_fma_f32 v235,v235,v211,v217\n"
MF "v[28:31],a[64:67],v[136:139],v[28:31]\n"
CV "v228,v228,v229\n"
CV "v229,v230,v231\n"
MF "v[28:31],a[104:107],v[140:143],v[28:31]\n"
"s_add_u32 s88,s88,s65\n"
"s_add_u32 m0,s79,49152\n"
CV "v230,v232,v233\n"
MF "v[24:27],a[104:107],v[116:119],v[24:27]\n"
LD "v192,s[48:51],s88 offen lds\n"
MF "v[24:27],a[64:67],v[112:115],v[24:27]\n"
CV "v231,v234,v235\n"
AW "a92,v228\n"
MF "v[24:27],a[24:27],v[108:111],v[24:27]\n"
AW "a93,v229\n"
AW "a94,v230\n"
MF "v[24:27],a[84:87],v[104:107],v[24:27]\n"
AW "a95,v231\n"
AR "v220,a16\n"
MF "v[24:27],a[44:47],v[100:103],v[24:27]\n"
LD "v193,s[48:51],s88 offen offset:1024 lds\n"
MF "v[24:27],a[4:7],v[96:99],v[24:27]\n"
AR "v221,a17\n"
AR "v222,a18\n"
MF "v[32:35],a[8:11],v[96:99],v[32:35]\n"
AR "v223,a19\n"
AR "v224,a36\n"
MF "v[32:35],a[48:51],v[100:103],v[32:35]\n"
AR "v225,a37\n"
AR "v226,a38\n"
MF "v[32:35],a[88:91],v[104:107],v[32:35]\n"
AR "v227,a39\n"
"v_fma_f32 v220,v220,v212,v218\n"
MF "v[32:35],a[28:31],v[108:111],v[32:35]\n"
LD "v194,s[48:51],s88 offen offset:2048 lds\n"
MF "v[32:35],a[68:71],v[112:115],v[32:35]\n"
"ds_read_b128 a[36:39],v236 offset:40960\n"
"v_fma_f32 v221,v221,v212,v218\n"
MF "v[32:35],a[108:111],v[116:119],v[32:35]\n"
"v_fma_f32 v222,v222,v212,v218\n"
"v_fma_f32 v223,v223,v212,v218\n"
MF "v[36:39],a[108:111],v[140:143],v[36:39]\n"
"v_fma_f32 v224,v224,v212,v218\n"
"v_fma_f32 v225,v225,v212,v218\n"
MF "v[36:39],a[68:71],v[136:139],v[36:39]\n"
"v_fma_f32 v226,v226,v212,v218\n"
"v_fma_f32 v227,v227,v212,v218\n"
MF "v[36:39],a[28:31],v[132:135],v[36:39]\n"
LD "v195,s[48:51],s88 offen offset:3072 lds\n"
MF "v[36:39],a[88:91],v[128:131],v[36:39]\n"
CV "v220,v220,v221\n"
CV "v221,v222,v223\n"
MF "v[36:39],a[48:51],v[124:127],v[36:39]\n"
CV "v222,v224,v225\n"
CV "v223,v226,v227\n"
MF "v[36:39],a[8:11],v[120:123],v[36:39]\n"
AW "a16,v220\n"
AW "a17,v221\n"
MF "v[72:75],a[8:11],v[144:147],v[72:75]\n"
"s_add_u32 m0,s79,53248\n"
AW "a18,v222\n"
AW "a19,v223\n"
MF "v[72:75],a[48:51],v[148:151],v[72:75]\n"
LD "v196,s[48:51],s88 offen lds\n"
MF "v[72:75],a[88:91],v[152:155],v[72:75]\n"
AR "v228,a56\n"
AR "v229,a57\n"
MF "v[72:75],a[28:31],v[156:159],v[72:75]\n"
AR "v230,a58\n"
AR "v231,a59\n"
MF "v[72:75],a[68:71],v[160:163],v[72:75]\n"
AR "v232,a76\n"
AR "v233,a77\n"
MF "v[72:75],a[108:111],v[164:167],v[72:75]\n"
AR "v234,a78\n"
AR "v235,a79\n"
MF "v[76:79],a[108:111],v[238:241],v[76:79]\n"
LD "v197,s[48:51],s88 offen offset:1024 lds\n"
MF "v[76:79],a[68:71],v[248:251],v[76:79]\n"
"ds_read_b128 a[76:79],v236 offset:61440\n"
"v_fma_f32 v228,v228,v212,v218\n"
MF "v[76:79],a[28:31],v[244:247],v[76:79]\n"
"v_fma_f32 v229,v229,v212,v218\n"
"v_fma_f32 v230,v230,v212,v218\n"
MF "v[76:79],a[88:91],v[204:207],v[76:79]\n"
"v_fma_f32 v231,v231,v212,v218\n"
"v_fma_f32 v232,v232,v212,v218\n"
MF "v[76:79],a[48:51],v[172:175],v[76:79]\n"
"v_fma_f32 v233,v233,v212,v218\n"
"v_fma_f32 v234,v234,v212,v218\n"
MF "v[76:79],a[8:11],v[168:171],v[76:79]\n"
"s_waitcnt lgkmcnt(2)\n"
LD "v198,s[48:51],s88 offen offset:2048 lds\n"
MF "v[84:87],a[12:15],v[168:171],v[84:87]\n"
"v_fma_f32 v235,v235,v212,v218\n"
CV "v228,v228,v229\n"
MF "v[84:87],a[52:55],v[172:175],v[84:87]\n"
CV "v229,v230,v231\n"
CV "v230,v232,v233\n"
MF "v[84:87],a[92:95],v[204:207],v[84:87]\n"
CV "v231,v234,v235\n"
AW "a56,v228\n"
MF "v[84:87],a[32:35],v[244:247],v[84:87]\n"
AW "a57,v229\n"
AW "a58,v230\n"
MF "v[84:87],a[72:75],v[248:251],v[84:87]\n"
LD "v199,s[48:51],s88 offen offset:3072 lds\n"
MF "v[84:87],a[112:115],v[238:241],v[84:87]\n"
AW "a59,v231\n"
AR "v220,a96\n"
MF "v[80:83],a[112:115],v[164:167],v[80:83]\n"
AR "v221,a97\n"
AR "v222,a98\n"
MF "v[80:83],a[72:75],v[160:163],v[80:83]\n"
AR "v223,a99\n"
AR "v224,a116\n"
MF "v[80:83],a[32:35],v[156:159],v[80:83]\n"
"s_add_u32 m0,s79,57344\n"
AR "v225,a117\n"
AR "v226,a118\n"
MF "v[80:83],a[92:95],v[152:155],v[80:83]\n"
LD "v200,s[48:51],s88 offen lds\n"
MF "v[80:83],a[52:55],v[148:151],v[80:83]\n"
AR "v227,a119\n"
"v_fma_f32 v220,v220,v212,v218\n"
MF "v[80:83],a[12:15],v[144:147],v[80:83]\n"
"ds_read_b128 a[116:119],v237 offset:16384\n"
"v_fma_f32 v221,v221,v212,v218\n"
MF "v[44:47],a[12:15],v[120:123],v[44:47]\n"
"v_fma_f32 v222,v222,v212,v218\n"
"v_fma_f32 v223,v223,v212,v218\n"
MF "v[44:47],a[52:55],v[124:127],v[44:47]\n"
"v_fma_f32 v224,v224,v212,v218\n"
"v_fma_f32 v225,v225,v212,v218\n"
MF "v[44:47],a[92:95],v[128:131],v[44:47]\n"
LD "v201,s[48:51],s88 offen offset:1024 lds\n"
MF "v[44:47],a[32:35],v[132:135],v[44:47]\n"
"v_fma_f32 v226,v226,v212,v218\n"
"v_fma_f32 v227,v227,v212,v218\n"
MF "v[44:47],a[72:75],v[136:139],v[44:47]\n"
CV "v220,v220,v221\n"
CV "v221,v222,v223\n"
MF "v[44:47],a[112:115],v[140:143],v[44:47]\n"
CV "v222,v224,v225\n"
CV "v223,v226,v227\n"
MF "v[40:43],a[112:115],v[116:119],v[40:43]\n"
AW "a96,v220\n"
AW "a97,v221\n"
MF "v[40:43],a[72:75],v[112:115],v[40:43]\n"
LD "v202,s[48:51],s88 offen offset:2048 lds\n"
MF "v[40:43],a[32:35],v[108:111],v[40:43]\n"
AW "a98,v222\n"
AW "a99,v223\n"
MF "v[40:43],a[92:95],v[104:107],v[40:43]\n"
AR "v8,a252\n"
AR "v11,a253\n"
MF "v[40:43],a[52:55],v[100:103],v[40:43]\n"
"v_bfe_u32 v224,v9,5,3\n"
"v_bfe_u32 v225,v9,8,2\n"
MF "v[40:43],a[12:15],v[96:99],v[40:43]\n"
"s_waitcnt lgkmcnt(0)\n"
"v_lshlrev_b32 v225,1,v225\n"
"v_and_b32 v226,16,v9\n"
MF "v[48:51],a[16:19],v[96:99],v[48:51]\n"
LD "v203,s[48:51],s88 offen offset:3072 lds\n"
MF "v[48:51],a[56:59],v[100:103],v[48:51]\n"
"v_lshlrev_b32_e64 v226,v226,s9\n"
"v_sub_u32 v224,v224,v225\n"
MF "v[48:51],a[96:99],v[104:107],v[48:51]\n"
"v_cmp_eq_u32 vcc,0,v224\n"
MF "v[48:51],a[36:39],v[108:111],v[48:51]\n"
"s_nop 0\n"
"v_cndmask_b32 v220,0,v226,vcc\n"
"v_cmp_eq_u32 vcc,1,v224\n"
MF "v[48:51],a[76:79],v[112:115],v[48:51]\n"
"s_nop 0\n"
"v_cndmask_b32 v221,0,v226,vcc\n"
AW "a248,0\n"
MF "v[48:51],a[116:119],v[116:119],v[48:51]\n"
AW "a249,0\n"
AW "a250,v220\n"
MF "v[52:55],a[116:119],v[140:143],v[52:55]\n"
AW "a251,v221\n"
AW "a252,v220\n"
MF "v[52:55],a[76:79],v[136:139],v[52:55]\n"
"s_waitcnt lgkmcnt(0)\n"
AW "a253,v221\n"
AW "a254,0\n"
MF "v[52:55],a[36:39],v[132:135],v[52:55]\n"
"s_barrier\n"
MF "v[52:55],a[96:99],v[128:131],v[52:55]\n"
"s_add_u32 s80,s88,122880\n"
TR "v[176:177],v0 offset:24576\n"
AW "a255,0\n"
MF "v[52:55],a[56:59],v[124:127],v[52:55]\n"
"s_add_u32 m0,s79,98304\n"
TR "v[178:179],v0 offset:26624\n"
"v_bfe_u32 v12,v9,4,4\n"
MF "v[52:55],a[16:19],v[120:123],v[52:55]\n"
LD "v192,s[48:51],s80 offen lds\n"
MF "v[88:91],a[16:19],v[144:147],v[88:91]\n"
"s_nop 0\n"
TR "v[180:181],v0 offset:28672\n"
"ds_write_addtid_b32 v16 offset:24576\n"
MF "v[88:91],a[56:59],v[148:151],v[88:91]\n"
"s_nop 0\n"
TR "v[182:183],v0 offset:30720\n"
"ds_write_addtid_b32 v17 offset:24832\n"
MF "v[88:91],a[96:99],v[152:155],v[88:91]\n"
LD "v193,s[48:51],s80 offen offset:1024 lds\n"
MF "v[88:91],a[36:39],v[156:159],v[88:91]\n"
"s_nop 0\n"
TR "v[184:185],v0 offset:32768\n"
"ds_write_addtid_b32 v18 offset:25088\n"
MF "v[88:91],a[76:79],v[160:163],v[88:91]\n"
"s_nop 0\n"
TR "v[186:187],v0 offset:34816\n"
"ds_write_addtid_b32 v19 offset:25344\n"
MF "v[88:91],a[116:119],v[164:167],v[88:91]\n"
"s_nop 0\n"
"ds_write_addtid_b32 v20 offset:25600\n"
"ds_write_addtid_b32 v21 offset:25856\n"
MF "v[92:95],a[116:119],v[238:241],v[92:95]\n"
LD "v194,s[48:51],s80 offen offset:2048 lds\n"
MF "v[92:95],a[76:79],v[248:251],v[92:95]\n"
"s_nop 0\n"
"ds_write_addtid_b32 v22 offset:26112\n"
"ds_write_addtid_b32 v23 offset:26368\n"
MF "v[92:95],a[36:39],v[244:247],v[92:95]\n"
"s_nop 0\n"
"ds_write_addtid_b32 v24 offset:26624\n"
"ds_write_addtid_b32 v25 offset:26880\n"
MF "v[92:95],a[96:99],v[204:207],v[92:95]\n"
"s_nop 0\n"
"ds_write_addtid_b32 v26 offset:27136\n"
"ds_write_addtid_b32 v27 offset:27392\n"
MF "v[92:95],a[56:59],v[172:175],v[92:95]\n"
"v_lshlrev_b32 v12,2,v12\n"
"v_lshrrev_b32 v13,2,v9\n"
MF "v[92:95],a[16:19],v[168:171],v[92:95]\n"
"s_waitcnt lgkmcnt(9)\n"
"v_add_u32 v13,s58,v13\n"
"v_add_u32 v4,24576,v4\n"
"v_add_u32 v5,24576,v5\n"
"v_add_u32 v6,24576,v6\n"
"v_add_u32 v7,24576,v7\n"
".Lpl05:\n"
MF "v[96:99],a[0:3],v[176:179],v[252:255]\n"
"s_nop 0\n"
TR "v[188:189],v1 offset:24576\n"
"ds_write_addtid_b32 v28 offset:27648\n"
MF "v[96:99],a[40:43],v[180:183],v[96:99]\n"
"s_nop 0\n"
TR "v[190:191],v1 offset:26624\n"
"ds_write_addtid_b32 v29 offset:27904\n"
MF "v[96:99],a[80:83],v[184:187],v[96:99]\n"
LD "v195,s[48:51],s80 offen offset:3072 lds\n"
MF "v[104:107],a[84:87],v[184:187],v[252:255]\n"
"s_add_u32 s60,s64,1\n"
TR "a[240:241],v1 offset:28672\n"
"ds_write_addtid_b32 v30 offset:28160\n"
MF "v[104:107],a[44:47],v[180:183],v[104:107]\n"
"s_add_u32 m0,s79,102400\n"
TR "a[242:243],v1 offset:30720\n"
"ds_write_addtid_b32 v31 offset:24320\n"
MF "v[104:107],a[4:7],v[176:179],v[104:107]\n"
LD "v196,s[48:51],s80 offen lds\n"
MF "v[112:115],a[8:11],v[176:179],v[252:255]\n"
"s_cmp_eq_u32 s60,8\n"
"s_cselect_b32 s60,0,s60\n"
TR "a[244:245],v1 offset:32768\n"
MF "v[112:115],a[48:51],v[180:183],v[112:115]\n"
"s_cmp_eq_u32 s60,10\n"
"s_cselect_b32 s60,s61,s60\n"
TR "a[246:247],v1 offset:34816\n"
MF "v[112:115],a[88:91],v[184:187],v[112:115]\n"
"s_sub_u32 s65,s60,s64\n"
"ds_write_addtid_b32 v32 offset:24576\n"
"ds_write_addtid_b32 v33 offset:24832\n"
MF "v[120:123],a[92:95],v[184:187],v[252:255]\n"
"s_lshl_b32 s65,s65,7\n"
"ds_write_addtid_b32 v34 offset:25088\n"
"ds_write_addtid_b32 v35 offset:25344\n"
MF "v[120:123],a[52:55],v[180:183],v[120:123]\n"
LD "v197,s[48:51],s80 offen offset:1024 lds\n"
MF "v[120:123],a[12:15],v[176:179],v[120:123]\n"
"s_mov_b32 s64,s60\n"
"ds_write_addtid_b32 v36 offset:25600\n"
"ds_write_addtid_b32 v37 offset:25856\n"
MF "v[128:131],a[16:19],v[176:179],v[252:255]\n"
"s_nop 0\n"
"ds_write_addtid_b32 v38 offset:26112\n"
"ds_write_addtid_b32 v39 offset:26368\n"
MF "v[128:131],a[56:59],v[180:183],v[128:131]\n"
"s_nop 0\n"
"ds_write_addtid_b32 v40 offset:26624\n"
"ds_write_addtid_b32 v41 offset:26880\n"
MF "v[128:131],a[96:99],v[184:187],v[128:131]\n"
"s_waitcnt lgkmcnt(10)\n"
LD "v198,s[48:51],s80 offen offset:2048 lds\n"
MF "v[132:135],a[96:99],a[244:247],v[252:255]\n"
"s_nop 0\n"
TR "v[176:177],v2 offset:24576\n"
"ds_write_addtid_b32 v42 offset:27136\n"
MF "v[132:135],a[56:59],a[240:243],v[132:135]\n"
"s_nop 0\n"
TR "v[178:179],v2 offset:26624\n"
"ds_write_addtid_b32 v43 offset:27392\n"
MF "v[132:135],a[16:19],v[188:191],v[132:135]\n"
"s_nop 0\n"
"ds_write_addtid_b32 v44 offset:27648\n"
"ds_write_addtid_b32 v45 offset:27904\n"
MF "v[124:127],a[12:15],v[188:191],v[252:255]\n"
"s_nop 0\n"
TR "v[180:181],v2 offset:28672\n"
"ds_write_addtid_b32 v46 offset:28160\n"
MF "v[124:127],a[52:55],a[240:243],v[124:127]\n"
"s_nop 0\n"
TR "v[182:183],v2 offset:30720\n"
"ds_write_addtid_b32 v47 offset:28416\n"
MF "v[124:127],a[92:95],a[244:247],v[124:127]\n"
LD "v199,s[48:51],s80 offen offset:3072 lds\n"
MF "v[116:119],a[88:91],a[244:247],v[252:255]\n"
"s_nop 0\n"
TR "v[184:185],v2 offset:32768\n"
"ds_write_addtid_b32 v48 offset:28672\n"
MF "v[116:119],a[48:51],a[240:243],v[116:119]\n"
"s_add_u32 m0,s79,106496\n"
TR "v[186:187],v2 offset:34816\n"
"ds_write_addtid_b32 v49 offset:24832\n"
MF "v[116:119],a[8:11],v[188:191],v[116:119]\n"
LD "v200,s[48:51],s80 offen lds\n"
MF "v[108:111],a[4:7],v[188:191],v[252:255]\n"
"s_nop 0\n"
"ds_write_addtid_b32 v50 offset:25088\n"
"ds_write_addtid_b32 v51 offset:25344\n"
MF "v[108:111],a[44:47],a[240:243],v[108:111]\n"
"s_nop 0\n"
"ds_write_addtid_b32 v52 offset:25600\n"
"ds_write_addtid_b32 v53 offset:25856\n"
MF "v[108:111],a[84:87],a[244:247],v[108:111]\n"
"s_nop 0\n"
"ds_write_addtid_b32 v54 offset:26112\n"
"ds_write_addtid_b32 v55 offset:26368\n"
MF "v[100:103],a[80:83],a[244:247],v[252:255]\n"
MF "v[100:103],a[40:43],a[240:243],v[100:103]\n"
LD "v201,s[48:51],s80 offen offset:1024 lds\n"
MF "v[100:103],a[0:3],v[188:191],v[100:103]\n"
"s_waitcnt lgkmcnt(7)\n"
MF "v[136:139],a[0:3],v[176:179],v[252:255]\n"
TR "v[188:189],v3 offset:24576\n"
MF "v[136:139],a[40:43],v[180:183],v[136:139]\n"
TR "v[190:191],v3 offset:26624\n"
MF "v[136:139],a[80:83],v[184:187],v[136:139]\n"
LD "v202,s[48:51],s80 offen offset:2048 lds\n"
MF "v[144:147],a[84:87],v[184:187],v[252:255]\n"
TR "a[240:241],v3 offset:28672\n"
MF "v[144:147],a[44:47],v[180:183],v[144:147]\n"
TR "a[242:243],v3 offset:30720\n"
MF "v[144:147],a[4:7],v[176:179],v[144:147]\n"
LD "v203,s[48:51],s80 offen offset:3072 lds\n"
MF "v[152:155],a[8:11],v[176:179],v[252:255]\n"
TR "a[244:245],v3 offset:32768\n"
MF "v[152:155],a[48:51],v[180:183],v[152:155]\n"
TR "a[246:247],v3 offset:34816\n"
MF "v[152:155],a[88:91],v[184:187],v[152:155]\n"
MF "v[160:163],a[92:95],v[184:187],v[252:255]\n"
MF "v[160:163],a[52:55],v[180:183],v[160:163]\n"
MF "v[160:163],a[12:15],v[176:179],v[160:163]\n"
MF "v[168:171],a[16:19],v[176:179],v[252:255]\n"
MF "v[168:171],a[56:59],v[180:183],v[168:171]\n"
"s_waitcnt vmcnt(24) lgkmcnt(0)\n"
MF "v[168:171],a[96:99],v[184:187],v[168:171]\n"
"s_barrier\n"
MF "v[172:175],a[96:99],a[244:247],v[252:255]\n"
"s_add_u32 s89,s89,s65\n"
TR "v[176:177],v0\n"
MF "v[172:175],a[56:59],a[240:243],v[172:175]\n"
"s_add_u32 m0,s79,24576\n"
TR "v[178:179],v0 offset:2048\n"
MF "v[172:175],a[16:19],v[188:191],v[172:175]\n"
LD "v192,s[48:51],s89 offen lds\n"
MF "v[164:167],a[12:15],v[188:191],v[252:255]\n"
TR "v[180:181],v0 offset:4096\n"
MF "v[164:167],a[52:55],a[240:243],v[164:167]\n"
TR "v[182:183],v0 offset:6144\n"
MF "v[164:167],a[92:95],a[244:247],v[164:167]\n"
LD "v193,s[48:51],s89 offen offset:1024 lds\n"
MF "v[156:159],a[88:91],a[244:247],v[252:255]\n"
TR "v[184:185],v0 offset:8192\n"
MF "v[156:159],a[48:51],a[240:243],v[156:159]\n"
"s_nop 0\n"
TR "v[186:187],v0 offset:10240\n"
"ds_write_addtid_b32 v56 offset:49152\n"
MF "v[156:159],a[8:11],v[188:191],v[156:159]\n"
"s_nop 0\n"
"ds_write_addtid_b32 v57 offset:49408\n"
"ds_write_addtid_b32 v58 offset:49664\n"
MF "v[148:151],a[4:7],v[188:191],v[252:255]\n"
"s_nop 0\n"
"ds_write_addtid_b32 v59 offset:49920\n"
"ds_write_addtid_b32 v60 offset:50176\n"
MF "v[148:151],a[44:47],a[240:243],v[148:151]\n"
LD "v194,s[48:51],s89 offen offset:2048 lds\n"
MF "v[148:151],a[84:87],a[244:247],v[148:151]\n"
"s_nop 0\n"
"ds_write_addtid_b32 v61 offset:50432\n"
"ds_write_addtid_b32 v62 offset:50688\n"
MF "v[140:143],a[80:83],a[244:247],v[252:255]\n"
"s_nop 0\n"
"ds_write_addtid_b32 v63 offset:50944\n"
"ds_write_addtid_b32 v64 offset:51200\n"
MF "v[140:143],a[40:43],a[240:243],v[140:143]\n"
"s_nop 0\n"
"ds_write_addtid_b32 v65 offset:51456\n"
"ds_write_addtid_b32 v66 offset:51712\n"
MF "v[140:143],a[0:3],v[188:191],v[140:143]\n"
"s_waitcnt lgkmcnt(11)\n"
LD "v195,s[48:51],s89 offen offset:3072 lds\n"
MF "v[96:99],a[120:123],v[176:179],v[96:99]\n"
"s_nop 0\n"
TR "v[188:189],v1\n"
"ds_write_addtid_b32 v67 offset:51968\n"
MF "v[96:99],a[160:163],v[180:183],v[96:99]\n"
"s_nop 0\n"
TR "v[190:191],v1 offset:2048\n"
"ds_write_addtid_b32 v68 offset:52224\n"
MF "v[96:99],a[200:203],v[184:187],v[96:99]\n"
"s_nop 0\n"
"ds_write_addtid_b32 v69 offset:52480\n"
"ds_write_addtid_b32 v70 offset:52736\n"
MF "v[104:107],a[204:207],v[184:187],v[104:107]\n"
"s_nop 0\n"
TR "a[240:241],v1 offset:4096\n"
"ds_write_addtid_b32 v71 offset:52992\n"
MF "v[104:107],a[164:167],v[180:183],v[104:107]\n"
"s_add_u32 m0,s79,28672\n"
TR "a[242:243],v1 offset:6144\n"
"ds_write_addtid_b32 v72 offset:49152\n"
MF "v[104:107],a[124:127],v[176:179],v[104:107]\n"
".Lpg26h:\n"
"s_cbranch_execz .Lpg30h\n"
LD "v196,s[48:51],s89 offen lds\n"
MF "v[112:115],a[128:131],v[176:179],v[112:115]\n"
"s_nop 0\n"
TR "a[244:245],v1 offset:8192\n"
"ds_write_addtid_b32 v73 offset:49408\n"
MF "v[112:115],a[168:171],v[180:183],v[112:115]\n"
"s_nop 0\n"
TR "a[246:247],v1 offset:10240\n"
"ds_write_addtid_b32 v74 offset:49664\n"
MF "v[112:115],a[208:211],v[184:187],v[112:115]\n"
LD "v197,s[48:51],s89 offen offset:1024 lds\n"
MF "v[120:123],a[212:215],v[184:187],v[120:123]\n"
"s_nop 0\n"
"ds_write_addtid_b32 v75 offset:49920\n"
"ds_write_addtid_b32 v76 offset:50176\n"
MF "v[120:123],a[172:175],v[180:183],v[120:123]\n"
"s_nop 0\n"
"ds_write_addtid_b32 v77 offset:50432\n"
"ds_write_addtid_b32 v78 offset:50688\n"
MF "v[120:123],a[132:135],v[176:179],v[120:123]\n"
"s_nop 0\n"
"ds_write_addtid_b32 v79 offset:50944\n"
"ds_write_addtid_b32 v80 offset:51200\n"
MF "v[128:131],a[136:139],v[176:179],v[128:131]\n"
"s_nop 0\n"
"ds_write_addtid_b32 v81 offset:51456\n"
"ds_write_addtid_b32 v82 offset:51712\n"
MF "v[128:131],a[176:179],v[180:183],v[128:131]\n"
LD "v198,s[48:51],s89 offen offset:2048 lds\n"
MF "v[128:131],a[216:219],v[184:187],v[128:131]\n"
"s_waitcnt lgkmcnt(9)\n"
"ds_write_addtid_b32 v83 offset:51968\n"
"ds_write_addtid_b32 v84 offset:52224\n"
MF "v[132:135],a[216:219],a[244:247],v[132:135]\n"
"s_nop 0\n"
TR "v[176:177],v2\n"
"ds_write_addtid_b32 v85 offset:52480\n"
MF "v[132:135],a[176:179],a[240:243],v[132:135]\n"
"s_nop 0\n"
TR "v[178:179],v2 offset:2048\n"
"ds_write_addtid_b32 v86 offset:52736\n"
MF "v[132:135],a[136:139],v[188:191],v[132:135]\n"
LD "v199,s[48:51],s89 offen offset:3072 lds\n"
MF "v[124:127],a[132:135],v[188:191],v[124:127]\n"
"s_nop 0\n"
TR "v[180:181],v2 offset:4096\n"
"ds_write_addtid_b32 v87 offset:52992\n"
MF "v[124:127],a[172:175],a[240:243],v[124:127]\n"
"s_nop 0\n"
TR "v[182:183],v2 offset:6144\n"
"ds_write_addtid_b32 v88 offset:53248\n"
MF "v[124:127],a[212:215],a[244:247],v[124:127]\n"
"s_nop 0\n"
"ds_write_addtid_b32 v89 offset:53504\n"
"ds_write_addtid_b32 v90 offset:53760\n"
MF "v[116:119],a[208:211],a[244:247],v[116:119]\n"
"s_nop 0\n"
TR "v[184:185],v2 offset:8192\n"
"ds_write_addtid_b32 v91 offset:54016\n"
MF "v[116:119],a[168:171],a[240:243],v[116:119]\n"
"s_add_u32 m0,s79,32768\n"
TR "v[186:187],v2 offset:10240\n"
"ds_write_addtid_b32 v92 offset:50176\n"
MF "v[116:119],a[128:131],v[188:191],v[116:119]\n"
LD "v200,s[48:51],s89 offen lds\n"
MF "v[108:111],a[124:127],v[188:191],v[108:111]\n"
"s_nop 0\n"
"ds_write_addtid_b32 v93 offset:50432\n"
"ds_write_addtid_b32 v94 offset:50688\n"
MF "v[108:111],a[164:167],a[240:243],v[108:111]\n"
"ds_write_addtid_b32 v95 offset:50944\n"
MF "v[108:111],a[204:207],a[244:247],v[108:111]\n"
MF "v[100:103],a[200:203],a[244:247],v[100:103]\n"
LD "v201,s[48:51],s89 offen offset:1024 lds\n"
MF "v[100:103],a[160:163],a[240:243],v[100:103]\n"
MF "v[100:103],a[120:123],v[188:191],v[100:103]\n"
"s_waitcnt lgkmcnt(4)\n"
MF "v[136:139],a[120:123],v[176:179],v[136:139]\n"
TR "v[188:189],v3\n"
MF "v[136:139],a[160:163],v[180:183],v[136:139]\n"
TR "v[190:191],v3 offset:2048\n"
MF "v[136:139],a[200:203],v[184:187],v[136:139]\n"
LD "v202,s[48:51],s89 offen offset:2048 lds\n"
MF "v[144:147],a[204:207],v[184:187],v[144:147]\n"
TR "a[240:241],v3 offset:4096\n"
MF "v[144:147],a[164:167],v[180:183],v[144:147]\n"
TR "a[242:243],v3 offset:6144\n"
MF "v[144:147],a[124:127],v[176:179],v[144:147]\n"
LD "v203,s[48:51],s89 offen offset:3072 lds\n"
MF "v[152:155],a[128:131],v[176:179],v[152:155]\n"
TR "a[244:245],v3 offset:8192\n"
MF "v[152:155],a[168:171],v[180:183],v[152:155]\n"
TR "a[246:247],v3 offset:10240\n"
MF "v[152:155],a[208:211],v[184:187],v[152:155]\n"
MF "v[160:163],a[212:215],v[184:187],v[160:163]\n"
MF "v[160:163],a[172:175],v[180:183],v[160:163]\n"
MF "v[160:163],a[132:135],v[176:179],v[160:163]\n"
MF "v[168:171],a[136:139],v[176:179],v[168:171]\n"
MF "v[168:171],a[176:179],v[180:183],v[168:171]\n"
"s_waitcnt vmcnt(24) lgkmcnt(0)\n"
MF "v[168:171],a[216:219],v[184:187],v[168:171]\n"
"s_barrier\n"
MF "v[172:175],a[216:219],a[244:247],v[172:175]\n"
"s_add_u32 s80,s89,122880\n"
TR "v[176:177],v0 offset:49152\n"
MF "v[172:175],a[176:179],a[240:243],v[172:175]\n"
"s_add_u32 m0,s79,0\n"
TR "v[178:179],v0 offset:51200\n"
MF "v[172:175],a[136:139],v[188:191],v[172:175]\n"
LD "v192,s[48:51],s80 offen lds\n"
MF "v[164:167],a[132:135],v[188:191],v[164:167]\n"
TR "v[180:181],v0 offset:53248\n"
MF "v[164:167],a[172:175],a[240:243],v[164:167]\n"
TR "v[182:183],v0 offset:55296\n"
MF "v[164:167],a[212:215],a[244:247],v[164:167]\n"
LD "v193,s[48:51],s80 offen offset:1024 lds\n"
MF "v[156:159],a[208:211],a[244:247],v[156:159]\n"
TR "v[184:185],v0 offset:57344\n"
MF "v[156:159],a[168:171],a[240:243],v[156:159]\n"
TR "v[186:187],v0 offset:59392\n"
MF "v[156:159],a[128:131],v[188:191],v[156:159]\n"
MF "v[148:151],a[124:127],v[188:191],v[148:151]\n"
MF "v[148:151],a[164:167],a[240:243],v[148:151]\n"
LD "v194,s[48:51],s80 offen offset:2048 lds\n"
MF "v[148:151],a[204:207],a[244:247],v[148:151]\n"
MF "v[140:143],a[200:203],a[244:247],v[140:143]\n"
MF "v[140:143],a[160:163],a[240:243],v[140:143]\n"
MF "v[140:143],a[120:123],v[188:191],v[140:143]\n"
"s_waitcnt lgkmcnt(0)\n"
LD "v195,s[48:51],s80 offen offset:3072 lds\n"
MF "v[96:99],a[20:23],v[176:179],v[96:99]\n"
TR "v[188:189],v1 offset:49152\n"
MF "v[96:99],a[60:63],v[180:183],v[96:99]\n"
TR "v[190:191],v1 offset:51200\n"
MF "v[96:99],a[100:103],v[184:187],v[96:99]\n"
MF "v[104:107],a[104:107],v[184:187],v[104:107]\n"
TR "a[240:241],v1 offset:53248\n"
MF "v[104:107],a[64:67],v[180:183],v[104:107]\n"
"s_add_u32 m0,s79,4096\n"
TR "a[242:243],v1 offset:55296\n"
MF "v[104:107],a[24:27],v[176:179],v[104:107]\n"
LD "v196,s[48:51],s80 offen lds\n"
MF "v[112:115],a[28:31],v[176:179],v[112:115]\n"
TR "a[244:245],v1 offset:57344\n"
MF "v[112:115],a[68:71],v[180:183],v[112:115]\n"
TR "a[246:247],v1 offset:59392\n"
MF "v[112:115],a[108:111],v[184:187],v[112:115]\n"
LD "v197,s[48:51],s80 offen offset:1024 lds\n"
MF "v[120:123],a[112:115],v[184:187],v[120:123]\n"
MF "v[120:123],a[72:75],v[180:183],v[120:123]\n"
MF "v[120:123],a[32:35],v[176:179],v[120:123]\n"
MF "v[128:131],a[36:39],v[176:179],v[128:131]\n"
MF "v[128:131],a[76:79],v[180:183],v[128:131]\n"
LD "v198,s[48:51],s80 offen offset:2048 lds\n"
MF "v[128:131],a[116:119],v[184:187],v[128:131]\n"
"s_waitcnt lgkmcnt(0)\n"
MF "v[132:135],a[116:119],a[244:247],v[132:135]\n"
TR "v[176:177],v2 offset:49152\n"
MF "v[132:135],a[76:79],a[240:243],v[132:135]\n"
TR "v[178:179],v2 offset:51200\n"
MF "v[132:135],a[36:39],v[188:191],v[132:135]\n"
LD "v199,s[48:51],s80 offen offset:3072 lds\n"
MF "v[124:127],a[32:35],v[188:191],v[124:127]\n"
TR "v[180:181],v2 offset:53248\n"
MF "v[124:127],a[72:75],a[240:243],v[124:127]\n"
TR "v[182:183],v2 offset:55296\n"
MF "v[124:127],a[112:115],a[244:247],v[124:127]\n"
MF "v[116:119],a[108:111],a[244:247],v[116:119]\n"
TR "v[184:185],v2 offset:57344\n"
MF "v[116:119],a[68:71],a[240:243],v[116:119]\n"
"s_add_u32 m0,s79,8192\n"
TR "v[186:187],v2 offset:59392\n"
MF "v[116:119],a[28:31],v[188:191],v[116:119]\n"
LD "v200,s[48:51],s80 offen lds\n"
MF "v[108:111],a[24:27],v[188:191],v[108:111]\n"
MF "v[108:111],a[64:67],a[240:243],v[108:111]\n"
MF "v[108:111],a[104:107],a[244:247],v[108:111]\n"
MF "v[100:103],a[100:103],a[244:247],v[100:103]\n"
LD "v201,s[48:51],s80 offen offset:1024 lds\n"
MF "v[100:103],a[60:63],a[240:243],v[100:103]\n"
MF "v[100:103],a[20:23],v[188:191],v[100:103]\n"
"s_waitcnt lgkmcnt(0)\n"
MF "v[136:139],a[20:23],v[176:179],v[136:139]\n"
TR "v[188:189],v3 offset:49152\n"
MF "v[136:139],a[60:63],v[180:183],v[136:139]\n"
TR "v[190:191],v3 offset:51200\n"
MF "v[136:139],a[100:103],v[184:187],v[136:139]\n"
LD "v202,s[48:51],s80 offen offset:2048 lds\n"
MF "v[144:147],a[104:107],v[184:187],v[144:147]\n"
TR "a[240:241],v3 offset:53248\n"
MF "v[144:147],a[64:67],v[180:183],v[144:147]\n"
TR "a[242:243],v3 offset:55296\n"
MF "v[144:147],a[24:27],v[176:179],v[144:147]\n"
LD "v203,s[48:51],s80 offen offset:3072 lds\n"
MF "v[152:155],a[28:31],v[176:179],v[152:155]\n"
TR "a[244:245],v3 offset:57344\n"
MF "v[152:155],a[68:71],v[180:183],v[152:155]\n"
TR "a[246:247],v3 offset:59392\n"
MF "v[152:155],a[108:111],v[184:187],v[152:155]\n"
MF "v[160:163],a[112:115],v[184:187],v[160:163]\n"
MF "v[160:163],a[72:75],v[180:183],v[160:163]\n"
MF "v[160:163],a[32:35],v[176:179],v[160:163]\n"
MF "v[168:171],a[36:39],v[176:179],v[168:171]\n"
MF "v[168:171],a[76:79],v[180:183],v[168:171]\n"
"s_waitcnt vmcnt(24) lgkmcnt(0)\n"
MF "v[168:171],a[116:119],v[184:187],v[168:171]\n"
"s_barrier\n"
MF "v[172:175],a[116:119],a[244:247],v[172:175]\n"
"s_add_u32 s88,s88,s65\n"
TR "v[176:177],v4\n"
MF "v[172:175],a[76:79],a[240:243],v[172:175]\n"
"s_add_u32 m0,s79,49152\n"
TR "v[178:179],v4 offset:2048\n"
MF "v[172:175],a[36:39],v[188:191],v[172:175]\n"
LD "v192,s[48:51],s88 offen lds\n"
MF "v[164:167],a[32:35],v[188:191],v[164:167]\n"
TR "v[180:181],v4 offset:4096\n"
MF "v[164:167],a[72:75],a[240:243],v[164:167]\n"
TR "v[182:183],v4 offset:6144\n"
MF "v[164:167],a[112:115],a[244:247],v[164:167]\n"
LD "v193,s[48:51],s88 offen offset:1024 lds\n"
MF "v[156:159],a[108:111],a[244:247],v[156:159]\n"
TR "v[184:185],v4 offset:8192\n"
MF "v[156:159],a[68:71],a[240:243],v[156:159]\n"
TR "v[186:187],v4 offset:10240\n"
MF "v[156:159],a[28:31],v[188:191],v[156:159]\n"
MF "v[148:151],a[24:27],v[188:191],v[148:151]\n"
MF "v[148:151],a[64:67],a[240:243],v[148:151]\n"
LD "v194,s[48:51],s88 offen offset:2048 lds\n"
MF "v[148:151],a[104:107],a[244:247],v[148:151]\n"
MF "v[140:143],a[100:103],a[244:247],v[140:143]\n"
MF "v[140:143],a[60:63],a[240:243],v[140:143]\n"
MF "v[140:143],a[20:23],v[188:191],v[140:143]\n"
"s_waitcnt lgkmcnt(0)\n"
LD "v195,s[48:51],s88 offen offset:3072 lds\n"
MF "v[96:99],a[140:143],v[176:179],v[96:99]\n"
TR "v[188:189],v5\n"
MF "v[96:99],a[180:183],v[180:183],v[96:99]\n"
TR "v[190:191],v5 offset:2048\n"
MF "v[96:99],a[220:223],v[184:187],v[96:99]\n"
MF "v[104:107],a[224:227],v[184:187],v[104:107]\n"
TR "a[240:241],v5 offset:4096\n"
MF "v[104:107],a[184:187],v[180:183],v[104:107]\n"
"s_add_u32 m0,s79,53248\n"
TR "a[242:243],v5 offset:6144\n"
MF "v[104:107],a[144:147],v[176:179],v[104:107]\n"
LD "v196,s[48:51],s88 offen lds\n"
MF "v[112:115],a[148:151],v[176:179],v[112:115]\n"
TR "a[244:245],v5 offset:8192\n"
MF "v[112:115],a[188:191],v[180:183],v[112:115]\n"
TR "a[246:247],v5 offset:10240\n"
MF "v[112:115],a[228:231],v[184:187],v[112:115]\n"
LD "v197,s[48:51],s88 offen offset:1024 lds\n"
MF "v[120:123],a[232:235],v[184:187],v[120:123]\n"
MF "v[120:123],a[192:195],v[180:183],v[120:123]\n"
MF "v[120:123],a[152:155],v[176:179],v[120:123]\n"
MF "v[128:131],a[156:159],v[176:179],v[128:131]\n"
MF "v[128:131],a[196:199],v[180:183],v[128:131]\n"
LD "v198,s[48:51],s88 offen offset:2048 lds\n"
MF "v[128:131],a[236:239],v[184:187],v[128:131]\n"
"s_waitcnt lgkmcnt(0)\n"
MF "v[132:135],a[236:239],a[244:247],v[132:135]\n"
TR "v[176:177],v6\n"
MF "v[132:135],a[196:199],a[240:243],v[132:135]\n"
TR "v[178:179],v6 offset:2048\n"
MF "v[132:135],a[156:159],v[188:191],v[132:135]\n"
LD "v199,s[48:51],s88 offen offset:3072 lds\n"
MF "v[124:127],a[152:155],v[188:191],v[124:127]\n"
TR "v[180:181],v6 offset:4096\n"
MF "v[124:127],a[192:195],a[240:243],v[124:127]\n"
TR "v[182:183],v6 offset:6144\n"
MF "v[124:127],a[232:235],a[244:247],v[124:127]\n"
MF "v[116:119],a[228:231],a[244:247],v[116:119]\n"
TR "v[184:185],v6 offset:8192\n"
MF "v[116:119],a[188:191],a[240:243],v[116:119]\n"
"s_add_u32 m0,s79,57344\n"
TR "v[186:187],v6 offset:10240\n"
MF "v[116:119],a[148:151],v[188:191],v[116:119]\n"
LD "v200,s[48:51],s88 offen lds\n"
MF "v[108:111],a[144:147],v[188:191],v[108:111]\n"
MF "v[108:111],a[184:187],a[240:243],v[108:111]\n"
MF "v[108:111],a[224:227],a[244:247],v[108:111]\n"
MF "v[100:103],a[220:223],a[244:247],v[100:103]\n"
LD "v201,s[48:51],s88 offen offset:1024 lds\n"
MF "v[100:103],a[180:183],a[240:243],v[100:103]\n"
MF "v[100:103],a[140:143],v[188:191],v[100:103]\n"
"s_waitcnt lgkmcnt(0)\n"
MF "v[136:139],a[140:143],v[176:179],v[136:139]\n"
TR "v[188:189],v7\n"
MF "v[136:139],a[180:183],v[180:183],v[136:139]\n"
TR "v[190:191],v7 offset:2048\n"
MF "v[136:139],a[220:223],v[184:187],v[136:139]\n"
LD "v202,s[48:51],s88 offen offset:2048 lds\n"
MF "v[144:147],a[224:227],v[184:187],v[144:147]\n"
TR "a[240:241],v7 offset:4096\n"
MF "v[144:147],a[184:187],v[180:183],v[144:147]\n"
TR "a[242:243],v7 offset:6144\n"
MF "v[144:147],a[144:147],v[176:179],v[144:147]\n"
LD "v203,s[48:51],s88 offen offset:3072 lds\n"
MF "v[152:155],a[148:151],v[176:179],v[152:155]\n"
TR "a[244:245],v7 offset:8192\n"
MF "v[152:155],a[188:191],v[180:183],v[152:155]\n"
TR "a[246:247],v7 offset:10240\n"
MF "v[152:155],a[228:231],v[184:187],v[152:155]\n"
MF "v[160:163],a[232:235],v[184:187],v[160:163]\n"
MF "v[160:163],a[192:195],v[180:183],v[160:163]\n"
MF "v[160:163],a[152:155],v[176:179],v[160:163]\n"
MF "v[168:171],a[156:159],v[176:179],v[168:171]\n"
MF "v[168:171],a[196:199],v[180:183],v[168:171]\n"
"s_waitcnt vmcnt(24) lgkmcnt(0)\n"
MF "v[168:171],a[236:239],v[184:187],v[168:171]\n"
"s_barrier\n"
MF "v[172:175],a[236:239],a[244:247],v[172:175]\n"
"s_add_u32 s80,s88,122880\n"
TR "v[176:177],v0 offset:24576\n"
MF "v[172:175],a[196:199],a[240:243],v[172:175]\n"
"s_add_u32 m0,s79,98304\n"
"s_add_u32 s85,s85,1\n"
TR "v[178:179],v0 offset:26624\n"
MF "v[172:175],a[156:159],v[188:191],v[172:175]\n"
LD "v192,s[48:51],s80 offen lds\n"
MF "v[164:167],a[152:155],v[188:191],v[164:167]\n"
"s_nop 0\n"
TR "v[180:181],v0 offset:28672\n"
"ds_write_addtid_b32 v96 offset:24576\n"
MF "v[164:167],a[192:195],a[240:243],v[164:167]\n"
"s_nop 0\n"
TR "v[182:183],v0 offset:30720\n"
"ds_write_addtid_b32 v97 offset:24832\n"
MF "v[164:167],a[232:235],a[244:247],v[164:167]\n"
LD "v193,s[48:51],s80 offen offset:1024 lds\n"
MF "v[156:159],a[228:231],a[244:247],v[156:159]\n"
"s_nop 0\n"
TR "v[184:185],v0 offset:32768\n"
"ds_write_addtid_b32 v98 offset:25088\n"
MF "v[156:159],a[188:191],a[240:243],v[156:159]\n"
"s_nop 0\n"
TR "v[186:187],v0 offset:34816\n"
"ds_write_addtid_b32 v99 offset:25344\n"
MF "v[156:159],a[148:151],v[188:191],v[156:159]\n"
"s_nop 0\n"
"ds_write_addtid_b32 v100 offset:25600\n"
"ds_write_addtid_b32 v101 offset:25856\n"
MF "v[148:151],a[144:147],v[188:191],v[148:151]\n"
LD "v194,s[48:51],s80 offen offset:2048 lds\n"
MF "v[148:151],a[184:187],a[240:243],v[148:151]\n"
"s_nop 0\n"
"ds_write_addtid_b32 v102 offset:26112\n"
"ds_write_addtid_b32 v103 offset:26368\n"
MF "v[148:151],a[224:227],a[244:247],v[148:151]\n"
"s_nop 0\n"
"ds_write_addtid_b32 v104 offset:26624\n"
"ds_write_addtid_b32 v105 offset:26880\n"
MF "v[140:143],a[220:223],a[244:247],v[140:143]\n"
"s_nop 0\n"
"ds_write_addtid_b32 v106 offset:27136\n"
"ds_write_addtid_b32 v107 offset:27392\n"
MF "v[140:143],a[180:183],a[240:243],v[140:143]\n"
MF "v[140:143],a[140:143],v[188:191],v[140:143]\n"
"s_waitcnt lgkmcnt(9)\n"
MF "v[16:19],a[0:3],v[176:179],v[252:255]\n"
"s_nop 0\n"
TR "v[188:189],v1 offset:24576\n"
"ds_write_addtid_b32 v108 offset:27648\n"
MF "v[16:19],a[40:43],v[180:183],v[16:19]\n"
"s_nop 0\n"
TR "v[190:191],v1 offset:26624\n"
"ds_write_addtid_b32 v109 offset:27904\n"
MF "v[16:19],a[80:83],v[184:187],v[16:19]\n"
LD "v195,s[48:51],s80 offen offset:3072 lds\n"
MF "v[24:27],a[84:87],v[184:187],v[252:255]\n"
"s_add_u32 s60,s64,1\n"
TR "a[240:241],v1 offset:28672\n"
"ds_write_addtid_b32 v110 offset:28160\n"
MF "v[24:27],a[44:47],v[180:183],v[24:27]\n"
"s_add_u32 m0,s79,102400\n"
TR "a[242:243],v1 offset:30720\n"
"ds_write_addtid_b32 v111 offset:24320\n"
MF "v[24:27],a[4:7],v[176:179],v[24:27]\n"
LD "v196,s[48:51],s80 offen lds\n"
MF "v[32:35],a[8:11],v[176:179],v[252:255]\n"
"s_cmp_eq_u32 s60,8\n"
"s_cselect_b32 s60,0,s60\n"
TR "a[244:245],v1 offset:32768\n"
MF "v[32:35],a[48:51],v[180:183],v[32:35]\n"
"s_cmp_eq_u32 s60,10\n"
"s_cselect_b32 s60,s61,s60\n"
TR "a[246:247],v1 offset:34816\n"
MF "v[32:35],a[88:91],v[184:187],v[32:35]\n"
"s_sub_u32 s65,s60,s64\n"
"ds_write_addtid_b32 v112 offset:24576\n"
"ds_write_addtid_b32 v113 offset:24832\n"
MF "v[40:43],a[92:95],v[184:187],v[252:255]\n"
"s_lshl_b32 s65,s65,7\n"
"ds_write_addtid_b32 v114 offset:25088\n"
"ds_write_addtid_b32 v115 offset:25344\n"
MF "v[40:43],a[52:55],v[180:183],v[40:43]\n"
LD "v197,s[48:51],s80 offen offset:1024 lds\n"
MF "v[40:43],a[12:15],v[176:179],v[40:43]\n"
"s_mov_b32 s64,s60\n"
"ds_write_addtid_b32 v116 offset:25600\n"
"ds_write_addtid_b32 v117 offset:25856\n"
MF "v[48:51],a[16:19],v[176:179],v[252:255]\n"
"s_nop 0\n"
"ds_write_addtid_b32 v118 offset:26112\n"
"ds_write_addtid_b32 v119 offset:26368\n"
MF "v[48:51],a[56:59],v[180:183],v[48:51]\n"
"s_nop 0\n"
"ds_write_addtid_b32 v120 offset:26624\n"
"ds_write_addtid_b32 v121 offset:26880\n"
MF "v[48:51],a[96:99],v[184:187],v[48:51]\n"
"s_waitcnt lgkmcnt(10)\n"
LD "v198,s[48:51],s80 offen offset:2048 lds\n"
MF "v[52:55],a[96:99],a[244:247],v[252:255]\n"
"s_nop 0\n"
TR "v[176:177],v2 offset:24576\n"
"ds_write_addtid_b32 v122 offset:27136\n"
MF "v[52:55],a[56:59],a[240:243],v[52:55]\n"
"s_nop 0\n"
TR "v[178:179],v2 offset:26624\n"
"ds_write_addtid_b32 v123 offset:27392\n"
MF "v[52:55],a[16:19],v[188:191],v[52:55]\n"
"s_nop 0\n"
"ds_write_addtid_b32 v124 offset:27648\n"
"ds_write_addtid_b32 v125 offset:27904\n"
MF "v[44:47],a[12:15],v[188:191],v[252:255]\n"
"s_nop 0\n"
TR "v[180:181],v2 offset:28672\n"
"ds_write_addtid_b32 v126 offset:28160\n"
MF "v[44:47],a[52:55],a[240:243],v[44:47]\n"
"s_nop 0\n"
TR "v[182:183],v2 offset:30720\n"
"ds_write_addtid_b32 v127 offset:28416\n"
MF "v[44:47],a[92:95],a[244:247],v[44:47]\n"
LD "v199,s[48:51],s80 offen offset:3072 lds\n"
MF "v[36:39],a[88:91],a[244:247],v[252:255]\n"
"s_nop 0\n"
TR "v[184:185],v2 offset:32768\n"
"ds_write_addtid_b32 v128 offset:28672\n"
MF "v[36:39],a[48:51],a[240:243],v[36:39]\n"
"s_add_u32 m0,s79,106496\n"
TR "v[186:187],v2 offset:34816\n"
"ds_write_addtid_b32 v129 offset:24832\n"
MF "v[36:39],a[8:11],v[188:191],v[36:39]\n"
LD "v200,s[48:51],s80 offen lds\n"
MF "v[28:31],a[4:7],v[188:191],v[252:255]\n"
"s_nop 0\n"
"ds_write_addtid_b32 v130 offset:25088\n"
"ds_write_addtid_b32 v131 offset:25344\n"
MF "v[28:31],a[44:47],a[240:243],v[28:31]\n"
"s_nop 0\n"
"ds_write_addtid_b32 v132 offset:25600\n"
"ds_write_addtid_b32 v133 offset:25856\n"
MF "v[28:31],a[84:87],a[244:247],v[28:31]\n"
"s_nop 0\n"
"ds_write_addtid_b32 v134 offset:26112\n"
"ds_write_addtid_b32 v135 offset:26368\n"
MF "v[20:23],a[80:83],a[244:247],v[252:255]\n"
MF "v[20:23],a[40:43],a[240:243],v[20:23]\n"
LD "v201,s[48:51],s80 offen offset:1024 lds\n"
MF "v[20:23],a[0:3],v[188:191],v[20:23]\n"
"s_waitcnt lgkmcnt(7)\n"
MF "v[56:59],a[0:3],v[176:179],v[252:255]\n"
TR "v[188:189],v3 offset:24576\n"
MF "v[56:59],a[40:43],v[180:183],v[56:59]\n"
TR "v[190:191],v3 offset:26624\n"
MF "v[56:59],a[80:83],v[184:187],v[56:59]\n"
LD "v202,s[48:51],s80 offen offset:2048 lds\n"
MF "v[64:67],a[84:87],v[184:187],v[252:255]\n"
TR "a[240:241],v3 offset:28672\n"
MF "v[64:67],a[44:47],v[180:183],v[64:67]\n"
TR "a[242:243],v3 offset:30720\n"
MF "v[64:67],a[4:7],v[176:179],v[64:67]\n"
LD "v203,s[48:51],s80 offen offset:3072 lds\n"
MF "v[72:75],a[8:11],v[176:179],v[252:255]\n"
TR "a[244:245],v3 offset:32768\n"
MF "v[72:75],a[48:51],v[180:183],v[72:75]\n"
TR "a[246:247],v3 offset:34816\n"
MF "v[72:75],a[88:91],v[184:187],v[72:75]\n"
MF "v[80:83],a[92:95],v[184:187],v[252:255]\n"
MF "v[80:83],a[52:55],v[180:183],v[80:83]\n"
MF "v[80:83],a[12:15],v[176:179],v[80:83]\n"
MF "v[88:91],a[16:19],v[176:179],v[252:255]\n"
MF "v[88:91],a[56:59],v[180:183],v[88:91]\n"
"s_waitcnt vmcnt(24) lgkmcnt(0)\n"
MF "v[88:91],a[96:99],v[184:187],v[88:91]\n"
"s_barrier\n"
MF "v[92:95],a[96:99],a[244:247],v[252:255]\n"
"s_add_u32 s89,s89,s65\n"
TR "v[176:177],v0\n"
MF "v[92:95],a[56:59],a[240:243],v[92:95]\n"
"s_add_u32 m0,s79,24576\n"
TR "v[178:179],v0 offset:2048\n"
MF "v[92:95],a[16:19],v[188:191],v[92:95]\n"
LD "v192,s[48:51],s89 offen lds\n"
MF "v[84:87],a[12:15],v[188:191],v[252:255]\n"
TR "v[180:181],v0 offset:4096\n"
MF "v[84:87],a[52:55],a[240:243],v[84:87]\n"
TR "v[182:183],v0 offset:6144\n"
MF "v[84:87],a[92:95],a[244:247],v[84:87]\n"
LD "v193,s[48:51],s89 offen offset:1024 lds\n"
MF "v[76:79],a[88:91],a[244:247],v[252:255]\n"
TR "v[184:185],v0 offset:8192\n"
MF "v[76:79],a[48:51],a[240:243],v[76:79]\n"
"s_nop 0\n"
TR "v[186:187],v0 offset:10240\n"
"ds_write_addtid_b32 v136 offset:49152\n"
MF "v[76:79],a[8:11],v[188:191],v[76:79]\n"
"s_nop 0\n"
"ds_write_addtid_b32 v137 offset:49408\n"
"ds_write_addtid_b32 v138 offset:49664\n"
MF "v[68:71],a[4:7],v[188:191],v[252:255]\n"
"s_nop 0\n"
"ds_write_addtid_b32 v139 offset:49920\n"
"ds_write_addtid_b32 v140 offset:50176\n"
MF "v[68:71],a[44:47],a[240:243],v[68:71]\n"
LD "v194,s[48:51],s89 offen offset:2048 lds\n"
MF "v[68:71],a[84:87],a[244:247],v[68:71]\n"
"s_nop 0\n"
"ds_write_addtid_b32 v141 offset:50432\n"
"ds_write_addtid_b32 v142 offset:50688\n"
MF "v[60:63],a[80:83],a[244:247],v[252:255]\n"
"s_nop 0\n"
"ds_write_addtid_b32 v143 offset:50944\n"
"ds_write_addtid_b32 v144 offset:51200\n"
MF "v[60:63],a[40:43],a[240:243],v[60:63]\n"
"s_nop 0\n"
"ds_write_addtid_b32 v145 offset:51456\n"
"ds_write_addtid_b32 v146 offset:51712\n"
MF "v[60:63],a[0:3],v[188:191],v[60:63]\n"
"s_waitcnt lgkmcnt(11)\n"
LD "v195,s[48:51],s89 offen offset:3072 lds\n"
MF "v[16:19],a[120:123],v[176:179],v[16:19]\n"
"s_nop 0\n"
TR "v[188:189],v1\n"
"ds_write_addtid_b32 v147 offset:51968\n"
MF "v[16:19],a[160:163],v[180:183],v[16:19]\n"
"s_nop 0\n"
TR "v[190:191],v1 offset:2048\n"
"ds_write_addtid_b32 v148 offset:52224\n"
MF "v[16:19],a[200:203],v[184:187],v[16:19]\n"
"s_nop 0\n"
"ds_write_addtid_b32 v149 offset:52480\n"
"ds_write_addtid_b32 v150 offset:52736\n"
MF "v[24:27],a[204:207],v[184:187],v[24:27]\n"
"s_nop 0\n"
TR "a[240:241],v1 offset:4096\n"
"ds_write_addtid_b32 v151 offset:52992\n"
MF "v[24:27],a[164:167],v[180:183],v[24:27]\n"
".Lpg27h:\n"
"s_cbranch_execz .Lpg31h\n"
"s_add_u32 m0,s79,28672\n"
TR "a[242:243],v1 offset:6144\n"
"ds_write_addtid_b32 v152 offset:49152\n"
MF "v[24:27],a[124:127],v[176:179],v[24:27]\n"
LD "v196,s[48:51],s89 offen lds\n"
MF "v[32:35],a[128:131],v[176:179],v[32:35]\n"
"s_nop 0\n"
TR "a[244:245],v1 offset:8192\n"
"ds_write_addtid_b32 v153 offset:49408\n"
MF "v[32:35],a[168:171],v[180:183],v[32:35]\n"
"s_nop 0\n"
TR "a[246:247],v1 offset:10240\n"
"ds_write_addtid_b32 v154 offset:49664\n"
MF "v[32:35],a[208:211],v[184:187],v[32:35]\n"
LD "v197,s[48:51],s89 offen offset:1024 lds\n"
MF "v[40:43],a[212:215],v[184:187],v[40:43]\n"
"s_nop 0\n"
"ds_write_addtid_b32 v155 offset:49920\n"
"ds_write_addtid_b32 v156 offset:50176\n"
MF "v[40:43],a[172:175],v[180:183],v[40:43]\n"
"s_nop 0\n"
"ds_write_addtid_b32 v157 offset:50432\n"
"ds_write_addtid_b32 v158 offset:50688\n"
MF "v[40:43],a[132:135],v[176:179],v[40:43]\n"
"s_nop 0\n"
"ds_write_addtid_b32 v159 offset:50944\n"
"ds_write_addtid_b32 v160 offset:51200\n"
MF "v[48:51],a[136:139],v[176:179],v[48:51]\n"
"s_nop 0\n"
"ds_write_addtid_b32 v161 offset:51456\n"
"ds_write_addtid_b32 v162 offset:51712\n"
MF "v[48:51],a[176:179],v[180:183],v[48:51]\n"
LD "v198,s[48:51],s89 offen offset:2048 lds\n"
MF "v[48:51],a[216:219],v[184:187],v[48:51]\n"
"s_waitcnt lgkmcnt(9)\n"
"ds_write_addtid_b32 v163 offset:51968\n"
"ds_write_addtid_b32 v164 offset:52224\n"
MF "v[52:55],a[216:219],a[244:247],v[52:55]\n"
"s_nop 0\n"
TR "v[176:177],v2\n"
"ds_write_addtid_b32 v165 offset:52480\n"
MF "v[52:55],a[176:179],a[240:243],v[52:55]\n"
"s_nop 0\n"
TR "v[178:179],v2 offset:2048\n"
"ds_write_addtid_b32 v166 offset:52736\n"
MF "v[52:55],a[136:139],v[188:191],v[52:55]\n"
LD "v199,s[48:51],s89 offen offset:3072 lds\n"
MF "v[44:47],a[132:135],v[188:191],v[44:47]\n"
"s_nop 0\n"
TR "v[180:181],v2 offset:4096\n"
"ds_write_addtid_b32 v167 offset:52992\n"
MF "v[44:47],a[172:175],a[240:243],v[44:47]\n"
"s_nop 0\n"
TR "v[182:183],v2 offset:6144\n"
"ds_write_addtid_b32 v168 offset:53248\n"
MF "v[44:47],a[212:215],a[244:247],v[44:47]\n"
"s_nop 0\n"
"ds_write_addtid_b32 v169 offset:53504\n"
"ds_write_addtid_b32 v170 offset:53760\n"
MF "v[36:39],a[208:211],a[244:247],v[36:39]\n"
"s_nop 0\n"
TR "v[184:185],v2 offset:8192\n"
"ds_write_addtid_b32 v171 offset:54016\n"
MF "v[36:39],a[168:171],a[240:243],v[36:39]\n"
"s_add_u32 m0,s79,32768\n"
TR "v[186:187],v2 offset:10240\n"
"ds_write_addtid_b32 v172 offset:50176\n"
MF "v[36:39],a[128:131],v[188:191],v[36:39]\n"
LD "v200,s[48:51],s89 offen lds\n"
MF "v[28:31],a[124:127],v[188:191],v[28:31]\n"
"s_nop 0\n"
"ds_write_addtid_b32 v173 offset:50432\n"
"ds_write_addtid_b32 v174 offset:50688\n"
MF "v[28:31],a[164:167],a[240:243],v[28:31]\n"
"ds_write_addtid_b32 v175 offset:50944\n"
MF "v[28:31],a[204:207],a[244:247],v[28:31]\n"
MF "v[20:23],a[200:203],a[244:247],v[20:23]\n"
LD "v201,s[48:51],s89 offen offset:1024 lds\n"
MF "v[20:23],a[160:163],a[240:243],v[20:23]\n"
MF "v[20:23],a[120:123],v[188:191],v[20:23]\n"
"s_waitcnt lgkmcnt(4)\n"
MF "v[56:59],a[120:123],v[176:179],v[56:59]\n"
TR "v[188:189],v3\n"
MF "v[56:59],a[160:163],v[180:183],v[56:59]\n"
TR "v[190:191],v3 offset:2048\n"
MF "v[56:59],a[200:203],v[184:187],v[56:59]\n"
LD "v202,s[48:51],s89 offen offset:2048 lds\n"
MF "v[64:67],a[204:207],v[184:187],v[64:67]\n"
TR "a[240:241],v3 offset:4096\n"
MF "v[64:67],a[164:167],v[180:183],v[64:67]\n"
TR "a[242:243],v3 offset:6144\n"
MF "v[64:67],a[124:127],v[176:179],v[64:67]\n"
LD "v203,s[48:51],s89 offen offset:3072 lds\n"
MF "v[72:75],a[128:131],v[176:179],v[72:75]\n"
TR "a[244:245],v3 offset:8192\n"
MF "v[72:75],a[168:171],v[180:183],v[72:75]\n"
TR "a[246:247],v3 offset:10240\n"
MF "v[72:75],a[208:211],v[184:187],v[72:75]\n"
MF "v[80:83],a[212:215],v[184:187],v[80:83]\n"
MF "v[80:83],a[172:175],v[180:183],v[80:83]\n"
MF "v[80:83],a[132:135],v[176:179],v[80:83]\n"
MF "v[88:91],a[136:139],v[176:179],v[88:91]\n"
MF "v[88:91],a[176:179],v[180:183],v[88:91]\n"
"s_waitcnt vmcnt(24) lgkmcnt(0)\n"
MF "v[88:91],a[216:219],v[184:187],v[88:91]\n"
"s_barrier\n"
MF "v[92:95],a[216:219],a[244:247],v[92:95]\n"
"s_add_u32 s80,s89,122880\n"
TR "v[176:177],v0 offset:49152\n"
MF "v[92:95],a[176:179],a[240:243],v[92:95]\n"
"s_add_u32 m0,s79,0\n"
TR "v[178:179],v0 offset:51200\n"
MF "v[92:95],a[136:139],v[188:191],v[92:95]\n"
LD "v192,s[48:51],s80 offen lds\n"
MF "v[84:87],a[132:135],v[188:191],v[84:87]\n"
TR "v[180:181],v0 offset:53248\n"
MF "v[84:87],a[172:175],a[240:243],v[84:87]\n"
TR "v[182:183],v0 offset:55296\n"
MF "v[84:87],a[212:215],a[244:247],v[84:87]\n"
LD "v193,s[48:51],s80 offen offset:1024 lds\n"
MF "v[76:79],a[208:211],a[244:247],v[76:79]\n"
TR "v[184:185],v0 offset:57344\n"
MF "v[76:79],a[168:171],a[240:243],v[76:79]\n"
TR "v[186:187],v0 offset:59392\n"
MF "v[76:79],a[128:131],v[188:191],v[76:79]\n"
MF "v[68:71],a[124:127],v[188:191],v[68:71]\n"
MF "v[68:71],a[164:167],a[240:243],v[68:71]\n"
LD "v194,s[48:51],s80 offen offset:2048 lds\n"
MF "v[68:71],a[204:207],a[244:247],v[68:71]\n"
MF "v[60:63],a[200:203],a[244:247],v[60:63]\n"
MF "v[60:63],a[160:163],a[240:243],v[60:63]\n"
MF "v[60:63],a[120:123],v[188:191],v[60:63]\n"
"s_waitcnt lgkmcnt(0)\n"
LD "v195,s[48:51],s80 offen offset:3072 lds\n"
MF "v[16:19],a[20:23],v[176:179],v[16:19]\n"
TR "v[188:189],v1 offset:49152\n"
MF "v[16:19],a[60:63],v[180:183],v[16:19]\n"
TR "v[190:191],v1 offset:51200\n"
MF "v[16:19],a[100:103],v[184:187],v[16:19]\n"
MF "v[24:27],a[104:107],v[184:187],v[24:27]\n"
TR "a[240:241],v1 offset:53248\n"
MF "v[24:27],a[64:67],v[180:183],v[24:27]\n"
"s_add_u32 m0,s79,4096\n"
TR "a[242:243],v1 offset:55296\n"
MF "v[24:27],a[24:27],v[176:179],v[24:27]\n"
LD "v196,s[48:51],s80 offen lds\n"
MF "v[32:35],a[28:31],v[176:179],v[32:35]\n"
TR "a[244:245],v1 offset:57344\n"
MF "v[32:35],a[68:71],v[180:183],v[32:35]\n"
TR "a[246:247],v1 offset:59392\n"
MF "v[32:35],a[108:111],v[184:187],v[32:35]\n"
LD "v197,s[48:51],s80 offen offset:1024 lds\n"
MF "v[40:43],a[112:115],v[184:187],v[40:43]\n"
MF "v[40:43],a[72:75],v[180:183],v[40:43]\n"
MF "v[40:43],a[32:35],v[176:179],v[40:43]\n"
MF "v[48:51],a[36:39],v[176:179],v[48:51]\n"
MF "v[48:51],a[76:79],v[180:183],v[48:51]\n"
LD "v198,s[48:51],s80 offen offset:2048 lds\n"
MF "v[48:51],a[116:119],v[184:187],v[48:51]\n"
"s_waitcnt lgkmcnt(0)\n"
MF "v[52:55],a[116:119],a[244:247],v[52:55]\n"
TR "v[176:177],v2 offset:49152\n"
MF "v[52:55],a[76:79],a[240:243],v[52:55]\n"
TR "v[178:179],v2 offset:51200\n"
MF "v[52:55],a[36:39],v[188:191],v[52:55]\n"
LD "v199,s[48:51],s80 offen offset:3072 lds\n"
MF "v[44:47],a[32:35],v[188:191],v[44:47]\n"
TR "v[180:181],v2 offset:53248\n"
MF "v[44:47],a[72:75],a[240:243],v[44:47]\n"
TR "v[182:183],v2 offset:55296\n"
MF "v[44:47],a[112:115],a[244:247],v[44:47]\n"
MF "v[36:39],a[108:111],a[244:247],v[36:39]\n"
TR "v[184:185],v2 offset:57344\n"
MF "v[36:39],a[68:71],a[240:243],v[36:39]\n"
"s_add_u32 m0,s79,8192\n"
TR "v[186:187],v2 offset:59392\n"
MF "v[36:39],a[28:31],v[188:191],v[36:39]\n"
LD "v200,s[48:51],s80 offen lds\n"
MF "v[28:31],a[24:27],v[188:191],v[28:31]\n"
MF "v[28:31],a[64:67],a[240:243],v[28:31]\n"
MF "v[28:31],a[104:107],a[244:247],v[28:31]\n"
MF "v[20:23],a[100:103],a[244:247],v[20:23]\n"
LD "v201,s[48:51],s80 offen offset:1024 lds\n"
MF "v[20:23],a[60:63],a[240:243],v[20:23]\n"
MF "v[20:23],a[20:23],v[188:191],v[20:23]\n"
"s_waitcnt lgkmcnt(0)\n"
MF "v[56:59],a[20:23],v[176:179],v[56:59]\n"
TR "v[188:189],v3 offset:49152\n"
MF "v[56:59],a[60:63],v[180:183],v[56:59]\n"
TR "v[190:191],v3 offset:51200\n"
MF "v[56:59],a[100:103],v[184:187],v[56:59]\n"
LD "v202,s[48:51],s80 offen offset:2048 lds\n"
MF "v[64:67],a[104:107],v[184:187],v[64:67]\n"
TR "a[240:241],v3 offset:53248\n"
MF "v[64:67],a[64:67],v[180:183],v[64:67]\n"
TR "a[242:243],v3 offset:55296\n"
MF "v[64:67],a[24:27],v[176:179],v[64:67]\n"
LD "v203,s[48:51],s80 offen offset:3072 lds\n"
MF "v[72:75],a[28:31],v[176:179],v[72:75]\n"
TR "a[244:245],v3 offset:57344\n"
MF "v[72:75],a[68:71],v[180:183],v[72:75]\n"
TR "a[246:247],v3 offset:59392\n"
MF "v[72:75],a[108:111],v[184:187],v[72:75]\n"
MF "v[80:83],a[112:115],v[184:187],v[80:83]\n"
MF "v[80:83],a[72:75],v[180:183],v[80:83]\n"
MF "v[80:83],a[32:35],v[176:179],v[80:83]\n"
MF "v[88:91],a[36:39],v[176:179],v[88:91]\n"
MF "v[88:91],a[76:79],v[180:183],v[88:91]\n"
"s_waitcnt vmcnt(24) lgkmcnt(0)\n"
MF "v[88:91],a[116:119],v[184:187],v[88:91]\n"
"s_barrier\n"
MF "v[92:95],a[116:119],a[244:247],v[92:95]\n"
"s_add_u32 s88,s88,s65\n"
TR "v[176:177],v4\n"
MF "v[92:95],a[76:79],a[240:243],v[92:95]\n"
"s_add_u32 m0,s79,49152\n"
TR "v[178:179],v4 offset:2048\n"
MF "v[92:95],a[36:39],v[188:191],v[92:95]\n"
LD "v192,s[48:51],s88 offen lds\n"
MF "v[84:87],a[32:35],v[188:191],v[84:87]\n"
TR "v[180:181],v4 offset:4096\n"
MF "v[84:87],a[72:75],a[240:243],v[84:87]\n"
TR "v[182:183],v4 offset:6144\n"
MF "v[84:87],a[112:115],a[244:247],v[84:87]\n"
LD "v193,s[48:51],s88 offen offset:1024 lds\n"
MF "v[76:79],a[108:111],a[244:247],v[76:79]\n"
TR "v[184:185],v4 offset:8192\n"
MF "v[76:79],a[68:71],a[240:243],v[76:79]\n"
TR "v[186:187],v4 offset:10240\n"
MF "v[76:79],a[28:31],v[188:191],v[76:79]\n"
MF "v[68:71],a[24:27],v[188:191],v[68:71]\n"
MF "v[68:71],a[64:67],a[240:243],v[68:71]\n"
LD "v194,s[48:51],s88 offen offset:2048 lds\n"
MF "v[68:71],a[104:107],a[244:247],v[68:71]\n"
MF "v[60:63],a[100:103],a[244:247],v[60:63]\n"
MF "v[60:63],a[60:63],a[240:243],v[60:63]\n"
MF "v[60:63],a[20:23],v[188:191],v[60:63]\n"
"s_waitcnt lgkmcnt(0)\n"
LD "v195,s[48:51],s88 offen offset:3072 lds\n"
MF "v[16:19],a[140:143],v[176:179],v[16:19]\n"
TR "v[188:189],v5\n"
MF "v[16:19],a[180:183],v[180:183],v[16:19]\n"
TR "v[190:191],v5 offset:2048\n"
MF "v[16:19],a[220:223],v[184:187],v[16:19]\n"
MF "v[24:27],a[224:227],v[184:187],v[24:27]\n"
TR "a[240:241],v5 offset:4096\n"
MF "v[24:27],a[184:187],v[180:183],v[24:27]\n"
"s_add_u32 m0,s79,53248\n"
TR "a[242:243],v5 offset:6144\n"
MF "v[24:27],a[144:147],v[176:179],v[24:27]\n"
LD "v196,s[48:51],s88 offen lds\n"
MF "v[32:35],a[148:151],v[176:179],v[32:35]\n"
TR "a[244:245],v5 offset:8192\n"
MF "v[32:35],a[188:191],v[180:183],v[32:35]\n"
TR "a[246:247],v5 offset:10240\n"
MF "v[32:35],a[228:231],v[184:187],v[32:35]\n"
LD "v197,s[48:51],s88 offen offset:1024 lds\n"
MF "v[40:43],a[232:235],v[184:187],v[40:43]\n"
MF "v[40:43],a[192:195],v[180:183],v[40:43]\n"
MF "v[40:43],a[152:155],v[176:179],v[40:43]\n"
MF "v[48:51],a[156:159],v[176:179],v[48:51]\n"
MF "v[48:51],a[196:199],v[180:183],v[48:51]\n"
LD "v198,s[48:51],s88 offen offset:2048 lds\n"
MF "v[48:51],a[236:239],v[184:187],v[48:51]\n"
"s_waitcnt lgkmcnt(0)\n"
MF "v[52:55],a[236:239],a[244:247],v[52:55]\n"
TR "v[176:177],v6\n"
MF "v[52:55],a[196:199],a[240:243],v[52:55]\n"
TR "v[178:179],v6 offset:2048\n"
MF "v[52:55],a[156:159],v[188:191],v[52:55]\n"
LD "v199,s[48:51],s88 offen offset:3072 lds\n"
MF "v[44:47],a[152:155],v[188:191],v[44:47]\n"
TR "v[180:181],v6 offset:4096\n"
MF "v[44:47],a[192:195],a[240:243],v[44:47]\n"
TR "v[182:183],v6 offset:6144\n"
MF "v[44:47],a[232:235],a[244:247],v[44:47]\n"
MF "v[36:39],a[228:231],a[244:247],v[36:39]\n"
TR "v[184:185],v6 offset:8192\n"
MF "v[36:39],a[188:191],a[240:243],v[36:39]\n"
"s_add_u32 m0,s79,57344\n"
TR "v[186:187],v6 offset:10240\n"
MF "v[36:39],a[148:151],v[188:191],v[36:39]\n"
LD "v200,s[48:51],s88 offen lds\n"
MF "v[28:31],a[144:147],v[188:191],v[28:31]\n"
MF "v[28:31],a[184:187],a[240:243],v[28:31]\n"
MF "v[28:31],a[224:227],a[244:247],v[28:31]\n"
MF "v[20:23],a[220:223],a[244:247],v[20:23]\n"
LD "v201,s[48:51],s88 offen offset:1024 lds\n"
MF "v[20:23],a[180:183],a[240:243],v[20:23]\n"
MF "v[20:23],a[140:143],v[188:191],v[20:23]\n"
"s_waitcnt lgkmcnt(0)\n"
MF "v[56:59],a[140:143],v[176:179],v[56:59]\n"
TR "v[188:189],v7\n"
MF "v[56:59],a[180:183],v[180:183],v[56:59]\n"
TR "v[190:191],v7 offset:2048\n"
MF "v[56:59],a[220:223],v[184:187],v[56:59]\n"
LD "v202,s[48:51],s88 offen offset:2048 lds\n"
MF "v[64:67],a[224:227],v[184:187],v[64:67]\n"
TR "a[240:241],v7 offset:4096\n"
"v_mov_b32 v244,0\n"
MF "v[64:67],a[184:187],v[180:183],v[64:67]\n"
TR "a[242:243],v7 offset:6144\n"
"v_mov_b32 v245,0\n"
MF "v[64:67],a[144:147],v[176:179],v[64:67]\n"
LD "v203,s[48:51],s88 offen offset:3072 lds\n"
MF "v[72:75],a[148:151],v[176:179],v[72:75]\n"
TR "a[244:245],v7 offset:8192\n"
"v_mov_b32 v246,0\n"
MF "v[72:75],a[188:191],v[180:183],v[72:75]\n"
TR "a[246:247],v7 offset:10240\n"
"v_mov_b32 v247,0\n"
MF "v[72:75],a[228:231],v[184:187],v[72:75]\n"
"v_mov_b32 v248,0\n"
"v_mov_b32 v249,0\n"
MF "v[80:83],a[232:235],v[184:187],v[80:83]\n"
"v_mov_b32 v250,0\n"
"v_mov_b32 v251,0\n"
MF "v[80:83],a[192:195],v[180:183],v[80:83]\n"
MF "v[80:83],a[152:155],v[176:179],v[80:83]\n"
MF "v[88:91],a[156:159],v[176:179],v[88:91]\n"
MF "v[88:91],a[196:199],v[180:183],v[88:91]\n"
"s_waitcnt vmcnt(24) lgkmcnt(0)\n"
MF "v[88:91],a[236:239],v[184:187],v[88:91]\n"
"s_barrier\n"
MF "v[92:95],a[236:239],a[244:247],v[92:95]\n"
"s_add_u32 s80,s88,122880\n"
TR "v[176:177],v0 offset:24576\n"
MF "v[92:95],a[196:199],a[240:243],v[92:95]\n"
"s_add_u32 m0,s79,98304\n"
"s_add_u32 s85,s85,1\n"
TR "v[178:179],v0 offset:26624\n"
MF "v[92:95],a[156:159],v[188:191],v[92:95]\n"
LD "v192,s[48:51],s80 offen lds\n"
MF "v[84:87],a[152:155],v[188:191],v[84:87]\n"
"s_nop 0\n"
TR "v[180:181],v0 offset:28672\n"
"ds_write_addtid_b32 v16 offset:24576\n"
MF "v[84:87],a[192:195],a[240:243],v[84:87]\n"
"s_nop 0\n"
TR "v[182:183],v0 offset:30720\n"
"ds_write_addtid_b32 v17 offset:24832\n"
MF "v[84:87],a[232:235],a[244:247],v[84:87]\n"
LD "v193,s[48:51],s80 offen offset:1024 lds\n"
MF "v[76:79],a[228:231],a[244:247],v[76:79]\n"
"s_nop 0\n"
TR "v[184:185],v0 offset:32768\n"
"ds_write_addtid_b32 v18 offset:25088\n"
MF "v[76:79],a[188:191],a[240:243],v[76:79]\n"
"s_nop 0\n"
TR "v[186:187],v0 offset:34816\n"
"ds_write_addtid_b32 v19 offset:25344\n"
MF "v[76:79],a[148:151],v[188:191],v[76:79]\n"
"s_nop 0\n"
"ds_write_addtid_b32 v20 offset:25600\n"
"ds_write_addtid_b32 v21 offset:25856\n"
MF "v[68:71],a[144:147],v[188:191],v[68:71]\n"
LD "v194,s[48:51],s80 offen offset:2048 lds\n"
MF "v[68:71],a[184:187],a[240:243],v[68:71]\n"
"s_nop 0\n"
"ds_write_addtid_b32 v22 offset:26112\n"
"ds_write_addtid_b32 v23 offset:26368\n"
MF "v[68:71],a[224:227],a[244:247],v[68:71]\n"
"s_nop 0\n"
"ds_write_addtid_b32 v24 offset:26624\n"
"ds_write_addtid_b32 v25 offset:26880\n"
MF "v[60:63],a[220:223],a[244:247],v[60:63]\n"
"s_nop 0\n"
"ds_write_addtid_b32 v26 offset:27136\n"
"ds_write_addtid_b32 v27 offset:27392\n"
MF "v[60:63],a[180:183],a[240:243],v[60:63]\n"
MF "v[60:63],a[140:143],v[188:191],v[60:63]\n"
"s_waitcnt lgkmcnt(9)\n"
"s_cmp_lg_u32 s85,5\n"
"s_cbranch_scc1 .Lpl05\n"
".Lpl09:\n"
MF "v[96:99],a[0:3],v[176:179],v[252:255]\n"
"s_nop 0\n"
TR "v[188:189],v1 offset:24576\n"
"ds_write_addtid_b32 v28 offset:27648\n"
MF "v[96:99],a[40:43],v[180:183],v[96:99]\n"
"s_nop 0\n"
TR "v[190:191],v1 offset:26624\n"
"ds_write_addtid_b32 v29 offset:27904\n"
MF "v[96:99],a[80:83],v[184:187],v[96:99]\n"
LD "v195,s[48:51],s80 offen offset:3072 lds\n"
MF "v[104:107],a[84:87],v[184:187],v[252:255]\n"
"s_add_u32 s60,s64,1\n"
TR "a[240:241],v1 offset:28672\n"
"ds_write_addtid_b32 v30 offset:28160\n"
MF "v[104:107],a[44:47],v[180:183],v[104:107]\n"
"s_add_u32 m0,s79,102400\n"
TR "a[242:243],v1 offset:30720\n"
"ds_write_addtid_b32 v31 offset:24320\n"
MF "v[104:107],a[4:7],v[176:179],v[104:107]\n"
LD "v196,s[48:51],s80 offen lds\n"
MF "v[112:115],a[8:11],v[176:179],v[252:255]\n"
"s_cmp_eq_u32 s60,8\n"
"s_cselect_b32 s60,0,s60\n"
TR "a[244:245],v1 offset:32768\n"
MF "v[112:115],a[48:51],v[180:183],v[112:115]\n"
"s_cmp_eq_u32 s60,10\n"
"s_cselect_b32 s60,s61,s60\n"
TR "a[246:247],v1 offset:34816\n"
MF "v[112:115],a[88:91],v[184:187],v[112:115]\n"
"s_sub_u32 s65,s60,s64\n"
"ds_write_addtid_b32 v32 offset:24576\n"
"ds_write_addtid_b32 v33 offset:24832\n"
MF "v[120:123],a[92:95],v[184:187],v[252:255]\n"
"s_lshl_b32 s65,s65,7\n"
"ds_write_addtid_b32 v34 offset:25088\n"
"ds_write_addtid_b32 v35 offset:25344\n"
MF "v[120:123],a[52:55],v[180:183],v[120:123]\n"
LD "v197,s[48:51],s80 offen offset:1024 lds\n"
MF "v[120:123],a[12:15],v[176:179],v[120:123]\n"
"s_mov_b32 s64,s60\n"
"ds_write_addtid_b32 v36 offset:25600\n"
"ds_write_addtid_b32 v37 offset:25856\n"
MF "v[128:131],a[16:19],v[176:179],v[252:255]\n"
"s_nop 0\n"
"ds_write_addtid_b32 v38 offset:26112\n"
"ds_write_addtid_b32 v39 offset:26368\n"
MF "v[128:131],a[56:59],v[180:183],v[128:131]\n"
"s_nop 0\n"
"ds_write_addtid_b32 v40 offset:26624\n"
"ds_write_addtid_b32 v41 offset:26880\n"
MF "v[128:131],a[96:99],v[184:187],v[128:131]\n"
"s_waitcnt lgkmcnt(10)\n"
LD "v198,s[48:51],s80 offen offset:2048 lds\n"
MF "v[132:135],a[96:99],a[244:247],v[252:255]\n"
"s_nop 0\n"
TR "v[176:177],v2 offset:24576\n"
"ds_write_addtid_b32 v42 offset:27136\n"
MF "v[132:135],a[56:59],a[240:243],v[132:135]\n"
"s_nop 0\n"
TR "v[178:179],v2 offset:26624\n"
"ds_write_addtid_b32 v43 offset:27392\n"
MF "v[132:135],a[16:19],v[188:191],v[132:135]\n"
"s_nop 0\n"
"ds_write_addtid_b32 v44 offset:27648\n"
"ds_write_addtid_b32 v45 offset:27904\n"
MF "v[124:127],a[12:15],v[188:191],v[252:255]\n"
"s_nop 0\n"
TR "v[180:181],v2 offset:28672\n"
"ds_write_addtid_b32 v46 offset:28160\n"
MF "v[124:127],a[52:55],a[240:243],v[124:127]\n"
"s_nop 0\n"
TR "v[182:183],v2 offset:30720\n"
"ds_write_addtid_b32 v47 offset:28416\n"
MF "v[124:127],a[92:95],a[244:247],v[124:127]\n"
LD "v199,s[48:51],s80 offen offset:3072 lds\n"
MF "v[116:119],a[88:91],a[244:247],v[252:255]\n"
"s_nop 0\n"
TR "v[184:185],v2 offset:32768\n"
"ds_write_addtid_b32 v48 offset:28672\n"
MF "v[116:119],a[48:51],a[240:243],v[116:119]\n"
"s_add_u32 m0,s79,106496\n"
TR "v[186:187],v2 offset:34816\n"
"ds_write_addtid_b32 v49 offset:24832\n"
MF "v[116:119],a[8:11],v[188:191],v[116:119]\n"
LD "v200,s[48:51],s80 offen lds\n"
MF "v[108:111],a[4:7],v[188:191],v[252:255]\n"
"s_nop 0\n"
"ds_write_addtid_b32 v50 offset:25088\n"
"ds_write_addtid_b32 v51 offset:25344\n"
MF "v[108:111],a[44:47],a[240:243],v[108:111]\n"
"s_nop 0\n"
"ds_write_addtid_b32 v52 offset:25600\n"
"ds_write_addtid_b32 v53 offset:25856\n"
MF "v[108:111],a[84:87],a[244:247],v[108:111]\n"
"s_nop 0\n"
"ds_write_addtid_b32 v54 offset:26112\n"
"ds_write_addtid_b32 v55 offset:26368\n"
MF "v[100:103],a[80:83],a[244:247],v[252:255]\n"
MF "v[100:103],a[40:43],a[240:243],v[100:103]\n"
LD "v201,s[48:51],s80 offen offset:1024 lds\n"
MF "v[100:103],a[0:3],v[188:191],v[100:103]\n"
"s_waitcnt lgkmcnt(7)\n"
MF "v[136:139],a[0:3],v[176:179],v[252:255]\n"
TR "v[188:189],v3 offset:24576\n"
MF "v[136:139],a[40:43],v[180:183],v[136:139]\n"
TR "v[190:191],v3 offset:26624\n"
MF "v[136:139],a[80:83],v[184:187],v[136:139]\n"
LD "v202,s[48:51],s80 offen offset:2048 lds\n"
MF "v[144:147],a[84:87],v[184:187],v[252:255]\n"
TR "a[240:241],v3 offset:28672\n"
MF "v[144:147],a[44:47],v[180:183],v[144:147]\n"
TR "a[242:243],v3 offset:30720\n"
MF "v[144:147],a[4:7],v[176:179],v[144:147]\n"
LD "v203,s[48:51],s80 offen offset:3072 lds\n"
MF "v[152:155],a[8:11],v[176:179],v[252:255]\n"
TR "a[244:245],v3 offset:32768\n"
MF "v[152:155],a[48:51],v[180:183],v[152:155]\n"
TR "a[246:247],v3 offset:34816\n"
MF "v[152:155],a[88:91],v[184:187],v[152:155]\n"
MF "v[160:163],a[92:95],v[184:187],v[252:255]\n"
MF "v[160:163],a[52:55],v[180:183],v[160:163]\n"
MF "v[160:163],a[12:15],v[176:179],v[160:163]\n"
MF "v[168:171],a[16:19],v[176:179],v[252:255]\n"
MF "v[168:171],a[56:59],v[180:183],v[168:171]\n"
"s_waitcnt vmcnt(24) lgkmcnt(0)\n"
MF "v[168:171],a[96:99],v[184:187],v[168:171]\n"
"s_barrier\n"
MF "v[172:175],a[96:99],a[244:247],v[252:255]\n"
"s_add_u32 s89,s89,s65\n"
TR "v[176:177],v0\n"
MF "v[172:175],a[56:59],a[240:243],v[172:175]\n"
"s_add_u32 m0,s79,24576\n"
TR "v[178:179],v0 offset:2048\n"
MF "v[172:175],a[16:19],v[188:191],v[172:175]\n"
LD "v192,s[48:51],s89 offen lds\n"
MF "v[164:167],a[12:15],v[188:191],v[252:255]\n"
TR "v[180:181],v0 offset:4096\n"
MF "v[164:167],a[52:55],a[240:243],v[164:167]\n"
TR "v[182:183],v0 offset:6144\n"
MF "v[164:167],a[92:95],a[244:247],v[164:167]\n"
LD "v193,s[48:51],s89 offen offset:1024 lds\n"
MF "v[156:159],a[88:91],a[244:247],v[252:255]\n"
TR "v[184:185],v0 offset:8192\n"
MF "v[156:159],a[48:51],a[240:243],v[156:159]\n"
"s_nop 0\n"
TR "v[186:187],v0 offset:10240\n"
"ds_write_addtid_b32 v56 offset:49152\n"
MF "v[156:159],a[8:11],v[188:191],v[156:159]\n"
"s_nop 0\n"
"ds_write_addtid_b32 v57 offset:49408\n"
"ds_write_addtid_b32 v58 offset:49664\n"
MF "v[148:151],a[4:7],v[188:191],v[252:255]\n"
LD "v194,s[48:51],s89 offen offset:2048 lds\n"
MF "v[148:151],a[44:47],a[240:243],v[148:151]\n"
"buffer_load_dword v10,v12,s[40:43],s86 offen sc1\n"
MF "v[148:151],a[84:87],a[244:247],v[148:151]\n"
"s_nop 0\n"
"ds_write_addtid_b32 v59 offset:49920\n"
"ds_write_addtid_b32 v60 offset:50176\n"
MF "v[140:143],a[80:83],a[244:247],v[252:255]\n"
"s_nop 0\n"
"ds_write_addtid_b32 v61 offset:50432\n"
"ds_write_addtid_b32 v62 offset:50688\n"
MF "v[140:143],a[40:43],a[240:243],v[140:143]\n"
"s_nop 0\n"
"ds_write_addtid_b32 v63 offset:50944\n"
"ds_write_addtid_b32 v64 offset:51200\n"
MF "v[140:143],a[0:3],v[188:191],v[140:143]\n"
"s_waitcnt lgkmcnt(9)\n"
LD "v195,s[48:51],s89 offen offset:3072 lds\n"
MF "v[96:99],a[120:123],v[176:179],v[96:99]\n"
"s_nop 0\n"
TR "v[188:189],v1\n"
"ds_write_addtid_b32 v65 offset:51456\n"
MF "v[96:99],a[160:163],v[180:183],v[96:99]\n"
"s_nop 0\n"
TR "v[190:191],v1 offset:2048\n"
"ds_write_addtid_b32 v66 offset:51712\n"
MF "v[96:99],a[200:203],v[184:187],v[96:99]\n"
".Lpg28h:\n"
"s_cbranch_execz .Lpgrole1_h\n"
"s_nop 0\n"
"ds_write_addtid_b32 v67 offset:51968\n"
"ds_write_addtid_b32 v68 offset:52224\n"
MF "v[104:107],a[204:207],v[184:187],v[104:107]\n"
"s_nop 0\n"
TR "a[240:241],v1 offset:4096\n"
"ds_write_addtid_b32 v69 offset:52480\n"
MF "v[104:107],a[164:167],v[180:183],v[104:107]\n"
"s_add_u32 m0,s79,28672\n"
TR "a[242:243],v1 offset:6144\n"
"ds_write_addtid_b32 v70 offset:48640\n"
MF "v[104:107],a[124:127],v[176:179],v[104:107]\n"
LD "v196,s[48:51],s89 offen lds\n"
MF "v[112:115],a[128:131],v[176:179],v[112:115]\n"
"s_nop 0\n"
TR "a[244:245],v1 offset:8192\n"
"ds_write_addtid_b32 v71 offset:48896\n"
MF "v[112:115],a[168:171],v[180:183],v[112:115]\n"
"s_nop 0\n"
TR "a[246:247],v1 offset:10240\n"
"ds_write_addtid_b32 v72 offset:49152\n"
MF "v[112:115],a[208:211],v[184:187],v[112:115]\n"
LD "v197,s[48:51],s89 offen offset:1024 lds\n"
MF "v[120:123],a[212:215],v[184:187],v[120:123]\n"
"s_nop 0\n"
"ds_write_addtid_b32 v73 offset:49408\n"
"ds_write_addtid_b32 v74 offset:49664\n"
MF "v[120:123],a[172:175],v[180:183],v[120:123]\n"
"s_nop 0\n"
"ds_write_addtid_b32 v75 offset:49920\n"
"ds_write_addtid_b32 v76 offset:50176\n"
MF "v[120:123],a[132:135],v[176:179],v[120:123]\n"
"s_nop 0\n"
"ds_write_addtid_b32 v77 offset:50432\n"
"ds_write_addtid_b32 v78 offset:50688\n"
MF "v[128:131],a[136:139],v[176:179],v[128:131]\n"
"s_nop 0\n"
"ds_write_addtid_b32 v79 offset:50944\n"
"ds_write_addtid_b32 v80 offset:51200\n"
MF "v[128:131],a[176:179],v[180:183],v[128:131]\n"
LD "v198,s[48:51],s89 offen offset:2048 lds\n"
MF "v[128:131],a[216:219],v[184:187],v[128:131]\n"
"s_waitcnt lgkmcnt(9)\n"
"ds_write_addtid_b32 v81 offset:51456\n"
"ds_write_addtid_b32 v82 offset:51712\n"
MF "v[132:135],a[216:219],a[244:247],v[132:135]\n"
"s_nop 0\n"
TR "v[176:177],v2\n"
"ds_write_addtid_b32 v83 offset:51968\n"
MF "v[132:135],a[176:179],a[240:243],v[132:135]\n"
"s_nop 0\n"
TR "v[178:179],v2 offset:2048\n"
"ds_write_addtid_b32 v84 offset:52224\n"
MF "v[132:135],a[136:139],v[188:191],v[132:135]\n"
LD "v199,s[48:51],s89 offen offset:3072 lds\n"
MF "v[124:127],a[132:135],v[188:191],v[124:127]\n"
"s_nop 0\n"
TR "v[180:181],v2 offset:4096\n"
"ds_write_addtid_b32 v85 offset:52480\n"
MF "v[124:127],a[172:175],a[240:243],v[124:127]\n"
"s_nop 0\n"
TR "v[182:183],v2 offset:6144\n"
"ds_write_addtid_b32 v86 offset:52736\n"
MF "v[124:127],a[212:215],a[244:247],v[124:127]\n"
"s_nop 0\n"
"ds_write_addtid_b32 v87 offset:52992\n"
"ds_write_addtid_b32 v88 offset:53248\n"
MF "v[116:119],a[208:211],a[244:247],v[116:119]\n"
"s_nop 0\n"
TR "v[184:185],v2 offset:8192\n"
"ds_write_addtid_b32 v89 offset:53504\n"
MF "v[116:119],a[168:171],a[240:243],v[116:119]\n"
"s_add_u32 m0,s79,32768\n"
TR "v[186:187],v2 offset:10240\n"
"ds_write_addtid_b32 v90 offset:49664\n"
MF "v[116:119],a[128:131],v[188:191],v[116:119]\n"
LD "v200,s[48:51],s89 offen lds\n"
MF "v[108:111],a[124:127],v[188:191],v[108:111]\n"
"s_nop 0\n"
"ds_write_addtid_b32 v91 offset:49920\n"
"ds_write_addtid_b32 v92 offset:50176\n"
MF "v[108:111],a[164:167],a[240:243],v[108:111]\n"
"s_nop 0\n"
"ds_write_addtid_b32 v93 offset:50432\n"
"ds_write_addtid_b32 v94 offset:50688\n"
MF "v[108:111],a[204:207],a[244:247],v[108:111]\n"
"ds_write_addtid_b32 v95 offset:50944\n"
MF "v[100:103],a[200:203],a[244:247],v[100:103]\n"
LD "v201,s[48:51],s89 offen offset:1024 lds\n"
MF "v[100:103],a[160:163],a[240:243],v[100:103]\n"
MF "v[100:103],a[120:123],v[188:191],v[100:103]\n"
"s_waitcnt lgkmcnt(6)\n"
MF "v[136:139],a[120:123],v[176:179],v[136:139]\n"
TR "v[188:189],v3\n"
MF "v[136:139],a[160:163],v[180:183],v[136:139]\n"
TR "v[190:191],v3 offset:2048\n"
MF "v[136:139],a[200:203],v[184:187],v[136:139]\n"
LD "v202,s[48:51],s89 offen offset:2048 lds\n"
MF "v[144:147],a[204:207],v[184:187],v[144:147]\n"
TR "a[240:241],v3 offset:4096\n"
MF "v[144:147],a[164:167],v[180:183],v[144:147]\n"
TR "a[242:243],v3 offset:6144\n"
MF "v[144:147],a[124:127],v[176:179],v[144:147]\n"
LD "v203,s[48:51],s89 offen offset:3072 lds\n"
MF "v[152:155],a[128:131],v[176:179],v[152:155]\n"
TR "a[244:245],v3 offset:8192\n"
MF "v[152:155],a[168:171],v[180:183],v[152:155]\n"
TR "a[246:247],v3 offset:10240\n"
MF "v[152:155],a[208:211],v[184:187],v[152:155]\n"
MF "v[160:163],a[212:215],v[184:187],v[160:163]\n"
MF "v[160:163],a[172:175],v[180:183],v[160:163]\n"
MF "v[160:163],a[132:135],v[176:179],v[160:163]\n"
MF "v[168:171],a[136:139],v[176:179],v[168:171]\n"
MF "v[168:171],a[176:179],v[180:183],v[168:171]\n"
"s_waitcnt vmcnt(25) lgkmcnt(0)\n"
MF "v[168:171],a[216:219],v[184:187],v[168:171]\n"
"s_barrier\n"
MF "v[172:175],a[216:219],a[244:247],v[172:175]\n"
"s_add_u32 s80,s89,122880\n"
TR "v[176:177],v0 offset:49152\n"
MF "v[172:175],a[176:179],a[240:243],v[172:175]\n"
"s_add_u32 m0,s79,0\n"
"s_mov_b32 s82,s70\n"
TR "v[178:179],v0 offset:51200\n"
MF "v[172:175],a[136:139],v[188:191],v[172:175]\n"
LD "v192,s[48:51],s80 offen lds\n"
MF "v[164:167],a[132:135],v[188:191],v[164:167]\n"
TR "v[180:181],v0 offset:53248\n"
MF "v[164:167],a[172:175],a[240:243],v[164:167]\n"
TR "v[182:183],v0 offset:55296\n"
MF "v[164:167],a[212:215],a[244:247],v[164:167]\n"
LD "v193,s[48:51],s80 offen offset:1024 lds\n"
MF "v[156:159],a[208:211],a[244:247],v[156:159]\n"
TR "v[184:185],v0 offset:57344\n"
MF "v[156:159],a[168:171],a[240:243],v[156:159]\n"
TR "v[186:187],v0 offset:59392\n"
MF "v[156:159],a[128:131],v[188:191],v[156:159]\n"
MF "v[148:151],a[124:127],v[188:191],v[148:151]\n"
MF "v[148:151],a[164:167],a[240:243],v[148:151]\n"
LD "v194,s[48:51],s80 offen offset:2048 lds\n"
MF "v[148:151],a[204:207],a[244:247],v[148:151]\n"
MF "v[140:143],a[200:203],a[244:247],v[140:143]\n"
MF "v[140:143],a[160:163],a[240:243],v[140:143]\n"
MF "v[140:143],a[120:123],v[188:191],v[140:143]\n"
"s_waitcnt lgkmcnt(0)\n"
LD "v195,s[48:51],s80 offen offset:3072 lds\n"
MF "v[96:99],a[20:23],v[176:179],v[96:99]\n"
TR "v[188:189],v1 offset:49152\n"
MF "v[96:99],a[60:63],v[180:183],v[96:99]\n"
TR "v[190:191],v1 offset:51200\n"
MF "v[96:99],a[100:103],v[184:187],v[96:99]\n"
MF "v[104:107],a[104:107],v[184:187],v[104:107]\n"
TR "a[240:241],v1 offset:53248\n"
MF "v[104:107],a[64:67],v[180:183],v[104:107]\n"
"s_add_u32 m0,s79,4096\n"
TR "a[242:243],v1 offset:55296\n"
MF "v[104:107],a[24:27],v[176:179],v[104:107]\n"
LD "v196,s[48:51],s80 offen lds\n"
MF "v[112:115],a[28:31],v[176:179],v[112:115]\n"
TR "a[244:245],v1 offset:57344\n"
MF "v[112:115],a[68:71],v[180:183],v[112:115]\n"
TR "a[246:247],v1 offset:59392\n"
MF "v[112:115],a[108:111],v[184:187],v[112:115]\n"
LD "v197,s[48:51],s80 offen offset:1024 lds\n"
MF "v[120:123],a[112:115],v[184:187],v[120:123]\n"
MF "v[120:123],a[72:75],v[180:183],v[120:123]\n"
MF "v[120:123],a[32:35],v[176:179],v[120:123]\n"
MF "v[128:131],a[36:39],v[176:179],v[128:131]\n"
MF "v[128:131],a[76:79],v[180:183],v[128:131]\n"
LD "v198,s[48:51],s80 offen offset:2048 lds\n"
MF "v[128:131],a[116:119],v[184:187],v[128:131]\n"
"s_waitcnt lgkmcnt(0)\n"
MF "v[132:135],a[116:119],a[244:247],v[132:135]\n"
TR "v[176:177],v2 offset:49152\n"
MF "v[132:135],a[76:79],a[240:243],v[132:135]\n"
TR "v[178:179],v2 offset:51200\n"
MF "v[132:135],a[36:39],v[188:191],v[132:135]\n"
LD "v199,s[48:51],s80 offen offset:3072 lds\n"
MF "v[124:127],a[32:35],v[188:191],v[124:127]\n"
TR "v[180:181],v2 offset:53248\n"
MF "v[124:127],a[72:75],a[240:243],v[124:127]\n"
TR "v[182:183],v2 offset:55296\n"
MF "v[124:127],a[112:115],a[244:247],v[124:127]\n"
MF "v[116:119],a[108:111],a[244:247],v[116:119]\n"
TR "v[184:185],v2 offset:57344\n"
MF "v[116:119],a[68:71],a[240:243],v[116:119]\n"
"s_add_u32 m0,s79,8192\n"
TR "v[186:187],v2 offset:59392\n"
MF "v[116:119],a[28:31],v[188:191],v[116:119]\n"
LD "v200,s[48:51],s80 offen lds\n"
MF "v[108:111],a[24:27],v[188:191],v[108:111]\n"
MF "v[108:111],a[64:67],a[240:243],v[108:111]\n"
MF "v[108:111],a[104:107],a[244:247],v[108:111]\n"
MF "v[100:103],a[100:103],a[244:247],v[100:103]\n"
LD "v201,s[48:51],s80 offen offset:1024 lds\n"
MF "v[100:103],a[60:63],a[240:243],v[100:103]\n"
"s_waitcnt vmcnt(19)\n"
"s_cmp_lt_u32 s85,6\n"
MF "v[100:103],a[20:23],v[188:191],v[100:103]\n"
"s_cbranch_scc1 .Ljs4\n"
"v_mov_b32 v236,v228\n"
"v_mov_b32 v237,v229\n"
MF "v[96:99],a[252:255],v[208:211],v[96:99]\n"
MF "v[104:107],a[252:255],v[212:215],v[104:107]\n"
MF "v[112:115],a[252:255],v[216:219],v[112:115]\n"
MF "v[120:123],a[252:255],v[220:223],v[120:123]\n"
MF "v[128:131],a[252:255],v[224:227],v[128:131]\n"
MF "v[100:103],a[248:251],v[208:211],v[100:103]\n"
MF "v[108:111],a[248:251],v[212:215],v[108:111]\n"
MF "v[116:119],a[248:251],v[216:219],v[116:119]\n"
MF "v[124:127],a[248:251],v[220:223],v[124:127]\n"
MF "v[132:135],a[248:251],v[224:227],v[132:135]\n"
MF "v[248:251],v[230:233],a[250:253],v[248:251]\n"
MF "v[244:247],v[234:237],a[250:253],v[244:247]\n"
".Ljs4:\n"
"s_waitcnt lgkmcnt(0)\n"
MF "v[136:139],a[20:23],v[176:179],v[136:139]\n"
TR "v[188:189],v3 offset:49152\n"
"v_sub_u32 v10,v10,v11\n"
MF "v[136:139],a[60:63],v[180:183],v[136:139]\n"
TR "v[190:191],v3 offset:51200\n"
MF "v[136:139],a[100:103],v[184:187],v[136:139]\n"
LD "v202,s[48:51],s80 offen offset:2048 lds\n"
MF "v[144:147],a[104:107],v[184:187],v[144:147]\n"
TR "a[240:241],v3 offset:53248\n"
MF "v[144:147],a[64:67],v[180:183],v[144:147]\n"
TR "a[242:243],v3 offset:55296\n"
MF "v[144:147],a[24:27],v[176:179],v[144:147]\n"
LD "v203,s[48:51],s80 offen offset:3072 lds\n"
MF "v[152:155],a[28:31],v[176:179],v[152:155]\n"
TR "a[244:245],v3 offset:57344\n"
MF "v[152:155],a[68:71],v[180:183],v[152:155]\n"
TR "a[246:247],v3 offset:59392\n"
MF "v[152:155],a[108:111],v[184:187],v[152:155]\n"
".Lnf0:\n"
"v_readlane_b32 s58,v10,s67\n"
"s_cmp_lt_u32 s58,s85\n"
"s_cbranch_scc1 .Lsp0\n"
".Lnb0:\n"
MF "v[160:163],a[112:115],v[184:187],v[160:163]\n"
"s_add_u32 s70,s70,20480\n"
".Lnf1:\n"
"v_cmp_gt_u32 vcc,4,v10\n"
"s_cbranch_vccnz .Lsp1\n"
".Lnb1:\n"
MF "v[160:163],a[72:75],v[180:183],v[160:163]\n"
MF "v[160:163],a[32:35],v[176:179],v[160:163]\n"
MF "v[168:171],a[36:39],v[176:179],v[168:171]\n"
MF "v[168:171],a[76:79],v[180:183],v[168:171]\n"
"s_waitcnt vmcnt(25) lgkmcnt(0)\n"
MF "v[168:171],a[116:119],v[184:187],v[168:171]\n"
"s_barrier\n"
MF "v[172:175],a[116:119],a[244:247],v[172:175]\n"
"s_add_u32 s88,s88,s65\n"
TR "v[176:177],v4\n"
MF "v[172:175],a[76:79],a[240:243],v[172:175]\n"
"s_add_u32 m0,s79,49152\n"
TR "v[178:179],v4 offset:2048\n"
MF "v[172:175],a[36:39],v[188:191],v[172:175]\n"
LD "v192,s[48:51],s88 offen lds\n"
MF "v[164:167],a[32:35],v[188:191],v[164:167]\n"
TR "v[180:181],v4 offset:4096\n"
MF "v[164:167],a[72:75],a[240:243],v[164:167]\n"
TR "v[182:183],v4 offset:6144\n"
MF "v[164:167],a[112:115],a[244:247],v[164:167]\n"
LD "v193,s[48:51],s88 offen offset:1024 lds\n"
MF "v[156:159],a[108:111],a[244:247],v[156:159]\n"
TR "v[184:185],v4 offset:8192\n"
MF "v[156:159],a[68:71],a[240:243],v[156:159]\n"
"s_add_u32 s2,s2,204800\n"
TR "v[186:187],v4 offset:10240\n"
MF "v[156:159],a[28:31],v[188:191],v[156:159]\n"
LD "v[228:231],v9,s[40:43],s2 offen sc1\n"
MF "v[148:151],a[24:27],v[188:191],v[148:151]\n"
MF "v[148:151],a[64:67],a[240:243],v[148:151]\n"
LD "v194,s[48:51],s88 offen offset:2048 lds\n"
MF "v[148:151],a[104:107],a[244:247],v[148:151]\n"
MF "v[140:143],a[100:103],a[244:247],v[140:143]\n"
MF "v[140:143],a[60:63],a[240:243],v[140:143]\n"
MF "v[140:143],a[20:23],v[188:191],v[140:143]\n"
"s_waitcnt lgkmcnt(0)\n"
LD "v195,s[48:51],s88 offen offset:3072 lds\n"
MF "v[96:99],a[140:143],v[176:179],v[96:99]\n"
TR "v[188:189],v5\n"
MF "v[96:99],a[180:183],v[180:183],v[96:99]\n"
"s_add_u32 s2,s2,204800\n"
TR "v[190:191],v5 offset:2048\n"
MF "v[96:99],a[220:223],v[184:187],v[96:99]\n"
LD "v[232:235],v9,s[40:43],s2 offen sc1\n"
MF "v[104:107],a[224:227],v[184:187],v[104:107]\n"
TR "a[240:241],v5 offset:4096\n"
MF "v[104:107],a[184:187],v[180:183],v[104:107]\n"
"s_add_u32 m0,s79,53248\n"
TR "a[242:243],v5 offset:6144\n"
MF "v[104:107],a[144:147],v[176:179],v[104:107]\n"
LD "v196,s[48:51],s88 offen lds\n"
MF "v[112:115],a[148:151],v[176:179],v[112:115]\n"
TR "a[244:245],v5 offset:8192\n"
MF "v[112:115],a[188:191],v[180:183],v[112:115]\n"
TR "a[246:247],v5 offset:10240\n"
MF "v[112:115],a[228:231],v[184:187],v[112:115]\n"
LD "v197,s[48:51],s88 offen offset:1024 lds\n"
MF "v[120:123],a[232:235],v[184:187],v[120:123]\n"
MF "v[120:123],a[192:195],v[180:183],v[120:123]\n"
MF "v[120:123],a[152:155],v[176:179],v[120:123]\n"
LD "v[208:211],v9,s[40:43],s82 offen sc1\n"
MF "v[128:131],a[156:159],v[176:179],v[128:131]\n"
MF "v[128:131],a[196:199],v[180:183],v[128:131]\n"
LD "v198,s[48:51],s88 offen offset:2048 lds\n"
MF "v[128:131],a[236:239],v[184:187],v[128:131]\n"
"s_waitcnt lgkmcnt(0)\n"
MF "v[132:135],a[236:239],a[244:247],v[132:135]\n"
TR "v[176:177],v6\n"
MF "v[132:135],a[196:199],a[240:243],v[132:135]\n"
TR "v[178:179],v6 offset:2048\n"
MF "v[132:135],a[156:159],v[188:191],v[132:135]\n"
LD "v199,s[48:51],s88 offen offset:3072 lds\n"
MF "v[124:127],a[152:155],v[188:191],v[124:127]\n"
TR "v[180:181],v6 offset:4096\n"
MF "v[124:127],a[192:195],a[240:243],v[124:127]\n"
TR "v[182:183],v6 offset:6144\n"
MF "v[124:127],a[232:235],a[244:247],v[124:127]\n"
LD "v[212:215],v9,s[40:43],s82 offen offset:1024 sc1\n"
MF "v[116:119],a[228:231],a[244:247],v[116:119]\n"
TR "v[184:185],v6 offset:8192\n"
MF "v[116:119],a[188:191],a[240:243],v[116:119]\n"
"s_add_u32 m0,s79,57344\n"
TR "v[186:187],v6 offset:10240\n"
MF "v[116:119],a[148:151],v[188:191],v[116:119]\n"
LD "v200,s[48:51],s88 offen lds\n"
MF "v[108:111],a[144:147],v[188:191],v[108:111]\n"
MF "v[108:111],a[184:187],a[240:243],v[108:111]\n"
MF "v[108:111],a[224:227],a[244:247],v[108:111]\n"
MF "v[100:103],a[220:223],a[244:247],v[100:103]\n"
LD "v201,s[48:51],s88 offen offset:1024 lds\n"
MF "v[100:103],a[180:183],a[240:243],v[100:103]\n"
MF "v[100:103],a[140:143],v[188:191],v[100:103]\n"
"s_waitcnt lgkmcnt(0)\n"
LD "v[216:219],v9,s[40:43],s82 offen offset:2048 sc1\n"
MF "v[136:139],a[140:143],v[176:179],v[136:139]\n"
TR "v[188:189],v7\n"
MF "v[136:139],a[180:183],v[180:183],v[136:139]\n"
TR "v[190:191],v7 offset:2048\n"
MF "v[136:139],a[220:223],v[184:187],v[136:139]\n"
LD "v202,s[48:51],s88 offen offset:2048 lds\n"
MF "v[144:147],a[224:227],v[184:187],v[144:147]\n"
TR "a[240:241],v7 offset:4096\n"
MF "v[144:147],a[184:187],v[180:183],v[144:147]\n"
TR "a[242:243],v7 offset:6144\n"
MF "v[144:147],a[144:147],v[176:179],v[144:147]\n"
LD "v203,s[48:51],s88 offen offset:3072 lds\n"
MF "v[152:155],a[148:151],v[176:179],v[152:155]\n"
TR "a[244:245],v7 offset:8192\n"
MF "v[152:155],a[188:191],v[180:183],v[152:155]\n"
TR "a[246:247],v7 offset:10240\n"
MF "v[152:155],a[228:231],v[184:187],v[152:155]\n"
LD "v[220:223],v9,s[40:43],s82 offen offset:3072 sc1\n"
MF "v[160:163],a[232:235],v[184:187],v[160:163]\n"
MF "v[160:163],a[192:195],v[180:183],v[160:163]\n"
MF "v[160:163],a[152:155],v[176:179],v[160:163]\n"
MF "v[168:171],a[156:159],v[176:179],v[168:171]\n"
"s_add_u32 s82,s82,4096\n"
MF "v[168:171],a[196:199],v[180:183],v[168:171]\n"
"s_waitcnt vmcnt(30) lgkmcnt(0)\n"
LD "v[224:227],v9,s[40:43],s82 offen sc1\n"
MF "v[168:171],a[236:239],v[184:187],v[168:171]\n"
"s_barrier\n"
MF "v[172:175],a[236:239],a[244:247],v[172:175]\n"
"s_add_u32 s80,s88,122880\n"
TR "v[176:177],v0 offset:24576\n"
MF "v[172:175],a[196:199],a[240:243],v[172:175]\n"
"s_add_u32 m0,s79,98304\n"
"s_add_u32 s85,s85,1\n"
TR "v[178:179],v0 offset:26624\n"
MF "v[172:175],a[156:159],v[188:191],v[172:175]\n"
LD "v192,s[48:51],s80 offen lds\n"
MF "v[164:167],a[152:155],v[188:191],v[164:167]\n"
"s_nop 0\n"
TR "v[180:181],v0 offset:28672\n"
"ds_write_addtid_b32 v96 offset:24576\n"
MF "v[164:167],a[192:195],a[240:243],v[164:167]\n"
"s_nop 0\n"
TR "v[182:183],v0 offset:30720\n"
"ds_write_addtid_b32 v97 offset:24832\n"
MF "v[164:167],a[232:235],a[244:247],v[164:167]\n"
LD "v193,s[48:51],s80 offen offset:1024 lds\n"
MF "v[156:159],a[228:231],a[244:247],v[156:159]\n"
"s_nop 0\n"
TR "v[184:185],v0 offset:32768\n"
"ds_write_addtid_b32 v98 offset:25088\n"
MF "v[156:159],a[188:191],a[240:243],v[156:159]\n"
"s_nop 0\n"
TR "v[186:187],v0 offset:34816\n"
"ds_write_addtid_b32 v99 offset:25344\n"
MF "v[156:159],a[148:151],v[188:191],v[156:159]\n"
"s_nop 0\n"
"ds_write_addtid_b32 v100 offset:25600\n"
"ds_write_addtid_b32 v101 offset:25856\n"
MF "v[148:151],a[144:147],v[188:191],v[148:151]\n"
LD "v194,s[48:51],s80 offen offset:2048 lds\n"
MF "v[148:151],a[184:187],a[240:243],v[148:151]\n"
"s_nop 0\n"
"ds_write_addtid_b32 v102 offset:26112\n"
"ds_write_addtid_b32 v103 offset:26368\n"
MF "v[148:151],a[224:227],a[244:247],v[148:151]\n"
"s_nop 0\n"
"ds_write_addtid_b32 v104 offset:26624\n"
"ds_write_addtid_b32 v105 offset:26880\n"
MF "v[140:143],a[220:223],a[244:247],v[140:143]\n"
"s_nop 0\n"
"ds_write_addtid_b32 v106 offset:27136\n"
"ds_write_addtid_b32 v107 offset:27392\n"
MF "v[140:143],a[180:183],a[240:243],v[140:143]\n"
MF "v[140:143],a[140:143],v[188:191],v[140:143]\n"
"s_waitcnt lgkmcnt(9)\n"
MF "v[16:19],a[0:3],v[176:179],v[252:255]\n"
"s_nop 0\n"
TR "v[188:189],v1 offset:24576\n"
"ds_write_addtid_b32 v108 offset:27648\n"
MF "v[16:19],a[40:43],v[180:183],v[16:19]\n"
"s_nop 0\n"
TR "v[190:191],v1 offset:26624\n"
"ds_write_addtid_b32 v109 offset:27904\n"
MF "v[16:19],a[80:83],v[184:187],v[16:19]\n"
LD "v195,s[48:51],s80 offen offset:3072 lds\n"
MF "v[24:27],a[84:87],v[184:187],v[252:255]\n"
"s_add_u32 s60,s64,1\n"
TR "a[240:241],v1 offset:28672\n"
"ds_write_addtid_b32 v110 offset:28160\n"
MF "v[24:27],a[44:47],v[180:183],v[24:27]\n"
"s_add_u32 m0,s79,102400\n"
TR "a[242:243],v1 offset:30720\n"
"ds_write_addtid_b32 v111 offset:24320\n"
MF "v[24:27],a[4:7],v[176:179],v[24:27]\n"
LD "v196,s[48:51],s80 offen lds\n"
MF "v[32:35],a[8:11],v[176:179],v[252:255]\n"
"s_cmp_eq_u32 s60,8\n"
"s_cselect_b32 s60,0,s60\n"
TR "a[244:245],v1 offset:32768\n"
MF "v[32:35],a[48:51],v[180:183],v[32:35]\n"
"s_cmp_eq_u32 s60,10\n"
"s_cselect_b32 s60,s61,s60\n"
TR "a[246:247],v1 offset:34816\n"
MF "v[32:35],a[88:91],v[184:187],v[32:35]\n"
"s_sub_u32 s65,s60,s64\n"
"ds_write_addtid_b32 v112 offset:24576\n"
"ds_write_addtid_b32 v113 offset:24832\n"
MF "v[40:43],a[92:95],v[184:187],v[252:255]\n"
"s_lshl_b32 s65,s65,7\n"
"ds_write_addtid_b32 v114 offset:25088\n"
"ds_write_addtid_b32 v115 offset:25344\n"
MF "v[40:43],a[52:55],v[180:183],v[40:43]\n"
LD "v197,s[48:51],s80 offen offset:1024 lds\n"
MF "v[40:43],a[12:15],v[176:179],v[40:43]\n"
"s_mov_b32 s64,s60\n"
"ds_write_addtid_b32 v116 offset:25600\n"
"ds_write_addtid_b32 v117 offset:25856\n"
MF "v[48:51],a[16:19],v[176:179],v[252:255]\n"
"s_nop 0\n"
"ds_write_addtid_b32 v118 offset:26112\n"
"ds_write_addtid_b32 v119 offset:26368\n"
MF "v[48:51],a[56:59],v[180:183],v[48:51]\n"
"s_nop 0\n"
"ds_write_addtid_b32 v120 offset:26624\n"
"ds_write_addtid_b32 v121 offset:26880\n"
MF "v[48:51],a[96:99],v[184:187],v[48:51]\n"
"s_waitcnt lgkmcnt(10)\n"
LD "v198,s[48:51],s80 offen offset:2048 lds\n"
MF "v[52:55],a[96:99],a[244:247],v[252:255]\n"
"s_nop 0\n"
TR "v[176:177],v2 offset:24576\n"
"ds_write_addtid_b32 v122 offset:27136\n"
MF "v[52:55],a[56:59],a[240:243],v[52:55]\n"
"s_nop 0\n"
TR "v[178:179],v2 offset:26624\n"
"ds_write_addtid_b32 v123 offset:27392\n"
MF "v[52:55],a[16:19],v[188:191],v[52:55]\n"
"s_nop 0\n"
"ds_write_addtid_b32 v124 offset:27648\n"
"ds_write_addtid_b32 v125 offset:27904\n"
MF "v[44:47],a[12:15],v[188:191],v[252:255]\n"
"s_nop 0\n"
TR "v[180:181],v2 offset:28672\n"
"ds_write_addtid_b32 v126 offset:28160\n"
MF "v[44:47],a[52:55],a[240:243],v[44:47]\n"
"s_nop 0\n"
TR "v[182:183],v2 offset:30720\n"
"ds_write_addtid_b32 v127 offset:28416\n"
MF "v[44:47],a[92:95],a[244:247],v[44:47]\n"
LD "v199,s[48:51],s80 offen offset:3072 lds\n"
MF "v[36:39],a[88:91],a[244:247],v[252:255]\n"
"s_nop 0\n"
TR "v[184:185],v2 offset:32768\n"
"ds_write_addtid_b32 v128 offset:28672\n"
MF "v[36:39],a[48:51],a[240:243],v[36:39]\n"
"s_add_u32 m0,s79,106496\n"
TR "v[186:187],v2 offset:34816\n"
"ds_write_addtid_b32 v129 offset:24832\n"
MF "v[36:39],a[8:11],v[188:191],v[36:39]\n"
LD "v200,s[48:51],s80 offen lds\n"
MF "v[28:31],a[4:7],v[188:191],v[252:255]\n"
"s_nop 0\n"
"ds_write_addtid_b32 v130 offset:25088\n"
"ds_write_addtid_b32 v131 offset:25344\n"
MF "v[28:31],a[44:47],a[240:243],v[28:31]\n"
"s_nop 0\n"
"ds_write_addtid_b32 v132 offset:25600\n"
"ds_write_addtid_b32 v133 offset:25856\n"
MF "v[28:31],a[84:87],a[244:247],v[28:31]\n"
"s_nop 0\n"
"ds_write_addtid_b32 v134 offset:26112\n"
"ds_write_addtid_b32 v135 offset:26368\n"
MF "v[20:23],a[80:83],a[244:247],v[252:255]\n"
MF "v[20:23],a[40:43],a[240:243],v[20:23]\n"
LD "v201,s[48:51],s80 offen offset:1024 lds\n"
MF "v[20:23],a[0:3],v[188:191],v[20:23]\n"
"s_waitcnt lgkmcnt(7)\n"
MF "v[56:59],a[0:3],v[176:179],v[252:255]\n"
TR "v[188:189],v3 offset:24576\n"
MF "v[56:59],a[40:43],v[180:183],v[56:59]\n"
TR "v[190:191],v3 offset:26624\n"
MF "v[56:59],a[80:83],v[184:187],v[56:59]\n"
LD "v202,s[48:51],s80 offen offset:2048 lds\n"
MF "v[64:67],a[84:87],v[184:187],v[252:255]\n"
TR "a[240:241],v3 offset:28672\n"
MF "v[64:67],a[44:47],v[180:183],v[64:67]\n"
TR "a[242:243],v3 offset:30720\n"
MF "v[64:67],a[4:7],v[176:179],v[64:67]\n"
LD "v203,s[48:51],s80 offen offset:3072 lds\n"
MF "v[72:75],a[8:11],v[176:179],v[252:255]\n"
TR "a[244:245],v3 offset:32768\n"
MF "v[72:75],a[48:51],v[180:183],v[72:75]\n"
TR "a[246:247],v3 offset:34816\n"
MF "v[72:75],a[88:91],v[184:187],v[72:75]\n"
MF "v[80:83],a[92:95],v[184:187],v[252:255]\n"
MF "v[80:83],a[52:55],v[180:183],v[80:83]\n"
MF "v[80:83],a[12:15],v[176:179],v[80:83]\n"
MF "v[88:91],a[16:19],v[176:179],v[252:255]\n"
MF "v[88:91],a[56:59],v[180:183],v[88:91]\n"
"s_waitcnt vmcnt(31) lgkmcnt(0)\n"
MF "v[88:91],a[96:99],v[184:187],v[88:91]\n"
"s_barrier\n"
MF "v[92:95],a[96:99],a[244:247],v[252:255]\n"
"s_add_u32 s89,s89,s65\n"
TR "v[176:177],v0\n"
MF "v[92:95],a[56:59],a[240:243],v[92:95]\n"
"s_add_u32 m0,s79,24576\n"
TR "v[178:179],v0 offset:2048\n"
MF "v[92:95],a[16:19],v[188:191],v[92:95]\n"
LD "v192,s[48:51],s89 offen lds\n"
MF "v[84:87],a[12:15],v[188:191],v[252:255]\n"
TR "v[180:181],v0 offset:4096\n"
MF "v[84:87],a[52:55],a[240:243],v[84:87]\n"
TR "v[182:183],v0 offset:6144\n"
MF "v[84:87],a[92:95],a[244:247],v[84:87]\n"
LD "v193,s[48:51],s89 offen offset:1024 lds\n"
MF "v[76:79],a[88:91],a[244:247],v[252:255]\n"
".Lpg29h:\n"
"s_cbranch_execz .Lpg32h\n"
TR "v[184:185],v0 offset:8192\n"
MF "v[76:79],a[48:51],a[240:243],v[76:79]\n"
"s_nop 0\n"
TR "v[186:187],v0 offset:10240\n"
"ds_write_addtid_b32 v136 offset:49152\n"
MF "v[76:79],a[8:11],v[188:191],v[76:79]\n"
"s_nop 0\n"
"ds_write_addtid_b32 v137 offset:49408\n"
"ds_write_addtid_b32 v138 offset:49664\n"
MF "v[68:71],a[4:7],v[188:191],v[252:255]\n"
LD "v194,s[48:51],s89 offen offset:2048 lds\n"
MF "v[68:71],a[44:47],a[240:243],v[68:71]\n"
"buffer_load_dword v10,v12,s[40:43],s86 offen sc1\n"
MF "v[68:71],a[84:87],a[244:247],v[68:71]\n"
"s_nop 0\n"
"ds_write_addtid_b32 v139 offset:49920\n"
"ds_write_addtid_b32 v140 offset:50176\n"
MF "v[60:63],a[80:83],a[244:247],v[252:255]\n"
"s_nop 0\n"
"ds_write_addtid_b32 v141 offset:50432\n"
"ds_write_addtid_b32 v142 offset:50688\n"
MF "v[60:63],a[40:43],a[240:243],v[60:63]\n"
"s_nop 0\n"
"ds_write_addtid_b32 v143 offset:50944\n"
"ds_write_addtid_b32 v144 offset:51200\n"
MF "v[60:63],a[0:3],v[188:191],v[60:63]\n"
"s_waitcnt lgkmcnt(9)\n"
LD "v195,s[48:51],s89 offen offset:3072 lds\n"
MF "v[16:19],a[120:123],v[176:179],v[16:19]\n"
"s_nop 0\n"
TR "v[188:189],v1\n"
"ds_write_addtid_b32 v145 offset:51456\n"
MF "v[16:19],a[160:163],v[180:183],v[16:19]\n"
"s_nop 0\n"
TR "v[190:191],v1 offset:2048\n"
"ds_write_addtid_b32 v146 offset:51712\n"
MF "v[16:19],a[200:203],v[184:187],v[16:19]\n"
"s_nop 0\n"
"ds_write_addtid_b32 v147 offset:51968\n"
"ds_write_addtid_b32 v148 offset:52224\n"
MF "v[24:27],a[204:207],v[184:187],v[24:27]\n"
"s_nop 0\n"
TR "a[240:241],v1 offset:4096\n"
"ds_write_addtid_b32 v149 offset:52480\n"
MF "v[24:27],a[164:167],v[180:183],v[24:27]\n"
"s_add_u32 m0,s79,28672\n"
TR "a[242:243],v1 offset:6144\n"
"ds_write_addtid_b32 v150 offset:48640\n"
MF "v[24:27],a[124:127],v[176:179],v[24:27]\n"
LD "v196,s[48:51],s89 offen lds\n"
MF "v[32:35],a[128:131],v[176:179],v[32:35]\n"
"s_nop 0\n"
TR "a[244:245],v1 offset:8192\n"
"ds_write_addtid_b32 v151 offset:48896\n"
MF "v[32:35],a[168:171],v[180:183],v[32:35]\n"
"s_nop 0\n"
TR "a[246:247],v1 offset:10240\n"
"ds_write_addtid_b32 v152 offset:49152\n"
MF "v[32:35],a[208:211],v[184:187],v[32:35]\n"
LD "v197,s[48:51],s89 offen offset:1024 lds\n"
MF "v[40:43],a[212:215],v[184:187],v[40:43]\n"
"s_nop 0\n"
"ds_write_addtid_b32 v153 offset:49408\n"
"ds_write_addtid_b32 v154 offset:49664\n"
MF "v[40:43],a[172:175],v[180:183],v[40:43]\n"
"s_nop 0\n"
"ds_write_addtid_b32 v155 offset:49920\n"
"ds_write_addtid_b32 v156 offset:50176\n"
MF "v[40:43],a[132:135],v[176:179],v[40:43]\n"
"s_nop 0\n"
"ds_write_addtid_b32 v157 offset:50432\n"
"ds_write_addtid_b32 v158 offset:50688\n"
MF "v[48:51],a[136:139],v[176:179],v[48:51]\n"
"s_nop 0\n"
"ds_write_addtid_b32 v159 offset:50944\n"
"ds_write_addtid_b32 v160 offset:51200\n"
MF "v[48:51],a[176:179],v[180:183],v[48:51]\n"
LD "v198,s[48:51],s89 offen offset:2048 lds\n"
MF "v[48:51],a[216:219],v[184:187],v[48:51]\n"
"s_waitcnt lgkmcnt(9)\n"
"ds_write_addtid_b32 v161 offset:51456\n"
"ds_write_addtid_b32 v162 offset:51712\n"
MF "v[52:55],a[216:219],a[244:247],v[52:55]\n"
"s_nop 0\n"
TR "v[176:177],v2\n"
"ds_write_addtid_b32 v163 offset:51968\n"
MF "v[52:55],a[176:179],a[240:243],v[52:55]\n"
"s_nop 0\n"
TR "v[178:179],v2 offset:2048\n"
"ds_write_addtid_b32 v164 offset:52224\n"
MF "v[52:55],a[136:139],v[188:191],v[52:55]\n"
LD "v199,s[48:51],s89 offen offset:3072 lds\n"
MF "v[44:47],a[132:135],v[188:191],v[44:47]\n"
"s_nop 0\n"
TR "v[180:181],v2 offset:4096\n"
"ds_write_addtid_b32 v165 offset:52480\n"
MF "v[44:47],a[172:175],a[240:243],v[44:47]\n"
"s_nop 0\n"
TR "v[182:183],v2 offset:6144\n"
"ds_write_addtid_b32 v166 offset:52736\n"
MF "v[44:47],a[212:215],a[244:247],v[44:47]\n"
"s_nop 0\n"
"ds_write_addtid_b32 v167 offset:52992\n"
"ds_write_addtid_b32 v168 offset:53248\n"
MF "v[36:39],a[208:211],a[244:247],v[36:39]\n"
"s_nop 0\n"
TR "v[184:185],v2 offset:8192\n"
"ds_write_addtid_b32 v169 offset:53504\n"
MF "v[36:39],a[168:171],a[240:243],v[36:39]\n"
"s_add_u32 m0,s79,32768\n"
TR "v[186:187],v2 offset:10240\n"
"ds_write_addtid_b32 v170 offset:49664\n"
MF "v[36:39],a[128:131],v[188:191],v[36:39]\n"
LD "v200,s[48:51],s89 offen lds\n"
MF "v[28:31],a[124:127],v[188:191],v[28:31]\n"
"s_nop 0\n"
"ds_write_addtid_b32 v171 offset:49920\n"
"ds_write_addtid_b32 v172 offset:50176\n"
MF "v[28:31],a[164:167],a[240:243],v[28:31]\n"
"s_nop 0\n"
"ds_write_addtid_b32 v173 offset:50432\n"
"ds_write_addtid_b32 v174 offset:50688\n"
MF "v[28:31],a[204:207],a[244:247],v[28:31]\n"
"ds_write_addtid_b32 v175 offset:50944\n"
MF "v[20:23],a[200:203],a[244:247],v[20:23]\n"
LD "v201,s[48:51],s89 offen offset:1024 lds\n"
MF "v[20:23],a[160:163],a[240:243],v[20:23]\n"
MF "v[20:23],a[120:123],v[188:191],v[20:23]\n"
"s_waitcnt lgkmcnt(6)\n"
MF "v[56:59],a[120:123],v[176:179],v[56:59]\n"
TR "v[188:189],v3\n"
MF "v[56:59],a[160:163],v[180:183],v[56:59]\n"
TR "v[190:191],v3 offset:2048\n"
MF "v[56:59],a[200:203],v[184:187],v[56:59]\n"
LD "v202,s[48:51],s89 offen offset:2048 lds\n"
MF "v[64:67],a[204:207],v[184:187],v[64:67]\n"
TR "a[240:241],v3 offset:4096\n"
MF "v[64:67],a[164:167],v[180:183],v[64:67]\n"
TR "a[242:243],v3 offset:6144\n"
MF "v[64:67],a[124:127],v[176:179],v[64:67]\n"
LD "v203,s[48:51],s89 offen offset:3072 lds\n"
MF "v[72:75],a[128:131],v[176:179],v[72:75]\n"
TR "a[244:245],v3 offset:8192\n"
MF "v[72:75],a[168:171],v[180:183],v[72:75]\n"
TR "a[246:247],v3 offset:10240\n"
MF "v[72:75],a[208:211],v[184:187],v[72:75]\n"
MF "v[80:83],a[212:215],v[184:187],v[80:83]\n"
MF "v[80:83],a[172:175],v[180:183],v[80:83]\n"
MF "v[80:83],a[132:135],v[176:179],v[80:83]\n"
MF "v[88:91],a[136:139],v[176:179],v[88:91]\n"
MF "v[88:91],a[176:179],v[180:183],v[88:91]\n"
"s_waitcnt vmcnt(27) lgkmcnt(0)\n"
MF "v[88:91],a[216:219],v[184:187],v[88:91]\n"
"s_barrier\n"
MF "v[92:95],a[216:219],a[244:247],v[92:95]\n"
"s_add_u32 s80,s89,122880\n"
TR "v[176:177],v0 offset:49152\n"
MF "v[92:95],a[176:179],a[240:243],v[92:95]\n"
"s_add_u32 m0,s79,0\n"
"s_mov_b32 s82,s70\n"
TR "v[178:179],v0 offset:51200\n"
MF "v[92:95],a[136:139],v[188:191],v[92:95]\n"
LD "v192,s[48:51],s80 offen lds\n"
MF "v[84:87],a[132:135],v[188:191],v[84:87]\n"
TR "v[180:181],v0 offset:53248\n"
MF "v[84:87],a[172:175],a[240:243],v[84:87]\n"
TR "v[182:183],v0 offset:55296\n"
MF "v[84:87],a[212:215],a[244:247],v[84:87]\n"
LD "v193,s[48:51],s80 offen offset:1024 lds\n"
MF "v[76:79],a[208:211],a[244:247],v[76:79]\n"
TR "v[184:185],v0 offset:57344\n"
MF "v[76:79],a[168:171],a[240:243],v[76:79]\n"
TR "v[186:187],v0 offset:59392\n"
MF "v[76:79],a[128:131],v[188:191],v[76:79]\n"
MF "v[68:71],a[124:127],v[188:191],v[68:71]\n"
MF "v[68:71],a[164:167],a[240:243],v[68:71]\n"
LD "v194,s[48:51],s80 offen offset:2048 lds\n"
MF "v[68:71],a[204:207],a[244:247],v[68:71]\n"
MF "v[60:63],a[200:203],a[244:247],v[60:63]\n"
MF "v[60:63],a[160:163],a[240:243],v[60:63]\n"
MF "v[60:63],a[120:123],v[188:191],v[60:63]\n"
"s_waitcnt lgkmcnt(0)\n"
LD "v195,s[48:51],s80 offen offset:3072 lds\n"
MF "v[16:19],a[20:23],v[176:179],v[16:19]\n"
TR "v[188:189],v1 offset:49152\n"
MF "v[16:19],a[60:63],v[180:183],v[16:19]\n"
TR "v[190:191],v1 offset:51200\n"
MF "v[16:19],a[100:103],v[184:187],v[16:19]\n"
MF "v[24:27],a[104:107],v[184:187],v[24:27]\n"
TR "a[240:241],v1 offset:53248\n"
MF "v[24:27],a[64:67],v[180:183],v[24:27]\n"
"s_add_u32 m0,s79,4096\n"
TR "a[242:243],v1 offset:55296\n"
MF "v[24:27],a[24:27],v[176:179],v[24:27]\n"
LD "v196,s[48:51],s80 offen lds\n"
MF "v[32:35],a[28:31],v[176:179],v[32:35]\n"
TR "a[244:245],v1 offset:57344\n"
MF "v[32:35],a[68:71],v[180:183],v[32:35]\n"
TR "a[246:247],v1 offset:59392\n"
MF "v[32:35],a[108:111],v[184:187],v[32:35]\n"
LD "v197,s[48:51],s80 offen offset:1024 lds\n"
MF "v[40:43],a[112:115],v[184:187],v[40:43]\n"
MF "v[40:43],a[72:75],v[180:183],v[40:43]\n"
MF "v[40:43],a[32:35],v[176:179],v[40:43]\n"
MF "v[48:51],a[36:39],v[176:179],v[48:51]\n"
MF "v[48:51],a[76:79],v[180:183],v[48:51]\n"
LD "v198,s[48:51],s80 offen offset:2048 lds\n"
MF "v[48:51],a[116:119],v[184:187],v[48:51]\n"
"s_waitcnt lgkmcnt(0)\n"
MF "v[52:55],a[116:119],a[244:247],v[52:55]\n"
TR "v[176:177],v2 offset:49152\n"
MF "v[52:55],a[76:79],a[240:243],v[52:55]\n"
TR "v[178:179],v2 offset:51200\n"
MF "v[52:55],a[36:39],v[188:191],v[52:55]\n"
LD "v199,s[48:51],s80 offen offset:3072 lds\n"
MF "v[44:47],a[32:35],v[188:191],v[44:47]\n"
TR "v[180:181],v2 offset:53248\n"
MF "v[44:47],a[72:75],a[240:243],v[44:47]\n"
TR "v[182:183],v2 offset:55296\n"
MF "v[44:47],a[112:115],a[244:247],v[44:47]\n"
MF "v[36:39],a[108:111],a[244:247],v[36:39]\n"
TR "v[184:185],v2 offset:57344\n"
MF "v[36:39],a[68:71],a[240:243],v[36:39]\n"
"s_add_u32 m0,s79,8192\n"
TR "v[186:187],v2 offset:59392\n"
MF "v[36:39],a[28:31],v[188:191],v[36:39]\n"
LD "v200,s[48:51],s80 offen lds\n"
MF "v[28:31],a[24:27],v[188:191],v[28:31]\n"
MF "v[28:31],a[64:67],a[240:243],v[28:31]\n"
MF "v[28:31],a[104:107],a[244:247],v[28:31]\n"
MF "v[20:23],a[100:103],a[244:247],v[20:23]\n"
LD "v201,s[48:51],s80 offen offset:1024 lds\n"
MF "v[20:23],a[60:63],a[240:243],v[20:23]\n"
"s_waitcnt vmcnt(19)\n"
MF "v[20:23],a[20:23],v[188:191],v[20:23]\n"
"v_mov_b32 v236,v228\n"
"v_mov_b32 v237,v229\n"
MF "v[16:19],a[252:255],v[208:211],v[16:19]\n"
MF "v[24:27],a[252:255],v[212:215],v[24:27]\n"
MF "v[32:35],a[252:255],v[216:219],v[32:35]\n"
MF "v[40:43],a[252:255],v[220:223],v[40:43]\n"
MF "v[48:51],a[252:255],v[224:227],v[48:51]\n"
MF "v[20:23],a[248:251],v[208:211],v[20:23]\n"
MF "v[28:31],a[248:251],v[212:215],v[28:31]\n"
MF "v[36:39],a[248:251],v[216:219],v[36:39]\n"
MF "v[44:47],a[248:251],v[220:223],v[44:47]\n"
MF "v[52:55],a[248:251],v[224:227],v[52:55]\n"
MF "v[248:251],v[230:233],a[250:253],v[248:251]\n"
MF "v[244:247],v[234:237],a[250:253],v[244:247]\n"
"s_waitcnt lgkmcnt(0)\n"
MF "v[56:59],a[20:23],v[176:179],v[56:59]\n"
TR "v[188:189],v3 offset:49152\n"
"v_sub_u32 v10,v10,v11\n"
MF "v[56:59],a[60:63],v[180:183],v[56:59]\n"
TR "v[190:191],v3 offset:51200\n"
MF "v[56:59],a[100:103],v[184:187],v[56:59]\n"
LD "v202,s[48:51],s80 offen offset:2048 lds\n"
MF "v[64:67],a[104:107],v[184:187],v[64:67]\n"
TR "a[240:241],v3 offset:53248\n"
MF "v[64:67],a[64:67],v[180:183],v[64:67]\n"
TR "a[242:243],v3 offset:55296\n"
MF "v[64:67],a[24:27],v[176:179],v[64:67]\n"
LD "v203,s[48:51],s80 offen offset:3072 lds\n"
MF "v[72:75],a[28:31],v[176:179],v[72:75]\n"
TR "a[244:245],v3 offset:57344\n"
MF "v[72:75],a[68:71],v[180:183],v[72:75]\n"
TR "a[246:247],v3 offset:59392\n"
MF "v[72:75],a[108:111],v[184:187],v[72:75]\n"
".Lnf2:\n"
"v_readlane_b32 s58,v10,s67\n"
"s_cmp_lt_u32 s58,s85\n"
"s_cbranch_scc1 .Lsp2\n"
".Lnb2:\n"
MF "v[80:83],a[112:115],v[184:187],v[80:83]\n"
"s_add_u32 s70,s70,20480\n"
MF "v[80:83],a[72:75],v[180:183],v[80:83]\n"
MF "v[80:83],a[32:35],v[176:179],v[80:83]\n"
MF "v[88:91],a[36:39],v[176:179],v[88:91]\n"
"s_add_u32 s2,s2,204800\n"
MF "v[88:91],a[76:79],v[180:183],v[88:91]\n"
"s_waitcnt vmcnt(25) lgkmcnt(0)\n"
LD "v[228:231],v9,s[40:43],s2 offen sc1\n"
MF "v[88:91],a[116:119],v[184:187],v[88:91]\n"
"s_barrier\n"
MF "v[92:95],a[116:119],a[244:247],v[92:95]\n"
"s_add_u32 s88,s88,s65\n"
TR "v[176:177],v4\n"
MF "v[92:95],a[76:79],a[240:243],v[92:95]\n"
"s_add_u32 m0,s79,49152\n"
TR "v[178:179],v4 offset:2048\n"
MF "v[92:95],a[36:39],v[188:191],v[92:95]\n"
LD "v192,s[48:51],s88 offen lds\n"
MF "v[84:87],a[32:35],v[188:191],v[84:87]\n"
TR "v[180:181],v4 offset:4096\n"
MF "v[84:87],a[72:75],a[240:243],v[84:87]\n"
TR "v[182:183],v4 offset:6144\n"
MF "v[84:87],a[112:115],a[244:247],v[84:87]\n"
LD "v193,s[48:51],s88 offen offset:1024 lds\n"
MF "v[76:79],a[108:111],a[244:247],v[76:79]\n"
TR "v[184:185],v4 offset:8192\n"
MF "v[76:79],a[68:71],a[240:243],v[76:79]\n"
"s_add_u32 s2,s2,204800\n"
TR "v[186:187],v4 offset:10240\n"
MF "v[76:79],a[28:31],v[188:191],v[76:79]\n"
LD "v[232:235],v9,s[40:43],s2 offen sc1\n"
MF "v[68:71],a[24:27],v[188:191],v[68:71]\n"
MF "v[68:71],a[64:67],a[240:243],v[68:71]\n"
LD "v194,s[48:51],s88 offen offset:2048 lds\n"
MF "v[68:71],a[104:107],a[244:247],v[68:71]\n"
MF "v[60:63],a[100:103],a[244:247],v[60:63]\n"
MF "v[60:63],a[60:63],a[240:243],v[60:63]\n"
MF "v[60:63],a[20:23],v[188:191],v[60:63]\n"
"s_waitcnt lgkmcnt(0)\n"
LD "v195,s[48:51],s88 offen offset:3072 lds\n"
MF "v[16:19],a[140:143],v[176:179],v[16:19]\n"
TR "v[188:189],v5\n"
MF "v[16:19],a[180:183],v[180:183],v[16:19]\n"
TR "v[190:191],v5 offset:2048\n"
MF "v[16:19],a[220:223],v[184:187],v[16:19]\n"
LD "v[208:211],v9,s[40:43],s82 offen sc1\n"
MF "v[24:27],a[224:227],v[184:187],v[24:27]\n"
TR "a[240:241],v5 offset:4096\n"
MF "v[24:27],a[184:187],v[180:183],v[24:27]\n"
"s_add_u32 m0,s79,53248\n"
TR "a[242:243],v5 offset:6144\n"
MF "v[24:27],a[144:147],v[176:179],v[24:27]\n"
LD "v196,s[48:51],s88 offen lds\n"
MF "v[32:35],a[148:151],v[176:179],v[32:35]\n"
TR "a[244:245],v5 offset:8192\n"
MF "v[32:35],a[188:191],v[180:183],v[32:35]\n"
TR "a[246:247],v5 offset:10240\n"
MF "v[32:35],a[228:231],v[184:187],v[32:35]\n"
LD "v197,s[48:51],s88 offen offset:1024 lds\n"
MF "v[40:43],a[232:235],v[184:187],v[40:43]\n"
MF "v[40:43],a[192:195],v[180:183],v[40:43]\n"
MF "v[40:43],a[152:155],v[176:179],v[40:43]\n"
MF "v[48:51],a[156:159],v[176:179],v[48:51]\n"
LD "v[212:215],v9,s[40:43],s82 offen offset:1024 sc1\n"
MF "v[48:51],a[196:199],v[180:183],v[48:51]\n"
LD "v198,s[48:51],s88 offen offset:2048 lds\n"
MF "v[48:51],a[236:239],v[184:187],v[48:51]\n"
"s_waitcnt lgkmcnt(0)\n"
MF "v[52:55],a[236:239],a[244:247],v[52:55]\n"
TR "v[176:177],v6\n"
MF "v[52:55],a[196:199],a[240:243],v[52:55]\n"
TR "v[178:179],v6 offset:2048\n"
MF "v[52:55],a[156:159],v[188:191],v[52:55]\n"
LD "v199,s[48:51],s88 offen offset:3072 lds\n"
MF "v[44:47],a[152:155],v[188:191],v[44:47]\n"
TR "v[180:181],v6 offset:4096\n"
MF "v[44:47],a[192:195],a[240:243],v[44:47]\n"
TR "v[182:183],v6 offset:6144\n"
MF "v[44:47],a[232:235],a[244:247],v[44:47]\n"
MF "v[36:39],a[228:231],a[244:247],v[36:39]\n"
TR "v[184:185],v6 offset:8192\n"
MF "v[36:39],a[188:191],a[240:243],v[36:39]\n"
"s_add_u32 m0,s79,57344\n"
TR "v[186:187],v6 offset:10240\n"
MF "v[36:39],a[148:151],v[188:191],v[36:39]\n"
LD "v200,s[48:51],s88 offen lds\n"
MF "v[28:31],a[144:147],v[188:191],v[28:31]\n"
LD "v[216:219],v9,s[40:43],s82 offen offset:2048 sc1\n"
MF "v[28:31],a[184:187],a[240:243],v[28:31]\n"
MF "v[28:31],a[224:227],a[244:247],v[28:31]\n"
MF "v[20:23],a[220:223],a[244:247],v[20:23]\n"
LD "v201,s[48:51],s88 offen offset:1024 lds\n"
MF "v[20:23],a[180:183],a[240:243],v[20:23]\n"
MF "v[20:23],a[140:143],v[188:191],v[20:23]\n"
"s_waitcnt lgkmcnt(0)\n"
MF "v[56:59],a[140:143],v[176:179],v[56:59]\n"
TR "v[188:189],v7\n"
MF "v[56:59],a[180:183],v[180:183],v[56:59]\n"
TR "v[190:191],v7 offset:2048\n"
MF "v[56:59],a[220:223],v[184:187],v[56:59]\n"
LD "v202,s[48:51],s88 offen offset:2048 lds\n"
MF "v[64:67],a[224:227],v[184:187],v[64:67]\n"
TR "a[240:241],v7 offset:4096\n"
MF "v[64:67],a[184:187],v[180:183],v[64:67]\n"
TR "a[242:243],v7 offset:6144\n"
MF "v[64:67],a[144:147],v[176:179],v[64:67]\n"
LD "v203,s[48:51],s88 offen offset:3072 lds\n"
MF "v[72:75],a[148:151],v[176:179],v[72:75]\n"
TR "a[244:245],v7 offset:8192\n"
MF "v[72:75],a[188:191],v[180:183],v[72:75]\n"
TR "a[246:247],v7 offset:10240\n"
MF "v[72:75],a[228:231],v[184:187],v[72:75]\n"
LD "v[220:223],v9,s[40:43],s82 offen offset:3072 sc1\n"
MF "v[80:83],a[232:235],v[184:187],v[80:83]\n"
MF "v[80:83],a[192:195],v[180:183],v[80:83]\n"
MF "v[80:83],a[152:155],v[176:179],v[80:83]\n"
MF "v[88:91],a[156:159],v[176:179],v[88:91]\n"
"s_add_u32 s82,s82,4096\n"
MF "v[88:91],a[196:199],v[180:183],v[88:91]\n"
"s_waitcnt vmcnt(30) lgkmcnt(0)\n"
LD "v[224:227],v9,s[40:43],s82 offen sc1\n"
MF "v[88:91],a[236:239],v[184:187],v[88:91]\n"
"s_barrier\n"
MF "v[92:95],a[236:239],a[244:247],v[92:95]\n"
"s_add_u32 s80,s88,122880\n"
TR "v[176:177],v0 offset:24576\n"
MF "v[92:95],a[196:199],a[240:243],v[92:95]\n"
"s_add_u32 m0,s79,98304\n"
"s_add_u32 s85,s85,1\n"
TR "v[178:179],v0 offset:26624\n"
MF "v[92:95],a[156:159],v[188:191],v[92:95]\n"
LD "v192,s[48:51],s80 offen lds\n"
MF "v[84:87],a[152:155],v[188:191],v[84:87]\n"
"s_nop 0\n"
TR "v[180:181],v0 offset:28672\n"
"ds_write_addtid_b32 v16 offset:24576\n"
MF "v[84:87],a[192:195],a[240:243],v[84:87]\n"
"s_nop 0\n"
TR "v[182:183],v0 offset:30720\n"
"ds_write_addtid_b32 v17 offset:24832\n"
MF "v[84:87],a[232:235],a[244:247],v[84:87]\n"
LD "v193,s[48:51],s80 offen offset:1024 lds\n"
MF "v[76:79],a[228:231],a[244:247],v[76:79]\n"
"s_nop 0\n"
TR "v[184:185],v0 offset:32768\n"
"ds_write_addtid_b32 v18 offset:25088\n"
MF "v[76:79],a[188:191],a[240:243],v[76:79]\n"
"s_nop 0\n"
TR "v[186:187],v0 offset:34816\n"
"ds_write_addtid_b32 v19 offset:25344\n"
MF "v[76:79],a[148:151],v[188:191],v[76:79]\n"
"s_nop 0\n"
"ds_write_addtid_b32 v20 offset:25600\n"
"ds_write_addtid_b32 v21 offset:25856\n"
MF "v[68:71],a[144:147],v[188:191],v[68:71]\n"
LD "v194,s[48:51],s80 offen offset:2048 lds\n"
MF "v[68:71],a[184:187],a[240:243],v[68:71]\n"
"s_nop 0\n"
"ds_write_addtid_b32 v22 offset:26112\n"
"ds_write_addtid_b32 v23 offset:26368\n"
MF "v[68:71],a[224:227],a[244:247],v[68:71]\n"
"s_nop 0\n"
"ds_write_addtid_b32 v24 offset:26624\n"
"ds_write_addtid_b32 v25 offset:26880\n"
MF "v[60:63],a[220:223],a[244:247],v[60:63]\n"
"s_nop 0\n"
"ds_write_addtid_b32 v26 offset:27136\n"
"ds_write_addtid_b32 v27 offset:27392\n"
MF "v[60:63],a[180:183],a[240:243],v[60:63]\n"
MF "v[60:63],a[140:143],v[188:191],v[60:63]\n"
"s_waitcnt lgkmcnt(9)\n"
"s_cmp_lg_u32 s85,9\n"
"s_cbranch_scc1 .Lpl09\n"
".Lplast0:\n"
MF "v[96:99],v[176:179],a[0:3],v[252:255]\n"
"s_nop 0\n"
TR "v[188:189],v1 offset:24576\n"
TR "v[190:191],v1 offset:26624\n"
MF "v[96:99],v[180:183],a[40:43],v[96:99]\n"
"s_nop 0\n"
TR "a[240:241],v1 offset:28672\n"
TR "a[242:243],v1 offset:30720\n"
MF "v[96:99],v[184:187],a[80:83],v[96:99]\n"
"s_add_u32 s90,s91,163840\n"
TR "a[244:245],v1 offset:32768\n"
TR "a[246:247],v1 offset:34816\n"
MF "v[104:107],v[184:187],a[84:87],v[252:255]\n"
LD "v195,s[48:51],s80 offen offset:3072 lds\n"
MF "v[104:107],v[180:183],a[44:47],v[104:107]\n"
"s_nop 0\n"
"ds_write_addtid_b32 v28 offset:27648\n"
"ds_write_addtid_b32 v29 offset:27904\n"
MF "v[104:107],v[176:179],a[4:7],v[104:107]\n"
"s_nop 0\n"
"ds_write_addtid_b32 v30 offset:28160\n"
"ds_write_addtid_b32 v31 offset:28416\n"
MF "v[112:115],v[176:179],a[8:11],v[252:255]\n"
"s_nop 0\n"
"ds_write_addtid_b32 v32 offset:28672\n"
"ds_write_addtid_b32 v33 offset:28928\n"
MF "v[112:115],v[180:183],a[48:51],v[112:115]\n"
"s_add_u32 m0,s79,102400\n"
"s_nop 0\n"
"ds_write_addtid_b32 v34 offset:25088\n"
"ds_write_addtid_b32 v35 offset:25344\n"
MF "v[112:115],v[184:187],a[88:91],v[112:115]\n"
LD "v196,s[48:51],s80 offen lds\n"
MF "v[120:123],v[184:187],a[92:95],v[252:255]\n"
"s_nop 0\n"
"ds_write_addtid_b32 v36 offset:25600\n"
"ds_write_addtid_b32 v37 offset:25856\n"
MF "v[120:123],v[180:183],a[52:55],v[120:123]\n"
"ds_write_addtid_b32 v38 offset:26112\n"
MF "v[120:123],v[176:179],a[12:15],v[120:123]\n"
"ds_write_addtid_b32 v39 offset:26368\n"
MF "v[128:131],v[176:179],a[16:19],v[252:255]\n"
"ds_write_addtid_b32 v40 offset:26624\n"
MF "v[128:131],v[180:183],a[56:59],v[128:131]\n"
"ds_write_addtid_b32 v41 offset:26880\n"
MF "v[128:131],v[184:187],a[96:99],v[128:131]\n"
"s_waitcnt lgkmcnt(14)\n"
LD "v197,s[48:51],s80 offen offset:1024 lds\n"
MF "v[132:135],a[244:247],a[96:99],v[252:255]\n"
"s_nop 0\n"
TR "v[176:177],v2 offset:24576\n"
TR "v[178:179],v2 offset:26624\n"
MF "v[132:135],a[240:243],a[56:59],v[132:135]\n"
"s_nop 0\n"
TR "v[180:181],v2 offset:28672\n"
TR "v[182:183],v2 offset:30720\n"
MF "v[132:135],v[188:191],a[16:19],v[132:135]\n"
"s_nop 0\n"
TR "v[184:185],v2 offset:32768\n"
TR "v[186:187],v2 offset:34816\n"
MF "v[124:127],v[188:191],a[12:15],v[252:255]\n"
"s_nop 0\n"
"ds_write_addtid_b32 v42 offset:27136\n"
"ds_write_addtid_b32 v43 offset:27392\n"
MF "v[124:127],a[240:243],a[52:55],v[124:127]\n"
"s_nop 0\n"
"ds_write_addtid_b32 v44 offset:27648\n"
"ds_write_addtid_b32 v45 offset:27904\n"
MF "v[124:127],a[244:247],a[92:95],v[124:127]\n"
"s_nop 0\n"
"ds_write_addtid_b32 v46 offset:28160\n"
"ds_write_addtid_b32 v47 offset:28416\n"
MF "v[116:119],a[244:247],a[88:91],v[252:255]\n"
LD "v198,s[48:51],s80 offen offset:2048 lds\n"
MF "v[116:119],a[240:243],a[48:51],v[116:119]\n"
"s_nop 0\n"
"ds_write_addtid_b32 v48 offset:28672\n"
"ds_write_addtid_b32 v49 offset:28928\n"
MF "v[116:119],v[188:191],a[8:11],v[116:119]\n"
"s_nop 0\n"
"ds_write_addtid_b32 v50 offset:29184\n"
"ds_write_addtid_b32 v51 offset:29440\n"
MF "v[108:111],v[188:191],a[4:7],v[252:255]\n"
"ds_write_addtid_b32 v52 offset:29696\n"
MF "v[108:111],a[240:243],a[44:47],v[108:111]\n"
"ds_write_addtid_b32 v53 offset:29952\n"
MF "v[108:111],a[244:247],a[84:87],v[108:111]\n"
"ds_write_addtid_b32 v54 offset:30208\n"
MF "v[100:103],a[244:247],a[80:83],v[252:255]\n"
"ds_write_addtid_b32 v55 offset:30464\n"
MF "v[100:103],a[240:243],a[40:43],v[100:103]\n"
LD "v199,s[48:51],s80 offen offset:3072 lds\n"
MF "v[100:103],v[188:191],a[0:3],v[100:103]\n"
"s_waitcnt lgkmcnt(14)\n"
MF "v[136:139],v[176:179],a[0:3],v[252:255]\n"
"s_nop 0\n"
TR "v[188:189],v3 offset:24576\n"
TR "v[190:191],v3 offset:26624\n"
MF "v[136:139],v[180:183],a[40:43],v[136:139]\n"
"s_add_u32 m0,s79,106496\n"
TR "a[240:241],v3 offset:28672\n"
TR "a[242:243],v3 offset:30720\n"
MF "v[136:139],v[184:187],a[80:83],v[136:139]\n"
"s_nop 0\n"
TR "a[244:245],v3 offset:32768\n"
TR "a[246:247],v3 offset:34816\n"
"s_waitcnt lgkmcnt(0)\n"
MF "v[144:147],v[184:187],a[84:87],v[252:255]\n"
".Lpg30h:\n"
"s_cbranch_execz .Lpg33h\n"
"s_barrier\n"
MF "v[144:147],v[180:183],a[44:47],v[144:147]\n"
LD "v200,s[48:51],s80 offen lds\n"
MF "v[144:147],v[176:179],a[4:7],v[144:147]\n"
"ds_read2st64_b32 v[16:17],v13 offset0:0 offset1:1\n"
MF "v[152:155],v[176:179],a[8:11],v[252:255]\n"
"ds_read2st64_b32 v[18:19],v13 offset0:2 offset1:3\n"
MF "v[152:155],v[180:183],a[48:51],v[152:155]\n"
"ds_read2st64_b32 v[20:21],v13 offset0:4 offset1:5\n"
MF "v[152:155],v[184:187],a[88:91],v[152:155]\n"
"ds_read2st64_b32 v[22:23],v13 offset0:6 offset1:7\n"
MF "v[160:163],v[184:187],a[92:95],v[252:255]\n"
"ds_read2st64_b32 v[24:25],v13 offset0:8 offset1:9\n"
MF "v[160:163],v[180:183],a[52:55],v[160:163]\n"
"ds_read2st64_b32 v[26:27],v13 offset0:10 offset1:11\n"
MF "v[160:163],v[176:179],a[12:15],v[160:163]\n"
LD "v201,s[48:51],s80 offen offset:1024 lds\n"
MF "v[168:171],v[176:179],a[16:19],v[252:255]\n"
"ds_read2st64_b32 v[28:29],v13 offset0:12 offset1:13\n"
MF "v[168:171],v[180:183],a[56:59],v[168:171]\n"
"ds_read2st64_b32 v[30:31],v13 offset0:14 offset1:15\n"
"s_waitcnt vmcnt(29) lgkmcnt(0)\n"
MF "v[168:171],v[184:187],a[96:99],v[168:171]\n"
"s_barrier\n"
MF "v[172:175],a[244:247],a[96:99],v[252:255]\n"
"s_nop 0\n"
TR "v[176:177],v0\n"
TR "v[178:179],v0 offset:2048\n"
MF "v[172:175],a[240:243],a[56:59],v[172:175]\n"
"s_nop 0\n"
TR "v[180:181],v0 offset:4096\n"
TR "v[182:183],v0 offset:6144\n"
MF "v[172:175],v[188:191],a[16:19],v[172:175]\n"
"s_nop 0\n"
TR "v[184:185],v0 offset:8192\n"
TR "v[186:187],v0 offset:10240\n"
MF "v[164:167],v[188:191],a[12:15],v[252:255]\n"
LD "v202,s[48:51],s80 offen offset:2048 lds\n"
MF "v[164:167],a[240:243],a[52:55],v[164:167]\n"
"ds_read2st64_b32 v[32:33],v13 offset0:16 offset1:17\n"
"v_add_f32 v56,v56,v16\n"
MF "v[164:167],a[244:247],a[92:95],v[164:167]\n"
"ds_read2st64_b32 v[34:35],v13 offset0:18 offset1:19\n"
"v_add_f32 v57,v57,v17\n"
MF "v[156:159],a[244:247],a[88:91],v[252:255]\n"
"ds_read2st64_b32 v[36:37],v13 offset0:20 offset1:21\n"
"v_add_f32 v58,v58,v18\n"
MF "v[156:159],a[240:243],a[48:51],v[156:159]\n"
"ds_read2st64_b32 v[38:39],v13 offset0:22 offset1:23\n"
"v_add_f32 v59,v59,v19\n"
MF "v[156:159],v[188:191],a[8:11],v[156:159]\n"
"ds_read2st64_b32 v[40:41],v13 offset0:24 offset1:25\n"
"v_add_f32 v60,v60,v20\n"
MF "v[148:151],v[188:191],a[4:7],v[252:255]\n"
LD "v203,s[48:51],s80 offen offset:3072 lds\n"
MF "v[148:151],a[240:243],a[44:47],v[148:151]\n"
"ds_read2st64_b32 v[42:43],v13 offset0:26 offset1:27\n"
"v_add_f32 v61,v61,v21\n"
MF "v[148:151],a[244:247],a[84:87],v[148:151]\n"
"ds_read2st64_b32 v[44:45],v13 offset0:28 offset1:29\n"
"v_add_f32 v62,v62,v22\n"
MF "v[140:143],a[244:247],a[80:83],v[252:255]\n"
"ds_read2st64_b32 v[46:47],v13 offset0:30 offset1:31\n"
"v_add_f32 v63,v63,v23\n"
MF "v[140:143],a[240:243],a[40:43],v[140:143]\n"
"ds_read2st64_b32 v[48:49],v13 offset0:32 offset1:33\n"
CV "v56,v56,v57\n"
MF "v[140:143],v[188:191],a[0:3],v[140:143]\n"
"s_waitcnt lgkmcnt(9)\n"
"ds_read2st64_b32 v[50:51],v13 offset0:34 offset1:35\n"
CV "v57,v58,v59\n"
MF "v[96:99],v[176:179],a[120:123],v[96:99]\n"
"s_nop 0\n"
TR "v[188:189],v1\n"
TR "v[190:191],v1 offset:2048\n"
MF "v[96:99],v[180:183],a[160:163],v[96:99]\n"
"s_nop 0\n"
TR "a[240:241],v1 offset:4096\n"
TR "a[242:243],v1 offset:6144\n"
MF "v[96:99],v[184:187],a[200:203],v[96:99]\n"
"s_nop 0\n"
TR "a[244:245],v1 offset:8192\n"
TR "a[246:247],v1 offset:10240\n"
MF "v[104:107],v[184:187],a[204:207],v[104:107]\n"
"ds_read2st64_b32 v[52:53],v13 offset0:36 offset1:37\n"
CV "v58,v60,v61\n"
MF "v[104:107],v[180:183],a[164:167],v[104:107]\n"
"s_add_u32 s87,s90,10240\n"
"ds_read2st64_b32 v[54:55],v13 offset0:38 offset1:39\n"
CV "v59,v62,v63\n"
MF "v[104:107],v[176:179],a[124:127],v[104:107]\n"
"buffer_store_dwordx4 v[56:59],v9,s[40:43],s87 offen sc1\n"
MF "v[112:115],v[176:179],a[128:131],v[112:115]\n"
"v_add_f32 v64,v64,v24\n"
"v_add_f32 v65,v65,v25\n"
MF "v[112:115],v[180:183],a[168:171],v[112:115]\n"
"v_add_f32 v66,v66,v26\n"
"v_add_f32 v67,v67,v27\n"
MF "v[112:115],v[184:187],a[208:211],v[112:115]\n"
"v_add_f32 v68,v68,v28\n"
"v_add_f32 v69,v69,v29\n"
MF "v[120:123],v[184:187],a[212:215],v[120:123]\n"
"v_add_f32 v70,v70,v30\n"
"v_add_f32 v71,v71,v31\n"
MF "v[120:123],v[180:183],a[172:175],v[120:123]\n"
CV "v64,v64,v65\n"
CV "v65,v66,v67\n"
MF "v[120:123],v[176:179],a[132:135],v[120:123]\n"
"s_add_u32 s87,s90,11264\n"
CV "v66,v68,v69\n"
CV "v67,v70,v71\n"
MF "v[128:131],v[176:179],a[136:139],v[128:131]\n"
"s_waitcnt lgkmcnt(14)\n"
"buffer_store_dwordx4 v[64:67],v9,s[40:43],s87 offen sc1\n"
MF "v[128:131],v[180:183],a[176:179],v[128:131]\n"
"v_add_f32 v72,v72,v32\n"
"v_add_f32 v73,v73,v33\n"
MF "v[128:131],v[184:187],a[216:219],v[128:131]\n"
"s_waitcnt lgkmcnt(2)\n"
"v_add_f32 v74,v74,v34\n"
"v_add_f32 v75,v75,v35\n"
MF "v[132:135],a[244:247],a[216:219],v[132:135]\n"
"s_nop 0\n"
TR "v[176:177],v2\n"
TR "v[178:179],v2 offset:2048\n"
MF "v[132:135],a[240:243],a[176:179],v[132:135]\n"
"s_nop 0\n"
TR "v[180:181],v2 offset:4096\n"
TR "v[182:183],v2 offset:6144\n"
MF "v[132:135],v[188:191],a[136:139],v[132:135]\n"
"s_nop 0\n"
TR "v[184:185],v2 offset:8192\n"
TR "v[186:187],v2 offset:10240\n"
MF "v[124:127],v[188:191],a[132:135],v[124:127]\n"
"v_add_f32 v76,v76,v36\n"
"v_add_f32 v77,v77,v37\n"
MF "v[124:127],a[240:243],a[172:175],v[124:127]\n"
"v_add_f32 v78,v78,v38\n"
"v_add_f32 v79,v79,v39\n"
MF "v[124:127],a[244:247],a[212:215],v[124:127]\n"
CV "v72,v72,v73\n"
CV "v73,v74,v75\n"
MF "v[116:119],a[244:247],a[208:211],v[116:119]\n"
"s_add_u32 s87,s90,12288\n"
CV "v74,v76,v77\n"
CV "v75,v78,v79\n"
MF "v[116:119],a[240:243],a[168:171],v[116:119]\n"
"buffer_store_dwordx4 v[72:75],v9,s[40:43],s87 offen sc1\n"
MF "v[116:119],v[188:191],a[128:131],v[116:119]\n"
"v_add_f32 v80,v80,v40\n"
"v_add_f32 v81,v81,v41\n"
MF "v[108:111],v[188:191],a[124:127],v[108:111]\n"
"v_add_f32 v82,v82,v42\n"
"v_add_f32 v83,v83,v43\n"
MF "v[108:111],a[240:243],a[164:167],v[108:111]\n"
"v_add_f32 v84,v84,v44\n"
"v_add_f32 v85,v85,v45\n"
MF "v[108:111],a[244:247],a[204:207],v[108:111]\n"
"v_add_f32 v86,v86,v46\n"
"v_add_f32 v87,v87,v47\n"
MF "v[100:103],a[244:247],a[200:203],v[100:103]\n"
CV "v80,v80,v81\n"
CV "v81,v82,v83\n"
MF "v[100:103],a[240:243],a[160:163],v[100:103]\n"
"s_add_u32 s87,s90,13312\n"
CV "v82,v84,v85\n"
CV "v83,v86,v87\n"
MF "v[100:103],v[188:191],a[120:123],v[100:103]\n"
"s_waitcnt lgkmcnt(0)\n"
"buffer_store_dwordx4 v[80:83],v9,s[40:43],s87 offen sc1\n"
MF "v[136:139],v[176:179],a[120:123],v[136:139]\n"
"s_nop 0\n"
TR "v[188:189],v3\n"
TR "v[190:191],v3 offset:2048\n"
MF "v[136:139],v[180:183],a[160:163],v[136:139]\n"
"s_nop 0\n"
TR "a[240:241],v3 offset:4096\n"
TR "a[242:243],v3 offset:6144\n"
MF "v[136:139],v[184:187],a[200:203],v[136:139]\n"
"s_nop 0\n"
TR "a[244:245],v3 offset:8192\n"
TR "a[246:247],v3 offset:10240\n"
MF "v[144:147],v[184:187],a[204:207],v[144:147]\n"
"v_add_f32 v88,v88,v48\n"
"v_add_f32 v89,v89,v49\n"
MF "v[144:147],v[180:183],a[164:167],v[144:147]\n"
"v_add_f32 v90,v90,v50\n"
"v_add_f32 v91,v91,v51\n"
MF "v[144:147],v[176:179],a[124:127],v[144:147]\n"
"v_add_f32 v92,v92,v52\n"
"v_add_f32 v93,v93,v53\n"
MF "v[152:155],v[176:179],a[128:131],v[152:155]\n"
"v_add_f32 v94,v94,v54\n"
"v_add_f32 v95,v95,v55\n"
MF "v[152:155],v[180:183],a[168:171],v[152:155]\n"
CV "v88,v88,v89\n"
CV "v89,v90,v91\n"
MF "v[152:155],v[184:187],a[208:211],v[152:155]\n"
"s_add_u32 s87,s90,14336\n"
CV "v90,v92,v93\n"
CV "v91,v94,v95\n"
MF "v[160:163],v[184:187],a[212:215],v[160:163]\n"
"buffer_store_dwordx4 v[88:91],v9,s[40:43],s87 offen sc1\n"
MF "v[160:163],v[180:183],a[172:175],v[160:163]\n"
MF "v[160:163],v[176:179],a[132:135],v[160:163]\n"
MF "v[168:171],v[176:179],a[136:139],v[168:171]\n"
MF "v[168:171],v[180:183],a[176:179],v[168:171]\n"
"s_waitcnt vmcnt(19) lgkmcnt(0)\n"
MF "v[168:171],v[184:187],a[216:219],v[168:171]\n"
"s_barrier\n"
MF "v[172:175],a[244:247],a[216:219],v[172:175]\n"
"s_nop 0\n"
TR "v[176:177],v0 offset:49152\n"
TR "v[178:179],v0 offset:51200\n"
MF "v[172:175],a[240:243],a[176:179],v[172:175]\n"
"s_nop 0\n"
TR "v[180:181],v0 offset:53248\n"
TR "v[182:183],v0 offset:55296\n"
MF "v[172:175],v[188:191],a[136:139],v[172:175]\n"
"s_nop 0\n"
TR "v[184:185],v0 offset:57344\n"
TR "v[186:187],v0 offset:59392\n"
MF "v[164:167],v[188:191],a[132:135],v[164:167]\n"
"buffer_load_dword v10,v12,s[40:43],s86 offen sc1\n"
MF "v[164:167],a[240:243],a[172:175],v[164:167]\n"
MF "v[164:167],a[244:247],a[212:215],v[164:167]\n"
MF "v[156:159],a[244:247],a[208:211],v[156:159]\n"
MF "v[156:159],a[240:243],a[168:171],v[156:159]\n"
MF "v[156:159],v[188:191],a[128:131],v[156:159]\n"
MF "v[148:151],v[188:191],a[124:127],v[148:151]\n"
MF "v[148:151],a[240:243],a[164:167],v[148:151]\n"
MF "v[148:151],a[244:247],a[204:207],v[148:151]\n"
MF "v[140:143],a[244:247],a[200:203],v[140:143]\n"
MF "v[140:143],a[240:243],a[160:163],v[140:143]\n"
MF "v[140:143],v[188:191],a[120:123],v[140:143]\n"
"s_waitcnt lgkmcnt(0)\n"
MF "v[96:99],v[176:179],a[20:23],v[96:99]\n"
"s_nop 0\n"
TR "v[188:189],v1 offset:49152\n"
TR "v[190:191],v1 offset:51200\n"
MF "v[96:99],v[180:183],a[60:63],v[96:99]\n"
"s_nop 0\n"
TR "a[240:241],v1 offset:53248\n"
TR "a[242:243],v1 offset:55296\n"
MF "v[96:99],v[184:187],a[100:103],v[96:99]\n"
"s_nop 0\n"
TR "a[244:245],v1 offset:57344\n"
TR "a[246:247],v1 offset:59392\n"
MF "v[104:107],v[184:187],a[104:107],v[104:107]\n"
MF "v[104:107],v[180:183],a[64:67],v[104:107]\n"
MF "v[104:107],v[176:179],a[24:27],v[104:107]\n"
"s_waitcnt vmcnt(18)\n"
"v_mov_b32 v236,v228\n"
"v_mov_b32 v237,v229\n"
MF "v[96:99],v[208:211],a[252:255],v[96:99]\n"
MF "v[104:107],v[212:215],a[252:255],v[104:107]\n"
MF "v[112:115],v[216:219],a[252:255],v[112:115]\n"
MF "v[120:123],v[220:223],a[252:255],v[120:123]\n"
MF "v[128:131],v[224:227],a[252:255],v[128:131]\n"
MF "v[100:103],v[208:211],a[248:251],v[100:103]\n"
MF "v[108:111],v[212:215],a[248:251],v[108:111]\n"
MF "v[116:119],v[216:219],a[248:251],v[116:119]\n"
MF "v[124:127],v[220:223],a[248:251],v[124:127]\n"
MF "v[132:135],v[224:227],a[248:251],v[132:135]\n"
MF "v[248:251],v[230:233],a[250:253],v[248:251]\n"
MF "v[244:247],v[234:237],a[250:253],v[244:247]\n"
MF "v[112:115],v[176:179],a[28:31],v[112:115]\n"
MF "v[112:115],v[180:183],a[68:71],v[112:115]\n"
MF "v[112:115],v[184:187],a[108:111],v[112:115]\n"
MF "v[120:123],v[184:187],a[112:115],v[120:123]\n"
"s_nop 3\n"
CV "v244,v244,v245\n"
CV "v245,v246,v247\n"
MF "v[120:123],v[180:183],a[72:75],v[120:123]\n"
"s_bfe_u32 s87,s22,0x30014\n"
CV "v246,v248,v249\n"
CV "v247,v250,v251\n"
MF "v[120:123],v[176:179],a[32:35],v[120:123]\n"
"s_lshl_b32 s87,s87,7\n"
"v_permlane16_swap_b32 v244,v246\n"
"v_permlane16_swap_b32 v245,v247\n"
MF "v[128:131],v[176:179],a[36:39],v[128:131]\n"
"buffer_store_dwordx4 v[244:247],v8,s[44:47],s0 offen sc1\n"
MF "v[128:131],v[180:183],a[76:79],v[128:131]\n"
"s_and_b32 s60,s56,1\n"
MF "v[128:131],v[184:187],a[116:119],v[128:131]\n"
"s_waitcnt lgkmcnt(0)\n"
MF "v[132:135],a[244:247],a[116:119],v[132:135]\n"
"s_mul_i32 s60,s60,204800\n"
TR "v[176:177],v2 offset:49152\n"
TR "v[178:179],v2 offset:51200\n"
MF "v[132:135],a[240:243],a[76:79],v[132:135]\n"
"s_add_u32 s87,s87,s60\n"
TR "v[180:181],v2 offset:53248\n"
TR "v[182:183],v2 offset:55296\n"
MF "v[132:135],v[188:191],a[36:39],v[132:135]\n"
"s_mov_b32 s71,204800\n"
TR "v[184:185],v2 offset:57344\n"
TR "v[186:187],v2 offset:59392\n"
MF "v[124:127],v[188:191],a[32:35],v[124:127]\n"
"s_mov_b32 s66,204800\n"
MF "v[124:127],a[240:243],a[72:75],v[124:127]\n"
"s_add_u32 s72,s78,2\n"
MF "v[124:127],a[244:247],a[112:115],v[124:127]\n"
"s_and_b32 s72,s72,15\n"
MF "v[116:119],a[244:247],a[108:111],v[116:119]\n"
"s_bfe_u32 s60,s22,0x10014\n"
MF "v[116:119],a[240:243],a[68:71],v[116:119]\n"
"s_lshl_b32 s60,s60,5\n"
MF "v[116:119],v[188:191],a[28:31],v[116:119]\n"
"v_xor_b32 v242,s60,v8\n"
MF "v[108:111],v[188:191],a[24:27],v[108:111]\n"
"s_add_u32 s16,s78,6\n"
MF "v[108:111],a[240:243],a[64:67],v[108:111]\n"
"s_and_b32 s16,s16,15\n"
MF "v[108:111],a[244:247],a[104:107],v[108:111]\n"
"s_add_u32 s17,s78,4\n"
MF "v[100:103],a[244:247],a[100:103],v[100:103]\n"
"s_and_b32 s17,s17,15\n"
MF "v[100:103],a[240:243],a[60:63],v[100:103]\n"
MF "v[100:103],v[188:191],a[20:23],v[100:103]\n"
"s_waitcnt lgkmcnt(0)\n"
MF "v[136:139],v[176:179],a[20:23],v[136:139]\n"
"s_nop 0\n"
TR "v[188:189],v3 offset:49152\n"
TR "v[190:191],v3 offset:51200\n"
MF "v[136:139],v[180:183],a[60:63],v[136:139]\n"
"s_nop 0\n"
TR "a[240:241],v3 offset:53248\n"
TR "a[242:243],v3 offset:55296\n"
MF "v[136:139],v[184:187],a[100:103],v[136:139]\n"
"s_nop 0\n"
TR "a[244:245],v3 offset:57344\n"
TR "a[246:247],v3 offset:59392\n"
MF "v[144:147],v[184:187],a[104:107],v[144:147]\n"
MF "v[144:147],v[180:183],a[64:67],v[144:147]\n"
MF "v[144:147],v[176:179],a[24:27],v[144:147]\n"
MF "v[152:155],v[176:179],a[28:31],v[152:155]\n"
MF "v[152:155],v[180:183],a[68:71],v[152:155]\n"
MF "v[152:155],v[184:187],a[108:111],v[152:155]\n"
MF "v[160:163],v[184:187],a[112:115],v[160:163]\n"
MF "v[160:163],v[180:183],a[72:75],v[160:163]\n"
MF "v[160:163],v[176:179],a[32:35],v[160:163]\n"
MF "v[168:171],v[176:179],a[36:39],v[168:171]\n"
MF "v[168:171],v[180:183],a[76:79],v[168:171]\n"
"s_waitcnt vmcnt(1) lgkmcnt(0)\n"
MF "v[168:171],v[184:187],a[116:119],v[168:171]\n"
"s_barrier\n"
MF "v[172:175],a[244:247],a[116:119],v[172:175]\n"
"v_sub_u32 v10,v10,v11\n"
MF "v[172:175],a[240:243],a[76:79],v[172:175]\n"
"s_nop 0\n"
TR "v[176:177],v4\n"
TR "v[178:179],v4 offset:2048\n"
MF "v[172:175],v[188:191],a[36:39],v[172:175]\n"
"s_nop 0\n"
TR "v[180:181],v4 offset:4096\n"
TR "v[182:183],v4 offset:6144\n"
MF "v[164:167],v[188:191],a[32:35],v[164:167]\n"
"s_nop 0\n"
TR "v[184:185],v4 offset:8192\n"
TR "v[186:187],v4 offset:10240\n"
MF "v[164:167],a[240:243],a[72:75],v[164:167]\n"
"v_readlane_b32 s16,v10,s16\n"
MF "v[164:167],a[244:247],a[112:115],v[164:167]\n"
"s_cmp_lt_u32 s16,9\n"
"s_cselect_b32 s16,0xf0000000,0\n"
MF "v[156:159],a[244:247],a[108:111],v[156:159]\n"
"v_readlane_b32 s17,v10,s17\n"
MF "v[156:159],a[240:243],a[68:71],v[156:159]\n"
"s_cmp_lt_u32 s17,10\n"
"s_cselect_b32 s17,0xf0000000,0\n"
MF "v[156:159],v[188:191],a[28:31],v[156:159]\n"
"s_bfe_u32 s62,s22,0x30014\n"
MF "v[148:151],v[188:191],a[24:27],v[148:151]\n"
"s_add_u32 s62,s62,3\n"
"s_and_b32 s62,s62,7\n"
"s_mul_i32 s62,s62,204800\n"
MF "v[148:151],a[240:243],a[64:67],v[148:151]\n"
"s_add_u32 s62,s62,s73\n"
"s_add_u32 s62,s62,133120\n"
"s_or_b32 s62,s62,s16\n"
MF "v[148:151],a[244:247],a[104:107],v[148:151]\n"
LD "v[16:19],v9,s[40:43],s62 offen sc1\n"
MF "v[140:143],a[244:247],a[100:103],v[140:143]\n"
LD "v[24:27],v9,s[40:43],s62 offen offset:1024 sc1\n"
MF "v[140:143],a[240:243],a[60:63],v[140:143]\n"
MF "v[140:143],v[188:191],a[20:23],v[140:143]\n"
"s_waitcnt lgkmcnt(0)\n"
LD "v[32:35],v9,s[40:43],s62 offen offset:2048 sc1\n"
MF "v[96:99],v[176:179],a[140:143],v[96:99]\n"
"s_nop 0\n"
TR "v[188:189],v5\n"
TR "v[190:191],v5 offset:2048\n"
MF "v[96:99],v[180:183],a[180:183],v[96:99]\n"
"s_nop 0\n"
TR "a[240:241],v5 offset:4096\n"
TR "a[242:243],v5 offset:6144\n"
MF "v[96:99],v[184:187],a[220:223],v[96:99]\n"
"s_nop 0\n"
TR "a[244:245],v5 offset:8192\n"
TR "a[246:247],v5 offset:10240\n"
MF "v[104:107],v[184:187],a[224:227],v[104:107]\n"
LD "v[40:43],v9,s[40:43],s62 offen offset:3072 sc1\n"
MF "v[104:107],v[180:183],a[184:187],v[104:107]\n"
MF "v[104:107],v[176:179],a[144:147],v[104:107]\n"
"s_add_u32 s62,s62,4096\n"
MF "v[112:115],v[176:179],a[148:151],v[112:115]\n"
LD "v[48:51],v9,s[40:43],s62 offen sc1\n"
MF "v[112:115],v[180:183],a[188:191],v[112:115]\n"
"s_bfe_u32 s62,s22,0x30014\n"
"ds_write_addtid_b32 v96 offset:16384\n"
"ds_write_addtid_b32 v97 offset:16640\n"
MF "v[112:115],v[184:187],a[228:231],v[112:115]\n"
"s_add_u32 s62,s62,2\n"
"s_and_b32 s62,s62,7\n"
"s_mul_i32 s62,s62,204800\n"
MF "v[120:123],v[184:187],a[232:235],v[120:123]\n"
"s_add_u32 s62,s62,s73\n"
"s_add_u32 s62,s62,153600\n"
"s_or_b32 s62,s62,s17\n"
MF "v[120:123],v[180:183],a[192:195],v[120:123]\n"
LD "v[20:23],v9,s[40:43],s62 offen sc1\n"
MF "v[120:123],v[176:179],a[152:155],v[120:123]\n"
"s_nop 0\n"
"ds_write_addtid_b32 v98 offset:16896\n"
"ds_write_addtid_b32 v99 offset:17152\n"
MF "v[128:131],v[176:179],a[156:159],v[128:131]\n"
LD "v[28:31],v9,s[40:43],s62 offen offset:1024 sc1\n"
MF "v[128:131],v[180:183],a[196:199],v[128:131]\n"
"s_nop 0\n"
"ds_write_addtid_b32 v104 offset:18432\n"
"ds_write_addtid_b32 v105 offset:18688\n"
MF "v[128:131],v[184:187],a[236:239],v[128:131]\n"
"s_waitcnt lgkmcnt(6)\n"
LD "v[36:39],v9,s[40:43],s62 offen offset:2048 sc1\n"
MF "v[100:103],v[188:191],a[140:143],v[100:103]\n"
"s_nop 0\n"
TR "v[176:177],v6\n"
TR "v[178:179],v6 offset:2048\n"
MF "v[100:103],a[240:243],a[180:183],v[100:103]\n"
"s_nop 0\n"
TR "v[180:181],v6 offset:4096\n"
TR "v[182:183],v6 offset:6144\n"
MF "v[100:103],a[244:247],a[220:223],v[100:103]\n"
"s_nop 0\n"
TR "v[184:185],v6 offset:8192\n"
TR "v[186:187],v6 offset:10240\n"
MF "v[108:111],a[244:247],a[224:227],v[108:111]\n"
LD "v[44:47],v9,s[40:43],s62 offen offset:3072 sc1\n"
MF "v[108:111],a[240:243],a[184:187],v[108:111]\n"
"s_nop 0\n"
"ds_write_addtid_b32 v106 offset:18944\n"
"ds_write_addtid_b32 v107 offset:19200\n"
MF "v[108:111],v[188:191],a[144:147],v[108:111]\n"
"s_add_u32 s62,s62,4096\n"
"ds_write_addtid_b32 v112 offset:20480\n"
"ds_write_addtid_b32 v113 offset:20736\n"
MF "v[116:119],v[188:191],a[148:151],v[116:119]\n"
LD "v[52:55],v9,s[40:43],s62 offen sc1\n"
MF "v[116:119],a[240:243],a[188:191],v[116:119]\n"
"s_nop 0\n"
"ds_write_addtid_b32 v114 offset:20992\n"
"ds_write_addtid_b32 v115 offset:21248\n"
MF "v[116:119],a[244:247],a[228:231],v[116:119]\n"
"s_nop 0\n"
"ds_write_addtid_b32 v120 offset:22528\n"
"ds_write_addtid_b32 v121 offset:22784\n"
MF "v[124:127],a[244:247],a[232:235],v[124:127]\n"
"s_nop 0\n"
"ds_write_addtid_b32 v122 offset:23040\n"
"ds_write_addtid_b32 v123 offset:23296\n"
MF "v[124:127],a[240:243],a[192:195],v[124:127]\n"
"s_nop 0\n"
"ds_write_addtid_b32 v128 offset:24576\n"
"ds_write_addtid_b32 v129 offset:24832\n"
MF "v[124:127],v[188:191],a[152:155],v[124:127]\n"
"buffer_load_dword v10,v12,s[40:43],s86 offen sc1\n"
MF "v[132:135],v[188:191],a[156:159],v[132:135]\n"
"s_nop 0\n"
"ds_write_addtid_b32 v130 offset:25088\n"
"ds_write_addtid_b32 v131 offset:25344\n"
MF "v[132:135],a[240:243],a[196:199],v[132:135]\n"
"s_nop 0\n"
"ds_write_addtid_b32 v100 offset:17408\n"
"ds_write_addtid_b32 v101 offset:17664\n"
MF "v[132:135],a[244:247],a[236:239],v[132:135]\n"
"s_waitcnt lgkmcnt(15)\n"
"ds_write_addtid_b32 v102 offset:17920\n"
"ds_write_addtid_b32 v103 offset:18176\n"
MF "v[136:139],v[176:179],a[140:143],v[136:139]\n"
"s_nop 0\n"
TR "v[188:189],v7\n"
TR "v[190:191],v7 offset:2048\n"
MF "v[136:139],v[180:183],a[180:183],v[136:139]\n"
"s_nop 0\n"
TR "a[240:241],v7 offset:4096\n"
TR "a[242:243],v7 offset:6144\n"
MF "v[136:139],v[184:187],a[220:223],v[136:139]\n"
"s_nop 0\n"
TR "a[244:245],v7 offset:8192\n"
TR "a[246:247],v7 offset:10240\n"
MF "v[144:147],v[184:187],a[224:227],v[144:147]\n"
"s_nop 0\n"
"ds_write_addtid_b32 v108 offset:19456\n"
"ds_write_addtid_b32 v109 offset:19712\n"
MF "v[144:147],v[180:183],a[184:187],v[144:147]\n"
"s_nop 0\n"
"ds_write_addtid_b32 v110 offset:19968\n"
"ds_write_addtid_b32 v111 offset:20224\n"
MF "v[144:147],v[176:179],a[144:147],v[144:147]\n"
"s_nop 0\n"
"ds_write_addtid_b32 v116 offset:21504\n"
"ds_write_addtid_b32 v117 offset:21760\n"
MF "v[152:155],v[176:179],a[148:151],v[152:155]\n"
"s_nop 0\n"
"ds_write_addtid_b32 v118 offset:22016\n"
"ds_write_addtid_b32 v119 offset:22272\n"
MF "v[152:155],v[180:183],a[188:191],v[152:155]\n"
"ds_write_addtid_b32 v124 offset:23552\n"
MF "v[152:155],v[184:187],a[228:231],v[152:155]\n"
"ds_write_addtid_b32 v125 offset:23808\n"
MF "v[160:163],v[184:187],a[232:235],v[160:163]\n"
"ds_write_addtid_b32 v126 offset:24064\n"
MF "v[160:163],v[180:183],a[192:195],v[160:163]\n"
"ds_write_addtid_b32 v127 offset:24320\n"
MF "v[160:163],v[176:179],a[152:155],v[160:163]\n"
"ds_write_addtid_b32 v132 offset:25600\n"
MF "v[168:171],v[176:179],a[156:159],v[168:171]\n"
"ds_write_addtid_b32 v133 offset:25856\n"
MF "v[168:171],v[180:183],a[196:199],v[168:171]\n"
"ds_write_addtid_b32 v134 offset:26112\n"
MF "v[168:171],v[184:187],a[236:239],v[168:171]\n"
"s_waitcnt lgkmcnt(15)\n"
"ds_write_addtid_b32 v135 offset:26368\n"
MF "v[140:143],v[188:191],a[140:143],v[140:143]\n"
MF "v[140:143],a[240:243],a[180:183],v[140:143]\n"
MF "v[140:143],a[244:247],a[220:223],v[140:143]\n"
MF "v[148:151],a[244:247],a[224:227],v[148:151]\n"
"s_waitcnt lgkmcnt(0)\n"
MF "v[148:151],a[240:243],a[184:187],v[148:151]\n"
"s_barrier\n"
MF "v[148:151],v[188:191],a[144:147],v[148:151]\n"
"ds_read2st64_b32 v[56:57],v13 offset0:0 offset1:1\n"
MF "v[156:159],v[188:191],a[148:151],v[156:159]\n"
"ds_read2st64_b32 v[58:59],v13 offset0:2 offset1:3\n"
MF "v[156:159],a[240:243],a[188:191],v[156:159]\n"
"ds_read2st64_b32 v[60:61],v13 offset0:4 offset1:5\n"
MF "v[156:159],a[244:247],a[228:231],v[156:159]\n"
"ds_read2st64_b32 v[62:63],v13 offset0:6 offset1:7\n"
MF "v[164:167],a[244:247],a[232:235],v[164:167]\n"
"ds_read2st64_b32 v[64:65],v13 offset0:8 offset1:9\n"
MF "v[164:167],a[240:243],a[192:195],v[164:167]\n"
"ds_read2st64_b32 v[66:67],v13 offset0:10 offset1:11\n"
MF "v[164:167],v[188:191],a[152:155],v[164:167]\n"
"ds_read2st64_b32 v[68:69],v13 offset0:12 offset1:13\n"
MF "v[172:175],v[188:191],a[156:159],v[172:175]\n"
"ds_read2st64_b32 v[70:71],v13 offset0:14 offset1:15\n"
MF "v[172:175],a[240:243],a[196:199],v[172:175]\n"
"ds_read2st64_b32 v[72:73],v13 offset0:16 offset1:17\n"
MF "v[172:175],a[244:247],a[236:239],v[172:175]\n"
"ds_read2st64_b32 v[74:75],v13 offset0:18 offset1:19\n"
"ds_read2st64_b32 v[76:77],v13 offset0:20 offset1:21\n"
"ds_read2st64_b32 v[78:79],v13 offset0:22 offset1:23\n"
"s_nop 0\n"
"ds_read2st64_b32 v[80:81],v13 offset0:24 offset1:25\n"
"ds_read2st64_b32 v[82:83],v13 offset0:26 offset1:27\n"
"ds_read2st64_b32 v[84:85],v13 offset0:28 offset1:29\n"
"ds_read2st64_b32 v[86:87],v13 offset0:30 offset1:31\n"
"ds_read2st64_b32 v[88:89],v13 offset0:32 offset1:33\n"
"ds_read2st64_b32 v[90:91],v13 offset0:34 offset1:35\n"
"ds_read2st64_b32 v[92:93],v13 offset0:36 offset1:37\n"
"ds_read2st64_b32 v[94:95],v13 offset0:38 offset1:39\n"
"s_waitcnt vmcnt(0)\n"
"v_sub_u32 v10,v10,v11\n"
"s_cmp_eq_u32 s16,0\n"
"s_cbranch_scc1 .Lhave03\n"
"s_add_u32 s59,s78,6\n"
"s_and_b32 s59,s59,15\n"
".Lpg31h:\n"
"s_cbranch_execz .Lpg34h\n"
"s_nop 0\n"
"s_nop 3\n"
".Lnf3:\n"
"v_readlane_b32 s58,v10,s59\n"
"s_cmp_lt_u32 s58,9\n"
"s_cbranch_scc1 .Lsp3\n"
".Lnb3:\n"
"s_bfe_u32 s62,s22,0x30014\n"
"s_add_u32 s62,s62,3\n"
"s_and_b32 s62,s62,7\n"
"s_mul_i32 s62,s62,204800\n"
"s_add_u32 s62,s62,s73\n"
"s_add_u32 s62,s62,133120\n"
LD "v[16:19],v9,s[40:43],s62 offen sc1\n"
LD "v[24:27],v9,s[40:43],s62 offen offset:1024 sc1\n"
LD "v[32:35],v9,s[40:43],s62 offen offset:2048 sc1\n"
LD "v[40:43],v9,s[40:43],s62 offen offset:3072 sc1\n"
"s_add_u32 s62,s62,4096\n"
LD "v[48:51],v9,s[40:43],s62 offen sc1\n"
".Lhave03:\n"
"s_cmp_eq_u32 s17,0\n"
"s_cbranch_scc1 .Lhave02\n"
"s_add_u32 s59,s78,4\n"
"s_and_b32 s59,s59,15\n"
"s_nop 3\n"
".Lnf4:\n"
"v_readlane_b32 s58,v10,s59\n"
"s_cmp_lt_u32 s58,10\n"
"s_cbranch_scc1 .Lsp4\n"
".Lnb4:\n"
"s_bfe_u32 s62,s22,0x30014\n"
"s_add_u32 s62,s62,2\n"
"s_and_b32 s62,s62,7\n"
"s_mul_i32 s62,s62,204800\n"
"s_add_u32 s62,s62,s73\n"
"s_add_u32 s62,s62,153600\n"
LD "v[20:23],v9,s[40:43],s62 offen sc1\n"
LD "v[28:31],v9,s[40:43],s62 offen offset:1024 sc1\n"
LD "v[36:39],v9,s[40:43],s62 offen offset:2048 sc1\n"
LD "v[44:47],v9,s[40:43],s62 offen offset:3072 sc1\n"
"s_add_u32 s62,s62,4096\n"
LD "v[52:55],v9,s[40:43],s62 offen sc1\n"
".Lhave02:\n"
".Lnf5:\n"
"v_readlane_b32 s58,v10,s72\n"
"s_cmp_lt_u32 s58,11\n"
"s_cbranch_scc1 .Lsp5\n"
".Lnb5:\n"
"s_bfe_u32 s62,s22,0x30014\n"
"s_add_u32 s62,s62,1\n"
"s_and_b32 s62,s62,7\n"
"s_mul_i32 s62,s62,204800\n"
"s_add_u32 s62,s62,s73\n"
"s_add_u32 s62,s62,174080\n"
LD "v[208:211],v9,s[40:43],s62 offen sc1\n"
LD "v[212:215],v9,s[40:43],s62 offen offset:1024 sc1\n"
LD "v[216:219],v9,s[40:43],s62 offen offset:2048 sc1\n"
LD "v[220:223],v9,s[40:43],s62 offen offset:3072 sc1\n"
"s_add_u32 s62,s62,4096\n"
LD "v[224:227],v9,s[40:43],s62 offen sc1\n"
"s_waitcnt lgkmcnt(15)\n"
"v_pk_add_f32 v[136:137],v[136:137],v[56:57]\n"
"v_pk_add_f32 v[138:139],v[138:139],v[58:59]\n"
"v_pk_add_f32 v[140:141],v[140:141],v[60:61]\n"
"v_pk_add_f32 v[142:143],v[142:143],v[62:63]\n"
"v_pk_add_f32 v[144:145],v[144:145],v[64:65]\n"
"s_waitcnt lgkmcnt(14)\n"
"v_pk_add_f32 v[146:147],v[146:147],v[66:67]\n"
"s_waitcnt lgkmcnt(13)\n"
"v_pk_add_f32 v[148:149],v[148:149],v[68:69]\n"
"s_waitcnt lgkmcnt(12)\n"
"v_pk_add_f32 v[150:151],v[150:151],v[70:71]\n"
"s_waitcnt lgkmcnt(11)\n"
"v_pk_add_f32 v[152:153],v[152:153],v[72:73]\n"
"s_waitcnt lgkmcnt(10)\n"
"v_pk_add_f32 v[154:155],v[154:155],v[74:75]\n"
"s_waitcnt lgkmcnt(9)\n"
"v_pk_add_f32 v[156:157],v[156:157],v[76:77]\n"
"s_waitcnt lgkmcnt(8)\n"
"v_pk_add_f32 v[158:159],v[158:159],v[78:79]\n"
"s_waitcnt lgkmcnt(7)\n"
"v_pk_add_f32 v[160:161],v[160:161],v[80:81]\n"
"s_waitcnt lgkmcnt(6)\n"
"v_pk_add_f32 v[162:163],v[162:163],v[82:83]\n"
"s_waitcnt lgkmcnt(5)\n"
"v_pk_add_f32 v[164:165],v[164:165],v[84:85]\n"
"s_waitcnt lgkmcnt(4)\n"
"v_pk_add_f32 v[166:167],v[166:167],v[86:87]\n"
"s_waitcnt lgkmcnt(3)\n"
"v_pk_add_f32 v[168:169],v[168:169],v[88:89]\n"
"s_waitcnt lgkmcnt(2)\n"
"v_pk_add_f32 v[170:171],v[170:171],v[90:91]\n"
"s_waitcnt lgkmcnt(1)\n"
"v_pk_add_f32 v[172:173],v[172:173],v[92:93]\n"
"s_waitcnt lgkmcnt(0)\n"
"v_pk_add_f32 v[174:175],v[174:175],v[94:95]\n"
"s_waitcnt vmcnt(5)\n"
"s_nop 0\n"
MF "v[140:143],v[16:19],a[252:255],v[140:143]\n"
MF "v[148:151],v[24:27],a[252:255],v[148:151]\n"
MF "v[156:159],v[32:35],a[252:255],v[156:159]\n"
MF "v[164:167],v[40:43],a[252:255],v[164:167]\n"
MF "v[172:175],v[48:51],a[252:255],v[172:175]\n"
MF "v[136:139],v[16:19],a[248:251],v[136:139]\n"
MF "v[144:147],v[24:27],a[248:251],v[144:147]\n"
MF "v[152:155],v[32:35],a[248:251],v[152:155]\n"
MF "v[160:163],v[40:43],a[248:251],v[160:163]\n"
MF "v[168:171],v[48:51],a[248:251],v[168:171]\n"
MF "v[136:139],v[20:23],a[252:255],v[136:139]\n"
MF "v[144:147],v[28:31],a[252:255],v[144:147]\n"
MF "v[152:155],v[36:39],a[252:255],v[152:155]\n"
MF "v[160:163],v[44:47],a[252:255],v[160:163]\n"
MF "v[168:171],v[52:55],a[252:255],v[168:171]\n"
MF "v[140:143],v[20:23],a[248:251],v[140:143]\n"
MF "v[148:151],v[28:31],a[248:251],v[148:151]\n"
MF "v[156:159],v[36:39],a[248:251],v[156:159]\n"
MF "v[164:167],v[44:47],a[248:251],v[164:167]\n"
MF "v[172:175],v[52:55],a[248:251],v[172:175]\n"
"s_waitcnt vmcnt(0)\n"
MF "v[140:143],v[208:211],a[252:255],v[140:143]\n"
MF "v[136:139],v[208:211],a[248:251],v[136:139]\n"
MF "v[148:151],v[212:215],a[252:255],v[148:151]\n"
MF "v[144:147],v[212:215],a[248:251],v[144:147]\n"
MF "v[156:159],v[216:219],a[252:255],v[156:159]\n"
"s_nop 4\n"
CV "v136,v136,v137\n"
CV "v137,v138,v139\n"
MF "v[152:155],v[216:219],a[248:251],v[152:155]\n"
CV "v138,v140,v141\n"
CV "v139,v142,v143\n"
MF "v[164:167],v[220:223],a[252:255],v[164:167]\n"
CV "v144,v144,v145\n"
CV "v145,v146,v147\n"
MF "v[160:163],v[220:223],a[248:251],v[160:163]\n"
CV "v146,v148,v149\n"
CV "v147,v150,v151\n"
"v_permlane16_swap_b32 v136,v138\n"
"v_permlane16_swap_b32 v137,v139\n"
"buffer_store_dwordx4 v[136:139],v242,s[44:47],s87 offen offset:64 sc1\n"
"s_add_u32 s87,s87,40960\n"
MF "v[172:175],v[224:227],a[252:255],v[172:175]\n"
CV "v152,v152,v153\n"
CV "v153,v154,v155\n"
MF "v[168:171],v[224:227],a[248:251],v[168:171]\n"
CV "v154,v156,v157\n"
CV "v155,v158,v159\n"
"v_permlane16_swap_b32 v144,v146\n"
"v_permlane16_swap_b32 v145,v147\n"
"buffer_store_dwordx4 v[144:147],v242,s[44:47],s87 offen offset:64 sc1\n"
"s_add_u32 s87,s87,40960\n"
CV "v160,v160,v161\n"
CV "v161,v162,v163\n"
CV "v162,v164,v165\n"
CV "v163,v166,v167\n"
"v_permlane16_swap_b32 v152,v154\n"
"v_permlane16_swap_b32 v153,v155\n"
"buffer_store_dwordx4 v[152:155],v242,s[44:47],s87 offen offset:64 sc1\n"
"s_add_u32 s87,s87,40960\n"
CV "v168,v168,v169\n"
CV "v169,v170,v171\n"
CV "v170,v172,v173\n"
CV "v171,v174,v175\n"
"v_permlane16_swap_b32 v160,v162\n"
"v_permlane16_swap_b32 v161,v163\n"
"buffer_store_dwordx4 v[160:163],v242,s[44:47],s87 offen offset:64 sc1\n"
"s_add_u32 s87,s87,40960\n"
"v_permlane16_swap_b32 v168,v170\n"
"v_permlane16_swap_b32 v169,v171\n"
"buffer_store_dwordx4 v[168:171],v242,s[44:47],s87 offen offset:64 sc1\n"
"s_add_u32 s87,s87,40960\n"
"s_endpgm\n"
".Lpgrole1_h:\n"
"s_cbranch_execz .Lpg35h\n"
".Lrole1:\n"
"s_waitcnt lgkmcnt(4)\n"
MF "v[16:19],a[120:123],v[176:179],v[252:255]\n"
"s_mov_b32 s65,128\n"
TR "v[188:189],v1 offset:24576\n"
AR "v220,a44\n"
"s_waitcnt lgkmcnt(3)\n"
MF "v[16:19],a[160:163],v[180:183],v[16:19]\n"
"s_mov_b32 s9,0x3f80\n"
TR "v[190:191],v1 offset:26624\n"
AR "v221,a45\n"
"s_waitcnt lgkmcnt(2)\n"
MF "v[16:19],a[200:203],v[184:187],v[16:19]\n"
AR "v222,a46\n"
AR "v223,a47\n"
MF "v[24:27],a[204:207],v[184:187],v[252:255]\n"
"s_bfe_u32 s58,s22,0x30014\n"
TR "a[240:241],v1 offset:28672\n"
AR "v224,a64\n"
MF "v[24:27],a[164:167],v[180:183],v[24:27]\n"
"s_add_u32 s61,s58,1\n"
TR "a[242:243],v1 offset:30720\n"
AR "v225,a65\n"
MF "v[24:27],a[124:127],v[176:179],v[24:27]\n"
"s_and_b32 s61,s61,7\n"
AR "v226,a66\n"
AR "v227,a67\n"
MF "v[32:35],a[128:131],v[176:179],v[252:255]\n"
"s_mov_b32 s64,9\n"
TR "a[244:245],v1 offset:32768\n"
"ds_read_b128 a[64:67],v236 offset:49152\n"
MF "v[32:35],a[168:171],v[180:183],v[32:35]\n"
"s_xor_b32 s67,s78,8\n"
TR "a[246:247],v1 offset:34816\n"
"v_fma_f32 v220,v220,v209,v215\n"
MF "v[32:35],a[208:211],v[184:187],v[32:35]\n"
"s_mov_b32 s71,204800\n"
"v_fma_f32 v221,v221,v209,v215\n"
"v_fma_f32 v222,v222,v209,v215\n"
MF "v[40:43],a[212:215],v[184:187],v[252:255]\n"
"s_mov_b32 s66,204800\n"
"v_fma_f32 v223,v223,v209,v215\n"
"v_fma_f32 v224,v224,v209,v215\n"
MF "v[40:43],a[172:175],v[180:183],v[40:43]\n"
"s_and_b32 s73,s56,1\n"
"v_fma_f32 v225,v225,v209,v215\n"
"v_fma_f32 v226,v226,v209,v215\n"
MF "v[40:43],a[132:135],v[176:179],v[40:43]\n"
"s_mul_i32 s73,s73,5120\n"
"v_fma_f32 v227,v227,v209,v215\n"
CV "v220,v220,v221\n"
MF "v[48:51],a[136:139],v[176:179],v[252:255]\n"
"s_add_u32 s73,s73,s93\n"
CV "v221,v222,v223\n"
CV "v222,v224,v225\n"
MF "v[48:51],a[176:179],v[180:183],v[48:51]\n"
"s_xor_b32 s58,s58,4\n"
CV "v223,v226,v227\n"
AW "a44,v220\n"
MF "v[48:51],a[216:219],v[184:187],v[48:51]\n"
"s_waitcnt lgkmcnt(0)\n"
AW "a45,v221\n"
AW "a46,v222\n"
MF "v[52:55],a[216:219],a[244:247],v[252:255]\n"
"s_mul_i32 s70,s58,204800\n"
TR "v[176:177],v2 offset:24576\n"
AW "a47,v223\n"
MF "v[52:55],a[176:179],a[240:243],v[52:55]\n"
"s_add_u32 s70,s70,s73\n"
TR "v[178:179],v2 offset:26624\n"
AR "v228,a84\n"
MF "v[52:55],a[136:139],v[188:191],v[52:55]\n"
"s_add_u32 s70,s70,0xc800\n"
AR "v229,a85\n"
AR "v230,a86\n"
MF "v[44:47],a[132:135],v[188:191],v[252:255]\n"
"s_mov_b32 s85,1\n"
TR "v[180:181],v2 offset:28672\n"
AR "v231,a87\n"
MF "v[44:47],a[172:175],a[240:243],v[44:47]\n"
"s_mov_b32 s83,0xf0000000\n"
TR "v[182:183],v2 offset:30720\n"
AR "v232,a104\n"
MF "v[44:47],a[212:215],a[244:247],v[44:47]\n"
"s_mov_b32 s82,0xf0000000\n"
AR "v233,a105\n"
AR "v234,a106\n"
MF "v[36:39],a[208:211],a[244:247],v[252:255]\n"
"s_add_u32 s58,s79,73728\n"
TR "v[184:185],v2 offset:32768\n"
AR "v235,a107\n"
MF "v[36:39],a[168:171],a[240:243],v[36:39]\n"
"s_mov_b32 s90,s91\n"
TR "v[186:187],v2 offset:34816\n"
"ds_read_b128 a[104:107],v237 offset:4096\n"
MF "v[36:39],a[128:131],v[188:191],v[36:39]\n"
"s_add_u32 s91,s91,20480\n"
"v_fma_f32 v228,v228,v209,v215\n"
"v_fma_f32 v229,v229,v209,v215\n"
MF "v[28:31],a[124:127],v[188:191],v[252:255]\n"
"s_bfe_u32 s74,s22,0x30004\n"
"v_fma_f32 v230,v230,v209,v215\n"
"v_fma_f32 v231,v231,v209,v215\n"
MF "v[28:31],a[164:167],a[240:243],v[28:31]\n"
"s_mul_i32 s74,s74,2\n"
"v_fma_f32 v232,v232,v209,v215\n"
"v_fma_f32 v233,v233,v209,v215\n"
MF "v[28:31],a[204:207],a[244:247],v[28:31]\n"
"s_add_u32 s74,s74,s56\n"
"v_fma_f32 v234,v234,v209,v215\n"
"v_fma_f32 v235,v235,v209,v215\n"
MF "v[20:23],a[200:203],a[244:247],v[252:255]\n"
"s_sub_u32 s74,s74,2\n"
CV "v228,v228,v229\n"
CV "v229,v230,v231\n"
MF "v[20:23],a[160:163],a[240:243],v[20:23]\n"
"s_mul_i32 s74,s74,30720\n"
CV "v230,v232,v233\n"
CV "v231,v234,v235\n"
MF "v[20:23],a[120:123],v[188:191],v[20:23]\n"
"s_waitcnt lgkmcnt(1)\n"
AW "a84,v228\n"
AW "a85,v229\n"
MF "v[56:59],a[120:123],v[176:179],v[252:255]\n"
"s_bfe_u32 s68,s22,0x10007\n"
TR "v[188:189],v3 offset:24576\n"
"v_add_u32 v198,59392,v192\n"
MF "v[56:59],a[160:163],v[180:183],v[56:59]\n"
"s_lshl_b32 s68,s68,3\n"
TR "v[190:191],v3 offset:26624\n"
"v_add_u32 v199,68608,v192\n"
MF "v[56:59],a[200:203],v[184:187],v[56:59]\n"
"s_mul_i32 s68,s68,491520\n"
"v_add_u32 v200,81920,v192\n"
"v_add_u32 v201,91136,v192\n"
MF "v[64:67],a[204:207],v[184:187],v[252:255]\n"
"s_add_u32 s74,s74,s68\n"
TR "a[240:241],v3 offset:28672\n"
"v_add_u32 v202,100352,v192\n"
MF "v[64:67],a[164:167],v[180:183],v[64:67]\n"
"s_bfe_u32 s68,s22,0x30014\n"
TR "a[242:243],v3 offset:30720\n"
"v_add_u32 v203,109568,v192\n"
MF "v[64:67],a[124:127],v[176:179],v[64:67]\n"
"s_mul_i32 s68,s68,491520\n"
AW "a86,v230\n"
AW "a87,v231\n"
MF "v[72:75],a[128:131],v[176:179],v[252:255]\n"
"s_add_u32 s74,s74,s68\n"
TR "a[244:245],v3 offset:32768\n"
AR "v220,a8\n"
MF "v[72:75],a[168:171],v[180:183],v[72:75]\n"
"s_bfe_u32 s68,s22,0x30014\n"
TR "a[246:247],v3 offset:34816\n"
AR "v221,a9\n"
MF "v[72:75],a[208:211],v[184:187],v[72:75]\n"
"s_add_u32 s68,s68,7\n"
AR "v222,a10\n"
AR "v223,a11\n"
MF "v[80:83],a[212:215],v[184:187],v[252:255]\n"
"s_lshl_b32 s68,s68,7\n"
AR "v224,a28\n"
AR "v225,a29\n"
MF "v[80:83],a[172:175],v[180:183],v[80:83]\n"
"s_and_b32 s68,s68,768\n"
AR "v226,a30\n"
AR "v227,a31\n"
MF "v[80:83],a[132:135],v[176:179],v[80:83]\n"
"s_bfe_u32 s4,s22,0x30014\n"
"ds_read_b128 a[28:31],v236 offset:32768\n"
"v_fma_f32 v220,v220,v210,v216\n"
MF "v[88:91],a[136:139],v[176:179],v[252:255]\n"
"s_add_u32 s4,s4,32\n"
"v_fma_f32 v221,v221,v210,v216\n"
"v_fma_f32 v222,v222,v210,v216\n"
MF "v[88:91],a[176:179],v[180:183],v[88:91]\n"
"s_waitcnt vmcnt(0) lgkmcnt(0)\n"
"v_fma_f32 v223,v223,v210,v216\n"
"v_fma_f32 v224,v224,v210,v216\n"
MF "v[88:91],a[216:219],v[184:187],v[88:91]\n"
"s_barrier\n"
MF "v[92:95],a[216:219],a[244:247],v[252:255]\n"
"s_add_u32 s89,s89,s65\n"
TR "v[176:177],v0\n"
"v_fma_f32 v225,v225,v210,v216\n"
MF "v[92:95],a[176:179],a[240:243],v[92:95]\n"
"s_add_u32 m0,s79,24576\n"
TR "v[178:179],v0 offset:2048\n"
"v_fma_f32 v226,v226,v210,v216\n"
MF "v[92:95],a[136:139],v[188:191],v[92:95]\n"
LD "v192,s[48:51],s89 offen lds\n"
MF "v[84:87],a[132:135],v[188:191],v[252:255]\n"
"s_sub_u32 s4,s4,s92\n"
TR "v[180:181],v0 offset:4096\n"
"v_fma_f32 v227,v227,v210,v216\n"
MF "v[84:87],a[172:175],a[240:243],v[84:87]\n"
"s_cmp_eq_u32 s56,2\n"
"s_cselect_b32 s4,s4,64\n"
TR "v[182:183],v0 offset:6144\n"
MF "v[84:87],a[212:215],a[244:247],v[84:87]\n"
LD "v193,s[48:51],s89 offen offset:1024 lds\n"
MF "v[76:79],a[208:211],a[244:247],v[252:255]\n"
"s_mul_i32 s95,s92,3277\n"
TR "v[184:185],v0 offset:8192\n"
CV "v220,v220,v221\n"
MF "v[76:79],a[168:171],a[240:243],v[76:79]\n"
"s_lshr_b32 s95,s95,16\n"
TR "v[186:187],v0 offset:10240\n"
CV "v221,v222,v223\n"
MF "v[76:79],a[128:131],v[188:191],v[76:79]\n"
"s_mul_i32 s59,s95,20\n"
TR "v[96:97],v0 offset:49152\n"
CV "v222,v224,v225\n"
MF "v[68:71],a[124:127],v[188:191],v[252:255]\n"
"s_sub_u32 s59,s92,s59\n"
TR "v[98:99],v0 offset:51200\n"
CV "v223,v226,v227\n"
MF "v[68:71],a[164:167],a[240:243],v[68:71]\n"
LD "v194,s[48:51],s89 offen offset:2048 lds\n"
MF "v[68:71],a[204:207],a[244:247],v[68:71]\n"
"s_cmp_ge_u32 s59,10\n"
"s_cselect_b32 s96,4294557760,0\n"
TR "v[100:101],v0 offset:53248\n"
MF "v[60:63],a[200:203],a[244:247],v[252:255]\n"
"s_add_u32 s96,s96,1024\n"
TR "v[102:103],v0 offset:55296\n"
AW "a8,v220\n"
MF "v[60:63],a[160:163],a[240:243],v[60:63]\n"
"s_mul_i32 s59,s59,40960\n"
TR "v[104:105],v0 offset:57344\n"
AW "a9,v221\n"
MF "v[60:63],a[120:123],v[188:191],v[60:63]\n"
"s_waitcnt lgkmcnt(5)\n"
LD "v195,s[48:51],s89 offen offset:3072 lds\n"
MF "v[16:19],a[140:143],v[176:179],v[16:19]\n"
"s_add_u32 s96,s96,s59\n"
TR "v[188:189],v1\n"
AW "a10,v222\n"
MF "v[16:19],a[180:183],v[180:183],v[16:19]\n"
"s_lshl_b32 s59,s95,7\n"
TR "v[190:191],v1 offset:2048\n"
AW "a11,v223\n"
MF "v[16:19],a[220:223],v[184:187],v[16:19]\n"
"s_add_u32 s96,s96,s59\n"
TR "v[106:107],v0 offset:59392\n"
AR "v228,a48\n"
MF "v[24:27],a[224:227],v[184:187],v[24:27]\n"
"s_lshl_b32 s2,s92,10\n"
TR "a[240:241],v1 offset:4096\n"
AR "v229,a49\n"
MF "v[24:27],a[184:187],v[180:183],v[24:27]\n"
"s_add_u32 m0,s79,28672\n"
TR "a[242:243],v1 offset:6144\n"
AR "v230,a50\n"
MF "v[24:27],a[144:147],v[176:179],v[24:27]\n"
LD "v196,s[48:51],s89 offen lds\n"
MF "v[32:35],a[148:151],v[176:179],v[32:35]\n"
"s_add_u32 s2,s2,s93\n"
TR "a[244:245],v1 offset:8192\n"
AR "v231,a51\n"
MF "v[32:35],a[188:191],v[180:183],v[32:35]\n"
"s_mov_b32 s0,s96\n"
TR "a[246:247],v1 offset:10240\n"
AR "v232,a68\n"
MF "v[32:35],a[228:231],v[184:187],v[32:35]\n"
LD "v197,s[48:51],s89 offen offset:1024 lds\n"
MF "v[40:43],a[232:235],v[184:187],v[40:43]\n"
"s_add_u32 s92,s92,s4\n"
TR "v[108:109],v4\n"
AR "v233,a69\n"
MF "v[40:43],a[192:195],v[180:183],v[40:43]\n"
"s_mul_i32 s95,s92,3277\n"
TR "v[110:111],v4 offset:2048\n"
AR "v234,a70\n"
MF "v[40:43],a[152:155],v[176:179],v[40:43]\n"
"s_lshr_b32 s95,s95,16\n"
TR "v[112:113],v4 offset:4096\n"
AR "v235,a71\n"
MF "v[48:51],a[156:159],v[176:179],v[48:51]\n"
"s_mul_i32 s59,s95,20\n"
TR "v[114:115],v4 offset:6144\n"
"ds_read_b128 a[68:71],v236 offset:53248\n"
MF "v[48:51],a[196:199],v[180:183],v[48:51]\n"
LD "v198,s[48:51],s89 offen offset:2048 lds\n"
MF "v[48:51],a[236:239],v[184:187],v[48:51]\n"
"s_waitcnt lgkmcnt(5)\n"
TR "v[116:117],v4 offset:8192\n"
"v_fma_f32 v228,v228,v210,v216\n"
MF "v[52:55],a[236:239],a[244:247],v[52:55]\n"
"s_sub_u32 s59,s92,s59\n"
TR "v[176:177],v2\n"
"v_fma_f32 v229,v229,v210,v216\n"
MF "v[52:55],a[196:199],a[240:243],v[52:55]\n"
"s_cmp_ge_u32 s59,10\n"
"s_cselect_b32 s96,4294557760,0\n"
TR "v[178:179],v2 offset:2048\n"
MF "v[52:55],a[156:159],v[188:191],v[52:55]\n"
LD "v199,s[48:51],s89 offen offset:3072 lds\n"
MF "v[44:47],a[152:155],v[188:191],v[44:47]\n"
"s_add_u32 s96,s96,1024\n"
TR "v[180:181],v2 offset:4096\n"
"v_fma_f32 v230,v230,v210,v216\n"
MF "v[44:47],a[192:195],a[240:243],v[44:47]\n"
"s_mul_i32 s59,s59,40960\n"
TR "v[182:183],v2 offset:6144\n"
"v_fma_f32 v231,v231,v210,v216\n"
MF "v[44:47],a[232:235],a[244:247],v[44:47]\n"
"s_add_u32 s96,s96,s59\n"
TR "v[118:119],v4 offset:10240\n"
"v_fma_f32 v232,v232,v210,v216\n"
MF "v[36:39],a[228:231],a[244:247],v[36:39]\n"
"s_lshl_b32 s59,s95,7\n"
TR "v[184:185],v2 offset:8192\n"
"v_fma_f32 v233,v233,v210,v216\n"
MF "v[36:39],a[188:191],a[240:243],v[36:39]\n"
"s_add_u32 m0,s79,32768\n"
TR "v[186:187],v2 offset:10240\n"
"v_fma_f32 v234,v234,v210,v216\n"
MF "v[36:39],a[148:151],v[188:191],v[36:39]\n"
LD "v200,s[48:51],s89 offen lds\n"
MF "v[28:31],a[144:147],v[188:191],v[28:31]\n"
"s_add_u32 s96,s96,s59\n"
TR "v[120:121],v1 offset:49152\n"
"v_fma_f32 v235,v235,v210,v216\n"
MF "v[28:31],a[184:187],a[240:243],v[28:31]\n"
"s_lshl_b32 s3,s92,10\n"
TR "v[122:123],v1 offset:51200\n"
CV "v228,v228,v229\n"
MF "v[28:31],a[224:227],a[244:247],v[28:31]\n"
"s_add_u32 s3,s3,s93\n"
TR "v[124:125],v1 offset:53248\n"
CV "v229,v230,v231\n"
MF "v[20:23],a[220:223],a[244:247],v[20:23]\n"
LD "v201,s[48:51],s89 offen offset:1024 lds\n"
MF "v[20:23],a[180:183],a[240:243],v[20:23]\n"
"s_cmp_lt_u32 s92,40\n"
"s_cselect_b32 s3,s3,0xf0000000\n"
TR "v[126:127],v1 offset:55296\n"
MF "v[20:23],a[140:143],v[188:191],v[20:23]\n"
"s_waitcnt lgkmcnt(4)\n"
TR "v[128:129],v1 offset:57344\n"
CV "v230,v232,v233\n"
MF "v[56:59],a[140:143],v[176:179],v[56:59]\n"
"s_cmp_lt_u32 s92,40\n"
"s_cselect_b32 s1,s96,0xf0000000\n"
TR "v[188:189],v3\n"
MF "v[56:59],a[180:183],v[180:183],v[56:59]\n"
"s_sub_u32 s92,s92,s4\n"
TR "v[190:191],v3 offset:2048\n"
CV "v231,v234,v235\n"
MF "v[56:59],a[220:223],v[184:187],v[56:59]\n"
LD "v202,s[48:51],s89 offen offset:2048 lds\n"
MF "v[64:67],a[224:227],v[184:187],v[64:67]\n"
"s_sub_u32 s2,s2,204800\n"
TR "a[240:241],v3 offset:4096\n"
AW "a48,v228\n"
MF "v[64:67],a[184:187],v[180:183],v[64:67]\n"
"s_mov_b32 s92,64\n"
TR "a[242:243],v3 offset:6144\n"
AW "a49,v229\n"
MF "v[64:67],a[144:147],v[176:179],v[64:67]\n"
LD "v203,s[48:51],s89 offen offset:3072 lds\n"
MF "v[72:75],a[148:151],v[176:179],v[72:75]\n"
TR "a[244:245],v3 offset:8192\n"
AW "a50,v230\n"
MF "v[72:75],a[188:191],v[180:183],v[72:75]\n"
TR "a[246:247],v3 offset:10240\n"
AW "a51,v231\n"
MF "v[72:75],a[228:231],v[184:187],v[72:75]\n"
TR "v[130:131],v1 offset:59392\n"
AR "v220,a88\n"
MF "v[80:83],a[232:235],v[184:187],v[80:83]\n"
TR "v[132:133],v5\n"
AR "v221,a89\n"
MF "v[80:83],a[192:195],v[180:183],v[80:83]\n"
TR "v[134:135],v5 offset:2048\n"
AR "v222,a90\n"
MF "v[80:83],a[152:155],v[176:179],v[80:83]\n"
TR "v[136:137],v5 offset:4096\n"
AR "v223,a91\n"
MF "v[88:91],a[156:159],v[176:179],v[88:91]\n"
TR "v[138:139],v5 offset:6144\n"
AR "v224,a108\n"
MF "v[88:91],a[196:199],v[180:183],v[88:91]\n"
TR "v[140:141],v5 offset:8192\n"
"s_waitcnt lgkmcnt(0)\n"
MF "v[88:91],a[236:239],v[184:187],v[88:91]\n"
"s_barrier\n"
MF "v[92:95],a[236:239],a[244:247],v[92:95]\n"
TR "v[142:143],v5 offset:10240\n"
AR "v225,a109\n"
MF "v[92:95],a[196:199],a[240:243],v[92:95]\n"
TR "v[144:145],v2 offset:49152\n"
AR "v226,a110\n"
MF "v[92:95],a[156:159],v[188:191],v[92:95]\n"
TR "v[146:147],v2 offset:51200\n"
AR "v227,a111\n"
MF "v[84:87],a[152:155],v[188:191],v[84:87]\n"
"s_nop 0\n"
TR "v[148:149],v2 offset:53248\n"
"ds_read_b128 a[108:111],v237 offset:8192\n"
MF "v[84:87],a[192:195],a[240:243],v[84:87]\n"
TR "v[150:151],v2 offset:55296\n"
"v_fma_f32 v220,v220,v210,v216\n"
MF "v[84:87],a[232:235],a[244:247],v[84:87]\n"
TR "v[152:153],v2 offset:57344\n"
"v_fma_f32 v221,v221,v210,v216\n"
MF "v[76:79],a[228:231],a[244:247],v[76:79]\n"
".Lpg32h:\n"
"s_cbranch_execz .Lpg36h\n"
TR "v[154:155],v2 offset:59392\n"
"v_fma_f32 v222,v222,v210,v216\n"
MF "v[76:79],a[188:191],a[240:243],v[76:79]\n"
TR "v[156:157],v6\n"
"v_fma_f32 v223,v223,v210,v216\n"
MF "v[76:79],a[148:151],v[188:191],v[76:79]\n"
TR "v[158:159],v6 offset:2048\n"
"v_fma_f32 v224,v224,v210,v216\n"
MF "v[68:71],a[144:147],v[188:191],v[68:71]\n"
TR "v[160:161],v6 offset:4096\n"
"v_fma_f32 v225,v225,v210,v216\n"
MF "v[68:71],a[184:187],a[240:243],v[68:71]\n"
TR "v[162:163],v6 offset:6144\n"
"v_fma_f32 v226,v226,v210,v216\n"
MF "v[68:71],a[224:227],a[244:247],v[68:71]\n"
TR "v[164:165],v6 offset:8192\n"
"v_fma_f32 v227,v227,v210,v216\n"
MF "v[60:63],a[220:223],a[244:247],v[60:63]\n"
TR "v[166:167],v6 offset:10240\n"
CV "v220,v220,v221\n"
MF "v[60:63],a[180:183],a[240:243],v[60:63]\n"
TR "v[168:169],v3 offset:49152\n"
CV "v221,v222,v223\n"
MF "v[60:63],a[140:143],v[188:191],v[60:63]\n"
TR "v[170:171],v3 offset:51200\n"
CV "v222,v224,v225\n"
MF "v[16:19],a[0:3],v[96:99],v[16:19]\n"
TR "v[172:173],v3 offset:53248\n"
CV "v223,v226,v227\n"
MF "v[16:19],a[40:43],v[100:103],v[16:19]\n"
TR "v[174:175],v3 offset:55296\n"
AW "a88,v220\n"
MF "v[16:19],a[80:83],v[104:107],v[16:19]\n"
TR "v[204:205],v3 offset:57344\n"
AW "a89,v221\n"
MF "v[16:19],a[20:23],v[108:111],v[16:19]\n"
TR "v[206:207],v3 offset:59392\n"
AW "a90,v222\n"
MF "v[16:19],a[60:63],v[112:115],v[16:19]\n"
TR "v[244:245],v7\n"
AW "a91,v223\n"
MF "v[16:19],a[100:103],v[116:119],v[16:19]\n"
"s_waitcnt lgkmcnt(15)\n"
TR "v[246:247],v7 offset:2048\n"
AR "v228,a12\n"
MF "v[20:23],a[100:103],v[140:143],v[20:23]\n"
TR "v[248:249],v7 offset:4096\n"
AR "v229,a13\n"
MF "v[20:23],a[60:63],v[136:139],v[20:23]\n"
TR "v[250:251],v7 offset:6144\n"
AR "v230,a14\n"
MF "v[20:23],a[20:23],v[132:135],v[20:23]\n"
TR "v[238:239],v7 offset:8192\n"
AR "v231,a15\n"
MF "v[20:23],a[80:83],v[128:131],v[20:23]\n"
TR "v[240:241],v7 offset:10240\n"
AR "v232,a32\n"
MF "v[20:23],a[40:43],v[124:127],v[20:23]\n"
AR "v233,a33\n"
AR "v234,a34\n"
MF "v[20:23],a[0:3],v[120:123],v[20:23]\n"
"s_waitcnt lgkmcnt(12)\n"
AR "v235,a35\n"
"v_fma_f32 v228,v228,v211,v217\n"
MF "v[56:59],a[0:3],v[144:147],v[56:59]\n"
"ds_read_b128 a[32:35],v236 offset:36864\n"
"v_fma_f32 v229,v229,v211,v217\n"
MF "v[56:59],a[40:43],v[148:151],v[56:59]\n"
"v_fma_f32 v230,v230,v211,v217\n"
"v_fma_f32 v231,v231,v211,v217\n"
MF "v[56:59],a[80:83],v[152:155],v[56:59]\n"
"v_fma_f32 v232,v232,v211,v217\n"
"v_fma_f32 v233,v233,v211,v217\n"
MF "v[56:59],a[20:23],v[156:159],v[56:59]\n"
"v_fma_f32 v234,v234,v211,v217\n"
"v_fma_f32 v235,v235,v211,v217\n"
MF "v[56:59],a[60:63],v[160:163],v[56:59]\n"
CV "v228,v228,v229\n"
CV "v229,v230,v231\n"
MF "v[56:59],a[100:103],v[164:167],v[56:59]\n"
"s_waitcnt lgkmcnt(1)\n"
CV "v230,v232,v233\n"
CV "v231,v234,v235\n"
MF "v[60:63],a[100:103],v[238:241],v[60:63]\n"
AW "a12,v228\n"
AW "a13,v229\n"
MF "v[60:63],a[60:63],v[248:251],v[60:63]\n"
AW "a14,v230\n"
AW "a15,v231\n"
MF "v[60:63],a[20:23],v[244:247],v[60:63]\n"
AR "v220,a52\n"
AR "v221,a53\n"
MF "v[60:63],a[80:83],v[204:207],v[60:63]\n"
AR "v222,a54\n"
AR "v223,a55\n"
MF "v[60:63],a[40:43],v[172:175],v[60:63]\n"
AR "v224,a72\n"
AR "v225,a73\n"
MF "v[60:63],a[0:3],v[168:171],v[60:63]\n"
AR "v226,a74\n"
AR "v227,a75\n"
MF "v[68:71],a[4:7],v[168:171],v[68:71]\n"
"ds_read_b128 a[72:75],v236 offset:57344\n"
"v_fma_f32 v220,v220,v211,v217\n"
MF "v[68:71],a[44:47],v[172:175],v[68:71]\n"
"v_fma_f32 v221,v221,v211,v217\n"
"v_fma_f32 v222,v222,v211,v217\n"
MF "v[68:71],a[84:87],v[204:207],v[68:71]\n"
"v_fma_f32 v223,v223,v211,v217\n"
"v_fma_f32 v224,v224,v211,v217\n"
MF "v[68:71],a[24:27],v[244:247],v[68:71]\n"
"v_fma_f32 v225,v225,v211,v217\n"
"v_fma_f32 v226,v226,v211,v217\n"
MF "v[68:71],a[64:67],v[248:251],v[68:71]\n"
"v_fma_f32 v227,v227,v211,v217\n"
CV "v220,v220,v221\n"
MF "v[68:71],a[104:107],v[238:241],v[68:71]\n"
"s_waitcnt lgkmcnt(0)\n"
CV "v221,v222,v223\n"
CV "v222,v224,v225\n"
MF "v[64:67],a[104:107],v[164:167],v[64:67]\n"
"s_barrier\n"
MF "v[64:67],a[64:67],v[160:163],v[64:67]\n"
CV "v223,v226,v227\n"
AW "a52,v220\n"
MF "v[64:67],a[24:27],v[156:159],v[64:67]\n"
AW "a53,v221\n"
AW "a54,v222\n"
MF "v[64:67],a[84:87],v[152:155],v[64:67]\n"
AW "a55,v223\n"
AR "v228,a92\n"
MF "v[64:67],a[44:47],v[148:151],v[64:67]\n"
AR "v229,a93\n"
AR "v230,a94\n"
MF "v[64:67],a[4:7],v[144:147],v[64:67]\n"
AR "v231,a95\n"
AR "v232,a112\n"
MF "v[28:31],a[4:7],v[120:123],v[28:31]\n"
AR "v233,a113\n"
AR "v234,a114\n"
MF "v[28:31],a[44:47],v[124:127],v[28:31]\n"
AR "v235,a115\n"
"v_fma_f32 v228,v228,v211,v217\n"
MF "v[28:31],a[84:87],v[128:131],v[28:31]\n"
"ds_read_b128 a[112:115],v237 offset:12288\n"
"v_fma_f32 v229,v229,v211,v217\n"
MF "v[28:31],a[24:27],v[132:135],v[28:31]\n"
"v_fma_f32 v230,v230,v211,v217\n"
"v_fma_f32 v231,v231,v211,v217\n"
MF "v[28:31],a[64:67],v[136:139],v[28:31]\n"
"v_fma_f32 v232,v232,v211,v217\n"
"v_fma_f32 v233,v233,v211,v217\n"
MF "v[28:31],a[104:107],v[140:143],v[28:31]\n"
"v_fma_f32 v234,v234,v211,v217\n"
"v_fma_f32 v235,v235,v211,v217\n"
MF "v[24:27],a[104:107],v[116:119],v[24:27]\n"
CV "v228,v228,v229\n"
CV "v229,v230,v231\n"
MF "v[24:27],a[64:67],v[112:115],v[24:27]\n"
CV "v230,v232,v233\n"
CV "v231,v234,v235\n"
MF "v[24:27],a[24:27],v[108:111],v[24:27]\n"
AW "a92,v228\n"
AW "a93,v229\n"
MF "v[24:27],a[84:87],v[104:107],v[24:27]\n"
AW "a94,v230\n"
AW "a95,v231\n"
MF "v[24:27],a[44:47],v[100:103],v[24:27]\n"
AR "v220,a16\n"
AR "v221,a17\n"
MF "v[24:27],a[4:7],v[96:99],v[24:27]\n"
AR "v222,a18\n"
AR "v223,a19\n"
MF "v[32:35],a[8:11],v[96:99],v[32:35]\n"
AR "v224,a36\n"
AR "v225,a37\n"
MF "v[32:35],a[48:51],v[100:103],v[32:35]\n"
AR "v226,a38\n"
AR "v227,a39\n"
MF "v[32:35],a[88:91],v[104:107],v[32:35]\n"
"ds_read_b128 a[36:39],v236 offset:40960\n"
"v_fma_f32 v220,v220,v212,v218\n"
MF "v[32:35],a[28:31],v[108:111],v[32:35]\n"
"v_fma_f32 v221,v221,v212,v218\n"
"v_fma_f32 v222,v222,v212,v218\n"
MF "v[32:35],a[68:71],v[112:115],v[32:35]\n"
"v_fma_f32 v223,v223,v212,v218\n"
"v_fma_f32 v224,v224,v212,v218\n"
MF "v[32:35],a[108:111],v[116:119],v[32:35]\n"
"v_fma_f32 v225,v225,v212,v218\n"
"v_fma_f32 v226,v226,v212,v218\n"
MF "v[36:39],a[108:111],v[140:143],v[36:39]\n"
"v_fma_f32 v227,v227,v212,v218\n"
CV "v220,v220,v221\n"
MF "v[36:39],a[68:71],v[136:139],v[36:39]\n"
CV "v221,v222,v223\n"
CV "v222,v224,v225\n"
MF "v[36:39],a[28:31],v[132:135],v[36:39]\n"
CV "v223,v226,v227\n"
AW "a16,v220\n"
MF "v[36:39],a[88:91],v[128:131],v[36:39]\n"
AW "a17,v221\n"
AW "a18,v222\n"
MF "v[36:39],a[48:51],v[124:127],v[36:39]\n"
AW "a19,v223\n"
AR "v228,a56\n"
MF "v[36:39],a[8:11],v[120:123],v[36:39]\n"
AR "v229,a57\n"
AR "v230,a58\n"
MF "v[72:75],a[8:11],v[144:147],v[72:75]\n"
AR "v231,a59\n"
AR "v232,a76\n"
MF "v[72:75],a[48:51],v[148:151],v[72:75]\n"
AR "v233,a77\n"
AR "v234,a78\n"
MF "v[72:75],a[88:91],v[152:155],v[72:75]\n"
AR "v235,a79\n"
"v_fma_f32 v228,v228,v212,v218\n"
MF "v[72:75],a[28:31],v[156:159],v[72:75]\n"
"ds_read_b128 a[76:79],v236 offset:61440\n"
"v_fma_f32 v229,v229,v212,v218\n"
MF "v[72:75],a[68:71],v[160:163],v[72:75]\n"
"v_fma_f32 v230,v230,v212,v218\n"
"v_fma_f32 v231,v231,v212,v218\n"
MF "v[72:75],a[108:111],v[164:167],v[72:75]\n"
"v_fma_f32 v232,v232,v212,v218\n"
"v_fma_f32 v233,v233,v212,v218\n"
MF "v[76:79],a[108:111],v[238:241],v[76:79]\n"
"v_fma_f32 v234,v234,v212,v218\n"
"v_fma_f32 v235,v235,v212,v218\n"
MF "v[76:79],a[68:71],v[248:251],v[76:79]\n"
CV "v228,v228,v229\n"
CV "v229,v230,v231\n"
MF "v[76:79],a[28:31],v[244:247],v[76:79]\n"
CV "v230,v232,v233\n"
CV "v231,v234,v235\n"
MF "v[76:79],a[88:91],v[204:207],v[76:79]\n"
AW "a56,v228\n"
AW "a57,v229\n"
MF "v[76:79],a[48:51],v[172:175],v[76:79]\n"
AW "a58,v230\n"
AW "a59,v231\n"
MF "v[76:79],a[8:11],v[168:171],v[76:79]\n"
"s_waitcnt lgkmcnt(2)\n"
AR "v220,a96\n"
AR "v221,a97\n"
MF "v[84:87],a[12:15],v[168:171],v[84:87]\n"
AR "v222,a98\n"
AR "v223,a99\n"
MF "v[84:87],a[52:55],v[172:175],v[84:87]\n"
AR "v224,a116\n"
AR "v225,a117\n"
MF "v[84:87],a[92:95],v[204:207],v[84:87]\n"
AR "v226,a118\n"
AR "v227,a119\n"
MF "v[84:87],a[32:35],v[244:247],v[84:87]\n"
"ds_read_b128 a[116:119],v237 offset:16384\n"
"v_fma_f32 v220,v220,v212,v218\n"
MF "v[84:87],a[72:75],v[248:251],v[84:87]\n"
"v_fma_f32 v221,v221,v212,v218\n"
"v_fma_f32 v222,v222,v212,v218\n"
MF "v[84:87],a[112:115],v[238:241],v[84:87]\n"
"v_fma_f32 v223,v223,v212,v218\n"
"v_fma_f32 v224,v224,v212,v218\n"
MF "v[80:83],a[112:115],v[164:167],v[80:83]\n"
"v_fma_f32 v225,v225,v212,v218\n"
"v_fma_f32 v226,v226,v212,v218\n"
MF "v[80:83],a[72:75],v[160:163],v[80:83]\n"
"v_fma_f32 v227,v227,v212,v218\n"
CV "v220,v220,v221\n"
MF "v[80:83],a[32:35],v[156:159],v[80:83]\n"
CV "v221,v222,v223\n"
CV "v222,v224,v225\n"
MF "v[80:83],a[92:95],v[152:155],v[80:83]\n"
CV "v223,v226,v227\n"
AW "a96,v220\n"
MF "v[80:83],a[52:55],v[148:151],v[80:83]\n"
AW "a97,v221\n"
AW "a98,v222\n"
MF "v[80:83],a[12:15],v[144:147],v[80:83]\n"
AW "a99,v223\n"
"v_mov_b32 v15,0\n"
MF "v[44:47],a[12:15],v[120:123],v[44:47]\n"
"v_lshrrev_b32 v242,5,v9\n"
"v_min_u32 v242,23,v242\n"
MF "v[44:47],a[52:55],v[124:127],v[44:47]\n"
"v_mul_u32_u24 v242,160,v242\n"
"v_and_or_b32 v242,v9,16,v242\n"
MF "v[44:47],a[92:95],v[128:131],v[44:47]\n"
"v_lshlrev_b32 v242,3,v242\n"
AR "v8,a252\n"
MF "v[44:47],a[32:35],v[132:135],v[44:47]\n"
AR "v11,a253\n"
"v_bfe_u32 v224,v9,5,3\n"
MF "v[44:47],a[72:75],v[136:139],v[44:47]\n"
"s_add_u32 s62,s74,s68\n"
"s_add_u32 s68,s68,256\n"
"s_and_b32 s68,s68,768\n"
MF "v[44:47],a[112:115],v[140:143],v[44:47]\n"
"buffer_load_dword v243,v242,s[48:51],s62 offen\n"
MF "v[40:43],a[112:115],v[116:119],v[40:43]\n"
"v_bfe_u32 v225,v9,8,2\n"
"v_lshlrev_b32 v225,1,v225\n"
MF "v[40:43],a[72:75],v[112:115],v[40:43]\n"
"v_mov_b32 v14,s76\n"
"s_add_u32 s76,s76,1\n"
MF "v[40:43],a[32:35],v[108:111],v[40:43]\n"
"buffer_store_dword v14,v15,s[40:43],s94 offen sc1\n"
MF "v[40:43],a[92:95],v[104:107],v[40:43]\n"
"v_and_b32 v226,16,v9\n"
"v_lshlrev_b32_e64 v226,v226,s9\n"
MF "v[40:43],a[52:55],v[100:103],v[40:43]\n"
"v_sub_u32 v224,v224,v225\n"
"v_cmp_eq_u32 vcc,0,v224\n"
MF "v[40:43],a[12:15],v[96:99],v[40:43]\n"
"s_waitcnt lgkmcnt(0)\n"
"v_cndmask_b32 v220,0,v226,vcc\n"
"v_cmp_eq_u32 vcc,1,v224\n"
MF "v[48:51],a[16:19],v[96:99],v[48:51]\n"
"s_nop 0\n"
"v_cndmask_b32 v221,0,v226,vcc\n"
AW "a248,0\n"
MF "v[48:51],a[56:59],v[100:103],v[48:51]\n"
AW "a249,0\n"
AW "a250,v220\n"
MF "v[48:51],a[96:99],v[104:107],v[48:51]\n"
AW "a251,v221\n"
AW "a252,v220\n"
MF "v[48:51],a[36:39],v[108:111],v[48:51]\n"
AW "a253,v221\n"
AW "a254,0\n"
MF "v[48:51],a[76:79],v[112:115],v[48:51]\n"
AW "a255,0\n"
"v_bfe_u32 v12,v9,4,4\n"
MF "v[48:51],a[116:119],v[116:119],v[48:51]\n"
"v_lshlrev_b32 v12,2,v12\n"
"v_lshrrev_b32 v13,2,v9\n"
MF "v[52:55],a[116:119],v[140:143],v[52:55]\n"
"v_add_u32 v13,s58,v13\n"
"v_add_u32 v4,24576,v4\n"
MF "v[52:55],a[76:79],v[136:139],v[52:55]\n"
"s_waitcnt vmcnt(2) lgkmcnt(0)\n"
"v_add_u32 v5,24576,v5\n"
"v_add_u32 v6,24576,v6\n"
MF "v[52:55],a[36:39],v[132:135],v[52:55]\n"
"s_barrier\n"
MF "v[52:55],a[96:99],v[128:131],v[52:55]\n"
TR "v[176:177],v0 offset:24576\n"
"v_add_u32 v7,24576,v7\n"
MF "v[52:55],a[56:59],v[124:127],v[52:55]\n"
TR "v[178:179],v0 offset:26624\n"
MF "v[52:55],a[16:19],v[120:123],v[52:55]\n"
MF "v[88:91],a[16:19],v[144:147],v[88:91]\n"
TR "v[180:181],v0 offset:28672\n"
MF "v[88:91],a[56:59],v[148:151],v[88:91]\n"
TR "v[182:183],v0 offset:30720\n"
MF "v[88:91],a[96:99],v[152:155],v[88:91]\n"
MF "v[88:91],a[36:39],v[156:159],v[88:91]\n"
TR "v[184:185],v0 offset:32768\n"
MF "v[88:91],a[76:79],v[160:163],v[88:91]\n"
TR "v[186:187],v0 offset:34816\n"
MF "v[88:91],a[116:119],v[164:167],v[88:91]\n"
MF "v[92:95],a[116:119],v[238:241],v[92:95]\n"
MF "v[92:95],a[76:79],v[248:251],v[92:95]\n"
MF "v[92:95],a[36:39],v[244:247],v[92:95]\n"
MF "v[92:95],a[96:99],v[204:207],v[92:95]\n"
MF "v[92:95],a[56:59],v[172:175],v[92:95]\n"
MF "v[92:95],a[16:19],v[168:171],v[92:95]\n"
"s_waitcnt lgkmcnt(0)\n"
".Lpl15:\n"
MF "v[96:99],a[0:3],v[176:179],v[252:255]\n"
TR "v[188:189],v1 offset:24576\n"
MF "v[96:99],a[40:43],v[180:183],v[96:99]\n"
TR "v[190:191],v1 offset:26624\n"
MF "v[96:99],a[80:83],v[184:187],v[96:99]\n"
MF "v[104:107],a[84:87],v[184:187],v[252:255]\n"
TR "a[240:241],v1 offset:28672\n"
MF "v[104:107],a[44:47],v[180:183],v[104:107]\n"
TR "a[242:243],v1 offset:30720\n"
MF "v[104:107],a[4:7],v[176:179],v[104:107]\n"
MF "v[112:115],a[8:11],v[176:179],v[252:255]\n"
TR "a[244:245],v1 offset:32768\n"
MF "v[112:115],a[48:51],v[180:183],v[112:115]\n"
TR "a[246:247],v1 offset:34816\n"
MF "v[112:115],a[88:91],v[184:187],v[112:115]\n"
MF "v[120:123],a[92:95],v[184:187],v[252:255]\n"
MF "v[120:123],a[52:55],v[180:183],v[120:123]\n"
MF "v[120:123],a[12:15],v[176:179],v[120:123]\n"
MF "v[128:131],a[16:19],v[176:179],v[252:255]\n"
MF "v[128:131],a[56:59],v[180:183],v[128:131]\n"
MF "v[128:131],a[96:99],v[184:187],v[128:131]\n"
"s_waitcnt lgkmcnt(0)\n"
MF "v[132:135],a[96:99],a[244:247],v[252:255]\n"
TR "v[176:177],v2 offset:24576\n"
MF "v[132:135],a[56:59],a[240:243],v[132:135]\n"
TR "v[178:179],v2 offset:26624\n"
MF "v[132:135],a[16:19],v[188:191],v[132:135]\n"
MF "v[124:127],a[12:15],v[188:191],v[252:255]\n"
TR "v[180:181],v2 offset:28672\n"
MF "v[124:127],a[52:55],a[240:243],v[124:127]\n"
TR "v[182:183],v2 offset:30720\n"
MF "v[124:127],a[92:95],a[244:247],v[124:127]\n"
MF "v[116:119],a[88:91],a[244:247],v[252:255]\n"
TR "v[184:185],v2 offset:32768\n"
MF "v[116:119],a[48:51],a[240:243],v[116:119]\n"
TR "v[186:187],v2 offset:34816\n"
MF "v[116:119],a[8:11],v[188:191],v[116:119]\n"
MF "v[108:111],a[4:7],v[188:191],v[252:255]\n"
MF "v[108:111],a[44:47],a[240:243],v[108:111]\n"
MF "v[108:111],a[84:87],a[244:247],v[108:111]\n"
MF "v[100:103],a[80:83],a[244:247],v[252:255]\n"
MF "v[100:103],a[40:43],a[240:243],v[100:103]\n"
MF "v[100:103],a[0:3],v[188:191],v[100:103]\n"
"s_waitcnt lgkmcnt(0)\n"
MF "v[136:139],a[0:3],v[176:179],v[252:255]\n"
TR "v[188:189],v3 offset:24576\n"
MF "v[136:139],a[40:43],v[180:183],v[136:139]\n"
TR "v[190:191],v3 offset:26624\n"
MF "v[136:139],a[80:83],v[184:187],v[136:139]\n"
MF "v[144:147],a[84:87],v[184:187],v[252:255]\n"
TR "a[240:241],v3 offset:28672\n"
MF "v[144:147],a[44:47],v[180:183],v[144:147]\n"
TR "a[242:243],v3 offset:30720\n"
MF "v[144:147],a[4:7],v[176:179],v[144:147]\n"
MF "v[152:155],a[8:11],v[176:179],v[252:255]\n"
TR "a[244:245],v3 offset:32768\n"
MF "v[152:155],a[48:51],v[180:183],v[152:155]\n"
TR "a[246:247],v3 offset:34816\n"
MF "v[152:155],a[88:91],v[184:187],v[152:155]\n"
MF "v[160:163],a[92:95],v[184:187],v[252:255]\n"
MF "v[160:163],a[52:55],v[180:183],v[160:163]\n"
MF "v[160:163],a[12:15],v[176:179],v[160:163]\n"
MF "v[168:171],a[16:19],v[176:179],v[252:255]\n"
MF "v[168:171],a[56:59],v[180:183],v[168:171]\n"
"s_waitcnt lgkmcnt(0)\n"
MF "v[168:171],a[96:99],v[184:187],v[168:171]\n"
"s_barrier\n"
MF "v[172:175],a[96:99],a[244:247],v[252:255]\n"
TR "v[176:177],v0\n"
MF "v[172:175],a[56:59],a[240:243],v[172:175]\n"
TR "v[178:179],v0 offset:2048\n"
MF "v[172:175],a[16:19],v[188:191],v[172:175]\n"
"ds_read2st64_b32 v[192:193],v13 offset0:192 offset1:193\n"
MF "v[164:167],a[12:15],v[188:191],v[252:255]\n"
TR "v[180:181],v0 offset:4096\n"
MF "v[164:167],a[52:55],a[240:243],v[164:167]\n"
TR "v[182:183],v0 offset:6144\n"
MF "v[164:167],a[92:95],a[244:247],v[164:167]\n"
"ds_read2st64_b32 v[194:195],v13 offset0:194 offset1:195\n"
MF "v[156:159],a[88:91],a[244:247],v[252:255]\n"
TR "v[184:185],v0 offset:8192\n"
MF "v[156:159],a[48:51],a[240:243],v[156:159]\n"
TR "v[186:187],v0 offset:10240\n"
MF "v[156:159],a[8:11],v[188:191],v[156:159]\n"
"ds_read2st64_b32 v[196:197],v13 offset0:196 offset1:197\n"
MF "v[148:151],a[4:7],v[188:191],v[252:255]\n"
"ds_read2st64_b32 v[198:199],v13 offset0:198 offset1:199\n"
MF "v[148:151],a[44:47],a[240:243],v[148:151]\n"
MF "v[148:151],a[84:87],a[244:247],v[148:151]\n"
MF "v[140:143],a[80:83],a[244:247],v[252:255]\n"
MF "v[140:143],a[40:43],a[240:243],v[140:143]\n"
MF "v[140:143],a[0:3],v[188:191],v[140:143]\n"
"s_waitcnt lgkmcnt(2)\n"
MF "v[96:99],a[120:123],v[176:179],v[96:99]\n"
"s_waitcnt lgkmcnt(0)\n"
TR "v[188:189],v1\n"
"v_add_f32 v16,v16,v192\n"
MF "v[96:99],a[160:163],v[180:183],v[96:99]\n"
TR "v[190:191],v1 offset:2048\n"
"v_add_f32 v17,v17,v193\n"
MF "v[96:99],a[200:203],v[184:187],v[96:99]\n"
"ds_read2st64_b32 v[200:201],v13 offset0:200 offset1:201\n"
"v_add_f32 v18,v18,v194\n"
"s_waitcnt vmcnt(0)\n"
"v_mov_b32 v14,s76\n"
"s_add_u32 s76,s76,1\n"
MF "v[104:107],a[204:207],v[184:187],v[104:107]\n"
TR "a[240:241],v1 offset:4096\n"
"v_add_f32 v19,v19,v195\n"
"buffer_store_dword v14,v15,s[40:43],s94 offen sc1\n"
MF "v[104:107],a[164:167],v[180:183],v[104:107]\n"
TR "a[242:243],v1 offset:6144\n"
"v_add_f32 v20,v20,v196\n"
MF "v[104:107],a[124:127],v[176:179],v[104:107]\n"
"ds_read2st64_b32 v[202:203],v13 offset0:202 offset1:203\n"
"v_add_f32 v21,v21,v197\n"
MF "v[112:115],a[128:131],v[176:179],v[112:115]\n"
TR "a[244:245],v1 offset:8192\n"
"v_add_f32 v22,v22,v198\n"
MF "v[112:115],a[168:171],v[180:183],v[112:115]\n"
TR "a[246:247],v1 offset:10240\n"
"v_add_f32 v23,v23,v199\n"
MF "v[112:115],a[208:211],v[184:187],v[112:115]\n"
"ds_read2st64_b32 v[204:205],v13 offset0:204 offset1:205\n"
CV "v16,v16,v17\n"
MF "v[120:123],a[212:215],v[184:187],v[120:123]\n"
"ds_read2st64_b32 v[206:207],v13 offset0:206 offset1:207\n"
CV "v17,v18,v19\n"
MF "v[120:123],a[172:175],v[180:183],v[120:123]\n"
"s_add_u32 s87,s90,0\n"
CV "v18,v20,v21\n"
CV "v19,v22,v23\n"
MF "v[120:123],a[132:135],v[176:179],v[120:123]\n"
MF "v[128:131],a[136:139],v[176:179],v[128:131]\n"
"buffer_store_dwordx4 v[16:19],v9,s[40:43],s87 offen sc1\n"
MF "v[128:131],a[176:179],v[180:183],v[128:131]\n"
MF "v[128:131],a[216:219],v[184:187],v[128:131]\n"
"s_waitcnt lgkmcnt(2)\n"
MF "v[132:135],a[216:219],a[244:247],v[132:135]\n"
"s_waitcnt lgkmcnt(0)\n"
TR "v[176:177],v2\n"
"v_add_f32 v24,v24,v200\n"
MF "v[132:135],a[176:179],a[240:243],v[132:135]\n"
TR "v[178:179],v2 offset:2048\n"
"v_add_f32 v25,v25,v201\n"
MF "v[132:135],a[136:139],v[188:191],v[132:135]\n"
"ds_read2st64_b32 v[192:193],v13 offset0:208 offset1:209\n"
"v_add_f32 v26,v26,v202\n"
MF "v[124:127],a[132:135],v[188:191],v[124:127]\n"
TR "v[180:181],v2 offset:4096\n"
"v_add_f32 v27,v27,v203\n"
MF "v[124:127],a[172:175],a[240:243],v[124:127]\n"
TR "v[182:183],v2 offset:6144\n"
"v_add_f32 v28,v28,v204\n"
MF "v[124:127],a[212:215],a[244:247],v[124:127]\n"
"ds_read2st64_b32 v[194:195],v13 offset0:210 offset1:211\n"
"v_add_f32 v29,v29,v205\n"
MF "v[116:119],a[208:211],a[244:247],v[116:119]\n"
TR "v[184:185],v2 offset:8192\n"
"v_add_f32 v30,v30,v206\n"
MF "v[116:119],a[168:171],a[240:243],v[116:119]\n"
TR "v[186:187],v2 offset:10240\n"
"v_add_f32 v31,v31,v207\n"
MF "v[116:119],a[128:131],v[188:191],v[116:119]\n"
"ds_read2st64_b32 v[196:197],v13 offset0:212 offset1:213\n"
CV "v24,v24,v25\n"
MF "v[108:111],a[124:127],v[188:191],v[108:111]\n"
"ds_read2st64_b32 v[198:199],v13 offset0:214 offset1:215\n"
CV "v25,v26,v27\n"
MF "v[108:111],a[164:167],a[240:243],v[108:111]\n"
".Lpg33h:\n"
"s_cbranch_execz .Lpg37h\n"
"s_add_u32 s87,s90,1024\n"
CV "v26,v28,v29\n"
CV "v27,v30,v31\n"
MF "v[108:111],a[204:207],a[244:247],v[108:111]\n"
MF "v[100:103],a[200:203],a[244:247],v[100:103]\n"
"buffer_store_dwordx4 v[24:27],v9,s[40:43],s87 offen sc1\n"
MF "v[100:103],a[160:163],a[240:243],v[100:103]\n"
MF "v[100:103],a[120:123],v[188:191],v[100:103]\n"
"s_waitcnt lgkmcnt(2)\n"
MF "v[136:139],a[120:123],v[176:179],v[136:139]\n"
"s_waitcnt lgkmcnt(0)\n"
TR "v[188:189],v3\n"
"v_add_f32 v32,v32,v192\n"
MF "v[136:139],a[160:163],v[180:183],v[136:139]\n"
TR "v[190:191],v3 offset:2048\n"
"v_add_f32 v33,v33,v193\n"
MF "v[136:139],a[200:203],v[184:187],v[136:139]\n"
"ds_read2st64_b32 v[200:201],v13 offset0:216 offset1:217\n"
"v_add_f32 v34,v34,v194\n"
MF "v[144:147],a[204:207],v[184:187],v[144:147]\n"
TR "a[240:241],v3 offset:4096\n"
"v_add_f32 v35,v35,v195\n"
MF "v[144:147],a[164:167],v[180:183],v[144:147]\n"
TR "a[242:243],v3 offset:6144\n"
"v_add_f32 v36,v36,v196\n"
MF "v[144:147],a[124:127],v[176:179],v[144:147]\n"
"ds_read2st64_b32 v[202:203],v13 offset0:218 offset1:219\n"
"v_add_f32 v37,v37,v197\n"
MF "v[152:155],a[128:131],v[176:179],v[152:155]\n"
TR "a[244:245],v3 offset:8192\n"
"v_add_f32 v38,v38,v198\n"
MF "v[152:155],a[168:171],v[180:183],v[152:155]\n"
TR "a[246:247],v3 offset:10240\n"
"v_add_f32 v39,v39,v199\n"
MF "v[152:155],a[208:211],v[184:187],v[152:155]\n"
"ds_read2st64_b32 v[204:205],v13 offset0:220 offset1:221\n"
CV "v32,v32,v33\n"
MF "v[160:163],a[212:215],v[184:187],v[160:163]\n"
"ds_read2st64_b32 v[206:207],v13 offset0:222 offset1:223\n"
CV "v33,v34,v35\n"
MF "v[160:163],a[172:175],v[180:183],v[160:163]\n"
"s_add_u32 s87,s90,2048\n"
CV "v34,v36,v37\n"
CV "v35,v38,v39\n"
MF "v[160:163],a[132:135],v[176:179],v[160:163]\n"
MF "v[168:171],a[136:139],v[176:179],v[168:171]\n"
"buffer_store_dwordx4 v[32:35],v9,s[40:43],s87 offen sc1\n"
MF "v[168:171],a[176:179],v[180:183],v[168:171]\n"
"s_waitcnt lgkmcnt(0)\n"
MF "v[168:171],a[216:219],v[184:187],v[168:171]\n"
"s_barrier\n"
MF "v[172:175],a[216:219],a[244:247],v[172:175]\n"
TR "v[176:177],v0 offset:49152\n"
"v_add_f32 v40,v40,v200\n"
MF "v[172:175],a[176:179],a[240:243],v[172:175]\n"
TR "v[178:179],v0 offset:51200\n"
"v_add_f32 v41,v41,v201\n"
MF "v[172:175],a[136:139],v[188:191],v[172:175]\n"
"ds_read2st64_b32 v[192:193],v13 offset0:224 offset1:225\n"
"v_add_f32 v42,v42,v202\n"
MF "v[164:167],a[132:135],v[188:191],v[164:167]\n"
TR "v[180:181],v0 offset:53248\n"
"v_add_f32 v43,v43,v203\n"
MF "v[164:167],a[172:175],a[240:243],v[164:167]\n"
TR "v[182:183],v0 offset:55296\n"
"v_add_f32 v44,v44,v204\n"
MF "v[164:167],a[212:215],a[244:247],v[164:167]\n"
"ds_read2st64_b32 v[194:195],v13 offset0:226 offset1:227\n"
"v_add_f32 v45,v45,v205\n"
MF "v[156:159],a[208:211],a[244:247],v[156:159]\n"
TR "v[184:185],v0 offset:57344\n"
"v_add_f32 v46,v46,v206\n"
MF "v[156:159],a[168:171],a[240:243],v[156:159]\n"
TR "v[186:187],v0 offset:59392\n"
"v_add_f32 v47,v47,v207\n"
MF "v[156:159],a[128:131],v[188:191],v[156:159]\n"
"ds_read2st64_b32 v[196:197],v13 offset0:228 offset1:229\n"
CV "v40,v40,v41\n"
MF "v[148:151],a[124:127],v[188:191],v[148:151]\n"
"ds_read2st64_b32 v[198:199],v13 offset0:230 offset1:231\n"
CV "v41,v42,v43\n"
MF "v[148:151],a[164:167],a[240:243],v[148:151]\n"
"s_add_u32 s87,s90,3072\n"
CV "v42,v44,v45\n"
CV "v43,v46,v47\n"
MF "v[148:151],a[204:207],a[244:247],v[148:151]\n"
MF "v[140:143],a[200:203],a[244:247],v[140:143]\n"
"buffer_store_dwordx4 v[40:43],v9,s[40:43],s87 offen sc1\n"
MF "v[140:143],a[160:163],a[240:243],v[140:143]\n"
MF "v[140:143],a[120:123],v[188:191],v[140:143]\n"
"s_waitcnt lgkmcnt(2)\n"
MF "v[96:99],a[20:23],v[176:179],v[96:99]\n"
"s_waitcnt lgkmcnt(0)\n"
TR "v[188:189],v1 offset:49152\n"
"v_add_f32 v48,v48,v192\n"
MF "v[96:99],a[60:63],v[180:183],v[96:99]\n"
TR "v[190:191],v1 offset:51200\n"
"v_add_f32 v49,v49,v193\n"
MF "v[96:99],a[100:103],v[184:187],v[96:99]\n"
"ds_read2st64_b32 v[200:201],v13 offset0:0 offset1:1\n"
"v_add_f32 v50,v50,v194\n"
MF "v[104:107],a[104:107],v[184:187],v[104:107]\n"
TR "a[240:241],v1 offset:53248\n"
"v_add_f32 v51,v51,v195\n"
MF "v[104:107],a[64:67],v[180:183],v[104:107]\n"
TR "a[242:243],v1 offset:55296\n"
"v_add_f32 v52,v52,v196\n"
MF "v[104:107],a[24:27],v[176:179],v[104:107]\n"
"ds_read2st64_b32 v[202:203],v13 offset0:2 offset1:3\n"
"v_add_f32 v53,v53,v197\n"
MF "v[112:115],a[28:31],v[176:179],v[112:115]\n"
TR "a[244:245],v1 offset:57344\n"
"v_add_f32 v54,v54,v198\n"
MF "v[112:115],a[68:71],v[180:183],v[112:115]\n"
TR "a[246:247],v1 offset:59392\n"
"v_add_f32 v55,v55,v199\n"
MF "v[112:115],a[108:111],v[184:187],v[112:115]\n"
"ds_read2st64_b32 v[204:205],v13 offset0:4 offset1:5\n"
CV "v48,v48,v49\n"
MF "v[120:123],a[112:115],v[184:187],v[120:123]\n"
"ds_read2st64_b32 v[206:207],v13 offset0:6 offset1:7\n"
CV "v49,v50,v51\n"
MF "v[120:123],a[72:75],v[180:183],v[120:123]\n"
"s_add_u32 s87,s90,4096\n"
CV "v50,v52,v53\n"
CV "v51,v54,v55\n"
MF "v[120:123],a[32:35],v[176:179],v[120:123]\n"
MF "v[128:131],a[36:39],v[176:179],v[128:131]\n"
"buffer_store_dwordx4 v[48:51],v9,s[40:43],s87 offen sc1\n"
MF "v[128:131],a[76:79],v[180:183],v[128:131]\n"
MF "v[128:131],a[116:119],v[184:187],v[128:131]\n"
"s_waitcnt lgkmcnt(2)\n"
MF "v[132:135],a[116:119],a[244:247],v[132:135]\n"
"s_waitcnt lgkmcnt(0)\n"
TR "v[176:177],v2 offset:49152\n"
"v_add_f32 v56,v56,v200\n"
MF "v[132:135],a[76:79],a[240:243],v[132:135]\n"
TR "v[178:179],v2 offset:51200\n"
"v_add_f32 v57,v57,v201\n"
MF "v[132:135],a[36:39],v[188:191],v[132:135]\n"
"ds_read2st64_b32 v[192:193],v13 offset0:8 offset1:9\n"
"v_add_f32 v58,v58,v202\n"
MF "v[124:127],a[32:35],v[188:191],v[124:127]\n"
TR "v[180:181],v2 offset:53248\n"
"v_add_f32 v59,v59,v203\n"
MF "v[124:127],a[72:75],a[240:243],v[124:127]\n"
TR "v[182:183],v2 offset:55296\n"
"v_add_f32 v60,v60,v204\n"
MF "v[124:127],a[112:115],a[244:247],v[124:127]\n"
"ds_read2st64_b32 v[194:195],v13 offset0:10 offset1:11\n"
"v_add_f32 v61,v61,v205\n"
MF "v[116:119],a[108:111],a[244:247],v[116:119]\n"
TR "v[184:185],v2 offset:57344\n"
"v_add_f32 v62,v62,v206\n"
MF "v[116:119],a[68:71],a[240:243],v[116:119]\n"
TR "v[186:187],v2 offset:59392\n"
"v_add_f32 v63,v63,v207\n"
MF "v[116:119],a[28:31],v[188:191],v[116:119]\n"
"ds_read2st64_b32 v[196:197],v13 offset0:12 offset1:13\n"
CV "v56,v56,v57\n"
MF "v[108:111],a[24:27],v[188:191],v[108:111]\n"
"ds_read2st64_b32 v[198:199],v13 offset0:14 offset1:15\n"
CV "v57,v58,v59\n"
MF "v[108:111],a[64:67],a[240:243],v[108:111]\n"
"s_add_u32 s87,s90,10240\n"
CV "v58,v60,v61\n"
CV "v59,v62,v63\n"
MF "v[108:111],a[104:107],a[244:247],v[108:111]\n"
MF "v[100:103],a[100:103],a[244:247],v[100:103]\n"
"buffer_store_dwordx4 v[56:59],v9,s[40:43],s87 offen sc1\n"
MF "v[100:103],a[60:63],a[240:243],v[100:103]\n"
MF "v[100:103],a[20:23],v[188:191],v[100:103]\n"
"s_waitcnt lgkmcnt(2)\n"
MF "v[136:139],a[20:23],v[176:179],v[136:139]\n"
"s_waitcnt lgkmcnt(0)\n"
TR "v[188:189],v3 offset:49152\n"
"v_add_f32 v64,v64,v192\n"
MF "v[136:139],a[60:63],v[180:183],v[136:139]\n"
TR "v[190:191],v3 offset:51200\n"
"v_add_f32 v65,v65,v193\n"
MF "v[136:139],a[100:103],v[184:187],v[136:139]\n"
"ds_read2st64_b32 v[200:201],v13 offset0:16 offset1:17\n"
"v_add_f32 v66,v66,v194\n"
MF "v[144:147],a[104:107],v[184:187],v[144:147]\n"
TR "a[240:241],v3 offset:53248\n"
"v_add_f32 v67,v67,v195\n"
MF "v[144:147],a[64:67],v[180:183],v[144:147]\n"
TR "a[242:243],v3 offset:55296\n"
"v_add_f32 v68,v68,v196\n"
MF "v[144:147],a[24:27],v[176:179],v[144:147]\n"
"ds_read2st64_b32 v[202:203],v13 offset0:18 offset1:19\n"
"v_add_f32 v69,v69,v197\n"
MF "v[152:155],a[28:31],v[176:179],v[152:155]\n"
TR "a[244:245],v3 offset:57344\n"
"v_add_f32 v70,v70,v198\n"
MF "v[152:155],a[68:71],v[180:183],v[152:155]\n"
TR "a[246:247],v3 offset:59392\n"
"v_add_f32 v71,v71,v199\n"
MF "v[152:155],a[108:111],v[184:187],v[152:155]\n"
"ds_read2st64_b32 v[204:205],v13 offset0:20 offset1:21\n"
CV "v64,v64,v65\n"
MF "v[160:163],a[112:115],v[184:187],v[160:163]\n"
"ds_read2st64_b32 v[206:207],v13 offset0:22 offset1:23\n"
CV "v65,v66,v67\n"
MF "v[160:163],a[72:75],v[180:183],v[160:163]\n"
"s_add_u32 s87,s90,11264\n"
CV "v66,v68,v69\n"
CV "v67,v70,v71\n"
MF "v[160:163],a[32:35],v[176:179],v[160:163]\n"
MF "v[168:171],a[36:39],v[176:179],v[168:171]\n"
"buffer_store_dwordx4 v[64:67],v9,s[40:43],s87 offen sc1\n"
MF "v[168:171],a[76:79],v[180:183],v[168:171]\n"
"s_waitcnt lgkmcnt(0)\n"
MF "v[168:171],a[116:119],v[184:187],v[168:171]\n"
"s_barrier\n"
MF "v[172:175],a[116:119],a[244:247],v[172:175]\n"
TR "v[176:177],v4\n"
"v_add_f32 v72,v72,v200\n"
MF "v[172:175],a[76:79],a[240:243],v[172:175]\n"
TR "v[178:179],v4 offset:2048\n"
"v_add_f32 v73,v73,v201\n"
MF "v[172:175],a[36:39],v[188:191],v[172:175]\n"
"ds_read2st64_b32 v[192:193],v13 offset0:24 offset1:25\n"
"v_add_f32 v74,v74,v202\n"
MF "v[164:167],a[32:35],v[188:191],v[164:167]\n"
TR "v[180:181],v4 offset:4096\n"
"v_add_f32 v75,v75,v203\n"
MF "v[164:167],a[72:75],a[240:243],v[164:167]\n"
TR "v[182:183],v4 offset:6144\n"
"v_add_f32 v76,v76,v204\n"
MF "v[164:167],a[112:115],a[244:247],v[164:167]\n"
"ds_read2st64_b32 v[194:195],v13 offset0:26 offset1:27\n"
"v_add_f32 v77,v77,v205\n"
MF "v[156:159],a[108:111],a[244:247],v[156:159]\n"
TR "v[184:185],v4 offset:8192\n"
"v_add_f32 v78,v78,v206\n"
MF "v[156:159],a[68:71],a[240:243],v[156:159]\n"
TR "v[186:187],v4 offset:10240\n"
"v_add_f32 v79,v79,v207\n"
MF "v[156:159],a[28:31],v[188:191],v[156:159]\n"
"ds_read2st64_b32 v[196:197],v13 offset0:28 offset1:29\n"
CV "v72,v72,v73\n"
MF "v[148:151],a[24:27],v[188:191],v[148:151]\n"
"ds_read2st64_b32 v[198:199],v13 offset0:30 offset1:31\n"
CV "v73,v74,v75\n"
MF "v[148:151],a[64:67],a[240:243],v[148:151]\n"
"s_add_u32 s87,s90,12288\n"
CV "v74,v76,v77\n"
CV "v75,v78,v79\n"
MF "v[148:151],a[104:107],a[244:247],v[148:151]\n"
MF "v[140:143],a[100:103],a[244:247],v[140:143]\n"
"buffer_store_dwordx4 v[72:75],v9,s[40:43],s87 offen sc1\n"
MF "v[140:143],a[60:63],a[240:243],v[140:143]\n"
MF "v[140:143],a[20:23],v[188:191],v[140:143]\n"
"s_waitcnt lgkmcnt(2)\n"
MF "v[96:99],a[140:143],v[176:179],v[96:99]\n"
"s_waitcnt lgkmcnt(0)\n"
TR "v[188:189],v5\n"
"v_add_f32 v80,v80,v192\n"
MF "v[96:99],a[180:183],v[180:183],v[96:99]\n"
TR "v[190:191],v5 offset:2048\n"
"v_add_f32 v81,v81,v193\n"
MF "v[96:99],a[220:223],v[184:187],v[96:99]\n"
"ds_read2st64_b32 v[200:201],v13 offset0:32 offset1:33\n"
"v_add_f32 v82,v82,v194\n"
MF "v[104:107],a[224:227],v[184:187],v[104:107]\n"
TR "a[240:241],v5 offset:4096\n"
"v_add_f32 v83,v83,v195\n"
MF "v[104:107],a[184:187],v[180:183],v[104:107]\n"
TR "a[242:243],v5 offset:6144\n"
"v_add_f32 v84,v84,v196\n"
MF "v[104:107],a[144:147],v[176:179],v[104:107]\n"
"ds_read2st64_b32 v[202:203],v13 offset0:34 offset1:35\n"
"v_add_f32 v85,v85,v197\n"
MF "v[112:115],a[148:151],v[176:179],v[112:115]\n"
TR "a[244:245],v5 offset:8192\n"
"v_add_f32 v86,v86,v198\n"
MF "v[112:115],a[188:191],v[180:183],v[112:115]\n"
TR "a[246:247],v5 offset:10240\n"
"v_add_f32 v87,v87,v199\n"
MF "v[112:115],a[228:231],v[184:187],v[112:115]\n"
"ds_read2st64_b32 v[204:205],v13 offset0:36 offset1:37\n"
CV "v80,v80,v81\n"
MF "v[120:123],a[232:235],v[184:187],v[120:123]\n"
"ds_read2st64_b32 v[206:207],v13 offset0:38 offset1:39\n"
CV "v81,v82,v83\n"
MF "v[120:123],a[192:195],v[180:183],v[120:123]\n"
"s_add_u32 s87,s90,13312\n"
CV "v82,v84,v85\n"
CV "v83,v86,v87\n"
MF "v[120:123],a[152:155],v[176:179],v[120:123]\n"
MF "v[128:131],a[156:159],v[176:179],v[128:131]\n"
"buffer_store_dwordx4 v[80:83],v9,s[40:43],s87 offen sc1\n"
MF "v[128:131],a[196:199],v[180:183],v[128:131]\n"
MF "v[128:131],a[236:239],v[184:187],v[128:131]\n"
"s_waitcnt lgkmcnt(2)\n"
MF "v[132:135],a[236:239],a[244:247],v[132:135]\n"
"s_waitcnt lgkmcnt(0)\n"
TR "v[176:177],v6\n"
"v_add_f32 v88,v88,v200\n"
MF "v[132:135],a[196:199],a[240:243],v[132:135]\n"
TR "v[178:179],v6 offset:2048\n"
"v_add_f32 v89,v89,v201\n"
MF "v[132:135],a[156:159],v[188:191],v[132:135]\n"
"v_add_f32 v90,v90,v202\n"
"v_add_f32 v91,v91,v203\n"
MF "v[124:127],a[152:155],v[188:191],v[124:127]\n"
TR "v[180:181],v6 offset:4096\n"
"v_add_f32 v92,v92,v204\n"
MF "v[124:127],a[192:195],a[240:243],v[124:127]\n"
TR "v[182:183],v6 offset:6144\n"
"v_add_f32 v93,v93,v205\n"
MF "v[124:127],a[232:235],a[244:247],v[124:127]\n"
"v_add_f32 v94,v94,v206\n"
"v_add_f32 v95,v95,v207\n"
MF "v[116:119],a[228:231],a[244:247],v[116:119]\n"
TR "v[184:185],v6 offset:8192\n"
CV "v88,v88,v89\n"
MF "v[116:119],a[188:191],a[240:243],v[116:119]\n"
TR "v[186:187],v6 offset:10240\n"
CV "v89,v90,v91\n"
MF "v[116:119],a[148:151],v[188:191],v[116:119]\n"
"s_add_u32 s87,s90,14336\n"
CV "v90,v92,v93\n"
CV "v91,v94,v95\n"
MF "v[108:111],a[144:147],v[188:191],v[108:111]\n"
MF "v[108:111],a[184:187],a[240:243],v[108:111]\n"
MF "v[108:111],a[224:227],a[244:247],v[108:111]\n"
MF "v[100:103],a[220:223],a[244:247],v[100:103]\n"
"buffer_store_dwordx4 v[88:91],v9,s[40:43],s87 offen sc1\n"
MF "v[100:103],a[180:183],a[240:243],v[100:103]\n"
"s_mov_b32 s90,s91\n"
"s_add_u32 s91,s91,20480\n"
MF "v[100:103],a[140:143],v[188:191],v[100:103]\n"
"s_waitcnt lgkmcnt(0)\n"
MF "v[136:139],a[140:143],v[176:179],v[136:139]\n"
TR "v[188:189],v7\n"
MF "v[136:139],a[180:183],v[180:183],v[136:139]\n"
TR "v[190:191],v7 offset:2048\n"
MF "v[136:139],a[220:223],v[184:187],v[136:139]\n"
MF "v[144:147],a[224:227],v[184:187],v[144:147]\n"
TR "a[240:241],v7 offset:4096\n"
MF "v[144:147],a[184:187],v[180:183],v[144:147]\n"
TR "a[242:243],v7 offset:6144\n"
MF "v[144:147],a[144:147],v[176:179],v[144:147]\n"
MF "v[152:155],a[148:151],v[176:179],v[152:155]\n"
TR "a[244:245],v7 offset:8192\n"
MF "v[152:155],a[188:191],v[180:183],v[152:155]\n"
TR "a[246:247],v7 offset:10240\n"
MF "v[152:155],a[228:231],v[184:187],v[152:155]\n"
MF "v[160:163],a[232:235],v[184:187],v[160:163]\n"
MF "v[160:163],a[192:195],v[180:183],v[160:163]\n"
MF "v[160:163],a[152:155],v[176:179],v[160:163]\n"
MF "v[168:171],a[156:159],v[176:179],v[168:171]\n"
MF "v[168:171],a[196:199],v[180:183],v[168:171]\n"
"s_waitcnt lgkmcnt(0)\n"
MF "v[168:171],a[236:239],v[184:187],v[168:171]\n"
"s_barrier\n"
MF "v[172:175],a[236:239],a[244:247],v[172:175]\n"
TR "v[176:177],v0 offset:24576\n"
MF "v[172:175],a[196:199],a[240:243],v[172:175]\n"
"s_add_u32 s85,s85,1\n"
TR "v[178:179],v0 offset:26624\n"
MF "v[172:175],a[156:159],v[188:191],v[172:175]\n"
MF "v[164:167],a[152:155],v[188:191],v[164:167]\n"
TR "v[180:181],v0 offset:28672\n"
MF "v[164:167],a[192:195],a[240:243],v[164:167]\n"
TR "v[182:183],v0 offset:30720\n"
MF "v[164:167],a[232:235],a[244:247],v[164:167]\n"
MF "v[156:159],a[228:231],a[244:247],v[156:159]\n"
TR "v[184:185],v0 offset:32768\n"
MF "v[156:159],a[188:191],a[240:243],v[156:159]\n"
TR "v[186:187],v0 offset:34816\n"
MF "v[156:159],a[148:151],v[188:191],v[156:159]\n"
MF "v[148:151],a[144:147],v[188:191],v[148:151]\n"
MF "v[148:151],a[184:187],a[240:243],v[148:151]\n"
MF "v[148:151],a[224:227],a[244:247],v[148:151]\n"
MF "v[140:143],a[220:223],a[244:247],v[140:143]\n"
MF "v[140:143],a[180:183],a[240:243],v[140:143]\n"
MF "v[140:143],a[140:143],v[188:191],v[140:143]\n"
"s_waitcnt lgkmcnt(0)\n"
MF "v[16:19],a[0:3],v[176:179],v[252:255]\n"
TR "v[188:189],v1 offset:24576\n"
MF "v[16:19],a[40:43],v[180:183],v[16:19]\n"
TR "v[190:191],v1 offset:26624\n"
MF "v[16:19],a[80:83],v[184:187],v[16:19]\n"
MF "v[24:27],a[84:87],v[184:187],v[252:255]\n"
TR "a[240:241],v1 offset:28672\n"
MF "v[24:27],a[44:47],v[180:183],v[24:27]\n"
TR "a[242:243],v1 offset:30720\n"
MF "v[24:27],a[4:7],v[176:179],v[24:27]\n"
MF "v[32:35],a[8:11],v[176:179],v[252:255]\n"
TR "a[244:245],v1 offset:32768\n"
MF "v[32:35],a[48:51],v[180:183],v[32:35]\n"
TR "a[246:247],v1 offset:34816\n"
MF "v[32:35],a[88:91],v[184:187],v[32:35]\n"
MF "v[40:43],a[92:95],v[184:187],v[252:255]\n"
MF "v[40:43],a[52:55],v[180:183],v[40:43]\n"
MF "v[40:43],a[12:15],v[176:179],v[40:43]\n"
MF "v[48:51],a[16:19],v[176:179],v[252:255]\n"
MF "v[48:51],a[56:59],v[180:183],v[48:51]\n"
MF "v[48:51],a[96:99],v[184:187],v[48:51]\n"
"s_waitcnt lgkmcnt(0)\n"
MF "v[52:55],a[96:99],a[244:247],v[252:255]\n"
TR "v[176:177],v2 offset:24576\n"
MF "v[52:55],a[56:59],a[240:243],v[52:55]\n"
TR "v[178:179],v2 offset:26624\n"
MF "v[52:55],a[16:19],v[188:191],v[52:55]\n"
MF "v[44:47],a[12:15],v[188:191],v[252:255]\n"
TR "v[180:181],v2 offset:28672\n"
MF "v[44:47],a[52:55],a[240:243],v[44:47]\n"
TR "v[182:183],v2 offset:30720\n"
MF "v[44:47],a[92:95],a[244:247],v[44:47]\n"
MF "v[36:39],a[88:91],a[244:247],v[252:255]\n"
TR "v[184:185],v2 offset:32768\n"
MF "v[36:39],a[48:51],a[240:243],v[36:39]\n"
TR "v[186:187],v2 offset:34816\n"
MF "v[36:39],a[8:11],v[188:191],v[36:39]\n"
MF "v[28:31],a[4:7],v[188:191],v[252:255]\n"
MF "v[28:31],a[44:47],a[240:243],v[28:31]\n"
MF "v[28:31],a[84:87],a[244:247],v[28:31]\n"
MF "v[20:23],a[80:83],a[244:247],v[252:255]\n"
MF "v[20:23],a[40:43],a[240:243],v[20:23]\n"
MF "v[20:23],a[0:3],v[188:191],v[20:23]\n"
"s_waitcnt lgkmcnt(0)\n"
MF "v[56:59],a[0:3],v[176:179],v[252:255]\n"
TR "v[188:189],v3 offset:24576\n"
MF "v[56:59],a[40:43],v[180:183],v[56:59]\n"
TR "v[190:191],v3 offset:26624\n"
MF "v[56:59],a[80:83],v[184:187],v[56:59]\n"
MF "v[64:67],a[84:87],v[184:187],v[252:255]\n"
TR "a[240:241],v3 offset:28672\n"
MF "v[64:67],a[44:47],v[180:183],v[64:67]\n"
TR "a[242:243],v3 offset:30720\n"
MF "v[64:67],a[4:7],v[176:179],v[64:67]\n"
MF "v[72:75],a[8:11],v[176:179],v[252:255]\n"
TR "a[244:245],v3 offset:32768\n"
MF "v[72:75],a[48:51],v[180:183],v[72:75]\n"
TR "a[246:247],v3 offset:34816\n"
MF "v[72:75],a[88:91],v[184:187],v[72:75]\n"
MF "v[80:83],a[92:95],v[184:187],v[252:255]\n"
MF "v[80:83],a[52:55],v[180:183],v[80:83]\n"
MF "v[80:83],a[12:15],v[176:179],v[80:83]\n"
MF "v[88:91],a[16:19],v[176:179],v[252:255]\n"
MF "v[88:91],a[56:59],v[180:183],v[88:91]\n"
"s_waitcnt lgkmcnt(0)\n"
MF "v[88:91],a[96:99],v[184:187],v[88:91]\n"
"s_barrier\n"
MF "v[92:95],a[96:99],a[244:247],v[252:255]\n"
TR "v[176:177],v0\n"
MF "v[92:95],a[56:59],a[240:243],v[92:95]\n"
TR "v[178:179],v0 offset:2048\n"
MF "v[92:95],a[16:19],v[188:191],v[92:95]\n"
"ds_read2st64_b32 v[192:193],v13 offset0:192 offset1:193\n"
MF "v[84:87],a[12:15],v[188:191],v[252:255]\n"
TR "v[180:181],v0 offset:4096\n"
MF "v[84:87],a[52:55],a[240:243],v[84:87]\n"
TR "v[182:183],v0 offset:6144\n"
MF "v[84:87],a[92:95],a[244:247],v[84:87]\n"
"ds_read2st64_b32 v[194:195],v13 offset0:194 offset1:195\n"
MF "v[76:79],a[88:91],a[244:247],v[252:255]\n"
TR "v[184:185],v0 offset:8192\n"
MF "v[76:79],a[48:51],a[240:243],v[76:79]\n"
TR "v[186:187],v0 offset:10240\n"
MF "v[76:79],a[8:11],v[188:191],v[76:79]\n"
"ds_read2st64_b32 v[196:197],v13 offset0:196 offset1:197\n"
MF "v[68:71],a[4:7],v[188:191],v[252:255]\n"
"ds_read2st64_b32 v[198:199],v13 offset0:198 offset1:199\n"
MF "v[68:71],a[44:47],a[240:243],v[68:71]\n"
MF "v[68:71],a[84:87],a[244:247],v[68:71]\n"
MF "v[60:63],a[80:83],a[244:247],v[252:255]\n"
MF "v[60:63],a[40:43],a[240:243],v[60:63]\n"
MF "v[60:63],a[0:3],v[188:191],v[60:63]\n"
"s_waitcnt lgkmcnt(2)\n"
MF "v[16:19],a[120:123],v[176:179],v[16:19]\n"
"s_waitcnt lgkmcnt(0)\n"
TR "v[188:189],v1\n"
"v_add_f32 v96,v96,v192\n"
MF "v[16:19],a[160:163],v[180:183],v[16:19]\n"
TR "v[190:191],v1 offset:2048\n"
"v_add_f32 v97,v97,v193\n"
MF "v[16:19],a[200:203],v[184:187],v[16:19]\n"
"ds_read2st64_b32 v[200:201],v13 offset0:200 offset1:201\n"
"v_add_f32 v98,v98,v194\n"
"s_waitcnt vmcnt(0)\n"
"v_mov_b32 v14,s76\n"
"s_add_u32 s76,s76,1\n"
MF "v[24:27],a[204:207],v[184:187],v[24:27]\n"
TR "a[240:241],v1 offset:4096\n"
"v_add_f32 v99,v99,v195\n"
"buffer_store_dword v14,v15,s[40:43],s94 offen sc1\n"
MF "v[24:27],a[164:167],v[180:183],v[24:27]\n"
TR "a[242:243],v1 offset:6144\n"
"v_add_f32 v100,v100,v196\n"
MF "v[24:27],a[124:127],v[176:179],v[24:27]\n"
"ds_read2st64_b32 v[202:203],v13 offset0:202 offset1:203\n"
"v_add_f32 v101,v101,v197\n"
MF "v[32:35],a[128:131],v[176:179],v[32:35]\n"
TR "a[244:245],v1 offset:8192\n"
"v_add_f32 v102,v102,v198\n"
MF "v[32:35],a[168:171],v[180:183],v[32:35]\n"
TR "a[246:247],v1 offset:10240\n"
"v_add_f32 v103,v103,v199\n"
MF "v[32:35],a[208:211],v[184:187],v[32:35]\n"
"ds_read2st64_b32 v[204:205],v13 offset0:204 offset1:205\n"
CV "v96,v96,v97\n"
MF "v[40:43],a[212:215],v[184:187],v[40:43]\n"
"ds_read2st64_b32 v[206:207],v13 offset0:206 offset1:207\n"
CV "v97,v98,v99\n"
MF "v[40:43],a[172:175],v[180:183],v[40:43]\n"
"s_add_u32 s87,s90,0\n"
CV "v98,v100,v101\n"
CV "v99,v102,v103\n"
MF "v[40:43],a[132:135],v[176:179],v[40:43]\n"
MF "v[48:51],a[136:139],v[176:179],v[48:51]\n"
"buffer_store_dwordx4 v[96:99],v9,s[40:43],s87 offen sc1\n"
MF "v[48:51],a[176:179],v[180:183],v[48:51]\n"
MF "v[48:51],a[216:219],v[184:187],v[48:51]\n"
"s_waitcnt lgkmcnt(2)\n"
MF "v[52:55],a[216:219],a[244:247],v[52:55]\n"
"s_waitcnt lgkmcnt(0)\n"
TR "v[176:177],v2\n"
"v_add_f32 v104,v104,v200\n"
MF "v[52:55],a[176:179],a[240:243],v[52:55]\n"
TR "v[178:179],v2 offset:2048\n"
"v_add_f32 v105,v105,v201\n"
MF "v[52:55],a[136:139],v[188:191],v[52:55]\n"
"ds_read2st64_b32 v[192:193],v13 offset0:208 offset1:209\n"
"v_add_f32 v106,v106,v202\n"
MF "v[44:47],a[132:135],v[188:191],v[44:47]\n"
TR "v[180:181],v2 offset:4096\n"
"v_add_f32 v107,v107,v203\n"
MF "v[44:47],a[172:175],a[240:243],v[44:47]\n"
TR "v[182:183],v2 offset:6144\n"
"v_add_f32 v108,v108,v204\n"
MF "v[44:47],a[212:215],a[244:247],v[44:47]\n"
"ds_read2st64_b32 v[194:195],v13 offset0:210 offset1:211\n"
"v_add_f32 v109,v109,v205\n"
MF "v[36:39],a[208:211],a[244:247],v[36:39]\n"
TR "v[184:185],v2 offset:8192\n"
"v_add_f32 v110,v110,v206\n"
MF "v[36:39],a[168:171],a[240:243],v[36:39]\n"
TR "v[186:187],v2 offset:10240\n"
"v_add_f32 v111,v111,v207\n"
MF "v[36:39],a[128:131],v[188:191],v[36:39]\n"
"ds_read2st64_b32 v[196:197],v13 offset0:212 offset1:213\n"
CV "v104,v104,v105\n"
MF "v[28:31],a[124:127],v[188:191],v[28:31]\n"
"ds_read2st64_b32 v[198:199],v13 offset0:214 offset1:215\n"
CV "v105,v106,v107\n"
MF "v[28:31],a[164:167],a[240:243],v[28:31]\n"
"s_add_u32 s87,s90,1024\n"
CV "v106,v108,v109\n"
CV "v107,v110,v111\n"
MF "v[28:31],a[204:207],a[244:247],v[28:31]\n"
MF "v[20:23],a[200:203],a[244:247],v[20:23]\n"
"buffer_store_dwordx4 v[104:107],v9,s[40:43],s87 offen sc1\n"
MF "v[20:23],a[160:163],a[240:243],v[20:23]\n"
MF "v[20:23],a[120:123],v[188:191],v[20:23]\n"
"s_waitcnt lgkmcnt(2)\n"
MF "v[56:59],a[120:123],v[176:179],v[56:59]\n"
"s_waitcnt lgkmcnt(0)\n"
TR "v[188:189],v3\n"
"v_add_f32 v112,v112,v192\n"
MF "v[56:59],a[160:163],v[180:183],v[56:59]\n"
TR "v[190:191],v3 offset:2048\n"
"v_add_f32 v113,v113,v193\n"
MF "v[56:59],a[200:203],v[184:187],v[56:59]\n"
"ds_read2st64_b32 v[200:201],v13 offset0:216 offset1:217\n"
"v_add_f32 v114,v114,v194\n"
MF "v[64:67],a[204:207],v[184:187],v[64:67]\n"
".Lpg34h:\n"
"s_cbranch_execz .Lpg38h\n"
TR "a[240:241],v3 offset:4096\n"
"v_add_f32 v115,v115,v195\n"
MF "v[64:67],a[164:167],v[180:183],v[64:67]\n"
TR "a[242:243],v3 offset:6144\n"
"v_add_f32 v116,v116,v196\n"
MF "v[64:67],a[124:127],v[176:179],v[64:67]\n"
"ds_read2st64_b32 v[202:203],v13 offset0:218 offset1:219\n"
"v_add_f32 v117,v117,v197\n"
MF "v[72:75],a[128:131],v[176:179],v[72:75]\n"
TR "a[244:245],v3 offset:8192\n"
"v_add_f32 v118,v118,v198\n"
MF "v[72:75],a[168:171],v[180:183],v[72:75]\n"
TR "a[246:247],v3 offset:10240\n"
"v_add_f32 v119,v119,v199\n"
MF "v[72:75],a[208:211],v[184:187],v[72:75]\n"
"ds_read2st64_b32 v[204:205],v13 offset0:220 offset1:221\n"
CV "v112,v112,v113\n"
MF "v[80:83],a[212:215],v[184:187],v[80:83]\n"
"ds_read2st64_b32 v[206:207],v13 offset0:222 offset1:223\n"
CV "v113,v114,v115\n"
MF "v[80:83],a[172:175],v[180:183],v[80:83]\n"
"s_add_u32 s87,s90,2048\n"
CV "v114,v116,v117\n"
CV "v115,v118,v119\n"
MF "v[80:83],a[132:135],v[176:179],v[80:83]\n"
MF "v[88:91],a[136:139],v[176:179],v[88:91]\n"
"buffer_store_dwordx4 v[112:115],v9,s[40:43],s87 offen sc1\n"
MF "v[88:91],a[176:179],v[180:183],v[88:91]\n"
"s_waitcnt lgkmcnt(0)\n"
MF "v[88:91],a[216:219],v[184:187],v[88:91]\n"
"s_barrier\n"
MF "v[92:95],a[216:219],a[244:247],v[92:95]\n"
TR "v[176:177],v0 offset:49152\n"
"v_add_f32 v120,v120,v200\n"
MF "v[92:95],a[176:179],a[240:243],v[92:95]\n"
TR "v[178:179],v0 offset:51200\n"
"v_add_f32 v121,v121,v201\n"
MF "v[92:95],a[136:139],v[188:191],v[92:95]\n"
"ds_read2st64_b32 v[192:193],v13 offset0:224 offset1:225\n"
"v_add_f32 v122,v122,v202\n"
MF "v[84:87],a[132:135],v[188:191],v[84:87]\n"
TR "v[180:181],v0 offset:53248\n"
"v_add_f32 v123,v123,v203\n"
MF "v[84:87],a[172:175],a[240:243],v[84:87]\n"
TR "v[182:183],v0 offset:55296\n"
"v_add_f32 v124,v124,v204\n"
MF "v[84:87],a[212:215],a[244:247],v[84:87]\n"
"ds_read2st64_b32 v[194:195],v13 offset0:226 offset1:227\n"
"v_add_f32 v125,v125,v205\n"
MF "v[76:79],a[208:211],a[244:247],v[76:79]\n"
TR "v[184:185],v0 offset:57344\n"
"v_add_f32 v126,v126,v206\n"
MF "v[76:79],a[168:171],a[240:243],v[76:79]\n"
TR "v[186:187],v0 offset:59392\n"
"v_add_f32 v127,v127,v207\n"
MF "v[76:79],a[128:131],v[188:191],v[76:79]\n"
"ds_read2st64_b32 v[196:197],v13 offset0:228 offset1:229\n"
CV "v120,v120,v121\n"
MF "v[68:71],a[124:127],v[188:191],v[68:71]\n"
"ds_read2st64_b32 v[198:199],v13 offset0:230 offset1:231\n"
CV "v121,v122,v123\n"
MF "v[68:71],a[164:167],a[240:243],v[68:71]\n"
"s_add_u32 s87,s90,3072\n"
CV "v122,v124,v125\n"
CV "v123,v126,v127\n"
MF "v[68:71],a[204:207],a[244:247],v[68:71]\n"
MF "v[60:63],a[200:203],a[244:247],v[60:63]\n"
"buffer_store_dwordx4 v[120:123],v9,s[40:43],s87 offen sc1\n"
MF "v[60:63],a[160:163],a[240:243],v[60:63]\n"
MF "v[60:63],a[120:123],v[188:191],v[60:63]\n"
"s_waitcnt lgkmcnt(2)\n"
MF "v[16:19],a[20:23],v[176:179],v[16:19]\n"
"s_waitcnt lgkmcnt(0)\n"
TR "v[188:189],v1 offset:49152\n"
"v_add_f32 v128,v128,v192\n"
MF "v[16:19],a[60:63],v[180:183],v[16:19]\n"
TR "v[190:191],v1 offset:51200\n"
"v_add_f32 v129,v129,v193\n"
MF "v[16:19],a[100:103],v[184:187],v[16:19]\n"
"ds_read2st64_b32 v[200:201],v13 offset0:0 offset1:1\n"
"v_add_f32 v130,v130,v194\n"
MF "v[24:27],a[104:107],v[184:187],v[24:27]\n"
TR "a[240:241],v1 offset:53248\n"
"v_add_f32 v131,v131,v195\n"
MF "v[24:27],a[64:67],v[180:183],v[24:27]\n"
TR "a[242:243],v1 offset:55296\n"
"v_add_f32 v132,v132,v196\n"
MF "v[24:27],a[24:27],v[176:179],v[24:27]\n"
"ds_read2st64_b32 v[202:203],v13 offset0:2 offset1:3\n"
"v_add_f32 v133,v133,v197\n"
MF "v[32:35],a[28:31],v[176:179],v[32:35]\n"
TR "a[244:245],v1 offset:57344\n"
"v_add_f32 v134,v134,v198\n"
MF "v[32:35],a[68:71],v[180:183],v[32:35]\n"
TR "a[246:247],v1 offset:59392\n"
"v_add_f32 v135,v135,v199\n"
MF "v[32:35],a[108:111],v[184:187],v[32:35]\n"
"ds_read2st64_b32 v[204:205],v13 offset0:4 offset1:5\n"
CV "v128,v128,v129\n"
MF "v[40:43],a[112:115],v[184:187],v[40:43]\n"
"ds_read2st64_b32 v[206:207],v13 offset0:6 offset1:7\n"
CV "v129,v130,v131\n"
MF "v[40:43],a[72:75],v[180:183],v[40:43]\n"
"s_add_u32 s87,s90,4096\n"
CV "v130,v132,v133\n"
CV "v131,v134,v135\n"
MF "v[40:43],a[32:35],v[176:179],v[40:43]\n"
MF "v[48:51],a[36:39],v[176:179],v[48:51]\n"
"buffer_store_dwordx4 v[128:131],v9,s[40:43],s87 offen sc1\n"
MF "v[48:51],a[76:79],v[180:183],v[48:51]\n"
MF "v[48:51],a[116:119],v[184:187],v[48:51]\n"
"s_waitcnt lgkmcnt(2)\n"
MF "v[52:55],a[116:119],a[244:247],v[52:55]\n"
"s_waitcnt lgkmcnt(0)\n"
TR "v[176:177],v2 offset:49152\n"
"v_add_f32 v136,v136,v200\n"
MF "v[52:55],a[76:79],a[240:243],v[52:55]\n"
TR "v[178:179],v2 offset:51200\n"
"v_add_f32 v137,v137,v201\n"
MF "v[52:55],a[36:39],v[188:191],v[52:55]\n"
"ds_read2st64_b32 v[192:193],v13 offset0:8 offset1:9\n"
"v_add_f32 v138,v138,v202\n"
MF "v[44:47],a[32:35],v[188:191],v[44:47]\n"
TR "v[180:181],v2 offset:53248\n"
"v_add_f32 v139,v139,v203\n"
MF "v[44:47],a[72:75],a[240:243],v[44:47]\n"
TR "v[182:183],v2 offset:55296\n"
"v_add_f32 v140,v140,v204\n"
MF "v[44:47],a[112:115],a[244:247],v[44:47]\n"
"ds_read2st64_b32 v[194:195],v13 offset0:10 offset1:11\n"
"v_add_f32 v141,v141,v205\n"
MF "v[36:39],a[108:111],a[244:247],v[36:39]\n"
TR "v[184:185],v2 offset:57344\n"
"v_add_f32 v142,v142,v206\n"
MF "v[36:39],a[68:71],a[240:243],v[36:39]\n"
TR "v[186:187],v2 offset:59392\n"
"v_add_f32 v143,v143,v207\n"
MF "v[36:39],a[28:31],v[188:191],v[36:39]\n"
"ds_read2st64_b32 v[196:197],v13 offset0:12 offset1:13\n"
CV "v136,v136,v137\n"
MF "v[28:31],a[24:27],v[188:191],v[28:31]\n"
"ds_read2st64_b32 v[198:199],v13 offset0:14 offset1:15\n"
CV "v137,v138,v139\n"
MF "v[28:31],a[64:67],a[240:243],v[28:31]\n"
"s_add_u32 s87,s90,10240\n"
CV "v138,v140,v141\n"
CV "v139,v142,v143\n"
MF "v[28:31],a[104:107],a[244:247],v[28:31]\n"
MF "v[20:23],a[100:103],a[244:247],v[20:23]\n"
"buffer_store_dwordx4 v[136:139],v9,s[40:43],s87 offen sc1\n"
MF "v[20:23],a[60:63],a[240:243],v[20:23]\n"
MF "v[20:23],a[20:23],v[188:191],v[20:23]\n"
"s_waitcnt lgkmcnt(2)\n"
MF "v[56:59],a[20:23],v[176:179],v[56:59]\n"
"s_waitcnt lgkmcnt(0)\n"
TR "v[188:189],v3 offset:49152\n"
"v_add_f32 v144,v144,v192\n"
MF "v[56:59],a[60:63],v[180:183],v[56:59]\n"
TR "v[190:191],v3 offset:51200\n"
"v_add_f32 v145,v145,v193\n"
MF "v[56:59],a[100:103],v[184:187],v[56:59]\n"
"ds_read2st64_b32 v[200:201],v13 offset0:16 offset1:17\n"
"v_add_f32 v146,v146,v194\n"
MF "v[64:67],a[104:107],v[184:187],v[64:67]\n"
TR "a[240:241],v3 offset:53248\n"
"v_add_f32 v147,v147,v195\n"
MF "v[64:67],a[64:67],v[180:183],v[64:67]\n"
TR "a[242:243],v3 offset:55296\n"
"v_add_f32 v148,v148,v196\n"
MF "v[64:67],a[24:27],v[176:179],v[64:67]\n"
"ds_read2st64_b32 v[202:203],v13 offset0:18 offset1:19\n"
"v_add_f32 v149,v149,v197\n"
MF "v[72:75],a[28:31],v[176:179],v[72:75]\n"
TR "a[244:245],v3 offset:57344\n"
"v_add_f32 v150,v150,v198\n"
MF "v[72:75],a[68:71],v[180:183],v[72:75]\n"
TR "a[246:247],v3 offset:59392\n"
"v_add_f32 v151,v151,v199\n"
MF "v[72:75],a[108:111],v[184:187],v[72:75]\n"
"ds_read2st64_b32 v[204:205],v13 offset0:20 offset1:21\n"
CV "v144,v144,v145\n"
MF "v[80:83],a[112:115],v[184:187],v[80:83]\n"
"ds_read2st64_b32 v[206:207],v13 offset0:22 offset1:23\n"
CV "v145,v146,v147\n"
MF "v[80:83],a[72:75],v[180:183],v[80:83]\n"
"s_add_u32 s87,s90,11264\n"
CV "v146,v148,v149\n"
CV "v147,v150,v151\n"
MF "v[80:83],a[32:35],v[176:179],v[80:83]\n"
MF "v[88:91],a[36:39],v[176:179],v[88:91]\n"
"buffer_store_dwordx4 v[144:147],v9,s[40:43],s87 offen sc1\n"
MF "v[88:91],a[76:79],v[180:183],v[88:91]\n"
"s_waitcnt lgkmcnt(0)\n"
MF "v[88:91],a[116:119],v[184:187],v[88:91]\n"
"s_barrier\n"
MF "v[92:95],a[116:119],a[244:247],v[92:95]\n"
TR "v[176:177],v4\n"
"v_add_f32 v152,v152,v200\n"
MF "v[92:95],a[76:79],a[240:243],v[92:95]\n"
TR "v[178:179],v4 offset:2048\n"
"v_add_f32 v153,v153,v201\n"
MF "v[92:95],a[36:39],v[188:191],v[92:95]\n"
"ds_read2st64_b32 v[192:193],v13 offset0:24 offset1:25\n"
"v_add_f32 v154,v154,v202\n"
MF "v[84:87],a[32:35],v[188:191],v[84:87]\n"
TR "v[180:181],v4 offset:4096\n"
"v_add_f32 v155,v155,v203\n"
MF "v[84:87],a[72:75],a[240:243],v[84:87]\n"
TR "v[182:183],v4 offset:6144\n"
"v_add_f32 v156,v156,v204\n"
MF "v[84:87],a[112:115],a[244:247],v[84:87]\n"
"ds_read2st64_b32 v[194:195],v13 offset0:26 offset1:27\n"
"v_add_f32 v157,v157,v205\n"
MF "v[76:79],a[108:111],a[244:247],v[76:79]\n"
TR "v[184:185],v4 offset:8192\n"
"v_add_f32 v158,v158,v206\n"
MF "v[76:79],a[68:71],a[240:243],v[76:79]\n"
TR "v[186:187],v4 offset:10240\n"
"v_add_f32 v159,v159,v207\n"
MF "v[76:79],a[28:31],v[188:191],v[76:79]\n"
"ds_read2st64_b32 v[196:197],v13 offset0:28 offset1:29\n"
CV "v152,v152,v153\n"
MF "v[68:71],a[24:27],v[188:191],v[68:71]\n"
"ds_read2st64_b32 v[198:199],v13 offset0:30 offset1:31\n"
CV "v153,v154,v155\n"
MF "v[68:71],a[64:67],a[240:243],v[68:71]\n"
"s_add_u32 s87,s90,12288\n"
CV "v154,v156,v157\n"
CV "v155,v158,v159\n"
MF "v[68:71],a[104:107],a[244:247],v[68:71]\n"
MF "v[60:63],a[100:103],a[244:247],v[60:63]\n"
"buffer_store_dwordx4 v[152:155],v9,s[40:43],s87 offen sc1\n"
MF "v[60:63],a[60:63],a[240:243],v[60:63]\n"
MF "v[60:63],a[20:23],v[188:191],v[60:63]\n"
"s_waitcnt lgkmcnt(2)\n"
MF "v[16:19],a[140:143],v[176:179],v[16:19]\n"
"s_waitcnt lgkmcnt(0)\n"
TR "v[188:189],v5\n"
"v_add_f32 v160,v160,v192\n"
MF "v[16:19],a[180:183],v[180:183],v[16:19]\n"
TR "v[190:191],v5 offset:2048\n"
"v_add_f32 v161,v161,v193\n"
MF "v[16:19],a[220:223],v[184:187],v[16:19]\n"
"ds_read2st64_b32 v[200:201],v13 offset0:32 offset1:33\n"
"v_add_f32 v162,v162,v194\n"
MF "v[24:27],a[224:227],v[184:187],v[24:27]\n"
TR "a[240:241],v5 offset:4096\n"
"v_add_f32 v163,v163,v195\n"
MF "v[24:27],a[184:187],v[180:183],v[24:27]\n"
TR "a[242:243],v5 offset:6144\n"
"v_add_f32 v164,v164,v196\n"
MF "v[24:27],a[144:147],v[176:179],v[24:27]\n"
"ds_read2st64_b32 v[202:203],v13 offset0:34 offset1:35\n"
"v_add_f32 v165,v165,v197\n"
MF "v[32:35],a[148:151],v[176:179],v[32:35]\n"
TR "a[244:245],v5 offset:8192\n"
"v_add_f32 v166,v166,v198\n"
MF "v[32:35],a[188:191],v[180:183],v[32:35]\n"
TR "a[246:247],v5 offset:10240\n"
"v_add_f32 v167,v167,v199\n"
MF "v[32:35],a[228:231],v[184:187],v[32:35]\n"
"ds_read2st64_b32 v[204:205],v13 offset0:36 offset1:37\n"
CV "v160,v160,v161\n"
MF "v[40:43],a[232:235],v[184:187],v[40:43]\n"
"ds_read2st64_b32 v[206:207],v13 offset0:38 offset1:39\n"
CV "v161,v162,v163\n"
MF "v[40:43],a[192:195],v[180:183],v[40:43]\n"
"s_add_u32 s87,s90,13312\n"
CV "v162,v164,v165\n"
CV "v163,v166,v167\n"
MF "v[40:43],a[152:155],v[176:179],v[40:43]\n"
MF "v[48:51],a[156:159],v[176:179],v[48:51]\n"
"buffer_store_dwordx4 v[160:163],v9,s[40:43],s87 offen sc1\n"
MF "v[48:51],a[196:199],v[180:183],v[48:51]\n"
MF "v[48:51],a[236:239],v[184:187],v[48:51]\n"
"s_waitcnt lgkmcnt(2)\n"
MF "v[52:55],a[236:239],a[244:247],v[52:55]\n"
"s_waitcnt lgkmcnt(0)\n"
TR "v[176:177],v6\n"
"v_add_f32 v168,v168,v200\n"
MF "v[52:55],a[196:199],a[240:243],v[52:55]\n"
TR "v[178:179],v6 offset:2048\n"
"v_add_f32 v169,v169,v201\n"
MF "v[52:55],a[156:159],v[188:191],v[52:55]\n"
"v_add_f32 v170,v170,v202\n"
"v_add_f32 v171,v171,v203\n"
MF "v[44:47],a[152:155],v[188:191],v[44:47]\n"
TR "v[180:181],v6 offset:4096\n"
"v_add_f32 v172,v172,v204\n"
MF "v[44:47],a[192:195],a[240:243],v[44:47]\n"
TR "v[182:183],v6 offset:6144\n"
"v_add_f32 v173,v173,v205\n"
MF "v[44:47],a[232:235],a[244:247],v[44:47]\n"
"v_add_f32 v174,v174,v206\n"
"v_add_f32 v175,v175,v207\n"
MF "v[36:39],a[228:231],a[244:247],v[36:39]\n"
TR "v[184:185],v6 offset:8192\n"
CV "v168,v168,v169\n"
MF "v[36:39],a[188:191],a[240:243],v[36:39]\n"
TR "v[186:187],v6 offset:10240\n"
CV "v169,v170,v171\n"
MF "v[36:39],a[148:151],v[188:191],v[36:39]\n"
"s_add_u32 s87,s90,14336\n"
CV "v170,v172,v173\n"
CV "v171,v174,v175\n"
MF "v[28:31],a[144:147],v[188:191],v[28:31]\n"
MF "v[28:31],a[184:187],a[240:243],v[28:31]\n"
MF "v[28:31],a[224:227],a[244:247],v[28:31]\n"
MF "v[20:23],a[220:223],a[244:247],v[20:23]\n"
"buffer_store_dwordx4 v[168:171],v9,s[40:43],s87 offen sc1\n"
MF "v[20:23],a[180:183],a[240:243],v[20:23]\n"
"s_mov_b32 s90,s91\n"
"s_add_u32 s91,s91,20480\n"
MF "v[20:23],a[140:143],v[188:191],v[20:23]\n"
"s_waitcnt lgkmcnt(0)\n"
MF "v[56:59],a[140:143],v[176:179],v[56:59]\n"
TR "v[188:189],v7\n"
MF "v[56:59],a[180:183],v[180:183],v[56:59]\n"
TR "v[190:191],v7 offset:2048\n"
MF "v[56:59],a[220:223],v[184:187],v[56:59]\n"
"v_mov_b32 v244,0\n"
"v_mov_b32 v245,0\n"
MF "v[64:67],a[224:227],v[184:187],v[64:67]\n"
TR "a[240:241],v7 offset:4096\n"
"v_mov_b32 v246,0\n"
MF "v[64:67],a[184:187],v[180:183],v[64:67]\n"
TR "a[242:243],v7 offset:6144\n"
"v_mov_b32 v247,0\n"
MF "v[64:67],a[144:147],v[176:179],v[64:67]\n"
"v_mov_b32 v248,0\n"
"v_mov_b32 v249,0\n"
MF "v[72:75],a[148:151],v[176:179],v[72:75]\n"
TR "a[244:245],v7 offset:8192\n"
"v_mov_b32 v250,0\n"
MF "v[72:75],a[188:191],v[180:183],v[72:75]\n"
TR "a[246:247],v7 offset:10240\n"
"v_mov_b32 v251,0\n"
MF "v[72:75],a[228:231],v[184:187],v[72:75]\n"
MF "v[80:83],a[232:235],v[184:187],v[80:83]\n"
MF "v[80:83],a[192:195],v[180:183],v[80:83]\n"
MF "v[80:83],a[152:155],v[176:179],v[80:83]\n"
MF "v[88:91],a[156:159],v[176:179],v[88:91]\n"
MF "v[88:91],a[196:199],v[180:183],v[88:91]\n"
"s_waitcnt lgkmcnt(0)\n"
MF "v[88:91],a[236:239],v[184:187],v[88:91]\n"
"s_barrier\n"
MF "v[92:95],a[236:239],a[244:247],v[92:95]\n"
TR "v[176:177],v0 offset:24576\n"
MF "v[92:95],a[196:199],a[240:243],v[92:95]\n"
"s_add_u32 s85,s85,1\n"
TR "v[178:179],v0 offset:26624\n"
MF "v[92:95],a[156:159],v[188:191],v[92:95]\n"
MF "v[84:87],a[152:155],v[188:191],v[84:87]\n"
TR "v[180:181],v0 offset:28672\n"
MF "v[84:87],a[192:195],a[240:243],v[84:87]\n"
TR "v[182:183],v0 offset:30720\n"
MF "v[84:87],a[232:235],a[244:247],v[84:87]\n"
MF "v[76:79],a[228:231],a[244:247],v[76:79]\n"
TR "v[184:185],v0 offset:32768\n"
MF "v[76:79],a[188:191],a[240:243],v[76:79]\n"
TR "v[186:187],v0 offset:34816\n"
MF "v[76:79],a[148:151],v[188:191],v[76:79]\n"
MF "v[68:71],a[144:147],v[188:191],v[68:71]\n"
MF "v[68:71],a[184:187],a[240:243],v[68:71]\n"
MF "v[68:71],a[224:227],a[244:247],v[68:71]\n"
MF "v[60:63],a[220:223],a[244:247],v[60:63]\n"
MF "v[60:63],a[180:183],a[240:243],v[60:63]\n"
MF "v[60:63],a[140:143],v[188:191],v[60:63]\n"
"s_waitcnt lgkmcnt(0)\n"
"s_cmp_lg_u32 s85,5\n"
"s_cbranch_scc1 .Lpl15\n"
".Lpl19:\n"
MF "v[96:99],a[0:3],v[176:179],v[252:255]\n"
TR "v[188:189],v1 offset:24576\n"
MF "v[96:99],a[40:43],v[180:183],v[96:99]\n"
"s_add_u32 s82,s82,4096\n"
TR "v[190:191],v1 offset:26624\n"
MF "v[96:99],a[80:83],v[184:187],v[96:99]\n"
LD "v[224:227],v9,s[40:43],s82 offen sc1\n"
MF "v[104:107],a[84:87],v[184:187],v[252:255]\n"
TR "a[240:241],v1 offset:28672\n"
MF "v[104:107],a[44:47],v[180:183],v[104:107]\n"
TR "a[242:243],v1 offset:30720\n"
MF "v[104:107],a[4:7],v[176:179],v[104:107]\n"
MF "v[112:115],a[8:11],v[176:179],v[252:255]\n"
TR "a[244:245],v1 offset:32768\n"
MF "v[112:115],a[48:51],v[180:183],v[112:115]\n"
TR "a[246:247],v1 offset:34816\n"
MF "v[112:115],a[88:91],v[184:187],v[112:115]\n"
MF "v[120:123],a[92:95],v[184:187],v[252:255]\n"
MF "v[120:123],a[52:55],v[180:183],v[120:123]\n"
MF "v[120:123],a[12:15],v[176:179],v[120:123]\n"
MF "v[128:131],a[16:19],v[176:179],v[252:255]\n"
MF "v[128:131],a[56:59],v[180:183],v[128:131]\n"
MF "v[128:131],a[96:99],v[184:187],v[128:131]\n"
"s_waitcnt lgkmcnt(0)\n"
MF "v[132:135],a[96:99],a[244:247],v[252:255]\n"
TR "v[176:177],v2 offset:24576\n"
MF "v[132:135],a[56:59],a[240:243],v[132:135]\n"
TR "v[178:179],v2 offset:26624\n"
MF "v[132:135],a[16:19],v[188:191],v[132:135]\n"
MF "v[124:127],a[12:15],v[188:191],v[252:255]\n"
TR "v[180:181],v2 offset:28672\n"
MF "v[124:127],a[52:55],a[240:243],v[124:127]\n"
TR "v[182:183],v2 offset:30720\n"
MF "v[124:127],a[92:95],a[244:247],v[124:127]\n"
MF "v[116:119],a[88:91],a[244:247],v[252:255]\n"
TR "v[184:185],v2 offset:32768\n"
MF "v[116:119],a[48:51],a[240:243],v[116:119]\n"
TR "v[186:187],v2 offset:34816\n"
MF "v[116:119],a[8:11],v[188:191],v[116:119]\n"
MF "v[108:111],a[4:7],v[188:191],v[252:255]\n"
MF "v[108:111],a[44:47],a[240:243],v[108:111]\n"
MF "v[108:111],a[84:87],a[244:247],v[108:111]\n"
MF "v[100:103],a[80:83],a[244:247],v[252:255]\n"
MF "v[100:103],a[40:43],a[240:243],v[100:103]\n"
MF "v[100:103],a[0:3],v[188:191],v[100:103]\n"
"s_waitcnt lgkmcnt(0)\n"
MF "v[136:139],a[0:3],v[176:179],v[252:255]\n"
TR "v[188:189],v3 offset:24576\n"
MF "v[136:139],a[40:43],v[180:183],v[136:139]\n"
TR "v[190:191],v3 offset:26624\n"
MF "v[136:139],a[80:83],v[184:187],v[136:139]\n"
MF "v[144:147],a[84:87],v[184:187],v[252:255]\n"
TR "a[240:241],v3 offset:28672\n"
MF "v[144:147],a[44:47],v[180:183],v[144:147]\n"
TR "a[242:243],v3 offset:30720\n"
MF "v[144:147],a[4:7],v[176:179],v[144:147]\n"
MF "v[152:155],a[8:11],v[176:179],v[252:255]\n"
TR "a[244:245],v3 offset:32768\n"
MF "v[152:155],a[48:51],v[180:183],v[152:155]\n"
TR "a[246:247],v3 offset:34816\n"
MF "v[152:155],a[88:91],v[184:187],v[152:155]\n"
MF "v[160:163],a[92:95],v[184:187],v[252:255]\n"
MF "v[160:163],a[52:55],v[180:183],v[160:163]\n"
MF "v[160:163],a[12:15],v[176:179],v[160:163]\n"
MF "v[168:171],a[16:19],v[176:179],v[252:255]\n"
MF "v[168:171],a[56:59],v[180:183],v[168:171]\n"
"s_waitcnt lgkmcnt(0)\n"
MF "v[168:171],a[96:99],v[184:187],v[168:171]\n"
"s_barrier\n"
MF "v[172:175],a[96:99],a[244:247],v[252:255]\n"
TR "v[176:177],v0\n"
MF "v[172:175],a[56:59],a[240:243],v[172:175]\n"
TR "v[178:179],v0 offset:2048\n"
MF "v[172:175],a[16:19],v[188:191],v[172:175]\n"
"ds_read2st64_b32 v[192:193],v13 offset0:192 offset1:193\n"
MF "v[164:167],a[12:15],v[188:191],v[252:255]\n"
TR "v[180:181],v0 offset:4096\n"
MF "v[164:167],a[52:55],a[240:243],v[164:167]\n"
TR "v[182:183],v0 offset:6144\n"
MF "v[164:167],a[92:95],a[244:247],v[164:167]\n"
"ds_read2st64_b32 v[194:195],v13 offset0:194 offset1:195\n"
MF "v[156:159],a[88:91],a[244:247],v[252:255]\n"
TR "v[184:185],v0 offset:8192\n"
MF "v[156:159],a[48:51],a[240:243],v[156:159]\n"
TR "v[186:187],v0 offset:10240\n"
MF "v[156:159],a[8:11],v[188:191],v[156:159]\n"
"ds_read2st64_b32 v[196:197],v13 offset0:196 offset1:197\n"
MF "v[148:151],a[4:7],v[188:191],v[252:255]\n"
"ds_read2st64_b32 v[198:199],v13 offset0:198 offset1:199\n"
MF "v[148:151],a[44:47],a[240:243],v[148:151]\n"
"buffer_load_dword v10,v12,s[40:43],s86 offen sc1\n"
MF "v[148:151],a[84:87],a[244:247],v[148:151]\n"
MF "v[140:143],a[80:83],a[244:247],v[252:255]\n"
MF "v[140:143],a[40:43],a[240:243],v[140:143]\n"
MF "v[140:143],a[0:3],v[188:191],v[140:143]\n"
"s_waitcnt lgkmcnt(2)\n"
MF "v[96:99],a[120:123],v[176:179],v[96:99]\n"
"s_waitcnt lgkmcnt(0)\n"
TR "v[188:189],v1\n"
"v_add_f32 v16,v16,v192\n"
MF "v[96:99],a[160:163],v[180:183],v[96:99]\n"
TR "v[190:191],v1 offset:2048\n"
"v_add_f32 v17,v17,v193\n"
MF "v[96:99],a[200:203],v[184:187],v[96:99]\n"
"ds_read2st64_b32 v[200:201],v13 offset0:200 offset1:201\n"
"v_add_f32 v18,v18,v194\n"
"s_waitcnt vmcnt(2)\n"
"v_mov_b32 v14,s76\n"
"s_add_u32 s76,s76,1\n"
MF "v[104:107],a[204:207],v[184:187],v[104:107]\n"
TR "a[240:241],v1 offset:4096\n"
"v_add_f32 v19,v19,v195\n"
"buffer_store_dword v14,v15,s[40:43],s94 offen sc1\n"
MF "v[104:107],a[164:167],v[180:183],v[104:107]\n"
TR "a[242:243],v1 offset:6144\n"
"v_add_f32 v20,v20,v196\n"
MF "v[104:107],a[124:127],v[176:179],v[104:107]\n"
"ds_read2st64_b32 v[202:203],v13 offset0:202 offset1:203\n"
"v_add_f32 v21,v21,v197\n"
MF "v[112:115],a[128:131],v[176:179],v[112:115]\n"
TR "a[244:245],v1 offset:8192\n"
"v_add_f32 v22,v22,v198\n"
MF "v[112:115],a[168:171],v[180:183],v[112:115]\n"
TR "a[246:247],v1 offset:10240\n"
"v_add_f32 v23,v23,v199\n"
MF "v[112:115],a[208:211],v[184:187],v[112:115]\n"
"ds_read2st64_b32 v[204:205],v13 offset0:204 offset1:205\n"
CV "v16,v16,v17\n"
MF "v[120:123],a[212:215],v[184:187],v[120:123]\n"
"ds_read2st64_b32 v[206:207],v13 offset0:206 offset1:207\n"
CV "v17,v18,v19\n"
MF "v[120:123],a[172:175],v[180:183],v[120:123]\n"
"s_add_u32 s87,s90,0\n"
CV "v18,v20,v21\n"
CV "v19,v22,v23\n"
MF "v[120:123],a[132:135],v[176:179],v[120:123]\n"
MF "v[128:131],a[136:139],v[176:179],v[128:131]\n"
"buffer_store_dwordx4 v[16:19],v9,s[40:43],s87 offen sc1\n"
MF "v[128:131],a[176:179],v[180:183],v[128:131]\n"
MF "v[128:131],a[216:219],v[184:187],v[128:131]\n"
"s_waitcnt lgkmcnt(2)\n"
MF "v[132:135],a[216:219],a[244:247],v[132:135]\n"
"s_waitcnt lgkmcnt(0)\n"
TR "v[176:177],v2\n"
"v_add_f32 v24,v24,v200\n"
MF "v[132:135],a[176:179],a[240:243],v[132:135]\n"
TR "v[178:179],v2 offset:2048\n"
"v_add_f32 v25,v25,v201\n"
MF "v[132:135],a[136:139],v[188:191],v[132:135]\n"
"ds_read2st64_b32 v[192:193],v13 offset0:208 offset1:209\n"
"v_add_f32 v26,v26,v202\n"
MF "v[124:127],a[132:135],v[188:191],v[124:127]\n"
TR "v[180:181],v2 offset:4096\n"
"v_add_f32 v27,v27,v203\n"
MF "v[124:127],a[172:175],a[240:243],v[124:127]\n"
TR "v[182:183],v2 offset:6144\n"
"v_add_f32 v28,v28,v204\n"
MF "v[124:127],a[212:215],a[244:247],v[124:127]\n"
"ds_read2st64_b32 v[194:195],v13 offset0:210 offset1:211\n"
"v_add_f32 v29,v29,v205\n"
MF "v[116:119],a[208:211],a[244:247],v[116:119]\n"
TR "v[184:185],v2 offset:8192\n"
"v_add_f32 v30,v30,v206\n"
MF "v[116:119],a[168:171],a[240:243],v[116:119]\n"
TR "v[186:187],v2 offset:10240\n"
"v_add_f32 v31,v31,v207\n"
MF "v[116:119],a[128:131],v[188:191],v[116:119]\n"
"ds_read2st64_b32 v[196:197],v13 offset0:212 offset1:213\n"
CV "v24,v24,v25\n"
MF "v[108:111],a[124:127],v[188:191],v[108:111]\n"
"ds_read2st64_b32 v[198:199],v13 offset0:214 offset1:215\n"
CV "v25,v26,v27\n"
MF "v[108:111],a[164:167],a[240:243],v[108:111]\n"
"s_add_u32 s87,s90,1024\n"
CV "v26,v28,v29\n"
CV "v27,v30,v31\n"
MF "v[108:111],a[204:207],a[244:247],v[108:111]\n"
MF "v[100:103],a[200:203],a[244:247],v[100:103]\n"
"buffer_store_dwordx4 v[24:27],v9,s[40:43],s87 offen sc1\n"
MF "v[100:103],a[160:163],a[240:243],v[100:103]\n"
MF "v[100:103],a[120:123],v[188:191],v[100:103]\n"
"s_waitcnt lgkmcnt(2)\n"
MF "v[136:139],a[120:123],v[176:179],v[136:139]\n"
"s_waitcnt lgkmcnt(0)\n"
TR "v[188:189],v3\n"
"v_add_f32 v32,v32,v192\n"
MF "v[136:139],a[160:163],v[180:183],v[136:139]\n"
TR "v[190:191],v3 offset:2048\n"
"v_add_f32 v33,v33,v193\n"
MF "v[136:139],a[200:203],v[184:187],v[136:139]\n"
"ds_read2st64_b32 v[200:201],v13 offset0:216 offset1:217\n"
"v_add_f32 v34,v34,v194\n"
MF "v[144:147],a[204:207],v[184:187],v[144:147]\n"
TR "a[240:241],v3 offset:4096\n"
"v_add_f32 v35,v35,v195\n"
MF "v[144:147],a[164:167],v[180:183],v[144:147]\n"
TR "a[242:243],v3 offset:6144\n"
"v_add_f32 v36,v36,v196\n"
MF "v[144:147],a[124:127],v[176:179],v[144:147]\n"
"ds_read2st64_b32 v[202:203],v13 offset0:218 offset1:219\n"
"v_add_f32 v37,v37,v197\n"
MF "v[152:155],a[128:131],v[176:179],v[152:155]\n"
".Lpg35h:\n"
"s_cbranch_execz .Lisr4\n"
TR "a[244:245],v3 offset:8192\n"
"v_add_f32 v38,v38,v198\n"
MF "v[152:155],a[168:171],v[180:183],v[152:155]\n"
TR "a[246:247],v3 offset:10240\n"
"v_add_f32 v39,v39,v199\n"
MF "v[152:155],a[208:211],v[184:187],v[152:155]\n"
"ds_read2st64_b32 v[204:205],v13 offset0:220 offset1:221\n"
CV "v32,v32,v33\n"
MF "v[160:163],a[212:215],v[184:187],v[160:163]\n"
"ds_read2st64_b32 v[206:207],v13 offset0:222 offset1:223\n"
CV "v33,v34,v35\n"
MF "v[160:163],a[172:175],v[180:183],v[160:163]\n"
"s_add_u32 s87,s90,2048\n"
CV "v34,v36,v37\n"
CV "v35,v38,v39\n"
MF "v[160:163],a[132:135],v[176:179],v[160:163]\n"
MF "v[168:171],a[136:139],v[176:179],v[168:171]\n"
"buffer_store_dwordx4 v[32:35],v9,s[40:43],s87 offen sc1\n"
MF "v[168:171],a[176:179],v[180:183],v[168:171]\n"
"s_waitcnt lgkmcnt(0)\n"
MF "v[168:171],a[216:219],v[184:187],v[168:171]\n"
"s_barrier\n"
MF "v[172:175],a[216:219],a[244:247],v[172:175]\n"
TR "v[176:177],v0 offset:49152\n"
"v_add_f32 v40,v40,v200\n"
MF "v[172:175],a[176:179],a[240:243],v[172:175]\n"
"s_mov_b32 s82,s70\n"
TR "v[178:179],v0 offset:51200\n"
"v_add_f32 v41,v41,v201\n"
MF "v[172:175],a[136:139],v[188:191],v[172:175]\n"
"ds_read2st64_b32 v[192:193],v13 offset0:224 offset1:225\n"
"v_add_f32 v42,v42,v202\n"
MF "v[164:167],a[132:135],v[188:191],v[164:167]\n"
TR "v[180:181],v0 offset:53248\n"
"v_add_f32 v43,v43,v203\n"
MF "v[164:167],a[172:175],a[240:243],v[164:167]\n"
TR "v[182:183],v0 offset:55296\n"
"v_add_f32 v44,v44,v204\n"
MF "v[164:167],a[212:215],a[244:247],v[164:167]\n"
"ds_read2st64_b32 v[194:195],v13 offset0:226 offset1:227\n"
"v_add_f32 v45,v45,v205\n"
MF "v[156:159],a[208:211],a[244:247],v[156:159]\n"
TR "v[184:185],v0 offset:57344\n"
"v_add_f32 v46,v46,v206\n"
MF "v[156:159],a[168:171],a[240:243],v[156:159]\n"
TR "v[186:187],v0 offset:59392\n"
"v_add_f32 v47,v47,v207\n"
MF "v[156:159],a[128:131],v[188:191],v[156:159]\n"
"ds_read2st64_b32 v[196:197],v13 offset0:228 offset1:229\n"
CV "v40,v40,v41\n"
MF "v[148:151],a[124:127],v[188:191],v[148:151]\n"
"ds_read2st64_b32 v[198:199],v13 offset0:230 offset1:231\n"
CV "v41,v42,v43\n"
MF "v[148:151],a[164:167],a[240:243],v[148:151]\n"
"s_add_u32 s87,s90,3072\n"
CV "v42,v44,v45\n"
CV "v43,v46,v47\n"
MF "v[148:151],a[204:207],a[244:247],v[148:151]\n"
MF "v[140:143],a[200:203],a[244:247],v[140:143]\n"
"buffer_store_dwordx4 v[40:43],v9,s[40:43],s87 offen sc1\n"
MF "v[140:143],a[160:163],a[240:243],v[140:143]\n"
MF "v[140:143],a[120:123],v[188:191],v[140:143]\n"
"s_waitcnt lgkmcnt(2)\n"
MF "v[96:99],a[20:23],v[176:179],v[96:99]\n"
"s_waitcnt lgkmcnt(0)\n"
TR "v[188:189],v1 offset:49152\n"
"v_add_f32 v48,v48,v192\n"
MF "v[96:99],a[60:63],v[180:183],v[96:99]\n"
TR "v[190:191],v1 offset:51200\n"
"v_add_f32 v49,v49,v193\n"
MF "v[96:99],a[100:103],v[184:187],v[96:99]\n"
"ds_read2st64_b32 v[200:201],v13 offset0:0 offset1:1\n"
"v_add_f32 v50,v50,v194\n"
MF "v[104:107],a[104:107],v[184:187],v[104:107]\n"
TR "a[240:241],v1 offset:53248\n"
"v_add_f32 v51,v51,v195\n"
MF "v[104:107],a[64:67],v[180:183],v[104:107]\n"
TR "a[242:243],v1 offset:55296\n"
"v_add_f32 v52,v52,v196\n"
MF "v[104:107],a[24:27],v[176:179],v[104:107]\n"
"ds_read2st64_b32 v[202:203],v13 offset0:2 offset1:3\n"
"v_add_f32 v53,v53,v197\n"
MF "v[112:115],a[28:31],v[176:179],v[112:115]\n"
TR "a[244:245],v1 offset:57344\n"
"v_add_f32 v54,v54,v198\n"
MF "v[112:115],a[68:71],v[180:183],v[112:115]\n"
TR "a[246:247],v1 offset:59392\n"
"v_add_f32 v55,v55,v199\n"
MF "v[112:115],a[108:111],v[184:187],v[112:115]\n"
"ds_read2st64_b32 v[204:205],v13 offset0:4 offset1:5\n"
CV "v48,v48,v49\n"
MF "v[120:123],a[112:115],v[184:187],v[120:123]\n"
"ds_read2st64_b32 v[206:207],v13 offset0:6 offset1:7\n"
CV "v49,v50,v51\n"
MF "v[120:123],a[72:75],v[180:183],v[120:123]\n"
"s_add_u32 s87,s90,4096\n"
CV "v50,v52,v53\n"
CV "v51,v54,v55\n"
MF "v[120:123],a[32:35],v[176:179],v[120:123]\n"
MF "v[128:131],a[36:39],v[176:179],v[128:131]\n"
"buffer_store_dwordx4 v[48:51],v9,s[40:43],s87 offen sc1\n"
MF "v[128:131],a[76:79],v[180:183],v[128:131]\n"
MF "v[128:131],a[116:119],v[184:187],v[128:131]\n"
"s_waitcnt lgkmcnt(2)\n"
MF "v[132:135],a[116:119],a[244:247],v[132:135]\n"
"s_waitcnt lgkmcnt(0)\n"
TR "v[176:177],v2 offset:49152\n"
"v_add_f32 v56,v56,v200\n"
MF "v[132:135],a[76:79],a[240:243],v[132:135]\n"
TR "v[178:179],v2 offset:51200\n"
"v_add_f32 v57,v57,v201\n"
MF "v[132:135],a[36:39],v[188:191],v[132:135]\n"
"ds_read2st64_b32 v[192:193],v13 offset0:8 offset1:9\n"
"v_add_f32 v58,v58,v202\n"
MF "v[124:127],a[32:35],v[188:191],v[124:127]\n"
TR "v[180:181],v2 offset:53248\n"
"v_add_f32 v59,v59,v203\n"
MF "v[124:127],a[72:75],a[240:243],v[124:127]\n"
TR "v[182:183],v2 offset:55296\n"
"v_add_f32 v60,v60,v204\n"
MF "v[124:127],a[112:115],a[244:247],v[124:127]\n"
"ds_read2st64_b32 v[194:195],v13 offset0:10 offset1:11\n"
"v_add_f32 v61,v61,v205\n"
MF "v[116:119],a[108:111],a[244:247],v[116:119]\n"
TR "v[184:185],v2 offset:57344\n"
"v_add_f32 v62,v62,v206\n"
MF "v[116:119],a[68:71],a[240:243],v[116:119]\n"
TR "v[186:187],v2 offset:59392\n"
"v_add_f32 v63,v63,v207\n"
MF "v[116:119],a[28:31],v[188:191],v[116:119]\n"
"ds_read2st64_b32 v[196:197],v13 offset0:12 offset1:13\n"
CV "v56,v56,v57\n"
MF "v[108:111],a[24:27],v[188:191],v[108:111]\n"
"ds_read2st64_b32 v[198:199],v13 offset0:14 offset1:15\n"
CV "v57,v58,v59\n"
MF "v[108:111],a[64:67],a[240:243],v[108:111]\n"
"s_add_u32 s87,s90,10240\n"
CV "v58,v60,v61\n"
CV "v59,v62,v63\n"
MF "v[108:111],a[104:107],a[244:247],v[108:111]\n"
MF "v[100:103],a[100:103],a[244:247],v[100:103]\n"
"buffer_store_dwordx4 v[56:59],v9,s[40:43],s87 offen sc1\n"
MF "v[100:103],a[60:63],a[240:243],v[100:103]\n"
"s_waitcnt vmcnt(7)\n"
"s_cmp_lt_u32 s85,6\n"
MF "v[100:103],a[20:23],v[188:191],v[100:103]\n"
"s_cbranch_scc1 .Ljs10\n"
"v_mov_b32 v236,v228\n"
"v_mov_b32 v237,v229\n"
MF "v[136:139],a[252:255],v[208:211],v[136:139]\n"
MF "v[144:147],a[252:255],v[212:215],v[144:147]\n"
MF "v[152:155],a[252:255],v[216:219],v[152:155]\n"
MF "v[160:163],a[252:255],v[220:223],v[160:163]\n"
MF "v[168:171],a[252:255],v[224:227],v[168:171]\n"
MF "v[140:143],a[248:251],v[208:211],v[140:143]\n"
MF "v[148:151],a[248:251],v[212:215],v[148:151]\n"
MF "v[156:159],a[248:251],v[216:219],v[156:159]\n"
MF "v[164:167],a[248:251],v[220:223],v[164:167]\n"
MF "v[172:175],a[248:251],v[224:227],v[172:175]\n"
MF "v[248:251],v[230:233],a[250:253],v[248:251]\n"
MF "v[244:247],v[234:237],a[250:253],v[244:247]\n"
".Ljs10:\n"
"s_waitcnt lgkmcnt(2)\n"
MF "v[136:139],a[20:23],v[176:179],v[136:139]\n"
"s_waitcnt lgkmcnt(0)\n"
TR "v[188:189],v3 offset:49152\n"
"v_add_f32 v64,v64,v192\n"
MF "v[136:139],a[60:63],v[180:183],v[136:139]\n"
TR "v[190:191],v3 offset:51200\n"
"v_add_f32 v65,v65,v193\n"
MF "v[136:139],a[100:103],v[184:187],v[136:139]\n"
"ds_read2st64_b32 v[200:201],v13 offset0:16 offset1:17\n"
"v_add_f32 v66,v66,v194\n"
MF "v[144:147],a[104:107],v[184:187],v[144:147]\n"
TR "a[240:241],v3 offset:53248\n"
"v_add_f32 v67,v67,v195\n"
MF "v[144:147],a[64:67],v[180:183],v[144:147]\n"
TR "a[242:243],v3 offset:55296\n"
"v_add_f32 v68,v68,v196\n"
MF "v[144:147],a[24:27],v[176:179],v[144:147]\n"
"ds_read2st64_b32 v[202:203],v13 offset0:18 offset1:19\n"
"v_add_f32 v69,v69,v197\n"
MF "v[152:155],a[28:31],v[176:179],v[152:155]\n"
TR "a[244:245],v3 offset:57344\n"
"v_add_f32 v70,v70,v198\n"
MF "v[152:155],a[68:71],v[180:183],v[152:155]\n"
TR "a[246:247],v3 offset:59392\n"
"v_add_f32 v71,v71,v199\n"
MF "v[152:155],a[108:111],v[184:187],v[152:155]\n"
"ds_read2st64_b32 v[204:205],v13 offset0:20 offset1:21\n"
CV "v64,v64,v65\n"
MF "v[160:163],a[112:115],v[184:187],v[160:163]\n"
"ds_read2st64_b32 v[206:207],v13 offset0:22 offset1:23\n"
CV "v65,v66,v67\n"
MF "v[160:163],a[72:75],v[180:183],v[160:163]\n"
"s_add_u32 s87,s90,11264\n"
CV "v66,v68,v69\n"
CV "v67,v70,v71\n"
MF "v[160:163],a[32:35],v[176:179],v[160:163]\n"
"v_sub_u32 v10,v10,v11\n"
MF "v[168:171],a[36:39],v[176:179],v[168:171]\n"
"buffer_store_dwordx4 v[64:67],v9,s[40:43],s87 offen sc1\n"
MF "v[168:171],a[76:79],v[180:183],v[168:171]\n"
"s_waitcnt lgkmcnt(0)\n"
MF "v[168:171],a[116:119],v[184:187],v[168:171]\n"
"s_barrier\n"
MF "v[172:175],a[116:119],a[244:247],v[172:175]\n"
TR "v[176:177],v4\n"
"v_add_f32 v72,v72,v200\n"
MF "v[172:175],a[76:79],a[240:243],v[172:175]\n"
TR "v[178:179],v4 offset:2048\n"
"v_add_f32 v73,v73,v201\n"
MF "v[172:175],a[36:39],v[188:191],v[172:175]\n"
"ds_read2st64_b32 v[192:193],v13 offset0:24 offset1:25\n"
"v_add_f32 v74,v74,v202\n"
MF "v[164:167],a[32:35],v[188:191],v[164:167]\n"
TR "v[180:181],v4 offset:4096\n"
"v_add_f32 v75,v75,v203\n"
MF "v[164:167],a[72:75],a[240:243],v[164:167]\n"
TR "v[182:183],v4 offset:6144\n"
"v_add_f32 v76,v76,v204\n"
MF "v[164:167],a[112:115],a[244:247],v[164:167]\n"
"ds_read2st64_b32 v[194:195],v13 offset0:26 offset1:27\n"
"v_add_f32 v77,v77,v205\n"
MF "v[156:159],a[108:111],a[244:247],v[156:159]\n"
TR "v[184:185],v4 offset:8192\n"
"v_add_f32 v78,v78,v206\n"
MF "v[156:159],a[68:71],a[240:243],v[156:159]\n"
TR "v[186:187],v4 offset:10240\n"
"v_add_f32 v79,v79,v207\n"
MF "v[156:159],a[28:31],v[188:191],v[156:159]\n"
"ds_read2st64_b32 v[196:197],v13 offset0:28 offset1:29\n"
CV "v72,v72,v73\n"
MF "v[148:151],a[24:27],v[188:191],v[148:151]\n"
"ds_read2st64_b32 v[198:199],v13 offset0:30 offset1:31\n"
CV "v73,v74,v75\n"
MF "v[148:151],a[64:67],a[240:243],v[148:151]\n"
"s_add_u32 s87,s90,12288\n"
CV "v74,v76,v77\n"
CV "v75,v78,v79\n"
MF "v[148:151],a[104:107],a[244:247],v[148:151]\n"
".Lnf6:\n"
"v_readlane_b32 s58,v10,s67\n"
"s_cmp_lt_u32 s58,s85\n"
"s_cbranch_scc1 .Lsp6\n"
".Lnb6:\n"
MF "v[140:143],a[100:103],a[244:247],v[140:143]\n"
"buffer_store_dwordx4 v[72:75],v9,s[40:43],s87 offen sc1\n"
MF "v[140:143],a[60:63],a[240:243],v[140:143]\n"
"s_add_u32 s70,s70,20480\n"
".Lnf7:\n"
"v_cmp_gt_u32 vcc,4,v10\n"
"s_cbranch_vccnz .Lsp7\n"
".Lnb7:\n"
MF "v[140:143],a[20:23],v[188:191],v[140:143]\n"
"s_waitcnt lgkmcnt(2)\n"
MF "v[96:99],a[140:143],v[176:179],v[96:99]\n"
"s_waitcnt lgkmcnt(0)\n"
TR "v[188:189],v5\n"
"v_add_f32 v80,v80,v192\n"
MF "v[96:99],a[180:183],v[180:183],v[96:99]\n"
TR "v[190:191],v5 offset:2048\n"
"v_add_f32 v81,v81,v193\n"
MF "v[96:99],a[220:223],v[184:187],v[96:99]\n"
"ds_read2st64_b32 v[200:201],v13 offset0:32 offset1:33\n"
"v_add_f32 v82,v82,v194\n"
MF "v[104:107],a[224:227],v[184:187],v[104:107]\n"
TR "a[240:241],v5 offset:4096\n"
"v_add_f32 v83,v83,v195\n"
MF "v[104:107],a[184:187],v[180:183],v[104:107]\n"
TR "a[242:243],v5 offset:6144\n"
"v_add_f32 v84,v84,v196\n"
MF "v[104:107],a[144:147],v[176:179],v[104:107]\n"
"ds_read2st64_b32 v[202:203],v13 offset0:34 offset1:35\n"
"v_add_f32 v85,v85,v197\n"
MF "v[112:115],a[148:151],v[176:179],v[112:115]\n"
TR "a[244:245],v5 offset:8192\n"
"v_add_f32 v86,v86,v198\n"
MF "v[112:115],a[188:191],v[180:183],v[112:115]\n"
TR "a[246:247],v5 offset:10240\n"
"v_add_f32 v87,v87,v199\n"
MF "v[112:115],a[228:231],v[184:187],v[112:115]\n"
"ds_read2st64_b32 v[204:205],v13 offset0:36 offset1:37\n"
CV "v80,v80,v81\n"
MF "v[120:123],a[232:235],v[184:187],v[120:123]\n"
"s_add_u32 s2,s2,204800\n"
"ds_read2st64_b32 v[206:207],v13 offset0:38 offset1:39\n"
CV "v81,v82,v83\n"
MF "v[120:123],a[192:195],v[180:183],v[120:123]\n"
"s_add_u32 s87,s90,13312\n"
CV "v82,v84,v85\n"
CV "v83,v86,v87\n"
MF "v[120:123],a[152:155],v[176:179],v[120:123]\n"
LD "v[228:231],v9,s[40:43],s2 offen sc1\n"
MF "v[128:131],a[156:159],v[176:179],v[128:131]\n"
"buffer_store_dwordx4 v[80:83],v9,s[40:43],s87 offen sc1\n"
MF "v[128:131],a[196:199],v[180:183],v[128:131]\n"
"s_add_u32 s2,s2,204800\n"
MF "v[128:131],a[236:239],v[184:187],v[128:131]\n"
"s_waitcnt lgkmcnt(2)\n"
LD "v[232:235],v9,s[40:43],s2 offen sc1\n"
MF "v[132:135],a[236:239],a[244:247],v[132:135]\n"
"s_waitcnt lgkmcnt(0)\n"
TR "v[176:177],v6\n"
"v_add_f32 v88,v88,v200\n"
MF "v[132:135],a[196:199],a[240:243],v[132:135]\n"
TR "v[178:179],v6 offset:2048\n"
"v_add_f32 v89,v89,v201\n"
MF "v[132:135],a[156:159],v[188:191],v[132:135]\n"
"v_add_f32 v90,v90,v202\n"
"v_add_f32 v91,v91,v203\n"
MF "v[124:127],a[152:155],v[188:191],v[124:127]\n"
TR "v[180:181],v6 offset:4096\n"
"v_add_f32 v92,v92,v204\n"
MF "v[124:127],a[192:195],a[240:243],v[124:127]\n"
TR "v[182:183],v6 offset:6144\n"
"v_add_f32 v93,v93,v205\n"
MF "v[124:127],a[232:235],a[244:247],v[124:127]\n"
"v_add_f32 v94,v94,v206\n"
"v_add_f32 v95,v95,v207\n"
MF "v[116:119],a[228:231],a[244:247],v[116:119]\n"
TR "v[184:185],v6 offset:8192\n"
CV "v88,v88,v89\n"
MF "v[116:119],a[188:191],a[240:243],v[116:119]\n"
TR "v[186:187],v6 offset:10240\n"
CV "v89,v90,v91\n"
MF "v[116:119],a[148:151],v[188:191],v[116:119]\n"
"s_add_u32 s87,s90,14336\n"
CV "v90,v92,v93\n"
CV "v91,v94,v95\n"
MF "v[108:111],a[144:147],v[188:191],v[108:111]\n"
LD "v[208:211],v9,s[40:43],s82 offen sc1\n"
MF "v[108:111],a[184:187],a[240:243],v[108:111]\n"
MF "v[108:111],a[224:227],a[244:247],v[108:111]\n"
MF "v[100:103],a[220:223],a[244:247],v[100:103]\n"
"buffer_store_dwordx4 v[88:91],v9,s[40:43],s87 offen sc1\n"
MF "v[100:103],a[180:183],a[240:243],v[100:103]\n"
"s_mov_b32 s90,s91\n"
"s_add_u32 s91,s91,20480\n"
MF "v[100:103],a[140:143],v[188:191],v[100:103]\n"
"s_waitcnt lgkmcnt(0)\n"
MF "v[136:139],a[140:143],v[176:179],v[136:139]\n"
TR "v[188:189],v7\n"
MF "v[136:139],a[180:183],v[180:183],v[136:139]\n"
TR "v[190:191],v7 offset:2048\n"
MF "v[136:139],a[220:223],v[184:187],v[136:139]\n"
LD "v[212:215],v9,s[40:43],s82 offen offset:1024 sc1\n"
MF "v[144:147],a[224:227],v[184:187],v[144:147]\n"
TR "a[240:241],v7 offset:4096\n"
MF "v[144:147],a[184:187],v[180:183],v[144:147]\n"
TR "a[242:243],v7 offset:6144\n"
MF "v[144:147],a[144:147],v[176:179],v[144:147]\n"
MF "v[152:155],a[148:151],v[176:179],v[152:155]\n"
TR "a[244:245],v7 offset:8192\n"
MF "v[152:155],a[188:191],v[180:183],v[152:155]\n"
TR "a[246:247],v7 offset:10240\n"
MF "v[152:155],a[228:231],v[184:187],v[152:155]\n"
MF "v[160:163],a[232:235],v[184:187],v[160:163]\n"
MF "v[160:163],a[192:195],v[180:183],v[160:163]\n"
LD "v[216:219],v9,s[40:43],s82 offen offset:2048 sc1\n"
MF "v[160:163],a[152:155],v[176:179],v[160:163]\n"
MF "v[168:171],a[156:159],v[176:179],v[168:171]\n"
MF "v[168:171],a[196:199],v[180:183],v[168:171]\n"
"s_waitcnt lgkmcnt(0)\n"
MF "v[168:171],a[236:239],v[184:187],v[168:171]\n"
"s_barrier\n"
MF "v[172:175],a[236:239],a[244:247],v[172:175]\n"
TR "v[176:177],v0 offset:24576\n"
MF "v[172:175],a[196:199],a[240:243],v[172:175]\n"
"s_add_u32 s85,s85,1\n"
TR "v[178:179],v0 offset:26624\n"
MF "v[172:175],a[156:159],v[188:191],v[172:175]\n"
MF "v[164:167],a[152:155],v[188:191],v[164:167]\n"
TR "v[180:181],v0 offset:28672\n"
MF "v[164:167],a[192:195],a[240:243],v[164:167]\n"
TR "v[182:183],v0 offset:30720\n"
MF "v[164:167],a[232:235],a[244:247],v[164:167]\n"
LD "v[220:223],v9,s[40:43],s82 offen offset:3072 sc1\n"
MF "v[156:159],a[228:231],a[244:247],v[156:159]\n"
TR "v[184:185],v0 offset:32768\n"
MF "v[156:159],a[188:191],a[240:243],v[156:159]\n"
TR "v[186:187],v0 offset:34816\n"
MF "v[156:159],a[148:151],v[188:191],v[156:159]\n"
MF "v[148:151],a[144:147],v[188:191],v[148:151]\n"
MF "v[148:151],a[184:187],a[240:243],v[148:151]\n"
MF "v[148:151],a[224:227],a[244:247],v[148:151]\n"
MF "v[140:143],a[220:223],a[244:247],v[140:143]\n"
MF "v[140:143],a[180:183],a[240:243],v[140:143]\n"
MF "v[140:143],a[140:143],v[188:191],v[140:143]\n"
"s_waitcnt lgkmcnt(0)\n"
MF "v[16:19],a[0:3],v[176:179],v[252:255]\n"
TR "v[188:189],v1 offset:24576\n"
MF "v[16:19],a[40:43],v[180:183],v[16:19]\n"
"s_add_u32 s82,s82,4096\n"
TR "v[190:191],v1 offset:26624\n"
MF "v[16:19],a[80:83],v[184:187],v[16:19]\n"
LD "v[224:227],v9,s[40:43],s82 offen sc1\n"
MF "v[24:27],a[84:87],v[184:187],v[252:255]\n"
TR "a[240:241],v1 offset:28672\n"
MF "v[24:27],a[44:47],v[180:183],v[24:27]\n"
TR "a[242:243],v1 offset:30720\n"
MF "v[24:27],a[4:7],v[176:179],v[24:27]\n"
MF "v[32:35],a[8:11],v[176:179],v[252:255]\n"
TR "a[244:245],v1 offset:32768\n"
MF "v[32:35],a[48:51],v[180:183],v[32:35]\n"
TR "a[246:247],v1 offset:34816\n"
MF "v[32:35],a[88:91],v[184:187],v[32:35]\n"
MF "v[40:43],a[92:95],v[184:187],v[252:255]\n"
MF "v[40:43],a[52:55],v[180:183],v[40:43]\n"
MF "v[40:43],a[12:15],v[176:179],v[40:43]\n"
MF "v[48:51],a[16:19],v[176:179],v[252:255]\n"
MF "v[48:51],a[56:59],v[180:183],v[48:51]\n"
MF "v[48:51],a[96:99],v[184:187],v[48:51]\n"
"s_waitcnt lgkmcnt(0)\n"
MF "v[52:55],a[96:99],a[244:247],v[252:255]\n"
TR "v[176:177],v2 offset:24576\n"
MF "v[52:55],a[56:59],a[240:243],v[52:55]\n"
TR "v[178:179],v2 offset:26624\n"
MF "v[52:55],a[16:19],v[188:191],v[52:55]\n"
MF "v[44:47],a[12:15],v[188:191],v[252:255]\n"
TR "v[180:181],v2 offset:28672\n"
MF "v[44:47],a[52:55],a[240:243],v[44:47]\n"
TR "v[182:183],v2 offset:30720\n"
MF "v[44:47],a[92:95],a[244:247],v[44:47]\n"
MF "v[36:39],a[88:91],a[244:247],v[252:255]\n"
TR "v[184:185],v2 offset:32768\n"
MF "v[36:39],a[48:51],a[240:243],v[36:39]\n"
TR "v[186:187],v2 offset:34816\n"
MF "v[36:39],a[8:11],v[188:191],v[36:39]\n"
MF "v[28:31],a[4:7],v[188:191],v[252:255]\n"
MF "v[28:31],a[44:47],a[240:243],v[28:31]\n"
MF "v[28:31],a[84:87],a[244:247],v[28:31]\n"
MF "v[20:23],a[80:83],a[244:247],v[252:255]\n"
MF "v[20:23],a[40:43],a[240:243],v[20:23]\n"
MF "v[20:23],a[0:3],v[188:191],v[20:23]\n"
"s_waitcnt lgkmcnt(0)\n"
MF "v[56:59],a[0:3],v[176:179],v[252:255]\n"
TR "v[188:189],v3 offset:24576\n"
MF "v[56:59],a[40:43],v[180:183],v[56:59]\n"
TR "v[190:191],v3 offset:26624\n"
MF "v[56:59],a[80:83],v[184:187],v[56:59]\n"
MF "v[64:67],a[84:87],v[184:187],v[252:255]\n"
TR "a[240:241],v3 offset:28672\n"
MF "v[64:67],a[44:47],v[180:183],v[64:67]\n"
TR "a[242:243],v3 offset:30720\n"
MF "v[64:67],a[4:7],v[176:179],v[64:67]\n"
MF "v[72:75],a[8:11],v[176:179],v[252:255]\n"
TR "a[244:245],v3 offset:32768\n"
MF "v[72:75],a[48:51],v[180:183],v[72:75]\n"
TR "a[246:247],v3 offset:34816\n"
MF "v[72:75],a[88:91],v[184:187],v[72:75]\n"
MF "v[80:83],a[92:95],v[184:187],v[252:255]\n"
MF "v[80:83],a[52:55],v[180:183],v[80:83]\n"
MF "v[80:83],a[12:15],v[176:179],v[80:83]\n"
MF "v[88:91],a[16:19],v[176:179],v[252:255]\n"
MF "v[88:91],a[56:59],v[180:183],v[88:91]\n"
"s_waitcnt lgkmcnt(0)\n"
MF "v[88:91],a[96:99],v[184:187],v[88:91]\n"
"s_barrier\n"
MF "v[92:95],a[96:99],a[244:247],v[252:255]\n"
TR "v[176:177],v0\n"
MF "v[92:95],a[56:59],a[240:243],v[92:95]\n"
TR "v[178:179],v0 offset:2048\n"
MF "v[92:95],a[16:19],v[188:191],v[92:95]\n"
"ds_read2st64_b32 v[192:193],v13 offset0:192 offset1:193\n"
MF "v[84:87],a[12:15],v[188:191],v[252:255]\n"
TR "v[180:181],v0 offset:4096\n"
MF "v[84:87],a[52:55],a[240:243],v[84:87]\n"
TR "v[182:183],v0 offset:6144\n"
MF "v[84:87],a[92:95],a[244:247],v[84:87]\n"
"ds_read2st64_b32 v[194:195],v13 offset0:194 offset1:195\n"
MF "v[76:79],a[88:91],a[244:247],v[252:255]\n"
TR "v[184:185],v0 offset:8192\n"
MF "v[76:79],a[48:51],a[240:243],v[76:79]\n"
TR "v[186:187],v0 offset:10240\n"
MF "v[76:79],a[8:11],v[188:191],v[76:79]\n"
"ds_read2st64_b32 v[196:197],v13 offset0:196 offset1:197\n"
MF "v[68:71],a[4:7],v[188:191],v[252:255]\n"
"ds_read2st64_b32 v[198:199],v13 offset0:198 offset1:199\n"
MF "v[68:71],a[44:47],a[240:243],v[68:71]\n"
"buffer_load_dword v10,v12,s[40:43],s86 offen sc1\n"
MF "v[68:71],a[84:87],a[244:247],v[68:71]\n"
MF "v[60:63],a[80:83],a[244:247],v[252:255]\n"
MF "v[60:63],a[40:43],a[240:243],v[60:63]\n"
MF "v[60:63],a[0:3],v[188:191],v[60:63]\n"
"s_waitcnt lgkmcnt(2)\n"
MF "v[16:19],a[120:123],v[176:179],v[16:19]\n"
"s_waitcnt lgkmcnt(0)\n"
TR "v[188:189],v1\n"
"v_add_f32 v96,v96,v192\n"
MF "v[16:19],a[160:163],v[180:183],v[16:19]\n"
TR "v[190:191],v1 offset:2048\n"
"v_add_f32 v97,v97,v193\n"
MF "v[16:19],a[200:203],v[184:187],v[16:19]\n"
"ds_read2st64_b32 v[200:201],v13 offset0:200 offset1:201\n"
"v_add_f32 v98,v98,v194\n"
"s_waitcnt vmcnt(5)\n"
"v_mov_b32 v14,s76\n"
"s_add_u32 s76,s76,1\n"
MF "v[24:27],a[204:207],v[184:187],v[24:27]\n"
TR "a[240:241],v1 offset:4096\n"
"v_add_f32 v99,v99,v195\n"
"buffer_store_dword v14,v15,s[40:43],s94 offen sc1\n"
MF "v[24:27],a[164:167],v[180:183],v[24:27]\n"
TR "a[242:243],v1 offset:6144\n"
"v_add_f32 v100,v100,v196\n"
MF "v[24:27],a[124:127],v[176:179],v[24:27]\n"
"ds_read2st64_b32 v[202:203],v13 offset0:202 offset1:203\n"
"v_add_f32 v101,v101,v197\n"
MF "v[32:35],a[128:131],v[176:179],v[32:35]\n"
TR "a[244:245],v1 offset:8192\n"
"v_add_f32 v102,v102,v198\n"
MF "v[32:35],a[168:171],v[180:183],v[32:35]\n"
TR "a[246:247],v1 offset:10240\n"
"v_add_f32 v103,v103,v199\n"
MF "v[32:35],a[208:211],v[184:187],v[32:35]\n"
"ds_read2st64_b32 v[204:205],v13 offset0:204 offset1:205\n"
CV "v96,v96,v97\n"
MF "v[40:43],a[212:215],v[184:187],v[40:43]\n"
"ds_read2st64_b32 v[206:207],v13 offset0:206 offset1:207\n"
CV "v97,v98,v99\n"
MF "v[40:43],a[172:175],v[180:183],v[40:43]\n"
"s_add_u32 s87,s90,0\n"
CV "v98,v100,v101\n"
CV "v99,v102,v103\n"
MF "v[40:43],a[132:135],v[176:179],v[40:43]\n"
MF "v[48:51],a[136:139],v[176:179],v[48:51]\n"
"buffer_store_dwordx4 v[96:99],v9,s[40:43],s87 offen sc1\n"
MF "v[48:51],a[176:179],v[180:183],v[48:51]\n"
MF "v[48:51],a[216:219],v[184:187],v[48:51]\n"
"s_waitcnt lgkmcnt(2)\n"
MF "v[52:55],a[216:219],a[244:247],v[52:55]\n"
"s_waitcnt lgkmcnt(0)\n"
TR "v[176:177],v2\n"
"v_add_f32 v104,v104,v200\n"
MF "v[52:55],a[176:179],a[240:243],v[52:55]\n"
TR "v[178:179],v2 offset:2048\n"
"v_add_f32 v105,v105,v201\n"
MF "v[52:55],a[136:139],v[188:191],v[52:55]\n"
"ds_read2st64_b32 v[192:193],v13 offset0:208 offset1:209\n"
"v_add_f32 v106,v106,v202\n"
MF "v[44:47],a[132:135],v[188:191],v[44:47]\n"
TR "v[180:181],v2 offset:4096\n"
"v_add_f32 v107,v107,v203\n"
MF "v[44:47],a[172:175],a[240:243],v[44:47]\n"
TR "v[182:183],v2 offset:6144\n"
"v_add_f32 v108,v108,v204\n"
MF "v[44:47],a[212:215],a[244:247],v[44:47]\n"
"ds_read2st64_b32 v[194:195],v13 offset0:210 offset1:211\n"
"v_add_f32 v109,v109,v205\n"
MF "v[36:39],a[208:211],a[244:247],v[36:39]\n"
TR "v[184:185],v2 offset:8192\n"
"v_add_f32 v110,v110,v206\n"
MF "v[36:39],a[168:171],a[240:243],v[36:39]\n"
TR "v[186:187],v2 offset:10240\n"
"v_add_f32 v111,v111,v207\n"
MF "v[36:39],a[128:131],v[188:191],v[36:39]\n"
"ds_read2st64_b32 v[196:197],v13 offset0:212 offset1:213\n"
CV "v104,v104,v105\n"
MF "v[28:31],a[124:127],v[188:191],v[28:31]\n"
"ds_read2st64_b32 v[198:199],v13 offset0:214 offset1:215\n"
CV "v105,v106,v107\n"
MF "v[28:31],a[164:167],a[240:243],v[28:31]\n"
"s_add_u32 s87,s90,1024\n"
CV "v106,v108,v109\n"
CV "v107,v110,v111\n"
MF "v[28:31],a[204:207],a[244:247],v[28:31]\n"
MF "v[20:23],a[200:203],a[244:247],v[20:23]\n"
"buffer_store_dwordx4 v[104:107],v9,s[40:43],s87 offen sc1\n"
MF "v[20:23],a[160:163],a[240:243],v[20:23]\n"
MF "v[20:23],a[120:123],v[188:191],v[20:23]\n"
"s_waitcnt lgkmcnt(2)\n"
MF "v[56:59],a[120:123],v[176:179],v[56:59]\n"
"s_waitcnt lgkmcnt(0)\n"
TR "v[188:189],v3\n"
"v_add_f32 v112,v112,v192\n"
MF "v[56:59],a[160:163],v[180:183],v[56:59]\n"
".Lpg36h:\n"
"s_cbranch_execz .Lisr4\n"
TR "v[190:191],v3 offset:2048\n"
"v_add_f32 v113,v113,v193\n"
MF "v[56:59],a[200:203],v[184:187],v[56:59]\n"
"ds_read2st64_b32 v[200:201],v13 offset0:216 offset1:217\n"
"v_add_f32 v114,v114,v194\n"
MF "v[64:67],a[204:207],v[184:187],v[64:67]\n"
TR "a[240:241],v3 offset:4096\n"
"v_add_f32 v115,v115,v195\n"
MF "v[64:67],a[164:167],v[180:183],v[64:67]\n"
TR "a[242:243],v3 offset:6144\n"
"v_add_f32 v116,v116,v196\n"
MF "v[64:67],a[124:127],v[176:179],v[64:67]\n"
"ds_read2st64_b32 v[202:203],v13 offset0:218 offset1:219\n"
"v_add_f32 v117,v117,v197\n"
MF "v[72:75],a[128:131],v[176:179],v[72:75]\n"
TR "a[244:245],v3 offset:8192\n"
"v_add_f32 v118,v118,v198\n"
MF "v[72:75],a[168:171],v[180:183],v[72:75]\n"
TR "a[246:247],v3 offset:10240\n"
"v_add_f32 v119,v119,v199\n"
MF "v[72:75],a[208:211],v[184:187],v[72:75]\n"
"ds_read2st64_b32 v[204:205],v13 offset0:220 offset1:221\n"
CV "v112,v112,v113\n"
MF "v[80:83],a[212:215],v[184:187],v[80:83]\n"
"ds_read2st64_b32 v[206:207],v13 offset0:222 offset1:223\n"
CV "v113,v114,v115\n"
MF "v[80:83],a[172:175],v[180:183],v[80:83]\n"
"s_add_u32 s87,s90,2048\n"
CV "v114,v116,v117\n"
CV "v115,v118,v119\n"
MF "v[80:83],a[132:135],v[176:179],v[80:83]\n"
MF "v[88:91],a[136:139],v[176:179],v[88:91]\n"
"buffer_store_dwordx4 v[112:115],v9,s[40:43],s87 offen sc1\n"
MF "v[88:91],a[176:179],v[180:183],v[88:91]\n"
"s_waitcnt lgkmcnt(0)\n"
MF "v[88:91],a[216:219],v[184:187],v[88:91]\n"
"s_barrier\n"
MF "v[92:95],a[216:219],a[244:247],v[92:95]\n"
TR "v[176:177],v0 offset:49152\n"
"v_add_f32 v120,v120,v200\n"
MF "v[92:95],a[176:179],a[240:243],v[92:95]\n"
"s_mov_b32 s82,s70\n"
TR "v[178:179],v0 offset:51200\n"
"v_add_f32 v121,v121,v201\n"
MF "v[92:95],a[136:139],v[188:191],v[92:95]\n"
"ds_read2st64_b32 v[192:193],v13 offset0:224 offset1:225\n"
"v_add_f32 v122,v122,v202\n"
MF "v[84:87],a[132:135],v[188:191],v[84:87]\n"
TR "v[180:181],v0 offset:53248\n"
"v_add_f32 v123,v123,v203\n"
MF "v[84:87],a[172:175],a[240:243],v[84:87]\n"
TR "v[182:183],v0 offset:55296\n"
"v_add_f32 v124,v124,v204\n"
MF "v[84:87],a[212:215],a[244:247],v[84:87]\n"
"ds_read2st64_b32 v[194:195],v13 offset0:226 offset1:227\n"
"v_add_f32 v125,v125,v205\n"
MF "v[76:79],a[208:211],a[244:247],v[76:79]\n"
TR "v[184:185],v0 offset:57344\n"
"v_add_f32 v126,v126,v206\n"
MF "v[76:79],a[168:171],a[240:243],v[76:79]\n"
TR "v[186:187],v0 offset:59392\n"
"v_add_f32 v127,v127,v207\n"
MF "v[76:79],a[128:131],v[188:191],v[76:79]\n"
"ds_read2st64_b32 v[196:197],v13 offset0:228 offset1:229\n"
CV "v120,v120,v121\n"
MF "v[68:71],a[124:127],v[188:191],v[68:71]\n"
"ds_read2st64_b32 v[198:199],v13 offset0:230 offset1:231\n"
CV "v121,v122,v123\n"
MF "v[68:71],a[164:167],a[240:243],v[68:71]\n"
"s_add_u32 s87,s90,3072\n"
CV "v122,v124,v125\n"
CV "v123,v126,v127\n"
MF "v[68:71],a[204:207],a[244:247],v[68:71]\n"
MF "v[60:63],a[200:203],a[244:247],v[60:63]\n"
"buffer_store_dwordx4 v[120:123],v9,s[40:43],s87 offen sc1\n"
MF "v[60:63],a[160:163],a[240:243],v[60:63]\n"
MF "v[60:63],a[120:123],v[188:191],v[60:63]\n"
"s_waitcnt lgkmcnt(2)\n"
MF "v[16:19],a[20:23],v[176:179],v[16:19]\n"
"s_waitcnt lgkmcnt(0)\n"
TR "v[188:189],v1 offset:49152\n"
"v_add_f32 v128,v128,v192\n"
MF "v[16:19],a[60:63],v[180:183],v[16:19]\n"
TR "v[190:191],v1 offset:51200\n"
"v_add_f32 v129,v129,v193\n"
MF "v[16:19],a[100:103],v[184:187],v[16:19]\n"
"ds_read2st64_b32 v[200:201],v13 offset0:0 offset1:1\n"
"v_add_f32 v130,v130,v194\n"
MF "v[24:27],a[104:107],v[184:187],v[24:27]\n"
TR "a[240:241],v1 offset:53248\n"
"v_add_f32 v131,v131,v195\n"
MF "v[24:27],a[64:67],v[180:183],v[24:27]\n"
TR "a[242:243],v1 offset:55296\n"
"v_add_f32 v132,v132,v196\n"
MF "v[24:27],a[24:27],v[176:179],v[24:27]\n"
"ds_read2st64_b32 v[202:203],v13 offset0:2 offset1:3\n"
"v_add_f32 v133,v133,v197\n"
MF "v[32:35],a[28:31],v[176:179],v[32:35]\n"
TR "a[244:245],v1 offset:57344\n"
"v_add_f32 v134,v134,v198\n"
MF "v[32:35],a[68:71],v[180:183],v[32:35]\n"
TR "a[246:247],v1 offset:59392\n"
"v_add_f32 v135,v135,v199\n"
MF "v[32:35],a[108:111],v[184:187],v[32:35]\n"
"ds_read2st64_b32 v[204:205],v13 offset0:4 offset1:5\n"
CV "v128,v128,v129\n"
MF "v[40:43],a[112:115],v[184:187],v[40:43]\n"
"ds_read2st64_b32 v[206:207],v13 offset0:6 offset1:7\n"
CV "v129,v130,v131\n"
MF "v[40:43],a[72:75],v[180:183],v[40:43]\n"
"s_add_u32 s87,s90,4096\n"
CV "v130,v132,v133\n"
CV "v131,v134,v135\n"
MF "v[40:43],a[32:35],v[176:179],v[40:43]\n"
MF "v[48:51],a[36:39],v[176:179],v[48:51]\n"
"buffer_store_dwordx4 v[128:131],v9,s[40:43],s87 offen sc1\n"
MF "v[48:51],a[76:79],v[180:183],v[48:51]\n"
MF "v[48:51],a[116:119],v[184:187],v[48:51]\n"
"s_waitcnt lgkmcnt(2)\n"
MF "v[52:55],a[116:119],a[244:247],v[52:55]\n"
"s_waitcnt lgkmcnt(0)\n"
TR "v[176:177],v2 offset:49152\n"
"v_add_f32 v136,v136,v200\n"
MF "v[52:55],a[76:79],a[240:243],v[52:55]\n"
TR "v[178:179],v2 offset:51200\n"
"v_add_f32 v137,v137,v201\n"
MF "v[52:55],a[36:39],v[188:191],v[52:55]\n"
"ds_read2st64_b32 v[192:193],v13 offset0:8 offset1:9\n"
"v_add_f32 v138,v138,v202\n"
MF "v[44:47],a[32:35],v[188:191],v[44:47]\n"
TR "v[180:181],v2 offset:53248\n"
"v_add_f32 v139,v139,v203\n"
MF "v[44:47],a[72:75],a[240:243],v[44:47]\n"
TR "v[182:183],v2 offset:55296\n"
"v_add_f32 v140,v140,v204\n"
MF "v[44:47],a[112:115],a[244:247],v[44:47]\n"
"ds_read2st64_b32 v[194:195],v13 offset0:10 offset1:11\n"
"v_add_f32 v141,v141,v205\n"
MF "v[36:39],a[108:111],a[244:247],v[36:39]\n"
TR "v[184:185],v2 offset:57344\n"
"v_add_f32 v142,v142,v206\n"
MF "v[36:39],a[68:71],a[240:243],v[36:39]\n"
TR "v[186:187],v2 offset:59392\n"
"v_add_f32 v143,v143,v207\n"
MF "v[36:39],a[28:31],v[188:191],v[36:39]\n"
"ds_read2st64_b32 v[196:197],v13 offset0:12 offset1:13\n"
CV "v136,v136,v137\n"
MF "v[28:31],a[24:27],v[188:191],v[28:31]\n"
"ds_read2st64_b32 v[198:199],v13 offset0:14 offset1:15\n"
CV "v137,v138,v139\n"
MF "v[28:31],a[64:67],a[240:243],v[28:31]\n"
"s_add_u32 s87,s90,10240\n"
CV "v138,v140,v141\n"
CV "v139,v142,v143\n"
MF "v[28:31],a[104:107],a[244:247],v[28:31]\n"
MF "v[20:23],a[100:103],a[244:247],v[20:23]\n"
"buffer_store_dwordx4 v[136:139],v9,s[40:43],s87 offen sc1\n"
MF "v[20:23],a[60:63],a[240:243],v[20:23]\n"
"s_waitcnt vmcnt(7)\n"
MF "v[20:23],a[20:23],v[188:191],v[20:23]\n"
"v_mov_b32 v236,v228\n"
"v_mov_b32 v237,v229\n"
MF "v[56:59],a[252:255],v[208:211],v[56:59]\n"
MF "v[64:67],a[252:255],v[212:215],v[64:67]\n"
MF "v[72:75],a[252:255],v[216:219],v[72:75]\n"
MF "v[80:83],a[252:255],v[220:223],v[80:83]\n"
MF "v[88:91],a[252:255],v[224:227],v[88:91]\n"
MF "v[60:63],a[248:251],v[208:211],v[60:63]\n"
MF "v[68:71],a[248:251],v[212:215],v[68:71]\n"
MF "v[76:79],a[248:251],v[216:219],v[76:79]\n"
MF "v[84:87],a[248:251],v[220:223],v[84:87]\n"
MF "v[92:95],a[248:251],v[224:227],v[92:95]\n"
MF "v[248:251],v[230:233],a[250:253],v[248:251]\n"
MF "v[244:247],v[234:237],a[250:253],v[244:247]\n"
"s_waitcnt lgkmcnt(2)\n"
MF "v[56:59],a[20:23],v[176:179],v[56:59]\n"
"s_waitcnt lgkmcnt(0)\n"
TR "v[188:189],v3 offset:49152\n"
"v_add_f32 v144,v144,v192\n"
MF "v[56:59],a[60:63],v[180:183],v[56:59]\n"
TR "v[190:191],v3 offset:51200\n"
"v_add_f32 v145,v145,v193\n"
MF "v[56:59],a[100:103],v[184:187],v[56:59]\n"
"ds_read2st64_b32 v[200:201],v13 offset0:16 offset1:17\n"
"v_add_f32 v146,v146,v194\n"
MF "v[64:67],a[104:107],v[184:187],v[64:67]\n"
TR "a[240:241],v3 offset:53248\n"
"v_add_f32 v147,v147,v195\n"
MF "v[64:67],a[64:67],v[180:183],v[64:67]\n"
TR "a[242:243],v3 offset:55296\n"
"v_add_f32 v148,v148,v196\n"
MF "v[64:67],a[24:27],v[176:179],v[64:67]\n"
"ds_read2st64_b32 v[202:203],v13 offset0:18 offset1:19\n"
"v_add_f32 v149,v149,v197\n"
MF "v[72:75],a[28:31],v[176:179],v[72:75]\n"
TR "a[244:245],v3 offset:57344\n"
"v_add_f32 v150,v150,v198\n"
MF "v[72:75],a[68:71],v[180:183],v[72:75]\n"
TR "a[246:247],v3 offset:59392\n"
"v_add_f32 v151,v151,v199\n"
MF "v[72:75],a[108:111],v[184:187],v[72:75]\n"
"ds_read2st64_b32 v[204:205],v13 offset0:20 offset1:21\n"
CV "v144,v144,v145\n"
MF "v[80:83],a[112:115],v[184:187],v[80:83]\n"
"ds_read2st64_b32 v[206:207],v13 offset0:22 offset1:23\n"
CV "v145,v146,v147\n"
MF "v[80:83],a[72:75],v[180:183],v[80:83]\n"
"s_add_u32 s87,s90,11264\n"
CV "v146,v148,v149\n"
CV "v147,v150,v151\n"
MF "v[80:83],a[32:35],v[176:179],v[80:83]\n"
"v_sub_u32 v10,v10,v11\n"
MF "v[88:91],a[36:39],v[176:179],v[88:91]\n"
"buffer_store_dwordx4 v[144:147],v9,s[40:43],s87 offen sc1\n"
MF "v[88:91],a[76:79],v[180:183],v[88:91]\n"
"s_waitcnt lgkmcnt(0)\n"
MF "v[88:91],a[116:119],v[184:187],v[88:91]\n"
"s_barrier\n"
MF "v[92:95],a[116:119],a[244:247],v[92:95]\n"
TR "v[176:177],v4\n"
"v_add_f32 v152,v152,v200\n"
MF "v[92:95],a[76:79],a[240:243],v[92:95]\n"
TR "v[178:179],v4 offset:2048\n"
"v_add_f32 v153,v153,v201\n"
MF "v[92:95],a[36:39],v[188:191],v[92:95]\n"
"ds_read2st64_b32 v[192:193],v13 offset0:24 offset1:25\n"
"v_add_f32 v154,v154,v202\n"
MF "v[84:87],a[32:35],v[188:191],v[84:87]\n"
TR "v[180:181],v4 offset:4096\n"
"v_add_f32 v155,v155,v203\n"
MF "v[84:87],a[72:75],a[240:243],v[84:87]\n"
TR "v[182:183],v4 offset:6144\n"
"v_add_f32 v156,v156,v204\n"
MF "v[84:87],a[112:115],a[244:247],v[84:87]\n"
"ds_read2st64_b32 v[194:195],v13 offset0:26 offset1:27\n"
"v_add_f32 v157,v157,v205\n"
MF "v[76:79],a[108:111],a[244:247],v[76:79]\n"
TR "v[184:185],v4 offset:8192\n"
"v_add_f32 v158,v158,v206\n"
MF "v[76:79],a[68:71],a[240:243],v[76:79]\n"
TR "v[186:187],v4 offset:10240\n"
"v_add_f32 v159,v159,v207\n"
MF "v[76:79],a[28:31],v[188:191],v[76:79]\n"
"ds_read2st64_b32 v[196:197],v13 offset0:28 offset1:29\n"
CV "v152,v152,v153\n"
MF "v[68:71],a[24:27],v[188:191],v[68:71]\n"
"ds_read2st64_b32 v[198:199],v13 offset0:30 offset1:31\n"
CV "v153,v154,v155\n"
MF "v[68:71],a[64:67],a[240:243],v[68:71]\n"
"s_add_u32 s87,s90,12288\n"
CV "v154,v156,v157\n"
CV "v155,v158,v159\n"
MF "v[68:71],a[104:107],a[244:247],v[68:71]\n"
".Lnf8:\n"
"v_readlane_b32 s58,v10,s67\n"
"s_cmp_lt_u32 s58,s85\n"
"s_cbranch_scc1 .Lsp8\n"
".Lnb8:\n"
MF "v[60:63],a[100:103],a[244:247],v[60:63]\n"
"buffer_store_dwordx4 v[152:155],v9,s[40:43],s87 offen sc1\n"
MF "v[60:63],a[60:63],a[240:243],v[60:63]\n"
"s_add_u32 s70,s70,20480\n"
"s_add_u32 s2,s2,204800\n"
MF "v[60:63],a[20:23],v[188:191],v[60:63]\n"
"s_waitcnt lgkmcnt(2)\n"
LD "v[228:231],v9,s[40:43],s2 offen sc1\n"
MF "v[16:19],a[140:143],v[176:179],v[16:19]\n"
"s_waitcnt lgkmcnt(0)\n"
TR "v[188:189],v5\n"
"v_add_f32 v160,v160,v192\n"
MF "v[16:19],a[180:183],v[180:183],v[16:19]\n"
TR "v[190:191],v5 offset:2048\n"
"v_add_f32 v161,v161,v193\n"
MF "v[16:19],a[220:223],v[184:187],v[16:19]\n"
"ds_read2st64_b32 v[200:201],v13 offset0:32 offset1:33\n"
"v_add_f32 v162,v162,v194\n"
MF "v[24:27],a[224:227],v[184:187],v[24:27]\n"
TR "a[240:241],v5 offset:4096\n"
"v_add_f32 v163,v163,v195\n"
MF "v[24:27],a[184:187],v[180:183],v[24:27]\n"
TR "a[242:243],v5 offset:6144\n"
"v_add_f32 v164,v164,v196\n"
MF "v[24:27],a[144:147],v[176:179],v[24:27]\n"
"ds_read2st64_b32 v[202:203],v13 offset0:34 offset1:35\n"
"v_add_f32 v165,v165,v197\n"
MF "v[32:35],a[148:151],v[176:179],v[32:35]\n"
TR "a[244:245],v5 offset:8192\n"
"v_add_f32 v166,v166,v198\n"
MF "v[32:35],a[188:191],v[180:183],v[32:35]\n"
TR "a[246:247],v5 offset:10240\n"
"v_add_f32 v167,v167,v199\n"
MF "v[32:35],a[228:231],v[184:187],v[32:35]\n"
"ds_read2st64_b32 v[204:205],v13 offset0:36 offset1:37\n"
CV "v160,v160,v161\n"
MF "v[40:43],a[232:235],v[184:187],v[40:43]\n"
"s_add_u32 s2,s2,204800\n"
"ds_read2st64_b32 v[206:207],v13 offset0:38 offset1:39\n"
CV "v161,v162,v163\n"
MF "v[40:43],a[192:195],v[180:183],v[40:43]\n"
"s_add_u32 s87,s90,13312\n"
CV "v162,v164,v165\n"
CV "v163,v166,v167\n"
MF "v[40:43],a[152:155],v[176:179],v[40:43]\n"
LD "v[232:235],v9,s[40:43],s2 offen sc1\n"
MF "v[48:51],a[156:159],v[176:179],v[48:51]\n"
"buffer_store_dwordx4 v[160:163],v9,s[40:43],s87 offen sc1\n"
MF "v[48:51],a[196:199],v[180:183],v[48:51]\n"
MF "v[48:51],a[236:239],v[184:187],v[48:51]\n"
"s_waitcnt lgkmcnt(2)\n"
MF "v[52:55],a[236:239],a[244:247],v[52:55]\n"
"s_waitcnt lgkmcnt(0)\n"
TR "v[176:177],v6\n"
"v_add_f32 v168,v168,v200\n"
MF "v[52:55],a[196:199],a[240:243],v[52:55]\n"
TR "v[178:179],v6 offset:2048\n"
"v_add_f32 v169,v169,v201\n"
MF "v[52:55],a[156:159],v[188:191],v[52:55]\n"
"v_add_f32 v170,v170,v202\n"
"v_add_f32 v171,v171,v203\n"
MF "v[44:47],a[152:155],v[188:191],v[44:47]\n"
TR "v[180:181],v6 offset:4096\n"
"v_add_f32 v172,v172,v204\n"
MF "v[44:47],a[192:195],a[240:243],v[44:47]\n"
TR "v[182:183],v6 offset:6144\n"
"v_add_f32 v173,v173,v205\n"
MF "v[44:47],a[232:235],a[244:247],v[44:47]\n"
"v_add_f32 v174,v174,v206\n"
"v_add_f32 v175,v175,v207\n"
MF "v[36:39],a[228:231],a[244:247],v[36:39]\n"
TR "v[184:185],v6 offset:8192\n"
CV "v168,v168,v169\n"
MF "v[36:39],a[188:191],a[240:243],v[36:39]\n"
TR "v[186:187],v6 offset:10240\n"
CV "v169,v170,v171\n"
MF "v[36:39],a[148:151],v[188:191],v[36:39]\n"
"s_add_u32 s87,s90,14336\n"
CV "v170,v172,v173\n"
CV "v171,v174,v175\n"
MF "v[28:31],a[144:147],v[188:191],v[28:31]\n"
LD "v[208:211],v9,s[40:43],s82 offen sc1\n"
MF "v[28:31],a[184:187],a[240:243],v[28:31]\n"
MF "v[28:31],a[224:227],a[244:247],v[28:31]\n"
MF "v[20:23],a[220:223],a[244:247],v[20:23]\n"
"buffer_store_dwordx4 v[168:171],v9,s[40:43],s87 offen sc1\n"
MF "v[20:23],a[180:183],a[240:243],v[20:23]\n"
"s_mov_b32 s90,s91\n"
"s_add_u32 s91,s91,20480\n"
MF "v[20:23],a[140:143],v[188:191],v[20:23]\n"
"s_waitcnt lgkmcnt(0)\n"
MF "v[56:59],a[140:143],v[176:179],v[56:59]\n"
TR "v[188:189],v7\n"
MF "v[56:59],a[180:183],v[180:183],v[56:59]\n"
TR "v[190:191],v7 offset:2048\n"
MF "v[56:59],a[220:223],v[184:187],v[56:59]\n"
LD "v[212:215],v9,s[40:43],s82 offen offset:1024 sc1\n"
MF "v[64:67],a[224:227],v[184:187],v[64:67]\n"
TR "a[240:241],v7 offset:4096\n"
MF "v[64:67],a[184:187],v[180:183],v[64:67]\n"
TR "a[242:243],v7 offset:6144\n"
MF "v[64:67],a[144:147],v[176:179],v[64:67]\n"
MF "v[72:75],a[148:151],v[176:179],v[72:75]\n"
TR "a[244:245],v7 offset:8192\n"
MF "v[72:75],a[188:191],v[180:183],v[72:75]\n"
TR "a[246:247],v7 offset:10240\n"
MF "v[72:75],a[228:231],v[184:187],v[72:75]\n"
MF "v[80:83],a[232:235],v[184:187],v[80:83]\n"
MF "v[80:83],a[192:195],v[180:183],v[80:83]\n"
LD "v[216:219],v9,s[40:43],s82 offen offset:2048 sc1\n"
MF "v[80:83],a[152:155],v[176:179],v[80:83]\n"
MF "v[88:91],a[156:159],v[176:179],v[88:91]\n"
MF "v[88:91],a[196:199],v[180:183],v[88:91]\n"
"s_waitcnt lgkmcnt(0)\n"
MF "v[88:91],a[236:239],v[184:187],v[88:91]\n"
"s_barrier\n"
MF "v[92:95],a[236:239],a[244:247],v[92:95]\n"
TR "v[176:177],v0 offset:24576\n"
MF "v[92:95],a[196:199],a[240:243],v[92:95]\n"
"s_add_u32 s85,s85,1\n"
TR "v[178:179],v0 offset:26624\n"
MF "v[92:95],a[156:159],v[188:191],v[92:95]\n"
MF "v[84:87],a[152:155],v[188:191],v[84:87]\n"
TR "v[180:181],v0 offset:28672\n"
MF "v[84:87],a[192:195],a[240:243],v[84:87]\n"
TR "v[182:183],v0 offset:30720\n"
MF "v[84:87],a[232:235],a[244:247],v[84:87]\n"
LD "v[220:223],v9,s[40:43],s82 offen offset:3072 sc1\n"
MF "v[76:79],a[228:231],a[244:247],v[76:79]\n"
TR "v[184:185],v0 offset:32768\n"
MF "v[76:79],a[188:191],a[240:243],v[76:79]\n"
TR "v[186:187],v0 offset:34816\n"
MF "v[76:79],a[148:151],v[188:191],v[76:79]\n"
MF "v[68:71],a[144:147],v[188:191],v[68:71]\n"
MF "v[68:71],a[184:187],a[240:243],v[68:71]\n"
MF "v[68:71],a[224:227],a[244:247],v[68:71]\n"
MF "v[60:63],a[220:223],a[244:247],v[60:63]\n"
MF "v[60:63],a[180:183],a[240:243],v[60:63]\n"
MF "v[60:63],a[140:143],v[188:191],v[60:63]\n"
"s_waitcnt lgkmcnt(0)\n"
"s_cmp_lg_u32 s85,9\n"
"s_cbranch_scc1 .Lpl19\n"
".Lplast1:\n"
MF "v[96:99],v[176:179],a[0:3],v[252:255]\n"
"s_add_u32 m0,s79,73728\n"
TR "v[188:189],v1 offset:24576\n"
TR "v[190:191],v1 offset:26624\n"
MF "v[96:99],v[180:183],a[40:43],v[96:99]\n"
"s_nop 0\n"
TR "a[240:241],v1 offset:28672\n"
TR "a[242:243],v1 offset:30720\n"
MF "v[96:99],v[184:187],a[80:83],v[96:99]\n"
"s_add_u32 s82,s82,4096\n"
TR "a[244:245],v1 offset:32768\n"
TR "a[246:247],v1 offset:34816\n"
MF "v[104:107],v[184:187],a[84:87],v[252:255]\n"
LD "v[224:227],v9,s[40:43],s82 offen sc1\n"
MF "v[104:107],v[180:183],a[44:47],v[104:107]\n"
"s_nop 0\n"
"ds_write_addtid_b32 v56\n"
"ds_write_addtid_b32 v57 offset:256\n"
MF "v[104:107],v[176:179],a[4:7],v[104:107]\n"
"s_nop 0\n"
"ds_write_addtid_b32 v58 offset:512\n"
"ds_write_addtid_b32 v59 offset:768\n"
MF "v[112:115],v[176:179],a[8:11],v[252:255]\n"
"s_nop 0\n"
"ds_write_addtid_b32 v60 offset:1024\n"
"ds_write_addtid_b32 v61 offset:1280\n"
MF "v[112:115],v[180:183],a[48:51],v[112:115]\n"
"s_nop 0\n"
"ds_write_addtid_b32 v62 offset:1536\n"
"ds_write_addtid_b32 v63 offset:1792\n"
MF "v[112:115],v[184:187],a[88:91],v[112:115]\n"
"s_nop 0\n"
"ds_write_addtid_b32 v64 offset:2048\n"
"ds_write_addtid_b32 v65 offset:2304\n"
MF "v[120:123],v[184:187],a[92:95],v[252:255]\n"
"s_nop 0\n"
"ds_write_addtid_b32 v66 offset:2560\n"
"ds_write_addtid_b32 v67 offset:2816\n"
MF "v[120:123],v[180:183],a[52:55],v[120:123]\n"
"s_nop 0\n"
"ds_write_addtid_b32 v68 offset:3072\n"
"ds_write_addtid_b32 v69 offset:3328\n"
MF "v[120:123],v[176:179],a[12:15],v[120:123]\n"
"s_nop 0\n"
"ds_write_addtid_b32 v70 offset:3584\n"
"ds_write_addtid_b32 v71 offset:3840\n"
MF "v[128:131],v[176:179],a[16:19],v[252:255]\n"
"s_nop 0\n"
"ds_write_addtid_b32 v72 offset:4096\n"
"ds_write_addtid_b32 v73 offset:4352\n"
MF "v[128:131],v[180:183],a[56:59],v[128:131]\n"
"s_nop 0\n"
"ds_write_addtid_b32 v74 offset:4608\n"
"ds_write_addtid_b32 v75 offset:4864\n"
MF "v[128:131],v[184:187],a[96:99],v[128:131]\n"
"s_waitcnt lgkmcnt(15)\n"
"ds_write_addtid_b32 v76 offset:5120\n"
MF "v[132:135],a[244:247],a[96:99],v[252:255]\n"
"s_nop 0\n"
TR "v[176:177],v2 offset:24576\n"
TR "v[178:179],v2 offset:26624\n"
MF "v[132:135],a[240:243],a[56:59],v[132:135]\n"
"s_nop 0\n"
TR "v[180:181],v2 offset:28672\n"
TR "v[182:183],v2 offset:30720\n"
MF "v[132:135],v[188:191],a[16:19],v[132:135]\n"
"s_nop 0\n"
TR "v[184:185],v2 offset:32768\n"
TR "v[186:187],v2 offset:34816\n"
MF "v[124:127],v[188:191],a[12:15],v[252:255]\n"
"s_nop 0\n"
"ds_write_addtid_b32 v77 offset:5376\n"
"ds_write_addtid_b32 v78 offset:5632\n"
MF "v[124:127],a[240:243],a[52:55],v[124:127]\n"
"s_nop 0\n"
"ds_write_addtid_b32 v79 offset:5888\n"
"ds_write_addtid_b32 v80 offset:6144\n"
MF "v[124:127],a[244:247],a[92:95],v[124:127]\n"
"s_nop 0\n"
"ds_write_addtid_b32 v81 offset:6400\n"
"ds_write_addtid_b32 v82 offset:6656\n"
MF "v[116:119],a[244:247],a[88:91],v[252:255]\n"
"s_nop 0\n"
"ds_write_addtid_b32 v83 offset:6912\n"
"ds_write_addtid_b32 v84 offset:7168\n"
MF "v[116:119],a[240:243],a[48:51],v[116:119]\n"
"s_nop 0\n"
"ds_write_addtid_b32 v85 offset:7424\n"
"ds_write_addtid_b32 v86 offset:7680\n"
MF "v[116:119],v[188:191],a[8:11],v[116:119]\n"
"s_nop 0\n"
"ds_write_addtid_b32 v87 offset:7936\n"
"ds_write_addtid_b32 v88 offset:8192\n"
MF "v[108:111],v[188:191],a[4:7],v[252:255]\n"
"s_nop 0\n"
"ds_write_addtid_b32 v89 offset:8448\n"
"ds_write_addtid_b32 v90 offset:8704\n"
MF "v[108:111],a[240:243],a[44:47],v[108:111]\n"
"s_nop 0\n"
"ds_write_addtid_b32 v91 offset:8960\n"
"ds_write_addtid_b32 v92 offset:9216\n"
MF "v[108:111],a[244:247],a[84:87],v[108:111]\n"
"s_nop 0\n"
"ds_write_addtid_b32 v93 offset:9472\n"
"ds_write_addtid_b32 v94 offset:9728\n"
MF "v[100:103],a[244:247],a[80:83],v[252:255]\n"
"ds_write_addtid_b32 v95 offset:9984\n"
MF "v[100:103],a[240:243],a[40:43],v[100:103]\n"
MF "v[100:103],v[188:191],a[0:3],v[100:103]\n"
"s_waitcnt lgkmcnt(15)\n"
MF "v[136:139],v[176:179],a[0:3],v[252:255]\n"
"s_nop 0\n"
TR "v[188:189],v3 offset:24576\n"
TR "v[190:191],v3 offset:26624\n"
MF "v[136:139],v[180:183],a[40:43],v[136:139]\n"
"s_nop 0\n"
TR "a[240:241],v3 offset:28672\n"
TR "a[242:243],v3 offset:30720\n"
MF "v[136:139],v[184:187],a[80:83],v[136:139]\n"
"s_nop 0\n"
TR "a[244:245],v3 offset:32768\n"
TR "a[246:247],v3 offset:34816\n"
"s_waitcnt lgkmcnt(0)\n"
MF "v[144:147],v[184:187],a[84:87],v[252:255]\n"
"s_barrier\n"
MF "v[144:147],v[180:183],a[44:47],v[144:147]\n"
"ds_read2st64_b32 v[56:57],v13 offset0:192 offset1:193\n"
MF "v[144:147],v[176:179],a[4:7],v[144:147]\n"
"ds_read2st64_b32 v[58:59],v13 offset0:194 offset1:195\n"
MF "v[152:155],v[176:179],a[8:11],v[252:255]\n"
"ds_read2st64_b32 v[60:61],v13 offset0:196 offset1:197\n"
MF "v[152:155],v[180:183],a[48:51],v[152:155]\n"
"ds_read2st64_b32 v[62:63],v13 offset0:198 offset1:199\n"
MF "v[152:155],v[184:187],a[88:91],v[152:155]\n"
"ds_read2st64_b32 v[64:65],v13 offset0:200 offset1:201\n"
MF "v[160:163],v[184:187],a[92:95],v[252:255]\n"
"ds_read2st64_b32 v[66:67],v13 offset0:202 offset1:203\n"
MF "v[160:163],v[180:183],a[52:55],v[160:163]\n"
"ds_read2st64_b32 v[68:69],v13 offset0:204 offset1:205\n"
MF "v[160:163],v[176:179],a[12:15],v[160:163]\n"
"ds_read2st64_b32 v[70:71],v13 offset0:206 offset1:207\n"
MF "v[168:171],v[176:179],a[16:19],v[252:255]\n"
"s_waitcnt lgkmcnt(4)\n"
"ds_read2st64_b32 v[72:73],v13 offset0:208 offset1:209\n"
"v_add_f32 v16,v16,v56\n"
MF "v[168:171],v[180:183],a[56:59],v[168:171]\n"
"ds_read2st64_b32 v[74:75],v13 offset0:210 offset1:211\n"
"s_waitcnt lgkmcnt(0)\n"
MF "v[168:171],v[184:187],a[96:99],v[168:171]\n"
"s_barrier\n"
MF "v[172:175],a[244:247],a[96:99],v[252:255]\n"
"s_nop 0\n"
TR "v[176:177],v0\n"
TR "v[178:179],v0 offset:2048\n"
MF "v[172:175],a[240:243],a[56:59],v[172:175]\n"
"s_nop 0\n"
TR "v[180:181],v0 offset:4096\n"
TR "v[182:183],v0 offset:6144\n"
MF "v[172:175],v[188:191],a[16:19],v[172:175]\n"
"s_nop 0\n"
TR "v[184:185],v0 offset:8192\n"
TR "v[186:187],v0 offset:10240\n"
MF "v[164:167],v[188:191],a[12:15],v[252:255]\n"
"ds_read2st64_b32 v[76:77],v13 offset0:212 offset1:213\n"
"v_add_f32 v17,v17,v57\n"
MF "v[164:167],a[240:243],a[52:55],v[164:167]\n"
"ds_read2st64_b32 v[78:79],v13 offset0:214 offset1:215\n"
"v_add_f32 v18,v18,v58\n"
MF "v[164:167],a[244:247],a[92:95],v[164:167]\n"
"ds_read2st64_b32 v[80:81],v13 offset0:216 offset1:217\n"
"v_add_f32 v19,v19,v59\n"
MF "v[156:159],a[244:247],a[88:91],v[252:255]\n"
"ds_read2st64_b32 v[82:83],v13 offset0:218 offset1:219\n"
"v_add_f32 v20,v20,v60\n"
MF "v[156:159],a[240:243],a[48:51],v[156:159]\n"
"ds_read2st64_b32 v[84:85],v13 offset0:220 offset1:221\n"
"v_add_f32 v21,v21,v61\n"
MF "v[156:159],v[188:191],a[8:11],v[156:159]\n"
"ds_read2st64_b32 v[86:87],v13 offset0:222 offset1:223\n"
"v_add_f32 v22,v22,v62\n"
MF "v[148:151],v[188:191],a[4:7],v[252:255]\n"
".Lpg37h:\n"
"s_cbranch_execz .Lisr4\n"
"s_waitcnt vmcnt(4)\n"
"v_mov_b32 v14,s76\n"
"s_add_u32 s76,s76,1\n"
MF "v[148:151],a[240:243],a[44:47],v[148:151]\n"
"buffer_store_dword v14,v15,s[40:43],s94 offen sc1\n"
MF "v[148:151],a[244:247],a[84:87],v[148:151]\n"
"ds_read2st64_b32 v[88:89],v13 offset0:224 offset1:225\n"
"v_add_f32 v23,v23,v63\n"
MF "v[140:143],a[244:247],a[80:83],v[252:255]\n"
"ds_read2st64_b32 v[90:91],v13 offset0:226 offset1:227\n"
CV "v16,v16,v17\n"
MF "v[140:143],a[240:243],a[40:43],v[140:143]\n"
"ds_read2st64_b32 v[92:93],v13 offset0:228 offset1:229\n"
CV "v17,v18,v19\n"
MF "v[140:143],v[188:191],a[0:3],v[140:143]\n"
"s_waitcnt lgkmcnt(9)\n"
"ds_read2st64_b32 v[94:95],v13 offset0:230 offset1:231\n"
CV "v18,v20,v21\n"
MF "v[96:99],v[176:179],a[120:123],v[96:99]\n"
"s_nop 0\n"
TR "v[188:189],v1\n"
TR "v[190:191],v1 offset:2048\n"
MF "v[96:99],v[180:183],a[160:163],v[96:99]\n"
"s_nop 0\n"
TR "a[240:241],v1 offset:4096\n"
TR "a[242:243],v1 offset:6144\n"
MF "v[96:99],v[184:187],a[200:203],v[96:99]\n"
"s_nop 0\n"
TR "a[244:245],v1 offset:8192\n"
TR "a[246:247],v1 offset:10240\n"
MF "v[104:107],v[184:187],a[204:207],v[104:107]\n"
"s_add_u32 s87,s90,0\n"
CV "v19,v22,v23\n"
MF "v[104:107],v[180:183],a[164:167],v[104:107]\n"
"buffer_store_dwordx4 v[16:19],v9,s[40:43],s87 offen sc1\n"
MF "v[104:107],v[176:179],a[124:127],v[104:107]\n"
"v_add_f32 v24,v24,v64\n"
"v_add_f32 v25,v25,v65\n"
MF "v[112:115],v[176:179],a[128:131],v[112:115]\n"
"v_add_f32 v26,v26,v66\n"
"v_add_f32 v27,v27,v67\n"
MF "v[112:115],v[180:183],a[168:171],v[112:115]\n"
"v_add_f32 v28,v28,v68\n"
"v_add_f32 v29,v29,v69\n"
MF "v[112:115],v[184:187],a[208:211],v[112:115]\n"
"v_add_f32 v30,v30,v70\n"
"v_add_f32 v31,v31,v71\n"
MF "v[120:123],v[184:187],a[212:215],v[120:123]\n"
CV "v24,v24,v25\n"
CV "v25,v26,v27\n"
MF "v[120:123],v[180:183],a[172:175],v[120:123]\n"
"s_add_u32 s87,s90,1024\n"
CV "v26,v28,v29\n"
CV "v27,v30,v31\n"
MF "v[120:123],v[176:179],a[132:135],v[120:123]\n"
"s_waitcnt lgkmcnt(14)\n"
"buffer_store_dwordx4 v[24:27],v9,s[40:43],s87 offen sc1\n"
MF "v[128:131],v[176:179],a[136:139],v[128:131]\n"
"v_add_f32 v32,v32,v72\n"
"v_add_f32 v33,v33,v73\n"
MF "v[128:131],v[180:183],a[176:179],v[128:131]\n"
"v_add_f32 v34,v34,v74\n"
"v_add_f32 v35,v35,v75\n"
MF "v[128:131],v[184:187],a[216:219],v[128:131]\n"
"s_waitcnt lgkmcnt(0)\n"
"v_add_f32 v36,v36,v76\n"
"v_add_f32 v37,v37,v77\n"
MF "v[132:135],a[244:247],a[216:219],v[132:135]\n"
"s_nop 0\n"
TR "v[176:177],v2\n"
TR "v[178:179],v2 offset:2048\n"
MF "v[132:135],a[240:243],a[176:179],v[132:135]\n"
"s_nop 0\n"
TR "v[180:181],v2 offset:4096\n"
TR "v[182:183],v2 offset:6144\n"
MF "v[132:135],v[188:191],a[136:139],v[132:135]\n"
"s_nop 0\n"
TR "v[184:185],v2 offset:8192\n"
TR "v[186:187],v2 offset:10240\n"
MF "v[124:127],v[188:191],a[132:135],v[124:127]\n"
"v_add_f32 v38,v38,v78\n"
"v_add_f32 v39,v39,v79\n"
MF "v[124:127],a[240:243],a[172:175],v[124:127]\n"
CV "v32,v32,v33\n"
CV "v33,v34,v35\n"
MF "v[124:127],a[244:247],a[212:215],v[124:127]\n"
"s_add_u32 s87,s90,2048\n"
CV "v34,v36,v37\n"
CV "v35,v38,v39\n"
MF "v[116:119],a[244:247],a[208:211],v[116:119]\n"
"buffer_store_dwordx4 v[32:35],v9,s[40:43],s87 offen sc1\n"
MF "v[116:119],a[240:243],a[168:171],v[116:119]\n"
"v_add_f32 v40,v40,v80\n"
"v_add_f32 v41,v41,v81\n"
MF "v[116:119],v[188:191],a[128:131],v[116:119]\n"
"v_add_f32 v42,v42,v82\n"
"v_add_f32 v43,v43,v83\n"
MF "v[108:111],v[188:191],a[124:127],v[108:111]\n"
"v_add_f32 v44,v44,v84\n"
"v_add_f32 v45,v45,v85\n"
MF "v[108:111],a[240:243],a[164:167],v[108:111]\n"
"v_add_f32 v46,v46,v86\n"
"v_add_f32 v47,v47,v87\n"
MF "v[108:111],a[244:247],a[204:207],v[108:111]\n"
CV "v40,v40,v41\n"
CV "v41,v42,v43\n"
MF "v[100:103],a[244:247],a[200:203],v[100:103]\n"
"s_add_u32 s87,s90,3072\n"
CV "v42,v44,v45\n"
CV "v43,v46,v47\n"
MF "v[100:103],a[240:243],a[160:163],v[100:103]\n"
"buffer_store_dwordx4 v[40:43],v9,s[40:43],s87 offen sc1\n"
MF "v[100:103],v[188:191],a[120:123],v[100:103]\n"
"s_waitcnt lgkmcnt(0)\n"
"v_add_f32 v48,v48,v88\n"
"v_add_f32 v49,v49,v89\n"
MF "v[136:139],v[176:179],a[120:123],v[136:139]\n"
"s_nop 0\n"
TR "v[188:189],v3\n"
TR "v[190:191],v3 offset:2048\n"
MF "v[136:139],v[180:183],a[160:163],v[136:139]\n"
"s_nop 0\n"
TR "a[240:241],v3 offset:4096\n"
TR "a[242:243],v3 offset:6144\n"
MF "v[136:139],v[184:187],a[200:203],v[136:139]\n"
"s_nop 0\n"
TR "a[244:245],v3 offset:8192\n"
TR "a[246:247],v3 offset:10240\n"
MF "v[144:147],v[184:187],a[204:207],v[144:147]\n"
"v_add_f32 v50,v50,v90\n"
"v_add_f32 v51,v51,v91\n"
MF "v[144:147],v[180:183],a[164:167],v[144:147]\n"
"v_add_f32 v52,v52,v92\n"
"v_add_f32 v53,v53,v93\n"
MF "v[144:147],v[176:179],a[124:127],v[144:147]\n"
"v_add_f32 v54,v54,v94\n"
"v_add_f32 v55,v55,v95\n"
MF "v[152:155],v[176:179],a[128:131],v[152:155]\n"
CV "v48,v48,v49\n"
CV "v49,v50,v51\n"
MF "v[152:155],v[180:183],a[168:171],v[152:155]\n"
"s_add_u32 s87,s90,4096\n"
CV "v50,v52,v53\n"
CV "v51,v54,v55\n"
MF "v[152:155],v[184:187],a[208:211],v[152:155]\n"
"buffer_store_dwordx4 v[48:51],v9,s[40:43],s87 offen sc1\n"
MF "v[160:163],v[184:187],a[212:215],v[160:163]\n"
MF "v[160:163],v[180:183],a[172:175],v[160:163]\n"
MF "v[160:163],v[176:179],a[132:135],v[160:163]\n"
MF "v[168:171],v[176:179],a[136:139],v[168:171]\n"
MF "v[168:171],v[180:183],a[176:179],v[168:171]\n"
"s_waitcnt lgkmcnt(0)\n"
MF "v[168:171],v[184:187],a[216:219],v[168:171]\n"
"s_barrier\n"
MF "v[172:175],a[244:247],a[216:219],v[172:175]\n"
"s_nop 0\n"
TR "v[176:177],v0 offset:49152\n"
TR "v[178:179],v0 offset:51200\n"
MF "v[172:175],a[240:243],a[176:179],v[172:175]\n"
"s_nop 0\n"
TR "v[180:181],v0 offset:53248\n"
TR "v[182:183],v0 offset:55296\n"
MF "v[172:175],v[188:191],a[136:139],v[172:175]\n"
"s_nop 0\n"
TR "v[184:185],v0 offset:57344\n"
TR "v[186:187],v0 offset:59392\n"
MF "v[164:167],v[188:191],a[132:135],v[164:167]\n"
"buffer_load_dword v10,v12,s[40:43],s86 offen sc1\n"
MF "v[164:167],a[240:243],a[172:175],v[164:167]\n"
MF "v[164:167],a[244:247],a[212:215],v[164:167]\n"
LD "a[0:3],v9,s[40:43],s3 offen sc1\n"
MF "v[156:159],a[244:247],a[208:211],v[156:159]\n"
MF "v[156:159],a[240:243],a[168:171],v[156:159]\n"
MF "v[156:159],v[188:191],a[128:131],v[156:159]\n"
MF "v[148:151],v[188:191],a[124:127],v[148:151]\n"
MF "v[148:151],a[240:243],a[164:167],v[148:151]\n"
"s_add_u32 s3,s3,204800\n"
MF "v[148:151],a[244:247],a[204:207],v[148:151]\n"
LD "a[4:7],v9,s[40:43],s3 offen sc1\n"
MF "v[140:143],a[244:247],a[200:203],v[140:143]\n"
MF "v[140:143],a[240:243],a[160:163],v[140:143]\n"
MF "v[140:143],v[188:191],a[120:123],v[140:143]\n"
"s_waitcnt lgkmcnt(0)\n"
MF "v[96:99],v[176:179],a[20:23],v[96:99]\n"
"s_nop 0\n"
TR "v[188:189],v1 offset:49152\n"
TR "v[190:191],v1 offset:51200\n"
MF "v[96:99],v[180:183],a[60:63],v[96:99]\n"
"s_nop 0\n"
TR "a[240:241],v1 offset:53248\n"
TR "a[242:243],v1 offset:55296\n"
MF "v[96:99],v[184:187],a[100:103],v[96:99]\n"
"s_add_u32 s3,s3,204800\n"
TR "a[244:245],v1 offset:57344\n"
TR "a[246:247],v1 offset:59392\n"
MF "v[104:107],v[184:187],a[104:107],v[104:107]\n"
LD "a[8:11],v9,s[40:43],s3 offen sc1\n"
MF "v[104:107],v[180:183],a[64:67],v[104:107]\n"
MF "v[104:107],v[176:179],a[24:27],v[104:107]\n"
"s_waitcnt vmcnt(10)\n"
"v_mov_b32 v236,v228\n"
"v_mov_b32 v237,v229\n"
MF "v[136:139],v[208:211],a[252:255],v[136:139]\n"
MF "v[144:147],v[212:215],a[252:255],v[144:147]\n"
MF "v[152:155],v[216:219],a[252:255],v[152:155]\n"
MF "v[160:163],v[220:223],a[252:255],v[160:163]\n"
MF "v[168:171],v[224:227],a[252:255],v[168:171]\n"
MF "v[140:143],v[208:211],a[248:251],v[140:143]\n"
MF "v[148:151],v[212:215],a[248:251],v[148:151]\n"
MF "v[156:159],v[216:219],a[248:251],v[156:159]\n"
MF "v[164:167],v[220:223],a[248:251],v[164:167]\n"
MF "v[172:175],v[224:227],a[248:251],v[172:175]\n"
MF "v[248:251],v[230:233],a[250:253],v[248:251]\n"
MF "v[244:247],v[234:237],a[250:253],v[244:247]\n"
MF "v[112:115],v[176:179],a[28:31],v[112:115]\n"
MF "v[112:115],v[180:183],a[68:71],v[112:115]\n"
"s_add_u32 s3,s3,204800\n"
MF "v[112:115],v[184:187],a[108:111],v[112:115]\n"
LD "a[12:15],v9,s[40:43],s3 offen sc1\n"
MF "v[120:123],v[184:187],a[112:115],v[120:123]\n"
"s_nop 1\n"
CV "v244,v244,v245\n"
CV "v245,v246,v247\n"
MF "v[120:123],v[180:183],a[72:75],v[120:123]\n"
"s_bfe_u32 s87,s22,0x30014\n"
CV "v246,v248,v249\n"
CV "v247,v250,v251\n"
MF "v[120:123],v[176:179],a[32:35],v[120:123]\n"
"s_lshl_b32 s87,s87,7\n"
"v_permlane16_swap_b32 v244,v246\n"
"v_permlane16_swap_b32 v245,v247\n"
MF "v[128:131],v[176:179],a[36:39],v[128:131]\n"
"buffer_store_dwordx4 v[244:247],v8,s[44:47],s0 offen sc1\n"
MF "v[128:131],v[180:183],a[76:79],v[128:131]\n"
"s_add_u32 s3,s3,204800\n"
MF "v[128:131],v[184:187],a[116:119],v[128:131]\n"
"s_waitcnt lgkmcnt(0)\n"
LD "a[40:43],v9,s[40:43],s3 offen sc1\n"
MF "v[132:135],a[244:247],a[116:119],v[132:135]\n"
"s_and_b32 s60,s56,1\n"
TR "v[176:177],v2 offset:49152\n"
TR "v[178:179],v2 offset:51200\n"
MF "v[132:135],a[240:243],a[76:79],v[132:135]\n"
"s_mul_i32 s60,s60,204800\n"
TR "v[180:181],v2 offset:53248\n"
TR "v[182:183],v2 offset:55296\n"
MF "v[132:135],v[188:191],a[36:39],v[132:135]\n"
"s_add_u32 s87,s87,s60\n"
TR "v[184:185],v2 offset:57344\n"
TR "v[186:187],v2 offset:59392\n"
MF "v[124:127],v[188:191],a[32:35],v[124:127]\n"
"s_mov_b32 s71,204800\n"
MF "v[124:127],a[240:243],a[72:75],v[124:127]\n"
"s_mov_b32 s66,204800\n"
MF "v[124:127],a[244:247],a[112:115],v[124:127]\n"
"s_add_u32 s72,s78,2\n"
MF "v[116:119],a[244:247],a[108:111],v[116:119]\n"
"s_add_u32 s3,s3,204800\n"
MF "v[116:119],a[240:243],a[68:71],v[116:119]\n"
LD "a[44:47],v9,s[40:43],s3 offen sc1\n"
MF "v[116:119],v[188:191],a[28:31],v[116:119]\n"
"s_and_b32 s72,s72,15\n"
MF "v[108:111],v[188:191],a[24:27],v[108:111]\n"
"s_add_u32 s16,s78,6\n"
MF "v[108:111],a[240:243],a[64:67],v[108:111]\n"
"s_and_b32 s16,s16,15\n"
MF "v[108:111],a[244:247],a[104:107],v[108:111]\n"
"s_add_u32 s17,s78,4\n"
MF "v[100:103],a[244:247],a[100:103],v[100:103]\n"
"s_and_b32 s17,s17,15\n"
MF "v[100:103],a[240:243],a[60:63],v[100:103]\n"
"s_add_u32 s3,s3,204800\n"
MF "v[100:103],v[188:191],a[20:23],v[100:103]\n"
"s_waitcnt lgkmcnt(0)\n"
LD "a[48:51],v9,s[40:43],s3 offen sc1\n"
MF "v[136:139],v[176:179],a[20:23],v[136:139]\n"
"s_bfe_u32 s60,s22,0x10014\n"
TR "v[188:189],v3 offset:49152\n"
TR "v[190:191],v3 offset:51200\n"
MF "v[136:139],v[180:183],a[60:63],v[136:139]\n"
"s_lshl_b32 s60,s60,5\n"
TR "a[240:241],v3 offset:53248\n"
TR "a[242:243],v3 offset:55296\n"
MF "v[136:139],v[184:187],a[100:103],v[136:139]\n"
"s_nop 0\n"
TR "a[244:245],v3 offset:57344\n"
TR "a[246:247],v3 offset:59392\n"
MF "v[144:147],v[184:187],a[104:107],v[144:147]\n"
"v_xor_b32 v242,s60,v8\n"
MF "v[144:147],v[180:183],a[64:67],v[144:147]\n"
"s_add_u32 s3,s3,204800\n"
"v_mov_b32 v14,s76\n"
"s_add_u32 s76,s76,1\n"
MF "v[144:147],v[176:179],a[24:27],v[144:147]\n"
LD "a[52:55],v9,s[40:43],s3 offen sc1\n"
MF "v[152:155],v[176:179],a[28:31],v[152:155]\n"
MF "v[152:155],v[180:183],a[68:71],v[152:155]\n"
MF "v[152:155],v[184:187],a[108:111],v[152:155]\n"
MF "v[160:163],v[184:187],a[112:115],v[160:163]\n"
MF "v[160:163],v[180:183],a[72:75],v[160:163]\n"
MF "v[160:163],v[176:179],a[32:35],v[160:163]\n"
MF "v[168:171],v[176:179],a[36:39],v[168:171]\n"
MF "v[168:171],v[180:183],a[76:79],v[168:171]\n"
"s_waitcnt vmcnt(9) lgkmcnt(0)\n"
MF "v[168:171],v[184:187],a[116:119],v[168:171]\n"
"s_barrier\n"
MF "v[172:175],a[244:247],a[116:119],v[172:175]\n"
"buffer_store_dword v14,v15,s[40:43],s94 offen sc1\n"
MF "v[172:175],a[240:243],a[76:79],v[172:175]\n"
"s_nop 0\n"
TR "v[176:177],v6\n"
TR "v[178:179],v6 offset:2048\n"
MF "v[172:175],v[188:191],a[36:39],v[172:175]\n"
"s_nop 0\n"
TR "v[180:181],v6 offset:4096\n"
TR "v[182:183],v6 offset:6144\n"
MF "v[164:167],v[188:191],a[32:35],v[164:167]\n"
"s_nop 0\n"
TR "v[184:185],v6 offset:8192\n"
TR "v[186:187],v6 offset:10240\n"
MF "v[164:167],a[240:243],a[72:75],v[164:167]\n"
"v_sub_u32 v10,v10,v11\n"
MF "v[164:167],a[244:247],a[112:115],v[164:167]\n"
"v_readlane_b32 s16,v10,s16\n"
MF "v[156:159],a[244:247],a[108:111],v[156:159]\n"
"s_cmp_lt_u32 s16,9\n"
"s_cselect_b32 s16,0xf0000000,0\n"
MF "v[156:159],a[240:243],a[68:71],v[156:159]\n"
"v_readlane_b32 s17,v10,s17\n"
MF "v[156:159],v[188:191],a[28:31],v[156:159]\n"
"s_cmp_lt_u32 s17,10\n"
"s_cselect_b32 s17,0xf0000000,0\n"
MF "v[148:151],v[188:191],a[24:27],v[148:151]\n"
"s_bfe_u32 s62,s22,0x30014\n"
MF "v[148:151],a[240:243],a[64:67],v[148:151]\n"
"s_add_u32 s62,s62,3\n"
"s_and_b32 s62,s62,7\n"
"s_mul_i32 s62,s62,204800\n"
MF "v[148:151],a[244:247],a[104:107],v[148:151]\n"
"s_add_u32 s62,s62,s73\n"
"s_add_u32 s62,s62,122880\n"
"s_or_b32 s62,s62,s16\n"
MF "v[140:143],a[244:247],a[100:103],v[140:143]\n"
LD "v[56:59],v9,s[40:43],s62 offen sc1\n"
MF "v[140:143],a[240:243],a[60:63],v[140:143]\n"
LD "v[64:67],v9,s[40:43],s62 offen offset:1024 sc1\n"
MF "v[140:143],v[188:191],a[20:23],v[140:143]\n"
"s_waitcnt lgkmcnt(0)\n"
LD "v[72:75],v9,s[40:43],s62 offen offset:2048 sc1\n"
MF "v[136:139],v[176:179],a[140:143],v[136:139]\n"
"s_nop 0\n"
TR "v[188:189],v7\n"
TR "v[190:191],v7 offset:2048\n"
MF "v[136:139],v[180:183],a[180:183],v[136:139]\n"
"s_nop 0\n"
TR "a[240:241],v7 offset:4096\n"
TR "a[242:243],v7 offset:6144\n"
MF "v[136:139],v[184:187],a[220:223],v[136:139]\n"
"s_nop 0\n"
TR "a[244:245],v7 offset:8192\n"
TR "a[246:247],v7 offset:10240\n"
MF "v[144:147],v[184:187],a[224:227],v[144:147]\n"
LD "v[80:83],v9,s[40:43],s62 offen offset:3072 sc1\n"
MF "v[144:147],v[180:183],a[184:187],v[144:147]\n"
MF "v[144:147],v[176:179],a[144:147],v[144:147]\n"
"s_add_u32 s62,s62,4096\n"
MF "v[152:155],v[176:179],a[148:151],v[152:155]\n"
LD "v[88:91],v9,s[40:43],s62 offen sc1\n"
MF "v[152:155],v[180:183],a[188:191],v[152:155]\n"
"s_bfe_u32 s62,s22,0x30014\n"
"ds_write_addtid_b32 v136\n"
"ds_write_addtid_b32 v137 offset:256\n"
MF "v[152:155],v[184:187],a[228:231],v[152:155]\n"
"s_add_u32 s62,s62,2\n"
"s_and_b32 s62,s62,7\n"
"s_mul_i32 s62,s62,204800\n"
MF "v[160:163],v[184:187],a[232:235],v[160:163]\n"
"s_add_u32 s62,s62,s73\n"
"s_add_u32 s62,s62,143360\n"
"s_or_b32 s62,s62,s17\n"
MF "v[160:163],v[180:183],a[192:195],v[160:163]\n"
LD "v[60:63],v9,s[40:43],s62 offen sc1\n"
MF "v[160:163],v[176:179],a[152:155],v[160:163]\n"
"s_nop 0\n"
"ds_write_addtid_b32 v138 offset:512\n"
"ds_write_addtid_b32 v139 offset:768\n"
MF "v[168:171],v[176:179],a[156:159],v[168:171]\n"
LD "v[68:71],v9,s[40:43],s62 offen offset:1024 sc1\n"
MF "v[168:171],v[180:183],a[196:199],v[168:171]\n"
"s_nop 0\n"
"ds_write_addtid_b32 v144 offset:2048\n"
"ds_write_addtid_b32 v145 offset:2304\n"
MF "v[168:171],v[184:187],a[236:239],v[168:171]\n"
"s_waitcnt lgkmcnt(6)\n"
LD "v[76:79],v9,s[40:43],s62 offen offset:2048 sc1\n"
MF "v[140:143],v[188:191],a[140:143],v[140:143]\n"
"s_nop 0\n"
TR "v[176:177],v4\n"
TR "v[178:179],v4 offset:2048\n"
MF "v[140:143],a[240:243],a[180:183],v[140:143]\n"
"s_nop 0\n"
TR "v[180:181],v4 offset:4096\n"
TR "v[182:183],v4 offset:6144\n"
MF "v[140:143],a[244:247],a[220:223],v[140:143]\n"
"s_nop 0\n"
TR "v[184:185],v4 offset:8192\n"
TR "v[186:187],v4 offset:10240\n"
MF "v[148:151],a[244:247],a[224:227],v[148:151]\n"
LD "v[84:87],v9,s[40:43],s62 offen offset:3072 sc1\n"
MF "v[148:151],a[240:243],a[184:187],v[148:151]\n"
"s_nop 0\n"
"ds_write_addtid_b32 v146 offset:2560\n"
"ds_write_addtid_b32 v147 offset:2816\n"
MF "v[148:151],v[188:191],a[144:147],v[148:151]\n"
"s_add_u32 s62,s62,4096\n"
"ds_write_addtid_b32 v152 offset:4096\n"
"ds_write_addtid_b32 v153 offset:4352\n"
MF "v[156:159],v[188:191],a[148:151],v[156:159]\n"
LD "v[92:95],v9,s[40:43],s62 offen sc1\n"
MF "v[156:159],a[240:243],a[188:191],v[156:159]\n"
"s_nop 0\n"
"ds_write_addtid_b32 v154 offset:4608\n"
"ds_write_addtid_b32 v155 offset:4864\n"
MF "v[156:159],a[244:247],a[228:231],v[156:159]\n"
"s_nop 0\n"
"ds_write_addtid_b32 v160 offset:6144\n"
"ds_write_addtid_b32 v161 offset:6400\n"
MF "v[164:167],a[244:247],a[232:235],v[164:167]\n"
"s_nop 0\n"
"ds_write_addtid_b32 v162 offset:6656\n"
"ds_write_addtid_b32 v163 offset:6912\n"
MF "v[164:167],a[240:243],a[192:195],v[164:167]\n"
"s_nop 0\n"
"ds_write_addtid_b32 v168 offset:8192\n"
"ds_write_addtid_b32 v169 offset:8448\n"
MF "v[164:167],v[188:191],a[152:155],v[164:167]\n"
"buffer_load_dword v10,v12,s[40:43],s86 offen sc1\n"
MF "v[172:175],v[188:191],a[156:159],v[172:175]\n"
"s_nop 0\n"
"ds_write_addtid_b32 v170 offset:8704\n"
"ds_write_addtid_b32 v171 offset:8960\n"
MF "v[172:175],a[240:243],a[196:199],v[172:175]\n"
"s_nop 0\n"
"ds_write_addtid_b32 v140 offset:1024\n"
"ds_write_addtid_b32 v141 offset:1280\n"
MF "v[172:175],a[244:247],a[236:239],v[172:175]\n"
"s_waitcnt lgkmcnt(15)\n"
"ds_write_addtid_b32 v142 offset:1536\n"
"ds_write_addtid_b32 v143 offset:1792\n"
MF "v[96:99],v[176:179],a[140:143],v[96:99]\n"
"s_nop 0\n"
TR "v[188:189],v5\n"
TR "v[190:191],v5 offset:2048\n"
MF "v[96:99],v[180:183],a[180:183],v[96:99]\n"
"s_nop 0\n"
TR "a[240:241],v5 offset:4096\n"
TR "a[242:243],v5 offset:6144\n"
MF "v[96:99],v[184:187],a[220:223],v[96:99]\n"
"s_nop 0\n"
TR "a[244:245],v5 offset:8192\n"
TR "a[246:247],v5 offset:10240\n"
MF "v[104:107],v[184:187],a[224:227],v[104:107]\n"
"s_nop 0\n"
"ds_write_addtid_b32 v148 offset:3072\n"
"ds_write_addtid_b32 v149 offset:3328\n"
MF "v[104:107],v[180:183],a[184:187],v[104:107]\n"
"s_nop 0\n"
"ds_write_addtid_b32 v150 offset:3584\n"
"ds_write_addtid_b32 v151 offset:3840\n"
MF "v[104:107],v[176:179],a[144:147],v[104:107]\n"
"s_nop 0\n"
"ds_write_addtid_b32 v156 offset:5120\n"
"ds_write_addtid_b32 v157 offset:5376\n"
MF "v[112:115],v[176:179],a[148:151],v[112:115]\n"
"s_nop 0\n"
"ds_write_addtid_b32 v158 offset:5632\n"
"ds_write_addtid_b32 v159 offset:5888\n"
MF "v[112:115],v[180:183],a[188:191],v[112:115]\n"
"ds_write_addtid_b32 v164 offset:7168\n"
MF "v[112:115],v[184:187],a[228:231],v[112:115]\n"
"ds_write_addtid_b32 v165 offset:7424\n"
MF "v[120:123],v[184:187],a[232:235],v[120:123]\n"
"ds_write_addtid_b32 v166 offset:7680\n"
MF "v[120:123],v[180:183],a[192:195],v[120:123]\n"
"ds_write_addtid_b32 v167 offset:7936\n"
MF "v[120:123],v[176:179],a[152:155],v[120:123]\n"
"ds_write_addtid_b32 v172 offset:9216\n"
MF "v[128:131],v[176:179],a[156:159],v[128:131]\n"
"ds_write_addtid_b32 v173 offset:9472\n"
MF "v[128:131],v[180:183],a[196:199],v[128:131]\n"
"ds_write_addtid_b32 v174 offset:9728\n"
MF "v[128:131],v[184:187],a[236:239],v[128:131]\n"
"s_waitcnt lgkmcnt(15)\n"
"ds_write_addtid_b32 v175 offset:9984\n"
MF "v[100:103],v[188:191],a[140:143],v[100:103]\n"
MF "v[100:103],a[240:243],a[180:183],v[100:103]\n"
MF "v[100:103],a[244:247],a[220:223],v[100:103]\n"
MF "v[108:111],a[244:247],a[224:227],v[108:111]\n"
"s_waitcnt lgkmcnt(0)\n"
MF "v[108:111],a[240:243],a[184:187],v[108:111]\n"
"s_barrier\n"
MF "v[108:111],v[188:191],a[144:147],v[108:111]\n"
"ds_read2st64_b32 v[16:17],v13 offset0:192 offset1:193\n"
MF "v[116:119],v[188:191],a[148:151],v[116:119]\n"
"ds_read2st64_b32 v[18:19],v13 offset0:194 offset1:195\n"
MF "v[116:119],a[240:243],a[188:191],v[116:119]\n"
"ds_read2st64_b32 v[20:21],v13 offset0:196 offset1:197\n"
MF "v[116:119],a[244:247],a[228:231],v[116:119]\n"
"ds_read2st64_b32 v[22:23],v13 offset0:198 offset1:199\n"
MF "v[124:127],a[244:247],a[232:235],v[124:127]\n"
"ds_read2st64_b32 v[24:25],v13 offset0:200 offset1:201\n"
MF "v[124:127],a[240:243],a[192:195],v[124:127]\n"
"ds_read2st64_b32 v[26:27],v13 offset0:202 offset1:203\n"
MF "v[124:127],v[188:191],a[152:155],v[124:127]\n"
"ds_read2st64_b32 v[28:29],v13 offset0:204 offset1:205\n"
MF "v[132:135],v[188:191],a[156:159],v[132:135]\n"
"ds_read2st64_b32 v[30:31],v13 offset0:206 offset1:207\n"
MF "v[132:135],a[240:243],a[196:199],v[132:135]\n"
"ds_read2st64_b32 v[32:33],v13 offset0:208 offset1:209\n"
MF "v[132:135],a[244:247],a[236:239],v[132:135]\n"
"ds_read2st64_b32 v[34:35],v13 offset0:210 offset1:211\n"
"ds_read2st64_b32 v[36:37],v13 offset0:212 offset1:213\n"
"ds_read2st64_b32 v[38:39],v13 offset0:214 offset1:215\n"
"ds_read2st64_b32 v[40:41],v13 offset0:216 offset1:217\n"
"ds_read2st64_b32 v[42:43],v13 offset0:218 offset1:219\n"
"ds_read2st64_b32 v[44:45],v13 offset0:220 offset1:221\n"
"ds_read2st64_b32 v[46:47],v13 offset0:222 offset1:223\n"
"ds_read2st64_b32 v[48:49],v13 offset0:224 offset1:225\n"
"ds_read2st64_b32 v[50:51],v13 offset0:226 offset1:227\n"
"ds_read2st64_b32 v[52:53],v13 offset0:228 offset1:229\n"
"ds_read2st64_b32 v[54:55],v13 offset0:230 offset1:231\n"
"s_waitcnt vmcnt(0)\n"
"v_sub_u32 v10,v10,v11\n"
"s_cmp_eq_u32 s16,0\n"
"s_cbranch_scc1 .Lhave13\n"
"s_add_u32 s59,s78,6\n"
"s_and_b32 s59,s59,15\n"
"s_nop 3\n"
".Lnf9:\n"
"v_readlane_b32 s58,v10,s59\n"
"s_cmp_lt_u32 s58,9\n"
"s_cbranch_scc1 .Lsp9\n"
".Lnb9:\n"
"s_bfe_u32 s62,s22,0x30014\n"
"s_add_u32 s62,s62,3\n"
"s_and_b32 s62,s62,7\n"
"s_mul_i32 s62,s62,204800\n"
"s_add_u32 s62,s62,s73\n"
"s_add_u32 s62,s62,122880\n"
LD "v[56:59],v9,s[40:43],s62 offen sc1\n"
LD "v[64:67],v9,s[40:43],s62 offen offset:1024 sc1\n"
LD "v[72:75],v9,s[40:43],s62 offen offset:2048 sc1\n"
LD "v[80:83],v9,s[40:43],s62 offen offset:3072 sc1\n"
"s_add_u32 s62,s62,4096\n"
LD "v[88:91],v9,s[40:43],s62 offen sc1\n"
".Lhave13:\n"
"s_cmp_eq_u32 s17,0\n"
"s_cbranch_scc1 .Lhave12\n"
"s_add_u32 s59,s78,4\n"
"s_and_b32 s59,s59,15\n"
"s_nop 3\n"
".Lnf10:\n"
"v_readlane_b32 s58,v10,s59\n"
"s_cmp_lt_u32 s58,10\n"
"s_cbranch_scc1 .Lsp10\n"
".Lnb10:\n"
"s_bfe_u32 s62,s22,0x30014\n"
"s_add_u32 s62,s62,2\n"
"s_and_b32 s62,s62,7\n"
"s_mul_i32 s62,s62,204800\n"
"s_add_u32 s62,s62,s73\n"
"s_add_u32 s62,s62,143360\n"
LD "v[60:63],v9,s[40:43],s62 offen sc1\n"
LD "v[68:71],v9,s[40:43],s62 offen offset:1024 sc1\n"
LD "v[76:79],v9,s[40:43],s62 offen offset:2048 sc1\n"
LD "v[84:87],v9,s[40:43],s62 offen offset:3072 sc1\n"
"s_add_u32 s62,s62,4096\n"
LD "v[92:95],v9,s[40:43],s62 offen sc1\n"
".Lhave12:\n"
".Lnf11:\n"
"v_readlane_b32 s58,v10,s72\n"
"s_cmp_lt_u32 s58,11\n"
"s_cbranch_scc1 .Lsp11\n"
".Lnb11:\n"
"s_bfe_u32 s62,s22,0x30014\n"
"s_add_u32 s62,s62,1\n"
"s_and_b32 s62,s62,7\n"
"s_mul_i32 s62,s62,204800\n"
"s_add_u32 s62,s62,s73\n"
"s_nop 0\n"
"s_add_u32 s62,s62,163840\n"
LD "v[208:211],v9,s[40:43],s62 offen sc1\n"
LD "v[212:215],v9,s[40:43],s62 offen offset:1024 sc1\n"
LD "v[216:219],v9,s[40:43],s62 offen offset:2048 sc1\n"
LD "v[220:223],v9,s[40:43],s62 offen offset:3072 sc1\n"
".Lpg38h:\n"
"s_cbranch_execz .Lisr4\n"
"s_nop 0\n"
"s_add_u32 s62,s62,4096\n"
LD "v[224:227],v9,s[40:43],s62 offen sc1\n"
"s_cmp_eq_u32 s1,0xf0000000\n"
"s_cbranch_scc1 .Ljx1\n"
"v_accvgpr_mov_b32 a16,a0\n"
"v_accvgpr_mov_b32 a17,a1\n"
"v_accvgpr_mov_b32 a56,a40\n"
"v_accvgpr_mov_b32 a57,a41\n"
MF "v[196:199],a[2:5],a[250:253],0\n"
MF "v[192:195],a[6:9],a[250:253],0\n"
MF "v[196:199],a[10:13],a[250:253],v[196:199]\n"
MF "v[192:195],a[14:17],a[250:253],v[192:195]\n"
MF "v[196:199],a[42:45],a[250:253],v[196:199]\n"
MF "v[192:195],a[46:49],a[250:253],v[192:195]\n"
MF "v[196:199],a[50:53],a[250:253],v[196:199]\n"
MF "v[192:195],a[54:57],a[250:253],v[192:195]\n"
"s_nop 7\n"
CV "v192,v192,v193\n"
CV "v193,v194,v195\n"
CV "v194,v196,v197\n"
CV "v195,v198,v199\n"
"s_nop 0\n"
"v_permlane16_swap_b32 v192,v194\n"
"v_permlane16_swap_b32 v193,v195\n"
"buffer_store_dwordx4 v[192:195],v8,s[44:47],s1 offen sc1\n"
".Ljx1:\n"
"s_waitcnt lgkmcnt(15)\n"
"v_pk_add_f32 v[96:97],v[96:97],v[16:17]\n"
"v_pk_add_f32 v[98:99],v[98:99],v[18:19]\n"
"v_pk_add_f32 v[100:101],v[100:101],v[20:21]\n"
"v_pk_add_f32 v[102:103],v[102:103],v[22:23]\n"
"v_pk_add_f32 v[104:105],v[104:105],v[24:25]\n"
"s_waitcnt lgkmcnt(14)\n"
"v_pk_add_f32 v[106:107],v[106:107],v[26:27]\n"
"s_waitcnt lgkmcnt(13)\n"
"v_pk_add_f32 v[108:109],v[108:109],v[28:29]\n"
"s_waitcnt lgkmcnt(12)\n"
"v_pk_add_f32 v[110:111],v[110:111],v[30:31]\n"
"s_waitcnt lgkmcnt(11)\n"
"v_pk_add_f32 v[112:113],v[112:113],v[32:33]\n"
"s_waitcnt lgkmcnt(10)\n"
"v_pk_add_f32 v[114:115],v[114:115],v[34:35]\n"
"s_waitcnt lgkmcnt(9)\n"
"v_pk_add_f32 v[116:117],v[116:117],v[36:37]\n"
"s_waitcnt lgkmcnt(8)\n"
"v_pk_add_f32 v[118:119],v[118:119],v[38:39]\n"
"s_waitcnt lgkmcnt(7)\n"
"v_pk_add_f32 v[120:121],v[120:121],v[40:41]\n"
"s_waitcnt lgkmcnt(6)\n"
"v_pk_add_f32 v[122:123],v[122:123],v[42:43]\n"
"s_waitcnt lgkmcnt(5)\n"
"v_pk_add_f32 v[124:125],v[124:125],v[44:45]\n"
"s_waitcnt lgkmcnt(4)\n"
"v_pk_add_f32 v[126:127],v[126:127],v[46:47]\n"
"s_waitcnt lgkmcnt(3)\n"
"v_pk_add_f32 v[128:129],v[128:129],v[48:49]\n"
"s_waitcnt lgkmcnt(2)\n"
"v_pk_add_f32 v[130:131],v[130:131],v[50:51]\n"
"s_waitcnt lgkmcnt(1)\n"
"v_pk_add_f32 v[132:133],v[132:133],v[52:53]\n"
"s_waitcnt lgkmcnt(0)\n"
"v_pk_add_f32 v[134:135],v[134:135],v[54:55]\n"
"s_waitcnt vmcnt(5)\n"
"s_nop 0\n"
MF "v[100:103],v[56:59],a[252:255],v[100:103]\n"
MF "v[108:111],v[64:67],a[252:255],v[108:111]\n"
MF "v[116:119],v[72:75],a[252:255],v[116:119]\n"
MF "v[124:127],v[80:83],a[252:255],v[124:127]\n"
MF "v[132:135],v[88:91],a[252:255],v[132:135]\n"
MF "v[96:99],v[56:59],a[248:251],v[96:99]\n"
MF "v[104:107],v[64:67],a[248:251],v[104:107]\n"
MF "v[112:115],v[72:75],a[248:251],v[112:115]\n"
MF "v[120:123],v[80:83],a[248:251],v[120:123]\n"
MF "v[128:131],v[88:91],a[248:251],v[128:131]\n"
MF "v[96:99],v[60:63],a[252:255],v[96:99]\n"
MF "v[104:107],v[68:71],a[252:255],v[104:107]\n"
MF "v[112:115],v[76:79],a[252:255],v[112:115]\n"
MF "v[120:123],v[84:87],a[252:255],v[120:123]\n"
MF "v[128:131],v[92:95],a[252:255],v[128:131]\n"
MF "v[100:103],v[60:63],a[248:251],v[100:103]\n"
MF "v[108:111],v[68:71],a[248:251],v[108:111]\n"
MF "v[116:119],v[76:79],a[248:251],v[116:119]\n"
MF "v[124:127],v[84:87],a[248:251],v[124:127]\n"
MF "v[132:135],v[92:95],a[248:251],v[132:135]\n"
"s_waitcnt vmcnt(0)\n"
MF "v[100:103],v[208:211],a[252:255],v[100:103]\n"
MF "v[96:99],v[208:211],a[248:251],v[96:99]\n"
MF "v[108:111],v[212:215],a[252:255],v[108:111]\n"
MF "v[104:107],v[212:215],a[248:251],v[104:107]\n"
MF "v[116:119],v[216:219],a[252:255],v[116:119]\n"
"s_nop 4\n"
CV "v96,v96,v97\n"
CV "v97,v98,v99\n"
MF "v[112:115],v[216:219],a[248:251],v[112:115]\n"
CV "v98,v100,v101\n"
CV "v99,v102,v103\n"
MF "v[124:127],v[220:223],a[252:255],v[124:127]\n"
CV "v104,v104,v105\n"
CV "v105,v106,v107\n"
MF "v[120:123],v[220:223],a[248:251],v[120:123]\n"
CV "v106,v108,v109\n"
CV "v107,v110,v111\n"
"v_permlane16_swap_b32 v96,v98\n"
"v_permlane16_swap_b32 v97,v99\n"
"buffer_store_dwordx4 v[96:99],v242,s[44:47],s87 offen sc1\n"
"s_add_u32 s87,s87,40960\n"
MF "v[132:135],v[224:227],a[252:255],v[132:135]\n"
CV "v112,v112,v113\n"
CV "v113,v114,v115\n"
MF "v[128:131],v[224:227],a[248:251],v[128:131]\n"
CV "v114,v116,v117\n"
CV "v115,v118,v119\n"
"v_permlane16_swap_b32 v104,v106\n"
"v_permlane16_swap_b32 v105,v107\n"
"buffer_store_dwordx4 v[104:107],v242,s[44:47],s87 offen sc1\n"
"s_add_u32 s87,s87,40960\n"
CV "v120,v120,v121\n"
CV "v121,v122,v123\n"
CV "v122,v124,v125\n"
CV "v123,v126,v127\n"
"v_permlane16_swap_b32 v112,v114\n"
"v_permlane16_swap_b32 v113,v115\n"
"buffer_store_dwordx4 v[112:115],v242,s[44:47],s87 offen sc1\n"
"s_add_u32 s87,s87,40960\n"
CV "v128,v128,v129\n"
CV "v129,v130,v131\n"
CV "v130,v132,v133\n"
CV "v131,v134,v135\n"
"v_permlane16_swap_b32 v120,v122\n"
"v_permlane16_swap_b32 v121,v123\n"
"buffer_store_dwordx4 v[120:123],v242,s[44:47],s87 offen sc1\n"
"s_add_u32 s87,s87,40960\n"
"v_permlane16_swap_b32 v128,v130\n"
"v_permlane16_swap_b32 v129,v131\n"
"buffer_store_dwordx4 v[128:131],v242,s[44:47],s87 offen sc1\n"
"s_add_u32 s87,s87,40960\n"
"s_endpgm\n"
".Ljobs:\n"
"s_cmp_ge_u32 s92,40\n"
"s_cbranch_scc1 .Ldone\n"
"buffer_load_dword v10,v12,s[40:43],s86 offen sc1\n"
"s_mul_i32 s95,s92,3277\n"
"s_lshr_b32 s95,s95,16\n"
"s_mul_i32 s59,s95,20\n"
"s_sub_u32 s59,s92,s59\n"
"s_cmp_ge_u32 s59,10\n"
"s_cselect_b32 s96,4294557760,0\n"
"s_add_u32 s96,s96,1024\n"
"s_mul_i32 s59,s59,40960\n"
"s_add_u32 s96,s96,s59\n"
"s_lshl_b32 s59,s95,7\n"
"s_add_u32 s96,s96,s59\n"
"s_add_u32 s95,s95,3\n"
"s_waitcnt vmcnt(0)\n"
".Ljtry:\n"
"s_cmp_eq_u32 s98,0\n"
"s_cselect_b32 s95,0,s95\n"
"v_sub_u32 v10,v10,v11\n"
"v_cmp_gt_u32 vcc,s95,v10\n"
"s_lshl_b32 s82,s92,10\n"
"s_add_u32 s82,s82,s93\n"
"s_cmp_eq_u64 vcc,0\n"
"s_cselect_b32 s82,s82,0xf0000000\n"
"s_cselect_b32 s84,s96,0xf0000000\n"
"s_cselect_b32 s59,s4,0\n"
"s_add_u32 s92,s92,s59\n"
"s_cmp_eq_u32 s59,0\n"
"s_cbranch_scc0 .Ljgo\n"
"s_sub_u32 s98,s98,1\n"
"s_sleep 2\n"
"s_branch .Ljobs\n"
".Ljgo:\n"
"s_mov_b32 s83,s84\n"
LD "v[208:211],v9,s[40:43],s82 offen sc1\n"
"s_add_u32 s82,s82,s71\n"
LD "v[212:215],v9,s[40:43],s82 offen sc1\n"
"s_add_u32 s82,s82,s71\n"
LD "v[216:219],v9,s[40:43],s82 offen sc1\n"
"s_add_u32 s82,s82,s71\n"
LD "v[220:223],v9,s[40:43],s82 offen sc1\n"
"s_add_u32 s82,s82,s71\n"
LD "v[224:227],v9,s[40:43],s82 offen sc1\n"
"s_add_u32 s82,s82,s66\n"
LD "v[228:231],v9,s[40:43],s82 offen sc1\n"
"s_add_u32 s82,s82,s71\n"
LD "v[232:235],v9,s[40:43],s82 offen sc1\n"
"s_add_u32 s82,s82,s71\n"
LD "v[236:239],v9,s[40:43],s82 offen sc1\n"
"buffer_load_dword v10,v12,s[40:43],s86 offen sc1\n"
"s_waitcnt vmcnt(0)\n"
".Ljfin:\n"
"s_nop 1\n"
"v_mov_b32 v240,v208\n"
"v_mov_b32 v241,v209\n"
MF "v[248:251],v[210:213],a[250:253],0\n"
MF "v[244:247],v[214:217],a[250:253],0\n"
MF "v[248:251],v[218:221],a[250:253],v[248:251]\n"
MF "v[244:247],v[222:225],a[250:253],v[244:247]\n"
MF "v[248:251],v[226:229],a[250:253],v[248:251]\n"
MF "v[244:247],v[230:233],a[250:253],v[244:247]\n"
MF "v[248:251],v[234:237],a[250:253],v[248:251]\n"
MF "v[244:247],v[238:241],a[250:253],v[244:247]\n"
"s_nop 7\n"
CV "v244,v244,v245\n"
CV "v245,v246,v247\n"
CV "v246,v248,v249\n"
CV "v247,v250,v251\n"
"s_nop 0\n"
"v_permlane16_swap_b32 v244,v246\n"
"v_permlane16_swap_b32 v245,v247\n"
"buffer_store_dwordx4 v[244:247],v8,s[44:47],s83 offen sc1\n"
"s_cmp_ge_u32 s92,40\n"
"s_cbranch_scc1 .Ldone\n"
"s_mul_i32 s95,s92,3277\n"
"s_lshr_b32 s95,s95,16\n"
"s_mul_i32 s59,s95,20\n"
"s_sub_u32 s59,s92,s59\n"
"s_cmp_ge_u32 s59,10\n"
"s_cselect_b32 s96,4294557760,0\n"
"s_add_u32 s96,s96,1024\n"
"s_mul_i32 s59,s59,40960\n"
"s_add_u32 s96,s96,s59\n"
"s_lshl_b32 s59,s95,7\n"
"s_add_u32 s96,s96,s59\n"
"s_add_u32 s95,s95,3\n"
"s_branch .Ljtry\n"
".Lool:\n"
".Lsp0:\n"
"s_cmp_eq_u32 s98,0\n"
"s_cbranch_scc1 .Lnb0\n"
"s_sub_u32 s98,s98,1\n"
"s_sleep 2\n"
"buffer_load_dword v10,v12,s[40:43],s86 offen sc1\n"
"s_waitcnt vmcnt(0)\n"
"v_sub_u32 v10,v10,v11\n"
"s_nop 1\n"
"s_branch .Lnf0\n"
".Lsp1:\n"
"s_cmp_eq_u32 s98,0\n"
"s_cbranch_scc1 .Lnb1\n"
"s_sub_u32 s98,s98,1\n"
"s_sleep 2\n"
"buffer_load_dword v10,v12,s[40:43],s86 offen sc1\n"
"s_waitcnt vmcnt(0)\n"
"v_sub_u32 v10,v10,v11\n"
"s_nop 1\n"
"s_branch .Lnf1\n"
".Lsp2:\n"
"s_cmp_eq_u32 s98,0\n"
"s_cbranch_scc1 .Lnb2\n"
"s_sub_u32 s98,s98,1\n"
"s_sleep 2\n"
"buffer_load_dword v10,v12,s[40:43],s86 offen sc1\n"
"s_waitcnt vmcnt(0)\n"
"v_sub_u32 v10,v10,v11\n"
"s_nop 1\n"
"s_branch .Lnf2\n"
".Lsp3:\n"
"s_cmp_eq_u32 s98,0\n"
"s_cbranch_scc1 .Lnb3\n"
"s_sub_u32 s98,s98,1\n"
"s_sleep 2\n"
"buffer_load_dword v10,v12,s[40:43],s86 offen sc1\n"
"s_waitcnt vmcnt(0)\n"
"v_sub_u32 v10,v10,v11\n"
"s_nop 1\n"
"s_branch .Lnf3\n"
".Lsp4:\n"
"s_cmp_eq_u32 s98,0\n"
"s_cbranch_scc1 .Lnb4\n"
"s_sub_u32 s98,s98,1\n"
"s_sleep 2\n"
"buffer_load_dword v10,v12,s[40:43],s86 offen sc1\n"
"s_waitcnt vmcnt(0)\n"
"v_sub_u32 v10,v10,v11\n"
"s_nop 1\n"
"s_branch .Lnf4\n"
".Lsp5:\n"
"s_cmp_eq_u32 s98,0\n"
"s_cbranch_scc1 .Lnb5\n"
"s_sub_u32 s98,s98,1\n"
"s_sleep 2\n"
"buffer_load_dword v10,v12,s[40:43],s86 offen sc1\n"
"s_waitcnt vmcnt(0)\n"
"v_sub_u32 v10,v10,v11\n"
"s_nop 1\n"
"s_branch .Lnf5\n"
".Lsp6:\n"
"s_cmp_eq_u32 s98,0\n"
"s_cbranch_scc1 .Lnb6\n"
"s_sub_u32 s98,s98,1\n"
"s_sleep 2\n"
"buffer_load_dword v10,v12,s[40:43],s86 offen sc1\n"
"s_waitcnt vmcnt(0)\n"
"v_sub_u32 v10,v10,v11\n"
"s_nop 1\n"
"s_branch .Lnf6\n"
".Lsp7:\n"
"s_cmp_eq_u32 s98,0\n"
"s_cbranch_scc1 .Lnb7\n"
"s_sub_u32 s98,s98,1\n"
"s_sleep 2\n"
"buffer_load_dword v10,v12,s[40:43],s86 offen sc1\n"
"s_waitcnt vmcnt(0)\n"
"v_sub_u32 v10,v10,v11\n"
"s_nop 1\n"
"s_branch .Lnf7\n"
".Lsp8:\n"
"s_cmp_eq_u32 s98,0\n"
"s_cbranch_scc1 .Lnb8\n"
"s_sub_u32 s98,s98,1\n"
"s_sleep 2\n"
"buffer_load_dword v10,v12,s[40:43],s86 offen sc1\n"
"s_waitcnt vmcnt(0)\n"
"v_sub_u32 v10,v10,v11\n"
"s_nop 1\n"
"s_branch .Lnf8\n"
".Lsp9:\n"
"s_cmp_eq_u32 s98,0\n"
"s_cbranch_scc1 .Lnb9\n"
"s_sub_u32 s98,s98,1\n"
"s_sleep 2\n"
"buffer_load_dword v10,v12,s[40:43],s86 offen sc1\n"
"s_waitcnt vmcnt(0)\n"
"v_sub_u32 v10,v10,v11\n"
"s_nop 1\n"
"s_branch .Lnf9\n"
".Lsp10:\n"
"s_cmp_eq_u32 s98,0\n"
"s_cbranch_scc1 .Lnb10\n"
"s_sub_u32 s98,s98,1\n"
"s_sleep 2\n"
"buffer_load_dword v10,v12,s[40:43],s86 offen sc1\n"
"s_waitcnt vmcnt(0)\n"
"v_sub_u32 v10,v10,v11\n"
"s_nop 1\n"
"s_branch .Lnf10\n"
".Lsp11:\n"
"s_cmp_eq_u32 s98,0\n"
"s_cbranch_scc1 .Lnb11\n"
"s_sub_u32 s98,s98,1\n"
"s_sleep 2\n"
"buffer_load_dword v10,v12,s[40:43],s86 offen sc1\n"
"s_waitcnt vmcnt(0)\n"
"v_sub_u32 v10,v10,v11\n"
"s_nop 1\n"
"s_branch .Lnf11\n"
".Lcode1:\n"
".Lslow_0:\n"
"s_nop 7\n"
"s_nop 7\n"
"v_and_b32 v10,240,v9\n"
"v_mul_u32_u24 v10,24,v10\n"
"v_lshrrev_b32 v11,4,v9\n"
"v_and_or_b32 v10,v11,48,v10\n"
"s_mov_b32 s59,s34\n"
"s_mov_b32 s34,s20\n"
"s_add_u32 s58,s74,0\n"
"s_nop 3\n"
LD "v[52:55],v10,s[32:35],s58 offen\n"
"s_waitcnt vmcnt(0)\n"
MF "v[72:75],v[52:55],v[136:139],v[32:35]\n"
MF "v[76:79],v[52:55],v[140:143],v[36:39]\n"
MF "v[80:83],v[52:55],v[144:147],v[40:43]\n"
MF "v[84:87],v[52:55],v[148:151],v[44:47]\n"
MF "v[88:91],v[52:55],v[152:155],v[48:51]\n"
"s_add_u32 s58,s74,0\n"
"s_nop 3\n"
LD "v[52:55],v10,s[32:35],s58 offen offset:64\n"
"s_waitcnt vmcnt(0)\n"
MF "v[72:75],v[52:55],v[156:159],v[72:75]\n"
MF "v[76:79],v[52:55],v[160:163],v[76:79]\n"
MF "v[80:83],v[52:55],v[164:167],v[80:83]\n"
MF "v[84:87],v[52:55],v[168:171],v[84:87]\n"
MF "v[88:91],v[52:55],v[172:175],v[88:91]\n"
"s_add_u32 s58,s74,0\n"
"s_nop 3\n"
LD "v[52:55],v10,s[32:35],s58 offen offset:128\n"
"s_waitcnt vmcnt(0)\n"
MF "v[72:75],v[52:55],v[176:179],v[72:75]\n"
MF "v[76:79],v[52:55],v[180:183],v[76:79]\n"
MF "v[80:83],v[52:55],v[184:187],v[80:83]\n"
MF "v[84:87],v[52:55],v[188:191],v[84:87]\n"
MF "v[88:91],v[52:55],v[192:195],v[88:91]\n"
"s_add_u32 s58,s74,0\n"
"s_nop 3\n"
LD "v[52:55],v10,s[32:35],s58 offen offset:192\n"
"s_waitcnt vmcnt(0)\n"
MF "v[72:75],v[52:55],v[196:199],v[72:75]\n"
MF "v[76:79],v[52:55],v[200:203],v[76:79]\n"
MF "v[80:83],v[52:55],v[204:207],v[80:83]\n"
MF "v[84:87],v[52:55],v[208:211],v[84:87]\n"
MF "v[88:91],v[52:55],v[212:215],v[88:91]\n"
"s_add_u32 s58,s74,0\n"
"s_nop 3\n"
LD "v[52:55],v10,s[32:35],s58 offen offset:256\n"
"s_waitcnt vmcnt(0)\n"
MF "v[72:75],v[52:55],v[216:219],v[72:75]\n"
MF "v[76:79],v[52:55],v[220:223],v[76:79]\n"
MF "v[80:83],v[52:55],v[224:227],v[80:83]\n"
MF "v[84:87],v[52:55],v[228:231],v[84:87]\n"
MF "v[88:91],v[52:55],v[232:235],v[88:91]\n"
"s_add_u32 s58,s74,0\n"
"s_nop 3\n"
LD "v[52:55],v10,s[32:35],s58 offen offset:320\n"
"s_waitcnt vmcnt(0)\n"
MF "v[72:75],v[52:55],v[236:239],v[72:75]\n"
MF "v[76:79],v[52:55],v[240:243],v[76:79]\n"
MF "v[80:83],v[52:55],v[244:247],v[80:83]\n"
MF "v[84:87],v[52:55],v[248:251],v[84:87]\n"
MF "v[88:91],v[52:55],v[252:255],v[88:91]\n"
"s_add_u32 s58,s74,6144\n"
"s_nop 3\n"
LD "v[52:55],v10,s[32:35],s58 offen\n"
"s_waitcnt vmcnt(0)\n"
MF "v[112:115],v[52:55],v[136:139],v[32:35]\n"
MF "v[116:119],v[52:55],v[140:143],v[36:39]\n"
MF "v[120:123],v[52:55],v[144:147],v[40:43]\n"
MF "v[124:127],v[52:55],v[148:151],v[44:47]\n"
MF "v[128:131],v[52:55],v[152:155],v[48:51]\n"
"s_add_u32 s58,s74,6144\n"
"s_nop 3\n"
LD "v[52:55],v10,s[32:35],s58 offen offset:64\n"
"s_waitcnt vmcnt(0)\n"
MF "v[112:115],v[52:55],v[156:159],v[112:115]\n"
MF "v[116:119],v[52:55],v[160:163],v[116:119]\n"
MF "v[120:123],v[52:55],v[164:167],v[120:123]\n"
MF "v[124:127],v[52:55],v[168:171],v[124:127]\n"
MF "v[128:131],v[52:55],v[172:175],v[128:131]\n"
"s_add_u32 s58,s74,6144\n"
"s_nop 3\n"
LD "v[52:55],v10,s[32:35],s58 offen offset:128\n"
"s_waitcnt vmcnt(0)\n"
MF "v[112:115],v[52:55],v[176:179],v[112:115]\n"
MF "v[116:119],v[52:55],v[180:183],v[116:119]\n"
MF "v[120:123],v[52:55],v[184:187],v[120:123]\n"
MF "v[124:127],v[52:55],v[188:191],v[124:127]\n"
MF "v[128:131],v[52:55],v[192:195],v[128:131]\n"
"s_add_u32 s58,s74,6144\n"
"s_nop 3\n"
LD "v[52:55],v10,s[32:35],s58 offen offset:192\n"
"s_waitcnt vmcnt(0)\n"
MF "v[112:115],v[52:55],v[196:199],v[112:115]\n"
MF "v[116:119],v[52:55],v[200:203],v[116:119]\n"
MF "v[120:123],v[52:55],v[204:207],v[120:123]\n"
MF "v[124:127],v[52:55],v[208:211],v[124:127]\n"
MF "v[128:131],v[52:55],v[212:215],v[128:131]\n"
"s_add_u32 s58,s74,6144\n"
"s_nop 3\n"
LD "v[52:55],v10,s[32:35],s58 offen offset:256\n"
"s_waitcnt vmcnt(0)\n"
MF "v[112:115],v[52:55],v[216:219],v[112:115]\n"
MF "v[116:119],v[52:55],v[220:223],v[116:119]\n"
MF "v[120:123],v[52:55],v[224:227],v[120:123]\n"
MF "v[124:127],v[52:55],v[228:231],v[124:127]\n"
MF "v[128:131],v[52:55],v[232:235],v[128:131]\n"
"s_add_u32 s58,s74,6144\n"
"s_nop 3\n"
LD "v[52:55],v10,s[32:35],s58 offen offset:320\n"
"s_waitcnt vmcnt(0)\n"
MF "v[112:115],v[52:55],v[236:239],v[112:115]\n"
MF "v[116:119],v[52:55],v[240:243],v[116:119]\n"
MF "v[120:123],v[52:55],v[244:247],v[120:123]\n"
MF "v[124:127],v[52:55],v[248:251],v[124:127]\n"
MF "v[128:131],v[52:55],v[252:255],v[128:131]\n"
"s_nop 7\n"
"s_mov_b32 s34,s59\n"
"v_max3_f32 v10,v72,v73,v74\n"
"s_nop 0\n"
"v_max3_f32 v11,v75,v112,v113\n"
"v_max3_f32 v10,v10,v114,v115\n"
"v_max_f32 v10,v10,v11\n"
"v_mov_b32 v11,v10\n"
"s_nop 1\n"
"v_permlane16_swap_b32 v10,v11\n"
"v_max_f32 v10,v10,v11\n"
"v_mov_b32 v11,v10\n"
"s_nop 1\n"
"v_permlane32_swap_b32 v10,v11\n"
"v_max_f32 v10,v10,v11\n"
"v_add_f32 v10,0x41300000,v10\n"
"v_max_f32 v10,0,v10\n"
"v_exp_f32 v11,-v10\n"
AR "v12,a0\n"
"v_subrev_f32 v12,s13,v12\n"
"v_fma_f32 v12,v12,v11,s13\n"
AW "a0,v12\n"
AR "v12,a20\n"
"v_subrev_f32 v12,s13,v12\n"
"v_fma_f32 v12,v12,v11,s13\n"
AW "a20,v12\n"
AR "v12,a40\n"
"v_subrev_f32 v12,s13,v12\n"
"v_fma_f32 v12,v12,v11,s13\n"
AW "a40,v12\n"
AR "v12,a60\n"
"v_subrev_f32 v12,s13,v12\n"
"v_fma_f32 v12,v12,v11,s13\n"
AW "a60,v12\n"
AR "v12,a80\n"
"v_subrev_f32 v12,s13,v12\n"
"v_fma_f32 v12,v12,v11,s13\n"
AW "a80,v12\n"
AR "v12,a100\n"
"v_subrev_f32 v12,s13,v12\n"
"v_fma_f32 v12,v12,v11,s13\n"
AW "a100,v12\n"
AR "v12,a120\n"
"v_subrev_f32 v12,s13,v12\n"
"v_fma_f32 v12,v12,v11,s13\n"
AW "a120,v12\n"
AR "v12,a140\n"
"v_subrev_f32 v12,s13,v12\n"
"v_fma_f32 v12,v12,v11,s13\n"
AW "a140,v12\n"
AR "v12,a160\n"
"v_subrev_f32 v12,s13,v12\n"
"v_fma_f32 v12,v12,v11,s13\n"
AW "a160,v12\n"
AR "v12,a180\n"
"v_subrev_f32 v12,s13,v12\n"
"v_fma_f32 v12,v12,v11,s13\n"
AW "a180,v12\n"
AR "v12,a200\n"
"v_subrev_f32 v12,s13,v12\n"
"v_fma_f32 v12,v12,v11,s13\n"
AW "a200,v12\n"
AR "v12,a220\n"
"v_subrev_f32 v12,s13,v12\n"
"v_fma_f32 v12,v12,v11,s13\n"
AW "a220,v12\n"
AR "v12,a1\n"
"v_subrev_f32 v12,s13,v12\n"
"v_fma_f32 v12,v12,v11,s13\n"
AW "a1,v12\n"
AR "v12,a21\n"
"v_subrev_f32 v12,s13,v12\n"
"v_fma_f32 v12,v12,v11,s13\n"
AW "a21,v12\n"
AR "v12,a41\n"
"v_subrev_f32 v12,s13,v12\n"
"v_fma_f32 v12,v12,v11,s13\n"
AW "a41,v12\n"
AR "v12,a61\n"
"v_subrev_f32 v12,s13,v12\n"
"v_fma_f32 v12,v12,v11,s13\n"
AW "a61,v12\n"
AR "v12,a81\n"
"v_subrev_f32 v12,s13,v12\n"
"v_fma_f32 v12,v12,v11,s13\n"
AW "a81,v12\n"
AR "v12,a101\n"
"v_subrev_f32 v12,s13,v12\n"
"v_fma_f32 v12,v12,v11,s13\n"
AW "a101,v12\n"
AR "v12,a121\n"
"v_subrev_f32 v12,s13,v12\n"
"v_fma_f32 v12,v12,v11,s13\n"
AW "a121,v12\n"
AR "v12,a141\n"
"v_subrev_f32 v12,s13,v12\n"
"v_fma_f32 v12,v12,v11,s13\n"
AW "a141,v12\n"
AR "v12,a161\n"
"v_subrev_f32 v12,s13,v12\n"
"v_fma_f32 v12,v12,v11,s13\n"
AW "a161,v12\n"
AR "v12,a181\n"
"v_subrev_f32 v12,s13,v12\n"
"v_fma_f32 v12,v12,v11,s13\n"
AW "a181,v12\n"
AR "v12,a201\n"
"v_subrev_f32 v12,s13,v12\n"
"v_fma_f32 v12,v12,v11,s13\n"
AW "a201,v12\n"
AR "v12,a221\n"
"v_subrev_f32 v12,s13,v12\n"
"v_fma_f32 v12,v12,v11,s13\n"
AW "a221,v12\n"
AR "v12,a2\n"
"v_subrev_f32 v12,s13,v12\n"
"v_fma_f32 v12,v12,v11,s13\n"
AW "a2,v12\n"
AR "v12,a22\n"
"v_subrev_f32 v12,s13,v12\n"
"v_fma_f32 v12,v12,v11,s13\n"
AW "a22,v12\n"
AR "v12,a42\n"
"v_subrev_f32 v12,s13,v12\n"
"v_fma_f32 v12,v12,v11,s13\n"
AW "a42,v12\n"
AR "v12,a62\n"
"v_subrev_f32 v12,s13,v12\n"
"v_fma_f32 v12,v12,v11,s13\n"
AW "a62,v12\n"
AR "v12,a82\n"
"v_subrev_f32 v12,s13,v12\n"
"v_fma_f32 v12,v12,v11,s13\n"
AW "a82,v12\n"
AR "v12,a102\n"
"v_subrev_f32 v12,s13,v12\n"
"v_fma_f32 v12,v12,v11,s13\n"
AW "a102,v12\n"
AR "v12,a122\n"
"v_subrev_f32 v12,s13,v12\n"
"v_fma_f32 v12,v12,v11,s13\n"
AW "a122,v12\n"
AR "v12,a142\n"
"v_subrev_f32 v12,s13,v12\n"
"v_fma_f32 v12,v12,v11,s13\n"
AW "a142,v12\n"
AR "v12,a162\n"
"v_subrev_f32 v12,s13,v12\n"
"v_fma_f32 v12,v12,v11,s13\n"
AW "a162,v12\n"
AR "v12,a182\n"
"v_subrev_f32 v12,s13,v12\n"
"v_fma_f32 v12,v12,v11,s13\n"
AW "a182,v12\n"
AR "v12,a202\n"
"v_subrev_f32 v12,s13,v12\n"
"v_fma_f32 v12,v12,v11,s13\n"
AW "a202,v12\n"
AR "v12,a222\n"
"v_subrev_f32 v12,s13,v12\n"
"v_fma_f32 v12,v12,v11,s13\n"
AW "a222,v12\n"
AR "v12,a3\n"
"v_subrev_f32 v12,s13,v12\n"
"v_fma_f32 v12,v12,v11,s13\n"
AW "a3,v12\n"
AR "v12,a23\n"
"v_subrev_f32 v12,s13,v12\n"
"v_fma_f32 v12,v12,v11,s13\n"
AW "a23,v12\n"
AR "v12,a43\n"
"v_subrev_f32 v12,s13,v12\n"
"v_fma_f32 v12,v12,v11,s13\n"
AW "a43,v12\n"
AR "v12,a63\n"
"v_subrev_f32 v12,s13,v12\n"
"v_fma_f32 v12,v12,v11,s13\n"
AW "a63,v12\n"
AR "v12,a83\n"
"v_subrev_f32 v12,s13,v12\n"
"v_fma_f32 v12,v12,v11,s13\n"
AW "a83,v12\n"
AR "v12,a103\n"
"v_subrev_f32 v12,s13,v12\n"
"v_fma_f32 v12,v12,v11,s13\n"
AW "a103,v12\n"
AR "v12,a123\n"
"v_subrev_f32 v12,s13,v12\n"
"v_fma_f32 v12,v12,v11,s13\n"
AW "a123,v12\n"
AR "v12,a143\n"
"v_subrev_f32 v12,s13,v12\n"
"v_fma_f32 v12,v12,v11,s13\n"
AW "a143,v12\n"
AR "v12,a163\n"
"v_subrev_f32 v12,s13,v12\n"
"v_fma_f32 v12,v12,v11,s13\n"
AW "a163,v12\n"
AR "v12,a183\n"
"v_subrev_f32 v12,s13,v12\n"
"v_fma_f32 v12,v12,v11,s13\n"
AW "a183,v12\n"
AR "v12,a203\n"
"v_subrev_f32 v12,s13,v12\n"
"v_fma_f32 v12,v12,v11,s13\n"
AW "a203,v12\n"
AR "v12,a223\n"
"v_subrev_f32 v12,s13,v12\n"
"v_fma_f32 v12,v12,v11,s13\n"
AW "a223,v12\n"
"v_mul_f32 v52,v57,v11\n"
"v_sub_f32 v32,v32,v10\n"
"v_sub_f32 v33,v33,v10\n"
"v_sub_f32 v34,v34,v10\n"
"v_sub_f32 v35,v35,v10\n"
"v_sub_f32 v92,v92,v10\n"
"v_sub_f32 v93,v93,v10\n"
"v_sub_f32 v94,v94,v10\n"
"v_sub_f32 v95,v95,v10\n"
"v_sub_f32 v72,v72,v10\n"
"v_sub_f32 v73,v73,v10\n"
"v_sub_f32 v74,v74,v10\n"
"v_sub_f32 v75,v75,v10\n"
"v_sub_f32 v112,v112,v10\n"
"v_sub_f32 v113,v113,v10\n"
"v_sub_f32 v114,v114,v10\n"
"v_sub_f32 v115,v115,v10\n"
"v_exp_f32 v72,v72\n"
"v_exp_f32 v73,v73\n"
"v_exp_f32 v74,v74\n"
"v_exp_f32 v75,v75\n"
"v_exp_f32 v112,v112\n"
"v_exp_f32 v113,v113\n"
"v_exp_f32 v114,v114\n"
"v_exp_f32 v115,v115\n"
"v_add_f32 v52,v52,v72\n"
"v_add_f32 v52,v52,v73\n"
"v_add_f32 v52,v52,v74\n"
"v_add_f32 v52,v52,v75\n"
"v_add_f32 v52,v52,v112\n"
"v_add_f32 v52,v52,v113\n"
"v_add_f32 v52,v52,v114\n"
"v_add_f32 v52,v52,v115\n"
CV "v72,v72,v73\n"
CV "v73,v74,v75\n"
CV "v74,v112,v113\n"
CV "v75,v114,v115\n"
"v_max3_f32 v10,v76,v77,v78\n"
"v_max3_f32 v11,v79,v116,v117\n"
"v_max3_f32 v10,v10,v118,v119\n"
"v_max_f32 v10,v10,v11\n"
"v_mov_b32 v11,v10\n"
"s_nop 1\n"
"v_permlane16_swap_b32 v10,v11\n"
"v_max_f32 v10,v10,v11\n"
"v_mov_b32 v11,v10\n"
"s_nop 1\n"
"v_permlane32_swap_b32 v10,v11\n"
"v_max_f32 v10,v10,v11\n"
"v_add_f32 v10,0x41300000,v10\n"
"v_max_f32 v10,0,v10\n"
"v_exp_f32 v11,-v10\n"
AR "v12,a4\n"
"v_subrev_f32 v12,s13,v12\n"
"v_fma_f32 v12,v12,v11,s13\n"
AW "a4,v12\n"
AR "v12,a24\n"
"v_subrev_f32 v12,s13,v12\n"
"v_fma_f32 v12,v12,v11,s13\n"
AW "a24,v12\n"
AR "v12,a44\n"
"v_subrev_f32 v12,s13,v12\n"
"v_fma_f32 v12,v12,v11,s13\n"
AW "a44,v12\n"
AR "v12,a64\n"
"v_subrev_f32 v12,s13,v12\n"
"v_fma_f32 v12,v12,v11,s13\n"
AW "a64,v12\n"
AR "v12,a84\n"
"v_subrev_f32 v12,s13,v12\n"
"v_fma_f32 v12,v12,v11,s13\n"
AW "a84,v12\n"
AR "v12,a104\n"
"v_subrev_f32 v12,s13,v12\n"
"v_fma_f32 v12,v12,v11,s13\n"
AW "a104,v12\n"
AR "v12,a124\n"
"v_subrev_f32 v12,s13,v12\n"
"v_fma_f32 v12,v12,v11,s13\n"
AW "a124,v12\n"
AR "v12,a144\n"
"v_subrev_f32 v12,s13,v12\n"
"v_fma_f32 v12,v12,v11,s13\n"
AW "a144,v12\n"
AR "v12,a164\n"
"v_subrev_f32 v12,s13,v12\n"
"v_fma_f32 v12,v12,v11,s13\n"
AW "a164,v12\n"
AR "v12,a184\n"
"v_subrev_f32 v12,s13,v12\n"
"v_fma_f32 v12,v12,v11,s13\n"
AW "a184,v12\n"
AR "v12,a204\n"
"v_subrev_f32 v12,s13,v12\n"
"v_fma_f32 v12,v12,v11,s13\n"
AW "a204,v12\n"
AR "v12,a224\n"
"v_subrev_f32 v12,s13,v12\n"
"v_fma_f32 v12,v12,v11,s13\n"
AW "a224,v12\n"
AR "v12,a5\n"
"v_subrev_f32 v12,s13,v12\n"
"v_fma_f32 v12,v12,v11,s13\n"
AW "a5,v12\n"
AR "v12,a25\n"
"v_subrev_f32 v12,s13,v12\n"
"v_fma_f32 v12,v12,v11,s13\n"
AW "a25,v12\n"
AR "v12,a45\n"
"v_subrev_f32 v12,s13,v12\n"
"v_fma_f32 v12,v12,v11,s13\n"
AW "a45,v12\n"
AR "v12,a65\n"
"v_subrev_f32 v12,s13,v12\n"
"v_fma_f32 v12,v12,v11,s13\n"
AW "a65,v12\n"
AR "v12,a85\n"
"v_subrev_f32 v12,s13,v12\n"
"v_fma_f32 v12,v12,v11,s13\n"
AW "a85,v12\n"
AR "v12,a105\n"
"v_subrev_f32 v12,s13,v12\n"
"v_fma_f32 v12,v12,v11,s13\n"
AW "a105,v12\n"
AR "v12,a125\n"
"v_subrev_f32 v12,s13,v12\n"
"v_fma_f32 v12,v12,v11,s13\n"
AW "a125,v12\n"
AR "v12,a145\n"
"v_subrev_f32 v12,s13,v12\n"
"v_fma_f32 v12,v12,v11,s13\n"
AW "a145,v12\n"
AR "v12,a165\n"
"v_subrev_f32 v12,s13,v12\n"
"v_fma_f32 v12,v12,v11,s13\n"
AW "a165,v12\n"
AR "v12,a185\n"
"v_subrev_f32 v12,s13,v12\n"
"v_fma_f32 v12,v12,v11,s13\n"
AW "a185,v12\n"
AR "v12,a205\n"
"v_subrev_f32 v12,s13,v12\n"
"v_fma_f32 v12,v12,v11,s13\n"
AW "a205,v12\n"
AR "v12,a225\n"
"v_subrev_f32 v12,s13,v12\n"
"v_fma_f32 v12,v12,v11,s13\n"
AW "a225,v12\n"
AR "v12,a6\n"
"v_subrev_f32 v12,s13,v12\n"
"v_fma_f32 v12,v12,v11,s13\n"
AW "a6,v12\n"
AR "v12,a26\n"
"v_subrev_f32 v12,s13,v12\n"
"v_fma_f32 v12,v12,v11,s13\n"
AW "a26,v12\n"
AR "v12,a46\n"
"v_subrev_f32 v12,s13,v12\n"
"v_fma_f32 v12,v12,v11,s13\n"
AW "a46,v12\n"
AR "v12,a66\n"
"v_subrev_f32 v12,s13,v12\n"
"v_fma_f32 v12,v12,v11,s13\n"
AW "a66,v12\n"
AR "v12,a86\n"
"v_subrev_f32 v12,s13,v12\n"
"v_fma_f32 v12,v12,v11,s13\n"
AW "a86,v12\n"
AR "v12,a106\n"
"v_subrev_f32 v12,s13,v12\n"
"v_fma_f32 v12,v12,v11,s13\n"
AW "a106,v12\n"
AR "v12,a126\n"
"v_subrev_f32 v12,s13,v12\n"
"v_fma_f32 v12,v12,v11,s13\n"
AW "a126,v12\n"
AR "v12,a146\n"
"v_subrev_f32 v12,s13,v12\n"
"v_fma_f32 v12,v12,v11,s13\n"
AW "a146,v12\n"
AR "v12,a166\n"
"v_subrev_f32 v12,s13,v12\n"
"v_fma_f32 v12,v12,v11,s13\n"
AW "a166,v12\n"
AR "v12,a186\n"
"v_subrev_f32 v12,s13,v12\n"
"v_fma_f32 v12,v12,v11,s13\n"
AW "a186,v12\n"
AR "v12,a206\n"
"v_subrev_f32 v12,s13,v12\n"
"v_fma_f32 v12,v12,v11,s13\n"
AW "a206,v12\n"
AR "v12,a226\n"
"v_subrev_f32 v12,s13,v12\n"
"v_fma_f32 v12,v12,v11,s13\n"
AW "a226,v12\n"
AR "v12,a7\n"
"v_subrev_f32 v12,s13,v12\n"
"v_fma_f32 v12,v12,v11,s13\n"
AW "a7,v12\n"
AR "v12,a27\n"
"v_subrev_f32 v12,s13,v12\n"
"v_fma_f32 v12,v12,v11,s13\n"
AW "a27,v12\n"
AR "v12,a47\n"
"v_subrev_f32 v12,s13,v12\n"
"v_fma_f32 v12,v12,v11,s13\n"
AW "a47,v12\n"
AR "v12,a67\n"
"v_subrev_f32 v12,s13,v12\n"
"v_fma_f32 v12,v12,v11,s13\n"
AW "a67,v12\n"
AR "v12,a87\n"
"v_subrev_f32 v12,s13,v12\n"
"v_fma_f32 v12,v12,v11,s13\n"
AW "a87,v12\n"
AR "v12,a107\n"
"v_subrev_f32 v12,s13,v12\n"
"v_fma_f32 v12,v12,v11,s13\n"
AW "a107,v12\n"
AR "v12,a127\n"
"v_subrev_f32 v12,s13,v12\n"
"v_fma_f32 v12,v12,v11,s13\n"
AW "a127,v12\n"
AR "v12,a147\n"
"v_subrev_f32 v12,s13,v12\n"
"v_fma_f32 v12,v12,v11,s13\n"
AW "a147,v12\n"
AR "v12,a167\n"
"v_subrev_f32 v12,s13,v12\n"
"v_fma_f32 v12,v12,v11,s13\n"
AW "a167,v12\n"
AR "v12,a187\n"
"v_subrev_f32 v12,s13,v12\n"
"v_fma_f32 v12,v12,v11,s13\n"
AW "a187,v12\n"
AR "v12,a207\n"
"v_subrev_f32 v12,s13,v12\n"
"v_fma_f32 v12,v12,v11,s13\n"
AW "a207,v12\n"
AR "v12,a227\n"
"v_subrev_f32 v12,s13,v12\n"
"v_fma_f32 v12,v12,v11,s13\n"
AW "a227,v12\n"
"v_mul_f32 v53,v58,v11\n"
"v_sub_f32 v36,v36,v10\n"
"v_sub_f32 v37,v37,v10\n"
"v_sub_f32 v38,v38,v10\n"
"v_sub_f32 v39,v39,v10\n"
"v_sub_f32 v96,v96,v10\n"
"v_sub_f32 v97,v97,v10\n"
"v_sub_f32 v98,v98,v10\n"
"v_sub_f32 v99,v99,v10\n"
"v_sub_f32 v76,v76,v10\n"
"v_sub_f32 v77,v77,v10\n"
"v_sub_f32 v78,v78,v10\n"
"v_sub_f32 v79,v79,v10\n"
"v_sub_f32 v116,v116,v10\n"
"v_sub_f32 v117,v117,v10\n"
"v_sub_f32 v118,v118,v10\n"
"v_sub_f32 v119,v119,v10\n"
"v_exp_f32 v76,v76\n"
"v_exp_f32 v77,v77\n"
"v_exp_f32 v78,v78\n"
"v_exp_f32 v79,v79\n"
"v_exp_f32 v116,v116\n"
"v_exp_f32 v117,v117\n"
"v_exp_f32 v118,v118\n"
"v_exp_f32 v119,v119\n"
"v_add_f32 v53,v53,v76\n"
"v_add_f32 v53,v53,v77\n"
"v_add_f32 v53,v53,v78\n"
"v_add_f32 v53,v53,v79\n"
"v_add_f32 v53,v53,v116\n"
"v_add_f32 v53,v53,v117\n"
"v_add_f32 v53,v53,v118\n"
"v_add_f32_e64 v53,v53,v119\n"
CV "v76,v76,v77\n"
CV "v77,v78,v79\n"
CV "v78,v116,v117\n"
CV "v79,v118,v119\n"
"v_max3_f32 v10,v80,v81,v82\n"
"v_max3_f32 v11,v83,v120,v121\n"
"v_max3_f32 v10,v10,v122,v123\n"
"v_max_f32 v10,v10,v11\n"
"v_mov_b32 v11,v10\n"
"s_nop 1\n"
"v_permlane16_swap_b32 v10,v11\n"
"v_max_f32 v10,v10,v11\n"
"v_mov_b32 v11,v10\n"
"s_nop 1\n"
"v_permlane32_swap_b32 v10,v11\n"
"v_max_f32 v10,v10,v11\n"
"v_add_f32 v10,0x41300000,v10\n"
"v_max_f32 v10,0,v10\n"
"v_exp_f32 v11,-v10\n"
AR "v12,a8\n"
"v_subrev_f32 v12,s13,v12\n"
"v_fma_f32 v12,v12,v11,s13\n"
AW "a8,v12\n"
AR "v12,a28\n"
"v_subrev_f32 v12,s13,v12\n"
"v_fma_f32 v12,v12,v11,s13\n"
AW "a28,v12\n"
AR "v12,a48\n"
"v_subrev_f32 v12,s13,v12\n"
"v_fma_f32 v12,v12,v11,s13\n"
AW "a48,v12\n"
AR "v12,a68\n"
"v_subrev_f32 v12,s13,v12\n"
"v_fma_f32 v12,v12,v11,s13\n"
AW "a68,v12\n"
AR "v12,a88\n"
"v_subrev_f32 v12,s13,v12\n"
"v_fma_f32 v12,v12,v11,s13\n"
AW "a88,v12\n"
AR "v12,a108\n"
"v_subrev_f32 v12,s13,v12\n"
"v_fma_f32 v12,v12,v11,s13\n"
AW "a108,v12\n"
AR "v12,a128\n"
"v_subrev_f32 v12,s13,v12\n"
"v_fma_f32 v12,v12,v11,s13\n"
AW "a128,v12\n"
AR "v12,a148\n"
"v_subrev_f32 v12,s13,v12\n"
"v_fma_f32 v12,v12,v11,s13\n"
AW "a148,v12\n"
AR "v12,a168\n"
"v_subrev_f32 v12,s13,v12\n"
"v_fma_f32 v12,v12,v11,s13\n"
AW "a168,v12\n"
AR "v12,a188\n"
"v_subrev_f32 v12,s13,v12\n"
"v_fma_f32 v12,v12,v11,s13\n"
AW "a188,v12\n"
AR "v12,a208\n"
"v_subrev_f32 v12,s13,v12\n"
"v_fma_f32 v12,v12,v11,s13\n"
AW "a208,v12\n"
AR "v12,a228\n"
"v_subrev_f32 v12,s13,v12\n"
"v_fma_f32 v12,v12,v11,s13\n"
AW "a228,v12\n"
AR "v12,a9\n"
"v_subrev_f32 v12,s13,v12\n"
"v_fma_f32 v12,v12,v11,s13\n"
AW "a9,v12\n"
AR "v12,a29\n"
"v_subrev_f32 v12,s13,v12\n"
"v_fma_f32 v12,v12,v11,s13\n"
AW "a29,v12\n"
AR "v12,a49\n"
"v_subrev_f32 v12,s13,v12\n"
"v_fma_f32 v12,v12,v11,s13\n"
AW "a49,v12\n"
AR "v12,a69\n"
"v_subrev_f32 v12,s13,v12\n"
"v_fma_f32 v12,v12,v11,s13\n"
AW "a69,v12\n"
AR "v12,a89\n"
"v_subrev_f32 v12,s13,v12\n"
"v_fma_f32 v12,v12,v11,s13\n"
AW "a89,v12\n"
AR "v12,a109\n"
"v_subrev_f32 v12,s13,v12\n"
"v_fma_f32 v12,v12,v11,s13\n"
AW "a109,v12\n"
AR "v12,a129\n"
"v_subrev_f32 v12,s13,v12\n"
"v_fma_f32 v12,v12,v11,s13\n"
AW "a129,v12\n"
AR "v12,a149\n"
"v_subrev_f32 v12,s13,v12\n"
"v_fma_f32 v12,v12,v11,s13\n"
AW "a149,v12\n"
AR "v12,a169\n"
"v_subrev_f32 v12,s13,v12\n"
"v_fma_f32 v12,v12,v11,s13\n"
AW "a169,v12\n"
AR "v12,a189\n"
"v_subrev_f32 v12,s13,v12\n"
"v_fma_f32 v12,v12,v11,s13\n"
AW "a189,v12\n"
AR "v12,a209\n"
"v_subrev_f32 v12,s13,v12\n"
"v_fma_f32 v12,v12,v11,s13\n"
AW "a209,v12\n"
AR "v12,a229\n"
"v_subrev_f32 v12,s13,v12\n"
"v_fma_f32 v12,v12,v11,s13\n"
AW "a229,v12\n"
AR "v12,a10\n"
"v_subrev_f32 v12,s13,v12\n"
"v_fma_f32 v12,v12,v11,s13\n"
AW "a10,v12\n"
AR "v12,a30\n"
"v_subrev_f32 v12,s13,v12\n"
"v_fma_f32 v12,v12,v11,s13\n"
AW "a30,v12\n"
AR "v12,a50\n"
"v_subrev_f32 v12,s13,v12\n"
"v_fma_f32 v12,v12,v11,s13\n"
AW "a50,v12\n"
AR "v12,a70\n"
"v_subrev_f32 v12,s13,v12\n"
"v_fma_f32 v12,v12,v11,s13\n"
AW "a70,v12\n"
AR "v12,a90\n"
"v_subrev_f32 v12,s13,v12\n"
"v_fma_f32 v12,v12,v11,s13\n"
AW "a90,v12\n"
AR "v12,a110\n"
"v_subrev_f32 v12,s13,v12\n"
"v_fma_f32 v12,v12,v11,s13\n"
AW "a110,v12\n"
AR "v12,a130\n"
"v_subrev_f32 v12,s13,v12\n"
"v_fma_f32 v12,v12,v11,s13\n"
AW "a130,v12\n"
AR "v12,a150\n"
"v_subrev_f32 v12,s13,v12\n"
"v_fma_f32 v12,v12,v11,s13\n"
AW "a150,v12\n"
AR "v12,a170\n"
"v_subrev_f32 v12,s13,v12\n"
"v_fma_f32 v12,v12,v11,s13\n"
AW "a170,v12\n"
AR "v12,a190\n"
"v_subrev_f32 v12,s13,v12\n"
"v_fma_f32 v12,v12,v11,s13\n"
AW "a190,v12\n"
AR "v12,a210\n"
"v_subrev_f32 v12,s13,v12\n"
"v_fma_f32 v12,v12,v11,s13\n"
AW "a210,v12\n"
AR "v12,a230\n"
"v_subrev_f32 v12,s13,v12\n"
"v_fma_f32 v12,v12,v11,s13\n"
AW "a230,v12\n"
AR "v12,a11\n"
"v_subrev_f32 v12,s13,v12\n"
"v_fma_f32 v12,v12,v11,s13\n"
AW "a11,v12\n"
AR "v12,a31\n"
"v_subrev_f32 v12,s13,v12\n"
"v_fma_f32 v12,v12,v11,s13\n"
AW "a31,v12\n"
AR "v12,a51\n"
"v_subrev_f32 v12,s13,v12\n"
"v_fma_f32 v12,v12,v11,s13\n"
AW "a51,v12\n"
AR "v12,a71\n"
"v_subrev_f32 v12,s13,v12\n"
"v_fma_f32 v12,v12,v11,s13\n"
AW "a71,v12\n"
AR "v12,a91\n"
"v_subrev_f32 v12,s13,v12\n"
"v_fma_f32 v12,v12,v11,s13\n"
AW "a91,v12\n"
AR "v12,a111\n"
"v_subrev_f32 v12,s13,v12\n"
"v_fma_f32 v12,v12,v11,s13\n"
AW "a111,v12\n"
AR "v12,a131\n"
"v_subrev_f32 v12,s13,v12\n"
"v_fma_f32 v12,v12,v11,s13\n"
AW "a131,v12\n"
AR "v12,a151\n"
"v_subrev_f32 v12,s13,v12\n"
"v_fma_f32 v12,v12,v11,s13\n"
AW "a151,v12\n"
AR "v12,a171\n"
"v_subrev_f32 v12,s13,v12\n"
"v_fma_f32 v12,v12,v11,s13\n"
AW "a171,v12\n"
AR "v12,a191\n"
"v_subrev_f32 v12,s13,v12\n"
"v_fma_f32 v12,v12,v11,s13\n"
AW "a191,v12\n"
AR "v12,a211\n"
"v_subrev_f32 v12,s13,v12\n"
"v_fma_f32 v12,v12,v11,s13\n"
AW "a211,v12\n"
AR "v12,a231\n"
"v_subrev_f32 v12,s13,v12\n"
"v_fma_f32 v12,v12,v11,s13\n"
AW "a231,v12\n"
"v_mul_f32 v54,v59,v11\n"
"v_sub_f32 v40,v40,v10\n"
"v_sub_f32 v41,v41,v10\n"
"v_sub_f32 v42,v42,v10\n"
"v_sub_f32 v43,v43,v10\n"
"v_sub_f32 v100,v100,v10\n"
"v_sub_f32 v101,v101,v10\n"
"v_sub_f32 v102,v102,v10\n"
"v_sub_f32 v103,v103,v10\n"
"v_sub_f32 v80,v80,v10\n"
"v_sub_f32 v81,v81,v10\n"
"v_sub_f32 v82,v82,v10\n"
"v_sub_f32 v83,v83,v10\n"
"v_sub_f32 v120,v120,v10\n"
"v_sub_f32 v121,v121,v10\n"
"v_sub_f32 v122,v122,v10\n"
"v_sub_f32 v123,v123,v10\n"
"v_exp_f32 v80,v80\n"
"v_exp_f32 v81,v81\n"
"v_exp_f32 v82,v82\n"
"v_exp_f32 v83,v83\n"
"v_exp_f32 v120,v120\n"
"v_exp_f32 v121,v121\n"
"v_exp_f32 v122,v122\n"
"v_exp_f32 v123,v123\n"
"v_add_f32 v54,v54,v80\n"
"v_add_f32 v54,v54,v81\n"
"v_add_f32 v54,v54,v82\n"
"v_add_f32 v54,v54,v83\n"
"v_add_f32 v54,v54,v120\n"
"v_add_f32 v54,v54,v121\n"
"v_add_f32 v54,v54,v122\n"
"v_add_f32_e64 v54,v54,v123\n"
CV "v80,v80,v81\n"
CV "v81,v82,v83\n"
CV "v82,v120,v121\n"
CV "v83,v122,v123\n"
"v_max3_f32 v10,v84,v85,v86\n"
"v_max3_f32 v11,v87,v124,v125\n"
"v_max3_f32 v10,v10,v126,v127\n"
"v_max_f32 v10,v10,v11\n"
"v_mov_b32 v11,v10\n"
"s_nop 1\n"
"v_permlane16_swap_b32 v10,v11\n"
"v_max_f32 v10,v10,v11\n"
"v_mov_b32 v11,v10\n"
"s_nop 1\n"
"v_permlane32_swap_b32 v10,v11\n"
"v_max_f32_e64 v10,v10,v11\n"
"v_add_f32 v10,0x41300000,v10\n"
"v_max_f32 v10,0,v10\n"
"v_exp_f32 v11,-v10\n"
AR "v12,a12\n"
"v_subrev_f32 v12,s13,v12\n"
"v_fma_f32 v12,v12,v11,s13\n"
AW "a12,v12\n"
AR "v12,a32\n"
"v_subrev_f32 v12,s13,v12\n"
"v_fma_f32 v12,v12,v11,s13\n"
AW "a32,v12\n"
AR "v12,a52\n"
"v_subrev_f32 v12,s13,v12\n"
"v_fma_f32 v12,v12,v11,s13\n"
AW "a52,v12\n"
AR "v12,a72\n"
"v_subrev_f32 v12,s13,v12\n"
"v_fma_f32 v12,v12,v11,s13\n"
AW "a72,v12\n"
AR "v12,a92\n"
"v_subrev_f32 v12,s13,v12\n"
"v_fma_f32 v12,v12,v11,s13\n"
AW "a92,v12\n"
AR "v12,a112\n"
"v_subrev_f32 v12,s13,v12\n"
"v_fma_f32 v12,v12,v11,s13\n"
AW "a112,v12\n"
AR "v12,a132\n"
"v_subrev_f32 v12,s13,v12\n"
"v_fma_f32 v12,v12,v11,s13\n"
AW "a132,v12\n"
AR "v12,a152\n"
"v_subrev_f32 v12,s13,v12\n"
"v_fma_f32 v12,v12,v11,s13\n"
AW "a152,v12\n"
AR "v12,a172\n"
"v_subrev_f32 v12,s13,v12\n"
"v_fma_f32 v12,v12,v11,s13\n"
AW "a172,v12\n"
AR "v12,a192\n"
"v_subrev_f32 v12,s13,v12\n"
"v_fma_f32 v12,v12,v11,s13\n"
AW "a192,v12\n"
AR "v12,a212\n"
"v_subrev_f32 v12,s13,v12\n"
"v_fma_f32 v12,v12,v11,s13\n"
AW "a212,v12\n"
AR "v12,a232\n"
"v_subrev_f32 v12,s13,v12\n"
"v_fma_f32 v12,v12,v11,s13\n"
AW "a232,v12\n"
AR "v12,a13\n"
"v_subrev_f32 v12,s13,v12\n"
"v_fma_f32 v12,v12,v11,s13\n"
AW "a13,v12\n"
AR "v12,a33\n"
"v_subrev_f32 v12,s13,v12\n"
"v_fma_f32 v12,v12,v11,s13\n"
AW "a33,v12\n"
AR "v12,a53\n"
"v_subrev_f32 v12,s13,v12\n"
"v_fma_f32 v12,v12,v11,s13\n"
AW "a53,v12\n"
AR "v12,a73\n"
"v_subrev_f32 v12,s13,v12\n"
"v_fma_f32 v12,v12,v11,s13\n"
AW "a73,v12\n"
AR "v12,a93\n"
"v_subrev_f32 v12,s13,v12\n"
"v_fma_f32 v12,v12,v11,s13\n"
AW "a93,v12\n"
AR "v12,a113\n"
"v_subrev_f32 v12,s13,v12\n"
"v_fma_f32 v12,v12,v11,s13\n"
AW "a113,v12\n"
AR "v12,a133\n"
"v_subrev_f32 v12,s13,v12\n"
"v_fma_f32 v12,v12,v11,s13\n"
AW "a133,v12\n"
AR "v12,a153\n"
"v_subrev_f32 v12,s13,v12\n"
"v_fma_f32 v12,v12,v11,s13\n"
AW "a153,v12\n"
AR "v12,a173\n"
"v_subrev_f32 v12,s13,v12\n"
"v_fma_f32 v12,v12,v11,s13\n"
AW "a173,v12\n"
AR "v12,a193\n"
"v_subrev_f32 v12,s13,v12\n"
"v_fma_f32 v12,v12,v11,s13\n"
AW "a193,v12\n"
AR "v12,a213\n"
"v_subrev_f32 v12,s13,v12\n"
"v_fma_f32 v12,v12,v11,s13\n"
AW "a213,v12\n"
AR "v12,a233\n"
"v_subrev_f32 v12,s13,v12\n"
"v_fma_f32 v12,v12,v11,s13\n"
AW "a233,v12\n"
AR "v12,a14\n"
"v_subrev_f32 v12,s13,v12\n"
"v_fma_f32 v12,v12,v11,s13\n"
AW "a14,v12\n"
AR "v12,a34\n"
"v_subrev_f32 v12,s13,v12\n"
"v_fma_f32 v12,v12,v11,s13\n"
AW "a34,v12\n"
AR "v12,a54\n"
"v_subrev_f32 v12,s13,v12\n"
"v_fma_f32 v12,v12,v11,s13\n"
AW "a54,v12\n"
AR "v12,a74\n"
"v_subrev_f32 v12,s13,v12\n"
"v_fma_f32 v12,v12,v11,s13\n"
AW "a74,v12\n"
AR "v12,a94\n"
"v_subrev_f32 v12,s13,v12\n"
"v_fma_f32 v12,v12,v11,s13\n"
AW "a94,v12\n"
AR "v12,a114\n"
"v_subrev_f32 v12,s13,v12\n"
"v_fma_f32 v12,v12,v11,s13\n"
AW "a114,v12\n"
AR "v12,a134\n"
"v_subrev_f32 v12,s13,v12\n"
"v_fma_f32 v12,v12,v11,s13\n"
AW "a134,v12\n"
AR "v12,a154\n"
"v_subrev_f32 v12,s13,v12\n"
"v_fma_f32 v12,v12,v11,s13\n"
AW "a154,v12\n"
AR "v12,a174\n"
"v_subrev_f32 v12,s13,v12\n"
"v_fma_f32 v12,v12,v11,s13\n"
AW "a174,v12\n"
AR "v12,a194\n"
"v_subrev_f32 v12,s13,v12\n"
"v_fma_f32 v12,v12,v11,s13\n"
AW "a194,v12\n"
AR "v12,a214\n"
"v_subrev_f32 v12,s13,v12\n"
"v_fma_f32 v12,v12,v11,s13\n"
AW "a214,v12\n"
AR "v12,a234\n"
"v_subrev_f32 v12,s13,v12\n"
"v_fma_f32 v12,v12,v11,s13\n"
AW "a234,v12\n"
AR "v12,a15\n"
"v_subrev_f32 v12,s13,v12\n"
"v_fma_f32 v12,v12,v11,s13\n"
AW "a15,v12\n"
AR "v12,a35\n"
"v_subrev_f32 v12,s13,v12\n"
"v_fma_f32 v12,v12,v11,s13\n"
AW "a35,v12\n"
AR "v12,a55\n"
"v_subrev_f32 v12,s13,v12\n"
"v_fma_f32 v12,v12,v11,s13\n"
AW "a55,v12\n"
AR "v12,a75\n"
"v_subrev_f32 v12,s13,v12\n"
"v_fma_f32 v12,v12,v11,s13\n"
AW "a75,v12\n"
AR "v12,a95\n"
"v_subrev_f32 v12,s13,v12\n"
"v_fma_f32 v12,v12,v11,s13\n"
AW "a95,v12\n"
AR "v12,a115\n"
"v_subrev_f32 v12,s13,v12\n"
"v_fma_f32 v12,v12,v11,s13\n"
AW "a115,v12\n"
AR "v12,a135\n"
"v_subrev_f32 v12,s13,v12\n"
"v_fma_f32 v12,v12,v11,s13\n"
AW "a135,v12\n"
AR "v12,a155\n"
"v_subrev_f32 v12,s13,v12\n"
"v_fma_f32 v12,v12,v11,s13\n"
AW "a155,v12\n"
AR "v12,a175\n"
"v_subrev_f32 v12,s13,v12\n"
"v_fma_f32 v12,v12,v11,s13\n"
AW "a175,v12\n"
AR "v12,a195\n"
"v_subrev_f32 v12,s13,v12\n"
"v_fma_f32 v12,v12,v11,s13\n"
AW "a195,v12\n"
AR "v12,a215\n"
"v_subrev_f32 v12,s13,v12\n"
"v_fma_f32 v12,v12,v11,s13\n"
AW "a215,v12\n"
AR "v12,a235\n"
"v_subrev_f32 v12,s13,v12\n"
"v_fma_f32 v12,v12,v11,s13\n"
AW "a235,v12\n"
"v_mul_f32 v55,v60,v11\n"
"v_sub_f32 v44,v44,v10\n"
"v_sub_f32 v45,v45,v10\n"
"v_sub_f32 v46,v46,v10\n"
"v_sub_f32 v47,v47,v10\n"
"v_sub_f32 v104,v104,v10\n"
"v_sub_f32 v105,v105,v10\n"
"v_sub_f32 v106,v106,v10\n"
"v_sub_f32 v107,v107,v10\n"
"v_sub_f32 v84,v84,v10\n"
"v_sub_f32 v85,v85,v10\n"
"v_sub_f32 v86,v86,v10\n"
"v_sub_f32 v87,v87,v10\n"
"v_sub_f32 v124,v124,v10\n"
"v_sub_f32 v125,v125,v10\n"
"v_sub_f32 v126,v126,v10\n"
"v_sub_f32 v127,v127,v10\n"
"v_exp_f32 v84,v84\n"
"v_exp_f32 v85,v85\n"
"v_exp_f32 v86,v86\n"
"v_exp_f32 v87,v87\n"
"v_exp_f32 v124,v124\n"
"v_exp_f32 v125,v125\n"
"v_exp_f32 v126,v126\n"
"v_exp_f32 v127,v127\n"
"v_add_f32 v55,v55,v84\n"
"v_add_f32 v55,v55,v85\n"
"v_add_f32 v55,v55,v86\n"
"v_add_f32 v55,v55,v87\n"
"v_add_f32 v55,v55,v124\n"
"v_add_f32 v55,v55,v125\n"
"v_add_f32 v55,v55,v126\n"
"v_add_f32 v55,v55,v127\n"
CV "v84,v84,v85\n"
CV "v85,v86,v87\n"
CV "v86,v124,v125\n"
CV "v87,v126,v127\n"
"v_max3_f32 v10,v88,v89,v90\n"
"v_max3_f32 v11,v91,v128,v129\n"
"v_max3_f32 v10,v10,v130,v131\n"
"v_max_f32 v10,v10,v11\n"
"v_mov_b32 v11,v10\n"
"s_nop 1\n"
"v_permlane16_swap_b32 v10,v11\n"
"v_max_f32 v10,v10,v11\n"
"v_mov_b32 v11,v10\n"
"s_nop 1\n"
"v_permlane32_swap_b32 v10,v11\n"
"v_max_f32 v10,v10,v11\n"
"v_add_f32 v10,0x41300000,v10\n"
"v_max_f32 v10,0,v10\n"
"v_exp_f32 v11,-v10\n"
AR "v12,a16\n"
"v_subrev_f32 v12,s13,v12\n"
"v_fma_f32 v12,v12,v11,s13\n"
AW "a16,v12\n"
AR "v12,a36\n"
"v_subrev_f32 v12,s13,v12\n"
"v_fma_f32 v12,v12,v11,s13\n"
AW "a36,v12\n"
AR "v12,a56\n"
"v_subrev_f32 v12,s13,v12\n"
"v_fma_f32 v12,v12,v11,s13\n"
AW "a56,v12\n"
AR "v12,a76\n"
"v_subrev_f32 v12,s13,v12\n"
"v_fma_f32 v12,v12,v11,s13\n"
AW "a76,v12\n"
AR "v12,a96\n"
"v_subrev_f32 v12,s13,v12\n"
"v_fma_f32 v12,v12,v11,s13\n"
AW "a96,v12\n"
AR "v12,a116\n"
"v_subrev_f32 v12,s13,v12\n"
"v_fma_f32 v12,v12,v11,s13\n"
AW "a116,v12\n"
AR "v12,a136\n"
"v_subrev_f32 v12,s13,v12\n"
"v_fma_f32 v12,v12,v11,s13\n"
AW "a136,v12\n"
AR "v12,a156\n"
"v_subrev_f32 v12,s13,v12\n"
"v_fma_f32 v12,v12,v11,s13\n"
AW "a156,v12\n"
AR "v12,a176\n"
"v_subrev_f32 v12,s13,v12\n"
"v_fma_f32 v12,v12,v11,s13\n"
AW "a176,v12\n"
AR "v12,a196\n"
"v_subrev_f32 v12,s13,v12\n"
"v_fma_f32 v12,v12,v11,s13\n"
AW "a196,v12\n"
AR "v12,a216\n"
"v_subrev_f32 v12,s13,v12\n"
"v_fma_f32 v12,v12,v11,s13\n"
AW "a216,v12\n"
AR "v12,a236\n"
"v_subrev_f32 v12,s13,v12\n"
"v_fma_f32 v12,v12,v11,s13\n"
AW "a236,v12\n"
AR "v12,a17\n"
"v_subrev_f32 v12,s13,v12\n"
"v_fma_f32 v12,v12,v11,s13\n"
AW "a17,v12\n"
AR "v12,a37\n"
"v_subrev_f32 v12,s13,v12\n"
"v_fma_f32 v12,v12,v11,s13\n"
AW "a37,v12\n"
AR "v12,a57\n"
"v_subrev_f32 v12,s13,v12\n"
"v_fma_f32 v12,v12,v11,s13\n"
AW "a57,v12\n"
AR "v12,a77\n"
"v_subrev_f32 v12,s13,v12\n"
"v_fma_f32 v12,v12,v11,s13\n"
AW "a77,v12\n"
AR "v12,a97\n"
"v_subrev_f32 v12,s13,v12\n"
"v_fma_f32 v12,v12,v11,s13\n"
AW "a97,v12\n"
AR "v12,a117\n"
"v_subrev_f32 v12,s13,v12\n"
"v_fma_f32 v12,v12,v11,s13\n"
AW "a117,v12\n"
AR "v12,a137\n"
"v_subrev_f32 v12,s13,v12\n"
"v_fma_f32 v12,v12,v11,s13\n"
AW "a137,v12\n"
AR "v12,a157\n"
"v_subrev_f32 v12,s13,v12\n"
"v_fma_f32 v12,v12,v11,s13\n"
AW "a157,v12\n"
AR "v12,a177\n"
"v_subrev_f32 v12,s13,v12\n"
"v_fma_f32 v12,v12,v11,s13\n"
AW "a177,v12\n"
AR "v12,a197\n"
"v_subrev_f32 v12,s13,v12\n"
"v_fma_f32 v12,v12,v11,s13\n"
AW "a197,v12\n"
AR "v12,a217\n"
"v_subrev_f32 v12,s13,v12\n"
"v_fma_f32 v12,v12,v11,s13\n"
AW "a217,v12\n"
AR "v12,a237\n"
"v_subrev_f32 v12,s13,v12\n"
"v_fma_f32 v12,v12,v11,s13\n"
AW "a237,v12\n"
AR "v12,a18\n"
"v_subrev_f32 v12,s13,v12\n"
"v_fma_f32 v12,v12,v11,s13\n"
AW "a18,v12\n"
AR "v12,a38\n"
"v_subrev_f32 v12,s13,v12\n"
"v_fma_f32 v12,v12,v11,s13\n"
AW "a38,v12\n"
AR "v12,a58\n"
"v_subrev_f32 v12,s13,v12\n"
"v_fma_f32 v12,v12,v11,s13\n"
AW "a58,v12\n"
AR "v12,a78\n"
"v_subrev_f32 v12,s13,v12\n"
"v_fma_f32 v12,v12,v11,s13\n"
AW "a78,v12\n"
AR "v12,a98\n"
"v_subrev_f32 v12,s13,v12\n"
"v_fma_f32 v12,v12,v11,s13\n"
AW "a98,v12\n"
AR "v12,a118\n"
"v_subrev_f32 v12,s13,v12\n"
"v_fma_f32 v12,v12,v11,s13\n"
AW "a118,v12\n"
AR "v12,a138\n"
"v_subrev_f32 v12,s13,v12\n"
"v_fma_f32 v12,v12,v11,s13\n"
AW "a138,v12\n"
AR "v12,a158\n"
"v_subrev_f32 v12,s13,v12\n"
"v_fma_f32 v12,v12,v11,s13\n"
AW "a158,v12\n"
AR "v12,a178\n"
"v_subrev_f32 v12,s13,v12\n"
"v_fma_f32 v12,v12,v11,s13\n"
AW "a178,v12\n"
AR "v12,a198\n"
"v_subrev_f32 v12,s13,v12\n"
"v_fma_f32 v12,v12,v11,s13\n"
AW "a198,v12\n"
AR "v12,a218\n"
"v_subrev_f32 v12,s13,v12\n"
"v_fma_f32 v12,v12,v11,s13\n"
AW "a218,v12\n"
AR "v12,a238\n"
"v_subrev_f32 v12,s13,v12\n"
"v_fma_f32 v12,v12,v11,s13\n"
AW "a238,v12\n"
AR "v12,a19\n"
"v_subrev_f32 v12,s13,v12\n"
"v_fma_f32 v12,v12,v11,s13\n"
AW "a19,v12\n"
AR "v12,a39\n"
"v_subrev_f32 v12,s13,v12\n"
"v_fma_f32 v12,v12,v11,s13\n"
AW "a39,v12\n"
AR "v12,a59\n"
"v_subrev_f32 v12,s13,v12\n"
"v_fma_f32 v12,v12,v11,s13\n"
AW "a59,v12\n"
AR "v12,a79\n"
"v_subrev_f32 v12,s13,v12\n"
"v_fma_f32 v12,v12,v11,s13\n"
AW "a79,v12\n"
AR "v12,a99\n"
"v_subrev_f32 v12,s13,v12\n"
"v_fma_f32 v12,v12,v11,s13\n"
AW "a99,v12\n"
AR "v12,a119\n"
"v_subrev_f32 v12,s13,v12\n"
"v_fma_f32 v12,v12,v11,s13\n"
AW "a119,v12\n"
AR "v12,a139\n"
"v_subrev_f32 v12,s13,v12\n"
"v_fma_f32 v12,v12,v11,s13\n"
AW "a139,v12\n"
AR "v12,a159\n"
"v_subrev_f32 v12,s13,v12\n"
"v_fma_f32 v12,v12,v11,s13\n"
AW "a159,v12\n"
AR "v12,a179\n"
"v_subrev_f32 v12,s13,v12\n"
"v_fma_f32 v12,v12,v11,s13\n"
AW "a179,v12\n"
AR "v12,a199\n"
"v_subrev_f32 v12,s13,v12\n"
"v_fma_f32 v12,v12,v11,s13\n"
AW "a199,v12\n"
AR "v12,a219\n"
"v_subrev_f32 v12,s13,v12\n"
"v_fma_f32 v12,v12,v11,s13\n"
AW "a219,v12\n"
AR "v12,a239\n"
"v_subrev_f32 v12,s13,v12\n"
"v_fma_f32 v12,v12,v11,s13\n"
AW "a239,v12\n"
"v_mul_f32 v56,v61,v11\n"
"v_sub_f32 v48,v48,v10\n"
"v_sub_f32 v49,v49,v10\n"
"v_sub_f32 v50,v50,v10\n"
"v_sub_f32 v51,v51,v10\n"
"v_sub_f32 v108,v108,v10\n"
"v_sub_f32 v109,v109,v10\n"
"v_sub_f32 v110,v110,v10\n"
"v_sub_f32 v111,v111,v10\n"
"v_sub_f32 v88,v88,v10\n"
"v_sub_f32 v89,v89,v10\n"
"v_sub_f32 v90,v90,v10\n"
"v_sub_f32 v91,v91,v10\n"
"v_sub_f32 v128,v128,v10\n"
"v_sub_f32 v129,v129,v10\n"
"v_sub_f32 v130,v130,v10\n"
"v_sub_f32 v131,v131,v10\n"
"v_exp_f32 v88,v88\n"
"v_exp_f32 v89,v89\n"
"v_exp_f32 v90,v90\n"
"v_exp_f32 v91,v91\n"
"v_exp_f32 v128,v128\n"
"v_exp_f32 v129,v129\n"
"v_exp_f32 v130,v130\n"
"v_exp_f32 v131,v131\n"
"v_add_f32 v56,v56,v88\n"
"v_add_f32 v56,v56,v89\n"
"v_add_f32 v56,v56,v90\n"
"v_add_f32 v56,v56,v91\n"
"v_add_f32 v56,v56,v128\n"
"v_add_f32 v56,v56,v129\n"
"v_add_f32 v56,v56,v130\n"
"v_add_f32_e64 v56,v56,v131\n"
CV "v88,v88,v89\n"
CV "v89,v90,v91\n"
CV "v90,v128,v129\n"
CV "v91,v130,v131\n"
"s_nop 7\n"
"s_setpc_b64 s[52:53]\n"
".Lslow_1:\n"
"s_nop 7\n"
"s_nop 7\n"
"v_and_b32 v10,240,v9\n"
"v_mul_u32_u24 v10,24,v10\n"
"v_lshrrev_b32 v11,4,v9\n"
"v_and_or_b32 v10,v11,48,v10\n"
"s_mov_b32 s59,s34\n"
"s_mov_b32 s34,s20\n"
"s_add_u32 s58,s74,0\n"
"s_nop 3\n"
LD "v[58:61],v10,s[32:35],s58 offen\n"
"s_waitcnt vmcnt(0)\n"
MF "v[92:95],v[58:61],v[136:139],v[32:35]\n"
MF "v[96:99],v[58:61],v[140:143],v[36:39]\n"
MF "v[100:103],v[58:61],v[144:147],v[40:43]\n"
MF "v[104:107],v[58:61],v[148:151],v[44:47]\n"
MF "v[108:111],v[58:61],v[152:155],v[48:51]\n"
"s_add_u32 s58,s74,0\n"
"s_nop 3\n"
LD "v[58:61],v10,s[32:35],s58 offen offset:64\n"
"s_waitcnt vmcnt(0)\n"
MF "v[92:95],v[58:61],v[156:159],v[92:95]\n"
MF "v[96:99],v[58:61],v[160:163],v[96:99]\n"
MF "v[100:103],v[58:61],v[164:167],v[100:103]\n"
MF "v[104:107],v[58:61],v[168:171],v[104:107]\n"
MF "v[108:111],v[58:61],v[172:175],v[108:111]\n"
"s_add_u32 s58,s74,0\n"
"s_nop 3\n"
LD "v[58:61],v10,s[32:35],s58 offen offset:128\n"
"s_waitcnt vmcnt(0)\n"
MF "v[92:95],v[58:61],v[176:179],v[92:95]\n"
MF "v[96:99],v[58:61],v[180:183],v[96:99]\n"
MF "v[100:103],v[58:61],v[184:187],v[100:103]\n"
MF "v[104:107],v[58:61],v[188:191],v[104:107]\n"
MF "v[108:111],v[58:61],v[192:195],v[108:111]\n"
"s_add_u32 s58,s74,0\n"
"s_nop 3\n"
LD "v[58:61],v10,s[32:35],s58 offen offset:192\n"
"s_waitcnt vmcnt(0)\n"
MF "v[92:95],v[58:61],v[196:199],v[92:95]\n"
MF "v[96:99],v[58:61],v[200:203],v[96:99]\n"
MF "v[100:103],v[58:61],v[204:207],v[100:103]\n"
MF "v[104:107],v[58:61],v[208:211],v[104:107]\n"
MF "v[108:111],v[58:61],v[212:215],v[108:111]\n"
"s_add_u32 s58,s74,0\n"
"s_nop 3\n"
LD "v[58:61],v10,s[32:35],s58 offen offset:256\n"
"s_waitcnt vmcnt(0)\n"
MF "v[92:95],v[58:61],v[216:219],v[92:95]\n"
MF "v[96:99],v[58:61],v[220:223],v[96:99]\n"
MF "v[100:103],v[58:61],v[224:227],v[100:103]\n"
MF "v[104:107],v[58:61],v[228:231],v[104:107]\n"
MF "v[108:111],v[58:61],v[232:235],v[108:111]\n"
"s_add_u32 s58,s74,0\n"
"s_nop 3\n"
LD "v[58:61],v10,s[32:35],s58 offen offset:320\n"
"s_waitcnt vmcnt(0)\n"
MF "v[92:95],v[58:61],v[236:239],v[92:95]\n"
MF "v[96:99],v[58:61],v[240:243],v[96:99]\n"
MF "v[100:103],v[58:61],v[244:247],v[100:103]\n"
MF "v[104:107],v[58:61],v[248:251],v[104:107]\n"
MF "v[108:111],v[58:61],v[252:255],v[108:111]\n"
"s_add_u32 s58,s74,6144\n"
"s_nop 3\n"
LD "v[58:61],v10,s[32:35],s58 offen\n"
"s_waitcnt vmcnt(0)\n"
MF "v[112:115],v[58:61],v[136:139],v[32:35]\n"
MF "v[116:119],v[58:61],v[140:143],v[36:39]\n"
MF "v[120:123],v[58:61],v[144:147],v[40:43]\n"
MF "v[124:127],v[58:61],v[148:151],v[44:47]\n"
MF "v[128:131],v[58:61],v[152:155],v[48:51]\n"
"s_add_u32 s58,s74,6144\n"
"s_nop 3\n"
LD "v[58:61],v10,s[32:35],s58 offen offset:64\n"
"s_waitcnt vmcnt(0)\n"
MF "v[112:115],v[58:61],v[156:159],v[112:115]\n"
MF "v[116:119],v[58:61],v[160:163],v[116:119]\n"
MF "v[120:123],v[58:61],v[164:167],v[120:123]\n"
MF "v[124:127],v[58:61],v[168:171],v[124:127]\n"
MF "v[128:131],v[58:61],v[172:175],v[128:131]\n"
"s_add_u32 s58,s74,6144\n"
"s_nop 3\n"
LD "v[58:61],v10,s[32:35],s58 offen offset:128\n"
"s_waitcnt vmcnt(0)\n"
MF "v[112:115],v[58:61],v[176:179],v[112:115]\n"
MF "v[116:119],v[58:61],v[180:183],v[116:119]\n"
MF "v[120:123],v[58:61],v[184:187],v[120:123]\n"
MF "v[124:127],v[58:61],v[188:191],v[124:127]\n"
MF "v[128:131],v[58:61],v[192:195],v[128:131]\n"
"s_add_u32 s58,s74,6144\n"
"s_nop 3\n"
LD "v[58:61],v10,s[32:35],s58 offen offset:192\n"
"s_waitcnt vmcnt(0)\n"
MF "v[112:115],v[58:61],v[196:199],v[112:115]\n"
MF "v[116:119],v[58:61],v[200:203],v[116:119]\n"
MF "v[120:123],v[58:61],v[204:207],v[120:123]\n"
MF "v[124:127],v[58:61],v[208:211],v[124:127]\n"
MF "v[128:131],v[58:61],v[212:215],v[128:131]\n"
"s_add_u32 s58,s74,6144\n"
"s_nop 3\n"
LD "v[58:61],v10,s[32:35],s58 offen offset:256\n"
"s_waitcnt vmcnt(0)\n"
MF "v[112:115],v[58:61],v[216:219],v[112:115]\n"
MF "v[116:119],v[58:61],v[220:223],v[116:119]\n"
MF "v[120:123],v[58:61],v[224:227],v[120:123]\n"
MF "v[124:127],v[58:61],v[228:231],v[124:127]\n"
MF "v[128:131],v[58:61],v[232:235],v[128:131]\n"
"s_add_u32 s58,s74,6144\n"
"s_nop 3\n"
LD "v[58:61],v10,s[32:35],s58 offen offset:320\n"
"s_waitcnt vmcnt(0)\n"
MF "v[112:115],v[58:61],v[236:239],v[112:115]\n"
MF "v[116:119],v[58:61],v[240:243],v[116:119]\n"
MF "v[120:123],v[58:61],v[244:247],v[120:123]\n"
MF "v[124:127],v[58:61],v[248:251],v[124:127]\n"
MF "v[128:131],v[58:61],v[252:255],v[128:131]\n"
"s_nop 7\n"
"s_mov_b32 s34,s59\n"
"v_max3_f32 v10,v92,v93,v94\n"
"s_nop 0\n"
"v_max3_f32 v11,v95,v112,v113\n"
"v_max3_f32 v10,v10,v114,v115\n"
"v_max_f32 v10,v10,v11\n"
"v_mov_b32 v11,v10\n"
"s_nop 1\n"
"v_permlane16_swap_b32 v10,v11\n"
"v_max_f32 v10,v10,v11\n"
"v_mov_b32 v11,v10\n"
"s_nop 1\n"
"v_permlane32_swap_b32 v10,v11\n"
"v_max_f32 v10,v10,v11\n"
"v_add_f32 v10,0x41300000,v10\n"
"v_max_f32_e64 v10,0,v10\n"
"v_exp_f32 v11,-v10\n"
AR "v12,a0\n"
"v_subrev_f32 v12,s13,v12\n"
"v_fma_f32 v12,v12,v11,s13\n"
AW "a0,v12\n"
AR "v12,a20\n"
"v_subrev_f32 v12,s13,v12\n"
"v_fma_f32 v12,v12,v11,s13\n"
AW "a20,v12\n"
AR "v12,a40\n"
"v_subrev_f32 v12,s13,v12\n"
"v_fma_f32 v12,v12,v11,s13\n"
AW "a40,v12\n"
AR "v12,a60\n"
"v_subrev_f32 v12,s13,v12\n"
"v_fma_f32 v12,v12,v11,s13\n"
AW "a60,v12\n"
AR "v12,a80\n"
"v_subrev_f32 v12,s13,v12\n"
"v_fma_f32 v12,v12,v11,s13\n"
AW "a80,v12\n"
AR "v12,a100\n"
"v_subrev_f32 v12,s13,v12\n"
"v_fma_f32 v12,v12,v11,s13\n"
AW "a100,v12\n"
AR "v12,a120\n"
"v_subrev_f32 v12,s13,v12\n"
"v_fma_f32 v12,v12,v11,s13\n"
AW "a120,v12\n"
AR "v12,a140\n"
"v_subrev_f32 v12,s13,v12\n"
"v_fma_f32 v12,v12,v11,s13\n"
AW "a140,v12\n"
AR "v12,a160\n"
"v_subrev_f32 v12,s13,v12\n"
"v_fma_f32 v12,v12,v11,s13\n"
AW "a160,v12\n"
AR "v12,a180\n"
"v_subrev_f32 v12,s13,v12\n"
"v_fma_f32 v12,v12,v11,s13\n"
AW "a180,v12\n"
AR "v12,a200\n"
"v_subrev_f32 v12,s13,v12\n"
"v_fma_f32 v12,v12,v11,s13\n"
AW "a200,v12\n"
AR "v12,a220\n"
"v_subrev_f32 v12,s13,v12\n"
"v_fma_f32 v12,v12,v11,s13\n"
AW "a220,v12\n"
AR "v12,a1\n"
"v_subrev_f32 v12,s13,v12\n"
"v_fma_f32 v12,v12,v11,s13\n"
AW "a1,v12\n"
AR "v12,a21\n"
"v_subrev_f32 v12,s13,v12\n"
"v_fma_f32 v12,v12,v11,s13\n"
AW "a21,v12\n"
AR "v12,a41\n"
"v_subrev_f32 v12,s13,v12\n"
"v_fma_f32 v12,v12,v11,s13\n"
AW "a41,v12\n"
AR "v12,a61\n"
"v_subrev_f32 v12,s13,v12\n"
"v_fma_f32 v12,v12,v11,s13\n"
AW "a61,v12\n"
AR "v12,a81\n"
"v_subrev_f32 v12,s13,v12\n"
"v_fma_f32 v12,v12,v11,s13\n"
AW "a81,v12\n"
AR "v12,a101\n"
"v_subrev_f32 v12,s13,v12\n"
"v_fma_f32 v12,v12,v11,s13\n"
AW "a101,v12\n"
AR "v12,a121\n"
"v_subrev_f32 v12,s13,v12\n"
"v_fma_f32 v12,v12,v11,s13\n"
AW "a121,v12\n"
AR "v12,a141\n"
"v_subrev_f32 v12,s13,v12\n"
"v_fma_f32 v12,v12,v11,s13\n"
AW "a141,v12\n"
AR "v12,a161\n"
"v_subrev_f32 v12,s13,v12\n"
"v_fma_f32 v12,v12,v11,s13\n"
AW "a161,v12\n"
AR "v12,a181\n"
"v_subrev_f32 v12,s13,v12\n"
"v_fma_f32 v12,v12,v11,s13\n"
AW "a181,v12\n"
AR "v12,a201\n"
"v_subrev_f32 v12,s13,v12\n"
"v_fma_f32 v12,v12,v11,s13\n"
AW "a201,v12\n"
AR "v12,a221\n"
"v_subrev_f32 v12,s13,v12\n"
"v_fma_f32 v12,v12,v11,s13\n"
AW "a221,v12\n"
AR "v12,a2\n"
"v_subrev_f32 v12,s13,v12\n"
"v_fma_f32 v12,v12,v11,s13\n"
AW "a2,v12\n"
AR "v12,a22\n"
"v_subrev_f32 v12,s13,v12\n"
"v_fma_f32 v12,v12,v11,s13\n"
AW "a22,v12\n"
AR "v12,a42\n"
"v_subrev_f32 v12,s13,v12\n"
"v_fma_f32 v12,v12,v11,s13\n"
AW "a42,v12\n"
AR "v12,a62\n"
"v_subrev_f32 v12,s13,v12\n"
"v_fma_f32 v12,v12,v11,s13\n"
AW "a62,v12\n"
AR "v12,a82\n"
"v_subrev_f32 v12,s13,v12\n"
"v_fma_f32 v12,v12,v11,s13\n"
AW "a82,v12\n"
AR "v12,a102\n"
"v_subrev_f32 v12,s13,v12\n"
"v_fma_f32 v12,v12,v11,s13\n"
AW "a102,v12\n"
AR "v12,a122\n"
"v_subrev_f32 v12,s13,v12\n"
"v_fma_f32 v12,v12,v11,s13\n"
AW "a122,v12\n"
AR "v12,a142\n"
"v_subrev_f32 v12,s13,v12\n"
"v_fma_f32 v12,v12,v11,s13\n"
AW "a142,v12\n"
AR "v12,a162\n"
"v_subrev_f32 v12,s13,v12\n"
"v_fma_f32 v12,v12,v11,s13\n"
AW "a162,v12\n"
AR "v12,a182\n"
"v_subrev_f32 v12,s13,v12\n"
"v_fma_f32 v12,v12,v11,s13\n"
AW "a182,v12\n"
AR "v12,a202\n"
"v_subrev_f32 v12,s13,v12\n"
"v_fma_f32 v12,v12,v11,s13\n"
AW "a202,v12\n"
AR "v12,a222\n"
"v_subrev_f32 v12,s13,v12\n"
"v_fma_f32 v12,v12,v11,s13\n"
AW "a222,v12\n"
AR "v12,a3\n"
"v_subrev_f32 v12,s13,v12\n"
"v_fma_f32 v12,v12,v11,s13\n"
AW "a3,v12\n"
AR "v12,a23\n"
"v_subrev_f32 v12,s13,v12\n"
"v_fma_f32 v12,v12,v11,s13\n"
AW "a23,v12\n"
AR "v12,a43\n"
"v_subrev_f32 v12,s13,v12\n"
"v_fma_f32 v12,v12,v11,s13\n"
AW "a43,v12\n"
AR "v12,a63\n"
"v_subrev_f32 v12,s13,v12\n"
"v_fma_f32 v12,v12,v11,s13\n"
AW "a63,v12\n"
AR "v12,a83\n"
"v_subrev_f32 v12,s13,v12\n"
"v_fma_f32 v12,v12,v11,s13\n"
AW "a83,v12\n"
AR "v12,a103\n"
"v_subrev_f32 v12,s13,v12\n"
"v_fma_f32 v12,v12,v11,s13\n"
AW "a103,v12\n"
AR "v12,a123\n"
"v_subrev_f32 v12,s13,v12\n"
"v_fma_f32 v12,v12,v11,s13\n"
AW "a123,v12\n"
AR "v12,a143\n"
"v_subrev_f32 v12,s13,v12\n"
"v_fma_f32 v12,v12,v11,s13\n"
AW "a143,v12\n"
AR "v12,a163\n"
"v_subrev_f32 v12,s13,v12\n"
"v_fma_f32 v12,v12,v11,s13\n"
AW "a163,v12\n"
AR "v12,a183\n"
"v_subrev_f32 v12,s13,v12\n"
"v_fma_f32 v12,v12,v11,s13\n"
AW "a183,v12\n"
AR "v12,a203\n"
"v_subrev_f32 v12,s13,v12\n"
"v_fma_f32 v12,v12,v11,s13\n"
AW "a203,v12\n"
AR "v12,a223\n"
"v_subrev_f32 v12,s13,v12\n"
"v_fma_f32 v12,v12,v11,s13\n"
AW "a223,v12\n"
"v_mul_f32 v57,v52,v11\n"
"v_sub_f32 v32,v32,v10\n"
"v_sub_f32 v33,v33,v10\n"
"v_sub_f32 v34,v34,v10\n"
"v_sub_f32 v35,v35,v10\n"
"v_sub_f32 v72,v72,v10\n"
"v_sub_f32 v73,v73,v10\n"
"v_sub_f32 v74,v74,v10\n"
"v_sub_f32 v75,v75,v10\n"
"v_sub_f32 v92,v92,v10\n"
"v_sub_f32 v93,v93,v10\n"
"v_sub_f32 v94,v94,v10\n"
"v_sub_f32 v95,v95,v10\n"
"v_sub_f32 v112,v112,v10\n"
"v_sub_f32 v113,v113,v10\n"
"v_sub_f32 v114,v114,v10\n"
"v_sub_f32 v115,v115,v10\n"
"v_exp_f32 v92,v92\n"
"v_exp_f32 v93,v93\n"
"v_exp_f32 v94,v94\n"
"v_exp_f32 v95,v95\n"
"v_exp_f32 v112,v112\n"
"v_exp_f32 v113,v113\n"
"v_exp_f32 v114,v114\n"
"v_exp_f32 v115,v115\n"
"v_add_f32 v57,v57,v92\n"
"v_add_f32 v57,v57,v93\n"
"v_add_f32 v57,v57,v94\n"
"v_add_f32 v57,v57,v95\n"
"v_add_f32 v57,v57,v112\n"
"v_add_f32 v57,v57,v113\n"
"v_add_f32 v57,v57,v114\n"
"v_add_f32_e64 v57,v57,v115\n"
CV "v92,v92,v93\n"
CV "v93,v94,v95\n"
CV "v94,v112,v113\n"
CV "v95,v114,v115\n"
"v_max3_f32 v10,v96,v97,v98\n"
"v_max3_f32 v11,v99,v116,v117\n"
"v_max3_f32 v10,v10,v118,v119\n"
"v_max_f32 v10,v10,v11\n"
"v_mov_b32 v11,v10\n"
"s_nop 1\n"
"v_permlane16_swap_b32 v10,v11\n"
"v_max_f32 v10,v10,v11\n"
"v_mov_b32 v11,v10\n"
"s_nop 1\n"
"v_permlane32_swap_b32 v10,v11\n"
"v_max_f32 v10,v10,v11\n"
"v_add_f32 v10,0x41300000,v10\n"
"v_max_f32 v10,0,v10\n"
"v_exp_f32 v11,-v10\n"
AR "v12,a4\n"
"v_subrev_f32 v12,s13,v12\n"
"v_fma_f32 v12,v12,v11,s13\n"
AW "a4,v12\n"
AR "v12,a24\n"
"v_subrev_f32 v12,s13,v12\n"
"v_fma_f32 v12,v12,v11,s13\n"
AW "a24,v12\n"
AR "v12,a44\n"
"v_subrev_f32 v12,s13,v12\n"
"v_fma_f32 v12,v12,v11,s13\n"
AW "a44,v12\n"
AR "v12,a64\n"
"v_subrev_f32 v12,s13,v12\n"
"v_fma_f32 v12,v12,v11,s13\n"
AW "a64,v12\n"
AR "v12,a84\n"
"v_subrev_f32 v12,s13,v12\n"
"v_fma_f32 v12,v12,v11,s13\n"
AW "a84,v12\n"
AR "v12,a104\n"
"v_subrev_f32 v12,s13,v12\n"
"v_fma_f32 v12,v12,v11,s13\n"
AW "a104,v12\n"
AR "v12,a124\n"
"v_subrev_f32 v12,s13,v12\n"
"v_fma_f32 v12,v12,v11,s13\n"
AW "a124,v12\n"
AR "v12,a144\n"
"v_subrev_f32 v12,s13,v12\n"
"v_fma_f32 v12,v12,v11,s13\n"
AW "a144,v12\n"
AR "v12,a164\n"
"v_subrev_f32 v12,s13,v12\n"
"v_fma_f32 v12,v12,v11,s13\n"
AW "a164,v12\n"
AR "v12,a184\n"
"v_subrev_f32 v12,s13,v12\n"
"v_fma_f32 v12,v12,v11,s13\n"
AW "a184,v12\n"
AR "v12,a204\n"
"v_subrev_f32 v12,s13,v12\n"
"v_fma_f32 v12,v12,v11,s13\n"
AW "a204,v12\n"
AR "v12,a224\n"
"v_subrev_f32 v12,s13,v12\n"
"v_fma_f32 v12,v12,v11,s13\n"
AW "a224,v12\n"
AR "v12,a5\n"
"v_subrev_f32 v12,s13,v12\n"
"v_fma_f32 v12,v12,v11,s13\n"
AW "a5,v12\n"
AR "v12,a25\n"
"v_subrev_f32 v12,s13,v12\n"
"v_fma_f32 v12,v12,v11,s13\n"
AW "a25,v12\n"
AR "v12,a45\n"
"v_subrev_f32 v12,s13,v12\n"
"v_fma_f32 v12,v12,v11,s13\n"
AW "a45,v12\n"
AR "v12,a65\n"
"v_subrev_f32 v12,s13,v12\n"
"v_fma_f32 v12,v12,v11,s13\n"
AW "a65,v12\n"
AR "v12,a85\n"
"v_subrev_f32 v12,s13,v12\n"
"v_fma_f32 v12,v12,v11,s13\n"
AW "a85,v12\n"
AR "v12,a105\n"
"v_subrev_f32 v12,s13,v12\n"
"v_fma_f32 v12,v12,v11,s13\n"
AW "a105,v12\n"
AR "v12,a125\n"
"v_subrev_f32 v12,s13,v12\n"
"v_fma_f32 v12,v12,v11,s13\n"
AW "a125,v12\n"
AR "v12,a145\n"
"v_subrev_f32 v12,s13,v12\n"
"v_fma_f32 v12,v12,v11,s13\n"
AW "a145,v12\n"
AR "v12,a165\n"
"v_subrev_f32 v12,s13,v12\n"
"v_fma_f32 v12,v12,v11,s13\n"
AW "a165,v12\n"
AR "v12,a185\n"
"v_subrev_f32 v12,s13,v12\n"
"v_fma_f32 v12,v12,v11,s13\n"
AW "a185,v12\n"
AR "v12,a205\n"
"v_subrev_f32 v12,s13,v12\n"
"v_fma_f32 v12,v12,v11,s13\n"
AW "a205,v12\n"
AR "v12,a225\n"
"v_subrev_f32 v12,s13,v12\n"
"v_fma_f32 v12,v12,v11,s13\n"
AW "a225,v12\n"
AR "v12,a6\n"
"v_subrev_f32 v12,s13,v12\n"
"v_fma_f32 v12,v12,v11,s13\n"
AW "a6,v12\n"
AR "v12,a26\n"
"v_subrev_f32 v12,s13,v12\n"
"v_fma_f32 v12,v12,v11,s13\n"
AW "a26,v12\n"
AR "v12,a46\n"
"v_subrev_f32 v12,s13,v12\n"
"v_fma_f32 v12,v12,v11,s13\n"
AW "a46,v12\n"
AR "v12,a66\n"
"v_subrev_f32 v12,s13,v12\n"
"v_fma_f32 v12,v12,v11,s13\n"
AW "a66,v12\n"
AR "v12,a86\n"
"v_subrev_f32 v12,s13,v12\n"
"v_fma_f32 v12,v12,v11,s13\n"
AW "a86,v12\n"
AR "v12,a106\n"
"v_subrev_f32 v12,s13,v12\n"
"v_fma_f32 v12,v12,v11,s13\n"
AW "a106,v12\n"
AR "v12,a126\n"
"v_subrev_f32 v12,s13,v12\n"
"v_fma_f32 v12,v12,v11,s13\n"
AW "a126,v12\n"
AR "v12,a146\n"
"v_subrev_f32 v12,s13,v12\n"
"v_fma_f32 v12,v12,v11,s13\n"
AW "a146,v12\n"
AR "v12,a166\n"
"v_subrev_f32 v12,s13,v12\n"
"v_fma_f32 v12,v12,v11,s13\n"
AW "a166,v12\n"
AR "v12,a186\n"
"v_subrev_f32 v12,s13,v12\n"
"v_fma_f32 v12,v12,v11,s13\n"
AW "a186,v12\n"
AR "v12,a206\n"
"v_subrev_f32 v12,s13,v12\n"
"v_fma_f32 v12,v12,v11,s13\n"
AW "a206,v12\n"
AR "v12,a226\n"
"v_subrev_f32 v12,s13,v12\n"
"v_fma_f32 v12,v12,v11,s13\n"
AW "a226,v12\n"
AR "v12,a7\n"
"v_subrev_f32 v12,s13,v12\n"
"v_fma_f32 v12,v12,v11,s13\n"
AW "a7,v12\n"
AR "v12,a27\n"
"v_subrev_f32 v12,s13,v12\n"
"v_fma_f32 v12,v12,v11,s13\n"
AW "a27,v12\n"
AR "v12,a47\n"
"v_subrev_f32 v12,s13,v12\n"
"v_fma_f32 v12,v12,v11,s13\n"
AW "a47,v12\n"
AR "v12,a67\n"
"v_subrev_f32 v12,s13,v12\n"
"v_fma_f32 v12,v12,v11,s13\n"
AW "a67,v12\n"
AR "v12,a87\n"
"v_subrev_f32 v12,s13,v12\n"
"v_fma_f32 v12,v12,v11,s13\n"
AW "a87,v12\n"
AR "v12,a107\n"
"v_subrev_f32 v12,s13,v12\n"
"v_fma_f32 v12,v12,v11,s13\n"
AW "a107,v12\n"
AR "v12,a127\n"
"v_subrev_f32 v12,s13,v12\n"
"v_fma_f32 v12,v12,v11,s13\n"
AW "a127,v12\n"
AR "v12,a147\n"
"v_subrev_f32 v12,s13,v12\n"
"v_fma_f32 v12,v12,v11,s13\n"
AW "a147,v12\n"
AR "v12,a167\n"
"v_subrev_f32 v12,s13,v12\n"
"v_fma_f32 v12,v12,v11,s13\n"
AW "a167,v12\n"
AR "v12,a187\n"
"v_subrev_f32 v12,s13,v12\n"
"v_fma_f32 v12,v12,v11,s13\n"
AW "a187,v12\n"
AR "v12,a207\n"
"v_subrev_f32 v12,s13,v12\n"
"v_fma_f32 v12,v12,v11,s13\n"
AW "a207,v12\n"
AR "v12,a227\n"
"v_subrev_f32 v12,s13,v12\n"
"v_fma_f32 v12,v12,v11,s13\n"
AW "a227,v12\n"
"v_mul_f32 v58,v53,v11\n"
"v_sub_f32 v36,v36,v10\n"
"v_sub_f32 v37,v37,v10\n"
"v_sub_f32 v38,v38,v10\n"
"v_sub_f32 v39,v39,v10\n"
"v_sub_f32 v76,v76,v10\n"
"v_sub_f32 v77,v77,v10\n"
"v_sub_f32 v78,v78,v10\n"
"v_sub_f32 v79,v79,v10\n"
"v_sub_f32 v96,v96,v10\n"
"v_sub_f32 v97,v97,v10\n"
"v_sub_f32 v98,v98,v10\n"
"v_sub_f32 v99,v99,v10\n"
"v_sub_f32 v116,v116,v10\n"
"v_sub_f32 v117,v117,v10\n"
"v_sub_f32 v118,v118,v10\n"
"v_sub_f32 v119,v119,v10\n"
"v_exp_f32 v96,v96\n"
"v_exp_f32 v97,v97\n"
"v_exp_f32 v98,v98\n"
"v_exp_f32 v99,v99\n"
"v_exp_f32 v116,v116\n"
"v_exp_f32 v117,v117\n"
"v_exp_f32 v118,v118\n"
"v_exp_f32 v119,v119\n"
"v_add_f32 v58,v58,v96\n"
"v_add_f32 v58,v58,v97\n"
"v_add_f32 v58,v58,v98\n"
"v_add_f32 v58,v58,v99\n"
"v_add_f32 v58,v58,v116\n"
"v_add_f32 v58,v58,v117\n"
"v_add_f32 v58,v58,v118\n"
"v_add_f32_e64 v58,v58,v119\n"
CV "v96,v96,v97\n"
CV "v97,v98,v99\n"
CV "v98,v116,v117\n"
CV "v99,v118,v119\n"
"v_max3_f32 v10,v100,v101,v102\n"
"v_max3_f32 v11,v103,v120,v121\n"
"v_max3_f32 v10,v10,v122,v123\n"
"v_max_f32 v10,v10,v11\n"
"v_mov_b32 v11,v10\n"
"s_nop 1\n"
"v_permlane16_swap_b32 v10,v11\n"
"v_max_f32 v10,v10,v11\n"
"v_mov_b32 v11,v10\n"
"s_nop 1\n"
"v_permlane32_swap_b32 v10,v11\n"
"v_max_f32 v10,v10,v11\n"
"v_add_f32 v10,0x41300000,v10\n"
"v_max_f32 v10,0,v10\n"
"v_exp_f32 v11,-v10\n"
AR "v12,a8\n"
"v_subrev_f32 v12,s13,v12\n"
"v_fma_f32 v12,v12,v11,s13\n"
AW "a8,v12\n"
AR "v12,a28\n"
"v_subrev_f32 v12,s13,v12\n"
"v_fma_f32 v12,v12,v11,s13\n"
AW "a28,v12\n"
AR "v12,a48\n"
"v_subrev_f32 v12,s13,v12\n"
"v_fma_f32 v12,v12,v11,s13\n"
AW "a48,v12\n"
AR "v12,a68\n"
"v_subrev_f32 v12,s13,v12\n"
"v_fma_f32 v12,v12,v11,s13\n"
AW "a68,v12\n"
AR "v12,a88\n"
"v_subrev_f32 v12,s13,v12\n"
"v_fma_f32 v12,v12,v11,s13\n"
AW "a88,v12\n"
AR "v12,a108\n"
"v_subrev_f32 v12,s13,v12\n"
"v_fma_f32 v12,v12,v11,s13\n"
AW "a108,v12\n"
AR "v12,a128\n"
"v_subrev_f32 v12,s13,v12\n"
"v_fma_f32 v12,v12,v11,s13\n"
AW "a128,v12\n"
AR "v12,a148\n"
"v_subrev_f32 v12,s13,v12\n"
"v_fma_f32 v12,v12,v11,s13\n"
AW "a148,v12\n"
AR "v12,a168\n"
"v_subrev_f32 v12,s13,v12\n"
"v_fma_f32 v12,v12,v11,s13\n"
AW "a168,v12\n"
AR "v12,a188\n"
"v_subrev_f32 v12,s13,v12\n"
"v_fma_f32 v12,v12,v11,s13\n"
AW "a188,v12\n"
AR "v12,a208\n"
"v_subrev_f32 v12,s13,v12\n"
"v_fma_f32 v12,v12,v11,s13\n"
AW "a208,v12\n"
AR "v12,a228\n"
"v_subrev_f32 v12,s13,v12\n"
"v_fma_f32 v12,v12,v11,s13\n"
AW "a228,v12\n"
AR "v12,a9\n"
"v_subrev_f32 v12,s13,v12\n"
"v_fma_f32 v12,v12,v11,s13\n"
AW "a9,v12\n"
AR "v12,a29\n"
"v_subrev_f32 v12,s13,v12\n"
"v_fma_f32 v12,v12,v11,s13\n"
AW "a29,v12\n"
AR "v12,a49\n"
"v_subrev_f32 v12,s13,v12\n"
"v_fma_f32 v12,v12,v11,s13\n"
AW "a49,v12\n"
AR "v12,a69\n"
"v_subrev_f32 v12,s13,v12\n"
"v_fma_f32 v12,v12,v11,s13\n"
AW "a69,v12\n"
AR "v12,a89\n"
"v_subrev_f32 v12,s13,v12\n"
"v_fma_f32 v12,v12,v11,s13\n"
AW "a89,v12\n"
AR "v12,a109\n"
"v_subrev_f32 v12,s13,v12\n"
"v_fma_f32 v12,v12,v11,s13\n"
AW "a109,v12\n"
AR "v12,a129\n"
"v_subrev_f32 v12,s13,v12\n"
"v_fma_f32 v12,v12,v11,s13\n"
AW "a129,v12\n"
AR "v12,a149\n"
"v_subrev_f32 v12,s13,v12\n"
"v_fma_f32 v12,v12,v11,s13\n"
AW "a149,v12\n"
AR "v12,a169\n"
"v_subrev_f32 v12,s13,v12\n"
"v_fma_f32 v12,v12,v11,s13\n"
AW "a169,v12\n"
AR "v12,a189\n"
"v_subrev_f32 v12,s13,v12\n"
"v_fma_f32 v12,v12,v11,s13\n"
AW "a189,v12\n"
AR "v12,a209\n"
"v_subrev_f32 v12,s13,v12\n"
"v_fma_f32 v12,v12,v11,s13\n"
AW "a209,v12\n"
AR "v12,a229\n"
"v_subrev_f32 v12,s13,v12\n"
"v_fma_f32 v12,v12,v11,s13\n"
AW "a229,v12\n"
AR "v12,a10\n"
"v_subrev_f32 v12,s13,v12\n"
"v_fma_f32 v12,v12,v11,s13\n"
AW "a10,v12\n"
AR "v12,a30\n"
"v_subrev_f32 v12,s13,v12\n"
"v_fma_f32 v12,v12,v11,s13\n"
AW "a30,v12\n"
AR "v12,a50\n"
"v_subrev_f32 v12,s13,v12\n"
"v_fma_f32 v12,v12,v11,s13\n"
AW "a50,v12\n"
AR "v12,a70\n"
"v_subrev_f32 v12,s13,v12\n"
"v_fma_f32 v12,v12,v11,s13\n"
AW "a70,v12\n"
AR "v12,a90\n"
"v_subrev_f32 v12,s13,v12\n"
"v_fma_f32 v12,v12,v11,s13\n"
AW "a90,v12\n"
AR "v12,a110\n"
"v_subrev_f32 v12,s13,v12\n"
"v_fma_f32 v12,v12,v11,s13\n"
AW "a110,v12\n"
AR "v12,a130\n"
"v_subrev_f32 v12,s13,v12\n"
"v_fma_f32 v12,v12,v11,s13\n"
AW "a130,v12\n"
AR "v12,a150\n"
"v_subrev_f32 v12,s13,v12\n"
"v_fma_f32 v12,v12,v11,s13\n"
AW "a150,v12\n"
AR "v12,a170\n"
"v_subrev_f32 v12,s13,v12\n"
"v_fma_f32 v12,v12,v11,s13\n"
AW "a170,v12\n"
AR "v12,a190\n"
"v_subrev_f32 v12,s13,v12\n"
"v_fma_f32 v12,v12,v11,s13\n"
AW "a190,v12\n"
AR "v12,a210\n"
"v_subrev_f32 v12,s13,v12\n"
"v_fma_f32 v12,v12,v11,s13\n"
AW "a210,v12\n"
AR "v12,a230\n"
"v_subrev_f32 v12,s13,v12\n"
"v_fma_f32 v12,v12,v11,s13\n"
AW "a230,v12\n"
AR "v12,a11\n"
"v_subrev_f32 v12,s13,v12\n"
"v_fma_f32 v12,v12,v11,s13\n"
AW "a11,v12\n"
AR "v12,a31\n"
"v_subrev_f32 v12,s13,v12\n"
"v_fma_f32 v12,v12,v11,s13\n"
AW "a31,v12\n"
AR "v12,a51\n"
"v_subrev_f32 v12,s13,v12\n"
"v_fma_f32 v12,v12,v11,s13\n"
AW "a51,v12\n"
AR "v12,a71\n"
"v_subrev_f32 v12,s13,v12\n"
"v_fma_f32 v12,v12,v11,s13\n"
AW "a71,v12\n"
AR "v12,a91\n"
"v_subrev_f32 v12,s13,v12\n"
"v_fma_f32 v12,v12,v11,s13\n"
AW "a91,v12\n"
AR "v12,a111\n"
"v_subrev_f32 v12,s13,v12\n"
"v_fma_f32 v12,v12,v11,s13\n"
AW "a111,v12\n"
AR "v12,a131\n"
"v_subrev_f32 v12,s13,v12\n"
"v_fma_f32 v12,v12,v11,s13\n"
AW "a131,v12\n"
AR "v12,a151\n"
"v_subrev_f32 v12,s13,v12\n"
"v_fma_f32 v12,v12,v11,s13\n"
AW "a151,v12\n"
AR "v12,a171\n"
"v_subrev_f32 v12,s13,v12\n"
"v_fma_f32 v12,v12,v11,s13\n"
AW "a171,v12\n"
AR "v12,a191\n"
"v_subrev_f32 v12,s13,v12\n"
"v_fma_f32 v12,v12,v11,s13\n"
AW "a191,v12\n"
AR "v12,a211\n"
"v_subrev_f32 v12,s13,v12\n"
"v_fma_f32 v12,v12,v11,s13\n"
AW "a211,v12\n"
AR "v12,a231\n"
"v_subrev_f32 v12,s13,v12\n"
"v_fma_f32 v12,v12,v11,s13\n"
AW "a231,v12\n"
"v_mul_f32 v59,v54,v11\n"
"v_sub_f32 v40,v40,v10\n"
"v_sub_f32 v41,v41,v10\n"
"v_sub_f32 v42,v42,v10\n"
"v_sub_f32 v43,v43,v10\n"
"v_sub_f32 v80,v80,v10\n"
"v_sub_f32 v81,v81,v10\n"
"v_sub_f32 v82,v82,v10\n"
"v_sub_f32 v83,v83,v10\n"
"v_sub_f32 v100,v100,v10\n"
"v_sub_f32 v101,v101,v10\n"
"v_sub_f32 v102,v102,v10\n"
"v_sub_f32 v103,v103,v10\n"
"v_sub_f32 v120,v120,v10\n"
"v_sub_f32 v121,v121,v10\n"
"v_sub_f32 v122,v122,v10\n"
"v_sub_f32 v123,v123,v10\n"
"v_exp_f32 v100,v100\n"
"v_exp_f32 v101,v101\n"
"v_exp_f32 v102,v102\n"
"v_exp_f32 v103,v103\n"
"v_exp_f32 v120,v120\n"
"v_exp_f32 v121,v121\n"
"v_exp_f32 v122,v122\n"
"v_exp_f32 v123,v123\n"
"v_add_f32 v59,v59,v100\n"
"v_add_f32 v59,v59,v101\n"
"v_add_f32 v59,v59,v102\n"
"v_add_f32 v59,v59,v103\n"
"v_add_f32 v59,v59,v120\n"
"v_add_f32 v59,v59,v121\n"
"v_add_f32 v59,v59,v122\n"
"v_add_f32_e64 v59,v59,v123\n"
CV "v100,v100,v101\n"
CV "v101,v102,v103\n"
CV "v102,v120,v121\n"
CV "v103,v122,v123\n"
"v_max3_f32 v10,v104,v105,v106\n"
"v_max3_f32 v11,v107,v124,v125\n"
"v_max3_f32 v10,v10,v126,v127\n"
"v_max_f32 v10,v10,v11\n"
"v_mov_b32 v11,v10\n"
"s_nop 1\n"
"v_permlane16_swap_b32 v10,v11\n"
"v_max_f32 v10,v10,v11\n"
"v_mov_b32 v11,v10\n"
"s_nop 1\n"
"v_permlane32_swap_b32 v10,v11\n"
"v_max_f32_e64 v10,v10,v11\n"
"v_add_f32 v10,0x41300000,v10\n"
"v_max_f32 v10,0,v10\n"
"v_exp_f32 v11,-v10\n"
AR "v12,a12\n"
"v_subrev_f32 v12,s13,v12\n"
"v_fma_f32 v12,v12,v11,s13\n"
AW "a12,v12\n"
AR "v12,a32\n"
"v_subrev_f32 v12,s13,v12\n"
"v_fma_f32 v12,v12,v11,s13\n"
AW "a32,v12\n"
AR "v12,a52\n"
"v_subrev_f32 v12,s13,v12\n"
"v_fma_f32 v12,v12,v11,s13\n"
AW "a52,v12\n"
AR "v12,a72\n"
"v_subrev_f32 v12,s13,v12\n"
"v_fma_f32 v12,v12,v11,s13\n"
AW "a72,v12\n"
AR "v12,a92\n"
"v_subrev_f32 v12,s13,v12\n"
"v_fma_f32 v12,v12,v11,s13\n"
AW "a92,v12\n"
AR "v12,a112\n"
"v_subrev_f32 v12,s13,v12\n"
"v_fma_f32 v12,v12,v11,s13\n"
AW "a112,v12\n"
AR "v12,a132\n"
"v_subrev_f32 v12,s13,v12\n"
"v_fma_f32 v12,v12,v11,s13\n"
AW "a132,v12\n"
AR "v12,a152\n"
"v_subrev_f32 v12,s13,v12\n"
"v_fma_f32 v12,v12,v11,s13\n"
AW "a152,v12\n"
AR "v12,a172\n"
"v_subrev_f32 v12,s13,v12\n"
"v_fma_f32 v12,v12,v11,s13\n"
AW "a172,v12\n"
AR "v12,a192\n"
"v_subrev_f32 v12,s13,v12\n"
"v_fma_f32 v12,v12,v11,s13\n"
AW "a192,v12\n"
AR "v12,a212\n"
"v_subrev_f32 v12,s13,v12\n"
"v_fma_f32 v12,v12,v11,s13\n"
AW "a212,v12\n"
AR "v12,a232\n"
"v_subrev_f32 v12,s13,v12\n"
"v_fma_f32 v12,v12,v11,s13\n"
AW "a232,v12\n"
AR "v12,a13\n"
"v_subrev_f32 v12,s13,v12\n"
"v_fma_f32 v12,v12,v11,s13\n"
AW "a13,v12\n"
AR "v12,a33\n"
"v_subrev_f32 v12,s13,v12\n"
"v_fma_f32 v12,v12,v11,s13\n"
AW "a33,v12\n"
AR "v12,a53\n"
"v_subrev_f32 v12,s13,v12\n"
"v_fma_f32 v12,v12,v11,s13\n"
AW "a53,v12\n"
AR "v12,a73\n"
"v_subrev_f32 v12,s13,v12\n"
"v_fma_f32 v12,v12,v11,s13\n"
AW "a73,v12\n"
AR "v12,a93\n"
"v_subrev_f32 v12,s13,v12\n"
"v_fma_f32 v12,v12,v11,s13\n"
AW "a93,v12\n"
AR "v12,a113\n"
"v_subrev_f32 v12,s13,v12\n"
"v_fma_f32 v12,v12,v11,s13\n"
AW "a113,v12\n"
AR "v12,a133\n"
"v_subrev_f32 v12,s13,v12\n"
"v_fma_f32 v12,v12,v11,s13\n"
AW "a133,v12\n"
AR "v12,a153\n"
"v_subrev_f32 v12,s13,v12\n"
"v_fma_f32 v12,v12,v11,s13\n"
AW "a153,v12\n"
AR "v12,a173\n"
"v_subrev_f32 v12,s13,v12\n"
"v_fma_f32 v12,v12,v11,s13\n"
AW "a173,v12\n"
AR "v12,a193\n"
"v_subrev_f32 v12,s13,v12\n"
"v_fma_f32 v12,v12,v11,s13\n"
AW "a193,v12\n"
AR "v12,a213\n"
"v_subrev_f32 v12,s13,v12\n"
"v_fma_f32 v12,v12,v11,s13\n"
AW "a213,v12\n"
AR "v12,a233\n"
"v_subrev_f32 v12,s13,v12\n"
"v_fma_f32 v12,v12,v11,s13\n"
AW "a233,v12\n"
AR "v12,a14\n"
"v_subrev_f32 v12,s13,v12\n"
"v_fma_f32 v12,v12,v11,s13\n"
AW "a14,v12\n"
AR "v12,a34\n"
"v_subrev_f32 v12,s13,v12\n"
"v_fma_f32 v12,v12,v11,s13\n"
AW "a34,v12\n"
AR "v12,a54\n"
"v_subrev_f32 v12,s13,v12\n"
"v_fma_f32 v12,v12,v11,s13\n"
AW "a54,v12\n"
AR "v12,a74\n"
"v_subrev_f32 v12,s13,v12\n"
"v_fma_f32 v12,v12,v11,s13\n"
AW "a74,v12\n"
AR "v12,a94\n"
"v_subrev_f32 v12,s13,v12\n"
"v_fma_f32 v12,v12,v11,s13\n"
AW "a94,v12\n"
AR "v12,a114\n"
"v_subrev_f32 v12,s13,v12\n"
"v_fma_f32 v12,v12,v11,s13\n"
AW "a114,v12\n"
AR "v12,a134\n"
"v_subrev_f32 v12,s13,v12\n"
"v_fma_f32 v12,v12,v11,s13\n"
AW "a134,v12\n"
AR "v12,a154\n"
"v_subrev_f32 v12,s13,v12\n"
"v_fma_f32 v12,v12,v11,s13\n"
AW "a154,v12\n"
AR "v12,a174\n"
"v_subrev_f32 v12,s13,v12\n"
"v_fma_f32 v12,v12,v11,s13\n"
AW "a174,v12\n"
AR "v12,a194\n"
"v_subrev_f32 v12,s13,v12\n"
"v_fma_f32 v12,v12,v11,s13\n"
AW "a194,v12\n"
AR "v12,a214\n"
"v_subrev_f32 v12,s13,v12\n"
"v_fma_f32 v12,v12,v11,s13\n"
AW "a214,v12\n"
AR "v12,a234\n"
"v_subrev_f32 v12,s13,v12\n"
"v_fma_f32 v12,v12,v11,s13\n"
AW "a234,v12\n"
AR "v12,a15\n"
"v_subrev_f32 v12,s13,v12\n"
"v_fma_f32 v12,v12,v11,s13\n"
AW "a15,v12\n"
AR "v12,a35\n"
"v_subrev_f32 v12,s13,v12\n"
"v_fma_f32 v12,v12,v11,s13\n"
AW "a35,v12\n"
AR "v12,a55\n"
"v_subrev_f32 v12,s13,v12\n"
"v_fma_f32 v12,v12,v11,s13\n"
AW "a55,v12\n"
AR "v12,a75\n"
"v_subrev_f32 v12,s13,v12\n"
"v_fma_f32 v12,v12,v11,s13\n"
AW "a75,v12\n"
AR "v12,a95\n"
"v_subrev_f32 v12,s13,v12\n"
"v_fma_f32 v12,v12,v11,s13\n"
AW "a95,v12\n"
AR "v12,a115\n"
"v_subrev_f32 v12,s13,v12\n"
"v_fma_f32 v12,v12,v11,s13\n"
AW "a115,v12\n"
AR "v12,a135\n"
"v_subrev_f32 v12,s13,v12\n"
"v_fma_f32 v12,v12,v11,s13\n"
AW "a135,v12\n"
AR "v12,a155\n"
"v_subrev_f32 v12,s13,v12\n"
"v_fma_f32 v12,v12,v11,s13\n"
AW "a155,v12\n"
AR "v12,a175\n"
"v_subrev_f32 v12,s13,v12\n"
"v_fma_f32 v12,v12,v11,s13\n"
AW "a175,v12\n"
AR "v12,a195\n"
"v_subrev_f32 v12,s13,v12\n"
"v_fma_f32 v12,v12,v11,s13\n"
AW "a195,v12\n"
AR "v12,a215\n"
"v_subrev_f32 v12,s13,v12\n"
"v_fma_f32 v12,v12,v11,s13\n"
AW "a215,v12\n"
AR "v12,a235\n"
"v_subrev_f32 v12,s13,v12\n"
"v_fma_f32 v12,v12,v11,s13\n"
AW "a235,v12\n"
"v_mul_f32 v60,v55,v11\n"
"v_sub_f32 v44,v44,v10\n"
"v_sub_f32 v45,v45,v10\n"
"v_sub_f32 v46,v46,v10\n"
"v_sub_f32 v47,v47,v10\n"
"v_sub_f32 v84,v84,v10\n"
"v_sub_f32 v85,v85,v10\n"
"v_sub_f32 v86,v86,v10\n"
"v_sub_f32 v87,v87,v10\n"
"v_sub_f32 v104,v104,v10\n"
"v_sub_f32 v105,v105,v10\n"
"v_sub_f32 v106,v106,v10\n"
"v_sub_f32 v107,v107,v10\n"
"v_sub_f32 v124,v124,v10\n"
"v_sub_f32 v125,v125,v10\n"
"v_sub_f32 v126,v126,v10\n"
"v_sub_f32 v127,v127,v10\n"
"v_exp_f32 v104,v104\n"
"v_exp_f32 v105,v105\n"
"v_exp_f32 v106,v106\n"
"v_exp_f32 v107,v107\n"
"v_exp_f32 v124,v124\n"
"v_exp_f32 v125,v125\n"
"v_exp_f32 v126,v126\n"
"v_exp_f32 v127,v127\n"
"v_add_f32 v60,v60,v104\n"
"v_add_f32 v60,v60,v105\n"
"v_add_f32 v60,v60,v106\n"
"v_add_f32 v60,v60,v107\n"
"v_add_f32 v60,v60,v124\n"
"v_add_f32 v60,v60,v125\n"
"v_add_f32 v60,v60,v126\n"
"v_add_f32 v60,v60,v127\n"
CV "v104,v104,v105\n"
CV "v105,v106,v107\n"
CV "v106,v124,v125\n"
CV "v107,v126,v127\n"
"v_max3_f32 v10,v108,v109,v110\n"
"v_max3_f32 v11,v111,v128,v129\n"
"v_max3_f32 v10,v10,v130,v131\n"
"v_max_f32 v10,v10,v11\n"
"v_mov_b32 v11,v10\n"
"s_nop 1\n"
"v_permlane16_swap_b32 v10,v11\n"
"v_max_f32 v10,v10,v11\n"
"v_mov_b32 v11,v10\n"
"s_nop 1\n"
"v_permlane32_swap_b32 v10,v11\n"
"v_max_f32 v10,v10,v11\n"
"v_add_f32 v10,0x41300000,v10\n"
"v_max_f32 v10,0,v10\n"
"v_exp_f32 v11,-v10\n"
AR "v12,a16\n"
"v_subrev_f32 v12,s13,v12\n"
"v_fma_f32 v12,v12,v11,s13\n"
AW "a16,v12\n"
AR "v12,a36\n"
"v_subrev_f32 v12,s13,v12\n"
"v_fma_f32 v12,v12,v11,s13\n"
AW "a36,v12\n"
AR "v12,a56\n"
"v_subrev_f32 v12,s13,v12\n"
"v_fma_f32 v12,v12,v11,s13\n"
AW "a56,v12\n"
AR "v12,a76\n"
"v_subrev_f32 v12,s13,v12\n"
"v_fma_f32 v12,v12,v11,s13\n"
AW "a76,v12\n"
AR "v12,a96\n"
"v_subrev_f32 v12,s13,v12\n"
"v_fma_f32 v12,v12,v11,s13\n"
AW "a96,v12\n"
AR "v12,a116\n"
"v_subrev_f32 v12,s13,v12\n"
"v_fma_f32 v12,v12,v11,s13\n"
AW "a116,v12\n"
AR "v12,a136\n"
"v_subrev_f32 v12,s13,v12\n"
"v_fma_f32 v12,v12,v11,s13\n"
AW "a136,v12\n"
AR "v12,a156\n"
"v_subrev_f32 v12,s13,v12\n"
"v_fma_f32 v12,v12,v11,s13\n"
AW "a156,v12\n"
AR "v12,a176\n"
"v_subrev_f32 v12,s13,v12\n"
"v_fma_f32 v12,v12,v11,s13\n"
AW "a176,v12\n"
AR "v12,a196\n"
"v_subrev_f32 v12,s13,v12\n"
"v_fma_f32 v12,v12,v11,s13\n"
AW "a196,v12\n"
AR "v12,a216\n"
"v_subrev_f32 v12,s13,v12\n"
"v_fma_f32 v12,v12,v11,s13\n"
AW "a216,v12\n"
AR "v12,a236\n"
"v_subrev_f32 v12,s13,v12\n"
"v_fma_f32 v12,v12,v11,s13\n"
AW "a236,v12\n"
AR "v12,a17\n"
"v_subrev_f32 v12,s13,v12\n"
"v_fma_f32 v12,v12,v11,s13\n"
AW "a17,v12\n"
AR "v12,a37\n"
"v_subrev_f32 v12,s13,v12\n"
"v_fma_f32 v12,v12,v11,s13\n"
AW "a37,v12\n"
AR "v12,a57\n"
"v_subrev_f32 v12,s13,v12\n"
"v_fma_f32 v12,v12,v11,s13\n"
AW "a57,v12\n"
AR "v12,a77\n"
"v_subrev_f32 v12,s13,v12\n"
"v_fma_f32 v12,v12,v11,s13\n"
AW "a77,v12\n"
AR "v12,a97\n"
"v_subrev_f32 v12,s13,v12\n"
"v_fma_f32 v12,v12,v11,s13\n"
AW "a97,v12\n"
AR "v12,a117\n"
"v_subrev_f32 v12,s13,v12\n"
"v_fma_f32 v12,v12,v11,s13\n"
AW "a117,v12\n"
AR "v12,a137\n"
"v_subrev_f32 v12,s13,v12\n"
"v_fma_f32 v12,v12,v11,s13\n"
AW "a137,v12\n"
AR "v12,a157\n"
"v_subrev_f32 v12,s13,v12\n"
"v_fma_f32 v12,v12,v11,s13\n"
AW "a157,v12\n"
AR "v12,a177\n"
"v_subrev_f32 v12,s13,v12\n"
"v_fma_f32 v12,v12,v11,s13\n"
AW "a177,v12\n"
AR "v12,a197\n"
"v_subrev_f32 v12,s13,v12\n"
"v_fma_f32 v12,v12,v11,s13\n"
AW "a197,v12\n"
AR "v12,a217\n"
"v_subrev_f32 v12,s13,v12\n"
"v_fma_f32 v12,v12,v11,s13\n"
AW "a217,v12\n"
AR "v12,a237\n"
"v_subrev_f32 v12,s13,v12\n"
"v_fma_f32 v12,v12,v11,s13\n"
AW "a237,v12\n"
AR "v12,a18\n"
"v_subrev_f32 v12,s13,v12\n"
"v_fma_f32 v12,v12,v11,s13\n"
AW "a18,v12\n"
AR "v12,a38\n"
"v_subrev_f32 v12,s13,v12\n"
"v_fma_f32 v12,v12,v11,s13\n"
AW "a38,v12\n"
AR "v12,a58\n"
"v_subrev_f32 v12,s13,v12\n"
"v_fma_f32 v12,v12,v11,s13\n"
AW "a58,v12\n"
AR "v12,a78\n"
"v_subrev_f32 v12,s13,v12\n"
"v_fma_f32 v12,v12,v11,s13\n"
AW "a78,v12\n"
AR "v12,a98\n"
"v_subrev_f32 v12,s13,v12\n"
"v_fma_f32 v12,v12,v11,s13\n"
AW "a98,v12\n"
AR "v12,a118\n"
"v_subrev_f32 v12,s13,v12\n"
"v_fma_f32 v12,v12,v11,s13\n"
AW "a118,v12\n"
AR "v12,a138\n"
"v_subrev_f32 v12,s13,v12\n"
"v_fma_f32 v12,v12,v11,s13\n"
AW "a138,v12\n"
AR "v12,a158\n"
"v_subrev_f32 v12,s13,v12\n"
"v_fma_f32 v12,v12,v11,s13\n"
AW "a158,v12\n"
AR "v12,a178\n"
"v_subrev_f32 v12,s13,v12\n"
"v_fma_f32 v12,v12,v11,s13\n"
AW "a178,v12\n"
AR "v12,a198\n"
"v_subrev_f32 v12,s13,v12\n"
"v_fma_f32 v12,v12,v11,s13\n"
AW "a198,v12\n"
AR "v12,a218\n"
"v_subrev_f32 v12,s13,v12\n"
"v_fma_f32 v12,v12,v11,s13\n"
AW "a218,v12\n"
AR "v12,a238\n"
"v_subrev_f32 v12,s13,v12\n"
"v_fma_f32 v12,v12,v11,s13\n"
AW "a238,v12\n"
AR "v12,a19\n"
"v_subrev_f32 v12,s13,v12\n"
"v_fma_f32 v12,v12,v11,s13\n"
AW "a19,v12\n"
AR "v12,a39\n"
"v_subrev_f32 v12,s13,v12\n"
"v_fma_f32 v12,v12,v11,s13\n"
AW "a39,v12\n"
AR "v12,a59\n"
"v_subrev_f32 v12,s13,v12\n"
"v_fma_f32 v12,v12,v11,s13\n"
AW "a59,v12\n"
AR "v12,a79\n"
"v_subrev_f32 v12,s13,v12\n"
"v_fma_f32 v12,v12,v11,s13\n"
AW "a79,v12\n"
AR "v12,a99\n"
"v_subrev_f32 v12,s13,v12\n"
"v_fma_f32 v12,v12,v11,s13\n"
AW "a99,v12\n"
AR "v12,a119\n"
"v_subrev_f32 v12,s13,v12\n"
"v_fma_f32 v12,v12,v11,s13\n"
AW "a119,v12\n"
AR "v12,a139\n"
"v_subrev_f32 v12,s13,v12\n"
"v_fma_f32 v12,v12,v11,s13\n"
AW "a139,v12\n"
AR "v12,a159\n"
"v_subrev_f32 v12,s13,v12\n"
"v_fma_f32 v12,v12,v11,s13\n"
AW "a159,v12\n"
AR "v12,a179\n"
"v_subrev_f32 v12,s13,v12\n"
"v_fma_f32 v12,v12,v11,s13\n"
AW "a179,v12\n"
AR "v12,a199\n"
"v_subrev_f32 v12,s13,v12\n"
"v_fma_f32 v12,v12,v11,s13\n"
AW "a199,v12\n"
AR "v12,a219\n"
"v_subrev_f32 v12,s13,v12\n"
"v_fma_f32 v12,v12,v11,s13\n"
AW "a219,v12\n"
AR "v12,a239\n"
"v_subrev_f32 v12,s13,v12\n"
"v_fma_f32 v12,v12,v11,s13\n"
AW "a239,v12\n"
"v_mul_f32 v61,v56,v11\n"
"v_sub_f32 v48,v48,v10\n"
"v_sub_f32 v49,v49,v10\n"
"v_sub_f32 v50,v50,v10\n"
"v_sub_f32 v51,v51,v10\n"
"v_sub_f32 v88,v88,v10\n"
"v_sub_f32 v89,v89,v10\n"
"v_sub_f32 v90,v90,v10\n"
"v_sub_f32 v91,v91,v10\n"
"v_sub_f32 v108,v108,v10\n"
"v_sub_f32 v109,v109,v10\n"
"v_sub_f32 v110,v110,v10\n"
"v_sub_f32 v111,v111,v10\n"
"v_sub_f32 v128,v128,v10\n"
"v_sub_f32 v129,v129,v10\n"
"v_sub_f32 v130,v130,v10\n"
"v_sub_f32 v131,v131,v10\n"
"v_exp_f32 v108,v108\n"
"v_exp_f32 v109,v109\n"
"v_exp_f32 v110,v110\n"
"v_exp_f32 v111,v111\n"
"v_exp_f32 v128,v128\n"
"v_exp_f32 v129,v129\n"
"v_exp_f32 v130,v130\n"
"v_exp_f32 v131,v131\n"
"v_add_f32 v61,v61,v108\n"
"v_add_f32 v61,v61,v109\n"
"v_add_f32 v61,v61,v110\n"
"v_add_f32 v61,v61,v111\n"
"v_add_f32 v61,v61,v128\n"
"v_add_f32 v61,v61,v129\n"
"v_add_f32 v61,v61,v130\n"
"v_add_f32_e64 v61,v61,v131\n"
CV "v108,v108,v109\n"
CV "v109,v110,v111\n"
CV "v110,v128,v129\n"
CV "v111,v130,v131\n"
"s_nop 7\n"
"s_setpc_b64 s[52:53]\n"
".Ldone:\n"
    : "+{v0}"(tid), "+{s23}"(bid), "+{s[20:21]}"(ka)
    :
    : "v1", "v2", "v3", "v4", "v5", "v6", "v7", "v8", "v9", "v10", "v11", "v12", "v13", "v14", "v15", "v16", "v17", "v18", "v19", "v20", "v21", "v22", "v23", "v24", "v25", "v26", "v27", "v28", "v29", "v30", "v31", "v32", "v33", "v34", "v35", "v36", "v37", "v38", "v39", "v40", "v41", "v42", "v43", "v44", "v45", "v46", "v47", "v48", "v49", "v50", "v51", "v52", "v53", "v54", "v55", "v56", "v57", "v58", "v59", "v60", "v61", "v62", "v63", "v64", "v65", "v66", "v67", "v68", "v69", "v70", "v71", "v72", "v73", "v74", "v75", "v76", "v77", "v78", "v79", "v80", "v81", "v82", "v83", "v84", "v85", "v86", "v87", "v88", "v89", "v90", "v91", "v92", "v93", "v94", "v95", "v96", "v97", "v98", "v99", "v100", "v101", "v102", "v103", "v104", "v105", "v106", "v107", "v108", "v109", "v110", "v111", "v112", "v113", "v114", "v115", "v116", "v117", "v118", "v119", "v120", "v121", "v122", "v123", "v124", "v125", "v126", "v127", "v128", "v129", "v130", "v131", "v132", "v133", "v134", "v135", "v136", "v137", "v138", "v139", "v140", "v141", "v142", "v143", "v144", "v145", "v146", "v147", "v148", "v149", "v150", "v151", "v152", "v153", "v154", "v155", "v156", "v157", "v158", "v159", "v160", "v161", "v162", "v163", "v164", "v165", "v166", "v167", "v168", "v169", "v170", "v171", "v172", "v173", "v174", "v175", "v176", "v177", "v178", "v179", "v180", "v181", "v182", "v183", "v184", "v185", "v186", "v187", "v188", "v189", "v190", "v191", "v192", "v193", "v194", "v195", "v196", "v197", "v198", "v199", "v200", "v201", "v202", "v203", "v204", "v205", "v206", "v207", "v208", "v209", "v210", "v211", "v212", "v213", "v214", "v215", "v216", "v217", "v218", "v219", "v220", "v221", "v222", "v223", "v224", "v225", "v226", "v227", "v228", "v229", "v230", "v231", "v232", "v233", "v234", "v235", "v236", "v237", "v238", "v239", "v240", "v241", "v242", "v243", "v244", "v245", "v246", "v247", "v248", "v249", "v250", "v251", "v252", "v253", "v254", "v255", "a0", "a1", "a2", "a3", "a4", "a5", "a6", "a7", "a8", "a9", "a10", "a11", "a12", "a13", "a14", "a15", "a16", "a17", "a18", "a19", "a20", "a21", "a22", "a23", "a24", "a25", "a26", "a27", "a28", "a29", "a30", "a31", "a32", "a33", "a34", "a35", "a36", "a37", "a38", "a39", "a40", "a41", "a42", "a43", "a44", "a45", "a46", "a47", "a48", "a49", "a50", "a51", "a52", "a53", "a54", "a55", "a56", "a57", "a58", "a59", "a60", "a61", "a62", "a63", "a64", "a65", "a66", "a67", "a68", "a69", "a70", "a71", "a72", "a73", "a74", "a75", "a76", "a77", "a78", "a79", "a80", "a81", "a82", "a83", "a84", "a85", "a86", "a87", "a88", "a89", "a90", "a91", "a92", "a93", "a94", "a95", "a96", "a97", "a98", "a99", "a100", "a101", "a102", "a103", "a104", "a105", "a106", "a107", "a108", "a109", "a110", "a111", "a112", "a113", "a114", "a115", "a116", "a117", "a118", "a119", "a120", "a121", "a122", "a123", "a124", "a125", "a126", "a127", "a128", "a129", "a130", "a131", "a132", "a133", "a134", "a135", "a136", "a137", "a138", "a139", "a140", "a141", "a142", "a143", "a144", "a145", "a146", "a147", "a148", "a149", "a150", "a151", "a152", "a153", "a154", "a155", "a156", "a157", "a158", "a159", "a160", "a161", "a162", "a163", "a164", "a165", "a166", "a167", "a168", "a169", "a170", "a171", "a172", "a173", "a174", "a175", "a176", "a177", "a178", "a179", "a180", "a181", "a182", "a183", "a184", "a185", "a186", "a187", "a188", "a189", "a190", "a191", "a192", "a193", "a194", "a195", "a196", "a197", "a198", "a199", "a200", "a201", "a202", "a203", "a204", "a205", "a206", "a207", "a208", "a209", "a210", "a211", "a212", "a213", "a214", "a215", "a216", "a217", "a218", "a219", "a220", "a221", "a222", "a223", "a224", "a225", "a226", "a227", "a228", "a229", "a230", "a231", "a232", "a233", "a234", "a235", "a236", "a237", "a238", "a239", "a240", "a241", "a242", "a243", "a244", "a245", "a246", "a247", "a248", "a249", "a250", "a251", "a252", "a253", "a254", "a255", "s0", "s1", "s2", "s3", "s4", "s5", "s6", "s7", "s8", "s9", "s10", "s11", "s12", "s13", "s14", "s15", "s16", "s17", "s18", "s19", "s22", "s24", "s25", "s26", "s27", "s28", "s29", "s30", "s31", "s32", "s33", "s34", "s35", "s36", "s37", "s38", "s39", "s40", "s41", "s42", "s43", "s44", "s45", "s46", "s47", "s48", "s49", "s50", "s51", "s52", "s53", "s54", "s55", "s56", "s57", "s58", "s59", "s60", "s61", "s62", "s63", "s64", "s65", "s66", "s67", "s68", "s69", "s70", "s71", "s72", "s73", "s74", "s75", "s76", "s77", "s78", "s79", "s80", "s81", "s82", "s83", "s84", "s85", "s86", "s87", "s88", "s89", "s90", "s91", "s92", "s93", "s94", "s95", "s96", "s97", "s98", "s99", "s100", "s101", "vcc", "scc", "memory");
}

extern "C" void kernel_launch(void* const* d_in, const int* in_sizes, int n_in, void* d_out, int out_size, void* d_ws, size_t ws_size, hipStream_t stream) {
  Ptrs P;
  for (int br = 0; br < 2; ++br) { P.x[br] = d_in[5 * br]; P.k[br] = d_in[5 * br + 1]; P.v[br] = d_in[5 * br + 2]; P.wq[br] = d_in[5 * br + 3]; P.wo[br] = d_in[5 * br + 4]; }
  (void)hipFuncSetAttribute((const void*)fused_kernel, hipFuncAttributeMaxDynamicSharedMemorySize, LDS1);
  fused_kernel<<<256, 256, LDS1, stream>>>(P, (char*)d_ws, (char*)d_out);
}
```

```cpp
#include <hip/hip_runtime.h>
#include <hip/hip_bf16.h>

#define MF "v_mfma_f32_16x16x32_bf16 "
#define TR "ds_read_b64_tr_b16 "
#define AR "v_accvgpr_read_b32 "
#define AW "v_accvgpr_write_b32 "
#define CV "v_cvt_pk_bf16_f32 "
#define LD "buffer_load_dwordx4 "
constexpr unsigned LDS1 = 160 * 1024;

struct Ptrs { const void* x[2]; const void* k[2]; const void* v[2]; const void* wq[2]; const void* wo[2]; };

__global__ __launch_bounds__(256, 1) __attribute__((aligned(4096))) void fused_kernel(Ptrs P, char* __restrict__ ws, char* __restrict__ out) {
  unsigned tid = threadIdx.x, bid = blockIdx.x;
  unsigned long long ka = (unsigned long long)__builtin_amdgcn_kernarg_segment_ptr();
  asm volatile(
".p2align 5\n"
".Lcode0:\n"
"s_and_b32 s58,s23,4\n"
"s_lshl_b32 s58,s58,1\n"
"s_load_dwordx2 s[28:29],s[20:21],s58 offset:48\n"
"s_load_dwordx2 s[24:25],s[20:21],s58\n"
"s_load_dwordx2 s[32:33],s[20:21],s58 offset:16\n"
"s_load_dwordx2 s[36:37],s[20:21],s58 offset:32\n"
"s_load_dwordx2 s[48:49],s[20:21],s58 offset:64\n"
"s_memtime s[100:101]\n"
"s_load_dwordx2 s[40:41],s[20:21],0x50\n"
"s_load_dwordx2 s[44:45],s[20:21],0x58\n"
"v_readfirstlane_b32 s22,v0\n"
"v_and_b32 v9,63,v0\n"
"v_bfe_u32 v10,v0,3,3\n"
"v_and_b32 v11,7,v0\n"
"s_lshr_b32 s22,s22,6\n"
"s_or_b32 s22,s22,s58\n"
"s_lshr_b32 s59,s23,4\n"
"s_and_b32 s58,s59,8\n"
"s_mul_i32 s59,s59,4112\n"
"s_or_b32 s22,s22,s59\n"
"s_and_b32 s59,s23,3\n"
"s_lshl_b32 s59,s59,1\n"
"s_bfe_u32 s60,s23,0x10003\n"
"s_or_b32 s59,s59,s60\n"
"s_lshl_b32 s60,s59,20\n"
"s_or_b32 s22,s22,s60\n"
"s_or_b32 s59,s59,s58\n"
"s_mul_i32 s59,s59,65792\n"
"s_xor_b32 s59,s59,524288\n"
"s_or_b32 s22,s22,s59\n"
"v_lshrrev_b32 v6,2,v0\n"
"v_and_b32 v6,4,v6\n"
"v_xor_b32 v6,v6,v11\n"
"v_lshlrev_b32_e64 v6,4,v6\n"
"s_mov_b32 s58,12288\n"
"v_mad_u32_u24 v6,v10,s58,v6\n"
"v_bfe_u32 v4,v0,4,3\n"
"v_xor_b32 v4,v4,v11\n"
"v_lshlrev_b32 v4,4,v4\n"
"s_mov_b32 s58,1280\n"
"v_mad_u32_u24 v4,v10,s58,v4\n"
"v_lshlrev_b32_e64 v9,4,v9\n"
"s_mov_b32 s26,3276800\n"
"s_mov_b32 s27,0x00020000\n"
"s_mov_b32 s30,7864320\n"
"s_mov_b32 s31,0x00020000\n"
"s_mov_b32 s34,0\n"
"s_mov_b32 s35,0x00020000\n"
"s_mov_b32 s38,0\n"
"s_mov_b32 s39,0x00020000\n"
"s_mov_b32 s42,268435456\n"
"s_mov_b32 s43,0x00020000\n"
"s_mov_b32 s46,6553600\n"
"s_mov_b32 s47,0x00020000\n"
"s_mov_b32 s50,7864320\n"
"s_mov_b32 s51,0x00020000\n"
"s_mov_b32 s98,20000\n"
"s_bfe_u32 s56,s22,0x20000\n"
"s_bfe_u32 s72,s22,0x10003\n"
"s_lshr_b32 s73,s56,1\n"
"s_and_b32 s74,s56,1\n"
"s_mul_i32 s62,s56,4096\n"
"s_mul_i32 s63,s56,5120\n"
"s_add_u32 s63,s63,98304\n"
"s_bfe_u32 s58,s22,0x40004\n"
"s_bfe_u32 s59,s22,0x40008\n"
"s_mul_i32 s68,s58,204800\n"
"s_mul_i32 s60,s56,51200\n"
"s_add_u32 s68,s68,s60\n"
"s_lshl_b32 s59,s59,1\n"
"s_add_u32 s59,s59,s73\n"
"s_mul_i32 s66,s59,384\n"
"s_mul_i32 s60,s74,393216\n"
"s_add_u32 s66,s66,s60\n"
"s_lshr_b32 s58,s22,6\n"
"s_xor_b32 s58,s58,s22\n"
"s_bfe_u32 s58,s58,0x10004\n"
"s_mul_i32 s81,s58,640\n"
"s_mul_i32 s60,s81,6144\n"
"s_add_u32 s66,s66,s60\n"
"v_xor_b32 v19,64,v4\n"
"v_add_u32 v16,97280,v6\n"
"v_add_u32 v17,194560,v6\n"
"v_add_u32 v18,291840,v6\n"
"s_waitcnt lgkmcnt(0)\n"
"s_mov_b32 s80,s100\n"
"s_mov_b32 s87,2\n"
"s_add_u32 m0,s62,0\n"
"s_add_u32 s82,s66,0\n"
"s_sub_u32 s15,s82,7864320\n"
"s_min_u32 s82,s82,s15\n"
LD "v6,s[28:31],s82 offen lds\n"
LD "v16,s[28:31],s82 offen offset:1024 lds\n"
LD "v17,s[28:31],s82 offen offset:2048 lds\n"
LD "v18,s[28:31],s82 offen offset:3072 lds\n"
"s_add_u32 s14,s81,0\n"
"s_sub_u32 s15,s14,1280\n"
"s_min_u32 s14,s14,s15\n"
"s_add_u32 s14,s14,s68\n"
"s_add_u32 s84,s14,0\n"
"s_add_u32 m0,s63,0\n"
"s_nop 0\n"
LD "v4,s[24:27],s84 offen lds\n"
"s_add_u32 s84,s14,9216\n"
LD "v19,s[24:27],s84 offen offset:1024 lds\n"
"s_add_u32 s84,s14,18432\n"
LD "v4,s[24:27],s84 offen offset:2048 lds\n"
"s_add_u32 s84,s14,27648\n"
LD "v19,s[24:27],s84 offen offset:3072 lds\n"
"s_add_u32 s84,s14,40960\n"
"s_add_u32 m0,s63,4096\n"
"s_nop 0\n"
LD "v4,s[24:27],s84 offen lds\n"
"s_lshr_b32 s58,s23,3\n"
"s_mul_i32 s59,s58,7\n"
"s_and_b32 s60,s23,7\n"
"s_mul_i32 s60,s60,3\n"
"s_add_u32 s59,s59,s60\n"
"s_and_b32 s59,s59,15\n"
"s_lshl_b32 s59,s59,8\n"
"s_or_b32 s58,s58,s59\n"
"s_and_b32 s59,s23,3\n"
"s_lshl_b32 s59,s59,5\n"
"s_or_b32 s58,s58,s59\n"
"s_bfe_u32 s59,s23,0x10000\n"
"s_lshl_b32 s59,s59,12\n"
"s_or_b32 s58,s58,s59\n"
"s_lshl_b32 s23,s23,16\n"
"s_or_b32 s23,s23,s58\n"
"v_bfe_u32 v20,v0,4,2\n"
"v_mul_u32_u24 v21,384,v10\n"
"v_xor_b32 v22,v11,v20\n"
"s_add_u32 m0,s62,16384\n"
"s_add_u32 s82,s66,128\n"
"s_sub_u32 s15,s82,7864320\n"
"s_min_u32 s82,s82,s15\n"
LD "v6,s[28:31],s82 offen lds\n"
"v_lshl_add_u32 v7,v22,4,v21\n"
"s_lshl_b32 s58,s74,6\n"
"v_xor_b32 v7,s58,v7\n"
"v_lshlrev_b32 v22,1,v20\n"
"v_xor_b32 v22,v11,v22\n"
"v_lshl_add_u32 v8,v22,4,v21\n"
"v_bfe_u32 v21,v0,1,3\n"
"v_xor_b32 v21,v21,v20\n"
"v_lshlrev_b32 v21,4,v21\n"
"v_lshl_add_u32 v21,v11,7,v21\n"
"v_bfe_u32 v22,v0,3,1\n"
"s_mul_i32 s58,s73,12288\n"
"v_mul_u32_u24 v23,3072,v22\n"
"v_add3_u32 v1,v23,v21,s58\n"
"s_mul_i32 s59,s74,10240\n"
"s_add_u32 s59,s59,98304\n"
"v_lshl_add_u32 v2,v22,10,v21\n"
"v_add_u32_e64 v2,s59,v2\n"
"v_bfe_u32 v21,v0,2,3\n"
LD "v16,s[28:31],s82 offen offset:1024 lds\n"
"v_lshlrev_b32_e64 v21,7,v21\n"
"v_bfe_u32 v23,v0,3,2\n"
"v_lshl_add_u32 v21,v23,5,v21\n"
"v_and_b32_e64 v23,3,v0\n"
"v_lshl_add_u32 v21,v23,3,v21\n"
"v_bfe_u32 v23,v0,5,1\n"
"v_mul_u32_u24 v3,3072,v23\n"
"v_add3_u32 v3,v3,v21,s58\n"
"s_bfe_u32 s58,s22,0x40004\n"
"s_mul_i32 s58,s58,409600\n"
"s_mul_i32 s59,s72,1280\n"
"s_add_u32 s58,s58,s59\n"
"v_and_b32 v21,15,v0\n"
"v_mul_u32_u24 v21,2560,v21\n"
"v_lshl_add_u32 v21,v23,4,v21\n"
"v_bfe_u32 v23,v0,4,1\n"
"v_lshl_add_u32 v21,v23,5,v21\n"
"v_add_u32_e64 v12,s58,v21\n"
"v_bfe_u32 v21,v0,2,2\n"
LD "v17,s[28:31],s82 offen offset:2048 lds\n"
"v_lshlrev_b32 v21,7,v21\n"
"v_lshl_add_u32 v21,v20,10,v21\n"
"v_lshl_add_u32 v21,v22,6,v21\n"
"v_and_b32 v23,3,v0\n"
"v_lshl_add_u32 v21,v23,4,v21\n"
"s_lshl_b32 s58,s73,13\n"
"v_add_u32 v0,s58,v21\n"
"s_getpc_b64 s[52:53]\n"
".Lcts:\n"
"s_sub_u32 s52,s52,.Lcts-.Lcode0\n"
"s_subb_u32 s53,s53,0\n"
"s_bfe_u32 s58,s23,0x50000\n"
"s_lshl_b32 s58,s58,2\n"
"s_add_u32 s58,s58,s56\n"
"s_mul_i32 s58,s58,1280\n"
"v_min_u32 v15,144,v9\n"
"v_lshl_add_u32 v15,v15,3,s58\n"
"v_min_u32 v15,.Lcode1-.Lcode0,v15\n"
LD "v18,s[28:31],s82 offen offset:3072 lds\n"
"s_bfe_u32 s58,s22,0x4000c\n"
"s_bfe_u32 s59,s22,0x40010\n"
"s_mul_i32 s69,s58,204800\n"
"s_mul_i32 s60,s56,51200\n"
"s_add_u32 s69,s69,s60\n"
"s_lshl_b32 s59,s59,1\n"
"s_add_u32 s59,s59,s73\n"
"s_mul_i32 s67,s59,384\n"
"s_mul_i32 s60,s74,393216\n"
"s_add_u32 s67,s67,s60\n"
"s_mul_i32 s60,s81,6144\n"
"s_add_u32 s67,s67,s60\n"
"s_bfe_u32 s58,s22,0x40004\n"
"s_bfe_u32 s59,s22,0x40008\n"
"s_lshl_b32 s59,s59,1\n"
"s_add_u32 s88,s59,s74\n"
"s_mul_i32 s88,s88,245760\n"
"s_add_u32 s59,s59,s73\n"
"s_lshl_b32 s70,s58,5\n"
"s_add_u32 m0,s62,32768\n"
"s_add_u32 s82,s66,256\n"
"s_sub_u32 s15,s82,7864320\n"
"s_min_u32 s82,s82,s15\n"
LD "v6,s[28:31],s82 offen lds\n"
"s_add_u32 s70,s70,s59\n"
"s_mul_i32 s70,s70,196608\n"
"s_bfe_u32 s58,s22,0x4000c\n"
"s_bfe_u32 s59,s22,0x40010\n"
"s_lshl_b32 s59,s59,1\n"
"s_add_u32 s89,s59,s74\n"
"s_mul_i32 s89,s89,245760\n"
"s_add_u32 s59,s59,s73\n"
"s_lshl_b32 s71,s58,5\n"
"s_add_u32 s71,s71,s59\n"
"s_mul_i32 s71,s71,196608\n"
"s_bitcmp1_b32 s23,12\n"
"s_cselect_b32 s58,s71,s70\n"
"s_cselect_b32 s71,s70,s71\n"
"s_mov_b32 s70,s58\n"
"s_cselect_b32 s58,s89,s88\n"
"s_cselect_b32 s89,s88,s89\n"
"s_mov_b32 s88,s58\n"
"s_cselect_b32 s69,98,1\n"
LD "v16,s[28:31],s82 offen offset:1024 lds\n"
"s_mov_b32 s54,1037384597\n"
"s_mov_b32 s55,1037384597\n"
"s_bfe_u32 s58,s22,0x40004\n"
"s_lshl_b32 s59,s72,4\n"
"s_add_u32 s59,s59,s58\n"
"s_lshl_b32 s86,s59,12\n"
"s_add_u32 s86,s86,100663296\n"
"s_mul_i32 s93,s59,1638400\n"
"s_add_u32 s93,s93,33554432\n"
"s_bfe_u32 s58,s22,0x30014\n"
"s_lshl_b32 s78,s58,1\n"
"s_add_u32 s78,s78,s74\n"
"s_lshl_b32 s94,s78,2\n"
"s_add_u32 s94,s94,s86\n"
"s_lshl_b32 s92,s58,2\n"
"s_add_u32 s92,s92,s56\n"
"s_mul_i32 s91,s58,204800\n"
"s_add_u32 s91,s91,s93\n"
"s_mul_i32 s75,s56,30720\n"
LD "v17,s[28:31],s82 offen offset:2048 lds\n"
"s_add_u32 s75,s75,s91\n"
"s_add_u32 s75,s75,0\n"
"s_mul_i32 s58,s74,5120\n"
"s_add_u32 s91,s91,s58\n"
"s_mov_b32 s90,0xf0000000\n"
"s_mov_b32 s77,0xf0000000\n"
"s_mov_b32 s85,0xf0000000\n"
AW "a240,v0\n"
AW "a244,v1\n"
AW "a241,v2\n"
AW "a245,v3\n"
AW "a242,v4\n"
AW "a243,v6\n"
AW "a252,v12\n"
"s_mov_b32 s57,0\n"
"s_mov_b32 s83,0\n"
LD "v18,s[28:31],s82 offen offset:3072 lds\n"
"global_load_dword a249,v15,s[52:53]\n"
AR "v0,a240\n"
AR "v2,a241\n"
AR "v4,a242\n"
AR "v6,a243\n"
"v_xor_b32 v1,64,v0\n"
"v_xor_b32 v3,64,v2\n"
"v_xor_b32_e64 v5,64,v4\n"
"v_add_u32 v11,97280,v6\n"
"v_add_u32 v12,194560,v6\n"
"v_add_u32 v13,291840,v6\n"
"s_mov_b64 vcc,0\n"
"s_waitcnt vmcnt(9) lgkmcnt(0)\n"
"s_barrier\n"
"s_nop 0\n"
TR "a[200:201],v0\n"
TR "a[202:203],v0 offset:512\n"
TR "a[204:205],v0 offset:4096\n"
TR "a[206:207],v0 offset:4608\n"
"ds_read_b128 a[60:63],v2\n"
"ds_read_b128 a[100:103],v3\n"
"ds_read_b128 a[64:67],v2 offset:2048\n"
"ds_read_b128 a[104:107],v3 offset:2048\n"
"ds_read_b128 a[68:71],v2 offset:4096\n"
"ds_read_b128 a[108:111],v3 offset:4096\n"
"ds_read_b128 a[72:75],v2 offset:6144\n"
"ds_read_b128 a[112:115],v3 offset:6144\n"
"ds_read_b128 a[76:79],v2 offset:8192\n"
"ds_read_b128 a[116:119],v3 offset:8192\n"
TR "a[208:209],v0 offset:8\n"
TR "a[210:211],v0 offset:520\n"
TR "a[212:213],v0 offset:4104\n"
TR "a[214:215],v0 offset:4616\n"
"s_waitcnt lgkmcnt(0)\n"
MF "v[16:19],a[200:203],a[60:63],0\n"
TR "a[216:217],v1\n"
MF "v[16:19],a[204:207],a[100:103],v[16:19]\n"
"s_add_u32 s14,s81,128\n"
TR "a[218:219],v1 offset:512\n"
MF "v[24:27],a[204:207],a[104:107],0\n"
"s_sub_u32 s15,s14,1280\n"
"v_lshrrev_b32 v15,5,v9\n"
"v_min_u32 v15,11,v15\n"
MF "v[24:27],a[200:203],a[64:67],v[24:27]\n"
"s_min_u32 s14,s14,s15\n"
TR "a[220:221],v1 offset:4096\n"
"v_mul_u32_u24 v15,160,v15\n"
MF "v[32:35],a[200:203],a[68:71],0\n"
"s_add_u32 s14,s14,s68\n"
TR "a[222:223],v1 offset:4608\n"
"v_and_or_b32 v15,v9,16,v15\n"
MF "v[32:35],a[204:207],a[108:111],v[32:35]\n"
"s_add_u32 s84,s14,0\n"
"v_max3_f32 v220,|v16|,|v17|,|v18|\n"
"v_max_f32_e64 v220,v220,|v19|\n"
MF "v[40:43],a[204:207],a[112:115],0\n"
"s_add_u32 m0,s63,20480\n"
"v_readlane_b32 s12,v220,3\n"
"v_readlane_b32 s6,v220,22\n"
MF "v[40:43],a[200:203],a[72:75],v[40:43]\n"
"s_min_u32 s12,s12,s6\n"
"v_readlane_b32 s7,v220,41\n"
"v_readlane_b32 s8,v220,60\n"
MF "v[48:51],a[200:203],a[76:79],0\n"
"s_min_u32 s7,s7,s8\n"
"v_lshlrev_b32 v15,3,v15\n"
MF "v[48:51],a[204:207],a[116:119],v[48:51]\n"
LD "v4,s[24:27],s84 offen lds\n"
MF "v[52:55],a[212:215],a[116:119],0\n"
"s_min_u32 s12,s12,s7\n"
TR "a[200:201],v1 offset:8\n"
MF "v[52:55],a[208:211],a[76:79],v[52:55]\n"
"s_and_b32 s12,s12,0x7f800000\n"
TR "a[202:203],v1 offset:520\n"
MF "v[44:47],a[208:211],a[72:75],0\n"
"s_sub_u32 s12,s12,0x14000000\n"
MF "v[44:47],a[212:215],a[112:115],v[44:47]\n"
"s_cmp_lt_u32 s12,0x50000000\n"
"s_cselect_b32 s12,s12,0xe8400000\n"
TR "a[204:205],v1 offset:4104\n"
MF "v[36:39],a[212:215],a[108:111],0\n"
"s_add_u32 s12,s12,0x17c00000\n"
TR "a[206:207],v1 offset:4616\n"
MF "v[36:39],a[208:211],a[68:71],v[36:39]\n"
"s_bfe_u32 s60,s23,0x70000\n"
"v_mov_b32 v220,s12\n"
"v_mov_b32 v221,s12\n"
MF "v[28:31],a[208:211],a[64:67],0\n"
"s_add_u32 s84,s14,9216\n"
"v_mov_b32 v222,s12\n"
"v_mov_b32 v223,s12\n"
MF "v[28:31],a[212:215],a[104:107],v[28:31]\n"
LD "v5,s[24:27],s84 offen offset:1024 lds\n"
MF "v[20:23],a[212:215],a[100:103],0\n"
"s_waitcnt vmcnt(7) lgkmcnt(0)\n"
MF "v[20:23],a[208:211],a[60:63],v[20:23]\n"
"s_barrier\n"
MF "v[56:59],a[216:219],a[60:63],v[220:223]\n"
"s_lshl_b32 s60,s60,2\n"
TR "a[208:209],v0 offset:16384\n"
MF "v[56:59],a[220:223],a[100:103],v[56:59]\n"
"s_add_u32 s84,s14,18432\n"
TR "a[210:211],v0 offset:16896\n"
MF "v[64:67],a[220:223],a[104:107],v[220:223]\n"
LD "v4,s[24:27],s84 offen offset:2048 lds\n"
MF "v[64:67],a[216:219],a[64:67],v[64:67]\n"
"s_add_u32 s60,s60,s56\n"
TR "a[212:213],v0 offset:20480\n"
MF "v[72:75],a[216:219],a[68:71],v[220:223]\n"
"s_mul_i32 s60,s60,15360\n"
TR "a[214:215],v0 offset:20992\n"
MF "v[72:75],a[220:223],a[108:111],v[72:75]\n"
MF "v[80:83],a[220:223],a[112:115],v[220:223]\n"
"s_add_u32 s84,s14,27648\n"
MF "v[80:83],a[216:219],a[72:75],v[80:83]\n"
LD "v5,s[24:27],s84 offen offset:3072 lds\n"
MF "v[88:91],a[216:219],a[76:79],v[220:223]\n"
MF "v[88:91],a[220:223],a[116:119],v[88:91]\n"
MF "v[92:95],a[204:207],a[116:119],v[220:223]\n"
"s_add_u32 s84,s14,40960\n"
TR "a[216:217],v0 offset:16392\n"
MF "v[92:95],a[200:203],a[76:79],v[92:95]\n"
"s_add_u32 m0,s63,24576\n"
TR "a[218:219],v0 offset:16904\n"
MF "v[84:87],a[200:203],a[72:75],v[220:223]\n"
LD "v4,s[24:27],s84 offen lds\n"
MF "v[84:87],a[204:207],a[112:115],v[84:87]\n"
"s_add_u32 m0,s62,49152\n"
TR "a[220:221],v0 offset:20488\n"
MF "v[76:79],a[204:207],a[108:111],v[220:223]\n"
"s_add_u32 s82,s66,786432\n"
TR "a[222:223],v0 offset:21000\n"
MF "v[76:79],a[200:203],a[68:71],v[76:79]\n"
"s_sub_u32 s15,s82,7864320\n"
MF "v[68:71],a[200:203],a[64:67],v[220:223]\n"
"s_min_u32 s82,s82,s15\n"
MF "v[68:71],a[204:207],a[104:107],v[68:71]\n"
LD "v6,s[28:31],s82 offen lds\n"
MF "v[60:63],a[204:207],a[100:103],v[220:223]\n"
MF "v[60:63],a[200:203],a[60:63],v[60:63]\n"
"s_waitcnt lgkmcnt(4)\n"
MF "v[96:99],a[208:211],a[60:63],v[220:223]\n"
TR "a[200:201],v1 offset:16384\n"
MF "v[96:99],a[212:215],a[100:103],v[96:99]\n"
TR "a[202:203],v1 offset:16896\n"
MF "v[104:107],a[212:215],a[104:107],v[220:223]\n"
LD "v11,s[28:31],s82 offen offset:1024 lds\n"
MF "v[104:107],a[208:211],a[64:67],v[104:107]\n"
TR "a[204:205],v1 offset:20480\n"
MF "v[112:115],a[208:211],a[68:71],v[220:223]\n"
TR "a[206:207],v1 offset:20992\n"
MF "v[112:115],a[212:215],a[108:111],v[112:115]\n"
MF "v[120:123],a[212:215],a[112:115],v[220:223]\n"
MF "v[120:123],a[208:211],a[72:75],v[120:123]\n"
LD "v12,s[28:31],s82 offen offset:2048 lds\n"
MF "v[128:131],a[208:211],a[76:79],v[220:223]\n"
MF "v[128:131],a[212:215],a[116:119],v[128:131]\n"
"s_waitcnt lgkmcnt(4)\n"
MF "v[132:135],a[220:223],a[116:119],v[220:223]\n"
TR "a[208:209],v1 offset:16392\n"
MF "v[132:135],a[216:219],a[76:79],v[132:135]\n"
TR "a[210:211],v1 offset:16904\n"
MF "v[124:127],a[216:219],a[72:75],v[220:223]\n"
LD "v13,s[28:31],s82 offen offset:3072 lds\n"
MF "v[124:127],a[220:223],a[112:115],v[124:127]\n"
"s_add_u32 m0,s62,65536\n"
TR "a[212:213],v1 offset:20488\n"
MF "v[116:119],a[220:223],a[108:111],v[220:223]\n"
"s_add_u32 s82,s66,786560\n"
TR "a[214:215],v1 offset:21000\n"
MF "v[116:119],a[216:219],a[68:71],v[116:119]\n"
"s_sub_u32 s15,s82,7864320\n"
MF "v[108:111],a[216:219],a[64:67],v[220:223]\n"
"s_min_u32 s82,s82,s15\n"
MF "v[108:111],a[220:223],a[104:107],v[108:111]\n"
LD "v6,s[28:31],s82 offen lds\n"
MF "v[100:103],a[220:223],a[100:103],v[220:223]\n"
"s_waitcnt vmcnt(5) lgkmcnt(0)\n"
MF "v[100:103],a[216:219],a[60:63],v[100:103]\n"
"s_barrier\n"
MF "v[136:139],a[200:203],a[60:63],v[220:223]\n"
TR "a[216:217],v0 offset:32768\n"
MF "v[136:139],a[204:207],a[100:103],v[136:139]\n"
TR "a[218:219],v0 offset:33280\n"
MF "v[144:147],a[204:207],a[104:107],v[220:223]\n"
"ds_read_b128 a[140:143],v2 offset:20480\n"
MF "v[144:147],a[200:203],a[64:67],v[144:147]\n"
TR "a[220:221],v0 offset:36864\n"
MF "v[152:155],a[200:203],a[68:71],v[220:223]\n"
TR "a[222:223],v0 offset:37376\n"
MF "v[152:155],a[204:207],a[108:111],v[152:155]\n"
LD "v11,s[28:31],s82 offen offset:1024 lds\n"
MF "v[160:163],a[204:207],a[112:115],v[220:223]\n"
"ds_read_b128 a[180:183],v3 offset:20480\n"
MF "v[160:163],a[200:203],a[72:75],v[160:163]\n"
MF "v[168:171],a[200:203],a[76:79],v[220:223]\n"
MF "v[168:171],a[204:207],a[116:119],v[168:171]\n"
LD "v12,s[28:31],s82 offen offset:2048 lds\n"
MF "v[172:175],a[212:215],a[116:119],v[220:223]\n"
TR "a[200:201],v0 offset:32776\n"
MF "v[172:175],a[208:211],a[76:79],v[172:175]\n"
TR "a[202:203],v0 offset:33288\n"
MF "v[164:167],a[208:211],a[72:75],v[220:223]\n"
"ds_read_b128 a[144:147],v2 offset:22528\n"
MF "v[164:167],a[212:215],a[112:115],v[164:167]\n"
TR "a[204:205],v0 offset:36872\n"
MF "v[156:159],a[212:215],a[108:111],v[220:223]\n"
TR "a[206:207],v0 offset:37384\n"
MF "v[156:159],a[208:211],a[68:71],v[156:159]\n"
LD "v13,s[28:31],s82 offen offset:3072 lds\n"
MF "v[148:151],a[208:211],a[64:67],v[220:223]\n"
"ds_read_b128 a[184:187],v3 offset:22528\n"
MF "v[148:151],a[212:215],a[104:107],v[148:151]\n"
"s_add_u32 m0,s62,81920\n"
MF "v[140:143],a[212:215],a[100:103],v[220:223]\n"
"s_add_u32 s82,s66,786688\n"
"s_sub_u32 s15,s82,7864320\n"
"s_min_u32 s82,s82,s15\n"
MF "v[140:143],a[208:211],a[60:63],v[140:143]\n"
"s_waitcnt lgkmcnt(7)\n"
LD "v6,s[28:31],s82 offen lds\n"
MF "v[176:179],a[216:219],a[60:63],v[220:223]\n"
TR "a[208:209],v1 offset:32768\n"
MF "v[176:179],a[220:223],a[100:103],v[176:179]\n"
TR "a[210:211],v1 offset:33280\n"
MF "v[184:187],a[220:223],a[104:107],v[220:223]\n"
"ds_read_b128 a[148:151],v2 offset:24576\n"
MF "v[184:187],a[216:219],a[64:67],v[184:187]\n"
TR "a[212:213],v1 offset:36864\n"
MF "v[192:195],a[216:219],a[68:71],v[220:223]\n"
TR "a[214:215],v1 offset:37376\n"
MF "v[192:195],a[220:223],a[108:111],v[192:195]\n"
LD "v11,s[28:31],s82 offen offset:1024 lds\n"
MF "v[200:203],a[220:223],a[112:115],v[220:223]\n"
"ds_read_b128 a[188:191],v3 offset:24576\n"
MF "v[200:203],a[216:219],a[72:75],v[200:203]\n"
MF "v[208:211],a[216:219],a[76:79],v[220:223]\n"
MF "v[208:211],a[220:223],a[116:119],v[208:211]\n"
"s_waitcnt lgkmcnt(7)\n"
LD "v12,s[28:31],s82 offen offset:2048 lds\n"
MF "v[212:215],a[204:207],a[116:119],v[220:223]\n"
TR "a[216:217],v1 offset:32776\n"
MF "v[212:215],a[200:203],a[76:79],v[212:215]\n"
TR "a[218:219],v1 offset:33288\n"
MF "v[204:207],a[200:203],a[72:75],v[220:223]\n"
"ds_read_b128 a[152:155],v2 offset:26624\n"
MF "v[204:207],a[204:207],a[112:115],v[204:207]\n"
TR "a[220:221],v1 offset:36872\n"
MF "v[196:199],a[204:207],a[108:111],v[220:223]\n"
TR "a[222:223],v1 offset:37384\n"
MF "v[196:199],a[200:203],a[68:71],v[196:199]\n"
LD "v13,s[28:31],s82 offen offset:3072 lds\n"
MF "v[188:191],a[200:203],a[64:67],v[220:223]\n"
"v_add_u32 v0,0xc000,v0\n"
MF "v[188:191],a[204:207],a[104:107],v[188:191]\n"
"s_add_u32 s14,s81,256\n"
"v_add_u32 v1,0xc000,v1\n"
MF "v[180:183],a[204:207],a[100:103],v[220:223]\n"
"s_waitcnt vmcnt(8) lgkmcnt(0)\n"
"s_mov_b64 exec,0\n"
"s_bitcmp1_b32 s23,12\n"
"s_cbranch_scc1 .Lisy1\n"
"s_cmp_eq_u32 s56,0\n"
"s_cbranch_scc1 .Lpg8b\n"
"s_cmp_eq_u32 s56,1\n"
"s_cbranch_scc1 .Lpg9b\n"
"s_cmp_eq_u32 s56,2\n"
"s_cbranch_scc1 .Lpgt0ex_b\n"
"s_branch .Lpg10b\n"
".Lisy1:\n"
"s_cmp_eq_u32 s56,0\n"
"s_cbranch_scc1 .Lpgq0_c\n"
"s_cmp_eq_u32 s56,1\n"
"s_cbranch_scc1 .Lpg4c\n"
"s_cmp_eq_u32 s56,2\n"
"s_cbranch_scc1 .Lpg5c\n"
"s_branch .Lpg6c\n"
".Lisr1:\n"
"s_mov_b64 exec,-1\n"
".Lisx1:\n"
MF "v[180:183],a[200:203],a[60:63],v[180:183]\n"
"s_barrier\n"
MF "v[216:219],a[208:211],a[60:63],v[220:223]\n"
"s_sub_u32 s15,s14,1280\n"
TR "a[200:201],v0\n"
MF "v[216:219],a[212:215],a[100:103],v[216:219]\n"
"s_min_u32 s14,s14,s15\n"
TR "a[202:203],v0 offset:512\n"
MF "v[224:227],a[212:215],a[104:107],v[220:223]\n"
"s_add_u32 s14,s14,s68\n"
"ds_read_b128 a[192:195],v3 offset:26624\n"
MF "v[224:227],a[208:211],a[64:67],v[224:227]\n"
"s_add_u32 s84,s14,0\n"
TR "a[204:205],v0 offset:4096\n"
MF "v[232:235],a[208:211],a[68:71],v[220:223]\n"
"s_add_u32 m0,s63,40960\n"
TR "a[206:207],v0 offset:4608\n"
MF "v[232:235],a[212:215],a[108:111],v[232:235]\n"
LD "v4,s[24:27],s84 offen lds\n"
MF "v[240:243],a[212:215],a[112:115],v[220:223]\n"
"ds_read_b128 a[156:159],v2 offset:28672\n"
MF "v[240:243],a[208:211],a[72:75],v[240:243]\n"
"ds_read_b128 a[196:199],v3 offset:28672\n"
MF "v[248:251],a[208:211],a[76:79],v[220:223]\n"
"s_add_u32 s84,s14,9216\n"
MF "v[248:251],a[212:215],a[116:119],v[248:251]\n"
LD "v5,s[24:27],s84 offen offset:1024 lds\n"
MF "v[252:255],a[220:223],a[116:119],v[220:223]\n"
TR "a[208:209],v0 offset:8\n"
MF "v[252:255],a[216:219],a[76:79],v[252:255]\n"
TR "a[210:211],v0 offset:520\n"
MF "v[244:247],a[216:219],a[72:75],v[220:223]\n"
MF "v[244:247],a[220:223],a[112:115],v[244:247]\n"
TR "a[212:213],v0 offset:4104\n"
MF "v[236:239],a[220:223],a[108:111],v[220:223]\n"
"s_add_u32 s84,s14,18432\n"
TR "a[214:215],v0 offset:4616\n"
MF "v[236:239],a[216:219],a[68:71],v[236:239]\n"
LD "v4,s[24:27],s84 offen offset:2048 lds\n"
MF "v[228:231],a[216:219],a[64:67],v[220:223]\n"
MF "v[228:231],a[220:223],a[104:107],v[228:231]\n"
MF "v[220:223],a[220:223],a[100:103],v[220:223]\n"
".Lpg1a:\n"
"s_cbranch_execz .Lpg7a\n"
"s_add_u32 s84,s14,27648\n"
MF "v[220:223],a[216:219],a[60:63],v[220:223]\n"
"s_waitcnt lgkmcnt(4)\n"
LD "v5,s[24:27],s84 offen offset:3072 lds\n"
MF "v[16:19],a[200:203],a[140:143],v[16:19]\n"
TR "a[216:217],v1\n"
MF "v[16:19],a[204:207],a[180:183],v[16:19]\n"
TR "a[218:219],v1 offset:512\n"
MF "v[24:27],a[204:207],a[184:187],v[24:27]\n"
MF "v[24:27],a[200:203],a[144:147],v[24:27]\n"
"s_add_u32 s84,s14,40960\n"
TR "a[220:221],v1 offset:4096\n"
MF "v[32:35],a[200:203],a[148:151],v[32:35]\n"
"s_add_u32 m0,s63,45056\n"
TR "a[222:223],v1 offset:4608\n"
MF "v[32:35],a[204:207],a[188:191],v[32:35]\n"
LD "v4,s[24:27],s84 offen lds\n"
MF "v[40:43],a[204:207],a[192:195],v[40:43]\n"
MF "v[40:43],a[200:203],a[152:155],v[40:43]\n"
"s_add_u32 m0,s62,0\n"
MF "v[48:51],a[200:203],a[156:159],v[48:51]\n"
"s_add_u32 s82,s66,1572864\n"
"s_sub_u32 s15,s82,7864320\n"
"s_min_u32 s82,s82,s15\n"
MF "v[48:51],a[204:207],a[196:199],v[48:51]\n"
"s_waitcnt lgkmcnt(4)\n"
LD "v6,s[28:31],s82 offen lds\n"
MF "v[52:55],a[212:215],a[196:199],v[52:55]\n"
TR "a[200:201],v1 offset:8\n"
MF "v[52:55],a[208:211],a[156:159],v[52:55]\n"
TR "a[202:203],v1 offset:520\n"
MF "v[44:47],a[208:211],a[152:155],v[44:47]\n"
MF "v[44:47],a[212:215],a[192:195],v[44:47]\n"
TR "a[204:205],v1 offset:4104\n"
MF "v[36:39],a[212:215],a[188:191],v[36:39]\n"
TR "a[206:207],v1 offset:4616\n"
MF "v[36:39],a[208:211],a[148:151],v[36:39]\n"
LD "v11,s[28:31],s82 offen offset:1024 lds\n"
MF "v[28:31],a[208:211],a[144:147],v[28:31]\n"
MF "v[28:31],a[212:215],a[184:187],v[28:31]\n"
MF "v[20:23],a[212:215],a[180:183],v[20:23]\n"
"s_waitcnt vmcnt(11) lgkmcnt(0)\n"
LD "v12,s[28:31],s82 offen offset:2048 lds\n"
MF "v[20:23],a[208:211],a[140:143],v[20:23]\n"
"s_barrier\n"
"v_bfe_u32 v14,v9,4,4\n"
"v_lshlrev_b32 v14,2,v14\n"
"buffer_load_dword v10,v14,s[40:43],s86 offen sc1\n"
".Lhs2:\n"
MF "v[56:59],a[216:219],a[140:143],v[56:59]\n"
TR "a[208:209],v0 offset:16384\n"
MF "v[56:59],a[220:223],a[180:183],v[56:59]\n"
TR "a[210:211],v0 offset:16896\n"
MF "v[64:67],a[220:223],a[184:187],v[64:67]\n"
LD "v13,s[28:31],s82 offen offset:3072 lds\n"
MF "v[64:67],a[216:219],a[144:147],v[64:67]\n"
TR "a[212:213],v0 offset:20480\n"
MF "v[72:75],a[216:219],a[148:151],v[72:75]\n"
TR "a[214:215],v0 offset:20992\n"
MF "v[72:75],a[220:223],a[188:191],v[72:75]\n"
"s_add_u32 s14,s81,384\n"
"s_sub_u32 s15,s14,1280\n"
"s_min_u32 s14,s14,s15\n"
MF "v[80:83],a[220:223],a[192:195],v[80:83]\n"
"s_add_u32 s14,s14,s68\n"
"s_add_u32 s84,s14,0\n"
"s_add_u32 m0,s63,0\n"
MF "v[80:83],a[216:219],a[152:155],v[80:83]\n"
LD "v4,s[24:27],s84 offen lds\n"
MF "v[88:91],a[216:219],a[156:159],v[88:91]\n"
MF "v[88:91],a[220:223],a[196:199],v[88:91]\n"
MF "v[92:95],a[204:207],a[196:199],v[92:95]\n"
TR "a[216:217],v0 offset:16392\n"
MF "v[92:95],a[200:203],a[156:159],v[92:95]\n"
"s_add_u32 s84,s14,9216\n"
TR "a[218:219],v0 offset:16904\n"
MF "v[84:87],a[200:203],a[152:155],v[84:87]\n"
LD "v5,s[24:27],s84 offen offset:1024 lds\n"
MF "v[84:87],a[204:207],a[192:195],v[84:87]\n"
"s_add_u32 m0,s62,16384\n"
TR "a[220:221],v0 offset:20488\n"
MF "v[76:79],a[204:207],a[188:191],v[76:79]\n"
"s_add_u32 s82,s66,1572992\n"
TR "a[222:223],v0 offset:21000\n"
MF "v[76:79],a[200:203],a[148:151],v[76:79]\n"
"s_sub_u32 s15,s82,7864320\n"
MF "v[68:71],a[200:203],a[144:147],v[68:71]\n"
"s_min_u32 s82,s82,s15\n"
MF "v[68:71],a[204:207],a[184:187],v[68:71]\n"
LD "v6,s[28:31],s82 offen lds\n"
MF "v[60:63],a[204:207],a[180:183],v[60:63]\n"
MF "v[60:63],a[200:203],a[140:143],v[60:63]\n"
"s_waitcnt lgkmcnt(4)\n"
MF "v[96:99],a[208:211],a[140:143],v[96:99]\n"
TR "a[200:201],v1 offset:16384\n"
MF "v[96:99],a[212:215],a[180:183],v[96:99]\n"
TR "a[202:203],v1 offset:16896\n"
MF "v[104:107],a[212:215],a[184:187],v[104:107]\n"
LD "v11,s[28:31],s82 offen offset:1024 lds\n"
MF "v[104:107],a[208:211],a[144:147],v[104:107]\n"
TR "a[204:205],v1 offset:20480\n"
MF "v[112:115],a[208:211],a[148:151],v[112:115]\n"
TR "a[206:207],v1 offset:20992\n"
MF "v[112:115],a[212:215],a[188:191],v[112:115]\n"
MF "v[120:123],a[212:215],a[192:195],v[120:123]\n"
MF "v[120:123],a[208:211],a[152:155],v[120:123]\n"
LD "v12,s[28:31],s82 offen offset:2048 lds\n"
MF "v[128:131],a[208:211],a[156:159],v[128:131]\n"
MF "v[128:131],a[212:215],a[196:199],v[128:131]\n"
"s_waitcnt lgkmcnt(4)\n"
MF "v[132:135],a[220:223],a[196:199],v[132:135]\n"
TR "a[208:209],v1 offset:16392\n"
MF "v[132:135],a[216:219],a[156:159],v[132:135]\n"
TR "a[210:211],v1 offset:16904\n"
MF "v[124:127],a[216:219],a[152:155],v[124:127]\n"
LD "v13,s[28:31],s82 offen offset:3072 lds\n"
MF "v[124:127],a[220:223],a[192:195],v[124:127]\n"
TR "a[212:213],v1 offset:20488\n"
MF "v[116:119],a[220:223],a[188:191],v[116:119]\n"
TR "a[214:215],v1 offset:21000\n"
MF "v[116:119],a[216:219],a[148:151],v[116:119]\n"
"s_add_u32 s84,s14,20480\n"
MF "v[108:111],a[216:219],a[144:147],v[108:111]\n"
"s_add_u32 m0,s63,2048\n"
MF "v[108:111],a[220:223],a[184:187],v[108:111]\n"
LD "v4,s[24:27],s84 offen lds\n"
MF "v[100:103],a[220:223],a[180:183],v[100:103]\n"
"s_waitcnt vmcnt(11) lgkmcnt(0)\n"
MF "v[100:103],a[216:219],a[140:143],v[100:103]\n"
"s_barrier\n"
MF "v[136:139],a[200:203],a[140:143],v[136:139]\n"
TR "a[216:217],v0 offset:32768\n"
MF "v[136:139],a[204:207],a[180:183],v[136:139]\n"
TR "a[218:219],v0 offset:33280\n"
MF "v[144:147],a[204:207],a[184:187],v[144:147]\n"
"ds_read_b128 a[60:63],v2 offset:40960\n"
MF "v[144:147],a[200:203],a[144:147],v[144:147]\n"
TR "a[220:221],v0 offset:36864\n"
MF "v[152:155],a[200:203],a[148:151],v[152:155]\n"
"s_add_u32 s84,s14,29696\n"
TR "a[222:223],v0 offset:37376\n"
MF "v[152:155],a[204:207],a[188:191],v[152:155]\n"
LD "v5,s[24:27],s84 offen offset:1024 lds\n"
MF "v[160:163],a[204:207],a[192:195],v[160:163]\n"
"ds_read_b128 a[100:103],v3 offset:40960\n"
MF "v[160:163],a[200:203],a[152:155],v[160:163]\n"
"s_add_u32 m0,s62,32768\n"
MF "v[168:171],a[200:203],a[156:159],v[168:171]\n"
"s_add_u32 s82,s66,1573120\n"
"s_sub_u32 s15,s82,7864320\n"
"s_min_u32 s82,s82,s15\n"
MF "v[168:171],a[204:207],a[196:199],v[168:171]\n"
LD "v6,s[28:31],s82 offen lds\n"
MF "v[172:175],a[212:215],a[196:199],v[172:175]\n"
TR "a[200:201],v0 offset:32776\n"
MF "v[172:175],a[208:211],a[156:159],v[172:175]\n"
TR "a[202:203],v0 offset:33288\n"
MF "v[164:167],a[208:211],a[152:155],v[164:167]\n"
"ds_read_b128 a[64:67],v2 offset:43008\n"
MF "v[164:167],a[212:215],a[192:195],v[164:167]\n"
TR "a[204:205],v0 offset:36872\n"
MF "v[156:159],a[212:215],a[188:191],v[156:159]\n"
TR "a[206:207],v0 offset:37384\n"
MF "v[156:159],a[208:211],a[148:151],v[156:159]\n"
LD "v11,s[28:31],s82 offen offset:1024 lds\n"
MF "v[148:151],a[208:211],a[144:147],v[148:151]\n"
"ds_read_b128 a[104:107],v3 offset:43008\n"
MF "v[148:151],a[212:215],a[184:187],v[148:151]\n"
MF "v[140:143],a[212:215],a[180:183],v[140:143]\n"
MF "v[140:143],a[208:211],a[140:143],v[140:143]\n"
"s_waitcnt lgkmcnt(7)\n"
LD "v12,s[28:31],s82 offen offset:2048 lds\n"
MF "v[176:179],a[216:219],a[140:143],v[176:179]\n"
TR "a[208:209],v1 offset:32768\n"
MF "v[176:179],a[220:223],a[180:183],v[176:179]\n"
TR "a[210:211],v1 offset:33280\n"
MF "v[184:187],a[220:223],a[184:187],v[184:187]\n"
"ds_read_b128 a[68:71],v2 offset:45056\n"
MF "v[184:187],a[216:219],a[144:147],v[184:187]\n"
TR "a[212:213],v1 offset:36864\n"
MF "v[192:195],a[216:219],a[148:151],v[192:195]\n"
TR "a[214:215],v1 offset:37376\n"
MF "v[192:195],a[220:223],a[188:191],v[192:195]\n"
LD "v13,s[28:31],s82 offen offset:3072 lds\n"
MF "v[200:203],a[220:223],a[192:195],v[200:203]\n"
"ds_read_b128 a[108:111],v3 offset:45056\n"
MF "v[200:203],a[216:219],a[152:155],v[200:203]\n"
"s_add_u32 s84,s14,40960\n"
MF "v[208:211],a[216:219],a[156:159],v[208:211]\n"
"s_add_u32 m0,s63,4096\n"
MF "v[208:211],a[220:223],a[196:199],v[208:211]\n"
"s_waitcnt lgkmcnt(7)\n"
LD "v4,s[24:27],s84 offen lds\n"
MF "v[212:215],a[204:207],a[196:199],v[212:215]\n"
"s_sub_u32 s66,s66,s83\n"
TR "a[216:217],v1 offset:32776\n"
MF "v[212:215],a[200:203],a[156:159],v[212:215]\n"
"s_add_u32 m0,s62,49152\n"
TR "a[218:219],v1 offset:33288\n"
MF "v[204:207],a[200:203],a[152:155],v[204:207]\n"
"s_add_u32 s82,s66,2359296\n"
"ds_read_b128 a[72:75],v2 offset:47104\n"
MF "v[204:207],a[204:207],a[192:195],v[204:207]\n"
"s_sub_u32 s15,s82,7864320\n"
TR "a[220:221],v1 offset:36872\n"
MF "v[196:199],a[204:207],a[188:191],v[196:199]\n"
"s_min_u32 s82,s82,s15\n"
TR "a[222:223],v1 offset:37384\n"
MF "v[196:199],a[200:203],a[148:151],v[196:199]\n"
LD "v6,s[28:31],s82 offen lds\n"
MF "v[188:191],a[200:203],a[144:147],v[188:191]\n"
"v_add_u32 v0,0xffff4000,v0\n"
MF "v[188:191],a[204:207],a[184:187],v[188:191]\n"
"v_add_u32 v1,0xffff4000,v1\n"
MF "v[180:183],a[204:207],a[180:183],v[180:183]\n"
"s_waitcnt vmcnt(14) lgkmcnt(0)\n"
LD "v11,s[28:31],s82 offen offset:1024 lds\n"
MF "v[180:183],a[200:203],a[140:143],v[180:183]\n"
"s_barrier\n"
MF "v[216:219],a[208:211],a[140:143],v[216:219]\n"
TR "a[200:201],v0\n"
MF "v[216:219],a[212:215],a[180:183],v[216:219]\n"
TR "a[202:203],v0 offset:512\n"
MF "v[224:227],a[212:215],a[184:187],v[224:227]\n"
"ds_read_b128 a[112:115],v3 offset:47104\n"
MF "v[224:227],a[208:211],a[144:147],v[224:227]\n"
TR "a[204:205],v0 offset:4096\n"
MF "v[232:235],a[208:211],a[148:151],v[232:235]\n"
TR "a[206:207],v0 offset:4608\n"
MF "v[232:235],a[212:215],a[188:191],v[232:235]\n"
LD "v12,s[28:31],s82 offen offset:2048 lds\n"
MF "v[240:243],a[212:215],a[192:195],v[240:243]\n"
"ds_read_b128 a[76:79],v2 offset:49152\n"
MF "v[240:243],a[208:211],a[152:155],v[240:243]\n"
"ds_read_b128 a[116:119],v3 offset:49152\n"
MF "v[248:251],a[208:211],a[156:159],v[248:251]\n"
MF "v[248:251],a[212:215],a[196:199],v[248:251]\n"
LD "v13,s[28:31],s82 offen offset:3072 lds\n"
MF "v[252:255],a[220:223],a[196:199],v[252:255]\n"
TR "a[208:209],v0 offset:8\n"
MF "v[252:255],a[216:219],a[156:159],v[252:255]\n"
TR "a[210:211],v0 offset:520\n"
MF "v[244:247],a[216:219],a[152:155],v[244:247]\n"
"s_add_u32 s14,s81,512\n"
"s_sub_u32 s15,s14,1280\n"
MF "v[244:247],a[220:223],a[192:195],v[244:247]\n"
"s_min_u32 s14,s14,s15\n"
"s_add_u32 s14,s14,s68\n"
TR "a[212:213],v0 offset:4104\n"
MF "v[236:239],a[220:223],a[188:191],v[236:239]\n"
"s_add_u32 s84,s14,0\n"
"s_add_u32 m0,s63,20480\n"
TR "a[214:215],v0 offset:4616\n"
MF "v[236:239],a[216:219],a[148:151],v[236:239]\n"
LD "v4,s[24:27],s84 offen lds\n"
MF "v[228:231],a[216:219],a[144:147],v[228:231]\n"
MF "v[228:231],a[220:223],a[184:187],v[228:231]\n"
MF "v[220:223],a[220:223],a[180:183],v[220:223]\n"
"s_add_u32 s84,s14,9216\n"
MF "v[220:223],a[216:219],a[140:143],v[220:223]\n"
"s_waitcnt lgkmcnt(4)\n"
LD "v5,s[24:27],s84 offen offset:1024 lds\n"
MF "v[16:19],a[200:203],a[60:63],v[16:19]\n"
TR "a[216:217],v1\n"
MF "v[16:19],a[204:207],a[100:103],v[16:19]\n"
"s_add_u32 m0,s62,65536\n"
TR "a[218:219],v1 offset:512\n"
MF "v[24:27],a[204:207],a[104:107],v[24:27]\n"
"s_add_u32 s82,s66,2359424\n"
MF "v[24:27],a[200:203],a[64:67],v[24:27]\n"
"s_sub_u32 s15,s82,7864320\n"
TR "a[220:221],v1 offset:4096\n"
MF "v[32:35],a[200:203],a[68:71],v[32:35]\n"
"s_min_u32 s82,s82,s15\n"
TR "a[222:223],v1 offset:4608\n"
MF "v[32:35],a[204:207],a[108:111],v[32:35]\n"
LD "v6,s[28:31],s82 offen lds\n"
MF "v[40:43],a[204:207],a[112:115],v[40:43]\n"
MF "v[40:43],a[200:203],a[72:75],v[40:43]\n"
MF "v[48:51],a[200:203],a[76:79],v[48:51]\n"
MF "v[48:51],a[204:207],a[116:119],v[48:51]\n"
"s_waitcnt lgkmcnt(4)\n"
LD "v11,s[28:31],s82 offen offset:1024 lds\n"
MF "v[52:55],a[212:215],a[116:119],v[52:55]\n"
TR "a[200:201],v1 offset:8\n"
MF "v[52:55],a[208:211],a[76:79],v[52:55]\n"
TR "a[202:203],v1 offset:520\n"
MF "v[44:47],a[208:211],a[72:75],v[44:47]\n"
MF "v[44:47],a[212:215],a[112:115],v[44:47]\n"
TR "a[204:205],v1 offset:4104\n"
MF "v[36:39],a[212:215],a[108:111],v[36:39]\n"
TR "a[206:207],v1 offset:4616\n"
MF "v[36:39],a[208:211],a[68:71],v[36:39]\n"
LD "v12,s[28:31],s82 offen offset:2048 lds\n"
MF "v[28:31],a[208:211],a[64:67],v[28:31]\n"
MF "v[28:31],a[212:215],a[104:107],v[28:31]\n"
MF "v[20:23],a[212:215],a[100:103],v[20:23]\n"
"s_waitcnt vmcnt(16) lgkmcnt(0)\n"
LD "v13,s[28:31],s82 offen offset:3072 lds\n"
MF "v[20:23],a[208:211],a[60:63],v[20:23]\n"
"s_barrier\n"
MF "v[56:59],a[216:219],a[60:63],v[56:59]\n"
"s_add_u32 s84,s14,20480\n"
TR "a[208:209],v0 offset:16384\n"
MF "v[56:59],a[220:223],a[100:103],v[56:59]\n"
"s_add_u32 m0,s63,22528\n"
TR "a[210:211],v0 offset:16896\n"
MF "v[64:67],a[220:223],a[104:107],v[64:67]\n"
LD "v4,s[24:27],s84 offen lds\n"
MF "v[64:67],a[216:219],a[64:67],v[64:67]\n"
TR "a[212:213],v0 offset:20480\n"
MF "v[72:75],a[216:219],a[68:71],v[72:75]\n"
TR "a[214:215],v0 offset:20992\n"
MF "v[72:75],a[220:223],a[108:111],v[72:75]\n"
MF "v[80:83],a[220:223],a[112:115],v[80:83]\n"
"s_add_u32 s84,s14,29696\n"
MF "v[80:83],a[216:219],a[72:75],v[80:83]\n"
LD "v5,s[24:27],s84 offen offset:1024 lds\n"
MF "v[88:91],a[216:219],a[76:79],v[88:91]\n"
"s_add_u32 m0,s62,81920\n"
MF "v[88:91],a[220:223],a[116:119],v[88:91]\n"
"s_add_u32 s82,s66,2359552\n"
MF "v[92:95],a[204:207],a[116:119],v[92:95]\n"
"s_sub_u32 s15,s82,7864320\n"
TR "a[216:217],v0 offset:16392\n"
MF "v[92:95],a[200:203],a[76:79],v[92:95]\n"
"s_min_u32 s82,s82,s15\n"
TR "a[218:219],v0 offset:16904\n"
MF "v[84:87],a[200:203],a[72:75],v[84:87]\n"
LD "v6,s[28:31],s82 offen lds\n"
MF "v[84:87],a[204:207],a[112:115],v[84:87]\n"
TR "a[220:221],v0 offset:20488\n"
MF "v[76:79],a[204:207],a[108:111],v[76:79]\n"
TR "a[222:223],v0 offset:21000\n"
MF "v[76:79],a[200:203],a[68:71],v[76:79]\n"
MF "v[68:71],a[200:203],a[64:67],v[68:71]\n"
MF "v[68:71],a[204:207],a[104:107],v[68:71]\n"
LD "v11,s[28:31],s82 offen offset:1024 lds\n"
MF "v[60:63],a[204:207],a[100:103],v[60:63]\n"
MF "v[60:63],a[200:203],a[60:63],v[60:63]\n"
"s_waitcnt lgkmcnt(4)\n"
MF "v[96:99],a[208:211],a[60:63],v[96:99]\n"
TR "a[200:201],v1 offset:16384\n"
MF "v[96:99],a[212:215],a[100:103],v[96:99]\n"
TR "a[202:203],v1 offset:16896\n"
MF "v[104:107],a[212:215],a[104:107],v[104:107]\n"
LD "v12,s[28:31],s82 offen offset:2048 lds\n"
MF "v[104:107],a[208:211],a[64:67],v[104:107]\n"
TR "a[204:205],v1 offset:20480\n"
MF "v[112:115],a[208:211],a[68:71],v[112:115]\n"
TR "a[206:207],v1 offset:20992\n"
MF "v[112:115],a[212:215],a[108:111],v[112:115]\n"
MF "v[120:123],a[212:215],a[112:115],v[120:123]\n"
MF "v[120:123],a[208:211],a[72:75],v[120:123]\n"
LD "v13,s[28:31],s82 offen offset:3072 lds\n"
MF "v[128:131],a[208:211],a[76:79],v[128:131]\n"
MF "v[128:131],a[212:215],a[116:119],v[128:131]\n"
"s_waitcnt lgkmcnt(4)\n"
MF "v[132:135],a[220:223],a[116:119],v[132:135]\n"
"s_add_u32 s84,s14,40960\n"
TR "a[208:209],v1 offset:16392\n"
MF "v[132:135],a[216:219],a[76:79],v[132:135]\n"
"s_add_u32 m0,s63,24576\n"
TR "a[210:211],v1 offset:16904\n"
MF "v[124:127],a[216:219],a[72:75],v[124:127]\n"
LD "v4,s[24:27],s84 offen lds\n"
MF "v[124:127],a[220:223],a[112:115],v[124:127]\n"
"s_add_u32 m0,s62,0\n"
TR "a[212:213],v1 offset:20488\n"
MF "v[116:119],a[220:223],a[108:111],v[116:119]\n"
"s_add_u32 s82,s66,3145728\n"
TR "a[214:215],v1 offset:21000\n"
MF "v[116:119],a[216:219],a[68:71],v[116:119]\n"
"s_sub_u32 s15,s82,7864320\n"
MF "v[108:111],a[216:219],a[64:67],v[108:111]\n"
"s_min_u32 s82,s82,s15\n"
MF "v[108:111],a[220:223],a[104:107],v[108:111]\n"
LD "v6,s[28:31],s82 offen lds\n"
MF "v[100:103],a[220:223],a[100:103],v[100:103]\n"
"s_waitcnt vmcnt(18) lgkmcnt(0)\n"
MF "v[100:103],a[216:219],a[60:63],v[100:103]\n"
"s_barrier\n"
MF "v[136:139],a[200:203],a[60:63],v[136:139]\n"
TR "a[216:217],v0 offset:32768\n"
MF "v[136:139],a[204:207],a[100:103],v[136:139]\n"
TR "a[218:219],v0 offset:33280\n"
MF "v[144:147],a[204:207],a[104:107],v[144:147]\n"
"ds_read_b128 a[140:143],v2\n"
MF "v[144:147],a[200:203],a[64:67],v[144:147]\n"
TR "a[220:221],v0 offset:36864\n"
MF "v[152:155],a[200:203],a[68:71],v[152:155]\n"
TR "a[222:223],v0 offset:37376\n"
MF "v[152:155],a[204:207],a[108:111],v[152:155]\n"
LD "v11,s[28:31],s82 offen offset:1024 lds\n"
MF "v[160:163],a[204:207],a[112:115],v[160:163]\n"
"ds_read_b128 a[180:183],v3\n"
MF "v[160:163],a[200:203],a[72:75],v[160:163]\n"
MF "v[168:171],a[200:203],a[76:79],v[168:171]\n"
MF "v[168:171],a[204:207],a[116:119],v[168:171]\n"
LD "v12,s[28:31],s82 offen offset:2048 lds\n"
MF "v[172:175],a[212:215],a[116:119],v[172:175]\n"
TR "a[200:201],v0 offset:32776\n"
MF "v[172:175],a[208:211],a[76:79],v[172:175]\n"
TR "a[202:203],v0 offset:33288\n"
MF "v[164:167],a[208:211],a[72:75],v[164:167]\n"
"ds_read_b128 a[144:147],v2 offset:2048\n"
MF "v[164:167],a[212:215],a[112:115],v[164:167]\n"
TR "a[204:205],v0 offset:36872\n"
MF "v[156:159],a[212:215],a[108:111],v[156:159]\n"
TR "a[206:207],v0 offset:37384\n"
MF "v[156:159],a[208:211],a[68:71],v[156:159]\n"
LD "v13,s[28:31],s82 offen offset:3072 lds\n"
MF "v[148:151],a[208:211],a[64:67],v[148:151]\n"
"ds_read_b128 a[184:187],v3 offset:2048\n"
MF "v[148:151],a[212:215],a[104:107],v[148:151]\n"
"s_add_u32 s14,s81,640\n"
"s_sub_u32 s15,s14,1280\n"
"s_min_u32 s14,s14,s15\n"
MF "v[140:143],a[212:215],a[100:103],v[140:143]\n"
"s_add_u32 s14,s14,s68\n"
"s_add_u32 s84,s14,0\n"
"s_add_u32 m0,s63,40960\n"
MF "v[140:143],a[208:211],a[60:63],v[140:143]\n"
"s_waitcnt lgkmcnt(7)\n"
LD "v4,s[24:27],s84 offen lds\n"
MF "v[176:179],a[216:219],a[60:63],v[176:179]\n"
TR "a[208:209],v1 offset:32768\n"
MF "v[176:179],a[220:223],a[100:103],v[176:179]\n"
TR "a[210:211],v1 offset:33280\n"
MF "v[184:187],a[220:223],a[104:107],v[184:187]\n"
"ds_read_b128 a[148:151],v2 offset:4096\n"
MF "v[184:187],a[216:219],a[64:67],v[184:187]\n"
TR "a[212:213],v1 offset:36864\n"
MF "v[192:195],a[216:219],a[68:71],v[192:195]\n"
"s_add_u32 s84,s14,9216\n"
TR "a[214:215],v1 offset:37376\n"
MF "v[192:195],a[220:223],a[108:111],v[192:195]\n"
LD "v5,s[24:27],s84 offen offset:1024 lds\n"
MF "v[200:203],a[220:223],a[112:115],v[200:203]\n"
"ds_read_b128 a[188:191],v3 offset:4096\n"
MF "v[200:203],a[216:219],a[72:75],v[200:203]\n"
"s_add_u32 m0,s62,16384\n"
MF "v[208:211],a[216:219],a[76:79],v[208:211]\n"
"s_add_u32 s82,s66,3145856\n"
"s_sub_u32 s15,s82,7864320\n"
"s_min_u32 s82,s82,s15\n"
MF "v[208:211],a[220:223],a[116:119],v[208:211]\n"
"s_waitcnt lgkmcnt(7)\n"
LD "v6,s[28:31],s82 offen lds\n"
MF "v[212:215],a[204:207],a[116:119],v[212:215]\n"
TR "a[216:217],v1 offset:32776\n"
MF "v[212:215],a[200:203],a[76:79],v[212:215]\n"
TR "a[218:219],v1 offset:33288\n"
MF "v[204:207],a[200:203],a[72:75],v[204:207]\n"
"ds_read_b128 a[152:155],v2 offset:6144\n"
MF "v[204:207],a[204:207],a[112:115],v[204:207]\n"
TR "a[220:221],v1 offset:36872\n"
MF "v[196:199],a[204:207],a[108:111],v[196:199]\n"
TR "a[222:223],v1 offset:37384\n"
MF "v[196:199],a[200:203],a[68:71],v[196:199]\n"
LD "v11,s[28:31],s82 offen offset:1024 lds\n"
MF "v[188:191],a[200:203],a[64:67],v[188:191]\n"
"v_add_u32 v0,0xc000,v0\n"
MF "v[188:191],a[204:207],a[104:107],v[188:191]\n"
"v_add_u32 v1,0xc000,v1\n"
MF "v[180:183],a[204:207],a[100:103],v[180:183]\n"
"s_waitcnt vmcnt(21) lgkmcnt(0)\n"
LD "v12,s[28:31],s82 offen offset:2048 lds\n"
MF "v[180:183],a[200:203],a[60:63],v[180:183]\n"
"s_barrier\n"
MF "v[216:219],a[208:211],a[60:63],v[216:219]\n"
TR "a[200:201],v0\n"
MF "v[216:219],a[212:215],a[100:103],v[216:219]\n"
TR "a[202:203],v0 offset:512\n"
MF "v[224:227],a[212:215],a[104:107],v[224:227]\n"
"ds_read_b128 a[192:195],v3 offset:6144\n"
MF "v[224:227],a[208:211],a[64:67],v[224:227]\n"
TR "a[204:205],v0 offset:4096\n"
MF "v[232:235],a[208:211],a[68:71],v[232:235]\n"
TR "a[206:207],v0 offset:4608\n"
MF "v[232:235],a[212:215],a[108:111],v[232:235]\n"
LD "v13,s[28:31],s82 offen offset:3072 lds\n"
MF "v[240:243],a[212:215],a[112:115],v[240:243]\n"
"ds_read_b128 a[156:159],v2 offset:8192\n"
MF "v[240:243],a[208:211],a[72:75],v[240:243]\n"
"s_add_u32 s84,s14,20480\n"
"ds_read_b128 a[196:199],v3 offset:8192\n"
MF "v[248:251],a[208:211],a[76:79],v[248:251]\n"
"s_add_u32 m0,s63,43008\n"
MF "v[248:251],a[212:215],a[116:119],v[248:251]\n"
LD "v4,s[24:27],s84 offen lds\n"
MF "v[252:255],a[220:223],a[116:119],v[252:255]\n"
TR "a[208:209],v0 offset:8\n"
MF "v[252:255],a[216:219],a[76:79],v[252:255]\n"
TR "a[210:211],v0 offset:520\n"
MF "v[244:247],a[216:219],a[72:75],v[244:247]\n"
MF "v[244:247],a[220:223],a[112:115],v[244:247]\n"
TR "a[212:213],v0 offset:4104\n"
MF "v[236:239],a[220:223],a[108:111],v[236:239]\n"
"s_add_u32 s84,s14,29696\n"
TR "a[214:215],v0 offset:4616\n"
MF "v[236:239],a[216:219],a[68:71],v[236:239]\n"
LD "v5,s[24:27],s84 offen offset:1024 lds\n"
MF "v[228:231],a[216:219],a[64:67],v[228:231]\n"
MF "v[228:231],a[220:223],a[104:107],v[228:231]\n"
"s_add_u32 m0,s62,32768\n"
MF "v[220:223],a[220:223],a[100:103],v[220:223]\n"
"s_add_u32 s82,s66,3145984\n"
"s_sub_u32 s15,s82,7864320\n"
"s_min_u32 s82,s82,s15\n"
MF "v[220:223],a[216:219],a[60:63],v[220:223]\n"
"s_waitcnt lgkmcnt(4)\n"
LD "v6,s[28:31],s82 offen lds\n"
MF "v[16:19],a[200:203],a[140:143],v[16:19]\n"
TR "a[216:217],v1\n"
MF "v[16:19],a[204:207],a[180:183],v[16:19]\n"
TR "a[218:219],v1 offset:512\n"
MF "v[24:27],a[204:207],a[184:187],v[24:27]\n"
MF "v[24:27],a[200:203],a[144:147],v[24:27]\n"
TR "a[220:221],v1 offset:4096\n"
MF "v[32:35],a[200:203],a[148:151],v[32:35]\n"
TR "a[222:223],v1 offset:4608\n"
MF "v[32:35],a[204:207],a[188:191],v[32:35]\n"
LD "v11,s[28:31],s82 offen offset:1024 lds\n"
MF "v[40:43],a[204:207],a[192:195],v[40:43]\n"
MF "v[40:43],a[200:203],a[152:155],v[40:43]\n"
MF "v[48:51],a[200:203],a[156:159],v[48:51]\n"
MF "v[48:51],a[204:207],a[196:199],v[48:51]\n"
"s_waitcnt lgkmcnt(4)\n"
LD "v12,s[28:31],s82 offen offset:2048 lds\n"
MF "v[52:55],a[212:215],a[196:199],v[52:55]\n"
TR "a[200:201],v1 offset:8\n"
MF "v[52:55],a[208:211],a[156:159],v[52:55]\n"
TR "a[202:203],v1 offset:520\n"
MF "v[44:47],a[208:211],a[152:155],v[44:47]\n"
MF "v[44:47],a[212:215],a[192:195],v[44:47]\n"
TR "a[204:205],v1 offset:4104\n"
MF "v[36:39],a[212:215],a[188:191],v[36:39]\n"
".Lpg2a:\n"
"s_cbranch_execz .Lisr1\n"
TR "a[206:207],v1 offset:4616\n"
MF "v[36:39],a[208:211],a[148:151],v[36:39]\n"
LD "v13,s[28:31],s82 offen offset:3072 lds\n"
MF "v[28:31],a[208:211],a[144:147],v[28:31]\n"
"s_add_u32 s84,s14,40960\n"
MF "v[28:31],a[212:215],a[184:187],v[28:31]\n"
"s_add_u32 m0,s63,45056\n"
MF "v[20:23],a[212:215],a[180:183],v[20:23]\n"
"s_waitcnt vmcnt(23) lgkmcnt(0)\n"
LD "v4,s[24:27],s84 offen lds\n"
MF "v[20:23],a[208:211],a[140:143],v[20:23]\n"
"s_barrier\n"
AW "a253,v10\n"
"v_add_u32 v10,1,v10\n"
"s_bfe_u32 s58,s22,0x30014\n"
"s_lshl_b32 s58,s58,2\n"
"s_add_u32 s58,s58,s56\n"
"s_lshl_b32 s58,s58,6\n"
"s_add_u32 s58,s58,s86\n"
"v_readlane_b32 s76,v10,s78\n"
"buffer_store_dword v10,v14,s[40:43],s58 offen offset:128 sc1\n"
".Lhs5:\n"
MF "v[56:59],a[216:219],a[140:143],v[56:59]\n"
"s_add_u32 m0,s62,49152\n"
"s_add_u32 s82,s66,3932160\n"
TR "a[208:209],v0 offset:16384\n"
MF "v[56:59],a[220:223],a[180:183],v[56:59]\n"
"s_sub_u32 s15,s82,7864320\n"
"s_min_u32 s82,s82,s15\n"
TR "a[210:211],v0 offset:16896\n"
MF "v[64:67],a[220:223],a[184:187],v[64:67]\n"
LD "v6,s[28:31],s82 offen lds\n"
MF "v[64:67],a[216:219],a[144:147],v[64:67]\n"
TR "a[212:213],v0 offset:20480\n"
MF "v[72:75],a[216:219],a[148:151],v[72:75]\n"
TR "a[214:215],v0 offset:20992\n"
MF "v[72:75],a[220:223],a[188:191],v[72:75]\n"
"buffer_load_dword a249,v15,s[48:51],s60 offen offset:1024\n"
MF "v[80:83],a[220:223],a[192:195],v[80:83]\n"
"s_bfe_u32 s59,s23,0x30003\n"
MF "v[80:83],a[216:219],a[152:155],v[80:83]\n"
LD "v11,s[28:31],s82 offen offset:1024 lds\n"
MF "v[88:91],a[216:219],a[156:159],v[88:91]\n"
"s_add_u32 s59,s59,1\n"
MF "v[88:91],a[220:223],a[196:199],v[88:91]\n"
"s_lshl_b32 s59,s59,7\n"
MF "v[92:95],a[204:207],a[196:199],v[92:95]\n"
"s_and_b32 s59,s59,768\n"
TR "a[216:217],v0 offset:16392\n"
MF "v[92:95],a[200:203],a[156:159],v[92:95]\n"
"s_add_u32 s59,s59,s60\n"
TR "a[218:219],v0 offset:16904\n"
MF "v[84:87],a[200:203],a[152:155],v[84:87]\n"
LD "v12,s[28:31],s82 offen offset:2048 lds\n"
MF "v[84:87],a[204:207],a[192:195],v[84:87]\n"
TR "a[220:221],v0 offset:20488\n"
MF "v[76:79],a[204:207],a[188:191],v[76:79]\n"
TR "a[222:223],v0 offset:21000\n"
MF "v[76:79],a[200:203],a[148:151],v[76:79]\n"
MF "v[68:71],a[200:203],a[144:147],v[68:71]\n"
MF "v[68:71],a[204:207],a[184:187],v[68:71]\n"
LD "v13,s[28:31],s82 offen offset:3072 lds\n"
MF "v[60:63],a[204:207],a[180:183],v[60:63]\n"
MF "v[60:63],a[200:203],a[140:143],v[60:63]\n"
"s_add_u32 s14,s81,768\n"
"s_sub_u32 s15,s14,1280\n"
"s_waitcnt lgkmcnt(4)\n"
MF "v[96:99],a[208:211],a[140:143],v[96:99]\n"
"s_min_u32 s14,s14,s15\n"
"s_add_u32 s14,s14,s68\n"
TR "a[200:201],v1 offset:16384\n"
MF "v[96:99],a[212:215],a[180:183],v[96:99]\n"
"s_add_u32 s84,s14,0\n"
"s_add_u32 m0,s63,0\n"
TR "a[202:203],v1 offset:16896\n"
MF "v[104:107],a[212:215],a[184:187],v[104:107]\n"
LD "v4,s[24:27],s84 offen lds\n"
MF "v[104:107],a[208:211],a[144:147],v[104:107]\n"
TR "a[204:205],v1 offset:20480\n"
MF "v[112:115],a[208:211],a[148:151],v[112:115]\n"
TR "a[206:207],v1 offset:20992\n"
MF "v[112:115],a[212:215],a[188:191],v[112:115]\n"
MF "v[120:123],a[212:215],a[192:195],v[120:123]\n"
"s_add_u32 s84,s14,9216\n"
MF "v[120:123],a[208:211],a[152:155],v[120:123]\n"
LD "v5,s[24:27],s84 offen offset:1024 lds\n"
MF "v[128:131],a[208:211],a[156:159],v[128:131]\n"
MF "v[128:131],a[212:215],a[196:199],v[128:131]\n"
"s_waitcnt lgkmcnt(4)\n"
MF "v[132:135],a[220:223],a[196:199],v[132:135]\n"
TR "a[208:209],v1 offset:16392\n"
MF "v[132:135],a[216:219],a[156:159],v[132:135]\n"
TR "a[210:211],v1 offset:16904\n"
MF "v[124:127],a[216:219],a[152:155],v[124:127]\n"
MF "v[124:127],a[220:223],a[192:195],v[124:127]\n"
TR "a[212:213],v1 offset:20488\n"
MF "v[116:119],a[220:223],a[188:191],v[116:119]\n"
TR "a[214:215],v1 offset:21000\n"
MF "v[116:119],a[216:219],a[148:151],v[116:119]\n"
MF "v[108:111],a[216:219],a[144:147],v[108:111]\n"
MF "v[108:111],a[220:223],a[184:187],v[108:111]\n"
MF "v[100:103],a[220:223],a[180:183],v[100:103]\n"
"s_waitcnt vmcnt(24) lgkmcnt(0)\n"
MF "v[100:103],a[216:219],a[140:143],v[100:103]\n"
"s_barrier\n"
MF "v[136:139],a[200:203],a[140:143],v[136:139]\n"
TR "a[216:217],v0 offset:32768\n"
MF "v[136:139],a[204:207],a[180:183],v[136:139]\n"
"s_add_u32 m0,s62,65536\n"
TR "a[218:219],v0 offset:33280\n"
MF "v[144:147],a[204:207],a[184:187],v[144:147]\n"
"s_add_u32 s82,s66,3932288\n"
"ds_read_b128 a[60:63],v2 offset:20480\n"
MF "v[144:147],a[200:203],a[144:147],v[144:147]\n"
"s_sub_u32 s15,s82,7864320\n"
TR "a[220:221],v0 offset:36864\n"
MF "v[152:155],a[200:203],a[148:151],v[152:155]\n"
"s_min_u32 s82,s82,s15\n"
TR "a[222:223],v0 offset:37376\n"
MF "v[152:155],a[204:207],a[188:191],v[152:155]\n"
LD "v6,s[28:31],s82 offen lds\n"
MF "v[160:163],a[204:207],a[192:195],v[160:163]\n"
"ds_read_b128 a[100:103],v3 offset:20480\n"
MF "v[160:163],a[200:203],a[152:155],v[160:163]\n"
MF "v[168:171],a[200:203],a[156:159],v[168:171]\n"
MF "v[168:171],a[204:207],a[196:199],v[168:171]\n"
LD "v11,s[28:31],s82 offen offset:1024 lds\n"
MF "v[172:175],a[212:215],a[196:199],v[172:175]\n"
TR "a[200:201],v0 offset:32776\n"
MF "v[172:175],a[208:211],a[156:159],v[172:175]\n"
TR "a[202:203],v0 offset:33288\n"
MF "v[164:167],a[208:211],a[152:155],v[164:167]\n"
"ds_read_b128 a[64:67],v2 offset:22528\n"
MF "v[164:167],a[212:215],a[192:195],v[164:167]\n"
TR "a[204:205],v0 offset:36872\n"
MF "v[156:159],a[212:215],a[188:191],v[156:159]\n"
TR "a[206:207],v0 offset:37384\n"
MF "v[156:159],a[208:211],a[148:151],v[156:159]\n"
LD "v12,s[28:31],s82 offen offset:2048 lds\n"
MF "v[148:151],a[208:211],a[144:147],v[148:151]\n"
"ds_read_b128 a[104:107],v3 offset:22528\n"
MF "v[148:151],a[212:215],a[184:187],v[148:151]\n"
MF "v[140:143],a[212:215],a[180:183],v[140:143]\n"
MF "v[140:143],a[208:211],a[140:143],v[140:143]\n"
"s_waitcnt lgkmcnt(7)\n"
LD "v13,s[28:31],s82 offen offset:3072 lds\n"
MF "v[176:179],a[216:219],a[140:143],v[176:179]\n"
TR "a[208:209],v1 offset:32768\n"
MF "v[176:179],a[220:223],a[180:183],v[176:179]\n"
TR "a[210:211],v1 offset:33280\n"
MF "v[184:187],a[220:223],a[184:187],v[184:187]\n"
"ds_read_b128 a[68:71],v2 offset:24576\n"
MF "v[184:187],a[216:219],a[144:147],v[184:187]\n"
"s_add_u32 s84,s14,20480\n"
TR "a[212:213],v1 offset:36864\n"
MF "v[192:195],a[216:219],a[148:151],v[192:195]\n"
"s_add_u32 m0,s63,2048\n"
TR "a[214:215],v1 offset:37376\n"
MF "v[192:195],a[220:223],a[188:191],v[192:195]\n"
LD "v4,s[24:27],s84 offen lds\n"
MF "v[200:203],a[220:223],a[192:195],v[200:203]\n"
"ds_read_b128 a[108:111],v3 offset:24576\n"
MF "v[200:203],a[216:219],a[152:155],v[200:203]\n"
MF "v[208:211],a[216:219],a[156:159],v[208:211]\n"
"s_add_u32 s84,s14,29696\n"
MF "v[208:211],a[220:223],a[196:199],v[208:211]\n"
"s_waitcnt lgkmcnt(7)\n"
LD "v5,s[24:27],s84 offen offset:1024 lds\n"
MF "v[212:215],a[204:207],a[196:199],v[212:215]\n"
TR "a[216:217],v1 offset:32776\n"
MF "v[212:215],a[200:203],a[156:159],v[212:215]\n"
TR "a[218:219],v1 offset:33288\n"
MF "v[204:207],a[200:203],a[152:155],v[204:207]\n"
"ds_read_b128 a[72:75],v2 offset:26624\n"
MF "v[204:207],a[204:207],a[192:195],v[204:207]\n"
TR "a[220:221],v1 offset:36872\n"
MF "v[196:199],a[204:207],a[188:191],v[196:199]\n"
TR "a[222:223],v1 offset:37384\n"
MF "v[196:199],a[200:203],a[148:151],v[196:199]\n"
MF "v[188:191],a[200:203],a[144:147],v[188:191]\n"
"v_add_u32 v0,0xffff4000,v0\n"
MF "v[188:191],a[204:207],a[184:187],v[188:191]\n"
"v_add_u32 v1,0xffff4000,v1\n"
MF "v[180:183],a[204:207],a[180:183],v[180:183]\n"
"s_waitcnt vmcnt(26) lgkmcnt(0)\n"
MF "v[180:183],a[200:203],a[140:143],v[180:183]\n"
"s_barrier\n"
MF "v[216:219],a[208:211],a[140:143],v[216:219]\n"
TR "a[200:201],v0\n"
MF "v[216:219],a[212:215],a[180:183],v[216:219]\n"
"s_add_u32 m0,s62,81920\n"
TR "a[202:203],v0 offset:512\n"
MF "v[224:227],a[212:215],a[184:187],v[224:227]\n"
"s_add_u32 s82,s66,3932416\n"
"ds_read_b128 a[112:115],v3 offset:26624\n"
MF "v[224:227],a[208:211],a[144:147],v[224:227]\n"
"s_sub_u32 s15,s82,7864320\n"
TR "a[204:205],v0 offset:4096\n"
MF "v[232:235],a[208:211],a[148:151],v[232:235]\n"
"s_min_u32 s82,s82,s15\n"
TR "a[206:207],v0 offset:4608\n"
MF "v[232:235],a[212:215],a[188:191],v[232:235]\n"
LD "v6,s[28:31],s82 offen lds\n"
MF "v[240:243],a[212:215],a[192:195],v[240:243]\n"
"ds_read_b128 a[76:79],v2 offset:28672\n"
MF "v[240:243],a[208:211],a[152:155],v[240:243]\n"
"ds_read_b128 a[116:119],v3 offset:28672\n"
MF "v[248:251],a[208:211],a[156:159],v[248:251]\n"
MF "v[248:251],a[212:215],a[196:199],v[248:251]\n"
LD "v11,s[28:31],s82 offen offset:1024 lds\n"
MF "v[252:255],a[220:223],a[196:199],v[252:255]\n"
TR "a[208:209],v0 offset:8\n"
MF "v[252:255],a[216:219],a[156:159],v[252:255]\n"
TR "a[210:211],v0 offset:520\n"
MF "v[244:247],a[216:219],a[152:155],v[244:247]\n"
MF "v[244:247],a[220:223],a[192:195],v[244:247]\n"
TR "a[212:213],v0 offset:4104\n"
MF "v[236:239],a[220:223],a[188:191],v[236:239]\n"
TR "a[214:215],v0 offset:4616\n"
MF "v[236:239],a[216:219],a[148:151],v[236:239]\n"
LD "v12,s[28:31],s82 offen offset:2048 lds\n"
MF "v[228:231],a[216:219],a[144:147],v[228:231]\n"
MF "v[228:231],a[220:223],a[184:187],v[228:231]\n"
MF "v[220:223],a[220:223],a[180:183],v[220:223]\n"
MF "v[220:223],a[216:219],a[140:143],v[220:223]\n"
"s_waitcnt lgkmcnt(4)\n"
LD "v13,s[28:31],s82 offen offset:3072 lds\n"
MF "v[16:19],a[200:203],a[60:63],v[16:19]\n"
TR "a[216:217],v1\n"
MF "v[16:19],a[204:207],a[100:103],v[16:19]\n"
TR "a[218:219],v1 offset:512\n"
MF "v[24:27],a[204:207],a[104:107],v[24:27]\n"
MF "v[24:27],a[200:203],a[64:67],v[24:27]\n"
"s_add_u32 s84,s14,40960\n"
TR "a[220:221],v1 offset:4096\n"
MF "v[32:35],a[200:203],a[68:71],v[32:35]\n"
"s_add_u32 m0,s63,4096\n"
TR "a[222:223],v1 offset:4608\n"
MF "v[32:35],a[204:207],a[108:111],v[32:35]\n"
LD "v4,s[24:27],s84 offen lds\n"
MF "v[40:43],a[204:207],a[112:115],v[40:43]\n"
MF "v[40:43],a[200:203],a[72:75],v[40:43]\n"
MF "v[48:51],a[200:203],a[76:79],v[48:51]\n"
MF "v[48:51],a[204:207],a[116:119],v[48:51]\n"
"s_waitcnt lgkmcnt(4)\n"
MF "v[52:55],a[212:215],a[116:119],v[52:55]\n"
TR "a[200:201],v1 offset:8\n"
MF "v[52:55],a[208:211],a[76:79],v[52:55]\n"
TR "a[202:203],v1 offset:520\n"
MF "v[44:47],a[208:211],a[72:75],v[44:47]\n"
MF "v[44:47],a[212:215],a[112:115],v[44:47]\n"
TR "a[204:205],v1 offset:4104\n"
MF "v[36:39],a[212:215],a[108:111],v[36:39]\n"
TR "a[206:207],v1 offset:4616\n"
MF "v[36:39],a[208:211],a[68:71],v[36:39]\n"
MF "v[28:31],a[208:211],a[64:67],v[28:31]\n"
MF "v[28:31],a[212:215],a[104:107],v[28:31]\n"
MF "v[20:23],a[212:215],a[100:103],v[20:23]\n"
"s_waitcnt vmcnt(25) lgkmcnt(0)\n"
MF "v[20:23],a[208:211],a[60:63],v[20:23]\n"
"s_barrier\n"
MF "v[56:59],a[216:219],a[60:63],v[56:59]\n"
"s_add_u32 m0,s62,0\n"
"s_add_u32 s82,s66,4718592\n"
TR "a[208:209],v0 offset:16384\n"
MF "v[56:59],a[220:223],a[100:103],v[56:59]\n"
"s_sub_u32 s15,s82,7864320\n"
"s_min_u32 s82,s82,s15\n"
TR "a[210:211],v0 offset:16896\n"
MF "v[64:67],a[220:223],a[104:107],v[64:67]\n"
LD "v6,s[28:31],s82 offen lds\n"
MF "v[64:67],a[216:219],a[64:67],v[64:67]\n"
TR "a[212:213],v0 offset:20480\n"
MF "v[72:75],a[216:219],a[68:71],v[72:75]\n"
TR "a[214:215],v0 offset:20992\n"
MF "v[72:75],a[220:223],a[108:111],v[72:75]\n"
"buffer_load_dword a249,v15,s[48:51],s59 offen\n"
MF "v[80:83],a[220:223],a[112:115],v[80:83]\n"
"s_bfe_u32 s59,s23,0x30003\n"
MF "v[80:83],a[216:219],a[72:75],v[80:83]\n"
LD "v11,s[28:31],s82 offen offset:1024 lds\n"
MF "v[88:91],a[216:219],a[76:79],v[88:91]\n"
"s_add_u32 s59,s59,3\n"
MF "v[88:91],a[220:223],a[116:119],v[88:91]\n"
"s_lshl_b32 s59,s59,7\n"
MF "v[92:95],a[204:207],a[116:119],v[92:95]\n"
"s_and_b32 s59,s59,768\n"
TR "a[216:217],v0 offset:16392\n"
MF "v[92:95],a[200:203],a[76:79],v[92:95]\n"
"s_add_u32 s59,s59,s60\n"
TR "a[218:219],v0 offset:16904\n"
MF "v[84:87],a[200:203],a[72:75],v[84:87]\n"
LD "v12,s[28:31],s82 offen offset:2048 lds\n"
MF "v[84:87],a[204:207],a[112:115],v[84:87]\n"
TR "a[220:221],v0 offset:20488\n"
MF "v[76:79],a[204:207],a[108:111],v[76:79]\n"
TR "a[222:223],v0 offset:21000\n"
MF "v[76:79],a[200:203],a[68:71],v[76:79]\n"
MF "v[68:71],a[200:203],a[64:67],v[68:71]\n"
MF "v[68:71],a[204:207],a[104:107],v[68:71]\n"
LD "v13,s[28:31],s82 offen offset:3072 lds\n"
MF "v[60:63],a[204:207],a[100:103],v[60:63]\n"
MF "v[60:63],a[200:203],a[60:63],v[60:63]\n"
"s_add_u32 s14,s81,896\n"
"s_sub_u32 s15,s14,1280\n"
"s_waitcnt lgkmcnt(4)\n"
MF "v[96:99],a[208:211],a[60:63],v[96:99]\n"
"s_min_u32 s14,s14,s15\n"
"s_add_u32 s14,s14,s68\n"
TR "a[200:201],v1 offset:16384\n"
MF "v[96:99],a[212:215],a[100:103],v[96:99]\n"
"s_add_u32 s84,s14,0\n"
"s_add_u32 m0,s63,20480\n"
TR "a[202:203],v1 offset:16896\n"
MF "v[104:107],a[212:215],a[104:107],v[104:107]\n"
LD "v4,s[24:27],s84 offen lds\n"
MF "v[104:107],a[208:211],a[64:67],v[104:107]\n"
TR "a[204:205],v1 offset:20480\n"
MF "v[112:115],a[208:211],a[68:71],v[112:115]\n"
TR "a[206:207],v1 offset:20992\n"
MF "v[112:115],a[212:215],a[108:111],v[112:115]\n"
MF "v[120:123],a[212:215],a[112:115],v[120:123]\n"
"s_add_u32 s84,s14,9216\n"
MF "v[120:123],a[208:211],a[72:75],v[120:123]\n"
LD "v5,s[24:27],s84 offen offset:1024 lds\n"
MF "v[128:131],a[208:211],a[76:79],v[128:131]\n"
MF "v[128:131],a[212:215],a[116:119],v[128:131]\n"
"s_waitcnt lgkmcnt(4)\n"
MF "v[132:135],a[220:223],a[116:119],v[132:135]\n"
TR "a[208:209],v1 offset:16392\n"
MF "v[132:135],a[216:219],a[76:79],v[132:135]\n"
TR "a[210:211],v1 offset:16904\n"
MF "v[124:127],a[216:219],a[72:75],v[124:127]\n"
MF "v[124:127],a[220:223],a[112:115],v[124:127]\n"
TR "a[212:213],v1 offset:20488\n"
MF "v[116:119],a[220:223],a[108:111],v[116:119]\n"
TR "a[214:215],v1 offset:21000\n"
MF "v[116:119],a[216:219],a[68:71],v[116:119]\n"
MF "v[108:111],a[216:219],a[64:67],v[108:111]\n"
MF "v[108:111],a[220:223],a[104:107],v[108:111]\n"
MF "v[100:103],a[220:223],a[100:103],v[100:103]\n"
"s_waitcnt vmcnt(25) lgkmcnt(0)\n"
MF "v[100:103],a[216:219],a[60:63],v[100:103]\n"
"s_barrier\n"
MF "v[136:139],a[200:203],a[60:63],v[136:139]\n"
TR "a[216:217],v0 offset:32768\n"
MF "v[136:139],a[204:207],a[100:103],v[136:139]\n"
"s_add_u32 m0,s62,16384\n"
TR "a[218:219],v0 offset:33280\n"
MF "v[144:147],a[204:207],a[104:107],v[144:147]\n"
"s_add_u32 s82,s66,4718720\n"
"ds_read_b128 a[140:143],v2 offset:40960\n"
MF "v[144:147],a[200:203],a[64:67],v[144:147]\n"
"s_sub_u32 s15,s82,7864320\n"
TR "a[220:221],v0 offset:36864\n"
MF "v[152:155],a[200:203],a[68:71],v[152:155]\n"
"s_min_u32 s82,s82,s15\n"
TR "a[222:223],v0 offset:37376\n"
MF "v[152:155],a[204:207],a[108:111],v[152:155]\n"
LD "v6,s[28:31],s82 offen lds\n"
MF "v[160:163],a[204:207],a[112:115],v[160:163]\n"
"ds_read_b128 a[180:183],v3 offset:40960\n"
MF "v[160:163],a[200:203],a[72:75],v[160:163]\n"
MF "v[168:171],a[200:203],a[76:79],v[168:171]\n"
MF "v[168:171],a[204:207],a[116:119],v[168:171]\n"
LD "v11,s[28:31],s82 offen offset:1024 lds\n"
MF "v[172:175],a[212:215],a[116:119],v[172:175]\n"
TR "a[200:201],v0 offset:32776\n"
MF "v[172:175],a[208:211],a[76:79],v[172:175]\n"
TR "a[202:203],v0 offset:33288\n"
MF "v[164:167],a[208:211],a[72:75],v[164:167]\n"
"ds_read_b128 a[144:147],v2 offset:43008\n"
MF "v[164:167],a[212:215],a[112:115],v[164:167]\n"
TR "a[204:205],v0 offset:36872\n"
MF "v[156:159],a[212:215],a[108:111],v[156:159]\n"
TR "a[206:207],v0 offset:37384\n"
MF "v[156:159],a[208:211],a[68:71],v[156:159]\n"
LD "v12,s[28:31],s82 offen offset:2048 lds\n"
MF "v[148:151],a[208:211],a[64:67],v[148:151]\n"
"ds_read_b128 a[184:187],v3 offset:43008\n"
MF "v[148:151],a[212:215],a[104:107],v[148:151]\n"
MF "v[140:143],a[212:215],a[100:103],v[140:143]\n"
MF "v[140:143],a[208:211],a[60:63],v[140:143]\n"
"s_waitcnt lgkmcnt(7)\n"
LD "v13,s[28:31],s82 offen offset:3072 lds\n"
MF "v[176:179],a[216:219],a[60:63],v[176:179]\n"
TR "a[208:209],v1 offset:32768\n"
MF "v[176:179],a[220:223],a[100:103],v[176:179]\n"
TR "a[210:211],v1 offset:33280\n"
MF "v[184:187],a[220:223],a[104:107],v[184:187]\n"
"ds_read_b128 a[148:151],v2 offset:45056\n"
MF "v[184:187],a[216:219],a[64:67],v[184:187]\n"
"s_add_u32 s84,s14,20480\n"
TR "a[212:213],v1 offset:36864\n"
MF "v[192:195],a[216:219],a[68:71],v[192:195]\n"
"s_add_u32 m0,s63,22528\n"
TR "a[214:215],v1 offset:37376\n"
MF "v[192:195],a[220:223],a[108:111],v[192:195]\n"
LD "v4,s[24:27],s84 offen lds\n"
MF "v[200:203],a[220:223],a[112:115],v[200:203]\n"
"ds_read_b128 a[188:191],v3 offset:45056\n"
MF "v[200:203],a[216:219],a[72:75],v[200:203]\n"
MF "v[208:211],a[216:219],a[76:79],v[208:211]\n"
"s_add_u32 s84,s14,29696\n"
MF "v[208:211],a[220:223],a[116:119],v[208:211]\n"
"s_waitcnt lgkmcnt(7)\n"
LD "v5,s[24:27],s84 offen offset:1024 lds\n"
MF "v[212:215],a[204:207],a[116:119],v[212:215]\n"
TR "a[216:217],v1 offset:32776\n"
MF "v[212:215],a[200:203],a[76:79],v[212:215]\n"
TR "a[218:219],v1 offset:33288\n"
MF "v[204:207],a[200:203],a[72:75],v[204:207]\n"
"ds_read_b128 a[152:155],v2 offset:47104\n"
MF "v[204:207],a[204:207],a[112:115],v[204:207]\n"
TR "a[220:221],v1 offset:36872\n"
MF "v[196:199],a[204:207],a[108:111],v[196:199]\n"
TR "a[222:223],v1 offset:37384\n"
MF "v[196:199],a[200:203],a[68:71],v[196:199]\n"
MF "v[188:191],a[200:203],a[64:67],v[188:191]\n"
"v_add_u32 v0,0xc000,v0\n"
MF "v[188:191],a[204:207],a[104:107],v[188:191]\n"
"v_add_u32 v1,0xc000,v1\n"
MF "v[180:183],a[204:207],a[100:103],v[180:183]\n"
"s_waitcnt vmcnt(26) lgkmcnt(0)\n"
MF "v[180:183],a[200:203],a[60:63],v[180:183]\n"
"s_barrier\n"
MF "v[216:219],a[208:211],a[60:63],v[216:219]\n"
TR "a[200:201],v0\n"
MF "v[216:219],a[212:215],a[100:103],v[216:219]\n"
"s_add_u32 m0,s62,32768\n"
TR "a[202:203],v0 offset:512\n"
MF "v[224:227],a[212:215],a[104:107],v[224:227]\n"
"s_add_u32 s82,s66,4718848\n"
"ds_read_b128 a[192:195],v3 offset:47104\n"
MF "v[224:227],a[208:211],a[64:67],v[224:227]\n"
"s_sub_u32 s15,s82,7864320\n"
TR "a[204:205],v0 offset:4096\n"
MF "v[232:235],a[208:211],a[68:71],v[232:235]\n"
"s_min_u32 s82,s82,s15\n"
TR "a[206:207],v0 offset:4608\n"
MF "v[232:235],a[212:215],a[108:111],v[232:235]\n"
LD "v6,s[28:31],s82 offen lds\n"
MF "v[240:243],a[212:215],a[112:115],v[240:243]\n"
"ds_read_b128 a[156:159],v2 offset:49152\n"
MF "v[240:243],a[208:211],a[72:75],v[240:243]\n"
"ds_read_b128 a[196:199],v3 offset:49152\n"
MF "v[248:251],a[208:211],a[76:79],v[248:251]\n"
MF "v[248:251],a[212:215],a[116:119],v[248:251]\n"
LD "v11,s[28:31],s82 offen offset:1024 lds\n"
MF "v[252:255],a[220:223],a[116:119],v[252:255]\n"
TR "a[208:209],v0 offset:8\n"
MF "v[252:255],a[216:219],a[76:79],v[252:255]\n"
TR "a[210:211],v0 offset:520\n"
MF "v[244:247],a[216:219],a[72:75],v[244:247]\n"
MF "v[244:247],a[220:223],a[112:115],v[244:247]\n"
TR "a[212:213],v0 offset:4104\n"
MF "v[236:239],a[220:223],a[108:111],v[236:239]\n"
TR "a[214:215],v0 offset:4616\n"
MF "v[236:239],a[216:219],a[68:71],v[236:239]\n"
LD "v12,s[28:31],s82 offen offset:2048 lds\n"
MF "v[228:231],a[216:219],a[64:67],v[228:231]\n"
MF "v[228:231],a[220:223],a[104:107],v[228:231]\n"
MF "v[220:223],a[220:223],a[100:103],v[220:223]\n"
MF "v[220:223],a[216:219],a[60:63],v[220:223]\n"
"s_waitcnt lgkmcnt(4)\n"
LD "v13,s[28:31],s82 offen offset:3072 lds\n"
MF "v[16:19],a[200:203],a[140:143],v[16:19]\n"
TR "a[216:217],v1\n"
MF "v[16:19],a[204:207],a[180:183],v[16:19]\n"
TR "a[218:219],v1 offset:512\n"
MF "v[24:27],a[204:207],a[184:187],v[24:27]\n"
MF "v[24:27],a[200:203],a[144:147],v[24:27]\n"
"s_add_u32 s84,s14,40960\n"
TR "a[220:221],v1 offset:4096\n"
MF "v[32:35],a[200:203],a[148:151],v[32:35]\n"
"s_add_u32 m0,s63,24576\n"
TR "a[222:223],v1 offset:4608\n"
MF "v[32:35],a[204:207],a[188:191],v[32:35]\n"
LD "v4,s[24:27],s84 offen lds\n"
MF "v[40:43],a[204:207],a[192:195],v[40:43]\n"
MF "v[40:43],a[200:203],a[152:155],v[40:43]\n"
MF "v[48:51],a[200:203],a[156:159],v[48:51]\n"
MF "v[48:51],a[204:207],a[196:199],v[48:51]\n"
"s_waitcnt lgkmcnt(4)\n"
MF "v[52:55],a[212:215],a[196:199],v[52:55]\n"
TR "a[200:201],v1 offset:8\n"
MF "v[52:55],a[208:211],a[156:159],v[52:55]\n"
TR "a[202:203],v1 offset:520\n"
MF "v[44:47],a[208:211],a[152:155],v[44:47]\n"
MF "v[44:47],a[212:215],a[192:195],v[44:47]\n"
TR "a[204:205],v1 offset:4104\n"
MF "v[36:39],a[212:215],a[188:191],v[36:39]\n"
TR "a[206:207],v1 offset:4616\n"
MF "v[36:39],a[208:211],a[148:151],v[36:39]\n"
MF "v[28:31],a[208:211],a[144:147],v[28:31]\n"
MF "v[28:31],a[212:215],a[184:187],v[28:31]\n"
MF "v[20:23],a[212:215],a[180:183],v[20:23]\n"
"s_waitcnt vmcnt(25) lgkmcnt(0)\n"
MF "v[20:23],a[208:211],a[140:143],v[20:23]\n"
"s_barrier\n"
MF "v[56:59],a[216:219],a[140:143],v[56:59]\n"
"s_add_u32 m0,s62,49152\n"
"s_add_u32 s82,s66,5505024\n"
TR "a[208:209],v0 offset:16384\n"
MF "v[56:59],a[220:223],a[180:183],v[56:59]\n"
"s_sub_u32 s15,s82,7864320\n"
"s_min_u32 s82,s82,s15\n"
TR "a[210:211],v0 offset:16896\n"
MF "v[64:67],a[220:223],a[184:187],v[64:67]\n"
LD "v6,s[28:31],s82 offen lds\n"
MF "v[64:67],a[216:219],a[144:147],v[64:67]\n"
TR "a[212:213],v0 offset:20480\n"
MF "v[72:75],a[216:219],a[148:151],v[72:75]\n"
TR "a[214:215],v0 offset:20992\n"
MF "v[72:75],a[220:223],a[188:191],v[72:75]\n"
"buffer_load_dword a249,v15,s[48:51],s59 offen\n"
MF "v[80:83],a[220:223],a[192:195],v[80:83]\n"
"s_bfe_u32 s59,s23,0x30003\n"
MF "v[80:83],a[216:219],a[152:155],v[80:83]\n"
LD "v11,s[28:31],s82 offen offset:1024 lds\n"
MF "v[88:91],a[216:219],a[156:159],v[88:91]\n"
"s_add_u32 s59,s59,5\n"
MF "v[88:91],a[220:223],a[196:199],v[88:91]\n"
"s_lshl_b32 s59,s59,7\n"
MF "v[92:95],a[204:207],a[196:199],v[92:95]\n"
"s_and_b32 s59,s59,768\n"
TR "a[216:217],v0 offset:16392\n"
MF "v[92:95],a[200:203],a[156:159],v[92:95]\n"
"s_add_u32 s59,s59,s60\n"
TR "a[218:219],v0 offset:16904\n"
MF "v[84:87],a[200:203],a[152:155],v[84:87]\n"
LD "v12,s[28:31],s82 offen offset:2048 lds\n"
MF "v[84:87],a[204:207],a[192:195],v[84:87]\n"
TR "a[220:221],v0 offset:20488\n"
MF "v[76:79],a[204:207],a[188:191],v[76:79]\n"
TR "a[222:223],v0 offset:21000\n"
MF "v[76:79],a[200:203],a[148:151],v[76:79]\n"
MF "v[68:71],a[200:203],a[144:147],v[68:71]\n"
MF "v[68:71],a[204:207],a[184:187],v[68:71]\n"
".Lpg3a:\n"
"s_cbranch_execz .Lisr1\n"
LD "v13,s[28:31],s82 offen offset:3072 lds\n"
MF "v[60:63],a[204:207],a[180:183],v[60:63]\n"
MF "v[60:63],a[200:203],a[140:143],v[60:63]\n"
"s_add_u32 s14,s81,1024\n"
"s_sub_u32 s15,s14,1280\n"
"s_waitcnt lgkmcnt(4)\n"
MF "v[96:99],a[208:211],a[140:143],v[96:99]\n"
"s_min_u32 s14,s14,s15\n"
"s_add_u32 s14,s14,s68\n"
TR "a[200:201],v1 offset:16384\n"
MF "v[96:99],a[212:215],a[180:183],v[96:99]\n"
"s_add_u32 s84,s14,0\n"
"s_add_u32 m0,s63,40960\n"
TR "a[202:203],v1 offset:16896\n"
MF "v[104:107],a[212:215],a[184:187],v[104:107]\n"
LD "v4,s[24:27],s84 offen lds\n"
MF "v[104:107],a[208:211],a[144:147],v[104:107]\n"
TR "a[204:205],v1 offset:20480\n"
MF "v[112:115],a[208:211],a[148:151],v[112:115]\n"
TR "a[206:207],v1 offset:20992\n"
MF "v[112:115],a[212:215],a[188:191],v[112:115]\n"
MF "v[120:123],a[212:215],a[192:195],v[120:123]\n"
"s_add_u32 s84,s14,9216\n"
MF "v[120:123],a[208:211],a[152:155],v[120:123]\n"
LD "v5,s[24:27],s84 offen offset:1024 lds\n"
MF "v[128:131],a[208:211],a[156:159],v[128:131]\n"
MF "v[128:131],a[212:215],a[196:199],v[128:131]\n"
"s_waitcnt lgkmcnt(4)\n"
MF "v[132:135],a[220:223],a[196:199],v[132:135]\n"
TR "a[208:209],v1 offset:16392\n"
MF "v[132:135],a[216:219],a[156:159],v[132:135]\n"
TR "a[210:211],v1 offset:16904\n"
MF "v[124:127],a[216:219],a[152:155],v[124:127]\n"
MF "v[124:127],a[220:223],a[192:195],v[124:127]\n"
TR "a[212:213],v1 offset:20488\n"
MF "v[116:119],a[220:223],a[188:191],v[116:119]\n"
TR "a[214:215],v1 offset:21000\n"
MF "v[116:119],a[216:219],a[148:151],v[116:119]\n"
MF "v[108:111],a[216:219],a[144:147],v[108:111]\n"
MF "v[108:111],a[220:223],a[184:187],v[108:111]\n"
MF "v[100:103],a[220:223],a[180:183],v[100:103]\n"
"s_waitcnt vmcnt(25) lgkmcnt(0)\n"
MF "v[100:103],a[216:219],a[140:143],v[100:103]\n"
"s_barrier\n"
MF "v[136:139],a[200:203],a[140:143],v[136:139]\n"
TR "a[216:217],v0 offset:32768\n"
MF "v[136:139],a[204:207],a[180:183],v[136:139]\n"
"s_add_u32 m0,s62,65536\n"
TR "a[218:219],v0 offset:33280\n"
MF "v[144:147],a[204:207],a[184:187],v[144:147]\n"
"s_add_u32 s82,s66,5505152\n"
"ds_read_b128 a[60:63],v2\n"
MF "v[144:147],a[200:203],a[144:147],v[144:147]\n"
"s_sub_u32 s15,s82,7864320\n"
TR "a[220:221],v0 offset:36864\n"
MF "v[152:155],a[200:203],a[148:151],v[152:155]\n"
"s_min_u32 s82,s82,s15\n"
TR "a[222:223],v0 offset:37376\n"
MF "v[152:155],a[204:207],a[188:191],v[152:155]\n"
LD "v6,s[28:31],s82 offen lds\n"
MF "v[160:163],a[204:207],a[192:195],v[160:163]\n"
"ds_read_b128 a[100:103],v3\n"
MF "v[160:163],a[200:203],a[152:155],v[160:163]\n"
MF "v[168:171],a[200:203],a[156:159],v[168:171]\n"
MF "v[168:171],a[204:207],a[196:199],v[168:171]\n"
LD "v11,s[28:31],s82 offen offset:1024 lds\n"
MF "v[172:175],a[212:215],a[196:199],v[172:175]\n"
TR "a[200:201],v0 offset:32776\n"
MF "v[172:175],a[208:211],a[156:159],v[172:175]\n"
TR "a[202:203],v0 offset:33288\n"
MF "v[164:167],a[208:211],a[152:155],v[164:167]\n"
"ds_read_b128 a[64:67],v2 offset:2048\n"
MF "v[164:167],a[212:215],a[192:195],v[164:167]\n"
TR "a[204:205],v0 offset:36872\n"
MF "v[156:159],a[212:215],a[188:191],v[156:159]\n"
TR "a[206:207],v0 offset:37384\n"
MF "v[156:159],a[208:211],a[148:151],v[156:159]\n"
LD "v12,s[28:31],s82 offen offset:2048 lds\n"
MF "v[148:151],a[208:211],a[144:147],v[148:151]\n"
"ds_read_b128 a[104:107],v3 offset:2048\n"
MF "v[148:151],a[212:215],a[184:187],v[148:151]\n"
MF "v[140:143],a[212:215],a[180:183],v[140:143]\n"
MF "v[140:143],a[208:211],a[140:143],v[140:143]\n"
"s_waitcnt lgkmcnt(7)\n"
LD "v13,s[28:31],s82 offen offset:3072 lds\n"
MF "v[176:179],a[216:219],a[140:143],v[176:179]\n"
TR "a[208:209],v1 offset:32768\n"
MF "v[176:179],a[220:223],a[180:183],v[176:179]\n"
TR "a[210:211],v1 offset:33280\n"
MF "v[184:187],a[220:223],a[184:187],v[184:187]\n"
"ds_read_b128 a[68:71],v2 offset:4096\n"
MF "v[184:187],a[216:219],a[144:147],v[184:187]\n"
"s_add_u32 s84,s14,20480\n"
TR "a[212:213],v1 offset:36864\n"
MF "v[192:195],a[216:219],a[148:151],v[192:195]\n"
"s_add_u32 m0,s63,43008\n"
TR "a[214:215],v1 offset:37376\n"
MF "v[192:195],a[220:223],a[188:191],v[192:195]\n"
LD "v4,s[24:27],s84 offen lds\n"
MF "v[200:203],a[220:223],a[192:195],v[200:203]\n"
"ds_read_b128 a[108:111],v3 offset:4096\n"
MF "v[200:203],a[216:219],a[152:155],v[200:203]\n"
MF "v[208:211],a[216:219],a[156:159],v[208:211]\n"
"s_add_u32 s84,s14,29696\n"
MF "v[208:211],a[220:223],a[196:199],v[208:211]\n"
"s_waitcnt lgkmcnt(7)\n"
LD "v5,s[24:27],s84 offen offset:1024 lds\n"
MF "v[212:215],a[204:207],a[196:199],v[212:215]\n"
TR "a[216:217],v1 offset:32776\n"
MF "v[212:215],a[200:203],a[156:159],v[212:215]\n"
TR "a[218:219],v1 offset:33288\n"
MF "v[204:207],a[200:203],a[152:155],v[204:207]\n"
"ds_read_b128 a[72:75],v2 offset:6144\n"
MF "v[204:207],a[204:207],a[192:195],v[204:207]\n"
TR "a[220:221],v1 offset:36872\n"
MF "v[196:199],a[204:207],a[188:191],v[196:199]\n"
TR "a[222:223],v1 offset:37384\n"
MF "v[196:199],a[200:203],a[148:151],v[196:199]\n"
MF "v[188:191],a[200:203],a[144:147],v[188:191]\n"
"v_add_u32 v0,0xffff4000,v0\n"
MF "v[188:191],a[204:207],a[184:187],v[188:191]\n"
"v_add_u32 v1,0xffff4000,v1\n"
MF "v[180:183],a[204:207],a[180:183],v[180:183]\n"
"s_waitcnt vmcnt(26) lgkmcnt(0)\n"
MF "v[180:183],a[200:203],a[140:143],v[180:183]\n"
"s_barrier\n"
MF "v[216:219],a[208:211],a[140:143],v[216:219]\n"
TR "a[200:201],v0\n"
MF "v[216:219],a[212:215],a[180:183],v[216:219]\n"
"s_add_u32 m0,s62,81920\n"
TR "a[202:203],v0 offset:512\n"
MF "v[224:227],a[212:215],a[184:187],v[224:227]\n"
"s_add_u32 s82,s66,5505280\n"
"ds_read_b128 a[112:115],v3 offset:6144\n"
MF "v[224:227],a[208:211],a[144:147],v[224:227]\n"
"s_sub_u32 s15,s82,7864320\n"
TR "a[204:205],v0 offset:4096\n"
MF "v[232:235],a[208:211],a[148:151],v[232:235]\n"
"s_min_u32 s82,s82,s15\n"
TR "a[206:207],v0 offset:4608\n"
MF "v[232:235],a[212:215],a[188:191],v[232:235]\n"
LD "v6,s[28:31],s82 offen lds\n"
MF "v[240:243],a[212:215],a[192:195],v[240:243]\n"
"ds_read_b128 a[76:79],v2 offset:8192\n"
MF "v[240:243],a[208:211],a[152:155],v[240:243]\n"
"ds_read_b128 a[116:119],v3 offset:8192\n"
MF "v[248:251],a[208:211],a[156:159],v[248:251]\n"
MF "v[248:251],a[212:215],a[196:199],v[248:251]\n"
LD "v11,s[28:31],s82 offen offset:1024 lds\n"
MF "v[252:255],a[220:223],a[196:199],v[252:255]\n"
TR "a[208:209],v0 offset:8\n"
MF "v[252:255],a[216:219],a[156:159],v[252:255]\n"
TR "a[210:211],v0 offset:520\n"
MF "v[244:247],a[216:219],a[152:155],v[244:247]\n"
MF "v[244:247],a[220:223],a[192:195],v[244:247]\n"
TR "a[212:213],v0 offset:4104\n"
MF "v[236:239],a[220:223],a[188:191],v[236:239]\n"
TR "a[214:215],v0 offset:4616\n"
MF "v[236:239],a[216:219],a[148:151],v[236:239]\n"
LD "v12,s[28:31],s82 offen offset:2048 lds\n"
MF "v[228:231],a[216:219],a[144:147],v[228:231]\n"
MF "v[228:231],a[220:223],a[184:187],v[228:231]\n"
MF "v[220:223],a[220:223],a[180:183],v[220:223]\n"
MF "v[220:223],a[216:219],a[140:143],v[220:223]\n"
"s_waitcnt lgkmcnt(4)\n"
LD "v13,s[28:31],s82 offen offset:3072 lds\n"
MF "v[16:19],a[200:203],a[60:63],v[16:19]\n"
TR "a[216:217],v1\n"
MF "v[16:19],a[204:207],a[100:103],v[16:19]\n"
TR "a[218:219],v1 offset:512\n"
MF "v[24:27],a[204:207],a[104:107],v[24:27]\n"
MF "v[24:27],a[200:203],a[64:67],v[24:27]\n"
"s_add_u32 s84,s14,40960\n"
TR "a[220:221],v1 offset:4096\n"
MF "v[32:35],a[200:203],a[68:71],v[32:35]\n"
"s_add_u32 m0,s63,45056\n"
TR "a[222:223],v1 offset:4608\n"
MF "v[32:35],a[204:207],a[108:111],v[32:35]\n"
LD "v4,s[24:27],s84 offen lds\n"
MF "v[40:43],a[204:207],a[112:115],v[40:43]\n"
MF "v[40:43],a[200:203],a[72:75],v[40:43]\n"
MF "v[48:51],a[200:203],a[76:79],v[48:51]\n"
MF "v[48:51],a[204:207],a[116:119],v[48:51]\n"
"s_waitcnt lgkmcnt(4)\n"
MF "v[52:55],a[212:215],a[116:119],v[52:55]\n"
TR "a[200:201],v1 offset:8\n"
MF "v[52:55],a[208:211],a[76:79],v[52:55]\n"
TR "a[202:203],v1 offset:520\n"
MF "v[44:47],a[208:211],a[72:75],v[44:47]\n"
MF "v[44:47],a[212:215],a[112:115],v[44:47]\n"
TR "a[204:205],v1 offset:4104\n"
MF "v[36:39],a[212:215],a[108:111],v[36:39]\n"
TR "a[206:207],v1 offset:4616\n"
MF "v[36:39],a[208:211],a[68:71],v[36:39]\n"
MF "v[28:31],a[208:211],a[64:67],v[28:31]\n"
MF "v[28:31],a[212:215],a[104:107],v[28:31]\n"
MF "v[20:23],a[212:215],a[100:103],v[20:23]\n"
"s_waitcnt vmcnt(25) lgkmcnt(0)\n"
MF "v[20:23],a[208:211],a[60:63],v[20:23]\n"
"s_barrier\n"
MF "v[56:59],a[216:219],a[60:63],v[56:59]\n"
"s_add_u32 m0,s62,0\n"
"s_add_u32 s82,s66,6291712\n"
TR "a[208:209],v0 offset:16384\n"
MF "v[56:59],a[220:223],a[100:103],v[56:59]\n"
"s_sub_u32 s15,s82,7864320\n"
"s_min_u32 s82,s82,s15\n"
TR "a[210:211],v0 offset:16896\n"
MF "v[64:67],a[220:223],a[104:107],v[64:67]\n"
LD "v6,s[28:31],s82 offen lds\n"
MF "v[64:67],a[216:219],a[64:67],v[64:67]\n"
TR "a[212:213],v0 offset:20480\n"
MF "v[72:75],a[216:219],a[68:71],v[72:75]\n"
TR "a[214:215],v0 offset:20992\n"
MF "v[72:75],a[220:223],a[108:111],v[72:75]\n"
"buffer_load_dword a249,v15,s[48:51],s59 offen\n"
MF "v[80:83],a[220:223],a[112:115],v[80:83]\n"
MF "v[80:83],a[216:219],a[72:75],v[80:83]\n"
LD "v11,s[28:31],s82 offen offset:1024 lds\n"
MF "v[88:91],a[216:219],a[76:79],v[88:91]\n"
MF "v[88:91],a[220:223],a[116:119],v[88:91]\n"
MF "v[92:95],a[204:207],a[116:119],v[92:95]\n"
TR "a[216:217],v0 offset:16392\n"
MF "v[92:95],a[200:203],a[76:79],v[92:95]\n"
TR "a[218:219],v0 offset:16904\n"
MF "v[84:87],a[200:203],a[72:75],v[84:87]\n"
LD "v12,s[28:31],s82 offen offset:2048 lds\n"
MF "v[84:87],a[204:207],a[112:115],v[84:87]\n"
TR "a[220:221],v0 offset:20488\n"
MF "v[76:79],a[204:207],a[108:111],v[76:79]\n"
TR "a[222:223],v0 offset:21000\n"
MF "v[76:79],a[200:203],a[68:71],v[76:79]\n"
MF "v[68:71],a[200:203],a[64:67],v[68:71]\n"
MF "v[68:71],a[204:207],a[104:107],v[68:71]\n"
LD "v13,s[28:31],s82 offen offset:3072 lds\n"
MF "v[60:63],a[204:207],a[100:103],v[60:63]\n"
MF "v[60:63],a[200:203],a[60:63],v[60:63]\n"
"s_add_u32 s14,s81,1152\n"
"s_sub_u32 s15,s14,1280\n"
"s_waitcnt lgkmcnt(4)\n"
MF "v[96:99],a[208:211],a[60:63],v[96:99]\n"
"s_min_u32 s14,s14,s15\n"
"s_add_u32 s14,s14,s68\n"
TR "a[200:201],v1 offset:16384\n"
MF "v[96:99],a[212:215],a[100:103],v[96:99]\n"
"s_add_u32 s84,s14,0\n"
"s_add_u32 m0,s63,0\n"
TR "a[202:203],v1 offset:16896\n"
MF "v[104:107],a[212:215],a[104:107],v[104:107]\n"
LD "v4,s[24:27],s84 offen lds\n"
MF "v[104:107],a[208:211],a[64:67],v[104:107]\n"
TR "a[204:205],v1 offset:20480\n"
MF "v[112:115],a[208:211],a[68:71],v[112:115]\n"
TR "a[206:207],v1 offset:20992\n"
MF "v[112:115],a[212:215],a[108:111],v[112:115]\n"
MF "v[120:123],a[212:215],a[112:115],v[120:123]\n"
"s_add_u32 s84,s14,9216\n"
MF "v[120:123],a[208:211],a[72:75],v[120:123]\n"
LD "v5,s[24:27],s84 offen offset:1024 lds\n"
MF "v[128:131],a[208:211],a[76:79],v[128:131]\n"
MF "v[128:131],a[212:215],a[116:119],v[128:131]\n"
"s_waitcnt lgkmcnt(4)\n"
MF "v[132:135],a[220:223],a[116:119],v[132:135]\n"
TR "a[208:209],v1 offset:16392\n"
MF "v[132:135],a[216:219],a[76:79],v[132:135]\n"
"s_add_u32 s84,s14,18432\n"
TR "a[210:211],v1 offset:16904\n"
MF "v[124:127],a[216:219],a[72:75],v[124:127]\n"
LD "v4,s[24:27],s84 offen offset:2048 lds\n"
MF "v[124:127],a[220:223],a[112:115],v[124:127]\n"
TR "a[212:213],v1 offset:20488\n"
MF "v[116:119],a[220:223],a[108:111],v[116:119]\n"
TR "a[214:215],v1 offset:21000\n"
MF "v[116:119],a[216:219],a[68:71],v[116:119]\n"
MF "v[108:111],a[216:219],a[64:67],v[108:111]\n"
MF "v[108:111],a[220:223],a[104:107],v[108:111]\n"
MF "v[100:103],a[220:223],a[100:103],v[100:103]\n"
"s_waitcnt vmcnt(26) lgkmcnt(0)\n"
MF "v[100:103],a[216:219],a[60:63],v[100:103]\n"
"s_barrier\n"
MF "v[136:139],a[200:203],a[60:63],v[136:139]\n"
TR "a[216:217],v0 offset:32768\n"
MF "v[136:139],a[204:207],a[100:103],v[136:139]\n"
"s_add_u32 m0,s62,16384\n"
TR "a[218:219],v0 offset:33280\n"
MF "v[144:147],a[204:207],a[104:107],v[144:147]\n"
"s_add_u32 s82,s66,7078144\n"
"ds_read_b128 a[140:143],v2 offset:20480\n"
MF "v[144:147],a[200:203],a[64:67],v[144:147]\n"
"s_sub_u32 s15,s82,7864320\n"
TR "a[220:221],v0 offset:36864\n"
MF "v[152:155],a[200:203],a[68:71],v[152:155]\n"
"s_min_u32 s82,s82,s15\n"
TR "a[222:223],v0 offset:37376\n"
MF "v[152:155],a[204:207],a[108:111],v[152:155]\n"
LD "v6,s[28:31],s82 offen lds\n"
MF "v[160:163],a[204:207],a[112:115],v[160:163]\n"
"ds_read_b128 a[180:183],v3 offset:20480\n"
MF "v[160:163],a[200:203],a[72:75],v[160:163]\n"
MF "v[168:171],a[200:203],a[76:79],v[168:171]\n"
MF "v[168:171],a[204:207],a[116:119],v[168:171]\n"
LD "v11,s[28:31],s82 offen offset:1024 lds\n"
MF "v[172:175],a[212:215],a[116:119],v[172:175]\n"
TR "a[200:201],v0 offset:32776\n"
MF "v[172:175],a[208:211],a[76:79],v[172:175]\n"
TR "a[202:203],v0 offset:33288\n"
MF "v[164:167],a[208:211],a[72:75],v[164:167]\n"
"ds_read_b128 a[144:147],v2 offset:22528\n"
MF "v[164:167],a[212:215],a[112:115],v[164:167]\n"
TR "a[204:205],v0 offset:36872\n"
MF "v[156:159],a[212:215],a[108:111],v[156:159]\n"
TR "a[206:207],v0 offset:37384\n"
MF "v[156:159],a[208:211],a[68:71],v[156:159]\n"
LD "v12,s[28:31],s82 offen offset:2048 lds\n"
MF "v[148:151],a[208:211],a[64:67],v[148:151]\n"
"ds_read_b128 a[184:187],v3 offset:22528\n"
MF "v[148:151],a[212:215],a[104:107],v[148:151]\n"
MF "v[140:143],a[212:215],a[100:103],v[140:143]\n"
MF "v[140:143],a[208:211],a[60:63],v[140:143]\n"
"s_waitcnt lgkmcnt(7)\n"
LD "v13,s[28:31],s82 offen offset:3072 lds\n"
MF "v[176:179],a[216:219],a[60:63],v[176:179]\n"
TR "a[208:209],v1 offset:32768\n"
MF "v[176:179],a[220:223],a[100:103],v[176:179]\n"
TR "a[210:211],v1 offset:33280\n"
MF "v[184:187],a[220:223],a[104:107],v[184:187]\n"
"ds_read_b128 a[148:151],v2 offset:24576\n"
MF "v[184:187],a[216:219],a[64:67],v[184:187]\n"
"s_add_u32 s84,s14,30720\n"
TR "a[212:213],v1 offset:36864\n"
MF "v[192:195],a[216:219],a[68:71],v[192:195]\n"
"s_add_u32 m0,s63,3072\n"
TR "a[214:215],v1 offset:37376\n"
MF "v[192:195],a[220:223],a[108:111],v[192:195]\n"
LD "v5,s[24:27],s84 offen lds\n"
MF "v[200:203],a[220:223],a[112:115],v[200:203]\n"
"ds_read_b128 a[188:191],v3 offset:24576\n"
MF "v[200:203],a[216:219],a[72:75],v[200:203]\n"
MF "v[208:211],a[216:219],a[76:79],v[208:211]\n"
"s_add_u32 s84,s14,39936\n"
MF "v[208:211],a[220:223],a[116:119],v[208:211]\n"
"s_waitcnt lgkmcnt(7)\n"
LD "v4,s[24:27],s84 offen offset:1024 lds\n"
MF "v[212:215],a[204:207],a[116:119],v[212:215]\n"
TR "a[216:217],v1 offset:32776\n"
MF "v[212:215],a[200:203],a[76:79],v[212:215]\n"
TR "a[218:219],v1 offset:33288\n"
MF "v[204:207],a[200:203],a[72:75],v[204:207]\n"
"ds_read_b128 a[152:155],v2 offset:26624\n"
MF "v[204:207],a[204:207],a[112:115],v[204:207]\n"
TR "a[220:221],v1 offset:36872\n"
MF "v[196:199],a[204:207],a[108:111],v[196:199]\n"
TR "a[222:223],v1 offset:37384\n"
MF "v[196:199],a[200:203],a[68:71],v[196:199]\n"
MF "v[188:191],a[200:203],a[64:67],v[188:191]\n"
"v_add_u32 v0,0xc000,v0\n"
MF "v[188:191],a[204:207],a[104:107],v[188:191]\n"
"v_add_u32 v1,0xc000,v1\n"
MF "v[180:183],a[204:207],a[100:103],v[180:183]\n"
"s_waitcnt vmcnt(27) lgkmcnt(0)\n"
"s_branch .Lqj\n"
".Lpgq0_c:\n"
"s_cbranch_execz .Lpgqy_c\n"
".Lpgq0_f:\n"
"s_cbranch_execz .Lpg12f\n"
".Lq0:\n"
MF "v[16:19],a[200:203],a[60:63],0\n"
"s_add_u32 m0,s62,49152\n"
"s_add_u32 s82,s66,786432\n"
TR "a[216:217],v1\n"
MF "v[16:19],a[204:207],a[100:103],v[16:19]\n"
"s_sub_u32 s15,s82,7864320\n"
"s_min_u32 s82,s82,s15\n"
TR "a[218:219],v1 offset:512\n"
MF "v[24:27],a[204:207],a[104:107],0\n"
LD "v6,s[28:31],s82 offen lds\n"
MF "v[24:27],a[200:203],a[64:67],v[24:27]\n"
TR "a[220:221],v1 offset:4096\n"
MF "v[32:35],a[200:203],a[68:71],0\n"
TR "a[222:223],v1 offset:4608\n"
MF "v[32:35],a[204:207],a[108:111],v[32:35]\n"
"v_max3_f32 v220,|v16|,|v17|,|v18|\n"
"v_max_f32_e64 v220,v220,|v19|\n"
MF "v[40:43],a[204:207],a[112:115],0\n"
"v_readlane_b32 s12,v220,3\n"
"v_readlane_b32 s6,v220,22\n"
MF "v[40:43],a[200:203],a[72:75],v[40:43]\n"
"s_min_u32 s12,s12,s6\n"
"v_readlane_b32 s7,v220,41\n"
"v_readlane_b32 s8,v220,60\n"
MF "v[48:51],a[200:203],a[76:79],0\n"
"s_min_u32 s7,s7,s8\n"
MF "v[48:51],a[204:207],a[116:119],v[48:51]\n"
LD "v11,s[28:31],s82 offen offset:1024 lds\n"
MF "v[52:55],a[212:215],a[116:119],0\n"
"s_min_u32 s12,s12,s7\n"
TR "a[200:201],v1 offset:8\n"
MF "v[52:55],a[208:211],a[76:79],v[52:55]\n"
"s_and_b32 s12,s12,0x7f800000\n"
TR "a[202:203],v1 offset:520\n"
MF "v[44:47],a[208:211],a[72:75],0\n"
"s_sub_u32 s12,s12,0x14000000\n"
MF "v[44:47],a[212:215],a[112:115],v[44:47]\n"
"s_cmp_lt_u32 s12,0x50000000\n"
"s_cselect_b32 s12,s12,0xe8400000\n"
TR "a[204:205],v1 offset:4104\n"
MF "v[36:39],a[212:215],a[108:111],0\n"
"s_add_u32 s12,s12,0x17c00000\n"
TR "a[206:207],v1 offset:4616\n"
MF "v[36:39],a[208:211],a[68:71],v[36:39]\n"
"v_mov_b32 v220,s12\n"
"v_mov_b32 v221,s12\n"
MF "v[28:31],a[208:211],a[64:67],0\n"
"v_mov_b32 v222,s12\n"
"v_mov_b32 v223,s12\n"
MF "v[28:31],a[212:215],a[104:107],v[28:31]\n"
LD "v12,s[28:31],s82 offen offset:2048 lds\n"
MF "v[20:23],a[212:215],a[100:103],0\n"
"s_waitcnt vmcnt(7) lgkmcnt(0)\n"
MF "v[20:23],a[208:211],a[60:63],v[20:23]\n"
"s_barrier\n"
MF "v[56:59],a[216:219],a[60:63],v[220:223]\n"
TR "a[208:209],v0 offset:16384\n"
MF "v[56:59],a[220:223],a[100:103],v[56:59]\n"
TR "a[210:211],v0 offset:16896\n"
MF "v[64:67],a[220:223],a[104:107],v[220:223]\n"
LD "v13,s[28:31],s82 offen offset:3072 lds\n"
MF "v[64:67],a[216:219],a[64:67],v[64:67]\n"
"s_add_u32 m0,s62,65536\n"
TR "a[212:213],v0 offset:20480\n"
MF "v[72:75],a[216:219],a[68:71],v[220:223]\n"
"s_add_u32 s82,s66,786560\n"
TR "a[214:215],v0 offset:20992\n"
MF "v[72:75],a[220:223],a[108:111],v[72:75]\n"
"s_sub_u32 s15,s82,7864320\n"
MF "v[80:83],a[220:223],a[112:115],v[220:223]\n"
"s_min_u32 s82,s82,s15\n"
MF "v[80:83],a[216:219],a[72:75],v[80:83]\n"
LD "v6,s[28:31],s82 offen lds\n"
MF "v[88:91],a[216:219],a[76:79],v[220:223]\n"
MF "v[88:91],a[220:223],a[116:119],v[88:91]\n"
MF "v[92:95],a[204:207],a[116:119],v[220:223]\n"
TR "a[216:217],v0 offset:16392\n"
MF "v[92:95],a[200:203],a[76:79],v[92:95]\n"
TR "a[218:219],v0 offset:16904\n"
MF "v[84:87],a[200:203],a[72:75],v[220:223]\n"
LD "v11,s[28:31],s82 offen offset:1024 lds\n"
MF "v[84:87],a[204:207],a[112:115],v[84:87]\n"
TR "a[220:221],v0 offset:20488\n"
MF "v[76:79],a[204:207],a[108:111],v[220:223]\n"
TR "a[222:223],v0 offset:21000\n"
MF "v[76:79],a[200:203],a[68:71],v[76:79]\n"
MF "v[68:71],a[200:203],a[64:67],v[220:223]\n"
MF "v[68:71],a[204:207],a[104:107],v[68:71]\n"
LD "v12,s[28:31],s82 offen offset:2048 lds\n"
MF "v[60:63],a[204:207],a[100:103],v[220:223]\n"
MF "v[60:63],a[200:203],a[60:63],v[60:63]\n"
"s_waitcnt lgkmcnt(4)\n"
MF "v[96:99],a[208:211],a[60:63],v[220:223]\n"
TR "a[200:201],v1 offset:16384\n"
MF "v[96:99],a[212:215],a[100:103],v[96:99]\n"
TR "a[202:203],v1 offset:16896\n"
MF "v[104:107],a[212:215],a[104:107],v[220:223]\n"
LD "v13,s[28:31],s82 offen offset:3072 lds\n"
MF "v[104:107],a[208:211],a[64:67],v[104:107]\n"
"s_add_u32 m0,s62,81920\n"
TR "a[204:205],v1 offset:20480\n"
MF "v[112:115],a[208:211],a[68:71],v[220:223]\n"
"s_add_u32 s82,s66,786688\n"
TR "a[206:207],v1 offset:20992\n"
MF "v[112:115],a[212:215],a[108:111],v[112:115]\n"
"s_sub_u32 s15,s82,7864320\n"
MF "v[120:123],a[212:215],a[112:115],v[220:223]\n"
"s_min_u32 s82,s82,s15\n"
MF "v[120:123],a[208:211],a[72:75],v[120:123]\n"
LD "v6,s[28:31],s82 offen lds\n"
MF "v[128:131],a[208:211],a[76:79],v[220:223]\n"
MF "v[128:131],a[212:215],a[116:119],v[128:131]\n"
"s_waitcnt lgkmcnt(4)\n"
MF "v[132:135],a[220:223],a[116:119],v[220:223]\n"
TR "a[208:209],v1 offset:16392\n"
MF "v[132:135],a[216:219],a[76:79],v[132:135]\n"
TR "a[210:211],v1 offset:16904\n"
MF "v[124:127],a[216:219],a[72:75],v[220:223]\n"
LD "v11,s[28:31],s82 offen offset:1024 lds\n"
MF "v[124:127],a[220:223],a[112:115],v[124:127]\n"
TR "a[212:213],v1 offset:20488\n"
MF "v[116:119],a[220:223],a[108:111],v[220:223]\n"
TR "a[214:215],v1 offset:21000\n"
MF "v[116:119],a[216:219],a[68:71],v[116:119]\n"
MF "v[108:111],a[216:219],a[64:67],v[220:223]\n"
MF "v[108:111],a[220:223],a[104:107],v[108:111]\n"
LD "v12,s[28:31],s82 offen offset:2048 lds\n"
MF "v[100:103],a[220:223],a[100:103],v[220:223]\n"
"s_waitcnt vmcnt(11) lgkmcnt(0)\n"
MF "v[100:103],a[216:219],a[60:63],v[100:103]\n"
"s_barrier\n"
MF "v[136:139],a[200:203],a[60:63],v[220:223]\n"
TR "a[216:217],v0 offset:32768\n"
MF "v[136:139],a[204:207],a[100:103],v[136:139]\n"
TR "a[218:219],v0 offset:33280\n"
MF "v[144:147],a[204:207],a[104:107],v[220:223]\n"
"ds_read_b128 a[140:143],v2 offset:20480\n"
MF "v[144:147],a[200:203],a[64:67],v[144:147]\n"
TR "a[220:221],v0 offset:36864\n"
MF "v[152:155],a[200:203],a[68:71],v[220:223]\n"
TR "a[222:223],v0 offset:37376\n"
MF "v[152:155],a[204:207],a[108:111],v[152:155]\n"
LD "v13,s[28:31],s82 offen offset:3072 lds\n"
MF "v[160:163],a[204:207],a[112:115],v[220:223]\n"
"ds_read_b128 a[180:183],v3 offset:20480\n"
MF "v[160:163],a[200:203],a[72:75],v[160:163]\n"
"s_add_u32 m0,s62,0\n"
MF "v[168:171],a[200:203],a[76:79],v[220:223]\n"
"s_add_u32 s82,s66,1572864\n"
"s_sub_u32 s15,s82,7864320\n"
"s_min_u32 s82,s82,s15\n"
MF "v[168:171],a[204:207],a[116:119],v[168:171]\n"
LD "v6,s[28:31],s82 offen lds\n"
MF "v[172:175],a[212:215],a[116:119],v[220:223]\n"
TR "a[200:201],v0 offset:32776\n"
MF "v[172:175],a[208:211],a[76:79],v[172:175]\n"
TR "a[202:203],v0 offset:33288\n"
MF "v[164:167],a[208:211],a[72:75],v[220:223]\n"
"ds_read_b128 a[144:147],v2 offset:22528\n"
MF "v[164:167],a[212:215],a[112:115],v[164:167]\n"
TR "a[204:205],v0 offset:36872\n"
MF "v[156:159],a[212:215],a[108:111],v[220:223]\n"
TR "a[206:207],v0 offset:37384\n"
MF "v[156:159],a[208:211],a[68:71],v[156:159]\n"
LD "v11,s[28:31],s82 offen offset:1024 lds\n"
MF "v[148:151],a[208:211],a[64:67],v[220:223]\n"
"ds_read_b128 a[184:187],v3 offset:22528\n"
MF "v[148:151],a[212:215],a[104:107],v[148:151]\n"
MF "v[140:143],a[212:215],a[100:103],v[220:223]\n"
MF "v[140:143],a[208:211],a[60:63],v[140:143]\n"
"s_waitcnt lgkmcnt(7)\n"
LD "v12,s[28:31],s82 offen offset:2048 lds\n"
MF "v[176:179],a[216:219],a[60:63],v[220:223]\n"
TR "a[208:209],v1 offset:32768\n"
MF "v[176:179],a[220:223],a[100:103],v[176:179]\n"
".Lpg4c:\n"
"s_cbranch_execz .Lpg14c\n"
".Lpg4f:\n"
"s_cbranch_execz .Lpg13f\n"
TR "a[210:211],v1 offset:33280\n"
MF "v[184:187],a[220:223],a[104:107],v[220:223]\n"
"ds_read_b128 a[148:151],v2 offset:24576\n"
MF "v[184:187],a[216:219],a[64:67],v[184:187]\n"
TR "a[212:213],v1 offset:36864\n"
MF "v[192:195],a[216:219],a[68:71],v[220:223]\n"
TR "a[214:215],v1 offset:37376\n"
MF "v[192:195],a[220:223],a[108:111],v[192:195]\n"
LD "v13,s[28:31],s82 offen offset:3072 lds\n"
MF "v[200:203],a[220:223],a[112:115],v[220:223]\n"
"ds_read_b128 a[188:191],v3 offset:24576\n"
MF "v[200:203],a[216:219],a[72:75],v[200:203]\n"
"s_add_u32 s14,s81,384\n"
"s_sub_u32 s15,s14,1280\n"
"s_min_u32 s14,s14,s15\n"
MF "v[208:211],a[216:219],a[76:79],v[220:223]\n"
"s_add_u32 s14,s14,s68\n"
"s_add_u32 s84,s14,0\n"
"s_add_u32 m0,s63,0\n"
MF "v[208:211],a[220:223],a[116:119],v[208:211]\n"
"s_waitcnt lgkmcnt(7)\n"
LD "v4,s[24:27],s84 offen lds\n"
MF "v[212:215],a[204:207],a[116:119],v[220:223]\n"
TR "a[216:217],v1 offset:32776\n"
MF "v[212:215],a[200:203],a[76:79],v[212:215]\n"
TR "a[218:219],v1 offset:33288\n"
MF "v[204:207],a[200:203],a[72:75],v[220:223]\n"
"ds_read_b128 a[152:155],v2 offset:26624\n"
MF "v[204:207],a[204:207],a[112:115],v[204:207]\n"
TR "a[220:221],v1 offset:36872\n"
MF "v[196:199],a[204:207],a[108:111],v[220:223]\n"
"s_add_u32 s84,s14,9216\n"
TR "a[222:223],v1 offset:37384\n"
MF "v[196:199],a[200:203],a[68:71],v[196:199]\n"
LD "v5,s[24:27],s84 offen offset:1024 lds\n"
MF "v[188:191],a[200:203],a[64:67],v[220:223]\n"
"s_add_u32 m0,s62,16384\n"
"s_add_u32 s82,s66,1572992\n"
"v_add_u32 v0,0xc000,v0\n"
MF "v[188:191],a[204:207],a[104:107],v[188:191]\n"
"s_sub_u32 s15,s82,7864320\n"
"s_min_u32 s82,s82,s15\n"
"v_add_u32 v1,0xc000,v1\n"
MF "v[180:183],a[204:207],a[100:103],v[220:223]\n"
"s_waitcnt vmcnt(14) lgkmcnt(0)\n"
LD "v6,s[28:31],s82 offen lds\n"
MF "v[180:183],a[200:203],a[60:63],v[180:183]\n"
"s_barrier\n"
MF "v[216:219],a[208:211],a[60:63],v[220:223]\n"
TR "a[200:201],v0\n"
MF "v[216:219],a[212:215],a[100:103],v[216:219]\n"
TR "a[202:203],v0 offset:512\n"
MF "v[224:227],a[212:215],a[104:107],v[220:223]\n"
"ds_read_b128 a[192:195],v3 offset:26624\n"
MF "v[224:227],a[208:211],a[64:67],v[224:227]\n"
TR "a[204:205],v0 offset:4096\n"
MF "v[232:235],a[208:211],a[68:71],v[220:223]\n"
TR "a[206:207],v0 offset:4608\n"
MF "v[232:235],a[212:215],a[108:111],v[232:235]\n"
LD "v11,s[28:31],s82 offen offset:1024 lds\n"
MF "v[240:243],a[212:215],a[112:115],v[220:223]\n"
"ds_read_b128 a[156:159],v2 offset:28672\n"
MF "v[240:243],a[208:211],a[72:75],v[240:243]\n"
"ds_read_b128 a[196:199],v3 offset:28672\n"
MF "v[248:251],a[208:211],a[76:79],v[220:223]\n"
MF "v[248:251],a[212:215],a[116:119],v[248:251]\n"
LD "v12,s[28:31],s82 offen offset:2048 lds\n"
MF "v[252:255],a[220:223],a[116:119],v[220:223]\n"
TR "a[208:209],v0 offset:8\n"
MF "v[252:255],a[216:219],a[76:79],v[252:255]\n"
TR "a[210:211],v0 offset:520\n"
MF "v[244:247],a[216:219],a[72:75],v[220:223]\n"
MF "v[244:247],a[220:223],a[112:115],v[244:247]\n"
TR "a[212:213],v0 offset:4104\n"
MF "v[236:239],a[220:223],a[108:111],v[220:223]\n"
TR "a[214:215],v0 offset:4616\n"
MF "v[236:239],a[216:219],a[68:71],v[236:239]\n"
LD "v13,s[28:31],s82 offen offset:3072 lds\n"
MF "v[228:231],a[216:219],a[64:67],v[220:223]\n"
MF "v[228:231],a[220:223],a[104:107],v[228:231]\n"
"s_add_u32 s84,s14,20480\n"
MF "v[220:223],a[220:223],a[100:103],v[220:223]\n"
"s_add_u32 m0,s63,2048\n"
MF "v[220:223],a[216:219],a[60:63],v[220:223]\n"
"s_waitcnt lgkmcnt(4)\n"
LD "v4,s[24:27],s84 offen lds\n"
MF "v[16:19],a[200:203],a[140:143],v[16:19]\n"
TR "a[216:217],v1\n"
MF "v[16:19],a[204:207],a[180:183],v[16:19]\n"
TR "a[218:219],v1 offset:512\n"
MF "v[24:27],a[204:207],a[184:187],v[24:27]\n"
MF "v[24:27],a[200:203],a[144:147],v[24:27]\n"
TR "a[220:221],v1 offset:4096\n"
MF "v[32:35],a[200:203],a[148:151],v[32:35]\n"
"s_add_u32 s84,s14,29696\n"
TR "a[222:223],v1 offset:4608\n"
MF "v[32:35],a[204:207],a[188:191],v[32:35]\n"
LD "v5,s[24:27],s84 offen offset:1024 lds\n"
MF "v[40:43],a[204:207],a[192:195],v[40:43]\n"
MF "v[40:43],a[200:203],a[152:155],v[40:43]\n"
"s_add_u32 m0,s62,32768\n"
MF "v[48:51],a[200:203],a[156:159],v[48:51]\n"
"s_add_u32 s82,s66,1573120\n"
"s_sub_u32 s15,s82,7864320\n"
"s_min_u32 s82,s82,s15\n"
MF "v[48:51],a[204:207],a[196:199],v[48:51]\n"
"s_waitcnt lgkmcnt(4)\n"
LD "v6,s[28:31],s82 offen lds\n"
MF "v[52:55],a[212:215],a[196:199],v[52:55]\n"
TR "a[200:201],v1 offset:8\n"
MF "v[52:55],a[208:211],a[156:159],v[52:55]\n"
TR "a[202:203],v1 offset:520\n"
MF "v[44:47],a[208:211],a[152:155],v[44:47]\n"
MF "v[44:47],a[212:215],a[192:195],v[44:47]\n"
TR "a[204:205],v1 offset:4104\n"
MF "v[36:39],a[212:215],a[188:191],v[36:39]\n"
TR "a[206:207],v1 offset:4616\n"
MF "v[36:39],a[208:211],a[148:151],v[36:39]\n"
LD "v11,s[28:31],s82 offen offset:1024 lds\n"
MF "v[28:31],a[208:211],a[144:147],v[28:31]\n"
MF "v[28:31],a[212:215],a[184:187],v[28:31]\n"
MF "v[20:23],a[212:215],a[180:183],v[20:23]\n"
"s_waitcnt vmcnt(18) lgkmcnt(0)\n"
LD "v12,s[28:31],s82 offen offset:2048 lds\n"
"s_bitcmp0_b32 s23,12\n"
"s_cbranch_scc1 .Lisx2\n"
"s_mov_b64 exec,0\n"
"s_cmp_eq_u32 s56,0\n"
"s_cbranch_scc1 .Lpg11d\n"
"s_cmp_eq_u32 s56,1\n"
"s_cbranch_scc1 .Lpg10d\n"
"s_cmp_eq_u32 s56,2\n"
"s_cbranch_scc1 .Lpgt0kx_d\n"
"s_branch .Lpg8d\n"
".Lisr2:\n"
"s_mov_b64 exec,-1\n"
".Lisx2:\n"
MF "v[20:23],a[208:211],a[140:143],v[20:23]\n"
"s_barrier\n"
MF "v[56:59],a[216:219],a[140:143],v[56:59]\n"
TR "a[208:209],v0 offset:16384\n"
MF "v[56:59],a[220:223],a[180:183],v[56:59]\n"
TR "a[210:211],v0 offset:16896\n"
MF "v[64:67],a[220:223],a[184:187],v[64:67]\n"
LD "v13,s[28:31],s82 offen offset:3072 lds\n"
MF "v[64:67],a[216:219],a[144:147],v[64:67]\n"
TR "a[212:213],v0 offset:20480\n"
MF "v[72:75],a[216:219],a[148:151],v[72:75]\n"
TR "a[214:215],v0 offset:20992\n"
MF "v[72:75],a[220:223],a[188:191],v[72:75]\n"
"s_add_u32 s84,s14,40960\n"
MF "v[80:83],a[220:223],a[192:195],v[80:83]\n"
"s_add_u32 m0,s63,4096\n"
MF "v[80:83],a[216:219],a[152:155],v[80:83]\n"
LD "v4,s[24:27],s84 offen lds\n"
MF "v[88:91],a[216:219],a[156:159],v[88:91]\n"
MF "v[88:91],a[220:223],a[196:199],v[88:91]\n"
"s_sub_u32 s66,s66,s83\n"
MF "v[92:95],a[204:207],a[196:199],v[92:95]\n"
"s_add_u32 m0,s62,49152\n"
"s_add_u32 s82,s66,2359296\n"
TR "a[216:217],v0 offset:16392\n"
MF "v[92:95],a[200:203],a[156:159],v[92:95]\n"
"s_sub_u32 s15,s82,7864320\n"
"s_min_u32 s82,s82,s15\n"
TR "a[218:219],v0 offset:16904\n"
MF "v[84:87],a[200:203],a[152:155],v[84:87]\n"
LD "v6,s[28:31],s82 offen lds\n"
MF "v[84:87],a[204:207],a[192:195],v[84:87]\n"
TR "a[220:221],v0 offset:20488\n"
MF "v[76:79],a[204:207],a[188:191],v[76:79]\n"
TR "a[222:223],v0 offset:21000\n"
MF "v[76:79],a[200:203],a[148:151],v[76:79]\n"
MF "v[68:71],a[200:203],a[144:147],v[68:71]\n"
MF "v[68:71],a[204:207],a[184:187],v[68:71]\n"
LD "v11,s[28:31],s82 offen offset:1024 lds\n"
MF "v[60:63],a[204:207],a[180:183],v[60:63]\n"
MF "v[60:63],a[200:203],a[140:143],v[60:63]\n"
"s_waitcnt lgkmcnt(4)\n"
MF "v[96:99],a[208:211],a[140:143],v[96:99]\n"
TR "a[200:201],v1 offset:16384\n"
MF "v[96:99],a[212:215],a[180:183],v[96:99]\n"
TR "a[202:203],v1 offset:16896\n"
MF "v[104:107],a[212:215],a[184:187],v[104:107]\n"
LD "v12,s[28:31],s82 offen offset:2048 lds\n"
MF "v[104:107],a[208:211],a[144:147],v[104:107]\n"
TR "a[204:205],v1 offset:20480\n"
MF "v[112:115],a[208:211],a[148:151],v[112:115]\n"
TR "a[206:207],v1 offset:20992\n"
MF "v[112:115],a[212:215],a[188:191],v[112:115]\n"
MF "v[120:123],a[212:215],a[192:195],v[120:123]\n"
MF "v[120:123],a[208:211],a[152:155],v[120:123]\n"
LD "v13,s[28:31],s82 offen offset:3072 lds\n"
MF "v[128:131],a[208:211],a[156:159],v[128:131]\n"
MF "v[128:131],a[212:215],a[196:199],v[128:131]\n"
"s_add_u32 s14,s81,512\n"
"s_sub_u32 s15,s14,1280\n"
"s_waitcnt lgkmcnt(4)\n"
MF "v[132:135],a[220:223],a[196:199],v[132:135]\n"
"s_min_u32 s14,s14,s15\n"
"s_add_u32 s14,s14,s68\n"
TR "a[208:209],v1 offset:16392\n"
MF "v[132:135],a[216:219],a[156:159],v[132:135]\n"
"s_add_u32 s84,s14,0\n"
"s_add_u32 m0,s63,20480\n"
TR "a[210:211],v1 offset:16904\n"
MF "v[124:127],a[216:219],a[152:155],v[124:127]\n"
LD "v4,s[24:27],s84 offen lds\n"
MF "v[124:127],a[220:223],a[192:195],v[124:127]\n"
TR "a[212:213],v1 offset:20488\n"
MF "v[116:119],a[220:223],a[188:191],v[116:119]\n"
TR "a[214:215],v1 offset:21000\n"
MF "v[116:119],a[216:219],a[148:151],v[116:119]\n"
MF "v[108:111],a[216:219],a[144:147],v[108:111]\n"
"s_add_u32 s84,s14,9216\n"
MF "v[108:111],a[220:223],a[184:187],v[108:111]\n"
LD "v5,s[24:27],s84 offen offset:1024 lds\n"
MF "v[100:103],a[220:223],a[180:183],v[100:103]\n"
"s_waitcnt vmcnt(23) lgkmcnt(0)\n"
MF "v[100:103],a[216:219],a[140:143],v[100:103]\n"
"s_barrier\n"
MF "v[136:139],a[200:203],a[140:143],v[136:139]\n"
TR "a[216:217],v0 offset:32768\n"
MF "v[136:139],a[204:207],a[180:183],v[136:139]\n"
"s_add_u32 m0,s62,65536\n"
TR "a[218:219],v0 offset:33280\n"
MF "v[144:147],a[204:207],a[184:187],v[144:147]\n"
"s_add_u32 s82,s66,2359424\n"
"ds_read_b128 a[60:63],v2 offset:40960\n"
MF "v[144:147],a[200:203],a[144:147],v[144:147]\n"
"s_sub_u32 s15,s82,7864320\n"
TR "a[220:221],v0 offset:36864\n"
MF "v[152:155],a[200:203],a[148:151],v[152:155]\n"
"s_min_u32 s82,s82,s15\n"
TR "a[222:223],v0 offset:37376\n"
MF "v[152:155],a[204:207],a[188:191],v[152:155]\n"
LD "v6,s[28:31],s82 offen lds\n"
MF "v[160:163],a[204:207],a[192:195],v[160:163]\n"
"ds_read_b128 a[100:103],v3 offset:40960\n"
MF "v[160:163],a[200:203],a[152:155],v[160:163]\n"
LD "v11,s[28:31],s82 offen offset:1024 lds\n"
MF "v[168:171],a[200:203],a[156:159],v[168:171]\n"
MF "v[168:171],a[204:207],a[196:199],v[168:171]\n"
LD "v12,s[28:31],s82 offen offset:2048 lds\n"
MF "v[172:175],a[212:215],a[196:199],v[172:175]\n"
TR "a[200:201],v0 offset:32776\n"
MF "v[172:175],a[208:211],a[156:159],v[172:175]\n"
TR "a[202:203],v0 offset:33288\n"
MF "v[164:167],a[208:211],a[152:155],v[164:167]\n"
"ds_read_b128 a[64:67],v2 offset:43008\n"
MF "v[164:167],a[212:215],a[192:195],v[164:167]\n"
TR "a[204:205],v0 offset:36872\n"
MF "v[156:159],a[212:215],a[188:191],v[156:159]\n"
TR "a[206:207],v0 offset:37384\n"
MF "v[156:159],a[208:211],a[148:151],v[156:159]\n"
MF "v[148:151],a[208:211],a[144:147],v[148:151]\n"
"ds_read_b128 a[104:107],v3 offset:43008\n"
MF "v[148:151],a[212:215],a[184:187],v[148:151]\n"
MF "v[140:143],a[212:215],a[180:183],v[140:143]\n"
LD "v13,s[28:31],s82 offen offset:3072 lds\n"
MF "v[140:143],a[208:211],a[140:143],v[140:143]\n"
"s_waitcnt lgkmcnt(7)\n"
MF "v[176:179],a[216:219],a[140:143],v[176:179]\n"
TR "a[208:209],v1 offset:32768\n"
MF "v[176:179],a[220:223],a[180:183],v[176:179]\n"
TR "a[210:211],v1 offset:33280\n"
MF "v[184:187],a[220:223],a[184:187],v[184:187]\n"
"ds_read_b128 a[68:71],v2 offset:45056\n"
MF "v[184:187],a[216:219],a[144:147],v[184:187]\n"
"s_add_u32 s84,s14,20480\n"
TR "a[212:213],v1 offset:36864\n"
MF "v[192:195],a[216:219],a[148:151],v[192:195]\n"
"s_add_u32 m0,s63,22528\n"
TR "a[214:215],v1 offset:37376\n"
MF "v[192:195],a[220:223],a[188:191],v[192:195]\n"
LD "v4,s[24:27],s84 offen lds\n"
MF "v[200:203],a[220:223],a[192:195],v[200:203]\n"
"ds_read_b128 a[108:111],v3 offset:45056\n"
MF "v[200:203],a[216:219],a[152:155],v[200:203]\n"
MF "v[208:211],a[216:219],a[156:159],v[208:211]\n"
"s_add_u32 s84,s14,29696\n"
MF "v[208:211],a[220:223],a[196:199],v[208:211]\n"
"s_waitcnt lgkmcnt(7)\n"
LD "v5,s[24:27],s84 offen offset:1024 lds\n"
MF "v[212:215],a[204:207],a[196:199],v[212:215]\n"
TR "a[216:217],v1 offset:32776\n"
MF "v[212:215],a[200:203],a[156:159],v[212:215]\n"
TR "a[218:219],v1 offset:33288\n"
MF "v[204:207],a[200:203],a[152:155],v[204:207]\n"
"ds_read_b128 a[72:75],v2 offset:47104\n"
MF "v[204:207],a[204:207],a[192:195],v[204:207]\n"
TR "a[220:221],v1 offset:36872\n"
MF "v[196:199],a[204:207],a[188:191],v[196:199]\n"
TR "a[222:223],v1 offset:37384\n"
MF "v[196:199],a[200:203],a[148:151],v[196:199]\n"
MF "v[188:191],a[200:203],a[144:147],v[188:191]\n"
"v_add_u32 v0,0xffff4000,v0\n"
MF "v[188:191],a[204:207],a[184:187],v[188:191]\n"
"v_add_u32 v1,0xffff4000,v1\n"
MF "v[180:183],a[204:207],a[180:183],v[180:183]\n"
"s_waitcnt vmcnt(25) lgkmcnt(0)\n"
MF "v[180:183],a[200:203],a[140:143],v[180:183]\n"
"s_barrier\n"
MF "v[216:219],a[208:211],a[140:143],v[216:219]\n"
TR "a[200:201],v0\n"
MF "v[216:219],a[212:215],a[180:183],v[216:219]\n"
"s_add_u32 m0,s62,81920\n"
TR "a[202:203],v0 offset:512\n"
MF "v[224:227],a[212:215],a[184:187],v[224:227]\n"
"s_add_u32 s82,s66,2359552\n"
"ds_read_b128 a[112:115],v3 offset:47104\n"
MF "v[224:227],a[208:211],a[144:147],v[224:227]\n"
"s_sub_u32 s15,s82,7864320\n"
TR "a[204:205],v0 offset:4096\n"
MF "v[232:235],a[208:211],a[148:151],v[232:235]\n"
"s_min_u32 s82,s82,s15\n"
TR "a[206:207],v0 offset:4608\n"
MF "v[232:235],a[212:215],a[188:191],v[232:235]\n"
LD "v6,s[28:31],s82 offen lds\n"
MF "v[240:243],a[212:215],a[192:195],v[240:243]\n"
"ds_read_b128 a[76:79],v2 offset:49152\n"
MF "v[240:243],a[208:211],a[152:155],v[240:243]\n"
"ds_read_b128 a[116:119],v3 offset:49152\n"
MF "v[248:251],a[208:211],a[156:159],v[248:251]\n"
LD "v11,s[28:31],s82 offen offset:1024 lds\n"
MF "v[248:251],a[212:215],a[196:199],v[248:251]\n"
MF "v[252:255],a[220:223],a[196:199],v[252:255]\n"
TR "a[208:209],v0 offset:8\n"
MF "v[252:255],a[216:219],a[156:159],v[252:255]\n"
TR "a[210:211],v0 offset:520\n"
MF "v[244:247],a[216:219],a[152:155],v[244:247]\n"
MF "v[244:247],a[220:223],a[192:195],v[244:247]\n"
TR "a[212:213],v0 offset:4104\n"
MF "v[236:239],a[220:223],a[188:191],v[236:239]\n"
TR "a[214:215],v0 offset:4616\n"
MF "v[236:239],a[216:219],a[148:151],v[236:239]\n"
LD "v12,s[28:31],s82 offen offset:2048 lds\n"
MF "v[228:231],a[216:219],a[144:147],v[228:231]\n"
MF "v[228:231],a[220:223],a[184:187],v[228:231]\n"
MF "v[220:223],a[220:223],a[180:183],v[220:223]\n"
MF "v[220:223],a[216:219],a[140:143],v[220:223]\n"
"s_waitcnt lgkmcnt(4)\n"
MF "v[16:19],a[200:203],a[60:63],v[16:19]\n"
TR "a[216:217],v1\n"
MF "v[16:19],a[204:207],a[100:103],v[16:19]\n"
TR "a[218:219],v1 offset:512\n"
MF "v[24:27],a[204:207],a[104:107],v[24:27]\n"
LD "v13,s[28:31],s82 offen offset:3072 lds\n"
MF "v[24:27],a[200:203],a[64:67],v[24:27]\n"
TR "a[220:221],v1 offset:4096\n"
MF "v[32:35],a[200:203],a[68:71],v[32:35]\n"
TR "a[222:223],v1 offset:4608\n"
MF "v[32:35],a[204:207],a[108:111],v[32:35]\n"
MF "v[40:43],a[204:207],a[112:115],v[40:43]\n"
MF "v[40:43],a[200:203],a[72:75],v[40:43]\n"
"s_add_u32 s84,s14,40960\n"
MF "v[48:51],a[200:203],a[76:79],v[48:51]\n"
"s_add_u32 m0,s63,24576\n"
MF "v[48:51],a[204:207],a[116:119],v[48:51]\n"
"s_waitcnt lgkmcnt(4)\n"
LD "v4,s[24:27],s84 offen lds\n"
MF "v[52:55],a[212:215],a[116:119],v[52:55]\n"
TR "a[200:201],v1 offset:8\n"
MF "v[52:55],a[208:211],a[76:79],v[52:55]\n"
TR "a[202:203],v1 offset:520\n"
MF "v[44:47],a[208:211],a[72:75],v[44:47]\n"
MF "v[44:47],a[212:215],a[112:115],v[44:47]\n"
TR "a[204:205],v1 offset:4104\n"
MF "v[36:39],a[212:215],a[108:111],v[36:39]\n"
TR "a[206:207],v1 offset:4616\n"
MF "v[36:39],a[208:211],a[68:71],v[36:39]\n"
MF "v[28:31],a[208:211],a[64:67],v[28:31]\n"
MF "v[28:31],a[212:215],a[104:107],v[28:31]\n"
MF "v[20:23],a[212:215],a[100:103],v[20:23]\n"
"s_waitcnt vmcnt(24) lgkmcnt(0)\n"
MF "v[20:23],a[208:211],a[60:63],v[20:23]\n"
"s_barrier\n"
MF "v[56:59],a[216:219],a[60:63],v[56:59]\n"
"s_add_u32 m0,s62,0\n"
"s_add_u32 s82,s66,3145728\n"
TR "a[208:209],v0 offset:16384\n"
MF "v[56:59],a[220:223],a[100:103],v[56:59]\n"
"s_sub_u32 s15,s82,7864320\n"
"s_min_u32 s82,s82,s15\n"
TR "a[210:211],v0 offset:16896\n"
MF "v[64:67],a[220:223],a[104:107],v[64:67]\n"
LD "v6,s[28:31],s82 offen lds\n"
MF "v[64:67],a[216:219],a[64:67],v[64:67]\n"
TR "a[212:213],v0 offset:20480\n"
MF "v[72:75],a[216:219],a[68:71],v[72:75]\n"
TR "a[214:215],v0 offset:20992\n"
MF "v[72:75],a[220:223],a[108:111],v[72:75]\n"
MF "v[80:83],a[220:223],a[112:115],v[80:83]\n"
LD "v11,s[28:31],s82 offen offset:1024 lds\n"
MF "v[80:83],a[216:219],a[72:75],v[80:83]\n"
MF "v[88:91],a[216:219],a[76:79],v[88:91]\n"
MF "v[88:91],a[220:223],a[116:119],v[88:91]\n"
MF "v[92:95],a[204:207],a[116:119],v[92:95]\n"
TR "a[216:217],v0 offset:16392\n"
MF "v[92:95],a[200:203],a[76:79],v[92:95]\n"
TR "a[218:219],v0 offset:16904\n"
MF "v[84:87],a[200:203],a[72:75],v[84:87]\n"
LD "v12,s[28:31],s82 offen offset:2048 lds\n"
MF "v[84:87],a[204:207],a[112:115],v[84:87]\n"
TR "a[220:221],v0 offset:20488\n"
MF "v[76:79],a[204:207],a[108:111],v[76:79]\n"
TR "a[222:223],v0 offset:21000\n"
MF "v[76:79],a[200:203],a[68:71],v[76:79]\n"
MF "v[68:71],a[200:203],a[64:67],v[68:71]\n"
MF "v[68:71],a[204:207],a[104:107],v[68:71]\n"
MF "v[60:63],a[204:207],a[100:103],v[60:63]\n"
LD "v13,s[28:31],s82 offen offset:3072 lds\n"
MF "v[60:63],a[200:203],a[60:63],v[60:63]\n"
"s_waitcnt lgkmcnt(4)\n"
MF "v[96:99],a[208:211],a[60:63],v[96:99]\n"
TR "a[200:201],v1 offset:16384\n"
MF "v[96:99],a[212:215],a[100:103],v[96:99]\n"
TR "a[202:203],v1 offset:16896\n"
MF "v[104:107],a[212:215],a[104:107],v[104:107]\n"
"s_add_u32 s14,s81,640\n"
"s_sub_u32 s15,s14,1280\n"
MF "v[104:107],a[208:211],a[64:67],v[104:107]\n"
"s_min_u32 s14,s14,s15\n"
"s_add_u32 s14,s14,s68\n"
TR "a[204:205],v1 offset:20480\n"
MF "v[112:115],a[208:211],a[68:71],v[112:115]\n"
"s_add_u32 s84,s14,0\n"
"s_add_u32 m0,s63,40960\n"
TR "a[206:207],v1 offset:20992\n"
MF "v[112:115],a[212:215],a[108:111],v[112:115]\n"
LD "v4,s[24:27],s84 offen lds\n"
MF "v[120:123],a[212:215],a[112:115],v[120:123]\n"
MF "v[120:123],a[208:211],a[72:75],v[120:123]\n"
MF "v[128:131],a[208:211],a[76:79],v[128:131]\n"
"s_add_u32 s84,s14,9216\n"
MF "v[128:131],a[212:215],a[116:119],v[128:131]\n"
"s_waitcnt lgkmcnt(4)\n"
LD "v5,s[24:27],s84 offen offset:1024 lds\n"
MF "v[132:135],a[220:223],a[116:119],v[132:135]\n"
TR "a[208:209],v1 offset:16392\n"
MF "v[132:135],a[216:219],a[76:79],v[132:135]\n"
TR "a[210:211],v1 offset:16904\n"
MF "v[124:127],a[216:219],a[72:75],v[124:127]\n"
MF "v[124:127],a[220:223],a[112:115],v[124:127]\n"
TR "a[212:213],v1 offset:20488\n"
MF "v[116:119],a[220:223],a[108:111],v[116:119]\n"
TR "a[214:215],v1 offset:21000\n"
MF "v[116:119],a[216:219],a[68:71],v[116:119]\n"
MF "v[108:111],a[216:219],a[64:67],v[108:111]\n"
MF "v[108:111],a[220:223],a[104:107],v[108:111]\n"
MF "v[100:103],a[220:223],a[100:103],v[100:103]\n"
"s_waitcnt vmcnt(23) lgkmcnt(0)\n"
MF "v[100:103],a[216:219],a[60:63],v[100:103]\n"
"s_barrier\n"
MF "v[136:139],a[200:203],a[60:63],v[136:139]\n"
TR "a[216:217],v0 offset:32768\n"
MF "v[136:139],a[204:207],a[100:103],v[136:139]\n"
"s_add_u32 m0,s62,16384\n"
TR "a[218:219],v0 offset:33280\n"
MF "v[144:147],a[204:207],a[104:107],v[144:147]\n"
"s_add_u32 s82,s66,3145856\n"
"ds_read_b128 a[140:143],v2\n"
MF "v[144:147],a[200:203],a[64:67],v[144:147]\n"
"s_sub_u32 s15,s82,7864320\n"
TR "a[220:221],v0 offset:36864\n"
MF "v[152:155],a[200:203],a[68:71],v[152:155]\n"
"s_min_u32 s82,s82,s15\n"
TR "a[222:223],v0 offset:37376\n"
MF "v[152:155],a[204:207],a[108:111],v[152:155]\n"
LD "v6,s[28:31],s82 offen lds\n"
MF "v[160:163],a[204:207],a[112:115],v[160:163]\n"
"ds_read_b128 a[180:183],v3\n"
MF "v[160:163],a[200:203],a[72:75],v[160:163]\n"
LD "v11,s[28:31],s82 offen offset:1024 lds\n"
MF "v[168:171],a[200:203],a[76:79],v[168:171]\n"
MF "v[168:171],a[204:207],a[116:119],v[168:171]\n"
LD "v12,s[28:31],s82 offen offset:2048 lds\n"
MF "v[172:175],a[212:215],a[116:119],v[172:175]\n"
TR "a[200:201],v0 offset:32776\n"
MF "v[172:175],a[208:211],a[76:79],v[172:175]\n"
TR "a[202:203],v0 offset:33288\n"
MF "v[164:167],a[208:211],a[72:75],v[164:167]\n"
"ds_read_b128 a[144:147],v2 offset:2048\n"
MF "v[164:167],a[212:215],a[112:115],v[164:167]\n"
TR "a[204:205],v0 offset:36872\n"
MF "v[156:159],a[212:215],a[108:111],v[156:159]\n"
TR "a[206:207],v0 offset:37384\n"
MF "v[156:159],a[208:211],a[68:71],v[156:159]\n"
MF "v[148:151],a[208:211],a[64:67],v[148:151]\n"
"ds_read_b128 a[184:187],v3 offset:2048\n"
MF "v[148:151],a[212:215],a[104:107],v[148:151]\n"
MF "v[140:143],a[212:215],a[100:103],v[140:143]\n"
LD "v13,s[28:31],s82 offen offset:3072 lds\n"
MF "v[140:143],a[208:211],a[60:63],v[140:143]\n"
"s_waitcnt lgkmcnt(7)\n"
MF "v[176:179],a[216:219],a[60:63],v[176:179]\n"
TR "a[208:209],v1 offset:32768\n"
MF "v[176:179],a[220:223],a[100:103],v[176:179]\n"
TR "a[210:211],v1 offset:33280\n"
MF "v[184:187],a[220:223],a[104:107],v[184:187]\n"
"ds_read_b128 a[148:151],v2 offset:4096\n"
MF "v[184:187],a[216:219],a[64:67],v[184:187]\n"
"s_add_u32 s84,s14,20480\n"
TR "a[212:213],v1 offset:36864\n"
MF "v[192:195],a[216:219],a[68:71],v[192:195]\n"
"s_add_u32 m0,s63,43008\n"
TR "a[214:215],v1 offset:37376\n"
MF "v[192:195],a[220:223],a[108:111],v[192:195]\n"
LD "v4,s[24:27],s84 offen lds\n"
MF "v[200:203],a[220:223],a[112:115],v[200:203]\n"
"ds_read_b128 a[188:191],v3 offset:4096\n"
MF "v[200:203],a[216:219],a[72:75],v[200:203]\n"
MF "v[208:211],a[216:219],a[76:79],v[208:211]\n"
"s_add_u32 s84,s14,29696\n"
MF "v[208:211],a[220:223],a[116:119],v[208:211]\n"
"s_waitcnt lgkmcnt(7)\n"
LD "v5,s[24:27],s84 offen offset:1024 lds\n"
MF "v[212:215],a[204:207],a[116:119],v[212:215]\n"
TR "a[216:217],v1 offset:32776\n"
MF "v[212:215],a[200:203],a[76:79],v[212:215]\n"
TR "a[218:219],v1 offset:33288\n"
MF "v[204:207],a[200:203],a[72:75],v[204:207]\n"
"ds_read_b128 a[152:155],v2 offset:6144\n"
MF "v[204:207],a[204:207],a[112:115],v[204:207]\n"
TR "a[220:221],v1 offset:36872\n"
MF "v[196:199],a[204:207],a[108:111],v[196:199]\n"
TR "a[222:223],v1 offset:37384\n"
MF "v[196:199],a[200:203],a[68:71],v[196:199]\n"
MF "v[188:191],a[200:203],a[64:67],v[188:191]\n"
"v_add_u32 v0,0xc000,v0\n"
MF "v[188:191],a[204:207],a[104:107],v[188:191]\n"
"v_add_u32 v1,0xc000,v1\n"
MF "v[180:183],a[204:207],a[100:103],v[180:183]\n"
"s_waitcnt vmcnt(25) lgkmcnt(0)\n"
MF "v[180:183],a[200:203],a[60:63],v[180:183]\n"
"s_barrier\n"
MF "v[216:219],a[208:211],a[60:63],v[216:219]\n"
TR "a[200:201],v0\n"
MF "v[216:219],a[212:215],a[100:103],v[216:219]\n"
"s_add_u32 m0,s62,32768\n"
TR "a[202:203],v0 offset:512\n"
MF "v[224:227],a[212:215],a[104:107],v[224:227]\n"
".Lpg5c:\n"
"s_cbranch_execz .Lpg15c\n"
".Lpg5f:\n"
"s_cbranch_execz .LpgattB_f\n"
"s_add_u32 s82,s66,3145984\n"
"ds_read_b128 a[192:195],v3 offset:6144\n"
MF "v[224:227],a[208:211],a[64:67],v[224:227]\n"
"s_sub_u32 s15,s82,7864320\n"
TR "a[204:205],v0 offset:4096\n"
MF "v[232:235],a[208:211],a[68:71],v[232:235]\n"
"s_min_u32 s82,s82,s15\n"
TR "a[206:207],v0 offset:4608\n"
MF "v[232:235],a[212:215],a[108:111],v[232:235]\n"
LD "v6,s[28:31],s82 offen lds\n"
MF "v[240:243],a[212:215],a[112:115],v[240:243]\n"
"ds_read_b128 a[156:159],v2 offset:8192\n"
MF "v[240:243],a[208:211],a[72:75],v[240:243]\n"
"ds_read_b128 a[196:199],v3 offset:8192\n"
MF "v[248:251],a[208:211],a[76:79],v[248:251]\n"
LD "v11,s[28:31],s82 offen offset:1024 lds\n"
MF "v[248:251],a[212:215],a[116:119],v[248:251]\n"
MF "v[252:255],a[220:223],a[116:119],v[252:255]\n"
TR "a[208:209],v0 offset:8\n"
MF "v[252:255],a[216:219],a[76:79],v[252:255]\n"
TR "a[210:211],v0 offset:520\n"
MF "v[244:247],a[216:219],a[72:75],v[244:247]\n"
MF "v[244:247],a[220:223],a[112:115],v[244:247]\n"
TR "a[212:213],v0 offset:4104\n"
MF "v[236:239],a[220:223],a[108:111],v[236:239]\n"
TR "a[214:215],v0 offset:4616\n"
MF "v[236:239],a[216:219],a[68:71],v[236:239]\n"
LD "v12,s[28:31],s82 offen offset:2048 lds\n"
MF "v[228:231],a[216:219],a[64:67],v[228:231]\n"
MF "v[228:231],a[220:223],a[104:107],v[228:231]\n"
MF "v[220:223],a[220:223],a[100:103],v[220:223]\n"
MF "v[220:223],a[216:219],a[60:63],v[220:223]\n"
"s_waitcnt lgkmcnt(4)\n"
MF "v[16:19],a[200:203],a[140:143],v[16:19]\n"
TR "a[216:217],v1\n"
MF "v[16:19],a[204:207],a[180:183],v[16:19]\n"
TR "a[218:219],v1 offset:512\n"
MF "v[24:27],a[204:207],a[184:187],v[24:27]\n"
LD "v13,s[28:31],s82 offen offset:3072 lds\n"
MF "v[24:27],a[200:203],a[144:147],v[24:27]\n"
TR "a[220:221],v1 offset:4096\n"
MF "v[32:35],a[200:203],a[148:151],v[32:35]\n"
TR "a[222:223],v1 offset:4608\n"
MF "v[32:35],a[204:207],a[188:191],v[32:35]\n"
MF "v[40:43],a[204:207],a[192:195],v[40:43]\n"
MF "v[40:43],a[200:203],a[152:155],v[40:43]\n"
"s_add_u32 s84,s14,40960\n"
MF "v[48:51],a[200:203],a[156:159],v[48:51]\n"
"s_add_u32 m0,s63,45056\n"
MF "v[48:51],a[204:207],a[196:199],v[48:51]\n"
"s_waitcnt lgkmcnt(4)\n"
LD "v4,s[24:27],s84 offen lds\n"
MF "v[52:55],a[212:215],a[196:199],v[52:55]\n"
TR "a[200:201],v1 offset:8\n"
MF "v[52:55],a[208:211],a[156:159],v[52:55]\n"
TR "a[202:203],v1 offset:520\n"
MF "v[44:47],a[208:211],a[152:155],v[44:47]\n"
MF "v[44:47],a[212:215],a[192:195],v[44:47]\n"
TR "a[204:205],v1 offset:4104\n"
MF "v[36:39],a[212:215],a[188:191],v[36:39]\n"
TR "a[206:207],v1 offset:4616\n"
MF "v[36:39],a[208:211],a[148:151],v[36:39]\n"
MF "v[28:31],a[208:211],a[144:147],v[28:31]\n"
MF "v[28:31],a[212:215],a[184:187],v[28:31]\n"
MF "v[20:23],a[212:215],a[180:183],v[20:23]\n"
"s_waitcnt vmcnt(24) lgkmcnt(0)\n"
MF "v[20:23],a[208:211],a[140:143],v[20:23]\n"
"s_barrier\n"
MF "v[56:59],a[216:219],a[140:143],v[56:59]\n"
"s_add_u32 m0,s62,49152\n"
"s_add_u32 s82,s66,3932160\n"
TR "a[208:209],v0 offset:16384\n"
MF "v[56:59],a[220:223],a[180:183],v[56:59]\n"
"s_sub_u32 s15,s82,7864320\n"
"s_min_u32 s82,s82,s15\n"
TR "a[210:211],v0 offset:16896\n"
MF "v[64:67],a[220:223],a[184:187],v[64:67]\n"
LD "v6,s[28:31],s82 offen lds\n"
MF "v[64:67],a[216:219],a[144:147],v[64:67]\n"
TR "a[212:213],v0 offset:20480\n"
MF "v[72:75],a[216:219],a[148:151],v[72:75]\n"
TR "a[214:215],v0 offset:20992\n"
MF "v[72:75],a[220:223],a[188:191],v[72:75]\n"
MF "v[80:83],a[220:223],a[192:195],v[80:83]\n"
LD "v11,s[28:31],s82 offen offset:1024 lds\n"
MF "v[80:83],a[216:219],a[152:155],v[80:83]\n"
MF "v[88:91],a[216:219],a[156:159],v[88:91]\n"
MF "v[88:91],a[220:223],a[196:199],v[88:91]\n"
MF "v[92:95],a[204:207],a[196:199],v[92:95]\n"
TR "a[216:217],v0 offset:16392\n"
MF "v[92:95],a[200:203],a[156:159],v[92:95]\n"
TR "a[218:219],v0 offset:16904\n"
MF "v[84:87],a[200:203],a[152:155],v[84:87]\n"
LD "v12,s[28:31],s82 offen offset:2048 lds\n"
MF "v[84:87],a[204:207],a[192:195],v[84:87]\n"
TR "a[220:221],v0 offset:20488\n"
MF "v[76:79],a[204:207],a[188:191],v[76:79]\n"
TR "a[222:223],v0 offset:21000\n"
MF "v[76:79],a[200:203],a[148:151],v[76:79]\n"
MF "v[68:71],a[200:203],a[144:147],v[68:71]\n"
MF "v[68:71],a[204:207],a[184:187],v[68:71]\n"
MF "v[60:63],a[204:207],a[180:183],v[60:63]\n"
LD "v13,s[28:31],s82 offen offset:3072 lds\n"
MF "v[60:63],a[200:203],a[140:143],v[60:63]\n"
"s_waitcnt lgkmcnt(4)\n"
MF "v[96:99],a[208:211],a[140:143],v[96:99]\n"
TR "a[200:201],v1 offset:16384\n"
MF "v[96:99],a[212:215],a[180:183],v[96:99]\n"
TR "a[202:203],v1 offset:16896\n"
MF "v[104:107],a[212:215],a[184:187],v[104:107]\n"
"s_add_u32 s14,s81,768\n"
"s_sub_u32 s15,s14,1280\n"
MF "v[104:107],a[208:211],a[144:147],v[104:107]\n"
"s_min_u32 s14,s14,s15\n"
"s_add_u32 s14,s14,s68\n"
TR "a[204:205],v1 offset:20480\n"
MF "v[112:115],a[208:211],a[148:151],v[112:115]\n"
"s_add_u32 s84,s14,0\n"
"s_add_u32 m0,s63,0\n"
TR "a[206:207],v1 offset:20992\n"
MF "v[112:115],a[212:215],a[188:191],v[112:115]\n"
LD "v4,s[24:27],s84 offen lds\n"
MF "v[120:123],a[212:215],a[192:195],v[120:123]\n"
MF "v[120:123],a[208:211],a[152:155],v[120:123]\n"
MF "v[128:131],a[208:211],a[156:159],v[128:131]\n"
"s_add_u32 s84,s14,9216\n"
MF "v[128:131],a[212:215],a[196:199],v[128:131]\n"
"s_waitcnt lgkmcnt(4)\n"
LD "v5,s[24:27],s84 offen offset:1024 lds\n"
MF "v[132:135],a[220:223],a[196:199],v[132:135]\n"
TR "a[208:209],v1 offset:16392\n"
MF "v[132:135],a[216:219],a[156:159],v[132:135]\n"
TR "a[210:211],v1 offset:16904\n"
MF "v[124:127],a[216:219],a[152:155],v[124:127]\n"
MF "v[124:127],a[220:223],a[192:195],v[124:127]\n"
TR "a[212:213],v1 offset:20488\n"
MF "v[116:119],a[220:223],a[188:191],v[116:119]\n"
TR "a[214:215],v1 offset:21000\n"
MF "v[116:119],a[216:219],a[148:151],v[116:119]\n"
MF "v[108:111],a[216:219],a[144:147],v[108:111]\n"
MF "v[108:111],a[220:223],a[184:187],v[108:111]\n"
MF "v[100:103],a[220:223],a[180:183],v[100:103]\n"
"s_waitcnt vmcnt(23) lgkmcnt(0)\n"
MF "v[100:103],a[216:219],a[140:143],v[100:103]\n"
"s_barrier\n"
MF "v[136:139],a[200:203],a[140:143],v[136:139]\n"
TR "a[216:217],v0 offset:32768\n"
MF "v[136:139],a[204:207],a[180:183],v[136:139]\n"
"s_add_u32 m0,s62,65536\n"
TR "a[218:219],v0 offset:33280\n"
MF "v[144:147],a[204:207],a[184:187],v[144:147]\n"
"s_add_u32 s82,s66,3932288\n"
"ds_read_b128 a[60:63],v2 offset:20480\n"
MF "v[144:147],a[200:203],a[144:147],v[144:147]\n"
"s_sub_u32 s15,s82,7864320\n"
TR "a[220:221],v0 offset:36864\n"
MF "v[152:155],a[200:203],a[148:151],v[152:155]\n"
"s_min_u32 s82,s82,s15\n"
TR "a[222:223],v0 offset:37376\n"
MF "v[152:155],a[204:207],a[188:191],v[152:155]\n"
LD "v6,s[28:31],s82 offen lds\n"
MF "v[160:163],a[204:207],a[192:195],v[160:163]\n"
"ds_read_b128 a[100:103],v3 offset:20480\n"
MF "v[160:163],a[200:203],a[152:155],v[160:163]\n"
LD "v11,s[28:31],s82 offen offset:1024 lds\n"
MF "v[168:171],a[200:203],a[156:159],v[168:171]\n"
MF "v[168:171],a[204:207],a[196:199],v[168:171]\n"
LD "v12,s[28:31],s82 offen offset:2048 lds\n"
MF "v[172:175],a[212:215],a[196:199],v[172:175]\n"
TR "a[200:201],v0 offset:32776\n"
MF "v[172:175],a[208:211],a[156:159],v[172:175]\n"
TR "a[202:203],v0 offset:33288\n"
MF "v[164:167],a[208:211],a[152:155],v[164:167]\n"
"ds_read_b128 a[64:67],v2 offset:22528\n"
MF "v[164:167],a[212:215],a[192:195],v[164:167]\n"
TR "a[204:205],v0 offset:36872\n"
MF "v[156:159],a[212:215],a[188:191],v[156:159]\n"
TR "a[206:207],v0 offset:37384\n"
MF "v[156:159],a[208:211],a[148:151],v[156:159]\n"
MF "v[148:151],a[208:211],a[144:147],v[148:151]\n"
"ds_read_b128 a[104:107],v3 offset:22528\n"
MF "v[148:151],a[212:215],a[184:187],v[148:151]\n"
MF "v[140:143],a[212:215],a[180:183],v[140:143]\n"
LD "v13,s[28:31],s82 offen offset:3072 lds\n"
MF "v[140:143],a[208:211],a[140:143],v[140:143]\n"
"s_waitcnt lgkmcnt(7)\n"
MF "v[176:179],a[216:219],a[140:143],v[176:179]\n"
TR "a[208:209],v1 offset:32768\n"
MF "v[176:179],a[220:223],a[180:183],v[176:179]\n"
TR "a[210:211],v1 offset:33280\n"
MF "v[184:187],a[220:223],a[184:187],v[184:187]\n"
"ds_read_b128 a[68:71],v2 offset:24576\n"
MF "v[184:187],a[216:219],a[144:147],v[184:187]\n"
"s_add_u32 s84,s14,20480\n"
TR "a[212:213],v1 offset:36864\n"
MF "v[192:195],a[216:219],a[148:151],v[192:195]\n"
"s_add_u32 m0,s63,2048\n"
TR "a[214:215],v1 offset:37376\n"
MF "v[192:195],a[220:223],a[188:191],v[192:195]\n"
LD "v4,s[24:27],s84 offen lds\n"
MF "v[200:203],a[220:223],a[192:195],v[200:203]\n"
"ds_read_b128 a[108:111],v3 offset:24576\n"
MF "v[200:203],a[216:219],a[152:155],v[200:203]\n"
MF "v[208:211],a[216:219],a[156:159],v[208:211]\n"
"s_add_u32 s84,s14,29696\n"
MF "v[208:211],a[220:223],a[196:199],v[208:211]\n"
"s_waitcnt lgkmcnt(7)\n"
LD "v5,s[24:27],s84 offen offset:1024 lds\n"
MF "v[212:215],a[204:207],a[196:199],v[212:215]\n"
TR "a[216:217],v1 offset:32776\n"
MF "v[212:215],a[200:203],a[156:159],v[212:215]\n"
TR "a[218:219],v1 offset:33288\n"
MF "v[204:207],a[200:203],a[152:155],v[204:207]\n"
"ds_read_b128 a[72:75],v2 offset:26624\n"
MF "v[204:207],a[204:207],a[192:195],v[204:207]\n"
TR "a[220:221],v1 offset:36872\n"
MF "v[196:199],a[204:207],a[188:191],v[196:199]\n"
TR "a[222:223],v1 offset:37384\n"
MF "v[196:199],a[200:203],a[148:151],v[196:199]\n"
MF "v[188:191],a[200:203],a[144:147],v[188:191]\n"
"v_add_u32 v0,0xffff4000,v0\n"
MF "v[188:191],a[204:207],a[184:187],v[188:191]\n"
"v_add_u32 v1,0xffff4000,v1\n"
MF "v[180:183],a[204:207],a[180:183],v[180:183]\n"
"s_waitcnt vmcnt(25) lgkmcnt(0)\n"
MF "v[180:183],a[200:203],a[140:143],v[180:183]\n"
"s_barrier\n"
MF "v[216:219],a[208:211],a[140:143],v[216:219]\n"
TR "a[200:201],v0\n"
MF "v[216:219],a[212:215],a[180:183],v[216:219]\n"
"s_add_u32 m0,s62,81920\n"
TR "a[202:203],v0 offset:512\n"
MF "v[224:227],a[212:215],a[184:187],v[224:227]\n"
"s_add_u32 s82,s66,3932416\n"
"ds_read_b128 a[112:115],v3 offset:26624\n"
MF "v[224:227],a[208:211],a[144:147],v[224:227]\n"
"s_sub_u32 s15,s82,7864320\n"
TR "a[204:205],v0 offset:4096\n"
MF "v[232:235],a[208:211],a[148:151],v[232:235]\n"
"s_min_u32 s82,s82,s15\n"
TR "a[206:207],v0 offset:4608\n"
MF "v[232:235],a[212:215],a[188:191],v[232:235]\n"
LD "v6,s[28:31],s82 offen lds\n"
MF "v[240:243],a[212:215],a[192:195],v[240:243]\n"
"ds_read_b128 a[76:79],v2 offset:28672\n"
MF "v[240:243],a[208:211],a[152:155],v[240:243]\n"
"ds_read_b128 a[116:119],v3 offset:28672\n"
MF "v[248:251],a[208:211],a[156:159],v[248:251]\n"
LD "v11,s[28:31],s82 offen offset:1024 lds\n"
MF "v[248:251],a[212:215],a[196:199],v[248:251]\n"
MF "v[252:255],a[220:223],a[196:199],v[252:255]\n"
TR "a[208:209],v0 offset:8\n"
MF "v[252:255],a[216:219],a[156:159],v[252:255]\n"
TR "a[210:211],v0 offset:520\n"
MF "v[244:247],a[216:219],a[152:155],v[244:247]\n"
MF "v[244:247],a[220:223],a[192:195],v[244:247]\n"
TR "a[212:213],v0 offset:4104\n"
MF "v[236:239],a[220:223],a[188:191],v[236:239]\n"
TR "a[214:215],v0 offset:4616\n"
MF "v[236:239],a[216:219],a[148:151],v[236:239]\n"
LD "v12,s[28:31],s82 offen offset:2048 lds\n"
MF "v[228:231],a[216:219],a[144:147],v[228:231]\n"
MF "v[228:231],a[220:223],a[184:187],v[228:231]\n"
MF "v[220:223],a[220:223],a[180:183],v[220:223]\n"
MF "v[220:223],a[216:219],a[140:143],v[220:223]\n"
"s_waitcnt lgkmcnt(4)\n"
MF "v[16:19],a[200:203],a[60:63],v[16:19]\n"
TR "a[216:217],v1\n"
MF "v[16:19],a[204:207],a[100:103],v[16:19]\n"
TR "a[218:219],v1 offset:512\n"
MF "v[24:27],a[204:207],a[104:107],v[24:27]\n"
LD "v13,s[28:31],s82 offen offset:3072 lds\n"
MF "v[24:27],a[200:203],a[64:67],v[24:27]\n"
TR "a[220:221],v1 offset:4096\n"
MF "v[32:35],a[200:203],a[68:71],v[32:35]\n"
TR "a[222:223],v1 offset:4608\n"
MF "v[32:35],a[204:207],a[108:111],v[32:35]\n"
MF "v[40:43],a[204:207],a[112:115],v[40:43]\n"
MF "v[40:43],a[200:203],a[72:75],v[40:43]\n"
"s_add_u32 s84,s14,40960\n"
MF "v[48:51],a[200:203],a[76:79],v[48:51]\n"
"s_add_u32 m0,s63,4096\n"
MF "v[48:51],a[204:207],a[116:119],v[48:51]\n"
"s_waitcnt lgkmcnt(4)\n"
LD "v4,s[24:27],s84 offen lds\n"
MF "v[52:55],a[212:215],a[116:119],v[52:55]\n"
TR "a[200:201],v1 offset:8\n"
MF "v[52:55],a[208:211],a[76:79],v[52:55]\n"
TR "a[202:203],v1 offset:520\n"
MF "v[44:47],a[208:211],a[72:75],v[44:47]\n"
MF "v[44:47],a[212:215],a[112:115],v[44:47]\n"
TR "a[204:205],v1 offset:4104\n"
MF "v[36:39],a[212:215],a[108:111],v[36:39]\n"
TR "a[206:207],v1 offset:4616\n"
MF "v[36:39],a[208:211],a[68:71],v[36:39]\n"
MF "v[28:31],a[208:211],a[64:67],v[28:31]\n"
MF "v[28:31],a[212:215],a[104:107],v[28:31]\n"
MF "v[20:23],a[212:215],a[100:103],v[20:23]\n"
"s_waitcnt vmcnt(24) lgkmcnt(0)\n"
MF "v[20:23],a[208:211],a[60:63],v[20:23]\n"
"s_barrier\n"
MF "v[56:59],a[216:219],a[60:63],v[56:59]\n"
"s_add_u32 m0,s62,0\n"
"s_add_u32 s82,s66,4718592\n"
TR "a[208:209],v0 offset:16384\n"
MF "v[56:59],a[220:223],a[100:103],v[56:59]\n"
"s_sub_u32 s15,s82,7864320\n"
"s_min_u32 s82,s82,s15\n"
TR "a[210:211],v0 offset:16896\n"
MF "v[64:67],a[220:223],a[104:107],v[64:67]\n"
LD "v6,s[28:31],s82 offen lds\n"
MF "v[64:67],a[216:219],a[64:67],v[64:67]\n"
TR "a[212:213],v0 offset:20480\n"
MF "v[72:75],a[216:219],a[68:71],v[72:75]\n"
TR "a[214:215],v0 offset:20992\n"
MF "v[72:75],a[220:223],a[108:111],v[72:75]\n"
MF "v[80:83],a[220:223],a[112:115],v[80:83]\n"
LD "v11,s[28:31],s82 offen offset:1024 lds\n"
MF "v[80:83],a[216:219],a[72:75],v[80:83]\n"
MF "v[88:91],a[216:219],a[76:79],v[88:91]\n"
MF "v[88:91],a[220:223],a[116:119],v[88:91]\n"
MF "v[92:95],a[204:207],a[116:119],v[92:95]\n"
TR "a[216:217],v0 offset:16392\n"
MF "v[92:95],a[200:203],a[76:79],v[92:95]\n"
TR "a[218:219],v0 offset:16904\n"
MF "v[84:87],a[200:203],a[72:75],v[84:87]\n"
LD "v12,s[28:31],s82 offen offset:2048 lds\n"
MF "v[84:87],a[204:207],a[112:115],v[84:87]\n"
TR "a[220:221],v0 offset:20488\n"
MF "v[76:79],a[204:207],a[108:111],v[76:79]\n"
TR "a[222:223],v0 offset:21000\n"
MF "v[76:79],a[200:203],a[68:71],v[76:79]\n"
MF "v[68:71],a[200:203],a[64:67],v[68:71]\n"
MF "v[68:71],a[204:207],a[104:107],v[68:71]\n"
MF "v[60:63],a[204:207],a[100:103],v[60:63]\n"
LD "v13,s[28:31],s82 offen offset:3072 lds\n"
MF "v[60:63],a[200:203],a[60:63],v[60:63]\n"
"s_waitcnt lgkmcnt(4)\n"
MF "v[96:99],a[208:211],a[60:63],v[96:99]\n"
TR "a[200:201],v1 offset:16384\n"
MF "v[96:99],a[212:215],a[100:103],v[96:99]\n"
TR "a[202:203],v1 offset:16896\n"
MF "v[104:107],a[212:215],a[104:107],v[104:107]\n"
"s_add_u32 s14,s81,896\n"
"s_sub_u32 s15,s14,1280\n"
MF "v[104:107],a[208:211],a[64:67],v[104:107]\n"
"s_min_u32 s14,s14,s15\n"
"s_add_u32 s14,s14,s68\n"
TR "a[204:205],v1 offset:20480\n"
MF "v[112:115],a[208:211],a[68:71],v[112:115]\n"
"s_add_u32 s84,s14,0\n"
"s_add_u32 m0,s63,20480\n"
TR "a[206:207],v1 offset:20992\n"
MF "v[112:115],a[212:215],a[108:111],v[112:115]\n"
LD "v4,s[24:27],s84 offen lds\n"
MF "v[120:123],a[212:215],a[112:115],v[120:123]\n"
MF "v[120:123],a[208:211],a[72:75],v[120:123]\n"
MF "v[128:131],a[208:211],a[76:79],v[128:131]\n"
"s_add_u32 s84,s14,9216\n"
MF "v[128:131],a[212:215],a[116:119],v[128:131]\n"
"s_waitcnt lgkmcnt(4)\n"
LD "v5,s[24:27],s84 offen offset:1024 lds\n"
MF "v[132:135],a[220:223],a[116:119],v[132:135]\n"
TR "a[208:209],v1 offset:16392\n"
MF "v[132:135],a[216:219],a[76:79],v[132:135]\n"
TR "a[210:211],v1 offset:16904\n"
MF "v[124:127],a[216:219],a[72:75],v[124:127]\n"
MF "v[124:127],a[220:223],a[112:115],v[124:127]\n"
TR "a[212:213],v1 offset:20488\n"
MF "v[116:119],a[220:223],a[108:111],v[116:119]\n"
TR "a[214:215],v1 offset:21000\n"
MF "v[116:119],a[216:219],a[68:71],v[116:119]\n"
MF "v[108:111],a[216:219],a[64:67],v[108:111]\n"
MF "v[108:111],a[220:223],a[104:107],v[108:111]\n"
MF "v[100:103],a[220:223],a[100:103],v[100:103]\n"
"s_waitcnt vmcnt(23) lgkmcnt(0)\n"
MF "v[100:103],a[216:219],a[60:63],v[100:103]\n"
"s_barrier\n"
MF "v[136:139],a[200:203],a[60:63],v[136:139]\n"
TR "a[216:217],v0 offset:32768\n"
MF "v[136:139],a[204:207],a[100:103],v[136:139]\n"
"s_add_u32 m0,s62,16384\n"
TR "a[218:219],v0 offset:33280\n"
MF "v[144:147],a[204:207],a[104:107],v[144:147]\n"
"s_add_u32 s82,s66,4718720\n"
"ds_read_b128 a[140:143],v2 offset:40960\n"
MF "v[144:147],a[200:203],a[64:67],v[144:147]\n"
"s_sub_u32 s15,s82,7864320\n"
TR "a[220:221],v0 offset:36864\n"
MF "v[152:155],a[200:203],a[68:71],v[152:155]\n"
"s_min_u32 s82,s82,s15\n"
TR "a[222:223],v0 offset:37376\n"
MF "v[152:155],a[204:207],a[108:111],v[152:155]\n"
LD "v6,s[28:31],s82 offen lds\n"
MF "v[160:163],a[204:207],a[112:115],v[160:163]\n"
"ds_read_b128 a[180:183],v3 offset:40960\n"
MF "v[160:163],a[200:203],a[72:75],v[160:163]\n"
LD "v11,s[28:31],s82 offen offset:1024 lds\n"
MF "v[168:171],a[200:203],a[76:79],v[168:171]\n"
MF "v[168:171],a[204:207],a[116:119],v[168:171]\n"
LD "v12,s[28:31],s82 offen offset:2048 lds\n"
MF "v[172:175],a[212:215],a[116:119],v[172:175]\n"
TR "a[200:201],v0 offset:32776\n"
MF "v[172:175],a[208:211],a[76:79],v[172:175]\n"
TR "a[202:203],v0 offset:33288\n"
MF "v[164:167],a[208:211],a[72:75],v[164:167]\n"
"ds_read_b128 a[144:147],v2 offset:43008\n"
MF "v[164:167],a[212:215],a[112:115],v[164:167]\n"
TR "a[204:205],v0 offset:36872\n"
MF "v[156:159],a[212:215],a[108:111],v[156:159]\n"
TR "a[206:207],v0 offset:37384\n"
MF "v[156:159],a[208:211],a[68:71],v[156:159]\n"
MF "v[148:151],a[208:211],a[64:67],v[148:151]\n"
"ds_read_b128 a[184:187],v3 offset:43008\n"
MF "v[148:151],a[212:215],a[104:107],v[148:151]\n"
MF "v[140:143],a[212:215],a[100:103],v[140:143]\n"
LD "v13,s[28:31],s82 offen offset:3072 lds\n"
MF "v[140:143],a[208:211],a[60:63],v[140:143]\n"
"s_waitcnt lgkmcnt(7)\n"
MF "v[176:179],a[216:219],a[60:63],v[176:179]\n"
TR "a[208:209],v1 offset:32768\n"
MF "v[176:179],a[220:223],a[100:103],v[176:179]\n"
TR "a[210:211],v1 offset:33280\n"
MF "v[184:187],a[220:223],a[104:107],v[184:187]\n"
"ds_read_b128 a[148:151],v2 offset:45056\n"
MF "v[184:187],a[216:219],a[64:67],v[184:187]\n"
"s_add_u32 s84,s14,20480\n"
TR "a[212:213],v1 offset:36864\n"
MF "v[192:195],a[216:219],a[68:71],v[192:195]\n"
"s_add_u32 m0,s63,22528\n"
TR "a[214:215],v1 offset:37376\n"
MF "v[192:195],a[220:223],a[108:111],v[192:195]\n"
LD "v4,s[24:27],s84 offen lds\n"
MF "v[200:203],a[220:223],a[112:115],v[200:203]\n"
"ds_read_b128 a[188:191],v3 offset:45056\n"
MF "v[200:203],a[216:219],a[72:75],v[200:203]\n"
MF "v[208:211],a[216:219],a[76:79],v[208:211]\n"
"s_add_u32 s84,s14,29696\n"
MF "v[208:211],a[220:223],a[116:119],v[208:211]\n"
"s_waitcnt lgkmcnt(7)\n"
LD "v5,s[24:27],s84 offen offset:1024 lds\n"
MF "v[212:215],a[204:207],a[116:119],v[212:215]\n"
TR "a[216:217],v1 offset:32776\n"
MF "v[212:215],a[200:203],a[76:79],v[212:215]\n"
TR "a[218:219],v1 offset:33288\n"
MF "v[204:207],a[200:203],a[72:75],v[204:207]\n"
"ds_read_b128 a[152:155],v2 offset:47104\n"
MF "v[204:207],a[204:207],a[112:115],v[204:207]\n"
TR "a[220:221],v1 offset:36872\n"
MF "v[196:199],a[204:207],a[108:111],v[196:199]\n"
TR "a[222:223],v1 offset:37384\n"
MF "v[196:199],a[200:203],a[68:71],v[196:199]\n"
MF "v[188:191],a[200:203],a[64:67],v[188:191]\n"
"v_add_u32 v0,0xc000,v0\n"
MF "v[188:191],a[204:207],a[104:107],v[188:191]\n"
"v_add_u32 v1,0xc000,v1\n"
MF "v[180:183],a[204:207],a[100:103],v[180:183]\n"
"s_waitcnt vmcnt(25) lgkmcnt(0)\n"
MF "v[180:183],a[200:203],a[60:63],v[180:183]\n"
"s_barrier\n"
MF "v[216:219],a[208:211],a[60:63],v[216:219]\n"
TR "a[200:201],v0\n"
MF "v[216:219],a[212:215],a[100:103],v[216:219]\n"
"s_add_u32 m0,s62,32768\n"
TR "a[202:203],v0 offset:512\n"
MF "v[224:227],a[212:215],a[104:107],v[224:227]\n"
"s_add_u32 s82,s66,4718848\n"
"ds_read_b128 a[192:195],v3 offset:47104\n"
MF "v[224:227],a[208:211],a[64:67],v[224:227]\n"
"s_sub_u32 s15,s82,7864320\n"
TR "a[204:205],v0 offset:4096\n"
MF "v[232:235],a[208:211],a[68:71],v[232:235]\n"
"s_min_u32 s82,s82,s15\n"
TR "a[206:207],v0 offset:4608\n"
MF "v[232:235],a[212:215],a[108:111],v[232:235]\n"
LD "v6,s[28:31],s82 offen lds\n"
MF "v[240:243],a[212:215],a[112:115],v[240:243]\n"
"ds_read_b128 a[156:159],v2 offset:49152\n"
MF "v[240:243],a[208:211],a[72:75],v[240:243]\n"
"ds_read_b128 a[196:199],v3 offset:49152\n"
MF "v[248:251],a[208:211],a[76:79],v[248:251]\n"
LD "v11,s[28:31],s82 offen offset:1024 lds\n"
MF "v[248:251],a[212:215],a[116:119],v[248:251]\n"
MF "v[252:255],a[220:223],a[116:119],v[252:255]\n"
TR "a[208:209],v0 offset:8\n"
MF "v[252:255],a[216:219],a[76:79],v[252:255]\n"
TR "a[210:211],v0 offset:520\n"
MF "v[244:247],a[216:219],a[72:75],v[244:247]\n"
MF "v[244:247],a[220:223],a[112:115],v[244:247]\n"
TR "a[212:213],v0 offset:4104\n"
MF "v[236:239],a[220:223],a[108:111],v[236:239]\n"
TR "a[214:215],v0 offset:4616\n"
MF "v[236:239],a[216:219],a[68:71],v[236:239]\n"
LD "v12,s[28:31],s82 offen offset:2048 lds\n"
MF "v[228:231],a[216:219],a[64:67],v[228:231]\n"
MF "v[228:231],a[220:223],a[104:107],v[228:231]\n"
MF "v[220:223],a[220:223],a[100:103],v[220:223]\n"
MF "v[220:223],a[216:219],a[60:63],v[220:223]\n"
"s_waitcnt lgkmcnt(4)\n"
MF "v[16:19],a[200:203],a[140:143],v[16:19]\n"
TR "a[216:217],v1\n"
MF "v[16:19],a[204:207],a[180:183],v[16:19]\n"
TR "a[218:219],v1 offset:512\n"
MF "v[24:27],a[204:207],a[184:187],v[24:27]\n"
LD "v13,s[28:31],s82 offen offset:3072 lds\n"
MF "v[24:27],a[200:203],a[144:147],v[24:27]\n"
TR "a[220:221],v1 offset:4096\n"
MF "v[32:35],a[200:203],a[148:151],v[32:35]\n"
TR "a[222:223],v1 offset:4608\n"
MF "v[32:35],a[204:207],a[188:191],v[32:35]\n"
MF "v[40:43],a[204:207],a[192:195],v[40:43]\n"
MF "v[40:43],a[200:203],a[152:155],v[40:43]\n"
"s_add_u32 s84,s14,40960\n"
MF "v[48:51],a[200:203],a[156:159],v[48:51]\n"
"s_add_u32 m0,s63,24576\n"
MF "v[48:51],a[204:207],a[196:199],v[48:51]\n"
"s_waitcnt lgkmcnt(4)\n"
LD "v4,s[24:27],s84 offen lds\n"
MF "v[52:55],a[212:215],a[196:199],v[52:55]\n"
TR "a[200:201],v1 offset:8\n"
MF "v[52:55],a[208:211],a[156:159],v[52:55]\n"
TR "a[202:203],v1 offset:520\n"
MF "v[44:47],a[208:211],a[152:155],v[44:47]\n"
MF "v[44:47],a[212:215],a[192:195],v[44:47]\n"
TR "a[204:205],v1 offset:4104\n"
MF "v[36:39],a[212:215],a[188:191],v[36:39]\n"
TR "a[206:207],v1 offset:4616\n"
MF "v[36:39],a[208:211],a[148:151],v[36:39]\n"
MF "v[28:31],a[208:211],a[144:147],v[28:31]\n"
MF "v[28:31],a[212:215],a[184:187],v[28:31]\n"
MF "v[20:23],a[212:215],a[180:183],v[20:23]\n"
".Lpg6c:\n"
"s_cbranch_execz .Lpg1a\n"
".Lpg6f:\n"
"s_cbranch_execz .Lpgt0k_f\n"
"s_waitcnt vmcnt(24) lgkmcnt(0)\n"
MF "v[20:23],a[208:211],a[140:143],v[20:23]\n"
"s_barrier\n"
MF "v[56:59],a[216:219],a[140:143],v[56:59]\n"
"s_add_u32 m0,s62,49152\n"
"s_add_u32 s82,s66,5505024\n"
TR "a[208:209],v0 offset:16384\n"
MF "v[56:59],a[220:223],a[180:183],v[56:59]\n"
"s_sub_u32 s15,s82,7864320\n"
"s_min_u32 s82,s82,s15\n"
TR "a[210:211],v0 offset:16896\n"
MF "v[64:67],a[220:223],a[184:187],v[64:67]\n"
LD "v6,s[28:31],s82 offen lds\n"
MF "v[64:67],a[216:219],a[144:147],v[64:67]\n"
TR "a[212:213],v0 offset:20480\n"
MF "v[72:75],a[216:219],a[148:151],v[72:75]\n"
TR "a[214:215],v0 offset:20992\n"
MF "v[72:75],a[220:223],a[188:191],v[72:75]\n"
MF "v[80:83],a[220:223],a[192:195],v[80:83]\n"
LD "v11,s[28:31],s82 offen offset:1024 lds\n"
MF "v[80:83],a[216:219],a[152:155],v[80:83]\n"
MF "v[88:91],a[216:219],a[156:159],v[88:91]\n"
MF "v[88:91],a[220:223],a[196:199],v[88:91]\n"
MF "v[92:95],a[204:207],a[196:199],v[92:95]\n"
TR "a[216:217],v0 offset:16392\n"
MF "v[92:95],a[200:203],a[156:159],v[92:95]\n"
TR "a[218:219],v0 offset:16904\n"
MF "v[84:87],a[200:203],a[152:155],v[84:87]\n"
LD "v12,s[28:31],s82 offen offset:2048 lds\n"
MF "v[84:87],a[204:207],a[192:195],v[84:87]\n"
TR "a[220:221],v0 offset:20488\n"
MF "v[76:79],a[204:207],a[188:191],v[76:79]\n"
TR "a[222:223],v0 offset:21000\n"
MF "v[76:79],a[200:203],a[148:151],v[76:79]\n"
MF "v[68:71],a[200:203],a[144:147],v[68:71]\n"
MF "v[68:71],a[204:207],a[184:187],v[68:71]\n"
MF "v[60:63],a[204:207],a[180:183],v[60:63]\n"
LD "v13,s[28:31],s82 offen offset:3072 lds\n"
MF "v[60:63],a[200:203],a[140:143],v[60:63]\n"
"s_waitcnt lgkmcnt(4)\n"
MF "v[96:99],a[208:211],a[140:143],v[96:99]\n"
TR "a[200:201],v1 offset:16384\n"
MF "v[96:99],a[212:215],a[180:183],v[96:99]\n"
TR "a[202:203],v1 offset:16896\n"
MF "v[104:107],a[212:215],a[184:187],v[104:107]\n"
"s_add_u32 s14,s81,1024\n"
"s_sub_u32 s15,s14,1280\n"
MF "v[104:107],a[208:211],a[144:147],v[104:107]\n"
"s_min_u32 s14,s14,s15\n"
"s_add_u32 s14,s14,s68\n"
TR "a[204:205],v1 offset:20480\n"
MF "v[112:115],a[208:211],a[148:151],v[112:115]\n"
"s_add_u32 s84,s14,0\n"
"s_add_u32 m0,s63,40960\n"
TR "a[206:207],v1 offset:20992\n"
MF "v[112:115],a[212:215],a[188:191],v[112:115]\n"
LD "v4,s[24:27],s84 offen lds\n"
MF "v[120:123],a[212:215],a[192:195],v[120:123]\n"
MF "v[120:123],a[208:211],a[152:155],v[120:123]\n"
MF "v[128:131],a[208:211],a[156:159],v[128:131]\n"
"s_add_u32 s84,s14,9216\n"
MF "v[128:131],a[212:215],a[196:199],v[128:131]\n"
"s_waitcnt lgkmcnt(4)\n"
LD "v5,s[24:27],s84 offen offset:1024 lds\n"
MF "v[132:135],a[220:223],a[196:199],v[132:135]\n"
TR "a[208:209],v1 offset:16392\n"
MF "v[132:135],a[216:219],a[156:159],v[132:135]\n"
TR "a[210:211],v1 offset:16904\n"
MF "v[124:127],a[216:219],a[152:155],v[124:127]\n"
MF "v[124:127],a[220:223],a[192:195],v[124:127]\n"
TR "a[212:213],v1 offset:20488\n"
MF "v[116:119],a[220:223],a[188:191],v[116:119]\n"
TR "a[214:215],v1 offset:21000\n"
MF "v[116:119],a[216:219],a[148:151],v[116:119]\n"
MF "v[108:111],a[216:219],a[144:147],v[108:111]\n"
MF "v[108:111],a[220:223],a[184:187],v[108:111]\n"
MF "v[100:103],a[220:223],a[180:183],v[100:103]\n"
"s_waitcnt vmcnt(23) lgkmcnt(0)\n"
MF "v[100:103],a[216:219],a[140:143],v[100:103]\n"
"s_barrier\n"
"s_cmp_eq_u32 s57,1\n"
"s_cbranch_scc0 .Lhs12\n"
"v_bfe_u32 v14,v9,4,5\n"
"v_lshlrev_b32 v14,6,v14\n"
"s_lshl_b32 s58,s78,2\n"
"s_add_u32 s58,s58,s86\n"
"buffer_load_dword v10,v14,s[40:43],s58 offen offset:128 sc1\n"
".Lhs12:\n"
MF "v[136:139],a[200:203],a[140:143],v[136:139]\n"
TR "a[216:217],v0 offset:32768\n"
MF "v[136:139],a[204:207],a[180:183],v[136:139]\n"
"s_add_u32 m0,s62,65536\n"
TR "a[218:219],v0 offset:33280\n"
MF "v[144:147],a[204:207],a[184:187],v[144:147]\n"
"s_add_u32 s82,s66,5505152\n"
"ds_read_b128 a[60:63],v2\n"
MF "v[144:147],a[200:203],a[144:147],v[144:147]\n"
"s_sub_u32 s15,s82,7864320\n"
TR "a[220:221],v0 offset:36864\n"
MF "v[152:155],a[200:203],a[148:151],v[152:155]\n"
"s_min_u32 s82,s82,s15\n"
TR "a[222:223],v0 offset:37376\n"
MF "v[152:155],a[204:207],a[188:191],v[152:155]\n"
LD "v6,s[28:31],s82 offen lds\n"
MF "v[160:163],a[204:207],a[192:195],v[160:163]\n"
"ds_read_b128 a[100:103],v3\n"
MF "v[160:163],a[200:203],a[152:155],v[160:163]\n"
LD "v11,s[28:31],s82 offen offset:1024 lds\n"
MF "v[168:171],a[200:203],a[156:159],v[168:171]\n"
MF "v[168:171],a[204:207],a[196:199],v[168:171]\n"
LD "v12,s[28:31],s82 offen offset:2048 lds\n"
MF "v[172:175],a[212:215],a[196:199],v[172:175]\n"
TR "a[200:201],v0 offset:32776\n"
MF "v[172:175],a[208:211],a[156:159],v[172:175]\n"
TR "a[202:203],v0 offset:33288\n"
MF "v[164:167],a[208:211],a[152:155],v[164:167]\n"
"ds_read_b128 a[64:67],v2 offset:2048\n"
MF "v[164:167],a[212:215],a[192:195],v[164:167]\n"
TR "a[204:205],v0 offset:36872\n"
MF "v[156:159],a[212:215],a[188:191],v[156:159]\n"
TR "a[206:207],v0 offset:37384\n"
MF "v[156:159],a[208:211],a[148:151],v[156:159]\n"
MF "v[148:151],a[208:211],a[144:147],v[148:151]\n"
"ds_read_b128 a[104:107],v3 offset:2048\n"
MF "v[148:151],a[212:215],a[184:187],v[148:151]\n"
MF "v[140:143],a[212:215],a[180:183],v[140:143]\n"
LD "v13,s[28:31],s82 offen offset:3072 lds\n"
MF "v[140:143],a[208:211],a[140:143],v[140:143]\n"
"s_waitcnt lgkmcnt(7)\n"
MF "v[176:179],a[216:219],a[140:143],v[176:179]\n"
TR "a[208:209],v1 offset:32768\n"
MF "v[176:179],a[220:223],a[180:183],v[176:179]\n"
TR "a[210:211],v1 offset:33280\n"
MF "v[184:187],a[220:223],a[184:187],v[184:187]\n"
"ds_read_b128 a[68:71],v2 offset:4096\n"
MF "v[184:187],a[216:219],a[144:147],v[184:187]\n"
"s_add_u32 s84,s14,20480\n"
TR "a[212:213],v1 offset:36864\n"
MF "v[192:195],a[216:219],a[148:151],v[192:195]\n"
"s_add_u32 m0,s63,43008\n"
TR "a[214:215],v1 offset:37376\n"
MF "v[192:195],a[220:223],a[188:191],v[192:195]\n"
LD "v4,s[24:27],s84 offen lds\n"
MF "v[200:203],a[220:223],a[192:195],v[200:203]\n"
"ds_read_b128 a[108:111],v3 offset:4096\n"
MF "v[200:203],a[216:219],a[152:155],v[200:203]\n"
MF "v[208:211],a[216:219],a[156:159],v[208:211]\n"
"s_add_u32 s84,s14,29696\n"
MF "v[208:211],a[220:223],a[196:199],v[208:211]\n"
"s_waitcnt lgkmcnt(7)\n"
LD "v5,s[24:27],s84 offen offset:1024 lds\n"
MF "v[212:215],a[204:207],a[196:199],v[212:215]\n"
TR "a[216:217],v1 offset:32776\n"
MF "v[212:215],a[200:203],a[156:159],v[212:215]\n"
TR "a[218:219],v1 offset:33288\n"
MF "v[204:207],a[200:203],a[152:155],v[204:207]\n"
"ds_read_b128 a[72:75],v2 offset:6144\n"
MF "v[204:207],a[204:207],a[192:195],v[204:207]\n"
TR "a[220:221],v1 offset:36872\n"
MF "v[196:199],a[204:207],a[188:191],v[196:199]\n"
TR "a[222:223],v1 offset:37384\n"
MF "v[196:199],a[200:203],a[148:151],v[196:199]\n"
MF "v[188:191],a[200:203],a[144:147],v[188:191]\n"
"v_add_u32 v0,0xffff4000,v0\n"
MF "v[188:191],a[204:207],a[184:187],v[188:191]\n"
"v_add_u32 v1,0xffff4000,v1\n"
MF "v[180:183],a[204:207],a[180:183],v[180:183]\n"
"s_waitcnt vmcnt(25) lgkmcnt(0)\n"
MF "v[180:183],a[200:203],a[140:143],v[180:183]\n"
"s_barrier\n"
MF "v[216:219],a[208:211],a[140:143],v[216:219]\n"
TR "a[200:201],v0\n"
MF "v[216:219],a[212:215],a[180:183],v[216:219]\n"
"s_add_u32 m0,s62,81920\n"
TR "a[202:203],v0 offset:512\n"
MF "v[224:227],a[212:215],a[184:187],v[224:227]\n"
"s_add_u32 s82,s66,5505280\n"
"ds_read_b128 a[112:115],v3 offset:6144\n"
MF "v[224:227],a[208:211],a[144:147],v[224:227]\n"
"s_sub_u32 s15,s82,7864320\n"
TR "a[204:205],v0 offset:4096\n"
MF "v[232:235],a[208:211],a[148:151],v[232:235]\n"
"s_min_u32 s82,s82,s15\n"
TR "a[206:207],v0 offset:4608\n"
MF "v[232:235],a[212:215],a[188:191],v[232:235]\n"
LD "v6,s[28:31],s82 offen lds\n"
MF "v[240:243],a[212:215],a[192:195],v[240:243]\n"
"ds_read_b128 a[76:79],v2 offset:8192\n"
MF "v[240:243],a[208:211],a[152:155],v[240:243]\n"
"ds_read_b128 a[116:119],v3 offset:8192\n"
MF "v[248:251],a[208:211],a[156:159],v[248:251]\n"
LD "v11,s[28:31],s82 offen offset:1024 lds\n"
MF "v[248:251],a[212:215],a[196:199],v[248:251]\n"
MF "v[252:255],a[220:223],a[196:199],v[252:255]\n"
TR "a[208:209],v0 offset:8\n"
MF "v[252:255],a[216:219],a[156:159],v[252:255]\n"
TR "a[210:211],v0 offset:520\n"
MF "v[244:247],a[216:219],a[152:155],v[244:247]\n"
MF "v[244:247],a[220:223],a[192:195],v[244:247]\n"
TR "a[212:213],v0 offset:4104\n"
MF "v[236:239],a[220:223],a[188:191],v[236:239]\n"
TR "a[214:215],v0 offset:4616\n"
MF "v[236:239],a[216:219],a[148:151],v[236:239]\n"
LD "v12,s[28:31],s82 offen offset:2048 lds\n"
MF "v[228:231],a[216:219],a[144:147],v[228:231]\n"
MF "v[228:231],a[220:223],a[184:187],v[228:231]\n"
MF "v[220:223],a[220:223],a[180:183],v[220:223]\n"
MF "v[220:223],a[216:219],a[140:143],v[220:223]\n"
"s_waitcnt lgkmcnt(4)\n"
MF "v[16:19],a[200:203],a[60:63],v[16:19]\n"
TR "a[216:217],v1\n"
MF "v[16:19],a[204:207],a[100:103],v[16:19]\n"
TR "a[218:219],v1 offset:512\n"
MF "v[24:27],a[204:207],a[104:107],v[24:27]\n"
LD "v13,s[28:31],s82 offen offset:3072 lds\n"
MF "v[24:27],a[200:203],a[64:67],v[24:27]\n"
TR "a[220:221],v1 offset:4096\n"
MF "v[32:35],a[200:203],a[68:71],v[32:35]\n"
TR "a[222:223],v1 offset:4608\n"
MF "v[32:35],a[204:207],a[108:111],v[32:35]\n"
MF "v[40:43],a[204:207],a[112:115],v[40:43]\n"
MF "v[40:43],a[200:203],a[72:75],v[40:43]\n"
"s_add_u32 s84,s14,40960\n"
MF "v[48:51],a[200:203],a[76:79],v[48:51]\n"
"s_add_u32 m0,s63,45056\n"
MF "v[48:51],a[204:207],a[116:119],v[48:51]\n"
"s_waitcnt lgkmcnt(4)\n"
LD "v4,s[24:27],s84 offen lds\n"
MF "v[52:55],a[212:215],a[116:119],v[52:55]\n"
TR "a[200:201],v1 offset:8\n"
MF "v[52:55],a[208:211],a[76:79],v[52:55]\n"
TR "a[202:203],v1 offset:520\n"
MF "v[44:47],a[208:211],a[72:75],v[44:47]\n"
MF "v[44:47],a[212:215],a[112:115],v[44:47]\n"
TR "a[204:205],v1 offset:4104\n"
MF "v[36:39],a[212:215],a[108:111],v[36:39]\n"
TR "a[206:207],v1 offset:4616\n"
MF "v[36:39],a[208:211],a[68:71],v[36:39]\n"
MF "v[28:31],a[208:211],a[64:67],v[28:31]\n"
MF "v[28:31],a[212:215],a[104:107],v[28:31]\n"
MF "v[20:23],a[212:215],a[100:103],v[20:23]\n"
"s_waitcnt vmcnt(24) lgkmcnt(0)\n"
MF "v[20:23],a[208:211],a[60:63],v[20:23]\n"
"s_barrier\n"
MF "v[56:59],a[216:219],a[60:63],v[56:59]\n"
"s_add_u32 m0,s62,0\n"
"s_add_u32 s82,s66,6291712\n"
TR "a[208:209],v0 offset:16384\n"
MF "v[56:59],a[220:223],a[100:103],v[56:59]\n"
"s_sub_u32 s15,s82,7864320\n"
"s_min_u32 s82,s82,s15\n"
TR "a[210:211],v0 offset:16896\n"
MF "v[64:67],a[220:223],a[104:107],v[64:67]\n"
LD "v6,s[28:31],s82 offen lds\n"
MF "v[64:67],a[216:219],a[64:67],v[64:67]\n"
TR "a[212:213],v0 offset:20480\n"
MF "v[72:75],a[216:219],a[68:71],v[72:75]\n"
TR "a[214:215],v0 offset:20992\n"
MF "v[72:75],a[220:223],a[108:111],v[72:75]\n"
MF "v[80:83],a[220:223],a[112:115],v[80:83]\n"
LD "v11,s[28:31],s82 offen offset:1024 lds\n"
MF "v[80:83],a[216:219],a[72:75],v[80:83]\n"
MF "v[88:91],a[216:219],a[76:79],v[88:91]\n"
MF "v[88:91],a[220:223],a[116:119],v[88:91]\n"
MF "v[92:95],a[204:207],a[116:119],v[92:95]\n"
TR "a[216:217],v0 offset:16392\n"
MF "v[92:95],a[200:203],a[76:79],v[92:95]\n"
TR "a[218:219],v0 offset:16904\n"
MF "v[84:87],a[200:203],a[72:75],v[84:87]\n"
LD "v12,s[28:31],s82 offen offset:2048 lds\n"
MF "v[84:87],a[204:207],a[112:115],v[84:87]\n"
TR "a[220:221],v0 offset:20488\n"
MF "v[76:79],a[204:207],a[108:111],v[76:79]\n"
TR "a[222:223],v0 offset:21000\n"
MF "v[76:79],a[200:203],a[68:71],v[76:79]\n"
MF "v[68:71],a[200:203],a[64:67],v[68:71]\n"
LD "v13,s[28:31],s82 offen offset:3072 lds\n"
MF "v[68:71],a[204:207],a[104:107],v[68:71]\n"
MF "v[60:63],a[204:207],a[100:103],v[60:63]\n"
MF "v[60:63],a[200:203],a[60:63],v[60:63]\n"
"s_add_u32 s14,s81,1152\n"
"s_sub_u32 s15,s14,1280\n"
"s_waitcnt lgkmcnt(4)\n"
MF "v[96:99],a[208:211],a[60:63],v[96:99]\n"
"s_min_u32 s14,s14,s15\n"
"s_add_u32 s14,s14,s68\n"
TR "a[200:201],v1 offset:16384\n"
MF "v[96:99],a[212:215],a[100:103],v[96:99]\n"
"s_add_u32 s84,s14,0\n"
"s_add_u32 m0,s63,0\n"
TR "a[202:203],v1 offset:16896\n"
MF "v[104:107],a[212:215],a[104:107],v[104:107]\n"
LD "v4,s[24:27],s84 offen lds\n"
MF "v[104:107],a[208:211],a[64:67],v[104:107]\n"
TR "a[204:205],v1 offset:20480\n"
MF "v[112:115],a[208:211],a[68:71],v[112:115]\n"
TR "a[206:207],v1 offset:20992\n"
MF "v[112:115],a[212:215],a[108:111],v[112:115]\n"
"s_add_u32 s84,s14,9216\n"
MF "v[120:123],a[212:215],a[112:115],v[120:123]\n"
LD "v5,s[24:27],s84 offen offset:1024 lds\n"
MF "v[120:123],a[208:211],a[72:75],v[120:123]\n"
MF "v[128:131],a[208:211],a[76:79],v[128:131]\n"
MF "v[128:131],a[212:215],a[116:119],v[128:131]\n"
"s_waitcnt lgkmcnt(4)\n"
MF "v[132:135],a[220:223],a[116:119],v[132:135]\n"
TR "a[208:209],v1 offset:16392\n"
MF "v[132:135],a[216:219],a[76:79],v[132:135]\n"
"s_add_u32 s84,s14,18432\n"
TR "a[210:211],v1 offset:16904\n"
MF "v[124:127],a[216:219],a[72:75],v[124:127]\n"
LD "v4,s[24:27],s84 offen offset:2048 lds\n"
MF "v[124:127],a[220:223],a[112:115],v[124:127]\n"
TR "a[212:213],v1 offset:20488\n"
MF "v[116:119],a[220:223],a[108:111],v[116:119]\n"
TR "a[214:215],v1 offset:21000\n"
MF "v[116:119],a[216:219],a[68:71],v[116:119]\n"
MF "v[108:111],a[216:219],a[64:67],v[108:111]\n"
MF "v[108:111],a[220:223],a[104:107],v[108:111]\n"
MF "v[100:103],a[220:223],a[100:103],v[100:103]\n"
"s_waitcnt vmcnt(24) lgkmcnt(0)\n"
MF "v[100:103],a[216:219],a[60:63],v[100:103]\n"
"s_barrier\n"
MF "v[136:139],a[200:203],a[60:63],v[136:139]\n"
TR "a[216:217],v0 offset:32768\n"
MF "v[136:139],a[204:207],a[100:103],v[136:139]\n"
"s_add_u32 m0,s62,16384\n"
TR "a[218:219],v0 offset:33280\n"
MF "v[144:147],a[204:207],a[104:107],v[144:147]\n"
"s_add_u32 s82,s66,7078144\n"
"ds_read_b128 a[140:143],v2 offset:20480\n"
MF "v[144:147],a[200:203],a[64:67],v[144:147]\n"
"s_sub_u32 s15,s82,7864320\n"
TR "a[220:221],v0 offset:36864\n"
MF "v[152:155],a[200:203],a[68:71],v[152:155]\n"
"s_min_u32 s82,s82,s15\n"
TR "a[222:223],v0 offset:37376\n"
MF "v[152:155],a[204:207],a[108:111],v[152:155]\n"
LD "v6,s[28:31],s82 offen lds\n"
MF "v[160:163],a[204:207],a[112:115],v[160:163]\n"
"ds_read_b128 a[180:183],v3 offset:20480\n"
MF "v[160:163],a[200:203],a[72:75],v[160:163]\n"
LD "v11,s[28:31],s82 offen offset:1024 lds\n"
MF "v[168:171],a[200:203],a[76:79],v[168:171]\n"
MF "v[168:171],a[204:207],a[116:119],v[168:171]\n"
LD "v12,s[28:31],s82 offen offset:2048 lds\n"
MF "v[172:175],a[212:215],a[116:119],v[172:175]\n"
TR "a[200:201],v0 offset:32776\n"
MF "v[172:175],a[208:211],a[76:79],v[172:175]\n"
TR "a[202:203],v0 offset:33288\n"
MF "v[164:167],a[208:211],a[72:75],v[164:167]\n"
"ds_read_b128 a[144:147],v2 offset:22528\n"
MF "v[164:167],a[212:215],a[112:115],v[164:167]\n"
TR "a[204:205],v0 offset:36872\n"
MF "v[156:159],a[212:215],a[108:111],v[156:159]\n"
TR "a[206:207],v0 offset:37384\n"
MF "v[156:159],a[208:211],a[68:71],v[156:159]\n"
MF "v[148:151],a[208:211],a[64:67],v[148:151]\n"
"ds_read_b128 a[184:187],v3 offset:22528\n"
MF "v[148:151],a[212:215],a[104:107],v[148:151]\n"
MF "v[140:143],a[212:215],a[100:103],v[140:143]\n"
LD "v13,s[28:31],s82 offen offset:3072 lds\n"
MF "v[140:143],a[208:211],a[60:63],v[140:143]\n"
"s_waitcnt lgkmcnt(7)\n"
MF "v[176:179],a[216:219],a[60:63],v[176:179]\n"
TR "a[208:209],v1 offset:32768\n"
MF "v[176:179],a[220:223],a[100:103],v[176:179]\n"
TR "a[210:211],v1 offset:33280\n"
MF "v[184:187],a[220:223],a[104:107],v[184:187]\n"
"ds_read_b128 a[148:151],v2 offset:24576\n"
MF "v[184:187],a[216:219],a[64:67],v[184:187]\n"
"s_add_u32 s84,s14,30720\n"
TR "a[212:213],v1 offset:36864\n"
MF "v[192:195],a[216:219],a[68:71],v[192:195]\n"
"s_add_u32 m0,s63,3072\n"
TR "a[214:215],v1 offset:37376\n"
MF "v[192:195],a[220:223],a[108:111],v[192:195]\n"
LD "v5,s[24:27],s84 offen lds\n"
MF "v[200:203],a[220:223],a[112:115],v[200:203]\n"
"ds_read_b128 a[188:191],v3 offset:24576\n"
MF "v[200:203],a[216:219],a[72:75],v[200:203]\n"
MF "v[208:211],a[216:219],a[76:79],v[208:211]\n"
"s_add_u32 s84,s14,39936\n"
MF "v[208:211],a[220:223],a[116:119],v[208:211]\n"
"s_waitcnt lgkmcnt(7)\n"
LD "v4,s[24:27],s84 offen offset:1024 lds\n"
MF "v[212:215],a[204:207],a[116:119],v[212:215]\n"
TR "a[216:217],v1 offset:32776\n"
MF "v[212:215],a[200:203],a[76:79],v[212:215]\n"
TR "a[218:219],v1 offset:33288\n"
MF "v[204:207],a[200:203],a[72:75],v[204:207]\n"
"ds_read_b128 a[152:155],v2 offset:26624\n"
MF "v[204:207],a[204:207],a[112:115],v[204:207]\n"
TR "a[220:221],v1 offset:36872\n"
MF "v[196:199],a[204:207],a[108:111],v[196:199]\n"
TR "a[222:223],v1 offset:37384\n"
MF "v[196:199],a[200:203],a[68:71],v[196:199]\n"
MF "v[188:191],a[200:203],a[64:67],v[188:191]\n"
"v_add_u32 v0,0xc000,v0\n"
MF "v[188:191],a[204:207],a[104:107],v[188:191]\n"
"v_add_u32 v1,0xc000,v1\n"
MF "v[180:183],a[204:207],a[100:103],v[180:183]\n"
"s_waitcnt vmcnt(26) lgkmcnt(0)\n"
".Lpgqj_a:\n"
"s_cbranch_execz .Lisr1\n"
".Lqj:\n"
MF "v[180:183],a[200:203],a[60:63],v[180:183]\n"
"s_barrier\n"
"s_getpc_b64 s[52:53]\n"
".Lctq:\n"
"s_sub_u32 s52,s52,.Lctq-.Lcode0\n"
"s_subb_u32 s53,s53,0\n"
"s_bfe_u32 s58,s23,0x50000\n"
"s_lshl_b32 s58,s58,2\n"
"s_add_u32 s58,s58,s56\n"
"s_nop 0\n"
"s_mul_i32 s58,s58,1280\n"
"v_min_u32 v15,144,v9\n"
"v_lshl_add_u32 v15,v15,3,s58\n"
"v_min_u32 v15,.Lcode1-.Lcode0,v15\n"
"global_load_dword a249,v15,s[52:53]\n"
MF "v[216:219],a[208:211],a[60:63],v[216:219]\n"
TR "a[200:201],v0\n"
MF "v[216:219],a[212:215],a[100:103],v[216:219]\n"
"s_add_u32 m0,s62,32768\n"
TR "a[202:203],v0 offset:512\n"
MF "v[224:227],a[212:215],a[104:107],v[224:227]\n"
"s_add_u32 s82,s66,6291584\n"
"ds_read_b128 a[192:195],v3 offset:26624\n"
MF "v[224:227],a[208:211],a[64:67],v[224:227]\n"
"s_sub_u32 s15,s82,7864320\n"
TR "a[204:205],v0 offset:4096\n"
MF "v[232:235],a[208:211],a[68:71],v[232:235]\n"
"s_min_u32 s82,s82,s15\n"
TR "a[206:207],v0 offset:4608\n"
MF "v[232:235],a[212:215],a[108:111],v[232:235]\n"
LD "v6,s[28:31],s82 offen lds\n"
MF "v[240:243],a[212:215],a[112:115],v[240:243]\n"
"ds_read_b128 a[156:159],v2 offset:28672\n"
MF "v[240:243],a[208:211],a[72:75],v[240:243]\n"
"ds_read_b128 a[196:199],v3 offset:28672\n"
MF "v[248:251],a[208:211],a[76:79],v[248:251]\n"
MF "v[248:251],a[212:215],a[116:119],v[248:251]\n"
LD "v11,s[28:31],s82 offen offset:1024 lds\n"
MF "v[252:255],a[220:223],a[116:119],v[252:255]\n"
TR "a[208:209],v0 offset:8\n"
MF "v[252:255],a[216:219],a[76:79],v[252:255]\n"
TR "a[210:211],v0 offset:520\n"
MF "v[244:247],a[216:219],a[72:75],v[244:247]\n"
MF "v[244:247],a[220:223],a[112:115],v[244:247]\n"
TR "a[212:213],v0 offset:4104\n"
MF "v[236:239],a[220:223],a[108:111],v[236:239]\n"
TR "a[214:215],v0 offset:4616\n"
MF "v[236:239],a[216:219],a[68:71],v[236:239]\n"
MF "v[228:231],a[216:219],a[64:67],v[228:231]\n"
MF "v[228:231],a[220:223],a[104:107],v[228:231]\n"
MF "v[220:223],a[220:223],a[100:103],v[220:223]\n"
LD "v12,s[28:31],s82 offen offset:2048 lds\n"
MF "v[220:223],a[216:219],a[60:63],v[220:223]\n"
"s_waitcnt lgkmcnt(4)\n"
MF "v[16:19],a[200:203],a[140:143],v[16:19]\n"
TR "a[216:217],v1\n"
MF "v[16:19],a[204:207],a[180:183],v[16:19]\n"
TR "a[218:219],v1 offset:512\n"
MF "v[24:27],a[204:207],a[184:187],v[24:27]\n"
MF "v[24:27],a[200:203],a[144:147],v[24:27]\n"
TR "a[220:221],v1 offset:4096\n"
MF "v[32:35],a[200:203],a[148:151],v[32:35]\n"
TR "a[222:223],v1 offset:4608\n"
MF "v[32:35],a[204:207],a[188:191],v[32:35]\n"
MF "v[40:43],a[204:207],a[192:195],v[40:43]\n"
MF "v[40:43],a[200:203],a[152:155],v[40:43]\n"
LD "v13,s[28:31],s82 offen offset:3072 lds\n"
MF "v[48:51],a[200:203],a[156:159],v[48:51]\n"
"s_memtime s[52:53]\n"
"s_memrealtime s[100:101]\n"
MF "v[48:51],a[204:207],a[196:199],v[48:51]\n"
"s_waitcnt lgkmcnt(4)\n"
MF "v[52:55],a[212:215],a[196:199],v[52:55]\n"
TR "a[200:201],v1 offset:8\n"
MF "v[52:55],a[208:211],a[156:159],v[52:55]\n"
TR "a[202:203],v1 offset:520\n"
MF "v[44:47],a[208:211],a[152:155],v[44:47]\n"
MF "v[44:47],a[212:215],a[192:195],v[44:47]\n"
TR "a[204:205],v1 offset:4104\n"
MF "v[36:39],a[212:215],a[188:191],v[36:39]\n"
TR "a[206:207],v1 offset:4616\n"
MF "v[36:39],a[208:211],a[148:151],v[36:39]\n"
MF "v[28:31],a[208:211],a[144:147],v[28:31]\n"
MF "v[28:31],a[212:215],a[184:187],v[28:31]\n"
MF "v[20:23],a[212:215],a[180:183],v[20:23]\n"
"s_waitcnt vmcnt(25) lgkmcnt(0)\n"
MF "v[20:23],a[208:211],a[140:143],v[20:23]\n"
"s_barrier\n"
MF "v[56:59],a[216:219],a[140:143],v[56:59]\n"
"s_add_u32 m0,s62,49152\n"
"s_add_u32 s82,s66,7078016\n"
TR "a[208:209],v0 offset:16384\n"
MF "v[56:59],a[220:223],a[180:183],v[56:59]\n"
"s_sub_u32 s15,s82,7864320\n"
"s_min_u32 s82,s82,s15\n"
TR "a[210:211],v0 offset:16896\n"
MF "v[64:67],a[220:223],a[184:187],v[64:67]\n"
LD "v6,s[28:31],s82 offen lds\n"
MF "v[64:67],a[216:219],a[144:147],v[64:67]\n"
"s_sub_u32 s53,s52,s80\n"
TR "a[212:213],v0 offset:20480\n"
"v_lshrrev_b32 v15,8,v9\n"
MF "v[72:75],a[216:219],a[148:151],v[72:75]\n"
TR "a[214:215],v0 offset:20992\n"
"v_mul_u32_u24 v15,48960,v15\n"
MF "v[72:75],a[220:223],a[188:191],v[72:75]\n"
"s_lshr_b32 s7,s56,1\n"
"v_add_u32 v15,v15,v9\n"
MF "v[80:83],a[220:223],a[192:195],v[80:83]\n"
"s_and_b32 s8,s56,1\n"
MF "v[80:83],a[216:219],a[152:155],v[80:83]\n"
"s_sub_u32 s7,s7,s8\n"
MF "v[88:91],a[216:219],a[156:159],v[88:91]\n"
"s_mul_i32 s7,s7,245760\n"
MF "v[88:91],a[220:223],a[196:199],v[88:91]\n"
LD "v11,s[28:31],s82 offen offset:1024 lds\n"
MF "v[92:95],a[204:207],a[196:199],v[92:95]\n"
"s_add_u32 s7,s7,s89\n"
TR "a[216:217],v0 offset:16392\n"
MF "v[92:95],a[200:203],a[156:159],v[92:95]\n"
"s_cmp_gt_u32 s53,24100\n"
"s_cselect_b32 s101,1,0\n"
TR "a[218:219],v0 offset:16904\n"
MF "v[84:87],a[200:203],a[152:155],v[84:87]\n"
"s_cmp_eq_u32 s87,2\n"
"s_cselect_b32 s87,s101,s87\n"
MF "v[84:87],a[204:207],a[192:195],v[84:87]\n"
"s_cmp_eq_u32 s69,99\n"
"s_cselect_b32 s97,124,0\n"
TR "a[220:221],v0 offset:20488\n"
MF "v[76:79],a[204:207],a[188:191],v[76:79]\n"
"s_cmp_eq_u32 s57,0\n"
"s_cselect_b32 s97,145,s97\n"
TR "a[222:223],v0 offset:21000\n"
MF "v[76:79],a[200:203],a[148:151],v[76:79]\n"
"s_mul_i32 s97,s97,s87\n"
MF "v[68:71],a[200:203],a[144:147],v[68:71]\n"
"s_getreg_b32 s101,hwreg(HW_REG_XCC_ID,0,1)\n"
MF "v[68:71],a[204:207],a[184:187],v[68:71]\n"
"s_add_u32 s101,s101,s69\n"
MF "v[60:63],a[204:207],a[180:183],v[60:63]\n"
LD "v12,s[28:31],s82 offen offset:2048 lds\n"
MF "v[60:63],a[200:203],a[140:143],v[60:63]\n"
".Lpg7a:\n"
"s_cbranch_execz .Lisr1\n"
"s_waitcnt lgkmcnt(4)\n"
MF "v[96:99],a[208:211],a[140:143],v[96:99]\n"
"s_cmp_eq_u32 s57,s101\n"
"s_cselect_b32 s97,180,s97\n"
TR "a[200:201],v1 offset:16384\n"
MF "v[96:99],a[212:215],a[180:183],v[96:99]\n"
"s_cmp_eq_u32 s97,0\n"
"s_cselect_b32 s95,0x47800000,0\n"
TR "a[202:203],v1 offset:16896\n"
MF "v[104:107],a[212:215],a[184:187],v[104:107]\n"
"s_mov_b32 s21,s100\n"
MF "v[104:107],a[208:211],a[144:147],v[104:107]\n"
"s_mov_b32 s77,s95\n"
TR "a[204:205],v1 offset:20480\n"
MF "v[112:115],a[208:211],a[148:151],v[112:115]\n"
TR "a[206:207],v1 offset:20992\n"
MF "v[112:115],a[212:215],a[188:191],v[112:115]\n"
MF "v[120:123],a[212:215],a[192:195],v[120:123]\n"
MF "v[120:123],a[208:211],a[152:155],v[120:123]\n"
LD "v13,s[28:31],s82 offen offset:3072 lds\n"
MF "v[128:131],a[208:211],a[156:159],v[128:131]\n"
MF "v[128:131],a[212:215],a[196:199],v[128:131]\n"
"s_waitcnt lgkmcnt(4)\n"
MF "v[132:135],a[220:223],a[196:199],v[132:135]\n"
TR "a[208:209],v1 offset:16392\n"
MF "v[132:135],a[216:219],a[156:159],v[132:135]\n"
TR "a[210:211],v1 offset:16904\n"
MF "v[124:127],a[216:219],a[152:155],v[124:127]\n"
MF "v[124:127],a[220:223],a[192:195],v[124:127]\n"
TR "a[212:213],v1 offset:20488\n"
MF "v[116:119],a[220:223],a[188:191],v[116:119]\n"
TR "a[214:215],v1 offset:21000\n"
MF "v[116:119],a[216:219],a[148:151],v[116:119]\n"
MF "v[108:111],a[216:219],a[144:147],v[108:111]\n"
MF "v[108:111],a[220:223],a[184:187],v[108:111]\n"
MF "v[100:103],a[220:223],a[180:183],v[100:103]\n"
"s_waitcnt vmcnt(22) lgkmcnt(0)\n"
MF "v[100:103],a[216:219],a[140:143],v[100:103]\n"
"s_barrier\n"
"s_cmp_eq_u32 s57,1\n"
"s_cbranch_scc0 .Lhs15\n"
".Lack_q:\n"
"v_cmp_ne_u32 vcc,s76,v10\n"
"s_cbranch_vccnz .Lackspin_q\n"
".Lackok_q:\n"
".Lhs15:\n"
MF "v[136:139],a[200:203],a[140:143],v[136:139]\n"
"s_and_b32 s58,s56,1\n"
TR "a[216:217],v0 offset:32768\n"
MF "v[136:139],a[204:207],a[180:183],v[136:139]\n"
"s_add_u32 m0,s62,65536\n"
TR "a[218:219],v0 offset:33280\n"
MF "v[144:147],a[204:207],a[184:187],v[144:147]\n"
"s_add_u32 s82,s66,6291456\n"
"ds_read_b128 a[60:63],v2 offset:40960\n"
MF "v[144:147],a[200:203],a[144:147],v[144:147]\n"
"s_sub_u32 s15,s82,7864320\n"
TR "a[220:221],v0 offset:36864\n"
MF "v[152:155],a[200:203],a[148:151],v[152:155]\n"
"s_min_u32 s82,s82,s15\n"
TR "a[222:223],v0 offset:37376\n"
MF "v[152:155],a[204:207],a[188:191],v[152:155]\n"
LD "v6,s[28:31],s82 offen lds\n"
MF "v[160:163],a[204:207],a[192:195],v[160:163]\n"
"s_mul_i32 s59,s58,3072\n"
"ds_read_b128 a[100:103],v3 offset:40960\n"
MF "v[160:163],a[200:203],a[152:155],v[160:163]\n"
"s_add_u32 s16,s70,s59\n"
MF "v[168:171],a[200:203],a[156:159],v[168:171]\n"
"s_mul_i32 s59,s58,6144\n"
MF "v[168:171],a[204:207],a[196:199],v[168:171]\n"
LD "v11,s[28:31],s82 offen offset:1024 lds\n"
MF "v[172:175],a[212:215],a[196:199],v[172:175]\n"
"s_add_u32 s18,s70,s59\n"
TR "a[200:201],v0 offset:32776\n"
MF "v[172:175],a[208:211],a[156:159],v[172:175]\n"
"s_add_u32 s17,s16,196608\n"
TR "a[202:203],v0 offset:33288\n"
MF "v[164:167],a[208:211],a[152:155],v[164:167]\n"
"s_add_u32 s19,s18,196608\n"
"ds_read_b128 a[64:67],v2 offset:43008\n"
MF "v[164:167],a[212:215],a[192:195],v[164:167]\n"
"s_add_u32 s20,s70,196608\n"
TR "a[204:205],v0 offset:36872\n"
MF "v[156:159],a[212:215],a[188:191],v[156:159]\n"
"s_bfe_u32 s58,s23,0x40008\n"
TR "a[206:207],v0 offset:37384\n"
MF "v[156:159],a[208:211],a[148:151],v[156:159]\n"
"s_lshr_b32 s59,s56,1\n"
MF "v[148:151],a[208:211],a[144:147],v[148:151]\n"
"s_mul_i32 s59,s59,5\n"
"ds_read_b128 a[104:107],v3 offset:43008\n"
MF "v[148:151],a[212:215],a[184:187],v[148:151]\n"
"s_add_u32 s58,s58,s59\n"
MF "v[140:143],a[212:215],a[180:183],v[140:143]\n"
LD "v12,s[28:31],s82 offen offset:2048 lds\n"
MF "v[140:143],a[208:211],a[140:143],v[140:143]\n"
"s_waitcnt lgkmcnt(7)\n"
MF "v[176:179],a[216:219],a[140:143],v[176:179]\n"
"s_and_b32 s58,s58,15\n"
TR "a[208:209],v1 offset:32768\n"
MF "v[176:179],a[220:223],a[180:183],v[176:179]\n"
"s_mul_i32 s58,s58,12288\n"
TR "a[210:211],v1 offset:33280\n"
MF "v[184:187],a[220:223],a[184:187],v[184:187]\n"
"s_add_u32 s72,s16,s58\n"
"ds_read_b128 a[68:71],v2 offset:45056\n"
MF "v[184:187],a[216:219],a[144:147],v[184:187]\n"
"s_add_u32 s73,s18,s58\n"
TR "a[212:213],v1 offset:36864\n"
MF "v[192:195],a[216:219],a[148:151],v[192:195]\n"
"s_add_u32 s74,s70,s58\n"
TR "a[214:215],v1 offset:37376\n"
MF "v[192:195],a[220:223],a[188:191],v[192:195]\n"
"s_add_u32 s74,s74,12288\n"
MF "v[200:203],a[220:223],a[192:195],v[200:203]\n"
"s_cmp_eq_u32 s74,s20\n"
"s_cselect_b32 s74,s70,s74\n"
"ds_read_b128 a[108:111],v3 offset:45056\n"
MF "v[200:203],a[216:219],a[152:155],v[200:203]\n"
LD "v13,s[28:31],s82 offen offset:3072 lds\n"
MF "v[208:211],a[216:219],a[156:159],v[208:211]\n"
"s_lshr_b32 s58,s56,1\n"
MF "v[208:211],a[220:223],a[196:199],v[208:211]\n"
"s_waitcnt lgkmcnt(7)\n"
MF "v[212:215],a[204:207],a[196:199],v[212:215]\n"
"s_mul_i32 s58,s58,6144\n"
TR "a[216:217],v1 offset:32776\n"
MF "v[212:215],a[200:203],a[156:159],v[212:215]\n"
"s_mul_i32 s64,s56,3072\n"
TR "a[218:219],v1 offset:33288\n"
MF "v[204:207],a[200:203],a[152:155],v[204:207]\n"
"s_add_u32 s64,s64,s58\n"
"ds_read_b128 a[72:75],v2 offset:47104\n"
MF "v[204:207],a[204:207],a[192:195],v[204:207]\n"
"s_mul_i32 s65,s56,6144\n"
TR "a[220:221],v1 offset:36872\n"
MF "v[196:199],a[204:207],a[188:191],v[196:199]\n"
"s_sub_u32 s58,s71,s70\n"
TR "a[222:223],v1 offset:37384\n"
MF "v[196:199],a[200:203],a[148:151],v[196:199]\n"
"s_add_u32 s96,s74,s58\n"
MF "v[188:191],a[200:203],a[144:147],v[188:191]\n"
"s_sub_u32 s58,s16,s70\n"
"v_add_u32 v0,0xffff4000,v0\n"
MF "v[188:191],a[204:207],a[184:187],v[188:191]\n"
"s_add_u32 s96,s96,s58\n"
"v_add_u32 v1,0xffff4000,v1\n"
MF "v[180:183],a[204:207],a[180:183],v[180:183]\n"
"s_waitcnt vmcnt(13) lgkmcnt(0)\n"
MF "v[180:183],a[200:203],a[140:143],v[180:183]\n"
"s_barrier\n"
MF "v[216:219],a[208:211],a[140:143],v[216:219]\n"
"s_max_u32 s60,s70,s71\n"
TR "a[200:201],v1 offset:8\n"
MF "v[216:219],a[212:215],a[180:183],v[216:219]\n"
"s_add_u32 m0,s62,81920\n"
TR "a[202:203],v1 offset:520\n"
"v_mov_b32 v14,s12\n"
MF "v[224:227],a[212:215],a[184:187],v[224:227]\n"
"s_add_u32 s82,s66,7077888\n"
"ds_read_b128 a[112:115],v3 offset:47104\n"
"v_mul_f32 v14,0xbdd53b95,v14\n"
MF "v[224:227],a[208:211],a[144:147],v[224:227]\n"
"s_sub_u32 s15,s82,7864320\n"
TR "a[204:205],v1 offset:4104\n"
MF "v[232:235],a[208:211],a[148:151],v[232:235]\n"
"s_min_u32 s82,s82,s15\n"
TR "a[206:207],v1 offset:4616\n"
MF "v[232:235],a[212:215],a[188:191],v[232:235]\n"
LD "v6,s[28:31],s82 offen lds\n"
MF "v[240:243],a[212:215],a[192:195],v[240:243]\n"
"s_add_u32 s34,s60,196608\n"
"ds_read_b128 a[76:79],v2 offset:49152\n"
MF "v[240:243],a[208:211],a[152:155],v[240:243]\n"
"s_add_u32 s38,s60,196608\n"
"ds_read_b128 a[116:119],v3 offset:49152\n"
MF "v[248:251],a[208:211],a[156:159],v[248:251]\n"
MF "v[248:251],a[212:215],a[196:199],v[248:251]\n"
LD "v11,s[28:31],s82 offen offset:1024 lds\n"
MF "v[220:223],a[216:219],a[140:143],v[220:223]\n"
TR "a[208:209],v1 offset:16392\n"
MF "v[220:223],a[220:223],a[180:183],v[220:223]\n"
TR "a[210:211],v1 offset:16904\n"
MF "v[228:231],a[220:223],a[184:187],v[228:231]\n"
"ds_read_b128 a[140:143],v2\n"
MF "v[228:231],a[216:219],a[144:147],v[228:231]\n"
TR "a[212:213],v1 offset:20488\n"
MF "v[236:239],a[216:219],a[148:151],v[236:239]\n"
TR "a[214:215],v1 offset:21000\n"
MF "v[236:239],a[220:223],a[188:191],v[236:239]\n"
"ds_read_b128 a[180:183],v3\n"
MF "v[244:247],a[220:223],a[192:195],v[244:247]\n"
"ds_read_b128 a[144:147],v2 offset:2048\n"
MF "v[244:247],a[216:219],a[152:155],v[244:247]\n"
"ds_read_b128 a[184:187],v3 offset:2048\n"
MF "v[252:255],a[216:219],a[156:159],v[252:255]\n"
"ds_read_b128 a[148:151],v2 offset:4096\n"
MF "v[252:255],a[220:223],a[196:199],v[252:255]\n"
"s_waitcnt lgkmcnt(9)\n"
"ds_read_b128 a[188:191],v3 offset:4096\n"
MF "v[220:223],a[200:203],a[60:63],v[220:223]\n"
TR "a[216:217],v1\n"
MF "v[220:223],a[204:207],a[100:103],v[220:223]\n"
TR "a[218:219],v1 offset:512\n"
MF "v[228:231],a[204:207],a[104:107],v[228:231]\n"
"ds_read_b128 a[152:155],v2 offset:6144\n"
MF "v[228:231],a[200:203],a[64:67],v[228:231]\n"
TR "a[220:221],v1 offset:4096\n"
MF "v[236:239],a[200:203],a[68:71],v[236:239]\n"
TR "a[222:223],v1 offset:4608\n"
MF "v[236:239],a[204:207],a[108:111],v[236:239]\n"
"ds_read_b128 a[192:195],v3 offset:6144\n"
MF "v[244:247],a[204:207],a[112:115],v[244:247]\n"
"ds_read_b128 a[156:159],v2 offset:8192\n"
MF "v[244:247],a[200:203],a[72:75],v[244:247]\n"
"ds_read_b128 a[196:199],v3 offset:8192\n"
MF "v[252:255],a[200:203],a[76:79],v[252:255]\n"
LD "v12,s[28:31],s82 offen offset:2048 lds\n"
MF "v[252:255],a[204:207],a[116:119],v[252:255]\n"
"s_waitcnt lgkmcnt(8)\n"
LD "v13,s[28:31],s82 offen offset:3072 lds\n"
MF "v[220:223],a[208:211],a[140:143],v[220:223]\n"
TR "a[200:201],v1 offset:16384\n"
MF "v[220:223],a[212:215],a[180:183],v[220:223]\n"
TR "a[202:203],v1 offset:16896\n"
MF "v[228:231],a[212:215],a[184:187],v[228:231]\n"
MF "v[228:231],a[208:211],a[144:147],v[228:231]\n"
TR "a[204:205],v1 offset:20480\n"
MF "v[236:239],a[208:211],a[148:151],v[236:239]\n"
TR "a[206:207],v1 offset:20992\n"
MF "v[236:239],a[212:215],a[188:191],v[236:239]\n"
"s_waitcnt lgkmcnt(4)\n"
MF "v[244:247],a[212:215],a[192:195],v[244:247]\n"
MF "v[244:247],a[208:211],a[152:155],v[244:247]\n"
MF "v[252:255],a[208:211],a[156:159],v[252:255]\n"
MF "v[252:255],a[212:215],a[196:199],v[252:255]\n"
"s_waitcnt lgkmcnt(0)\n"
"s_cmp_eq_u32 s69,98\n"
"s_cbranch_scc1 .Lqy\n"
".Lqf:\n"
MF "v[216:219],a[216:219],a[60:63],v[216:219]\n"
TR "a[208:209],v0 offset:8\n"
MF "v[216:219],a[220:223],a[100:103],v[216:219]\n"
TR "a[210:211],v0 offset:520\n"
MF "v[224:227],a[220:223],a[104:107],v[224:227]\n"
"buffer_load_dword a224,v15,s[36:39],s70 offen offset:2688 nt\n"
MF "v[224:227],a[216:219],a[64:67],v[224:227]\n"
TR "a[212:213],v0 offset:4104\n"
MF "v[232:235],a[216:219],a[68:71],v[232:235]\n"
TR "a[214:215],v0 offset:4616\n"
MF "v[232:235],a[220:223],a[108:111],v[232:235]\n"
"buffer_load_dword a225,v15,s[36:39],s71 offen offset:2688 nt\n"
MF "v[240:243],a[220:223],a[112:115],v[240:243]\n"
MF "v[240:243],a[216:219],a[72:75],v[240:243]\n"
"buffer_load_dword a226,v15,s[48:51],s7 offen offset:2688\n"
MF "v[248:251],a[216:219],a[76:79],v[248:251]\n"
MF "v[248:251],a[220:223],a[116:119],v[248:251]\n"
MF "v[248:251],a[200:203],a[156:159],v[248:251]\n"
TR "a[216:217],v0 offset:16392\n"
MF "v[248:251],a[204:207],a[196:199],v[248:251]\n"
TR "a[218:219],v0 offset:16904\n"
MF "v[240:243],a[204:207],a[192:195],v[240:243]\n"
MF "v[240:243],a[200:203],a[152:155],v[240:243]\n"
TR "a[220:221],v0 offset:20488\n"
MF "v[232:235],a[200:203],a[148:151],v[232:235]\n"
TR "a[222:223],v0 offset:21000\n"
MF "v[232:235],a[204:207],a[188:191],v[232:235]\n"
MF "v[224:227],a[204:207],a[184:187],v[224:227]\n"
MF "v[224:227],a[200:203],a[144:147],v[224:227]\n"
"v_fma_f32 v248,v248,s54,v14\n"
"v_fma_f32 v249,v249,s54,v14\n"
MF "v[216:219],a[200:203],a[140:143],v[216:219]\n"
"v_fma_f32 v250,v250,s54,v14\n"
"v_fma_f32 v251,v251,s54,v14\n"
MF "v[216:219],a[204:207],a[180:183],v[216:219]\n"
"s_waitcnt lgkmcnt(4)\n"
"v_fma_f32 v252,v252,s54,v14\n"
"v_fma_f32 v253,v253,s54,v14\n"
MF "v[180:183],a[208:211],a[60:63],v[180:183]\n"
TR "a[200:201],v0\n"
"v_fma_f32 v254,v254,s54,v14\n"
MF "v[180:183],a[212:215],a[100:103],v[180:183]\n"
TR "a[202:203],v0 offset:512\n"
"v_fma_f32 v255,v255,s54,v14\n"
MF "v[188:191],a[212:215],a[104:107],v[188:191]\n"
CV "v255,v254,v255\n"
CV "v254,v252,v253\n"
MF "v[188:191],a[208:211],a[64:67],v[188:191]\n"
TR "a[204:205],v0 offset:4096\n"
CV "v253,v250,v251\n"
MF "v[196:199],a[208:211],a[68:71],v[196:199]\n"
TR "a[206:207],v0 offset:4608\n"
CV "v252,v248,v249\n"
MF "v[196:199],a[212:215],a[108:111],v[196:199]\n"
"v_fma_f32 v240,v240,s54,v14\n"
"v_fma_f32 v241,v241,s54,v14\n"
MF "v[204:207],a[212:215],a[112:115],v[204:207]\n"
"v_fma_f32 v242,v242,s54,v14\n"
"v_fma_f32 v243,v243,s54,v14\n"
MF "v[204:207],a[208:211],a[72:75],v[204:207]\n"
"v_fma_f32 v244,v244,s54,v14\n"
"v_fma_f32 v245,v245,s54,v14\n"
MF "v[212:215],a[208:211],a[76:79],v[212:215]\n"
"v_fma_f32 v246,v246,s54,v14\n"
"v_fma_f32 v247,v247,s54,v14\n"
MF "v[212:215],a[212:215],a[116:119],v[212:215]\n"
"s_waitcnt lgkmcnt(4)\n"
CV "v251,v246,v247\n"
CV "v250,v244,v245\n"
MF "v[180:183],a[216:219],a[140:143],v[180:183]\n"
TR "a[208:209],v0 offset:16384\n"
CV "v249,v242,v243\n"
MF "v[180:183],a[220:223],a[180:183],v[180:183]\n"
TR "a[210:211],v0 offset:16896\n"
CV "v248,v240,v241\n"
MF "v[188:191],a[220:223],a[184:187],v[188:191]\n"
"v_fma_f32 v232,v232,s54,v14\n"
"v_fma_f32 v233,v233,s54,v14\n"
MF "v[188:191],a[216:219],a[144:147],v[188:191]\n"
TR "a[212:213],v0 offset:20480\n"
"v_fma_f32 v234,v234,s54,v14\n"
MF "v[196:199],a[216:219],a[148:151],v[196:199]\n"
TR "a[214:215],v0 offset:20992\n"
"v_fma_f32 v235,v235,s54,v14\n"
MF "v[196:199],a[220:223],a[188:191],v[196:199]\n"
"v_fma_f32 v236,v236,s54,v14\n"
"v_fma_f32 v237,v237,s54,v14\n"
MF "v[204:207],a[220:223],a[192:195],v[204:207]\n"
"v_fma_f32 v238,v238,s54,v14\n"
"v_fma_f32 v239,v239,s54,v14\n"
MF "v[204:207],a[216:219],a[152:155],v[204:207]\n"
CV "v247,v238,v239\n"
CV "v246,v236,v237\n"
MF "v[212:215],a[216:219],a[156:159],v[212:215]\n"
"s_waitcnt vmcnt(11) lgkmcnt(0)\n"
CV "v245,v234,v235\n"
CV "v244,v232,v233\n"
MF "v[212:215],a[220:223],a[196:199],v[212:215]\n"
"s_barrier\n"
MF "v[176:179],a[200:203],a[60:63],v[176:179]\n"
TR "a[216:217],v1 offset:32776\n"
"v_fma_f32 v224,v224,s54,v14\n"
MF "v[176:179],a[204:207],a[100:103],v[176:179]\n"
"s_add_u32 m0,s64,0\n"
TR "a[218:219],v1 offset:33288\n"
"v_fma_f32 v225,v225,s54,v14\n"
MF "v[184:187],a[204:207],a[104:107],v[184:187]\n"
LD "v7,s[32:35],s72 offen nt lds\n"
MF "v[184:187],a[200:203],a[64:67],v[184:187]\n"
TR "a[220:221],v1 offset:36872\n"
"v_fma_f32 v226,v226,s54,v14\n"
MF "v[192:195],a[200:203],a[68:71],v[192:195]\n"
TR "a[222:223],v1 offset:37384\n"
"v_fma_f32 v227,v227,s54,v14\n"
MF "v[192:195],a[204:207],a[108:111],v[192:195]\n"
"s_add_u32 m0,s64,896\n"
"v_fma_f32 v228,v228,s54,v14\n"
"v_fma_f32 v229,v229,s54,v14\n"
MF "v[200:203],a[204:207],a[112:115],v[200:203]\n"
LD "v7,s[32:35],s72 offen offset:128 nt lds\n"
MF "v[200:203],a[200:203],a[72:75],v[200:203]\n"
"v_fma_f32 v230,v230,s54,v14\n"
"v_fma_f32 v231,v231,s54,v14\n"
MF "v[208:211],a[200:203],a[76:79],v[208:211]\n"
CV "v243,v230,v231\n"
CV "v242,v228,v229\n"
MF "v[208:211],a[204:207],a[116:119],v[208:211]\n"
CV "v241,v226,v227\n"
CV "v240,v224,v225\n"
MF "v[208:211],a[208:211],a[156:159],v[208:211]\n"
TR "a[200:201],v1 offset:49160\n"
"v_fma_f32 v216,v216,s54,v14\n"
MF "v[208:211],a[212:215],a[196:199],v[208:211]\n"
"s_add_u32 m0,s64,1792\n"
TR "a[202:203],v1 offset:49672\n"
"v_fma_f32 v217,v217,s54,v14\n"
MF "v[200:203],a[212:215],a[192:195],v[200:203]\n"
LD "v7,s[32:35],s72 offen offset:256 nt lds\n"
MF "v[200:203],a[208:211],a[152:155],v[200:203]\n"
TR "a[204:205],v1 offset:53256\n"
"v_fma_f32 v218,v218,s54,v14\n"
MF "v[192:195],a[208:211],a[148:151],v[192:195]\n"
TR "a[206:207],v1 offset:53768\n"
"v_fma_f32 v219,v219,s54,v14\n"
MF "v[192:195],a[212:215],a[188:191],v[192:195]\n"
"v_fma_f32 v220,v220,s54,v14\n"
"v_fma_f32 v221,v221,s54,v14\n"
MF "v[184:187],a[212:215],a[184:187],v[184:187]\n"
"s_add_u32 s72,s72,6144\n"
"v_fma_f32 v222,v222,s54,v14\n"
"v_fma_f32 v223,v223,s54,v14\n"
MF "v[184:187],a[208:211],a[144:147],v[184:187]\n"
"s_add_u32 m0,s64,6144\n"
CV "v239,v222,v223\n"
CV "v238,v220,v221\n"
MF "v[176:179],a[208:211],a[140:143],v[176:179]\n"
LD "v7,s[32:35],s72 offen nt lds\n"
MF "v[176:179],a[212:215],a[180:183],v[176:179]\n"
"s_waitcnt lgkmcnt(4)\n"
CV "v237,v218,v219\n"
CV "v236,v216,v217\n"
MF "v[140:143],a[216:219],a[60:63],v[140:143]\n"
TR "a[208:209],v1 offset:32768\n"
"v_fma_f32 v208,v208,s54,v14\n"
MF "v[140:143],a[220:223],a[100:103],v[140:143]\n"
TR "a[210:211],v1 offset:33280\n"
"v_fma_f32 v209,v209,s54,v14\n"
MF "v[148:151],a[220:223],a[104:107],v[148:151]\n"
"v_fma_f32 v210,v210,s54,v14\n"
"v_fma_f32 v211,v211,s54,v14\n"
MF "v[148:151],a[216:219],a[64:67],v[148:151]\n"
TR "a[212:213],v1 offset:36864\n"
"v_fma_f32 v212,v212,s54,v14\n"
MF "v[156:159],a[216:219],a[68:71],v[156:159]\n"
"s_add_u32 m0,s64,7040\n"
TR "a[214:215],v1 offset:37376\n"
"v_fma_f32 v213,v213,s54,v14\n"
MF "v[156:159],a[220:223],a[108:111],v[156:159]\n"
LD "v7,s[32:35],s72 offen offset:128 nt lds\n"
MF "v[164:167],a[220:223],a[112:115],v[164:167]\n"
"v_fma_f32 v214,v214,s54,v14\n"
"v_fma_f32 v215,v215,s54,v14\n"
MF "v[164:167],a[216:219],a[72:75],v[164:167]\n"
CV "v235,v214,v215\n"
CV "v234,v212,v213\n"
MF "v[172:175],a[216:219],a[76:79],v[172:175]\n"
"s_add_u32 m0,s64,7936\n"
CV "v233,v210,v211\n"
CV "v232,v208,v209\n"
MF "v[172:175],a[220:223],a[116:119],v[172:175]\n"
"s_waitcnt lgkmcnt(4)\n"
LD "v7,s[32:35],s72 offen offset:256 nt lds\n"
MF "v[140:143],a[200:203],a[140:143],v[140:143]\n"
TR "a[216:217],v1 offset:49152\n"
"v_fma_f32 v200,v200,s54,v14\n"
MF "v[140:143],a[204:207],a[180:183],v[140:143]\n"
TR "a[218:219],v1 offset:49664\n"
"v_fma_f32 v201,v201,s54,v14\n"
MF "v[148:151],a[204:207],a[184:187],v[148:151]\n"
"v_fma_f32 v202,v202,s54,v14\n"
"v_fma_f32 v203,v203,s54,v14\n"
MF "v[148:151],a[200:203],a[144:147],v[148:151]\n"
TR "a[220:221],v1 offset:53248\n"
"v_fma_f32 v204,v204,s54,v14\n"
MF "v[156:159],a[200:203],a[148:151],v[156:159]\n"
TR "a[222:223],v1 offset:53760\n"
"v_fma_f32 v205,v205,s54,v14\n"
MF "v[156:159],a[204:207],a[188:191],v[156:159]\n"
"v_fma_f32 v206,v206,s54,v14\n"
"v_fma_f32 v207,v207,s54,v14\n"
MF "v[164:167],a[204:207],a[192:195],v[164:167]\n"
CV "v231,v206,v207\n"
CV "v230,v204,v205\n"
MF "v[164:167],a[200:203],a[152:155],v[164:167]\n"
CV "v229,v202,v203\n"
CV "v228,v200,v201\n"
MF "v[172:175],a[200:203],a[156:159],v[172:175]\n"
"v_fma_f32 v192,v192,s54,v14\n"
"v_fma_f32 v193,v193,s54,v14\n"
MF "v[172:175],a[204:207],a[196:199],v[172:175]\n"
"s_waitcnt lgkmcnt(0)\n"
"v_fma_f32 v194,v194,s54,v14\n"
"v_fma_f32 v195,v195,s54,v14\n"
MF "v[136:139],a[208:211],a[60:63],v[136:139]\n"
TR "a[200:201],v0 offset:32776\n"
"v_fma_f32 v196,v196,s54,v14\n"
MF "v[136:139],a[212:215],a[100:103],v[136:139]\n"
TR "a[202:203],v0 offset:33288\n"
"v_fma_f32 v197,v197,s54,v14\n"
MF "v[144:147],a[212:215],a[104:107],v[144:147]\n"
"v_fma_f32 v198,v198,s54,v14\n"
"v_fma_f32 v199,v199,s54,v14\n"
MF "v[144:147],a[208:211],a[64:67],v[144:147]\n"
TR "a[204:205],v0 offset:36872\n"
CV "v227,v198,v199\n"
MF "v[152:155],a[208:211],a[68:71],v[152:155]\n"
TR "a[206:207],v0 offset:37384\n"
CV "v226,v196,v197\n"
MF "v[152:155],a[212:215],a[108:111],v[152:155]\n"
CV "v225,v194,v195\n"
CV "v224,v192,v193\n"
MF "v[160:163],a[212:215],a[112:115],v[160:163]\n"
"v_fma_f32 v184,v184,s54,v14\n"
"v_fma_f32 v185,v185,s54,v14\n"
MF "v[160:163],a[208:211],a[72:75],v[160:163]\n"
"v_fma_f32 v186,v186,s54,v14\n"
"v_fma_f32 v187,v187,s54,v14\n"
MF "v[168:171],a[208:211],a[76:79],v[168:171]\n"
"v_fma_f32 v188,v188,s54,v14\n"
"v_fma_f32 v189,v189,s54,v14\n"
MF "v[168:171],a[212:215],a[116:119],v[168:171]\n"
"v_fma_f32 v190,v190,s54,v14\n"
"v_fma_f32 v191,v191,s54,v14\n"
MF "v[168:171],a[216:219],a[156:159],v[168:171]\n"
TR "a[208:209],v0 offset:49160\n"
CV "v223,v190,v191\n"
MF "v[168:171],a[220:223],a[196:199],v[168:171]\n"
TR "a[210:211],v0 offset:49672\n"
CV "v222,v188,v189\n"
MF "v[160:163],a[220:223],a[192:195],v[160:163]\n"
CV "v221,v186,v187\n"
CV "v220,v184,v185\n"
MF "v[160:163],a[216:219],a[152:155],v[160:163]\n"
TR "a[212:213],v0 offset:53256\n"
"v_fma_f32 v176,v176,s54,v14\n"
MF "v[152:155],a[216:219],a[148:151],v[152:155]\n"
TR "a[214:215],v0 offset:53768\n"
"v_fma_f32 v177,v177,s54,v14\n"
MF "v[152:155],a[220:223],a[188:191],v[152:155]\n"
"v_fma_f32 v178,v178,s54,v14\n"
"v_fma_f32 v179,v179,s54,v14\n"
MF "v[144:147],a[220:223],a[184:187],v[144:147]\n"
"v_fma_f32 v180,v180,s54,v14\n"
"v_fma_f32 v181,v181,s54,v14\n"
MF "v[144:147],a[216:219],a[144:147],v[144:147]\n"
"v_fma_f32 v182,v182,s54,v14\n"
"v_fma_f32 v183,v183,s54,v14\n"
MF "v[136:139],a[216:219],a[140:143],v[136:139]\n"
CV "v219,v182,v183\n"
CV "v218,v180,v181\n"
MF "v[136:139],a[220:223],a[180:183],v[136:139]\n"
"s_waitcnt lgkmcnt(4)\n"
CV "v217,v178,v179\n"
CV "v216,v176,v177\n"
MF "v[100:103],a[200:203],a[60:63],v[100:103]\n"
TR "a[216:217],v0 offset:32768\n"
"v_fma_f32 v168,v168,s54,v14\n"
MF "v[100:103],a[204:207],a[100:103],v[100:103]\n"
TR "a[218:219],v0 offset:33280\n"
"v_fma_f32 v169,v169,s54,v14\n"
MF "v[108:111],a[204:207],a[104:107],v[108:111]\n"
"v_fma_f32 v170,v170,s54,v14\n"
"v_fma_f32 v171,v171,s54,v14\n"
MF "v[108:111],a[200:203],a[64:67],v[108:111]\n"
TR "a[220:221],v0 offset:36864\n"
"v_fma_f32 v172,v172,s54,v14\n"
MF "v[116:119],a[200:203],a[68:71],v[116:119]\n"
TR "a[222:223],v0 offset:37376\n"
"v_fma_f32 v173,v173,s54,v14\n"
MF "v[116:119],a[204:207],a[108:111],v[116:119]\n"
".Lpg8b:\n"
"s_cbranch_execz .Lpg11b\n"
".Lpg8d:\n"
"s_cbranch_execz .Lisr2\n"
"v_fma_f32 v174,v174,s54,v14\n"
"v_fma_f32 v175,v175,s54,v14\n"
MF "v[124:127],a[204:207],a[112:115],v[124:127]\n"
CV "v215,v174,v175\n"
CV "v214,v172,v173\n"
MF "v[124:127],a[200:203],a[72:75],v[124:127]\n"
CV "v213,v170,v171\n"
CV "v212,v168,v169\n"
MF "v[132:135],a[200:203],a[76:79],v[132:135]\n"
"v_fma_f32 v160,v160,s54,v14\n"
"v_fma_f32 v161,v161,s54,v14\n"
MF "v[132:135],a[204:207],a[116:119],v[132:135]\n"
"s_waitcnt lgkmcnt(4)\n"
"v_fma_f32 v162,v162,s54,v14\n"
"v_fma_f32 v163,v163,s54,v14\n"
MF "v[100:103],a[208:211],a[140:143],v[100:103]\n"
TR "a[200:201],v0 offset:49152\n"
"v_fma_f32 v164,v164,s54,v14\n"
MF "v[100:103],a[212:215],a[180:183],v[100:103]\n"
TR "a[202:203],v0 offset:49664\n"
"v_fma_f32 v165,v165,s54,v14\n"
MF "v[108:111],a[212:215],a[184:187],v[108:111]\n"
"v_fma_f32 v166,v166,s54,v14\n"
"v_fma_f32 v167,v167,s54,v14\n"
MF "v[108:111],a[208:211],a[144:147],v[108:111]\n"
TR "a[204:205],v0 offset:53248\n"
CV "v211,v166,v167\n"
MF "v[116:119],a[208:211],a[148:151],v[116:119]\n"
TR "a[206:207],v0 offset:53760\n"
CV "v210,v164,v165\n"
MF "v[116:119],a[212:215],a[188:191],v[116:119]\n"
CV "v209,v162,v163\n"
CV "v208,v160,v161\n"
MF "v[124:127],a[212:215],a[192:195],v[124:127]\n"
"v_add_u32 v0,0xc000,v0\n"
"v_fma_f32 v152,v152,s54,v14\n"
MF "v[124:127],a[208:211],a[152:155],v[124:127]\n"
"v_add_u32 v1,0xc000,v1\n"
"v_fma_f32 v153,v153,s54,v14\n"
MF "v[132:135],a[208:211],a[156:159],v[132:135]\n"
"s_waitcnt vmcnt(9) lgkmcnt(0)\n"
"v_fma_f32 v154,v154,s54,v14\n"
"v_fma_f32 v155,v155,s54,v14\n"
MF "v[132:135],a[212:215],a[196:199],v[132:135]\n"
"s_barrier\n"
MF "v[96:99],a[216:219],a[60:63],v[96:99]\n"
TR "a[208:209],v1 offset:16392\n"
"v_fma_f32 v156,v156,s54,v14\n"
MF "v[96:99],a[220:223],a[100:103],v[96:99]\n"
TR "a[210:211],v1 offset:16904\n"
"v_fma_f32 v157,v157,s54,v14\n"
MF "v[104:107],a[220:223],a[104:107],v[104:107]\n"
"s_add_u32 s72,s72,6144\n"
"v_fma_f32 v158,v158,s54,v14\n"
"v_fma_f32 v159,v159,s54,v14\n"
MF "v[104:107],a[216:219],a[64:67],v[104:107]\n"
"s_cmp_eq_u32 s72,s17\n"
"s_cselect_b32 s72,s16,s72\n"
TR "a[212:213],v1 offset:20488\n"
MF "v[112:115],a[216:219],a[68:71],v[112:115]\n"
"s_add_u32 m0,s64,24576\n"
TR "a[214:215],v1 offset:21000\n"
CV "v207,v158,v159\n"
MF "v[112:115],a[220:223],a[108:111],v[112:115]\n"
LD "v7,s[32:35],s72 offen nt lds\n"
MF "v[120:123],a[220:223],a[112:115],v[120:123]\n"
"s_add_u32 m0,s64,25472\n"
CV "v206,v156,v157\n"
CV "v205,v154,v155\n"
MF "v[120:123],a[216:219],a[72:75],v[120:123]\n"
LD "v7,s[32:35],s72 offen offset:128 nt lds\n"
MF "v[128:131],a[216:219],a[76:79],v[128:131]\n"
CV "v204,v152,v153\n"
"v_fma_f32 v144,v144,s54,v14\n"
MF "v[128:131],a[220:223],a[116:119],v[128:131]\n"
"v_fma_f32 v145,v145,s54,v14\n"
"v_fma_f32 v146,v146,s54,v14\n"
MF "v[128:131],a[200:203],a[156:159],v[128:131]\n"
TR "a[216:217],v1 offset:32776\n"
"v_fma_f32 v147,v147,s54,v14\n"
MF "v[128:131],a[204:207],a[196:199],v[128:131]\n"
"s_add_u32 m0,s64,26368\n"
TR "a[218:219],v1 offset:33288\n"
"v_fma_f32 v148,v148,s54,v14\n"
MF "v[120:123],a[204:207],a[192:195],v[120:123]\n"
LD "v7,s[32:35],s72 offen offset:256 nt lds\n"
MF "v[120:123],a[200:203],a[152:155],v[120:123]\n"
TR "a[220:221],v1 offset:36872\n"
"v_fma_f32 v149,v149,s54,v14\n"
MF "v[112:115],a[200:203],a[148:151],v[112:115]\n"
TR "a[222:223],v1 offset:37384\n"
"v_fma_f32 v150,v150,s54,v14\n"
MF "v[112:115],a[204:207],a[188:191],v[112:115]\n"
"v_fma_f32 v151,v151,s54,v14\n"
CV "v203,v150,v151\n"
MF "v[104:107],a[204:207],a[184:187],v[104:107]\n"
"s_add_u32 s72,s72,6144\n"
CV "v202,v148,v149\n"
CV "v201,v146,v147\n"
MF "v[104:107],a[200:203],a[144:147],v[104:107]\n"
"s_add_u32 m0,s64,30720\n"
CV "v200,v144,v145\n"
"v_fma_f32 v136,v136,s54,v14\n"
MF "v[96:99],a[200:203],a[140:143],v[96:99]\n"
LD "v7,s[32:35],s72 offen nt lds\n"
MF "v[96:99],a[204:207],a[180:183],v[96:99]\n"
"s_waitcnt lgkmcnt(4)\n"
"v_fma_f32 v137,v137,s54,v14\n"
"v_fma_f32 v138,v138,s54,v14\n"
MF "v[60:63],a[208:211],a[60:63],v[60:63]\n"
TR "a[200:201],v1 offset:16384\n"
"v_fma_f32 v139,v139,s54,v14\n"
MF "v[60:63],a[212:215],a[100:103],v[60:63]\n"
TR "a[202:203],v1 offset:16896\n"
"v_fma_f32 v140,v140,s54,v14\n"
MF "v[68:71],a[212:215],a[104:107],v[68:71]\n"
"v_fma_f32 v141,v141,s54,v14\n"
"v_fma_f32 v142,v142,s54,v14\n"
MF "v[68:71],a[208:211],a[64:67],v[68:71]\n"
TR "a[204:205],v1 offset:20480\n"
"v_fma_f32 v143,v143,s54,v14\n"
MF "v[76:79],a[208:211],a[68:71],v[76:79]\n"
"s_add_u32 m0,s64,31616\n"
TR "a[206:207],v1 offset:20992\n"
CV "v199,v142,v143\n"
MF "v[76:79],a[212:215],a[108:111],v[76:79]\n"
LD "v7,s[32:35],s72 offen offset:128 nt lds\n"
MF "v[84:87],a[212:215],a[112:115],v[84:87]\n"
CV "v198,v140,v141\n"
CV "v197,v138,v139\n"
MF "v[84:87],a[208:211],a[72:75],v[84:87]\n"
CV "v196,v136,v137\n"
"v_fma_f32 v128,v128,s54,v14\n"
MF "v[92:95],a[208:211],a[76:79],v[92:95]\n"
"s_add_u32 m0,s64,32512\n"
"v_fma_f32 v129,v129,s54,v14\n"
"v_fma_f32 v130,v130,s54,v14\n"
MF "v[92:95],a[212:215],a[116:119],v[92:95]\n"
"s_waitcnt lgkmcnt(4)\n"
LD "v7,s[32:35],s72 offen offset:256 nt lds\n"
MF "v[60:63],a[216:219],a[140:143],v[60:63]\n"
TR "a[208:209],v1 offset:32768\n"
"v_fma_f32 v131,v131,s54,v14\n"
MF "v[60:63],a[220:223],a[180:183],v[60:63]\n"
TR "a[210:211],v1 offset:33280\n"
"v_fma_f32 v132,v132,s54,v14\n"
MF "v[68:71],a[220:223],a[184:187],v[68:71]\n"
"v_fma_f32 v133,v133,s54,v14\n"
"v_fma_f32 v134,v134,s54,v14\n"
MF "v[68:71],a[216:219],a[144:147],v[68:71]\n"
TR "a[212:213],v1 offset:36864\n"
"v_fma_f32 v135,v135,s54,v14\n"
MF "v[76:79],a[216:219],a[148:151],v[76:79]\n"
TR "a[214:215],v1 offset:37376\n"
CV "v195,v134,v135\n"
MF "v[76:79],a[220:223],a[188:191],v[76:79]\n"
CV "v194,v132,v133\n"
CV "v193,v130,v131\n"
MF "v[84:87],a[220:223],a[192:195],v[84:87]\n"
CV "v192,v128,v129\n"
"v_fma_f32 v120,v120,s54,v14\n"
MF "v[84:87],a[216:219],a[152:155],v[84:87]\n"
"v_fma_f32 v121,v121,s54,v14\n"
"v_fma_f32 v122,v122,s54,v14\n"
MF "v[92:95],a[216:219],a[156:159],v[92:95]\n"
"v_fma_f32 v123,v123,s54,v14\n"
"v_fma_f32 v124,v124,s54,v14\n"
MF "v[92:95],a[220:223],a[196:199],v[92:95]\n"
"s_waitcnt lgkmcnt(0)\n"
"v_fma_f32 v125,v125,s54,v14\n"
"v_fma_f32 v126,v126,s54,v14\n"
MF "v[56:59],a[200:203],a[60:63],v[56:59]\n"
TR "a[216:217],v0 offset:16392\n"
"v_fma_f32 v127,v127,s54,v14\n"
MF "v[56:59],a[204:207],a[100:103],v[56:59]\n"
TR "a[218:219],v0 offset:16904\n"
CV "v191,v126,v127\n"
MF "v[64:67],a[204:207],a[104:107],v[64:67]\n"
CV "v190,v124,v125\n"
CV "v189,v122,v123\n"
MF "v[64:67],a[200:203],a[64:67],v[64:67]\n"
TR "a[220:221],v0 offset:20488\n"
CV "v188,v120,v121\n"
MF "v[72:75],a[200:203],a[68:71],v[72:75]\n"
TR "a[222:223],v0 offset:21000\n"
"v_fma_f32 v112,v112,s54,v14\n"
MF "v[72:75],a[204:207],a[108:111],v[72:75]\n"
"v_fma_f32 v113,v113,s54,v14\n"
"v_fma_f32 v114,v114,s54,v14\n"
MF "v[80:83],a[204:207],a[112:115],v[80:83]\n"
"v_fma_f32 v115,v115,s54,v14\n"
"v_fma_f32 v116,v116,s54,v14\n"
MF "v[80:83],a[200:203],a[72:75],v[80:83]\n"
"v_fma_f32 v117,v117,s54,v14\n"
"v_fma_f32 v118,v118,s54,v14\n"
MF "v[88:91],a[200:203],a[76:79],v[88:91]\n"
"v_fma_f32 v119,v119,s54,v14\n"
CV "v187,v118,v119\n"
MF "v[88:91],a[204:207],a[116:119],v[88:91]\n"
CV "v186,v116,v117\n"
CV "v185,v114,v115\n"
MF "v[88:91],a[208:211],a[156:159],v[88:91]\n"
TR "a[200:201],v0 offset:32776\n"
CV "v184,v112,v113\n"
MF "v[88:91],a[212:215],a[196:199],v[88:91]\n"
TR "a[202:203],v0 offset:33288\n"
"v_fma_f32 v104,v104,s54,v14\n"
MF "v[80:83],a[212:215],a[192:195],v[80:83]\n"
"v_fma_f32 v105,v105,s54,v14\n"
"v_fma_f32 v106,v106,s54,v14\n"
MF "v[80:83],a[208:211],a[152:155],v[80:83]\n"
TR "a[204:205],v0 offset:36872\n"
"v_fma_f32 v107,v107,s54,v14\n"
MF "v[72:75],a[208:211],a[148:151],v[72:75]\n"
TR "a[206:207],v0 offset:37384\n"
"v_fma_f32 v108,v108,s54,v14\n"
MF "v[72:75],a[212:215],a[188:191],v[72:75]\n"
"v_fma_f32 v109,v109,s54,v14\n"
"v_fma_f32 v110,v110,s54,v14\n"
MF "v[64:67],a[212:215],a[184:187],v[64:67]\n"
"v_fma_f32 v111,v111,s54,v14\n"
CV "v183,v110,v111\n"
MF "v[64:67],a[208:211],a[144:147],v[64:67]\n"
CV "v182,v108,v109\n"
CV "v181,v106,v107\n"
MF "v[56:59],a[208:211],a[140:143],v[56:59]\n"
CV "v180,v104,v105\n"
"v_fma_f32 v96,v96,s54,v14\n"
MF "v[56:59],a[212:215],a[180:183],v[56:59]\n"
"s_waitcnt lgkmcnt(4)\n"
"v_fma_f32 v97,v97,s54,v14\n"
"v_fma_f32 v98,v98,s54,v14\n"
MF "v[20:23],a[216:219],a[60:63],v[20:23]\n"
TR "a[208:209],v0 offset:16384\n"
"v_fma_f32 v99,v99,s54,v14\n"
MF "v[20:23],a[220:223],a[100:103],v[20:23]\n"
TR "a[210:211],v0 offset:16896\n"
"v_fma_f32 v100,v100,s54,v14\n"
MF "v[28:31],a[220:223],a[104:107],v[28:31]\n"
"v_fma_f32 v101,v101,s54,v14\n"
"v_fma_f32 v102,v102,s54,v14\n"
MF "v[28:31],a[216:219],a[64:67],v[28:31]\n"
TR "a[212:213],v0 offset:20480\n"
"v_fma_f32 v103,v103,s54,v14\n"
MF "v[36:39],a[216:219],a[68:71],v[36:39]\n"
TR "a[214:215],v0 offset:20992\n"
CV "v179,v102,v103\n"
MF "v[36:39],a[220:223],a[108:111],v[36:39]\n"
CV "v178,v100,v101\n"
CV "v177,v98,v99\n"
MF "v[44:47],a[220:223],a[112:115],v[44:47]\n"
CV "v176,v96,v97\n"
"v_fma_f32 v88,v88,s54,v14\n"
MF "v[44:47],a[216:219],a[72:75],v[44:47]\n"
"v_fma_f32 v89,v89,s54,v14\n"
"v_fma_f32 v90,v90,s54,v14\n"
MF "v[52:55],a[216:219],a[76:79],v[52:55]\n"
"v_fma_f32 v91,v91,s54,v14\n"
"v_fma_f32 v92,v92,s54,v14\n"
MF "v[52:55],a[220:223],a[116:119],v[52:55]\n"
"s_waitcnt lgkmcnt(4)\n"
"v_fma_f32 v93,v93,s54,v14\n"
"v_fma_f32 v94,v94,s54,v14\n"
MF "v[20:23],a[200:203],a[140:143],v[20:23]\n"
TR "a[216:217],v0 offset:32768\n"
"v_fma_f32 v95,v95,s54,v14\n"
MF "v[20:23],a[204:207],a[180:183],v[20:23]\n"
TR "a[218:219],v0 offset:33280\n"
CV "v175,v94,v95\n"
MF "v[28:31],a[204:207],a[184:187],v[28:31]\n"
CV "v174,v92,v93\n"
CV "v173,v90,v91\n"
MF "v[28:31],a[200:203],a[144:147],v[28:31]\n"
TR "a[220:221],v0 offset:36864\n"
CV "v172,v88,v89\n"
MF "v[36:39],a[200:203],a[148:151],v[36:39]\n"
TR "a[222:223],v0 offset:37376\n"
"v_fma_f32 v80,v80,s54,v14\n"
MF "v[36:39],a[204:207],a[188:191],v[36:39]\n"
"v_fma_f32 v81,v81,s54,v14\n"
"v_fma_f32 v82,v82,s54,v14\n"
MF "v[44:47],a[204:207],a[192:195],v[44:47]\n"
"v_fma_f32 v83,v83,s54,v14\n"
"v_fma_f32 v84,v84,s54,v14\n"
MF "v[44:47],a[200:203],a[152:155],v[44:47]\n"
"v_fma_f32 v85,v85,s54,v14\n"
"v_fma_f32 v86,v86,s54,v14\n"
MF "v[52:55],a[200:203],a[156:159],v[52:55]\n"
"s_waitcnt lgkmcnt(0)\n"
"v_fma_f32 v87,v87,s54,v14\n"
CV "v171,v86,v87\n"
MF "v[52:55],a[204:207],a[196:199],v[52:55]\n"
"s_barrier\n"
MF "v[16:19],a[208:211],a[60:63],v[16:19]\n"
"s_add_u32 m0,s65,49152\n"
CV "v170,v84,v85\n"
CV "v169,v82,v83\n"
MF "v[16:19],a[212:215],a[100:103],v[16:19]\n"
LD "v8,s[36:39],s73 offen nt lds\n"
MF "v[24:27],a[212:215],a[104:107],v[24:27]\n"
"s_add_u32 m0,s65,50048\n"
CV "v168,v80,v81\n"
"v_fma_f32 v72,v72,s54,v14\n"
MF "v[24:27],a[208:211],a[64:67],v[24:27]\n"
LD "v8,s[36:39],s73 offen offset:128 nt lds\n"
MF "v[32:35],a[208:211],a[68:71],v[32:35]\n"
"v_fma_f32 v73,v73,s54,v14\n"
"v_fma_f32 v74,v74,s54,v14\n"
MF "v[32:35],a[212:215],a[108:111],v[32:35]\n"
"s_add_u32 m0,s65,50944\n"
"v_fma_f32 v75,v75,s54,v14\n"
"v_fma_f32 v76,v76,s54,v14\n"
MF "v[40:43],a[212:215],a[112:115],v[40:43]\n"
LD "v8,s[36:39],s73 offen offset:256 nt lds\n"
MF "v[40:43],a[208:211],a[72:75],v[40:43]\n"
"v_fma_f32 v77,v77,s54,v14\n"
"v_fma_f32 v78,v78,s54,v14\n"
MF "v[48:51],a[208:211],a[76:79],v[48:51]\n"
"s_add_u32 m0,s65,49152\n"
"v_fma_f32 v79,v79,s54,v14\n"
CV "v167,v78,v79\n"
MF "v[48:51],a[212:215],a[116:119],v[48:51]\n"
LD "v8,s[36:39],s73 offen offset:3072 nt lds\n"
MF "v[48:51],a[216:219],a[156:159],v[48:51]\n"
"s_add_u32 m0,s65,50048\n"
CV "v166,v76,v77\n"
CV "v165,v74,v75\n"
MF "v[48:51],a[220:223],a[196:199],v[48:51]\n"
LD "v8,s[36:39],s73 offen offset:3200 nt lds\n"
MF "v[40:43],a[220:223],a[192:195],v[40:43]\n"
CV "v164,v72,v73\n"
"v_fma_f32 v64,v64,s54,v14\n"
MF "v[40:43],a[216:219],a[152:155],v[40:43]\n"
"s_add_u32 m0,s65,50944\n"
"v_fma_f32 v65,v65,s54,v14\n"
"v_fma_f32 v66,v66,s54,v14\n"
MF "v[32:35],a[216:219],a[148:151],v[32:35]\n"
LD "v8,s[36:39],s73 offen offset:3328 nt lds\n"
MF "v[32:35],a[220:223],a[188:191],v[32:35]\n"
"v_fma_f32 v67,v67,s54,v14\n"
"v_fma_f32 v68,v68,s54,v14\n"
MF "v[24:27],a[220:223],a[184:187],v[24:27]\n"
"v_fma_f32 v69,v69,s54,v14\n"
"v_fma_f32 v70,v70,s54,v14\n"
MF "v[24:27],a[216:219],a[144:147],v[24:27]\n"
"v_fma_f32 v71,v71,s54,v14\n"
CV "v163,v70,v71\n"
MF "v[16:19],a[216:219],a[140:143],v[16:19]\n"
CV "v162,v68,v69\n"
CV "v161,v66,v67\n"
MF "v[16:19],a[220:223],a[180:183],v[16:19]\n"
CV "v160,v64,v65\n"
AR "v0,a244\n"
"v_xor_b32 v1,64,v0\n"
"s_waitcnt vmcnt(12) lgkmcnt(0)\n"
"s_barrier\n"
"ds_read_b128 v[92:95],v0\n"
"ds_read_b128 v[96:99],v1\n"
"ds_read_b128 v[100:103],v0 offset:1024\n"
"v_pk_fma_f32 v[56:57],v[56:57],s[54:55],v[14:15] op_sel_hi:[1,1,0]\n"
"v_pk_fma_f32 v[58:59],v[58:59],s[54:55],v[14:15] op_sel_hi:[1,1,0]\n"
"v_pk_fma_f32 v[60:61],v[60:61],s[54:55],v[14:15] op_sel_hi:[1,1,0]\n"
"v_pk_fma_f32 v[62:63],v[62:63],s[54:55],v[14:15] op_sel_hi:[1,1,0]\n"
CV "v159,v62,v63\n"
CV "v158,v60,v61\n"
CV "v157,v58,v59\n"
CV "v156,v56,v57\n"
"v_pk_mul_f32 v[48:49],v[48:49],s[54:55]\n"
"v_pk_mul_f32 v[50:51],v[50:51],s[54:55]\n"
"v_pk_mul_f32 v[52:53],v[52:53],s[54:55]\n"
"v_pk_mul_f32 v[54:55],v[54:55],s[54:55]\n"
CV "v155,v54,v55\n"
CV "v154,v52,v53\n"
CV "v153,v50,v51\n"
CV "v152,v48,v49\n"
"v_pk_mul_f32 v[40:41],v[40:41],s[54:55]\n"
"v_pk_mul_f32 v[42:43],v[42:43],s[54:55]\n"
"v_pk_mul_f32 v[44:45],v[44:45],s[54:55]\n"
"v_pk_mul_f32 v[46:47],v[46:47],s[54:55]\n"
CV "v151,v46,v47\n"
CV "v150,v44,v45\n"
CV "v149,v42,v43\n"
CV "v148,v40,v41\n"
"v_pk_mul_f32 v[32:33],v[32:33],s[54:55]\n"
"v_pk_mul_f32 v[34:35],v[34:35],s[54:55]\n"
"v_pk_mul_f32 v[36:37],v[36:37],s[54:55]\n"
"v_pk_mul_f32 v[38:39],v[38:39],s[54:55]\n"
CV "v147,v38,v39\n"
CV "v146,v36,v37\n"
CV "v145,v34,v35\n"
CV "v144,v32,v33\n"
"v_pk_mul_f32 v[24:25],v[24:25],s[54:55]\n"
"v_pk_mul_f32 v[26:27],v[26:27],s[54:55]\n"
"v_pk_mul_f32 v[28:29],v[28:29],s[54:55]\n"
"v_pk_mul_f32 v[30:31],v[30:31],s[54:55]\n"
CV "v143,v30,v31\n"
CV "v142,v28,v29\n"
CV "v141,v26,v27\n"
CV "v140,v24,v25\n"
"v_pk_mul_f32 v[16:17],v[16:17],s[54:55]\n"
"v_pk_mul_f32 v[18:19],v[18:19],s[54:55]\n"
"v_pk_mul_f32 v[20:21],v[20:21],s[54:55]\n"
"v_pk_mul_f32 v[22:23],v[22:23],s[54:55]\n"
CV "v139,v22,v23\n"
CV "v138,v20,v21\n"
CV "v137,v18,v19\n"
CV "v136,v16,v17\n"
"s_cmp_eq_u32 s57,s69\n"
"s_cbranch_scc1 .LattB\n"
AR "v2,a245\n"
"v_add_u32 v2,49152,v2\n"
"v_xor_b32 v3,32,v2\n"
"v_xor_b32 v4,64,v2\n"
"v_xor_b32 v5,96,v2\n"
"s_mov_b32 s61,7\n"
".Latt2x:\n"
"s_cmp_eq_u32 s57,1\n"
"s_cbranch_scc1 .Lt0kx\n"
".Lt0nx:\n"
"s_waitcnt lgkmcnt(2)\n"
MF "v[72:75],v[92:95],v[136:139],0\n"
"s_add_u32 s73,s73,12288\n"
"ds_read_b128 v[104:107],v1 offset:1024\n"
AR "v10,a224\n"
MF "v[76:79],v[92:95],v[140:143],0\n"
"s_cmp_eq_u32 s73,s19\n"
"s_cselect_b32 s73,s18,s73\n"
"s_add_u32 m0,s65,73728\n"
MF "v[80:83],v[92:95],v[144:147],0\n"
LD "v8,s[36:39],s73 offen nt lds\n"
MF "v[84:87],v[92:95],v[148:151],0\n"
"v_max_f32_dpp v10,|v10|,|v10| quad_perm:[1,0,3,2] row_mask:0xf bank_mask:0xf\n"
AR "v11,a225\n"
MF "v[88:91],v[92:95],v[152:155],0\n"
"s_waitcnt lgkmcnt(2)\n"
"ds_read_b128 v[92:95],v0 offset:2048\n"
AR "v12,a226\n"
MF "v[72:75],v[96:99],v[156:159],v[72:75]\n"
"s_add_u32 m0,s65,74624\n"
"v_max_f32_dpp v10,v10,v10 quad_perm:[2,3,0,1] row_mask:0xf bank_mask:0xf\n"
"v_max_f32_dpp v11,|v11|,|v11| quad_perm:[1,0,3,2] row_mask:0xf bank_mask:0xf\n"
MF "v[76:79],v[96:99],v[160:163],v[76:79]\n"
LD "v8,s[36:39],s73 offen offset:128 nt lds\n"
MF "v[80:83],v[96:99],v[164:167],v[80:83]\n"
"v_max_f32_dpp v10,v10,v10 row_half_mirror row_mask:0xf bank_mask:0xf\n"
"v_max_f32_dpp v11,v11,v11 quad_perm:[2,3,0,1] row_mask:0xf bank_mask:0xf\n"
MF "v[84:87],v[96:99],v[168:171],v[84:87]\n"
"v_readlane_b32 s13,v10,0\n"
"v_readlane_b32 s0,v10,24\n"
MF "v[88:91],v[96:99],v[172:175],v[88:91]\n"
"s_waitcnt lgkmcnt(2)\n"
"ds_read_b128 v[96:99],v1 offset:2048\n"
"v_readlane_b32 s1,v10,32\n"
MF "v[72:75],v[100:103],v[176:179],v[72:75]\n"
"s_add_u32 m0,s65,75520\n"
"v_readlane_b32 s2,v10,56\n"
"v_max_f32_dpp v11,v11,v11 row_half_mirror row_mask:0xf bank_mask:0xf\n"
MF "v[76:79],v[100:103],v[180:183],v[76:79]\n"
LD "v8,s[36:39],s73 offen offset:256 nt lds\n"
MF "v[80:83],v[100:103],v[184:187],v[80:83]\n"
"s_min_u32 s13,s13,s0\n"
"v_readlane_b32 s11,v11,0\n"
"v_readlane_b32 s3,v11,24\n"
MF "v[84:87],v[100:103],v[188:191],v[84:87]\n"
"s_min_u32 s1,s1,s2\n"
"v_readlane_b32 s4,v11,32\n"
"v_readlane_b32 s5,v11,56\n"
MF "v[88:91],v[100:103],v[192:195],v[88:91]\n"
"s_waitcnt lgkmcnt(2)\n"
"ds_read_b128 v[100:103],v0 offset:6144\n"
"v_max_f32_dpp v12,|v12|,|v12| quad_perm:[1,0,3,2] row_mask:0xf bank_mask:0xf\n"
MF "v[72:75],v[104:107],v[196:199],v[72:75]\n"
"s_add_u32 m0,s65,73728\n"
MF "v[76:79],v[104:107],v[200:203],v[76:79]\n"
LD "v8,s[36:39],s73 offen offset:3072 nt lds\n"
MF "v[80:83],v[104:107],v[204:207],v[80:83]\n"
"s_min_u32 s13,s13,s1\n"
"v_max_f32_dpp v12,v12,v12 quad_perm:[2,3,0,1] row_mask:0xf bank_mask:0xf\n"
MF "v[84:87],v[104:107],v[208:211],v[84:87]\n"
"s_and_b32 s13,s13,0x7f800000\n"
MF "v[88:91],v[104:107],v[212:215],v[88:91]\n"
"s_waitcnt lgkmcnt(2)\n"
"ds_read_b128 v[104:107],v1 offset:6144\n"
"v_max_f32_dpp v12,v12,v12 row_half_mirror row_mask:0xf bank_mask:0xf\n"
MF "v[72:75],v[92:95],v[216:219],v[72:75]\n"
"s_add_u32 m0,s65,74624\n"
"v_readlane_b32 s10,v12,0\n"
"v_readlane_b32 s6,v12,24\n"
MF "v[76:79],v[92:95],v[220:223],v[76:79]\n"
LD "v8,s[36:39],s73 offen offset:3200 nt lds\n"
MF "v[80:83],v[92:95],v[224:227],v[80:83]\n"
"s_sub_u32 s13,s13,0x14000000\n"
"v_readlane_b32 s7,v12,32\n"
"v_readlane_b32 s8,v12,56\n"
MF "v[84:87],v[92:95],v[228:231],v[84:87]\n"
"s_cmp_lt_u32 s13,0x50000000\n"
"s_cselect_b32 s13,s13,0xed400000\n"
MF "v[88:91],v[92:95],v[232:235],v[88:91]\n"
"s_waitcnt lgkmcnt(2)\n"
"ds_read_b128 v[92:95],v0 offset:7168\n"
MF "v[72:75],v[96:99],v[236:239],v[72:75]\n"
"s_add_u32 m0,s65,75520\n"
MF "v[76:79],v[96:99],v[240:243],v[76:79]\n"
LD "v8,s[36:39],s73 offen offset:3328 nt lds\n"
MF "v[80:83],v[96:99],v[244:247],v[80:83]\n"
"s_add_u32 s13,s13,0x12c00000\n"
MF "v[84:87],v[96:99],v[248:251],v[84:87]\n"
"s_min_u32 s11,s11,s3\n"
MF "v[88:91],v[96:99],v[252:255],v[88:91]\n"
"s_waitcnt lgkmcnt(2)\n"
"ds_read_b128 v[96:99],v1 offset:7168\n"
MF "v[112:115],v[100:103],v[136:139],0\n"
"s_min_u32 s4,s4,s5\n"
MF "v[116:119],v[100:103],v[140:143],0\n"
"s_min_u32 s11,s11,s4\n"
MF "v[120:123],v[100:103],v[144:147],0\n"
"s_and_b32 s11,s11,0x7f800000\n"
MF "v[124:127],v[100:103],v[148:151],0\n"
"s_sub_u32 s11,s11,0x14000000\n"
MF "v[128:131],v[100:103],v[152:155],0\n"
"s_waitcnt lgkmcnt(2)\n"
"ds_read_b128 v[100:103],v0 offset:8192\n"
MF "v[112:115],v[104:107],v[156:159],v[112:115]\n"
"s_min_u32 s10,s10,s6\n"
"v_max3_f32 v52,v72,v73,v74\n"
"v_max_f32 v52,v52,v75\n"
MF "v[116:119],v[104:107],v[160:163],v[116:119]\n"
"s_min_u32 s7,s7,s8\n"
"v_max3_f32 v53,v76,v77,v78\n"
"v_max_f32 v53,v53,v79\n"
MF "v[120:123],v[104:107],v[164:167],v[120:123]\n"
"s_min_u32 s10,s10,s7\n"
"v_max3_f32 v54,v80,v81,v82\n"
"v_max_f32 v54,v54,v83\n"
MF "v[124:127],v[104:107],v[168:171],v[124:127]\n"
".Lpg9b:\n"
"s_cbranch_execz .Lpgpsl_b\n"
"v_max3_f32 v55,v84,v85,v86\n"
"v_max_f32 v55,v55,v87\n"
MF "v[128:131],v[104:107],v[172:175],v[128:131]\n"
"s_waitcnt lgkmcnt(2)\n"
"ds_read_b128 v[104:107],v1 offset:8192\n"
"v_max3_f32 v56,v88,v89,v90\n"
MF "v[112:115],v[92:95],v[176:179],v[112:115]\n"
"s_cmp_lt_u32 s11,0x50000000\n"
"s_cselect_b32 s11,s11,0xed400000\n"
"v_max_f32 v56,v56,v91\n"
MF "v[116:119],v[92:95],v[180:183],v[116:119]\n"
"s_add_u32 s11,s11,0x12c00000\n"
MF "v[120:123],v[92:95],v[184:187],v[120:123]\n"
MF "v[124:127],v[92:95],v[188:191],v[124:127]\n"
MF "v[128:131],v[92:95],v[192:195],v[128:131]\n"
"s_waitcnt lgkmcnt(2)\n"
MF "v[112:115],v[96:99],v[196:199],v[112:115]\n"
"s_waitcnt lgkmcnt(0)\n"
MF "v[116:119],v[96:99],v[200:203],v[116:119]\n"
"s_barrier\n"
MF "v[120:123],v[96:99],v[204:207],v[120:123]\n"
"s_add_u32 s72,s72,6144\n"
MF "v[124:127],v[96:99],v[208:211],v[124:127]\n"
"s_cmp_eq_u32 s72,s17\n"
"s_cselect_b32 s72,s16,s72\n"
"s_add_u32 m0,s64,0\n"
MF "v[128:131],v[96:99],v[212:215],v[128:131]\n"
LD "v7,s[32:35],s72 offen nt lds\n"
MF "v[112:115],v[100:103],v[216:219],v[112:115]\n"
"s_add_u32 m0,s64,896\n"
MF "v[116:119],v[100:103],v[220:223],v[116:119]\n"
LD "v7,s[32:35],s72 offen offset:128 nt lds\n"
MF "v[120:123],v[100:103],v[224:227],v[120:123]\n"
"s_add_u32 m0,s64,1792\n"
MF "v[124:127],v[100:103],v[228:231],v[124:127]\n"
LD "v7,s[32:35],s72 offen offset:256 nt lds\n"
MF "v[128:131],v[100:103],v[232:235],v[128:131]\n"
"s_add_u32 s72,s72,6144\n"
"s_add_u32 m0,s64,6144\n"
MF "v[112:115],v[104:107],v[236:239],v[112:115]\n"
LD "v7,s[32:35],s72 offen nt lds\n"
MF "v[116:119],v[104:107],v[240:243],v[116:119]\n"
"s_add_u32 m0,s64,7040\n"
MF "v[120:123],v[104:107],v[244:247],v[120:123]\n"
LD "v7,s[32:35],s72 offen offset:128 nt lds\n"
MF "v[124:127],v[104:107],v[248:251],v[124:127]\n"
"s_add_u32 m0,s64,7936\n"
MF "v[128:131],v[104:107],v[252:255],v[128:131]\n"
LD "v7,s[32:35],s72 offen offset:256 nt lds\n"
"s_branch .Lt0ex\n"
".Lpgt0kx_d:\n"
"s_cbranch_execz .Lisr2\n"
".Lt0kx:\n"
"s_lshl_b32 s58,s56,10\n"
"s_add_u32 s58,s58,73728\n"
"v_add_u32 v32,s58,v9\n"
"v_add_u32 v33,65536,v32\n"
"s_waitcnt lgkmcnt(2)\n"
MF "v[72:75],v[92:95],v[136:139],0\n"
"s_add_u32 s73,s73,12288\n"
"ds_read_b128 v[104:107],v1 offset:1024\n"
AR "v10,a224\n"
MF "v[76:79],v[92:95],v[140:143],0\n"
"s_cmp_eq_u32 s73,s19\n"
"s_cselect_b32 s73,s18,s73\n"
"s_add_u32 m0,s65,73728\n"
MF "v[80:83],v[92:95],v[144:147],0\n"
LD "v8,s[36:39],s73 offen nt lds\n"
MF "v[84:87],v[92:95],v[148:151],0\n"
"v_max_f32_dpp v10,|v10|,|v10| quad_perm:[1,0,3,2] row_mask:0xf bank_mask:0xf\n"
AR "v11,a225\n"
MF "v[88:91],v[92:95],v[152:155],0\n"
"s_waitcnt lgkmcnt(2)\n"
"ds_read_b128 v[92:95],v0 offset:2048\n"
AR "v12,a226\n"
MF "v[72:75],v[96:99],v[156:159],v[72:75]\n"
"s_add_u32 m0,s65,74624\n"
"v_max_f32_dpp v10,v10,v10 quad_perm:[2,3,0,1] row_mask:0xf bank_mask:0xf\n"
"v_max_f32_dpp v11,|v11|,|v11| quad_perm:[1,0,3,2] row_mask:0xf bank_mask:0xf\n"
MF "v[76:79],v[96:99],v[160:163],v[76:79]\n"
LD "v8,s[36:39],s73 offen offset:128 nt lds\n"
MF "v[80:83],v[96:99],v[164:167],v[80:83]\n"
"v_max_f32_dpp v10,v10,v10 row_half_mirror row_mask:0xf bank_mask:0xf\n"
"v_max_f32_dpp v11,v11,v11 quad_perm:[2,3,0,1] row_mask:0xf bank_mask:0xf\n"
MF "v[84:87],v[96:99],v[168:171],v[84:87]\n"
"v_readlane_b32 s13,v10,0\n"
"v_readlane_b32 s0,v10,24\n"
MF "v[88:91],v[96:99],v[172:175],v[88:91]\n"
"s_waitcnt lgkmcnt(2)\n"
"ds_read_b128 v[96:99],v1 offset:2048\n"
"v_readlane_b32 s1,v10,32\n"
MF "v[72:75],v[100:103],v[176:179],v[72:75]\n"
"s_add_u32 m0,s65,75520\n"
"v_readlane_b32 s2,v10,56\n"
"v_max_f32_dpp v11,v11,v11 row_half_mirror row_mask:0xf bank_mask:0xf\n"
MF "v[76:79],v[100:103],v[180:183],v[76:79]\n"
LD "v8,s[36:39],s73 offen offset:256 nt lds\n"
MF "v[80:83],v[100:103],v[184:187],v[80:83]\n"
"s_min_u32 s13,s13,s0\n"
"v_readlane_b32 s11,v11,0\n"
"v_readlane_b32 s3,v11,24\n"
MF "v[84:87],v[100:103],v[188:191],v[84:87]\n"
"s_min_u32 s1,s1,s2\n"
"v_readlane_b32 s4,v11,32\n"
"v_readlane_b32 s5,v11,56\n"
MF "v[88:91],v[100:103],v[192:195],v[88:91]\n"
"s_waitcnt lgkmcnt(2)\n"
"ds_read_b128 v[100:103],v0 offset:6144\n"
"v_max_f32_dpp v12,|v12|,|v12| quad_perm:[1,0,3,2] row_mask:0xf bank_mask:0xf\n"
MF "v[72:75],v[104:107],v[196:199],v[72:75]\n"
"s_add_u32 m0,s65,73728\n"
"ds_write_b128 v32,a[0:3] offset:24576\n"
MF "v[76:79],v[104:107],v[200:203],v[76:79]\n"
LD "v8,s[36:39],s73 offen offset:3072 nt lds\n"
MF "v[80:83],v[104:107],v[204:207],v[80:83]\n"
"s_min_u32 s13,s13,s1\n"
"v_max_f32_dpp v12,v12,v12 quad_perm:[2,3,0,1] row_mask:0xf bank_mask:0xf\n"
AR "v62,a20\n"
MF "v[84:87],v[104:107],v[208:211],v[84:87]\n"
"s_and_b32 s13,s13,0x7f800000\n"
"ds_write_b128 v32,a[4:7] offset:28672\n"
MF "v[88:91],v[104:107],v[212:215],v[88:91]\n"
"s_waitcnt lgkmcnt(4)\n"
"ds_read_b128 v[104:107],v1 offset:6144\n"
"v_max_f32_dpp v12,v12,v12 row_half_mirror row_mask:0xf bank_mask:0xf\n"
MF "v[72:75],v[92:95],v[216:219],v[72:75]\n"
"s_add_u32 m0,s65,74624\n"
"v_readlane_b32 s10,v12,0\n"
"v_readlane_b32 s6,v12,24\n"
MF "v[76:79],v[92:95],v[220:223],v[76:79]\n"
LD "v8,s[36:39],s73 offen offset:3200 nt lds\n"
MF "v[80:83],v[92:95],v[224:227],v[80:83]\n"
"s_sub_u32 s13,s13,0x14000000\n"
"v_readlane_b32 s7,v12,32\n"
"v_readlane_b32 s8,v12,56\n"
MF "v[84:87],v[92:95],v[228:231],v[84:87]\n"
"s_cmp_lt_u32 s13,0x50000000\n"
"s_cselect_b32 s13,s13,0xed400000\n"
AR "v63,a21\n"
MF "v[88:91],v[92:95],v[232:235],v[88:91]\n"
"s_waitcnt lgkmcnt(4)\n"
"ds_read_b128 v[92:95],v0 offset:7168\n"
AR "v64,a22\n"
MF "v[72:75],v[96:99],v[236:239],v[72:75]\n"
"s_add_u32 m0,s65,75520\n"
"ds_write_b128 v32,a[8:11] offset:32768\n"
MF "v[76:79],v[96:99],v[240:243],v[76:79]\n"
LD "v8,s[36:39],s73 offen offset:3328 nt lds\n"
MF "v[80:83],v[96:99],v[244:247],v[80:83]\n"
"s_add_u32 s13,s13,0x12c00000\n"
"ds_write_b128 v32,a[12:15] offset:36864\n"
MF "v[84:87],v[96:99],v[248:251],v[84:87]\n"
"s_min_u32 s11,s11,s3\n"
"ds_write_b128 v32,a[16:19] offset:40960\n"
MF "v[88:91],v[96:99],v[252:255],v[88:91]\n"
"s_waitcnt lgkmcnt(7)\n"
"ds_read_b128 v[96:99],v1 offset:7168\n"
AR "v65,a23\n"
MF "v[112:115],v[100:103],v[136:139],0\n"
"s_min_u32 s4,s4,s5\n"
"ds_write_b128 v32,a[40:43] offset:45056\n"
MF "v[116:119],v[100:103],v[140:143],0\n"
"s_min_u32 s11,s11,s4\n"
"ds_write_b128 v32,a[44:47] offset:49152\n"
MF "v[120:123],v[100:103],v[144:147],0\n"
"s_and_b32 s11,s11,0x7f800000\n"
"ds_write_b128 v32,a[48:51] offset:53248\n"
MF "v[124:127],v[100:103],v[148:151],0\n"
"s_sub_u32 s11,s11,0x14000000\n"
"ds_write_b128 v32,a[52:55] offset:57344\n"
MF "v[128:131],v[100:103],v[152:155],0\n"
"s_waitcnt lgkmcnt(9)\n"
"ds_read_b128 v[100:103],v0 offset:8192\n"
AR "v66,a24\n"
MF "v[112:115],v[104:107],v[156:159],v[112:115]\n"
"s_min_u32 s10,s10,s6\n"
"v_max3_f32 v52,v72,v73,v74\n"
"v_max_f32 v52,v52,v75\n"
MF "v[116:119],v[104:107],v[160:163],v[116:119]\n"
"s_min_u32 s7,s7,s8\n"
"v_max3_f32 v53,v76,v77,v78\n"
"v_max_f32 v53,v53,v79\n"
MF "v[120:123],v[104:107],v[164:167],v[120:123]\n"
"s_min_u32 s10,s10,s7\n"
"v_max3_f32 v54,v80,v81,v82\n"
"v_max_f32 v54,v54,v83\n"
MF "v[124:127],v[104:107],v[168:171],v[124:127]\n"
"v_max3_f32 v55,v84,v85,v86\n"
"v_max_f32 v55,v55,v87\n"
MF "v[128:131],v[104:107],v[172:175],v[128:131]\n"
"s_waitcnt lgkmcnt(9)\n"
"ds_read_b128 v[104:107],v1 offset:8192\n"
"v_max3_f32 v56,v88,v89,v90\n"
MF "v[112:115],v[92:95],v[176:179],v[112:115]\n"
"s_cmp_lt_u32 s11,0x50000000\n"
"s_cselect_b32 s11,s11,0xed400000\n"
"v_max_f32 v56,v56,v91\n"
MF "v[116:119],v[92:95],v[180:183],v[116:119]\n"
"s_add_u32 s11,s11,0x12c00000\n"
"ds_write_b128 v32,a[56:59] offset:61440\n"
MF "v[120:123],v[92:95],v[184:187],v[120:123]\n"
"ds_write_b128 v33,a[80:83]\n"
MF "v[124:127],v[92:95],v[188:191],v[124:127]\n"
"ds_write_b128 v33,a[84:87] offset:4096\n"
MF "v[128:131],v[92:95],v[192:195],v[128:131]\n"
"s_waitcnt lgkmcnt(9)\n"
"ds_write_b128 v33,a[88:91] offset:8192\n"
MF "v[112:115],v[96:99],v[196:199],v[112:115]\n"
"s_waitcnt lgkmcnt(0)\n"
AR "v67,a25\n"
AR "v68,a26\n"
MF "v[116:119],v[96:99],v[200:203],v[116:119]\n"
"s_barrier\n"
MF "v[120:123],v[96:99],v[204:207],v[120:123]\n"
"s_add_u32 s72,s72,6144\n"
"ds_write_b128 v33,a[92:95] offset:12288\n"
MF "v[124:127],v[96:99],v[208:211],v[124:127]\n"
"s_cmp_eq_u32 s72,s17\n"
"s_cselect_b32 s72,s16,s72\n"
"s_add_u32 m0,s64,0\n"
MF "v[128:131],v[96:99],v[212:215],v[128:131]\n"
LD "v7,s[32:35],s72 offen nt lds\n"
MF "v[112:115],v[100:103],v[216:219],v[112:115]\n"
"s_add_u32 m0,s64,896\n"
"ds_write_b128 v33,a[96:99] offset:16384\n"
MF "v[116:119],v[100:103],v[220:223],v[116:119]\n"
LD "v7,s[32:35],s72 offen offset:128 nt lds\n"
MF "v[120:123],v[100:103],v[224:227],v[120:123]\n"
"s_add_u32 m0,s64,1792\n"
AR "v69,a27\n"
AR "v70,a28\n"
MF "v[124:127],v[100:103],v[228:231],v[124:127]\n"
LD "v7,s[32:35],s72 offen offset:256 nt lds\n"
MF "v[128:131],v[100:103],v[232:235],v[128:131]\n"
"s_add_u32 s72,s72,6144\n"
"s_add_u32 m0,s64,6144\n"
AR "v71,a29\n"
MF "v[112:115],v[104:107],v[236:239],v[112:115]\n"
LD "v7,s[32:35],s72 offen nt lds\n"
MF "v[116:119],v[104:107],v[240:243],v[116:119]\n"
"s_add_u32 m0,s64,7040\n"
"ds_write_b128 v33,a[120:123] offset:20480\n"
MF "v[120:123],v[104:107],v[244:247],v[120:123]\n"
LD "v7,s[32:35],s72 offen offset:128 nt lds\n"
MF "v[124:127],v[104:107],v[248:251],v[124:127]\n"
"s_add_u32 m0,s64,7936\n"
AR "v132,a30\n"
AR "v133,a31\n"
MF "v[128:131],v[104:107],v[252:255],v[128:131]\n"
LD "v7,s[32:35],s72 offen offset:256 nt lds\n"
AR "v134,a32\n"
AR "v135,a33\n"
AR "v6,a34\n"
AR "v13,a35\n"
AR "v14,a36\n"
AR "v15,a37\n"
"v_accvgpr_mov_b32 a254,a38\n"
"v_accvgpr_mov_b32 a255,a39\n"
"v_accvgpr_mov_b32 a240,a168\n"
"v_accvgpr_mov_b32 a241,a169\n"
"v_accvgpr_mov_b32 a242,a170\n"
"v_accvgpr_mov_b32 a243,a171\n"
"v_accvgpr_mov_b32 a244,a172\n"
"v_accvgpr_mov_b32 a245,a173\n"
"v_accvgpr_mov_b32 a246,a174\n"
"v_accvgpr_mov_b32 a247,a175\n"
"v_accvgpr_mov_b32 a248,a176\n"
"v_accvgpr_mov_b32 a249,a177\n"
"v_accvgpr_mov_b32 a250,a178\n"
"v_accvgpr_mov_b32 a251,a179\n"
".Lpgt0ex_b:\n"
"s_cbranch_execz .Lpg2a\n"
".Lt0ex:\n"
"s_nop 1\n"
"v_max3_f32 v52,v52,v112,v113\n"
"v_max3_f32 v53,v53,v116,v117\n"
"v_max3_f32 v54,v54,v120,v121\n"
"v_max3_f32 v55,v55,v124,v125\n"
"v_max3_f32 v56,v56,v128,v129\n"
"v_max3_f32 v52,v52,v114,v115\n"
"v_max3_f32 v53,v53,v118,v119\n"
"v_max3_f32 v54,v54,v122,v123\n"
"v_max3_f32 v55,v55,v126,v127\n"
"v_max3_f32 v56,v56,v130,v131\n"
"v_mov_b32 v57,v52\n"
"v_mov_b32 v58,v53\n"
"v_mov_b32 v59,v54\n"
"v_mov_b32 v60,v55\n"
"v_mov_b32 v61,v56\n"
"v_permlane16_swap_b32 v52,v57\n"
"v_permlane16_swap_b32 v53,v58\n"
"v_permlane16_swap_b32 v54,v59\n"
"v_permlane16_swap_b32 v55,v60\n"
"v_permlane16_swap_b32 v56,v61\n"
"v_max_f32 v52,v52,v57\n"
"v_max_f32 v53,v53,v58\n"
"v_max_f32 v54,v54,v59\n"
"v_max_f32 v55,v55,v60\n"
"v_max_f32 v56,v56,v61\n"
"v_mov_b32 v57,v52\n"
"v_mov_b32 v58,v53\n"
"v_mov_b32 v59,v54\n"
"v_mov_b32 v60,v55\n"
"v_mov_b32 v61,v56\n"
"v_permlane32_swap_b32 v52,v57\n"
"v_permlane32_swap_b32 v53,v58\n"
"v_permlane32_swap_b32 v54,v59\n"
"v_permlane32_swap_b32 v55,v60\n"
"v_permlane32_swap_b32 v56,v61\n"
"v_max_f32 v52,v52,v57\n"
"v_max_f32 v53,v53,v58\n"
"v_max_f32 v54,v54,v59\n"
"v_max_f32 v55,v55,v60\n"
"v_max_f32 v56,v56,v61\n"
"v_sub_f32 v32,0xc1300000,v52\n"
"v_sub_f32 v36,0xc1300000,v53\n"
"v_sub_f32 v40,0xc1300000,v54\n"
"v_sub_f32 v44,0xc1300000,v55\n"
"v_sub_f32 v48,0xc1300000,v56\n"
"v_mov_b32_e64 v33,v32\n"
"v_pk_mov_b32 v[34:35],v[32:33],v[32:33]\n"
"v_mov_b32 v37,v36\n"
"v_pk_mov_b32 v[38:39],v[36:37],v[36:37]\n"
"v_mov_b32 v41,v40\n"
"v_pk_mov_b32 v[42:43],v[40:41],v[40:41]\n"
"v_mov_b32 v45,v44\n"
"v_pk_mov_b32 v[46:47],v[44:45],v[44:45]\n"
"v_mov_b32 v49,v48\n"
"v_pk_mov_b32 v[50:51],v[48:49],v[48:49]\n"
"v_pk_add_f32 v[72:73],v[72:73],v[32:33]\n"
"v_pk_add_f32 v[74:75],v[74:75],v[32:33]\n"
"v_pk_add_f32 v[112:113],v[112:113],v[32:33]\n"
"v_pk_add_f32 v[114:115],v[114:115],v[32:33]\n"
"v_exp_f32 v72,v72\n"
"v_exp_f32 v73,v73\n"
"v_exp_f32 v74,v74\n"
"v_exp_f32 v75,v75\n"
"v_exp_f32 v112,v112\n"
"v_exp_f32 v113,v113\n"
"v_exp_f32 v114,v114\n"
"v_exp_f32 v115,v115\n"
"v_pk_add_f32 v[10:11],v[72:73],v[74:75]\n"
"v_pk_add_f32 v[28:29],v[112:113],v[114:115]\n"
"v_pk_add_f32 v[10:11],v[10:11],v[28:29]\n"
"v_add_f32_e64 v52,v10,v11\n"
CV "v72,v72,v73\n"
CV "v73,v74,v75\n"
CV "v74,v112,v113\n"
CV "v75,v114,v115\n"
"v_pk_add_f32 v[76:77],v[76:77],v[36:37]\n"
"v_pk_add_f32 v[78:79],v[78:79],v[36:37]\n"
"v_pk_add_f32 v[116:117],v[116:117],v[36:37]\n"
"v_pk_add_f32 v[118:119],v[118:119],v[36:37]\n"
"v_exp_f32 v76,v76\n"
"v_exp_f32 v77,v77\n"
"v_exp_f32 v78,v78\n"
"v_exp_f32 v79,v79\n"
"v_exp_f32 v116,v116\n"
"v_exp_f32 v117,v117\n"
"v_exp_f32 v118,v118\n"
"v_exp_f32 v119,v119\n"
"v_pk_add_f32 v[10:11],v[76:77],v[78:79]\n"
"v_pk_add_f32 v[28:29],v[116:117],v[118:119]\n"
"v_pk_add_f32 v[10:11],v[10:11],v[28:29]\n"
"v_add_f32_e64 v53,v10,v11\n"
CV "v76,v76,v77\n"
CV "v77,v78,v79\n"
CV "v78,v116,v117\n"
CV "v79,v118,v119\n"
"v_pk_add_f32 v[80:81],v[80:81],v[40:41]\n"
"v_pk_add_f32 v[82:83],v[82:83],v[40:41]\n"
"v_pk_add_f32 v[120:121],v[120:121],v[40:41]\n"
"v_pk_add_f32 v[122:123],v[122:123],v[40:41]\n"
"v_exp_f32 v80,v80\n"
"v_exp_f32 v81,v81\n"
"v_exp_f32 v82,v82\n"
"v_exp_f32 v83,v83\n"
"v_exp_f32 v120,v120\n"
"v_exp_f32 v121,v121\n"
"v_exp_f32 v122,v122\n"
"v_exp_f32 v123,v123\n"
"v_pk_add_f32 v[10:11],v[80:81],v[82:83]\n"
"v_pk_add_f32 v[28:29],v[120:121],v[122:123]\n"
"v_pk_add_f32 v[10:11],v[10:11],v[28:29]\n"
"v_add_f32_e64 v54,v10,v11\n"
CV "v80,v80,v81\n"
CV "v81,v82,v83\n"
CV "v82,v120,v121\n"
CV "v83,v122,v123\n"
"v_pk_add_f32 v[84:85],v[84:85],v[44:45]\n"
"v_pk_add_f32 v[86:87],v[86:87],v[44:45]\n"
"v_pk_add_f32 v[124:125],v[124:125],v[44:45]\n"
"v_pk_add_f32 v[126:127],v[126:127],v[44:45]\n"
"v_mov_b32_e64 v55,0\n"
"v_pk_add_f32 v[88:89],v[88:89],v[48:49]\n"
"v_pk_add_f32 v[90:91],v[90:91],v[48:49]\n"
"v_pk_add_f32 v[128:129],v[128:129],v[48:49]\n"
"v_pk_add_f32 v[130:131],v[130:131],v[48:49]\n"
"v_mov_b32 v56,0\n"
"s_waitcnt vmcnt(18) lgkmcnt(0)\n"
"s_barrier\n"
"ds_read_b128 v[16:19],v0 offset:24576\n"
"ds_read_b128 v[20:23],v1 offset:24576\n"
AW "a220,s13\n"
AW "a221,s13\n"
AW "a222,s13\n"
AW "a223,s13\n"
"s_waitcnt lgkmcnt(1)\n"
MF "v[92:95],v[16:19],v[136:139],v[32:35]\n"
"s_nop 0\n"
"ds_read_b128 v[24:27],v0 offset:25600\n"
"ds_read_b128 v[28:31],v1 offset:25600\n"
"s_waitcnt lgkmcnt(2)\n"
MF "v[92:95],v[20:23],v[156:159],v[92:95]\n"
"v_exp_f32 v84,v84\n"
MF "v[96:99],v[20:23],v[160:163],v[36:39]\n"
"v_exp_f32 v85,v85\n"
MF "v[96:99],v[16:19],v[140:143],v[96:99]\n"
"v_exp_f32 v86,v86\n"
MF "v[100:103],v[16:19],v[144:147],v[40:43]\n"
"v_exp_f32 v87,v87\n"
MF "v[100:103],v[20:23],v[164:167],v[100:103]\n"
"v_add_f32 v55,v55,v84\n"
"v_add_f32 v55,v55,v85\n"
MF "v[104:107],v[20:23],v[168:171],v[44:47]\n"
CV "v84,v84,v85\n"
"v_add_f32 v55,v55,v86\n"
MF "v[104:107],v[16:19],v[148:151],v[104:107]\n"
"v_add_f32 v55,v55,v87\n"
CV "v85,v86,v87\n"
MF "v[108:111],v[16:19],v[152:155],v[48:51]\n"
"v_exp_f32 v124,v124\n"
MF "v[108:111],v[20:23],v[172:175],v[108:111]\n"
"s_waitcnt lgkmcnt(0)\n"
"ds_read_b128 v[16:19],v0 offset:26624\n"
"ds_read_b128 v[20:23],v1 offset:26624\n"
MF "v[108:111],v[24:27],v[192:195],v[108:111]\n"
"v_exp_f32 v125,v125\n"
MF "v[108:111],v[28:31],v[212:215],v[108:111]\n"
"v_exp_f32 v126,v126\n"
MF "v[104:107],v[28:31],v[208:211],v[104:107]\n"
"v_exp_f32 v127,v127\n"
MF "v[104:107],v[24:27],v[188:191],v[104:107]\n"
"v_add_f32 v55,v55,v124\n"
"v_add_f32 v55,v55,v125\n"
MF "v[100:103],v[24:27],v[184:187],v[100:103]\n"
CV "v86,v124,v125\n"
"v_add_f32 v55,v55,v126\n"
MF "v[100:103],v[28:31],v[204:207],v[100:103]\n"
"v_add_f32 v55,v55,v127\n"
CV "v87,v126,v127\n"
MF "v[96:99],v[28:31],v[200:203],v[96:99]\n"
"v_exp_f32 v88,v88\n"
MF "v[96:99],v[24:27],v[180:183],v[96:99]\n"
"v_exp_f32 v89,v89\n"
MF "v[92:95],v[24:27],v[176:179],v[92:95]\n"
"v_exp_f32 v90,v90\n"
MF "v[92:95],v[28:31],v[196:199],v[92:95]\n"
"s_waitcnt lgkmcnt(0)\n"
"ds_read_b128 v[24:27],v0 offset:30720\n"
"ds_read_b128 v[28:31],v1 offset:30720\n"
MF "v[92:95],v[16:19],v[216:219],v[92:95]\n"
"v_exp_f32 v91,v91\n"
MF "v[92:95],v[20:23],v[236:239],v[92:95]\n"
"v_add_f32 v56,v56,v88\n"
"v_add_f32 v56,v56,v89\n"
MF "v[96:99],v[20:23],v[240:243],v[96:99]\n"
CV "v88,v88,v89\n"
"v_add_f32 v56,v56,v90\n"
MF "v[96:99],v[16:19],v[220:223],v[96:99]\n"
"v_add_f32 v56,v56,v91\n"
CV "v89,v90,v91\n"
MF "v[100:103],v[16:19],v[224:227],v[100:103]\n"
"v_exp_f32 v128,v128\n"
MF "v[100:103],v[20:23],v[244:247],v[100:103]\n"
"v_exp_f32 v129,v129\n"
MF "v[104:107],v[20:23],v[248:251],v[104:107]\n"
"v_exp_f32 v130,v130\n"
MF "v[104:107],v[16:19],v[228:231],v[104:107]\n"
"v_exp_f32 v131,v131\n"
MF "v[108:111],v[16:19],v[232:235],v[108:111]\n"
"v_add_f32 v56,v56,v128\n"
"v_add_f32 v56,v56,v129\n"
MF "v[108:111],v[20:23],v[252:255],v[108:111]\n"
"s_waitcnt lgkmcnt(0)\n"
"ds_read_b128 v[16:19],v0 offset:31744\n"
"ds_read_b128 v[20:23],v1 offset:31744\n"
MF "v[112:115],v[24:27],v[136:139],v[32:35]\n"
CV "v90,v128,v129\n"
"v_add_f32 v56,v56,v130\n"
MF "v[112:115],v[28:31],v[156:159],v[112:115]\n"
"v_add_f32 v56,v56,v131\n"
CV "v91,v130,v131\n"
MF "v[116:119],v[28:31],v[160:163],v[36:39]\n"
"v_exp_f32 v92,v92\n"
MF "v[116:119],v[24:27],v[140:143],v[116:119]\n"
"v_exp_f32 v93,v93\n"
MF "v[120:123],v[24:27],v[144:147],v[40:43]\n"
"v_exp_f32 v94,v94\n"
MF "v[120:123],v[28:31],v[164:167],v[120:123]\n"
"v_exp_f32 v95,v95\n"
MF "v[124:127],v[28:31],v[168:171],v[44:47]\n"
"v_add_f32 v57,v52,v92\n"
"v_add_f32 v57,v57,v93\n"
MF "v[124:127],v[24:27],v[148:151],v[124:127]\n"
CV "v92,v92,v93\n"
"v_add_f32 v57,v57,v94\n"
MF "v[128:131],v[24:27],v[152:155],v[48:51]\n"
"v_add_f32 v57,v57,v95\n"
CV "v93,v94,v95\n"
MF "v[128:131],v[28:31],v[172:175],v[128:131]\n"
"s_waitcnt lgkmcnt(0)\n"
"ds_read_b128 v[24:27],v0 offset:32768\n"
"ds_read_b128 v[28:31],v1 offset:32768\n"
MF "v[128:131],v[16:19],v[192:195],v[128:131]\n"
"v_exp_f32 v96,v96\n"
MF "v[128:131],v[20:23],v[212:215],v[128:131]\n"
"v_exp_f32 v97,v97\n"
MF "v[124:127],v[20:23],v[208:211],v[124:127]\n"
"v_exp_f32 v98,v98\n"
MF "v[124:127],v[16:19],v[188:191],v[124:127]\n"
"v_exp_f32 v99,v99\n"
MF "v[120:123],v[16:19],v[184:187],v[120:123]\n"
"v_add_f32 v58,v53,v96\n"
"v_add_f32 v58,v58,v97\n"
MF "v[120:123],v[20:23],v[204:207],v[120:123]\n"
CV "v96,v96,v97\n"
"v_add_f32 v58,v58,v98\n"
MF "v[116:119],v[20:23],v[200:203],v[116:119]\n"
"s_waitcnt vmcnt(12) lgkmcnt(0)\n"
"v_add_f32 v58,v58,v99\n"
CV "v97,v98,v99\n"
MF "v[116:119],v[16:19],v[180:183],v[116:119]\n"
"s_barrier\n"
MF "v[112:115],v[16:19],v[176:179],v[112:115]\n"
"v_exp_f32 v100,v100\n"
MF "v[112:115],v[20:23],v[196:199],v[112:115]\n"
"s_nop 0\n"
TR "v[16:17],v2\n"
TR "v[18:19],v2 offset:6144\n"
MF "v[112:115],v[24:27],v[216:219],v[112:115]\n"
"s_add_u32 s72,s72,6144\n"
TR "v[20:21],v3\n"
TR "v[22:23],v3 offset:6144\n"
MF "v[112:115],v[28:31],v[236:239],v[112:115]\n"
"s_cmp_eq_u32 s72,s17\n"
"s_cselect_b32 s72,s16,s72\n"
"s_add_u32 m0,s64,24576\n"
MF "v[116:119],v[28:31],v[240:243],v[116:119]\n"
LD "v7,s[32:35],s72 offen nt lds\n"
MF "v[116:119],v[24:27],v[220:223],v[116:119]\n"
"s_add_u32 m0,s64,25472\n"
"v_exp_f32 v101,v101\n"
MF "v[120:123],v[24:27],v[224:227],v[120:123]\n"
LD "v7,s[32:35],s72 offen offset:128 nt lds\n"
MF "v[120:123],v[28:31],v[244:247],v[120:123]\n"
"v_exp_f32 v102,v102\n"
MF "v[124:127],v[28:31],v[248:251],v[124:127]\n"
"s_add_u32 m0,s64,26368\n"
"v_exp_f32 v103,v103\n"
MF "v[124:127],v[24:27],v[228:231],v[124:127]\n"
LD "v7,s[32:35],s72 offen offset:256 nt lds\n"
MF "v[128:131],v[24:27],v[232:235],v[128:131]\n"
"s_add_u32 s72,s72,6144\n"
"v_add_f32 v59,v54,v100\n"
"v_add_f32 v59,v59,v101\n"
MF "v[128:131],v[28:31],v[252:255],v[128:131]\n"
"s_waitcnt lgkmcnt(0)\n"
TR "v[24:25],v4\n"
TR "v[26:27],v4 offset:6144\n"
MF "a[0:3],v[16:19],v[72:75],a[220:223]\n"
"s_add_u32 m0,s64,30720\n"
TR "v[28:29],v5\n"
TR "v[30:31],v5 offset:6144\n"
MF "a[20:23],v[20:23],v[72:75],a[220:223]\n"
LD "v7,s[32:35],s72 offen nt lds\n"
MF "a[24:27],v[20:23],v[76:79],a[220:223]\n"
CV "v100,v100,v101\n"
"v_add_f32 v59,v59,v102\n"
MF "a[4:7],v[16:19],v[76:79],a[220:223]\n"
"s_add_u32 m0,s64,31616\n"
"v_add_f32 v59,v59,v103\n"
CV "v101,v102,v103\n"
MF "a[8:11],v[16:19],v[80:83],a[220:223]\n"
LD "v7,s[32:35],s72 offen offset:128 nt lds\n"
MF "a[28:31],v[20:23],v[80:83],a[220:223]\n"
"v_exp_f32 v104,v104\n"
MF "a[32:35],v[20:23],v[84:87],a[220:223]\n"
"s_add_u32 m0,s64,32512\n"
"v_exp_f32 v105,v105\n"
MF "a[12:15],v[16:19],v[84:87],a[220:223]\n"
LD "v7,s[32:35],s72 offen offset:256 nt lds\n"
MF "a[16:19],v[16:19],v[88:91],a[220:223]\n"
"v_exp_f32 v106,v106\n"
MF "a[36:39],v[20:23],v[88:91],a[220:223]\n"
"s_waitcnt lgkmcnt(0)\n"
TR "v[16:17],v2 offset:1024\n"
TR "v[18:19],v2 offset:7168\n"
MF "a[56:59],v[24:27],v[88:91],a[220:223]\n"
"s_nop 0\n"
TR "v[20:21],v3 offset:1024\n"
TR "v[22:23],v3 offset:7168\n"
MF "a[76:79],v[28:31],v[88:91],a[220:223]\n"
"v_exp_f32 v107,v107\n"
MF "a[72:75],v[28:31],v[84:87],a[220:223]\n"
"v_add_f32 v60,v55,v104\n"
"v_add_f32 v60,v60,v105\n"
MF "a[52:55],v[24:27],v[84:87],a[220:223]\n"
CV "v104,v104,v105\n"
"v_add_f32 v60,v60,v106\n"
MF "a[48:51],v[24:27],v[80:83],a[220:223]\n"
"v_add_f32 v60,v60,v107\n"
CV "v105,v106,v107\n"
MF "a[68:71],v[28:31],v[80:83],a[220:223]\n"
"v_exp_f32 v108,v108\n"
MF "a[64:67],v[28:31],v[76:79],a[220:223]\n"
".Lpg10b:\n"
"s_cbranch_execz .Lpg1a\n"
".Lpg10d:\n"
"s_cbranch_execz .Lisr2\n"
"v_exp_f32 v109,v109\n"
MF "a[44:47],v[24:27],v[76:79],a[220:223]\n"
"v_exp_f32 v110,v110\n"
MF "a[40:43],v[24:27],v[72:75],a[220:223]\n"
"v_exp_f32 v111,v111\n"
MF "a[60:63],v[28:31],v[72:75],a[220:223]\n"
"s_waitcnt lgkmcnt(0)\n"
TR "v[24:25],v4 offset:1024\n"
TR "v[26:27],v4 offset:7168\n"
MF "a[80:83],v[16:19],v[72:75],a[220:223]\n"
"s_nop 0\n"
TR "v[28:29],v5 offset:1024\n"
TR "v[30:31],v5 offset:7168\n"
MF "a[100:103],v[20:23],v[72:75],a[220:223]\n"
"v_add_f32 v61,v56,v108\n"
"v_add_f32 v61,v61,v109\n"
MF "a[104:107],v[20:23],v[76:79],a[220:223]\n"
CV "v108,v108,v109\n"
"v_add_f32 v61,v61,v110\n"
MF "a[84:87],v[16:19],v[76:79],a[220:223]\n"
"v_add_f32 v61,v61,v111\n"
CV "v109,v110,v111\n"
MF "a[88:91],v[16:19],v[80:83],a[220:223]\n"
"v_exp_f32 v112,v112\n"
MF "a[108:111],v[20:23],v[80:83],a[220:223]\n"
"v_exp_f32 v113,v113\n"
MF "a[112:115],v[20:23],v[84:87],a[220:223]\n"
"v_exp_f32 v114,v114\n"
MF "a[92:95],v[16:19],v[84:87],a[220:223]\n"
"v_exp_f32 v115,v115\n"
MF "a[96:99],v[16:19],v[88:91],a[220:223]\n"
"v_add_f32 v57,v57,v112\n"
"v_add_f32 v57,v57,v113\n"
MF "a[116:119],v[20:23],v[88:91],a[220:223]\n"
"s_waitcnt lgkmcnt(0)\n"
TR "v[16:17],v2 offset:2048\n"
TR "v[18:19],v2 offset:8192\n"
MF "a[136:139],v[24:27],v[88:91],a[220:223]\n"
"s_nop 0\n"
TR "v[20:21],v3 offset:2048\n"
TR "v[22:23],v3 offset:8192\n"
MF "a[156:159],v[28:31],v[88:91],a[220:223]\n"
CV "v94,v112,v113\n"
"v_add_f32 v57,v57,v114\n"
MF "a[152:155],v[28:31],v[84:87],a[220:223]\n"
"v_add_f32 v57,v57,v115\n"
CV "v95,v114,v115\n"
MF "a[132:135],v[24:27],v[84:87],a[220:223]\n"
"v_exp_f32 v116,v116\n"
MF "a[128:131],v[24:27],v[80:83],a[220:223]\n"
"v_exp_f32 v117,v117\n"
MF "a[148:151],v[28:31],v[80:83],a[220:223]\n"
"v_exp_f32 v118,v118\n"
MF "a[144:147],v[28:31],v[76:79],a[220:223]\n"
"v_exp_f32 v119,v119\n"
MF "a[124:127],v[24:27],v[76:79],a[220:223]\n"
"v_add_f32 v58,v58,v116\n"
"v_add_f32 v58,v58,v117\n"
MF "a[120:123],v[24:27],v[72:75],a[220:223]\n"
CV "v98,v116,v117\n"
"v_add_f32 v58,v58,v118\n"
MF "a[140:143],v[28:31],v[72:75],a[220:223]\n"
"s_waitcnt lgkmcnt(0)\n"
TR "v[24:25],v4 offset:2048\n"
TR "v[26:27],v4 offset:8192\n"
MF "a[160:163],v[16:19],v[72:75],a[220:223]\n"
"s_nop 0\n"
TR "v[28:29],v5 offset:2048\n"
TR "v[30:31],v5 offset:8192\n"
MF "a[180:183],v[20:23],v[72:75],a[220:223]\n"
"v_add_f32 v58,v58,v119\n"
CV "v99,v118,v119\n"
MF "a[184:187],v[20:23],v[76:79],a[220:223]\n"
"v_exp_f32 v120,v120\n"
MF "a[164:167],v[16:19],v[76:79],a[220:223]\n"
"v_exp_f32 v121,v121\n"
MF "a[168:171],v[16:19],v[80:83],a[220:223]\n"
"v_exp_f32 v122,v122\n"
MF "a[188:191],v[20:23],v[80:83],a[220:223]\n"
"v_exp_f32 v123,v123\n"
MF "a[192:195],v[20:23],v[84:87],a[220:223]\n"
"v_add_f32 v59,v59,v120\n"
"v_add_f32 v59,v59,v121\n"
"s_cmp_eq_u32 s57,0\n"
"s_cbranch_scc1 .Lisx6\n"
"s_mov_b64 exec,0\n"
"s_cmp_eq_u32 s56,0\n"
"s_cbranch_scc1 .Lpg18g\n"
"s_cmp_eq_u32 s56,1\n"
"s_cbranch_scc1 .Lpg17g\n"
"s_cmp_eq_u32 s56,2\n"
"s_cbranch_scc1 .Lpg16g\n"
"s_branch .Lpgep_g\n"
".Lisr6:\n"
"s_mov_b64 exec,-1\n"
".Lisx6:\n"
MF "a[172:175],v[16:19],v[84:87],a[220:223]\n"
"s_waitcnt vmcnt(6) lgkmcnt(0)\n"
CV "v102,v120,v121\n"
"v_add_f32 v59,v59,v122\n"
MF "a[176:179],v[16:19],v[88:91],a[220:223]\n"
"s_barrier\n"
MF "a[196:199],v[20:23],v[88:91],a[220:223]\n"
"s_add_u32 s73,s73,12288\n"
"ds_read_b128 v[16:19],v0\n"
"ds_read_b128 v[20:23],v1\n"
MF "a[216:219],v[24:27],v[88:91],a[220:223]\n"
"s_cmp_eq_u32 s73,s19\n"
"s_cselect_b32 s73,s18,s73\n"
"s_add_u32 m0,s65,49152\n"
MF "a[236:239],v[28:31],v[88:91],a[220:223]\n"
LD "v8,s[36:39],s73 offen nt lds\n"
MF "a[232:235],v[28:31],v[84:87],a[220:223]\n"
"s_add_u32 m0,s65,50048\n"
"v_add_f32 v59,v59,v123\n"
CV "v103,v122,v123\n"
MF "a[212:215],v[24:27],v[84:87],a[220:223]\n"
LD "v8,s[36:39],s73 offen offset:128 nt lds\n"
MF "a[208:211],v[24:27],v[80:83],a[220:223]\n"
"s_add_u32 m0,s65,50944\n"
MF "a[228:231],v[28:31],v[80:83],a[220:223]\n"
LD "v8,s[36:39],s73 offen offset:256 nt lds\n"
MF "a[224:227],v[28:31],v[76:79],a[220:223]\n"
MF "a[204:207],v[24:27],v[76:79],a[220:223]\n"
MF "a[200:203],v[24:27],v[72:75],a[220:223]\n"
MF "a[220:223],v[28:31],v[72:75],a[220:223]\n"
"s_waitcnt lgkmcnt(0)\n"
".Laloopx:\n"
MF "v[72:75],v[16:19],v[136:139],v[32:35]\n"
"s_nop 0\n"
"ds_read_b128 v[24:27],v0 offset:1024\n"
"ds_read_b128 v[28:31],v1 offset:1024\n"
MF "v[72:75],v[20:23],v[156:159],v[72:75]\n"
MF "v[76:79],v[20:23],v[160:163],v[36:39]\n"
MF "v[76:79],v[16:19],v[140:143],v[76:79]\n"
MF "v[80:83],v[16:19],v[144:147],v[40:43]\n"
MF "v[80:83],v[20:23],v[164:167],v[80:83]\n"
MF "v[84:87],v[20:23],v[168:171],v[44:47]\n"
MF "v[84:87],v[16:19],v[148:151],v[84:87]\n"
"s_add_u32 m0,s65,49152\n"
MF "v[88:91],v[16:19],v[152:155],v[48:51]\n"
LD "v8,s[36:39],s73 offen offset:3072 nt lds\n"
MF "v[88:91],v[20:23],v[172:175],v[88:91]\n"
"s_waitcnt lgkmcnt(0)\n"
"ds_read_b128 v[16:19],v0 offset:2048\n"
"ds_read_b128 v[20:23],v1 offset:2048\n"
MF "v[88:91],v[24:27],v[192:195],v[88:91]\n"
"v_exp_f32 v124,v124\n"
MF "v[88:91],v[28:31],v[212:215],v[88:91]\n"
"s_add_u32 m0,s65,50048\n"
"v_exp_f32 v125,v125\n"
MF "v[84:87],v[28:31],v[208:211],v[84:87]\n"
LD "v8,s[36:39],s73 offen offset:3200 nt lds\n"
MF "v[84:87],v[24:27],v[188:191],v[84:87]\n"
"v_exp_f32 v126,v126\n"
MF "v[80:83],v[24:27],v[184:187],v[80:83]\n"
"v_exp_f32 v127,v127\n"
MF "v[80:83],v[28:31],v[204:207],v[80:83]\n"
"s_add_u32 m0,s65,50944\n"
"v_add_f32 v60,v60,v124\n"
"v_add_f32 v60,v60,v125\n"
MF "v[76:79],v[28:31],v[200:203],v[76:79]\n"
LD "v8,s[36:39],s73 offen offset:3328 nt lds\n"
MF "v[76:79],v[24:27],v[180:183],v[76:79]\n"
CV "v106,v124,v125\n"
"v_add_f32 v60,v60,v126\n"
MF "v[72:75],v[24:27],v[176:179],v[72:75]\n"
"v_add_f32 v60,v60,v127\n"
CV "v107,v126,v127\n"
MF "v[72:75],v[28:31],v[196:199],v[72:75]\n"
"s_waitcnt lgkmcnt(0)\n"
"ds_read_b128 v[24:27],v0 offset:6144\n"
"ds_read_b128 v[28:31],v1 offset:6144\n"
MF "v[72:75],v[16:19],v[216:219],v[72:75]\n"
"v_exp_f32 v128,v128\n"
MF "v[72:75],v[20:23],v[236:239],v[72:75]\n"
"v_exp_f32 v129,v129\n"
MF "v[76:79],v[20:23],v[240:243],v[76:79]\n"
"v_exp_f32 v130,v130\n"
MF "v[76:79],v[16:19],v[220:223],v[76:79]\n"
"v_exp_f32 v131,v131\n"
MF "v[80:83],v[16:19],v[224:227],v[80:83]\n"
"v_add_f32 v61,v61,v128\n"
"v_add_f32 v61,v61,v129\n"
MF "v[80:83],v[20:23],v[244:247],v[80:83]\n"
CV "v110,v128,v129\n"
"v_add_f32 v61,v61,v130\n"
MF "v[84:87],v[20:23],v[248:251],v[84:87]\n"
"v_add_f32 v61,v61,v131\n"
CV "v111,v130,v131\n"
MF "v[84:87],v[16:19],v[228:231],v[84:87]\n"
"v_max3_f32 v10,v57,v58,v59\n"
"v_max3_f32 v10,v10,v60,v61\n"
MF "v[88:91],v[16:19],v[232:235],v[88:91]\n"
"v_cmp_ngt_f32 vcc,s77,v10\n"
MF "v[88:91],v[20:23],v[252:255],v[88:91]\n"
"s_waitcnt lgkmcnt(0)\n"
"s_cbranch_vccnz .Lstub_6\n"
".Lback_6:\n"
MF "v[112:115],v[24:27],v[136:139],v[32:35]\n"
"s_add_u32 s74,s74,12288\n"
"ds_read_b128 v[16:19],v0 offset:7168\n"
"ds_read_b128 v[20:23],v1 offset:7168\n"
MF "v[112:115],v[28:31],v[156:159],v[112:115]\n"
"s_cmp_eq_u32 s74,s20\n"
"s_cselect_b32 s74,s70,s74\n"
MF "v[116:119],v[28:31],v[160:163],v[36:39]\n"
"v_exp_f32 v72,v72\n"
MF "v[116:119],v[24:27],v[140:143],v[116:119]\n"
"v_exp_f32 v73,v73\n"
MF "v[120:123],v[24:27],v[144:147],v[40:43]\n"
"v_exp_f32 v74,v74\n"
MF "v[120:123],v[28:31],v[164:167],v[120:123]\n"
"v_exp_f32 v75,v75\n"
MF "v[124:127],v[28:31],v[168:171],v[44:47]\n"
"v_add_f32 v52,v57,v72\n"
"v_add_f32 v52,v52,v73\n"
MF "v[124:127],v[24:27],v[148:151],v[124:127]\n"
CV "v72,v72,v73\n"
"v_add_f32 v52,v52,v74\n"
MF "v[128:131],v[24:27],v[152:155],v[48:51]\n"
"v_add_f32 v52,v52,v75\n"
CV "v73,v74,v75\n"
MF "v[128:131],v[28:31],v[172:175],v[128:131]\n"
"s_waitcnt lgkmcnt(0)\n"
"ds_read_b128 v[24:27],v0 offset:8192\n"
"ds_read_b128 v[28:31],v1 offset:8192\n"
MF "v[128:131],v[16:19],v[192:195],v[128:131]\n"
"v_exp_f32 v76,v76\n"
MF "v[128:131],v[20:23],v[212:215],v[128:131]\n"
"v_exp_f32 v77,v77\n"
MF "v[124:127],v[20:23],v[208:211],v[124:127]\n"
"v_exp_f32 v78,v78\n"
MF "v[124:127],v[16:19],v[188:191],v[124:127]\n"
"v_exp_f32 v79,v79\n"
MF "v[120:123],v[16:19],v[184:187],v[120:123]\n"
"v_add_f32 v53,v58,v76\n"
"v_add_f32 v53,v53,v77\n"
MF "v[120:123],v[20:23],v[204:207],v[120:123]\n"
CV "v76,v76,v77\n"
"v_add_f32 v53,v53,v78\n"
MF "v[116:119],v[20:23],v[200:203],v[116:119]\n"
"s_waitcnt vmcnt(12) lgkmcnt(0)\n"
"v_add_f32 v53,v53,v79\n"
CV "v77,v78,v79\n"
MF "v[116:119],v[16:19],v[180:183],v[116:119]\n"
"s_barrier\n"
MF "v[112:115],v[16:19],v[176:179],v[112:115]\n"
"v_exp_f32 v80,v80\n"
MF "v[112:115],v[20:23],v[196:199],v[112:115]\n"
"s_nop 0\n"
TR "v[16:17],v2 offset:24576\n"
TR "v[18:19],v2 offset:30720\n"
MF "v[112:115],v[24:27],v[216:219],v[112:115]\n"
"s_add_u32 s72,s72,6144\n"
TR "v[20:21],v3 offset:24576\n"
TR "v[22:23],v3 offset:30720\n"
MF "v[112:115],v[28:31],v[236:239],v[112:115]\n"
"s_cmp_eq_u32 s72,s17\n"
"s_cselect_b32 s72,s16,s72\n"
"s_add_u32 m0,s64,0\n"
MF "v[116:119],v[28:31],v[240:243],v[116:119]\n"
LD "v7,s[32:35],s72 offen nt lds\n"
MF "v[116:119],v[24:27],v[220:223],v[116:119]\n"
"s_add_u32 m0,s64,896\n"
"v_exp_f32 v81,v81\n"
MF "v[120:123],v[24:27],v[224:227],v[120:123]\n"
LD "v7,s[32:35],s72 offen offset:128 nt lds\n"
MF "v[120:123],v[28:31],v[244:247],v[120:123]\n"
"v_exp_f32 v82,v82\n"
MF "v[124:127],v[28:31],v[248:251],v[124:127]\n"
"s_add_u32 m0,s64,1792\n"
"v_exp_f32 v83,v83\n"
MF "v[124:127],v[24:27],v[228:231],v[124:127]\n"
LD "v7,s[32:35],s72 offen offset:256 nt lds\n"
MF "v[128:131],v[24:27],v[232:235],v[128:131]\n"
"s_add_u32 s72,s72,6144\n"
"v_add_f32 v54,v59,v80\n"
"v_add_f32 v54,v54,v81\n"
MF "v[128:131],v[28:31],v[252:255],v[128:131]\n"
"s_waitcnt lgkmcnt(0)\n"
TR "v[24:25],v4 offset:24576\n"
TR "v[26:27],v4 offset:30720\n"
MF "a[0:3],v[16:19],v[92:95],a[0:3]\n"
"s_add_u32 m0,s64,6144\n"
TR "v[28:29],v5 offset:24576\n"
TR "v[30:31],v5 offset:30720\n"
MF "a[20:23],v[20:23],v[92:95],a[20:23]\n"
LD "v7,s[32:35],s72 offen nt lds\n"
MF "a[24:27],v[20:23],v[96:99],a[24:27]\n"
CV "v80,v80,v81\n"
"v_add_f32 v54,v54,v82\n"
MF "a[4:7],v[16:19],v[96:99],a[4:7]\n"
"s_add_u32 m0,s64,7040\n"
"v_add_f32 v54,v54,v83\n"
CV "v81,v82,v83\n"
MF "a[8:11],v[16:19],v[100:103],a[8:11]\n"
LD "v7,s[32:35],s72 offen offset:128 nt lds\n"
MF "a[28:31],v[20:23],v[100:103],a[28:31]\n"
"v_exp_f32 v84,v84\n"
MF "a[32:35],v[20:23],v[104:107],a[32:35]\n"
"s_add_u32 m0,s64,7936\n"
"v_exp_f32 v85,v85\n"
MF "a[12:15],v[16:19],v[104:107],a[12:15]\n"
LD "v7,s[32:35],s72 offen offset:256 nt lds\n"
MF "a[16:19],v[16:19],v[108:111],a[16:19]\n"
"v_exp_f32 v86,v86\n"
MF "a[36:39],v[20:23],v[108:111],a[36:39]\n"
"s_waitcnt lgkmcnt(0)\n"
TR "v[16:17],v2 offset:25600\n"
TR "v[18:19],v2 offset:31744\n"
MF "a[56:59],v[24:27],v[108:111],a[56:59]\n"
"s_nop 0\n"
TR "v[20:21],v3 offset:25600\n"
TR "v[22:23],v3 offset:31744\n"
MF "a[76:79],v[28:31],v[108:111],a[76:79]\n"
"v_exp_f32 v87,v87\n"
MF "a[72:75],v[28:31],v[104:107],a[72:75]\n"
"v_add_f32 v55,v60,v84\n"
"v_add_f32 v55,v55,v85\n"
MF "a[52:55],v[24:27],v[104:107],a[52:55]\n"
CV "v84,v84,v85\n"
"v_add_f32 v55,v55,v86\n"
MF "a[48:51],v[24:27],v[100:103],a[48:51]\n"
"v_add_f32 v55,v55,v87\n"
CV "v85,v86,v87\n"
MF "a[68:71],v[28:31],v[100:103],a[68:71]\n"
"v_exp_f32 v88,v88\n"
MF "a[64:67],v[28:31],v[96:99],a[64:67]\n"
"v_exp_f32 v89,v89\n"
MF "a[44:47],v[24:27],v[96:99],a[44:47]\n"
"v_exp_f32 v90,v90\n"
MF "a[40:43],v[24:27],v[92:95],a[40:43]\n"
"v_exp_f32 v91,v91\n"
MF "a[60:63],v[28:31],v[92:95],a[60:63]\n"
"s_waitcnt lgkmcnt(0)\n"
TR "v[24:25],v4 offset:25600\n"
TR "v[26:27],v4 offset:31744\n"
MF "a[80:83],v[16:19],v[92:95],a[80:83]\n"
"s_nop 0\n"
TR "v[28:29],v5 offset:25600\n"
TR "v[30:31],v5 offset:31744\n"
MF "a[100:103],v[20:23],v[92:95],a[100:103]\n"
"v_add_f32 v56,v61,v88\n"
"v_add_f32 v56,v56,v89\n"
MF "a[104:107],v[20:23],v[96:99],a[104:107]\n"
CV "v88,v88,v89\n"
"v_add_f32 v56,v56,v90\n"
MF "a[84:87],v[16:19],v[96:99],a[84:87]\n"
"v_add_f32 v56,v56,v91\n"
CV "v89,v90,v91\n"
MF "a[88:91],v[16:19],v[100:103],a[88:91]\n"
"v_exp_f32 v112,v112\n"
MF "a[108:111],v[20:23],v[100:103],a[108:111]\n"
"v_exp_f32 v113,v113\n"
MF "a[112:115],v[20:23],v[104:107],a[112:115]\n"
"v_exp_f32 v114,v114\n"
MF "a[92:95],v[16:19],v[104:107],a[92:95]\n"
"v_exp_f32 v115,v115\n"
MF "a[96:99],v[16:19],v[108:111],a[96:99]\n"
"v_add_f32 v52,v52,v112\n"
"v_add_f32 v52,v52,v113\n"
MF "a[116:119],v[20:23],v[108:111],a[116:119]\n"
"s_waitcnt lgkmcnt(0)\n"
TR "v[16:17],v2 offset:26624\n"
TR "v[18:19],v2 offset:32768\n"
MF "a[136:139],v[24:27],v[108:111],a[136:139]\n"
"s_nop 0\n"
TR "v[20:21],v3 offset:26624\n"
TR "v[22:23],v3 offset:32768\n"
MF "a[156:159],v[28:31],v[108:111],a[156:159]\n"
CV "v74,v112,v113\n"
"v_add_f32 v52,v52,v114\n"
MF "a[152:155],v[28:31],v[104:107],a[152:155]\n"
"v_add_f32 v52,v52,v115\n"
CV "v75,v114,v115\n"
MF "a[132:135],v[24:27],v[104:107],a[132:135]\n"
"v_exp_f32 v116,v116\n"
MF "a[128:131],v[24:27],v[100:103],a[128:131]\n"
"v_exp_f32 v117,v117\n"
MF "a[148:151],v[28:31],v[100:103],a[148:151]\n"
"v_exp_f32 v118,v118\n"
MF "a[144:147],v[28:31],v[96:99],a[144:147]\n"
"v_exp_f32 v119,v119\n"
MF "a[124:127],v[24:27],v[96:99],a[124:127]\n"
"v_add_f32 v53,v53,v116\n"
"v_add_f32 v53,v53,v117\n"
MF "a[120:123],v[24:27],v[92:95],a[120:123]\n"
CV "v78,v116,v117\n"
"v_add_f32 v53,v53,v118\n"
MF "a[140:143],v[28:31],v[92:95],a[140:143]\n"
"s_waitcnt lgkmcnt(0)\n"
TR "v[24:25],v4 offset:26624\n"
TR "v[26:27],v4 offset:32768\n"
MF "a[160:163],v[16:19],v[92:95],a[160:163]\n"
"s_nop 0\n"
TR "v[28:29],v5 offset:26624\n"
TR "v[30:31],v5 offset:32768\n"
MF "a[180:183],v[20:23],v[92:95],a[180:183]\n"
"v_add_f32 v53,v53,v119\n"
CV "v79,v118,v119\n"
MF "a[184:187],v[20:23],v[96:99],a[184:187]\n"
"v_exp_f32 v120,v120\n"
MF "a[164:167],v[16:19],v[96:99],a[164:167]\n"
"v_exp_f32 v121,v121\n"
MF "a[168:171],v[16:19],v[100:103],a[168:171]\n"
"v_exp_f32 v122,v122\n"
MF "a[188:191],v[20:23],v[100:103],a[188:191]\n"
"v_exp_f32 v123,v123\n"
MF "a[192:195],v[20:23],v[104:107],a[192:195]\n"
"v_add_f32 v54,v54,v120\n"
"v_add_f32 v54,v54,v121\n"
MF "a[172:175],v[16:19],v[104:107],a[172:175]\n"
"s_waitcnt vmcnt(12) lgkmcnt(0)\n"
CV "v82,v120,v121\n"
"v_add_f32 v54,v54,v122\n"
MF "a[176:179],v[16:19],v[108:111],a[176:179]\n"
"s_barrier\n"
MF "a[196:199],v[20:23],v[108:111],a[196:199]\n"
"s_add_u32 s73,s73,12288\n"
"ds_read_b128 v[16:19],v0 offset:24576\n"
"ds_read_b128 v[20:23],v1 offset:24576\n"
MF "a[216:219],v[24:27],v[108:111],a[216:219]\n"
"s_cmp_eq_u32 s73,s19\n"
"s_cselect_b32 s73,s18,s73\n"
"s_add_u32 m0,s65,73728\n"
MF "a[236:239],v[28:31],v[108:111],a[236:239]\n"
LD "v8,s[36:39],s73 offen nt lds\n"
MF "a[232:235],v[28:31],v[104:107],a[232:235]\n"
"s_add_u32 m0,s65,74624\n"
"v_add_f32 v54,v54,v123\n"
CV "v83,v122,v123\n"
MF "a[212:215],v[24:27],v[104:107],a[212:215]\n"
LD "v8,s[36:39],s73 offen offset:128 nt lds\n"
MF "a[208:211],v[24:27],v[100:103],a[208:211]\n"
"s_add_u32 m0,s65,75520\n"
MF "a[228:231],v[28:31],v[100:103],a[228:231]\n"
LD "v8,s[36:39],s73 offen offset:256 nt lds\n"
MF "a[224:227],v[28:31],v[96:99],a[224:227]\n"
MF "a[204:207],v[24:27],v[96:99],a[204:207]\n"
MF "a[200:203],v[24:27],v[92:95],a[200:203]\n"
MF "a[220:223],v[28:31],v[92:95],a[220:223]\n"
"s_waitcnt lgkmcnt(0)\n"
MF "v[92:95],v[16:19],v[136:139],v[32:35]\n"
"s_add_u32 m0,s65,73728\n"
"ds_read_b128 v[24:27],v0 offset:25600\n"
"ds_read_b128 v[28:31],v1 offset:25600\n"
MF "v[92:95],v[20:23],v[156:159],v[92:95]\n"
LD "v8,s[36:39],s73 offen offset:3072 nt lds\n"
MF "v[96:99],v[20:23],v[160:163],v[36:39]\n"
"v_exp_f32 v124,v124\n"
MF "v[96:99],v[16:19],v[140:143],v[96:99]\n"
"v_exp_f32 v125,v125\n"
MF "v[100:103],v[16:19],v[144:147],v[40:43]\n"
"s_add_u32 m0,s65,74624\n"
"v_exp_f32 v126,v126\n"
MF "v[100:103],v[20:23],v[164:167],v[100:103]\n"
LD "v8,s[36:39],s73 offen offset:3200 nt lds\n"
MF "v[104:107],v[20:23],v[168:171],v[44:47]\n"
"v_exp_f32 v127,v127\n"
MF "v[104:107],v[16:19],v[148:151],v[104:107]\n"
"s_add_u32 m0,s65,75520\n"
"v_add_f32 v55,v55,v124\n"
"v_add_f32 v55,v55,v125\n"
MF "v[108:111],v[16:19],v[152:155],v[48:51]\n"
LD "v8,s[36:39],s73 offen offset:3328 nt lds\n"
MF "v[108:111],v[20:23],v[172:175],v[108:111]\n"
"s_waitcnt lgkmcnt(0)\n"
"ds_read_b128 v[16:19],v0 offset:26624\n"
"ds_read_b128 v[20:23],v1 offset:26624\n"
MF "v[108:111],v[24:27],v[192:195],v[108:111]\n"
CV "v86,v124,v125\n"
"v_add_f32 v55,v55,v126\n"
MF "v[108:111],v[28:31],v[212:215],v[108:111]\n"
"v_add_f32 v55,v55,v127\n"
CV "v87,v126,v127\n"
MF "v[104:107],v[28:31],v[208:211],v[104:107]\n"
"v_exp_f32 v128,v128\n"
MF "v[104:107],v[24:27],v[188:191],v[104:107]\n"
"v_exp_f32 v129,v129\n"
MF "v[100:103],v[24:27],v[184:187],v[100:103]\n"
"v_exp_f32 v130,v130\n"
MF "v[100:103],v[28:31],v[204:207],v[100:103]\n"
"v_exp_f32 v131,v131\n"
MF "v[96:99],v[28:31],v[200:203],v[96:99]\n"
"v_add_f32 v56,v56,v128\n"
"v_add_f32 v56,v56,v129\n"
MF "v[96:99],v[24:27],v[180:183],v[96:99]\n"
CV "v90,v128,v129\n"
"v_add_f32 v56,v56,v130\n"
MF "v[92:95],v[24:27],v[176:179],v[92:95]\n"
"v_add_f32 v56,v56,v131\n"
CV "v91,v130,v131\n"
MF "v[92:95],v[28:31],v[196:199],v[92:95]\n"
"s_waitcnt lgkmcnt(0)\n"
"ds_read_b128 v[24:27],v0 offset:30720\n"
"ds_read_b128 v[28:31],v1 offset:30720\n"
MF "v[92:95],v[16:19],v[216:219],v[92:95]\n"
MF "v[92:95],v[20:23],v[236:239],v[92:95]\n"
MF "v[96:99],v[20:23],v[240:243],v[96:99]\n"
MF "v[96:99],v[16:19],v[220:223],v[96:99]\n"
MF "v[100:103],v[16:19],v[224:227],v[100:103]\n"
MF "v[100:103],v[20:23],v[244:247],v[100:103]\n"
MF "v[104:107],v[20:23],v[248:251],v[104:107]\n"
MF "v[104:107],v[16:19],v[228:231],v[104:107]\n"
"v_max3_f32 v10,v52,v53,v54\n"
"v_max3_f32 v10,v10,v55,v56\n"
MF "v[108:111],v[16:19],v[232:235],v[108:111]\n"
"v_cmp_ngt_f32 vcc,s77,v10\n"
MF "v[108:111],v[20:23],v[252:255],v[108:111]\n"
"s_waitcnt lgkmcnt(0)\n"
"s_cbranch_vccnz .Lstub_7\n"
".Lback_7:\n"
MF "v[112:115],v[24:27],v[136:139],v[32:35]\n"
"s_add_u32 s74,s74,12288\n"
"ds_read_b128 v[16:19],v0 offset:31744\n"
"ds_read_b128 v[20:23],v1 offset:31744\n"
MF "v[112:115],v[28:31],v[156:159],v[112:115]\n"
"s_cmp_eq_u32 s74,s20\n"
"s_cselect_b32 s74,s70,s74\n"
MF "v[116:119],v[28:31],v[160:163],v[36:39]\n"
"v_exp_f32 v92,v92\n"
MF "v[116:119],v[24:27],v[140:143],v[116:119]\n"
"v_exp_f32 v93,v93\n"
MF "v[120:123],v[24:27],v[144:147],v[40:43]\n"
"v_exp_f32 v94,v94\n"
MF "v[120:123],v[28:31],v[164:167],v[120:123]\n"
"v_exp_f32 v95,v95\n"
MF "v[124:127],v[28:31],v[168:171],v[44:47]\n"
"v_add_f32 v57,v52,v92\n"
"v_add_f32 v57,v57,v93\n"
MF "v[124:127],v[24:27],v[148:151],v[124:127]\n"
CV "v92,v92,v93\n"
"v_add_f32 v57,v57,v94\n"
MF "v[128:131],v[24:27],v[152:155],v[48:51]\n"
"v_add_f32 v57,v57,v95\n"
CV "v93,v94,v95\n"
MF "v[128:131],v[28:31],v[172:175],v[128:131]\n"
"s_waitcnt lgkmcnt(0)\n"
"ds_read_b128 v[24:27],v0 offset:32768\n"
"ds_read_b128 v[28:31],v1 offset:32768\n"
MF "v[128:131],v[16:19],v[192:195],v[128:131]\n"
"s_sub_u32 s61,s61,1\n"
"v_exp_f32 v96,v96\n"
MF "v[128:131],v[20:23],v[212:215],v[128:131]\n"
"v_exp_f32 v97,v97\n"
MF "v[124:127],v[20:23],v[208:211],v[124:127]\n"
"v_exp_f32 v98,v98\n"
MF "v[124:127],v[16:19],v[188:191],v[124:127]\n"
"v_exp_f32 v99,v99\n"
MF "v[120:123],v[16:19],v[184:187],v[120:123]\n"
"v_add_f32 v58,v53,v96\n"
"v_add_f32 v58,v58,v97\n"
MF "v[120:123],v[20:23],v[204:207],v[120:123]\n"
CV "v96,v96,v97\n"
"v_add_f32 v58,v58,v98\n"
MF "v[116:119],v[20:23],v[200:203],v[116:119]\n"
"s_waitcnt vmcnt(12) lgkmcnt(0)\n"
"v_add_f32 v58,v58,v99\n"
CV "v97,v98,v99\n"
MF "v[116:119],v[16:19],v[180:183],v[116:119]\n"
"s_barrier\n"
MF "v[112:115],v[16:19],v[176:179],v[112:115]\n"
"v_exp_f32 v100,v100\n"
MF "v[112:115],v[20:23],v[196:199],v[112:115]\n"
"s_nop 0\n"
TR "v[16:17],v2\n"
TR "v[18:19],v2 offset:6144\n"
MF "v[112:115],v[24:27],v[216:219],v[112:115]\n"
"s_add_u32 s72,s72,6144\n"
TR "v[20:21],v3\n"
TR "v[22:23],v3 offset:6144\n"
MF "v[112:115],v[28:31],v[236:239],v[112:115]\n"
"s_cmp_eq_u32 s72,s17\n"
"s_cselect_b32 s72,s16,s72\n"
"s_add_u32 m0,s64,24576\n"
MF "v[116:119],v[28:31],v[240:243],v[116:119]\n"
LD "v7,s[32:35],s72 offen nt lds\n"
MF "v[116:119],v[24:27],v[220:223],v[116:119]\n"
"s_add_u32 m0,s64,25472\n"
"v_exp_f32 v101,v101\n"
MF "v[120:123],v[24:27],v[224:227],v[120:123]\n"
LD "v7,s[32:35],s72 offen offset:128 nt lds\n"
MF "v[120:123],v[28:31],v[244:247],v[120:123]\n"
"v_exp_f32 v102,v102\n"
MF "v[124:127],v[28:31],v[248:251],v[124:127]\n"
"s_add_u32 m0,s64,26368\n"
"v_exp_f32 v103,v103\n"
MF "v[124:127],v[24:27],v[228:231],v[124:127]\n"
LD "v7,s[32:35],s72 offen offset:256 nt lds\n"
MF "v[128:131],v[24:27],v[232:235],v[128:131]\n"
"s_add_u32 s72,s72,6144\n"
"v_add_f32 v59,v54,v100\n"
"v_add_f32 v59,v59,v101\n"
MF "v[128:131],v[28:31],v[252:255],v[128:131]\n"
"s_waitcnt lgkmcnt(0)\n"
TR "v[24:25],v4\n"
TR "v[26:27],v4 offset:6144\n"
MF "a[0:3],v[16:19],v[72:75],a[0:3]\n"
"s_add_u32 m0,s64,30720\n"
TR "v[28:29],v5\n"
TR "v[30:31],v5 offset:6144\n"
MF "a[20:23],v[20:23],v[72:75],a[20:23]\n"
LD "v7,s[32:35],s72 offen nt lds\n"
MF "a[24:27],v[20:23],v[76:79],a[24:27]\n"
CV "v100,v100,v101\n"
"v_add_f32 v59,v59,v102\n"
MF "a[4:7],v[16:19],v[76:79],a[4:7]\n"
"s_add_u32 m0,s64,31616\n"
"v_add_f32 v59,v59,v103\n"
CV "v101,v102,v103\n"
MF "a[8:11],v[16:19],v[80:83],a[8:11]\n"
LD "v7,s[32:35],s72 offen offset:128 nt lds\n"
MF "a[28:31],v[20:23],v[80:83],a[28:31]\n"
"v_exp_f32 v104,v104\n"
MF "a[32:35],v[20:23],v[84:87],a[32:35]\n"
"s_add_u32 m0,s64,32512\n"
"v_exp_f32 v105,v105\n"
MF "a[12:15],v[16:19],v[84:87],a[12:15]\n"
LD "v7,s[32:35],s72 offen offset:256 nt lds\n"
MF "a[16:19],v[16:19],v[88:91],a[16:19]\n"
"v_exp_f32 v106,v106\n"
MF "a[36:39],v[20:23],v[88:91],a[36:39]\n"
"s_waitcnt lgkmcnt(0)\n"
TR "v[16:17],v2 offset:1024\n"
TR "v[18:19],v2 offset:7168\n"
MF "a[56:59],v[24:27],v[88:91],a[56:59]\n"
".Lpg11b:\n"
"s_cbranch_execz .Lpgqj_a\n"
".Lpg11d:\n"
"s_cbranch_execz .Lisr2\n"
"s_nop 0\n"
TR "v[20:21],v3 offset:1024\n"
TR "v[22:23],v3 offset:7168\n"
MF "a[76:79],v[28:31],v[88:91],a[76:79]\n"
"v_exp_f32 v107,v107\n"
MF "a[72:75],v[28:31],v[84:87],a[72:75]\n"
"v_add_f32 v60,v55,v104\n"
"v_add_f32 v60,v60,v105\n"
MF "a[52:55],v[24:27],v[84:87],a[52:55]\n"
CV "v104,v104,v105\n"
"v_add_f32 v60,v60,v106\n"
MF "a[48:51],v[24:27],v[80:83],a[48:51]\n"
"v_add_f32 v60,v60,v107\n"
CV "v105,v106,v107\n"
MF "a[68:71],v[28:31],v[80:83],a[68:71]\n"
"v_exp_f32 v108,v108\n"
MF "a[64:67],v[28:31],v[76:79],a[64:67]\n"
"v_exp_f32 v109,v109\n"
MF "a[44:47],v[24:27],v[76:79],a[44:47]\n"
"v_exp_f32 v110,v110\n"
MF "a[40:43],v[24:27],v[72:75],a[40:43]\n"
"v_exp_f32 v111,v111\n"
MF "a[60:63],v[28:31],v[72:75],a[60:63]\n"
"s_waitcnt lgkmcnt(0)\n"
TR "v[24:25],v4 offset:1024\n"
TR "v[26:27],v4 offset:7168\n"
MF "a[80:83],v[16:19],v[72:75],a[80:83]\n"
"s_nop 0\n"
TR "v[28:29],v5 offset:1024\n"
TR "v[30:31],v5 offset:7168\n"
MF "a[100:103],v[20:23],v[72:75],a[100:103]\n"
"v_add_f32 v61,v56,v108\n"
"v_add_f32 v61,v61,v109\n"
MF "a[104:107],v[20:23],v[76:79],a[104:107]\n"
CV "v108,v108,v109\n"
"v_add_f32 v61,v61,v110\n"
MF "a[84:87],v[16:19],v[76:79],a[84:87]\n"
"v_add_f32 v61,v61,v111\n"
CV "v109,v110,v111\n"
MF "a[88:91],v[16:19],v[80:83],a[88:91]\n"
"v_exp_f32 v112,v112\n"
MF "a[108:111],v[20:23],v[80:83],a[108:111]\n"
"v_exp_f32 v113,v113\n"
MF "a[112:115],v[20:23],v[84:87],a[112:115]\n"
"v_exp_f32 v114,v114\n"
MF "a[92:95],v[16:19],v[84:87],a[92:95]\n"
"v_exp_f32 v115,v115\n"
MF "a[96:99],v[16:19],v[88:91],a[96:99]\n"
"v_add_f32 v57,v57,v112\n"
"v_add_f32 v57,v57,v113\n"
MF "a[116:119],v[20:23],v[88:91],a[116:119]\n"
"s_waitcnt lgkmcnt(0)\n"
TR "v[16:17],v2 offset:2048\n"
TR "v[18:19],v2 offset:8192\n"
MF "a[136:139],v[24:27],v[88:91],a[136:139]\n"
"s_nop 0\n"
TR "v[20:21],v3 offset:2048\n"
TR "v[22:23],v3 offset:8192\n"
MF "a[156:159],v[28:31],v[88:91],a[156:159]\n"
CV "v94,v112,v113\n"
"v_add_f32 v57,v57,v114\n"
MF "a[152:155],v[28:31],v[84:87],a[152:155]\n"
"v_add_f32 v57,v57,v115\n"
CV "v95,v114,v115\n"
MF "a[132:135],v[24:27],v[84:87],a[132:135]\n"
"v_exp_f32 v116,v116\n"
MF "a[128:131],v[24:27],v[80:83],a[128:131]\n"
"v_exp_f32 v117,v117\n"
MF "a[148:151],v[28:31],v[80:83],a[148:151]\n"
"v_exp_f32 v118,v118\n"
MF "a[144:147],v[28:31],v[76:79],a[144:147]\n"
"v_exp_f32 v119,v119\n"
MF "a[124:127],v[24:27],v[76:79],a[124:127]\n"
"v_add_f32 v58,v58,v116\n"
"v_add_f32 v58,v58,v117\n"
MF "a[120:123],v[24:27],v[72:75],a[120:123]\n"
CV "v98,v116,v117\n"
"v_add_f32 v58,v58,v118\n"
MF "a[140:143],v[28:31],v[72:75],a[140:143]\n"
"s_waitcnt lgkmcnt(0)\n"
TR "v[24:25],v4 offset:2048\n"
TR "v[26:27],v4 offset:8192\n"
MF "a[160:163],v[16:19],v[72:75],a[160:163]\n"
"s_nop 0\n"
TR "v[28:29],v5 offset:2048\n"
TR "v[30:31],v5 offset:8192\n"
MF "a[180:183],v[20:23],v[72:75],a[180:183]\n"
"v_add_f32 v58,v58,v119\n"
CV "v99,v118,v119\n"
MF "a[184:187],v[20:23],v[76:79],a[184:187]\n"
"v_exp_f32 v120,v120\n"
MF "a[164:167],v[16:19],v[76:79],a[164:167]\n"
"v_exp_f32 v121,v121\n"
MF "a[168:171],v[16:19],v[80:83],a[168:171]\n"
"v_exp_f32 v122,v122\n"
MF "a[188:191],v[20:23],v[80:83],a[188:191]\n"
"v_exp_f32 v123,v123\n"
MF "a[192:195],v[20:23],v[84:87],a[192:195]\n"
"v_add_f32 v59,v59,v120\n"
"v_add_f32 v59,v59,v121\n"
MF "a[172:175],v[16:19],v[84:87],a[172:175]\n"
"s_waitcnt vmcnt(12) lgkmcnt(0)\n"
CV "v102,v120,v121\n"
"v_add_f32 v59,v59,v122\n"
MF "a[176:179],v[16:19],v[88:91],a[176:179]\n"
"s_barrier\n"
MF "a[196:199],v[20:23],v[88:91],a[196:199]\n"
"s_add_u32 s73,s73,12288\n"
"ds_read_b128 v[16:19],v0\n"
"ds_read_b128 v[20:23],v1\n"
MF "a[216:219],v[24:27],v[88:91],a[216:219]\n"
"s_cmp_eq_u32 s73,s19\n"
"s_cselect_b32 s73,s18,s73\n"
"s_add_u32 m0,s65,49152\n"
MF "a[236:239],v[28:31],v[88:91],a[236:239]\n"
LD "v8,s[36:39],s73 offen nt lds\n"
MF "a[232:235],v[28:31],v[84:87],a[232:235]\n"
"s_add_u32 m0,s65,50048\n"
"v_add_f32 v59,v59,v123\n"
CV "v103,v122,v123\n"
MF "a[212:215],v[24:27],v[84:87],a[212:215]\n"
LD "v8,s[36:39],s73 offen offset:128 nt lds\n"
MF "a[208:211],v[24:27],v[80:83],a[208:211]\n"
"s_add_u32 m0,s65,50944\n"
MF "a[228:231],v[28:31],v[80:83],a[228:231]\n"
LD "v8,s[36:39],s73 offen offset:256 nt lds\n"
MF "a[224:227],v[28:31],v[76:79],a[224:227]\n"
MF "a[204:207],v[24:27],v[76:79],a[204:207]\n"
"s_waitcnt lgkmcnt(0)\n"
MF "a[200:203],v[24:27],v[72:75],a[200:203]\n"
"s_cmp_lg_u32 s61,1\n"
MF "a[220:223],v[28:31],v[72:75],a[220:223]\n"
"s_cbranch_scc1 .Laloopx\n"
"s_cmp_eq_u32 s57,0\n"
"s_cbranch_scc0 .Lep\n"
".Lepx:\n"
MF "v[72:75],v[16:19],v[136:139],v[32:35]\n"
"s_add_u32 m0,s65,49152\n"
"ds_read_b128 v[24:27],v0 offset:1024\n"
"ds_read_b128 v[28:31],v1 offset:1024\n"
MF "v[72:75],v[20:23],v[156:159],v[72:75]\n"
LD "v8,s[36:39],s73 offen offset:3072 nt lds\n"
MF "v[76:79],v[20:23],v[160:163],v[36:39]\n"
"v_exp_f32 v124,v124\n"
MF "v[76:79],v[16:19],v[140:143],v[76:79]\n"
"v_exp_f32 v125,v125\n"
MF "v[80:83],v[16:19],v[144:147],v[40:43]\n"
"s_add_u32 m0,s65,50048\n"
"v_exp_f32 v126,v126\n"
MF "v[80:83],v[20:23],v[164:167],v[80:83]\n"
LD "v8,s[36:39],s73 offen offset:3200 nt lds\n"
MF "v[84:87],v[20:23],v[168:171],v[44:47]\n"
"v_exp_f32 v127,v127\n"
MF "v[84:87],v[16:19],v[148:151],v[84:87]\n"
"s_add_u32 m0,s65,50944\n"
"v_add_f32 v60,v60,v124\n"
"v_add_f32 v60,v60,v125\n"
MF "v[88:91],v[16:19],v[152:155],v[48:51]\n"
LD "v8,s[36:39],s73 offen offset:3328 nt lds\n"
MF "v[88:91],v[20:23],v[172:175],v[88:91]\n"
"s_waitcnt lgkmcnt(0)\n"
"ds_read_b128 v[16:19],v0 offset:2048\n"
"ds_read_b128 v[20:23],v1 offset:2048\n"
MF "v[88:91],v[24:27],v[192:195],v[88:91]\n"
CV "v106,v124,v125\n"
"v_add_f32 v60,v60,v126\n"
MF "v[88:91],v[28:31],v[212:215],v[88:91]\n"
"v_add_f32 v60,v60,v127\n"
CV "v107,v126,v127\n"
MF "v[84:87],v[28:31],v[208:211],v[84:87]\n"
"v_exp_f32 v128,v128\n"
MF "v[84:87],v[24:27],v[188:191],v[84:87]\n"
"v_exp_f32 v129,v129\n"
MF "v[80:83],v[24:27],v[184:187],v[80:83]\n"
"v_exp_f32 v130,v130\n"
MF "v[80:83],v[28:31],v[204:207],v[80:83]\n"
"v_exp_f32 v131,v131\n"
MF "v[76:79],v[28:31],v[200:203],v[76:79]\n"
"v_add_f32 v61,v61,v128\n"
"v_add_f32 v61,v61,v129\n"
MF "v[76:79],v[24:27],v[180:183],v[76:79]\n"
CV "v110,v128,v129\n"
"v_add_f32 v61,v61,v130\n"
MF "v[72:75],v[24:27],v[176:179],v[72:75]\n"
"v_add_f32 v61,v61,v131\n"
CV "v111,v130,v131\n"
MF "v[72:75],v[28:31],v[196:199],v[72:75]\n"
"s_waitcnt lgkmcnt(0)\n"
"ds_read_b128 v[24:27],v0 offset:6144\n"
"ds_read_b128 v[28:31],v1 offset:6144\n"
MF "v[72:75],v[16:19],v[216:219],v[72:75]\n"
MF "v[72:75],v[20:23],v[236:239],v[72:75]\n"
MF "v[76:79],v[20:23],v[240:243],v[76:79]\n"
MF "v[76:79],v[16:19],v[220:223],v[76:79]\n"
MF "v[80:83],v[16:19],v[224:227],v[80:83]\n"
MF "v[80:83],v[20:23],v[244:247],v[80:83]\n"
MF "v[84:87],v[20:23],v[248:251],v[84:87]\n"
MF "v[84:87],v[16:19],v[228:231],v[84:87]\n"
"v_max3_f32 v10,v57,v58,v59\n"
"v_max3_f32 v10,v10,v60,v61\n"
MF "v[88:91],v[16:19],v[232:235],v[88:91]\n"
"v_cmp_le_f32 vcc,0x47800000,v10\n"
MF "v[88:91],v[20:23],v[252:255],v[88:91]\n"
"s_waitcnt lgkmcnt(0)\n"
"s_cbranch_vccnz .Lstub_8\n"
".Lback_8:\n"
MF "v[112:115],v[24:27],v[136:139],v[32:35]\n"
"s_add_u32 s74,s74,12288\n"
"ds_read_b128 v[16:19],v0 offset:7168\n"
"ds_read_b128 v[20:23],v1 offset:7168\n"
MF "v[112:115],v[28:31],v[156:159],v[112:115]\n"
"s_cmp_eq_u32 s74,s20\n"
"s_cselect_b32 s74,s70,s74\n"
MF "v[116:119],v[28:31],v[160:163],v[36:39]\n"
"v_exp_f32 v72,v72\n"
MF "v[116:119],v[24:27],v[140:143],v[116:119]\n"
"v_exp_f32 v73,v73\n"
MF "v[120:123],v[24:27],v[144:147],v[40:43]\n"
"v_exp_f32 v74,v74\n"
MF "v[120:123],v[28:31],v[164:167],v[120:123]\n"
"v_exp_f32 v75,v75\n"
MF "v[124:127],v[28:31],v[168:171],v[44:47]\n"
"v_add_f32 v52,v57,v72\n"
"v_add_f32 v52,v52,v73\n"
MF "v[124:127],v[24:27],v[148:151],v[124:127]\n"
CV "v72,v72,v73\n"
"v_add_f32 v52,v52,v74\n"
MF "v[128:131],v[24:27],v[152:155],v[48:51]\n"
"v_add_f32 v52,v52,v75\n"
CV "v73,v74,v75\n"
MF "v[128:131],v[28:31],v[172:175],v[128:131]\n"
"s_waitcnt lgkmcnt(0)\n"
"ds_read_b128 v[24:27],v0 offset:8192\n"
"ds_read_b128 v[28:31],v1 offset:8192\n"
MF "v[128:131],v[16:19],v[192:195],v[128:131]\n"
"v_exp_f32 v76,v76\n"
MF "v[128:131],v[20:23],v[212:215],v[128:131]\n"
"v_exp_f32 v77,v77\n"
MF "v[124:127],v[20:23],v[208:211],v[124:127]\n"
"v_exp_f32 v78,v78\n"
MF "v[124:127],v[16:19],v[188:191],v[124:127]\n"
"v_exp_f32 v79,v79\n"
MF "v[120:123],v[16:19],v[184:187],v[120:123]\n"
"v_add_f32 v53,v58,v76\n"
"v_add_f32 v53,v53,v77\n"
MF "v[120:123],v[20:23],v[204:207],v[120:123]\n"
CV "v76,v76,v77\n"
"v_add_f32 v53,v53,v78\n"
MF "v[116:119],v[20:23],v[200:203],v[116:119]\n"
"s_waitcnt vmcnt(12) lgkmcnt(0)\n"
"v_add_f32 v53,v53,v79\n"
CV "v77,v78,v79\n"
"s_mov_b64 exec,0\n"
"s_cmp_eq_u32 s56,0\n"
"s_cbranch_scc1 .Lpg6f\n"
"s_cmp_eq_u32 s56,1\n"
"s_cbranch_scc1 .Lpg5f\n"
"s_cmp_eq_u32 s56,2\n"
"s_cbranch_scc1 .Lpg4f\n"
"s_branch .Lpgq0_f\n"
".Lisr3:\n"
"s_mov_b64 exec,-1\n"
".Lisx3:\n"
MF "v[116:119],v[16:19],v[180:183],v[116:119]\n"
"s_barrier\n"
MF "v[112:115],v[16:19],v[176:179],v[112:115]\n"
"v_exp_f32 v80,v80\n"
MF "v[112:115],v[20:23],v[196:199],v[112:115]\n"
"s_nop 0\n"
TR "v[16:17],v2 offset:24576\n"
TR "v[18:19],v2 offset:30720\n"
MF "v[112:115],v[24:27],v[216:219],v[112:115]\n"
"s_add_u32 s66,s67,4718592\n"
TR "v[20:21],v3 offset:24576\n"
TR "v[22:23],v3 offset:30720\n"
MF "v[112:115],v[28:31],v[236:239],v[112:115]\n"
"s_sub_u32 s81,s81,384\n"
"s_mov_b32 s83,7077888\n"
"s_add_u32 m0,s62,0\n"
MF "v[116:119],v[28:31],v[240:243],v[116:119]\n"
"s_add_u32 s82,s66,2359296\n"
"s_sub_u32 s15,s82,7864320\n"
"s_min_u32 s82,s82,s15\n"
MF "v[116:119],v[24:27],v[220:223],v[116:119]\n"
LD "v6,s[28:31],s82 offen lds\n"
MF "v[120:123],v[24:27],v[224:227],v[120:123]\n"
"s_add_u32 s82,s82,97280\n"
"v_exp_f32 v81,v81\n"
MF "v[120:123],v[28:31],v[244:247],v[120:123]\n"
LD "v6,s[28:31],s82 offen offset:1024 lds\n"
MF "v[124:127],v[28:31],v[248:251],v[124:127]\n"
"v_exp_f32 v82,v82\n"
MF "v[124:127],v[24:27],v[228:231],v[124:127]\n"
"v_exp_f32 v83,v83\n"
MF "v[128:131],v[24:27],v[232:235],v[128:131]\n"
"v_add_f32 v54,v59,v80\n"
"v_add_f32 v54,v54,v81\n"
MF "v[128:131],v[28:31],v[252:255],v[128:131]\n"
"s_waitcnt lgkmcnt(0)\n"
TR "v[24:25],v4 offset:24576\n"
TR "v[26:27],v4 offset:30720\n"
MF "a[0:3],v[16:19],v[92:95],a[0:3]\n"
"s_add_u32 s82,s82,97280\n"
TR "v[28:29],v5 offset:24576\n"
TR "v[30:31],v5 offset:30720\n"
MF "a[20:23],v[20:23],v[92:95],a[20:23]\n"
LD "v6,s[28:31],s82 offen offset:2048 lds\n"
MF "a[24:27],v[20:23],v[96:99],a[24:27]\n"
CV "v80,v80,v81\n"
"v_add_f32 v54,v54,v82\n"
MF "a[4:7],v[16:19],v[96:99],a[4:7]\n"
"v_add_f32 v54,v54,v83\n"
CV "v81,v82,v83\n"
MF "a[8:11],v[16:19],v[100:103],a[8:11]\n"
"s_add_u32 s82,s82,97280\n"
"v_exp_f32 v84,v84\n"
MF "a[28:31],v[20:23],v[100:103],a[28:31]\n"
LD "v6,s[28:31],s82 offen offset:3072 lds\n"
MF "a[32:35],v[20:23],v[104:107],a[32:35]\n"
"v_exp_f32 v85,v85\n"
MF "a[12:15],v[16:19],v[104:107],a[12:15]\n"
"v_exp_f32 v86,v86\n"
MF "a[16:19],v[16:19],v[108:111],a[16:19]\n"
"v_exp_f32 v87,v87\n"
MF "a[36:39],v[20:23],v[108:111],a[36:39]\n"
"s_waitcnt lgkmcnt(0)\n"
TR "v[16:17],v2 offset:25600\n"
TR "v[18:19],v2 offset:31744\n"
MF "a[56:59],v[24:27],v[108:111],a[56:59]\n"
"s_nop 0\n"
TR "v[20:21],v3 offset:25600\n"
TR "v[22:23],v3 offset:31744\n"
MF "a[76:79],v[28:31],v[108:111],a[76:79]\n"
"v_add_f32 v55,v60,v84\n"
"v_add_f32 v55,v55,v85\n"
MF "a[72:75],v[28:31],v[104:107],a[72:75]\n"
CV "v84,v84,v85\n"
"v_add_f32 v55,v55,v86\n"
MF "a[52:55],v[24:27],v[104:107],a[52:55]\n"
"v_add_f32 v55,v55,v87\n"
CV "v85,v86,v87\n"
MF "a[48:51],v[24:27],v[100:103],a[48:51]\n"
"v_exp_f32 v88,v88\n"
MF "a[68:71],v[28:31],v[100:103],a[68:71]\n"
"v_exp_f32 v89,v89\n"
MF "a[64:67],v[28:31],v[96:99],a[64:67]\n"
"v_exp_f32 v90,v90\n"
MF "a[44:47],v[24:27],v[96:99],a[44:47]\n"
"v_exp_f32 v91,v91\n"
MF "a[40:43],v[24:27],v[92:95],a[40:43]\n"
"v_add_f32 v56,v61,v88\n"
"v_add_f32 v56,v56,v89\n"
MF "a[60:63],v[28:31],v[92:95],a[60:63]\n"
"s_waitcnt lgkmcnt(0)\n"
TR "v[24:25],v4 offset:25600\n"
TR "v[26:27],v4 offset:31744\n"
MF "a[80:83],v[16:19],v[92:95],a[80:83]\n"
"s_nop 0\n"
TR "v[28:29],v5 offset:25600\n"
TR "v[30:31],v5 offset:31744\n"
MF "a[100:103],v[20:23],v[92:95],a[100:103]\n"
CV "v88,v88,v89\n"
"v_add_f32 v56,v56,v90\n"
MF "a[104:107],v[20:23],v[96:99],a[104:107]\n"
"v_add_f32 v56,v56,v91\n"
CV "v89,v90,v91\n"
MF "a[84:87],v[16:19],v[96:99],a[84:87]\n"
"v_exp_f32 v112,v112\n"
MF "a[88:91],v[16:19],v[100:103],a[88:91]\n"
"v_exp_f32 v113,v113\n"
MF "a[108:111],v[20:23],v[100:103],a[108:111]\n"
"v_exp_f32 v114,v114\n"
MF "a[112:115],v[20:23],v[104:107],a[112:115]\n"
"v_exp_f32 v115,v115\n"
MF "a[92:95],v[16:19],v[104:107],a[92:95]\n"
"v_add_f32 v52,v52,v112\n"
"v_add_f32 v52,v52,v113\n"
MF "a[96:99],v[16:19],v[108:111],a[96:99]\n"
CV "v74,v112,v113\n"
"v_add_f32 v52,v52,v114\n"
MF "a[116:119],v[20:23],v[108:111],a[116:119]\n"
"s_waitcnt lgkmcnt(0)\n"
TR "v[16:17],v2 offset:26624\n"
TR "v[18:19],v2 offset:32768\n"
MF "a[136:139],v[24:27],v[108:111],a[136:139]\n"
"s_nop 0\n"
TR "v[20:21],v3 offset:26624\n"
TR "v[22:23],v3 offset:32768\n"
MF "a[156:159],v[28:31],v[108:111],a[156:159]\n"
"v_add_f32 v52,v52,v115\n"
CV "v75,v114,v115\n"
MF "a[152:155],v[28:31],v[104:107],a[152:155]\n"
"v_exp_f32 v116,v116\n"
MF "a[132:135],v[24:27],v[104:107],a[132:135]\n"
"v_exp_f32 v117,v117\n"
MF "a[128:131],v[24:27],v[100:103],a[128:131]\n"
"v_exp_f32 v118,v118\n"
MF "a[148:151],v[28:31],v[100:103],a[148:151]\n"
"v_exp_f32 v119,v119\n"
MF "a[144:147],v[28:31],v[96:99],a[144:147]\n"
"v_add_f32 v53,v53,v116\n"
"v_add_f32 v53,v53,v117\n"
MF "a[124:127],v[24:27],v[96:99],a[124:127]\n"
CV "v78,v116,v117\n"
"v_add_f32 v53,v53,v118\n"
MF "a[120:123],v[24:27],v[92:95],a[120:123]\n"
"v_add_f32 v53,v53,v119\n"
CV "v79,v118,v119\n"
MF "a[140:143],v[28:31],v[92:95],a[140:143]\n"
"s_waitcnt lgkmcnt(0)\n"
TR "v[24:25],v4 offset:26624\n"
TR "v[26:27],v4 offset:32768\n"
MF "a[160:163],v[16:19],v[92:95],a[160:163]\n"
"s_nop 0\n"
TR "v[28:29],v5 offset:26624\n"
TR "v[30:31],v5 offset:32768\n"
MF "a[180:183],v[20:23],v[92:95],a[180:183]\n"
"v_exp_f32 v120,v120\n"
MF "a[184:187],v[20:23],v[96:99],a[184:187]\n"
"v_exp_f32 v121,v121\n"
MF "a[164:167],v[16:19],v[96:99],a[164:167]\n"
"v_exp_f32 v122,v122\n"
MF "a[168:171],v[16:19],v[100:103],a[168:171]\n"
"v_exp_f32 v123,v123\n"
MF "a[188:191],v[20:23],v[100:103],a[188:191]\n"
"v_add_f32 v54,v54,v120\n"
"v_add_f32 v54,v54,v121\n"
MF "a[192:195],v[20:23],v[104:107],a[192:195]\n"
CV "v82,v120,v121\n"
"v_add_f32 v54,v54,v122\n"
MF "a[172:175],v[16:19],v[104:107],a[172:175]\n"
"s_waitcnt vmcnt(10) lgkmcnt(0)\n"
"v_add_f32 v54,v54,v123\n"
CV "v83,v122,v123\n"
MF "a[176:179],v[16:19],v[108:111],a[176:179]\n"
"s_barrier\n"
MF "a[196:199],v[20:23],v[108:111],a[196:199]\n"
"s_add_u32 s73,s73,12288\n"
"ds_read_b128 v[16:19],v0 offset:24576\n"
"ds_read_b128 v[20:23],v1 offset:24576\n"
MF "a[216:219],v[24:27],v[108:111],a[216:219]\n"
"s_cmp_eq_u32 s73,s19\n"
"s_cselect_b32 s73,s18,s73\n"
"s_add_u32 m0,s65,73728\n"
MF "a[236:239],v[28:31],v[108:111],a[236:239]\n"
LD "v8,s[36:39],s73 offen nt lds\n"
MF "a[232:235],v[28:31],v[104:107],a[232:235]\n"
"s_add_u32 m0,s65,74624\n"
MF "a[212:215],v[24:27],v[104:107],a[212:215]\n"
LD "v8,s[36:39],s73 offen offset:128 nt lds\n"
MF "a[208:211],v[24:27],v[100:103],a[208:211]\n"
"s_add_u32 m0,s65,75520\n"
MF "a[228:231],v[28:31],v[100:103],a[228:231]\n"
LD "v8,s[36:39],s73 offen offset:256 nt lds\n"
MF "a[224:227],v[28:31],v[96:99],a[224:227]\n"
MF "a[204:207],v[24:27],v[96:99],a[204:207]\n"
MF "a[200:203],v[24:27],v[92:95],a[200:203]\n"
MF "a[220:223],v[28:31],v[92:95],a[220:223]\n"
"s_waitcnt lgkmcnt(0)\n"
MF "v[92:95],v[16:19],v[136:139],v[32:35]\n"
"s_add_u32 m0,s65,73728\n"
"ds_read_b128 v[24:27],v0 offset:25600\n"
"ds_read_b128 v[28:31],v1 offset:25600\n"
MF "v[92:95],v[20:23],v[156:159],v[92:95]\n"
LD "v8,s[36:39],s73 offen offset:3072 nt lds\n"
MF "v[96:99],v[20:23],v[160:163],v[36:39]\n"
"v_exp_f32 v124,v124\n"
MF "v[96:99],v[16:19],v[140:143],v[96:99]\n"
"v_exp_f32 v125,v125\n"
MF "v[100:103],v[16:19],v[144:147],v[40:43]\n"
"s_add_u32 m0,s65,74624\n"
"v_exp_f32 v126,v126\n"
MF "v[100:103],v[20:23],v[164:167],v[100:103]\n"
LD "v8,s[36:39],s73 offen offset:3200 nt lds\n"
MF "v[104:107],v[20:23],v[168:171],v[44:47]\n"
"v_exp_f32 v127,v127\n"
MF "v[104:107],v[16:19],v[148:151],v[104:107]\n"
"s_add_u32 m0,s65,75520\n"
"v_add_f32 v55,v55,v124\n"
"v_add_f32 v55,v55,v125\n"
MF "v[108:111],v[16:19],v[152:155],v[48:51]\n"
LD "v8,s[36:39],s73 offen offset:3328 nt lds\n"
MF "v[108:111],v[20:23],v[172:175],v[108:111]\n"
"s_waitcnt lgkmcnt(0)\n"
"ds_read_b128 v[16:19],v0 offset:26624\n"
"ds_read_b128 v[20:23],v1 offset:26624\n"
MF "v[108:111],v[24:27],v[192:195],v[108:111]\n"
CV "v86,v124,v125\n"
"v_add_f32 v55,v55,v126\n"
MF "v[108:111],v[28:31],v[212:215],v[108:111]\n"
"v_add_f32 v55,v55,v127\n"
CV "v87,v126,v127\n"
MF "v[104:107],v[28:31],v[208:211],v[104:107]\n"
"v_exp_f32 v128,v128\n"
MF "v[104:107],v[24:27],v[188:191],v[104:107]\n"
"v_exp_f32 v129,v129\n"
MF "v[100:103],v[24:27],v[184:187],v[100:103]\n"
"v_exp_f32 v130,v130\n"
MF "v[100:103],v[28:31],v[204:207],v[100:103]\n"
"v_exp_f32 v131,v131\n"
MF "v[96:99],v[28:31],v[200:203],v[96:99]\n"
"v_add_f32 v56,v56,v128\n"
"v_add_f32 v56,v56,v129\n"
MF "v[96:99],v[24:27],v[180:183],v[96:99]\n"
CV "v90,v128,v129\n"
"v_add_f32 v56,v56,v130\n"
MF "v[92:95],v[24:27],v[176:179],v[92:95]\n"
"v_add_f32 v56,v56,v131\n"
CV "v91,v130,v131\n"
MF "v[92:95],v[28:31],v[196:199],v[92:95]\n"
"s_waitcnt lgkmcnt(0)\n"
"ds_read_b128 v[24:27],v0 offset:30720\n"
"ds_read_b128 v[28:31],v1 offset:30720\n"
MF "v[92:95],v[16:19],v[216:219],v[92:95]\n"
MF "v[92:95],v[20:23],v[236:239],v[92:95]\n"
MF "v[96:99],v[20:23],v[240:243],v[96:99]\n"
MF "v[96:99],v[16:19],v[220:223],v[96:99]\n"
MF "v[100:103],v[16:19],v[224:227],v[100:103]\n"
MF "v[100:103],v[20:23],v[244:247],v[100:103]\n"
MF "v[104:107],v[20:23],v[248:251],v[104:107]\n"
MF "v[104:107],v[16:19],v[228:231],v[104:107]\n"
"v_max3_f32 v10,v52,v53,v54\n"
"v_max3_f32 v10,v10,v55,v56\n"
MF "v[108:111],v[16:19],v[232:235],v[108:111]\n"
"v_cmp_le_f32 vcc,0x47800000,v10\n"
MF "v[108:111],v[20:23],v[252:255],v[108:111]\n"
"s_waitcnt lgkmcnt(0)\n"
"s_cbranch_vccnz .Lstub_9\n"
".Lback_9:\n"
MF "v[112:115],v[24:27],v[136:139],v[32:35]\n"
"s_add_u32 s74,s74,12288\n"
"ds_read_b128 v[16:19],v0 offset:31744\n"
"ds_read_b128 v[20:23],v1 offset:31744\n"
MF "v[112:115],v[28:31],v[156:159],v[112:115]\n"
"s_cmp_eq_u32 s74,s20\n"
"s_cselect_b32 s74,s70,s74\n"
MF "v[116:119],v[28:31],v[160:163],v[36:39]\n"
"v_exp_f32 v92,v92\n"
MF "v[116:119],v[24:27],v[140:143],v[116:119]\n"
"v_exp_f32 v93,v93\n"
MF "v[120:123],v[24:27],v[144:147],v[40:43]\n"
"v_exp_f32 v94,v94\n"
MF "v[120:123],v[28:31],v[164:167],v[120:123]\n"
"v_exp_f32 v95,v95\n"
MF "v[124:127],v[28:31],v[168:171],v[44:47]\n"
"v_add_f32 v57,v52,v92\n"
"v_add_f32 v57,v57,v93\n"
MF "v[124:127],v[24:27],v[148:151],v[124:127]\n"
CV "v92,v92,v93\n"
"v_add_f32 v57,v57,v94\n"
MF "v[128:131],v[24:27],v[152:155],v[48:51]\n"
"v_add_f32 v57,v57,v95\n"
CV "v93,v94,v95\n"
MF "v[128:131],v[28:31],v[172:175],v[128:131]\n"
"s_waitcnt lgkmcnt(0)\n"
"ds_read_b128 v[24:27],v0 offset:32768\n"
"ds_read_b128 v[28:31],v1 offset:32768\n"
MF "v[128:131],v[16:19],v[192:195],v[128:131]\n"
"v_exp_f32 v96,v96\n"
MF "v[128:131],v[20:23],v[212:215],v[128:131]\n"
"v_exp_f32 v97,v97\n"
MF "v[124:127],v[20:23],v[208:211],v[124:127]\n"
"v_exp_f32 v98,v98\n"
MF "v[124:127],v[16:19],v[188:191],v[124:127]\n"
"v_exp_f32 v99,v99\n"
MF "v[120:123],v[16:19],v[184:187],v[120:123]\n"
"v_add_f32 v58,v53,v96\n"
"v_add_f32 v58,v58,v97\n"
MF "v[120:123],v[20:23],v[204:207],v[120:123]\n"
CV "v96,v96,v97\n"
"v_add_f32 v58,v58,v98\n"
MF "v[116:119],v[20:23],v[200:203],v[116:119]\n"
"s_waitcnt vmcnt(10) lgkmcnt(0)\n"
"v_add_f32 v58,v58,v99\n"
CV "v97,v98,v99\n"
MF "v[116:119],v[16:19],v[180:183],v[116:119]\n"
"s_barrier\n"
MF "v[112:115],v[16:19],v[176:179],v[112:115]\n"
"v_exp_f32 v100,v100\n"
MF "v[112:115],v[20:23],v[196:199],v[112:115]\n"
"s_nop 0\n"
TR "v[16:17],v2\n"
TR "v[18:19],v2 offset:6144\n"
MF "v[112:115],v[24:27],v[216:219],v[112:115]\n"
"s_add_u32 m0,s62,16384\n"
TR "v[20:21],v3\n"
TR "v[22:23],v3 offset:6144\n"
MF "v[112:115],v[28:31],v[236:239],v[112:115]\n"
"s_add_u32 s82,s66,2359424\n"
"s_sub_u32 s15,s82,7864320\n"
"s_min_u32 s82,s82,s15\n"
MF "v[116:119],v[28:31],v[240:243],v[116:119]\n"
LD "v6,s[28:31],s82 offen lds\n"
MF "v[116:119],v[24:27],v[220:223],v[116:119]\n"
"s_add_u32 s82,s82,97280\n"
"v_exp_f32 v101,v101\n"
MF "v[120:123],v[24:27],v[224:227],v[120:123]\n"
LD "v6,s[28:31],s82 offen offset:1024 lds\n"
MF "v[120:123],v[28:31],v[244:247],v[120:123]\n"
"v_exp_f32 v102,v102\n"
MF "v[124:127],v[28:31],v[248:251],v[124:127]\n"
"s_add_u32 s82,s82,97280\n"
"v_exp_f32 v103,v103\n"
MF "v[124:127],v[24:27],v[228:231],v[124:127]\n"
LD "v6,s[28:31],s82 offen offset:2048 lds\n"
MF "v[128:131],v[24:27],v[232:235],v[128:131]\n"
"v_add_f32 v59,v54,v100\n"
"v_add_f32 v59,v59,v101\n"
MF "v[128:131],v[28:31],v[252:255],v[128:131]\n"
"s_waitcnt lgkmcnt(0)\n"
TR "v[24:25],v4\n"
TR "v[26:27],v4 offset:6144\n"
MF "a[0:3],v[16:19],v[72:75],a[0:3]\n"
"s_add_u32 s82,s82,97280\n"
TR "v[28:29],v5\n"
TR "v[30:31],v5 offset:6144\n"
MF "a[20:23],v[20:23],v[72:75],a[20:23]\n"
LD "v6,s[28:31],s82 offen offset:3072 lds\n"
MF "a[24:27],v[20:23],v[76:79],a[24:27]\n"
"s_add_u32 m0,s62,32768\n"
CV "v100,v100,v101\n"
"v_add_f32 v59,v59,v102\n"
MF "a[4:7],v[16:19],v[76:79],a[4:7]\n"
"s_add_u32 s82,s66,2359552\n"
"s_sub_u32 s15,s82,7864320\n"
"s_min_u32 s82,s82,s15\n"
MF "a[8:11],v[16:19],v[80:83],a[8:11]\n"
".Lpg12f:\n"
"s_cbranch_execz .Lpg19f\n"
LD "v6,s[28:31],s82 offen lds\n"
MF "a[28:31],v[20:23],v[80:83],a[28:31]\n"
"v_add_f32 v59,v59,v103\n"
CV "v101,v102,v103\n"
MF "a[32:35],v[20:23],v[84:87],a[32:35]\n"
"s_add_u32 s82,s82,97280\n"
"v_exp_f32 v104,v104\n"
MF "a[12:15],v[16:19],v[84:87],a[12:15]\n"
LD "v6,s[28:31],s82 offen offset:1024 lds\n"
MF "a[16:19],v[16:19],v[88:91],a[16:19]\n"
"v_exp_f32 v105,v105\n"
MF "a[36:39],v[20:23],v[88:91],a[36:39]\n"
"s_waitcnt lgkmcnt(0)\n"
TR "v[16:17],v2 offset:1024\n"
TR "v[18:19],v2 offset:7168\n"
MF "a[56:59],v[24:27],v[88:91],a[56:59]\n"
"s_nop 0\n"
TR "v[20:21],v3 offset:1024\n"
TR "v[22:23],v3 offset:7168\n"
MF "a[76:79],v[28:31],v[88:91],a[76:79]\n"
"v_exp_f32 v106,v106\n"
MF "a[72:75],v[28:31],v[84:87],a[72:75]\n"
"v_exp_f32 v107,v107\n"
MF "a[52:55],v[24:27],v[84:87],a[52:55]\n"
"v_add_f32 v60,v55,v104\n"
"v_add_f32 v60,v60,v105\n"
MF "a[48:51],v[24:27],v[80:83],a[48:51]\n"
CV "v104,v104,v105\n"
"v_add_f32 v60,v60,v106\n"
MF "a[68:71],v[28:31],v[80:83],a[68:71]\n"
"v_add_f32 v60,v60,v107\n"
CV "v105,v106,v107\n"
MF "a[64:67],v[28:31],v[76:79],a[64:67]\n"
"v_exp_f32 v108,v108\n"
MF "a[44:47],v[24:27],v[76:79],a[44:47]\n"
"v_exp_f32 v109,v109\n"
MF "a[40:43],v[24:27],v[72:75],a[40:43]\n"
"v_exp_f32 v110,v110\n"
MF "a[60:63],v[28:31],v[72:75],a[60:63]\n"
"s_waitcnt lgkmcnt(0)\n"
TR "v[24:25],v4 offset:1024\n"
TR "v[26:27],v4 offset:7168\n"
MF "a[80:83],v[16:19],v[72:75],a[80:83]\n"
"s_nop 0\n"
TR "v[28:29],v5 offset:1024\n"
TR "v[30:31],v5 offset:7168\n"
MF "a[100:103],v[20:23],v[72:75],a[100:103]\n"
"v_exp_f32 v111,v111\n"
MF "a[104:107],v[20:23],v[76:79],a[104:107]\n"
"v_add_f32 v61,v56,v108\n"
"v_add_f32 v61,v61,v109\n"
MF "a[84:87],v[16:19],v[76:79],a[84:87]\n"
CV "v108,v108,v109\n"
"v_add_f32 v61,v61,v110\n"
MF "a[88:91],v[16:19],v[80:83],a[88:91]\n"
"v_add_f32 v61,v61,v111\n"
CV "v109,v110,v111\n"
MF "a[108:111],v[20:23],v[80:83],a[108:111]\n"
"v_exp_f32 v112,v112\n"
MF "a[112:115],v[20:23],v[84:87],a[112:115]\n"
"v_exp_f32 v113,v113\n"
MF "a[92:95],v[16:19],v[84:87],a[92:95]\n"
"v_exp_f32 v114,v114\n"
MF "a[96:99],v[16:19],v[88:91],a[96:99]\n"
"v_exp_f32 v115,v115\n"
MF "a[116:119],v[20:23],v[88:91],a[116:119]\n"
"s_waitcnt lgkmcnt(0)\n"
TR "v[16:17],v2 offset:2048\n"
TR "v[18:19],v2 offset:8192\n"
MF "a[136:139],v[24:27],v[88:91],a[136:139]\n"
"s_nop 0\n"
TR "v[20:21],v3 offset:2048\n"
TR "v[22:23],v3 offset:8192\n"
MF "a[156:159],v[28:31],v[88:91],a[156:159]\n"
"v_add_f32 v57,v57,v112\n"
"v_add_f32 v57,v57,v113\n"
MF "a[152:155],v[28:31],v[84:87],a[152:155]\n"
CV "v94,v112,v113\n"
"v_add_f32 v57,v57,v114\n"
MF "a[132:135],v[24:27],v[84:87],a[132:135]\n"
"v_add_f32 v57,v57,v115\n"
CV "v95,v114,v115\n"
MF "a[128:131],v[24:27],v[80:83],a[128:131]\n"
"v_exp_f32 v116,v116\n"
MF "a[148:151],v[28:31],v[80:83],a[148:151]\n"
"v_exp_f32 v117,v117\n"
MF "a[144:147],v[28:31],v[76:79],a[144:147]\n"
"v_exp_f32 v118,v118\n"
MF "a[124:127],v[24:27],v[76:79],a[124:127]\n"
"v_exp_f32 v119,v119\n"
MF "a[120:123],v[24:27],v[72:75],a[120:123]\n"
"v_add_f32 v58,v58,v116\n"
"v_add_f32 v58,v58,v117\n"
MF "a[140:143],v[28:31],v[72:75],a[140:143]\n"
"s_waitcnt lgkmcnt(0)\n"
TR "v[24:25],v4 offset:2048\n"
TR "v[26:27],v4 offset:8192\n"
MF "a[160:163],v[16:19],v[72:75],a[160:163]\n"
"s_nop 0\n"
TR "v[28:29],v5 offset:2048\n"
TR "v[30:31],v5 offset:8192\n"
MF "a[180:183],v[20:23],v[72:75],a[180:183]\n"
CV "v98,v116,v117\n"
"v_add_f32 v58,v58,v118\n"
MF "a[184:187],v[20:23],v[76:79],a[184:187]\n"
"v_add_f32 v58,v58,v119\n"
CV "v99,v118,v119\n"
MF "a[164:167],v[16:19],v[76:79],a[164:167]\n"
"v_exp_f32 v120,v120\n"
MF "a[168:171],v[16:19],v[80:83],a[168:171]\n"
"v_exp_f32 v121,v121\n"
MF "a[188:191],v[20:23],v[80:83],a[188:191]\n"
"v_exp_f32 v122,v122\n"
MF "a[192:195],v[20:23],v[84:87],a[192:195]\n"
"v_exp_f32 v123,v123\n"
MF "a[172:175],v[16:19],v[84:87],a[172:175]\n"
"v_add_f32 v59,v59,v120\n"
"v_add_f32 v59,v59,v121\n"
MF "a[176:179],v[16:19],v[88:91],a[176:179]\n"
"s_waitcnt lgkmcnt(0)\n"
CV "v102,v120,v121\n"
"v_add_f32 v59,v59,v122\n"
MF "a[196:199],v[20:23],v[88:91],a[196:199]\n"
"s_add_u32 s82,s82,97280\n"
"v_add_f32 v59,v59,v123\n"
CV "v103,v122,v123\n"
MF "a[216:219],v[24:27],v[88:91],a[216:219]\n"
LD "v6,s[28:31],s82 offen offset:2048 lds\n"
MF "a[236:239],v[28:31],v[88:91],a[236:239]\n"
"v_exp_f32 v124,v124\n"
MF "a[232:235],v[28:31],v[84:87],a[232:235]\n"
"s_add_u32 s82,s82,97280\n"
"v_exp_f32 v125,v125\n"
MF "a[212:215],v[24:27],v[84:87],a[212:215]\n"
LD "v6,s[28:31],s82 offen offset:3072 lds\n"
MF "a[208:211],v[24:27],v[80:83],a[208:211]\n"
"v_exp_f32 v126,v126\n"
MF "a[228:231],v[28:31],v[80:83],a[228:231]\n"
"v_exp_f32 v127,v127\n"
MF "a[224:227],v[28:31],v[76:79],a[224:227]\n"
"v_add_f32 v60,v60,v124\n"
"v_add_f32 v60,v60,v125\n"
MF "a[204:207],v[24:27],v[76:79],a[204:207]\n"
CV "v106,v124,v125\n"
"v_add_f32 v60,v60,v126\n"
MF "a[200:203],v[24:27],v[72:75],a[200:203]\n"
"v_add_f32 v60,v60,v127\n"
CV "v107,v126,v127\n"
MF "a[220:223],v[28:31],v[72:75],a[220:223]\n"
"v_exp_f32 v128,v128\n"
"v_exp_f32 v129,v129\n"
"v_exp_f32 v130,v130\n"
"v_exp_f32 v131,v131\n"
"v_add_f32 v61,v61,v128\n"
"v_add_f32 v61,v61,v129\n"
CV "v110,v128,v129\n"
"v_add_f32 v61,v61,v130\n"
"v_add_f32_e64 v61,v61,v131\n"
CV "v111,v130,v131\n"
"v_max3_f32 v10,v57,v58,v59\n"
"v_max3_f32 v10,v10,v60,v61\n"
"v_cmp_le_f32 vcc,0x47800000,v10\n"
"s_cbranch_vccnz .Lstub_10\n"
".Lback_10:\n"
"s_waitcnt vmcnt(8) lgkmcnt(0)\n"
"s_barrier\n"
"v_add_u32 v11,97280,v6\n"
"v_add_u32 v12,194560,v6\n"
"v_add_u32 v13,291840,v6\n"
"v_mov_b32 v10,1.0\n"
TR "v[136:137],v2 offset:24576\n"
TR "v[138:139],v2 offset:30720\n"
"v_mfma_f32_16x16x4_f32 v[16:19],v10,v57,0\n"
TR "v[140:141],v3 offset:24576\n"
TR "v[142:143],v3 offset:30720\n"
TR "v[144:145],v4 offset:24576\n"
TR "v[146:147],v4 offset:30720\n"
TR "v[148:149],v5 offset:24576\n"
TR "v[150:151],v5 offset:30720\n"
"v_mfma_f32_16x16x4_f32 v[20:23],v10,v58,0\n"
TR "v[152:153],v2 offset:25600\n"
TR "v[154:155],v2 offset:31744\n"
TR "v[156:157],v3 offset:25600\n"
TR "v[158:159],v3 offset:31744\n"
TR "v[160:161],v4 offset:25600\n"
TR "v[162:163],v4 offset:31744\n"
"v_mfma_f32_16x16x4_f32 v[24:27],v10,v59,0\n"
TR "v[164:165],v5 offset:25600\n"
TR "v[166:167],v5 offset:31744\n"
TR "v[168:169],v2 offset:26624\n"
TR "v[170:171],v2 offset:32768\n"
TR "v[172:173],v3 offset:26624\n"
TR "v[174:175],v3 offset:32768\n"
"v_mfma_f32_16x16x4_f32 v[28:31],v10,v60,0\n"
TR "v[176:177],v4 offset:26624\n"
TR "v[178:179],v4 offset:32768\n"
TR "v[180:181],v5 offset:26624\n"
TR "v[182:183],v5 offset:32768\n"
"v_mfma_f32_16x16x4_f32 v[32:35],v10,v61,0\n"
"s_waitcnt lgkmcnt(15)\n"
MF "a[0:3],v[136:139],v[92:95],a[0:3]\n"
MF "a[20:23],v[140:143],v[92:95],a[20:23]\n"
"v_rcp_f32 v57,v16\n"
"v_rcp_f32 v58,v20\n"
"v_rcp_f32 v59,v24\n"
"v_rcp_f32 v60,v28\n"
"s_nop 2\n"
"v_rcp_f32_e64 v61,v32\n"
MF "a[4:7],v[136:139],v[96:99],a[4:7]\n"
"v_mul_f32_e64 v52,-s13,v57\n"
"v_mul_f32_e64 v53,-s13,v58\n"
"v_mul_f32_e64 v54,-s13,v59\n"
"v_mul_f32_e64 v55,-s13,v60\n"
"v_mul_f32_e64 v56,-s13,v61\n"
AR "v72,a0\n"
AR "v73,a1\n"
AR "v74,a2\n"
MF "a[24:27],v[140:143],v[96:99],a[24:27]\n"
AR "v75,a3\n"
AR "v76,a20\n"
AR "v77,a21\n"
AR "v78,a22\n"
AR "v79,a23\n"
"v_pk_fma_f32 v[72:73],v[72:73],v[56:57],v[52:53] op_sel:[0,1,0] op_sel_hi:[1,1,0]\n"
"v_pk_fma_f32 v[74:75],v[74:75],v[56:57],v[52:53] op_sel:[0,1,0] op_sel_hi:[1,1,0]\n"
"v_pk_fma_f32 v[76:77],v[76:77],v[56:57],v[52:53] op_sel:[0,1,0] op_sel_hi:[1,1,0]\n"
"v_pk_fma_f32 v[78:79],v[78:79],v[56:57],v[52:53] op_sel:[0,1,0] op_sel_hi:[1,1,0]\n"
CV "v112,v72,v73\n"
CV "v113,v74,v75\n"
CV "v114,v76,v77\n"
CV "v115,v78,v79\n"
MF "a[8:11],v[136:139],v[100:103],a[8:11]\n"
AW "a0,v112\n"
AW "a1,v113\n"
AW "a2,v114\n"
AW "a3,v115\n"
AR "v80,a4\n"
AR "v81,a5\n"
AR "v82,a6\n"
MF "a[28:31],v[140:143],v[100:103],a[28:31]\n"
AR "v83,a7\n"
AR "v84,a24\n"
AR "v85,a25\n"
AR "v86,a26\n"
AR "v87,a27\n"
"v_pk_fma_f32 v[80:81],v[80:81],v[58:59],v[52:53] op_sel:[0,0,1] op_sel_hi:[1,0,1]\n"
"v_pk_fma_f32 v[82:83],v[82:83],v[58:59],v[52:53] op_sel:[0,0,1] op_sel_hi:[1,0,1]\n"
"v_pk_fma_f32 v[84:85],v[84:85],v[58:59],v[52:53] op_sel:[0,0,1] op_sel_hi:[1,0,1]\n"
"v_pk_fma_f32 v[86:87],v[86:87],v[58:59],v[52:53] op_sel:[0,0,1] op_sel_hi:[1,0,1]\n"
CV "v116,v80,v81\n"
CV "v117,v82,v83\n"
CV "v118,v84,v85\n"
CV "v119,v86,v87\n"
MF "a[12:15],v[136:139],v[104:107],a[12:15]\n"
AW "a4,v116\n"
AW "a5,v117\n"
AW "a6,v118\n"
AW "a7,v119\n"
AR "v72,a8\n"
AR "v73,a9\n"
AR "v74,a10\n"
MF "a[32:35],v[140:143],v[104:107],a[32:35]\n"
AR "v75,a11\n"
AR "v76,a28\n"
AR "v77,a29\n"
AR "v78,a30\n"
AR "v79,a31\n"
"v_pk_fma_f32 v[72:73],v[72:73],v[58:59],v[54:55] op_sel:[0,1,0] op_sel_hi:[1,1,0]\n"
"v_pk_fma_f32 v[74:75],v[74:75],v[58:59],v[54:55] op_sel:[0,1,0] op_sel_hi:[1,1,0]\n"
"v_pk_fma_f32 v[76:77],v[76:77],v[58:59],v[54:55] op_sel:[0,1,0] op_sel_hi:[1,1,0]\n"
"v_pk_fma_f32 v[78:79],v[78:79],v[58:59],v[54:55] op_sel:[0,1,0] op_sel_hi:[1,1,0]\n"
CV "v112,v72,v73\n"
CV "v113,v74,v75\n"
CV "v114,v76,v77\n"
CV "v115,v78,v79\n"
MF "a[16:19],v[136:139],v[108:111],a[16:19]\n"
AW "a8,v112\n"
AW "a9,v113\n"
AW "a10,v114\n"
AW "a11,v115\n"
AR "v80,a12\n"
AR "v81,a13\n"
AR "v82,a14\n"
MF "a[36:39],v[140:143],v[108:111],a[36:39]\n"
AR "v83,a15\n"
AR "v84,a32\n"
AR "v85,a33\n"
AR "v86,a34\n"
AR "v87,a35\n"
"v_pk_fma_f32 v[80:81],v[80:81],v[60:61],v[54:55] op_sel:[0,0,1] op_sel_hi:[1,0,1]\n"
"v_pk_fma_f32 v[82:83],v[82:83],v[60:61],v[54:55] op_sel:[0,0,1] op_sel_hi:[1,0,1]\n"
"v_pk_fma_f32 v[84:85],v[84:85],v[60:61],v[54:55] op_sel:[0,0,1] op_sel_hi:[1,0,1]\n"
"v_pk_fma_f32 v[86:87],v[86:87],v[60:61],v[54:55] op_sel:[0,0,1] op_sel_hi:[1,0,1]\n"
CV "v116,v80,v81\n"
CV "v117,v82,v83\n"
CV "v118,v84,v85\n"
CV "v119,v86,v87\n"
MF "a[40:43],v[144:147],v[92:95],a[40:43]\n"
AW "a12,v116\n"
AW "a13,v117\n"
AW "a14,v118\n"
AW "a15,v119\n"
AR "v72,a16\n"
AR "v73,a17\n"
AR "v74,a18\n"
MF "a[60:63],v[148:151],v[92:95],a[60:63]\n"
AR "v75,a19\n"
AR "v76,a36\n"
AR "v77,a37\n"
AR "v78,a38\n"
AR "v79,a39\n"
"v_pk_fma_f32 v[72:73],v[72:73],v[60:61],v[56:57] op_sel:[0,1,0] op_sel_hi:[1,1,0]\n"
"v_pk_fma_f32 v[74:75],v[74:75],v[60:61],v[56:57] op_sel:[0,1,0] op_sel_hi:[1,1,0]\n"
"v_pk_fma_f32 v[76:77],v[76:77],v[60:61],v[56:57] op_sel:[0,1,0] op_sel_hi:[1,1,0]\n"
"v_pk_fma_f32 v[78:79],v[78:79],v[60:61],v[56:57] op_sel:[0,1,0] op_sel_hi:[1,1,0]\n"
CV "v112,v72,v73\n"
CV "v113,v74,v75\n"
CV "v114,v76,v77\n"
CV "v115,v78,v79\n"
MF "a[44:47],v[144:147],v[96:99],a[44:47]\n"
AW "a16,v112\n"
AW "a17,v113\n"
AW "a18,v114\n"
AW "a19,v115\n"
AR "v80,a40\n"
AR "v81,a41\n"
AR "v82,a42\n"
MF "a[64:67],v[148:151],v[96:99],a[64:67]\n"
AR "v83,a43\n"
AR "v84,a60\n"
AR "v85,a61\n"
AR "v86,a62\n"
AR "v87,a63\n"
"v_pk_fma_f32 v[80:81],v[80:81],v[56:57],v[52:53] op_sel:[0,1,0] op_sel_hi:[1,1,0]\n"
"v_pk_fma_f32 v[82:83],v[82:83],v[56:57],v[52:53] op_sel:[0,1,0] op_sel_hi:[1,1,0]\n"
"v_pk_fma_f32 v[84:85],v[84:85],v[56:57],v[52:53] op_sel:[0,1,0] op_sel_hi:[1,1,0]\n"
"v_pk_fma_f32 v[86:87],v[86:87],v[56:57],v[52:53] op_sel:[0,1,0] op_sel_hi:[1,1,0]\n"
CV "v116,v80,v81\n"
CV "v117,v82,v83\n"
CV "v118,v84,v85\n"
CV "v119,v86,v87\n"
MF "a[48:51],v[144:147],v[100:103],a[48:51]\n"
AW "a40,v116\n"
AW "a41,v117\n"
AW "a42,v118\n"
AW "a43,v119\n"
AR "v72,a44\n"
AR "v73,a45\n"
AR "v74,a46\n"
MF "a[68:71],v[148:151],v[100:103],a[68:71]\n"
AR "v75,a47\n"
AR "v76,a64\n"
AR "v77,a65\n"
AR "v78,a66\n"
AR "v79,a67\n"
"v_pk_fma_f32 v[72:73],v[72:73],v[58:59],v[52:53] op_sel:[0,0,1] op_sel_hi:[1,0,1]\n"
"v_pk_fma_f32 v[74:75],v[74:75],v[58:59],v[52:53] op_sel:[0,0,1] op_sel_hi:[1,0,1]\n"
"v_pk_fma_f32 v[76:77],v[76:77],v[58:59],v[52:53] op_sel:[0,0,1] op_sel_hi:[1,0,1]\n"
"v_pk_fma_f32 v[78:79],v[78:79],v[58:59],v[52:53] op_sel:[0,0,1] op_sel_hi:[1,0,1]\n"
CV "v112,v72,v73\n"
CV "v113,v74,v75\n"
CV "v114,v76,v77\n"
CV "v115,v78,v79\n"
MF "a[52:55],v[144:147],v[104:107],a[52:55]\n"
AW "a44,v112\n"
AW "a45,v113\n"
AW "a46,v114\n"
AW "a47,v115\n"
AR "v80,a48\n"
AR "v81,a49\n"
AR "v82,a50\n"
MF "a[72:75],v[148:151],v[104:107],a[72:75]\n"
AR "v83,a51\n"
AR "v84,a68\n"
AR "v85,a69\n"
AR "v86,a70\n"
AR "v87,a71\n"
"v_pk_fma_f32 v[80:81],v[80:81],v[58:59],v[54:55] op_sel:[0,1,0] op_sel_hi:[1,1,0]\n"
"v_pk_fma_f32 v[82:83],v[82:83],v[58:59],v[54:55] op_sel:[0,1,0] op_sel_hi:[1,1,0]\n"
"v_pk_fma_f32 v[84:85],v[84:85],v[58:59],v[54:55] op_sel:[0,1,0] op_sel_hi:[1,1,0]\n"
"v_pk_fma_f32 v[86:87],v[86:87],v[58:59],v[54:55] op_sel:[0,1,0] op_sel_hi:[1,1,0]\n"
CV "v116,v80,v81\n"
CV "v117,v82,v83\n"
CV "v118,v84,v85\n"
CV "v119,v86,v87\n"
MF "a[56:59],v[144:147],v[108:111],a[56:59]\n"
AW "a48,v116\n"
AW "a49,v117\n"
AW "a50,v118\n"
AW "a51,v119\n"
AR "v72,a52\n"
AR "v73,a53\n"
AR "v74,a54\n"
MF "a[76:79],v[148:151],v[108:111],a[76:79]\n"
AR "v75,a55\n"
AR "v76,a72\n"
AR "v77,a73\n"
AR "v78,a74\n"
AR "v79,a75\n"
"v_pk_fma_f32 v[72:73],v[72:73],v[60:61],v[54:55] op_sel:[0,0,1] op_sel_hi:[1,0,1]\n"
"v_pk_fma_f32 v[74:75],v[74:75],v[60:61],v[54:55] op_sel:[0,0,1] op_sel_hi:[1,0,1]\n"
"v_pk_fma_f32 v[76:77],v[76:77],v[60:61],v[54:55] op_sel:[0,0,1] op_sel_hi:[1,0,1]\n"
"v_pk_fma_f32 v[78:79],v[78:79],v[60:61],v[54:55] op_sel:[0,0,1] op_sel_hi:[1,0,1]\n"
CV "v112,v72,v73\n"
CV "v113,v74,v75\n"
CV "v114,v76,v77\n"
CV "v115,v78,v79\n"
"s_waitcnt lgkmcnt(14)\n"
MF "a[80:83],v[152:155],v[92:95],a[80:83]\n"
AW "a52,v112\n"
AW "a53,v113\n"
AW "a54,v114\n"
AW "a55,v115\n"
AR "v80,a56\n"
AR "v81,a57\n"
AR "v82,a58\n"
"s_waitcnt lgkmcnt(12)\n"
MF "a[100:103],v[156:159],v[92:95],a[100:103]\n"
AR "v83,a59\n"
AR "v84,a76\n"
AR "v85,a77\n"
AR "v86,a78\n"
AR "v87,a79\n"
"v_pk_fma_f32 v[80:81],v[80:81],v[60:61],v[56:57] op_sel:[0,1,0] op_sel_hi:[1,1,0]\n"
"v_pk_fma_f32 v[82:83],v[82:83],v[60:61],v[56:57] op_sel:[0,1,0] op_sel_hi:[1,1,0]\n"
"v_pk_fma_f32 v[84:85],v[84:85],v[60:61],v[56:57] op_sel:[0,1,0] op_sel_hi:[1,1,0]\n"
"v_pk_fma_f32 v[86:87],v[86:87],v[60:61],v[56:57] op_sel:[0,1,0] op_sel_hi:[1,1,0]\n"
CV "v116,v80,v81\n"
CV "v117,v82,v83\n"
CV "v118,v84,v85\n"
CV "v119,v86,v87\n"
MF "a[84:87],v[152:155],v[96:99],a[84:87]\n"
AW "a56,v116\n"
AW "a57,v117\n"
AW "a58,v118\n"
AW "a59,v119\n"
AR "v72,a80\n"
AR "v73,a81\n"
AR "v74,a82\n"
MF "a[104:107],v[156:159],v[96:99],a[104:107]\n"
AR "v75,a83\n"
AR "v76,a100\n"
AR "v77,a101\n"
AR "v78,a102\n"
AR "v79,a103\n"
"v_pk_fma_f32 v[72:73],v[72:73],v[56:57],v[52:53] op_sel:[0,1,0] op_sel_hi:[1,1,0]\n"
"v_pk_fma_f32 v[74:75],v[74:75],v[56:57],v[52:53] op_sel:[0,1,0] op_sel_hi:[1,1,0]\n"
"v_pk_fma_f32 v[76:77],v[76:77],v[56:57],v[52:53] op_sel:[0,1,0] op_sel_hi:[1,1,0]\n"
"v_pk_fma_f32 v[78:79],v[78:79],v[56:57],v[52:53] op_sel:[0,1,0] op_sel_hi:[1,1,0]\n"
CV "v112,v72,v73\n"
CV "v113,v74,v75\n"
CV "v114,v76,v77\n"
CV "v115,v78,v79\n"
MF "a[88:91],v[152:155],v[100:103],a[88:91]\n"
AW "a80,v112\n"
AW "a81,v113\n"
AW "a82,v114\n"
AW "a83,v115\n"
AR "v80,a84\n"
AR "v81,a85\n"
AR "v82,a86\n"
MF "a[108:111],v[156:159],v[100:103],a[108:111]\n"
AR "v83,a87\n"
AR "v84,a104\n"
AR "v85,a105\n"
AR "v86,a106\n"
AR "v87,a107\n"
"v_pk_fma_f32 v[80:81],v[80:81],v[58:59],v[52:53] op_sel:[0,0,1] op_sel_hi:[1,0,1]\n"
"v_pk_fma_f32 v[82:83],v[82:83],v[58:59],v[52:53] op_sel:[0,0,1] op_sel_hi:[1,0,1]\n"
"v_pk_fma_f32 v[84:85],v[84:85],v[58:59],v[52:53] op_sel:[0,0,1] op_sel_hi:[1,0,1]\n"
"v_pk_fma_f32 v[86:87],v[86:87],v[58:59],v[52:53] op_sel:[0,0,1] op_sel_hi:[1,0,1]\n"
CV "v116,v80,v81\n"
CV "v117,v82,v83\n"
CV "v118,v84,v85\n"
CV "v119,v86,v87\n"
MF "a[92:95],v[152:155],v[104:107],a[92:95]\n"
AW "a84,v116\n"
AW "a85,v117\n"
AW "a86,v118\n"
AW "a87,v119\n"
AR "v72,a88\n"
AR "v73,a89\n"
AR "v74,a90\n"
MF "a[112:115],v[156:159],v[104:107],a[112:115]\n"
AR "v75,a91\n"
AR "v76,a108\n"
AR "v77,a109\n"
AR "v78,a110\n"
AR "v79,a111\n"
"v_pk_fma_f32 v[72:73],v[72:73],v[58:59],v[54:55] op_sel:[0,1,0] op_sel_hi:[1,1,0]\n"
"v_pk_fma_f32 v[74:75],v[74:75],v[58:59],v[54:55] op_sel:[0,1,0] op_sel_hi:[1,1,0]\n"
"v_pk_fma_f32 v[76:77],v[76:77],v[58:59],v[54:55] op_sel:[0,1,0] op_sel_hi:[1,1,0]\n"
"v_pk_fma_f32 v[78:79],v[78:79],v[58:59],v[54:55] op_sel:[0,1,0] op_sel_hi:[1,1,0]\n"
CV "v112,v72,v73\n"
CV "v113,v74,v75\n"
CV "v114,v76,v77\n"
CV "v115,v78,v79\n"
MF "a[96:99],v[152:155],v[108:111],a[96:99]\n"
AW "a88,v112\n"
AW "a89,v113\n"
AW "a90,v114\n"
AW "a91,v115\n"
AR "v80,a92\n"
AR "v81,a93\n"
AR "v82,a94\n"
MF "a[116:119],v[156:159],v[108:111],a[116:119]\n"
AR "v83,a95\n"
AR "v84,a112\n"
AR "v85,a113\n"
AR "v86,a114\n"
AR "v87,a115\n"
"v_pk_fma_f32 v[80:81],v[80:81],v[60:61],v[54:55] op_sel:[0,0,1] op_sel_hi:[1,0,1]\n"
"v_pk_fma_f32 v[82:83],v[82:83],v[60:61],v[54:55] op_sel:[0,0,1] op_sel_hi:[1,0,1]\n"
"v_pk_fma_f32 v[84:85],v[84:85],v[60:61],v[54:55] op_sel:[0,0,1] op_sel_hi:[1,0,1]\n"
"v_pk_fma_f32 v[86:87],v[86:87],v[60:61],v[54:55] op_sel:[0,0,1] op_sel_hi:[1,0,1]\n"
CV "v116,v80,v81\n"
CV "v117,v82,v83\n"
CV "v118,v84,v85\n"
CV "v119,v86,v87\n"
"s_waitcnt lgkmcnt(10)\n"
MF "a[120:123],v[160:163],v[92:95],a[120:123]\n"
AW "a92,v116\n"
AW "a93,v117\n"
AW "a94,v118\n"
AW "a95,v119\n"
AR "v72,a96\n"
AR "v73,a97\n"
AR "v74,a98\n"
"s_waitcnt lgkmcnt(8)\n"
MF "a[140:143],v[164:167],v[92:95],a[140:143]\n"
AR "v75,a99\n"
AR "v76,a116\n"
AR "v77,a117\n"
AR "v78,a118\n"
AR "v79,a119\n"
"v_pk_fma_f32 v[72:73],v[72:73],v[60:61],v[56:57] op_sel:[0,1,0] op_sel_hi:[1,1,0]\n"
"v_pk_fma_f32 v[74:75],v[74:75],v[60:61],v[56:57] op_sel:[0,1,0] op_sel_hi:[1,1,0]\n"
"v_pk_fma_f32 v[76:77],v[76:77],v[60:61],v[56:57] op_sel:[0,1,0] op_sel_hi:[1,1,0]\n"
"v_pk_fma_f32 v[78:79],v[78:79],v[60:61],v[56:57] op_sel:[0,1,0] op_sel_hi:[1,1,0]\n"
CV "v112,v72,v73\n"
CV "v113,v74,v75\n"
CV "v114,v76,v77\n"
CV "v115,v78,v79\n"
MF "a[124:127],v[160:163],v[96:99],a[124:127]\n"
AW "a96,v112\n"
AW "a97,v113\n"
AW "a98,v114\n"
AW "a99,v115\n"
AR "v0,a240\n"
AR "v2,a241\n"
".Lpg13f:\n"
"s_cbranch_execz .Lisr3\n"
"s_nop 0\n"
AR "v4,a242\n"
"v_xor_b32 v1,64,v0\n"
"v_xor_b32 v3,64,v2\n"
"v_xor_b32_e64 v5,64,v4\n"
AR "v80,a120\n"
AR "v81,a121\n"
AR "v82,a122\n"
MF "a[144:147],v[164:167],v[96:99],a[144:147]\n"
AR "v83,a123\n"
AR "v84,a140\n"
AR "v85,a141\n"
AR "v86,a142\n"
AR "v87,a143\n"
"v_pk_fma_f32 v[80:81],v[80:81],v[56:57],v[52:53] op_sel:[0,1,0] op_sel_hi:[1,1,0]\n"
"v_pk_fma_f32 v[82:83],v[82:83],v[56:57],v[52:53] op_sel:[0,1,0] op_sel_hi:[1,1,0]\n"
"v_pk_fma_f32 v[84:85],v[84:85],v[56:57],v[52:53] op_sel:[0,1,0] op_sel_hi:[1,1,0]\n"
"v_pk_fma_f32 v[86:87],v[86:87],v[56:57],v[52:53] op_sel:[0,1,0] op_sel_hi:[1,1,0]\n"
CV "v116,v80,v81\n"
CV "v117,v82,v83\n"
CV "v118,v84,v85\n"
CV "v119,v86,v87\n"
MF "a[128:131],v[160:163],v[100:103],a[128:131]\n"
AW "a120,v116\n"
AW "a121,v117\n"
AW "a122,v118\n"
AW "a123,v119\n"
AR "v72,a124\n"
AR "v73,a125\n"
AR "v74,a126\n"
MF "a[148:151],v[164:167],v[100:103],a[148:151]\n"
AR "v75,a127\n"
AR "v76,a144\n"
AR "v77,a145\n"
AR "v78,a146\n"
AR "v79,a147\n"
"v_pk_fma_f32 v[72:73],v[72:73],v[58:59],v[52:53] op_sel:[0,0,1] op_sel_hi:[1,0,1]\n"
"v_pk_fma_f32 v[74:75],v[74:75],v[58:59],v[52:53] op_sel:[0,0,1] op_sel_hi:[1,0,1]\n"
"v_pk_fma_f32 v[76:77],v[76:77],v[58:59],v[52:53] op_sel:[0,0,1] op_sel_hi:[1,0,1]\n"
"v_pk_fma_f32 v[78:79],v[78:79],v[58:59],v[52:53] op_sel:[0,0,1] op_sel_hi:[1,0,1]\n"
CV "v112,v72,v73\n"
CV "v113,v74,v75\n"
CV "v114,v76,v77\n"
CV "v115,v78,v79\n"
MF "a[132:135],v[160:163],v[104:107],a[132:135]\n"
"ds_read_b128 a[60:63],v2\n"
AW "a124,v112\n"
AW "a125,v113\n"
AW "a126,v114\n"
AW "a127,v115\n"
AR "v80,a128\n"
AR "v81,a129\n"
AR "v82,a130\n"
MF "a[152:155],v[164:167],v[104:107],a[152:155]\n"
AR "v83,a131\n"
AR "v84,a148\n"
AR "v85,a149\n"
AR "v86,a150\n"
AR "v87,a151\n"
"v_pk_fma_f32 v[80:81],v[80:81],v[58:59],v[54:55] op_sel:[0,1,0] op_sel_hi:[1,1,0]\n"
"v_pk_fma_f32 v[82:83],v[82:83],v[58:59],v[54:55] op_sel:[0,1,0] op_sel_hi:[1,1,0]\n"
"v_pk_fma_f32 v[84:85],v[84:85],v[58:59],v[54:55] op_sel:[0,1,0] op_sel_hi:[1,1,0]\n"
"v_pk_fma_f32 v[86:87],v[86:87],v[58:59],v[54:55] op_sel:[0,1,0] op_sel_hi:[1,1,0]\n"
CV "v116,v80,v81\n"
CV "v117,v82,v83\n"
CV "v118,v84,v85\n"
CV "v119,v86,v87\n"
MF "a[136:139],v[160:163],v[108:111],a[136:139]\n"
"ds_read_b128 a[100:103],v3\n"
AW "a128,v116\n"
AW "a129,v117\n"
AW "a130,v118\n"
AW "a131,v119\n"
AR "v72,a132\n"
AR "v73,a133\n"
AR "v74,a134\n"
MF "a[156:159],v[164:167],v[108:111],a[156:159]\n"
AR "v75,a135\n"
AR "v76,a152\n"
AR "v77,a153\n"
AR "v78,a154\n"
AR "v79,a155\n"
"v_pk_fma_f32 v[72:73],v[72:73],v[60:61],v[54:55] op_sel:[0,0,1] op_sel_hi:[1,0,1]\n"
"v_pk_fma_f32 v[74:75],v[74:75],v[60:61],v[54:55] op_sel:[0,0,1] op_sel_hi:[1,0,1]\n"
"v_pk_fma_f32 v[76:77],v[76:77],v[60:61],v[54:55] op_sel:[0,0,1] op_sel_hi:[1,0,1]\n"
"v_pk_fma_f32 v[78:79],v[78:79],v[60:61],v[54:55] op_sel:[0,0,1] op_sel_hi:[1,0,1]\n"
CV "v112,v72,v73\n"
CV "v113,v74,v75\n"
CV "v114,v76,v77\n"
CV "v115,v78,v79\n"
"s_waitcnt lgkmcnt(8)\n"
MF "a[160:163],v[168:171],v[92:95],a[160:163]\n"
"ds_read_b128 a[64:67],v2 offset:2048\n"
AW "a132,v112\n"
AW "a133,v113\n"
AW "a134,v114\n"
AW "a135,v115\n"
AR "v80,a136\n"
AR "v81,a137\n"
AR "v82,a138\n"
"s_waitcnt lgkmcnt(7)\n"
MF "a[180:183],v[172:175],v[92:95],a[180:183]\n"
AR "v83,a139\n"
AR "v84,a156\n"
AR "v85,a157\n"
AR "v86,a158\n"
AR "v87,a159\n"
"v_pk_fma_f32 v[80:81],v[80:81],v[60:61],v[56:57] op_sel:[0,1,0] op_sel_hi:[1,1,0]\n"
"v_pk_fma_f32 v[82:83],v[82:83],v[60:61],v[56:57] op_sel:[0,1,0] op_sel_hi:[1,1,0]\n"
"v_pk_fma_f32 v[84:85],v[84:85],v[60:61],v[56:57] op_sel:[0,1,0] op_sel_hi:[1,1,0]\n"
"v_pk_fma_f32 v[86:87],v[86:87],v[60:61],v[56:57] op_sel:[0,1,0] op_sel_hi:[1,1,0]\n"
CV "v116,v80,v81\n"
CV "v117,v82,v83\n"
CV "v118,v84,v85\n"
CV "v119,v86,v87\n"
MF "a[164:167],v[168:171],v[96:99],a[164:167]\n"
"ds_read_b128 a[104:107],v3 offset:2048\n"
AW "a136,v116\n"
AW "a137,v117\n"
AW "a138,v118\n"
AW "a139,v119\n"
AR "v72,a160\n"
AR "v73,a161\n"
AR "v74,a162\n"
MF "a[184:187],v[172:175],v[96:99],a[184:187]\n"
AR "v75,a163\n"
AR "v76,a180\n"
AR "v77,a181\n"
AR "v78,a182\n"
AR "v79,a183\n"
"v_pk_fma_f32 v[72:73],v[72:73],v[56:57],v[52:53] op_sel:[0,1,0] op_sel_hi:[1,1,0]\n"
"v_pk_fma_f32 v[74:75],v[74:75],v[56:57],v[52:53] op_sel:[0,1,0] op_sel_hi:[1,1,0]\n"
"v_pk_fma_f32 v[76:77],v[76:77],v[56:57],v[52:53] op_sel:[0,1,0] op_sel_hi:[1,1,0]\n"
"v_pk_fma_f32 v[78:79],v[78:79],v[56:57],v[52:53] op_sel:[0,1,0] op_sel_hi:[1,1,0]\n"
CV "v112,v72,v73\n"
CV "v113,v74,v75\n"
CV "v114,v76,v77\n"
CV "v115,v78,v79\n"
MF "a[168:171],v[168:171],v[100:103],a[168:171]\n"
"ds_read_b128 a[68:71],v2 offset:4096\n"
AW "a160,v112\n"
AW "a161,v113\n"
AW "a162,v114\n"
AW "a163,v115\n"
AR "v80,a164\n"
AR "v81,a165\n"
AR "v82,a166\n"
MF "a[188:191],v[172:175],v[100:103],a[188:191]\n"
AR "v83,a167\n"
AR "v84,a184\n"
AR "v85,a185\n"
AR "v86,a186\n"
AR "v87,a187\n"
"v_pk_fma_f32 v[80:81],v[80:81],v[58:59],v[52:53] op_sel:[0,0,1] op_sel_hi:[1,0,1]\n"
"v_pk_fma_f32 v[82:83],v[82:83],v[58:59],v[52:53] op_sel:[0,0,1] op_sel_hi:[1,0,1]\n"
"v_pk_fma_f32 v[84:85],v[84:85],v[58:59],v[52:53] op_sel:[0,0,1] op_sel_hi:[1,0,1]\n"
"v_pk_fma_f32 v[86:87],v[86:87],v[58:59],v[52:53] op_sel:[0,0,1] op_sel_hi:[1,0,1]\n"
CV "v116,v80,v81\n"
CV "v117,v82,v83\n"
CV "v118,v84,v85\n"
CV "v119,v86,v87\n"
MF "a[172:175],v[168:171],v[104:107],a[172:175]\n"
"ds_read_b128 a[108:111],v3 offset:4096\n"
AW "a164,v116\n"
AW "a165,v117\n"
AW "a166,v118\n"
AW "a167,v119\n"
AR "v72,a168\n"
AR "v73,a169\n"
AR "v74,a170\n"
MF "a[192:195],v[172:175],v[104:107],a[192:195]\n"
AR "v75,a171\n"
AR "v76,a188\n"
AR "v77,a189\n"
AR "v78,a190\n"
AR "v79,a191\n"
"v_pk_fma_f32 v[72:73],v[72:73],v[58:59],v[54:55] op_sel:[0,1,0] op_sel_hi:[1,1,0]\n"
"v_pk_fma_f32 v[74:75],v[74:75],v[58:59],v[54:55] op_sel:[0,1,0] op_sel_hi:[1,1,0]\n"
"v_pk_fma_f32 v[76:77],v[76:77],v[58:59],v[54:55] op_sel:[0,1,0] op_sel_hi:[1,1,0]\n"
"v_pk_fma_f32 v[78:79],v[78:79],v[58:59],v[54:55] op_sel:[0,1,0] op_sel_hi:[1,1,0]\n"
CV "v112,v72,v73\n"
CV "v113,v74,v75\n"
CV "v114,v76,v77\n"
CV "v115,v78,v79\n"
MF "a[176:179],v[168:171],v[108:111],a[176:179]\n"
"ds_read_b128 a[72:75],v2 offset:6144\n"
AW "a168,v112\n"
AW "a169,v113\n"
AW "a170,v114\n"
AW "a171,v115\n"
AR "v80,a172\n"
AR "v81,a173\n"
AR "v82,a174\n"
MF "a[196:199],v[172:175],v[108:111],a[196:199]\n"
AR "v83,a175\n"
AR "v84,a192\n"
AR "v85,a193\n"
AR "v86,a194\n"
AR "v87,a195\n"
"v_pk_fma_f32 v[80:81],v[80:81],v[60:61],v[54:55] op_sel:[0,0,1] op_sel_hi:[1,0,1]\n"
"v_pk_fma_f32 v[82:83],v[82:83],v[60:61],v[54:55] op_sel:[0,0,1] op_sel_hi:[1,0,1]\n"
"v_pk_fma_f32 v[84:85],v[84:85],v[60:61],v[54:55] op_sel:[0,0,1] op_sel_hi:[1,0,1]\n"
"v_pk_fma_f32 v[86:87],v[86:87],v[60:61],v[54:55] op_sel:[0,0,1] op_sel_hi:[1,0,1]\n"
CV "v116,v80,v81\n"
CV "v117,v82,v83\n"
CV "v118,v84,v85\n"
CV "v119,v86,v87\n"
"s_waitcnt lgkmcnt(9)\n"
MF "a[200:203],v[176:179],v[92:95],a[200:203]\n"
"ds_read_b128 a[112:115],v3 offset:6144\n"
AW "a172,v116\n"
AW "a173,v117\n"
AW "a174,v118\n"
AW "a175,v119\n"
AR "v72,a176\n"
AR "v73,a177\n"
AR "v74,a178\n"
"s_waitcnt lgkmcnt(8)\n"
MF "a[220:223],v[180:183],v[92:95],a[220:223]\n"
AR "v75,a179\n"
AR "v76,a196\n"
AR "v77,a197\n"
AR "v78,a198\n"
AR "v79,a199\n"
"v_pk_fma_f32 v[72:73],v[72:73],v[60:61],v[56:57] op_sel:[0,1,0] op_sel_hi:[1,1,0]\n"
"v_pk_fma_f32 v[74:75],v[74:75],v[60:61],v[56:57] op_sel:[0,1,0] op_sel_hi:[1,1,0]\n"
"v_pk_fma_f32 v[76:77],v[76:77],v[60:61],v[56:57] op_sel:[0,1,0] op_sel_hi:[1,1,0]\n"
"v_pk_fma_f32 v[78:79],v[78:79],v[60:61],v[56:57] op_sel:[0,1,0] op_sel_hi:[1,1,0]\n"
CV "v112,v72,v73\n"
CV "v113,v74,v75\n"
CV "v114,v76,v77\n"
CV "v115,v78,v79\n"
MF "a[204:207],v[176:179],v[96:99],a[204:207]\n"
"ds_read_b128 a[76:79],v2 offset:8192\n"
AW "a176,v112\n"
AW "a177,v113\n"
AW "a178,v114\n"
AW "a179,v115\n"
AR "v80,a200\n"
AR "v81,a201\n"
AR "v82,a202\n"
MF "a[224:227],v[180:183],v[96:99],a[224:227]\n"
AR "v83,a203\n"
AR "v84,a220\n"
AR "v85,a221\n"
AR "v86,a222\n"
AR "v87,a223\n"
"v_pk_fma_f32 v[80:81],v[80:81],v[56:57],v[52:53] op_sel:[0,1,0] op_sel_hi:[1,1,0]\n"
"v_pk_fma_f32 v[82:83],v[82:83],v[56:57],v[52:53] op_sel:[0,1,0] op_sel_hi:[1,1,0]\n"
"v_pk_fma_f32 v[84:85],v[84:85],v[56:57],v[52:53] op_sel:[0,1,0] op_sel_hi:[1,1,0]\n"
"v_pk_fma_f32 v[86:87],v[86:87],v[56:57],v[52:53] op_sel:[0,1,0] op_sel_hi:[1,1,0]\n"
CV "v116,v80,v81\n"
CV "v117,v82,v83\n"
CV "v118,v84,v85\n"
CV "v119,v86,v87\n"
MF "a[208:211],v[176:179],v[100:103],a[208:211]\n"
"ds_read_b128 a[116:119],v3 offset:8192\n"
AW "a20,v116\n"
AW "a21,v117\n"
AW "a22,v118\n"
AW "a23,v119\n"
AR "v72,a204\n"
AR "v73,a205\n"
AR "v74,a206\n"
MF "a[228:231],v[180:183],v[100:103],a[228:231]\n"
AR "v75,a207\n"
AR "v76,a224\n"
AR "v77,a225\n"
AR "v78,a226\n"
AR "v79,a227\n"
"v_pk_fma_f32 v[72:73],v[72:73],v[58:59],v[52:53] op_sel:[0,0,1] op_sel_hi:[1,0,1]\n"
"v_pk_fma_f32 v[74:75],v[74:75],v[58:59],v[52:53] op_sel:[0,0,1] op_sel_hi:[1,0,1]\n"
"v_pk_fma_f32 v[76:77],v[76:77],v[58:59],v[52:53] op_sel:[0,0,1] op_sel_hi:[1,0,1]\n"
"v_pk_fma_f32 v[78:79],v[78:79],v[58:59],v[52:53] op_sel:[0,0,1] op_sel_hi:[1,0,1]\n"
CV "v112,v72,v73\n"
CV "v113,v74,v75\n"
CV "v114,v76,v77\n"
CV "v115,v78,v79\n"
MF "a[212:215],v[176:179],v[104:107],a[212:215]\n"
AW "a24,v112\n"
AW "a25,v113\n"
AW "a26,v114\n"
AW "a27,v115\n"
AR "v80,a208\n"
AR "v81,a209\n"
AR "v82,a210\n"
MF "a[232:235],v[180:183],v[104:107],a[232:235]\n"
AR "v83,a211\n"
AR "v84,a228\n"
AR "v85,a229\n"
AR "v86,a230\n"
AR "v87,a231\n"
"v_pk_fma_f32 v[80:81],v[80:81],v[58:59],v[54:55] op_sel:[0,1,0] op_sel_hi:[1,1,0]\n"
"v_pk_fma_f32 v[82:83],v[82:83],v[58:59],v[54:55] op_sel:[0,1,0] op_sel_hi:[1,1,0]\n"
"v_pk_fma_f32 v[84:85],v[84:85],v[58:59],v[54:55] op_sel:[0,1,0] op_sel_hi:[1,1,0]\n"
"v_pk_fma_f32 v[86:87],v[86:87],v[58:59],v[54:55] op_sel:[0,1,0] op_sel_hi:[1,1,0]\n"
CV "v116,v80,v81\n"
CV "v117,v82,v83\n"
CV "v118,v84,v85\n"
CV "v119,v86,v87\n"
MF "a[216:219],v[176:179],v[108:111],a[216:219]\n"
"s_waitcnt vmcnt(14) lgkmcnt(0)\n"
"s_barrier\n"
TR "a[200:201],v0\n"
TR "a[202:203],v0 offset:512\n"
TR "a[204:205],v0 offset:4096\n"
TR "a[206:207],v0 offset:4608\n"
AW "a28,v116\n"
AW "a29,v117\n"
AW "a30,v118\n"
AW "a31,v119\n"
AR "v72,a212\n"
AR "v73,a213\n"
AR "v74,a214\n"
MF "a[236:239],v[180:183],v[108:111],a[236:239]\n"
AR "v75,a215\n"
AR "v76,a232\n"
AR "v77,a233\n"
AR "v78,a234\n"
AR "v79,a235\n"
"v_pk_fma_f32 v[72:73],v[72:73],v[60:61],v[54:55] op_sel:[0,0,1] op_sel_hi:[1,0,1]\n"
"v_pk_fma_f32 v[74:75],v[74:75],v[60:61],v[54:55] op_sel:[0,0,1] op_sel_hi:[1,0,1]\n"
"v_pk_fma_f32 v[76:77],v[76:77],v[60:61],v[54:55] op_sel:[0,0,1] op_sel_hi:[1,0,1]\n"
"v_pk_fma_f32 v[78:79],v[78:79],v[60:61],v[54:55] op_sel:[0,0,1] op_sel_hi:[1,0,1]\n"
CV "v112,v72,v73\n"
CV "v113,v74,v75\n"
CV "v114,v76,v77\n"
CV "v115,v78,v79\n"
TR "a[208:209],v0 offset:8\n"
TR "a[210:211],v0 offset:520\n"
TR "a[212:213],v0 offset:4104\n"
TR "a[214:215],v0 offset:4616\n"
AW "a32,v112\n"
AW "a33,v113\n"
AW "a34,v114\n"
AW "a35,v115\n"
AR "v80,a216\n"
AR "v81,a217\n"
AR "v82,a218\n"
AR "v83,a219\n"
AR "v84,a236\n"
AR "v85,a237\n"
AR "v86,a238\n"
AR "v87,a239\n"
"v_pk_fma_f32 v[80:81],v[80:81],v[60:61],v[56:57] op_sel:[0,1,0] op_sel_hi:[1,1,0]\n"
"v_pk_fma_f32 v[82:83],v[82:83],v[60:61],v[56:57] op_sel:[0,1,0] op_sel_hi:[1,1,0]\n"
"v_pk_fma_f32 v[84:85],v[84:85],v[60:61],v[56:57] op_sel:[0,1,0] op_sel_hi:[1,1,0]\n"
"v_pk_fma_f32 v[86:87],v[86:87],v[60:61],v[56:57] op_sel:[0,1,0] op_sel_hi:[1,1,0]\n"
CV "v116,v80,v81\n"
CV "v117,v82,v83\n"
CV "v118,v84,v85\n"
CV "v119,v86,v87\n"
AW "a36,v116\n"
AW "a37,v117\n"
AW "a38,v118\n"
AW "a39,v119\n"
"s_mov_b32 s13,s11\n"
"s_mov_b32 s70,s71\n"
"s_mov_b32 s57,1\n"
"s_waitcnt lgkmcnt(0)\n"
"s_branch .Lq0\n"
".Lpgqy_c:\n"
"s_cbranch_execz .Lpgqj_a\n"
".Lqy:\n"
MF "v[216:219],a[216:219],a[60:63],v[216:219]\n"
TR "a[208:209],v0 offset:8\n"
MF "v[216:219],a[220:223],a[100:103],v[216:219]\n"
TR "a[210:211],v0 offset:520\n"
MF "v[224:227],a[220:223],a[104:107],v[224:227]\n"
MF "v[224:227],a[216:219],a[64:67],v[224:227]\n"
TR "a[212:213],v0 offset:4104\n"
MF "v[232:235],a[216:219],a[68:71],v[232:235]\n"
TR "a[214:215],v0 offset:4616\n"
MF "v[232:235],a[220:223],a[108:111],v[232:235]\n"
MF "v[240:243],a[220:223],a[112:115],v[240:243]\n"
MF "v[240:243],a[216:219],a[72:75],v[240:243]\n"
MF "v[248:251],a[216:219],a[76:79],v[248:251]\n"
MF "v[248:251],a[220:223],a[116:119],v[248:251]\n"
MF "v[248:251],a[200:203],a[156:159],v[248:251]\n"
TR "a[216:217],v0 offset:16392\n"
MF "v[248:251],a[204:207],a[196:199],v[248:251]\n"
TR "a[218:219],v0 offset:16904\n"
MF "v[240:243],a[204:207],a[192:195],v[240:243]\n"
MF "v[240:243],a[200:203],a[152:155],v[240:243]\n"
TR "a[220:221],v0 offset:20488\n"
MF "v[232:235],a[200:203],a[148:151],v[232:235]\n"
TR "a[222:223],v0 offset:21000\n"
MF "v[232:235],a[204:207],a[188:191],v[232:235]\n"
MF "v[224:227],a[204:207],a[184:187],v[224:227]\n"
MF "v[224:227],a[200:203],a[144:147],v[224:227]\n"
"v_fma_f32 v248,v248,s54,v14\n"
"v_fma_f32 v249,v249,s54,v14\n"
MF "v[216:219],a[200:203],a[140:143],v[216:219]\n"
"v_fma_f32 v250,v250,s54,v14\n"
"v_fma_f32 v251,v251,s54,v14\n"
MF "v[216:219],a[204:207],a[180:183],v[216:219]\n"
"s_waitcnt lgkmcnt(4)\n"
"v_fma_f32 v252,v252,s54,v14\n"
"v_fma_f32 v253,v253,s54,v14\n"
MF "v[180:183],a[208:211],a[60:63],v[180:183]\n"
TR "a[200:201],v0\n"
"v_fma_f32 v254,v254,s54,v14\n"
MF "v[180:183],a[212:215],a[100:103],v[180:183]\n"
TR "a[202:203],v0 offset:512\n"
"v_fma_f32 v255,v255,s54,v14\n"
MF "v[188:191],a[212:215],a[104:107],v[188:191]\n"
CV "v255,v254,v255\n"
CV "v254,v252,v253\n"
MF "v[188:191],a[208:211],a[64:67],v[188:191]\n"
TR "a[204:205],v0 offset:4096\n"
CV "v253,v250,v251\n"
MF "v[196:199],a[208:211],a[68:71],v[196:199]\n"
TR "a[206:207],v0 offset:4608\n"
CV "v252,v248,v249\n"
MF "v[196:199],a[212:215],a[108:111],v[196:199]\n"
"s_add_u32 s58,s75,4096\n"
"v_fma_f32 v240,v240,s54,v14\n"
"v_fma_f32 v241,v241,s54,v14\n"
MF "v[204:207],a[212:215],a[112:115],v[204:207]\n"
"buffer_store_dwordx4 v[252:255],v9,s[40:43],s58 offen offset:1024 sc1\n"
MF "v[204:207],a[208:211],a[72:75],v[204:207]\n"
"v_fma_f32 v242,v242,s54,v14\n"
"v_fma_f32 v243,v243,s54,v14\n"
MF "v[212:215],a[208:211],a[76:79],v[212:215]\n"
"v_fma_f32 v244,v244,s54,v14\n"
"v_fma_f32 v245,v245,s54,v14\n"
MF "v[212:215],a[212:215],a[116:119],v[212:215]\n"
"s_waitcnt lgkmcnt(4)\n"
"v_fma_f32 v246,v246,s54,v14\n"
"v_fma_f32 v247,v247,s54,v14\n"
MF "v[180:183],a[216:219],a[140:143],v[180:183]\n"
TR "a[208:209],v0 offset:16384\n"
CV "v251,v246,v247\n"
MF "v[180:183],a[220:223],a[180:183],v[180:183]\n"
TR "a[210:211],v0 offset:16896\n"
CV "v250,v244,v245\n"
MF "v[188:191],a[220:223],a[184:187],v[188:191]\n"
CV "v249,v242,v243\n"
CV "v248,v240,v241\n"
MF "v[188:191],a[216:219],a[144:147],v[188:191]\n"
TR "a[212:213],v0 offset:20480\n"
"v_fma_f32 v232,v232,s54,v14\n"
MF "v[196:199],a[216:219],a[148:151],v[196:199]\n"
TR "a[214:215],v0 offset:20992\n"
"v_fma_f32 v233,v233,s54,v14\n"
MF "v[196:199],a[220:223],a[188:191],v[196:199]\n"
"buffer_store_dwordx4 v[248:251],v9,s[40:43],s58 offen sc1\n"
MF "v[204:207],a[220:223],a[192:195],v[204:207]\n"
"v_fma_f32 v234,v234,s54,v14\n"
"v_fma_f32 v235,v235,s54,v14\n"
MF "v[204:207],a[216:219],a[152:155],v[204:207]\n"
"v_fma_f32 v236,v236,s54,v14\n"
"v_fma_f32 v237,v237,s54,v14\n"
MF "v[212:215],a[216:219],a[156:159],v[212:215]\n"
"s_waitcnt vmcnt(10) lgkmcnt(0)\n"
"v_fma_f32 v238,v238,s54,v14\n"
"v_fma_f32 v239,v239,s54,v14\n"
MF "v[212:215],a[220:223],a[196:199],v[212:215]\n"
"s_barrier\n"
MF "v[176:179],a[200:203],a[60:63],v[176:179]\n"
TR "a[216:217],v1 offset:32776\n"
CV "v247,v238,v239\n"
MF "v[176:179],a[204:207],a[100:103],v[176:179]\n"
TR "a[218:219],v1 offset:33288\n"
CV "v246,v236,v237\n"
MF "v[184:187],a[204:207],a[104:107],v[184:187]\n"
"s_add_u32 s66,s67,4718592\n"
"s_sub_u32 s81,s81,384\n"
"s_mov_b32 s83,7077888\n"
MF "v[184:187],a[200:203],a[64:67],v[184:187]\n"
"s_add_u32 m0,s62,0\n"
"s_add_u32 s82,s66,2359296\n"
TR "a[220:221],v1 offset:36872\n"
MF "v[192:195],a[200:203],a[68:71],v[192:195]\n"
"s_sub_u32 s15,s82,7864320\n"
"s_min_u32 s82,s82,s15\n"
TR "a[222:223],v1 offset:37384\n"
MF "v[192:195],a[204:207],a[108:111],v[192:195]\n"
LD "v6,s[28:31],s82 offen lds\n"
MF "v[200:203],a[204:207],a[112:115],v[200:203]\n"
CV "v245,v234,v235\n"
CV "v244,v232,v233\n"
MF "v[200:203],a[200:203],a[72:75],v[200:203]\n"
"v_fma_f32 v224,v224,s54,v14\n"
"v_fma_f32 v225,v225,s54,v14\n"
MF "v[208:211],a[200:203],a[76:79],v[208:211]\n"
"buffer_store_dwordx4 v[244:247],v9,s[40:43],s75 offen offset:3072 sc1\n"
MF "v[208:211],a[204:207],a[116:119],v[208:211]\n"
LD "v11,s[28:31],s82 offen offset:1024 lds\n"
MF "v[208:211],a[208:211],a[156:159],v[208:211]\n"
TR "a[200:201],v1 offset:49160\n"
"v_fma_f32 v226,v226,s54,v14\n"
MF "v[208:211],a[212:215],a[196:199],v[208:211]\n"
TR "a[202:203],v1 offset:49672\n"
"v_fma_f32 v227,v227,s54,v14\n"
MF "v[200:203],a[212:215],a[192:195],v[200:203]\n"
"v_fma_f32 v228,v228,s54,v14\n"
"v_fma_f32 v229,v229,s54,v14\n"
MF "v[200:203],a[208:211],a[152:155],v[200:203]\n"
TR "a[204:205],v1 offset:53256\n"
"v_fma_f32 v230,v230,s54,v14\n"
MF "v[192:195],a[208:211],a[148:151],v[192:195]\n"
TR "a[206:207],v1 offset:53768\n"
"v_fma_f32 v231,v231,s54,v14\n"
MF "v[192:195],a[212:215],a[188:191],v[192:195]\n"
CV "v243,v230,v231\n"
CV "v242,v228,v229\n"
MF "v[184:187],a[212:215],a[184:187],v[184:187]\n"
CV "v241,v226,v227\n"
CV "v240,v224,v225\n"
MF "v[184:187],a[208:211],a[144:147],v[184:187]\n"
"buffer_store_dwordx4 v[240:243],v9,s[40:43],s75 offen offset:2048 sc1\n"
MF "v[176:179],a[208:211],a[140:143],v[176:179]\n"
LD "v12,s[28:31],s82 offen offset:2048 lds\n"
MF "v[176:179],a[212:215],a[180:183],v[176:179]\n"
"s_waitcnt lgkmcnt(4)\n"
"v_fma_f32 v216,v216,s54,v14\n"
"v_fma_f32 v217,v217,s54,v14\n"
MF "v[140:143],a[216:219],a[60:63],v[140:143]\n"
TR "a[208:209],v1 offset:32768\n"
"v_fma_f32 v218,v218,s54,v14\n"
MF "v[140:143],a[220:223],a[100:103],v[140:143]\n"
TR "a[210:211],v1 offset:33280\n"
"v_fma_f32 v219,v219,s54,v14\n"
MF "v[148:151],a[220:223],a[104:107],v[148:151]\n"
".Lpg14c:\n"
"s_cbranch_execz .Lpg3a\n"
"v_fma_f32 v220,v220,s54,v14\n"
"v_fma_f32 v221,v221,s54,v14\n"
MF "v[148:151],a[216:219],a[64:67],v[148:151]\n"
TR "a[212:213],v1 offset:36864\n"
"v_fma_f32 v222,v222,s54,v14\n"
MF "v[156:159],a[216:219],a[68:71],v[156:159]\n"
TR "a[214:215],v1 offset:37376\n"
"v_fma_f32 v223,v223,s54,v14\n"
MF "v[156:159],a[220:223],a[108:111],v[156:159]\n"
CV "v239,v222,v223\n"
CV "v238,v220,v221\n"
MF "v[164:167],a[220:223],a[112:115],v[164:167]\n"
CV "v237,v218,v219\n"
CV "v236,v216,v217\n"
MF "v[164:167],a[216:219],a[72:75],v[164:167]\n"
LD "v13,s[28:31],s82 offen offset:3072 lds\n"
MF "v[172:175],a[216:219],a[76:79],v[172:175]\n"
"buffer_store_dwordx4 v[236:239],v9,s[40:43],s75 offen offset:1024 sc1\n"
MF "v[172:175],a[220:223],a[116:119],v[172:175]\n"
"s_waitcnt lgkmcnt(4)\n"
"v_fma_f32 v208,v208,s54,v14\n"
"v_fma_f32 v209,v209,s54,v14\n"
MF "v[140:143],a[200:203],a[140:143],v[140:143]\n"
TR "a[216:217],v1 offset:49152\n"
"v_fma_f32 v210,v210,s54,v14\n"
MF "v[140:143],a[204:207],a[180:183],v[140:143]\n"
TR "a[218:219],v1 offset:49664\n"
"v_fma_f32 v211,v211,s54,v14\n"
MF "v[148:151],a[204:207],a[184:187],v[148:151]\n"
"v_fma_f32 v212,v212,s54,v14\n"
"v_fma_f32 v213,v213,s54,v14\n"
MF "v[148:151],a[200:203],a[144:147],v[148:151]\n"
TR "a[220:221],v1 offset:53248\n"
"v_fma_f32 v214,v214,s54,v14\n"
MF "v[156:159],a[200:203],a[148:151],v[156:159]\n"
TR "a[222:223],v1 offset:53760\n"
"v_fma_f32 v215,v215,s54,v14\n"
MF "v[156:159],a[204:207],a[188:191],v[156:159]\n"
CV "v235,v214,v215\n"
CV "v234,v212,v213\n"
MF "v[164:167],a[204:207],a[192:195],v[164:167]\n"
CV "v233,v210,v211\n"
CV "v232,v208,v209\n"
MF "v[164:167],a[200:203],a[152:155],v[164:167]\n"
"v_fma_f32 v200,v200,s54,v14\n"
"v_fma_f32 v201,v201,s54,v14\n"
MF "v[172:175],a[200:203],a[156:159],v[172:175]\n"
"buffer_store_dwordx4 v[232:235],v9,s[40:43],s75 offen sc1\n"
MF "v[172:175],a[204:207],a[196:199],v[172:175]\n"
"s_waitcnt lgkmcnt(0)\n"
"v_fma_f32 v202,v202,s54,v14\n"
"v_fma_f32 v203,v203,s54,v14\n"
MF "v[136:139],a[208:211],a[60:63],v[136:139]\n"
"s_add_u32 m0,s62,16384\n"
"s_add_u32 s82,s66,2359424\n"
TR "a[200:201],v0 offset:32776\n"
MF "v[136:139],a[212:215],a[100:103],v[136:139]\n"
"s_sub_u32 s15,s82,7864320\n"
"s_min_u32 s82,s82,s15\n"
TR "a[202:203],v0 offset:33288\n"
MF "v[144:147],a[212:215],a[104:107],v[144:147]\n"
LD "v6,s[28:31],s82 offen lds\n"
MF "v[144:147],a[208:211],a[64:67],v[144:147]\n"
TR "a[204:205],v0 offset:36872\n"
"v_fma_f32 v204,v204,s54,v14\n"
MF "v[152:155],a[208:211],a[68:71],v[152:155]\n"
TR "a[206:207],v0 offset:37384\n"
"v_fma_f32 v205,v205,s54,v14\n"
MF "v[152:155],a[212:215],a[108:111],v[152:155]\n"
"v_fma_f32 v206,v206,s54,v14\n"
"v_fma_f32 v207,v207,s54,v14\n"
MF "v[160:163],a[212:215],a[112:115],v[160:163]\n"
CV "v231,v206,v207\n"
CV "v230,v204,v205\n"
MF "v[160:163],a[208:211],a[72:75],v[160:163]\n"
CV "v229,v202,v203\n"
CV "v228,v200,v201\n"
MF "v[168:171],a[208:211],a[76:79],v[168:171]\n"
AW "a84,v228\n"
AW "a85,v229\n"
MF "v[168:171],a[212:215],a[116:119],v[168:171]\n"
LD "v11,s[28:31],s82 offen offset:1024 lds\n"
MF "v[168:171],a[216:219],a[156:159],v[168:171]\n"
TR "a[208:209],v0 offset:49160\n"
AW "a86,v230\n"
MF "v[168:171],a[220:223],a[196:199],v[168:171]\n"
TR "a[210:211],v0 offset:49672\n"
AW "a87,v231\n"
MF "v[160:163],a[220:223],a[192:195],v[160:163]\n"
"v_fma_f32 v192,v192,s54,v14\n"
"v_fma_f32 v193,v193,s54,v14\n"
MF "v[160:163],a[216:219],a[152:155],v[160:163]\n"
TR "a[212:213],v0 offset:53256\n"
"v_fma_f32 v194,v194,s54,v14\n"
MF "v[152:155],a[216:219],a[148:151],v[152:155]\n"
TR "a[214:215],v0 offset:53768\n"
"v_fma_f32 v195,v195,s54,v14\n"
MF "v[152:155],a[220:223],a[188:191],v[152:155]\n"
"v_fma_f32 v196,v196,s54,v14\n"
"v_fma_f32 v197,v197,s54,v14\n"
MF "v[144:147],a[220:223],a[184:187],v[144:147]\n"
"v_fma_f32 v198,v198,s54,v14\n"
"v_fma_f32 v199,v199,s54,v14\n"
MF "v[144:147],a[216:219],a[144:147],v[144:147]\n"
CV "v227,v198,v199\n"
CV "v226,v196,v197\n"
MF "v[136:139],a[216:219],a[140:143],v[136:139]\n"
LD "v12,s[28:31],s82 offen offset:2048 lds\n"
MF "v[136:139],a[220:223],a[180:183],v[136:139]\n"
"s_waitcnt lgkmcnt(4)\n"
CV "v225,v194,v195\n"
CV "v224,v192,v193\n"
MF "v[100:103],a[200:203],a[60:63],v[100:103]\n"
TR "a[216:217],v0 offset:32768\n"
AW "a80,v224\n"
MF "v[100:103],a[204:207],a[100:103],v[100:103]\n"
TR "a[218:219],v0 offset:33280\n"
AW "a81,v225\n"
MF "v[108:111],a[204:207],a[104:107],v[108:111]\n"
AW "a82,v226\n"
AW "a83,v227\n"
MF "v[108:111],a[200:203],a[64:67],v[108:111]\n"
TR "a[220:221],v0 offset:36864\n"
"v_fma_f32 v184,v184,s54,v14\n"
MF "v[116:119],a[200:203],a[68:71],v[116:119]\n"
TR "a[222:223],v0 offset:37376\n"
"v_fma_f32 v185,v185,s54,v14\n"
MF "v[116:119],a[204:207],a[108:111],v[116:119]\n"
"v_fma_f32 v186,v186,s54,v14\n"
"v_fma_f32 v187,v187,s54,v14\n"
MF "v[124:127],a[204:207],a[112:115],v[124:127]\n"
"v_fma_f32 v188,v188,s54,v14\n"
"v_fma_f32 v189,v189,s54,v14\n"
MF "v[124:127],a[200:203],a[72:75],v[124:127]\n"
LD "v13,s[28:31],s82 offen offset:3072 lds\n"
MF "v[132:135],a[200:203],a[76:79],v[132:135]\n"
"v_fma_f32 v190,v190,s54,v14\n"
"v_fma_f32 v191,v191,s54,v14\n"
MF "v[132:135],a[204:207],a[116:119],v[132:135]\n"
"s_waitcnt lgkmcnt(4)\n"
CV "v223,v190,v191\n"
CV "v222,v188,v189\n"
MF "v[100:103],a[208:211],a[140:143],v[100:103]\n"
TR "a[200:201],v0 offset:49152\n"
CV "v221,v186,v187\n"
MF "v[100:103],a[212:215],a[180:183],v[100:103]\n"
TR "a[202:203],v0 offset:49664\n"
CV "v220,v184,v185\n"
MF "v[108:111],a[212:215],a[184:187],v[108:111]\n"
AW "a56,v220\n"
AW "a57,v221\n"
MF "v[108:111],a[208:211],a[144:147],v[108:111]\n"
TR "a[204:205],v0 offset:53248\n"
AW "a58,v222\n"
MF "v[116:119],a[208:211],a[148:151],v[116:119]\n"
TR "a[206:207],v0 offset:53760\n"
AW "a59,v223\n"
MF "v[116:119],a[212:215],a[188:191],v[116:119]\n"
"v_fma_f32 v176,v176,s54,v14\n"
"v_fma_f32 v177,v177,s54,v14\n"
MF "v[124:127],a[212:215],a[192:195],v[124:127]\n"
"v_add_u32 v0,0xc000,v0\n"
"v_fma_f32 v178,v178,s54,v14\n"
MF "v[124:127],a[208:211],a[152:155],v[124:127]\n"
"v_add_u32 v1,0xc000,v1\n"
"v_fma_f32 v179,v179,s54,v14\n"
MF "v[132:135],a[208:211],a[156:159],v[132:135]\n"
"s_waitcnt vmcnt(14) lgkmcnt(0)\n"
"v_fma_f32 v180,v180,s54,v14\n"
"v_fma_f32 v181,v181,s54,v14\n"
MF "v[132:135],a[212:215],a[196:199],v[132:135]\n"
"s_barrier\n"
MF "v[96:99],a[216:219],a[60:63],v[96:99]\n"
"s_add_u32 m0,s62,32768\n"
"s_add_u32 s82,s66,2359552\n"
TR "a[208:209],v1 offset:16392\n"
MF "v[96:99],a[220:223],a[100:103],v[96:99]\n"
"s_sub_u32 s15,s82,7864320\n"
"s_min_u32 s82,s82,s15\n"
TR "a[210:211],v1 offset:16904\n"
MF "v[104:107],a[220:223],a[104:107],v[104:107]\n"
LD "v6,s[28:31],s82 offen lds\n"
MF "v[104:107],a[216:219],a[64:67],v[104:107]\n"
TR "a[212:213],v1 offset:20488\n"
"v_fma_f32 v182,v182,s54,v14\n"
MF "v[112:115],a[216:219],a[68:71],v[112:115]\n"
TR "a[214:215],v1 offset:21000\n"
"v_fma_f32 v183,v183,s54,v14\n"
MF "v[112:115],a[220:223],a[108:111],v[112:115]\n"
CV "v219,v182,v183\n"
CV "v218,v180,v181\n"
MF "v[120:123],a[220:223],a[112:115],v[120:123]\n"
CV "v217,v178,v179\n"
CV "v216,v176,v177\n"
MF "v[120:123],a[216:219],a[72:75],v[120:123]\n"
AW "a52,v216\n"
AW "a53,v217\n"
MF "v[128:131],a[216:219],a[76:79],v[128:131]\n"
AW "a54,v218\n"
AW "a55,v219\n"
MF "v[128:131],a[220:223],a[116:119],v[128:131]\n"
LD "v11,s[28:31],s82 offen offset:1024 lds\n"
MF "v[128:131],a[200:203],a[156:159],v[128:131]\n"
TR "a[216:217],v1 offset:32776\n"
"v_fma_f32 v168,v168,s54,v14\n"
MF "v[128:131],a[204:207],a[196:199],v[128:131]\n"
TR "a[218:219],v1 offset:33288\n"
"v_fma_f32 v169,v169,s54,v14\n"
MF "v[120:123],a[204:207],a[192:195],v[120:123]\n"
"v_fma_f32 v170,v170,s54,v14\n"
"v_fma_f32 v171,v171,s54,v14\n"
MF "v[120:123],a[200:203],a[152:155],v[120:123]\n"
TR "a[220:221],v1 offset:36872\n"
"v_fma_f32 v172,v172,s54,v14\n"
MF "v[112:115],a[200:203],a[148:151],v[112:115]\n"
TR "a[222:223],v1 offset:37384\n"
"v_fma_f32 v173,v173,s54,v14\n"
MF "v[112:115],a[204:207],a[188:191],v[112:115]\n"
"v_fma_f32 v174,v174,s54,v14\n"
"v_fma_f32 v175,v175,s54,v14\n"
MF "v[104:107],a[204:207],a[184:187],v[104:107]\n"
CV "v215,v174,v175\n"
CV "v214,v172,v173\n"
MF "v[104:107],a[200:203],a[144:147],v[104:107]\n"
CV "v213,v170,v171\n"
CV "v212,v168,v169\n"
MF "v[96:99],a[200:203],a[140:143],v[96:99]\n"
LD "v12,s[28:31],s82 offen offset:2048 lds\n"
MF "v[96:99],a[204:207],a[180:183],v[96:99]\n"
"s_waitcnt lgkmcnt(4)\n"
AW "a48,v212\n"
AW "a49,v213\n"
MF "v[60:63],a[208:211],a[60:63],v[60:63]\n"
TR "a[200:201],v1 offset:16384\n"
AW "a50,v214\n"
MF "v[60:63],a[212:215],a[100:103],v[60:63]\n"
TR "a[202:203],v1 offset:16896\n"
AW "a51,v215\n"
MF "v[68:71],a[212:215],a[104:107],v[68:71]\n"
"v_fma_f32 v160,v160,s54,v14\n"
"v_fma_f32 v161,v161,s54,v14\n"
MF "v[68:71],a[208:211],a[64:67],v[68:71]\n"
TR "a[204:205],v1 offset:20480\n"
"v_fma_f32 v162,v162,s54,v14\n"
MF "v[76:79],a[208:211],a[68:71],v[76:79]\n"
TR "a[206:207],v1 offset:20992\n"
"v_fma_f32 v163,v163,s54,v14\n"
MF "v[76:79],a[212:215],a[108:111],v[76:79]\n"
"v_fma_f32 v164,v164,s54,v14\n"
"v_fma_f32 v165,v165,s54,v14\n"
MF "v[84:87],a[212:215],a[112:115],v[84:87]\n"
"v_fma_f32 v166,v166,s54,v14\n"
"v_fma_f32 v167,v167,s54,v14\n"
MF "v[84:87],a[208:211],a[72:75],v[84:87]\n"
LD "v13,s[28:31],s82 offen offset:3072 lds\n"
MF "v[92:95],a[208:211],a[76:79],v[92:95]\n"
CV "v211,v166,v167\n"
CV "v210,v164,v165\n"
MF "v[92:95],a[212:215],a[116:119],v[92:95]\n"
"s_waitcnt lgkmcnt(4)\n"
CV "v209,v162,v163\n"
CV "v208,v160,v161\n"
MF "v[60:63],a[216:219],a[140:143],v[60:63]\n"
TR "a[208:209],v1 offset:32768\n"
AW "a44,v208\n"
MF "v[60:63],a[220:223],a[180:183],v[60:63]\n"
TR "a[210:211],v1 offset:33280\n"
AW "a45,v209\n"
MF "v[68:71],a[220:223],a[184:187],v[68:71]\n"
AW "a46,v210\n"
AW "a47,v211\n"
MF "v[68:71],a[216:219],a[144:147],v[68:71]\n"
TR "a[212:213],v1 offset:36864\n"
"v_fma_f32 v152,v152,s54,v14\n"
MF "v[76:79],a[216:219],a[148:151],v[76:79]\n"
TR "a[214:215],v1 offset:37376\n"
"v_fma_f32 v153,v153,s54,v14\n"
MF "v[76:79],a[220:223],a[188:191],v[76:79]\n"
"v_fma_f32 v154,v154,s54,v14\n"
"v_fma_f32 v155,v155,s54,v14\n"
MF "v[84:87],a[220:223],a[192:195],v[84:87]\n"
"v_fma_f32 v156,v156,s54,v14\n"
"v_fma_f32 v157,v157,s54,v14\n"
MF "v[84:87],a[216:219],a[152:155],v[84:87]\n"
"v_fma_f32 v158,v158,s54,v14\n"
"v_fma_f32 v159,v159,s54,v14\n"
MF "v[92:95],a[216:219],a[156:159],v[92:95]\n"
CV "v207,v158,v159\n"
CV "v206,v156,v157\n"
MF "v[92:95],a[220:223],a[196:199],v[92:95]\n"
"s_waitcnt lgkmcnt(0)\n"
CV "v205,v154,v155\n"
CV "v204,v152,v153\n"
MF "v[56:59],a[200:203],a[60:63],v[56:59]\n"
TR "a[216:217],v0 offset:16392\n"
AW "a40,v204\n"
MF "v[56:59],a[204:207],a[100:103],v[56:59]\n"
TR "a[218:219],v0 offset:16904\n"
AW "a41,v205\n"
MF "v[64:67],a[204:207],a[104:107],v[64:67]\n"
AW "a42,v206\n"
AW "a43,v207\n"
MF "v[64:67],a[200:203],a[64:67],v[64:67]\n"
TR "a[220:221],v0 offset:20488\n"
"v_fma_f32 v144,v144,s54,v14\n"
MF "v[72:75],a[200:203],a[68:71],v[72:75]\n"
TR "a[222:223],v0 offset:21000\n"
"v_fma_f32 v145,v145,s54,v14\n"
MF "v[72:75],a[204:207],a[108:111],v[72:75]\n"
"v_fma_f32 v146,v146,s54,v14\n"
"v_fma_f32 v147,v147,s54,v14\n"
MF "v[80:83],a[204:207],a[112:115],v[80:83]\n"
"v_fma_f32 v148,v148,s54,v14\n"
"v_fma_f32 v149,v149,s54,v14\n"
MF "v[80:83],a[200:203],a[72:75],v[80:83]\n"
"v_fma_f32 v150,v150,s54,v14\n"
"v_fma_f32 v151,v151,s54,v14\n"
MF "v[88:91],a[200:203],a[76:79],v[88:91]\n"
CV "v203,v150,v151\n"
CV "v202,v148,v149\n"
MF "v[88:91],a[204:207],a[116:119],v[88:91]\n"
CV "v201,v146,v147\n"
CV "v200,v144,v145\n"
MF "v[88:91],a[208:211],a[156:159],v[88:91]\n"
TR "a[200:201],v0 offset:32776\n"
AW "a16,v200\n"
MF "v[88:91],a[212:215],a[196:199],v[88:91]\n"
TR "a[202:203],v0 offset:33288\n"
AW "a17,v201\n"
MF "v[80:83],a[212:215],a[192:195],v[80:83]\n"
AW "a18,v202\n"
AW "a19,v203\n"
MF "v[80:83],a[208:211],a[152:155],v[80:83]\n"
TR "a[204:205],v0 offset:36872\n"
"v_fma_f32 v136,v136,s54,v14\n"
MF "v[72:75],a[208:211],a[148:151],v[72:75]\n"
TR "a[206:207],v0 offset:37384\n"
"v_fma_f32 v137,v137,s54,v14\n"
MF "v[72:75],a[212:215],a[188:191],v[72:75]\n"
"v_fma_f32 v138,v138,s54,v14\n"
"v_fma_f32 v139,v139,s54,v14\n"
MF "v[64:67],a[212:215],a[184:187],v[64:67]\n"
"v_fma_f32 v140,v140,s54,v14\n"
"v_fma_f32 v141,v141,s54,v14\n"
MF "v[64:67],a[208:211],a[144:147],v[64:67]\n"
"v_fma_f32 v142,v142,s54,v14\n"
"v_fma_f32 v143,v143,s54,v14\n"
MF "v[56:59],a[208:211],a[140:143],v[56:59]\n"
CV "v199,v142,v143\n"
CV "v198,v140,v141\n"
MF "v[56:59],a[212:215],a[180:183],v[56:59]\n"
"s_waitcnt lgkmcnt(4)\n"
CV "v197,v138,v139\n"
CV "v196,v136,v137\n"
MF "v[20:23],a[216:219],a[60:63],v[20:23]\n"
TR "a[208:209],v0 offset:16384\n"
AW "a12,v196\n"
MF "v[20:23],a[220:223],a[100:103],v[20:23]\n"
TR "a[210:211],v0 offset:16896\n"
AW "a13,v197\n"
MF "v[28:31],a[220:223],a[104:107],v[28:31]\n"
AW "a14,v198\n"
AW "a15,v199\n"
MF "v[28:31],a[216:219],a[64:67],v[28:31]\n"
TR "a[212:213],v0 offset:20480\n"
"v_fma_f32 v128,v128,s54,v14\n"
MF "v[36:39],a[216:219],a[68:71],v[36:39]\n"
TR "a[214:215],v0 offset:20992\n"
"v_fma_f32 v129,v129,s54,v14\n"
MF "v[36:39],a[220:223],a[108:111],v[36:39]\n"
"v_fma_f32 v130,v130,s54,v14\n"
"v_fma_f32 v131,v131,s54,v14\n"
MF "v[44:47],a[220:223],a[112:115],v[44:47]\n"
"v_fma_f32 v132,v132,s54,v14\n"
"v_fma_f32 v133,v133,s54,v14\n"
MF "v[44:47],a[216:219],a[72:75],v[44:47]\n"
"v_fma_f32 v134,v134,s54,v14\n"
"v_fma_f32 v135,v135,s54,v14\n"
MF "v[52:55],a[216:219],a[76:79],v[52:55]\n"
CV "v195,v134,v135\n"
CV "v194,v132,v133\n"
MF "v[52:55],a[220:223],a[116:119],v[52:55]\n"
"s_waitcnt lgkmcnt(4)\n"
CV "v193,v130,v131\n"
CV "v192,v128,v129\n"
MF "v[20:23],a[200:203],a[140:143],v[20:23]\n"
TR "a[216:217],v0 offset:32768\n"
AW "a8,v192\n"
MF "v[20:23],a[204:207],a[180:183],v[20:23]\n"
TR "a[218:219],v0 offset:33280\n"
AW "a9,v193\n"
MF "v[28:31],a[204:207],a[184:187],v[28:31]\n"
AW "a10,v194\n"
AW "a11,v195\n"
MF "v[28:31],a[200:203],a[144:147],v[28:31]\n"
TR "a[220:221],v0 offset:36864\n"
"v_fma_f32 v120,v120,s54,v14\n"
MF "v[36:39],a[200:203],a[148:151],v[36:39]\n"
TR "a[222:223],v0 offset:37376\n"
"v_fma_f32 v121,v121,s54,v14\n"
MF "v[36:39],a[204:207],a[188:191],v[36:39]\n"
"v_fma_f32 v122,v122,s54,v14\n"
"v_fma_f32 v123,v123,s54,v14\n"
MF "v[44:47],a[204:207],a[192:195],v[44:47]\n"
"v_fma_f32 v124,v124,s54,v14\n"
"v_fma_f32 v125,v125,s54,v14\n"
MF "v[44:47],a[200:203],a[152:155],v[44:47]\n"
"v_fma_f32 v126,v126,s54,v14\n"
"v_fma_f32 v127,v127,s54,v14\n"
MF "v[52:55],a[200:203],a[156:159],v[52:55]\n"
"s_waitcnt lgkmcnt(0)\n"
CV "v191,v126,v127\n"
CV "v190,v124,v125\n"
MF "v[52:55],a[204:207],a[196:199],v[52:55]\n"
"s_barrier\n"
MF "v[16:19],a[208:211],a[60:63],v[16:19]\n"
CV "v189,v122,v123\n"
CV "v188,v120,v121\n"
MF "v[16:19],a[212:215],a[100:103],v[16:19]\n"
AW "a4,v188\n"
AW "a5,v189\n"
MF "v[24:27],a[212:215],a[104:107],v[24:27]\n"
AW "a6,v190\n"
AW "a7,v191\n"
MF "v[24:27],a[208:211],a[64:67],v[24:27]\n"
"v_fma_f32 v112,v112,s54,v14\n"
"v_fma_f32 v113,v113,s54,v14\n"
MF "v[32:35],a[208:211],a[68:71],v[32:35]\n"
"v_fma_f32 v114,v114,s54,v14\n"
"v_fma_f32 v115,v115,s54,v14\n"
MF "v[32:35],a[212:215],a[108:111],v[32:35]\n"
"v_fma_f32 v116,v116,s54,v14\n"
"v_fma_f32 v117,v117,s54,v14\n"
MF "v[40:43],a[212:215],a[112:115],v[40:43]\n"
"v_fma_f32 v118,v118,s54,v14\n"
"v_fma_f32 v119,v119,s54,v14\n"
MF "v[40:43],a[208:211],a[72:75],v[40:43]\n"
CV "v187,v118,v119\n"
CV "v186,v116,v117\n"
MF "v[48:51],a[208:211],a[76:79],v[48:51]\n"
CV "v185,v114,v115\n"
CV "v184,v112,v113\n"
MF "v[48:51],a[212:215],a[116:119],v[48:51]\n"
AW "a0,v184\n"
AW "a1,v185\n"
MF "v[48:51],a[216:219],a[156:159],v[48:51]\n"
AW "a2,v186\n"
AW "a3,v187\n"
MF "v[48:51],a[220:223],a[196:199],v[48:51]\n"
"v_fma_f32 v104,v104,s54,v14\n"
"v_fma_f32 v105,v105,s54,v14\n"
MF "v[40:43],a[220:223],a[192:195],v[40:43]\n"
"v_fma_f32 v106,v106,s54,v14\n"
"v_fma_f32 v107,v107,s54,v14\n"
MF "v[40:43],a[216:219],a[152:155],v[40:43]\n"
"v_fma_f32 v108,v108,s54,v14\n"
"v_fma_f32 v109,v109,s54,v14\n"
MF "v[32:35],a[216:219],a[148:151],v[32:35]\n"
"v_fma_f32 v110,v110,s54,v14\n"
"v_fma_f32 v111,v111,s54,v14\n"
MF "v[32:35],a[220:223],a[188:191],v[32:35]\n"
CV "v183,v110,v111\n"
CV "v182,v108,v109\n"
MF "v[24:27],a[220:223],a[184:187],v[24:27]\n"
CV "v181,v106,v107\n"
CV "v180,v104,v105\n"
MF "v[24:27],a[216:219],a[144:147],v[24:27]\n"
AW "a36,v180\n"
AW "a37,v181\n"
MF "v[16:19],a[216:219],a[140:143],v[16:19]\n"
AW "a38,v182\n"
AW "a39,v183\n"
MF "v[16:19],a[220:223],a[180:183],v[16:19]\n"
"s_mov_b32 s57,1\n"
"s_mov_b32 s69,99\n"
AR "v0,a240\n"
AR "v2,a241\n"
AR "v4,a242\n"
AR "v6,a243\n"
"v_xor_b32 v1,64,v0\n"
"v_xor_b32 v3,64,v2\n"
"v_xor_b32_e64 v5,64,v4\n"
"v_add_u32 v11,97280,v6\n"
"v_add_u32 v12,194560,v6\n"
"v_add_u32 v13,291840,v6\n"
"s_mov_b64 vcc,-1\n"
"s_waitcnt vmcnt(10) lgkmcnt(0)\n"
"s_barrier\n"
"s_nop 0\n"
TR "a[200:201],v0\n"
TR "a[202:203],v0 offset:512\n"
TR "a[204:205],v0 offset:4096\n"
TR "a[206:207],v0 offset:4608\n"
"ds_read_b128 a[60:63],v2\n"
"ds_read_b128 a[100:103],v3\n"
"ds_read_b128 a[64:67],v2 offset:2048\n"
"ds_read_b128 a[104:107],v3 offset:2048\n"
"ds_read_b128 a[68:71],v2 offset:4096\n"
"ds_read_b128 a[108:111],v3 offset:4096\n"
"ds_read_b128 a[72:75],v2 offset:6144\n"
"ds_read_b128 a[112:115],v3 offset:6144\n"
"ds_read_b128 a[76:79],v2 offset:8192\n"
"ds_read_b128 a[116:119],v3 offset:8192\n"
TR "a[208:209],v0 offset:8\n"
TR "a[210:211],v0 offset:520\n"
TR "a[212:213],v0 offset:4104\n"
TR "a[214:215],v0 offset:4616\n"
"v_pk_fma_f32 v[96:97],v[96:97],s[54:55],v[14:15] op_sel_hi:[1,1,0]\n"
"v_pk_fma_f32 v[98:99],v[98:99],s[54:55],v[14:15] op_sel_hi:[1,1,0]\n"
"v_pk_fma_f32 v[100:101],v[100:101],s[54:55],v[14:15] op_sel_hi:[1,1,0]\n"
"v_pk_fma_f32 v[102:103],v[102:103],s[54:55],v[14:15] op_sel_hi:[1,1,0]\n"
CV "v179,v102,v103\n"
CV "v178,v100,v101\n"
CV "v177,v98,v99\n"
CV "v176,v96,v97\n"
AW "a32,v176\n"
AW "a33,v177\n"
AW "a34,v178\n"
AW "a35,v179\n"
"v_pk_fma_f32 v[88:89],v[88:89],s[54:55],v[14:15] op_sel_hi:[1,1,0]\n"
"v_pk_fma_f32 v[90:91],v[90:91],s[54:55],v[14:15] op_sel_hi:[1,1,0]\n"
"v_pk_fma_f32 v[92:93],v[92:93],s[54:55],v[14:15] op_sel_hi:[1,1,0]\n"
".Lpg15c:\n"
"s_cbranch_execz .Lpg2a\n"
"s_nop 0\n"
"v_pk_fma_f32 v[94:95],v[94:95],s[54:55],v[14:15] op_sel_hi:[1,1,0]\n"
CV "v175,v94,v95\n"
CV "v174,v92,v93\n"
CV "v173,v90,v91\n"
CV "v172,v88,v89\n"
AW "a28,v172\n"
AW "a29,v173\n"
AW "a30,v174\n"
AW "a31,v175\n"
"v_pk_fma_f32 v[80:81],v[80:81],s[54:55],v[14:15] op_sel_hi:[1,1,0]\n"
"v_pk_fma_f32 v[82:83],v[82:83],s[54:55],v[14:15] op_sel_hi:[1,1,0]\n"
"v_pk_fma_f32 v[84:85],v[84:85],s[54:55],v[14:15] op_sel_hi:[1,1,0]\n"
"v_pk_fma_f32 v[86:87],v[86:87],s[54:55],v[14:15] op_sel_hi:[1,1,0]\n"
CV "v171,v86,v87\n"
CV "v170,v84,v85\n"
CV "v169,v82,v83\n"
CV "v168,v80,v81\n"
AW "a24,v168\n"
AW "a25,v169\n"
AW "a26,v170\n"
AW "a27,v171\n"
"v_pk_fma_f32 v[72:73],v[72:73],s[54:55],v[14:15] op_sel_hi:[1,1,0]\n"
"v_pk_fma_f32 v[74:75],v[74:75],s[54:55],v[14:15] op_sel_hi:[1,1,0]\n"
"v_pk_fma_f32 v[76:77],v[76:77],s[54:55],v[14:15] op_sel_hi:[1,1,0]\n"
"v_pk_fma_f32 v[78:79],v[78:79],s[54:55],v[14:15] op_sel_hi:[1,1,0]\n"
CV "v167,v78,v79\n"
CV "v166,v76,v77\n"
CV "v165,v74,v75\n"
CV "v164,v72,v73\n"
AW "a20,v164\n"
AW "a21,v165\n"
AW "a22,v166\n"
AW "a23,v167\n"
"v_pk_fma_f32 v[64:65],v[64:65],s[54:55],v[14:15] op_sel_hi:[1,1,0]\n"
"v_pk_fma_f32 v[66:67],v[66:67],s[54:55],v[14:15] op_sel_hi:[1,1,0]\n"
"v_pk_fma_f32 v[68:69],v[68:69],s[54:55],v[14:15] op_sel_hi:[1,1,0]\n"
"v_pk_fma_f32 v[70:71],v[70:71],s[54:55],v[14:15] op_sel_hi:[1,1,0]\n"
CV "v163,v70,v71\n"
CV "v162,v68,v69\n"
CV "v161,v66,v67\n"
CV "v160,v64,v65\n"
AW "a176,v160\n"
AW "a177,v161\n"
AW "a178,v162\n"
AW "a179,v163\n"
"v_pk_fma_f32 v[56:57],v[56:57],s[54:55],v[14:15] op_sel_hi:[1,1,0]\n"
"v_pk_fma_f32 v[58:59],v[58:59],s[54:55],v[14:15] op_sel_hi:[1,1,0]\n"
"v_pk_fma_f32 v[60:61],v[60:61],s[54:55],v[14:15] op_sel_hi:[1,1,0]\n"
"v_pk_fma_f32 v[62:63],v[62:63],s[54:55],v[14:15] op_sel_hi:[1,1,0]\n"
CV "v159,v62,v63\n"
CV "v158,v60,v61\n"
CV "v157,v58,v59\n"
CV "v156,v56,v57\n"
AW "a172,v156\n"
AW "a173,v157\n"
AW "a174,v158\n"
AW "a175,v159\n"
"v_pk_mul_f32 v[48:49],v[48:49],s[54:55]\n"
"v_pk_mul_f32 v[50:51],v[50:51],s[54:55]\n"
"v_pk_mul_f32 v[52:53],v[52:53],s[54:55]\n"
"v_pk_mul_f32 v[54:55],v[54:55],s[54:55]\n"
CV "v155,v54,v55\n"
CV "v154,v52,v53\n"
CV "v153,v50,v51\n"
CV "v152,v48,v49\n"
AW "a168,v152\n"
AW "a169,v153\n"
AW "a170,v154\n"
AW "a171,v155\n"
"v_pk_mul_f32 v[40:41],v[40:41],s[54:55]\n"
"v_pk_mul_f32 v[42:43],v[42:43],s[54:55]\n"
"v_pk_mul_f32 v[44:45],v[44:45],s[54:55]\n"
"v_pk_mul_f32 v[46:47],v[46:47],s[54:55]\n"
CV "v151,v46,v47\n"
CV "v150,v44,v45\n"
CV "v149,v42,v43\n"
CV "v148,v40,v41\n"
AW "a120,v148\n"
AW "a121,v149\n"
AW "a122,v150\n"
AW "a123,v151\n"
"v_pk_mul_f32 v[32:33],v[32:33],s[54:55]\n"
"v_pk_mul_f32 v[34:35],v[34:35],s[54:55]\n"
"v_pk_mul_f32 v[36:37],v[36:37],s[54:55]\n"
"v_pk_mul_f32 v[38:39],v[38:39],s[54:55]\n"
CV "v147,v38,v39\n"
CV "v146,v36,v37\n"
CV "v145,v34,v35\n"
CV "v144,v32,v33\n"
AW "a96,v144\n"
AW "a97,v145\n"
AW "a98,v146\n"
AW "a99,v147\n"
"v_pk_mul_f32 v[24:25],v[24:25],s[54:55]\n"
"v_pk_mul_f32 v[26:27],v[26:27],s[54:55]\n"
"v_pk_mul_f32 v[28:29],v[28:29],s[54:55]\n"
"v_pk_mul_f32 v[30:31],v[30:31],s[54:55]\n"
CV "v143,v30,v31\n"
CV "v142,v28,v29\n"
CV "v141,v26,v27\n"
CV "v140,v24,v25\n"
AW "a92,v140\n"
AW "a93,v141\n"
AW "a94,v142\n"
AW "a95,v143\n"
"v_pk_mul_f32 v[16:17],v[16:17],s[54:55]\n"
"v_pk_mul_f32 v[18:19],v[18:19],s[54:55]\n"
"v_pk_mul_f32 v[20:21],v[20:21],s[54:55]\n"
"v_pk_mul_f32 v[22:23],v[22:23],s[54:55]\n"
CV "v139,v22,v23\n"
CV "v138,v20,v21\n"
CV "v137,v18,v19\n"
CV "v136,v16,v17\n"
AW "a88,v136\n"
AW "a89,v137\n"
AW "a90,v138\n"
AW "a91,v139\n"
"s_waitcnt lgkmcnt(0)\n"
"s_branch .Lq0\n"
".Lpgpsl_b:\n"
"s_cbranch_execz .Lpg3a\n"
".Lpsl:\n"
"s_sleep 1\n"
".Lpsub:\n"
"s_memrealtime s[100:101]\n"
"s_waitcnt lgkmcnt(0)\n"
"s_sub_u32 s101,s21,s100\n"
"s_cmp_gt_i32 s101,0\n"
"s_cbranch_scc1 .Lpsl\n"
"s_add_u32 s21,s100,s97\n"
"s_setpc_b64 s[52:53]\n"
".Lstub_6:\n"
"v_cmp_le_f32 vcc,0x47800000,v10\n"
"s_cmp_eq_u32 s77,0\n"
"s_cbranch_scc0 .Lnop_6\n"
"s_call_b64 s[52:53],.Lpsub\n"
".Lnop_6:\n"
"s_cbranch_vccz .Lback_6\n"
"s_call_b64 s[52:53],.Lslow_1\n"
"s_branch .Lback_6\n"
".Lstub_7:\n"
"v_cmp_le_f32 vcc,0x47800000,v10\n"
"s_cmp_eq_u32 s77,0\n"
"s_cbranch_scc0 .Lnop_7\n"
"s_call_b64 s[52:53],.Lpsub\n"
"s_mul_i32 s58,s57,2\n"
"s_add_u32 s58,s58,s61\n"
"s_cmp_eq_u32 s58,2\n"
"s_cselect_b32 s77,0x47800000,s77\n"
"s_cselect_b32 s95,0x47800000,s95\n"
".Lnop_7:\n"
"s_cbranch_vccz .Lback_7\n"
"s_call_b64 s[52:53],.Lslow_0\n"
"s_branch .Lback_7\n"
".Lstub_8:\n"
"s_call_b64 s[52:53],.Lslow_1\n"
"s_branch .Lback_8\n"
".Lstub_9:\n"
"s_call_b64 s[52:53],.Lslow_0\n"
"s_branch .Lback_9\n"
".Lstub_10:\n"
"s_call_b64 s[52:53],.Lslow_1\n"
"s_branch .Lback_10\n"
".Lstub_11:\n"
"s_call_b64 s[52:53],.Lslow_1\n"
"s_branch .Lback_11\n"
".Lstub_12:\n"
"s_call_b64 s[52:53],.Lslow_0\n"
"s_branch .Lback_12\n"
".Lstub_13:\n"
"s_call_b64 s[52:53],.Lslow_1\n"
"s_branch .Lback_13\n"
".Lstub_20:\n"
"v_cmp_le_f32 vcc,0x47800000,v10\n"
"s_cmp_eq_u32 s77,0\n"
"s_cbranch_scc0 .Lnop_20\n"
"s_call_b64 s[52:53],.Lpsub\n"
".Lnop_20:\n"
"s_cbranch_vccz .Lback_20\n"
"s_call_b64 s[52:53],.Lslow_1\n"
"s_branch .Lback_20\n"
".Lstub_21:\n"
"v_cmp_le_f32 vcc,0x47800000,v10\n"
"s_cmp_eq_u32 s77,0\n"
"s_cbranch_scc0 .Lnop_21\n"
"s_call_b64 s[52:53],.Lpsub\n"
".Lnop_21:\n"
"s_cbranch_vccz .Lback_21\n"
"s_call_b64 s[52:53],.Lslow_0\n"
"s_branch .Lback_21\n"
".Lstub_22:\n"
"v_cmp_le_f32 vcc,0x47800000,v10\n"
"s_cmp_eq_u32 s77,0\n"
"s_cbranch_scc0 .Lnop_22\n"
"s_call_b64 s[52:53],.Lpsub\n"
".Lnop_22:\n"
"s_cbranch_vccz .Lback_22\n"
"s_call_b64 s[52:53],.Lslow_1\n"
"s_branch .Lback_22\n"
".Lstub_23:\n"
"v_cmp_le_f32 vcc,0x47800000,v10\n"
"s_cmp_eq_u32 s77,0\n"
"s_cbranch_scc0 .Lnop_23\n"
"s_call_b64 s[52:53],.Lpsub\n"
".Lnop_23:\n"
"s_cbranch_vccz .Lback_23\n"
"s_call_b64 s[52:53],.Lslow_0\n"
"s_branch .Lback_23\n"
".Lstub_24:\n"
"v_cmp_le_f32 vcc,0x47800000,v10\n"
"s_cmp_eq_u32 s61,1\n"
"s_cbranch_scc0 .Lhook_24\n"
"s_mov_b64 exec,0\n"
"s_cmp_eq_u32 s56,0\n"
"s_cbranch_scc1 .Lpg26h\n"
"s_cmp_eq_u32 s56,1\n"
"s_cbranch_scc1 .Lpg25h\n"
"s_cmp_eq_u32 s56,2\n"
"s_cbranch_scc1 .Lpg24h\n"
"s_branch .Lpg23h\n"
".Lisr4:\n"
"s_mov_b64 exec,-1\n"
".Lisx4:\n"
"s_and_b32 s58,s56,1\n"
"s_mul_i32 s79,s58,12288\n"
"s_lshl_b32 s15,s58,10\n"
"s_lshr_b32 s58,s56,1\n"
"s_mul_i32 s5,s58,6144\n"
"s_add_u32 s5,s5,s79\n"
"s_lshl_b32 s59,s58,8\n"
"s_add_u32 s15,s15,s59\n"
"s_add_u32 s15,s15,159744\n"
"s_add_u32 s88,s88,1024\n"
"s_add_u32 s89,s89,1024\n"
"s_mul_i32 s58,s58,61440\n"
"s_add_u32 s2,s89,s58\n"
"s_add_u32 s3,s88,s58\n"
"s_add_u32 s0,s2,122880\n"
"s_add_u32 s1,s3,122880\n"
"s_lshl_b32 s58,s56,10\n"
"s_add_u32 s9,s58,73728\n"
"s_add_u32 s14,s58,159744\n"
"s_bfe_u32 s4,s22,0x10014\n"
"s_lshl_b32 s4,s4,5\n"
"s_mov_b32 s6,896\n"
"s_and_b32 s58,s56,2\n"
"s_lshl_b32 s58,s58,5\n"
"s_or_b32 s4,s4,s58\n"
".Lhook_24:\n"
"s_cmp_eq_u32 s77,0\n"
"s_cbranch_scc0 .Lnop_24\n"
"s_call_b64 s[52:53],.Lpsub\n"
".Lnop_24:\n"
"s_cbranch_vccz .Lback_24\n"
"s_call_b64 s[52:53],.Lslow_1\n"
"s_branch .Lback_24\n"
".Lstub_25:\n"
"v_cmp_le_f32 vcc,0x47800000,v10\n"
"s_cmp_eq_u32 s77,0\n"
"s_cbranch_scc0 .Lnop_25\n"
"s_call_b64 s[52:53],.Lpsub\n"
".Lnop_25:\n"
"s_cbranch_vccz .Lback_25\n"
"s_call_b64 s[52:53],.Lslow_0\n"
"s_branch .Lback_25\n"
".Lstub_26:\n"
"s_call_b64 s[52:53],.Lslow_1\n"
"s_branch .Lback_26\n"
".Lstub_27:\n"
"s_call_b64 s[52:53],.Lslow_0\n"
"s_branch .Lback_27\n"
".Lstub_28:\n"
"s_call_b64 s[52:53],.Lslow_1\n"
"s_branch .Lback_28\n"
".Lackspin_q:\n"
"s_cmp_eq_u32 s98,0\n"
"s_cbranch_scc1 .Lackok_q\n"
"s_sub_u32 s98,s98,1\n"
"s_sleep 8\n"
"v_bfe_u32 v14,v9,4,5\n"
"v_lshlrev_b32 v14,6,v14\n"
"s_lshl_b32 s58,s78,2\n"
"s_add_u32 s58,s58,s86\n"
"buffer_load_dword v10,v14,s[40:43],s58 offen offset:128 sc1\n"
"s_waitcnt vmcnt(0)\n"
"s_branch .Lack_q\n"
".Lpgep_g:\n"
"s_cbranch_execz .Lpgt0e_g\n"
".Lep:\n"
MF "v[72:75],v[16:19],v[136:139],v[32:35]\n"
"s_nop 0\n"
"ds_read_b128 v[24:27],v0 offset:1024\n"
"ds_read_b128 v[28:31],v1 offset:1024\n"
MF "v[72:75],v[20:23],v[156:159],v[72:75]\n"
MF "v[76:79],v[20:23],v[160:163],v[36:39]\n"
MF "v[76:79],v[16:19],v[140:143],v[76:79]\n"
MF "v[80:83],v[16:19],v[144:147],v[40:43]\n"
MF "v[80:83],v[20:23],v[164:167],v[80:83]\n"
MF "v[84:87],v[20:23],v[168:171],v[44:47]\n"
MF "v[84:87],v[16:19],v[148:151],v[84:87]\n"
"s_add_u32 m0,s65,49152\n"
MF "v[88:91],v[16:19],v[152:155],v[48:51]\n"
LD "v8,s[36:39],s73 offen offset:3072 nt lds\n"
MF "v[88:91],v[20:23],v[172:175],v[88:91]\n"
"s_waitcnt lgkmcnt(0)\n"
"ds_read_b128 v[16:19],v0 offset:2048\n"
"ds_read_b128 v[20:23],v1 offset:2048\n"
MF "v[88:91],v[24:27],v[192:195],v[88:91]\n"
"v_exp_f32 v124,v124\n"
MF "v[88:91],v[28:31],v[212:215],v[88:91]\n"
"s_add_u32 m0,s65,50048\n"
"v_exp_f32 v125,v125\n"
MF "v[84:87],v[28:31],v[208:211],v[84:87]\n"
LD "v8,s[36:39],s73 offen offset:3200 nt lds\n"
MF "v[84:87],v[24:27],v[188:191],v[84:87]\n"
"v_exp_f32 v126,v126\n"
MF "v[80:83],v[24:27],v[184:187],v[80:83]\n"
"v_exp_f32 v127,v127\n"
MF "v[80:83],v[28:31],v[204:207],v[80:83]\n"
"s_add_u32 m0,s65,50944\n"
"v_add_f32 v60,v60,v124\n"
"v_add_f32 v60,v60,v125\n"
MF "v[76:79],v[28:31],v[200:203],v[76:79]\n"
LD "v8,s[36:39],s73 offen offset:3328 nt lds\n"
MF "v[76:79],v[24:27],v[180:183],v[76:79]\n"
CV "v106,v124,v125\n"
"v_add_f32 v60,v60,v126\n"
MF "v[72:75],v[24:27],v[176:179],v[72:75]\n"
"v_add_f32 v60,v60,v127\n"
CV "v107,v126,v127\n"
MF "v[72:75],v[28:31],v[196:199],v[72:75]\n"
"s_waitcnt lgkmcnt(0)\n"
"ds_read_b128 v[24:27],v0 offset:6144\n"
"ds_read_b128 v[28:31],v1 offset:6144\n"
MF "v[72:75],v[16:19],v[216:219],v[72:75]\n"
"v_exp_f32 v128,v128\n"
MF "v[72:75],v[20:23],v[236:239],v[72:75]\n"
"v_exp_f32 v129,v129\n"
MF "v[76:79],v[20:23],v[240:243],v[76:79]\n"
"v_exp_f32 v130,v130\n"
MF "v[76:79],v[16:19],v[220:223],v[76:79]\n"
"v_exp_f32 v131,v131\n"
MF "v[80:83],v[16:19],v[224:227],v[80:83]\n"
"v_add_f32 v61,v61,v128\n"
"v_add_f32 v61,v61,v129\n"
MF "v[80:83],v[20:23],v[244:247],v[80:83]\n"
CV "v110,v128,v129\n"
"v_add_f32 v61,v61,v130\n"
MF "v[84:87],v[20:23],v[248:251],v[84:87]\n"
"v_add_f32 v61,v61,v131\n"
CV "v111,v130,v131\n"
MF "v[84:87],v[16:19],v[228:231],v[84:87]\n"
"v_max3_f32 v10,v57,v58,v59\n"
"v_max3_f32 v10,v10,v60,v61\n"
MF "v[88:91],v[16:19],v[232:235],v[88:91]\n"
"v_cmp_le_f32 vcc,0x47800000,v10\n"
MF "v[88:91],v[20:23],v[252:255],v[88:91]\n"
"s_waitcnt lgkmcnt(0)\n"
"s_cbranch_vccnz .Lstub_11\n"
".Lback_11:\n"
MF "v[112:115],v[24:27],v[136:139],v[32:35]\n"
"s_add_u32 s74,s74,12288\n"
"ds_read_b128 v[16:19],v0 offset:7168\n"
"ds_read_b128 v[20:23],v1 offset:7168\n"
MF "v[112:115],v[28:31],v[156:159],v[112:115]\n"
"s_cmp_eq_u32 s74,s20\n"
"s_cselect_b32 s74,s70,s74\n"
MF "v[116:119],v[28:31],v[160:163],v[36:39]\n"
"v_exp_f32 v72,v72\n"
MF "v[116:119],v[24:27],v[140:143],v[116:119]\n"
"v_exp_f32 v73,v73\n"
MF "v[120:123],v[24:27],v[144:147],v[40:43]\n"
"v_exp_f32 v74,v74\n"
MF "v[120:123],v[28:31],v[164:167],v[120:123]\n"
"v_exp_f32 v75,v75\n"
MF "v[124:127],v[28:31],v[168:171],v[44:47]\n"
"v_add_f32 v52,v57,v72\n"
"v_add_f32 v52,v52,v73\n"
MF "v[124:127],v[24:27],v[148:151],v[124:127]\n"
CV "v72,v72,v73\n"
"v_add_f32 v52,v52,v74\n"
MF "v[128:131],v[24:27],v[152:155],v[48:51]\n"
"v_add_f32 v52,v52,v75\n"
CV "v73,v74,v75\n"
MF "v[128:131],v[28:31],v[172:175],v[128:131]\n"
"s_waitcnt lgkmcnt(0)\n"
"ds_read_b128 v[24:27],v0 offset:8192\n"
"ds_read_b128 v[28:31],v1 offset:8192\n"
MF "v[128:131],v[16:19],v[192:195],v[128:131]\n"
"v_exp_f32 v76,v76\n"
MF "v[128:131],v[20:23],v[212:215],v[128:131]\n"
"v_exp_f32 v77,v77\n"
MF "v[124:127],v[20:23],v[208:211],v[124:127]\n"
"v_exp_f32 v78,v78\n"
MF "v[124:127],v[16:19],v[188:191],v[124:127]\n"
"v_exp_f32 v79,v79\n"
MF "v[120:123],v[16:19],v[184:187],v[120:123]\n"
"v_add_f32 v53,v58,v76\n"
"v_add_f32 v53,v53,v77\n"
MF "v[120:123],v[20:23],v[204:207],v[120:123]\n"
CV "v76,v76,v77\n"
"v_add_f32 v53,v53,v78\n"
MF "v[116:119],v[20:23],v[200:203],v[116:119]\n"
"s_waitcnt vmcnt(12) lgkmcnt(0)\n"
"v_add_f32 v53,v53,v79\n"
CV "v77,v78,v79\n"
MF "v[116:119],v[16:19],v[180:183],v[116:119]\n"
"s_barrier\n"
MF "v[112:115],v[16:19],v[176:179],v[112:115]\n"
"v_exp_f32 v80,v80\n"
MF "v[112:115],v[20:23],v[196:199],v[112:115]\n"
"s_sub_u32 s58,s71,s70\n"
TR "v[16:17],v2 offset:24576\n"
TR "v[18:19],v2 offset:30720\n"
MF "v[112:115],v[24:27],v[216:219],v[112:115]\n"
"s_add_u32 s72,s72,s58\n"
TR "v[20:21],v3 offset:24576\n"
TR "v[22:23],v3 offset:30720\n"
MF "v[112:115],v[28:31],v[236:239],v[112:115]\n"
"s_add_u32 s16,s16,s58\n"
"s_add_u32 s17,s17,s58\n"
"s_add_u32 s72,s72,6144\n"
MF "v[116:119],v[28:31],v[240:243],v[116:119]\n"
"s_cmp_eq_u32 s72,s17\n"
"s_cselect_b32 s72,s16,s72\n"
"s_add_u32 m0,s64,0\n"
MF "v[116:119],v[24:27],v[220:223],v[116:119]\n"
LD "v7,s[32:35],s72 offen nt lds\n"
MF "v[120:123],v[24:27],v[224:227],v[120:123]\n"
"s_add_u32 m0,s64,896\n"
"v_exp_f32 v81,v81\n"
MF "v[120:123],v[28:31],v[244:247],v[120:123]\n"
LD "v7,s[32:35],s72 offen offset:128 nt lds\n"
MF "v[124:127],v[28:31],v[248:251],v[124:127]\n"
"s_add_u32 m0,s64,1792\n"
"v_exp_f32 v82,v82\n"
MF "v[124:127],v[24:27],v[228:231],v[124:127]\n"
LD "v7,s[32:35],s72 offen offset:256 nt lds\n"
MF "v[128:131],v[24:27],v[232:235],v[128:131]\n"
"s_add_u32 s72,s72,6144\n"
"v_exp_f32 v83,v83\n"
MF "v[128:131],v[28:31],v[252:255],v[128:131]\n"
"s_waitcnt lgkmcnt(0)\n"
TR "v[24:25],v4 offset:24576\n"
TR "v[26:27],v4 offset:30720\n"
MF "a[0:3],v[16:19],v[92:95],a[0:3]\n"
"s_add_u32 m0,s64,6144\n"
TR "v[28:29],v5 offset:24576\n"
TR "v[30:31],v5 offset:30720\n"
MF "a[20:23],v[20:23],v[92:95],a[20:23]\n"
LD "v7,s[32:35],s72 offen nt lds\n"
MF "a[24:27],v[20:23],v[96:99],a[24:27]\n"
"v_add_f32 v54,v59,v80\n"
"v_add_f32 v54,v54,v81\n"
MF "a[4:7],v[16:19],v[96:99],a[4:7]\n"
"s_add_u32 m0,s64,7040\n"
CV "v80,v80,v81\n"
"v_add_f32 v54,v54,v82\n"
MF "a[8:11],v[16:19],v[100:103],a[8:11]\n"
LD "v7,s[32:35],s72 offen offset:128 nt lds\n"
MF "a[28:31],v[20:23],v[100:103],a[28:31]\n"
"v_add_f32 v54,v54,v83\n"
CV "v81,v82,v83\n"
MF "a[32:35],v[20:23],v[104:107],a[32:35]\n"
"s_add_u32 m0,s64,7936\n"
"v_exp_f32 v84,v84\n"
MF "a[12:15],v[16:19],v[104:107],a[12:15]\n"
LD "v7,s[32:35],s72 offen offset:256 nt lds\n"
MF "a[16:19],v[16:19],v[108:111],a[16:19]\n"
"v_exp_f32 v85,v85\n"
MF "a[36:39],v[20:23],v[108:111],a[36:39]\n"
"s_waitcnt lgkmcnt(0)\n"
TR "v[16:17],v2 offset:25600\n"
TR "v[18:19],v2 offset:31744\n"
MF "a[56:59],v[24:27],v[108:111],a[56:59]\n"
"s_nop 0\n"
TR "v[20:21],v3 offset:25600\n"
TR "v[22:23],v3 offset:31744\n"
MF "a[76:79],v[28:31],v[108:111],a[76:79]\n"
"v_exp_f32 v86,v86\n"
MF "a[72:75],v[28:31],v[104:107],a[72:75]\n"
"v_exp_f32 v87,v87\n"
MF "a[52:55],v[24:27],v[104:107],a[52:55]\n"
"v_add_f32 v55,v60,v84\n"
"v_add_f32 v55,v55,v85\n"
MF "a[48:51],v[24:27],v[100:103],a[48:51]\n"
CV "v84,v84,v85\n"
"v_add_f32 v55,v55,v86\n"
MF "a[68:71],v[28:31],v[100:103],a[68:71]\n"
"v_add_f32 v55,v55,v87\n"
CV "v85,v86,v87\n"
MF "a[64:67],v[28:31],v[96:99],a[64:67]\n"
"v_exp_f32 v88,v88\n"
MF "a[44:47],v[24:27],v[96:99],a[44:47]\n"
"v_exp_f32 v89,v89\n"
MF "a[40:43],v[24:27],v[92:95],a[40:43]\n"
"v_exp_f32 v90,v90\n"
MF "a[60:63],v[28:31],v[92:95],a[60:63]\n"
"s_waitcnt lgkmcnt(0)\n"
TR "v[24:25],v4 offset:25600\n"
TR "v[26:27],v4 offset:31744\n"
MF "a[80:83],v[16:19],v[92:95],a[80:83]\n"
"s_nop 0\n"
TR "v[28:29],v5 offset:25600\n"
TR "v[30:31],v5 offset:31744\n"
MF "a[100:103],v[20:23],v[92:95],a[100:103]\n"
"v_exp_f32 v91,v91\n"
MF "a[104:107],v[20:23],v[96:99],a[104:107]\n"
"v_add_f32 v56,v61,v88\n"
"v_add_f32 v56,v56,v89\n"
MF "a[84:87],v[16:19],v[96:99],a[84:87]\n"
CV "v88,v88,v89\n"
"v_add_f32 v56,v56,v90\n"
MF "a[88:91],v[16:19],v[100:103],a[88:91]\n"
"v_add_f32 v56,v56,v91\n"
CV "v89,v90,v91\n"
MF "a[108:111],v[20:23],v[100:103],a[108:111]\n"
"v_exp_f32 v112,v112\n"
MF "a[112:115],v[20:23],v[104:107],a[112:115]\n"
"v_exp_f32 v113,v113\n"
MF "a[92:95],v[16:19],v[104:107],a[92:95]\n"
"v_exp_f32 v114,v114\n"
MF "a[96:99],v[16:19],v[108:111],a[96:99]\n"
"v_exp_f32 v115,v115\n"
MF "a[116:119],v[20:23],v[108:111],a[116:119]\n"
"s_waitcnt lgkmcnt(0)\n"
TR "v[16:17],v2 offset:26624\n"
TR "v[18:19],v2 offset:32768\n"
MF "a[136:139],v[24:27],v[108:111],a[136:139]\n"
"s_nop 0\n"
TR "v[20:21],v3 offset:26624\n"
TR "v[22:23],v3 offset:32768\n"
MF "a[156:159],v[28:31],v[108:111],a[156:159]\n"
"v_add_f32 v52,v52,v112\n"
"v_add_f32 v52,v52,v113\n"
MF "a[152:155],v[28:31],v[104:107],a[152:155]\n"
CV "v74,v112,v113\n"
"v_add_f32 v52,v52,v114\n"
MF "a[132:135],v[24:27],v[104:107],a[132:135]\n"
"v_add_f32 v52,v52,v115\n"
CV "v75,v114,v115\n"
MF "a[128:131],v[24:27],v[100:103],a[128:131]\n"
"v_exp_f32 v116,v116\n"
MF "a[148:151],v[28:31],v[100:103],a[148:151]\n"
"v_exp_f32 v117,v117\n"
MF "a[144:147],v[28:31],v[96:99],a[144:147]\n"
"v_exp_f32 v118,v118\n"
MF "a[124:127],v[24:27],v[96:99],a[124:127]\n"
"v_exp_f32 v119,v119\n"
MF "a[120:123],v[24:27],v[92:95],a[120:123]\n"
"v_add_f32 v53,v53,v116\n"
"v_add_f32 v53,v53,v117\n"
MF "a[140:143],v[28:31],v[92:95],a[140:143]\n"
"s_waitcnt lgkmcnt(0)\n"
TR "v[24:25],v4 offset:26624\n"
TR "v[26:27],v4 offset:32768\n"
MF "a[160:163],v[16:19],v[92:95],a[160:163]\n"
"s_nop 0\n"
TR "v[28:29],v5 offset:26624\n"
TR "v[30:31],v5 offset:32768\n"
MF "a[180:183],v[20:23],v[92:95],a[180:183]\n"
CV "v78,v116,v117\n"
"v_add_f32 v53,v53,v118\n"
MF "a[184:187],v[20:23],v[96:99],a[184:187]\n"
"v_add_f32 v53,v53,v119\n"
CV "v79,v118,v119\n"
MF "a[164:167],v[16:19],v[96:99],a[164:167]\n"
"v_exp_f32 v120,v120\n"
MF "a[168:171],v[16:19],v[100:103],a[168:171]\n"
"v_exp_f32 v121,v121\n"
MF "a[188:191],v[20:23],v[100:103],a[188:191]\n"
"v_exp_f32 v122,v122\n"
MF "a[192:195],v[20:23],v[104:107],a[192:195]\n"
"v_exp_f32 v123,v123\n"
MF "a[172:175],v[16:19],v[104:107],a[172:175]\n"
"s_waitcnt vmcnt(12) lgkmcnt(0)\n"
"v_add_f32 v54,v54,v120\n"
"v_add_f32 v54,v54,v121\n"
MF "a[176:179],v[16:19],v[108:111],a[176:179]\n"
"s_barrier\n"
MF "a[196:199],v[20:23],v[108:111],a[196:199]\n"
"s_add_u32 s73,s73,12288\n"
"ds_read_b128 v[16:19],v0 offset:24576\n"
"ds_read_b128 v[20:23],v1 offset:24576\n"
MF "a[216:219],v[24:27],v[108:111],a[216:219]\n"
"s_cmp_eq_u32 s73,s19\n"
"s_cselect_b32 s73,s18,s73\n"
"s_add_u32 m0,s65,73728\n"
MF "a[236:239],v[28:31],v[108:111],a[236:239]\n"
LD "v8,s[36:39],s73 offen nt lds\n"
MF "a[232:235],v[28:31],v[104:107],a[232:235]\n"
"s_add_u32 m0,s65,74624\n"
CV "v82,v120,v121\n"
"v_add_f32 v54,v54,v122\n"
MF "a[212:215],v[24:27],v[104:107],a[212:215]\n"
LD "v8,s[36:39],s73 offen offset:128 nt lds\n"
MF "a[208:211],v[24:27],v[100:103],a[208:211]\n"
"s_add_u32 m0,s65,75520\n"
"v_add_f32 v54,v54,v123\n"
CV "v83,v122,v123\n"
MF "a[228:231],v[28:31],v[100:103],a[228:231]\n"
LD "v8,s[36:39],s73 offen offset:256 nt lds\n"
MF "a[224:227],v[28:31],v[96:99],a[224:227]\n"
MF "a[204:207],v[24:27],v[96:99],a[204:207]\n"
MF "a[200:203],v[24:27],v[92:95],a[200:203]\n"
MF "a[220:223],v[28:31],v[92:95],a[220:223]\n"
"s_waitcnt lgkmcnt(0)\n"
MF "v[92:95],v[16:19],v[136:139],v[32:35]\n"
"s_add_u32 m0,s65,73728\n"
"ds_read_b128 v[24:27],v0 offset:25600\n"
"ds_read_b128 v[28:31],v1 offset:25600\n"
MF "v[92:95],v[20:23],v[156:159],v[92:95]\n"
LD "v8,s[36:39],s73 offen offset:3072 nt lds\n"
MF "v[96:99],v[20:23],v[160:163],v[36:39]\n"
"v_exp_f32 v124,v124\n"
MF "v[96:99],v[16:19],v[140:143],v[96:99]\n"
"v_exp_f32 v125,v125\n"
MF "v[100:103],v[16:19],v[144:147],v[40:43]\n"
"s_add_u32 m0,s65,74624\n"
"v_exp_f32 v126,v126\n"
MF "v[100:103],v[20:23],v[164:167],v[100:103]\n"
LD "v8,s[36:39],s73 offen offset:3200 nt lds\n"
MF "v[104:107],v[20:23],v[168:171],v[44:47]\n"
"v_exp_f32 v127,v127\n"
MF "v[104:107],v[16:19],v[148:151],v[104:107]\n"
"s_add_u32 m0,s65,75520\n"
"v_add_f32 v55,v55,v124\n"
"v_add_f32 v55,v55,v125\n"
MF "v[108:111],v[16:19],v[152:155],v[48:51]\n"
LD "v8,s[36:39],s73 offen offset:3328 nt lds\n"
MF "v[108:111],v[20:23],v[172:175],v[108:111]\n"
"s_waitcnt lgkmcnt(0)\n"
"ds_read_b128 v[16:19],v0 offset:26624\n"
"ds_read_b128 v[20:23],v1 offset:26624\n"
MF "v[108:111],v[24:27],v[192:195],v[108:111]\n"
CV "v86,v124,v125\n"
"v_add_f32 v55,v55,v126\n"
MF "v[108:111],v[28:31],v[212:215],v[108:111]\n"
"v_add_f32 v55,v55,v127\n"
CV "v87,v126,v127\n"
MF "v[104:107],v[28:31],v[208:211],v[104:107]\n"
"v_exp_f32 v128,v128\n"
MF "v[104:107],v[24:27],v[188:191],v[104:107]\n"
"v_exp_f32 v129,v129\n"
MF "v[100:103],v[24:27],v[184:187],v[100:103]\n"
"v_exp_f32 v130,v130\n"
MF "v[100:103],v[28:31],v[204:207],v[100:103]\n"
"v_exp_f32 v131,v131\n"
MF "v[96:99],v[28:31],v[200:203],v[96:99]\n"
"v_add_f32 v56,v56,v128\n"
"v_add_f32 v56,v56,v129\n"
MF "v[96:99],v[24:27],v[180:183],v[96:99]\n"
CV "v90,v128,v129\n"
"v_add_f32 v56,v56,v130\n"
MF "v[92:95],v[24:27],v[176:179],v[92:95]\n"
"v_add_f32 v56,v56,v131\n"
CV "v91,v130,v131\n"
MF "v[92:95],v[28:31],v[196:199],v[92:95]\n"
"s_waitcnt lgkmcnt(0)\n"
"ds_read_b128 v[24:27],v0 offset:30720\n"
"ds_read_b128 v[28:31],v1 offset:30720\n"
MF "v[92:95],v[16:19],v[216:219],v[92:95]\n"
MF "v[92:95],v[20:23],v[236:239],v[92:95]\n"
MF "v[96:99],v[20:23],v[240:243],v[96:99]\n"
MF "v[96:99],v[16:19],v[220:223],v[96:99]\n"
MF "v[100:103],v[16:19],v[224:227],v[100:103]\n"
MF "v[100:103],v[20:23],v[244:247],v[100:103]\n"
".Lpg16g:\n"
"s_cbranch_execz .Lpg19g\n"
MF "v[104:107],v[20:23],v[248:251],v[104:107]\n"
MF "v[104:107],v[16:19],v[228:231],v[104:107]\n"
"v_max3_f32 v10,v52,v53,v54\n"
"v_max3_f32 v10,v10,v55,v56\n"
MF "v[108:111],v[16:19],v[232:235],v[108:111]\n"
"v_cmp_le_f32 vcc,0x47800000,v10\n"
MF "v[108:111],v[20:23],v[252:255],v[108:111]\n"
"s_waitcnt lgkmcnt(0)\n"
"s_cbranch_vccnz .Lstub_12\n"
".Lback_12:\n"
MF "v[112:115],v[24:27],v[136:139],v[32:35]\n"
"s_add_u32 s74,s74,12288\n"
"ds_read_b128 v[16:19],v0 offset:31744\n"
"ds_read_b128 v[20:23],v1 offset:31744\n"
MF "v[112:115],v[28:31],v[156:159],v[112:115]\n"
"s_cmp_eq_u32 s74,s20\n"
"s_cselect_b32 s74,s70,s74\n"
MF "v[116:119],v[28:31],v[160:163],v[36:39]\n"
"v_exp_f32 v92,v92\n"
MF "v[116:119],v[24:27],v[140:143],v[116:119]\n"
"v_exp_f32 v93,v93\n"
MF "v[120:123],v[24:27],v[144:147],v[40:43]\n"
"v_exp_f32 v94,v94\n"
MF "v[120:123],v[28:31],v[164:167],v[120:123]\n"
"v_exp_f32 v95,v95\n"
MF "v[124:127],v[28:31],v[168:171],v[44:47]\n"
"v_add_f32 v57,v52,v92\n"
"v_add_f32 v57,v57,v93\n"
MF "v[124:127],v[24:27],v[148:151],v[124:127]\n"
CV "v92,v92,v93\n"
"v_add_f32 v57,v57,v94\n"
MF "v[128:131],v[24:27],v[152:155],v[48:51]\n"
"v_add_f32 v57,v57,v95\n"
CV "v93,v94,v95\n"
MF "v[128:131],v[28:31],v[172:175],v[128:131]\n"
"s_waitcnt lgkmcnt(0)\n"
"ds_read_b128 v[24:27],v0 offset:32768\n"
"ds_read_b128 v[28:31],v1 offset:32768\n"
MF "v[128:131],v[16:19],v[192:195],v[128:131]\n"
"v_exp_f32 v96,v96\n"
MF "v[128:131],v[20:23],v[212:215],v[128:131]\n"
"v_exp_f32 v97,v97\n"
MF "v[124:127],v[20:23],v[208:211],v[124:127]\n"
"v_exp_f32 v98,v98\n"
MF "v[124:127],v[16:19],v[188:191],v[124:127]\n"
"v_exp_f32 v99,v99\n"
MF "v[120:123],v[16:19],v[184:187],v[120:123]\n"
"v_add_f32 v58,v53,v96\n"
"v_add_f32 v58,v58,v97\n"
MF "v[120:123],v[20:23],v[204:207],v[120:123]\n"
CV "v96,v96,v97\n"
"v_add_f32 v58,v58,v98\n"
MF "v[116:119],v[20:23],v[200:203],v[116:119]\n"
"s_waitcnt vmcnt(12) lgkmcnt(0)\n"
"v_add_f32 v58,v58,v99\n"
CV "v97,v98,v99\n"
MF "v[116:119],v[16:19],v[180:183],v[116:119]\n"
"s_barrier\n"
MF "v[112:115],v[16:19],v[176:179],v[112:115]\n"
"v_exp_f32 v100,v100\n"
MF "v[112:115],v[20:23],v[196:199],v[112:115]\n"
"s_nop 0\n"
TR "v[16:17],v2\n"
TR "v[18:19],v2 offset:6144\n"
MF "v[112:115],v[24:27],v[216:219],v[112:115]\n"
"s_add_u32 s72,s72,6144\n"
TR "v[20:21],v3\n"
TR "v[22:23],v3 offset:6144\n"
MF "v[112:115],v[28:31],v[236:239],v[112:115]\n"
"s_cmp_eq_u32 s72,s17\n"
"s_cselect_b32 s72,s16,s72\n"
"s_add_u32 m0,s64,24576\n"
MF "v[116:119],v[28:31],v[240:243],v[116:119]\n"
LD "v7,s[32:35],s72 offen nt lds\n"
MF "v[116:119],v[24:27],v[220:223],v[116:119]\n"
"s_add_u32 m0,s64,25472\n"
"v_exp_f32 v101,v101\n"
MF "v[120:123],v[24:27],v[224:227],v[120:123]\n"
LD "v7,s[32:35],s72 offen offset:128 nt lds\n"
MF "v[120:123],v[28:31],v[244:247],v[120:123]\n"
"v_exp_f32 v102,v102\n"
MF "v[124:127],v[28:31],v[248:251],v[124:127]\n"
"s_add_u32 m0,s64,26368\n"
"v_exp_f32 v103,v103\n"
MF "v[124:127],v[24:27],v[228:231],v[124:127]\n"
LD "v7,s[32:35],s72 offen offset:256 nt lds\n"
MF "v[128:131],v[24:27],v[232:235],v[128:131]\n"
"s_add_u32 s72,s72,6144\n"
"v_add_f32 v59,v54,v100\n"
"v_add_f32 v59,v59,v101\n"
MF "v[128:131],v[28:31],v[252:255],v[128:131]\n"
"s_waitcnt lgkmcnt(0)\n"
TR "v[24:25],v4\n"
TR "v[26:27],v4 offset:6144\n"
MF "a[0:3],v[16:19],v[72:75],a[0:3]\n"
"s_add_u32 m0,s64,30720\n"
TR "v[28:29],v5\n"
TR "v[30:31],v5 offset:6144\n"
MF "a[20:23],v[20:23],v[72:75],a[20:23]\n"
LD "v7,s[32:35],s72 offen nt lds\n"
MF "a[24:27],v[20:23],v[76:79],a[24:27]\n"
CV "v100,v100,v101\n"
"v_add_f32 v59,v59,v102\n"
MF "a[4:7],v[16:19],v[76:79],a[4:7]\n"
"s_add_u32 m0,s64,31616\n"
"v_add_f32 v59,v59,v103\n"
CV "v101,v102,v103\n"
MF "a[8:11],v[16:19],v[80:83],a[8:11]\n"
LD "v7,s[32:35],s72 offen offset:128 nt lds\n"
MF "a[28:31],v[20:23],v[80:83],a[28:31]\n"
"v_exp_f32 v104,v104\n"
MF "a[32:35],v[20:23],v[84:87],a[32:35]\n"
"s_add_u32 m0,s64,32512\n"
"v_exp_f32 v105,v105\n"
MF "a[12:15],v[16:19],v[84:87],a[12:15]\n"
LD "v7,s[32:35],s72 offen offset:256 nt lds\n"
MF "a[16:19],v[16:19],v[88:91],a[16:19]\n"
"v_exp_f32 v106,v106\n"
MF "a[36:39],v[20:23],v[88:91],a[36:39]\n"
"s_waitcnt lgkmcnt(0)\n"
TR "v[16:17],v2 offset:1024\n"
TR "v[18:19],v2 offset:7168\n"
MF "a[56:59],v[24:27],v[88:91],a[56:59]\n"
"s_nop 0\n"
TR "v[20:21],v3 offset:1024\n"
TR "v[22:23],v3 offset:7168\n"
MF "a[76:79],v[28:31],v[88:91],a[76:79]\n"
"v_exp_f32 v107,v107\n"
MF "a[72:75],v[28:31],v[84:87],a[72:75]\n"
"v_add_f32 v60,v55,v104\n"
"v_add_f32 v60,v60,v105\n"
MF "a[52:55],v[24:27],v[84:87],a[52:55]\n"
CV "v104,v104,v105\n"
"v_add_f32 v60,v60,v106\n"
MF "a[48:51],v[24:27],v[80:83],a[48:51]\n"
"v_add_f32 v60,v60,v107\n"
CV "v105,v106,v107\n"
MF "a[68:71],v[28:31],v[80:83],a[68:71]\n"
"v_exp_f32 v108,v108\n"
MF "a[64:67],v[28:31],v[76:79],a[64:67]\n"
"v_exp_f32 v109,v109\n"
MF "a[44:47],v[24:27],v[76:79],a[44:47]\n"
"v_exp_f32 v110,v110\n"
MF "a[40:43],v[24:27],v[72:75],a[40:43]\n"
"v_exp_f32 v111,v111\n"
MF "a[60:63],v[28:31],v[72:75],a[60:63]\n"
"s_waitcnt lgkmcnt(0)\n"
TR "v[24:25],v4 offset:1024\n"
TR "v[26:27],v4 offset:7168\n"
MF "a[80:83],v[16:19],v[72:75],a[80:83]\n"
"s_nop 0\n"
TR "v[28:29],v5 offset:1024\n"
TR "v[30:31],v5 offset:7168\n"
MF "a[100:103],v[20:23],v[72:75],a[100:103]\n"
"v_add_f32 v61,v56,v108\n"
"v_add_f32 v61,v61,v109\n"
MF "a[104:107],v[20:23],v[76:79],a[104:107]\n"
CV "v108,v108,v109\n"
"v_add_f32 v61,v61,v110\n"
MF "a[84:87],v[16:19],v[76:79],a[84:87]\n"
"v_add_f32 v61,v61,v111\n"
CV "v109,v110,v111\n"
MF "a[88:91],v[16:19],v[80:83],a[88:91]\n"
"v_exp_f32 v112,v112\n"
MF "a[108:111],v[20:23],v[80:83],a[108:111]\n"
"v_exp_f32 v113,v113\n"
MF "a[112:115],v[20:23],v[84:87],a[112:115]\n"
"v_exp_f32 v114,v114\n"
MF "a[92:95],v[16:19],v[84:87],a[92:95]\n"
"v_exp_f32 v115,v115\n"
MF "a[96:99],v[16:19],v[88:91],a[96:99]\n"
"v_add_f32 v57,v57,v112\n"
"v_add_f32 v57,v57,v113\n"
MF "a[116:119],v[20:23],v[88:91],a[116:119]\n"
"s_waitcnt lgkmcnt(0)\n"
TR "v[16:17],v2 offset:2048\n"
TR "v[18:19],v2 offset:8192\n"
MF "a[136:139],v[24:27],v[88:91],a[136:139]\n"
"s_nop 0\n"
TR "v[20:21],v3 offset:2048\n"
TR "v[22:23],v3 offset:8192\n"
MF "a[156:159],v[28:31],v[88:91],a[156:159]\n"
CV "v94,v112,v113\n"
"v_add_f32 v57,v57,v114\n"
MF "a[152:155],v[28:31],v[84:87],a[152:155]\n"
"v_add_f32 v57,v57,v115\n"
CV "v95,v114,v115\n"
MF "a[132:135],v[24:27],v[84:87],a[132:135]\n"
"v_exp_f32 v116,v116\n"
MF "a[128:131],v[24:27],v[80:83],a[128:131]\n"
"v_exp_f32 v117,v117\n"
MF "a[148:151],v[28:31],v[80:83],a[148:151]\n"
"v_exp_f32 v118,v118\n"
MF "a[144:147],v[28:31],v[76:79],a[144:147]\n"
"v_exp_f32 v119,v119\n"
MF "a[124:127],v[24:27],v[76:79],a[124:127]\n"
"v_add_f32 v58,v58,v116\n"
"v_add_f32 v58,v58,v117\n"
MF "a[120:123],v[24:27],v[72:75],a[120:123]\n"
CV "v98,v116,v117\n"
"v_add_f32 v58,v58,v118\n"
MF "a[140:143],v[28:31],v[72:75],a[140:143]\n"
"s_waitcnt lgkmcnt(0)\n"
TR "v[24:25],v4 offset:2048\n"
TR "v[26:27],v4 offset:8192\n"
MF "a[160:163],v[16:19],v[72:75],a[160:163]\n"
"s_nop 0\n"
TR "v[28:29],v5 offset:2048\n"
TR "v[30:31],v5 offset:8192\n"
MF "a[180:183],v[20:23],v[72:75],a[180:183]\n"
"v_add_f32 v58,v58,v119\n"
CV "v99,v118,v119\n"
MF "a[184:187],v[20:23],v[76:79],a[184:187]\n"
"v_exp_f32 v120,v120\n"
MF "a[164:167],v[16:19],v[76:79],a[164:167]\n"
"v_exp_f32 v121,v121\n"
MF "a[168:171],v[16:19],v[80:83],a[168:171]\n"
"v_exp_f32 v122,v122\n"
MF "a[188:191],v[20:23],v[80:83],a[188:191]\n"
"v_exp_f32 v123,v123\n"
MF "a[192:195],v[20:23],v[84:87],a[192:195]\n"
"v_add_f32 v59,v59,v120\n"
"v_add_f32 v59,v59,v121\n"
MF "a[172:175],v[16:19],v[84:87],a[172:175]\n"
"s_waitcnt lgkmcnt(0)\n"
CV "v102,v120,v121\n"
"v_add_f32 v59,v59,v122\n"
MF "a[176:179],v[16:19],v[88:91],a[176:179]\n"
"s_barrier\n"
MF "a[196:199],v[20:23],v[88:91],a[196:199]\n"
"s_sub_u32 s58,s71,s70\n"
"s_add_u32 s73,s73,s58\n"
"v_add_f32 v59,v59,v123\n"
MF "a[216:219],v[24:27],v[88:91],a[216:219]\n"
"s_add_u32 s18,s18,s58\n"
"s_add_u32 s19,s19,s58\n"
"s_add_u32 s73,s73,12288\n"
MF "a[236:239],v[28:31],v[88:91],a[236:239]\n"
"s_cmp_eq_u32 s73,s19\n"
"s_cselect_b32 s73,s18,s73\n"
"s_add_u32 m0,s65,49152\n"
MF "a[232:235],v[28:31],v[84:87],a[232:235]\n"
LD "v8,s[36:39],s73 offen nt lds\n"
MF "a[212:215],v[24:27],v[84:87],a[212:215]\n"
"s_add_u32 m0,s65,50048\n"
CV "v103,v122,v123\n"
MF "a[208:211],v[24:27],v[80:83],a[208:211]\n"
LD "v8,s[36:39],s73 offen offset:128 nt lds\n"
MF "a[228:231],v[28:31],v[80:83],a[228:231]\n"
"s_add_u32 m0,s65,50944\n"
"v_exp_f32 v124,v124\n"
MF "a[224:227],v[28:31],v[76:79],a[224:227]\n"
LD "v8,s[36:39],s73 offen offset:256 nt lds\n"
MF "a[204:207],v[24:27],v[76:79],a[204:207]\n"
"v_exp_f32 v125,v125\n"
MF "a[200:203],v[24:27],v[72:75],a[200:203]\n"
"v_exp_f32 v126,v126\n"
MF "a[220:223],v[28:31],v[72:75],a[220:223]\n"
"v_exp_f32 v127,v127\n"
"v_add_f32 v60,v60,v124\n"
"v_add_f32 v60,v60,v125\n"
CV "v106,v124,v125\n"
"v_add_f32 v60,v60,v126\n"
"v_add_f32 v60,v60,v127\n"
CV "v107,v126,v127\n"
"v_exp_f32 v128,v128\n"
"v_exp_f32 v129,v129\n"
"v_exp_f32 v130,v130\n"
"v_exp_f32 v131,v131\n"
"v_add_f32 v61,v61,v128\n"
"v_add_f32 v61,v61,v129\n"
CV "v110,v128,v129\n"
"v_add_f32 v61,v61,v130\n"
"v_add_f32 v61,v61,v131\n"
CV "v111,v130,v131\n"
"v_max3_f32 v10,v57,v58,v59\n"
"v_max3_f32 v10,v10,v60,v61\n"
"v_cmp_le_f32 vcc,0x47800000,v10\n"
"s_cbranch_vccnz .Lstub_13\n"
".Lback_13:\n"
"s_waitcnt vmcnt(9) lgkmcnt(0)\n"
"s_barrier\n"
"s_lshl_b32 s58,s56,10\n"
"s_add_u32 s58,s58,73728\n"
"v_add_u32_e64 v120,s58,v9\n"
"v_add_u32 v121,65536,v120\n"
"s_sub_u32 s59,s71,s70\n"
"v_mov_b32 v10,1.0\n"
TR "v[136:137],v2 offset:24576\n"
TR "v[138:139],v2 offset:30720\n"
"v_mfma_f32_16x16x4_f32 v[16:19],v10,v57,0\n"
TR "v[140:141],v3 offset:24576\n"
TR "v[142:143],v3 offset:30720\n"
TR "v[144:145],v4 offset:24576\n"
TR "v[146:147],v4 offset:30720\n"
TR "v[148:149],v5 offset:24576\n"
TR "v[150:151],v5 offset:30720\n"
"v_mfma_f32_16x16x4_f32 v[20:23],v10,v58,0\n"
TR "v[152:153],v2 offset:25600\n"
TR "v[154:155],v2 offset:31744\n"
TR "v[156:157],v3 offset:25600\n"
TR "v[158:159],v3 offset:31744\n"
TR "v[160:161],v4 offset:25600\n"
TR "v[162:163],v4 offset:31744\n"
"v_mfma_f32_16x16x4_f32 v[24:27],v10,v59,0\n"
TR "v[164:165],v5 offset:25600\n"
TR "v[166:167],v5 offset:31744\n"
TR "v[168:169],v2 offset:26624\n"
TR "v[170:171],v2 offset:32768\n"
TR "v[172:173],v3 offset:26624\n"
TR "v[174:175],v3 offset:32768\n"
"v_mfma_f32_16x16x4_f32 v[28:31],v10,v60,0\n"
TR "v[176:177],v4 offset:26624\n"
TR "v[178:179],v4 offset:32768\n"
TR "v[180:181],v5 offset:26624\n"
TR "v[182:183],v5 offset:32768\n"
"v_mfma_f32_16x16x4_f32 v[32:35],v10,v61,0\n"
"s_add_u32 m0,s65,49152\n"
"s_nop 0\n"
LD "v8,s[36:39],s73 offen offset:3072 nt lds\n"
"s_add_u32 m0,s65,50048\n"
"s_nop 0\n"
LD "v8,s[36:39],s73 offen offset:3200 nt lds\n"
"s_waitcnt lgkmcnt(15)\n"
MF "a[0:3],v[136:139],v[92:95],a[0:3]\n"
MF "a[20:23],v[140:143],v[92:95],a[20:23]\n"
"s_add_u32 m0,s65,50944\n"
"s_nop 0\n"
LD "v8,s[36:39],s73 offen offset:3328 nt lds\n"
"s_sub_u32 s74,s96,s16\n"
"s_add_u32 s74,s74,s71\n"
"s_add_u32 s20,s20,s59\n"
"s_mov_b32 s70,s71\n"
LD "v[232:235],v9,s[40:43],s75 offen sc1\n"
"v_rcp_f32 v57,v16\n"
"v_rcp_f32 v58,v20\n"
"v_rcp_f32 v59,v24\n"
"v_rcp_f32 v60,v28\n"
"v_rcp_f32_e64 v61,v32\n"
MF "a[4:7],v[136:139],v[96:99],a[4:7]\n"
"v_mul_f32_e64 v52,-s13,v57\n"
"v_mul_f32_e64 v53,-s13,v58\n"
"v_mul_f32_e64 v54,-s13,v59\n"
"v_mul_f32_e64 v55,-s13,v60\n"
"v_mul_f32_e64 v56,-s13,v61\n"
AR "v72,a0\n"
AR "v73,a1\n"
AR "v74,a2\n"
MF "a[24:27],v[140:143],v[96:99],a[24:27]\n"
AR "v75,a3\n"
AR "v76,a20\n"
AR "v77,a21\n"
AR "v78,a22\n"
AR "v79,a23\n"
"v_pk_fma_f32 v[72:73],v[72:73],v[56:57],v[52:53] op_sel:[0,1,0] op_sel_hi:[1,1,0]\n"
"v_pk_fma_f32 v[74:75],v[74:75],v[56:57],v[52:53] op_sel:[0,1,0] op_sel_hi:[1,1,0]\n"
"v_pk_fma_f32 v[76:77],v[76:77],v[56:57],v[52:53] op_sel:[0,1,0] op_sel_hi:[1,1,0]\n"
"v_pk_fma_f32 v[78:79],v[78:79],v[56:57],v[52:53] op_sel:[0,1,0] op_sel_hi:[1,1,0]\n"
CV "v112,v72,v73\n"
CV "v113,v74,v75\n"
CV "v114,v76,v77\n"
CV "v115,v78,v79\n"
MF "a[8:11],v[136:139],v[100:103],a[8:11]\n"
"ds_read_b128 v[184:187],v120 offset:24576\n"
"ds_write_b128 v120,v[112:115] offset:24576\n"
LD "v[236:239],v9,s[40:43],s75 offen offset:1024 sc1\n"
AR "v80,a4\n"
AR "v81,a5\n"
AR "v82,a6\n"
MF "a[28:31],v[140:143],v[100:103],a[28:31]\n"
AR "v83,a7\n"
AR "v84,a24\n"
AR "v85,a25\n"
AR "v86,a26\n"
AR "v87,a27\n"
"v_pk_fma_f32 v[80:81],v[80:81],v[58:59],v[52:53] op_sel:[0,0,1] op_sel_hi:[1,0,1]\n"
"v_pk_fma_f32 v[82:83],v[82:83],v[58:59],v[52:53] op_sel:[0,0,1] op_sel_hi:[1,0,1]\n"
"v_pk_fma_f32 v[84:85],v[84:85],v[58:59],v[52:53] op_sel:[0,0,1] op_sel_hi:[1,0,1]\n"
"v_pk_fma_f32 v[86:87],v[86:87],v[58:59],v[52:53] op_sel:[0,0,1] op_sel_hi:[1,0,1]\n"
CV "v116,v80,v81\n"
CV "v117,v82,v83\n"
CV "v118,v84,v85\n"
CV "v119,v86,v87\n"
MF "a[12:15],v[136:139],v[104:107],a[12:15]\n"
"ds_read_b128 v[188:191],v120 offset:28672\n"
"ds_write_b128 v120,v[116:119] offset:28672\n"
AR "v72,a8\n"
AR "v73,a9\n"
AR "v74,a10\n"
MF "a[32:35],v[140:143],v[104:107],a[32:35]\n"
AR "v75,a11\n"
AR "v76,a28\n"
AR "v77,a29\n"
AR "v78,a30\n"
AR "v79,a31\n"
"v_pk_fma_f32 v[72:73],v[72:73],v[58:59],v[54:55] op_sel:[0,1,0] op_sel_hi:[1,1,0]\n"
"v_pk_fma_f32 v[74:75],v[74:75],v[58:59],v[54:55] op_sel:[0,1,0] op_sel_hi:[1,1,0]\n"
"v_pk_fma_f32 v[76:77],v[76:77],v[58:59],v[54:55] op_sel:[0,1,0] op_sel_hi:[1,1,0]\n"
"v_pk_fma_f32 v[78:79],v[78:79],v[58:59],v[54:55] op_sel:[0,1,0] op_sel_hi:[1,1,0]\n"
CV "v112,v72,v73\n"
CV "v113,v74,v75\n"
CV "v114,v76,v77\n"
CV "v115,v78,v79\n"
MF "a[16:19],v[136:139],v[108:111],a[16:19]\n"
"ds_read_b128 v[192:195],v120 offset:32768\n"
"ds_write_b128 v120,v[112:115] offset:32768\n"
"s_waitcnt lgkmcnt(0)\n"
"s_barrier\n"
LD "v[240:243],v9,s[40:43],s75 offen offset:2048 sc1\n"
AR "v80,a12\n"
AR "v81,a13\n"
AR "v82,a14\n"
MF "a[36:39],v[140:143],v[108:111],a[36:39]\n"
AR "v83,a15\n"
AR "v84,a32\n"
AR "v85,a33\n"
AR "v86,a34\n"
AR "v87,a35\n"
"v_pk_fma_f32 v[80:81],v[80:81],v[60:61],v[54:55] op_sel:[0,0,1] op_sel_hi:[1,0,1]\n"
"v_pk_fma_f32 v[82:83],v[82:83],v[60:61],v[54:55] op_sel:[0,0,1] op_sel_hi:[1,0,1]\n"
"v_pk_fma_f32 v[84:85],v[84:85],v[60:61],v[54:55] op_sel:[0,0,1] op_sel_hi:[1,0,1]\n"
"v_pk_fma_f32 v[86:87],v[86:87],v[60:61],v[54:55] op_sel:[0,0,1] op_sel_hi:[1,0,1]\n"
CV "v116,v80,v81\n"
CV "v117,v82,v83\n"
CV "v118,v84,v85\n"
CV "v119,v86,v87\n"
MF "a[40:43],v[144:147],v[92:95],a[40:43]\n"
"ds_read_b128 v[196:199],v120 offset:36864\n"
"ds_write_b128 v120,v[116:119] offset:36864\n"
LD "v[244:247],v9,s[40:43],s75 offen offset:3072 sc1\n"
AR "v72,a16\n"
AR "v73,a17\n"
AR "v74,a18\n"
MF "a[60:63],v[148:151],v[92:95],a[60:63]\n"
AR "v75,a19\n"
AR "v76,a36\n"
AR "v77,a37\n"
AR "v78,a38\n"
AR "v79,a39\n"
"v_pk_fma_f32 v[72:73],v[72:73],v[60:61],v[56:57] op_sel:[0,1,0] op_sel_hi:[1,1,0]\n"
"v_pk_fma_f32 v[74:75],v[74:75],v[60:61],v[56:57] op_sel:[0,1,0] op_sel_hi:[1,1,0]\n"
"v_pk_fma_f32 v[76:77],v[76:77],v[60:61],v[56:57] op_sel:[0,1,0] op_sel_hi:[1,1,0]\n"
"v_pk_fma_f32 v[78:79],v[78:79],v[60:61],v[56:57] op_sel:[0,1,0] op_sel_hi:[1,1,0]\n"
CV "v112,v72,v73\n"
CV "v113,v74,v75\n"
CV "v114,v76,v77\n"
CV "v115,v78,v79\n"
MF "a[44:47],v[144:147],v[96:99],a[44:47]\n"
"ds_read_b128 v[200:203],v120 offset:40960\n"
"ds_write_b128 v120,v[112:115] offset:40960\n"
AR "v80,a40\n"
AR "v81,a41\n"
AR "v82,a42\n"
MF "a[64:67],v[148:151],v[96:99],a[64:67]\n"
AR "v83,a43\n"
AR "v84,a60\n"
AR "v85,a61\n"
AR "v86,a62\n"
AR "v87,a63\n"
"v_pk_fma_f32 v[80:81],v[80:81],v[56:57],v[52:53] op_sel:[0,1,0] op_sel_hi:[1,1,0]\n"
"v_pk_fma_f32 v[82:83],v[82:83],v[56:57],v[52:53] op_sel:[0,1,0] op_sel_hi:[1,1,0]\n"
"v_pk_fma_f32 v[84:85],v[84:85],v[56:57],v[52:53] op_sel:[0,1,0] op_sel_hi:[1,1,0]\n"
"v_pk_fma_f32 v[86:87],v[86:87],v[56:57],v[52:53] op_sel:[0,1,0] op_sel_hi:[1,1,0]\n"
CV "v116,v80,v81\n"
CV "v117,v82,v83\n"
CV "v118,v84,v85\n"
CV "v119,v86,v87\n"
MF "a[48:51],v[144:147],v[100:103],a[48:51]\n"
"ds_read_b128 v[204:207],v120 offset:45056\n"
"ds_write_b128 v120,v[116:119] offset:45056\n"
"s_add_u32 s58,s75,4096\n"
LD "v[248:251],v9,s[40:43],s58 offen sc1\n"
AR "v72,a44\n"
AR "v73,a45\n"
AR "v74,a46\n"
MF "a[68:71],v[148:151],v[100:103],a[68:71]\n"
AR "v75,a47\n"
AR "v76,a64\n"
AR "v77,a65\n"
AR "v78,a66\n"
AR "v79,a67\n"
"v_pk_fma_f32 v[72:73],v[72:73],v[58:59],v[52:53] op_sel:[0,0,1] op_sel_hi:[1,0,1]\n"
"v_pk_fma_f32 v[74:75],v[74:75],v[58:59],v[52:53] op_sel:[0,0,1] op_sel_hi:[1,0,1]\n"
"v_pk_fma_f32 v[76:77],v[76:77],v[58:59],v[52:53] op_sel:[0,0,1] op_sel_hi:[1,0,1]\n"
"v_pk_fma_f32 v[78:79],v[78:79],v[58:59],v[52:53] op_sel:[0,0,1] op_sel_hi:[1,0,1]\n"
CV "v112,v72,v73\n"
CV "v113,v74,v75\n"
CV "v114,v76,v77\n"
CV "v115,v78,v79\n"
MF "a[52:55],v[144:147],v[104:107],a[52:55]\n"
"ds_read_b128 v[208:211],v120 offset:49152\n"
"ds_write_b128 v120,v[112:115] offset:49152\n"
LD "v[252:255],v9,s[40:43],s58 offen offset:1024 sc1\n"
AR "v80,a48\n"
AR "v81,a49\n"
AR "v82,a50\n"
MF "a[72:75],v[148:151],v[104:107],a[72:75]\n"
AR "v83,a51\n"
AR "v84,a68\n"
AR "v85,a69\n"
AR "v86,a70\n"
AR "v87,a71\n"
"v_pk_fma_f32 v[80:81],v[80:81],v[58:59],v[54:55] op_sel:[0,1,0] op_sel_hi:[1,1,0]\n"
"v_pk_fma_f32 v[82:83],v[82:83],v[58:59],v[54:55] op_sel:[0,1,0] op_sel_hi:[1,1,0]\n"
"v_pk_fma_f32 v[84:85],v[84:85],v[58:59],v[54:55] op_sel:[0,1,0] op_sel_hi:[1,1,0]\n"
"v_pk_fma_f32 v[86:87],v[86:87],v[58:59],v[54:55] op_sel:[0,1,0] op_sel_hi:[1,1,0]\n"
CV "v116,v80,v81\n"
CV "v117,v82,v83\n"
CV "v118,v84,v85\n"
CV "v119,v86,v87\n"
MF "a[56:59],v[144:147],v[108:111],a[56:59]\n"
"ds_read_b128 v[212:215],v120 offset:53248\n"
"ds_write_b128 v120,v[116:119] offset:53248\n"
AR "v72,a52\n"
AR "v73,a53\n"
AR "v74,a54\n"
MF "a[76:79],v[148:151],v[108:111],a[76:79]\n"
AR "v75,a55\n"
AR "v76,a72\n"
AR "v77,a73\n"
AR "v78,a74\n"
AR "v79,a75\n"
"v_pk_fma_f32 v[72:73],v[72:73],v[60:61],v[54:55] op_sel:[0,0,1] op_sel_hi:[1,0,1]\n"
"v_pk_fma_f32 v[74:75],v[74:75],v[60:61],v[54:55] op_sel:[0,0,1] op_sel_hi:[1,0,1]\n"
"v_pk_fma_f32 v[76:77],v[76:77],v[60:61],v[54:55] op_sel:[0,0,1] op_sel_hi:[1,0,1]\n"
"v_pk_fma_f32 v[78:79],v[78:79],v[60:61],v[54:55] op_sel:[0,0,1] op_sel_hi:[1,0,1]\n"
CV "v112,v72,v73\n"
CV "v113,v74,v75\n"
CV "v114,v76,v77\n"
CV "v115,v78,v79\n"
MF "a[80:83],v[152:155],v[92:95],a[80:83]\n"
"ds_read_b128 v[216:219],v120 offset:57344\n"
"ds_write_b128 v120,v[112:115] offset:57344\n"
AR "v80,a56\n"
AR "v81,a57\n"
AR "v82,a58\n"
MF "a[100:103],v[156:159],v[92:95],a[100:103]\n"
AR "v83,a59\n"
AR "v84,a76\n"
AR "v85,a77\n"
AR "v86,a78\n"
AR "v87,a79\n"
"v_pk_fma_f32 v[80:81],v[80:81],v[60:61],v[56:57] op_sel:[0,1,0] op_sel_hi:[1,1,0]\n"
"v_pk_fma_f32 v[82:83],v[82:83],v[60:61],v[56:57] op_sel:[0,1,0] op_sel_hi:[1,1,0]\n"
"v_pk_fma_f32 v[84:85],v[84:85],v[60:61],v[56:57] op_sel:[0,1,0] op_sel_hi:[1,1,0]\n"
"v_pk_fma_f32 v[86:87],v[86:87],v[60:61],v[56:57] op_sel:[0,1,0] op_sel_hi:[1,1,0]\n"
CV "v116,v80,v81\n"
CV "v117,v82,v83\n"
CV "v118,v84,v85\n"
CV "v119,v86,v87\n"
MF "a[84:87],v[152:155],v[96:99],a[84:87]\n"
"ds_read_b128 v[220:223],v120 offset:61440\n"
"ds_write_b128 v120,v[116:119] offset:61440\n"
AR "v72,a80\n"
AR "v73,a81\n"
AR "v74,a82\n"
MF "a[104:107],v[156:159],v[96:99],a[104:107]\n"
AR "v75,a83\n"
AR "v76,a100\n"
AR "v77,a101\n"
AR "v78,a102\n"
AR "v79,a103\n"
"v_pk_fma_f32 v[72:73],v[72:73],v[56:57],v[52:53] op_sel:[0,1,0] op_sel_hi:[1,1,0]\n"
"v_pk_fma_f32 v[74:75],v[74:75],v[56:57],v[52:53] op_sel:[0,1,0] op_sel_hi:[1,1,0]\n"
"v_pk_fma_f32 v[76:77],v[76:77],v[56:57],v[52:53] op_sel:[0,1,0] op_sel_hi:[1,1,0]\n"
"v_pk_fma_f32 v[78:79],v[78:79],v[56:57],v[52:53] op_sel:[0,1,0] op_sel_hi:[1,1,0]\n"
CV "v112,v72,v73\n"
CV "v113,v74,v75\n"
CV "v114,v76,v77\n"
CV "v115,v78,v79\n"
MF "a[88:91],v[152:155],v[100:103],a[88:91]\n"
"ds_read_b128 v[224:227],v121\n"
"ds_write_b128 v121,v[112:115]\n"
AR "v80,a84\n"
AR "v81,a85\n"
AR "v82,a86\n"
MF "a[108:111],v[156:159],v[100:103],a[108:111]\n"
".Lpg17g:\n"
"s_cbranch_execz .Lisr6\n"
AR "v83,a87\n"
AR "v84,a104\n"
AR "v85,a105\n"
"s_nop 0\n"
AR "v86,a106\n"
AR "v87,a107\n"
"v_pk_fma_f32 v[80:81],v[80:81],v[58:59],v[52:53] op_sel:[0,0,1] op_sel_hi:[1,0,1]\n"
"v_pk_fma_f32 v[82:83],v[82:83],v[58:59],v[52:53] op_sel:[0,0,1] op_sel_hi:[1,0,1]\n"
"v_pk_fma_f32 v[84:85],v[84:85],v[58:59],v[52:53] op_sel:[0,0,1] op_sel_hi:[1,0,1]\n"
"v_pk_fma_f32 v[86:87],v[86:87],v[58:59],v[52:53] op_sel:[0,0,1] op_sel_hi:[1,0,1]\n"
CV "v116,v80,v81\n"
CV "v117,v82,v83\n"
CV "v118,v84,v85\n"
CV "v119,v86,v87\n"
MF "a[92:95],v[152:155],v[104:107],a[92:95]\n"
"ds_read_b128 v[228:231],v121 offset:4096\n"
"ds_write_b128 v121,v[116:119] offset:4096\n"
AR "v72,a88\n"
AR "v73,a89\n"
AR "v74,a90\n"
MF "a[112:115],v[156:159],v[104:107],a[112:115]\n"
AR "v75,a91\n"
AR "v76,a108\n"
AR "v77,a109\n"
AR "v78,a110\n"
AR "v79,a111\n"
"v_pk_fma_f32 v[72:73],v[72:73],v[58:59],v[54:55] op_sel:[0,1,0] op_sel_hi:[1,1,0]\n"
"v_pk_fma_f32 v[74:75],v[74:75],v[58:59],v[54:55] op_sel:[0,1,0] op_sel_hi:[1,1,0]\n"
"v_pk_fma_f32 v[76:77],v[76:77],v[58:59],v[54:55] op_sel:[0,1,0] op_sel_hi:[1,1,0]\n"
"v_pk_fma_f32 v[78:79],v[78:79],v[58:59],v[54:55] op_sel:[0,1,0] op_sel_hi:[1,1,0]\n"
CV "v112,v72,v73\n"
CV "v113,v74,v75\n"
CV "v114,v76,v77\n"
CV "v115,v78,v79\n"
MF "a[96:99],v[152:155],v[108:111],a[96:99]\n"
"ds_read_b128 v[136:139],v121 offset:8192\n"
"ds_write_b128 v121,v[112:115] offset:8192\n"
AR "v80,a92\n"
AR "v81,a93\n"
AR "v82,a94\n"
MF "a[116:119],v[156:159],v[108:111],a[116:119]\n"
AR "v83,a95\n"
AR "v84,a112\n"
AR "v85,a113\n"
AR "v86,a114\n"
AR "v87,a115\n"
"v_pk_fma_f32 v[80:81],v[80:81],v[60:61],v[54:55] op_sel:[0,0,1] op_sel_hi:[1,0,1]\n"
"v_pk_fma_f32 v[82:83],v[82:83],v[60:61],v[54:55] op_sel:[0,0,1] op_sel_hi:[1,0,1]\n"
"v_pk_fma_f32 v[84:85],v[84:85],v[60:61],v[54:55] op_sel:[0,0,1] op_sel_hi:[1,0,1]\n"
"v_pk_fma_f32 v[86:87],v[86:87],v[60:61],v[54:55] op_sel:[0,0,1] op_sel_hi:[1,0,1]\n"
CV "v116,v80,v81\n"
CV "v117,v82,v83\n"
CV "v118,v84,v85\n"
CV "v119,v86,v87\n"
MF "a[120:123],v[160:163],v[92:95],a[120:123]\n"
"ds_read_b128 v[140:143],v121 offset:12288\n"
"ds_write_b128 v121,v[116:119] offset:12288\n"
AR "v72,a96\n"
AR "v73,a97\n"
AR "v74,a98\n"
MF "a[140:143],v[164:167],v[92:95],a[140:143]\n"
AR "v75,a99\n"
AR "v76,a116\n"
AR "v77,a117\n"
AR "v78,a118\n"
AR "v79,a119\n"
"v_pk_fma_f32 v[72:73],v[72:73],v[60:61],v[56:57] op_sel:[0,1,0] op_sel_hi:[1,1,0]\n"
"v_pk_fma_f32 v[74:75],v[74:75],v[60:61],v[56:57] op_sel:[0,1,0] op_sel_hi:[1,1,0]\n"
"v_pk_fma_f32 v[76:77],v[76:77],v[60:61],v[56:57] op_sel:[0,1,0] op_sel_hi:[1,1,0]\n"
"v_pk_fma_f32 v[78:79],v[78:79],v[60:61],v[56:57] op_sel:[0,1,0] op_sel_hi:[1,1,0]\n"
CV "v112,v72,v73\n"
CV "v113,v74,v75\n"
CV "v114,v76,v77\n"
CV "v115,v78,v79\n"
MF "a[124:127],v[160:163],v[96:99],a[124:127]\n"
"ds_read_b128 v[144:147],v121 offset:16384\n"
"ds_write_b128 v121,v[112:115] offset:16384\n"
AR "v80,a120\n"
AR "v81,a121\n"
AR "v82,a122\n"
MF "a[144:147],v[164:167],v[96:99],a[144:147]\n"
AR "v83,a123\n"
AR "v84,a140\n"
AR "v85,a141\n"
AR "v86,a142\n"
AR "v87,a143\n"
"v_pk_fma_f32 v[80:81],v[80:81],v[56:57],v[52:53] op_sel:[0,1,0] op_sel_hi:[1,1,0]\n"
"v_pk_fma_f32 v[82:83],v[82:83],v[56:57],v[52:53] op_sel:[0,1,0] op_sel_hi:[1,1,0]\n"
"v_pk_fma_f32 v[84:85],v[84:85],v[56:57],v[52:53] op_sel:[0,1,0] op_sel_hi:[1,1,0]\n"
"v_pk_fma_f32 v[86:87],v[86:87],v[56:57],v[52:53] op_sel:[0,1,0] op_sel_hi:[1,1,0]\n"
CV "v116,v80,v81\n"
CV "v117,v82,v83\n"
CV "v118,v84,v85\n"
CV "v119,v86,v87\n"
MF "a[128:131],v[160:163],v[100:103],a[128:131]\n"
"ds_read_b128 v[148:151],v121 offset:20480\n"
"ds_write_b128 v121,v[116:119] offset:20480\n"
AR "v72,a124\n"
AR "v73,a125\n"
AR "v74,a126\n"
MF "a[148:151],v[164:167],v[100:103],a[148:151]\n"
AR "v75,a127\n"
AR "v76,a144\n"
AR "v77,a145\n"
AR "v78,a146\n"
AR "v79,a147\n"
"v_pk_fma_f32 v[72:73],v[72:73],v[58:59],v[52:53] op_sel:[0,0,1] op_sel_hi:[1,0,1]\n"
"v_pk_fma_f32 v[74:75],v[74:75],v[58:59],v[52:53] op_sel:[0,0,1] op_sel_hi:[1,0,1]\n"
"v_pk_fma_f32 v[76:77],v[76:77],v[58:59],v[52:53] op_sel:[0,0,1] op_sel_hi:[1,0,1]\n"
"v_pk_fma_f32 v[78:79],v[78:79],v[58:59],v[52:53] op_sel:[0,0,1] op_sel_hi:[1,0,1]\n"
CV "v112,v72,v73\n"
CV "v113,v74,v75\n"
CV "v114,v76,v77\n"
CV "v115,v78,v79\n"
MF "a[132:135],v[160:163],v[104:107],a[132:135]\n"
"ds_write_b128 v120,v[112:115]\n"
AR "v80,a128\n"
AR "v81,a129\n"
AR "v82,a130\n"
MF "a[152:155],v[164:167],v[104:107],a[152:155]\n"
AR "v83,a131\n"
AR "v84,a148\n"
AR "v85,a149\n"
AR "v86,a150\n"
AR "v87,a151\n"
"v_pk_fma_f32 v[80:81],v[80:81],v[58:59],v[54:55] op_sel:[0,1,0] op_sel_hi:[1,1,0]\n"
"v_pk_fma_f32 v[82:83],v[82:83],v[58:59],v[54:55] op_sel:[0,1,0] op_sel_hi:[1,1,0]\n"
"v_pk_fma_f32 v[84:85],v[84:85],v[58:59],v[54:55] op_sel:[0,1,0] op_sel_hi:[1,1,0]\n"
"v_pk_fma_f32 v[86:87],v[86:87],v[58:59],v[54:55] op_sel:[0,1,0] op_sel_hi:[1,1,0]\n"
CV "v116,v80,v81\n"
CV "v117,v82,v83\n"
CV "v118,v84,v85\n"
CV "v119,v86,v87\n"
MF "a[136:139],v[160:163],v[108:111],a[136:139]\n"
"ds_write_b128 v120,v[116:119] offset:4096\n"
AR "v72,a132\n"
AR "v73,a133\n"
AR "v74,a134\n"
MF "a[156:159],v[164:167],v[108:111],a[156:159]\n"
AR "v75,a135\n"
AR "v76,a152\n"
AR "v77,a153\n"
AR "v78,a154\n"
AR "v79,a155\n"
"v_pk_fma_f32 v[72:73],v[72:73],v[60:61],v[54:55] op_sel:[0,0,1] op_sel_hi:[1,0,1]\n"
"v_pk_fma_f32 v[74:75],v[74:75],v[60:61],v[54:55] op_sel:[0,0,1] op_sel_hi:[1,0,1]\n"
"v_pk_fma_f32 v[76:77],v[76:77],v[60:61],v[54:55] op_sel:[0,0,1] op_sel_hi:[1,0,1]\n"
"v_pk_fma_f32 v[78:79],v[78:79],v[60:61],v[54:55] op_sel:[0,0,1] op_sel_hi:[1,0,1]\n"
CV "v112,v72,v73\n"
CV "v113,v74,v75\n"
CV "v114,v76,v77\n"
CV "v115,v78,v79\n"
MF "a[160:163],v[168:171],v[92:95],a[160:163]\n"
"ds_write_b128 v120,v[112:115] offset:8192\n"
AR "v80,a136\n"
AR "v81,a137\n"
AR "v82,a138\n"
MF "a[180:183],v[172:175],v[92:95],a[180:183]\n"
AR "v83,a139\n"
AR "v84,a156\n"
AR "v85,a157\n"
AR "v86,a158\n"
AR "v87,a159\n"
"v_pk_fma_f32 v[80:81],v[80:81],v[60:61],v[56:57] op_sel:[0,1,0] op_sel_hi:[1,1,0]\n"
"v_pk_fma_f32 v[82:83],v[82:83],v[60:61],v[56:57] op_sel:[0,1,0] op_sel_hi:[1,1,0]\n"
"v_pk_fma_f32 v[84:85],v[84:85],v[60:61],v[56:57] op_sel:[0,1,0] op_sel_hi:[1,1,0]\n"
"v_pk_fma_f32 v[86:87],v[86:87],v[60:61],v[56:57] op_sel:[0,1,0] op_sel_hi:[1,1,0]\n"
CV "v116,v80,v81\n"
CV "v117,v82,v83\n"
CV "v118,v84,v85\n"
CV "v119,v86,v87\n"
MF "a[164:167],v[168:171],v[96:99],a[164:167]\n"
"ds_write_b128 v120,v[116:119] offset:12288\n"
AR "v72,a160\n"
AR "v73,a161\n"
AR "v74,a162\n"
MF "a[184:187],v[172:175],v[96:99],a[184:187]\n"
AR "v75,a163\n"
AR "v76,a180\n"
AR "v77,a181\n"
AR "v78,a182\n"
AR "v79,a183\n"
"v_pk_fma_f32 v[72:73],v[72:73],v[56:57],v[52:53] op_sel:[0,1,0] op_sel_hi:[1,1,0]\n"
"v_pk_fma_f32 v[74:75],v[74:75],v[56:57],v[52:53] op_sel:[0,1,0] op_sel_hi:[1,1,0]\n"
"v_pk_fma_f32 v[76:77],v[76:77],v[56:57],v[52:53] op_sel:[0,1,0] op_sel_hi:[1,1,0]\n"
"v_pk_fma_f32 v[78:79],v[78:79],v[56:57],v[52:53] op_sel:[0,1,0] op_sel_hi:[1,1,0]\n"
CV "v112,v72,v73\n"
CV "v113,v74,v75\n"
CV "v114,v76,v77\n"
CV "v115,v78,v79\n"
MF "a[168:171],v[168:171],v[100:103],a[168:171]\n"
"ds_write_b128 v120,v[112:115] offset:16384\n"
AR "v80,a164\n"
AR "v81,a165\n"
AR "v82,a166\n"
MF "a[188:191],v[172:175],v[100:103],a[188:191]\n"
AR "v83,a167\n"
AR "v84,a184\n"
AR "v85,a185\n"
AR "v86,a186\n"
AR "v87,a187\n"
"v_pk_fma_f32 v[80:81],v[80:81],v[58:59],v[52:53] op_sel:[0,0,1] op_sel_hi:[1,0,1]\n"
"v_pk_fma_f32 v[82:83],v[82:83],v[58:59],v[52:53] op_sel:[0,0,1] op_sel_hi:[1,0,1]\n"
"v_pk_fma_f32 v[84:85],v[84:85],v[58:59],v[52:53] op_sel:[0,0,1] op_sel_hi:[1,0,1]\n"
"v_pk_fma_f32 v[86:87],v[86:87],v[58:59],v[52:53] op_sel:[0,0,1] op_sel_hi:[1,0,1]\n"
CV "v116,v80,v81\n"
CV "v117,v82,v83\n"
CV "v118,v84,v85\n"
CV "v119,v86,v87\n"
MF "a[172:175],v[168:171],v[104:107],a[172:175]\n"
"ds_write_b128 v120,v[116:119] offset:20480\n"
AR "v72,a168\n"
AR "v73,a169\n"
AR "v74,a170\n"
MF "a[192:195],v[172:175],v[104:107],a[192:195]\n"
AR "v75,a171\n"
AR "v76,a188\n"
AR "v77,a189\n"
AR "v78,a190\n"
AR "v79,a191\n"
"v_pk_fma_f32 v[72:73],v[72:73],v[58:59],v[54:55] op_sel:[0,1,0] op_sel_hi:[1,1,0]\n"
"v_pk_fma_f32 v[74:75],v[74:75],v[58:59],v[54:55] op_sel:[0,1,0] op_sel_hi:[1,1,0]\n"
"v_pk_fma_f32 v[76:77],v[76:77],v[58:59],v[54:55] op_sel:[0,1,0] op_sel_hi:[1,1,0]\n"
"v_pk_fma_f32 v[78:79],v[78:79],v[58:59],v[54:55] op_sel:[0,1,0] op_sel_hi:[1,1,0]\n"
CV "v112,v72,v73\n"
CV "v113,v74,v75\n"
CV "v114,v76,v77\n"
CV "v115,v78,v79\n"
MF "a[176:179],v[168:171],v[108:111],a[176:179]\n"
AR "v152,a240\n"
AW "a240,v112\n"
AR "v153,a241\n"
AW "a241,v113\n"
AR "v154,a242\n"
AW "a242,v114\n"
AR "v155,a243\n"
AW "a243,v115\n"
AR "v80,a172\n"
AR "v81,a173\n"
AR "v82,a174\n"
MF "a[196:199],v[172:175],v[108:111],a[196:199]\n"
AR "v83,a175\n"
AR "v84,a192\n"
AR "v85,a193\n"
AR "v86,a194\n"
AR "v87,a195\n"
"v_pk_fma_f32 v[80:81],v[80:81],v[60:61],v[54:55] op_sel:[0,0,1] op_sel_hi:[1,0,1]\n"
"v_pk_fma_f32 v[82:83],v[82:83],v[60:61],v[54:55] op_sel:[0,0,1] op_sel_hi:[1,0,1]\n"
"v_pk_fma_f32 v[84:85],v[84:85],v[60:61],v[54:55] op_sel:[0,0,1] op_sel_hi:[1,0,1]\n"
"v_pk_fma_f32 v[86:87],v[86:87],v[60:61],v[54:55] op_sel:[0,0,1] op_sel_hi:[1,0,1]\n"
CV "v116,v80,v81\n"
CV "v117,v82,v83\n"
CV "v118,v84,v85\n"
CV "v119,v86,v87\n"
MF "a[200:203],v[176:179],v[92:95],a[200:203]\n"
AR "v156,a244\n"
AW "a244,v116\n"
AR "v157,a245\n"
AW "a245,v117\n"
AR "v158,a246\n"
AW "a246,v118\n"
AR "v159,a247\n"
AW "a247,v119\n"
AR "v72,a176\n"
AR "v73,a177\n"
AR "v74,a178\n"
MF "a[220:223],v[180:183],v[92:95],a[220:223]\n"
AR "v75,a179\n"
AR "v76,a196\n"
AR "v77,a197\n"
AR "v78,a198\n"
AR "v79,a199\n"
"v_pk_fma_f32 v[72:73],v[72:73],v[60:61],v[56:57] op_sel:[0,1,0] op_sel_hi:[1,1,0]\n"
"v_pk_fma_f32 v[74:75],v[74:75],v[60:61],v[56:57] op_sel:[0,1,0] op_sel_hi:[1,1,0]\n"
"v_pk_fma_f32 v[76:77],v[76:77],v[60:61],v[56:57] op_sel:[0,1,0] op_sel_hi:[1,1,0]\n"
"v_pk_fma_f32 v[78:79],v[78:79],v[60:61],v[56:57] op_sel:[0,1,0] op_sel_hi:[1,1,0]\n"
CV "v112,v72,v73\n"
CV "v113,v74,v75\n"
CV "v114,v76,v77\n"
CV "v115,v78,v79\n"
MF "a[204:207],v[176:179],v[96:99],a[204:207]\n"
AR "v160,a248\n"
AW "a248,v112\n"
AR "v161,a249\n"
AW "a249,v113\n"
AR "v162,a250\n"
AW "a250,v114\n"
AR "v163,a251\n"
AW "a251,v115\n"
AR "v80,a200\n"
AR "v81,a201\n"
AR "v82,a202\n"
MF "a[224:227],v[180:183],v[96:99],a[224:227]\n"
AR "v83,a203\n"
AR "v84,a220\n"
AR "v85,a221\n"
AR "v86,a222\n"
AR "v87,a223\n"
"v_pk_fma_f32 v[80:81],v[80:81],v[56:57],v[52:53] op_sel:[0,1,0] op_sel_hi:[1,1,0]\n"
"v_pk_fma_f32 v[82:83],v[82:83],v[56:57],v[52:53] op_sel:[0,1,0] op_sel_hi:[1,1,0]\n"
"v_pk_fma_f32 v[84:85],v[84:85],v[56:57],v[52:53] op_sel:[0,1,0] op_sel_hi:[1,1,0]\n"
"v_pk_fma_f32 v[86:87],v[86:87],v[56:57],v[52:53] op_sel:[0,1,0] op_sel_hi:[1,1,0]\n"
CV "v116,v80,v81\n"
CV "v117,v82,v83\n"
CV "v118,v84,v85\n"
CV "v119,v86,v87\n"
MF "a[208:211],v[176:179],v[100:103],a[208:211]\n"
"v_mov_b32 v164,v62\n"
"v_mov_b32 v62,v116\n"
"v_mov_b32 v165,v63\n"
"v_mov_b32 v63,v117\n"
"v_mov_b32 v166,v64\n"
"v_mov_b32 v64,v118\n"
"v_mov_b32 v167,v65\n"
"v_mov_b32 v65,v119\n"
AR "v72,a204\n"
AR "v73,a205\n"
AR "v74,a206\n"
MF "a[228:231],v[180:183],v[100:103],a[228:231]\n"
AR "v75,a207\n"
AR "v76,a224\n"
AR "v77,a225\n"
AR "v78,a226\n"
AR "v79,a227\n"
"v_pk_fma_f32 v[72:73],v[72:73],v[58:59],v[52:53] op_sel:[0,0,1] op_sel_hi:[1,0,1]\n"
"v_pk_fma_f32 v[74:75],v[74:75],v[58:59],v[52:53] op_sel:[0,0,1] op_sel_hi:[1,0,1]\n"
"v_pk_fma_f32 v[76:77],v[76:77],v[58:59],v[52:53] op_sel:[0,0,1] op_sel_hi:[1,0,1]\n"
"v_pk_fma_f32 v[78:79],v[78:79],v[58:59],v[52:53] op_sel:[0,0,1] op_sel_hi:[1,0,1]\n"
CV "v112,v72,v73\n"
CV "v113,v74,v75\n"
CV "v114,v76,v77\n"
CV "v115,v78,v79\n"
MF "a[212:215],v[176:179],v[104:107],a[212:215]\n"
"v_mov_b32 v168,v66\n"
"v_mov_b32 v66,v112\n"
"v_mov_b32 v169,v67\n"
"v_mov_b32 v67,v113\n"
"v_mov_b32 v170,v68\n"
"v_mov_b32 v68,v114\n"
"v_mov_b32 v171,v69\n"
"v_mov_b32 v69,v115\n"
AR "v80,a208\n"
AR "v81,a209\n"
AR "v82,a210\n"
MF "a[232:235],v[180:183],v[104:107],a[232:235]\n"
AR "v83,a211\n"
AR "v84,a228\n"
AR "v85,a229\n"
AR "v86,a230\n"
AR "v87,a231\n"
"v_pk_fma_f32 v[80:81],v[80:81],v[58:59],v[54:55] op_sel:[0,1,0] op_sel_hi:[1,1,0]\n"
"v_pk_fma_f32 v[82:83],v[82:83],v[58:59],v[54:55] op_sel:[0,1,0] op_sel_hi:[1,1,0]\n"
"v_pk_fma_f32 v[84:85],v[84:85],v[58:59],v[54:55] op_sel:[0,1,0] op_sel_hi:[1,1,0]\n"
"v_pk_fma_f32 v[86:87],v[86:87],v[58:59],v[54:55] op_sel:[0,1,0] op_sel_hi:[1,1,0]\n"
CV "v116,v80,v81\n"
CV "v117,v82,v83\n"
CV "v118,v84,v85\n"
CV "v119,v86,v87\n"
MF "a[216:219],v[176:179],v[108:111],a[216:219]\n"
"v_mov_b32 v172,v70\n"
"v_mov_b32 v70,v116\n"
"v_mov_b32 v173,v71\n"
"v_mov_b32 v71,v117\n"
"v_mov_b32 v174,v132\n"
"v_mov_b32 v132,v118\n"
"v_mov_b32 v175,v133\n"
"v_mov_b32 v133,v119\n"
AR "v72,a212\n"
AR "v73,a213\n"
AR "v74,a214\n"
MF "a[236:239],v[180:183],v[108:111],a[236:239]\n"
AR "v75,a215\n"
AR "v76,a232\n"
AR "v77,a233\n"
AR "v78,a234\n"
AR "v79,a235\n"
"v_pk_fma_f32 v[72:73],v[72:73],v[60:61],v[54:55] op_sel:[0,0,1] op_sel_hi:[1,0,1]\n"
"v_pk_fma_f32 v[74:75],v[74:75],v[60:61],v[54:55] op_sel:[0,0,1] op_sel_hi:[1,0,1]\n"
"v_pk_fma_f32 v[76:77],v[76:77],v[60:61],v[54:55] op_sel:[0,0,1] op_sel_hi:[1,0,1]\n"
"v_pk_fma_f32 v[78:79],v[78:79],v[60:61],v[54:55] op_sel:[0,0,1] op_sel_hi:[1,0,1]\n"
CV "v112,v72,v73\n"
CV "v113,v74,v75\n"
CV "v114,v76,v77\n"
CV "v115,v78,v79\n"
"s_waitcnt vmcnt(0) lgkmcnt(0)\n"
"s_barrier\n"
"ds_read_b128 v[92:95],v0\n"
"ds_read_b128 v[96:99],v1\n"
"ds_read_b128 v[100:103],v0 offset:1024\n"
"v_mov_b32 v176,v134\n"
"v_mov_b32 v134,v112\n"
"v_mov_b32 v177,v135\n"
"v_mov_b32 v135,v113\n"
"v_mov_b32 v178,v6\n"
"v_mov_b32 v6,v114\n"
"v_mov_b32 v179,v13\n"
"v_mov_b32 v13,v115\n"
AR "v80,a216\n"
AR "v81,a217\n"
AR "v82,a218\n"
AR "v83,a219\n"
AR "v84,a236\n"
AR "v85,a237\n"
AR "v86,a238\n"
AR "v87,a239\n"
"v_pk_fma_f32 v[80:81],v[80:81],v[60:61],v[56:57] op_sel:[0,1,0] op_sel_hi:[1,1,0]\n"
"v_pk_fma_f32 v[82:83],v[82:83],v[60:61],v[56:57] op_sel:[0,1,0] op_sel_hi:[1,1,0]\n"
"v_pk_fma_f32 v[84:85],v[84:85],v[60:61],v[56:57] op_sel:[0,1,0] op_sel_hi:[1,1,0]\n"
"v_pk_fma_f32 v[86:87],v[86:87],v[60:61],v[56:57] op_sel:[0,1,0] op_sel_hi:[1,1,0]\n"
CV "v116,v80,v81\n"
CV "v117,v82,v83\n"
CV "v118,v84,v85\n"
CV "v119,v86,v87\n"
"v_mov_b32 v180,v14\n"
"v_mov_b32 v14,v116\n"
"v_mov_b32 v181,v15\n"
"v_mov_b32 v15,v117\n"
AR "v182,a254\n"
AW "a254,v118\n"
AR "v183,a255\n"
AW "a255,v119\n"
"s_mov_b32 s13,s11\n"
"v_subrev_u32 v2,49152,v2\n"
"v_subrev_u32 v3,49152,v3\n"
"v_subrev_u32 v4,49152,v4\n"
"v_subrev_u32 v5,49152,v5\n"
"s_mov_b32 s57,2\n"
"s_mov_b32 s69,1\n"
"s_mov_b32 s61,2\n"
"s_getreg_b32 s58,hwreg(HW_REG_XCC_ID,0,1)\n"
"s_mov_b32 s97,170\n"
"s_mul_i32 s95,s58,0x47800000\n"
"s_mov_b32 s77,s95\n"
"s_branch .Latt2\n"
".LpgattB_f:\n"
"s_cbranch_execz .Lisr3\n"
".LattB:\n"
AR "v2,a245\n"
"v_xor_b32 v3,32,v2\n"
"v_xor_b32 v4,64,v2\n"
"v_xor_b32 v5,96,v2\n"
"s_mov_b32 s61,2\n"
".Latt2:\n"
"s_cmp_eq_u32 s57,1\n"
"s_cbranch_scc1 .Lt0k\n"
".Lt0n:\n"
"s_waitcnt lgkmcnt(2)\n"
MF "v[72:75],v[92:95],v[136:139],0\n"
"ds_read_b128 v[104:107],v1 offset:1024\n"
MF "v[76:79],v[92:95],v[140:143],0\n"
MF "v[80:83],v[92:95],v[144:147],0\n"
MF "v[84:87],v[92:95],v[148:151],0\n"
MF "v[88:91],v[92:95],v[152:155],0\n"
"s_waitcnt lgkmcnt(2)\n"
"ds_read_b128 v[92:95],v0 offset:2048\n"
MF "v[72:75],v[96:99],v[156:159],v[72:75]\n"
MF "v[76:79],v[96:99],v[160:163],v[76:79]\n"
MF "v[80:83],v[96:99],v[164:167],v[80:83]\n"
MF "v[84:87],v[96:99],v[168:171],v[84:87]\n"
MF "v[88:91],v[96:99],v[172:175],v[88:91]\n"
"s_waitcnt lgkmcnt(2)\n"
"ds_read_b128 v[96:99],v1 offset:2048\n"
MF "v[72:75],v[100:103],v[176:179],v[72:75]\n"
MF "v[76:79],v[100:103],v[180:183],v[76:79]\n"
MF "v[80:83],v[100:103],v[184:187],v[80:83]\n"
MF "v[84:87],v[100:103],v[188:191],v[84:87]\n"
MF "v[88:91],v[100:103],v[192:195],v[88:91]\n"
"s_waitcnt lgkmcnt(2)\n"
"ds_read_b128 v[100:103],v0 offset:6144\n"
MF "v[72:75],v[104:107],v[196:199],v[72:75]\n"
MF "v[76:79],v[104:107],v[200:203],v[76:79]\n"
MF "v[80:83],v[104:107],v[204:207],v[80:83]\n"
MF "v[84:87],v[104:107],v[208:211],v[84:87]\n"
MF "v[88:91],v[104:107],v[212:215],v[88:91]\n"
"s_waitcnt lgkmcnt(2)\n"
"ds_read_b128 v[104:107],v1 offset:6144\n"
MF "v[72:75],v[92:95],v[216:219],v[72:75]\n"
MF "v[76:79],v[92:95],v[220:223],v[76:79]\n"
MF "v[80:83],v[92:95],v[224:227],v[80:83]\n"
MF "v[84:87],v[92:95],v[228:231],v[84:87]\n"
MF "v[88:91],v[92:95],v[232:235],v[88:91]\n"
"s_waitcnt lgkmcnt(2)\n"
"ds_read_b128 v[92:95],v0 offset:7168\n"
MF "v[72:75],v[96:99],v[236:239],v[72:75]\n"
MF "v[76:79],v[96:99],v[240:243],v[76:79]\n"
MF "v[80:83],v[96:99],v[244:247],v[80:83]\n"
MF "v[84:87],v[96:99],v[248:251],v[84:87]\n"
MF "v[88:91],v[96:99],v[252:255],v[88:91]\n"
"s_waitcnt lgkmcnt(2)\n"
"ds_read_b128 v[96:99],v1 offset:7168\n"
MF "v[112:115],v[100:103],v[136:139],0\n"
MF "v[116:119],v[100:103],v[140:143],0\n"
MF "v[120:123],v[100:103],v[144:147],0\n"
MF "v[124:127],v[100:103],v[148:151],0\n"
MF "v[128:131],v[100:103],v[152:155],0\n"
"s_waitcnt lgkmcnt(2)\n"
"ds_read_b128 v[100:103],v0 offset:8192\n"
MF "v[112:115],v[104:107],v[156:159],v[112:115]\n"
"v_max3_f32 v52,v72,v73,v74\n"
"v_max_f32 v52,v52,v75\n"
MF "v[116:119],v[104:107],v[160:163],v[116:119]\n"
"v_max3_f32 v53,v76,v77,v78\n"
"v_max_f32 v53,v53,v79\n"
MF "v[120:123],v[104:107],v[164:167],v[120:123]\n"
"v_max3_f32 v54,v80,v81,v82\n"
"v_max_f32 v54,v54,v83\n"
MF "v[124:127],v[104:107],v[168:171],v[124:127]\n"
"v_max3_f32 v55,v84,v85,v86\n"
"v_max_f32 v55,v55,v87\n"
MF "v[128:131],v[104:107],v[172:175],v[128:131]\n"
"s_waitcnt lgkmcnt(2)\n"
"ds_read_b128 v[104:107],v1 offset:8192\n"
"v_max3_f32 v56,v88,v89,v90\n"
MF "v[112:115],v[92:95],v[176:179],v[112:115]\n"
"v_max_f32 v56,v56,v91\n"
MF "v[116:119],v[92:95],v[180:183],v[116:119]\n"
MF "v[120:123],v[92:95],v[184:187],v[120:123]\n"
MF "v[124:127],v[92:95],v[188:191],v[124:127]\n"
MF "v[128:131],v[92:95],v[192:195],v[128:131]\n"
"s_waitcnt lgkmcnt(2)\n"
MF "v[112:115],v[96:99],v[196:199],v[112:115]\n"
"s_waitcnt lgkmcnt(0)\n"
MF "v[116:119],v[96:99],v[200:203],v[116:119]\n"
"s_barrier\n"
MF "v[120:123],v[96:99],v[204:207],v[120:123]\n"
"s_add_u32 s72,s72,6144\n"
MF "v[124:127],v[96:99],v[208:211],v[124:127]\n"
"s_cmp_eq_u32 s72,s17\n"
"s_cselect_b32 s72,s16,s72\n"
"s_add_u32 m0,s64,0\n"
MF "v[128:131],v[96:99],v[212:215],v[128:131]\n"
LD "v7,s[32:35],s72 offen nt lds\n"
MF "v[112:115],v[100:103],v[216:219],v[112:115]\n"
"s_add_u32 m0,s64,896\n"
MF "v[116:119],v[100:103],v[220:223],v[116:119]\n"
LD "v7,s[32:35],s72 offen offset:128 nt lds\n"
MF "v[120:123],v[100:103],v[224:227],v[120:123]\n"
"s_add_u32 m0,s64,1792\n"
MF "v[124:127],v[100:103],v[228:231],v[124:127]\n"
LD "v7,s[32:35],s72 offen offset:256 nt lds\n"
MF "v[128:131],v[100:103],v[232:235],v[128:131]\n"
"s_add_u32 s72,s72,6144\n"
"s_add_u32 m0,s64,6144\n"
MF "v[112:115],v[104:107],v[236:239],v[112:115]\n"
LD "v7,s[32:35],s72 offen nt lds\n"
MF "v[116:119],v[104:107],v[240:243],v[116:119]\n"
"s_add_u32 m0,s64,7040\n"
MF "v[120:123],v[104:107],v[244:247],v[120:123]\n"
LD "v7,s[32:35],s72 offen offset:128 nt lds\n"
MF "v[124:127],v[104:107],v[248:251],v[124:127]\n"
"s_add_u32 m0,s64,7936\n"
MF "v[128:131],v[104:107],v[252:255],v[128:131]\n"
".Lpg18g:\n"
"s_cbranch_execz .Lisr6\n"
LD "v7,s[32:35],s72 offen offset:256 nt lds\n"
"s_branch .Lt0e\n"
".Lpgt0k_f:\n"
"s_cbranch_execz .Lisr3\n"
".Lt0k:\n"
"s_lshl_b32 s58,s56,10\n"
"s_add_u32 s58,s58,73728\n"
"v_add_u32 v32,s58,v9\n"
"v_add_u32 v33,65536,v32\n"
"s_waitcnt lgkmcnt(2)\n"
MF "v[72:75],v[92:95],v[136:139],0\n"
"ds_read_b128 v[104:107],v1 offset:1024\n"
AR "v62,a20\n"
MF "v[76:79],v[92:95],v[140:143],0\n"
"ds_write_b128 v32,a[0:3] offset:24576\n"
MF "v[80:83],v[92:95],v[144:147],0\n"
"ds_write_b128 v32,a[4:7] offset:28672\n"
MF "v[84:87],v[92:95],v[148:151],0\n"
AR "v63,a21\n"
AR "v64,a22\n"
MF "v[88:91],v[92:95],v[152:155],0\n"
"s_waitcnt lgkmcnt(4)\n"
"ds_read_b128 v[92:95],v0 offset:2048\n"
AR "v65,a23\n"
MF "v[72:75],v[96:99],v[156:159],v[72:75]\n"
"ds_write_b128 v32,a[8:11] offset:32768\n"
MF "v[76:79],v[96:99],v[160:163],v[76:79]\n"
AR "v66,a24\n"
AR "v67,a25\n"
MF "v[80:83],v[96:99],v[164:167],v[80:83]\n"
"ds_write_b128 v32,a[12:15] offset:36864\n"
MF "v[84:87],v[96:99],v[168:171],v[84:87]\n"
AR "v68,a26\n"
AR "v69,a27\n"
MF "v[88:91],v[96:99],v[172:175],v[88:91]\n"
"s_waitcnt lgkmcnt(6)\n"
"ds_read_b128 v[96:99],v1 offset:2048\n"
AR "v70,a28\n"
MF "v[72:75],v[100:103],v[176:179],v[72:75]\n"
"ds_write_b128 v32,a[16:19] offset:40960\n"
MF "v[76:79],v[100:103],v[180:183],v[76:79]\n"
AR "v71,a29\n"
AR "v132,a30\n"
MF "v[80:83],v[100:103],v[184:187],v[80:83]\n"
AR "v133,a31\n"
AR "v134,a32\n"
MF "v[84:87],v[100:103],v[188:191],v[84:87]\n"
"ds_write_b128 v32,a[40:43] offset:45056\n"
MF "v[88:91],v[100:103],v[192:195],v[88:91]\n"
"s_waitcnt lgkmcnt(8)\n"
"ds_read_b128 v[100:103],v0 offset:6144\n"
AR "v135,a33\n"
MF "v[72:75],v[104:107],v[196:199],v[72:75]\n"
"ds_write_b128 v32,a[44:47] offset:49152\n"
MF "v[76:79],v[104:107],v[200:203],v[76:79]\n"
AR "v6,a34\n"
AR "v13,a35\n"
MF "v[80:83],v[104:107],v[204:207],v[80:83]\n"
AR "v14,a36\n"
AR "v15,a37\n"
MF "v[84:87],v[104:107],v[208:211],v[84:87]\n"
"ds_write_b128 v32,a[48:51] offset:53248\n"
MF "v[88:91],v[104:107],v[212:215],v[88:91]\n"
"s_waitcnt lgkmcnt(8)\n"
"ds_read_b128 v[104:107],v1 offset:6144\n"
"v_accvgpr_mov_b32 a254,a38\n"
MF "v[72:75],v[92:95],v[216:219],v[72:75]\n"
"v_accvgpr_mov_b32 a255,a39\n"
"v_accvgpr_mov_b32 a240,a168\n"
MF "v[76:79],v[92:95],v[220:223],v[76:79]\n"
"ds_write_b128 v32,a[52:55] offset:57344\n"
MF "v[80:83],v[92:95],v[224:227],v[80:83]\n"
"v_accvgpr_mov_b32 a241,a169\n"
"v_accvgpr_mov_b32 a242,a170\n"
MF "v[84:87],v[92:95],v[228:231],v[84:87]\n"
"ds_write_b128 v32,a[56:59] offset:61440\n"
MF "v[88:91],v[92:95],v[232:235],v[88:91]\n"
"s_waitcnt lgkmcnt(8)\n"
"ds_read_b128 v[92:95],v0 offset:7168\n"
"v_accvgpr_mov_b32 a243,a171\n"
MF "v[72:75],v[96:99],v[236:239],v[72:75]\n"
"v_accvgpr_mov_b32 a244,a172\n"
"v_accvgpr_mov_b32 a245,a173\n"
MF "v[76:79],v[96:99],v[240:243],v[76:79]\n"
"ds_write_b128 v33,a[80:83]\n"
MF "v[80:83],v[96:99],v[244:247],v[80:83]\n"
"v_accvgpr_mov_b32 a246,a174\n"
"v_accvgpr_mov_b32 a247,a175\n"
MF "v[84:87],v[96:99],v[248:251],v[84:87]\n"
"v_accvgpr_mov_b32 a248,a176\n"
"v_accvgpr_mov_b32 a249,a177\n"
MF "v[88:91],v[96:99],v[252:255],v[88:91]\n"
"s_waitcnt lgkmcnt(7)\n"
"ds_read_b128 v[96:99],v1 offset:7168\n"
"v_accvgpr_mov_b32 a250,a178\n"
MF "v[112:115],v[100:103],v[136:139],0\n"
"ds_write_b128 v33,a[84:87] offset:4096\n"
MF "v[116:119],v[100:103],v[140:143],0\n"
"ds_write_b128 v33,a[88:91] offset:8192\n"
MF "v[120:123],v[100:103],v[144:147],0\n"
"v_accvgpr_mov_b32 a251,a179\n"
MF "v[124:127],v[100:103],v[148:151],0\n"
MF "v[128:131],v[100:103],v[152:155],0\n"
"s_waitcnt lgkmcnt(7)\n"
"ds_read_b128 v[100:103],v0 offset:8192\n"
MF "v[112:115],v[104:107],v[156:159],v[112:115]\n"
"v_max3_f32 v52,v72,v73,v74\n"
"v_max_f32 v52,v52,v75\n"
MF "v[116:119],v[104:107],v[160:163],v[116:119]\n"
"v_max3_f32 v53,v76,v77,v78\n"
"v_max_f32 v53,v53,v79\n"
MF "v[120:123],v[104:107],v[164:167],v[120:123]\n"
"v_max3_f32 v54,v80,v81,v82\n"
"v_max_f32 v54,v54,v83\n"
MF "v[124:127],v[104:107],v[168:171],v[124:127]\n"
"v_max3_f32 v55,v84,v85,v86\n"
"v_max_f32 v55,v55,v87\n"
MF "v[128:131],v[104:107],v[172:175],v[128:131]\n"
"s_waitcnt lgkmcnt(5)\n"
"ds_read_b128 v[104:107],v1 offset:8192\n"
"v_max3_f32 v56,v88,v89,v90\n"
MF "v[112:115],v[92:95],v[176:179],v[112:115]\n"
"v_max_f32 v56,v56,v91\n"
MF "v[116:119],v[92:95],v[180:183],v[116:119]\n"
"ds_write_b128 v33,a[92:95] offset:12288\n"
MF "v[120:123],v[92:95],v[184:187],v[120:123]\n"
"ds_write_b128 v33,a[96:99] offset:16384\n"
MF "v[124:127],v[92:95],v[188:191],v[124:127]\n"
"ds_write_b128 v33,a[120:123] offset:20480\n"
MF "v[128:131],v[92:95],v[192:195],v[128:131]\n"
"s_waitcnt lgkmcnt(7)\n"
"ds_write_b128 v32,a[124:127]\n"
MF "v[112:115],v[96:99],v[196:199],v[112:115]\n"
"s_waitcnt lgkmcnt(0)\n"
MF "v[116:119],v[96:99],v[200:203],v[116:119]\n"
"s_barrier\n"
MF "v[120:123],v[96:99],v[204:207],v[120:123]\n"
"s_add_u32 s72,s72,6144\n"
"ds_write_b128 v32,a[128:131] offset:4096\n"
MF "v[124:127],v[96:99],v[208:211],v[124:127]\n"
"s_cmp_eq_u32 s72,s17\n"
"s_cselect_b32 s72,s16,s72\n"
"s_add_u32 m0,s64,0\n"
MF "v[128:131],v[96:99],v[212:215],v[128:131]\n"
LD "v7,s[32:35],s72 offen nt lds\n"
MF "v[112:115],v[100:103],v[216:219],v[112:115]\n"
"s_add_u32 m0,s64,896\n"
"ds_write_b128 v32,a[132:135] offset:8192\n"
MF "v[116:119],v[100:103],v[220:223],v[116:119]\n"
LD "v7,s[32:35],s72 offen offset:128 nt lds\n"
MF "v[120:123],v[100:103],v[224:227],v[120:123]\n"
"s_add_u32 m0,s64,1792\n"
"ds_write_b128 v32,a[136:139] offset:12288\n"
MF "v[124:127],v[100:103],v[228:231],v[124:127]\n"
LD "v7,s[32:35],s72 offen offset:256 nt lds\n"
MF "v[128:131],v[100:103],v[232:235],v[128:131]\n"
"s_add_u32 s72,s72,6144\n"
"s_add_u32 m0,s64,6144\n"
MF "v[112:115],v[104:107],v[236:239],v[112:115]\n"
LD "v7,s[32:35],s72 offen nt lds\n"
MF "v[116:119],v[104:107],v[240:243],v[116:119]\n"
"s_add_u32 m0,s64,7040\n"
"ds_write_b128 v32,a[160:163] offset:16384\n"
MF "v[120:123],v[104:107],v[244:247],v[120:123]\n"
LD "v7,s[32:35],s72 offen offset:128 nt lds\n"
MF "v[124:127],v[104:107],v[248:251],v[124:127]\n"
"s_add_u32 m0,s64,7936\n"
"ds_write_b128 v32,a[164:167] offset:20480\n"
MF "v[128:131],v[104:107],v[252:255],v[128:131]\n"
LD "v7,s[32:35],s72 offen offset:256 nt lds\n"
".Lpgt0e_g:\n"
"s_cbranch_execz .Lisr6\n"
".Lt0e:\n"
"s_nop 1\n"
"v_max3_f32 v52,v52,v112,v113\n"
"v_max3_f32 v53,v53,v116,v117\n"
"v_max3_f32 v54,v54,v120,v121\n"
"v_max3_f32 v55,v55,v124,v125\n"
"s_nop 1\n"
"v_max3_f32 v56,v56,v128,v129\n"
"v_max3_f32 v52,v52,v114,v115\n"
"v_max3_f32 v53,v53,v118,v119\n"
"v_max3_f32 v54,v54,v122,v123\n"
"v_max3_f32 v55,v55,v126,v127\n"
"v_max3_f32 v56,v56,v130,v131\n"
"v_mov_b32 v57,v52\n"
"v_mov_b32 v58,v53\n"
"v_mov_b32 v59,v54\n"
"v_mov_b32 v60,v55\n"
"v_mov_b32 v61,v56\n"
"v_permlane16_swap_b32 v52,v57\n"
"v_permlane16_swap_b32 v53,v58\n"
"v_permlane16_swap_b32 v54,v59\n"
"v_permlane16_swap_b32 v55,v60\n"
"v_permlane16_swap_b32 v56,v61\n"
"v_max_f32 v52,v52,v57\n"
"v_max_f32 v53,v53,v58\n"
"v_max_f32 v54,v54,v59\n"
"v_max_f32 v55,v55,v60\n"
"v_max_f32 v56,v56,v61\n"
"v_mov_b32 v57,v52\n"
"v_mov_b32 v58,v53\n"
"v_mov_b32 v59,v54\n"
"v_mov_b32 v60,v55\n"
"v_mov_b32 v61,v56\n"
"v_permlane32_swap_b32 v52,v57\n"
"v_permlane32_swap_b32 v53,v58\n"
"v_permlane32_swap_b32 v54,v59\n"
"v_permlane32_swap_b32 v55,v60\n"
"v_permlane32_swap_b32 v56,v61\n"
"v_max_f32 v52,v52,v57\n"
"v_max_f32 v53,v53,v58\n"
"v_max_f32 v54,v54,v59\n"
"v_max_f32 v55,v55,v60\n"
"v_max_f32_e64 v56,v56,v61\n"
"v_sub_f32 v32,0xc1300000,v52\n"
"v_sub_f32 v36,0xc1300000,v53\n"
"v_sub_f32 v40,0xc1300000,v54\n"
"v_sub_f32 v44,0xc1300000,v55\n"
"v_sub_f32 v48,0xc1300000,v56\n"
"v_mov_b32 v33,v32\n"
"v_pk_mov_b32 v[34:35],v[32:33],v[32:33]\n"
"v_mov_b32 v37,v36\n"
"v_pk_mov_b32 v[38:39],v[36:37],v[36:37]\n"
"v_mov_b32 v41,v40\n"
"v_pk_mov_b32 v[42:43],v[40:41],v[40:41]\n"
"v_mov_b32 v45,v44\n"
"v_pk_mov_b32 v[46:47],v[44:45],v[44:45]\n"
"v_mov_b32_e64 v49,v48\n"
"v_pk_mov_b32 v[50:51],v[48:49],v[48:49]\n"
"v_pk_add_f32 v[72:73],v[72:73],v[32:33]\n"
"v_pk_add_f32 v[74:75],v[74:75],v[32:33]\n"
"v_pk_add_f32 v[112:113],v[112:113],v[32:33]\n"
"v_pk_add_f32 v[114:115],v[114:115],v[32:33]\n"
"v_exp_f32 v72,v72\n"
"v_exp_f32 v73,v73\n"
"v_exp_f32 v74,v74\n"
"v_exp_f32 v75,v75\n"
"v_exp_f32 v112,v112\n"
"v_exp_f32 v113,v113\n"
"v_exp_f32 v114,v114\n"
"v_exp_f32 v115,v115\n"
"v_pk_add_f32 v[10:11],v[72:73],v[74:75]\n"
"v_pk_add_f32 v[28:29],v[112:113],v[114:115]\n"
"v_pk_add_f32 v[10:11],v[10:11],v[28:29]\n"
"v_add_f32_e64 v52,v10,v11\n"
CV "v72,v72,v73\n"
CV "v73,v74,v75\n"
CV "v74,v112,v113\n"
CV "v75,v114,v115\n"
"v_pk_add_f32 v[76:77],v[76:77],v[36:37]\n"
"v_pk_add_f32 v[78:79],v[78:79],v[36:37]\n"
"v_pk_add_f32 v[116:117],v[116:117],v[36:37]\n"
"v_pk_add_f32 v[118:119],v[118:119],v[36:37]\n"
"v_exp_f32 v76,v76\n"
"v_exp_f32 v77,v77\n"
"v_exp_f32 v78,v78\n"
"v_exp_f32 v79,v79\n"
"v_exp_f32 v116,v116\n"
"v_exp_f32 v117,v117\n"
"v_exp_f32 v118,v118\n"
"v_exp_f32 v119,v119\n"
"v_pk_add_f32 v[10:11],v[76:77],v[78:79]\n"
"v_pk_add_f32 v[28:29],v[116:117],v[118:119]\n"
"v_pk_add_f32 v[10:11],v[10:11],v[28:29]\n"
"v_add_f32_e64 v53,v10,v11\n"
CV "v76,v76,v77\n"
CV "v77,v78,v79\n"
CV "v78,v116,v117\n"
CV "v79,v118,v119\n"
"v_pk_add_f32 v[80:81],v[80:81],v[40:41]\n"
"v_pk_add_f32 v[82:83],v[82:83],v[40:41]\n"
"v_pk_add_f32 v[120:121],v[120:121],v[40:41]\n"
"v_pk_add_f32 v[122:123],v[122:123],v[40:41]\n"
"v_exp_f32 v80,v80\n"
"v_exp_f32 v81,v81\n"
"v_exp_f32 v82,v82\n"
"v_exp_f32 v83,v83\n"
"v_exp_f32 v120,v120\n"
"v_exp_f32 v121,v121\n"
"v_exp_f32 v122,v122\n"
"v_exp_f32 v123,v123\n"
"v_pk_add_f32 v[10:11],v[80:81],v[82:83]\n"
"v_pk_add_f32 v[28:29],v[120:121],v[122:123]\n"
"v_pk_add_f32 v[10:11],v[10:11],v[28:29]\n"
"v_add_f32_e64 v54,v10,v11\n"
CV "v80,v80,v81\n"
CV "v81,v82,v83\n"
CV "v82,v120,v121\n"
CV "v83,v122,v123\n"
"v_pk_add_f32 v[84:85],v[84:85],v[44:45]\n"
"v_pk_add_f32 v[86:87],v[86:87],v[44:45]\n"
"v_pk_add_f32 v[124:125],v[124:125],v[44:45]\n"
"v_pk_add_f32 v[126:127],v[126:127],v[44:45]\n"
"v_mov_b32_e64 v55,0\n"
"v_pk_add_f32 v[88:89],v[88:89],v[48:49]\n"
"v_pk_add_f32 v[90:91],v[90:91],v[48:49]\n"
"v_pk_add_f32 v[128:129],v[128:129],v[48:49]\n"
"v_pk_add_f32 v[130:131],v[130:131],v[48:49]\n"
"v_mov_b32 v56,0\n"
"s_waitcnt vmcnt(12) lgkmcnt(0)\n"
"s_barrier\n"
"ds_read_b128 v[16:19],v0 offset:24576\n"
"ds_read_b128 v[20:23],v1 offset:24576\n"
AW "a220,s13\n"
AW "a221,s13\n"
AW "a222,s13\n"
AW "a223,s13\n"
"s_waitcnt lgkmcnt(1)\n"
MF "v[92:95],v[16:19],v[136:139],v[32:35]\n"
"s_nop 0\n"
"ds_read_b128 v[24:27],v0 offset:25600\n"
"ds_read_b128 v[28:31],v1 offset:25600\n"
"s_waitcnt lgkmcnt(2)\n"
MF "v[92:95],v[20:23],v[156:159],v[92:95]\n"
"v_exp_f32 v84,v84\n"
MF "v[96:99],v[20:23],v[160:163],v[36:39]\n"
"v_exp_f32 v85,v85\n"
MF "v[96:99],v[16:19],v[140:143],v[96:99]\n"
"v_exp_f32 v86,v86\n"
MF "v[100:103],v[16:19],v[144:147],v[40:43]\n"
"v_exp_f32 v87,v87\n"
MF "v[100:103],v[20:23],v[164:167],v[100:103]\n"
"v_add_f32 v55,v55,v84\n"
"v_add_f32 v55,v55,v85\n"
MF "v[104:107],v[20:23],v[168:171],v[44:47]\n"
CV "v84,v84,v85\n"
"v_add_f32 v55,v55,v86\n"
MF "v[104:107],v[16:19],v[148:151],v[104:107]\n"
"v_add_f32 v55,v55,v87\n"
CV "v85,v86,v87\n"
MF "v[108:111],v[16:19],v[152:155],v[48:51]\n"
"v_exp_f32 v124,v124\n"
MF "v[108:111],v[20:23],v[172:175],v[108:111]\n"
"s_waitcnt lgkmcnt(0)\n"
"ds_read_b128 v[16:19],v0 offset:26624\n"
"ds_read_b128 v[20:23],v1 offset:26624\n"
MF "v[108:111],v[24:27],v[192:195],v[108:111]\n"
"v_exp_f32 v125,v125\n"
MF "v[108:111],v[28:31],v[212:215],v[108:111]\n"
"v_exp_f32 v126,v126\n"
MF "v[104:107],v[28:31],v[208:211],v[104:107]\n"
"v_exp_f32 v127,v127\n"
MF "v[104:107],v[24:27],v[188:191],v[104:107]\n"
"v_add_f32 v55,v55,v124\n"
"v_add_f32 v55,v55,v125\n"
MF "v[100:103],v[24:27],v[184:187],v[100:103]\n"
CV "v86,v124,v125\n"
"v_add_f32 v55,v55,v126\n"
MF "v[100:103],v[28:31],v[204:207],v[100:103]\n"
"v_add_f32 v55,v55,v127\n"
CV "v87,v126,v127\n"
MF "v[96:99],v[28:31],v[200:203],v[96:99]\n"
"v_exp_f32 v88,v88\n"
MF "v[96:99],v[24:27],v[180:183],v[96:99]\n"
"v_exp_f32 v89,v89\n"
MF "v[92:95],v[24:27],v[176:179],v[92:95]\n"
"v_exp_f32 v90,v90\n"
MF "v[92:95],v[28:31],v[196:199],v[92:95]\n"
"s_waitcnt lgkmcnt(0)\n"
"ds_read_b128 v[24:27],v0 offset:30720\n"
"ds_read_b128 v[28:31],v1 offset:30720\n"
MF "v[92:95],v[16:19],v[216:219],v[92:95]\n"
"v_exp_f32 v91,v91\n"
MF "v[92:95],v[20:23],v[236:239],v[92:95]\n"
"v_add_f32 v56,v56,v88\n"
"v_add_f32 v56,v56,v89\n"
MF "v[96:99],v[20:23],v[240:243],v[96:99]\n"
CV "v88,v88,v89\n"
"v_add_f32 v56,v56,v90\n"
MF "v[96:99],v[16:19],v[220:223],v[96:99]\n"
"v_add_f32 v56,v56,v91\n"
CV "v89,v90,v91\n"
MF "v[100:103],v[16:19],v[224:227],v[100:103]\n"
"v_exp_f32 v128,v128\n"
MF "v[100:103],v[20:23],v[244:247],v[100:103]\n"
"v_exp_f32 v129,v129\n"
MF "v[104:107],v[20:23],v[248:251],v[104:107]\n"
"v_exp_f32 v130,v130\n"
MF "v[104:107],v[16:19],v[228:231],v[104:107]\n"
"v_exp_f32 v131,v131\n"
MF "v[108:111],v[16:19],v[232:235],v[108:111]\n"
"v_add_f32 v56,v56,v128\n"
"v_add_f32 v56,v56,v129\n"
MF "v[108:111],v[20:23],v[252:255],v[108:111]\n"
"s_waitcnt lgkmcnt(0)\n"
"ds_read_b128 v[16:19],v0 offset:31744\n"
"ds_read_b128 v[20:23],v1 offset:31744\n"
MF "v[112:115],v[24:27],v[136:139],v[32:35]\n"
CV "v90,v128,v129\n"
"v_add_f32 v56,v56,v130\n"
MF "v[112:115],v[28:31],v[156:159],v[112:115]\n"
"v_add_f32 v56,v56,v131\n"
CV "v91,v130,v131\n"
MF "v[116:119],v[28:31],v[160:163],v[36:39]\n"
"v_exp_f32 v92,v92\n"
MF "v[116:119],v[24:27],v[140:143],v[116:119]\n"
"v_exp_f32 v93,v93\n"
MF "v[120:123],v[24:27],v[144:147],v[40:43]\n"
"v_exp_f32 v94,v94\n"
MF "v[120:123],v[28:31],v[164:167],v[120:123]\n"
"v_exp_f32 v95,v95\n"
MF "v[124:127],v[28:31],v[168:171],v[44:47]\n"
"v_add_f32 v57,v52,v92\n"
"v_add_f32 v57,v57,v93\n"
MF "v[124:127],v[24:27],v[148:151],v[124:127]\n"
CV "v92,v92,v93\n"
"v_add_f32 v57,v57,v94\n"
MF "v[128:131],v[24:27],v[152:155],v[48:51]\n"
"v_add_f32 v57,v57,v95\n"
CV "v93,v94,v95\n"
MF "v[128:131],v[28:31],v[172:175],v[128:131]\n"
"s_waitcnt lgkmcnt(0)\n"
"ds_read_b128 v[24:27],v0 offset:32768\n"
"ds_read_b128 v[28:31],v1 offset:32768\n"
MF "v[128:131],v[16:19],v[192:195],v[128:131]\n"
"v_exp_f32 v96,v96\n"
MF "v[128:131],v[20:23],v[212:215],v[128:131]\n"
"v_exp_f32 v97,v97\n"
MF "v[124:127],v[20:23],v[208:211],v[124:127]\n"
"v_exp_f32 v98,v98\n"
MF "v[124:127],v[16:19],v[188:191],v[124:127]\n"
"v_exp_f32 v99,v99\n"
MF "v[120:123],v[16:19],v[184:187],v[120:123]\n"
"v_add_f32 v58,v53,v96\n"
"v_add_f32 v58,v58,v97\n"
MF "v[120:123],v[20:23],v[204:207],v[120:123]\n"
CV "v96,v96,v97\n"
"v_add_f32 v58,v58,v98\n"
MF "v[116:119],v[20:23],v[200:203],v[116:119]\n"
"s_waitcnt vmcnt(6) lgkmcnt(0)\n"
"v_add_f32 v58,v58,v99\n"
CV "v97,v98,v99\n"
MF "v[116:119],v[16:19],v[180:183],v[116:119]\n"
"s_barrier\n"
MF "v[112:115],v[16:19],v[176:179],v[112:115]\n"
"v_exp_f32 v100,v100\n"
MF "v[112:115],v[20:23],v[196:199],v[112:115]\n"
"s_nop 0\n"
TR "v[16:17],v2 offset:49152\n"
TR "v[18:19],v2 offset:55296\n"
MF "v[112:115],v[24:27],v[216:219],v[112:115]\n"
"s_add_u32 s73,s73,12288\n"
TR "v[20:21],v3 offset:49152\n"
TR "v[22:23],v3 offset:55296\n"
MF "v[112:115],v[28:31],v[236:239],v[112:115]\n"
"s_cmp_eq_u32 s73,s19\n"
"s_cselect_b32 s73,s18,s73\n"
"s_add_u32 m0,s65,24576\n"
MF "v[116:119],v[28:31],v[240:243],v[116:119]\n"
LD "v8,s[36:39],s73 offen nt lds\n"
MF "v[116:119],v[24:27],v[220:223],v[116:119]\n"
"s_add_u32 m0,s65,25472\n"
"v_exp_f32 v101,v101\n"
MF "v[120:123],v[24:27],v[224:227],v[120:123]\n"
LD "v8,s[36:39],s73 offen offset:128 nt lds\n"
MF "v[120:123],v[28:31],v[244:247],v[120:123]\n"
"v_exp_f32 v102,v102\n"
MF "v[124:127],v[28:31],v[248:251],v[124:127]\n"
"s_add_u32 m0,s65,26368\n"
"v_exp_f32 v103,v103\n"
MF "v[124:127],v[24:27],v[228:231],v[124:127]\n"
LD "v8,s[36:39],s73 offen offset:256 nt lds\n"
MF "v[128:131],v[24:27],v[232:235],v[128:131]\n"
"v_add_f32 v59,v54,v100\n"
"v_add_f32 v59,v59,v101\n"
MF "v[128:131],v[28:31],v[252:255],v[128:131]\n"
"s_waitcnt lgkmcnt(0)\n"
TR "v[24:25],v4 offset:49152\n"
TR "v[26:27],v4 offset:55296\n"
MF "a[0:3],v[16:19],v[72:75],a[220:223]\n"
"s_add_u32 m0,s65,24576\n"
TR "v[28:29],v5 offset:49152\n"
TR "v[30:31],v5 offset:55296\n"
MF "a[20:23],v[20:23],v[72:75],a[220:223]\n"
LD "v8,s[36:39],s73 offen offset:3072 nt lds\n"
MF "a[24:27],v[20:23],v[76:79],a[220:223]\n"
CV "v100,v100,v101\n"
"v_add_f32 v59,v59,v102\n"
MF "a[4:7],v[16:19],v[76:79],a[220:223]\n"
"s_add_u32 m0,s65,25472\n"
"v_add_f32 v59,v59,v103\n"
CV "v101,v102,v103\n"
MF "a[8:11],v[16:19],v[80:83],a[220:223]\n"
LD "v8,s[36:39],s73 offen offset:3200 nt lds\n"
MF "a[28:31],v[20:23],v[80:83],a[220:223]\n"
"v_exp_f32 v104,v104\n"
MF "a[32:35],v[20:23],v[84:87],a[220:223]\n"
"s_add_u32 m0,s65,26368\n"
"v_exp_f32 v105,v105\n"
MF "a[12:15],v[16:19],v[84:87],a[220:223]\n"
LD "v8,s[36:39],s73 offen offset:3328 nt lds\n"
MF "a[16:19],v[16:19],v[88:91],a[220:223]\n"
"v_exp_f32 v106,v106\n"
MF "a[36:39],v[20:23],v[88:91],a[220:223]\n"
"s_waitcnt lgkmcnt(0)\n"
TR "v[16:17],v2 offset:50176\n"
TR "v[18:19],v2 offset:56320\n"
MF "a[56:59],v[24:27],v[88:91],a[220:223]\n"
"s_nop 0\n"
TR "v[20:21],v3 offset:50176\n"
TR "v[22:23],v3 offset:56320\n"
MF "a[76:79],v[28:31],v[88:91],a[220:223]\n"
"v_exp_f32 v107,v107\n"
MF "a[72:75],v[28:31],v[84:87],a[220:223]\n"
"v_add_f32 v60,v55,v104\n"
"v_add_f32 v60,v60,v105\n"
MF "a[52:55],v[24:27],v[84:87],a[220:223]\n"
CV "v104,v104,v105\n"
"v_add_f32 v60,v60,v106\n"
MF "a[48:51],v[24:27],v[80:83],a[220:223]\n"
"v_add_f32 v60,v60,v107\n"
CV "v105,v106,v107\n"
MF "a[68:71],v[28:31],v[80:83],a[220:223]\n"
"v_exp_f32 v108,v108\n"
MF "a[64:67],v[28:31],v[76:79],a[220:223]\n"
"v_exp_f32 v109,v109\n"
MF "a[44:47],v[24:27],v[76:79],a[220:223]\n"
"v_exp_f32 v110,v110\n"
MF "a[40:43],v[24:27],v[72:75],a[220:223]\n"
"v_exp_f32 v111,v111\n"
MF "a[60:63],v[28:31],v[72:75],a[220:223]\n"
"s_waitcnt lgkmcnt(0)\n"
TR "v[24:25],v4 offset:50176\n"
TR "v[26:27],v4 offset:56320\n"
MF "a[80:83],v[16:19],v[72:75],a[220:223]\n"
"s_nop 0\n"
TR "v[28:29],v5 offset:50176\n"
TR "v[30:31],v5 offset:56320\n"
MF "a[100:103],v[20:23],v[72:75],a[220:223]\n"
"v_add_f32 v61,v56,v108\n"
"v_add_f32 v61,v61,v109\n"
MF "a[104:107],v[20:23],v[76:79],a[220:223]\n"
CV "v108,v108,v109\n"
"v_add_f32 v61,v61,v110\n"
MF "a[84:87],v[16:19],v[76:79],a[220:223]\n"
"v_add_f32 v61,v61,v111\n"
CV "v109,v110,v111\n"
MF "a[88:91],v[16:19],v[80:83],a[220:223]\n"
"v_exp_f32 v112,v112\n"
MF "a[108:111],v[20:23],v[80:83],a[220:223]\n"
"v_exp_f32 v113,v113\n"
MF "a[112:115],v[20:23],v[84:87],a[220:223]\n"
"v_exp_f32 v114,v114\n"
MF "a[92:95],v[16:19],v[84:87],a[220:223]\n"
"v_exp_f32 v115,v115\n"
MF "a[96:99],v[16:19],v[88:91],a[220:223]\n"
"v_add_f32 v57,v57,v112\n"
"v_add_f32 v57,v57,v113\n"
MF "a[116:119],v[20:23],v[88:91],a[220:223]\n"
"s_waitcnt lgkmcnt(0)\n"
TR "v[16:17],v2 offset:51200\n"
TR "v[18:19],v2 offset:57344\n"
MF "a[136:139],v[24:27],v[88:91],a[220:223]\n"
"s_nop 0\n"
TR "v[20:21],v3 offset:51200\n"
TR "v[22:23],v3 offset:57344\n"
MF "a[156:159],v[28:31],v[88:91],a[220:223]\n"
CV "v94,v112,v113\n"
"v_add_f32 v57,v57,v114\n"
MF "a[152:155],v[28:31],v[84:87],a[220:223]\n"
"v_add_f32 v57,v57,v115\n"
CV "v95,v114,v115\n"
MF "a[132:135],v[24:27],v[84:87],a[220:223]\n"
"v_exp_f32 v116,v116\n"
MF "a[128:131],v[24:27],v[80:83],a[220:223]\n"
"v_exp_f32 v117,v117\n"
MF "a[148:151],v[28:31],v[80:83],a[220:223]\n"
"v_exp_f32 v118,v118\n"
MF "a[144:147],v[28:31],v[76:79],a[220:223]\n"
"v_exp_f32 v119,v119\n"
MF "a[124:127],v[24:27],v[76:79],a[220:223]\n"
"v_add_f32 v58,v58,v116\n"
"v_add_f32 v58,v58,v117\n"
MF "a[120:123],v[24:27],v[72:75],a[220:223]\n"
CV "v98,v116,v117\n"
"v_add_f32 v58,v58,v118\n"
MF "a[140:143],v[28:31],v[72:75],a[220:223]\n"
"s_waitcnt lgkmcnt(0)\n"
TR "v[24:25],v4 offset:51200\n"
TR "v[26:27],v4 offset:57344\n"
MF "a[160:163],v[16:19],v[72:75],a[220:223]\n"
"s_nop 0\n"
TR "v[28:29],v5 offset:51200\n"
TR "v[30:31],v5 offset:57344\n"
MF "a[180:183],v[20:23],v[72:75],a[220:223]\n"
"v_add_f32 v58,v58,v119\n"
CV "v99,v118,v119\n"
MF "a[184:187],v[20:23],v[76:79],a[220:223]\n"
"v_exp_f32 v120,v120\n"
MF "a[164:167],v[16:19],v[76:79],a[220:223]\n"
"v_exp_f32 v121,v121\n"
MF "a[168:171],v[16:19],v[80:83],a[220:223]\n"
"v_exp_f32 v122,v122\n"
MF "a[188:191],v[20:23],v[80:83],a[220:223]\n"
"v_exp_f32 v123,v123\n"
MF "a[192:195],v[20:23],v[84:87],a[220:223]\n"
"v_add_f32 v59,v59,v120\n"
"v_add_f32 v59,v59,v121\n"
MF "a[172:175],v[16:19],v[84:87],a[220:223]\n"
"s_waitcnt vmcnt(6) lgkmcnt(0)\n"
CV "v102,v120,v121\n"
"v_add_f32 v59,v59,v122\n"
MF "a[176:179],v[16:19],v[88:91],a[220:223]\n"
"s_barrier\n"
MF "a[196:199],v[20:23],v[88:91],a[220:223]\n"
"s_add_u32 s72,s72,6144\n"
"ds_read_b128 v[16:19],v0\n"
"ds_read_b128 v[20:23],v1\n"
MF "a[216:219],v[24:27],v[88:91],a[220:223]\n"
"s_cmp_eq_u32 s72,s17\n"
"s_cselect_b32 s72,s16,s72\n"
"s_add_u32 m0,s64,49152\n"
MF "a[236:239],v[28:31],v[88:91],a[220:223]\n"
LD "v7,s[32:35],s72 offen nt lds\n"
MF "a[232:235],v[28:31],v[84:87],a[220:223]\n"
"s_add_u32 m0,s64,50048\n"
"v_add_f32 v59,v59,v123\n"
CV "v103,v122,v123\n"
MF "a[212:215],v[24:27],v[84:87],a[220:223]\n"
LD "v7,s[32:35],s72 offen offset:128 nt lds\n"
MF "a[208:211],v[24:27],v[80:83],a[220:223]\n"
"s_add_u32 m0,s64,50944\n"
MF "a[228:231],v[28:31],v[80:83],a[220:223]\n"
LD "v7,s[32:35],s72 offen offset:256 nt lds\n"
MF "a[224:227],v[28:31],v[76:79],a[220:223]\n"
".Lpg19f:\n"
"s_cbranch_execz .Lisr3\n"
".Lpg19g:\n"
"s_cbranch_execz .Lisr6\n"
MF "a[204:207],v[24:27],v[76:79],a[220:223]\n"
MF "a[200:203],v[24:27],v[72:75],a[220:223]\n"
MF "a[220:223],v[28:31],v[72:75],a[220:223]\n"
"s_waitcnt lgkmcnt(0)\n"
".Laloop:\n"
MF "v[72:75],v[16:19],v[136:139],v[32:35]\n"
"s_nop 0\n"
"ds_read_b128 v[24:27],v0 offset:1024\n"
"ds_read_b128 v[28:31],v1 offset:1024\n"
MF "v[72:75],v[20:23],v[156:159],v[72:75]\n"
MF "v[76:79],v[20:23],v[160:163],v[36:39]\n"
MF "v[76:79],v[16:19],v[140:143],v[76:79]\n"
MF "v[80:83],v[16:19],v[144:147],v[40:43]\n"
MF "v[80:83],v[20:23],v[164:167],v[80:83]\n"
MF "v[84:87],v[20:23],v[168:171],v[44:47]\n"
MF "v[84:87],v[16:19],v[148:151],v[84:87]\n"
"s_add_u32 s72,s72,6144\n"
"s_add_u32 m0,s64,55296\n"
MF "v[88:91],v[16:19],v[152:155],v[48:51]\n"
LD "v7,s[32:35],s72 offen nt lds\n"
MF "v[88:91],v[20:23],v[172:175],v[88:91]\n"
"s_waitcnt lgkmcnt(0)\n"
"ds_read_b128 v[16:19],v0 offset:2048\n"
"ds_read_b128 v[20:23],v1 offset:2048\n"
MF "v[88:91],v[24:27],v[192:195],v[88:91]\n"
"v_exp_f32 v124,v124\n"
MF "v[88:91],v[28:31],v[212:215],v[88:91]\n"
"s_add_u32 m0,s64,56192\n"
"v_exp_f32 v125,v125\n"
MF "v[84:87],v[28:31],v[208:211],v[84:87]\n"
LD "v7,s[32:35],s72 offen offset:128 nt lds\n"
MF "v[84:87],v[24:27],v[188:191],v[84:87]\n"
"v_exp_f32 v126,v126\n"
MF "v[80:83],v[24:27],v[184:187],v[80:83]\n"
"v_exp_f32 v127,v127\n"
MF "v[80:83],v[28:31],v[204:207],v[80:83]\n"
"s_add_u32 m0,s64,57088\n"
"v_add_f32 v60,v60,v124\n"
"v_add_f32 v60,v60,v125\n"
MF "v[76:79],v[28:31],v[200:203],v[76:79]\n"
LD "v7,s[32:35],s72 offen offset:256 nt lds\n"
MF "v[76:79],v[24:27],v[180:183],v[76:79]\n"
CV "v106,v124,v125\n"
"v_add_f32 v60,v60,v126\n"
MF "v[72:75],v[24:27],v[176:179],v[72:75]\n"
"v_add_f32 v60,v60,v127\n"
CV "v107,v126,v127\n"
MF "v[72:75],v[28:31],v[196:199],v[72:75]\n"
"s_waitcnt lgkmcnt(0)\n"
"ds_read_b128 v[24:27],v0 offset:6144\n"
"ds_read_b128 v[28:31],v1 offset:6144\n"
MF "v[72:75],v[16:19],v[216:219],v[72:75]\n"
"v_exp_f32 v128,v128\n"
MF "v[72:75],v[20:23],v[236:239],v[72:75]\n"
"v_exp_f32 v129,v129\n"
MF "v[76:79],v[20:23],v[240:243],v[76:79]\n"
"v_exp_f32 v130,v130\n"
MF "v[76:79],v[16:19],v[220:223],v[76:79]\n"
"v_exp_f32 v131,v131\n"
MF "v[80:83],v[16:19],v[224:227],v[80:83]\n"
"v_add_f32 v61,v61,v128\n"
"v_add_f32 v61,v61,v129\n"
MF "v[80:83],v[20:23],v[244:247],v[80:83]\n"
CV "v110,v128,v129\n"
"v_add_f32 v61,v61,v130\n"
MF "v[84:87],v[20:23],v[248:251],v[84:87]\n"
"v_add_f32 v61,v61,v131\n"
CV "v111,v130,v131\n"
MF "v[84:87],v[16:19],v[228:231],v[84:87]\n"
"v_max3_f32 v10,v57,v58,v59\n"
"v_max3_f32 v10,v10,v60,v61\n"
MF "v[88:91],v[16:19],v[232:235],v[88:91]\n"
"v_cmp_ngt_f32 vcc,s77,v10\n"
MF "v[88:91],v[20:23],v[252:255],v[88:91]\n"
"s_waitcnt lgkmcnt(0)\n"
"s_cbranch_vccnz .Lstub_20\n"
".Lback_20:\n"
MF "v[112:115],v[24:27],v[136:139],v[32:35]\n"
"s_add_u32 s74,s74,12288\n"
"ds_read_b128 v[16:19],v0 offset:7168\n"
"ds_read_b128 v[20:23],v1 offset:7168\n"
MF "v[112:115],v[28:31],v[156:159],v[112:115]\n"
"s_cmp_eq_u32 s74,s20\n"
"s_cselect_b32 s74,s70,s74\n"
MF "v[116:119],v[28:31],v[160:163],v[36:39]\n"
"v_exp_f32 v72,v72\n"
MF "v[116:119],v[24:27],v[140:143],v[116:119]\n"
"v_exp_f32 v73,v73\n"
MF "v[120:123],v[24:27],v[144:147],v[40:43]\n"
"v_exp_f32 v74,v74\n"
MF "v[120:123],v[28:31],v[164:167],v[120:123]\n"
"v_exp_f32 v75,v75\n"
MF "v[124:127],v[28:31],v[168:171],v[44:47]\n"
"v_add_f32 v52,v57,v72\n"
"v_add_f32 v52,v52,v73\n"
MF "v[124:127],v[24:27],v[148:151],v[124:127]\n"
CV "v72,v72,v73\n"
"v_add_f32 v52,v52,v74\n"
MF "v[128:131],v[24:27],v[152:155],v[48:51]\n"
"v_add_f32 v52,v52,v75\n"
CV "v73,v74,v75\n"
MF "v[128:131],v[28:31],v[172:175],v[128:131]\n"
"s_waitcnt lgkmcnt(0)\n"
"ds_read_b128 v[24:27],v0 offset:8192\n"
"ds_read_b128 v[28:31],v1 offset:8192\n"
MF "v[128:131],v[16:19],v[192:195],v[128:131]\n"
"v_exp_f32 v76,v76\n"
MF "v[128:131],v[20:23],v[212:215],v[128:131]\n"
"v_exp_f32 v77,v77\n"
MF "v[124:127],v[20:23],v[208:211],v[124:127]\n"
"v_exp_f32 v78,v78\n"
MF "v[124:127],v[16:19],v[188:191],v[124:127]\n"
"v_exp_f32 v79,v79\n"
MF "v[120:123],v[16:19],v[184:187],v[120:123]\n"
"v_add_f32 v53,v58,v76\n"
"v_add_f32 v53,v53,v77\n"
MF "v[120:123],v[20:23],v[204:207],v[120:123]\n"
CV "v76,v76,v77\n"
"v_add_f32 v53,v53,v78\n"
MF "v[116:119],v[20:23],v[200:203],v[116:119]\n"
"s_waitcnt vmcnt(6) lgkmcnt(0)\n"
"v_add_f32 v53,v53,v79\n"
CV "v77,v78,v79\n"
MF "v[116:119],v[16:19],v[180:183],v[116:119]\n"
"s_barrier\n"
MF "v[112:115],v[16:19],v[176:179],v[112:115]\n"
"v_exp_f32 v80,v80\n"
MF "v[112:115],v[20:23],v[196:199],v[112:115]\n"
"s_nop 0\n"
TR "v[16:17],v2 offset:24576\n"
TR "v[18:19],v2 offset:30720\n"
MF "v[112:115],v[24:27],v[216:219],v[112:115]\n"
"s_add_u32 s73,s73,12288\n"
TR "v[20:21],v3 offset:24576\n"
TR "v[22:23],v3 offset:30720\n"
MF "v[112:115],v[28:31],v[236:239],v[112:115]\n"
"s_cmp_eq_u32 s73,s19\n"
"s_cselect_b32 s73,s18,s73\n"
"s_add_u32 m0,s65,0\n"
MF "v[116:119],v[28:31],v[240:243],v[116:119]\n"
LD "v8,s[36:39],s73 offen nt lds\n"
MF "v[116:119],v[24:27],v[220:223],v[116:119]\n"
"s_add_u32 m0,s65,896\n"
"v_exp_f32 v81,v81\n"
MF "v[120:123],v[24:27],v[224:227],v[120:123]\n"
LD "v8,s[36:39],s73 offen offset:128 nt lds\n"
MF "v[120:123],v[28:31],v[244:247],v[120:123]\n"
"v_exp_f32 v82,v82\n"
MF "v[124:127],v[28:31],v[248:251],v[124:127]\n"
"s_add_u32 m0,s65,1792\n"
"v_exp_f32 v83,v83\n"
MF "v[124:127],v[24:27],v[228:231],v[124:127]\n"
LD "v8,s[36:39],s73 offen offset:256 nt lds\n"
MF "v[128:131],v[24:27],v[232:235],v[128:131]\n"
"v_add_f32 v54,v59,v80\n"
"v_add_f32 v54,v54,v81\n"
MF "v[128:131],v[28:31],v[252:255],v[128:131]\n"
"s_waitcnt lgkmcnt(0)\n"
TR "v[24:25],v4 offset:24576\n"
TR "v[26:27],v4 offset:30720\n"
MF "a[0:3],v[16:19],v[92:95],a[0:3]\n"
"s_add_u32 m0,s65,0\n"
TR "v[28:29],v5 offset:24576\n"
TR "v[30:31],v5 offset:30720\n"
MF "a[20:23],v[20:23],v[92:95],a[20:23]\n"
LD "v8,s[36:39],s73 offen offset:3072 nt lds\n"
MF "a[24:27],v[20:23],v[96:99],a[24:27]\n"
CV "v80,v80,v81\n"
"v_add_f32 v54,v54,v82\n"
MF "a[4:7],v[16:19],v[96:99],a[4:7]\n"
"s_add_u32 m0,s65,896\n"
"v_add_f32 v54,v54,v83\n"
CV "v81,v82,v83\n"
MF "a[8:11],v[16:19],v[100:103],a[8:11]\n"
LD "v8,s[36:39],s73 offen offset:3200 nt lds\n"
MF "a[28:31],v[20:23],v[100:103],a[28:31]\n"
"v_exp_f32 v84,v84\n"
MF "a[32:35],v[20:23],v[104:107],a[32:35]\n"
"s_add_u32 m0,s65,1792\n"
"v_exp_f32 v85,v85\n"
MF "a[12:15],v[16:19],v[104:107],a[12:15]\n"
LD "v8,s[36:39],s73 offen offset:3328 nt lds\n"
MF "a[16:19],v[16:19],v[108:111],a[16:19]\n"
"v_exp_f32 v86,v86\n"
MF "a[36:39],v[20:23],v[108:111],a[36:39]\n"
"s_waitcnt lgkmcnt(0)\n"
TR "v[16:17],v2 offset:25600\n"
TR "v[18:19],v2 offset:31744\n"
MF "a[56:59],v[24:27],v[108:111],a[56:59]\n"
"s_nop 0\n"
TR "v[20:21],v3 offset:25600\n"
TR "v[22:23],v3 offset:31744\n"
MF "a[76:79],v[28:31],v[108:111],a[76:79]\n"
"v_exp_f32 v87,v87\n"
MF "a[72:75],v[28:31],v[104:107],a[72:75]\n"
"v_add_f32 v55,v60,v84\n"
"v_add_f32 v55,v55,v85\n"
MF "a[52:55],v[24:27],v[104:107],a[52:55]\n"
CV "v84,v84,v85\n"
"v_add_f32 v55,v55,v86\n"
MF "a[48:51],v[24:27],v[100:103],a[48:51]\n"
"v_add_f32 v55,v55,v87\n"
CV "v85,v86,v87\n"
MF "a[68:71],v[28:31],v[100:103],a[68:71]\n"
"v_exp_f32 v88,v88\n"
MF "a[64:67],v[28:31],v[96:99],a[64:67]\n"
"v_exp_f32 v89,v89\n"
MF "a[44:47],v[24:27],v[96:99],a[44:47]\n"
"v_exp_f32 v90,v90\n"
MF "a[40:43],v[24:27],v[92:95],a[40:43]\n"
"v_exp_f32 v91,v91\n"
MF "a[60:63],v[28:31],v[92:95],a[60:63]\n"
"s_waitcnt lgkmcnt(0)\n"
TR "v[24:25],v4 offset:25600\n"
TR "v[26:27],v4 offset:31744\n"
MF "a[80:83],v[16:19],v[92:95],a[80:83]\n"
"s_nop 0\n"
TR "v[28:29],v5 offset:25600\n"
TR "v[30:31],v5 offset:31744\n"
MF "a[100:103],v[20:23],v[92:95],a[100:103]\n"
"v_add_f32 v56,v61,v88\n"
"v_add_f32 v56,v56,v89\n"
MF "a[104:107],v[20:23],v[96:99],a[104:107]\n"
CV "v88,v88,v89\n"
"v_add_f32 v56,v56,v90\n"
MF "a[84:87],v[16:19],v[96:99],a[84:87]\n"
"v_add_f32 v56,v56,v91\n"
CV "v89,v90,v91\n"
MF "a[88:91],v[16:19],v[100:103],a[88:91]\n"
"v_exp_f32 v112,v112\n"
MF "a[108:111],v[20:23],v[100:103],a[108:111]\n"
"v_exp_f32 v113,v113\n"
MF "a[112:115],v[20:23],v[104:107],a[112:115]\n"
"v_exp_f32 v114,v114\n"
MF "a[92:95],v[16:19],v[104:107],a[92:95]\n"
"v_exp_f32 v115,v115\n"
MF "a[96:99],v[16:19],v[108:111],a[96:99]\n"
"v_add_f32 v52,v52,v112\n"
"v_add_f32 v52,v52,v113\n"
MF "a[116:119],v[20:23],v[108:111],a[116:119]\n"
"s_waitcnt lgkmcnt(0)\n"
TR "v[16:17],v2 offset:26624\n"
TR "v[18:19],v2 offset:32768\n"
MF "a[136:139],v[24:27],v[108:111],a[136:139]\n"
"s_nop 0\n"
TR "v[20:21],v3 offset:26624\n"
TR "v[22:23],v3 offset:32768\n"
MF "a[156:159],v[28:31],v[108:111],a[156:159]\n"
CV "v74,v112,v113\n"
"v_add_f32 v52,v52,v114\n"
MF "a[152:155],v[28:31],v[104:107],a[152:155]\n"
"v_add_f32 v52,v52,v115\n"
CV "v75,v114,v115\n"
MF "a[132:135],v[24:27],v[104:107],a[132:135]\n"
"v_exp_f32 v116,v116\n"
MF "a[128:131],v[24:27],v[100:103],a[128:131]\n"
"v_exp_f32 v117,v117\n"
MF "a[148:151],v[28:31],v[100:103],a[148:151]\n"
"v_exp_f32 v118,v118\n"
MF "a[144:147],v[28:31],v[96:99],a[144:147]\n"
"v_exp_f32 v119,v119\n"
MF "a[124:127],v[24:27],v[96:99],a[124:127]\n"
"v_add_f32 v53,v53,v116\n"
"v_add_f32 v53,v53,v117\n"
MF "a[120:123],v[24:27],v[92:95],a[120:123]\n"
CV "v78,v116,v117\n"
"v_add_f32 v53,v53,v118\n"
MF "a[140:143],v[28:31],v[92:95],a[140:143]\n"
"s_waitcnt lgkmcnt(0)\n"
TR "v[24:25],v4 offset:26624\n"
TR "v[26:27],v4 offset:32768\n"
MF "a[160:163],v[16:19],v[92:95],a[160:163]\n"
"s_nop 0\n"
TR "v[28:29],v5 offset:26624\n"
TR "v[30:31],v5 offset:32768\n"
MF "a[180:183],v[20:23],v[92:95],a[180:183]\n"
"v_add_f32 v53,v53,v119\n"
CV "v79,v118,v119\n"
MF "a[184:187],v[20:23],v[96:99],a[184:187]\n"
"v_exp_f32 v120,v120\n"
MF "a[164:167],v[16:19],v[96:99],a[164:167]\n"
"v_exp_f32 v121,v121\n"
MF "a[168:171],v[16:19],v[100:103],a[168:171]\n"
"v_exp_f32 v122,v122\n"
MF "a[188:191],v[20:23],v[100:103],a[188:191]\n"
"v_exp_f32 v123,v123\n"
MF "a[192:195],v[20:23],v[104:107],a[192:195]\n"
"v_add_f32 v54,v54,v120\n"
"v_add_f32 v54,v54,v121\n"
MF "a[172:175],v[16:19],v[104:107],a[172:175]\n"
"s_waitcnt vmcnt(6) lgkmcnt(0)\n"
CV "v82,v120,v121\n"
"v_add_f32 v54,v54,v122\n"
MF "a[176:179],v[16:19],v[108:111],a[176:179]\n"
"s_barrier\n"
MF "a[196:199],v[20:23],v[108:111],a[196:199]\n"
"s_add_u32 s72,s72,6144\n"
"ds_read_b128 v[16:19],v0 offset:49152\n"
"ds_read_b128 v[20:23],v1 offset:49152\n"
MF "a[216:219],v[24:27],v[108:111],a[216:219]\n"
"s_cmp_eq_u32 s72,s17\n"
"s_cselect_b32 s72,s16,s72\n"
"s_add_u32 m0,s64,24576\n"
MF "a[236:239],v[28:31],v[108:111],a[236:239]\n"
LD "v7,s[32:35],s72 offen nt lds\n"
MF "a[232:235],v[28:31],v[104:107],a[232:235]\n"
"s_add_u32 m0,s64,25472\n"
"v_add_f32 v54,v54,v123\n"
CV "v83,v122,v123\n"
MF "a[212:215],v[24:27],v[104:107],a[212:215]\n"
LD "v7,s[32:35],s72 offen offset:128 nt lds\n"
MF "a[208:211],v[24:27],v[100:103],a[208:211]\n"
"s_add_u32 m0,s64,26368\n"
MF "a[228:231],v[28:31],v[100:103],a[228:231]\n"
LD "v7,s[32:35],s72 offen offset:256 nt lds\n"
MF "a[224:227],v[28:31],v[96:99],a[224:227]\n"
MF "a[204:207],v[24:27],v[96:99],a[204:207]\n"
MF "a[200:203],v[24:27],v[92:95],a[200:203]\n"
MF "a[220:223],v[28:31],v[92:95],a[220:223]\n"
"s_waitcnt lgkmcnt(0)\n"
MF "v[92:95],v[16:19],v[136:139],v[32:35]\n"
"s_add_u32 s72,s72,6144\n"
"ds_read_b128 v[24:27],v0 offset:50176\n"
"ds_read_b128 v[28:31],v1 offset:50176\n"
MF "v[92:95],v[20:23],v[156:159],v[92:95]\n"
"s_add_u32 m0,s64,30720\n"
"v_exp_f32 v124,v124\n"
MF "v[96:99],v[20:23],v[160:163],v[36:39]\n"
LD "v7,s[32:35],s72 offen nt lds\n"
MF "v[96:99],v[16:19],v[140:143],v[96:99]\n"
"v_exp_f32 v125,v125\n"
MF "v[100:103],v[16:19],v[144:147],v[40:43]\n"
"s_add_u32 m0,s64,31616\n"
"v_exp_f32 v126,v126\n"
MF "v[100:103],v[20:23],v[164:167],v[100:103]\n"
LD "v7,s[32:35],s72 offen offset:128 nt lds\n"
MF "v[104:107],v[20:23],v[168:171],v[44:47]\n"
"v_exp_f32 v127,v127\n"
MF "v[104:107],v[16:19],v[148:151],v[104:107]\n"
"s_add_u32 m0,s64,32512\n"
"v_add_f32 v55,v55,v124\n"
"v_add_f32 v55,v55,v125\n"
MF "v[108:111],v[16:19],v[152:155],v[48:51]\n"
LD "v7,s[32:35],s72 offen offset:256 nt lds\n"
MF "v[108:111],v[20:23],v[172:175],v[108:111]\n"
"s_waitcnt lgkmcnt(0)\n"
"ds_read_b128 v[16:19],v0 offset:51200\n"
"ds_read_b128 v[20:23],v1 offset:51200\n"
MF "v[108:111],v[24:27],v[192:195],v[108:111]\n"
CV "v86,v124,v125\n"
"v_add_f32 v55,v55,v126\n"
MF "v[108:111],v[28:31],v[212:215],v[108:111]\n"
"v_add_f32 v55,v55,v127\n"
CV "v87,v126,v127\n"
MF "v[104:107],v[28:31],v[208:211],v[104:107]\n"
"v_exp_f32 v128,v128\n"
MF "v[104:107],v[24:27],v[188:191],v[104:107]\n"
"v_exp_f32 v129,v129\n"
MF "v[100:103],v[24:27],v[184:187],v[100:103]\n"
"v_exp_f32 v130,v130\n"
MF "v[100:103],v[28:31],v[204:207],v[100:103]\n"
"v_exp_f32 v131,v131\n"
MF "v[96:99],v[28:31],v[200:203],v[96:99]\n"
"v_add_f32 v56,v56,v128\n"
"v_add_f32 v56,v56,v129\n"
MF "v[96:99],v[24:27],v[180:183],v[96:99]\n"
CV "v90,v128,v129\n"
"v_add_f32 v56,v56,v130\n"
MF "v[92:95],v[24:27],v[176:179],v[92:95]\n"
"v_add_f32 v56,v56,v131\n"
CV "v91,v130,v131\n"
MF "v[92:95],v[28:31],v[196:199],v[92:95]\n"
"s_waitcnt lgkmcnt(0)\n"
"ds_read_b128 v[24:27],v0 offset:55296\n"
"ds_read_b128 v[28:31],v1 offset:55296\n"
MF "v[92:95],v[16:19],v[216:219],v[92:95]\n"
MF "v[92:95],v[20:23],v[236:239],v[92:95]\n"
MF "v[96:99],v[20:23],v[240:243],v[96:99]\n"
MF "v[96:99],v[16:19],v[220:223],v[96:99]\n"
MF "v[100:103],v[16:19],v[224:227],v[100:103]\n"
MF "v[100:103],v[20:23],v[244:247],v[100:103]\n"
MF "v[104:107],v[20:23],v[248:251],v[104:107]\n"
MF "v[104:107],v[16:19],v[228:231],v[104:107]\n"
"v_max3_f32 v10,v52,v53,v54\n"
"v_max3_f32 v10,v10,v55,v56\n"
MF "v[108:111],v[16:19],v[232:235],v[108:111]\n"
"v_cmp_ngt_f32 vcc,s77,v10\n"
MF "v[108:111],v[20:23],v[252:255],v[108:111]\n"
"s_waitcnt lgkmcnt(0)\n"
"s_cbranch_vccnz .Lstub_21\n"
".Lback_21:\n"
MF "v[112:115],v[24:27],v[136:139],v[32:35]\n"
"s_add_u32 s74,s74,12288\n"
"ds_read_b128 v[16:19],v0 offset:56320\n"
"ds_read_b128 v[20:23],v1 offset:56320\n"
MF "v[112:115],v[28:31],v[156:159],v[112:115]\n"
"s_cmp_eq_u32 s74,s20\n"
"s_cselect_b32 s74,s70,s74\n"
MF "v[116:119],v[28:31],v[160:163],v[36:39]\n"
"v_exp_f32 v92,v92\n"
MF "v[116:119],v[24:27],v[140:143],v[116:119]\n"
"v_exp_f32 v93,v93\n"
MF "v[120:123],v[24:27],v[144:147],v[40:43]\n"
"v_exp_f32 v94,v94\n"
MF "v[120:123],v[28:31],v[164:167],v[120:123]\n"
"v_exp_f32 v95,v95\n"
MF "v[124:127],v[28:31],v[168:171],v[44:47]\n"
"v_add_f32 v57,v52,v92\n"
"v_add_f32 v57,v57,v93\n"
MF "v[124:127],v[24:27],v[148:151],v[124:127]\n"
CV "v92,v92,v93\n"
"v_add_f32 v57,v57,v94\n"
MF "v[128:131],v[24:27],v[152:155],v[48:51]\n"
"v_add_f32 v57,v57,v95\n"
CV "v93,v94,v95\n"
MF "v[128:131],v[28:31],v[172:175],v[128:131]\n"
"s_waitcnt lgkmcnt(0)\n"
"ds_read_b128 v[24:27],v0 offset:57344\n"
"ds_read_b128 v[28:31],v1 offset:57344\n"
MF "v[128:131],v[16:19],v[192:195],v[128:131]\n"
"v_exp_f32 v96,v96\n"
MF "v[128:131],v[20:23],v[212:215],v[128:131]\n"
"v_exp_f32 v97,v97\n"
MF "v[124:127],v[20:23],v[208:211],v[124:127]\n"
"v_exp_f32 v98,v98\n"
MF "v[124:127],v[16:19],v[188:191],v[124:127]\n"
"v_exp_f32 v99,v99\n"
MF "v[120:123],v[16:19],v[184:187],v[120:123]\n"
"v_add_f32 v58,v53,v96\n"
"v_add_f32 v58,v58,v97\n"
MF "v[120:123],v[20:23],v[204:207],v[120:123]\n"
CV "v96,v96,v97\n"
"v_add_f32 v58,v58,v98\n"
MF "v[116:119],v[20:23],v[200:203],v[116:119]\n"
"s_waitcnt vmcnt(6) lgkmcnt(0)\n"
"v_add_f32 v58,v58,v99\n"
CV "v97,v98,v99\n"
MF "v[116:119],v[16:19],v[180:183],v[116:119]\n"
"s_barrier\n"
MF "v[112:115],v[16:19],v[176:179],v[112:115]\n"
"v_exp_f32 v100,v100\n"
MF "v[112:115],v[20:23],v[196:199],v[112:115]\n"
"s_nop 0\n"
TR "v[16:17],v2\n"
TR "v[18:19],v2 offset:6144\n"
MF "v[112:115],v[24:27],v[216:219],v[112:115]\n"
"s_add_u32 s73,s73,12288\n"
TR "v[20:21],v3\n"
TR "v[22:23],v3 offset:6144\n"
MF "v[112:115],v[28:31],v[236:239],v[112:115]\n"
"s_cmp_eq_u32 s73,s19\n"
"s_cselect_b32 s73,s18,s73\n"
"s_add_u32 m0,s65,49152\n"
MF "v[116:119],v[28:31],v[240:243],v[116:119]\n"
LD "v8,s[36:39],s73 offen nt lds\n"
MF "v[116:119],v[24:27],v[220:223],v[116:119]\n"
"s_add_u32 m0,s65,50048\n"
"v_exp_f32 v101,v101\n"
MF "v[120:123],v[24:27],v[224:227],v[120:123]\n"
LD "v8,s[36:39],s73 offen offset:128 nt lds\n"
MF "v[120:123],v[28:31],v[244:247],v[120:123]\n"
"v_exp_f32 v102,v102\n"
MF "v[124:127],v[28:31],v[248:251],v[124:127]\n"
"s_add_u32 m0,s65,50944\n"
"v_exp_f32 v103,v103\n"
MF "v[124:127],v[24:27],v[228:231],v[124:127]\n"
LD "v8,s[36:39],s73 offen offset:256 nt lds\n"
MF "v[128:131],v[24:27],v[232:235],v[128:131]\n"
"v_add_f32 v59,v54,v100\n"
"v_add_f32 v59,v59,v101\n"
MF "v[128:131],v[28:31],v[252:255],v[128:131]\n"
"s_waitcnt lgkmcnt(0)\n"
TR "v[24:25],v4\n"
TR "v[26:27],v4 offset:6144\n"
MF "a[0:3],v[16:19],v[72:75],a[0:3]\n"
"s_add_u32 m0,s65,49152\n"
TR "v[28:29],v5\n"
TR "v[30:31],v5 offset:6144\n"
MF "a[20:23],v[20:23],v[72:75],a[20:23]\n"
LD "v8,s[36:39],s73 offen offset:3072 nt lds\n"
MF "a[24:27],v[20:23],v[76:79],a[24:27]\n"
CV "v100,v100,v101\n"
"v_add_f32 v59,v59,v102\n"
MF "a[4:7],v[16:19],v[76:79],a[4:7]\n"
"s_add_u32 m0,s65,50048\n"
"v_add_f32 v59,v59,v103\n"
CV "v101,v102,v103\n"
MF "a[8:11],v[16:19],v[80:83],a[8:11]\n"
LD "v8,s[36:39],s73 offen offset:3200 nt lds\n"
MF "a[28:31],v[20:23],v[80:83],a[28:31]\n"
"v_exp_f32 v104,v104\n"
MF "a[32:35],v[20:23],v[84:87],a[32:35]\n"
"s_add_u32 m0,s65,50944\n"
"v_exp_f32 v105,v105\n"
MF "a[12:15],v[16:19],v[84:87],a[12:15]\n"
LD "v8,s[36:39],s73 offen offset:3328 nt lds\n"
MF "a[16:19],v[16:19],v[88:91],a[16:19]\n"
"v_exp_f32 v106,v106\n"
MF "a[36:39],v[20:23],v[88:91],a[36:39]\n"
"s_waitcnt lgkmcnt(0)\n"
TR "v[16:17],v2 offset:1024\n"
TR "v[18:19],v2 offset:7168\n"
MF "a[56:59],v[24:27],v[88:91],a[56:59]\n"
"s_nop 0\n"
TR "v[20:21],v3 offset:1024\n"
TR "v[22:23],v3 offset:7168\n"
MF "a[76:79],v[28:31],v[88:91],a[76:79]\n"
"v_exp_f32 v107,v107\n"
MF "a[72:75],v[28:31],v[84:87],a[72:75]\n"
"v_add_f32 v60,v55,v104\n"
"v_add_f32 v60,v60,v105\n"
MF "a[52:55],v[24:27],v[84:87],a[52:55]\n"
CV "v104,v104,v105\n"
"v_add_f32 v60,v60,v106\n"
MF "a[48:51],v[24:27],v[80:83],a[48:51]\n"
"v_add_f32 v60,v60,v107\n"
CV "v105,v106,v107\n"
MF "a[68:71],v[28:31],v[80:83],a[68:71]\n"
"v_exp_f32 v108,v108\n"
MF "a[64:67],v[28:31],v[76:79],a[64:67]\n"
"v_exp_f32 v109,v109\n"
MF "a[44:47],v[24:27],v[76:79],a[44:47]\n"
"v_exp_f32 v110,v110\n"
MF "a[40:43],v[24:27],v[72:75],a[40:43]\n"
"v_exp_f32 v111,v111\n"
MF "a[60:63],v[28:31],v[72:75],a[60:63]\n"
"s_waitcnt lgkmcnt(0)\n"
TR "v[24:25],v4 offset:1024\n"
TR "v[26:27],v4 offset:7168\n"
MF "a[80:83],v[16:19],v[72:75],a[80:83]\n"
"s_nop 0\n"
TR "v[28:29],v5 offset:1024\n"
TR "v[30:31],v5 offset:7168\n"
MF "a[100:103],v[20:23],v[72:75],a[100:103]\n"
"v_add_f32 v61,v56,v108\n"
"v_add_f32 v61,v61,v109\n"
MF "a[104:107],v[20:23],v[76:79],a[104:107]\n"
CV "v108,v108,v109\n"
"v_add_f32 v61,v61,v110\n"
MF "a[84:87],v[16:19],v[76:79],a[84:87]\n"
"v_add_f32 v61,v61,v111\n"
CV "v109,v110,v111\n"
MF "a[88:91],v[16:19],v[80:83],a[88:91]\n"
"v_exp_f32 v112,v112\n"
MF "a[108:111],v[20:23],v[80:83],a[108:111]\n"
"v_exp_f32 v113,v113\n"
MF "a[112:115],v[20:23],v[84:87],a[112:115]\n"
"v_exp_f32 v114,v114\n"
MF "a[92:95],v[16:19],v[84:87],a[92:95]\n"
"v_exp_f32 v115,v115\n"
MF "a[96:99],v[16:19],v[88:91],a[96:99]\n"
"v_add_f32 v57,v57,v112\n"
"v_add_f32 v57,v57,v113\n"
MF "a[116:119],v[20:23],v[88:91],a[116:119]\n"
"s_waitcnt lgkmcnt(0)\n"
TR "v[16:17],v2 offset:2048\n"
TR "v[18:19],v2 offset:8192\n"
MF "a[136:139],v[24:27],v[88:91],a[136:139]\n"
"s_nop 0\n"
TR "v[20:21],v3 offset:2048\n"
TR "v[22:23],v3 offset:8192\n"
MF "a[156:159],v[28:31],v[88:91],a[156:159]\n"
CV "v94,v112,v113\n"
"v_add_f32 v57,v57,v114\n"
MF "a[152:155],v[28:31],v[84:87],a[152:155]\n"
"v_add_f32 v57,v57,v115\n"
CV "v95,v114,v115\n"
MF "a[132:135],v[24:27],v[84:87],a[132:135]\n"
"v_exp_f32 v116,v116\n"
MF "a[128:131],v[24:27],v[80:83],a[128:131]\n"
"v_exp_f32 v117,v117\n"
MF "a[148:151],v[28:31],v[80:83],a[148:151]\n"
"v_exp_f32 v118,v118\n"
MF "a[144:147],v[28:31],v[76:79],a[144:147]\n"
"v_exp_f32 v119,v119\n"
MF "a[124:127],v[24:27],v[76:79],a[124:127]\n"
"v_add_f32 v58,v58,v116\n"
"v_add_f32 v58,v58,v117\n"
MF "a[120:123],v[24:27],v[72:75],a[120:123]\n"
CV "v98,v116,v117\n"
"v_add_f32 v58,v58,v118\n"
MF "a[140:143],v[28:31],v[72:75],a[140:143]\n"
"s_waitcnt lgkmcnt(0)\n"
TR "v[24:25],v4 offset:2048\n"
TR "v[26:27],v4 offset:8192\n"
MF "a[160:163],v[16:19],v[72:75],a[160:163]\n"
"s_nop 0\n"
TR "v[28:29],v5 offset:2048\n"
TR "v[30:31],v5 offset:8192\n"
MF "a[180:183],v[20:23],v[72:75],a[180:183]\n"
"v_add_f32 v58,v58,v119\n"
CV "v99,v118,v119\n"
MF "a[184:187],v[20:23],v[76:79],a[184:187]\n"
"v_exp_f32 v120,v120\n"
MF "a[164:167],v[16:19],v[76:79],a[164:167]\n"
"v_exp_f32 v121,v121\n"
MF "a[168:171],v[16:19],v[80:83],a[168:171]\n"
"v_exp_f32 v122,v122\n"
MF "a[188:191],v[20:23],v[80:83],a[188:191]\n"
"v_exp_f32 v123,v123\n"
MF "a[192:195],v[20:23],v[84:87],a[192:195]\n"
"v_add_f32 v59,v59,v120\n"
"v_add_f32 v59,v59,v121\n"
MF "a[172:175],v[16:19],v[84:87],a[172:175]\n"
"s_waitcnt vmcnt(6) lgkmcnt(0)\n"
CV "v102,v120,v121\n"
"v_add_f32 v59,v59,v122\n"
MF "a[176:179],v[16:19],v[88:91],a[176:179]\n"
"s_barrier\n"
MF "a[196:199],v[20:23],v[88:91],a[196:199]\n"
"s_add_u32 s72,s72,6144\n"
"ds_read_b128 v[16:19],v0 offset:24576\n"
"ds_read_b128 v[20:23],v1 offset:24576\n"
MF "a[216:219],v[24:27],v[88:91],a[216:219]\n"
"s_cmp_eq_u32 s72,s17\n"
"s_cselect_b32 s72,s16,s72\n"
"s_add_u32 m0,s64,0\n"
MF "a[236:239],v[28:31],v[88:91],a[236:239]\n"
LD "v7,s[32:35],s72 offen nt lds\n"
MF "a[232:235],v[28:31],v[84:87],a[232:235]\n"
"s_add_u32 m0,s64,896\n"
"v_add_f32 v59,v59,v123\n"
CV "v103,v122,v123\n"
MF "a[212:215],v[24:27],v[84:87],a[212:215]\n"
LD "v7,s[32:35],s72 offen offset:128 nt lds\n"
MF "a[208:211],v[24:27],v[80:83],a[208:211]\n"
"s_add_u32 m0,s64,1792\n"
MF "a[228:231],v[28:31],v[80:83],a[228:231]\n"
LD "v7,s[32:35],s72 offen offset:256 nt lds\n"
MF "a[224:227],v[28:31],v[76:79],a[224:227]\n"
MF "a[204:207],v[24:27],v[76:79],a[204:207]\n"
MF "a[200:203],v[24:27],v[72:75],a[200:203]\n"
MF "a[220:223],v[28:31],v[72:75],a[220:223]\n"
"s_waitcnt lgkmcnt(0)\n"
MF "v[72:75],v[16:19],v[136:139],v[32:35]\n"
"s_add_u32 s72,s72,6144\n"
"ds_read_b128 v[24:27],v0 offset:25600\n"
"ds_read_b128 v[28:31],v1 offset:25600\n"
MF "v[72:75],v[20:23],v[156:159],v[72:75]\n"
"s_add_u32 m0,s64,6144\n"
"v_exp_f32 v124,v124\n"
MF "v[76:79],v[20:23],v[160:163],v[36:39]\n"
LD "v7,s[32:35],s72 offen nt lds\n"
MF "v[76:79],v[16:19],v[140:143],v[76:79]\n"
"v_exp_f32 v125,v125\n"
MF "v[80:83],v[16:19],v[144:147],v[40:43]\n"
"s_add_u32 m0,s64,7040\n"
"v_exp_f32 v126,v126\n"
MF "v[80:83],v[20:23],v[164:167],v[80:83]\n"
LD "v7,s[32:35],s72 offen offset:128 nt lds\n"
MF "v[84:87],v[20:23],v[168:171],v[44:47]\n"
"v_exp_f32 v127,v127\n"
MF "v[84:87],v[16:19],v[148:151],v[84:87]\n"
"s_add_u32 m0,s64,7936\n"
"v_add_f32 v60,v60,v124\n"
"v_add_f32 v60,v60,v125\n"
MF "v[88:91],v[16:19],v[152:155],v[48:51]\n"
LD "v7,s[32:35],s72 offen offset:256 nt lds\n"
MF "v[88:91],v[20:23],v[172:175],v[88:91]\n"
"s_waitcnt lgkmcnt(0)\n"
"ds_read_b128 v[16:19],v0 offset:26624\n"
"ds_read_b128 v[20:23],v1 offset:26624\n"
MF "v[88:91],v[24:27],v[192:195],v[88:91]\n"
CV "v106,v124,v125\n"
"v_add_f32 v60,v60,v126\n"
MF "v[88:91],v[28:31],v[212:215],v[88:91]\n"
"v_add_f32 v60,v60,v127\n"
CV "v107,v126,v127\n"
MF "v[84:87],v[28:31],v[208:211],v[84:87]\n"
"v_exp_f32 v128,v128\n"
MF "v[84:87],v[24:27],v[188:191],v[84:87]\n"
"v_exp_f32 v129,v129\n"
MF "v[80:83],v[24:27],v[184:187],v[80:83]\n"
"v_exp_f32 v130,v130\n"
MF "v[80:83],v[28:31],v[204:207],v[80:83]\n"
"v_exp_f32 v131,v131\n"
MF "v[76:79],v[28:31],v[200:203],v[76:79]\n"
"v_add_f32 v61,v61,v128\n"
"v_add_f32 v61,v61,v129\n"
MF "v[76:79],v[24:27],v[180:183],v[76:79]\n"
CV "v110,v128,v129\n"
"v_add_f32 v61,v61,v130\n"
MF "v[72:75],v[24:27],v[176:179],v[72:75]\n"
"v_add_f32 v61,v61,v131\n"
CV "v111,v130,v131\n"
MF "v[72:75],v[28:31],v[196:199],v[72:75]\n"
"s_waitcnt lgkmcnt(0)\n"
"ds_read_b128 v[24:27],v0 offset:30720\n"
"ds_read_b128 v[28:31],v1 offset:30720\n"
MF "v[72:75],v[16:19],v[216:219],v[72:75]\n"
MF "v[72:75],v[20:23],v[236:239],v[72:75]\n"
MF "v[76:79],v[20:23],v[240:243],v[76:79]\n"
MF "v[76:79],v[16:19],v[220:223],v[76:79]\n"
MF "v[80:83],v[16:19],v[224:227],v[80:83]\n"
MF "v[80:83],v[20:23],v[244:247],v[80:83]\n"
MF "v[84:87],v[20:23],v[248:251],v[84:87]\n"
MF "v[84:87],v[16:19],v[228:231],v[84:87]\n"
"v_max3_f32 v10,v57,v58,v59\n"
"v_max3_f32 v10,v10,v60,v61\n"
MF "v[88:91],v[16:19],v[232:235],v[88:91]\n"
"v_cmp_ngt_f32 vcc,s77,v10\n"
MF "v[88:91],v[20:23],v[252:255],v[88:91]\n"
"s_waitcnt lgkmcnt(0)\n"
"s_cbranch_vccnz .Lstub_22\n"
".Lback_22:\n"
MF "v[112:115],v[24:27],v[136:139],v[32:35]\n"
"s_add_u32 s74,s74,12288\n"
"ds_read_b128 v[16:19],v0 offset:31744\n"
"ds_read_b128 v[20:23],v1 offset:31744\n"
MF "v[112:115],v[28:31],v[156:159],v[112:115]\n"
"s_cmp_eq_u32 s74,s20\n"
"s_cselect_b32 s74,s70,s74\n"
MF "v[116:119],v[28:31],v[160:163],v[36:39]\n"
"v_exp_f32 v72,v72\n"
MF "v[116:119],v[24:27],v[140:143],v[116:119]\n"
"v_exp_f32 v73,v73\n"
MF "v[120:123],v[24:27],v[144:147],v[40:43]\n"
"v_exp_f32 v74,v74\n"
MF "v[120:123],v[28:31],v[164:167],v[120:123]\n"
"v_exp_f32 v75,v75\n"
MF "v[124:127],v[28:31],v[168:171],v[44:47]\n"
"v_add_f32 v52,v57,v72\n"
"v_add_f32 v52,v52,v73\n"
MF "v[124:127],v[24:27],v[148:151],v[124:127]\n"
CV "v72,v72,v73\n"
"v_add_f32 v52,v52,v74\n"
MF "v[128:131],v[24:27],v[152:155],v[48:51]\n"
"v_add_f32 v52,v52,v75\n"
CV "v73,v74,v75\n"
MF "v[128:131],v[28:31],v[172:175],v[128:131]\n"
"s_waitcnt lgkmcnt(0)\n"
"ds_read_b128 v[24:27],v0 offset:32768\n"
"ds_read_b128 v[28:31],v1 offset:32768\n"
MF "v[128:131],v[16:19],v[192:195],v[128:131]\n"
"v_exp_f32 v76,v76\n"
MF "v[128:131],v[20:23],v[212:215],v[128:131]\n"
"v_exp_f32 v77,v77\n"
MF "v[124:127],v[20:23],v[208:211],v[124:127]\n"
"v_exp_f32 v78,v78\n"
MF "v[124:127],v[16:19],v[188:191],v[124:127]\n"
"v_exp_f32 v79,v79\n"
MF "v[120:123],v[16:19],v[184:187],v[120:123]\n"
"v_add_f32 v53,v58,v76\n"
"v_add_f32 v53,v53,v77\n"
MF "v[120:123],v[20:23],v[204:207],v[120:123]\n"
CV "v76,v76,v77\n"
"v_add_f32 v53,v53,v78\n"
MF "v[116:119],v[20:23],v[200:203],v[116:119]\n"
"s_waitcnt vmcnt(6) lgkmcnt(0)\n"
"v_add_f32 v53,v53,v79\n"
CV "v77,v78,v79\n"
MF "v[116:119],v[16:19],v[180:183],v[116:119]\n"
"s_barrier\n"
MF "v[112:115],v[16:19],v[176:179],v[112:115]\n"
"v_exp_f32 v80,v80\n"
MF "v[112:115],v[20:23],v[196:199],v[112:115]\n"
"s_nop 0\n"
TR "v[16:17],v2 offset:49152\n"
TR "v[18:19],v2 offset:55296\n"
MF "v[112:115],v[24:27],v[216:219],v[112:115]\n"
"s_add_u32 s73,s73,12288\n"
TR "v[20:21],v3 offset:49152\n"
TR "v[22:23],v3 offset:55296\n"
MF "v[112:115],v[28:31],v[236:239],v[112:115]\n"
"s_cmp_eq_u32 s73,s19\n"
"s_cselect_b32 s73,s18,s73\n"
"s_add_u32 m0,s65,24576\n"
MF "v[116:119],v[28:31],v[240:243],v[116:119]\n"
LD "v8,s[36:39],s73 offen nt lds\n"
MF "v[116:119],v[24:27],v[220:223],v[116:119]\n"
"s_add_u32 m0,s65,25472\n"
"v_exp_f32 v81,v81\n"
MF "v[120:123],v[24:27],v[224:227],v[120:123]\n"
LD "v8,s[36:39],s73 offen offset:128 nt lds\n"
MF "v[120:123],v[28:31],v[244:247],v[120:123]\n"
"v_exp_f32 v82,v82\n"
MF "v[124:127],v[28:31],v[248:251],v[124:127]\n"
"s_add_u32 m0,s65,26368\n"
"v_exp_f32 v83,v83\n"
MF "v[124:127],v[24:27],v[228:231],v[124:127]\n"
LD "v8,s[36:39],s73 offen offset:256 nt lds\n"
MF "v[128:131],v[24:27],v[232:235],v[128:131]\n"
"v_add_f32 v54,v59,v80\n"
"v_add_f32 v54,v54,v81\n"
MF "v[128:131],v[28:31],v[252:255],v[128:131]\n"
"s_waitcnt lgkmcnt(0)\n"
TR "v[24:25],v4 offset:49152\n"
TR "v[26:27],v4 offset:55296\n"
MF "a[0:3],v[16:19],v[92:95],a[0:3]\n"
"s_add_u32 m0,s65,24576\n"
TR "v[28:29],v5 offset:49152\n"
TR "v[30:31],v5 offset:55296\n"
MF "a[20:23],v[20:23],v[92:95],a[20:23]\n"
LD "v8,s[36:39],s73 offen offset:3072 nt lds\n"
MF "a[24:27],v[20:23],v[96:99],a[24:27]\n"
CV "v80,v80,v81\n"
"v_add_f32 v54,v54,v82\n"
MF "a[4:7],v[16:19],v[96:99],a[4:7]\n"
"s_add_u32 m0,s65,25472\n"
"v_add_f32 v54,v54,v83\n"
CV "v81,v82,v83\n"
MF "a[8:11],v[16:19],v[100:103],a[8:11]\n"
LD "v8,s[36:39],s73 offen offset:3200 nt lds\n"
MF "a[28:31],v[20:23],v[100:103],a[28:31]\n"
"v_exp_f32 v84,v84\n"
MF "a[32:35],v[20:23],v[104:107],a[32:35]\n"
"s_add_u32 m0,s65,26368\n"
"v_exp_f32 v85,v85\n"
MF "a[12:15],v[16:19],v[104:107],a[12:15]\n"
LD "v8,s[36:39],s73 offen offset:3328 nt lds\n"
MF "a[16:19],v[16:19],v[108:111],a[16:19]\n"
"v_exp_f32 v86,v86\n"
MF "a[36:39],v[20:23],v[108:111],a[36:39]\n"
"s_waitcnt lgkmcnt(0)\n"
TR "v[16:17],v2 offset:50176\n"
TR "v[18:19],v2 offset:56320\n"
MF "a[56:59],v[24:27],v[108:111],a[56:59]\n"
"s_nop 0\n"
TR "v[20:21],v3 offset:50176\n"
TR "v[22:23],v3 offset:56320\n"
MF "a[76:79],v[28:31],v[108:111],a[76:79]\n"
"v_exp_f32 v87,v87\n"
MF "a[72:75],v[28:31],v[104:107],a[72:75]\n"
"v_add_f32 v55,v60,v84\n"
"v_add_f32 v55,v55,v85\n"
MF "a[52:55],v[24:27],v[104:107],a[52:55]\n"
CV "v84,v84,v85\n"
"v_add_f32 v55,v55,v86\n"
MF "a[48:51],v[24:27],v[100:103],a[48:51]\n"
"v_add_f32 v55,v55,v87\n"
CV "v85,v86,v87\n"
MF "a[68:71],v[28:31],v[100:103],a[68:71]\n"
"v_exp_f32 v88,v88\n"
MF "a[64:67],v[28:31],v[96:99],a[64:67]\n"
"v_exp_f32 v89,v89\n"
MF "a[44:47],v[24:27],v[96:99],a[44:47]\n"
"v_exp_f32 v90,v90\n"
MF "a[40:43],v[24:27],v[92:95],a[40:43]\n"
"v_exp_f32 v91,v91\n"
MF "a[60:63],v[28:31],v[92:95],a[60:63]\n"
"s_waitcnt lgkmcnt(0)\n"
TR "v[24:25],v4 offset:50176\n"
TR "v[26:27],v4 offset:56320\n"
MF "a[80:83],v[16:19],v[92:95],a[80:83]\n"
"s_nop 0\n"
TR "v[28:29],v5 offset:50176\n"
TR "v[30:31],v5 offset:56320\n"
MF "a[100:103],v[20:23],v[92:95],a[100:103]\n"
"v_add_f32 v56,v61,v88\n"
"v_add_f32 v56,v56,v89\n"
MF "a[104:107],v[20:23],v[96:99],a[104:107]\n"
CV "v88,v88,v89\n"
"v_add_f32 v56,v56,v90\n"
MF "a[84:87],v[16:19],v[96:99],a[84:87]\n"
"v_add_f32 v56,v56,v91\n"
CV "v89,v90,v91\n"
MF "a[88:91],v[16:19],v[100:103],a[88:91]\n"
"v_exp_f32 v112,v112\n"
MF "a[108:111],v[20:23],v[100:103],a[108:111]\n"
"v_exp_f32 v113,v113\n"
MF "a[112:115],v[20:23],v[104:107],a[112:115]\n"
"v_exp_f32 v114,v114\n"
MF "a[92:95],v[16:19],v[104:107],a[92:95]\n"
"v_exp_f32 v115,v115\n"
MF "a[96:99],v[16:19],v[108:111],a[96:99]\n"
"v_add_f32 v52,v52,v112\n"
"v_add_f32 v52,v52,v113\n"
MF "a[116:119],v[20:23],v[108:111],a[116:119]\n"
"s_waitcnt lgkmcnt(0)\n"
TR "v[16:17],v2 offset:51200\n"
TR "v[18:19],v2 offset:57344\n"
MF "a[136:139],v[24:27],v[108:111],a[136:139]\n"
"s_nop 0\n"
TR "v[20:21],v3 offset:51200\n"
TR "v[22:23],v3 offset:57344\n"
MF "a[156:159],v[28:31],v[108:111],a[156:159]\n"
CV "v74,v112,v113\n"
"v_add_f32 v52,v52,v114\n"
MF "a[152:155],v[28:31],v[104:107],a[152:155]\n"
"v_add_f32 v52,v52,v115\n"
CV "v75,v114,v115\n"
MF "a[132:135],v[24:27],v[104:107],a[132:135]\n"
"v_exp_f32 v116,v116\n"
MF "a[128:131],v[24:27],v[100:103],a[128:131]\n"
"v_exp_f32 v117,v117\n"
MF "a[148:151],v[28:31],v[100:103],a[148:151]\n"
"v_exp_f32 v118,v118\n"
MF "a[144:147],v[28:31],v[96:99],a[144:147]\n"
"v_exp_f32 v119,v119\n"
MF "a[124:127],v[24:27],v[96:99],a[124:127]\n"
"v_add_f32 v53,v53,v116\n"
"v_add_f32 v53,v53,v117\n"
MF "a[120:123],v[24:27],v[92:95],a[120:123]\n"
CV "v78,v116,v117\n"
"v_add_f32 v53,v53,v118\n"
MF "a[140:143],v[28:31],v[92:95],a[140:143]\n"
"s_waitcnt lgkmcnt(0)\n"
TR "v[24:25],v4 offset:51200\n"
TR "v[26:27],v4 offset:57344\n"
MF "a[160:163],v[16:19],v[92:95],a[160:163]\n"
"s_nop 0\n"
TR "v[28:29],v5 offset:51200\n"
TR "v[30:31],v5 offset:57344\n"
MF "a[180:183],v[20:23],v[92:95],a[180:183]\n"
"v_add_f32 v53,v53,v119\n"
CV "v79,v118,v119\n"
MF "a[184:187],v[20:23],v[96:99],a[184:187]\n"
"v_exp_f32 v120,v120\n"
MF "a[164:167],v[16:19],v[96:99],a[164:167]\n"
"v_exp_f32 v121,v121\n"
MF "a[168:171],v[16:19],v[100:103],a[168:171]\n"
"v_exp_f32 v122,v122\n"
MF "a[188:191],v[20:23],v[100:103],a[188:191]\n"
"v_exp_f32 v123,v123\n"
MF "a[192:195],v[20:23],v[104:107],a[192:195]\n"
"v_add_f32 v54,v54,v120\n"
"v_add_f32 v54,v54,v121\n"
MF "a[172:175],v[16:19],v[104:107],a[172:175]\n"
"s_waitcnt vmcnt(6) lgkmcnt(0)\n"
CV "v82,v120,v121\n"
"v_add_f32 v54,v54,v122\n"
MF "a[176:179],v[16:19],v[108:111],a[176:179]\n"
"s_barrier\n"
MF "a[196:199],v[20:23],v[108:111],a[196:199]\n"
"s_add_u32 s72,s72,6144\n"
"ds_read_b128 v[16:19],v0\n"
"ds_read_b128 v[20:23],v1\n"
MF "a[216:219],v[24:27],v[108:111],a[216:219]\n"
"s_cmp_eq_u32 s72,s17\n"
"s_cselect_b32 s72,s16,s72\n"
"s_add_u32 m0,s64,49152\n"
MF "a[236:239],v[28:31],v[108:111],a[236:239]\n"
LD "v7,s[32:35],s72 offen nt lds\n"
MF "a[232:235],v[28:31],v[104:107],a[232:235]\n"
"s_add_u32 m0,s64,50048\n"
"v_add_f32 v54,v54,v123\n"
CV "v83,v122,v123\n"
MF "a[212:215],v[24:27],v[104:107],a[212:215]\n"
LD "v7,s[32:35],s72 offen offset:128 nt lds\n"
MF "a[208:211],v[24:27],v[100:103],a[208:211]\n"
"s_add_u32 m0,s64,50944\n"
MF "a[228:231],v[28:31],v[100:103],a[228:231]\n"
LD "v7,s[32:35],s72 offen offset:256 nt lds\n"
MF "a[224:227],v[28:31],v[96:99],a[224:227]\n"
MF "a[204:207],v[24:27],v[96:99],a[204:207]\n"
MF "a[200:203],v[24:27],v[92:95],a[200:203]\n"
MF "a[220:223],v[28:31],v[92:95],a[220:223]\n"
"s_waitcnt lgkmcnt(0)\n"
MF "v[92:95],v[16:19],v[136:139],v[32:35]\n"
"s_add_u32 s72,s72,6144\n"
"ds_read_b128 v[24:27],v0 offset:1024\n"
"ds_read_b128 v[28:31],v1 offset:1024\n"
MF "v[92:95],v[20:23],v[156:159],v[92:95]\n"
"s_add_u32 m0,s64,55296\n"
"v_exp_f32 v124,v124\n"
MF "v[96:99],v[20:23],v[160:163],v[36:39]\n"
LD "v7,s[32:35],s72 offen nt lds\n"
MF "v[96:99],v[16:19],v[140:143],v[96:99]\n"
"v_exp_f32 v125,v125\n"
MF "v[100:103],v[16:19],v[144:147],v[40:43]\n"
"s_add_u32 m0,s64,56192\n"
"v_exp_f32 v126,v126\n"
MF "v[100:103],v[20:23],v[164:167],v[100:103]\n"
LD "v7,s[32:35],s72 offen offset:128 nt lds\n"
MF "v[104:107],v[20:23],v[168:171],v[44:47]\n"
"v_exp_f32 v127,v127\n"
MF "v[104:107],v[16:19],v[148:151],v[104:107]\n"
"s_add_u32 m0,s64,57088\n"
"v_add_f32 v55,v55,v124\n"
"v_add_f32 v55,v55,v125\n"
MF "v[108:111],v[16:19],v[152:155],v[48:51]\n"
LD "v7,s[32:35],s72 offen offset:256 nt lds\n"
MF "v[108:111],v[20:23],v[172:175],v[108:111]\n"
"s_waitcnt lgkmcnt(0)\n"
"ds_read_b128 v[16:19],v0 offset:2048\n"
"ds_read_b128 v[20:23],v1 offset:2048\n"
MF "v[108:111],v[24:27],v[192:195],v[108:111]\n"
CV "v86,v124,v125\n"
"v_add_f32 v55,v55,v126\n"
MF "v[108:111],v[28:31],v[212:215],v[108:111]\n"
"v_add_f32 v55,v55,v127\n"
CV "v87,v126,v127\n"
MF "v[104:107],v[28:31],v[208:211],v[104:107]\n"
"v_exp_f32 v128,v128\n"
MF "v[104:107],v[24:27],v[188:191],v[104:107]\n"
"v_exp_f32 v129,v129\n"
MF "v[100:103],v[24:27],v[184:187],v[100:103]\n"
"v_exp_f32 v130,v130\n"
MF "v[100:103],v[28:31],v[204:207],v[100:103]\n"
"v_exp_f32 v131,v131\n"
MF "v[96:99],v[28:31],v[200:203],v[96:99]\n"
"v_add_f32 v56,v56,v128\n"
"v_add_f32 v56,v56,v129\n"
MF "v[96:99],v[24:27],v[180:183],v[96:99]\n"
CV "v90,v128,v129\n"
"v_add_f32 v56,v56,v130\n"
MF "v[92:95],v[24:27],v[176:179],v[92:95]\n"
"v_add_f32 v56,v56,v131\n"
CV "v91,v130,v131\n"
MF "v[92:95],v[28:31],v[196:199],v[92:95]\n"
"s_waitcnt lgkmcnt(0)\n"
"ds_read_b128 v[24:27],v0 offset:6144\n"
"ds_read_b128 v[28:31],v1 offset:6144\n"
MF "v[92:95],v[16:19],v[216:219],v[92:95]\n"
MF "v[92:95],v[20:23],v[236:239],v[92:95]\n"
MF "v[96:99],v[20:23],v[240:243],v[96:99]\n"
MF "v[96:99],v[16:19],v[220:223],v[96:99]\n"
MF "v[100:103],v[16:19],v[224:227],v[100:103]\n"
MF "v[100:103],v[20:23],v[244:247],v[100:103]\n"
MF "v[104:107],v[20:23],v[248:251],v[104:107]\n"
MF "v[104:107],v[16:19],v[228:231],v[104:107]\n"
"v_max3_f32 v10,v52,v53,v54\n"
"v_max3_f32 v10,v10,v55,v56\n"
MF "v[108:111],v[16:19],v[232:235],v[108:111]\n"
"v_cmp_ngt_f32 vcc,s77,v10\n"
MF "v[108:111],v[20:23],v[252:255],v[108:111]\n"
"s_waitcnt lgkmcnt(0)\n"
"s_cbranch_vccnz .Lstub_23\n"
".Lback_23:\n"
MF "v[112:115],v[24:27],v[136:139],v[32:35]\n"
"s_add_u32 s74,s74,12288\n"
"ds_read_b128 v[16:19],v0 offset:7168\n"
"ds_read_b128 v[20:23],v1 offset:7168\n"
MF "v[112:115],v[28:31],v[156:159],v[112:115]\n"
"s_cmp_eq_u32 s74,s20\n"
"s_cselect_b32 s74,s70,s74\n"
MF "v[116:119],v[28:31],v[160:163],v[36:39]\n"
"v_exp_f32 v92,v92\n"
MF "v[116:119],v[24:27],v[140:143],v[116:119]\n"
"v_exp_f32 v93,v93\n"
MF "v[120:123],v[24:27],v[144:147],v[40:43]\n"
"v_exp_f32 v94,v94\n"
MF "v[120:123],v[28:31],v[164:167],v[120:123]\n"
"v_exp_f32 v95,v95\n"
MF "v[124:127],v[28:31],v[168:171],v[44:47]\n"
"v_add_f32 v57,v52,v92\n"
"v_add_f32 v57,v57,v93\n"
MF "v[124:127],v[24:27],v[148:151],v[124:127]\n"
CV "v92,v92,v93\n"
"v_add_f32 v57,v57,v94\n"
MF "v[128:131],v[24:27],v[152:155],v[48:51]\n"
"v_add_f32 v57,v57,v95\n"
CV "v93,v94,v95\n"
MF "v[128:131],v[28:31],v[172:175],v[128:131]\n"
"s_waitcnt lgkmcnt(0)\n"
"ds_read_b128 v[24:27],v0 offset:8192\n"
"ds_read_b128 v[28:31],v1 offset:8192\n"
MF "v[128:131],v[16:19],v[192:195],v[128:131]\n"
"v_exp_f32 v96,v96\n"
MF "v[128:131],v[20:23],v[212:215],v[128:131]\n"
"v_exp_f32 v97,v97\n"
MF "v[124:127],v[20:23],v[208:211],v[124:127]\n"
"v_exp_f32 v98,v98\n"
MF "v[124:127],v[16:19],v[188:191],v[124:127]\n"
"v_exp_f32 v99,v99\n"
MF "v[120:123],v[16:19],v[184:187],v[120:123]\n"
"v_add_f32 v58,v53,v96\n"
"v_add_f32 v58,v58,v97\n"
MF "v[120:123],v[20:23],v[204:207],v[120:123]\n"
CV "v96,v96,v97\n"
"v_add_f32 v58,v58,v98\n"
MF "v[116:119],v[20:23],v[200:203],v[116:119]\n"
"s_waitcnt vmcnt(6) lgkmcnt(0)\n"
"v_add_f32 v58,v58,v99\n"
CV "v97,v98,v99\n"
MF "v[116:119],v[16:19],v[180:183],v[116:119]\n"
"s_barrier\n"
MF "v[112:115],v[16:19],v[176:179],v[112:115]\n"
"v_exp_f32 v100,v100\n"
MF "v[112:115],v[20:23],v[196:199],v[112:115]\n"
"s_nop 0\n"
TR "v[16:17],v2 offset:24576\n"
TR "v[18:19],v2 offset:30720\n"
MF "v[112:115],v[24:27],v[216:219],v[112:115]\n"
"s_add_u32 s73,s73,12288\n"
TR "v[20:21],v3 offset:24576\n"
TR "v[22:23],v3 offset:30720\n"
MF "v[112:115],v[28:31],v[236:239],v[112:115]\n"
"s_cmp_eq_u32 s73,s19\n"
"s_cselect_b32 s73,s18,s73\n"
"s_add_u32 m0,s65,0\n"
MF "v[116:119],v[28:31],v[240:243],v[116:119]\n"
LD "v8,s[36:39],s73 offen nt lds\n"
MF "v[116:119],v[24:27],v[220:223],v[116:119]\n"
"s_add_u32 m0,s65,896\n"
"v_exp_f32 v101,v101\n"
MF "v[120:123],v[24:27],v[224:227],v[120:123]\n"
LD "v8,s[36:39],s73 offen offset:128 nt lds\n"
MF "v[120:123],v[28:31],v[244:247],v[120:123]\n"
"v_exp_f32 v102,v102\n"
MF "v[124:127],v[28:31],v[248:251],v[124:127]\n"
"s_add_u32 m0,s65,1792\n"
"v_exp_f32 v103,v103\n"
MF "v[124:127],v[24:27],v[228:231],v[124:127]\n"
LD "v8,s[36:39],s73 offen offset:256 nt lds\n"
MF "v[128:131],v[24:27],v[232:235],v[128:131]\n"
"v_add_f32 v59,v54,v100\n"
"v_add_f32 v59,v59,v101\n"
MF "v[128:131],v[28:31],v[252:255],v[128:131]\n"
"s_waitcnt lgkmcnt(0)\n"
TR "v[24:25],v4 offset:24576\n"
TR "v[26:27],v4 offset:30720\n"
MF "a[0:3],v[16:19],v[72:75],a[0:3]\n"
"s_add_u32 m0,s65,0\n"
TR "v[28:29],v5 offset:24576\n"
TR "v[30:31],v5 offset:30720\n"
MF "a[20:23],v[20:23],v[72:75],a[20:23]\n"
LD "v8,s[36:39],s73 offen offset:3072 nt lds\n"
MF "a[24:27],v[20:23],v[76:79],a[24:27]\n"
CV "v100,v100,v101\n"
"v_add_f32 v59,v59,v102\n"
MF "a[4:7],v[16:19],v[76:79],a[4:7]\n"
"s_add_u32 m0,s65,896\n"
"v_add_f32 v59,v59,v103\n"
CV "v101,v102,v103\n"
MF "a[8:11],v[16:19],v[80:83],a[8:11]\n"
LD "v8,s[36:39],s73 offen offset:3200 nt lds\n"
MF "a[28:31],v[20:23],v[80:83],a[28:31]\n"
"v_exp_f32 v104,v104\n"
MF "a[32:35],v[20:23],v[84:87],a[32:35]\n"
"s_add_u32 m0,s65,1792\n"
"v_exp_f32 v105,v105\n"
MF "a[12:15],v[16:19],v[84:87],a[12:15]\n"
LD "v8,s[36:39],s73 offen offset:3328 nt lds\n"
MF "a[16:19],v[16:19],v[88:91],a[16:19]\n"
"v_exp_f32 v106,v106\n"
MF "a[36:39],v[20:23],v[88:91],a[36:39]\n"
"s_waitcnt lgkmcnt(0)\n"
TR "v[16:17],v2 offset:25600\n"
TR "v[18:19],v2 offset:31744\n"
MF "a[56:59],v[24:27],v[88:91],a[56:59]\n"
"s_nop 0\n"
TR "v[20:21],v3 offset:25600\n"
TR "v[22:23],v3 offset:31744\n"
MF "a[76:79],v[28:31],v[88:91],a[76:79]\n"
"v_exp_f32 v107,v107\n"
MF "a[72:75],v[28:31],v[84:87],a[72:75]\n"
"v_add_f32 v60,v55,v104\n"
"v_add_f32 v60,v60,v105\n"
MF "a[52:55],v[24:27],v[84:87],a[52:55]\n"
CV "v104,v104,v105\n"
"v_add_f32 v60,v60,v106\n"
MF "a[48:51],v[24:27],v[80:83],a[48:51]\n"
"v_add_f32 v60,v60,v107\n"
CV "v105,v106,v107\n"
MF "a[68:71],v[28:31],v[80:83],a[68:71]\n"
"v_exp_f32 v108,v108\n"
MF "a[64:67],v[28:31],v[76:79],a[64:67]\n"
"v_exp_f32 v109,v109\n"
MF "a[44:47],v[24:27],v[76:79],a[44:47]\n"
"v_exp_f32 v110,v110\n"
MF "a[40:43],v[24:27],v[72:75],a[40:43]\n"
"v_exp_f32 v111,v111\n"
MF "a[60:63],v[28:31],v[72:75],a[60:63]\n"
"s_waitcnt lgkmcnt(0)\n"
TR "v[24:25],v4 offset:25600\n"
TR "v[26:27],v4 offset:31744\n"
MF "a[80:83],v[16:19],v[72:75],a[80:83]\n"
"s_nop 0\n"
TR "v[28:29],v5 offset:25600\n"
TR "v[30:31],v5 offset:31744\n"
MF "a[100:103],v[20:23],v[72:75],a[100:103]\n"
"v_add_f32 v61,v56,v108\n"
"v_add_f32 v61,v61,v109\n"
MF "a[104:107],v[20:23],v[76:79],a[104:107]\n"
CV "v108,v108,v109\n"
"v_add_f32 v61,v61,v110\n"
MF "a[84:87],v[16:19],v[76:79],a[84:87]\n"
"v_add_f32 v61,v61,v111\n"
CV "v109,v110,v111\n"
MF "a[88:91],v[16:19],v[80:83],a[88:91]\n"
"v_exp_f32 v112,v112\n"
MF "a[108:111],v[20:23],v[80:83],a[108:111]\n"
"v_exp_f32 v113,v113\n"
MF "a[112:115],v[20:23],v[84:87],a[112:115]\n"
"v_exp_f32 v114,v114\n"
MF "a[92:95],v[16:19],v[84:87],a[92:95]\n"
"v_exp_f32 v115,v115\n"
MF "a[96:99],v[16:19],v[88:91],a[96:99]\n"
"v_add_f32 v57,v57,v112\n"
"v_add_f32 v57,v57,v113\n"
MF "a[116:119],v[20:23],v[88:91],a[116:119]\n"
"s_waitcnt lgkmcnt(0)\n"
TR "v[16:17],v2 offset:26624\n"
TR "v[18:19],v2 offset:32768\n"
MF "a[136:139],v[24:27],v[88:91],a[136:139]\n"
"s_nop 0\n"
TR "v[20:21],v3 offset:26624\n"
TR "v[22:23],v3 offset:32768\n"
MF "a[156:159],v[28:31],v[88:91],a[156:159]\n"
CV "v94,v112,v113\n"
"v_add_f32 v57,v57,v114\n"
MF "a[152:155],v[28:31],v[84:87],a[152:155]\n"
"v_add_f32 v57,v57,v115\n"
CV "v95,v114,v115\n"
MF "a[132:135],v[24:27],v[84:87],a[132:135]\n"
"v_exp_f32 v116,v116\n"
MF "a[128:131],v[24:27],v[80:83],a[128:131]\n"
"v_exp_f32 v117,v117\n"
MF "a[148:151],v[28:31],v[80:83],a[148:151]\n"
"v_exp_f32 v118,v118\n"
MF "a[144:147],v[28:31],v[76:79],a[144:147]\n"
"v_exp_f32 v119,v119\n"
MF "a[124:127],v[24:27],v[76:79],a[124:127]\n"
"v_add_f32 v58,v58,v116\n"
"v_add_f32 v58,v58,v117\n"
MF "a[120:123],v[24:27],v[72:75],a[120:123]\n"
CV "v98,v116,v117\n"
"v_add_f32 v58,v58,v118\n"
MF "a[140:143],v[28:31],v[72:75],a[140:143]\n"
"s_waitcnt lgkmcnt(0)\n"
TR "v[24:25],v4 offset:26624\n"
TR "v[26:27],v4 offset:32768\n"
MF "a[160:163],v[16:19],v[72:75],a[160:163]\n"
"s_nop 0\n"
TR "v[28:29],v5 offset:26624\n"
TR "v[30:31],v5 offset:32768\n"
MF "a[180:183],v[20:23],v[72:75],a[180:183]\n"
"v_add_f32 v58,v58,v119\n"
CV "v99,v118,v119\n"
MF "a[184:187],v[20:23],v[76:79],a[184:187]\n"
"v_exp_f32 v120,v120\n"
MF "a[164:167],v[16:19],v[76:79],a[164:167]\n"
"v_exp_f32 v121,v121\n"
MF "a[168:171],v[16:19],v[80:83],a[168:171]\n"
"v_exp_f32 v122,v122\n"
MF "a[188:191],v[20:23],v[80:83],a[188:191]\n"
"v_exp_f32 v123,v123\n"
MF "a[192:195],v[20:23],v[84:87],a[192:195]\n"
"v_add_f32 v59,v59,v120\n"
"v_add_f32 v59,v59,v121\n"
MF "a[172:175],v[16:19],v[84:87],a[172:175]\n"
"s_waitcnt vmcnt(6) lgkmcnt(0)\n"
CV "v102,v120,v121\n"
"v_add_f32 v59,v59,v122\n"
MF "a[176:179],v[16:19],v[88:91],a[176:179]\n"
"s_barrier\n"
MF "a[196:199],v[20:23],v[88:91],a[196:199]\n"
"s_add_u32 s72,s72,6144\n"
"ds_read_b128 v[16:19],v0 offset:49152\n"
"ds_read_b128 v[20:23],v1 offset:49152\n"
MF "a[216:219],v[24:27],v[88:91],a[216:219]\n"
"s_cmp_eq_u32 s72,s17\n"
"s_cselect_b32 s72,s16,s72\n"
"s_add_u32 m0,s64,24576\n"
MF "a[236:239],v[28:31],v[88:91],a[236:239]\n"
LD "v7,s[32:35],s72 offen nt lds\n"
MF "a[232:235],v[28:31],v[84:87],a[232:235]\n"
"s_add_u32 m0,s64,25472\n"
"v_add_f32 v59,v59,v123\n"
CV "v103,v122,v123\n"
MF "a[212:215],v[24:27],v[84:87],a[212:215]\n"
LD "v7,s[32:35],s72 offen offset:128 nt lds\n"
MF "a[208:211],v[24:27],v[80:83],a[208:211]\n"
"s_add_u32 m0,s64,26368\n"
MF "a[228:231],v[28:31],v[80:83],a[228:231]\n"
LD "v7,s[32:35],s72 offen offset:256 nt lds\n"
MF "a[224:227],v[28:31],v[76:79],a[224:227]\n"
MF "a[204:207],v[24:27],v[76:79],a[204:207]\n"
MF "a[200:203],v[24:27],v[72:75],a[200:203]\n"
MF "a[220:223],v[28:31],v[72:75],a[220:223]\n"
"s_waitcnt lgkmcnt(0)\n"
MF "v[72:75],v[16:19],v[136:139],v[32:35]\n"
"s_add_u32 s72,s72,6144\n"
"ds_read_b128 v[24:27],v0 offset:50176\n"
"ds_read_b128 v[28:31],v1 offset:50176\n"
MF "v[72:75],v[20:23],v[156:159],v[72:75]\n"
"s_add_u32 m0,s64,30720\n"
"v_exp_f32 v124,v124\n"
MF "v[76:79],v[20:23],v[160:163],v[36:39]\n"
LD "v7,s[32:35],s72 offen nt lds\n"
MF "v[76:79],v[16:19],v[140:143],v[76:79]\n"
"v_exp_f32 v125,v125\n"
MF "v[80:83],v[16:19],v[144:147],v[40:43]\n"
"s_add_u32 m0,s64,31616\n"
"v_exp_f32 v126,v126\n"
MF "v[80:83],v[20:23],v[164:167],v[80:83]\n"
LD "v7,s[32:35],s72 offen offset:128 nt lds\n"
MF "v[84:87],v[20:23],v[168:171],v[44:47]\n"
"v_exp_f32 v127,v127\n"
MF "v[84:87],v[16:19],v[148:151],v[84:87]\n"
"s_add_u32 m0,s64,32512\n"
"v_add_f32 v60,v60,v124\n"
"v_add_f32 v60,v60,v125\n"
MF "v[88:91],v[16:19],v[152:155],v[48:51]\n"
LD "v7,s[32:35],s72 offen offset:256 nt lds\n"
MF "v[88:91],v[20:23],v[172:175],v[88:91]\n"
"s_waitcnt lgkmcnt(0)\n"
"ds_read_b128 v[16:19],v0 offset:51200\n"
"ds_read_b128 v[20:23],v1 offset:51200\n"
MF "v[88:91],v[24:27],v[192:195],v[88:91]\n"
CV "v106,v124,v125\n"
"v_add_f32 v60,v60,v126\n"
MF "v[88:91],v[28:31],v[212:215],v[88:91]\n"
"v_add_f32 v60,v60,v127\n"
CV "v107,v126,v127\n"
MF "v[84:87],v[28:31],v[208:211],v[84:87]\n"
"v_exp_f32 v128,v128\n"
MF "v[84:87],v[24:27],v[188:191],v[84:87]\n"
"v_exp_f32 v129,v129\n"
MF "v[80:83],v[24:27],v[184:187],v[80:83]\n"
"v_exp_f32 v130,v130\n"
MF "v[80:83],v[28:31],v[204:207],v[80:83]\n"
"v_exp_f32 v131,v131\n"
MF "v[76:79],v[28:31],v[200:203],v[76:79]\n"
"v_add_f32 v61,v61,v128\n"
"v_add_f32 v61,v61,v129\n"
MF "v[76:79],v[24:27],v[180:183],v[76:79]\n"
CV "v110,v128,v129\n"
"v_add_f32 v61,v61,v130\n"
MF "v[72:75],v[24:27],v[176:179],v[72:75]\n"
"v_add_f32 v61,v61,v131\n"
CV "v111,v130,v131\n"
MF "v[72:75],v[28:31],v[196:199],v[72:75]\n"
"s_waitcnt lgkmcnt(0)\n"
"ds_read_b128 v[24:27],v0 offset:55296\n"
"ds_read_b128 v[28:31],v1 offset:55296\n"
MF "v[72:75],v[16:19],v[216:219],v[72:75]\n"
MF "v[72:75],v[20:23],v[236:239],v[72:75]\n"
MF "v[76:79],v[20:23],v[240:243],v[76:79]\n"
MF "v[76:79],v[16:19],v[220:223],v[76:79]\n"
MF "v[80:83],v[16:19],v[224:227],v[80:83]\n"
MF "v[80:83],v[20:23],v[244:247],v[80:83]\n"
MF "v[84:87],v[20:23],v[248:251],v[84:87]\n"
MF "v[84:87],v[16:19],v[228:231],v[84:87]\n"
"v_max3_f32 v10,v57,v58,v59\n"
"v_max3_f32 v10,v10,v60,v61\n"
MF "v[88:91],v[16:19],v[232:235],v[88:91]\n"
"v_cmp_ngt_f32 vcc,s95,v10\n"
MF "v[88:91],v[20:23],v[252:255],v[88:91]\n"
"s_waitcnt lgkmcnt(0)\n"
"s_cbranch_vccnz .Lstub_24\n"
".Lback_24:\n"
MF "v[112:115],v[24:27],v[136:139],v[32:35]\n"
"s_add_u32 s74,s74,12288\n"
"ds_read_b128 v[16:19],v0 offset:56320\n"
"ds_read_b128 v[20:23],v1 offset:56320\n"
MF "v[112:115],v[28:31],v[156:159],v[112:115]\n"
"s_cmp_eq_u32 s74,s20\n"
"s_cselect_b32 s74,s70,s74\n"
MF "v[116:119],v[28:31],v[160:163],v[36:39]\n"
"v_exp_f32 v72,v72\n"
MF "v[116:119],v[24:27],v[140:143],v[116:119]\n"
"v_exp_f32 v73,v73\n"
MF "v[120:123],v[24:27],v[144:147],v[40:43]\n"
"v_exp_f32 v74,v74\n"
MF "v[120:123],v[28:31],v[164:167],v[120:123]\n"
"v_exp_f32 v75,v75\n"
MF "v[124:127],v[28:31],v[168:171],v[44:47]\n"
"v_add_f32 v52,v57,v72\n"
"v_add_f32 v52,v52,v73\n"
MF "v[124:127],v[24:27],v[148:151],v[124:127]\n"
CV "v72,v72,v73\n"
"v_add_f32 v52,v52,v74\n"
MF "v[128:131],v[24:27],v[152:155],v[48:51]\n"
"v_add_f32 v52,v52,v75\n"
CV "v73,v74,v75\n"
MF "v[128:131],v[28:31],v[172:175],v[128:131]\n"
"s_waitcnt lgkmcnt(0)\n"
"ds_read_b128 v[24:27],v0 offset:57344\n"
"ds_read_b128 v[28:31],v1 offset:57344\n"
MF "v[128:131],v[16:19],v[192:195],v[128:131]\n"
"v_exp_f32 v76,v76\n"
MF "v[128:131],v[20:23],v[212:215],v[128:131]\n"
"v_exp_f32 v77,v77\n"
MF "v[124:127],v[20:23],v[208:211],v[124:127]\n"
"v_exp_f32 v78,v78\n"
MF "v[124:127],v[16:19],v[188:191],v[124:127]\n"
"v_exp_f32 v79,v79\n"
MF "v[120:123],v[16:19],v[184:187],v[120:123]\n"
"v_add_f32 v53,v58,v76\n"
"v_add_f32 v53,v53,v77\n"
MF "v[120:123],v[20:23],v[204:207],v[120:123]\n"
CV "v76,v76,v77\n"
"v_add_f32 v53,v53,v78\n"
MF "v[116:119],v[20:23],v[200:203],v[116:119]\n"
"s_waitcnt vmcnt(6) lgkmcnt(0)\n"
"v_add_f32 v53,v53,v79\n"
CV "v77,v78,v79\n"
MF "v[116:119],v[16:19],v[180:183],v[116:119]\n"
"s_barrier\n"
MF "v[112:115],v[16:19],v[176:179],v[112:115]\n"
"v_exp_f32 v80,v80\n"
MF "v[112:115],v[20:23],v[196:199],v[112:115]\n"
"s_nop 0\n"
TR "v[16:17],v2\n"
TR "v[18:19],v2 offset:6144\n"
MF "v[112:115],v[24:27],v[216:219],v[112:115]\n"
"s_add_u32 s73,s73,12288\n"
TR "v[20:21],v3\n"
TR "v[22:23],v3 offset:6144\n"
MF "v[112:115],v[28:31],v[236:239],v[112:115]\n"
"s_cmp_eq_u32 s73,s19\n"
"s_cselect_b32 s73,s18,s73\n"
"s_add_u32 m0,s65,49152\n"
MF "v[116:119],v[28:31],v[240:243],v[116:119]\n"
LD "v8,s[36:39],s73 offen nt lds\n"
MF "v[116:119],v[24:27],v[220:223],v[116:119]\n"
"s_add_u32 m0,s65,50048\n"
"v_exp_f32 v81,v81\n"
MF "v[120:123],v[24:27],v[224:227],v[120:123]\n"
LD "v8,s[36:39],s73 offen offset:128 nt lds\n"
MF "v[120:123],v[28:31],v[244:247],v[120:123]\n"
"v_exp_f32 v82,v82\n"
MF "v[124:127],v[28:31],v[248:251],v[124:127]\n"
"s_add_u32 m0,s65,50944\n"
"v_exp_f32 v83,v83\n"
MF "v[124:127],v[24:27],v[228:231],v[124:127]\n"
LD "v8,s[36:39],s73 offen offset:256 nt lds\n"
MF "v[128:131],v[24:27],v[232:235],v[128:131]\n"
"v_add_f32 v54,v59,v80\n"
"v_add_f32 v54,v54,v81\n"
MF "v[128:131],v[28:31],v[252:255],v[128:131]\n"
"s_waitcnt lgkmcnt(0)\n"
TR "v[24:25],v4\n"
TR "v[26:27],v4 offset:6144\n"
MF "a[0:3],v[16:19],v[92:95],a[0:3]\n"
"s_add_u32 m0,s65,49152\n"
TR "v[28:29],v5\n"
TR "v[30:31],v5 offset:6144\n"
MF "a[20:23],v[20:23],v[92:95],a[20:23]\n"
LD "v8,s[36:39],s73 offen offset:3072 nt lds\n"
MF "a[24:27],v[20:23],v[96:99],a[24:27]\n"
CV "v80,v80,v81\n"
"v_add_f32 v54,v54,v82\n"
MF "a[4:7],v[16:19],v[96:99],a[4:7]\n"
"s_add_u32 m0,s65,50048\n"
"v_add_f32 v54,v54,v83\n"
CV "v81,v82,v83\n"
MF "a[8:11],v[16:19],v[100:103],a[8:11]\n"
LD "v8,s[36:39],s73 offen offset:3200 nt lds\n"
MF "a[28:31],v[20:23],v[100:103],a[28:31]\n"
"v_exp_f32 v84,v84\n"
MF "a[32:35],v[20:23],v[104:107],a[32:35]\n"
"s_add_u32 m0,s65,50944\n"
"v_exp_f32 v85,v85\n"
MF "a[12:15],v[16:19],v[104:107],a[12:15]\n"
LD "v8,s[36:39],s73 offen offset:3328 nt lds\n"
MF "a[16:19],v[16:19],v[108:111],a[16:19]\n"
"v_exp_f32 v86,v86\n"
MF "a[36:39],v[20:23],v[108:111],a[36:39]\n"
"s_waitcnt lgkmcnt(0)\n"
TR "v[16:17],v2 offset:1024\n"
TR "v[18:19],v2 offset:7168\n"
MF "a[56:59],v[24:27],v[108:111],a[56:59]\n"
"s_nop 0\n"
TR "v[20:21],v3 offset:1024\n"
TR "v[22:23],v3 offset:7168\n"
MF "a[76:79],v[28:31],v[108:111],a[76:79]\n"
"v_exp_f32 v87,v87\n"
MF "a[72:75],v[28:31],v[104:107],a[72:75]\n"
"v_add_f32 v55,v60,v84\n"
"v_add_f32 v55,v55,v85\n"
MF "a[52:55],v[24:27],v[104:107],a[52:55]\n"
CV "v84,v84,v85\n"
"v_add_f32 v55,v55,v86\n"
MF "a[48:51],v[24:27],v[100:103],a[48:51]\n"
"v_add_f32 v55,v55,v87\n"
CV "v85,v86,v87\n"
MF "a[68:71],v[28:31],v[100:103],a[68:71]\n"
"v_exp_f32 v88,v88\n"
MF "a[64:67],v[28:31],v[96:99],a[64:67]\n"
"v_exp_f32 v89,v89\n"
MF "a[44:47],v[24:27],v[96:99],a[44:47]\n"
"v_exp_f32 v90,v90\n"
MF "a[40:43],v[24:27],v[92:95],a[40:43]\n"
"v_exp_f32 v91,v91\n"
MF "a[60:63],v[28:31],v[92:95],a[60:63]\n"
"s_waitcnt lgkmcnt(0)\n"
TR "v[24:25],v4 offset:1024\n"
TR "v[26:27],v4 offset:7168\n"
MF "a[80:83],v[16:19],v[92:95],a[80:83]\n"
"s_nop 0\n"
TR "v[28:29],v5 offset:1024\n"
TR "v[30:31],v5 offset:7168\n"
MF "a[100:103],v[20:23],v[92:95],a[100:103]\n"
"v_add_f32 v56,v61,v88\n"
"v_add_f32 v56,v56,v89\n"
MF "a[104:107],v[20:23],v[96:99],a[104:107]\n"
CV "v88,v88,v89\n"
"v_add_f32 v56,v56,v90\n"
MF "a[84:87],v[16:19],v[96:99],a[84:87]\n"
"v_add_f32 v56,v56,v91\n"
CV "v89,v90,v91\n"
MF "a[88:91],v[16:19],v[100:103],a[88:91]\n"
"v_exp_f32 v112,v112\n"
MF "a[108:111],v[20:23],v[100:103],a[108:111]\n"
"v_exp_f32 v113,v113\n"
MF "a[112:115],v[20:23],v[104:107],a[112:115]\n"
"v_exp_f32 v114,v114\n"
MF "a[92:95],v[16:19],v[104:107],a[92:95]\n"
"v_exp_f32 v115,v115\n"
MF "a[96:99],v[16:19],v[108:111],a[96:99]\n"
"v_add_f32 v52,v52,v112\n"
"v_add_f32 v52,v52,v113\n"
MF "a[116:119],v[20:23],v[108:111],a[116:119]\n"
"s_waitcnt lgkmcnt(0)\n"
TR "v[16:17],v2 offset:2048\n"
TR "v[18:19],v2 offset:8192\n"
MF "a[136:139],v[24:27],v[108:111],a[136:139]\n"
"s_nop 0\n"
TR "v[20:21],v3 offset:2048\n"
TR "v[22:23],v3 offset:8192\n"
MF "a[156:159],v[28:31],v[108:111],a[156:159]\n"
CV "v74,v112,v113\n"
"v_add_f32 v52,v52,v114\n"
MF "a[152:155],v[28:31],v[104:107],a[152:155]\n"
"v_add_f32 v52,v52,v115\n"
CV "v75,v114,v115\n"
MF "a[132:135],v[24:27],v[104:107],a[132:135]\n"
"v_exp_f32 v116,v116\n"
MF "a[128:131],v[24:27],v[100:103],a[128:131]\n"
"v_exp_f32 v117,v117\n"
MF "a[148:151],v[28:31],v[100:103],a[148:151]\n"
"v_exp_f32 v118,v118\n"
MF "a[144:147],v[28:31],v[96:99],a[144:147]\n"
"v_exp_f32 v119,v119\n"
MF "a[124:127],v[24:27],v[96:99],a[124:127]\n"
"v_add_f32 v53,v53,v116\n"
"v_add_f32 v53,v53,v117\n"
MF "a[120:123],v[24:27],v[92:95],a[120:123]\n"
CV "v78,v116,v117\n"
"v_add_f32 v53,v53,v118\n"
MF "a[140:143],v[28:31],v[92:95],a[140:143]\n"
"s_waitcnt lgkmcnt(0)\n"
TR "v[24:25],v4 offset:2048\n"
TR "v[26:27],v4 offset:8192\n"
MF "a[160:163],v[16:19],v[92:95],a[160:163]\n"
"s_nop 0\n"
TR "v[28:29],v5 offset:2048\n"
TR "v[30:31],v5 offset:8192\n"
MF "a[180:183],v[20:23],v[92:95],a[180:183]\n"
"v_add_f32 v53,v53,v119\n"
CV "v79,v118,v119\n"
MF "a[184:187],v[20:23],v[96:99],a[184:187]\n"
"v_exp_f32 v120,v120\n"
MF "a[164:167],v[16:19],v[96:99],a[164:167]\n"
"v_exp_f32 v121,v121\n"
MF "a[168:171],v[16:19],v[100:103],a[168:171]\n"
"v_exp_f32 v122,v122\n"
MF "a[188:191],v[20:23],v[100:103],a[188:191]\n"
"v_exp_f32 v123,v123\n"
MF "a[192:195],v[20:23],v[104:107],a[192:195]\n"
"v_add_f32 v54,v54,v120\n"
"v_add_f32 v54,v54,v121\n"
MF "a[172:175],v[16:19],v[104:107],a[172:175]\n"
"s_waitcnt vmcnt(6) lgkmcnt(0)\n"
CV "v82,v120,v121\n"
"v_add_f32 v54,v54,v122\n"
MF "a[176:179],v[16:19],v[108:111],a[176:179]\n"
"s_barrier\n"
MF "a[196:199],v[20:23],v[108:111],a[196:199]\n"
"s_add_u32 s72,s72,6144\n"
"ds_read_b128 v[16:19],v0 offset:24576\n"
"ds_read_b128 v[20:23],v1 offset:24576\n"
MF "a[216:219],v[24:27],v[108:111],a[216:219]\n"
"s_cmp_eq_u32 s72,s17\n"
"s_cselect_b32 s72,s16,s72\n"
"s_add_u32 m0,s64,0\n"
MF "a[236:239],v[28:31],v[108:111],a[236:239]\n"
LD "v7,s[32:35],s72 offen nt lds\n"
MF "a[232:235],v[28:31],v[104:107],a[232:235]\n"
"s_add_u32 m0,s64,896\n"
"v_add_f32 v54,v54,v123\n"
CV "v83,v122,v123\n"
MF "a[212:215],v[24:27],v[104:107],a[212:215]\n"
LD "v7,s[32:35],s72 offen offset:128 nt lds\n"
MF "a[208:211],v[24:27],v[100:103],a[208:211]\n"
"s_add_u32 m0,s64,1792\n"
MF "a[228:231],v[28:31],v[100:103],a[228:231]\n"
LD "v7,s[32:35],s72 offen offset:256 nt lds\n"
MF "a[224:227],v[28:31],v[96:99],a[224:227]\n"
MF "a[204:207],v[24:27],v[96:99],a[204:207]\n"
MF "a[200:203],v[24:27],v[92:95],a[200:203]\n"
MF "a[220:223],v[28:31],v[92:95],a[220:223]\n"
"s_waitcnt lgkmcnt(0)\n"
MF "v[92:95],v[16:19],v[136:139],v[32:35]\n"
"s_add_u32 s72,s72,6144\n"
"ds_read_b128 v[24:27],v0 offset:25600\n"
"ds_read_b128 v[28:31],v1 offset:25600\n"
MF "v[92:95],v[20:23],v[156:159],v[92:95]\n"
"s_add_u32 m0,s64,6144\n"
"v_exp_f32 v124,v124\n"
MF "v[96:99],v[20:23],v[160:163],v[36:39]\n"
LD "v7,s[32:35],s72 offen nt lds\n"
MF "v[96:99],v[16:19],v[140:143],v[96:99]\n"
"v_exp_f32 v125,v125\n"
MF "v[100:103],v[16:19],v[144:147],v[40:43]\n"
"s_add_u32 m0,s64,7040\n"
"v_exp_f32 v126,v126\n"
MF "v[100:103],v[20:23],v[164:167],v[100:103]\n"
LD "v7,s[32:35],s72 offen offset:128 nt lds\n"
MF "v[104:107],v[20:23],v[168:171],v[44:47]\n"
"v_exp_f32 v127,v127\n"
MF "v[104:107],v[16:19],v[148:151],v[104:107]\n"
"s_add_u32 m0,s64,7936\n"
"v_add_f32 v55,v55,v124\n"
"v_add_f32 v55,v55,v125\n"
MF "v[108:111],v[16:19],v[152:155],v[48:51]\n"
LD "v7,s[32:35],s72 offen offset:256 nt lds\n"
MF "v[108:111],v[20:23],v[172:175],v[108:111]\n"
"s_waitcnt lgkmcnt(0)\n"
"ds_read_b128 v[16:19],v0 offset:26624\n"
"ds_read_b128 v[20:23],v1 offset:26624\n"
MF "v[108:111],v[24:27],v[192:195],v[108:111]\n"
CV "v86,v124,v125\n"
"v_add_f32 v55,v55,v126\n"
MF "v[108:111],v[28:31],v[212:215],v[108:111]\n"
"v_add_f32 v55,v55,v127\n"
CV "v87,v126,v127\n"
MF "v[104:107],v[28:31],v[208:211],v[104:107]\n"
"v_exp_f32 v128,v128\n"
MF "v[104:107],v[24:27],v[188:191],v[104:107]\n"
"v_exp_f32 v129,v129\n"
MF "v[100:103],v[24:27],v[184:187],v[100:103]\n"
"v_exp_f32 v130,v130\n"
MF "v[100:103],v[28:31],v[204:207],v[100:103]\n"
"v_exp_f32 v131,v131\n"
MF "v[96:99],v[28:31],v[200:203],v[96:99]\n"
"v_add_f32 v56,v56,v128\n"
"v_add_f32 v56,v56,v129\n"
MF "v[96:99],v[24:27],v[180:183],v[96:99]\n"
CV "v90,v128,v129\n"
"v_add_f32 v56,v56,v130\n"
MF "v[92:95],v[24:27],v[176:179],v[92:95]\n"
"v_add_f32 v56,v56,v131\n"
CV "v91,v130,v131\n"
MF "v[92:95],v[28:31],v[196:199],v[92:95]\n"
"s_waitcnt lgkmcnt(0)\n"
"ds_read_b128 v[24:27],v0 offset:30720\n"
"ds_read_b128 v[28:31],v1 offset:30720\n"
MF "v[92:95],v[16:19],v[216:219],v[92:95]\n"
MF "v[92:95],v[20:23],v[236:239],v[92:95]\n"
MF "v[96:99],v[20:23],v[240:243],v[96:99]\n"
MF "v[96:99],v[16:19],v[220:223],v[96:99]\n"
MF "v[100:103],v[16:19],v[224:227],v[100:103]\n"
MF "v[100:103],v[20:23],v[244:247],v[100:103]\n"
MF "v[104:107],v[20:23],v[248:251],v[104:107]\n"
MF "v[104:107],v[16:19],v[228:231],v[104:107]\n"
"v_max3_f32 v10,v52,v53,v54\n"
"v_max3_f32 v10,v10,v55,v56\n"
MF "v[108:111],v[16:19],v[232:235],v[108:111]\n"
"v_cmp_ngt_f32 vcc,s77,v10\n"
MF "v[108:111],v[20:23],v[252:255],v[108:111]\n"
"s_waitcnt lgkmcnt(0)\n"
"s_cbranch_vccnz .Lstub_25\n"
".Lback_25:\n"
MF "v[112:115],v[24:27],v[136:139],v[32:35]\n"
"s_add_u32 s74,s74,12288\n"
"ds_read_b128 v[16:19],v0 offset:31744\n"
"ds_read_b128 v[20:23],v1 offset:31744\n"
MF "v[112:115],v[28:31],v[156:159],v[112:115]\n"
"s_cmp_eq_u32 s74,s20\n"
"s_cselect_b32 s74,s70,s74\n"
MF "v[116:119],v[28:31],v[160:163],v[36:39]\n"
"v_exp_f32 v92,v92\n"
MF "v[116:119],v[24:27],v[140:143],v[116:119]\n"
"v_exp_f32 v93,v93\n"
MF "v[120:123],v[24:27],v[144:147],v[40:43]\n"
"v_exp_f32 v94,v94\n"
MF "v[120:123],v[28:31],v[164:167],v[120:123]\n"
"v_exp_f32 v95,v95\n"
MF "v[124:127],v[28:31],v[168:171],v[44:47]\n"
"v_add_f32 v57,v52,v92\n"
"v_add_f32 v57,v57,v93\n"
MF "v[124:127],v[24:27],v[148:151],v[124:127]\n"
CV "v92,v92,v93\n"
"v_add_f32 v57,v57,v94\n"
MF "v[128:131],v[24:27],v[152:155],v[48:51]\n"
"v_add_f32 v57,v57,v95\n"
CV "v93,v94,v95\n"
MF "v[128:131],v[28:31],v[172:175],v[128:131]\n"
"s_waitcnt lgkmcnt(0)\n"
"ds_read_b128 v[24:27],v0 offset:32768\n"
"ds_read_b128 v[28:31],v1 offset:32768\n"
MF "v[128:131],v[16:19],v[192:195],v[128:131]\n"
"s_sub_u32 s61,s61,1\n"
"v_exp_f32 v96,v96\n"
MF "v[128:131],v[20:23],v[212:215],v[128:131]\n"
"s_mov_b32 s95,0\n"
"v_exp_f32 v97,v97\n"
MF "v[124:127],v[20:23],v[208:211],v[124:127]\n"
"v_exp_f32 v98,v98\n"
MF "v[124:127],v[16:19],v[188:191],v[124:127]\n"
"v_exp_f32 v99,v99\n"
MF "v[120:123],v[16:19],v[184:187],v[120:123]\n"
"v_add_f32 v58,v53,v96\n"
"v_add_f32 v58,v58,v97\n"
MF "v[120:123],v[20:23],v[204:207],v[120:123]\n"
CV "v96,v96,v97\n"
"v_add_f32 v58,v58,v98\n"
MF "v[116:119],v[20:23],v[200:203],v[116:119]\n"
"s_waitcnt vmcnt(6) lgkmcnt(0)\n"
"v_add_f32 v58,v58,v99\n"
CV "v97,v98,v99\n"
MF "v[116:119],v[16:19],v[180:183],v[116:119]\n"
"s_barrier\n"
MF "v[112:115],v[16:19],v[176:179],v[112:115]\n"
"v_exp_f32 v100,v100\n"
MF "v[112:115],v[20:23],v[196:199],v[112:115]\n"
"s_nop 0\n"
TR "v[16:17],v2 offset:49152\n"
TR "v[18:19],v2 offset:55296\n"
MF "v[112:115],v[24:27],v[216:219],v[112:115]\n"
"s_add_u32 s73,s73,12288\n"
TR "v[20:21],v3 offset:49152\n"
TR "v[22:23],v3 offset:55296\n"
MF "v[112:115],v[28:31],v[236:239],v[112:115]\n"
"s_cmp_eq_u32 s73,s19\n"
"s_cselect_b32 s73,s18,s73\n"
"s_add_u32 m0,s65,24576\n"
MF "v[116:119],v[28:31],v[240:243],v[116:119]\n"
LD "v8,s[36:39],s73 offen nt lds\n"
MF "v[116:119],v[24:27],v[220:223],v[116:119]\n"
"s_add_u32 m0,s65,25472\n"
"v_exp_f32 v101,v101\n"
MF "v[120:123],v[24:27],v[224:227],v[120:123]\n"
LD "v8,s[36:39],s73 offen offset:128 nt lds\n"
MF "v[120:123],v[28:31],v[244:247],v[120:123]\n"
"v_exp_f32 v102,v102\n"
MF "v[124:127],v[28:31],v[248:251],v[124:127]\n"
"s_add_u32 m0,s65,26368\n"
"v_exp_f32 v103,v103\n"
MF "v[124:127],v[24:27],v[228:231],v[124:127]\n"
LD "v8,s[36:39],s73 offen offset:256 nt lds\n"
MF "v[128:131],v[24:27],v[232:235],v[128:131]\n"
"v_add_f32 v59,v54,v100\n"
"v_add_f32 v59,v59,v101\n"
MF "v[128:131],v[28:31],v[252:255],v[128:131]\n"
"s_waitcnt lgkmcnt(0)\n"
TR "v[24:25],v4 offset:49152\n"
TR "v[26:27],v4 offset:55296\n"
MF "a[0:3],v[16:19],v[72:75],a[0:3]\n"
"s_add_u32 m0,s65,24576\n"
TR "v[28:29],v5 offset:49152\n"
TR "v[30:31],v5 offset:55296\n"
MF "a[20:23],v[20:23],v[72:75],a[20:23]\n"
LD "v8,s[36:39],s73 offen offset:3072 nt lds\n"
MF "a[24:27],v[20:23],v[76:79],a[24:27]\n"
CV "v100,v100,v101\n"
"v_add_f32 v59,v59,v102\n"
MF "a[4:7],v[16:19],v[76:79],a[4:7]\n"
"s_add_u32 m0,s65,25472\n"
"v_add_f32 v59,v59,v103\n"
CV "v101,v102,v103\n"
MF "a[8:11],v[16:19],v[80:83],a[8:11]\n"
LD "v8,s[36:39],s73 offen offset:3200 nt lds\n"
MF "a[28:31],v[20:23],v[80:83],a[28:31]\n"
"v_exp_f32 v104,v104\n"
MF "a[32:35],v[20:23],v[84:87],a[32:35]\n"
"s_add_u32 m0,s65,26368\n"
"v_exp_f32 v105,v105\n"
MF "a[12:15],v[16:19],v[84:87],a[12:15]\n"
LD "v8,s[36:39],s73 offen offset:3328 nt lds\n"
MF "a[16:19],v[16:19],v[88:91],a[16:19]\n"
"v_exp_f32 v106,v106\n"
MF "a[36:39],v[20:23],v[88:91],a[36:39]\n"
"s_waitcnt lgkmcnt(0)\n"
TR "v[16:17],v2 offset:50176\n"
TR "v[18:19],v2 offset:56320\n"
MF "a[56:59],v[24:27],v[88:91],a[56:59]\n"
"s_nop 0\n"
TR "v[20:21],v3 offset:50176\n"
TR "v[22:23],v3 offset:56320\n"
MF "a[76:79],v[28:31],v[88:91],a[76:79]\n"
"v_exp_f32 v107,v107\n"
MF "a[72:75],v[28:31],v[84:87],a[72:75]\n"
"v_add_f32 v60,v55,v104\n"
"v_add_f32 v60,v60,v105\n"
MF "a[52:55],v[24:27],v[84:87],a[52:55]\n"
CV "v104,v104,v105\n"
"v_add_f32 v60,v60,v106\n"
MF "a[48:51],v[24:27],v[80:83],a[48:51]\n"
"v_add_f32 v60,v60,v107\n"
CV "v105,v106,v107\n"
MF "a[68:71],v[28:31],v[80:83],a[68:71]\n"
"v_exp_f32 v108,v108\n"
MF "a[64:67],v[28:31],v[76:79],a[64:67]\n"
"v_exp_f32 v109,v109\n"
MF "a[44:47],v[24:27],v[76:79],a[44:47]\n"
"v_exp_f32 v110,v110\n"
MF "a[40:43],v[24:27],v[72:75],a[40:43]\n"
"v_exp_f32 v111,v111\n"
MF "a[60:63],v[28:31],v[72:75],a[60:63]\n"
"s_waitcnt lgkmcnt(0)\n"
TR "v[24:25],v4 offset:50176\n"
TR "v[26:27],v4 offset:56320\n"
MF "a[80:83],v[16:19],v[72:75],a[80:83]\n"
"s_nop 0\n"
TR "v[28:29],v5 offset:50176\n"
TR "v[30:31],v5 offset:56320\n"
MF "a[100:103],v[20:23],v[72:75],a[100:103]\n"
"v_add_f32 v61,v56,v108\n"
"v_add_f32 v61,v61,v109\n"
MF "a[104:107],v[20:23],v[76:79],a[104:107]\n"
CV "v108,v108,v109\n"
"v_add_f32 v61,v61,v110\n"
MF "a[84:87],v[16:19],v[76:79],a[84:87]\n"
"v_add_f32 v61,v61,v111\n"
CV "v109,v110,v111\n"
MF "a[88:91],v[16:19],v[80:83],a[88:91]\n"
"v_exp_f32 v112,v112\n"
MF "a[108:111],v[20:23],v[80:83],a[108:111]\n"
"v_exp_f32 v113,v113\n"
MF "a[112:115],v[20:23],v[84:87],a[112:115]\n"
"v_exp_f32 v114,v114\n"
MF "a[92:95],v[16:19],v[84:87],a[92:95]\n"
"v_exp_f32 v115,v115\n"
MF "a[96:99],v[16:19],v[88:91],a[96:99]\n"
"v_add_f32 v57,v57,v112\n"
"v_add_f32 v57,v57,v113\n"
MF "a[116:119],v[20:23],v[88:91],a[116:119]\n"
"s_waitcnt lgkmcnt(0)\n"
TR "v[16:17],v2 offset:51200\n"
TR "v[18:19],v2 offset:57344\n"
MF "a[136:139],v[24:27],v[88:91],a[136:139]\n"
"s_nop 0\n"
TR "v[20:21],v3 offset:51200\n"
TR "v[22:23],v3 offset:57344\n"
MF "a[156:159],v[28:31],v[88:91],a[156:159]\n"
CV "v94,v112,v113\n"
"v_add_f32 v57,v57,v114\n"
MF "a[152:155],v[28:31],v[84:87],a[152:155]\n"
"v_add_f32 v57,v57,v115\n"
CV "v95,v114,v115\n"
MF "a[132:135],v[24:27],v[84:87],a[132:135]\n"
"v_exp_f32 v116,v116\n"
MF "a[128:131],v[24:27],v[80:83],a[128:131]\n"
"v_exp_f32 v117,v117\n"
MF "a[148:151],v[28:31],v[80:83],a[148:151]\n"
"v_exp_f32 v118,v118\n"
MF "a[144:147],v[28:31],v[76:79],a[144:147]\n"
"v_exp_f32 v119,v119\n"
MF "a[124:127],v[24:27],v[76:79],a[124:127]\n"
"v_add_f32 v58,v58,v116\n"
"v_add_f32 v58,v58,v117\n"
MF "a[120:123],v[24:27],v[72:75],a[120:123]\n"
CV "v98,v116,v117\n"
"v_add_f32 v58,v58,v118\n"
MF "a[140:143],v[28:31],v[72:75],a[140:143]\n"
"s_waitcnt lgkmcnt(0)\n"
TR "v[24:25],v4 offset:51200\n"
TR "v[26:27],v4 offset:57344\n"
MF "a[160:163],v[16:19],v[72:75],a[160:163]\n"
"s_nop 0\n"
TR "v[28:29],v5 offset:51200\n"
TR "v[30:31],v5 offset:57344\n"
MF "a[180:183],v[20:23],v[72:75],a[180:183]\n"
"v_add_f32 v58,v58,v119\n"
CV "v99,v118,v119\n"
MF "a[184:187],v[20:23],v[76:79],a[184:187]\n"
"v_exp_f32 v120,v120\n"
MF "a[164:167],v[16:19],v[76:79],a[164:167]\n"
"v_exp_f32 v121,v121\n"
MF "a[168:171],v[16:19],v[80:83],a[168:171]\n"
"v_exp_f32 v122,v122\n"
MF "a[188:191],v[20:23],v[80:83],a[188:191]\n"
"v_exp_f32 v123,v123\n"
MF "a[192:195],v[20:23],v[84:87],a[192:195]\n"
"v_add_f32 v59,v59,v120\n"
"v_add_f32 v59,v59,v121\n"
MF "a[172:175],v[16:19],v[84:87],a[172:175]\n"
"s_waitcnt vmcnt(6) lgkmcnt(0)\n"
CV "v102,v120,v121\n"
"v_add_f32 v59,v59,v122\n"
MF "a[176:179],v[16:19],v[88:91],a[176:179]\n"
"s_barrier\n"
MF "a[196:199],v[20:23],v[88:91],a[196:199]\n"
"s_add_u32 s72,s72,6144\n"
"ds_read_b128 v[16:19],v0\n"
"ds_read_b128 v[20:23],v1\n"
MF "a[216:219],v[24:27],v[88:91],a[216:219]\n"
"s_cmp_eq_u32 s72,s17\n"
"s_cselect_b32 s72,s16,s72\n"
"s_add_u32 m0,s64,49152\n"
MF "a[236:239],v[28:31],v[88:91],a[236:239]\n"
LD "v7,s[32:35],s72 offen nt lds\n"
MF "a[232:235],v[28:31],v[84:87],a[232:235]\n"
"s_add_u32 m0,s64,50048\n"
"v_add_f32 v59,v59,v123\n"
CV "v103,v122,v123\n"
MF "a[212:215],v[24:27],v[84:87],a[212:215]\n"
LD "v7,s[32:35],s72 offen offset:128 nt lds\n"
MF "a[208:211],v[24:27],v[80:83],a[208:211]\n"
"s_add_u32 m0,s64,50944\n"
MF "a[228:231],v[28:31],v[80:83],a[228:231]\n"
LD "v7,s[32:35],s72 offen offset:256 nt lds\n"
MF "a[224:227],v[28:31],v[76:79],a[224:227]\n"
MF "a[204:207],v[24:27],v[76:79],a[204:207]\n"
"s_waitcnt lgkmcnt(0)\n"
MF "a[200:203],v[24:27],v[72:75],a[200:203]\n"
"s_cmp_lg_u32 s61,0\n"
MF "a[220:223],v[28:31],v[72:75],a[220:223]\n"
"s_cbranch_scc1 .Laloop\n"
".Lepi2:\n"
MF "v[72:75],v[16:19],v[136:139],v[32:35]\n"
"s_add_u32 s72,s72,6144\n"
"ds_read_b128 v[24:27],v0 offset:1024\n"
"ds_read_b128 v[28:31],v1 offset:1024\n"
MF "v[72:75],v[20:23],v[156:159],v[72:75]\n"
"s_add_u32 m0,s64,55296\n"
"v_exp_f32 v124,v124\n"
MF "v[76:79],v[20:23],v[160:163],v[36:39]\n"
LD "v7,s[32:35],s72 offen nt lds\n"
MF "v[76:79],v[16:19],v[140:143],v[76:79]\n"
"v_exp_f32 v125,v125\n"
MF "v[80:83],v[16:19],v[144:147],v[40:43]\n"
"s_add_u32 m0,s64,56192\n"
"v_exp_f32 v126,v126\n"
MF "v[80:83],v[20:23],v[164:167],v[80:83]\n"
LD "v7,s[32:35],s72 offen offset:128 nt lds\n"
MF "v[84:87],v[20:23],v[168:171],v[44:47]\n"
"v_exp_f32 v127,v127\n"
MF "v[84:87],v[16:19],v[148:151],v[84:87]\n"
"s_add_u32 m0,s64,57088\n"
"v_add_f32 v60,v60,v124\n"
"v_add_f32 v60,v60,v125\n"
MF "v[88:91],v[16:19],v[152:155],v[48:51]\n"
LD "v7,s[32:35],s72 offen offset:256 nt lds\n"
MF "v[88:91],v[20:23],v[172:175],v[88:91]\n"
"s_waitcnt lgkmcnt(0)\n"
"ds_read_b128 v[16:19],v0 offset:2048\n"
"ds_read_b128 v[20:23],v1 offset:2048\n"
MF "v[88:91],v[24:27],v[192:195],v[88:91]\n"
CV "v106,v124,v125\n"
"v_add_f32 v60,v60,v126\n"
MF "v[88:91],v[28:31],v[212:215],v[88:91]\n"
"v_add_f32 v60,v60,v127\n"
CV "v107,v126,v127\n"
MF "v[84:87],v[28:31],v[208:211],v[84:87]\n"
"v_exp_f32 v128,v128\n"
MF "v[84:87],v[24:27],v[188:191],v[84:87]\n"
"v_exp_f32 v129,v129\n"
MF "v[80:83],v[24:27],v[184:187],v[80:83]\n"
"v_exp_f32 v130,v130\n"
MF "v[80:83],v[28:31],v[204:207],v[80:83]\n"
"v_exp_f32 v131,v131\n"
MF "v[76:79],v[28:31],v[200:203],v[76:79]\n"
"v_add_f32 v61,v61,v128\n"
"v_add_f32 v61,v61,v129\n"
MF "v[76:79],v[24:27],v[180:183],v[76:79]\n"
CV "v110,v128,v129\n"
"v_add_f32 v61,v61,v130\n"
MF "v[72:75],v[24:27],v[176:179],v[72:75]\n"
"v_add_f32 v61,v61,v131\n"
CV "v111,v130,v131\n"
MF "v[72:75],v[28:31],v[196:199],v[72:75]\n"
"s_waitcnt lgkmcnt(0)\n"
"ds_read_b128 v[24:27],v0 offset:6144\n"
"ds_read_b128 v[28:31],v1 offset:6144\n"
MF "v[72:75],v[16:19],v[216:219],v[72:75]\n"
MF "v[72:75],v[20:23],v[236:239],v[72:75]\n"
MF "v[76:79],v[20:23],v[240:243],v[76:79]\n"
MF "v[76:79],v[16:19],v[220:223],v[76:79]\n"
MF "v[80:83],v[16:19],v[224:227],v[80:83]\n"
MF "v[80:83],v[20:23],v[244:247],v[80:83]\n"
MF "v[84:87],v[20:23],v[248:251],v[84:87]\n"
MF "v[84:87],v[16:19],v[228:231],v[84:87]\n"
"v_max3_f32 v10,v57,v58,v59\n"
"v_max3_f32 v10,v10,v60,v61\n"
MF "v[88:91],v[16:19],v[232:235],v[88:91]\n"
"v_cmp_le_f32 vcc,0x47800000,v10\n"
MF "v[88:91],v[20:23],v[252:255],v[88:91]\n"
"s_waitcnt lgkmcnt(0)\n"
"s_cbranch_vccnz .Lstub_26\n"
".Lback_26:\n"
MF "v[112:115],v[24:27],v[136:139],v[32:35]\n"
"s_add_u32 s74,s74,12288\n"
"ds_read_b128 v[16:19],v0 offset:7168\n"
"ds_read_b128 v[20:23],v1 offset:7168\n"
MF "v[112:115],v[28:31],v[156:159],v[112:115]\n"
"s_cmp_eq_u32 s74,s20\n"
"s_cselect_b32 s74,s70,s74\n"
MF "v[116:119],v[28:31],v[160:163],v[36:39]\n"
"v_exp_f32 v72,v72\n"
MF "v[116:119],v[24:27],v[140:143],v[116:119]\n"
"v_exp_f32 v73,v73\n"
MF "v[120:123],v[24:27],v[144:147],v[40:43]\n"
"v_exp_f32 v74,v74\n"
MF "v[120:123],v[28:31],v[164:167],v[120:123]\n"
"v_exp_f32 v75,v75\n"
MF "v[124:127],v[28:31],v[168:171],v[44:47]\n"
"v_add_f32 v52,v57,v72\n"
"v_add_f32 v52,v52,v73\n"
MF "v[124:127],v[24:27],v[148:151],v[124:127]\n"
CV "v72,v72,v73\n"
"v_add_f32 v52,v52,v74\n"
MF "v[128:131],v[24:27],v[152:155],v[48:51]\n"
"v_add_f32 v52,v52,v75\n"
CV "v73,v74,v75\n"
MF "v[128:131],v[28:31],v[172:175],v[128:131]\n"
"s_waitcnt lgkmcnt(0)\n"
"ds_read_b128 v[24:27],v0 offset:8192\n"
"ds_read_b128 v[28:31],v1 offset:8192\n"
MF "v[128:131],v[16:19],v[192:195],v[128:131]\n"
"v_exp_f32 v76,v76\n"
MF "v[128:131],v[20:23],v[212:215],v[128:131]\n"
"v_exp_f32 v77,v77\n"
MF "v[124:127],v[20:23],v[208:211],v[124:127]\n"
"v_exp_f32 v78,v78\n"
MF "v[124:127],v[16:19],v[188:191],v[124:127]\n"
"v_exp_f32 v79,v79\n"
MF "v[120:123],v[16:19],v[184:187],v[120:123]\n"
"v_add_f32 v53,v58,v76\n"
"v_add_f32 v53,v53,v77\n"
MF "v[120:123],v[20:23],v[204:207],v[120:123]\n"
CV "v76,v76,v77\n"
"v_add_f32 v53,v53,v78\n"
MF "v[116:119],v[20:23],v[200:203],v[116:119]\n"
"s_waitcnt vmcnt(6) lgkmcnt(0)\n"
"v_add_f32 v53,v53,v79\n"
CV "v77,v78,v79\n"
MF "v[116:119],v[16:19],v[180:183],v[116:119]\n"
"s_barrier\n"
MF "v[112:115],v[16:19],v[176:179],v[112:115]\n"
"v_lshrrev_b32 v7,7,v9\n"
"v_mad_u32_u24 v7,v7,s6,v8\n"
MF "v[112:115],v[20:23],v[196:199],v[112:115]\n"
"s_nop 0\n"
TR "v[16:17],v2 offset:24576\n"
TR "v[18:19],v2 offset:30720\n"
MF "v[112:115],v[24:27],v[216:219],v[112:115]\n"
"s_add_u32 s73,s73,12288\n"
TR "v[20:21],v3 offset:24576\n"
TR "v[22:23],v3 offset:30720\n"
MF "v[112:115],v[28:31],v[236:239],v[112:115]\n"
"s_cmp_eq_u32 s73,s19\n"
"s_cselect_b32 s73,s18,s73\n"
"s_add_u32 m0,s65,0\n"
MF "v[116:119],v[28:31],v[240:243],v[116:119]\n"
LD "v8,s[36:39],s73 offen nt lds\n"
MF "v[116:119],v[24:27],v[220:223],v[116:119]\n"
"s_add_u32 m0,s65,896\n"
"v_exp_f32 v80,v80\n"
MF "v[120:123],v[24:27],v[224:227],v[120:123]\n"
LD "v8,s[36:39],s73 offen offset:128 nt lds\n"
MF "v[120:123],v[28:31],v[244:247],v[120:123]\n"
"v_exp_f32 v81,v81\n"
MF "v[124:127],v[28:31],v[248:251],v[124:127]\n"
"s_add_u32 m0,s65,1792\n"
"v_exp_f32 v82,v82\n"
MF "v[124:127],v[24:27],v[228:231],v[124:127]\n"
LD "v8,s[36:39],s73 offen offset:256 nt lds\n"
MF "v[128:131],v[24:27],v[232:235],v[128:131]\n"
"v_exp_f32 v83,v83\n"
MF "v[128:131],v[28:31],v[252:255],v[128:131]\n"
"s_waitcnt lgkmcnt(0)\n"
TR "v[24:25],v4 offset:24576\n"
TR "v[26:27],v4 offset:30720\n"
MF "a[0:3],v[16:19],v[92:95],a[0:3]\n"
"s_add_u32 m0,s65,0\n"
TR "v[28:29],v5 offset:24576\n"
TR "v[30:31],v5 offset:30720\n"
MF "a[20:23],v[20:23],v[92:95],a[20:23]\n"
LD "v8,s[36:39],s73 offen offset:3072 nt lds\n"
MF "a[24:27],v[20:23],v[96:99],a[24:27]\n"
"v_add_f32 v54,v59,v80\n"
"v_add_f32 v54,v54,v81\n"
MF "a[4:7],v[16:19],v[96:99],a[4:7]\n"
"s_add_u32 m0,s65,896\n"
CV "v80,v80,v81\n"
"v_add_f32 v54,v54,v82\n"
MF "a[8:11],v[16:19],v[100:103],a[8:11]\n"
LD "v8,s[36:39],s73 offen offset:3200 nt lds\n"
MF "a[28:31],v[20:23],v[100:103],a[28:31]\n"
"v_add_f32 v54,v54,v83\n"
CV "v81,v82,v83\n"
MF "a[32:35],v[20:23],v[104:107],a[32:35]\n"
"s_add_u32 m0,s65,1792\n"
"v_exp_f32 v84,v84\n"
MF "a[12:15],v[16:19],v[104:107],a[12:15]\n"
LD "v8,s[36:39],s73 offen offset:3328 nt lds\n"
MF "a[16:19],v[16:19],v[108:111],a[16:19]\n"
"v_exp_f32 v85,v85\n"
MF "a[36:39],v[20:23],v[108:111],a[36:39]\n"
"s_waitcnt lgkmcnt(0)\n"
TR "v[16:17],v2 offset:25600\n"
TR "v[18:19],v2 offset:31744\n"
MF "a[56:59],v[24:27],v[108:111],a[56:59]\n"
"s_nop 0\n"
TR "v[20:21],v3 offset:25600\n"
TR "v[22:23],v3 offset:31744\n"
MF "a[76:79],v[28:31],v[108:111],a[76:79]\n"
"v_exp_f32 v86,v86\n"
MF "a[72:75],v[28:31],v[104:107],a[72:75]\n"
"v_exp_f32 v87,v87\n"
MF "a[52:55],v[24:27],v[104:107],a[52:55]\n"
"v_add_f32 v55,v60,v84\n"
"v_add_f32 v55,v55,v85\n"
MF "a[48:51],v[24:27],v[100:103],a[48:51]\n"
CV "v84,v84,v85\n"
"v_add_f32 v55,v55,v86\n"
MF "a[68:71],v[28:31],v[100:103],a[68:71]\n"
"v_add_f32 v55,v55,v87\n"
CV "v85,v86,v87\n"
MF "a[64:67],v[28:31],v[96:99],a[64:67]\n"
"v_exp_f32 v88,v88\n"
MF "a[44:47],v[24:27],v[96:99],a[44:47]\n"
"v_exp_f32 v89,v89\n"
MF "a[40:43],v[24:27],v[92:95],a[40:43]\n"
"v_exp_f32 v90,v90\n"
MF "a[60:63],v[28:31],v[92:95],a[60:63]\n"
"s_waitcnt lgkmcnt(0)\n"
TR "v[24:25],v4 offset:25600\n"
TR "v[26:27],v4 offset:31744\n"
MF "a[80:83],v[16:19],v[92:95],a[80:83]\n"
"s_nop 0\n"
TR "v[28:29],v5 offset:25600\n"
TR "v[30:31],v5 offset:31744\n"
MF "a[100:103],v[20:23],v[92:95],a[100:103]\n"
"v_exp_f32 v91,v91\n"
MF "a[104:107],v[20:23],v[96:99],a[104:107]\n"
"v_add_f32 v56,v61,v88\n"
"v_add_f32 v56,v56,v89\n"
MF "a[84:87],v[16:19],v[96:99],a[84:87]\n"
CV "v88,v88,v89\n"
"v_add_f32 v56,v56,v90\n"
MF "a[88:91],v[16:19],v[100:103],a[88:91]\n"
"v_add_f32 v56,v56,v91\n"
CV "v89,v90,v91\n"
MF "a[108:111],v[20:23],v[100:103],a[108:111]\n"
"v_exp_f32 v112,v112\n"
MF "a[112:115],v[20:23],v[104:107],a[112:115]\n"
"v_exp_f32 v113,v113\n"
MF "a[92:95],v[16:19],v[104:107],a[92:95]\n"
"v_exp_f32 v114,v114\n"
MF "a[96:99],v[16:19],v[108:111],a[96:99]\n"
"v_exp_f32 v115,v115\n"
MF "a[116:119],v[20:23],v[108:111],a[116:119]\n"
"s_waitcnt lgkmcnt(0)\n"
TR "v[16:17],v2 offset:26624\n"
TR "v[18:19],v2 offset:32768\n"
MF "a[136:139],v[24:27],v[108:111],a[136:139]\n"
"s_nop 0\n"
TR "v[20:21],v3 offset:26624\n"
TR "v[22:23],v3 offset:32768\n"
MF "a[156:159],v[28:31],v[108:111],a[156:159]\n"
"v_add_f32 v52,v52,v112\n"
"v_add_f32 v52,v52,v113\n"
MF "a[152:155],v[28:31],v[104:107],a[152:155]\n"
CV "v74,v112,v113\n"
"v_add_f32 v52,v52,v114\n"
MF "a[132:135],v[24:27],v[104:107],a[132:135]\n"
"v_add_f32 v52,v52,v115\n"
CV "v75,v114,v115\n"
MF "a[128:131],v[24:27],v[100:103],a[128:131]\n"
"v_exp_f32 v116,v116\n"
MF "a[148:151],v[28:31],v[100:103],a[148:151]\n"
"v_exp_f32 v117,v117\n"
MF "a[144:147],v[28:31],v[96:99],a[144:147]\n"
"v_exp_f32 v118,v118\n"
MF "a[124:127],v[24:27],v[96:99],a[124:127]\n"
"v_exp_f32 v119,v119\n"
MF "a[120:123],v[24:27],v[92:95],a[120:123]\n"
"v_add_f32 v53,v53,v116\n"
"v_add_f32 v53,v53,v117\n"
MF "a[140:143],v[28:31],v[92:95],a[140:143]\n"
"s_waitcnt lgkmcnt(0)\n"
TR "v[24:25],v4 offset:26624\n"
TR "v[26:27],v4 offset:32768\n"
MF "a[160:163],v[16:19],v[92:95],a[160:163]\n"
"s_nop 0\n"
TR "v[28:29],v5 offset:26624\n"
TR "v[30:31],v5 offset:32768\n"
MF "a[180:183],v[20:23],v[92:95],a[180:183]\n"
CV "v78,v116,v117\n"
"v_add_f32 v53,v53,v118\n"
MF "a[184:187],v[20:23],v[96:99],a[184:187]\n"
"v_add_f32 v53,v53,v119\n"
CV "v79,v118,v119\n"
MF "a[164:167],v[16:19],v[96:99],a[164:167]\n"
"v_exp_f32 v120,v120\n"
MF "a[168:171],v[16:19],v[100:103],a[168:171]\n"
"v_exp_f32 v121,v121\n"
MF "a[188:191],v[20:23],v[100:103],a[188:191]\n"
"v_exp_f32 v122,v122\n"
MF "a[192:195],v[20:23],v[104:107],a[192:195]\n"
"v_exp_f32 v123,v123\n"
MF "a[172:175],v[16:19],v[104:107],a[172:175]\n"
"s_waitcnt vmcnt(6) lgkmcnt(0)\n"
"v_add_f32 v54,v54,v120\n"
"v_add_f32 v54,v54,v121\n"
MF "a[176:179],v[16:19],v[108:111],a[176:179]\n"
"s_barrier\n"
MF "a[196:199],v[20:23],v[108:111],a[196:199]\n"
"s_add_u32 s60,s0,0\n"
"ds_read_b128 v[16:19],v0 offset:49152\n"
"ds_read_b128 v[20:23],v1 offset:49152\n"
MF "a[216:219],v[24:27],v[108:111],a[216:219]\n"
"s_add_u32 m0,s5,24576\n"
CV "v82,v120,v121\n"
"v_add_f32 v54,v54,v122\n"
MF "a[236:239],v[28:31],v[108:111],a[236:239]\n"
LD "v7,s[48:51],s60 offen lds\n"
MF "a[232:235],v[28:31],v[104:107],a[232:235]\n"
"s_add_u32 s60,s0,10240\n"
"s_add_u32 m0,s5,25600\n"
"v_add_f32 v54,v54,v123\n"
MF "a[212:215],v[24:27],v[104:107],a[212:215]\n"
LD "v7,s[48:51],s60 offen lds\n"
MF "a[208:211],v[24:27],v[100:103],a[208:211]\n"
"s_add_u32 s60,s0,20480\n"
"s_add_u32 m0,s5,26624\n"
CV "v83,v122,v123\n"
MF "a[228:231],v[28:31],v[100:103],a[228:231]\n"
LD "v7,s[48:51],s60 offen lds\n"
MF "a[224:227],v[28:31],v[96:99],a[224:227]\n"
MF "a[204:207],v[24:27],v[96:99],a[204:207]\n"
MF "a[200:203],v[24:27],v[92:95],a[200:203]\n"
MF "a[220:223],v[28:31],v[92:95],a[220:223]\n"
"s_waitcnt lgkmcnt(0)\n"
MF "v[92:95],v[16:19],v[136:139],v[32:35]\n"
"s_add_u32 s60,s0,30720\n"
"ds_read_b128 v[24:27],v0 offset:50176\n"
"ds_read_b128 v[28:31],v1 offset:50176\n"
MF "v[92:95],v[20:23],v[156:159],v[92:95]\n"
"s_add_u32 m0,s5,27648\n"
"v_exp_f32 v124,v124\n"
MF "v[96:99],v[20:23],v[160:163],v[36:39]\n"
LD "v7,s[48:51],s60 offen lds\n"
MF "v[96:99],v[16:19],v[140:143],v[96:99]\n"
"s_add_u32 s60,s0,40960\n"
"v_exp_f32 v125,v125\n"
MF "v[100:103],v[16:19],v[144:147],v[40:43]\n"
"s_add_u32 m0,s5,28672\n"
"v_exp_f32 v126,v126\n"
MF "v[100:103],v[20:23],v[164:167],v[100:103]\n"
LD "v7,s[48:51],s60 offen lds\n"
MF "v[104:107],v[20:23],v[168:171],v[44:47]\n"
"s_add_u32 s60,s0,51200\n"
"v_exp_f32 v127,v127\n"
MF "v[104:107],v[16:19],v[148:151],v[104:107]\n"
"s_add_u32 m0,s5,29696\n"
"v_add_f32 v55,v55,v124\n"
"v_add_f32 v55,v55,v125\n"
MF "v[108:111],v[16:19],v[152:155],v[48:51]\n"
LD "v7,s[48:51],s60 offen lds\n"
MF "v[108:111],v[20:23],v[172:175],v[108:111]\n"
"s_waitcnt lgkmcnt(0)\n"
"ds_read_b128 v[16:19],v0 offset:51200\n"
"ds_read_b128 v[20:23],v1 offset:51200\n"
MF "v[108:111],v[24:27],v[192:195],v[108:111]\n"
CV "v86,v124,v125\n"
"v_add_f32 v55,v55,v126\n"
MF "v[108:111],v[28:31],v[212:215],v[108:111]\n"
"v_add_f32 v55,v55,v127\n"
CV "v87,v126,v127\n"
MF "v[104:107],v[28:31],v[208:211],v[104:107]\n"
"v_exp_f32 v128,v128\n"
MF "v[104:107],v[24:27],v[188:191],v[104:107]\n"
"v_exp_f32 v129,v129\n"
MF "v[100:103],v[24:27],v[184:187],v[100:103]\n"
"v_exp_f32 v130,v130\n"
MF "v[100:103],v[28:31],v[204:207],v[100:103]\n"
"v_exp_f32 v131,v131\n"
MF "v[96:99],v[28:31],v[200:203],v[96:99]\n"
"v_add_f32 v56,v56,v128\n"
"v_add_f32 v56,v56,v129\n"
MF "v[96:99],v[24:27],v[180:183],v[96:99]\n"
CV "v90,v128,v129\n"
"v_add_f32 v56,v56,v130\n"
MF "v[92:95],v[24:27],v[176:179],v[92:95]\n"
"v_add_f32 v56,v56,v131\n"
CV "v91,v130,v131\n"
MF "v[92:95],v[28:31],v[196:199],v[92:95]\n"
"s_waitcnt lgkmcnt(0)\n"
"ds_read_b128 v[24:27],v0 offset:55296\n"
"ds_read_b128 v[28:31],v1 offset:55296\n"
MF "v[92:95],v[16:19],v[216:219],v[92:95]\n"
MF "v[92:95],v[20:23],v[236:239],v[92:95]\n"
MF "v[96:99],v[20:23],v[240:243],v[96:99]\n"
MF "v[96:99],v[16:19],v[220:223],v[96:99]\n"
MF "v[100:103],v[16:19],v[224:227],v[100:103]\n"
MF "v[100:103],v[20:23],v[244:247],v[100:103]\n"
MF "v[104:107],v[20:23],v[248:251],v[104:107]\n"
MF "v[104:107],v[16:19],v[228:231],v[104:107]\n"
"v_max3_f32 v10,v52,v53,v54\n"
"v_max3_f32 v10,v10,v55,v56\n"
MF "v[108:111],v[16:19],v[232:235],v[108:111]\n"
"v_cmp_le_f32 vcc,0x47800000,v10\n"
MF "v[108:111],v[20:23],v[252:255],v[108:111]\n"
"s_waitcnt lgkmcnt(0)\n"
"s_cbranch_vccnz .Lstub_27\n"
".Lback_27:\n"
MF "v[112:115],v[24:27],v[136:139],v[32:35]\n"
"s_add_u32 s74,s74,12288\n"
"ds_read_b128 v[16:19],v0 offset:56320\n"
"ds_read_b128 v[20:23],v1 offset:56320\n"
MF "v[112:115],v[28:31],v[156:159],v[112:115]\n"
"s_cmp_eq_u32 s74,s20\n"
"s_cselect_b32 s74,s70,s74\n"
MF "v[116:119],v[28:31],v[160:163],v[36:39]\n"
"v_exp_f32 v92,v92\n"
MF "v[116:119],v[24:27],v[140:143],v[116:119]\n"
"v_exp_f32 v93,v93\n"
MF "v[120:123],v[24:27],v[144:147],v[40:43]\n"
"v_exp_f32 v94,v94\n"
MF "v[120:123],v[28:31],v[164:167],v[120:123]\n"
"v_exp_f32 v95,v95\n"
MF "v[124:127],v[28:31],v[168:171],v[44:47]\n"
"v_add_f32 v57,v52,v92\n"
"v_add_f32 v57,v57,v93\n"
MF "v[124:127],v[24:27],v[148:151],v[124:127]\n"
CV "v92,v92,v93\n"
"v_add_f32 v57,v57,v94\n"
MF "v[128:131],v[24:27],v[152:155],v[48:51]\n"
"v_add_f32 v57,v57,v95\n"
CV "v93,v94,v95\n"
MF "v[128:131],v[28:31],v[172:175],v[128:131]\n"
"s_waitcnt lgkmcnt(0)\n"
"ds_read_b128 v[24:27],v0 offset:57344\n"
"ds_read_b128 v[28:31],v1 offset:57344\n"
MF "v[128:131],v[16:19],v[192:195],v[128:131]\n"
"v_exp_f32 v96,v96\n"
MF "v[128:131],v[20:23],v[212:215],v[128:131]\n"
"v_exp_f32 v97,v97\n"
MF "v[124:127],v[20:23],v[208:211],v[124:127]\n"
"v_exp_f32 v98,v98\n"
MF "v[124:127],v[16:19],v[188:191],v[124:127]\n"
"v_exp_f32 v99,v99\n"
MF "v[120:123],v[16:19],v[184:187],v[120:123]\n"
"v_add_f32 v58,v53,v96\n"
"v_add_f32 v58,v58,v97\n"
MF "v[120:123],v[20:23],v[204:207],v[120:123]\n"
CV "v96,v96,v97\n"
"v_add_f32 v58,v58,v98\n"
MF "v[116:119],v[20:23],v[200:203],v[116:119]\n"
"s_waitcnt vmcnt(6) lgkmcnt(0)\n"
"v_add_f32 v58,v58,v99\n"
CV "v97,v98,v99\n"
MF "v[116:119],v[16:19],v[180:183],v[116:119]\n"
"s_barrier\n"
MF "v[112:115],v[16:19],v[176:179],v[112:115]\n"
"v_exp_f32 v100,v100\n"
MF "v[112:115],v[20:23],v[196:199],v[112:115]\n"
"s_nop 0\n"
TR "v[16:17],v2\n"
TR "v[18:19],v2 offset:6144\n"
MF "v[112:115],v[24:27],v[216:219],v[112:115]\n"
"s_add_u32 s73,s73,12288\n"
TR "v[20:21],v3\n"
TR "v[22:23],v3 offset:6144\n"
MF "v[112:115],v[28:31],v[236:239],v[112:115]\n"
"s_cmp_eq_u32 s73,s19\n"
"s_cselect_b32 s73,s18,s73\n"
"s_add_u32 m0,s65,49152\n"
MF "v[116:119],v[28:31],v[240:243],v[116:119]\n"
LD "v8,s[36:39],s73 offen nt lds\n"
MF "v[116:119],v[24:27],v[220:223],v[116:119]\n"
"s_add_u32 m0,s65,50048\n"
"v_exp_f32 v101,v101\n"
MF "v[120:123],v[24:27],v[224:227],v[120:123]\n"
LD "v8,s[36:39],s73 offen offset:128 nt lds\n"
MF "v[120:123],v[28:31],v[244:247],v[120:123]\n"
"v_exp_f32 v102,v102\n"
MF "v[124:127],v[28:31],v[248:251],v[124:127]\n"
"s_add_u32 m0,s65,50944\n"
"v_exp_f32 v103,v103\n"
MF "v[124:127],v[24:27],v[228:231],v[124:127]\n"
LD "v8,s[36:39],s73 offen offset:256 nt lds\n"
MF "v[128:131],v[24:27],v[232:235],v[128:131]\n"
"v_add_f32 v59,v54,v100\n"
"v_add_f32 v59,v59,v101\n"
MF "v[128:131],v[28:31],v[252:255],v[128:131]\n"
"s_waitcnt lgkmcnt(0)\n"
TR "v[24:25],v4\n"
TR "v[26:27],v4 offset:6144\n"
MF "a[0:3],v[16:19],v[72:75],a[0:3]\n"
"s_add_u32 m0,s65,49152\n"
TR "v[28:29],v5\n"
TR "v[30:31],v5 offset:6144\n"
MF "a[20:23],v[20:23],v[72:75],a[20:23]\n"
LD "v8,s[36:39],s73 offen offset:3072 nt lds\n"
MF "a[24:27],v[20:23],v[76:79],a[24:27]\n"
CV "v100,v100,v101\n"
"v_add_f32 v59,v59,v102\n"
MF "a[4:7],v[16:19],v[76:79],a[4:7]\n"
"s_add_u32 m0,s65,50048\n"
"v_add_f32 v59,v59,v103\n"
CV "v101,v102,v103\n"
MF "a[8:11],v[16:19],v[80:83],a[8:11]\n"
LD "v8,s[36:39],s73 offen offset:3200 nt lds\n"
MF "a[28:31],v[20:23],v[80:83],a[28:31]\n"
"v_exp_f32 v104,v104\n"
MF "a[32:35],v[20:23],v[84:87],a[32:35]\n"
"s_add_u32 m0,s65,50944\n"
"v_exp_f32 v105,v105\n"
MF "a[12:15],v[16:19],v[84:87],a[12:15]\n"
LD "v8,s[36:39],s73 offen offset:3328 nt lds\n"
MF "a[16:19],v[16:19],v[88:91],a[16:19]\n"
"v_exp_f32 v106,v106\n"
MF "a[36:39],v[20:23],v[88:91],a[36:39]\n"
"s_waitcnt lgkmcnt(0)\n"
TR "v[16:17],v2 offset:1024\n"
TR "v[18:19],v2 offset:7168\n"
MF "a[56:59],v[24:27],v[88:91],a[56:59]\n"
"s_nop 0\n"
TR "v[20:21],v3 offset:1024\n"
TR "v[22:23],v3 offset:7168\n"
MF "a[76:79],v[28:31],v[88:91],a[76:79]\n"
"v_exp_f32 v107,v107\n"
MF "a[72:75],v[28:31],v[84:87],a[72:75]\n"
"v_add_f32 v60,v55,v104\n"
"v_add_f32 v60,v60,v105\n"
MF "a[52:55],v[24:27],v[84:87],a[52:55]\n"
CV "v104,v104,v105\n"
"v_add_f32 v60,v60,v106\n"
MF "a[48:51],v[24:27],v[80:83],a[48:51]\n"
"v_add_f32 v60,v60,v107\n"
CV "v105,v106,v107\n"
MF "a[68:71],v[28:31],v[80:83],a[68:71]\n"
"v_exp_f32 v108,v108\n"
MF "a[64:67],v[28:31],v[76:79],a[64:67]\n"
"v_exp_f32 v109,v109\n"
MF "a[44:47],v[24:27],v[76:79],a[44:47]\n"
"v_exp_f32 v110,v110\n"
MF "a[40:43],v[24:27],v[72:75],a[40:43]\n"
"v_exp_f32 v111,v111\n"
MF "a[60:63],v[28:31],v[72:75],a[60:63]\n"
"s_waitcnt lgkmcnt(0)\n"
TR "v[24:25],v4 offset:1024\n"
TR "v[26:27],v4 offset:7168\n"
MF "a[80:83],v[16:19],v[72:75],a[80:83]\n"
"s_nop 0\n"
TR "v[28:29],v5 offset:1024\n"
TR "v[30:31],v5 offset:7168\n"
MF "a[100:103],v[20:23],v[72:75],a[100:103]\n"
"v_add_f32 v61,v56,v108\n"
"v_add_f32 v61,v61,v109\n"
MF "a[104:107],v[20:23],v[76:79],a[104:107]\n"
CV "v108,v108,v109\n"
"v_add_f32 v61,v61,v110\n"
MF "a[84:87],v[16:19],v[76:79],a[84:87]\n"
"v_add_f32 v61,v61,v111\n"
CV "v109,v110,v111\n"
MF "a[88:91],v[16:19],v[80:83],a[88:91]\n"
"v_exp_f32 v112,v112\n"
MF "a[108:111],v[20:23],v[80:83],a[108:111]\n"
"v_exp_f32 v113,v113\n"
MF "a[112:115],v[20:23],v[84:87],a[112:115]\n"
"v_exp_f32 v114,v114\n"
MF "a[92:95],v[16:19],v[84:87],a[92:95]\n"
"v_exp_f32 v115,v115\n"
MF "a[96:99],v[16:19],v[88:91],a[96:99]\n"
"v_add_f32 v57,v57,v112\n"
"v_add_f32 v57,v57,v113\n"
MF "a[116:119],v[20:23],v[88:91],a[116:119]\n"
"s_waitcnt lgkmcnt(0)\n"
TR "v[16:17],v2 offset:2048\n"
TR "v[18:19],v2 offset:8192\n"
MF "a[136:139],v[24:27],v[88:91],a[136:139]\n"
"s_nop 0\n"
TR "v[20:21],v3 offset:2048\n"
TR "v[22:23],v3 offset:8192\n"
MF "a[156:159],v[28:31],v[88:91],a[156:159]\n"
CV "v94,v112,v113\n"
"v_add_f32 v57,v57,v114\n"
MF "a[152:155],v[28:31],v[84:87],a[152:155]\n"
"v_add_f32 v57,v57,v115\n"
CV "v95,v114,v115\n"
MF "a[132:135],v[24:27],v[84:87],a[132:135]\n"
"v_exp_f32 v116,v116\n"
MF "a[128:131],v[24:27],v[80:83],a[128:131]\n"
"v_exp_f32 v117,v117\n"
MF "a[148:151],v[28:31],v[80:83],a[148:151]\n"
"v_exp_f32 v118,v118\n"
MF "a[144:147],v[28:31],v[76:79],a[144:147]\n"
"v_exp_f32 v119,v119\n"
MF "a[124:127],v[24:27],v[76:79],a[124:127]\n"
"v_add_f32 v58,v58,v116\n"
"v_add_f32 v58,v58,v117\n"
MF "a[120:123],v[24:27],v[72:75],a[120:123]\n"
CV "v98,v116,v117\n"
"v_add_f32 v58,v58,v118\n"
MF "a[140:143],v[28:31],v[72:75],a[140:143]\n"
"s_waitcnt lgkmcnt(0)\n"
TR "v[24:25],v4 offset:2048\n"
TR "v[26:27],v4 offset:8192\n"
MF "a[160:163],v[16:19],v[72:75],a[160:163]\n"
"s_nop 0\n"
TR "v[28:29],v5 offset:2048\n"
TR "v[30:31],v5 offset:8192\n"
MF "a[180:183],v[20:23],v[72:75],a[180:183]\n"
"v_add_f32 v58,v58,v119\n"
CV "v99,v118,v119\n"
MF "a[184:187],v[20:23],v[76:79],a[184:187]\n"
".Lpg23h:\n"
"s_cbranch_execz .Lpg27h\n"
"v_exp_f32 v120,v120\n"
MF "a[164:167],v[16:19],v[76:79],a[164:167]\n"
"v_exp_f32 v121,v121\n"
MF "a[168:171],v[16:19],v[80:83],a[168:171]\n"
"v_exp_f32 v122,v122\n"
MF "a[188:191],v[20:23],v[80:83],a[188:191]\n"
"v_exp_f32 v123,v123\n"
MF "a[192:195],v[20:23],v[84:87],a[192:195]\n"
"v_add_f32 v59,v59,v120\n"
"v_add_f32 v59,v59,v121\n"
MF "a[172:175],v[16:19],v[84:87],a[172:175]\n"
"s_waitcnt lgkmcnt(0)\n"
CV "v102,v120,v121\n"
"v_add_f32 v59,v59,v122\n"
MF "a[176:179],v[16:19],v[88:91],a[176:179]\n"
"s_barrier\n"
MF "a[196:199],v[20:23],v[88:91],a[196:199]\n"
"s_add_u32 s60,s1,0\n"
"s_add_u32 m0,s5,0\n"
"v_add_f32 v59,v59,v123\n"
MF "a[216:219],v[24:27],v[88:91],a[216:219]\n"
LD "v7,s[48:51],s60 offen lds\n"
MF "a[236:239],v[28:31],v[88:91],a[236:239]\n"
"s_add_u32 s60,s1,10240\n"
"s_add_u32 m0,s5,1024\n"
CV "v103,v122,v123\n"
MF "a[232:235],v[28:31],v[84:87],a[232:235]\n"
LD "v7,s[48:51],s60 offen lds\n"
MF "a[212:215],v[24:27],v[84:87],a[212:215]\n"
"s_add_u32 s60,s1,20480\n"
"s_add_u32 m0,s5,2048\n"
MF "a[208:211],v[24:27],v[80:83],a[208:211]\n"
LD "v7,s[48:51],s60 offen lds\n"
MF "a[228:231],v[28:31],v[80:83],a[228:231]\n"
"v_exp_f32 v124,v124\n"
MF "a[224:227],v[28:31],v[76:79],a[224:227]\n"
"v_exp_f32 v125,v125\n"
MF "a[204:207],v[24:27],v[76:79],a[204:207]\n"
"v_exp_f32 v126,v126\n"
MF "a[200:203],v[24:27],v[72:75],a[200:203]\n"
"v_exp_f32 v127,v127\n"
MF "a[220:223],v[28:31],v[72:75],a[220:223]\n"
"v_add_f32 v60,v60,v124\n"
"v_add_f32 v60,v60,v125\n"
CV "v106,v124,v125\n"
"v_add_f32 v60,v60,v126\n"
"v_add_f32 v60,v60,v127\n"
CV "v107,v126,v127\n"
"v_exp_f32 v128,v128\n"
"v_exp_f32 v129,v129\n"
"v_exp_f32 v130,v130\n"
"v_exp_f32 v131,v131\n"
"v_add_f32 v61,v61,v128\n"
"v_add_f32 v61,v61,v129\n"
CV "v110,v128,v129\n"
"v_add_f32 v61,v61,v130\n"
"v_add_f32 v61,v61,v131\n"
CV "v111,v130,v131\n"
"v_max3_f32 v10,v57,v58,v59\n"
"v_max3_f32 v10,v10,v60,v61\n"
"v_cmp_le_f32 vcc,0x47800000,v10\n"
"s_cbranch_vccnz .Lstub_28\n"
".Lback_28:\n"
"s_waitcnt vmcnt(3) lgkmcnt(0)\n"
"s_barrier\n"
"v_add_u32 v236,s9,v9\n"
"v_add_u32 v237,65536,v236\n"
"v_mov_b32 v10,1.0\n"
TR "v[160:161],v4 offset:50176\n"
TR "v[162:163],v4 offset:56320\n"
"v_mfma_f32_16x16x4_f32 v[16:19],v10,v57,0\n"
TR "v[164:165],v5 offset:50176\n"
TR "v[166:167],v5 offset:56320\n"
TR "v[168:169],v2 offset:51200\n"
TR "v[170:171],v2 offset:57344\n"
TR "v[172:173],v3 offset:51200\n"
TR "v[174:175],v3 offset:57344\n"
"v_mfma_f32_16x16x4_f32 v[20:23],v10,v58,0\n"
TR "v[176:177],v4 offset:51200\n"
TR "v[178:179],v4 offset:57344\n"
TR "v[180:181],v5 offset:51200\n"
TR "v[182:183],v5 offset:57344\n"
TR "v[136:137],v2 offset:49152\n"
TR "v[138:139],v2 offset:55296\n"
"v_mfma_f32_16x16x4_f32 v[24:27],v10,v59,0\n"
TR "v[140:141],v3 offset:49152\n"
TR "v[142:143],v3 offset:55296\n"
TR "v[144:145],v4 offset:49152\n"
TR "v[146:147],v4 offset:55296\n"
TR "v[148:149],v5 offset:49152\n"
TR "v[150:151],v5 offset:55296\n"
"v_mfma_f32_16x16x4_f32 v[28:31],v10,v60,0\n"
TR "v[152:153],v2 offset:50176\n"
TR "v[154:155],v2 offset:56320\n"
TR "v[156:157],v3 offset:50176\n"
TR "v[158:159],v3 offset:56320\n"
"v_mfma_f32_16x16x4_f32 v[32:35],v10,v61,0\n"
"s_add_u32 m0,s5,0\n"
"v_add_u32 v195,27648,v7\n"
LD "v195,s[48:51],s1 offen offset:3072 lds\n"
"s_add_u32 m0,s5,4096\n"
"v_add_u32 v196,40960,v7\n"
LD "v196,s[48:51],s1 offen lds\n"
"s_waitcnt lgkmcnt(15)\n"
MF "a[120:123],v[160:163],v[92:95],a[120:123]\n"
MF "a[140:143],v[164:167],v[92:95],a[140:143]\n"
"v_add_u32 v197,50176,v7\n"
LD "v197,s[48:51],s1 offen offset:1024 lds\n"
"v_rcp_f32 v208,v16\n"
"v_rcp_f32 v209,v20\n"
"v_rcp_f32 v210,v24\n"
"v_rcp_f32 v211,v28\n"
"v_rcp_f32 v212,v32\n"
MF "a[124:127],v[160:163],v[96:99],a[124:127]\n"
"v_mul_f32_e64 v214,-s13,v208\n"
"v_mul_f32_e64 v215,-s13,v209\n"
"v_mul_f32_e64 v216,-s13,v210\n"
"v_mul_f32_e64 v217,-s13,v211\n"
"v_mul_f32_e64 v218,-s13,v212\n"
AR "v72,a120\n"
AR "v73,a121\n"
AR "v74,a122\n"
MF "a[144:147],v[164:167],v[96:99],a[144:147]\n"
AR "v75,a123\n"
AR "v76,a140\n"
AR "v77,a141\n"
AR "v78,a142\n"
AR "v79,a143\n"
"v_pk_fma_f32 v[72:73],v[72:73],v[208:209],v[214:215] op_sel:[0,0,0] op_sel_hi:[1,0,0]\n"
"v_pk_fma_f32 v[74:75],v[74:75],v[208:209],v[214:215] op_sel:[0,0,0] op_sel_hi:[1,0,0]\n"
"v_pk_fma_f32 v[76:77],v[76:77],v[208:209],v[214:215] op_sel:[0,0,0] op_sel_hi:[1,0,0]\n"
"v_pk_fma_f32 v[78:79],v[78:79],v[208:209],v[214:215] op_sel:[0,0,0] op_sel_hi:[1,0,0]\n"
CV "v112,v72,v73\n"
CV "v113,v74,v75\n"
CV "v114,v76,v77\n"
CV "v115,v78,v79\n"
"s_waitcnt lgkmcnt(0)\n"
MF "a[128:131],v[160:163],v[100:103],a[128:131]\n"
"ds_read_b128 a[140:143],v237 offset:20480\n"
AW "a120,v112\n"
AW "a121,v113\n"
AW "a122,v114\n"
AW "a123,v115\n"
MF "a[44:47],v[144:147],v[96:99],a[44:47]\n"
AR "v80,a124\n"
AR "v81,a125\n"
AR "v82,a126\n"
MF "a[148:151],v[164:167],v[100:103],a[148:151]\n"
AR "v83,a127\n"
AR "v84,a144\n"
AR "v85,a145\n"
"s_nop 0\n"
AR "v86,a146\n"
AR "v87,a147\n"
"v_pk_fma_f32 v[80:81],v[80:81],v[208:209],v[214:215] op_sel:[0,1,1] op_sel_hi:[1,1,1]\n"
"v_pk_fma_f32 v[82:83],v[82:83],v[208:209],v[214:215] op_sel:[0,1,1] op_sel_hi:[1,1,1]\n"
"v_pk_fma_f32 v[84:85],v[84:85],v[208:209],v[214:215] op_sel:[0,1,1] op_sel_hi:[1,1,1]\n"
"v_pk_fma_f32 v[86:87],v[86:87],v[208:209],v[214:215] op_sel:[0,1,1] op_sel_hi:[1,1,1]\n"
CV "v116,v80,v81\n"
CV "v117,v82,v83\n"
CV "v118,v84,v85\n"
CV "v119,v86,v87\n"
MF "a[132:135],v[160:163],v[104:107],a[132:135]\n"
"ds_read_b128 a[144:147],v236\n"
AW "a124,v116\n"
AW "a125,v117\n"
AW "a126,v118\n"
AW "a127,v119\n"
MF "a[64:67],v[148:151],v[96:99],a[64:67]\n"
AR "v72,a128\n"
AR "v73,a129\n"
AR "v74,a130\n"
MF "a[152:155],v[164:167],v[104:107],a[152:155]\n"
AR "v75,a131\n"
AR "v76,a148\n"
AR "v77,a149\n"
AR "v78,a150\n"
AR "v79,a151\n"
"v_pk_fma_f32 v[72:73],v[72:73],v[210:211],v[216:217] op_sel:[0,0,0] op_sel_hi:[1,0,0]\n"
"v_pk_fma_f32 v[74:75],v[74:75],v[210:211],v[216:217] op_sel:[0,0,0] op_sel_hi:[1,0,0]\n"
"v_pk_fma_f32 v[76:77],v[76:77],v[210:211],v[216:217] op_sel:[0,0,0] op_sel_hi:[1,0,0]\n"
"v_pk_fma_f32 v[78:79],v[78:79],v[210:211],v[216:217] op_sel:[0,0,0] op_sel_hi:[1,0,0]\n"
CV "v112,v72,v73\n"
CV "v113,v74,v75\n"
CV "v114,v76,v77\n"
CV "v115,v78,v79\n"
MF "a[136:139],v[160:163],v[108:111],a[136:139]\n"
"ds_read_b128 a[148:151],v236 offset:4096\n"
AW "a128,v112\n"
AW "a129,v113\n"
AW "a130,v114\n"
AW "a131,v115\n"
MF "a[84:87],v[152:155],v[96:99],a[84:87]\n"
AR "v80,a132\n"
AR "v81,a133\n"
AR "v82,a134\n"
MF "a[156:159],v[164:167],v[108:111],a[156:159]\n"
AR "v83,a135\n"
AR "v84,a152\n"
AR "v85,a153\n"
AR "v86,a154\n"
AR "v87,a155\n"
"v_pk_fma_f32 v[80:81],v[80:81],v[210:211],v[216:217] op_sel:[0,1,1] op_sel_hi:[1,1,1]\n"
"v_pk_fma_f32 v[82:83],v[82:83],v[210:211],v[216:217] op_sel:[0,1,1] op_sel_hi:[1,1,1]\n"
"v_pk_fma_f32 v[84:85],v[84:85],v[210:211],v[216:217] op_sel:[0,1,1] op_sel_hi:[1,1,1]\n"
"v_pk_fma_f32 v[86:87],v[86:87],v[210:211],v[216:217] op_sel:[0,1,1] op_sel_hi:[1,1,1]\n"
CV "v116,v80,v81\n"
CV "v117,v82,v83\n"
CV "v118,v84,v85\n"
CV "v119,v86,v87\n"
MF "a[160:163],v[168:171],v[92:95],a[160:163]\n"
"ds_read_b128 a[152:155],v236 offset:8192\n"
AW "a132,v116\n"
AW "a133,v117\n"
AW "a134,v118\n"
AW "a135,v119\n"
MF "a[104:107],v[156:159],v[96:99],a[104:107]\n"
AR "v72,a136\n"
AR "v73,a137\n"
AR "v74,a138\n"
MF "a[180:183],v[172:175],v[92:95],a[180:183]\n"
AR "v75,a139\n"
AR "v76,a156\n"
AR "v77,a157\n"
AR "v78,a158\n"
AR "v79,a159\n"
"v_pk_fma_f32 v[72:73],v[72:73],v[212:213],v[218:219] op_sel:[0,0,0] op_sel_hi:[1,0,0]\n"
"v_pk_fma_f32 v[74:75],v[74:75],v[212:213],v[218:219] op_sel:[0,0,0] op_sel_hi:[1,0,0]\n"
"v_pk_fma_f32 v[76:77],v[76:77],v[212:213],v[218:219] op_sel:[0,0,0] op_sel_hi:[1,0,0]\n"
"v_pk_fma_f32 v[78:79],v[78:79],v[212:213],v[218:219] op_sel:[0,0,0] op_sel_hi:[1,0,0]\n"
CV "v112,v72,v73\n"
CV "v113,v74,v75\n"
CV "v114,v76,v77\n"
CV "v115,v78,v79\n"
MF "a[164:167],v[168:171],v[96:99],a[164:167]\n"
"ds_read_b128 a[156:159],v236 offset:12288\n"
AW "a136,v112\n"
AW "a137,v113\n"
AW "a138,v114\n"
AW "a139,v115\n"
"v_max3_f32 v10,|v72|,|v73|,|v74|\n"
"v_max3_f32 v10,v10,|v75|,|v76|\n"
"v_max3_f32 v10,v10,|v77|,|v78|\n"
"v_max3_f32 v10,v10,|v78|,|v79|\n"
"v_mul_f32 v10,s10,v10\n"
"v_lshrrev_b32 v11,2,v9\n"
"v_readlane_b32 s12,v10,3\n"
"v_readlane_b32 s6,v10,22\n"
"v_readlane_b32 s7,v10,41\n"
"v_readlane_b32 s8,v10,60\n"
"v_add_u32 v11,s14,v11\n"
"s_min_u32 s12,s12,s6\n"
"s_min_u32 s7,s7,s8\n"
"s_min_u32 s12,s12,s7\n"
"s_and_b32 s12,s12,0x7f800000\n"
"s_sub_u32 s12,s12,0x14000000\n"
"s_cmp_lt_u32 s12,0x50000000\n"
"s_cselect_b32 s12,s12,0xe7400000\n"
"s_add_u32 s12,s12,0x18c00000\n"
"v_mov_b32_e64 v10,s12\n"
"v_or_b32 v12,0x80000000,v10\n"
"ds_write2st64_b32 v11,v10,v12 offset1:1\n"
MF "a[8:11],v[136:139],v[100:103],a[8:11]\n"
AR "v80,a160\n"
AR "v81,a161\n"
AR "v82,a162\n"
MF "a[184:187],v[172:175],v[96:99],a[184:187]\n"
AR "v83,a163\n"
AR "v84,a180\n"
AR "v85,a181\n"
AR "v86,a182\n"
AR "v87,a183\n"
"v_pk_fma_f32 v[80:81],v[80:81],v[208:209],v[214:215] op_sel:[0,0,0] op_sel_hi:[1,0,0]\n"
"v_pk_fma_f32 v[82:83],v[82:83],v[208:209],v[214:215] op_sel:[0,0,0] op_sel_hi:[1,0,0]\n"
"v_pk_fma_f32 v[84:85],v[84:85],v[208:209],v[214:215] op_sel:[0,0,0] op_sel_hi:[1,0,0]\n"
"v_pk_fma_f32 v[86:87],v[86:87],v[208:209],v[214:215] op_sel:[0,0,0] op_sel_hi:[1,0,0]\n"
CV "v116,v80,v81\n"
CV "v117,v82,v83\n"
CV "v118,v84,v85\n"
CV "v119,v86,v87\n"
MF "a[168:171],v[168:171],v[100:103],a[168:171]\n"
"ds_read_b128 a[180:183],v236 offset:16384\n"
AW "a160,v116\n"
AW "a161,v117\n"
AW "a162,v118\n"
AW "a163,v119\n"
MF "a[28:31],v[140:143],v[100:103],a[28:31]\n"
AR "v72,a164\n"
AR "v73,a165\n"
AR "v74,a166\n"
MF "a[188:191],v[172:175],v[100:103],a[188:191]\n"
AR "v75,a167\n"
AR "v76,a184\n"
AR "v77,a185\n"
AR "v78,a186\n"
AR "v79,a187\n"
"v_pk_fma_f32 v[72:73],v[72:73],v[208:209],v[214:215] op_sel:[0,1,1] op_sel_hi:[1,1,1]\n"
"v_pk_fma_f32 v[74:75],v[74:75],v[208:209],v[214:215] op_sel:[0,1,1] op_sel_hi:[1,1,1]\n"
"v_pk_fma_f32 v[76:77],v[76:77],v[208:209],v[214:215] op_sel:[0,1,1] op_sel_hi:[1,1,1]\n"
"v_pk_fma_f32 v[78:79],v[78:79],v[208:209],v[214:215] op_sel:[0,1,1] op_sel_hi:[1,1,1]\n"
CV "v112,v72,v73\n"
CV "v113,v74,v75\n"
CV "v114,v76,v77\n"
CV "v115,v78,v79\n"
MF "a[172:175],v[168:171],v[104:107],a[172:175]\n"
"ds_read_b128 a[184:187],v236 offset:20480\n"
AW "a164,v112\n"
AW "a165,v113\n"
AW "a166,v114\n"
AW "a167,v115\n"
MF "a[48:51],v[144:147],v[100:103],a[48:51]\n"
"s_waitcnt lgkmcnt(0)\n"
"s_barrier\n"
"s_add_u32 m0,s5,49152\n"
"v_add_u32_e64 v192,0,v7\n"
LD "v192,s[48:51],s2 offen lds\n"
AR "v80,a168\n"
AR "v81,a169\n"
AR "v82,a170\n"
MF "a[192:195],v[172:175],v[104:107],a[192:195]\n"
AR "v83,a171\n"
AR "v84,a188\n"
AR "v85,a189\n"
AR "v86,a190\n"
AR "v87,a191\n"
"v_pk_fma_f32 v[80:81],v[80:81],v[210:211],v[216:217] op_sel:[0,0,0] op_sel_hi:[1,0,0]\n"
"v_pk_fma_f32 v[82:83],v[82:83],v[210:211],v[216:217] op_sel:[0,0,0] op_sel_hi:[1,0,0]\n"
"v_pk_fma_f32 v[84:85],v[84:85],v[210:211],v[216:217] op_sel:[0,0,0] op_sel_hi:[1,0,0]\n"
"v_pk_fma_f32 v[86:87],v[86:87],v[210:211],v[216:217] op_sel:[0,0,0] op_sel_hi:[1,0,0]\n"
"v_add_u32 v193,9216,v7\n"
LD "v193,s[48:51],s2 offen offset:1024 lds\n"
CV "v116,v80,v81\n"
CV "v117,v82,v83\n"
CV "v118,v84,v85\n"
CV "v119,v86,v87\n"
MF "a[176:179],v[168:171],v[108:111],a[176:179]\n"
"v_accvgpr_mov_b32 a188,a240\n"
"v_accvgpr_mov_b32 a189,a241\n"
"v_accvgpr_mov_b32 a190,a242\n"
"v_accvgpr_mov_b32 a191,a243\n"
AW "a168,v116\n"
AW "a169,v117\n"
AW "a170,v118\n"
AW "a171,v119\n"
MF "a[68:71],v[148:151],v[100:103],a[68:71]\n"
"v_add_u32 v194,18432,v7\n"
LD "v194,s[48:51],s2 offen offset:2048 lds\n"
AR "v72,a172\n"
AR "v73,a173\n"
AR "v74,a174\n"
MF "a[196:199],v[172:175],v[108:111],a[196:199]\n"
AR "v75,a175\n"
AR "v76,a192\n"
AR "v77,a193\n"
AR "v78,a194\n"
AR "v79,a195\n"
"v_pk_fma_f32 v[72:73],v[72:73],v[210:211],v[216:217] op_sel:[0,1,1] op_sel_hi:[1,1,1]\n"
"v_pk_fma_f32 v[74:75],v[74:75],v[210:211],v[216:217] op_sel:[0,1,1] op_sel_hi:[1,1,1]\n"
"v_pk_fma_f32 v[76:77],v[76:77],v[210:211],v[216:217] op_sel:[0,1,1] op_sel_hi:[1,1,1]\n"
"v_pk_fma_f32 v[78:79],v[78:79],v[210:211],v[216:217] op_sel:[0,1,1] op_sel_hi:[1,1,1]\n"
LD "v195,s[48:51],s2 offen offset:3072 lds\n"
CV "v112,v72,v73\n"
CV "v113,v74,v75\n"
CV "v114,v76,v77\n"
CV "v115,v78,v79\n"
MF "a[200:203],v[176:179],v[92:95],a[200:203]\n"
"s_add_u32 m0,s5,53248\n"
"v_accvgpr_mov_b32 a192,a244\n"
"v_accvgpr_mov_b32 a193,a245\n"
"v_accvgpr_mov_b32 a194,a246\n"
"v_accvgpr_mov_b32 a195,a247\n"
AW "a172,v112\n"
AW "a173,v113\n"
AW "a174,v114\n"
AW "a175,v115\n"
MF "a[88:91],v[152:155],v[100:103],a[88:91]\n"
LD "v196,s[48:51],s2 offen lds\n"
AR "v80,a176\n"
AR "v81,a177\n"
AR "v82,a178\n"
MF "a[220:223],v[180:183],v[92:95],a[220:223]\n"
AR "v83,a179\n"
AR "v84,a196\n"
AR "v85,a197\n"
AR "v86,a198\n"
AR "v87,a199\n"
"v_pk_fma_f32 v[80:81],v[80:81],v[212:213],v[218:219] op_sel:[0,0,0] op_sel_hi:[1,0,0]\n"
"v_pk_fma_f32 v[82:83],v[82:83],v[212:213],v[218:219] op_sel:[0,0,0] op_sel_hi:[1,0,0]\n"
"v_pk_fma_f32 v[84:85],v[84:85],v[212:213],v[218:219] op_sel:[0,0,0] op_sel_hi:[1,0,0]\n"
"v_pk_fma_f32 v[86:87],v[86:87],v[212:213],v[218:219] op_sel:[0,0,0] op_sel_hi:[1,0,0]\n"
CV "v116,v80,v81\n"
CV "v117,v82,v83\n"
CV "v118,v84,v85\n"
CV "v119,v86,v87\n"
MF "a[204:207],v[176:179],v[96:99],a[204:207]\n"
"v_accvgpr_mov_b32 a196,a248\n"
"v_accvgpr_mov_b32 a197,a249\n"
"v_accvgpr_mov_b32 a198,a250\n"
"v_accvgpr_mov_b32 a199,a251\n"
AW "a176,v116\n"
AW "a177,v117\n"
AW "a178,v118\n"
AW "a179,v119\n"
MF "a[108:111],v[156:159],v[100:103],a[108:111]\n"
LD "v197,s[48:51],s2 offen offset:1024 lds\n"
AR "v72,a200\n"
AR "v73,a201\n"
AR "v74,a202\n"
MF "a[224:227],v[180:183],v[96:99],a[224:227]\n"
AR "v75,a203\n"
AR "v76,a220\n"
AR "v77,a221\n"
AR "v78,a222\n"
AR "v79,a223\n"
"v_pk_fma_f32 v[72:73],v[72:73],v[208:209],v[214:215] op_sel:[0,0,0] op_sel_hi:[1,0,0]\n"
"v_pk_fma_f32 v[74:75],v[74:75],v[208:209],v[214:215] op_sel:[0,0,0] op_sel_hi:[1,0,0]\n"
"v_pk_fma_f32 v[76:77],v[76:77],v[208:209],v[214:215] op_sel:[0,0,0] op_sel_hi:[1,0,0]\n"
"v_pk_fma_f32 v[78:79],v[78:79],v[208:209],v[214:215] op_sel:[0,0,0] op_sel_hi:[1,0,0]\n"
CV "v112,v72,v73\n"
CV "v113,v74,v75\n"
CV "v114,v76,v77\n"
CV "v115,v78,v79\n"
MF "a[208:211],v[176:179],v[100:103],a[208:211]\n"
"s_add_u32 m0,s5,73728\n"
AW "a220,v62\n"
AW "a221,v63\n"
AW "a222,v64\n"
AW "a223,v65\n"
AW "a200,v112\n"
AW "a201,v113\n"
AW "a202,v114\n"
AW "a203,v115\n"
MF "a[12:15],v[136:139],v[104:107],a[12:15]\n"
LD "v192,s[48:51],s3 offen lds\n"
AR "v80,a204\n"
AR "v81,a205\n"
AR "v82,a206\n"
MF "a[228:231],v[180:183],v[100:103],a[228:231]\n"
AR "v83,a207\n"
AR "v84,a224\n"
AR "v85,a225\n"
AR "v86,a226\n"
AR "v87,a227\n"
"v_pk_fma_f32 v[80:81],v[80:81],v[208:209],v[214:215] op_sel:[0,1,1] op_sel_hi:[1,1,1]\n"
"v_pk_fma_f32 v[82:83],v[82:83],v[208:209],v[214:215] op_sel:[0,1,1] op_sel_hi:[1,1,1]\n"
"v_pk_fma_f32 v[84:85],v[84:85],v[208:209],v[214:215] op_sel:[0,1,1] op_sel_hi:[1,1,1]\n"
"v_pk_fma_f32 v[86:87],v[86:87],v[208:209],v[214:215] op_sel:[0,1,1] op_sel_hi:[1,1,1]\n"
CV "v116,v80,v81\n"
CV "v117,v82,v83\n"
CV "v118,v84,v85\n"
CV "v119,v86,v87\n"
MF "a[212:215],v[176:179],v[104:107],a[212:215]\n"
AW "a224,v66\n"
AW "a225,v67\n"
AW "a226,v68\n"
AW "a227,v69\n"
AW "a204,v116\n"
AW "a205,v117\n"
AW "a206,v118\n"
AW "a207,v119\n"
MF "a[32:35],v[140:143],v[104:107],a[32:35]\n"
LD "v193,s[48:51],s3 offen offset:1024 lds\n"
AR "v72,a208\n"
AR "v73,a209\n"
AR "v74,a210\n"
MF "a[232:235],v[180:183],v[104:107],a[232:235]\n"
AR "v75,a211\n"
AR "v76,a228\n"
AR "v77,a229\n"
AR "v78,a230\n"
AR "v79,a231\n"
"v_pk_fma_f32 v[72:73],v[72:73],v[210:211],v[216:217] op_sel:[0,0,0] op_sel_hi:[1,0,0]\n"
"v_pk_fma_f32 v[74:75],v[74:75],v[210:211],v[216:217] op_sel:[0,0,0] op_sel_hi:[1,0,0]\n"
"v_pk_fma_f32 v[76:77],v[76:77],v[210:211],v[216:217] op_sel:[0,0,0] op_sel_hi:[1,0,0]\n"
"v_pk_fma_f32 v[78:79],v[78:79],v[210:211],v[216:217] op_sel:[0,0,0] op_sel_hi:[1,0,0]\n"
CV "v112,v72,v73\n"
CV "v113,v74,v75\n"
CV "v114,v76,v77\n"
CV "v115,v78,v79\n"
MF "a[216:219],v[176:179],v[108:111],a[216:219]\n"
AW "a228,v70\n"
AW "a229,v71\n"
AW "a230,v132\n"
AW "a231,v133\n"
AW "a208,v112\n"
AW "a209,v113\n"
AW "a210,v114\n"
AW "a211,v115\n"
MF "a[52:55],v[144:147],v[104:107],a[52:55]\n"
LD "v194,s[48:51],s3 offen offset:2048 lds\n"
AR "v80,a212\n"
AR "v81,a213\n"
AR "v82,a214\n"
MF "a[236:239],v[180:183],v[108:111],a[236:239]\n"
AR "v83,a215\n"
AR "v84,a232\n"
AR "v85,a233\n"
AR "v86,a234\n"
AR "v87,a235\n"
"v_pk_fma_f32 v[80:81],v[80:81],v[210:211],v[216:217] op_sel:[0,1,1] op_sel_hi:[1,1,1]\n"
"v_pk_fma_f32 v[82:83],v[82:83],v[210:211],v[216:217] op_sel:[0,1,1] op_sel_hi:[1,1,1]\n"
"v_pk_fma_f32 v[84:85],v[84:85],v[210:211],v[216:217] op_sel:[0,1,1] op_sel_hi:[1,1,1]\n"
"v_pk_fma_f32 v[86:87],v[86:87],v[210:211],v[216:217] op_sel:[0,1,1] op_sel_hi:[1,1,1]\n"
CV "v116,v80,v81\n"
CV "v117,v82,v83\n"
CV "v118,v84,v85\n"
CV "v119,v86,v87\n"
MF "a[0:3],v[136:139],v[92:95],a[0:3]\n"
AW "a232,v134\n"
AW "a233,v135\n"
AW "a234,v6\n"
AW "a235,v13\n"
AW "a212,v116\n"
AW "a213,v117\n"
AW "a214,v118\n"
AW "a215,v119\n"
MF "a[72:75],v[148:151],v[104:107],a[72:75]\n"
LD "v195,s[48:51],s3 offen offset:3072 lds\n"
AR "v72,a216\n"
AR "v73,a217\n"
AR "v74,a218\n"
MF "a[20:23],v[140:143],v[92:95],a[20:23]\n"
AR "v75,a219\n"
AR "v76,a236\n"
AR "v77,a237\n"
AR "v78,a238\n"
AR "v79,a239\n"
"v_pk_fma_f32 v[72:73],v[72:73],v[212:213],v[218:219] op_sel:[0,0,0] op_sel_hi:[1,0,0]\n"
"v_pk_fma_f32 v[74:75],v[74:75],v[212:213],v[218:219] op_sel:[0,0,0] op_sel_hi:[1,0,0]\n"
"v_pk_fma_f32 v[76:77],v[76:77],v[212:213],v[218:219] op_sel:[0,0,0] op_sel_hi:[1,0,0]\n"
"v_pk_fma_f32 v[78:79],v[78:79],v[212:213],v[218:219] op_sel:[0,0,0] op_sel_hi:[1,0,0]\n"
CV "v112,v72,v73\n"
CV "v113,v74,v75\n"
CV "v114,v76,v77\n"
CV "v115,v78,v79\n"
MF "a[40:43],v[144:147],v[92:95],a[40:43]\n"
"s_add_u32 m0,s5,77824\n"
AW "a236,v14\n"
AW "a237,v15\n"
"v_accvgpr_mov_b32 a238,a254\n"
"v_accvgpr_mov_b32 a239,a255\n"
AW "a216,v112\n"
AW "a217,v113\n"
AW "a218,v114\n"
AW "a219,v115\n"
MF "a[92:95],v[152:155],v[104:107],a[92:95]\n"
".Lpg24h:\n"
"s_cbranch_execz .Lpg28h\n"
LD "v196,s[48:51],s3 offen lds\n"
AR "v80,a0\n"
AR "v81,a1\n"
AR "v82,a2\n"
MF "a[60:63],v[148:151],v[92:95],a[60:63]\n"
AR "v83,a3\n"
AR "v84,a20\n"
AR "v85,a21\n"
"s_nop 0\n"
AR "v86,a22\n"
AR "v87,a23\n"
"v_pk_fma_f32 v[80:81],v[80:81],v[208:209],v[214:215] op_sel:[0,0,0] op_sel_hi:[1,0,0]\n"
"v_pk_fma_f32 v[82:83],v[82:83],v[208:209],v[214:215] op_sel:[0,0,0] op_sel_hi:[1,0,0]\n"
"v_pk_fma_f32 v[84:85],v[84:85],v[208:209],v[214:215] op_sel:[0,0,0] op_sel_hi:[1,0,0]\n"
"v_pk_fma_f32 v[86:87],v[86:87],v[208:209],v[214:215] op_sel:[0,0,0] op_sel_hi:[1,0,0]\n"
CV "v116,v80,v81\n"
CV "v117,v82,v83\n"
CV "v118,v84,v85\n"
CV "v119,v86,v87\n"
MF "a[80:83],v[152:155],v[92:95],a[80:83]\n"
"ds_read_b128 a[20:23],v236 offset:24576\n"
AW "a0,v116\n"
AW "a1,v117\n"
AW "a2,v118\n"
AW "a3,v119\n"
MF "a[112:115],v[156:159],v[104:107],a[112:115]\n"
LD "v197,s[48:51],s3 offen offset:1024 lds\n"
AR "v72,a40\n"
AR "v73,a41\n"
AR "v74,a42\n"
MF "a[100:103],v[156:159],v[92:95],a[100:103]\n"
AR "v75,a43\n"
AR "v76,a60\n"
AR "v77,a61\n"
AR "v78,a62\n"
AR "v79,a63\n"
"v_pk_fma_f32 v[72:73],v[72:73],v[208:209],v[214:215] op_sel:[0,0,0] op_sel_hi:[1,0,0]\n"
"v_pk_fma_f32 v[74:75],v[74:75],v[208:209],v[214:215] op_sel:[0,0,0] op_sel_hi:[1,0,0]\n"
"v_pk_fma_f32 v[76:77],v[76:77],v[208:209],v[214:215] op_sel:[0,0,0] op_sel_hi:[1,0,0]\n"
"v_pk_fma_f32 v[78:79],v[78:79],v[208:209],v[214:215] op_sel:[0,0,0] op_sel_hi:[1,0,0]\n"
CV "v112,v72,v73\n"
CV "v113,v74,v75\n"
CV "v114,v76,v77\n"
CV "v115,v78,v79\n"
MF "a[4:7],v[136:139],v[96:99],a[4:7]\n"
"ds_read_b128 a[60:63],v236 offset:45056\n"
AW "a40,v112\n"
AW "a41,v113\n"
AW "a42,v114\n"
AW "a43,v115\n"
MF "a[16:19],v[136:139],v[108:111],a[16:19]\n"
AR "v80,a80\n"
AR "v81,a81\n"
AR "v82,a82\n"
MF "a[24:27],v[140:143],v[96:99],a[24:27]\n"
AR "v83,a83\n"
AR "v84,a100\n"
AR "v85,a101\n"
AR "v86,a102\n"
AR "v87,a103\n"
"v_pk_fma_f32 v[80:81],v[80:81],v[208:209],v[214:215] op_sel:[0,0,0] op_sel_hi:[1,0,0]\n"
"v_pk_fma_f32 v[82:83],v[82:83],v[208:209],v[214:215] op_sel:[0,0,0] op_sel_hi:[1,0,0]\n"
"v_pk_fma_f32 v[84:85],v[84:85],v[208:209],v[214:215] op_sel:[0,0,0] op_sel_hi:[1,0,0]\n"
"v_pk_fma_f32 v[86:87],v[86:87],v[208:209],v[214:215] op_sel:[0,0,0] op_sel_hi:[1,0,0]\n"
CV "v116,v80,v81\n"
CV "v117,v82,v83\n"
CV "v118,v84,v85\n"
CV "v119,v86,v87\n"
MF "a[36:39],v[140:143],v[108:111],a[36:39]\n"
"ds_read_b128 a[100:103],v237\n"
AW "a80,v116\n"
AW "a81,v117\n"
AW "a82,v118\n"
AW "a83,v119\n"
MF "a[56:59],v[144:147],v[108:111],a[56:59]\n"
AR "v72,a4\n"
AR "v73,a5\n"
AR "v74,a6\n"
MF "a[76:79],v[148:151],v[108:111],a[76:79]\n"
AR "v75,a7\n"
AR "v76,a24\n"
AR "v77,a25\n"
AR "v78,a26\n"
AR "v79,a27\n"
"v_pk_fma_f32 v[72:73],v[72:73],v[208:209],v[214:215] op_sel:[0,1,1] op_sel_hi:[1,1,1]\n"
"v_pk_fma_f32 v[74:75],v[74:75],v[208:209],v[214:215] op_sel:[0,1,1] op_sel_hi:[1,1,1]\n"
"v_pk_fma_f32 v[76:77],v[76:77],v[208:209],v[214:215] op_sel:[0,1,1] op_sel_hi:[1,1,1]\n"
"v_pk_fma_f32 v[78:79],v[78:79],v[208:209],v[214:215] op_sel:[0,1,1] op_sel_hi:[1,1,1]\n"
CV "v112,v72,v73\n"
CV "v113,v74,v75\n"
CV "v114,v76,v77\n"
CV "v115,v78,v79\n"
MF "a[96:99],v[152:155],v[108:111],a[96:99]\n"
"ds_read_b128 a[24:27],v236 offset:28672\n"
AW "a4,v112\n"
AW "a5,v113\n"
AW "a6,v114\n"
AW "a7,v115\n"
MF "a[116:119],v[156:159],v[108:111],a[116:119]\n"
"v_and_b32 v0,512,v9\n"
"v_lshlrev_b32 v0,2,v0\n"
"v_sub_u32 v0,v2,v0\n"
"v_xor_b32 v0,s4,v0\n"
"v_mov_b32 v252,s15\n"
"s_waitcnt vmcnt(12) lgkmcnt(0)\n"
"s_barrier\n"
"ds_read_b128 v[252:255],v252\n"
TR "v[176:177],v0 offset:24576\n"
TR "v[178:179],v0 offset:26624\n"
TR "v[180:181],v0 offset:28672\n"
TR "v[182:183],v0 offset:30720\n"
TR "v[184:185],v0 offset:32768\n"
TR "v[186:187],v0 offset:34816\n"
"v_xor_b32 v1,32,v0\n"
"v_xor_b32 v2,64,v0\n"
"v_xor_b32 v3,96,v0\n"
"v_add_u32 v4,73728,v0\n"
"v_add_u32 v5,73728,v1\n"
"v_add_u32 v6,73728,v2\n"
"v_add_u32 v7,73728,v3\n"
"s_waitcnt lgkmcnt(4)\n"
MF "v[16:19],a[120:123],v[176:179],v[252:255]\n"
"s_mov_b32 s65,128\n"
TR "v[188:189],v1 offset:24576\n"
AR "v220,a44\n"
"s_waitcnt lgkmcnt(3)\n"
MF "v[16:19],a[160:163],v[180:183],v[16:19]\n"
"s_mov_b32 s9,0x3f80\n"
TR "v[190:191],v1 offset:26624\n"
AR "v221,a45\n"
"s_waitcnt lgkmcnt(2)\n"
MF "v[16:19],a[200:203],v[184:187],v[16:19]\n"
"s_mov_b32 s89,s2\n"
AR "v222,a46\n"
AR "v223,a47\n"
MF "v[24:27],a[204:207],v[184:187],v[252:255]\n"
"s_mov_b32 s88,s3\n"
TR "a[240:241],v1 offset:28672\n"
AR "v224,a64\n"
MF "v[24:27],a[164:167],v[180:183],v[24:27]\n"
"s_bfe_u32 s58,s22,0x30014\n"
TR "a[242:243],v1 offset:30720\n"
AR "v225,a65\n"
MF "v[24:27],a[124:127],v[176:179],v[24:27]\n"
"s_add_u32 s61,s58,1\n"
AR "v226,a66\n"
AR "v227,a67\n"
MF "v[32:35],a[128:131],v[176:179],v[252:255]\n"
"s_and_b32 s61,s61,7\n"
TR "a[244:245],v1 offset:32768\n"
"ds_read_b128 a[64:67],v236 offset:49152\n"
MF "v[32:35],a[168:171],v[180:183],v[32:35]\n"
"s_mov_b32 s64,9\n"
TR "a[246:247],v1 offset:34816\n"
"v_fma_f32 v220,v220,v209,v215\n"
MF "v[32:35],a[208:211],v[184:187],v[32:35]\n"
"s_xor_b32 s67,s78,8\n"
"v_fma_f32 v221,v221,v209,v215\n"
"v_fma_f32 v222,v222,v209,v215\n"
MF "v[40:43],a[212:215],v[184:187],v[252:255]\n"
"s_lshr_b32 s60,s56,1\n"
"v_fma_f32 v223,v223,v209,v215\n"
"v_fma_f32 v224,v224,v209,v215\n"
MF "v[40:43],a[172:175],v[180:183],v[40:43]\n"
"s_xor_b32 s60,s60,1\n"
"v_fma_f32 v225,v225,v209,v215\n"
"v_fma_f32 v226,v226,v209,v215\n"
MF "v[40:43],a[132:135],v[176:179],v[40:43]\n"
"s_mul_i32 s59,s60,10240\n"
"v_fma_f32 v227,v227,v209,v215\n"
CV "v220,v220,v221\n"
MF "v[48:51],a[136:139],v[176:179],v[252:255]\n"
"s_add_u32 s90,s91,s59\n"
CV "v221,v222,v223\n"
CV "v222,v224,v225\n"
MF "v[48:51],a[176:179],v[180:183],v[48:51]\n"
"s_and_b32 s73,s56,1\n"
CV "v223,v226,v227\n"
AW "a44,v220\n"
MF "v[48:51],a[216:219],v[184:187],v[48:51]\n"
"s_waitcnt lgkmcnt(0)\n"
AW "a45,v221\n"
AW "a46,v222\n"
MF "v[52:55],a[216:219],a[244:247],v[252:255]\n"
"s_mul_i32 s72,s73,5120\n"
TR "v[176:177],v2 offset:24576\n"
AW "a47,v223\n"
MF "v[52:55],a[176:179],a[240:243],v[52:55]\n"
"s_add_u32 s59,s59,s72\n"
TR "v[178:179],v2 offset:26624\n"
AR "v228,a84\n"
MF "v[52:55],a[136:139],v[188:191],v[52:55]\n"
"s_add_u32 s59,s59,s93\n"
AR "v229,a85\n"
AR "v230,a86\n"
MF "v[44:47],a[132:135],v[188:191],v[252:255]\n"
"s_add_u32 s70,s58,4\n"
TR "v[180:181],v2 offset:28672\n"
AR "v231,a87\n"
MF "v[44:47],a[172:175],a[240:243],v[44:47]\n"
"s_and_b32 s70,s70,7\n"
TR "v[182:183],v2 offset:30720\n"
AR "v232,a104\n"
MF "v[44:47],a[212:215],a[244:247],v[44:47]\n"
"s_mul_i32 s70,s70,204800\n"
AR "v233,a105\n"
AR "v234,a106\n"
MF "v[36:39],a[208:211],a[244:247],v[252:255]\n"
"s_add_u32 s70,s70,s59\n"
TR "v[184:185],v2 offset:32768\n"
AR "v235,a107\n"
MF "v[36:39],a[168:171],a[240:243],v[36:39]\n"
"s_add_u32 s70,s70,40960\n"
TR "v[186:187],v2 offset:34816\n"
"ds_read_b128 a[104:107],v237 offset:4096\n"
MF "v[36:39],a[128:131],v[188:191],v[36:39]\n"
"s_add_u32 s6,s58,3\n"
"v_fma_f32 v228,v228,v209,v215\n"
"v_fma_f32 v229,v229,v209,v215\n"
MF "v[28:31],a[124:127],v[188:191],v[252:255]\n"
"s_and_b32 s6,s6,7\n"
"v_fma_f32 v230,v230,v209,v215\n"
"v_fma_f32 v231,v231,v209,v215\n"
MF "v[28:31],a[164:167],a[240:243],v[28:31]\n"
"s_mul_i32 s6,s6,204800\n"
"v_fma_f32 v232,v232,v209,v215\n"
"v_fma_f32 v233,v233,v209,v215\n"
MF "v[28:31],a[204:207],a[244:247],v[28:31]\n"
"s_add_u32 s6,s6,s59\n"
"v_fma_f32 v234,v234,v209,v215\n"
"v_fma_f32 v235,v235,v209,v215\n"
MF "v[20:23],a[200:203],a[244:247],v[252:255]\n"
"s_add_u32 s6,s6,122880\n"
CV "v228,v228,v229\n"
CV "v229,v230,v231\n"
MF "v[20:23],a[160:163],a[240:243],v[20:23]\n"
"s_add_u32 s16,s78,6\n"
CV "v230,v232,v233\n"
CV "v231,v234,v235\n"
MF "v[20:23],a[120:123],v[188:191],v[20:23]\n"
"s_waitcnt lgkmcnt(1)\n"
AW "a84,v228\n"
AW "a85,v229\n"
MF "v[56:59],a[120:123],v[176:179],v[252:255]\n"
"s_and_b32 s16,s16,15\n"
TR "v[188:189],v3 offset:24576\n"
AW "a86,v230\n"
MF "v[56:59],a[160:163],v[180:183],v[56:59]\n"
"s_add_u32 s7,s58,2\n"
TR "v[190:191],v3 offset:26624\n"
AW "a87,v231\n"
MF "v[56:59],a[200:203],v[184:187],v[56:59]\n"
"s_and_b32 s7,s7,7\n"
AR "v220,a48\n"
AR "v221,a49\n"
MF "v[64:67],a[204:207],v[184:187],v[252:255]\n"
"s_mul_i32 s7,s7,204800\n"
TR "a[240:241],v3 offset:28672\n"
AR "v222,a50\n"
MF "v[64:67],a[164:167],v[180:183],v[64:67]\n"
"s_add_u32 s7,s7,s59\n"
TR "a[242:243],v3 offset:30720\n"
AR "v223,a51\n"
MF "v[64:67],a[124:127],v[176:179],v[64:67]\n"
"s_add_u32 s7,s7,143360\n"
AR "v224,a68\n"
AR "v225,a69\n"
MF "v[72:75],a[128:131],v[176:179],v[252:255]\n"
"s_add_u32 s17,s78,4\n"
TR "a[244:245],v3 offset:32768\n"
AR "v226,a70\n"
MF "v[72:75],a[168:171],v[180:183],v[72:75]\n"
"s_and_b32 s17,s17,15\n"
TR "a[246:247],v3 offset:34816\n"
AR "v227,a71\n"
MF "v[72:75],a[208:211],v[184:187],v[72:75]\n"
"s_add_u32 s8,s58,1\n"
"ds_read_b128 a[68:71],v236 offset:53248\n"
"v_fma_f32 v220,v220,v210,v216\n"
MF "v[80:83],a[212:215],v[184:187],v[252:255]\n"
"s_and_b32 s8,s8,7\n"
"v_fma_f32 v221,v221,v210,v216\n"
"v_fma_f32 v222,v222,v210,v216\n"
MF "v[80:83],a[172:175],v[180:183],v[80:83]\n"
"s_mul_i32 s8,s8,204800\n"
"v_fma_f32 v223,v223,v210,v216\n"
"v_fma_f32 v224,v224,v210,v216\n"
MF "v[80:83],a[132:135],v[176:179],v[80:83]\n"
"s_add_u32 s8,s8,s59\n"
"v_fma_f32 v225,v225,v210,v216\n"
"v_fma_f32 v226,v226,v210,v216\n"
MF "v[88:91],a[136:139],v[176:179],v[252:255]\n"
"s_add_u32 s8,s8,163840\n"
"v_fma_f32 v227,v227,v210,v216\n"
CV "v220,v220,v221\n"
MF "v[88:91],a[176:179],v[180:183],v[88:91]\n"
"s_waitcnt vmcnt(0) lgkmcnt(0)\n"
CV "v221,v222,v223\n"
CV "v222,v224,v225\n"
MF "v[88:91],a[216:219],v[184:187],v[88:91]\n"
"s_barrier\n"
MF "v[92:95],a[216:219],a[244:247],v[252:255]\n"
"s_add_u32 s89,s89,s65\n"
TR "v[176:177],v0\n"
CV "v223,v226,v227\n"
MF "v[92:95],a[176:179],a[240:243],v[92:95]\n"
"s_add_u32 s88,s88,s65\n"
"s_add_u32 m0,s5,24576\n"
TR "v[178:179],v0 offset:2048\n"
MF "v[92:95],a[136:139],v[188:191],v[92:95]\n"
LD "v192,s[48:51],s89 offen lds\n"
MF "v[84:87],a[132:135],v[188:191],v[252:255]\n"
"s_add_u32 s18,s78,2\n"
TR "v[180:181],v0 offset:4096\n"
AW "a48,v220\n"
MF "v[84:87],a[172:175],a[240:243],v[84:87]\n"
"s_and_b32 s18,s18,15\n"
TR "v[182:183],v0 offset:6144\n"
AW "a49,v221\n"
MF "v[84:87],a[212:215],a[244:247],v[84:87]\n"
"s_mul_i32 s73,s73,204800\n"
TR "v[96:97],v0 offset:49152\n"
AW "a50,v222\n"
MF "v[76:79],a[208:211],a[244:247],v[252:255]\n"
"s_lshl_b32 s58,s58,1\n"
TR "v[184:185],v0 offset:8192\n"
AW "a51,v223\n"
MF "v[76:79],a[168:171],a[240:243],v[76:79]\n"
"s_add_u32 s58,s58,s60\n"
TR "v[186:187],v0 offset:10240\n"
AR "v228,a52\n"
MF "v[76:79],a[128:131],v[188:191],v[76:79]\n"
"s_lshl_b32 s58,s58,6\n"
TR "v[98:99],v0 offset:51200\n"
AR "v229,a53\n"
MF "v[68:71],a[124:127],v[188:191],v[252:255]\n"
"s_add_u32 s73,s73,s58\n"
TR "v[100:101],v0 offset:53248\n"
AR "v230,a54\n"
MF "v[68:71],a[164:167],a[240:243],v[68:71]\n"
LD "v193,s[48:51],s89 offen offset:1024 lds\n"
MF "v[68:71],a[204:207],a[244:247],v[68:71]\n"
"s_mul_i32 s59,s60,0xf0000000\n"
TR "v[102:103],v0 offset:55296\n"
AR "v231,a55\n"
MF "v[60:63],a[200:203],a[244:247],v[252:255]\n"
"s_or_b32 s94,s94,s59\n"
TR "v[104:105],v0 offset:57344\n"
AR "v232,a72\n"
MF "v[60:63],a[160:163],a[240:243],v[60:63]\n"
"s_mov_b32 s85,1\n"
TR "v[106:107],v0 offset:59392\n"
AR "v233,a73\n"
MF "v[60:63],a[120:123],v[188:191],v[60:63]\n"
"s_waitcnt lgkmcnt(5)\n"
TR "v[108:109],v4\n"
AR "v234,a74\n"
MF "v[16:19],a[140:143],v[176:179],v[16:19]\n"
"s_mov_b32 s82,0xf0000000\n"
TR "v[188:189],v1\n"
AR "v235,a75\n"
MF "v[16:19],a[180:183],v[180:183],v[16:19]\n"
"s_bfe_u32 s74,s22,0x30004\n"
TR "v[190:191],v1 offset:2048\n"
"ds_read_b128 a[72:75],v236 offset:57344\n"
MF "v[16:19],a[220:223],v[184:187],v[16:19]\n"
"s_mul_i32 s74,s74,4\n"
TR "v[110:111],v4 offset:2048\n"
"v_fma_f32 v228,v228,v211,v217\n"
MF "v[24:27],a[224:227],v[184:187],v[24:27]\n"
"s_add_u32 s74,s74,s56\n"
TR "a[240:241],v1 offset:4096\n"
"v_fma_f32 v229,v229,v211,v217\n"
MF "v[24:27],a[184:187],v[180:183],v[24:27]\n"
"s_mul_i32 s74,s74,15360\n"
TR "a[242:243],v1 offset:6144\n"
"v_fma_f32 v230,v230,v211,v217\n"
MF "v[24:27],a[144:147],v[176:179],v[24:27]\n"
LD "v194,s[48:51],s89 offen offset:2048 lds\n"
MF "v[32:35],a[148:151],v[176:179],v[32:35]\n"
"s_bfe_u32 s68,s22,0x10007\n"
TR "a[244:245],v1 offset:8192\n"
"v_fma_f32 v231,v231,v211,v217\n"
MF "v[32:35],a[188:191],v[180:183],v[32:35]\n"
"s_lshl_b32 s68,s68,3\n"
TR "a[246:247],v1 offset:10240\n"
"v_fma_f32 v232,v232,v211,v217\n"
MF "v[32:35],a[228:231],v[184:187],v[32:35]\n"
"s_mul_i32 s68,s68,491520\n"
TR "v[112:113],v4 offset:4096\n"
"v_fma_f32 v233,v233,v211,v217\n"
MF "v[40:43],a[232:235],v[184:187],v[40:43]\n"
"s_add_u32 s74,s74,s68\n"
TR "v[114:115],v4 offset:6144\n"
"v_fma_f32 v234,v234,v211,v217\n"
MF "v[40:43],a[192:195],v[180:183],v[40:43]\n"
"s_bfe_u32 s68,s22,0x30014\n"
TR "v[116:117],v4 offset:8192\n"
"v_fma_f32 v235,v235,v211,v217\n"
MF "v[40:43],a[152:155],v[176:179],v[40:43]\n"
"s_mul_i32 s68,s68,491520\n"
TR "v[118:119],v4 offset:10240\n"
CV "v228,v228,v229\n"
MF "v[48:51],a[156:159],v[176:179],v[48:51]\n"
"s_add_u32 s74,s74,s68\n"
TR "v[120:121],v1 offset:49152\n"
CV "v229,v230,v231\n"
MF "v[48:51],a[196:199],v[180:183],v[48:51]\n"
LD "v195,s[48:51],s89 offen offset:3072 lds\n"
MF "v[48:51],a[236:239],v[184:187],v[48:51]\n"
"s_waitcnt lgkmcnt(5)\n"
TR "v[122:123],v1 offset:51200\n"
CV "v230,v232,v233\n"
MF "v[52:55],a[236:239],a[244:247],v[52:55]\n"
"s_bfe_u32 s68,s22,0x30014\n"
TR "v[176:177],v2\n"
CV "v231,v234,v235\n"
MF "v[52:55],a[196:199],a[240:243],v[52:55]\n"
"s_add_u32 s68,s68,7\n"
TR "v[178:179],v2 offset:2048\n"
AW "a52,v228\n"
MF "v[52:55],a[156:159],v[188:191],v[52:55]\n"
"s_lshl_b32 s68,s68,7\n"
TR "v[124:125],v1 offset:53248\n"
AW "a53,v229\n"
MF "v[44:47],a[152:155],v[188:191],v[44:47]\n"
"s_and_b32 s68,s68,768\n"
TR "v[180:181],v2 offset:4096\n"
AW "a54,v230\n"
MF "v[44:47],a[192:195],a[240:243],v[44:47]\n"
"s_mul_i32 s58,s60,6144\n"
TR "v[182:183],v2 offset:6144\n"
AW "a55,v231\n"
MF "v[44:47],a[232:235],a[244:247],v[44:47]\n"
"s_add_u32 s58,s58,s79\n"
TR "v[126:127],v1 offset:55296\n"
AR "v220,a56\n"
MF "v[36:39],a[228:231],a[244:247],v[36:39]\n"
"s_add_u32 s58,s58,73728\n"
TR "v[184:185],v2 offset:8192\n"
AR "v221,a57\n"
MF "v[36:39],a[188:191],a[240:243],v[36:39]\n"
"s_add_u32 m0,s5,28672\n"
TR "v[186:187],v2 offset:10240\n"
AR "v222,a58\n"
MF "v[36:39],a[148:151],v[188:191],v[36:39]\n"
LD "v196,s[48:51],s89 offen lds\n"
MF "v[28:31],a[144:147],v[188:191],v[28:31]\n"
"s_bfe_u32 s4,s22,0x30014\n"
TR "v[128:129],v1 offset:57344\n"
AR "v223,a59\n"
MF "v[28:31],a[184:187],a[240:243],v[28:31]\n"
"s_add_u32 s4,s4,32\n"
TR "v[130:131],v1 offset:59392\n"
AR "v224,a76\n"
MF "v[28:31],a[224:227],a[244:247],v[28:31]\n"
"s_sub_u32 s4,s4,s92\n"
TR "v[132:133],v5\n"
AR "v225,a77\n"
MF "v[20:23],a[220:223],a[244:247],v[20:23]\n"
"s_cmp_eq_u32 s56,2\n"
"s_cselect_b32 s4,s4,64\n"
TR "v[134:135],v5 offset:2048\n"
MF "v[20:23],a[180:183],a[240:243],v[20:23]\n"
"s_mul_i32 s95,s92,3277\n"
TR "v[136:137],v5 offset:4096\n"
AR "v226,a78\n"
MF "v[20:23],a[140:143],v[188:191],v[20:23]\n"
"s_waitcnt lgkmcnt(5)\n"
TR "v[138:139],v5 offset:6144\n"
AR "v227,a79\n"
MF "v[56:59],a[140:143],v[176:179],v[56:59]\n"
"s_lshr_b32 s95,s95,16\n"
TR "v[188:189],v3\n"
"ds_read_b128 a[76:79],v236 offset:61440\n"
MF "v[56:59],a[180:183],v[180:183],v[56:59]\n"
"s_mul_i32 s59,s95,20\n"
TR "v[190:191],v3 offset:2048\n"
"v_fma_f32 v220,v220,v212,v218\n"
MF "v[56:59],a[220:223],v[184:187],v[56:59]\n"
LD "v197,s[48:51],s89 offen offset:1024 lds\n"
MF "v[64:67],a[224:227],v[184:187],v[64:67]\n"
"s_sub_u32 s59,s92,s59\n"
TR "a[240:241],v3 offset:4096\n"
"v_fma_f32 v221,v221,v212,v218\n"
MF "v[64:67],a[184:187],v[180:183],v[64:67]\n"
"s_cmp_ge_u32 s59,10\n"
"s_cselect_b32 s96,4294557760,0\n"
TR "a[242:243],v3 offset:6144\n"
MF "v[64:67],a[144:147],v[176:179],v[64:67]\n"
"s_add_u32 s96,s96,1024\n"
TR "v[140:141],v5 offset:8192\n"
"v_fma_f32 v222,v222,v212,v218\n"
MF "v[72:75],a[148:151],v[176:179],v[72:75]\n"
"s_mul_i32 s59,s59,40960\n"
TR "a[244:245],v3 offset:8192\n"
"v_fma_f32 v223,v223,v212,v218\n"
MF "v[72:75],a[188:191],v[180:183],v[72:75]\n"
"s_add_u32 s96,s96,s59\n"
TR "a[246:247],v3 offset:10240\n"
"v_fma_f32 v224,v224,v212,v218\n"
MF "v[72:75],a[228:231],v[184:187],v[72:75]\n"
"s_lshl_b32 s59,s95,7\n"
TR "v[142:143],v5 offset:10240\n"
"v_fma_f32 v225,v225,v212,v218\n"
MF "v[80:83],a[232:235],v[184:187],v[80:83]\n"
"s_add_u32 s96,s96,s59\n"
TR "v[144:145],v2 offset:49152\n"
"v_fma_f32 v226,v226,v212,v218\n"
MF "v[80:83],a[192:195],v[180:183],v[80:83]\n"
"s_lshl_b32 s2,s92,10\n"
TR "v[146:147],v2 offset:51200\n"
"v_fma_f32 v227,v227,v212,v218\n"
MF "v[80:83],a[152:155],v[176:179],v[80:83]\n"
"s_add_u32 s2,s2,s93\n"
TR "v[148:149],v2 offset:53248\n"
CV "v220,v220,v221\n"
MF "v[88:91],a[156:159],v[176:179],v[88:91]\n"
"s_mov_b32 s0,s96\n"
TR "v[150:151],v2 offset:55296\n"
CV "v221,v222,v223\n"
MF "v[88:91],a[196:199],v[180:183],v[88:91]\n"
TR "v[152:153],v2 offset:57344\n"
"s_waitcnt lgkmcnt(0)\n"
MF "v[88:91],a[236:239],v[184:187],v[88:91]\n"
"s_barrier\n"
MF "v[92:95],a[236:239],a[244:247],v[92:95]\n"
"s_sub_u32 s65,s61,9\n"
TR "v[154:155],v2 offset:59392\n"
CV "v222,v224,v225\n"
MF "v[92:95],a[196:199],a[240:243],v[92:95]\n"
"s_lshl_b32 s65,s65,7\n"
TR "v[156:157],v6\n"
CV "v223,v226,v227\n"
MF "v[92:95],a[156:159],v[188:191],v[92:95]\n"
"s_mov_b32 s64,s61\n"
TR "v[158:159],v6 offset:2048\n"
AW "a56,v220\n"
MF "v[84:87],a[152:155],v[188:191],v[84:87]\n"
"s_add_u32 s92,s92,s4\n"
TR "v[160:161],v6 offset:4096\n"
AW "a57,v221\n"
MF "v[84:87],a[192:195],a[240:243],v[84:87]\n"
"s_mul_i32 s95,s92,3277\n"
TR "v[162:163],v6 offset:6144\n"
AW "a58,v222\n"
MF "v[84:87],a[232:235],a[244:247],v[84:87]\n"
"s_add_u32 s80,s89,122880\n"
"s_add_u32 m0,s5,0\n"
TR "v[164:165],v6 offset:8192\n"
MF "v[76:79],a[228:231],a[244:247],v[76:79]\n"
LD "v192,s[48:51],s80 offen lds\n"
MF "v[76:79],a[188:191],a[240:243],v[76:79]\n"
".Lpg25h:\n"
"s_cbranch_execz .Lpg29h\n"
"s_lshr_b32 s95,s95,16\n"
TR "v[166:167],v6 offset:10240\n"
AW "a59,v223\n"
MF "v[76:79],a[148:151],v[188:191],v[76:79]\n"
"s_mul_i32 s59,s95,20\n"
TR "v[168:169],v3 offset:49152\n"
AR "v228,a8\n"
MF "v[68:71],a[144:147],v[188:191],v[68:71]\n"
"s_sub_u32 s59,s92,s59\n"
TR "v[170:171],v3 offset:51200\n"
AR "v229,a9\n"
MF "v[68:71],a[184:187],a[240:243],v[68:71]\n"
LD "v193,s[48:51],s80 offen offset:1024 lds\n"
MF "v[68:71],a[224:227],a[244:247],v[68:71]\n"
"s_cmp_ge_u32 s59,10\n"
"s_cselect_b32 s96,4294557760,0\n"
TR "v[172:173],v3 offset:53248\n"
MF "v[60:63],a[220:223],a[244:247],v[60:63]\n"
"s_add_u32 s96,s96,1024\n"
TR "v[174:175],v3 offset:55296\n"
AR "v230,a10\n"
MF "v[60:63],a[180:183],a[240:243],v[60:63]\n"
"s_mul_i32 s59,s59,40960\n"
TR "v[204:205],v3 offset:57344\n"
AR "v231,a11\n"
MF "v[60:63],a[140:143],v[188:191],v[60:63]\n"
LD "v194,s[48:51],s80 offen offset:2048 lds\n"
MF "v[16:19],a[0:3],v[96:99],v[16:19]\n"
"s_add_u32 s96,s96,s59\n"
TR "v[206:207],v3 offset:59392\n"
AR "v232,a28\n"
MF "v[16:19],a[40:43],v[100:103],v[16:19]\n"
"s_lshl_b32 s59,s95,7\n"
TR "v[244:245],v7\n"
AR "v233,a29\n"
MF "v[16:19],a[80:83],v[104:107],v[16:19]\n"
"s_add_u32 s96,s96,s59\n"
TR "v[246:247],v7 offset:2048\n"
AR "v234,a30\n"
MF "v[16:19],a[20:23],v[108:111],v[16:19]\n"
LD "v195,s[48:51],s80 offen offset:3072 lds\n"
MF "v[16:19],a[60:63],v[112:115],v[16:19]\n"
"s_lshl_b32 s3,s92,10\n"
TR "v[248:249],v7 offset:4096\n"
AR "v235,a31\n"
MF "v[16:19],a[100:103],v[116:119],v[16:19]\n"
"s_nop 0\n"
TR "v[250:251],v7 offset:6144\n"
"ds_read_b128 a[28:31],v236 offset:32768\n"
MF "v[20:23],a[100:103],v[140:143],v[20:23]\n"
"s_add_u32 m0,s5,4096\n"
TR "v[238:239],v7 offset:8192\n"
"v_fma_f32 v228,v228,v210,v216\n"
MF "v[20:23],a[60:63],v[136:139],v[20:23]\n"
LD "v196,s[48:51],s80 offen lds\n"
MF "v[20:23],a[20:23],v[132:135],v[20:23]\n"
"s_add_u32 s3,s3,s93\n"
TR "v[240:241],v7 offset:10240\n"
"v_fma_f32 v229,v229,v210,v216\n"
MF "v[20:23],a[80:83],v[128:131],v[20:23]\n"
"s_cmp_lt_u32 s92,40\n"
"s_cselect_b32 s3,s3,0xf0000000\n"
"v_fma_f32 v230,v230,v210,v216\n"
MF "v[20:23],a[40:43],v[124:127],v[20:23]\n"
"s_cmp_lt_u32 s92,40\n"
"s_cselect_b32 s1,s96,0xf0000000\n"
"v_fma_f32 v231,v231,v210,v216\n"
MF "v[20:23],a[0:3],v[120:123],v[20:23]\n"
"s_waitcnt lgkmcnt(13)\n"
LD "v197,s[48:51],s80 offen offset:1024 lds\n"
MF "v[56:59],a[0:3],v[144:147],v[56:59]\n"
"s_sub_u32 s92,s92,s4\n"
"v_fma_f32 v232,v232,v210,v216\n"
"v_fma_f32 v233,v233,v210,v216\n"
MF "v[56:59],a[40:43],v[148:151],v[56:59]\n"
"s_sub_u32 s2,s2,204800\n"
"v_fma_f32 v234,v234,v210,v216\n"
"v_fma_f32 v235,v235,v210,v216\n"
MF "v[56:59],a[80:83],v[152:155],v[56:59]\n"
"s_mov_b32 s92,64\n"
CV "v228,v228,v229\n"
CV "v229,v230,v231\n"
MF "v[56:59],a[20:23],v[156:159],v[56:59]\n"
CV "v230,v232,v233\n"
CV "v231,v234,v235\n"
MF "v[56:59],a[60:63],v[160:163],v[56:59]\n"
AW "a8,v228\n"
AW "a9,v229\n"
MF "v[56:59],a[100:103],v[164:167],v[56:59]\n"
"s_waitcnt lgkmcnt(0)\n"
AW "a10,v230\n"
AW "a11,v231\n"
MF "v[60:63],a[100:103],v[238:241],v[60:63]\n"
AR "v220,a88\n"
AR "v221,a89\n"
MF "v[60:63],a[60:63],v[248:251],v[60:63]\n"
AR "v222,a90\n"
AR "v223,a91\n"
MF "v[60:63],a[20:23],v[244:247],v[60:63]\n"
AR "v224,a108\n"
AR "v225,a109\n"
MF "v[60:63],a[80:83],v[204:207],v[60:63]\n"
AR "v226,a110\n"
AR "v227,a111\n"
MF "v[60:63],a[40:43],v[172:175],v[60:63]\n"
"ds_read_b128 a[108:111],v237 offset:8192\n"
"v_fma_f32 v220,v220,v210,v216\n"
MF "v[60:63],a[0:3],v[168:171],v[60:63]\n"
"v_fma_f32 v221,v221,v210,v216\n"
"v_fma_f32 v222,v222,v210,v216\n"
MF "v[68:71],a[4:7],v[168:171],v[68:71]\n"
"v_fma_f32 v223,v223,v210,v216\n"
"v_fma_f32 v224,v224,v210,v216\n"
MF "v[68:71],a[44:47],v[172:175],v[68:71]\n"
"v_fma_f32 v225,v225,v210,v216\n"
"v_fma_f32 v226,v226,v210,v216\n"
MF "v[68:71],a[84:87],v[204:207],v[68:71]\n"
"v_fma_f32 v227,v227,v210,v216\n"
CV "v220,v220,v221\n"
MF "v[68:71],a[24:27],v[244:247],v[68:71]\n"
CV "v221,v222,v223\n"
CV "v222,v224,v225\n"
MF "v[68:71],a[64:67],v[248:251],v[68:71]\n"
CV "v223,v226,v227\n"
AW "a88,v220\n"
MF "v[68:71],a[104:107],v[238:241],v[68:71]\n"
"s_waitcnt lgkmcnt(0)\n"
AW "a89,v221\n"
AW "a90,v222\n"
MF "v[64:67],a[104:107],v[164:167],v[64:67]\n"
"s_barrier\n"
MF "v[64:67],a[64:67],v[160:163],v[64:67]\n"
"s_add_u32 m0,s5,49152\n"
AW "a91,v223\n"
AR "v228,a12\n"
MF "v[64:67],a[24:27],v[156:159],v[64:67]\n"
LD "v192,s[48:51],s88 offen lds\n"
MF "v[64:67],a[84:87],v[152:155],v[64:67]\n"
AR "v229,a13\n"
AR "v230,a14\n"
MF "v[64:67],a[44:47],v[148:151],v[64:67]\n"
LD "v193,s[48:51],s88 offen offset:1024 lds\n"
MF "v[64:67],a[4:7],v[144:147],v[64:67]\n"
AR "v231,a15\n"
AR "v232,a32\n"
MF "v[28:31],a[4:7],v[120:123],v[28:31]\n"
AR "v233,a33\n"
AR "v234,a34\n"
MF "v[28:31],a[44:47],v[124:127],v[28:31]\n"
LD "v194,s[48:51],s88 offen offset:2048 lds\n"
MF "v[28:31],a[84:87],v[128:131],v[28:31]\n"
AR "v235,a35\n"
"v_fma_f32 v228,v228,v211,v217\n"
MF "v[28:31],a[24:27],v[132:135],v[28:31]\n"
"ds_read_b128 a[32:35],v236 offset:36864\n"
"v_fma_f32 v229,v229,v211,v217\n"
MF "v[28:31],a[64:67],v[136:139],v[28:31]\n"
LD "v195,s[48:51],s88 offen offset:3072 lds\n"
MF "v[28:31],a[104:107],v[140:143],v[28:31]\n"
"v_fma_f32 v230,v230,v211,v217\n"
"v_fma_f32 v231,v231,v211,v217\n"
MF "v[24:27],a[104:107],v[116:119],v[24:27]\n"
"v_fma_f32 v232,v232,v211,v217\n"
"v_fma_f32 v233,v233,v211,v217\n"
MF "v[24:27],a[64:67],v[112:115],v[24:27]\n"
"v_fma_f32 v234,v234,v211,v217\n"
"v_fma_f32 v235,v235,v211,v217\n"
MF "v[24:27],a[24:27],v[108:111],v[24:27]\n"
CV "v228,v228,v229\n"
CV "v229,v230,v231\n"
MF "v[24:27],a[84:87],v[104:107],v[24:27]\n"
CV "v230,v232,v233\n"
CV "v231,v234,v235\n"
MF "v[24:27],a[44:47],v[100:103],v[24:27]\n"
AW "a12,v228\n"
AW "a13,v229\n"
MF "v[24:27],a[4:7],v[96:99],v[24:27]\n"
AW "a14,v230\n"
AW "a15,v231\n"
MF "v[32:35],a[8:11],v[96:99],v[32:35]\n"
AR "v220,a92\n"
AR "v221,a93\n"
MF "v[32:35],a[48:51],v[100:103],v[32:35]\n"
AR "v222,a94\n"
AR "v223,a95\n"
MF "v[32:35],a[88:91],v[104:107],v[32:35]\n"
AR "v224,a112\n"
AR "v225,a113\n"
MF "v[32:35],a[28:31],v[108:111],v[32:35]\n"
AR "v226,a114\n"
AR "v227,a115\n"
MF "v[32:35],a[68:71],v[112:115],v[32:35]\n"
"ds_read_b128 a[112:115],v237 offset:12288\n"
"v_fma_f32 v220,v220,v211,v217\n"
MF "v[32:35],a[108:111],v[116:119],v[32:35]\n"
"v_fma_f32 v221,v221,v211,v217\n"
"v_fma_f32 v222,v222,v211,v217\n"
MF "v[36:39],a[108:111],v[140:143],v[36:39]\n"
"v_fma_f32 v223,v223,v211,v217\n"
"v_fma_f32 v224,v224,v211,v217\n"
MF "v[36:39],a[68:71],v[136:139],v[36:39]\n"
"v_fma_f32 v225,v225,v211,v217\n"
"v_fma_f32 v226,v226,v211,v217\n"
MF "v[36:39],a[28:31],v[132:135],v[36:39]\n"
"v_fma_f32 v227,v227,v211,v217\n"
CV "v220,v220,v221\n"
MF "v[36:39],a[88:91],v[128:131],v[36:39]\n"
CV "v221,v222,v223\n"
CV "v222,v224,v225\n"
MF "v[36:39],a[48:51],v[124:127],v[36:39]\n"
CV "v223,v226,v227\n"
AW "a92,v220\n"
MF "v[36:39],a[8:11],v[120:123],v[36:39]\n"
AW "a93,v221\n"
AW "a94,v222\n"
MF "v[72:75],a[8:11],v[144:147],v[72:75]\n"
AW "a95,v223\n"
AR "v228,a16\n"
MF "v[72:75],a[48:51],v[148:151],v[72:75]\n"
AR "v229,a17\n"
AR "v230,a18\n"
MF "v[72:75],a[88:91],v[152:155],v[72:75]\n"
AR "v231,a19\n"
AR "v232,a36\n"
MF "v[72:75],a[28:31],v[156:159],v[72:75]\n"
AR "v233,a37\n"
AR "v234,a38\n"
MF "v[72:75],a[68:71],v[160:163],v[72:75]\n"
AR "v235,a39\n"
"v_fma_f32 v228,v228,v212,v218\n"
MF "v[72:75],a[108:111],v[164:167],v[72:75]\n"
"ds_read_b128 a[36:39],v236 offset:40960\n"
"v_fma_f32 v229,v229,v212,v218\n"
MF "v[76:79],a[108:111],v[238:241],v[76:79]\n"
"v_fma_f32 v230,v230,v212,v218\n"
"v_fma_f32 v231,v231,v212,v218\n"
MF "v[76:79],a[68:71],v[248:251],v[76:79]\n"
"v_fma_f32 v232,v232,v212,v218\n"
"v_fma_f32 v233,v233,v212,v218\n"
MF "v[76:79],a[28:31],v[244:247],v[76:79]\n"
"v_fma_f32 v234,v234,v212,v218\n"
"v_fma_f32 v235,v235,v212,v218\n"
MF "v[76:79],a[88:91],v[204:207],v[76:79]\n"
CV "v228,v228,v229\n"
CV "v229,v230,v231\n"
MF "v[76:79],a[48:51],v[172:175],v[76:79]\n"
CV "v230,v232,v233\n"
CV "v231,v234,v235\n"
MF "v[76:79],a[8:11],v[168:171],v[76:79]\n"
"s_waitcnt lgkmcnt(1)\n"
AW "a16,v228\n"
AW "a17,v229\n"
MF "v[84:87],a[12:15],v[168:171],v[84:87]\n"
AW "a18,v230\n"
AW "a19,v231\n"
MF "v[84:87],a[52:55],v[172:175],v[84:87]\n"
AR "v220,a96\n"
AR "v221,a97\n"
MF "v[84:87],a[92:95],v[204:207],v[84:87]\n"
AR "v222,a98\n"
AR "v223,a99\n"
MF "v[84:87],a[32:35],v[244:247],v[84:87]\n"
AR "v224,a116\n"
AR "v225,a117\n"
MF "v[84:87],a[72:75],v[248:251],v[84:87]\n"
AR "v226,a118\n"
AR "v227,a119\n"
MF "v[84:87],a[112:115],v[238:241],v[84:87]\n"
"ds_read_b128 a[116:119],v237 offset:16384\n"
"v_fma_f32 v220,v220,v212,v218\n"
MF "v[80:83],a[112:115],v[164:167],v[80:83]\n"
"v_fma_f32 v221,v221,v212,v218\n"
"v_fma_f32 v222,v222,v212,v218\n"
MF "v[80:83],a[72:75],v[160:163],v[80:83]\n"
"v_fma_f32 v223,v223,v212,v218\n"
"v_fma_f32 v224,v224,v212,v218\n"
MF "v[80:83],a[32:35],v[156:159],v[80:83]\n"
"v_fma_f32 v225,v225,v212,v218\n"
"v_fma_f32 v226,v226,v212,v218\n"
MF "v[80:83],a[92:95],v[152:155],v[80:83]\n"
"v_fma_f32 v227,v227,v212,v218\n"
CV "v220,v220,v221\n"
MF "v[80:83],a[52:55],v[148:151],v[80:83]\n"
CV "v221,v222,v223\n"
CV "v222,v224,v225\n"
MF "v[80:83],a[12:15],v[144:147],v[80:83]\n"
CV "v223,v226,v227\n"
AW "a96,v220\n"
MF "v[44:47],a[12:15],v[120:123],v[44:47]\n"
AW "a97,v221\n"
AW "a98,v222\n"
MF "v[44:47],a[52:55],v[124:127],v[44:47]\n"
"s_add_u32 m0,s5,73728\n"
AW "a99,v223\n"
"v_lshrrev_b32 v13,2,v9\n"
MF "v[44:47],a[92:95],v[128:131],v[44:47]\n"
"s_nop 0\n"
"ds_write_addtid_b32 v16\n"
"ds_write_addtid_b32 v17 offset:256\n"
MF "v[44:47],a[32:35],v[132:135],v[44:47]\n"
"s_nop 0\n"
"ds_write_addtid_b32 v18 offset:512\n"
"ds_write_addtid_b32 v19 offset:768\n"
MF "v[44:47],a[72:75],v[136:139],v[44:47]\n"
"s_nop 0\n"
"ds_write_addtid_b32 v20 offset:1024\n"
"ds_write_addtid_b32 v21 offset:1280\n"
MF "v[44:47],a[112:115],v[140:143],v[44:47]\n"
"s_nop 0\n"
"ds_write_addtid_b32 v22 offset:1536\n"
"ds_write_addtid_b32 v23 offset:1792\n"
MF "v[40:43],a[112:115],v[116:119],v[40:43]\n"
"s_nop 0\n"
"ds_write_addtid_b32 v24 offset:2048\n"
"ds_write_addtid_b32 v25 offset:2304\n"
MF "v[40:43],a[72:75],v[112:115],v[40:43]\n"
"s_nop 0\n"
"ds_write_addtid_b32 v26 offset:2560\n"
"ds_write_addtid_b32 v27 offset:2816\n"
MF "v[40:43],a[32:35],v[108:111],v[40:43]\n"
"s_nop 0\n"
"ds_write_addtid_b32 v28 offset:3072\n"
"ds_write_addtid_b32 v29 offset:3328\n"
MF "v[40:43],a[92:95],v[104:107],v[40:43]\n"
"s_nop 0\n"
"ds_write_addtid_b32 v30 offset:3584\n"
"ds_write_addtid_b32 v31 offset:3840\n"
MF "v[40:43],a[52:55],v[100:103],v[40:43]\n"
"s_nop 0\n"
"ds_write_addtid_b32 v32 offset:4096\n"
"ds_write_addtid_b32 v33 offset:4352\n"
MF "v[40:43],a[12:15],v[96:99],v[40:43]\n"
"s_waitcnt lgkmcnt(15)\n"
"ds_write_addtid_b32 v34 offset:4608\n"
"ds_write_addtid_b32 v35 offset:4864\n"
MF "v[48:51],a[16:19],v[96:99],v[48:51]\n"
"s_nop 0\n"
"ds_write_addtid_b32 v36 offset:49152\n"
"ds_write_addtid_b32 v37 offset:49408\n"
MF "v[48:51],a[56:59],v[100:103],v[48:51]\n"
"s_nop 0\n"
"ds_write_addtid_b32 v38 offset:49664\n"
"ds_write_addtid_b32 v39 offset:49920\n"
MF "v[48:51],a[96:99],v[104:107],v[48:51]\n"
"s_nop 0\n"
"ds_write_addtid_b32 v44 offset:51200\n"
"ds_write_addtid_b32 v45 offset:51456\n"
MF "v[48:51],a[36:39],v[108:111],v[48:51]\n"
"s_nop 0\n"
"ds_write_addtid_b32 v46 offset:51712\n"
"ds_write_addtid_b32 v47 offset:51968\n"
MF "v[48:51],a[76:79],v[112:115],v[48:51]\n"
"s_nop 0\n"
"ds_write_addtid_b32 v40 offset:50176\n"
"ds_write_addtid_b32 v41 offset:50432\n"
MF "v[48:51],a[116:119],v[116:119],v[48:51]\n"
"s_nop 0\n"
"ds_write_addtid_b32 v42 offset:50688\n"
"ds_write_addtid_b32 v43 offset:50944\n"
MF "v[52:55],a[116:119],v[140:143],v[52:55]\n"
"v_add_u32 v13,s58,v13\n"
AR "v8,a252\n"
MF "v[52:55],a[76:79],v[136:139],v[52:55]\n"
"s_waitcnt vmcnt(10) lgkmcnt(0)\n"
AR "v11,a253\n"
MF "v[52:55],a[36:39],v[132:135],v[52:55]\n"
"s_barrier\n"
MF "v[52:55],a[96:99],v[128:131],v[52:55]\n"
"s_nop 0\n"
TR "v[176:177],v0 offset:24576\n"
TR "v[178:179],v0 offset:26624\n"
MF "v[52:55],a[56:59],v[124:127],v[52:55]\n"
"s_nop 0\n"
TR "v[180:181],v0 offset:28672\n"
TR "v[182:183],v0 offset:30720\n"
MF "v[52:55],a[16:19],v[120:123],v[52:55]\n"
"s_nop 0\n"
TR "v[184:185],v0 offset:32768\n"
TR "v[186:187],v0 offset:34816\n"
MF "v[88:91],a[16:19],v[144:147],v[88:91]\n"
"s_nop 0\n"
"ds_write_addtid_b32 v48 offset:52224\n"
"ds_write_addtid_b32 v49 offset:52480\n"
MF "v[88:91],a[56:59],v[148:151],v[88:91]\n"
"s_nop 0\n"
"ds_write_addtid_b32 v50 offset:52736\n"
"ds_write_addtid_b32 v51 offset:52992\n"
MF "v[88:91],a[96:99],v[152:155],v[88:91]\n"
"s_nop 0\n"
"ds_write_addtid_b32 v52 offset:53248\n"
"ds_write_addtid_b32 v53 offset:53504\n"
MF "v[88:91],a[36:39],v[156:159],v[88:91]\n"
"s_nop 0\n"
"ds_write_addtid_b32 v54 offset:53760\n"
"ds_write_addtid_b32 v55 offset:54016\n"
MF "v[88:91],a[76:79],v[160:163],v[88:91]\n"
MF "v[88:91],a[116:119],v[164:167],v[88:91]\n"
MF "v[92:95],a[116:119],v[238:241],v[92:95]\n"
MF "v[92:95],a[76:79],v[248:251],v[92:95]\n"
"s_waitcnt lgkmcnt(0)\n"
MF "v[92:95],a[36:39],v[244:247],v[92:95]\n"
"s_barrier\n"
MF "v[92:95],a[96:99],v[204:207],v[92:95]\n"
"ds_read2st64_b32 v[48:49],v13 offset0:204 offset1:205\n"
MF "v[92:95],a[56:59],v[172:175],v[92:95]\n"
"ds_read2st64_b32 v[50:51],v13 offset0:206 offset1:207\n"
MF "v[92:95],a[16:19],v[168:171],v[92:95]\n"
"ds_read2st64_b32 v[52:53],v13 offset0:208 offset1:209\n"
"ds_read2st64_b32 v[54:55],v13 offset0:210 offset1:211\n"
"ds_read2st64_b32 v[40:41],v13 offset0:196 offset1:197\n"
"ds_read2st64_b32 v[42:43],v13 offset0:198 offset1:199\n"
".Lps5:\n"
MF "v[96:99],a[0:3],v[176:179],v[252:255]\n"
"s_nop 0\n"
TR "v[188:189],v1 offset:24576\n"
TR "v[190:191],v1 offset:26624\n"
MF "v[96:99],a[40:43],v[180:183],v[96:99]\n"
"s_nop 0\n"
TR "a[240:241],v1 offset:28672\n"
TR "a[242:243],v1 offset:30720\n"
MF "v[96:99],a[80:83],v[184:187],v[96:99]\n"
"s_add_u32 m0,s5,53248\n"
TR "a[244:245],v1 offset:32768\n"
TR "a[246:247],v1 offset:34816\n"
MF "v[104:107],a[84:87],v[184:187],v[252:255]\n"
"s_waitcnt lgkmcnt(8)\n"
LD "v196,s[48:51],s88 offen lds\n"
MF "v[104:107],a[44:47],v[180:183],v[104:107]\n"
"ds_read2st64_b32 v[44:45],v13 offset0:200 offset1:201\n"
"v_add_f32 v88,v88,v48\n"
MF "v[104:107],a[4:7],v[176:179],v[104:107]\n"
"ds_read2st64_b32 v[46:47],v13 offset0:202 offset1:203\n"
"v_add_f32 v89,v89,v49\n"
MF "v[112:115],a[8:11],v[176:179],v[252:255]\n"
"ds_read2st64_b32 v[32:33],v13 offset0:16 offset1:17\n"
"v_add_f32 v90,v90,v50\n"
MF "v[112:115],a[48:51],v[180:183],v[112:115]\n"
LD "v197,s[48:51],s88 offen offset:1024 lds\n"
MF "v[112:115],a[88:91],v[184:187],v[112:115]\n"
"ds_read2st64_b32 v[34:35],v13 offset0:18 offset1:19\n"
"v_add_f32 v91,v91,v51\n"
MF "v[120:123],a[92:95],v[184:187],v[252:255]\n"
"ds_read2st64_b32 v[36:37],v13 offset0:192 offset1:193\n"
"v_add_f32 v92,v92,v52\n"
MF "v[120:123],a[52:55],v[180:183],v[120:123]\n"
"s_add_u32 s80,s88,122880\n"
"ds_read2st64_b32 v[38:39],v13 offset0:194 offset1:195\n"
"v_add_f32 v93,v93,v53\n"
MF "v[120:123],a[12:15],v[176:179],v[120:123]\n"
"s_add_u32 m0,s5,98304\n"
"ds_read2st64_b32 v[24:25],v13 offset0:8 offset1:9\n"
"v_add_f32 v94,v94,v54\n"
MF "v[128:131],a[16:19],v[176:179],v[252:255]\n"
LD "v192,s[48:51],s80 offen lds\n"
MF "v[128:131],a[56:59],v[180:183],v[128:131]\n"
"ds_read2st64_b32 v[26:27],v13 offset0:10 offset1:11\n"
"v_add_f32 v95,v95,v55\n"
MF "v[128:131],a[96:99],v[184:187],v[128:131]\n"
"s_waitcnt lgkmcnt(8)\n"
LD "v193,s[48:51],s80 offen offset:1024 lds\n"
MF "v[132:135],a[96:99],a[244:247],v[252:255]\n"
"s_nop 0\n"
TR "v[176:177],v2 offset:24576\n"
TR "v[178:179],v2 offset:26624\n"
MF "v[132:135],a[56:59],a[240:243],v[132:135]\n"
"s_nop 0\n"
TR "v[180:181],v2 offset:28672\n"
TR "v[182:183],v2 offset:30720\n"
MF "v[132:135],a[16:19],v[188:191],v[132:135]\n"
"s_nop 0\n"
TR "v[184:185],v2 offset:32768\n"
TR "v[186:187],v2 offset:34816\n"
MF "v[124:127],a[12:15],v[188:191],v[252:255]\n"
"ds_read2st64_b32 v[28:29],v13 offset0:12 offset1:13\n"
CV "v88,v88,v89\n"
MF "v[124:127],a[52:55],a[240:243],v[124:127]\n"
"ds_read2st64_b32 v[30:31],v13 offset0:14 offset1:15\n"
CV "v89,v90,v91\n"
MF "v[124:127],a[92:95],a[244:247],v[124:127]\n"
"ds_read2st64_b32 v[16:17],v13 offset0:0 offset1:1\n"
CV "v90,v92,v93\n"
MF "v[116:119],a[88:91],a[244:247],v[252:255]\n"
LD "v194,s[48:51],s80 offen offset:2048 lds\n"
MF "v[116:119],a[48:51],a[240:243],v[116:119]\n"
"s_waitcnt lgkmcnt(15)\n"
"ds_read2st64_b32 v[18:19],v13 offset0:2 offset1:3\n"
CV "v91,v94,v95\n"
MF "v[116:119],a[8:11],v[188:191],v[116:119]\n"
"ds_read2st64_b32 v[20:21],v13 offset0:4 offset1:5\n"
"v_add_f32 v80,v80,v40\n"
MF "v[108:111],a[4:7],v[188:191],v[252:255]\n"
"s_add_u32 s87,s90,4096\n"
"ds_read2st64_b32 v[22:23],v13 offset0:6 offset1:7\n"
"v_add_f32 v81,v81,v41\n"
MF "v[108:111],a[44:47],a[240:243],v[108:111]\n"
"buffer_store_dwordx4 v[88:91],v9,s[40:43],s87 offen sc1\n"
MF "v[108:111],a[84:87],a[244:247],v[108:111]\n"
LD "v195,s[48:51],s80 offen offset:3072 lds\n"
MF "v[100:103],a[80:83],a[244:247],v[252:255]\n"
"v_add_f32 v82,v82,v42\n"
"v_add_f32 v83,v83,v43\n"
MF "v[100:103],a[40:43],a[240:243],v[100:103]\n"
"s_add_u32 m0,s5,102400\n"
"v_add_f32 v84,v84,v44\n"
"v_add_f32 v85,v85,v45\n"
MF "v[100:103],a[0:3],v[188:191],v[100:103]\n"
"s_waitcnt lgkmcnt(6)\n"
LD "v196,s[48:51],s80 offen lds\n"
MF "v[136:139],a[0:3],v[176:179],v[252:255]\n"
"s_nop 0\n"
TR "v[188:189],v3 offset:24576\n"
TR "v[190:191],v3 offset:26624\n"
MF "v[136:139],a[40:43],v[180:183],v[136:139]\n"
"s_nop 0\n"
TR "a[240:241],v3 offset:28672\n"
TR "a[242:243],v3 offset:30720\n"
MF "v[136:139],a[80:83],v[184:187],v[136:139]\n"
"s_nop 0\n"
TR "a[244:245],v3 offset:32768\n"
TR "a[246:247],v3 offset:34816\n"
MF "v[144:147],a[84:87],v[184:187],v[252:255]\n"
"v_add_f32 v86,v86,v46\n"
"v_add_f32 v87,v87,v47\n"
MF "v[144:147],a[44:47],v[180:183],v[144:147]\n"
CV "v80,v80,v81\n"
CV "v81,v82,v83\n"
MF "v[144:147],a[4:7],v[176:179],v[144:147]\n"
LD "v197,s[48:51],s80 offen offset:1024 lds\n"
MF "v[152:155],a[8:11],v[176:179],v[252:255]\n"
CV "v82,v84,v85\n"
CV "v83,v86,v87\n"
MF "v[152:155],a[48:51],v[180:183],v[152:155]\n"
"buffer_store_dwordx4 v[80:83],v9,s[40:43],s90 offen offset:3072 sc1\n"
MF "v[152:155],a[88:91],v[184:187],v[152:155]\n"
"v_add_f32 v72,v72,v32\n"
"v_add_f32 v73,v73,v33\n"
MF "v[160:163],a[92:95],v[184:187],v[252:255]\n"
"v_add_f32 v74,v74,v34\n"
"v_add_f32 v75,v75,v35\n"
MF "v[160:163],a[52:55],v[180:183],v[160:163]\n"
"v_add_f32 v76,v76,v36\n"
"v_add_f32 v77,v77,v37\n"
MF "v[160:163],a[12:15],v[176:179],v[160:163]\n"
"v_add_f32 v78,v78,v38\n"
"v_add_f32 v79,v79,v39\n"
MF "v[168:171],a[16:19],v[176:179],v[252:255]\n"
CV "v72,v72,v73\n"
CV "v73,v74,v75\n"
MF "v[168:171],a[56:59],v[180:183],v[168:171]\n"
"s_waitcnt vmcnt(14) lgkmcnt(0)\n"
CV "v74,v76,v77\n"
CV "v75,v78,v79\n"
MF "v[168:171],a[96:99],v[184:187],v[168:171]\n"
"s_barrier\n"
MF "v[172:175],a[96:99],a[244:247],v[252:255]\n"
".Lpg26h:\n"
"s_cbranch_execz .Lpg30h\n"
TR "v[176:177],v0\n"
MF "v[172:175],a[56:59],a[240:243],v[172:175]\n"
"s_add_u32 s89,s89,s65\n"
TR "v[178:179],v0 offset:2048\n"
MF "v[172:175],a[16:19],v[188:191],v[172:175]\n"
"buffer_store_dwordx4 v[72:75],v9,s[40:43],s90 offen offset:2048 sc1\n"
MF "v[164:167],a[12:15],v[188:191],v[252:255]\n"
"s_add_u32 s88,s88,s65\n"
TR "v[180:181],v0 offset:4096\n"
MF "v[164:167],a[52:55],a[240:243],v[164:167]\n"
"s_add_u32 m0,s5,24576\n"
TR "v[182:183],v0 offset:6144\n"
MF "v[164:167],a[92:95],a[244:247],v[164:167]\n"
LD "v192,s[48:51],s89 offen lds\n"
MF "v[156:159],a[88:91],a[244:247],v[252:255]\n"
TR "v[184:185],v0 offset:8192\n"
MF "v[156:159],a[48:51],a[240:243],v[156:159]\n"
TR "v[186:187],v0 offset:10240\n"
MF "v[156:159],a[8:11],v[188:191],v[156:159]\n"
LD "v193,s[48:51],s89 offen offset:1024 lds\n"
MF "v[148:151],a[4:7],v[188:191],v[252:255]\n"
"v_add_f32 v64,v64,v24\n"
"v_add_f32 v65,v65,v25\n"
MF "v[148:151],a[44:47],a[240:243],v[148:151]\n"
"v_add_f32 v66,v66,v26\n"
"v_add_f32 v67,v67,v27\n"
MF "v[148:151],a[84:87],a[244:247],v[148:151]\n"
"v_add_f32 v68,v68,v28\n"
"v_add_f32 v69,v69,v29\n"
MF "v[140:143],a[80:83],a[244:247],v[252:255]\n"
"v_add_f32 v70,v70,v30\n"
"v_add_f32 v71,v71,v31\n"
MF "v[140:143],a[40:43],a[240:243],v[140:143]\n"
CV "v64,v64,v65\n"
CV "v65,v66,v67\n"
MF "v[140:143],a[0:3],v[188:191],v[140:143]\n"
"s_waitcnt lgkmcnt(0)\n"
CV "v66,v68,v69\n"
CV "v67,v70,v71\n"
MF "v[96:99],a[120:123],v[176:179],v[96:99]\n"
TR "v[188:189],v1\n"
MF "v[96:99],a[160:163],v[180:183],v[96:99]\n"
TR "v[190:191],v1 offset:2048\n"
MF "v[96:99],a[200:203],v[184:187],v[96:99]\n"
"buffer_store_dwordx4 v[64:67],v9,s[40:43],s90 offen offset:1024 sc1\n"
MF "v[104:107],a[204:207],v[184:187],v[104:107]\n"
TR "a[240:241],v1 offset:4096\n"
MF "v[104:107],a[164:167],v[180:183],v[104:107]\n"
TR "a[242:243],v1 offset:6144\n"
MF "v[104:107],a[124:127],v[176:179],v[104:107]\n"
LD "v194,s[48:51],s89 offen offset:2048 lds\n"
MF "v[112:115],a[128:131],v[176:179],v[112:115]\n"
TR "a[244:245],v1 offset:8192\n"
MF "v[112:115],a[168:171],v[180:183],v[112:115]\n"
TR "a[246:247],v1 offset:10240\n"
MF "v[112:115],a[208:211],v[184:187],v[112:115]\n"
MF "v[120:123],a[212:215],v[184:187],v[120:123]\n"
MF "v[120:123],a[172:175],v[180:183],v[120:123]\n"
MF "v[120:123],a[132:135],v[176:179],v[120:123]\n"
LD "v195,s[48:51],s89 offen offset:3072 lds\n"
MF "v[128:131],a[136:139],v[176:179],v[128:131]\n"
"v_add_f32 v56,v56,v16\n"
"v_add_f32 v57,v57,v17\n"
MF "v[128:131],a[176:179],v[180:183],v[128:131]\n"
"v_add_f32 v58,v58,v18\n"
"v_add_f32 v59,v59,v19\n"
MF "v[128:131],a[216:219],v[184:187],v[128:131]\n"
"s_waitcnt lgkmcnt(0)\n"
"v_add_f32 v60,v60,v20\n"
"v_add_f32 v61,v61,v21\n"
MF "v[132:135],a[216:219],a[244:247],v[132:135]\n"
TR "v[176:177],v2\n"
"v_add_f32 v62,v62,v22\n"
MF "v[132:135],a[176:179],a[240:243],v[132:135]\n"
TR "v[178:179],v2 offset:2048\n"
"v_add_f32 v63,v63,v23\n"
MF "v[132:135],a[136:139],v[188:191],v[132:135]\n"
CV "v56,v56,v57\n"
CV "v57,v58,v59\n"
MF "v[124:127],a[132:135],v[188:191],v[124:127]\n"
TR "v[180:181],v2 offset:4096\n"
CV "v58,v60,v61\n"
MF "v[124:127],a[172:175],a[240:243],v[124:127]\n"
TR "v[182:183],v2 offset:6144\n"
CV "v59,v62,v63\n"
MF "v[124:127],a[212:215],a[244:247],v[124:127]\n"
"buffer_store_dwordx4 v[56:59],v9,s[40:43],s90 offen sc1\n"
MF "v[116:119],a[208:211],a[244:247],v[116:119]\n"
"s_add_u32 s90,s90,20480\n"
TR "v[184:185],v2 offset:8192\n"
MF "v[116:119],a[168:171],a[240:243],v[116:119]\n"
"s_add_u32 m0,s5,28672\n"
TR "v[186:187],v2 offset:10240\n"
MF "v[116:119],a[128:131],v[188:191],v[116:119]\n"
LD "v196,s[48:51],s89 offen lds\n"
MF "v[108:111],a[124:127],v[188:191],v[108:111]\n"
MF "v[108:111],a[164:167],a[240:243],v[108:111]\n"
"v_add_u32 v4,98304,v0\n"
"v_add_u32 v5,98304,v1\n"
MF "v[108:111],a[204:207],a[244:247],v[108:111]\n"
"v_add_u32 v6,98304,v2\n"
"v_add_u32 v7,98304,v3\n"
MF "v[100:103],a[200:203],a[244:247],v[100:103]\n"
"v_mov_b32 v15,0\n"
"v_lshrrev_b32 v242,5,v9\n"
MF "v[100:103],a[160:163],a[240:243],v[100:103]\n"
"v_min_u32 v242,11,v242\n"
"v_mul_u32_u24 v242,160,v242\n"
MF "v[100:103],a[120:123],v[188:191],v[100:103]\n"
"s_waitcnt lgkmcnt(0)\n"
"v_and_or_b32 v242,v9,16,v242\n"
"v_lshlrev_b32 v242,3,v242\n"
MF "v[136:139],a[120:123],v[176:179],v[136:139]\n"
TR "v[188:189],v3\n"
"v_bfe_u32 v224,v9,5,3\n"
MF "v[136:139],a[160:163],v[180:183],v[136:139]\n"
TR "v[190:191],v3 offset:2048\n"
"v_bfe_u32 v225,v9,8,2\n"
MF "v[136:139],a[200:203],v[184:187],v[136:139]\n"
LD "v197,s[48:51],s89 offen offset:1024 lds\n"
MF "v[144:147],a[204:207],v[184:187],v[144:147]\n"
TR "a[240:241],v3 offset:4096\n"
"v_lshlrev_b32 v225,1,v225\n"
MF "v[144:147],a[164:167],v[180:183],v[144:147]\n"
TR "a[242:243],v3 offset:6144\n"
"v_and_b32 v226,16,v9\n"
MF "v[144:147],a[124:127],v[176:179],v[144:147]\n"
"v_lshlrev_b32_e64 v226,v226,s9\n"
"v_sub_u32 v224,v224,v225\n"
MF "v[152:155],a[128:131],v[176:179],v[152:155]\n"
TR "a[244:245],v3 offset:8192\n"
"v_cmp_eq_u32 vcc,0,v224\n"
MF "v[152:155],a[168:171],v[180:183],v[152:155]\n"
TR "a[246:247],v3 offset:10240\n"
"v_cndmask_b32 v220,0,v226,vcc\n"
MF "v[152:155],a[208:211],v[184:187],v[152:155]\n"
"v_cmp_eq_u32 vcc,1,v224\n"
MF "v[160:163],a[212:215],v[184:187],v[160:163]\n"
"s_nop 0\n"
"v_cndmask_b32 v221,0,v226,vcc\n"
AW "a248,0\n"
MF "v[160:163],a[172:175],v[180:183],v[160:163]\n"
AW "a249,0\n"
AW "a250,v220\n"
MF "v[160:163],a[132:135],v[176:179],v[160:163]\n"
AW "a251,v221\n"
AW "a252,v220\n"
MF "v[168:171],a[136:139],v[176:179],v[168:171]\n"
AW "a253,v221\n"
AW "a254,0\n"
MF "v[168:171],a[176:179],v[180:183],v[168:171]\n"
"s_waitcnt vmcnt(17) lgkmcnt(0)\n"
AW "a255,0\n"
"v_bfe_u32 v12,v9,4,4\n"
MF "v[168:171],a[216:219],v[184:187],v[168:171]\n"
"s_barrier\n"
MF "v[172:175],a[216:219],a[244:247],v[172:175]\n"
"s_add_u32 s80,s89,122880\n"
TR "v[176:177],v0 offset:49152\n"
"v_lshlrev_b32 v12,2,v12\n"
MF "v[172:175],a[176:179],a[240:243],v[172:175]\n"
"s_add_u32 m0,s5,0\n"
TR "v[178:179],v0 offset:51200\n"
MF "v[172:175],a[136:139],v[188:191],v[172:175]\n"
LD "v192,s[48:51],s80 offen lds\n"
MF "v[164:167],a[132:135],v[188:191],v[164:167]\n"
"s_add_u32 s60,s64,1\n"
TR "v[180:181],v0 offset:53248\n"
MF "v[164:167],a[172:175],a[240:243],v[164:167]\n"
"s_cmp_eq_u32 s60,8\n"
"s_cselect_b32 s60,0,s60\n"
TR "v[182:183],v0 offset:55296\n"
MF "v[164:167],a[212:215],a[244:247],v[164:167]\n"
LD "v193,s[48:51],s80 offen offset:1024 lds\n"
MF "v[156:159],a[208:211],a[244:247],v[156:159]\n"
"s_cmp_eq_u32 s60,10\n"
"s_cselect_b32 s60,s61,s60\n"
TR "v[184:185],v0 offset:57344\n"
MF "v[156:159],a[168:171],a[240:243],v[156:159]\n"
"s_sub_u32 s65,s60,s64\n"
TR "v[186:187],v0 offset:59392\n"
MF "v[156:159],a[128:131],v[188:191],v[156:159]\n"
"s_lshl_b32 s65,s65,7\n"
MF "v[148:151],a[124:127],v[188:191],v[148:151]\n"
"s_mov_b32 s64,s60\n"
MF "v[148:151],a[164:167],a[240:243],v[148:151]\n"
MF "v[148:151],a[204:207],a[244:247],v[148:151]\n"
LD "v194,s[48:51],s80 offen offset:2048 lds\n"
MF "v[140:143],a[200:203],a[244:247],v[140:143]\n"
MF "v[140:143],a[160:163],a[240:243],v[140:143]\n"
MF "v[140:143],a[120:123],v[188:191],v[140:143]\n"
"s_waitcnt lgkmcnt(0)\n"
MF "v[96:99],a[20:23],v[176:179],v[96:99]\n"
TR "v[188:189],v1 offset:49152\n"
MF "v[96:99],a[60:63],v[180:183],v[96:99]\n"
TR "v[190:191],v1 offset:51200\n"
MF "v[96:99],a[100:103],v[184:187],v[96:99]\n"
LD "v195,s[48:51],s80 offen offset:3072 lds\n"
MF "v[104:107],a[104:107],v[184:187],v[104:107]\n"
TR "a[240:241],v1 offset:53248\n"
MF "v[104:107],a[64:67],v[180:183],v[104:107]\n"
"s_add_u32 m0,s5,4096\n"
TR "a[242:243],v1 offset:55296\n"
MF "v[104:107],a[24:27],v[176:179],v[104:107]\n"
LD "v196,s[48:51],s80 offen lds\n"
MF "v[112:115],a[28:31],v[176:179],v[112:115]\n"
TR "a[244:245],v1 offset:57344\n"
MF "v[112:115],a[68:71],v[180:183],v[112:115]\n"
TR "a[246:247],v1 offset:59392\n"
MF "v[112:115],a[108:111],v[184:187],v[112:115]\n"
MF "v[120:123],a[112:115],v[184:187],v[120:123]\n"
MF "v[120:123],a[72:75],v[180:183],v[120:123]\n"
MF "v[120:123],a[32:35],v[176:179],v[120:123]\n"
LD "v197,s[48:51],s80 offen offset:1024 lds\n"
MF "v[128:131],a[36:39],v[176:179],v[128:131]\n"
MF "v[128:131],a[76:79],v[180:183],v[128:131]\n"
MF "v[128:131],a[116:119],v[184:187],v[128:131]\n"
"s_waitcnt lgkmcnt(0)\n"
MF "v[132:135],a[116:119],a[244:247],v[132:135]\n"
TR "v[176:177],v2 offset:49152\n"
MF "v[132:135],a[76:79],a[240:243],v[132:135]\n"
TR "v[178:179],v2 offset:51200\n"
MF "v[132:135],a[36:39],v[188:191],v[132:135]\n"
MF "v[124:127],a[32:35],v[188:191],v[124:127]\n"
TR "v[180:181],v2 offset:53248\n"
MF "v[124:127],a[72:75],a[240:243],v[124:127]\n"
"s_add_u32 s62,s74,s68\n"
TR "v[182:183],v2 offset:55296\n"
MF "v[124:127],a[112:115],a[244:247],v[124:127]\n"
"buffer_load_dword v243,v242,s[48:51],s62 offen\n"
MF "v[116:119],a[108:111],a[244:247],v[116:119]\n"
TR "v[184:185],v2 offset:57344\n"
MF "v[116:119],a[68:71],a[240:243],v[116:119]\n"
TR "v[186:187],v2 offset:59392\n"
MF "v[116:119],a[28:31],v[188:191],v[116:119]\n"
MF "v[108:111],a[24:27],v[188:191],v[108:111]\n"
MF "v[108:111],a[64:67],a[240:243],v[108:111]\n"
MF "v[108:111],a[104:107],a[244:247],v[108:111]\n"
MF "v[100:103],a[100:103],a[244:247],v[100:103]\n"
MF "v[100:103],a[60:63],a[240:243],v[100:103]\n"
MF "v[100:103],a[20:23],v[188:191],v[100:103]\n"
"s_waitcnt lgkmcnt(0)\n"
MF "v[136:139],a[20:23],v[176:179],v[136:139]\n"
TR "v[188:189],v3 offset:49152\n"
MF "v[136:139],a[60:63],v[180:183],v[136:139]\n"
TR "v[190:191],v3 offset:51200\n"
MF "v[136:139],a[100:103],v[184:187],v[136:139]\n"
"v_mov_b32 v14,s76\n"
"s_add_u32 s76,s76,1\n"
MF "v[144:147],a[104:107],v[184:187],v[144:147]\n"
TR "a[240:241],v3 offset:53248\n"
MF "v[144:147],a[64:67],v[180:183],v[144:147]\n"
TR "a[242:243],v3 offset:55296\n"
MF "v[144:147],a[24:27],v[176:179],v[144:147]\n"
MF "v[152:155],a[28:31],v[176:179],v[152:155]\n"
TR "a[244:245],v3 offset:57344\n"
MF "v[152:155],a[68:71],v[180:183],v[152:155]\n"
TR "a[246:247],v3 offset:59392\n"
MF "v[152:155],a[108:111],v[184:187],v[152:155]\n"
MF "v[160:163],a[112:115],v[184:187],v[160:163]\n"
MF "v[160:163],a[72:75],v[180:183],v[160:163]\n"
MF "v[160:163],a[32:35],v[176:179],v[160:163]\n"
MF "v[168:171],a[36:39],v[176:179],v[168:171]\n"
MF "v[168:171],a[76:79],v[180:183],v[168:171]\n"
"s_waitcnt vmcnt(9) lgkmcnt(0)\n"
MF "v[168:171],a[116:119],v[184:187],v[168:171]\n"
"s_barrier\n"
MF "v[172:175],a[116:119],a[244:247],v[172:175]\n"
TR "v[176:177],v4\n"
MF "v[172:175],a[76:79],a[240:243],v[172:175]\n"
TR "v[178:179],v4 offset:2048\n"
MF "v[172:175],a[36:39],v[188:191],v[172:175]\n"
"buffer_store_dword v14,v15,s[40:43],s94 offen sc1\n"
MF "v[164:167],a[32:35],v[188:191],v[164:167]\n"
TR "v[180:181],v4 offset:4096\n"
MF "v[164:167],a[72:75],a[240:243],v[164:167]\n"
TR "v[182:183],v4 offset:6144\n"
MF "v[164:167],a[112:115],a[244:247],v[164:167]\n"
MF "v[156:159],a[108:111],a[244:247],v[156:159]\n"
TR "v[184:185],v4 offset:8192\n"
MF "v[156:159],a[68:71],a[240:243],v[156:159]\n"
TR "v[186:187],v4 offset:10240\n"
MF "v[156:159],a[28:31],v[188:191],v[156:159]\n"
MF "v[148:151],a[24:27],v[188:191],v[148:151]\n"
MF "v[148:151],a[64:67],a[240:243],v[148:151]\n"
"s_add_u32 m0,s5,73728\n"
MF "v[148:151],a[104:107],a[244:247],v[148:151]\n"
MF "v[140:143],a[100:103],a[244:247],v[140:143]\n"
MF "v[140:143],a[60:63],a[240:243],v[140:143]\n"
MF "v[140:143],a[20:23],v[188:191],v[140:143]\n"
"s_waitcnt lgkmcnt(0)\n"
MF "v[96:99],a[140:143],v[176:179],v[96:99]\n"
"s_nop 0\n"
TR "v[188:189],v5\n"
TR "v[190:191],v5 offset:2048\n"
MF "v[96:99],a[180:183],v[180:183],v[96:99]\n"
"s_nop 0\n"
TR "a[240:241],v5 offset:4096\n"
TR "a[242:243],v5 offset:6144\n"
MF "v[96:99],a[220:223],v[184:187],v[96:99]\n"
"s_nop 0\n"
TR "a[244:245],v5 offset:8192\n"
TR "a[246:247],v5 offset:10240\n"
MF "v[104:107],a[224:227],v[184:187],v[104:107]\n"
MF "v[104:107],a[184:187],v[180:183],v[104:107]\n"
MF "v[104:107],a[144:147],v[176:179],v[104:107]\n"
"s_nop 0\n"
"s_nop 0\n"
"ds_write_addtid_b32 v96\n"
"ds_write_addtid_b32 v97 offset:256\n"
MF "v[112:115],a[148:151],v[176:179],v[112:115]\n"
"s_nop 0\n"
"ds_write_addtid_b32 v98 offset:512\n"
"ds_write_addtid_b32 v99 offset:768\n"
MF "v[112:115],a[188:191],v[180:183],v[112:115]\n"
MF "v[112:115],a[228:231],v[184:187],v[112:115]\n"
"s_nop 0\n"
"ds_write_addtid_b32 v104 offset:2048\n"
"ds_write_addtid_b32 v105 offset:2304\n"
MF "v[120:123],a[232:235],v[184:187],v[120:123]\n"
"s_nop 0\n"
"ds_write_addtid_b32 v106 offset:2560\n"
"ds_write_addtid_b32 v107 offset:2816\n"
MF "v[120:123],a[192:195],v[180:183],v[120:123]\n"
MF "v[120:123],a[152:155],v[176:179],v[120:123]\n"
"s_nop 0\n"
"ds_write_addtid_b32 v112 offset:4096\n"
"ds_write_addtid_b32 v113 offset:4352\n"
MF "v[128:131],a[156:159],v[176:179],v[128:131]\n"
"s_nop 0\n"
"ds_write_addtid_b32 v114 offset:4608\n"
"ds_write_addtid_b32 v115 offset:4864\n"
MF "v[128:131],a[196:199],v[180:183],v[128:131]\n"
MF "v[128:131],a[236:239],v[184:187],v[128:131]\n"
"s_waitcnt lgkmcnt(12)\n"
"ds_write_addtid_b32 v120 offset:50176\n"
"ds_write_addtid_b32 v121 offset:50432\n"
MF "v[132:135],a[236:239],a[244:247],v[132:135]\n"
"s_nop 0\n"
TR "v[176:177],v6\n"
TR "v[178:179],v6 offset:2048\n"
MF "v[132:135],a[196:199],a[240:243],v[132:135]\n"
"s_nop 0\n"
TR "v[180:181],v6 offset:4096\n"
TR "v[182:183],v6 offset:6144\n"
MF "v[132:135],a[156:159],v[188:191],v[132:135]\n"
"s_nop 0\n"
TR "v[184:185],v6 offset:8192\n"
TR "v[186:187],v6 offset:10240\n"
MF "v[124:127],a[152:155],v[188:191],v[124:127]\n"
"s_nop 0\n"
"ds_write_addtid_b32 v122 offset:50688\n"
"ds_write_addtid_b32 v123 offset:50944\n"
MF "v[124:127],a[192:195],a[240:243],v[124:127]\n"
"s_nop 0\n"
"ds_write_addtid_b32 v128 offset:52224\n"
"ds_write_addtid_b32 v129 offset:52480\n"
MF "v[124:127],a[232:235],a[244:247],v[124:127]\n"
"s_nop 0\n"
"ds_write_addtid_b32 v130 offset:52736\n"
"ds_write_addtid_b32 v131 offset:52992\n"
MF "v[116:119],a[228:231],a[244:247],v[116:119]\n"
"s_nop 0\n"
"ds_write_addtid_b32 v132 offset:53248\n"
"ds_write_addtid_b32 v133 offset:53504\n"
MF "v[116:119],a[188:191],a[240:243],v[116:119]\n"
"s_nop 0\n"
"ds_write_addtid_b32 v134 offset:53760\n"
"ds_write_addtid_b32 v135 offset:54016\n"
MF "v[116:119],a[148:151],v[188:191],v[116:119]\n"
"s_nop 0\n"
"ds_write_addtid_b32 v124 offset:51200\n"
"ds_write_addtid_b32 v125 offset:51456\n"
MF "v[108:111],a[144:147],v[188:191],v[108:111]\n"
"s_nop 0\n"
"ds_write_addtid_b32 v126 offset:51712\n"
"ds_write_addtid_b32 v127 offset:51968\n"
MF "v[108:111],a[184:187],a[240:243],v[108:111]\n"
MF "v[108:111],a[224:227],a[244:247],v[108:111]\n"
"s_nop 0\n"
"ds_write_addtid_b32 v116 offset:49152\n"
"ds_write_addtid_b32 v117 offset:49408\n"
MF "v[100:103],a[220:223],a[244:247],v[100:103]\n"
"s_nop 0\n"
"ds_write_addtid_b32 v118 offset:49664\n"
"ds_write_addtid_b32 v119 offset:49920\n"
MF "v[100:103],a[180:183],a[240:243],v[100:103]\n"
MF "v[100:103],a[140:143],v[188:191],v[100:103]\n"
"s_waitcnt lgkmcnt(15)\n"
"ds_write_addtid_b32 v108 offset:3072\n"
"ds_write_addtid_b32 v109 offset:3328\n"
MF "v[136:139],a[140:143],v[176:179],v[136:139]\n"
"s_nop 0\n"
TR "v[188:189],v7\n"
TR "v[190:191],v7 offset:2048\n"
MF "v[136:139],a[180:183],v[180:183],v[136:139]\n"
"s_nop 0\n"
TR "a[240:241],v7 offset:4096\n"
TR "a[242:243],v7 offset:6144\n"
MF "v[136:139],a[220:223],v[184:187],v[136:139]\n"
"s_nop 0\n"
TR "a[244:245],v7 offset:8192\n"
TR "a[246:247],v7 offset:10240\n"
MF "v[144:147],a[224:227],v[184:187],v[144:147]\n"
"s_nop 0\n"
"ds_write_addtid_b32 v110 offset:3584\n"
"ds_write_addtid_b32 v111 offset:3840\n"
MF "v[144:147],a[184:187],v[180:183],v[144:147]\n"
"s_nop 0\n"
"ds_write_addtid_b32 v100 offset:1024\n"
"ds_write_addtid_b32 v101 offset:1280\n"
MF "v[144:147],a[144:147],v[176:179],v[144:147]\n"
"s_nop 0\n"
"ds_write_addtid_b32 v102 offset:1536\n"
"ds_write_addtid_b32 v103 offset:1792\n"
MF "v[152:155],a[148:151],v[176:179],v[152:155]\n"
"s_add_u32 m0,s5,49152\n"
MF "v[152:155],a[188:191],v[180:183],v[152:155]\n"
LD "v192,s[48:51],s88 offen lds\n"
MF "v[152:155],a[228:231],v[184:187],v[152:155]\n"
MF "v[160:163],a[232:235],v[184:187],v[160:163]\n"
MF "v[160:163],a[192:195],v[180:183],v[160:163]\n"
MF "v[160:163],a[152:155],v[176:179],v[160:163]\n"
MF "v[168:171],a[156:159],v[176:179],v[168:171]\n"
LD "v193,s[48:51],s88 offen offset:1024 lds\n"
MF "v[168:171],a[196:199],v[180:183],v[168:171]\n"
"s_waitcnt vmcnt(10) lgkmcnt(0)\n"
MF "v[168:171],a[236:239],v[184:187],v[168:171]\n"
"s_barrier\n"
MF "v[172:175],a[236:239],a[244:247],v[172:175]\n"
"s_nop 0\n"
TR "v[176:177],v0 offset:24576\n"
TR "v[178:179],v0 offset:26624\n"
MF "v[172:175],a[196:199],a[240:243],v[172:175]\n"
"s_add_u32 s85,s85,1\n"
TR "v[180:181],v0 offset:28672\n"
TR "v[182:183],v0 offset:30720\n"
MF "v[172:175],a[156:159],v[188:191],v[172:175]\n"
"s_nop 0\n"
TR "v[184:185],v0 offset:32768\n"
TR "v[186:187],v0 offset:34816\n"
MF "v[164:167],a[152:155],v[188:191],v[164:167]\n"
LD "v194,s[48:51],s88 offen offset:2048 lds\n"
MF "v[164:167],a[192:195],a[240:243],v[164:167]\n"
"ds_read2st64_b32 v[128:129],v13 offset0:204 offset1:205\n"
MF "v[164:167],a[232:235],a[244:247],v[164:167]\n"
"ds_read2st64_b32 v[130:131],v13 offset0:206 offset1:207\n"
MF "v[156:159],a[228:231],a[244:247],v[156:159]\n"
"ds_read2st64_b32 v[132:133],v13 offset0:208 offset1:209\n"
MF "v[156:159],a[188:191],a[240:243],v[156:159]\n"
LD "v195,s[48:51],s88 offen offset:3072 lds\n"
MF "v[156:159],a[148:151],v[188:191],v[156:159]\n"
"ds_read2st64_b32 v[134:135],v13 offset0:210 offset1:211\n"
MF "v[148:151],a[144:147],v[188:191],v[148:151]\n"
"ds_read2st64_b32 v[120:121],v13 offset0:196 offset1:197\n"
MF "v[148:151],a[184:187],a[240:243],v[148:151]\n"
"ds_read2st64_b32 v[122:123],v13 offset0:198 offset1:199\n"
MF "v[148:151],a[224:227],a[244:247],v[148:151]\n"
MF "v[140:143],a[220:223],a[244:247],v[140:143]\n"
MF "v[140:143],a[180:183],a[240:243],v[140:143]\n"
MF "v[140:143],a[140:143],v[188:191],v[140:143]\n"
"s_waitcnt lgkmcnt(6)\n"
MF "v[16:19],a[0:3],v[176:179],v[252:255]\n"
"s_nop 0\n"
TR "v[188:189],v1 offset:24576\n"
TR "v[190:191],v1 offset:26624\n"
MF "v[16:19],a[40:43],v[180:183],v[16:19]\n"
"s_nop 0\n"
TR "a[240:241],v1 offset:28672\n"
TR "a[242:243],v1 offset:30720\n"
MF "v[16:19],a[80:83],v[184:187],v[16:19]\n"
"s_add_u32 m0,s5,53248\n"
TR "a[244:245],v1 offset:32768\n"
TR "a[246:247],v1 offset:34816\n"
MF "v[24:27],a[84:87],v[184:187],v[252:255]\n"
"s_waitcnt lgkmcnt(8)\n"
LD "v196,s[48:51],s88 offen lds\n"
MF "v[24:27],a[44:47],v[180:183],v[24:27]\n"
"ds_read2st64_b32 v[124:125],v13 offset0:200 offset1:201\n"
"v_add_f32 v168,v168,v128\n"
MF "v[24:27],a[4:7],v[176:179],v[24:27]\n"
"ds_read2st64_b32 v[126:127],v13 offset0:202 offset1:203\n"
"v_add_f32 v169,v169,v129\n"
MF "v[32:35],a[8:11],v[176:179],v[252:255]\n"
"ds_read2st64_b32 v[112:113],v13 offset0:16 offset1:17\n"
"v_add_f32 v170,v170,v130\n"
MF "v[32:35],a[48:51],v[180:183],v[32:35]\n"
LD "v197,s[48:51],s88 offen offset:1024 lds\n"
MF "v[32:35],a[88:91],v[184:187],v[32:35]\n"
"ds_read2st64_b32 v[114:115],v13 offset0:18 offset1:19\n"
"v_add_f32 v171,v171,v131\n"
MF "v[40:43],a[92:95],v[184:187],v[252:255]\n"
"ds_read2st64_b32 v[116:117],v13 offset0:192 offset1:193\n"
"v_add_f32 v172,v172,v132\n"
MF "v[40:43],a[52:55],v[180:183],v[40:43]\n"
"s_add_u32 s80,s88,122880\n"
"ds_read2st64_b32 v[118:119],v13 offset0:194 offset1:195\n"
"v_add_f32 v173,v173,v133\n"
MF "v[40:43],a[12:15],v[176:179],v[40:43]\n"
"s_add_u32 m0,s5,98304\n"
"ds_read2st64_b32 v[104:105],v13 offset0:8 offset1:9\n"
"v_add_f32 v174,v174,v134\n"
MF "v[48:51],a[16:19],v[176:179],v[252:255]\n"
LD "v192,s[48:51],s80 offen lds\n"
MF "v[48:51],a[56:59],v[180:183],v[48:51]\n"
"ds_read2st64_b32 v[106:107],v13 offset0:10 offset1:11\n"
"v_add_f32 v175,v175,v135\n"
MF "v[48:51],a[96:99],v[184:187],v[48:51]\n"
"s_waitcnt lgkmcnt(8)\n"
LD "v193,s[48:51],s80 offen offset:1024 lds\n"
MF "v[52:55],a[96:99],a[244:247],v[252:255]\n"
"s_nop 0\n"
TR "v[176:177],v2 offset:24576\n"
TR "v[178:179],v2 offset:26624\n"
MF "v[52:55],a[56:59],a[240:243],v[52:55]\n"
"s_nop 0\n"
TR "v[180:181],v2 offset:28672\n"
TR "v[182:183],v2 offset:30720\n"
MF "v[52:55],a[16:19],v[188:191],v[52:55]\n"
"s_nop 0\n"
TR "v[184:185],v2 offset:32768\n"
TR "v[186:187],v2 offset:34816\n"
MF "v[44:47],a[12:15],v[188:191],v[252:255]\n"
"ds_read2st64_b32 v[108:109],v13 offset0:12 offset1:13\n"
CV "v168,v168,v169\n"
MF "v[44:47],a[52:55],a[240:243],v[44:47]\n"
"ds_read2st64_b32 v[110:111],v13 offset0:14 offset1:15\n"
CV "v169,v170,v171\n"
MF "v[44:47],a[92:95],a[244:247],v[44:47]\n"
"ds_read2st64_b32 v[96:97],v13 offset0:0 offset1:1\n"
CV "v170,v172,v173\n"
MF "v[36:39],a[88:91],a[244:247],v[252:255]\n"
LD "v194,s[48:51],s80 offen offset:2048 lds\n"
MF "v[36:39],a[48:51],a[240:243],v[36:39]\n"
"s_waitcnt lgkmcnt(15)\n"
"ds_read2st64_b32 v[98:99],v13 offset0:2 offset1:3\n"
CV "v171,v174,v175\n"
MF "v[36:39],a[8:11],v[188:191],v[36:39]\n"
"ds_read2st64_b32 v[100:101],v13 offset0:4 offset1:5\n"
"v_add_f32 v160,v160,v120\n"
MF "v[28:31],a[4:7],v[188:191],v[252:255]\n"
"s_add_u32 s87,s90,4096\n"
"ds_read2st64_b32 v[102:103],v13 offset0:6 offset1:7\n"
"v_add_f32 v161,v161,v121\n"
MF "v[28:31],a[44:47],a[240:243],v[28:31]\n"
"buffer_store_dwordx4 v[168:171],v9,s[40:43],s87 offen sc1\n"
MF "v[28:31],a[84:87],a[244:247],v[28:31]\n"
LD "v195,s[48:51],s80 offen offset:3072 lds\n"
MF "v[20:23],a[80:83],a[244:247],v[252:255]\n"
"v_add_f32 v162,v162,v122\n"
"v_add_f32 v163,v163,v123\n"
MF "v[20:23],a[40:43],a[240:243],v[20:23]\n"
"s_add_u32 m0,s5,102400\n"
"v_add_f32 v164,v164,v124\n"
"v_add_f32 v165,v165,v125\n"
MF "v[20:23],a[0:3],v[188:191],v[20:23]\n"
"s_waitcnt lgkmcnt(6)\n"
LD "v196,s[48:51],s80 offen lds\n"
MF "v[56:59],a[0:3],v[176:179],v[252:255]\n"
"s_nop 0\n"
TR "v[188:189],v3 offset:24576\n"
TR "v[190:191],v3 offset:26624\n"
MF "v[56:59],a[40:43],v[180:183],v[56:59]\n"
"s_nop 0\n"
TR "a[240:241],v3 offset:28672\n"
TR "a[242:243],v3 offset:30720\n"
MF "v[56:59],a[80:83],v[184:187],v[56:59]\n"
"s_nop 0\n"
TR "a[244:245],v3 offset:32768\n"
TR "a[246:247],v3 offset:34816\n"
MF "v[64:67],a[84:87],v[184:187],v[252:255]\n"
".Lpg27h:\n"
"s_cbranch_execz .Lpg31h\n"
"v_add_f32 v166,v166,v126\n"
"v_add_f32 v167,v167,v127\n"
MF "v[64:67],a[44:47],v[180:183],v[64:67]\n"
CV "v160,v160,v161\n"
CV "v161,v162,v163\n"
MF "v[64:67],a[4:7],v[176:179],v[64:67]\n"
LD "v197,s[48:51],s80 offen offset:1024 lds\n"
MF "v[72:75],a[8:11],v[176:179],v[252:255]\n"
CV "v162,v164,v165\n"
CV "v163,v166,v167\n"
MF "v[72:75],a[48:51],v[180:183],v[72:75]\n"
"buffer_store_dwordx4 v[160:163],v9,s[40:43],s90 offen offset:3072 sc1\n"
MF "v[72:75],a[88:91],v[184:187],v[72:75]\n"
"v_add_f32 v152,v152,v112\n"
"v_add_f32 v153,v153,v113\n"
MF "v[80:83],a[92:95],v[184:187],v[252:255]\n"
"v_add_f32 v154,v154,v114\n"
"v_add_f32 v155,v155,v115\n"
MF "v[80:83],a[52:55],v[180:183],v[80:83]\n"
"v_add_f32 v156,v156,v116\n"
"v_add_f32 v157,v157,v117\n"
MF "v[80:83],a[12:15],v[176:179],v[80:83]\n"
"v_add_f32 v158,v158,v118\n"
"v_add_f32 v159,v159,v119\n"
MF "v[88:91],a[16:19],v[176:179],v[252:255]\n"
CV "v152,v152,v153\n"
CV "v153,v154,v155\n"
MF "v[88:91],a[56:59],v[180:183],v[88:91]\n"
"s_waitcnt vmcnt(16) lgkmcnt(0)\n"
CV "v154,v156,v157\n"
CV "v155,v158,v159\n"
MF "v[88:91],a[96:99],v[184:187],v[88:91]\n"
"s_barrier\n"
MF "v[92:95],a[96:99],a[244:247],v[252:255]\n"
TR "v[176:177],v0\n"
MF "v[92:95],a[56:59],a[240:243],v[92:95]\n"
"s_add_u32 s89,s89,s65\n"
TR "v[178:179],v0 offset:2048\n"
MF "v[92:95],a[16:19],v[188:191],v[92:95]\n"
"buffer_store_dwordx4 v[152:155],v9,s[40:43],s90 offen offset:2048 sc1\n"
MF "v[84:87],a[12:15],v[188:191],v[252:255]\n"
"s_add_u32 s88,s88,s65\n"
TR "v[180:181],v0 offset:4096\n"
MF "v[84:87],a[52:55],a[240:243],v[84:87]\n"
"s_add_u32 m0,s5,24576\n"
TR "v[182:183],v0 offset:6144\n"
MF "v[84:87],a[92:95],a[244:247],v[84:87]\n"
LD "v192,s[48:51],s89 offen lds\n"
MF "v[76:79],a[88:91],a[244:247],v[252:255]\n"
TR "v[184:185],v0 offset:8192\n"
MF "v[76:79],a[48:51],a[240:243],v[76:79]\n"
TR "v[186:187],v0 offset:10240\n"
MF "v[76:79],a[8:11],v[188:191],v[76:79]\n"
LD "v193,s[48:51],s89 offen offset:1024 lds\n"
MF "v[68:71],a[4:7],v[188:191],v[252:255]\n"
"v_add_f32 v144,v144,v104\n"
"v_add_f32 v145,v145,v105\n"
MF "v[68:71],a[44:47],a[240:243],v[68:71]\n"
"v_add_f32 v146,v146,v106\n"
"v_add_f32 v147,v147,v107\n"
MF "v[68:71],a[84:87],a[244:247],v[68:71]\n"
"v_add_f32 v148,v148,v108\n"
"v_add_f32 v149,v149,v109\n"
MF "v[60:63],a[80:83],a[244:247],v[252:255]\n"
"v_add_f32 v150,v150,v110\n"
"v_add_f32 v151,v151,v111\n"
MF "v[60:63],a[40:43],a[240:243],v[60:63]\n"
CV "v144,v144,v145\n"
CV "v145,v146,v147\n"
MF "v[60:63],a[0:3],v[188:191],v[60:63]\n"
"s_waitcnt lgkmcnt(0)\n"
CV "v146,v148,v149\n"
CV "v147,v150,v151\n"
MF "v[16:19],a[120:123],v[176:179],v[16:19]\n"
TR "v[188:189],v1\n"
MF "v[16:19],a[160:163],v[180:183],v[16:19]\n"
TR "v[190:191],v1 offset:2048\n"
MF "v[16:19],a[200:203],v[184:187],v[16:19]\n"
"buffer_store_dwordx4 v[144:147],v9,s[40:43],s90 offen offset:1024 sc1\n"
MF "v[24:27],a[204:207],v[184:187],v[24:27]\n"
TR "a[240:241],v1 offset:4096\n"
MF "v[24:27],a[164:167],v[180:183],v[24:27]\n"
TR "a[242:243],v1 offset:6144\n"
MF "v[24:27],a[124:127],v[176:179],v[24:27]\n"
LD "v194,s[48:51],s89 offen offset:2048 lds\n"
MF "v[32:35],a[128:131],v[176:179],v[32:35]\n"
TR "a[244:245],v1 offset:8192\n"
MF "v[32:35],a[168:171],v[180:183],v[32:35]\n"
TR "a[246:247],v1 offset:10240\n"
MF "v[32:35],a[208:211],v[184:187],v[32:35]\n"
MF "v[40:43],a[212:215],v[184:187],v[40:43]\n"
MF "v[40:43],a[172:175],v[180:183],v[40:43]\n"
MF "v[40:43],a[132:135],v[176:179],v[40:43]\n"
LD "v195,s[48:51],s89 offen offset:3072 lds\n"
MF "v[48:51],a[136:139],v[176:179],v[48:51]\n"
"v_add_f32 v136,v136,v96\n"
"v_add_f32 v137,v137,v97\n"
MF "v[48:51],a[176:179],v[180:183],v[48:51]\n"
"v_add_f32 v138,v138,v98\n"
"v_add_f32 v139,v139,v99\n"
MF "v[48:51],a[216:219],v[184:187],v[48:51]\n"
"s_waitcnt lgkmcnt(0)\n"
"v_add_f32 v140,v140,v100\n"
"v_add_f32 v141,v141,v101\n"
MF "v[52:55],a[216:219],a[244:247],v[52:55]\n"
TR "v[176:177],v2\n"
"v_add_f32 v142,v142,v102\n"
MF "v[52:55],a[176:179],a[240:243],v[52:55]\n"
TR "v[178:179],v2 offset:2048\n"
"v_add_f32 v143,v143,v103\n"
MF "v[52:55],a[136:139],v[188:191],v[52:55]\n"
CV "v136,v136,v137\n"
CV "v137,v138,v139\n"
MF "v[44:47],a[132:135],v[188:191],v[44:47]\n"
TR "v[180:181],v2 offset:4096\n"
CV "v138,v140,v141\n"
MF "v[44:47],a[172:175],a[240:243],v[44:47]\n"
TR "v[182:183],v2 offset:6144\n"
CV "v139,v142,v143\n"
MF "v[44:47],a[212:215],a[244:247],v[44:47]\n"
"buffer_store_dwordx4 v[136:139],v9,s[40:43],s90 offen sc1\n"
MF "v[36:39],a[208:211],a[244:247],v[36:39]\n"
"s_add_u32 s90,s90,20480\n"
TR "v[184:185],v2 offset:8192\n"
MF "v[36:39],a[168:171],a[240:243],v[36:39]\n"
"s_add_u32 m0,s5,28672\n"
TR "v[186:187],v2 offset:10240\n"
MF "v[36:39],a[128:131],v[188:191],v[36:39]\n"
LD "v196,s[48:51],s89 offen lds\n"
MF "v[28:31],a[124:127],v[188:191],v[28:31]\n"
MF "v[28:31],a[164:167],a[240:243],v[28:31]\n"
MF "v[28:31],a[204:207],a[244:247],v[28:31]\n"
MF "v[20:23],a[200:203],a[244:247],v[20:23]\n"
MF "v[20:23],a[160:163],a[240:243],v[20:23]\n"
MF "v[20:23],a[120:123],v[188:191],v[20:23]\n"
"s_waitcnt lgkmcnt(0)\n"
MF "v[56:59],a[120:123],v[176:179],v[56:59]\n"
TR "v[188:189],v3\n"
MF "v[56:59],a[160:163],v[180:183],v[56:59]\n"
TR "v[190:191],v3 offset:2048\n"
MF "v[56:59],a[200:203],v[184:187],v[56:59]\n"
LD "v197,s[48:51],s89 offen offset:1024 lds\n"
MF "v[64:67],a[204:207],v[184:187],v[64:67]\n"
TR "a[240:241],v3 offset:4096\n"
MF "v[64:67],a[164:167],v[180:183],v[64:67]\n"
TR "a[242:243],v3 offset:6144\n"
MF "v[64:67],a[124:127],v[176:179],v[64:67]\n"
MF "v[72:75],a[128:131],v[176:179],v[72:75]\n"
TR "a[244:245],v3 offset:8192\n"
MF "v[72:75],a[168:171],v[180:183],v[72:75]\n"
TR "a[246:247],v3 offset:10240\n"
MF "v[72:75],a[208:211],v[184:187],v[72:75]\n"
MF "v[80:83],a[212:215],v[184:187],v[80:83]\n"
MF "v[80:83],a[172:175],v[180:183],v[80:83]\n"
MF "v[80:83],a[132:135],v[176:179],v[80:83]\n"
MF "v[88:91],a[136:139],v[176:179],v[88:91]\n"
MF "v[88:91],a[176:179],v[180:183],v[88:91]\n"
"s_waitcnt vmcnt(17) lgkmcnt(0)\n"
MF "v[88:91],a[216:219],v[184:187],v[88:91]\n"
"s_barrier\n"
MF "v[92:95],a[216:219],a[244:247],v[92:95]\n"
"s_add_u32 s80,s89,122880\n"
TR "v[176:177],v0 offset:49152\n"
MF "v[92:95],a[176:179],a[240:243],v[92:95]\n"
"s_add_u32 m0,s5,0\n"
TR "v[178:179],v0 offset:51200\n"
MF "v[92:95],a[136:139],v[188:191],v[92:95]\n"
LD "v192,s[48:51],s80 offen lds\n"
MF "v[84:87],a[132:135],v[188:191],v[84:87]\n"
"s_add_u32 s60,s64,1\n"
TR "v[180:181],v0 offset:53248\n"
MF "v[84:87],a[172:175],a[240:243],v[84:87]\n"
"s_cmp_eq_u32 s60,8\n"
"s_cselect_b32 s60,0,s60\n"
TR "v[182:183],v0 offset:55296\n"
MF "v[84:87],a[212:215],a[244:247],v[84:87]\n"
LD "v193,s[48:51],s80 offen offset:1024 lds\n"
MF "v[76:79],a[208:211],a[244:247],v[76:79]\n"
"s_cmp_eq_u32 s60,10\n"
"s_cselect_b32 s60,s61,s60\n"
TR "v[184:185],v0 offset:57344\n"
MF "v[76:79],a[168:171],a[240:243],v[76:79]\n"
"s_sub_u32 s65,s60,s64\n"
TR "v[186:187],v0 offset:59392\n"
"v_mov_b32 v244,0\n"
MF "v[76:79],a[128:131],v[188:191],v[76:79]\n"
"s_lshl_b32 s65,s65,7\n"
"v_mov_b32 v245,0\n"
"v_mov_b32 v246,0\n"
MF "v[68:71],a[124:127],v[188:191],v[68:71]\n"
"s_mov_b32 s64,s60\n"
"v_mov_b32 v247,0\n"
"v_mov_b32 v248,0\n"
MF "v[68:71],a[164:167],a[240:243],v[68:71]\n"
"v_mov_b32 v249,0\n"
"v_mov_b32 v250,0\n"
MF "v[68:71],a[204:207],a[244:247],v[68:71]\n"
LD "v194,s[48:51],s80 offen offset:2048 lds\n"
MF "v[60:63],a[200:203],a[244:247],v[60:63]\n"
"v_mov_b32 v251,0\n"
MF "v[60:63],a[160:163],a[240:243],v[60:63]\n"
MF "v[60:63],a[120:123],v[188:191],v[60:63]\n"
"s_waitcnt lgkmcnt(0)\n"
MF "v[16:19],a[20:23],v[176:179],v[16:19]\n"
TR "v[188:189],v1 offset:49152\n"
MF "v[16:19],a[60:63],v[180:183],v[16:19]\n"
TR "v[190:191],v1 offset:51200\n"
MF "v[16:19],a[100:103],v[184:187],v[16:19]\n"
LD "v195,s[48:51],s80 offen offset:3072 lds\n"
MF "v[24:27],a[104:107],v[184:187],v[24:27]\n"
TR "a[240:241],v1 offset:53248\n"
MF "v[24:27],a[64:67],v[180:183],v[24:27]\n"
"s_add_u32 m0,s5,4096\n"
TR "a[242:243],v1 offset:55296\n"
MF "v[24:27],a[24:27],v[176:179],v[24:27]\n"
LD "v196,s[48:51],s80 offen lds\n"
MF "v[32:35],a[28:31],v[176:179],v[32:35]\n"
TR "a[244:245],v1 offset:57344\n"
MF "v[32:35],a[68:71],v[180:183],v[32:35]\n"
TR "a[246:247],v1 offset:59392\n"
MF "v[32:35],a[108:111],v[184:187],v[32:35]\n"
MF "v[40:43],a[112:115],v[184:187],v[40:43]\n"
MF "v[40:43],a[72:75],v[180:183],v[40:43]\n"
MF "v[40:43],a[32:35],v[176:179],v[40:43]\n"
LD "v197,s[48:51],s80 offen offset:1024 lds\n"
MF "v[48:51],a[36:39],v[176:179],v[48:51]\n"
MF "v[48:51],a[76:79],v[180:183],v[48:51]\n"
MF "v[48:51],a[116:119],v[184:187],v[48:51]\n"
"s_waitcnt lgkmcnt(0)\n"
MF "v[52:55],a[116:119],a[244:247],v[52:55]\n"
TR "v[176:177],v2 offset:49152\n"
MF "v[52:55],a[76:79],a[240:243],v[52:55]\n"
TR "v[178:179],v2 offset:51200\n"
MF "v[52:55],a[36:39],v[188:191],v[52:55]\n"
MF "v[44:47],a[32:35],v[188:191],v[44:47]\n"
TR "v[180:181],v2 offset:53248\n"
MF "v[44:47],a[72:75],a[240:243],v[44:47]\n"
TR "v[182:183],v2 offset:55296\n"
MF "v[44:47],a[112:115],a[244:247],v[44:47]\n"
MF "v[36:39],a[108:111],a[244:247],v[36:39]\n"
TR "v[184:185],v2 offset:57344\n"
MF "v[36:39],a[68:71],a[240:243],v[36:39]\n"
TR "v[186:187],v2 offset:59392\n"
MF "v[36:39],a[28:31],v[188:191],v[36:39]\n"
MF "v[28:31],a[24:27],v[188:191],v[28:31]\n"
MF "v[28:31],a[64:67],a[240:243],v[28:31]\n"
MF "v[28:31],a[104:107],a[244:247],v[28:31]\n"
MF "v[20:23],a[100:103],a[244:247],v[20:23]\n"
MF "v[20:23],a[60:63],a[240:243],v[20:23]\n"
MF "v[20:23],a[20:23],v[188:191],v[20:23]\n"
"s_waitcnt lgkmcnt(0)\n"
MF "v[56:59],a[20:23],v[176:179],v[56:59]\n"
TR "v[188:189],v3 offset:49152\n"
MF "v[56:59],a[60:63],v[180:183],v[56:59]\n"
TR "v[190:191],v3 offset:51200\n"
MF "v[56:59],a[100:103],v[184:187],v[56:59]\n"
"v_mov_b32 v14,s76\n"
"s_add_u32 s76,s76,1\n"
MF "v[64:67],a[104:107],v[184:187],v[64:67]\n"
TR "a[240:241],v3 offset:53248\n"
MF "v[64:67],a[64:67],v[180:183],v[64:67]\n"
TR "a[242:243],v3 offset:55296\n"
MF "v[64:67],a[24:27],v[176:179],v[64:67]\n"
MF "v[72:75],a[28:31],v[176:179],v[72:75]\n"
TR "a[244:245],v3 offset:57344\n"
MF "v[72:75],a[68:71],v[180:183],v[72:75]\n"
TR "a[246:247],v3 offset:59392\n"
MF "v[72:75],a[108:111],v[184:187],v[72:75]\n"
MF "v[80:83],a[112:115],v[184:187],v[80:83]\n"
MF "v[80:83],a[72:75],v[180:183],v[80:83]\n"
MF "v[80:83],a[32:35],v[176:179],v[80:83]\n"
MF "v[88:91],a[36:39],v[176:179],v[88:91]\n"
MF "v[88:91],a[76:79],v[180:183],v[88:91]\n"
"s_waitcnt vmcnt(8) lgkmcnt(0)\n"
MF "v[88:91],a[116:119],v[184:187],v[88:91]\n"
"s_barrier\n"
MF "v[92:95],a[116:119],a[244:247],v[92:95]\n"
TR "v[176:177],v4\n"
MF "v[92:95],a[76:79],a[240:243],v[92:95]\n"
TR "v[178:179],v4 offset:2048\n"
MF "v[92:95],a[36:39],v[188:191],v[92:95]\n"
"buffer_store_dword v14,v15,s[40:43],s94 offen sc1\n"
MF "v[84:87],a[32:35],v[188:191],v[84:87]\n"
TR "v[180:181],v4 offset:4096\n"
MF "v[84:87],a[72:75],a[240:243],v[84:87]\n"
TR "v[182:183],v4 offset:6144\n"
MF "v[84:87],a[112:115],a[244:247],v[84:87]\n"
MF "v[76:79],a[108:111],a[244:247],v[76:79]\n"
TR "v[184:185],v4 offset:8192\n"
MF "v[76:79],a[68:71],a[240:243],v[76:79]\n"
TR "v[186:187],v4 offset:10240\n"
MF "v[76:79],a[28:31],v[188:191],v[76:79]\n"
MF "v[68:71],a[24:27],v[188:191],v[68:71]\n"
MF "v[68:71],a[64:67],a[240:243],v[68:71]\n"
"s_add_u32 m0,s5,73728\n"
MF "v[68:71],a[104:107],a[244:247],v[68:71]\n"
MF "v[60:63],a[100:103],a[244:247],v[60:63]\n"
MF "v[60:63],a[60:63],a[240:243],v[60:63]\n"
MF "v[60:63],a[20:23],v[188:191],v[60:63]\n"
"s_waitcnt lgkmcnt(0)\n"
MF "v[16:19],a[140:143],v[176:179],v[16:19]\n"
"s_nop 0\n"
TR "v[188:189],v5\n"
TR "v[190:191],v5 offset:2048\n"
MF "v[16:19],a[180:183],v[180:183],v[16:19]\n"
"s_nop 0\n"
TR "a[240:241],v5 offset:4096\n"
TR "a[242:243],v5 offset:6144\n"
MF "v[16:19],a[220:223],v[184:187],v[16:19]\n"
"s_nop 0\n"
TR "a[244:245],v5 offset:8192\n"
TR "a[246:247],v5 offset:10240\n"
MF "v[24:27],a[224:227],v[184:187],v[24:27]\n"
MF "v[24:27],a[184:187],v[180:183],v[24:27]\n"
MF "v[24:27],a[144:147],v[176:179],v[24:27]\n"
"s_nop 0\n"
"s_nop 0\n"
"ds_write_addtid_b32 v16\n"
"ds_write_addtid_b32 v17 offset:256\n"
MF "v[32:35],a[148:151],v[176:179],v[32:35]\n"
"s_nop 0\n"
"ds_write_addtid_b32 v18 offset:512\n"
"ds_write_addtid_b32 v19 offset:768\n"
MF "v[32:35],a[188:191],v[180:183],v[32:35]\n"
MF "v[32:35],a[228:231],v[184:187],v[32:35]\n"
"s_nop 0\n"
"ds_write_addtid_b32 v24 offset:2048\n"
"ds_write_addtid_b32 v25 offset:2304\n"
MF "v[40:43],a[232:235],v[184:187],v[40:43]\n"
"s_nop 0\n"
"ds_write_addtid_b32 v26 offset:2560\n"
"ds_write_addtid_b32 v27 offset:2816\n"
MF "v[40:43],a[192:195],v[180:183],v[40:43]\n"
MF "v[40:43],a[152:155],v[176:179],v[40:43]\n"
"s_nop 0\n"
"ds_write_addtid_b32 v32 offset:4096\n"
"ds_write_addtid_b32 v33 offset:4352\n"
MF "v[48:51],a[156:159],v[176:179],v[48:51]\n"
"s_nop 0\n"
"ds_write_addtid_b32 v34 offset:4608\n"
"ds_write_addtid_b32 v35 offset:4864\n"
MF "v[48:51],a[196:199],v[180:183],v[48:51]\n"
MF "v[48:51],a[236:239],v[184:187],v[48:51]\n"
"s_waitcnt lgkmcnt(12)\n"
"ds_write_addtid_b32 v40 offset:50176\n"
"ds_write_addtid_b32 v41 offset:50432\n"
MF "v[52:55],a[236:239],a[244:247],v[52:55]\n"
"s_nop 0\n"
TR "v[176:177],v6\n"
TR "v[178:179],v6 offset:2048\n"
MF "v[52:55],a[196:199],a[240:243],v[52:55]\n"
"s_nop 0\n"
TR "v[180:181],v6 offset:4096\n"
TR "v[182:183],v6 offset:6144\n"
MF "v[52:55],a[156:159],v[188:191],v[52:55]\n"
"s_nop 0\n"
TR "v[184:185],v6 offset:8192\n"
TR "v[186:187],v6 offset:10240\n"
MF "v[44:47],a[152:155],v[188:191],v[44:47]\n"
"s_nop 0\n"
"ds_write_addtid_b32 v42 offset:50688\n"
"ds_write_addtid_b32 v43 offset:50944\n"
MF "v[44:47],a[192:195],a[240:243],v[44:47]\n"
"s_nop 0\n"
"ds_write_addtid_b32 v48 offset:52224\n"
"ds_write_addtid_b32 v49 offset:52480\n"
MF "v[44:47],a[232:235],a[244:247],v[44:47]\n"
"s_nop 0\n"
"ds_write_addtid_b32 v50 offset:52736\n"
"ds_write_addtid_b32 v51 offset:52992\n"
MF "v[36:39],a[228:231],a[244:247],v[36:39]\n"
"s_nop 0\n"
"ds_write_addtid_b32 v52 offset:53248\n"
"ds_write_addtid_b32 v53 offset:53504\n"
MF "v[36:39],a[188:191],a[240:243],v[36:39]\n"
"s_nop 0\n"
"ds_write_addtid_b32 v54 offset:53760\n"
"ds_write_addtid_b32 v55 offset:54016\n"
MF "v[36:39],a[148:151],v[188:191],v[36:39]\n"
"s_nop 0\n"
"ds_write_addtid_b32 v44 offset:51200\n"
"ds_write_addtid_b32 v45 offset:51456\n"
MF "v[28:31],a[144:147],v[188:191],v[28:31]\n"
"s_nop 0\n"
"ds_write_addtid_b32 v46 offset:51712\n"
"ds_write_addtid_b32 v47 offset:51968\n"
MF "v[28:31],a[184:187],a[240:243],v[28:31]\n"
MF "v[28:31],a[224:227],a[244:247],v[28:31]\n"
"s_nop 0\n"
"ds_write_addtid_b32 v36 offset:49152\n"
"ds_write_addtid_b32 v37 offset:49408\n"
MF "v[20:23],a[220:223],a[244:247],v[20:23]\n"
"s_nop 0\n"
"ds_write_addtid_b32 v38 offset:49664\n"
"ds_write_addtid_b32 v39 offset:49920\n"
MF "v[20:23],a[180:183],a[240:243],v[20:23]\n"
MF "v[20:23],a[140:143],v[188:191],v[20:23]\n"
"s_waitcnt lgkmcnt(15)\n"
"ds_write_addtid_b32 v28 offset:3072\n"
"ds_write_addtid_b32 v29 offset:3328\n"
MF "v[56:59],a[140:143],v[176:179],v[56:59]\n"
"s_nop 0\n"
TR "v[188:189],v7\n"
TR "v[190:191],v7 offset:2048\n"
MF "v[56:59],a[180:183],v[180:183],v[56:59]\n"
"s_nop 0\n"
TR "a[240:241],v7 offset:4096\n"
TR "a[242:243],v7 offset:6144\n"
MF "v[56:59],a[220:223],v[184:187],v[56:59]\n"
"s_nop 0\n"
TR "a[244:245],v7 offset:8192\n"
TR "a[246:247],v7 offset:10240\n"
MF "v[64:67],a[224:227],v[184:187],v[64:67]\n"
"s_nop 0\n"
"ds_write_addtid_b32 v30 offset:3584\n"
"ds_write_addtid_b32 v31 offset:3840\n"
MF "v[64:67],a[184:187],v[180:183],v[64:67]\n"
"s_nop 0\n"
"ds_write_addtid_b32 v20 offset:1024\n"
"ds_write_addtid_b32 v21 offset:1280\n"
MF "v[64:67],a[144:147],v[176:179],v[64:67]\n"
"s_nop 0\n"
"ds_write_addtid_b32 v22 offset:1536\n"
"ds_write_addtid_b32 v23 offset:1792\n"
MF "v[72:75],a[148:151],v[176:179],v[72:75]\n"
"s_add_u32 m0,s5,49152\n"
MF "v[72:75],a[188:191],v[180:183],v[72:75]\n"
LD "v192,s[48:51],s88 offen lds\n"
MF "v[72:75],a[228:231],v[184:187],v[72:75]\n"
MF "v[80:83],a[232:235],v[184:187],v[80:83]\n"
MF "v[80:83],a[192:195],v[180:183],v[80:83]\n"
MF "v[80:83],a[152:155],v[176:179],v[80:83]\n"
MF "v[88:91],a[156:159],v[176:179],v[88:91]\n"
LD "v193,s[48:51],s88 offen offset:1024 lds\n"
MF "v[88:91],a[196:199],v[180:183],v[88:91]\n"
"s_waitcnt vmcnt(9) lgkmcnt(0)\n"
MF "v[88:91],a[236:239],v[184:187],v[88:91]\n"
"s_barrier\n"
MF "v[92:95],a[236:239],a[244:247],v[92:95]\n"
"s_nop 0\n"
TR "v[176:177],v0 offset:24576\n"
TR "v[178:179],v0 offset:26624\n"
MF "v[92:95],a[196:199],a[240:243],v[92:95]\n"
"s_add_u32 s85,s85,1\n"
TR "v[180:181],v0 offset:28672\n"
TR "v[182:183],v0 offset:30720\n"
MF "v[92:95],a[156:159],v[188:191],v[92:95]\n"
"s_nop 0\n"
TR "v[184:185],v0 offset:32768\n"
TR "v[186:187],v0 offset:34816\n"
MF "v[84:87],a[152:155],v[188:191],v[84:87]\n"
LD "v194,s[48:51],s88 offen offset:2048 lds\n"
MF "v[84:87],a[192:195],a[240:243],v[84:87]\n"
"ds_read2st64_b32 v[48:49],v13 offset0:204 offset1:205\n"
MF "v[84:87],a[232:235],a[244:247],v[84:87]\n"
"ds_read2st64_b32 v[50:51],v13 offset0:206 offset1:207\n"
MF "v[76:79],a[228:231],a[244:247],v[76:79]\n"
"ds_read2st64_b32 v[52:53],v13 offset0:208 offset1:209\n"
MF "v[76:79],a[188:191],a[240:243],v[76:79]\n"
LD "v195,s[48:51],s88 offen offset:3072 lds\n"
MF "v[76:79],a[148:151],v[188:191],v[76:79]\n"
"ds_read2st64_b32 v[54:55],v13 offset0:210 offset1:211\n"
MF "v[68:71],a[144:147],v[188:191],v[68:71]\n"
"ds_read2st64_b32 v[40:41],v13 offset0:196 offset1:197\n"
MF "v[68:71],a[184:187],a[240:243],v[68:71]\n"
"ds_read2st64_b32 v[42:43],v13 offset0:198 offset1:199\n"
MF "v[68:71],a[224:227],a[244:247],v[68:71]\n"
MF "v[60:63],a[220:223],a[244:247],v[60:63]\n"
MF "v[60:63],a[180:183],a[240:243],v[60:63]\n"
"s_cmp_lg_u32 s85,5\n"
MF "v[60:63],a[140:143],v[188:191],v[60:63]\n"
"s_waitcnt lgkmcnt(6)\n"
"s_cbranch_scc1 .Lps5\n"
".Lps9:\n"
MF "v[96:99],a[0:3],v[176:179],v[252:255]\n"
"s_nop 0\n"
TR "v[188:189],v1 offset:24576\n"
TR "v[190:191],v1 offset:26624\n"
MF "v[96:99],a[40:43],v[180:183],v[96:99]\n"
"s_nop 0\n"
TR "a[240:241],v1 offset:28672\n"
TR "a[242:243],v1 offset:30720\n"
MF "v[96:99],a[80:83],v[184:187],v[96:99]\n"
"s_add_u32 m0,s5,53248\n"
TR "a[244:245],v1 offset:32768\n"
TR "a[246:247],v1 offset:34816\n"
MF "v[104:107],a[84:87],v[184:187],v[252:255]\n"
"s_waitcnt lgkmcnt(8)\n"
LD "v196,s[48:51],s88 offen lds\n"
MF "v[104:107],a[44:47],v[180:183],v[104:107]\n"
"ds_read2st64_b32 v[44:45],v13 offset0:200 offset1:201\n"
"v_add_f32 v88,v88,v48\n"
MF "v[104:107],a[4:7],v[176:179],v[104:107]\n"
"ds_read2st64_b32 v[46:47],v13 offset0:202 offset1:203\n"
"v_add_f32 v89,v89,v49\n"
MF "v[112:115],a[8:11],v[176:179],v[252:255]\n"
"ds_read2st64_b32 v[32:33],v13 offset0:16 offset1:17\n"
"v_add_f32 v90,v90,v50\n"
MF "v[112:115],a[48:51],v[180:183],v[112:115]\n"
LD "v197,s[48:51],s88 offen offset:1024 lds\n"
MF "v[112:115],a[88:91],v[184:187],v[112:115]\n"
"ds_read2st64_b32 v[34:35],v13 offset0:18 offset1:19\n"
"v_add_f32 v91,v91,v51\n"
MF "v[120:123],a[92:95],v[184:187],v[252:255]\n"
"ds_read2st64_b32 v[36:37],v13 offset0:192 offset1:193\n"
"v_add_f32 v92,v92,v52\n"
MF "v[120:123],a[52:55],v[180:183],v[120:123]\n"
"s_add_u32 s80,s88,122880\n"
"ds_read2st64_b32 v[38:39],v13 offset0:194 offset1:195\n"
"v_add_f32 v93,v93,v53\n"
MF "v[120:123],a[12:15],v[176:179],v[120:123]\n"
"s_add_u32 m0,s5,98304\n"
"ds_read2st64_b32 v[24:25],v13 offset0:8 offset1:9\n"
"v_add_f32 v94,v94,v54\n"
MF "v[128:131],a[16:19],v[176:179],v[252:255]\n"
LD "v192,s[48:51],s80 offen lds\n"
MF "v[128:131],a[56:59],v[180:183],v[128:131]\n"
"ds_read2st64_b32 v[26:27],v13 offset0:10 offset1:11\n"
"v_add_f32 v95,v95,v55\n"
MF "v[128:131],a[96:99],v[184:187],v[128:131]\n"
"s_waitcnt lgkmcnt(8)\n"
LD "v193,s[48:51],s80 offen offset:1024 lds\n"
MF "v[132:135],a[96:99],a[244:247],v[252:255]\n"
"s_nop 0\n"
TR "v[176:177],v2 offset:24576\n"
TR "v[178:179],v2 offset:26624\n"
MF "v[132:135],a[56:59],a[240:243],v[132:135]\n"
"s_nop 0\n"
TR "v[180:181],v2 offset:28672\n"
TR "v[182:183],v2 offset:30720\n"
MF "v[132:135],a[16:19],v[188:191],v[132:135]\n"
"s_nop 0\n"
TR "v[184:185],v2 offset:32768\n"
TR "v[186:187],v2 offset:34816\n"
MF "v[124:127],a[12:15],v[188:191],v[252:255]\n"
"ds_read2st64_b32 v[28:29],v13 offset0:12 offset1:13\n"
CV "v88,v88,v89\n"
MF "v[124:127],a[52:55],a[240:243],v[124:127]\n"
"ds_read2st64_b32 v[30:31],v13 offset0:14 offset1:15\n"
CV "v89,v90,v91\n"
MF "v[124:127],a[92:95],a[244:247],v[124:127]\n"
"ds_read2st64_b32 v[16:17],v13 offset0:0 offset1:1\n"
CV "v90,v92,v93\n"
MF "v[116:119],a[88:91],a[244:247],v[252:255]\n"
LD "v194,s[48:51],s80 offen offset:2048 lds\n"
MF "v[116:119],a[48:51],a[240:243],v[116:119]\n"
"s_waitcnt lgkmcnt(15)\n"
"ds_read2st64_b32 v[18:19],v13 offset0:2 offset1:3\n"
CV "v91,v94,v95\n"
MF "v[116:119],a[8:11],v[188:191],v[116:119]\n"
"ds_read2st64_b32 v[20:21],v13 offset0:4 offset1:5\n"
"v_add_f32 v80,v80,v40\n"
MF "v[108:111],a[4:7],v[188:191],v[252:255]\n"
"s_add_u32 s87,s90,4096\n"
"ds_read2st64_b32 v[22:23],v13 offset0:6 offset1:7\n"
"v_add_f32 v81,v81,v41\n"
MF "v[108:111],a[44:47],a[240:243],v[108:111]\n"
"buffer_store_dwordx4 v[88:91],v9,s[40:43],s87 offen sc1\n"
MF "v[108:111],a[84:87],a[244:247],v[108:111]\n"
LD "v195,s[48:51],s80 offen offset:3072 lds\n"
MF "v[100:103],a[80:83],a[244:247],v[252:255]\n"
"v_add_f32 v82,v82,v42\n"
"v_add_f32 v83,v83,v43\n"
MF "v[100:103],a[40:43],a[240:243],v[100:103]\n"
"s_add_u32 m0,s5,102400\n"
"v_add_f32 v84,v84,v44\n"
"v_add_f32 v85,v85,v45\n"
MF "v[100:103],a[0:3],v[188:191],v[100:103]\n"
"s_waitcnt lgkmcnt(6)\n"
LD "v196,s[48:51],s80 offen lds\n"
MF "v[136:139],a[0:3],v[176:179],v[252:255]\n"
".Lpg28h:\n"
"s_cbranch_execz .Lisr4\n"
"s_nop 0\n"
TR "v[188:189],v3 offset:24576\n"
TR "v[190:191],v3 offset:26624\n"
MF "v[136:139],a[40:43],v[180:183],v[136:139]\n"
"s_nop 0\n"
TR "a[240:241],v3 offset:28672\n"
TR "a[242:243],v3 offset:30720\n"
MF "v[136:139],a[80:83],v[184:187],v[136:139]\n"
"s_nop 0\n"
TR "a[244:245],v3 offset:32768\n"
TR "a[246:247],v3 offset:34816\n"
MF "v[144:147],a[84:87],v[184:187],v[252:255]\n"
"v_add_f32 v86,v86,v46\n"
"v_add_f32 v87,v87,v47\n"
MF "v[144:147],a[44:47],v[180:183],v[144:147]\n"
CV "v80,v80,v81\n"
CV "v81,v82,v83\n"
MF "v[144:147],a[4:7],v[176:179],v[144:147]\n"
LD "v197,s[48:51],s80 offen offset:1024 lds\n"
MF "v[152:155],a[8:11],v[176:179],v[252:255]\n"
CV "v82,v84,v85\n"
CV "v83,v86,v87\n"
MF "v[152:155],a[48:51],v[180:183],v[152:155]\n"
"buffer_store_dwordx4 v[80:83],v9,s[40:43],s90 offen offset:3072 sc1\n"
MF "v[152:155],a[88:91],v[184:187],v[152:155]\n"
"v_add_f32 v72,v72,v32\n"
"v_add_f32 v73,v73,v33\n"
MF "v[160:163],a[92:95],v[184:187],v[252:255]\n"
"v_add_f32 v74,v74,v34\n"
"v_add_f32 v75,v75,v35\n"
MF "v[160:163],a[52:55],v[180:183],v[160:163]\n"
"v_add_f32 v76,v76,v36\n"
"v_add_f32 v77,v77,v37\n"
MF "v[160:163],a[12:15],v[176:179],v[160:163]\n"
"v_add_f32 v78,v78,v38\n"
"v_add_f32 v79,v79,v39\n"
MF "v[168:171],a[16:19],v[176:179],v[252:255]\n"
CV "v72,v72,v73\n"
CV "v73,v74,v75\n"
MF "v[168:171],a[56:59],v[180:183],v[168:171]\n"
"s_waitcnt vmcnt(15) lgkmcnt(0)\n"
CV "v74,v76,v77\n"
CV "v75,v78,v79\n"
MF "v[168:171],a[96:99],v[184:187],v[168:171]\n"
"s_barrier\n"
MF "v[172:175],a[96:99],a[244:247],v[252:255]\n"
TR "v[176:177],v0\n"
MF "v[172:175],a[56:59],a[240:243],v[172:175]\n"
"s_add_u32 s89,s89,s65\n"
"s_mov_b32 s82,s70\n"
TR "v[178:179],v0 offset:2048\n"
MF "v[172:175],a[16:19],v[188:191],v[172:175]\n"
"buffer_store_dwordx4 v[72:75],v9,s[40:43],s90 offen offset:2048 sc1\n"
MF "v[164:167],a[12:15],v[188:191],v[252:255]\n"
"s_add_u32 s88,s88,s65\n"
"s_sub_u32 s72,s85,2\n"
TR "v[180:181],v0 offset:4096\n"
MF "v[164:167],a[52:55],a[240:243],v[164:167]\n"
"s_add_u32 m0,s5,24576\n"
TR "v[182:183],v0 offset:6144\n"
MF "v[164:167],a[92:95],a[244:247],v[164:167]\n"
LD "v192,s[48:51],s89 offen lds\n"
MF "v[156:159],a[88:91],a[244:247],v[252:255]\n"
TR "v[184:185],v0 offset:8192\n"
MF "v[156:159],a[48:51],a[240:243],v[156:159]\n"
TR "v[186:187],v0 offset:10240\n"
MF "v[156:159],a[8:11],v[188:191],v[156:159]\n"
LD "v193,s[48:51],s89 offen offset:1024 lds\n"
MF "v[148:151],a[4:7],v[188:191],v[252:255]\n"
"v_add_f32 v64,v64,v24\n"
"v_add_f32 v65,v65,v25\n"
MF "v[148:151],a[44:47],a[240:243],v[148:151]\n"
"v_add_f32 v66,v66,v26\n"
"v_add_f32 v67,v67,v27\n"
MF "v[148:151],a[84:87],a[244:247],v[148:151]\n"
"v_add_f32 v68,v68,v28\n"
"v_add_f32 v69,v69,v29\n"
MF "v[140:143],a[80:83],a[244:247],v[252:255]\n"
"v_add_f32 v70,v70,v30\n"
"v_add_f32 v71,v71,v31\n"
MF "v[140:143],a[40:43],a[240:243],v[140:143]\n"
CV "v64,v64,v65\n"
CV "v65,v66,v67\n"
MF "v[140:143],a[0:3],v[188:191],v[140:143]\n"
"s_waitcnt lgkmcnt(0)\n"
CV "v66,v68,v69\n"
CV "v67,v70,v71\n"
MF "v[96:99],a[120:123],v[176:179],v[96:99]\n"
TR "v[188:189],v1\n"
MF "v[96:99],a[160:163],v[180:183],v[96:99]\n"
TR "v[190:191],v1 offset:2048\n"
MF "v[96:99],a[200:203],v[184:187],v[96:99]\n"
"buffer_store_dwordx4 v[64:67],v9,s[40:43],s90 offen offset:1024 sc1\n"
MF "v[104:107],a[204:207],v[184:187],v[104:107]\n"
TR "a[240:241],v1 offset:4096\n"
MF "v[104:107],a[164:167],v[180:183],v[104:107]\n"
TR "a[242:243],v1 offset:6144\n"
MF "v[104:107],a[124:127],v[176:179],v[104:107]\n"
LD "v194,s[48:51],s89 offen offset:2048 lds\n"
MF "v[112:115],a[128:131],v[176:179],v[112:115]\n"
TR "a[244:245],v1 offset:8192\n"
MF "v[112:115],a[168:171],v[180:183],v[112:115]\n"
TR "a[246:247],v1 offset:10240\n"
MF "v[112:115],a[208:211],v[184:187],v[112:115]\n"
MF "v[120:123],a[212:215],v[184:187],v[120:123]\n"
MF "v[120:123],a[172:175],v[180:183],v[120:123]\n"
MF "v[120:123],a[132:135],v[176:179],v[120:123]\n"
LD "v195,s[48:51],s89 offen offset:3072 lds\n"
MF "v[128:131],a[136:139],v[176:179],v[128:131]\n"
"v_add_f32 v56,v56,v16\n"
"v_add_f32 v57,v57,v17\n"
MF "v[128:131],a[176:179],v[180:183],v[128:131]\n"
"v_add_f32 v58,v58,v18\n"
"v_add_f32 v59,v59,v19\n"
MF "v[128:131],a[216:219],v[184:187],v[128:131]\n"
"s_waitcnt lgkmcnt(0)\n"
"v_add_f32 v60,v60,v20\n"
"v_add_f32 v61,v61,v21\n"
MF "v[132:135],a[216:219],a[244:247],v[132:135]\n"
TR "v[176:177],v2\n"
"v_add_f32 v62,v62,v22\n"
MF "v[132:135],a[176:179],a[240:243],v[132:135]\n"
TR "v[178:179],v2 offset:2048\n"
"v_add_f32 v63,v63,v23\n"
MF "v[132:135],a[136:139],v[188:191],v[132:135]\n"
CV "v56,v56,v57\n"
CV "v57,v58,v59\n"
MF "v[124:127],a[132:135],v[188:191],v[124:127]\n"
TR "v[180:181],v2 offset:4096\n"
CV "v58,v60,v61\n"
MF "v[124:127],a[172:175],a[240:243],v[124:127]\n"
TR "v[182:183],v2 offset:6144\n"
CV "v59,v62,v63\n"
MF "v[124:127],a[212:215],a[244:247],v[124:127]\n"
"buffer_load_dword v10,v12,s[40:43],s86 offen sc1\n"
MF "v[116:119],a[208:211],a[244:247],v[116:119]\n"
TR "v[184:185],v2 offset:8192\n"
MF "v[116:119],a[168:171],a[240:243],v[116:119]\n"
TR "v[186:187],v2 offset:10240\n"
MF "v[116:119],a[128:131],v[188:191],v[116:119]\n"
"s_add_u32 m0,s5,28672\n"
"buffer_store_dwordx4 v[56:59],v9,s[40:43],s90 offen sc1\n"
MF "v[108:111],a[124:127],v[188:191],v[108:111]\n"
"s_add_u32 s90,s90,20480\n"
MF "v[108:111],a[164:167],a[240:243],v[108:111]\n"
LD "v196,s[48:51],s89 offen lds\n"
MF "v[108:111],a[204:207],a[244:247],v[108:111]\n"
MF "v[100:103],a[200:203],a[244:247],v[100:103]\n"
MF "v[100:103],a[160:163],a[240:243],v[100:103]\n"
MF "v[100:103],a[120:123],v[188:191],v[100:103]\n"
"s_waitcnt lgkmcnt(0)\n"
MF "v[136:139],a[120:123],v[176:179],v[136:139]\n"
TR "v[188:189],v3\n"
MF "v[136:139],a[160:163],v[180:183],v[136:139]\n"
TR "v[190:191],v3 offset:2048\n"
MF "v[136:139],a[200:203],v[184:187],v[136:139]\n"
LD "v197,s[48:51],s89 offen offset:1024 lds\n"
MF "v[144:147],a[204:207],v[184:187],v[144:147]\n"
TR "a[240:241],v3 offset:4096\n"
MF "v[144:147],a[164:167],v[180:183],v[144:147]\n"
TR "a[242:243],v3 offset:6144\n"
MF "v[144:147],a[124:127],v[176:179],v[144:147]\n"
MF "v[152:155],a[128:131],v[176:179],v[152:155]\n"
TR "a[244:245],v3 offset:8192\n"
MF "v[152:155],a[168:171],v[180:183],v[152:155]\n"
TR "a[246:247],v3 offset:10240\n"
MF "v[152:155],a[208:211],v[184:187],v[152:155]\n"
MF "v[160:163],a[212:215],v[184:187],v[160:163]\n"
MF "v[160:163],a[172:175],v[180:183],v[160:163]\n"
MF "v[160:163],a[132:135],v[176:179],v[160:163]\n"
MF "v[168:171],a[136:139],v[176:179],v[168:171]\n"
MF "v[168:171],a[176:179],v[180:183],v[168:171]\n"
"s_waitcnt vmcnt(18) lgkmcnt(0)\n"
MF "v[168:171],a[216:219],v[184:187],v[168:171]\n"
"s_barrier\n"
MF "v[172:175],a[216:219],a[244:247],v[172:175]\n"
"s_add_u32 s80,s89,122880\n"
TR "v[176:177],v0 offset:49152\n"
MF "v[172:175],a[176:179],a[240:243],v[172:175]\n"
"s_add_u32 m0,s5,0\n"
TR "v[178:179],v0 offset:51200\n"
MF "v[172:175],a[136:139],v[188:191],v[172:175]\n"
LD "v192,s[48:51],s80 offen lds\n"
MF "v[164:167],a[132:135],v[188:191],v[164:167]\n"
"s_add_u32 s60,s64,1\n"
TR "v[180:181],v0 offset:53248\n"
MF "v[164:167],a[172:175],a[240:243],v[164:167]\n"
"s_cmp_eq_u32 s60,8\n"
"s_cselect_b32 s60,0,s60\n"
TR "v[182:183],v0 offset:55296\n"
MF "v[164:167],a[212:215],a[244:247],v[164:167]\n"
LD "v193,s[48:51],s80 offen offset:1024 lds\n"
MF "v[156:159],a[208:211],a[244:247],v[156:159]\n"
"s_cmp_eq_u32 s60,10\n"
"s_cselect_b32 s60,s61,s60\n"
TR "v[184:185],v0 offset:57344\n"
MF "v[156:159],a[168:171],a[240:243],v[156:159]\n"
"s_sub_u32 s65,s60,s64\n"
TR "v[186:187],v0 offset:59392\n"
MF "v[156:159],a[128:131],v[188:191],v[156:159]\n"
"s_lshl_b32 s65,s65,7\n"
MF "v[148:151],a[124:127],v[188:191],v[148:151]\n"
"s_mov_b32 s64,s60\n"
MF "v[148:151],a[164:167],a[240:243],v[148:151]\n"
MF "v[148:151],a[204:207],a[244:247],v[148:151]\n"
LD "v194,s[48:51],s80 offen offset:2048 lds\n"
MF "v[140:143],a[200:203],a[244:247],v[140:143]\n"
MF "v[140:143],a[160:163],a[240:243],v[140:143]\n"
MF "v[140:143],a[120:123],v[188:191],v[140:143]\n"
"s_waitcnt lgkmcnt(0)\n"
MF "v[96:99],a[20:23],v[176:179],v[96:99]\n"
TR "v[188:189],v1 offset:49152\n"
MF "v[96:99],a[60:63],v[180:183],v[96:99]\n"
TR "v[190:191],v1 offset:51200\n"
MF "v[96:99],a[100:103],v[184:187],v[96:99]\n"
LD "v195,s[48:51],s80 offen offset:3072 lds\n"
MF "v[104:107],a[104:107],v[184:187],v[104:107]\n"
TR "a[240:241],v1 offset:53248\n"
MF "v[104:107],a[64:67],v[180:183],v[104:107]\n"
"s_add_u32 m0,s5,4096\n"
TR "a[242:243],v1 offset:55296\n"
MF "v[104:107],a[24:27],v[176:179],v[104:107]\n"
LD "v196,s[48:51],s80 offen lds\n"
MF "v[112:115],a[28:31],v[176:179],v[112:115]\n"
TR "a[244:245],v1 offset:57344\n"
MF "v[112:115],a[68:71],v[180:183],v[112:115]\n"
TR "a[246:247],v1 offset:59392\n"
MF "v[112:115],a[108:111],v[184:187],v[112:115]\n"
MF "v[120:123],a[112:115],v[184:187],v[120:123]\n"
MF "v[120:123],a[72:75],v[180:183],v[120:123]\n"
MF "v[120:123],a[32:35],v[176:179],v[120:123]\n"
LD "v197,s[48:51],s80 offen offset:1024 lds\n"
MF "v[128:131],a[36:39],v[176:179],v[128:131]\n"
MF "v[128:131],a[76:79],v[180:183],v[128:131]\n"
MF "v[128:131],a[116:119],v[184:187],v[128:131]\n"
"s_waitcnt lgkmcnt(0)\n"
MF "v[132:135],a[116:119],a[244:247],v[132:135]\n"
TR "v[176:177],v2 offset:49152\n"
MF "v[132:135],a[76:79],a[240:243],v[132:135]\n"
TR "v[178:179],v2 offset:51200\n"
MF "v[132:135],a[36:39],v[188:191],v[132:135]\n"
MF "v[124:127],a[32:35],v[188:191],v[124:127]\n"
TR "v[180:181],v2 offset:53248\n"
MF "v[124:127],a[72:75],a[240:243],v[124:127]\n"
TR "v[182:183],v2 offset:55296\n"
MF "v[124:127],a[112:115],a[244:247],v[124:127]\n"
MF "v[116:119],a[108:111],a[244:247],v[116:119]\n"
TR "v[184:185],v2 offset:57344\n"
MF "v[116:119],a[68:71],a[240:243],v[116:119]\n"
TR "v[186:187],v2 offset:59392\n"
MF "v[116:119],a[28:31],v[188:191],v[116:119]\n"
MF "v[108:111],a[24:27],v[188:191],v[108:111]\n"
MF "v[108:111],a[64:67],a[240:243],v[108:111]\n"
MF "v[108:111],a[104:107],a[244:247],v[108:111]\n"
MF "v[100:103],a[100:103],a[244:247],v[100:103]\n"
MF "v[100:103],a[60:63],a[240:243],v[100:103]\n"
"s_waitcnt vmcnt(9)\n"
"s_cmp_lt_u32 s85,6\n"
MF "v[100:103],a[20:23],v[188:191],v[100:103]\n"
"s_cbranch_scc1 .Ljs4\n"
"v_mov_b32 v236,v228\n"
"v_mov_b32 v237,v229\n"
MF "v[136:139],a[252:255],v[208:211],v[136:139]\n"
MF "v[144:147],a[252:255],v[212:215],v[144:147]\n"
MF "v[152:155],a[252:255],v[216:219],v[152:155]\n"
MF "v[160:163],a[252:255],v[220:223],v[160:163]\n"
MF "v[168:171],a[252:255],v[224:227],v[168:171]\n"
MF "v[140:143],a[248:251],v[208:211],v[140:143]\n"
MF "v[148:151],a[248:251],v[212:215],v[148:151]\n"
MF "v[156:159],a[248:251],v[216:219],v[156:159]\n"
MF "v[164:167],a[248:251],v[220:223],v[164:167]\n"
MF "v[172:175],a[248:251],v[224:227],v[172:175]\n"
MF "v[248:251],v[230:233],a[250:253],v[248:251]\n"
MF "v[244:247],v[234:237],a[250:253],v[244:247]\n"
".Ljs4:\n"
"s_waitcnt lgkmcnt(0)\n"
MF "v[136:139],a[20:23],v[176:179],v[136:139]\n"
TR "v[188:189],v3 offset:49152\n"
"v_sub_u32 v10,v10,v11\n"
MF "v[136:139],a[60:63],v[180:183],v[136:139]\n"
TR "v[190:191],v3 offset:51200\n"
MF "v[136:139],a[100:103],v[184:187],v[136:139]\n"
"v_mov_b32 v14,s76\n"
"s_add_u32 s76,s76,1\n"
MF "v[144:147],a[104:107],v[184:187],v[144:147]\n"
TR "a[240:241],v3 offset:53248\n"
MF "v[144:147],a[64:67],v[180:183],v[144:147]\n"
TR "a[242:243],v3 offset:55296\n"
MF "v[144:147],a[24:27],v[176:179],v[144:147]\n"
".Lnf0:\n"
"v_readlane_b32 s58,v10,s67\n"
"s_cmp_lt_u32 s58,s72\n"
"s_cbranch_scc1 .Lsp0\n"
".Lnb0:\n"
MF "v[152:155],a[28:31],v[176:179],v[152:155]\n"
"s_add_u32 s70,s70,20480\n"
TR "a[244:245],v3 offset:57344\n"
MF "v[152:155],a[68:71],v[180:183],v[152:155]\n"
TR "a[246:247],v3 offset:59392\n"
MF "v[152:155],a[108:111],v[184:187],v[152:155]\n"
".Lnf1:\n"
"v_cmp_gt_u32 vcc,2,v10\n"
"s_cbranch_vccnz .Lsp1\n"
".Lnb1:\n"
MF "v[160:163],a[112:115],v[184:187],v[160:163]\n"
MF "v[160:163],a[72:75],v[180:183],v[160:163]\n"
LD "v[208:211],v9,s[40:43],s82 offen sc1\n"
MF "v[160:163],a[32:35],v[176:179],v[160:163]\n"
MF "v[168:171],a[36:39],v[176:179],v[168:171]\n"
MF "v[168:171],a[76:79],v[180:183],v[168:171]\n"
"s_waitcnt vmcnt(9) lgkmcnt(0)\n"
LD "v[212:215],v9,s[40:43],s82 offen offset:1024 sc1\n"
MF "v[168:171],a[116:119],v[184:187],v[168:171]\n"
"s_barrier\n"
MF "v[172:175],a[116:119],a[244:247],v[172:175]\n"
TR "v[176:177],v4\n"
MF "v[172:175],a[76:79],a[240:243],v[172:175]\n"
TR "v[178:179],v4 offset:2048\n"
MF "v[172:175],a[36:39],v[188:191],v[172:175]\n"
"buffer_store_dword v14,v15,s[40:43],s94 offen sc1\n"
MF "v[164:167],a[32:35],v[188:191],v[164:167]\n"
TR "v[180:181],v4 offset:4096\n"
MF "v[164:167],a[72:75],a[240:243],v[164:167]\n"
TR "v[182:183],v4 offset:6144\n"
MF "v[164:167],a[112:115],a[244:247],v[164:167]\n"
LD "v[216:219],v9,s[40:43],s82 offen offset:2048 sc1\n"
MF "v[156:159],a[108:111],a[244:247],v[156:159]\n"
TR "v[184:185],v4 offset:8192\n"
MF "v[156:159],a[68:71],a[240:243],v[156:159]\n"
TR "v[186:187],v4 offset:10240\n"
MF "v[156:159],a[28:31],v[188:191],v[156:159]\n"
LD "v[220:223],v9,s[40:43],s82 offen offset:3072 sc1\n"
MF "v[148:151],a[24:27],v[188:191],v[148:151]\n"
"s_add_u32 s82,s82,4096\n"
MF "v[148:151],a[64:67],a[240:243],v[148:151]\n"
"s_add_u32 m0,s5,73728\n"
LD "v[224:227],v9,s[40:43],s82 offen sc1\n"
MF "v[148:151],a[104:107],a[244:247],v[148:151]\n"
"s_add_u32 s2,s2,204800\n"
MF "v[140:143],a[100:103],a[244:247],v[140:143]\n"
LD "v[228:231],v9,s[40:43],s2 offen sc1\n"
MF "v[140:143],a[60:63],a[240:243],v[140:143]\n"
"s_add_u32 s2,s2,204800\n"
MF "v[140:143],a[20:23],v[188:191],v[140:143]\n"
"s_waitcnt lgkmcnt(0)\n"
LD "v[232:235],v9,s[40:43],s2 offen sc1\n"
MF "v[96:99],a[140:143],v[176:179],v[96:99]\n"
"s_nop 0\n"
TR "v[188:189],v5\n"
TR "v[190:191],v5 offset:2048\n"
MF "v[96:99],a[180:183],v[180:183],v[96:99]\n"
"s_nop 0\n"
TR "a[240:241],v5 offset:4096\n"
TR "a[242:243],v5 offset:6144\n"
MF "v[96:99],a[220:223],v[184:187],v[96:99]\n"
"s_nop 0\n"
TR "a[244:245],v5 offset:8192\n"
TR "a[246:247],v5 offset:10240\n"
MF "v[104:107],a[224:227],v[184:187],v[104:107]\n"
MF "v[104:107],a[184:187],v[180:183],v[104:107]\n"
MF "v[104:107],a[144:147],v[176:179],v[104:107]\n"
"s_nop 0\n"
"s_nop 0\n"
"ds_write_addtid_b32 v96\n"
"ds_write_addtid_b32 v97 offset:256\n"
MF "v[112:115],a[148:151],v[176:179],v[112:115]\n"
"s_nop 0\n"
"ds_write_addtid_b32 v98 offset:512\n"
"ds_write_addtid_b32 v99 offset:768\n"
MF "v[112:115],a[188:191],v[180:183],v[112:115]\n"
MF "v[112:115],a[228:231],v[184:187],v[112:115]\n"
"s_nop 0\n"
"ds_write_addtid_b32 v104 offset:2048\n"
"ds_write_addtid_b32 v105 offset:2304\n"
MF "v[120:123],a[232:235],v[184:187],v[120:123]\n"
"s_nop 0\n"
"ds_write_addtid_b32 v106 offset:2560\n"
"ds_write_addtid_b32 v107 offset:2816\n"
MF "v[120:123],a[192:195],v[180:183],v[120:123]\n"
MF "v[120:123],a[152:155],v[176:179],v[120:123]\n"
"s_nop 0\n"
"ds_write_addtid_b32 v112 offset:4096\n"
"ds_write_addtid_b32 v113 offset:4352\n"
MF "v[128:131],a[156:159],v[176:179],v[128:131]\n"
"s_nop 0\n"
"ds_write_addtid_b32 v114 offset:4608\n"
"ds_write_addtid_b32 v115 offset:4864\n"
MF "v[128:131],a[196:199],v[180:183],v[128:131]\n"
MF "v[128:131],a[236:239],v[184:187],v[128:131]\n"
"s_waitcnt lgkmcnt(12)\n"
"ds_write_addtid_b32 v120 offset:50176\n"
"ds_write_addtid_b32 v121 offset:50432\n"
MF "v[132:135],a[236:239],a[244:247],v[132:135]\n"
"s_nop 0\n"
TR "v[176:177],v6\n"
TR "v[178:179],v6 offset:2048\n"
MF "v[132:135],a[196:199],a[240:243],v[132:135]\n"
"s_nop 0\n"
TR "v[180:181],v6 offset:4096\n"
TR "v[182:183],v6 offset:6144\n"
MF "v[132:135],a[156:159],v[188:191],v[132:135]\n"
"s_nop 0\n"
TR "v[184:185],v6 offset:8192\n"
TR "v[186:187],v6 offset:10240\n"
MF "v[124:127],a[152:155],v[188:191],v[124:127]\n"
"s_nop 0\n"
"ds_write_addtid_b32 v122 offset:50688\n"
"ds_write_addtid_b32 v123 offset:50944\n"
MF "v[124:127],a[192:195],a[240:243],v[124:127]\n"
"s_nop 0\n"
"ds_write_addtid_b32 v128 offset:52224\n"
"ds_write_addtid_b32 v129 offset:52480\n"
MF "v[124:127],a[232:235],a[244:247],v[124:127]\n"
"s_nop 0\n"
"ds_write_addtid_b32 v130 offset:52736\n"
"ds_write_addtid_b32 v131 offset:52992\n"
MF "v[116:119],a[228:231],a[244:247],v[116:119]\n"
"s_nop 0\n"
"ds_write_addtid_b32 v132 offset:53248\n"
"ds_write_addtid_b32 v133 offset:53504\n"
MF "v[116:119],a[188:191],a[240:243],v[116:119]\n"
"s_nop 0\n"
"ds_write_addtid_b32 v134 offset:53760\n"
"ds_write_addtid_b32 v135 offset:54016\n"
MF "v[116:119],a[148:151],v[188:191],v[116:119]\n"
"s_nop 0\n"
"ds_write_addtid_b32 v124 offset:51200\n"
"ds_write_addtid_b32 v125 offset:51456\n"
MF "v[108:111],a[144:147],v[188:191],v[108:111]\n"
"s_nop 0\n"
"ds_write_addtid_b32 v126 offset:51712\n"
"ds_write_addtid_b32 v127 offset:51968\n"
MF "v[108:111],a[184:187],a[240:243],v[108:111]\n"
MF "v[108:111],a[224:227],a[244:247],v[108:111]\n"
"s_nop 0\n"
"ds_write_addtid_b32 v116 offset:49152\n"
"ds_write_addtid_b32 v117 offset:49408\n"
MF "v[100:103],a[220:223],a[244:247],v[100:103]\n"
"s_nop 0\n"
"ds_write_addtid_b32 v118 offset:49664\n"
"ds_write_addtid_b32 v119 offset:49920\n"
MF "v[100:103],a[180:183],a[240:243],v[100:103]\n"
MF "v[100:103],a[140:143],v[188:191],v[100:103]\n"
"s_waitcnt lgkmcnt(15)\n"
"ds_write_addtid_b32 v108 offset:3072\n"
"ds_write_addtid_b32 v109 offset:3328\n"
MF "v[136:139],a[140:143],v[176:179],v[136:139]\n"
"s_nop 0\n"
TR "v[188:189],v7\n"
TR "v[190:191],v7 offset:2048\n"
MF "v[136:139],a[180:183],v[180:183],v[136:139]\n"
"s_nop 0\n"
TR "a[240:241],v7 offset:4096\n"
TR "a[242:243],v7 offset:6144\n"
MF "v[136:139],a[220:223],v[184:187],v[136:139]\n"
"s_nop 0\n"
TR "a[244:245],v7 offset:8192\n"
TR "a[246:247],v7 offset:10240\n"
MF "v[144:147],a[224:227],v[184:187],v[144:147]\n"
"s_nop 0\n"
"ds_write_addtid_b32 v110 offset:3584\n"
"ds_write_addtid_b32 v111 offset:3840\n"
MF "v[144:147],a[184:187],v[180:183],v[144:147]\n"
"s_nop 0\n"
"ds_write_addtid_b32 v100 offset:1024\n"
"ds_write_addtid_b32 v101 offset:1280\n"
MF "v[144:147],a[144:147],v[176:179],v[144:147]\n"
"s_nop 0\n"
"ds_write_addtid_b32 v102 offset:1536\n"
"ds_write_addtid_b32 v103 offset:1792\n"
MF "v[152:155],a[148:151],v[176:179],v[152:155]\n"
"s_add_u32 m0,s5,49152\n"
MF "v[152:155],a[188:191],v[180:183],v[152:155]\n"
LD "v192,s[48:51],s88 offen lds\n"
MF "v[152:155],a[228:231],v[184:187],v[152:155]\n"
MF "v[160:163],a[232:235],v[184:187],v[160:163]\n"
MF "v[160:163],a[192:195],v[180:183],v[160:163]\n"
MF "v[160:163],a[152:155],v[176:179],v[160:163]\n"
MF "v[168:171],a[156:159],v[176:179],v[168:171]\n"
LD "v193,s[48:51],s88 offen offset:1024 lds\n"
MF "v[168:171],a[196:199],v[180:183],v[168:171]\n"
"s_waitcnt vmcnt(16) lgkmcnt(0)\n"
MF "v[168:171],a[236:239],v[184:187],v[168:171]\n"
"s_barrier\n"
MF "v[172:175],a[236:239],a[244:247],v[172:175]\n"
"s_nop 0\n"
TR "v[176:177],v0 offset:24576\n"
TR "v[178:179],v0 offset:26624\n"
MF "v[172:175],a[196:199],a[240:243],v[172:175]\n"
"s_add_u32 s85,s85,1\n"
TR "v[180:181],v0 offset:28672\n"
TR "v[182:183],v0 offset:30720\n"
MF "v[172:175],a[156:159],v[188:191],v[172:175]\n"
"s_nop 0\n"
TR "v[184:185],v0 offset:32768\n"
TR "v[186:187],v0 offset:34816\n"
MF "v[164:167],a[152:155],v[188:191],v[164:167]\n"
LD "v194,s[48:51],s88 offen offset:2048 lds\n"
MF "v[164:167],a[192:195],a[240:243],v[164:167]\n"
"ds_read2st64_b32 v[128:129],v13 offset0:204 offset1:205\n"
MF "v[164:167],a[232:235],a[244:247],v[164:167]\n"
"ds_read2st64_b32 v[130:131],v13 offset0:206 offset1:207\n"
MF "v[156:159],a[228:231],a[244:247],v[156:159]\n"
"ds_read2st64_b32 v[132:133],v13 offset0:208 offset1:209\n"
MF "v[156:159],a[188:191],a[240:243],v[156:159]\n"
LD "v195,s[48:51],s88 offen offset:3072 lds\n"
MF "v[156:159],a[148:151],v[188:191],v[156:159]\n"
"ds_read2st64_b32 v[134:135],v13 offset0:210 offset1:211\n"
MF "v[148:151],a[144:147],v[188:191],v[148:151]\n"
"ds_read2st64_b32 v[120:121],v13 offset0:196 offset1:197\n"
MF "v[148:151],a[184:187],a[240:243],v[148:151]\n"
"ds_read2st64_b32 v[122:123],v13 offset0:198 offset1:199\n"
MF "v[148:151],a[224:227],a[244:247],v[148:151]\n"
MF "v[140:143],a[220:223],a[244:247],v[140:143]\n"
MF "v[140:143],a[180:183],a[240:243],v[140:143]\n"
MF "v[140:143],a[140:143],v[188:191],v[140:143]\n"
"s_waitcnt lgkmcnt(6)\n"
MF "v[16:19],a[0:3],v[176:179],v[252:255]\n"
"s_nop 0\n"
TR "v[188:189],v1 offset:24576\n"
TR "v[190:191],v1 offset:26624\n"
MF "v[16:19],a[40:43],v[180:183],v[16:19]\n"
"s_nop 0\n"
TR "a[240:241],v1 offset:28672\n"
TR "a[242:243],v1 offset:30720\n"
MF "v[16:19],a[80:83],v[184:187],v[16:19]\n"
"s_add_u32 m0,s5,53248\n"
TR "a[244:245],v1 offset:32768\n"
TR "a[246:247],v1 offset:34816\n"
MF "v[24:27],a[84:87],v[184:187],v[252:255]\n"
"s_waitcnt lgkmcnt(8)\n"
LD "v196,s[48:51],s88 offen lds\n"
MF "v[24:27],a[44:47],v[180:183],v[24:27]\n"
"ds_read2st64_b32 v[124:125],v13 offset0:200 offset1:201\n"
"v_add_f32 v168,v168,v128\n"
MF "v[24:27],a[4:7],v[176:179],v[24:27]\n"
"ds_read2st64_b32 v[126:127],v13 offset0:202 offset1:203\n"
"v_add_f32 v169,v169,v129\n"
MF "v[32:35],a[8:11],v[176:179],v[252:255]\n"
"ds_read2st64_b32 v[112:113],v13 offset0:16 offset1:17\n"
"v_add_f32 v170,v170,v130\n"
MF "v[32:35],a[48:51],v[180:183],v[32:35]\n"
LD "v197,s[48:51],s88 offen offset:1024 lds\n"
MF "v[32:35],a[88:91],v[184:187],v[32:35]\n"
"ds_read2st64_b32 v[114:115],v13 offset0:18 offset1:19\n"
"v_add_f32 v171,v171,v131\n"
MF "v[40:43],a[92:95],v[184:187],v[252:255]\n"
"ds_read2st64_b32 v[116:117],v13 offset0:192 offset1:193\n"
"v_add_f32 v172,v172,v132\n"
MF "v[40:43],a[52:55],v[180:183],v[40:43]\n"
"s_add_u32 s80,s88,122880\n"
"ds_read2st64_b32 v[118:119],v13 offset0:194 offset1:195\n"
"v_add_f32 v173,v173,v133\n"
MF "v[40:43],a[12:15],v[176:179],v[40:43]\n"
"s_add_u32 m0,s5,98304\n"
"ds_read2st64_b32 v[104:105],v13 offset0:8 offset1:9\n"
"v_add_f32 v174,v174,v134\n"
MF "v[48:51],a[16:19],v[176:179],v[252:255]\n"
LD "v192,s[48:51],s80 offen lds\n"
MF "v[48:51],a[56:59],v[180:183],v[48:51]\n"
"ds_read2st64_b32 v[106:107],v13 offset0:10 offset1:11\n"
"v_add_f32 v175,v175,v135\n"
MF "v[48:51],a[96:99],v[184:187],v[48:51]\n"
"s_waitcnt lgkmcnt(8)\n"
LD "v193,s[48:51],s80 offen offset:1024 lds\n"
MF "v[52:55],a[96:99],a[244:247],v[252:255]\n"
"s_nop 0\n"
TR "v[176:177],v2 offset:24576\n"
TR "v[178:179],v2 offset:26624\n"
MF "v[52:55],a[56:59],a[240:243],v[52:55]\n"
"s_nop 0\n"
TR "v[180:181],v2 offset:28672\n"
TR "v[182:183],v2 offset:30720\n"
MF "v[52:55],a[16:19],v[188:191],v[52:55]\n"
"s_nop 0\n"
TR "v[184:185],v2 offset:32768\n"
TR "v[186:187],v2 offset:34816\n"
MF "v[44:47],a[12:15],v[188:191],v[252:255]\n"
".Lpg29h:\n"
"s_cbranch_execz .Lisr4\n"
"ds_read2st64_b32 v[108:109],v13 offset0:12 offset1:13\n"
CV "v168,v168,v169\n"
MF "v[44:47],a[52:55],a[240:243],v[44:47]\n"
"ds_read2st64_b32 v[110:111],v13 offset0:14 offset1:15\n"
CV "v169,v170,v171\n"
MF "v[44:47],a[92:95],a[244:247],v[44:47]\n"
"ds_read2st64_b32 v[96:97],v13 offset0:0 offset1:1\n"
CV "v170,v172,v173\n"
MF "v[36:39],a[88:91],a[244:247],v[252:255]\n"
LD "v194,s[48:51],s80 offen offset:2048 lds\n"
MF "v[36:39],a[48:51],a[240:243],v[36:39]\n"
"s_waitcnt lgkmcnt(15)\n"
"ds_read2st64_b32 v[98:99],v13 offset0:2 offset1:3\n"
CV "v171,v174,v175\n"
MF "v[36:39],a[8:11],v[188:191],v[36:39]\n"
"ds_read2st64_b32 v[100:101],v13 offset0:4 offset1:5\n"
"v_add_f32 v160,v160,v120\n"
MF "v[28:31],a[4:7],v[188:191],v[252:255]\n"
"s_add_u32 s87,s90,4096\n"
"ds_read2st64_b32 v[102:103],v13 offset0:6 offset1:7\n"
"v_add_f32 v161,v161,v121\n"
MF "v[28:31],a[44:47],a[240:243],v[28:31]\n"
"buffer_store_dwordx4 v[168:171],v9,s[40:43],s87 offen sc1\n"
MF "v[28:31],a[84:87],a[244:247],v[28:31]\n"
LD "v195,s[48:51],s80 offen offset:3072 lds\n"
MF "v[20:23],a[80:83],a[244:247],v[252:255]\n"
"v_add_f32 v162,v162,v122\n"
"v_add_f32 v163,v163,v123\n"
MF "v[20:23],a[40:43],a[240:243],v[20:23]\n"
"s_add_u32 m0,s5,102400\n"
"v_add_f32 v164,v164,v124\n"
"v_add_f32 v165,v165,v125\n"
MF "v[20:23],a[0:3],v[188:191],v[20:23]\n"
"s_waitcnt lgkmcnt(6)\n"
LD "v196,s[48:51],s80 offen lds\n"
MF "v[56:59],a[0:3],v[176:179],v[252:255]\n"
"s_nop 0\n"
TR "v[188:189],v3 offset:24576\n"
TR "v[190:191],v3 offset:26624\n"
MF "v[56:59],a[40:43],v[180:183],v[56:59]\n"
"s_nop 0\n"
TR "a[240:241],v3 offset:28672\n"
TR "a[242:243],v3 offset:30720\n"
MF "v[56:59],a[80:83],v[184:187],v[56:59]\n"
"s_nop 0\n"
TR "a[244:245],v3 offset:32768\n"
TR "a[246:247],v3 offset:34816\n"
MF "v[64:67],a[84:87],v[184:187],v[252:255]\n"
"v_add_f32 v166,v166,v126\n"
"v_add_f32 v167,v167,v127\n"
MF "v[64:67],a[44:47],v[180:183],v[64:67]\n"
CV "v160,v160,v161\n"
CV "v161,v162,v163\n"
MF "v[64:67],a[4:7],v[176:179],v[64:67]\n"
LD "v197,s[48:51],s80 offen offset:1024 lds\n"
MF "v[72:75],a[8:11],v[176:179],v[252:255]\n"
CV "v162,v164,v165\n"
CV "v163,v166,v167\n"
MF "v[72:75],a[48:51],v[180:183],v[72:75]\n"
"buffer_store_dwordx4 v[160:163],v9,s[40:43],s90 offen offset:3072 sc1\n"
MF "v[72:75],a[88:91],v[184:187],v[72:75]\n"
"v_add_f32 v152,v152,v112\n"
"v_add_f32 v153,v153,v113\n"
MF "v[80:83],a[92:95],v[184:187],v[252:255]\n"
"v_add_f32 v154,v154,v114\n"
"v_add_f32 v155,v155,v115\n"
MF "v[80:83],a[52:55],v[180:183],v[80:83]\n"
"v_add_f32 v156,v156,v116\n"
"v_add_f32 v157,v157,v117\n"
MF "v[80:83],a[12:15],v[176:179],v[80:83]\n"
"v_add_f32 v158,v158,v118\n"
"v_add_f32 v159,v159,v119\n"
MF "v[88:91],a[16:19],v[176:179],v[252:255]\n"
CV "v152,v152,v153\n"
CV "v153,v154,v155\n"
MF "v[88:91],a[56:59],v[180:183],v[88:91]\n"
"s_waitcnt vmcnt(22) lgkmcnt(0)\n"
CV "v154,v156,v157\n"
CV "v155,v158,v159\n"
MF "v[88:91],a[96:99],v[184:187],v[88:91]\n"
"s_barrier\n"
MF "v[92:95],a[96:99],a[244:247],v[252:255]\n"
TR "v[176:177],v0\n"
MF "v[92:95],a[56:59],a[240:243],v[92:95]\n"
"s_add_u32 s89,s89,s65\n"
"s_mov_b32 s82,s70\n"
TR "v[178:179],v0 offset:2048\n"
MF "v[92:95],a[16:19],v[188:191],v[92:95]\n"
"buffer_store_dwordx4 v[152:155],v9,s[40:43],s90 offen offset:2048 sc1\n"
MF "v[84:87],a[12:15],v[188:191],v[252:255]\n"
"s_add_u32 s88,s88,s65\n"
"s_sub_u32 s72,s85,2\n"
TR "v[180:181],v0 offset:4096\n"
MF "v[84:87],a[52:55],a[240:243],v[84:87]\n"
"s_add_u32 m0,s5,24576\n"
TR "v[182:183],v0 offset:6144\n"
MF "v[84:87],a[92:95],a[244:247],v[84:87]\n"
LD "v192,s[48:51],s89 offen lds\n"
MF "v[76:79],a[88:91],a[244:247],v[252:255]\n"
TR "v[184:185],v0 offset:8192\n"
MF "v[76:79],a[48:51],a[240:243],v[76:79]\n"
TR "v[186:187],v0 offset:10240\n"
MF "v[76:79],a[8:11],v[188:191],v[76:79]\n"
LD "v193,s[48:51],s89 offen offset:1024 lds\n"
MF "v[68:71],a[4:7],v[188:191],v[252:255]\n"
"v_add_f32 v144,v144,v104\n"
"v_add_f32 v145,v145,v105\n"
MF "v[68:71],a[44:47],a[240:243],v[68:71]\n"
"v_add_f32 v146,v146,v106\n"
"v_add_f32 v147,v147,v107\n"
MF "v[68:71],a[84:87],a[244:247],v[68:71]\n"
"v_add_f32 v148,v148,v108\n"
"v_add_f32 v149,v149,v109\n"
MF "v[60:63],a[80:83],a[244:247],v[252:255]\n"
"v_add_f32 v150,v150,v110\n"
"v_add_f32 v151,v151,v111\n"
MF "v[60:63],a[40:43],a[240:243],v[60:63]\n"
CV "v144,v144,v145\n"
CV "v145,v146,v147\n"
MF "v[60:63],a[0:3],v[188:191],v[60:63]\n"
"s_waitcnt lgkmcnt(0)\n"
CV "v146,v148,v149\n"
CV "v147,v150,v151\n"
MF "v[16:19],a[120:123],v[176:179],v[16:19]\n"
TR "v[188:189],v1\n"
MF "v[16:19],a[160:163],v[180:183],v[16:19]\n"
TR "v[190:191],v1 offset:2048\n"
MF "v[16:19],a[200:203],v[184:187],v[16:19]\n"
"buffer_store_dwordx4 v[144:147],v9,s[40:43],s90 offen offset:1024 sc1\n"
MF "v[24:27],a[204:207],v[184:187],v[24:27]\n"
TR "a[240:241],v1 offset:4096\n"
MF "v[24:27],a[164:167],v[180:183],v[24:27]\n"
TR "a[242:243],v1 offset:6144\n"
MF "v[24:27],a[124:127],v[176:179],v[24:27]\n"
LD "v194,s[48:51],s89 offen offset:2048 lds\n"
MF "v[32:35],a[128:131],v[176:179],v[32:35]\n"
TR "a[244:245],v1 offset:8192\n"
MF "v[32:35],a[168:171],v[180:183],v[32:35]\n"
TR "a[246:247],v1 offset:10240\n"
MF "v[32:35],a[208:211],v[184:187],v[32:35]\n"
MF "v[40:43],a[212:215],v[184:187],v[40:43]\n"
MF "v[40:43],a[172:175],v[180:183],v[40:43]\n"
MF "v[40:43],a[132:135],v[176:179],v[40:43]\n"
LD "v195,s[48:51],s89 offen offset:3072 lds\n"
MF "v[48:51],a[136:139],v[176:179],v[48:51]\n"
"v_add_f32 v136,v136,v96\n"
"v_add_f32 v137,v137,v97\n"
MF "v[48:51],a[176:179],v[180:183],v[48:51]\n"
"v_add_f32 v138,v138,v98\n"
"v_add_f32 v139,v139,v99\n"
MF "v[48:51],a[216:219],v[184:187],v[48:51]\n"
"s_waitcnt lgkmcnt(0)\n"
"v_add_f32 v140,v140,v100\n"
"v_add_f32 v141,v141,v101\n"
MF "v[52:55],a[216:219],a[244:247],v[52:55]\n"
TR "v[176:177],v2\n"
"v_add_f32 v142,v142,v102\n"
MF "v[52:55],a[176:179],a[240:243],v[52:55]\n"
TR "v[178:179],v2 offset:2048\n"
"v_add_f32 v143,v143,v103\n"
MF "v[52:55],a[136:139],v[188:191],v[52:55]\n"
CV "v136,v136,v137\n"
CV "v137,v138,v139\n"
MF "v[44:47],a[132:135],v[188:191],v[44:47]\n"
TR "v[180:181],v2 offset:4096\n"
CV "v138,v140,v141\n"
MF "v[44:47],a[172:175],a[240:243],v[44:47]\n"
TR "v[182:183],v2 offset:6144\n"
CV "v139,v142,v143\n"
MF "v[44:47],a[212:215],a[244:247],v[44:47]\n"
"buffer_load_dword v10,v12,s[40:43],s86 offen sc1\n"
MF "v[36:39],a[208:211],a[244:247],v[36:39]\n"
TR "v[184:185],v2 offset:8192\n"
MF "v[36:39],a[168:171],a[240:243],v[36:39]\n"
TR "v[186:187],v2 offset:10240\n"
MF "v[36:39],a[128:131],v[188:191],v[36:39]\n"
"s_add_u32 m0,s5,28672\n"
"buffer_store_dwordx4 v[136:139],v9,s[40:43],s90 offen sc1\n"
MF "v[28:31],a[124:127],v[188:191],v[28:31]\n"
"s_add_u32 s90,s90,20480\n"
MF "v[28:31],a[164:167],a[240:243],v[28:31]\n"
LD "v196,s[48:51],s89 offen lds\n"
MF "v[28:31],a[204:207],a[244:247],v[28:31]\n"
MF "v[20:23],a[200:203],a[244:247],v[20:23]\n"
MF "v[20:23],a[160:163],a[240:243],v[20:23]\n"
MF "v[20:23],a[120:123],v[188:191],v[20:23]\n"
"s_waitcnt lgkmcnt(0)\n"
MF "v[56:59],a[120:123],v[176:179],v[56:59]\n"
TR "v[188:189],v3\n"
MF "v[56:59],a[160:163],v[180:183],v[56:59]\n"
TR "v[190:191],v3 offset:2048\n"
MF "v[56:59],a[200:203],v[184:187],v[56:59]\n"
LD "v197,s[48:51],s89 offen offset:1024 lds\n"
MF "v[64:67],a[204:207],v[184:187],v[64:67]\n"
TR "a[240:241],v3 offset:4096\n"
MF "v[64:67],a[164:167],v[180:183],v[64:67]\n"
TR "a[242:243],v3 offset:6144\n"
MF "v[64:67],a[124:127],v[176:179],v[64:67]\n"
MF "v[72:75],a[128:131],v[176:179],v[72:75]\n"
TR "a[244:245],v3 offset:8192\n"
MF "v[72:75],a[168:171],v[180:183],v[72:75]\n"
TR "a[246:247],v3 offset:10240\n"
MF "v[72:75],a[208:211],v[184:187],v[72:75]\n"
MF "v[80:83],a[212:215],v[184:187],v[80:83]\n"
MF "v[80:83],a[172:175],v[180:183],v[80:83]\n"
MF "v[80:83],a[132:135],v[176:179],v[80:83]\n"
MF "v[88:91],a[136:139],v[176:179],v[88:91]\n"
MF "v[88:91],a[176:179],v[180:183],v[88:91]\n"
"s_waitcnt vmcnt(18) lgkmcnt(0)\n"
MF "v[88:91],a[216:219],v[184:187],v[88:91]\n"
"s_barrier\n"
MF "v[92:95],a[216:219],a[244:247],v[92:95]\n"
"s_add_u32 s80,s89,122880\n"
TR "v[176:177],v0 offset:49152\n"
MF "v[92:95],a[176:179],a[240:243],v[92:95]\n"
"s_add_u32 m0,s5,0\n"
TR "v[178:179],v0 offset:51200\n"
MF "v[92:95],a[136:139],v[188:191],v[92:95]\n"
LD "v192,s[48:51],s80 offen lds\n"
MF "v[84:87],a[132:135],v[188:191],v[84:87]\n"
"s_add_u32 s60,s64,1\n"
TR "v[180:181],v0 offset:53248\n"
MF "v[84:87],a[172:175],a[240:243],v[84:87]\n"
"s_cmp_eq_u32 s60,8\n"
"s_cselect_b32 s60,0,s60\n"
TR "v[182:183],v0 offset:55296\n"
MF "v[84:87],a[212:215],a[244:247],v[84:87]\n"
LD "v193,s[48:51],s80 offen offset:1024 lds\n"
MF "v[76:79],a[208:211],a[244:247],v[76:79]\n"
"s_cmp_eq_u32 s60,10\n"
"s_cselect_b32 s60,s61,s60\n"
TR "v[184:185],v0 offset:57344\n"
MF "v[76:79],a[168:171],a[240:243],v[76:79]\n"
"s_sub_u32 s65,s60,s64\n"
TR "v[186:187],v0 offset:59392\n"
MF "v[76:79],a[128:131],v[188:191],v[76:79]\n"
"s_lshl_b32 s65,s65,7\n"
MF "v[68:71],a[124:127],v[188:191],v[68:71]\n"
"s_mov_b32 s64,s60\n"
MF "v[68:71],a[164:167],a[240:243],v[68:71]\n"
MF "v[68:71],a[204:207],a[244:247],v[68:71]\n"
LD "v194,s[48:51],s80 offen offset:2048 lds\n"
MF "v[60:63],a[200:203],a[244:247],v[60:63]\n"
MF "v[60:63],a[160:163],a[240:243],v[60:63]\n"
MF "v[60:63],a[120:123],v[188:191],v[60:63]\n"
"s_waitcnt lgkmcnt(0)\n"
MF "v[16:19],a[20:23],v[176:179],v[16:19]\n"
TR "v[188:189],v1 offset:49152\n"
MF "v[16:19],a[60:63],v[180:183],v[16:19]\n"
TR "v[190:191],v1 offset:51200\n"
MF "v[16:19],a[100:103],v[184:187],v[16:19]\n"
LD "v195,s[48:51],s80 offen offset:3072 lds\n"
MF "v[24:27],a[104:107],v[184:187],v[24:27]\n"
TR "a[240:241],v1 offset:53248\n"
MF "v[24:27],a[64:67],v[180:183],v[24:27]\n"
"s_add_u32 m0,s5,4096\n"
TR "a[242:243],v1 offset:55296\n"
MF "v[24:27],a[24:27],v[176:179],v[24:27]\n"
LD "v196,s[48:51],s80 offen lds\n"
MF "v[32:35],a[28:31],v[176:179],v[32:35]\n"
TR "a[244:245],v1 offset:57344\n"
MF "v[32:35],a[68:71],v[180:183],v[32:35]\n"
TR "a[246:247],v1 offset:59392\n"
MF "v[32:35],a[108:111],v[184:187],v[32:35]\n"
MF "v[40:43],a[112:115],v[184:187],v[40:43]\n"
MF "v[40:43],a[72:75],v[180:183],v[40:43]\n"
MF "v[40:43],a[32:35],v[176:179],v[40:43]\n"
LD "v197,s[48:51],s80 offen offset:1024 lds\n"
MF "v[48:51],a[36:39],v[176:179],v[48:51]\n"
MF "v[48:51],a[76:79],v[180:183],v[48:51]\n"
MF "v[48:51],a[116:119],v[184:187],v[48:51]\n"
"s_waitcnt lgkmcnt(0)\n"
MF "v[52:55],a[116:119],a[244:247],v[52:55]\n"
TR "v[176:177],v2 offset:49152\n"
MF "v[52:55],a[76:79],a[240:243],v[52:55]\n"
TR "v[178:179],v2 offset:51200\n"
MF "v[52:55],a[36:39],v[188:191],v[52:55]\n"
MF "v[44:47],a[32:35],v[188:191],v[44:47]\n"
TR "v[180:181],v2 offset:53248\n"
MF "v[44:47],a[72:75],a[240:243],v[44:47]\n"
TR "v[182:183],v2 offset:55296\n"
MF "v[44:47],a[112:115],a[244:247],v[44:47]\n"
MF "v[36:39],a[108:111],a[244:247],v[36:39]\n"
TR "v[184:185],v2 offset:57344\n"
MF "v[36:39],a[68:71],a[240:243],v[36:39]\n"
TR "v[186:187],v2 offset:59392\n"
MF "v[36:39],a[28:31],v[188:191],v[36:39]\n"
MF "v[28:31],a[24:27],v[188:191],v[28:31]\n"
MF "v[28:31],a[64:67],a[240:243],v[28:31]\n"
MF "v[28:31],a[104:107],a[244:247],v[28:31]\n"
MF "v[20:23],a[100:103],a[244:247],v[20:23]\n"
MF "v[20:23],a[60:63],a[240:243],v[20:23]\n"
"s_waitcnt vmcnt(9)\n"
MF "v[20:23],a[20:23],v[188:191],v[20:23]\n"
"v_mov_b32 v236,v228\n"
"v_mov_b32 v237,v229\n"
MF "v[56:59],a[252:255],v[208:211],v[56:59]\n"
MF "v[64:67],a[252:255],v[212:215],v[64:67]\n"
MF "v[72:75],a[252:255],v[216:219],v[72:75]\n"
MF "v[80:83],a[252:255],v[220:223],v[80:83]\n"
MF "v[88:91],a[252:255],v[224:227],v[88:91]\n"
MF "v[60:63],a[248:251],v[208:211],v[60:63]\n"
MF "v[68:71],a[248:251],v[212:215],v[68:71]\n"
MF "v[76:79],a[248:251],v[216:219],v[76:79]\n"
MF "v[84:87],a[248:251],v[220:223],v[84:87]\n"
MF "v[92:95],a[248:251],v[224:227],v[92:95]\n"
MF "v[248:251],v[230:233],a[250:253],v[248:251]\n"
MF "v[244:247],v[234:237],a[250:253],v[244:247]\n"
"s_waitcnt lgkmcnt(0)\n"
MF "v[56:59],a[20:23],v[176:179],v[56:59]\n"
TR "v[188:189],v3 offset:49152\n"
"v_sub_u32 v10,v10,v11\n"
MF "v[56:59],a[60:63],v[180:183],v[56:59]\n"
TR "v[190:191],v3 offset:51200\n"
MF "v[56:59],a[100:103],v[184:187],v[56:59]\n"
"v_mov_b32 v14,s76\n"
"s_add_u32 s76,s76,1\n"
MF "v[64:67],a[104:107],v[184:187],v[64:67]\n"
TR "a[240:241],v3 offset:53248\n"
MF "v[64:67],a[64:67],v[180:183],v[64:67]\n"
TR "a[242:243],v3 offset:55296\n"
MF "v[64:67],a[24:27],v[176:179],v[64:67]\n"
".Lnf2:\n"
"v_readlane_b32 s58,v10,s67\n"
"s_cmp_lt_u32 s58,s72\n"
"s_cbranch_scc1 .Lsp2\n"
".Lnb2:\n"
MF "v[72:75],a[28:31],v[176:179],v[72:75]\n"
"s_add_u32 s70,s70,20480\n"
TR "a[244:245],v3 offset:57344\n"
MF "v[72:75],a[68:71],v[180:183],v[72:75]\n"
TR "a[246:247],v3 offset:59392\n"
MF "v[72:75],a[108:111],v[184:187],v[72:75]\n"
LD "v[208:211],v9,s[40:43],s82 offen sc1\n"
MF "v[80:83],a[112:115],v[184:187],v[80:83]\n"
MF "v[80:83],a[72:75],v[180:183],v[80:83]\n"
LD "v[212:215],v9,s[40:43],s82 offen offset:1024 sc1\n"
MF "v[80:83],a[32:35],v[176:179],v[80:83]\n"
MF "v[88:91],a[36:39],v[176:179],v[88:91]\n"
MF "v[88:91],a[76:79],v[180:183],v[88:91]\n"
"s_waitcnt vmcnt(10) lgkmcnt(0)\n"
LD "v[216:219],v9,s[40:43],s82 offen offset:2048 sc1\n"
MF "v[88:91],a[116:119],v[184:187],v[88:91]\n"
"s_barrier\n"
MF "v[92:95],a[116:119],a[244:247],v[92:95]\n"
TR "v[176:177],v4\n"
MF "v[92:95],a[76:79],a[240:243],v[92:95]\n"
TR "v[178:179],v4 offset:2048\n"
MF "v[92:95],a[36:39],v[188:191],v[92:95]\n"
"buffer_store_dword v14,v15,s[40:43],s94 offen sc1\n"
MF "v[84:87],a[32:35],v[188:191],v[84:87]\n"
TR "v[180:181],v4 offset:4096\n"
MF "v[84:87],a[72:75],a[240:243],v[84:87]\n"
TR "v[182:183],v4 offset:6144\n"
MF "v[84:87],a[112:115],a[244:247],v[84:87]\n"
LD "v[220:223],v9,s[40:43],s82 offen offset:3072 sc1\n"
MF "v[76:79],a[108:111],a[244:247],v[76:79]\n"
TR "v[184:185],v4 offset:8192\n"
MF "v[76:79],a[68:71],a[240:243],v[76:79]\n"
"s_add_u32 s82,s82,4096\n"
TR "v[186:187],v4 offset:10240\n"
MF "v[76:79],a[28:31],v[188:191],v[76:79]\n"
LD "v[224:227],v9,s[40:43],s82 offen sc1\n"
MF "v[68:71],a[24:27],v[188:191],v[68:71]\n"
"s_add_u32 s2,s2,204800\n"
MF "v[68:71],a[64:67],a[240:243],v[68:71]\n"
"s_add_u32 m0,s5,73728\n"
MF "v[68:71],a[104:107],a[244:247],v[68:71]\n"
LD "v[228:231],v9,s[40:43],s2 offen sc1\n"
MF "v[60:63],a[100:103],a[244:247],v[60:63]\n"
MF "v[60:63],a[60:63],a[240:243],v[60:63]\n"
"s_add_u32 s2,s2,204800\n"
MF "v[60:63],a[20:23],v[188:191],v[60:63]\n"
"s_waitcnt lgkmcnt(0)\n"
LD "v[232:235],v9,s[40:43],s2 offen sc1\n"
MF "v[16:19],a[140:143],v[176:179],v[16:19]\n"
"s_nop 0\n"
TR "v[188:189],v5\n"
TR "v[190:191],v5 offset:2048\n"
MF "v[16:19],a[180:183],v[180:183],v[16:19]\n"
"s_nop 0\n"
TR "a[240:241],v5 offset:4096\n"
TR "a[242:243],v5 offset:6144\n"
MF "v[16:19],a[220:223],v[184:187],v[16:19]\n"
"s_nop 0\n"
TR "a[244:245],v5 offset:8192\n"
TR "a[246:247],v5 offset:10240\n"
MF "v[24:27],a[224:227],v[184:187],v[24:27]\n"
MF "v[24:27],a[184:187],v[180:183],v[24:27]\n"
MF "v[24:27],a[144:147],v[176:179],v[24:27]\n"
"s_nop 0\n"
"s_nop 0\n"
"ds_write_addtid_b32 v16\n"
"ds_write_addtid_b32 v17 offset:256\n"
MF "v[32:35],a[148:151],v[176:179],v[32:35]\n"
"s_nop 0\n"
"ds_write_addtid_b32 v18 offset:512\n"
"ds_write_addtid_b32 v19 offset:768\n"
MF "v[32:35],a[188:191],v[180:183],v[32:35]\n"
MF "v[32:35],a[228:231],v[184:187],v[32:35]\n"
"s_nop 0\n"
"ds_write_addtid_b32 v24 offset:2048\n"
"ds_write_addtid_b32 v25 offset:2304\n"
MF "v[40:43],a[232:235],v[184:187],v[40:43]\n"
"s_nop 0\n"
"ds_write_addtid_b32 v26 offset:2560\n"
"ds_write_addtid_b32 v27 offset:2816\n"
MF "v[40:43],a[192:195],v[180:183],v[40:43]\n"
MF "v[40:43],a[152:155],v[176:179],v[40:43]\n"
"s_nop 0\n"
"ds_write_addtid_b32 v32 offset:4096\n"
"ds_write_addtid_b32 v33 offset:4352\n"
MF "v[48:51],a[156:159],v[176:179],v[48:51]\n"
"s_nop 0\n"
"ds_write_addtid_b32 v34 offset:4608\n"
"ds_write_addtid_b32 v35 offset:4864\n"
MF "v[48:51],a[196:199],v[180:183],v[48:51]\n"
MF "v[48:51],a[236:239],v[184:187],v[48:51]\n"
"s_waitcnt lgkmcnt(12)\n"
"ds_write_addtid_b32 v40 offset:50176\n"
"ds_write_addtid_b32 v41 offset:50432\n"
MF "v[52:55],a[236:239],a[244:247],v[52:55]\n"
"s_nop 0\n"
TR "v[176:177],v6\n"
TR "v[178:179],v6 offset:2048\n"
MF "v[52:55],a[196:199],a[240:243],v[52:55]\n"
"s_nop 0\n"
TR "v[180:181],v6 offset:4096\n"
TR "v[182:183],v6 offset:6144\n"
MF "v[52:55],a[156:159],v[188:191],v[52:55]\n"
"s_nop 0\n"
TR "v[184:185],v6 offset:8192\n"
TR "v[186:187],v6 offset:10240\n"
MF "v[44:47],a[152:155],v[188:191],v[44:47]\n"
"s_nop 0\n"
"ds_write_addtid_b32 v42 offset:50688\n"
"ds_write_addtid_b32 v43 offset:50944\n"
MF "v[44:47],a[192:195],a[240:243],v[44:47]\n"
"s_nop 0\n"
"ds_write_addtid_b32 v48 offset:52224\n"
"ds_write_addtid_b32 v49 offset:52480\n"
MF "v[44:47],a[232:235],a[244:247],v[44:47]\n"
"s_nop 0\n"
"ds_write_addtid_b32 v50 offset:52736\n"
"ds_write_addtid_b32 v51 offset:52992\n"
MF "v[36:39],a[228:231],a[244:247],v[36:39]\n"
"s_nop 0\n"
"ds_write_addtid_b32 v52 offset:53248\n"
"ds_write_addtid_b32 v53 offset:53504\n"
MF "v[36:39],a[188:191],a[240:243],v[36:39]\n"
"s_nop 0\n"
"ds_write_addtid_b32 v54 offset:53760\n"
"ds_write_addtid_b32 v55 offset:54016\n"
MF "v[36:39],a[148:151],v[188:191],v[36:39]\n"
"s_nop 0\n"
"ds_write_addtid_b32 v44 offset:51200\n"
"ds_write_addtid_b32 v45 offset:51456\n"
MF "v[28:31],a[144:147],v[188:191],v[28:31]\n"
"s_nop 0\n"
"ds_write_addtid_b32 v46 offset:51712\n"
"ds_write_addtid_b32 v47 offset:51968\n"
MF "v[28:31],a[184:187],a[240:243],v[28:31]\n"
MF "v[28:31],a[224:227],a[244:247],v[28:31]\n"
"s_nop 0\n"
"ds_write_addtid_b32 v36 offset:49152\n"
"ds_write_addtid_b32 v37 offset:49408\n"
MF "v[20:23],a[220:223],a[244:247],v[20:23]\n"
"s_nop 0\n"
"ds_write_addtid_b32 v38 offset:49664\n"
"ds_write_addtid_b32 v39 offset:49920\n"
MF "v[20:23],a[180:183],a[240:243],v[20:23]\n"
MF "v[20:23],a[140:143],v[188:191],v[20:23]\n"
"s_waitcnt lgkmcnt(15)\n"
"ds_write_addtid_b32 v28 offset:3072\n"
"ds_write_addtid_b32 v29 offset:3328\n"
MF "v[56:59],a[140:143],v[176:179],v[56:59]\n"
"s_nop 0\n"
TR "v[188:189],v7\n"
TR "v[190:191],v7 offset:2048\n"
MF "v[56:59],a[180:183],v[180:183],v[56:59]\n"
"s_nop 0\n"
TR "a[240:241],v7 offset:4096\n"
TR "a[242:243],v7 offset:6144\n"
MF "v[56:59],a[220:223],v[184:187],v[56:59]\n"
"s_nop 0\n"
TR "a[244:245],v7 offset:8192\n"
TR "a[246:247],v7 offset:10240\n"
MF "v[64:67],a[224:227],v[184:187],v[64:67]\n"
"s_nop 0\n"
"ds_write_addtid_b32 v30 offset:3584\n"
"ds_write_addtid_b32 v31 offset:3840\n"
MF "v[64:67],a[184:187],v[180:183],v[64:67]\n"
"s_nop 0\n"
"ds_write_addtid_b32 v20 offset:1024\n"
"ds_write_addtid_b32 v21 offset:1280\n"
MF "v[64:67],a[144:147],v[176:179],v[64:67]\n"
"s_nop 0\n"
"ds_write_addtid_b32 v22 offset:1536\n"
"ds_write_addtid_b32 v23 offset:1792\n"
MF "v[72:75],a[148:151],v[176:179],v[72:75]\n"
"s_add_u32 m0,s5,49152\n"
MF "v[72:75],a[188:191],v[180:183],v[72:75]\n"
LD "v192,s[48:51],s88 offen lds\n"
MF "v[72:75],a[228:231],v[184:187],v[72:75]\n"
MF "v[80:83],a[232:235],v[184:187],v[80:83]\n"
MF "v[80:83],a[192:195],v[180:183],v[80:83]\n"
MF "v[80:83],a[152:155],v[176:179],v[80:83]\n"
MF "v[88:91],a[156:159],v[176:179],v[88:91]\n"
LD "v193,s[48:51],s88 offen offset:1024 lds\n"
MF "v[88:91],a[196:199],v[180:183],v[88:91]\n"
"s_waitcnt vmcnt(16) lgkmcnt(0)\n"
MF "v[88:91],a[236:239],v[184:187],v[88:91]\n"
"s_barrier\n"
MF "v[92:95],a[236:239],a[244:247],v[92:95]\n"
"s_nop 0\n"
TR "v[176:177],v0 offset:24576\n"
TR "v[178:179],v0 offset:26624\n"
MF "v[92:95],a[196:199],a[240:243],v[92:95]\n"
"s_add_u32 s85,s85,1\n"
TR "v[180:181],v0 offset:28672\n"
TR "v[182:183],v0 offset:30720\n"
MF "v[92:95],a[156:159],v[188:191],v[92:95]\n"
"s_nop 0\n"
TR "v[184:185],v0 offset:32768\n"
TR "v[186:187],v0 offset:34816\n"
MF "v[84:87],a[152:155],v[188:191],v[84:87]\n"
LD "v194,s[48:51],s88 offen offset:2048 lds\n"
MF "v[84:87],a[192:195],a[240:243],v[84:87]\n"
"ds_read2st64_b32 v[48:49],v13 offset0:204 offset1:205\n"
MF "v[84:87],a[232:235],a[244:247],v[84:87]\n"
"ds_read2st64_b32 v[50:51],v13 offset0:206 offset1:207\n"
MF "v[76:79],a[228:231],a[244:247],v[76:79]\n"
"ds_read2st64_b32 v[52:53],v13 offset0:208 offset1:209\n"
MF "v[76:79],a[188:191],a[240:243],v[76:79]\n"
LD "v195,s[48:51],s88 offen offset:3072 lds\n"
MF "v[76:79],a[148:151],v[188:191],v[76:79]\n"
"ds_read2st64_b32 v[54:55],v13 offset0:210 offset1:211\n"
MF "v[68:71],a[144:147],v[188:191],v[68:71]\n"
"ds_read2st64_b32 v[40:41],v13 offset0:196 offset1:197\n"
MF "v[68:71],a[184:187],a[240:243],v[68:71]\n"
"ds_read2st64_b32 v[42:43],v13 offset0:198 offset1:199\n"
MF "v[68:71],a[224:227],a[244:247],v[68:71]\n"
MF "v[60:63],a[220:223],a[244:247],v[60:63]\n"
MF "v[60:63],a[180:183],a[240:243],v[60:63]\n"
"s_cmp_lg_u32 s85,9\n"
MF "v[60:63],a[140:143],v[188:191],v[60:63]\n"
"s_waitcnt lgkmcnt(6)\n"
"s_cbranch_scc1 .Lps9\n"
MF "v[96:99],v[176:179],a[0:3],v[252:255]\n"
"s_nop 0\n"
TR "v[188:189],v1 offset:24576\n"
TR "v[190:191],v1 offset:26624\n"
MF "v[96:99],v[180:183],a[40:43],v[96:99]\n"
"s_nop 0\n"
TR "a[240:241],v1 offset:28672\n"
TR "a[242:243],v1 offset:30720\n"
MF "v[96:99],v[184:187],a[80:83],v[96:99]\n"
"s_waitcnt lgkmcnt(6)\n"
TR "a[244:245],v1 offset:32768\n"
TR "a[246:247],v1 offset:34816\n"
MF "v[104:107],v[184:187],a[84:87],v[252:255]\n"
"buffer_load_dword v10,v12,s[40:43],s86 offen sc1\n"
MF "v[104:107],v[180:183],a[44:47],v[104:107]\n"
"s_add_u32 m0,s5,53248\n"
"ds_read2st64_b32 v[44:45],v13 offset0:200 offset1:201\n"
"v_add_f32 v88,v88,v48\n"
MF "v[104:107],v[176:179],a[4:7],v[104:107]\n"
".Lpg30h:\n"
"s_cbranch_execz .Lisr4\n"
"ds_read2st64_b32 v[46:47],v13 offset0:202 offset1:203\n"
"v_add_f32 v89,v89,v49\n"
LD "v196,s[48:51],s88 offen lds\n"
MF "v[112:115],v[176:179],a[8:11],v[252:255]\n"
"ds_read2st64_b32 v[32:33],v13 offset0:16 offset1:17\n"
"v_add_f32 v90,v90,v50\n"
MF "v[112:115],v[180:183],a[48:51],v[112:115]\n"
"ds_read2st64_b32 v[34:35],v13 offset0:18 offset1:19\n"
"v_add_f32 v91,v91,v51\n"
MF "v[112:115],v[184:187],a[88:91],v[112:115]\n"
"ds_read2st64_b32 v[36:37],v13 offset0:192 offset1:193\n"
"v_add_f32 v92,v92,v52\n"
MF "v[120:123],v[184:187],a[92:95],v[252:255]\n"
"ds_read2st64_b32 v[38:39],v13 offset0:194 offset1:195\n"
"v_add_f32 v93,v93,v53\n"
MF "v[120:123],v[180:183],a[52:55],v[120:123]\n"
"ds_read2st64_b32 v[24:25],v13 offset0:8 offset1:9\n"
"v_add_f32 v94,v94,v54\n"
LD "v197,s[48:51],s88 offen offset:1024 lds\n"
MF "v[120:123],v[176:179],a[12:15],v[120:123]\n"
"ds_read2st64_b32 v[26:27],v13 offset0:10 offset1:11\n"
"v_add_f32 v95,v95,v55\n"
MF "v[128:131],v[176:179],a[16:19],v[252:255]\n"
"ds_read2st64_b32 v[28:29],v13 offset0:12 offset1:13\n"
CV "v88,v88,v89\n"
MF "v[128:131],v[180:183],a[56:59],v[128:131]\n"
"ds_read2st64_b32 v[30:31],v13 offset0:14 offset1:15\n"
CV "v89,v90,v91\n"
MF "v[128:131],v[184:187],a[96:99],v[128:131]\n"
"s_add_u32 s80,s88,122880\n"
"s_add_u32 m0,s5,98304\n"
"s_waitcnt lgkmcnt(10)\n"
"ds_read2st64_b32 v[16:17],v13 offset0:0 offset1:1\n"
CV "v90,v92,v93\n"
MF "v[132:135],a[244:247],a[96:99],v[252:255]\n"
"s_nop 0\n"
TR "v[176:177],v2 offset:24576\n"
TR "v[178:179],v2 offset:26624\n"
LD "v192,s[48:51],s80 offen lds\n"
MF "v[132:135],a[240:243],a[56:59],v[132:135]\n"
"s_nop 0\n"
TR "v[180:181],v2 offset:28672\n"
TR "v[182:183],v2 offset:30720\n"
MF "v[132:135],v[188:191],a[16:19],v[132:135]\n"
"s_nop 0\n"
TR "v[184:185],v2 offset:32768\n"
TR "v[186:187],v2 offset:34816\n"
MF "v[124:127],v[188:191],a[12:15],v[252:255]\n"
"s_waitcnt lgkmcnt(15)\n"
"ds_read2st64_b32 v[18:19],v13 offset0:2 offset1:3\n"
CV "v91,v94,v95\n"
MF "v[124:127],a[240:243],a[52:55],v[124:127]\n"
"ds_read2st64_b32 v[20:21],v13 offset0:4 offset1:5\n"
"v_add_f32 v80,v80,v40\n"
MF "v[124:127],a[244:247],a[92:95],v[124:127]\n"
"s_add_u32 s87,s90,4096\n"
"ds_read2st64_b32 v[22:23],v13 offset0:6 offset1:7\n"
"v_add_f32 v81,v81,v41\n"
LD "v193,s[48:51],s80 offen offset:1024 lds\n"
MF "v[116:119],a[244:247],a[88:91],v[252:255]\n"
"buffer_store_dwordx4 v[88:91],v9,s[40:43],s87 offen sc1\n"
MF "v[116:119],a[240:243],a[48:51],v[116:119]\n"
"v_add_f32 v82,v82,v42\n"
"v_add_f32 v83,v83,v43\n"
MF "v[116:119],v[188:191],a[8:11],v[116:119]\n"
"v_add_f32 v84,v84,v44\n"
"v_add_f32 v85,v85,v45\n"
MF "v[108:111],v[188:191],a[4:7],v[252:255]\n"
"v_add_f32 v86,v86,v46\n"
"v_add_f32 v87,v87,v47\n"
MF "v[108:111],a[240:243],a[44:47],v[108:111]\n"
CV "v80,v80,v81\n"
CV "v81,v82,v83\n"
LD "v194,s[48:51],s80 offen offset:2048 lds\n"
MF "v[108:111],a[244:247],a[84:87],v[108:111]\n"
"s_waitcnt lgkmcnt(14)\n"
CV "v82,v84,v85\n"
CV "v83,v86,v87\n"
MF "v[100:103],a[244:247],a[80:83],v[252:255]\n"
"buffer_store_dwordx4 v[80:83],v9,s[40:43],s90 offen offset:3072 sc1\n"
MF "v[100:103],a[240:243],a[40:43],v[100:103]\n"
"v_add_f32 v72,v72,v32\n"
"v_add_f32 v73,v73,v33\n"
MF "v[100:103],v[188:191],a[0:3],v[100:103]\n"
"s_waitcnt lgkmcnt(3)\n"
"v_add_f32 v74,v74,v34\n"
"v_add_f32 v75,v75,v35\n"
MF "v[136:139],v[176:179],a[0:3],v[252:255]\n"
"s_nop 0\n"
TR "v[188:189],v3 offset:24576\n"
TR "v[190:191],v3 offset:26624\n"
LD "v195,s[48:51],s80 offen offset:3072 lds\n"
MF "v[136:139],v[180:183],a[40:43],v[136:139]\n"
"s_nop 0\n"
TR "a[240:241],v3 offset:28672\n"
TR "a[242:243],v3 offset:30720\n"
MF "v[136:139],v[184:187],a[80:83],v[136:139]\n"
"s_nop 0\n"
TR "a[244:245],v3 offset:32768\n"
TR "a[246:247],v3 offset:34816\n"
MF "v[144:147],v[184:187],a[84:87],v[252:255]\n"
"v_add_f32 v76,v76,v36\n"
"v_add_f32 v77,v77,v37\n"
MF "v[144:147],v[180:183],a[44:47],v[144:147]\n"
"s_add_u32 m0,s5,102400\n"
"v_add_f32 v78,v78,v38\n"
"v_add_f32 v79,v79,v39\n"
MF "v[144:147],v[176:179],a[4:7],v[144:147]\n"
CV "v72,v72,v73\n"
CV "v73,v74,v75\n"
LD "v196,s[48:51],s80 offen lds\n"
MF "v[152:155],v[176:179],a[8:11],v[252:255]\n"
CV "v74,v76,v77\n"
CV "v75,v78,v79\n"
MF "v[152:155],v[180:183],a[48:51],v[152:155]\n"
"buffer_store_dwordx4 v[72:75],v9,s[40:43],s90 offen offset:2048 sc1\n"
MF "v[152:155],v[184:187],a[88:91],v[152:155]\n"
"v_add_f32 v64,v64,v24\n"
"v_add_f32 v65,v65,v25\n"
MF "v[160:163],v[184:187],a[92:95],v[252:255]\n"
"v_add_f32 v66,v66,v26\n"
"v_add_f32 v67,v67,v27\n"
MF "v[160:163],v[180:183],a[52:55],v[160:163]\n"
"v_add_f32 v68,v68,v28\n"
"v_add_f32 v69,v69,v29\n"
LD "v197,s[48:51],s80 offen offset:1024 lds\n"
MF "v[160:163],v[176:179],a[12:15],v[160:163]\n"
"v_add_f32 v70,v70,v30\n"
"v_add_f32 v71,v71,v31\n"
MF "v[168:171],v[176:179],a[16:19],v[252:255]\n"
CV "v64,v64,v65\n"
CV "v65,v66,v67\n"
MF "v[168:171],v[180:183],a[56:59],v[168:171]\n"
"s_waitcnt vmcnt(24) lgkmcnt(0)\n"
CV "v66,v68,v69\n"
CV "v67,v70,v71\n"
MF "v[168:171],v[184:187],a[96:99],v[168:171]\n"
"s_barrier\n"
MF "v[172:175],a[244:247],a[96:99],v[252:255]\n"
TR "v[176:177],v0\n"
"v_add_f32 v56,v56,v16\n"
MF "v[172:175],a[240:243],a[56:59],v[172:175]\n"
TR "v[178:179],v0 offset:2048\n"
"v_add_f32 v57,v57,v17\n"
MF "v[172:175],v[188:191],a[16:19],v[172:175]\n"
"buffer_store_dwordx4 v[64:67],v9,s[40:43],s90 offen offset:1024 sc1\n"
MF "v[164:167],v[188:191],a[12:15],v[252:255]\n"
TR "v[180:181],v0 offset:4096\n"
"v_add_f32 v58,v58,v18\n"
MF "v[164:167],a[240:243],a[52:55],v[164:167]\n"
TR "v[182:183],v0 offset:6144\n"
"v_add_f32 v59,v59,v19\n"
MF "v[164:167],a[244:247],a[92:95],v[164:167]\n"
"v_add_f32 v60,v60,v20\n"
"v_add_f32 v61,v61,v21\n"
MF "v[156:159],a[244:247],a[88:91],v[252:255]\n"
TR "v[184:185],v0 offset:8192\n"
"v_add_f32 v62,v62,v22\n"
MF "v[156:159],a[240:243],a[48:51],v[156:159]\n"
TR "v[186:187],v0 offset:10240\n"
"v_add_f32 v63,v63,v23\n"
MF "v[156:159],v[188:191],a[8:11],v[156:159]\n"
CV "v56,v56,v57\n"
CV "v57,v58,v59\n"
MF "v[148:151],v[188:191],a[4:7],v[252:255]\n"
CV "v58,v60,v61\n"
CV "v59,v62,v63\n"
MF "v[148:151],a[240:243],a[44:47],v[148:151]\n"
"buffer_store_dwordx4 v[56:59],v9,s[40:43],s90 offen sc1\n"
MF "v[148:151],a[244:247],a[84:87],v[148:151]\n"
"s_add_u32 s90,s90,20480\n"
MF "v[140:143],a[244:247],a[80:83],v[252:255]\n"
MF "v[140:143],a[240:243],a[40:43],v[140:143]\n"
"s_waitcnt vmcnt(13)\n"
"v_sub_u32 v10,v10,v11\n"
MF "v[140:143],v[188:191],a[0:3],v[140:143]\n"
"s_waitcnt lgkmcnt(0)\n"
"v_readlane_b32 s16,v10,s16\n"
MF "v[96:99],v[176:179],a[120:123],v[96:99]\n"
"s_cmp_lt_u32 s16,7\n"
"s_cselect_b32 s16,0xf0000000,0\n"
TR "v[188:189],v1\n"
MF "v[96:99],v[180:183],a[160:163],v[96:99]\n"
TR "v[190:191],v1 offset:2048\n"
"v_readlane_b32 s17,v10,s17\n"
MF "v[96:99],v[184:187],a[200:203],v[96:99]\n"
"s_cmp_lt_u32 s17,8\n"
"s_cselect_b32 s17,0xf0000000,0\n"
MF "v[104:107],v[184:187],a[204:207],v[104:107]\n"
TR "a[240:241],v1 offset:4096\n"
MF "v[104:107],v[180:183],a[164:167],v[104:107]\n"
"s_or_b32 s62,s6,s16\n"
TR "a[242:243],v1 offset:6144\n"
MF "v[104:107],v[176:179],a[124:127],v[104:107]\n"
LD "a[0:3],v9,s[40:43],s62 offen sc1\n"
MF "v[112:115],v[176:179],a[128:131],v[112:115]\n"
TR "a[244:245],v1 offset:8192\n"
MF "v[112:115],v[180:183],a[168:171],v[112:115]\n"
TR "a[246:247],v1 offset:10240\n"
MF "v[112:115],v[184:187],a[208:211],v[112:115]\n"
MF "v[120:123],v[184:187],a[212:215],v[120:123]\n"
LD "a[4:7],v9,s[40:43],s62 offen offset:1024 sc1\n"
MF "v[120:123],v[180:183],a[172:175],v[120:123]\n"
MF "v[120:123],v[176:179],a[132:135],v[120:123]\n"
MF "v[128:131],v[176:179],a[136:139],v[128:131]\n"
MF "v[128:131],v[180:183],a[176:179],v[128:131]\n"
LD "a[8:11],v9,s[40:43],s62 offen offset:2048 sc1\n"
MF "v[128:131],v[184:187],a[216:219],v[128:131]\n"
"s_waitcnt vmcnt(21)\n"
"v_mov_b32 v236,v228\n"
"v_mov_b32 v237,v229\n"
MF "v[136:139],v[208:211],a[252:255],v[136:139]\n"
MF "v[144:147],v[212:215],a[252:255],v[144:147]\n"
MF "v[152:155],v[216:219],a[252:255],v[152:155]\n"
MF "v[160:163],v[220:223],a[252:255],v[160:163]\n"
MF "v[168:171],v[224:227],a[252:255],v[168:171]\n"
MF "v[140:143],v[208:211],a[248:251],v[140:143]\n"
MF "v[148:151],v[212:215],a[248:251],v[148:151]\n"
MF "v[156:159],v[216:219],a[248:251],v[156:159]\n"
MF "v[164:167],v[220:223],a[248:251],v[164:167]\n"
MF "v[172:175],v[224:227],a[248:251],v[172:175]\n"
MF "v[248:251],v[230:233],a[250:253],v[248:251]\n"
MF "v[244:247],v[234:237],a[250:253],v[244:247]\n"
"s_waitcnt lgkmcnt(0)\n"
MF "v[132:135],a[244:247],a[216:219],v[132:135]\n"
TR "v[176:177],v2\n"
MF "v[132:135],a[240:243],a[176:179],v[132:135]\n"
TR "v[178:179],v2 offset:2048\n"
MF "v[132:135],v[188:191],a[136:139],v[132:135]\n"
LD "a[12:15],v9,s[40:43],s62 offen offset:3072 sc1\n"
MF "v[124:127],v[188:191],a[132:135],v[124:127]\n"
TR "v[180:181],v2 offset:4096\n"
CV "v244,v244,v245\n"
MF "v[124:127],a[240:243],a[172:175],v[124:127]\n"
TR "v[182:183],v2 offset:6144\n"
CV "v245,v246,v247\n"
MF "v[124:127],a[244:247],a[212:215],v[124:127]\n"
CV "v246,v248,v249\n"
CV "v247,v250,v251\n"
MF "v[116:119],a[244:247],a[208:211],v[116:119]\n"
TR "v[184:185],v2 offset:8192\n"
"v_permlane16_swap_b32 v244,v246\n"
MF "v[116:119],a[240:243],a[168:171],v[116:119]\n"
"s_add_u32 s62,s62,4096\n"
TR "v[186:187],v2 offset:10240\n"
"v_permlane16_swap_b32 v245,v247\n"
MF "v[116:119],v[188:191],a[128:131],v[116:119]\n"
"buffer_store_dwordx4 v[244:247],v8,s[44:47],s0 offen sc1\n"
MF "v[108:111],v[188:191],a[124:127],v[108:111]\n"
LD "a[16:19],v9,s[40:43],s62 offen sc1\n"
MF "v[108:111],a[240:243],a[164:167],v[108:111]\n"
MF "v[108:111],a[244:247],a[204:207],v[108:111]\n"
MF "v[100:103],a[244:247],a[200:203],v[100:103]\n"
MF "v[100:103],a[240:243],a[160:163],v[100:103]\n"
"s_or_b32 s62,s7,s17\n"
MF "v[100:103],v[188:191],a[120:123],v[100:103]\n"
"s_waitcnt lgkmcnt(0)\n"
LD "a[40:43],v9,s[40:43],s62 offen sc1\n"
MF "v[136:139],v[176:179],a[120:123],v[136:139]\n"
TR "v[188:189],v3\n"
MF "v[136:139],v[180:183],a[160:163],v[136:139]\n"
TR "v[190:191],v3 offset:2048\n"
MF "v[136:139],v[184:187],a[200:203],v[136:139]\n"
MF "v[144:147],v[184:187],a[204:207],v[144:147]\n"
TR "a[240:241],v3 offset:4096\n"
MF "v[144:147],v[180:183],a[164:167],v[144:147]\n"
TR "a[242:243],v3 offset:6144\n"
MF "v[144:147],v[176:179],a[124:127],v[144:147]\n"
LD "a[44:47],v9,s[40:43],s62 offen offset:1024 sc1\n"
MF "v[152:155],v[176:179],a[128:131],v[152:155]\n"
TR "a[244:245],v3 offset:8192\n"
MF "v[152:155],v[180:183],a[168:171],v[152:155]\n"
TR "a[246:247],v3 offset:10240\n"
MF "v[152:155],v[184:187],a[208:211],v[152:155]\n"
MF "v[160:163],v[184:187],a[212:215],v[160:163]\n"
LD "a[48:51],v9,s[40:43],s62 offen offset:2048 sc1\n"
MF "v[160:163],v[180:183],a[172:175],v[160:163]\n"
MF "v[160:163],v[176:179],a[132:135],v[160:163]\n"
MF "v[168:171],v[176:179],a[136:139],v[168:171]\n"
MF "v[168:171],v[180:183],a[176:179],v[168:171]\n"
"s_waitcnt vmcnt(20) lgkmcnt(0)\n"
LD "a[52:55],v9,s[40:43],s62 offen offset:3072 sc1\n"
MF "v[168:171],v[184:187],a[216:219],v[168:171]\n"
"s_barrier\n"
MF "v[172:175],a[244:247],a[216:219],v[172:175]\n"
TR "v[176:177],v0 offset:49152\n"
MF "v[172:175],a[240:243],a[176:179],v[172:175]\n"
TR "v[178:179],v0 offset:51200\n"
"v_mov_b32 v14,s76\n"
MF "v[172:175],v[188:191],a[136:139],v[172:175]\n"
"s_add_u32 s76,s76,1\n"
MF "v[164:167],v[188:191],a[132:135],v[164:167]\n"
TR "v[180:181],v0 offset:53248\n"
MF "v[164:167],a[240:243],a[172:175],v[164:167]\n"
"s_add_u32 s62,s62,4096\n"
TR "v[182:183],v0 offset:55296\n"
MF "v[164:167],a[244:247],a[212:215],v[164:167]\n"
LD "a[56:59],v9,s[40:43],s62 offen sc1\n"
MF "v[156:159],a[244:247],a[208:211],v[156:159]\n"
TR "v[184:185],v0 offset:57344\n"
MF "v[156:159],a[240:243],a[168:171],v[156:159]\n"
TR "v[186:187],v0 offset:59392\n"
MF "v[156:159],v[188:191],a[128:131],v[156:159]\n"
MF "v[148:151],v[188:191],a[124:127],v[148:151]\n"
MF "v[148:151],a[240:243],a[164:167],v[148:151]\n"
MF "v[148:151],a[244:247],a[204:207],v[148:151]\n"
MF "v[140:143],a[244:247],a[200:203],v[140:143]\n"
"s_waitcnt vmcnt(11) lgkmcnt(0)\n"
MF "v[140:143],a[240:243],a[160:163],v[140:143]\n"
"s_barrier\n"
MF "v[140:143],v[188:191],a[120:123],v[140:143]\n"
"buffer_store_dword v14,v15,s[40:43],s94 offen sc1\n"
MF "v[96:99],v[176:179],a[20:23],v[96:99]\n"
TR "v[188:189],v1 offset:49152\n"
MF "v[96:99],v[180:183],a[60:63],v[96:99]\n"
TR "v[190:191],v1 offset:51200\n"
MF "v[96:99],v[184:187],a[100:103],v[96:99]\n"
LD "a[80:83],v9,s[40:43],s3 offen sc1\n"
MF "v[104:107],v[184:187],a[104:107],v[104:107]\n"
TR "a[240:241],v1 offset:53248\n"
MF "v[104:107],v[180:183],a[64:67],v[104:107]\n"
"s_add_u32 s3,s3,204800\n"
TR "a[242:243],v1 offset:55296\n"
MF "v[104:107],v[176:179],a[24:27],v[104:107]\n"
LD "a[84:87],v9,s[40:43],s3 offen sc1\n"
MF "v[112:115],v[176:179],a[28:31],v[112:115]\n"
TR "a[244:245],v1 offset:57344\n"
MF "v[112:115],v[180:183],a[68:71],v[112:115]\n"
"s_add_u32 s3,s3,204800\n"
TR "a[246:247],v1 offset:59392\n"
MF "v[112:115],v[184:187],a[108:111],v[112:115]\n"
LD "a[88:91],v9,s[40:43],s3 offen sc1\n"
MF "v[120:123],v[184:187],a[112:115],v[120:123]\n"
MF "v[120:123],v[180:183],a[72:75],v[120:123]\n"
"s_add_u32 s3,s3,204800\n"
MF "v[120:123],v[176:179],a[32:35],v[120:123]\n"
LD "a[92:95],v9,s[40:43],s3 offen sc1\n"
MF "v[128:131],v[176:179],a[36:39],v[128:131]\n"
"s_bfe_u32 s60,s22,0x10014\n"
MF "v[128:131],v[180:183],a[76:79],v[128:131]\n"
"s_add_u32 s3,s3,204800\n"
MF "v[128:131],v[184:187],a[116:119],v[128:131]\n"
"s_waitcnt lgkmcnt(0)\n"
LD "a[120:123],v9,s[40:43],s3 offen sc1\n"
MF "v[132:135],a[244:247],a[116:119],v[132:135]\n"
"s_lshl_b32 s60,s60,5\n"
TR "v[176:177],v2 offset:49152\n"
MF "v[132:135],a[240:243],a[76:79],v[132:135]\n"
"s_add_u32 s3,s3,204800\n"
TR "v[178:179],v2 offset:51200\n"
"v_xor_b32 v242,s60,v8\n"
MF "v[132:135],v[188:191],a[36:39],v[132:135]\n"
LD "a[124:127],v9,s[40:43],s3 offen sc1\n"
MF "v[124:127],v[188:191],a[32:35],v[124:127]\n"
TR "v[180:181],v2 offset:53248\n"
MF "v[124:127],a[240:243],a[72:75],v[124:127]\n"
"s_add_u32 s3,s3,204800\n"
TR "v[182:183],v2 offset:55296\n"
MF "v[124:127],a[244:247],a[112:115],v[124:127]\n"
LD "a[128:131],v9,s[40:43],s3 offen sc1\n"
MF "v[116:119],a[244:247],a[108:111],v[116:119]\n"
TR "v[184:185],v2 offset:57344\n"
MF "v[116:119],a[240:243],a[68:71],v[116:119]\n"
"s_add_u32 s3,s3,204800\n"
TR "v[186:187],v2 offset:59392\n"
MF "v[116:119],v[188:191],a[28:31],v[116:119]\n"
LD "a[132:135],v9,s[40:43],s3 offen sc1\n"
MF "v[108:111],v[188:191],a[24:27],v[108:111]\n"
MF "v[108:111],a[240:243],a[64:67],v[108:111]\n"
MF "v[108:111],a[244:247],a[104:107],v[108:111]\n"
MF "v[100:103],a[244:247],a[100:103],v[100:103]\n"
MF "v[100:103],a[240:243],a[60:63],v[100:103]\n"
MF "v[100:103],v[188:191],a[20:23],v[100:103]\n"
"s_waitcnt lgkmcnt(0)\n"
MF "v[136:139],v[176:179],a[20:23],v[136:139]\n"
TR "v[188:189],v3 offset:49152\n"
MF "v[136:139],v[180:183],a[60:63],v[136:139]\n"
TR "v[190:191],v3 offset:51200\n"
MF "v[136:139],v[184:187],a[100:103],v[136:139]\n"
"buffer_load_dword v10,v12,s[40:43],s86 offen sc1\n"
MF "v[144:147],v[184:187],a[104:107],v[144:147]\n"
TR "a[240:241],v3 offset:53248\n"
MF "v[144:147],v[180:183],a[64:67],v[144:147]\n"
TR "a[242:243],v3 offset:55296\n"
MF "v[144:147],v[176:179],a[24:27],v[144:147]\n"
MF "v[152:155],v[176:179],a[28:31],v[152:155]\n"
TR "a[244:245],v3 offset:57344\n"
MF "v[152:155],v[180:183],a[68:71],v[152:155]\n"
TR "a[246:247],v3 offset:59392\n"
MF "v[152:155],v[184:187],a[108:111],v[152:155]\n"
MF "v[160:163],v[184:187],a[112:115],v[160:163]\n"
MF "v[160:163],v[180:183],a[72:75],v[160:163]\n"
MF "v[160:163],v[176:179],a[32:35],v[160:163]\n"
MF "v[168:171],v[176:179],a[36:39],v[168:171]\n"
MF "v[168:171],v[180:183],a[76:79],v[168:171]\n"
"s_waitcnt vmcnt(23) lgkmcnt(0)\n"
MF "v[168:171],v[184:187],a[116:119],v[168:171]\n"
"s_barrier\n"
MF "v[172:175],a[244:247],a[116:119],v[172:175]\n"
TR "v[176:177],v4\n"
MF "v[172:175],a[240:243],a[76:79],v[172:175]\n"
TR "v[178:179],v4 offset:2048\n"
MF "v[172:175],v[188:191],a[36:39],v[172:175]\n"
MF "v[164:167],v[188:191],a[32:35],v[164:167]\n"
TR "v[180:181],v4 offset:4096\n"
MF "v[164:167],a[240:243],a[72:75],v[164:167]\n"
TR "v[182:183],v4 offset:6144\n"
MF "v[164:167],a[244:247],a[112:115],v[164:167]\n"
"s_add_u32 m0,s5,73728\n"
MF "v[156:159],a[244:247],a[108:111],v[156:159]\n"
TR "v[184:185],v4 offset:8192\n"
MF "v[156:159],a[240:243],a[68:71],v[156:159]\n"
TR "v[186:187],v4 offset:10240\n"
MF "v[156:159],v[188:191],a[28:31],v[156:159]\n"
MF "v[148:151],v[188:191],a[24:27],v[148:151]\n"
MF "v[148:151],a[240:243],a[64:67],v[148:151]\n"
MF "v[148:151],a[244:247],a[104:107],v[148:151]\n"
MF "v[140:143],a[244:247],a[100:103],v[140:143]\n"
MF "v[140:143],a[240:243],a[60:63],v[140:143]\n"
MF "v[140:143],v[188:191],a[20:23],v[140:143]\n"
"s_waitcnt lgkmcnt(0)\n"
MF "v[96:99],v[176:179],a[140:143],v[96:99]\n"
"s_nop 0\n"
TR "v[188:189],v5\n"
TR "v[190:191],v5 offset:2048\n"
MF "v[96:99],v[180:183],a[180:183],v[96:99]\n"
"s_nop 0\n"
TR "a[240:241],v5 offset:4096\n"
TR "a[242:243],v5 offset:6144\n"
MF "v[96:99],v[184:187],a[220:223],v[96:99]\n"
"s_nop 0\n"
TR "a[244:245],v5 offset:8192\n"
TR "a[246:247],v5 offset:10240\n"
MF "v[104:107],v[184:187],a[224:227],v[104:107]\n"
MF "v[104:107],v[180:183],a[184:187],v[104:107]\n"
MF "v[104:107],v[176:179],a[144:147],v[104:107]\n"
"s_nop 0\n"
"s_nop 0\n"
"ds_write_addtid_b32 v96\n"
"ds_write_addtid_b32 v97 offset:256\n"
MF "v[112:115],v[176:179],a[148:151],v[112:115]\n"
"s_nop 0\n"
"ds_write_addtid_b32 v98 offset:512\n"
"ds_write_addtid_b32 v99 offset:768\n"
MF "v[112:115],v[180:183],a[188:191],v[112:115]\n"
MF "v[112:115],v[184:187],a[228:231],v[112:115]\n"
"s_nop 0\n"
"ds_write_addtid_b32 v104 offset:2048\n"
"ds_write_addtid_b32 v105 offset:2304\n"
MF "v[120:123],v[184:187],a[232:235],v[120:123]\n"
"s_nop 0\n"
"ds_write_addtid_b32 v106 offset:2560\n"
"ds_write_addtid_b32 v107 offset:2816\n"
MF "v[120:123],v[180:183],a[192:195],v[120:123]\n"
MF "v[120:123],v[176:179],a[152:155],v[120:123]\n"
"s_nop 0\n"
"ds_write_addtid_b32 v112 offset:4096\n"
"ds_write_addtid_b32 v113 offset:4352\n"
MF "v[128:131],v[176:179],a[156:159],v[128:131]\n"
"s_nop 0\n"
"ds_write_addtid_b32 v114 offset:4608\n"
"ds_write_addtid_b32 v115 offset:4864\n"
MF "v[128:131],v[180:183],a[196:199],v[128:131]\n"
MF "v[128:131],v[184:187],a[236:239],v[128:131]\n"
"s_waitcnt lgkmcnt(12)\n"
"ds_write_addtid_b32 v120 offset:50176\n"
"ds_write_addtid_b32 v121 offset:50432\n"
MF "v[132:135],a[244:247],a[236:239],v[132:135]\n"
"s_nop 0\n"
TR "v[176:177],v6\n"
TR "v[178:179],v6 offset:2048\n"
MF "v[132:135],a[240:243],a[196:199],v[132:135]\n"
"s_nop 0\n"
TR "v[180:181],v6 offset:4096\n"
TR "v[182:183],v6 offset:6144\n"
MF "v[132:135],v[188:191],a[156:159],v[132:135]\n"
"s_nop 0\n"
TR "v[184:185],v6 offset:8192\n"
TR "v[186:187],v6 offset:10240\n"
MF "v[124:127],v[188:191],a[152:155],v[124:127]\n"
"s_nop 0\n"
"ds_write_addtid_b32 v122 offset:50688\n"
"ds_write_addtid_b32 v123 offset:50944\n"
MF "v[124:127],a[240:243],a[192:195],v[124:127]\n"
"s_nop 0\n"
"ds_write_addtid_b32 v128 offset:52224\n"
"ds_write_addtid_b32 v129 offset:52480\n"
MF "v[124:127],a[244:247],a[232:235],v[124:127]\n"
"s_nop 0\n"
"ds_write_addtid_b32 v130 offset:52736\n"
"ds_write_addtid_b32 v131 offset:52992\n"
MF "v[116:119],a[244:247],a[228:231],v[116:119]\n"
"s_nop 0\n"
"ds_write_addtid_b32 v132 offset:53248\n"
"ds_write_addtid_b32 v133 offset:53504\n"
MF "v[116:119],a[240:243],a[188:191],v[116:119]\n"
"s_nop 0\n"
"ds_write_addtid_b32 v134 offset:53760\n"
"ds_write_addtid_b32 v135 offset:54016\n"
MF "v[116:119],v[188:191],a[148:151],v[116:119]\n"
"s_nop 0\n"
"ds_write_addtid_b32 v124 offset:51200\n"
"ds_write_addtid_b32 v125 offset:51456\n"
MF "v[108:111],v[188:191],a[144:147],v[108:111]\n"
"s_nop 0\n"
"ds_write_addtid_b32 v126 offset:51712\n"
"ds_write_addtid_b32 v127 offset:51968\n"
MF "v[108:111],a[240:243],a[184:187],v[108:111]\n"
MF "v[108:111],a[244:247],a[224:227],v[108:111]\n"
"s_nop 0\n"
"ds_write_addtid_b32 v116 offset:49152\n"
"ds_write_addtid_b32 v117 offset:49408\n"
MF "v[100:103],a[244:247],a[220:223],v[100:103]\n"
"s_nop 0\n"
"ds_write_addtid_b32 v118 offset:49664\n"
"ds_write_addtid_b32 v119 offset:49920\n"
MF "v[100:103],a[240:243],a[180:183],v[100:103]\n"
"s_waitcnt vmcnt(0)\n"
"v_sub_u32 v10,v10,v11\n"
MF "v[100:103],v[188:191],a[140:143],v[100:103]\n"
"s_waitcnt lgkmcnt(15)\n"
"ds_write_addtid_b32 v108 offset:3072\n"
"ds_write_addtid_b32 v109 offset:3328\n"
MF "v[136:139],v[176:179],a[140:143],v[136:139]\n"
"s_nop 0\n"
TR "v[188:189],v7\n"
TR "v[190:191],v7 offset:2048\n"
MF "v[136:139],v[180:183],a[180:183],v[136:139]\n"
"s_nop 0\n"
TR "a[240:241],v7 offset:4096\n"
TR "a[242:243],v7 offset:6144\n"
MF "v[136:139],v[184:187],a[220:223],v[136:139]\n"
"s_nop 0\n"
TR "a[244:245],v7 offset:8192\n"
TR "a[246:247],v7 offset:10240\n"
MF "v[144:147],v[184:187],a[224:227],v[144:147]\n"
"s_nop 0\n"
"ds_write_addtid_b32 v110 offset:3584\n"
"ds_write_addtid_b32 v111 offset:3840\n"
MF "v[144:147],v[180:183],a[184:187],v[144:147]\n"
"s_nop 0\n"
"ds_write_addtid_b32 v100 offset:1024\n"
"ds_write_addtid_b32 v101 offset:1280\n"
MF "v[144:147],v[176:179],a[144:147],v[144:147]\n"
"s_nop 0\n"
"ds_write_addtid_b32 v102 offset:1536\n"
"ds_write_addtid_b32 v103 offset:1792\n"
MF "v[152:155],v[176:179],a[148:151],v[152:155]\n"
"v_readlane_b32 s18,v10,s18\n"
MF "v[152:155],v[180:183],a[188:191],v[152:155]\n"
"s_cmp_lt_u32 s18,9\n"
"s_cselect_b32 s18,0xf0000000,0\n"
"s_or_b32 s62,s8,s18\n"
MF "v[152:155],v[184:187],a[228:231],v[152:155]\n"
LD "v[208:211],v9,s[40:43],s62 offen sc1\n"
MF "v[160:163],v[184:187],a[232:235],v[160:163]\n"
LD "v[212:215],v9,s[40:43],s62 offen offset:1024 sc1\n"
MF "v[160:163],v[180:183],a[192:195],v[160:163]\n"
LD "v[216:219],v9,s[40:43],s62 offen offset:2048 sc1\n"
MF "v[160:163],v[176:179],a[152:155],v[160:163]\n"
LD "v[220:223],v9,s[40:43],s62 offen offset:3072 sc1\n"
MF "v[168:171],v[176:179],a[156:159],v[168:171]\n"
"s_add_u32 s62,s62,4096\n"
MF "v[168:171],v[180:183],a[196:199],v[168:171]\n"
"s_waitcnt lgkmcnt(0)\n"
LD "v[224:227],v9,s[40:43],s62 offen sc1\n"
MF "v[168:171],v[184:187],a[236:239],v[168:171]\n"
"s_barrier\n"
MF "v[172:175],a[244:247],a[236:239],v[172:175]\n"
".Lpg31h:\n"
"s_cbranch_execz .Lisr4\n"
"buffer_load_dword v10,v12,s[40:43],s86 offen sc1\n"
MF "v[172:175],a[240:243],a[196:199],v[172:175]\n"
"ds_read2st64_b32 v[128:129],v13 offset0:204 offset1:205\n"
MF "v[172:175],v[188:191],a[156:159],v[172:175]\n"
"ds_read2st64_b32 v[130:131],v13 offset0:206 offset1:207\n"
MF "v[164:167],v[188:191],a[152:155],v[164:167]\n"
"ds_read2st64_b32 v[132:133],v13 offset0:208 offset1:209\n"
MF "v[164:167],a[240:243],a[192:195],v[164:167]\n"
"ds_read2st64_b32 v[134:135],v13 offset0:210 offset1:211\n"
MF "v[164:167],a[244:247],a[232:235],v[164:167]\n"
"ds_read2st64_b32 v[120:121],v13 offset0:196 offset1:197\n"
MF "v[156:159],a[244:247],a[228:231],v[156:159]\n"
"ds_read2st64_b32 v[122:123],v13 offset0:198 offset1:199\n"
MF "v[156:159],a[240:243],a[188:191],v[156:159]\n"
"ds_read2st64_b32 v[124:125],v13 offset0:200 offset1:201\n"
MF "v[156:159],v[188:191],a[148:151],v[156:159]\n"
"ds_read2st64_b32 v[126:127],v13 offset0:202 offset1:203\n"
MF "v[148:151],v[188:191],a[144:147],v[148:151]\n"
"ds_read2st64_b32 v[112:113],v13 offset0:16 offset1:17\n"
MF "v[148:151],a[240:243],a[184:187],v[148:151]\n"
"ds_read2st64_b32 v[114:115],v13 offset0:18 offset1:19\n"
MF "v[148:151],a[244:247],a[224:227],v[148:151]\n"
"ds_read2st64_b32 v[116:117],v13 offset0:192 offset1:193\n"
MF "v[140:143],a[244:247],a[220:223],v[140:143]\n"
"ds_read2st64_b32 v[118:119],v13 offset0:194 offset1:195\n"
MF "v[140:143],a[240:243],a[180:183],v[140:143]\n"
"ds_read2st64_b32 v[104:105],v13 offset0:8 offset1:9\n"
MF "v[140:143],v[188:191],a[140:143],v[140:143]\n"
"ds_read2st64_b32 v[106:107],v13 offset0:10 offset1:11\n"
"ds_read2st64_b32 v[108:109],v13 offset0:12 offset1:13\n"
"ds_read2st64_b32 v[110:111],v13 offset0:14 offset1:15\n"
"ds_read2st64_b32 v[96:97],v13 offset0:0 offset1:1\n"
"ds_read2st64_b32 v[98:99],v13 offset0:2 offset1:3\n"
"ds_read2st64_b32 v[100:101],v13 offset0:4 offset1:5\n"
"ds_read2st64_b32 v[102:103],v13 offset0:6 offset1:7\n"
"s_or_b32 s59,s16,s17\n"
"s_or_b32 s60,s59,s18\n"
"s_cmp_eq_u32 s60,0\n"
"s_cbranch_scc1 .Lhave1\n"
"s_waitcnt vmcnt(0)\n"
"v_sub_u32 v10,v10,v11\n"
"s_cmp_eq_u32 s16,0\n"
"s_cbranch_scc1 .Lhave3\n"
"s_add_u32 s72,s78,6\n"
"s_and_b32 s72,s72,15\n"
"s_nop 3\n"
".Lnf3:\n"
"v_readlane_b32 s58,v10,s72\n"
"s_cmp_lt_u32 s58,7\n"
"s_cbranch_scc1 .Lsp3\n"
".Lnb3:\n"
"s_or_b32 s62,s6,0\n"
LD "a[0:3],v9,s[40:43],s62 offen sc1\n"
LD "a[4:7],v9,s[40:43],s62 offen offset:1024 sc1\n"
LD "a[8:11],v9,s[40:43],s62 offen offset:2048 sc1\n"
LD "a[12:15],v9,s[40:43],s62 offen offset:3072 sc1\n"
"s_add_u32 s62,s62,4096\n"
LD "a[16:19],v9,s[40:43],s62 offen sc1\n"
".Lhave3:\n"
"s_cmp_eq_u32 s17,0\n"
"s_cbranch_scc1 .Lhave2\n"
"s_add_u32 s72,s78,4\n"
"s_and_b32 s72,s72,15\n"
"s_nop 3\n"
".Lnf4:\n"
"v_readlane_b32 s58,v10,s72\n"
"s_cmp_lt_u32 s58,8\n"
"s_cbranch_scc1 .Lsp4\n"
".Lnb4:\n"
"s_or_b32 s62,s7,0\n"
LD "a[40:43],v9,s[40:43],s62 offen sc1\n"
LD "a[44:47],v9,s[40:43],s62 offen offset:1024 sc1\n"
LD "a[48:51],v9,s[40:43],s62 offen offset:2048 sc1\n"
LD "a[52:55],v9,s[40:43],s62 offen offset:3072 sc1\n"
"s_add_u32 s62,s62,4096\n"
LD "a[56:59],v9,s[40:43],s62 offen sc1\n"
".Lhave2:\n"
"s_cmp_eq_u32 s18,0\n"
"s_cbranch_scc1 .Lhave1\n"
"s_add_u32 s72,s78,2\n"
"s_and_b32 s72,s72,15\n"
"s_nop 3\n"
".Lnf5:\n"
"v_readlane_b32 s58,v10,s72\n"
"s_cmp_lt_u32 s58,9\n"
"s_cbranch_scc1 .Lsp5\n"
".Lnb5:\n"
"s_or_b32 s62,s8,0\n"
LD "v[208:211],v9,s[40:43],s62 offen sc1\n"
LD "v[212:215],v9,s[40:43],s62 offen offset:1024 sc1\n"
LD "v[216:219],v9,s[40:43],s62 offen offset:2048 sc1\n"
LD "v[220:223],v9,s[40:43],s62 offen offset:3072 sc1\n"
"s_add_u32 s62,s62,4096\n"
LD "v[224:227],v9,s[40:43],s62 offen sc1\n"
".Lhave1:\n"
"s_cmp_eq_u32 s1,0xf0000000\n"
"s_cbranch_scc1 .Ljx\n"
"v_accvgpr_mov_b32 a96,a80\n"
"v_accvgpr_mov_b32 a97,a81\n"
"v_accvgpr_mov_b32 a136,a120\n"
"v_accvgpr_mov_b32 a137,a121\n"
MF "v[196:199],a[82:85],a[250:253],0\n"
MF "v[192:195],a[86:89],a[250:253],0\n"
MF "v[196:199],a[90:93],a[250:253],v[196:199]\n"
MF "v[192:195],a[94:97],a[250:253],v[192:195]\n"
MF "v[196:199],a[122:125],a[250:253],v[196:199]\n"
MF "v[192:195],a[126:129],a[250:253],v[192:195]\n"
MF "v[196:199],a[130:133],a[250:253],v[196:199]\n"
MF "v[192:195],a[134:137],a[250:253],v[192:195]\n"
"s_nop 7\n"
CV "v192,v192,v193\n"
CV "v193,v194,v195\n"
CV "v194,v196,v197\n"
CV "v195,v198,v199\n"
"s_nop 0\n"
"v_permlane16_swap_b32 v192,v194\n"
"v_permlane16_swap_b32 v193,v195\n"
"buffer_store_dwordx4 v[192:195],v8,s[44:47],s1 offen sc1\n"
".Ljx:\n"
"s_waitcnt lgkmcnt(15)\n"
"v_pk_add_f32 v[168:169],v[168:169],v[128:129]\n"
"v_pk_add_f32 v[170:171],v[170:171],v[130:131]\n"
"v_pk_add_f32 v[172:173],v[172:173],v[132:133]\n"
"v_pk_add_f32 v[174:175],v[174:175],v[134:135]\n"
"v_pk_add_f32 v[160:161],v[160:161],v[120:121]\n"
"s_waitcnt lgkmcnt(14)\n"
"v_pk_add_f32 v[162:163],v[162:163],v[122:123]\n"
"s_waitcnt lgkmcnt(13)\n"
"v_pk_add_f32 v[164:165],v[164:165],v[124:125]\n"
"s_waitcnt lgkmcnt(12)\n"
"v_pk_add_f32 v[166:167],v[166:167],v[126:127]\n"
"s_waitcnt lgkmcnt(11)\n"
"v_pk_add_f32 v[152:153],v[152:153],v[112:113]\n"
"s_waitcnt lgkmcnt(10)\n"
"v_pk_add_f32 v[154:155],v[154:155],v[114:115]\n"
"s_waitcnt lgkmcnt(9)\n"
"v_pk_add_f32 v[156:157],v[156:157],v[116:117]\n"
"s_waitcnt lgkmcnt(8)\n"
"v_pk_add_f32 v[158:159],v[158:159],v[118:119]\n"
"s_waitcnt lgkmcnt(7)\n"
"v_pk_add_f32 v[144:145],v[144:145],v[104:105]\n"
"s_waitcnt lgkmcnt(6)\n"
"v_pk_add_f32 v[146:147],v[146:147],v[106:107]\n"
"s_waitcnt lgkmcnt(5)\n"
"v_pk_add_f32 v[148:149],v[148:149],v[108:109]\n"
"s_waitcnt lgkmcnt(4)\n"
"v_pk_add_f32 v[150:151],v[150:151],v[110:111]\n"
"s_waitcnt lgkmcnt(3)\n"
"v_pk_add_f32 v[136:137],v[136:137],v[96:97]\n"
"s_waitcnt lgkmcnt(2)\n"
"v_pk_add_f32 v[138:139],v[138:139],v[98:99]\n"
"s_waitcnt lgkmcnt(1)\n"
"v_pk_add_f32 v[140:141],v[140:141],v[100:101]\n"
"s_waitcnt lgkmcnt(0)\n"
"v_pk_add_f32 v[142:143],v[142:143],v[102:103]\n"
"s_cmp_eq_u32 s59,0\n"
"s_cbranch_scc1 .Lhw\n"
"s_waitcnt vmcnt(0)\n"
".Lhw:\n"
MF "v[140:143],a[0:3],a[252:255],v[140:143]\n"
MF "v[148:151],a[4:7],a[252:255],v[148:151]\n"
MF "v[156:159],a[8:11],a[252:255],v[156:159]\n"
MF "v[164:167],a[12:15],a[252:255],v[164:167]\n"
MF "v[172:175],a[16:19],a[252:255],v[172:175]\n"
MF "v[136:139],a[0:3],a[248:251],v[136:139]\n"
MF "v[144:147],a[4:7],a[248:251],v[144:147]\n"
MF "v[152:155],a[8:11],a[248:251],v[152:155]\n"
MF "v[160:163],a[12:15],a[248:251],v[160:163]\n"
MF "v[168:171],a[16:19],a[248:251],v[168:171]\n"
MF "v[136:139],a[40:43],a[252:255],v[136:139]\n"
MF "v[144:147],a[44:47],a[252:255],v[144:147]\n"
MF "v[152:155],a[48:51],a[252:255],v[152:155]\n"
MF "v[160:163],a[52:55],a[252:255],v[160:163]\n"
MF "v[168:171],a[56:59],a[252:255],v[168:171]\n"
MF "v[140:143],a[40:43],a[248:251],v[140:143]\n"
MF "v[148:151],a[44:47],a[248:251],v[148:151]\n"
MF "v[156:159],a[48:51],a[248:251],v[156:159]\n"
MF "v[164:167],a[52:55],a[248:251],v[164:167]\n"
MF "v[172:175],a[56:59],a[248:251],v[172:175]\n"
"s_waitcnt vmcnt(0)\n"
MF "v[140:143],v[208:211],a[252:255],v[140:143]\n"
MF "v[136:139],v[208:211],a[248:251],v[136:139]\n"
MF "v[148:151],v[212:215],a[252:255],v[148:151]\n"
MF "v[144:147],v[212:215],a[248:251],v[144:147]\n"
MF "v[156:159],v[216:219],a[252:255],v[156:159]\n"
"s_nop 4\n"
CV "v136,v136,v137\n"
CV "v137,v138,v139\n"
MF "v[152:155],v[216:219],a[248:251],v[152:155]\n"
CV "v138,v140,v141\n"
CV "v139,v142,v143\n"
MF "v[164:167],v[220:223],a[252:255],v[164:167]\n"
CV "v144,v144,v145\n"
CV "v145,v146,v147\n"
MF "v[160:163],v[220:223],a[248:251],v[160:163]\n"
CV "v146,v148,v149\n"
CV "v147,v150,v151\n"
"v_permlane16_swap_b32 v136,v138\n"
"v_permlane16_swap_b32 v137,v139\n"
"buffer_store_dwordx4 v[136:139],v242,s[44:47],s73 offen sc1\n"
"s_add_u32 s73,s73,40960\n"
MF "v[172:175],v[224:227],a[252:255],v[172:175]\n"
CV "v152,v152,v153\n"
CV "v153,v154,v155\n"
MF "v[168:171],v[224:227],a[248:251],v[168:171]\n"
CV "v154,v156,v157\n"
CV "v155,v158,v159\n"
"v_permlane16_swap_b32 v144,v146\n"
"v_permlane16_swap_b32 v145,v147\n"
"buffer_store_dwordx4 v[144:147],v242,s[44:47],s73 offen sc1\n"
"s_add_u32 s73,s73,40960\n"
CV "v160,v160,v161\n"
CV "v161,v162,v163\n"
CV "v162,v164,v165\n"
CV "v163,v166,v167\n"
"v_permlane16_swap_b32 v152,v154\n"
"v_permlane16_swap_b32 v153,v155\n"
"buffer_store_dwordx4 v[152:155],v242,s[44:47],s73 offen sc1\n"
"s_add_u32 s73,s73,40960\n"
CV "v168,v168,v169\n"
CV "v169,v170,v171\n"
CV "v170,v172,v173\n"
CV "v171,v174,v175\n"
"v_permlane16_swap_b32 v160,v162\n"
"v_permlane16_swap_b32 v161,v163\n"
"buffer_store_dwordx4 v[160:163],v242,s[44:47],s73 offen sc1\n"
"s_add_u32 s73,s73,40960\n"
"v_permlane16_swap_b32 v168,v170\n"
"v_permlane16_swap_b32 v169,v171\n"
"buffer_store_dwordx4 v[168:171],v242,s[44:47],s73 offen sc1\n"
"s_add_u32 s73,s73,40960\n"
"s_endpgm\n"
".Lool:\n"
".Lsp0:\n"
"s_cmp_eq_u32 s98,0\n"
"s_cbranch_scc1 .Lnb0\n"
"s_sub_u32 s98,s98,1\n"
"s_sleep 2\n"
"buffer_load_dword v10,v12,s[40:43],s86 offen sc1\n"
"s_waitcnt vmcnt(0)\n"
"v_sub_u32 v10,v10,v11\n"
"s_nop 1\n"
"s_branch .Lnf0\n"
".Lsp1:\n"
"s_cmp_eq_u32 s98,0\n"
"s_cbranch_scc1 .Lnb1\n"
"s_sub_u32 s98,s98,1\n"
"s_sleep 2\n"
"buffer_load_dword v10,v12,s[40:43],s86 offen sc1\n"
"s_waitcnt vmcnt(0)\n"
"v_sub_u32 v10,v10,v11\n"
"s_nop 1\n"
"s_branch .Lnf1\n"
".Lsp2:\n"
"s_cmp_eq_u32 s98,0\n"
"s_cbranch_scc1 .Lnb2\n"
"s_sub_u32 s98,s98,1\n"
"s_sleep 2\n"
"buffer_load_dword v10,v12,s[40:43],s86 offen sc1\n"
"s_waitcnt vmcnt(0)\n"
"v_sub_u32 v10,v10,v11\n"
"s_nop 1\n"
"s_branch .Lnf2\n"
".Lsp3:\n"
"s_cmp_eq_u32 s98,0\n"
"s_cbranch_scc1 .Lnb3\n"
"s_sub_u32 s98,s98,1\n"
"s_sleep 2\n"
"buffer_load_dword v10,v12,s[40:43],s86 offen sc1\n"
"s_waitcnt vmcnt(0)\n"
"v_sub_u32 v10,v10,v11\n"
"s_nop 1\n"
"s_branch .Lnf3\n"
".Lsp4:\n"
"s_cmp_eq_u32 s98,0\n"
"s_cbranch_scc1 .Lnb4\n"
"s_sub_u32 s98,s98,1\n"
"s_sleep 2\n"
"buffer_load_dword v10,v12,s[40:43],s86 offen sc1\n"
"s_waitcnt vmcnt(0)\n"
"v_sub_u32 v10,v10,v11\n"
"s_nop 1\n"
"s_branch .Lnf4\n"
".Lsp5:\n"
"s_cmp_eq_u32 s98,0\n"
"s_cbranch_scc1 .Lnb5\n"
"s_sub_u32 s98,s98,1\n"
"s_sleep 2\n"
"buffer_load_dword v10,v12,s[40:43],s86 offen sc1\n"
"s_waitcnt vmcnt(0)\n"
"v_sub_u32 v10,v10,v11\n"
"s_nop 1\n"
"s_branch .Lnf5\n"
".Lcode1:\n"
".Lslow_0:\n"
"s_nop 7\n"
"s_nop 7\n"
"v_and_b32 v10,240,v9\n"
"v_mul_u32_u24 v10,24,v10\n"
"v_lshrrev_b32_e64 v11,4,v9\n"
"v_and_or_b32 v10,v11,48,v10\n"
"s_mov_b32 s59,s34\n"
"s_mov_b32 s34,s20\n"
"s_add_u32 s58,s74,0\n"
"s_nop 3\n"
LD "v[52:55],v10,s[32:35],s58 offen\n"
"s_waitcnt vmcnt(0)\n"
MF "v[72:75],v[52:55],v[136:139],v[32:35]\n"
MF "v[76:79],v[52:55],v[140:143],v[36:39]\n"
MF "v[80:83],v[52:55],v[144:147],v[40:43]\n"
MF "v[84:87],v[52:55],v[148:151],v[44:47]\n"
MF "v[88:91],v[52:55],v[152:155],v[48:51]\n"
"s_add_u32 s58,s74,0\n"
"s_nop 3\n"
LD "v[52:55],v10,s[32:35],s58 offen offset:64\n"
"s_waitcnt vmcnt(0)\n"
MF "v[72:75],v[52:55],v[156:159],v[72:75]\n"
MF "v[76:79],v[52:55],v[160:163],v[76:79]\n"
MF "v[80:83],v[52:55],v[164:167],v[80:83]\n"
MF "v[84:87],v[52:55],v[168:171],v[84:87]\n"
MF "v[88:91],v[52:55],v[172:175],v[88:91]\n"
"s_add_u32 s58,s74,0\n"
"s_nop 3\n"
LD "v[52:55],v10,s[32:35],s58 offen offset:128\n"
"s_waitcnt vmcnt(0)\n"
MF "v[72:75],v[52:55],v[176:179],v[72:75]\n"
MF "v[76:79],v[52:55],v[180:183],v[76:79]\n"
MF "v[80:83],v[52:55],v[184:187],v[80:83]\n"
MF "v[84:87],v[52:55],v[188:191],v[84:87]\n"
MF "v[88:91],v[52:55],v[192:195],v[88:91]\n"
"s_add_u32 s58,s74,0\n"
"s_nop 3\n"
LD "v[52:55],v10,s[32:35],s58 offen offset:192\n"
"s_waitcnt vmcnt(0)\n"
MF "v[72:75],v[52:55],v[196:199],v[72:75]\n"
MF "v[76:79],v[52:55],v[200:203],v[76:79]\n"
MF "v[80:83],v[52:55],v[204:207],v[80:83]\n"
MF "v[84:87],v[52:55],v[208:211],v[84:87]\n"
MF "v[88:91],v[52:55],v[212:215],v[88:91]\n"
"s_add_u32 s58,s74,0\n"
"s_nop 3\n"
LD "v[52:55],v10,s[32:35],s58 offen offset:256\n"
"s_waitcnt vmcnt(0)\n"
MF "v[72:75],v[52:55],v[216:219],v[72:75]\n"
MF "v[76:79],v[52:55],v[220:223],v[76:79]\n"
MF "v[80:83],v[52:55],v[224:227],v[80:83]\n"
MF "v[84:87],v[52:55],v[228:231],v[84:87]\n"
MF "v[88:91],v[52:55],v[232:235],v[88:91]\n"
"s_add_u32 s58,s74,0\n"
"s_nop 3\n"
LD "v[52:55],v10,s[32:35],s58 offen offset:320\n"
"s_waitcnt vmcnt(0)\n"
MF "v[72:75],v[52:55],v[236:239],v[72:75]\n"
MF "v[76:79],v[52:55],v[240:243],v[76:79]\n"
MF "v[80:83],v[52:55],v[244:247],v[80:83]\n"
MF "v[84:87],v[52:55],v[248:251],v[84:87]\n"
MF "v[88:91],v[52:55],v[252:255],v[88:91]\n"
"s_add_u32 s58,s74,6144\n"
"s_nop 3\n"
LD "v[52:55],v10,s[32:35],s58 offen\n"
"s_waitcnt vmcnt(0)\n"
MF "v[112:115],v[52:55],v[136:139],v[32:35]\n"
MF "v[116:119],v[52:55],v[140:143],v[36:39]\n"
MF "v[120:123],v[52:55],v[144:147],v[40:43]\n"
MF "v[124:127],v[52:55],v[148:151],v[44:47]\n"
MF "v[128:131],v[52:55],v[152:155],v[48:51]\n"
"s_add_u32 s58,s74,6144\n"
"s_nop 3\n"
LD "v[52:55],v10,s[32:35],s58 offen offset:64\n"
"s_waitcnt vmcnt(0)\n"
MF "v[112:115],v[52:55],v[156:159],v[112:115]\n"
MF "v[116:119],v[52:55],v[160:163],v[116:119]\n"
MF "v[120:123],v[52:55],v[164:167],v[120:123]\n"
MF "v[124:127],v[52:55],v[168:171],v[124:127]\n"
MF "v[128:131],v[52:55],v[172:175],v[128:131]\n"
"s_add_u32 s58,s74,6144\n"
"s_nop 3\n"
LD "v[52:55],v10,s[32:35],s58 offen offset:128\n"
"s_waitcnt vmcnt(0)\n"
MF "v[112:115],v[52:55],v[176:179],v[112:115]\n"
MF "v[116:119],v[52:55],v[180:183],v[116:119]\n"
MF "v[120:123],v[52:55],v[184:187],v[120:123]\n"
MF "v[124:127],v[52:55],v[188:191],v[124:127]\n"
MF "v[128:131],v[52:55],v[192:195],v[128:131]\n"
"s_add_u32 s58,s74,6144\n"
"s_nop 3\n"
LD "v[52:55],v10,s[32:35],s58 offen offset:192\n"
"s_waitcnt vmcnt(0)\n"
MF "v[112:115],v[52:55],v[196:199],v[112:115]\n"
MF "v[116:119],v[52:55],v[200:203],v[116:119]\n"
MF "v[120:123],v[52:55],v[204:207],v[120:123]\n"
MF "v[124:127],v[52:55],v[208:211],v[124:127]\n"
MF "v[128:131],v[52:55],v[212:215],v[128:131]\n"
"s_add_u32 s58,s74,6144\n"
"s_nop 3\n"
LD "v[52:55],v10,s[32:35],s58 offen offset:256\n"
"s_waitcnt vmcnt(0)\n"
MF "v[112:115],v[52:55],v[216:219],v[112:115]\n"
MF "v[116:119],v[52:55],v[220:223],v[116:119]\n"
MF "v[120:123],v[52:55],v[224:227],v[120:123]\n"
MF "v[124:127],v[52:55],v[228:231],v[124:127]\n"
MF "v[128:131],v[52:55],v[232:235],v[128:131]\n"
"s_add_u32 s58,s74,6144\n"
"s_nop 3\n"
LD "v[52:55],v10,s[32:35],s58 offen offset:320\n"
"s_waitcnt vmcnt(0)\n"
MF "v[112:115],v[52:55],v[236:239],v[112:115]\n"
MF "v[116:119],v[52:55],v[240:243],v[116:119]\n"
MF "v[120:123],v[52:55],v[244:247],v[120:123]\n"
MF "v[124:127],v[52:55],v[248:251],v[124:127]\n"
MF "v[128:131],v[52:55],v[252:255],v[128:131]\n"
"s_nop 7\n"
"s_mov_b32 s34,s59\n"
"v_max3_f32 v10,v72,v73,v74\n"
"s_nop 0\n"
"v_max3_f32 v11,v75,v112,v113\n"
"v_max3_f32 v10,v10,v114,v115\n"
"v_max_f32 v10,v10,v11\n"
"v_mov_b32 v11,v10\n"
"s_nop 1\n"
"v_permlane16_swap_b32 v10,v11\n"
"v_max_f32 v10,v10,v11\n"
"v_mov_b32 v11,v10\n"
"s_nop 1\n"
"v_permlane32_swap_b32 v10,v11\n"
"v_max_f32 v10,v10,v11\n"
"v_add_f32 v10,0x41300000,v10\n"
"v_max_f32 v10,0,v10\n"
"v_exp_f32 v11,-v10\n"
AR "v12,a0\n"
"v_subrev_f32 v12,s13,v12\n"
"v_fma_f32 v12,v12,v11,s13\n"
AW "a0,v12\n"
AR "v12,a20\n"
"v_subrev_f32 v12,s13,v12\n"
"v_fma_f32 v12,v12,v11,s13\n"
AW "a20,v12\n"
AR "v12,a40\n"
"v_subrev_f32 v12,s13,v12\n"
"v_fma_f32 v12,v12,v11,s13\n"
AW "a40,v12\n"
AR "v12,a60\n"
"v_subrev_f32 v12,s13,v12\n"
"v_fma_f32 v12,v12,v11,s13\n"
AW "a60,v12\n"
AR "v12,a80\n"
"v_subrev_f32 v12,s13,v12\n"
"v_fma_f32 v12,v12,v11,s13\n"
AW "a80,v12\n"
AR "v12,a100\n"
"v_subrev_f32 v12,s13,v12\n"
"v_fma_f32 v12,v12,v11,s13\n"
AW "a100,v12\n"
AR "v12,a120\n"
"v_subrev_f32 v12,s13,v12\n"
"v_fma_f32 v12,v12,v11,s13\n"
AW "a120,v12\n"
AR "v12,a140\n"
"v_subrev_f32 v12,s13,v12\n"
"v_fma_f32 v12,v12,v11,s13\n"
AW "a140,v12\n"
AR "v12,a160\n"
"v_subrev_f32 v12,s13,v12\n"
"v_fma_f32 v12,v12,v11,s13\n"
AW "a160,v12\n"
AR "v12,a180\n"
"v_subrev_f32 v12,s13,v12\n"
"v_fma_f32 v12,v12,v11,s13\n"
AW "a180,v12\n"
AR "v12,a200\n"
"v_subrev_f32 v12,s13,v12\n"
"v_fma_f32 v12,v12,v11,s13\n"
AW "a200,v12\n"
AR "v12,a220\n"
"v_subrev_f32 v12,s13,v12\n"
"v_fma_f32 v12,v12,v11,s13\n"
AW "a220,v12\n"
AR "v12,a1\n"
"v_subrev_f32 v12,s13,v12\n"
"v_fma_f32 v12,v12,v11,s13\n"
AW "a1,v12\n"
AR "v12,a21\n"
"v_subrev_f32 v12,s13,v12\n"
"v_fma_f32 v12,v12,v11,s13\n"
AW "a21,v12\n"
AR "v12,a41\n"
"v_subrev_f32 v12,s13,v12\n"
"v_fma_f32 v12,v12,v11,s13\n"
AW "a41,v12\n"
AR "v12,a61\n"
"v_subrev_f32 v12,s13,v12\n"
"v_fma_f32 v12,v12,v11,s13\n"
AW "a61,v12\n"
AR "v12,a81\n"
"v_subrev_f32 v12,s13,v12\n"
"v_fma_f32 v12,v12,v11,s13\n"
AW "a81,v12\n"
AR "v12,a101\n"
"v_subrev_f32 v12,s13,v12\n"
"v_fma_f32 v12,v12,v11,s13\n"
AW "a101,v12\n"
AR "v12,a121\n"
"v_subrev_f32 v12,s13,v12\n"
"v_fma_f32 v12,v12,v11,s13\n"
AW "a121,v12\n"
AR "v12,a141\n"
"v_subrev_f32 v12,s13,v12\n"
"v_fma_f32 v12,v12,v11,s13\n"
AW "a141,v12\n"
AR "v12,a161\n"
"v_subrev_f32 v12,s13,v12\n"
"v_fma_f32 v12,v12,v11,s13\n"
AW "a161,v12\n"
AR "v12,a181\n"
"v_subrev_f32 v12,s13,v12\n"
"v_fma_f32 v12,v12,v11,s13\n"
AW "a181,v12\n"
AR "v12,a201\n"
"v_subrev_f32 v12,s13,v12\n"
"v_fma_f32 v12,v12,v11,s13\n"
AW "a201,v12\n"
AR "v12,a221\n"
"v_subrev_f32 v12,s13,v12\n"
"v_fma_f32 v12,v12,v11,s13\n"
AW "a221,v12\n"
AR "v12,a2\n"
"v_subrev_f32 v12,s13,v12\n"
"v_fma_f32 v12,v12,v11,s13\n"
AW "a2,v12\n"
AR "v12,a22\n"
"v_subrev_f32 v12,s13,v12\n"
"v_fma_f32 v12,v12,v11,s13\n"
AW "a22,v12\n"
AR "v12,a42\n"
"v_subrev_f32 v12,s13,v12\n"
"v_fma_f32 v12,v12,v11,s13\n"
AW "a42,v12\n"
AR "v12,a62\n"
"v_subrev_f32 v12,s13,v12\n"
"v_fma_f32 v12,v12,v11,s13\n"
AW "a62,v12\n"
AR "v12,a82\n"
"v_subrev_f32 v12,s13,v12\n"
"v_fma_f32 v12,v12,v11,s13\n"
AW "a82,v12\n"
AR "v12,a102\n"
"v_subrev_f32 v12,s13,v12\n"
"v_fma_f32 v12,v12,v11,s13\n"
AW "a102,v12\n"
AR "v12,a122\n"
"v_subrev_f32 v12,s13,v12\n"
"v_fma_f32 v12,v12,v11,s13\n"
AW "a122,v12\n"
AR "v12,a142\n"
"v_subrev_f32 v12,s13,v12\n"
"v_fma_f32 v12,v12,v11,s13\n"
AW "a142,v12\n"
AR "v12,a162\n"
"v_subrev_f32 v12,s13,v12\n"
"v_fma_f32 v12,v12,v11,s13\n"
AW "a162,v12\n"
AR "v12,a182\n"
"v_subrev_f32 v12,s13,v12\n"
"v_fma_f32 v12,v12,v11,s13\n"
AW "a182,v12\n"
AR "v12,a202\n"
"v_subrev_f32 v12,s13,v12\n"
"v_fma_f32 v12,v12,v11,s13\n"
AW "a202,v12\n"
AR "v12,a222\n"
"v_subrev_f32 v12,s13,v12\n"
"v_fma_f32 v12,v12,v11,s13\n"
AW "a222,v12\n"
AR "v12,a3\n"
"v_subrev_f32 v12,s13,v12\n"
"v_fma_f32 v12,v12,v11,s13\n"
AW "a3,v12\n"
AR "v12,a23\n"
"v_subrev_f32 v12,s13,v12\n"
"v_fma_f32 v12,v12,v11,s13\n"
AW "a23,v12\n"
AR "v12,a43\n"
"v_subrev_f32 v12,s13,v12\n"
"v_fma_f32 v12,v12,v11,s13\n"
AW "a43,v12\n"
AR "v12,a63\n"
"v_subrev_f32 v12,s13,v12\n"
"v_fma_f32 v12,v12,v11,s13\n"
AW "a63,v12\n"
AR "v12,a83\n"
"v_subrev_f32 v12,s13,v12\n"
"v_fma_f32 v12,v12,v11,s13\n"
AW "a83,v12\n"
AR "v12,a103\n"
"v_subrev_f32 v12,s13,v12\n"
"v_fma_f32 v12,v12,v11,s13\n"
AW "a103,v12\n"
AR "v12,a123\n"
"v_subrev_f32 v12,s13,v12\n"
"v_fma_f32 v12,v12,v11,s13\n"
AW "a123,v12\n"
AR "v12,a143\n"
"v_subrev_f32 v12,s13,v12\n"
"v_fma_f32 v12,v12,v11,s13\n"
AW "a143,v12\n"
AR "v12,a163\n"
"v_subrev_f32 v12,s13,v12\n"
"v_fma_f32 v12,v12,v11,s13\n"
AW "a163,v12\n"
AR "v12,a183\n"
"v_subrev_f32 v12,s13,v12\n"
"v_fma_f32 v12,v12,v11,s13\n"
AW "a183,v12\n"
AR "v12,a203\n"
"v_subrev_f32 v12,s13,v12\n"
"v_fma_f32 v12,v12,v11,s13\n"
AW "a203,v12\n"
AR "v12,a223\n"
"v_subrev_f32 v12,s13,v12\n"
"v_fma_f32 v12,v12,v11,s13\n"
AW "a223,v12\n"
"v_mul_f32 v52,v57,v11\n"
"v_sub_f32 v32,v32,v10\n"
"v_sub_f32 v33,v33,v10\n"
"v_sub_f32 v34,v34,v10\n"
"v_sub_f32 v35,v35,v10\n"
"v_sub_f32 v92,v92,v10\n"
"v_sub_f32 v93,v93,v10\n"
"v_sub_f32 v94,v94,v10\n"
"v_sub_f32 v95,v95,v10\n"
"v_sub_f32 v72,v72,v10\n"
"v_sub_f32 v73,v73,v10\n"
"v_sub_f32 v74,v74,v10\n"
"v_sub_f32 v75,v75,v10\n"
"v_sub_f32 v112,v112,v10\n"
"v_sub_f32 v113,v113,v10\n"
"v_sub_f32 v114,v114,v10\n"
"v_sub_f32 v115,v115,v10\n"
"v_exp_f32 v72,v72\n"
"v_exp_f32 v73,v73\n"
"v_exp_f32 v74,v74\n"
"v_exp_f32 v75,v75\n"
"v_exp_f32 v112,v112\n"
"v_exp_f32 v113,v113\n"
"v_exp_f32 v114,v114\n"
"v_exp_f32 v115,v115\n"
"v_add_f32 v52,v52,v72\n"
"v_add_f32 v52,v52,v73\n"
"v_add_f32 v52,v52,v74\n"
"v_add_f32 v52,v52,v75\n"
"v_add_f32 v52,v52,v112\n"
"v_add_f32 v52,v52,v113\n"
"v_add_f32 v52,v52,v114\n"
"v_add_f32 v52,v52,v115\n"
CV "v72,v72,v73\n"
CV "v73,v74,v75\n"
CV "v74,v112,v113\n"
CV "v75,v114,v115\n"
"v_max3_f32 v10,v76,v77,v78\n"
"v_max3_f32 v11,v79,v116,v117\n"
"v_max3_f32 v10,v10,v118,v119\n"
"v_max_f32 v10,v10,v11\n"
"v_mov_b32 v11,v10\n"
"s_nop 1\n"
"v_permlane16_swap_b32 v10,v11\n"
"v_max_f32 v10,v10,v11\n"
"v_mov_b32 v11,v10\n"
"s_nop 1\n"
"v_permlane32_swap_b32 v10,v11\n"
"v_max_f32 v10,v10,v11\n"
"v_add_f32 v10,0x41300000,v10\n"
"v_max_f32 v10,0,v10\n"
"v_exp_f32 v11,-v10\n"
AR "v12,a4\n"
"v_subrev_f32 v12,s13,v12\n"
"v_fma_f32 v12,v12,v11,s13\n"
AW "a4,v12\n"
AR "v12,a24\n"
"v_subrev_f32 v12,s13,v12\n"
"v_fma_f32 v12,v12,v11,s13\n"
AW "a24,v12\n"
AR "v12,a44\n"
"v_subrev_f32 v12,s13,v12\n"
"v_fma_f32 v12,v12,v11,s13\n"
AW "a44,v12\n"
AR "v12,a64\n"
"v_subrev_f32 v12,s13,v12\n"
"v_fma_f32 v12,v12,v11,s13\n"
AW "a64,v12\n"
AR "v12,a84\n"
"v_subrev_f32 v12,s13,v12\n"
"v_fma_f32 v12,v12,v11,s13\n"
AW "a84,v12\n"
AR "v12,a104\n"
"v_subrev_f32 v12,s13,v12\n"
"v_fma_f32 v12,v12,v11,s13\n"
AW "a104,v12\n"
AR "v12,a124\n"
"v_subrev_f32 v12,s13,v12\n"
"v_fma_f32 v12,v12,v11,s13\n"
AW "a124,v12\n"
AR "v12,a144\n"
"v_subrev_f32 v12,s13,v12\n"
"v_fma_f32 v12,v12,v11,s13\n"
AW "a144,v12\n"
AR "v12,a164\n"
"v_subrev_f32 v12,s13,v12\n"
"v_fma_f32 v12,v12,v11,s13\n"
AW "a164,v12\n"
AR "v12,a184\n"
"v_subrev_f32 v12,s13,v12\n"
"v_fma_f32 v12,v12,v11,s13\n"
AW "a184,v12\n"
AR "v12,a204\n"
"v_subrev_f32 v12,s13,v12\n"
"v_fma_f32 v12,v12,v11,s13\n"
AW "a204,v12\n"
AR "v12,a224\n"
"v_subrev_f32 v12,s13,v12\n"
"v_fma_f32 v12,v12,v11,s13\n"
AW "a224,v12\n"
AR "v12,a5\n"
"v_subrev_f32 v12,s13,v12\n"
"v_fma_f32 v12,v12,v11,s13\n"
AW "a5,v12\n"
AR "v12,a25\n"
"v_subrev_f32 v12,s13,v12\n"
"v_fma_f32 v12,v12,v11,s13\n"
AW "a25,v12\n"
AR "v12,a45\n"
"v_subrev_f32 v12,s13,v12\n"
"v_fma_f32 v12,v12,v11,s13\n"
AW "a45,v12\n"
AR "v12,a65\n"
"v_subrev_f32 v12,s13,v12\n"
"v_fma_f32 v12,v12,v11,s13\n"
AW "a65,v12\n"
AR "v12,a85\n"
"v_subrev_f32 v12,s13,v12\n"
"v_fma_f32 v12,v12,v11,s13\n"
AW "a85,v12\n"
AR "v12,a105\n"
"v_subrev_f32 v12,s13,v12\n"
"v_fma_f32 v12,v12,v11,s13\n"
AW "a105,v12\n"
AR "v12,a125\n"
"v_subrev_f32 v12,s13,v12\n"
"v_fma_f32 v12,v12,v11,s13\n"
AW "a125,v12\n"
AR "v12,a145\n"
"v_subrev_f32 v12,s13,v12\n"
"v_fma_f32 v12,v12,v11,s13\n"
AW "a145,v12\n"
AR "v12,a165\n"
"v_subrev_f32 v12,s13,v12\n"
"v_fma_f32 v12,v12,v11,s13\n"
AW "a165,v12\n"
AR "v12,a185\n"
"v_subrev_f32 v12,s13,v12\n"
"v_fma_f32 v12,v12,v11,s13\n"
AW "a185,v12\n"
AR "v12,a205\n"
"v_subrev_f32 v12,s13,v12\n"
"v_fma_f32 v12,v12,v11,s13\n"
AW "a205,v12\n"
AR "v12,a225\n"
"v_subrev_f32 v12,s13,v12\n"
"v_fma_f32 v12,v12,v11,s13\n"
AW "a225,v12\n"
AR "v12,a6\n"
"v_subrev_f32 v12,s13,v12\n"
"v_fma_f32 v12,v12,v11,s13\n"
AW "a6,v12\n"
AR "v12,a26\n"
"v_subrev_f32 v12,s13,v12\n"
"v_fma_f32 v12,v12,v11,s13\n"
AW "a26,v12\n"
AR "v12,a46\n"
"v_subrev_f32 v12,s13,v12\n"
"v_fma_f32 v12,v12,v11,s13\n"
AW "a46,v12\n"
AR "v12,a66\n"
"v_subrev_f32 v12,s13,v12\n"
"v_fma_f32 v12,v12,v11,s13\n"
AW "a66,v12\n"
AR "v12,a86\n"
"v_subrev_f32 v12,s13,v12\n"
"v_fma_f32 v12,v12,v11,s13\n"
AW "a86,v12\n"
AR "v12,a106\n"
"v_subrev_f32 v12,s13,v12\n"
"v_fma_f32 v12,v12,v11,s13\n"
AW "a106,v12\n"
AR "v12,a126\n"
"v_subrev_f32 v12,s13,v12\n"
"v_fma_f32 v12,v12,v11,s13\n"
AW "a126,v12\n"
AR "v12,a146\n"
"v_subrev_f32 v12,s13,v12\n"
"v_fma_f32 v12,v12,v11,s13\n"
AW "a146,v12\n"
AR "v12,a166\n"
"v_subrev_f32 v12,s13,v12\n"
"v_fma_f32 v12,v12,v11,s13\n"
AW "a166,v12\n"
AR "v12,a186\n"
"v_subrev_f32 v12,s13,v12\n"
"v_fma_f32 v12,v12,v11,s13\n"
AW "a186,v12\n"
AR "v12,a206\n"
"v_subrev_f32 v12,s13,v12\n"
"v_fma_f32 v12,v12,v11,s13\n"
AW "a206,v12\n"
AR "v12,a226\n"
"v_subrev_f32 v12,s13,v12\n"
"v_fma_f32 v12,v12,v11,s13\n"
AW "a226,v12\n"
AR "v12,a7\n"
"v_subrev_f32 v12,s13,v12\n"
"v_fma_f32 v12,v12,v11,s13\n"
AW "a7,v12\n"
AR "v12,a27\n"
"v_subrev_f32 v12,s13,v12\n"
"v_fma_f32 v12,v12,v11,s13\n"
AW "a27,v12\n"
AR "v12,a47\n"
"v_subrev_f32 v12,s13,v12\n"
"v_fma_f32 v12,v12,v11,s13\n"
AW "a47,v12\n"
AR "v12,a67\n"
"v_subrev_f32 v12,s13,v12\n"
"v_fma_f32 v12,v12,v11,s13\n"
AW "a67,v12\n"
AR "v12,a87\n"
"v_subrev_f32 v12,s13,v12\n"
"v_fma_f32 v12,v12,v11,s13\n"
AW "a87,v12\n"
AR "v12,a107\n"
"v_subrev_f32 v12,s13,v12\n"
"v_fma_f32 v12,v12,v11,s13\n"
AW "a107,v12\n"
AR "v12,a127\n"
"v_subrev_f32 v12,s13,v12\n"
"v_fma_f32 v12,v12,v11,s13\n"
AW "a127,v12\n"
AR "v12,a147\n"
"v_subrev_f32 v12,s13,v12\n"
"v_fma_f32 v12,v12,v11,s13\n"
AW "a147,v12\n"
AR "v12,a167\n"
"v_subrev_f32 v12,s13,v12\n"
"v_fma_f32 v12,v12,v11,s13\n"
AW "a167,v12\n"
AR "v12,a187\n"
"v_subrev_f32 v12,s13,v12\n"
"v_fma_f32 v12,v12,v11,s13\n"
AW "a187,v12\n"
AR "v12,a207\n"
"v_subrev_f32 v12,s13,v12\n"
"v_fma_f32 v12,v12,v11,s13\n"
AW "a207,v12\n"
AR "v12,a227\n"
"v_subrev_f32 v12,s13,v12\n"
"v_fma_f32 v12,v12,v11,s13\n"
AW "a227,v12\n"
"v_mul_f32 v53,v58,v11\n"
"v_sub_f32 v36,v36,v10\n"
"v_sub_f32 v37,v37,v10\n"
"v_sub_f32 v38,v38,v10\n"
"v_sub_f32 v39,v39,v10\n"
"v_sub_f32 v96,v96,v10\n"
"v_sub_f32 v97,v97,v10\n"
"v_sub_f32 v98,v98,v10\n"
"v_sub_f32 v99,v99,v10\n"
"v_sub_f32 v76,v76,v10\n"
"v_sub_f32 v77,v77,v10\n"
"v_sub_f32 v78,v78,v10\n"
"v_sub_f32 v79,v79,v10\n"
"v_sub_f32 v116,v116,v10\n"
"v_sub_f32 v117,v117,v10\n"
"v_sub_f32 v118,v118,v10\n"
"v_sub_f32 v119,v119,v10\n"
"v_exp_f32 v76,v76\n"
"v_exp_f32 v77,v77\n"
"v_exp_f32 v78,v78\n"
"v_exp_f32 v79,v79\n"
"v_exp_f32 v116,v116\n"
"v_exp_f32 v117,v117\n"
"v_exp_f32 v118,v118\n"
"v_exp_f32 v119,v119\n"
"v_add_f32 v53,v53,v76\n"
"v_add_f32 v53,v53,v77\n"
"v_add_f32 v53,v53,v78\n"
"v_add_f32 v53,v53,v79\n"
"v_add_f32 v53,v53,v116\n"
"v_add_f32 v53,v53,v117\n"
"v_add_f32 v53,v53,v118\n"
"v_add_f32_e64 v53,v53,v119\n"
CV "v76,v76,v77\n"
CV "v77,v78,v79\n"
CV "v78,v116,v117\n"
CV "v79,v118,v119\n"
"v_max3_f32 v10,v80,v81,v82\n"
"v_max3_f32 v11,v83,v120,v121\n"
"v_max3_f32 v10,v10,v122,v123\n"
"v_max_f32 v10,v10,v11\n"
"v_mov_b32 v11,v10\n"
"s_nop 1\n"
"v_permlane16_swap_b32 v10,v11\n"
"v_max_f32 v10,v10,v11\n"
"v_mov_b32 v11,v10\n"
"s_nop 1\n"
"v_permlane32_swap_b32 v10,v11\n"
"v_max_f32 v10,v10,v11\n"
"v_add_f32 v10,0x41300000,v10\n"
"v_max_f32 v10,0,v10\n"
"v_exp_f32 v11,-v10\n"
AR "v12,a8\n"
"v_subrev_f32 v12,s13,v12\n"
"v_fma_f32 v12,v12,v11,s13\n"
AW "a8,v12\n"
AR "v12,a28\n"
"v_subrev_f32 v12,s13,v12\n"
"v_fma_f32 v12,v12,v11,s13\n"
AW "a28,v12\n"
AR "v12,a48\n"
"v_subrev_f32 v12,s13,v12\n"
"v_fma_f32 v12,v12,v11,s13\n"
AW "a48,v12\n"
AR "v12,a68\n"
"v_subrev_f32 v12,s13,v12\n"
"v_fma_f32 v12,v12,v11,s13\n"
AW "a68,v12\n"
AR "v12,a88\n"
"v_subrev_f32 v12,s13,v12\n"
"v_fma_f32 v12,v12,v11,s13\n"
AW "a88,v12\n"
AR "v12,a108\n"
"v_subrev_f32 v12,s13,v12\n"
"v_fma_f32 v12,v12,v11,s13\n"
AW "a108,v12\n"
AR "v12,a128\n"
"v_subrev_f32 v12,s13,v12\n"
"v_fma_f32 v12,v12,v11,s13\n"
AW "a128,v12\n"
AR "v12,a148\n"
"v_subrev_f32 v12,s13,v12\n"
"v_fma_f32 v12,v12,v11,s13\n"
AW "a148,v12\n"
AR "v12,a168\n"
"v_subrev_f32 v12,s13,v12\n"
"v_fma_f32 v12,v12,v11,s13\n"
AW "a168,v12\n"
AR "v12,a188\n"
"v_subrev_f32 v12,s13,v12\n"
"v_fma_f32 v12,v12,v11,s13\n"
AW "a188,v12\n"
AR "v12,a208\n"
"v_subrev_f32 v12,s13,v12\n"
"v_fma_f32 v12,v12,v11,s13\n"
AW "a208,v12\n"
AR "v12,a228\n"
"v_subrev_f32 v12,s13,v12\n"
"v_fma_f32 v12,v12,v11,s13\n"
AW "a228,v12\n"
AR "v12,a9\n"
"v_subrev_f32 v12,s13,v12\n"
"v_fma_f32 v12,v12,v11,s13\n"
AW "a9,v12\n"
AR "v12,a29\n"
"v_subrev_f32 v12,s13,v12\n"
"v_fma_f32 v12,v12,v11,s13\n"
AW "a29,v12\n"
AR "v12,a49\n"
"v_subrev_f32 v12,s13,v12\n"
"v_fma_f32 v12,v12,v11,s13\n"
AW "a49,v12\n"
AR "v12,a69\n"
"v_subrev_f32 v12,s13,v12\n"
"v_fma_f32 v12,v12,v11,s13\n"
AW "a69,v12\n"
AR "v12,a89\n"
"v_subrev_f32 v12,s13,v12\n"
"v_fma_f32 v12,v12,v11,s13\n"
AW "a89,v12\n"
AR "v12,a109\n"
"v_subrev_f32 v12,s13,v12\n"
"v_fma_f32 v12,v12,v11,s13\n"
AW "a109,v12\n"
AR "v12,a129\n"
"v_subrev_f32 v12,s13,v12\n"
"v_fma_f32 v12,v12,v11,s13\n"
AW "a129,v12\n"
AR "v12,a149\n"
"v_subrev_f32 v12,s13,v12\n"
"v_fma_f32 v12,v12,v11,s13\n"
AW "a149,v12\n"
AR "v12,a169\n"
"v_subrev_f32 v12,s13,v12\n"
"v_fma_f32 v12,v12,v11,s13\n"
AW "a169,v12\n"
AR "v12,a189\n"
"v_subrev_f32 v12,s13,v12\n"
"v_fma_f32 v12,v12,v11,s13\n"
AW "a189,v12\n"
AR "v12,a209\n"
"v_subrev_f32 v12,s13,v12\n"
"v_fma_f32 v12,v12,v11,s13\n"
AW "a209,v12\n"
AR "v12,a229\n"
"v_subrev_f32 v12,s13,v12\n"
"v_fma_f32 v12,v12,v11,s13\n"
AW "a229,v12\n"
AR "v12,a10\n"
"v_subrev_f32 v12,s13,v12\n"
"v_fma_f32 v12,v12,v11,s13\n"
AW "a10,v12\n"
AR "v12,a30\n"
"v_subrev_f32 v12,s13,v12\n"
"v_fma_f32 v12,v12,v11,s13\n"
AW "a30,v12\n"
AR "v12,a50\n"
"v_subrev_f32 v12,s13,v12\n"
"v_fma_f32 v12,v12,v11,s13\n"
AW "a50,v12\n"
AR "v12,a70\n"
"v_subrev_f32 v12,s13,v12\n"
"v_fma_f32 v12,v12,v11,s13\n"
AW "a70,v12\n"
AR "v12,a90\n"
"v_subrev_f32 v12,s13,v12\n"
"v_fma_f32 v12,v12,v11,s13\n"
AW "a90,v12\n"
AR "v12,a110\n"
"v_subrev_f32 v12,s13,v12\n"
"v_fma_f32 v12,v12,v11,s13\n"
AW "a110,v12\n"
AR "v12,a130\n"
"v_subrev_f32 v12,s13,v12\n"
"v_fma_f32 v12,v12,v11,s13\n"
AW "a130,v12\n"
AR "v12,a150\n"
"v_subrev_f32 v12,s13,v12\n"
"v_fma_f32 v12,v12,v11,s13\n"
AW "a150,v12\n"
AR "v12,a170\n"
"v_subrev_f32 v12,s13,v12\n"
"v_fma_f32 v12,v12,v11,s13\n"
AW "a170,v12\n"
AR "v12,a190\n"
"v_subrev_f32 v12,s13,v12\n"
"v_fma_f32 v12,v12,v11,s13\n"
AW "a190,v12\n"
AR "v12,a210\n"
"v_subrev_f32 v12,s13,v12\n"
"v_fma_f32 v12,v12,v11,s13\n"
AW "a210,v12\n"
AR "v12,a230\n"
"v_subrev_f32 v12,s13,v12\n"
"v_fma_f32 v12,v12,v11,s13\n"
AW "a230,v12\n"
AR "v12,a11\n"
"v_subrev_f32 v12,s13,v12\n"
"v_fma_f32 v12,v12,v11,s13\n"
AW "a11,v12\n"
AR "v12,a31\n"
"v_subrev_f32 v12,s13,v12\n"
"v_fma_f32 v12,v12,v11,s13\n"
AW "a31,v12\n"
AR "v12,a51\n"
"v_subrev_f32 v12,s13,v12\n"
"v_fma_f32 v12,v12,v11,s13\n"
AW "a51,v12\n"
AR "v12,a71\n"
"v_subrev_f32 v12,s13,v12\n"
"v_fma_f32 v12,v12,v11,s13\n"
AW "a71,v12\n"
AR "v12,a91\n"
"v_subrev_f32 v12,s13,v12\n"
"v_fma_f32 v12,v12,v11,s13\n"
AW "a91,v12\n"
AR "v12,a111\n"
"v_subrev_f32 v12,s13,v12\n"
"v_fma_f32 v12,v12,v11,s13\n"
AW "a111,v12\n"
AR "v12,a131\n"
"v_subrev_f32 v12,s13,v12\n"
"v_fma_f32 v12,v12,v11,s13\n"
AW "a131,v12\n"
AR "v12,a151\n"
"v_subrev_f32 v12,s13,v12\n"
"v_fma_f32 v12,v12,v11,s13\n"
AW "a151,v12\n"
AR "v12,a171\n"
"v_subrev_f32 v12,s13,v12\n"
"v_fma_f32 v12,v12,v11,s13\n"
AW "a171,v12\n"
AR "v12,a191\n"
"v_subrev_f32 v12,s13,v12\n"
"v_fma_f32 v12,v12,v11,s13\n"
AW "a191,v12\n"
AR "v12,a211\n"
"v_subrev_f32 v12,s13,v12\n"
"v_fma_f32 v12,v12,v11,s13\n"
AW "a211,v12\n"
AR "v12,a231\n"
"v_subrev_f32 v12,s13,v12\n"
"v_fma_f32 v12,v12,v11,s13\n"
AW "a231,v12\n"
"v_mul_f32 v54,v59,v11\n"
"v_sub_f32 v40,v40,v10\n"
"v_sub_f32 v41,v41,v10\n"
"v_sub_f32 v42,v42,v10\n"
"v_sub_f32 v43,v43,v10\n"
"v_sub_f32 v100,v100,v10\n"
"v_sub_f32 v101,v101,v10\n"
"v_sub_f32 v102,v102,v10\n"
"v_sub_f32 v103,v103,v10\n"
"v_sub_f32 v80,v80,v10\n"
"v_sub_f32 v81,v81,v10\n"
"v_sub_f32 v82,v82,v10\n"
"v_sub_f32 v83,v83,v10\n"
"v_sub_f32 v120,v120,v10\n"
"v_sub_f32 v121,v121,v10\n"
"v_sub_f32 v122,v122,v10\n"
"v_sub_f32 v123,v123,v10\n"
"v_exp_f32 v80,v80\n"
"v_exp_f32 v81,v81\n"
"v_exp_f32 v82,v82\n"
"v_exp_f32 v83,v83\n"
"v_exp_f32 v120,v120\n"
"v_exp_f32 v121,v121\n"
"v_exp_f32 v122,v122\n"
"v_exp_f32 v123,v123\n"
"v_add_f32 v54,v54,v80\n"
"v_add_f32 v54,v54,v81\n"
"v_add_f32 v54,v54,v82\n"
"v_add_f32 v54,v54,v83\n"
"v_add_f32 v54,v54,v120\n"
"v_add_f32 v54,v54,v121\n"
"v_add_f32 v54,v54,v122\n"
"v_add_f32_e64 v54,v54,v123\n"
CV "v80,v80,v81\n"
CV "v81,v82,v83\n"
CV "v82,v120,v121\n"
CV "v83,v122,v123\n"
"v_max3_f32 v10,v84,v85,v86\n"
"v_max3_f32 v11,v87,v124,v125\n"
"v_max3_f32 v10,v10,v126,v127\n"
"v_max_f32 v10,v10,v11\n"
"v_mov_b32 v11,v10\n"
"s_nop 1\n"
"v_permlane16_swap_b32 v10,v11\n"
"v_max_f32 v10,v10,v11\n"
"v_mov_b32 v11,v10\n"
"s_nop 1\n"
"v_permlane32_swap_b32 v10,v11\n"
"v_max_f32 v10,v10,v11\n"
"v_add_f32 v10,0x41300000,v10\n"
"v_max_f32 v10,0,v10\n"
"v_exp_f32 v11,-v10\n"
AR "v12,a12\n"
"v_subrev_f32 v12,s13,v12\n"
"v_fma_f32 v12,v12,v11,s13\n"
AW "a12,v12\n"
AR "v12,a32\n"
"v_subrev_f32 v12,s13,v12\n"
"v_fma_f32 v12,v12,v11,s13\n"
AW "a32,v12\n"
AR "v12,a52\n"
"v_subrev_f32 v12,s13,v12\n"
"v_fma_f32 v12,v12,v11,s13\n"
AW "a52,v12\n"
AR "v12,a72\n"
"v_subrev_f32 v12,s13,v12\n"
"v_fma_f32 v12,v12,v11,s13\n"
AW "a72,v12\n"
AR "v12,a92\n"
"v_subrev_f32 v12,s13,v12\n"
"v_fma_f32 v12,v12,v11,s13\n"
AW "a92,v12\n"
AR "v12,a112\n"
"v_subrev_f32 v12,s13,v12\n"
"v_fma_f32 v12,v12,v11,s13\n"
AW "a112,v12\n"
AR "v12,a132\n"
"v_subrev_f32 v12,s13,v12\n"
"v_fma_f32 v12,v12,v11,s13\n"
AW "a132,v12\n"
AR "v12,a152\n"
"v_subrev_f32 v12,s13,v12\n"
"v_fma_f32 v12,v12,v11,s13\n"
AW "a152,v12\n"
AR "v12,a172\n"
"v_subrev_f32 v12,s13,v12\n"
"v_fma_f32 v12,v12,v11,s13\n"
AW "a172,v12\n"
AR "v12,a192\n"
"v_subrev_f32 v12,s13,v12\n"
"v_fma_f32 v12,v12,v11,s13\n"
AW "a192,v12\n"
AR "v12,a212\n"
"v_subrev_f32 v12,s13,v12\n"
"v_fma_f32 v12,v12,v11,s13\n"
AW "a212,v12\n"
AR "v12,a232\n"
"v_subrev_f32 v12,s13,v12\n"
"v_fma_f32 v12,v12,v11,s13\n"
AW "a232,v12\n"
AR "v12,a13\n"
"v_subrev_f32 v12,s13,v12\n"
"v_fma_f32 v12,v12,v11,s13\n"
AW "a13,v12\n"
AR "v12,a33\n"
"v_subrev_f32 v12,s13,v12\n"
"v_fma_f32 v12,v12,v11,s13\n"
AW "a33,v12\n"
AR "v12,a53\n"
"v_subrev_f32 v12,s13,v12\n"
"v_fma_f32 v12,v12,v11,s13\n"
AW "a53,v12\n"
AR "v12,a73\n"
"v_subrev_f32 v12,s13,v12\n"
"v_fma_f32 v12,v12,v11,s13\n"
AW "a73,v12\n"
AR "v12,a93\n"
"v_subrev_f32 v12,s13,v12\n"
"v_fma_f32 v12,v12,v11,s13\n"
AW "a93,v12\n"
AR "v12,a113\n"
"v_subrev_f32 v12,s13,v12\n"
"v_fma_f32 v12,v12,v11,s13\n"
AW "a113,v12\n"
AR "v12,a133\n"
"v_subrev_f32 v12,s13,v12\n"
"v_fma_f32 v12,v12,v11,s13\n"
AW "a133,v12\n"
AR "v12,a153\n"
"v_subrev_f32 v12,s13,v12\n"
"v_fma_f32 v12,v12,v11,s13\n"
AW "a153,v12\n"
AR "v12,a173\n"
"v_subrev_f32 v12,s13,v12\n"
"v_fma_f32 v12,v12,v11,s13\n"
AW "a173,v12\n"
AR "v12,a193\n"
"v_subrev_f32 v12,s13,v12\n"
"v_fma_f32 v12,v12,v11,s13\n"
AW "a193,v12\n"
AR "v12,a213\n"
"v_subrev_f32 v12,s13,v12\n"
"v_fma_f32 v12,v12,v11,s13\n"
AW "a213,v12\n"
AR "v12,a233\n"
"v_subrev_f32 v12,s13,v12\n"
"v_fma_f32 v12,v12,v11,s13\n"
AW "a233,v12\n"
AR "v12,a14\n"
"v_subrev_f32 v12,s13,v12\n"
"v_fma_f32 v12,v12,v11,s13\n"
AW "a14,v12\n"
AR "v12,a34\n"
"v_subrev_f32 v12,s13,v12\n"
"v_fma_f32 v12,v12,v11,s13\n"
AW "a34,v12\n"
AR "v12,a54\n"
"v_subrev_f32 v12,s13,v12\n"
"v_fma_f32 v12,v12,v11,s13\n"
AW "a54,v12\n"
AR "v12,a74\n"
"v_subrev_f32 v12,s13,v12\n"
"v_fma_f32 v12,v12,v11,s13\n"
AW "a74,v12\n"
AR "v12,a94\n"
"v_subrev_f32 v12,s13,v12\n"
"v_fma_f32 v12,v12,v11,s13\n"
AW "a94,v12\n"
AR "v12,a114\n"
"v_subrev_f32 v12,s13,v12\n"
"v_fma_f32 v12,v12,v11,s13\n"
AW "a114,v12\n"
AR "v12,a134\n"
"v_subrev_f32 v12,s13,v12\n"
"v_fma_f32 v12,v12,v11,s13\n"
AW "a134,v12\n"
AR "v12,a154\n"
"v_subrev_f32 v12,s13,v12\n"
"v_fma_f32 v12,v12,v11,s13\n"
AW "a154,v12\n"
AR "v12,a174\n"
"v_subrev_f32 v12,s13,v12\n"
"v_fma_f32 v12,v12,v11,s13\n"
AW "a174,v12\n"
AR "v12,a194\n"
"v_subrev_f32 v12,s13,v12\n"
"v_fma_f32 v12,v12,v11,s13\n"
AW "a194,v12\n"
AR "v12,a214\n"
"v_subrev_f32 v12,s13,v12\n"
"v_fma_f32 v12,v12,v11,s13\n"
AW "a214,v12\n"
AR "v12,a234\n"
"v_subrev_f32 v12,s13,v12\n"
"v_fma_f32 v12,v12,v11,s13\n"
AW "a234,v12\n"
AR "v12,a15\n"
"v_subrev_f32 v12,s13,v12\n"
"v_fma_f32 v12,v12,v11,s13\n"
AW "a15,v12\n"
AR "v12,a35\n"
"v_subrev_f32 v12,s13,v12\n"
"v_fma_f32 v12,v12,v11,s13\n"
AW "a35,v12\n"
AR "v12,a55\n"
"v_subrev_f32 v12,s13,v12\n"
"v_fma_f32 v12,v12,v11,s13\n"
AW "a55,v12\n"
AR "v12,a75\n"
"v_subrev_f32 v12,s13,v12\n"
"v_fma_f32 v12,v12,v11,s13\n"
AW "a75,v12\n"
AR "v12,a95\n"
"v_subrev_f32 v12,s13,v12\n"
"v_fma_f32 v12,v12,v11,s13\n"
AW "a95,v12\n"
AR "v12,a115\n"
"v_subrev_f32 v12,s13,v12\n"
"v_fma_f32 v12,v12,v11,s13\n"
AW "a115,v12\n"
AR "v12,a135\n"
"v_subrev_f32 v12,s13,v12\n"
"v_fma_f32 v12,v12,v11,s13\n"
AW "a135,v12\n"
AR "v12,a155\n"
"v_subrev_f32 v12,s13,v12\n"
"v_fma_f32 v12,v12,v11,s13\n"
AW "a155,v12\n"
AR "v12,a175\n"
"v_subrev_f32 v12,s13,v12\n"
"v_fma_f32 v12,v12,v11,s13\n"
AW "a175,v12\n"
AR "v12,a195\n"
"v_subrev_f32 v12,s13,v12\n"
"v_fma_f32 v12,v12,v11,s13\n"
AW "a195,v12\n"
AR "v12,a215\n"
"v_subrev_f32 v12,s13,v12\n"
"v_fma_f32 v12,v12,v11,s13\n"
AW "a215,v12\n"
AR "v12,a235\n"
"v_subrev_f32 v12,s13,v12\n"
"v_fma_f32 v12,v12,v11,s13\n"
AW "a235,v12\n"
"v_mul_f32 v55,v60,v11\n"
"v_sub_f32 v44,v44,v10\n"
"v_sub_f32 v45,v45,v10\n"
"v_sub_f32 v46,v46,v10\n"
"v_sub_f32 v47,v47,v10\n"
"v_sub_f32 v104,v104,v10\n"
"v_sub_f32 v105,v105,v10\n"
"v_sub_f32 v106,v106,v10\n"
"v_sub_f32 v107,v107,v10\n"
"v_sub_f32 v84,v84,v10\n"
"v_sub_f32 v85,v85,v10\n"
"v_sub_f32 v86,v86,v10\n"
"v_sub_f32 v87,v87,v10\n"
"v_sub_f32 v124,v124,v10\n"
"v_sub_f32 v125,v125,v10\n"
"v_sub_f32 v126,v126,v10\n"
"v_sub_f32 v127,v127,v10\n"
"v_exp_f32 v84,v84\n"
"v_exp_f32 v85,v85\n"
"v_exp_f32 v86,v86\n"
"v_exp_f32 v87,v87\n"
"v_exp_f32 v124,v124\n"
"v_exp_f32 v125,v125\n"
"v_exp_f32 v126,v126\n"
"v_exp_f32 v127,v127\n"
"v_add_f32 v55,v55,v84\n"
"v_add_f32 v55,v55,v85\n"
"v_add_f32 v55,v55,v86\n"
"v_add_f32 v55,v55,v87\n"
"v_add_f32 v55,v55,v124\n"
"v_add_f32 v55,v55,v125\n"
"v_add_f32 v55,v55,v126\n"
"v_add_f32_e64 v55,v55,v127\n"
CV "v84,v84,v85\n"
CV "v85,v86,v87\n"
CV "v86,v124,v125\n"
CV "v87,v126,v127\n"
"v_max3_f32 v10,v88,v89,v90\n"
"v_max3_f32 v11,v91,v128,v129\n"
"v_max3_f32 v10,v10,v130,v131\n"
"v_max_f32 v10,v10,v11\n"
"v_mov_b32 v11,v10\n"
"s_nop 1\n"
"v_permlane16_swap_b32 v10,v11\n"
"v_max_f32 v10,v10,v11\n"
"v_mov_b32 v11,v10\n"
"s_nop 1\n"
"v_permlane32_swap_b32 v10,v11\n"
"v_max_f32_e64 v10,v10,v11\n"
"v_add_f32 v10,0x41300000,v10\n"
"v_max_f32 v10,0,v10\n"
"v_exp_f32 v11,-v10\n"
AR "v12,a16\n"
"v_subrev_f32 v12,s13,v12\n"
"v_fma_f32 v12,v12,v11,s13\n"
AW "a16,v12\n"
AR "v12,a36\n"
"v_subrev_f32 v12,s13,v12\n"
"v_fma_f32 v12,v12,v11,s13\n"
AW "a36,v12\n"
AR "v12,a56\n"
"v_subrev_f32 v12,s13,v12\n"
"v_fma_f32 v12,v12,v11,s13\n"
AW "a56,v12\n"
AR "v12,a76\n"
"v_subrev_f32 v12,s13,v12\n"
"v_fma_f32 v12,v12,v11,s13\n"
AW "a76,v12\n"
AR "v12,a96\n"
"v_subrev_f32 v12,s13,v12\n"
"v_fma_f32 v12,v12,v11,s13\n"
AW "a96,v12\n"
AR "v12,a116\n"
"v_subrev_f32 v12,s13,v12\n"
"v_fma_f32 v12,v12,v11,s13\n"
AW "a116,v12\n"
AR "v12,a136\n"
"v_subrev_f32 v12,s13,v12\n"
"v_fma_f32 v12,v12,v11,s13\n"
AW "a136,v12\n"
AR "v12,a156\n"
"v_subrev_f32 v12,s13,v12\n"
"v_fma_f32 v12,v12,v11,s13\n"
AW "a156,v12\n"
AR "v12,a176\n"
"v_subrev_f32 v12,s13,v12\n"
"v_fma_f32 v12,v12,v11,s13\n"
AW "a176,v12\n"
AR "v12,a196\n"
"v_subrev_f32 v12,s13,v12\n"
"v_fma_f32 v12,v12,v11,s13\n"
AW "a196,v12\n"
AR "v12,a216\n"
"v_subrev_f32 v12,s13,v12\n"
"v_fma_f32 v12,v12,v11,s13\n"
AW "a216,v12\n"
AR "v12,a236\n"
"v_subrev_f32 v12,s13,v12\n"
"v_fma_f32 v12,v12,v11,s13\n"
AW "a236,v12\n"
AR "v12,a17\n"
"v_subrev_f32 v12,s13,v12\n"
"v_fma_f32 v12,v12,v11,s13\n"
AW "a17,v12\n"
AR "v12,a37\n"
"v_subrev_f32 v12,s13,v12\n"
"v_fma_f32 v12,v12,v11,s13\n"
AW "a37,v12\n"
AR "v12,a57\n"
"v_subrev_f32 v12,s13,v12\n"
"v_fma_f32 v12,v12,v11,s13\n"
AW "a57,v12\n"
AR "v12,a77\n"
"v_subrev_f32 v12,s13,v12\n"
"v_fma_f32 v12,v12,v11,s13\n"
AW "a77,v12\n"
AR "v12,a97\n"
"v_subrev_f32 v12,s13,v12\n"
"v_fma_f32 v12,v12,v11,s13\n"
AW "a97,v12\n"
AR "v12,a117\n"
"v_subrev_f32 v12,s13,v12\n"
"v_fma_f32 v12,v12,v11,s13\n"
AW "a117,v12\n"
AR "v12,a137\n"
"v_subrev_f32 v12,s13,v12\n"
"v_fma_f32 v12,v12,v11,s13\n"
AW "a137,v12\n"
AR "v12,a157\n"
"v_subrev_f32 v12,s13,v12\n"
"v_fma_f32 v12,v12,v11,s13\n"
AW "a157,v12\n"
AR "v12,a177\n"
"v_subrev_f32 v12,s13,v12\n"
"v_fma_f32 v12,v12,v11,s13\n"
AW "a177,v12\n"
AR "v12,a197\n"
"v_subrev_f32 v12,s13,v12\n"
"v_fma_f32 v12,v12,v11,s13\n"
AW "a197,v12\n"
AR "v12,a217\n"
"v_subrev_f32 v12,s13,v12\n"
"v_fma_f32 v12,v12,v11,s13\n"
AW "a217,v12\n"
AR "v12,a237\n"
"v_subrev_f32 v12,s13,v12\n"
"v_fma_f32 v12,v12,v11,s13\n"
AW "a237,v12\n"
AR "v12,a18\n"
"v_subrev_f32 v12,s13,v12\n"
"v_fma_f32 v12,v12,v11,s13\n"
AW "a18,v12\n"
AR "v12,a38\n"
"v_subrev_f32 v12,s13,v12\n"
"v_fma_f32 v12,v12,v11,s13\n"
AW "a38,v12\n"
AR "v12,a58\n"
"v_subrev_f32 v12,s13,v12\n"
"v_fma_f32 v12,v12,v11,s13\n"
AW "a58,v12\n"
AR "v12,a78\n"
"v_subrev_f32 v12,s13,v12\n"
"v_fma_f32 v12,v12,v11,s13\n"
AW "a78,v12\n"
AR "v12,a98\n"
"v_subrev_f32 v12,s13,v12\n"
"v_fma_f32 v12,v12,v11,s13\n"
AW "a98,v12\n"
AR "v12,a118\n"
"v_subrev_f32 v12,s13,v12\n"
"v_fma_f32 v12,v12,v11,s13\n"
AW "a118,v12\n"
AR "v12,a138\n"
"v_subrev_f32 v12,s13,v12\n"
"v_fma_f32 v12,v12,v11,s13\n"
AW "a138,v12\n"
AR "v12,a158\n"
"v_subrev_f32 v12,s13,v12\n"
"v_fma_f32 v12,v12,v11,s13\n"
AW "a158,v12\n"
AR "v12,a178\n"
"v_subrev_f32 v12,s13,v12\n"
"v_fma_f32 v12,v12,v11,s13\n"
AW "a178,v12\n"
AR "v12,a198\n"
"v_subrev_f32 v12,s13,v12\n"
"v_fma_f32 v12,v12,v11,s13\n"
AW "a198,v12\n"
AR "v12,a218\n"
"v_subrev_f32 v12,s13,v12\n"
"v_fma_f32 v12,v12,v11,s13\n"
AW "a218,v12\n"
AR "v12,a238\n"
"v_subrev_f32 v12,s13,v12\n"
"v_fma_f32 v12,v12,v11,s13\n"
AW "a238,v12\n"
AR "v12,a19\n"
"v_subrev_f32 v12,s13,v12\n"
"v_fma_f32 v12,v12,v11,s13\n"
AW "a19,v12\n"
AR "v12,a39\n"
"v_subrev_f32 v12,s13,v12\n"
"v_fma_f32 v12,v12,v11,s13\n"
AW "a39,v12\n"
AR "v12,a59\n"
"v_subrev_f32 v12,s13,v12\n"
"v_fma_f32 v12,v12,v11,s13\n"
AW "a59,v12\n"
AR "v12,a79\n"
"v_subrev_f32 v12,s13,v12\n"
"v_fma_f32 v12,v12,v11,s13\n"
AW "a79,v12\n"
AR "v12,a99\n"
"v_subrev_f32 v12,s13,v12\n"
"v_fma_f32 v12,v12,v11,s13\n"
AW "a99,v12\n"
AR "v12,a119\n"
"v_subrev_f32 v12,s13,v12\n"
"v_fma_f32 v12,v12,v11,s13\n"
AW "a119,v12\n"
AR "v12,a139\n"
"v_subrev_f32 v12,s13,v12\n"
"v_fma_f32 v12,v12,v11,s13\n"
AW "a139,v12\n"
AR "v12,a159\n"
"v_subrev_f32 v12,s13,v12\n"
"v_fma_f32 v12,v12,v11,s13\n"
AW "a159,v12\n"
AR "v12,a179\n"
"v_subrev_f32 v12,s13,v12\n"
"v_fma_f32 v12,v12,v11,s13\n"
AW "a179,v12\n"
AR "v12,a199\n"
"v_subrev_f32 v12,s13,v12\n"
"v_fma_f32 v12,v12,v11,s13\n"
AW "a199,v12\n"
AR "v12,a219\n"
"v_subrev_f32 v12,s13,v12\n"
"v_fma_f32 v12,v12,v11,s13\n"
AW "a219,v12\n"
AR "v12,a239\n"
"v_subrev_f32 v12,s13,v12\n"
"v_fma_f32 v12,v12,v11,s13\n"
AW "a239,v12\n"
"v_mul_f32 v56,v61,v11\n"
"v_sub_f32 v48,v48,v10\n"
"v_sub_f32 v49,v49,v10\n"
"v_sub_f32 v50,v50,v10\n"
"v_sub_f32 v51,v51,v10\n"
"v_sub_f32 v108,v108,v10\n"
"v_sub_f32 v109,v109,v10\n"
"v_sub_f32 v110,v110,v10\n"
"v_sub_f32 v111,v111,v10\n"
"v_sub_f32 v88,v88,v10\n"
"v_sub_f32 v89,v89,v10\n"
"v_sub_f32 v90,v90,v10\n"
"v_sub_f32 v91,v91,v10\n"
"v_sub_f32 v128,v128,v10\n"
"v_sub_f32 v129,v129,v10\n"
"v_sub_f32 v130,v130,v10\n"
"v_sub_f32 v131,v131,v10\n"
"v_exp_f32 v88,v88\n"
"v_exp_f32 v89,v89\n"
"v_exp_f32 v90,v90\n"
"v_exp_f32 v91,v91\n"
"v_exp_f32 v128,v128\n"
"v_exp_f32 v129,v129\n"
"v_exp_f32 v130,v130\n"
"v_exp_f32 v131,v131\n"
"v_add_f32 v56,v56,v88\n"
"v_add_f32 v56,v56,v89\n"
"v_add_f32 v56,v56,v90\n"
"v_add_f32 v56,v56,v91\n"
"v_add_f32 v56,v56,v128\n"
"v_add_f32 v56,v56,v129\n"
"v_add_f32 v56,v56,v130\n"
"v_add_f32 v56,v56,v131\n"
CV "v88,v88,v89\n"
CV "v89,v90,v91\n"
CV "v90,v128,v129\n"
CV "v91,v130,v131\n"
"s_nop 7\n"
"s_setpc_b64 s[52:53]\n"
".Lslow_1:\n"
"s_nop 7\n"
"s_nop 7\n"
"v_and_b32 v10,240,v9\n"
"v_mul_u32_u24 v10,24,v10\n"
"v_lshrrev_b32 v11,4,v9\n"
"v_and_or_b32 v10,v11,48,v10\n"
"s_mov_b32 s59,s34\n"
"s_mov_b32 s34,s20\n"
"s_add_u32 s58,s74,0\n"
"s_nop 3\n"
LD "v[58:61],v10,s[32:35],s58 offen\n"
"s_waitcnt vmcnt(0)\n"
MF "v[92:95],v[58:61],v[136:139],v[32:35]\n"
MF "v[96:99],v[58:61],v[140:143],v[36:39]\n"
MF "v[100:103],v[58:61],v[144:147],v[40:43]\n"
MF "v[104:107],v[58:61],v[148:151],v[44:47]\n"
MF "v[108:111],v[58:61],v[152:155],v[48:51]\n"
"s_add_u32 s58,s74,0\n"
"s_nop 3\n"
LD "v[58:61],v10,s[32:35],s58 offen offset:64\n"
"s_waitcnt vmcnt(0)\n"
MF "v[92:95],v[58:61],v[156:159],v[92:95]\n"
MF "v[96:99],v[58:61],v[160:163],v[96:99]\n"
MF "v[100:103],v[58:61],v[164:167],v[100:103]\n"
MF "v[104:107],v[58:61],v[168:171],v[104:107]\n"
MF "v[108:111],v[58:61],v[172:175],v[108:111]\n"
"s_add_u32 s58,s74,0\n"
"s_nop 3\n"
LD "v[58:61],v10,s[32:35],s58 offen offset:128\n"
"s_waitcnt vmcnt(0)\n"
MF "v[92:95],v[58:61],v[176:179],v[92:95]\n"
MF "v[96:99],v[58:61],v[180:183],v[96:99]\n"
MF "v[100:103],v[58:61],v[184:187],v[100:103]\n"
MF "v[104:107],v[58:61],v[188:191],v[104:107]\n"
MF "v[108:111],v[58:61],v[192:195],v[108:111]\n"
"s_add_u32 s58,s74,0\n"
"s_nop 3\n"
LD "v[58:61],v10,s[32:35],s58 offen offset:192\n"
"s_waitcnt vmcnt(0)\n"
MF "v[92:95],v[58:61],v[196:199],v[92:95]\n"
MF "v[96:99],v[58:61],v[200:203],v[96:99]\n"
MF "v[100:103],v[58:61],v[204:207],v[100:103]\n"
MF "v[104:107],v[58:61],v[208:211],v[104:107]\n"
MF "v[108:111],v[58:61],v[212:215],v[108:111]\n"
"s_add_u32 s58,s74,0\n"
"s_nop 3\n"
LD "v[58:61],v10,s[32:35],s58 offen offset:256\n"
"s_waitcnt vmcnt(0)\n"
MF "v[92:95],v[58:61],v[216:219],v[92:95]\n"
MF "v[96:99],v[58:61],v[220:223],v[96:99]\n"
MF "v[100:103],v[58:61],v[224:227],v[100:103]\n"
MF "v[104:107],v[58:61],v[228:231],v[104:107]\n"
MF "v[108:111],v[58:61],v[232:235],v[108:111]\n"
"s_add_u32 s58,s74,0\n"
"s_nop 3\n"
LD "v[58:61],v10,s[32:35],s58 offen offset:320\n"
"s_waitcnt vmcnt(0)\n"
MF "v[92:95],v[58:61],v[236:239],v[92:95]\n"
MF "v[96:99],v[58:61],v[240:243],v[96:99]\n"
MF "v[100:103],v[58:61],v[244:247],v[100:103]\n"
MF "v[104:107],v[58:61],v[248:251],v[104:107]\n"
MF "v[108:111],v[58:61],v[252:255],v[108:111]\n"
"s_add_u32 s58,s74,6144\n"
"s_nop 3\n"
LD "v[58:61],v10,s[32:35],s58 offen\n"
"s_waitcnt vmcnt(0)\n"
MF "v[112:115],v[58:61],v[136:139],v[32:35]\n"
MF "v[116:119],v[58:61],v[140:143],v[36:39]\n"
MF "v[120:123],v[58:61],v[144:147],v[40:43]\n"
MF "v[124:127],v[58:61],v[148:151],v[44:47]\n"
MF "v[128:131],v[58:61],v[152:155],v[48:51]\n"
"s_add_u32 s58,s74,6144\n"
"s_nop 3\n"
LD "v[58:61],v10,s[32:35],s58 offen offset:64\n"
"s_waitcnt vmcnt(0)\n"
MF "v[112:115],v[58:61],v[156:159],v[112:115]\n"
MF "v[116:119],v[58:61],v[160:163],v[116:119]\n"
MF "v[120:123],v[58:61],v[164:167],v[120:123]\n"
MF "v[124:127],v[58:61],v[168:171],v[124:127]\n"
MF "v[128:131],v[58:61],v[172:175],v[128:131]\n"
"s_add_u32 s58,s74,6144\n"
"s_nop 3\n"
LD "v[58:61],v10,s[32:35],s58 offen offset:128\n"
"s_waitcnt vmcnt(0)\n"
MF "v[112:115],v[58:61],v[176:179],v[112:115]\n"
MF "v[116:119],v[58:61],v[180:183],v[116:119]\n"
MF "v[120:123],v[58:61],v[184:187],v[120:123]\n"
MF "v[124:127],v[58:61],v[188:191],v[124:127]\n"
MF "v[128:131],v[58:61],v[192:195],v[128:131]\n"
"s_add_u32 s58,s74,6144\n"
"s_nop 3\n"
LD "v[58:61],v10,s[32:35],s58 offen offset:192\n"
"s_waitcnt vmcnt(0)\n"
MF "v[112:115],v[58:61],v[196:199],v[112:115]\n"
MF "v[116:119],v[58:61],v[200:203],v[116:119]\n"
MF "v[120:123],v[58:61],v[204:207],v[120:123]\n"
MF "v[124:127],v[58:61],v[208:211],v[124:127]\n"
MF "v[128:131],v[58:61],v[212:215],v[128:131]\n"
"s_add_u32 s58,s74,6144\n"
"s_nop 3\n"
LD "v[58:61],v10,s[32:35],s58 offen offset:256\n"
"s_waitcnt vmcnt(0)\n"
MF "v[112:115],v[58:61],v[216:219],v[112:115]\n"
MF "v[116:119],v[58:61],v[220:223],v[116:119]\n"
MF "v[120:123],v[58:61],v[224:227],v[120:123]\n"
MF "v[124:127],v[58:61],v[228:231],v[124:127]\n"
MF "v[128:131],v[58:61],v[232:235],v[128:131]\n"
"s_add_u32 s58,s74,6144\n"
"s_nop 3\n"
LD "v[58:61],v10,s[32:35],s58 offen offset:320\n"
"s_waitcnt vmcnt(0)\n"
MF "v[112:115],v[58:61],v[236:239],v[112:115]\n"
MF "v[116:119],v[58:61],v[240:243],v[116:119]\n"
MF "v[120:123],v[58:61],v[244:247],v[120:123]\n"
MF "v[124:127],v[58:61],v[248:251],v[124:127]\n"
MF "v[128:131],v[58:61],v[252:255],v[128:131]\n"
"s_nop 7\n"
"s_mov_b32 s34,s59\n"
"v_max3_f32 v10,v92,v93,v94\n"
"s_nop 0\n"
"v_max3_f32 v11,v95,v112,v113\n"
"v_max3_f32 v10,v10,v114,v115\n"
"v_max_f32 v10,v10,v11\n"
"v_mov_b32 v11,v10\n"
"s_nop 1\n"
"v_permlane16_swap_b32 v10,v11\n"
"v_max_f32 v10,v10,v11\n"
"v_mov_b32 v11,v10\n"
"s_nop 1\n"
"v_permlane32_swap_b32 v10,v11\n"
"v_max_f32 v10,v10,v11\n"
"v_add_f32 v10,0x41300000,v10\n"
"v_max_f32 v10,0,v10\n"
"v_exp_f32 v11,-v10\n"
AR "v12,a0\n"
"v_subrev_f32 v12,s13,v12\n"
"v_fma_f32 v12,v12,v11,s13\n"
AW "a0,v12\n"
AR "v12,a20\n"
"v_subrev_f32 v12,s13,v12\n"
"v_fma_f32 v12,v12,v11,s13\n"
AW "a20,v12\n"
AR "v12,a40\n"
"v_subrev_f32 v12,s13,v12\n"
"v_fma_f32 v12,v12,v11,s13\n"
AW "a40,v12\n"
AR "v12,a60\n"
"v_subrev_f32 v12,s13,v12\n"
"v_fma_f32 v12,v12,v11,s13\n"
AW "a60,v12\n"
AR "v12,a80\n"
"v_subrev_f32 v12,s13,v12\n"
"v_fma_f32 v12,v12,v11,s13\n"
AW "a80,v12\n"
AR "v12,a100\n"
"v_subrev_f32 v12,s13,v12\n"
"v_fma_f32 v12,v12,v11,s13\n"
AW "a100,v12\n"
AR "v12,a120\n"
"v_subrev_f32 v12,s13,v12\n"
"v_fma_f32 v12,v12,v11,s13\n"
AW "a120,v12\n"
AR "v12,a140\n"
"v_subrev_f32 v12,s13,v12\n"
"v_fma_f32 v12,v12,v11,s13\n"
AW "a140,v12\n"
AR "v12,a160\n"
"v_subrev_f32 v12,s13,v12\n"
"v_fma_f32 v12,v12,v11,s13\n"
AW "a160,v12\n"
AR "v12,a180\n"
"v_subrev_f32 v12,s13,v12\n"
"v_fma_f32 v12,v12,v11,s13\n"
AW "a180,v12\n"
AR "v12,a200\n"
"v_subrev_f32 v12,s13,v12\n"
"v_fma_f32 v12,v12,v11,s13\n"
AW "a200,v12\n"
AR "v12,a220\n"
"v_subrev_f32 v12,s13,v12\n"
"v_fma_f32 v12,v12,v11,s13\n"
AW "a220,v12\n"
AR "v12,a1\n"
"v_subrev_f32 v12,s13,v12\n"
"v_fma_f32 v12,v12,v11,s13\n"
AW "a1,v12\n"
AR "v12,a21\n"
"v_subrev_f32 v12,s13,v12\n"
"v_fma_f32 v12,v12,v11,s13\n"
AW "a21,v12\n"
AR "v12,a41\n"
"v_subrev_f32 v12,s13,v12\n"
"v_fma_f32 v12,v12,v11,s13\n"
AW "a41,v12\n"
AR "v12,a61\n"
"v_subrev_f32 v12,s13,v12\n"
"v_fma_f32 v12,v12,v11,s13\n"
AW "a61,v12\n"
AR "v12,a81\n"
"v_subrev_f32 v12,s13,v12\n"
"v_fma_f32 v12,v12,v11,s13\n"
AW "a81,v12\n"
AR "v12,a101\n"
"v_subrev_f32 v12,s13,v12\n"
"v_fma_f32 v12,v12,v11,s13\n"
AW "a101,v12\n"
AR "v12,a121\n"
"v_subrev_f32 v12,s13,v12\n"
"v_fma_f32 v12,v12,v11,s13\n"
AW "a121,v12\n"
AR "v12,a141\n"
"v_subrev_f32 v12,s13,v12\n"
"v_fma_f32 v12,v12,v11,s13\n"
AW "a141,v12\n"
AR "v12,a161\n"
"v_subrev_f32 v12,s13,v12\n"
"v_fma_f32 v12,v12,v11,s13\n"
AW "a161,v12\n"
AR "v12,a181\n"
"v_subrev_f32 v12,s13,v12\n"
"v_fma_f32 v12,v12,v11,s13\n"
AW "a181,v12\n"
AR "v12,a201\n"
"v_subrev_f32 v12,s13,v12\n"
"v_fma_f32 v12,v12,v11,s13\n"
AW "a201,v12\n"
AR "v12,a221\n"
"v_subrev_f32 v12,s13,v12\n"
"v_fma_f32 v12,v12,v11,s13\n"
AW "a221,v12\n"
AR "v12,a2\n"
"v_subrev_f32 v12,s13,v12\n"
"v_fma_f32 v12,v12,v11,s13\n"
AW "a2,v12\n"
AR "v12,a22\n"
"v_subrev_f32 v12,s13,v12\n"
"v_fma_f32 v12,v12,v11,s13\n"
AW "a22,v12\n"
AR "v12,a42\n"
"v_subrev_f32 v12,s13,v12\n"
"v_fma_f32 v12,v12,v11,s13\n"
AW "a42,v12\n"
AR "v12,a62\n"
"v_subrev_f32 v12,s13,v12\n"
"v_fma_f32 v12,v12,v11,s13\n"
AW "a62,v12\n"
AR "v12,a82\n"
"v_subrev_f32 v12,s13,v12\n"
"v_fma_f32 v12,v12,v11,s13\n"
AW "a82,v12\n"
AR "v12,a102\n"
"v_subrev_f32 v12,s13,v12\n"
"v_fma_f32 v12,v12,v11,s13\n"
AW "a102,v12\n"
AR "v12,a122\n"
"v_subrev_f32 v12,s13,v12\n"
"v_fma_f32 v12,v12,v11,s13\n"
AW "a122,v12\n"
AR "v12,a142\n"
"v_subrev_f32 v12,s13,v12\n"
"v_fma_f32 v12,v12,v11,s13\n"
AW "a142,v12\n"
AR "v12,a162\n"
"v_subrev_f32 v12,s13,v12\n"
"v_fma_f32 v12,v12,v11,s13\n"
AW "a162,v12\n"
AR "v12,a182\n"
"v_subrev_f32 v12,s13,v12\n"
"v_fma_f32 v12,v12,v11,s13\n"
AW "a182,v12\n"
AR "v12,a202\n"
"v_subrev_f32 v12,s13,v12\n"
"v_fma_f32 v12,v12,v11,s13\n"
AW "a202,v12\n"
AR "v12,a222\n"
"v_subrev_f32 v12,s13,v12\n"
"v_fma_f32 v12,v12,v11,s13\n"
AW "a222,v12\n"
AR "v12,a3\n"
"v_subrev_f32 v12,s13,v12\n"
"v_fma_f32 v12,v12,v11,s13\n"
AW "a3,v12\n"
AR "v12,a23\n"
"v_subrev_f32 v12,s13,v12\n"
"v_fma_f32 v12,v12,v11,s13\n"
AW "a23,v12\n"
AR "v12,a43\n"
"v_subrev_f32 v12,s13,v12\n"
"v_fma_f32 v12,v12,v11,s13\n"
AW "a43,v12\n"
AR "v12,a63\n"
"v_subrev_f32 v12,s13,v12\n"
"v_fma_f32 v12,v12,v11,s13\n"
AW "a63,v12\n"
AR "v12,a83\n"
"v_subrev_f32 v12,s13,v12\n"
"v_fma_f32 v12,v12,v11,s13\n"
AW "a83,v12\n"
AR "v12,a103\n"
"v_subrev_f32 v12,s13,v12\n"
"v_fma_f32 v12,v12,v11,s13\n"
AW "a103,v12\n"
AR "v12,a123\n"
"v_subrev_f32 v12,s13,v12\n"
"v_fma_f32 v12,v12,v11,s13\n"
AW "a123,v12\n"
AR "v12,a143\n"
"v_subrev_f32 v12,s13,v12\n"
"v_fma_f32 v12,v12,v11,s13\n"
AW "a143,v12\n"
AR "v12,a163\n"
"v_subrev_f32 v12,s13,v12\n"
"v_fma_f32 v12,v12,v11,s13\n"
AW "a163,v12\n"
AR "v12,a183\n"
"v_subrev_f32 v12,s13,v12\n"
"v_fma_f32 v12,v12,v11,s13\n"
AW "a183,v12\n"
AR "v12,a203\n"
"v_subrev_f32 v12,s13,v12\n"
"v_fma_f32 v12,v12,v11,s13\n"
AW "a203,v12\n"
AR "v12,a223\n"
"v_subrev_f32 v12,s13,v12\n"
"v_fma_f32 v12,v12,v11,s13\n"
AW "a223,v12\n"
"v_mul_f32 v57,v52,v11\n"
"v_sub_f32 v32,v32,v10\n"
"v_sub_f32 v33,v33,v10\n"
"v_sub_f32 v34,v34,v10\n"
"v_sub_f32 v35,v35,v10\n"
"v_sub_f32 v72,v72,v10\n"
"v_sub_f32 v73,v73,v10\n"
"v_sub_f32 v74,v74,v10\n"
"v_sub_f32 v75,v75,v10\n"
"v_sub_f32 v92,v92,v10\n"
"v_sub_f32 v93,v93,v10\n"
"v_sub_f32 v94,v94,v10\n"
"v_sub_f32 v95,v95,v10\n"
"v_sub_f32 v112,v112,v10\n"
"v_sub_f32 v113,v113,v10\n"
"v_sub_f32 v114,v114,v10\n"
"v_sub_f32 v115,v115,v10\n"
"v_exp_f32 v92,v92\n"
"v_exp_f32 v93,v93\n"
"v_exp_f32 v94,v94\n"
"v_exp_f32 v95,v95\n"
"v_exp_f32 v112,v112\n"
"v_exp_f32 v113,v113\n"
"v_exp_f32 v114,v114\n"
"v_exp_f32 v115,v115\n"
"v_add_f32 v57,v57,v92\n"
"v_add_f32 v57,v57,v93\n"
"v_add_f32 v57,v57,v94\n"
"v_add_f32 v57,v57,v95\n"
"v_add_f32 v57,v57,v112\n"
"v_add_f32 v57,v57,v113\n"
"v_add_f32 v57,v57,v114\n"
"v_add_f32 v57,v57,v115\n"
CV "v92,v92,v93\n"
CV "v93,v94,v95\n"
CV "v94,v112,v113\n"
CV "v95,v114,v115\n"
"v_max3_f32 v10,v96,v97,v98\n"
"v_max3_f32 v11,v99,v116,v117\n"
"v_max3_f32 v10,v10,v118,v119\n"
"v_max_f32 v10,v10,v11\n"
"v_mov_b32 v11,v10\n"
"s_nop 1\n"
"v_permlane16_swap_b32 v10,v11\n"
"v_max_f32 v10,v10,v11\n"
"v_mov_b32 v11,v10\n"
"s_nop 1\n"
"v_permlane32_swap_b32 v10,v11\n"
"v_max_f32 v10,v10,v11\n"
"v_add_f32 v10,0x41300000,v10\n"
"v_max_f32 v10,0,v10\n"
"v_exp_f32 v11,-v10\n"
AR "v12,a4\n"
"v_subrev_f32 v12,s13,v12\n"
"v_fma_f32 v12,v12,v11,s13\n"
AW "a4,v12\n"
AR "v12,a24\n"
"v_subrev_f32 v12,s13,v12\n"
"v_fma_f32 v12,v12,v11,s13\n"
AW "a24,v12\n"
AR "v12,a44\n"
"v_subrev_f32 v12,s13,v12\n"
"v_fma_f32 v12,v12,v11,s13\n"
AW "a44,v12\n"
AR "v12,a64\n"
"v_subrev_f32 v12,s13,v12\n"
"v_fma_f32 v12,v12,v11,s13\n"
AW "a64,v12\n"
AR "v12,a84\n"
"v_subrev_f32 v12,s13,v12\n"
"v_fma_f32 v12,v12,v11,s13\n"
AW "a84,v12\n"
AR "v12,a104\n"
"v_subrev_f32 v12,s13,v12\n"
"v_fma_f32 v12,v12,v11,s13\n"
AW "a104,v12\n"
AR "v12,a124\n"
"v_subrev_f32 v12,s13,v12\n"
"v_fma_f32 v12,v12,v11,s13\n"
AW "a124,v12\n"
AR "v12,a144\n"
"v_subrev_f32 v12,s13,v12\n"
"v_fma_f32 v12,v12,v11,s13\n"
AW "a144,v12\n"
AR "v12,a164\n"
"v_subrev_f32 v12,s13,v12\n"
"v_fma_f32 v12,v12,v11,s13\n"
AW "a164,v12\n"
AR "v12,a184\n"
"v_subrev_f32 v12,s13,v12\n"
"v_fma_f32 v12,v12,v11,s13\n"
AW "a184,v12\n"
AR "v12,a204\n"
"v_subrev_f32 v12,s13,v12\n"
"v_fma_f32 v12,v12,v11,s13\n"
AW "a204,v12\n"
AR "v12,a224\n"
"v_subrev_f32 v12,s13,v12\n"
"v_fma_f32 v12,v12,v11,s13\n"
AW "a224,v12\n"
AR "v12,a5\n"
"v_subrev_f32 v12,s13,v12\n"
"v_fma_f32 v12,v12,v11,s13\n"
AW "a5,v12\n"
AR "v12,a25\n"
"v_subrev_f32 v12,s13,v12\n"
"v_fma_f32 v12,v12,v11,s13\n"
AW "a25,v12\n"
AR "v12,a45\n"
"v_subrev_f32 v12,s13,v12\n"
"v_fma_f32 v12,v12,v11,s13\n"
AW "a45,v12\n"
AR "v12,a65\n"
"v_subrev_f32 v12,s13,v12\n"
"v_fma_f32 v12,v12,v11,s13\n"
AW "a65,v12\n"
AR "v12,a85\n"
"v_subrev_f32 v12,s13,v12\n"
"v_fma_f32 v12,v12,v11,s13\n"
AW "a85,v12\n"
AR "v12,a105\n"
"v_subrev_f32 v12,s13,v12\n"
"v_fma_f32 v12,v12,v11,s13\n"
AW "a105,v12\n"
AR "v12,a125\n"
"v_subrev_f32 v12,s13,v12\n"
"v_fma_f32 v12,v12,v11,s13\n"
AW "a125,v12\n"
AR "v12,a145\n"
"v_subrev_f32 v12,s13,v12\n"
"v_fma_f32 v12,v12,v11,s13\n"
AW "a145,v12\n"
AR "v12,a165\n"
"v_subrev_f32 v12,s13,v12\n"
"v_fma_f32 v12,v12,v11,s13\n"
AW "a165,v12\n"
AR "v12,a185\n"
"v_subrev_f32 v12,s13,v12\n"
"v_fma_f32 v12,v12,v11,s13\n"
AW "a185,v12\n"
AR "v12,a205\n"
"v_subrev_f32 v12,s13,v12\n"
"v_fma_f32 v12,v12,v11,s13\n"
AW "a205,v12\n"
AR "v12,a225\n"
"v_subrev_f32 v12,s13,v12\n"
"v_fma_f32 v12,v12,v11,s13\n"
AW "a225,v12\n"
AR "v12,a6\n"
"v_subrev_f32 v12,s13,v12\n"
"v_fma_f32 v12,v12,v11,s13\n"
AW "a6,v12\n"
AR "v12,a26\n"
"v_subrev_f32 v12,s13,v12\n"
"v_fma_f32 v12,v12,v11,s13\n"
AW "a26,v12\n"
AR "v12,a46\n"
"v_subrev_f32 v12,s13,v12\n"
"v_fma_f32 v12,v12,v11,s13\n"
AW "a46,v12\n"
AR "v12,a66\n"
"v_subrev_f32 v12,s13,v12\n"
"v_fma_f32 v12,v12,v11,s13\n"
AW "a66,v12\n"
AR "v12,a86\n"
"v_subrev_f32 v12,s13,v12\n"
"v_fma_f32 v12,v12,v11,s13\n"
AW "a86,v12\n"
AR "v12,a106\n"
"v_subrev_f32 v12,s13,v12\n"
"v_fma_f32 v12,v12,v11,s13\n"
AW "a106,v12\n"
AR "v12,a126\n"
"v_subrev_f32 v12,s13,v12\n"
"v_fma_f32 v12,v12,v11,s13\n"
AW "a126,v12\n"
AR "v12,a146\n"
"v_subrev_f32 v12,s13,v12\n"
"v_fma_f32 v12,v12,v11,s13\n"
AW "a146,v12\n"
AR "v12,a166\n"
"v_subrev_f32 v12,s13,v12\n"
"v_fma_f32 v12,v12,v11,s13\n"
AW "a166,v12\n"
AR "v12,a186\n"
"v_subrev_f32 v12,s13,v12\n"
"v_fma_f32 v12,v12,v11,s13\n"
AW "a186,v12\n"
AR "v12,a206\n"
"v_subrev_f32 v12,s13,v12\n"
"v_fma_f32 v12,v12,v11,s13\n"
AW "a206,v12\n"
AR "v12,a226\n"
"v_subrev_f32 v12,s13,v12\n"
"v_fma_f32 v12,v12,v11,s13\n"
AW "a226,v12\n"
AR "v12,a7\n"
"v_subrev_f32 v12,s13,v12\n"
"v_fma_f32 v12,v12,v11,s13\n"
AW "a7,v12\n"
AR "v12,a27\n"
"v_subrev_f32 v12,s13,v12\n"
"v_fma_f32 v12,v12,v11,s13\n"
AW "a27,v12\n"
AR "v12,a47\n"
"v_subrev_f32 v12,s13,v12\n"
"v_fma_f32 v12,v12,v11,s13\n"
AW "a47,v12\n"
AR "v12,a67\n"
"v_subrev_f32 v12,s13,v12\n"
"v_fma_f32 v12,v12,v11,s13\n"
AW "a67,v12\n"
AR "v12,a87\n"
"v_subrev_f32 v12,s13,v12\n"
"v_fma_f32 v12,v12,v11,s13\n"
AW "a87,v12\n"
AR "v12,a107\n"
"v_subrev_f32 v12,s13,v12\n"
"v_fma_f32 v12,v12,v11,s13\n"
AW "a107,v12\n"
AR "v12,a127\n"
"v_subrev_f32 v12,s13,v12\n"
"v_fma_f32 v12,v12,v11,s13\n"
AW "a127,v12\n"
AR "v12,a147\n"
"v_subrev_f32 v12,s13,v12\n"
"v_fma_f32 v12,v12,v11,s13\n"
AW "a147,v12\n"
AR "v12,a167\n"
"v_subrev_f32 v12,s13,v12\n"
"v_fma_f32 v12,v12,v11,s13\n"
AW "a167,v12\n"
AR "v12,a187\n"
"v_subrev_f32 v12,s13,v12\n"
"v_fma_f32 v12,v12,v11,s13\n"
AW "a187,v12\n"
AR "v12,a207\n"
"v_subrev_f32 v12,s13,v12\n"
"v_fma_f32 v12,v12,v11,s13\n"
AW "a207,v12\n"
AR "v12,a227\n"
"v_subrev_f32 v12,s13,v12\n"
"v_fma_f32 v12,v12,v11,s13\n"
AW "a227,v12\n"
"v_mul_f32 v58,v53,v11\n"
"v_sub_f32 v36,v36,v10\n"
"v_sub_f32 v37,v37,v10\n"
"v_sub_f32 v38,v38,v10\n"
"v_sub_f32 v39,v39,v10\n"
"v_sub_f32 v76,v76,v10\n"
"v_sub_f32 v77,v77,v10\n"
"v_sub_f32 v78,v78,v10\n"
"v_sub_f32 v79,v79,v10\n"
"v_sub_f32 v96,v96,v10\n"
"v_sub_f32 v97,v97,v10\n"
"v_sub_f32 v98,v98,v10\n"
"v_sub_f32 v99,v99,v10\n"
"v_sub_f32 v116,v116,v10\n"
"v_sub_f32 v117,v117,v10\n"
"v_sub_f32 v118,v118,v10\n"
"v_sub_f32 v119,v119,v10\n"
"v_exp_f32 v96,v96\n"
"v_exp_f32 v97,v97\n"
"v_exp_f32 v98,v98\n"
"v_exp_f32 v99,v99\n"
"v_exp_f32 v116,v116\n"
"v_exp_f32 v117,v117\n"
"v_exp_f32 v118,v118\n"
"v_exp_f32 v119,v119\n"
"v_add_f32 v58,v58,v96\n"
"v_add_f32 v58,v58,v97\n"
"v_add_f32 v58,v58,v98\n"
"v_add_f32 v58,v58,v99\n"
"v_add_f32 v58,v58,v116\n"
"v_add_f32 v58,v58,v117\n"
"v_add_f32 v58,v58,v118\n"
"v_add_f32_e64 v58,v58,v119\n"
CV "v96,v96,v97\n"
CV "v97,v98,v99\n"
CV "v98,v116,v117\n"
CV "v99,v118,v119\n"
"v_max3_f32 v10,v100,v101,v102\n"
"v_max3_f32 v11,v103,v120,v121\n"
"v_max3_f32 v10,v10,v122,v123\n"
"v_max_f32 v10,v10,v11\n"
"v_mov_b32 v11,v10\n"
"s_nop 1\n"
"v_permlane16_swap_b32 v10,v11\n"
"v_max_f32 v10,v10,v11\n"
"v_mov_b32 v11,v10\n"
"s_nop 1\n"
"v_permlane32_swap_b32 v10,v11\n"
"v_max_f32 v10,v10,v11\n"
"v_add_f32 v10,0x41300000,v10\n"
"v_max_f32 v10,0,v10\n"
"v_exp_f32 v11,-v10\n"
AR "v12,a8\n"
"v_subrev_f32 v12,s13,v12\n"
"v_fma_f32 v12,v12,v11,s13\n"
AW "a8,v12\n"
AR "v12,a28\n"
"v_subrev_f32 v12,s13,v12\n"
"v_fma_f32 v12,v12,v11,s13\n"
AW "a28,v12\n"
AR "v12,a48\n"
"v_subrev_f32 v12,s13,v12\n"
"v_fma_f32 v12,v12,v11,s13\n"
AW "a48,v12\n"
AR "v12,a68\n"
"v_subrev_f32 v12,s13,v12\n"
"v_fma_f32 v12,v12,v11,s13\n"
AW "a68,v12\n"
AR "v12,a88\n"
"v_subrev_f32 v12,s13,v12\n"
"v_fma_f32 v12,v12,v11,s13\n"
AW "a88,v12\n"
AR "v12,a108\n"
"v_subrev_f32 v12,s13,v12\n"
"v_fma_f32 v12,v12,v11,s13\n"
AW "a108,v12\n"
AR "v12,a128\n"
"v_subrev_f32 v12,s13,v12\n"
"v_fma_f32 v12,v12,v11,s13\n"
AW "a128,v12\n"
AR "v12,a148\n"
"v_subrev_f32 v12,s13,v12\n"
"v_fma_f32 v12,v12,v11,s13\n"
AW "a148,v12\n"
AR "v12,a168\n"
"v_subrev_f32 v12,s13,v12\n"
"v_fma_f32 v12,v12,v11,s13\n"
AW "a168,v12\n"
AR "v12,a188\n"
"v_subrev_f32 v12,s13,v12\n"
"v_fma_f32 v12,v12,v11,s13\n"
AW "a188,v12\n"
AR "v12,a208\n"
"v_subrev_f32 v12,s13,v12\n"
"v_fma_f32 v12,v12,v11,s13\n"
AW "a208,v12\n"
AR "v12,a228\n"
"v_subrev_f32 v12,s13,v12\n"
"v_fma_f32 v12,v12,v11,s13\n"
AW "a228,v12\n"
AR "v12,a9\n"
"v_subrev_f32 v12,s13,v12\n"
"v_fma_f32 v12,v12,v11,s13\n"
AW "a9,v12\n"
AR "v12,a29\n"
"v_subrev_f32 v12,s13,v12\n"
"v_fma_f32 v12,v12,v11,s13\n"
AW "a29,v12\n"
AR "v12,a49\n"
"v_subrev_f32 v12,s13,v12\n"
"v_fma_f32 v12,v12,v11,s13\n"
AW "a49,v12\n"
AR "v12,a69\n"
"v_subrev_f32 v12,s13,v12\n"
"v_fma_f32 v12,v12,v11,s13\n"
AW "a69,v12\n"
AR "v12,a89\n"
"v_subrev_f32 v12,s13,v12\n"
"v_fma_f32 v12,v12,v11,s13\n"
AW "a89,v12\n"
AR "v12,a109\n"
"v_subrev_f32 v12,s13,v12\n"
"v_fma_f32 v12,v12,v11,s13\n"
AW "a109,v12\n"
AR "v12,a129\n"
"v_subrev_f32 v12,s13,v12\n"
"v_fma_f32 v12,v12,v11,s13\n"
AW "a129,v12\n"
AR "v12,a149\n"
"v_subrev_f32 v12,s13,v12\n"
"v_fma_f32 v12,v12,v11,s13\n"
AW "a149,v12\n"
AR "v12,a169\n"
"v_subrev_f32 v12,s13,v12\n"
"v_fma_f32 v12,v12,v11,s13\n"
AW "a169,v12\n"
AR "v12,a189\n"
"v_subrev_f32 v12,s13,v12\n"
"v_fma_f32 v12,v12,v11,s13\n"
AW "a189,v12\n"
AR "v12,a209\n"
"v_subrev_f32 v12,s13,v12\n"
"v_fma_f32 v12,v12,v11,s13\n"
AW "a209,v12\n"
AR "v12,a229\n"
"v_subrev_f32 v12,s13,v12\n"
"v_fma_f32 v12,v12,v11,s13\n"
AW "a229,v12\n"
AR "v12,a10\n"
"v_subrev_f32 v12,s13,v12\n"
"v_fma_f32 v12,v12,v11,s13\n"
AW "a10,v12\n"
AR "v12,a30\n"
"v_subrev_f32 v12,s13,v12\n"
"v_fma_f32 v12,v12,v11,s13\n"
AW "a30,v12\n"
AR "v12,a50\n"
"v_subrev_f32 v12,s13,v12\n"
"v_fma_f32 v12,v12,v11,s13\n"
AW "a50,v12\n"
AR "v12,a70\n"
"v_subrev_f32 v12,s13,v12\n"
"v_fma_f32 v12,v12,v11,s13\n"
AW "a70,v12\n"
AR "v12,a90\n"
"v_subrev_f32 v12,s13,v12\n"
"v_fma_f32 v12,v12,v11,s13\n"
AW "a90,v12\n"
AR "v12,a110\n"
"v_subrev_f32 v12,s13,v12\n"
"v_fma_f32 v12,v12,v11,s13\n"
AW "a110,v12\n"
AR "v12,a130\n"
"v_subrev_f32 v12,s13,v12\n"
"v_fma_f32 v12,v12,v11,s13\n"
AW "a130,v12\n"
AR "v12,a150\n"
"v_subrev_f32 v12,s13,v12\n"
"v_fma_f32 v12,v12,v11,s13\n"
AW "a150,v12\n"
AR "v12,a170\n"
"v_subrev_f32 v12,s13,v12\n"
"v_fma_f32 v12,v12,v11,s13\n"
AW "a170,v12\n"
AR "v12,a190\n"
"v_subrev_f32 v12,s13,v12\n"
"v_fma_f32 v12,v12,v11,s13\n"
AW "a190,v12\n"
AR "v12,a210\n"
"v_subrev_f32 v12,s13,v12\n"
"v_fma_f32 v12,v12,v11,s13\n"
AW "a210,v12\n"
AR "v12,a230\n"
"v_subrev_f32 v12,s13,v12\n"
"v_fma_f32 v12,v12,v11,s13\n"
AW "a230,v12\n"
AR "v12,a11\n"
"v_subrev_f32 v12,s13,v12\n"
"v_fma_f32 v12,v12,v11,s13\n"
AW "a11,v12\n"
AR "v12,a31\n"
"v_subrev_f32 v12,s13,v12\n"
"v_fma_f32 v12,v12,v11,s13\n"
AW "a31,v12\n"
AR "v12,a51\n"
"v_subrev_f32 v12,s13,v12\n"
"v_fma_f32 v12,v12,v11,s13\n"
AW "a51,v12\n"
AR "v12,a71\n"
"v_subrev_f32 v12,s13,v12\n"
"v_fma_f32 v12,v12,v11,s13\n"
AW "a71,v12\n"
AR "v12,a91\n"
"v_subrev_f32 v12,s13,v12\n"
"v_fma_f32 v12,v12,v11,s13\n"
AW "a91,v12\n"
AR "v12,a111\n"
"v_subrev_f32 v12,s13,v12\n"
"v_fma_f32 v12,v12,v11,s13\n"
AW "a111,v12\n"
AR "v12,a131\n"
"v_subrev_f32 v12,s13,v12\n"
"v_fma_f32 v12,v12,v11,s13\n"
AW "a131,v12\n"
AR "v12,a151\n"
"v_subrev_f32 v12,s13,v12\n"
"v_fma_f32 v12,v12,v11,s13\n"
AW "a151,v12\n"
AR "v12,a171\n"
"v_subrev_f32 v12,s13,v12\n"
"v_fma_f32 v12,v12,v11,s13\n"
AW "a171,v12\n"
AR "v12,a191\n"
"v_subrev_f32 v12,s13,v12\n"
"v_fma_f32 v12,v12,v11,s13\n"
AW "a191,v12\n"
AR "v12,a211\n"
"v_subrev_f32 v12,s13,v12\n"
"v_fma_f32 v12,v12,v11,s13\n"
AW "a211,v12\n"
AR "v12,a231\n"
"v_subrev_f32 v12,s13,v12\n"
"v_fma_f32 v12,v12,v11,s13\n"
AW "a231,v12\n"
"v_mul_f32 v59,v54,v11\n"
"v_sub_f32 v40,v40,v10\n"
"v_sub_f32 v41,v41,v10\n"
"v_sub_f32 v42,v42,v10\n"
"v_sub_f32 v43,v43,v10\n"
"v_sub_f32 v80,v80,v10\n"
"v_sub_f32 v81,v81,v10\n"
"v_sub_f32 v82,v82,v10\n"
"v_sub_f32 v83,v83,v10\n"
"v_sub_f32 v100,v100,v10\n"
"v_sub_f32 v101,v101,v10\n"
"v_sub_f32 v102,v102,v10\n"
"v_sub_f32 v103,v103,v10\n"
"v_sub_f32 v120,v120,v10\n"
"v_sub_f32 v121,v121,v10\n"
"v_sub_f32 v122,v122,v10\n"
"v_sub_f32 v123,v123,v10\n"
"v_exp_f32 v100,v100\n"
"v_exp_f32 v101,v101\n"
"v_exp_f32 v102,v102\n"
"v_exp_f32 v103,v103\n"
"v_exp_f32 v120,v120\n"
"v_exp_f32 v121,v121\n"
"v_exp_f32 v122,v122\n"
"v_exp_f32 v123,v123\n"
"v_add_f32 v59,v59,v100\n"
"v_add_f32 v59,v59,v101\n"
"v_add_f32 v59,v59,v102\n"
"v_add_f32 v59,v59,v103\n"
"v_add_f32 v59,v59,v120\n"
"v_add_f32 v59,v59,v121\n"
"v_add_f32 v59,v59,v122\n"
"v_add_f32_e64 v59,v59,v123\n"
CV "v100,v100,v101\n"
CV "v101,v102,v103\n"
CV "v102,v120,v121\n"
CV "v103,v122,v123\n"
"v_max3_f32 v10,v104,v105,v106\n"
"v_max3_f32 v11,v107,v124,v125\n"
"v_max3_f32 v10,v10,v126,v127\n"
"v_max_f32 v10,v10,v11\n"
"v_mov_b32 v11,v10\n"
"s_nop 1\n"
"v_permlane16_swap_b32 v10,v11\n"
"v_max_f32 v10,v10,v11\n"
"v_mov_b32 v11,v10\n"
"s_nop 1\n"
"v_permlane32_swap_b32 v10,v11\n"
"v_max_f32_e64 v10,v10,v11\n"
"v_add_f32 v10,0x41300000,v10\n"
"v_max_f32 v10,0,v10\n"
"v_exp_f32 v11,-v10\n"
AR "v12,a12\n"
"v_subrev_f32 v12,s13,v12\n"
"v_fma_f32 v12,v12,v11,s13\n"
AW "a12,v12\n"
AR "v12,a32\n"
"v_subrev_f32 v12,s13,v12\n"
"v_fma_f32 v12,v12,v11,s13\n"
AW "a32,v12\n"
AR "v12,a52\n"
"v_subrev_f32 v12,s13,v12\n"
"v_fma_f32 v12,v12,v11,s13\n"
AW "a52,v12\n"
AR "v12,a72\n"
"v_subrev_f32 v12,s13,v12\n"
"v_fma_f32 v12,v12,v11,s13\n"
AW "a72,v12\n"
AR "v12,a92\n"
"v_subrev_f32 v12,s13,v12\n"
"v_fma_f32 v12,v12,v11,s13\n"
AW "a92,v12\n"
AR "v12,a112\n"
"v_subrev_f32 v12,s13,v12\n"
"v_fma_f32 v12,v12,v11,s13\n"
AW "a112,v12\n"
AR "v12,a132\n"
"v_subrev_f32 v12,s13,v12\n"
"v_fma_f32 v12,v12,v11,s13\n"
AW "a132,v12\n"
AR "v12,a152\n"
"v_subrev_f32 v12,s13,v12\n"
"v_fma_f32 v12,v12,v11,s13\n"
AW "a152,v12\n"
AR "v12,a172\n"
"v_subrev_f32 v12,s13,v12\n"
"v_fma_f32 v12,v12,v11,s13\n"
AW "a172,v12\n"
AR "v12,a192\n"
"v_subrev_f32 v12,s13,v12\n"
"v_fma_f32 v12,v12,v11,s13\n"
AW "a192,v12\n"
AR "v12,a212\n"
"v_subrev_f32 v12,s13,v12\n"
"v_fma_f32 v12,v12,v11,s13\n"
AW "a212,v12\n"
AR "v12,a232\n"
"v_subrev_f32 v12,s13,v12\n"
"v_fma_f32 v12,v12,v11,s13\n"
AW "a232,v12\n"
AR "v12,a13\n"
"v_subrev_f32 v12,s13,v12\n"
"v_fma_f32 v12,v12,v11,s13\n"
AW "a13,v12\n"
AR "v12,a33\n"
"v_subrev_f32 v12,s13,v12\n"
"v_fma_f32 v12,v12,v11,s13\n"
AW "a33,v12\n"
AR "v12,a53\n"
"v_subrev_f32 v12,s13,v12\n"
"v_fma_f32 v12,v12,v11,s13\n"
AW "a53,v12\n"
AR "v12,a73\n"
"v_subrev_f32 v12,s13,v12\n"
"v_fma_f32 v12,v12,v11,s13\n"
AW "a73,v12\n"
AR "v12,a93\n"
"v_subrev_f32 v12,s13,v12\n"
"v_fma_f32 v12,v12,v11,s13\n"
AW "a93,v12\n"
AR "v12,a113\n"
"v_subrev_f32 v12,s13,v12\n"
"v_fma_f32 v12,v12,v11,s13\n"
AW "a113,v12\n"
AR "v12,a133\n"
"v_subrev_f32 v12,s13,v12\n"
"v_fma_f32 v12,v12,v11,s13\n"
AW "a133,v12\n"
AR "v12,a153\n"
"v_subrev_f32 v12,s13,v12\n"
"v_fma_f32 v12,v12,v11,s13\n"
AW "a153,v12\n"
AR "v12,a173\n"
"v_subrev_f32 v12,s13,v12\n"
"v_fma_f32 v12,v12,v11,s13\n"
AW "a173,v12\n"
AR "v12,a193\n"
"v_subrev_f32 v12,s13,v12\n"
"v_fma_f32 v12,v12,v11,s13\n"
AW "a193,v12\n"
AR "v12,a213\n"
"v_subrev_f32 v12,s13,v12\n"
"v_fma_f32 v12,v12,v11,s13\n"
AW "a213,v12\n"
AR "v12,a233\n"
"v_subrev_f32 v12,s13,v12\n"
"v_fma_f32 v12,v12,v11,s13\n"
AW "a233,v12\n"
AR "v12,a14\n"
"v_subrev_f32 v12,s13,v12\n"
"v_fma_f32 v12,v12,v11,s13\n"
AW "a14,v12\n"
AR "v12,a34\n"
"v_subrev_f32 v12,s13,v12\n"
"v_fma_f32 v12,v12,v11,s13\n"
AW "a34,v12\n"
AR "v12,a54\n"
"v_subrev_f32 v12,s13,v12\n"
"v_fma_f32 v12,v12,v11,s13\n"
AW "a54,v12\n"
AR "v12,a74\n"
"v_subrev_f32 v12,s13,v12\n"
"v_fma_f32 v12,v12,v11,s13\n"
AW "a74,v12\n"
AR "v12,a94\n"
"v_subrev_f32 v12,s13,v12\n"
"v_fma_f32 v12,v12,v11,s13\n"
AW "a94,v12\n"
AR "v12,a114\n"
"v_subrev_f32 v12,s13,v12\n"
"v_fma_f32 v12,v12,v11,s13\n"
AW "a114,v12\n"
AR "v12,a134\n"
"v_subrev_f32 v12,s13,v12\n"
"v_fma_f32 v12,v12,v11,s13\n"
AW "a134,v12\n"
AR "v12,a154\n"
"v_subrev_f32 v12,s13,v12\n"
"v_fma_f32 v12,v12,v11,s13\n"
AW "a154,v12\n"
AR "v12,a174\n"
"v_subrev_f32 v12,s13,v12\n"
"v_fma_f32 v12,v12,v11,s13\n"
AW "a174,v12\n"
AR "v12,a194\n"
"v_subrev_f32 v12,s13,v12\n"
"v_fma_f32 v12,v12,v11,s13\n"
AW "a194,v12\n"
AR "v12,a214\n"
"v_subrev_f32 v12,s13,v12\n"
"v_fma_f32 v12,v12,v11,s13\n"
AW "a214,v12\n"
AR "v12,a234\n"
"v_subrev_f32 v12,s13,v12\n"
"v_fma_f32 v12,v12,v11,s13\n"
AW "a234,v12\n"
AR "v12,a15\n"
"v_subrev_f32 v12,s13,v12\n"
"v_fma_f32 v12,v12,v11,s13\n"
AW "a15,v12\n"
AR "v12,a35\n"
"v_subrev_f32 v12,s13,v12\n"
"v_fma_f32 v12,v12,v11,s13\n"
AW "a35,v12\n"
AR "v12,a55\n"
"v_subrev_f32 v12,s13,v12\n"
"v_fma_f32 v12,v12,v11,s13\n"
AW "a55,v12\n"
AR "v12,a75\n"
"v_subrev_f32 v12,s13,v12\n"
"v_fma_f32 v12,v12,v11,s13\n"
AW "a75,v12\n"
AR "v12,a95\n"
"v_subrev_f32 v12,s13,v12\n"
"v_fma_f32 v12,v12,v11,s13\n"
AW "a95,v12\n"
AR "v12,a115\n"
"v_subrev_f32 v12,s13,v12\n"
"v_fma_f32 v12,v12,v11,s13\n"
AW "a115,v12\n"
AR "v12,a135\n"
"v_subrev_f32 v12,s13,v12\n"
"v_fma_f32 v12,v12,v11,s13\n"
AW "a135,v12\n"
AR "v12,a155\n"
"v_subrev_f32 v12,s13,v12\n"
"v_fma_f32 v12,v12,v11,s13\n"
AW "a155,v12\n"
AR "v12,a175\n"
"v_subrev_f32 v12,s13,v12\n"
"v_fma_f32 v12,v12,v11,s13\n"
AW "a175,v12\n"
AR "v12,a195\n"
"v_subrev_f32 v12,s13,v12\n"
"v_fma_f32 v12,v12,v11,s13\n"
AW "a195,v12\n"
AR "v12,a215\n"
"v_subrev_f32 v12,s13,v12\n"
"v_fma_f32 v12,v12,v11,s13\n"
AW "a215,v12\n"
AR "v12,a235\n"
"v_subrev_f32 v12,s13,v12\n"
"v_fma_f32 v12,v12,v11,s13\n"
AW "a235,v12\n"
"v_mul_f32 v60,v55,v11\n"
"v_sub_f32 v44,v44,v10\n"
"v_sub_f32 v45,v45,v10\n"
"v_sub_f32 v46,v46,v10\n"
"v_sub_f32 v47,v47,v10\n"
"v_sub_f32 v84,v84,v10\n"
"v_sub_f32 v85,v85,v10\n"
"v_sub_f32 v86,v86,v10\n"
"v_sub_f32 v87,v87,v10\n"
"v_sub_f32 v104,v104,v10\n"
"v_sub_f32 v105,v105,v10\n"
"v_sub_f32 v106,v106,v10\n"
"v_sub_f32 v107,v107,v10\n"
"v_sub_f32 v124,v124,v10\n"
"v_sub_f32 v125,v125,v10\n"
"v_sub_f32 v126,v126,v10\n"
"v_sub_f32 v127,v127,v10\n"
"v_exp_f32 v104,v104\n"
"v_exp_f32 v105,v105\n"
"v_exp_f32 v106,v106\n"
"v_exp_f32 v107,v107\n"
"v_exp_f32 v124,v124\n"
"v_exp_f32 v125,v125\n"
"v_exp_f32 v126,v126\n"
"v_exp_f32 v127,v127\n"
"v_add_f32 v60,v60,v104\n"
"v_add_f32 v60,v60,v105\n"
"v_add_f32 v60,v60,v106\n"
"v_add_f32 v60,v60,v107\n"
"v_add_f32 v60,v60,v124\n"
"v_add_f32 v60,v60,v125\n"
"v_add_f32 v60,v60,v126\n"
"v_add_f32 v60,v60,v127\n"
CV "v104,v104,v105\n"
CV "v105,v106,v107\n"
CV "v106,v124,v125\n"
CV "v107,v126,v127\n"
"v_max3_f32 v10,v108,v109,v110\n"
"v_max3_f32 v11,v111,v128,v129\n"
"v_max3_f32 v10,v10,v130,v131\n"
"v_max_f32 v10,v10,v11\n"
"v_mov_b32 v11,v10\n"
"s_nop 1\n"
"v_permlane16_swap_b32 v10,v11\n"
"v_max_f32 v10,v10,v11\n"
"v_mov_b32 v11,v10\n"
"s_nop 1\n"
"v_permlane32_swap_b32 v10,v11\n"
"v_max_f32 v10,v10,v11\n"
"v_add_f32 v10,0x41300000,v10\n"
"v_max_f32 v10,0,v10\n"
"v_exp_f32 v11,-v10\n"
AR "v12,a16\n"
"v_subrev_f32 v12,s13,v12\n"
"v_fma_f32 v12,v12,v11,s13\n"
AW "a16,v12\n"
AR "v12,a36\n"
"v_subrev_f32 v12,s13,v12\n"
"v_fma_f32 v12,v12,v11,s13\n"
AW "a36,v12\n"
AR "v12,a56\n"
"v_subrev_f32 v12,s13,v12\n"
"v_fma_f32 v12,v12,v11,s13\n"
AW "a56,v12\n"
AR "v12,a76\n"
"v_subrev_f32 v12,s13,v12\n"
"v_fma_f32 v12,v12,v11,s13\n"
AW "a76,v12\n"
AR "v12,a96\n"
"v_subrev_f32 v12,s13,v12\n"
"v_fma_f32 v12,v12,v11,s13\n"
AW "a96,v12\n"
AR "v12,a116\n"
"v_subrev_f32 v12,s13,v12\n"
"v_fma_f32 v12,v12,v11,s13\n"
AW "a116,v12\n"
AR "v12,a136\n"
"v_subrev_f32 v12,s13,v12\n"
"v_fma_f32 v12,v12,v11,s13\n"
AW "a136,v12\n"
AR "v12,a156\n"
"v_subrev_f32 v12,s13,v12\n"
"v_fma_f32 v12,v12,v11,s13\n"
AW "a156,v12\n"
AR "v12,a176\n"
"v_subrev_f32 v12,s13,v12\n"
"v_fma_f32 v12,v12,v11,s13\n"
AW "a176,v12\n"
AR "v12,a196\n"
"v_subrev_f32 v12,s13,v12\n"
"v_fma_f32 v12,v12,v11,s13\n"
AW "a196,v12\n"
AR "v12,a216\n"
"v_subrev_f32 v12,s13,v12\n"
"v_fma_f32 v12,v12,v11,s13\n"
AW "a216,v12\n"
AR "v12,a236\n"
"v_subrev_f32 v12,s13,v12\n"
"v_fma_f32 v12,v12,v11,s13\n"
AW "a236,v12\n"
AR "v12,a17\n"
"v_subrev_f32 v12,s13,v12\n"
"v_fma_f32 v12,v12,v11,s13\n"
AW "a17,v12\n"
AR "v12,a37\n"
"v_subrev_f32 v12,s13,v12\n"
"v_fma_f32 v12,v12,v11,s13\n"
AW "a37,v12\n"
AR "v12,a57\n"
"v_subrev_f32 v12,s13,v12\n"
"v_fma_f32 v12,v12,v11,s13\n"
AW "a57,v12\n"
AR "v12,a77\n"
"v_subrev_f32 v12,s13,v12\n"
"v_fma_f32 v12,v12,v11,s13\n"
AW "a77,v12\n"
AR "v12,a97\n"
"v_subrev_f32 v12,s13,v12\n"
"v_fma_f32 v12,v12,v11,s13\n"
AW "a97,v12\n"
AR "v12,a117\n"
"v_subrev_f32 v12,s13,v12\n"
"v_fma_f32 v12,v12,v11,s13\n"
AW "a117,v12\n"
AR "v12,a137\n"
"v_subrev_f32 v12,s13,v12\n"
"v_fma_f32 v12,v12,v11,s13\n"
AW "a137,v12\n"
AR "v12,a157\n"
"v_subrev_f32 v12,s13,v12\n"
"v_fma_f32 v12,v12,v11,s13\n"
AW "a157,v12\n"
AR "v12,a177\n"
"v_subrev_f32 v12,s13,v12\n"
"v_fma_f32 v12,v12,v11,s13\n"
AW "a177,v12\n"
AR "v12,a197\n"
"v_subrev_f32 v12,s13,v12\n"
"v_fma_f32 v12,v12,v11,s13\n"
AW "a197,v12\n"
AR "v12,a217\n"
"v_subrev_f32 v12,s13,v12\n"
"v_fma_f32 v12,v12,v11,s13\n"
AW "a217,v12\n"
AR "v12,a237\n"
"v_subrev_f32 v12,s13,v12\n"
"v_fma_f32 v12,v12,v11,s13\n"
AW "a237,v12\n"
AR "v12,a18\n"
"v_subrev_f32 v12,s13,v12\n"
"v_fma_f32 v12,v12,v11,s13\n"
AW "a18,v12\n"
AR "v12,a38\n"
"v_subrev_f32 v12,s13,v12\n"
"v_fma_f32 v12,v12,v11,s13\n"
AW "a38,v12\n"
AR "v12,a58\n"
"v_subrev_f32 v12,s13,v12\n"
"v_fma_f32 v12,v12,v11,s13\n"
AW "a58,v12\n"
AR "v12,a78\n"
"v_subrev_f32 v12,s13,v12\n"
"v_fma_f32 v12,v12,v11,s13\n"
AW "a78,v12\n"
AR "v12,a98\n"
"v_subrev_f32 v12,s13,v12\n"
"v_fma_f32 v12,v12,v11,s13\n"
AW "a98,v12\n"
AR "v12,a118\n"
"v_subrev_f32 v12,s13,v12\n"
"v_fma_f32 v12,v12,v11,s13\n"
AW "a118,v12\n"
AR "v12,a138\n"
"v_subrev_f32 v12,s13,v12\n"
"v_fma_f32 v12,v12,v11,s13\n"
AW "a138,v12\n"
AR "v12,a158\n"
"v_subrev_f32 v12,s13,v12\n"
"v_fma_f32 v12,v12,v11,s13\n"
AW "a158,v12\n"
AR "v12,a178\n"
"v_subrev_f32 v12,s13,v12\n"
"v_fma_f32 v12,v12,v11,s13\n"
AW "a178,v12\n"
AR "v12,a198\n"
"v_subrev_f32 v12,s13,v12\n"
"v_fma_f32 v12,v12,v11,s13\n"
AW "a198,v12\n"
AR "v12,a218\n"
"v_subrev_f32 v12,s13,v12\n"
"v_fma_f32 v12,v12,v11,s13\n"
AW "a218,v12\n"
AR "v12,a238\n"
"v_subrev_f32 v12,s13,v12\n"
"v_fma_f32 v12,v12,v11,s13\n"
AW "a238,v12\n"
AR "v12,a19\n"
"v_subrev_f32 v12,s13,v12\n"
"v_fma_f32 v12,v12,v11,s13\n"
AW "a19,v12\n"
AR "v12,a39\n"
"v_subrev_f32 v12,s13,v12\n"
"v_fma_f32 v12,v12,v11,s13\n"
AW "a39,v12\n"
AR "v12,a59\n"
"v_subrev_f32 v12,s13,v12\n"
"v_fma_f32 v12,v12,v11,s13\n"
AW "a59,v12\n"
AR "v12,a79\n"
"v_subrev_f32 v12,s13,v12\n"
"v_fma_f32 v12,v12,v11,s13\n"
AW "a79,v12\n"
AR "v12,a99\n"
"v_subrev_f32 v12,s13,v12\n"
"v_fma_f32 v12,v12,v11,s13\n"
AW "a99,v12\n"
AR "v12,a119\n"
"v_subrev_f32 v12,s13,v12\n"
"v_fma_f32 v12,v12,v11,s13\n"
AW "a119,v12\n"
AR "v12,a139\n"
"v_subrev_f32 v12,s13,v12\n"
"v_fma_f32 v12,v12,v11,s13\n"
AW "a139,v12\n"
AR "v12,a159\n"
"v_subrev_f32 v12,s13,v12\n"
"v_fma_f32 v12,v12,v11,s13\n"
AW "a159,v12\n"
AR "v12,a179\n"
"v_subrev_f32 v12,s13,v12\n"
"v_fma_f32 v12,v12,v11,s13\n"
AW "a179,v12\n"
AR "v12,a199\n"
"v_subrev_f32 v12,s13,v12\n"
"v_fma_f32 v12,v12,v11,s13\n"
AW "a199,v12\n"
AR "v12,a219\n"
"v_subrev_f32 v12,s13,v12\n"
"v_fma_f32 v12,v12,v11,s13\n"
AW "a219,v12\n"
AR "v12,a239\n"
"v_subrev_f32 v12,s13,v12\n"
"v_fma_f32 v12,v12,v11,s13\n"
AW "a239,v12\n"
"v_mul_f32 v61,v56,v11\n"
"v_sub_f32 v48,v48,v10\n"
"v_sub_f32 v49,v49,v10\n"
"v_sub_f32 v50,v50,v10\n"
"v_sub_f32 v51,v51,v10\n"
"v_sub_f32 v88,v88,v10\n"
"v_sub_f32 v89,v89,v10\n"
"v_sub_f32 v90,v90,v10\n"
"v_sub_f32 v91,v91,v10\n"
"v_sub_f32 v108,v108,v10\n"
"v_sub_f32 v109,v109,v10\n"
"v_sub_f32 v110,v110,v10\n"
"v_sub_f32 v111,v111,v10\n"
"v_sub_f32 v128,v128,v10\n"
"v_sub_f32 v129,v129,v10\n"
"v_sub_f32 v130,v130,v10\n"
"v_sub_f32 v131,v131,v10\n"
"v_exp_f32 v108,v108\n"
"v_exp_f32 v109,v109\n"
"v_exp_f32 v110,v110\n"
"v_exp_f32 v111,v111\n"
"v_exp_f32 v128,v128\n"
"v_exp_f32 v129,v129\n"
"v_exp_f32 v130,v130\n"
"v_exp_f32 v131,v131\n"
"v_add_f32 v61,v61,v108\n"
"v_add_f32 v61,v61,v109\n"
"v_add_f32 v61,v61,v110\n"
"v_add_f32 v61,v61,v111\n"
"v_add_f32 v61,v61,v128\n"
"v_add_f32 v61,v61,v129\n"
"v_add_f32 v61,v61,v130\n"
"v_add_f32_e64 v61,v61,v131\n"
CV "v108,v108,v109\n"
CV "v109,v110,v111\n"
CV "v110,v128,v129\n"
CV "v111,v130,v131\n"
"s_nop 7\n"
"s_setpc_b64 s[52:53]\n"
".Ldone:\n"
    : "+{v0}"(tid), "+{s23}"(bid), "+{s[20:21]}"(ka)
    :
    : "v1", "v2", "v3", "v4", "v5", "v6", "v7", "v8", "v9", "v10", "v11", "v12", "v13", "v14", "v15", "v16", "v17", "v18", "v19", "v20", "v21", "v22", "v23", "v24", "v25", "v26", "v27", "v28", "v29", "v30", "v31", "v32", "v33", "v34", "v35", "v36", "v37", "v38", "v39", "v40", "v41", "v42", "v43", "v44", "v45", "v46", "v47", "v48", "v49", "v50", "v51", "v52", "v53", "v54", "v55", "v56", "v57", "v58", "v59", "v60", "v61", "v62", "v63", "v64", "v65", "v66", "v67", "v68", "v69", "v70", "v71", "v72", "v73", "v74", "v75", "v76", "v77", "v78", "v79", "v80", "v81", "v82", "v83", "v84", "v85", "v86", "v87", "v88", "v89", "v90", "v91", "v92", "v93", "v94", "v95", "v96", "v97", "v98", "v99", "v100", "v101", "v102", "v103", "v104", "v105", "v106", "v107", "v108", "v109", "v110", "v111", "v112", "v113", "v114", "v115", "v116", "v117", "v118", "v119", "v120", "v121", "v122", "v123", "v124", "v125", "v126", "v127", "v128", "v129", "v130", "v131", "v132", "v133", "v134", "v135", "v136", "v137", "v138", "v139", "v140", "v141", "v142", "v143", "v144", "v145", "v146", "v147", "v148", "v149", "v150", "v151", "v152", "v153", "v154", "v155", "v156", "v157", "v158", "v159", "v160", "v161", "v162", "v163", "v164", "v165", "v166", "v167", "v168", "v169", "v170", "v171", "v172", "v173", "v174", "v175", "v176", "v177", "v178", "v179", "v180", "v181", "v182", "v183", "v184", "v185", "v186", "v187", "v188", "v189", "v190", "v191", "v192", "v193", "v194", "v195", "v196", "v197", "v198", "v199", "v200", "v201", "v202", "v203", "v204", "v205", "v206", "v207", "v208", "v209", "v210", "v211", "v212", "v213", "v214", "v215", "v216", "v217", "v218", "v219", "v220", "v221", "v222", "v223", "v224", "v225", "v226", "v227", "v228", "v229", "v230", "v231", "v232", "v233", "v234", "v235", "v236", "v237", "v238", "v239", "v240", "v241", "v242", "v243", "v244", "v245", "v246", "v247", "v248", "v249", "v250", "v251", "v252", "v253", "v254", "v255", "a0", "a1", "a2", "a3", "a4", "a5", "a6", "a7", "a8", "a9", "a10", "a11", "a12", "a13", "a14", "a15", "a16", "a17", "a18", "a19", "a20", "a21", "a22", "a23", "a24", "a25", "a26", "a27", "a28", "a29", "a30", "a31", "a32", "a33", "a34", "a35", "a36", "a37", "a38", "a39", "a40", "a41", "a42", "a43", "a44", "a45", "a46", "a47", "a48", "a49", "a50", "a51", "a52", "a53", "a54", "a55", "a56", "a57", "a58", "a59", "a60", "a61", "a62", "a63", "a64", "a65", "a66", "a67", "a68", "a69", "a70", "a71", "a72", "a73", "a74", "a75", "a76", "a77", "a78", "a79", "a80", "a81", "a82", "a83", "a84", "a85", "a86", "a87", "a88", "a89", "a90", "a91", "a92", "a93", "a94", "a95", "a96", "a97", "a98", "a99", "a100", "a101", "a102", "a103", "a104", "a105", "a106", "a107", "a108", "a109", "a110", "a111", "a112", "a113", "a114", "a115", "a116", "a117", "a118", "a119", "a120", "a121", "a122", "a123", "a124", "a125", "a126", "a127", "a128", "a129", "a130", "a131", "a132", "a133", "a134", "a135", "a136", "a137", "a138", "a139", "a140", "a141", "a142", "a143", "a144", "a145", "a146", "a147", "a148", "a149", "a150", "a151", "a152", "a153", "a154", "a155", "a156", "a157", "a158", "a159", "a160", "a161", "a162", "a163", "a164", "a165", "a166", "a167", "a168", "a169", "a170", "a171", "a172", "a173", "a174", "a175", "a176", "a177", "a178", "a179", "a180", "a181", "a182", "a183", "a184", "a185", "a186", "a187", "a188", "a189", "a190", "a191", "a192", "a193", "a194", "a195", "a196", "a197", "a198", "a199", "a200", "a201", "a202", "a203", "a204", "a205", "a206", "a207", "a208", "a209", "a210", "a211", "a212", "a213", "a214", "a215", "a216", "a217", "a218", "a219", "a220", "a221", "a222", "a223", "a224", "a225", "a226", "a227", "a228", "a229", "a230", "a231", "a232", "a233", "a234", "a235", "a236", "a237", "a238", "a239", "a240", "a241", "a242", "a243", "a244", "a245", "a246", "a247", "a248", "a249", "a250", "a251", "a252", "a253", "a254", "a255", "s0", "s1", "s2", "s3", "s4", "s5", "s6", "s7", "s8", "s9", "s10", "s11", "s12", "s13", "s14", "s15", "s16", "s17", "s18", "s19", "s22", "s24", "s25", "s26", "s27", "s28", "s29", "s30", "s31", "s32", "s33", "s34", "s35", "s36", "s37", "s38", "s39", "s40", "s41", "s42", "s43", "s44", "s45", "s46", "s47", "s48", "s49", "s50", "s51", "s52", "s53", "s54", "s55", "s56", "s57", "s58", "s59", "s60", "s61", "s62", "s63", "s64", "s65", "s66", "s67", "s68", "s69", "s70", "s71", "s72", "s73", "s74", "s75", "s76", "s77", "s78", "s79", "s80", "s81", "s82", "s83", "s84", "s85", "s86", "s87", "s88", "s89", "s90", "s91", "s92", "s93", "s94", "s95", "s96", "s97", "s98", "s99", "s100", "s101", "vcc", "scc", "memory");
}

extern "C" void kernel_launch(void* const* d_in, const int* in_sizes, int n_in, void* d_out, int out_size, void* d_ws, size_t ws_size, hipStream_t stream) {
  Ptrs P;
  for (int br = 0; br < 2; ++br) { P.x[br] = d_in[5 * br]; P.k[br] = d_in[5 * br + 1]; P.v[br] = d_in[5 * br + 2]; P.wq[br] = d_in[5 * br + 3]; P.wo[br] = d_in[5 * br + 4]; }
  (void)hipFuncSetAttribute((const void*)fused_kernel, hipFuncAttributeMaxDynamicSharedMemorySize, LDS1);
  fused_kernel<<<256, 256, LDS1, stream>>>(P, (char*)d_ws, (char*)d_out);
}
```

```cpp
#include <hip/hip_runtime.h>
#include <hip/hip_bf16.h>

#define MF "v_mfma_f32_16x16x32_bf16 "
#define TR "ds_read_b64_tr_b16 "
#define AR "v_accvgpr_read_b32 "
#define AW "v_accvgpr_write_b32 "
#define CV "v_cvt_pk_bf16_f32 "
#define LD "buffer_load_dwordx4 "
constexpr unsigned LDS1 = 160 * 1024;

struct Ptrs { const void* x[2]; const void* k[2]; const void* v[2]; const void* wq[2]; const void* wo[2]; };

__global__ __launch_bounds__(256, 1) __attribute__((aligned(4096))) void fused_kernel(Ptrs P, char* __restrict__ ws, char* __restrict__ out) {
  unsigned tid = threadIdx.x, bid = blockIdx.x;
  unsigned long long ka = (unsigned long long)__builtin_amdgcn_kernarg_segment_ptr();
  asm volatile(
".p2align 5\n"
".Lcode0:\n"
"s_and_b32 s58,s23,4\n"
"s_lshl_b32 s58,s58,1\n"
"s_load_dwordx2 s[28:29],s[20:21],s58 offset:48\n"
"s_load_dwordx2 s[24:25],s[20:21],s58\n"
"s_load_dwordx2 s[32:33],s[20:21],s58 offset:16\n"
"s_load_dwordx2 s[36:37],s[20:21],s58 offset:32\n"
"s_load_dwordx2 s[48:49],s[20:21],s58 offset:64\n"
"s_memtime s[100:101]\n"
"s_load_dwordx2 s[40:41],s[20:21],0x50\n"
"s_load_dwordx2 s[44:45],s[20:21],0x58\n"
"v_readfirstlane_b32 s22,v0\n"
"v_and_b32 v9,63,v0\n"
"v_bfe_u32 v10,v0,3,3\n"
"v_and_b32 v11,7,v0\n"
"s_lshr_b32 s22,s22,6\n"
"s_or_b32 s22,s22,s58\n"
"s_lshr_b32 s59,s23,4\n"
"s_and_b32 s58,s59,8\n"
"s_mul_i32 s59,s59,4112\n"
"s_or_b32 s22,s22,s59\n"
"s_and_b32 s59,s23,3\n"
"s_lshl_b32 s59,s59,1\n"
"s_bfe_u32 s60,s23,0x10003\n"
"s_or_b32 s59,s59,s60\n"
"s_lshl_b32 s60,s59,20\n"
"s_or_b32 s22,s22,s60\n"
"s_or_b32 s59,s59,s58\n"
"s_mul_i32 s59,s59,65792\n"
"s_xor_b32 s59,s59,524288\n"
"s_or_b32 s22,s22,s59\n"
"v_lshrrev_b32 v6,2,v0\n"
"v_and_b32 v6,4,v6\n"
"v_xor_b32 v6,v6,v11\n"
"v_lshlrev_b32_e64 v6,4,v6\n"
"s_mov_b32 s58,12288\n"
"v_mad_u32_u24 v6,v10,s58,v6\n"
"v_bfe_u32 v4,v0,4,3\n"
"v_xor_b32 v4,v4,v11\n"
"v_lshlrev_b32 v4,4,v4\n"
"s_mov_b32 s58,1280\n"
"v_mad_u32_u24 v4,v10,s58,v4\n"
"v_lshlrev_b32_e64 v9,4,v9\n"
"s_mov_b32 s26,3276800\n"
"s_mov_b32 s27,0x00020000\n"
"s_mov_b32 s30,7864320\n"
"s_mov_b32 s31,0x00020000\n"
"s_mov_b32 s34,0\n"
"s_mov_b32 s35,0x00020000\n"
"s_mov_b32 s38,0\n"
"s_mov_b32 s39,0x00020000\n"
"s_mov_b32 s42,268435456\n"
"s_mov_b32 s43,0x00020000\n"
"s_mov_b32 s46,6553600\n"
"s_mov_b32 s47,0x00020000\n"
"s_mov_b32 s50,7864320\n"
"s_mov_b32 s51,0x00020000\n"
"s_mov_b32 s98,20000\n"
"s_bfe_u32 s56,s22,0x20000\n"
"s_bfe_u32 s72,s22,0x10003\n"
"s_lshr_b32 s73,s56,1\n"
"s_and_b32 s74,s56,1\n"
"s_mul_i32 s62,s56,4096\n"
"s_mul_i32 s63,s56,5120\n"
"s_add_u32 s63,s63,98304\n"
"s_bfe_u32 s58,s22,0x40004\n"
"s_bfe_u32 s59,s22,0x40008\n"
"s_mul_i32 s68,s58,204800\n"
"s_mul_i32 s60,s56,51200\n"
"s_add_u32 s68,s68,s60\n"
"s_lshl_b32 s59,s59,1\n"
"s_add_u32 s59,s59,s73\n"
"s_mul_i32 s66,s59,384\n"
"s_mul_i32 s60,s74,393216\n"
"s_add_u32 s66,s66,s60\n"
"s_lshr_b32 s58,s22,6\n"
"s_xor_b32 s58,s58,s22\n"
"s_bfe_u32 s58,s58,0x10004\n"
"s_mul_i32 s81,s58,640\n"
"s_mul_i32 s60,s81,6144\n"
"s_add_u32 s66,s66,s60\n"
"v_xor_b32 v19,64,v4\n"
"v_add_u32 v16,97280,v6\n"
"v_add_u32 v17,194560,v6\n"
"v_add_u32 v18,291840,v6\n"
"s_waitcnt lgkmcnt(0)\n"
"s_mov_b32 s80,s100\n"
"s_mov_b32 s87,2\n"
"s_add_u32 m0,s62,0\n"
"s_add_u32 s82,s66,0\n"
"s_sub_u32 s15,s82,7864320\n"
"s_min_u32 s82,s82,s15\n"
LD "v6,s[28:31],s82 offen lds\n"
LD "v16,s[28:31],s82 offen offset:1024 lds\n"
LD "v17,s[28:31],s82 offen offset:2048 lds\n"
LD "v18,s[28:31],s82 offen offset:3072 lds\n"
"s_add_u32 s14,s81,0\n"
"s_sub_u32 s15,s14,1280\n"
"s_min_u32 s14,s14,s15\n"
"s_add_u32 s14,s14,s68\n"
"s_add_u32 s84,s14,0\n"
"s_add_u32 m0,s63,0\n"
"s_nop 0\n"
LD "v4,s[24:27],s84 offen lds\n"
"s_add_u32 s84,s14,9216\n"
LD "v19,s[24:27],s84 offen offset:1024 lds\n"
"s_add_u32 s84,s14,18432\n"
LD "v4,s[24:27],s84 offen offset:2048 lds\n"
"s_add_u32 s84,s14,27648\n"
LD "v19,s[24:27],s84 offen offset:3072 lds\n"
"s_add_u32 s84,s14,40960\n"
"s_add_u32 m0,s63,4096\n"
"s_nop 0\n"
LD "v4,s[24:27],s84 offen lds\n"
"s_lshr_b32 s58,s23,3\n"
"s_mul_i32 s59,s58,7\n"
"s_and_b32 s60,s23,7\n"
"s_mul_i32 s60,s60,3\n"
"s_add_u32 s59,s59,s60\n"
"s_and_b32 s59,s59,15\n"
"s_lshl_b32 s59,s59,8\n"
"s_or_b32 s58,s58,s59\n"
"s_and_b32 s59,s23,3\n"
"s_lshl_b32 s59,s59,5\n"
"s_or_b32 s58,s58,s59\n"
"s_bfe_u32 s59,s23,0x10000\n"
"s_lshl_b32 s59,s59,12\n"
"s_or_b32 s58,s58,s59\n"
"s_lshl_b32 s23,s23,16\n"
"s_or_b32 s23,s23,s58\n"
"v_bfe_u32 v20,v0,4,2\n"
"v_mul_u32_u24 v21,384,v10\n"
"v_xor_b32 v22,v11,v20\n"
"s_add_u32 m0,s62,16384\n"
"s_add_u32 s82,s66,128\n"
"s_sub_u32 s15,s82,7864320\n"
"s_min_u32 s82,s82,s15\n"
LD "v6,s[28:31],s82 offen lds\n"
"v_lshl_add_u32 v7,v22,4,v21\n"
"s_lshl_b32 s58,s74,6\n"
"v_xor_b32 v7,s58,v7\n"
"v_lshlrev_b32 v22,1,v20\n"
"v_xor_b32 v22,v11,v22\n"
"v_lshl_add_u32 v8,v22,4,v21\n"
"v_bfe_u32 v21,v0,1,3\n"
"v_xor_b32 v21,v21,v20\n"
"v_lshlrev_b32 v21,4,v21\n"
"v_lshl_add_u32 v21,v11,7,v21\n"
"v_bfe_u32 v22,v0,3,1\n"
"s_mul_i32 s58,s73,12288\n"
"v_mul_u32_u24 v23,3072,v22\n"
"v_add3_u32 v1,v23,v21,s58\n"
"s_mul_i32 s59,s74,10240\n"
"s_add_u32 s59,s59,98304\n"
"v_lshl_add_u32 v2,v22,10,v21\n"
"v_add_u32_e64 v2,s59,v2\n"
"v_bfe_u32 v21,v0,2,3\n"
LD "v16,s[28:31],s82 offen offset:1024 lds\n"
"v_lshlrev_b32_e64 v21,7,v21\n"
"v_bfe_u32 v23,v0,3,2\n"
"v_lshl_add_u32 v21,v23,5,v21\n"
"v_and_b32_e64 v23,3,v0\n"
"v_lshl_add_u32 v21,v23,3,v21\n"
"v_bfe_u32 v23,v0,5,1\n"
"v_mul_u32_u24 v3,3072,v23\n"
"v_add3_u32 v3,v3,v21,s58\n"
"s_bfe_u32 s58,s22,0x40004\n"
"s_mul_i32 s58,s58,409600\n"
"s_mul_i32 s59,s72,1280\n"
"s_add_u32 s58,s58,s59\n"
"v_and_b32 v21,15,v0\n"
"v_mul_u32_u24 v21,2560,v21\n"
"v_lshl_add_u32 v21,v23,4,v21\n"
"v_bfe_u32 v23,v0,4,1\n"
"v_lshl_add_u32 v21,v23,5,v21\n"
"v_add_u32_e64 v12,s58,v21\n"
"v_bfe_u32 v21,v0,2,2\n"
LD "v17,s[28:31],s82 offen offset:2048 lds\n"
"v_lshlrev_b32 v21,7,v21\n"
"v_lshl_add_u32 v21,v20,10,v21\n"
"v_lshl_add_u32 v21,v22,6,v21\n"
"v_and_b32 v23,3,v0\n"
"v_lshl_add_u32 v21,v23,4,v21\n"
"s_lshl_b32 s58,s73,13\n"
"v_add_u32 v0,s58,v21\n"
"s_getpc_b64 s[52:53]\n"
".Lcts:\n"
"s_sub_u32 s52,s52,.Lcts-.Lcode0\n"
"s_subb_u32 s53,s53,0\n"
"s_bfe_u32 s58,s23,0x50000\n"
"s_lshl_b32 s58,s58,2\n"
"s_add_u32 s58,s58,s56\n"
"s_mul_i32 s58,s58,1280\n"
"v_min_u32 v15,144,v9\n"
"v_lshl_add_u32 v15,v15,3,s58\n"
"v_min_u32 v15,.Lcode1-.Lcode0,v15\n"
LD "v18,s[28:31],s82 offen offset:3072 lds\n"
"s_bfe_u32 s58,s22,0x4000c\n"
"s_bfe_u32 s59,s22,0x40010\n"
"s_mul_i32 s69,s58,204800\n"
"s_mul_i32 s60,s56,51200\n"
"s_add_u32 s69,s69,s60\n"
"s_lshl_b32 s59,s59,1\n"
"s_add_u32 s59,s59,s73\n"
"s_mul_i32 s67,s59,384\n"
"s_mul_i32 s60,s74,393216\n"
"s_add_u32 s67,s67,s60\n"
"s_mul_i32 s60,s81,6144\n"
"s_add_u32 s67,s67,s60\n"
"s_bfe_u32 s58,s22,0x40004\n"
"s_bfe_u32 s59,s22,0x40008\n"
"s_lshl_b32 s59,s59,1\n"
"s_add_u32 s88,s59,s74\n"
"s_mul_i32 s88,s88,245760\n"
"s_add_u32 s59,s59,s73\n"
"s_lshl_b32 s70,s58,5\n"
"s_add_u32 m0,s62,32768\n"
"s_add_u32 s82,s66,256\n"
"s_sub_u32 s15,s82,7864320\n"
"s_min_u32 s82,s82,s15\n"
LD "v6,s[28:31],s82 offen lds\n"
"s_add_u32 s70,s70,s59\n"
"s_mul_i32 s70,s70,196608\n"
"s_bfe_u32 s58,s22,0x4000c\n"
"s_bfe_u32 s59,s22,0x40010\n"
"s_lshl_b32 s59,s59,1\n"
"s_add_u32 s89,s59,s74\n"
"s_mul_i32 s89,s89,245760\n"
"s_add_u32 s59,s59,s73\n"
"s_lshl_b32 s71,s58,5\n"
"s_add_u32 s71,s71,s59\n"
"s_mul_i32 s71,s71,196608\n"
"s_bitcmp1_b32 s23,12\n"
"s_cselect_b32 s58,s71,s70\n"
"s_cselect_b32 s71,s70,s71\n"
"s_mov_b32 s70,s58\n"
"s_cselect_b32 s58,s89,s88\n"
"s_cselect_b32 s89,s88,s89\n"
"s_mov_b32 s88,s58\n"
"s_cselect_b32 s69,98,1\n"
LD "v16,s[28:31],s82 offen offset:1024 lds\n"
"s_mov_b32 s54,1037384597\n"
"s_mov_b32 s55,1037384597\n"
"s_bfe_u32 s58,s22,0x40004\n"
"s_lshl_b32 s59,s72,4\n"
"s_add_u32 s59,s59,s58\n"
"s_lshl_b32 s86,s59,12\n"
"s_add_u32 s86,s86,100663296\n"
"s_mul_i32 s93,s59,1638400\n"
"s_add_u32 s93,s93,33554432\n"
"s_bfe_u32 s58,s22,0x30014\n"
"s_lshl_b32 s78,s58,1\n"
"s_add_u32 s78,s78,s74\n"
"s_lshl_b32 s94,s78,2\n"
"s_add_u32 s94,s94,s86\n"
"s_lshl_b32 s92,s58,2\n"
"s_add_u32 s92,s92,s56\n"
"s_mul_i32 s91,s58,204800\n"
"s_add_u32 s91,s91,s93\n"
"s_mul_i32 s75,s56,30720\n"
LD "v17,s[28:31],s82 offen offset:2048 lds\n"
"s_add_u32 s75,s75,s91\n"
"s_add_u32 s75,s75,0\n"
"s_mul_i32 s58,s74,5120\n"
"s_add_u32 s91,s91,s58\n"
"s_mov_b32 s90,0xf0000000\n"
"s_mov_b32 s77,0xf0000000\n"
"s_mov_b32 s85,0xf0000000\n"
AW "a240,v0\n"
AW "a244,v1\n"
AW "a241,v2\n"
AW "a245,v3\n"
AW "a242,v4\n"
AW "a243,v6\n"
AW "a252,v12\n"
"s_mov_b32 s57,0\n"
"s_mov_b32 s83,0\n"
LD "v18,s[28:31],s82 offen offset:3072 lds\n"
"global_load_dword a249,v15,s[52:53]\n"
AR "v0,a240\n"
AR "v2,a241\n"
AR "v4,a242\n"
AR "v6,a243\n"
"v_xor_b32 v1,64,v0\n"
"v_xor_b32 v3,64,v2\n"
"v_xor_b32_e64 v5,64,v4\n"
"v_add_u32 v11,97280,v6\n"
"v_add_u32 v12,194560,v6\n"
"v_add_u32 v13,291840,v6\n"
"s_mov_b64 vcc,0\n"
"s_waitcnt vmcnt(9) lgkmcnt(0)\n"
"s_barrier\n"
"s_nop 0\n"
TR "a[200:201],v0\n"
TR "a[202:203],v0 offset:512\n"
TR "a[204:205],v0 offset:4096\n"
TR "a[206:207],v0 offset:4608\n"
"ds_read_b128 a[60:63],v2\n"
"ds_read_b128 a[100:103],v3\n"
"ds_read_b128 a[64:67],v2 offset:2048\n"
"ds_read_b128 a[104:107],v3 offset:2048\n"
"ds_read_b128 a[68:71],v2 offset:4096\n"
"ds_read_b128 a[108:111],v3 offset:4096\n"
"ds_read_b128 a[72:75],v2 offset:6144\n"
"ds_read_b128 a[112:115],v3 offset:6144\n"
"ds_read_b128 a[76:79],v2 offset:8192\n"
"ds_read_b128 a[116:119],v3 offset:8192\n"
TR "a[208:209],v0 offset:8\n"
TR "a[210:211],v0 offset:520\n"
TR "a[212:213],v0 offset:4104\n"
TR "a[214:215],v0 offset:4616\n"
"s_waitcnt lgkmcnt(0)\n"
MF "v[16:19],a[200:203],a[60:63],0\n"
TR "a[216:217],v1\n"
MF "v[16:19],a[204:207],a[100:103],v[16:19]\n"
"s_add_u32 s14,s81,128\n"
TR "a[218:219],v1 offset:512\n"
MF "v[24:27],a[204:207],a[104:107],0\n"
"s_sub_u32 s15,s14,1280\n"
"v_lshrrev_b32 v15,5,v9\n"
"v_min_u32 v15,11,v15\n"
MF "v[24:27],a[200:203],a[64:67],v[24:27]\n"
"s_min_u32 s14,s14,s15\n"
TR "a[220:221],v1 offset:4096\n"
"v_mul_u32_u24 v15,160,v15\n"
MF "v[32:35],a[200:203],a[68:71],0\n"
"s_add_u32 s14,s14,s68\n"
TR "a[222:223],v1 offset:4608\n"
"v_and_or_b32 v15,v9,16,v15\n"
MF "v[32:35],a[204:207],a[108:111],v[32:35]\n"
"s_add_u32 s84,s14,0\n"
"v_max3_f32 v220,|v16|,|v17|,|v18|\n"
"v_max_f32_e64 v220,v220,|v19|\n"
MF "v[40:43],a[204:207],a[112:115],0\n"
"s_add_u32 m0,s63,20480\n"
"v_readlane_b32 s12,v220,3\n"
"v_readlane_b32 s6,v220,22\n"
MF "v[40:43],a[200:203],a[72:75],v[40:43]\n"
"s_min_u32 s12,s12,s6\n"
"v_readlane_b32 s7,v220,41\n"
"v_readlane_b32 s8,v220,60\n"
MF "v[48:51],a[200:203],a[76:79],0\n"
"s_min_u32 s7,s7,s8\n"
"v_lshlrev_b32 v15,3,v15\n"
MF "v[48:51],a[204:207],a[116:119],v[48:51]\n"
LD "v4,s[24:27],s84 offen lds\n"
MF "v[52:55],a[212:215],a[116:119],0\n"
"s_min_u32 s12,s12,s7\n"
TR "a[200:201],v1 offset:8\n"
MF "v[52:55],a[208:211],a[76:79],v[52:55]\n"
"s_and_b32 s12,s12,0x7f800000\n"
TR "a[202:203],v1 offset:520\n"
MF "v[44:47],a[208:211],a[72:75],0\n"
"s_sub_u32 s12,s12,0x14000000\n"
MF "v[44:47],a[212:215],a[112:115],v[44:47]\n"
"s_cmp_lt_u32 s12,0x50000000\n"
"s_cselect_b32 s12,s12,0xe8400000\n"
TR "a[204:205],v1 offset:4104\n"
MF "v[36:39],a[212:215],a[108:111],0\n"
"s_add_u32 s12,s12,0x17c00000\n"
TR "a[206:207],v1 offset:4616\n"
MF "v[36:39],a[208:211],a[68:71],v[36:39]\n"
"s_bfe_u32 s60,s23,0x70000\n"
"v_mov_b32 v220,s12\n"
"v_mov_b32 v221,s12\n"
MF "v[28:31],a[208:211],a[64:67],0\n"
"s_add_u32 s84,s14,9216\n"
"v_mov_b32 v222,s12\n"
"v_mov_b32 v223,s12\n"
MF "v[28:31],a[212:215],a[104:107],v[28:31]\n"
LD "v5,s[24:27],s84 offen offset:1024 lds\n"
MF "v[20:23],a[212:215],a[100:103],0\n"
"s_waitcnt vmcnt(7) lgkmcnt(0)\n"
MF "v[20:23],a[208:211],a[60:63],v[20:23]\n"
"s_barrier\n"
MF "v[56:59],a[216:219],a[60:63],v[220:223]\n"
"s_lshl_b32 s60,s60,2\n"
TR "a[208:209],v0 offset:16384\n"
MF "v[56:59],a[220:223],a[100:103],v[56:59]\n"
"s_add_u32 s84,s14,18432\n"
TR "a[210:211],v0 offset:16896\n"
MF "v[64:67],a[220:223],a[104:107],v[220:223]\n"
LD "v4,s[24:27],s84 offen offset:2048 lds\n"
MF "v[64:67],a[216:219],a[64:67],v[64:67]\n"
"s_add_u32 s60,s60,s56\n"
TR "a[212:213],v0 offset:20480\n"
MF "v[72:75],a[216:219],a[68:71],v[220:223]\n"
"s_mul_i32 s60,s60,15360\n"
TR "a[214:215],v0 offset:20992\n"
MF "v[72:75],a[220:223],a[108:111],v[72:75]\n"
MF "v[80:83],a[220:223],a[112:115],v[220:223]\n"
"s_add_u32 s84,s14,27648\n"
MF "v[80:83],a[216:219],a[72:75],v[80:83]\n"
LD "v5,s[24:27],s84 offen offset:3072 lds\n"
MF "v[88:91],a[216:219],a[76:79],v[220:223]\n"
MF "v[88:91],a[220:223],a[116:119],v[88:91]\n"
MF "v[92:95],a[204:207],a[116:119],v[220:223]\n"
"s_add_u32 s84,s14,40960\n"
TR "a[216:217],v0 offset:16392\n"
MF "v[92:95],a[200:203],a[76:79],v[92:95]\n"
"s_add_u32 m0,s63,24576\n"
TR "a[218:219],v0 offset:16904\n"
MF "v[84:87],a[200:203],a[72:75],v[220:223]\n"
LD "v4,s[24:27],s84 offen lds\n"
MF "v[84:87],a[204:207],a[112:115],v[84:87]\n"
"s_add_u32 m0,s62,49152\n"
TR "a[220:221],v0 offset:20488\n"
MF "v[76:79],a[204:207],a[108:111],v[220:223]\n"
"s_add_u32 s82,s66,786432\n"
TR "a[222:223],v0 offset:21000\n"
MF "v[76:79],a[200:203],a[68:71],v[76:79]\n"
"s_sub_u32 s15,s82,7864320\n"
MF "v[68:71],a[200:203],a[64:67],v[220:223]\n"
"s_min_u32 s82,s82,s15\n"
MF "v[68:71],a[204:207],a[104:107],v[68:71]\n"
LD "v6,s[28:31],s82 offen lds\n"
MF "v[60:63],a[204:207],a[100:103],v[220:223]\n"
MF "v[60:63],a[200:203],a[60:63],v[60:63]\n"
"s_waitcnt lgkmcnt(4)\n"
MF "v[96:99],a[208:211],a[60:63],v[220:223]\n"
TR "a[200:201],v1 offset:16384\n"
MF "v[96:99],a[212:215],a[100:103],v[96:99]\n"
TR "a[202:203],v1 offset:16896\n"
MF "v[104:107],a[212:215],a[104:107],v[220:223]\n"
LD "v11,s[28:31],s82 offen offset:1024 lds\n"
MF "v[104:107],a[208:211],a[64:67],v[104:107]\n"
TR "a[204:205],v1 offset:20480\n"
MF "v[112:115],a[208:211],a[68:71],v[220:223]\n"
TR "a[206:207],v1 offset:20992\n"
MF "v[112:115],a[212:215],a[108:111],v[112:115]\n"
MF "v[120:123],a[212:215],a[112:115],v[220:223]\n"
MF "v[120:123],a[208:211],a[72:75],v[120:123]\n"
LD "v12,s[28:31],s82 offen offset:2048 lds\n"
MF "v[128:131],a[208:211],a[76:79],v[220:223]\n"
MF "v[128:131],a[212:215],a[116:119],v[128:131]\n"
"s_waitcnt lgkmcnt(4)\n"
MF "v[132:135],a[220:223],a[116:119],v[220:223]\n"
TR "a[208:209],v1 offset:16392\n"
MF "v[132:135],a[216:219],a[76:79],v[132:135]\n"
TR "a[210:211],v1 offset:16904\n"
MF "v[124:127],a[216:219],a[72:75],v[220:223]\n"
LD "v13,s[28:31],s82 offen offset:3072 lds\n"
MF "v[124:127],a[220:223],a[112:115],v[124:127]\n"
"s_add_u32 m0,s62,65536\n"
TR "a[212:213],v1 offset:20488\n"
MF "v[116:119],a[220:223],a[108:111],v[220:223]\n"
"s_add_u32 s82,s66,786560\n"
TR "a[214:215],v1 offset:21000\n"
MF "v[116:119],a[216:219],a[68:71],v[116:119]\n"
"s_sub_u32 s15,s82,7864320\n"
MF "v[108:111],a[216:219],a[64:67],v[220:223]\n"
"s_min_u32 s82,s82,s15\n"
MF "v[108:111],a[220:223],a[104:107],v[108:111]\n"
LD "v6,s[28:31],s82 offen lds\n"
MF "v[100:103],a[220:223],a[100:103],v[220:223]\n"
"s_waitcnt vmcnt(5) lgkmcnt(0)\n"
MF "v[100:103],a[216:219],a[60:63],v[100:103]\n"
"s_barrier\n"
MF "v[136:139],a[200:203],a[60:63],v[220:223]\n"
TR "a[216:217],v0 offset:32768\n"
MF "v[136:139],a[204:207],a[100:103],v[136:139]\n"
TR "a[218:219],v0 offset:33280\n"
MF "v[144:147],a[204:207],a[104:107],v[220:223]\n"
"ds_read_b128 a[140:143],v2 offset:20480\n"
MF "v[144:147],a[200:203],a[64:67],v[144:147]\n"
TR "a[220:221],v0 offset:36864\n"
MF "v[152:155],a[200:203],a[68:71],v[220:223]\n"
TR "a[222:223],v0 offset:37376\n"
MF "v[152:155],a[204:207],a[108:111],v[152:155]\n"
LD "v11,s[28:31],s82 offen offset:1024 lds\n"
MF "v[160:163],a[204:207],a[112:115],v[220:223]\n"
"ds_read_b128 a[180:183],v3 offset:20480\n"
MF "v[160:163],a[200:203],a[72:75],v[160:163]\n"
MF "v[168:171],a[200:203],a[76:79],v[220:223]\n"
MF "v[168:171],a[204:207],a[116:119],v[168:171]\n"
LD "v12,s[28:31],s82 offen offset:2048 lds\n"
MF "v[172:175],a[212:215],a[116:119],v[220:223]\n"
TR "a[200:201],v0 offset:32776\n"
MF "v[172:175],a[208:211],a[76:79],v[172:175]\n"
TR "a[202:203],v0 offset:33288\n"
MF "v[164:167],a[208:211],a[72:75],v[220:223]\n"
"ds_read_b128 a[144:147],v2 offset:22528\n"
MF "v[164:167],a[212:215],a[112:115],v[164:167]\n"
TR "a[204:205],v0 offset:36872\n"
MF "v[156:159],a[212:215],a[108:111],v[220:223]\n"
TR "a[206:207],v0 offset:37384\n"
MF "v[156:159],a[208:211],a[68:71],v[156:159]\n"
LD "v13,s[28:31],s82 offen offset:3072 lds\n"
MF "v[148:151],a[208:211],a[64:67],v[220:223]\n"
"ds_read_b128 a[184:187],v3 offset:22528\n"
MF "v[148:151],a[212:215],a[104:107],v[148:151]\n"
"s_add_u32 m0,s62,81920\n"
MF "v[140:143],a[212:215],a[100:103],v[220:223]\n"
"s_add_u32 s82,s66,786688\n"
"s_sub_u32 s15,s82,7864320\n"
"s_min_u32 s82,s82,s15\n"
MF "v[140:143],a[208:211],a[60:63],v[140:143]\n"
"s_waitcnt lgkmcnt(7)\n"
LD "v6,s[28:31],s82 offen lds\n"
MF "v[176:179],a[216:219],a[60:63],v[220:223]\n"
TR "a[208:209],v1 offset:32768\n"
MF "v[176:179],a[220:223],a[100:103],v[176:179]\n"
TR "a[210:211],v1 offset:33280\n"
MF "v[184:187],a[220:223],a[104:107],v[220:223]\n"
"ds_read_b128 a[148:151],v2 offset:24576\n"
MF "v[184:187],a[216:219],a[64:67],v[184:187]\n"
TR "a[212:213],v1 offset:36864\n"
MF "v[192:195],a[216:219],a[68:71],v[220:223]\n"
TR "a[214:215],v1 offset:37376\n"
MF "v[192:195],a[220:223],a[108:111],v[192:195]\n"
LD "v11,s[28:31],s82 offen offset:1024 lds\n"
MF "v[200:203],a[220:223],a[112:115],v[220:223]\n"
"ds_read_b128 a[188:191],v3 offset:24576\n"
MF "v[200:203],a[216:219],a[72:75],v[200:203]\n"
MF "v[208:211],a[216:219],a[76:79],v[220:223]\n"
MF "v[208:211],a[220:223],a[116:119],v[208:211]\n"
"s_waitcnt lgkmcnt(7)\n"
LD "v12,s[28:31],s82 offen offset:2048 lds\n"
MF "v[212:215],a[204:207],a[116:119],v[220:223]\n"
TR "a[216:217],v1 offset:32776\n"
MF "v[212:215],a[200:203],a[76:79],v[212:215]\n"
TR "a[218:219],v1 offset:33288\n"
MF "v[204:207],a[200:203],a[72:75],v[220:223]\n"
"ds_read_b128 a[152:155],v2 offset:26624\n"
MF "v[204:207],a[204:207],a[112:115],v[204:207]\n"
TR "a[220:221],v1 offset:36872\n"
MF "v[196:199],a[204:207],a[108:111],v[220:223]\n"
TR "a[222:223],v1 offset:37384\n"
MF "v[196:199],a[200:203],a[68:71],v[196:199]\n"
LD "v13,s[28:31],s82 offen offset:3072 lds\n"
MF "v[188:191],a[200:203],a[64:67],v[220:223]\n"
"v_add_u32 v0,0xc000,v0\n"
MF "v[188:191],a[204:207],a[104:107],v[188:191]\n"
"s_add_u32 s14,s81,256\n"
"v_add_u32 v1,0xc000,v1\n"
MF "v[180:183],a[204:207],a[100:103],v[220:223]\n"
"s_waitcnt vmcnt(8) lgkmcnt(0)\n"
"s_mov_b64 exec,0\n"
"s_bitcmp1_b32 s23,12\n"
"s_cbranch_scc1 .Lisy1\n"
"s_cmp_eq_u32 s56,0\n"
"s_cbranch_scc1 .Lpg8b\n"
"s_cmp_eq_u32 s56,1\n"
"s_cbranch_scc1 .Lpg9b\n"
"s_cmp_eq_u32 s56,2\n"
"s_cbranch_scc1 .Lpgt0ex_b\n"
"s_branch .Lpg10b\n"
".Lisy1:\n"
"s_cmp_eq_u32 s56,0\n"
"s_cbranch_scc1 .Lpgq0_c\n"
"s_cmp_eq_u32 s56,1\n"
"s_cbranch_scc1 .Lpg4c\n"
"s_cmp_eq_u32 s56,2\n"
"s_cbranch_scc1 .Lpg5c\n"
"s_branch .Lpg6c\n"
".Lisr1:\n"
"s_mov_b64 exec,-1\n"
".Lisx1:\n"
MF "v[180:183],a[200:203],a[60:63],v[180:183]\n"
"s_barrier\n"
MF "v[216:219],a[208:211],a[60:63],v[220:223]\n"
"s_sub_u32 s15,s14,1280\n"
TR "a[200:201],v0\n"
MF "v[216:219],a[212:215],a[100:103],v[216:219]\n"
"s_min_u32 s14,s14,s15\n"
TR "a[202:203],v0 offset:512\n"
MF "v[224:227],a[212:215],a[104:107],v[220:223]\n"
"s_add_u32 s14,s14,s68\n"
"ds_read_b128 a[192:195],v3 offset:26624\n"
MF "v[224:227],a[208:211],a[64:67],v[224:227]\n"
"s_add_u32 s84,s14,0\n"
TR "a[204:205],v0 offset:4096\n"
MF "v[232:235],a[208:211],a[68:71],v[220:223]\n"
"s_add_u32 m0,s63,40960\n"
TR "a[206:207],v0 offset:4608\n"
MF "v[232:235],a[212:215],a[108:111],v[232:235]\n"
LD "v4,s[24:27],s84 offen lds\n"
MF "v[240:243],a[212:215],a[112:115],v[220:223]\n"
"ds_read_b128 a[156:159],v2 offset:28672\n"
MF "v[240:243],a[208:211],a[72:75],v[240:243]\n"
"ds_read_b128 a[196:199],v3 offset:28672\n"
MF "v[248:251],a[208:211],a[76:79],v[220:223]\n"
"s_add_u32 s84,s14,9216\n"
MF "v[248:251],a[212:215],a[116:119],v[248:251]\n"
LD "v5,s[24:27],s84 offen offset:1024 lds\n"
MF "v[252:255],a[220:223],a[116:119],v[220:223]\n"
TR "a[208:209],v0 offset:8\n"
MF "v[252:255],a[216:219],a[76:79],v[252:255]\n"
TR "a[210:211],v0 offset:520\n"
MF "v[244:247],a[216:219],a[72:75],v[220:223]\n"
MF "v[244:247],a[220:223],a[112:115],v[244:247]\n"
TR "a[212:213],v0 offset:4104\n"
MF "v[236:239],a[220:223],a[108:111],v[220:223]\n"
"s_add_u32 s84,s14,18432\n"
TR "a[214:215],v0 offset:4616\n"
MF "v[236:239],a[216:219],a[68:71],v[236:239]\n"
LD "v4,s[24:27],s84 offen offset:2048 lds\n"
MF "v[228:231],a[216:219],a[64:67],v[220:223]\n"
MF "v[228:231],a[220:223],a[104:107],v[228:231]\n"
MF "v[220:223],a[220:223],a[100:103],v[220:223]\n"
".Lpg1a:\n"
"s_cbranch_execz .Lpg7a\n"
"s_add_u32 s84,s14,27648\n"
MF "v[220:223],a[216:219],a[60:63],v[220:223]\n"
"s_waitcnt lgkmcnt(4)\n"
LD "v5,s[24:27],s84 offen offset:3072 lds\n"
MF "v[16:19],a[200:203],a[140:143],v[16:19]\n"
TR "a[216:217],v1\n"
MF "v[16:19],a[204:207],a[180:183],v[16:19]\n"
TR "a[218:219],v1 offset:512\n"
MF "v[24:27],a[204:207],a[184:187],v[24:27]\n"
MF "v[24:27],a[200:203],a[144:147],v[24:27]\n"
"s_add_u32 s84,s14,40960\n"
TR "a[220:221],v1 offset:4096\n"
MF "v[32:35],a[200:203],a[148:151],v[32:35]\n"
"s_add_u32 m0,s63,45056\n"
TR "a[222:223],v1 offset:4608\n"
MF "v[32:35],a[204:207],a[188:191],v[32:35]\n"
LD "v4,s[24:27],s84 offen lds\n"
MF "v[40:43],a[204:207],a[192:195],v[40:43]\n"
MF "v[40:43],a[200:203],a[152:155],v[40:43]\n"
"s_add_u32 m0,s62,0\n"
MF "v[48:51],a[200:203],a[156:159],v[48:51]\n"
"s_add_u32 s82,s66,1572864\n"
"s_sub_u32 s15,s82,7864320\n"
"s_min_u32 s82,s82,s15\n"
MF "v[48:51],a[204:207],a[196:199],v[48:51]\n"
"s_waitcnt lgkmcnt(4)\n"
LD "v6,s[28:31],s82 offen lds\n"
MF "v[52:55],a[212:215],a[196:199],v[52:55]\n"
TR "a[200:201],v1 offset:8\n"
MF "v[52:55],a[208:211],a[156:159],v[52:55]\n"
TR "a[202:203],v1 offset:520\n"
MF "v[44:47],a[208:211],a[152:155],v[44:47]\n"
MF "v[44:47],a[212:215],a[192:195],v[44:47]\n"
TR "a[204:205],v1 offset:4104\n"
MF "v[36:39],a[212:215],a[188:191],v[36:39]\n"
TR "a[206:207],v1 offset:4616\n"
MF "v[36:39],a[208:211],a[148:151],v[36:39]\n"
LD "v11,s[28:31],s82 offen offset:1024 lds\n"
MF "v[28:31],a[208:211],a[144:147],v[28:31]\n"
MF "v[28:31],a[212:215],a[184:187],v[28:31]\n"
MF "v[20:23],a[212:215],a[180:183],v[20:23]\n"
"s_waitcnt vmcnt(11) lgkmcnt(0)\n"
LD "v12,s[28:31],s82 offen offset:2048 lds\n"
MF "v[20:23],a[208:211],a[140:143],v[20:23]\n"
"s_barrier\n"
"v_bfe_u32 v14,v9,4,4\n"
"v_lshlrev_b32 v14,2,v14\n"
"buffer_load_dword v10,v14,s[40:43],s86 offen sc1\n"
".Lhs2:\n"
MF "v[56:59],a[216:219],a[140:143],v[56:59]\n"
TR "a[208:209],v0 offset:16384\n"
MF "v[56:59],a[220:223],a[180:183],v[56:59]\n"
TR "a[210:211],v0 offset:16896\n"
MF "v[64:67],a[220:223],a[184:187],v[64:67]\n"
LD "v13,s[28:31],s82 offen offset:3072 lds\n"
MF "v[64:67],a[216:219],a[144:147],v[64:67]\n"
TR "a[212:213],v0 offset:20480\n"
MF "v[72:75],a[216:219],a[148:151],v[72:75]\n"
TR "a[214:215],v0 offset:20992\n"
MF "v[72:75],a[220:223],a[188:191],v[72:75]\n"
"s_add_u32 s14,s81,384\n"
"s_sub_u32 s15,s14,1280\n"
"s_min_u32 s14,s14,s15\n"
MF "v[80:83],a[220:223],a[192:195],v[80:83]\n"
"s_add_u32 s14,s14,s68\n"
"s_add_u32 s84,s14,0\n"
"s_add_u32 m0,s63,0\n"
MF "v[80:83],a[216:219],a[152:155],v[80:83]\n"
LD "v4,s[24:27],s84 offen lds\n"
MF "v[88:91],a[216:219],a[156:159],v[88:91]\n"
MF "v[88:91],a[220:223],a[196:199],v[88:91]\n"
MF "v[92:95],a[204:207],a[196:199],v[92:95]\n"
TR "a[216:217],v0 offset:16392\n"
MF "v[92:95],a[200:203],a[156:159],v[92:95]\n"
"s_add_u32 s84,s14,9216\n"
TR "a[218:219],v0 offset:16904\n"
MF "v[84:87],a[200:203],a[152:155],v[84:87]\n"
LD "v5,s[24:27],s84 offen offset:1024 lds\n"
MF "v[84:87],a[204:207],a[192:195],v[84:87]\n"
"s_add_u32 m0,s62,16384\n"
TR "a[220:221],v0 offset:20488\n"
MF "v[76:79],a[204:207],a[188:191],v[76:79]\n"
"s_add_u32 s82,s66,1572992\n"
TR "a[222:223],v0 offset:21000\n"
MF "v[76:79],a[200:203],a[148:151],v[76:79]\n"
"s_sub_u32 s15,s82,7864320\n"
MF "v[68:71],a[200:203],a[144:147],v[68:71]\n"
"s_min_u32 s82,s82,s15\n"
MF "v[68:71],a[204:207],a[184:187],v[68:71]\n"
LD "v6,s[28:31],s82 offen lds\n"
MF "v[60:63],a[204:207],a[180:183],v[60:63]\n"
MF "v[60:63],a[200:203],a[140:143],v[60:63]\n"
"s_waitcnt lgkmcnt(4)\n"
MF "v[96:99],a[208:211],a[140:143],v[96:99]\n"
TR "a[200:201],v1 offset:16384\n"
MF "v[96:99],a[212:215],a[180:183],v[96:99]\n"
TR "a[202:203],v1 offset:16896\n"
MF "v[104:107],a[212:215],a[184:187],v[104:107]\n"
LD "v11,s[28:31],s82 offen offset:1024 lds\n"
MF "v[104:107],a[208:211],a[144:147],v[104:107]\n"
TR "a[204:205],v1 offset:20480\n"
MF "v[112:115],a[208:211],a[148:151],v[112:115]\n"
TR "a[206:207],v1 offset:20992\n"
MF "v[112:115],a[212:215],a[188:191],v[112:115]\n"
MF "v[120:123],a[212:215],a[192:195],v[120:123]\n"
MF "v[120:123],a[208:211],a[152:155],v[120:123]\n"
LD "v12,s[28:31],s82 offen offset:2048 lds\n"
MF "v[128:131],a[208:211],a[156:159],v[128:131]\n"
MF "v[128:131],a[212:215],a[196:199],v[128:131]\n"
"s_waitcnt lgkmcnt(4)\n"
MF "v[132:135],a[220:223],a[196:199],v[132:135]\n"
TR "a[208:209],v1 offset:16392\n"
MF "v[132:135],a[216:219],a[156:159],v[132:135]\n"
TR "a[210:211],v1 offset:16904\n"
MF "v[124:127],a[216:219],a[152:155],v[124:127]\n"
LD "v13,s[28:31],s82 offen offset:3072 lds\n"
MF "v[124:127],a[220:223],a[192:195],v[124:127]\n"
TR "a[212:213],v1 offset:20488\n"
MF "v[116:119],a[220:223],a[188:191],v[116:119]\n"
TR "a[214:215],v1 offset:21000\n"
MF "v[116:119],a[216:219],a[148:151],v[116:119]\n"
"s_add_u32 s84,s14,20480\n"
MF "v[108:111],a[216:219],a[144:147],v[108:111]\n"
"s_add_u32 m0,s63,2048\n"
MF "v[108:111],a[220:223],a[184:187],v[108:111]\n"
LD "v4,s[24:27],s84 offen lds\n"
MF "v[100:103],a[220:223],a[180:183],v[100:103]\n"
"s_waitcnt vmcnt(11) lgkmcnt(0)\n"
MF "v[100:103],a[216:219],a[140:143],v[100:103]\n"
"s_barrier\n"
MF "v[136:139],a[200:203],a[140:143],v[136:139]\n"
TR "a[216:217],v0 offset:32768\n"
MF "v[136:139],a[204:207],a[180:183],v[136:139]\n"
TR "a[218:219],v0 offset:33280\n"
MF "v[144:147],a[204:207],a[184:187],v[144:147]\n"
"ds_read_b128 a[60:63],v2 offset:40960\n"
MF "v[144:147],a[200:203],a[144:147],v[144:147]\n"
TR "a[220:221],v0 offset:36864\n"
MF "v[152:155],a[200:203],a[148:151],v[152:155]\n"
"s_add_u32 s84,s14,29696\n"
TR "a[222:223],v0 offset:37376\n"
MF "v[152:155],a[204:207],a[188:191],v[152:155]\n"
LD "v5,s[24:27],s84 offen offset:1024 lds\n"
MF "v[160:163],a[204:207],a[192:195],v[160:163]\n"
"ds_read_b128 a[100:103],v3 offset:40960\n"
MF "v[160:163],a[200:203],a[152:155],v[160:163]\n"
"s_add_u32 m0,s62,32768\n"
MF "v[168:171],a[200:203],a[156:159],v[168:171]\n"
"s_add_u32 s82,s66,1573120\n"
"s_sub_u32 s15,s82,7864320\n"
"s_min_u32 s82,s82,s15\n"
MF "v[168:171],a[204:207],a[196:199],v[168:171]\n"
LD "v6,s[28:31],s82 offen lds\n"
MF "v[172:175],a[212:215],a[196:199],v[172:175]\n"
TR "a[200:201],v0 offset:32776\n"
MF "v[172:175],a[208:211],a[156:159],v[172:175]\n"
TR "a[202:203],v0 offset:33288\n"
MF "v[164:167],a[208:211],a[152:155],v[164:167]\n"
"ds_read_b128 a[64:67],v2 offset:43008\n"
MF "v[164:167],a[212:215],a[192:195],v[164:167]\n"
TR "a[204:205],v0 offset:36872\n"
MF "v[156:159],a[212:215],a[188:191],v[156:159]\n"
TR "a[206:207],v0 offset:37384\n"
MF "v[156:159],a[208:211],a[148:151],v[156:159]\n"
LD "v11,s[28:31],s82 offen offset:1024 lds\n"
MF "v[148:151],a[208:211],a[144:147],v[148:151]\n"
"ds_read_b128 a[104:107],v3 offset:43008\n"
MF "v[148:151],a[212:215],a[184:187],v[148:151]\n"
MF "v[140:143],a[212:215],a[180:183],v[140:143]\n"
MF "v[140:143],a[208:211],a[140:143],v[140:143]\n"
"s_waitcnt lgkmcnt(7)\n"
LD "v12,s[28:31],s82 offen offset:2048 lds\n"
MF "v[176:179],a[216:219],a[140:143],v[176:179]\n"
TR "a[208:209],v1 offset:32768\n"
MF "v[176:179],a[220:223],a[180:183],v[176:179]\n"
TR "a[210:211],v1 offset:33280\n"
MF "v[184:187],a[220:223],a[184:187],v[184:187]\n"
"ds_read_b128 a[68:71],v2 offset:45056\n"
MF "v[184:187],a[216:219],a[144:147],v[184:187]\n"
TR "a[212:213],v1 offset:36864\n"
MF "v[192:195],a[216:219],a[148:151],v[192:195]\n"
TR "a[214:215],v1 offset:37376\n"
MF "v[192:195],a[220:223],a[188:191],v[192:195]\n"
LD "v13,s[28:31],s82 offen offset:3072 lds\n"
MF "v[200:203],a[220:223],a[192:195],v[200:203]\n"
"ds_read_b128 a[108:111],v3 offset:45056\n"
MF "v[200:203],a[216:219],a[152:155],v[200:203]\n"
"s_add_u32 s84,s14,40960\n"
MF "v[208:211],a[216:219],a[156:159],v[208:211]\n"
"s_add_u32 m0,s63,4096\n"
MF "v[208:211],a[220:223],a[196:199],v[208:211]\n"
"s_waitcnt lgkmcnt(7)\n"
LD "v4,s[24:27],s84 offen lds\n"
MF "v[212:215],a[204:207],a[196:199],v[212:215]\n"
"s_sub_u32 s66,s66,s83\n"
TR "a[216:217],v1 offset:32776\n"
MF "v[212:215],a[200:203],a[156:159],v[212:215]\n"
"s_add_u32 m0,s62,49152\n"
TR "a[218:219],v1 offset:33288\n"
MF "v[204:207],a[200:203],a[152:155],v[204:207]\n"
"s_add_u32 s82,s66,2359296\n"
"ds_read_b128 a[72:75],v2 offset:47104\n"
MF "v[204:207],a[204:207],a[192:195],v[204:207]\n"
"s_sub_u32 s15,s82,7864320\n"
TR "a[220:221],v1 offset:36872\n"
MF "v[196:199],a[204:207],a[188:191],v[196:199]\n"
"s_min_u32 s82,s82,s15\n"
TR "a[222:223],v1 offset:37384\n"
MF "v[196:199],a[200:203],a[148:151],v[196:199]\n"
LD "v6,s[28:31],s82 offen lds\n"
MF "v[188:191],a[200:203],a[144:147],v[188:191]\n"
"v_add_u32 v0,0xffff4000,v0\n"
MF "v[188:191],a[204:207],a[184:187],v[188:191]\n"
"v_add_u32 v1,0xffff4000,v1\n"
MF "v[180:183],a[204:207],a[180:183],v[180:183]\n"
"s_waitcnt vmcnt(14) lgkmcnt(0)\n"
LD "v11,s[28:31],s82 offen offset:1024 lds\n"
MF "v[180:183],a[200:203],a[140:143],v[180:183]\n"
"s_barrier\n"
MF "v[216:219],a[208:211],a[140:143],v[216:219]\n"
TR "a[200:201],v0\n"
MF "v[216:219],a[212:215],a[180:183],v[216:219]\n"
TR "a[202:203],v0 offset:512\n"
MF "v[224:227],a[212:215],a[184:187],v[224:227]\n"
"ds_read_b128 a[112:115],v3 offset:47104\n"
MF "v[224:227],a[208:211],a[144:147],v[224:227]\n"
TR "a[204:205],v0 offset:4096\n"
MF "v[232:235],a[208:211],a[148:151],v[232:235]\n"
TR "a[206:207],v0 offset:4608\n"
MF "v[232:235],a[212:215],a[188:191],v[232:235]\n"
LD "v12,s[28:31],s82 offen offset:2048 lds\n"
MF "v[240:243],a[212:215],a[192:195],v[240:243]\n"
"ds_read_b128 a[76:79],v2 offset:49152\n"
MF "v[240:243],a[208:211],a[152:155],v[240:243]\n"
"ds_read_b128 a[116:119],v3 offset:49152\n"
MF "v[248:251],a[208:211],a[156:159],v[248:251]\n"
MF "v[248:251],a[212:215],a[196:199],v[248:251]\n"
LD "v13,s[28:31],s82 offen offset:3072 lds\n"
MF "v[252:255],a[220:223],a[196:199],v[252:255]\n"
TR "a[208:209],v0 offset:8\n"
MF "v[252:255],a[216:219],a[156:159],v[252:255]\n"
TR "a[210:211],v0 offset:520\n"
MF "v[244:247],a[216:219],a[152:155],v[244:247]\n"
"s_add_u32 s14,s81,512\n"
"s_sub_u32 s15,s14,1280\n"
MF "v[244:247],a[220:223],a[192:195],v[244:247]\n"
"s_min_u32 s14,s14,s15\n"
"s_add_u32 s14,s14,s68\n"
TR "a[212:213],v0 offset:4104\n"
MF "v[236:239],a[220:223],a[188:191],v[236:239]\n"
"s_add_u32 s84,s14,0\n"
"s_add_u32 m0,s63,20480\n"
TR "a[214:215],v0 offset:4616\n"
MF "v[236:239],a[216:219],a[148:151],v[236:239]\n"
LD "v4,s[24:27],s84 offen lds\n"
MF "v[228:231],a[216:219],a[144:147],v[228:231]\n"
MF "v[228:231],a[220:223],a[184:187],v[228:231]\n"
MF "v[220:223],a[220:223],a[180:183],v[220:223]\n"
"s_add_u32 s84,s14,9216\n"
MF "v[220:223],a[216:219],a[140:143],v[220:223]\n"
"s_waitcnt lgkmcnt(4)\n"
LD "v5,s[24:27],s84 offen offset:1024 lds\n"
MF "v[16:19],a[200:203],a[60:63],v[16:19]\n"
TR "a[216:217],v1\n"
MF "v[16:19],a[204:207],a[100:103],v[16:19]\n"
"s_add_u32 m0,s62,65536\n"
TR "a[218:219],v1 offset:512\n"
MF "v[24:27],a[204:207],a[104:107],v[24:27]\n"
"s_add_u32 s82,s66,2359424\n"
MF "v[24:27],a[200:203],a[64:67],v[24:27]\n"
"s_sub_u32 s15,s82,7864320\n"
TR "a[220:221],v1 offset:4096\n"
MF "v[32:35],a[200:203],a[68:71],v[32:35]\n"
"s_min_u32 s82,s82,s15\n"
TR "a[222:223],v1 offset:4608\n"
MF "v[32:35],a[204:207],a[108:111],v[32:35]\n"
LD "v6,s[28:31],s82 offen lds\n"
MF "v[40:43],a[204:207],a[112:115],v[40:43]\n"
MF "v[40:43],a[200:203],a[72:75],v[40:43]\n"
MF "v[48:51],a[200:203],a[76:79],v[48:51]\n"
MF "v[48:51],a[204:207],a[116:119],v[48:51]\n"
"s_waitcnt lgkmcnt(4)\n"
LD "v11,s[28:31],s82 offen offset:1024 lds\n"
MF "v[52:55],a[212:215],a[116:119],v[52:55]\n"
TR "a[200:201],v1 offset:8\n"
MF "v[52:55],a[208:211],a[76:79],v[52:55]\n"
TR "a[202:203],v1 offset:520\n"
MF "v[44:47],a[208:211],a[72:75],v[44:47]\n"
MF "v[44:47],a[212:215],a[112:115],v[44:47]\n"
TR "a[204:205],v1 offset:4104\n"
MF "v[36:39],a[212:215],a[108:111],v[36:39]\n"
TR "a[206:207],v1 offset:4616\n"
MF "v[36:39],a[208:211],a[68:71],v[36:39]\n"
LD "v12,s[28:31],s82 offen offset:2048 lds\n"
MF "v[28:31],a[208:211],a[64:67],v[28:31]\n"
MF "v[28:31],a[212:215],a[104:107],v[28:31]\n"
MF "v[20:23],a[212:215],a[100:103],v[20:23]\n"
"s_waitcnt vmcnt(16) lgkmcnt(0)\n"
LD "v13,s[28:31],s82 offen offset:3072 lds\n"
MF "v[20:23],a[208:211],a[60:63],v[20:23]\n"
"s_barrier\n"
MF "v[56:59],a[216:219],a[60:63],v[56:59]\n"
"s_add_u32 s84,s14,20480\n"
TR "a[208:209],v0 offset:16384\n"
MF "v[56:59],a[220:223],a[100:103],v[56:59]\n"
"s_add_u32 m0,s63,22528\n"
TR "a[210:211],v0 offset:16896\n"
MF "v[64:67],a[220:223],a[104:107],v[64:67]\n"
LD "v4,s[24:27],s84 offen lds\n"
MF "v[64:67],a[216:219],a[64:67],v[64:67]\n"
TR "a[212:213],v0 offset:20480\n"
MF "v[72:75],a[216:219],a[68:71],v[72:75]\n"
TR "a[214:215],v0 offset:20992\n"
MF "v[72:75],a[220:223],a[108:111],v[72:75]\n"
MF "v[80:83],a[220:223],a[112:115],v[80:83]\n"
"s_add_u32 s84,s14,29696\n"
MF "v[80:83],a[216:219],a[72:75],v[80:83]\n"
LD "v5,s[24:27],s84 offen offset:1024 lds\n"
MF "v[88:91],a[216:219],a[76:79],v[88:91]\n"
"s_add_u32 m0,s62,81920\n"
MF "v[88:91],a[220:223],a[116:119],v[88:91]\n"
"s_add_u32 s82,s66,2359552\n"
MF "v[92:95],a[204:207],a[116:119],v[92:95]\n"
"s_sub_u32 s15,s82,7864320\n"
TR "a[216:217],v0 offset:16392\n"
MF "v[92:95],a[200:203],a[76:79],v[92:95]\n"
"s_min_u32 s82,s82,s15\n"
TR "a[218:219],v0 offset:16904\n"
MF "v[84:87],a[200:203],a[72:75],v[84:87]\n"
LD "v6,s[28:31],s82 offen lds\n"
MF "v[84:87],a[204:207],a[112:115],v[84:87]\n"
TR "a[220:221],v0 offset:20488\n"
MF "v[76:79],a[204:207],a[108:111],v[76:79]\n"
TR "a[222:223],v0 offset:21000\n"
MF "v[76:79],a[200:203],a[68:71],v[76:79]\n"
MF "v[68:71],a[200:203],a[64:67],v[68:71]\n"
MF "v[68:71],a[204:207],a[104:107],v[68:71]\n"
LD "v11,s[28:31],s82 offen offset:1024 lds\n"
MF "v[60:63],a[204:207],a[100:103],v[60:63]\n"
MF "v[60:63],a[200:203],a[60:63],v[60:63]\n"
"s_waitcnt lgkmcnt(4)\n"
MF "v[96:99],a[208:211],a[60:63],v[96:99]\n"
TR "a[200:201],v1 offset:16384\n"
MF "v[96:99],a[212:215],a[100:103],v[96:99]\n"
TR "a[202:203],v1 offset:16896\n"
MF "v[104:107],a[212:215],a[104:107],v[104:107]\n"
LD "v12,s[28:31],s82 offen offset:2048 lds\n"
MF "v[104:107],a[208:211],a[64:67],v[104:107]\n"
TR "a[204:205],v1 offset:20480\n"
MF "v[112:115],a[208:211],a[68:71],v[112:115]\n"
TR "a[206:207],v1 offset:20992\n"
MF "v[112:115],a[212:215],a[108:111],v[112:115]\n"
MF "v[120:123],a[212:215],a[112:115],v[120:123]\n"
MF "v[120:123],a[208:211],a[72:75],v[120:123]\n"
LD "v13,s[28:31],s82 offen offset:3072 lds\n"
MF "v[128:131],a[208:211],a[76:79],v[128:131]\n"
MF "v[128:131],a[212:215],a[116:119],v[128:131]\n"
"s_waitcnt lgkmcnt(4)\n"
MF "v[132:135],a[220:223],a[116:119],v[132:135]\n"
"s_add_u32 s84,s14,40960\n"
TR "a[208:209],v1 offset:16392\n"
MF "v[132:135],a[216:219],a[76:79],v[132:135]\n"
"s_add_u32 m0,s63,24576\n"
TR "a[210:211],v1 offset:16904\n"
MF "v[124:127],a[216:219],a[72:75],v[124:127]\n"
LD "v4,s[24:27],s84 offen lds\n"
MF "v[124:127],a[220:223],a[112:115],v[124:127]\n"
"s_add_u32 m0,s62,0\n"
TR "a[212:213],v1 offset:20488\n"
MF "v[116:119],a[220:223],a[108:111],v[116:119]\n"
"s_add_u32 s82,s66,3145728\n"
TR "a[214:215],v1 offset:21000\n"
MF "v[116:119],a[216:219],a[68:71],v[116:119]\n"
"s_sub_u32 s15,s82,7864320\n"
MF "v[108:111],a[216:219],a[64:67],v[108:111]\n"
"s_min_u32 s82,s82,s15\n"
MF "v[108:111],a[220:223],a[104:107],v[108:111]\n"
LD "v6,s[28:31],s82 offen lds\n"
MF "v[100:103],a[220:223],a[100:103],v[100:103]\n"
"s_waitcnt vmcnt(18) lgkmcnt(0)\n"
MF "v[100:103],a[216:219],a[60:63],v[100:103]\n"
"s_barrier\n"
MF "v[136:139],a[200:203],a[60:63],v[136:139]\n"
TR "a[216:217],v0 offset:32768\n"
MF "v[136:139],a[204:207],a[100:103],v[136:139]\n"
TR "a[218:219],v0 offset:33280\n"
MF "v[144:147],a[204:207],a[104:107],v[144:147]\n"
"ds_read_b128 a[140:143],v2\n"
MF "v[144:147],a[200:203],a[64:67],v[144:147]\n"
TR "a[220:221],v0 offset:36864\n"
MF "v[152:155],a[200:203],a[68:71],v[152:155]\n"
TR "a[222:223],v0 offset:37376\n"
MF "v[152:155],a[204:207],a[108:111],v[152:155]\n"
LD "v11,s[28:31],s82 offen offset:1024 lds\n"
MF "v[160:163],a[204:207],a[112:115],v[160:163]\n"
"ds_read_b128 a[180:183],v3\n"
MF "v[160:163],a[200:203],a[72:75],v[160:163]\n"
MF "v[168:171],a[200:203],a[76:79],v[168:171]\n"
MF "v[168:171],a[204:207],a[116:119],v[168:171]\n"
LD "v12,s[28:31],s82 offen offset:2048 lds\n"
MF "v[172:175],a[212:215],a[116:119],v[172:175]\n"
TR "a[200:201],v0 offset:32776\n"
MF "v[172:175],a[208:211],a[76:79],v[172:175]\n"
TR "a[202:203],v0 offset:33288\n"
MF "v[164:167],a[208:211],a[72:75],v[164:167]\n"
"ds_read_b128 a[144:147],v2 offset:2048\n"
MF "v[164:167],a[212:215],a[112:115],v[164:167]\n"
TR "a[204:205],v0 offset:36872\n"
MF "v[156:159],a[212:215],a[108:111],v[156:159]\n"
TR "a[206:207],v0 offset:37384\n"
MF "v[156:159],a[208:211],a[68:71],v[156:159]\n"
LD "v13,s[28:31],s82 offen offset:3072 lds\n"
MF "v[148:151],a[208:211],a[64:67],v[148:151]\n"
"ds_read_b128 a[184:187],v3 offset:2048\n"
MF "v[148:151],a[212:215],a[104:107],v[148:151]\n"
"s_add_u32 s14,s81,640\n"
"s_sub_u32 s15,s14,1280\n"
"s_min_u32 s14,s14,s15\n"
MF "v[140:143],a[212:215],a[100:103],v[140:143]\n"
"s_add_u32 s14,s14,s68\n"
"s_add_u32 s84,s14,0\n"
"s_add_u32 m0,s63,40960\n"
MF "v[140:143],a[208:211],a[60:63],v[140:143]\n"
"s_waitcnt lgkmcnt(7)\n"
LD "v4,s[24:27],s84 offen lds\n"
MF "v[176:179],a[216:219],a[60:63],v[176:179]\n"
TR "a[208:209],v1 offset:32768\n"
MF "v[176:179],a[220:223],a[100:103],v[176:179]\n"
TR "a[210:211],v1 offset:33280\n"
MF "v[184:187],a[220:223],a[104:107],v[184:187]\n"
"ds_read_b128 a[148:151],v2 offset:4096\n"
MF "v[184:187],a[216:219],a[64:67],v[184:187]\n"
TR "a[212:213],v1 offset:36864\n"
MF "v[192:195],a[216:219],a[68:71],v[192:195]\n"
"s_add_u32 s84,s14,9216\n"
TR "a[214:215],v1 offset:37376\n"
MF "v[192:195],a[220:223],a[108:111],v[192:195]\n"
LD "v5,s[24:27],s84 offen offset:1024 lds\n"
MF "v[200:203],a[220:223],a[112:115],v[200:203]\n"
"ds_read_b128 a[188:191],v3 offset:4096\n"
MF "v[200:203],a[216:219],a[72:75],v[200:203]\n"
"s_add_u32 m0,s62,16384\n"
MF "v[208:211],a[216:219],a[76:79],v[208:211]\n"
"s_add_u32 s82,s66,3145856\n"
"s_sub_u32 s15,s82,7864320\n"
"s_min_u32 s82,s82,s15\n"
MF "v[208:211],a[220:223],a[116:119],v[208:211]\n"
"s_waitcnt lgkmcnt(7)\n"
LD "v6,s[28:31],s82 offen lds\n"
MF "v[212:215],a[204:207],a[116:119],v[212:215]\n"
TR "a[216:217],v1 offset:32776\n"
MF "v[212:215],a[200:203],a[76:79],v[212:215]\n"
TR "a[218:219],v1 offset:33288\n"
MF "v[204:207],a[200:203],a[72:75],v[204:207]\n"
"ds_read_b128 a[152:155],v2 offset:6144\n"
MF "v[204:207],a[204:207],a[112:115],v[204:207]\n"
TR "a[220:221],v1 offset:36872\n"
MF "v[196:199],a[204:207],a[108:111],v[196:199]\n"
TR "a[222:223],v1 offset:37384\n"
MF "v[196:199],a[200:203],a[68:71],v[196:199]\n"
LD "v11,s[28:31],s82 offen offset:1024 lds\n"
MF "v[188:191],a[200:203],a[64:67],v[188:191]\n"
"v_add_u32 v0,0xc000,v0\n"
MF "v[188:191],a[204:207],a[104:107],v[188:191]\n"
"v_add_u32 v1,0xc000,v1\n"
MF "v[180:183],a[204:207],a[100:103],v[180:183]\n"
"s_waitcnt vmcnt(21) lgkmcnt(0)\n"
LD "v12,s[28:31],s82 offen offset:2048 lds\n"
MF "v[180:183],a[200:203],a[60:63],v[180:183]\n"
"s_barrier\n"
MF "v[216:219],a[208:211],a[60:63],v[216:219]\n"
TR "a[200:201],v0\n"
MF "v[216:219],a[212:215],a[100:103],v[216:219]\n"
TR "a[202:203],v0 offset:512\n"
MF "v[224:227],a[212:215],a[104:107],v[224:227]\n"
"ds_read_b128 a[192:195],v3 offset:6144\n"
MF "v[224:227],a[208:211],a[64:67],v[224:227]\n"
TR "a[204:205],v0 offset:4096\n"
MF "v[232:235],a[208:211],a[68:71],v[232:235]\n"
TR "a[206:207],v0 offset:4608\n"
MF "v[232:235],a[212:215],a[108:111],v[232:235]\n"
LD "v13,s[28:31],s82 offen offset:3072 lds\n"
MF "v[240:243],a[212:215],a[112:115],v[240:243]\n"
"ds_read_b128 a[156:159],v2 offset:8192\n"
MF "v[240:243],a[208:211],a[72:75],v[240:243]\n"
"s_add_u32 s84,s14,20480\n"
"ds_read_b128 a[196:199],v3 offset:8192\n"
MF "v[248:251],a[208:211],a[76:79],v[248:251]\n"
"s_add_u32 m0,s63,43008\n"
MF "v[248:251],a[212:215],a[116:119],v[248:251]\n"
LD "v4,s[24:27],s84 offen lds\n"
MF "v[252:255],a[220:223],a[116:119],v[252:255]\n"
TR "a[208:209],v0 offset:8\n"
MF "v[252:255],a[216:219],a[76:79],v[252:255]\n"
TR "a[210:211],v0 offset:520\n"
MF "v[244:247],a[216:219],a[72:75],v[244:247]\n"
MF "v[244:247],a[220:223],a[112:115],v[244:247]\n"
TR "a[212:213],v0 offset:4104\n"
MF "v[236:239],a[220:223],a[108:111],v[236:239]\n"
"s_add_u32 s84,s14,29696\n"
TR "a[214:215],v0 offset:4616\n"
MF "v[236:239],a[216:219],a[68:71],v[236:239]\n"
LD "v5,s[24:27],s84 offen offset:1024 lds\n"
MF "v[228:231],a[216:219],a[64:67],v[228:231]\n"
MF "v[228:231],a[220:223],a[104:107],v[228:231]\n"
"s_add_u32 m0,s62,32768\n"
MF "v[220:223],a[220:223],a[100:103],v[220:223]\n"
"s_add_u32 s82,s66,3145984\n"
"s_sub_u32 s15,s82,7864320\n"
"s_min_u32 s82,s82,s15\n"
MF "v[220:223],a[216:219],a[60:63],v[220:223]\n"
"s_waitcnt lgkmcnt(4)\n"
LD "v6,s[28:31],s82 offen lds\n"
MF "v[16:19],a[200:203],a[140:143],v[16:19]\n"
TR "a[216:217],v1\n"
MF "v[16:19],a[204:207],a[180:183],v[16:19]\n"
TR "a[218:219],v1 offset:512\n"
MF "v[24:27],a[204:207],a[184:187],v[24:27]\n"
MF "v[24:27],a[200:203],a[144:147],v[24:27]\n"
TR "a[220:221],v1 offset:4096\n"
MF "v[32:35],a[200:203],a[148:151],v[32:35]\n"
TR "a[222:223],v1 offset:4608\n"
MF "v[32:35],a[204:207],a[188:191],v[32:35]\n"
LD "v11,s[28:31],s82 offen offset:1024 lds\n"
MF "v[40:43],a[204:207],a[192:195],v[40:43]\n"
MF "v[40:43],a[200:203],a[152:155],v[40:43]\n"
MF "v[48:51],a[200:203],a[156:159],v[48:51]\n"
MF "v[48:51],a[204:207],a[196:199],v[48:51]\n"
"s_waitcnt lgkmcnt(4)\n"
LD "v12,s[28:31],s82 offen offset:2048 lds\n"
MF "v[52:55],a[212:215],a[196:199],v[52:55]\n"
TR "a[200:201],v1 offset:8\n"
MF "v[52:55],a[208:211],a[156:159],v[52:55]\n"
TR "a[202:203],v1 offset:520\n"
MF "v[44:47],a[208:211],a[152:155],v[44:47]\n"
MF "v[44:47],a[212:215],a[192:195],v[44:47]\n"
TR "a[204:205],v1 offset:4104\n"
MF "v[36:39],a[212:215],a[188:191],v[36:39]\n"
".Lpg2a:\n"
"s_cbranch_execz .Lisr1\n"
TR "a[206:207],v1 offset:4616\n"
MF "v[36:39],a[208:211],a[148:151],v[36:39]\n"
LD "v13,s[28:31],s82 offen offset:3072 lds\n"
MF "v[28:31],a[208:211],a[144:147],v[28:31]\n"
"s_add_u32 s84,s14,40960\n"
MF "v[28:31],a[212:215],a[184:187],v[28:31]\n"
"s_add_u32 m0,s63,45056\n"
MF "v[20:23],a[212:215],a[180:183],v[20:23]\n"
"s_waitcnt vmcnt(23) lgkmcnt(0)\n"
LD "v4,s[24:27],s84 offen lds\n"
MF "v[20:23],a[208:211],a[140:143],v[20:23]\n"
"s_barrier\n"
AW "a253,v10\n"
"v_add_u32 v10,1,v10\n"
"s_bfe_u32 s58,s22,0x30014\n"
"s_lshl_b32 s58,s58,2\n"
"s_add_u32 s58,s58,s56\n"
"s_lshl_b32 s58,s58,6\n"
"s_add_u32 s58,s58,s86\n"
"v_readlane_b32 s76,v10,s78\n"
"buffer_store_dword v10,v14,s[40:43],s58 offen offset:128 sc1\n"
".Lhs5:\n"
MF "v[56:59],a[216:219],a[140:143],v[56:59]\n"
"s_add_u32 m0,s62,49152\n"
"s_add_u32 s82,s66,3932160\n"
TR "a[208:209],v0 offset:16384\n"
MF "v[56:59],a[220:223],a[180:183],v[56:59]\n"
"s_sub_u32 s15,s82,7864320\n"
"s_min_u32 s82,s82,s15\n"
TR "a[210:211],v0 offset:16896\n"
MF "v[64:67],a[220:223],a[184:187],v[64:67]\n"
LD "v6,s[28:31],s82 offen lds\n"
MF "v[64:67],a[216:219],a[144:147],v[64:67]\n"
TR "a[212:213],v0 offset:20480\n"
MF "v[72:75],a[216:219],a[148:151],v[72:75]\n"
TR "a[214:215],v0 offset:20992\n"
MF "v[72:75],a[220:223],a[188:191],v[72:75]\n"
"buffer_load_dword a249,v15,s[48:51],s60 offen offset:1024\n"
MF "v[80:83],a[220:223],a[192:195],v[80:83]\n"
"s_bfe_u32 s59,s23,0x30003\n"
MF "v[80:83],a[216:219],a[152:155],v[80:83]\n"
LD "v11,s[28:31],s82 offen offset:1024 lds\n"
MF "v[88:91],a[216:219],a[156:159],v[88:91]\n"
"s_add_u32 s59,s59,1\n"
MF "v[88:91],a[220:223],a[196:199],v[88:91]\n"
"s_lshl_b32 s59,s59,7\n"
MF "v[92:95],a[204:207],a[196:199],v[92:95]\n"
"s_and_b32 s59,s59,768\n"
TR "a[216:217],v0 offset:16392\n"
MF "v[92:95],a[200:203],a[156:159],v[92:95]\n"
"s_add_u32 s59,s59,s60\n"
TR "a[218:219],v0 offset:16904\n"
MF "v[84:87],a[200:203],a[152:155],v[84:87]\n"
LD "v12,s[28:31],s82 offen offset:2048 lds\n"
MF "v[84:87],a[204:207],a[192:195],v[84:87]\n"
TR "a[220:221],v0 offset:20488\n"
MF "v[76:79],a[204:207],a[188:191],v[76:79]\n"
TR "a[222:223],v0 offset:21000\n"
MF "v[76:79],a[200:203],a[148:151],v[76:79]\n"
MF "v[68:71],a[200:203],a[144:147],v[68:71]\n"
MF "v[68:71],a[204:207],a[184:187],v[68:71]\n"
LD "v13,s[28:31],s82 offen offset:3072 lds\n"
MF "v[60:63],a[204:207],a[180:183],v[60:63]\n"
MF "v[60:63],a[200:203],a[140:143],v[60:63]\n"
"s_add_u32 s14,s81,768\n"
"s_sub_u32 s15,s14,1280\n"
"s_waitcnt lgkmcnt(4)\n"
MF "v[96:99],a[208:211],a[140:143],v[96:99]\n"
"s_min_u32 s14,s14,s15\n"
"s_add_u32 s14,s14,s68\n"
TR "a[200:201],v1 offset:16384\n"
MF "v[96:99],a[212:215],a[180:183],v[96:99]\n"
"s_add_u32 s84,s14,0\n"
"s_add_u32 m0,s63,0\n"
TR "a[202:203],v1 offset:16896\n"
MF "v[104:107],a[212:215],a[184:187],v[104:107]\n"
LD "v4,s[24:27],s84 offen lds\n"
MF "v[104:107],a[208:211],a[144:147],v[104:107]\n"
TR "a[204:205],v1 offset:20480\n"
MF "v[112:115],a[208:211],a[148:151],v[112:115]\n"
TR "a[206:207],v1 offset:20992\n"
MF "v[112:115],a[212:215],a[188:191],v[112:115]\n"
MF "v[120:123],a[212:215],a[192:195],v[120:123]\n"
"s_add_u32 s84,s14,9216\n"
MF "v[120:123],a[208:211],a[152:155],v[120:123]\n"
LD "v5,s[24:27],s84 offen offset:1024 lds\n"
MF "v[128:131],a[208:211],a[156:159],v[128:131]\n"
MF "v[128:131],a[212:215],a[196:199],v[128:131]\n"
"s_waitcnt lgkmcnt(4)\n"
MF "v[132:135],a[220:223],a[196:199],v[132:135]\n"
TR "a[208:209],v1 offset:16392\n"
MF "v[132:135],a[216:219],a[156:159],v[132:135]\n"
TR "a[210:211],v1 offset:16904\n"
MF "v[124:127],a[216:219],a[152:155],v[124:127]\n"
MF "v[124:127],a[220:223],a[192:195],v[124:127]\n"
TR "a[212:213],v1 offset:20488\n"
MF "v[116:119],a[220:223],a[188:191],v[116:119]\n"
TR "a[214:215],v1 offset:21000\n"
MF "v[116:119],a[216:219],a[148:151],v[116:119]\n"
MF "v[108:111],a[216:219],a[144:147],v[108:111]\n"
MF "v[108:111],a[220:223],a[184:187],v[108:111]\n"
MF "v[100:103],a[220:223],a[180:183],v[100:103]\n"
"s_waitcnt vmcnt(24) lgkmcnt(0)\n"
MF "v[100:103],a[216:219],a[140:143],v[100:103]\n"
"s_barrier\n"
MF "v[136:139],a[200:203],a[140:143],v[136:139]\n"
TR "a[216:217],v0 offset:32768\n"
MF "v[136:139],a[204:207],a[180:183],v[136:139]\n"
"s_add_u32 m0,s62,65536\n"
TR "a[218:219],v0 offset:33280\n"
MF "v[144:147],a[204:207],a[184:187],v[144:147]\n"
"s_add_u32 s82,s66,3932288\n"
"ds_read_b128 a[60:63],v2 offset:20480\n"
MF "v[144:147],a[200:203],a[144:147],v[144:147]\n"
"s_sub_u32 s15,s82,7864320\n"
TR "a[220:221],v0 offset:36864\n"
MF "v[152:155],a[200:203],a[148:151],v[152:155]\n"
"s_min_u32 s82,s82,s15\n"
TR "a[222:223],v0 offset:37376\n"
MF "v[152:155],a[204:207],a[188:191],v[152:155]\n"
LD "v6,s[28:31],s82 offen lds\n"
MF "v[160:163],a[204:207],a[192:195],v[160:163]\n"
"ds_read_b128 a[100:103],v3 offset:20480\n"
MF "v[160:163],a[200:203],a[152:155],v[160:163]\n"
MF "v[168:171],a[200:203],a[156:159],v[168:171]\n"
MF "v[168:171],a[204:207],a[196:199],v[168:171]\n"
LD "v11,s[28:31],s82 offen offset:1024 lds\n"
MF "v[172:175],a[212:215],a[196:199],v[172:175]\n"
TR "a[200:201],v0 offset:32776\n"
MF "v[172:175],a[208:211],a[156:159],v[172:175]\n"
TR "a[202:203],v0 offset:33288\n"
MF "v[164:167],a[208:211],a[152:155],v[164:167]\n"
"ds_read_b128 a[64:67],v2 offset:22528\n"
MF "v[164:167],a[212:215],a[192:195],v[164:167]\n"
TR "a[204:205],v0 offset:36872\n"
MF "v[156:159],a[212:215],a[188:191],v[156:159]\n"
TR "a[206:207],v0 offset:37384\n"
MF "v[156:159],a[208:211],a[148:151],v[156:159]\n"
LD "v12,s[28:31],s82 offen offset:2048 lds\n"
MF "v[148:151],a[208:211],a[144:147],v[148:151]\n"
"ds_read_b128 a[104:107],v3 offset:22528\n"
MF "v[148:151],a[212:215],a[184:187],v[148:151]\n"
MF "v[140:143],a[212:215],a[180:183],v[140:143]\n"
MF "v[140:143],a[208:211],a[140:143],v[140:143]\n"
"s_waitcnt lgkmcnt(7)\n"
LD "v13,s[28:31],s82 offen offset:3072 lds\n"
MF "v[176:179],a[216:219],a[140:143],v[176:179]\n"
TR "a[208:209],v1 offset:32768\n"
MF "v[176:179],a[220:223],a[180:183],v[176:179]\n"
TR "a[210:211],v1 offset:33280\n"
MF "v[184:187],a[220:223],a[184:187],v[184:187]\n"
"ds_read_b128 a[68:71],v2 offset:24576\n"
MF "v[184:187],a[216:219],a[144:147],v[184:187]\n"
"s_add_u32 s84,s14,20480\n"
TR "a[212:213],v1 offset:36864\n"
MF "v[192:195],a[216:219],a[148:151],v[192:195]\n"
"s_add_u32 m0,s63,2048\n"
TR "a[214:215],v1 offset:37376\n"
MF "v[192:195],a[220:223],a[188:191],v[192:195]\n"
LD "v4,s[24:27],s84 offen lds\n"
MF "v[200:203],a[220:223],a[192:195],v[200:203]\n"
"ds_read_b128 a[108:111],v3 offset:24576\n"
MF "v[200:203],a[216:219],a[152:155],v[200:203]\n"
MF "v[208:211],a[216:219],a[156:159],v[208:211]\n"
"s_add_u32 s84,s14,29696\n"
MF "v[208:211],a[220:223],a[196:199],v[208:211]\n"
"s_waitcnt lgkmcnt(7)\n"
LD "v5,s[24:27],s84 offen offset:1024 lds\n"
MF "v[212:215],a[204:207],a[196:199],v[212:215]\n"
TR "a[216:217],v1 offset:32776\n"
MF "v[212:215],a[200:203],a[156:159],v[212:215]\n"
TR "a[218:219],v1 offset:33288\n"
MF "v[204:207],a[200:203],a[152:155],v[204:207]\n"
"ds_read_b128 a[72:75],v2 offset:26624\n"
MF "v[204:207],a[204:207],a[192:195],v[204:207]\n"
TR "a[220:221],v1 offset:36872\n"
MF "v[196:199],a[204:207],a[188:191],v[196:199]\n"
TR "a[222:223],v1 offset:37384\n"
MF "v[196:199],a[200:203],a[148:151],v[196:199]\n"
MF "v[188:191],a[200:203],a[144:147],v[188:191]\n"
"v_add_u32 v0,0xffff4000,v0\n"
MF "v[188:191],a[204:207],a[184:187],v[188:191]\n"
"v_add_u32 v1,0xffff4000,v1\n"
MF "v[180:183],a[204:207],a[180:183],v[180:183]\n"
"s_waitcnt vmcnt(26) lgkmcnt(0)\n"
MF "v[180:183],a[200:203],a[140:143],v[180:183]\n"
"s_barrier\n"
MF "v[216:219],a[208:211],a[140:143],v[216:219]\n"
TR "a[200:201],v0\n"
MF "v[216:219],a[212:215],a[180:183],v[216:219]\n"
"s_add_u32 m0,s62,81920\n"
TR "a[202:203],v0 offset:512\n"
MF "v[224:227],a[212:215],a[184:187],v[224:227]\n"
"s_add_u32 s82,s66,3932416\n"
"ds_read_b128 a[112:115],v3 offset:26624\n"
MF "v[224:227],a[208:211],a[144:147],v[224:227]\n"
"s_sub_u32 s15,s82,7864320\n"
TR "a[204:205],v0 offset:4096\n"
MF "v[232:235],a[208:211],a[148:151],v[232:235]\n"
"s_min_u32 s82,s82,s15\n"
TR "a[206:207],v0 offset:4608\n"
MF "v[232:235],a[212:215],a[188:191],v[232:235]\n"
LD "v6,s[28:31],s82 offen lds\n"
MF "v[240:243],a[212:215],a[192:195],v[240:243]\n"
"ds_read_b128 a[76:79],v2 offset:28672\n"
MF "v[240:243],a[208:211],a[152:155],v[240:243]\n"
"ds_read_b128 a[116:119],v3 offset:28672\n"
MF "v[248:251],a[208:211],a[156:159],v[248:251]\n"
MF "v[248:251],a[212:215],a[196:199],v[248:251]\n"
LD "v11,s[28:31],s82 offen offset:1024 lds\n"
MF "v[252:255],a[220:223],a[196:199],v[252:255]\n"
TR "a[208:209],v0 offset:8\n"
MF "v[252:255],a[216:219],a[156:159],v[252:255]\n"
TR "a[210:211],v0 offset:520\n"
MF "v[244:247],a[216:219],a[152:155],v[244:247]\n"
MF "v[244:247],a[220:223],a[192:195],v[244:247]\n"
TR "a[212:213],v0 offset:4104\n"
MF "v[236:239],a[220:223],a[188:191],v[236:239]\n"
TR "a[214:215],v0 offset:4616\n"
MF "v[236:239],a[216:219],a[148:151],v[236:239]\n"
LD "v12,s[28:31],s82 offen offset:2048 lds\n"
MF "v[228:231],a[216:219],a[144:147],v[228:231]\n"
MF "v[228:231],a[220:223],a[184:187],v[228:231]\n"
MF "v[220:223],a[220:223],a[180:183],v[220:223]\n"
MF "v[220:223],a[216:219],a[140:143],v[220:223]\n"
"s_waitcnt lgkmcnt(4)\n"
LD "v13,s[28:31],s82 offen offset:3072 lds\n"
MF "v[16:19],a[200:203],a[60:63],v[16:19]\n"
TR "a[216:217],v1\n"
MF "v[16:19],a[204:207],a[100:103],v[16:19]\n"
TR "a[218:219],v1 offset:512\n"
MF "v[24:27],a[204:207],a[104:107],v[24:27]\n"
MF "v[24:27],a[200:203],a[64:67],v[24:27]\n"
"s_add_u32 s84,s14,40960\n"
TR "a[220:221],v1 offset:4096\n"
MF "v[32:35],a[200:203],a[68:71],v[32:35]\n"
"s_add_u32 m0,s63,4096\n"
TR "a[222:223],v1 offset:4608\n"
MF "v[32:35],a[204:207],a[108:111],v[32:35]\n"
LD "v4,s[24:27],s84 offen lds\n"
MF "v[40:43],a[204:207],a[112:115],v[40:43]\n"
MF "v[40:43],a[200:203],a[72:75],v[40:43]\n"
MF "v[48:51],a[200:203],a[76:79],v[48:51]\n"
MF "v[48:51],a[204:207],a[116:119],v[48:51]\n"
"s_waitcnt lgkmcnt(4)\n"
MF "v[52:55],a[212:215],a[116:119],v[52:55]\n"
TR "a[200:201],v1 offset:8\n"
MF "v[52:55],a[208:211],a[76:79],v[52:55]\n"
TR "a[202:203],v1 offset:520\n"
MF "v[44:47],a[208:211],a[72:75],v[44:47]\n"
MF "v[44:47],a[212:215],a[112:115],v[44:47]\n"
TR "a[204:205],v1 offset:4104\n"
MF "v[36:39],a[212:215],a[108:111],v[36:39]\n"
TR "a[206:207],v1 offset:4616\n"
MF "v[36:39],a[208:211],a[68:71],v[36:39]\n"
MF "v[28:31],a[208:211],a[64:67],v[28:31]\n"
MF "v[28:31],a[212:215],a[104:107],v[28:31]\n"
MF "v[20:23],a[212:215],a[100:103],v[20:23]\n"
"s_waitcnt vmcnt(25) lgkmcnt(0)\n"
MF "v[20:23],a[208:211],a[60:63],v[20:23]\n"
"s_barrier\n"
MF "v[56:59],a[216:219],a[60:63],v[56:59]\n"
"s_add_u32 m0,s62,0\n"
"s_add_u32 s82,s66,4718592\n"
TR "a[208:209],v0 offset:16384\n"
MF "v[56:59],a[220:223],a[100:103],v[56:59]\n"
"s_sub_u32 s15,s82,7864320\n"
"s_min_u32 s82,s82,s15\n"
TR "a[210:211],v0 offset:16896\n"
MF "v[64:67],a[220:223],a[104:107],v[64:67]\n"
LD "v6,s[28:31],s82 offen lds\n"
MF "v[64:67],a[216:219],a[64:67],v[64:67]\n"
TR "a[212:213],v0 offset:20480\n"
MF "v[72:75],a[216:219],a[68:71],v[72:75]\n"
TR "a[214:215],v0 offset:20992\n"
MF "v[72:75],a[220:223],a[108:111],v[72:75]\n"
"buffer_load_dword a249,v15,s[48:51],s59 offen\n"
MF "v[80:83],a[220:223],a[112:115],v[80:83]\n"
"s_bfe_u32 s59,s23,0x30003\n"
MF "v[80:83],a[216:219],a[72:75],v[80:83]\n"
LD "v11,s[28:31],s82 offen offset:1024 lds\n"
MF "v[88:91],a[216:219],a[76:79],v[88:91]\n"
"s_add_u32 s59,s59,3\n"
MF "v[88:91],a[220:223],a[116:119],v[88:91]\n"
"s_lshl_b32 s59,s59,7\n"
MF "v[92:95],a[204:207],a[116:119],v[92:95]\n"
"s_and_b32 s59,s59,768\n"
TR "a[216:217],v0 offset:16392\n"
MF "v[92:95],a[200:203],a[76:79],v[92:95]\n"
"s_add_u32 s59,s59,s60\n"
TR "a[218:219],v0 offset:16904\n"
MF "v[84:87],a[200:203],a[72:75],v[84:87]\n"
LD "v12,s[28:31],s82 offen offset:2048 lds\n"
MF "v[84:87],a[204:207],a[112:115],v[84:87]\n"
TR "a[220:221],v0 offset:20488\n"
MF "v[76:79],a[204:207],a[108:111],v[76:79]\n"
TR "a[222:223],v0 offset:21000\n"
MF "v[76:79],a[200:203],a[68:71],v[76:79]\n"
MF "v[68:71],a[200:203],a[64:67],v[68:71]\n"
MF "v[68:71],a[204:207],a[104:107],v[68:71]\n"
LD "v13,s[28:31],s82 offen offset:3072 lds\n"
MF "v[60:63],a[204:207],a[100:103],v[60:63]\n"
MF "v[60:63],a[200:203],a[60:63],v[60:63]\n"
"s_add_u32 s14,s81,896\n"
"s_sub_u32 s15,s14,1280\n"
"s_waitcnt lgkmcnt(4)\n"
MF "v[96:99],a[208:211],a[60:63],v[96:99]\n"
"s_min_u32 s14,s14,s15\n"
"s_add_u32 s14,s14,s68\n"
TR "a[200:201],v1 offset:16384\n"
MF "v[96:99],a[212:215],a[100:103],v[96:99]\n"
"s_add_u32 s84,s14,0\n"
"s_add_u32 m0,s63,20480\n"
TR "a[202:203],v1 offset:16896\n"
MF "v[104:107],a[212:215],a[104:107],v[104:107]\n"
LD "v4,s[24:27],s84 offen lds\n"
MF "v[104:107],a[208:211],a[64:67],v[104:107]\n"
TR "a[204:205],v1 offset:20480\n"
MF "v[112:115],a[208:211],a[68:71],v[112:115]\n"
TR "a[206:207],v1 offset:20992\n"
MF "v[112:115],a[212:215],a[108:111],v[112:115]\n"
MF "v[120:123],a[212:215],a[112:115],v[120:123]\n"
"s_add_u32 s84,s14,9216\n"
MF "v[120:123],a[208:211],a[72:75],v[120:123]\n"
LD "v5,s[24:27],s84 offen offset:1024 lds\n"
MF "v[128:131],a[208:211],a[76:79],v[128:131]\n"
MF "v[128:131],a[212:215],a[116:119],v[128:131]\n"
"s_waitcnt lgkmcnt(4)\n"
MF "v[132:135],a[220:223],a[116:119],v[132:135]\n"
TR "a[208:209],v1 offset:16392\n"
MF "v[132:135],a[216:219],a[76:79],v[132:135]\n"
TR "a[210:211],v1 offset:16904\n"
MF "v[124:127],a[216:219],a[72:75],v[124:127]\n"
MF "v[124:127],a[220:223],a[112:115],v[124:127]\n"
TR "a[212:213],v1 offset:20488\n"
MF "v[116:119],a[220:223],a[108:111],v[116:119]\n"
TR "a[214:215],v1 offset:21000\n"
MF "v[116:119],a[216:219],a[68:71],v[116:119]\n"
MF "v[108:111],a[216:219],a[64:67],v[108:111]\n"
MF "v[108:111],a[220:223],a[104:107],v[108:111]\n"
MF "v[100:103],a[220:223],a[100:103],v[100:103]\n"
"s_waitcnt vmcnt(25) lgkmcnt(0)\n"
MF "v[100:103],a[216:219],a[60:63],v[100:103]\n"
"s_barrier\n"
MF "v[136:139],a[200:203],a[60:63],v[136:139]\n"
TR "a[216:217],v0 offset:32768\n"
MF "v[136:139],a[204:207],a[100:103],v[136:139]\n"
"s_add_u32 m0,s62,16384\n"
TR "a[218:219],v0 offset:33280\n"
MF "v[144:147],a[204:207],a[104:107],v[144:147]\n"
"s_add_u32 s82,s66,4718720\n"
"ds_read_b128 a[140:143],v2 offset:40960\n"
MF "v[144:147],a[200:203],a[64:67],v[144:147]\n"
"s_sub_u32 s15,s82,7864320\n"
TR "a[220:221],v0 offset:36864\n"
MF "v[152:155],a[200:203],a[68:71],v[152:155]\n"
"s_min_u32 s82,s82,s15\n"
TR "a[222:223],v0 offset:37376\n"
MF "v[152:155],a[204:207],a[108:111],v[152:155]\n"
LD "v6,s[28:31],s82 offen lds\n"
MF "v[160:163],a[204:207],a[112:115],v[160:163]\n"
"ds_read_b128 a[180:183],v3 offset:40960\n"
MF "v[160:163],a[200:203],a[72:75],v[160:163]\n"
MF "v[168:171],a[200:203],a[76:79],v[168:171]\n"
MF "v[168:171],a[204:207],a[116:119],v[168:171]\n"
LD "v11,s[28:31],s82 offen offset:1024 lds\n"
MF "v[172:175],a[212:215],a[116:119],v[172:175]\n"
TR "a[200:201],v0 offset:32776\n"
MF "v[172:175],a[208:211],a[76:79],v[172:175]\n"
TR "a[202:203],v0 offset:33288\n"
MF "v[164:167],a[208:211],a[72:75],v[164:167]\n"
"ds_read_b128 a[144:147],v2 offset:43008\n"
MF "v[164:167],a[212:215],a[112:115],v[164:167]\n"
TR "a[204:205],v0 offset:36872\n"
MF "v[156:159],a[212:215],a[108:111],v[156:159]\n"
TR "a[206:207],v0 offset:37384\n"
MF "v[156:159],a[208:211],a[68:71],v[156:159]\n"
LD "v12,s[28:31],s82 offen offset:2048 lds\n"
MF "v[148:151],a[208:211],a[64:67],v[148:151]\n"
"ds_read_b128 a[184:187],v3 offset:43008\n"
MF "v[148:151],a[212:215],a[104:107],v[148:151]\n"
MF "v[140:143],a[212:215],a[100:103],v[140:143]\n"
MF "v[140:143],a[208:211],a[60:63],v[140:143]\n"
"s_waitcnt lgkmcnt(7)\n"
LD "v13,s[28:31],s82 offen offset:3072 lds\n"
MF "v[176:179],a[216:219],a[60:63],v[176:179]\n"
TR "a[208:209],v1 offset:32768\n"
MF "v[176:179],a[220:223],a[100:103],v[176:179]\n"
TR "a[210:211],v1 offset:33280\n"
MF "v[184:187],a[220:223],a[104:107],v[184:187]\n"
"ds_read_b128 a[148:151],v2 offset:45056\n"
MF "v[184:187],a[216:219],a[64:67],v[184:187]\n"
"s_add_u32 s84,s14,20480\n"
TR "a[212:213],v1 offset:36864\n"
MF "v[192:195],a[216:219],a[68:71],v[192:195]\n"
"s_add_u32 m0,s63,22528\n"
TR "a[214:215],v1 offset:37376\n"
MF "v[192:195],a[220:223],a[108:111],v[192:195]\n"
LD "v4,s[24:27],s84 offen lds\n"
MF "v[200:203],a[220:223],a[112:115],v[200:203]\n"
"ds_read_b128 a[188:191],v3 offset:45056\n"
MF "v[200:203],a[216:219],a[72:75],v[200:203]\n"
MF "v[208:211],a[216:219],a[76:79],v[208:211]\n"
"s_add_u32 s84,s14,29696\n"
MF "v[208:211],a[220:223],a[116:119],v[208:211]\n"
"s_waitcnt lgkmcnt(7)\n"
LD "v5,s[24:27],s84 offen offset:1024 lds\n"
MF "v[212:215],a[204:207],a[116:119],v[212:215]\n"
TR "a[216:217],v1 offset:32776\n"
MF "v[212:215],a[200:203],a[76:79],v[212:215]\n"
TR "a[218:219],v1 offset:33288\n"
MF "v[204:207],a[200:203],a[72:75],v[204:207]\n"
"ds_read_b128 a[152:155],v2 offset:47104\n"
MF "v[204:207],a[204:207],a[112:115],v[204:207]\n"
TR "a[220:221],v1 offset:36872\n"
MF "v[196:199],a[204:207],a[108:111],v[196:199]\n"
TR "a[222:223],v1 offset:37384\n"
MF "v[196:199],a[200:203],a[68:71],v[196:199]\n"
MF "v[188:191],a[200:203],a[64:67],v[188:191]\n"
"v_add_u32 v0,0xc000,v0\n"
MF "v[188:191],a[204:207],a[104:107],v[188:191]\n"
"v_add_u32 v1,0xc000,v1\n"
MF "v[180:183],a[204:207],a[100:103],v[180:183]\n"
"s_waitcnt vmcnt(26) lgkmcnt(0)\n"
MF "v[180:183],a[200:203],a[60:63],v[180:183]\n"
"s_barrier\n"
MF "v[216:219],a[208:211],a[60:63],v[216:219]\n"
TR "a[200:201],v0\n"
MF "v[216:219],a[212:215],a[100:103],v[216:219]\n"
"s_add_u32 m0,s62,32768\n"
TR "a[202:203],v0 offset:512\n"
MF "v[224:227],a[212:215],a[104:107],v[224:227]\n"
"s_add_u32 s82,s66,4718848\n"
"ds_read_b128 a[192:195],v3 offset:47104\n"
MF "v[224:227],a[208:211],a[64:67],v[224:227]\n"
"s_sub_u32 s15,s82,7864320\n"
TR "a[204:205],v0 offset:4096\n"
MF "v[232:235],a[208:211],a[68:71],v[232:235]\n"
"s_min_u32 s82,s82,s15\n"
TR "a[206:207],v0 offset:4608\n"
MF "v[232:235],a[212:215],a[108:111],v[232:235]\n"
LD "v6,s[28:31],s82 offen lds\n"
MF "v[240:243],a[212:215],a[112:115],v[240:243]\n"
"ds_read_b128 a[156:159],v2 offset:49152\n"
MF "v[240:243],a[208:211],a[72:75],v[240:243]\n"
"ds_read_b128 a[196:199],v3 offset:49152\n"
MF "v[248:251],a[208:211],a[76:79],v[248:251]\n"
MF "v[248:251],a[212:215],a[116:119],v[248:251]\n"
LD "v11,s[28:31],s82 offen offset:1024 lds\n"
MF "v[252:255],a[220:223],a[116:119],v[252:255]\n"
TR "a[208:209],v0 offset:8\n"
MF "v[252:255],a[216:219],a[76:79],v[252:255]\n"
TR "a[210:211],v0 offset:520\n"
MF "v[244:247],a[216:219],a[72:75],v[244:247]\n"
MF "v[244:247],a[220:223],a[112:115],v[244:247]\n"
TR "a[212:213],v0 offset:4104\n"
MF "v[236:239],a[220:223],a[108:111],v[236:239]\n"
TR "a[214:215],v0 offset:4616\n"
MF "v[236:239],a[216:219],a[68:71],v[236:239]\n"
LD "v12,s[28:31],s82 offen offset:2048 lds\n"
MF "v[228:231],a[216:219],a[64:67],v[228:231]\n"
MF "v[228:231],a[220:223],a[104:107],v[228:231]\n"
MF "v[220:223],a[220:223],a[100:103],v[220:223]\n"
MF "v[220:223],a[216:219],a[60:63],v[220:223]\n"
"s_waitcnt lgkmcnt(4)\n"
LD "v13,s[28:31],s82 offen offset:3072 lds\n"
MF "v[16:19],a[200:203],a[140:143],v[16:19]\n"
TR "a[216:217],v1\n"
MF "v[16:19],a[204:207],a[180:183],v[16:19]\n"
TR "a[218:219],v1 offset:512\n"
MF "v[24:27],a[204:207],a[184:187],v[24:27]\n"
MF "v[24:27],a[200:203],a[144:147],v[24:27]\n"
"s_add_u32 s84,s14,40960\n"
TR "a[220:221],v1 offset:4096\n"
MF "v[32:35],a[200:203],a[148:151],v[32:35]\n"
"s_add_u32 m0,s63,24576\n"
TR "a[222:223],v1 offset:4608\n"
MF "v[32:35],a[204:207],a[188:191],v[32:35]\n"
LD "v4,s[24:27],s84 offen lds\n"
MF "v[40:43],a[204:207],a[192:195],v[40:43]\n"
MF "v[40:43],a[200:203],a[152:155],v[40:43]\n"
MF "v[48:51],a[200:203],a[156:159],v[48:51]\n"
MF "v[48:51],a[204:207],a[196:199],v[48:51]\n"
"s_waitcnt lgkmcnt(4)\n"
MF "v[52:55],a[212:215],a[196:199],v[52:55]\n"
TR "a[200:201],v1 offset:8\n"
MF "v[52:55],a[208:211],a[156:159],v[52:55]\n"
TR "a[202:203],v1 offset:520\n"
MF "v[44:47],a[208:211],a[152:155],v[44:47]\n"
MF "v[44:47],a[212:215],a[192:195],v[44:47]\n"
TR "a[204:205],v1 offset:4104\n"
MF "v[36:39],a[212:215],a[188:191],v[36:39]\n"
TR "a[206:207],v1 offset:4616\n"
MF "v[36:39],a[208:211],a[148:151],v[36:39]\n"
MF "v[28:31],a[208:211],a[144:147],v[28:31]\n"
MF "v[28:31],a[212:215],a[184:187],v[28:31]\n"
MF "v[20:23],a[212:215],a[180:183],v[20:23]\n"
"s_waitcnt vmcnt(25) lgkmcnt(0)\n"
MF "v[20:23],a[208:211],a[140:143],v[20:23]\n"
"s_barrier\n"
MF "v[56:59],a[216:219],a[140:143],v[56:59]\n"
"s_add_u32 m0,s62,49152\n"
"s_add_u32 s82,s66,5505024\n"
TR "a[208:209],v0 offset:16384\n"
MF "v[56:59],a[220:223],a[180:183],v[56:59]\n"
"s_sub_u32 s15,s82,7864320\n"
"s_min_u32 s82,s82,s15\n"
TR "a[210:211],v0 offset:16896\n"
MF "v[64:67],a[220:223],a[184:187],v[64:67]\n"
LD "v6,s[28:31],s82 offen lds\n"
MF "v[64:67],a[216:219],a[144:147],v[64:67]\n"
TR "a[212:213],v0 offset:20480\n"
MF "v[72:75],a[216:219],a[148:151],v[72:75]\n"
TR "a[214:215],v0 offset:20992\n"
MF "v[72:75],a[220:223],a[188:191],v[72:75]\n"
"buffer_load_dword a249,v15,s[48:51],s59 offen\n"
MF "v[80:83],a[220:223],a[192:195],v[80:83]\n"
"s_bfe_u32 s59,s23,0x30003\n"
MF "v[80:83],a[216:219],a[152:155],v[80:83]\n"
LD "v11,s[28:31],s82 offen offset:1024 lds\n"
MF "v[88:91],a[216:219],a[156:159],v[88:91]\n"
"s_add_u32 s59,s59,5\n"
MF "v[88:91],a[220:223],a[196:199],v[88:91]\n"
"s_lshl_b32 s59,s59,7\n"
MF "v[92:95],a[204:207],a[196:199],v[92:95]\n"
"s_and_b32 s59,s59,768\n"
TR "a[216:217],v0 offset:16392\n"
MF "v[92:95],a[200:203],a[156:159],v[92:95]\n"
"s_add_u32 s59,s59,s60\n"
TR "a[218:219],v0 offset:16904\n"
MF "v[84:87],a[200:203],a[152:155],v[84:87]\n"
LD "v12,s[28:31],s82 offen offset:2048 lds\n"
MF "v[84:87],a[204:207],a[192:195],v[84:87]\n"
TR "a[220:221],v0 offset:20488\n"
MF "v[76:79],a[204:207],a[188:191],v[76:79]\n"
TR "a[222:223],v0 offset:21000\n"
MF "v[76:79],a[200:203],a[148:151],v[76:79]\n"
MF "v[68:71],a[200:203],a[144:147],v[68:71]\n"
MF "v[68:71],a[204:207],a[184:187],v[68:71]\n"
".Lpg3a:\n"
"s_cbranch_execz .Lisr1\n"
LD "v13,s[28:31],s82 offen offset:3072 lds\n"
MF "v[60:63],a[204:207],a[180:183],v[60:63]\n"
MF "v[60:63],a[200:203],a[140:143],v[60:63]\n"
"s_add_u32 s14,s81,1024\n"
"s_sub_u32 s15,s14,1280\n"
"s_waitcnt lgkmcnt(4)\n"
MF "v[96:99],a[208:211],a[140:143],v[96:99]\n"
"s_min_u32 s14,s14,s15\n"
"s_add_u32 s14,s14,s68\n"
TR "a[200:201],v1 offset:16384\n"
MF "v[96:99],a[212:215],a[180:183],v[96:99]\n"
"s_add_u32 s84,s14,0\n"
"s_add_u32 m0,s63,40960\n"
TR "a[202:203],v1 offset:16896\n"
MF "v[104:107],a[212:215],a[184:187],v[104:107]\n"
LD "v4,s[24:27],s84 offen lds\n"
MF "v[104:107],a[208:211],a[144:147],v[104:107]\n"
TR "a[204:205],v1 offset:20480\n"
MF "v[112:115],a[208:211],a[148:151],v[112:115]\n"
TR "a[206:207],v1 offset:20992\n"
MF "v[112:115],a[212:215],a[188:191],v[112:115]\n"
MF "v[120:123],a[212:215],a[192:195],v[120:123]\n"
"s_add_u32 s84,s14,9216\n"
MF "v[120:123],a[208:211],a[152:155],v[120:123]\n"
LD "v5,s[24:27],s84 offen offset:1024 lds\n"
MF "v[128:131],a[208:211],a[156:159],v[128:131]\n"
MF "v[128:131],a[212:215],a[196:199],v[128:131]\n"
"s_waitcnt lgkmcnt(4)\n"
MF "v[132:135],a[220:223],a[196:199],v[132:135]\n"
TR "a[208:209],v1 offset:16392\n"
MF "v[132:135],a[216:219],a[156:159],v[132:135]\n"
TR "a[210:211],v1 offset:16904\n"
MF "v[124:127],a[216:219],a[152:155],v[124:127]\n"
MF "v[124:127],a[220:223],a[192:195],v[124:127]\n"
TR "a[212:213],v1 offset:20488\n"
MF "v[116:119],a[220:223],a[188:191],v[116:119]\n"
TR "a[214:215],v1 offset:21000\n"
MF "v[116:119],a[216:219],a[148:151],v[116:119]\n"
MF "v[108:111],a[216:219],a[144:147],v[108:111]\n"
MF "v[108:111],a[220:223],a[184:187],v[108:111]\n"
MF "v[100:103],a[220:223],a[180:183],v[100:103]\n"
"s_waitcnt vmcnt(25) lgkmcnt(0)\n"
MF "v[100:103],a[216:219],a[140:143],v[100:103]\n"
"s_barrier\n"
MF "v[136:139],a[200:203],a[140:143],v[136:139]\n"
TR "a[216:217],v0 offset:32768\n"
MF "v[136:139],a[204:207],a[180:183],v[136:139]\n"
"s_add_u32 m0,s62,65536\n"
TR "a[218:219],v0 offset:33280\n"
MF "v[144:147],a[204:207],a[184:187],v[144:147]\n"
"s_add_u32 s82,s66,5505152\n"
"ds_read_b128 a[60:63],v2\n"
MF "v[144:147],a[200:203],a[144:147],v[144:147]\n"
"s_sub_u32 s15,s82,7864320\n"
TR "a[220:221],v0 offset:36864\n"
MF "v[152:155],a[200:203],a[148:151],v[152:155]\n"
"s_min_u32 s82,s82,s15\n"
TR "a[222:223],v0 offset:37376\n"
MF "v[152:155],a[204:207],a[188:191],v[152:155]\n"
LD "v6,s[28:31],s82 offen lds\n"
MF "v[160:163],a[204:207],a[192:195],v[160:163]\n"
"ds_read_b128 a[100:103],v3\n"
MF "v[160:163],a[200:203],a[152:155],v[160:163]\n"
MF "v[168:171],a[200:203],a[156:159],v[168:171]\n"
MF "v[168:171],a[204:207],a[196:199],v[168:171]\n"
LD "v11,s[28:31],s82 offen offset:1024 lds\n"
MF "v[172:175],a[212:215],a[196:199],v[172:175]\n"
TR "a[200:201],v0 offset:32776\n"
MF "v[172:175],a[208:211],a[156:159],v[172:175]\n"
TR "a[202:203],v0 offset:33288\n"
MF "v[164:167],a[208:211],a[152:155],v[164:167]\n"
"ds_read_b128 a[64:67],v2 offset:2048\n"
MF "v[164:167],a[212:215],a[192:195],v[164:167]\n"
TR "a[204:205],v0 offset:36872\n"
MF "v[156:159],a[212:215],a[188:191],v[156:159]\n"
TR "a[206:207],v0 offset:37384\n"
MF "v[156:159],a[208:211],a[148:151],v[156:159]\n"
LD "v12,s[28:31],s82 offen offset:2048 lds\n"
MF "v[148:151],a[208:211],a[144:147],v[148:151]\n"
"ds_read_b128 a[104:107],v3 offset:2048\n"
MF "v[148:151],a[212:215],a[184:187],v[148:151]\n"
MF "v[140:143],a[212:215],a[180:183],v[140:143]\n"
MF "v[140:143],a[208:211],a[140:143],v[140:143]\n"
"s_waitcnt lgkmcnt(7)\n"
LD "v13,s[28:31],s82 offen offset:3072 lds\n"
MF "v[176:179],a[216:219],a[140:143],v[176:179]\n"
TR "a[208:209],v1 offset:32768\n"
MF "v[176:179],a[220:223],a[180:183],v[176:179]\n"
TR "a[210:211],v1 offset:33280\n"
MF "v[184:187],a[220:223],a[184:187],v[184:187]\n"
"ds_read_b128 a[68:71],v2 offset:4096\n"
MF "v[184:187],a[216:219],a[144:147],v[184:187]\n"
"s_add_u32 s84,s14,20480\n"
TR "a[212:213],v1 offset:36864\n"
MF "v[192:195],a[216:219],a[148:151],v[192:195]\n"
"s_add_u32 m0,s63,43008\n"
TR "a[214:215],v1 offset:37376\n"
MF "v[192:195],a[220:223],a[188:191],v[192:195]\n"
LD "v4,s[24:27],s84 offen lds\n"
MF "v[200:203],a[220:223],a[192:195],v[200:203]\n"
"ds_read_b128 a[108:111],v3 offset:4096\n"
MF "v[200:203],a[216:219],a[152:155],v[200:203]\n"
MF "v[208:211],a[216:219],a[156:159],v[208:211]\n"
"s_add_u32 s84,s14,29696\n"
MF "v[208:211],a[220:223],a[196:199],v[208:211]\n"
"s_waitcnt lgkmcnt(7)\n"
LD "v5,s[24:27],s84 offen offset:1024 lds\n"
MF "v[212:215],a[204:207],a[196:199],v[212:215]\n"
TR "a[216:217],v1 offset:32776\n"
MF "v[212:215],a[200:203],a[156:159],v[212:215]\n"
TR "a[218:219],v1 offset:33288\n"
MF "v[204:207],a[200:203],a[152:155],v[204:207]\n"
"ds_read_b128 a[72:75],v2 offset:6144\n"
MF "v[204:207],a[204:207],a[192:195],v[204:207]\n"
TR "a[220:221],v1 offset:36872\n"
MF "v[196:199],a[204:207],a[188:191],v[196:199]\n"
TR "a[222:223],v1 offset:37384\n"
MF "v[196:199],a[200:203],a[148:151],v[196:199]\n"
MF "v[188:191],a[200:203],a[144:147],v[188:191]\n"
"v_add_u32 v0,0xffff4000,v0\n"
MF "v[188:191],a[204:207],a[184:187],v[188:191]\n"
"v_add_u32 v1,0xffff4000,v1\n"
MF "v[180:183],a[204:207],a[180:183],v[180:183]\n"
"s_waitcnt vmcnt(26) lgkmcnt(0)\n"
MF "v[180:183],a[200:203],a[140:143],v[180:183]\n"
"s_barrier\n"
MF "v[216:219],a[208:211],a[140:143],v[216:219]\n"
TR "a[200:201],v0\n"
MF "v[216:219],a[212:215],a[180:183],v[216:219]\n"
"s_add_u32 m0,s62,81920\n"
TR "a[202:203],v0 offset:512\n"
MF "v[224:227],a[212:215],a[184:187],v[224:227]\n"
"s_add_u32 s82,s66,5505280\n"
"ds_read_b128 a[112:115],v3 offset:6144\n"
MF "v[224:227],a[208:211],a[144:147],v[224:227]\n"
"s_sub_u32 s15,s82,7864320\n"
TR "a[204:205],v0 offset:4096\n"
MF "v[232:235],a[208:211],a[148:151],v[232:235]\n"
"s_min_u32 s82,s82,s15\n"
TR "a[206:207],v0 offset:4608\n"
MF "v[232:235],a[212:215],a[188:191],v[232:235]\n"
LD "v6,s[28:31],s82 offen lds\n"
MF "v[240:243],a[212:215],a[192:195],v[240:243]\n"
"ds_read_b128 a[76:79],v2 offset:8192\n"
MF "v[240:243],a[208:211],a[152:155],v[240:243]\n"
"ds_read_b128 a[116:119],v3 offset:8192\n"
MF "v[248:251],a[208:211],a[156:159],v[248:251]\n"
MF "v[248:251],a[212:215],a[196:199],v[248:251]\n"
LD "v11,s[28:31],s82 offen offset:1024 lds\n"
MF "v[252:255],a[220:223],a[196:199],v[252:255]\n"
TR "a[208:209],v0 offset:8\n"
MF "v[252:255],a[216:219],a[156:159],v[252:255]\n"
TR "a[210:211],v0 offset:520\n"
MF "v[244:247],a[216:219],a[152:155],v[244:247]\n"
MF "v[244:247],a[220:223],a[192:195],v[244:247]\n"
TR "a[212:213],v0 offset:4104\n"
MF "v[236:239],a[220:223],a[188:191],v[236:239]\n"
TR "a[214:215],v0 offset:4616\n"
MF "v[236:239],a[216:219],a[148:151],v[236:239]\n"
LD "v12,s[28:31],s82 offen offset:2048 lds\n"
MF "v[228:231],a[216:219],a[144:147],v[228:231]\n"
MF "v[228:231],a[220:223],a[184:187],v[228:231]\n"
MF "v[220:223],a[220:223],a[180:183],v[220:223]\n"
MF "v[220:223],a[216:219],a[140:143],v[220:223]\n"
"s_waitcnt lgkmcnt(4)\n"
LD "v13,s[28:31],s82 offen offset:3072 lds\n"
MF "v[16:19],a[200:203],a[60:63],v[16:19]\n"
TR "a[216:217],v1\n"
MF "v[16:19],a[204:207],a[100:103],v[16:19]\n"
TR "a[218:219],v1 offset:512\n"
MF "v[24:27],a[204:207],a[104:107],v[24:27]\n"
MF "v[24:27],a[200:203],a[64:67],v[24:27]\n"
"s_add_u32 s84,s14,40960\n"
TR "a[220:221],v1 offset:4096\n"
MF "v[32:35],a[200:203],a[68:71],v[32:35]\n"
"s_add_u32 m0,s63,45056\n"
TR "a[222:223],v1 offset:4608\n"
MF "v[32:35],a[204:207],a[108:111],v[32:35]\n"
LD "v4,s[24:27],s84 offen lds\n"
MF "v[40:43],a[204:207],a[112:115],v[40:43]\n"
MF "v[40:43],a[200:203],a[72:75],v[40:43]\n"
MF "v[48:51],a[200:203],a[76:79],v[48:51]\n"
MF "v[48:51],a[204:207],a[116:119],v[48:51]\n"
"s_waitcnt lgkmcnt(4)\n"
MF "v[52:55],a[212:215],a[116:119],v[52:55]\n"
TR "a[200:201],v1 offset:8\n"
MF "v[52:55],a[208:211],a[76:79],v[52:55]\n"
TR "a[202:203],v1 offset:520\n"
MF "v[44:47],a[208:211],a[72:75],v[44:47]\n"
MF "v[44:47],a[212:215],a[112:115],v[44:47]\n"
TR "a[204:205],v1 offset:4104\n"
MF "v[36:39],a[212:215],a[108:111],v[36:39]\n"
TR "a[206:207],v1 offset:4616\n"
MF "v[36:39],a[208:211],a[68:71],v[36:39]\n"
MF "v[28:31],a[208:211],a[64:67],v[28:31]\n"
MF "v[28:31],a[212:215],a[104:107],v[28:31]\n"
MF "v[20:23],a[212:215],a[100:103],v[20:23]\n"
"s_waitcnt vmcnt(25) lgkmcnt(0)\n"
MF "v[20:23],a[208:211],a[60:63],v[20:23]\n"
"s_barrier\n"
MF "v[56:59],a[216:219],a[60:63],v[56:59]\n"
"s_add_u32 m0,s62,0\n"
"s_add_u32 s82,s66,6291712\n"
TR "a[208:209],v0 offset:16384\n"
MF "v[56:59],a[220:223],a[100:103],v[56:59]\n"
"s_sub_u32 s15,s82,7864320\n"
"s_min_u32 s82,s82,s15\n"
TR "a[210:211],v0 offset:16896\n"
MF "v[64:67],a[220:223],a[104:107],v[64:67]\n"
LD "v6,s[28:31],s82 offen lds\n"
MF "v[64:67],a[216:219],a[64:67],v[64:67]\n"
TR "a[212:213],v0 offset:20480\n"
MF "v[72:75],a[216:219],a[68:71],v[72:75]\n"
TR "a[214:215],v0 offset:20992\n"
MF "v[72:75],a[220:223],a[108:111],v[72:75]\n"
"buffer_load_dword a249,v15,s[48:51],s59 offen\n"
MF "v[80:83],a[220:223],a[112:115],v[80:83]\n"
MF "v[80:83],a[216:219],a[72:75],v[80:83]\n"
LD "v11,s[28:31],s82 offen offset:1024 lds\n"
MF "v[88:91],a[216:219],a[76:79],v[88:91]\n"
MF "v[88:91],a[220:223],a[116:119],v[88:91]\n"
MF "v[92:95],a[204:207],a[116:119],v[92:95]\n"
TR "a[216:217],v0 offset:16392\n"
MF "v[92:95],a[200:203],a[76:79],v[92:95]\n"
TR "a[218:219],v0 offset:16904\n"
MF "v[84:87],a[200:203],a[72:75],v[84:87]\n"
LD "v12,s[28:31],s82 offen offset:2048 lds\n"
MF "v[84:87],a[204:207],a[112:115],v[84:87]\n"
TR "a[220:221],v0 offset:20488\n"
MF "v[76:79],a[204:207],a[108:111],v[76:79]\n"
TR "a[222:223],v0 offset:21000\n"
MF "v[76:79],a[200:203],a[68:71],v[76:79]\n"
MF "v[68:71],a[200:203],a[64:67],v[68:71]\n"
MF "v[68:71],a[204:207],a[104:107],v[68:71]\n"
LD "v13,s[28:31],s82 offen offset:3072 lds\n"
MF "v[60:63],a[204:207],a[100:103],v[60:63]\n"
MF "v[60:63],a[200:203],a[60:63],v[60:63]\n"
"s_add_u32 s14,s81,1152\n"
"s_sub_u32 s15,s14,1280\n"
"s_waitcnt lgkmcnt(4)\n"
MF "v[96:99],a[208:211],a[60:63],v[96:99]\n"
"s_min_u32 s14,s14,s15\n"
"s_add_u32 s14,s14,s68\n"
TR "a[200:201],v1 offset:16384\n"
MF "v[96:99],a[212:215],a[100:103],v[96:99]\n"
"s_add_u32 s84,s14,0\n"
"s_add_u32 m0,s63,0\n"
TR "a[202:203],v1 offset:16896\n"
MF "v[104:107],a[212:215],a[104:107],v[104:107]\n"
LD "v4,s[24:27],s84 offen lds\n"
MF "v[104:107],a[208:211],a[64:67],v[104:107]\n"
TR "a[204:205],v1 offset:20480\n"
MF "v[112:115],a[208:211],a[68:71],v[112:115]\n"
TR "a[206:207],v1 offset:20992\n"
MF "v[112:115],a[212:215],a[108:111],v[112:115]\n"
MF "v[120:123],a[212:215],a[112:115],v[120:123]\n"
"s_add_u32 s84,s14,9216\n"
MF "v[120:123],a[208:211],a[72:75],v[120:123]\n"
LD "v5,s[24:27],s84 offen offset:1024 lds\n"
MF "v[128:131],a[208:211],a[76:79],v[128:131]\n"
MF "v[128:131],a[212:215],a[116:119],v[128:131]\n"
"s_waitcnt lgkmcnt(4)\n"
MF "v[132:135],a[220:223],a[116:119],v[132:135]\n"
TR "a[208:209],v1 offset:16392\n"
MF "v[132:135],a[216:219],a[76:79],v[132:135]\n"
"s_add_u32 s84,s14,18432\n"
TR "a[210:211],v1 offset:16904\n"
MF "v[124:127],a[216:219],a[72:75],v[124:127]\n"
LD "v4,s[24:27],s84 offen offset:2048 lds\n"
MF "v[124:127],a[220:223],a[112:115],v[124:127]\n"
TR "a[212:213],v1 offset:20488\n"
MF "v[116:119],a[220:223],a[108:111],v[116:119]\n"
TR "a[214:215],v1 offset:21000\n"
MF "v[116:119],a[216:219],a[68:71],v[116:119]\n"
MF "v[108:111],a[216:219],a[64:67],v[108:111]\n"
MF "v[108:111],a[220:223],a[104:107],v[108:111]\n"
MF "v[100:103],a[220:223],a[100:103],v[100:103]\n"
"s_waitcnt vmcnt(26) lgkmcnt(0)\n"
MF "v[100:103],a[216:219],a[60:63],v[100:103]\n"
"s_barrier\n"
MF "v[136:139],a[200:203],a[60:63],v[136:139]\n"
TR "a[216:217],v0 offset:32768\n"
MF "v[136:139],a[204:207],a[100:103],v[136:139]\n"
"s_add_u32 m0,s62,16384\n"
TR "a[218:219],v0 offset:33280\n"
MF "v[144:147],a[204:207],a[104:107],v[144:147]\n"
"s_add_u32 s82,s66,7078144\n"
"ds_read_b128 a[140:143],v2 offset:20480\n"
MF "v[144:147],a[200:203],a[64:67],v[144:147]\n"
"s_sub_u32 s15,s82,7864320\n"
TR "a[220:221],v0 offset:36864\n"
MF "v[152:155],a[200:203],a[68:71],v[152:155]\n"
"s_min_u32 s82,s82,s15\n"
TR "a[222:223],v0 offset:37376\n"
MF "v[152:155],a[204:207],a[108:111],v[152:155]\n"
LD "v6,s[28:31],s82 offen lds\n"
MF "v[160:163],a[204:207],a[112:115],v[160:163]\n"
"ds_read_b128 a[180:183],v3 offset:20480\n"
MF "v[160:163],a[200:203],a[72:75],v[160:163]\n"
MF "v[168:171],a[200:203],a[76:79],v[168:171]\n"
MF "v[168:171],a[204:207],a[116:119],v[168:171]\n"
LD "v11,s[28:31],s82 offen offset:1024 lds\n"
MF "v[172:175],a[212:215],a[116:119],v[172:175]\n"
TR "a[200:201],v0 offset:32776\n"
MF "v[172:175],a[208:211],a[76:79],v[172:175]\n"
TR "a[202:203],v0 offset:33288\n"
MF "v[164:167],a[208:211],a[72:75],v[164:167]\n"
"ds_read_b128 a[144:147],v2 offset:22528\n"
MF "v[164:167],a[212:215],a[112:115],v[164:167]\n"
TR "a[204:205],v0 offset:36872\n"
MF "v[156:159],a[212:215],a[108:111],v[156:159]\n"
TR "a[206:207],v0 offset:37384\n"
MF "v[156:159],a[208:211],a[68:71],v[156:159]\n"
LD "v12,s[28:31],s82 offen offset:2048 lds\n"
MF "v[148:151],a[208:211],a[64:67],v[148:151]\n"
"ds_read_b128 a[184:187],v3 offset:22528\n"
MF "v[148:151],a[212:215],a[104:107],v[148:151]\n"
MF "v[140:143],a[212:215],a[100:103],v[140:143]\n"
MF "v[140:143],a[208:211],a[60:63],v[140:143]\n"
"s_waitcnt lgkmcnt(7)\n"
LD "v13,s[28:31],s82 offen offset:3072 lds\n"
MF "v[176:179],a[216:219],a[60:63],v[176:179]\n"
TR "a[208:209],v1 offset:32768\n"
MF "v[176:179],a[220:223],a[100:103],v[176:179]\n"
TR "a[210:211],v1 offset:33280\n"
MF "v[184:187],a[220:223],a[104:107],v[184:187]\n"
"ds_read_b128 a[148:151],v2 offset:24576\n"
MF "v[184:187],a[216:219],a[64:67],v[184:187]\n"
"s_add_u32 s84,s14,30720\n"
TR "a[212:213],v1 offset:36864\n"
MF "v[192:195],a[216:219],a[68:71],v[192:195]\n"
"s_add_u32 m0,s63,3072\n"
TR "a[214:215],v1 offset:37376\n"
MF "v[192:195],a[220:223],a[108:111],v[192:195]\n"
LD "v5,s[24:27],s84 offen lds\n"
MF "v[200:203],a[220:223],a[112:115],v[200:203]\n"
"ds_read_b128 a[188:191],v3 offset:24576\n"
MF "v[200:203],a[216:219],a[72:75],v[200:203]\n"
MF "v[208:211],a[216:219],a[76:79],v[208:211]\n"
"s_add_u32 s84,s14,39936\n"
MF "v[208:211],a[220:223],a[116:119],v[208:211]\n"
"s_waitcnt lgkmcnt(7)\n"
LD "v4,s[24:27],s84 offen offset:1024 lds\n"
MF "v[212:215],a[204:207],a[116:119],v[212:215]\n"
TR "a[216:217],v1 offset:32776\n"
MF "v[212:215],a[200:203],a[76:79],v[212:215]\n"
TR "a[218:219],v1 offset:33288\n"
MF "v[204:207],a[200:203],a[72:75],v[204:207]\n"
"ds_read_b128 a[152:155],v2 offset:26624\n"
MF "v[204:207],a[204:207],a[112:115],v[204:207]\n"
TR "a[220:221],v1 offset:36872\n"
MF "v[196:199],a[204:207],a[108:111],v[196:199]\n"
TR "a[222:223],v1 offset:37384\n"
MF "v[196:199],a[200:203],a[68:71],v[196:199]\n"
MF "v[188:191],a[200:203],a[64:67],v[188:191]\n"
"v_add_u32 v0,0xc000,v0\n"
MF "v[188:191],a[204:207],a[104:107],v[188:191]\n"
"v_add_u32 v1,0xc000,v1\n"
MF "v[180:183],a[204:207],a[100:103],v[180:183]\n"
"s_waitcnt vmcnt(27) lgkmcnt(0)\n"
"s_branch .Lqj\n"
".Lpgq0_c:\n"
"s_cbranch_execz .Lpgqy_c\n"
".Lpgq0_f:\n"
"s_cbranch_execz .Lpg12f\n"
".Lq0:\n"
MF "v[16:19],a[200:203],a[60:63],0\n"
"s_add_u32 m0,s62,49152\n"
"s_add_u32 s82,s66,786432\n"
TR "a[216:217],v1\n"
MF "v[16:19],a[204:207],a[100:103],v[16:19]\n"
"s_sub_u32 s15,s82,7864320\n"
"s_min_u32 s82,s82,s15\n"
TR "a[218:219],v1 offset:512\n"
MF "v[24:27],a[204:207],a[104:107],0\n"
LD "v6,s[28:31],s82 offen lds\n"
MF "v[24:27],a[200:203],a[64:67],v[24:27]\n"
TR "a[220:221],v1 offset:4096\n"
AW "a88,v136\n"
MF "v[32:35],a[200:203],a[68:71],0\n"
TR "a[222:223],v1 offset:4608\n"
AW "a89,v137\n"
MF "v[32:35],a[204:207],a[108:111],v[32:35]\n"
"v_max3_f32 v220,|v16|,|v17|,|v18|\n"
"v_max_f32_e64 v220,v220,|v19|\n"
MF "v[40:43],a[204:207],a[112:115],0\n"
"v_readlane_b32 s12,v220,3\n"
"v_readlane_b32 s6,v220,22\n"
MF "v[40:43],a[200:203],a[72:75],v[40:43]\n"
"s_min_u32 s12,s12,s6\n"
"v_readlane_b32 s7,v220,41\n"
"v_readlane_b32 s8,v220,60\n"
MF "v[48:51],a[200:203],a[76:79],0\n"
"s_min_u32 s7,s7,s8\n"
AW "a90,v138\n"
AW "a91,v139\n"
MF "v[48:51],a[204:207],a[116:119],v[48:51]\n"
LD "v11,s[28:31],s82 offen offset:1024 lds\n"
MF "v[52:55],a[212:215],a[116:119],0\n"
"s_min_u32 s12,s12,s7\n"
TR "a[200:201],v1 offset:8\n"
AW "a96,v144\n"
MF "v[52:55],a[208:211],a[76:79],v[52:55]\n"
"s_and_b32 s12,s12,0x7f800000\n"
TR "a[202:203],v1 offset:520\n"
AW "a97,v145\n"
MF "v[44:47],a[208:211],a[72:75],0\n"
"s_sub_u32 s12,s12,0x14000000\n"
AW "a98,v146\n"
AW "a99,v147\n"
MF "v[44:47],a[212:215],a[112:115],v[44:47]\n"
"s_cmp_lt_u32 s12,0x50000000\n"
"s_cselect_b32 s12,s12,0xe8400000\n"
TR "a[204:205],v1 offset:4104\n"
MF "v[36:39],a[212:215],a[108:111],0\n"
"s_add_u32 s12,s12,0x17c00000\n"
TR "a[206:207],v1 offset:4616\n"
AW "a168,v152\n"
MF "v[36:39],a[208:211],a[68:71],v[36:39]\n"
"v_mov_b32 v220,s12\n"
"v_mov_b32 v221,s12\n"
MF "v[28:31],a[208:211],a[64:67],0\n"
"v_mov_b32 v222,s12\n"
"v_mov_b32 v223,s12\n"
MF "v[28:31],a[212:215],a[104:107],v[28:31]\n"
LD "v12,s[28:31],s82 offen offset:2048 lds\n"
MF "v[20:23],a[212:215],a[100:103],0\n"
"s_waitcnt vmcnt(7) lgkmcnt(0)\n"
AW "a169,v153\n"
AW "a170,v154\n"
MF "v[20:23],a[208:211],a[60:63],v[20:23]\n"
"s_barrier\n"
MF "v[56:59],a[216:219],a[60:63],v[220:223]\n"
TR "a[208:209],v0 offset:16384\n"
AW "a171,v155\n"
MF "v[56:59],a[220:223],a[100:103],v[56:59]\n"
TR "a[210:211],v0 offset:16896\n"
AW "a176,v160\n"
MF "v[64:67],a[220:223],a[104:107],v[220:223]\n"
LD "v13,s[28:31],s82 offen offset:3072 lds\n"
MF "v[64:67],a[216:219],a[64:67],v[64:67]\n"
"s_add_u32 m0,s62,65536\n"
TR "a[212:213],v0 offset:20480\n"
AW "a177,v161\n"
MF "v[72:75],a[216:219],a[68:71],v[220:223]\n"
"s_add_u32 s82,s66,786560\n"
TR "a[214:215],v0 offset:20992\n"
AW "a178,v162\n"
MF "v[72:75],a[220:223],a[108:111],v[72:75]\n"
"s_sub_u32 s15,s82,7864320\n"
AW "a179,v163\n"
AW "a24,v168\n"
MF "v[80:83],a[220:223],a[112:115],v[220:223]\n"
"s_min_u32 s82,s82,s15\n"
AW "a25,v169\n"
AW "a26,v170\n"
MF "v[80:83],a[216:219],a[72:75],v[80:83]\n"
LD "v6,s[28:31],s82 offen lds\n"
MF "v[88:91],a[216:219],a[76:79],v[220:223]\n"
AW "a27,v171\n"
AW "a28,v172\n"
MF "v[88:91],a[220:223],a[116:119],v[88:91]\n"
AW "a29,v173\n"
AW "a30,v174\n"
MF "v[92:95],a[204:207],a[116:119],v[220:223]\n"
TR "a[216:217],v0 offset:16392\n"
AW "a31,v175\n"
MF "v[92:95],a[200:203],a[76:79],v[92:95]\n"
TR "a[218:219],v0 offset:16904\n"
AW "a20,v164\n"
MF "v[84:87],a[200:203],a[72:75],v[220:223]\n"
LD "v11,s[28:31],s82 offen offset:1024 lds\n"
MF "v[84:87],a[204:207],a[112:115],v[84:87]\n"
TR "a[220:221],v0 offset:20488\n"
AW "a21,v165\n"
MF "v[76:79],a[204:207],a[108:111],v[220:223]\n"
TR "a[222:223],v0 offset:21000\n"
AW "a22,v166\n"
MF "v[76:79],a[200:203],a[68:71],v[76:79]\n"
AW "a23,v167\n"
AW "a172,v156\n"
MF "v[68:71],a[200:203],a[64:67],v[220:223]\n"
AW "a173,v157\n"
AW "a174,v158\n"
MF "v[68:71],a[204:207],a[104:107],v[68:71]\n"
LD "v12,s[28:31],s82 offen offset:2048 lds\n"
MF "v[60:63],a[204:207],a[100:103],v[220:223]\n"
AW "a175,v159\n"
AW "a120,v148\n"
MF "v[60:63],a[200:203],a[60:63],v[60:63]\n"
"s_waitcnt lgkmcnt(4)\n"
AW "a121,v149\n"
AW "a122,v150\n"
MF "v[96:99],a[208:211],a[60:63],v[220:223]\n"
TR "a[200:201],v1 offset:16384\n"
AW "a123,v151\n"
MF "v[96:99],a[212:215],a[100:103],v[96:99]\n"
TR "a[202:203],v1 offset:16896\n"
AW "a92,v140\n"
MF "v[104:107],a[212:215],a[104:107],v[220:223]\n"
LD "v13,s[28:31],s82 offen offset:3072 lds\n"
MF "v[104:107],a[208:211],a[64:67],v[104:107]\n"
"s_add_u32 m0,s62,81920\n"
TR "a[204:205],v1 offset:20480\n"
AW "a93,v141\n"
MF "v[112:115],a[208:211],a[68:71],v[220:223]\n"
"s_add_u32 s82,s66,786688\n"
TR "a[206:207],v1 offset:20992\n"
AW "a94,v142\n"
MF "v[112:115],a[212:215],a[108:111],v[112:115]\n"
"s_sub_u32 s15,s82,7864320\n"
AW "a95,v143\n"
AW "a32,v176\n"
MF "v[120:123],a[212:215],a[112:115],v[220:223]\n"
"s_min_u32 s82,s82,s15\n"
AW "a33,v177\n"
AW "a34,v178\n"
MF "v[120:123],a[208:211],a[72:75],v[120:123]\n"
LD "v6,s[28:31],s82 offen lds\n"
MF "v[128:131],a[208:211],a[76:79],v[220:223]\n"
AW "a35,v179\n"
AW "a0,v184\n"
MF "v[128:131],a[212:215],a[116:119],v[128:131]\n"
"s_waitcnt lgkmcnt(4)\n"
AW "a1,v185\n"
AW "a2,v186\n"
MF "v[132:135],a[220:223],a[116:119],v[220:223]\n"
TR "a[208:209],v1 offset:16392\n"
AW "a3,v187\n"
MF "v[132:135],a[216:219],a[76:79],v[132:135]\n"
TR "a[210:211],v1 offset:16904\n"
AW "a8,v192\n"
MF "v[124:127],a[216:219],a[72:75],v[220:223]\n"
LD "v11,s[28:31],s82 offen offset:1024 lds\n"
MF "v[124:127],a[220:223],a[112:115],v[124:127]\n"
TR "a[212:213],v1 offset:20488\n"
AW "a9,v193\n"
MF "v[116:119],a[220:223],a[108:111],v[220:223]\n"
TR "a[214:215],v1 offset:21000\n"
AW "a10,v194\n"
MF "v[116:119],a[216:219],a[68:71],v[116:119]\n"
".Lpg4c:\n"
"s_cbranch_execz .Lpg14c\n"
".Lpg4f:\n"
"s_cbranch_execz .Lpg13f\n"
AW "a11,v195\n"
AW "a16,v200\n"
MF "v[108:111],a[216:219],a[64:67],v[220:223]\n"
AW "a17,v201\n"
AW "a18,v202\n"
MF "v[108:111],a[220:223],a[104:107],v[108:111]\n"
LD "v12,s[28:31],s82 offen offset:2048 lds\n"
MF "v[100:103],a[220:223],a[100:103],v[220:223]\n"
"s_waitcnt vmcnt(11) lgkmcnt(0)\n"
AW "a19,v203\n"
AW "a44,v208\n"
MF "v[100:103],a[216:219],a[60:63],v[100:103]\n"
"s_barrier\n"
MF "v[136:139],a[200:203],a[60:63],v[220:223]\n"
TR "a[216:217],v0 offset:32768\n"
AW "a45,v209\n"
MF "v[136:139],a[204:207],a[100:103],v[136:139]\n"
TR "a[218:219],v0 offset:33280\n"
AW "a46,v210\n"
MF "v[144:147],a[204:207],a[104:107],v[220:223]\n"
"ds_read_b128 a[140:143],v2 offset:20480\n"
AW "a47,v211\n"
MF "v[144:147],a[200:203],a[64:67],v[144:147]\n"
TR "a[220:221],v0 offset:36864\n"
AW "a48,v212\n"
MF "v[152:155],a[200:203],a[68:71],v[220:223]\n"
TR "a[222:223],v0 offset:37376\n"
AW "a49,v213\n"
MF "v[152:155],a[204:207],a[108:111],v[152:155]\n"
LD "v13,s[28:31],s82 offen offset:3072 lds\n"
MF "v[160:163],a[204:207],a[112:115],v[220:223]\n"
"ds_read_b128 a[180:183],v3 offset:20480\n"
AW "a50,v214\n"
MF "v[160:163],a[200:203],a[72:75],v[160:163]\n"
"s_add_u32 m0,s62,0\n"
AW "a51,v215\n"
AW "a40,v204\n"
MF "v[168:171],a[200:203],a[76:79],v[220:223]\n"
"s_add_u32 s82,s66,1572864\n"
"s_sub_u32 s15,s82,7864320\n"
"s_min_u32 s82,s82,s15\n"
MF "v[168:171],a[204:207],a[116:119],v[168:171]\n"
LD "v6,s[28:31],s82 offen lds\n"
MF "v[172:175],a[212:215],a[116:119],v[220:223]\n"
TR "a[200:201],v0 offset:32776\n"
AW "a41,v205\n"
MF "v[172:175],a[208:211],a[76:79],v[172:175]\n"
TR "a[202:203],v0 offset:33288\n"
AW "a42,v206\n"
MF "v[164:167],a[208:211],a[72:75],v[220:223]\n"
"ds_read_b128 a[144:147],v2 offset:22528\n"
AW "a43,v207\n"
MF "v[164:167],a[212:215],a[112:115],v[164:167]\n"
TR "a[204:205],v0 offset:36872\n"
AW "a12,v196\n"
MF "v[156:159],a[212:215],a[108:111],v[220:223]\n"
TR "a[206:207],v0 offset:37384\n"
AW "a13,v197\n"
MF "v[156:159],a[208:211],a[68:71],v[156:159]\n"
LD "v11,s[28:31],s82 offen offset:1024 lds\n"
MF "v[148:151],a[208:211],a[64:67],v[220:223]\n"
"ds_read_b128 a[184:187],v3 offset:22528\n"
AW "a14,v198\n"
MF "v[148:151],a[212:215],a[104:107],v[148:151]\n"
AW "a15,v199\n"
AW "a4,v188\n"
MF "v[140:143],a[212:215],a[100:103],v[220:223]\n"
AW "a5,v189\n"
AW "a6,v190\n"
MF "v[140:143],a[208:211],a[60:63],v[140:143]\n"
"s_waitcnt lgkmcnt(7)\n"
LD "v12,s[28:31],s82 offen offset:2048 lds\n"
MF "v[176:179],a[216:219],a[60:63],v[220:223]\n"
TR "a[208:209],v1 offset:32768\n"
AW "a7,v191\n"
MF "v[176:179],a[220:223],a[100:103],v[176:179]\n"
TR "a[210:211],v1 offset:33280\n"
AW "a36,v180\n"
MF "v[184:187],a[220:223],a[104:107],v[220:223]\n"
"ds_read_b128 a[148:151],v2 offset:24576\n"
AW "a37,v181\n"
MF "v[184:187],a[216:219],a[64:67],v[184:187]\n"
TR "a[212:213],v1 offset:36864\n"
AW "a38,v182\n"
MF "v[192:195],a[216:219],a[68:71],v[220:223]\n"
TR "a[214:215],v1 offset:37376\n"
AW "a39,v183\n"
MF "v[192:195],a[220:223],a[108:111],v[192:195]\n"
LD "v13,s[28:31],s82 offen offset:3072 lds\n"
MF "v[200:203],a[220:223],a[112:115],v[220:223]\n"
"ds_read_b128 a[188:191],v3 offset:24576\n"
AW "a52,v216\n"
MF "v[200:203],a[216:219],a[72:75],v[200:203]\n"
"s_add_u32 s14,s81,384\n"
"s_sub_u32 s15,s14,1280\n"
"s_min_u32 s14,s14,s15\n"
MF "v[208:211],a[216:219],a[76:79],v[220:223]\n"
"s_add_u32 s14,s14,s68\n"
"s_add_u32 s84,s14,0\n"
"s_add_u32 m0,s63,0\n"
MF "v[208:211],a[220:223],a[116:119],v[208:211]\n"
"s_waitcnt lgkmcnt(7)\n"
LD "v4,s[24:27],s84 offen lds\n"
MF "v[212:215],a[204:207],a[116:119],v[220:223]\n"
TR "a[216:217],v1 offset:32776\n"
AW "a53,v217\n"
MF "v[212:215],a[200:203],a[76:79],v[212:215]\n"
TR "a[218:219],v1 offset:33288\n"
AW "a54,v218\n"
MF "v[204:207],a[200:203],a[72:75],v[220:223]\n"
"ds_read_b128 a[152:155],v2 offset:26624\n"
AW "a55,v219\n"
MF "v[204:207],a[204:207],a[112:115],v[204:207]\n"
TR "a[220:221],v1 offset:36872\n"
AW "a80,v224\n"
MF "v[196:199],a[204:207],a[108:111],v[220:223]\n"
"s_add_u32 s84,s14,9216\n"
TR "a[222:223],v1 offset:37384\n"
AW "a81,v225\n"
MF "v[196:199],a[200:203],a[68:71],v[196:199]\n"
LD "v5,s[24:27],s84 offen offset:1024 lds\n"
MF "v[188:191],a[200:203],a[64:67],v[220:223]\n"
"s_add_u32 m0,s62,16384\n"
"s_add_u32 s82,s66,1572992\n"
"v_add_u32 v0,0xc000,v0\n"
MF "v[188:191],a[204:207],a[104:107],v[188:191]\n"
"s_sub_u32 s15,s82,7864320\n"
"s_min_u32 s82,s82,s15\n"
"v_add_u32 v1,0xc000,v1\n"
MF "v[180:183],a[204:207],a[100:103],v[220:223]\n"
"s_waitcnt vmcnt(14) lgkmcnt(0)\n"
LD "v6,s[28:31],s82 offen lds\n"
MF "v[180:183],a[200:203],a[60:63],v[180:183]\n"
"s_barrier\n"
MF "v[216:219],a[208:211],a[60:63],v[220:223]\n"
TR "a[200:201],v0\n"
AW "a82,v226\n"
MF "v[216:219],a[212:215],a[100:103],v[216:219]\n"
TR "a[202:203],v0 offset:512\n"
AW "a83,v227\n"
MF "v[224:227],a[212:215],a[104:107],v[220:223]\n"
"ds_read_b128 a[192:195],v3 offset:26624\n"
AW "a84,v228\n"
MF "v[224:227],a[208:211],a[64:67],v[224:227]\n"
TR "a[204:205],v0 offset:4096\n"
AW "a85,v229\n"
MF "v[232:235],a[208:211],a[68:71],v[220:223]\n"
TR "a[206:207],v0 offset:4608\n"
AW "a86,v230\n"
MF "v[232:235],a[212:215],a[108:111],v[232:235]\n"
LD "v11,s[28:31],s82 offen offset:1024 lds\n"
MF "v[240:243],a[212:215],a[112:115],v[220:223]\n"
"ds_read_b128 a[156:159],v2 offset:28672\n"
AW "a87,v231\n"
MF "v[240:243],a[208:211],a[72:75],v[240:243]\n"
"ds_read_b128 a[196:199],v3 offset:28672\n"
MF "v[248:251],a[208:211],a[76:79],v[220:223]\n"
MF "v[248:251],a[212:215],a[116:119],v[248:251]\n"
LD "v12,s[28:31],s82 offen offset:2048 lds\n"
MF "v[252:255],a[220:223],a[116:119],v[220:223]\n"
TR "a[208:209],v0 offset:8\n"
MF "v[252:255],a[216:219],a[76:79],v[252:255]\n"
TR "a[210:211],v0 offset:520\n"
MF "v[244:247],a[216:219],a[72:75],v[220:223]\n"
MF "v[244:247],a[220:223],a[112:115],v[244:247]\n"
TR "a[212:213],v0 offset:4104\n"
MF "v[236:239],a[220:223],a[108:111],v[220:223]\n"
TR "a[214:215],v0 offset:4616\n"
MF "v[236:239],a[216:219],a[68:71],v[236:239]\n"
LD "v13,s[28:31],s82 offen offset:3072 lds\n"
MF "v[228:231],a[216:219],a[64:67],v[220:223]\n"
MF "v[228:231],a[220:223],a[104:107],v[228:231]\n"
"s_add_u32 s84,s14,20480\n"
MF "v[220:223],a[220:223],a[100:103],v[220:223]\n"
"s_add_u32 m0,s63,2048\n"
MF "v[220:223],a[216:219],a[60:63],v[220:223]\n"
"s_waitcnt lgkmcnt(4)\n"
LD "v4,s[24:27],s84 offen lds\n"
MF "v[16:19],a[200:203],a[140:143],v[16:19]\n"
TR "a[216:217],v1\n"
MF "v[16:19],a[204:207],a[180:183],v[16:19]\n"
TR "a[218:219],v1 offset:512\n"
MF "v[24:27],a[204:207],a[184:187],v[24:27]\n"
MF "v[24:27],a[200:203],a[144:147],v[24:27]\n"
TR "a[220:221],v1 offset:4096\n"
MF "v[32:35],a[200:203],a[148:151],v[32:35]\n"
"s_add_u32 s84,s14,29696\n"
TR "a[222:223],v1 offset:4608\n"
MF "v[32:35],a[204:207],a[188:191],v[32:35]\n"
LD "v5,s[24:27],s84 offen offset:1024 lds\n"
MF "v[40:43],a[204:207],a[192:195],v[40:43]\n"
MF "v[40:43],a[200:203],a[152:155],v[40:43]\n"
"s_add_u32 m0,s62,32768\n"
MF "v[48:51],a[200:203],a[156:159],v[48:51]\n"
"s_add_u32 s82,s66,1573120\n"
"s_sub_u32 s15,s82,7864320\n"
"s_min_u32 s82,s82,s15\n"
MF "v[48:51],a[204:207],a[196:199],v[48:51]\n"
"s_waitcnt lgkmcnt(4)\n"
LD "v6,s[28:31],s82 offen lds\n"
MF "v[52:55],a[212:215],a[196:199],v[52:55]\n"
TR "a[200:201],v1 offset:8\n"
MF "v[52:55],a[208:211],a[156:159],v[52:55]\n"
TR "a[202:203],v1 offset:520\n"
MF "v[44:47],a[208:211],a[152:155],v[44:47]\n"
MF "v[44:47],a[212:215],a[192:195],v[44:47]\n"
TR "a[204:205],v1 offset:4104\n"
MF "v[36:39],a[212:215],a[188:191],v[36:39]\n"
TR "a[206:207],v1 offset:4616\n"
MF "v[36:39],a[208:211],a[148:151],v[36:39]\n"
LD "v11,s[28:31],s82 offen offset:1024 lds\n"
MF "v[28:31],a[208:211],a[144:147],v[28:31]\n"
MF "v[28:31],a[212:215],a[184:187],v[28:31]\n"
MF "v[20:23],a[212:215],a[180:183],v[20:23]\n"
"s_waitcnt vmcnt(18) lgkmcnt(0)\n"
LD "v12,s[28:31],s82 offen offset:2048 lds\n"
"s_bitcmp0_b32 s23,12\n"
"s_cbranch_scc1 .Lisx2\n"
"s_mov_b64 exec,0\n"
"s_cmp_eq_u32 s56,0\n"
"s_cbranch_scc1 .Lpg11d\n"
"s_cmp_eq_u32 s56,1\n"
"s_cbranch_scc1 .Lpg10d\n"
"s_cmp_eq_u32 s56,2\n"
"s_cbranch_scc1 .Lpgt0kx_d\n"
"s_branch .Lpg8d\n"
".Lisr2:\n"
"s_mov_b64 exec,-1\n"
".Lisx2:\n"
MF "v[20:23],a[208:211],a[140:143],v[20:23]\n"
"s_barrier\n"
MF "v[56:59],a[216:219],a[140:143],v[56:59]\n"
TR "a[208:209],v0 offset:16384\n"
MF "v[56:59],a[220:223],a[180:183],v[56:59]\n"
TR "a[210:211],v0 offset:16896\n"
MF "v[64:67],a[220:223],a[184:187],v[64:67]\n"
LD "v13,s[28:31],s82 offen offset:3072 lds\n"
MF "v[64:67],a[216:219],a[144:147],v[64:67]\n"
TR "a[212:213],v0 offset:20480\n"
MF "v[72:75],a[216:219],a[148:151],v[72:75]\n"
TR "a[214:215],v0 offset:20992\n"
MF "v[72:75],a[220:223],a[188:191],v[72:75]\n"
"s_add_u32 s84,s14,40960\n"
MF "v[80:83],a[220:223],a[192:195],v[80:83]\n"
"s_add_u32 m0,s63,4096\n"
MF "v[80:83],a[216:219],a[152:155],v[80:83]\n"
LD "v4,s[24:27],s84 offen lds\n"
MF "v[88:91],a[216:219],a[156:159],v[88:91]\n"
MF "v[88:91],a[220:223],a[196:199],v[88:91]\n"
"s_sub_u32 s66,s66,s83\n"
MF "v[92:95],a[204:207],a[196:199],v[92:95]\n"
"s_add_u32 m0,s62,49152\n"
"s_add_u32 s82,s66,2359296\n"
TR "a[216:217],v0 offset:16392\n"
MF "v[92:95],a[200:203],a[156:159],v[92:95]\n"
"s_sub_u32 s15,s82,7864320\n"
"s_min_u32 s82,s82,s15\n"
TR "a[218:219],v0 offset:16904\n"
MF "v[84:87],a[200:203],a[152:155],v[84:87]\n"
LD "v6,s[28:31],s82 offen lds\n"
MF "v[84:87],a[204:207],a[192:195],v[84:87]\n"
TR "a[220:221],v0 offset:20488\n"
MF "v[76:79],a[204:207],a[188:191],v[76:79]\n"
TR "a[222:223],v0 offset:21000\n"
MF "v[76:79],a[200:203],a[148:151],v[76:79]\n"
MF "v[68:71],a[200:203],a[144:147],v[68:71]\n"
MF "v[68:71],a[204:207],a[184:187],v[68:71]\n"
LD "v11,s[28:31],s82 offen offset:1024 lds\n"
MF "v[60:63],a[204:207],a[180:183],v[60:63]\n"
MF "v[60:63],a[200:203],a[140:143],v[60:63]\n"
"s_waitcnt lgkmcnt(4)\n"
MF "v[96:99],a[208:211],a[140:143],v[96:99]\n"
TR "a[200:201],v1 offset:16384\n"
MF "v[96:99],a[212:215],a[180:183],v[96:99]\n"
TR "a[202:203],v1 offset:16896\n"
MF "v[104:107],a[212:215],a[184:187],v[104:107]\n"
LD "v12,s[28:31],s82 offen offset:2048 lds\n"
MF "v[104:107],a[208:211],a[144:147],v[104:107]\n"
TR "a[204:205],v1 offset:20480\n"
MF "v[112:115],a[208:211],a[148:151],v[112:115]\n"
TR "a[206:207],v1 offset:20992\n"
MF "v[112:115],a[212:215],a[188:191],v[112:115]\n"
MF "v[120:123],a[212:215],a[192:195],v[120:123]\n"
MF "v[120:123],a[208:211],a[152:155],v[120:123]\n"
LD "v13,s[28:31],s82 offen offset:3072 lds\n"
MF "v[128:131],a[208:211],a[156:159],v[128:131]\n"
MF "v[128:131],a[212:215],a[196:199],v[128:131]\n"
"s_add_u32 s14,s81,512\n"
"s_sub_u32 s15,s14,1280\n"
"s_waitcnt lgkmcnt(4)\n"
MF "v[132:135],a[220:223],a[196:199],v[132:135]\n"
"s_min_u32 s14,s14,s15\n"
"s_add_u32 s14,s14,s68\n"
TR "a[208:209],v1 offset:16392\n"
MF "v[132:135],a[216:219],a[156:159],v[132:135]\n"
"s_add_u32 s84,s14,0\n"
"s_add_u32 m0,s63,20480\n"
TR "a[210:211],v1 offset:16904\n"
MF "v[124:127],a[216:219],a[152:155],v[124:127]\n"
LD "v4,s[24:27],s84 offen lds\n"
MF "v[124:127],a[220:223],a[192:195],v[124:127]\n"
TR "a[212:213],v1 offset:20488\n"
MF "v[116:119],a[220:223],a[188:191],v[116:119]\n"
TR "a[214:215],v1 offset:21000\n"
MF "v[116:119],a[216:219],a[148:151],v[116:119]\n"
MF "v[108:111],a[216:219],a[144:147],v[108:111]\n"
"s_add_u32 s84,s14,9216\n"
MF "v[108:111],a[220:223],a[184:187],v[108:111]\n"
LD "v5,s[24:27],s84 offen offset:1024 lds\n"
MF "v[100:103],a[220:223],a[180:183],v[100:103]\n"
"s_waitcnt vmcnt(23) lgkmcnt(0)\n"
MF "v[100:103],a[216:219],a[140:143],v[100:103]\n"
"s_barrier\n"
MF "v[136:139],a[200:203],a[140:143],v[136:139]\n"
TR "a[216:217],v0 offset:32768\n"
MF "v[136:139],a[204:207],a[180:183],v[136:139]\n"
"s_add_u32 m0,s62,65536\n"
TR "a[218:219],v0 offset:33280\n"
MF "v[144:147],a[204:207],a[184:187],v[144:147]\n"
"s_add_u32 s82,s66,2359424\n"
"ds_read_b128 a[60:63],v2 offset:40960\n"
MF "v[144:147],a[200:203],a[144:147],v[144:147]\n"
"s_sub_u32 s15,s82,7864320\n"
TR "a[220:221],v0 offset:36864\n"
MF "v[152:155],a[200:203],a[148:151],v[152:155]\n"
"s_min_u32 s82,s82,s15\n"
TR "a[222:223],v0 offset:37376\n"
MF "v[152:155],a[204:207],a[188:191],v[152:155]\n"
LD "v6,s[28:31],s82 offen lds\n"
MF "v[160:163],a[204:207],a[192:195],v[160:163]\n"
"ds_read_b128 a[100:103],v3 offset:40960\n"
MF "v[160:163],a[200:203],a[152:155],v[160:163]\n"
LD "v11,s[28:31],s82 offen offset:1024 lds\n"
MF "v[168:171],a[200:203],a[156:159],v[168:171]\n"
MF "v[168:171],a[204:207],a[196:199],v[168:171]\n"
LD "v12,s[28:31],s82 offen offset:2048 lds\n"
MF "v[172:175],a[212:215],a[196:199],v[172:175]\n"
TR "a[200:201],v0 offset:32776\n"
MF "v[172:175],a[208:211],a[156:159],v[172:175]\n"
TR "a[202:203],v0 offset:33288\n"
MF "v[164:167],a[208:211],a[152:155],v[164:167]\n"
"ds_read_b128 a[64:67],v2 offset:43008\n"
MF "v[164:167],a[212:215],a[192:195],v[164:167]\n"
TR "a[204:205],v0 offset:36872\n"
MF "v[156:159],a[212:215],a[188:191],v[156:159]\n"
TR "a[206:207],v0 offset:37384\n"
MF "v[156:159],a[208:211],a[148:151],v[156:159]\n"
MF "v[148:151],a[208:211],a[144:147],v[148:151]\n"
"ds_read_b128 a[104:107],v3 offset:43008\n"
MF "v[148:151],a[212:215],a[184:187],v[148:151]\n"
MF "v[140:143],a[212:215],a[180:183],v[140:143]\n"
LD "v13,s[28:31],s82 offen offset:3072 lds\n"
MF "v[140:143],a[208:211],a[140:143],v[140:143]\n"
"s_waitcnt lgkmcnt(7)\n"
MF "v[176:179],a[216:219],a[140:143],v[176:179]\n"
TR "a[208:209],v1 offset:32768\n"
MF "v[176:179],a[220:223],a[180:183],v[176:179]\n"
TR "a[210:211],v1 offset:33280\n"
MF "v[184:187],a[220:223],a[184:187],v[184:187]\n"
"ds_read_b128 a[68:71],v2 offset:45056\n"
MF "v[184:187],a[216:219],a[144:147],v[184:187]\n"
"s_add_u32 s84,s14,20480\n"
TR "a[212:213],v1 offset:36864\n"
MF "v[192:195],a[216:219],a[148:151],v[192:195]\n"
"s_add_u32 m0,s63,22528\n"
TR "a[214:215],v1 offset:37376\n"
MF "v[192:195],a[220:223],a[188:191],v[192:195]\n"
LD "v4,s[24:27],s84 offen lds\n"
MF "v[200:203],a[220:223],a[192:195],v[200:203]\n"
"ds_read_b128 a[108:111],v3 offset:45056\n"
MF "v[200:203],a[216:219],a[152:155],v[200:203]\n"
MF "v[208:211],a[216:219],a[156:159],v[208:211]\n"
"s_add_u32 s84,s14,29696\n"
MF "v[208:211],a[220:223],a[196:199],v[208:211]\n"
"s_waitcnt lgkmcnt(7)\n"
LD "v5,s[24:27],s84 offen offset:1024 lds\n"
MF "v[212:215],a[204:207],a[196:199],v[212:215]\n"
TR "a[216:217],v1 offset:32776\n"
MF "v[212:215],a[200:203],a[156:159],v[212:215]\n"
TR "a[218:219],v1 offset:33288\n"
MF "v[204:207],a[200:203],a[152:155],v[204:207]\n"
"ds_read_b128 a[72:75],v2 offset:47104\n"
MF "v[204:207],a[204:207],a[192:195],v[204:207]\n"
TR "a[220:221],v1 offset:36872\n"
MF "v[196:199],a[204:207],a[188:191],v[196:199]\n"
TR "a[222:223],v1 offset:37384\n"
MF "v[196:199],a[200:203],a[148:151],v[196:199]\n"
MF "v[188:191],a[200:203],a[144:147],v[188:191]\n"
"v_add_u32 v0,0xffff4000,v0\n"
MF "v[188:191],a[204:207],a[184:187],v[188:191]\n"
"v_add_u32 v1,0xffff4000,v1\n"
MF "v[180:183],a[204:207],a[180:183],v[180:183]\n"
"s_waitcnt vmcnt(25) lgkmcnt(0)\n"
MF "v[180:183],a[200:203],a[140:143],v[180:183]\n"
"s_barrier\n"
MF "v[216:219],a[208:211],a[140:143],v[216:219]\n"
TR "a[200:201],v0\n"
MF "v[216:219],a[212:215],a[180:183],v[216:219]\n"
"s_add_u32 m0,s62,81920\n"
TR "a[202:203],v0 offset:512\n"
MF "v[224:227],a[212:215],a[184:187],v[224:227]\n"
"s_add_u32 s82,s66,2359552\n"
"ds_read_b128 a[112:115],v3 offset:47104\n"
MF "v[224:227],a[208:211],a[144:147],v[224:227]\n"
"s_sub_u32 s15,s82,7864320\n"
TR "a[204:205],v0 offset:4096\n"
MF "v[232:235],a[208:211],a[148:151],v[232:235]\n"
"s_min_u32 s82,s82,s15\n"
TR "a[206:207],v0 offset:4608\n"
MF "v[232:235],a[212:215],a[188:191],v[232:235]\n"
LD "v6,s[28:31],s82 offen lds\n"
MF "v[240:243],a[212:215],a[192:195],v[240:243]\n"
"ds_read_b128 a[76:79],v2 offset:49152\n"
MF "v[240:243],a[208:211],a[152:155],v[240:243]\n"
"ds_read_b128 a[116:119],v3 offset:49152\n"
MF "v[248:251],a[208:211],a[156:159],v[248:251]\n"
LD "v11,s[28:31],s82 offen offset:1024 lds\n"
MF "v[248:251],a[212:215],a[196:199],v[248:251]\n"
MF "v[252:255],a[220:223],a[196:199],v[252:255]\n"
TR "a[208:209],v0 offset:8\n"
MF "v[252:255],a[216:219],a[156:159],v[252:255]\n"
TR "a[210:211],v0 offset:520\n"
MF "v[244:247],a[216:219],a[152:155],v[244:247]\n"
MF "v[244:247],a[220:223],a[192:195],v[244:247]\n"
TR "a[212:213],v0 offset:4104\n"
MF "v[236:239],a[220:223],a[188:191],v[236:239]\n"
TR "a[214:215],v0 offset:4616\n"
MF "v[236:239],a[216:219],a[148:151],v[236:239]\n"
LD "v12,s[28:31],s82 offen offset:2048 lds\n"
MF "v[228:231],a[216:219],a[144:147],v[228:231]\n"
MF "v[228:231],a[220:223],a[184:187],v[228:231]\n"
MF "v[220:223],a[220:223],a[180:183],v[220:223]\n"
MF "v[220:223],a[216:219],a[140:143],v[220:223]\n"
"s_waitcnt lgkmcnt(4)\n"
MF "v[16:19],a[200:203],a[60:63],v[16:19]\n"
TR "a[216:217],v1\n"
MF "v[16:19],a[204:207],a[100:103],v[16:19]\n"
TR "a[218:219],v1 offset:512\n"
MF "v[24:27],a[204:207],a[104:107],v[24:27]\n"
LD "v13,s[28:31],s82 offen offset:3072 lds\n"
MF "v[24:27],a[200:203],a[64:67],v[24:27]\n"
TR "a[220:221],v1 offset:4096\n"
MF "v[32:35],a[200:203],a[68:71],v[32:35]\n"
TR "a[222:223],v1 offset:4608\n"
MF "v[32:35],a[204:207],a[108:111],v[32:35]\n"
MF "v[40:43],a[204:207],a[112:115],v[40:43]\n"
MF "v[40:43],a[200:203],a[72:75],v[40:43]\n"
"s_add_u32 s84,s14,40960\n"
MF "v[48:51],a[200:203],a[76:79],v[48:51]\n"
"s_add_u32 m0,s63,24576\n"
MF "v[48:51],a[204:207],a[116:119],v[48:51]\n"
"s_waitcnt lgkmcnt(4)\n"
LD "v4,s[24:27],s84 offen lds\n"
MF "v[52:55],a[212:215],a[116:119],v[52:55]\n"
TR "a[200:201],v1 offset:8\n"
MF "v[52:55],a[208:211],a[76:79],v[52:55]\n"
TR "a[202:203],v1 offset:520\n"
MF "v[44:47],a[208:211],a[72:75],v[44:47]\n"
MF "v[44:47],a[212:215],a[112:115],v[44:47]\n"
TR "a[204:205],v1 offset:4104\n"
MF "v[36:39],a[212:215],a[108:111],v[36:39]\n"
TR "a[206:207],v1 offset:4616\n"
MF "v[36:39],a[208:211],a[68:71],v[36:39]\n"
MF "v[28:31],a[208:211],a[64:67],v[28:31]\n"
MF "v[28:31],a[212:215],a[104:107],v[28:31]\n"
MF "v[20:23],a[212:215],a[100:103],v[20:23]\n"
"s_waitcnt vmcnt(24) lgkmcnt(0)\n"
MF "v[20:23],a[208:211],a[60:63],v[20:23]\n"
"s_barrier\n"
MF "v[56:59],a[216:219],a[60:63],v[56:59]\n"
"s_add_u32 m0,s62,0\n"
"s_add_u32 s82,s66,3145728\n"
TR "a[208:209],v0 offset:16384\n"
MF "v[56:59],a[220:223],a[100:103],v[56:59]\n"
"s_sub_u32 s15,s82,7864320\n"
"s_min_u32 s82,s82,s15\n"
TR "a[210:211],v0 offset:16896\n"
MF "v[64:67],a[220:223],a[104:107],v[64:67]\n"
LD "v6,s[28:31],s82 offen lds\n"
MF "v[64:67],a[216:219],a[64:67],v[64:67]\n"
TR "a[212:213],v0 offset:20480\n"
MF "v[72:75],a[216:219],a[68:71],v[72:75]\n"
TR "a[214:215],v0 offset:20992\n"
MF "v[72:75],a[220:223],a[108:111],v[72:75]\n"
MF "v[80:83],a[220:223],a[112:115],v[80:83]\n"
LD "v11,s[28:31],s82 offen offset:1024 lds\n"
MF "v[80:83],a[216:219],a[72:75],v[80:83]\n"
MF "v[88:91],a[216:219],a[76:79],v[88:91]\n"
MF "v[88:91],a[220:223],a[116:119],v[88:91]\n"
MF "v[92:95],a[204:207],a[116:119],v[92:95]\n"
TR "a[216:217],v0 offset:16392\n"
MF "v[92:95],a[200:203],a[76:79],v[92:95]\n"
TR "a[218:219],v0 offset:16904\n"
MF "v[84:87],a[200:203],a[72:75],v[84:87]\n"
LD "v12,s[28:31],s82 offen offset:2048 lds\n"
MF "v[84:87],a[204:207],a[112:115],v[84:87]\n"
TR "a[220:221],v0 offset:20488\n"
MF "v[76:79],a[204:207],a[108:111],v[76:79]\n"
TR "a[222:223],v0 offset:21000\n"
MF "v[76:79],a[200:203],a[68:71],v[76:79]\n"
MF "v[68:71],a[200:203],a[64:67],v[68:71]\n"
MF "v[68:71],a[204:207],a[104:107],v[68:71]\n"
MF "v[60:63],a[204:207],a[100:103],v[60:63]\n"
LD "v13,s[28:31],s82 offen offset:3072 lds\n"
MF "v[60:63],a[200:203],a[60:63],v[60:63]\n"
"s_waitcnt lgkmcnt(4)\n"
MF "v[96:99],a[208:211],a[60:63],v[96:99]\n"
TR "a[200:201],v1 offset:16384\n"
MF "v[96:99],a[212:215],a[100:103],v[96:99]\n"
TR "a[202:203],v1 offset:16896\n"
MF "v[104:107],a[212:215],a[104:107],v[104:107]\n"
"s_add_u32 s14,s81,640\n"
"s_sub_u32 s15,s14,1280\n"
MF "v[104:107],a[208:211],a[64:67],v[104:107]\n"
"s_min_u32 s14,s14,s15\n"
"s_add_u32 s14,s14,s68\n"
TR "a[204:205],v1 offset:20480\n"
MF "v[112:115],a[208:211],a[68:71],v[112:115]\n"
"s_add_u32 s84,s14,0\n"
"s_add_u32 m0,s63,40960\n"
TR "a[206:207],v1 offset:20992\n"
MF "v[112:115],a[212:215],a[108:111],v[112:115]\n"
LD "v4,s[24:27],s84 offen lds\n"
MF "v[120:123],a[212:215],a[112:115],v[120:123]\n"
MF "v[120:123],a[208:211],a[72:75],v[120:123]\n"
MF "v[128:131],a[208:211],a[76:79],v[128:131]\n"
"s_add_u32 s84,s14,9216\n"
MF "v[128:131],a[212:215],a[116:119],v[128:131]\n"
"s_waitcnt lgkmcnt(4)\n"
LD "v5,s[24:27],s84 offen offset:1024 lds\n"
MF "v[132:135],a[220:223],a[116:119],v[132:135]\n"
TR "a[208:209],v1 offset:16392\n"
MF "v[132:135],a[216:219],a[76:79],v[132:135]\n"
TR "a[210:211],v1 offset:16904\n"
MF "v[124:127],a[216:219],a[72:75],v[124:127]\n"
MF "v[124:127],a[220:223],a[112:115],v[124:127]\n"
TR "a[212:213],v1 offset:20488\n"
MF "v[116:119],a[220:223],a[108:111],v[116:119]\n"
".Lpg5c:\n"
"s_cbranch_execz .Lpg15c\n"
".Lpg5f:\n"
"s_cbranch_execz .LpgattB_f\n"
TR "a[214:215],v1 offset:21000\n"
MF "v[116:119],a[216:219],a[68:71],v[116:119]\n"
MF "v[108:111],a[216:219],a[64:67],v[108:111]\n"
MF "v[108:111],a[220:223],a[104:107],v[108:111]\n"
MF "v[100:103],a[220:223],a[100:103],v[100:103]\n"
"s_waitcnt vmcnt(23) lgkmcnt(0)\n"
MF "v[100:103],a[216:219],a[60:63],v[100:103]\n"
"s_barrier\n"
MF "v[136:139],a[200:203],a[60:63],v[136:139]\n"
TR "a[216:217],v0 offset:32768\n"
MF "v[136:139],a[204:207],a[100:103],v[136:139]\n"
"s_add_u32 m0,s62,16384\n"
TR "a[218:219],v0 offset:33280\n"
MF "v[144:147],a[204:207],a[104:107],v[144:147]\n"
"s_add_u32 s82,s66,3145856\n"
"ds_read_b128 a[140:143],v2\n"
MF "v[144:147],a[200:203],a[64:67],v[144:147]\n"
"s_sub_u32 s15,s82,7864320\n"
TR "a[220:221],v0 offset:36864\n"
MF "v[152:155],a[200:203],a[68:71],v[152:155]\n"
"s_min_u32 s82,s82,s15\n"
TR "a[222:223],v0 offset:37376\n"
MF "v[152:155],a[204:207],a[108:111],v[152:155]\n"
LD "v6,s[28:31],s82 offen lds\n"
MF "v[160:163],a[204:207],a[112:115],v[160:163]\n"
"ds_read_b128 a[180:183],v3\n"
MF "v[160:163],a[200:203],a[72:75],v[160:163]\n"
LD "v11,s[28:31],s82 offen offset:1024 lds\n"
MF "v[168:171],a[200:203],a[76:79],v[168:171]\n"
MF "v[168:171],a[204:207],a[116:119],v[168:171]\n"
LD "v12,s[28:31],s82 offen offset:2048 lds\n"
MF "v[172:175],a[212:215],a[116:119],v[172:175]\n"
TR "a[200:201],v0 offset:32776\n"
MF "v[172:175],a[208:211],a[76:79],v[172:175]\n"
TR "a[202:203],v0 offset:33288\n"
MF "v[164:167],a[208:211],a[72:75],v[164:167]\n"
"ds_read_b128 a[144:147],v2 offset:2048\n"
MF "v[164:167],a[212:215],a[112:115],v[164:167]\n"
TR "a[204:205],v0 offset:36872\n"
MF "v[156:159],a[212:215],a[108:111],v[156:159]\n"
TR "a[206:207],v0 offset:37384\n"
MF "v[156:159],a[208:211],a[68:71],v[156:159]\n"
MF "v[148:151],a[208:211],a[64:67],v[148:151]\n"
"ds_read_b128 a[184:187],v3 offset:2048\n"
MF "v[148:151],a[212:215],a[104:107],v[148:151]\n"
MF "v[140:143],a[212:215],a[100:103],v[140:143]\n"
LD "v13,s[28:31],s82 offen offset:3072 lds\n"
MF "v[140:143],a[208:211],a[60:63],v[140:143]\n"
"s_waitcnt lgkmcnt(7)\n"
MF "v[176:179],a[216:219],a[60:63],v[176:179]\n"
TR "a[208:209],v1 offset:32768\n"
MF "v[176:179],a[220:223],a[100:103],v[176:179]\n"
TR "a[210:211],v1 offset:33280\n"
MF "v[184:187],a[220:223],a[104:107],v[184:187]\n"
"ds_read_b128 a[148:151],v2 offset:4096\n"
MF "v[184:187],a[216:219],a[64:67],v[184:187]\n"
"s_add_u32 s84,s14,20480\n"
TR "a[212:213],v1 offset:36864\n"
MF "v[192:195],a[216:219],a[68:71],v[192:195]\n"
"s_add_u32 m0,s63,43008\n"
TR "a[214:215],v1 offset:37376\n"
MF "v[192:195],a[220:223],a[108:111],v[192:195]\n"
LD "v4,s[24:27],s84 offen lds\n"
MF "v[200:203],a[220:223],a[112:115],v[200:203]\n"
"ds_read_b128 a[188:191],v3 offset:4096\n"
MF "v[200:203],a[216:219],a[72:75],v[200:203]\n"
MF "v[208:211],a[216:219],a[76:79],v[208:211]\n"
"s_add_u32 s84,s14,29696\n"
MF "v[208:211],a[220:223],a[116:119],v[208:211]\n"
"s_waitcnt lgkmcnt(7)\n"
LD "v5,s[24:27],s84 offen offset:1024 lds\n"
MF "v[212:215],a[204:207],a[116:119],v[212:215]\n"
TR "a[216:217],v1 offset:32776\n"
MF "v[212:215],a[200:203],a[76:79],v[212:215]\n"
TR "a[218:219],v1 offset:33288\n"
MF "v[204:207],a[200:203],a[72:75],v[204:207]\n"
"ds_read_b128 a[152:155],v2 offset:6144\n"
MF "v[204:207],a[204:207],a[112:115],v[204:207]\n"
TR "a[220:221],v1 offset:36872\n"
MF "v[196:199],a[204:207],a[108:111],v[196:199]\n"
TR "a[222:223],v1 offset:37384\n"
MF "v[196:199],a[200:203],a[68:71],v[196:199]\n"
MF "v[188:191],a[200:203],a[64:67],v[188:191]\n"
"v_add_u32 v0,0xc000,v0\n"
MF "v[188:191],a[204:207],a[104:107],v[188:191]\n"
"v_add_u32 v1,0xc000,v1\n"
MF "v[180:183],a[204:207],a[100:103],v[180:183]\n"
"s_waitcnt vmcnt(25) lgkmcnt(0)\n"
MF "v[180:183],a[200:203],a[60:63],v[180:183]\n"
"s_barrier\n"
MF "v[216:219],a[208:211],a[60:63],v[216:219]\n"
TR "a[200:201],v0\n"
MF "v[216:219],a[212:215],a[100:103],v[216:219]\n"
"s_add_u32 m0,s62,32768\n"
TR "a[202:203],v0 offset:512\n"
MF "v[224:227],a[212:215],a[104:107],v[224:227]\n"
"s_add_u32 s82,s66,3145984\n"
"ds_read_b128 a[192:195],v3 offset:6144\n"
MF "v[224:227],a[208:211],a[64:67],v[224:227]\n"
"s_sub_u32 s15,s82,7864320\n"
TR "a[204:205],v0 offset:4096\n"
MF "v[232:235],a[208:211],a[68:71],v[232:235]\n"
"s_min_u32 s82,s82,s15\n"
TR "a[206:207],v0 offset:4608\n"
MF "v[232:235],a[212:215],a[108:111],v[232:235]\n"
LD "v6,s[28:31],s82 offen lds\n"
MF "v[240:243],a[212:215],a[112:115],v[240:243]\n"
"ds_read_b128 a[156:159],v2 offset:8192\n"
MF "v[240:243],a[208:211],a[72:75],v[240:243]\n"
"ds_read_b128 a[196:199],v3 offset:8192\n"
MF "v[248:251],a[208:211],a[76:79],v[248:251]\n"
LD "v11,s[28:31],s82 offen offset:1024 lds\n"
MF "v[248:251],a[212:215],a[116:119],v[248:251]\n"
MF "v[252:255],a[220:223],a[116:119],v[252:255]\n"
TR "a[208:209],v0 offset:8\n"
MF "v[252:255],a[216:219],a[76:79],v[252:255]\n"
TR "a[210:211],v0 offset:520\n"
MF "v[244:247],a[216:219],a[72:75],v[244:247]\n"
MF "v[244:247],a[220:223],a[112:115],v[244:247]\n"
TR "a[212:213],v0 offset:4104\n"
MF "v[236:239],a[220:223],a[108:111],v[236:239]\n"
TR "a[214:215],v0 offset:4616\n"
MF "v[236:239],a[216:219],a[68:71],v[236:239]\n"
LD "v12,s[28:31],s82 offen offset:2048 lds\n"
MF "v[228:231],a[216:219],a[64:67],v[228:231]\n"
MF "v[228:231],a[220:223],a[104:107],v[228:231]\n"
MF "v[220:223],a[220:223],a[100:103],v[220:223]\n"
MF "v[220:223],a[216:219],a[60:63],v[220:223]\n"
"s_waitcnt lgkmcnt(4)\n"
MF "v[16:19],a[200:203],a[140:143],v[16:19]\n"
TR "a[216:217],v1\n"
MF "v[16:19],a[204:207],a[180:183],v[16:19]\n"
TR "a[218:219],v1 offset:512\n"
MF "v[24:27],a[204:207],a[184:187],v[24:27]\n"
LD "v13,s[28:31],s82 offen offset:3072 lds\n"
MF "v[24:27],a[200:203],a[144:147],v[24:27]\n"
TR "a[220:221],v1 offset:4096\n"
MF "v[32:35],a[200:203],a[148:151],v[32:35]\n"
TR "a[222:223],v1 offset:4608\n"
MF "v[32:35],a[204:207],a[188:191],v[32:35]\n"
MF "v[40:43],a[204:207],a[192:195],v[40:43]\n"
MF "v[40:43],a[200:203],a[152:155],v[40:43]\n"
"s_add_u32 s84,s14,40960\n"
MF "v[48:51],a[200:203],a[156:159],v[48:51]\n"
"s_add_u32 m0,s63,45056\n"
MF "v[48:51],a[204:207],a[196:199],v[48:51]\n"
"s_waitcnt lgkmcnt(4)\n"
LD "v4,s[24:27],s84 offen lds\n"
MF "v[52:55],a[212:215],a[196:199],v[52:55]\n"
TR "a[200:201],v1 offset:8\n"
MF "v[52:55],a[208:211],a[156:159],v[52:55]\n"
TR "a[202:203],v1 offset:520\n"
MF "v[44:47],a[208:211],a[152:155],v[44:47]\n"
MF "v[44:47],a[212:215],a[192:195],v[44:47]\n"
TR "a[204:205],v1 offset:4104\n"
MF "v[36:39],a[212:215],a[188:191],v[36:39]\n"
TR "a[206:207],v1 offset:4616\n"
MF "v[36:39],a[208:211],a[148:151],v[36:39]\n"
MF "v[28:31],a[208:211],a[144:147],v[28:31]\n"
MF "v[28:31],a[212:215],a[184:187],v[28:31]\n"
MF "v[20:23],a[212:215],a[180:183],v[20:23]\n"
"s_waitcnt vmcnt(24) lgkmcnt(0)\n"
MF "v[20:23],a[208:211],a[140:143],v[20:23]\n"
"s_barrier\n"
MF "v[56:59],a[216:219],a[140:143],v[56:59]\n"
"s_add_u32 m0,s62,49152\n"
"s_add_u32 s82,s66,3932160\n"
TR "a[208:209],v0 offset:16384\n"
MF "v[56:59],a[220:223],a[180:183],v[56:59]\n"
"s_sub_u32 s15,s82,7864320\n"
"s_min_u32 s82,s82,s15\n"
TR "a[210:211],v0 offset:16896\n"
MF "v[64:67],a[220:223],a[184:187],v[64:67]\n"
LD "v6,s[28:31],s82 offen lds\n"
MF "v[64:67],a[216:219],a[144:147],v[64:67]\n"
TR "a[212:213],v0 offset:20480\n"
MF "v[72:75],a[216:219],a[148:151],v[72:75]\n"
TR "a[214:215],v0 offset:20992\n"
MF "v[72:75],a[220:223],a[188:191],v[72:75]\n"
MF "v[80:83],a[220:223],a[192:195],v[80:83]\n"
LD "v11,s[28:31],s82 offen offset:1024 lds\n"
MF "v[80:83],a[216:219],a[152:155],v[80:83]\n"
MF "v[88:91],a[216:219],a[156:159],v[88:91]\n"
MF "v[88:91],a[220:223],a[196:199],v[88:91]\n"
MF "v[92:95],a[204:207],a[196:199],v[92:95]\n"
TR "a[216:217],v0 offset:16392\n"
MF "v[92:95],a[200:203],a[156:159],v[92:95]\n"
TR "a[218:219],v0 offset:16904\n"
MF "v[84:87],a[200:203],a[152:155],v[84:87]\n"
LD "v12,s[28:31],s82 offen offset:2048 lds\n"
MF "v[84:87],a[204:207],a[192:195],v[84:87]\n"
TR "a[220:221],v0 offset:20488\n"
MF "v[76:79],a[204:207],a[188:191],v[76:79]\n"
TR "a[222:223],v0 offset:21000\n"
MF "v[76:79],a[200:203],a[148:151],v[76:79]\n"
MF "v[68:71],a[200:203],a[144:147],v[68:71]\n"
MF "v[68:71],a[204:207],a[184:187],v[68:71]\n"
MF "v[60:63],a[204:207],a[180:183],v[60:63]\n"
LD "v13,s[28:31],s82 offen offset:3072 lds\n"
MF "v[60:63],a[200:203],a[140:143],v[60:63]\n"
"s_waitcnt lgkmcnt(4)\n"
MF "v[96:99],a[208:211],a[140:143],v[96:99]\n"
TR "a[200:201],v1 offset:16384\n"
MF "v[96:99],a[212:215],a[180:183],v[96:99]\n"
TR "a[202:203],v1 offset:16896\n"
MF "v[104:107],a[212:215],a[184:187],v[104:107]\n"
"s_add_u32 s14,s81,768\n"
"s_sub_u32 s15,s14,1280\n"
MF "v[104:107],a[208:211],a[144:147],v[104:107]\n"
"s_min_u32 s14,s14,s15\n"
"s_add_u32 s14,s14,s68\n"
TR "a[204:205],v1 offset:20480\n"
MF "v[112:115],a[208:211],a[148:151],v[112:115]\n"
"s_add_u32 s84,s14,0\n"
"s_add_u32 m0,s63,0\n"
TR "a[206:207],v1 offset:20992\n"
MF "v[112:115],a[212:215],a[188:191],v[112:115]\n"
LD "v4,s[24:27],s84 offen lds\n"
MF "v[120:123],a[212:215],a[192:195],v[120:123]\n"
MF "v[120:123],a[208:211],a[152:155],v[120:123]\n"
MF "v[128:131],a[208:211],a[156:159],v[128:131]\n"
"s_add_u32 s84,s14,9216\n"
MF "v[128:131],a[212:215],a[196:199],v[128:131]\n"
"s_waitcnt lgkmcnt(4)\n"
LD "v5,s[24:27],s84 offen offset:1024 lds\n"
MF "v[132:135],a[220:223],a[196:199],v[132:135]\n"
TR "a[208:209],v1 offset:16392\n"
MF "v[132:135],a[216:219],a[156:159],v[132:135]\n"
TR "a[210:211],v1 offset:16904\n"
MF "v[124:127],a[216:219],a[152:155],v[124:127]\n"
MF "v[124:127],a[220:223],a[192:195],v[124:127]\n"
TR "a[212:213],v1 offset:20488\n"
MF "v[116:119],a[220:223],a[188:191],v[116:119]\n"
TR "a[214:215],v1 offset:21000\n"
MF "v[116:119],a[216:219],a[148:151],v[116:119]\n"
MF "v[108:111],a[216:219],a[144:147],v[108:111]\n"
MF "v[108:111],a[220:223],a[184:187],v[108:111]\n"
MF "v[100:103],a[220:223],a[180:183],v[100:103]\n"
"s_waitcnt vmcnt(23) lgkmcnt(0)\n"
MF "v[100:103],a[216:219],a[140:143],v[100:103]\n"
"s_barrier\n"
MF "v[136:139],a[200:203],a[140:143],v[136:139]\n"
TR "a[216:217],v0 offset:32768\n"
MF "v[136:139],a[204:207],a[180:183],v[136:139]\n"
"s_add_u32 m0,s62,65536\n"
TR "a[218:219],v0 offset:33280\n"
MF "v[144:147],a[204:207],a[184:187],v[144:147]\n"
"s_add_u32 s82,s66,3932288\n"
"ds_read_b128 a[60:63],v2 offset:20480\n"
MF "v[144:147],a[200:203],a[144:147],v[144:147]\n"
"s_sub_u32 s15,s82,7864320\n"
TR "a[220:221],v0 offset:36864\n"
MF "v[152:155],a[200:203],a[148:151],v[152:155]\n"
"s_min_u32 s82,s82,s15\n"
TR "a[222:223],v0 offset:37376\n"
MF "v[152:155],a[204:207],a[188:191],v[152:155]\n"
LD "v6,s[28:31],s82 offen lds\n"
MF "v[160:163],a[204:207],a[192:195],v[160:163]\n"
"ds_read_b128 a[100:103],v3 offset:20480\n"
MF "v[160:163],a[200:203],a[152:155],v[160:163]\n"
LD "v11,s[28:31],s82 offen offset:1024 lds\n"
MF "v[168:171],a[200:203],a[156:159],v[168:171]\n"
MF "v[168:171],a[204:207],a[196:199],v[168:171]\n"
LD "v12,s[28:31],s82 offen offset:2048 lds\n"
MF "v[172:175],a[212:215],a[196:199],v[172:175]\n"
TR "a[200:201],v0 offset:32776\n"
MF "v[172:175],a[208:211],a[156:159],v[172:175]\n"
TR "a[202:203],v0 offset:33288\n"
MF "v[164:167],a[208:211],a[152:155],v[164:167]\n"
"ds_read_b128 a[64:67],v2 offset:22528\n"
MF "v[164:167],a[212:215],a[192:195],v[164:167]\n"
TR "a[204:205],v0 offset:36872\n"
MF "v[156:159],a[212:215],a[188:191],v[156:159]\n"
TR "a[206:207],v0 offset:37384\n"
MF "v[156:159],a[208:211],a[148:151],v[156:159]\n"
MF "v[148:151],a[208:211],a[144:147],v[148:151]\n"
"ds_read_b128 a[104:107],v3 offset:22528\n"
MF "v[148:151],a[212:215],a[184:187],v[148:151]\n"
MF "v[140:143],a[212:215],a[180:183],v[140:143]\n"
LD "v13,s[28:31],s82 offen offset:3072 lds\n"
MF "v[140:143],a[208:211],a[140:143],v[140:143]\n"
"s_waitcnt lgkmcnt(7)\n"
MF "v[176:179],a[216:219],a[140:143],v[176:179]\n"
TR "a[208:209],v1 offset:32768\n"
MF "v[176:179],a[220:223],a[180:183],v[176:179]\n"
TR "a[210:211],v1 offset:33280\n"
MF "v[184:187],a[220:223],a[184:187],v[184:187]\n"
"ds_read_b128 a[68:71],v2 offset:24576\n"
MF "v[184:187],a[216:219],a[144:147],v[184:187]\n"
"s_add_u32 s84,s14,20480\n"
TR "a[212:213],v1 offset:36864\n"
MF "v[192:195],a[216:219],a[148:151],v[192:195]\n"
"s_add_u32 m0,s63,2048\n"
TR "a[214:215],v1 offset:37376\n"
MF "v[192:195],a[220:223],a[188:191],v[192:195]\n"
LD "v4,s[24:27],s84 offen lds\n"
MF "v[200:203],a[220:223],a[192:195],v[200:203]\n"
"ds_read_b128 a[108:111],v3 offset:24576\n"
MF "v[200:203],a[216:219],a[152:155],v[200:203]\n"
MF "v[208:211],a[216:219],a[156:159],v[208:211]\n"
"s_add_u32 s84,s14,29696\n"
MF "v[208:211],a[220:223],a[196:199],v[208:211]\n"
"s_waitcnt lgkmcnt(7)\n"
LD "v5,s[24:27],s84 offen offset:1024 lds\n"
MF "v[212:215],a[204:207],a[196:199],v[212:215]\n"
TR "a[216:217],v1 offset:32776\n"
MF "v[212:215],a[200:203],a[156:159],v[212:215]\n"
TR "a[218:219],v1 offset:33288\n"
MF "v[204:207],a[200:203],a[152:155],v[204:207]\n"
"ds_read_b128 a[72:75],v2 offset:26624\n"
MF "v[204:207],a[204:207],a[192:195],v[204:207]\n"
TR "a[220:221],v1 offset:36872\n"
MF "v[196:199],a[204:207],a[188:191],v[196:199]\n"
TR "a[222:223],v1 offset:37384\n"
MF "v[196:199],a[200:203],a[148:151],v[196:199]\n"
MF "v[188:191],a[200:203],a[144:147],v[188:191]\n"
"v_add_u32 v0,0xffff4000,v0\n"
MF "v[188:191],a[204:207],a[184:187],v[188:191]\n"
"v_add_u32 v1,0xffff4000,v1\n"
MF "v[180:183],a[204:207],a[180:183],v[180:183]\n"
"s_waitcnt vmcnt(25) lgkmcnt(0)\n"
MF "v[180:183],a[200:203],a[140:143],v[180:183]\n"
"s_barrier\n"
MF "v[216:219],a[208:211],a[140:143],v[216:219]\n"
TR "a[200:201],v0\n"
MF "v[216:219],a[212:215],a[180:183],v[216:219]\n"
"s_add_u32 m0,s62,81920\n"
TR "a[202:203],v0 offset:512\n"
MF "v[224:227],a[212:215],a[184:187],v[224:227]\n"
"s_add_u32 s82,s66,3932416\n"
"ds_read_b128 a[112:115],v3 offset:26624\n"
MF "v[224:227],a[208:211],a[144:147],v[224:227]\n"
"s_sub_u32 s15,s82,7864320\n"
TR "a[204:205],v0 offset:4096\n"
MF "v[232:235],a[208:211],a[148:151],v[232:235]\n"
"s_min_u32 s82,s82,s15\n"
TR "a[206:207],v0 offset:4608\n"
MF "v[232:235],a[212:215],a[188:191],v[232:235]\n"
LD "v6,s[28:31],s82 offen lds\n"
MF "v[240:243],a[212:215],a[192:195],v[240:243]\n"
"ds_read_b128 a[76:79],v2 offset:28672\n"
MF "v[240:243],a[208:211],a[152:155],v[240:243]\n"
"ds_read_b128 a[116:119],v3 offset:28672\n"
MF "v[248:251],a[208:211],a[156:159],v[248:251]\n"
LD "v11,s[28:31],s82 offen offset:1024 lds\n"
MF "v[248:251],a[212:215],a[196:199],v[248:251]\n"
MF "v[252:255],a[220:223],a[196:199],v[252:255]\n"
TR "a[208:209],v0 offset:8\n"
MF "v[252:255],a[216:219],a[156:159],v[252:255]\n"
TR "a[210:211],v0 offset:520\n"
MF "v[244:247],a[216:219],a[152:155],v[244:247]\n"
MF "v[244:247],a[220:223],a[192:195],v[244:247]\n"
TR "a[212:213],v0 offset:4104\n"
MF "v[236:239],a[220:223],a[188:191],v[236:239]\n"
TR "a[214:215],v0 offset:4616\n"
MF "v[236:239],a[216:219],a[148:151],v[236:239]\n"
LD "v12,s[28:31],s82 offen offset:2048 lds\n"
MF "v[228:231],a[216:219],a[144:147],v[228:231]\n"
MF "v[228:231],a[220:223],a[184:187],v[228:231]\n"
MF "v[220:223],a[220:223],a[180:183],v[220:223]\n"
MF "v[220:223],a[216:219],a[140:143],v[220:223]\n"
"s_waitcnt lgkmcnt(4)\n"
MF "v[16:19],a[200:203],a[60:63],v[16:19]\n"
TR "a[216:217],v1\n"
MF "v[16:19],a[204:207],a[100:103],v[16:19]\n"
TR "a[218:219],v1 offset:512\n"
MF "v[24:27],a[204:207],a[104:107],v[24:27]\n"
LD "v13,s[28:31],s82 offen offset:3072 lds\n"
MF "v[24:27],a[200:203],a[64:67],v[24:27]\n"
TR "a[220:221],v1 offset:4096\n"
MF "v[32:35],a[200:203],a[68:71],v[32:35]\n"
TR "a[222:223],v1 offset:4608\n"
MF "v[32:35],a[204:207],a[108:111],v[32:35]\n"
MF "v[40:43],a[204:207],a[112:115],v[40:43]\n"
MF "v[40:43],a[200:203],a[72:75],v[40:43]\n"
"s_add_u32 s84,s14,40960\n"
MF "v[48:51],a[200:203],a[76:79],v[48:51]\n"
"s_add_u32 m0,s63,4096\n"
MF "v[48:51],a[204:207],a[116:119],v[48:51]\n"
"s_waitcnt lgkmcnt(4)\n"
LD "v4,s[24:27],s84 offen lds\n"
MF "v[52:55],a[212:215],a[116:119],v[52:55]\n"
TR "a[200:201],v1 offset:8\n"
MF "v[52:55],a[208:211],a[76:79],v[52:55]\n"
TR "a[202:203],v1 offset:520\n"
MF "v[44:47],a[208:211],a[72:75],v[44:47]\n"
MF "v[44:47],a[212:215],a[112:115],v[44:47]\n"
TR "a[204:205],v1 offset:4104\n"
MF "v[36:39],a[212:215],a[108:111],v[36:39]\n"
TR "a[206:207],v1 offset:4616\n"
MF "v[36:39],a[208:211],a[68:71],v[36:39]\n"
MF "v[28:31],a[208:211],a[64:67],v[28:31]\n"
MF "v[28:31],a[212:215],a[104:107],v[28:31]\n"
MF "v[20:23],a[212:215],a[100:103],v[20:23]\n"
"s_waitcnt vmcnt(24) lgkmcnt(0)\n"
MF "v[20:23],a[208:211],a[60:63],v[20:23]\n"
"s_barrier\n"
MF "v[56:59],a[216:219],a[60:63],v[56:59]\n"
"s_add_u32 m0,s62,0\n"
"s_add_u32 s82,s66,4718592\n"
TR "a[208:209],v0 offset:16384\n"
MF "v[56:59],a[220:223],a[100:103],v[56:59]\n"
"s_sub_u32 s15,s82,7864320\n"
"s_min_u32 s82,s82,s15\n"
TR "a[210:211],v0 offset:16896\n"
MF "v[64:67],a[220:223],a[104:107],v[64:67]\n"
LD "v6,s[28:31],s82 offen lds\n"
MF "v[64:67],a[216:219],a[64:67],v[64:67]\n"
TR "a[212:213],v0 offset:20480\n"
MF "v[72:75],a[216:219],a[68:71],v[72:75]\n"
TR "a[214:215],v0 offset:20992\n"
MF "v[72:75],a[220:223],a[108:111],v[72:75]\n"
MF "v[80:83],a[220:223],a[112:115],v[80:83]\n"
LD "v11,s[28:31],s82 offen offset:1024 lds\n"
MF "v[80:83],a[216:219],a[72:75],v[80:83]\n"
MF "v[88:91],a[216:219],a[76:79],v[88:91]\n"
MF "v[88:91],a[220:223],a[116:119],v[88:91]\n"
MF "v[92:95],a[204:207],a[116:119],v[92:95]\n"
TR "a[216:217],v0 offset:16392\n"
MF "v[92:95],a[200:203],a[76:79],v[92:95]\n"
TR "a[218:219],v0 offset:16904\n"
MF "v[84:87],a[200:203],a[72:75],v[84:87]\n"
LD "v12,s[28:31],s82 offen offset:2048 lds\n"
MF "v[84:87],a[204:207],a[112:115],v[84:87]\n"
TR "a[220:221],v0 offset:20488\n"
MF "v[76:79],a[204:207],a[108:111],v[76:79]\n"
TR "a[222:223],v0 offset:21000\n"
MF "v[76:79],a[200:203],a[68:71],v[76:79]\n"
MF "v[68:71],a[200:203],a[64:67],v[68:71]\n"
MF "v[68:71],a[204:207],a[104:107],v[68:71]\n"
MF "v[60:63],a[204:207],a[100:103],v[60:63]\n"
LD "v13,s[28:31],s82 offen offset:3072 lds\n"
MF "v[60:63],a[200:203],a[60:63],v[60:63]\n"
"s_waitcnt lgkmcnt(4)\n"
MF "v[96:99],a[208:211],a[60:63],v[96:99]\n"
TR "a[200:201],v1 offset:16384\n"
MF "v[96:99],a[212:215],a[100:103],v[96:99]\n"
TR "a[202:203],v1 offset:16896\n"
MF "v[104:107],a[212:215],a[104:107],v[104:107]\n"
"s_add_u32 s14,s81,896\n"
"s_sub_u32 s15,s14,1280\n"
MF "v[104:107],a[208:211],a[64:67],v[104:107]\n"
"s_min_u32 s14,s14,s15\n"
"s_add_u32 s14,s14,s68\n"
TR "a[204:205],v1 offset:20480\n"
MF "v[112:115],a[208:211],a[68:71],v[112:115]\n"
"s_add_u32 s84,s14,0\n"
"s_add_u32 m0,s63,20480\n"
TR "a[206:207],v1 offset:20992\n"
MF "v[112:115],a[212:215],a[108:111],v[112:115]\n"
LD "v4,s[24:27],s84 offen lds\n"
MF "v[120:123],a[212:215],a[112:115],v[120:123]\n"
MF "v[120:123],a[208:211],a[72:75],v[120:123]\n"
MF "v[128:131],a[208:211],a[76:79],v[128:131]\n"
"s_add_u32 s84,s14,9216\n"
MF "v[128:131],a[212:215],a[116:119],v[128:131]\n"
"s_waitcnt lgkmcnt(4)\n"
LD "v5,s[24:27],s84 offen offset:1024 lds\n"
MF "v[132:135],a[220:223],a[116:119],v[132:135]\n"
TR "a[208:209],v1 offset:16392\n"
MF "v[132:135],a[216:219],a[76:79],v[132:135]\n"
TR "a[210:211],v1 offset:16904\n"
MF "v[124:127],a[216:219],a[72:75],v[124:127]\n"
MF "v[124:127],a[220:223],a[112:115],v[124:127]\n"
TR "a[212:213],v1 offset:20488\n"
MF "v[116:119],a[220:223],a[108:111],v[116:119]\n"
TR "a[214:215],v1 offset:21000\n"
MF "v[116:119],a[216:219],a[68:71],v[116:119]\n"
MF "v[108:111],a[216:219],a[64:67],v[108:111]\n"
MF "v[108:111],a[220:223],a[104:107],v[108:111]\n"
MF "v[100:103],a[220:223],a[100:103],v[100:103]\n"
"s_waitcnt vmcnt(23) lgkmcnt(0)\n"
MF "v[100:103],a[216:219],a[60:63],v[100:103]\n"
"s_barrier\n"
MF "v[136:139],a[200:203],a[60:63],v[136:139]\n"
TR "a[216:217],v0 offset:32768\n"
MF "v[136:139],a[204:207],a[100:103],v[136:139]\n"
"s_add_u32 m0,s62,16384\n"
TR "a[218:219],v0 offset:33280\n"
MF "v[144:147],a[204:207],a[104:107],v[144:147]\n"
"s_add_u32 s82,s66,4718720\n"
"ds_read_b128 a[140:143],v2 offset:40960\n"
MF "v[144:147],a[200:203],a[64:67],v[144:147]\n"
"s_sub_u32 s15,s82,7864320\n"
TR "a[220:221],v0 offset:36864\n"
MF "v[152:155],a[200:203],a[68:71],v[152:155]\n"
"s_min_u32 s82,s82,s15\n"
TR "a[222:223],v0 offset:37376\n"
MF "v[152:155],a[204:207],a[108:111],v[152:155]\n"
LD "v6,s[28:31],s82 offen lds\n"
MF "v[160:163],a[204:207],a[112:115],v[160:163]\n"
"ds_read_b128 a[180:183],v3 offset:40960\n"
MF "v[160:163],a[200:203],a[72:75],v[160:163]\n"
LD "v11,s[28:31],s82 offen offset:1024 lds\n"
MF "v[168:171],a[200:203],a[76:79],v[168:171]\n"
MF "v[168:171],a[204:207],a[116:119],v[168:171]\n"
LD "v12,s[28:31],s82 offen offset:2048 lds\n"
MF "v[172:175],a[212:215],a[116:119],v[172:175]\n"
TR "a[200:201],v0 offset:32776\n"
MF "v[172:175],a[208:211],a[76:79],v[172:175]\n"
TR "a[202:203],v0 offset:33288\n"
MF "v[164:167],a[208:211],a[72:75],v[164:167]\n"
"ds_read_b128 a[144:147],v2 offset:43008\n"
MF "v[164:167],a[212:215],a[112:115],v[164:167]\n"
TR "a[204:205],v0 offset:36872\n"
MF "v[156:159],a[212:215],a[108:111],v[156:159]\n"
TR "a[206:207],v0 offset:37384\n"
MF "v[156:159],a[208:211],a[68:71],v[156:159]\n"
MF "v[148:151],a[208:211],a[64:67],v[148:151]\n"
"ds_read_b128 a[184:187],v3 offset:43008\n"
MF "v[148:151],a[212:215],a[104:107],v[148:151]\n"
MF "v[140:143],a[212:215],a[100:103],v[140:143]\n"
LD "v13,s[28:31],s82 offen offset:3072 lds\n"
MF "v[140:143],a[208:211],a[60:63],v[140:143]\n"
"s_waitcnt lgkmcnt(7)\n"
MF "v[176:179],a[216:219],a[60:63],v[176:179]\n"
TR "a[208:209],v1 offset:32768\n"
MF "v[176:179],a[220:223],a[100:103],v[176:179]\n"
TR "a[210:211],v1 offset:33280\n"
MF "v[184:187],a[220:223],a[104:107],v[184:187]\n"
"ds_read_b128 a[148:151],v2 offset:45056\n"
MF "v[184:187],a[216:219],a[64:67],v[184:187]\n"
"s_add_u32 s84,s14,20480\n"
TR "a[212:213],v1 offset:36864\n"
MF "v[192:195],a[216:219],a[68:71],v[192:195]\n"
"s_add_u32 m0,s63,22528\n"
TR "a[214:215],v1 offset:37376\n"
MF "v[192:195],a[220:223],a[108:111],v[192:195]\n"
LD "v4,s[24:27],s84 offen lds\n"
MF "v[200:203],a[220:223],a[112:115],v[200:203]\n"
"ds_read_b128 a[188:191],v3 offset:45056\n"
MF "v[200:203],a[216:219],a[72:75],v[200:203]\n"
".Lpg6c:\n"
"s_cbranch_execz .Lpg1a\n"
".Lpg6f:\n"
"s_cbranch_execz .Lpgt0k_f\n"
MF "v[208:211],a[216:219],a[76:79],v[208:211]\n"
"s_add_u32 s84,s14,29696\n"
MF "v[208:211],a[220:223],a[116:119],v[208:211]\n"
"s_waitcnt lgkmcnt(7)\n"
LD "v5,s[24:27],s84 offen offset:1024 lds\n"
MF "v[212:215],a[204:207],a[116:119],v[212:215]\n"
TR "a[216:217],v1 offset:32776\n"
MF "v[212:215],a[200:203],a[76:79],v[212:215]\n"
TR "a[218:219],v1 offset:33288\n"
MF "v[204:207],a[200:203],a[72:75],v[204:207]\n"
"ds_read_b128 a[152:155],v2 offset:47104\n"
MF "v[204:207],a[204:207],a[112:115],v[204:207]\n"
TR "a[220:221],v1 offset:36872\n"
MF "v[196:199],a[204:207],a[108:111],v[196:199]\n"
TR "a[222:223],v1 offset:37384\n"
MF "v[196:199],a[200:203],a[68:71],v[196:199]\n"
MF "v[188:191],a[200:203],a[64:67],v[188:191]\n"
"v_add_u32 v0,0xc000,v0\n"
MF "v[188:191],a[204:207],a[104:107],v[188:191]\n"
"v_add_u32 v1,0xc000,v1\n"
MF "v[180:183],a[204:207],a[100:103],v[180:183]\n"
"s_waitcnt vmcnt(25) lgkmcnt(0)\n"
MF "v[180:183],a[200:203],a[60:63],v[180:183]\n"
"s_barrier\n"
MF "v[216:219],a[208:211],a[60:63],v[216:219]\n"
TR "a[200:201],v0\n"
MF "v[216:219],a[212:215],a[100:103],v[216:219]\n"
"s_add_u32 m0,s62,32768\n"
TR "a[202:203],v0 offset:512\n"
MF "v[224:227],a[212:215],a[104:107],v[224:227]\n"
"s_add_u32 s82,s66,4718848\n"
"ds_read_b128 a[192:195],v3 offset:47104\n"
MF "v[224:227],a[208:211],a[64:67],v[224:227]\n"
"s_sub_u32 s15,s82,7864320\n"
TR "a[204:205],v0 offset:4096\n"
MF "v[232:235],a[208:211],a[68:71],v[232:235]\n"
"s_min_u32 s82,s82,s15\n"
TR "a[206:207],v0 offset:4608\n"
MF "v[232:235],a[212:215],a[108:111],v[232:235]\n"
LD "v6,s[28:31],s82 offen lds\n"
MF "v[240:243],a[212:215],a[112:115],v[240:243]\n"
"ds_read_b128 a[156:159],v2 offset:49152\n"
MF "v[240:243],a[208:211],a[72:75],v[240:243]\n"
"ds_read_b128 a[196:199],v3 offset:49152\n"
MF "v[248:251],a[208:211],a[76:79],v[248:251]\n"
LD "v11,s[28:31],s82 offen offset:1024 lds\n"
MF "v[248:251],a[212:215],a[116:119],v[248:251]\n"
MF "v[252:255],a[220:223],a[116:119],v[252:255]\n"
TR "a[208:209],v0 offset:8\n"
MF "v[252:255],a[216:219],a[76:79],v[252:255]\n"
TR "a[210:211],v0 offset:520\n"
MF "v[244:247],a[216:219],a[72:75],v[244:247]\n"
MF "v[244:247],a[220:223],a[112:115],v[244:247]\n"
TR "a[212:213],v0 offset:4104\n"
MF "v[236:239],a[220:223],a[108:111],v[236:239]\n"
TR "a[214:215],v0 offset:4616\n"
MF "v[236:239],a[216:219],a[68:71],v[236:239]\n"
LD "v12,s[28:31],s82 offen offset:2048 lds\n"
MF "v[228:231],a[216:219],a[64:67],v[228:231]\n"
MF "v[228:231],a[220:223],a[104:107],v[228:231]\n"
MF "v[220:223],a[220:223],a[100:103],v[220:223]\n"
MF "v[220:223],a[216:219],a[60:63],v[220:223]\n"
"s_waitcnt lgkmcnt(4)\n"
MF "v[16:19],a[200:203],a[140:143],v[16:19]\n"
TR "a[216:217],v1\n"
MF "v[16:19],a[204:207],a[180:183],v[16:19]\n"
TR "a[218:219],v1 offset:512\n"
MF "v[24:27],a[204:207],a[184:187],v[24:27]\n"
LD "v13,s[28:31],s82 offen offset:3072 lds\n"
MF "v[24:27],a[200:203],a[144:147],v[24:27]\n"
TR "a[220:221],v1 offset:4096\n"
MF "v[32:35],a[200:203],a[148:151],v[32:35]\n"
TR "a[222:223],v1 offset:4608\n"
MF "v[32:35],a[204:207],a[188:191],v[32:35]\n"
MF "v[40:43],a[204:207],a[192:195],v[40:43]\n"
MF "v[40:43],a[200:203],a[152:155],v[40:43]\n"
"s_add_u32 s84,s14,40960\n"
MF "v[48:51],a[200:203],a[156:159],v[48:51]\n"
"s_add_u32 m0,s63,24576\n"
MF "v[48:51],a[204:207],a[196:199],v[48:51]\n"
"s_waitcnt lgkmcnt(4)\n"
LD "v4,s[24:27],s84 offen lds\n"
MF "v[52:55],a[212:215],a[196:199],v[52:55]\n"
TR "a[200:201],v1 offset:8\n"
MF "v[52:55],a[208:211],a[156:159],v[52:55]\n"
TR "a[202:203],v1 offset:520\n"
MF "v[44:47],a[208:211],a[152:155],v[44:47]\n"
MF "v[44:47],a[212:215],a[192:195],v[44:47]\n"
TR "a[204:205],v1 offset:4104\n"
MF "v[36:39],a[212:215],a[188:191],v[36:39]\n"
TR "a[206:207],v1 offset:4616\n"
MF "v[36:39],a[208:211],a[148:151],v[36:39]\n"
MF "v[28:31],a[208:211],a[144:147],v[28:31]\n"
MF "v[28:31],a[212:215],a[184:187],v[28:31]\n"
MF "v[20:23],a[212:215],a[180:183],v[20:23]\n"
"s_waitcnt vmcnt(24) lgkmcnt(0)\n"
MF "v[20:23],a[208:211],a[140:143],v[20:23]\n"
"s_barrier\n"
MF "v[56:59],a[216:219],a[140:143],v[56:59]\n"
"s_add_u32 m0,s62,49152\n"
"s_add_u32 s82,s66,5505024\n"
TR "a[208:209],v0 offset:16384\n"
MF "v[56:59],a[220:223],a[180:183],v[56:59]\n"
"s_sub_u32 s15,s82,7864320\n"
"s_min_u32 s82,s82,s15\n"
TR "a[210:211],v0 offset:16896\n"
MF "v[64:67],a[220:223],a[184:187],v[64:67]\n"
LD "v6,s[28:31],s82 offen lds\n"
MF "v[64:67],a[216:219],a[144:147],v[64:67]\n"
TR "a[212:213],v0 offset:20480\n"
MF "v[72:75],a[216:219],a[148:151],v[72:75]\n"
TR "a[214:215],v0 offset:20992\n"
MF "v[72:75],a[220:223],a[188:191],v[72:75]\n"
MF "v[80:83],a[220:223],a[192:195],v[80:83]\n"
LD "v11,s[28:31],s82 offen offset:1024 lds\n"
MF "v[80:83],a[216:219],a[152:155],v[80:83]\n"
MF "v[88:91],a[216:219],a[156:159],v[88:91]\n"
MF "v[88:91],a[220:223],a[196:199],v[88:91]\n"
MF "v[92:95],a[204:207],a[196:199],v[92:95]\n"
TR "a[216:217],v0 offset:16392\n"
MF "v[92:95],a[200:203],a[156:159],v[92:95]\n"
TR "a[218:219],v0 offset:16904\n"
MF "v[84:87],a[200:203],a[152:155],v[84:87]\n"
LD "v12,s[28:31],s82 offen offset:2048 lds\n"
MF "v[84:87],a[204:207],a[192:195],v[84:87]\n"
TR "a[220:221],v0 offset:20488\n"
MF "v[76:79],a[204:207],a[188:191],v[76:79]\n"
TR "a[222:223],v0 offset:21000\n"
MF "v[76:79],a[200:203],a[148:151],v[76:79]\n"
MF "v[68:71],a[200:203],a[144:147],v[68:71]\n"
MF "v[68:71],a[204:207],a[184:187],v[68:71]\n"
MF "v[60:63],a[204:207],a[180:183],v[60:63]\n"
LD "v13,s[28:31],s82 offen offset:3072 lds\n"
MF "v[60:63],a[200:203],a[140:143],v[60:63]\n"
"s_waitcnt lgkmcnt(4)\n"
MF "v[96:99],a[208:211],a[140:143],v[96:99]\n"
TR "a[200:201],v1 offset:16384\n"
MF "v[96:99],a[212:215],a[180:183],v[96:99]\n"
TR "a[202:203],v1 offset:16896\n"
MF "v[104:107],a[212:215],a[184:187],v[104:107]\n"
"s_add_u32 s14,s81,1024\n"
"s_sub_u32 s15,s14,1280\n"
MF "v[104:107],a[208:211],a[144:147],v[104:107]\n"
"s_min_u32 s14,s14,s15\n"
"s_add_u32 s14,s14,s68\n"
TR "a[204:205],v1 offset:20480\n"
MF "v[112:115],a[208:211],a[148:151],v[112:115]\n"
"s_add_u32 s84,s14,0\n"
"s_add_u32 m0,s63,40960\n"
TR "a[206:207],v1 offset:20992\n"
MF "v[112:115],a[212:215],a[188:191],v[112:115]\n"
LD "v4,s[24:27],s84 offen lds\n"
MF "v[120:123],a[212:215],a[192:195],v[120:123]\n"
MF "v[120:123],a[208:211],a[152:155],v[120:123]\n"
MF "v[128:131],a[208:211],a[156:159],v[128:131]\n"
"s_add_u32 s84,s14,9216\n"
MF "v[128:131],a[212:215],a[196:199],v[128:131]\n"
"s_waitcnt lgkmcnt(4)\n"
LD "v5,s[24:27],s84 offen offset:1024 lds\n"
MF "v[132:135],a[220:223],a[196:199],v[132:135]\n"
TR "a[208:209],v1 offset:16392\n"
MF "v[132:135],a[216:219],a[156:159],v[132:135]\n"
TR "a[210:211],v1 offset:16904\n"
MF "v[124:127],a[216:219],a[152:155],v[124:127]\n"
MF "v[124:127],a[220:223],a[192:195],v[124:127]\n"
TR "a[212:213],v1 offset:20488\n"
MF "v[116:119],a[220:223],a[188:191],v[116:119]\n"
TR "a[214:215],v1 offset:21000\n"
MF "v[116:119],a[216:219],a[148:151],v[116:119]\n"
MF "v[108:111],a[216:219],a[144:147],v[108:111]\n"
MF "v[108:111],a[220:223],a[184:187],v[108:111]\n"
MF "v[100:103],a[220:223],a[180:183],v[100:103]\n"
"s_waitcnt vmcnt(23) lgkmcnt(0)\n"
MF "v[100:103],a[216:219],a[140:143],v[100:103]\n"
"s_barrier\n"
"s_cmp_eq_u32 s57,1\n"
"s_cbranch_scc0 .Lhs12\n"
"v_bfe_u32 v14,v9,4,5\n"
"v_lshlrev_b32 v14,6,v14\n"
"s_lshl_b32 s58,s78,2\n"
"s_add_u32 s58,s58,s86\n"
"buffer_load_dword v10,v14,s[40:43],s58 offen offset:128 sc1\n"
".Lhs12:\n"
MF "v[136:139],a[200:203],a[140:143],v[136:139]\n"
TR "a[216:217],v0 offset:32768\n"
MF "v[136:139],a[204:207],a[180:183],v[136:139]\n"
"s_add_u32 m0,s62,65536\n"
TR "a[218:219],v0 offset:33280\n"
MF "v[144:147],a[204:207],a[184:187],v[144:147]\n"
"s_add_u32 s82,s66,5505152\n"
"ds_read_b128 a[60:63],v2\n"
MF "v[144:147],a[200:203],a[144:147],v[144:147]\n"
"s_sub_u32 s15,s82,7864320\n"
TR "a[220:221],v0 offset:36864\n"
MF "v[152:155],a[200:203],a[148:151],v[152:155]\n"
"s_min_u32 s82,s82,s15\n"
TR "a[222:223],v0 offset:37376\n"
MF "v[152:155],a[204:207],a[188:191],v[152:155]\n"
LD "v6,s[28:31],s82 offen lds\n"
MF "v[160:163],a[204:207],a[192:195],v[160:163]\n"
"ds_read_b128 a[100:103],v3\n"
MF "v[160:163],a[200:203],a[152:155],v[160:163]\n"
LD "v11,s[28:31],s82 offen offset:1024 lds\n"
MF "v[168:171],a[200:203],a[156:159],v[168:171]\n"
MF "v[168:171],a[204:207],a[196:199],v[168:171]\n"
LD "v12,s[28:31],s82 offen offset:2048 lds\n"
MF "v[172:175],a[212:215],a[196:199],v[172:175]\n"
TR "a[200:201],v0 offset:32776\n"
MF "v[172:175],a[208:211],a[156:159],v[172:175]\n"
TR "a[202:203],v0 offset:33288\n"
MF "v[164:167],a[208:211],a[152:155],v[164:167]\n"
"ds_read_b128 a[64:67],v2 offset:2048\n"
MF "v[164:167],a[212:215],a[192:195],v[164:167]\n"
TR "a[204:205],v0 offset:36872\n"
MF "v[156:159],a[212:215],a[188:191],v[156:159]\n"
TR "a[206:207],v0 offset:37384\n"
MF "v[156:159],a[208:211],a[148:151],v[156:159]\n"
MF "v[148:151],a[208:211],a[144:147],v[148:151]\n"
"ds_read_b128 a[104:107],v3 offset:2048\n"
MF "v[148:151],a[212:215],a[184:187],v[148:151]\n"
MF "v[140:143],a[212:215],a[180:183],v[140:143]\n"
LD "v13,s[28:31],s82 offen offset:3072 lds\n"
MF "v[140:143],a[208:211],a[140:143],v[140:143]\n"
"s_waitcnt lgkmcnt(7)\n"
MF "v[176:179],a[216:219],a[140:143],v[176:179]\n"
TR "a[208:209],v1 offset:32768\n"
MF "v[176:179],a[220:223],a[180:183],v[176:179]\n"
TR "a[210:211],v1 offset:33280\n"
MF "v[184:187],a[220:223],a[184:187],v[184:187]\n"
"ds_read_b128 a[68:71],v2 offset:4096\n"
MF "v[184:187],a[216:219],a[144:147],v[184:187]\n"
"s_add_u32 s84,s14,20480\n"
TR "a[212:213],v1 offset:36864\n"
MF "v[192:195],a[216:219],a[148:151],v[192:195]\n"
"s_add_u32 m0,s63,43008\n"
TR "a[214:215],v1 offset:37376\n"
MF "v[192:195],a[220:223],a[188:191],v[192:195]\n"
LD "v4,s[24:27],s84 offen lds\n"
MF "v[200:203],a[220:223],a[192:195],v[200:203]\n"
"ds_read_b128 a[108:111],v3 offset:4096\n"
MF "v[200:203],a[216:219],a[152:155],v[200:203]\n"
MF "v[208:211],a[216:219],a[156:159],v[208:211]\n"
"s_add_u32 s84,s14,29696\n"
MF "v[208:211],a[220:223],a[196:199],v[208:211]\n"
"s_waitcnt lgkmcnt(7)\n"
LD "v5,s[24:27],s84 offen offset:1024 lds\n"
MF "v[212:215],a[204:207],a[196:199],v[212:215]\n"
TR "a[216:217],v1 offset:32776\n"
MF "v[212:215],a[200:203],a[156:159],v[212:215]\n"
TR "a[218:219],v1 offset:33288\n"
MF "v[204:207],a[200:203],a[152:155],v[204:207]\n"
"ds_read_b128 a[72:75],v2 offset:6144\n"
MF "v[204:207],a[204:207],a[192:195],v[204:207]\n"
TR "a[220:221],v1 offset:36872\n"
MF "v[196:199],a[204:207],a[188:191],v[196:199]\n"
TR "a[222:223],v1 offset:37384\n"
MF "v[196:199],a[200:203],a[148:151],v[196:199]\n"
MF "v[188:191],a[200:203],a[144:147],v[188:191]\n"
"v_add_u32 v0,0xffff4000,v0\n"
MF "v[188:191],a[204:207],a[184:187],v[188:191]\n"
"v_add_u32 v1,0xffff4000,v1\n"
MF "v[180:183],a[204:207],a[180:183],v[180:183]\n"
"s_waitcnt vmcnt(25) lgkmcnt(0)\n"
MF "v[180:183],a[200:203],a[140:143],v[180:183]\n"
"s_barrier\n"
MF "v[216:219],a[208:211],a[140:143],v[216:219]\n"
TR "a[200:201],v0\n"
MF "v[216:219],a[212:215],a[180:183],v[216:219]\n"
"s_add_u32 m0,s62,81920\n"
TR "a[202:203],v0 offset:512\n"
MF "v[224:227],a[212:215],a[184:187],v[224:227]\n"
"s_add_u32 s82,s66,5505280\n"
"ds_read_b128 a[112:115],v3 offset:6144\n"
MF "v[224:227],a[208:211],a[144:147],v[224:227]\n"
"s_sub_u32 s15,s82,7864320\n"
TR "a[204:205],v0 offset:4096\n"
MF "v[232:235],a[208:211],a[148:151],v[232:235]\n"
"s_min_u32 s82,s82,s15\n"
TR "a[206:207],v0 offset:4608\n"
MF "v[232:235],a[212:215],a[188:191],v[232:235]\n"
LD "v6,s[28:31],s82 offen lds\n"
MF "v[240:243],a[212:215],a[192:195],v[240:243]\n"
"ds_read_b128 a[76:79],v2 offset:8192\n"
MF "v[240:243],a[208:211],a[152:155],v[240:243]\n"
"ds_read_b128 a[116:119],v3 offset:8192\n"
MF "v[248:251],a[208:211],a[156:159],v[248:251]\n"
LD "v11,s[28:31],s82 offen offset:1024 lds\n"
MF "v[248:251],a[212:215],a[196:199],v[248:251]\n"
MF "v[252:255],a[220:223],a[196:199],v[252:255]\n"
TR "a[208:209],v0 offset:8\n"
MF "v[252:255],a[216:219],a[156:159],v[252:255]\n"
TR "a[210:211],v0 offset:520\n"
MF "v[244:247],a[216:219],a[152:155],v[244:247]\n"
MF "v[244:247],a[220:223],a[192:195],v[244:247]\n"
TR "a[212:213],v0 offset:4104\n"
MF "v[236:239],a[220:223],a[188:191],v[236:239]\n"
TR "a[214:215],v0 offset:4616\n"
MF "v[236:239],a[216:219],a[148:151],v[236:239]\n"
LD "v12,s[28:31],s82 offen offset:2048 lds\n"
MF "v[228:231],a[216:219],a[144:147],v[228:231]\n"
MF "v[228:231],a[220:223],a[184:187],v[228:231]\n"
MF "v[220:223],a[220:223],a[180:183],v[220:223]\n"
MF "v[220:223],a[216:219],a[140:143],v[220:223]\n"
"s_waitcnt lgkmcnt(4)\n"
MF "v[16:19],a[200:203],a[60:63],v[16:19]\n"
TR "a[216:217],v1\n"
MF "v[16:19],a[204:207],a[100:103],v[16:19]\n"
TR "a[218:219],v1 offset:512\n"
MF "v[24:27],a[204:207],a[104:107],v[24:27]\n"
LD "v13,s[28:31],s82 offen offset:3072 lds\n"
MF "v[24:27],a[200:203],a[64:67],v[24:27]\n"
TR "a[220:221],v1 offset:4096\n"
MF "v[32:35],a[200:203],a[68:71],v[32:35]\n"
TR "a[222:223],v1 offset:4608\n"
MF "v[32:35],a[204:207],a[108:111],v[32:35]\n"
MF "v[40:43],a[204:207],a[112:115],v[40:43]\n"
MF "v[40:43],a[200:203],a[72:75],v[40:43]\n"
"s_add_u32 s84,s14,40960\n"
MF "v[48:51],a[200:203],a[76:79],v[48:51]\n"
"s_add_u32 m0,s63,45056\n"
MF "v[48:51],a[204:207],a[116:119],v[48:51]\n"
"s_waitcnt lgkmcnt(4)\n"
LD "v4,s[24:27],s84 offen lds\n"
MF "v[52:55],a[212:215],a[116:119],v[52:55]\n"
TR "a[200:201],v1 offset:8\n"
MF "v[52:55],a[208:211],a[76:79],v[52:55]\n"
TR "a[202:203],v1 offset:520\n"
MF "v[44:47],a[208:211],a[72:75],v[44:47]\n"
MF "v[44:47],a[212:215],a[112:115],v[44:47]\n"
TR "a[204:205],v1 offset:4104\n"
MF "v[36:39],a[212:215],a[108:111],v[36:39]\n"
TR "a[206:207],v1 offset:4616\n"
MF "v[36:39],a[208:211],a[68:71],v[36:39]\n"
MF "v[28:31],a[208:211],a[64:67],v[28:31]\n"
MF "v[28:31],a[212:215],a[104:107],v[28:31]\n"
MF "v[20:23],a[212:215],a[100:103],v[20:23]\n"
"s_waitcnt vmcnt(24) lgkmcnt(0)\n"
MF "v[20:23],a[208:211],a[60:63],v[20:23]\n"
"s_barrier\n"
MF "v[56:59],a[216:219],a[60:63],v[56:59]\n"
"s_add_u32 m0,s62,0\n"
"s_add_u32 s82,s66,6291712\n"
TR "a[208:209],v0 offset:16384\n"
MF "v[56:59],a[220:223],a[100:103],v[56:59]\n"
"s_sub_u32 s15,s82,7864320\n"
"s_min_u32 s82,s82,s15\n"
TR "a[210:211],v0 offset:16896\n"
MF "v[64:67],a[220:223],a[104:107],v[64:67]\n"
LD "v6,s[28:31],s82 offen lds\n"
MF "v[64:67],a[216:219],a[64:67],v[64:67]\n"
TR "a[212:213],v0 offset:20480\n"
MF "v[72:75],a[216:219],a[68:71],v[72:75]\n"
TR "a[214:215],v0 offset:20992\n"
MF "v[72:75],a[220:223],a[108:111],v[72:75]\n"
MF "v[80:83],a[220:223],a[112:115],v[80:83]\n"
LD "v11,s[28:31],s82 offen offset:1024 lds\n"
MF "v[80:83],a[216:219],a[72:75],v[80:83]\n"
MF "v[88:91],a[216:219],a[76:79],v[88:91]\n"
MF "v[88:91],a[220:223],a[116:119],v[88:91]\n"
MF "v[92:95],a[204:207],a[116:119],v[92:95]\n"
TR "a[216:217],v0 offset:16392\n"
MF "v[92:95],a[200:203],a[76:79],v[92:95]\n"
TR "a[218:219],v0 offset:16904\n"
MF "v[84:87],a[200:203],a[72:75],v[84:87]\n"
LD "v12,s[28:31],s82 offen offset:2048 lds\n"
MF "v[84:87],a[204:207],a[112:115],v[84:87]\n"
TR "a[220:221],v0 offset:20488\n"
MF "v[76:79],a[204:207],a[108:111],v[76:79]\n"
TR "a[222:223],v0 offset:21000\n"
MF "v[76:79],a[200:203],a[68:71],v[76:79]\n"
MF "v[68:71],a[200:203],a[64:67],v[68:71]\n"
LD "v13,s[28:31],s82 offen offset:3072 lds\n"
MF "v[68:71],a[204:207],a[104:107],v[68:71]\n"
MF "v[60:63],a[204:207],a[100:103],v[60:63]\n"
MF "v[60:63],a[200:203],a[60:63],v[60:63]\n"
"s_add_u32 s14,s81,1152\n"
"s_sub_u32 s15,s14,1280\n"
"s_waitcnt lgkmcnt(4)\n"
MF "v[96:99],a[208:211],a[60:63],v[96:99]\n"
"s_min_u32 s14,s14,s15\n"
"s_add_u32 s14,s14,s68\n"
TR "a[200:201],v1 offset:16384\n"
MF "v[96:99],a[212:215],a[100:103],v[96:99]\n"
"s_add_u32 s84,s14,0\n"
"s_add_u32 m0,s63,0\n"
TR "a[202:203],v1 offset:16896\n"
MF "v[104:107],a[212:215],a[104:107],v[104:107]\n"
LD "v4,s[24:27],s84 offen lds\n"
MF "v[104:107],a[208:211],a[64:67],v[104:107]\n"
TR "a[204:205],v1 offset:20480\n"
MF "v[112:115],a[208:211],a[68:71],v[112:115]\n"
TR "a[206:207],v1 offset:20992\n"
MF "v[112:115],a[212:215],a[108:111],v[112:115]\n"
"s_add_u32 s84,s14,9216\n"
MF "v[120:123],a[212:215],a[112:115],v[120:123]\n"
LD "v5,s[24:27],s84 offen offset:1024 lds\n"
MF "v[120:123],a[208:211],a[72:75],v[120:123]\n"
MF "v[128:131],a[208:211],a[76:79],v[128:131]\n"
MF "v[128:131],a[212:215],a[116:119],v[128:131]\n"
"s_waitcnt lgkmcnt(4)\n"
MF "v[132:135],a[220:223],a[116:119],v[132:135]\n"
TR "a[208:209],v1 offset:16392\n"
MF "v[132:135],a[216:219],a[76:79],v[132:135]\n"
"s_add_u32 s84,s14,18432\n"
TR "a[210:211],v1 offset:16904\n"
MF "v[124:127],a[216:219],a[72:75],v[124:127]\n"
LD "v4,s[24:27],s84 offen offset:2048 lds\n"
MF "v[124:127],a[220:223],a[112:115],v[124:127]\n"
TR "a[212:213],v1 offset:20488\n"
MF "v[116:119],a[220:223],a[108:111],v[116:119]\n"
TR "a[214:215],v1 offset:21000\n"
MF "v[116:119],a[216:219],a[68:71],v[116:119]\n"
MF "v[108:111],a[216:219],a[64:67],v[108:111]\n"
MF "v[108:111],a[220:223],a[104:107],v[108:111]\n"
MF "v[100:103],a[220:223],a[100:103],v[100:103]\n"
"s_waitcnt vmcnt(24) lgkmcnt(0)\n"
MF "v[100:103],a[216:219],a[60:63],v[100:103]\n"
"s_barrier\n"
MF "v[136:139],a[200:203],a[60:63],v[136:139]\n"
TR "a[216:217],v0 offset:32768\n"
MF "v[136:139],a[204:207],a[100:103],v[136:139]\n"
"s_add_u32 m0,s62,16384\n"
TR "a[218:219],v0 offset:33280\n"
MF "v[144:147],a[204:207],a[104:107],v[144:147]\n"
"s_add_u32 s82,s66,7078144\n"
"ds_read_b128 a[140:143],v2 offset:20480\n"
MF "v[144:147],a[200:203],a[64:67],v[144:147]\n"
"s_sub_u32 s15,s82,7864320\n"
TR "a[220:221],v0 offset:36864\n"
MF "v[152:155],a[200:203],a[68:71],v[152:155]\n"
"s_min_u32 s82,s82,s15\n"
TR "a[222:223],v0 offset:37376\n"
MF "v[152:155],a[204:207],a[108:111],v[152:155]\n"
LD "v6,s[28:31],s82 offen lds\n"
MF "v[160:163],a[204:207],a[112:115],v[160:163]\n"
"ds_read_b128 a[180:183],v3 offset:20480\n"
MF "v[160:163],a[200:203],a[72:75],v[160:163]\n"
LD "v11,s[28:31],s82 offen offset:1024 lds\n"
MF "v[168:171],a[200:203],a[76:79],v[168:171]\n"
MF "v[168:171],a[204:207],a[116:119],v[168:171]\n"
LD "v12,s[28:31],s82 offen offset:2048 lds\n"
MF "v[172:175],a[212:215],a[116:119],v[172:175]\n"
TR "a[200:201],v0 offset:32776\n"
MF "v[172:175],a[208:211],a[76:79],v[172:175]\n"
TR "a[202:203],v0 offset:33288\n"
MF "v[164:167],a[208:211],a[72:75],v[164:167]\n"
"ds_read_b128 a[144:147],v2 offset:22528\n"
MF "v[164:167],a[212:215],a[112:115],v[164:167]\n"
TR "a[204:205],v0 offset:36872\n"
MF "v[156:159],a[212:215],a[108:111],v[156:159]\n"
TR "a[206:207],v0 offset:37384\n"
MF "v[156:159],a[208:211],a[68:71],v[156:159]\n"
MF "v[148:151],a[208:211],a[64:67],v[148:151]\n"
"ds_read_b128 a[184:187],v3 offset:22528\n"
MF "v[148:151],a[212:215],a[104:107],v[148:151]\n"
MF "v[140:143],a[212:215],a[100:103],v[140:143]\n"
LD "v13,s[28:31],s82 offen offset:3072 lds\n"
MF "v[140:143],a[208:211],a[60:63],v[140:143]\n"
"s_waitcnt lgkmcnt(7)\n"
MF "v[176:179],a[216:219],a[60:63],v[176:179]\n"
TR "a[208:209],v1 offset:32768\n"
MF "v[176:179],a[220:223],a[100:103],v[176:179]\n"
TR "a[210:211],v1 offset:33280\n"
MF "v[184:187],a[220:223],a[104:107],v[184:187]\n"
"ds_read_b128 a[148:151],v2 offset:24576\n"
MF "v[184:187],a[216:219],a[64:67],v[184:187]\n"
"s_add_u32 s84,s14,30720\n"
TR "a[212:213],v1 offset:36864\n"
MF "v[192:195],a[216:219],a[68:71],v[192:195]\n"
"s_add_u32 m0,s63,3072\n"
TR "a[214:215],v1 offset:37376\n"
MF "v[192:195],a[220:223],a[108:111],v[192:195]\n"
LD "v5,s[24:27],s84 offen lds\n"
MF "v[200:203],a[220:223],a[112:115],v[200:203]\n"
"ds_read_b128 a[188:191],v3 offset:24576\n"
MF "v[200:203],a[216:219],a[72:75],v[200:203]\n"
MF "v[208:211],a[216:219],a[76:79],v[208:211]\n"
"s_add_u32 s84,s14,39936\n"
MF "v[208:211],a[220:223],a[116:119],v[208:211]\n"
"s_waitcnt lgkmcnt(7)\n"
LD "v4,s[24:27],s84 offen offset:1024 lds\n"
MF "v[212:215],a[204:207],a[116:119],v[212:215]\n"
TR "a[216:217],v1 offset:32776\n"
MF "v[212:215],a[200:203],a[76:79],v[212:215]\n"
TR "a[218:219],v1 offset:33288\n"
MF "v[204:207],a[200:203],a[72:75],v[204:207]\n"
"ds_read_b128 a[152:155],v2 offset:26624\n"
MF "v[204:207],a[204:207],a[112:115],v[204:207]\n"
TR "a[220:221],v1 offset:36872\n"
MF "v[196:199],a[204:207],a[108:111],v[196:199]\n"
TR "a[222:223],v1 offset:37384\n"
MF "v[196:199],a[200:203],a[68:71],v[196:199]\n"
MF "v[188:191],a[200:203],a[64:67],v[188:191]\n"
"v_add_u32 v0,0xc000,v0\n"
MF "v[188:191],a[204:207],a[104:107],v[188:191]\n"
"v_add_u32 v1,0xc000,v1\n"
MF "v[180:183],a[204:207],a[100:103],v[180:183]\n"
"s_waitcnt vmcnt(26) lgkmcnt(0)\n"
".Lpgqj_a:\n"
"s_cbranch_execz .Lisr1\n"
".Lqj:\n"
MF "v[180:183],a[200:203],a[60:63],v[180:183]\n"
"s_barrier\n"
"s_getpc_b64 s[52:53]\n"
".Lctq:\n"
"s_sub_u32 s52,s52,.Lctq-.Lcode0\n"
"s_subb_u32 s53,s53,0\n"
"s_bfe_u32 s58,s23,0x50000\n"
"s_lshl_b32 s58,s58,2\n"
"s_add_u32 s58,s58,s56\n"
"s_nop 0\n"
"s_mul_i32 s58,s58,1280\n"
"v_min_u32 v15,144,v9\n"
"v_lshl_add_u32 v15,v15,3,s58\n"
"v_min_u32 v15,.Lcode1-.Lcode0,v15\n"
"global_load_dword a249,v15,s[52:53]\n"
MF "v[216:219],a[208:211],a[60:63],v[216:219]\n"
TR "a[200:201],v0\n"
MF "v[216:219],a[212:215],a[100:103],v[216:219]\n"
"s_add_u32 m0,s62,32768\n"
TR "a[202:203],v0 offset:512\n"
MF "v[224:227],a[212:215],a[104:107],v[224:227]\n"
"s_add_u32 s82,s66,6291584\n"
"ds_read_b128 a[192:195],v3 offset:26624\n"
MF "v[224:227],a[208:211],a[64:67],v[224:227]\n"
"s_sub_u32 s15,s82,7864320\n"
TR "a[204:205],v0 offset:4096\n"
MF "v[232:235],a[208:211],a[68:71],v[232:235]\n"
"s_min_u32 s82,s82,s15\n"
TR "a[206:207],v0 offset:4608\n"
MF "v[232:235],a[212:215],a[108:111],v[232:235]\n"
LD "v6,s[28:31],s82 offen lds\n"
MF "v[240:243],a[212:215],a[112:115],v[240:243]\n"
"ds_read_b128 a[156:159],v2 offset:28672\n"
MF "v[240:243],a[208:211],a[72:75],v[240:243]\n"
"ds_read_b128 a[196:199],v3 offset:28672\n"
MF "v[248:251],a[208:211],a[76:79],v[248:251]\n"
MF "v[248:251],a[212:215],a[116:119],v[248:251]\n"
LD "v11,s[28:31],s82 offen offset:1024 lds\n"
MF "v[252:255],a[220:223],a[116:119],v[252:255]\n"
TR "a[208:209],v0 offset:8\n"
MF "v[252:255],a[216:219],a[76:79],v[252:255]\n"
".Lpg7a:\n"
"s_cbranch_execz .Lisr1\n"
TR "a[210:211],v0 offset:520\n"
MF "v[244:247],a[216:219],a[72:75],v[244:247]\n"
MF "v[244:247],a[220:223],a[112:115],v[244:247]\n"
TR "a[212:213],v0 offset:4104\n"
MF "v[236:239],a[220:223],a[108:111],v[236:239]\n"
TR "a[214:215],v0 offset:4616\n"
MF "v[236:239],a[216:219],a[68:71],v[236:239]\n"
MF "v[228:231],a[216:219],a[64:67],v[228:231]\n"
MF "v[228:231],a[220:223],a[104:107],v[228:231]\n"
MF "v[220:223],a[220:223],a[100:103],v[220:223]\n"
LD "v12,s[28:31],s82 offen offset:2048 lds\n"
MF "v[220:223],a[216:219],a[60:63],v[220:223]\n"
"s_waitcnt lgkmcnt(4)\n"
MF "v[16:19],a[200:203],a[140:143],v[16:19]\n"
TR "a[216:217],v1\n"
MF "v[16:19],a[204:207],a[180:183],v[16:19]\n"
TR "a[218:219],v1 offset:512\n"
MF "v[24:27],a[204:207],a[184:187],v[24:27]\n"
MF "v[24:27],a[200:203],a[144:147],v[24:27]\n"
TR "a[220:221],v1 offset:4096\n"
MF "v[32:35],a[200:203],a[148:151],v[32:35]\n"
TR "a[222:223],v1 offset:4608\n"
MF "v[32:35],a[204:207],a[188:191],v[32:35]\n"
MF "v[40:43],a[204:207],a[192:195],v[40:43]\n"
MF "v[40:43],a[200:203],a[152:155],v[40:43]\n"
LD "v13,s[28:31],s82 offen offset:3072 lds\n"
MF "v[48:51],a[200:203],a[156:159],v[48:51]\n"
"s_memtime s[52:53]\n"
"s_memrealtime s[100:101]\n"
MF "v[48:51],a[204:207],a[196:199],v[48:51]\n"
"s_waitcnt lgkmcnt(4)\n"
MF "v[52:55],a[212:215],a[196:199],v[52:55]\n"
TR "a[200:201],v1 offset:8\n"
MF "v[52:55],a[208:211],a[156:159],v[52:55]\n"
TR "a[202:203],v1 offset:520\n"
MF "v[44:47],a[208:211],a[152:155],v[44:47]\n"
MF "v[44:47],a[212:215],a[192:195],v[44:47]\n"
TR "a[204:205],v1 offset:4104\n"
MF "v[36:39],a[212:215],a[188:191],v[36:39]\n"
TR "a[206:207],v1 offset:4616\n"
MF "v[36:39],a[208:211],a[148:151],v[36:39]\n"
MF "v[28:31],a[208:211],a[144:147],v[28:31]\n"
MF "v[28:31],a[212:215],a[184:187],v[28:31]\n"
MF "v[20:23],a[212:215],a[180:183],v[20:23]\n"
"s_waitcnt vmcnt(25) lgkmcnt(0)\n"
MF "v[20:23],a[208:211],a[140:143],v[20:23]\n"
"s_barrier\n"
MF "v[56:59],a[216:219],a[140:143],v[56:59]\n"
"s_add_u32 m0,s62,49152\n"
"s_add_u32 s82,s66,7078016\n"
TR "a[208:209],v0 offset:16384\n"
MF "v[56:59],a[220:223],a[180:183],v[56:59]\n"
"s_sub_u32 s15,s82,7864320\n"
"s_min_u32 s82,s82,s15\n"
TR "a[210:211],v0 offset:16896\n"
MF "v[64:67],a[220:223],a[184:187],v[64:67]\n"
LD "v6,s[28:31],s82 offen lds\n"
MF "v[64:67],a[216:219],a[144:147],v[64:67]\n"
"s_sub_u32 s53,s52,s80\n"
TR "a[212:213],v0 offset:20480\n"
"v_lshrrev_b32 v15,8,v9\n"
MF "v[72:75],a[216:219],a[148:151],v[72:75]\n"
TR "a[214:215],v0 offset:20992\n"
"v_mul_u32_u24 v15,48960,v15\n"
MF "v[72:75],a[220:223],a[188:191],v[72:75]\n"
"s_lshr_b32 s7,s56,1\n"
"v_add_u32 v15,v15,v9\n"
MF "v[80:83],a[220:223],a[192:195],v[80:83]\n"
"s_and_b32 s8,s56,1\n"
MF "v[80:83],a[216:219],a[152:155],v[80:83]\n"
"s_sub_u32 s7,s7,s8\n"
MF "v[88:91],a[216:219],a[156:159],v[88:91]\n"
"s_mul_i32 s7,s7,245760\n"
MF "v[88:91],a[220:223],a[196:199],v[88:91]\n"
LD "v11,s[28:31],s82 offen offset:1024 lds\n"
MF "v[92:95],a[204:207],a[196:199],v[92:95]\n"
"s_add_u32 s7,s7,s89\n"
TR "a[216:217],v0 offset:16392\n"
MF "v[92:95],a[200:203],a[156:159],v[92:95]\n"
"s_cmp_gt_u32 s53,24100\n"
"s_cselect_b32 s101,1,0\n"
TR "a[218:219],v0 offset:16904\n"
MF "v[84:87],a[200:203],a[152:155],v[84:87]\n"
"s_cmp_eq_u32 s87,2\n"
"s_cselect_b32 s87,s101,s87\n"
MF "v[84:87],a[204:207],a[192:195],v[84:87]\n"
"s_cmp_eq_u32 s69,99\n"
"s_cselect_b32 s97,124,0\n"
TR "a[220:221],v0 offset:20488\n"
MF "v[76:79],a[204:207],a[188:191],v[76:79]\n"
"s_cmp_eq_u32 s57,0\n"
"s_cselect_b32 s97,145,s97\n"
TR "a[222:223],v0 offset:21000\n"
MF "v[76:79],a[200:203],a[148:151],v[76:79]\n"
"s_mul_i32 s97,s97,s87\n"
MF "v[68:71],a[200:203],a[144:147],v[68:71]\n"
"s_getreg_b32 s101,hwreg(HW_REG_XCC_ID,0,1)\n"
MF "v[68:71],a[204:207],a[184:187],v[68:71]\n"
"s_add_u32 s101,s101,s69\n"
MF "v[60:63],a[204:207],a[180:183],v[60:63]\n"
LD "v12,s[28:31],s82 offen offset:2048 lds\n"
MF "v[60:63],a[200:203],a[140:143],v[60:63]\n"
"s_waitcnt lgkmcnt(4)\n"
MF "v[96:99],a[208:211],a[140:143],v[96:99]\n"
"s_cmp_eq_u32 s57,s101\n"
"s_cselect_b32 s97,180,s97\n"
TR "a[200:201],v1 offset:16384\n"
MF "v[96:99],a[212:215],a[180:183],v[96:99]\n"
"s_cmp_eq_u32 s97,0\n"
"s_cselect_b32 s95,0x47800000,0\n"
TR "a[202:203],v1 offset:16896\n"
MF "v[104:107],a[212:215],a[184:187],v[104:107]\n"
"s_mov_b32 s21,s100\n"
MF "v[104:107],a[208:211],a[144:147],v[104:107]\n"
"s_mov_b32 s77,s95\n"
TR "a[204:205],v1 offset:20480\n"
MF "v[112:115],a[208:211],a[148:151],v[112:115]\n"
TR "a[206:207],v1 offset:20992\n"
MF "v[112:115],a[212:215],a[188:191],v[112:115]\n"
MF "v[120:123],a[212:215],a[192:195],v[120:123]\n"
MF "v[120:123],a[208:211],a[152:155],v[120:123]\n"
LD "v13,s[28:31],s82 offen offset:3072 lds\n"
MF "v[128:131],a[208:211],a[156:159],v[128:131]\n"
MF "v[128:131],a[212:215],a[196:199],v[128:131]\n"
"s_waitcnt lgkmcnt(4)\n"
MF "v[132:135],a[220:223],a[196:199],v[132:135]\n"
TR "a[208:209],v1 offset:16392\n"
MF "v[132:135],a[216:219],a[156:159],v[132:135]\n"
TR "a[210:211],v1 offset:16904\n"
MF "v[124:127],a[216:219],a[152:155],v[124:127]\n"
MF "v[124:127],a[220:223],a[192:195],v[124:127]\n"
TR "a[212:213],v1 offset:20488\n"
MF "v[116:119],a[220:223],a[188:191],v[116:119]\n"
TR "a[214:215],v1 offset:21000\n"
MF "v[116:119],a[216:219],a[148:151],v[116:119]\n"
MF "v[108:111],a[216:219],a[144:147],v[108:111]\n"
MF "v[108:111],a[220:223],a[184:187],v[108:111]\n"
MF "v[100:103],a[220:223],a[180:183],v[100:103]\n"
"s_waitcnt vmcnt(22) lgkmcnt(0)\n"
MF "v[100:103],a[216:219],a[140:143],v[100:103]\n"
"s_barrier\n"
"s_cmp_eq_u32 s57,1\n"
"s_cbranch_scc0 .Lhs15\n"
".Lack_q:\n"
"v_cmp_ne_u32 vcc,s76,v10\n"
"s_cbranch_vccnz .Lackspin_q\n"
".Lackok_q:\n"
".Lhs15:\n"
MF "v[136:139],a[200:203],a[140:143],v[136:139]\n"
"s_and_b32 s58,s56,1\n"
TR "a[216:217],v0 offset:32768\n"
MF "v[136:139],a[204:207],a[180:183],v[136:139]\n"
"s_add_u32 m0,s62,65536\n"
TR "a[218:219],v0 offset:33280\n"
MF "v[144:147],a[204:207],a[184:187],v[144:147]\n"
"s_add_u32 s82,s66,6291456\n"
"ds_read_b128 a[60:63],v2 offset:40960\n"
MF "v[144:147],a[200:203],a[144:147],v[144:147]\n"
"s_sub_u32 s15,s82,7864320\n"
TR "a[220:221],v0 offset:36864\n"
MF "v[152:155],a[200:203],a[148:151],v[152:155]\n"
"s_min_u32 s82,s82,s15\n"
TR "a[222:223],v0 offset:37376\n"
MF "v[152:155],a[204:207],a[188:191],v[152:155]\n"
LD "v6,s[28:31],s82 offen lds\n"
MF "v[160:163],a[204:207],a[192:195],v[160:163]\n"
"s_mul_i32 s59,s58,3072\n"
"ds_read_b128 a[100:103],v3 offset:40960\n"
MF "v[160:163],a[200:203],a[152:155],v[160:163]\n"
"s_add_u32 s16,s70,s59\n"
MF "v[168:171],a[200:203],a[156:159],v[168:171]\n"
"s_mul_i32 s59,s58,6144\n"
MF "v[168:171],a[204:207],a[196:199],v[168:171]\n"
LD "v11,s[28:31],s82 offen offset:1024 lds\n"
MF "v[172:175],a[212:215],a[196:199],v[172:175]\n"
"s_add_u32 s18,s70,s59\n"
TR "a[200:201],v0 offset:32776\n"
MF "v[172:175],a[208:211],a[156:159],v[172:175]\n"
"s_add_u32 s17,s16,196608\n"
TR "a[202:203],v0 offset:33288\n"
MF "v[164:167],a[208:211],a[152:155],v[164:167]\n"
"s_add_u32 s19,s18,196608\n"
"ds_read_b128 a[64:67],v2 offset:43008\n"
MF "v[164:167],a[212:215],a[192:195],v[164:167]\n"
"s_add_u32 s20,s70,196608\n"
TR "a[204:205],v0 offset:36872\n"
MF "v[156:159],a[212:215],a[188:191],v[156:159]\n"
"s_bfe_u32 s58,s23,0x40008\n"
TR "a[206:207],v0 offset:37384\n"
MF "v[156:159],a[208:211],a[148:151],v[156:159]\n"
"s_lshr_b32 s59,s56,1\n"
MF "v[148:151],a[208:211],a[144:147],v[148:151]\n"
"s_mul_i32 s59,s59,5\n"
"ds_read_b128 a[104:107],v3 offset:43008\n"
MF "v[148:151],a[212:215],a[184:187],v[148:151]\n"
"s_add_u32 s58,s58,s59\n"
MF "v[140:143],a[212:215],a[180:183],v[140:143]\n"
LD "v12,s[28:31],s82 offen offset:2048 lds\n"
MF "v[140:143],a[208:211],a[140:143],v[140:143]\n"
"s_waitcnt lgkmcnt(7)\n"
MF "v[176:179],a[216:219],a[140:143],v[176:179]\n"
"s_and_b32 s58,s58,15\n"
TR "a[208:209],v1 offset:32768\n"
MF "v[176:179],a[220:223],a[180:183],v[176:179]\n"
"s_mul_i32 s58,s58,12288\n"
TR "a[210:211],v1 offset:33280\n"
MF "v[184:187],a[220:223],a[184:187],v[184:187]\n"
"s_add_u32 s72,s16,s58\n"
"ds_read_b128 a[68:71],v2 offset:45056\n"
MF "v[184:187],a[216:219],a[144:147],v[184:187]\n"
"s_add_u32 s73,s18,s58\n"
TR "a[212:213],v1 offset:36864\n"
MF "v[192:195],a[216:219],a[148:151],v[192:195]\n"
"s_add_u32 s74,s70,s58\n"
TR "a[214:215],v1 offset:37376\n"
MF "v[192:195],a[220:223],a[188:191],v[192:195]\n"
"s_add_u32 s74,s74,12288\n"
MF "v[200:203],a[220:223],a[192:195],v[200:203]\n"
"s_cmp_eq_u32 s74,s20\n"
"s_cselect_b32 s74,s70,s74\n"
"ds_read_b128 a[108:111],v3 offset:45056\n"
MF "v[200:203],a[216:219],a[152:155],v[200:203]\n"
LD "v13,s[28:31],s82 offen offset:3072 lds\n"
MF "v[208:211],a[216:219],a[156:159],v[208:211]\n"
"s_lshr_b32 s58,s56,1\n"
MF "v[208:211],a[220:223],a[196:199],v[208:211]\n"
"s_waitcnt lgkmcnt(7)\n"
MF "v[212:215],a[204:207],a[196:199],v[212:215]\n"
"s_mul_i32 s58,s58,6144\n"
TR "a[216:217],v1 offset:32776\n"
MF "v[212:215],a[200:203],a[156:159],v[212:215]\n"
"s_mul_i32 s64,s56,3072\n"
TR "a[218:219],v1 offset:33288\n"
MF "v[204:207],a[200:203],a[152:155],v[204:207]\n"
"s_add_u32 s64,s64,s58\n"
"ds_read_b128 a[72:75],v2 offset:47104\n"
MF "v[204:207],a[204:207],a[192:195],v[204:207]\n"
"s_mul_i32 s65,s56,6144\n"
TR "a[220:221],v1 offset:36872\n"
MF "v[196:199],a[204:207],a[188:191],v[196:199]\n"
"s_sub_u32 s58,s71,s70\n"
TR "a[222:223],v1 offset:37384\n"
MF "v[196:199],a[200:203],a[148:151],v[196:199]\n"
"s_add_u32 s96,s74,s58\n"
MF "v[188:191],a[200:203],a[144:147],v[188:191]\n"
"s_sub_u32 s58,s16,s70\n"
"v_add_u32 v0,0xffff4000,v0\n"
MF "v[188:191],a[204:207],a[184:187],v[188:191]\n"
"s_add_u32 s96,s96,s58\n"
"v_add_u32 v1,0xffff4000,v1\n"
MF "v[180:183],a[204:207],a[180:183],v[180:183]\n"
"s_waitcnt vmcnt(13) lgkmcnt(0)\n"
MF "v[180:183],a[200:203],a[140:143],v[180:183]\n"
"s_barrier\n"
MF "v[216:219],a[208:211],a[140:143],v[216:219]\n"
"s_max_u32 s60,s70,s71\n"
TR "a[200:201],v1 offset:8\n"
MF "v[216:219],a[212:215],a[180:183],v[216:219]\n"
"s_add_u32 m0,s62,81920\n"
TR "a[202:203],v1 offset:520\n"
"v_mov_b32 v14,s12\n"
MF "v[224:227],a[212:215],a[184:187],v[224:227]\n"
"s_add_u32 s82,s66,7077888\n"
"ds_read_b128 a[112:115],v3 offset:47104\n"
"v_mul_f32 v14,0xbdd53b95,v14\n"
MF "v[224:227],a[208:211],a[144:147],v[224:227]\n"
"s_sub_u32 s15,s82,7864320\n"
TR "a[204:205],v1 offset:4104\n"
MF "v[232:235],a[208:211],a[148:151],v[232:235]\n"
"s_min_u32 s82,s82,s15\n"
TR "a[206:207],v1 offset:4616\n"
MF "v[232:235],a[212:215],a[188:191],v[232:235]\n"
LD "v6,s[28:31],s82 offen lds\n"
MF "v[240:243],a[212:215],a[192:195],v[240:243]\n"
"s_add_u32 s34,s60,196608\n"
"ds_read_b128 a[76:79],v2 offset:49152\n"
MF "v[240:243],a[208:211],a[152:155],v[240:243]\n"
"s_add_u32 s38,s60,196608\n"
"ds_read_b128 a[116:119],v3 offset:49152\n"
MF "v[248:251],a[208:211],a[156:159],v[248:251]\n"
MF "v[248:251],a[212:215],a[196:199],v[248:251]\n"
LD "v11,s[28:31],s82 offen offset:1024 lds\n"
MF "v[220:223],a[216:219],a[140:143],v[220:223]\n"
TR "a[208:209],v1 offset:16392\n"
MF "v[220:223],a[220:223],a[180:183],v[220:223]\n"
TR "a[210:211],v1 offset:16904\n"
MF "v[228:231],a[220:223],a[184:187],v[228:231]\n"
"ds_read_b128 a[140:143],v2\n"
MF "v[228:231],a[216:219],a[144:147],v[228:231]\n"
TR "a[212:213],v1 offset:20488\n"
MF "v[236:239],a[216:219],a[148:151],v[236:239]\n"
TR "a[214:215],v1 offset:21000\n"
MF "v[236:239],a[220:223],a[188:191],v[236:239]\n"
"ds_read_b128 a[180:183],v3\n"
MF "v[244:247],a[220:223],a[192:195],v[244:247]\n"
"ds_read_b128 a[144:147],v2 offset:2048\n"
MF "v[244:247],a[216:219],a[152:155],v[244:247]\n"
"ds_read_b128 a[184:187],v3 offset:2048\n"
MF "v[252:255],a[216:219],a[156:159],v[252:255]\n"
"ds_read_b128 a[148:151],v2 offset:4096\n"
MF "v[252:255],a[220:223],a[196:199],v[252:255]\n"
"s_waitcnt lgkmcnt(9)\n"
"ds_read_b128 a[188:191],v3 offset:4096\n"
MF "v[220:223],a[200:203],a[60:63],v[220:223]\n"
TR "a[216:217],v1\n"
MF "v[220:223],a[204:207],a[100:103],v[220:223]\n"
TR "a[218:219],v1 offset:512\n"
MF "v[228:231],a[204:207],a[104:107],v[228:231]\n"
"ds_read_b128 a[152:155],v2 offset:6144\n"
MF "v[228:231],a[200:203],a[64:67],v[228:231]\n"
TR "a[220:221],v1 offset:4096\n"
MF "v[236:239],a[200:203],a[68:71],v[236:239]\n"
TR "a[222:223],v1 offset:4608\n"
MF "v[236:239],a[204:207],a[108:111],v[236:239]\n"
"ds_read_b128 a[192:195],v3 offset:6144\n"
MF "v[244:247],a[204:207],a[112:115],v[244:247]\n"
"ds_read_b128 a[156:159],v2 offset:8192\n"
MF "v[244:247],a[200:203],a[72:75],v[244:247]\n"
"ds_read_b128 a[196:199],v3 offset:8192\n"
MF "v[252:255],a[200:203],a[76:79],v[252:255]\n"
LD "v12,s[28:31],s82 offen offset:2048 lds\n"
MF "v[252:255],a[204:207],a[116:119],v[252:255]\n"
"s_waitcnt lgkmcnt(8)\n"
LD "v13,s[28:31],s82 offen offset:3072 lds\n"
MF "v[220:223],a[208:211],a[140:143],v[220:223]\n"
TR "a[200:201],v1 offset:16384\n"
MF "v[220:223],a[212:215],a[180:183],v[220:223]\n"
TR "a[202:203],v1 offset:16896\n"
MF "v[228:231],a[212:215],a[184:187],v[228:231]\n"
MF "v[228:231],a[208:211],a[144:147],v[228:231]\n"
TR "a[204:205],v1 offset:20480\n"
MF "v[236:239],a[208:211],a[148:151],v[236:239]\n"
TR "a[206:207],v1 offset:20992\n"
MF "v[236:239],a[212:215],a[188:191],v[236:239]\n"
"s_waitcnt lgkmcnt(4)\n"
MF "v[244:247],a[212:215],a[192:195],v[244:247]\n"
MF "v[244:247],a[208:211],a[152:155],v[244:247]\n"
MF "v[252:255],a[208:211],a[156:159],v[252:255]\n"
MF "v[252:255],a[212:215],a[196:199],v[252:255]\n"
"s_waitcnt lgkmcnt(0)\n"
"s_cmp_eq_u32 s69,98\n"
"s_cbranch_scc1 .Lqy\n"
".Lqf:\n"
MF "v[216:219],a[216:219],a[60:63],v[216:219]\n"
TR "a[208:209],v0 offset:8\n"
MF "v[216:219],a[220:223],a[100:103],v[216:219]\n"
TR "a[210:211],v0 offset:520\n"
MF "v[224:227],a[220:223],a[104:107],v[224:227]\n"
"buffer_load_dword a224,v15,s[36:39],s70 offen offset:2688 nt\n"
MF "v[224:227],a[216:219],a[64:67],v[224:227]\n"
TR "a[212:213],v0 offset:4104\n"
MF "v[232:235],a[216:219],a[68:71],v[232:235]\n"
TR "a[214:215],v0 offset:4616\n"
MF "v[232:235],a[220:223],a[108:111],v[232:235]\n"
"buffer_load_dword a225,v15,s[36:39],s71 offen offset:2688 nt\n"
MF "v[240:243],a[220:223],a[112:115],v[240:243]\n"
MF "v[240:243],a[216:219],a[72:75],v[240:243]\n"
"buffer_load_dword a226,v15,s[48:51],s7 offen offset:2688\n"
MF "v[248:251],a[216:219],a[76:79],v[248:251]\n"
MF "v[248:251],a[220:223],a[116:119],v[248:251]\n"
MF "v[248:251],a[200:203],a[156:159],v[248:251]\n"
TR "a[216:217],v0 offset:16392\n"
MF "v[248:251],a[204:207],a[196:199],v[248:251]\n"
TR "a[218:219],v0 offset:16904\n"
MF "v[240:243],a[204:207],a[192:195],v[240:243]\n"
MF "v[240:243],a[200:203],a[152:155],v[240:243]\n"
TR "a[220:221],v0 offset:20488\n"
MF "v[232:235],a[200:203],a[148:151],v[232:235]\n"
TR "a[222:223],v0 offset:21000\n"
MF "v[232:235],a[204:207],a[188:191],v[232:235]\n"
MF "v[224:227],a[204:207],a[184:187],v[224:227]\n"
MF "v[224:227],a[200:203],a[144:147],v[224:227]\n"
"v_fma_f32 v248,v248,s54,v14\n"
"v_fma_f32 v249,v249,s54,v14\n"
MF "v[216:219],a[200:203],a[140:143],v[216:219]\n"
"v_fma_f32 v250,v250,s54,v14\n"
"v_fma_f32 v251,v251,s54,v14\n"
MF "v[216:219],a[204:207],a[180:183],v[216:219]\n"
"s_waitcnt lgkmcnt(4)\n"
"v_fma_f32 v252,v252,s54,v14\n"
"v_fma_f32 v253,v253,s54,v14\n"
MF "v[180:183],a[208:211],a[60:63],v[180:183]\n"
TR "a[200:201],v0\n"
"v_fma_f32 v254,v254,s54,v14\n"
MF "v[180:183],a[212:215],a[100:103],v[180:183]\n"
TR "a[202:203],v0 offset:512\n"
"v_fma_f32 v255,v255,s54,v14\n"
MF "v[188:191],a[212:215],a[104:107],v[188:191]\n"
CV "v255,v254,v255\n"
CV "v254,v252,v253\n"
MF "v[188:191],a[208:211],a[64:67],v[188:191]\n"
TR "a[204:205],v0 offset:4096\n"
CV "v253,v250,v251\n"
MF "v[196:199],a[208:211],a[68:71],v[196:199]\n"
TR "a[206:207],v0 offset:4608\n"
CV "v252,v248,v249\n"
MF "v[196:199],a[212:215],a[108:111],v[196:199]\n"
"v_fma_f32 v240,v240,s54,v14\n"
"v_fma_f32 v241,v241,s54,v14\n"
MF "v[204:207],a[212:215],a[112:115],v[204:207]\n"
"v_fma_f32 v242,v242,s54,v14\n"
"v_fma_f32 v243,v243,s54,v14\n"
MF "v[204:207],a[208:211],a[72:75],v[204:207]\n"
"v_fma_f32 v244,v244,s54,v14\n"
"v_fma_f32 v245,v245,s54,v14\n"
MF "v[212:215],a[208:211],a[76:79],v[212:215]\n"
"v_fma_f32 v246,v246,s54,v14\n"
"v_fma_f32 v247,v247,s54,v14\n"
MF "v[212:215],a[212:215],a[116:119],v[212:215]\n"
"s_waitcnt lgkmcnt(4)\n"
CV "v251,v246,v247\n"
CV "v250,v244,v245\n"
MF "v[180:183],a[216:219],a[140:143],v[180:183]\n"
TR "a[208:209],v0 offset:16384\n"
CV "v249,v242,v243\n"
MF "v[180:183],a[220:223],a[180:183],v[180:183]\n"
TR "a[210:211],v0 offset:16896\n"
CV "v248,v240,v241\n"
MF "v[188:191],a[220:223],a[184:187],v[188:191]\n"
"v_fma_f32 v232,v232,s54,v14\n"
"v_fma_f32 v233,v233,s54,v14\n"
MF "v[188:191],a[216:219],a[144:147],v[188:191]\n"
TR "a[212:213],v0 offset:20480\n"
"v_fma_f32 v234,v234,s54,v14\n"
MF "v[196:199],a[216:219],a[148:151],v[196:199]\n"
TR "a[214:215],v0 offset:20992\n"
"v_fma_f32 v235,v235,s54,v14\n"
MF "v[196:199],a[220:223],a[188:191],v[196:199]\n"
"v_fma_f32 v236,v236,s54,v14\n"
"v_fma_f32 v237,v237,s54,v14\n"
MF "v[204:207],a[220:223],a[192:195],v[204:207]\n"
"v_fma_f32 v238,v238,s54,v14\n"
"v_fma_f32 v239,v239,s54,v14\n"
MF "v[204:207],a[216:219],a[152:155],v[204:207]\n"
CV "v247,v238,v239\n"
CV "v246,v236,v237\n"
MF "v[212:215],a[216:219],a[156:159],v[212:215]\n"
"s_waitcnt vmcnt(11) lgkmcnt(0)\n"
CV "v245,v234,v235\n"
CV "v244,v232,v233\n"
MF "v[212:215],a[220:223],a[196:199],v[212:215]\n"
"s_barrier\n"
MF "v[176:179],a[200:203],a[60:63],v[176:179]\n"
TR "a[216:217],v1 offset:32776\n"
"v_fma_f32 v224,v224,s54,v14\n"
MF "v[176:179],a[204:207],a[100:103],v[176:179]\n"
"s_add_u32 m0,s64,0\n"
TR "a[218:219],v1 offset:33288\n"
"v_fma_f32 v225,v225,s54,v14\n"
MF "v[184:187],a[204:207],a[104:107],v[184:187]\n"
LD "v7,s[32:35],s72 offen nt lds\n"
MF "v[184:187],a[200:203],a[64:67],v[184:187]\n"
TR "a[220:221],v1 offset:36872\n"
"v_fma_f32 v226,v226,s54,v14\n"
MF "v[192:195],a[200:203],a[68:71],v[192:195]\n"
TR "a[222:223],v1 offset:37384\n"
"v_fma_f32 v227,v227,s54,v14\n"
MF "v[192:195],a[204:207],a[108:111],v[192:195]\n"
"s_add_u32 m0,s64,896\n"
"v_fma_f32 v228,v228,s54,v14\n"
"v_fma_f32 v229,v229,s54,v14\n"
MF "v[200:203],a[204:207],a[112:115],v[200:203]\n"
LD "v7,s[32:35],s72 offen offset:128 nt lds\n"
MF "v[200:203],a[200:203],a[72:75],v[200:203]\n"
"v_fma_f32 v230,v230,s54,v14\n"
"v_fma_f32 v231,v231,s54,v14\n"
MF "v[208:211],a[200:203],a[76:79],v[208:211]\n"
CV "v243,v230,v231\n"
CV "v242,v228,v229\n"
MF "v[208:211],a[204:207],a[116:119],v[208:211]\n"
CV "v241,v226,v227\n"
CV "v240,v224,v225\n"
MF "v[208:211],a[208:211],a[156:159],v[208:211]\n"
TR "a[200:201],v1 offset:49160\n"
"v_fma_f32 v216,v216,s54,v14\n"
MF "v[208:211],a[212:215],a[196:199],v[208:211]\n"
"s_add_u32 m0,s64,1792\n"
TR "a[202:203],v1 offset:49672\n"
"v_fma_f32 v217,v217,s54,v14\n"
MF "v[200:203],a[212:215],a[192:195],v[200:203]\n"
LD "v7,s[32:35],s72 offen offset:256 nt lds\n"
MF "v[200:203],a[208:211],a[152:155],v[200:203]\n"
TR "a[204:205],v1 offset:53256\n"
"v_fma_f32 v218,v218,s54,v14\n"
MF "v[192:195],a[208:211],a[148:151],v[192:195]\n"
TR "a[206:207],v1 offset:53768\n"
"v_fma_f32 v219,v219,s54,v14\n"
MF "v[192:195],a[212:215],a[188:191],v[192:195]\n"
"v_fma_f32 v220,v220,s54,v14\n"
"v_fma_f32 v221,v221,s54,v14\n"
MF "v[184:187],a[212:215],a[184:187],v[184:187]\n"
"s_add_u32 s72,s72,6144\n"
"v_fma_f32 v222,v222,s54,v14\n"
"v_fma_f32 v223,v223,s54,v14\n"
MF "v[184:187],a[208:211],a[144:147],v[184:187]\n"
"s_add_u32 m0,s64,6144\n"
CV "v239,v222,v223\n"
CV "v238,v220,v221\n"
MF "v[176:179],a[208:211],a[140:143],v[176:179]\n"
LD "v7,s[32:35],s72 offen nt lds\n"
MF "v[176:179],a[212:215],a[180:183],v[176:179]\n"
"s_waitcnt lgkmcnt(4)\n"
CV "v237,v218,v219\n"
CV "v236,v216,v217\n"
MF "v[140:143],a[216:219],a[60:63],v[140:143]\n"
TR "a[208:209],v1 offset:32768\n"
"v_fma_f32 v208,v208,s54,v14\n"
MF "v[140:143],a[220:223],a[100:103],v[140:143]\n"
TR "a[210:211],v1 offset:33280\n"
"v_fma_f32 v209,v209,s54,v14\n"
MF "v[148:151],a[220:223],a[104:107],v[148:151]\n"
"v_fma_f32 v210,v210,s54,v14\n"
"v_fma_f32 v211,v211,s54,v14\n"
MF "v[148:151],a[216:219],a[64:67],v[148:151]\n"
TR "a[212:213],v1 offset:36864\n"
"v_fma_f32 v212,v212,s54,v14\n"
MF "v[156:159],a[216:219],a[68:71],v[156:159]\n"
"s_add_u32 m0,s64,7040\n"
TR "a[214:215],v1 offset:37376\n"
"v_fma_f32 v213,v213,s54,v14\n"
MF "v[156:159],a[220:223],a[108:111],v[156:159]\n"
LD "v7,s[32:35],s72 offen offset:128 nt lds\n"
MF "v[164:167],a[220:223],a[112:115],v[164:167]\n"
"v_fma_f32 v214,v214,s54,v14\n"
"v_fma_f32 v215,v215,s54,v14\n"
MF "v[164:167],a[216:219],a[72:75],v[164:167]\n"
CV "v235,v214,v215\n"
CV "v234,v212,v213\n"
MF "v[172:175],a[216:219],a[76:79],v[172:175]\n"
"s_add_u32 m0,s64,7936\n"
CV "v233,v210,v211\n"
CV "v232,v208,v209\n"
MF "v[172:175],a[220:223],a[116:119],v[172:175]\n"
"s_waitcnt lgkmcnt(4)\n"
LD "v7,s[32:35],s72 offen offset:256 nt lds\n"
MF "v[140:143],a[200:203],a[140:143],v[140:143]\n"
TR "a[216:217],v1 offset:49152\n"
"v_fma_f32 v200,v200,s54,v14\n"
MF "v[140:143],a[204:207],a[180:183],v[140:143]\n"
TR "a[218:219],v1 offset:49664\n"
"v_fma_f32 v201,v201,s54,v14\n"
MF "v[148:151],a[204:207],a[184:187],v[148:151]\n"
"v_fma_f32 v202,v202,s54,v14\n"
"v_fma_f32 v203,v203,s54,v14\n"
MF "v[148:151],a[200:203],a[144:147],v[148:151]\n"
TR "a[220:221],v1 offset:53248\n"
"v_fma_f32 v204,v204,s54,v14\n"
MF "v[156:159],a[200:203],a[148:151],v[156:159]\n"
TR "a[222:223],v1 offset:53760\n"
"v_fma_f32 v205,v205,s54,v14\n"
MF "v[156:159],a[204:207],a[188:191],v[156:159]\n"
".Lpg8b:\n"
"s_cbranch_execz .Lpg11b\n"
".Lpg8d:\n"
"s_cbranch_execz .Lisr2\n"
"v_fma_f32 v206,v206,s54,v14\n"
"v_fma_f32 v207,v207,s54,v14\n"
MF "v[164:167],a[204:207],a[192:195],v[164:167]\n"
CV "v231,v206,v207\n"
CV "v230,v204,v205\n"
MF "v[164:167],a[200:203],a[152:155],v[164:167]\n"
CV "v229,v202,v203\n"
CV "v228,v200,v201\n"
MF "v[172:175],a[200:203],a[156:159],v[172:175]\n"
"v_fma_f32 v192,v192,s54,v14\n"
"v_fma_f32 v193,v193,s54,v14\n"
MF "v[172:175],a[204:207],a[196:199],v[172:175]\n"
"s_waitcnt lgkmcnt(0)\n"
"v_fma_f32 v194,v194,s54,v14\n"
"v_fma_f32 v195,v195,s54,v14\n"
MF "v[136:139],a[208:211],a[60:63],v[136:139]\n"
TR "a[200:201],v0 offset:32776\n"
"v_fma_f32 v196,v196,s54,v14\n"
MF "v[136:139],a[212:215],a[100:103],v[136:139]\n"
TR "a[202:203],v0 offset:33288\n"
"v_fma_f32 v197,v197,s54,v14\n"
MF "v[144:147],a[212:215],a[104:107],v[144:147]\n"
"v_fma_f32 v198,v198,s54,v14\n"
"v_fma_f32 v199,v199,s54,v14\n"
MF "v[144:147],a[208:211],a[64:67],v[144:147]\n"
TR "a[204:205],v0 offset:36872\n"
CV "v227,v198,v199\n"
MF "v[152:155],a[208:211],a[68:71],v[152:155]\n"
TR "a[206:207],v0 offset:37384\n"
CV "v226,v196,v197\n"
MF "v[152:155],a[212:215],a[108:111],v[152:155]\n"
CV "v225,v194,v195\n"
CV "v224,v192,v193\n"
MF "v[160:163],a[212:215],a[112:115],v[160:163]\n"
"v_fma_f32 v184,v184,s54,v14\n"
"v_fma_f32 v185,v185,s54,v14\n"
MF "v[160:163],a[208:211],a[72:75],v[160:163]\n"
"v_fma_f32 v186,v186,s54,v14\n"
"v_fma_f32 v187,v187,s54,v14\n"
MF "v[168:171],a[208:211],a[76:79],v[168:171]\n"
"v_fma_f32 v188,v188,s54,v14\n"
"v_fma_f32 v189,v189,s54,v14\n"
MF "v[168:171],a[212:215],a[116:119],v[168:171]\n"
"v_fma_f32 v190,v190,s54,v14\n"
"v_fma_f32 v191,v191,s54,v14\n"
MF "v[168:171],a[216:219],a[156:159],v[168:171]\n"
TR "a[208:209],v0 offset:49160\n"
CV "v223,v190,v191\n"
MF "v[168:171],a[220:223],a[196:199],v[168:171]\n"
TR "a[210:211],v0 offset:49672\n"
CV "v222,v188,v189\n"
MF "v[160:163],a[220:223],a[192:195],v[160:163]\n"
CV "v221,v186,v187\n"
CV "v220,v184,v185\n"
MF "v[160:163],a[216:219],a[152:155],v[160:163]\n"
TR "a[212:213],v0 offset:53256\n"
"v_fma_f32 v176,v176,s54,v14\n"
MF "v[152:155],a[216:219],a[148:151],v[152:155]\n"
TR "a[214:215],v0 offset:53768\n"
"v_fma_f32 v177,v177,s54,v14\n"
MF "v[152:155],a[220:223],a[188:191],v[152:155]\n"
"v_fma_f32 v178,v178,s54,v14\n"
"v_fma_f32 v179,v179,s54,v14\n"
MF "v[144:147],a[220:223],a[184:187],v[144:147]\n"
"v_fma_f32 v180,v180,s54,v14\n"
"v_fma_f32 v181,v181,s54,v14\n"
MF "v[144:147],a[216:219],a[144:147],v[144:147]\n"
"v_fma_f32 v182,v182,s54,v14\n"
"v_fma_f32 v183,v183,s54,v14\n"
MF "v[136:139],a[216:219],a[140:143],v[136:139]\n"
CV "v219,v182,v183\n"
CV "v218,v180,v181\n"
MF "v[136:139],a[220:223],a[180:183],v[136:139]\n"
"s_waitcnt lgkmcnt(4)\n"
CV "v217,v178,v179\n"
CV "v216,v176,v177\n"
MF "v[100:103],a[200:203],a[60:63],v[100:103]\n"
TR "a[216:217],v0 offset:32768\n"
"v_fma_f32 v168,v168,s54,v14\n"
MF "v[100:103],a[204:207],a[100:103],v[100:103]\n"
TR "a[218:219],v0 offset:33280\n"
"v_fma_f32 v169,v169,s54,v14\n"
MF "v[108:111],a[204:207],a[104:107],v[108:111]\n"
"v_fma_f32 v170,v170,s54,v14\n"
"v_fma_f32 v171,v171,s54,v14\n"
MF "v[108:111],a[200:203],a[64:67],v[108:111]\n"
TR "a[220:221],v0 offset:36864\n"
"v_fma_f32 v172,v172,s54,v14\n"
MF "v[116:119],a[200:203],a[68:71],v[116:119]\n"
TR "a[222:223],v0 offset:37376\n"
"v_fma_f32 v173,v173,s54,v14\n"
MF "v[116:119],a[204:207],a[108:111],v[116:119]\n"
"v_fma_f32 v174,v174,s54,v14\n"
"v_fma_f32 v175,v175,s54,v14\n"
MF "v[124:127],a[204:207],a[112:115],v[124:127]\n"
CV "v215,v174,v175\n"
CV "v214,v172,v173\n"
MF "v[124:127],a[200:203],a[72:75],v[124:127]\n"
CV "v213,v170,v171\n"
CV "v212,v168,v169\n"
MF "v[132:135],a[200:203],a[76:79],v[132:135]\n"
"v_fma_f32 v160,v160,s54,v14\n"
"v_fma_f32 v161,v161,s54,v14\n"
MF "v[132:135],a[204:207],a[116:119],v[132:135]\n"
"s_waitcnt lgkmcnt(4)\n"
"v_fma_f32 v162,v162,s54,v14\n"
"v_fma_f32 v163,v163,s54,v14\n"
MF "v[100:103],a[208:211],a[140:143],v[100:103]\n"
TR "a[200:201],v0 offset:49152\n"
"v_fma_f32 v164,v164,s54,v14\n"
MF "v[100:103],a[212:215],a[180:183],v[100:103]\n"
TR "a[202:203],v0 offset:49664\n"
"v_fma_f32 v165,v165,s54,v14\n"
MF "v[108:111],a[212:215],a[184:187],v[108:111]\n"
"v_fma_f32 v166,v166,s54,v14\n"
"v_fma_f32 v167,v167,s54,v14\n"
MF "v[108:111],a[208:211],a[144:147],v[108:111]\n"
TR "a[204:205],v0 offset:53248\n"
CV "v211,v166,v167\n"
MF "v[116:119],a[208:211],a[148:151],v[116:119]\n"
TR "a[206:207],v0 offset:53760\n"
CV "v210,v164,v165\n"
MF "v[116:119],a[212:215],a[188:191],v[116:119]\n"
CV "v209,v162,v163\n"
CV "v208,v160,v161\n"
MF "v[124:127],a[212:215],a[192:195],v[124:127]\n"
"v_add_u32 v0,0xc000,v0\n"
"v_fma_f32 v152,v152,s54,v14\n"
MF "v[124:127],a[208:211],a[152:155],v[124:127]\n"
"v_add_u32 v1,0xc000,v1\n"
"v_fma_f32 v153,v153,s54,v14\n"
MF "v[132:135],a[208:211],a[156:159],v[132:135]\n"
"s_waitcnt vmcnt(9) lgkmcnt(0)\n"
"v_fma_f32 v154,v154,s54,v14\n"
"v_fma_f32 v155,v155,s54,v14\n"
MF "v[132:135],a[212:215],a[196:199],v[132:135]\n"
"s_barrier\n"
MF "v[96:99],a[216:219],a[60:63],v[96:99]\n"
TR "a[208:209],v1 offset:16392\n"
"v_fma_f32 v156,v156,s54,v14\n"
MF "v[96:99],a[220:223],a[100:103],v[96:99]\n"
TR "a[210:211],v1 offset:16904\n"
"v_fma_f32 v157,v157,s54,v14\n"
MF "v[104:107],a[220:223],a[104:107],v[104:107]\n"
"s_add_u32 s72,s72,6144\n"
"v_fma_f32 v158,v158,s54,v14\n"
"v_fma_f32 v159,v159,s54,v14\n"
MF "v[104:107],a[216:219],a[64:67],v[104:107]\n"
"s_cmp_eq_u32 s72,s17\n"
"s_cselect_b32 s72,s16,s72\n"
TR "a[212:213],v1 offset:20488\n"
MF "v[112:115],a[216:219],a[68:71],v[112:115]\n"
"s_add_u32 m0,s64,24576\n"
TR "a[214:215],v1 offset:21000\n"
CV "v207,v158,v159\n"
MF "v[112:115],a[220:223],a[108:111],v[112:115]\n"
LD "v7,s[32:35],s72 offen nt lds\n"
MF "v[120:123],a[220:223],a[112:115],v[120:123]\n"
"s_add_u32 m0,s64,25472\n"
CV "v206,v156,v157\n"
CV "v205,v154,v155\n"
MF "v[120:123],a[216:219],a[72:75],v[120:123]\n"
LD "v7,s[32:35],s72 offen offset:128 nt lds\n"
MF "v[128:131],a[216:219],a[76:79],v[128:131]\n"
CV "v204,v152,v153\n"
"v_fma_f32 v144,v144,s54,v14\n"
MF "v[128:131],a[220:223],a[116:119],v[128:131]\n"
"v_fma_f32 v145,v145,s54,v14\n"
"v_fma_f32 v146,v146,s54,v14\n"
MF "v[128:131],a[200:203],a[156:159],v[128:131]\n"
TR "a[216:217],v1 offset:32776\n"
"v_fma_f32 v147,v147,s54,v14\n"
MF "v[128:131],a[204:207],a[196:199],v[128:131]\n"
"s_add_u32 m0,s64,26368\n"
TR "a[218:219],v1 offset:33288\n"
"v_fma_f32 v148,v148,s54,v14\n"
MF "v[120:123],a[204:207],a[192:195],v[120:123]\n"
LD "v7,s[32:35],s72 offen offset:256 nt lds\n"
MF "v[120:123],a[200:203],a[152:155],v[120:123]\n"
TR "a[220:221],v1 offset:36872\n"
"v_fma_f32 v149,v149,s54,v14\n"
MF "v[112:115],a[200:203],a[148:151],v[112:115]\n"
TR "a[222:223],v1 offset:37384\n"
"v_fma_f32 v150,v150,s54,v14\n"
MF "v[112:115],a[204:207],a[188:191],v[112:115]\n"
"v_fma_f32 v151,v151,s54,v14\n"
CV "v203,v150,v151\n"
MF "v[104:107],a[204:207],a[184:187],v[104:107]\n"
"s_add_u32 s72,s72,6144\n"
CV "v202,v148,v149\n"
CV "v201,v146,v147\n"
MF "v[104:107],a[200:203],a[144:147],v[104:107]\n"
"s_add_u32 m0,s64,30720\n"
CV "v200,v144,v145\n"
"v_fma_f32 v136,v136,s54,v14\n"
MF "v[96:99],a[200:203],a[140:143],v[96:99]\n"
LD "v7,s[32:35],s72 offen nt lds\n"
MF "v[96:99],a[204:207],a[180:183],v[96:99]\n"
"s_waitcnt lgkmcnt(4)\n"
"v_fma_f32 v137,v137,s54,v14\n"
"v_fma_f32 v138,v138,s54,v14\n"
MF "v[60:63],a[208:211],a[60:63],v[60:63]\n"
TR "a[200:201],v1 offset:16384\n"
"v_fma_f32 v139,v139,s54,v14\n"
MF "v[60:63],a[212:215],a[100:103],v[60:63]\n"
TR "a[202:203],v1 offset:16896\n"
"v_fma_f32 v140,v140,s54,v14\n"
MF "v[68:71],a[212:215],a[104:107],v[68:71]\n"
"v_fma_f32 v141,v141,s54,v14\n"
"v_fma_f32 v142,v142,s54,v14\n"
MF "v[68:71],a[208:211],a[64:67],v[68:71]\n"
TR "a[204:205],v1 offset:20480\n"
"v_fma_f32 v143,v143,s54,v14\n"
MF "v[76:79],a[208:211],a[68:71],v[76:79]\n"
"s_add_u32 m0,s64,31616\n"
TR "a[206:207],v1 offset:20992\n"
CV "v199,v142,v143\n"
MF "v[76:79],a[212:215],a[108:111],v[76:79]\n"
LD "v7,s[32:35],s72 offen offset:128 nt lds\n"
MF "v[84:87],a[212:215],a[112:115],v[84:87]\n"
CV "v198,v140,v141\n"
CV "v197,v138,v139\n"
MF "v[84:87],a[208:211],a[72:75],v[84:87]\n"
CV "v196,v136,v137\n"
"v_fma_f32 v128,v128,s54,v14\n"
MF "v[92:95],a[208:211],a[76:79],v[92:95]\n"
"s_add_u32 m0,s64,32512\n"
"v_fma_f32 v129,v129,s54,v14\n"
"v_fma_f32 v130,v130,s54,v14\n"
MF "v[92:95],a[212:215],a[116:119],v[92:95]\n"
"s_waitcnt lgkmcnt(4)\n"
LD "v7,s[32:35],s72 offen offset:256 nt lds\n"
MF "v[60:63],a[216:219],a[140:143],v[60:63]\n"
TR "a[208:209],v1 offset:32768\n"
"v_fma_f32 v131,v131,s54,v14\n"
MF "v[60:63],a[220:223],a[180:183],v[60:63]\n"
TR "a[210:211],v1 offset:33280\n"
"v_fma_f32 v132,v132,s54,v14\n"
MF "v[68:71],a[220:223],a[184:187],v[68:71]\n"
"v_fma_f32 v133,v133,s54,v14\n"
"v_fma_f32 v134,v134,s54,v14\n"
MF "v[68:71],a[216:219],a[144:147],v[68:71]\n"
TR "a[212:213],v1 offset:36864\n"
"v_fma_f32 v135,v135,s54,v14\n"
MF "v[76:79],a[216:219],a[148:151],v[76:79]\n"
TR "a[214:215],v1 offset:37376\n"
CV "v195,v134,v135\n"
MF "v[76:79],a[220:223],a[188:191],v[76:79]\n"
CV "v194,v132,v133\n"
CV "v193,v130,v131\n"
MF "v[84:87],a[220:223],a[192:195],v[84:87]\n"
CV "v192,v128,v129\n"
"v_fma_f32 v120,v120,s54,v14\n"
MF "v[84:87],a[216:219],a[152:155],v[84:87]\n"
"v_fma_f32 v121,v121,s54,v14\n"
"v_fma_f32 v122,v122,s54,v14\n"
MF "v[92:95],a[216:219],a[156:159],v[92:95]\n"
"v_fma_f32 v123,v123,s54,v14\n"
"v_fma_f32 v124,v124,s54,v14\n"
MF "v[92:95],a[220:223],a[196:199],v[92:95]\n"
"s_waitcnt lgkmcnt(0)\n"
"v_fma_f32 v125,v125,s54,v14\n"
"v_fma_f32 v126,v126,s54,v14\n"
MF "v[56:59],a[200:203],a[60:63],v[56:59]\n"
TR "a[216:217],v0 offset:16392\n"
"v_fma_f32 v127,v127,s54,v14\n"
MF "v[56:59],a[204:207],a[100:103],v[56:59]\n"
TR "a[218:219],v0 offset:16904\n"
CV "v191,v126,v127\n"
MF "v[64:67],a[204:207],a[104:107],v[64:67]\n"
CV "v190,v124,v125\n"
CV "v189,v122,v123\n"
MF "v[64:67],a[200:203],a[64:67],v[64:67]\n"
TR "a[220:221],v0 offset:20488\n"
CV "v188,v120,v121\n"
MF "v[72:75],a[200:203],a[68:71],v[72:75]\n"
TR "a[222:223],v0 offset:21000\n"
"v_fma_f32 v112,v112,s54,v14\n"
MF "v[72:75],a[204:207],a[108:111],v[72:75]\n"
"v_fma_f32 v113,v113,s54,v14\n"
"v_fma_f32 v114,v114,s54,v14\n"
MF "v[80:83],a[204:207],a[112:115],v[80:83]\n"
"v_fma_f32 v115,v115,s54,v14\n"
"v_fma_f32 v116,v116,s54,v14\n"
MF "v[80:83],a[200:203],a[72:75],v[80:83]\n"
"v_fma_f32 v117,v117,s54,v14\n"
"v_fma_f32 v118,v118,s54,v14\n"
MF "v[88:91],a[200:203],a[76:79],v[88:91]\n"
"v_fma_f32 v119,v119,s54,v14\n"
CV "v187,v118,v119\n"
MF "v[88:91],a[204:207],a[116:119],v[88:91]\n"
CV "v186,v116,v117\n"
CV "v185,v114,v115\n"
MF "v[88:91],a[208:211],a[156:159],v[88:91]\n"
TR "a[200:201],v0 offset:32776\n"
CV "v184,v112,v113\n"
MF "v[88:91],a[212:215],a[196:199],v[88:91]\n"
TR "a[202:203],v0 offset:33288\n"
"v_fma_f32 v104,v104,s54,v14\n"
MF "v[80:83],a[212:215],a[192:195],v[80:83]\n"
"v_fma_f32 v105,v105,s54,v14\n"
"v_fma_f32 v106,v106,s54,v14\n"
MF "v[80:83],a[208:211],a[152:155],v[80:83]\n"
TR "a[204:205],v0 offset:36872\n"
"v_fma_f32 v107,v107,s54,v14\n"
MF "v[72:75],a[208:211],a[148:151],v[72:75]\n"
TR "a[206:207],v0 offset:37384\n"
"v_fma_f32 v108,v108,s54,v14\n"
MF "v[72:75],a[212:215],a[188:191],v[72:75]\n"
"v_fma_f32 v109,v109,s54,v14\n"
"v_fma_f32 v110,v110,s54,v14\n"
MF "v[64:67],a[212:215],a[184:187],v[64:67]\n"
"v_fma_f32 v111,v111,s54,v14\n"
CV "v183,v110,v111\n"
MF "v[64:67],a[208:211],a[144:147],v[64:67]\n"
CV "v182,v108,v109\n"
CV "v181,v106,v107\n"
MF "v[56:59],a[208:211],a[140:143],v[56:59]\n"
CV "v180,v104,v105\n"
"v_fma_f32 v96,v96,s54,v14\n"
MF "v[56:59],a[212:215],a[180:183],v[56:59]\n"
"s_waitcnt lgkmcnt(4)\n"
"v_fma_f32 v97,v97,s54,v14\n"
"v_fma_f32 v98,v98,s54,v14\n"
MF "v[20:23],a[216:219],a[60:63],v[20:23]\n"
TR "a[208:209],v0 offset:16384\n"
"v_fma_f32 v99,v99,s54,v14\n"
MF "v[20:23],a[220:223],a[100:103],v[20:23]\n"
TR "a[210:211],v0 offset:16896\n"
"v_fma_f32 v100,v100,s54,v14\n"
MF "v[28:31],a[220:223],a[104:107],v[28:31]\n"
"v_fma_f32 v101,v101,s54,v14\n"
"v_fma_f32 v102,v102,s54,v14\n"
MF "v[28:31],a[216:219],a[64:67],v[28:31]\n"
TR "a[212:213],v0 offset:20480\n"
"v_fma_f32 v103,v103,s54,v14\n"
MF "v[36:39],a[216:219],a[68:71],v[36:39]\n"
TR "a[214:215],v0 offset:20992\n"
CV "v179,v102,v103\n"
MF "v[36:39],a[220:223],a[108:111],v[36:39]\n"
CV "v178,v100,v101\n"
CV "v177,v98,v99\n"
MF "v[44:47],a[220:223],a[112:115],v[44:47]\n"
CV "v176,v96,v97\n"
"v_fma_f32 v88,v88,s54,v14\n"
MF "v[44:47],a[216:219],a[72:75],v[44:47]\n"
"v_fma_f32 v89,v89,s54,v14\n"
"v_fma_f32 v90,v90,s54,v14\n"
MF "v[52:55],a[216:219],a[76:79],v[52:55]\n"
"v_fma_f32 v91,v91,s54,v14\n"
"v_fma_f32 v92,v92,s54,v14\n"
MF "v[52:55],a[220:223],a[116:119],v[52:55]\n"
"s_waitcnt lgkmcnt(4)\n"
"v_fma_f32 v93,v93,s54,v14\n"
"v_fma_f32 v94,v94,s54,v14\n"
MF "v[20:23],a[200:203],a[140:143],v[20:23]\n"
TR "a[216:217],v0 offset:32768\n"
"v_fma_f32 v95,v95,s54,v14\n"
MF "v[20:23],a[204:207],a[180:183],v[20:23]\n"
TR "a[218:219],v0 offset:33280\n"
CV "v175,v94,v95\n"
MF "v[28:31],a[204:207],a[184:187],v[28:31]\n"
CV "v174,v92,v93\n"
CV "v173,v90,v91\n"
MF "v[28:31],a[200:203],a[144:147],v[28:31]\n"
TR "a[220:221],v0 offset:36864\n"
CV "v172,v88,v89\n"
MF "v[36:39],a[200:203],a[148:151],v[36:39]\n"
TR "a[222:223],v0 offset:37376\n"
"v_fma_f32 v80,v80,s54,v14\n"
MF "v[36:39],a[204:207],a[188:191],v[36:39]\n"
"v_fma_f32 v81,v81,s54,v14\n"
"v_fma_f32 v82,v82,s54,v14\n"
MF "v[44:47],a[204:207],a[192:195],v[44:47]\n"
"v_fma_f32 v83,v83,s54,v14\n"
"v_fma_f32 v84,v84,s54,v14\n"
MF "v[44:47],a[200:203],a[152:155],v[44:47]\n"
"v_fma_f32 v85,v85,s54,v14\n"
"v_fma_f32 v86,v86,s54,v14\n"
MF "v[52:55],a[200:203],a[156:159],v[52:55]\n"
"s_waitcnt lgkmcnt(0)\n"
"v_fma_f32 v87,v87,s54,v14\n"
CV "v171,v86,v87\n"
MF "v[52:55],a[204:207],a[196:199],v[52:55]\n"
"s_barrier\n"
MF "v[16:19],a[208:211],a[60:63],v[16:19]\n"
"s_add_u32 m0,s65,49152\n"
CV "v170,v84,v85\n"
CV "v169,v82,v83\n"
MF "v[16:19],a[212:215],a[100:103],v[16:19]\n"
LD "v8,s[36:39],s73 offen nt lds\n"
MF "v[24:27],a[212:215],a[104:107],v[24:27]\n"
"s_add_u32 m0,s65,50048\n"
CV "v168,v80,v81\n"
"v_fma_f32 v72,v72,s54,v14\n"
MF "v[24:27],a[208:211],a[64:67],v[24:27]\n"
LD "v8,s[36:39],s73 offen offset:128 nt lds\n"
MF "v[32:35],a[208:211],a[68:71],v[32:35]\n"
"v_fma_f32 v73,v73,s54,v14\n"
"v_fma_f32 v74,v74,s54,v14\n"
MF "v[32:35],a[212:215],a[108:111],v[32:35]\n"
"s_add_u32 m0,s65,50944\n"
"v_fma_f32 v75,v75,s54,v14\n"
"v_fma_f32 v76,v76,s54,v14\n"
MF "v[40:43],a[212:215],a[112:115],v[40:43]\n"
LD "v8,s[36:39],s73 offen offset:256 nt lds\n"
MF "v[40:43],a[208:211],a[72:75],v[40:43]\n"
"v_fma_f32 v77,v77,s54,v14\n"
"v_fma_f32 v78,v78,s54,v14\n"
MF "v[48:51],a[208:211],a[76:79],v[48:51]\n"
"s_add_u32 m0,s65,49152\n"
"v_fma_f32 v79,v79,s54,v14\n"
CV "v167,v78,v79\n"
MF "v[48:51],a[212:215],a[116:119],v[48:51]\n"
LD "v8,s[36:39],s73 offen offset:3072 nt lds\n"
MF "v[48:51],a[216:219],a[156:159],v[48:51]\n"
"s_add_u32 m0,s65,50048\n"
CV "v166,v76,v77\n"
CV "v165,v74,v75\n"
MF "v[48:51],a[220:223],a[196:199],v[48:51]\n"
LD "v8,s[36:39],s73 offen offset:3200 nt lds\n"
MF "v[40:43],a[220:223],a[192:195],v[40:43]\n"
CV "v164,v72,v73\n"
"v_fma_f32 v64,v64,s54,v14\n"
MF "v[40:43],a[216:219],a[152:155],v[40:43]\n"
"s_add_u32 m0,s65,50944\n"
"v_fma_f32 v65,v65,s54,v14\n"
"v_fma_f32 v66,v66,s54,v14\n"
MF "v[32:35],a[216:219],a[148:151],v[32:35]\n"
LD "v8,s[36:39],s73 offen offset:3328 nt lds\n"
MF "v[32:35],a[220:223],a[188:191],v[32:35]\n"
"v_fma_f32 v67,v67,s54,v14\n"
"v_fma_f32 v68,v68,s54,v14\n"
MF "v[24:27],a[220:223],a[184:187],v[24:27]\n"
"v_fma_f32 v69,v69,s54,v14\n"
"v_fma_f32 v70,v70,s54,v14\n"
MF "v[24:27],a[216:219],a[144:147],v[24:27]\n"
"v_fma_f32 v71,v71,s54,v14\n"
CV "v163,v70,v71\n"
MF "v[16:19],a[216:219],a[140:143],v[16:19]\n"
CV "v162,v68,v69\n"
CV "v161,v66,v67\n"
MF "v[16:19],a[220:223],a[180:183],v[16:19]\n"
CV "v160,v64,v65\n"
AR "v0,a244\n"
"v_xor_b32 v1,64,v0\n"
"s_waitcnt vmcnt(12) lgkmcnt(0)\n"
"s_barrier\n"
"ds_read_b128 v[92:95],v0\n"
"ds_read_b128 v[96:99],v1\n"
"ds_read_b128 v[100:103],v0 offset:1024\n"
"v_pk_fma_f32 v[56:57],v[56:57],s[54:55],v[14:15] op_sel_hi:[1,1,0]\n"
"v_pk_fma_f32 v[58:59],v[58:59],s[54:55],v[14:15] op_sel_hi:[1,1,0]\n"
"v_pk_fma_f32 v[60:61],v[60:61],s[54:55],v[14:15] op_sel_hi:[1,1,0]\n"
"v_pk_fma_f32 v[62:63],v[62:63],s[54:55],v[14:15] op_sel_hi:[1,1,0]\n"
CV "v159,v62,v63\n"
CV "v158,v60,v61\n"
CV "v157,v58,v59\n"
CV "v156,v56,v57\n"
"v_pk_mul_f32 v[48:49],v[48:49],s[54:55]\n"
"v_pk_mul_f32 v[50:51],v[50:51],s[54:55]\n"
"v_pk_mul_f32 v[52:53],v[52:53],s[54:55]\n"
"v_pk_mul_f32 v[54:55],v[54:55],s[54:55]\n"
CV "v155,v54,v55\n"
CV "v154,v52,v53\n"
CV "v153,v50,v51\n"
CV "v152,v48,v49\n"
"v_pk_mul_f32 v[40:41],v[40:41],s[54:55]\n"
"v_pk_mul_f32 v[42:43],v[42:43],s[54:55]\n"
"v_pk_mul_f32 v[44:45],v[44:45],s[54:55]\n"
"v_pk_mul_f32 v[46:47],v[46:47],s[54:55]\n"
CV "v151,v46,v47\n"
CV "v150,v44,v45\n"
CV "v149,v42,v43\n"
CV "v148,v40,v41\n"
"v_pk_mul_f32 v[32:33],v[32:33],s[54:55]\n"
"v_pk_mul_f32 v[34:35],v[34:35],s[54:55]\n"
"v_pk_mul_f32 v[36:37],v[36:37],s[54:55]\n"
"v_pk_mul_f32 v[38:39],v[38:39],s[54:55]\n"
CV "v147,v38,v39\n"
CV "v146,v36,v37\n"
CV "v145,v34,v35\n"
CV "v144,v32,v33\n"
"v_pk_mul_f32 v[24:25],v[24:25],s[54:55]\n"
"v_pk_mul_f32 v[26:27],v[26:27],s[54:55]\n"
"v_pk_mul_f32 v[28:29],v[28:29],s[54:55]\n"
"v_pk_mul_f32 v[30:31],v[30:31],s[54:55]\n"
CV "v143,v30,v31\n"
CV "v142,v28,v29\n"
CV "v141,v26,v27\n"
CV "v140,v24,v25\n"
"v_pk_mul_f32 v[16:17],v[16:17],s[54:55]\n"
"v_pk_mul_f32 v[18:19],v[18:19],s[54:55]\n"
"v_pk_mul_f32 v[20:21],v[20:21],s[54:55]\n"
"v_pk_mul_f32 v[22:23],v[22:23],s[54:55]\n"
CV "v139,v22,v23\n"
CV "v138,v20,v21\n"
CV "v137,v18,v19\n"
CV "v136,v16,v17\n"
"s_cmp_eq_u32 s57,s69\n"
"s_cbranch_scc1 .LattB\n"
AR "v2,a245\n"
"v_add_u32 v2,49152,v2\n"
"v_xor_b32 v3,32,v2\n"
"v_xor_b32 v4,64,v2\n"
"v_xor_b32 v5,96,v2\n"
"s_mov_b32 s61,7\n"
".Latt2x:\n"
"s_cmp_eq_u32 s57,1\n"
"s_cbranch_scc1 .Lt0kx\n"
".Lt0nx:\n"
"s_waitcnt lgkmcnt(2)\n"
MF "v[72:75],v[92:95],v[136:139],0\n"
"s_add_u32 s73,s73,12288\n"
"ds_read_b128 v[104:107],v1 offset:1024\n"
AR "v10,a224\n"
MF "v[76:79],v[92:95],v[140:143],0\n"
"s_cmp_eq_u32 s73,s19\n"
"s_cselect_b32 s73,s18,s73\n"
"s_add_u32 m0,s65,73728\n"
MF "v[80:83],v[92:95],v[144:147],0\n"
LD "v8,s[36:39],s73 offen nt lds\n"
MF "v[84:87],v[92:95],v[148:151],0\n"
"v_max_f32_dpp v10,|v10|,|v10| quad_perm:[1,0,3,2] row_mask:0xf bank_mask:0xf\n"
AR "v11,a225\n"
MF "v[88:91],v[92:95],v[152:155],0\n"
".Lpg9b:\n"
"s_cbranch_execz .Lpgpsl_b\n"
"s_waitcnt lgkmcnt(2)\n"
"ds_read_b128 v[92:95],v0 offset:2048\n"
AR "v12,a226\n"
MF "v[72:75],v[96:99],v[156:159],v[72:75]\n"
"s_add_u32 m0,s65,74624\n"
"v_max_f32_dpp v10,v10,v10 quad_perm:[2,3,0,1] row_mask:0xf bank_mask:0xf\n"
"v_max_f32_dpp v11,|v11|,|v11| quad_perm:[1,0,3,2] row_mask:0xf bank_mask:0xf\n"
MF "v[76:79],v[96:99],v[160:163],v[76:79]\n"
LD "v8,s[36:39],s73 offen offset:128 nt lds\n"
MF "v[80:83],v[96:99],v[164:167],v[80:83]\n"
"v_max_f32_dpp v10,v10,v10 row_half_mirror row_mask:0xf bank_mask:0xf\n"
"v_max_f32_dpp v11,v11,v11 quad_perm:[2,3,0,1] row_mask:0xf bank_mask:0xf\n"
MF "v[84:87],v[96:99],v[168:171],v[84:87]\n"
"v_readlane_b32 s13,v10,0\n"
"v_readlane_b32 s0,v10,24\n"
MF "v[88:91],v[96:99],v[172:175],v[88:91]\n"
"s_waitcnt lgkmcnt(2)\n"
"ds_read_b128 v[96:99],v1 offset:2048\n"
"v_readlane_b32 s1,v10,32\n"
MF "v[72:75],v[100:103],v[176:179],v[72:75]\n"
"s_add_u32 m0,s65,75520\n"
"v_readlane_b32 s2,v10,56\n"
"v_max_f32_dpp v11,v11,v11 row_half_mirror row_mask:0xf bank_mask:0xf\n"
MF "v[76:79],v[100:103],v[180:183],v[76:79]\n"
LD "v8,s[36:39],s73 offen offset:256 nt lds\n"
MF "v[80:83],v[100:103],v[184:187],v[80:83]\n"
"s_min_u32 s13,s13,s0\n"
"v_readlane_b32 s11,v11,0\n"
"v_readlane_b32 s3,v11,24\n"
MF "v[84:87],v[100:103],v[188:191],v[84:87]\n"
"s_min_u32 s1,s1,s2\n"
"v_readlane_b32 s4,v11,32\n"
"v_readlane_b32 s5,v11,56\n"
MF "v[88:91],v[100:103],v[192:195],v[88:91]\n"
"s_waitcnt lgkmcnt(2)\n"
"ds_read_b128 v[100:103],v0 offset:6144\n"
"v_max_f32_dpp v12,|v12|,|v12| quad_perm:[1,0,3,2] row_mask:0xf bank_mask:0xf\n"
MF "v[72:75],v[104:107],v[196:199],v[72:75]\n"
"s_add_u32 m0,s65,73728\n"
MF "v[76:79],v[104:107],v[200:203],v[76:79]\n"
LD "v8,s[36:39],s73 offen offset:3072 nt lds\n"
MF "v[80:83],v[104:107],v[204:207],v[80:83]\n"
"s_min_u32 s13,s13,s1\n"
"v_max_f32_dpp v12,v12,v12 quad_perm:[2,3,0,1] row_mask:0xf bank_mask:0xf\n"
MF "v[84:87],v[104:107],v[208:211],v[84:87]\n"
"s_and_b32 s13,s13,0x7f800000\n"
MF "v[88:91],v[104:107],v[212:215],v[88:91]\n"
"s_waitcnt lgkmcnt(2)\n"
"ds_read_b128 v[104:107],v1 offset:6144\n"
"v_max_f32_dpp v12,v12,v12 row_half_mirror row_mask:0xf bank_mask:0xf\n"
MF "v[72:75],v[92:95],v[216:219],v[72:75]\n"
"s_add_u32 m0,s65,74624\n"
"v_readlane_b32 s10,v12,0\n"
"v_readlane_b32 s6,v12,24\n"
MF "v[76:79],v[92:95],v[220:223],v[76:79]\n"
LD "v8,s[36:39],s73 offen offset:3200 nt lds\n"
MF "v[80:83],v[92:95],v[224:227],v[80:83]\n"
"s_sub_u32 s13,s13,0x14000000\n"
"v_readlane_b32 s7,v12,32\n"
"v_readlane_b32 s8,v12,56\n"
MF "v[84:87],v[92:95],v[228:231],v[84:87]\n"
"s_cmp_lt_u32 s13,0x50000000\n"
"s_cselect_b32 s13,s13,0xed400000\n"
MF "v[88:91],v[92:95],v[232:235],v[88:91]\n"
"s_waitcnt lgkmcnt(2)\n"
"ds_read_b128 v[92:95],v0 offset:7168\n"
MF "v[72:75],v[96:99],v[236:239],v[72:75]\n"
"s_add_u32 m0,s65,75520\n"
MF "v[76:79],v[96:99],v[240:243],v[76:79]\n"
LD "v8,s[36:39],s73 offen offset:3328 nt lds\n"
MF "v[80:83],v[96:99],v[244:247],v[80:83]\n"
"s_add_u32 s13,s13,0x12c00000\n"
MF "v[84:87],v[96:99],v[248:251],v[84:87]\n"
"s_min_u32 s11,s11,s3\n"
MF "v[88:91],v[96:99],v[252:255],v[88:91]\n"
"s_waitcnt lgkmcnt(2)\n"
"ds_read_b128 v[96:99],v1 offset:7168\n"
MF "v[112:115],v[100:103],v[136:139],0\n"
"s_min_u32 s4,s4,s5\n"
MF "v[116:119],v[100:103],v[140:143],0\n"
"s_min_u32 s11,s11,s4\n"
MF "v[120:123],v[100:103],v[144:147],0\n"
"s_and_b32 s11,s11,0x7f800000\n"
MF "v[124:127],v[100:103],v[148:151],0\n"
"s_sub_u32 s11,s11,0x14000000\n"
MF "v[128:131],v[100:103],v[152:155],0\n"
"s_waitcnt lgkmcnt(2)\n"
"ds_read_b128 v[100:103],v0 offset:8192\n"
MF "v[112:115],v[104:107],v[156:159],v[112:115]\n"
"s_min_u32 s10,s10,s6\n"
"v_max3_f32 v52,v72,v73,v74\n"
"v_max_f32 v52,v52,v75\n"
MF "v[116:119],v[104:107],v[160:163],v[116:119]\n"
"s_min_u32 s7,s7,s8\n"
"v_max3_f32 v53,v76,v77,v78\n"
"v_max_f32 v53,v53,v79\n"
MF "v[120:123],v[104:107],v[164:167],v[120:123]\n"
"s_min_u32 s10,s10,s7\n"
"v_max3_f32 v54,v80,v81,v82\n"
"v_max_f32 v54,v54,v83\n"
MF "v[124:127],v[104:107],v[168:171],v[124:127]\n"
"v_max3_f32 v55,v84,v85,v86\n"
"v_max_f32 v55,v55,v87\n"
MF "v[128:131],v[104:107],v[172:175],v[128:131]\n"
"s_waitcnt lgkmcnt(2)\n"
"ds_read_b128 v[104:107],v1 offset:8192\n"
"v_max3_f32 v56,v88,v89,v90\n"
MF "v[112:115],v[92:95],v[176:179],v[112:115]\n"
"s_cmp_lt_u32 s11,0x50000000\n"
"s_cselect_b32 s11,s11,0xed400000\n"
"v_max_f32 v56,v56,v91\n"
MF "v[116:119],v[92:95],v[180:183],v[116:119]\n"
"s_add_u32 s11,s11,0x12c00000\n"
MF "v[120:123],v[92:95],v[184:187],v[120:123]\n"
MF "v[124:127],v[92:95],v[188:191],v[124:127]\n"
MF "v[128:131],v[92:95],v[192:195],v[128:131]\n"
"s_waitcnt lgkmcnt(2)\n"
MF "v[112:115],v[96:99],v[196:199],v[112:115]\n"
"s_waitcnt lgkmcnt(0)\n"
MF "v[116:119],v[96:99],v[200:203],v[116:119]\n"
"s_barrier\n"
MF "v[120:123],v[96:99],v[204:207],v[120:123]\n"
"s_add_u32 s72,s72,6144\n"
MF "v[124:127],v[96:99],v[208:211],v[124:127]\n"
"s_cmp_eq_u32 s72,s17\n"
"s_cselect_b32 s72,s16,s72\n"
"s_add_u32 m0,s64,0\n"
MF "v[128:131],v[96:99],v[212:215],v[128:131]\n"
LD "v7,s[32:35],s72 offen nt lds\n"
MF "v[112:115],v[100:103],v[216:219],v[112:115]\n"
"s_add_u32 m0,s64,896\n"
MF "v[116:119],v[100:103],v[220:223],v[116:119]\n"
LD "v7,s[32:35],s72 offen offset:128 nt lds\n"
MF "v[120:123],v[100:103],v[224:227],v[120:123]\n"
"s_add_u32 m0,s64,1792\n"
MF "v[124:127],v[100:103],v[228:231],v[124:127]\n"
LD "v7,s[32:35],s72 offen offset:256 nt lds\n"
MF "v[128:131],v[100:103],v[232:235],v[128:131]\n"
"s_add_u32 s72,s72,6144\n"
"s_add_u32 m0,s64,6144\n"
MF "v[112:115],v[104:107],v[236:239],v[112:115]\n"
LD "v7,s[32:35],s72 offen nt lds\n"
MF "v[116:119],v[104:107],v[240:243],v[116:119]\n"
"s_add_u32 m0,s64,7040\n"
MF "v[120:123],v[104:107],v[244:247],v[120:123]\n"
LD "v7,s[32:35],s72 offen offset:128 nt lds\n"
MF "v[124:127],v[104:107],v[248:251],v[124:127]\n"
"s_add_u32 m0,s64,7936\n"
MF "v[128:131],v[104:107],v[252:255],v[128:131]\n"
LD "v7,s[32:35],s72 offen offset:256 nt lds\n"
"s_branch .Lt0ex\n"
".Lpgt0kx_d:\n"
"s_cbranch_execz .Lisr2\n"
".Lt0kx:\n"
"s_lshl_b32 s58,s56,10\n"
"s_add_u32 s58,s58,73728\n"
"v_add_u32 v32,s58,v9\n"
"v_add_u32 v33,65536,v32\n"
"s_waitcnt lgkmcnt(2)\n"
MF "v[72:75],v[92:95],v[136:139],0\n"
"s_add_u32 s73,s73,12288\n"
"ds_read_b128 v[104:107],v1 offset:1024\n"
AR "v10,a224\n"
MF "v[76:79],v[92:95],v[140:143],0\n"
"s_cmp_eq_u32 s73,s19\n"
"s_cselect_b32 s73,s18,s73\n"
"s_add_u32 m0,s65,73728\n"
MF "v[80:83],v[92:95],v[144:147],0\n"
LD "v8,s[36:39],s73 offen nt lds\n"
MF "v[84:87],v[92:95],v[148:151],0\n"
"v_max_f32_dpp v10,|v10|,|v10| quad_perm:[1,0,3,2] row_mask:0xf bank_mask:0xf\n"
AR "v11,a225\n"
MF "v[88:91],v[92:95],v[152:155],0\n"
"s_waitcnt lgkmcnt(2)\n"
"ds_read_b128 v[92:95],v0 offset:2048\n"
AR "v12,a226\n"
MF "v[72:75],v[96:99],v[156:159],v[72:75]\n"
"s_add_u32 m0,s65,74624\n"
"v_max_f32_dpp v10,v10,v10 quad_perm:[2,3,0,1] row_mask:0xf bank_mask:0xf\n"
"v_max_f32_dpp v11,|v11|,|v11| quad_perm:[1,0,3,2] row_mask:0xf bank_mask:0xf\n"
MF "v[76:79],v[96:99],v[160:163],v[76:79]\n"
LD "v8,s[36:39],s73 offen offset:128 nt lds\n"
MF "v[80:83],v[96:99],v[164:167],v[80:83]\n"
"v_max_f32_dpp v10,v10,v10 row_half_mirror row_mask:0xf bank_mask:0xf\n"
"v_max_f32_dpp v11,v11,v11 quad_perm:[2,3,0,1] row_mask:0xf bank_mask:0xf\n"
MF "v[84:87],v[96:99],v[168:171],v[84:87]\n"
"v_readlane_b32 s13,v10,0\n"
"v_readlane_b32 s0,v10,24\n"
MF "v[88:91],v[96:99],v[172:175],v[88:91]\n"
"s_waitcnt lgkmcnt(2)\n"
"ds_read_b128 v[96:99],v1 offset:2048\n"
"v_readlane_b32 s1,v10,32\n"
MF "v[72:75],v[100:103],v[176:179],v[72:75]\n"
"s_add_u32 m0,s65,75520\n"
"v_readlane_b32 s2,v10,56\n"
"v_max_f32_dpp v11,v11,v11 row_half_mirror row_mask:0xf bank_mask:0xf\n"
MF "v[76:79],v[100:103],v[180:183],v[76:79]\n"
LD "v8,s[36:39],s73 offen offset:256 nt lds\n"
MF "v[80:83],v[100:103],v[184:187],v[80:83]\n"
"s_min_u32 s13,s13,s0\n"
"v_readlane_b32 s11,v11,0\n"
"v_readlane_b32 s3,v11,24\n"
MF "v[84:87],v[100:103],v[188:191],v[84:87]\n"
"s_min_u32 s1,s1,s2\n"
"v_readlane_b32 s4,v11,32\n"
"v_readlane_b32 s5,v11,56\n"
MF "v[88:91],v[100:103],v[192:195],v[88:91]\n"
"s_waitcnt lgkmcnt(2)\n"
"ds_read_b128 v[100:103],v0 offset:6144\n"
"v_max_f32_dpp v12,|v12|,|v12| quad_perm:[1,0,3,2] row_mask:0xf bank_mask:0xf\n"
MF "v[72:75],v[104:107],v[196:199],v[72:75]\n"
"s_add_u32 m0,s65,73728\n"
"ds_write_b128 v32,a[0:3] offset:24576\n"
MF "v[76:79],v[104:107],v[200:203],v[76:79]\n"
LD "v8,s[36:39],s73 offen offset:3072 nt lds\n"
MF "v[80:83],v[104:107],v[204:207],v[80:83]\n"
"s_min_u32 s13,s13,s1\n"
"v_max_f32_dpp v12,v12,v12 quad_perm:[2,3,0,1] row_mask:0xf bank_mask:0xf\n"
AR "v62,a20\n"
MF "v[84:87],v[104:107],v[208:211],v[84:87]\n"
"s_and_b32 s13,s13,0x7f800000\n"
"ds_write_b128 v32,a[4:7] offset:28672\n"
MF "v[88:91],v[104:107],v[212:215],v[88:91]\n"
"s_waitcnt lgkmcnt(4)\n"
"ds_read_b128 v[104:107],v1 offset:6144\n"
"v_max_f32_dpp v12,v12,v12 row_half_mirror row_mask:0xf bank_mask:0xf\n"
MF "v[72:75],v[92:95],v[216:219],v[72:75]\n"
"s_add_u32 m0,s65,74624\n"
"v_readlane_b32 s10,v12,0\n"
"v_readlane_b32 s6,v12,24\n"
MF "v[76:79],v[92:95],v[220:223],v[76:79]\n"
LD "v8,s[36:39],s73 offen offset:3200 nt lds\n"
MF "v[80:83],v[92:95],v[224:227],v[80:83]\n"
"s_sub_u32 s13,s13,0x14000000\n"
"v_readlane_b32 s7,v12,32\n"
"v_readlane_b32 s8,v12,56\n"
MF "v[84:87],v[92:95],v[228:231],v[84:87]\n"
"s_cmp_lt_u32 s13,0x50000000\n"
"s_cselect_b32 s13,s13,0xed400000\n"
AR "v63,a21\n"
MF "v[88:91],v[92:95],v[232:235],v[88:91]\n"
"s_waitcnt lgkmcnt(4)\n"
"ds_read_b128 v[92:95],v0 offset:7168\n"
AR "v64,a22\n"
MF "v[72:75],v[96:99],v[236:239],v[72:75]\n"
"s_add_u32 m0,s65,75520\n"
"ds_write_b128 v32,a[8:11] offset:32768\n"
MF "v[76:79],v[96:99],v[240:243],v[76:79]\n"
LD "v8,s[36:39],s73 offen offset:3328 nt lds\n"
MF "v[80:83],v[96:99],v[244:247],v[80:83]\n"
"s_add_u32 s13,s13,0x12c00000\n"
"ds_write_b128 v32,a[12:15] offset:36864\n"
MF "v[84:87],v[96:99],v[248:251],v[84:87]\n"
"s_min_u32 s11,s11,s3\n"
"ds_write_b128 v32,a[16:19] offset:40960\n"
MF "v[88:91],v[96:99],v[252:255],v[88:91]\n"
"s_waitcnt lgkmcnt(7)\n"
"ds_read_b128 v[96:99],v1 offset:7168\n"
AR "v65,a23\n"
MF "v[112:115],v[100:103],v[136:139],0\n"
"s_min_u32 s4,s4,s5\n"
"ds_write_b128 v32,a[40:43] offset:45056\n"
MF "v[116:119],v[100:103],v[140:143],0\n"
"s_min_u32 s11,s11,s4\n"
"ds_write_b128 v32,a[44:47] offset:49152\n"
MF "v[120:123],v[100:103],v[144:147],0\n"
"s_and_b32 s11,s11,0x7f800000\n"
"ds_write_b128 v32,a[48:51] offset:53248\n"
MF "v[124:127],v[100:103],v[148:151],0\n"
"s_sub_u32 s11,s11,0x14000000\n"
"ds_write_b128 v32,a[52:55] offset:57344\n"
MF "v[128:131],v[100:103],v[152:155],0\n"
"s_waitcnt lgkmcnt(9)\n"
"ds_read_b128 v[100:103],v0 offset:8192\n"
AR "v66,a24\n"
MF "v[112:115],v[104:107],v[156:159],v[112:115]\n"
"s_min_u32 s10,s10,s6\n"
"v_max3_f32 v52,v72,v73,v74\n"
"v_max_f32 v52,v52,v75\n"
MF "v[116:119],v[104:107],v[160:163],v[116:119]\n"
"s_min_u32 s7,s7,s8\n"
"v_max3_f32 v53,v76,v77,v78\n"
"v_max_f32 v53,v53,v79\n"
MF "v[120:123],v[104:107],v[164:167],v[120:123]\n"
"s_min_u32 s10,s10,s7\n"
"v_max3_f32 v54,v80,v81,v82\n"
"v_max_f32 v54,v54,v83\n"
MF "v[124:127],v[104:107],v[168:171],v[124:127]\n"
"v_max3_f32 v55,v84,v85,v86\n"
"v_max_f32 v55,v55,v87\n"
MF "v[128:131],v[104:107],v[172:175],v[128:131]\n"
"s_waitcnt lgkmcnt(9)\n"
"ds_read_b128 v[104:107],v1 offset:8192\n"
"v_max3_f32 v56,v88,v89,v90\n"
MF "v[112:115],v[92:95],v[176:179],v[112:115]\n"
"s_cmp_lt_u32 s11,0x50000000\n"
"s_cselect_b32 s11,s11,0xed400000\n"
"v_max_f32 v56,v56,v91\n"
MF "v[116:119],v[92:95],v[180:183],v[116:119]\n"
"s_add_u32 s11,s11,0x12c00000\n"
"ds_write_b128 v32,a[56:59] offset:61440\n"
MF "v[120:123],v[92:95],v[184:187],v[120:123]\n"
"ds_write_b128 v33,a[80:83]\n"
MF "v[124:127],v[92:95],v[188:191],v[124:127]\n"
"ds_write_b128 v33,a[84:87] offset:4096\n"
MF "v[128:131],v[92:95],v[192:195],v[128:131]\n"
"s_waitcnt lgkmcnt(9)\n"
"ds_write_b128 v33,a[88:91] offset:8192\n"
MF "v[112:115],v[96:99],v[196:199],v[112:115]\n"
"s_waitcnt lgkmcnt(0)\n"
AR "v67,a25\n"
AR "v68,a26\n"
MF "v[116:119],v[96:99],v[200:203],v[116:119]\n"
"s_barrier\n"
MF "v[120:123],v[96:99],v[204:207],v[120:123]\n"
"s_add_u32 s72,s72,6144\n"
"ds_write_b128 v33,a[92:95] offset:12288\n"
MF "v[124:127],v[96:99],v[208:211],v[124:127]\n"
"s_cmp_eq_u32 s72,s17\n"
"s_cselect_b32 s72,s16,s72\n"
"s_add_u32 m0,s64,0\n"
MF "v[128:131],v[96:99],v[212:215],v[128:131]\n"
LD "v7,s[32:35],s72 offen nt lds\n"
MF "v[112:115],v[100:103],v[216:219],v[112:115]\n"
"s_add_u32 m0,s64,896\n"
"ds_write_b128 v33,a[96:99] offset:16384\n"
MF "v[116:119],v[100:103],v[220:223],v[116:119]\n"
LD "v7,s[32:35],s72 offen offset:128 nt lds\n"
MF "v[120:123],v[100:103],v[224:227],v[120:123]\n"
"s_add_u32 m0,s64,1792\n"
AR "v69,a27\n"
AR "v70,a28\n"
MF "v[124:127],v[100:103],v[228:231],v[124:127]\n"
LD "v7,s[32:35],s72 offen offset:256 nt lds\n"
MF "v[128:131],v[100:103],v[232:235],v[128:131]\n"
"s_add_u32 s72,s72,6144\n"
"s_add_u32 m0,s64,6144\n"
AR "v71,a29\n"
MF "v[112:115],v[104:107],v[236:239],v[112:115]\n"
LD "v7,s[32:35],s72 offen nt lds\n"
MF "v[116:119],v[104:107],v[240:243],v[116:119]\n"
"s_add_u32 m0,s64,7040\n"
"ds_write_b128 v33,a[120:123] offset:20480\n"
MF "v[120:123],v[104:107],v[244:247],v[120:123]\n"
LD "v7,s[32:35],s72 offen offset:128 nt lds\n"
MF "v[124:127],v[104:107],v[248:251],v[124:127]\n"
"s_add_u32 m0,s64,7936\n"
AR "v132,a30\n"
AR "v133,a31\n"
MF "v[128:131],v[104:107],v[252:255],v[128:131]\n"
LD "v7,s[32:35],s72 offen offset:256 nt lds\n"
AR "v134,a32\n"
AR "v135,a33\n"
AR "v6,a34\n"
AR "v13,a35\n"
AR "v14,a36\n"
AR "v15,a37\n"
"v_accvgpr_mov_b32 a254,a38\n"
"v_accvgpr_mov_b32 a255,a39\n"
"v_accvgpr_mov_b32 a240,a168\n"
"v_accvgpr_mov_b32 a241,a169\n"
"v_accvgpr_mov_b32 a242,a170\n"
"v_accvgpr_mov_b32 a243,a171\n"
"v_accvgpr_mov_b32 a244,a172\n"
"v_accvgpr_mov_b32 a245,a173\n"
"v_accvgpr_mov_b32 a246,a174\n"
"v_accvgpr_mov_b32 a247,a175\n"
"v_accvgpr_mov_b32 a248,a176\n"
"v_accvgpr_mov_b32 a249,a177\n"
"v_accvgpr_mov_b32 a250,a178\n"
"v_accvgpr_mov_b32 a251,a179\n"
".Lpgt0ex_b:\n"
"s_cbranch_execz .Lpg2a\n"
".Lt0ex:\n"
"s_nop 1\n"
"v_max3_f32 v52,v52,v112,v113\n"
"v_max3_f32 v53,v53,v116,v117\n"
"v_max3_f32 v54,v54,v120,v121\n"
"v_max3_f32 v55,v55,v124,v125\n"
"v_max3_f32 v56,v56,v128,v129\n"
"v_max3_f32 v52,v52,v114,v115\n"
"v_max3_f32 v53,v53,v118,v119\n"
"v_max3_f32 v54,v54,v122,v123\n"
"v_max3_f32 v55,v55,v126,v127\n"
"v_max3_f32 v56,v56,v130,v131\n"
"v_mov_b32 v57,v52\n"
"v_mov_b32 v58,v53\n"
"v_mov_b32 v59,v54\n"
"v_mov_b32 v60,v55\n"
"v_mov_b32 v61,v56\n"
"v_permlane16_swap_b32 v52,v57\n"
"v_permlane16_swap_b32 v53,v58\n"
"v_permlane16_swap_b32 v54,v59\n"
"v_permlane16_swap_b32 v55,v60\n"
"v_permlane16_swap_b32 v56,v61\n"
"v_max_f32 v52,v52,v57\n"
"v_max_f32 v53,v53,v58\n"
"v_max_f32 v54,v54,v59\n"
"v_max_f32 v55,v55,v60\n"
"v_max_f32 v56,v56,v61\n"
"v_mov_b32 v57,v52\n"
"v_mov_b32 v58,v53\n"
"v_mov_b32 v59,v54\n"
"v_mov_b32 v60,v55\n"
"v_mov_b32 v61,v56\n"
"v_permlane32_swap_b32 v52,v57\n"
"v_permlane32_swap_b32 v53,v58\n"
"v_permlane32_swap_b32 v54,v59\n"
"v_permlane32_swap_b32 v55,v60\n"
"v_permlane32_swap_b32 v56,v61\n"
"v_max_f32 v52,v52,v57\n"
"v_max_f32 v53,v53,v58\n"
"v_max_f32 v54,v54,v59\n"
"v_max_f32 v55,v55,v60\n"
"v_max_f32 v56,v56,v61\n"
"v_sub_f32 v32,0xc1300000,v52\n"
"v_sub_f32 v36,0xc1300000,v53\n"
"v_sub_f32 v40,0xc1300000,v54\n"
"v_sub_f32 v44,0xc1300000,v55\n"
"v_sub_f32 v48,0xc1300000,v56\n"
"v_mov_b32_e64 v33,v32\n"
"v_pk_mov_b32 v[34:35],v[32:33],v[32:33]\n"
"v_mov_b32 v37,v36\n"
"v_pk_mov_b32 v[38:39],v[36:37],v[36:37]\n"
"v_mov_b32 v41,v40\n"
"v_pk_mov_b32 v[42:43],v[40:41],v[40:41]\n"
"v_mov_b32 v45,v44\n"
"v_pk_mov_b32 v[46:47],v[44:45],v[44:45]\n"
"v_mov_b32 v49,v48\n"
"v_pk_mov_b32 v[50:51],v[48:49],v[48:49]\n"
"v_pk_add_f32 v[72:73],v[72:73],v[32:33]\n"
"v_pk_add_f32 v[74:75],v[74:75],v[32:33]\n"
"v_pk_add_f32 v[112:113],v[112:113],v[32:33]\n"
"v_pk_add_f32 v[114:115],v[114:115],v[32:33]\n"
"v_exp_f32 v72,v72\n"
"v_exp_f32 v73,v73\n"
"v_exp_f32 v74,v74\n"
"v_exp_f32 v75,v75\n"
"v_exp_f32 v112,v112\n"
"v_exp_f32 v113,v113\n"
"v_exp_f32 v114,v114\n"
"v_exp_f32 v115,v115\n"
"v_pk_add_f32 v[10:11],v[72:73],v[74:75]\n"
"v_pk_add_f32 v[28:29],v[112:113],v[114:115]\n"
"v_pk_add_f32 v[10:11],v[10:11],v[28:29]\n"
"v_add_f32_e64 v52,v10,v11\n"
CV "v72,v72,v73\n"
CV "v73,v74,v75\n"
CV "v74,v112,v113\n"
CV "v75,v114,v115\n"
"v_pk_add_f32 v[76:77],v[76:77],v[36:37]\n"
"v_pk_add_f32 v[78:79],v[78:79],v[36:37]\n"
"v_pk_add_f32 v[116:117],v[116:117],v[36:37]\n"
"v_pk_add_f32 v[118:119],v[118:119],v[36:37]\n"
"v_exp_f32 v76,v76\n"
"v_exp_f32 v77,v77\n"
"v_exp_f32 v78,v78\n"
"v_exp_f32 v79,v79\n"
"v_exp_f32 v116,v116\n"
"v_exp_f32 v117,v117\n"
"v_exp_f32 v118,v118\n"
"v_exp_f32 v119,v119\n"
"v_pk_add_f32 v[10:11],v[76:77],v[78:79]\n"
"v_pk_add_f32 v[28:29],v[116:117],v[118:119]\n"
"v_pk_add_f32 v[10:11],v[10:11],v[28:29]\n"
"v_add_f32_e64 v53,v10,v11\n"
CV "v76,v76,v77\n"
CV "v77,v78,v79\n"
CV "v78,v116,v117\n"
CV "v79,v118,v119\n"
"v_pk_add_f32 v[80:81],v[80:81],v[40:41]\n"
"v_pk_add_f32 v[82:83],v[82:83],v[40:41]\n"
"v_pk_add_f32 v[120:121],v[120:121],v[40:41]\n"
"v_pk_add_f32 v[122:123],v[122:123],v[40:41]\n"
"v_exp_f32 v80,v80\n"
"v_exp_f32 v81,v81\n"
"v_exp_f32 v82,v82\n"
"v_exp_f32 v83,v83\n"
"v_exp_f32 v120,v120\n"
"v_exp_f32 v121,v121\n"
"v_exp_f32 v122,v122\n"
"v_exp_f32 v123,v123\n"
"v_pk_add_f32 v[10:11],v[80:81],v[82:83]\n"
"v_pk_add_f32 v[28:29],v[120:121],v[122:123]\n"
"v_pk_add_f32 v[10:11],v[10:11],v[28:29]\n"
"v_add_f32_e64 v54,v10,v11\n"
CV "v80,v80,v81\n"
CV "v81,v82,v83\n"
CV "v82,v120,v121\n"
CV "v83,v122,v123\n"
"v_pk_add_f32 v[84:85],v[84:85],v[44:45]\n"
"v_pk_add_f32 v[86:87],v[86:87],v[44:45]\n"
"v_pk_add_f32 v[124:125],v[124:125],v[44:45]\n"
"v_pk_add_f32 v[126:127],v[126:127],v[44:45]\n"
"v_mov_b32_e64 v55,0\n"
"v_pk_add_f32 v[88:89],v[88:89],v[48:49]\n"
"v_pk_add_f32 v[90:91],v[90:91],v[48:49]\n"
"v_pk_add_f32 v[128:129],v[128:129],v[48:49]\n"
"v_pk_add_f32 v[130:131],v[130:131],v[48:49]\n"
"v_mov_b32 v56,0\n"
"s_waitcnt vmcnt(18) lgkmcnt(0)\n"
"s_barrier\n"
"ds_read_b128 v[16:19],v0 offset:24576\n"
"ds_read_b128 v[20:23],v1 offset:24576\n"
AW "a220,s13\n"
AW "a221,s13\n"
AW "a222,s13\n"
AW "a223,s13\n"
"s_waitcnt lgkmcnt(1)\n"
MF "v[92:95],v[16:19],v[136:139],v[32:35]\n"
"s_nop 0\n"
"ds_read_b128 v[24:27],v0 offset:25600\n"
"ds_read_b128 v[28:31],v1 offset:25600\n"
"s_waitcnt lgkmcnt(2)\n"
MF "v[92:95],v[20:23],v[156:159],v[92:95]\n"
"v_exp_f32 v84,v84\n"
MF "v[96:99],v[20:23],v[160:163],v[36:39]\n"
"v_exp_f32 v85,v85\n"
MF "v[96:99],v[16:19],v[140:143],v[96:99]\n"
"v_exp_f32 v86,v86\n"
MF "v[100:103],v[16:19],v[144:147],v[40:43]\n"
"v_exp_f32 v87,v87\n"
MF "v[100:103],v[20:23],v[164:167],v[100:103]\n"
"v_add_f32 v55,v55,v84\n"
"v_add_f32 v55,v55,v85\n"
MF "v[104:107],v[20:23],v[168:171],v[44:47]\n"
CV "v84,v84,v85\n"
"v_add_f32 v55,v55,v86\n"
MF "v[104:107],v[16:19],v[148:151],v[104:107]\n"
"v_add_f32 v55,v55,v87\n"
CV "v85,v86,v87\n"
MF "v[108:111],v[16:19],v[152:155],v[48:51]\n"
"v_exp_f32 v124,v124\n"
MF "v[108:111],v[20:23],v[172:175],v[108:111]\n"
"s_waitcnt lgkmcnt(0)\n"
"ds_read_b128 v[16:19],v0 offset:26624\n"
"ds_read_b128 v[20:23],v1 offset:26624\n"
MF "v[108:111],v[24:27],v[192:195],v[108:111]\n"
"v_exp_f32 v125,v125\n"
MF "v[108:111],v[28:31],v[212:215],v[108:111]\n"
"v_exp_f32 v126,v126\n"
MF "v[104:107],v[28:31],v[208:211],v[104:107]\n"
"v_exp_f32 v127,v127\n"
MF "v[104:107],v[24:27],v[188:191],v[104:107]\n"
"v_add_f32 v55,v55,v124\n"
"v_add_f32 v55,v55,v125\n"
MF "v[100:103],v[24:27],v[184:187],v[100:103]\n"
CV "v86,v124,v125\n"
"v_add_f32 v55,v55,v126\n"
MF "v[100:103],v[28:31],v[204:207],v[100:103]\n"
"v_add_f32 v55,v55,v127\n"
CV "v87,v126,v127\n"
MF "v[96:99],v[28:31],v[200:203],v[96:99]\n"
"v_exp_f32 v88,v88\n"
MF "v[96:99],v[24:27],v[180:183],v[96:99]\n"
"v_exp_f32 v89,v89\n"
MF "v[92:95],v[24:27],v[176:179],v[92:95]\n"
"v_exp_f32 v90,v90\n"
MF "v[92:95],v[28:31],v[196:199],v[92:95]\n"
"s_waitcnt lgkmcnt(0)\n"
"ds_read_b128 v[24:27],v0 offset:30720\n"
"ds_read_b128 v[28:31],v1 offset:30720\n"
MF "v[92:95],v[16:19],v[216:219],v[92:95]\n"
"v_exp_f32 v91,v91\n"
MF "v[92:95],v[20:23],v[236:239],v[92:95]\n"
"v_add_f32 v56,v56,v88\n"
"v_add_f32 v56,v56,v89\n"
MF "v[96:99],v[20:23],v[240:243],v[96:99]\n"
CV "v88,v88,v89\n"
"v_add_f32 v56,v56,v90\n"
MF "v[96:99],v[16:19],v[220:223],v[96:99]\n"
"v_add_f32 v56,v56,v91\n"
CV "v89,v90,v91\n"
MF "v[100:103],v[16:19],v[224:227],v[100:103]\n"
"v_exp_f32 v128,v128\n"
MF "v[100:103],v[20:23],v[244:247],v[100:103]\n"
"v_exp_f32 v129,v129\n"
MF "v[104:107],v[20:23],v[248:251],v[104:107]\n"
"v_exp_f32 v130,v130\n"
MF "v[104:107],v[16:19],v[228:231],v[104:107]\n"
"v_exp_f32 v131,v131\n"
MF "v[108:111],v[16:19],v[232:235],v[108:111]\n"
"v_add_f32 v56,v56,v128\n"
"v_add_f32 v56,v56,v129\n"
MF "v[108:111],v[20:23],v[252:255],v[108:111]\n"
"s_waitcnt lgkmcnt(0)\n"
"ds_read_b128 v[16:19],v0 offset:31744\n"
"ds_read_b128 v[20:23],v1 offset:31744\n"
MF "v[112:115],v[24:27],v[136:139],v[32:35]\n"
CV "v90,v128,v129\n"
"v_add_f32 v56,v56,v130\n"
MF "v[112:115],v[28:31],v[156:159],v[112:115]\n"
"v_add_f32 v56,v56,v131\n"
CV "v91,v130,v131\n"
MF "v[116:119],v[28:31],v[160:163],v[36:39]\n"
"v_exp_f32 v92,v92\n"
MF "v[116:119],v[24:27],v[140:143],v[116:119]\n"
"v_exp_f32 v93,v93\n"
MF "v[120:123],v[24:27],v[144:147],v[40:43]\n"
"v_exp_f32 v94,v94\n"
MF "v[120:123],v[28:31],v[164:167],v[120:123]\n"
"v_exp_f32 v95,v95\n"
MF "v[124:127],v[28:31],v[168:171],v[44:47]\n"
"v_add_f32 v57,v52,v92\n"
"v_add_f32 v57,v57,v93\n"
MF "v[124:127],v[24:27],v[148:151],v[124:127]\n"
CV "v92,v92,v93\n"
"v_add_f32 v57,v57,v94\n"
MF "v[128:131],v[24:27],v[152:155],v[48:51]\n"
".Lpg10b:\n"
"s_cbranch_execz .Lpg1a\n"
".Lpg10d:\n"
"s_cbranch_execz .Lisr2\n"
"v_add_f32 v57,v57,v95\n"
CV "v93,v94,v95\n"
MF "v[128:131],v[28:31],v[172:175],v[128:131]\n"
"s_waitcnt lgkmcnt(0)\n"
"ds_read_b128 v[24:27],v0 offset:32768\n"
"ds_read_b128 v[28:31],v1 offset:32768\n"
MF "v[128:131],v[16:19],v[192:195],v[128:131]\n"
"v_exp_f32 v96,v96\n"
MF "v[128:131],v[20:23],v[212:215],v[128:131]\n"
"v_exp_f32 v97,v97\n"
MF "v[124:127],v[20:23],v[208:211],v[124:127]\n"
"v_exp_f32 v98,v98\n"
MF "v[124:127],v[16:19],v[188:191],v[124:127]\n"
"v_exp_f32 v99,v99\n"
MF "v[120:123],v[16:19],v[184:187],v[120:123]\n"
"v_add_f32 v58,v53,v96\n"
"v_add_f32 v58,v58,v97\n"
MF "v[120:123],v[20:23],v[204:207],v[120:123]\n"
CV "v96,v96,v97\n"
"v_add_f32 v58,v58,v98\n"
MF "v[116:119],v[20:23],v[200:203],v[116:119]\n"
"s_waitcnt vmcnt(12) lgkmcnt(0)\n"
"v_add_f32 v58,v58,v99\n"
CV "v97,v98,v99\n"
MF "v[116:119],v[16:19],v[180:183],v[116:119]\n"
"s_barrier\n"
MF "v[112:115],v[16:19],v[176:179],v[112:115]\n"
"v_exp_f32 v100,v100\n"
MF "v[112:115],v[20:23],v[196:199],v[112:115]\n"
"s_nop 0\n"
TR "v[16:17],v2\n"
TR "v[18:19],v2 offset:6144\n"
MF "v[112:115],v[24:27],v[216:219],v[112:115]\n"
"s_add_u32 s72,s72,6144\n"
TR "v[20:21],v3\n"
TR "v[22:23],v3 offset:6144\n"
MF "v[112:115],v[28:31],v[236:239],v[112:115]\n"
"s_cmp_eq_u32 s72,s17\n"
"s_cselect_b32 s72,s16,s72\n"
"s_add_u32 m0,s64,24576\n"
MF "v[116:119],v[28:31],v[240:243],v[116:119]\n"
LD "v7,s[32:35],s72 offen nt lds\n"
MF "v[116:119],v[24:27],v[220:223],v[116:119]\n"
"s_add_u32 m0,s64,25472\n"
"v_exp_f32 v101,v101\n"
MF "v[120:123],v[24:27],v[224:227],v[120:123]\n"
LD "v7,s[32:35],s72 offen offset:128 nt lds\n"
MF "v[120:123],v[28:31],v[244:247],v[120:123]\n"
"v_exp_f32 v102,v102\n"
MF "v[124:127],v[28:31],v[248:251],v[124:127]\n"
"s_add_u32 m0,s64,26368\n"
"v_exp_f32 v103,v103\n"
MF "v[124:127],v[24:27],v[228:231],v[124:127]\n"
LD "v7,s[32:35],s72 offen offset:256 nt lds\n"
MF "v[128:131],v[24:27],v[232:235],v[128:131]\n"
"s_add_u32 s72,s72,6144\n"
"v_add_f32 v59,v54,v100\n"
"v_add_f32 v59,v59,v101\n"
MF "v[128:131],v[28:31],v[252:255],v[128:131]\n"
"s_waitcnt lgkmcnt(0)\n"
TR "v[24:25],v4\n"
TR "v[26:27],v4 offset:6144\n"
MF "a[0:3],v[16:19],v[72:75],a[220:223]\n"
"s_add_u32 m0,s64,30720\n"
TR "v[28:29],v5\n"
TR "v[30:31],v5 offset:6144\n"
MF "a[20:23],v[20:23],v[72:75],a[220:223]\n"
LD "v7,s[32:35],s72 offen nt lds\n"
MF "a[24:27],v[20:23],v[76:79],a[220:223]\n"
CV "v100,v100,v101\n"
"v_add_f32 v59,v59,v102\n"
MF "a[4:7],v[16:19],v[76:79],a[220:223]\n"
"s_add_u32 m0,s64,31616\n"
"v_add_f32 v59,v59,v103\n"
CV "v101,v102,v103\n"
MF "a[8:11],v[16:19],v[80:83],a[220:223]\n"
LD "v7,s[32:35],s72 offen offset:128 nt lds\n"
MF "a[28:31],v[20:23],v[80:83],a[220:223]\n"
"v_exp_f32 v104,v104\n"
MF "a[32:35],v[20:23],v[84:87],a[220:223]\n"
"s_add_u32 m0,s64,32512\n"
"v_exp_f32 v105,v105\n"
MF "a[12:15],v[16:19],v[84:87],a[220:223]\n"
LD "v7,s[32:35],s72 offen offset:256 nt lds\n"
MF "a[16:19],v[16:19],v[88:91],a[220:223]\n"
"v_exp_f32 v106,v106\n"
MF "a[36:39],v[20:23],v[88:91],a[220:223]\n"
"s_waitcnt lgkmcnt(0)\n"
TR "v[16:17],v2 offset:1024\n"
TR "v[18:19],v2 offset:7168\n"
MF "a[56:59],v[24:27],v[88:91],a[220:223]\n"
"s_nop 0\n"
TR "v[20:21],v3 offset:1024\n"
TR "v[22:23],v3 offset:7168\n"
MF "a[76:79],v[28:31],v[88:91],a[220:223]\n"
"v_exp_f32 v107,v107\n"
MF "a[72:75],v[28:31],v[84:87],a[220:223]\n"
"v_add_f32 v60,v55,v104\n"
"v_add_f32 v60,v60,v105\n"
MF "a[52:55],v[24:27],v[84:87],a[220:223]\n"
CV "v104,v104,v105\n"
"v_add_f32 v60,v60,v106\n"
MF "a[48:51],v[24:27],v[80:83],a[220:223]\n"
"v_add_f32 v60,v60,v107\n"
CV "v105,v106,v107\n"
MF "a[68:71],v[28:31],v[80:83],a[220:223]\n"
"v_exp_f32 v108,v108\n"
MF "a[64:67],v[28:31],v[76:79],a[220:223]\n"
"v_exp_f32 v109,v109\n"
MF "a[44:47],v[24:27],v[76:79],a[220:223]\n"
"v_exp_f32 v110,v110\n"
MF "a[40:43],v[24:27],v[72:75],a[220:223]\n"
"v_exp_f32 v111,v111\n"
MF "a[60:63],v[28:31],v[72:75],a[220:223]\n"
"s_waitcnt lgkmcnt(0)\n"
TR "v[24:25],v4 offset:1024\n"
TR "v[26:27],v4 offset:7168\n"
MF "a[80:83],v[16:19],v[72:75],a[220:223]\n"
"s_nop 0\n"
TR "v[28:29],v5 offset:1024\n"
TR "v[30:31],v5 offset:7168\n"
MF "a[100:103],v[20:23],v[72:75],a[220:223]\n"
"v_add_f32 v61,v56,v108\n"
"v_add_f32 v61,v61,v109\n"
MF "a[104:107],v[20:23],v[76:79],a[220:223]\n"
CV "v108,v108,v109\n"
"v_add_f32 v61,v61,v110\n"
MF "a[84:87],v[16:19],v[76:79],a[220:223]\n"
"v_add_f32 v61,v61,v111\n"
CV "v109,v110,v111\n"
MF "a[88:91],v[16:19],v[80:83],a[220:223]\n"
"v_exp_f32 v112,v112\n"
MF "a[108:111],v[20:23],v[80:83],a[220:223]\n"
"v_exp_f32 v113,v113\n"
MF "a[112:115],v[20:23],v[84:87],a[220:223]\n"
"v_exp_f32 v114,v114\n"
MF "a[92:95],v[16:19],v[84:87],a[220:223]\n"
"v_exp_f32 v115,v115\n"
MF "a[96:99],v[16:19],v[88:91],a[220:223]\n"
"v_add_f32 v57,v57,v112\n"
"v_add_f32 v57,v57,v113\n"
MF "a[116:119],v[20:23],v[88:91],a[220:223]\n"
"s_waitcnt lgkmcnt(0)\n"
TR "v[16:17],v2 offset:2048\n"
TR "v[18:19],v2 offset:8192\n"
MF "a[136:139],v[24:27],v[88:91],a[220:223]\n"
"s_nop 0\n"
TR "v[20:21],v3 offset:2048\n"
TR "v[22:23],v3 offset:8192\n"
MF "a[156:159],v[28:31],v[88:91],a[220:223]\n"
CV "v94,v112,v113\n"
"v_add_f32 v57,v57,v114\n"
MF "a[152:155],v[28:31],v[84:87],a[220:223]\n"
"v_add_f32 v57,v57,v115\n"
CV "v95,v114,v115\n"
MF "a[132:135],v[24:27],v[84:87],a[220:223]\n"
"v_exp_f32 v116,v116\n"
MF "a[128:131],v[24:27],v[80:83],a[220:223]\n"
"v_exp_f32 v117,v117\n"
MF "a[148:151],v[28:31],v[80:83],a[220:223]\n"
"v_exp_f32 v118,v118\n"
MF "a[144:147],v[28:31],v[76:79],a[220:223]\n"
"v_exp_f32 v119,v119\n"
MF "a[124:127],v[24:27],v[76:79],a[220:223]\n"
"v_add_f32 v58,v58,v116\n"
"v_add_f32 v58,v58,v117\n"
MF "a[120:123],v[24:27],v[72:75],a[220:223]\n"
CV "v98,v116,v117\n"
"v_add_f32 v58,v58,v118\n"
MF "a[140:143],v[28:31],v[72:75],a[220:223]\n"
"s_waitcnt lgkmcnt(0)\n"
TR "v[24:25],v4 offset:2048\n"
TR "v[26:27],v4 offset:8192\n"
MF "a[160:163],v[16:19],v[72:75],a[220:223]\n"
"s_nop 0\n"
TR "v[28:29],v5 offset:2048\n"
TR "v[30:31],v5 offset:8192\n"
MF "a[180:183],v[20:23],v[72:75],a[220:223]\n"
"v_add_f32 v58,v58,v119\n"
CV "v99,v118,v119\n"
MF "a[184:187],v[20:23],v[76:79],a[220:223]\n"
"v_exp_f32 v120,v120\n"
MF "a[164:167],v[16:19],v[76:79],a[220:223]\n"
"v_exp_f32 v121,v121\n"
MF "a[168:171],v[16:19],v[80:83],a[220:223]\n"
"v_exp_f32 v122,v122\n"
MF "a[188:191],v[20:23],v[80:83],a[220:223]\n"
"v_exp_f32 v123,v123\n"
MF "a[192:195],v[20:23],v[84:87],a[220:223]\n"
"v_add_f32 v59,v59,v120\n"
"v_add_f32 v59,v59,v121\n"
"s_cmp_eq_u32 s57,0\n"
"s_cbranch_scc1 .Lisx6\n"
"s_mov_b64 exec,0\n"
"s_cmp_eq_u32 s56,0\n"
"s_cbranch_scc1 .Lpgt0e_g\n"
"s_cmp_eq_u32 s56,1\n"
"s_cbranch_scc1 .Lpg17g\n"
"s_cmp_eq_u32 s56,2\n"
"s_cbranch_scc1 .Lpg16g\n"
"s_branch .Lpgep_g\n"
".Lisr6:\n"
"s_mov_b64 exec,-1\n"
".Lisx6:\n"
MF "a[172:175],v[16:19],v[84:87],a[220:223]\n"
"s_waitcnt vmcnt(6) lgkmcnt(0)\n"
CV "v102,v120,v121\n"
"v_add_f32 v59,v59,v122\n"
MF "a[176:179],v[16:19],v[88:91],a[220:223]\n"
"s_barrier\n"
MF "a[196:199],v[20:23],v[88:91],a[220:223]\n"
"s_add_u32 s73,s73,12288\n"
"ds_read_b128 v[16:19],v0\n"
"ds_read_b128 v[20:23],v1\n"
MF "a[216:219],v[24:27],v[88:91],a[220:223]\n"
"s_cmp_eq_u32 s73,s19\n"
"s_cselect_b32 s73,s18,s73\n"
"s_add_u32 m0,s65,49152\n"
MF "a[236:239],v[28:31],v[88:91],a[220:223]\n"
LD "v8,s[36:39],s73 offen nt lds\n"
MF "a[232:235],v[28:31],v[84:87],a[220:223]\n"
"s_add_u32 m0,s65,50048\n"
"v_add_f32 v59,v59,v123\n"
CV "v103,v122,v123\n"
MF "a[212:215],v[24:27],v[84:87],a[220:223]\n"
LD "v8,s[36:39],s73 offen offset:128 nt lds\n"
MF "a[208:211],v[24:27],v[80:83],a[220:223]\n"
"s_add_u32 m0,s65,50944\n"
MF "a[228:231],v[28:31],v[80:83],a[220:223]\n"
LD "v8,s[36:39],s73 offen offset:256 nt lds\n"
MF "a[224:227],v[28:31],v[76:79],a[220:223]\n"
MF "a[204:207],v[24:27],v[76:79],a[220:223]\n"
MF "a[200:203],v[24:27],v[72:75],a[220:223]\n"
MF "a[220:223],v[28:31],v[72:75],a[220:223]\n"
"s_waitcnt lgkmcnt(0)\n"
".Laloopx:\n"
MF "v[72:75],v[16:19],v[136:139],v[32:35]\n"
"s_nop 0\n"
"ds_read_b128 v[24:27],v0 offset:1024\n"
"ds_read_b128 v[28:31],v1 offset:1024\n"
MF "v[72:75],v[20:23],v[156:159],v[72:75]\n"
MF "v[76:79],v[20:23],v[160:163],v[36:39]\n"
MF "v[76:79],v[16:19],v[140:143],v[76:79]\n"
MF "v[80:83],v[16:19],v[144:147],v[40:43]\n"
MF "v[80:83],v[20:23],v[164:167],v[80:83]\n"
MF "v[84:87],v[20:23],v[168:171],v[44:47]\n"
MF "v[84:87],v[16:19],v[148:151],v[84:87]\n"
"s_add_u32 m0,s65,49152\n"
MF "v[88:91],v[16:19],v[152:155],v[48:51]\n"
LD "v8,s[36:39],s73 offen offset:3072 nt lds\n"
MF "v[88:91],v[20:23],v[172:175],v[88:91]\n"
"s_waitcnt lgkmcnt(0)\n"
"ds_read_b128 v[16:19],v0 offset:2048\n"
"ds_read_b128 v[20:23],v1 offset:2048\n"
MF "v[88:91],v[24:27],v[192:195],v[88:91]\n"
"v_exp_f32 v124,v124\n"
MF "v[88:91],v[28:31],v[212:215],v[88:91]\n"
"s_add_u32 m0,s65,50048\n"
"v_exp_f32 v125,v125\n"
MF "v[84:87],v[28:31],v[208:211],v[84:87]\n"
LD "v8,s[36:39],s73 offen offset:3200 nt lds\n"
MF "v[84:87],v[24:27],v[188:191],v[84:87]\n"
"v_exp_f32 v126,v126\n"
MF "v[80:83],v[24:27],v[184:187],v[80:83]\n"
"v_exp_f32 v127,v127\n"
MF "v[80:83],v[28:31],v[204:207],v[80:83]\n"
"s_add_u32 m0,s65,50944\n"
"v_add_f32 v60,v60,v124\n"
"v_add_f32 v60,v60,v125\n"
MF "v[76:79],v[28:31],v[200:203],v[76:79]\n"
LD "v8,s[36:39],s73 offen offset:3328 nt lds\n"
MF "v[76:79],v[24:27],v[180:183],v[76:79]\n"
CV "v106,v124,v125\n"
"v_add_f32 v60,v60,v126\n"
MF "v[72:75],v[24:27],v[176:179],v[72:75]\n"
"v_add_f32 v60,v60,v127\n"
CV "v107,v126,v127\n"
MF "v[72:75],v[28:31],v[196:199],v[72:75]\n"
"s_waitcnt lgkmcnt(0)\n"
"ds_read_b128 v[24:27],v0 offset:6144\n"
"ds_read_b128 v[28:31],v1 offset:6144\n"
MF "v[72:75],v[16:19],v[216:219],v[72:75]\n"
"v_exp_f32 v128,v128\n"
MF "v[72:75],v[20:23],v[236:239],v[72:75]\n"
"v_exp_f32 v129,v129\n"
MF "v[76:79],v[20:23],v[240:243],v[76:79]\n"
"v_exp_f32 v130,v130\n"
MF "v[76:79],v[16:19],v[220:223],v[76:79]\n"
"v_exp_f32 v131,v131\n"
MF "v[80:83],v[16:19],v[224:227],v[80:83]\n"
"v_add_f32 v61,v61,v128\n"
"v_add_f32 v61,v61,v129\n"
MF "v[80:83],v[20:23],v[244:247],v[80:83]\n"
CV "v110,v128,v129\n"
"v_add_f32 v61,v61,v130\n"
MF "v[84:87],v[20:23],v[248:251],v[84:87]\n"
"v_add_f32 v61,v61,v131\n"
CV "v111,v130,v131\n"
MF "v[84:87],v[16:19],v[228:231],v[84:87]\n"
"v_max3_f32 v10,v57,v58,v59\n"
"v_max3_f32 v10,v10,v60,v61\n"
MF "v[88:91],v[16:19],v[232:235],v[88:91]\n"
"v_cmp_ngt_f32 vcc,s77,v10\n"
MF "v[88:91],v[20:23],v[252:255],v[88:91]\n"
"s_waitcnt lgkmcnt(0)\n"
"s_cbranch_vccnz .Lstub_6\n"
".Lback_6:\n"
MF "v[112:115],v[24:27],v[136:139],v[32:35]\n"
"s_add_u32 s74,s74,12288\n"
"ds_read_b128 v[16:19],v0 offset:7168\n"
"ds_read_b128 v[20:23],v1 offset:7168\n"
MF "v[112:115],v[28:31],v[156:159],v[112:115]\n"
"s_cmp_eq_u32 s74,s20\n"
"s_cselect_b32 s74,s70,s74\n"
MF "v[116:119],v[28:31],v[160:163],v[36:39]\n"
"v_exp_f32 v72,v72\n"
MF "v[116:119],v[24:27],v[140:143],v[116:119]\n"
"v_exp_f32 v73,v73\n"
MF "v[120:123],v[24:27],v[144:147],v[40:43]\n"
"v_exp_f32 v74,v74\n"
MF "v[120:123],v[28:31],v[164:167],v[120:123]\n"
"v_exp_f32 v75,v75\n"
MF "v[124:127],v[28:31],v[168:171],v[44:47]\n"
"v_add_f32 v52,v57,v72\n"
"v_add_f32 v52,v52,v73\n"
MF "v[124:127],v[24:27],v[148:151],v[124:127]\n"
CV "v72,v72,v73\n"
"v_add_f32 v52,v52,v74\n"
MF "v[128:131],v[24:27],v[152:155],v[48:51]\n"
"v_add_f32 v52,v52,v75\n"
CV "v73,v74,v75\n"
MF "v[128:131],v[28:31],v[172:175],v[128:131]\n"
"s_waitcnt lgkmcnt(0)\n"
"ds_read_b128 v[24:27],v0 offset:8192\n"
"ds_read_b128 v[28:31],v1 offset:8192\n"
MF "v[128:131],v[16:19],v[192:195],v[128:131]\n"
"v_exp_f32 v76,v76\n"
MF "v[128:131],v[20:23],v[212:215],v[128:131]\n"
"v_exp_f32 v77,v77\n"
MF "v[124:127],v[20:23],v[208:211],v[124:127]\n"
"v_exp_f32 v78,v78\n"
MF "v[124:127],v[16:19],v[188:191],v[124:127]\n"
"v_exp_f32 v79,v79\n"
MF "v[120:123],v[16:19],v[184:187],v[120:123]\n"
"v_add_f32 v53,v58,v76\n"
"v_add_f32 v53,v53,v77\n"
MF "v[120:123],v[20:23],v[204:207],v[120:123]\n"
CV "v76,v76,v77\n"
"v_add_f32 v53,v53,v78\n"
MF "v[116:119],v[20:23],v[200:203],v[116:119]\n"
"s_waitcnt vmcnt(12) lgkmcnt(0)\n"
"v_add_f32 v53,v53,v79\n"
CV "v77,v78,v79\n"
MF "v[116:119],v[16:19],v[180:183],v[116:119]\n"
"s_barrier\n"
MF "v[112:115],v[16:19],v[176:179],v[112:115]\n"
"v_exp_f32 v80,v80\n"
MF "v[112:115],v[20:23],v[196:199],v[112:115]\n"
"s_nop 0\n"
TR "v[16:17],v2 offset:24576\n"
TR "v[18:19],v2 offset:30720\n"
MF "v[112:115],v[24:27],v[216:219],v[112:115]\n"
"s_add_u32 s72,s72,6144\n"
TR "v[20:21],v3 offset:24576\n"
TR "v[22:23],v3 offset:30720\n"
MF "v[112:115],v[28:31],v[236:239],v[112:115]\n"
"s_cmp_eq_u32 s72,s17\n"
"s_cselect_b32 s72,s16,s72\n"
"s_add_u32 m0,s64,0\n"
MF "v[116:119],v[28:31],v[240:243],v[116:119]\n"
LD "v7,s[32:35],s72 offen nt lds\n"
MF "v[116:119],v[24:27],v[220:223],v[116:119]\n"
"s_add_u32 m0,s64,896\n"
"v_exp_f32 v81,v81\n"
MF "v[120:123],v[24:27],v[224:227],v[120:123]\n"
LD "v7,s[32:35],s72 offen offset:128 nt lds\n"
MF "v[120:123],v[28:31],v[244:247],v[120:123]\n"
"v_exp_f32 v82,v82\n"
MF "v[124:127],v[28:31],v[248:251],v[124:127]\n"
"s_add_u32 m0,s64,1792\n"
"v_exp_f32 v83,v83\n"
MF "v[124:127],v[24:27],v[228:231],v[124:127]\n"
LD "v7,s[32:35],s72 offen offset:256 nt lds\n"
MF "v[128:131],v[24:27],v[232:235],v[128:131]\n"
"s_add_u32 s72,s72,6144\n"
"v_add_f32 v54,v59,v80\n"
"v_add_f32 v54,v54,v81\n"
MF "v[128:131],v[28:31],v[252:255],v[128:131]\n"
"s_waitcnt lgkmcnt(0)\n"
TR "v[24:25],v4 offset:24576\n"
TR "v[26:27],v4 offset:30720\n"
MF "a[0:3],v[16:19],v[92:95],a[0:3]\n"
"s_add_u32 m0,s64,6144\n"
TR "v[28:29],v5 offset:24576\n"
TR "v[30:31],v5 offset:30720\n"
MF "a[20:23],v[20:23],v[92:95],a[20:23]\n"
LD "v7,s[32:35],s72 offen nt lds\n"
MF "a[24:27],v[20:23],v[96:99],a[24:27]\n"
CV "v80,v80,v81\n"
"v_add_f32 v54,v54,v82\n"
MF "a[4:7],v[16:19],v[96:99],a[4:7]\n"
"s_add_u32 m0,s64,7040\n"
"v_add_f32 v54,v54,v83\n"
CV "v81,v82,v83\n"
MF "a[8:11],v[16:19],v[100:103],a[8:11]\n"
LD "v7,s[32:35],s72 offen offset:128 nt lds\n"
MF "a[28:31],v[20:23],v[100:103],a[28:31]\n"
"v_exp_f32 v84,v84\n"
MF "a[32:35],v[20:23],v[104:107],a[32:35]\n"
"s_add_u32 m0,s64,7936\n"
"v_exp_f32 v85,v85\n"
MF "a[12:15],v[16:19],v[104:107],a[12:15]\n"
LD "v7,s[32:35],s72 offen offset:256 nt lds\n"
MF "a[16:19],v[16:19],v[108:111],a[16:19]\n"
"v_exp_f32 v86,v86\n"
MF "a[36:39],v[20:23],v[108:111],a[36:39]\n"
"s_waitcnt lgkmcnt(0)\n"
TR "v[16:17],v2 offset:25600\n"
TR "v[18:19],v2 offset:31744\n"
MF "a[56:59],v[24:27],v[108:111],a[56:59]\n"
"s_nop 0\n"
TR "v[20:21],v3 offset:25600\n"
TR "v[22:23],v3 offset:31744\n"
MF "a[76:79],v[28:31],v[108:111],a[76:79]\n"
"v_exp_f32 v87,v87\n"
MF "a[72:75],v[28:31],v[104:107],a[72:75]\n"
"v_add_f32 v55,v60,v84\n"
"v_add_f32 v55,v55,v85\n"
MF "a[52:55],v[24:27],v[104:107],a[52:55]\n"
CV "v84,v84,v85\n"
"v_add_f32 v55,v55,v86\n"
MF "a[48:51],v[24:27],v[100:103],a[48:51]\n"
"v_add_f32 v55,v55,v87\n"
CV "v85,v86,v87\n"
MF "a[68:71],v[28:31],v[100:103],a[68:71]\n"
"v_exp_f32 v88,v88\n"
MF "a[64:67],v[28:31],v[96:99],a[64:67]\n"
"v_exp_f32 v89,v89\n"
MF "a[44:47],v[24:27],v[96:99],a[44:47]\n"
"v_exp_f32 v90,v90\n"
MF "a[40:43],v[24:27],v[92:95],a[40:43]\n"
"v_exp_f32 v91,v91\n"
MF "a[60:63],v[28:31],v[92:95],a[60:63]\n"
"s_waitcnt lgkmcnt(0)\n"
TR "v[24:25],v4 offset:25600\n"
TR "v[26:27],v4 offset:31744\n"
MF "a[80:83],v[16:19],v[92:95],a[80:83]\n"
"s_nop 0\n"
TR "v[28:29],v5 offset:25600\n"
TR "v[30:31],v5 offset:31744\n"
MF "a[100:103],v[20:23],v[92:95],a[100:103]\n"
"v_add_f32 v56,v61,v88\n"
"v_add_f32 v56,v56,v89\n"
MF "a[104:107],v[20:23],v[96:99],a[104:107]\n"
CV "v88,v88,v89\n"
"v_add_f32 v56,v56,v90\n"
MF "a[84:87],v[16:19],v[96:99],a[84:87]\n"
"v_add_f32 v56,v56,v91\n"
CV "v89,v90,v91\n"
MF "a[88:91],v[16:19],v[100:103],a[88:91]\n"
"v_exp_f32 v112,v112\n"
MF "a[108:111],v[20:23],v[100:103],a[108:111]\n"
"v_exp_f32 v113,v113\n"
MF "a[112:115],v[20:23],v[104:107],a[112:115]\n"
"v_exp_f32 v114,v114\n"
MF "a[92:95],v[16:19],v[104:107],a[92:95]\n"
"v_exp_f32 v115,v115\n"
MF "a[96:99],v[16:19],v[108:111],a[96:99]\n"
"v_add_f32 v52,v52,v112\n"
"v_add_f32 v52,v52,v113\n"
MF "a[116:119],v[20:23],v[108:111],a[116:119]\n"
"s_waitcnt lgkmcnt(0)\n"
TR "v[16:17],v2 offset:26624\n"
TR "v[18:19],v2 offset:32768\n"
MF "a[136:139],v[24:27],v[108:111],a[136:139]\n"
"s_nop 0\n"
TR "v[20:21],v3 offset:26624\n"
TR "v[22:23],v3 offset:32768\n"
MF "a[156:159],v[28:31],v[108:111],a[156:159]\n"
CV "v74,v112,v113\n"
"v_add_f32 v52,v52,v114\n"
MF "a[152:155],v[28:31],v[104:107],a[152:155]\n"
"v_add_f32 v52,v52,v115\n"
CV "v75,v114,v115\n"
MF "a[132:135],v[24:27],v[104:107],a[132:135]\n"
"v_exp_f32 v116,v116\n"
MF "a[128:131],v[24:27],v[100:103],a[128:131]\n"
"v_exp_f32 v117,v117\n"
MF "a[148:151],v[28:31],v[100:103],a[148:151]\n"
"v_exp_f32 v118,v118\n"
MF "a[144:147],v[28:31],v[96:99],a[144:147]\n"
"v_exp_f32 v119,v119\n"
MF "a[124:127],v[24:27],v[96:99],a[124:127]\n"
"v_add_f32 v53,v53,v116\n"
"v_add_f32 v53,v53,v117\n"
MF "a[120:123],v[24:27],v[92:95],a[120:123]\n"
CV "v78,v116,v117\n"
"v_add_f32 v53,v53,v118\n"
MF "a[140:143],v[28:31],v[92:95],a[140:143]\n"
"s_waitcnt lgkmcnt(0)\n"
TR "v[24:25],v4 offset:26624\n"
TR "v[26:27],v4 offset:32768\n"
MF "a[160:163],v[16:19],v[92:95],a[160:163]\n"
"s_nop 0\n"
TR "v[28:29],v5 offset:26624\n"
TR "v[30:31],v5 offset:32768\n"
MF "a[180:183],v[20:23],v[92:95],a[180:183]\n"
"v_add_f32 v53,v53,v119\n"
CV "v79,v118,v119\n"
MF "a[184:187],v[20:23],v[96:99],a[184:187]\n"
"v_exp_f32 v120,v120\n"
MF "a[164:167],v[16:19],v[96:99],a[164:167]\n"
"v_exp_f32 v121,v121\n"
MF "a[168:171],v[16:19],v[100:103],a[168:171]\n"
"v_exp_f32 v122,v122\n"
MF "a[188:191],v[20:23],v[100:103],a[188:191]\n"
"v_exp_f32 v123,v123\n"
MF "a[192:195],v[20:23],v[104:107],a[192:195]\n"
"v_add_f32 v54,v54,v120\n"
"v_add_f32 v54,v54,v121\n"
MF "a[172:175],v[16:19],v[104:107],a[172:175]\n"
"s_waitcnt vmcnt(12) lgkmcnt(0)\n"
CV "v82,v120,v121\n"
"v_add_f32 v54,v54,v122\n"
MF "a[176:179],v[16:19],v[108:111],a[176:179]\n"
"s_barrier\n"
MF "a[196:199],v[20:23],v[108:111],a[196:199]\n"
"s_add_u32 s73,s73,12288\n"
"ds_read_b128 v[16:19],v0 offset:24576\n"
"ds_read_b128 v[20:23],v1 offset:24576\n"
MF "a[216:219],v[24:27],v[108:111],a[216:219]\n"
"s_cmp_eq_u32 s73,s19\n"
"s_cselect_b32 s73,s18,s73\n"
"s_add_u32 m0,s65,73728\n"
MF "a[236:239],v[28:31],v[108:111],a[236:239]\n"
LD "v8,s[36:39],s73 offen nt lds\n"
MF "a[232:235],v[28:31],v[104:107],a[232:235]\n"
"s_add_u32 m0,s65,74624\n"
"v_add_f32 v54,v54,v123\n"
CV "v83,v122,v123\n"
MF "a[212:215],v[24:27],v[104:107],a[212:215]\n"
LD "v8,s[36:39],s73 offen offset:128 nt lds\n"
MF "a[208:211],v[24:27],v[100:103],a[208:211]\n"
"s_add_u32 m0,s65,75520\n"
MF "a[228:231],v[28:31],v[100:103],a[228:231]\n"
LD "v8,s[36:39],s73 offen offset:256 nt lds\n"
MF "a[224:227],v[28:31],v[96:99],a[224:227]\n"
MF "a[204:207],v[24:27],v[96:99],a[204:207]\n"
MF "a[200:203],v[24:27],v[92:95],a[200:203]\n"
MF "a[220:223],v[28:31],v[92:95],a[220:223]\n"
"s_waitcnt lgkmcnt(0)\n"
MF "v[92:95],v[16:19],v[136:139],v[32:35]\n"
"s_add_u32 m0,s65,73728\n"
"ds_read_b128 v[24:27],v0 offset:25600\n"
"ds_read_b128 v[28:31],v1 offset:25600\n"
MF "v[92:95],v[20:23],v[156:159],v[92:95]\n"
LD "v8,s[36:39],s73 offen offset:3072 nt lds\n"
MF "v[96:99],v[20:23],v[160:163],v[36:39]\n"
"v_exp_f32 v124,v124\n"
MF "v[96:99],v[16:19],v[140:143],v[96:99]\n"
"v_exp_f32 v125,v125\n"
MF "v[100:103],v[16:19],v[144:147],v[40:43]\n"
"s_add_u32 m0,s65,74624\n"
"v_exp_f32 v126,v126\n"
MF "v[100:103],v[20:23],v[164:167],v[100:103]\n"
LD "v8,s[36:39],s73 offen offset:3200 nt lds\n"
MF "v[104:107],v[20:23],v[168:171],v[44:47]\n"
"v_exp_f32 v127,v127\n"
MF "v[104:107],v[16:19],v[148:151],v[104:107]\n"
"s_add_u32 m0,s65,75520\n"
"v_add_f32 v55,v55,v124\n"
"v_add_f32 v55,v55,v125\n"
MF "v[108:111],v[16:19],v[152:155],v[48:51]\n"
LD "v8,s[36:39],s73 offen offset:3328 nt lds\n"
MF "v[108:111],v[20:23],v[172:175],v[108:111]\n"
"s_waitcnt lgkmcnt(0)\n"
"ds_read_b128 v[16:19],v0 offset:26624\n"
"ds_read_b128 v[20:23],v1 offset:26624\n"
MF "v[108:111],v[24:27],v[192:195],v[108:111]\n"
CV "v86,v124,v125\n"
"v_add_f32 v55,v55,v126\n"
MF "v[108:111],v[28:31],v[212:215],v[108:111]\n"
"v_add_f32 v55,v55,v127\n"
CV "v87,v126,v127\n"
MF "v[104:107],v[28:31],v[208:211],v[104:107]\n"
"v_exp_f32 v128,v128\n"
MF "v[104:107],v[24:27],v[188:191],v[104:107]\n"
"v_exp_f32 v129,v129\n"
MF "v[100:103],v[24:27],v[184:187],v[100:103]\n"
"v_exp_f32 v130,v130\n"
MF "v[100:103],v[28:31],v[204:207],v[100:103]\n"
"v_exp_f32 v131,v131\n"
MF "v[96:99],v[28:31],v[200:203],v[96:99]\n"
"v_add_f32 v56,v56,v128\n"
"v_add_f32 v56,v56,v129\n"
MF "v[96:99],v[24:27],v[180:183],v[96:99]\n"
CV "v90,v128,v129\n"
"v_add_f32 v56,v56,v130\n"
MF "v[92:95],v[24:27],v[176:179],v[92:95]\n"
"v_add_f32 v56,v56,v131\n"
CV "v91,v130,v131\n"
MF "v[92:95],v[28:31],v[196:199],v[92:95]\n"
"s_waitcnt lgkmcnt(0)\n"
"ds_read_b128 v[24:27],v0 offset:30720\n"
"ds_read_b128 v[28:31],v1 offset:30720\n"
MF "v[92:95],v[16:19],v[216:219],v[92:95]\n"
MF "v[92:95],v[20:23],v[236:239],v[92:95]\n"
MF "v[96:99],v[20:23],v[240:243],v[96:99]\n"
MF "v[96:99],v[16:19],v[220:223],v[96:99]\n"
MF "v[100:103],v[16:19],v[224:227],v[100:103]\n"
MF "v[100:103],v[20:23],v[244:247],v[100:103]\n"
MF "v[104:107],v[20:23],v[248:251],v[104:107]\n"
MF "v[104:107],v[16:19],v[228:231],v[104:107]\n"
"v_max3_f32 v10,v52,v53,v54\n"
"v_max3_f32 v10,v10,v55,v56\n"
MF "v[108:111],v[16:19],v[232:235],v[108:111]\n"
"v_cmp_ngt_f32 vcc,s77,v10\n"
MF "v[108:111],v[20:23],v[252:255],v[108:111]\n"
"s_waitcnt lgkmcnt(0)\n"
"s_cbranch_vccnz .Lstub_7\n"
".Lback_7:\n"
MF "v[112:115],v[24:27],v[136:139],v[32:35]\n"
"s_add_u32 s74,s74,12288\n"
"ds_read_b128 v[16:19],v0 offset:31744\n"
"ds_read_b128 v[20:23],v1 offset:31744\n"
MF "v[112:115],v[28:31],v[156:159],v[112:115]\n"
".Lpg11b:\n"
"s_cbranch_execz .Lpgqj_a\n"
".Lpg11d:\n"
"s_cbranch_execz .Lisr2\n"
"s_cmp_eq_u32 s74,s20\n"
"s_cselect_b32 s74,s70,s74\n"
MF "v[116:119],v[28:31],v[160:163],v[36:39]\n"
"v_exp_f32 v92,v92\n"
MF "v[116:119],v[24:27],v[140:143],v[116:119]\n"
"v_exp_f32 v93,v93\n"
MF "v[120:123],v[24:27],v[144:147],v[40:43]\n"
"v_exp_f32 v94,v94\n"
MF "v[120:123],v[28:31],v[164:167],v[120:123]\n"
"v_exp_f32 v95,v95\n"
MF "v[124:127],v[28:31],v[168:171],v[44:47]\n"
"v_add_f32 v57,v52,v92\n"
"v_add_f32 v57,v57,v93\n"
MF "v[124:127],v[24:27],v[148:151],v[124:127]\n"
CV "v92,v92,v93\n"
"v_add_f32 v57,v57,v94\n"
MF "v[128:131],v[24:27],v[152:155],v[48:51]\n"
"v_add_f32 v57,v57,v95\n"
CV "v93,v94,v95\n"
MF "v[128:131],v[28:31],v[172:175],v[128:131]\n"
"s_waitcnt lgkmcnt(0)\n"
"ds_read_b128 v[24:27],v0 offset:32768\n"
"ds_read_b128 v[28:31],v1 offset:32768\n"
MF "v[128:131],v[16:19],v[192:195],v[128:131]\n"
"s_sub_u32 s61,s61,1\n"
"v_exp_f32 v96,v96\n"
MF "v[128:131],v[20:23],v[212:215],v[128:131]\n"
"v_exp_f32 v97,v97\n"
MF "v[124:127],v[20:23],v[208:211],v[124:127]\n"
"v_exp_f32 v98,v98\n"
MF "v[124:127],v[16:19],v[188:191],v[124:127]\n"
"v_exp_f32 v99,v99\n"
MF "v[120:123],v[16:19],v[184:187],v[120:123]\n"
"v_add_f32 v58,v53,v96\n"
"v_add_f32 v58,v58,v97\n"
MF "v[120:123],v[20:23],v[204:207],v[120:123]\n"
CV "v96,v96,v97\n"
"v_add_f32 v58,v58,v98\n"
MF "v[116:119],v[20:23],v[200:203],v[116:119]\n"
"s_waitcnt vmcnt(12) lgkmcnt(0)\n"
"v_add_f32 v58,v58,v99\n"
CV "v97,v98,v99\n"
MF "v[116:119],v[16:19],v[180:183],v[116:119]\n"
"s_barrier\n"
MF "v[112:115],v[16:19],v[176:179],v[112:115]\n"
"v_exp_f32 v100,v100\n"
MF "v[112:115],v[20:23],v[196:199],v[112:115]\n"
"s_nop 0\n"
TR "v[16:17],v2\n"
TR "v[18:19],v2 offset:6144\n"
MF "v[112:115],v[24:27],v[216:219],v[112:115]\n"
"s_add_u32 s72,s72,6144\n"
TR "v[20:21],v3\n"
TR "v[22:23],v3 offset:6144\n"
MF "v[112:115],v[28:31],v[236:239],v[112:115]\n"
"s_cmp_eq_u32 s72,s17\n"
"s_cselect_b32 s72,s16,s72\n"
"s_add_u32 m0,s64,24576\n"
MF "v[116:119],v[28:31],v[240:243],v[116:119]\n"
LD "v7,s[32:35],s72 offen nt lds\n"
MF "v[116:119],v[24:27],v[220:223],v[116:119]\n"
"s_add_u32 m0,s64,25472\n"
"v_exp_f32 v101,v101\n"
MF "v[120:123],v[24:27],v[224:227],v[120:123]\n"
LD "v7,s[32:35],s72 offen offset:128 nt lds\n"
MF "v[120:123],v[28:31],v[244:247],v[120:123]\n"
"v_exp_f32 v102,v102\n"
MF "v[124:127],v[28:31],v[248:251],v[124:127]\n"
"s_add_u32 m0,s64,26368\n"
"v_exp_f32 v103,v103\n"
MF "v[124:127],v[24:27],v[228:231],v[124:127]\n"
LD "v7,s[32:35],s72 offen offset:256 nt lds\n"
MF "v[128:131],v[24:27],v[232:235],v[128:131]\n"
"s_add_u32 s72,s72,6144\n"
"v_add_f32 v59,v54,v100\n"
"v_add_f32 v59,v59,v101\n"
MF "v[128:131],v[28:31],v[252:255],v[128:131]\n"
"s_waitcnt lgkmcnt(0)\n"
TR "v[24:25],v4\n"
TR "v[26:27],v4 offset:6144\n"
MF "a[0:3],v[16:19],v[72:75],a[0:3]\n"
"s_add_u32 m0,s64,30720\n"
TR "v[28:29],v5\n"
TR "v[30:31],v5 offset:6144\n"
MF "a[20:23],v[20:23],v[72:75],a[20:23]\n"
LD "v7,s[32:35],s72 offen nt lds\n"
MF "a[24:27],v[20:23],v[76:79],a[24:27]\n"
CV "v100,v100,v101\n"
"v_add_f32 v59,v59,v102\n"
MF "a[4:7],v[16:19],v[76:79],a[4:7]\n"
"s_add_u32 m0,s64,31616\n"
"v_add_f32 v59,v59,v103\n"
CV "v101,v102,v103\n"
MF "a[8:11],v[16:19],v[80:83],a[8:11]\n"
LD "v7,s[32:35],s72 offen offset:128 nt lds\n"
MF "a[28:31],v[20:23],v[80:83],a[28:31]\n"
"v_exp_f32 v104,v104\n"
MF "a[32:35],v[20:23],v[84:87],a[32:35]\n"
"s_add_u32 m0,s64,32512\n"
"v_exp_f32 v105,v105\n"
MF "a[12:15],v[16:19],v[84:87],a[12:15]\n"
LD "v7,s[32:35],s72 offen offset:256 nt lds\n"
MF "a[16:19],v[16:19],v[88:91],a[16:19]\n"
"v_exp_f32 v106,v106\n"
MF "a[36:39],v[20:23],v[88:91],a[36:39]\n"
"s_waitcnt lgkmcnt(0)\n"
TR "v[16:17],v2 offset:1024\n"
TR "v[18:19],v2 offset:7168\n"
MF "a[56:59],v[24:27],v[88:91],a[56:59]\n"
"s_nop 0\n"
TR "v[20:21],v3 offset:1024\n"
TR "v[22:23],v3 offset:7168\n"
MF "a[76:79],v[28:31],v[88:91],a[76:79]\n"
"v_exp_f32 v107,v107\n"
MF "a[72:75],v[28:31],v[84:87],a[72:75]\n"
"v_add_f32 v60,v55,v104\n"
"v_add_f32 v60,v60,v105\n"
MF "a[52:55],v[24:27],v[84:87],a[52:55]\n"
CV "v104,v104,v105\n"
"v_add_f32 v60,v60,v106\n"
MF "a[48:51],v[24:27],v[80:83],a[48:51]\n"
"v_add_f32 v60,v60,v107\n"
CV "v105,v106,v107\n"
MF "a[68:71],v[28:31],v[80:83],a[68:71]\n"
"v_exp_f32 v108,v108\n"
MF "a[64:67],v[28:31],v[76:79],a[64:67]\n"
"v_exp_f32 v109,v109\n"
MF "a[44:47],v[24:27],v[76:79],a[44:47]\n"
"v_exp_f32 v110,v110\n"
MF "a[40:43],v[24:27],v[72:75],a[40:43]\n"
"v_exp_f32 v111,v111\n"
MF "a[60:63],v[28:31],v[72:75],a[60:63]\n"
"s_waitcnt lgkmcnt(0)\n"
TR "v[24:25],v4 offset:1024\n"
TR "v[26:27],v4 offset:7168\n"
MF "a[80:83],v[16:19],v[72:75],a[80:83]\n"
"s_nop 0\n"
TR "v[28:29],v5 offset:1024\n"
TR "v[30:31],v5 offset:7168\n"
MF "a[100:103],v[20:23],v[72:75],a[100:103]\n"
"v_add_f32 v61,v56,v108\n"
"v_add_f32 v61,v61,v109\n"
MF "a[104:107],v[20:23],v[76:79],a[104:107]\n"
CV "v108,v108,v109\n"
"v_add_f32 v61,v61,v110\n"
MF "a[84:87],v[16:19],v[76:79],a[84:87]\n"
"v_add_f32 v61,v61,v111\n"
CV "v109,v110,v111\n"
MF "a[88:91],v[16:19],v[80:83],a[88:91]\n"
"v_exp_f32 v112,v112\n"
MF "a[108:111],v[20:23],v[80:83],a[108:111]\n"
"v_exp_f32 v113,v113\n"
MF "a[112:115],v[20:23],v[84:87],a[112:115]\n"
"v_exp_f32 v114,v114\n"
MF "a[92:95],v[16:19],v[84:87],a[92:95]\n"
"v_exp_f32 v115,v115\n"
MF "a[96:99],v[16:19],v[88:91],a[96:99]\n"
"v_add_f32 v57,v57,v112\n"
"v_add_f32 v57,v57,v113\n"
MF "a[116:119],v[20:23],v[88:91],a[116:119]\n"
"s_waitcnt lgkmcnt(0)\n"
TR "v[16:17],v2 offset:2048\n"
TR "v[18:19],v2 offset:8192\n"
MF "a[136:139],v[24:27],v[88:91],a[136:139]\n"
"s_nop 0\n"
TR "v[20:21],v3 offset:2048\n"
TR "v[22:23],v3 offset:8192\n"
MF "a[156:159],v[28:31],v[88:91],a[156:159]\n"
CV "v94,v112,v113\n"
"v_add_f32 v57,v57,v114\n"
MF "a[152:155],v[28:31],v[84:87],a[152:155]\n"
"v_add_f32 v57,v57,v115\n"
CV "v95,v114,v115\n"
MF "a[132:135],v[24:27],v[84:87],a[132:135]\n"
"v_exp_f32 v116,v116\n"
MF "a[128:131],v[24:27],v[80:83],a[128:131]\n"
"v_exp_f32 v117,v117\n"
MF "a[148:151],v[28:31],v[80:83],a[148:151]\n"
"v_exp_f32 v118,v118\n"
MF "a[144:147],v[28:31],v[76:79],a[144:147]\n"
"v_exp_f32 v119,v119\n"
MF "a[124:127],v[24:27],v[76:79],a[124:127]\n"
"v_add_f32 v58,v58,v116\n"
"v_add_f32 v58,v58,v117\n"
MF "a[120:123],v[24:27],v[72:75],a[120:123]\n"
CV "v98,v116,v117\n"
"v_add_f32 v58,v58,v118\n"
MF "a[140:143],v[28:31],v[72:75],a[140:143]\n"
"s_waitcnt lgkmcnt(0)\n"
TR "v[24:25],v4 offset:2048\n"
TR "v[26:27],v4 offset:8192\n"
MF "a[160:163],v[16:19],v[72:75],a[160:163]\n"
"s_nop 0\n"
TR "v[28:29],v5 offset:2048\n"
TR "v[30:31],v5 offset:8192\n"
MF "a[180:183],v[20:23],v[72:75],a[180:183]\n"
"v_add_f32 v58,v58,v119\n"
CV "v99,v118,v119\n"
MF "a[184:187],v[20:23],v[76:79],a[184:187]\n"
"v_exp_f32 v120,v120\n"
MF "a[164:167],v[16:19],v[76:79],a[164:167]\n"
"v_exp_f32 v121,v121\n"
MF "a[168:171],v[16:19],v[80:83],a[168:171]\n"
"v_exp_f32 v122,v122\n"
MF "a[188:191],v[20:23],v[80:83],a[188:191]\n"
"v_exp_f32 v123,v123\n"
MF "a[192:195],v[20:23],v[84:87],a[192:195]\n"
"v_add_f32 v59,v59,v120\n"
"v_add_f32 v59,v59,v121\n"
MF "a[172:175],v[16:19],v[84:87],a[172:175]\n"
"s_waitcnt vmcnt(12) lgkmcnt(0)\n"
CV "v102,v120,v121\n"
"v_add_f32 v59,v59,v122\n"
MF "a[176:179],v[16:19],v[88:91],a[176:179]\n"
"s_barrier\n"
MF "a[196:199],v[20:23],v[88:91],a[196:199]\n"
"s_add_u32 s73,s73,12288\n"
"ds_read_b128 v[16:19],v0\n"
"ds_read_b128 v[20:23],v1\n"
MF "a[216:219],v[24:27],v[88:91],a[216:219]\n"
"s_cmp_eq_u32 s73,s19\n"
"s_cselect_b32 s73,s18,s73\n"
"s_add_u32 m0,s65,49152\n"
MF "a[236:239],v[28:31],v[88:91],a[236:239]\n"
LD "v8,s[36:39],s73 offen nt lds\n"
MF "a[232:235],v[28:31],v[84:87],a[232:235]\n"
"s_add_u32 m0,s65,50048\n"
"v_add_f32 v59,v59,v123\n"
CV "v103,v122,v123\n"
MF "a[212:215],v[24:27],v[84:87],a[212:215]\n"
LD "v8,s[36:39],s73 offen offset:128 nt lds\n"
MF "a[208:211],v[24:27],v[80:83],a[208:211]\n"
"s_add_u32 m0,s65,50944\n"
MF "a[228:231],v[28:31],v[80:83],a[228:231]\n"
LD "v8,s[36:39],s73 offen offset:256 nt lds\n"
MF "a[224:227],v[28:31],v[76:79],a[224:227]\n"
MF "a[204:207],v[24:27],v[76:79],a[204:207]\n"
"s_waitcnt lgkmcnt(0)\n"
MF "a[200:203],v[24:27],v[72:75],a[200:203]\n"
"s_cmp_lg_u32 s61,1\n"
MF "a[220:223],v[28:31],v[72:75],a[220:223]\n"
"s_cbranch_scc1 .Laloopx\n"
"s_cmp_eq_u32 s57,0\n"
"s_cbranch_scc0 .Lep\n"
".Lepx:\n"
MF "v[72:75],v[16:19],v[136:139],v[32:35]\n"
"s_add_u32 m0,s65,49152\n"
"ds_read_b128 v[24:27],v0 offset:1024\n"
"ds_read_b128 v[28:31],v1 offset:1024\n"
MF "v[72:75],v[20:23],v[156:159],v[72:75]\n"
LD "v8,s[36:39],s73 offen offset:3072 nt lds\n"
MF "v[76:79],v[20:23],v[160:163],v[36:39]\n"
"v_exp_f32 v124,v124\n"
MF "v[76:79],v[16:19],v[140:143],v[76:79]\n"
"v_exp_f32 v125,v125\n"
MF "v[80:83],v[16:19],v[144:147],v[40:43]\n"
"s_add_u32 m0,s65,50048\n"
"v_exp_f32 v126,v126\n"
MF "v[80:83],v[20:23],v[164:167],v[80:83]\n"
LD "v8,s[36:39],s73 offen offset:3200 nt lds\n"
MF "v[84:87],v[20:23],v[168:171],v[44:47]\n"
"v_exp_f32 v127,v127\n"
MF "v[84:87],v[16:19],v[148:151],v[84:87]\n"
"s_add_u32 m0,s65,50944\n"
"v_add_f32 v60,v60,v124\n"
"v_add_f32 v60,v60,v125\n"
MF "v[88:91],v[16:19],v[152:155],v[48:51]\n"
LD "v8,s[36:39],s73 offen offset:3328 nt lds\n"
MF "v[88:91],v[20:23],v[172:175],v[88:91]\n"
"s_waitcnt lgkmcnt(0)\n"
"ds_read_b128 v[16:19],v0 offset:2048\n"
"ds_read_b128 v[20:23],v1 offset:2048\n"
MF "v[88:91],v[24:27],v[192:195],v[88:91]\n"
CV "v106,v124,v125\n"
"v_add_f32 v60,v60,v126\n"
MF "v[88:91],v[28:31],v[212:215],v[88:91]\n"
"v_add_f32 v60,v60,v127\n"
CV "v107,v126,v127\n"
MF "v[84:87],v[28:31],v[208:211],v[84:87]\n"
"v_exp_f32 v128,v128\n"
MF "v[84:87],v[24:27],v[188:191],v[84:87]\n"
"v_exp_f32 v129,v129\n"
MF "v[80:83],v[24:27],v[184:187],v[80:83]\n"
"v_exp_f32 v130,v130\n"
MF "v[80:83],v[28:31],v[204:207],v[80:83]\n"
"v_exp_f32 v131,v131\n"
MF "v[76:79],v[28:31],v[200:203],v[76:79]\n"
"v_add_f32 v61,v61,v128\n"
"v_add_f32 v61,v61,v129\n"
MF "v[76:79],v[24:27],v[180:183],v[76:79]\n"
CV "v110,v128,v129\n"
"v_add_f32 v61,v61,v130\n"
MF "v[72:75],v[24:27],v[176:179],v[72:75]\n"
"v_add_f32 v61,v61,v131\n"
CV "v111,v130,v131\n"
MF "v[72:75],v[28:31],v[196:199],v[72:75]\n"
"s_waitcnt lgkmcnt(0)\n"
"ds_read_b128 v[24:27],v0 offset:6144\n"
"ds_read_b128 v[28:31],v1 offset:6144\n"
MF "v[72:75],v[16:19],v[216:219],v[72:75]\n"
MF "v[72:75],v[20:23],v[236:239],v[72:75]\n"
MF "v[76:79],v[20:23],v[240:243],v[76:79]\n"
MF "v[76:79],v[16:19],v[220:223],v[76:79]\n"
MF "v[80:83],v[16:19],v[224:227],v[80:83]\n"
MF "v[80:83],v[20:23],v[244:247],v[80:83]\n"
MF "v[84:87],v[20:23],v[248:251],v[84:87]\n"
MF "v[84:87],v[16:19],v[228:231],v[84:87]\n"
"v_max3_f32 v10,v57,v58,v59\n"
"v_max3_f32 v10,v10,v60,v61\n"
MF "v[88:91],v[16:19],v[232:235],v[88:91]\n"
"v_cmp_le_f32 vcc,0x47800000,v10\n"
MF "v[88:91],v[20:23],v[252:255],v[88:91]\n"
"s_waitcnt lgkmcnt(0)\n"
"s_cbranch_vccnz .Lstub_8\n"
".Lback_8:\n"
MF "v[112:115],v[24:27],v[136:139],v[32:35]\n"
"s_add_u32 s74,s74,12288\n"
"ds_read_b128 v[16:19],v0 offset:7168\n"
"ds_read_b128 v[20:23],v1 offset:7168\n"
MF "v[112:115],v[28:31],v[156:159],v[112:115]\n"
"s_cmp_eq_u32 s74,s20\n"
"s_cselect_b32 s74,s70,s74\n"
MF "v[116:119],v[28:31],v[160:163],v[36:39]\n"
"v_exp_f32 v72,v72\n"
MF "v[116:119],v[24:27],v[140:143],v[116:119]\n"
"v_exp_f32 v73,v73\n"
MF "v[120:123],v[24:27],v[144:147],v[40:43]\n"
"v_exp_f32 v74,v74\n"
MF "v[120:123],v[28:31],v[164:167],v[120:123]\n"
"v_exp_f32 v75,v75\n"
MF "v[124:127],v[28:31],v[168:171],v[44:47]\n"
"v_add_f32 v52,v57,v72\n"
"v_add_f32 v52,v52,v73\n"
MF "v[124:127],v[24:27],v[148:151],v[124:127]\n"
CV "v72,v72,v73\n"
"v_add_f32 v52,v52,v74\n"
MF "v[128:131],v[24:27],v[152:155],v[48:51]\n"
"v_add_f32 v52,v52,v75\n"
CV "v73,v74,v75\n"
MF "v[128:131],v[28:31],v[172:175],v[128:131]\n"
"s_waitcnt lgkmcnt(0)\n"
"ds_read_b128 v[24:27],v0 offset:8192\n"
"ds_read_b128 v[28:31],v1 offset:8192\n"
MF "v[128:131],v[16:19],v[192:195],v[128:131]\n"
"v_exp_f32 v76,v76\n"
MF "v[128:131],v[20:23],v[212:215],v[128:131]\n"
"v_exp_f32 v77,v77\n"
MF "v[124:127],v[20:23],v[208:211],v[124:127]\n"
"v_exp_f32 v78,v78\n"
MF "v[124:127],v[16:19],v[188:191],v[124:127]\n"
"v_exp_f32 v79,v79\n"
MF "v[120:123],v[16:19],v[184:187],v[120:123]\n"
"v_add_f32 v53,v58,v76\n"
"v_add_f32 v53,v53,v77\n"
MF "v[120:123],v[20:23],v[204:207],v[120:123]\n"
CV "v76,v76,v77\n"
"v_add_f32 v53,v53,v78\n"
MF "v[116:119],v[20:23],v[200:203],v[116:119]\n"
"s_waitcnt vmcnt(12) lgkmcnt(0)\n"
"v_add_f32 v53,v53,v79\n"
CV "v77,v78,v79\n"
"s_mov_b64 exec,0\n"
"s_cmp_eq_u32 s56,0\n"
"s_cbranch_scc1 .Lpg6f\n"
"s_cmp_eq_u32 s56,1\n"
"s_cbranch_scc1 .Lpg5f\n"
"s_cmp_eq_u32 s56,2\n"
"s_cbranch_scc1 .Lpg4f\n"
"s_branch .Lpgq0_f\n"
".Lisr3:\n"
"s_mov_b64 exec,-1\n"
".Lisx3:\n"
MF "v[116:119],v[16:19],v[180:183],v[116:119]\n"
"s_barrier\n"
MF "v[112:115],v[16:19],v[176:179],v[112:115]\n"
"v_exp_f32 v80,v80\n"
MF "v[112:115],v[20:23],v[196:199],v[112:115]\n"
"s_nop 0\n"
TR "v[16:17],v2 offset:24576\n"
TR "v[18:19],v2 offset:30720\n"
MF "v[112:115],v[24:27],v[216:219],v[112:115]\n"
"s_add_u32 s66,s67,4718592\n"
TR "v[20:21],v3 offset:24576\n"
TR "v[22:23],v3 offset:30720\n"
MF "v[112:115],v[28:31],v[236:239],v[112:115]\n"
"s_sub_u32 s81,s81,384\n"
"s_mov_b32 s83,7077888\n"
"s_add_u32 m0,s62,0\n"
MF "v[116:119],v[28:31],v[240:243],v[116:119]\n"
"s_add_u32 s82,s66,2359296\n"
"s_sub_u32 s15,s82,7864320\n"
"s_min_u32 s82,s82,s15\n"
MF "v[116:119],v[24:27],v[220:223],v[116:119]\n"
LD "v6,s[28:31],s82 offen lds\n"
MF "v[120:123],v[24:27],v[224:227],v[120:123]\n"
"s_add_u32 s82,s82,97280\n"
"v_exp_f32 v81,v81\n"
MF "v[120:123],v[28:31],v[244:247],v[120:123]\n"
LD "v6,s[28:31],s82 offen offset:1024 lds\n"
MF "v[124:127],v[28:31],v[248:251],v[124:127]\n"
"v_exp_f32 v82,v82\n"
MF "v[124:127],v[24:27],v[228:231],v[124:127]\n"
"v_exp_f32 v83,v83\n"
MF "v[128:131],v[24:27],v[232:235],v[128:131]\n"
"v_add_f32 v54,v59,v80\n"
"v_add_f32 v54,v54,v81\n"
MF "v[128:131],v[28:31],v[252:255],v[128:131]\n"
"s_waitcnt lgkmcnt(0)\n"
TR "v[24:25],v4 offset:24576\n"
TR "v[26:27],v4 offset:30720\n"
MF "a[0:3],v[16:19],v[92:95],a[0:3]\n"
"s_add_u32 s82,s82,97280\n"
TR "v[28:29],v5 offset:24576\n"
TR "v[30:31],v5 offset:30720\n"
MF "a[20:23],v[20:23],v[92:95],a[20:23]\n"
LD "v6,s[28:31],s82 offen offset:2048 lds\n"
MF "a[24:27],v[20:23],v[96:99],a[24:27]\n"
CV "v80,v80,v81\n"
"v_add_f32 v54,v54,v82\n"
MF "a[4:7],v[16:19],v[96:99],a[4:7]\n"
"v_add_f32 v54,v54,v83\n"
CV "v81,v82,v83\n"
MF "a[8:11],v[16:19],v[100:103],a[8:11]\n"
"s_add_u32 s82,s82,97280\n"
"v_exp_f32 v84,v84\n"
MF "a[28:31],v[20:23],v[100:103],a[28:31]\n"
LD "v6,s[28:31],s82 offen offset:3072 lds\n"
MF "a[32:35],v[20:23],v[104:107],a[32:35]\n"
"v_exp_f32 v85,v85\n"
MF "a[12:15],v[16:19],v[104:107],a[12:15]\n"
"v_exp_f32 v86,v86\n"
MF "a[16:19],v[16:19],v[108:111],a[16:19]\n"
"v_exp_f32 v87,v87\n"
MF "a[36:39],v[20:23],v[108:111],a[36:39]\n"
"s_waitcnt lgkmcnt(0)\n"
TR "v[16:17],v2 offset:25600\n"
TR "v[18:19],v2 offset:31744\n"
MF "a[56:59],v[24:27],v[108:111],a[56:59]\n"
"s_nop 0\n"
TR "v[20:21],v3 offset:25600\n"
TR "v[22:23],v3 offset:31744\n"
MF "a[76:79],v[28:31],v[108:111],a[76:79]\n"
"v_add_f32 v55,v60,v84\n"
"v_add_f32 v55,v55,v85\n"
MF "a[72:75],v[28:31],v[104:107],a[72:75]\n"
CV "v84,v84,v85\n"
"v_add_f32 v55,v55,v86\n"
MF "a[52:55],v[24:27],v[104:107],a[52:55]\n"
"v_add_f32 v55,v55,v87\n"
CV "v85,v86,v87\n"
MF "a[48:51],v[24:27],v[100:103],a[48:51]\n"
"v_exp_f32 v88,v88\n"
MF "a[68:71],v[28:31],v[100:103],a[68:71]\n"
"v_exp_f32 v89,v89\n"
MF "a[64:67],v[28:31],v[96:99],a[64:67]\n"
"v_exp_f32 v90,v90\n"
MF "a[44:47],v[24:27],v[96:99],a[44:47]\n"
"v_exp_f32 v91,v91\n"
MF "a[40:43],v[24:27],v[92:95],a[40:43]\n"
"v_add_f32 v56,v61,v88\n"
"v_add_f32 v56,v56,v89\n"
MF "a[60:63],v[28:31],v[92:95],a[60:63]\n"
"s_waitcnt lgkmcnt(0)\n"
TR "v[24:25],v4 offset:25600\n"
TR "v[26:27],v4 offset:31744\n"
MF "a[80:83],v[16:19],v[92:95],a[80:83]\n"
"s_nop 0\n"
TR "v[28:29],v5 offset:25600\n"
TR "v[30:31],v5 offset:31744\n"
MF "a[100:103],v[20:23],v[92:95],a[100:103]\n"
CV "v88,v88,v89\n"
"v_add_f32 v56,v56,v90\n"
MF "a[104:107],v[20:23],v[96:99],a[104:107]\n"
"v_add_f32 v56,v56,v91\n"
CV "v89,v90,v91\n"
MF "a[84:87],v[16:19],v[96:99],a[84:87]\n"
"v_exp_f32 v112,v112\n"
MF "a[88:91],v[16:19],v[100:103],a[88:91]\n"
"v_exp_f32 v113,v113\n"
MF "a[108:111],v[20:23],v[100:103],a[108:111]\n"
"v_exp_f32 v114,v114\n"
MF "a[112:115],v[20:23],v[104:107],a[112:115]\n"
"v_exp_f32 v115,v115\n"
MF "a[92:95],v[16:19],v[104:107],a[92:95]\n"
"v_add_f32 v52,v52,v112\n"
"v_add_f32 v52,v52,v113\n"
MF "a[96:99],v[16:19],v[108:111],a[96:99]\n"
CV "v74,v112,v113\n"
"v_add_f32 v52,v52,v114\n"
MF "a[116:119],v[20:23],v[108:111],a[116:119]\n"
"s_waitcnt lgkmcnt(0)\n"
TR "v[16:17],v2 offset:26624\n"
TR "v[18:19],v2 offset:32768\n"
MF "a[136:139],v[24:27],v[108:111],a[136:139]\n"
"s_nop 0\n"
TR "v[20:21],v3 offset:26624\n"
TR "v[22:23],v3 offset:32768\n"
MF "a[156:159],v[28:31],v[108:111],a[156:159]\n"
"v_add_f32 v52,v52,v115\n"
CV "v75,v114,v115\n"
MF "a[152:155],v[28:31],v[104:107],a[152:155]\n"
"v_exp_f32 v116,v116\n"
MF "a[132:135],v[24:27],v[104:107],a[132:135]\n"
"v_exp_f32 v117,v117\n"
MF "a[128:131],v[24:27],v[100:103],a[128:131]\n"
"v_exp_f32 v118,v118\n"
MF "a[148:151],v[28:31],v[100:103],a[148:151]\n"
"v_exp_f32 v119,v119\n"
MF "a[144:147],v[28:31],v[96:99],a[144:147]\n"
"v_add_f32 v53,v53,v116\n"
"v_add_f32 v53,v53,v117\n"
MF "a[124:127],v[24:27],v[96:99],a[124:127]\n"
CV "v78,v116,v117\n"
"v_add_f32 v53,v53,v118\n"
MF "a[120:123],v[24:27],v[92:95],a[120:123]\n"
"v_add_f32 v53,v53,v119\n"
CV "v79,v118,v119\n"
MF "a[140:143],v[28:31],v[92:95],a[140:143]\n"
"s_waitcnt lgkmcnt(0)\n"
TR "v[24:25],v4 offset:26624\n"
TR "v[26:27],v4 offset:32768\n"
MF "a[160:163],v[16:19],v[92:95],a[160:163]\n"
"s_nop 0\n"
TR "v[28:29],v5 offset:26624\n"
TR "v[30:31],v5 offset:32768\n"
MF "a[180:183],v[20:23],v[92:95],a[180:183]\n"
"v_exp_f32 v120,v120\n"
MF "a[184:187],v[20:23],v[96:99],a[184:187]\n"
"v_exp_f32 v121,v121\n"
MF "a[164:167],v[16:19],v[96:99],a[164:167]\n"
"v_exp_f32 v122,v122\n"
MF "a[168:171],v[16:19],v[100:103],a[168:171]\n"
"v_exp_f32 v123,v123\n"
MF "a[188:191],v[20:23],v[100:103],a[188:191]\n"
"v_add_f32 v54,v54,v120\n"
"v_add_f32 v54,v54,v121\n"
MF "a[192:195],v[20:23],v[104:107],a[192:195]\n"
CV "v82,v120,v121\n"
"v_add_f32 v54,v54,v122\n"
MF "a[172:175],v[16:19],v[104:107],a[172:175]\n"
"s_waitcnt vmcnt(10) lgkmcnt(0)\n"
"v_add_f32 v54,v54,v123\n"
CV "v83,v122,v123\n"
MF "a[176:179],v[16:19],v[108:111],a[176:179]\n"
"s_barrier\n"
MF "a[196:199],v[20:23],v[108:111],a[196:199]\n"
"s_add_u32 s73,s73,12288\n"
"ds_read_b128 v[16:19],v0 offset:24576\n"
"ds_read_b128 v[20:23],v1 offset:24576\n"
MF "a[216:219],v[24:27],v[108:111],a[216:219]\n"
"s_cmp_eq_u32 s73,s19\n"
"s_cselect_b32 s73,s18,s73\n"
"s_add_u32 m0,s65,73728\n"
MF "a[236:239],v[28:31],v[108:111],a[236:239]\n"
LD "v8,s[36:39],s73 offen nt lds\n"
MF "a[232:235],v[28:31],v[104:107],a[232:235]\n"
"s_add_u32 m0,s65,74624\n"
MF "a[212:215],v[24:27],v[104:107],a[212:215]\n"
LD "v8,s[36:39],s73 offen offset:128 nt lds\n"
MF "a[208:211],v[24:27],v[100:103],a[208:211]\n"
"s_add_u32 m0,s65,75520\n"
MF "a[228:231],v[28:31],v[100:103],a[228:231]\n"
LD "v8,s[36:39],s73 offen offset:256 nt lds\n"
MF "a[224:227],v[28:31],v[96:99],a[224:227]\n"
MF "a[204:207],v[24:27],v[96:99],a[204:207]\n"
MF "a[200:203],v[24:27],v[92:95],a[200:203]\n"
MF "a[220:223],v[28:31],v[92:95],a[220:223]\n"
"s_waitcnt lgkmcnt(0)\n"
MF "v[92:95],v[16:19],v[136:139],v[32:35]\n"
"s_add_u32 m0,s65,73728\n"
"ds_read_b128 v[24:27],v0 offset:25600\n"
"ds_read_b128 v[28:31],v1 offset:25600\n"
MF "v[92:95],v[20:23],v[156:159],v[92:95]\n"
LD "v8,s[36:39],s73 offen offset:3072 nt lds\n"
MF "v[96:99],v[20:23],v[160:163],v[36:39]\n"
"v_exp_f32 v124,v124\n"
MF "v[96:99],v[16:19],v[140:143],v[96:99]\n"
"v_exp_f32 v125,v125\n"
MF "v[100:103],v[16:19],v[144:147],v[40:43]\n"
"s_add_u32 m0,s65,74624\n"
"v_exp_f32 v126,v126\n"
MF "v[100:103],v[20:23],v[164:167],v[100:103]\n"
LD "v8,s[36:39],s73 offen offset:3200 nt lds\n"
MF "v[104:107],v[20:23],v[168:171],v[44:47]\n"
"v_exp_f32 v127,v127\n"
MF "v[104:107],v[16:19],v[148:151],v[104:107]\n"
"s_add_u32 m0,s65,75520\n"
"v_add_f32 v55,v55,v124\n"
"v_add_f32 v55,v55,v125\n"
MF "v[108:111],v[16:19],v[152:155],v[48:51]\n"
LD "v8,s[36:39],s73 offen offset:3328 nt lds\n"
MF "v[108:111],v[20:23],v[172:175],v[108:111]\n"
"s_waitcnt lgkmcnt(0)\n"
"ds_read_b128 v[16:19],v0 offset:26624\n"
"ds_read_b128 v[20:23],v1 offset:26624\n"
MF "v[108:111],v[24:27],v[192:195],v[108:111]\n"
CV "v86,v124,v125\n"
"v_add_f32 v55,v55,v126\n"
MF "v[108:111],v[28:31],v[212:215],v[108:111]\n"
"v_add_f32 v55,v55,v127\n"
CV "v87,v126,v127\n"
MF "v[104:107],v[28:31],v[208:211],v[104:107]\n"
"v_exp_f32 v128,v128\n"
MF "v[104:107],v[24:27],v[188:191],v[104:107]\n"
"v_exp_f32 v129,v129\n"
MF "v[100:103],v[24:27],v[184:187],v[100:103]\n"
"v_exp_f32 v130,v130\n"
MF "v[100:103],v[28:31],v[204:207],v[100:103]\n"
"v_exp_f32 v131,v131\n"
MF "v[96:99],v[28:31],v[200:203],v[96:99]\n"
"v_add_f32 v56,v56,v128\n"
"v_add_f32 v56,v56,v129\n"
MF "v[96:99],v[24:27],v[180:183],v[96:99]\n"
CV "v90,v128,v129\n"
"v_add_f32 v56,v56,v130\n"
MF "v[92:95],v[24:27],v[176:179],v[92:95]\n"
"v_add_f32 v56,v56,v131\n"
CV "v91,v130,v131\n"
MF "v[92:95],v[28:31],v[196:199],v[92:95]\n"
"s_waitcnt lgkmcnt(0)\n"
"ds_read_b128 v[24:27],v0 offset:30720\n"
"ds_read_b128 v[28:31],v1 offset:30720\n"
MF "v[92:95],v[16:19],v[216:219],v[92:95]\n"
MF "v[92:95],v[20:23],v[236:239],v[92:95]\n"
".Lpg12f:\n"
"s_cbranch_execz .Lpg18f\n"
MF "v[96:99],v[20:23],v[240:243],v[96:99]\n"
MF "v[96:99],v[16:19],v[220:223],v[96:99]\n"
MF "v[100:103],v[16:19],v[224:227],v[100:103]\n"
MF "v[100:103],v[20:23],v[244:247],v[100:103]\n"
MF "v[104:107],v[20:23],v[248:251],v[104:107]\n"
MF "v[104:107],v[16:19],v[228:231],v[104:107]\n"
"v_max3_f32 v10,v52,v53,v54\n"
"v_max3_f32 v10,v10,v55,v56\n"
MF "v[108:111],v[16:19],v[232:235],v[108:111]\n"
"v_cmp_le_f32 vcc,0x47800000,v10\n"
MF "v[108:111],v[20:23],v[252:255],v[108:111]\n"
"s_waitcnt lgkmcnt(0)\n"
"s_cbranch_vccnz .Lstub_9\n"
".Lback_9:\n"
MF "v[112:115],v[24:27],v[136:139],v[32:35]\n"
"s_add_u32 s74,s74,12288\n"
"ds_read_b128 v[16:19],v0 offset:31744\n"
"ds_read_b128 v[20:23],v1 offset:31744\n"
MF "v[112:115],v[28:31],v[156:159],v[112:115]\n"
"s_cmp_eq_u32 s74,s20\n"
"s_cselect_b32 s74,s70,s74\n"
MF "v[116:119],v[28:31],v[160:163],v[36:39]\n"
"v_exp_f32 v92,v92\n"
MF "v[116:119],v[24:27],v[140:143],v[116:119]\n"
"v_exp_f32 v93,v93\n"
MF "v[120:123],v[24:27],v[144:147],v[40:43]\n"
"v_exp_f32 v94,v94\n"
MF "v[120:123],v[28:31],v[164:167],v[120:123]\n"
"v_exp_f32 v95,v95\n"
MF "v[124:127],v[28:31],v[168:171],v[44:47]\n"
"v_add_f32 v57,v52,v92\n"
"v_add_f32 v57,v57,v93\n"
MF "v[124:127],v[24:27],v[148:151],v[124:127]\n"
CV "v92,v92,v93\n"
"v_add_f32 v57,v57,v94\n"
MF "v[128:131],v[24:27],v[152:155],v[48:51]\n"
"v_add_f32 v57,v57,v95\n"
CV "v93,v94,v95\n"
MF "v[128:131],v[28:31],v[172:175],v[128:131]\n"
"s_waitcnt lgkmcnt(0)\n"
"ds_read_b128 v[24:27],v0 offset:32768\n"
"ds_read_b128 v[28:31],v1 offset:32768\n"
MF "v[128:131],v[16:19],v[192:195],v[128:131]\n"
"v_exp_f32 v96,v96\n"
MF "v[128:131],v[20:23],v[212:215],v[128:131]\n"
"v_exp_f32 v97,v97\n"
MF "v[124:127],v[20:23],v[208:211],v[124:127]\n"
"v_exp_f32 v98,v98\n"
MF "v[124:127],v[16:19],v[188:191],v[124:127]\n"
"v_exp_f32 v99,v99\n"
MF "v[120:123],v[16:19],v[184:187],v[120:123]\n"
"v_add_f32 v58,v53,v96\n"
"v_add_f32 v58,v58,v97\n"
MF "v[120:123],v[20:23],v[204:207],v[120:123]\n"
CV "v96,v96,v97\n"
"v_add_f32 v58,v58,v98\n"
MF "v[116:119],v[20:23],v[200:203],v[116:119]\n"
"s_waitcnt vmcnt(10) lgkmcnt(0)\n"
"v_add_f32 v58,v58,v99\n"
CV "v97,v98,v99\n"
MF "v[116:119],v[16:19],v[180:183],v[116:119]\n"
"s_barrier\n"
MF "v[112:115],v[16:19],v[176:179],v[112:115]\n"
"v_exp_f32 v100,v100\n"
MF "v[112:115],v[20:23],v[196:199],v[112:115]\n"
"s_nop 0\n"
TR "v[16:17],v2\n"
TR "v[18:19],v2 offset:6144\n"
MF "v[112:115],v[24:27],v[216:219],v[112:115]\n"
"s_add_u32 m0,s62,16384\n"
TR "v[20:21],v3\n"
TR "v[22:23],v3 offset:6144\n"
MF "v[112:115],v[28:31],v[236:239],v[112:115]\n"
"s_add_u32 s82,s66,2359424\n"
"s_sub_u32 s15,s82,7864320\n"
"s_min_u32 s82,s82,s15\n"
MF "v[116:119],v[28:31],v[240:243],v[116:119]\n"
LD "v6,s[28:31],s82 offen lds\n"
MF "v[116:119],v[24:27],v[220:223],v[116:119]\n"
"s_add_u32 s82,s82,97280\n"
"v_exp_f32 v101,v101\n"
MF "v[120:123],v[24:27],v[224:227],v[120:123]\n"
LD "v6,s[28:31],s82 offen offset:1024 lds\n"
MF "v[120:123],v[28:31],v[244:247],v[120:123]\n"
"v_exp_f32 v102,v102\n"
MF "v[124:127],v[28:31],v[248:251],v[124:127]\n"
"s_add_u32 s82,s82,97280\n"
"v_exp_f32 v103,v103\n"
MF "v[124:127],v[24:27],v[228:231],v[124:127]\n"
LD "v6,s[28:31],s82 offen offset:2048 lds\n"
MF "v[128:131],v[24:27],v[232:235],v[128:131]\n"
"v_add_f32 v59,v54,v100\n"
"v_add_f32 v59,v59,v101\n"
MF "v[128:131],v[28:31],v[252:255],v[128:131]\n"
"s_waitcnt lgkmcnt(0)\n"
TR "v[24:25],v4\n"
TR "v[26:27],v4 offset:6144\n"
MF "a[0:3],v[16:19],v[72:75],a[0:3]\n"
"s_add_u32 s82,s82,97280\n"
TR "v[28:29],v5\n"
TR "v[30:31],v5 offset:6144\n"
MF "a[20:23],v[20:23],v[72:75],a[20:23]\n"
LD "v6,s[28:31],s82 offen offset:3072 lds\n"
MF "a[24:27],v[20:23],v[76:79],a[24:27]\n"
"s_add_u32 m0,s62,32768\n"
CV "v100,v100,v101\n"
"v_add_f32 v59,v59,v102\n"
MF "a[4:7],v[16:19],v[76:79],a[4:7]\n"
"s_add_u32 s82,s66,2359552\n"
"s_sub_u32 s15,s82,7864320\n"
"s_min_u32 s82,s82,s15\n"
MF "a[8:11],v[16:19],v[80:83],a[8:11]\n"
LD "v6,s[28:31],s82 offen lds\n"
MF "a[28:31],v[20:23],v[80:83],a[28:31]\n"
"v_add_f32 v59,v59,v103\n"
CV "v101,v102,v103\n"
MF "a[32:35],v[20:23],v[84:87],a[32:35]\n"
"s_add_u32 s82,s82,97280\n"
"v_exp_f32 v104,v104\n"
MF "a[12:15],v[16:19],v[84:87],a[12:15]\n"
LD "v6,s[28:31],s82 offen offset:1024 lds\n"
MF "a[16:19],v[16:19],v[88:91],a[16:19]\n"
"v_exp_f32 v105,v105\n"
MF "a[36:39],v[20:23],v[88:91],a[36:39]\n"
"s_waitcnt lgkmcnt(0)\n"
TR "v[16:17],v2 offset:1024\n"
TR "v[18:19],v2 offset:7168\n"
MF "a[56:59],v[24:27],v[88:91],a[56:59]\n"
"s_nop 0\n"
TR "v[20:21],v3 offset:1024\n"
TR "v[22:23],v3 offset:7168\n"
MF "a[76:79],v[28:31],v[88:91],a[76:79]\n"
"v_exp_f32 v106,v106\n"
MF "a[72:75],v[28:31],v[84:87],a[72:75]\n"
"v_exp_f32 v107,v107\n"
MF "a[52:55],v[24:27],v[84:87],a[52:55]\n"
"v_add_f32 v60,v55,v104\n"
"v_add_f32 v60,v60,v105\n"
MF "a[48:51],v[24:27],v[80:83],a[48:51]\n"
CV "v104,v104,v105\n"
"v_add_f32 v60,v60,v106\n"
MF "a[68:71],v[28:31],v[80:83],a[68:71]\n"
"v_add_f32 v60,v60,v107\n"
CV "v105,v106,v107\n"
MF "a[64:67],v[28:31],v[76:79],a[64:67]\n"
"v_exp_f32 v108,v108\n"
MF "a[44:47],v[24:27],v[76:79],a[44:47]\n"
"v_exp_f32 v109,v109\n"
MF "a[40:43],v[24:27],v[72:75],a[40:43]\n"
"v_exp_f32 v110,v110\n"
MF "a[60:63],v[28:31],v[72:75],a[60:63]\n"
"s_waitcnt lgkmcnt(0)\n"
TR "v[24:25],v4 offset:1024\n"
TR "v[26:27],v4 offset:7168\n"
MF "a[80:83],v[16:19],v[72:75],a[80:83]\n"
"s_nop 0\n"
TR "v[28:29],v5 offset:1024\n"
TR "v[30:31],v5 offset:7168\n"
MF "a[100:103],v[20:23],v[72:75],a[100:103]\n"
"v_exp_f32 v111,v111\n"
MF "a[104:107],v[20:23],v[76:79],a[104:107]\n"
"v_add_f32 v61,v56,v108\n"
"v_add_f32 v61,v61,v109\n"
MF "a[84:87],v[16:19],v[76:79],a[84:87]\n"
CV "v108,v108,v109\n"
"v_add_f32 v61,v61,v110\n"
MF "a[88:91],v[16:19],v[80:83],a[88:91]\n"
"v_add_f32 v61,v61,v111\n"
CV "v109,v110,v111\n"
MF "a[108:111],v[20:23],v[80:83],a[108:111]\n"
"v_exp_f32 v112,v112\n"
MF "a[112:115],v[20:23],v[84:87],a[112:115]\n"
"v_exp_f32 v113,v113\n"
MF "a[92:95],v[16:19],v[84:87],a[92:95]\n"
"v_exp_f32 v114,v114\n"
MF "a[96:99],v[16:19],v[88:91],a[96:99]\n"
"v_exp_f32 v115,v115\n"
MF "a[116:119],v[20:23],v[88:91],a[116:119]\n"
"s_waitcnt lgkmcnt(0)\n"
TR "v[16:17],v2 offset:2048\n"
TR "v[18:19],v2 offset:8192\n"
MF "a[136:139],v[24:27],v[88:91],a[136:139]\n"
"s_nop 0\n"
TR "v[20:21],v3 offset:2048\n"
TR "v[22:23],v3 offset:8192\n"
MF "a[156:159],v[28:31],v[88:91],a[156:159]\n"
"v_add_f32 v57,v57,v112\n"
"v_add_f32 v57,v57,v113\n"
MF "a[152:155],v[28:31],v[84:87],a[152:155]\n"
CV "v94,v112,v113\n"
"v_add_f32 v57,v57,v114\n"
MF "a[132:135],v[24:27],v[84:87],a[132:135]\n"
"v_add_f32 v57,v57,v115\n"
CV "v95,v114,v115\n"
MF "a[128:131],v[24:27],v[80:83],a[128:131]\n"
"v_exp_f32 v116,v116\n"
MF "a[148:151],v[28:31],v[80:83],a[148:151]\n"
"v_exp_f32 v117,v117\n"
MF "a[144:147],v[28:31],v[76:79],a[144:147]\n"
"v_exp_f32 v118,v118\n"
MF "a[124:127],v[24:27],v[76:79],a[124:127]\n"
"v_exp_f32 v119,v119\n"
MF "a[120:123],v[24:27],v[72:75],a[120:123]\n"
"v_add_f32 v58,v58,v116\n"
"v_add_f32 v58,v58,v117\n"
MF "a[140:143],v[28:31],v[72:75],a[140:143]\n"
"s_waitcnt lgkmcnt(0)\n"
TR "v[24:25],v4 offset:2048\n"
TR "v[26:27],v4 offset:8192\n"
MF "a[160:163],v[16:19],v[72:75],a[160:163]\n"
"s_nop 0\n"
TR "v[28:29],v5 offset:2048\n"
TR "v[30:31],v5 offset:8192\n"
MF "a[180:183],v[20:23],v[72:75],a[180:183]\n"
CV "v98,v116,v117\n"
"v_add_f32 v58,v58,v118\n"
MF "a[184:187],v[20:23],v[76:79],a[184:187]\n"
"v_add_f32 v58,v58,v119\n"
CV "v99,v118,v119\n"
MF "a[164:167],v[16:19],v[76:79],a[164:167]\n"
"v_exp_f32 v120,v120\n"
MF "a[168:171],v[16:19],v[80:83],a[168:171]\n"
"v_exp_f32 v121,v121\n"
MF "a[188:191],v[20:23],v[80:83],a[188:191]\n"
"v_exp_f32 v122,v122\n"
MF "a[192:195],v[20:23],v[84:87],a[192:195]\n"
"v_exp_f32 v123,v123\n"
MF "a[172:175],v[16:19],v[84:87],a[172:175]\n"
"v_add_f32 v59,v59,v120\n"
"v_add_f32 v59,v59,v121\n"
MF "a[176:179],v[16:19],v[88:91],a[176:179]\n"
"s_waitcnt lgkmcnt(0)\n"
CV "v102,v120,v121\n"
"v_add_f32 v59,v59,v122\n"
MF "a[196:199],v[20:23],v[88:91],a[196:199]\n"
"s_add_u32 s82,s82,97280\n"
"v_add_f32 v59,v59,v123\n"
CV "v103,v122,v123\n"
MF "a[216:219],v[24:27],v[88:91],a[216:219]\n"
LD "v6,s[28:31],s82 offen offset:2048 lds\n"
MF "a[236:239],v[28:31],v[88:91],a[236:239]\n"
"v_exp_f32 v124,v124\n"
MF "a[232:235],v[28:31],v[84:87],a[232:235]\n"
"s_add_u32 s82,s82,97280\n"
"v_exp_f32 v125,v125\n"
MF "a[212:215],v[24:27],v[84:87],a[212:215]\n"
LD "v6,s[28:31],s82 offen offset:3072 lds\n"
MF "a[208:211],v[24:27],v[80:83],a[208:211]\n"
"v_exp_f32 v126,v126\n"
MF "a[228:231],v[28:31],v[80:83],a[228:231]\n"
"v_exp_f32 v127,v127\n"
MF "a[224:227],v[28:31],v[76:79],a[224:227]\n"
"v_add_f32 v60,v60,v124\n"
"v_add_f32 v60,v60,v125\n"
MF "a[204:207],v[24:27],v[76:79],a[204:207]\n"
CV "v106,v124,v125\n"
"v_add_f32 v60,v60,v126\n"
MF "a[200:203],v[24:27],v[72:75],a[200:203]\n"
"v_add_f32 v60,v60,v127\n"
CV "v107,v126,v127\n"
MF "a[220:223],v[28:31],v[72:75],a[220:223]\n"
"v_exp_f32 v128,v128\n"
"v_exp_f32 v129,v129\n"
"v_exp_f32 v130,v130\n"
"v_exp_f32 v131,v131\n"
"v_add_f32 v61,v61,v128\n"
"v_add_f32 v61,v61,v129\n"
CV "v110,v128,v129\n"
"v_add_f32 v61,v61,v130\n"
"v_add_f32_e64 v61,v61,v131\n"
CV "v111,v130,v131\n"
"v_max3_f32 v10,v57,v58,v59\n"
"v_max3_f32 v10,v10,v60,v61\n"
"v_cmp_le_f32 vcc,0x47800000,v10\n"
"s_cbranch_vccnz .Lstub_10\n"
".Lback_10:\n"
"s_waitcnt vmcnt(8) lgkmcnt(0)\n"
"s_barrier\n"
"v_add_u32 v11,97280,v6\n"
"v_add_u32 v12,194560,v6\n"
"v_add_u32 v13,291840,v6\n"
"v_mov_b32 v10,1.0\n"
TR "v[136:137],v2 offset:24576\n"
TR "v[138:139],v2 offset:30720\n"
"v_mfma_f32_16x16x4_f32 v[16:19],v10,v57,0\n"
TR "v[140:141],v3 offset:24576\n"
TR "v[142:143],v3 offset:30720\n"
TR "v[144:145],v4 offset:24576\n"
TR "v[146:147],v4 offset:30720\n"
TR "v[148:149],v5 offset:24576\n"
TR "v[150:151],v5 offset:30720\n"
"v_mfma_f32_16x16x4_f32 v[20:23],v10,v58,0\n"
TR "v[152:153],v2 offset:25600\n"
TR "v[154:155],v2 offset:31744\n"
TR "v[156:157],v3 offset:25600\n"
TR "v[158:159],v3 offset:31744\n"
TR "v[160:161],v4 offset:25600\n"
TR "v[162:163],v4 offset:31744\n"
"v_mfma_f32_16x16x4_f32 v[24:27],v10,v59,0\n"
TR "v[164:165],v5 offset:25600\n"
TR "v[166:167],v5 offset:31744\n"
TR "v[168:169],v2 offset:26624\n"
TR "v[170:171],v2 offset:32768\n"
TR "v[172:173],v3 offset:26624\n"
TR "v[174:175],v3 offset:32768\n"
"v_mfma_f32_16x16x4_f32 v[28:31],v10,v60,0\n"
TR "v[176:177],v4 offset:26624\n"
TR "v[178:179],v4 offset:32768\n"
TR "v[180:181],v5 offset:26624\n"
TR "v[182:183],v5 offset:32768\n"
"v_mfma_f32_16x16x4_f32 v[32:35],v10,v61,0\n"
"s_waitcnt lgkmcnt(15)\n"
MF "a[0:3],v[136:139],v[92:95],a[0:3]\n"
MF "a[20:23],v[140:143],v[92:95],a[20:23]\n"
"v_rcp_f32 v57,v16\n"
"v_rcp_f32 v58,v20\n"
"v_rcp_f32 v59,v24\n"
"v_rcp_f32 v60,v28\n"
"s_nop 2\n"
"v_rcp_f32_e64 v61,v32\n"
MF "a[4:7],v[136:139],v[96:99],a[4:7]\n"
"v_mul_f32_e64 v52,-s13,v57\n"
"v_mul_f32_e64 v53,-s13,v58\n"
"v_mul_f32_e64 v54,-s13,v59\n"
"v_mul_f32_e64 v55,-s13,v60\n"
"v_mul_f32_e64 v56,-s13,v61\n"
AR "v72,a0\n"
AR "v73,a1\n"
AR "v74,a2\n"
MF "a[24:27],v[140:143],v[96:99],a[24:27]\n"
AR "v75,a3\n"
AR "v76,a20\n"
AR "v77,a21\n"
AR "v78,a22\n"
AR "v79,a23\n"
"v_pk_fma_f32 v[72:73],v[72:73],v[56:57],v[52:53] op_sel:[0,1,0] op_sel_hi:[1,1,0]\n"
"v_pk_fma_f32 v[74:75],v[74:75],v[56:57],v[52:53] op_sel:[0,1,0] op_sel_hi:[1,1,0]\n"
"v_pk_fma_f32 v[76:77],v[76:77],v[56:57],v[52:53] op_sel:[0,1,0] op_sel_hi:[1,1,0]\n"
"v_pk_fma_f32 v[78:79],v[78:79],v[56:57],v[52:53] op_sel:[0,1,0] op_sel_hi:[1,1,0]\n"
CV "v184,v72,v73\n"
CV "v185,v74,v75\n"
CV "v186,v76,v77\n"
CV "v187,v78,v79\n"
MF "a[8:11],v[136:139],v[100:103],a[8:11]\n"
AR "v80,a4\n"
AR "v81,a5\n"
AR "v82,a6\n"
MF "a[28:31],v[140:143],v[100:103],a[28:31]\n"
AR "v83,a7\n"
AR "v84,a24\n"
AR "v85,a25\n"
AR "v86,a26\n"
AR "v87,a27\n"
"v_pk_fma_f32 v[80:81],v[80:81],v[58:59],v[52:53] op_sel:[0,0,1] op_sel_hi:[1,0,1]\n"
"v_pk_fma_f32 v[82:83],v[82:83],v[58:59],v[52:53] op_sel:[0,0,1] op_sel_hi:[1,0,1]\n"
"v_pk_fma_f32 v[84:85],v[84:85],v[58:59],v[52:53] op_sel:[0,0,1] op_sel_hi:[1,0,1]\n"
"v_pk_fma_f32 v[86:87],v[86:87],v[58:59],v[52:53] op_sel:[0,0,1] op_sel_hi:[1,0,1]\n"
CV "v188,v80,v81\n"
CV "v189,v82,v83\n"
CV "v190,v84,v85\n"
CV "v191,v86,v87\n"
MF "a[12:15],v[136:139],v[104:107],a[12:15]\n"
AR "v72,a8\n"
AR "v73,a9\n"
AR "v74,a10\n"
MF "a[32:35],v[140:143],v[104:107],a[32:35]\n"
AR "v75,a11\n"
AR "v76,a28\n"
AR "v77,a29\n"
AR "v78,a30\n"
AR "v79,a31\n"
"v_pk_fma_f32 v[72:73],v[72:73],v[58:59],v[54:55] op_sel:[0,1,0] op_sel_hi:[1,1,0]\n"
"v_pk_fma_f32 v[74:75],v[74:75],v[58:59],v[54:55] op_sel:[0,1,0] op_sel_hi:[1,1,0]\n"
"v_pk_fma_f32 v[76:77],v[76:77],v[58:59],v[54:55] op_sel:[0,1,0] op_sel_hi:[1,1,0]\n"
"v_pk_fma_f32 v[78:79],v[78:79],v[58:59],v[54:55] op_sel:[0,1,0] op_sel_hi:[1,1,0]\n"
CV "v192,v72,v73\n"
CV "v193,v74,v75\n"
CV "v194,v76,v77\n"
CV "v195,v78,v79\n"
MF "a[16:19],v[136:139],v[108:111],a[16:19]\n"
AR "v80,a12\n"
AR "v81,a13\n"
AR "v82,a14\n"
MF "a[36:39],v[140:143],v[108:111],a[36:39]\n"
AR "v83,a15\n"
AR "v84,a32\n"
AR "v85,a33\n"
AR "v86,a34\n"
AR "v87,a35\n"
"v_pk_fma_f32 v[80:81],v[80:81],v[60:61],v[54:55] op_sel:[0,0,1] op_sel_hi:[1,0,1]\n"
"v_pk_fma_f32 v[82:83],v[82:83],v[60:61],v[54:55] op_sel:[0,0,1] op_sel_hi:[1,0,1]\n"
"v_pk_fma_f32 v[84:85],v[84:85],v[60:61],v[54:55] op_sel:[0,0,1] op_sel_hi:[1,0,1]\n"
"v_pk_fma_f32 v[86:87],v[86:87],v[60:61],v[54:55] op_sel:[0,0,1] op_sel_hi:[1,0,1]\n"
CV "v196,v80,v81\n"
CV "v197,v82,v83\n"
CV "v198,v84,v85\n"
CV "v199,v86,v87\n"
MF "a[40:43],v[144:147],v[92:95],a[40:43]\n"
AR "v72,a16\n"
AR "v73,a17\n"
AR "v74,a18\n"
MF "a[60:63],v[148:151],v[92:95],a[60:63]\n"
AR "v75,a19\n"
AR "v76,a36\n"
AR "v77,a37\n"
AR "v78,a38\n"
AR "v79,a39\n"
"v_pk_fma_f32 v[72:73],v[72:73],v[60:61],v[56:57] op_sel:[0,1,0] op_sel_hi:[1,1,0]\n"
"v_pk_fma_f32 v[74:75],v[74:75],v[60:61],v[56:57] op_sel:[0,1,0] op_sel_hi:[1,1,0]\n"
"v_pk_fma_f32 v[76:77],v[76:77],v[60:61],v[56:57] op_sel:[0,1,0] op_sel_hi:[1,1,0]\n"
"v_pk_fma_f32 v[78:79],v[78:79],v[60:61],v[56:57] op_sel:[0,1,0] op_sel_hi:[1,1,0]\n"
CV "v200,v72,v73\n"
CV "v201,v74,v75\n"
CV "v202,v76,v77\n"
CV "v203,v78,v79\n"
MF "a[44:47],v[144:147],v[96:99],a[44:47]\n"
AR "v80,a40\n"
AR "v81,a41\n"
AR "v82,a42\n"
MF "a[64:67],v[148:151],v[96:99],a[64:67]\n"
AR "v83,a43\n"
AR "v84,a60\n"
AR "v85,a61\n"
AR "v86,a62\n"
AR "v87,a63\n"
"v_pk_fma_f32 v[80:81],v[80:81],v[56:57],v[52:53] op_sel:[0,1,0] op_sel_hi:[1,1,0]\n"
"v_pk_fma_f32 v[82:83],v[82:83],v[56:57],v[52:53] op_sel:[0,1,0] op_sel_hi:[1,1,0]\n"
"v_pk_fma_f32 v[84:85],v[84:85],v[56:57],v[52:53] op_sel:[0,1,0] op_sel_hi:[1,1,0]\n"
"v_pk_fma_f32 v[86:87],v[86:87],v[56:57],v[52:53] op_sel:[0,1,0] op_sel_hi:[1,1,0]\n"
CV "v204,v80,v81\n"
CV "v205,v82,v83\n"
CV "v206,v84,v85\n"
CV "v207,v86,v87\n"
MF "a[48:51],v[144:147],v[100:103],a[48:51]\n"
AR "v72,a44\n"
AR "v73,a45\n"
AR "v74,a46\n"
MF "a[68:71],v[148:151],v[100:103],a[68:71]\n"
AR "v75,a47\n"
AR "v76,a64\n"
AR "v77,a65\n"
AR "v78,a66\n"
AR "v79,a67\n"
"v_pk_fma_f32 v[72:73],v[72:73],v[58:59],v[52:53] op_sel:[0,0,1] op_sel_hi:[1,0,1]\n"
"v_pk_fma_f32 v[74:75],v[74:75],v[58:59],v[52:53] op_sel:[0,0,1] op_sel_hi:[1,0,1]\n"
"v_pk_fma_f32 v[76:77],v[76:77],v[58:59],v[52:53] op_sel:[0,0,1] op_sel_hi:[1,0,1]\n"
"v_pk_fma_f32 v[78:79],v[78:79],v[58:59],v[52:53] op_sel:[0,0,1] op_sel_hi:[1,0,1]\n"
CV "v208,v72,v73\n"
CV "v209,v74,v75\n"
CV "v210,v76,v77\n"
CV "v211,v78,v79\n"
MF "a[52:55],v[144:147],v[104:107],a[52:55]\n"
AR "v80,a48\n"
AR "v81,a49\n"
AR "v82,a50\n"
MF "a[72:75],v[148:151],v[104:107],a[72:75]\n"
AR "v83,a51\n"
AR "v84,a68\n"
AR "v85,a69\n"
AR "v86,a70\n"
AR "v87,a71\n"
"v_pk_fma_f32 v[80:81],v[80:81],v[58:59],v[54:55] op_sel:[0,1,0] op_sel_hi:[1,1,0]\n"
"v_pk_fma_f32 v[82:83],v[82:83],v[58:59],v[54:55] op_sel:[0,1,0] op_sel_hi:[1,1,0]\n"
"v_pk_fma_f32 v[84:85],v[84:85],v[58:59],v[54:55] op_sel:[0,1,0] op_sel_hi:[1,1,0]\n"
"v_pk_fma_f32 v[86:87],v[86:87],v[58:59],v[54:55] op_sel:[0,1,0] op_sel_hi:[1,1,0]\n"
CV "v212,v80,v81\n"
CV "v213,v82,v83\n"
CV "v214,v84,v85\n"
CV "v215,v86,v87\n"
MF "a[56:59],v[144:147],v[108:111],a[56:59]\n"
AR "v72,a52\n"
AR "v73,a53\n"
AR "v74,a54\n"
MF "a[76:79],v[148:151],v[108:111],a[76:79]\n"
AR "v75,a55\n"
AR "v76,a72\n"
AR "v77,a73\n"
AR "v78,a74\n"
AR "v79,a75\n"
"v_pk_fma_f32 v[72:73],v[72:73],v[60:61],v[54:55] op_sel:[0,0,1] op_sel_hi:[1,0,1]\n"
"v_pk_fma_f32 v[74:75],v[74:75],v[60:61],v[54:55] op_sel:[0,0,1] op_sel_hi:[1,0,1]\n"
"v_pk_fma_f32 v[76:77],v[76:77],v[60:61],v[54:55] op_sel:[0,0,1] op_sel_hi:[1,0,1]\n"
"v_pk_fma_f32 v[78:79],v[78:79],v[60:61],v[54:55] op_sel:[0,0,1] op_sel_hi:[1,0,1]\n"
CV "v216,v72,v73\n"
CV "v217,v74,v75\n"
CV "v218,v76,v77\n"
CV "v219,v78,v79\n"
"s_waitcnt lgkmcnt(14)\n"
MF "a[80:83],v[152:155],v[92:95],a[80:83]\n"
AR "v80,a56\n"
AR "v81,a57\n"
AR "v82,a58\n"
"s_waitcnt lgkmcnt(12)\n"
MF "a[100:103],v[156:159],v[92:95],a[100:103]\n"
AR "v83,a59\n"
AR "v84,a76\n"
AR "v85,a77\n"
AR "v86,a78\n"
AR "v87,a79\n"
"v_pk_fma_f32 v[80:81],v[80:81],v[60:61],v[56:57] op_sel:[0,1,0] op_sel_hi:[1,1,0]\n"
"v_pk_fma_f32 v[82:83],v[82:83],v[60:61],v[56:57] op_sel:[0,1,0] op_sel_hi:[1,1,0]\n"
"v_pk_fma_f32 v[84:85],v[84:85],v[60:61],v[56:57] op_sel:[0,1,0] op_sel_hi:[1,1,0]\n"
"v_pk_fma_f32 v[86:87],v[86:87],v[60:61],v[56:57] op_sel:[0,1,0] op_sel_hi:[1,1,0]\n"
CV "v116,v80,v81\n"
CV "v117,v82,v83\n"
CV "v118,v84,v85\n"
CV "v119,v86,v87\n"
MF "a[84:87],v[152:155],v[96:99],a[84:87]\n"
AW "a56,v116\n"
AW "a57,v117\n"
AW "a58,v118\n"
AW "a59,v119\n"
AR "v72,a80\n"
AR "v73,a81\n"
AR "v74,a82\n"
MF "a[104:107],v[156:159],v[96:99],a[104:107]\n"
AR "v75,a83\n"
AR "v76,a100\n"
AR "v77,a101\n"
AR "v78,a102\n"
AR "v79,a103\n"
"v_pk_fma_f32 v[72:73],v[72:73],v[56:57],v[52:53] op_sel:[0,1,0] op_sel_hi:[1,1,0]\n"
"v_pk_fma_f32 v[74:75],v[74:75],v[56:57],v[52:53] op_sel:[0,1,0] op_sel_hi:[1,1,0]\n"
"v_pk_fma_f32 v[76:77],v[76:77],v[56:57],v[52:53] op_sel:[0,1,0] op_sel_hi:[1,1,0]\n"
"v_pk_fma_f32 v[78:79],v[78:79],v[56:57],v[52:53] op_sel:[0,1,0] op_sel_hi:[1,1,0]\n"
CV "v224,v72,v73\n"
CV "v225,v74,v75\n"
CV "v226,v76,v77\n"
CV "v227,v78,v79\n"
MF "a[88:91],v[152:155],v[100:103],a[88:91]\n"
AR "v80,a84\n"
AR "v81,a85\n"
AR "v82,a86\n"
MF "a[108:111],v[156:159],v[100:103],a[108:111]\n"
AR "v83,a87\n"
AR "v84,a104\n"
AR "v85,a105\n"
AR "v86,a106\n"
AR "v87,a107\n"
"v_pk_fma_f32 v[80:81],v[80:81],v[58:59],v[52:53] op_sel:[0,0,1] op_sel_hi:[1,0,1]\n"
"v_pk_fma_f32 v[82:83],v[82:83],v[58:59],v[52:53] op_sel:[0,0,1] op_sel_hi:[1,0,1]\n"
"v_pk_fma_f32 v[84:85],v[84:85],v[58:59],v[52:53] op_sel:[0,0,1] op_sel_hi:[1,0,1]\n"
"v_pk_fma_f32 v[86:87],v[86:87],v[58:59],v[52:53] op_sel:[0,0,1] op_sel_hi:[1,0,1]\n"
CV "v228,v80,v81\n"
CV "v229,v82,v83\n"
CV "v230,v84,v85\n"
CV "v231,v86,v87\n"
MF "a[92:95],v[152:155],v[104:107],a[92:95]\n"
AR "v72,a88\n"
AR "v73,a89\n"
AR "v74,a90\n"
MF "a[112:115],v[156:159],v[104:107],a[112:115]\n"
AR "v75,a91\n"
AR "v76,a108\n"
AR "v77,a109\n"
AR "v78,a110\n"
AR "v79,a111\n"
"v_pk_fma_f32 v[72:73],v[72:73],v[58:59],v[54:55] op_sel:[0,1,0] op_sel_hi:[1,1,0]\n"
"v_pk_fma_f32 v[74:75],v[74:75],v[58:59],v[54:55] op_sel:[0,1,0] op_sel_hi:[1,1,0]\n"
"v_pk_fma_f32 v[76:77],v[76:77],v[58:59],v[54:55] op_sel:[0,1,0] op_sel_hi:[1,1,0]\n"
"v_pk_fma_f32 v[78:79],v[78:79],v[58:59],v[54:55] op_sel:[0,1,0] op_sel_hi:[1,1,0]\n"
CV "v136,v72,v73\n"
CV "v137,v74,v75\n"
CV "v138,v76,v77\n"
CV "v139,v78,v79\n"
MF "a[96:99],v[152:155],v[108:111],a[96:99]\n"
AR "v80,a92\n"
AR "v81,a93\n"
AR "v82,a94\n"
MF "a[116:119],v[156:159],v[108:111],a[116:119]\n"
".Lpg13f:\n"
"s_cbranch_execz .Lisr3\n"
AR "v83,a95\n"
AR "v84,a112\n"
AR "v85,a113\n"
"s_nop 0\n"
AR "v86,a114\n"
AR "v87,a115\n"
"v_pk_fma_f32 v[80:81],v[80:81],v[60:61],v[54:55] op_sel:[0,0,1] op_sel_hi:[1,0,1]\n"
"v_pk_fma_f32 v[82:83],v[82:83],v[60:61],v[54:55] op_sel:[0,0,1] op_sel_hi:[1,0,1]\n"
"v_pk_fma_f32 v[84:85],v[84:85],v[60:61],v[54:55] op_sel:[0,0,1] op_sel_hi:[1,0,1]\n"
"v_pk_fma_f32 v[86:87],v[86:87],v[60:61],v[54:55] op_sel:[0,0,1] op_sel_hi:[1,0,1]\n"
CV "v140,v80,v81\n"
CV "v141,v82,v83\n"
CV "v142,v84,v85\n"
CV "v143,v86,v87\n"
"s_waitcnt lgkmcnt(10)\n"
MF "a[120:123],v[160:163],v[92:95],a[120:123]\n"
AR "v72,a96\n"
AR "v73,a97\n"
AR "v74,a98\n"
"s_waitcnt lgkmcnt(8)\n"
MF "a[140:143],v[164:167],v[92:95],a[140:143]\n"
AR "v75,a99\n"
AR "v76,a116\n"
AR "v77,a117\n"
AR "v78,a118\n"
AR "v79,a119\n"
"v_pk_fma_f32 v[72:73],v[72:73],v[60:61],v[56:57] op_sel:[0,1,0] op_sel_hi:[1,1,0]\n"
"v_pk_fma_f32 v[74:75],v[74:75],v[60:61],v[56:57] op_sel:[0,1,0] op_sel_hi:[1,1,0]\n"
"v_pk_fma_f32 v[76:77],v[76:77],v[60:61],v[56:57] op_sel:[0,1,0] op_sel_hi:[1,1,0]\n"
"v_pk_fma_f32 v[78:79],v[78:79],v[60:61],v[56:57] op_sel:[0,1,0] op_sel_hi:[1,1,0]\n"
CV "v144,v72,v73\n"
CV "v145,v74,v75\n"
CV "v146,v76,v77\n"
CV "v147,v78,v79\n"
MF "a[124:127],v[160:163],v[96:99],a[124:127]\n"
AR "v0,a240\n"
AR "v2,a241\n"
AR "v4,a242\n"
"v_xor_b32 v1,64,v0\n"
"v_xor_b32 v3,64,v2\n"
"v_xor_b32_e64 v5,64,v4\n"
AR "v80,a120\n"
AR "v81,a121\n"
AR "v82,a122\n"
MF "a[144:147],v[164:167],v[96:99],a[144:147]\n"
AR "v83,a123\n"
AR "v84,a140\n"
AR "v85,a141\n"
AR "v86,a142\n"
AR "v87,a143\n"
"v_pk_fma_f32 v[80:81],v[80:81],v[56:57],v[52:53] op_sel:[0,1,0] op_sel_hi:[1,1,0]\n"
"v_pk_fma_f32 v[82:83],v[82:83],v[56:57],v[52:53] op_sel:[0,1,0] op_sel_hi:[1,1,0]\n"
"v_pk_fma_f32 v[84:85],v[84:85],v[56:57],v[52:53] op_sel:[0,1,0] op_sel_hi:[1,1,0]\n"
"v_pk_fma_f32 v[86:87],v[86:87],v[56:57],v[52:53] op_sel:[0,1,0] op_sel_hi:[1,1,0]\n"
CV "v148,v80,v81\n"
CV "v149,v82,v83\n"
CV "v150,v84,v85\n"
CV "v151,v86,v87\n"
MF "a[128:131],v[160:163],v[100:103],a[128:131]\n"
AR "v72,a124\n"
AR "v73,a125\n"
AR "v74,a126\n"
MF "a[148:151],v[164:167],v[100:103],a[148:151]\n"
AR "v75,a127\n"
AR "v76,a144\n"
AR "v77,a145\n"
AR "v78,a146\n"
AR "v79,a147\n"
"v_pk_fma_f32 v[72:73],v[72:73],v[58:59],v[52:53] op_sel:[0,0,1] op_sel_hi:[1,0,1]\n"
"v_pk_fma_f32 v[74:75],v[74:75],v[58:59],v[52:53] op_sel:[0,0,1] op_sel_hi:[1,0,1]\n"
"v_pk_fma_f32 v[76:77],v[76:77],v[58:59],v[52:53] op_sel:[0,0,1] op_sel_hi:[1,0,1]\n"
"v_pk_fma_f32 v[78:79],v[78:79],v[58:59],v[52:53] op_sel:[0,0,1] op_sel_hi:[1,0,1]\n"
CV "v112,v72,v73\n"
CV "v113,v74,v75\n"
CV "v114,v76,v77\n"
CV "v115,v78,v79\n"
MF "a[132:135],v[160:163],v[104:107],a[132:135]\n"
"ds_read_b128 a[60:63],v2\n"
AW "a124,v112\n"
AW "a125,v113\n"
AW "a126,v114\n"
AW "a127,v115\n"
AR "v80,a128\n"
AR "v81,a129\n"
AR "v82,a130\n"
MF "a[152:155],v[164:167],v[104:107],a[152:155]\n"
AR "v83,a131\n"
AR "v84,a148\n"
AR "v85,a149\n"
AR "v86,a150\n"
AR "v87,a151\n"
"v_pk_fma_f32 v[80:81],v[80:81],v[58:59],v[54:55] op_sel:[0,1,0] op_sel_hi:[1,1,0]\n"
"v_pk_fma_f32 v[82:83],v[82:83],v[58:59],v[54:55] op_sel:[0,1,0] op_sel_hi:[1,1,0]\n"
"v_pk_fma_f32 v[84:85],v[84:85],v[58:59],v[54:55] op_sel:[0,1,0] op_sel_hi:[1,1,0]\n"
"v_pk_fma_f32 v[86:87],v[86:87],v[58:59],v[54:55] op_sel:[0,1,0] op_sel_hi:[1,1,0]\n"
CV "v116,v80,v81\n"
CV "v117,v82,v83\n"
CV "v118,v84,v85\n"
CV "v119,v86,v87\n"
MF "a[136:139],v[160:163],v[108:111],a[136:139]\n"
"ds_read_b128 a[100:103],v3\n"
AW "a128,v116\n"
AW "a129,v117\n"
AW "a130,v118\n"
AW "a131,v119\n"
AR "v72,a132\n"
AR "v73,a133\n"
AR "v74,a134\n"
MF "a[156:159],v[164:167],v[108:111],a[156:159]\n"
AR "v75,a135\n"
AR "v76,a152\n"
AR "v77,a153\n"
AR "v78,a154\n"
AR "v79,a155\n"
"v_pk_fma_f32 v[72:73],v[72:73],v[60:61],v[54:55] op_sel:[0,0,1] op_sel_hi:[1,0,1]\n"
"v_pk_fma_f32 v[74:75],v[74:75],v[60:61],v[54:55] op_sel:[0,0,1] op_sel_hi:[1,0,1]\n"
"v_pk_fma_f32 v[76:77],v[76:77],v[60:61],v[54:55] op_sel:[0,0,1] op_sel_hi:[1,0,1]\n"
"v_pk_fma_f32 v[78:79],v[78:79],v[60:61],v[54:55] op_sel:[0,0,1] op_sel_hi:[1,0,1]\n"
CV "v112,v72,v73\n"
CV "v113,v74,v75\n"
CV "v114,v76,v77\n"
CV "v115,v78,v79\n"
"s_waitcnt lgkmcnt(8)\n"
MF "a[160:163],v[168:171],v[92:95],a[160:163]\n"
"ds_read_b128 a[64:67],v2 offset:2048\n"
AW "a132,v112\n"
AW "a133,v113\n"
AW "a134,v114\n"
AW "a135,v115\n"
AR "v80,a136\n"
AR "v81,a137\n"
AR "v82,a138\n"
"s_waitcnt lgkmcnt(7)\n"
MF "a[180:183],v[172:175],v[92:95],a[180:183]\n"
AR "v83,a139\n"
AR "v84,a156\n"
AR "v85,a157\n"
AR "v86,a158\n"
AR "v87,a159\n"
"v_pk_fma_f32 v[80:81],v[80:81],v[60:61],v[56:57] op_sel:[0,1,0] op_sel_hi:[1,1,0]\n"
"v_pk_fma_f32 v[82:83],v[82:83],v[60:61],v[56:57] op_sel:[0,1,0] op_sel_hi:[1,1,0]\n"
"v_pk_fma_f32 v[84:85],v[84:85],v[60:61],v[56:57] op_sel:[0,1,0] op_sel_hi:[1,1,0]\n"
"v_pk_fma_f32 v[86:87],v[86:87],v[60:61],v[56:57] op_sel:[0,1,0] op_sel_hi:[1,1,0]\n"
CV "v116,v80,v81\n"
CV "v117,v82,v83\n"
CV "v118,v84,v85\n"
CV "v119,v86,v87\n"
MF "a[164:167],v[168:171],v[96:99],a[164:167]\n"
"ds_read_b128 a[104:107],v3 offset:2048\n"
AW "a136,v116\n"
AW "a137,v117\n"
AW "a138,v118\n"
AW "a139,v119\n"
AR "v72,a160\n"
AR "v73,a161\n"
AR "v74,a162\n"
MF "a[184:187],v[172:175],v[96:99],a[184:187]\n"
AR "v75,a163\n"
AR "v76,a180\n"
AR "v77,a181\n"
AR "v78,a182\n"
AR "v79,a183\n"
"v_pk_fma_f32 v[72:73],v[72:73],v[56:57],v[52:53] op_sel:[0,1,0] op_sel_hi:[1,1,0]\n"
"v_pk_fma_f32 v[74:75],v[74:75],v[56:57],v[52:53] op_sel:[0,1,0] op_sel_hi:[1,1,0]\n"
"v_pk_fma_f32 v[76:77],v[76:77],v[56:57],v[52:53] op_sel:[0,1,0] op_sel_hi:[1,1,0]\n"
"v_pk_fma_f32 v[78:79],v[78:79],v[56:57],v[52:53] op_sel:[0,1,0] op_sel_hi:[1,1,0]\n"
CV "v112,v72,v73\n"
CV "v113,v74,v75\n"
CV "v114,v76,v77\n"
CV "v115,v78,v79\n"
MF "a[168:171],v[168:171],v[100:103],a[168:171]\n"
"ds_read_b128 a[68:71],v2 offset:4096\n"
AW "a160,v112\n"
AW "a161,v113\n"
AW "a162,v114\n"
AW "a163,v115\n"
AR "v80,a164\n"
AR "v81,a165\n"
AR "v82,a166\n"
MF "a[188:191],v[172:175],v[100:103],a[188:191]\n"
AR "v83,a167\n"
AR "v84,a184\n"
AR "v85,a185\n"
AR "v86,a186\n"
AR "v87,a187\n"
"v_pk_fma_f32 v[80:81],v[80:81],v[58:59],v[52:53] op_sel:[0,0,1] op_sel_hi:[1,0,1]\n"
"v_pk_fma_f32 v[82:83],v[82:83],v[58:59],v[52:53] op_sel:[0,0,1] op_sel_hi:[1,0,1]\n"
"v_pk_fma_f32 v[84:85],v[84:85],v[58:59],v[52:53] op_sel:[0,0,1] op_sel_hi:[1,0,1]\n"
"v_pk_fma_f32 v[86:87],v[86:87],v[58:59],v[52:53] op_sel:[0,0,1] op_sel_hi:[1,0,1]\n"
CV "v116,v80,v81\n"
CV "v117,v82,v83\n"
CV "v118,v84,v85\n"
CV "v119,v86,v87\n"
MF "a[172:175],v[168:171],v[104:107],a[172:175]\n"
"ds_read_b128 a[108:111],v3 offset:4096\n"
AW "a164,v116\n"
AW "a165,v117\n"
AW "a166,v118\n"
AW "a167,v119\n"
AR "v72,a168\n"
AR "v73,a169\n"
AR "v74,a170\n"
MF "a[192:195],v[172:175],v[104:107],a[192:195]\n"
AR "v75,a171\n"
AR "v76,a188\n"
AR "v77,a189\n"
AR "v78,a190\n"
AR "v79,a191\n"
"v_pk_fma_f32 v[72:73],v[72:73],v[58:59],v[54:55] op_sel:[0,1,0] op_sel_hi:[1,1,0]\n"
"v_pk_fma_f32 v[74:75],v[74:75],v[58:59],v[54:55] op_sel:[0,1,0] op_sel_hi:[1,1,0]\n"
"v_pk_fma_f32 v[76:77],v[76:77],v[58:59],v[54:55] op_sel:[0,1,0] op_sel_hi:[1,1,0]\n"
"v_pk_fma_f32 v[78:79],v[78:79],v[58:59],v[54:55] op_sel:[0,1,0] op_sel_hi:[1,1,0]\n"
CV "v152,v72,v73\n"
CV "v153,v74,v75\n"
CV "v154,v76,v77\n"
CV "v155,v78,v79\n"
MF "a[176:179],v[168:171],v[108:111],a[176:179]\n"
"ds_read_b128 a[72:75],v2 offset:6144\n"
AR "v80,a172\n"
AR "v81,a173\n"
AR "v82,a174\n"
MF "a[196:199],v[172:175],v[108:111],a[196:199]\n"
AR "v83,a175\n"
AR "v84,a192\n"
AR "v85,a193\n"
AR "v86,a194\n"
AR "v87,a195\n"
"v_pk_fma_f32 v[80:81],v[80:81],v[60:61],v[54:55] op_sel:[0,0,1] op_sel_hi:[1,0,1]\n"
"v_pk_fma_f32 v[82:83],v[82:83],v[60:61],v[54:55] op_sel:[0,0,1] op_sel_hi:[1,0,1]\n"
"v_pk_fma_f32 v[84:85],v[84:85],v[60:61],v[54:55] op_sel:[0,0,1] op_sel_hi:[1,0,1]\n"
"v_pk_fma_f32 v[86:87],v[86:87],v[60:61],v[54:55] op_sel:[0,0,1] op_sel_hi:[1,0,1]\n"
CV "v156,v80,v81\n"
CV "v157,v82,v83\n"
CV "v158,v84,v85\n"
CV "v159,v86,v87\n"
"s_waitcnt lgkmcnt(9)\n"
MF "a[200:203],v[176:179],v[92:95],a[200:203]\n"
"ds_read_b128 a[112:115],v3 offset:6144\n"
AR "v72,a176\n"
AR "v73,a177\n"
AR "v74,a178\n"
"s_waitcnt lgkmcnt(8)\n"
MF "a[220:223],v[180:183],v[92:95],a[220:223]\n"
AR "v75,a179\n"
AR "v76,a196\n"
AR "v77,a197\n"
AR "v78,a198\n"
AR "v79,a199\n"
"v_pk_fma_f32 v[72:73],v[72:73],v[60:61],v[56:57] op_sel:[0,1,0] op_sel_hi:[1,1,0]\n"
"v_pk_fma_f32 v[74:75],v[74:75],v[60:61],v[56:57] op_sel:[0,1,0] op_sel_hi:[1,1,0]\n"
"v_pk_fma_f32 v[76:77],v[76:77],v[60:61],v[56:57] op_sel:[0,1,0] op_sel_hi:[1,1,0]\n"
"v_pk_fma_f32 v[78:79],v[78:79],v[60:61],v[56:57] op_sel:[0,1,0] op_sel_hi:[1,1,0]\n"
CV "v160,v72,v73\n"
CV "v161,v74,v75\n"
CV "v162,v76,v77\n"
CV "v163,v78,v79\n"
MF "a[204:207],v[176:179],v[96:99],a[204:207]\n"
"ds_read_b128 a[76:79],v2 offset:8192\n"
AR "v80,a200\n"
AR "v81,a201\n"
AR "v82,a202\n"
MF "a[224:227],v[180:183],v[96:99],a[224:227]\n"
AR "v83,a203\n"
AR "v84,a220\n"
AR "v85,a221\n"
AR "v86,a222\n"
AR "v87,a223\n"
"v_pk_fma_f32 v[80:81],v[80:81],v[56:57],v[52:53] op_sel:[0,1,0] op_sel_hi:[1,1,0]\n"
"v_pk_fma_f32 v[82:83],v[82:83],v[56:57],v[52:53] op_sel:[0,1,0] op_sel_hi:[1,1,0]\n"
"v_pk_fma_f32 v[84:85],v[84:85],v[56:57],v[52:53] op_sel:[0,1,0] op_sel_hi:[1,1,0]\n"
"v_pk_fma_f32 v[86:87],v[86:87],v[56:57],v[52:53] op_sel:[0,1,0] op_sel_hi:[1,1,0]\n"
CV "v164,v80,v81\n"
CV "v165,v82,v83\n"
CV "v166,v84,v85\n"
CV "v167,v86,v87\n"
MF "a[208:211],v[176:179],v[100:103],a[208:211]\n"
"ds_read_b128 a[116:119],v3 offset:8192\n"
AR "v72,a204\n"
AR "v73,a205\n"
AR "v74,a206\n"
MF "a[228:231],v[180:183],v[100:103],a[228:231]\n"
AR "v75,a207\n"
AR "v76,a224\n"
AR "v77,a225\n"
AR "v78,a226\n"
AR "v79,a227\n"
"v_pk_fma_f32 v[72:73],v[72:73],v[58:59],v[52:53] op_sel:[0,0,1] op_sel_hi:[1,0,1]\n"
"v_pk_fma_f32 v[74:75],v[74:75],v[58:59],v[52:53] op_sel:[0,0,1] op_sel_hi:[1,0,1]\n"
"v_pk_fma_f32 v[76:77],v[76:77],v[58:59],v[52:53] op_sel:[0,0,1] op_sel_hi:[1,0,1]\n"
"v_pk_fma_f32 v[78:79],v[78:79],v[58:59],v[52:53] op_sel:[0,0,1] op_sel_hi:[1,0,1]\n"
CV "v168,v72,v73\n"
CV "v169,v74,v75\n"
CV "v170,v76,v77\n"
CV "v171,v78,v79\n"
MF "a[212:215],v[176:179],v[104:107],a[212:215]\n"
AR "v80,a208\n"
AR "v81,a209\n"
AR "v82,a210\n"
MF "a[232:235],v[180:183],v[104:107],a[232:235]\n"
AR "v83,a211\n"
AR "v84,a228\n"
AR "v85,a229\n"
AR "v86,a230\n"
AR "v87,a231\n"
"v_pk_fma_f32 v[80:81],v[80:81],v[58:59],v[54:55] op_sel:[0,1,0] op_sel_hi:[1,1,0]\n"
"v_pk_fma_f32 v[82:83],v[82:83],v[58:59],v[54:55] op_sel:[0,1,0] op_sel_hi:[1,1,0]\n"
"v_pk_fma_f32 v[84:85],v[84:85],v[58:59],v[54:55] op_sel:[0,1,0] op_sel_hi:[1,1,0]\n"
"v_pk_fma_f32 v[86:87],v[86:87],v[58:59],v[54:55] op_sel:[0,1,0] op_sel_hi:[1,1,0]\n"
CV "v172,v80,v81\n"
CV "v173,v82,v83\n"
CV "v174,v84,v85\n"
CV "v175,v86,v87\n"
MF "a[216:219],v[176:179],v[108:111],a[216:219]\n"
"s_waitcnt vmcnt(14) lgkmcnt(0)\n"
"s_barrier\n"
TR "a[200:201],v0\n"
TR "a[202:203],v0 offset:512\n"
TR "a[204:205],v0 offset:4096\n"
TR "a[206:207],v0 offset:4608\n"
AR "v72,a212\n"
AR "v73,a213\n"
AR "v74,a214\n"
MF "a[236:239],v[180:183],v[108:111],a[236:239]\n"
AR "v75,a215\n"
AR "v76,a232\n"
AR "v77,a233\n"
AR "v78,a234\n"
AR "v79,a235\n"
"v_pk_fma_f32 v[72:73],v[72:73],v[60:61],v[54:55] op_sel:[0,0,1] op_sel_hi:[1,0,1]\n"
"v_pk_fma_f32 v[74:75],v[74:75],v[60:61],v[54:55] op_sel:[0,0,1] op_sel_hi:[1,0,1]\n"
"v_pk_fma_f32 v[76:77],v[76:77],v[60:61],v[54:55] op_sel:[0,0,1] op_sel_hi:[1,0,1]\n"
"v_pk_fma_f32 v[78:79],v[78:79],v[60:61],v[54:55] op_sel:[0,0,1] op_sel_hi:[1,0,1]\n"
CV "v176,v72,v73\n"
CV "v177,v74,v75\n"
CV "v178,v76,v77\n"
CV "v179,v78,v79\n"
TR "a[208:209],v0 offset:8\n"
TR "a[210:211],v0 offset:520\n"
TR "a[212:213],v0 offset:4104\n"
TR "a[214:215],v0 offset:4616\n"
AR "v80,a216\n"
AR "v81,a217\n"
AR "v82,a218\n"
AR "v83,a219\n"
AR "v84,a236\n"
AR "v85,a237\n"
AR "v86,a238\n"
AR "v87,a239\n"
"v_pk_fma_f32 v[80:81],v[80:81],v[60:61],v[56:57] op_sel:[0,1,0] op_sel_hi:[1,1,0]\n"
"v_pk_fma_f32 v[82:83],v[82:83],v[60:61],v[56:57] op_sel:[0,1,0] op_sel_hi:[1,1,0]\n"
"v_pk_fma_f32 v[84:85],v[84:85],v[60:61],v[56:57] op_sel:[0,1,0] op_sel_hi:[1,1,0]\n"
"v_pk_fma_f32 v[86:87],v[86:87],v[60:61],v[56:57] op_sel:[0,1,0] op_sel_hi:[1,1,0]\n"
CV "v180,v80,v81\n"
CV "v181,v82,v83\n"
CV "v182,v84,v85\n"
CV "v183,v86,v87\n"
"s_mov_b32 s13,s11\n"
"s_mov_b32 s70,s71\n"
"s_mov_b32 s57,1\n"
"s_waitcnt lgkmcnt(0)\n"
"s_branch .Lq0\n"
".Lpgqy_c:\n"
"s_cbranch_execz .Lpgqj_a\n"
".Lqy:\n"
MF "v[216:219],a[216:219],a[60:63],v[216:219]\n"
TR "a[208:209],v0 offset:8\n"
MF "v[216:219],a[220:223],a[100:103],v[216:219]\n"
TR "a[210:211],v0 offset:520\n"
MF "v[224:227],a[220:223],a[104:107],v[224:227]\n"
MF "v[224:227],a[216:219],a[64:67],v[224:227]\n"
TR "a[212:213],v0 offset:4104\n"
MF "v[232:235],a[216:219],a[68:71],v[232:235]\n"
TR "a[214:215],v0 offset:4616\n"
MF "v[232:235],a[220:223],a[108:111],v[232:235]\n"
MF "v[240:243],a[220:223],a[112:115],v[240:243]\n"
MF "v[240:243],a[216:219],a[72:75],v[240:243]\n"
MF "v[248:251],a[216:219],a[76:79],v[248:251]\n"
MF "v[248:251],a[220:223],a[116:119],v[248:251]\n"
MF "v[248:251],a[200:203],a[156:159],v[248:251]\n"
TR "a[216:217],v0 offset:16392\n"
MF "v[248:251],a[204:207],a[196:199],v[248:251]\n"
TR "a[218:219],v0 offset:16904\n"
MF "v[240:243],a[204:207],a[192:195],v[240:243]\n"
MF "v[240:243],a[200:203],a[152:155],v[240:243]\n"
TR "a[220:221],v0 offset:20488\n"
MF "v[232:235],a[200:203],a[148:151],v[232:235]\n"
TR "a[222:223],v0 offset:21000\n"
MF "v[232:235],a[204:207],a[188:191],v[232:235]\n"
MF "v[224:227],a[204:207],a[184:187],v[224:227]\n"
MF "v[224:227],a[200:203],a[144:147],v[224:227]\n"
"v_fma_f32 v248,v248,s54,v14\n"
"v_fma_f32 v249,v249,s54,v14\n"
MF "v[216:219],a[200:203],a[140:143],v[216:219]\n"
"v_fma_f32 v250,v250,s54,v14\n"
"v_fma_f32 v251,v251,s54,v14\n"
MF "v[216:219],a[204:207],a[180:183],v[216:219]\n"
"s_waitcnt lgkmcnt(4)\n"
"v_fma_f32 v252,v252,s54,v14\n"
"v_fma_f32 v253,v253,s54,v14\n"
MF "v[180:183],a[208:211],a[60:63],v[180:183]\n"
TR "a[200:201],v0\n"
"v_fma_f32 v254,v254,s54,v14\n"
MF "v[180:183],a[212:215],a[100:103],v[180:183]\n"
TR "a[202:203],v0 offset:512\n"
"v_fma_f32 v255,v255,s54,v14\n"
MF "v[188:191],a[212:215],a[104:107],v[188:191]\n"
CV "v255,v254,v255\n"
CV "v254,v252,v253\n"
MF "v[188:191],a[208:211],a[64:67],v[188:191]\n"
TR "a[204:205],v0 offset:4096\n"
CV "v253,v250,v251\n"
MF "v[196:199],a[208:211],a[68:71],v[196:199]\n"
TR "a[206:207],v0 offset:4608\n"
CV "v252,v248,v249\n"
MF "v[196:199],a[212:215],a[108:111],v[196:199]\n"
"s_add_u32 s58,s75,4096\n"
"v_fma_f32 v240,v240,s54,v14\n"
"v_fma_f32 v241,v241,s54,v14\n"
MF "v[204:207],a[212:215],a[112:115],v[204:207]\n"
"buffer_store_dwordx4 v[252:255],v9,s[40:43],s58 offen offset:1024 sc1\n"
MF "v[204:207],a[208:211],a[72:75],v[204:207]\n"
"v_fma_f32 v242,v242,s54,v14\n"
"v_fma_f32 v243,v243,s54,v14\n"
MF "v[212:215],a[208:211],a[76:79],v[212:215]\n"
"v_fma_f32 v244,v244,s54,v14\n"
"v_fma_f32 v245,v245,s54,v14\n"
MF "v[212:215],a[212:215],a[116:119],v[212:215]\n"
"s_waitcnt lgkmcnt(4)\n"
"v_fma_f32 v246,v246,s54,v14\n"
"v_fma_f32 v247,v247,s54,v14\n"
MF "v[180:183],a[216:219],a[140:143],v[180:183]\n"
TR "a[208:209],v0 offset:16384\n"
CV "v251,v246,v247\n"
MF "v[180:183],a[220:223],a[180:183],v[180:183]\n"
TR "a[210:211],v0 offset:16896\n"
CV "v250,v244,v245\n"
MF "v[188:191],a[220:223],a[184:187],v[188:191]\n"
CV "v249,v242,v243\n"
CV "v248,v240,v241\n"
MF "v[188:191],a[216:219],a[144:147],v[188:191]\n"
TR "a[212:213],v0 offset:20480\n"
"v_fma_f32 v232,v232,s54,v14\n"
MF "v[196:199],a[216:219],a[148:151],v[196:199]\n"
TR "a[214:215],v0 offset:20992\n"
"v_fma_f32 v233,v233,s54,v14\n"
MF "v[196:199],a[220:223],a[188:191],v[196:199]\n"
"buffer_store_dwordx4 v[248:251],v9,s[40:43],s58 offen sc1\n"
MF "v[204:207],a[220:223],a[192:195],v[204:207]\n"
"v_fma_f32 v234,v234,s54,v14\n"
"v_fma_f32 v235,v235,s54,v14\n"
MF "v[204:207],a[216:219],a[152:155],v[204:207]\n"
"v_fma_f32 v236,v236,s54,v14\n"
"v_fma_f32 v237,v237,s54,v14\n"
MF "v[212:215],a[216:219],a[156:159],v[212:215]\n"
"s_waitcnt vmcnt(10) lgkmcnt(0)\n"
"v_fma_f32 v238,v238,s54,v14\n"
"v_fma_f32 v239,v239,s54,v14\n"
MF "v[212:215],a[220:223],a[196:199],v[212:215]\n"
"s_barrier\n"
MF "v[176:179],a[200:203],a[60:63],v[176:179]\n"
TR "a[216:217],v1 offset:32776\n"
CV "v247,v238,v239\n"
MF "v[176:179],a[204:207],a[100:103],v[176:179]\n"
TR "a[218:219],v1 offset:33288\n"
CV "v246,v236,v237\n"
MF "v[184:187],a[204:207],a[104:107],v[184:187]\n"
"s_add_u32 s66,s67,4718592\n"
"s_sub_u32 s81,s81,384\n"
"s_mov_b32 s83,7077888\n"
MF "v[184:187],a[200:203],a[64:67],v[184:187]\n"
"s_add_u32 m0,s62,0\n"
"s_add_u32 s82,s66,2359296\n"
TR "a[220:221],v1 offset:36872\n"
MF "v[192:195],a[200:203],a[68:71],v[192:195]\n"
"s_sub_u32 s15,s82,7864320\n"
"s_min_u32 s82,s82,s15\n"
TR "a[222:223],v1 offset:37384\n"
MF "v[192:195],a[204:207],a[108:111],v[192:195]\n"
LD "v6,s[28:31],s82 offen lds\n"
MF "v[200:203],a[204:207],a[112:115],v[200:203]\n"
CV "v245,v234,v235\n"
CV "v244,v232,v233\n"
MF "v[200:203],a[200:203],a[72:75],v[200:203]\n"
"v_fma_f32 v224,v224,s54,v14\n"
"v_fma_f32 v225,v225,s54,v14\n"
MF "v[208:211],a[200:203],a[76:79],v[208:211]\n"
"buffer_store_dwordx4 v[244:247],v9,s[40:43],s75 offen offset:3072 sc1\n"
MF "v[208:211],a[204:207],a[116:119],v[208:211]\n"
LD "v11,s[28:31],s82 offen offset:1024 lds\n"
MF "v[208:211],a[208:211],a[156:159],v[208:211]\n"
TR "a[200:201],v1 offset:49160\n"
"v_fma_f32 v226,v226,s54,v14\n"
MF "v[208:211],a[212:215],a[196:199],v[208:211]\n"
TR "a[202:203],v1 offset:49672\n"
"v_fma_f32 v227,v227,s54,v14\n"
MF "v[200:203],a[212:215],a[192:195],v[200:203]\n"
"v_fma_f32 v228,v228,s54,v14\n"
"v_fma_f32 v229,v229,s54,v14\n"
MF "v[200:203],a[208:211],a[152:155],v[200:203]\n"
TR "a[204:205],v1 offset:53256\n"
"v_fma_f32 v230,v230,s54,v14\n"
MF "v[192:195],a[208:211],a[148:151],v[192:195]\n"
TR "a[206:207],v1 offset:53768\n"
"v_fma_f32 v231,v231,s54,v14\n"
MF "v[192:195],a[212:215],a[188:191],v[192:195]\n"
CV "v243,v230,v231\n"
CV "v242,v228,v229\n"
MF "v[184:187],a[212:215],a[184:187],v[184:187]\n"
CV "v241,v226,v227\n"
CV "v240,v224,v225\n"
MF "v[184:187],a[208:211],a[144:147],v[184:187]\n"
"buffer_store_dwordx4 v[240:243],v9,s[40:43],s75 offen offset:2048 sc1\n"
MF "v[176:179],a[208:211],a[140:143],v[176:179]\n"
LD "v12,s[28:31],s82 offen offset:2048 lds\n"
MF "v[176:179],a[212:215],a[180:183],v[176:179]\n"
"s_waitcnt lgkmcnt(4)\n"
"v_fma_f32 v216,v216,s54,v14\n"
"v_fma_f32 v217,v217,s54,v14\n"
MF "v[140:143],a[216:219],a[60:63],v[140:143]\n"
TR "a[208:209],v1 offset:32768\n"
"v_fma_f32 v218,v218,s54,v14\n"
MF "v[140:143],a[220:223],a[100:103],v[140:143]\n"
TR "a[210:211],v1 offset:33280\n"
"v_fma_f32 v219,v219,s54,v14\n"
MF "v[148:151],a[220:223],a[104:107],v[148:151]\n"
".Lpg14c:\n"
"s_cbranch_execz .Lpg3a\n"
"v_fma_f32 v220,v220,s54,v14\n"
"v_fma_f32 v221,v221,s54,v14\n"
MF "v[148:151],a[216:219],a[64:67],v[148:151]\n"
TR "a[212:213],v1 offset:36864\n"
"v_fma_f32 v222,v222,s54,v14\n"
MF "v[156:159],a[216:219],a[68:71],v[156:159]\n"
TR "a[214:215],v1 offset:37376\n"
"v_fma_f32 v223,v223,s54,v14\n"
MF "v[156:159],a[220:223],a[108:111],v[156:159]\n"
CV "v239,v222,v223\n"
CV "v238,v220,v221\n"
MF "v[164:167],a[220:223],a[112:115],v[164:167]\n"
CV "v237,v218,v219\n"
CV "v236,v216,v217\n"
MF "v[164:167],a[216:219],a[72:75],v[164:167]\n"
LD "v13,s[28:31],s82 offen offset:3072 lds\n"
MF "v[172:175],a[216:219],a[76:79],v[172:175]\n"
"buffer_store_dwordx4 v[236:239],v9,s[40:43],s75 offen offset:1024 sc1\n"
MF "v[172:175],a[220:223],a[116:119],v[172:175]\n"
"s_waitcnt lgkmcnt(4)\n"
"v_fma_f32 v208,v208,s54,v14\n"
"v_fma_f32 v209,v209,s54,v14\n"
MF "v[140:143],a[200:203],a[140:143],v[140:143]\n"
TR "a[216:217],v1 offset:49152\n"
"v_fma_f32 v210,v210,s54,v14\n"
MF "v[140:143],a[204:207],a[180:183],v[140:143]\n"
TR "a[218:219],v1 offset:49664\n"
"v_fma_f32 v211,v211,s54,v14\n"
MF "v[148:151],a[204:207],a[184:187],v[148:151]\n"
"v_fma_f32 v212,v212,s54,v14\n"
"v_fma_f32 v213,v213,s54,v14\n"
MF "v[148:151],a[200:203],a[144:147],v[148:151]\n"
TR "a[220:221],v1 offset:53248\n"
"v_fma_f32 v214,v214,s54,v14\n"
MF "v[156:159],a[200:203],a[148:151],v[156:159]\n"
TR "a[222:223],v1 offset:53760\n"
"v_fma_f32 v215,v215,s54,v14\n"
MF "v[156:159],a[204:207],a[188:191],v[156:159]\n"
CV "v235,v214,v215\n"
CV "v234,v212,v213\n"
MF "v[164:167],a[204:207],a[192:195],v[164:167]\n"
CV "v233,v210,v211\n"
CV "v232,v208,v209\n"
MF "v[164:167],a[200:203],a[152:155],v[164:167]\n"
"v_fma_f32 v200,v200,s54,v14\n"
"v_fma_f32 v201,v201,s54,v14\n"
MF "v[172:175],a[200:203],a[156:159],v[172:175]\n"
"buffer_store_dwordx4 v[232:235],v9,s[40:43],s75 offen sc1\n"
MF "v[172:175],a[204:207],a[196:199],v[172:175]\n"
"s_waitcnt lgkmcnt(0)\n"
"v_fma_f32 v202,v202,s54,v14\n"
"v_fma_f32 v203,v203,s54,v14\n"
MF "v[136:139],a[208:211],a[60:63],v[136:139]\n"
"s_add_u32 m0,s62,16384\n"
"s_add_u32 s82,s66,2359424\n"
TR "a[200:201],v0 offset:32776\n"
MF "v[136:139],a[212:215],a[100:103],v[136:139]\n"
"s_sub_u32 s15,s82,7864320\n"
"s_min_u32 s82,s82,s15\n"
TR "a[202:203],v0 offset:33288\n"
MF "v[144:147],a[212:215],a[104:107],v[144:147]\n"
LD "v6,s[28:31],s82 offen lds\n"
MF "v[144:147],a[208:211],a[64:67],v[144:147]\n"
TR "a[204:205],v0 offset:36872\n"
"v_fma_f32 v204,v204,s54,v14\n"
MF "v[152:155],a[208:211],a[68:71],v[152:155]\n"
TR "a[206:207],v0 offset:37384\n"
"v_fma_f32 v205,v205,s54,v14\n"
MF "v[152:155],a[212:215],a[108:111],v[152:155]\n"
"v_fma_f32 v206,v206,s54,v14\n"
"v_fma_f32 v207,v207,s54,v14\n"
MF "v[160:163],a[212:215],a[112:115],v[160:163]\n"
CV "v231,v206,v207\n"
CV "v230,v204,v205\n"
MF "v[160:163],a[208:211],a[72:75],v[160:163]\n"
CV "v229,v202,v203\n"
CV "v228,v200,v201\n"
MF "v[168:171],a[208:211],a[76:79],v[168:171]\n"
"v_fma_f32 v192,v192,s54,v14\n"
"v_fma_f32 v193,v193,s54,v14\n"
MF "v[168:171],a[212:215],a[116:119],v[168:171]\n"
LD "v11,s[28:31],s82 offen offset:1024 lds\n"
MF "v[168:171],a[216:219],a[156:159],v[168:171]\n"
TR "a[208:209],v0 offset:49160\n"
"v_fma_f32 v194,v194,s54,v14\n"
MF "v[168:171],a[220:223],a[196:199],v[168:171]\n"
TR "a[210:211],v0 offset:49672\n"
"v_fma_f32 v195,v195,s54,v14\n"
MF "v[160:163],a[220:223],a[192:195],v[160:163]\n"
"v_fma_f32 v196,v196,s54,v14\n"
"v_fma_f32 v197,v197,s54,v14\n"
MF "v[160:163],a[216:219],a[152:155],v[160:163]\n"
TR "a[212:213],v0 offset:53256\n"
"v_fma_f32 v198,v198,s54,v14\n"
MF "v[152:155],a[216:219],a[148:151],v[152:155]\n"
TR "a[214:215],v0 offset:53768\n"
"v_fma_f32 v199,v199,s54,v14\n"
MF "v[152:155],a[220:223],a[188:191],v[152:155]\n"
CV "v227,v198,v199\n"
CV "v226,v196,v197\n"
MF "v[144:147],a[220:223],a[184:187],v[144:147]\n"
CV "v225,v194,v195\n"
CV "v224,v192,v193\n"
MF "v[144:147],a[216:219],a[144:147],v[144:147]\n"
"v_fma_f32 v184,v184,s54,v14\n"
"v_fma_f32 v185,v185,s54,v14\n"
MF "v[136:139],a[216:219],a[140:143],v[136:139]\n"
LD "v12,s[28:31],s82 offen offset:2048 lds\n"
MF "v[136:139],a[220:223],a[180:183],v[136:139]\n"
"s_waitcnt lgkmcnt(4)\n"
"v_fma_f32 v186,v186,s54,v14\n"
"v_fma_f32 v187,v187,s54,v14\n"
MF "v[100:103],a[200:203],a[60:63],v[100:103]\n"
TR "a[216:217],v0 offset:32768\n"
"v_fma_f32 v188,v188,s54,v14\n"
MF "v[100:103],a[204:207],a[100:103],v[100:103]\n"
TR "a[218:219],v0 offset:33280\n"
"v_fma_f32 v189,v189,s54,v14\n"
MF "v[108:111],a[204:207],a[104:107],v[108:111]\n"
"v_fma_f32 v190,v190,s54,v14\n"
"v_fma_f32 v191,v191,s54,v14\n"
MF "v[108:111],a[200:203],a[64:67],v[108:111]\n"
TR "a[220:221],v0 offset:36864\n"
CV "v223,v190,v191\n"
MF "v[116:119],a[200:203],a[68:71],v[116:119]\n"
TR "a[222:223],v0 offset:37376\n"
CV "v222,v188,v189\n"
MF "v[116:119],a[204:207],a[108:111],v[116:119]\n"
CV "v221,v186,v187\n"
CV "v220,v184,v185\n"
MF "v[124:127],a[204:207],a[112:115],v[124:127]\n"
AW "a56,v220\n"
AW "a57,v221\n"
MF "v[124:127],a[200:203],a[72:75],v[124:127]\n"
LD "v13,s[28:31],s82 offen offset:3072 lds\n"
MF "v[132:135],a[200:203],a[76:79],v[132:135]\n"
AW "a58,v222\n"
AW "a59,v223\n"
MF "v[132:135],a[204:207],a[116:119],v[132:135]\n"
"s_waitcnt lgkmcnt(4)\n"
"v_fma_f32 v176,v176,s54,v14\n"
"v_fma_f32 v177,v177,s54,v14\n"
MF "v[100:103],a[208:211],a[140:143],v[100:103]\n"
TR "a[200:201],v0 offset:49152\n"
"v_fma_f32 v178,v178,s54,v14\n"
MF "v[100:103],a[212:215],a[180:183],v[100:103]\n"
TR "a[202:203],v0 offset:49664\n"
"v_fma_f32 v179,v179,s54,v14\n"
MF "v[108:111],a[212:215],a[184:187],v[108:111]\n"
"v_fma_f32 v180,v180,s54,v14\n"
"v_fma_f32 v181,v181,s54,v14\n"
MF "v[108:111],a[208:211],a[144:147],v[108:111]\n"
TR "a[204:205],v0 offset:53248\n"
"v_fma_f32 v182,v182,s54,v14\n"
MF "v[116:119],a[208:211],a[148:151],v[116:119]\n"
TR "a[206:207],v0 offset:53760\n"
"v_fma_f32 v183,v183,s54,v14\n"
MF "v[116:119],a[212:215],a[188:191],v[116:119]\n"
CV "v219,v182,v183\n"
CV "v218,v180,v181\n"
MF "v[124:127],a[212:215],a[192:195],v[124:127]\n"
"v_add_u32 v0,0xc000,v0\n"
CV "v217,v178,v179\n"
MF "v[124:127],a[208:211],a[152:155],v[124:127]\n"
"v_add_u32 v1,0xc000,v1\n"
CV "v216,v176,v177\n"
MF "v[132:135],a[208:211],a[156:159],v[132:135]\n"
"s_waitcnt vmcnt(14) lgkmcnt(0)\n"
"v_fma_f32 v168,v168,s54,v14\n"
"v_fma_f32 v169,v169,s54,v14\n"
MF "v[132:135],a[212:215],a[196:199],v[132:135]\n"
"s_barrier\n"
MF "v[96:99],a[216:219],a[60:63],v[96:99]\n"
"s_add_u32 m0,s62,32768\n"
"s_add_u32 s82,s66,2359552\n"
TR "a[208:209],v1 offset:16392\n"
MF "v[96:99],a[220:223],a[100:103],v[96:99]\n"
"s_sub_u32 s15,s82,7864320\n"
"s_min_u32 s82,s82,s15\n"
TR "a[210:211],v1 offset:16904\n"
MF "v[104:107],a[220:223],a[104:107],v[104:107]\n"
LD "v6,s[28:31],s82 offen lds\n"
MF "v[104:107],a[216:219],a[64:67],v[104:107]\n"
TR "a[212:213],v1 offset:20488\n"
"v_fma_f32 v170,v170,s54,v14\n"
MF "v[112:115],a[216:219],a[68:71],v[112:115]\n"
TR "a[214:215],v1 offset:21000\n"
"v_fma_f32 v171,v171,s54,v14\n"
MF "v[112:115],a[220:223],a[108:111],v[112:115]\n"
"v_fma_f32 v172,v172,s54,v14\n"
"v_fma_f32 v173,v173,s54,v14\n"
MF "v[120:123],a[220:223],a[112:115],v[120:123]\n"
"v_fma_f32 v174,v174,s54,v14\n"
"v_fma_f32 v175,v175,s54,v14\n"
MF "v[120:123],a[216:219],a[72:75],v[120:123]\n"
CV "v215,v174,v175\n"
CV "v214,v172,v173\n"
MF "v[128:131],a[216:219],a[76:79],v[128:131]\n"
CV "v213,v170,v171\n"
CV "v212,v168,v169\n"
MF "v[128:131],a[220:223],a[116:119],v[128:131]\n"
LD "v11,s[28:31],s82 offen offset:1024 lds\n"
MF "v[128:131],a[200:203],a[156:159],v[128:131]\n"
TR "a[216:217],v1 offset:32776\n"
"v_fma_f32 v160,v160,s54,v14\n"
MF "v[128:131],a[204:207],a[196:199],v[128:131]\n"
TR "a[218:219],v1 offset:33288\n"
"v_fma_f32 v161,v161,s54,v14\n"
MF "v[120:123],a[204:207],a[192:195],v[120:123]\n"
"v_fma_f32 v162,v162,s54,v14\n"
"v_fma_f32 v163,v163,s54,v14\n"
MF "v[120:123],a[200:203],a[152:155],v[120:123]\n"
TR "a[220:221],v1 offset:36872\n"
"v_fma_f32 v164,v164,s54,v14\n"
MF "v[112:115],a[200:203],a[148:151],v[112:115]\n"
TR "a[222:223],v1 offset:37384\n"
"v_fma_f32 v165,v165,s54,v14\n"
MF "v[112:115],a[204:207],a[188:191],v[112:115]\n"
"v_fma_f32 v166,v166,s54,v14\n"
"v_fma_f32 v167,v167,s54,v14\n"
MF "v[104:107],a[204:207],a[184:187],v[104:107]\n"
CV "v211,v166,v167\n"
CV "v210,v164,v165\n"
MF "v[104:107],a[200:203],a[144:147],v[104:107]\n"
CV "v209,v162,v163\n"
CV "v208,v160,v161\n"
MF "v[96:99],a[200:203],a[140:143],v[96:99]\n"
LD "v12,s[28:31],s82 offen offset:2048 lds\n"
MF "v[96:99],a[204:207],a[180:183],v[96:99]\n"
"s_waitcnt lgkmcnt(4)\n"
"v_fma_f32 v152,v152,s54,v14\n"
"v_fma_f32 v153,v153,s54,v14\n"
MF "v[60:63],a[208:211],a[60:63],v[60:63]\n"
TR "a[200:201],v1 offset:16384\n"
"v_fma_f32 v154,v154,s54,v14\n"
MF "v[60:63],a[212:215],a[100:103],v[60:63]\n"
TR "a[202:203],v1 offset:16896\n"
"v_fma_f32 v155,v155,s54,v14\n"
MF "v[68:71],a[212:215],a[104:107],v[68:71]\n"
"v_fma_f32 v156,v156,s54,v14\n"
"v_fma_f32 v157,v157,s54,v14\n"
MF "v[68:71],a[208:211],a[64:67],v[68:71]\n"
TR "a[204:205],v1 offset:20480\n"
"v_fma_f32 v158,v158,s54,v14\n"
MF "v[76:79],a[208:211],a[68:71],v[76:79]\n"
TR "a[206:207],v1 offset:20992\n"
"v_fma_f32 v159,v159,s54,v14\n"
MF "v[76:79],a[212:215],a[108:111],v[76:79]\n"
CV "v207,v158,v159\n"
CV "v206,v156,v157\n"
MF "v[84:87],a[212:215],a[112:115],v[84:87]\n"
CV "v205,v154,v155\n"
CV "v204,v152,v153\n"
MF "v[84:87],a[208:211],a[72:75],v[84:87]\n"
LD "v13,s[28:31],s82 offen offset:3072 lds\n"
MF "v[92:95],a[208:211],a[76:79],v[92:95]\n"
"v_fma_f32 v144,v144,s54,v14\n"
"v_fma_f32 v145,v145,s54,v14\n"
MF "v[92:95],a[212:215],a[116:119],v[92:95]\n"
"s_waitcnt lgkmcnt(4)\n"
"v_fma_f32 v146,v146,s54,v14\n"
"v_fma_f32 v147,v147,s54,v14\n"
MF "v[60:63],a[216:219],a[140:143],v[60:63]\n"
TR "a[208:209],v1 offset:32768\n"
"v_fma_f32 v148,v148,s54,v14\n"
MF "v[60:63],a[220:223],a[180:183],v[60:63]\n"
TR "a[210:211],v1 offset:33280\n"
"v_fma_f32 v149,v149,s54,v14\n"
MF "v[68:71],a[220:223],a[184:187],v[68:71]\n"
"v_fma_f32 v150,v150,s54,v14\n"
"v_fma_f32 v151,v151,s54,v14\n"
MF "v[68:71],a[216:219],a[144:147],v[68:71]\n"
TR "a[212:213],v1 offset:36864\n"
CV "v203,v150,v151\n"
MF "v[76:79],a[216:219],a[148:151],v[76:79]\n"
TR "a[214:215],v1 offset:37376\n"
CV "v202,v148,v149\n"
MF "v[76:79],a[220:223],a[188:191],v[76:79]\n"
CV "v201,v146,v147\n"
CV "v200,v144,v145\n"
MF "v[84:87],a[220:223],a[192:195],v[84:87]\n"
"v_fma_f32 v136,v136,s54,v14\n"
"v_fma_f32 v137,v137,s54,v14\n"
MF "v[84:87],a[216:219],a[152:155],v[84:87]\n"
"v_fma_f32 v138,v138,s54,v14\n"
"v_fma_f32 v139,v139,s54,v14\n"
MF "v[92:95],a[216:219],a[156:159],v[92:95]\n"
"v_fma_f32 v140,v140,s54,v14\n"
"v_fma_f32 v141,v141,s54,v14\n"
MF "v[92:95],a[220:223],a[196:199],v[92:95]\n"
"s_waitcnt lgkmcnt(0)\n"
"v_fma_f32 v142,v142,s54,v14\n"
"v_fma_f32 v143,v143,s54,v14\n"
MF "v[56:59],a[200:203],a[60:63],v[56:59]\n"
TR "a[216:217],v0 offset:16392\n"
CV "v199,v142,v143\n"
MF "v[56:59],a[204:207],a[100:103],v[56:59]\n"
TR "a[218:219],v0 offset:16904\n"
CV "v198,v140,v141\n"
MF "v[64:67],a[204:207],a[104:107],v[64:67]\n"
CV "v197,v138,v139\n"
CV "v196,v136,v137\n"
MF "v[64:67],a[200:203],a[64:67],v[64:67]\n"
TR "a[220:221],v0 offset:20488\n"
"v_fma_f32 v128,v128,s54,v14\n"
MF "v[72:75],a[200:203],a[68:71],v[72:75]\n"
TR "a[222:223],v0 offset:21000\n"
"v_fma_f32 v129,v129,s54,v14\n"
MF "v[72:75],a[204:207],a[108:111],v[72:75]\n"
"v_fma_f32 v130,v130,s54,v14\n"
"v_fma_f32 v131,v131,s54,v14\n"
MF "v[80:83],a[204:207],a[112:115],v[80:83]\n"
"v_fma_f32 v132,v132,s54,v14\n"
"v_fma_f32 v133,v133,s54,v14\n"
MF "v[80:83],a[200:203],a[72:75],v[80:83]\n"
"v_fma_f32 v134,v134,s54,v14\n"
"v_fma_f32 v135,v135,s54,v14\n"
MF "v[88:91],a[200:203],a[76:79],v[88:91]\n"
CV "v195,v134,v135\n"
CV "v194,v132,v133\n"
MF "v[88:91],a[204:207],a[116:119],v[88:91]\n"
CV "v193,v130,v131\n"
CV "v192,v128,v129\n"
MF "v[88:91],a[208:211],a[156:159],v[88:91]\n"
TR "a[200:201],v0 offset:32776\n"
"v_fma_f32 v120,v120,s54,v14\n"
MF "v[88:91],a[212:215],a[196:199],v[88:91]\n"
TR "a[202:203],v0 offset:33288\n"
"v_fma_f32 v121,v121,s54,v14\n"
MF "v[80:83],a[212:215],a[192:195],v[80:83]\n"
"v_fma_f32 v122,v122,s54,v14\n"
"v_fma_f32 v123,v123,s54,v14\n"
MF "v[80:83],a[208:211],a[152:155],v[80:83]\n"
TR "a[204:205],v0 offset:36872\n"
"v_fma_f32 v124,v124,s54,v14\n"
MF "v[72:75],a[208:211],a[148:151],v[72:75]\n"
TR "a[206:207],v0 offset:37384\n"
"v_fma_f32 v125,v125,s54,v14\n"
MF "v[72:75],a[212:215],a[188:191],v[72:75]\n"
"v_fma_f32 v126,v126,s54,v14\n"
"v_fma_f32 v127,v127,s54,v14\n"
MF "v[64:67],a[212:215],a[184:187],v[64:67]\n"
CV "v191,v126,v127\n"
CV "v190,v124,v125\n"
MF "v[64:67],a[208:211],a[144:147],v[64:67]\n"
CV "v189,v122,v123\n"
CV "v188,v120,v121\n"
MF "v[56:59],a[208:211],a[140:143],v[56:59]\n"
"v_fma_f32 v112,v112,s54,v14\n"
"v_fma_f32 v113,v113,s54,v14\n"
MF "v[56:59],a[212:215],a[180:183],v[56:59]\n"
"s_waitcnt lgkmcnt(4)\n"
"v_fma_f32 v114,v114,s54,v14\n"
"v_fma_f32 v115,v115,s54,v14\n"
MF "v[20:23],a[216:219],a[60:63],v[20:23]\n"
TR "a[208:209],v0 offset:16384\n"
"v_fma_f32 v116,v116,s54,v14\n"
MF "v[20:23],a[220:223],a[100:103],v[20:23]\n"
TR "a[210:211],v0 offset:16896\n"
"v_fma_f32 v117,v117,s54,v14\n"
MF "v[28:31],a[220:223],a[104:107],v[28:31]\n"
"v_fma_f32 v118,v118,s54,v14\n"
"v_fma_f32 v119,v119,s54,v14\n"
MF "v[28:31],a[216:219],a[64:67],v[28:31]\n"
TR "a[212:213],v0 offset:20480\n"
CV "v187,v118,v119\n"
MF "v[36:39],a[216:219],a[68:71],v[36:39]\n"
TR "a[214:215],v0 offset:20992\n"
CV "v186,v116,v117\n"
MF "v[36:39],a[220:223],a[108:111],v[36:39]\n"
CV "v185,v114,v115\n"
CV "v184,v112,v113\n"
MF "v[44:47],a[220:223],a[112:115],v[44:47]\n"
"v_fma_f32 v104,v104,s54,v14\n"
"v_fma_f32 v105,v105,s54,v14\n"
MF "v[44:47],a[216:219],a[72:75],v[44:47]\n"
"v_fma_f32 v106,v106,s54,v14\n"
"v_fma_f32 v107,v107,s54,v14\n"
MF "v[52:55],a[216:219],a[76:79],v[52:55]\n"
"v_fma_f32 v108,v108,s54,v14\n"
"v_fma_f32 v109,v109,s54,v14\n"
MF "v[52:55],a[220:223],a[116:119],v[52:55]\n"
"s_waitcnt lgkmcnt(4)\n"
"v_fma_f32 v110,v110,s54,v14\n"
"v_fma_f32 v111,v111,s54,v14\n"
MF "v[20:23],a[200:203],a[140:143],v[20:23]\n"
TR "a[216:217],v0 offset:32768\n"
CV "v183,v110,v111\n"
MF "v[20:23],a[204:207],a[180:183],v[20:23]\n"
TR "a[218:219],v0 offset:33280\n"
CV "v182,v108,v109\n"
MF "v[28:31],a[204:207],a[184:187],v[28:31]\n"
CV "v181,v106,v107\n"
CV "v180,v104,v105\n"
MF "v[28:31],a[200:203],a[144:147],v[28:31]\n"
TR "a[220:221],v0 offset:36864\n"
"v_fma_f32 v96,v96,s54,v14\n"
MF "v[36:39],a[200:203],a[148:151],v[36:39]\n"
TR "a[222:223],v0 offset:37376\n"
"v_fma_f32 v97,v97,s54,v14\n"
MF "v[36:39],a[204:207],a[188:191],v[36:39]\n"
"v_fma_f32 v98,v98,s54,v14\n"
"v_fma_f32 v99,v99,s54,v14\n"
MF "v[44:47],a[204:207],a[192:195],v[44:47]\n"
"v_fma_f32 v100,v100,s54,v14\n"
"v_fma_f32 v101,v101,s54,v14\n"
MF "v[44:47],a[200:203],a[152:155],v[44:47]\n"
"v_fma_f32 v102,v102,s54,v14\n"
"v_fma_f32 v103,v103,s54,v14\n"
MF "v[52:55],a[200:203],a[156:159],v[52:55]\n"
"s_waitcnt lgkmcnt(0)\n"
CV "v179,v102,v103\n"
CV "v178,v100,v101\n"
MF "v[52:55],a[204:207],a[196:199],v[52:55]\n"
"s_barrier\n"
MF "v[16:19],a[208:211],a[60:63],v[16:19]\n"
CV "v177,v98,v99\n"
CV "v176,v96,v97\n"
MF "v[16:19],a[212:215],a[100:103],v[16:19]\n"
"v_fma_f32 v88,v88,s54,v14\n"
"v_fma_f32 v89,v89,s54,v14\n"
MF "v[24:27],a[212:215],a[104:107],v[24:27]\n"
"v_fma_f32 v90,v90,s54,v14\n"
"v_fma_f32 v91,v91,s54,v14\n"
MF "v[24:27],a[208:211],a[64:67],v[24:27]\n"
"v_fma_f32 v92,v92,s54,v14\n"
"v_fma_f32 v93,v93,s54,v14\n"
MF "v[32:35],a[208:211],a[68:71],v[32:35]\n"
"v_fma_f32 v94,v94,s54,v14\n"
"v_fma_f32 v95,v95,s54,v14\n"
MF "v[32:35],a[212:215],a[108:111],v[32:35]\n"
CV "v175,v94,v95\n"
CV "v174,v92,v93\n"
MF "v[40:43],a[212:215],a[112:115],v[40:43]\n"
CV "v173,v90,v91\n"
CV "v172,v88,v89\n"
MF "v[40:43],a[208:211],a[72:75],v[40:43]\n"
"v_fma_f32 v80,v80,s54,v14\n"
"v_fma_f32 v81,v81,s54,v14\n"
MF "v[48:51],a[208:211],a[76:79],v[48:51]\n"
"v_fma_f32 v82,v82,s54,v14\n"
"v_fma_f32 v83,v83,s54,v14\n"
MF "v[48:51],a[212:215],a[116:119],v[48:51]\n"
"v_fma_f32 v84,v84,s54,v14\n"
"v_fma_f32 v85,v85,s54,v14\n"
MF "v[48:51],a[216:219],a[156:159],v[48:51]\n"
"v_fma_f32 v86,v86,s54,v14\n"
"v_fma_f32 v87,v87,s54,v14\n"
MF "v[48:51],a[220:223],a[196:199],v[48:51]\n"
CV "v171,v86,v87\n"
CV "v170,v84,v85\n"
MF "v[40:43],a[220:223],a[192:195],v[40:43]\n"
CV "v169,v82,v83\n"
CV "v168,v80,v81\n"
MF "v[40:43],a[216:219],a[152:155],v[40:43]\n"
"v_fma_f32 v72,v72,s54,v14\n"
"v_fma_f32 v73,v73,s54,v14\n"
MF "v[32:35],a[216:219],a[148:151],v[32:35]\n"
"v_fma_f32 v74,v74,s54,v14\n"
"v_fma_f32 v75,v75,s54,v14\n"
MF "v[32:35],a[220:223],a[188:191],v[32:35]\n"
"v_fma_f32 v76,v76,s54,v14\n"
"v_fma_f32 v77,v77,s54,v14\n"
MF "v[24:27],a[220:223],a[184:187],v[24:27]\n"
"v_fma_f32 v78,v78,s54,v14\n"
"v_fma_f32 v79,v79,s54,v14\n"
MF "v[24:27],a[216:219],a[144:147],v[24:27]\n"
CV "v167,v78,v79\n"
CV "v166,v76,v77\n"
MF "v[16:19],a[216:219],a[140:143],v[16:19]\n"
CV "v165,v74,v75\n"
CV "v164,v72,v73\n"
MF "v[16:19],a[220:223],a[180:183],v[16:19]\n"
"s_mov_b32 s57,1\n"
"s_mov_b32 s69,99\n"
AR "v0,a240\n"
AR "v2,a241\n"
AR "v4,a242\n"
AR "v6,a243\n"
"v_xor_b32 v1,64,v0\n"
"v_xor_b32 v3,64,v2\n"
"v_xor_b32_e64 v5,64,v4\n"
"v_add_u32 v11,97280,v6\n"
"v_add_u32 v12,194560,v6\n"
"v_add_u32 v13,291840,v6\n"
"s_mov_b64 vcc,-1\n"
"s_waitcnt vmcnt(10) lgkmcnt(0)\n"
"s_barrier\n"
"s_nop 0\n"
TR "a[200:201],v0\n"
TR "a[202:203],v0 offset:512\n"
TR "a[204:205],v0 offset:4096\n"
TR "a[206:207],v0 offset:4608\n"
"ds_read_b128 a[60:63],v2\n"
"ds_read_b128 a[100:103],v3\n"
"ds_read_b128 a[64:67],v2 offset:2048\n"
"ds_read_b128 a[104:107],v3 offset:2048\n"
"ds_read_b128 a[68:71],v2 offset:4096\n"
"ds_read_b128 a[108:111],v3 offset:4096\n"
"ds_read_b128 a[72:75],v2 offset:6144\n"
"ds_read_b128 a[112:115],v3 offset:6144\n"
"ds_read_b128 a[76:79],v2 offset:8192\n"
"ds_read_b128 a[116:119],v3 offset:8192\n"
TR "a[208:209],v0 offset:8\n"
TR "a[210:211],v0 offset:520\n"
TR "a[212:213],v0 offset:4104\n"
TR "a[214:215],v0 offset:4616\n"
"v_pk_fma_f32 v[64:65],v[64:65],s[54:55],v[14:15] op_sel_hi:[1,1,0]\n"
"v_pk_fma_f32 v[66:67],v[66:67],s[54:55],v[14:15] op_sel_hi:[1,1,0]\n"
"v_pk_fma_f32 v[68:69],v[68:69],s[54:55],v[14:15] op_sel_hi:[1,1,0]\n"
"v_pk_fma_f32 v[70:71],v[70:71],s[54:55],v[14:15] op_sel_hi:[1,1,0]\n"
CV "v163,v70,v71\n"
CV "v162,v68,v69\n"
CV "v161,v66,v67\n"
CV "v160,v64,v65\n"
"v_pk_fma_f32 v[56:57],v[56:57],s[54:55],v[14:15] op_sel_hi:[1,1,0]\n"
"v_pk_fma_f32 v[58:59],v[58:59],s[54:55],v[14:15] op_sel_hi:[1,1,0]\n"
"v_pk_fma_f32 v[60:61],v[60:61],s[54:55],v[14:15] op_sel_hi:[1,1,0]\n"
"v_pk_fma_f32 v[62:63],v[62:63],s[54:55],v[14:15] op_sel_hi:[1,1,0]\n"
CV "v159,v62,v63\n"
CV "v158,v60,v61\n"
CV "v157,v58,v59\n"
".Lpg15c:\n"
"s_cbranch_execz .Lpg2a\n"
"s_nop 0\n"
CV "v156,v56,v57\n"
"v_pk_mul_f32 v[48:49],v[48:49],s[54:55]\n"
"v_pk_mul_f32 v[50:51],v[50:51],s[54:55]\n"
"v_pk_mul_f32 v[52:53],v[52:53],s[54:55]\n"
"v_pk_mul_f32 v[54:55],v[54:55],s[54:55]\n"
CV "v155,v54,v55\n"
CV "v154,v52,v53\n"
CV "v153,v50,v51\n"
CV "v152,v48,v49\n"
"v_pk_mul_f32 v[40:41],v[40:41],s[54:55]\n"
"v_pk_mul_f32 v[42:43],v[42:43],s[54:55]\n"
"v_pk_mul_f32 v[44:45],v[44:45],s[54:55]\n"
"v_pk_mul_f32 v[46:47],v[46:47],s[54:55]\n"
CV "v151,v46,v47\n"
CV "v150,v44,v45\n"
CV "v149,v42,v43\n"
CV "v148,v40,v41\n"
"v_pk_mul_f32 v[32:33],v[32:33],s[54:55]\n"
"v_pk_mul_f32 v[34:35],v[34:35],s[54:55]\n"
"v_pk_mul_f32 v[36:37],v[36:37],s[54:55]\n"
"v_pk_mul_f32 v[38:39],v[38:39],s[54:55]\n"
CV "v147,v38,v39\n"
CV "v146,v36,v37\n"
CV "v145,v34,v35\n"
CV "v144,v32,v33\n"
"v_pk_mul_f32 v[24:25],v[24:25],s[54:55]\n"
"v_pk_mul_f32 v[26:27],v[26:27],s[54:55]\n"
"v_pk_mul_f32 v[28:29],v[28:29],s[54:55]\n"
"v_pk_mul_f32 v[30:31],v[30:31],s[54:55]\n"
CV "v143,v30,v31\n"
CV "v142,v28,v29\n"
CV "v141,v26,v27\n"
CV "v140,v24,v25\n"
"v_pk_mul_f32 v[16:17],v[16:17],s[54:55]\n"
"v_pk_mul_f32 v[18:19],v[18:19],s[54:55]\n"
"v_pk_mul_f32 v[20:21],v[20:21],s[54:55]\n"
"v_pk_mul_f32 v[22:23],v[22:23],s[54:55]\n"
CV "v139,v22,v23\n"
CV "v138,v20,v21\n"
CV "v137,v18,v19\n"
CV "v136,v16,v17\n"
"s_waitcnt lgkmcnt(0)\n"
"s_branch .Lq0\n"
".Lpgpsl_b:\n"
"s_cbranch_execz .Lpg3a\n"
".Lpsl:\n"
"s_sleep 1\n"
".Lpsub:\n"
"s_memrealtime s[100:101]\n"
"s_waitcnt lgkmcnt(0)\n"
"s_sub_u32 s101,s21,s100\n"
"s_cmp_gt_i32 s101,0\n"
"s_cbranch_scc1 .Lpsl\n"
"s_add_u32 s21,s100,s97\n"
"s_setpc_b64 s[52:53]\n"
".Lstub_6:\n"
"v_cmp_le_f32 vcc,0x47800000,v10\n"
"s_cmp_eq_u32 s77,0\n"
"s_cbranch_scc0 .Lnop_6\n"
"s_call_b64 s[52:53],.Lpsub\n"
".Lnop_6:\n"
"s_cbranch_vccz .Lback_6\n"
"s_call_b64 s[52:53],.Lslow_1\n"
"s_branch .Lback_6\n"
".Lstub_7:\n"
"v_cmp_le_f32 vcc,0x47800000,v10\n"
"s_cmp_eq_u32 s77,0\n"
"s_cbranch_scc0 .Lnop_7\n"
"s_call_b64 s[52:53],.Lpsub\n"
"s_mul_i32 s58,s57,2\n"
"s_add_u32 s58,s58,s61\n"
"s_cmp_eq_u32 s58,2\n"
"s_cselect_b32 s77,0x47800000,s77\n"
"s_cselect_b32 s95,0x47800000,s95\n"
".Lnop_7:\n"
"s_cbranch_vccz .Lback_7\n"
"s_call_b64 s[52:53],.Lslow_0\n"
"s_branch .Lback_7\n"
".Lstub_8:\n"
"s_call_b64 s[52:53],.Lslow_1\n"
"s_branch .Lback_8\n"
".Lstub_9:\n"
"s_call_b64 s[52:53],.Lslow_0\n"
"s_branch .Lback_9\n"
".Lstub_10:\n"
"s_call_b64 s[52:53],.Lslow_1\n"
"s_branch .Lback_10\n"
".Lstub_11:\n"
"s_call_b64 s[52:53],.Lslow_1\n"
"s_branch .Lback_11\n"
".Lstub_12:\n"
"s_call_b64 s[52:53],.Lslow_0\n"
"s_branch .Lback_12\n"
".Lstub_13:\n"
"s_call_b64 s[52:53],.Lslow_1\n"
"s_branch .Lback_13\n"
".Lstub_20:\n"
"v_cmp_le_f32 vcc,0x47800000,v10\n"
"s_cmp_eq_u32 s77,0\n"
"s_cbranch_scc0 .Lnop_20\n"
"s_call_b64 s[52:53],.Lpsub\n"
".Lnop_20:\n"
"s_cbranch_vccz .Lback_20\n"
"s_call_b64 s[52:53],.Lslow_1\n"
"s_branch .Lback_20\n"
".Lstub_21:\n"
"v_cmp_le_f32 vcc,0x47800000,v10\n"
"s_cmp_eq_u32 s77,0\n"
"s_cbranch_scc0 .Lnop_21\n"
"s_call_b64 s[52:53],.Lpsub\n"
".Lnop_21:\n"
"s_cbranch_vccz .Lback_21\n"
"s_call_b64 s[52:53],.Lslow_0\n"
"s_branch .Lback_21\n"
".Lstub_22:\n"
"v_cmp_le_f32 vcc,0x47800000,v10\n"
"s_cmp_eq_u32 s77,0\n"
"s_cbranch_scc0 .Lnop_22\n"
"s_call_b64 s[52:53],.Lpsub\n"
".Lnop_22:\n"
"s_cbranch_vccz .Lback_22\n"
"s_call_b64 s[52:53],.Lslow_1\n"
"s_branch .Lback_22\n"
".Lstub_23:\n"
"v_cmp_le_f32 vcc,0x47800000,v10\n"
"s_cmp_eq_u32 s77,0\n"
"s_cbranch_scc0 .Lnop_23\n"
"s_call_b64 s[52:53],.Lpsub\n"
".Lnop_23:\n"
"s_cbranch_vccz .Lback_23\n"
"s_call_b64 s[52:53],.Lslow_0\n"
"s_branch .Lback_23\n"
".Lstub_24:\n"
"v_cmp_le_f32 vcc,0x47800000,v10\n"
"s_cmp_eq_u32 s61,1\n"
"s_cbranch_scc0 .Lhook_24\n"
"s_mov_b64 exec,0\n"
"s_cmp_eq_u32 s56,0\n"
"s_cbranch_scc1 .Lpg25h\n"
"s_cmp_eq_u32 s56,1\n"
"s_cbranch_scc1 .Lpg24h\n"
"s_cmp_eq_u32 s56,2\n"
"s_cbranch_scc1 .Lpg23h\n"
"s_branch .Lpg22h\n"
".Lisr4:\n"
"s_mov_b64 exec,-1\n"
".Lisx4:\n"
"s_and_b32 s58,s56,1\n"
"s_mul_i32 s79,s58,12288\n"
"s_lshl_b32 s15,s58,10\n"
"s_lshr_b32 s58,s56,1\n"
"s_mul_i32 s5,s58,6144\n"
"s_add_u32 s5,s5,s79\n"
"s_lshl_b32 s59,s58,8\n"
"s_add_u32 s15,s15,s59\n"
"s_add_u32 s15,s15,159744\n"
"s_add_u32 s88,s88,1024\n"
"s_add_u32 s89,s89,1024\n"
"s_mul_i32 s58,s58,61440\n"
"s_add_u32 s2,s89,s58\n"
"s_add_u32 s3,s88,s58\n"
"s_add_u32 s0,s2,122880\n"
"s_add_u32 s1,s3,122880\n"
"s_lshl_b32 s58,s56,10\n"
"s_add_u32 s9,s58,73728\n"
"s_add_u32 s14,s58,159744\n"
"s_bfe_u32 s4,s22,0x10014\n"
"s_lshl_b32 s4,s4,5\n"
"s_mov_b32 s6,896\n"
"s_and_b32 s58,s56,2\n"
"s_lshl_b32 s58,s58,5\n"
"s_or_b32 s4,s4,s58\n"
".Lhook_24:\n"
"s_cmp_eq_u32 s77,0\n"
"s_cbranch_scc0 .Lnop_24\n"
"s_call_b64 s[52:53],.Lpsub\n"
".Lnop_24:\n"
"s_cbranch_vccz .Lback_24\n"
"s_call_b64 s[52:53],.Lslow_1\n"
"s_branch .Lback_24\n"
".Lstub_25:\n"
"v_cmp_le_f32 vcc,0x47800000,v10\n"
"s_cmp_eq_u32 s77,0\n"
"s_cbranch_scc0 .Lnop_25\n"
"s_call_b64 s[52:53],.Lpsub\n"
".Lnop_25:\n"
"s_cbranch_vccz .Lback_25\n"
"s_call_b64 s[52:53],.Lslow_0\n"
"s_branch .Lback_25\n"
".Lstub_26:\n"
"s_call_b64 s[52:53],.Lslow_1\n"
"s_branch .Lback_26\n"
".Lstub_27:\n"
"s_call_b64 s[52:53],.Lslow_0\n"
"s_branch .Lback_27\n"
".Lstub_28:\n"
"s_call_b64 s[52:53],.Lslow_1\n"
"s_branch .Lback_28\n"
".Lackspin_q:\n"
"s_cmp_eq_u32 s98,0\n"
"s_cbranch_scc1 .Lackok_q\n"
"s_sub_u32 s98,s98,1\n"
"s_sleep 8\n"
"v_bfe_u32 v14,v9,4,5\n"
"v_lshlrev_b32 v14,6,v14\n"
"s_lshl_b32 s58,s78,2\n"
"s_add_u32 s58,s58,s86\n"
"buffer_load_dword v10,v14,s[40:43],s58 offen offset:128 sc1\n"
"s_waitcnt vmcnt(0)\n"
"s_branch .Lack_q\n"
".Lpgep_g:\n"
"s_cbranch_execz .Lisr6\n"
".Lep:\n"
MF "v[72:75],v[16:19],v[136:139],v[32:35]\n"
"s_nop 0\n"
"ds_read_b128 v[24:27],v0 offset:1024\n"
"ds_read_b128 v[28:31],v1 offset:1024\n"
MF "v[72:75],v[20:23],v[156:159],v[72:75]\n"
MF "v[76:79],v[20:23],v[160:163],v[36:39]\n"
MF "v[76:79],v[16:19],v[140:143],v[76:79]\n"
MF "v[80:83],v[16:19],v[144:147],v[40:43]\n"
MF "v[80:83],v[20:23],v[164:167],v[80:83]\n"
MF "v[84:87],v[20:23],v[168:171],v[44:47]\n"
MF "v[84:87],v[16:19],v[148:151],v[84:87]\n"
"s_add_u32 m0,s65,49152\n"
MF "v[88:91],v[16:19],v[152:155],v[48:51]\n"
LD "v8,s[36:39],s73 offen offset:3072 nt lds\n"
MF "v[88:91],v[20:23],v[172:175],v[88:91]\n"
"s_waitcnt lgkmcnt(0)\n"
"ds_read_b128 v[16:19],v0 offset:2048\n"
"ds_read_b128 v[20:23],v1 offset:2048\n"
MF "v[88:91],v[24:27],v[192:195],v[88:91]\n"
"v_exp_f32 v124,v124\n"
MF "v[88:91],v[28:31],v[212:215],v[88:91]\n"
"s_add_u32 m0,s65,50048\n"
"v_exp_f32 v125,v125\n"
MF "v[84:87],v[28:31],v[208:211],v[84:87]\n"
LD "v8,s[36:39],s73 offen offset:3200 nt lds\n"
MF "v[84:87],v[24:27],v[188:191],v[84:87]\n"
"v_exp_f32 v126,v126\n"
MF "v[80:83],v[24:27],v[184:187],v[80:83]\n"
"v_exp_f32 v127,v127\n"
MF "v[80:83],v[28:31],v[204:207],v[80:83]\n"
"s_add_u32 m0,s65,50944\n"
"v_add_f32 v60,v60,v124\n"
"v_add_f32 v60,v60,v125\n"
MF "v[76:79],v[28:31],v[200:203],v[76:79]\n"
LD "v8,s[36:39],s73 offen offset:3328 nt lds\n"
MF "v[76:79],v[24:27],v[180:183],v[76:79]\n"
CV "v106,v124,v125\n"
"v_add_f32 v60,v60,v126\n"
MF "v[72:75],v[24:27],v[176:179],v[72:75]\n"
"v_add_f32 v60,v60,v127\n"
CV "v107,v126,v127\n"
MF "v[72:75],v[28:31],v[196:199],v[72:75]\n"
"s_waitcnt lgkmcnt(0)\n"
"ds_read_b128 v[24:27],v0 offset:6144\n"
"ds_read_b128 v[28:31],v1 offset:6144\n"
MF "v[72:75],v[16:19],v[216:219],v[72:75]\n"
"v_exp_f32 v128,v128\n"
MF "v[72:75],v[20:23],v[236:239],v[72:75]\n"
"v_exp_f32 v129,v129\n"
MF "v[76:79],v[20:23],v[240:243],v[76:79]\n"
"v_exp_f32 v130,v130\n"
MF "v[76:79],v[16:19],v[220:223],v[76:79]\n"
"v_exp_f32 v131,v131\n"
MF "v[80:83],v[16:19],v[224:227],v[80:83]\n"
"v_add_f32 v61,v61,v128\n"
"v_add_f32 v61,v61,v129\n"
MF "v[80:83],v[20:23],v[244:247],v[80:83]\n"
CV "v110,v128,v129\n"
"v_add_f32 v61,v61,v130\n"
MF "v[84:87],v[20:23],v[248:251],v[84:87]\n"
"v_add_f32 v61,v61,v131\n"
CV "v111,v130,v131\n"
MF "v[84:87],v[16:19],v[228:231],v[84:87]\n"
"v_max3_f32 v10,v57,v58,v59\n"
"v_max3_f32 v10,v10,v60,v61\n"
MF "v[88:91],v[16:19],v[232:235],v[88:91]\n"
"v_cmp_le_f32 vcc,0x47800000,v10\n"
MF "v[88:91],v[20:23],v[252:255],v[88:91]\n"
"s_waitcnt lgkmcnt(0)\n"
"s_cbranch_vccnz .Lstub_11\n"
".Lback_11:\n"
MF "v[112:115],v[24:27],v[136:139],v[32:35]\n"
"s_add_u32 s74,s74,12288\n"
"ds_read_b128 v[16:19],v0 offset:7168\n"
"ds_read_b128 v[20:23],v1 offset:7168\n"
MF "v[112:115],v[28:31],v[156:159],v[112:115]\n"
"s_cmp_eq_u32 s74,s20\n"
"s_cselect_b32 s74,s70,s74\n"
MF "v[116:119],v[28:31],v[160:163],v[36:39]\n"
"v_exp_f32 v72,v72\n"
MF "v[116:119],v[24:27],v[140:143],v[116:119]\n"
"v_exp_f32 v73,v73\n"
MF "v[120:123],v[24:27],v[144:147],v[40:43]\n"
"v_exp_f32 v74,v74\n"
MF "v[120:123],v[28:31],v[164:167],v[120:123]\n"
"v_exp_f32 v75,v75\n"
MF "v[124:127],v[28:31],v[168:171],v[44:47]\n"
"v_add_f32 v52,v57,v72\n"
"v_add_f32 v52,v52,v73\n"
MF "v[124:127],v[24:27],v[148:151],v[124:127]\n"
CV "v72,v72,v73\n"
"v_add_f32 v52,v52,v74\n"
MF "v[128:131],v[24:27],v[152:155],v[48:51]\n"
"v_add_f32 v52,v52,v75\n"
CV "v73,v74,v75\n"
MF "v[128:131],v[28:31],v[172:175],v[128:131]\n"
"s_waitcnt lgkmcnt(0)\n"
"ds_read_b128 v[24:27],v0 offset:8192\n"
"ds_read_b128 v[28:31],v1 offset:8192\n"
MF "v[128:131],v[16:19],v[192:195],v[128:131]\n"
"v_exp_f32 v76,v76\n"
MF "v[128:131],v[20:23],v[212:215],v[128:131]\n"
"v_exp_f32 v77,v77\n"
MF "v[124:127],v[20:23],v[208:211],v[124:127]\n"
"v_exp_f32 v78,v78\n"
MF "v[124:127],v[16:19],v[188:191],v[124:127]\n"
"v_exp_f32 v79,v79\n"
MF "v[120:123],v[16:19],v[184:187],v[120:123]\n"
"v_add_f32 v53,v58,v76\n"
"v_add_f32 v53,v53,v77\n"
MF "v[120:123],v[20:23],v[204:207],v[120:123]\n"
CV "v76,v76,v77\n"
"v_add_f32 v53,v53,v78\n"
MF "v[116:119],v[20:23],v[200:203],v[116:119]\n"
"s_waitcnt vmcnt(12) lgkmcnt(0)\n"
"v_add_f32 v53,v53,v79\n"
CV "v77,v78,v79\n"
MF "v[116:119],v[16:19],v[180:183],v[116:119]\n"
"s_barrier\n"
MF "v[112:115],v[16:19],v[176:179],v[112:115]\n"
"v_exp_f32 v80,v80\n"
MF "v[112:115],v[20:23],v[196:199],v[112:115]\n"
"s_sub_u32 s58,s71,s70\n"
TR "v[16:17],v2 offset:24576\n"
TR "v[18:19],v2 offset:30720\n"
MF "v[112:115],v[24:27],v[216:219],v[112:115]\n"
"s_add_u32 s72,s72,s58\n"
TR "v[20:21],v3 offset:24576\n"
TR "v[22:23],v3 offset:30720\n"
MF "v[112:115],v[28:31],v[236:239],v[112:115]\n"
"s_add_u32 s16,s16,s58\n"
"s_add_u32 s17,s17,s58\n"
"s_add_u32 s72,s72,6144\n"
MF "v[116:119],v[28:31],v[240:243],v[116:119]\n"
"s_cmp_eq_u32 s72,s17\n"
"s_cselect_b32 s72,s16,s72\n"
"s_add_u32 m0,s64,0\n"
MF "v[116:119],v[24:27],v[220:223],v[116:119]\n"
LD "v7,s[32:35],s72 offen nt lds\n"
MF "v[120:123],v[24:27],v[224:227],v[120:123]\n"
"s_add_u32 m0,s64,896\n"
"v_exp_f32 v81,v81\n"
MF "v[120:123],v[28:31],v[244:247],v[120:123]\n"
LD "v7,s[32:35],s72 offen offset:128 nt lds\n"
MF "v[124:127],v[28:31],v[248:251],v[124:127]\n"
"s_add_u32 m0,s64,1792\n"
"v_exp_f32 v82,v82\n"
MF "v[124:127],v[24:27],v[228:231],v[124:127]\n"
LD "v7,s[32:35],s72 offen offset:256 nt lds\n"
MF "v[128:131],v[24:27],v[232:235],v[128:131]\n"
"s_add_u32 s72,s72,6144\n"
"v_exp_f32 v83,v83\n"
MF "v[128:131],v[28:31],v[252:255],v[128:131]\n"
"s_waitcnt lgkmcnt(0)\n"
TR "v[24:25],v4 offset:24576\n"
TR "v[26:27],v4 offset:30720\n"
MF "a[0:3],v[16:19],v[92:95],a[0:3]\n"
"s_add_u32 m0,s64,6144\n"
TR "v[28:29],v5 offset:24576\n"
TR "v[30:31],v5 offset:30720\n"
MF "a[20:23],v[20:23],v[92:95],a[20:23]\n"
LD "v7,s[32:35],s72 offen nt lds\n"
MF "a[24:27],v[20:23],v[96:99],a[24:27]\n"
"v_add_f32 v54,v59,v80\n"
"v_add_f32 v54,v54,v81\n"
MF "a[4:7],v[16:19],v[96:99],a[4:7]\n"
"s_add_u32 m0,s64,7040\n"
CV "v80,v80,v81\n"
"v_add_f32 v54,v54,v82\n"
MF "a[8:11],v[16:19],v[100:103],a[8:11]\n"
LD "v7,s[32:35],s72 offen offset:128 nt lds\n"
MF "a[28:31],v[20:23],v[100:103],a[28:31]\n"
"v_add_f32 v54,v54,v83\n"
CV "v81,v82,v83\n"
MF "a[32:35],v[20:23],v[104:107],a[32:35]\n"
"s_add_u32 m0,s64,7936\n"
"v_exp_f32 v84,v84\n"
MF "a[12:15],v[16:19],v[104:107],a[12:15]\n"
LD "v7,s[32:35],s72 offen offset:256 nt lds\n"
MF "a[16:19],v[16:19],v[108:111],a[16:19]\n"
"v_exp_f32 v85,v85\n"
MF "a[36:39],v[20:23],v[108:111],a[36:39]\n"
"s_waitcnt lgkmcnt(0)\n"
TR "v[16:17],v2 offset:25600\n"
TR "v[18:19],v2 offset:31744\n"
MF "a[56:59],v[24:27],v[108:111],a[56:59]\n"
"s_nop 0\n"
TR "v[20:21],v3 offset:25600\n"
TR "v[22:23],v3 offset:31744\n"
MF "a[76:79],v[28:31],v[108:111],a[76:79]\n"
"v_exp_f32 v86,v86\n"
MF "a[72:75],v[28:31],v[104:107],a[72:75]\n"
"v_exp_f32 v87,v87\n"
MF "a[52:55],v[24:27],v[104:107],a[52:55]\n"
"v_add_f32 v55,v60,v84\n"
"v_add_f32 v55,v55,v85\n"
MF "a[48:51],v[24:27],v[100:103],a[48:51]\n"
CV "v84,v84,v85\n"
"v_add_f32 v55,v55,v86\n"
MF "a[68:71],v[28:31],v[100:103],a[68:71]\n"
"v_add_f32 v55,v55,v87\n"
CV "v85,v86,v87\n"
MF "a[64:67],v[28:31],v[96:99],a[64:67]\n"
"v_exp_f32 v88,v88\n"
MF "a[44:47],v[24:27],v[96:99],a[44:47]\n"
"v_exp_f32 v89,v89\n"
MF "a[40:43],v[24:27],v[92:95],a[40:43]\n"
"v_exp_f32 v90,v90\n"
MF "a[60:63],v[28:31],v[92:95],a[60:63]\n"
"s_waitcnt lgkmcnt(0)\n"
TR "v[24:25],v4 offset:25600\n"
TR "v[26:27],v4 offset:31744\n"
MF "a[80:83],v[16:19],v[92:95],a[80:83]\n"
"s_nop 0\n"
TR "v[28:29],v5 offset:25600\n"
TR "v[30:31],v5 offset:31744\n"
MF "a[100:103],v[20:23],v[92:95],a[100:103]\n"
"v_exp_f32 v91,v91\n"
MF "a[104:107],v[20:23],v[96:99],a[104:107]\n"
"v_add_f32 v56,v61,v88\n"
"v_add_f32 v56,v56,v89\n"
MF "a[84:87],v[16:19],v[96:99],a[84:87]\n"
CV "v88,v88,v89\n"
"v_add_f32 v56,v56,v90\n"
MF "a[88:91],v[16:19],v[100:103],a[88:91]\n"
"v_add_f32 v56,v56,v91\n"
CV "v89,v90,v91\n"
MF "a[108:111],v[20:23],v[100:103],a[108:111]\n"
"v_exp_f32 v112,v112\n"
MF "a[112:115],v[20:23],v[104:107],a[112:115]\n"
"v_exp_f32 v113,v113\n"
MF "a[92:95],v[16:19],v[104:107],a[92:95]\n"
"v_exp_f32 v114,v114\n"
MF "a[96:99],v[16:19],v[108:111],a[96:99]\n"
"v_exp_f32 v115,v115\n"
MF "a[116:119],v[20:23],v[108:111],a[116:119]\n"
"s_waitcnt lgkmcnt(0)\n"
TR "v[16:17],v2 offset:26624\n"
TR "v[18:19],v2 offset:32768\n"
MF "a[136:139],v[24:27],v[108:111],a[136:139]\n"
"s_nop 0\n"
TR "v[20:21],v3 offset:26624\n"
TR "v[22:23],v3 offset:32768\n"
MF "a[156:159],v[28:31],v[108:111],a[156:159]\n"
"v_add_f32 v52,v52,v112\n"
"v_add_f32 v52,v52,v113\n"
MF "a[152:155],v[28:31],v[104:107],a[152:155]\n"
CV "v74,v112,v113\n"
"v_add_f32 v52,v52,v114\n"
MF "a[132:135],v[24:27],v[104:107],a[132:135]\n"
"v_add_f32 v52,v52,v115\n"
CV "v75,v114,v115\n"
MF "a[128:131],v[24:27],v[100:103],a[128:131]\n"
"v_exp_f32 v116,v116\n"
MF "a[148:151],v[28:31],v[100:103],a[148:151]\n"
"v_exp_f32 v117,v117\n"
MF "a[144:147],v[28:31],v[96:99],a[144:147]\n"
"v_exp_f32 v118,v118\n"
MF "a[124:127],v[24:27],v[96:99],a[124:127]\n"
"v_exp_f32 v119,v119\n"
MF "a[120:123],v[24:27],v[92:95],a[120:123]\n"
"v_add_f32 v53,v53,v116\n"
"v_add_f32 v53,v53,v117\n"
MF "a[140:143],v[28:31],v[92:95],a[140:143]\n"
"s_waitcnt lgkmcnt(0)\n"
TR "v[24:25],v4 offset:26624\n"
TR "v[26:27],v4 offset:32768\n"
MF "a[160:163],v[16:19],v[92:95],a[160:163]\n"
"s_nop 0\n"
TR "v[28:29],v5 offset:26624\n"
TR "v[30:31],v5 offset:32768\n"
MF "a[180:183],v[20:23],v[92:95],a[180:183]\n"
CV "v78,v116,v117\n"
"v_add_f32 v53,v53,v118\n"
MF "a[184:187],v[20:23],v[96:99],a[184:187]\n"
"v_add_f32 v53,v53,v119\n"
CV "v79,v118,v119\n"
MF "a[164:167],v[16:19],v[96:99],a[164:167]\n"
"v_exp_f32 v120,v120\n"
MF "a[168:171],v[16:19],v[100:103],a[168:171]\n"
"v_exp_f32 v121,v121\n"
MF "a[188:191],v[20:23],v[100:103],a[188:191]\n"
"v_exp_f32 v122,v122\n"
MF "a[192:195],v[20:23],v[104:107],a[192:195]\n"
"v_exp_f32 v123,v123\n"
MF "a[172:175],v[16:19],v[104:107],a[172:175]\n"
"s_waitcnt vmcnt(12) lgkmcnt(0)\n"
"v_add_f32 v54,v54,v120\n"
"v_add_f32 v54,v54,v121\n"
MF "a[176:179],v[16:19],v[108:111],a[176:179]\n"
"s_barrier\n"
MF "a[196:199],v[20:23],v[108:111],a[196:199]\n"
"s_add_u32 s73,s73,12288\n"
"ds_read_b128 v[16:19],v0 offset:24576\n"
"ds_read_b128 v[20:23],v1 offset:24576\n"
MF "a[216:219],v[24:27],v[108:111],a[216:219]\n"
"s_cmp_eq_u32 s73,s19\n"
"s_cselect_b32 s73,s18,s73\n"
"s_add_u32 m0,s65,73728\n"
MF "a[236:239],v[28:31],v[108:111],a[236:239]\n"
LD "v8,s[36:39],s73 offen nt lds\n"
MF "a[232:235],v[28:31],v[104:107],a[232:235]\n"
"s_add_u32 m0,s65,74624\n"
CV "v82,v120,v121\n"
"v_add_f32 v54,v54,v122\n"
MF "a[212:215],v[24:27],v[104:107],a[212:215]\n"
LD "v8,s[36:39],s73 offen offset:128 nt lds\n"
MF "a[208:211],v[24:27],v[100:103],a[208:211]\n"
"s_add_u32 m0,s65,75520\n"
"v_add_f32 v54,v54,v123\n"
CV "v83,v122,v123\n"
MF "a[228:231],v[28:31],v[100:103],a[228:231]\n"
LD "v8,s[36:39],s73 offen offset:256 nt lds\n"
MF "a[224:227],v[28:31],v[96:99],a[224:227]\n"
MF "a[204:207],v[24:27],v[96:99],a[204:207]\n"
MF "a[200:203],v[24:27],v[92:95],a[200:203]\n"
MF "a[220:223],v[28:31],v[92:95],a[220:223]\n"
"s_waitcnt lgkmcnt(0)\n"
MF "v[92:95],v[16:19],v[136:139],v[32:35]\n"
"s_add_u32 m0,s65,73728\n"
"ds_read_b128 v[24:27],v0 offset:25600\n"
"ds_read_b128 v[28:31],v1 offset:25600\n"
MF "v[92:95],v[20:23],v[156:159],v[92:95]\n"
LD "v8,s[36:39],s73 offen offset:3072 nt lds\n"
MF "v[96:99],v[20:23],v[160:163],v[36:39]\n"
"v_exp_f32 v124,v124\n"
MF "v[96:99],v[16:19],v[140:143],v[96:99]\n"
"v_exp_f32 v125,v125\n"
MF "v[100:103],v[16:19],v[144:147],v[40:43]\n"
"s_add_u32 m0,s65,74624\n"
"v_exp_f32 v126,v126\n"
MF "v[100:103],v[20:23],v[164:167],v[100:103]\n"
LD "v8,s[36:39],s73 offen offset:3200 nt lds\n"
MF "v[104:107],v[20:23],v[168:171],v[44:47]\n"
"v_exp_f32 v127,v127\n"
MF "v[104:107],v[16:19],v[148:151],v[104:107]\n"
"s_add_u32 m0,s65,75520\n"
"v_add_f32 v55,v55,v124\n"
"v_add_f32 v55,v55,v125\n"
MF "v[108:111],v[16:19],v[152:155],v[48:51]\n"
LD "v8,s[36:39],s73 offen offset:3328 nt lds\n"
MF "v[108:111],v[20:23],v[172:175],v[108:111]\n"
"s_waitcnt lgkmcnt(0)\n"
"ds_read_b128 v[16:19],v0 offset:26624\n"
"ds_read_b128 v[20:23],v1 offset:26624\n"
MF "v[108:111],v[24:27],v[192:195],v[108:111]\n"
CV "v86,v124,v125\n"
"v_add_f32 v55,v55,v126\n"
MF "v[108:111],v[28:31],v[212:215],v[108:111]\n"
"v_add_f32 v55,v55,v127\n"
CV "v87,v126,v127\n"
MF "v[104:107],v[28:31],v[208:211],v[104:107]\n"
"v_exp_f32 v128,v128\n"
MF "v[104:107],v[24:27],v[188:191],v[104:107]\n"
"v_exp_f32 v129,v129\n"
MF "v[100:103],v[24:27],v[184:187],v[100:103]\n"
"v_exp_f32 v130,v130\n"
MF "v[100:103],v[28:31],v[204:207],v[100:103]\n"
"v_exp_f32 v131,v131\n"
MF "v[96:99],v[28:31],v[200:203],v[96:99]\n"
"v_add_f32 v56,v56,v128\n"
"v_add_f32 v56,v56,v129\n"
MF "v[96:99],v[24:27],v[180:183],v[96:99]\n"
CV "v90,v128,v129\n"
"v_add_f32 v56,v56,v130\n"
MF "v[92:95],v[24:27],v[176:179],v[92:95]\n"
"v_add_f32 v56,v56,v131\n"
CV "v91,v130,v131\n"
MF "v[92:95],v[28:31],v[196:199],v[92:95]\n"
"s_waitcnt lgkmcnt(0)\n"
"ds_read_b128 v[24:27],v0 offset:30720\n"
"ds_read_b128 v[28:31],v1 offset:30720\n"
MF "v[92:95],v[16:19],v[216:219],v[92:95]\n"
MF "v[92:95],v[20:23],v[236:239],v[92:95]\n"
MF "v[96:99],v[20:23],v[240:243],v[96:99]\n"
MF "v[96:99],v[16:19],v[220:223],v[96:99]\n"
MF "v[100:103],v[16:19],v[224:227],v[100:103]\n"
MF "v[100:103],v[20:23],v[244:247],v[100:103]\n"
MF "v[104:107],v[20:23],v[248:251],v[104:107]\n"
MF "v[104:107],v[16:19],v[228:231],v[104:107]\n"
"v_max3_f32 v10,v52,v53,v54\n"
"v_max3_f32 v10,v10,v55,v56\n"
MF "v[108:111],v[16:19],v[232:235],v[108:111]\n"
"v_cmp_le_f32 vcc,0x47800000,v10\n"
MF "v[108:111],v[20:23],v[252:255],v[108:111]\n"
"s_waitcnt lgkmcnt(0)\n"
"s_cbranch_vccnz .Lstub_12\n"
".Lback_12:\n"
MF "v[112:115],v[24:27],v[136:139],v[32:35]\n"
"s_add_u32 s74,s74,12288\n"
"ds_read_b128 v[16:19],v0 offset:31744\n"
"ds_read_b128 v[20:23],v1 offset:31744\n"
MF "v[112:115],v[28:31],v[156:159],v[112:115]\n"
"s_cmp_eq_u32 s74,s20\n"
"s_cselect_b32 s74,s70,s74\n"
MF "v[116:119],v[28:31],v[160:163],v[36:39]\n"
"v_exp_f32 v92,v92\n"
MF "v[116:119],v[24:27],v[140:143],v[116:119]\n"
"v_exp_f32 v93,v93\n"
MF "v[120:123],v[24:27],v[144:147],v[40:43]\n"
"v_exp_f32 v94,v94\n"
MF "v[120:123],v[28:31],v[164:167],v[120:123]\n"
"v_exp_f32 v95,v95\n"
MF "v[124:127],v[28:31],v[168:171],v[44:47]\n"
"v_add_f32 v57,v52,v92\n"
"v_add_f32 v57,v57,v93\n"
MF "v[124:127],v[24:27],v[148:151],v[124:127]\n"
CV "v92,v92,v93\n"
"v_add_f32 v57,v57,v94\n"
MF "v[128:131],v[24:27],v[152:155],v[48:51]\n"
"v_add_f32 v57,v57,v95\n"
CV "v93,v94,v95\n"
MF "v[128:131],v[28:31],v[172:175],v[128:131]\n"
"s_waitcnt lgkmcnt(0)\n"
"ds_read_b128 v[24:27],v0 offset:32768\n"
"ds_read_b128 v[28:31],v1 offset:32768\n"
MF "v[128:131],v[16:19],v[192:195],v[128:131]\n"
"v_exp_f32 v96,v96\n"
MF "v[128:131],v[20:23],v[212:215],v[128:131]\n"
"v_exp_f32 v97,v97\n"
MF "v[124:127],v[20:23],v[208:211],v[124:127]\n"
"v_exp_f32 v98,v98\n"
MF "v[124:127],v[16:19],v[188:191],v[124:127]\n"
"v_exp_f32 v99,v99\n"
MF "v[120:123],v[16:19],v[184:187],v[120:123]\n"
"v_add_f32 v58,v53,v96\n"
"v_add_f32 v58,v58,v97\n"
MF "v[120:123],v[20:23],v[204:207],v[120:123]\n"
CV "v96,v96,v97\n"
"v_add_f32 v58,v58,v98\n"
MF "v[116:119],v[20:23],v[200:203],v[116:119]\n"
"s_waitcnt vmcnt(12) lgkmcnt(0)\n"
"v_add_f32 v58,v58,v99\n"
CV "v97,v98,v99\n"
MF "v[116:119],v[16:19],v[180:183],v[116:119]\n"
"s_barrier\n"
MF "v[112:115],v[16:19],v[176:179],v[112:115]\n"
"v_exp_f32 v100,v100\n"
MF "v[112:115],v[20:23],v[196:199],v[112:115]\n"
"s_nop 0\n"
TR "v[16:17],v2\n"
TR "v[18:19],v2 offset:6144\n"
MF "v[112:115],v[24:27],v[216:219],v[112:115]\n"
"s_add_u32 s72,s72,6144\n"
TR "v[20:21],v3\n"
TR "v[22:23],v3 offset:6144\n"
MF "v[112:115],v[28:31],v[236:239],v[112:115]\n"
"s_cmp_eq_u32 s72,s17\n"
"s_cselect_b32 s72,s16,s72\n"
"s_add_u32 m0,s64,24576\n"
MF "v[116:119],v[28:31],v[240:243],v[116:119]\n"
LD "v7,s[32:35],s72 offen nt lds\n"
MF "v[116:119],v[24:27],v[220:223],v[116:119]\n"
"s_add_u32 m0,s64,25472\n"
"v_exp_f32 v101,v101\n"
MF "v[120:123],v[24:27],v[224:227],v[120:123]\n"
LD "v7,s[32:35],s72 offen offset:128 nt lds\n"
MF "v[120:123],v[28:31],v[244:247],v[120:123]\n"
"v_exp_f32 v102,v102\n"
MF "v[124:127],v[28:31],v[248:251],v[124:127]\n"
"s_add_u32 m0,s64,26368\n"
"v_exp_f32 v103,v103\n"
MF "v[124:127],v[24:27],v[228:231],v[124:127]\n"
LD "v7,s[32:35],s72 offen offset:256 nt lds\n"
MF "v[128:131],v[24:27],v[232:235],v[128:131]\n"
"s_add_u32 s72,s72,6144\n"
"v_add_f32 v59,v54,v100\n"
"v_add_f32 v59,v59,v101\n"
MF "v[128:131],v[28:31],v[252:255],v[128:131]\n"
"s_waitcnt lgkmcnt(0)\n"
TR "v[24:25],v4\n"
TR "v[26:27],v4 offset:6144\n"
MF "a[0:3],v[16:19],v[72:75],a[0:3]\n"
".Lpg16g:\n"
"s_cbranch_execz .Lisr6\n"
"s_add_u32 m0,s64,30720\n"
TR "v[28:29],v5\n"
TR "v[30:31],v5 offset:6144\n"
MF "a[20:23],v[20:23],v[72:75],a[20:23]\n"
LD "v7,s[32:35],s72 offen nt lds\n"
MF "a[24:27],v[20:23],v[76:79],a[24:27]\n"
CV "v100,v100,v101\n"
"v_add_f32 v59,v59,v102\n"
MF "a[4:7],v[16:19],v[76:79],a[4:7]\n"
"s_add_u32 m0,s64,31616\n"
"v_add_f32 v59,v59,v103\n"
CV "v101,v102,v103\n"
MF "a[8:11],v[16:19],v[80:83],a[8:11]\n"
LD "v7,s[32:35],s72 offen offset:128 nt lds\n"
MF "a[28:31],v[20:23],v[80:83],a[28:31]\n"
"v_exp_f32 v104,v104\n"
MF "a[32:35],v[20:23],v[84:87],a[32:35]\n"
"s_add_u32 m0,s64,32512\n"
"v_exp_f32 v105,v105\n"
MF "a[12:15],v[16:19],v[84:87],a[12:15]\n"
LD "v7,s[32:35],s72 offen offset:256 nt lds\n"
MF "a[16:19],v[16:19],v[88:91],a[16:19]\n"
"v_exp_f32 v106,v106\n"
MF "a[36:39],v[20:23],v[88:91],a[36:39]\n"
"s_waitcnt lgkmcnt(0)\n"
TR "v[16:17],v2 offset:1024\n"
TR "v[18:19],v2 offset:7168\n"
MF "a[56:59],v[24:27],v[88:91],a[56:59]\n"
"s_nop 0\n"
TR "v[20:21],v3 offset:1024\n"
TR "v[22:23],v3 offset:7168\n"
MF "a[76:79],v[28:31],v[88:91],a[76:79]\n"
"v_exp_f32 v107,v107\n"
MF "a[72:75],v[28:31],v[84:87],a[72:75]\n"
"v_add_f32 v60,v55,v104\n"
"v_add_f32 v60,v60,v105\n"
MF "a[52:55],v[24:27],v[84:87],a[52:55]\n"
CV "v104,v104,v105\n"
"v_add_f32 v60,v60,v106\n"
MF "a[48:51],v[24:27],v[80:83],a[48:51]\n"
"v_add_f32 v60,v60,v107\n"
CV "v105,v106,v107\n"
MF "a[68:71],v[28:31],v[80:83],a[68:71]\n"
"v_exp_f32 v108,v108\n"
MF "a[64:67],v[28:31],v[76:79],a[64:67]\n"
"v_exp_f32 v109,v109\n"
MF "a[44:47],v[24:27],v[76:79],a[44:47]\n"
"v_exp_f32 v110,v110\n"
MF "a[40:43],v[24:27],v[72:75],a[40:43]\n"
"v_exp_f32 v111,v111\n"
MF "a[60:63],v[28:31],v[72:75],a[60:63]\n"
"s_waitcnt lgkmcnt(0)\n"
TR "v[24:25],v4 offset:1024\n"
TR "v[26:27],v4 offset:7168\n"
MF "a[80:83],v[16:19],v[72:75],a[80:83]\n"
"s_nop 0\n"
TR "v[28:29],v5 offset:1024\n"
TR "v[30:31],v5 offset:7168\n"
MF "a[100:103],v[20:23],v[72:75],a[100:103]\n"
"v_add_f32 v61,v56,v108\n"
"v_add_f32 v61,v61,v109\n"
MF "a[104:107],v[20:23],v[76:79],a[104:107]\n"
CV "v108,v108,v109\n"
"v_add_f32 v61,v61,v110\n"
MF "a[84:87],v[16:19],v[76:79],a[84:87]\n"
"v_add_f32 v61,v61,v111\n"
CV "v109,v110,v111\n"
MF "a[88:91],v[16:19],v[80:83],a[88:91]\n"
"v_exp_f32 v112,v112\n"
MF "a[108:111],v[20:23],v[80:83],a[108:111]\n"
"v_exp_f32 v113,v113\n"
MF "a[112:115],v[20:23],v[84:87],a[112:115]\n"
"v_exp_f32 v114,v114\n"
MF "a[92:95],v[16:19],v[84:87],a[92:95]\n"
"v_exp_f32 v115,v115\n"
MF "a[96:99],v[16:19],v[88:91],a[96:99]\n"
"v_add_f32 v57,v57,v112\n"
"v_add_f32 v57,v57,v113\n"
MF "a[116:119],v[20:23],v[88:91],a[116:119]\n"
"s_waitcnt lgkmcnt(0)\n"
TR "v[16:17],v2 offset:2048\n"
TR "v[18:19],v2 offset:8192\n"
MF "a[136:139],v[24:27],v[88:91],a[136:139]\n"
"s_nop 0\n"
TR "v[20:21],v3 offset:2048\n"
TR "v[22:23],v3 offset:8192\n"
MF "a[156:159],v[28:31],v[88:91],a[156:159]\n"
CV "v94,v112,v113\n"
"v_add_f32 v57,v57,v114\n"
MF "a[152:155],v[28:31],v[84:87],a[152:155]\n"
"v_add_f32 v57,v57,v115\n"
CV "v95,v114,v115\n"
MF "a[132:135],v[24:27],v[84:87],a[132:135]\n"
"v_exp_f32 v116,v116\n"
MF "a[128:131],v[24:27],v[80:83],a[128:131]\n"
"v_exp_f32 v117,v117\n"
MF "a[148:151],v[28:31],v[80:83],a[148:151]\n"
"v_exp_f32 v118,v118\n"
MF "a[144:147],v[28:31],v[76:79],a[144:147]\n"
"v_exp_f32 v119,v119\n"
MF "a[124:127],v[24:27],v[76:79],a[124:127]\n"
"v_add_f32 v58,v58,v116\n"
"v_add_f32 v58,v58,v117\n"
MF "a[120:123],v[24:27],v[72:75],a[120:123]\n"
CV "v98,v116,v117\n"
"v_add_f32 v58,v58,v118\n"
MF "a[140:143],v[28:31],v[72:75],a[140:143]\n"
"s_waitcnt lgkmcnt(0)\n"
TR "v[24:25],v4 offset:2048\n"
TR "v[26:27],v4 offset:8192\n"
MF "a[160:163],v[16:19],v[72:75],a[160:163]\n"
"s_nop 0\n"
TR "v[28:29],v5 offset:2048\n"
TR "v[30:31],v5 offset:8192\n"
MF "a[180:183],v[20:23],v[72:75],a[180:183]\n"
"v_add_f32 v58,v58,v119\n"
CV "v99,v118,v119\n"
MF "a[184:187],v[20:23],v[76:79],a[184:187]\n"
"v_exp_f32 v120,v120\n"
MF "a[164:167],v[16:19],v[76:79],a[164:167]\n"
"v_exp_f32 v121,v121\n"
MF "a[168:171],v[16:19],v[80:83],a[168:171]\n"
"v_exp_f32 v122,v122\n"
MF "a[188:191],v[20:23],v[80:83],a[188:191]\n"
"v_exp_f32 v123,v123\n"
MF "a[192:195],v[20:23],v[84:87],a[192:195]\n"
"v_add_f32 v59,v59,v120\n"
"v_add_f32 v59,v59,v121\n"
MF "a[172:175],v[16:19],v[84:87],a[172:175]\n"
"s_waitcnt lgkmcnt(0)\n"
CV "v102,v120,v121\n"
"v_add_f32 v59,v59,v122\n"
MF "a[176:179],v[16:19],v[88:91],a[176:179]\n"
"s_barrier\n"
MF "a[196:199],v[20:23],v[88:91],a[196:199]\n"
"s_sub_u32 s58,s71,s70\n"
"s_add_u32 s73,s73,s58\n"
"v_add_f32 v59,v59,v123\n"
MF "a[216:219],v[24:27],v[88:91],a[216:219]\n"
"s_add_u32 s18,s18,s58\n"
"s_add_u32 s19,s19,s58\n"
"s_add_u32 s73,s73,12288\n"
MF "a[236:239],v[28:31],v[88:91],a[236:239]\n"
"s_cmp_eq_u32 s73,s19\n"
"s_cselect_b32 s73,s18,s73\n"
"s_add_u32 m0,s65,49152\n"
MF "a[232:235],v[28:31],v[84:87],a[232:235]\n"
LD "v8,s[36:39],s73 offen nt lds\n"
MF "a[212:215],v[24:27],v[84:87],a[212:215]\n"
"s_add_u32 m0,s65,50048\n"
CV "v103,v122,v123\n"
MF "a[208:211],v[24:27],v[80:83],a[208:211]\n"
LD "v8,s[36:39],s73 offen offset:128 nt lds\n"
MF "a[228:231],v[28:31],v[80:83],a[228:231]\n"
"s_add_u32 m0,s65,50944\n"
"v_exp_f32 v124,v124\n"
MF "a[224:227],v[28:31],v[76:79],a[224:227]\n"
LD "v8,s[36:39],s73 offen offset:256 nt lds\n"
MF "a[204:207],v[24:27],v[76:79],a[204:207]\n"
"v_exp_f32 v125,v125\n"
MF "a[200:203],v[24:27],v[72:75],a[200:203]\n"
"v_exp_f32 v126,v126\n"
MF "a[220:223],v[28:31],v[72:75],a[220:223]\n"
"v_exp_f32 v127,v127\n"
"v_add_f32 v60,v60,v124\n"
"v_add_f32 v60,v60,v125\n"
CV "v106,v124,v125\n"
"v_add_f32 v60,v60,v126\n"
"v_add_f32 v60,v60,v127\n"
CV "v107,v126,v127\n"
"v_exp_f32 v128,v128\n"
"v_exp_f32 v129,v129\n"
"v_exp_f32 v130,v130\n"
"v_exp_f32 v131,v131\n"
"v_add_f32 v61,v61,v128\n"
"v_add_f32 v61,v61,v129\n"
CV "v110,v128,v129\n"
"v_add_f32 v61,v61,v130\n"
"v_add_f32 v61,v61,v131\n"
CV "v111,v130,v131\n"
"v_max3_f32 v10,v57,v58,v59\n"
"v_max3_f32 v10,v10,v60,v61\n"
"v_cmp_le_f32 vcc,0x47800000,v10\n"
"s_cbranch_vccnz .Lstub_13\n"
".Lback_13:\n"
"s_waitcnt vmcnt(9) lgkmcnt(0)\n"
"s_barrier\n"
"s_lshl_b32 s58,s56,10\n"
"s_add_u32 s58,s58,73728\n"
"v_add_u32_e64 v120,s58,v9\n"
"v_add_u32 v121,65536,v120\n"
"s_sub_u32 s59,s71,s70\n"
"v_mov_b32 v10,1.0\n"
TR "v[136:137],v2 offset:24576\n"
TR "v[138:139],v2 offset:30720\n"
"v_mfma_f32_16x16x4_f32 v[16:19],v10,v57,0\n"
TR "v[140:141],v3 offset:24576\n"
TR "v[142:143],v3 offset:30720\n"
TR "v[144:145],v4 offset:24576\n"
TR "v[146:147],v4 offset:30720\n"
TR "v[148:149],v5 offset:24576\n"
TR "v[150:151],v5 offset:30720\n"
"v_mfma_f32_16x16x4_f32 v[20:23],v10,v58,0\n"
TR "v[152:153],v2 offset:25600\n"
TR "v[154:155],v2 offset:31744\n"
TR "v[156:157],v3 offset:25600\n"
TR "v[158:159],v3 offset:31744\n"
TR "v[160:161],v4 offset:25600\n"
TR "v[162:163],v4 offset:31744\n"
"v_mfma_f32_16x16x4_f32 v[24:27],v10,v59,0\n"
TR "v[164:165],v5 offset:25600\n"
TR "v[166:167],v5 offset:31744\n"
TR "v[168:169],v2 offset:26624\n"
TR "v[170:171],v2 offset:32768\n"
TR "v[172:173],v3 offset:26624\n"
TR "v[174:175],v3 offset:32768\n"
"v_mfma_f32_16x16x4_f32 v[28:31],v10,v60,0\n"
TR "v[176:177],v4 offset:26624\n"
TR "v[178:179],v4 offset:32768\n"
TR "v[180:181],v5 offset:26624\n"
TR "v[182:183],v5 offset:32768\n"
"v_mfma_f32_16x16x4_f32 v[32:35],v10,v61,0\n"
"s_add_u32 m0,s65,49152\n"
"s_nop 0\n"
LD "v8,s[36:39],s73 offen offset:3072 nt lds\n"
"s_add_u32 m0,s65,50048\n"
"s_nop 0\n"
LD "v8,s[36:39],s73 offen offset:3200 nt lds\n"
"s_waitcnt lgkmcnt(15)\n"
MF "a[0:3],v[136:139],v[92:95],a[0:3]\n"
MF "a[20:23],v[140:143],v[92:95],a[20:23]\n"
"s_add_u32 m0,s65,50944\n"
"s_nop 0\n"
LD "v8,s[36:39],s73 offen offset:3328 nt lds\n"
"s_sub_u32 s74,s96,s16\n"
"s_add_u32 s74,s74,s71\n"
"s_add_u32 s20,s20,s59\n"
"s_mov_b32 s70,s71\n"
LD "v[232:235],v9,s[40:43],s75 offen sc1\n"
"v_rcp_f32 v57,v16\n"
"v_rcp_f32 v58,v20\n"
"v_rcp_f32 v59,v24\n"
"v_rcp_f32 v60,v28\n"
"v_rcp_f32_e64 v61,v32\n"
MF "a[4:7],v[136:139],v[96:99],a[4:7]\n"
"v_mul_f32_e64 v52,-s13,v57\n"
"v_mul_f32_e64 v53,-s13,v58\n"
"v_mul_f32_e64 v54,-s13,v59\n"
"v_mul_f32_e64 v55,-s13,v60\n"
"v_mul_f32_e64 v56,-s13,v61\n"
AR "v72,a0\n"
AR "v73,a1\n"
AR "v74,a2\n"
MF "a[24:27],v[140:143],v[96:99],a[24:27]\n"
AR "v75,a3\n"
AR "v76,a20\n"
AR "v77,a21\n"
AR "v78,a22\n"
AR "v79,a23\n"
"v_pk_fma_f32 v[72:73],v[72:73],v[56:57],v[52:53] op_sel:[0,1,0] op_sel_hi:[1,1,0]\n"
"v_pk_fma_f32 v[74:75],v[74:75],v[56:57],v[52:53] op_sel:[0,1,0] op_sel_hi:[1,1,0]\n"
"v_pk_fma_f32 v[76:77],v[76:77],v[56:57],v[52:53] op_sel:[0,1,0] op_sel_hi:[1,1,0]\n"
"v_pk_fma_f32 v[78:79],v[78:79],v[56:57],v[52:53] op_sel:[0,1,0] op_sel_hi:[1,1,0]\n"
CV "v112,v72,v73\n"
CV "v113,v74,v75\n"
CV "v114,v76,v77\n"
CV "v115,v78,v79\n"
MF "a[8:11],v[136:139],v[100:103],a[8:11]\n"
"ds_read_b128 v[184:187],v120 offset:24576\n"
"ds_write_b128 v120,v[112:115] offset:24576\n"
LD "v[236:239],v9,s[40:43],s75 offen offset:1024 sc1\n"
AR "v80,a4\n"
AR "v81,a5\n"
AR "v82,a6\n"
MF "a[28:31],v[140:143],v[100:103],a[28:31]\n"
AR "v83,a7\n"
AR "v84,a24\n"
AR "v85,a25\n"
AR "v86,a26\n"
AR "v87,a27\n"
"v_pk_fma_f32 v[80:81],v[80:81],v[58:59],v[52:53] op_sel:[0,0,1] op_sel_hi:[1,0,1]\n"
"v_pk_fma_f32 v[82:83],v[82:83],v[58:59],v[52:53] op_sel:[0,0,1] op_sel_hi:[1,0,1]\n"
"v_pk_fma_f32 v[84:85],v[84:85],v[58:59],v[52:53] op_sel:[0,0,1] op_sel_hi:[1,0,1]\n"
"v_pk_fma_f32 v[86:87],v[86:87],v[58:59],v[52:53] op_sel:[0,0,1] op_sel_hi:[1,0,1]\n"
CV "v116,v80,v81\n"
CV "v117,v82,v83\n"
CV "v118,v84,v85\n"
CV "v119,v86,v87\n"
MF "a[12:15],v[136:139],v[104:107],a[12:15]\n"
"ds_read_b128 v[188:191],v120 offset:28672\n"
"ds_write_b128 v120,v[116:119] offset:28672\n"
AR "v72,a8\n"
AR "v73,a9\n"
AR "v74,a10\n"
MF "a[32:35],v[140:143],v[104:107],a[32:35]\n"
AR "v75,a11\n"
AR "v76,a28\n"
AR "v77,a29\n"
AR "v78,a30\n"
AR "v79,a31\n"
"v_pk_fma_f32 v[72:73],v[72:73],v[58:59],v[54:55] op_sel:[0,1,0] op_sel_hi:[1,1,0]\n"
"v_pk_fma_f32 v[74:75],v[74:75],v[58:59],v[54:55] op_sel:[0,1,0] op_sel_hi:[1,1,0]\n"
"v_pk_fma_f32 v[76:77],v[76:77],v[58:59],v[54:55] op_sel:[0,1,0] op_sel_hi:[1,1,0]\n"
"v_pk_fma_f32 v[78:79],v[78:79],v[58:59],v[54:55] op_sel:[0,1,0] op_sel_hi:[1,1,0]\n"
CV "v112,v72,v73\n"
CV "v113,v74,v75\n"
CV "v114,v76,v77\n"
CV "v115,v78,v79\n"
MF "a[16:19],v[136:139],v[108:111],a[16:19]\n"
"ds_read_b128 v[192:195],v120 offset:32768\n"
"ds_write_b128 v120,v[112:115] offset:32768\n"
"s_waitcnt lgkmcnt(0)\n"
"s_barrier\n"
LD "v[240:243],v9,s[40:43],s75 offen offset:2048 sc1\n"
AR "v80,a12\n"
AR "v81,a13\n"
AR "v82,a14\n"
MF "a[36:39],v[140:143],v[108:111],a[36:39]\n"
AR "v83,a15\n"
AR "v84,a32\n"
AR "v85,a33\n"
AR "v86,a34\n"
AR "v87,a35\n"
"v_pk_fma_f32 v[80:81],v[80:81],v[60:61],v[54:55] op_sel:[0,0,1] op_sel_hi:[1,0,1]\n"
"v_pk_fma_f32 v[82:83],v[82:83],v[60:61],v[54:55] op_sel:[0,0,1] op_sel_hi:[1,0,1]\n"
"v_pk_fma_f32 v[84:85],v[84:85],v[60:61],v[54:55] op_sel:[0,0,1] op_sel_hi:[1,0,1]\n"
"v_pk_fma_f32 v[86:87],v[86:87],v[60:61],v[54:55] op_sel:[0,0,1] op_sel_hi:[1,0,1]\n"
CV "v116,v80,v81\n"
CV "v117,v82,v83\n"
CV "v118,v84,v85\n"
CV "v119,v86,v87\n"
MF "a[40:43],v[144:147],v[92:95],a[40:43]\n"
"ds_read_b128 v[196:199],v120 offset:36864\n"
"ds_write_b128 v120,v[116:119] offset:36864\n"
LD "v[244:247],v9,s[40:43],s75 offen offset:3072 sc1\n"
AR "v72,a16\n"
AR "v73,a17\n"
AR "v74,a18\n"
MF "a[60:63],v[148:151],v[92:95],a[60:63]\n"
AR "v75,a19\n"
AR "v76,a36\n"
AR "v77,a37\n"
AR "v78,a38\n"
AR "v79,a39\n"
"v_pk_fma_f32 v[72:73],v[72:73],v[60:61],v[56:57] op_sel:[0,1,0] op_sel_hi:[1,1,0]\n"
"v_pk_fma_f32 v[74:75],v[74:75],v[60:61],v[56:57] op_sel:[0,1,0] op_sel_hi:[1,1,0]\n"
"v_pk_fma_f32 v[76:77],v[76:77],v[60:61],v[56:57] op_sel:[0,1,0] op_sel_hi:[1,1,0]\n"
"v_pk_fma_f32 v[78:79],v[78:79],v[60:61],v[56:57] op_sel:[0,1,0] op_sel_hi:[1,1,0]\n"
CV "v112,v72,v73\n"
CV "v113,v74,v75\n"
CV "v114,v76,v77\n"
CV "v115,v78,v79\n"
MF "a[44:47],v[144:147],v[96:99],a[44:47]\n"
"ds_read_b128 v[200:203],v120 offset:40960\n"
"ds_write_b128 v120,v[112:115] offset:40960\n"
AR "v80,a40\n"
AR "v81,a41\n"
AR "v82,a42\n"
MF "a[64:67],v[148:151],v[96:99],a[64:67]\n"
AR "v83,a43\n"
AR "v84,a60\n"
AR "v85,a61\n"
AR "v86,a62\n"
AR "v87,a63\n"
"v_pk_fma_f32 v[80:81],v[80:81],v[56:57],v[52:53] op_sel:[0,1,0] op_sel_hi:[1,1,0]\n"
"v_pk_fma_f32 v[82:83],v[82:83],v[56:57],v[52:53] op_sel:[0,1,0] op_sel_hi:[1,1,0]\n"
"v_pk_fma_f32 v[84:85],v[84:85],v[56:57],v[52:53] op_sel:[0,1,0] op_sel_hi:[1,1,0]\n"
"v_pk_fma_f32 v[86:87],v[86:87],v[56:57],v[52:53] op_sel:[0,1,0] op_sel_hi:[1,1,0]\n"
CV "v116,v80,v81\n"
CV "v117,v82,v83\n"
CV "v118,v84,v85\n"
CV "v119,v86,v87\n"
MF "a[48:51],v[144:147],v[100:103],a[48:51]\n"
"ds_read_b128 v[204:207],v120 offset:45056\n"
"ds_write_b128 v120,v[116:119] offset:45056\n"
"s_add_u32 s58,s75,4096\n"
LD "v[248:251],v9,s[40:43],s58 offen sc1\n"
AR "v72,a44\n"
AR "v73,a45\n"
AR "v74,a46\n"
MF "a[68:71],v[148:151],v[100:103],a[68:71]\n"
AR "v75,a47\n"
AR "v76,a64\n"
AR "v77,a65\n"
AR "v78,a66\n"
AR "v79,a67\n"
"v_pk_fma_f32 v[72:73],v[72:73],v[58:59],v[52:53] op_sel:[0,0,1] op_sel_hi:[1,0,1]\n"
"v_pk_fma_f32 v[74:75],v[74:75],v[58:59],v[52:53] op_sel:[0,0,1] op_sel_hi:[1,0,1]\n"
"v_pk_fma_f32 v[76:77],v[76:77],v[58:59],v[52:53] op_sel:[0,0,1] op_sel_hi:[1,0,1]\n"
"v_pk_fma_f32 v[78:79],v[78:79],v[58:59],v[52:53] op_sel:[0,0,1] op_sel_hi:[1,0,1]\n"
CV "v112,v72,v73\n"
CV "v113,v74,v75\n"
CV "v114,v76,v77\n"
CV "v115,v78,v79\n"
MF "a[52:55],v[144:147],v[104:107],a[52:55]\n"
"ds_read_b128 v[208:211],v120 offset:49152\n"
"ds_write_b128 v120,v[112:115] offset:49152\n"
LD "v[252:255],v9,s[40:43],s58 offen offset:1024 sc1\n"
AR "v80,a48\n"
AR "v81,a49\n"
AR "v82,a50\n"
MF "a[72:75],v[148:151],v[104:107],a[72:75]\n"
AR "v83,a51\n"
AR "v84,a68\n"
AR "v85,a69\n"
AR "v86,a70\n"
AR "v87,a71\n"
"v_pk_fma_f32 v[80:81],v[80:81],v[58:59],v[54:55] op_sel:[0,1,0] op_sel_hi:[1,1,0]\n"
"v_pk_fma_f32 v[82:83],v[82:83],v[58:59],v[54:55] op_sel:[0,1,0] op_sel_hi:[1,1,0]\n"
"v_pk_fma_f32 v[84:85],v[84:85],v[58:59],v[54:55] op_sel:[0,1,0] op_sel_hi:[1,1,0]\n"
"v_pk_fma_f32 v[86:87],v[86:87],v[58:59],v[54:55] op_sel:[0,1,0] op_sel_hi:[1,1,0]\n"
CV "v116,v80,v81\n"
CV "v117,v82,v83\n"
CV "v118,v84,v85\n"
CV "v119,v86,v87\n"
MF "a[56:59],v[144:147],v[108:111],a[56:59]\n"
"ds_read_b128 v[212:215],v120 offset:53248\n"
"ds_write_b128 v120,v[116:119] offset:53248\n"
AR "v72,a52\n"
AR "v73,a53\n"
AR "v74,a54\n"
MF "a[76:79],v[148:151],v[108:111],a[76:79]\n"
AR "v75,a55\n"
AR "v76,a72\n"
AR "v77,a73\n"
AR "v78,a74\n"
AR "v79,a75\n"
"v_pk_fma_f32 v[72:73],v[72:73],v[60:61],v[54:55] op_sel:[0,0,1] op_sel_hi:[1,0,1]\n"
"v_pk_fma_f32 v[74:75],v[74:75],v[60:61],v[54:55] op_sel:[0,0,1] op_sel_hi:[1,0,1]\n"
"v_pk_fma_f32 v[76:77],v[76:77],v[60:61],v[54:55] op_sel:[0,0,1] op_sel_hi:[1,0,1]\n"
"v_pk_fma_f32 v[78:79],v[78:79],v[60:61],v[54:55] op_sel:[0,0,1] op_sel_hi:[1,0,1]\n"
CV "v112,v72,v73\n"
CV "v113,v74,v75\n"
CV "v114,v76,v77\n"
CV "v115,v78,v79\n"
MF "a[80:83],v[152:155],v[92:95],a[80:83]\n"
"ds_read_b128 v[216:219],v120 offset:57344\n"
"ds_write_b128 v120,v[112:115] offset:57344\n"
AR "v80,a56\n"
AR "v81,a57\n"
AR "v82,a58\n"
MF "a[100:103],v[156:159],v[92:95],a[100:103]\n"
AR "v83,a59\n"
AR "v84,a76\n"
AR "v85,a77\n"
AR "v86,a78\n"
AR "v87,a79\n"
"v_pk_fma_f32 v[80:81],v[80:81],v[60:61],v[56:57] op_sel:[0,1,0] op_sel_hi:[1,1,0]\n"
"v_pk_fma_f32 v[82:83],v[82:83],v[60:61],v[56:57] op_sel:[0,1,0] op_sel_hi:[1,1,0]\n"
"v_pk_fma_f32 v[84:85],v[84:85],v[60:61],v[56:57] op_sel:[0,1,0] op_sel_hi:[1,1,0]\n"
"v_pk_fma_f32 v[86:87],v[86:87],v[60:61],v[56:57] op_sel:[0,1,0] op_sel_hi:[1,1,0]\n"
CV "v116,v80,v81\n"
CV "v117,v82,v83\n"
CV "v118,v84,v85\n"
CV "v119,v86,v87\n"
MF "a[84:87],v[152:155],v[96:99],a[84:87]\n"
"ds_read_b128 v[220:223],v120 offset:61440\n"
"ds_write_b128 v120,v[116:119] offset:61440\n"
AR "v72,a80\n"
AR "v73,a81\n"
AR "v74,a82\n"
MF "a[104:107],v[156:159],v[96:99],a[104:107]\n"
AR "v75,a83\n"
AR "v76,a100\n"
AR "v77,a101\n"
AR "v78,a102\n"
AR "v79,a103\n"
"v_pk_fma_f32 v[72:73],v[72:73],v[56:57],v[52:53] op_sel:[0,1,0] op_sel_hi:[1,1,0]\n"
"v_pk_fma_f32 v[74:75],v[74:75],v[56:57],v[52:53] op_sel:[0,1,0] op_sel_hi:[1,1,0]\n"
"v_pk_fma_f32 v[76:77],v[76:77],v[56:57],v[52:53] op_sel:[0,1,0] op_sel_hi:[1,1,0]\n"
"v_pk_fma_f32 v[78:79],v[78:79],v[56:57],v[52:53] op_sel:[0,1,0] op_sel_hi:[1,1,0]\n"
CV "v112,v72,v73\n"
CV "v113,v74,v75\n"
CV "v114,v76,v77\n"
CV "v115,v78,v79\n"
MF "a[88:91],v[152:155],v[100:103],a[88:91]\n"
"ds_read_b128 v[224:227],v121\n"
"ds_write_b128 v121,v[112:115]\n"
AR "v80,a84\n"
AR "v81,a85\n"
AR "v82,a86\n"
MF "a[108:111],v[156:159],v[100:103],a[108:111]\n"
AR "v83,a87\n"
AR "v84,a104\n"
AR "v85,a105\n"
AR "v86,a106\n"
AR "v87,a107\n"
"v_pk_fma_f32 v[80:81],v[80:81],v[58:59],v[52:53] op_sel:[0,0,1] op_sel_hi:[1,0,1]\n"
"v_pk_fma_f32 v[82:83],v[82:83],v[58:59],v[52:53] op_sel:[0,0,1] op_sel_hi:[1,0,1]\n"
"v_pk_fma_f32 v[84:85],v[84:85],v[58:59],v[52:53] op_sel:[0,0,1] op_sel_hi:[1,0,1]\n"
"v_pk_fma_f32 v[86:87],v[86:87],v[58:59],v[52:53] op_sel:[0,0,1] op_sel_hi:[1,0,1]\n"
CV "v116,v80,v81\n"
CV "v117,v82,v83\n"
CV "v118,v84,v85\n"
CV "v119,v86,v87\n"
MF "a[92:95],v[152:155],v[104:107],a[92:95]\n"
"ds_read_b128 v[228:231],v121 offset:4096\n"
"ds_write_b128 v121,v[116:119] offset:4096\n"
AR "v72,a88\n"
AR "v73,a89\n"
AR "v74,a90\n"
MF "a[112:115],v[156:159],v[104:107],a[112:115]\n"
AR "v75,a91\n"
AR "v76,a108\n"
AR "v77,a109\n"
AR "v78,a110\n"
AR "v79,a111\n"
"v_pk_fma_f32 v[72:73],v[72:73],v[58:59],v[54:55] op_sel:[0,1,0] op_sel_hi:[1,1,0]\n"
"v_pk_fma_f32 v[74:75],v[74:75],v[58:59],v[54:55] op_sel:[0,1,0] op_sel_hi:[1,1,0]\n"
"v_pk_fma_f32 v[76:77],v[76:77],v[58:59],v[54:55] op_sel:[0,1,0] op_sel_hi:[1,1,0]\n"
"v_pk_fma_f32 v[78:79],v[78:79],v[58:59],v[54:55] op_sel:[0,1,0] op_sel_hi:[1,1,0]\n"
CV "v112,v72,v73\n"
CV "v113,v74,v75\n"
CV "v114,v76,v77\n"
CV "v115,v78,v79\n"
MF "a[96:99],v[152:155],v[108:111],a[96:99]\n"
"ds_read_b128 v[136:139],v121 offset:8192\n"
"ds_write_b128 v121,v[112:115] offset:8192\n"
AR "v80,a92\n"
AR "v81,a93\n"
AR "v82,a94\n"
MF "a[116:119],v[156:159],v[108:111],a[116:119]\n"
AR "v83,a95\n"
AR "v84,a112\n"
AR "v85,a113\n"
AR "v86,a114\n"
AR "v87,a115\n"
"v_pk_fma_f32 v[80:81],v[80:81],v[60:61],v[54:55] op_sel:[0,0,1] op_sel_hi:[1,0,1]\n"
"v_pk_fma_f32 v[82:83],v[82:83],v[60:61],v[54:55] op_sel:[0,0,1] op_sel_hi:[1,0,1]\n"
"v_pk_fma_f32 v[84:85],v[84:85],v[60:61],v[54:55] op_sel:[0,0,1] op_sel_hi:[1,0,1]\n"
"v_pk_fma_f32 v[86:87],v[86:87],v[60:61],v[54:55] op_sel:[0,0,1] op_sel_hi:[1,0,1]\n"
CV "v116,v80,v81\n"
CV "v117,v82,v83\n"
CV "v118,v84,v85\n"
CV "v119,v86,v87\n"
MF "a[120:123],v[160:163],v[92:95],a[120:123]\n"
"ds_read_b128 v[140:143],v121 offset:12288\n"
"ds_write_b128 v121,v[116:119] offset:12288\n"
AR "v72,a96\n"
AR "v73,a97\n"
AR "v74,a98\n"
MF "a[140:143],v[164:167],v[92:95],a[140:143]\n"
AR "v75,a99\n"
AR "v76,a116\n"
AR "v77,a117\n"
AR "v78,a118\n"
AR "v79,a119\n"
"v_pk_fma_f32 v[72:73],v[72:73],v[60:61],v[56:57] op_sel:[0,1,0] op_sel_hi:[1,1,0]\n"
"v_pk_fma_f32 v[74:75],v[74:75],v[60:61],v[56:57] op_sel:[0,1,0] op_sel_hi:[1,1,0]\n"
"v_pk_fma_f32 v[76:77],v[76:77],v[60:61],v[56:57] op_sel:[0,1,0] op_sel_hi:[1,1,0]\n"
"v_pk_fma_f32 v[78:79],v[78:79],v[60:61],v[56:57] op_sel:[0,1,0] op_sel_hi:[1,1,0]\n"
CV "v112,v72,v73\n"
CV "v113,v74,v75\n"
CV "v114,v76,v77\n"
CV "v115,v78,v79\n"
MF "a[124:127],v[160:163],v[96:99],a[124:127]\n"
"ds_read_b128 v[144:147],v121 offset:16384\n"
"ds_write_b128 v121,v[112:115] offset:16384\n"
AR "v80,a120\n"
AR "v81,a121\n"
AR "v82,a122\n"
MF "a[144:147],v[164:167],v[96:99],a[144:147]\n"
".Lpg17g:\n"
"s_cbranch_execz .Lisr6\n"
AR "v83,a123\n"
AR "v84,a140\n"
AR "v85,a141\n"
"s_nop 0\n"
AR "v86,a142\n"
AR "v87,a143\n"
"v_pk_fma_f32 v[80:81],v[80:81],v[56:57],v[52:53] op_sel:[0,1,0] op_sel_hi:[1,1,0]\n"
"v_pk_fma_f32 v[82:83],v[82:83],v[56:57],v[52:53] op_sel:[0,1,0] op_sel_hi:[1,1,0]\n"
"v_pk_fma_f32 v[84:85],v[84:85],v[56:57],v[52:53] op_sel:[0,1,0] op_sel_hi:[1,1,0]\n"
"v_pk_fma_f32 v[86:87],v[86:87],v[56:57],v[52:53] op_sel:[0,1,0] op_sel_hi:[1,1,0]\n"
CV "v116,v80,v81\n"
CV "v117,v82,v83\n"
CV "v118,v84,v85\n"
CV "v119,v86,v87\n"
MF "a[128:131],v[160:163],v[100:103],a[128:131]\n"
"ds_read_b128 v[148:151],v121 offset:20480\n"
"ds_write_b128 v121,v[116:119] offset:20480\n"
AR "v72,a124\n"
AR "v73,a125\n"
AR "v74,a126\n"
MF "a[148:151],v[164:167],v[100:103],a[148:151]\n"
AR "v75,a127\n"
AR "v76,a144\n"
AR "v77,a145\n"
AR "v78,a146\n"
AR "v79,a147\n"
"v_pk_fma_f32 v[72:73],v[72:73],v[58:59],v[52:53] op_sel:[0,0,1] op_sel_hi:[1,0,1]\n"
"v_pk_fma_f32 v[74:75],v[74:75],v[58:59],v[52:53] op_sel:[0,0,1] op_sel_hi:[1,0,1]\n"
"v_pk_fma_f32 v[76:77],v[76:77],v[58:59],v[52:53] op_sel:[0,0,1] op_sel_hi:[1,0,1]\n"
"v_pk_fma_f32 v[78:79],v[78:79],v[58:59],v[52:53] op_sel:[0,0,1] op_sel_hi:[1,0,1]\n"
CV "v112,v72,v73\n"
CV "v113,v74,v75\n"
CV "v114,v76,v77\n"
CV "v115,v78,v79\n"
MF "a[132:135],v[160:163],v[104:107],a[132:135]\n"
"ds_write_b128 v120,v[112:115]\n"
AR "v80,a128\n"
AR "v81,a129\n"
AR "v82,a130\n"
MF "a[152:155],v[164:167],v[104:107],a[152:155]\n"
AR "v83,a131\n"
AR "v84,a148\n"
AR "v85,a149\n"
AR "v86,a150\n"
AR "v87,a151\n"
"v_pk_fma_f32 v[80:81],v[80:81],v[58:59],v[54:55] op_sel:[0,1,0] op_sel_hi:[1,1,0]\n"
"v_pk_fma_f32 v[82:83],v[82:83],v[58:59],v[54:55] op_sel:[0,1,0] op_sel_hi:[1,1,0]\n"
"v_pk_fma_f32 v[84:85],v[84:85],v[58:59],v[54:55] op_sel:[0,1,0] op_sel_hi:[1,1,0]\n"
"v_pk_fma_f32 v[86:87],v[86:87],v[58:59],v[54:55] op_sel:[0,1,0] op_sel_hi:[1,1,0]\n"
CV "v116,v80,v81\n"
CV "v117,v82,v83\n"
CV "v118,v84,v85\n"
CV "v119,v86,v87\n"
MF "a[136:139],v[160:163],v[108:111],a[136:139]\n"
"ds_write_b128 v120,v[116:119] offset:4096\n"
AR "v72,a132\n"
AR "v73,a133\n"
AR "v74,a134\n"
MF "a[156:159],v[164:167],v[108:111],a[156:159]\n"
AR "v75,a135\n"
AR "v76,a152\n"
AR "v77,a153\n"
AR "v78,a154\n"
AR "v79,a155\n"
"v_pk_fma_f32 v[72:73],v[72:73],v[60:61],v[54:55] op_sel:[0,0,1] op_sel_hi:[1,0,1]\n"
"v_pk_fma_f32 v[74:75],v[74:75],v[60:61],v[54:55] op_sel:[0,0,1] op_sel_hi:[1,0,1]\n"
"v_pk_fma_f32 v[76:77],v[76:77],v[60:61],v[54:55] op_sel:[0,0,1] op_sel_hi:[1,0,1]\n"
"v_pk_fma_f32 v[78:79],v[78:79],v[60:61],v[54:55] op_sel:[0,0,1] op_sel_hi:[1,0,1]\n"
CV "v112,v72,v73\n"
CV "v113,v74,v75\n"
CV "v114,v76,v77\n"
CV "v115,v78,v79\n"
MF "a[160:163],v[168:171],v[92:95],a[160:163]\n"
"ds_write_b128 v120,v[112:115] offset:8192\n"
AR "v80,a136\n"
AR "v81,a137\n"
AR "v82,a138\n"
MF "a[180:183],v[172:175],v[92:95],a[180:183]\n"
AR "v83,a139\n"
AR "v84,a156\n"
AR "v85,a157\n"
AR "v86,a158\n"
AR "v87,a159\n"
"v_pk_fma_f32 v[80:81],v[80:81],v[60:61],v[56:57] op_sel:[0,1,0] op_sel_hi:[1,1,0]\n"
"v_pk_fma_f32 v[82:83],v[82:83],v[60:61],v[56:57] op_sel:[0,1,0] op_sel_hi:[1,1,0]\n"
"v_pk_fma_f32 v[84:85],v[84:85],v[60:61],v[56:57] op_sel:[0,1,0] op_sel_hi:[1,1,0]\n"
"v_pk_fma_f32 v[86:87],v[86:87],v[60:61],v[56:57] op_sel:[0,1,0] op_sel_hi:[1,1,0]\n"
CV "v116,v80,v81\n"
CV "v117,v82,v83\n"
CV "v118,v84,v85\n"
CV "v119,v86,v87\n"
MF "a[164:167],v[168:171],v[96:99],a[164:167]\n"
"ds_write_b128 v120,v[116:119] offset:12288\n"
AR "v72,a160\n"
AR "v73,a161\n"
AR "v74,a162\n"
MF "a[184:187],v[172:175],v[96:99],a[184:187]\n"
AR "v75,a163\n"
AR "v76,a180\n"
AR "v77,a181\n"
AR "v78,a182\n"
AR "v79,a183\n"
"v_pk_fma_f32 v[72:73],v[72:73],v[56:57],v[52:53] op_sel:[0,1,0] op_sel_hi:[1,1,0]\n"
"v_pk_fma_f32 v[74:75],v[74:75],v[56:57],v[52:53] op_sel:[0,1,0] op_sel_hi:[1,1,0]\n"
"v_pk_fma_f32 v[76:77],v[76:77],v[56:57],v[52:53] op_sel:[0,1,0] op_sel_hi:[1,1,0]\n"
"v_pk_fma_f32 v[78:79],v[78:79],v[56:57],v[52:53] op_sel:[0,1,0] op_sel_hi:[1,1,0]\n"
CV "v112,v72,v73\n"
CV "v113,v74,v75\n"
CV "v114,v76,v77\n"
CV "v115,v78,v79\n"
MF "a[168:171],v[168:171],v[100:103],a[168:171]\n"
"ds_write_b128 v120,v[112:115] offset:16384\n"
AR "v80,a164\n"
AR "v81,a165\n"
AR "v82,a166\n"
MF "a[188:191],v[172:175],v[100:103],a[188:191]\n"
AR "v83,a167\n"
AR "v84,a184\n"
AR "v85,a185\n"
AR "v86,a186\n"
AR "v87,a187\n"
"v_pk_fma_f32 v[80:81],v[80:81],v[58:59],v[52:53] op_sel:[0,0,1] op_sel_hi:[1,0,1]\n"
"v_pk_fma_f32 v[82:83],v[82:83],v[58:59],v[52:53] op_sel:[0,0,1] op_sel_hi:[1,0,1]\n"
"v_pk_fma_f32 v[84:85],v[84:85],v[58:59],v[52:53] op_sel:[0,0,1] op_sel_hi:[1,0,1]\n"
"v_pk_fma_f32 v[86:87],v[86:87],v[58:59],v[52:53] op_sel:[0,0,1] op_sel_hi:[1,0,1]\n"
CV "v116,v80,v81\n"
CV "v117,v82,v83\n"
CV "v118,v84,v85\n"
CV "v119,v86,v87\n"
MF "a[172:175],v[168:171],v[104:107],a[172:175]\n"
"ds_write_b128 v120,v[116:119] offset:20480\n"
AR "v72,a168\n"
AR "v73,a169\n"
AR "v74,a170\n"
MF "a[192:195],v[172:175],v[104:107],a[192:195]\n"
AR "v75,a171\n"
AR "v76,a188\n"
AR "v77,a189\n"
AR "v78,a190\n"
AR "v79,a191\n"
"v_pk_fma_f32 v[72:73],v[72:73],v[58:59],v[54:55] op_sel:[0,1,0] op_sel_hi:[1,1,0]\n"
"v_pk_fma_f32 v[74:75],v[74:75],v[58:59],v[54:55] op_sel:[0,1,0] op_sel_hi:[1,1,0]\n"
"v_pk_fma_f32 v[76:77],v[76:77],v[58:59],v[54:55] op_sel:[0,1,0] op_sel_hi:[1,1,0]\n"
"v_pk_fma_f32 v[78:79],v[78:79],v[58:59],v[54:55] op_sel:[0,1,0] op_sel_hi:[1,1,0]\n"
CV "v112,v72,v73\n"
CV "v113,v74,v75\n"
CV "v114,v76,v77\n"
CV "v115,v78,v79\n"
MF "a[176:179],v[168:171],v[108:111],a[176:179]\n"
AR "v152,a240\n"
AW "a240,v112\n"
AR "v153,a241\n"
AW "a241,v113\n"
AR "v154,a242\n"
AW "a242,v114\n"
AR "v155,a243\n"
AW "a243,v115\n"
AR "v80,a172\n"
AR "v81,a173\n"
AR "v82,a174\n"
MF "a[196:199],v[172:175],v[108:111],a[196:199]\n"
AR "v83,a175\n"
AR "v84,a192\n"
AR "v85,a193\n"
AR "v86,a194\n"
AR "v87,a195\n"
"v_pk_fma_f32 v[80:81],v[80:81],v[60:61],v[54:55] op_sel:[0,0,1] op_sel_hi:[1,0,1]\n"
"v_pk_fma_f32 v[82:83],v[82:83],v[60:61],v[54:55] op_sel:[0,0,1] op_sel_hi:[1,0,1]\n"
"v_pk_fma_f32 v[84:85],v[84:85],v[60:61],v[54:55] op_sel:[0,0,1] op_sel_hi:[1,0,1]\n"
"v_pk_fma_f32 v[86:87],v[86:87],v[60:61],v[54:55] op_sel:[0,0,1] op_sel_hi:[1,0,1]\n"
CV "v116,v80,v81\n"
CV "v117,v82,v83\n"
CV "v118,v84,v85\n"
CV "v119,v86,v87\n"
MF "a[200:203],v[176:179],v[92:95],a[200:203]\n"
AR "v156,a244\n"
AW "a244,v116\n"
AR "v157,a245\n"
AW "a245,v117\n"
AR "v158,a246\n"
AW "a246,v118\n"
AR "v159,a247\n"
AW "a247,v119\n"
AR "v72,a176\n"
AR "v73,a177\n"
AR "v74,a178\n"
MF "a[220:223],v[180:183],v[92:95],a[220:223]\n"
AR "v75,a179\n"
AR "v76,a196\n"
AR "v77,a197\n"
AR "v78,a198\n"
AR "v79,a199\n"
"v_pk_fma_f32 v[72:73],v[72:73],v[60:61],v[56:57] op_sel:[0,1,0] op_sel_hi:[1,1,0]\n"
"v_pk_fma_f32 v[74:75],v[74:75],v[60:61],v[56:57] op_sel:[0,1,0] op_sel_hi:[1,1,0]\n"
"v_pk_fma_f32 v[76:77],v[76:77],v[60:61],v[56:57] op_sel:[0,1,0] op_sel_hi:[1,1,0]\n"
"v_pk_fma_f32 v[78:79],v[78:79],v[60:61],v[56:57] op_sel:[0,1,0] op_sel_hi:[1,1,0]\n"
CV "v112,v72,v73\n"
CV "v113,v74,v75\n"
CV "v114,v76,v77\n"
CV "v115,v78,v79\n"
MF "a[204:207],v[176:179],v[96:99],a[204:207]\n"
AR "v160,a248\n"
AW "a248,v112\n"
AR "v161,a249\n"
AW "a249,v113\n"
AR "v162,a250\n"
AW "a250,v114\n"
AR "v163,a251\n"
AW "a251,v115\n"
AR "v80,a200\n"
AR "v81,a201\n"
AR "v82,a202\n"
MF "a[224:227],v[180:183],v[96:99],a[224:227]\n"
"v_mov_b64 v[164:165],v[62:63]\n"
"v_mov_b64 v[166:167],v[64:65]\n"
AR "v83,a203\n"
AR "v84,a220\n"
AR "v85,a221\n"
AR "v86,a222\n"
AR "v87,a223\n"
"v_pk_fma_f32 v[80:81],v[80:81],v[56:57],v[52:53] op_sel:[0,1,0] op_sel_hi:[1,1,0]\n"
"v_pk_fma_f32 v[82:83],v[82:83],v[56:57],v[52:53] op_sel:[0,1,0] op_sel_hi:[1,1,0]\n"
"v_pk_fma_f32 v[84:85],v[84:85],v[56:57],v[52:53] op_sel:[0,1,0] op_sel_hi:[1,1,0]\n"
"v_pk_fma_f32 v[86:87],v[86:87],v[56:57],v[52:53] op_sel:[0,1,0] op_sel_hi:[1,1,0]\n"
CV "v62,v80,v81\n"
CV "v63,v82,v83\n"
CV "v64,v84,v85\n"
CV "v65,v86,v87\n"
MF "a[208:211],v[176:179],v[100:103],a[208:211]\n"
AR "v72,a204\n"
AR "v73,a205\n"
AR "v74,a206\n"
MF "a[228:231],v[180:183],v[100:103],a[228:231]\n"
"v_mov_b64 v[168:169],v[66:67]\n"
"v_mov_b64 v[170:171],v[68:69]\n"
AR "v75,a207\n"
AR "v76,a224\n"
AR "v77,a225\n"
AR "v78,a226\n"
AR "v79,a227\n"
"v_pk_fma_f32 v[72:73],v[72:73],v[58:59],v[52:53] op_sel:[0,0,1] op_sel_hi:[1,0,1]\n"
"v_pk_fma_f32 v[74:75],v[74:75],v[58:59],v[52:53] op_sel:[0,0,1] op_sel_hi:[1,0,1]\n"
"v_pk_fma_f32 v[76:77],v[76:77],v[58:59],v[52:53] op_sel:[0,0,1] op_sel_hi:[1,0,1]\n"
"v_pk_fma_f32 v[78:79],v[78:79],v[58:59],v[52:53] op_sel:[0,0,1] op_sel_hi:[1,0,1]\n"
CV "v66,v72,v73\n"
CV "v67,v74,v75\n"
CV "v68,v76,v77\n"
CV "v69,v78,v79\n"
MF "a[212:215],v[176:179],v[104:107],a[212:215]\n"
AR "v80,a208\n"
AR "v81,a209\n"
AR "v82,a210\n"
MF "a[232:235],v[180:183],v[104:107],a[232:235]\n"
"v_mov_b64 v[172:173],v[70:71]\n"
"v_mov_b64 v[174:175],v[132:133]\n"
AR "v83,a211\n"
AR "v84,a228\n"
AR "v85,a229\n"
AR "v86,a230\n"
AR "v87,a231\n"
"v_pk_fma_f32 v[80:81],v[80:81],v[58:59],v[54:55] op_sel:[0,1,0] op_sel_hi:[1,1,0]\n"
"v_pk_fma_f32 v[82:83],v[82:83],v[58:59],v[54:55] op_sel:[0,1,0] op_sel_hi:[1,1,0]\n"
"v_pk_fma_f32 v[84:85],v[84:85],v[58:59],v[54:55] op_sel:[0,1,0] op_sel_hi:[1,1,0]\n"
"v_pk_fma_f32 v[86:87],v[86:87],v[58:59],v[54:55] op_sel:[0,1,0] op_sel_hi:[1,1,0]\n"
CV "v70,v80,v81\n"
CV "v71,v82,v83\n"
CV "v132,v84,v85\n"
CV "v133,v86,v87\n"
MF "a[216:219],v[176:179],v[108:111],a[216:219]\n"
AR "v72,a212\n"
AR "v73,a213\n"
AR "v74,a214\n"
MF "a[236:239],v[180:183],v[108:111],a[236:239]\n"
"v_mov_b64 v[176:177],v[134:135]\n"
"v_mov_b32 v178,v6\n"
"v_mov_b32_e64 v179,v13\n"
AR "v75,a215\n"
AR "v76,a232\n"
AR "v77,a233\n"
AR "v78,a234\n"
AR "v79,a235\n"
"v_pk_fma_f32 v[72:73],v[72:73],v[60:61],v[54:55] op_sel:[0,0,1] op_sel_hi:[1,0,1]\n"
"v_pk_fma_f32 v[74:75],v[74:75],v[60:61],v[54:55] op_sel:[0,0,1] op_sel_hi:[1,0,1]\n"
"v_pk_fma_f32 v[76:77],v[76:77],v[60:61],v[54:55] op_sel:[0,0,1] op_sel_hi:[1,0,1]\n"
"v_pk_fma_f32 v[78:79],v[78:79],v[60:61],v[54:55] op_sel:[0,0,1] op_sel_hi:[1,0,1]\n"
CV "v134,v72,v73\n"
CV "v135,v74,v75\n"
CV "v6,v76,v77\n"
CV "v13,v78,v79\n"
"s_waitcnt vmcnt(0) lgkmcnt(0)\n"
"s_barrier\n"
"ds_read_b128 v[92:95],v0\n"
"ds_read_b128 v[96:99],v1\n"
"ds_read_b128 v[100:103],v0 offset:1024\n"
AR "v80,a216\n"
AR "v81,a217\n"
AR "v82,a218\n"
"v_mov_b64 v[180:181],v[14:15]\n"
"s_nop 0\n"
AR "v83,a219\n"
AR "v84,a236\n"
AR "v85,a237\n"
AR "v86,a238\n"
AR "v87,a239\n"
"v_pk_fma_f32 v[80:81],v[80:81],v[60:61],v[56:57] op_sel:[0,1,0] op_sel_hi:[1,1,0]\n"
"v_pk_fma_f32 v[82:83],v[82:83],v[60:61],v[56:57] op_sel:[0,1,0] op_sel_hi:[1,1,0]\n"
"v_pk_fma_f32 v[84:85],v[84:85],v[60:61],v[56:57] op_sel:[0,1,0] op_sel_hi:[1,1,0]\n"
"v_pk_fma_f32 v[86:87],v[86:87],v[60:61],v[56:57] op_sel:[0,1,0] op_sel_hi:[1,1,0]\n"
CV "v14,v80,v81\n"
CV "v15,v82,v83\n"
CV "v118,v84,v85\n"
CV "v119,v86,v87\n"
AR "v182,a254\n"
AW "a254,v118\n"
AR "v183,a255\n"
AW "a255,v119\n"
"s_mov_b32 s13,s11\n"
"v_subrev_u32 v2,49152,v2\n"
"v_subrev_u32 v3,49152,v3\n"
"v_subrev_u32 v4,49152,v4\n"
"v_subrev_u32 v5,49152,v5\n"
"s_mov_b32 s57,2\n"
"s_mov_b32 s69,1\n"
"s_mov_b32 s61,2\n"
"s_getreg_b32 s58,hwreg(HW_REG_XCC_ID,0,1)\n"
"s_mov_b32 s97,170\n"
"s_mul_i32 s95,s58,0x47800000\n"
"s_mov_b32 s77,s95\n"
"s_branch .Latt2\n"
".LpgattB_f:\n"
"s_cbranch_execz .Lisr3\n"
".LattB:\n"
AR "v2,a245\n"
"v_xor_b32 v3,32,v2\n"
"v_xor_b32 v4,64,v2\n"
"v_xor_b32 v5,96,v2\n"
"s_mov_b32 s61,2\n"
".Latt2:\n"
"s_cmp_eq_u32 s57,1\n"
"s_cbranch_scc1 .Lt0k\n"
".Lt0n:\n"
"s_waitcnt lgkmcnt(2)\n"
MF "v[72:75],v[92:95],v[136:139],0\n"
"ds_read_b128 v[104:107],v1 offset:1024\n"
MF "v[76:79],v[92:95],v[140:143],0\n"
MF "v[80:83],v[92:95],v[144:147],0\n"
MF "v[84:87],v[92:95],v[148:151],0\n"
MF "v[88:91],v[92:95],v[152:155],0\n"
"s_waitcnt lgkmcnt(2)\n"
"ds_read_b128 v[92:95],v0 offset:2048\n"
MF "v[72:75],v[96:99],v[156:159],v[72:75]\n"
MF "v[76:79],v[96:99],v[160:163],v[76:79]\n"
MF "v[80:83],v[96:99],v[164:167],v[80:83]\n"
MF "v[84:87],v[96:99],v[168:171],v[84:87]\n"
MF "v[88:91],v[96:99],v[172:175],v[88:91]\n"
"s_waitcnt lgkmcnt(2)\n"
"ds_read_b128 v[96:99],v1 offset:2048\n"
MF "v[72:75],v[100:103],v[176:179],v[72:75]\n"
MF "v[76:79],v[100:103],v[180:183],v[76:79]\n"
MF "v[80:83],v[100:103],v[184:187],v[80:83]\n"
MF "v[84:87],v[100:103],v[188:191],v[84:87]\n"
MF "v[88:91],v[100:103],v[192:195],v[88:91]\n"
"s_waitcnt lgkmcnt(2)\n"
"ds_read_b128 v[100:103],v0 offset:6144\n"
MF "v[72:75],v[104:107],v[196:199],v[72:75]\n"
MF "v[76:79],v[104:107],v[200:203],v[76:79]\n"
MF "v[80:83],v[104:107],v[204:207],v[80:83]\n"
MF "v[84:87],v[104:107],v[208:211],v[84:87]\n"
MF "v[88:91],v[104:107],v[212:215],v[88:91]\n"
"s_waitcnt lgkmcnt(2)\n"
"ds_read_b128 v[104:107],v1 offset:6144\n"
MF "v[72:75],v[92:95],v[216:219],v[72:75]\n"
MF "v[76:79],v[92:95],v[220:223],v[76:79]\n"
MF "v[80:83],v[92:95],v[224:227],v[80:83]\n"
MF "v[84:87],v[92:95],v[228:231],v[84:87]\n"
MF "v[88:91],v[92:95],v[232:235],v[88:91]\n"
"s_waitcnt lgkmcnt(2)\n"
"ds_read_b128 v[92:95],v0 offset:7168\n"
MF "v[72:75],v[96:99],v[236:239],v[72:75]\n"
MF "v[76:79],v[96:99],v[240:243],v[76:79]\n"
MF "v[80:83],v[96:99],v[244:247],v[80:83]\n"
MF "v[84:87],v[96:99],v[248:251],v[84:87]\n"
MF "v[88:91],v[96:99],v[252:255],v[88:91]\n"
"s_waitcnt lgkmcnt(2)\n"
"ds_read_b128 v[96:99],v1 offset:7168\n"
MF "v[112:115],v[100:103],v[136:139],0\n"
MF "v[116:119],v[100:103],v[140:143],0\n"
MF "v[120:123],v[100:103],v[144:147],0\n"
MF "v[124:127],v[100:103],v[148:151],0\n"
MF "v[128:131],v[100:103],v[152:155],0\n"
"s_waitcnt lgkmcnt(2)\n"
"ds_read_b128 v[100:103],v0 offset:8192\n"
MF "v[112:115],v[104:107],v[156:159],v[112:115]\n"
"v_max3_f32 v52,v72,v73,v74\n"
"v_max_f32 v52,v52,v75\n"
MF "v[116:119],v[104:107],v[160:163],v[116:119]\n"
"v_max3_f32 v53,v76,v77,v78\n"
"v_max_f32 v53,v53,v79\n"
MF "v[120:123],v[104:107],v[164:167],v[120:123]\n"
"v_max3_f32 v54,v80,v81,v82\n"
"v_max_f32 v54,v54,v83\n"
MF "v[124:127],v[104:107],v[168:171],v[124:127]\n"
"v_max3_f32 v55,v84,v85,v86\n"
"v_max_f32 v55,v55,v87\n"
MF "v[128:131],v[104:107],v[172:175],v[128:131]\n"
"s_waitcnt lgkmcnt(2)\n"
"ds_read_b128 v[104:107],v1 offset:8192\n"
"v_max3_f32 v56,v88,v89,v90\n"
MF "v[112:115],v[92:95],v[176:179],v[112:115]\n"
"v_max_f32 v56,v56,v91\n"
MF "v[116:119],v[92:95],v[180:183],v[116:119]\n"
MF "v[120:123],v[92:95],v[184:187],v[120:123]\n"
MF "v[124:127],v[92:95],v[188:191],v[124:127]\n"
MF "v[128:131],v[92:95],v[192:195],v[128:131]\n"
"s_waitcnt lgkmcnt(2)\n"
MF "v[112:115],v[96:99],v[196:199],v[112:115]\n"
"s_waitcnt lgkmcnt(0)\n"
MF "v[116:119],v[96:99],v[200:203],v[116:119]\n"
"s_barrier\n"
MF "v[120:123],v[96:99],v[204:207],v[120:123]\n"
"s_add_u32 s72,s72,6144\n"
MF "v[124:127],v[96:99],v[208:211],v[124:127]\n"
"s_cmp_eq_u32 s72,s17\n"
"s_cselect_b32 s72,s16,s72\n"
"s_add_u32 m0,s64,0\n"
MF "v[128:131],v[96:99],v[212:215],v[128:131]\n"
LD "v7,s[32:35],s72 offen nt lds\n"
MF "v[112:115],v[100:103],v[216:219],v[112:115]\n"
"s_add_u32 m0,s64,896\n"
MF "v[116:119],v[100:103],v[220:223],v[116:119]\n"
LD "v7,s[32:35],s72 offen offset:128 nt lds\n"
MF "v[120:123],v[100:103],v[224:227],v[120:123]\n"
"s_add_u32 m0,s64,1792\n"
MF "v[124:127],v[100:103],v[228:231],v[124:127]\n"
LD "v7,s[32:35],s72 offen offset:256 nt lds\n"
MF "v[128:131],v[100:103],v[232:235],v[128:131]\n"
"s_add_u32 s72,s72,6144\n"
"s_add_u32 m0,s64,6144\n"
MF "v[112:115],v[104:107],v[236:239],v[112:115]\n"
LD "v7,s[32:35],s72 offen nt lds\n"
MF "v[116:119],v[104:107],v[240:243],v[116:119]\n"
"s_add_u32 m0,s64,7040\n"
MF "v[120:123],v[104:107],v[244:247],v[120:123]\n"
LD "v7,s[32:35],s72 offen offset:128 nt lds\n"
MF "v[124:127],v[104:107],v[248:251],v[124:127]\n"
"s_add_u32 m0,s64,7936\n"
MF "v[128:131],v[104:107],v[252:255],v[128:131]\n"
LD "v7,s[32:35],s72 offen offset:256 nt lds\n"
"s_branch .Lt0e\n"
".Lpgt0k_f:\n"
"s_cbranch_execz .Lisr3\n"
".Lt0k:\n"
"s_lshl_b32 s58,s56,10\n"
"s_add_u32 s58,s58,73728\n"
"v_add_u32 v32,s58,v9\n"
"v_add_u32 v33,65536,v32\n"
"s_waitcnt lgkmcnt(2)\n"
MF "v[72:75],v[92:95],v[136:139],0\n"
"ds_read_b128 v[104:107],v1 offset:1024\n"
AR "v62,a20\n"
MF "v[76:79],v[92:95],v[140:143],0\n"
"ds_write_b128 v32,a[0:3] offset:24576\n"
MF "v[80:83],v[92:95],v[144:147],0\n"
"ds_write_b128 v32,a[4:7] offset:28672\n"
MF "v[84:87],v[92:95],v[148:151],0\n"
AR "v63,a21\n"
AR "v64,a22\n"
MF "v[88:91],v[92:95],v[152:155],0\n"
"s_waitcnt lgkmcnt(4)\n"
"ds_read_b128 v[92:95],v0 offset:2048\n"
AR "v65,a23\n"
MF "v[72:75],v[96:99],v[156:159],v[72:75]\n"
"ds_write_b128 v32,a[8:11] offset:32768\n"
MF "v[76:79],v[96:99],v[160:163],v[76:79]\n"
AR "v66,a24\n"
AR "v67,a25\n"
MF "v[80:83],v[96:99],v[164:167],v[80:83]\n"
"ds_write_b128 v32,a[12:15] offset:36864\n"
MF "v[84:87],v[96:99],v[168:171],v[84:87]\n"
AR "v68,a26\n"
AR "v69,a27\n"
MF "v[88:91],v[96:99],v[172:175],v[88:91]\n"
"s_waitcnt lgkmcnt(6)\n"
"ds_read_b128 v[96:99],v1 offset:2048\n"
AR "v70,a28\n"
MF "v[72:75],v[100:103],v[176:179],v[72:75]\n"
"ds_write_b128 v32,a[16:19] offset:40960\n"
MF "v[76:79],v[100:103],v[180:183],v[76:79]\n"
AR "v71,a29\n"
AR "v132,a30\n"
MF "v[80:83],v[100:103],v[184:187],v[80:83]\n"
AR "v133,a31\n"
AR "v134,a32\n"
MF "v[84:87],v[100:103],v[188:191],v[84:87]\n"
"ds_write_b128 v32,a[40:43] offset:45056\n"
MF "v[88:91],v[100:103],v[192:195],v[88:91]\n"
"s_waitcnt lgkmcnt(8)\n"
"ds_read_b128 v[100:103],v0 offset:6144\n"
AR "v135,a33\n"
MF "v[72:75],v[104:107],v[196:199],v[72:75]\n"
"ds_write_b128 v32,a[44:47] offset:49152\n"
MF "v[76:79],v[104:107],v[200:203],v[76:79]\n"
AR "v6,a34\n"
AR "v13,a35\n"
MF "v[80:83],v[104:107],v[204:207],v[80:83]\n"
AR "v14,a36\n"
AR "v15,a37\n"
MF "v[84:87],v[104:107],v[208:211],v[84:87]\n"
"ds_write_b128 v32,a[48:51] offset:53248\n"
MF "v[88:91],v[104:107],v[212:215],v[88:91]\n"
"s_waitcnt lgkmcnt(8)\n"
"ds_read_b128 v[104:107],v1 offset:6144\n"
"v_accvgpr_mov_b32 a254,a38\n"
MF "v[72:75],v[92:95],v[216:219],v[72:75]\n"
"v_accvgpr_mov_b32 a255,a39\n"
"v_accvgpr_mov_b32 a240,a168\n"
MF "v[76:79],v[92:95],v[220:223],v[76:79]\n"
"ds_write_b128 v32,a[52:55] offset:57344\n"
MF "v[80:83],v[92:95],v[224:227],v[80:83]\n"
"v_accvgpr_mov_b32 a241,a169\n"
"v_accvgpr_mov_b32 a242,a170\n"
MF "v[84:87],v[92:95],v[228:231],v[84:87]\n"
"ds_write_b128 v32,a[56:59] offset:61440\n"
MF "v[88:91],v[92:95],v[232:235],v[88:91]\n"
"s_waitcnt lgkmcnt(8)\n"
"ds_read_b128 v[92:95],v0 offset:7168\n"
"v_accvgpr_mov_b32 a243,a171\n"
MF "v[72:75],v[96:99],v[236:239],v[72:75]\n"
"v_accvgpr_mov_b32 a244,a172\n"
"v_accvgpr_mov_b32 a245,a173\n"
MF "v[76:79],v[96:99],v[240:243],v[76:79]\n"
"ds_write_b128 v33,a[80:83]\n"
MF "v[80:83],v[96:99],v[244:247],v[80:83]\n"
"v_accvgpr_mov_b32 a246,a174\n"
"v_accvgpr_mov_b32 a247,a175\n"
MF "v[84:87],v[96:99],v[248:251],v[84:87]\n"
"v_accvgpr_mov_b32 a248,a176\n"
"v_accvgpr_mov_b32 a249,a177\n"
MF "v[88:91],v[96:99],v[252:255],v[88:91]\n"
"s_waitcnt lgkmcnt(7)\n"
"ds_read_b128 v[96:99],v1 offset:7168\n"
"v_accvgpr_mov_b32 a250,a178\n"
MF "v[112:115],v[100:103],v[136:139],0\n"
"ds_write_b128 v33,a[84:87] offset:4096\n"
MF "v[116:119],v[100:103],v[140:143],0\n"
"ds_write_b128 v33,a[88:91] offset:8192\n"
MF "v[120:123],v[100:103],v[144:147],0\n"
"v_accvgpr_mov_b32 a251,a179\n"
MF "v[124:127],v[100:103],v[148:151],0\n"
".Lpg18f:\n"
"s_cbranch_execz .Lisr3\n"
MF "v[128:131],v[100:103],v[152:155],0\n"
"s_waitcnt lgkmcnt(7)\n"
"ds_read_b128 v[100:103],v0 offset:8192\n"
MF "v[112:115],v[104:107],v[156:159],v[112:115]\n"
"v_max3_f32 v52,v72,v73,v74\n"
"v_max_f32 v52,v52,v75\n"
MF "v[116:119],v[104:107],v[160:163],v[116:119]\n"
"v_max3_f32 v53,v76,v77,v78\n"
"v_max_f32 v53,v53,v79\n"
MF "v[120:123],v[104:107],v[164:167],v[120:123]\n"
"v_max3_f32 v54,v80,v81,v82\n"
"v_max_f32 v54,v54,v83\n"
MF "v[124:127],v[104:107],v[168:171],v[124:127]\n"
"v_max3_f32 v55,v84,v85,v86\n"
"v_max_f32 v55,v55,v87\n"
MF "v[128:131],v[104:107],v[172:175],v[128:131]\n"
"s_waitcnt lgkmcnt(5)\n"
"ds_read_b128 v[104:107],v1 offset:8192\n"
"v_max3_f32 v56,v88,v89,v90\n"
MF "v[112:115],v[92:95],v[176:179],v[112:115]\n"
"v_max_f32 v56,v56,v91\n"
MF "v[116:119],v[92:95],v[180:183],v[116:119]\n"
"ds_write_b128 v33,a[92:95] offset:12288\n"
MF "v[120:123],v[92:95],v[184:187],v[120:123]\n"
"ds_write_b128 v33,a[96:99] offset:16384\n"
MF "v[124:127],v[92:95],v[188:191],v[124:127]\n"
"ds_write_b128 v33,a[120:123] offset:20480\n"
MF "v[128:131],v[92:95],v[192:195],v[128:131]\n"
"s_waitcnt lgkmcnt(7)\n"
"ds_write_b128 v32,a[124:127]\n"
MF "v[112:115],v[96:99],v[196:199],v[112:115]\n"
"s_waitcnt lgkmcnt(0)\n"
MF "v[116:119],v[96:99],v[200:203],v[116:119]\n"
"s_barrier\n"
MF "v[120:123],v[96:99],v[204:207],v[120:123]\n"
"s_add_u32 s72,s72,6144\n"
"ds_write_b128 v32,a[128:131] offset:4096\n"
MF "v[124:127],v[96:99],v[208:211],v[124:127]\n"
"s_cmp_eq_u32 s72,s17\n"
"s_cselect_b32 s72,s16,s72\n"
"s_add_u32 m0,s64,0\n"
MF "v[128:131],v[96:99],v[212:215],v[128:131]\n"
LD "v7,s[32:35],s72 offen nt lds\n"
MF "v[112:115],v[100:103],v[216:219],v[112:115]\n"
"s_add_u32 m0,s64,896\n"
"ds_write_b128 v32,a[132:135] offset:8192\n"
MF "v[116:119],v[100:103],v[220:223],v[116:119]\n"
LD "v7,s[32:35],s72 offen offset:128 nt lds\n"
MF "v[120:123],v[100:103],v[224:227],v[120:123]\n"
"s_add_u32 m0,s64,1792\n"
"ds_write_b128 v32,a[136:139] offset:12288\n"
MF "v[124:127],v[100:103],v[228:231],v[124:127]\n"
LD "v7,s[32:35],s72 offen offset:256 nt lds\n"
MF "v[128:131],v[100:103],v[232:235],v[128:131]\n"
"s_add_u32 s72,s72,6144\n"
"s_add_u32 m0,s64,6144\n"
MF "v[112:115],v[104:107],v[236:239],v[112:115]\n"
LD "v7,s[32:35],s72 offen nt lds\n"
MF "v[116:119],v[104:107],v[240:243],v[116:119]\n"
"s_add_u32 m0,s64,7040\n"
"ds_write_b128 v32,a[160:163] offset:16384\n"
MF "v[120:123],v[104:107],v[244:247],v[120:123]\n"
LD "v7,s[32:35],s72 offen offset:128 nt lds\n"
MF "v[124:127],v[104:107],v[248:251],v[124:127]\n"
"s_add_u32 m0,s64,7936\n"
"ds_write_b128 v32,a[164:167] offset:20480\n"
MF "v[128:131],v[104:107],v[252:255],v[128:131]\n"
LD "v7,s[32:35],s72 offen offset:256 nt lds\n"
".Lpgt0e_g:\n"
"s_cbranch_execz .Lisr6\n"
".Lt0e:\n"
"s_nop 1\n"
"v_max3_f32 v52,v52,v112,v113\n"
"v_max3_f32 v53,v53,v116,v117\n"
"v_max3_f32 v54,v54,v120,v121\n"
"v_max3_f32 v55,v55,v124,v125\n"
"s_nop 1\n"
"v_max3_f32 v56,v56,v128,v129\n"
"v_max3_f32 v52,v52,v114,v115\n"
"v_max3_f32 v53,v53,v118,v119\n"
"v_max3_f32 v54,v54,v122,v123\n"
"v_max3_f32 v55,v55,v126,v127\n"
"v_max3_f32 v56,v56,v130,v131\n"
"v_mov_b32 v57,v52\n"
"v_mov_b32 v58,v53\n"
"v_mov_b32 v59,v54\n"
"v_mov_b32 v60,v55\n"
"v_mov_b32 v61,v56\n"
"v_permlane16_swap_b32 v52,v57\n"
"v_permlane16_swap_b32 v53,v58\n"
"v_permlane16_swap_b32 v54,v59\n"
"v_permlane16_swap_b32 v55,v60\n"
"v_permlane16_swap_b32 v56,v61\n"
"v_max_f32 v52,v52,v57\n"
"v_max_f32 v53,v53,v58\n"
"v_max_f32 v54,v54,v59\n"
"v_max_f32 v55,v55,v60\n"
"v_max_f32 v56,v56,v61\n"
"v_mov_b32 v57,v52\n"
"v_mov_b32 v58,v53\n"
"v_mov_b32 v59,v54\n"
"v_mov_b32 v60,v55\n"
"v_mov_b32 v61,v56\n"
"v_permlane32_swap_b32 v52,v57\n"
"v_permlane32_swap_b32 v53,v58\n"
"v_permlane32_swap_b32 v54,v59\n"
"v_permlane32_swap_b32 v55,v60\n"
"v_permlane32_swap_b32 v56,v61\n"
"v_max_f32 v52,v52,v57\n"
"v_max_f32 v53,v53,v58\n"
"v_max_f32 v54,v54,v59\n"
"v_max_f32 v55,v55,v60\n"
"v_max_f32_e64 v56,v56,v61\n"
"v_sub_f32 v32,0xc1300000,v52\n"
"v_sub_f32 v36,0xc1300000,v53\n"
"v_sub_f32 v40,0xc1300000,v54\n"
"v_sub_f32 v44,0xc1300000,v55\n"
"v_sub_f32 v48,0xc1300000,v56\n"
"v_mov_b32 v33,v32\n"
"v_pk_mov_b32 v[34:35],v[32:33],v[32:33]\n"
"v_mov_b32 v37,v36\n"
"v_pk_mov_b32 v[38:39],v[36:37],v[36:37]\n"
"v_mov_b32 v41,v40\n"
"v_pk_mov_b32 v[42:43],v[40:41],v[40:41]\n"
"v_mov_b32 v45,v44\n"
"v_pk_mov_b32 v[46:47],v[44:45],v[44:45]\n"
"v_mov_b32_e64 v49,v48\n"
"v_pk_mov_b32 v[50:51],v[48:49],v[48:49]\n"
"v_pk_add_f32 v[72:73],v[72:73],v[32:33]\n"
"v_pk_add_f32 v[74:75],v[74:75],v[32:33]\n"
"v_pk_add_f32 v[112:113],v[112:113],v[32:33]\n"
"v_pk_add_f32 v[114:115],v[114:115],v[32:33]\n"
"v_exp_f32 v72,v72\n"
"v_exp_f32 v73,v73\n"
"v_exp_f32 v74,v74\n"
"v_exp_f32 v75,v75\n"
"v_exp_f32 v112,v112\n"
"v_exp_f32 v113,v113\n"
"v_exp_f32 v114,v114\n"
"v_exp_f32 v115,v115\n"
"v_pk_add_f32 v[10:11],v[72:73],v[74:75]\n"
"v_pk_add_f32 v[28:29],v[112:113],v[114:115]\n"
"v_pk_add_f32 v[10:11],v[10:11],v[28:29]\n"
"v_add_f32_e64 v52,v10,v11\n"
CV "v72,v72,v73\n"
CV "v73,v74,v75\n"
CV "v74,v112,v113\n"
CV "v75,v114,v115\n"
"v_pk_add_f32 v[76:77],v[76:77],v[36:37]\n"
"v_pk_add_f32 v[78:79],v[78:79],v[36:37]\n"
"v_pk_add_f32 v[116:117],v[116:117],v[36:37]\n"
"v_pk_add_f32 v[118:119],v[118:119],v[36:37]\n"
"v_exp_f32 v76,v76\n"
"v_exp_f32 v77,v77\n"
"v_exp_f32 v78,v78\n"
"v_exp_f32 v79,v79\n"
"v_exp_f32 v116,v116\n"
"v_exp_f32 v117,v117\n"
"v_exp_f32 v118,v118\n"
"v_exp_f32 v119,v119\n"
"v_pk_add_f32 v[10:11],v[76:77],v[78:79]\n"
"v_pk_add_f32 v[28:29],v[116:117],v[118:119]\n"
"v_pk_add_f32 v[10:11],v[10:11],v[28:29]\n"
"v_add_f32_e64 v53,v10,v11\n"
CV "v76,v76,v77\n"
CV "v77,v78,v79\n"
CV "v78,v116,v117\n"
CV "v79,v118,v119\n"
"v_pk_add_f32 v[80:81],v[80:81],v[40:41]\n"
"v_pk_add_f32 v[82:83],v[82:83],v[40:41]\n"
"v_pk_add_f32 v[120:121],v[120:121],v[40:41]\n"
"v_pk_add_f32 v[122:123],v[122:123],v[40:41]\n"
"v_exp_f32 v80,v80\n"
"v_exp_f32 v81,v81\n"
"v_exp_f32 v82,v82\n"
"v_exp_f32 v83,v83\n"
"v_exp_f32 v120,v120\n"
"v_exp_f32 v121,v121\n"
"v_exp_f32 v122,v122\n"
"v_exp_f32 v123,v123\n"
"v_pk_add_f32 v[10:11],v[80:81],v[82:83]\n"
"v_pk_add_f32 v[28:29],v[120:121],v[122:123]\n"
"v_pk_add_f32 v[10:11],v[10:11],v[28:29]\n"
"v_add_f32_e64 v54,v10,v11\n"
CV "v80,v80,v81\n"
CV "v81,v82,v83\n"
CV "v82,v120,v121\n"
CV "v83,v122,v123\n"
"v_pk_add_f32 v[84:85],v[84:85],v[44:45]\n"
"v_pk_add_f32 v[86:87],v[86:87],v[44:45]\n"
"v_pk_add_f32 v[124:125],v[124:125],v[44:45]\n"
"v_pk_add_f32 v[126:127],v[126:127],v[44:45]\n"
"v_mov_b32_e64 v55,0\n"
"v_pk_add_f32 v[88:89],v[88:89],v[48:49]\n"
"v_pk_add_f32 v[90:91],v[90:91],v[48:49]\n"
"v_pk_add_f32 v[128:129],v[128:129],v[48:49]\n"
"v_pk_add_f32 v[130:131],v[130:131],v[48:49]\n"
"v_mov_b32 v56,0\n"
"s_waitcnt vmcnt(12) lgkmcnt(0)\n"
"s_barrier\n"
"ds_read_b128 v[16:19],v0 offset:24576\n"
"ds_read_b128 v[20:23],v1 offset:24576\n"
AW "a220,s13\n"
AW "a221,s13\n"
AW "a222,s13\n"
AW "a223,s13\n"
"s_waitcnt lgkmcnt(1)\n"
MF "v[92:95],v[16:19],v[136:139],v[32:35]\n"
"s_nop 0\n"
"ds_read_b128 v[24:27],v0 offset:25600\n"
"ds_read_b128 v[28:31],v1 offset:25600\n"
"s_waitcnt lgkmcnt(2)\n"
MF "v[92:95],v[20:23],v[156:159],v[92:95]\n"
"v_exp_f32 v84,v84\n"
MF "v[96:99],v[20:23],v[160:163],v[36:39]\n"
"v_exp_f32 v85,v85\n"
MF "v[96:99],v[16:19],v[140:143],v[96:99]\n"
"v_exp_f32 v86,v86\n"
MF "v[100:103],v[16:19],v[144:147],v[40:43]\n"
"v_exp_f32 v87,v87\n"
MF "v[100:103],v[20:23],v[164:167],v[100:103]\n"
"v_add_f32 v55,v55,v84\n"
"v_add_f32 v55,v55,v85\n"
MF "v[104:107],v[20:23],v[168:171],v[44:47]\n"
CV "v84,v84,v85\n"
"v_add_f32 v55,v55,v86\n"
MF "v[104:107],v[16:19],v[148:151],v[104:107]\n"
"v_add_f32 v55,v55,v87\n"
CV "v85,v86,v87\n"
MF "v[108:111],v[16:19],v[152:155],v[48:51]\n"
"v_exp_f32 v124,v124\n"
MF "v[108:111],v[20:23],v[172:175],v[108:111]\n"
"s_waitcnt lgkmcnt(0)\n"
"ds_read_b128 v[16:19],v0 offset:26624\n"
"ds_read_b128 v[20:23],v1 offset:26624\n"
MF "v[108:111],v[24:27],v[192:195],v[108:111]\n"
"v_exp_f32 v125,v125\n"
MF "v[108:111],v[28:31],v[212:215],v[108:111]\n"
"v_exp_f32 v126,v126\n"
MF "v[104:107],v[28:31],v[208:211],v[104:107]\n"
"v_exp_f32 v127,v127\n"
MF "v[104:107],v[24:27],v[188:191],v[104:107]\n"
"v_add_f32 v55,v55,v124\n"
"v_add_f32 v55,v55,v125\n"
MF "v[100:103],v[24:27],v[184:187],v[100:103]\n"
CV "v86,v124,v125\n"
"v_add_f32 v55,v55,v126\n"
MF "v[100:103],v[28:31],v[204:207],v[100:103]\n"
"v_add_f32 v55,v55,v127\n"
CV "v87,v126,v127\n"
MF "v[96:99],v[28:31],v[200:203],v[96:99]\n"
"v_exp_f32 v88,v88\n"
MF "v[96:99],v[24:27],v[180:183],v[96:99]\n"
"v_exp_f32 v89,v89\n"
MF "v[92:95],v[24:27],v[176:179],v[92:95]\n"
"v_exp_f32 v90,v90\n"
MF "v[92:95],v[28:31],v[196:199],v[92:95]\n"
"s_waitcnt lgkmcnt(0)\n"
"ds_read_b128 v[24:27],v0 offset:30720\n"
"ds_read_b128 v[28:31],v1 offset:30720\n"
MF "v[92:95],v[16:19],v[216:219],v[92:95]\n"
"v_exp_f32 v91,v91\n"
MF "v[92:95],v[20:23],v[236:239],v[92:95]\n"
"v_add_f32 v56,v56,v88\n"
"v_add_f32 v56,v56,v89\n"
MF "v[96:99],v[20:23],v[240:243],v[96:99]\n"
CV "v88,v88,v89\n"
"v_add_f32 v56,v56,v90\n"
MF "v[96:99],v[16:19],v[220:223],v[96:99]\n"
"v_add_f32 v56,v56,v91\n"
CV "v89,v90,v91\n"
MF "v[100:103],v[16:19],v[224:227],v[100:103]\n"
"v_exp_f32 v128,v128\n"
MF "v[100:103],v[20:23],v[244:247],v[100:103]\n"
"v_exp_f32 v129,v129\n"
MF "v[104:107],v[20:23],v[248:251],v[104:107]\n"
"v_exp_f32 v130,v130\n"
MF "v[104:107],v[16:19],v[228:231],v[104:107]\n"
"v_exp_f32 v131,v131\n"
MF "v[108:111],v[16:19],v[232:235],v[108:111]\n"
"v_add_f32 v56,v56,v128\n"
"v_add_f32 v56,v56,v129\n"
MF "v[108:111],v[20:23],v[252:255],v[108:111]\n"
"s_waitcnt lgkmcnt(0)\n"
"ds_read_b128 v[16:19],v0 offset:31744\n"
"ds_read_b128 v[20:23],v1 offset:31744\n"
MF "v[112:115],v[24:27],v[136:139],v[32:35]\n"
CV "v90,v128,v129\n"
"v_add_f32 v56,v56,v130\n"
MF "v[112:115],v[28:31],v[156:159],v[112:115]\n"
"v_add_f32 v56,v56,v131\n"
CV "v91,v130,v131\n"
MF "v[116:119],v[28:31],v[160:163],v[36:39]\n"
"v_exp_f32 v92,v92\n"
MF "v[116:119],v[24:27],v[140:143],v[116:119]\n"
"v_exp_f32 v93,v93\n"
MF "v[120:123],v[24:27],v[144:147],v[40:43]\n"
"v_exp_f32 v94,v94\n"
MF "v[120:123],v[28:31],v[164:167],v[120:123]\n"
"v_exp_f32 v95,v95\n"
MF "v[124:127],v[28:31],v[168:171],v[44:47]\n"
"v_add_f32 v57,v52,v92\n"
"v_add_f32 v57,v57,v93\n"
MF "v[124:127],v[24:27],v[148:151],v[124:127]\n"
CV "v92,v92,v93\n"
"v_add_f32 v57,v57,v94\n"
MF "v[128:131],v[24:27],v[152:155],v[48:51]\n"
"v_add_f32 v57,v57,v95\n"
CV "v93,v94,v95\n"
MF "v[128:131],v[28:31],v[172:175],v[128:131]\n"
"s_waitcnt lgkmcnt(0)\n"
"ds_read_b128 v[24:27],v0 offset:32768\n"
"ds_read_b128 v[28:31],v1 offset:32768\n"
MF "v[128:131],v[16:19],v[192:195],v[128:131]\n"
"v_exp_f32 v96,v96\n"
MF "v[128:131],v[20:23],v[212:215],v[128:131]\n"
"v_exp_f32 v97,v97\n"
MF "v[124:127],v[20:23],v[208:211],v[124:127]\n"
"v_exp_f32 v98,v98\n"
MF "v[124:127],v[16:19],v[188:191],v[124:127]\n"
"v_exp_f32 v99,v99\n"
MF "v[120:123],v[16:19],v[184:187],v[120:123]\n"
"v_add_f32 v58,v53,v96\n"
"v_add_f32 v58,v58,v97\n"
MF "v[120:123],v[20:23],v[204:207],v[120:123]\n"
CV "v96,v96,v97\n"
"v_add_f32 v58,v58,v98\n"
MF "v[116:119],v[20:23],v[200:203],v[116:119]\n"
"s_waitcnt vmcnt(6) lgkmcnt(0)\n"
"v_add_f32 v58,v58,v99\n"
CV "v97,v98,v99\n"
MF "v[116:119],v[16:19],v[180:183],v[116:119]\n"
"s_barrier\n"
MF "v[112:115],v[16:19],v[176:179],v[112:115]\n"
"v_exp_f32 v100,v100\n"
MF "v[112:115],v[20:23],v[196:199],v[112:115]\n"
"s_nop 0\n"
TR "v[16:17],v2 offset:49152\n"
TR "v[18:19],v2 offset:55296\n"
MF "v[112:115],v[24:27],v[216:219],v[112:115]\n"
"s_add_u32 s73,s73,12288\n"
TR "v[20:21],v3 offset:49152\n"
TR "v[22:23],v3 offset:55296\n"
MF "v[112:115],v[28:31],v[236:239],v[112:115]\n"
"s_cmp_eq_u32 s73,s19\n"
"s_cselect_b32 s73,s18,s73\n"
"s_add_u32 m0,s65,24576\n"
MF "v[116:119],v[28:31],v[240:243],v[116:119]\n"
LD "v8,s[36:39],s73 offen nt lds\n"
MF "v[116:119],v[24:27],v[220:223],v[116:119]\n"
"s_add_u32 m0,s65,25472\n"
"v_exp_f32 v101,v101\n"
MF "v[120:123],v[24:27],v[224:227],v[120:123]\n"
LD "v8,s[36:39],s73 offen offset:128 nt lds\n"
MF "v[120:123],v[28:31],v[244:247],v[120:123]\n"
"v_exp_f32 v102,v102\n"
MF "v[124:127],v[28:31],v[248:251],v[124:127]\n"
"s_add_u32 m0,s65,26368\n"
"v_exp_f32 v103,v103\n"
MF "v[124:127],v[24:27],v[228:231],v[124:127]\n"
LD "v8,s[36:39],s73 offen offset:256 nt lds\n"
MF "v[128:131],v[24:27],v[232:235],v[128:131]\n"
"v_add_f32 v59,v54,v100\n"
"v_add_f32 v59,v59,v101\n"
MF "v[128:131],v[28:31],v[252:255],v[128:131]\n"
"s_waitcnt lgkmcnt(0)\n"
TR "v[24:25],v4 offset:49152\n"
TR "v[26:27],v4 offset:55296\n"
MF "a[0:3],v[16:19],v[72:75],a[220:223]\n"
"s_add_u32 m0,s65,24576\n"
TR "v[28:29],v5 offset:49152\n"
TR "v[30:31],v5 offset:55296\n"
MF "a[20:23],v[20:23],v[72:75],a[220:223]\n"
LD "v8,s[36:39],s73 offen offset:3072 nt lds\n"
MF "a[24:27],v[20:23],v[76:79],a[220:223]\n"
CV "v100,v100,v101\n"
"v_add_f32 v59,v59,v102\n"
MF "a[4:7],v[16:19],v[76:79],a[220:223]\n"
"s_add_u32 m0,s65,25472\n"
"v_add_f32 v59,v59,v103\n"
CV "v101,v102,v103\n"
MF "a[8:11],v[16:19],v[80:83],a[220:223]\n"
LD "v8,s[36:39],s73 offen offset:3200 nt lds\n"
MF "a[28:31],v[20:23],v[80:83],a[220:223]\n"
"v_exp_f32 v104,v104\n"
MF "a[32:35],v[20:23],v[84:87],a[220:223]\n"
"s_add_u32 m0,s65,26368\n"
"v_exp_f32 v105,v105\n"
MF "a[12:15],v[16:19],v[84:87],a[220:223]\n"
LD "v8,s[36:39],s73 offen offset:3328 nt lds\n"
MF "a[16:19],v[16:19],v[88:91],a[220:223]\n"
"v_exp_f32 v106,v106\n"
MF "a[36:39],v[20:23],v[88:91],a[220:223]\n"
"s_waitcnt lgkmcnt(0)\n"
TR "v[16:17],v2 offset:50176\n"
TR "v[18:19],v2 offset:56320\n"
MF "a[56:59],v[24:27],v[88:91],a[220:223]\n"
"s_nop 0\n"
TR "v[20:21],v3 offset:50176\n"
TR "v[22:23],v3 offset:56320\n"
MF "a[76:79],v[28:31],v[88:91],a[220:223]\n"
"v_exp_f32 v107,v107\n"
MF "a[72:75],v[28:31],v[84:87],a[220:223]\n"
"v_add_f32 v60,v55,v104\n"
"v_add_f32 v60,v60,v105\n"
MF "a[52:55],v[24:27],v[84:87],a[220:223]\n"
CV "v104,v104,v105\n"
"v_add_f32 v60,v60,v106\n"
MF "a[48:51],v[24:27],v[80:83],a[220:223]\n"
"v_add_f32 v60,v60,v107\n"
CV "v105,v106,v107\n"
MF "a[68:71],v[28:31],v[80:83],a[220:223]\n"
"v_exp_f32 v108,v108\n"
MF "a[64:67],v[28:31],v[76:79],a[220:223]\n"
"v_exp_f32 v109,v109\n"
MF "a[44:47],v[24:27],v[76:79],a[220:223]\n"
"v_exp_f32 v110,v110\n"
MF "a[40:43],v[24:27],v[72:75],a[220:223]\n"
"v_exp_f32 v111,v111\n"
MF "a[60:63],v[28:31],v[72:75],a[220:223]\n"
"s_waitcnt lgkmcnt(0)\n"
TR "v[24:25],v4 offset:50176\n"
TR "v[26:27],v4 offset:56320\n"
MF "a[80:83],v[16:19],v[72:75],a[220:223]\n"
"s_nop 0\n"
TR "v[28:29],v5 offset:50176\n"
TR "v[30:31],v5 offset:56320\n"
MF "a[100:103],v[20:23],v[72:75],a[220:223]\n"
"v_add_f32 v61,v56,v108\n"
"v_add_f32 v61,v61,v109\n"
MF "a[104:107],v[20:23],v[76:79],a[220:223]\n"
CV "v108,v108,v109\n"
"v_add_f32 v61,v61,v110\n"
MF "a[84:87],v[16:19],v[76:79],a[220:223]\n"
"v_add_f32 v61,v61,v111\n"
CV "v109,v110,v111\n"
MF "a[88:91],v[16:19],v[80:83],a[220:223]\n"
"v_exp_f32 v112,v112\n"
MF "a[108:111],v[20:23],v[80:83],a[220:223]\n"
"v_exp_f32 v113,v113\n"
MF "a[112:115],v[20:23],v[84:87],a[220:223]\n"
"v_exp_f32 v114,v114\n"
MF "a[92:95],v[16:19],v[84:87],a[220:223]\n"
"v_exp_f32 v115,v115\n"
MF "a[96:99],v[16:19],v[88:91],a[220:223]\n"
"v_add_f32 v57,v57,v112\n"
"v_add_f32 v57,v57,v113\n"
MF "a[116:119],v[20:23],v[88:91],a[220:223]\n"
"s_waitcnt lgkmcnt(0)\n"
TR "v[16:17],v2 offset:51200\n"
TR "v[18:19],v2 offset:57344\n"
MF "a[136:139],v[24:27],v[88:91],a[220:223]\n"
"s_nop 0\n"
TR "v[20:21],v3 offset:51200\n"
TR "v[22:23],v3 offset:57344\n"
MF "a[156:159],v[28:31],v[88:91],a[220:223]\n"
CV "v94,v112,v113\n"
"v_add_f32 v57,v57,v114\n"
MF "a[152:155],v[28:31],v[84:87],a[220:223]\n"
"v_add_f32 v57,v57,v115\n"
CV "v95,v114,v115\n"
MF "a[132:135],v[24:27],v[84:87],a[220:223]\n"
"v_exp_f32 v116,v116\n"
MF "a[128:131],v[24:27],v[80:83],a[220:223]\n"
"v_exp_f32 v117,v117\n"
MF "a[148:151],v[28:31],v[80:83],a[220:223]\n"
"v_exp_f32 v118,v118\n"
MF "a[144:147],v[28:31],v[76:79],a[220:223]\n"
"v_exp_f32 v119,v119\n"
MF "a[124:127],v[24:27],v[76:79],a[220:223]\n"
"v_add_f32 v58,v58,v116\n"
"v_add_f32 v58,v58,v117\n"
MF "a[120:123],v[24:27],v[72:75],a[220:223]\n"
CV "v98,v116,v117\n"
"v_add_f32 v58,v58,v118\n"
MF "a[140:143],v[28:31],v[72:75],a[220:223]\n"
"s_waitcnt lgkmcnt(0)\n"
TR "v[24:25],v4 offset:51200\n"
TR "v[26:27],v4 offset:57344\n"
MF "a[160:163],v[16:19],v[72:75],a[220:223]\n"
"s_nop 0\n"
TR "v[28:29],v5 offset:51200\n"
TR "v[30:31],v5 offset:57344\n"
MF "a[180:183],v[20:23],v[72:75],a[220:223]\n"
"v_add_f32 v58,v58,v119\n"
CV "v99,v118,v119\n"
MF "a[184:187],v[20:23],v[76:79],a[220:223]\n"
"v_exp_f32 v120,v120\n"
MF "a[164:167],v[16:19],v[76:79],a[220:223]\n"
"v_exp_f32 v121,v121\n"
MF "a[168:171],v[16:19],v[80:83],a[220:223]\n"
"v_exp_f32 v122,v122\n"
MF "a[188:191],v[20:23],v[80:83],a[220:223]\n"
"v_exp_f32 v123,v123\n"
MF "a[192:195],v[20:23],v[84:87],a[220:223]\n"
"v_add_f32 v59,v59,v120\n"
"v_add_f32 v59,v59,v121\n"
MF "a[172:175],v[16:19],v[84:87],a[220:223]\n"
"s_waitcnt vmcnt(6) lgkmcnt(0)\n"
CV "v102,v120,v121\n"
"v_add_f32 v59,v59,v122\n"
MF "a[176:179],v[16:19],v[88:91],a[220:223]\n"
"s_barrier\n"
MF "a[196:199],v[20:23],v[88:91],a[220:223]\n"
"s_add_u32 s72,s72,6144\n"
"ds_read_b128 v[16:19],v0\n"
"ds_read_b128 v[20:23],v1\n"
MF "a[216:219],v[24:27],v[88:91],a[220:223]\n"
"s_cmp_eq_u32 s72,s17\n"
"s_cselect_b32 s72,s16,s72\n"
"s_add_u32 m0,s64,49152\n"
MF "a[236:239],v[28:31],v[88:91],a[220:223]\n"
LD "v7,s[32:35],s72 offen nt lds\n"
MF "a[232:235],v[28:31],v[84:87],a[220:223]\n"
"s_add_u32 m0,s64,50048\n"
"v_add_f32 v59,v59,v123\n"
CV "v103,v122,v123\n"
MF "a[212:215],v[24:27],v[84:87],a[220:223]\n"
LD "v7,s[32:35],s72 offen offset:128 nt lds\n"
MF "a[208:211],v[24:27],v[80:83],a[220:223]\n"
"s_add_u32 m0,s64,50944\n"
MF "a[228:231],v[28:31],v[80:83],a[220:223]\n"
LD "v7,s[32:35],s72 offen offset:256 nt lds\n"
MF "a[224:227],v[28:31],v[76:79],a[220:223]\n"
MF "a[204:207],v[24:27],v[76:79],a[220:223]\n"
MF "a[200:203],v[24:27],v[72:75],a[220:223]\n"
MF "a[220:223],v[28:31],v[72:75],a[220:223]\n"
"s_waitcnt lgkmcnt(0)\n"
".Laloop:\n"
MF "v[72:75],v[16:19],v[136:139],v[32:35]\n"
"s_nop 0\n"
"ds_read_b128 v[24:27],v0 offset:1024\n"
"ds_read_b128 v[28:31],v1 offset:1024\n"
MF "v[72:75],v[20:23],v[156:159],v[72:75]\n"
MF "v[76:79],v[20:23],v[160:163],v[36:39]\n"
MF "v[76:79],v[16:19],v[140:143],v[76:79]\n"
MF "v[80:83],v[16:19],v[144:147],v[40:43]\n"
MF "v[80:83],v[20:23],v[164:167],v[80:83]\n"
MF "v[84:87],v[20:23],v[168:171],v[44:47]\n"
MF "v[84:87],v[16:19],v[148:151],v[84:87]\n"
"s_add_u32 s72,s72,6144\n"
"s_add_u32 m0,s64,55296\n"
MF "v[88:91],v[16:19],v[152:155],v[48:51]\n"
LD "v7,s[32:35],s72 offen nt lds\n"
MF "v[88:91],v[20:23],v[172:175],v[88:91]\n"
"s_waitcnt lgkmcnt(0)\n"
"ds_read_b128 v[16:19],v0 offset:2048\n"
"ds_read_b128 v[20:23],v1 offset:2048\n"
MF "v[88:91],v[24:27],v[192:195],v[88:91]\n"
"v_exp_f32 v124,v124\n"
MF "v[88:91],v[28:31],v[212:215],v[88:91]\n"
"s_add_u32 m0,s64,56192\n"
"v_exp_f32 v125,v125\n"
MF "v[84:87],v[28:31],v[208:211],v[84:87]\n"
LD "v7,s[32:35],s72 offen offset:128 nt lds\n"
MF "v[84:87],v[24:27],v[188:191],v[84:87]\n"
"v_exp_f32 v126,v126\n"
MF "v[80:83],v[24:27],v[184:187],v[80:83]\n"
"v_exp_f32 v127,v127\n"
MF "v[80:83],v[28:31],v[204:207],v[80:83]\n"
"s_add_u32 m0,s64,57088\n"
"v_add_f32 v60,v60,v124\n"
"v_add_f32 v60,v60,v125\n"
MF "v[76:79],v[28:31],v[200:203],v[76:79]\n"
LD "v7,s[32:35],s72 offen offset:256 nt lds\n"
MF "v[76:79],v[24:27],v[180:183],v[76:79]\n"
CV "v106,v124,v125\n"
"v_add_f32 v60,v60,v126\n"
MF "v[72:75],v[24:27],v[176:179],v[72:75]\n"
"v_add_f32 v60,v60,v127\n"
CV "v107,v126,v127\n"
MF "v[72:75],v[28:31],v[196:199],v[72:75]\n"
"s_waitcnt lgkmcnt(0)\n"
"ds_read_b128 v[24:27],v0 offset:6144\n"
"ds_read_b128 v[28:31],v1 offset:6144\n"
MF "v[72:75],v[16:19],v[216:219],v[72:75]\n"
"v_exp_f32 v128,v128\n"
MF "v[72:75],v[20:23],v[236:239],v[72:75]\n"
"v_exp_f32 v129,v129\n"
MF "v[76:79],v[20:23],v[240:243],v[76:79]\n"
"v_exp_f32 v130,v130\n"
MF "v[76:79],v[16:19],v[220:223],v[76:79]\n"
"v_exp_f32 v131,v131\n"
MF "v[80:83],v[16:19],v[224:227],v[80:83]\n"
"v_add_f32 v61,v61,v128\n"
"v_add_f32 v61,v61,v129\n"
MF "v[80:83],v[20:23],v[244:247],v[80:83]\n"
CV "v110,v128,v129\n"
"v_add_f32 v61,v61,v130\n"
MF "v[84:87],v[20:23],v[248:251],v[84:87]\n"
"v_add_f32 v61,v61,v131\n"
CV "v111,v130,v131\n"
MF "v[84:87],v[16:19],v[228:231],v[84:87]\n"
"v_max3_f32 v10,v57,v58,v59\n"
"v_max3_f32 v10,v10,v60,v61\n"
MF "v[88:91],v[16:19],v[232:235],v[88:91]\n"
"v_cmp_ngt_f32 vcc,s77,v10\n"
MF "v[88:91],v[20:23],v[252:255],v[88:91]\n"
"s_waitcnt lgkmcnt(0)\n"
"s_cbranch_vccnz .Lstub_20\n"
".Lback_20:\n"
MF "v[112:115],v[24:27],v[136:139],v[32:35]\n"
"s_add_u32 s74,s74,12288\n"
"ds_read_b128 v[16:19],v0 offset:7168\n"
"ds_read_b128 v[20:23],v1 offset:7168\n"
MF "v[112:115],v[28:31],v[156:159],v[112:115]\n"
"s_cmp_eq_u32 s74,s20\n"
"s_cselect_b32 s74,s70,s74\n"
MF "v[116:119],v[28:31],v[160:163],v[36:39]\n"
"v_exp_f32 v72,v72\n"
MF "v[116:119],v[24:27],v[140:143],v[116:119]\n"
"v_exp_f32 v73,v73\n"
MF "v[120:123],v[24:27],v[144:147],v[40:43]\n"
"v_exp_f32 v74,v74\n"
MF "v[120:123],v[28:31],v[164:167],v[120:123]\n"
"v_exp_f32 v75,v75\n"
MF "v[124:127],v[28:31],v[168:171],v[44:47]\n"
"v_add_f32 v52,v57,v72\n"
"v_add_f32 v52,v52,v73\n"
MF "v[124:127],v[24:27],v[148:151],v[124:127]\n"
CV "v72,v72,v73\n"
"v_add_f32 v52,v52,v74\n"
MF "v[128:131],v[24:27],v[152:155],v[48:51]\n"
"v_add_f32 v52,v52,v75\n"
CV "v73,v74,v75\n"
MF "v[128:131],v[28:31],v[172:175],v[128:131]\n"
"s_waitcnt lgkmcnt(0)\n"
"ds_read_b128 v[24:27],v0 offset:8192\n"
"ds_read_b128 v[28:31],v1 offset:8192\n"
MF "v[128:131],v[16:19],v[192:195],v[128:131]\n"
"v_exp_f32 v76,v76\n"
MF "v[128:131],v[20:23],v[212:215],v[128:131]\n"
"v_exp_f32 v77,v77\n"
MF "v[124:127],v[20:23],v[208:211],v[124:127]\n"
"v_exp_f32 v78,v78\n"
MF "v[124:127],v[16:19],v[188:191],v[124:127]\n"
"v_exp_f32 v79,v79\n"
MF "v[120:123],v[16:19],v[184:187],v[120:123]\n"
"v_add_f32 v53,v58,v76\n"
"v_add_f32 v53,v53,v77\n"
MF "v[120:123],v[20:23],v[204:207],v[120:123]\n"
CV "v76,v76,v77\n"
"v_add_f32 v53,v53,v78\n"
MF "v[116:119],v[20:23],v[200:203],v[116:119]\n"
"s_waitcnt vmcnt(6) lgkmcnt(0)\n"
"v_add_f32 v53,v53,v79\n"
CV "v77,v78,v79\n"
MF "v[116:119],v[16:19],v[180:183],v[116:119]\n"
"s_barrier\n"
MF "v[112:115],v[16:19],v[176:179],v[112:115]\n"
"v_exp_f32 v80,v80\n"
MF "v[112:115],v[20:23],v[196:199],v[112:115]\n"
"s_nop 0\n"
TR "v[16:17],v2 offset:24576\n"
TR "v[18:19],v2 offset:30720\n"
MF "v[112:115],v[24:27],v[216:219],v[112:115]\n"
"s_add_u32 s73,s73,12288\n"
TR "v[20:21],v3 offset:24576\n"
TR "v[22:23],v3 offset:30720\n"
MF "v[112:115],v[28:31],v[236:239],v[112:115]\n"
"s_cmp_eq_u32 s73,s19\n"
"s_cselect_b32 s73,s18,s73\n"
"s_add_u32 m0,s65,0\n"
MF "v[116:119],v[28:31],v[240:243],v[116:119]\n"
LD "v8,s[36:39],s73 offen nt lds\n"
MF "v[116:119],v[24:27],v[220:223],v[116:119]\n"
"s_add_u32 m0,s65,896\n"
"v_exp_f32 v81,v81\n"
MF "v[120:123],v[24:27],v[224:227],v[120:123]\n"
LD "v8,s[36:39],s73 offen offset:128 nt lds\n"
MF "v[120:123],v[28:31],v[244:247],v[120:123]\n"
"v_exp_f32 v82,v82\n"
MF "v[124:127],v[28:31],v[248:251],v[124:127]\n"
"s_add_u32 m0,s65,1792\n"
"v_exp_f32 v83,v83\n"
MF "v[124:127],v[24:27],v[228:231],v[124:127]\n"
LD "v8,s[36:39],s73 offen offset:256 nt lds\n"
MF "v[128:131],v[24:27],v[232:235],v[128:131]\n"
"v_add_f32 v54,v59,v80\n"
"v_add_f32 v54,v54,v81\n"
MF "v[128:131],v[28:31],v[252:255],v[128:131]\n"
"s_waitcnt lgkmcnt(0)\n"
TR "v[24:25],v4 offset:24576\n"
TR "v[26:27],v4 offset:30720\n"
MF "a[0:3],v[16:19],v[92:95],a[0:3]\n"
"s_add_u32 m0,s65,0\n"
TR "v[28:29],v5 offset:24576\n"
TR "v[30:31],v5 offset:30720\n"
MF "a[20:23],v[20:23],v[92:95],a[20:23]\n"
LD "v8,s[36:39],s73 offen offset:3072 nt lds\n"
MF "a[24:27],v[20:23],v[96:99],a[24:27]\n"
CV "v80,v80,v81\n"
"v_add_f32 v54,v54,v82\n"
MF "a[4:7],v[16:19],v[96:99],a[4:7]\n"
"s_add_u32 m0,s65,896\n"
"v_add_f32 v54,v54,v83\n"
CV "v81,v82,v83\n"
MF "a[8:11],v[16:19],v[100:103],a[8:11]\n"
LD "v8,s[36:39],s73 offen offset:3200 nt lds\n"
MF "a[28:31],v[20:23],v[100:103],a[28:31]\n"
"v_exp_f32 v84,v84\n"
MF "a[32:35],v[20:23],v[104:107],a[32:35]\n"
"s_add_u32 m0,s65,1792\n"
"v_exp_f32 v85,v85\n"
MF "a[12:15],v[16:19],v[104:107],a[12:15]\n"
LD "v8,s[36:39],s73 offen offset:3328 nt lds\n"
MF "a[16:19],v[16:19],v[108:111],a[16:19]\n"
"v_exp_f32 v86,v86\n"
MF "a[36:39],v[20:23],v[108:111],a[36:39]\n"
"s_waitcnt lgkmcnt(0)\n"
TR "v[16:17],v2 offset:25600\n"
TR "v[18:19],v2 offset:31744\n"
MF "a[56:59],v[24:27],v[108:111],a[56:59]\n"
"s_nop 0\n"
TR "v[20:21],v3 offset:25600\n"
TR "v[22:23],v3 offset:31744\n"
MF "a[76:79],v[28:31],v[108:111],a[76:79]\n"
"v_exp_f32 v87,v87\n"
MF "a[72:75],v[28:31],v[104:107],a[72:75]\n"
"v_add_f32 v55,v60,v84\n"
"v_add_f32 v55,v55,v85\n"
MF "a[52:55],v[24:27],v[104:107],a[52:55]\n"
CV "v84,v84,v85\n"
"v_add_f32 v55,v55,v86\n"
MF "a[48:51],v[24:27],v[100:103],a[48:51]\n"
"v_add_f32 v55,v55,v87\n"
CV "v85,v86,v87\n"
MF "a[68:71],v[28:31],v[100:103],a[68:71]\n"
"v_exp_f32 v88,v88\n"
MF "a[64:67],v[28:31],v[96:99],a[64:67]\n"
"v_exp_f32 v89,v89\n"
MF "a[44:47],v[24:27],v[96:99],a[44:47]\n"
"v_exp_f32 v90,v90\n"
MF "a[40:43],v[24:27],v[92:95],a[40:43]\n"
"v_exp_f32 v91,v91\n"
MF "a[60:63],v[28:31],v[92:95],a[60:63]\n"
"s_waitcnt lgkmcnt(0)\n"
TR "v[24:25],v4 offset:25600\n"
TR "v[26:27],v4 offset:31744\n"
MF "a[80:83],v[16:19],v[92:95],a[80:83]\n"
"s_nop 0\n"
TR "v[28:29],v5 offset:25600\n"
TR "v[30:31],v5 offset:31744\n"
MF "a[100:103],v[20:23],v[92:95],a[100:103]\n"
"v_add_f32 v56,v61,v88\n"
"v_add_f32 v56,v56,v89\n"
MF "a[104:107],v[20:23],v[96:99],a[104:107]\n"
CV "v88,v88,v89\n"
"v_add_f32 v56,v56,v90\n"
MF "a[84:87],v[16:19],v[96:99],a[84:87]\n"
"v_add_f32 v56,v56,v91\n"
CV "v89,v90,v91\n"
MF "a[88:91],v[16:19],v[100:103],a[88:91]\n"
"v_exp_f32 v112,v112\n"
MF "a[108:111],v[20:23],v[100:103],a[108:111]\n"
"v_exp_f32 v113,v113\n"
MF "a[112:115],v[20:23],v[104:107],a[112:115]\n"
"v_exp_f32 v114,v114\n"
MF "a[92:95],v[16:19],v[104:107],a[92:95]\n"
"v_exp_f32 v115,v115\n"
MF "a[96:99],v[16:19],v[108:111],a[96:99]\n"
"v_add_f32 v52,v52,v112\n"
"v_add_f32 v52,v52,v113\n"
MF "a[116:119],v[20:23],v[108:111],a[116:119]\n"
"s_waitcnt lgkmcnt(0)\n"
TR "v[16:17],v2 offset:26624\n"
TR "v[18:19],v2 offset:32768\n"
MF "a[136:139],v[24:27],v[108:111],a[136:139]\n"
"s_nop 0\n"
TR "v[20:21],v3 offset:26624\n"
TR "v[22:23],v3 offset:32768\n"
MF "a[156:159],v[28:31],v[108:111],a[156:159]\n"
CV "v74,v112,v113\n"
"v_add_f32 v52,v52,v114\n"
MF "a[152:155],v[28:31],v[104:107],a[152:155]\n"
"v_add_f32 v52,v52,v115\n"
CV "v75,v114,v115\n"
MF "a[132:135],v[24:27],v[104:107],a[132:135]\n"
"v_exp_f32 v116,v116\n"
MF "a[128:131],v[24:27],v[100:103],a[128:131]\n"
"v_exp_f32 v117,v117\n"
MF "a[148:151],v[28:31],v[100:103],a[148:151]\n"
"v_exp_f32 v118,v118\n"
MF "a[144:147],v[28:31],v[96:99],a[144:147]\n"
"v_exp_f32 v119,v119\n"
MF "a[124:127],v[24:27],v[96:99],a[124:127]\n"
"v_add_f32 v53,v53,v116\n"
"v_add_f32 v53,v53,v117\n"
MF "a[120:123],v[24:27],v[92:95],a[120:123]\n"
CV "v78,v116,v117\n"
"v_add_f32 v53,v53,v118\n"
MF "a[140:143],v[28:31],v[92:95],a[140:143]\n"
"s_waitcnt lgkmcnt(0)\n"
TR "v[24:25],v4 offset:26624\n"
TR "v[26:27],v4 offset:32768\n"
MF "a[160:163],v[16:19],v[92:95],a[160:163]\n"
"s_nop 0\n"
TR "v[28:29],v5 offset:26624\n"
TR "v[30:31],v5 offset:32768\n"
MF "a[180:183],v[20:23],v[92:95],a[180:183]\n"
"v_add_f32 v53,v53,v119\n"
CV "v79,v118,v119\n"
MF "a[184:187],v[20:23],v[96:99],a[184:187]\n"
"v_exp_f32 v120,v120\n"
MF "a[164:167],v[16:19],v[96:99],a[164:167]\n"
"v_exp_f32 v121,v121\n"
MF "a[168:171],v[16:19],v[100:103],a[168:171]\n"
"v_exp_f32 v122,v122\n"
MF "a[188:191],v[20:23],v[100:103],a[188:191]\n"
"v_exp_f32 v123,v123\n"
MF "a[192:195],v[20:23],v[104:107],a[192:195]\n"
"v_add_f32 v54,v54,v120\n"
"v_add_f32 v54,v54,v121\n"
MF "a[172:175],v[16:19],v[104:107],a[172:175]\n"
"s_waitcnt vmcnt(6) lgkmcnt(0)\n"
CV "v82,v120,v121\n"
"v_add_f32 v54,v54,v122\n"
MF "a[176:179],v[16:19],v[108:111],a[176:179]\n"
"s_barrier\n"
MF "a[196:199],v[20:23],v[108:111],a[196:199]\n"
"s_add_u32 s72,s72,6144\n"
"ds_read_b128 v[16:19],v0 offset:49152\n"
"ds_read_b128 v[20:23],v1 offset:49152\n"
MF "a[216:219],v[24:27],v[108:111],a[216:219]\n"
"s_cmp_eq_u32 s72,s17\n"
"s_cselect_b32 s72,s16,s72\n"
"s_add_u32 m0,s64,24576\n"
MF "a[236:239],v[28:31],v[108:111],a[236:239]\n"
LD "v7,s[32:35],s72 offen nt lds\n"
MF "a[232:235],v[28:31],v[104:107],a[232:235]\n"
"s_add_u32 m0,s64,25472\n"
"v_add_f32 v54,v54,v123\n"
CV "v83,v122,v123\n"
MF "a[212:215],v[24:27],v[104:107],a[212:215]\n"
LD "v7,s[32:35],s72 offen offset:128 nt lds\n"
MF "a[208:211],v[24:27],v[100:103],a[208:211]\n"
"s_add_u32 m0,s64,26368\n"
MF "a[228:231],v[28:31],v[100:103],a[228:231]\n"
LD "v7,s[32:35],s72 offen offset:256 nt lds\n"
MF "a[224:227],v[28:31],v[96:99],a[224:227]\n"
MF "a[204:207],v[24:27],v[96:99],a[204:207]\n"
MF "a[200:203],v[24:27],v[92:95],a[200:203]\n"
MF "a[220:223],v[28:31],v[92:95],a[220:223]\n"
"s_waitcnt lgkmcnt(0)\n"
MF "v[92:95],v[16:19],v[136:139],v[32:35]\n"
"s_add_u32 s72,s72,6144\n"
"ds_read_b128 v[24:27],v0 offset:50176\n"
"ds_read_b128 v[28:31],v1 offset:50176\n"
MF "v[92:95],v[20:23],v[156:159],v[92:95]\n"
"s_add_u32 m0,s64,30720\n"
"v_exp_f32 v124,v124\n"
MF "v[96:99],v[20:23],v[160:163],v[36:39]\n"
LD "v7,s[32:35],s72 offen nt lds\n"
MF "v[96:99],v[16:19],v[140:143],v[96:99]\n"
"v_exp_f32 v125,v125\n"
MF "v[100:103],v[16:19],v[144:147],v[40:43]\n"
"s_add_u32 m0,s64,31616\n"
"v_exp_f32 v126,v126\n"
MF "v[100:103],v[20:23],v[164:167],v[100:103]\n"
LD "v7,s[32:35],s72 offen offset:128 nt lds\n"
MF "v[104:107],v[20:23],v[168:171],v[44:47]\n"
"v_exp_f32 v127,v127\n"
MF "v[104:107],v[16:19],v[148:151],v[104:107]\n"
"s_add_u32 m0,s64,32512\n"
"v_add_f32 v55,v55,v124\n"
"v_add_f32 v55,v55,v125\n"
MF "v[108:111],v[16:19],v[152:155],v[48:51]\n"
LD "v7,s[32:35],s72 offen offset:256 nt lds\n"
MF "v[108:111],v[20:23],v[172:175],v[108:111]\n"
"s_waitcnt lgkmcnt(0)\n"
"ds_read_b128 v[16:19],v0 offset:51200\n"
"ds_read_b128 v[20:23],v1 offset:51200\n"
MF "v[108:111],v[24:27],v[192:195],v[108:111]\n"
CV "v86,v124,v125\n"
"v_add_f32 v55,v55,v126\n"
MF "v[108:111],v[28:31],v[212:215],v[108:111]\n"
"v_add_f32 v55,v55,v127\n"
CV "v87,v126,v127\n"
MF "v[104:107],v[28:31],v[208:211],v[104:107]\n"
"v_exp_f32 v128,v128\n"
MF "v[104:107],v[24:27],v[188:191],v[104:107]\n"
"v_exp_f32 v129,v129\n"
MF "v[100:103],v[24:27],v[184:187],v[100:103]\n"
"v_exp_f32 v130,v130\n"
MF "v[100:103],v[28:31],v[204:207],v[100:103]\n"
"v_exp_f32 v131,v131\n"
MF "v[96:99],v[28:31],v[200:203],v[96:99]\n"
"v_add_f32 v56,v56,v128\n"
"v_add_f32 v56,v56,v129\n"
MF "v[96:99],v[24:27],v[180:183],v[96:99]\n"
CV "v90,v128,v129\n"
"v_add_f32 v56,v56,v130\n"
MF "v[92:95],v[24:27],v[176:179],v[92:95]\n"
"v_add_f32 v56,v56,v131\n"
CV "v91,v130,v131\n"
MF "v[92:95],v[28:31],v[196:199],v[92:95]\n"
"s_waitcnt lgkmcnt(0)\n"
"ds_read_b128 v[24:27],v0 offset:55296\n"
"ds_read_b128 v[28:31],v1 offset:55296\n"
MF "v[92:95],v[16:19],v[216:219],v[92:95]\n"
MF "v[92:95],v[20:23],v[236:239],v[92:95]\n"
MF "v[96:99],v[20:23],v[240:243],v[96:99]\n"
MF "v[96:99],v[16:19],v[220:223],v[96:99]\n"
MF "v[100:103],v[16:19],v[224:227],v[100:103]\n"
MF "v[100:103],v[20:23],v[244:247],v[100:103]\n"
MF "v[104:107],v[20:23],v[248:251],v[104:107]\n"
MF "v[104:107],v[16:19],v[228:231],v[104:107]\n"
"v_max3_f32 v10,v52,v53,v54\n"
"v_max3_f32 v10,v10,v55,v56\n"
MF "v[108:111],v[16:19],v[232:235],v[108:111]\n"
"v_cmp_ngt_f32 vcc,s77,v10\n"
MF "v[108:111],v[20:23],v[252:255],v[108:111]\n"
"s_waitcnt lgkmcnt(0)\n"
"s_cbranch_vccnz .Lstub_21\n"
".Lback_21:\n"
MF "v[112:115],v[24:27],v[136:139],v[32:35]\n"
"s_add_u32 s74,s74,12288\n"
"ds_read_b128 v[16:19],v0 offset:56320\n"
"ds_read_b128 v[20:23],v1 offset:56320\n"
MF "v[112:115],v[28:31],v[156:159],v[112:115]\n"
"s_cmp_eq_u32 s74,s20\n"
"s_cselect_b32 s74,s70,s74\n"
MF "v[116:119],v[28:31],v[160:163],v[36:39]\n"
"v_exp_f32 v92,v92\n"
MF "v[116:119],v[24:27],v[140:143],v[116:119]\n"
"v_exp_f32 v93,v93\n"
MF "v[120:123],v[24:27],v[144:147],v[40:43]\n"
"v_exp_f32 v94,v94\n"
MF "v[120:123],v[28:31],v[164:167],v[120:123]\n"
"v_exp_f32 v95,v95\n"
MF "v[124:127],v[28:31],v[168:171],v[44:47]\n"
"v_add_f32 v57,v52,v92\n"
"v_add_f32 v57,v57,v93\n"
MF "v[124:127],v[24:27],v[148:151],v[124:127]\n"
CV "v92,v92,v93\n"
"v_add_f32 v57,v57,v94\n"
MF "v[128:131],v[24:27],v[152:155],v[48:51]\n"
"v_add_f32 v57,v57,v95\n"
CV "v93,v94,v95\n"
MF "v[128:131],v[28:31],v[172:175],v[128:131]\n"
"s_waitcnt lgkmcnt(0)\n"
"ds_read_b128 v[24:27],v0 offset:57344\n"
"ds_read_b128 v[28:31],v1 offset:57344\n"
MF "v[128:131],v[16:19],v[192:195],v[128:131]\n"
"v_exp_f32 v96,v96\n"
MF "v[128:131],v[20:23],v[212:215],v[128:131]\n"
"v_exp_f32 v97,v97\n"
MF "v[124:127],v[20:23],v[208:211],v[124:127]\n"
"v_exp_f32 v98,v98\n"
MF "v[124:127],v[16:19],v[188:191],v[124:127]\n"
"v_exp_f32 v99,v99\n"
MF "v[120:123],v[16:19],v[184:187],v[120:123]\n"
"v_add_f32 v58,v53,v96\n"
"v_add_f32 v58,v58,v97\n"
MF "v[120:123],v[20:23],v[204:207],v[120:123]\n"
CV "v96,v96,v97\n"
"v_add_f32 v58,v58,v98\n"
MF "v[116:119],v[20:23],v[200:203],v[116:119]\n"
"s_waitcnt vmcnt(6) lgkmcnt(0)\n"
"v_add_f32 v58,v58,v99\n"
CV "v97,v98,v99\n"
MF "v[116:119],v[16:19],v[180:183],v[116:119]\n"
"s_barrier\n"
MF "v[112:115],v[16:19],v[176:179],v[112:115]\n"
"v_exp_f32 v100,v100\n"
MF "v[112:115],v[20:23],v[196:199],v[112:115]\n"
"s_nop 0\n"
TR "v[16:17],v2\n"
TR "v[18:19],v2 offset:6144\n"
MF "v[112:115],v[24:27],v[216:219],v[112:115]\n"
"s_add_u32 s73,s73,12288\n"
TR "v[20:21],v3\n"
TR "v[22:23],v3 offset:6144\n"
MF "v[112:115],v[28:31],v[236:239],v[112:115]\n"
"s_cmp_eq_u32 s73,s19\n"
"s_cselect_b32 s73,s18,s73\n"
"s_add_u32 m0,s65,49152\n"
MF "v[116:119],v[28:31],v[240:243],v[116:119]\n"
LD "v8,s[36:39],s73 offen nt lds\n"
MF "v[116:119],v[24:27],v[220:223],v[116:119]\n"
"s_add_u32 m0,s65,50048\n"
"v_exp_f32 v101,v101\n"
MF "v[120:123],v[24:27],v[224:227],v[120:123]\n"
LD "v8,s[36:39],s73 offen offset:128 nt lds\n"
MF "v[120:123],v[28:31],v[244:247],v[120:123]\n"
"v_exp_f32 v102,v102\n"
MF "v[124:127],v[28:31],v[248:251],v[124:127]\n"
"s_add_u32 m0,s65,50944\n"
"v_exp_f32 v103,v103\n"
MF "v[124:127],v[24:27],v[228:231],v[124:127]\n"
LD "v8,s[36:39],s73 offen offset:256 nt lds\n"
MF "v[128:131],v[24:27],v[232:235],v[128:131]\n"
"v_add_f32 v59,v54,v100\n"
"v_add_f32 v59,v59,v101\n"
MF "v[128:131],v[28:31],v[252:255],v[128:131]\n"
"s_waitcnt lgkmcnt(0)\n"
TR "v[24:25],v4\n"
TR "v[26:27],v4 offset:6144\n"
MF "a[0:3],v[16:19],v[72:75],a[0:3]\n"
"s_add_u32 m0,s65,49152\n"
TR "v[28:29],v5\n"
TR "v[30:31],v5 offset:6144\n"
MF "a[20:23],v[20:23],v[72:75],a[20:23]\n"
LD "v8,s[36:39],s73 offen offset:3072 nt lds\n"
MF "a[24:27],v[20:23],v[76:79],a[24:27]\n"
CV "v100,v100,v101\n"
"v_add_f32 v59,v59,v102\n"
MF "a[4:7],v[16:19],v[76:79],a[4:7]\n"
"s_add_u32 m0,s65,50048\n"
"v_add_f32 v59,v59,v103\n"
CV "v101,v102,v103\n"
MF "a[8:11],v[16:19],v[80:83],a[8:11]\n"
LD "v8,s[36:39],s73 offen offset:3200 nt lds\n"
MF "a[28:31],v[20:23],v[80:83],a[28:31]\n"
"v_exp_f32 v104,v104\n"
MF "a[32:35],v[20:23],v[84:87],a[32:35]\n"
"s_add_u32 m0,s65,50944\n"
"v_exp_f32 v105,v105\n"
MF "a[12:15],v[16:19],v[84:87],a[12:15]\n"
LD "v8,s[36:39],s73 offen offset:3328 nt lds\n"
MF "a[16:19],v[16:19],v[88:91],a[16:19]\n"
"v_exp_f32 v106,v106\n"
MF "a[36:39],v[20:23],v[88:91],a[36:39]\n"
"s_waitcnt lgkmcnt(0)\n"
TR "v[16:17],v2 offset:1024\n"
TR "v[18:19],v2 offset:7168\n"
MF "a[56:59],v[24:27],v[88:91],a[56:59]\n"
"s_nop 0\n"
TR "v[20:21],v3 offset:1024\n"
TR "v[22:23],v3 offset:7168\n"
MF "a[76:79],v[28:31],v[88:91],a[76:79]\n"
"v_exp_f32 v107,v107\n"
MF "a[72:75],v[28:31],v[84:87],a[72:75]\n"
"v_add_f32 v60,v55,v104\n"
"v_add_f32 v60,v60,v105\n"
MF "a[52:55],v[24:27],v[84:87],a[52:55]\n"
CV "v104,v104,v105\n"
"v_add_f32 v60,v60,v106\n"
MF "a[48:51],v[24:27],v[80:83],a[48:51]\n"
"v_add_f32 v60,v60,v107\n"
CV "v105,v106,v107\n"
MF "a[68:71],v[28:31],v[80:83],a[68:71]\n"
"v_exp_f32 v108,v108\n"
MF "a[64:67],v[28:31],v[76:79],a[64:67]\n"
"v_exp_f32 v109,v109\n"
MF "a[44:47],v[24:27],v[76:79],a[44:47]\n"
"v_exp_f32 v110,v110\n"
MF "a[40:43],v[24:27],v[72:75],a[40:43]\n"
"v_exp_f32 v111,v111\n"
MF "a[60:63],v[28:31],v[72:75],a[60:63]\n"
"s_waitcnt lgkmcnt(0)\n"
TR "v[24:25],v4 offset:1024\n"
TR "v[26:27],v4 offset:7168\n"
MF "a[80:83],v[16:19],v[72:75],a[80:83]\n"
"s_nop 0\n"
TR "v[28:29],v5 offset:1024\n"
TR "v[30:31],v5 offset:7168\n"
MF "a[100:103],v[20:23],v[72:75],a[100:103]\n"
"v_add_f32 v61,v56,v108\n"
"v_add_f32 v61,v61,v109\n"
MF "a[104:107],v[20:23],v[76:79],a[104:107]\n"
CV "v108,v108,v109\n"
"v_add_f32 v61,v61,v110\n"
MF "a[84:87],v[16:19],v[76:79],a[84:87]\n"
"v_add_f32 v61,v61,v111\n"
CV "v109,v110,v111\n"
MF "a[88:91],v[16:19],v[80:83],a[88:91]\n"
"v_exp_f32 v112,v112\n"
MF "a[108:111],v[20:23],v[80:83],a[108:111]\n"
"v_exp_f32 v113,v113\n"
MF "a[112:115],v[20:23],v[84:87],a[112:115]\n"
"v_exp_f32 v114,v114\n"
MF "a[92:95],v[16:19],v[84:87],a[92:95]\n"
"v_exp_f32 v115,v115\n"
MF "a[96:99],v[16:19],v[88:91],a[96:99]\n"
"v_add_f32 v57,v57,v112\n"
"v_add_f32 v57,v57,v113\n"
MF "a[116:119],v[20:23],v[88:91],a[116:119]\n"
"s_waitcnt lgkmcnt(0)\n"
TR "v[16:17],v2 offset:2048\n"
TR "v[18:19],v2 offset:8192\n"
MF "a[136:139],v[24:27],v[88:91],a[136:139]\n"
"s_nop 0\n"
TR "v[20:21],v3 offset:2048\n"
TR "v[22:23],v3 offset:8192\n"
MF "a[156:159],v[28:31],v[88:91],a[156:159]\n"
CV "v94,v112,v113\n"
"v_add_f32 v57,v57,v114\n"
MF "a[152:155],v[28:31],v[84:87],a[152:155]\n"
"v_add_f32 v57,v57,v115\n"
CV "v95,v114,v115\n"
MF "a[132:135],v[24:27],v[84:87],a[132:135]\n"
"v_exp_f32 v116,v116\n"
MF "a[128:131],v[24:27],v[80:83],a[128:131]\n"
"v_exp_f32 v117,v117\n"
MF "a[148:151],v[28:31],v[80:83],a[148:151]\n"
"v_exp_f32 v118,v118\n"
MF "a[144:147],v[28:31],v[76:79],a[144:147]\n"
"v_exp_f32 v119,v119\n"
MF "a[124:127],v[24:27],v[76:79],a[124:127]\n"
"v_add_f32 v58,v58,v116\n"
"v_add_f32 v58,v58,v117\n"
MF "a[120:123],v[24:27],v[72:75],a[120:123]\n"
CV "v98,v116,v117\n"
"v_add_f32 v58,v58,v118\n"
MF "a[140:143],v[28:31],v[72:75],a[140:143]\n"
"s_waitcnt lgkmcnt(0)\n"
TR "v[24:25],v4 offset:2048\n"
TR "v[26:27],v4 offset:8192\n"
MF "a[160:163],v[16:19],v[72:75],a[160:163]\n"
"s_nop 0\n"
TR "v[28:29],v5 offset:2048\n"
TR "v[30:31],v5 offset:8192\n"
MF "a[180:183],v[20:23],v[72:75],a[180:183]\n"
"v_add_f32 v58,v58,v119\n"
CV "v99,v118,v119\n"
MF "a[184:187],v[20:23],v[76:79],a[184:187]\n"
"v_exp_f32 v120,v120\n"
MF "a[164:167],v[16:19],v[76:79],a[164:167]\n"
"v_exp_f32 v121,v121\n"
MF "a[168:171],v[16:19],v[80:83],a[168:171]\n"
"v_exp_f32 v122,v122\n"
MF "a[188:191],v[20:23],v[80:83],a[188:191]\n"
"v_exp_f32 v123,v123\n"
MF "a[192:195],v[20:23],v[84:87],a[192:195]\n"
"v_add_f32 v59,v59,v120\n"
"v_add_f32 v59,v59,v121\n"
MF "a[172:175],v[16:19],v[84:87],a[172:175]\n"
"s_waitcnt vmcnt(6) lgkmcnt(0)\n"
CV "v102,v120,v121\n"
"v_add_f32 v59,v59,v122\n"
MF "a[176:179],v[16:19],v[88:91],a[176:179]\n"
"s_barrier\n"
MF "a[196:199],v[20:23],v[88:91],a[196:199]\n"
"s_add_u32 s72,s72,6144\n"
"ds_read_b128 v[16:19],v0 offset:24576\n"
"ds_read_b128 v[20:23],v1 offset:24576\n"
MF "a[216:219],v[24:27],v[88:91],a[216:219]\n"
"s_cmp_eq_u32 s72,s17\n"
"s_cselect_b32 s72,s16,s72\n"
"s_add_u32 m0,s64,0\n"
MF "a[236:239],v[28:31],v[88:91],a[236:239]\n"
LD "v7,s[32:35],s72 offen nt lds\n"
MF "a[232:235],v[28:31],v[84:87],a[232:235]\n"
"s_add_u32 m0,s64,896\n"
"v_add_f32 v59,v59,v123\n"
CV "v103,v122,v123\n"
MF "a[212:215],v[24:27],v[84:87],a[212:215]\n"
LD "v7,s[32:35],s72 offen offset:128 nt lds\n"
MF "a[208:211],v[24:27],v[80:83],a[208:211]\n"
"s_add_u32 m0,s64,1792\n"
MF "a[228:231],v[28:31],v[80:83],a[228:231]\n"
LD "v7,s[32:35],s72 offen offset:256 nt lds\n"
MF "a[224:227],v[28:31],v[76:79],a[224:227]\n"
MF "a[204:207],v[24:27],v[76:79],a[204:207]\n"
MF "a[200:203],v[24:27],v[72:75],a[200:203]\n"
MF "a[220:223],v[28:31],v[72:75],a[220:223]\n"
"s_waitcnt lgkmcnt(0)\n"
MF "v[72:75],v[16:19],v[136:139],v[32:35]\n"
"s_add_u32 s72,s72,6144\n"
"ds_read_b128 v[24:27],v0 offset:25600\n"
"ds_read_b128 v[28:31],v1 offset:25600\n"
MF "v[72:75],v[20:23],v[156:159],v[72:75]\n"
"s_add_u32 m0,s64,6144\n"
"v_exp_f32 v124,v124\n"
MF "v[76:79],v[20:23],v[160:163],v[36:39]\n"
LD "v7,s[32:35],s72 offen nt lds\n"
MF "v[76:79],v[16:19],v[140:143],v[76:79]\n"
"v_exp_f32 v125,v125\n"
MF "v[80:83],v[16:19],v[144:147],v[40:43]\n"
"s_add_u32 m0,s64,7040\n"
"v_exp_f32 v126,v126\n"
MF "v[80:83],v[20:23],v[164:167],v[80:83]\n"
LD "v7,s[32:35],s72 offen offset:128 nt lds\n"
MF "v[84:87],v[20:23],v[168:171],v[44:47]\n"
"v_exp_f32 v127,v127\n"
MF "v[84:87],v[16:19],v[148:151],v[84:87]\n"
"s_add_u32 m0,s64,7936\n"
"v_add_f32 v60,v60,v124\n"
"v_add_f32 v60,v60,v125\n"
MF "v[88:91],v[16:19],v[152:155],v[48:51]\n"
LD "v7,s[32:35],s72 offen offset:256 nt lds\n"
MF "v[88:91],v[20:23],v[172:175],v[88:91]\n"
"s_waitcnt lgkmcnt(0)\n"
"ds_read_b128 v[16:19],v0 offset:26624\n"
"ds_read_b128 v[20:23],v1 offset:26624\n"
MF "v[88:91],v[24:27],v[192:195],v[88:91]\n"
CV "v106,v124,v125\n"
"v_add_f32 v60,v60,v126\n"
MF "v[88:91],v[28:31],v[212:215],v[88:91]\n"
"v_add_f32 v60,v60,v127\n"
CV "v107,v126,v127\n"
MF "v[84:87],v[28:31],v[208:211],v[84:87]\n"
"v_exp_f32 v128,v128\n"
MF "v[84:87],v[24:27],v[188:191],v[84:87]\n"
"v_exp_f32 v129,v129\n"
MF "v[80:83],v[24:27],v[184:187],v[80:83]\n"
"v_exp_f32 v130,v130\n"
MF "v[80:83],v[28:31],v[204:207],v[80:83]\n"
"v_exp_f32 v131,v131\n"
MF "v[76:79],v[28:31],v[200:203],v[76:79]\n"
"v_add_f32 v61,v61,v128\n"
"v_add_f32 v61,v61,v129\n"
MF "v[76:79],v[24:27],v[180:183],v[76:79]\n"
CV "v110,v128,v129\n"
"v_add_f32 v61,v61,v130\n"
MF "v[72:75],v[24:27],v[176:179],v[72:75]\n"
"v_add_f32 v61,v61,v131\n"
CV "v111,v130,v131\n"
MF "v[72:75],v[28:31],v[196:199],v[72:75]\n"
"s_waitcnt lgkmcnt(0)\n"
"ds_read_b128 v[24:27],v0 offset:30720\n"
"ds_read_b128 v[28:31],v1 offset:30720\n"
MF "v[72:75],v[16:19],v[216:219],v[72:75]\n"
MF "v[72:75],v[20:23],v[236:239],v[72:75]\n"
MF "v[76:79],v[20:23],v[240:243],v[76:79]\n"
MF "v[76:79],v[16:19],v[220:223],v[76:79]\n"
MF "v[80:83],v[16:19],v[224:227],v[80:83]\n"
MF "v[80:83],v[20:23],v[244:247],v[80:83]\n"
MF "v[84:87],v[20:23],v[248:251],v[84:87]\n"
MF "v[84:87],v[16:19],v[228:231],v[84:87]\n"
"v_max3_f32 v10,v57,v58,v59\n"
"v_max3_f32 v10,v10,v60,v61\n"
MF "v[88:91],v[16:19],v[232:235],v[88:91]\n"
"v_cmp_ngt_f32 vcc,s77,v10\n"
MF "v[88:91],v[20:23],v[252:255],v[88:91]\n"
"s_waitcnt lgkmcnt(0)\n"
"s_cbranch_vccnz .Lstub_22\n"
".Lback_22:\n"
MF "v[112:115],v[24:27],v[136:139],v[32:35]\n"
"s_add_u32 s74,s74,12288\n"
"ds_read_b128 v[16:19],v0 offset:31744\n"
"ds_read_b128 v[20:23],v1 offset:31744\n"
MF "v[112:115],v[28:31],v[156:159],v[112:115]\n"
"s_cmp_eq_u32 s74,s20\n"
"s_cselect_b32 s74,s70,s74\n"
MF "v[116:119],v[28:31],v[160:163],v[36:39]\n"
"v_exp_f32 v72,v72\n"
MF "v[116:119],v[24:27],v[140:143],v[116:119]\n"
"v_exp_f32 v73,v73\n"
MF "v[120:123],v[24:27],v[144:147],v[40:43]\n"
"v_exp_f32 v74,v74\n"
MF "v[120:123],v[28:31],v[164:167],v[120:123]\n"
"v_exp_f32 v75,v75\n"
MF "v[124:127],v[28:31],v[168:171],v[44:47]\n"
"v_add_f32 v52,v57,v72\n"
"v_add_f32 v52,v52,v73\n"
MF "v[124:127],v[24:27],v[148:151],v[124:127]\n"
CV "v72,v72,v73\n"
"v_add_f32 v52,v52,v74\n"
MF "v[128:131],v[24:27],v[152:155],v[48:51]\n"
"v_add_f32 v52,v52,v75\n"
CV "v73,v74,v75\n"
MF "v[128:131],v[28:31],v[172:175],v[128:131]\n"
"s_waitcnt lgkmcnt(0)\n"
"ds_read_b128 v[24:27],v0 offset:32768\n"
"ds_read_b128 v[28:31],v1 offset:32768\n"
MF "v[128:131],v[16:19],v[192:195],v[128:131]\n"
"v_exp_f32 v76,v76\n"
MF "v[128:131],v[20:23],v[212:215],v[128:131]\n"
"v_exp_f32 v77,v77\n"
MF "v[124:127],v[20:23],v[208:211],v[124:127]\n"
"v_exp_f32 v78,v78\n"
MF "v[124:127],v[16:19],v[188:191],v[124:127]\n"
"v_exp_f32 v79,v79\n"
MF "v[120:123],v[16:19],v[184:187],v[120:123]\n"
"v_add_f32 v53,v58,v76\n"
"v_add_f32 v53,v53,v77\n"
MF "v[120:123],v[20:23],v[204:207],v[120:123]\n"
CV "v76,v76,v77\n"
"v_add_f32 v53,v53,v78\n"
MF "v[116:119],v[20:23],v[200:203],v[116:119]\n"
"s_waitcnt vmcnt(6) lgkmcnt(0)\n"
"v_add_f32 v53,v53,v79\n"
CV "v77,v78,v79\n"
MF "v[116:119],v[16:19],v[180:183],v[116:119]\n"
"s_barrier\n"
MF "v[112:115],v[16:19],v[176:179],v[112:115]\n"
"v_exp_f32 v80,v80\n"
MF "v[112:115],v[20:23],v[196:199],v[112:115]\n"
"s_nop 0\n"
TR "v[16:17],v2 offset:49152\n"
TR "v[18:19],v2 offset:55296\n"
MF "v[112:115],v[24:27],v[216:219],v[112:115]\n"
"s_add_u32 s73,s73,12288\n"
TR "v[20:21],v3 offset:49152\n"
TR "v[22:23],v3 offset:55296\n"
MF "v[112:115],v[28:31],v[236:239],v[112:115]\n"
"s_cmp_eq_u32 s73,s19\n"
"s_cselect_b32 s73,s18,s73\n"
"s_add_u32 m0,s65,24576\n"
MF "v[116:119],v[28:31],v[240:243],v[116:119]\n"
LD "v8,s[36:39],s73 offen nt lds\n"
MF "v[116:119],v[24:27],v[220:223],v[116:119]\n"
"s_add_u32 m0,s65,25472\n"
"v_exp_f32 v81,v81\n"
MF "v[120:123],v[24:27],v[224:227],v[120:123]\n"
LD "v8,s[36:39],s73 offen offset:128 nt lds\n"
MF "v[120:123],v[28:31],v[244:247],v[120:123]\n"
"v_exp_f32 v82,v82\n"
MF "v[124:127],v[28:31],v[248:251],v[124:127]\n"
"s_add_u32 m0,s65,26368\n"
"v_exp_f32 v83,v83\n"
MF "v[124:127],v[24:27],v[228:231],v[124:127]\n"
LD "v8,s[36:39],s73 offen offset:256 nt lds\n"
MF "v[128:131],v[24:27],v[232:235],v[128:131]\n"
"v_add_f32 v54,v59,v80\n"
"v_add_f32 v54,v54,v81\n"
MF "v[128:131],v[28:31],v[252:255],v[128:131]\n"
"s_waitcnt lgkmcnt(0)\n"
TR "v[24:25],v4 offset:49152\n"
TR "v[26:27],v4 offset:55296\n"
MF "a[0:3],v[16:19],v[92:95],a[0:3]\n"
"s_add_u32 m0,s65,24576\n"
TR "v[28:29],v5 offset:49152\n"
TR "v[30:31],v5 offset:55296\n"
MF "a[20:23],v[20:23],v[92:95],a[20:23]\n"
LD "v8,s[36:39],s73 offen offset:3072 nt lds\n"
MF "a[24:27],v[20:23],v[96:99],a[24:27]\n"
CV "v80,v80,v81\n"
"v_add_f32 v54,v54,v82\n"
MF "a[4:7],v[16:19],v[96:99],a[4:7]\n"
"s_add_u32 m0,s65,25472\n"
"v_add_f32 v54,v54,v83\n"
CV "v81,v82,v83\n"
MF "a[8:11],v[16:19],v[100:103],a[8:11]\n"
LD "v8,s[36:39],s73 offen offset:3200 nt lds\n"
MF "a[28:31],v[20:23],v[100:103],a[28:31]\n"
"v_exp_f32 v84,v84\n"
MF "a[32:35],v[20:23],v[104:107],a[32:35]\n"
"s_add_u32 m0,s65,26368\n"
"v_exp_f32 v85,v85\n"
MF "a[12:15],v[16:19],v[104:107],a[12:15]\n"
LD "v8,s[36:39],s73 offen offset:3328 nt lds\n"
MF "a[16:19],v[16:19],v[108:111],a[16:19]\n"
"v_exp_f32 v86,v86\n"
MF "a[36:39],v[20:23],v[108:111],a[36:39]\n"
"s_waitcnt lgkmcnt(0)\n"
TR "v[16:17],v2 offset:50176\n"
TR "v[18:19],v2 offset:56320\n"
MF "a[56:59],v[24:27],v[108:111],a[56:59]\n"
"s_nop 0\n"
TR "v[20:21],v3 offset:50176\n"
TR "v[22:23],v3 offset:56320\n"
MF "a[76:79],v[28:31],v[108:111],a[76:79]\n"
"v_exp_f32 v87,v87\n"
MF "a[72:75],v[28:31],v[104:107],a[72:75]\n"
"v_add_f32 v55,v60,v84\n"
"v_add_f32 v55,v55,v85\n"
MF "a[52:55],v[24:27],v[104:107],a[52:55]\n"
CV "v84,v84,v85\n"
"v_add_f32 v55,v55,v86\n"
MF "a[48:51],v[24:27],v[100:103],a[48:51]\n"
"v_add_f32 v55,v55,v87\n"
CV "v85,v86,v87\n"
MF "a[68:71],v[28:31],v[100:103],a[68:71]\n"
"v_exp_f32 v88,v88\n"
MF "a[64:67],v[28:31],v[96:99],a[64:67]\n"
"v_exp_f32 v89,v89\n"
MF "a[44:47],v[24:27],v[96:99],a[44:47]\n"
"v_exp_f32 v90,v90\n"
MF "a[40:43],v[24:27],v[92:95],a[40:43]\n"
"v_exp_f32 v91,v91\n"
MF "a[60:63],v[28:31],v[92:95],a[60:63]\n"
"s_waitcnt lgkmcnt(0)\n"
TR "v[24:25],v4 offset:50176\n"
TR "v[26:27],v4 offset:56320\n"
MF "a[80:83],v[16:19],v[92:95],a[80:83]\n"
"s_nop 0\n"
TR "v[28:29],v5 offset:50176\n"
TR "v[30:31],v5 offset:56320\n"
MF "a[100:103],v[20:23],v[92:95],a[100:103]\n"
"v_add_f32 v56,v61,v88\n"
"v_add_f32 v56,v56,v89\n"
MF "a[104:107],v[20:23],v[96:99],a[104:107]\n"
CV "v88,v88,v89\n"
"v_add_f32 v56,v56,v90\n"
MF "a[84:87],v[16:19],v[96:99],a[84:87]\n"
"v_add_f32 v56,v56,v91\n"
CV "v89,v90,v91\n"
MF "a[88:91],v[16:19],v[100:103],a[88:91]\n"
"v_exp_f32 v112,v112\n"
MF "a[108:111],v[20:23],v[100:103],a[108:111]\n"
"v_exp_f32 v113,v113\n"
MF "a[112:115],v[20:23],v[104:107],a[112:115]\n"
"v_exp_f32 v114,v114\n"
MF "a[92:95],v[16:19],v[104:107],a[92:95]\n"
"v_exp_f32 v115,v115\n"
MF "a[96:99],v[16:19],v[108:111],a[96:99]\n"
"v_add_f32 v52,v52,v112\n"
"v_add_f32 v52,v52,v113\n"
MF "a[116:119],v[20:23],v[108:111],a[116:119]\n"
"s_waitcnt lgkmcnt(0)\n"
TR "v[16:17],v2 offset:51200\n"
TR "v[18:19],v2 offset:57344\n"
MF "a[136:139],v[24:27],v[108:111],a[136:139]\n"
"s_nop 0\n"
TR "v[20:21],v3 offset:51200\n"
TR "v[22:23],v3 offset:57344\n"
MF "a[156:159],v[28:31],v[108:111],a[156:159]\n"
CV "v74,v112,v113\n"
"v_add_f32 v52,v52,v114\n"
MF "a[152:155],v[28:31],v[104:107],a[152:155]\n"
"v_add_f32 v52,v52,v115\n"
CV "v75,v114,v115\n"
MF "a[132:135],v[24:27],v[104:107],a[132:135]\n"
"v_exp_f32 v116,v116\n"
MF "a[128:131],v[24:27],v[100:103],a[128:131]\n"
"v_exp_f32 v117,v117\n"
MF "a[148:151],v[28:31],v[100:103],a[148:151]\n"
"v_exp_f32 v118,v118\n"
MF "a[144:147],v[28:31],v[96:99],a[144:147]\n"
"v_exp_f32 v119,v119\n"
MF "a[124:127],v[24:27],v[96:99],a[124:127]\n"
"v_add_f32 v53,v53,v116\n"
"v_add_f32 v53,v53,v117\n"
MF "a[120:123],v[24:27],v[92:95],a[120:123]\n"
CV "v78,v116,v117\n"
"v_add_f32 v53,v53,v118\n"
MF "a[140:143],v[28:31],v[92:95],a[140:143]\n"
"s_waitcnt lgkmcnt(0)\n"
TR "v[24:25],v4 offset:51200\n"
TR "v[26:27],v4 offset:57344\n"
MF "a[160:163],v[16:19],v[92:95],a[160:163]\n"
"s_nop 0\n"
TR "v[28:29],v5 offset:51200\n"
TR "v[30:31],v5 offset:57344\n"
MF "a[180:183],v[20:23],v[92:95],a[180:183]\n"
"v_add_f32 v53,v53,v119\n"
CV "v79,v118,v119\n"
MF "a[184:187],v[20:23],v[96:99],a[184:187]\n"
"v_exp_f32 v120,v120\n"
MF "a[164:167],v[16:19],v[96:99],a[164:167]\n"
"v_exp_f32 v121,v121\n"
MF "a[168:171],v[16:19],v[100:103],a[168:171]\n"
"v_exp_f32 v122,v122\n"
MF "a[188:191],v[20:23],v[100:103],a[188:191]\n"
"v_exp_f32 v123,v123\n"
MF "a[192:195],v[20:23],v[104:107],a[192:195]\n"
"v_add_f32 v54,v54,v120\n"
"v_add_f32 v54,v54,v121\n"
MF "a[172:175],v[16:19],v[104:107],a[172:175]\n"
"s_waitcnt vmcnt(6) lgkmcnt(0)\n"
CV "v82,v120,v121\n"
"v_add_f32 v54,v54,v122\n"
MF "a[176:179],v[16:19],v[108:111],a[176:179]\n"
"s_barrier\n"
MF "a[196:199],v[20:23],v[108:111],a[196:199]\n"
"s_add_u32 s72,s72,6144\n"
"ds_read_b128 v[16:19],v0\n"
"ds_read_b128 v[20:23],v1\n"
MF "a[216:219],v[24:27],v[108:111],a[216:219]\n"
"s_cmp_eq_u32 s72,s17\n"
"s_cselect_b32 s72,s16,s72\n"
"s_add_u32 m0,s64,49152\n"
MF "a[236:239],v[28:31],v[108:111],a[236:239]\n"
LD "v7,s[32:35],s72 offen nt lds\n"
MF "a[232:235],v[28:31],v[104:107],a[232:235]\n"
"s_add_u32 m0,s64,50048\n"
"v_add_f32 v54,v54,v123\n"
CV "v83,v122,v123\n"
MF "a[212:215],v[24:27],v[104:107],a[212:215]\n"
LD "v7,s[32:35],s72 offen offset:128 nt lds\n"
MF "a[208:211],v[24:27],v[100:103],a[208:211]\n"
"s_add_u32 m0,s64,50944\n"
MF "a[228:231],v[28:31],v[100:103],a[228:231]\n"
LD "v7,s[32:35],s72 offen offset:256 nt lds\n"
MF "a[224:227],v[28:31],v[96:99],a[224:227]\n"
MF "a[204:207],v[24:27],v[96:99],a[204:207]\n"
MF "a[200:203],v[24:27],v[92:95],a[200:203]\n"
MF "a[220:223],v[28:31],v[92:95],a[220:223]\n"
"s_waitcnt lgkmcnt(0)\n"
MF "v[92:95],v[16:19],v[136:139],v[32:35]\n"
"s_add_u32 s72,s72,6144\n"
"ds_read_b128 v[24:27],v0 offset:1024\n"
"ds_read_b128 v[28:31],v1 offset:1024\n"
MF "v[92:95],v[20:23],v[156:159],v[92:95]\n"
"s_add_u32 m0,s64,55296\n"
"v_exp_f32 v124,v124\n"
MF "v[96:99],v[20:23],v[160:163],v[36:39]\n"
LD "v7,s[32:35],s72 offen nt lds\n"
MF "v[96:99],v[16:19],v[140:143],v[96:99]\n"
"v_exp_f32 v125,v125\n"
MF "v[100:103],v[16:19],v[144:147],v[40:43]\n"
"s_add_u32 m0,s64,56192\n"
"v_exp_f32 v126,v126\n"
MF "v[100:103],v[20:23],v[164:167],v[100:103]\n"
LD "v7,s[32:35],s72 offen offset:128 nt lds\n"
MF "v[104:107],v[20:23],v[168:171],v[44:47]\n"
"v_exp_f32 v127,v127\n"
MF "v[104:107],v[16:19],v[148:151],v[104:107]\n"
"s_add_u32 m0,s64,57088\n"
"v_add_f32 v55,v55,v124\n"
"v_add_f32 v55,v55,v125\n"
MF "v[108:111],v[16:19],v[152:155],v[48:51]\n"
LD "v7,s[32:35],s72 offen offset:256 nt lds\n"
MF "v[108:111],v[20:23],v[172:175],v[108:111]\n"
"s_waitcnt lgkmcnt(0)\n"
"ds_read_b128 v[16:19],v0 offset:2048\n"
"ds_read_b128 v[20:23],v1 offset:2048\n"
MF "v[108:111],v[24:27],v[192:195],v[108:111]\n"
CV "v86,v124,v125\n"
"v_add_f32 v55,v55,v126\n"
MF "v[108:111],v[28:31],v[212:215],v[108:111]\n"
"v_add_f32 v55,v55,v127\n"
CV "v87,v126,v127\n"
MF "v[104:107],v[28:31],v[208:211],v[104:107]\n"
"v_exp_f32 v128,v128\n"
MF "v[104:107],v[24:27],v[188:191],v[104:107]\n"
"v_exp_f32 v129,v129\n"
MF "v[100:103],v[24:27],v[184:187],v[100:103]\n"
"v_exp_f32 v130,v130\n"
MF "v[100:103],v[28:31],v[204:207],v[100:103]\n"
"v_exp_f32 v131,v131\n"
MF "v[96:99],v[28:31],v[200:203],v[96:99]\n"
"v_add_f32 v56,v56,v128\n"
"v_add_f32 v56,v56,v129\n"
MF "v[96:99],v[24:27],v[180:183],v[96:99]\n"
CV "v90,v128,v129\n"
"v_add_f32 v56,v56,v130\n"
MF "v[92:95],v[24:27],v[176:179],v[92:95]\n"
"v_add_f32 v56,v56,v131\n"
CV "v91,v130,v131\n"
MF "v[92:95],v[28:31],v[196:199],v[92:95]\n"
"s_waitcnt lgkmcnt(0)\n"
"ds_read_b128 v[24:27],v0 offset:6144\n"
"ds_read_b128 v[28:31],v1 offset:6144\n"
MF "v[92:95],v[16:19],v[216:219],v[92:95]\n"
MF "v[92:95],v[20:23],v[236:239],v[92:95]\n"
MF "v[96:99],v[20:23],v[240:243],v[96:99]\n"
MF "v[96:99],v[16:19],v[220:223],v[96:99]\n"
MF "v[100:103],v[16:19],v[224:227],v[100:103]\n"
MF "v[100:103],v[20:23],v[244:247],v[100:103]\n"
MF "v[104:107],v[20:23],v[248:251],v[104:107]\n"
MF "v[104:107],v[16:19],v[228:231],v[104:107]\n"
"v_max3_f32 v10,v52,v53,v54\n"
"v_max3_f32 v10,v10,v55,v56\n"
MF "v[108:111],v[16:19],v[232:235],v[108:111]\n"
"v_cmp_ngt_f32 vcc,s77,v10\n"
MF "v[108:111],v[20:23],v[252:255],v[108:111]\n"
"s_waitcnt lgkmcnt(0)\n"
"s_cbranch_vccnz .Lstub_23\n"
".Lback_23:\n"
MF "v[112:115],v[24:27],v[136:139],v[32:35]\n"
"s_add_u32 s74,s74,12288\n"
"ds_read_b128 v[16:19],v0 offset:7168\n"
"ds_read_b128 v[20:23],v1 offset:7168\n"
MF "v[112:115],v[28:31],v[156:159],v[112:115]\n"
"s_cmp_eq_u32 s74,s20\n"
"s_cselect_b32 s74,s70,s74\n"
MF "v[116:119],v[28:31],v[160:163],v[36:39]\n"
"v_exp_f32 v92,v92\n"
MF "v[116:119],v[24:27],v[140:143],v[116:119]\n"
"v_exp_f32 v93,v93\n"
MF "v[120:123],v[24:27],v[144:147],v[40:43]\n"
"v_exp_f32 v94,v94\n"
MF "v[120:123],v[28:31],v[164:167],v[120:123]\n"
"v_exp_f32 v95,v95\n"
MF "v[124:127],v[28:31],v[168:171],v[44:47]\n"
"v_add_f32 v57,v52,v92\n"
"v_add_f32 v57,v57,v93\n"
MF "v[124:127],v[24:27],v[148:151],v[124:127]\n"
CV "v92,v92,v93\n"
"v_add_f32 v57,v57,v94\n"
MF "v[128:131],v[24:27],v[152:155],v[48:51]\n"
"v_add_f32 v57,v57,v95\n"
CV "v93,v94,v95\n"
MF "v[128:131],v[28:31],v[172:175],v[128:131]\n"
"s_waitcnt lgkmcnt(0)\n"
"ds_read_b128 v[24:27],v0 offset:8192\n"
"ds_read_b128 v[28:31],v1 offset:8192\n"
MF "v[128:131],v[16:19],v[192:195],v[128:131]\n"
"v_exp_f32 v96,v96\n"
MF "v[128:131],v[20:23],v[212:215],v[128:131]\n"
"v_exp_f32 v97,v97\n"
MF "v[124:127],v[20:23],v[208:211],v[124:127]\n"
"v_exp_f32 v98,v98\n"
MF "v[124:127],v[16:19],v[188:191],v[124:127]\n"
"v_exp_f32 v99,v99\n"
MF "v[120:123],v[16:19],v[184:187],v[120:123]\n"
"v_add_f32 v58,v53,v96\n"
"v_add_f32 v58,v58,v97\n"
MF "v[120:123],v[20:23],v[204:207],v[120:123]\n"
CV "v96,v96,v97\n"
"v_add_f32 v58,v58,v98\n"
MF "v[116:119],v[20:23],v[200:203],v[116:119]\n"
"s_waitcnt vmcnt(6) lgkmcnt(0)\n"
"v_add_f32 v58,v58,v99\n"
CV "v97,v98,v99\n"
MF "v[116:119],v[16:19],v[180:183],v[116:119]\n"
"s_barrier\n"
MF "v[112:115],v[16:19],v[176:179],v[112:115]\n"
"v_exp_f32 v100,v100\n"
MF "v[112:115],v[20:23],v[196:199],v[112:115]\n"
"s_nop 0\n"
TR "v[16:17],v2 offset:24576\n"
TR "v[18:19],v2 offset:30720\n"
MF "v[112:115],v[24:27],v[216:219],v[112:115]\n"
"s_add_u32 s73,s73,12288\n"
TR "v[20:21],v3 offset:24576\n"
TR "v[22:23],v3 offset:30720\n"
MF "v[112:115],v[28:31],v[236:239],v[112:115]\n"
"s_cmp_eq_u32 s73,s19\n"
"s_cselect_b32 s73,s18,s73\n"
"s_add_u32 m0,s65,0\n"
MF "v[116:119],v[28:31],v[240:243],v[116:119]\n"
LD "v8,s[36:39],s73 offen nt lds\n"
MF "v[116:119],v[24:27],v[220:223],v[116:119]\n"
"s_add_u32 m0,s65,896\n"
"v_exp_f32 v101,v101\n"
MF "v[120:123],v[24:27],v[224:227],v[120:123]\n"
LD "v8,s[36:39],s73 offen offset:128 nt lds\n"
MF "v[120:123],v[28:31],v[244:247],v[120:123]\n"
"v_exp_f32 v102,v102\n"
MF "v[124:127],v[28:31],v[248:251],v[124:127]\n"
"s_add_u32 m0,s65,1792\n"
"v_exp_f32 v103,v103\n"
MF "v[124:127],v[24:27],v[228:231],v[124:127]\n"
LD "v8,s[36:39],s73 offen offset:256 nt lds\n"
MF "v[128:131],v[24:27],v[232:235],v[128:131]\n"
"v_add_f32 v59,v54,v100\n"
"v_add_f32 v59,v59,v101\n"
MF "v[128:131],v[28:31],v[252:255],v[128:131]\n"
"s_waitcnt lgkmcnt(0)\n"
TR "v[24:25],v4 offset:24576\n"
TR "v[26:27],v4 offset:30720\n"
MF "a[0:3],v[16:19],v[72:75],a[0:3]\n"
"s_add_u32 m0,s65,0\n"
TR "v[28:29],v5 offset:24576\n"
TR "v[30:31],v5 offset:30720\n"
MF "a[20:23],v[20:23],v[72:75],a[20:23]\n"
LD "v8,s[36:39],s73 offen offset:3072 nt lds\n"
MF "a[24:27],v[20:23],v[76:79],a[24:27]\n"
CV "v100,v100,v101\n"
"v_add_f32 v59,v59,v102\n"
MF "a[4:7],v[16:19],v[76:79],a[4:7]\n"
"s_add_u32 m0,s65,896\n"
"v_add_f32 v59,v59,v103\n"
CV "v101,v102,v103\n"
MF "a[8:11],v[16:19],v[80:83],a[8:11]\n"
LD "v8,s[36:39],s73 offen offset:3200 nt lds\n"
MF "a[28:31],v[20:23],v[80:83],a[28:31]\n"
"v_exp_f32 v104,v104\n"
MF "a[32:35],v[20:23],v[84:87],a[32:35]\n"
"s_add_u32 m0,s65,1792\n"
"v_exp_f32 v105,v105\n"
MF "a[12:15],v[16:19],v[84:87],a[12:15]\n"
LD "v8,s[36:39],s73 offen offset:3328 nt lds\n"
MF "a[16:19],v[16:19],v[88:91],a[16:19]\n"
"v_exp_f32 v106,v106\n"
MF "a[36:39],v[20:23],v[88:91],a[36:39]\n"
"s_waitcnt lgkmcnt(0)\n"
TR "v[16:17],v2 offset:25600\n"
TR "v[18:19],v2 offset:31744\n"
MF "a[56:59],v[24:27],v[88:91],a[56:59]\n"
"s_nop 0\n"
TR "v[20:21],v3 offset:25600\n"
TR "v[22:23],v3 offset:31744\n"
MF "a[76:79],v[28:31],v[88:91],a[76:79]\n"
"v_exp_f32 v107,v107\n"
MF "a[72:75],v[28:31],v[84:87],a[72:75]\n"
"v_add_f32 v60,v55,v104\n"
"v_add_f32 v60,v60,v105\n"
MF "a[52:55],v[24:27],v[84:87],a[52:55]\n"
CV "v104,v104,v105\n"
"v_add_f32 v60,v60,v106\n"
MF "a[48:51],v[24:27],v[80:83],a[48:51]\n"
"v_add_f32 v60,v60,v107\n"
CV "v105,v106,v107\n"
MF "a[68:71],v[28:31],v[80:83],a[68:71]\n"
"v_exp_f32 v108,v108\n"
MF "a[64:67],v[28:31],v[76:79],a[64:67]\n"
"v_exp_f32 v109,v109\n"
MF "a[44:47],v[24:27],v[76:79],a[44:47]\n"
"v_exp_f32 v110,v110\n"
MF "a[40:43],v[24:27],v[72:75],a[40:43]\n"
"v_exp_f32 v111,v111\n"
MF "a[60:63],v[28:31],v[72:75],a[60:63]\n"
"s_waitcnt lgkmcnt(0)\n"
TR "v[24:25],v4 offset:25600\n"
TR "v[26:27],v4 offset:31744\n"
MF "a[80:83],v[16:19],v[72:75],a[80:83]\n"
"s_nop 0\n"
TR "v[28:29],v5 offset:25600\n"
TR "v[30:31],v5 offset:31744\n"
MF "a[100:103],v[20:23],v[72:75],a[100:103]\n"
"v_add_f32 v61,v56,v108\n"
"v_add_f32 v61,v61,v109\n"
MF "a[104:107],v[20:23],v[76:79],a[104:107]\n"
CV "v108,v108,v109\n"
"v_add_f32 v61,v61,v110\n"
MF "a[84:87],v[16:19],v[76:79],a[84:87]\n"
"v_add_f32 v61,v61,v111\n"
CV "v109,v110,v111\n"
MF "a[88:91],v[16:19],v[80:83],a[88:91]\n"
"v_exp_f32 v112,v112\n"
MF "a[108:111],v[20:23],v[80:83],a[108:111]\n"
"v_exp_f32 v113,v113\n"
MF "a[112:115],v[20:23],v[84:87],a[112:115]\n"
"v_exp_f32 v114,v114\n"
MF "a[92:95],v[16:19],v[84:87],a[92:95]\n"
"v_exp_f32 v115,v115\n"
MF "a[96:99],v[16:19],v[88:91],a[96:99]\n"
"v_add_f32 v57,v57,v112\n"
"v_add_f32 v57,v57,v113\n"
MF "a[116:119],v[20:23],v[88:91],a[116:119]\n"
"s_waitcnt lgkmcnt(0)\n"
TR "v[16:17],v2 offset:26624\n"
TR "v[18:19],v2 offset:32768\n"
MF "a[136:139],v[24:27],v[88:91],a[136:139]\n"
"s_nop 0\n"
TR "v[20:21],v3 offset:26624\n"
TR "v[22:23],v3 offset:32768\n"
MF "a[156:159],v[28:31],v[88:91],a[156:159]\n"
CV "v94,v112,v113\n"
"v_add_f32 v57,v57,v114\n"
MF "a[152:155],v[28:31],v[84:87],a[152:155]\n"
"v_add_f32 v57,v57,v115\n"
CV "v95,v114,v115\n"
MF "a[132:135],v[24:27],v[84:87],a[132:135]\n"
"v_exp_f32 v116,v116\n"
MF "a[128:131],v[24:27],v[80:83],a[128:131]\n"
"v_exp_f32 v117,v117\n"
MF "a[148:151],v[28:31],v[80:83],a[148:151]\n"
"v_exp_f32 v118,v118\n"
MF "a[144:147],v[28:31],v[76:79],a[144:147]\n"
"v_exp_f32 v119,v119\n"
MF "a[124:127],v[24:27],v[76:79],a[124:127]\n"
"v_add_f32 v58,v58,v116\n"
"v_add_f32 v58,v58,v117\n"
MF "a[120:123],v[24:27],v[72:75],a[120:123]\n"
CV "v98,v116,v117\n"
"v_add_f32 v58,v58,v118\n"
MF "a[140:143],v[28:31],v[72:75],a[140:143]\n"
"s_waitcnt lgkmcnt(0)\n"
TR "v[24:25],v4 offset:26624\n"
TR "v[26:27],v4 offset:32768\n"
MF "a[160:163],v[16:19],v[72:75],a[160:163]\n"
"s_nop 0\n"
TR "v[28:29],v5 offset:26624\n"
TR "v[30:31],v5 offset:32768\n"
MF "a[180:183],v[20:23],v[72:75],a[180:183]\n"
"v_add_f32 v58,v58,v119\n"
CV "v99,v118,v119\n"
MF "a[184:187],v[20:23],v[76:79],a[184:187]\n"
"v_exp_f32 v120,v120\n"
MF "a[164:167],v[16:19],v[76:79],a[164:167]\n"
"v_exp_f32 v121,v121\n"
MF "a[168:171],v[16:19],v[80:83],a[168:171]\n"
"v_exp_f32 v122,v122\n"
MF "a[188:191],v[20:23],v[80:83],a[188:191]\n"
"v_exp_f32 v123,v123\n"
MF "a[192:195],v[20:23],v[84:87],a[192:195]\n"
"v_add_f32 v59,v59,v120\n"
"v_add_f32 v59,v59,v121\n"
MF "a[172:175],v[16:19],v[84:87],a[172:175]\n"
"s_waitcnt vmcnt(6) lgkmcnt(0)\n"
CV "v102,v120,v121\n"
"v_add_f32 v59,v59,v122\n"
MF "a[176:179],v[16:19],v[88:91],a[176:179]\n"
"s_barrier\n"
MF "a[196:199],v[20:23],v[88:91],a[196:199]\n"
"s_add_u32 s72,s72,6144\n"
"ds_read_b128 v[16:19],v0 offset:49152\n"
"ds_read_b128 v[20:23],v1 offset:49152\n"
MF "a[216:219],v[24:27],v[88:91],a[216:219]\n"
"s_cmp_eq_u32 s72,s17\n"
"s_cselect_b32 s72,s16,s72\n"
"s_add_u32 m0,s64,24576\n"
MF "a[236:239],v[28:31],v[88:91],a[236:239]\n"
LD "v7,s[32:35],s72 offen nt lds\n"
MF "a[232:235],v[28:31],v[84:87],a[232:235]\n"
"s_add_u32 m0,s64,25472\n"
"v_add_f32 v59,v59,v123\n"
CV "v103,v122,v123\n"
MF "a[212:215],v[24:27],v[84:87],a[212:215]\n"
LD "v7,s[32:35],s72 offen offset:128 nt lds\n"
MF "a[208:211],v[24:27],v[80:83],a[208:211]\n"
"s_add_u32 m0,s64,26368\n"
MF "a[228:231],v[28:31],v[80:83],a[228:231]\n"
LD "v7,s[32:35],s72 offen offset:256 nt lds\n"
MF "a[224:227],v[28:31],v[76:79],a[224:227]\n"
MF "a[204:207],v[24:27],v[76:79],a[204:207]\n"
MF "a[200:203],v[24:27],v[72:75],a[200:203]\n"
MF "a[220:223],v[28:31],v[72:75],a[220:223]\n"
"s_waitcnt lgkmcnt(0)\n"
MF "v[72:75],v[16:19],v[136:139],v[32:35]\n"
"s_add_u32 s72,s72,6144\n"
"ds_read_b128 v[24:27],v0 offset:50176\n"
"ds_read_b128 v[28:31],v1 offset:50176\n"
MF "v[72:75],v[20:23],v[156:159],v[72:75]\n"
"s_add_u32 m0,s64,30720\n"
"v_exp_f32 v124,v124\n"
MF "v[76:79],v[20:23],v[160:163],v[36:39]\n"
LD "v7,s[32:35],s72 offen nt lds\n"
MF "v[76:79],v[16:19],v[140:143],v[76:79]\n"
"v_exp_f32 v125,v125\n"
MF "v[80:83],v[16:19],v[144:147],v[40:43]\n"
"s_add_u32 m0,s64,31616\n"
"v_exp_f32 v126,v126\n"
MF "v[80:83],v[20:23],v[164:167],v[80:83]\n"
LD "v7,s[32:35],s72 offen offset:128 nt lds\n"
MF "v[84:87],v[20:23],v[168:171],v[44:47]\n"
"v_exp_f32 v127,v127\n"
MF "v[84:87],v[16:19],v[148:151],v[84:87]\n"
"s_add_u32 m0,s64,32512\n"
"v_add_f32 v60,v60,v124\n"
"v_add_f32 v60,v60,v125\n"
MF "v[88:91],v[16:19],v[152:155],v[48:51]\n"
LD "v7,s[32:35],s72 offen offset:256 nt lds\n"
MF "v[88:91],v[20:23],v[172:175],v[88:91]\n"
"s_waitcnt lgkmcnt(0)\n"
"ds_read_b128 v[16:19],v0 offset:51200\n"
"ds_read_b128 v[20:23],v1 offset:51200\n"
MF "v[88:91],v[24:27],v[192:195],v[88:91]\n"
CV "v106,v124,v125\n"
"v_add_f32 v60,v60,v126\n"
MF "v[88:91],v[28:31],v[212:215],v[88:91]\n"
"v_add_f32 v60,v60,v127\n"
CV "v107,v126,v127\n"
MF "v[84:87],v[28:31],v[208:211],v[84:87]\n"
"v_exp_f32 v128,v128\n"
MF "v[84:87],v[24:27],v[188:191],v[84:87]\n"
"v_exp_f32 v129,v129\n"
MF "v[80:83],v[24:27],v[184:187],v[80:83]\n"
"v_exp_f32 v130,v130\n"
MF "v[80:83],v[28:31],v[204:207],v[80:83]\n"
"v_exp_f32 v131,v131\n"
MF "v[76:79],v[28:31],v[200:203],v[76:79]\n"
"v_add_f32 v61,v61,v128\n"
"v_add_f32 v61,v61,v129\n"
MF "v[76:79],v[24:27],v[180:183],v[76:79]\n"
CV "v110,v128,v129\n"
"v_add_f32 v61,v61,v130\n"
MF "v[72:75],v[24:27],v[176:179],v[72:75]\n"
"v_add_f32 v61,v61,v131\n"
CV "v111,v130,v131\n"
MF "v[72:75],v[28:31],v[196:199],v[72:75]\n"
"s_waitcnt lgkmcnt(0)\n"
"ds_read_b128 v[24:27],v0 offset:55296\n"
"ds_read_b128 v[28:31],v1 offset:55296\n"
MF "v[72:75],v[16:19],v[216:219],v[72:75]\n"
MF "v[72:75],v[20:23],v[236:239],v[72:75]\n"
MF "v[76:79],v[20:23],v[240:243],v[76:79]\n"
MF "v[76:79],v[16:19],v[220:223],v[76:79]\n"
MF "v[80:83],v[16:19],v[224:227],v[80:83]\n"
MF "v[80:83],v[20:23],v[244:247],v[80:83]\n"
MF "v[84:87],v[20:23],v[248:251],v[84:87]\n"
MF "v[84:87],v[16:19],v[228:231],v[84:87]\n"
"v_max3_f32 v10,v57,v58,v59\n"
"v_max3_f32 v10,v10,v60,v61\n"
MF "v[88:91],v[16:19],v[232:235],v[88:91]\n"
"v_cmp_ngt_f32 vcc,s95,v10\n"
MF "v[88:91],v[20:23],v[252:255],v[88:91]\n"
"s_waitcnt lgkmcnt(0)\n"
"s_cbranch_vccnz .Lstub_24\n"
".Lback_24:\n"
MF "v[112:115],v[24:27],v[136:139],v[32:35]\n"
"s_add_u32 s74,s74,12288\n"
"ds_read_b128 v[16:19],v0 offset:56320\n"
"ds_read_b128 v[20:23],v1 offset:56320\n"
MF "v[112:115],v[28:31],v[156:159],v[112:115]\n"
"s_cmp_eq_u32 s74,s20\n"
"s_cselect_b32 s74,s70,s74\n"
MF "v[116:119],v[28:31],v[160:163],v[36:39]\n"
"v_exp_f32 v72,v72\n"
MF "v[116:119],v[24:27],v[140:143],v[116:119]\n"
"v_exp_f32 v73,v73\n"
MF "v[120:123],v[24:27],v[144:147],v[40:43]\n"
"v_exp_f32 v74,v74\n"
MF "v[120:123],v[28:31],v[164:167],v[120:123]\n"
"v_exp_f32 v75,v75\n"
MF "v[124:127],v[28:31],v[168:171],v[44:47]\n"
"v_add_f32 v52,v57,v72\n"
"v_add_f32 v52,v52,v73\n"
MF "v[124:127],v[24:27],v[148:151],v[124:127]\n"
CV "v72,v72,v73\n"
"v_add_f32 v52,v52,v74\n"
MF "v[128:131],v[24:27],v[152:155],v[48:51]\n"
"v_add_f32 v52,v52,v75\n"
CV "v73,v74,v75\n"
MF "v[128:131],v[28:31],v[172:175],v[128:131]\n"
"s_waitcnt lgkmcnt(0)\n"
"ds_read_b128 v[24:27],v0 offset:57344\n"
"ds_read_b128 v[28:31],v1 offset:57344\n"
MF "v[128:131],v[16:19],v[192:195],v[128:131]\n"
"v_exp_f32 v76,v76\n"
MF "v[128:131],v[20:23],v[212:215],v[128:131]\n"
"v_exp_f32 v77,v77\n"
MF "v[124:127],v[20:23],v[208:211],v[124:127]\n"
"v_exp_f32 v78,v78\n"
MF "v[124:127],v[16:19],v[188:191],v[124:127]\n"
"v_exp_f32 v79,v79\n"
MF "v[120:123],v[16:19],v[184:187],v[120:123]\n"
"v_add_f32 v53,v58,v76\n"
"v_add_f32 v53,v53,v77\n"
MF "v[120:123],v[20:23],v[204:207],v[120:123]\n"
CV "v76,v76,v77\n"
"v_add_f32 v53,v53,v78\n"
MF "v[116:119],v[20:23],v[200:203],v[116:119]\n"
"s_waitcnt vmcnt(6) lgkmcnt(0)\n"
"v_add_f32 v53,v53,v79\n"
CV "v77,v78,v79\n"
MF "v[116:119],v[16:19],v[180:183],v[116:119]\n"
"s_barrier\n"
MF "v[112:115],v[16:19],v[176:179],v[112:115]\n"
"v_exp_f32 v80,v80\n"
MF "v[112:115],v[20:23],v[196:199],v[112:115]\n"
"s_nop 0\n"
TR "v[16:17],v2\n"
TR "v[18:19],v2 offset:6144\n"
MF "v[112:115],v[24:27],v[216:219],v[112:115]\n"
"s_add_u32 s73,s73,12288\n"
TR "v[20:21],v3\n"
TR "v[22:23],v3 offset:6144\n"
MF "v[112:115],v[28:31],v[236:239],v[112:115]\n"
"s_cmp_eq_u32 s73,s19\n"
"s_cselect_b32 s73,s18,s73\n"
"s_add_u32 m0,s65,49152\n"
MF "v[116:119],v[28:31],v[240:243],v[116:119]\n"
LD "v8,s[36:39],s73 offen nt lds\n"
MF "v[116:119],v[24:27],v[220:223],v[116:119]\n"
"s_add_u32 m0,s65,50048\n"
"v_exp_f32 v81,v81\n"
MF "v[120:123],v[24:27],v[224:227],v[120:123]\n"
LD "v8,s[36:39],s73 offen offset:128 nt lds\n"
MF "v[120:123],v[28:31],v[244:247],v[120:123]\n"
"v_exp_f32 v82,v82\n"
MF "v[124:127],v[28:31],v[248:251],v[124:127]\n"
"s_add_u32 m0,s65,50944\n"
"v_exp_f32 v83,v83\n"
MF "v[124:127],v[24:27],v[228:231],v[124:127]\n"
LD "v8,s[36:39],s73 offen offset:256 nt lds\n"
MF "v[128:131],v[24:27],v[232:235],v[128:131]\n"
"v_add_f32 v54,v59,v80\n"
"v_add_f32 v54,v54,v81\n"
MF "v[128:131],v[28:31],v[252:255],v[128:131]\n"
"s_waitcnt lgkmcnt(0)\n"
TR "v[24:25],v4\n"
TR "v[26:27],v4 offset:6144\n"
MF "a[0:3],v[16:19],v[92:95],a[0:3]\n"
"s_add_u32 m0,s65,49152\n"
TR "v[28:29],v5\n"
TR "v[30:31],v5 offset:6144\n"
MF "a[20:23],v[20:23],v[92:95],a[20:23]\n"
LD "v8,s[36:39],s73 offen offset:3072 nt lds\n"
MF "a[24:27],v[20:23],v[96:99],a[24:27]\n"
CV "v80,v80,v81\n"
"v_add_f32 v54,v54,v82\n"
MF "a[4:7],v[16:19],v[96:99],a[4:7]\n"
"s_add_u32 m0,s65,50048\n"
"v_add_f32 v54,v54,v83\n"
CV "v81,v82,v83\n"
MF "a[8:11],v[16:19],v[100:103],a[8:11]\n"
LD "v8,s[36:39],s73 offen offset:3200 nt lds\n"
MF "a[28:31],v[20:23],v[100:103],a[28:31]\n"
"v_exp_f32 v84,v84\n"
MF "a[32:35],v[20:23],v[104:107],a[32:35]\n"
"s_add_u32 m0,s65,50944\n"
"v_exp_f32 v85,v85\n"
MF "a[12:15],v[16:19],v[104:107],a[12:15]\n"
LD "v8,s[36:39],s73 offen offset:3328 nt lds\n"
MF "a[16:19],v[16:19],v[108:111],a[16:19]\n"
"v_exp_f32 v86,v86\n"
MF "a[36:39],v[20:23],v[108:111],a[36:39]\n"
"s_waitcnt lgkmcnt(0)\n"
TR "v[16:17],v2 offset:1024\n"
TR "v[18:19],v2 offset:7168\n"
MF "a[56:59],v[24:27],v[108:111],a[56:59]\n"
"s_nop 0\n"
TR "v[20:21],v3 offset:1024\n"
TR "v[22:23],v3 offset:7168\n"
MF "a[76:79],v[28:31],v[108:111],a[76:79]\n"
"v_exp_f32 v87,v87\n"
MF "a[72:75],v[28:31],v[104:107],a[72:75]\n"
"v_add_f32 v55,v60,v84\n"
"v_add_f32 v55,v55,v85\n"
MF "a[52:55],v[24:27],v[104:107],a[52:55]\n"
CV "v84,v84,v85\n"
"v_add_f32 v55,v55,v86\n"
MF "a[48:51],v[24:27],v[100:103],a[48:51]\n"
"v_add_f32 v55,v55,v87\n"
CV "v85,v86,v87\n"
MF "a[68:71],v[28:31],v[100:103],a[68:71]\n"
"v_exp_f32 v88,v88\n"
MF "a[64:67],v[28:31],v[96:99],a[64:67]\n"
"v_exp_f32 v89,v89\n"
MF "a[44:47],v[24:27],v[96:99],a[44:47]\n"
"v_exp_f32 v90,v90\n"
MF "a[40:43],v[24:27],v[92:95],a[40:43]\n"
"v_exp_f32 v91,v91\n"
MF "a[60:63],v[28:31],v[92:95],a[60:63]\n"
"s_waitcnt lgkmcnt(0)\n"
TR "v[24:25],v4 offset:1024\n"
TR "v[26:27],v4 offset:7168\n"
MF "a[80:83],v[16:19],v[92:95],a[80:83]\n"
"s_nop 0\n"
TR "v[28:29],v5 offset:1024\n"
TR "v[30:31],v5 offset:7168\n"
MF "a[100:103],v[20:23],v[92:95],a[100:103]\n"
"v_add_f32 v56,v61,v88\n"
"v_add_f32 v56,v56,v89\n"
MF "a[104:107],v[20:23],v[96:99],a[104:107]\n"
CV "v88,v88,v89\n"
"v_add_f32 v56,v56,v90\n"
MF "a[84:87],v[16:19],v[96:99],a[84:87]\n"
"v_add_f32 v56,v56,v91\n"
CV "v89,v90,v91\n"
MF "a[88:91],v[16:19],v[100:103],a[88:91]\n"
"v_exp_f32 v112,v112\n"
MF "a[108:111],v[20:23],v[100:103],a[108:111]\n"
"v_exp_f32 v113,v113\n"
MF "a[112:115],v[20:23],v[104:107],a[112:115]\n"
"v_exp_f32 v114,v114\n"
MF "a[92:95],v[16:19],v[104:107],a[92:95]\n"
"v_exp_f32 v115,v115\n"
MF "a[96:99],v[16:19],v[108:111],a[96:99]\n"
"v_add_f32 v52,v52,v112\n"
"v_add_f32 v52,v52,v113\n"
MF "a[116:119],v[20:23],v[108:111],a[116:119]\n"
"s_waitcnt lgkmcnt(0)\n"
TR "v[16:17],v2 offset:2048\n"
TR "v[18:19],v2 offset:8192\n"
MF "a[136:139],v[24:27],v[108:111],a[136:139]\n"
"s_nop 0\n"
TR "v[20:21],v3 offset:2048\n"
TR "v[22:23],v3 offset:8192\n"
MF "a[156:159],v[28:31],v[108:111],a[156:159]\n"
CV "v74,v112,v113\n"
"v_add_f32 v52,v52,v114\n"
MF "a[152:155],v[28:31],v[104:107],a[152:155]\n"
"v_add_f32 v52,v52,v115\n"
CV "v75,v114,v115\n"
MF "a[132:135],v[24:27],v[104:107],a[132:135]\n"
"v_exp_f32 v116,v116\n"
MF "a[128:131],v[24:27],v[100:103],a[128:131]\n"
"v_exp_f32 v117,v117\n"
MF "a[148:151],v[28:31],v[100:103],a[148:151]\n"
"v_exp_f32 v118,v118\n"
MF "a[144:147],v[28:31],v[96:99],a[144:147]\n"
"v_exp_f32 v119,v119\n"
MF "a[124:127],v[24:27],v[96:99],a[124:127]\n"
"v_add_f32 v53,v53,v116\n"
"v_add_f32 v53,v53,v117\n"
MF "a[120:123],v[24:27],v[92:95],a[120:123]\n"
CV "v78,v116,v117\n"
"v_add_f32 v53,v53,v118\n"
MF "a[140:143],v[28:31],v[92:95],a[140:143]\n"
"s_waitcnt lgkmcnt(0)\n"
TR "v[24:25],v4 offset:2048\n"
TR "v[26:27],v4 offset:8192\n"
MF "a[160:163],v[16:19],v[92:95],a[160:163]\n"
"s_nop 0\n"
TR "v[28:29],v5 offset:2048\n"
TR "v[30:31],v5 offset:8192\n"
MF "a[180:183],v[20:23],v[92:95],a[180:183]\n"
"v_add_f32 v53,v53,v119\n"
CV "v79,v118,v119\n"
MF "a[184:187],v[20:23],v[96:99],a[184:187]\n"
"v_exp_f32 v120,v120\n"
MF "a[164:167],v[16:19],v[96:99],a[164:167]\n"
"v_exp_f32 v121,v121\n"
MF "a[168:171],v[16:19],v[100:103],a[168:171]\n"
"v_exp_f32 v122,v122\n"
MF "a[188:191],v[20:23],v[100:103],a[188:191]\n"
"v_exp_f32 v123,v123\n"
MF "a[192:195],v[20:23],v[104:107],a[192:195]\n"
"v_add_f32 v54,v54,v120\n"
"v_add_f32 v54,v54,v121\n"
MF "a[172:175],v[16:19],v[104:107],a[172:175]\n"
"s_waitcnt vmcnt(6) lgkmcnt(0)\n"
CV "v82,v120,v121\n"
"v_add_f32 v54,v54,v122\n"
MF "a[176:179],v[16:19],v[108:111],a[176:179]\n"
"s_barrier\n"
MF "a[196:199],v[20:23],v[108:111],a[196:199]\n"
"s_add_u32 s72,s72,6144\n"
"ds_read_b128 v[16:19],v0 offset:24576\n"
"ds_read_b128 v[20:23],v1 offset:24576\n"
MF "a[216:219],v[24:27],v[108:111],a[216:219]\n"
"s_cmp_eq_u32 s72,s17\n"
"s_cselect_b32 s72,s16,s72\n"
"s_add_u32 m0,s64,0\n"
MF "a[236:239],v[28:31],v[108:111],a[236:239]\n"
LD "v7,s[32:35],s72 offen nt lds\n"
MF "a[232:235],v[28:31],v[104:107],a[232:235]\n"
"s_add_u32 m0,s64,896\n"
"v_add_f32 v54,v54,v123\n"
CV "v83,v122,v123\n"
MF "a[212:215],v[24:27],v[104:107],a[212:215]\n"
LD "v7,s[32:35],s72 offen offset:128 nt lds\n"
MF "a[208:211],v[24:27],v[100:103],a[208:211]\n"
"s_add_u32 m0,s64,1792\n"
MF "a[228:231],v[28:31],v[100:103],a[228:231]\n"
LD "v7,s[32:35],s72 offen offset:256 nt lds\n"
MF "a[224:227],v[28:31],v[96:99],a[224:227]\n"
MF "a[204:207],v[24:27],v[96:99],a[204:207]\n"
MF "a[200:203],v[24:27],v[92:95],a[200:203]\n"
MF "a[220:223],v[28:31],v[92:95],a[220:223]\n"
"s_waitcnt lgkmcnt(0)\n"
MF "v[92:95],v[16:19],v[136:139],v[32:35]\n"
"s_add_u32 s72,s72,6144\n"
"ds_read_b128 v[24:27],v0 offset:25600\n"
"ds_read_b128 v[28:31],v1 offset:25600\n"
MF "v[92:95],v[20:23],v[156:159],v[92:95]\n"
"s_add_u32 m0,s64,6144\n"
"v_exp_f32 v124,v124\n"
MF "v[96:99],v[20:23],v[160:163],v[36:39]\n"
LD "v7,s[32:35],s72 offen nt lds\n"
MF "v[96:99],v[16:19],v[140:143],v[96:99]\n"
"v_exp_f32 v125,v125\n"
MF "v[100:103],v[16:19],v[144:147],v[40:43]\n"
"s_add_u32 m0,s64,7040\n"
"v_exp_f32 v126,v126\n"
MF "v[100:103],v[20:23],v[164:167],v[100:103]\n"
LD "v7,s[32:35],s72 offen offset:128 nt lds\n"
MF "v[104:107],v[20:23],v[168:171],v[44:47]\n"
"v_exp_f32 v127,v127\n"
MF "v[104:107],v[16:19],v[148:151],v[104:107]\n"
"s_add_u32 m0,s64,7936\n"
"v_add_f32 v55,v55,v124\n"
"v_add_f32 v55,v55,v125\n"
MF "v[108:111],v[16:19],v[152:155],v[48:51]\n"
LD "v7,s[32:35],s72 offen offset:256 nt lds\n"
MF "v[108:111],v[20:23],v[172:175],v[108:111]\n"
"s_waitcnt lgkmcnt(0)\n"
"ds_read_b128 v[16:19],v0 offset:26624\n"
"ds_read_b128 v[20:23],v1 offset:26624\n"
MF "v[108:111],v[24:27],v[192:195],v[108:111]\n"
CV "v86,v124,v125\n"
"v_add_f32 v55,v55,v126\n"
MF "v[108:111],v[28:31],v[212:215],v[108:111]\n"
"v_add_f32 v55,v55,v127\n"
CV "v87,v126,v127\n"
MF "v[104:107],v[28:31],v[208:211],v[104:107]\n"
"v_exp_f32 v128,v128\n"
MF "v[104:107],v[24:27],v[188:191],v[104:107]\n"
"v_exp_f32 v129,v129\n"
MF "v[100:103],v[24:27],v[184:187],v[100:103]\n"
"v_exp_f32 v130,v130\n"
MF "v[100:103],v[28:31],v[204:207],v[100:103]\n"
"v_exp_f32 v131,v131\n"
MF "v[96:99],v[28:31],v[200:203],v[96:99]\n"
"v_add_f32 v56,v56,v128\n"
"v_add_f32 v56,v56,v129\n"
MF "v[96:99],v[24:27],v[180:183],v[96:99]\n"
CV "v90,v128,v129\n"
"v_add_f32 v56,v56,v130\n"
MF "v[92:95],v[24:27],v[176:179],v[92:95]\n"
"v_add_f32 v56,v56,v131\n"
CV "v91,v130,v131\n"
MF "v[92:95],v[28:31],v[196:199],v[92:95]\n"
"s_waitcnt lgkmcnt(0)\n"
"ds_read_b128 v[24:27],v0 offset:30720\n"
"ds_read_b128 v[28:31],v1 offset:30720\n"
MF "v[92:95],v[16:19],v[216:219],v[92:95]\n"
MF "v[92:95],v[20:23],v[236:239],v[92:95]\n"
MF "v[96:99],v[20:23],v[240:243],v[96:99]\n"
MF "v[96:99],v[16:19],v[220:223],v[96:99]\n"
MF "v[100:103],v[16:19],v[224:227],v[100:103]\n"
MF "v[100:103],v[20:23],v[244:247],v[100:103]\n"
MF "v[104:107],v[20:23],v[248:251],v[104:107]\n"
MF "v[104:107],v[16:19],v[228:231],v[104:107]\n"
"v_max3_f32 v10,v52,v53,v54\n"
"v_max3_f32 v10,v10,v55,v56\n"
MF "v[108:111],v[16:19],v[232:235],v[108:111]\n"
"v_cmp_ngt_f32 vcc,s77,v10\n"
MF "v[108:111],v[20:23],v[252:255],v[108:111]\n"
"s_waitcnt lgkmcnt(0)\n"
"s_cbranch_vccnz .Lstub_25\n"
".Lback_25:\n"
MF "v[112:115],v[24:27],v[136:139],v[32:35]\n"
"s_add_u32 s74,s74,12288\n"
"ds_read_b128 v[16:19],v0 offset:31744\n"
"ds_read_b128 v[20:23],v1 offset:31744\n"
MF "v[112:115],v[28:31],v[156:159],v[112:115]\n"
"s_cmp_eq_u32 s74,s20\n"
"s_cselect_b32 s74,s70,s74\n"
MF "v[116:119],v[28:31],v[160:163],v[36:39]\n"
"v_exp_f32 v92,v92\n"
MF "v[116:119],v[24:27],v[140:143],v[116:119]\n"
"v_exp_f32 v93,v93\n"
MF "v[120:123],v[24:27],v[144:147],v[40:43]\n"
"v_exp_f32 v94,v94\n"
MF "v[120:123],v[28:31],v[164:167],v[120:123]\n"
"v_exp_f32 v95,v95\n"
MF "v[124:127],v[28:31],v[168:171],v[44:47]\n"
"v_add_f32 v57,v52,v92\n"
"v_add_f32 v57,v57,v93\n"
MF "v[124:127],v[24:27],v[148:151],v[124:127]\n"
CV "v92,v92,v93\n"
"v_add_f32 v57,v57,v94\n"
MF "v[128:131],v[24:27],v[152:155],v[48:51]\n"
"v_add_f32 v57,v57,v95\n"
CV "v93,v94,v95\n"
MF "v[128:131],v[28:31],v[172:175],v[128:131]\n"
"s_waitcnt lgkmcnt(0)\n"
"ds_read_b128 v[24:27],v0 offset:32768\n"
"ds_read_b128 v[28:31],v1 offset:32768\n"
MF "v[128:131],v[16:19],v[192:195],v[128:131]\n"
"s_sub_u32 s61,s61,1\n"
"v_exp_f32 v96,v96\n"
MF "v[128:131],v[20:23],v[212:215],v[128:131]\n"
"s_mov_b32 s95,0\n"
"v_exp_f32 v97,v97\n"
MF "v[124:127],v[20:23],v[208:211],v[124:127]\n"
"v_exp_f32 v98,v98\n"
MF "v[124:127],v[16:19],v[188:191],v[124:127]\n"
"v_exp_f32 v99,v99\n"
MF "v[120:123],v[16:19],v[184:187],v[120:123]\n"
"v_add_f32 v58,v53,v96\n"
"v_add_f32 v58,v58,v97\n"
MF "v[120:123],v[20:23],v[204:207],v[120:123]\n"
CV "v96,v96,v97\n"
"v_add_f32 v58,v58,v98\n"
MF "v[116:119],v[20:23],v[200:203],v[116:119]\n"
"s_waitcnt vmcnt(6) lgkmcnt(0)\n"
"v_add_f32 v58,v58,v99\n"
CV "v97,v98,v99\n"
MF "v[116:119],v[16:19],v[180:183],v[116:119]\n"
"s_barrier\n"
MF "v[112:115],v[16:19],v[176:179],v[112:115]\n"
"v_exp_f32 v100,v100\n"
MF "v[112:115],v[20:23],v[196:199],v[112:115]\n"
"s_nop 0\n"
TR "v[16:17],v2 offset:49152\n"
TR "v[18:19],v2 offset:55296\n"
MF "v[112:115],v[24:27],v[216:219],v[112:115]\n"
"s_add_u32 s73,s73,12288\n"
TR "v[20:21],v3 offset:49152\n"
TR "v[22:23],v3 offset:55296\n"
MF "v[112:115],v[28:31],v[236:239],v[112:115]\n"
"s_cmp_eq_u32 s73,s19\n"
"s_cselect_b32 s73,s18,s73\n"
"s_add_u32 m0,s65,24576\n"
MF "v[116:119],v[28:31],v[240:243],v[116:119]\n"
LD "v8,s[36:39],s73 offen nt lds\n"
MF "v[116:119],v[24:27],v[220:223],v[116:119]\n"
"s_add_u32 m0,s65,25472\n"
"v_exp_f32 v101,v101\n"
MF "v[120:123],v[24:27],v[224:227],v[120:123]\n"
LD "v8,s[36:39],s73 offen offset:128 nt lds\n"
MF "v[120:123],v[28:31],v[244:247],v[120:123]\n"
"v_exp_f32 v102,v102\n"
MF "v[124:127],v[28:31],v[248:251],v[124:127]\n"
"s_add_u32 m0,s65,26368\n"
"v_exp_f32 v103,v103\n"
MF "v[124:127],v[24:27],v[228:231],v[124:127]\n"
LD "v8,s[36:39],s73 offen offset:256 nt lds\n"
MF "v[128:131],v[24:27],v[232:235],v[128:131]\n"
"v_add_f32 v59,v54,v100\n"
"v_add_f32 v59,v59,v101\n"
MF "v[128:131],v[28:31],v[252:255],v[128:131]\n"
"s_waitcnt lgkmcnt(0)\n"
TR "v[24:25],v4 offset:49152\n"
TR "v[26:27],v4 offset:55296\n"
MF "a[0:3],v[16:19],v[72:75],a[0:3]\n"
"s_add_u32 m0,s65,24576\n"
TR "v[28:29],v5 offset:49152\n"
TR "v[30:31],v5 offset:55296\n"
MF "a[20:23],v[20:23],v[72:75],a[20:23]\n"
LD "v8,s[36:39],s73 offen offset:3072 nt lds\n"
MF "a[24:27],v[20:23],v[76:79],a[24:27]\n"
CV "v100,v100,v101\n"
"v_add_f32 v59,v59,v102\n"
MF "a[4:7],v[16:19],v[76:79],a[4:7]\n"
"s_add_u32 m0,s65,25472\n"
"v_add_f32 v59,v59,v103\n"
CV "v101,v102,v103\n"
MF "a[8:11],v[16:19],v[80:83],a[8:11]\n"
LD "v8,s[36:39],s73 offen offset:3200 nt lds\n"
MF "a[28:31],v[20:23],v[80:83],a[28:31]\n"
"v_exp_f32 v104,v104\n"
MF "a[32:35],v[20:23],v[84:87],a[32:35]\n"
"s_add_u32 m0,s65,26368\n"
"v_exp_f32 v105,v105\n"
MF "a[12:15],v[16:19],v[84:87],a[12:15]\n"
LD "v8,s[36:39],s73 offen offset:3328 nt lds\n"
MF "a[16:19],v[16:19],v[88:91],a[16:19]\n"
"v_exp_f32 v106,v106\n"
MF "a[36:39],v[20:23],v[88:91],a[36:39]\n"
"s_waitcnt lgkmcnt(0)\n"
TR "v[16:17],v2 offset:50176\n"
TR "v[18:19],v2 offset:56320\n"
MF "a[56:59],v[24:27],v[88:91],a[56:59]\n"
"s_nop 0\n"
TR "v[20:21],v3 offset:50176\n"
TR "v[22:23],v3 offset:56320\n"
MF "a[76:79],v[28:31],v[88:91],a[76:79]\n"
"v_exp_f32 v107,v107\n"
MF "a[72:75],v[28:31],v[84:87],a[72:75]\n"
"v_add_f32 v60,v55,v104\n"
"v_add_f32 v60,v60,v105\n"
MF "a[52:55],v[24:27],v[84:87],a[52:55]\n"
CV "v104,v104,v105\n"
"v_add_f32 v60,v60,v106\n"
MF "a[48:51],v[24:27],v[80:83],a[48:51]\n"
"v_add_f32 v60,v60,v107\n"
CV "v105,v106,v107\n"
MF "a[68:71],v[28:31],v[80:83],a[68:71]\n"
"v_exp_f32 v108,v108\n"
MF "a[64:67],v[28:31],v[76:79],a[64:67]\n"
"v_exp_f32 v109,v109\n"
MF "a[44:47],v[24:27],v[76:79],a[44:47]\n"
"v_exp_f32 v110,v110\n"
MF "a[40:43],v[24:27],v[72:75],a[40:43]\n"
"v_exp_f32 v111,v111\n"
MF "a[60:63],v[28:31],v[72:75],a[60:63]\n"
"s_waitcnt lgkmcnt(0)\n"
TR "v[24:25],v4 offset:50176\n"
TR "v[26:27],v4 offset:56320\n"
MF "a[80:83],v[16:19],v[72:75],a[80:83]\n"
"s_nop 0\n"
TR "v[28:29],v5 offset:50176\n"
TR "v[30:31],v5 offset:56320\n"
MF "a[100:103],v[20:23],v[72:75],a[100:103]\n"
"v_add_f32 v61,v56,v108\n"
"v_add_f32 v61,v61,v109\n"
MF "a[104:107],v[20:23],v[76:79],a[104:107]\n"
CV "v108,v108,v109\n"
"v_add_f32 v61,v61,v110\n"
MF "a[84:87],v[16:19],v[76:79],a[84:87]\n"
"v_add_f32 v61,v61,v111\n"
CV "v109,v110,v111\n"
MF "a[88:91],v[16:19],v[80:83],a[88:91]\n"
"v_exp_f32 v112,v112\n"
MF "a[108:111],v[20:23],v[80:83],a[108:111]\n"
"v_exp_f32 v113,v113\n"
MF "a[112:115],v[20:23],v[84:87],a[112:115]\n"
"v_exp_f32 v114,v114\n"
MF "a[92:95],v[16:19],v[84:87],a[92:95]\n"
"v_exp_f32 v115,v115\n"
MF "a[96:99],v[16:19],v[88:91],a[96:99]\n"
"v_add_f32 v57,v57,v112\n"
"v_add_f32 v57,v57,v113\n"
MF "a[116:119],v[20:23],v[88:91],a[116:119]\n"
"s_waitcnt lgkmcnt(0)\n"
TR "v[16:17],v2 offset:51200\n"
TR "v[18:19],v2 offset:57344\n"
MF "a[136:139],v[24:27],v[88:91],a[136:139]\n"
"s_nop 0\n"
TR "v[20:21],v3 offset:51200\n"
TR "v[22:23],v3 offset:57344\n"
MF "a[156:159],v[28:31],v[88:91],a[156:159]\n"
CV "v94,v112,v113\n"
"v_add_f32 v57,v57,v114\n"
MF "a[152:155],v[28:31],v[84:87],a[152:155]\n"
"v_add_f32 v57,v57,v115\n"
CV "v95,v114,v115\n"
MF "a[132:135],v[24:27],v[84:87],a[132:135]\n"
"v_exp_f32 v116,v116\n"
MF "a[128:131],v[24:27],v[80:83],a[128:131]\n"
"v_exp_f32 v117,v117\n"
MF "a[148:151],v[28:31],v[80:83],a[148:151]\n"
"v_exp_f32 v118,v118\n"
MF "a[144:147],v[28:31],v[76:79],a[144:147]\n"
"v_exp_f32 v119,v119\n"
MF "a[124:127],v[24:27],v[76:79],a[124:127]\n"
"v_add_f32 v58,v58,v116\n"
"v_add_f32 v58,v58,v117\n"
MF "a[120:123],v[24:27],v[72:75],a[120:123]\n"
CV "v98,v116,v117\n"
"v_add_f32 v58,v58,v118\n"
MF "a[140:143],v[28:31],v[72:75],a[140:143]\n"
"s_waitcnt lgkmcnt(0)\n"
TR "v[24:25],v4 offset:51200\n"
TR "v[26:27],v4 offset:57344\n"
MF "a[160:163],v[16:19],v[72:75],a[160:163]\n"
"s_nop 0\n"
TR "v[28:29],v5 offset:51200\n"
TR "v[30:31],v5 offset:57344\n"
MF "a[180:183],v[20:23],v[72:75],a[180:183]\n"
"v_add_f32 v58,v58,v119\n"
CV "v99,v118,v119\n"
MF "a[184:187],v[20:23],v[76:79],a[184:187]\n"
"v_exp_f32 v120,v120\n"
MF "a[164:167],v[16:19],v[76:79],a[164:167]\n"
"v_exp_f32 v121,v121\n"
MF "a[168:171],v[16:19],v[80:83],a[168:171]\n"
"v_exp_f32 v122,v122\n"
MF "a[188:191],v[20:23],v[80:83],a[188:191]\n"
"v_exp_f32 v123,v123\n"
MF "a[192:195],v[20:23],v[84:87],a[192:195]\n"
"v_add_f32 v59,v59,v120\n"
"v_add_f32 v59,v59,v121\n"
MF "a[172:175],v[16:19],v[84:87],a[172:175]\n"
"s_waitcnt vmcnt(6) lgkmcnt(0)\n"
CV "v102,v120,v121\n"
"v_add_f32 v59,v59,v122\n"
MF "a[176:179],v[16:19],v[88:91],a[176:179]\n"
"s_barrier\n"
MF "a[196:199],v[20:23],v[88:91],a[196:199]\n"
"s_add_u32 s72,s72,6144\n"
"ds_read_b128 v[16:19],v0\n"
"ds_read_b128 v[20:23],v1\n"
MF "a[216:219],v[24:27],v[88:91],a[216:219]\n"
"s_cmp_eq_u32 s72,s17\n"
"s_cselect_b32 s72,s16,s72\n"
"s_add_u32 m0,s64,49152\n"
MF "a[236:239],v[28:31],v[88:91],a[236:239]\n"
LD "v7,s[32:35],s72 offen nt lds\n"
MF "a[232:235],v[28:31],v[84:87],a[232:235]\n"
"s_add_u32 m0,s64,50048\n"
"v_add_f32 v59,v59,v123\n"
CV "v103,v122,v123\n"
MF "a[212:215],v[24:27],v[84:87],a[212:215]\n"
LD "v7,s[32:35],s72 offen offset:128 nt lds\n"
MF "a[208:211],v[24:27],v[80:83],a[208:211]\n"
"s_add_u32 m0,s64,50944\n"
MF "a[228:231],v[28:31],v[80:83],a[228:231]\n"
LD "v7,s[32:35],s72 offen offset:256 nt lds\n"
MF "a[224:227],v[28:31],v[76:79],a[224:227]\n"
MF "a[204:207],v[24:27],v[76:79],a[204:207]\n"
"s_waitcnt lgkmcnt(0)\n"
MF "a[200:203],v[24:27],v[72:75],a[200:203]\n"
"s_cmp_lg_u32 s61,0\n"
MF "a[220:223],v[28:31],v[72:75],a[220:223]\n"
"s_cbranch_scc1 .Laloop\n"
".Lepi2:\n"
MF "v[72:75],v[16:19],v[136:139],v[32:35]\n"
"s_add_u32 s72,s72,6144\n"
"ds_read_b128 v[24:27],v0 offset:1024\n"
"ds_read_b128 v[28:31],v1 offset:1024\n"
MF "v[72:75],v[20:23],v[156:159],v[72:75]\n"
"s_add_u32 m0,s64,55296\n"
"v_exp_f32 v124,v124\n"
MF "v[76:79],v[20:23],v[160:163],v[36:39]\n"
LD "v7,s[32:35],s72 offen nt lds\n"
MF "v[76:79],v[16:19],v[140:143],v[76:79]\n"
"v_exp_f32 v125,v125\n"
MF "v[80:83],v[16:19],v[144:147],v[40:43]\n"
"s_add_u32 m0,s64,56192\n"
"v_exp_f32 v126,v126\n"
MF "v[80:83],v[20:23],v[164:167],v[80:83]\n"
LD "v7,s[32:35],s72 offen offset:128 nt lds\n"
MF "v[84:87],v[20:23],v[168:171],v[44:47]\n"
"v_exp_f32 v127,v127\n"
MF "v[84:87],v[16:19],v[148:151],v[84:87]\n"
"s_add_u32 m0,s64,57088\n"
"v_add_f32 v60,v60,v124\n"
"v_add_f32 v60,v60,v125\n"
MF "v[88:91],v[16:19],v[152:155],v[48:51]\n"
LD "v7,s[32:35],s72 offen offset:256 nt lds\n"
MF "v[88:91],v[20:23],v[172:175],v[88:91]\n"
"s_waitcnt lgkmcnt(0)\n"
"ds_read_b128 v[16:19],v0 offset:2048\n"
"ds_read_b128 v[20:23],v1 offset:2048\n"
MF "v[88:91],v[24:27],v[192:195],v[88:91]\n"
CV "v106,v124,v125\n"
"v_add_f32 v60,v60,v126\n"
MF "v[88:91],v[28:31],v[212:215],v[88:91]\n"
"v_add_f32 v60,v60,v127\n"
CV "v107,v126,v127\n"
MF "v[84:87],v[28:31],v[208:211],v[84:87]\n"
"v_exp_f32 v128,v128\n"
MF "v[84:87],v[24:27],v[188:191],v[84:87]\n"
"v_exp_f32 v129,v129\n"
MF "v[80:83],v[24:27],v[184:187],v[80:83]\n"
"v_exp_f32 v130,v130\n"
MF "v[80:83],v[28:31],v[204:207],v[80:83]\n"
"v_exp_f32 v131,v131\n"
MF "v[76:79],v[28:31],v[200:203],v[76:79]\n"
"v_add_f32 v61,v61,v128\n"
"v_add_f32 v61,v61,v129\n"
MF "v[76:79],v[24:27],v[180:183],v[76:79]\n"
CV "v110,v128,v129\n"
"v_add_f32 v61,v61,v130\n"
MF "v[72:75],v[24:27],v[176:179],v[72:75]\n"
"v_add_f32 v61,v61,v131\n"
CV "v111,v130,v131\n"
MF "v[72:75],v[28:31],v[196:199],v[72:75]\n"
"s_waitcnt lgkmcnt(0)\n"
"ds_read_b128 v[24:27],v0 offset:6144\n"
"ds_read_b128 v[28:31],v1 offset:6144\n"
MF "v[72:75],v[16:19],v[216:219],v[72:75]\n"
MF "v[72:75],v[20:23],v[236:239],v[72:75]\n"
MF "v[76:79],v[20:23],v[240:243],v[76:79]\n"
MF "v[76:79],v[16:19],v[220:223],v[76:79]\n"
MF "v[80:83],v[16:19],v[224:227],v[80:83]\n"
MF "v[80:83],v[20:23],v[244:247],v[80:83]\n"
MF "v[84:87],v[20:23],v[248:251],v[84:87]\n"
MF "v[84:87],v[16:19],v[228:231],v[84:87]\n"
"v_max3_f32 v10,v57,v58,v59\n"
"v_max3_f32 v10,v10,v60,v61\n"
MF "v[88:91],v[16:19],v[232:235],v[88:91]\n"
"v_cmp_le_f32 vcc,0x47800000,v10\n"
MF "v[88:91],v[20:23],v[252:255],v[88:91]\n"
"s_waitcnt lgkmcnt(0)\n"
"s_cbranch_vccnz .Lstub_26\n"
".Lback_26:\n"
MF "v[112:115],v[24:27],v[136:139],v[32:35]\n"
"s_add_u32 s74,s74,12288\n"
"ds_read_b128 v[16:19],v0 offset:7168\n"
"ds_read_b128 v[20:23],v1 offset:7168\n"
MF "v[112:115],v[28:31],v[156:159],v[112:115]\n"
".Lpg22h:\n"
"s_cbranch_execz .Lpg26h\n"
"s_cmp_eq_u32 s74,s20\n"
"s_cselect_b32 s74,s70,s74\n"
MF "v[116:119],v[28:31],v[160:163],v[36:39]\n"
"v_exp_f32 v72,v72\n"
MF "v[116:119],v[24:27],v[140:143],v[116:119]\n"
"v_exp_f32 v73,v73\n"
MF "v[120:123],v[24:27],v[144:147],v[40:43]\n"
"v_exp_f32 v74,v74\n"
MF "v[120:123],v[28:31],v[164:167],v[120:123]\n"
"v_exp_f32 v75,v75\n"
MF "v[124:127],v[28:31],v[168:171],v[44:47]\n"
"v_add_f32 v52,v57,v72\n"
"v_add_f32 v52,v52,v73\n"
MF "v[124:127],v[24:27],v[148:151],v[124:127]\n"
CV "v72,v72,v73\n"
"v_add_f32 v52,v52,v74\n"
MF "v[128:131],v[24:27],v[152:155],v[48:51]\n"
"v_add_f32 v52,v52,v75\n"
CV "v73,v74,v75\n"
MF "v[128:131],v[28:31],v[172:175],v[128:131]\n"
"s_waitcnt lgkmcnt(0)\n"
"ds_read_b128 v[24:27],v0 offset:8192\n"
"ds_read_b128 v[28:31],v1 offset:8192\n"
MF "v[128:131],v[16:19],v[192:195],v[128:131]\n"
"v_exp_f32 v76,v76\n"
MF "v[128:131],v[20:23],v[212:215],v[128:131]\n"
"v_exp_f32 v77,v77\n"
MF "v[124:127],v[20:23],v[208:211],v[124:127]\n"
"v_exp_f32 v78,v78\n"
MF "v[124:127],v[16:19],v[188:191],v[124:127]\n"
"v_exp_f32 v79,v79\n"
MF "v[120:123],v[16:19],v[184:187],v[120:123]\n"
"v_add_f32 v53,v58,v76\n"
"v_add_f32 v53,v53,v77\n"
MF "v[120:123],v[20:23],v[204:207],v[120:123]\n"
CV "v76,v76,v77\n"
"v_add_f32 v53,v53,v78\n"
MF "v[116:119],v[20:23],v[200:203],v[116:119]\n"
"s_waitcnt vmcnt(6) lgkmcnt(0)\n"
"v_add_f32 v53,v53,v79\n"
CV "v77,v78,v79\n"
MF "v[116:119],v[16:19],v[180:183],v[116:119]\n"
"s_barrier\n"
MF "v[112:115],v[16:19],v[176:179],v[112:115]\n"
"v_lshrrev_b32 v7,7,v9\n"
"v_mad_u32_u24 v7,v7,s6,v8\n"
MF "v[112:115],v[20:23],v[196:199],v[112:115]\n"
"s_nop 0\n"
TR "v[16:17],v2 offset:24576\n"
TR "v[18:19],v2 offset:30720\n"
MF "v[112:115],v[24:27],v[216:219],v[112:115]\n"
"s_add_u32 s73,s73,12288\n"
TR "v[20:21],v3 offset:24576\n"
TR "v[22:23],v3 offset:30720\n"
MF "v[112:115],v[28:31],v[236:239],v[112:115]\n"
"s_cmp_eq_u32 s73,s19\n"
"s_cselect_b32 s73,s18,s73\n"
"s_add_u32 m0,s65,0\n"
MF "v[116:119],v[28:31],v[240:243],v[116:119]\n"
LD "v8,s[36:39],s73 offen nt lds\n"
MF "v[116:119],v[24:27],v[220:223],v[116:119]\n"
"s_add_u32 m0,s65,896\n"
"v_exp_f32 v80,v80\n"
MF "v[120:123],v[24:27],v[224:227],v[120:123]\n"
LD "v8,s[36:39],s73 offen offset:128 nt lds\n"
MF "v[120:123],v[28:31],v[244:247],v[120:123]\n"
"v_exp_f32 v81,v81\n"
MF "v[124:127],v[28:31],v[248:251],v[124:127]\n"
"s_add_u32 m0,s65,1792\n"
"v_exp_f32 v82,v82\n"
MF "v[124:127],v[24:27],v[228:231],v[124:127]\n"
LD "v8,s[36:39],s73 offen offset:256 nt lds\n"
MF "v[128:131],v[24:27],v[232:235],v[128:131]\n"
"v_exp_f32 v83,v83\n"
MF "v[128:131],v[28:31],v[252:255],v[128:131]\n"
"s_waitcnt lgkmcnt(0)\n"
TR "v[24:25],v4 offset:24576\n"
TR "v[26:27],v4 offset:30720\n"
MF "a[0:3],v[16:19],v[92:95],a[0:3]\n"
"s_add_u32 m0,s65,0\n"
TR "v[28:29],v5 offset:24576\n"
TR "v[30:31],v5 offset:30720\n"
MF "a[20:23],v[20:23],v[92:95],a[20:23]\n"
LD "v8,s[36:39],s73 offen offset:3072 nt lds\n"
MF "a[24:27],v[20:23],v[96:99],a[24:27]\n"
"v_add_f32 v54,v59,v80\n"
"v_add_f32 v54,v54,v81\n"
MF "a[4:7],v[16:19],v[96:99],a[4:7]\n"
"s_add_u32 m0,s65,896\n"
CV "v80,v80,v81\n"
"v_add_f32 v54,v54,v82\n"
MF "a[8:11],v[16:19],v[100:103],a[8:11]\n"
LD "v8,s[36:39],s73 offen offset:3200 nt lds\n"
MF "a[28:31],v[20:23],v[100:103],a[28:31]\n"
"v_add_f32 v54,v54,v83\n"
CV "v81,v82,v83\n"
MF "a[32:35],v[20:23],v[104:107],a[32:35]\n"
"s_add_u32 m0,s65,1792\n"
"v_exp_f32 v84,v84\n"
MF "a[12:15],v[16:19],v[104:107],a[12:15]\n"
LD "v8,s[36:39],s73 offen offset:3328 nt lds\n"
MF "a[16:19],v[16:19],v[108:111],a[16:19]\n"
"v_exp_f32 v85,v85\n"
MF "a[36:39],v[20:23],v[108:111],a[36:39]\n"
"s_waitcnt lgkmcnt(0)\n"
TR "v[16:17],v2 offset:25600\n"
TR "v[18:19],v2 offset:31744\n"
MF "a[56:59],v[24:27],v[108:111],a[56:59]\n"
"s_nop 0\n"
TR "v[20:21],v3 offset:25600\n"
TR "v[22:23],v3 offset:31744\n"
MF "a[76:79],v[28:31],v[108:111],a[76:79]\n"
"v_exp_f32 v86,v86\n"
MF "a[72:75],v[28:31],v[104:107],a[72:75]\n"
"v_exp_f32 v87,v87\n"
MF "a[52:55],v[24:27],v[104:107],a[52:55]\n"
"v_add_f32 v55,v60,v84\n"
"v_add_f32 v55,v55,v85\n"
MF "a[48:51],v[24:27],v[100:103],a[48:51]\n"
CV "v84,v84,v85\n"
"v_add_f32 v55,v55,v86\n"
MF "a[68:71],v[28:31],v[100:103],a[68:71]\n"
"v_add_f32 v55,v55,v87\n"
CV "v85,v86,v87\n"
MF "a[64:67],v[28:31],v[96:99],a[64:67]\n"
"v_exp_f32 v88,v88\n"
MF "a[44:47],v[24:27],v[96:99],a[44:47]\n"
"v_exp_f32 v89,v89\n"
MF "a[40:43],v[24:27],v[92:95],a[40:43]\n"
"v_exp_f32 v90,v90\n"
MF "a[60:63],v[28:31],v[92:95],a[60:63]\n"
"s_waitcnt lgkmcnt(0)\n"
TR "v[24:25],v4 offset:25600\n"
TR "v[26:27],v4 offset:31744\n"
MF "a[80:83],v[16:19],v[92:95],a[80:83]\n"
"s_nop 0\n"
TR "v[28:29],v5 offset:25600\n"
TR "v[30:31],v5 offset:31744\n"
MF "a[100:103],v[20:23],v[92:95],a[100:103]\n"
"v_exp_f32 v91,v91\n"
MF "a[104:107],v[20:23],v[96:99],a[104:107]\n"
"v_add_f32 v56,v61,v88\n"
"v_add_f32 v56,v56,v89\n"
MF "a[84:87],v[16:19],v[96:99],a[84:87]\n"
CV "v88,v88,v89\n"
"v_add_f32 v56,v56,v90\n"
MF "a[88:91],v[16:19],v[100:103],a[88:91]\n"
"v_add_f32 v56,v56,v91\n"
CV "v89,v90,v91\n"
MF "a[108:111],v[20:23],v[100:103],a[108:111]\n"
"v_exp_f32 v112,v112\n"
MF "a[112:115],v[20:23],v[104:107],a[112:115]\n"
"v_exp_f32 v113,v113\n"
MF "a[92:95],v[16:19],v[104:107],a[92:95]\n"
"v_exp_f32 v114,v114\n"
MF "a[96:99],v[16:19],v[108:111],a[96:99]\n"
"v_exp_f32 v115,v115\n"
MF "a[116:119],v[20:23],v[108:111],a[116:119]\n"
"s_waitcnt lgkmcnt(0)\n"
TR "v[16:17],v2 offset:26624\n"
TR "v[18:19],v2 offset:32768\n"
MF "a[136:139],v[24:27],v[108:111],a[136:139]\n"
"s_nop 0\n"
TR "v[20:21],v3 offset:26624\n"
TR "v[22:23],v3 offset:32768\n"
MF "a[156:159],v[28:31],v[108:111],a[156:159]\n"
"v_add_f32 v52,v52,v112\n"
"v_add_f32 v52,v52,v113\n"
MF "a[152:155],v[28:31],v[104:107],a[152:155]\n"
CV "v74,v112,v113\n"
"v_add_f32 v52,v52,v114\n"
MF "a[132:135],v[24:27],v[104:107],a[132:135]\n"
"v_add_f32 v52,v52,v115\n"
CV "v75,v114,v115\n"
MF "a[128:131],v[24:27],v[100:103],a[128:131]\n"
"v_exp_f32 v116,v116\n"
MF "a[148:151],v[28:31],v[100:103],a[148:151]\n"
"v_exp_f32 v117,v117\n"
MF "a[144:147],v[28:31],v[96:99],a[144:147]\n"
"v_exp_f32 v118,v118\n"
MF "a[124:127],v[24:27],v[96:99],a[124:127]\n"
"v_exp_f32 v119,v119\n"
MF "a[120:123],v[24:27],v[92:95],a[120:123]\n"
"v_add_f32 v53,v53,v116\n"
"v_add_f32 v53,v53,v117\n"
MF "a[140:143],v[28:31],v[92:95],a[140:143]\n"
"s_waitcnt lgkmcnt(0)\n"
TR "v[24:25],v4 offset:26624\n"
TR "v[26:27],v4 offset:32768\n"
MF "a[160:163],v[16:19],v[92:95],a[160:163]\n"
"s_nop 0\n"
TR "v[28:29],v5 offset:26624\n"
TR "v[30:31],v5 offset:32768\n"
MF "a[180:183],v[20:23],v[92:95],a[180:183]\n"
CV "v78,v116,v117\n"
"v_add_f32 v53,v53,v118\n"
MF "a[184:187],v[20:23],v[96:99],a[184:187]\n"
"v_add_f32 v53,v53,v119\n"
CV "v79,v118,v119\n"
MF "a[164:167],v[16:19],v[96:99],a[164:167]\n"
"v_exp_f32 v120,v120\n"
MF "a[168:171],v[16:19],v[100:103],a[168:171]\n"
"v_exp_f32 v121,v121\n"
MF "a[188:191],v[20:23],v[100:103],a[188:191]\n"
"v_exp_f32 v122,v122\n"
MF "a[192:195],v[20:23],v[104:107],a[192:195]\n"
"v_exp_f32 v123,v123\n"
MF "a[172:175],v[16:19],v[104:107],a[172:175]\n"
"s_waitcnt vmcnt(6) lgkmcnt(0)\n"
"v_add_f32 v54,v54,v120\n"
"v_add_f32 v54,v54,v121\n"
MF "a[176:179],v[16:19],v[108:111],a[176:179]\n"
"s_barrier\n"
MF "a[196:199],v[20:23],v[108:111],a[196:199]\n"
"s_add_u32 s60,s0,0\n"
"ds_read_b128 v[16:19],v0 offset:49152\n"
"ds_read_b128 v[20:23],v1 offset:49152\n"
MF "a[216:219],v[24:27],v[108:111],a[216:219]\n"
"s_add_u32 m0,s5,24576\n"
CV "v82,v120,v121\n"
"v_add_f32 v54,v54,v122\n"
MF "a[236:239],v[28:31],v[108:111],a[236:239]\n"
LD "v7,s[48:51],s60 offen lds\n"
MF "a[232:235],v[28:31],v[104:107],a[232:235]\n"
"s_add_u32 s60,s0,10240\n"
"s_add_u32 m0,s5,25600\n"
"v_add_f32 v54,v54,v123\n"
MF "a[212:215],v[24:27],v[104:107],a[212:215]\n"
LD "v7,s[48:51],s60 offen lds\n"
MF "a[208:211],v[24:27],v[100:103],a[208:211]\n"
"s_add_u32 s60,s0,20480\n"
"s_add_u32 m0,s5,26624\n"
CV "v83,v122,v123\n"
MF "a[228:231],v[28:31],v[100:103],a[228:231]\n"
LD "v7,s[48:51],s60 offen lds\n"
MF "a[224:227],v[28:31],v[96:99],a[224:227]\n"
MF "a[204:207],v[24:27],v[96:99],a[204:207]\n"
MF "a[200:203],v[24:27],v[92:95],a[200:203]\n"
MF "a[220:223],v[28:31],v[92:95],a[220:223]\n"
"s_waitcnt lgkmcnt(0)\n"
MF "v[92:95],v[16:19],v[136:139],v[32:35]\n"
"s_add_u32 s60,s0,30720\n"
"ds_read_b128 v[24:27],v0 offset:50176\n"
"ds_read_b128 v[28:31],v1 offset:50176\n"
MF "v[92:95],v[20:23],v[156:159],v[92:95]\n"
"s_add_u32 m0,s5,27648\n"
"v_exp_f32 v124,v124\n"
MF "v[96:99],v[20:23],v[160:163],v[36:39]\n"
LD "v7,s[48:51],s60 offen lds\n"
MF "v[96:99],v[16:19],v[140:143],v[96:99]\n"
"s_add_u32 s60,s0,40960\n"
"v_exp_f32 v125,v125\n"
MF "v[100:103],v[16:19],v[144:147],v[40:43]\n"
"s_add_u32 m0,s5,28672\n"
"v_exp_f32 v126,v126\n"
MF "v[100:103],v[20:23],v[164:167],v[100:103]\n"
LD "v7,s[48:51],s60 offen lds\n"
MF "v[104:107],v[20:23],v[168:171],v[44:47]\n"
"s_add_u32 s60,s0,51200\n"
"v_exp_f32 v127,v127\n"
MF "v[104:107],v[16:19],v[148:151],v[104:107]\n"
"s_add_u32 m0,s5,29696\n"
"v_add_f32 v55,v55,v124\n"
"v_add_f32 v55,v55,v125\n"
MF "v[108:111],v[16:19],v[152:155],v[48:51]\n"
LD "v7,s[48:51],s60 offen lds\n"
MF "v[108:111],v[20:23],v[172:175],v[108:111]\n"
"s_waitcnt lgkmcnt(0)\n"
"ds_read_b128 v[16:19],v0 offset:51200\n"
"ds_read_b128 v[20:23],v1 offset:51200\n"
MF "v[108:111],v[24:27],v[192:195],v[108:111]\n"
CV "v86,v124,v125\n"
"v_add_f32 v55,v55,v126\n"
MF "v[108:111],v[28:31],v[212:215],v[108:111]\n"
"v_add_f32 v55,v55,v127\n"
CV "v87,v126,v127\n"
MF "v[104:107],v[28:31],v[208:211],v[104:107]\n"
"v_exp_f32 v128,v128\n"
MF "v[104:107],v[24:27],v[188:191],v[104:107]\n"
"v_exp_f32 v129,v129\n"
MF "v[100:103],v[24:27],v[184:187],v[100:103]\n"
"v_exp_f32 v130,v130\n"
MF "v[100:103],v[28:31],v[204:207],v[100:103]\n"
"v_exp_f32 v131,v131\n"
MF "v[96:99],v[28:31],v[200:203],v[96:99]\n"
"v_add_f32 v56,v56,v128\n"
"v_add_f32 v56,v56,v129\n"
MF "v[96:99],v[24:27],v[180:183],v[96:99]\n"
CV "v90,v128,v129\n"
"v_add_f32 v56,v56,v130\n"
MF "v[92:95],v[24:27],v[176:179],v[92:95]\n"
"v_add_f32 v56,v56,v131\n"
CV "v91,v130,v131\n"
MF "v[92:95],v[28:31],v[196:199],v[92:95]\n"
"s_waitcnt lgkmcnt(0)\n"
"ds_read_b128 v[24:27],v0 offset:55296\n"
"ds_read_b128 v[28:31],v1 offset:55296\n"
MF "v[92:95],v[16:19],v[216:219],v[92:95]\n"
MF "v[92:95],v[20:23],v[236:239],v[92:95]\n"
MF "v[96:99],v[20:23],v[240:243],v[96:99]\n"
MF "v[96:99],v[16:19],v[220:223],v[96:99]\n"
MF "v[100:103],v[16:19],v[224:227],v[100:103]\n"
MF "v[100:103],v[20:23],v[244:247],v[100:103]\n"
MF "v[104:107],v[20:23],v[248:251],v[104:107]\n"
MF "v[104:107],v[16:19],v[228:231],v[104:107]\n"
"v_max3_f32 v10,v52,v53,v54\n"
"v_max3_f32 v10,v10,v55,v56\n"
MF "v[108:111],v[16:19],v[232:235],v[108:111]\n"
"v_cmp_le_f32 vcc,0x47800000,v10\n"
MF "v[108:111],v[20:23],v[252:255],v[108:111]\n"
"s_waitcnt lgkmcnt(0)\n"
"s_cbranch_vccnz .Lstub_27\n"
".Lback_27:\n"
MF "v[112:115],v[24:27],v[136:139],v[32:35]\n"
"s_add_u32 s74,s74,12288\n"
"ds_read_b128 v[16:19],v0 offset:56320\n"
"ds_read_b128 v[20:23],v1 offset:56320\n"
MF "v[112:115],v[28:31],v[156:159],v[112:115]\n"
"s_cmp_eq_u32 s74,s20\n"
"s_cselect_b32 s74,s70,s74\n"
MF "v[116:119],v[28:31],v[160:163],v[36:39]\n"
"v_exp_f32 v92,v92\n"
MF "v[116:119],v[24:27],v[140:143],v[116:119]\n"
"v_exp_f32 v93,v93\n"
MF "v[120:123],v[24:27],v[144:147],v[40:43]\n"
"v_exp_f32 v94,v94\n"
MF "v[120:123],v[28:31],v[164:167],v[120:123]\n"
"v_exp_f32 v95,v95\n"
MF "v[124:127],v[28:31],v[168:171],v[44:47]\n"
"v_add_f32 v57,v52,v92\n"
"v_add_f32 v57,v57,v93\n"
MF "v[124:127],v[24:27],v[148:151],v[124:127]\n"
CV "v92,v92,v93\n"
"v_add_f32 v57,v57,v94\n"
MF "v[128:131],v[24:27],v[152:155],v[48:51]\n"
"v_add_f32 v57,v57,v95\n"
CV "v93,v94,v95\n"
MF "v[128:131],v[28:31],v[172:175],v[128:131]\n"
"s_waitcnt lgkmcnt(0)\n"
"ds_read_b128 v[24:27],v0 offset:57344\n"
"ds_read_b128 v[28:31],v1 offset:57344\n"
MF "v[128:131],v[16:19],v[192:195],v[128:131]\n"
"v_exp_f32 v96,v96\n"
MF "v[128:131],v[20:23],v[212:215],v[128:131]\n"
"v_exp_f32 v97,v97\n"
MF "v[124:127],v[20:23],v[208:211],v[124:127]\n"
"v_exp_f32 v98,v98\n"
MF "v[124:127],v[16:19],v[188:191],v[124:127]\n"
"v_exp_f32 v99,v99\n"
MF "v[120:123],v[16:19],v[184:187],v[120:123]\n"
"v_add_f32 v58,v53,v96\n"
"v_add_f32 v58,v58,v97\n"
MF "v[120:123],v[20:23],v[204:207],v[120:123]\n"
CV "v96,v96,v97\n"
"v_add_f32 v58,v58,v98\n"
MF "v[116:119],v[20:23],v[200:203],v[116:119]\n"
"s_waitcnt vmcnt(6) lgkmcnt(0)\n"
"v_add_f32 v58,v58,v99\n"
CV "v97,v98,v99\n"
MF "v[116:119],v[16:19],v[180:183],v[116:119]\n"
"s_barrier\n"
MF "v[112:115],v[16:19],v[176:179],v[112:115]\n"
"v_exp_f32 v100,v100\n"
MF "v[112:115],v[20:23],v[196:199],v[112:115]\n"
"s_nop 0\n"
TR "v[16:17],v2\n"
TR "v[18:19],v2 offset:6144\n"
MF "v[112:115],v[24:27],v[216:219],v[112:115]\n"
"s_add_u32 s73,s73,12288\n"
TR "v[20:21],v3\n"
TR "v[22:23],v3 offset:6144\n"
MF "v[112:115],v[28:31],v[236:239],v[112:115]\n"
"s_cmp_eq_u32 s73,s19\n"
"s_cselect_b32 s73,s18,s73\n"
"s_add_u32 m0,s65,49152\n"
MF "v[116:119],v[28:31],v[240:243],v[116:119]\n"
LD "v8,s[36:39],s73 offen nt lds\n"
MF "v[116:119],v[24:27],v[220:223],v[116:119]\n"
"s_add_u32 m0,s65,50048\n"
"v_exp_f32 v101,v101\n"
MF "v[120:123],v[24:27],v[224:227],v[120:123]\n"
LD "v8,s[36:39],s73 offen offset:128 nt lds\n"
MF "v[120:123],v[28:31],v[244:247],v[120:123]\n"
"v_exp_f32 v102,v102\n"
MF "v[124:127],v[28:31],v[248:251],v[124:127]\n"
"s_add_u32 m0,s65,50944\n"
"v_exp_f32 v103,v103\n"
MF "v[124:127],v[24:27],v[228:231],v[124:127]\n"
LD "v8,s[36:39],s73 offen offset:256 nt lds\n"
MF "v[128:131],v[24:27],v[232:235],v[128:131]\n"
"v_add_f32 v59,v54,v100\n"
"v_add_f32 v59,v59,v101\n"
MF "v[128:131],v[28:31],v[252:255],v[128:131]\n"
"s_waitcnt lgkmcnt(0)\n"
TR "v[24:25],v4\n"
TR "v[26:27],v4 offset:6144\n"
MF "a[0:3],v[16:19],v[72:75],a[0:3]\n"
"s_add_u32 m0,s65,49152\n"
TR "v[28:29],v5\n"
TR "v[30:31],v5 offset:6144\n"
MF "a[20:23],v[20:23],v[72:75],a[20:23]\n"
LD "v8,s[36:39],s73 offen offset:3072 nt lds\n"
MF "a[24:27],v[20:23],v[76:79],a[24:27]\n"
CV "v100,v100,v101\n"
"v_add_f32 v59,v59,v102\n"
MF "a[4:7],v[16:19],v[76:79],a[4:7]\n"
"s_add_u32 m0,s65,50048\n"
"v_add_f32 v59,v59,v103\n"
CV "v101,v102,v103\n"
MF "a[8:11],v[16:19],v[80:83],a[8:11]\n"
LD "v8,s[36:39],s73 offen offset:3200 nt lds\n"
MF "a[28:31],v[20:23],v[80:83],a[28:31]\n"
"v_exp_f32 v104,v104\n"
MF "a[32:35],v[20:23],v[84:87],a[32:35]\n"
"s_add_u32 m0,s65,50944\n"
"v_exp_f32 v105,v105\n"
MF "a[12:15],v[16:19],v[84:87],a[12:15]\n"
LD "v8,s[36:39],s73 offen offset:3328 nt lds\n"
MF "a[16:19],v[16:19],v[88:91],a[16:19]\n"
"v_exp_f32 v106,v106\n"
MF "a[36:39],v[20:23],v[88:91],a[36:39]\n"
"s_waitcnt lgkmcnt(0)\n"
TR "v[16:17],v2 offset:1024\n"
TR "v[18:19],v2 offset:7168\n"
MF "a[56:59],v[24:27],v[88:91],a[56:59]\n"
"s_nop 0\n"
TR "v[20:21],v3 offset:1024\n"
TR "v[22:23],v3 offset:7168\n"
MF "a[76:79],v[28:31],v[88:91],a[76:79]\n"
"v_exp_f32 v107,v107\n"
MF "a[72:75],v[28:31],v[84:87],a[72:75]\n"
"v_add_f32 v60,v55,v104\n"
"v_add_f32 v60,v60,v105\n"
MF "a[52:55],v[24:27],v[84:87],a[52:55]\n"
CV "v104,v104,v105\n"
"v_add_f32 v60,v60,v106\n"
MF "a[48:51],v[24:27],v[80:83],a[48:51]\n"
"v_add_f32 v60,v60,v107\n"
CV "v105,v106,v107\n"
MF "a[68:71],v[28:31],v[80:83],a[68:71]\n"
"v_exp_f32 v108,v108\n"
MF "a[64:67],v[28:31],v[76:79],a[64:67]\n"
"v_exp_f32 v109,v109\n"
MF "a[44:47],v[24:27],v[76:79],a[44:47]\n"
"v_exp_f32 v110,v110\n"
MF "a[40:43],v[24:27],v[72:75],a[40:43]\n"
"v_exp_f32 v111,v111\n"
MF "a[60:63],v[28:31],v[72:75],a[60:63]\n"
"s_waitcnt lgkmcnt(0)\n"
TR "v[24:25],v4 offset:1024\n"
TR "v[26:27],v4 offset:7168\n"
MF "a[80:83],v[16:19],v[72:75],a[80:83]\n"
"s_nop 0\n"
TR "v[28:29],v5 offset:1024\n"
TR "v[30:31],v5 offset:7168\n"
MF "a[100:103],v[20:23],v[72:75],a[100:103]\n"
"v_add_f32 v61,v56,v108\n"
"v_add_f32 v61,v61,v109\n"
MF "a[104:107],v[20:23],v[76:79],a[104:107]\n"
CV "v108,v108,v109\n"
"v_add_f32 v61,v61,v110\n"
MF "a[84:87],v[16:19],v[76:79],a[84:87]\n"
"v_add_f32 v61,v61,v111\n"
CV "v109,v110,v111\n"
MF "a[88:91],v[16:19],v[80:83],a[88:91]\n"
"v_exp_f32 v112,v112\n"
MF "a[108:111],v[20:23],v[80:83],a[108:111]\n"
"v_exp_f32 v113,v113\n"
MF "a[112:115],v[20:23],v[84:87],a[112:115]\n"
"v_exp_f32 v114,v114\n"
MF "a[92:95],v[16:19],v[84:87],a[92:95]\n"
"v_exp_f32 v115,v115\n"
MF "a[96:99],v[16:19],v[88:91],a[96:99]\n"
"v_add_f32 v57,v57,v112\n"
"v_add_f32 v57,v57,v113\n"
MF "a[116:119],v[20:23],v[88:91],a[116:119]\n"
"s_waitcnt lgkmcnt(0)\n"
TR "v[16:17],v2 offset:2048\n"
TR "v[18:19],v2 offset:8192\n"
MF "a[136:139],v[24:27],v[88:91],a[136:139]\n"
"s_nop 0\n"
TR "v[20:21],v3 offset:2048\n"
TR "v[22:23],v3 offset:8192\n"
MF "a[156:159],v[28:31],v[88:91],a[156:159]\n"
CV "v94,v112,v113\n"
"v_add_f32 v57,v57,v114\n"
MF "a[152:155],v[28:31],v[84:87],a[152:155]\n"
"v_add_f32 v57,v57,v115\n"
CV "v95,v114,v115\n"
MF "a[132:135],v[24:27],v[84:87],a[132:135]\n"
"v_exp_f32 v116,v116\n"
MF "a[128:131],v[24:27],v[80:83],a[128:131]\n"
"v_exp_f32 v117,v117\n"
MF "a[148:151],v[28:31],v[80:83],a[148:151]\n"
"v_exp_f32 v118,v118\n"
MF "a[144:147],v[28:31],v[76:79],a[144:147]\n"
"v_exp_f32 v119,v119\n"
MF "a[124:127],v[24:27],v[76:79],a[124:127]\n"
"v_add_f32 v58,v58,v116\n"
"v_add_f32 v58,v58,v117\n"
MF "a[120:123],v[24:27],v[72:75],a[120:123]\n"
CV "v98,v116,v117\n"
"v_add_f32 v58,v58,v118\n"
MF "a[140:143],v[28:31],v[72:75],a[140:143]\n"
"s_waitcnt lgkmcnt(0)\n"
TR "v[24:25],v4 offset:2048\n"
TR "v[26:27],v4 offset:8192\n"
MF "a[160:163],v[16:19],v[72:75],a[160:163]\n"
"s_nop 0\n"
TR "v[28:29],v5 offset:2048\n"
TR "v[30:31],v5 offset:8192\n"
MF "a[180:183],v[20:23],v[72:75],a[180:183]\n"
"v_add_f32 v58,v58,v119\n"
CV "v99,v118,v119\n"
MF "a[184:187],v[20:23],v[76:79],a[184:187]\n"
"v_exp_f32 v120,v120\n"
MF "a[164:167],v[16:19],v[76:79],a[164:167]\n"
"v_exp_f32 v121,v121\n"
MF "a[168:171],v[16:19],v[80:83],a[168:171]\n"
"v_exp_f32 v122,v122\n"
MF "a[188:191],v[20:23],v[80:83],a[188:191]\n"
"v_exp_f32 v123,v123\n"
MF "a[192:195],v[20:23],v[84:87],a[192:195]\n"
"v_add_f32 v59,v59,v120\n"
"v_add_f32 v59,v59,v121\n"
MF "a[172:175],v[16:19],v[84:87],a[172:175]\n"
"s_waitcnt lgkmcnt(0)\n"
CV "v102,v120,v121\n"
"v_add_f32 v59,v59,v122\n"
MF "a[176:179],v[16:19],v[88:91],a[176:179]\n"
"s_barrier\n"
MF "a[196:199],v[20:23],v[88:91],a[196:199]\n"
"s_add_u32 s60,s1,0\n"
"s_add_u32 m0,s5,0\n"
"v_add_f32 v59,v59,v123\n"
MF "a[216:219],v[24:27],v[88:91],a[216:219]\n"
LD "v7,s[48:51],s60 offen lds\n"
MF "a[236:239],v[28:31],v[88:91],a[236:239]\n"
"s_add_u32 s60,s1,10240\n"
"s_add_u32 m0,s5,1024\n"
CV "v103,v122,v123\n"
MF "a[232:235],v[28:31],v[84:87],a[232:235]\n"
LD "v7,s[48:51],s60 offen lds\n"
MF "a[212:215],v[24:27],v[84:87],a[212:215]\n"
"s_add_u32 s60,s1,20480\n"
"s_add_u32 m0,s5,2048\n"
MF "a[208:211],v[24:27],v[80:83],a[208:211]\n"
LD "v7,s[48:51],s60 offen lds\n"
MF "a[228:231],v[28:31],v[80:83],a[228:231]\n"
"v_exp_f32 v124,v124\n"
MF "a[224:227],v[28:31],v[76:79],a[224:227]\n"
"v_exp_f32 v125,v125\n"
MF "a[204:207],v[24:27],v[76:79],a[204:207]\n"
"v_exp_f32 v126,v126\n"
MF "a[200:203],v[24:27],v[72:75],a[200:203]\n"
"v_exp_f32 v127,v127\n"
MF "a[220:223],v[28:31],v[72:75],a[220:223]\n"
"v_add_f32 v60,v60,v124\n"
"v_add_f32 v60,v60,v125\n"
CV "v106,v124,v125\n"
"v_add_f32 v60,v60,v126\n"
"v_add_f32 v60,v60,v127\n"
CV "v107,v126,v127\n"
"v_exp_f32 v128,v128\n"
"v_exp_f32 v129,v129\n"
"v_exp_f32 v130,v130\n"
"v_exp_f32 v131,v131\n"
"v_add_f32 v61,v61,v128\n"
"v_add_f32 v61,v61,v129\n"
CV "v110,v128,v129\n"
"v_add_f32 v61,v61,v130\n"
"v_add_f32 v61,v61,v131\n"
CV "v111,v130,v131\n"
"v_max3_f32 v10,v57,v58,v59\n"
"v_max3_f32 v10,v10,v60,v61\n"
"v_cmp_le_f32 vcc,0x47800000,v10\n"
"s_cbranch_vccnz .Lstub_28\n"
".Lback_28:\n"
"s_waitcnt vmcnt(3) lgkmcnt(0)\n"
"s_barrier\n"
"v_add_u32 v236,s9,v9\n"
"v_add_u32 v237,65536,v236\n"
"v_mov_b32 v10,1.0\n"
TR "v[160:161],v4 offset:50176\n"
TR "v[162:163],v4 offset:56320\n"
"v_mfma_f32_16x16x4_f32 v[16:19],v10,v57,0\n"
TR "v[164:165],v5 offset:50176\n"
TR "v[166:167],v5 offset:56320\n"
TR "v[168:169],v2 offset:51200\n"
TR "v[170:171],v2 offset:57344\n"
TR "v[172:173],v3 offset:51200\n"
TR "v[174:175],v3 offset:57344\n"
"v_mfma_f32_16x16x4_f32 v[20:23],v10,v58,0\n"
TR "v[176:177],v4 offset:51200\n"
TR "v[178:179],v4 offset:57344\n"
TR "v[180:181],v5 offset:51200\n"
TR "v[182:183],v5 offset:57344\n"
TR "v[136:137],v2 offset:49152\n"
TR "v[138:139],v2 offset:55296\n"
"v_mfma_f32_16x16x4_f32 v[24:27],v10,v59,0\n"
TR "v[140:141],v3 offset:49152\n"
TR "v[142:143],v3 offset:55296\n"
TR "v[144:145],v4 offset:49152\n"
TR "v[146:147],v4 offset:55296\n"
TR "v[148:149],v5 offset:49152\n"
TR "v[150:151],v5 offset:55296\n"
"v_mfma_f32_16x16x4_f32 v[28:31],v10,v60,0\n"
TR "v[152:153],v2 offset:50176\n"
TR "v[154:155],v2 offset:56320\n"
TR "v[156:157],v3 offset:50176\n"
TR "v[158:159],v3 offset:56320\n"
"v_mfma_f32_16x16x4_f32 v[32:35],v10,v61,0\n"
"s_add_u32 m0,s5,0\n"
"v_add_u32 v195,27648,v7\n"
LD "v195,s[48:51],s1 offen offset:3072 lds\n"
"s_add_u32 m0,s5,4096\n"
"v_add_u32 v196,40960,v7\n"
LD "v196,s[48:51],s1 offen lds\n"
"s_waitcnt lgkmcnt(15)\n"
MF "a[120:123],v[160:163],v[92:95],a[120:123]\n"
MF "a[140:143],v[164:167],v[92:95],a[140:143]\n"
".Lpg23h:\n"
"s_cbranch_execz .Lpg27h\n"
"v_add_u32 v197,50176,v7\n"
LD "v197,s[48:51],s1 offen offset:1024 lds\n"
"v_rcp_f32 v208,v16\n"
"v_rcp_f32 v209,v20\n"
"v_rcp_f32 v210,v24\n"
"v_rcp_f32 v211,v28\n"
"v_rcp_f32_e64 v212,v32\n"
MF "a[124:127],v[160:163],v[96:99],a[124:127]\n"
"v_mul_f32_e64 v214,-s13,v208\n"
"v_mul_f32_e64 v215,-s13,v209\n"
"v_mul_f32_e64 v216,-s13,v210\n"
"v_mul_f32_e64 v217,-s13,v211\n"
"v_mul_f32_e64 v218,-s13,v212\n"
AR "v72,a120\n"
AR "v73,a121\n"
AR "v74,a122\n"
MF "a[144:147],v[164:167],v[96:99],a[144:147]\n"
AR "v75,a123\n"
AR "v76,a140\n"
AR "v77,a141\n"
AR "v78,a142\n"
AR "v79,a143\n"
"v_pk_fma_f32 v[72:73],v[72:73],v[208:209],v[214:215] op_sel:[0,0,0] op_sel_hi:[1,0,0]\n"
"v_pk_fma_f32 v[74:75],v[74:75],v[208:209],v[214:215] op_sel:[0,0,0] op_sel_hi:[1,0,0]\n"
"v_pk_fma_f32 v[76:77],v[76:77],v[208:209],v[214:215] op_sel:[0,0,0] op_sel_hi:[1,0,0]\n"
"v_pk_fma_f32 v[78:79],v[78:79],v[208:209],v[214:215] op_sel:[0,0,0] op_sel_hi:[1,0,0]\n"
CV "v112,v72,v73\n"
CV "v113,v74,v75\n"
CV "v114,v76,v77\n"
CV "v115,v78,v79\n"
"s_waitcnt lgkmcnt(0)\n"
MF "a[128:131],v[160:163],v[100:103],a[128:131]\n"
"ds_read_b128 a[140:143],v237 offset:20480\n"
AW "a120,v112\n"
AW "a121,v113\n"
AW "a122,v114\n"
AW "a123,v115\n"
MF "a[44:47],v[144:147],v[96:99],a[44:47]\n"
AR "v80,a124\n"
AR "v81,a125\n"
AR "v82,a126\n"
MF "a[148:151],v[164:167],v[100:103],a[148:151]\n"
AR "v83,a127\n"
AR "v84,a144\n"
AR "v85,a145\n"
"s_nop 0\n"
AR "v86,a146\n"
AR "v87,a147\n"
"v_pk_fma_f32 v[80:81],v[80:81],v[208:209],v[214:215] op_sel:[0,1,1] op_sel_hi:[1,1,1]\n"
"v_pk_fma_f32 v[82:83],v[82:83],v[208:209],v[214:215] op_sel:[0,1,1] op_sel_hi:[1,1,1]\n"
"v_pk_fma_f32 v[84:85],v[84:85],v[208:209],v[214:215] op_sel:[0,1,1] op_sel_hi:[1,1,1]\n"
"v_pk_fma_f32 v[86:87],v[86:87],v[208:209],v[214:215] op_sel:[0,1,1] op_sel_hi:[1,1,1]\n"
CV "v116,v80,v81\n"
CV "v117,v82,v83\n"
CV "v118,v84,v85\n"
CV "v119,v86,v87\n"
MF "a[132:135],v[160:163],v[104:107],a[132:135]\n"
"ds_read_b128 a[144:147],v236\n"
AW "a124,v116\n"
AW "a125,v117\n"
AW "a126,v118\n"
AW "a127,v119\n"
MF "a[64:67],v[148:151],v[96:99],a[64:67]\n"
AR "v72,a128\n"
AR "v73,a129\n"
AR "v74,a130\n"
MF "a[152:155],v[164:167],v[104:107],a[152:155]\n"
AR "v75,a131\n"
AR "v76,a148\n"
AR "v77,a149\n"
AR "v78,a150\n"
AR "v79,a151\n"
"v_pk_fma_f32 v[72:73],v[72:73],v[210:211],v[216:217] op_sel:[0,0,0] op_sel_hi:[1,0,0]\n"
"v_pk_fma_f32 v[74:75],v[74:75],v[210:211],v[216:217] op_sel:[0,0,0] op_sel_hi:[1,0,0]\n"
"v_pk_fma_f32 v[76:77],v[76:77],v[210:211],v[216:217] op_sel:[0,0,0] op_sel_hi:[1,0,0]\n"
"v_pk_fma_f32 v[78:79],v[78:79],v[210:211],v[216:217] op_sel:[0,0,0] op_sel_hi:[1,0,0]\n"
CV "v112,v72,v73\n"
CV "v113,v74,v75\n"
CV "v114,v76,v77\n"
CV "v115,v78,v79\n"
MF "a[136:139],v[160:163],v[108:111],a[136:139]\n"
"ds_read_b128 a[148:151],v236 offset:4096\n"
AW "a128,v112\n"
AW "a129,v113\n"
AW "a130,v114\n"
AW "a131,v115\n"
MF "a[84:87],v[152:155],v[96:99],a[84:87]\n"
AR "v80,a132\n"
AR "v81,a133\n"
AR "v82,a134\n"
MF "a[156:159],v[164:167],v[108:111],a[156:159]\n"
AR "v83,a135\n"
AR "v84,a152\n"
AR "v85,a153\n"
AR "v86,a154\n"
AR "v87,a155\n"
"v_pk_fma_f32 v[80:81],v[80:81],v[210:211],v[216:217] op_sel:[0,1,1] op_sel_hi:[1,1,1]\n"
"v_pk_fma_f32 v[82:83],v[82:83],v[210:211],v[216:217] op_sel:[0,1,1] op_sel_hi:[1,1,1]\n"
"v_pk_fma_f32 v[84:85],v[84:85],v[210:211],v[216:217] op_sel:[0,1,1] op_sel_hi:[1,1,1]\n"
"v_pk_fma_f32 v[86:87],v[86:87],v[210:211],v[216:217] op_sel:[0,1,1] op_sel_hi:[1,1,1]\n"
CV "v116,v80,v81\n"
CV "v117,v82,v83\n"
CV "v118,v84,v85\n"
CV "v119,v86,v87\n"
MF "a[160:163],v[168:171],v[92:95],a[160:163]\n"
"ds_read_b128 a[152:155],v236 offset:8192\n"
AW "a132,v116\n"
AW "a133,v117\n"
AW "a134,v118\n"
AW "a135,v119\n"
MF "a[104:107],v[156:159],v[96:99],a[104:107]\n"
AR "v72,a136\n"
AR "v73,a137\n"
AR "v74,a138\n"
MF "a[180:183],v[172:175],v[92:95],a[180:183]\n"
AR "v75,a139\n"
AR "v76,a156\n"
AR "v77,a157\n"
AR "v78,a158\n"
AR "v79,a159\n"
"v_pk_fma_f32 v[72:73],v[72:73],v[212:213],v[218:219] op_sel:[0,0,0] op_sel_hi:[1,0,0]\n"
"v_pk_fma_f32 v[74:75],v[74:75],v[212:213],v[218:219] op_sel:[0,0,0] op_sel_hi:[1,0,0]\n"
"v_pk_fma_f32 v[76:77],v[76:77],v[212:213],v[218:219] op_sel:[0,0,0] op_sel_hi:[1,0,0]\n"
"v_pk_fma_f32 v[78:79],v[78:79],v[212:213],v[218:219] op_sel:[0,0,0] op_sel_hi:[1,0,0]\n"
CV "v112,v72,v73\n"
CV "v113,v74,v75\n"
CV "v114,v76,v77\n"
CV "v115,v78,v79\n"
MF "a[164:167],v[168:171],v[96:99],a[164:167]\n"
"ds_read_b128 a[156:159],v236 offset:12288\n"
AW "a136,v112\n"
AW "a137,v113\n"
AW "a138,v114\n"
AW "a139,v115\n"
"v_max3_f32 v10,|v72|,|v73|,|v74|\n"
"v_max3_f32 v10,v10,|v75|,|v76|\n"
"v_max3_f32 v10,v10,|v77|,|v78|\n"
"v_max3_f32 v10,v10,|v78|,|v79|\n"
"v_mul_f32 v10,s10,v10\n"
"v_lshrrev_b32 v11,2,v9\n"
"v_readlane_b32 s12,v10,3\n"
"v_readlane_b32 s6,v10,22\n"
"v_readlane_b32 s7,v10,41\n"
"v_readlane_b32 s8,v10,60\n"
"v_add_u32 v11,s14,v11\n"
"s_min_u32 s12,s12,s6\n"
"s_min_u32 s7,s7,s8\n"
"s_min_u32 s12,s12,s7\n"
"s_and_b32 s12,s12,0x7f800000\n"
"s_sub_u32 s12,s12,0x14000000\n"
"s_cmp_lt_u32 s12,0x50000000\n"
"s_cselect_b32 s12,s12,0xe7400000\n"
"s_add_u32 s12,s12,0x18c00000\n"
"v_mov_b32_e64 v10,s12\n"
"v_or_b32 v12,0x80000000,v10\n"
"ds_write2st64_b32 v11,v10,v12 offset1:1\n"
MF "a[8:11],v[136:139],v[100:103],a[8:11]\n"
AR "v80,a160\n"
AR "v81,a161\n"
AR "v82,a162\n"
MF "a[184:187],v[172:175],v[96:99],a[184:187]\n"
AR "v83,a163\n"
AR "v84,a180\n"
AR "v85,a181\n"
AR "v86,a182\n"
AR "v87,a183\n"
"v_pk_fma_f32 v[80:81],v[80:81],v[208:209],v[214:215] op_sel:[0,0,0] op_sel_hi:[1,0,0]\n"
"v_pk_fma_f32 v[82:83],v[82:83],v[208:209],v[214:215] op_sel:[0,0,0] op_sel_hi:[1,0,0]\n"
"v_pk_fma_f32 v[84:85],v[84:85],v[208:209],v[214:215] op_sel:[0,0,0] op_sel_hi:[1,0,0]\n"
"v_pk_fma_f32 v[86:87],v[86:87],v[208:209],v[214:215] op_sel:[0,0,0] op_sel_hi:[1,0,0]\n"
CV "v116,v80,v81\n"
CV "v117,v82,v83\n"
CV "v118,v84,v85\n"
CV "v119,v86,v87\n"
MF "a[168:171],v[168:171],v[100:103],a[168:171]\n"
"ds_read_b128 a[180:183],v236 offset:16384\n"
AW "a160,v116\n"
AW "a161,v117\n"
AW "a162,v118\n"
AW "a163,v119\n"
MF "a[28:31],v[140:143],v[100:103],a[28:31]\n"
AR "v72,a164\n"
AR "v73,a165\n"
AR "v74,a166\n"
MF "a[188:191],v[172:175],v[100:103],a[188:191]\n"
AR "v75,a167\n"
AR "v76,a184\n"
AR "v77,a185\n"
AR "v78,a186\n"
AR "v79,a187\n"
"v_pk_fma_f32 v[72:73],v[72:73],v[208:209],v[214:215] op_sel:[0,1,1] op_sel_hi:[1,1,1]\n"
"v_pk_fma_f32 v[74:75],v[74:75],v[208:209],v[214:215] op_sel:[0,1,1] op_sel_hi:[1,1,1]\n"
"v_pk_fma_f32 v[76:77],v[76:77],v[208:209],v[214:215] op_sel:[0,1,1] op_sel_hi:[1,1,1]\n"
"v_pk_fma_f32 v[78:79],v[78:79],v[208:209],v[214:215] op_sel:[0,1,1] op_sel_hi:[1,1,1]\n"
CV "v112,v72,v73\n"
CV "v113,v74,v75\n"
CV "v114,v76,v77\n"
CV "v115,v78,v79\n"
MF "a[172:175],v[168:171],v[104:107],a[172:175]\n"
"ds_read_b128 a[184:187],v236 offset:20480\n"
AW "a164,v112\n"
AW "a165,v113\n"
AW "a166,v114\n"
AW "a167,v115\n"
MF "a[48:51],v[144:147],v[100:103],a[48:51]\n"
"s_waitcnt lgkmcnt(0)\n"
"s_barrier\n"
"s_add_u32 m0,s5,49152\n"
"v_add_u32_e64 v192,0,v7\n"
LD "v192,s[48:51],s2 offen lds\n"
AR "v80,a168\n"
AR "v81,a169\n"
AR "v82,a170\n"
MF "a[192:195],v[172:175],v[104:107],a[192:195]\n"
AR "v83,a171\n"
AR "v84,a188\n"
AR "v85,a189\n"
AR "v86,a190\n"
AR "v87,a191\n"
"v_pk_fma_f32 v[80:81],v[80:81],v[210:211],v[216:217] op_sel:[0,0,0] op_sel_hi:[1,0,0]\n"
"v_pk_fma_f32 v[82:83],v[82:83],v[210:211],v[216:217] op_sel:[0,0,0] op_sel_hi:[1,0,0]\n"
"v_pk_fma_f32 v[84:85],v[84:85],v[210:211],v[216:217] op_sel:[0,0,0] op_sel_hi:[1,0,0]\n"
"v_pk_fma_f32 v[86:87],v[86:87],v[210:211],v[216:217] op_sel:[0,0,0] op_sel_hi:[1,0,0]\n"
"v_add_u32 v193,9216,v7\n"
LD "v193,s[48:51],s2 offen offset:1024 lds\n"
CV "v116,v80,v81\n"
CV "v117,v82,v83\n"
CV "v118,v84,v85\n"
CV "v119,v86,v87\n"
MF "a[176:179],v[168:171],v[108:111],a[176:179]\n"
"v_accvgpr_mov_b32 a188,a240\n"
"v_accvgpr_mov_b32 a189,a241\n"
"v_accvgpr_mov_b32 a190,a242\n"
"v_accvgpr_mov_b32 a191,a243\n"
AW "a168,v116\n"
AW "a169,v117\n"
AW "a170,v118\n"
AW "a171,v119\n"
MF "a[68:71],v[148:151],v[100:103],a[68:71]\n"
"v_add_u32 v194,18432,v7\n"
LD "v194,s[48:51],s2 offen offset:2048 lds\n"
AR "v72,a172\n"
AR "v73,a173\n"
AR "v74,a174\n"
MF "a[196:199],v[172:175],v[108:111],a[196:199]\n"
AR "v75,a175\n"
AR "v76,a192\n"
AR "v77,a193\n"
AR "v78,a194\n"
AR "v79,a195\n"
"v_pk_fma_f32 v[72:73],v[72:73],v[210:211],v[216:217] op_sel:[0,1,1] op_sel_hi:[1,1,1]\n"
"v_pk_fma_f32 v[74:75],v[74:75],v[210:211],v[216:217] op_sel:[0,1,1] op_sel_hi:[1,1,1]\n"
"v_pk_fma_f32 v[76:77],v[76:77],v[210:211],v[216:217] op_sel:[0,1,1] op_sel_hi:[1,1,1]\n"
"v_pk_fma_f32 v[78:79],v[78:79],v[210:211],v[216:217] op_sel:[0,1,1] op_sel_hi:[1,1,1]\n"
LD "v195,s[48:51],s2 offen offset:3072 lds\n"
CV "v112,v72,v73\n"
CV "v113,v74,v75\n"
CV "v114,v76,v77\n"
CV "v115,v78,v79\n"
MF "a[200:203],v[176:179],v[92:95],a[200:203]\n"
"s_add_u32 m0,s5,53248\n"
"v_accvgpr_mov_b32 a192,a244\n"
"v_accvgpr_mov_b32 a193,a245\n"
"v_accvgpr_mov_b32 a194,a246\n"
"v_accvgpr_mov_b32 a195,a247\n"
AW "a172,v112\n"
AW "a173,v113\n"
AW "a174,v114\n"
AW "a175,v115\n"
MF "a[88:91],v[152:155],v[100:103],a[88:91]\n"
LD "v196,s[48:51],s2 offen lds\n"
AR "v80,a176\n"
AR "v81,a177\n"
AR "v82,a178\n"
MF "a[220:223],v[180:183],v[92:95],a[220:223]\n"
AR "v83,a179\n"
AR "v84,a196\n"
AR "v85,a197\n"
AR "v86,a198\n"
AR "v87,a199\n"
"v_pk_fma_f32 v[80:81],v[80:81],v[212:213],v[218:219] op_sel:[0,0,0] op_sel_hi:[1,0,0]\n"
"v_pk_fma_f32 v[82:83],v[82:83],v[212:213],v[218:219] op_sel:[0,0,0] op_sel_hi:[1,0,0]\n"
"v_pk_fma_f32 v[84:85],v[84:85],v[212:213],v[218:219] op_sel:[0,0,0] op_sel_hi:[1,0,0]\n"
"v_pk_fma_f32 v[86:87],v[86:87],v[212:213],v[218:219] op_sel:[0,0,0] op_sel_hi:[1,0,0]\n"
CV "v116,v80,v81\n"
CV "v117,v82,v83\n"
CV "v118,v84,v85\n"
CV "v119,v86,v87\n"
MF "a[204:207],v[176:179],v[96:99],a[204:207]\n"
"v_accvgpr_mov_b32 a196,a248\n"
"v_accvgpr_mov_b32 a197,a249\n"
"v_accvgpr_mov_b32 a198,a250\n"
"v_accvgpr_mov_b32 a199,a251\n"
AW "a176,v116\n"
AW "a177,v117\n"
AW "a178,v118\n"
AW "a179,v119\n"
MF "a[108:111],v[156:159],v[100:103],a[108:111]\n"
LD "v197,s[48:51],s2 offen offset:1024 lds\n"
AR "v72,a200\n"
AR "v73,a201\n"
AR "v74,a202\n"
MF "a[224:227],v[180:183],v[96:99],a[224:227]\n"
AR "v75,a203\n"
AR "v76,a220\n"
AR "v77,a221\n"
AR "v78,a222\n"
AR "v79,a223\n"
"v_pk_fma_f32 v[72:73],v[72:73],v[208:209],v[214:215] op_sel:[0,0,0] op_sel_hi:[1,0,0]\n"
"v_pk_fma_f32 v[74:75],v[74:75],v[208:209],v[214:215] op_sel:[0,0,0] op_sel_hi:[1,0,0]\n"
"v_pk_fma_f32 v[76:77],v[76:77],v[208:209],v[214:215] op_sel:[0,0,0] op_sel_hi:[1,0,0]\n"
"v_pk_fma_f32 v[78:79],v[78:79],v[208:209],v[214:215] op_sel:[0,0,0] op_sel_hi:[1,0,0]\n"
CV "v112,v72,v73\n"
CV "v113,v74,v75\n"
CV "v114,v76,v77\n"
CV "v115,v78,v79\n"
MF "a[208:211],v[176:179],v[100:103],a[208:211]\n"
"s_add_u32 m0,s5,73728\n"
AW "a220,v62\n"
AW "a221,v63\n"
AW "a222,v64\n"
AW "a223,v65\n"
AW "a200,v112\n"
AW "a201,v113\n"
AW "a202,v114\n"
AW "a203,v115\n"
MF "a[12:15],v[136:139],v[104:107],a[12:15]\n"
LD "v192,s[48:51],s3 offen lds\n"
AR "v80,a204\n"
AR "v81,a205\n"
AR "v82,a206\n"
MF "a[228:231],v[180:183],v[100:103],a[228:231]\n"
AR "v83,a207\n"
AR "v84,a224\n"
AR "v85,a225\n"
AR "v86,a226\n"
AR "v87,a227\n"
"v_pk_fma_f32 v[80:81],v[80:81],v[208:209],v[214:215] op_sel:[0,1,1] op_sel_hi:[1,1,1]\n"
"v_pk_fma_f32 v[82:83],v[82:83],v[208:209],v[214:215] op_sel:[0,1,1] op_sel_hi:[1,1,1]\n"
"v_pk_fma_f32 v[84:85],v[84:85],v[208:209],v[214:215] op_sel:[0,1,1] op_sel_hi:[1,1,1]\n"
"v_pk_fma_f32 v[86:87],v[86:87],v[208:209],v[214:215] op_sel:[0,1,1] op_sel_hi:[1,1,1]\n"
CV "v116,v80,v81\n"
CV "v117,v82,v83\n"
CV "v118,v84,v85\n"
CV "v119,v86,v87\n"
MF "a[212:215],v[176:179],v[104:107],a[212:215]\n"
AW "a224,v66\n"
AW "a225,v67\n"
AW "a226,v68\n"
AW "a227,v69\n"
AW "a204,v116\n"
AW "a205,v117\n"
AW "a206,v118\n"
AW "a207,v119\n"
MF "a[32:35],v[140:143],v[104:107],a[32:35]\n"
LD "v193,s[48:51],s3 offen offset:1024 lds\n"
AR "v72,a208\n"
AR "v73,a209\n"
AR "v74,a210\n"
MF "a[232:235],v[180:183],v[104:107],a[232:235]\n"
AR "v75,a211\n"
AR "v76,a228\n"
AR "v77,a229\n"
AR "v78,a230\n"
AR "v79,a231\n"
"v_pk_fma_f32 v[72:73],v[72:73],v[210:211],v[216:217] op_sel:[0,0,0] op_sel_hi:[1,0,0]\n"
"v_pk_fma_f32 v[74:75],v[74:75],v[210:211],v[216:217] op_sel:[0,0,0] op_sel_hi:[1,0,0]\n"
"v_pk_fma_f32 v[76:77],v[76:77],v[210:211],v[216:217] op_sel:[0,0,0] op_sel_hi:[1,0,0]\n"
"v_pk_fma_f32 v[78:79],v[78:79],v[210:211],v[216:217] op_sel:[0,0,0] op_sel_hi:[1,0,0]\n"
CV "v112,v72,v73\n"
CV "v113,v74,v75\n"
CV "v114,v76,v77\n"
CV "v115,v78,v79\n"
MF "a[216:219],v[176:179],v[108:111],a[216:219]\n"
AW "a228,v70\n"
AW "a229,v71\n"
AW "a230,v132\n"
AW "a231,v133\n"
AW "a208,v112\n"
AW "a209,v113\n"
AW "a210,v114\n"
AW "a211,v115\n"
MF "a[52:55],v[144:147],v[104:107],a[52:55]\n"
LD "v194,s[48:51],s3 offen offset:2048 lds\n"
AR "v80,a212\n"
AR "v81,a213\n"
AR "v82,a214\n"
MF "a[236:239],v[180:183],v[108:111],a[236:239]\n"
AR "v83,a215\n"
AR "v84,a232\n"
AR "v85,a233\n"
AR "v86,a234\n"
AR "v87,a235\n"
"v_pk_fma_f32 v[80:81],v[80:81],v[210:211],v[216:217] op_sel:[0,1,1] op_sel_hi:[1,1,1]\n"
"v_pk_fma_f32 v[82:83],v[82:83],v[210:211],v[216:217] op_sel:[0,1,1] op_sel_hi:[1,1,1]\n"
"v_pk_fma_f32 v[84:85],v[84:85],v[210:211],v[216:217] op_sel:[0,1,1] op_sel_hi:[1,1,1]\n"
"v_pk_fma_f32 v[86:87],v[86:87],v[210:211],v[216:217] op_sel:[0,1,1] op_sel_hi:[1,1,1]\n"
CV "v116,v80,v81\n"
CV "v117,v82,v83\n"
CV "v118,v84,v85\n"
CV "v119,v86,v87\n"
MF "a[0:3],v[136:139],v[92:95],a[0:3]\n"
AW "a232,v134\n"
AW "a233,v135\n"
AW "a234,v6\n"
AW "a235,v13\n"
AW "a212,v116\n"
AW "a213,v117\n"
AW "a214,v118\n"
AW "a215,v119\n"
MF "a[72:75],v[148:151],v[104:107],a[72:75]\n"
LD "v195,s[48:51],s3 offen offset:3072 lds\n"
AR "v72,a216\n"
AR "v73,a217\n"
AR "v74,a218\n"
MF "a[20:23],v[140:143],v[92:95],a[20:23]\n"
AR "v75,a219\n"
AR "v76,a236\n"
AR "v77,a237\n"
AR "v78,a238\n"
AR "v79,a239\n"
"v_pk_fma_f32 v[72:73],v[72:73],v[212:213],v[218:219] op_sel:[0,0,0] op_sel_hi:[1,0,0]\n"
"v_pk_fma_f32 v[74:75],v[74:75],v[212:213],v[218:219] op_sel:[0,0,0] op_sel_hi:[1,0,0]\n"
"v_pk_fma_f32 v[76:77],v[76:77],v[212:213],v[218:219] op_sel:[0,0,0] op_sel_hi:[1,0,0]\n"
"v_pk_fma_f32 v[78:79],v[78:79],v[212:213],v[218:219] op_sel:[0,0,0] op_sel_hi:[1,0,0]\n"
CV "v112,v72,v73\n"
CV "v113,v74,v75\n"
CV "v114,v76,v77\n"
CV "v115,v78,v79\n"
MF "a[40:43],v[144:147],v[92:95],a[40:43]\n"
"s_add_u32 m0,s5,77824\n"
AW "a236,v14\n"
AW "a237,v15\n"
"v_accvgpr_mov_b32 a238,a254\n"
"v_accvgpr_mov_b32 a239,a255\n"
AW "a216,v112\n"
AW "a217,v113\n"
AW "a218,v114\n"
AW "a219,v115\n"
MF "a[92:95],v[152:155],v[104:107],a[92:95]\n"
LD "v196,s[48:51],s3 offen lds\n"
AR "v80,a0\n"
AR "v81,a1\n"
AR "v82,a2\n"
MF "a[60:63],v[148:151],v[92:95],a[60:63]\n"
AR "v83,a3\n"
AR "v84,a20\n"
AR "v85,a21\n"
AR "v86,a22\n"
AR "v87,a23\n"
"v_pk_fma_f32 v[80:81],v[80:81],v[208:209],v[214:215] op_sel:[0,0,0] op_sel_hi:[1,0,0]\n"
"v_pk_fma_f32 v[82:83],v[82:83],v[208:209],v[214:215] op_sel:[0,0,0] op_sel_hi:[1,0,0]\n"
"v_pk_fma_f32 v[84:85],v[84:85],v[208:209],v[214:215] op_sel:[0,0,0] op_sel_hi:[1,0,0]\n"
"v_pk_fma_f32 v[86:87],v[86:87],v[208:209],v[214:215] op_sel:[0,0,0] op_sel_hi:[1,0,0]\n"
CV "v116,v80,v81\n"
CV "v117,v82,v83\n"
CV "v118,v84,v85\n"
CV "v119,v86,v87\n"
MF "a[80:83],v[152:155],v[92:95],a[80:83]\n"
"ds_read_b128 a[20:23],v236 offset:24576\n"
AW "a0,v116\n"
AW "a1,v117\n"
AW "a2,v118\n"
AW "a3,v119\n"
MF "a[112:115],v[156:159],v[104:107],a[112:115]\n"
LD "v197,s[48:51],s3 offen offset:1024 lds\n"
AR "v72,a40\n"
AR "v73,a41\n"
AR "v74,a42\n"
MF "a[100:103],v[156:159],v[92:95],a[100:103]\n"
AR "v75,a43\n"
AR "v76,a60\n"
AR "v77,a61\n"
AR "v78,a62\n"
AR "v79,a63\n"
"v_pk_fma_f32 v[72:73],v[72:73],v[208:209],v[214:215] op_sel:[0,0,0] op_sel_hi:[1,0,0]\n"
"v_pk_fma_f32 v[74:75],v[74:75],v[208:209],v[214:215] op_sel:[0,0,0] op_sel_hi:[1,0,0]\n"
"v_pk_fma_f32 v[76:77],v[76:77],v[208:209],v[214:215] op_sel:[0,0,0] op_sel_hi:[1,0,0]\n"
"v_pk_fma_f32 v[78:79],v[78:79],v[208:209],v[214:215] op_sel:[0,0,0] op_sel_hi:[1,0,0]\n"
CV "v112,v72,v73\n"
CV "v113,v74,v75\n"
CV "v114,v76,v77\n"
CV "v115,v78,v79\n"
MF "a[4:7],v[136:139],v[96:99],a[4:7]\n"
"ds_read_b128 a[60:63],v236 offset:45056\n"
AW "a40,v112\n"
AW "a41,v113\n"
AW "a42,v114\n"
AW "a43,v115\n"
MF "a[16:19],v[136:139],v[108:111],a[16:19]\n"
AR "v80,a80\n"
AR "v81,a81\n"
AR "v82,a82\n"
MF "a[24:27],v[140:143],v[96:99],a[24:27]\n"
AR "v83,a83\n"
AR "v84,a100\n"
AR "v85,a101\n"
AR "v86,a102\n"
AR "v87,a103\n"
"v_pk_fma_f32 v[80:81],v[80:81],v[208:209],v[214:215] op_sel:[0,0,0] op_sel_hi:[1,0,0]\n"
"v_pk_fma_f32 v[82:83],v[82:83],v[208:209],v[214:215] op_sel:[0,0,0] op_sel_hi:[1,0,0]\n"
"v_pk_fma_f32 v[84:85],v[84:85],v[208:209],v[214:215] op_sel:[0,0,0] op_sel_hi:[1,0,0]\n"
"v_pk_fma_f32 v[86:87],v[86:87],v[208:209],v[214:215] op_sel:[0,0,0] op_sel_hi:[1,0,0]\n"
CV "v116,v80,v81\n"
CV "v117,v82,v83\n"
CV "v118,v84,v85\n"
CV "v119,v86,v87\n"
MF "a[36:39],v[140:143],v[108:111],a[36:39]\n"
"ds_read_b128 a[100:103],v237\n"
AW "a80,v116\n"
AW "a81,v117\n"
AW "a82,v118\n"
AW "a83,v119\n"
MF "a[56:59],v[144:147],v[108:111],a[56:59]\n"
AR "v72,a4\n"
AR "v73,a5\n"
AR "v74,a6\n"
MF "a[76:79],v[148:151],v[108:111],a[76:79]\n"
AR "v75,a7\n"
AR "v76,a24\n"
AR "v77,a25\n"
AR "v78,a26\n"
AR "v79,a27\n"
"v_pk_fma_f32 v[72:73],v[72:73],v[208:209],v[214:215] op_sel:[0,1,1] op_sel_hi:[1,1,1]\n"
"v_pk_fma_f32 v[74:75],v[74:75],v[208:209],v[214:215] op_sel:[0,1,1] op_sel_hi:[1,1,1]\n"
".Lpg24h:\n"
"s_cbranch_execz .Lpg28h\n"
"s_nop 0\n"
"v_pk_fma_f32 v[76:77],v[76:77],v[208:209],v[214:215] op_sel:[0,1,1] op_sel_hi:[1,1,1]\n"
"v_pk_fma_f32 v[78:79],v[78:79],v[208:209],v[214:215] op_sel:[0,1,1] op_sel_hi:[1,1,1]\n"
CV "v112,v72,v73\n"
CV "v113,v74,v75\n"
CV "v114,v76,v77\n"
CV "v115,v78,v79\n"
MF "a[96:99],v[152:155],v[108:111],a[96:99]\n"
"ds_read_b128 a[24:27],v236 offset:28672\n"
AW "a4,v112\n"
AW "a5,v113\n"
AW "a6,v114\n"
AW "a7,v115\n"
MF "a[116:119],v[156:159],v[108:111],a[116:119]\n"
"v_and_b32 v0,512,v9\n"
"v_lshlrev_b32 v0,2,v0\n"
"v_sub_u32 v0,v2,v0\n"
"v_xor_b32 v0,s4,v0\n"
"v_mov_b32 v252,s15\n"
"s_waitcnt vmcnt(12) lgkmcnt(0)\n"
"s_barrier\n"
"ds_read_b128 v[252:255],v252\n"
TR "v[176:177],v0 offset:24576\n"
TR "v[178:179],v0 offset:26624\n"
TR "v[180:181],v0 offset:28672\n"
TR "v[182:183],v0 offset:30720\n"
TR "v[184:185],v0 offset:32768\n"
TR "v[186:187],v0 offset:34816\n"
"v_xor_b32 v1,32,v0\n"
"v_xor_b32 v2,64,v0\n"
"v_xor_b32 v3,96,v0\n"
"v_add_u32 v4,73728,v0\n"
"v_add_u32 v5,73728,v1\n"
"v_add_u32 v6,73728,v2\n"
"v_add_u32 v7,73728,v3\n"
"s_waitcnt lgkmcnt(4)\n"
MF "v[16:19],a[120:123],v[176:179],v[252:255]\n"
"s_mov_b32 s65,128\n"
TR "v[188:189],v1 offset:24576\n"
AR "v220,a44\n"
"s_waitcnt lgkmcnt(3)\n"
MF "v[16:19],a[160:163],v[180:183],v[16:19]\n"
"s_mov_b32 s9,0x3f80\n"
TR "v[190:191],v1 offset:26624\n"
AR "v221,a45\n"
"s_waitcnt lgkmcnt(2)\n"
MF "v[16:19],a[200:203],v[184:187],v[16:19]\n"
"s_mov_b32 s89,s2\n"
AR "v222,a46\n"
AR "v223,a47\n"
MF "v[24:27],a[204:207],v[184:187],v[252:255]\n"
"s_mov_b32 s88,s3\n"
TR "a[240:241],v1 offset:28672\n"
AR "v224,a64\n"
MF "v[24:27],a[164:167],v[180:183],v[24:27]\n"
"s_bfe_u32 s58,s22,0x30014\n"
TR "a[242:243],v1 offset:30720\n"
AR "v225,a65\n"
MF "v[24:27],a[124:127],v[176:179],v[24:27]\n"
"s_add_u32 s61,s58,1\n"
AR "v226,a66\n"
AR "v227,a67\n"
MF "v[32:35],a[128:131],v[176:179],v[252:255]\n"
"s_and_b32 s61,s61,7\n"
TR "a[244:245],v1 offset:32768\n"
"ds_read_b128 a[64:67],v236 offset:49152\n"
MF "v[32:35],a[168:171],v[180:183],v[32:35]\n"
"s_mov_b32 s64,9\n"
TR "a[246:247],v1 offset:34816\n"
"v_fma_f32 v220,v220,v209,v215\n"
MF "v[32:35],a[208:211],v[184:187],v[32:35]\n"
"s_xor_b32 s67,s78,8\n"
"v_fma_f32 v221,v221,v209,v215\n"
"v_fma_f32 v222,v222,v209,v215\n"
MF "v[40:43],a[212:215],v[184:187],v[252:255]\n"
"s_lshr_b32 s60,s56,1\n"
"v_fma_f32 v223,v223,v209,v215\n"
"v_fma_f32 v224,v224,v209,v215\n"
MF "v[40:43],a[172:175],v[180:183],v[40:43]\n"
"s_xor_b32 s60,s60,1\n"
"v_fma_f32 v225,v225,v209,v215\n"
"v_fma_f32 v226,v226,v209,v215\n"
MF "v[40:43],a[132:135],v[176:179],v[40:43]\n"
"s_mul_i32 s59,s60,10240\n"
"v_fma_f32 v227,v227,v209,v215\n"
CV "v220,v220,v221\n"
MF "v[48:51],a[136:139],v[176:179],v[252:255]\n"
"s_add_u32 s90,s91,s59\n"
CV "v221,v222,v223\n"
CV "v222,v224,v225\n"
MF "v[48:51],a[176:179],v[180:183],v[48:51]\n"
"s_and_b32 s73,s56,1\n"
CV "v223,v226,v227\n"
AW "a44,v220\n"
MF "v[48:51],a[216:219],v[184:187],v[48:51]\n"
"s_waitcnt lgkmcnt(0)\n"
AW "a45,v221\n"
AW "a46,v222\n"
MF "v[52:55],a[216:219],a[244:247],v[252:255]\n"
"s_mul_i32 s72,s73,5120\n"
TR "v[176:177],v2 offset:24576\n"
AW "a47,v223\n"
MF "v[52:55],a[176:179],a[240:243],v[52:55]\n"
"s_add_u32 s59,s59,s72\n"
TR "v[178:179],v2 offset:26624\n"
AR "v228,a84\n"
MF "v[52:55],a[136:139],v[188:191],v[52:55]\n"
"s_add_u32 s59,s59,s93\n"
AR "v229,a85\n"
AR "v230,a86\n"
MF "v[44:47],a[132:135],v[188:191],v[252:255]\n"
"s_add_u32 s70,s58,4\n"
TR "v[180:181],v2 offset:28672\n"
AR "v231,a87\n"
MF "v[44:47],a[172:175],a[240:243],v[44:47]\n"
"s_and_b32 s70,s70,7\n"
TR "v[182:183],v2 offset:30720\n"
AR "v232,a104\n"
MF "v[44:47],a[212:215],a[244:247],v[44:47]\n"
"s_mul_i32 s70,s70,204800\n"
AR "v233,a105\n"
AR "v234,a106\n"
MF "v[36:39],a[208:211],a[244:247],v[252:255]\n"
"s_add_u32 s70,s70,s59\n"
TR "v[184:185],v2 offset:32768\n"
AR "v235,a107\n"
MF "v[36:39],a[168:171],a[240:243],v[36:39]\n"
"s_add_u32 s70,s70,40960\n"
TR "v[186:187],v2 offset:34816\n"
"ds_read_b128 a[104:107],v237 offset:4096\n"
MF "v[36:39],a[128:131],v[188:191],v[36:39]\n"
"s_add_u32 s6,s58,3\n"
"v_fma_f32 v228,v228,v209,v215\n"
"v_fma_f32 v229,v229,v209,v215\n"
MF "v[28:31],a[124:127],v[188:191],v[252:255]\n"
"s_and_b32 s6,s6,7\n"
"v_fma_f32 v230,v230,v209,v215\n"
"v_fma_f32 v231,v231,v209,v215\n"
MF "v[28:31],a[164:167],a[240:243],v[28:31]\n"
"s_mul_i32 s6,s6,204800\n"
"v_fma_f32 v232,v232,v209,v215\n"
"v_fma_f32 v233,v233,v209,v215\n"
MF "v[28:31],a[204:207],a[244:247],v[28:31]\n"
"s_add_u32 s6,s6,s59\n"
"v_fma_f32 v234,v234,v209,v215\n"
"v_fma_f32 v235,v235,v209,v215\n"
MF "v[20:23],a[200:203],a[244:247],v[252:255]\n"
"s_add_u32 s6,s6,122880\n"
CV "v228,v228,v229\n"
CV "v229,v230,v231\n"
MF "v[20:23],a[160:163],a[240:243],v[20:23]\n"
"s_add_u32 s16,s78,6\n"
CV "v230,v232,v233\n"
CV "v231,v234,v235\n"
MF "v[20:23],a[120:123],v[188:191],v[20:23]\n"
"s_waitcnt lgkmcnt(1)\n"
AW "a84,v228\n"
AW "a85,v229\n"
MF "v[56:59],a[120:123],v[176:179],v[252:255]\n"
"s_and_b32 s16,s16,15\n"
TR "v[188:189],v3 offset:24576\n"
AW "a86,v230\n"
MF "v[56:59],a[160:163],v[180:183],v[56:59]\n"
"s_add_u32 s7,s58,2\n"
TR "v[190:191],v3 offset:26624\n"
AW "a87,v231\n"
MF "v[56:59],a[200:203],v[184:187],v[56:59]\n"
"s_and_b32 s7,s7,7\n"
AR "v220,a48\n"
AR "v221,a49\n"
MF "v[64:67],a[204:207],v[184:187],v[252:255]\n"
"s_mul_i32 s7,s7,204800\n"
TR "a[240:241],v3 offset:28672\n"
AR "v222,a50\n"
MF "v[64:67],a[164:167],v[180:183],v[64:67]\n"
"s_add_u32 s7,s7,s59\n"
TR "a[242:243],v3 offset:30720\n"
AR "v223,a51\n"
MF "v[64:67],a[124:127],v[176:179],v[64:67]\n"
"s_add_u32 s7,s7,143360\n"
AR "v224,a68\n"
AR "v225,a69\n"
MF "v[72:75],a[128:131],v[176:179],v[252:255]\n"
"s_add_u32 s17,s78,4\n"
TR "a[244:245],v3 offset:32768\n"
AR "v226,a70\n"
MF "v[72:75],a[168:171],v[180:183],v[72:75]\n"
"s_and_b32 s17,s17,15\n"
TR "a[246:247],v3 offset:34816\n"
AR "v227,a71\n"
MF "v[72:75],a[208:211],v[184:187],v[72:75]\n"
"s_add_u32 s8,s58,1\n"
"ds_read_b128 a[68:71],v236 offset:53248\n"
"v_fma_f32 v220,v220,v210,v216\n"
MF "v[80:83],a[212:215],v[184:187],v[252:255]\n"
"s_and_b32 s8,s8,7\n"
"v_fma_f32 v221,v221,v210,v216\n"
"v_fma_f32 v222,v222,v210,v216\n"
MF "v[80:83],a[172:175],v[180:183],v[80:83]\n"
"s_mul_i32 s8,s8,204800\n"
"v_fma_f32 v223,v223,v210,v216\n"
"v_fma_f32 v224,v224,v210,v216\n"
MF "v[80:83],a[132:135],v[176:179],v[80:83]\n"
"s_add_u32 s8,s8,s59\n"
"v_fma_f32 v225,v225,v210,v216\n"
"v_fma_f32 v226,v226,v210,v216\n"
MF "v[88:91],a[136:139],v[176:179],v[252:255]\n"
"s_add_u32 s8,s8,163840\n"
"v_fma_f32 v227,v227,v210,v216\n"
CV "v220,v220,v221\n"
MF "v[88:91],a[176:179],v[180:183],v[88:91]\n"
"s_waitcnt vmcnt(0) lgkmcnt(0)\n"
CV "v221,v222,v223\n"
CV "v222,v224,v225\n"
MF "v[88:91],a[216:219],v[184:187],v[88:91]\n"
"s_barrier\n"
MF "v[92:95],a[216:219],a[244:247],v[252:255]\n"
"s_add_u32 s89,s89,s65\n"
TR "v[176:177],v0\n"
CV "v223,v226,v227\n"
MF "v[92:95],a[176:179],a[240:243],v[92:95]\n"
"s_add_u32 s88,s88,s65\n"
"s_add_u32 m0,s5,24576\n"
TR "v[178:179],v0 offset:2048\n"
MF "v[92:95],a[136:139],v[188:191],v[92:95]\n"
LD "v192,s[48:51],s89 offen lds\n"
MF "v[84:87],a[132:135],v[188:191],v[252:255]\n"
"s_add_u32 s18,s78,2\n"
TR "v[180:181],v0 offset:4096\n"
AW "a48,v220\n"
MF "v[84:87],a[172:175],a[240:243],v[84:87]\n"
"s_and_b32 s18,s18,15\n"
TR "v[182:183],v0 offset:6144\n"
AW "a49,v221\n"
MF "v[84:87],a[212:215],a[244:247],v[84:87]\n"
"s_mul_i32 s73,s73,204800\n"
TR "v[96:97],v0 offset:49152\n"
AW "a50,v222\n"
MF "v[76:79],a[208:211],a[244:247],v[252:255]\n"
"s_lshl_b32 s58,s58,1\n"
TR "v[184:185],v0 offset:8192\n"
AW "a51,v223\n"
MF "v[76:79],a[168:171],a[240:243],v[76:79]\n"
"s_add_u32 s58,s58,s60\n"
TR "v[186:187],v0 offset:10240\n"
AR "v228,a52\n"
MF "v[76:79],a[128:131],v[188:191],v[76:79]\n"
"s_lshl_b32 s58,s58,6\n"
TR "v[98:99],v0 offset:51200\n"
AR "v229,a53\n"
MF "v[68:71],a[124:127],v[188:191],v[252:255]\n"
"s_add_u32 s73,s73,s58\n"
TR "v[100:101],v0 offset:53248\n"
AR "v230,a54\n"
MF "v[68:71],a[164:167],a[240:243],v[68:71]\n"
LD "v193,s[48:51],s89 offen offset:1024 lds\n"
MF "v[68:71],a[204:207],a[244:247],v[68:71]\n"
"s_mul_i32 s59,s60,0xf0000000\n"
TR "v[102:103],v0 offset:55296\n"
AR "v231,a55\n"
MF "v[60:63],a[200:203],a[244:247],v[252:255]\n"
"s_or_b32 s94,s94,s59\n"
TR "v[104:105],v0 offset:57344\n"
AR "v232,a72\n"
MF "v[60:63],a[160:163],a[240:243],v[60:63]\n"
"s_mov_b32 s85,1\n"
TR "v[106:107],v0 offset:59392\n"
AR "v233,a73\n"
MF "v[60:63],a[120:123],v[188:191],v[60:63]\n"
"s_waitcnt lgkmcnt(5)\n"
TR "v[108:109],v4\n"
AR "v234,a74\n"
MF "v[16:19],a[140:143],v[176:179],v[16:19]\n"
"s_mov_b32 s82,0xf0000000\n"
TR "v[188:189],v1\n"
AR "v235,a75\n"
MF "v[16:19],a[180:183],v[180:183],v[16:19]\n"
"s_bfe_u32 s74,s22,0x30004\n"
TR "v[190:191],v1 offset:2048\n"
"ds_read_b128 a[72:75],v236 offset:57344\n"
MF "v[16:19],a[220:223],v[184:187],v[16:19]\n"
"s_mul_i32 s74,s74,4\n"
TR "v[110:111],v4 offset:2048\n"
"v_fma_f32 v228,v228,v211,v217\n"
MF "v[24:27],a[224:227],v[184:187],v[24:27]\n"
"s_add_u32 s74,s74,s56\n"
TR "a[240:241],v1 offset:4096\n"
"v_fma_f32 v229,v229,v211,v217\n"
MF "v[24:27],a[184:187],v[180:183],v[24:27]\n"
"s_mul_i32 s74,s74,15360\n"
TR "a[242:243],v1 offset:6144\n"
"v_fma_f32 v230,v230,v211,v217\n"
MF "v[24:27],a[144:147],v[176:179],v[24:27]\n"
LD "v194,s[48:51],s89 offen offset:2048 lds\n"
MF "v[32:35],a[148:151],v[176:179],v[32:35]\n"
"s_bfe_u32 s68,s22,0x10007\n"
TR "a[244:245],v1 offset:8192\n"
"v_fma_f32 v231,v231,v211,v217\n"
MF "v[32:35],a[188:191],v[180:183],v[32:35]\n"
"s_lshl_b32 s68,s68,3\n"
TR "a[246:247],v1 offset:10240\n"
"v_fma_f32 v232,v232,v211,v217\n"
MF "v[32:35],a[228:231],v[184:187],v[32:35]\n"
"s_mul_i32 s68,s68,491520\n"
TR "v[112:113],v4 offset:4096\n"
"v_fma_f32 v233,v233,v211,v217\n"
MF "v[40:43],a[232:235],v[184:187],v[40:43]\n"
"s_add_u32 s74,s74,s68\n"
TR "v[114:115],v4 offset:6144\n"
"v_fma_f32 v234,v234,v211,v217\n"
MF "v[40:43],a[192:195],v[180:183],v[40:43]\n"
"s_bfe_u32 s68,s22,0x30014\n"
TR "v[116:117],v4 offset:8192\n"
"v_fma_f32 v235,v235,v211,v217\n"
MF "v[40:43],a[152:155],v[176:179],v[40:43]\n"
"s_mul_i32 s68,s68,491520\n"
TR "v[118:119],v4 offset:10240\n"
CV "v228,v228,v229\n"
MF "v[48:51],a[156:159],v[176:179],v[48:51]\n"
"s_add_u32 s74,s74,s68\n"
TR "v[120:121],v1 offset:49152\n"
CV "v229,v230,v231\n"
MF "v[48:51],a[196:199],v[180:183],v[48:51]\n"
LD "v195,s[48:51],s89 offen offset:3072 lds\n"
MF "v[48:51],a[236:239],v[184:187],v[48:51]\n"
"s_waitcnt lgkmcnt(5)\n"
TR "v[122:123],v1 offset:51200\n"
CV "v230,v232,v233\n"
MF "v[52:55],a[236:239],a[244:247],v[52:55]\n"
"s_bfe_u32 s68,s22,0x30014\n"
TR "v[176:177],v2\n"
CV "v231,v234,v235\n"
MF "v[52:55],a[196:199],a[240:243],v[52:55]\n"
"s_add_u32 s68,s68,7\n"
TR "v[178:179],v2 offset:2048\n"
AW "a52,v228\n"
MF "v[52:55],a[156:159],v[188:191],v[52:55]\n"
"s_lshl_b32 s68,s68,7\n"
TR "v[124:125],v1 offset:53248\n"
AW "a53,v229\n"
MF "v[44:47],a[152:155],v[188:191],v[44:47]\n"
"s_and_b32 s68,s68,768\n"
TR "v[180:181],v2 offset:4096\n"
AW "a54,v230\n"
MF "v[44:47],a[192:195],a[240:243],v[44:47]\n"
"s_mul_i32 s58,s60,6144\n"
TR "v[182:183],v2 offset:6144\n"
AW "a55,v231\n"
MF "v[44:47],a[232:235],a[244:247],v[44:47]\n"
"s_add_u32 s58,s58,s79\n"
TR "v[126:127],v1 offset:55296\n"
AR "v220,a56\n"
MF "v[36:39],a[228:231],a[244:247],v[36:39]\n"
"s_add_u32 s58,s58,73728\n"
TR "v[184:185],v2 offset:8192\n"
AR "v221,a57\n"
MF "v[36:39],a[188:191],a[240:243],v[36:39]\n"
"s_add_u32 m0,s5,28672\n"
TR "v[186:187],v2 offset:10240\n"
AR "v222,a58\n"
MF "v[36:39],a[148:151],v[188:191],v[36:39]\n"
LD "v196,s[48:51],s89 offen lds\n"
MF "v[28:31],a[144:147],v[188:191],v[28:31]\n"
"s_bfe_u32 s4,s22,0x30014\n"
TR "v[128:129],v1 offset:57344\n"
AR "v223,a59\n"
MF "v[28:31],a[184:187],a[240:243],v[28:31]\n"
"s_add_u32 s4,s4,32\n"
TR "v[130:131],v1 offset:59392\n"
AR "v224,a76\n"
MF "v[28:31],a[224:227],a[244:247],v[28:31]\n"
"s_sub_u32 s4,s4,s92\n"
TR "v[132:133],v5\n"
AR "v225,a77\n"
MF "v[20:23],a[220:223],a[244:247],v[20:23]\n"
"s_cmp_eq_u32 s56,2\n"
"s_cselect_b32 s4,s4,64\n"
TR "v[134:135],v5 offset:2048\n"
MF "v[20:23],a[180:183],a[240:243],v[20:23]\n"
"s_mul_i32 s95,s92,3277\n"
TR "v[136:137],v5 offset:4096\n"
AR "v226,a78\n"
MF "v[20:23],a[140:143],v[188:191],v[20:23]\n"
"s_waitcnt lgkmcnt(5)\n"
TR "v[138:139],v5 offset:6144\n"
AR "v227,a79\n"
MF "v[56:59],a[140:143],v[176:179],v[56:59]\n"
"s_lshr_b32 s95,s95,16\n"
TR "v[188:189],v3\n"
"ds_read_b128 a[76:79],v236 offset:61440\n"
MF "v[56:59],a[180:183],v[180:183],v[56:59]\n"
"s_mul_i32 s59,s95,20\n"
TR "v[190:191],v3 offset:2048\n"
"v_fma_f32 v220,v220,v212,v218\n"
MF "v[56:59],a[220:223],v[184:187],v[56:59]\n"
LD "v197,s[48:51],s89 offen offset:1024 lds\n"
MF "v[64:67],a[224:227],v[184:187],v[64:67]\n"
"s_sub_u32 s59,s92,s59\n"
TR "a[240:241],v3 offset:4096\n"
"v_fma_f32 v221,v221,v212,v218\n"
MF "v[64:67],a[184:187],v[180:183],v[64:67]\n"
"s_cmp_ge_u32 s59,10\n"
"s_cselect_b32 s96,4294557760,0\n"
TR "a[242:243],v3 offset:6144\n"
MF "v[64:67],a[144:147],v[176:179],v[64:67]\n"
"s_add_u32 s96,s96,1024\n"
TR "v[140:141],v5 offset:8192\n"
"v_fma_f32 v222,v222,v212,v218\n"
MF "v[72:75],a[148:151],v[176:179],v[72:75]\n"
"s_mul_i32 s59,s59,40960\n"
TR "a[244:245],v3 offset:8192\n"
"v_fma_f32 v223,v223,v212,v218\n"
MF "v[72:75],a[188:191],v[180:183],v[72:75]\n"
"s_add_u32 s96,s96,s59\n"
TR "a[246:247],v3 offset:10240\n"
"v_fma_f32 v224,v224,v212,v218\n"
MF "v[72:75],a[228:231],v[184:187],v[72:75]\n"
"s_lshl_b32 s59,s95,7\n"
TR "v[142:143],v5 offset:10240\n"
"v_fma_f32 v225,v225,v212,v218\n"
MF "v[80:83],a[232:235],v[184:187],v[80:83]\n"
"s_add_u32 s96,s96,s59\n"
TR "v[144:145],v2 offset:49152\n"
"v_fma_f32 v226,v226,v212,v218\n"
MF "v[80:83],a[192:195],v[180:183],v[80:83]\n"
"s_lshl_b32 s2,s92,10\n"
TR "v[146:147],v2 offset:51200\n"
"v_fma_f32 v227,v227,v212,v218\n"
MF "v[80:83],a[152:155],v[176:179],v[80:83]\n"
"s_add_u32 s2,s2,s93\n"
TR "v[148:149],v2 offset:53248\n"
CV "v220,v220,v221\n"
MF "v[88:91],a[156:159],v[176:179],v[88:91]\n"
"s_mov_b32 s0,s96\n"
TR "v[150:151],v2 offset:55296\n"
CV "v221,v222,v223\n"
MF "v[88:91],a[196:199],v[180:183],v[88:91]\n"
TR "v[152:153],v2 offset:57344\n"
"s_waitcnt lgkmcnt(0)\n"
MF "v[88:91],a[236:239],v[184:187],v[88:91]\n"
"s_barrier\n"
MF "v[92:95],a[236:239],a[244:247],v[92:95]\n"
"s_sub_u32 s65,s61,9\n"
TR "v[154:155],v2 offset:59392\n"
CV "v222,v224,v225\n"
MF "v[92:95],a[196:199],a[240:243],v[92:95]\n"
"s_lshl_b32 s65,s65,7\n"
TR "v[156:157],v6\n"
CV "v223,v226,v227\n"
MF "v[92:95],a[156:159],v[188:191],v[92:95]\n"
"s_mov_b32 s64,s61\n"
TR "v[158:159],v6 offset:2048\n"
AW "a56,v220\n"
MF "v[84:87],a[152:155],v[188:191],v[84:87]\n"
"s_add_u32 s92,s92,s4\n"
TR "v[160:161],v6 offset:4096\n"
AW "a57,v221\n"
MF "v[84:87],a[192:195],a[240:243],v[84:87]\n"
"s_mul_i32 s95,s92,3277\n"
TR "v[162:163],v6 offset:6144\n"
AW "a58,v222\n"
MF "v[84:87],a[232:235],a[244:247],v[84:87]\n"
"s_add_u32 s80,s89,122880\n"
"s_add_u32 m0,s5,0\n"
TR "v[164:165],v6 offset:8192\n"
MF "v[76:79],a[228:231],a[244:247],v[76:79]\n"
LD "v192,s[48:51],s80 offen lds\n"
MF "v[76:79],a[188:191],a[240:243],v[76:79]\n"
"s_lshr_b32 s95,s95,16\n"
TR "v[166:167],v6 offset:10240\n"
AW "a59,v223\n"
MF "v[76:79],a[148:151],v[188:191],v[76:79]\n"
"s_mul_i32 s59,s95,20\n"
TR "v[168:169],v3 offset:49152\n"
AR "v228,a8\n"
MF "v[68:71],a[144:147],v[188:191],v[68:71]\n"
"s_sub_u32 s59,s92,s59\n"
TR "v[170:171],v3 offset:51200\n"
AR "v229,a9\n"
MF "v[68:71],a[184:187],a[240:243],v[68:71]\n"
LD "v193,s[48:51],s80 offen offset:1024 lds\n"
MF "v[68:71],a[224:227],a[244:247],v[68:71]\n"
"s_cmp_ge_u32 s59,10\n"
"s_cselect_b32 s96,4294557760,0\n"
TR "v[172:173],v3 offset:53248\n"
MF "v[60:63],a[220:223],a[244:247],v[60:63]\n"
"s_add_u32 s96,s96,1024\n"
TR "v[174:175],v3 offset:55296\n"
AR "v230,a10\n"
MF "v[60:63],a[180:183],a[240:243],v[60:63]\n"
"s_mul_i32 s59,s59,40960\n"
TR "v[204:205],v3 offset:57344\n"
AR "v231,a11\n"
MF "v[60:63],a[140:143],v[188:191],v[60:63]\n"
LD "v194,s[48:51],s80 offen offset:2048 lds\n"
MF "v[16:19],a[0:3],v[96:99],v[16:19]\n"
"s_add_u32 s96,s96,s59\n"
TR "v[206:207],v3 offset:59392\n"
AR "v232,a28\n"
MF "v[16:19],a[40:43],v[100:103],v[16:19]\n"
"s_lshl_b32 s59,s95,7\n"
TR "v[244:245],v7\n"
AR "v233,a29\n"
MF "v[16:19],a[80:83],v[104:107],v[16:19]\n"
"s_add_u32 s96,s96,s59\n"
TR "v[246:247],v7 offset:2048\n"
AR "v234,a30\n"
MF "v[16:19],a[20:23],v[108:111],v[16:19]\n"
LD "v195,s[48:51],s80 offen offset:3072 lds\n"
MF "v[16:19],a[60:63],v[112:115],v[16:19]\n"
"s_lshl_b32 s3,s92,10\n"
TR "v[248:249],v7 offset:4096\n"
AR "v235,a31\n"
MF "v[16:19],a[100:103],v[116:119],v[16:19]\n"
"s_nop 0\n"
TR "v[250:251],v7 offset:6144\n"
"ds_read_b128 a[28:31],v236 offset:32768\n"
MF "v[20:23],a[100:103],v[140:143],v[20:23]\n"
"s_add_u32 m0,s5,4096\n"
TR "v[238:239],v7 offset:8192\n"
"v_fma_f32 v228,v228,v210,v216\n"
MF "v[20:23],a[60:63],v[136:139],v[20:23]\n"
LD "v196,s[48:51],s80 offen lds\n"
MF "v[20:23],a[20:23],v[132:135],v[20:23]\n"
"s_add_u32 s3,s3,s93\n"
TR "v[240:241],v7 offset:10240\n"
"v_fma_f32 v229,v229,v210,v216\n"
MF "v[20:23],a[80:83],v[128:131],v[20:23]\n"
"s_cmp_lt_u32 s92,40\n"
"s_cselect_b32 s3,s3,0xf0000000\n"
"v_fma_f32 v230,v230,v210,v216\n"
MF "v[20:23],a[40:43],v[124:127],v[20:23]\n"
"s_cmp_lt_u32 s92,40\n"
"s_cselect_b32 s1,s96,0xf0000000\n"
"v_fma_f32 v231,v231,v210,v216\n"
MF "v[20:23],a[0:3],v[120:123],v[20:23]\n"
"s_waitcnt lgkmcnt(13)\n"
LD "v197,s[48:51],s80 offen offset:1024 lds\n"
MF "v[56:59],a[0:3],v[144:147],v[56:59]\n"
"s_sub_u32 s92,s92,s4\n"
"v_fma_f32 v232,v232,v210,v216\n"
"v_fma_f32 v233,v233,v210,v216\n"
MF "v[56:59],a[40:43],v[148:151],v[56:59]\n"
"s_sub_u32 s2,s2,204800\n"
"v_fma_f32 v234,v234,v210,v216\n"
"v_fma_f32 v235,v235,v210,v216\n"
MF "v[56:59],a[80:83],v[152:155],v[56:59]\n"
"s_mov_b32 s92,64\n"
CV "v228,v228,v229\n"
CV "v229,v230,v231\n"
MF "v[56:59],a[20:23],v[156:159],v[56:59]\n"
CV "v230,v232,v233\n"
CV "v231,v234,v235\n"
MF "v[56:59],a[60:63],v[160:163],v[56:59]\n"
AW "a8,v228\n"
AW "a9,v229\n"
MF "v[56:59],a[100:103],v[164:167],v[56:59]\n"
"s_waitcnt lgkmcnt(0)\n"
AW "a10,v230\n"
AW "a11,v231\n"
MF "v[60:63],a[100:103],v[238:241],v[60:63]\n"
AR "v220,a88\n"
AR "v221,a89\n"
MF "v[60:63],a[60:63],v[248:251],v[60:63]\n"
".Lpg25h:\n"
"s_cbranch_execz .Lpg29h\n"
AR "v222,a90\n"
AR "v223,a91\n"
MF "v[60:63],a[20:23],v[244:247],v[60:63]\n"
AR "v224,a108\n"
AR "v225,a109\n"
MF "v[60:63],a[80:83],v[204:207],v[60:63]\n"
AR "v226,a110\n"
AR "v227,a111\n"
MF "v[60:63],a[40:43],v[172:175],v[60:63]\n"
"ds_read_b128 a[108:111],v237 offset:8192\n"
"v_fma_f32 v220,v220,v210,v216\n"
MF "v[60:63],a[0:3],v[168:171],v[60:63]\n"
"v_fma_f32 v221,v221,v210,v216\n"
"v_fma_f32 v222,v222,v210,v216\n"
MF "v[68:71],a[4:7],v[168:171],v[68:71]\n"
"v_fma_f32 v223,v223,v210,v216\n"
"v_fma_f32 v224,v224,v210,v216\n"
MF "v[68:71],a[44:47],v[172:175],v[68:71]\n"
"v_fma_f32 v225,v225,v210,v216\n"
"v_fma_f32 v226,v226,v210,v216\n"
MF "v[68:71],a[84:87],v[204:207],v[68:71]\n"
"v_fma_f32 v227,v227,v210,v216\n"
CV "v220,v220,v221\n"
MF "v[68:71],a[24:27],v[244:247],v[68:71]\n"
CV "v221,v222,v223\n"
CV "v222,v224,v225\n"
MF "v[68:71],a[64:67],v[248:251],v[68:71]\n"
CV "v223,v226,v227\n"
AW "a88,v220\n"
MF "v[68:71],a[104:107],v[238:241],v[68:71]\n"
"s_waitcnt lgkmcnt(0)\n"
AW "a89,v221\n"
AW "a90,v222\n"
MF "v[64:67],a[104:107],v[164:167],v[64:67]\n"
"s_barrier\n"
MF "v[64:67],a[64:67],v[160:163],v[64:67]\n"
"s_add_u32 m0,s5,49152\n"
AW "a91,v223\n"
AR "v228,a12\n"
MF "v[64:67],a[24:27],v[156:159],v[64:67]\n"
LD "v192,s[48:51],s88 offen lds\n"
MF "v[64:67],a[84:87],v[152:155],v[64:67]\n"
AR "v229,a13\n"
AR "v230,a14\n"
MF "v[64:67],a[44:47],v[148:151],v[64:67]\n"
LD "v193,s[48:51],s88 offen offset:1024 lds\n"
MF "v[64:67],a[4:7],v[144:147],v[64:67]\n"
AR "v231,a15\n"
AR "v232,a32\n"
MF "v[28:31],a[4:7],v[120:123],v[28:31]\n"
AR "v233,a33\n"
AR "v234,a34\n"
MF "v[28:31],a[44:47],v[124:127],v[28:31]\n"
LD "v194,s[48:51],s88 offen offset:2048 lds\n"
MF "v[28:31],a[84:87],v[128:131],v[28:31]\n"
AR "v235,a35\n"
"v_fma_f32 v228,v228,v211,v217\n"
MF "v[28:31],a[24:27],v[132:135],v[28:31]\n"
"ds_read_b128 a[32:35],v236 offset:36864\n"
"v_fma_f32 v229,v229,v211,v217\n"
MF "v[28:31],a[64:67],v[136:139],v[28:31]\n"
LD "v195,s[48:51],s88 offen offset:3072 lds\n"
MF "v[28:31],a[104:107],v[140:143],v[28:31]\n"
"v_fma_f32 v230,v230,v211,v217\n"
"v_fma_f32 v231,v231,v211,v217\n"
MF "v[24:27],a[104:107],v[116:119],v[24:27]\n"
"v_fma_f32 v232,v232,v211,v217\n"
"v_fma_f32 v233,v233,v211,v217\n"
MF "v[24:27],a[64:67],v[112:115],v[24:27]\n"
"v_fma_f32 v234,v234,v211,v217\n"
"v_fma_f32 v235,v235,v211,v217\n"
MF "v[24:27],a[24:27],v[108:111],v[24:27]\n"
CV "v228,v228,v229\n"
CV "v229,v230,v231\n"
MF "v[24:27],a[84:87],v[104:107],v[24:27]\n"
CV "v230,v232,v233\n"
CV "v231,v234,v235\n"
MF "v[24:27],a[44:47],v[100:103],v[24:27]\n"
AW "a12,v228\n"
AW "a13,v229\n"
MF "v[24:27],a[4:7],v[96:99],v[24:27]\n"
AW "a14,v230\n"
AW "a15,v231\n"
MF "v[32:35],a[8:11],v[96:99],v[32:35]\n"
AR "v220,a92\n"
AR "v221,a93\n"
MF "v[32:35],a[48:51],v[100:103],v[32:35]\n"
AR "v222,a94\n"
AR "v223,a95\n"
MF "v[32:35],a[88:91],v[104:107],v[32:35]\n"
AR "v224,a112\n"
AR "v225,a113\n"
MF "v[32:35],a[28:31],v[108:111],v[32:35]\n"
AR "v226,a114\n"
AR "v227,a115\n"
MF "v[32:35],a[68:71],v[112:115],v[32:35]\n"
"ds_read_b128 a[112:115],v237 offset:12288\n"
"v_fma_f32 v220,v220,v211,v217\n"
MF "v[32:35],a[108:111],v[116:119],v[32:35]\n"
"v_fma_f32 v221,v221,v211,v217\n"
"v_fma_f32 v222,v222,v211,v217\n"
MF "v[36:39],a[108:111],v[140:143],v[36:39]\n"
"v_fma_f32 v223,v223,v211,v217\n"
"v_fma_f32 v224,v224,v211,v217\n"
MF "v[36:39],a[68:71],v[136:139],v[36:39]\n"
"v_fma_f32 v225,v225,v211,v217\n"
"v_fma_f32 v226,v226,v211,v217\n"
MF "v[36:39],a[28:31],v[132:135],v[36:39]\n"
"v_fma_f32 v227,v227,v211,v217\n"
CV "v220,v220,v221\n"
MF "v[36:39],a[88:91],v[128:131],v[36:39]\n"
CV "v221,v222,v223\n"
CV "v222,v224,v225\n"
MF "v[36:39],a[48:51],v[124:127],v[36:39]\n"
CV "v223,v226,v227\n"
AW "a92,v220\n"
MF "v[36:39],a[8:11],v[120:123],v[36:39]\n"
AW "a93,v221\n"
AW "a94,v222\n"
MF "v[72:75],a[8:11],v[144:147],v[72:75]\n"
AW "a95,v223\n"
AR "v228,a16\n"
MF "v[72:75],a[48:51],v[148:151],v[72:75]\n"
AR "v229,a17\n"
AR "v230,a18\n"
MF "v[72:75],a[88:91],v[152:155],v[72:75]\n"
AR "v231,a19\n"
AR "v232,a36\n"
MF "v[72:75],a[28:31],v[156:159],v[72:75]\n"
AR "v233,a37\n"
AR "v234,a38\n"
MF "v[72:75],a[68:71],v[160:163],v[72:75]\n"
AR "v235,a39\n"
"v_fma_f32 v228,v228,v212,v218\n"
MF "v[72:75],a[108:111],v[164:167],v[72:75]\n"
"ds_read_b128 a[36:39],v236 offset:40960\n"
"v_fma_f32 v229,v229,v212,v218\n"
MF "v[76:79],a[108:111],v[238:241],v[76:79]\n"
"v_fma_f32 v230,v230,v212,v218\n"
"v_fma_f32 v231,v231,v212,v218\n"
MF "v[76:79],a[68:71],v[248:251],v[76:79]\n"
"v_fma_f32 v232,v232,v212,v218\n"
"v_fma_f32 v233,v233,v212,v218\n"
MF "v[76:79],a[28:31],v[244:247],v[76:79]\n"
"v_fma_f32 v234,v234,v212,v218\n"
"v_fma_f32 v235,v235,v212,v218\n"
MF "v[76:79],a[88:91],v[204:207],v[76:79]\n"
CV "v228,v228,v229\n"
CV "v229,v230,v231\n"
MF "v[76:79],a[48:51],v[172:175],v[76:79]\n"
CV "v230,v232,v233\n"
CV "v231,v234,v235\n"
MF "v[76:79],a[8:11],v[168:171],v[76:79]\n"
"s_waitcnt lgkmcnt(1)\n"
AW "a16,v228\n"
AW "a17,v229\n"
MF "v[84:87],a[12:15],v[168:171],v[84:87]\n"
AW "a18,v230\n"
AW "a19,v231\n"
MF "v[84:87],a[52:55],v[172:175],v[84:87]\n"
AR "v220,a96\n"
AR "v221,a97\n"
MF "v[84:87],a[92:95],v[204:207],v[84:87]\n"
AR "v222,a98\n"
AR "v223,a99\n"
MF "v[84:87],a[32:35],v[244:247],v[84:87]\n"
AR "v224,a116\n"
AR "v225,a117\n"
MF "v[84:87],a[72:75],v[248:251],v[84:87]\n"
AR "v226,a118\n"
AR "v227,a119\n"
MF "v[84:87],a[112:115],v[238:241],v[84:87]\n"
"ds_read_b128 a[116:119],v237 offset:16384\n"
"v_fma_f32 v220,v220,v212,v218\n"
MF "v[80:83],a[112:115],v[164:167],v[80:83]\n"
"v_fma_f32 v221,v221,v212,v218\n"
"v_fma_f32 v222,v222,v212,v218\n"
MF "v[80:83],a[72:75],v[160:163],v[80:83]\n"
"v_fma_f32 v223,v223,v212,v218\n"
"v_fma_f32 v224,v224,v212,v218\n"
MF "v[80:83],a[32:35],v[156:159],v[80:83]\n"
"v_fma_f32 v225,v225,v212,v218\n"
"v_fma_f32 v226,v226,v212,v218\n"
MF "v[80:83],a[92:95],v[152:155],v[80:83]\n"
"v_fma_f32 v227,v227,v212,v218\n"
CV "v220,v220,v221\n"
MF "v[80:83],a[52:55],v[148:151],v[80:83]\n"
CV "v221,v222,v223\n"
CV "v222,v224,v225\n"
MF "v[80:83],a[12:15],v[144:147],v[80:83]\n"
CV "v223,v226,v227\n"
AW "a96,v220\n"
MF "v[44:47],a[12:15],v[120:123],v[44:47]\n"
AW "a97,v221\n"
AW "a98,v222\n"
MF "v[44:47],a[52:55],v[124:127],v[44:47]\n"
"s_add_u32 m0,s5,73728\n"
AW "a99,v223\n"
"v_lshrrev_b32 v13,2,v9\n"
MF "v[44:47],a[92:95],v[128:131],v[44:47]\n"
"s_nop 0\n"
"ds_write_addtid_b32 v16\n"
"ds_write_addtid_b32 v17 offset:256\n"
MF "v[44:47],a[32:35],v[132:135],v[44:47]\n"
"s_nop 0\n"
"ds_write_addtid_b32 v18 offset:512\n"
"ds_write_addtid_b32 v19 offset:768\n"
MF "v[44:47],a[72:75],v[136:139],v[44:47]\n"
"s_nop 0\n"
"ds_write_addtid_b32 v20 offset:1024\n"
"ds_write_addtid_b32 v21 offset:1280\n"
MF "v[44:47],a[112:115],v[140:143],v[44:47]\n"
"s_nop 0\n"
"ds_write_addtid_b32 v22 offset:1536\n"
"ds_write_addtid_b32 v23 offset:1792\n"
MF "v[40:43],a[112:115],v[116:119],v[40:43]\n"
"s_nop 0\n"
"ds_write_addtid_b32 v24 offset:2048\n"
"ds_write_addtid_b32 v25 offset:2304\n"
MF "v[40:43],a[72:75],v[112:115],v[40:43]\n"
"s_nop 0\n"
"ds_write_addtid_b32 v26 offset:2560\n"
"ds_write_addtid_b32 v27 offset:2816\n"
MF "v[40:43],a[32:35],v[108:111],v[40:43]\n"
"s_nop 0\n"
"ds_write_addtid_b32 v28 offset:3072\n"
"ds_write_addtid_b32 v29 offset:3328\n"
MF "v[40:43],a[92:95],v[104:107],v[40:43]\n"
"s_nop 0\n"
"ds_write_addtid_b32 v30 offset:3584\n"
"ds_write_addtid_b32 v31 offset:3840\n"
MF "v[40:43],a[52:55],v[100:103],v[40:43]\n"
"s_nop 0\n"
"ds_write_addtid_b32 v32 offset:4096\n"
"ds_write_addtid_b32 v33 offset:4352\n"
MF "v[40:43],a[12:15],v[96:99],v[40:43]\n"
"s_waitcnt lgkmcnt(15)\n"
"ds_write_addtid_b32 v34 offset:4608\n"
"ds_write_addtid_b32 v35 offset:4864\n"
MF "v[48:51],a[16:19],v[96:99],v[48:51]\n"
"s_nop 0\n"
"ds_write_addtid_b32 v36 offset:49152\n"
"ds_write_addtid_b32 v37 offset:49408\n"
MF "v[48:51],a[56:59],v[100:103],v[48:51]\n"
"s_nop 0\n"
"ds_write_addtid_b32 v38 offset:49664\n"
"ds_write_addtid_b32 v39 offset:49920\n"
MF "v[48:51],a[96:99],v[104:107],v[48:51]\n"
"s_nop 0\n"
"ds_write_addtid_b32 v44 offset:51200\n"
"ds_write_addtid_b32 v45 offset:51456\n"
MF "v[48:51],a[36:39],v[108:111],v[48:51]\n"
"s_nop 0\n"
"ds_write_addtid_b32 v46 offset:51712\n"
"ds_write_addtid_b32 v47 offset:51968\n"
MF "v[48:51],a[76:79],v[112:115],v[48:51]\n"
"s_nop 0\n"
"ds_write_addtid_b32 v40 offset:50176\n"
"ds_write_addtid_b32 v41 offset:50432\n"
MF "v[48:51],a[116:119],v[116:119],v[48:51]\n"
"s_nop 0\n"
"ds_write_addtid_b32 v42 offset:50688\n"
"ds_write_addtid_b32 v43 offset:50944\n"
MF "v[52:55],a[116:119],v[140:143],v[52:55]\n"
"v_add_u32 v13,s58,v13\n"
AR "v8,a252\n"
MF "v[52:55],a[76:79],v[136:139],v[52:55]\n"
"s_waitcnt vmcnt(10) lgkmcnt(0)\n"
AR "v11,a253\n"
MF "v[52:55],a[36:39],v[132:135],v[52:55]\n"
"s_barrier\n"
MF "v[52:55],a[96:99],v[128:131],v[52:55]\n"
"s_nop 0\n"
TR "v[176:177],v0 offset:24576\n"
TR "v[178:179],v0 offset:26624\n"
MF "v[52:55],a[56:59],v[124:127],v[52:55]\n"
"s_nop 0\n"
TR "v[180:181],v0 offset:28672\n"
TR "v[182:183],v0 offset:30720\n"
MF "v[52:55],a[16:19],v[120:123],v[52:55]\n"
"s_nop 0\n"
TR "v[184:185],v0 offset:32768\n"
TR "v[186:187],v0 offset:34816\n"
MF "v[88:91],a[16:19],v[144:147],v[88:91]\n"
"s_nop 0\n"
"ds_write_addtid_b32 v48 offset:52224\n"
"ds_write_addtid_b32 v49 offset:52480\n"
MF "v[88:91],a[56:59],v[148:151],v[88:91]\n"
"s_nop 0\n"
"ds_write_addtid_b32 v50 offset:52736\n"
"ds_write_addtid_b32 v51 offset:52992\n"
MF "v[88:91],a[96:99],v[152:155],v[88:91]\n"
"s_nop 0\n"
"ds_write_addtid_b32 v52 offset:53248\n"
"ds_write_addtid_b32 v53 offset:53504\n"
MF "v[88:91],a[36:39],v[156:159],v[88:91]\n"
"s_nop 0\n"
"ds_write_addtid_b32 v54 offset:53760\n"
"ds_write_addtid_b32 v55 offset:54016\n"
MF "v[88:91],a[76:79],v[160:163],v[88:91]\n"
MF "v[88:91],a[116:119],v[164:167],v[88:91]\n"
MF "v[92:95],a[116:119],v[238:241],v[92:95]\n"
MF "v[92:95],a[76:79],v[248:251],v[92:95]\n"
"s_waitcnt lgkmcnt(0)\n"
MF "v[92:95],a[36:39],v[244:247],v[92:95]\n"
"s_barrier\n"
MF "v[92:95],a[96:99],v[204:207],v[92:95]\n"
"ds_read2st64_b32 v[48:49],v13 offset0:204 offset1:205\n"
MF "v[92:95],a[56:59],v[172:175],v[92:95]\n"
"ds_read2st64_b32 v[50:51],v13 offset0:206 offset1:207\n"
MF "v[92:95],a[16:19],v[168:171],v[92:95]\n"
"ds_read2st64_b32 v[52:53],v13 offset0:208 offset1:209\n"
"ds_read2st64_b32 v[54:55],v13 offset0:210 offset1:211\n"
"ds_read2st64_b32 v[40:41],v13 offset0:196 offset1:197\n"
"ds_read2st64_b32 v[42:43],v13 offset0:198 offset1:199\n"
".Lps5:\n"
MF "v[96:99],a[0:3],v[176:179],v[252:255]\n"
"s_nop 0\n"
TR "v[188:189],v1 offset:24576\n"
TR "v[190:191],v1 offset:26624\n"
MF "v[96:99],a[40:43],v[180:183],v[96:99]\n"
"s_nop 0\n"
TR "a[240:241],v1 offset:28672\n"
TR "a[242:243],v1 offset:30720\n"
MF "v[96:99],a[80:83],v[184:187],v[96:99]\n"
"s_add_u32 m0,s5,53248\n"
TR "a[244:245],v1 offset:32768\n"
TR "a[246:247],v1 offset:34816\n"
MF "v[104:107],a[84:87],v[184:187],v[252:255]\n"
"s_waitcnt lgkmcnt(8)\n"
LD "v196,s[48:51],s88 offen lds\n"
MF "v[104:107],a[44:47],v[180:183],v[104:107]\n"
"ds_read2st64_b32 v[44:45],v13 offset0:200 offset1:201\n"
"v_add_f32 v88,v88,v48\n"
MF "v[104:107],a[4:7],v[176:179],v[104:107]\n"
"ds_read2st64_b32 v[46:47],v13 offset0:202 offset1:203\n"
"v_add_f32 v89,v89,v49\n"
MF "v[112:115],a[8:11],v[176:179],v[252:255]\n"
"ds_read2st64_b32 v[32:33],v13 offset0:16 offset1:17\n"
"v_add_f32 v90,v90,v50\n"
MF "v[112:115],a[48:51],v[180:183],v[112:115]\n"
LD "v197,s[48:51],s88 offen offset:1024 lds\n"
MF "v[112:115],a[88:91],v[184:187],v[112:115]\n"
"ds_read2st64_b32 v[34:35],v13 offset0:18 offset1:19\n"
"v_add_f32 v91,v91,v51\n"
MF "v[120:123],a[92:95],v[184:187],v[252:255]\n"
"ds_read2st64_b32 v[36:37],v13 offset0:192 offset1:193\n"
"v_add_f32 v92,v92,v52\n"
MF "v[120:123],a[52:55],v[180:183],v[120:123]\n"
"s_add_u32 s80,s88,122880\n"
"ds_read2st64_b32 v[38:39],v13 offset0:194 offset1:195\n"
"v_add_f32 v93,v93,v53\n"
MF "v[120:123],a[12:15],v[176:179],v[120:123]\n"
"s_add_u32 m0,s5,98304\n"
"ds_read2st64_b32 v[24:25],v13 offset0:8 offset1:9\n"
"v_add_f32 v94,v94,v54\n"
MF "v[128:131],a[16:19],v[176:179],v[252:255]\n"
LD "v192,s[48:51],s80 offen lds\n"
MF "v[128:131],a[56:59],v[180:183],v[128:131]\n"
"ds_read2st64_b32 v[26:27],v13 offset0:10 offset1:11\n"
"v_add_f32 v95,v95,v55\n"
MF "v[128:131],a[96:99],v[184:187],v[128:131]\n"
"s_waitcnt lgkmcnt(8)\n"
LD "v193,s[48:51],s80 offen offset:1024 lds\n"
MF "v[132:135],a[96:99],a[244:247],v[252:255]\n"
"s_nop 0\n"
TR "v[176:177],v2 offset:24576\n"
TR "v[178:179],v2 offset:26624\n"
MF "v[132:135],a[56:59],a[240:243],v[132:135]\n"
"s_nop 0\n"
TR "v[180:181],v2 offset:28672\n"
TR "v[182:183],v2 offset:30720\n"
MF "v[132:135],a[16:19],v[188:191],v[132:135]\n"
"s_nop 0\n"
TR "v[184:185],v2 offset:32768\n"
TR "v[186:187],v2 offset:34816\n"
MF "v[124:127],a[12:15],v[188:191],v[252:255]\n"
"ds_read2st64_b32 v[28:29],v13 offset0:12 offset1:13\n"
CV "v88,v88,v89\n"
MF "v[124:127],a[52:55],a[240:243],v[124:127]\n"
"ds_read2st64_b32 v[30:31],v13 offset0:14 offset1:15\n"
CV "v89,v90,v91\n"
MF "v[124:127],a[92:95],a[244:247],v[124:127]\n"
"ds_read2st64_b32 v[16:17],v13 offset0:0 offset1:1\n"
CV "v90,v92,v93\n"
MF "v[116:119],a[88:91],a[244:247],v[252:255]\n"
LD "v194,s[48:51],s80 offen offset:2048 lds\n"
MF "v[116:119],a[48:51],a[240:243],v[116:119]\n"
"s_waitcnt lgkmcnt(15)\n"
"ds_read2st64_b32 v[18:19],v13 offset0:2 offset1:3\n"
CV "v91,v94,v95\n"
MF "v[116:119],a[8:11],v[188:191],v[116:119]\n"
"ds_read2st64_b32 v[20:21],v13 offset0:4 offset1:5\n"
"v_add_f32 v80,v80,v40\n"
MF "v[108:111],a[4:7],v[188:191],v[252:255]\n"
"s_add_u32 s87,s90,4096\n"
"ds_read2st64_b32 v[22:23],v13 offset0:6 offset1:7\n"
"v_add_f32 v81,v81,v41\n"
MF "v[108:111],a[44:47],a[240:243],v[108:111]\n"
"buffer_store_dwordx4 v[88:91],v9,s[40:43],s87 offen sc1\n"
MF "v[108:111],a[84:87],a[244:247],v[108:111]\n"
LD "v195,s[48:51],s80 offen offset:3072 lds\n"
MF "v[100:103],a[80:83],a[244:247],v[252:255]\n"
"v_add_f32 v82,v82,v42\n"
"v_add_f32 v83,v83,v43\n"
MF "v[100:103],a[40:43],a[240:243],v[100:103]\n"
"s_add_u32 m0,s5,102400\n"
"v_add_f32 v84,v84,v44\n"
"v_add_f32 v85,v85,v45\n"
MF "v[100:103],a[0:3],v[188:191],v[100:103]\n"
"s_waitcnt lgkmcnt(6)\n"
LD "v196,s[48:51],s80 offen lds\n"
MF "v[136:139],a[0:3],v[176:179],v[252:255]\n"
"s_nop 0\n"
TR "v[188:189],v3 offset:24576\n"
TR "v[190:191],v3 offset:26624\n"
MF "v[136:139],a[40:43],v[180:183],v[136:139]\n"
"s_nop 0\n"
TR "a[240:241],v3 offset:28672\n"
TR "a[242:243],v3 offset:30720\n"
MF "v[136:139],a[80:83],v[184:187],v[136:139]\n"
"s_nop 0\n"
TR "a[244:245],v3 offset:32768\n"
TR "a[246:247],v3 offset:34816\n"
MF "v[144:147],a[84:87],v[184:187],v[252:255]\n"
"v_add_f32 v86,v86,v46\n"
"v_add_f32 v87,v87,v47\n"
MF "v[144:147],a[44:47],v[180:183],v[144:147]\n"
CV "v80,v80,v81\n"
CV "v81,v82,v83\n"
MF "v[144:147],a[4:7],v[176:179],v[144:147]\n"
LD "v197,s[48:51],s80 offen offset:1024 lds\n"
MF "v[152:155],a[8:11],v[176:179],v[252:255]\n"
CV "v82,v84,v85\n"
CV "v83,v86,v87\n"
MF "v[152:155],a[48:51],v[180:183],v[152:155]\n"
"buffer_store_dwordx4 v[80:83],v9,s[40:43],s90 offen offset:3072 sc1\n"
MF "v[152:155],a[88:91],v[184:187],v[152:155]\n"
"v_add_f32 v72,v72,v32\n"
"v_add_f32 v73,v73,v33\n"
MF "v[160:163],a[92:95],v[184:187],v[252:255]\n"
"v_add_f32 v74,v74,v34\n"
"v_add_f32 v75,v75,v35\n"
MF "v[160:163],a[52:55],v[180:183],v[160:163]\n"
"v_add_f32 v76,v76,v36\n"
"v_add_f32 v77,v77,v37\n"
MF "v[160:163],a[12:15],v[176:179],v[160:163]\n"
"v_add_f32 v78,v78,v38\n"
"v_add_f32 v79,v79,v39\n"
MF "v[168:171],a[16:19],v[176:179],v[252:255]\n"
CV "v72,v72,v73\n"
CV "v73,v74,v75\n"
MF "v[168:171],a[56:59],v[180:183],v[168:171]\n"
"s_waitcnt vmcnt(14) lgkmcnt(0)\n"
CV "v74,v76,v77\n"
CV "v75,v78,v79\n"
MF "v[168:171],a[96:99],v[184:187],v[168:171]\n"
"s_barrier\n"
MF "v[172:175],a[96:99],a[244:247],v[252:255]\n"
TR "v[176:177],v0\n"
MF "v[172:175],a[56:59],a[240:243],v[172:175]\n"
"s_add_u32 s89,s89,s65\n"
TR "v[178:179],v0 offset:2048\n"
MF "v[172:175],a[16:19],v[188:191],v[172:175]\n"
"buffer_store_dwordx4 v[72:75],v9,s[40:43],s90 offen offset:2048 sc1\n"
MF "v[164:167],a[12:15],v[188:191],v[252:255]\n"
"s_add_u32 s88,s88,s65\n"
TR "v[180:181],v0 offset:4096\n"
MF "v[164:167],a[52:55],a[240:243],v[164:167]\n"
"s_add_u32 m0,s5,24576\n"
TR "v[182:183],v0 offset:6144\n"
MF "v[164:167],a[92:95],a[244:247],v[164:167]\n"
LD "v192,s[48:51],s89 offen lds\n"
MF "v[156:159],a[88:91],a[244:247],v[252:255]\n"
TR "v[184:185],v0 offset:8192\n"
MF "v[156:159],a[48:51],a[240:243],v[156:159]\n"
TR "v[186:187],v0 offset:10240\n"
MF "v[156:159],a[8:11],v[188:191],v[156:159]\n"
LD "v193,s[48:51],s89 offen offset:1024 lds\n"
MF "v[148:151],a[4:7],v[188:191],v[252:255]\n"
"v_add_f32 v64,v64,v24\n"
"v_add_f32 v65,v65,v25\n"
MF "v[148:151],a[44:47],a[240:243],v[148:151]\n"
"v_add_f32 v66,v66,v26\n"
"v_add_f32 v67,v67,v27\n"
MF "v[148:151],a[84:87],a[244:247],v[148:151]\n"
"v_add_f32 v68,v68,v28\n"
"v_add_f32 v69,v69,v29\n"
MF "v[140:143],a[80:83],a[244:247],v[252:255]\n"
"v_add_f32 v70,v70,v30\n"
"v_add_f32 v71,v71,v31\n"
MF "v[140:143],a[40:43],a[240:243],v[140:143]\n"
CV "v64,v64,v65\n"
CV "v65,v66,v67\n"
MF "v[140:143],a[0:3],v[188:191],v[140:143]\n"
"s_waitcnt lgkmcnt(0)\n"
CV "v66,v68,v69\n"
CV "v67,v70,v71\n"
MF "v[96:99],a[120:123],v[176:179],v[96:99]\n"
TR "v[188:189],v1\n"
MF "v[96:99],a[160:163],v[180:183],v[96:99]\n"
TR "v[190:191],v1 offset:2048\n"
MF "v[96:99],a[200:203],v[184:187],v[96:99]\n"
"buffer_store_dwordx4 v[64:67],v9,s[40:43],s90 offen offset:1024 sc1\n"
MF "v[104:107],a[204:207],v[184:187],v[104:107]\n"
TR "a[240:241],v1 offset:4096\n"
MF "v[104:107],a[164:167],v[180:183],v[104:107]\n"
TR "a[242:243],v1 offset:6144\n"
MF "v[104:107],a[124:127],v[176:179],v[104:107]\n"
LD "v194,s[48:51],s89 offen offset:2048 lds\n"
MF "v[112:115],a[128:131],v[176:179],v[112:115]\n"
TR "a[244:245],v1 offset:8192\n"
MF "v[112:115],a[168:171],v[180:183],v[112:115]\n"
TR "a[246:247],v1 offset:10240\n"
MF "v[112:115],a[208:211],v[184:187],v[112:115]\n"
MF "v[120:123],a[212:215],v[184:187],v[120:123]\n"
MF "v[120:123],a[172:175],v[180:183],v[120:123]\n"
MF "v[120:123],a[132:135],v[176:179],v[120:123]\n"
LD "v195,s[48:51],s89 offen offset:3072 lds\n"
MF "v[128:131],a[136:139],v[176:179],v[128:131]\n"
"v_add_f32 v56,v56,v16\n"
"v_add_f32 v57,v57,v17\n"
MF "v[128:131],a[176:179],v[180:183],v[128:131]\n"
"v_add_f32 v58,v58,v18\n"
"v_add_f32 v59,v59,v19\n"
MF "v[128:131],a[216:219],v[184:187],v[128:131]\n"
"s_waitcnt lgkmcnt(0)\n"
"v_add_f32 v60,v60,v20\n"
"v_add_f32 v61,v61,v21\n"
MF "v[132:135],a[216:219],a[244:247],v[132:135]\n"
TR "v[176:177],v2\n"
"v_add_f32 v62,v62,v22\n"
MF "v[132:135],a[176:179],a[240:243],v[132:135]\n"
TR "v[178:179],v2 offset:2048\n"
"v_add_f32 v63,v63,v23\n"
MF "v[132:135],a[136:139],v[188:191],v[132:135]\n"
CV "v56,v56,v57\n"
CV "v57,v58,v59\n"
MF "v[124:127],a[132:135],v[188:191],v[124:127]\n"
TR "v[180:181],v2 offset:4096\n"
CV "v58,v60,v61\n"
MF "v[124:127],a[172:175],a[240:243],v[124:127]\n"
TR "v[182:183],v2 offset:6144\n"
CV "v59,v62,v63\n"
MF "v[124:127],a[212:215],a[244:247],v[124:127]\n"
"buffer_store_dwordx4 v[56:59],v9,s[40:43],s90 offen sc1\n"
MF "v[116:119],a[208:211],a[244:247],v[116:119]\n"
"s_add_u32 s90,s90,20480\n"
TR "v[184:185],v2 offset:8192\n"
MF "v[116:119],a[168:171],a[240:243],v[116:119]\n"
"s_add_u32 m0,s5,28672\n"
TR "v[186:187],v2 offset:10240\n"
MF "v[116:119],a[128:131],v[188:191],v[116:119]\n"
LD "v196,s[48:51],s89 offen lds\n"
MF "v[108:111],a[124:127],v[188:191],v[108:111]\n"
MF "v[108:111],a[164:167],a[240:243],v[108:111]\n"
".Lpg26h:\n"
"s_cbranch_execz .Lpg30h\n"
"v_add_u32 v4,98304,v0\n"
"v_add_u32 v5,98304,v1\n"
MF "v[108:111],a[204:207],a[244:247],v[108:111]\n"
"v_add_u32 v6,98304,v2\n"
"v_add_u32 v7,98304,v3\n"
MF "v[100:103],a[200:203],a[244:247],v[100:103]\n"
"v_mov_b32 v15,0\n"
"v_lshrrev_b32 v242,5,v9\n"
MF "v[100:103],a[160:163],a[240:243],v[100:103]\n"
"v_min_u32 v242,11,v242\n"
"v_mul_u32_u24 v242,160,v242\n"
MF "v[100:103],a[120:123],v[188:191],v[100:103]\n"
"s_waitcnt lgkmcnt(0)\n"
"v_and_or_b32 v242,v9,16,v242\n"
"v_lshlrev_b32 v242,3,v242\n"
MF "v[136:139],a[120:123],v[176:179],v[136:139]\n"
TR "v[188:189],v3\n"
"v_bfe_u32 v224,v9,5,3\n"
MF "v[136:139],a[160:163],v[180:183],v[136:139]\n"
TR "v[190:191],v3 offset:2048\n"
"v_bfe_u32 v225,v9,8,2\n"
MF "v[136:139],a[200:203],v[184:187],v[136:139]\n"
LD "v197,s[48:51],s89 offen offset:1024 lds\n"
MF "v[144:147],a[204:207],v[184:187],v[144:147]\n"
TR "a[240:241],v3 offset:4096\n"
"v_lshlrev_b32 v225,1,v225\n"
MF "v[144:147],a[164:167],v[180:183],v[144:147]\n"
TR "a[242:243],v3 offset:6144\n"
"v_and_b32 v226,16,v9\n"
MF "v[144:147],a[124:127],v[176:179],v[144:147]\n"
"v_lshlrev_b32_e64 v226,v226,s9\n"
"v_sub_u32 v224,v224,v225\n"
MF "v[152:155],a[128:131],v[176:179],v[152:155]\n"
TR "a[244:245],v3 offset:8192\n"
"v_cmp_eq_u32 vcc,0,v224\n"
MF "v[152:155],a[168:171],v[180:183],v[152:155]\n"
TR "a[246:247],v3 offset:10240\n"
"v_cndmask_b32 v220,0,v226,vcc\n"
MF "v[152:155],a[208:211],v[184:187],v[152:155]\n"
"v_cmp_eq_u32 vcc,1,v224\n"
MF "v[160:163],a[212:215],v[184:187],v[160:163]\n"
"s_nop 0\n"
"v_cndmask_b32 v221,0,v226,vcc\n"
AW "a248,0\n"
MF "v[160:163],a[172:175],v[180:183],v[160:163]\n"
AW "a249,0\n"
AW "a250,v220\n"
MF "v[160:163],a[132:135],v[176:179],v[160:163]\n"
AW "a251,v221\n"
AW "a252,v220\n"
MF "v[168:171],a[136:139],v[176:179],v[168:171]\n"
AW "a253,v221\n"
AW "a254,0\n"
MF "v[168:171],a[176:179],v[180:183],v[168:171]\n"
"s_waitcnt vmcnt(17) lgkmcnt(0)\n"
AW "a255,0\n"
"v_bfe_u32 v12,v9,4,4\n"
MF "v[168:171],a[216:219],v[184:187],v[168:171]\n"
"s_barrier\n"
MF "v[172:175],a[216:219],a[244:247],v[172:175]\n"
"s_add_u32 s80,s89,122880\n"
TR "v[176:177],v0 offset:49152\n"
"v_lshlrev_b32 v12,2,v12\n"
MF "v[172:175],a[176:179],a[240:243],v[172:175]\n"
"s_add_u32 m0,s5,0\n"
TR "v[178:179],v0 offset:51200\n"
MF "v[172:175],a[136:139],v[188:191],v[172:175]\n"
LD "v192,s[48:51],s80 offen lds\n"
MF "v[164:167],a[132:135],v[188:191],v[164:167]\n"
"s_add_u32 s60,s64,1\n"
TR "v[180:181],v0 offset:53248\n"
MF "v[164:167],a[172:175],a[240:243],v[164:167]\n"
"s_cmp_eq_u32 s60,8\n"
"s_cselect_b32 s60,0,s60\n"
TR "v[182:183],v0 offset:55296\n"
MF "v[164:167],a[212:215],a[244:247],v[164:167]\n"
LD "v193,s[48:51],s80 offen offset:1024 lds\n"
MF "v[156:159],a[208:211],a[244:247],v[156:159]\n"
"s_cmp_eq_u32 s60,10\n"
"s_cselect_b32 s60,s61,s60\n"
TR "v[184:185],v0 offset:57344\n"
MF "v[156:159],a[168:171],a[240:243],v[156:159]\n"
"s_sub_u32 s65,s60,s64\n"
TR "v[186:187],v0 offset:59392\n"
MF "v[156:159],a[128:131],v[188:191],v[156:159]\n"
"s_lshl_b32 s65,s65,7\n"
MF "v[148:151],a[124:127],v[188:191],v[148:151]\n"
"s_mov_b32 s64,s60\n"
MF "v[148:151],a[164:167],a[240:243],v[148:151]\n"
MF "v[148:151],a[204:207],a[244:247],v[148:151]\n"
LD "v194,s[48:51],s80 offen offset:2048 lds\n"
MF "v[140:143],a[200:203],a[244:247],v[140:143]\n"
MF "v[140:143],a[160:163],a[240:243],v[140:143]\n"
MF "v[140:143],a[120:123],v[188:191],v[140:143]\n"
"s_waitcnt lgkmcnt(0)\n"
MF "v[96:99],a[20:23],v[176:179],v[96:99]\n"
TR "v[188:189],v1 offset:49152\n"
MF "v[96:99],a[60:63],v[180:183],v[96:99]\n"
TR "v[190:191],v1 offset:51200\n"
MF "v[96:99],a[100:103],v[184:187],v[96:99]\n"
LD "v195,s[48:51],s80 offen offset:3072 lds\n"
MF "v[104:107],a[104:107],v[184:187],v[104:107]\n"
TR "a[240:241],v1 offset:53248\n"
MF "v[104:107],a[64:67],v[180:183],v[104:107]\n"
"s_add_u32 m0,s5,4096\n"
TR "a[242:243],v1 offset:55296\n"
MF "v[104:107],a[24:27],v[176:179],v[104:107]\n"
LD "v196,s[48:51],s80 offen lds\n"
MF "v[112:115],a[28:31],v[176:179],v[112:115]\n"
TR "a[244:245],v1 offset:57344\n"
MF "v[112:115],a[68:71],v[180:183],v[112:115]\n"
TR "a[246:247],v1 offset:59392\n"
MF "v[112:115],a[108:111],v[184:187],v[112:115]\n"
MF "v[120:123],a[112:115],v[184:187],v[120:123]\n"
MF "v[120:123],a[72:75],v[180:183],v[120:123]\n"
MF "v[120:123],a[32:35],v[176:179],v[120:123]\n"
LD "v197,s[48:51],s80 offen offset:1024 lds\n"
MF "v[128:131],a[36:39],v[176:179],v[128:131]\n"
MF "v[128:131],a[76:79],v[180:183],v[128:131]\n"
MF "v[128:131],a[116:119],v[184:187],v[128:131]\n"
"s_waitcnt lgkmcnt(0)\n"
MF "v[132:135],a[116:119],a[244:247],v[132:135]\n"
TR "v[176:177],v2 offset:49152\n"
MF "v[132:135],a[76:79],a[240:243],v[132:135]\n"
TR "v[178:179],v2 offset:51200\n"
MF "v[132:135],a[36:39],v[188:191],v[132:135]\n"
MF "v[124:127],a[32:35],v[188:191],v[124:127]\n"
TR "v[180:181],v2 offset:53248\n"
MF "v[124:127],a[72:75],a[240:243],v[124:127]\n"
"s_add_u32 s62,s74,s68\n"
TR "v[182:183],v2 offset:55296\n"
MF "v[124:127],a[112:115],a[244:247],v[124:127]\n"
"buffer_load_dword v243,v242,s[48:51],s62 offen\n"
MF "v[116:119],a[108:111],a[244:247],v[116:119]\n"
TR "v[184:185],v2 offset:57344\n"
MF "v[116:119],a[68:71],a[240:243],v[116:119]\n"
TR "v[186:187],v2 offset:59392\n"
MF "v[116:119],a[28:31],v[188:191],v[116:119]\n"
MF "v[108:111],a[24:27],v[188:191],v[108:111]\n"
MF "v[108:111],a[64:67],a[240:243],v[108:111]\n"
MF "v[108:111],a[104:107],a[244:247],v[108:111]\n"
MF "v[100:103],a[100:103],a[244:247],v[100:103]\n"
MF "v[100:103],a[60:63],a[240:243],v[100:103]\n"
MF "v[100:103],a[20:23],v[188:191],v[100:103]\n"
"s_waitcnt lgkmcnt(0)\n"
MF "v[136:139],a[20:23],v[176:179],v[136:139]\n"
TR "v[188:189],v3 offset:49152\n"
MF "v[136:139],a[60:63],v[180:183],v[136:139]\n"
TR "v[190:191],v3 offset:51200\n"
MF "v[136:139],a[100:103],v[184:187],v[136:139]\n"
"v_mov_b32 v14,s76\n"
"s_add_u32 s76,s76,1\n"
MF "v[144:147],a[104:107],v[184:187],v[144:147]\n"
TR "a[240:241],v3 offset:53248\n"
MF "v[144:147],a[64:67],v[180:183],v[144:147]\n"
TR "a[242:243],v3 offset:55296\n"
MF "v[144:147],a[24:27],v[176:179],v[144:147]\n"
MF "v[152:155],a[28:31],v[176:179],v[152:155]\n"
TR "a[244:245],v3 offset:57344\n"
MF "v[152:155],a[68:71],v[180:183],v[152:155]\n"
TR "a[246:247],v3 offset:59392\n"
MF "v[152:155],a[108:111],v[184:187],v[152:155]\n"
MF "v[160:163],a[112:115],v[184:187],v[160:163]\n"
MF "v[160:163],a[72:75],v[180:183],v[160:163]\n"
MF "v[160:163],a[32:35],v[176:179],v[160:163]\n"
MF "v[168:171],a[36:39],v[176:179],v[168:171]\n"
MF "v[168:171],a[76:79],v[180:183],v[168:171]\n"
"s_waitcnt vmcnt(9) lgkmcnt(0)\n"
MF "v[168:171],a[116:119],v[184:187],v[168:171]\n"
"s_barrier\n"
MF "v[172:175],a[116:119],a[244:247],v[172:175]\n"
TR "v[176:177],v4\n"
MF "v[172:175],a[76:79],a[240:243],v[172:175]\n"
TR "v[178:179],v4 offset:2048\n"
MF "v[172:175],a[36:39],v[188:191],v[172:175]\n"
"buffer_store_dword v14,v15,s[40:43],s94 offen sc1\n"
MF "v[164:167],a[32:35],v[188:191],v[164:167]\n"
TR "v[180:181],v4 offset:4096\n"
MF "v[164:167],a[72:75],a[240:243],v[164:167]\n"
TR "v[182:183],v4 offset:6144\n"
MF "v[164:167],a[112:115],a[244:247],v[164:167]\n"
MF "v[156:159],a[108:111],a[244:247],v[156:159]\n"
TR "v[184:185],v4 offset:8192\n"
MF "v[156:159],a[68:71],a[240:243],v[156:159]\n"
TR "v[186:187],v4 offset:10240\n"
MF "v[156:159],a[28:31],v[188:191],v[156:159]\n"
MF "v[148:151],a[24:27],v[188:191],v[148:151]\n"
MF "v[148:151],a[64:67],a[240:243],v[148:151]\n"
"s_add_u32 m0,s5,73728\n"
MF "v[148:151],a[104:107],a[244:247],v[148:151]\n"
MF "v[140:143],a[100:103],a[244:247],v[140:143]\n"
MF "v[140:143],a[60:63],a[240:243],v[140:143]\n"
MF "v[140:143],a[20:23],v[188:191],v[140:143]\n"
"s_waitcnt lgkmcnt(0)\n"
MF "v[96:99],a[140:143],v[176:179],v[96:99]\n"
"s_nop 0\n"
TR "v[188:189],v5\n"
TR "v[190:191],v5 offset:2048\n"
MF "v[96:99],a[180:183],v[180:183],v[96:99]\n"
"s_nop 0\n"
TR "a[240:241],v5 offset:4096\n"
TR "a[242:243],v5 offset:6144\n"
MF "v[96:99],a[220:223],v[184:187],v[96:99]\n"
"s_nop 0\n"
TR "a[244:245],v5 offset:8192\n"
TR "a[246:247],v5 offset:10240\n"
MF "v[104:107],a[224:227],v[184:187],v[104:107]\n"
MF "v[104:107],a[184:187],v[180:183],v[104:107]\n"
MF "v[104:107],a[144:147],v[176:179],v[104:107]\n"
"s_nop 0\n"
"s_nop 0\n"
"ds_write_addtid_b32 v96\n"
"ds_write_addtid_b32 v97 offset:256\n"
MF "v[112:115],a[148:151],v[176:179],v[112:115]\n"
"s_nop 0\n"
"ds_write_addtid_b32 v98 offset:512\n"
"ds_write_addtid_b32 v99 offset:768\n"
MF "v[112:115],a[188:191],v[180:183],v[112:115]\n"
MF "v[112:115],a[228:231],v[184:187],v[112:115]\n"
"s_nop 0\n"
"ds_write_addtid_b32 v104 offset:2048\n"
"ds_write_addtid_b32 v105 offset:2304\n"
MF "v[120:123],a[232:235],v[184:187],v[120:123]\n"
"s_nop 0\n"
"ds_write_addtid_b32 v106 offset:2560\n"
"ds_write_addtid_b32 v107 offset:2816\n"
MF "v[120:123],a[192:195],v[180:183],v[120:123]\n"
MF "v[120:123],a[152:155],v[176:179],v[120:123]\n"
"s_nop 0\n"
"ds_write_addtid_b32 v112 offset:4096\n"
"ds_write_addtid_b32 v113 offset:4352\n"
MF "v[128:131],a[156:159],v[176:179],v[128:131]\n"
"s_nop 0\n"
"ds_write_addtid_b32 v114 offset:4608\n"
"ds_write_addtid_b32 v115 offset:4864\n"
MF "v[128:131],a[196:199],v[180:183],v[128:131]\n"
MF "v[128:131],a[236:239],v[184:187],v[128:131]\n"
"s_waitcnt lgkmcnt(12)\n"
"ds_write_addtid_b32 v120 offset:50176\n"
"ds_write_addtid_b32 v121 offset:50432\n"
MF "v[132:135],a[236:239],a[244:247],v[132:135]\n"
"s_nop 0\n"
TR "v[176:177],v6\n"
TR "v[178:179],v6 offset:2048\n"
MF "v[132:135],a[196:199],a[240:243],v[132:135]\n"
"s_nop 0\n"
TR "v[180:181],v6 offset:4096\n"
TR "v[182:183],v6 offset:6144\n"
MF "v[132:135],a[156:159],v[188:191],v[132:135]\n"
"s_nop 0\n"
TR "v[184:185],v6 offset:8192\n"
TR "v[186:187],v6 offset:10240\n"
MF "v[124:127],a[152:155],v[188:191],v[124:127]\n"
"s_nop 0\n"
"ds_write_addtid_b32 v122 offset:50688\n"
"ds_write_addtid_b32 v123 offset:50944\n"
MF "v[124:127],a[192:195],a[240:243],v[124:127]\n"
"s_nop 0\n"
"ds_write_addtid_b32 v128 offset:52224\n"
"ds_write_addtid_b32 v129 offset:52480\n"
MF "v[124:127],a[232:235],a[244:247],v[124:127]\n"
"s_nop 0\n"
"ds_write_addtid_b32 v130 offset:52736\n"
"ds_write_addtid_b32 v131 offset:52992\n"
MF "v[116:119],a[228:231],a[244:247],v[116:119]\n"
"s_nop 0\n"
"ds_write_addtid_b32 v132 offset:53248\n"
"ds_write_addtid_b32 v133 offset:53504\n"
MF "v[116:119],a[188:191],a[240:243],v[116:119]\n"
"s_nop 0\n"
"ds_write_addtid_b32 v134 offset:53760\n"
"ds_write_addtid_b32 v135 offset:54016\n"
MF "v[116:119],a[148:151],v[188:191],v[116:119]\n"
"s_nop 0\n"
"ds_write_addtid_b32 v124 offset:51200\n"
"ds_write_addtid_b32 v125 offset:51456\n"
MF "v[108:111],a[144:147],v[188:191],v[108:111]\n"
"s_nop 0\n"
"ds_write_addtid_b32 v126 offset:51712\n"
"ds_write_addtid_b32 v127 offset:51968\n"
MF "v[108:111],a[184:187],a[240:243],v[108:111]\n"
MF "v[108:111],a[224:227],a[244:247],v[108:111]\n"
"s_nop 0\n"
"ds_write_addtid_b32 v116 offset:49152\n"
"ds_write_addtid_b32 v117 offset:49408\n"
MF "v[100:103],a[220:223],a[244:247],v[100:103]\n"
"s_nop 0\n"
"ds_write_addtid_b32 v118 offset:49664\n"
"ds_write_addtid_b32 v119 offset:49920\n"
MF "v[100:103],a[180:183],a[240:243],v[100:103]\n"
MF "v[100:103],a[140:143],v[188:191],v[100:103]\n"
"s_waitcnt lgkmcnt(15)\n"
"ds_write_addtid_b32 v108 offset:3072\n"
"ds_write_addtid_b32 v109 offset:3328\n"
MF "v[136:139],a[140:143],v[176:179],v[136:139]\n"
"s_nop 0\n"
TR "v[188:189],v7\n"
TR "v[190:191],v7 offset:2048\n"
MF "v[136:139],a[180:183],v[180:183],v[136:139]\n"
"s_nop 0\n"
TR "a[240:241],v7 offset:4096\n"
TR "a[242:243],v7 offset:6144\n"
MF "v[136:139],a[220:223],v[184:187],v[136:139]\n"
"s_nop 0\n"
TR "a[244:245],v7 offset:8192\n"
TR "a[246:247],v7 offset:10240\n"
MF "v[144:147],a[224:227],v[184:187],v[144:147]\n"
"s_nop 0\n"
"ds_write_addtid_b32 v110 offset:3584\n"
"ds_write_addtid_b32 v111 offset:3840\n"
MF "v[144:147],a[184:187],v[180:183],v[144:147]\n"
"s_nop 0\n"
"ds_write_addtid_b32 v100 offset:1024\n"
"ds_write_addtid_b32 v101 offset:1280\n"
MF "v[144:147],a[144:147],v[176:179],v[144:147]\n"
"s_nop 0\n"
"ds_write_addtid_b32 v102 offset:1536\n"
"ds_write_addtid_b32 v103 offset:1792\n"
MF "v[152:155],a[148:151],v[176:179],v[152:155]\n"
"s_add_u32 m0,s5,49152\n"
MF "v[152:155],a[188:191],v[180:183],v[152:155]\n"
LD "v192,s[48:51],s88 offen lds\n"
MF "v[152:155],a[228:231],v[184:187],v[152:155]\n"
MF "v[160:163],a[232:235],v[184:187],v[160:163]\n"
MF "v[160:163],a[192:195],v[180:183],v[160:163]\n"
MF "v[160:163],a[152:155],v[176:179],v[160:163]\n"
MF "v[168:171],a[156:159],v[176:179],v[168:171]\n"
LD "v193,s[48:51],s88 offen offset:1024 lds\n"
MF "v[168:171],a[196:199],v[180:183],v[168:171]\n"
"s_waitcnt vmcnt(10) lgkmcnt(0)\n"
MF "v[168:171],a[236:239],v[184:187],v[168:171]\n"
"s_barrier\n"
MF "v[172:175],a[236:239],a[244:247],v[172:175]\n"
"s_nop 0\n"
TR "v[176:177],v0 offset:24576\n"
TR "v[178:179],v0 offset:26624\n"
MF "v[172:175],a[196:199],a[240:243],v[172:175]\n"
"s_add_u32 s85,s85,1\n"
TR "v[180:181],v0 offset:28672\n"
TR "v[182:183],v0 offset:30720\n"
MF "v[172:175],a[156:159],v[188:191],v[172:175]\n"
"s_nop 0\n"
TR "v[184:185],v0 offset:32768\n"
TR "v[186:187],v0 offset:34816\n"
MF "v[164:167],a[152:155],v[188:191],v[164:167]\n"
LD "v194,s[48:51],s88 offen offset:2048 lds\n"
MF "v[164:167],a[192:195],a[240:243],v[164:167]\n"
"ds_read2st64_b32 v[128:129],v13 offset0:204 offset1:205\n"
MF "v[164:167],a[232:235],a[244:247],v[164:167]\n"
"ds_read2st64_b32 v[130:131],v13 offset0:206 offset1:207\n"
MF "v[156:159],a[228:231],a[244:247],v[156:159]\n"
"ds_read2st64_b32 v[132:133],v13 offset0:208 offset1:209\n"
MF "v[156:159],a[188:191],a[240:243],v[156:159]\n"
LD "v195,s[48:51],s88 offen offset:3072 lds\n"
MF "v[156:159],a[148:151],v[188:191],v[156:159]\n"
"ds_read2st64_b32 v[134:135],v13 offset0:210 offset1:211\n"
MF "v[148:151],a[144:147],v[188:191],v[148:151]\n"
"ds_read2st64_b32 v[120:121],v13 offset0:196 offset1:197\n"
MF "v[148:151],a[184:187],a[240:243],v[148:151]\n"
"ds_read2st64_b32 v[122:123],v13 offset0:198 offset1:199\n"
MF "v[148:151],a[224:227],a[244:247],v[148:151]\n"
MF "v[140:143],a[220:223],a[244:247],v[140:143]\n"
MF "v[140:143],a[180:183],a[240:243],v[140:143]\n"
MF "v[140:143],a[140:143],v[188:191],v[140:143]\n"
"s_waitcnt lgkmcnt(6)\n"
MF "v[16:19],a[0:3],v[176:179],v[252:255]\n"
"s_nop 0\n"
TR "v[188:189],v1 offset:24576\n"
TR "v[190:191],v1 offset:26624\n"
MF "v[16:19],a[40:43],v[180:183],v[16:19]\n"
"s_nop 0\n"
TR "a[240:241],v1 offset:28672\n"
TR "a[242:243],v1 offset:30720\n"
MF "v[16:19],a[80:83],v[184:187],v[16:19]\n"
"s_add_u32 m0,s5,53248\n"
TR "a[244:245],v1 offset:32768\n"
TR "a[246:247],v1 offset:34816\n"
MF "v[24:27],a[84:87],v[184:187],v[252:255]\n"
"s_waitcnt lgkmcnt(8)\n"
LD "v196,s[48:51],s88 offen lds\n"
MF "v[24:27],a[44:47],v[180:183],v[24:27]\n"
"ds_read2st64_b32 v[124:125],v13 offset0:200 offset1:201\n"
"v_add_f32 v168,v168,v128\n"
MF "v[24:27],a[4:7],v[176:179],v[24:27]\n"
"ds_read2st64_b32 v[126:127],v13 offset0:202 offset1:203\n"
"v_add_f32 v169,v169,v129\n"
MF "v[32:35],a[8:11],v[176:179],v[252:255]\n"
"ds_read2st64_b32 v[112:113],v13 offset0:16 offset1:17\n"
"v_add_f32 v170,v170,v130\n"
MF "v[32:35],a[48:51],v[180:183],v[32:35]\n"
LD "v197,s[48:51],s88 offen offset:1024 lds\n"
MF "v[32:35],a[88:91],v[184:187],v[32:35]\n"
"ds_read2st64_b32 v[114:115],v13 offset0:18 offset1:19\n"
"v_add_f32 v171,v171,v131\n"
MF "v[40:43],a[92:95],v[184:187],v[252:255]\n"
"ds_read2st64_b32 v[116:117],v13 offset0:192 offset1:193\n"
"v_add_f32 v172,v172,v132\n"
MF "v[40:43],a[52:55],v[180:183],v[40:43]\n"
"s_add_u32 s80,s88,122880\n"
"ds_read2st64_b32 v[118:119],v13 offset0:194 offset1:195\n"
"v_add_f32 v173,v173,v133\n"
MF "v[40:43],a[12:15],v[176:179],v[40:43]\n"
"s_add_u32 m0,s5,98304\n"
"ds_read2st64_b32 v[104:105],v13 offset0:8 offset1:9\n"
"v_add_f32 v174,v174,v134\n"
MF "v[48:51],a[16:19],v[176:179],v[252:255]\n"
LD "v192,s[48:51],s80 offen lds\n"
MF "v[48:51],a[56:59],v[180:183],v[48:51]\n"
"ds_read2st64_b32 v[106:107],v13 offset0:10 offset1:11\n"
"v_add_f32 v175,v175,v135\n"
MF "v[48:51],a[96:99],v[184:187],v[48:51]\n"
"s_waitcnt lgkmcnt(8)\n"
LD "v193,s[48:51],s80 offen offset:1024 lds\n"
MF "v[52:55],a[96:99],a[244:247],v[252:255]\n"
"s_nop 0\n"
TR "v[176:177],v2 offset:24576\n"
TR "v[178:179],v2 offset:26624\n"
MF "v[52:55],a[56:59],a[240:243],v[52:55]\n"
"s_nop 0\n"
TR "v[180:181],v2 offset:28672\n"
TR "v[182:183],v2 offset:30720\n"
MF "v[52:55],a[16:19],v[188:191],v[52:55]\n"
"s_nop 0\n"
TR "v[184:185],v2 offset:32768\n"
TR "v[186:187],v2 offset:34816\n"
MF "v[44:47],a[12:15],v[188:191],v[252:255]\n"
"ds_read2st64_b32 v[108:109],v13 offset0:12 offset1:13\n"
CV "v168,v168,v169\n"
MF "v[44:47],a[52:55],a[240:243],v[44:47]\n"
"ds_read2st64_b32 v[110:111],v13 offset0:14 offset1:15\n"
CV "v169,v170,v171\n"
MF "v[44:47],a[92:95],a[244:247],v[44:47]\n"
"ds_read2st64_b32 v[96:97],v13 offset0:0 offset1:1\n"
CV "v170,v172,v173\n"
MF "v[36:39],a[88:91],a[244:247],v[252:255]\n"
LD "v194,s[48:51],s80 offen offset:2048 lds\n"
MF "v[36:39],a[48:51],a[240:243],v[36:39]\n"
"s_waitcnt lgkmcnt(15)\n"
"ds_read2st64_b32 v[98:99],v13 offset0:2 offset1:3\n"
CV "v171,v174,v175\n"
MF "v[36:39],a[8:11],v[188:191],v[36:39]\n"
"ds_read2st64_b32 v[100:101],v13 offset0:4 offset1:5\n"
"v_add_f32 v160,v160,v120\n"
MF "v[28:31],a[4:7],v[188:191],v[252:255]\n"
"s_add_u32 s87,s90,4096\n"
"ds_read2st64_b32 v[102:103],v13 offset0:6 offset1:7\n"
"v_add_f32 v161,v161,v121\n"
MF "v[28:31],a[44:47],a[240:243],v[28:31]\n"
"buffer_store_dwordx4 v[168:171],v9,s[40:43],s87 offen sc1\n"
MF "v[28:31],a[84:87],a[244:247],v[28:31]\n"
LD "v195,s[48:51],s80 offen offset:3072 lds\n"
MF "v[20:23],a[80:83],a[244:247],v[252:255]\n"
"v_add_f32 v162,v162,v122\n"
"v_add_f32 v163,v163,v123\n"
MF "v[20:23],a[40:43],a[240:243],v[20:23]\n"
"s_add_u32 m0,s5,102400\n"
"v_add_f32 v164,v164,v124\n"
"v_add_f32 v165,v165,v125\n"
MF "v[20:23],a[0:3],v[188:191],v[20:23]\n"
"s_waitcnt lgkmcnt(6)\n"
LD "v196,s[48:51],s80 offen lds\n"
MF "v[56:59],a[0:3],v[176:179],v[252:255]\n"
"s_nop 0\n"
TR "v[188:189],v3 offset:24576\n"
TR "v[190:191],v3 offset:26624\n"
MF "v[56:59],a[40:43],v[180:183],v[56:59]\n"
"s_nop 0\n"
TR "a[240:241],v3 offset:28672\n"
TR "a[242:243],v3 offset:30720\n"
MF "v[56:59],a[80:83],v[184:187],v[56:59]\n"
"s_nop 0\n"
TR "a[244:245],v3 offset:32768\n"
TR "a[246:247],v3 offset:34816\n"
MF "v[64:67],a[84:87],v[184:187],v[252:255]\n"
"v_add_f32 v166,v166,v126\n"
"v_add_f32 v167,v167,v127\n"
MF "v[64:67],a[44:47],v[180:183],v[64:67]\n"
CV "v160,v160,v161\n"
CV "v161,v162,v163\n"
MF "v[64:67],a[4:7],v[176:179],v[64:67]\n"
LD "v197,s[48:51],s80 offen offset:1024 lds\n"
MF "v[72:75],a[8:11],v[176:179],v[252:255]\n"
CV "v162,v164,v165\n"
CV "v163,v166,v167\n"
MF "v[72:75],a[48:51],v[180:183],v[72:75]\n"
"buffer_store_dwordx4 v[160:163],v9,s[40:43],s90 offen offset:3072 sc1\n"
MF "v[72:75],a[88:91],v[184:187],v[72:75]\n"
"v_add_f32 v152,v152,v112\n"
"v_add_f32 v153,v153,v113\n"
MF "v[80:83],a[92:95],v[184:187],v[252:255]\n"
"v_add_f32 v154,v154,v114\n"
"v_add_f32 v155,v155,v115\n"
MF "v[80:83],a[52:55],v[180:183],v[80:83]\n"
"v_add_f32 v156,v156,v116\n"
"v_add_f32 v157,v157,v117\n"
MF "v[80:83],a[12:15],v[176:179],v[80:83]\n"
"v_add_f32 v158,v158,v118\n"
"v_add_f32 v159,v159,v119\n"
MF "v[88:91],a[16:19],v[176:179],v[252:255]\n"
CV "v152,v152,v153\n"
CV "v153,v154,v155\n"
MF "v[88:91],a[56:59],v[180:183],v[88:91]\n"
"s_waitcnt vmcnt(16) lgkmcnt(0)\n"
CV "v154,v156,v157\n"
CV "v155,v158,v159\n"
MF "v[88:91],a[96:99],v[184:187],v[88:91]\n"
"s_barrier\n"
MF "v[92:95],a[96:99],a[244:247],v[252:255]\n"
TR "v[176:177],v0\n"
MF "v[92:95],a[56:59],a[240:243],v[92:95]\n"
"s_add_u32 s89,s89,s65\n"
TR "v[178:179],v0 offset:2048\n"
MF "v[92:95],a[16:19],v[188:191],v[92:95]\n"
"buffer_store_dwordx4 v[152:155],v9,s[40:43],s90 offen offset:2048 sc1\n"
MF "v[84:87],a[12:15],v[188:191],v[252:255]\n"
"s_add_u32 s88,s88,s65\n"
TR "v[180:181],v0 offset:4096\n"
MF "v[84:87],a[52:55],a[240:243],v[84:87]\n"
"s_add_u32 m0,s5,24576\n"
TR "v[182:183],v0 offset:6144\n"
MF "v[84:87],a[92:95],a[244:247],v[84:87]\n"
LD "v192,s[48:51],s89 offen lds\n"
MF "v[76:79],a[88:91],a[244:247],v[252:255]\n"
TR "v[184:185],v0 offset:8192\n"
MF "v[76:79],a[48:51],a[240:243],v[76:79]\n"
TR "v[186:187],v0 offset:10240\n"
MF "v[76:79],a[8:11],v[188:191],v[76:79]\n"
LD "v193,s[48:51],s89 offen offset:1024 lds\n"
MF "v[68:71],a[4:7],v[188:191],v[252:255]\n"
"v_add_f32 v144,v144,v104\n"
"v_add_f32 v145,v145,v105\n"
MF "v[68:71],a[44:47],a[240:243],v[68:71]\n"
"v_add_f32 v146,v146,v106\n"
"v_add_f32 v147,v147,v107\n"
MF "v[68:71],a[84:87],a[244:247],v[68:71]\n"
"v_add_f32 v148,v148,v108\n"
"v_add_f32 v149,v149,v109\n"
MF "v[60:63],a[80:83],a[244:247],v[252:255]\n"
"v_add_f32 v150,v150,v110\n"
"v_add_f32 v151,v151,v111\n"
MF "v[60:63],a[40:43],a[240:243],v[60:63]\n"
CV "v144,v144,v145\n"
CV "v145,v146,v147\n"
MF "v[60:63],a[0:3],v[188:191],v[60:63]\n"
"s_waitcnt lgkmcnt(0)\n"
CV "v146,v148,v149\n"
CV "v147,v150,v151\n"
MF "v[16:19],a[120:123],v[176:179],v[16:19]\n"
TR "v[188:189],v1\n"
MF "v[16:19],a[160:163],v[180:183],v[16:19]\n"
TR "v[190:191],v1 offset:2048\n"
MF "v[16:19],a[200:203],v[184:187],v[16:19]\n"
"buffer_store_dwordx4 v[144:147],v9,s[40:43],s90 offen offset:1024 sc1\n"
MF "v[24:27],a[204:207],v[184:187],v[24:27]\n"
TR "a[240:241],v1 offset:4096\n"
MF "v[24:27],a[164:167],v[180:183],v[24:27]\n"
TR "a[242:243],v1 offset:6144\n"
MF "v[24:27],a[124:127],v[176:179],v[24:27]\n"
LD "v194,s[48:51],s89 offen offset:2048 lds\n"
MF "v[32:35],a[128:131],v[176:179],v[32:35]\n"
TR "a[244:245],v1 offset:8192\n"
MF "v[32:35],a[168:171],v[180:183],v[32:35]\n"
TR "a[246:247],v1 offset:10240\n"
MF "v[32:35],a[208:211],v[184:187],v[32:35]\n"
MF "v[40:43],a[212:215],v[184:187],v[40:43]\n"
MF "v[40:43],a[172:175],v[180:183],v[40:43]\n"
MF "v[40:43],a[132:135],v[176:179],v[40:43]\n"
LD "v195,s[48:51],s89 offen offset:3072 lds\n"
MF "v[48:51],a[136:139],v[176:179],v[48:51]\n"
"v_add_f32 v136,v136,v96\n"
"v_add_f32 v137,v137,v97\n"
MF "v[48:51],a[176:179],v[180:183],v[48:51]\n"
".Lpg27h:\n"
"s_cbranch_execz .Lpg31h\n"
"v_add_f32 v138,v138,v98\n"
"v_add_f32 v139,v139,v99\n"
MF "v[48:51],a[216:219],v[184:187],v[48:51]\n"
"s_waitcnt lgkmcnt(0)\n"
"v_add_f32 v140,v140,v100\n"
"v_add_f32 v141,v141,v101\n"
MF "v[52:55],a[216:219],a[244:247],v[52:55]\n"
TR "v[176:177],v2\n"
"v_add_f32 v142,v142,v102\n"
MF "v[52:55],a[176:179],a[240:243],v[52:55]\n"
TR "v[178:179],v2 offset:2048\n"
"v_add_f32 v143,v143,v103\n"
MF "v[52:55],a[136:139],v[188:191],v[52:55]\n"
CV "v136,v136,v137\n"
CV "v137,v138,v139\n"
MF "v[44:47],a[132:135],v[188:191],v[44:47]\n"
TR "v[180:181],v2 offset:4096\n"
CV "v138,v140,v141\n"
MF "v[44:47],a[172:175],a[240:243],v[44:47]\n"
TR "v[182:183],v2 offset:6144\n"
CV "v139,v142,v143\n"
MF "v[44:47],a[212:215],a[244:247],v[44:47]\n"
"buffer_store_dwordx4 v[136:139],v9,s[40:43],s90 offen sc1\n"
MF "v[36:39],a[208:211],a[244:247],v[36:39]\n"
"s_add_u32 s90,s90,20480\n"
TR "v[184:185],v2 offset:8192\n"
MF "v[36:39],a[168:171],a[240:243],v[36:39]\n"
"s_add_u32 m0,s5,28672\n"
TR "v[186:187],v2 offset:10240\n"
MF "v[36:39],a[128:131],v[188:191],v[36:39]\n"
LD "v196,s[48:51],s89 offen lds\n"
MF "v[28:31],a[124:127],v[188:191],v[28:31]\n"
MF "v[28:31],a[164:167],a[240:243],v[28:31]\n"
MF "v[28:31],a[204:207],a[244:247],v[28:31]\n"
MF "v[20:23],a[200:203],a[244:247],v[20:23]\n"
MF "v[20:23],a[160:163],a[240:243],v[20:23]\n"
MF "v[20:23],a[120:123],v[188:191],v[20:23]\n"
"s_waitcnt lgkmcnt(0)\n"
MF "v[56:59],a[120:123],v[176:179],v[56:59]\n"
TR "v[188:189],v3\n"
MF "v[56:59],a[160:163],v[180:183],v[56:59]\n"
TR "v[190:191],v3 offset:2048\n"
MF "v[56:59],a[200:203],v[184:187],v[56:59]\n"
LD "v197,s[48:51],s89 offen offset:1024 lds\n"
MF "v[64:67],a[204:207],v[184:187],v[64:67]\n"
TR "a[240:241],v3 offset:4096\n"
MF "v[64:67],a[164:167],v[180:183],v[64:67]\n"
TR "a[242:243],v3 offset:6144\n"
MF "v[64:67],a[124:127],v[176:179],v[64:67]\n"
MF "v[72:75],a[128:131],v[176:179],v[72:75]\n"
TR "a[244:245],v3 offset:8192\n"
MF "v[72:75],a[168:171],v[180:183],v[72:75]\n"
TR "a[246:247],v3 offset:10240\n"
MF "v[72:75],a[208:211],v[184:187],v[72:75]\n"
MF "v[80:83],a[212:215],v[184:187],v[80:83]\n"
MF "v[80:83],a[172:175],v[180:183],v[80:83]\n"
MF "v[80:83],a[132:135],v[176:179],v[80:83]\n"
MF "v[88:91],a[136:139],v[176:179],v[88:91]\n"
MF "v[88:91],a[176:179],v[180:183],v[88:91]\n"
"s_waitcnt vmcnt(17) lgkmcnt(0)\n"
MF "v[88:91],a[216:219],v[184:187],v[88:91]\n"
"s_barrier\n"
MF "v[92:95],a[216:219],a[244:247],v[92:95]\n"
"s_add_u32 s80,s89,122880\n"
TR "v[176:177],v0 offset:49152\n"
MF "v[92:95],a[176:179],a[240:243],v[92:95]\n"
"s_add_u32 m0,s5,0\n"
TR "v[178:179],v0 offset:51200\n"
MF "v[92:95],a[136:139],v[188:191],v[92:95]\n"
LD "v192,s[48:51],s80 offen lds\n"
MF "v[84:87],a[132:135],v[188:191],v[84:87]\n"
"s_add_u32 s60,s64,1\n"
TR "v[180:181],v0 offset:53248\n"
MF "v[84:87],a[172:175],a[240:243],v[84:87]\n"
"s_cmp_eq_u32 s60,8\n"
"s_cselect_b32 s60,0,s60\n"
TR "v[182:183],v0 offset:55296\n"
MF "v[84:87],a[212:215],a[244:247],v[84:87]\n"
LD "v193,s[48:51],s80 offen offset:1024 lds\n"
MF "v[76:79],a[208:211],a[244:247],v[76:79]\n"
"s_cmp_eq_u32 s60,10\n"
"s_cselect_b32 s60,s61,s60\n"
TR "v[184:185],v0 offset:57344\n"
MF "v[76:79],a[168:171],a[240:243],v[76:79]\n"
"s_sub_u32 s65,s60,s64\n"
TR "v[186:187],v0 offset:59392\n"
"v_mov_b32 v244,0\n"
MF "v[76:79],a[128:131],v[188:191],v[76:79]\n"
"s_lshl_b32 s65,s65,7\n"
"v_mov_b32 v245,0\n"
"v_mov_b32 v246,0\n"
MF "v[68:71],a[124:127],v[188:191],v[68:71]\n"
"s_mov_b32 s64,s60\n"
"v_mov_b32 v247,0\n"
"v_mov_b32 v248,0\n"
MF "v[68:71],a[164:167],a[240:243],v[68:71]\n"
"v_mov_b32 v249,0\n"
"v_mov_b32 v250,0\n"
MF "v[68:71],a[204:207],a[244:247],v[68:71]\n"
LD "v194,s[48:51],s80 offen offset:2048 lds\n"
MF "v[60:63],a[200:203],a[244:247],v[60:63]\n"
"v_mov_b32 v251,0\n"
MF "v[60:63],a[160:163],a[240:243],v[60:63]\n"
MF "v[60:63],a[120:123],v[188:191],v[60:63]\n"
"s_waitcnt lgkmcnt(0)\n"
MF "v[16:19],a[20:23],v[176:179],v[16:19]\n"
TR "v[188:189],v1 offset:49152\n"
MF "v[16:19],a[60:63],v[180:183],v[16:19]\n"
TR "v[190:191],v1 offset:51200\n"
MF "v[16:19],a[100:103],v[184:187],v[16:19]\n"
LD "v195,s[48:51],s80 offen offset:3072 lds\n"
MF "v[24:27],a[104:107],v[184:187],v[24:27]\n"
TR "a[240:241],v1 offset:53248\n"
MF "v[24:27],a[64:67],v[180:183],v[24:27]\n"
"s_add_u32 m0,s5,4096\n"
TR "a[242:243],v1 offset:55296\n"
MF "v[24:27],a[24:27],v[176:179],v[24:27]\n"
LD "v196,s[48:51],s80 offen lds\n"
MF "v[32:35],a[28:31],v[176:179],v[32:35]\n"
TR "a[244:245],v1 offset:57344\n"
MF "v[32:35],a[68:71],v[180:183],v[32:35]\n"
TR "a[246:247],v1 offset:59392\n"
MF "v[32:35],a[108:111],v[184:187],v[32:35]\n"
MF "v[40:43],a[112:115],v[184:187],v[40:43]\n"
MF "v[40:43],a[72:75],v[180:183],v[40:43]\n"
MF "v[40:43],a[32:35],v[176:179],v[40:43]\n"
LD "v197,s[48:51],s80 offen offset:1024 lds\n"
MF "v[48:51],a[36:39],v[176:179],v[48:51]\n"
MF "v[48:51],a[76:79],v[180:183],v[48:51]\n"
MF "v[48:51],a[116:119],v[184:187],v[48:51]\n"
"s_waitcnt lgkmcnt(0)\n"
MF "v[52:55],a[116:119],a[244:247],v[52:55]\n"
TR "v[176:177],v2 offset:49152\n"
MF "v[52:55],a[76:79],a[240:243],v[52:55]\n"
TR "v[178:179],v2 offset:51200\n"
MF "v[52:55],a[36:39],v[188:191],v[52:55]\n"
MF "v[44:47],a[32:35],v[188:191],v[44:47]\n"
TR "v[180:181],v2 offset:53248\n"
MF "v[44:47],a[72:75],a[240:243],v[44:47]\n"
TR "v[182:183],v2 offset:55296\n"
MF "v[44:47],a[112:115],a[244:247],v[44:47]\n"
MF "v[36:39],a[108:111],a[244:247],v[36:39]\n"
TR "v[184:185],v2 offset:57344\n"
MF "v[36:39],a[68:71],a[240:243],v[36:39]\n"
TR "v[186:187],v2 offset:59392\n"
MF "v[36:39],a[28:31],v[188:191],v[36:39]\n"
MF "v[28:31],a[24:27],v[188:191],v[28:31]\n"
MF "v[28:31],a[64:67],a[240:243],v[28:31]\n"
MF "v[28:31],a[104:107],a[244:247],v[28:31]\n"
MF "v[20:23],a[100:103],a[244:247],v[20:23]\n"
MF "v[20:23],a[60:63],a[240:243],v[20:23]\n"
MF "v[20:23],a[20:23],v[188:191],v[20:23]\n"
"s_waitcnt lgkmcnt(0)\n"
MF "v[56:59],a[20:23],v[176:179],v[56:59]\n"
TR "v[188:189],v3 offset:49152\n"
MF "v[56:59],a[60:63],v[180:183],v[56:59]\n"
TR "v[190:191],v3 offset:51200\n"
MF "v[56:59],a[100:103],v[184:187],v[56:59]\n"
"v_mov_b32 v14,s76\n"
"s_add_u32 s76,s76,1\n"
MF "v[64:67],a[104:107],v[184:187],v[64:67]\n"
TR "a[240:241],v3 offset:53248\n"
MF "v[64:67],a[64:67],v[180:183],v[64:67]\n"
TR "a[242:243],v3 offset:55296\n"
MF "v[64:67],a[24:27],v[176:179],v[64:67]\n"
MF "v[72:75],a[28:31],v[176:179],v[72:75]\n"
TR "a[244:245],v3 offset:57344\n"
MF "v[72:75],a[68:71],v[180:183],v[72:75]\n"
TR "a[246:247],v3 offset:59392\n"
MF "v[72:75],a[108:111],v[184:187],v[72:75]\n"
MF "v[80:83],a[112:115],v[184:187],v[80:83]\n"
MF "v[80:83],a[72:75],v[180:183],v[80:83]\n"
MF "v[80:83],a[32:35],v[176:179],v[80:83]\n"
MF "v[88:91],a[36:39],v[176:179],v[88:91]\n"
MF "v[88:91],a[76:79],v[180:183],v[88:91]\n"
"s_waitcnt vmcnt(8) lgkmcnt(0)\n"
MF "v[88:91],a[116:119],v[184:187],v[88:91]\n"
"s_barrier\n"
MF "v[92:95],a[116:119],a[244:247],v[92:95]\n"
TR "v[176:177],v4\n"
MF "v[92:95],a[76:79],a[240:243],v[92:95]\n"
TR "v[178:179],v4 offset:2048\n"
MF "v[92:95],a[36:39],v[188:191],v[92:95]\n"
"buffer_store_dword v14,v15,s[40:43],s94 offen sc1\n"
MF "v[84:87],a[32:35],v[188:191],v[84:87]\n"
TR "v[180:181],v4 offset:4096\n"
MF "v[84:87],a[72:75],a[240:243],v[84:87]\n"
TR "v[182:183],v4 offset:6144\n"
MF "v[84:87],a[112:115],a[244:247],v[84:87]\n"
MF "v[76:79],a[108:111],a[244:247],v[76:79]\n"
TR "v[184:185],v4 offset:8192\n"
MF "v[76:79],a[68:71],a[240:243],v[76:79]\n"
TR "v[186:187],v4 offset:10240\n"
MF "v[76:79],a[28:31],v[188:191],v[76:79]\n"
MF "v[68:71],a[24:27],v[188:191],v[68:71]\n"
MF "v[68:71],a[64:67],a[240:243],v[68:71]\n"
"s_add_u32 m0,s5,73728\n"
MF "v[68:71],a[104:107],a[244:247],v[68:71]\n"
MF "v[60:63],a[100:103],a[244:247],v[60:63]\n"
MF "v[60:63],a[60:63],a[240:243],v[60:63]\n"
MF "v[60:63],a[20:23],v[188:191],v[60:63]\n"
"s_waitcnt lgkmcnt(0)\n"
MF "v[16:19],a[140:143],v[176:179],v[16:19]\n"
"s_nop 0\n"
TR "v[188:189],v5\n"
TR "v[190:191],v5 offset:2048\n"
MF "v[16:19],a[180:183],v[180:183],v[16:19]\n"
"s_nop 0\n"
TR "a[240:241],v5 offset:4096\n"
TR "a[242:243],v5 offset:6144\n"
MF "v[16:19],a[220:223],v[184:187],v[16:19]\n"
"s_nop 0\n"
TR "a[244:245],v5 offset:8192\n"
TR "a[246:247],v5 offset:10240\n"
MF "v[24:27],a[224:227],v[184:187],v[24:27]\n"
MF "v[24:27],a[184:187],v[180:183],v[24:27]\n"
MF "v[24:27],a[144:147],v[176:179],v[24:27]\n"
"s_nop 0\n"
"s_nop 0\n"
"ds_write_addtid_b32 v16\n"
"ds_write_addtid_b32 v17 offset:256\n"
MF "v[32:35],a[148:151],v[176:179],v[32:35]\n"
"s_nop 0\n"
"ds_write_addtid_b32 v18 offset:512\n"
"ds_write_addtid_b32 v19 offset:768\n"
MF "v[32:35],a[188:191],v[180:183],v[32:35]\n"
MF "v[32:35],a[228:231],v[184:187],v[32:35]\n"
"s_nop 0\n"
"ds_write_addtid_b32 v24 offset:2048\n"
"ds_write_addtid_b32 v25 offset:2304\n"
MF "v[40:43],a[232:235],v[184:187],v[40:43]\n"
"s_nop 0\n"
"ds_write_addtid_b32 v26 offset:2560\n"
"ds_write_addtid_b32 v27 offset:2816\n"
MF "v[40:43],a[192:195],v[180:183],v[40:43]\n"
MF "v[40:43],a[152:155],v[176:179],v[40:43]\n"
"s_nop 0\n"
"ds_write_addtid_b32 v32 offset:4096\n"
"ds_write_addtid_b32 v33 offset:4352\n"
MF "v[48:51],a[156:159],v[176:179],v[48:51]\n"
"s_nop 0\n"
"ds_write_addtid_b32 v34 offset:4608\n"
"ds_write_addtid_b32 v35 offset:4864\n"
MF "v[48:51],a[196:199],v[180:183],v[48:51]\n"
MF "v[48:51],a[236:239],v[184:187],v[48:51]\n"
"s_waitcnt lgkmcnt(12)\n"
"ds_write_addtid_b32 v40 offset:50176\n"
"ds_write_addtid_b32 v41 offset:50432\n"
MF "v[52:55],a[236:239],a[244:247],v[52:55]\n"
"s_nop 0\n"
TR "v[176:177],v6\n"
TR "v[178:179],v6 offset:2048\n"
MF "v[52:55],a[196:199],a[240:243],v[52:55]\n"
"s_nop 0\n"
TR "v[180:181],v6 offset:4096\n"
TR "v[182:183],v6 offset:6144\n"
MF "v[52:55],a[156:159],v[188:191],v[52:55]\n"
"s_nop 0\n"
TR "v[184:185],v6 offset:8192\n"
TR "v[186:187],v6 offset:10240\n"
MF "v[44:47],a[152:155],v[188:191],v[44:47]\n"
"s_nop 0\n"
"ds_write_addtid_b32 v42 offset:50688\n"
"ds_write_addtid_b32 v43 offset:50944\n"
MF "v[44:47],a[192:195],a[240:243],v[44:47]\n"
"s_nop 0\n"
"ds_write_addtid_b32 v48 offset:52224\n"
"ds_write_addtid_b32 v49 offset:52480\n"
MF "v[44:47],a[232:235],a[244:247],v[44:47]\n"
"s_nop 0\n"
"ds_write_addtid_b32 v50 offset:52736\n"
"ds_write_addtid_b32 v51 offset:52992\n"
MF "v[36:39],a[228:231],a[244:247],v[36:39]\n"
"s_nop 0\n"
"ds_write_addtid_b32 v52 offset:53248\n"
"ds_write_addtid_b32 v53 offset:53504\n"
MF "v[36:39],a[188:191],a[240:243],v[36:39]\n"
"s_nop 0\n"
"ds_write_addtid_b32 v54 offset:53760\n"
"ds_write_addtid_b32 v55 offset:54016\n"
MF "v[36:39],a[148:151],v[188:191],v[36:39]\n"
"s_nop 0\n"
"ds_write_addtid_b32 v44 offset:51200\n"
"ds_write_addtid_b32 v45 offset:51456\n"
MF "v[28:31],a[144:147],v[188:191],v[28:31]\n"
"s_nop 0\n"
"ds_write_addtid_b32 v46 offset:51712\n"
"ds_write_addtid_b32 v47 offset:51968\n"
MF "v[28:31],a[184:187],a[240:243],v[28:31]\n"
MF "v[28:31],a[224:227],a[244:247],v[28:31]\n"
"s_nop 0\n"
"ds_write_addtid_b32 v36 offset:49152\n"
"ds_write_addtid_b32 v37 offset:49408\n"
MF "v[20:23],a[220:223],a[244:247],v[20:23]\n"
"s_nop 0\n"
"ds_write_addtid_b32 v38 offset:49664\n"
"ds_write_addtid_b32 v39 offset:49920\n"
MF "v[20:23],a[180:183],a[240:243],v[20:23]\n"
MF "v[20:23],a[140:143],v[188:191],v[20:23]\n"
"s_waitcnt lgkmcnt(15)\n"
"ds_write_addtid_b32 v28 offset:3072\n"
"ds_write_addtid_b32 v29 offset:3328\n"
MF "v[56:59],a[140:143],v[176:179],v[56:59]\n"
"s_nop 0\n"
TR "v[188:189],v7\n"
TR "v[190:191],v7 offset:2048\n"
MF "v[56:59],a[180:183],v[180:183],v[56:59]\n"
"s_nop 0\n"
TR "a[240:241],v7 offset:4096\n"
TR "a[242:243],v7 offset:6144\n"
MF "v[56:59],a[220:223],v[184:187],v[56:59]\n"
"s_nop 0\n"
TR "a[244:245],v7 offset:8192\n"
TR "a[246:247],v7 offset:10240\n"
MF "v[64:67],a[224:227],v[184:187],v[64:67]\n"
"s_nop 0\n"
"ds_write_addtid_b32 v30 offset:3584\n"
"ds_write_addtid_b32 v31 offset:3840\n"
MF "v[64:67],a[184:187],v[180:183],v[64:67]\n"
"s_nop 0\n"
"ds_write_addtid_b32 v20 offset:1024\n"
"ds_write_addtid_b32 v21 offset:1280\n"
MF "v[64:67],a[144:147],v[176:179],v[64:67]\n"
"s_nop 0\n"
"ds_write_addtid_b32 v22 offset:1536\n"
"ds_write_addtid_b32 v23 offset:1792\n"
MF "v[72:75],a[148:151],v[176:179],v[72:75]\n"
"s_add_u32 m0,s5,49152\n"
MF "v[72:75],a[188:191],v[180:183],v[72:75]\n"
LD "v192,s[48:51],s88 offen lds\n"
MF "v[72:75],a[228:231],v[184:187],v[72:75]\n"
MF "v[80:83],a[232:235],v[184:187],v[80:83]\n"
MF "v[80:83],a[192:195],v[180:183],v[80:83]\n"
MF "v[80:83],a[152:155],v[176:179],v[80:83]\n"
MF "v[88:91],a[156:159],v[176:179],v[88:91]\n"
LD "v193,s[48:51],s88 offen offset:1024 lds\n"
MF "v[88:91],a[196:199],v[180:183],v[88:91]\n"
"s_waitcnt vmcnt(9) lgkmcnt(0)\n"
MF "v[88:91],a[236:239],v[184:187],v[88:91]\n"
"s_barrier\n"
MF "v[92:95],a[236:239],a[244:247],v[92:95]\n"
"s_nop 0\n"
TR "v[176:177],v0 offset:24576\n"
TR "v[178:179],v0 offset:26624\n"
MF "v[92:95],a[196:199],a[240:243],v[92:95]\n"
"s_add_u32 s85,s85,1\n"
TR "v[180:181],v0 offset:28672\n"
TR "v[182:183],v0 offset:30720\n"
MF "v[92:95],a[156:159],v[188:191],v[92:95]\n"
"s_nop 0\n"
TR "v[184:185],v0 offset:32768\n"
TR "v[186:187],v0 offset:34816\n"
MF "v[84:87],a[152:155],v[188:191],v[84:87]\n"
LD "v194,s[48:51],s88 offen offset:2048 lds\n"
MF "v[84:87],a[192:195],a[240:243],v[84:87]\n"
"ds_read2st64_b32 v[48:49],v13 offset0:204 offset1:205\n"
MF "v[84:87],a[232:235],a[244:247],v[84:87]\n"
"ds_read2st64_b32 v[50:51],v13 offset0:206 offset1:207\n"
MF "v[76:79],a[228:231],a[244:247],v[76:79]\n"
"ds_read2st64_b32 v[52:53],v13 offset0:208 offset1:209\n"
MF "v[76:79],a[188:191],a[240:243],v[76:79]\n"
LD "v195,s[48:51],s88 offen offset:3072 lds\n"
MF "v[76:79],a[148:151],v[188:191],v[76:79]\n"
"ds_read2st64_b32 v[54:55],v13 offset0:210 offset1:211\n"
MF "v[68:71],a[144:147],v[188:191],v[68:71]\n"
"ds_read2st64_b32 v[40:41],v13 offset0:196 offset1:197\n"
MF "v[68:71],a[184:187],a[240:243],v[68:71]\n"
"ds_read2st64_b32 v[42:43],v13 offset0:198 offset1:199\n"
MF "v[68:71],a[224:227],a[244:247],v[68:71]\n"
MF "v[60:63],a[220:223],a[244:247],v[60:63]\n"
MF "v[60:63],a[180:183],a[240:243],v[60:63]\n"
"s_cmp_lg_u32 s85,5\n"
MF "v[60:63],a[140:143],v[188:191],v[60:63]\n"
"s_waitcnt lgkmcnt(6)\n"
"s_cbranch_scc1 .Lps5\n"
".Lps9:\n"
MF "v[96:99],a[0:3],v[176:179],v[252:255]\n"
"s_nop 0\n"
TR "v[188:189],v1 offset:24576\n"
TR "v[190:191],v1 offset:26624\n"
MF "v[96:99],a[40:43],v[180:183],v[96:99]\n"
"s_nop 0\n"
TR "a[240:241],v1 offset:28672\n"
TR "a[242:243],v1 offset:30720\n"
MF "v[96:99],a[80:83],v[184:187],v[96:99]\n"
"s_add_u32 m0,s5,53248\n"
TR "a[244:245],v1 offset:32768\n"
TR "a[246:247],v1 offset:34816\n"
MF "v[104:107],a[84:87],v[184:187],v[252:255]\n"
"s_waitcnt lgkmcnt(8)\n"
LD "v196,s[48:51],s88 offen lds\n"
MF "v[104:107],a[44:47],v[180:183],v[104:107]\n"
"ds_read2st64_b32 v[44:45],v13 offset0:200 offset1:201\n"
"v_add_f32 v88,v88,v48\n"
MF "v[104:107],a[4:7],v[176:179],v[104:107]\n"
"ds_read2st64_b32 v[46:47],v13 offset0:202 offset1:203\n"
"v_add_f32 v89,v89,v49\n"
MF "v[112:115],a[8:11],v[176:179],v[252:255]\n"
"ds_read2st64_b32 v[32:33],v13 offset0:16 offset1:17\n"
"v_add_f32 v90,v90,v50\n"
MF "v[112:115],a[48:51],v[180:183],v[112:115]\n"
LD "v197,s[48:51],s88 offen offset:1024 lds\n"
MF "v[112:115],a[88:91],v[184:187],v[112:115]\n"
"ds_read2st64_b32 v[34:35],v13 offset0:18 offset1:19\n"
"v_add_f32 v91,v91,v51\n"
MF "v[120:123],a[92:95],v[184:187],v[252:255]\n"
"ds_read2st64_b32 v[36:37],v13 offset0:192 offset1:193\n"
"v_add_f32 v92,v92,v52\n"
MF "v[120:123],a[52:55],v[180:183],v[120:123]\n"
"s_add_u32 s80,s88,122880\n"
"ds_read2st64_b32 v[38:39],v13 offset0:194 offset1:195\n"
"v_add_f32 v93,v93,v53\n"
MF "v[120:123],a[12:15],v[176:179],v[120:123]\n"
"s_add_u32 m0,s5,98304\n"
"ds_read2st64_b32 v[24:25],v13 offset0:8 offset1:9\n"
"v_add_f32 v94,v94,v54\n"
MF "v[128:131],a[16:19],v[176:179],v[252:255]\n"
LD "v192,s[48:51],s80 offen lds\n"
MF "v[128:131],a[56:59],v[180:183],v[128:131]\n"
"ds_read2st64_b32 v[26:27],v13 offset0:10 offset1:11\n"
"v_add_f32 v95,v95,v55\n"
MF "v[128:131],a[96:99],v[184:187],v[128:131]\n"
"s_waitcnt lgkmcnt(8)\n"
LD "v193,s[48:51],s80 offen offset:1024 lds\n"
MF "v[132:135],a[96:99],a[244:247],v[252:255]\n"
"s_nop 0\n"
TR "v[176:177],v2 offset:24576\n"
TR "v[178:179],v2 offset:26624\n"
MF "v[132:135],a[56:59],a[240:243],v[132:135]\n"
"s_nop 0\n"
TR "v[180:181],v2 offset:28672\n"
TR "v[182:183],v2 offset:30720\n"
MF "v[132:135],a[16:19],v[188:191],v[132:135]\n"
"s_nop 0\n"
TR "v[184:185],v2 offset:32768\n"
TR "v[186:187],v2 offset:34816\n"
MF "v[124:127],a[12:15],v[188:191],v[252:255]\n"
"ds_read2st64_b32 v[28:29],v13 offset0:12 offset1:13\n"
CV "v88,v88,v89\n"
MF "v[124:127],a[52:55],a[240:243],v[124:127]\n"
"ds_read2st64_b32 v[30:31],v13 offset0:14 offset1:15\n"
CV "v89,v90,v91\n"
MF "v[124:127],a[92:95],a[244:247],v[124:127]\n"
"ds_read2st64_b32 v[16:17],v13 offset0:0 offset1:1\n"
CV "v90,v92,v93\n"
MF "v[116:119],a[88:91],a[244:247],v[252:255]\n"
LD "v194,s[48:51],s80 offen offset:2048 lds\n"
MF "v[116:119],a[48:51],a[240:243],v[116:119]\n"
"s_waitcnt lgkmcnt(15)\n"
"ds_read2st64_b32 v[18:19],v13 offset0:2 offset1:3\n"
CV "v91,v94,v95\n"
MF "v[116:119],a[8:11],v[188:191],v[116:119]\n"
"ds_read2st64_b32 v[20:21],v13 offset0:4 offset1:5\n"
"v_add_f32 v80,v80,v40\n"
MF "v[108:111],a[4:7],v[188:191],v[252:255]\n"
"s_add_u32 s87,s90,4096\n"
"ds_read2st64_b32 v[22:23],v13 offset0:6 offset1:7\n"
"v_add_f32 v81,v81,v41\n"
MF "v[108:111],a[44:47],a[240:243],v[108:111]\n"
"buffer_store_dwordx4 v[88:91],v9,s[40:43],s87 offen sc1\n"
MF "v[108:111],a[84:87],a[244:247],v[108:111]\n"
LD "v195,s[48:51],s80 offen offset:3072 lds\n"
MF "v[100:103],a[80:83],a[244:247],v[252:255]\n"
"v_add_f32 v82,v82,v42\n"
"v_add_f32 v83,v83,v43\n"
MF "v[100:103],a[40:43],a[240:243],v[100:103]\n"
"s_add_u32 m0,s5,102400\n"
"v_add_f32 v84,v84,v44\n"
"v_add_f32 v85,v85,v45\n"
MF "v[100:103],a[0:3],v[188:191],v[100:103]\n"
"s_waitcnt lgkmcnt(6)\n"
LD "v196,s[48:51],s80 offen lds\n"
MF "v[136:139],a[0:3],v[176:179],v[252:255]\n"
"s_nop 0\n"
TR "v[188:189],v3 offset:24576\n"
TR "v[190:191],v3 offset:26624\n"
MF "v[136:139],a[40:43],v[180:183],v[136:139]\n"
"s_nop 0\n"
TR "a[240:241],v3 offset:28672\n"
TR "a[242:243],v3 offset:30720\n"
MF "v[136:139],a[80:83],v[184:187],v[136:139]\n"
"s_nop 0\n"
TR "a[244:245],v3 offset:32768\n"
TR "a[246:247],v3 offset:34816\n"
MF "v[144:147],a[84:87],v[184:187],v[252:255]\n"
"v_add_f32 v86,v86,v46\n"
"v_add_f32 v87,v87,v47\n"
MF "v[144:147],a[44:47],v[180:183],v[144:147]\n"
CV "v80,v80,v81\n"
CV "v81,v82,v83\n"
MF "v[144:147],a[4:7],v[176:179],v[144:147]\n"
LD "v197,s[48:51],s80 offen offset:1024 lds\n"
MF "v[152:155],a[8:11],v[176:179],v[252:255]\n"
CV "v82,v84,v85\n"
CV "v83,v86,v87\n"
MF "v[152:155],a[48:51],v[180:183],v[152:155]\n"
"buffer_store_dwordx4 v[80:83],v9,s[40:43],s90 offen offset:3072 sc1\n"
MF "v[152:155],a[88:91],v[184:187],v[152:155]\n"
"v_add_f32 v72,v72,v32\n"
"v_add_f32 v73,v73,v33\n"
MF "v[160:163],a[92:95],v[184:187],v[252:255]\n"
"v_add_f32 v74,v74,v34\n"
"v_add_f32 v75,v75,v35\n"
MF "v[160:163],a[52:55],v[180:183],v[160:163]\n"
"v_add_f32 v76,v76,v36\n"
"v_add_f32 v77,v77,v37\n"
MF "v[160:163],a[12:15],v[176:179],v[160:163]\n"
"v_add_f32 v78,v78,v38\n"
"v_add_f32 v79,v79,v39\n"
MF "v[168:171],a[16:19],v[176:179],v[252:255]\n"
CV "v72,v72,v73\n"
CV "v73,v74,v75\n"
MF "v[168:171],a[56:59],v[180:183],v[168:171]\n"
"s_waitcnt vmcnt(15) lgkmcnt(0)\n"
CV "v74,v76,v77\n"
CV "v75,v78,v79\n"
MF "v[168:171],a[96:99],v[184:187],v[168:171]\n"
"s_barrier\n"
MF "v[172:175],a[96:99],a[244:247],v[252:255]\n"
TR "v[176:177],v0\n"
MF "v[172:175],a[56:59],a[240:243],v[172:175]\n"
"s_add_u32 s89,s89,s65\n"
"s_mov_b32 s82,s70\n"
TR "v[178:179],v0 offset:2048\n"
MF "v[172:175],a[16:19],v[188:191],v[172:175]\n"
"buffer_store_dwordx4 v[72:75],v9,s[40:43],s90 offen offset:2048 sc1\n"
MF "v[164:167],a[12:15],v[188:191],v[252:255]\n"
"s_add_u32 s88,s88,s65\n"
"s_sub_u32 s72,s85,2\n"
TR "v[180:181],v0 offset:4096\n"
MF "v[164:167],a[52:55],a[240:243],v[164:167]\n"
"s_add_u32 m0,s5,24576\n"
TR "v[182:183],v0 offset:6144\n"
MF "v[164:167],a[92:95],a[244:247],v[164:167]\n"
LD "v192,s[48:51],s89 offen lds\n"
MF "v[156:159],a[88:91],a[244:247],v[252:255]\n"
TR "v[184:185],v0 offset:8192\n"
MF "v[156:159],a[48:51],a[240:243],v[156:159]\n"
TR "v[186:187],v0 offset:10240\n"
MF "v[156:159],a[8:11],v[188:191],v[156:159]\n"
LD "v193,s[48:51],s89 offen offset:1024 lds\n"
MF "v[148:151],a[4:7],v[188:191],v[252:255]\n"
"v_add_f32 v64,v64,v24\n"
"v_add_f32 v65,v65,v25\n"
MF "v[148:151],a[44:47],a[240:243],v[148:151]\n"
"v_add_f32 v66,v66,v26\n"
"v_add_f32 v67,v67,v27\n"
MF "v[148:151],a[84:87],a[244:247],v[148:151]\n"
"v_add_f32 v68,v68,v28\n"
"v_add_f32 v69,v69,v29\n"
MF "v[140:143],a[80:83],a[244:247],v[252:255]\n"
"v_add_f32 v70,v70,v30\n"
"v_add_f32 v71,v71,v31\n"
MF "v[140:143],a[40:43],a[240:243],v[140:143]\n"
CV "v64,v64,v65\n"
CV "v65,v66,v67\n"
MF "v[140:143],a[0:3],v[188:191],v[140:143]\n"
"s_waitcnt lgkmcnt(0)\n"
CV "v66,v68,v69\n"
CV "v67,v70,v71\n"
MF "v[96:99],a[120:123],v[176:179],v[96:99]\n"
TR "v[188:189],v1\n"
MF "v[96:99],a[160:163],v[180:183],v[96:99]\n"
TR "v[190:191],v1 offset:2048\n"
MF "v[96:99],a[200:203],v[184:187],v[96:99]\n"
"buffer_store_dwordx4 v[64:67],v9,s[40:43],s90 offen offset:1024 sc1\n"
MF "v[104:107],a[204:207],v[184:187],v[104:107]\n"
TR "a[240:241],v1 offset:4096\n"
MF "v[104:107],a[164:167],v[180:183],v[104:107]\n"
TR "a[242:243],v1 offset:6144\n"
MF "v[104:107],a[124:127],v[176:179],v[104:107]\n"
LD "v194,s[48:51],s89 offen offset:2048 lds\n"
MF "v[112:115],a[128:131],v[176:179],v[112:115]\n"
TR "a[244:245],v1 offset:8192\n"
MF "v[112:115],a[168:171],v[180:183],v[112:115]\n"
".Lpg28h:\n"
"s_cbranch_execz .Lisr4\n"
TR "a[246:247],v1 offset:10240\n"
MF "v[112:115],a[208:211],v[184:187],v[112:115]\n"
MF "v[120:123],a[212:215],v[184:187],v[120:123]\n"
MF "v[120:123],a[172:175],v[180:183],v[120:123]\n"
MF "v[120:123],a[132:135],v[176:179],v[120:123]\n"
LD "v195,s[48:51],s89 offen offset:3072 lds\n"
MF "v[128:131],a[136:139],v[176:179],v[128:131]\n"
"v_add_f32 v56,v56,v16\n"
"v_add_f32 v57,v57,v17\n"
MF "v[128:131],a[176:179],v[180:183],v[128:131]\n"
"v_add_f32 v58,v58,v18\n"
"v_add_f32 v59,v59,v19\n"
MF "v[128:131],a[216:219],v[184:187],v[128:131]\n"
"s_waitcnt lgkmcnt(0)\n"
"v_add_f32 v60,v60,v20\n"
"v_add_f32 v61,v61,v21\n"
MF "v[132:135],a[216:219],a[244:247],v[132:135]\n"
TR "v[176:177],v2\n"
"v_add_f32 v62,v62,v22\n"
MF "v[132:135],a[176:179],a[240:243],v[132:135]\n"
TR "v[178:179],v2 offset:2048\n"
"v_add_f32 v63,v63,v23\n"
MF "v[132:135],a[136:139],v[188:191],v[132:135]\n"
CV "v56,v56,v57\n"
CV "v57,v58,v59\n"
MF "v[124:127],a[132:135],v[188:191],v[124:127]\n"
TR "v[180:181],v2 offset:4096\n"
CV "v58,v60,v61\n"
MF "v[124:127],a[172:175],a[240:243],v[124:127]\n"
TR "v[182:183],v2 offset:6144\n"
CV "v59,v62,v63\n"
MF "v[124:127],a[212:215],a[244:247],v[124:127]\n"
"buffer_load_dword v10,v12,s[40:43],s86 offen sc1\n"
MF "v[116:119],a[208:211],a[244:247],v[116:119]\n"
TR "v[184:185],v2 offset:8192\n"
MF "v[116:119],a[168:171],a[240:243],v[116:119]\n"
TR "v[186:187],v2 offset:10240\n"
MF "v[116:119],a[128:131],v[188:191],v[116:119]\n"
"s_add_u32 m0,s5,28672\n"
"buffer_store_dwordx4 v[56:59],v9,s[40:43],s90 offen sc1\n"
MF "v[108:111],a[124:127],v[188:191],v[108:111]\n"
"s_add_u32 s90,s90,20480\n"
MF "v[108:111],a[164:167],a[240:243],v[108:111]\n"
LD "v196,s[48:51],s89 offen lds\n"
MF "v[108:111],a[204:207],a[244:247],v[108:111]\n"
MF "v[100:103],a[200:203],a[244:247],v[100:103]\n"
MF "v[100:103],a[160:163],a[240:243],v[100:103]\n"
MF "v[100:103],a[120:123],v[188:191],v[100:103]\n"
"s_waitcnt lgkmcnt(0)\n"
MF "v[136:139],a[120:123],v[176:179],v[136:139]\n"
TR "v[188:189],v3\n"
MF "v[136:139],a[160:163],v[180:183],v[136:139]\n"
TR "v[190:191],v3 offset:2048\n"
MF "v[136:139],a[200:203],v[184:187],v[136:139]\n"
LD "v197,s[48:51],s89 offen offset:1024 lds\n"
MF "v[144:147],a[204:207],v[184:187],v[144:147]\n"
TR "a[240:241],v3 offset:4096\n"
MF "v[144:147],a[164:167],v[180:183],v[144:147]\n"
TR "a[242:243],v3 offset:6144\n"
MF "v[144:147],a[124:127],v[176:179],v[144:147]\n"
MF "v[152:155],a[128:131],v[176:179],v[152:155]\n"
TR "a[244:245],v3 offset:8192\n"
MF "v[152:155],a[168:171],v[180:183],v[152:155]\n"
TR "a[246:247],v3 offset:10240\n"
MF "v[152:155],a[208:211],v[184:187],v[152:155]\n"
MF "v[160:163],a[212:215],v[184:187],v[160:163]\n"
MF "v[160:163],a[172:175],v[180:183],v[160:163]\n"
MF "v[160:163],a[132:135],v[176:179],v[160:163]\n"
MF "v[168:171],a[136:139],v[176:179],v[168:171]\n"
MF "v[168:171],a[176:179],v[180:183],v[168:171]\n"
"s_waitcnt vmcnt(18) lgkmcnt(0)\n"
MF "v[168:171],a[216:219],v[184:187],v[168:171]\n"
"s_barrier\n"
MF "v[172:175],a[216:219],a[244:247],v[172:175]\n"
"s_add_u32 s80,s89,122880\n"
TR "v[176:177],v0 offset:49152\n"
MF "v[172:175],a[176:179],a[240:243],v[172:175]\n"
"s_add_u32 m0,s5,0\n"
TR "v[178:179],v0 offset:51200\n"
MF "v[172:175],a[136:139],v[188:191],v[172:175]\n"
LD "v192,s[48:51],s80 offen lds\n"
MF "v[164:167],a[132:135],v[188:191],v[164:167]\n"
"s_add_u32 s60,s64,1\n"
TR "v[180:181],v0 offset:53248\n"
MF "v[164:167],a[172:175],a[240:243],v[164:167]\n"
"s_cmp_eq_u32 s60,8\n"
"s_cselect_b32 s60,0,s60\n"
TR "v[182:183],v0 offset:55296\n"
MF "v[164:167],a[212:215],a[244:247],v[164:167]\n"
LD "v193,s[48:51],s80 offen offset:1024 lds\n"
MF "v[156:159],a[208:211],a[244:247],v[156:159]\n"
"s_cmp_eq_u32 s60,10\n"
"s_cselect_b32 s60,s61,s60\n"
TR "v[184:185],v0 offset:57344\n"
MF "v[156:159],a[168:171],a[240:243],v[156:159]\n"
"s_sub_u32 s65,s60,s64\n"
TR "v[186:187],v0 offset:59392\n"
MF "v[156:159],a[128:131],v[188:191],v[156:159]\n"
"s_lshl_b32 s65,s65,7\n"
MF "v[148:151],a[124:127],v[188:191],v[148:151]\n"
"s_mov_b32 s64,s60\n"
MF "v[148:151],a[164:167],a[240:243],v[148:151]\n"
MF "v[148:151],a[204:207],a[244:247],v[148:151]\n"
LD "v194,s[48:51],s80 offen offset:2048 lds\n"
MF "v[140:143],a[200:203],a[244:247],v[140:143]\n"
MF "v[140:143],a[160:163],a[240:243],v[140:143]\n"
MF "v[140:143],a[120:123],v[188:191],v[140:143]\n"
"s_waitcnt lgkmcnt(0)\n"
MF "v[96:99],a[20:23],v[176:179],v[96:99]\n"
TR "v[188:189],v1 offset:49152\n"
MF "v[96:99],a[60:63],v[180:183],v[96:99]\n"
TR "v[190:191],v1 offset:51200\n"
MF "v[96:99],a[100:103],v[184:187],v[96:99]\n"
LD "v195,s[48:51],s80 offen offset:3072 lds\n"
MF "v[104:107],a[104:107],v[184:187],v[104:107]\n"
TR "a[240:241],v1 offset:53248\n"
MF "v[104:107],a[64:67],v[180:183],v[104:107]\n"
"s_add_u32 m0,s5,4096\n"
TR "a[242:243],v1 offset:55296\n"
MF "v[104:107],a[24:27],v[176:179],v[104:107]\n"
LD "v196,s[48:51],s80 offen lds\n"
MF "v[112:115],a[28:31],v[176:179],v[112:115]\n"
TR "a[244:245],v1 offset:57344\n"
MF "v[112:115],a[68:71],v[180:183],v[112:115]\n"
TR "a[246:247],v1 offset:59392\n"
MF "v[112:115],a[108:111],v[184:187],v[112:115]\n"
MF "v[120:123],a[112:115],v[184:187],v[120:123]\n"
MF "v[120:123],a[72:75],v[180:183],v[120:123]\n"
MF "v[120:123],a[32:35],v[176:179],v[120:123]\n"
LD "v197,s[48:51],s80 offen offset:1024 lds\n"
MF "v[128:131],a[36:39],v[176:179],v[128:131]\n"
MF "v[128:131],a[76:79],v[180:183],v[128:131]\n"
MF "v[128:131],a[116:119],v[184:187],v[128:131]\n"
"s_waitcnt lgkmcnt(0)\n"
MF "v[132:135],a[116:119],a[244:247],v[132:135]\n"
TR "v[176:177],v2 offset:49152\n"
MF "v[132:135],a[76:79],a[240:243],v[132:135]\n"
TR "v[178:179],v2 offset:51200\n"
MF "v[132:135],a[36:39],v[188:191],v[132:135]\n"
MF "v[124:127],a[32:35],v[188:191],v[124:127]\n"
TR "v[180:181],v2 offset:53248\n"
MF "v[124:127],a[72:75],a[240:243],v[124:127]\n"
TR "v[182:183],v2 offset:55296\n"
MF "v[124:127],a[112:115],a[244:247],v[124:127]\n"
MF "v[116:119],a[108:111],a[244:247],v[116:119]\n"
TR "v[184:185],v2 offset:57344\n"
MF "v[116:119],a[68:71],a[240:243],v[116:119]\n"
TR "v[186:187],v2 offset:59392\n"
MF "v[116:119],a[28:31],v[188:191],v[116:119]\n"
MF "v[108:111],a[24:27],v[188:191],v[108:111]\n"
MF "v[108:111],a[64:67],a[240:243],v[108:111]\n"
MF "v[108:111],a[104:107],a[244:247],v[108:111]\n"
MF "v[100:103],a[100:103],a[244:247],v[100:103]\n"
MF "v[100:103],a[60:63],a[240:243],v[100:103]\n"
"s_waitcnt vmcnt(9)\n"
"s_cmp_lt_u32 s85,6\n"
MF "v[100:103],a[20:23],v[188:191],v[100:103]\n"
"s_cbranch_scc1 .Ljs4\n"
"v_mov_b32 v236,v228\n"
"v_mov_b32 v237,v229\n"
MF "v[136:139],a[252:255],v[208:211],v[136:139]\n"
MF "v[144:147],a[252:255],v[212:215],v[144:147]\n"
MF "v[152:155],a[252:255],v[216:219],v[152:155]\n"
MF "v[160:163],a[252:255],v[220:223],v[160:163]\n"
MF "v[168:171],a[252:255],v[224:227],v[168:171]\n"
MF "v[140:143],a[248:251],v[208:211],v[140:143]\n"
MF "v[148:151],a[248:251],v[212:215],v[148:151]\n"
MF "v[156:159],a[248:251],v[216:219],v[156:159]\n"
MF "v[164:167],a[248:251],v[220:223],v[164:167]\n"
MF "v[172:175],a[248:251],v[224:227],v[172:175]\n"
MF "v[248:251],v[230:233],a[250:253],v[248:251]\n"
MF "v[244:247],v[234:237],a[250:253],v[244:247]\n"
".Ljs4:\n"
"s_waitcnt lgkmcnt(0)\n"
MF "v[136:139],a[20:23],v[176:179],v[136:139]\n"
TR "v[188:189],v3 offset:49152\n"
"v_sub_u32 v10,v10,v11\n"
MF "v[136:139],a[60:63],v[180:183],v[136:139]\n"
TR "v[190:191],v3 offset:51200\n"
MF "v[136:139],a[100:103],v[184:187],v[136:139]\n"
"v_mov_b32 v14,s76\n"
"s_add_u32 s76,s76,1\n"
MF "v[144:147],a[104:107],v[184:187],v[144:147]\n"
TR "a[240:241],v3 offset:53248\n"
MF "v[144:147],a[64:67],v[180:183],v[144:147]\n"
TR "a[242:243],v3 offset:55296\n"
MF "v[144:147],a[24:27],v[176:179],v[144:147]\n"
".Lnf0:\n"
"v_readlane_b32 s58,v10,s67\n"
"s_cmp_lt_u32 s58,s72\n"
"s_cbranch_scc1 .Lsp0\n"
".Lnb0:\n"
MF "v[152:155],a[28:31],v[176:179],v[152:155]\n"
"s_add_u32 s70,s70,20480\n"
TR "a[244:245],v3 offset:57344\n"
MF "v[152:155],a[68:71],v[180:183],v[152:155]\n"
TR "a[246:247],v3 offset:59392\n"
MF "v[152:155],a[108:111],v[184:187],v[152:155]\n"
".Lnf1:\n"
"v_cmp_gt_u32 vcc,2,v10\n"
"s_cbranch_vccnz .Lsp1\n"
".Lnb1:\n"
MF "v[160:163],a[112:115],v[184:187],v[160:163]\n"
MF "v[160:163],a[72:75],v[180:183],v[160:163]\n"
LD "v[208:211],v9,s[40:43],s82 offen sc1\n"
MF "v[160:163],a[32:35],v[176:179],v[160:163]\n"
MF "v[168:171],a[36:39],v[176:179],v[168:171]\n"
MF "v[168:171],a[76:79],v[180:183],v[168:171]\n"
"s_waitcnt vmcnt(9) lgkmcnt(0)\n"
LD "v[212:215],v9,s[40:43],s82 offen offset:1024 sc1\n"
MF "v[168:171],a[116:119],v[184:187],v[168:171]\n"
"s_barrier\n"
MF "v[172:175],a[116:119],a[244:247],v[172:175]\n"
TR "v[176:177],v4\n"
MF "v[172:175],a[76:79],a[240:243],v[172:175]\n"
TR "v[178:179],v4 offset:2048\n"
MF "v[172:175],a[36:39],v[188:191],v[172:175]\n"
"buffer_store_dword v14,v15,s[40:43],s94 offen sc1\n"
MF "v[164:167],a[32:35],v[188:191],v[164:167]\n"
TR "v[180:181],v4 offset:4096\n"
MF "v[164:167],a[72:75],a[240:243],v[164:167]\n"
TR "v[182:183],v4 offset:6144\n"
MF "v[164:167],a[112:115],a[244:247],v[164:167]\n"
LD "v[216:219],v9,s[40:43],s82 offen offset:2048 sc1\n"
MF "v[156:159],a[108:111],a[244:247],v[156:159]\n"
TR "v[184:185],v4 offset:8192\n"
MF "v[156:159],a[68:71],a[240:243],v[156:159]\n"
TR "v[186:187],v4 offset:10240\n"
MF "v[156:159],a[28:31],v[188:191],v[156:159]\n"
LD "v[220:223],v9,s[40:43],s82 offen offset:3072 sc1\n"
MF "v[148:151],a[24:27],v[188:191],v[148:151]\n"
"s_add_u32 s82,s82,4096\n"
MF "v[148:151],a[64:67],a[240:243],v[148:151]\n"
"s_add_u32 m0,s5,73728\n"
LD "v[224:227],v9,s[40:43],s82 offen sc1\n"
MF "v[148:151],a[104:107],a[244:247],v[148:151]\n"
"s_add_u32 s2,s2,204800\n"
MF "v[140:143],a[100:103],a[244:247],v[140:143]\n"
LD "v[228:231],v9,s[40:43],s2 offen sc1\n"
MF "v[140:143],a[60:63],a[240:243],v[140:143]\n"
"s_add_u32 s2,s2,204800\n"
MF "v[140:143],a[20:23],v[188:191],v[140:143]\n"
"s_waitcnt lgkmcnt(0)\n"
LD "v[232:235],v9,s[40:43],s2 offen sc1\n"
MF "v[96:99],a[140:143],v[176:179],v[96:99]\n"
"s_nop 0\n"
TR "v[188:189],v5\n"
TR "v[190:191],v5 offset:2048\n"
MF "v[96:99],a[180:183],v[180:183],v[96:99]\n"
"s_nop 0\n"
TR "a[240:241],v5 offset:4096\n"
TR "a[242:243],v5 offset:6144\n"
MF "v[96:99],a[220:223],v[184:187],v[96:99]\n"
"s_nop 0\n"
TR "a[244:245],v5 offset:8192\n"
TR "a[246:247],v5 offset:10240\n"
MF "v[104:107],a[224:227],v[184:187],v[104:107]\n"
MF "v[104:107],a[184:187],v[180:183],v[104:107]\n"
MF "v[104:107],a[144:147],v[176:179],v[104:107]\n"
"s_nop 0\n"
"s_nop 0\n"
"ds_write_addtid_b32 v96\n"
"ds_write_addtid_b32 v97 offset:256\n"
MF "v[112:115],a[148:151],v[176:179],v[112:115]\n"
"s_nop 0\n"
"ds_write_addtid_b32 v98 offset:512\n"
"ds_write_addtid_b32 v99 offset:768\n"
MF "v[112:115],a[188:191],v[180:183],v[112:115]\n"
MF "v[112:115],a[228:231],v[184:187],v[112:115]\n"
"s_nop 0\n"
"ds_write_addtid_b32 v104 offset:2048\n"
"ds_write_addtid_b32 v105 offset:2304\n"
MF "v[120:123],a[232:235],v[184:187],v[120:123]\n"
"s_nop 0\n"
"ds_write_addtid_b32 v106 offset:2560\n"
"ds_write_addtid_b32 v107 offset:2816\n"
MF "v[120:123],a[192:195],v[180:183],v[120:123]\n"
MF "v[120:123],a[152:155],v[176:179],v[120:123]\n"
"s_nop 0\n"
"ds_write_addtid_b32 v112 offset:4096\n"
"ds_write_addtid_b32 v113 offset:4352\n"
MF "v[128:131],a[156:159],v[176:179],v[128:131]\n"
"s_nop 0\n"
"ds_write_addtid_b32 v114 offset:4608\n"
"ds_write_addtid_b32 v115 offset:4864\n"
MF "v[128:131],a[196:199],v[180:183],v[128:131]\n"
MF "v[128:131],a[236:239],v[184:187],v[128:131]\n"
"s_waitcnt lgkmcnt(12)\n"
"ds_write_addtid_b32 v120 offset:50176\n"
"ds_write_addtid_b32 v121 offset:50432\n"
MF "v[132:135],a[236:239],a[244:247],v[132:135]\n"
"s_nop 0\n"
TR "v[176:177],v6\n"
TR "v[178:179],v6 offset:2048\n"
MF "v[132:135],a[196:199],a[240:243],v[132:135]\n"
"s_nop 0\n"
TR "v[180:181],v6 offset:4096\n"
TR "v[182:183],v6 offset:6144\n"
MF "v[132:135],a[156:159],v[188:191],v[132:135]\n"
"s_nop 0\n"
TR "v[184:185],v6 offset:8192\n"
TR "v[186:187],v6 offset:10240\n"
MF "v[124:127],a[152:155],v[188:191],v[124:127]\n"
"s_nop 0\n"
"ds_write_addtid_b32 v122 offset:50688\n"
"ds_write_addtid_b32 v123 offset:50944\n"
MF "v[124:127],a[192:195],a[240:243],v[124:127]\n"
"s_nop 0\n"
"ds_write_addtid_b32 v128 offset:52224\n"
"ds_write_addtid_b32 v129 offset:52480\n"
MF "v[124:127],a[232:235],a[244:247],v[124:127]\n"
"s_nop 0\n"
"ds_write_addtid_b32 v130 offset:52736\n"
"ds_write_addtid_b32 v131 offset:52992\n"
MF "v[116:119],a[228:231],a[244:247],v[116:119]\n"
"s_nop 0\n"
"ds_write_addtid_b32 v132 offset:53248\n"
"ds_write_addtid_b32 v133 offset:53504\n"
MF "v[116:119],a[188:191],a[240:243],v[116:119]\n"
"s_nop 0\n"
"ds_write_addtid_b32 v134 offset:53760\n"
"ds_write_addtid_b32 v135 offset:54016\n"
MF "v[116:119],a[148:151],v[188:191],v[116:119]\n"
"s_nop 0\n"
"ds_write_addtid_b32 v124 offset:51200\n"
"ds_write_addtid_b32 v125 offset:51456\n"
MF "v[108:111],a[144:147],v[188:191],v[108:111]\n"
"s_nop 0\n"
"ds_write_addtid_b32 v126 offset:51712\n"
"ds_write_addtid_b32 v127 offset:51968\n"
MF "v[108:111],a[184:187],a[240:243],v[108:111]\n"
MF "v[108:111],a[224:227],a[244:247],v[108:111]\n"
"s_nop 0\n"
"ds_write_addtid_b32 v116 offset:49152\n"
"ds_write_addtid_b32 v117 offset:49408\n"
MF "v[100:103],a[220:223],a[244:247],v[100:103]\n"
"s_nop 0\n"
"ds_write_addtid_b32 v118 offset:49664\n"
"ds_write_addtid_b32 v119 offset:49920\n"
MF "v[100:103],a[180:183],a[240:243],v[100:103]\n"
MF "v[100:103],a[140:143],v[188:191],v[100:103]\n"
"s_waitcnt lgkmcnt(15)\n"
"ds_write_addtid_b32 v108 offset:3072\n"
"ds_write_addtid_b32 v109 offset:3328\n"
MF "v[136:139],a[140:143],v[176:179],v[136:139]\n"
"s_nop 0\n"
TR "v[188:189],v7\n"
TR "v[190:191],v7 offset:2048\n"
MF "v[136:139],a[180:183],v[180:183],v[136:139]\n"
"s_nop 0\n"
TR "a[240:241],v7 offset:4096\n"
TR "a[242:243],v7 offset:6144\n"
MF "v[136:139],a[220:223],v[184:187],v[136:139]\n"
"s_nop 0\n"
TR "a[244:245],v7 offset:8192\n"
TR "a[246:247],v7 offset:10240\n"
MF "v[144:147],a[224:227],v[184:187],v[144:147]\n"
"s_nop 0\n"
"ds_write_addtid_b32 v110 offset:3584\n"
"ds_write_addtid_b32 v111 offset:3840\n"
MF "v[144:147],a[184:187],v[180:183],v[144:147]\n"
"s_nop 0\n"
"ds_write_addtid_b32 v100 offset:1024\n"
"ds_write_addtid_b32 v101 offset:1280\n"
MF "v[144:147],a[144:147],v[176:179],v[144:147]\n"
"s_nop 0\n"
"ds_write_addtid_b32 v102 offset:1536\n"
"ds_write_addtid_b32 v103 offset:1792\n"
MF "v[152:155],a[148:151],v[176:179],v[152:155]\n"
"s_add_u32 m0,s5,49152\n"
MF "v[152:155],a[188:191],v[180:183],v[152:155]\n"
LD "v192,s[48:51],s88 offen lds\n"
MF "v[152:155],a[228:231],v[184:187],v[152:155]\n"
MF "v[160:163],a[232:235],v[184:187],v[160:163]\n"
MF "v[160:163],a[192:195],v[180:183],v[160:163]\n"
MF "v[160:163],a[152:155],v[176:179],v[160:163]\n"
MF "v[168:171],a[156:159],v[176:179],v[168:171]\n"
LD "v193,s[48:51],s88 offen offset:1024 lds\n"
MF "v[168:171],a[196:199],v[180:183],v[168:171]\n"
"s_waitcnt vmcnt(16) lgkmcnt(0)\n"
MF "v[168:171],a[236:239],v[184:187],v[168:171]\n"
"s_barrier\n"
MF "v[172:175],a[236:239],a[244:247],v[172:175]\n"
"s_nop 0\n"
TR "v[176:177],v0 offset:24576\n"
TR "v[178:179],v0 offset:26624\n"
MF "v[172:175],a[196:199],a[240:243],v[172:175]\n"
"s_add_u32 s85,s85,1\n"
TR "v[180:181],v0 offset:28672\n"
TR "v[182:183],v0 offset:30720\n"
MF "v[172:175],a[156:159],v[188:191],v[172:175]\n"
"s_nop 0\n"
TR "v[184:185],v0 offset:32768\n"
TR "v[186:187],v0 offset:34816\n"
MF "v[164:167],a[152:155],v[188:191],v[164:167]\n"
LD "v194,s[48:51],s88 offen offset:2048 lds\n"
MF "v[164:167],a[192:195],a[240:243],v[164:167]\n"
"ds_read2st64_b32 v[128:129],v13 offset0:204 offset1:205\n"
MF "v[164:167],a[232:235],a[244:247],v[164:167]\n"
"ds_read2st64_b32 v[130:131],v13 offset0:206 offset1:207\n"
MF "v[156:159],a[228:231],a[244:247],v[156:159]\n"
"ds_read2st64_b32 v[132:133],v13 offset0:208 offset1:209\n"
MF "v[156:159],a[188:191],a[240:243],v[156:159]\n"
LD "v195,s[48:51],s88 offen offset:3072 lds\n"
MF "v[156:159],a[148:151],v[188:191],v[156:159]\n"
"ds_read2st64_b32 v[134:135],v13 offset0:210 offset1:211\n"
MF "v[148:151],a[144:147],v[188:191],v[148:151]\n"
"ds_read2st64_b32 v[120:121],v13 offset0:196 offset1:197\n"
MF "v[148:151],a[184:187],a[240:243],v[148:151]\n"
"ds_read2st64_b32 v[122:123],v13 offset0:198 offset1:199\n"
MF "v[148:151],a[224:227],a[244:247],v[148:151]\n"
MF "v[140:143],a[220:223],a[244:247],v[140:143]\n"
MF "v[140:143],a[180:183],a[240:243],v[140:143]\n"
MF "v[140:143],a[140:143],v[188:191],v[140:143]\n"
"s_waitcnt lgkmcnt(6)\n"
MF "v[16:19],a[0:3],v[176:179],v[252:255]\n"
"s_nop 0\n"
TR "v[188:189],v1 offset:24576\n"
TR "v[190:191],v1 offset:26624\n"
MF "v[16:19],a[40:43],v[180:183],v[16:19]\n"
"s_nop 0\n"
TR "a[240:241],v1 offset:28672\n"
TR "a[242:243],v1 offset:30720\n"
MF "v[16:19],a[80:83],v[184:187],v[16:19]\n"
"s_add_u32 m0,s5,53248\n"
TR "a[244:245],v1 offset:32768\n"
TR "a[246:247],v1 offset:34816\n"
MF "v[24:27],a[84:87],v[184:187],v[252:255]\n"
"s_waitcnt lgkmcnt(8)\n"
LD "v196,s[48:51],s88 offen lds\n"
MF "v[24:27],a[44:47],v[180:183],v[24:27]\n"
"ds_read2st64_b32 v[124:125],v13 offset0:200 offset1:201\n"
"v_add_f32 v168,v168,v128\n"
MF "v[24:27],a[4:7],v[176:179],v[24:27]\n"
"ds_read2st64_b32 v[126:127],v13 offset0:202 offset1:203\n"
"v_add_f32 v169,v169,v129\n"
MF "v[32:35],a[8:11],v[176:179],v[252:255]\n"
"ds_read2st64_b32 v[112:113],v13 offset0:16 offset1:17\n"
"v_add_f32 v170,v170,v130\n"
MF "v[32:35],a[48:51],v[180:183],v[32:35]\n"
LD "v197,s[48:51],s88 offen offset:1024 lds\n"
MF "v[32:35],a[88:91],v[184:187],v[32:35]\n"
"ds_read2st64_b32 v[114:115],v13 offset0:18 offset1:19\n"
"v_add_f32 v171,v171,v131\n"
MF "v[40:43],a[92:95],v[184:187],v[252:255]\n"
"ds_read2st64_b32 v[116:117],v13 offset0:192 offset1:193\n"
"v_add_f32 v172,v172,v132\n"
MF "v[40:43],a[52:55],v[180:183],v[40:43]\n"
"s_add_u32 s80,s88,122880\n"
"ds_read2st64_b32 v[118:119],v13 offset0:194 offset1:195\n"
"v_add_f32 v173,v173,v133\n"
MF "v[40:43],a[12:15],v[176:179],v[40:43]\n"
"s_add_u32 m0,s5,98304\n"
"ds_read2st64_b32 v[104:105],v13 offset0:8 offset1:9\n"
"v_add_f32 v174,v174,v134\n"
MF "v[48:51],a[16:19],v[176:179],v[252:255]\n"
LD "v192,s[48:51],s80 offen lds\n"
MF "v[48:51],a[56:59],v[180:183],v[48:51]\n"
"ds_read2st64_b32 v[106:107],v13 offset0:10 offset1:11\n"
"v_add_f32 v175,v175,v135\n"
MF "v[48:51],a[96:99],v[184:187],v[48:51]\n"
"s_waitcnt lgkmcnt(8)\n"
LD "v193,s[48:51],s80 offen offset:1024 lds\n"
MF "v[52:55],a[96:99],a[244:247],v[252:255]\n"
"s_nop 0\n"
TR "v[176:177],v2 offset:24576\n"
TR "v[178:179],v2 offset:26624\n"
MF "v[52:55],a[56:59],a[240:243],v[52:55]\n"
"s_nop 0\n"
TR "v[180:181],v2 offset:28672\n"
TR "v[182:183],v2 offset:30720\n"
MF "v[52:55],a[16:19],v[188:191],v[52:55]\n"
"s_nop 0\n"
TR "v[184:185],v2 offset:32768\n"
TR "v[186:187],v2 offset:34816\n"
MF "v[44:47],a[12:15],v[188:191],v[252:255]\n"
"ds_read2st64_b32 v[108:109],v13 offset0:12 offset1:13\n"
CV "v168,v168,v169\n"
MF "v[44:47],a[52:55],a[240:243],v[44:47]\n"
"ds_read2st64_b32 v[110:111],v13 offset0:14 offset1:15\n"
CV "v169,v170,v171\n"
MF "v[44:47],a[92:95],a[244:247],v[44:47]\n"
"ds_read2st64_b32 v[96:97],v13 offset0:0 offset1:1\n"
CV "v170,v172,v173\n"
MF "v[36:39],a[88:91],a[244:247],v[252:255]\n"
LD "v194,s[48:51],s80 offen offset:2048 lds\n"
MF "v[36:39],a[48:51],a[240:243],v[36:39]\n"
"s_waitcnt lgkmcnt(15)\n"
"ds_read2st64_b32 v[98:99],v13 offset0:2 offset1:3\n"
CV "v171,v174,v175\n"
MF "v[36:39],a[8:11],v[188:191],v[36:39]\n"
"ds_read2st64_b32 v[100:101],v13 offset0:4 offset1:5\n"
"v_add_f32 v160,v160,v120\n"
MF "v[28:31],a[4:7],v[188:191],v[252:255]\n"
"s_add_u32 s87,s90,4096\n"
"ds_read2st64_b32 v[102:103],v13 offset0:6 offset1:7\n"
"v_add_f32 v161,v161,v121\n"
MF "v[28:31],a[44:47],a[240:243],v[28:31]\n"
"buffer_store_dwordx4 v[168:171],v9,s[40:43],s87 offen sc1\n"
MF "v[28:31],a[84:87],a[244:247],v[28:31]\n"
LD "v195,s[48:51],s80 offen offset:3072 lds\n"
MF "v[20:23],a[80:83],a[244:247],v[252:255]\n"
"v_add_f32 v162,v162,v122\n"
"v_add_f32 v163,v163,v123\n"
MF "v[20:23],a[40:43],a[240:243],v[20:23]\n"
"s_add_u32 m0,s5,102400\n"
"v_add_f32 v164,v164,v124\n"
"v_add_f32 v165,v165,v125\n"
MF "v[20:23],a[0:3],v[188:191],v[20:23]\n"
"s_waitcnt lgkmcnt(6)\n"
LD "v196,s[48:51],s80 offen lds\n"
MF "v[56:59],a[0:3],v[176:179],v[252:255]\n"
"s_nop 0\n"
TR "v[188:189],v3 offset:24576\n"
TR "v[190:191],v3 offset:26624\n"
MF "v[56:59],a[40:43],v[180:183],v[56:59]\n"
"s_nop 0\n"
TR "a[240:241],v3 offset:28672\n"
TR "a[242:243],v3 offset:30720\n"
MF "v[56:59],a[80:83],v[184:187],v[56:59]\n"
"s_nop 0\n"
TR "a[244:245],v3 offset:32768\n"
TR "a[246:247],v3 offset:34816\n"
MF "v[64:67],a[84:87],v[184:187],v[252:255]\n"
"v_add_f32 v166,v166,v126\n"
"v_add_f32 v167,v167,v127\n"
MF "v[64:67],a[44:47],v[180:183],v[64:67]\n"
CV "v160,v160,v161\n"
CV "v161,v162,v163\n"
MF "v[64:67],a[4:7],v[176:179],v[64:67]\n"
LD "v197,s[48:51],s80 offen offset:1024 lds\n"
MF "v[72:75],a[8:11],v[176:179],v[252:255]\n"
CV "v162,v164,v165\n"
CV "v163,v166,v167\n"
MF "v[72:75],a[48:51],v[180:183],v[72:75]\n"
"buffer_store_dwordx4 v[160:163],v9,s[40:43],s90 offen offset:3072 sc1\n"
MF "v[72:75],a[88:91],v[184:187],v[72:75]\n"
"v_add_f32 v152,v152,v112\n"
"v_add_f32 v153,v153,v113\n"
MF "v[80:83],a[92:95],v[184:187],v[252:255]\n"
"v_add_f32 v154,v154,v114\n"
"v_add_f32 v155,v155,v115\n"
MF "v[80:83],a[52:55],v[180:183],v[80:83]\n"
"v_add_f32 v156,v156,v116\n"
"v_add_f32 v157,v157,v117\n"
MF "v[80:83],a[12:15],v[176:179],v[80:83]\n"
"v_add_f32 v158,v158,v118\n"
"v_add_f32 v159,v159,v119\n"
MF "v[88:91],a[16:19],v[176:179],v[252:255]\n"
CV "v152,v152,v153\n"
CV "v153,v154,v155\n"
MF "v[88:91],a[56:59],v[180:183],v[88:91]\n"
"s_waitcnt vmcnt(22) lgkmcnt(0)\n"
CV "v154,v156,v157\n"
CV "v155,v158,v159\n"
MF "v[88:91],a[96:99],v[184:187],v[88:91]\n"
"s_barrier\n"
MF "v[92:95],a[96:99],a[244:247],v[252:255]\n"
TR "v[176:177],v0\n"
MF "v[92:95],a[56:59],a[240:243],v[92:95]\n"
"s_add_u32 s89,s89,s65\n"
"s_mov_b32 s82,s70\n"
TR "v[178:179],v0 offset:2048\n"
MF "v[92:95],a[16:19],v[188:191],v[92:95]\n"
"buffer_store_dwordx4 v[152:155],v9,s[40:43],s90 offen offset:2048 sc1\n"
MF "v[84:87],a[12:15],v[188:191],v[252:255]\n"
"s_add_u32 s88,s88,s65\n"
"s_sub_u32 s72,s85,2\n"
TR "v[180:181],v0 offset:4096\n"
MF "v[84:87],a[52:55],a[240:243],v[84:87]\n"
"s_add_u32 m0,s5,24576\n"
TR "v[182:183],v0 offset:6144\n"
MF "v[84:87],a[92:95],a[244:247],v[84:87]\n"
LD "v192,s[48:51],s89 offen lds\n"
MF "v[76:79],a[88:91],a[244:247],v[252:255]\n"
".Lpg29h:\n"
"s_cbranch_execz .Lisr4\n"
TR "v[184:185],v0 offset:8192\n"
MF "v[76:79],a[48:51],a[240:243],v[76:79]\n"
TR "v[186:187],v0 offset:10240\n"
MF "v[76:79],a[8:11],v[188:191],v[76:79]\n"
LD "v193,s[48:51],s89 offen offset:1024 lds\n"
MF "v[68:71],a[4:7],v[188:191],v[252:255]\n"
"v_add_f32 v144,v144,v104\n"
"v_add_f32 v145,v145,v105\n"
MF "v[68:71],a[44:47],a[240:243],v[68:71]\n"
"v_add_f32 v146,v146,v106\n"
"v_add_f32 v147,v147,v107\n"
MF "v[68:71],a[84:87],a[244:247],v[68:71]\n"
"v_add_f32 v148,v148,v108\n"
"v_add_f32 v149,v149,v109\n"
MF "v[60:63],a[80:83],a[244:247],v[252:255]\n"
"v_add_f32 v150,v150,v110\n"
"v_add_f32 v151,v151,v111\n"
MF "v[60:63],a[40:43],a[240:243],v[60:63]\n"
CV "v144,v144,v145\n"
CV "v145,v146,v147\n"
MF "v[60:63],a[0:3],v[188:191],v[60:63]\n"
"s_waitcnt lgkmcnt(0)\n"
CV "v146,v148,v149\n"
CV "v147,v150,v151\n"
MF "v[16:19],a[120:123],v[176:179],v[16:19]\n"
TR "v[188:189],v1\n"
MF "v[16:19],a[160:163],v[180:183],v[16:19]\n"
TR "v[190:191],v1 offset:2048\n"
MF "v[16:19],a[200:203],v[184:187],v[16:19]\n"
"buffer_store_dwordx4 v[144:147],v9,s[40:43],s90 offen offset:1024 sc1\n"
MF "v[24:27],a[204:207],v[184:187],v[24:27]\n"
TR "a[240:241],v1 offset:4096\n"
MF "v[24:27],a[164:167],v[180:183],v[24:27]\n"
TR "a[242:243],v1 offset:6144\n"
MF "v[24:27],a[124:127],v[176:179],v[24:27]\n"
LD "v194,s[48:51],s89 offen offset:2048 lds\n"
MF "v[32:35],a[128:131],v[176:179],v[32:35]\n"
TR "a[244:245],v1 offset:8192\n"
MF "v[32:35],a[168:171],v[180:183],v[32:35]\n"
TR "a[246:247],v1 offset:10240\n"
MF "v[32:35],a[208:211],v[184:187],v[32:35]\n"
MF "v[40:43],a[212:215],v[184:187],v[40:43]\n"
MF "v[40:43],a[172:175],v[180:183],v[40:43]\n"
MF "v[40:43],a[132:135],v[176:179],v[40:43]\n"
LD "v195,s[48:51],s89 offen offset:3072 lds\n"
MF "v[48:51],a[136:139],v[176:179],v[48:51]\n"
"v_add_f32 v136,v136,v96\n"
"v_add_f32 v137,v137,v97\n"
MF "v[48:51],a[176:179],v[180:183],v[48:51]\n"
"v_add_f32 v138,v138,v98\n"
"v_add_f32 v139,v139,v99\n"
MF "v[48:51],a[216:219],v[184:187],v[48:51]\n"
"s_waitcnt lgkmcnt(0)\n"
"v_add_f32 v140,v140,v100\n"
"v_add_f32 v141,v141,v101\n"
MF "v[52:55],a[216:219],a[244:247],v[52:55]\n"
TR "v[176:177],v2\n"
"v_add_f32 v142,v142,v102\n"
MF "v[52:55],a[176:179],a[240:243],v[52:55]\n"
TR "v[178:179],v2 offset:2048\n"
"v_add_f32 v143,v143,v103\n"
MF "v[52:55],a[136:139],v[188:191],v[52:55]\n"
CV "v136,v136,v137\n"
CV "v137,v138,v139\n"
MF "v[44:47],a[132:135],v[188:191],v[44:47]\n"
TR "v[180:181],v2 offset:4096\n"
CV "v138,v140,v141\n"
MF "v[44:47],a[172:175],a[240:243],v[44:47]\n"
TR "v[182:183],v2 offset:6144\n"
CV "v139,v142,v143\n"
MF "v[44:47],a[212:215],a[244:247],v[44:47]\n"
"buffer_load_dword v10,v12,s[40:43],s86 offen sc1\n"
MF "v[36:39],a[208:211],a[244:247],v[36:39]\n"
TR "v[184:185],v2 offset:8192\n"
MF "v[36:39],a[168:171],a[240:243],v[36:39]\n"
TR "v[186:187],v2 offset:10240\n"
MF "v[36:39],a[128:131],v[188:191],v[36:39]\n"
"s_add_u32 m0,s5,28672\n"
"buffer_store_dwordx4 v[136:139],v9,s[40:43],s90 offen sc1\n"
MF "v[28:31],a[124:127],v[188:191],v[28:31]\n"
"s_add_u32 s90,s90,20480\n"
MF "v[28:31],a[164:167],a[240:243],v[28:31]\n"
LD "v196,s[48:51],s89 offen lds\n"
MF "v[28:31],a[204:207],a[244:247],v[28:31]\n"
MF "v[20:23],a[200:203],a[244:247],v[20:23]\n"
MF "v[20:23],a[160:163],a[240:243],v[20:23]\n"
MF "v[20:23],a[120:123],v[188:191],v[20:23]\n"
"s_waitcnt lgkmcnt(0)\n"
MF "v[56:59],a[120:123],v[176:179],v[56:59]\n"
TR "v[188:189],v3\n"
MF "v[56:59],a[160:163],v[180:183],v[56:59]\n"
TR "v[190:191],v3 offset:2048\n"
MF "v[56:59],a[200:203],v[184:187],v[56:59]\n"
LD "v197,s[48:51],s89 offen offset:1024 lds\n"
MF "v[64:67],a[204:207],v[184:187],v[64:67]\n"
TR "a[240:241],v3 offset:4096\n"
MF "v[64:67],a[164:167],v[180:183],v[64:67]\n"
TR "a[242:243],v3 offset:6144\n"
MF "v[64:67],a[124:127],v[176:179],v[64:67]\n"
MF "v[72:75],a[128:131],v[176:179],v[72:75]\n"
TR "a[244:245],v3 offset:8192\n"
MF "v[72:75],a[168:171],v[180:183],v[72:75]\n"
TR "a[246:247],v3 offset:10240\n"
MF "v[72:75],a[208:211],v[184:187],v[72:75]\n"
MF "v[80:83],a[212:215],v[184:187],v[80:83]\n"
MF "v[80:83],a[172:175],v[180:183],v[80:83]\n"
MF "v[80:83],a[132:135],v[176:179],v[80:83]\n"
MF "v[88:91],a[136:139],v[176:179],v[88:91]\n"
MF "v[88:91],a[176:179],v[180:183],v[88:91]\n"
"s_waitcnt vmcnt(18) lgkmcnt(0)\n"
MF "v[88:91],a[216:219],v[184:187],v[88:91]\n"
"s_barrier\n"
MF "v[92:95],a[216:219],a[244:247],v[92:95]\n"
"s_add_u32 s80,s89,122880\n"
TR "v[176:177],v0 offset:49152\n"
MF "v[92:95],a[176:179],a[240:243],v[92:95]\n"
"s_add_u32 m0,s5,0\n"
TR "v[178:179],v0 offset:51200\n"
MF "v[92:95],a[136:139],v[188:191],v[92:95]\n"
LD "v192,s[48:51],s80 offen lds\n"
MF "v[84:87],a[132:135],v[188:191],v[84:87]\n"
"s_add_u32 s60,s64,1\n"
TR "v[180:181],v0 offset:53248\n"
MF "v[84:87],a[172:175],a[240:243],v[84:87]\n"
"s_cmp_eq_u32 s60,8\n"
"s_cselect_b32 s60,0,s60\n"
TR "v[182:183],v0 offset:55296\n"
MF "v[84:87],a[212:215],a[244:247],v[84:87]\n"
LD "v193,s[48:51],s80 offen offset:1024 lds\n"
MF "v[76:79],a[208:211],a[244:247],v[76:79]\n"
"s_cmp_eq_u32 s60,10\n"
"s_cselect_b32 s60,s61,s60\n"
TR "v[184:185],v0 offset:57344\n"
MF "v[76:79],a[168:171],a[240:243],v[76:79]\n"
"s_sub_u32 s65,s60,s64\n"
TR "v[186:187],v0 offset:59392\n"
MF "v[76:79],a[128:131],v[188:191],v[76:79]\n"
"s_lshl_b32 s65,s65,7\n"
MF "v[68:71],a[124:127],v[188:191],v[68:71]\n"
"s_mov_b32 s64,s60\n"
MF "v[68:71],a[164:167],a[240:243],v[68:71]\n"
MF "v[68:71],a[204:207],a[244:247],v[68:71]\n"
LD "v194,s[48:51],s80 offen offset:2048 lds\n"
MF "v[60:63],a[200:203],a[244:247],v[60:63]\n"
MF "v[60:63],a[160:163],a[240:243],v[60:63]\n"
MF "v[60:63],a[120:123],v[188:191],v[60:63]\n"
"s_waitcnt lgkmcnt(0)\n"
MF "v[16:19],a[20:23],v[176:179],v[16:19]\n"
TR "v[188:189],v1 offset:49152\n"
MF "v[16:19],a[60:63],v[180:183],v[16:19]\n"
TR "v[190:191],v1 offset:51200\n"
MF "v[16:19],a[100:103],v[184:187],v[16:19]\n"
LD "v195,s[48:51],s80 offen offset:3072 lds\n"
MF "v[24:27],a[104:107],v[184:187],v[24:27]\n"
TR "a[240:241],v1 offset:53248\n"
MF "v[24:27],a[64:67],v[180:183],v[24:27]\n"
"s_add_u32 m0,s5,4096\n"
TR "a[242:243],v1 offset:55296\n"
MF "v[24:27],a[24:27],v[176:179],v[24:27]\n"
LD "v196,s[48:51],s80 offen lds\n"
MF "v[32:35],a[28:31],v[176:179],v[32:35]\n"
TR "a[244:245],v1 offset:57344\n"
MF "v[32:35],a[68:71],v[180:183],v[32:35]\n"
TR "a[246:247],v1 offset:59392\n"
MF "v[32:35],a[108:111],v[184:187],v[32:35]\n"
MF "v[40:43],a[112:115],v[184:187],v[40:43]\n"
MF "v[40:43],a[72:75],v[180:183],v[40:43]\n"
MF "v[40:43],a[32:35],v[176:179],v[40:43]\n"
LD "v197,s[48:51],s80 offen offset:1024 lds\n"
MF "v[48:51],a[36:39],v[176:179],v[48:51]\n"
MF "v[48:51],a[76:79],v[180:183],v[48:51]\n"
MF "v[48:51],a[116:119],v[184:187],v[48:51]\n"
"s_waitcnt lgkmcnt(0)\n"
MF "v[52:55],a[116:119],a[244:247],v[52:55]\n"
TR "v[176:177],v2 offset:49152\n"
MF "v[52:55],a[76:79],a[240:243],v[52:55]\n"
TR "v[178:179],v2 offset:51200\n"
MF "v[52:55],a[36:39],v[188:191],v[52:55]\n"
MF "v[44:47],a[32:35],v[188:191],v[44:47]\n"
TR "v[180:181],v2 offset:53248\n"
MF "v[44:47],a[72:75],a[240:243],v[44:47]\n"
TR "v[182:183],v2 offset:55296\n"
MF "v[44:47],a[112:115],a[244:247],v[44:47]\n"
MF "v[36:39],a[108:111],a[244:247],v[36:39]\n"
TR "v[184:185],v2 offset:57344\n"
MF "v[36:39],a[68:71],a[240:243],v[36:39]\n"
TR "v[186:187],v2 offset:59392\n"
MF "v[36:39],a[28:31],v[188:191],v[36:39]\n"
MF "v[28:31],a[24:27],v[188:191],v[28:31]\n"
MF "v[28:31],a[64:67],a[240:243],v[28:31]\n"
MF "v[28:31],a[104:107],a[244:247],v[28:31]\n"
MF "v[20:23],a[100:103],a[244:247],v[20:23]\n"
MF "v[20:23],a[60:63],a[240:243],v[20:23]\n"
"s_waitcnt vmcnt(9)\n"
MF "v[20:23],a[20:23],v[188:191],v[20:23]\n"
"v_mov_b32 v236,v228\n"
"v_mov_b32 v237,v229\n"
MF "v[56:59],a[252:255],v[208:211],v[56:59]\n"
MF "v[64:67],a[252:255],v[212:215],v[64:67]\n"
MF "v[72:75],a[252:255],v[216:219],v[72:75]\n"
MF "v[80:83],a[252:255],v[220:223],v[80:83]\n"
MF "v[88:91],a[252:255],v[224:227],v[88:91]\n"
MF "v[60:63],a[248:251],v[208:211],v[60:63]\n"
MF "v[68:71],a[248:251],v[212:215],v[68:71]\n"
MF "v[76:79],a[248:251],v[216:219],v[76:79]\n"
MF "v[84:87],a[248:251],v[220:223],v[84:87]\n"
MF "v[92:95],a[248:251],v[224:227],v[92:95]\n"
MF "v[248:251],v[230:233],a[250:253],v[248:251]\n"
MF "v[244:247],v[234:237],a[250:253],v[244:247]\n"
"s_waitcnt lgkmcnt(0)\n"
MF "v[56:59],a[20:23],v[176:179],v[56:59]\n"
TR "v[188:189],v3 offset:49152\n"
"v_sub_u32 v10,v10,v11\n"
MF "v[56:59],a[60:63],v[180:183],v[56:59]\n"
TR "v[190:191],v3 offset:51200\n"
MF "v[56:59],a[100:103],v[184:187],v[56:59]\n"
"v_mov_b32 v14,s76\n"
"s_add_u32 s76,s76,1\n"
MF "v[64:67],a[104:107],v[184:187],v[64:67]\n"
TR "a[240:241],v3 offset:53248\n"
MF "v[64:67],a[64:67],v[180:183],v[64:67]\n"
TR "a[242:243],v3 offset:55296\n"
MF "v[64:67],a[24:27],v[176:179],v[64:67]\n"
".Lnf2:\n"
"v_readlane_b32 s58,v10,s67\n"
"s_cmp_lt_u32 s58,s72\n"
"s_cbranch_scc1 .Lsp2\n"
".Lnb2:\n"
MF "v[72:75],a[28:31],v[176:179],v[72:75]\n"
"s_add_u32 s70,s70,20480\n"
TR "a[244:245],v3 offset:57344\n"
MF "v[72:75],a[68:71],v[180:183],v[72:75]\n"
TR "a[246:247],v3 offset:59392\n"
MF "v[72:75],a[108:111],v[184:187],v[72:75]\n"
LD "v[208:211],v9,s[40:43],s82 offen sc1\n"
MF "v[80:83],a[112:115],v[184:187],v[80:83]\n"
MF "v[80:83],a[72:75],v[180:183],v[80:83]\n"
LD "v[212:215],v9,s[40:43],s82 offen offset:1024 sc1\n"
MF "v[80:83],a[32:35],v[176:179],v[80:83]\n"
MF "v[88:91],a[36:39],v[176:179],v[88:91]\n"
MF "v[88:91],a[76:79],v[180:183],v[88:91]\n"
"s_waitcnt vmcnt(10) lgkmcnt(0)\n"
LD "v[216:219],v9,s[40:43],s82 offen offset:2048 sc1\n"
MF "v[88:91],a[116:119],v[184:187],v[88:91]\n"
"s_barrier\n"
MF "v[92:95],a[116:119],a[244:247],v[92:95]\n"
TR "v[176:177],v4\n"
MF "v[92:95],a[76:79],a[240:243],v[92:95]\n"
TR "v[178:179],v4 offset:2048\n"
MF "v[92:95],a[36:39],v[188:191],v[92:95]\n"
"buffer_store_dword v14,v15,s[40:43],s94 offen sc1\n"
MF "v[84:87],a[32:35],v[188:191],v[84:87]\n"
TR "v[180:181],v4 offset:4096\n"
MF "v[84:87],a[72:75],a[240:243],v[84:87]\n"
TR "v[182:183],v4 offset:6144\n"
MF "v[84:87],a[112:115],a[244:247],v[84:87]\n"
LD "v[220:223],v9,s[40:43],s82 offen offset:3072 sc1\n"
MF "v[76:79],a[108:111],a[244:247],v[76:79]\n"
TR "v[184:185],v4 offset:8192\n"
MF "v[76:79],a[68:71],a[240:243],v[76:79]\n"
"s_add_u32 s82,s82,4096\n"
TR "v[186:187],v4 offset:10240\n"
MF "v[76:79],a[28:31],v[188:191],v[76:79]\n"
LD "v[224:227],v9,s[40:43],s82 offen sc1\n"
MF "v[68:71],a[24:27],v[188:191],v[68:71]\n"
"s_add_u32 s2,s2,204800\n"
MF "v[68:71],a[64:67],a[240:243],v[68:71]\n"
"s_add_u32 m0,s5,73728\n"
MF "v[68:71],a[104:107],a[244:247],v[68:71]\n"
LD "v[228:231],v9,s[40:43],s2 offen sc1\n"
MF "v[60:63],a[100:103],a[244:247],v[60:63]\n"
MF "v[60:63],a[60:63],a[240:243],v[60:63]\n"
"s_add_u32 s2,s2,204800\n"
MF "v[60:63],a[20:23],v[188:191],v[60:63]\n"
"s_waitcnt lgkmcnt(0)\n"
LD "v[232:235],v9,s[40:43],s2 offen sc1\n"
MF "v[16:19],a[140:143],v[176:179],v[16:19]\n"
"s_nop 0\n"
TR "v[188:189],v5\n"
TR "v[190:191],v5 offset:2048\n"
MF "v[16:19],a[180:183],v[180:183],v[16:19]\n"
"s_nop 0\n"
TR "a[240:241],v5 offset:4096\n"
TR "a[242:243],v5 offset:6144\n"
MF "v[16:19],a[220:223],v[184:187],v[16:19]\n"
"s_nop 0\n"
TR "a[244:245],v5 offset:8192\n"
TR "a[246:247],v5 offset:10240\n"
MF "v[24:27],a[224:227],v[184:187],v[24:27]\n"
MF "v[24:27],a[184:187],v[180:183],v[24:27]\n"
MF "v[24:27],a[144:147],v[176:179],v[24:27]\n"
"s_nop 0\n"
"s_nop 0\n"
"ds_write_addtid_b32 v16\n"
"ds_write_addtid_b32 v17 offset:256\n"
MF "v[32:35],a[148:151],v[176:179],v[32:35]\n"
"s_nop 0\n"
"ds_write_addtid_b32 v18 offset:512\n"
"ds_write_addtid_b32 v19 offset:768\n"
MF "v[32:35],a[188:191],v[180:183],v[32:35]\n"
MF "v[32:35],a[228:231],v[184:187],v[32:35]\n"
"s_nop 0\n"
"ds_write_addtid_b32 v24 offset:2048\n"
"ds_write_addtid_b32 v25 offset:2304\n"
MF "v[40:43],a[232:235],v[184:187],v[40:43]\n"
"s_nop 0\n"
"ds_write_addtid_b32 v26 offset:2560\n"
"ds_write_addtid_b32 v27 offset:2816\n"
MF "v[40:43],a[192:195],v[180:183],v[40:43]\n"
MF "v[40:43],a[152:155],v[176:179],v[40:43]\n"
"s_nop 0\n"
"ds_write_addtid_b32 v32 offset:4096\n"
"ds_write_addtid_b32 v33 offset:4352\n"
MF "v[48:51],a[156:159],v[176:179],v[48:51]\n"
"s_nop 0\n"
"ds_write_addtid_b32 v34 offset:4608\n"
"ds_write_addtid_b32 v35 offset:4864\n"
MF "v[48:51],a[196:199],v[180:183],v[48:51]\n"
MF "v[48:51],a[236:239],v[184:187],v[48:51]\n"
"s_waitcnt lgkmcnt(12)\n"
"ds_write_addtid_b32 v40 offset:50176\n"
"ds_write_addtid_b32 v41 offset:50432\n"
MF "v[52:55],a[236:239],a[244:247],v[52:55]\n"
"s_nop 0\n"
TR "v[176:177],v6\n"
TR "v[178:179],v6 offset:2048\n"
MF "v[52:55],a[196:199],a[240:243],v[52:55]\n"
"s_nop 0\n"
TR "v[180:181],v6 offset:4096\n"
TR "v[182:183],v6 offset:6144\n"
MF "v[52:55],a[156:159],v[188:191],v[52:55]\n"
"s_nop 0\n"
TR "v[184:185],v6 offset:8192\n"
TR "v[186:187],v6 offset:10240\n"
MF "v[44:47],a[152:155],v[188:191],v[44:47]\n"
"s_nop 0\n"
"ds_write_addtid_b32 v42 offset:50688\n"
"ds_write_addtid_b32 v43 offset:50944\n"
MF "v[44:47],a[192:195],a[240:243],v[44:47]\n"
"s_nop 0\n"
"ds_write_addtid_b32 v48 offset:52224\n"
"ds_write_addtid_b32 v49 offset:52480\n"
MF "v[44:47],a[232:235],a[244:247],v[44:47]\n"
"s_nop 0\n"
"ds_write_addtid_b32 v50 offset:52736\n"
"ds_write_addtid_b32 v51 offset:52992\n"
MF "v[36:39],a[228:231],a[244:247],v[36:39]\n"
"s_nop 0\n"
"ds_write_addtid_b32 v52 offset:53248\n"
"ds_write_addtid_b32 v53 offset:53504\n"
MF "v[36:39],a[188:191],a[240:243],v[36:39]\n"
"s_nop 0\n"
"ds_write_addtid_b32 v54 offset:53760\n"
"ds_write_addtid_b32 v55 offset:54016\n"
MF "v[36:39],a[148:151],v[188:191],v[36:39]\n"
"s_nop 0\n"
"ds_write_addtid_b32 v44 offset:51200\n"
"ds_write_addtid_b32 v45 offset:51456\n"
MF "v[28:31],a[144:147],v[188:191],v[28:31]\n"
"s_nop 0\n"
"ds_write_addtid_b32 v46 offset:51712\n"
"ds_write_addtid_b32 v47 offset:51968\n"
MF "v[28:31],a[184:187],a[240:243],v[28:31]\n"
MF "v[28:31],a[224:227],a[244:247],v[28:31]\n"
"s_nop 0\n"
"ds_write_addtid_b32 v36 offset:49152\n"
"ds_write_addtid_b32 v37 offset:49408\n"
MF "v[20:23],a[220:223],a[244:247],v[20:23]\n"
"s_nop 0\n"
"ds_write_addtid_b32 v38 offset:49664\n"
"ds_write_addtid_b32 v39 offset:49920\n"
MF "v[20:23],a[180:183],a[240:243],v[20:23]\n"
MF "v[20:23],a[140:143],v[188:191],v[20:23]\n"
"s_waitcnt lgkmcnt(15)\n"
"ds_write_addtid_b32 v28 offset:3072\n"
"ds_write_addtid_b32 v29 offset:3328\n"
MF "v[56:59],a[140:143],v[176:179],v[56:59]\n"
"s_nop 0\n"
TR "v[188:189],v7\n"
TR "v[190:191],v7 offset:2048\n"
MF "v[56:59],a[180:183],v[180:183],v[56:59]\n"
"s_nop 0\n"
TR "a[240:241],v7 offset:4096\n"
TR "a[242:243],v7 offset:6144\n"
MF "v[56:59],a[220:223],v[184:187],v[56:59]\n"
"s_nop 0\n"
TR "a[244:245],v7 offset:8192\n"
TR "a[246:247],v7 offset:10240\n"
MF "v[64:67],a[224:227],v[184:187],v[64:67]\n"
"s_nop 0\n"
"ds_write_addtid_b32 v30 offset:3584\n"
"ds_write_addtid_b32 v31 offset:3840\n"
MF "v[64:67],a[184:187],v[180:183],v[64:67]\n"
"s_nop 0\n"
"ds_write_addtid_b32 v20 offset:1024\n"
"ds_write_addtid_b32 v21 offset:1280\n"
MF "v[64:67],a[144:147],v[176:179],v[64:67]\n"
"s_nop 0\n"
"ds_write_addtid_b32 v22 offset:1536\n"
"ds_write_addtid_b32 v23 offset:1792\n"
MF "v[72:75],a[148:151],v[176:179],v[72:75]\n"
"s_add_u32 m0,s5,49152\n"
MF "v[72:75],a[188:191],v[180:183],v[72:75]\n"
LD "v192,s[48:51],s88 offen lds\n"
MF "v[72:75],a[228:231],v[184:187],v[72:75]\n"
MF "v[80:83],a[232:235],v[184:187],v[80:83]\n"
MF "v[80:83],a[192:195],v[180:183],v[80:83]\n"
MF "v[80:83],a[152:155],v[176:179],v[80:83]\n"
MF "v[88:91],a[156:159],v[176:179],v[88:91]\n"
LD "v193,s[48:51],s88 offen offset:1024 lds\n"
MF "v[88:91],a[196:199],v[180:183],v[88:91]\n"
"s_waitcnt vmcnt(16) lgkmcnt(0)\n"
MF "v[88:91],a[236:239],v[184:187],v[88:91]\n"
"s_barrier\n"
MF "v[92:95],a[236:239],a[244:247],v[92:95]\n"
"s_nop 0\n"
TR "v[176:177],v0 offset:24576\n"
TR "v[178:179],v0 offset:26624\n"
MF "v[92:95],a[196:199],a[240:243],v[92:95]\n"
"s_add_u32 s85,s85,1\n"
TR "v[180:181],v0 offset:28672\n"
TR "v[182:183],v0 offset:30720\n"
MF "v[92:95],a[156:159],v[188:191],v[92:95]\n"
"s_nop 0\n"
TR "v[184:185],v0 offset:32768\n"
TR "v[186:187],v0 offset:34816\n"
MF "v[84:87],a[152:155],v[188:191],v[84:87]\n"
LD "v194,s[48:51],s88 offen offset:2048 lds\n"
MF "v[84:87],a[192:195],a[240:243],v[84:87]\n"
"ds_read2st64_b32 v[48:49],v13 offset0:204 offset1:205\n"
MF "v[84:87],a[232:235],a[244:247],v[84:87]\n"
"ds_read2st64_b32 v[50:51],v13 offset0:206 offset1:207\n"
MF "v[76:79],a[228:231],a[244:247],v[76:79]\n"
"ds_read2st64_b32 v[52:53],v13 offset0:208 offset1:209\n"
MF "v[76:79],a[188:191],a[240:243],v[76:79]\n"
LD "v195,s[48:51],s88 offen offset:3072 lds\n"
MF "v[76:79],a[148:151],v[188:191],v[76:79]\n"
"ds_read2st64_b32 v[54:55],v13 offset0:210 offset1:211\n"
MF "v[68:71],a[144:147],v[188:191],v[68:71]\n"
"ds_read2st64_b32 v[40:41],v13 offset0:196 offset1:197\n"
MF "v[68:71],a[184:187],a[240:243],v[68:71]\n"
"ds_read2st64_b32 v[42:43],v13 offset0:198 offset1:199\n"
MF "v[68:71],a[224:227],a[244:247],v[68:71]\n"
MF "v[60:63],a[220:223],a[244:247],v[60:63]\n"
MF "v[60:63],a[180:183],a[240:243],v[60:63]\n"
"s_cmp_lg_u32 s85,9\n"
MF "v[60:63],a[140:143],v[188:191],v[60:63]\n"
"s_waitcnt lgkmcnt(6)\n"
"s_cbranch_scc1 .Lps9\n"
MF "v[96:99],v[176:179],a[0:3],v[252:255]\n"
"s_nop 0\n"
TR "v[188:189],v1 offset:24576\n"
TR "v[190:191],v1 offset:26624\n"
MF "v[96:99],v[180:183],a[40:43],v[96:99]\n"
"s_nop 0\n"
TR "a[240:241],v1 offset:28672\n"
TR "a[242:243],v1 offset:30720\n"
MF "v[96:99],v[184:187],a[80:83],v[96:99]\n"
"s_waitcnt lgkmcnt(6)\n"
TR "a[244:245],v1 offset:32768\n"
TR "a[246:247],v1 offset:34816\n"
MF "v[104:107],v[184:187],a[84:87],v[252:255]\n"
"buffer_load_dword v10,v12,s[40:43],s86 offen sc1\n"
MF "v[104:107],v[180:183],a[44:47],v[104:107]\n"
"s_add_u32 m0,s5,53248\n"
"ds_read2st64_b32 v[44:45],v13 offset0:200 offset1:201\n"
"v_add_f32 v88,v88,v48\n"
MF "v[104:107],v[176:179],a[4:7],v[104:107]\n"
"ds_read2st64_b32 v[46:47],v13 offset0:202 offset1:203\n"
"v_add_f32 v89,v89,v49\n"
LD "v196,s[48:51],s88 offen lds\n"
MF "v[112:115],v[176:179],a[8:11],v[252:255]\n"
"ds_read2st64_b32 v[32:33],v13 offset0:16 offset1:17\n"
"v_add_f32 v90,v90,v50\n"
MF "v[112:115],v[180:183],a[48:51],v[112:115]\n"
"ds_read2st64_b32 v[34:35],v13 offset0:18 offset1:19\n"
"v_add_f32 v91,v91,v51\n"
MF "v[112:115],v[184:187],a[88:91],v[112:115]\n"
"ds_read2st64_b32 v[36:37],v13 offset0:192 offset1:193\n"
"v_add_f32 v92,v92,v52\n"
MF "v[120:123],v[184:187],a[92:95],v[252:255]\n"
"ds_read2st64_b32 v[38:39],v13 offset0:194 offset1:195\n"
"v_add_f32 v93,v93,v53\n"
MF "v[120:123],v[180:183],a[52:55],v[120:123]\n"
"ds_read2st64_b32 v[24:25],v13 offset0:8 offset1:9\n"
"v_add_f32 v94,v94,v54\n"
LD "v197,s[48:51],s88 offen offset:1024 lds\n"
MF "v[120:123],v[176:179],a[12:15],v[120:123]\n"
"ds_read2st64_b32 v[26:27],v13 offset0:10 offset1:11\n"
"v_add_f32 v95,v95,v55\n"
MF "v[128:131],v[176:179],a[16:19],v[252:255]\n"
"ds_read2st64_b32 v[28:29],v13 offset0:12 offset1:13\n"
CV "v88,v88,v89\n"
MF "v[128:131],v[180:183],a[56:59],v[128:131]\n"
"ds_read2st64_b32 v[30:31],v13 offset0:14 offset1:15\n"
CV "v89,v90,v91\n"
MF "v[128:131],v[184:187],a[96:99],v[128:131]\n"
"s_add_u32 s80,s88,122880\n"
"s_add_u32 m0,s5,98304\n"
"s_waitcnt lgkmcnt(10)\n"
"ds_read2st64_b32 v[16:17],v13 offset0:0 offset1:1\n"
CV "v90,v92,v93\n"
MF "v[132:135],a[244:247],a[96:99],v[252:255]\n"
"s_nop 0\n"
TR "v[176:177],v2 offset:24576\n"
TR "v[178:179],v2 offset:26624\n"
LD "v192,s[48:51],s80 offen lds\n"
MF "v[132:135],a[240:243],a[56:59],v[132:135]\n"
"s_nop 0\n"
TR "v[180:181],v2 offset:28672\n"
TR "v[182:183],v2 offset:30720\n"
MF "v[132:135],v[188:191],a[16:19],v[132:135]\n"
"s_nop 0\n"
TR "v[184:185],v2 offset:32768\n"
TR "v[186:187],v2 offset:34816\n"
MF "v[124:127],v[188:191],a[12:15],v[252:255]\n"
"s_waitcnt lgkmcnt(15)\n"
"ds_read2st64_b32 v[18:19],v13 offset0:2 offset1:3\n"
CV "v91,v94,v95\n"
MF "v[124:127],a[240:243],a[52:55],v[124:127]\n"
"ds_read2st64_b32 v[20:21],v13 offset0:4 offset1:5\n"
"v_add_f32 v80,v80,v40\n"
MF "v[124:127],a[244:247],a[92:95],v[124:127]\n"
"s_add_u32 s87,s90,4096\n"
"ds_read2st64_b32 v[22:23],v13 offset0:6 offset1:7\n"
"v_add_f32 v81,v81,v41\n"
LD "v193,s[48:51],s80 offen offset:1024 lds\n"
MF "v[116:119],a[244:247],a[88:91],v[252:255]\n"
"buffer_store_dwordx4 v[88:91],v9,s[40:43],s87 offen sc1\n"
MF "v[116:119],a[240:243],a[48:51],v[116:119]\n"
"v_add_f32 v82,v82,v42\n"
"v_add_f32 v83,v83,v43\n"
MF "v[116:119],v[188:191],a[8:11],v[116:119]\n"
"v_add_f32 v84,v84,v44\n"
"v_add_f32 v85,v85,v45\n"
MF "v[108:111],v[188:191],a[4:7],v[252:255]\n"
"v_add_f32 v86,v86,v46\n"
"v_add_f32 v87,v87,v47\n"
MF "v[108:111],a[240:243],a[44:47],v[108:111]\n"
CV "v80,v80,v81\n"
CV "v81,v82,v83\n"
LD "v194,s[48:51],s80 offen offset:2048 lds\n"
MF "v[108:111],a[244:247],a[84:87],v[108:111]\n"
"s_waitcnt lgkmcnt(14)\n"
CV "v82,v84,v85\n"
CV "v83,v86,v87\n"
MF "v[100:103],a[244:247],a[80:83],v[252:255]\n"
"buffer_store_dwordx4 v[80:83],v9,s[40:43],s90 offen offset:3072 sc1\n"
MF "v[100:103],a[240:243],a[40:43],v[100:103]\n"
"v_add_f32 v72,v72,v32\n"
"v_add_f32 v73,v73,v33\n"
MF "v[100:103],v[188:191],a[0:3],v[100:103]\n"
"s_waitcnt lgkmcnt(3)\n"
"v_add_f32 v74,v74,v34\n"
"v_add_f32 v75,v75,v35\n"
MF "v[136:139],v[176:179],a[0:3],v[252:255]\n"
"s_nop 0\n"
TR "v[188:189],v3 offset:24576\n"
TR "v[190:191],v3 offset:26624\n"
LD "v195,s[48:51],s80 offen offset:3072 lds\n"
MF "v[136:139],v[180:183],a[40:43],v[136:139]\n"
"s_nop 0\n"
TR "a[240:241],v3 offset:28672\n"
TR "a[242:243],v3 offset:30720\n"
MF "v[136:139],v[184:187],a[80:83],v[136:139]\n"
"s_nop 0\n"
TR "a[244:245],v3 offset:32768\n"
TR "a[246:247],v3 offset:34816\n"
MF "v[144:147],v[184:187],a[84:87],v[252:255]\n"
"v_add_f32 v76,v76,v36\n"
"v_add_f32 v77,v77,v37\n"
MF "v[144:147],v[180:183],a[44:47],v[144:147]\n"
".Lpg30h:\n"
"s_cbranch_execz .Lisr4\n"
"s_add_u32 m0,s5,102400\n"
"v_add_f32 v78,v78,v38\n"
"v_add_f32 v79,v79,v39\n"
MF "v[144:147],v[176:179],a[4:7],v[144:147]\n"
CV "v72,v72,v73\n"
CV "v73,v74,v75\n"
LD "v196,s[48:51],s80 offen lds\n"
MF "v[152:155],v[176:179],a[8:11],v[252:255]\n"
CV "v74,v76,v77\n"
CV "v75,v78,v79\n"
MF "v[152:155],v[180:183],a[48:51],v[152:155]\n"
"buffer_store_dwordx4 v[72:75],v9,s[40:43],s90 offen offset:2048 sc1\n"
MF "v[152:155],v[184:187],a[88:91],v[152:155]\n"
"v_add_f32 v64,v64,v24\n"
"v_add_f32 v65,v65,v25\n"
MF "v[160:163],v[184:187],a[92:95],v[252:255]\n"
"v_add_f32 v66,v66,v26\n"
"v_add_f32 v67,v67,v27\n"
MF "v[160:163],v[180:183],a[52:55],v[160:163]\n"
"v_add_f32 v68,v68,v28\n"
"v_add_f32 v69,v69,v29\n"
LD "v197,s[48:51],s80 offen offset:1024 lds\n"
MF "v[160:163],v[176:179],a[12:15],v[160:163]\n"
"v_add_f32 v70,v70,v30\n"
"v_add_f32 v71,v71,v31\n"
MF "v[168:171],v[176:179],a[16:19],v[252:255]\n"
CV "v64,v64,v65\n"
CV "v65,v66,v67\n"
MF "v[168:171],v[180:183],a[56:59],v[168:171]\n"
"s_waitcnt vmcnt(24) lgkmcnt(0)\n"
CV "v66,v68,v69\n"
CV "v67,v70,v71\n"
MF "v[168:171],v[184:187],a[96:99],v[168:171]\n"
"s_barrier\n"
MF "v[172:175],a[244:247],a[96:99],v[252:255]\n"
TR "v[176:177],v0\n"
"v_add_f32 v56,v56,v16\n"
MF "v[172:175],a[240:243],a[56:59],v[172:175]\n"
TR "v[178:179],v0 offset:2048\n"
"v_add_f32 v57,v57,v17\n"
MF "v[172:175],v[188:191],a[16:19],v[172:175]\n"
"buffer_store_dwordx4 v[64:67],v9,s[40:43],s90 offen offset:1024 sc1\n"
MF "v[164:167],v[188:191],a[12:15],v[252:255]\n"
TR "v[180:181],v0 offset:4096\n"
"v_add_f32 v58,v58,v18\n"
MF "v[164:167],a[240:243],a[52:55],v[164:167]\n"
TR "v[182:183],v0 offset:6144\n"
"v_add_f32 v59,v59,v19\n"
MF "v[164:167],a[244:247],a[92:95],v[164:167]\n"
"v_add_f32 v60,v60,v20\n"
"v_add_f32 v61,v61,v21\n"
MF "v[156:159],a[244:247],a[88:91],v[252:255]\n"
TR "v[184:185],v0 offset:8192\n"
"v_add_f32 v62,v62,v22\n"
MF "v[156:159],a[240:243],a[48:51],v[156:159]\n"
TR "v[186:187],v0 offset:10240\n"
"v_add_f32 v63,v63,v23\n"
MF "v[156:159],v[188:191],a[8:11],v[156:159]\n"
CV "v56,v56,v57\n"
CV "v57,v58,v59\n"
MF "v[148:151],v[188:191],a[4:7],v[252:255]\n"
CV "v58,v60,v61\n"
CV "v59,v62,v63\n"
MF "v[148:151],a[240:243],a[44:47],v[148:151]\n"
"buffer_store_dwordx4 v[56:59],v9,s[40:43],s90 offen sc1\n"
MF "v[148:151],a[244:247],a[84:87],v[148:151]\n"
"s_add_u32 s90,s90,20480\n"
MF "v[140:143],a[244:247],a[80:83],v[252:255]\n"
MF "v[140:143],a[240:243],a[40:43],v[140:143]\n"
"s_waitcnt vmcnt(13)\n"
"v_sub_u32 v10,v10,v11\n"
MF "v[140:143],v[188:191],a[0:3],v[140:143]\n"
"s_waitcnt lgkmcnt(0)\n"
"v_readlane_b32 s16,v10,s16\n"
MF "v[96:99],v[176:179],a[120:123],v[96:99]\n"
"s_cmp_lt_u32 s16,7\n"
"s_cselect_b32 s16,0xf0000000,0\n"
TR "v[188:189],v1\n"
MF "v[96:99],v[180:183],a[160:163],v[96:99]\n"
TR "v[190:191],v1 offset:2048\n"
"v_readlane_b32 s17,v10,s17\n"
MF "v[96:99],v[184:187],a[200:203],v[96:99]\n"
"s_cmp_lt_u32 s17,8\n"
"s_cselect_b32 s17,0xf0000000,0\n"
MF "v[104:107],v[184:187],a[204:207],v[104:107]\n"
TR "a[240:241],v1 offset:4096\n"
MF "v[104:107],v[180:183],a[164:167],v[104:107]\n"
"s_or_b32 s62,s6,s16\n"
TR "a[242:243],v1 offset:6144\n"
MF "v[104:107],v[176:179],a[124:127],v[104:107]\n"
LD "a[0:3],v9,s[40:43],s62 offen sc1\n"
MF "v[112:115],v[176:179],a[128:131],v[112:115]\n"
TR "a[244:245],v1 offset:8192\n"
MF "v[112:115],v[180:183],a[168:171],v[112:115]\n"
TR "a[246:247],v1 offset:10240\n"
MF "v[112:115],v[184:187],a[208:211],v[112:115]\n"
MF "v[120:123],v[184:187],a[212:215],v[120:123]\n"
LD "a[4:7],v9,s[40:43],s62 offen offset:1024 sc1\n"
MF "v[120:123],v[180:183],a[172:175],v[120:123]\n"
MF "v[120:123],v[176:179],a[132:135],v[120:123]\n"
MF "v[128:131],v[176:179],a[136:139],v[128:131]\n"
MF "v[128:131],v[180:183],a[176:179],v[128:131]\n"
LD "a[8:11],v9,s[40:43],s62 offen offset:2048 sc1\n"
MF "v[128:131],v[184:187],a[216:219],v[128:131]\n"
"s_waitcnt vmcnt(21)\n"
"v_mov_b32 v236,v228\n"
"v_mov_b32 v237,v229\n"
MF "v[136:139],v[208:211],a[252:255],v[136:139]\n"
MF "v[144:147],v[212:215],a[252:255],v[144:147]\n"
MF "v[152:155],v[216:219],a[252:255],v[152:155]\n"
MF "v[160:163],v[220:223],a[252:255],v[160:163]\n"
MF "v[168:171],v[224:227],a[252:255],v[168:171]\n"
MF "v[140:143],v[208:211],a[248:251],v[140:143]\n"
MF "v[148:151],v[212:215],a[248:251],v[148:151]\n"
MF "v[156:159],v[216:219],a[248:251],v[156:159]\n"
MF "v[164:167],v[220:223],a[248:251],v[164:167]\n"
MF "v[172:175],v[224:227],a[248:251],v[172:175]\n"
MF "v[248:251],v[230:233],a[250:253],v[248:251]\n"
MF "v[244:247],v[234:237],a[250:253],v[244:247]\n"
"s_waitcnt lgkmcnt(0)\n"
MF "v[132:135],a[244:247],a[216:219],v[132:135]\n"
TR "v[176:177],v2\n"
MF "v[132:135],a[240:243],a[176:179],v[132:135]\n"
TR "v[178:179],v2 offset:2048\n"
MF "v[132:135],v[188:191],a[136:139],v[132:135]\n"
LD "a[12:15],v9,s[40:43],s62 offen offset:3072 sc1\n"
MF "v[124:127],v[188:191],a[132:135],v[124:127]\n"
TR "v[180:181],v2 offset:4096\n"
CV "v244,v244,v245\n"
MF "v[124:127],a[240:243],a[172:175],v[124:127]\n"
TR "v[182:183],v2 offset:6144\n"
CV "v245,v246,v247\n"
MF "v[124:127],a[244:247],a[212:215],v[124:127]\n"
CV "v246,v248,v249\n"
CV "v247,v250,v251\n"
MF "v[116:119],a[244:247],a[208:211],v[116:119]\n"
TR "v[184:185],v2 offset:8192\n"
"v_permlane16_swap_b32 v244,v246\n"
MF "v[116:119],a[240:243],a[168:171],v[116:119]\n"
"s_add_u32 s62,s62,4096\n"
TR "v[186:187],v2 offset:10240\n"
"v_permlane16_swap_b32 v245,v247\n"
MF "v[116:119],v[188:191],a[128:131],v[116:119]\n"
"buffer_store_dwordx4 v[244:247],v8,s[44:47],s0 offen sc1\n"
MF "v[108:111],v[188:191],a[124:127],v[108:111]\n"
LD "a[16:19],v9,s[40:43],s62 offen sc1\n"
MF "v[108:111],a[240:243],a[164:167],v[108:111]\n"
MF "v[108:111],a[244:247],a[204:207],v[108:111]\n"
MF "v[100:103],a[244:247],a[200:203],v[100:103]\n"
MF "v[100:103],a[240:243],a[160:163],v[100:103]\n"
"s_or_b32 s62,s7,s17\n"
MF "v[100:103],v[188:191],a[120:123],v[100:103]\n"
"s_waitcnt lgkmcnt(0)\n"
LD "a[40:43],v9,s[40:43],s62 offen sc1\n"
MF "v[136:139],v[176:179],a[120:123],v[136:139]\n"
TR "v[188:189],v3\n"
MF "v[136:139],v[180:183],a[160:163],v[136:139]\n"
TR "v[190:191],v3 offset:2048\n"
MF "v[136:139],v[184:187],a[200:203],v[136:139]\n"
MF "v[144:147],v[184:187],a[204:207],v[144:147]\n"
TR "a[240:241],v3 offset:4096\n"
MF "v[144:147],v[180:183],a[164:167],v[144:147]\n"
TR "a[242:243],v3 offset:6144\n"
MF "v[144:147],v[176:179],a[124:127],v[144:147]\n"
LD "a[44:47],v9,s[40:43],s62 offen offset:1024 sc1\n"
MF "v[152:155],v[176:179],a[128:131],v[152:155]\n"
TR "a[244:245],v3 offset:8192\n"
MF "v[152:155],v[180:183],a[168:171],v[152:155]\n"
TR "a[246:247],v3 offset:10240\n"
MF "v[152:155],v[184:187],a[208:211],v[152:155]\n"
MF "v[160:163],v[184:187],a[212:215],v[160:163]\n"
LD "a[48:51],v9,s[40:43],s62 offen offset:2048 sc1\n"
MF "v[160:163],v[180:183],a[172:175],v[160:163]\n"
MF "v[160:163],v[176:179],a[132:135],v[160:163]\n"
MF "v[168:171],v[176:179],a[136:139],v[168:171]\n"
MF "v[168:171],v[180:183],a[176:179],v[168:171]\n"
"s_waitcnt vmcnt(20) lgkmcnt(0)\n"
LD "a[52:55],v9,s[40:43],s62 offen offset:3072 sc1\n"
MF "v[168:171],v[184:187],a[216:219],v[168:171]\n"
"s_barrier\n"
MF "v[172:175],a[244:247],a[216:219],v[172:175]\n"
TR "v[176:177],v0 offset:49152\n"
MF "v[172:175],a[240:243],a[176:179],v[172:175]\n"
TR "v[178:179],v0 offset:51200\n"
"v_mov_b32 v14,s76\n"
MF "v[172:175],v[188:191],a[136:139],v[172:175]\n"
"s_add_u32 s76,s76,1\n"
MF "v[164:167],v[188:191],a[132:135],v[164:167]\n"
TR "v[180:181],v0 offset:53248\n"
MF "v[164:167],a[240:243],a[172:175],v[164:167]\n"
"s_add_u32 s62,s62,4096\n"
TR "v[182:183],v0 offset:55296\n"
MF "v[164:167],a[244:247],a[212:215],v[164:167]\n"
LD "a[56:59],v9,s[40:43],s62 offen sc1\n"
MF "v[156:159],a[244:247],a[208:211],v[156:159]\n"
TR "v[184:185],v0 offset:57344\n"
MF "v[156:159],a[240:243],a[168:171],v[156:159]\n"
TR "v[186:187],v0 offset:59392\n"
MF "v[156:159],v[188:191],a[128:131],v[156:159]\n"
MF "v[148:151],v[188:191],a[124:127],v[148:151]\n"
MF "v[148:151],a[240:243],a[164:167],v[148:151]\n"
MF "v[148:151],a[244:247],a[204:207],v[148:151]\n"
MF "v[140:143],a[244:247],a[200:203],v[140:143]\n"
"s_waitcnt vmcnt(11) lgkmcnt(0)\n"
MF "v[140:143],a[240:243],a[160:163],v[140:143]\n"
"s_barrier\n"
MF "v[140:143],v[188:191],a[120:123],v[140:143]\n"
"buffer_store_dword v14,v15,s[40:43],s94 offen sc1\n"
MF "v[96:99],v[176:179],a[20:23],v[96:99]\n"
TR "v[188:189],v1 offset:49152\n"
MF "v[96:99],v[180:183],a[60:63],v[96:99]\n"
TR "v[190:191],v1 offset:51200\n"
MF "v[96:99],v[184:187],a[100:103],v[96:99]\n"
LD "a[80:83],v9,s[40:43],s3 offen sc1\n"
MF "v[104:107],v[184:187],a[104:107],v[104:107]\n"
TR "a[240:241],v1 offset:53248\n"
MF "v[104:107],v[180:183],a[64:67],v[104:107]\n"
"s_add_u32 s3,s3,204800\n"
TR "a[242:243],v1 offset:55296\n"
MF "v[104:107],v[176:179],a[24:27],v[104:107]\n"
LD "a[84:87],v9,s[40:43],s3 offen sc1\n"
MF "v[112:115],v[176:179],a[28:31],v[112:115]\n"
TR "a[244:245],v1 offset:57344\n"
MF "v[112:115],v[180:183],a[68:71],v[112:115]\n"
"s_add_u32 s3,s3,204800\n"
TR "a[246:247],v1 offset:59392\n"
MF "v[112:115],v[184:187],a[108:111],v[112:115]\n"
LD "a[88:91],v9,s[40:43],s3 offen sc1\n"
MF "v[120:123],v[184:187],a[112:115],v[120:123]\n"
MF "v[120:123],v[180:183],a[72:75],v[120:123]\n"
"s_add_u32 s3,s3,204800\n"
MF "v[120:123],v[176:179],a[32:35],v[120:123]\n"
LD "a[92:95],v9,s[40:43],s3 offen sc1\n"
MF "v[128:131],v[176:179],a[36:39],v[128:131]\n"
"s_bfe_u32 s60,s22,0x10014\n"
MF "v[128:131],v[180:183],a[76:79],v[128:131]\n"
"s_add_u32 s3,s3,204800\n"
MF "v[128:131],v[184:187],a[116:119],v[128:131]\n"
"s_waitcnt lgkmcnt(0)\n"
LD "a[120:123],v9,s[40:43],s3 offen sc1\n"
MF "v[132:135],a[244:247],a[116:119],v[132:135]\n"
"s_lshl_b32 s60,s60,5\n"
TR "v[176:177],v2 offset:49152\n"
MF "v[132:135],a[240:243],a[76:79],v[132:135]\n"
"s_add_u32 s3,s3,204800\n"
TR "v[178:179],v2 offset:51200\n"
"v_xor_b32 v242,s60,v8\n"
MF "v[132:135],v[188:191],a[36:39],v[132:135]\n"
LD "a[124:127],v9,s[40:43],s3 offen sc1\n"
MF "v[124:127],v[188:191],a[32:35],v[124:127]\n"
TR "v[180:181],v2 offset:53248\n"
MF "v[124:127],a[240:243],a[72:75],v[124:127]\n"
"s_add_u32 s3,s3,204800\n"
TR "v[182:183],v2 offset:55296\n"
MF "v[124:127],a[244:247],a[112:115],v[124:127]\n"
LD "a[128:131],v9,s[40:43],s3 offen sc1\n"
MF "v[116:119],a[244:247],a[108:111],v[116:119]\n"
TR "v[184:185],v2 offset:57344\n"
MF "v[116:119],a[240:243],a[68:71],v[116:119]\n"
"s_add_u32 s3,s3,204800\n"
TR "v[186:187],v2 offset:59392\n"
MF "v[116:119],v[188:191],a[28:31],v[116:119]\n"
LD "a[132:135],v9,s[40:43],s3 offen sc1\n"
MF "v[108:111],v[188:191],a[24:27],v[108:111]\n"
MF "v[108:111],a[240:243],a[64:67],v[108:111]\n"
MF "v[108:111],a[244:247],a[104:107],v[108:111]\n"
MF "v[100:103],a[244:247],a[100:103],v[100:103]\n"
MF "v[100:103],a[240:243],a[60:63],v[100:103]\n"
MF "v[100:103],v[188:191],a[20:23],v[100:103]\n"
"s_waitcnt lgkmcnt(0)\n"
MF "v[136:139],v[176:179],a[20:23],v[136:139]\n"
TR "v[188:189],v3 offset:49152\n"
MF "v[136:139],v[180:183],a[60:63],v[136:139]\n"
TR "v[190:191],v3 offset:51200\n"
MF "v[136:139],v[184:187],a[100:103],v[136:139]\n"
"buffer_load_dword v10,v12,s[40:43],s86 offen sc1\n"
MF "v[144:147],v[184:187],a[104:107],v[144:147]\n"
TR "a[240:241],v3 offset:53248\n"
MF "v[144:147],v[180:183],a[64:67],v[144:147]\n"
TR "a[242:243],v3 offset:55296\n"
MF "v[144:147],v[176:179],a[24:27],v[144:147]\n"
MF "v[152:155],v[176:179],a[28:31],v[152:155]\n"
TR "a[244:245],v3 offset:57344\n"
MF "v[152:155],v[180:183],a[68:71],v[152:155]\n"
TR "a[246:247],v3 offset:59392\n"
MF "v[152:155],v[184:187],a[108:111],v[152:155]\n"
MF "v[160:163],v[184:187],a[112:115],v[160:163]\n"
MF "v[160:163],v[180:183],a[72:75],v[160:163]\n"
MF "v[160:163],v[176:179],a[32:35],v[160:163]\n"
MF "v[168:171],v[176:179],a[36:39],v[168:171]\n"
MF "v[168:171],v[180:183],a[76:79],v[168:171]\n"
"s_waitcnt vmcnt(23) lgkmcnt(0)\n"
MF "v[168:171],v[184:187],a[116:119],v[168:171]\n"
"s_barrier\n"
MF "v[172:175],a[244:247],a[116:119],v[172:175]\n"
TR "v[176:177],v4\n"
MF "v[172:175],a[240:243],a[76:79],v[172:175]\n"
TR "v[178:179],v4 offset:2048\n"
MF "v[172:175],v[188:191],a[36:39],v[172:175]\n"
MF "v[164:167],v[188:191],a[32:35],v[164:167]\n"
TR "v[180:181],v4 offset:4096\n"
MF "v[164:167],a[240:243],a[72:75],v[164:167]\n"
TR "v[182:183],v4 offset:6144\n"
MF "v[164:167],a[244:247],a[112:115],v[164:167]\n"
"s_add_u32 m0,s5,73728\n"
MF "v[156:159],a[244:247],a[108:111],v[156:159]\n"
TR "v[184:185],v4 offset:8192\n"
MF "v[156:159],a[240:243],a[68:71],v[156:159]\n"
TR "v[186:187],v4 offset:10240\n"
MF "v[156:159],v[188:191],a[28:31],v[156:159]\n"
MF "v[148:151],v[188:191],a[24:27],v[148:151]\n"
MF "v[148:151],a[240:243],a[64:67],v[148:151]\n"
MF "v[148:151],a[244:247],a[104:107],v[148:151]\n"
MF "v[140:143],a[244:247],a[100:103],v[140:143]\n"
MF "v[140:143],a[240:243],a[60:63],v[140:143]\n"
MF "v[140:143],v[188:191],a[20:23],v[140:143]\n"
"s_waitcnt lgkmcnt(0)\n"
MF "v[96:99],v[176:179],a[140:143],v[96:99]\n"
"s_nop 0\n"
TR "v[188:189],v5\n"
TR "v[190:191],v5 offset:2048\n"
MF "v[96:99],v[180:183],a[180:183],v[96:99]\n"
"s_nop 0\n"
TR "a[240:241],v5 offset:4096\n"
TR "a[242:243],v5 offset:6144\n"
MF "v[96:99],v[184:187],a[220:223],v[96:99]\n"
"s_nop 0\n"
TR "a[244:245],v5 offset:8192\n"
TR "a[246:247],v5 offset:10240\n"
MF "v[104:107],v[184:187],a[224:227],v[104:107]\n"
MF "v[104:107],v[180:183],a[184:187],v[104:107]\n"
MF "v[104:107],v[176:179],a[144:147],v[104:107]\n"
"s_nop 0\n"
"s_nop 0\n"
"ds_write_addtid_b32 v96\n"
"ds_write_addtid_b32 v97 offset:256\n"
MF "v[112:115],v[176:179],a[148:151],v[112:115]\n"
"s_nop 0\n"
"ds_write_addtid_b32 v98 offset:512\n"
"ds_write_addtid_b32 v99 offset:768\n"
MF "v[112:115],v[180:183],a[188:191],v[112:115]\n"
MF "v[112:115],v[184:187],a[228:231],v[112:115]\n"
"s_nop 0\n"
"ds_write_addtid_b32 v104 offset:2048\n"
"ds_write_addtid_b32 v105 offset:2304\n"
MF "v[120:123],v[184:187],a[232:235],v[120:123]\n"
"s_nop 0\n"
"ds_write_addtid_b32 v106 offset:2560\n"
"ds_write_addtid_b32 v107 offset:2816\n"
MF "v[120:123],v[180:183],a[192:195],v[120:123]\n"
MF "v[120:123],v[176:179],a[152:155],v[120:123]\n"
"s_nop 0\n"
"ds_write_addtid_b32 v112 offset:4096\n"
"ds_write_addtid_b32 v113 offset:4352\n"
MF "v[128:131],v[176:179],a[156:159],v[128:131]\n"
"s_nop 0\n"
"ds_write_addtid_b32 v114 offset:4608\n"
"ds_write_addtid_b32 v115 offset:4864\n"
MF "v[128:131],v[180:183],a[196:199],v[128:131]\n"
MF "v[128:131],v[184:187],a[236:239],v[128:131]\n"
"s_waitcnt lgkmcnt(12)\n"
"ds_write_addtid_b32 v120 offset:50176\n"
"ds_write_addtid_b32 v121 offset:50432\n"
MF "v[132:135],a[244:247],a[236:239],v[132:135]\n"
"s_nop 0\n"
TR "v[176:177],v6\n"
TR "v[178:179],v6 offset:2048\n"
MF "v[132:135],a[240:243],a[196:199],v[132:135]\n"
"s_nop 0\n"
TR "v[180:181],v6 offset:4096\n"
TR "v[182:183],v6 offset:6144\n"
MF "v[132:135],v[188:191],a[156:159],v[132:135]\n"
"s_nop 0\n"
TR "v[184:185],v6 offset:8192\n"
TR "v[186:187],v6 offset:10240\n"
MF "v[124:127],v[188:191],a[152:155],v[124:127]\n"
"s_nop 0\n"
"ds_write_addtid_b32 v122 offset:50688\n"
"ds_write_addtid_b32 v123 offset:50944\n"
MF "v[124:127],a[240:243],a[192:195],v[124:127]\n"
"s_nop 0\n"
"ds_write_addtid_b32 v128 offset:52224\n"
"ds_write_addtid_b32 v129 offset:52480\n"
MF "v[124:127],a[244:247],a[232:235],v[124:127]\n"
"s_nop 0\n"
"ds_write_addtid_b32 v130 offset:52736\n"
"ds_write_addtid_b32 v131 offset:52992\n"
MF "v[116:119],a[244:247],a[228:231],v[116:119]\n"
"s_nop 0\n"
"ds_write_addtid_b32 v132 offset:53248\n"
"ds_write_addtid_b32 v133 offset:53504\n"
MF "v[116:119],a[240:243],a[188:191],v[116:119]\n"
"s_nop 0\n"
"ds_write_addtid_b32 v134 offset:53760\n"
"ds_write_addtid_b32 v135 offset:54016\n"
MF "v[116:119],v[188:191],a[148:151],v[116:119]\n"
"s_nop 0\n"
"ds_write_addtid_b32 v124 offset:51200\n"
"ds_write_addtid_b32 v125 offset:51456\n"
MF "v[108:111],v[188:191],a[144:147],v[108:111]\n"
"s_nop 0\n"
"ds_write_addtid_b32 v126 offset:51712\n"
"ds_write_addtid_b32 v127 offset:51968\n"
MF "v[108:111],a[240:243],a[184:187],v[108:111]\n"
MF "v[108:111],a[244:247],a[224:227],v[108:111]\n"
"s_nop 0\n"
"ds_write_addtid_b32 v116 offset:49152\n"
"ds_write_addtid_b32 v117 offset:49408\n"
MF "v[100:103],a[244:247],a[220:223],v[100:103]\n"
"s_nop 0\n"
"ds_write_addtid_b32 v118 offset:49664\n"
"ds_write_addtid_b32 v119 offset:49920\n"
MF "v[100:103],a[240:243],a[180:183],v[100:103]\n"
"s_waitcnt vmcnt(0)\n"
"v_sub_u32 v10,v10,v11\n"
MF "v[100:103],v[188:191],a[140:143],v[100:103]\n"
"s_waitcnt lgkmcnt(15)\n"
"ds_write_addtid_b32 v108 offset:3072\n"
"ds_write_addtid_b32 v109 offset:3328\n"
MF "v[136:139],v[176:179],a[140:143],v[136:139]\n"
"s_nop 0\n"
TR "v[188:189],v7\n"
TR "v[190:191],v7 offset:2048\n"
MF "v[136:139],v[180:183],a[180:183],v[136:139]\n"
"s_nop 0\n"
TR "a[240:241],v7 offset:4096\n"
TR "a[242:243],v7 offset:6144\n"
MF "v[136:139],v[184:187],a[220:223],v[136:139]\n"
"s_nop 0\n"
TR "a[244:245],v7 offset:8192\n"
TR "a[246:247],v7 offset:10240\n"
MF "v[144:147],v[184:187],a[224:227],v[144:147]\n"
"s_nop 0\n"
"ds_write_addtid_b32 v110 offset:3584\n"
"ds_write_addtid_b32 v111 offset:3840\n"
MF "v[144:147],v[180:183],a[184:187],v[144:147]\n"
"s_nop 0\n"
"ds_write_addtid_b32 v100 offset:1024\n"
"ds_write_addtid_b32 v101 offset:1280\n"
MF "v[144:147],v[176:179],a[144:147],v[144:147]\n"
"s_nop 0\n"
"ds_write_addtid_b32 v102 offset:1536\n"
"ds_write_addtid_b32 v103 offset:1792\n"
MF "v[152:155],v[176:179],a[148:151],v[152:155]\n"
"v_readlane_b32 s18,v10,s18\n"
MF "v[152:155],v[180:183],a[188:191],v[152:155]\n"
"s_cmp_lt_u32 s18,9\n"
"s_cselect_b32 s18,0xf0000000,0\n"
"s_or_b32 s62,s8,s18\n"
MF "v[152:155],v[184:187],a[228:231],v[152:155]\n"
LD "v[208:211],v9,s[40:43],s62 offen sc1\n"
MF "v[160:163],v[184:187],a[232:235],v[160:163]\n"
LD "v[212:215],v9,s[40:43],s62 offen offset:1024 sc1\n"
MF "v[160:163],v[180:183],a[192:195],v[160:163]\n"
LD "v[216:219],v9,s[40:43],s62 offen offset:2048 sc1\n"
MF "v[160:163],v[176:179],a[152:155],v[160:163]\n"
LD "v[220:223],v9,s[40:43],s62 offen offset:3072 sc1\n"
MF "v[168:171],v[176:179],a[156:159],v[168:171]\n"
"s_add_u32 s62,s62,4096\n"
MF "v[168:171],v[180:183],a[196:199],v[168:171]\n"
"s_waitcnt lgkmcnt(0)\n"
LD "v[224:227],v9,s[40:43],s62 offen sc1\n"
MF "v[168:171],v[184:187],a[236:239],v[168:171]\n"
"s_barrier\n"
MF "v[172:175],a[244:247],a[236:239],v[172:175]\n"
"buffer_load_dword v10,v12,s[40:43],s86 offen sc1\n"
MF "v[172:175],a[240:243],a[196:199],v[172:175]\n"
"ds_read2st64_b32 v[128:129],v13 offset0:204 offset1:205\n"
MF "v[172:175],v[188:191],a[156:159],v[172:175]\n"
"ds_read2st64_b32 v[130:131],v13 offset0:206 offset1:207\n"
MF "v[164:167],v[188:191],a[152:155],v[164:167]\n"
"ds_read2st64_b32 v[132:133],v13 offset0:208 offset1:209\n"
MF "v[164:167],a[240:243],a[192:195],v[164:167]\n"
"ds_read2st64_b32 v[134:135],v13 offset0:210 offset1:211\n"
MF "v[164:167],a[244:247],a[232:235],v[164:167]\n"
"ds_read2st64_b32 v[120:121],v13 offset0:196 offset1:197\n"
MF "v[156:159],a[244:247],a[228:231],v[156:159]\n"
"ds_read2st64_b32 v[122:123],v13 offset0:198 offset1:199\n"
MF "v[156:159],a[240:243],a[188:191],v[156:159]\n"
"ds_read2st64_b32 v[124:125],v13 offset0:200 offset1:201\n"
MF "v[156:159],v[188:191],a[148:151],v[156:159]\n"
"ds_read2st64_b32 v[126:127],v13 offset0:202 offset1:203\n"
MF "v[148:151],v[188:191],a[144:147],v[148:151]\n"
"ds_read2st64_b32 v[112:113],v13 offset0:16 offset1:17\n"
MF "v[148:151],a[240:243],a[184:187],v[148:151]\n"
"ds_read2st64_b32 v[114:115],v13 offset0:18 offset1:19\n"
MF "v[148:151],a[244:247],a[224:227],v[148:151]\n"
"ds_read2st64_b32 v[116:117],v13 offset0:192 offset1:193\n"
MF "v[140:143],a[244:247],a[220:223],v[140:143]\n"
"ds_read2st64_b32 v[118:119],v13 offset0:194 offset1:195\n"
MF "v[140:143],a[240:243],a[180:183],v[140:143]\n"
"ds_read2st64_b32 v[104:105],v13 offset0:8 offset1:9\n"
MF "v[140:143],v[188:191],a[140:143],v[140:143]\n"
"ds_read2st64_b32 v[106:107],v13 offset0:10 offset1:11\n"
"ds_read2st64_b32 v[108:109],v13 offset0:12 offset1:13\n"
"ds_read2st64_b32 v[110:111],v13 offset0:14 offset1:15\n"
"ds_read2st64_b32 v[96:97],v13 offset0:0 offset1:1\n"
"ds_read2st64_b32 v[98:99],v13 offset0:2 offset1:3\n"
"ds_read2st64_b32 v[100:101],v13 offset0:4 offset1:5\n"
"ds_read2st64_b32 v[102:103],v13 offset0:6 offset1:7\n"
"s_or_b32 s59,s16,s17\n"
"s_or_b32 s60,s59,s18\n"
"s_cmp_eq_u32 s60,0\n"
"s_cbranch_scc1 .Lhave1\n"
"s_waitcnt vmcnt(0)\n"
"v_sub_u32 v10,v10,v11\n"
"s_cmp_eq_u32 s16,0\n"
"s_cbranch_scc1 .Lhave3\n"
"s_add_u32 s72,s78,6\n"
"s_and_b32 s72,s72,15\n"
"s_nop 3\n"
".Lnf3:\n"
"v_readlane_b32 s58,v10,s72\n"
"s_cmp_lt_u32 s58,7\n"
"s_cbranch_scc1 .Lsp3\n"
".Lnb3:\n"
"s_or_b32 s62,s6,0\n"
LD "a[0:3],v9,s[40:43],s62 offen sc1\n"
LD "a[4:7],v9,s[40:43],s62 offen offset:1024 sc1\n"
LD "a[8:11],v9,s[40:43],s62 offen offset:2048 sc1\n"
LD "a[12:15],v9,s[40:43],s62 offen offset:3072 sc1\n"
"s_add_u32 s62,s62,4096\n"
LD "a[16:19],v9,s[40:43],s62 offen sc1\n"
".Lhave3:\n"
"s_cmp_eq_u32 s17,0\n"
"s_cbranch_scc1 .Lhave2\n"
"s_add_u32 s72,s78,4\n"
"s_and_b32 s72,s72,15\n"
"s_nop 3\n"
".Lnf4:\n"
"v_readlane_b32 s58,v10,s72\n"
"s_cmp_lt_u32 s58,8\n"
"s_cbranch_scc1 .Lsp4\n"
".Lnb4:\n"
"s_or_b32 s62,s7,0\n"
LD "a[40:43],v9,s[40:43],s62 offen sc1\n"
LD "a[44:47],v9,s[40:43],s62 offen offset:1024 sc1\n"
LD "a[48:51],v9,s[40:43],s62 offen offset:2048 sc1\n"
LD "a[52:55],v9,s[40:43],s62 offen offset:3072 sc1\n"
"s_add_u32 s62,s62,4096\n"
LD "a[56:59],v9,s[40:43],s62 offen sc1\n"
".Lhave2:\n"
"s_cmp_eq_u32 s18,0\n"
"s_cbranch_scc1 .Lhave1\n"
"s_add_u32 s72,s78,2\n"
"s_and_b32 s72,s72,15\n"
"s_nop 3\n"
".Lnf5:\n"
"v_readlane_b32 s58,v10,s72\n"
"s_cmp_lt_u32 s58,9\n"
"s_cbranch_scc1 .Lsp5\n"
".Lnb5:\n"
"s_or_b32 s62,s8,0\n"
LD "v[208:211],v9,s[40:43],s62 offen sc1\n"
LD "v[212:215],v9,s[40:43],s62 offen offset:1024 sc1\n"
LD "v[216:219],v9,s[40:43],s62 offen offset:2048 sc1\n"
LD "v[220:223],v9,s[40:43],s62 offen offset:3072 sc1\n"
"s_add_u32 s62,s62,4096\n"
LD "v[224:227],v9,s[40:43],s62 offen sc1\n"
".Lhave1:\n"
"s_cmp_eq_u32 s1,0xf0000000\n"
"s_cbranch_scc1 .Ljx\n"
"v_accvgpr_mov_b32 a96,a80\n"
"v_accvgpr_mov_b32 a97,a81\n"
"v_accvgpr_mov_b32 a136,a120\n"
"v_accvgpr_mov_b32 a137,a121\n"
MF "v[196:199],a[82:85],a[250:253],0\n"
MF "v[192:195],a[86:89],a[250:253],0\n"
MF "v[196:199],a[90:93],a[250:253],v[196:199]\n"
MF "v[192:195],a[94:97],a[250:253],v[192:195]\n"
MF "v[196:199],a[122:125],a[250:253],v[196:199]\n"
MF "v[192:195],a[126:129],a[250:253],v[192:195]\n"
MF "v[196:199],a[130:133],a[250:253],v[196:199]\n"
MF "v[192:195],a[134:137],a[250:253],v[192:195]\n"
"s_nop 7\n"
CV "v192,v192,v193\n"
CV "v193,v194,v195\n"
CV "v194,v196,v197\n"
CV "v195,v198,v199\n"
"s_nop 0\n"
".Lpg31h:\n"
"s_cbranch_execz .Lisr4\n"
"s_nop 0\n"
"v_permlane16_swap_b32 v192,v194\n"
"v_permlane16_swap_b32 v193,v195\n"
"buffer_store_dwordx4 v[192:195],v8,s[44:47],s1 offen sc1\n"
".Ljx:\n"
"s_waitcnt lgkmcnt(15)\n"
"v_pk_add_f32 v[168:169],v[168:169],v[128:129]\n"
"v_pk_add_f32 v[170:171],v[170:171],v[130:131]\n"
"v_pk_add_f32 v[172:173],v[172:173],v[132:133]\n"
"v_pk_add_f32 v[174:175],v[174:175],v[134:135]\n"
"v_pk_add_f32 v[160:161],v[160:161],v[120:121]\n"
"s_waitcnt lgkmcnt(14)\n"
"v_pk_add_f32 v[162:163],v[162:163],v[122:123]\n"
"s_waitcnt lgkmcnt(13)\n"
"v_pk_add_f32 v[164:165],v[164:165],v[124:125]\n"
"s_waitcnt lgkmcnt(12)\n"
"v_pk_add_f32 v[166:167],v[166:167],v[126:127]\n"
"s_waitcnt lgkmcnt(11)\n"
"v_pk_add_f32 v[152:153],v[152:153],v[112:113]\n"
"s_waitcnt lgkmcnt(10)\n"
"v_pk_add_f32 v[154:155],v[154:155],v[114:115]\n"
"s_waitcnt lgkmcnt(9)\n"
"v_pk_add_f32 v[156:157],v[156:157],v[116:117]\n"
"s_waitcnt lgkmcnt(8)\n"
"v_pk_add_f32 v[158:159],v[158:159],v[118:119]\n"
"s_waitcnt lgkmcnt(7)\n"
"v_pk_add_f32 v[144:145],v[144:145],v[104:105]\n"
"s_waitcnt lgkmcnt(6)\n"
"v_pk_add_f32 v[146:147],v[146:147],v[106:107]\n"
"s_waitcnt lgkmcnt(5)\n"
"v_pk_add_f32 v[148:149],v[148:149],v[108:109]\n"
"s_waitcnt lgkmcnt(4)\n"
"v_pk_add_f32 v[150:151],v[150:151],v[110:111]\n"
"s_waitcnt lgkmcnt(3)\n"
"v_pk_add_f32 v[136:137],v[136:137],v[96:97]\n"
"s_waitcnt lgkmcnt(2)\n"
"v_pk_add_f32 v[138:139],v[138:139],v[98:99]\n"
"s_waitcnt lgkmcnt(1)\n"
"v_pk_add_f32 v[140:141],v[140:141],v[100:101]\n"
"s_waitcnt lgkmcnt(0)\n"
"v_pk_add_f32 v[142:143],v[142:143],v[102:103]\n"
"s_cmp_eq_u32 s59,0\n"
"s_cbranch_scc1 .Lhw\n"
"s_waitcnt vmcnt(0)\n"
".Lhw:\n"
MF "v[140:143],a[0:3],a[252:255],v[140:143]\n"
MF "v[148:151],a[4:7],a[252:255],v[148:151]\n"
MF "v[156:159],a[8:11],a[252:255],v[156:159]\n"
MF "v[164:167],a[12:15],a[252:255],v[164:167]\n"
MF "v[172:175],a[16:19],a[252:255],v[172:175]\n"
MF "v[136:139],a[0:3],a[248:251],v[136:139]\n"
MF "v[144:147],a[4:7],a[248:251],v[144:147]\n"
MF "v[152:155],a[8:11],a[248:251],v[152:155]\n"
MF "v[160:163],a[12:15],a[248:251],v[160:163]\n"
MF "v[168:171],a[16:19],a[248:251],v[168:171]\n"
MF "v[136:139],a[40:43],a[252:255],v[136:139]\n"
MF "v[144:147],a[44:47],a[252:255],v[144:147]\n"
MF "v[152:155],a[48:51],a[252:255],v[152:155]\n"
MF "v[160:163],a[52:55],a[252:255],v[160:163]\n"
MF "v[168:171],a[56:59],a[252:255],v[168:171]\n"
MF "v[140:143],a[40:43],a[248:251],v[140:143]\n"
MF "v[148:151],a[44:47],a[248:251],v[148:151]\n"
MF "v[156:159],a[48:51],a[248:251],v[156:159]\n"
MF "v[164:167],a[52:55],a[248:251],v[164:167]\n"
MF "v[172:175],a[56:59],a[248:251],v[172:175]\n"
"s_waitcnt vmcnt(0)\n"
MF "v[140:143],v[208:211],a[252:255],v[140:143]\n"
MF "v[136:139],v[208:211],a[248:251],v[136:139]\n"
MF "v[148:151],v[212:215],a[252:255],v[148:151]\n"
MF "v[144:147],v[212:215],a[248:251],v[144:147]\n"
MF "v[156:159],v[216:219],a[252:255],v[156:159]\n"
"s_nop 4\n"
CV "v136,v136,v137\n"
CV "v137,v138,v139\n"
MF "v[152:155],v[216:219],a[248:251],v[152:155]\n"
CV "v138,v140,v141\n"
CV "v139,v142,v143\n"
MF "v[164:167],v[220:223],a[252:255],v[164:167]\n"
CV "v144,v144,v145\n"
CV "v145,v146,v147\n"
MF "v[160:163],v[220:223],a[248:251],v[160:163]\n"
CV "v146,v148,v149\n"
CV "v147,v150,v151\n"
"v_permlane16_swap_b32 v136,v138\n"
"v_permlane16_swap_b32 v137,v139\n"
"buffer_store_dwordx4 v[136:139],v242,s[44:47],s73 offen sc1\n"
"s_add_u32 s73,s73,40960\n"
MF "v[172:175],v[224:227],a[252:255],v[172:175]\n"
CV "v152,v152,v153\n"
CV "v153,v154,v155\n"
MF "v[168:171],v[224:227],a[248:251],v[168:171]\n"
CV "v154,v156,v157\n"
CV "v155,v158,v159\n"
"v_permlane16_swap_b32 v144,v146\n"
"v_permlane16_swap_b32 v145,v147\n"
"buffer_store_dwordx4 v[144:147],v242,s[44:47],s73 offen sc1\n"
"s_add_u32 s73,s73,40960\n"
CV "v160,v160,v161\n"
CV "v161,v162,v163\n"
CV "v162,v164,v165\n"
CV "v163,v166,v167\n"
"v_permlane16_swap_b32 v152,v154\n"
"v_permlane16_swap_b32 v153,v155\n"
"buffer_store_dwordx4 v[152:155],v242,s[44:47],s73 offen sc1\n"
"s_add_u32 s73,s73,40960\n"
CV "v168,v168,v169\n"
CV "v169,v170,v171\n"
CV "v170,v172,v173\n"
CV "v171,v174,v175\n"
"v_permlane16_swap_b32 v160,v162\n"
"v_permlane16_swap_b32 v161,v163\n"
"buffer_store_dwordx4 v[160:163],v242,s[44:47],s73 offen sc1\n"
"s_add_u32 s73,s73,40960\n"
"v_permlane16_swap_b32 v168,v170\n"
"v_permlane16_swap_b32 v169,v171\n"
"buffer_store_dwordx4 v[168:171],v242,s[44:47],s73 offen sc1\n"
"s_add_u32 s73,s73,40960\n"
"s_endpgm\n"
".Lool:\n"
".Lsp0:\n"
"s_cmp_eq_u32 s98,0\n"
"s_cbranch_scc1 .Lnb0\n"
"s_sub_u32 s98,s98,1\n"
"s_sleep 2\n"
"buffer_load_dword v10,v12,s[40:43],s86 offen sc1\n"
"s_waitcnt vmcnt(0)\n"
"v_sub_u32 v10,v10,v11\n"
"s_nop 1\n"
"s_branch .Lnf0\n"
".Lsp1:\n"
"s_cmp_eq_u32 s98,0\n"
"s_cbranch_scc1 .Lnb1\n"
"s_sub_u32 s98,s98,1\n"
"s_sleep 2\n"
"buffer_load_dword v10,v12,s[40:43],s86 offen sc1\n"
"s_waitcnt vmcnt(0)\n"
"v_sub_u32 v10,v10,v11\n"
"s_nop 1\n"
"s_branch .Lnf1\n"
".Lsp2:\n"
"s_cmp_eq_u32 s98,0\n"
"s_cbranch_scc1 .Lnb2\n"
"s_sub_u32 s98,s98,1\n"
"s_sleep 2\n"
"buffer_load_dword v10,v12,s[40:43],s86 offen sc1\n"
"s_waitcnt vmcnt(0)\n"
"v_sub_u32 v10,v10,v11\n"
"s_nop 1\n"
"s_branch .Lnf2\n"
".Lsp3:\n"
"s_cmp_eq_u32 s98,0\n"
"s_cbranch_scc1 .Lnb3\n"
"s_sub_u32 s98,s98,1\n"
"s_sleep 2\n"
"buffer_load_dword v10,v12,s[40:43],s86 offen sc1\n"
"s_waitcnt vmcnt(0)\n"
"v_sub_u32 v10,v10,v11\n"
"s_nop 1\n"
"s_branch .Lnf3\n"
".Lsp4:\n"
"s_cmp_eq_u32 s98,0\n"
"s_cbranch_scc1 .Lnb4\n"
"s_sub_u32 s98,s98,1\n"
"s_sleep 2\n"
"buffer_load_dword v10,v12,s[40:43],s86 offen sc1\n"
"s_waitcnt vmcnt(0)\n"
"v_sub_u32 v10,v10,v11\n"
"s_nop 1\n"
"s_branch .Lnf4\n"
".Lsp5:\n"
"s_cmp_eq_u32 s98,0\n"
"s_cbranch_scc1 .Lnb5\n"
"s_sub_u32 s98,s98,1\n"
"s_sleep 2\n"
"buffer_load_dword v10,v12,s[40:43],s86 offen sc1\n"
"s_waitcnt vmcnt(0)\n"
"v_sub_u32 v10,v10,v11\n"
"s_nop 1\n"
"s_branch .Lnf5\n"
".Lcode1:\n"
".Lslow_0:\n"
"s_nop 7\n"
"s_nop 7\n"
"v_and_b32 v10,240,v9\n"
"v_mul_u32_u24 v10,24,v10\n"
"v_lshrrev_b32 v11,4,v9\n"
"v_and_or_b32 v10,v11,48,v10\n"
"s_mov_b32 s59,s34\n"
"s_mov_b32 s34,s20\n"
"s_add_u32 s58,s74,0\n"
"s_nop 3\n"
LD "v[52:55],v10,s[32:35],s58 offen\n"
"s_waitcnt vmcnt(0)\n"
MF "v[72:75],v[52:55],v[136:139],v[32:35]\n"
MF "v[76:79],v[52:55],v[140:143],v[36:39]\n"
MF "v[80:83],v[52:55],v[144:147],v[40:43]\n"
MF "v[84:87],v[52:55],v[148:151],v[44:47]\n"
MF "v[88:91],v[52:55],v[152:155],v[48:51]\n"
"s_add_u32 s58,s74,0\n"
"s_nop 3\n"
LD "v[52:55],v10,s[32:35],s58 offen offset:64\n"
"s_waitcnt vmcnt(0)\n"
MF "v[72:75],v[52:55],v[156:159],v[72:75]\n"
MF "v[76:79],v[52:55],v[160:163],v[76:79]\n"
MF "v[80:83],v[52:55],v[164:167],v[80:83]\n"
MF "v[84:87],v[52:55],v[168:171],v[84:87]\n"
MF "v[88:91],v[52:55],v[172:175],v[88:91]\n"
"s_add_u32 s58,s74,0\n"
"s_nop 3\n"
LD "v[52:55],v10,s[32:35],s58 offen offset:128\n"
"s_waitcnt vmcnt(0)\n"
MF "v[72:75],v[52:55],v[176:179],v[72:75]\n"
MF "v[76:79],v[52:55],v[180:183],v[76:79]\n"
MF "v[80:83],v[52:55],v[184:187],v[80:83]\n"
MF "v[84:87],v[52:55],v[188:191],v[84:87]\n"
MF "v[88:91],v[52:55],v[192:195],v[88:91]\n"
"s_add_u32 s58,s74,0\n"
"s_nop 3\n"
LD "v[52:55],v10,s[32:35],s58 offen offset:192\n"
"s_waitcnt vmcnt(0)\n"
MF "v[72:75],v[52:55],v[196:199],v[72:75]\n"
MF "v[76:79],v[52:55],v[200:203],v[76:79]\n"
MF "v[80:83],v[52:55],v[204:207],v[80:83]\n"
MF "v[84:87],v[52:55],v[208:211],v[84:87]\n"
MF "v[88:91],v[52:55],v[212:215],v[88:91]\n"
"s_add_u32 s58,s74,0\n"
"s_nop 3\n"
LD "v[52:55],v10,s[32:35],s58 offen offset:256\n"
"s_waitcnt vmcnt(0)\n"
MF "v[72:75],v[52:55],v[216:219],v[72:75]\n"
MF "v[76:79],v[52:55],v[220:223],v[76:79]\n"
MF "v[80:83],v[52:55],v[224:227],v[80:83]\n"
MF "v[84:87],v[52:55],v[228:231],v[84:87]\n"
MF "v[88:91],v[52:55],v[232:235],v[88:91]\n"
"s_add_u32 s58,s74,0\n"
"s_nop 3\n"
LD "v[52:55],v10,s[32:35],s58 offen offset:320\n"
"s_waitcnt vmcnt(0)\n"
MF "v[72:75],v[52:55],v[236:239],v[72:75]\n"
MF "v[76:79],v[52:55],v[240:243],v[76:79]\n"
MF "v[80:83],v[52:55],v[244:247],v[80:83]\n"
MF "v[84:87],v[52:55],v[248:251],v[84:87]\n"
MF "v[88:91],v[52:55],v[252:255],v[88:91]\n"
"s_add_u32 s58,s74,6144\n"
"s_nop 3\n"
LD "v[52:55],v10,s[32:35],s58 offen\n"
"s_waitcnt vmcnt(0)\n"
MF "v[112:115],v[52:55],v[136:139],v[32:35]\n"
MF "v[116:119],v[52:55],v[140:143],v[36:39]\n"
MF "v[120:123],v[52:55],v[144:147],v[40:43]\n"
MF "v[124:127],v[52:55],v[148:151],v[44:47]\n"
MF "v[128:131],v[52:55],v[152:155],v[48:51]\n"
"s_add_u32 s58,s74,6144\n"
"s_nop 3\n"
LD "v[52:55],v10,s[32:35],s58 offen offset:64\n"
"s_waitcnt vmcnt(0)\n"
MF "v[112:115],v[52:55],v[156:159],v[112:115]\n"
MF "v[116:119],v[52:55],v[160:163],v[116:119]\n"
MF "v[120:123],v[52:55],v[164:167],v[120:123]\n"
MF "v[124:127],v[52:55],v[168:171],v[124:127]\n"
MF "v[128:131],v[52:55],v[172:175],v[128:131]\n"
"s_add_u32 s58,s74,6144\n"
"s_nop 3\n"
LD "v[52:55],v10,s[32:35],s58 offen offset:128\n"
"s_waitcnt vmcnt(0)\n"
MF "v[112:115],v[52:55],v[176:179],v[112:115]\n"
MF "v[116:119],v[52:55],v[180:183],v[116:119]\n"
MF "v[120:123],v[52:55],v[184:187],v[120:123]\n"
MF "v[124:127],v[52:55],v[188:191],v[124:127]\n"
MF "v[128:131],v[52:55],v[192:195],v[128:131]\n"
"s_add_u32 s58,s74,6144\n"
"s_nop 3\n"
LD "v[52:55],v10,s[32:35],s58 offen offset:192\n"
"s_waitcnt vmcnt(0)\n"
MF "v[112:115],v[52:55],v[196:199],v[112:115]\n"
MF "v[116:119],v[52:55],v[200:203],v[116:119]\n"
MF "v[120:123],v[52:55],v[204:207],v[120:123]\n"
MF "v[124:127],v[52:55],v[208:211],v[124:127]\n"
MF "v[128:131],v[52:55],v[212:215],v[128:131]\n"
"s_add_u32 s58,s74,6144\n"
"s_nop 3\n"
LD "v[52:55],v10,s[32:35],s58 offen offset:256\n"
"s_waitcnt vmcnt(0)\n"
MF "v[112:115],v[52:55],v[216:219],v[112:115]\n"
MF "v[116:119],v[52:55],v[220:223],v[116:119]\n"
MF "v[120:123],v[52:55],v[224:227],v[120:123]\n"
MF "v[124:127],v[52:55],v[228:231],v[124:127]\n"
MF "v[128:131],v[52:55],v[232:235],v[128:131]\n"
"s_add_u32 s58,s74,6144\n"
"s_nop 3\n"
LD "v[52:55],v10,s[32:35],s58 offen offset:320\n"
"s_waitcnt vmcnt(0)\n"
MF "v[112:115],v[52:55],v[236:239],v[112:115]\n"
MF "v[116:119],v[52:55],v[240:243],v[116:119]\n"
MF "v[120:123],v[52:55],v[244:247],v[120:123]\n"
MF "v[124:127],v[52:55],v[248:251],v[124:127]\n"
MF "v[128:131],v[52:55],v[252:255],v[128:131]\n"
"s_nop 7\n"
"s_mov_b32 s34,s59\n"
"v_max3_f32 v10,v72,v73,v74\n"
"s_nop 0\n"
"v_max3_f32 v11,v75,v112,v113\n"
"v_max3_f32 v10,v10,v114,v115\n"
"v_max_f32 v10,v10,v11\n"
"v_mov_b32 v11,v10\n"
"s_nop 1\n"
"v_permlane16_swap_b32 v10,v11\n"
"v_max_f32 v10,v10,v11\n"
"v_mov_b32 v11,v10\n"
"s_nop 1\n"
"v_permlane32_swap_b32 v10,v11\n"
"v_max_f32 v10,v10,v11\n"
"v_add_f32 v10,0x41300000,v10\n"
"v_max_f32 v10,0,v10\n"
"v_exp_f32 v11,-v10\n"
AR "v12,a0\n"
"v_subrev_f32 v12,s13,v12\n"
"v_fma_f32 v12,v12,v11,s13\n"
AW "a0,v12\n"
AR "v12,a20\n"
"v_subrev_f32 v12,s13,v12\n"
"v_fma_f32 v12,v12,v11,s13\n"
AW "a20,v12\n"
AR "v12,a40\n"
"v_subrev_f32 v12,s13,v12\n"
"v_fma_f32 v12,v12,v11,s13\n"
AW "a40,v12\n"
AR "v12,a60\n"
"v_subrev_f32 v12,s13,v12\n"
"v_fma_f32 v12,v12,v11,s13\n"
AW "a60,v12\n"
AR "v12,a80\n"
"v_subrev_f32 v12,s13,v12\n"
"v_fma_f32 v12,v12,v11,s13\n"
AW "a80,v12\n"
AR "v12,a100\n"
"v_subrev_f32 v12,s13,v12\n"
"v_fma_f32 v12,v12,v11,s13\n"
AW "a100,v12\n"
AR "v12,a120\n"
"v_subrev_f32 v12,s13,v12\n"
"v_fma_f32 v12,v12,v11,s13\n"
AW "a120,v12\n"
AR "v12,a140\n"
"v_subrev_f32 v12,s13,v12\n"
"v_fma_f32 v12,v12,v11,s13\n"
AW "a140,v12\n"
AR "v12,a160\n"
"v_subrev_f32 v12,s13,v12\n"
"v_fma_f32 v12,v12,v11,s13\n"
AW "a160,v12\n"
AR "v12,a180\n"
"v_subrev_f32 v12,s13,v12\n"
"v_fma_f32 v12,v12,v11,s13\n"
AW "a180,v12\n"
AR "v12,a200\n"
"v_subrev_f32 v12,s13,v12\n"
"v_fma_f32 v12,v12,v11,s13\n"
AW "a200,v12\n"
AR "v12,a220\n"
"v_subrev_f32 v12,s13,v12\n"
"v_fma_f32 v12,v12,v11,s13\n"
AW "a220,v12\n"
AR "v12,a1\n"
"v_subrev_f32 v12,s13,v12\n"
"v_fma_f32 v12,v12,v11,s13\n"
AW "a1,v12\n"
AR "v12,a21\n"
"v_subrev_f32 v12,s13,v12\n"
"v_fma_f32 v12,v12,v11,s13\n"
AW "a21,v12\n"
AR "v12,a41\n"
"v_subrev_f32 v12,s13,v12\n"
"v_fma_f32 v12,v12,v11,s13\n"
AW "a41,v12\n"
AR "v12,a61\n"
"v_subrev_f32 v12,s13,v12\n"
"v_fma_f32 v12,v12,v11,s13\n"
AW "a61,v12\n"
AR "v12,a81\n"
"v_subrev_f32 v12,s13,v12\n"
"v_fma_f32 v12,v12,v11,s13\n"
AW "a81,v12\n"
AR "v12,a101\n"
"v_subrev_f32 v12,s13,v12\n"
"v_fma_f32 v12,v12,v11,s13\n"
AW "a101,v12\n"
AR "v12,a121\n"
"v_subrev_f32 v12,s13,v12\n"
"v_fma_f32 v12,v12,v11,s13\n"
AW "a121,v12\n"
AR "v12,a141\n"
"v_subrev_f32 v12,s13,v12\n"
"v_fma_f32 v12,v12,v11,s13\n"
AW "a141,v12\n"
AR "v12,a161\n"
"v_subrev_f32 v12,s13,v12\n"
"v_fma_f32 v12,v12,v11,s13\n"
AW "a161,v12\n"
AR "v12,a181\n"
"v_subrev_f32 v12,s13,v12\n"
"v_fma_f32 v12,v12,v11,s13\n"
AW "a181,v12\n"
AR "v12,a201\n"
"v_subrev_f32 v12,s13,v12\n"
"v_fma_f32 v12,v12,v11,s13\n"
AW "a201,v12\n"
AR "v12,a221\n"
"v_subrev_f32 v12,s13,v12\n"
"v_fma_f32 v12,v12,v11,s13\n"
AW "a221,v12\n"
AR "v12,a2\n"
"v_subrev_f32 v12,s13,v12\n"
"v_fma_f32 v12,v12,v11,s13\n"
AW "a2,v12\n"
AR "v12,a22\n"
"v_subrev_f32 v12,s13,v12\n"
"v_fma_f32 v12,v12,v11,s13\n"
AW "a22,v12\n"
AR "v12,a42\n"
"v_subrev_f32 v12,s13,v12\n"
"v_fma_f32 v12,v12,v11,s13\n"
AW "a42,v12\n"
AR "v12,a62\n"
"v_subrev_f32 v12,s13,v12\n"
"v_fma_f32 v12,v12,v11,s13\n"
AW "a62,v12\n"
AR "v12,a82\n"
"v_subrev_f32 v12,s13,v12\n"
"v_fma_f32 v12,v12,v11,s13\n"
AW "a82,v12\n"
AR "v12,a102\n"
"v_subrev_f32 v12,s13,v12\n"
"v_fma_f32 v12,v12,v11,s13\n"
AW "a102,v12\n"
AR "v12,a122\n"
"v_subrev_f32 v12,s13,v12\n"
"v_fma_f32 v12,v12,v11,s13\n"
AW "a122,v12\n"
AR "v12,a142\n"
"v_subrev_f32 v12,s13,v12\n"
"v_fma_f32 v12,v12,v11,s13\n"
AW "a142,v12\n"
AR "v12,a162\n"
"v_subrev_f32 v12,s13,v12\n"
"v_fma_f32 v12,v12,v11,s13\n"
AW "a162,v12\n"
AR "v12,a182\n"
"v_subrev_f32 v12,s13,v12\n"
"v_fma_f32 v12,v12,v11,s13\n"
AW "a182,v12\n"
AR "v12,a202\n"
"v_subrev_f32 v12,s13,v12\n"
"v_fma_f32 v12,v12,v11,s13\n"
AW "a202,v12\n"
AR "v12,a222\n"
"v_subrev_f32 v12,s13,v12\n"
"v_fma_f32 v12,v12,v11,s13\n"
AW "a222,v12\n"
AR "v12,a3\n"
"v_subrev_f32 v12,s13,v12\n"
"v_fma_f32 v12,v12,v11,s13\n"
AW "a3,v12\n"
AR "v12,a23\n"
"v_subrev_f32 v12,s13,v12\n"
"v_fma_f32 v12,v12,v11,s13\n"
AW "a23,v12\n"
AR "v12,a43\n"
"v_subrev_f32 v12,s13,v12\n"
"v_fma_f32 v12,v12,v11,s13\n"
AW "a43,v12\n"
AR "v12,a63\n"
"v_subrev_f32 v12,s13,v12\n"
"v_fma_f32 v12,v12,v11,s13\n"
AW "a63,v12\n"
AR "v12,a83\n"
"v_subrev_f32 v12,s13,v12\n"
"v_fma_f32 v12,v12,v11,s13\n"
AW "a83,v12\n"
AR "v12,a103\n"
"v_subrev_f32 v12,s13,v12\n"
"v_fma_f32 v12,v12,v11,s13\n"
AW "a103,v12\n"
AR "v12,a123\n"
"v_subrev_f32 v12,s13,v12\n"
"v_fma_f32 v12,v12,v11,s13\n"
AW "a123,v12\n"
AR "v12,a143\n"
"v_subrev_f32 v12,s13,v12\n"
"v_fma_f32 v12,v12,v11,s13\n"
AW "a143,v12\n"
AR "v12,a163\n"
"v_subrev_f32 v12,s13,v12\n"
"v_fma_f32 v12,v12,v11,s13\n"
AW "a163,v12\n"
AR "v12,a183\n"
"v_subrev_f32 v12,s13,v12\n"
"v_fma_f32 v12,v12,v11,s13\n"
AW "a183,v12\n"
AR "v12,a203\n"
"v_subrev_f32 v12,s13,v12\n"
"v_fma_f32 v12,v12,v11,s13\n"
AW "a203,v12\n"
AR "v12,a223\n"
"v_subrev_f32 v12,s13,v12\n"
"v_fma_f32 v12,v12,v11,s13\n"
AW "a223,v12\n"
"v_mul_f32 v52,v57,v11\n"
"v_sub_f32 v32,v32,v10\n"
"v_sub_f32 v33,v33,v10\n"
"v_sub_f32 v34,v34,v10\n"
"v_sub_f32 v35,v35,v10\n"
"v_sub_f32 v92,v92,v10\n"
"v_sub_f32 v93,v93,v10\n"
"v_sub_f32 v94,v94,v10\n"
"v_sub_f32 v95,v95,v10\n"
"v_sub_f32 v72,v72,v10\n"
"v_sub_f32 v73,v73,v10\n"
"v_sub_f32 v74,v74,v10\n"
"v_sub_f32 v75,v75,v10\n"
"v_sub_f32 v112,v112,v10\n"
"v_sub_f32 v113,v113,v10\n"
"v_sub_f32 v114,v114,v10\n"
"v_sub_f32 v115,v115,v10\n"
"v_exp_f32 v72,v72\n"
"v_exp_f32 v73,v73\n"
"v_exp_f32 v74,v74\n"
"v_exp_f32 v75,v75\n"
"v_exp_f32 v112,v112\n"
"v_exp_f32 v113,v113\n"
"v_exp_f32 v114,v114\n"
"v_exp_f32 v115,v115\n"
"v_add_f32 v52,v52,v72\n"
"v_add_f32 v52,v52,v73\n"
"v_add_f32 v52,v52,v74\n"
"v_add_f32 v52,v52,v75\n"
"v_add_f32 v52,v52,v112\n"
"v_add_f32 v52,v52,v113\n"
"v_add_f32 v52,v52,v114\n"
"v_add_f32 v52,v52,v115\n"
CV "v72,v72,v73\n"
CV "v73,v74,v75\n"
CV "v74,v112,v113\n"
CV "v75,v114,v115\n"
"v_max3_f32 v10,v76,v77,v78\n"
"v_max3_f32 v11,v79,v116,v117\n"
"v_max3_f32 v10,v10,v118,v119\n"
"v_max_f32 v10,v10,v11\n"
"v_mov_b32 v11,v10\n"
"s_nop 1\n"
"v_permlane16_swap_b32 v10,v11\n"
"v_max_f32 v10,v10,v11\n"
"v_mov_b32 v11,v10\n"
"s_nop 1\n"
"v_permlane32_swap_b32 v10,v11\n"
"v_max_f32 v10,v10,v11\n"
"v_add_f32 v10,0x41300000,v10\n"
"v_max_f32 v10,0,v10\n"
"v_exp_f32 v11,-v10\n"
AR "v12,a4\n"
"v_subrev_f32 v12,s13,v12\n"
"v_fma_f32 v12,v12,v11,s13\n"
AW "a4,v12\n"
AR "v12,a24\n"
"v_subrev_f32 v12,s13,v12\n"
"v_fma_f32 v12,v12,v11,s13\n"
AW "a24,v12\n"
AR "v12,a44\n"
"v_subrev_f32 v12,s13,v12\n"
"v_fma_f32 v12,v12,v11,s13\n"
AW "a44,v12\n"
AR "v12,a64\n"
"v_subrev_f32 v12,s13,v12\n"
"v_fma_f32 v12,v12,v11,s13\n"
AW "a64,v12\n"
AR "v12,a84\n"
"v_subrev_f32 v12,s13,v12\n"
"v_fma_f32 v12,v12,v11,s13\n"
AW "a84,v12\n"
AR "v12,a104\n"
"v_subrev_f32 v12,s13,v12\n"
"v_fma_f32 v12,v12,v11,s13\n"
AW "a104,v12\n"
AR "v12,a124\n"
"v_subrev_f32 v12,s13,v12\n"
"v_fma_f32 v12,v12,v11,s13\n"
AW "a124,v12\n"
AR "v12,a144\n"
"v_subrev_f32 v12,s13,v12\n"
"v_fma_f32 v12,v12,v11,s13\n"
AW "a144,v12\n"
AR "v12,a164\n"
"v_subrev_f32 v12,s13,v12\n"
"v_fma_f32 v12,v12,v11,s13\n"
AW "a164,v12\n"
AR "v12,a184\n"
"v_subrev_f32 v12,s13,v12\n"
"v_fma_f32 v12,v12,v11,s13\n"
AW "a184,v12\n"
AR "v12,a204\n"
"v_subrev_f32 v12,s13,v12\n"
"v_fma_f32 v12,v12,v11,s13\n"
AW "a204,v12\n"
AR "v12,a224\n"
"v_subrev_f32 v12,s13,v12\n"
"v_fma_f32 v12,v12,v11,s13\n"
AW "a224,v12\n"
AR "v12,a5\n"
"v_subrev_f32 v12,s13,v12\n"
"v_fma_f32 v12,v12,v11,s13\n"
AW "a5,v12\n"
AR "v12,a25\n"
"v_subrev_f32 v12,s13,v12\n"
"v_fma_f32 v12,v12,v11,s13\n"
AW "a25,v12\n"
AR "v12,a45\n"
"v_subrev_f32 v12,s13,v12\n"
"v_fma_f32 v12,v12,v11,s13\n"
AW "a45,v12\n"
AR "v12,a65\n"
"v_subrev_f32 v12,s13,v12\n"
"v_fma_f32 v12,v12,v11,s13\n"
AW "a65,v12\n"
AR "v12,a85\n"
"v_subrev_f32 v12,s13,v12\n"
"v_fma_f32 v12,v12,v11,s13\n"
AW "a85,v12\n"
AR "v12,a105\n"
"v_subrev_f32 v12,s13,v12\n"
"v_fma_f32 v12,v12,v11,s13\n"
AW "a105,v12\n"
AR "v12,a125\n"
"v_subrev_f32 v12,s13,v12\n"
"v_fma_f32 v12,v12,v11,s13\n"
AW "a125,v12\n"
AR "v12,a145\n"
"v_subrev_f32 v12,s13,v12\n"
"v_fma_f32 v12,v12,v11,s13\n"
AW "a145,v12\n"
AR "v12,a165\n"
"v_subrev_f32 v12,s13,v12\n"
"v_fma_f32 v12,v12,v11,s13\n"
AW "a165,v12\n"
AR "v12,a185\n"
"v_subrev_f32 v12,s13,v12\n"
"v_fma_f32 v12,v12,v11,s13\n"
AW "a185,v12\n"
AR "v12,a205\n"
"v_subrev_f32 v12,s13,v12\n"
"v_fma_f32 v12,v12,v11,s13\n"
AW "a205,v12\n"
AR "v12,a225\n"
"v_subrev_f32 v12,s13,v12\n"
"v_fma_f32 v12,v12,v11,s13\n"
AW "a225,v12\n"
AR "v12,a6\n"
"v_subrev_f32 v12,s13,v12\n"
"v_fma_f32 v12,v12,v11,s13\n"
AW "a6,v12\n"
AR "v12,a26\n"
"v_subrev_f32 v12,s13,v12\n"
"v_fma_f32 v12,v12,v11,s13\n"
AW "a26,v12\n"
AR "v12,a46\n"
"v_subrev_f32 v12,s13,v12\n"
"v_fma_f32 v12,v12,v11,s13\n"
AW "a46,v12\n"
AR "v12,a66\n"
"v_subrev_f32 v12,s13,v12\n"
"v_fma_f32 v12,v12,v11,s13\n"
AW "a66,v12\n"
AR "v12,a86\n"
"v_subrev_f32 v12,s13,v12\n"
"v_fma_f32 v12,v12,v11,s13\n"
AW "a86,v12\n"
AR "v12,a106\n"
"v_subrev_f32 v12,s13,v12\n"
"v_fma_f32 v12,v12,v11,s13\n"
AW "a106,v12\n"
AR "v12,a126\n"
"v_subrev_f32 v12,s13,v12\n"
"v_fma_f32 v12,v12,v11,s13\n"
AW "a126,v12\n"
AR "v12,a146\n"
"v_subrev_f32 v12,s13,v12\n"
"v_fma_f32 v12,v12,v11,s13\n"
AW "a146,v12\n"
AR "v12,a166\n"
"v_subrev_f32 v12,s13,v12\n"
"v_fma_f32 v12,v12,v11,s13\n"
AW "a166,v12\n"
AR "v12,a186\n"
"v_subrev_f32 v12,s13,v12\n"
"v_fma_f32 v12,v12,v11,s13\n"
AW "a186,v12\n"
AR "v12,a206\n"
"v_subrev_f32 v12,s13,v12\n"
"v_fma_f32 v12,v12,v11,s13\n"
AW "a206,v12\n"
AR "v12,a226\n"
"v_subrev_f32 v12,s13,v12\n"
"v_fma_f32 v12,v12,v11,s13\n"
AW "a226,v12\n"
AR "v12,a7\n"
"v_subrev_f32 v12,s13,v12\n"
"v_fma_f32 v12,v12,v11,s13\n"
AW "a7,v12\n"
AR "v12,a27\n"
"v_subrev_f32 v12,s13,v12\n"
"v_fma_f32 v12,v12,v11,s13\n"
AW "a27,v12\n"
AR "v12,a47\n"
"v_subrev_f32 v12,s13,v12\n"
"v_fma_f32 v12,v12,v11,s13\n"
AW "a47,v12\n"
AR "v12,a67\n"
"v_subrev_f32 v12,s13,v12\n"
"v_fma_f32 v12,v12,v11,s13\n"
AW "a67,v12\n"
AR "v12,a87\n"
"v_subrev_f32 v12,s13,v12\n"
"v_fma_f32 v12,v12,v11,s13\n"
AW "a87,v12\n"
AR "v12,a107\n"
"v_subrev_f32 v12,s13,v12\n"
"v_fma_f32 v12,v12,v11,s13\n"
AW "a107,v12\n"
AR "v12,a127\n"
"v_subrev_f32 v12,s13,v12\n"
"v_fma_f32 v12,v12,v11,s13\n"
AW "a127,v12\n"
AR "v12,a147\n"
"v_subrev_f32 v12,s13,v12\n"
"v_fma_f32 v12,v12,v11,s13\n"
AW "a147,v12\n"
AR "v12,a167\n"
"v_subrev_f32 v12,s13,v12\n"
"v_fma_f32 v12,v12,v11,s13\n"
AW "a167,v12\n"
AR "v12,a187\n"
"v_subrev_f32 v12,s13,v12\n"
"v_fma_f32 v12,v12,v11,s13\n"
AW "a187,v12\n"
AR "v12,a207\n"
"v_subrev_f32 v12,s13,v12\n"
"v_fma_f32 v12,v12,v11,s13\n"
AW "a207,v12\n"
AR "v12,a227\n"
"v_subrev_f32 v12,s13,v12\n"
"v_fma_f32 v12,v12,v11,s13\n"
AW "a227,v12\n"
"v_mul_f32 v53,v58,v11\n"
"v_sub_f32 v36,v36,v10\n"
"v_sub_f32 v37,v37,v10\n"
"v_sub_f32 v38,v38,v10\n"
"v_sub_f32 v39,v39,v10\n"
"v_sub_f32 v96,v96,v10\n"
"v_sub_f32 v97,v97,v10\n"
"v_sub_f32 v98,v98,v10\n"
"v_sub_f32 v99,v99,v10\n"
"v_sub_f32 v76,v76,v10\n"
"v_sub_f32 v77,v77,v10\n"
"v_sub_f32 v78,v78,v10\n"
"v_sub_f32 v79,v79,v10\n"
"v_sub_f32 v116,v116,v10\n"
"v_sub_f32 v117,v117,v10\n"
"v_sub_f32 v118,v118,v10\n"
"v_sub_f32 v119,v119,v10\n"
"v_exp_f32 v76,v76\n"
"v_exp_f32 v77,v77\n"
"v_exp_f32 v78,v78\n"
"v_exp_f32 v79,v79\n"
"v_exp_f32 v116,v116\n"
"v_exp_f32 v117,v117\n"
"v_exp_f32 v118,v118\n"
"v_exp_f32 v119,v119\n"
"v_add_f32 v53,v53,v76\n"
"v_add_f32 v53,v53,v77\n"
"v_add_f32 v53,v53,v78\n"
"v_add_f32 v53,v53,v79\n"
"v_add_f32 v53,v53,v116\n"
"v_add_f32 v53,v53,v117\n"
"v_add_f32 v53,v53,v118\n"
"v_add_f32_e64 v53,v53,v119\n"
CV "v76,v76,v77\n"
CV "v77,v78,v79\n"
CV "v78,v116,v117\n"
CV "v79,v118,v119\n"
"v_max3_f32 v10,v80,v81,v82\n"
"v_max3_f32 v11,v83,v120,v121\n"
"v_max3_f32 v10,v10,v122,v123\n"
"v_max_f32 v10,v10,v11\n"
"v_mov_b32 v11,v10\n"
"s_nop 1\n"
"v_permlane16_swap_b32 v10,v11\n"
"v_max_f32 v10,v10,v11\n"
"v_mov_b32 v11,v10\n"
"s_nop 1\n"
"v_permlane32_swap_b32 v10,v11\n"
"v_max_f32 v10,v10,v11\n"
"v_add_f32 v10,0x41300000,v10\n"
"v_max_f32 v10,0,v10\n"
"v_exp_f32 v11,-v10\n"
AR "v12,a8\n"
"v_subrev_f32 v12,s13,v12\n"
"v_fma_f32 v12,v12,v11,s13\n"
AW "a8,v12\n"
AR "v12,a28\n"
"v_subrev_f32 v12,s13,v12\n"
"v_fma_f32 v12,v12,v11,s13\n"
AW "a28,v12\n"
AR "v12,a48\n"
"v_subrev_f32 v12,s13,v12\n"
"v_fma_f32 v12,v12,v11,s13\n"
AW "a48,v12\n"
AR "v12,a68\n"
"v_subrev_f32 v12,s13,v12\n"
"v_fma_f32 v12,v12,v11,s13\n"
AW "a68,v12\n"
AR "v12,a88\n"
"v_subrev_f32 v12,s13,v12\n"
"v_fma_f32 v12,v12,v11,s13\n"
AW "a88,v12\n"
AR "v12,a108\n"
"v_subrev_f32 v12,s13,v12\n"
"v_fma_f32 v12,v12,v11,s13\n"
AW "a108,v12\n"
AR "v12,a128\n"
"v_subrev_f32 v12,s13,v12\n"
"v_fma_f32 v12,v12,v11,s13\n"
AW "a128,v12\n"
AR "v12,a148\n"
"v_subrev_f32 v12,s13,v12\n"
"v_fma_f32 v12,v12,v11,s13\n"
AW "a148,v12\n"
AR "v12,a168\n"
"v_subrev_f32 v12,s13,v12\n"
"v_fma_f32 v12,v12,v11,s13\n"
AW "a168,v12\n"
AR "v12,a188\n"
"v_subrev_f32 v12,s13,v12\n"
"v_fma_f32 v12,v12,v11,s13\n"
AW "a188,v12\n"
AR "v12,a208\n"
"v_subrev_f32 v12,s13,v12\n"
"v_fma_f32 v12,v12,v11,s13\n"
AW "a208,v12\n"
AR "v12,a228\n"
"v_subrev_f32 v12,s13,v12\n"
"v_fma_f32 v12,v12,v11,s13\n"
AW "a228,v12\n"
AR "v12,a9\n"
"v_subrev_f32 v12,s13,v12\n"
"v_fma_f32 v12,v12,v11,s13\n"
AW "a9,v12\n"
AR "v12,a29\n"
"v_subrev_f32 v12,s13,v12\n"
"v_fma_f32 v12,v12,v11,s13\n"
AW "a29,v12\n"
AR "v12,a49\n"
"v_subrev_f32 v12,s13,v12\n"
"v_fma_f32 v12,v12,v11,s13\n"
AW "a49,v12\n"
AR "v12,a69\n"
"v_subrev_f32 v12,s13,v12\n"
"v_fma_f32 v12,v12,v11,s13\n"
AW "a69,v12\n"
AR "v12,a89\n"
"v_subrev_f32 v12,s13,v12\n"
"v_fma_f32 v12,v12,v11,s13\n"
AW "a89,v12\n"
AR "v12,a109\n"
"v_subrev_f32 v12,s13,v12\n"
"v_fma_f32 v12,v12,v11,s13\n"
AW "a109,v12\n"
AR "v12,a129\n"
"v_subrev_f32 v12,s13,v12\n"
"v_fma_f32 v12,v12,v11,s13\n"
AW "a129,v12\n"
AR "v12,a149\n"
"v_subrev_f32 v12,s13,v12\n"
"v_fma_f32 v12,v12,v11,s13\n"
AW "a149,v12\n"
AR "v12,a169\n"
"v_subrev_f32 v12,s13,v12\n"
"v_fma_f32 v12,v12,v11,s13\n"
AW "a169,v12\n"
AR "v12,a189\n"
"v_subrev_f32 v12,s13,v12\n"
"v_fma_f32 v12,v12,v11,s13\n"
AW "a189,v12\n"
AR "v12,a209\n"
"v_subrev_f32 v12,s13,v12\n"
"v_fma_f32 v12,v12,v11,s13\n"
AW "a209,v12\n"
AR "v12,a229\n"
"v_subrev_f32 v12,s13,v12\n"
"v_fma_f32 v12,v12,v11,s13\n"
AW "a229,v12\n"
AR "v12,a10\n"
"v_subrev_f32 v12,s13,v12\n"
"v_fma_f32 v12,v12,v11,s13\n"
AW "a10,v12\n"
AR "v12,a30\n"
"v_subrev_f32 v12,s13,v12\n"
"v_fma_f32 v12,v12,v11,s13\n"
AW "a30,v12\n"
AR "v12,a50\n"
"v_subrev_f32 v12,s13,v12\n"
"v_fma_f32 v12,v12,v11,s13\n"
AW "a50,v12\n"
AR "v12,a70\n"
"v_subrev_f32 v12,s13,v12\n"
"v_fma_f32 v12,v12,v11,s13\n"
AW "a70,v12\n"
AR "v12,a90\n"
"v_subrev_f32 v12,s13,v12\n"
"v_fma_f32 v12,v12,v11,s13\n"
AW "a90,v12\n"
AR "v12,a110\n"
"v_subrev_f32 v12,s13,v12\n"
"v_fma_f32 v12,v12,v11,s13\n"
AW "a110,v12\n"
AR "v12,a130\n"
"v_subrev_f32 v12,s13,v12\n"
"v_fma_f32 v12,v12,v11,s13\n"
AW "a130,v12\n"
AR "v12,a150\n"
"v_subrev_f32 v12,s13,v12\n"
"v_fma_f32 v12,v12,v11,s13\n"
AW "a150,v12\n"
AR "v12,a170\n"
"v_subrev_f32 v12,s13,v12\n"
"v_fma_f32 v12,v12,v11,s13\n"
AW "a170,v12\n"
AR "v12,a190\n"
"v_subrev_f32 v12,s13,v12\n"
"v_fma_f32 v12,v12,v11,s13\n"
AW "a190,v12\n"
AR "v12,a210\n"
"v_subrev_f32 v12,s13,v12\n"
"v_fma_f32 v12,v12,v11,s13\n"
AW "a210,v12\n"
AR "v12,a230\n"
"v_subrev_f32 v12,s13,v12\n"
"v_fma_f32 v12,v12,v11,s13\n"
AW "a230,v12\n"
AR "v12,a11\n"
"v_subrev_f32 v12,s13,v12\n"
"v_fma_f32 v12,v12,v11,s13\n"
AW "a11,v12\n"
AR "v12,a31\n"
"v_subrev_f32 v12,s13,v12\n"
"v_fma_f32 v12,v12,v11,s13\n"
AW "a31,v12\n"
AR "v12,a51\n"
"v_subrev_f32 v12,s13,v12\n"
"v_fma_f32 v12,v12,v11,s13\n"
AW "a51,v12\n"
AR "v12,a71\n"
"v_subrev_f32 v12,s13,v12\n"
"v_fma_f32 v12,v12,v11,s13\n"
AW "a71,v12\n"
AR "v12,a91\n"
"v_subrev_f32 v12,s13,v12\n"
"v_fma_f32 v12,v12,v11,s13\n"
AW "a91,v12\n"
AR "v12,a111\n"
"v_subrev_f32 v12,s13,v12\n"
"v_fma_f32 v12,v12,v11,s13\n"
AW "a111,v12\n"
AR "v12,a131\n"
"v_subrev_f32 v12,s13,v12\n"
"v_fma_f32 v12,v12,v11,s13\n"
AW "a131,v12\n"
AR "v12,a151\n"
"v_subrev_f32 v12,s13,v12\n"
"v_fma_f32 v12,v12,v11,s13\n"
AW "a151,v12\n"
AR "v12,a171\n"
"v_subrev_f32 v12,s13,v12\n"
"v_fma_f32 v12,v12,v11,s13\n"
AW "a171,v12\n"
AR "v12,a191\n"
"v_subrev_f32 v12,s13,v12\n"
"v_fma_f32 v12,v12,v11,s13\n"
AW "a191,v12\n"
AR "v12,a211\n"
"v_subrev_f32 v12,s13,v12\n"
"v_fma_f32 v12,v12,v11,s13\n"
AW "a211,v12\n"
AR "v12,a231\n"
"v_subrev_f32 v12,s13,v12\n"
"v_fma_f32 v12,v12,v11,s13\n"
AW "a231,v12\n"
"v_mul_f32 v54,v59,v11\n"
"v_sub_f32 v40,v40,v10\n"
"v_sub_f32 v41,v41,v10\n"
"v_sub_f32 v42,v42,v10\n"
"v_sub_f32 v43,v43,v10\n"
"v_sub_f32 v100,v100,v10\n"
"v_sub_f32 v101,v101,v10\n"
"v_sub_f32 v102,v102,v10\n"
"v_sub_f32 v103,v103,v10\n"
"v_sub_f32 v80,v80,v10\n"
"v_sub_f32 v81,v81,v10\n"
"v_sub_f32 v82,v82,v10\n"
"v_sub_f32 v83,v83,v10\n"
"v_sub_f32 v120,v120,v10\n"
"v_sub_f32 v121,v121,v10\n"
"v_sub_f32 v122,v122,v10\n"
"v_sub_f32 v123,v123,v10\n"
"v_exp_f32 v80,v80\n"
"v_exp_f32 v81,v81\n"
"v_exp_f32 v82,v82\n"
"v_exp_f32 v83,v83\n"
"v_exp_f32 v120,v120\n"
"v_exp_f32 v121,v121\n"
"v_exp_f32 v122,v122\n"
"v_exp_f32 v123,v123\n"
"v_add_f32 v54,v54,v80\n"
"v_add_f32 v54,v54,v81\n"
"v_add_f32 v54,v54,v82\n"
"v_add_f32 v54,v54,v83\n"
"v_add_f32 v54,v54,v120\n"
"v_add_f32 v54,v54,v121\n"
"v_add_f32 v54,v54,v122\n"
"v_add_f32_e64 v54,v54,v123\n"
CV "v80,v80,v81\n"
CV "v81,v82,v83\n"
CV "v82,v120,v121\n"
CV "v83,v122,v123\n"
"v_max3_f32 v10,v84,v85,v86\n"
"v_max3_f32 v11,v87,v124,v125\n"
"v_max3_f32 v10,v10,v126,v127\n"
"v_max_f32 v10,v10,v11\n"
"v_mov_b32 v11,v10\n"
"s_nop 1\n"
"v_permlane16_swap_b32 v10,v11\n"
"v_max_f32 v10,v10,v11\n"
"v_mov_b32 v11,v10\n"
"s_nop 1\n"
"v_permlane32_swap_b32 v10,v11\n"
"v_max_f32 v10,v10,v11\n"
"v_add_f32 v10,0x41300000,v10\n"
"v_max_f32 v10,0,v10\n"
"v_exp_f32 v11,-v10\n"
AR "v12,a12\n"
"v_subrev_f32 v12,s13,v12\n"
"v_fma_f32 v12,v12,v11,s13\n"
AW "a12,v12\n"
AR "v12,a32\n"
"v_subrev_f32 v12,s13,v12\n"
"v_fma_f32 v12,v12,v11,s13\n"
AW "a32,v12\n"
AR "v12,a52\n"
"v_subrev_f32 v12,s13,v12\n"
"v_fma_f32 v12,v12,v11,s13\n"
AW "a52,v12\n"
AR "v12,a72\n"
"v_subrev_f32 v12,s13,v12\n"
"v_fma_f32 v12,v12,v11,s13\n"
AW "a72,v12\n"
AR "v12,a92\n"
"v_subrev_f32 v12,s13,v12\n"
"v_fma_f32 v12,v12,v11,s13\n"
AW "a92,v12\n"
AR "v12,a112\n"
"v_subrev_f32 v12,s13,v12\n"
"v_fma_f32 v12,v12,v11,s13\n"
AW "a112,v12\n"
AR "v12,a132\n"
"v_subrev_f32 v12,s13,v12\n"
"v_fma_f32 v12,v12,v11,s13\n"
AW "a132,v12\n"
AR "v12,a152\n"
"v_subrev_f32 v12,s13,v12\n"
"v_fma_f32 v12,v12,v11,s13\n"
AW "a152,v12\n"
AR "v12,a172\n"
"v_subrev_f32 v12,s13,v12\n"
"v_fma_f32 v12,v12,v11,s13\n"
AW "a172,v12\n"
AR "v12,a192\n"
"v_subrev_f32 v12,s13,v12\n"
"v_fma_f32 v12,v12,v11,s13\n"
AW "a192,v12\n"
AR "v12,a212\n"
"v_subrev_f32 v12,s13,v12\n"
"v_fma_f32 v12,v12,v11,s13\n"
AW "a212,v12\n"
AR "v12,a232\n"
"v_subrev_f32 v12,s13,v12\n"
"v_fma_f32 v12,v12,v11,s13\n"
AW "a232,v12\n"
AR "v12,a13\n"
"v_subrev_f32 v12,s13,v12\n"
"v_fma_f32 v12,v12,v11,s13\n"
AW "a13,v12\n"
AR "v12,a33\n"
"v_subrev_f32 v12,s13,v12\n"
"v_fma_f32 v12,v12,v11,s13\n"
AW "a33,v12\n"
AR "v12,a53\n"
"v_subrev_f32 v12,s13,v12\n"
"v_fma_f32 v12,v12,v11,s13\n"
AW "a53,v12\n"
AR "v12,a73\n"
"v_subrev_f32 v12,s13,v12\n"
"v_fma_f32 v12,v12,v11,s13\n"
AW "a73,v12\n"
AR "v12,a93\n"
"v_subrev_f32 v12,s13,v12\n"
"v_fma_f32 v12,v12,v11,s13\n"
AW "a93,v12\n"
AR "v12,a113\n"
"v_subrev_f32 v12,s13,v12\n"
"v_fma_f32 v12,v12,v11,s13\n"
AW "a113,v12\n"
AR "v12,a133\n"
"v_subrev_f32 v12,s13,v12\n"
"v_fma_f32 v12,v12,v11,s13\n"
AW "a133,v12\n"
AR "v12,a153\n"
"v_subrev_f32 v12,s13,v12\n"
"v_fma_f32 v12,v12,v11,s13\n"
AW "a153,v12\n"
AR "v12,a173\n"
"v_subrev_f32 v12,s13,v12\n"
"v_fma_f32 v12,v12,v11,s13\n"
AW "a173,v12\n"
AR "v12,a193\n"
"v_subrev_f32 v12,s13,v12\n"
"v_fma_f32 v12,v12,v11,s13\n"
AW "a193,v12\n"
AR "v12,a213\n"
"v_subrev_f32 v12,s13,v12\n"
"v_fma_f32 v12,v12,v11,s13\n"
AW "a213,v12\n"
AR "v12,a233\n"
"v_subrev_f32 v12,s13,v12\n"
"v_fma_f32 v12,v12,v11,s13\n"
AW "a233,v12\n"
AR "v12,a14\n"
"v_subrev_f32 v12,s13,v12\n"
"v_fma_f32 v12,v12,v11,s13\n"
AW "a14,v12\n"
AR "v12,a34\n"
"v_subrev_f32 v12,s13,v12\n"
"v_fma_f32 v12,v12,v11,s13\n"
AW "a34,v12\n"
AR "v12,a54\n"
"v_subrev_f32 v12,s13,v12\n"
"v_fma_f32 v12,v12,v11,s13\n"
AW "a54,v12\n"
AR "v12,a74\n"
"v_subrev_f32 v12,s13,v12\n"
"v_fma_f32 v12,v12,v11,s13\n"
AW "a74,v12\n"
AR "v12,a94\n"
"v_subrev_f32 v12,s13,v12\n"
"v_fma_f32 v12,v12,v11,s13\n"
AW "a94,v12\n"
AR "v12,a114\n"
"v_subrev_f32 v12,s13,v12\n"
"v_fma_f32 v12,v12,v11,s13\n"
AW "a114,v12\n"
AR "v12,a134\n"
"v_subrev_f32 v12,s13,v12\n"
"v_fma_f32 v12,v12,v11,s13\n"
AW "a134,v12\n"
AR "v12,a154\n"
"v_subrev_f32 v12,s13,v12\n"
"v_fma_f32 v12,v12,v11,s13\n"
AW "a154,v12\n"
AR "v12,a174\n"
"v_subrev_f32 v12,s13,v12\n"
"v_fma_f32 v12,v12,v11,s13\n"
AW "a174,v12\n"
AR "v12,a194\n"
"v_subrev_f32 v12,s13,v12\n"
"v_fma_f32 v12,v12,v11,s13\n"
AW "a194,v12\n"
AR "v12,a214\n"
"v_subrev_f32 v12,s13,v12\n"
"v_fma_f32 v12,v12,v11,s13\n"
AW "a214,v12\n"
AR "v12,a234\n"
"v_subrev_f32 v12,s13,v12\n"
"v_fma_f32 v12,v12,v11,s13\n"
AW "a234,v12\n"
AR "v12,a15\n"
"v_subrev_f32 v12,s13,v12\n"
"v_fma_f32 v12,v12,v11,s13\n"
AW "a15,v12\n"
AR "v12,a35\n"
"v_subrev_f32 v12,s13,v12\n"
"v_fma_f32 v12,v12,v11,s13\n"
AW "a35,v12\n"
AR "v12,a55\n"
"v_subrev_f32 v12,s13,v12\n"
"v_fma_f32 v12,v12,v11,s13\n"
AW "a55,v12\n"
AR "v12,a75\n"
"v_subrev_f32 v12,s13,v12\n"
"v_fma_f32 v12,v12,v11,s13\n"
AW "a75,v12\n"
AR "v12,a95\n"
"v_subrev_f32 v12,s13,v12\n"
"v_fma_f32 v12,v12,v11,s13\n"
AW "a95,v12\n"
AR "v12,a115\n"
"v_subrev_f32 v12,s13,v12\n"
"v_fma_f32 v12,v12,v11,s13\n"
AW "a115,v12\n"
AR "v12,a135\n"
"v_subrev_f32 v12,s13,v12\n"
"v_fma_f32 v12,v12,v11,s13\n"
AW "a135,v12\n"
AR "v12,a155\n"
"v_subrev_f32 v12,s13,v12\n"
"v_fma_f32 v12,v12,v11,s13\n"
AW "a155,v12\n"
AR "v12,a175\n"
"v_subrev_f32 v12,s13,v12\n"
"v_fma_f32 v12,v12,v11,s13\n"
AW "a175,v12\n"
AR "v12,a195\n"
"v_subrev_f32 v12,s13,v12\n"
"v_fma_f32 v12,v12,v11,s13\n"
AW "a195,v12\n"
AR "v12,a215\n"
"v_subrev_f32 v12,s13,v12\n"
"v_fma_f32 v12,v12,v11,s13\n"
AW "a215,v12\n"
AR "v12,a235\n"
"v_subrev_f32 v12,s13,v12\n"
"v_fma_f32 v12,v12,v11,s13\n"
AW "a235,v12\n"
"v_mul_f32 v55,v60,v11\n"
"v_sub_f32 v44,v44,v10\n"
"v_sub_f32 v45,v45,v10\n"
"v_sub_f32 v46,v46,v10\n"
"v_sub_f32 v47,v47,v10\n"
"v_sub_f32 v104,v104,v10\n"
"v_sub_f32 v105,v105,v10\n"
"v_sub_f32 v106,v106,v10\n"
"v_sub_f32 v107,v107,v10\n"
"v_sub_f32 v84,v84,v10\n"
"v_sub_f32 v85,v85,v10\n"
"v_sub_f32 v86,v86,v10\n"
"v_sub_f32 v87,v87,v10\n"
"v_sub_f32 v124,v124,v10\n"
"v_sub_f32 v125,v125,v10\n"
"v_sub_f32 v126,v126,v10\n"
"v_sub_f32 v127,v127,v10\n"
"v_exp_f32 v84,v84\n"
"v_exp_f32 v85,v85\n"
"v_exp_f32 v86,v86\n"
"v_exp_f32 v87,v87\n"
"v_exp_f32 v124,v124\n"
"v_exp_f32 v125,v125\n"
"v_exp_f32 v126,v126\n"
"v_exp_f32 v127,v127\n"
"v_add_f32 v55,v55,v84\n"
"v_add_f32 v55,v55,v85\n"
"v_add_f32 v55,v55,v86\n"
"v_add_f32 v55,v55,v87\n"
"v_add_f32 v55,v55,v124\n"
"v_add_f32 v55,v55,v125\n"
"v_add_f32 v55,v55,v126\n"
"v_add_f32_e64 v55,v55,v127\n"
CV "v84,v84,v85\n"
CV "v85,v86,v87\n"
CV "v86,v124,v125\n"
CV "v87,v126,v127\n"
"v_max3_f32 v10,v88,v89,v90\n"
"v_max3_f32 v11,v91,v128,v129\n"
"v_max3_f32 v10,v10,v130,v131\n"
"v_max_f32 v10,v10,v11\n"
"v_mov_b32 v11,v10\n"
"s_nop 1\n"
"v_permlane16_swap_b32 v10,v11\n"
"v_max_f32 v10,v10,v11\n"
"v_mov_b32 v11,v10\n"
"s_nop 1\n"
"v_permlane32_swap_b32 v10,v11\n"
"v_max_f32_e64 v10,v10,v11\n"
"v_add_f32 v10,0x41300000,v10\n"
"v_max_f32 v10,0,v10\n"
"v_exp_f32 v11,-v10\n"
AR "v12,a16\n"
"v_subrev_f32 v12,s13,v12\n"
"v_fma_f32 v12,v12,v11,s13\n"
AW "a16,v12\n"
AR "v12,a36\n"
"v_subrev_f32 v12,s13,v12\n"
"v_fma_f32 v12,v12,v11,s13\n"
AW "a36,v12\n"
AR "v12,a56\n"
"v_subrev_f32 v12,s13,v12\n"
"v_fma_f32 v12,v12,v11,s13\n"
AW "a56,v12\n"
AR "v12,a76\n"
"v_subrev_f32 v12,s13,v12\n"
"v_fma_f32 v12,v12,v11,s13\n"
AW "a76,v12\n"
AR "v12,a96\n"
"v_subrev_f32 v12,s13,v12\n"
"v_fma_f32 v12,v12,v11,s13\n"
AW "a96,v12\n"
AR "v12,a116\n"
"v_subrev_f32 v12,s13,v12\n"
"v_fma_f32 v12,v12,v11,s13\n"
AW "a116,v12\n"
AR "v12,a136\n"
"v_subrev_f32 v12,s13,v12\n"
"v_fma_f32 v12,v12,v11,s13\n"
AW "a136,v12\n"
AR "v12,a156\n"
"v_subrev_f32 v12,s13,v12\n"
"v_fma_f32 v12,v12,v11,s13\n"
AW "a156,v12\n"
AR "v12,a176\n"
"v_subrev_f32 v12,s13,v12\n"
"v_fma_f32 v12,v12,v11,s13\n"
AW "a176,v12\n"
AR "v12,a196\n"
"v_subrev_f32 v12,s13,v12\n"
"v_fma_f32 v12,v12,v11,s13\n"
AW "a196,v12\n"
AR "v12,a216\n"
"v_subrev_f32 v12,s13,v12\n"
"v_fma_f32 v12,v12,v11,s13\n"
AW "a216,v12\n"
AR "v12,a236\n"
"v_subrev_f32 v12,s13,v12\n"
"v_fma_f32 v12,v12,v11,s13\n"
AW "a236,v12\n"
AR "v12,a17\n"
"v_subrev_f32 v12,s13,v12\n"
"v_fma_f32 v12,v12,v11,s13\n"
AW "a17,v12\n"
AR "v12,a37\n"
"v_subrev_f32 v12,s13,v12\n"
"v_fma_f32 v12,v12,v11,s13\n"
AW "a37,v12\n"
AR "v12,a57\n"
"v_subrev_f32 v12,s13,v12\n"
"v_fma_f32 v12,v12,v11,s13\n"
AW "a57,v12\n"
AR "v12,a77\n"
"v_subrev_f32 v12,s13,v12\n"
"v_fma_f32 v12,v12,v11,s13\n"
AW "a77,v12\n"
AR "v12,a97\n"
"v_subrev_f32 v12,s13,v12\n"
"v_fma_f32 v12,v12,v11,s13\n"
AW "a97,v12\n"
AR "v12,a117\n"
"v_subrev_f32 v12,s13,v12\n"
"v_fma_f32 v12,v12,v11,s13\n"
AW "a117,v12\n"
AR "v12,a137\n"
"v_subrev_f32 v12,s13,v12\n"
"v_fma_f32 v12,v12,v11,s13\n"
AW "a137,v12\n"
AR "v12,a157\n"
"v_subrev_f32 v12,s13,v12\n"
"v_fma_f32 v12,v12,v11,s13\n"
AW "a157,v12\n"
AR "v12,a177\n"
"v_subrev_f32 v12,s13,v12\n"
"v_fma_f32 v12,v12,v11,s13\n"
AW "a177,v12\n"
AR "v12,a197\n"
"v_subrev_f32 v12,s13,v12\n"
"v_fma_f32 v12,v12,v11,s13\n"
AW "a197,v12\n"
AR "v12,a217\n"
"v_subrev_f32 v12,s13,v12\n"
"v_fma_f32 v12,v12,v11,s13\n"
AW "a217,v12\n"
AR "v12,a237\n"
"v_subrev_f32 v12,s13,v12\n"
"v_fma_f32 v12,v12,v11,s13\n"
AW "a237,v12\n"
AR "v12,a18\n"
"v_subrev_f32 v12,s13,v12\n"
"v_fma_f32 v12,v12,v11,s13\n"
AW "a18,v12\n"
AR "v12,a38\n"
"v_subrev_f32 v12,s13,v12\n"
"v_fma_f32 v12,v12,v11,s13\n"
AW "a38,v12\n"
AR "v12,a58\n"
"v_subrev_f32 v12,s13,v12\n"
"v_fma_f32 v12,v12,v11,s13\n"
AW "a58,v12\n"
AR "v12,a78\n"
"v_subrev_f32 v12,s13,v12\n"
"v_fma_f32 v12,v12,v11,s13\n"
AW "a78,v12\n"
AR "v12,a98\n"
"v_subrev_f32 v12,s13,v12\n"
"v_fma_f32 v12,v12,v11,s13\n"
AW "a98,v12\n"
AR "v12,a118\n"
"v_subrev_f32 v12,s13,v12\n"
"v_fma_f32 v12,v12,v11,s13\n"
AW "a118,v12\n"
AR "v12,a138\n"
"v_subrev_f32 v12,s13,v12\n"
"v_fma_f32 v12,v12,v11,s13\n"
AW "a138,v12\n"
AR "v12,a158\n"
"v_subrev_f32 v12,s13,v12\n"
"v_fma_f32 v12,v12,v11,s13\n"
AW "a158,v12\n"
AR "v12,a178\n"
"v_subrev_f32 v12,s13,v12\n"
"v_fma_f32 v12,v12,v11,s13\n"
AW "a178,v12\n"
AR "v12,a198\n"
"v_subrev_f32 v12,s13,v12\n"
"v_fma_f32 v12,v12,v11,s13\n"
AW "a198,v12\n"
AR "v12,a218\n"
"v_subrev_f32 v12,s13,v12\n"
"v_fma_f32 v12,v12,v11,s13\n"
AW "a218,v12\n"
AR "v12,a238\n"
"v_subrev_f32 v12,s13,v12\n"
"v_fma_f32 v12,v12,v11,s13\n"
AW "a238,v12\n"
AR "v12,a19\n"
"v_subrev_f32 v12,s13,v12\n"
"v_fma_f32 v12,v12,v11,s13\n"
AW "a19,v12\n"
AR "v12,a39\n"
"v_subrev_f32 v12,s13,v12\n"
"v_fma_f32 v12,v12,v11,s13\n"
AW "a39,v12\n"
AR "v12,a59\n"
"v_subrev_f32 v12,s13,v12\n"
"v_fma_f32 v12,v12,v11,s13\n"
AW "a59,v12\n"
AR "v12,a79\n"
"v_subrev_f32 v12,s13,v12\n"
"v_fma_f32 v12,v12,v11,s13\n"
AW "a79,v12\n"
AR "v12,a99\n"
"v_subrev_f32 v12,s13,v12\n"
"v_fma_f32 v12,v12,v11,s13\n"
AW "a99,v12\n"
AR "v12,a119\n"
"v_subrev_f32 v12,s13,v12\n"
"v_fma_f32 v12,v12,v11,s13\n"
AW "a119,v12\n"
AR "v12,a139\n"
"v_subrev_f32 v12,s13,v12\n"
"v_fma_f32 v12,v12,v11,s13\n"
AW "a139,v12\n"
AR "v12,a159\n"
"v_subrev_f32 v12,s13,v12\n"
"v_fma_f32 v12,v12,v11,s13\n"
AW "a159,v12\n"
AR "v12,a179\n"
"v_subrev_f32 v12,s13,v12\n"
"v_fma_f32 v12,v12,v11,s13\n"
AW "a179,v12\n"
AR "v12,a199\n"
"v_subrev_f32 v12,s13,v12\n"
"v_fma_f32 v12,v12,v11,s13\n"
AW "a199,v12\n"
AR "v12,a219\n"
"v_subrev_f32 v12,s13,v12\n"
"v_fma_f32 v12,v12,v11,s13\n"
AW "a219,v12\n"
AR "v12,a239\n"
"v_subrev_f32 v12,s13,v12\n"
"v_fma_f32 v12,v12,v11,s13\n"
AW "a239,v12\n"
"v_mul_f32 v56,v61,v11\n"
"v_sub_f32 v48,v48,v10\n"
"v_sub_f32 v49,v49,v10\n"
"v_sub_f32 v50,v50,v10\n"
"v_sub_f32 v51,v51,v10\n"
"v_sub_f32 v108,v108,v10\n"
"v_sub_f32 v109,v109,v10\n"
"v_sub_f32 v110,v110,v10\n"
"v_sub_f32 v111,v111,v10\n"
"v_sub_f32 v88,v88,v10\n"
"v_sub_f32 v89,v89,v10\n"
"v_sub_f32 v90,v90,v10\n"
"v_sub_f32 v91,v91,v10\n"
"v_sub_f32 v128,v128,v10\n"
"v_sub_f32 v129,v129,v10\n"
"v_sub_f32 v130,v130,v10\n"
"v_sub_f32 v131,v131,v10\n"
"v_exp_f32 v88,v88\n"
"v_exp_f32 v89,v89\n"
"v_exp_f32 v90,v90\n"
"v_exp_f32 v91,v91\n"
"v_exp_f32 v128,v128\n"
"v_exp_f32 v129,v129\n"
"v_exp_f32 v130,v130\n"
"v_exp_f32 v131,v131\n"
"v_add_f32 v56,v56,v88\n"
"v_add_f32 v56,v56,v89\n"
"v_add_f32 v56,v56,v90\n"
"v_add_f32 v56,v56,v91\n"
"v_add_f32 v56,v56,v128\n"
"v_add_f32 v56,v56,v129\n"
"v_add_f32 v56,v56,v130\n"
"v_add_f32 v56,v56,v131\n"
CV "v88,v88,v89\n"
CV "v89,v90,v91\n"
CV "v90,v128,v129\n"
CV "v91,v130,v131\n"
"s_nop 7\n"
"s_setpc_b64 s[52:53]\n"
".Lslow_1:\n"
"s_nop 7\n"
"s_nop 7\n"
"v_and_b32 v10,240,v9\n"
"v_mul_u32_u24 v10,24,v10\n"
"v_lshrrev_b32 v11,4,v9\n"
"v_and_or_b32 v10,v11,48,v10\n"
"s_mov_b32 s59,s34\n"
"s_mov_b32 s34,s20\n"
"s_add_u32 s58,s74,0\n"
"s_nop 3\n"
LD "v[58:61],v10,s[32:35],s58 offen\n"
"s_waitcnt vmcnt(0)\n"
MF "v[92:95],v[58:61],v[136:139],v[32:35]\n"
MF "v[96:99],v[58:61],v[140:143],v[36:39]\n"
MF "v[100:103],v[58:61],v[144:147],v[40:43]\n"
MF "v[104:107],v[58:61],v[148:151],v[44:47]\n"
MF "v[108:111],v[58:61],v[152:155],v[48:51]\n"
"s_add_u32 s58,s74,0\n"
"s_nop 3\n"
LD "v[58:61],v10,s[32:35],s58 offen offset:64\n"
"s_waitcnt vmcnt(0)\n"
MF "v[92:95],v[58:61],v[156:159],v[92:95]\n"
MF "v[96:99],v[58:61],v[160:163],v[96:99]\n"
MF "v[100:103],v[58:61],v[164:167],v[100:103]\n"
MF "v[104:107],v[58:61],v[168:171],v[104:107]\n"
MF "v[108:111],v[58:61],v[172:175],v[108:111]\n"
"s_add_u32 s58,s74,0\n"
"s_nop 3\n"
LD "v[58:61],v10,s[32:35],s58 offen offset:128\n"
"s_waitcnt vmcnt(0)\n"
MF "v[92:95],v[58:61],v[176:179],v[92:95]\n"
MF "v[96:99],v[58:61],v[180:183],v[96:99]\n"
MF "v[100:103],v[58:61],v[184:187],v[100:103]\n"
MF "v[104:107],v[58:61],v[188:191],v[104:107]\n"
MF "v[108:111],v[58:61],v[192:195],v[108:111]\n"
"s_add_u32 s58,s74,0\n"
"s_nop 3\n"
LD "v[58:61],v10,s[32:35],s58 offen offset:192\n"
"s_waitcnt vmcnt(0)\n"
MF "v[92:95],v[58:61],v[196:199],v[92:95]\n"
MF "v[96:99],v[58:61],v[200:203],v[96:99]\n"
MF "v[100:103],v[58:61],v[204:207],v[100:103]\n"
MF "v[104:107],v[58:61],v[208:211],v[104:107]\n"
MF "v[108:111],v[58:61],v[212:215],v[108:111]\n"
"s_add_u32 s58,s74,0\n"
"s_nop 3\n"
LD "v[58:61],v10,s[32:35],s58 offen offset:256\n"
"s_waitcnt vmcnt(0)\n"
MF "v[92:95],v[58:61],v[216:219],v[92:95]\n"
MF "v[96:99],v[58:61],v[220:223],v[96:99]\n"
MF "v[100:103],v[58:61],v[224:227],v[100:103]\n"
MF "v[104:107],v[58:61],v[228:231],v[104:107]\n"
MF "v[108:111],v[58:61],v[232:235],v[108:111]\n"
"s_add_u32 s58,s74,0\n"
"s_nop 3\n"
LD "v[58:61],v10,s[32:35],s58 offen offset:320\n"
"s_waitcnt vmcnt(0)\n"
MF "v[92:95],v[58:61],v[236:239],v[92:95]\n"
MF "v[96:99],v[58:61],v[240:243],v[96:99]\n"
MF "v[100:103],v[58:61],v[244:247],v[100:103]\n"
MF "v[104:107],v[58:61],v[248:251],v[104:107]\n"
MF "v[108:111],v[58:61],v[252:255],v[108:111]\n"
"s_add_u32 s58,s74,6144\n"
"s_nop 3\n"
LD "v[58:61],v10,s[32:35],s58 offen\n"
"s_waitcnt vmcnt(0)\n"
MF "v[112:115],v[58:61],v[136:139],v[32:35]\n"
MF "v[116:119],v[58:61],v[140:143],v[36:39]\n"
MF "v[120:123],v[58:61],v[144:147],v[40:43]\n"
MF "v[124:127],v[58:61],v[148:151],v[44:47]\n"
MF "v[128:131],v[58:61],v[152:155],v[48:51]\n"
"s_add_u32 s58,s74,6144\n"
"s_nop 3\n"
LD "v[58:61],v10,s[32:35],s58 offen offset:64\n"
"s_waitcnt vmcnt(0)\n"
MF "v[112:115],v[58:61],v[156:159],v[112:115]\n"
MF "v[116:119],v[58:61],v[160:163],v[116:119]\n"
MF "v[120:123],v[58:61],v[164:167],v[120:123]\n"
MF "v[124:127],v[58:61],v[168:171],v[124:127]\n"
MF "v[128:131],v[58:61],v[172:175],v[128:131]\n"
"s_add_u32 s58,s74,6144\n"
"s_nop 3\n"
LD "v[58:61],v10,s[32:35],s58 offen offset:128\n"
"s_waitcnt vmcnt(0)\n"
MF "v[112:115],v[58:61],v[176:179],v[112:115]\n"
MF "v[116:119],v[58:61],v[180:183],v[116:119]\n"
MF "v[120:123],v[58:61],v[184:187],v[120:123]\n"
MF "v[124:127],v[58:61],v[188:191],v[124:127]\n"
MF "v[128:131],v[58:61],v[192:195],v[128:131]\n"
"s_add_u32 s58,s74,6144\n"
"s_nop 3\n"
LD "v[58:61],v10,s[32:35],s58 offen offset:192\n"
"s_waitcnt vmcnt(0)\n"
MF "v[112:115],v[58:61],v[196:199],v[112:115]\n"
MF "v[116:119],v[58:61],v[200:203],v[116:119]\n"
MF "v[120:123],v[58:61],v[204:207],v[120:123]\n"
MF "v[124:127],v[58:61],v[208:211],v[124:127]\n"
MF "v[128:131],v[58:61],v[212:215],v[128:131]\n"
"s_add_u32 s58,s74,6144\n"
"s_nop 3\n"
LD "v[58:61],v10,s[32:35],s58 offen offset:256\n"
"s_waitcnt vmcnt(0)\n"
MF "v[112:115],v[58:61],v[216:219],v[112:115]\n"
MF "v[116:119],v[58:61],v[220:223],v[116:119]\n"
MF "v[120:123],v[58:61],v[224:227],v[120:123]\n"
MF "v[124:127],v[58:61],v[228:231],v[124:127]\n"
MF "v[128:131],v[58:61],v[232:235],v[128:131]\n"
"s_add_u32 s58,s74,6144\n"
"s_nop 3\n"
LD "v[58:61],v10,s[32:35],s58 offen offset:320\n"
"s_waitcnt vmcnt(0)\n"
MF "v[112:115],v[58:61],v[236:239],v[112:115]\n"
MF "v[116:119],v[58:61],v[240:243],v[116:119]\n"
MF "v[120:123],v[58:61],v[244:247],v[120:123]\n"
MF "v[124:127],v[58:61],v[248:251],v[124:127]\n"
MF "v[128:131],v[58:61],v[252:255],v[128:131]\n"
"s_nop 7\n"
"s_mov_b32 s34,s59\n"
"v_max3_f32 v10,v92,v93,v94\n"
"s_nop 0\n"
"v_max3_f32 v11,v95,v112,v113\n"
"v_max3_f32 v10,v10,v114,v115\n"
"v_max_f32 v10,v10,v11\n"
"v_mov_b32 v11,v10\n"
"s_nop 1\n"
"v_permlane16_swap_b32 v10,v11\n"
"v_max_f32 v10,v10,v11\n"
"v_mov_b32 v11,v10\n"
"s_nop 1\n"
"v_permlane32_swap_b32 v10,v11\n"
"v_max_f32 v10,v10,v11\n"
"v_add_f32 v10,0x41300000,v10\n"
"v_max_f32 v10,0,v10\n"
"v_exp_f32 v11,-v10\n"
AR "v12,a0\n"
"v_subrev_f32 v12,s13,v12\n"
"v_fma_f32 v12,v12,v11,s13\n"
AW "a0,v12\n"
AR "v12,a20\n"
"v_subrev_f32 v12,s13,v12\n"
"v_fma_f32 v12,v12,v11,s13\n"
AW "a20,v12\n"
AR "v12,a40\n"
"v_subrev_f32 v12,s13,v12\n"
"v_fma_f32 v12,v12,v11,s13\n"
AW "a40,v12\n"
AR "v12,a60\n"
"v_subrev_f32 v12,s13,v12\n"
"v_fma_f32 v12,v12,v11,s13\n"
AW "a60,v12\n"
AR "v12,a80\n"
"v_subrev_f32 v12,s13,v12\n"
"v_fma_f32 v12,v12,v11,s13\n"
AW "a80,v12\n"
AR "v12,a100\n"
"v_subrev_f32 v12,s13,v12\n"
"v_fma_f32 v12,v12,v11,s13\n"
AW "a100,v12\n"
AR "v12,a120\n"
"v_subrev_f32 v12,s13,v12\n"
"v_fma_f32 v12,v12,v11,s13\n"
AW "a120,v12\n"
AR "v12,a140\n"
"v_subrev_f32 v12,s13,v12\n"
"v_fma_f32 v12,v12,v11,s13\n"
AW "a140,v12\n"
AR "v12,a160\n"
"v_subrev_f32 v12,s13,v12\n"
"v_fma_f32 v12,v12,v11,s13\n"
AW "a160,v12\n"
AR "v12,a180\n"
"v_subrev_f32 v12,s13,v12\n"
"v_fma_f32 v12,v12,v11,s13\n"
AW "a180,v12\n"
AR "v12,a200\n"
"v_subrev_f32 v12,s13,v12\n"
"v_fma_f32 v12,v12,v11,s13\n"
AW "a200,v12\n"
AR "v12,a220\n"
"v_subrev_f32 v12,s13,v12\n"
"v_fma_f32 v12,v12,v11,s13\n"
AW "a220,v12\n"
AR "v12,a1\n"
"v_subrev_f32 v12,s13,v12\n"
"v_fma_f32 v12,v12,v11,s13\n"
AW "a1,v12\n"
AR "v12,a21\n"
"v_subrev_f32 v12,s13,v12\n"
"v_fma_f32 v12,v12,v11,s13\n"
AW "a21,v12\n"
AR "v12,a41\n"
"v_subrev_f32 v12,s13,v12\n"
"v_fma_f32 v12,v12,v11,s13\n"
AW "a41,v12\n"
AR "v12,a61\n"
"v_subrev_f32 v12,s13,v12\n"
"v_fma_f32 v12,v12,v11,s13\n"
AW "a61,v12\n"
AR "v12,a81\n"
"v_subrev_f32 v12,s13,v12\n"
"v_fma_f32 v12,v12,v11,s13\n"
AW "a81,v12\n"
AR "v12,a101\n"
"v_subrev_f32 v12,s13,v12\n"
"v_fma_f32 v12,v12,v11,s13\n"
AW "a101,v12\n"
AR "v12,a121\n"
"v_subrev_f32 v12,s13,v12\n"
"v_fma_f32 v12,v12,v11,s13\n"
AW "a121,v12\n"
AR "v12,a141\n"
"v_subrev_f32 v12,s13,v12\n"
"v_fma_f32 v12,v12,v11,s13\n"
AW "a141,v12\n"
AR "v12,a161\n"
"v_subrev_f32 v12,s13,v12\n"
"v_fma_f32 v12,v12,v11,s13\n"
AW "a161,v12\n"
AR "v12,a181\n"
"v_subrev_f32 v12,s13,v12\n"
"v_fma_f32 v12,v12,v11,s13\n"
AW "a181,v12\n"
AR "v12,a201\n"
"v_subrev_f32 v12,s13,v12\n"
"v_fma_f32 v12,v12,v11,s13\n"
AW "a201,v12\n"
AR "v12,a221\n"
"v_subrev_f32 v12,s13,v12\n"
"v_fma_f32 v12,v12,v11,s13\n"
AW "a221,v12\n"
AR "v12,a2\n"
"v_subrev_f32 v12,s13,v12\n"
"v_fma_f32 v12,v12,v11,s13\n"
AW "a2,v12\n"
AR "v12,a22\n"
"v_subrev_f32 v12,s13,v12\n"
"v_fma_f32 v12,v12,v11,s13\n"
AW "a22,v12\n"
AR "v12,a42\n"
"v_subrev_f32 v12,s13,v12\n"
"v_fma_f32 v12,v12,v11,s13\n"
AW "a42,v12\n"
AR "v12,a62\n"
"v_subrev_f32 v12,s13,v12\n"
"v_fma_f32 v12,v12,v11,s13\n"
AW "a62,v12\n"
AR "v12,a82\n"
"v_subrev_f32 v12,s13,v12\n"
"v_fma_f32 v12,v12,v11,s13\n"
AW "a82,v12\n"
AR "v12,a102\n"
"v_subrev_f32 v12,s13,v12\n"
"v_fma_f32 v12,v12,v11,s13\n"
AW "a102,v12\n"
AR "v12,a122\n"
"v_subrev_f32 v12,s13,v12\n"
"v_fma_f32 v12,v12,v11,s13\n"
AW "a122,v12\n"
AR "v12,a142\n"
"v_subrev_f32 v12,s13,v12\n"
"v_fma_f32 v12,v12,v11,s13\n"
AW "a142,v12\n"
AR "v12,a162\n"
"v_subrev_f32 v12,s13,v12\n"
"v_fma_f32 v12,v12,v11,s13\n"
AW "a162,v12\n"
AR "v12,a182\n"
"v_subrev_f32 v12,s13,v12\n"
"v_fma_f32 v12,v12,v11,s13\n"
AW "a182,v12\n"
AR "v12,a202\n"
"v_subrev_f32 v12,s13,v12\n"
"v_fma_f32 v12,v12,v11,s13\n"
AW "a202,v12\n"
AR "v12,a222\n"
"v_subrev_f32 v12,s13,v12\n"
"v_fma_f32 v12,v12,v11,s13\n"
AW "a222,v12\n"
AR "v12,a3\n"
"v_subrev_f32 v12,s13,v12\n"
"v_fma_f32 v12,v12,v11,s13\n"
AW "a3,v12\n"
AR "v12,a23\n"
"v_subrev_f32 v12,s13,v12\n"
"v_fma_f32 v12,v12,v11,s13\n"
AW "a23,v12\n"
AR "v12,a43\n"
"v_subrev_f32 v12,s13,v12\n"
"v_fma_f32 v12,v12,v11,s13\n"
AW "a43,v12\n"
AR "v12,a63\n"
"v_subrev_f32 v12,s13,v12\n"
"v_fma_f32 v12,v12,v11,s13\n"
AW "a63,v12\n"
AR "v12,a83\n"
"v_subrev_f32 v12,s13,v12\n"
"v_fma_f32 v12,v12,v11,s13\n"
AW "a83,v12\n"
AR "v12,a103\n"
"v_subrev_f32 v12,s13,v12\n"
"v_fma_f32 v12,v12,v11,s13\n"
AW "a103,v12\n"
AR "v12,a123\n"
"v_subrev_f32 v12,s13,v12\n"
"v_fma_f32 v12,v12,v11,s13\n"
AW "a123,v12\n"
AR "v12,a143\n"
"v_subrev_f32 v12,s13,v12\n"
"v_fma_f32 v12,v12,v11,s13\n"
AW "a143,v12\n"
AR "v12,a163\n"
"v_subrev_f32 v12,s13,v12\n"
"v_fma_f32 v12,v12,v11,s13\n"
AW "a163,v12\n"
AR "v12,a183\n"
"v_subrev_f32 v12,s13,v12\n"
"v_fma_f32 v12,v12,v11,s13\n"
AW "a183,v12\n"
AR "v12,a203\n"
"v_subrev_f32 v12,s13,v12\n"
"v_fma_f32 v12,v12,v11,s13\n"
AW "a203,v12\n"
AR "v12,a223\n"
"v_subrev_f32 v12,s13,v12\n"
"v_fma_f32 v12,v12,v11,s13\n"
AW "a223,v12\n"
"v_mul_f32 v57,v52,v11\n"
"v_sub_f32 v32,v32,v10\n"
"v_sub_f32 v33,v33,v10\n"
"v_sub_f32 v34,v34,v10\n"
"v_sub_f32 v35,v35,v10\n"
"v_sub_f32 v72,v72,v10\n"
"v_sub_f32 v73,v73,v10\n"
"v_sub_f32 v74,v74,v10\n"
"v_sub_f32 v75,v75,v10\n"
"v_sub_f32 v92,v92,v10\n"
"v_sub_f32 v93,v93,v10\n"
"v_sub_f32 v94,v94,v10\n"
"v_sub_f32 v95,v95,v10\n"
"v_sub_f32 v112,v112,v10\n"
"v_sub_f32 v113,v113,v10\n"
"v_sub_f32 v114,v114,v10\n"
"v_sub_f32 v115,v115,v10\n"
"v_exp_f32 v92,v92\n"
"v_exp_f32 v93,v93\n"
"v_exp_f32 v94,v94\n"
"v_exp_f32 v95,v95\n"
"v_exp_f32 v112,v112\n"
"v_exp_f32 v113,v113\n"
"v_exp_f32 v114,v114\n"
"v_exp_f32 v115,v115\n"
"v_add_f32 v57,v57,v92\n"
"v_add_f32 v57,v57,v93\n"
"v_add_f32 v57,v57,v94\n"
"v_add_f32 v57,v57,v95\n"
"v_add_f32 v57,v57,v112\n"
"v_add_f32 v57,v57,v113\n"
"v_add_f32 v57,v57,v114\n"
"v_add_f32 v57,v57,v115\n"
CV "v92,v92,v93\n"
CV "v93,v94,v95\n"
CV "v94,v112,v113\n"
CV "v95,v114,v115\n"
"v_max3_f32 v10,v96,v97,v98\n"
"v_max3_f32 v11,v99,v116,v117\n"
"v_max3_f32 v10,v10,v118,v119\n"
"v_max_f32 v10,v10,v11\n"
"v_mov_b32 v11,v10\n"
"s_nop 1\n"
"v_permlane16_swap_b32 v10,v11\n"
"v_max_f32 v10,v10,v11\n"
"v_mov_b32 v11,v10\n"
"s_nop 1\n"
"v_permlane32_swap_b32 v10,v11\n"
"v_max_f32 v10,v10,v11\n"
"v_add_f32 v10,0x41300000,v10\n"
"v_max_f32 v10,0,v10\n"
"v_exp_f32 v11,-v10\n"
AR "v12,a4\n"
"v_subrev_f32 v12,s13,v12\n"
"v_fma_f32 v12,v12,v11,s13\n"
AW "a4,v12\n"
AR "v12,a24\n"
"v_subrev_f32 v12,s13,v12\n"
"v_fma_f32 v12,v12,v11,s13\n"
AW "a24,v12\n"
AR "v12,a44\n"
"v_subrev_f32 v12,s13,v12\n"
"v_fma_f32 v12,v12,v11,s13\n"
AW "a44,v12\n"
AR "v12,a64\n"
"v_subrev_f32 v12,s13,v12\n"
"v_fma_f32 v12,v12,v11,s13\n"
AW "a64,v12\n"
AR "v12,a84\n"
"v_subrev_f32 v12,s13,v12\n"
"v_fma_f32 v12,v12,v11,s13\n"
AW "a84,v12\n"
AR "v12,a104\n"
"v_subrev_f32 v12,s13,v12\n"
"v_fma_f32 v12,v12,v11,s13\n"
AW "a104,v12\n"
AR "v12,a124\n"
"v_subrev_f32 v12,s13,v12\n"
"v_fma_f32 v12,v12,v11,s13\n"
AW "a124,v12\n"
AR "v12,a144\n"
"v_subrev_f32 v12,s13,v12\n"
"v_fma_f32 v12,v12,v11,s13\n"
AW "a144,v12\n"
AR "v12,a164\n"
"v_subrev_f32 v12,s13,v12\n"
"v_fma_f32 v12,v12,v11,s13\n"
AW "a164,v12\n"
AR "v12,a184\n"
"v_subrev_f32 v12,s13,v12\n"
"v_fma_f32 v12,v12,v11,s13\n"
AW "a184,v12\n"
AR "v12,a204\n"
"v_subrev_f32 v12,s13,v12\n"
"v_fma_f32 v12,v12,v11,s13\n"
AW "a204,v12\n"
AR "v12,a224\n"
"v_subrev_f32 v12,s13,v12\n"
"v_fma_f32 v12,v12,v11,s13\n"
AW "a224,v12\n"
AR "v12,a5\n"
"v_subrev_f32 v12,s13,v12\n"
"v_fma_f32 v12,v12,v11,s13\n"
AW "a5,v12\n"
AR "v12,a25\n"
"v_subrev_f32 v12,s13,v12\n"
"v_fma_f32 v12,v12,v11,s13\n"
AW "a25,v12\n"
AR "v12,a45\n"
"v_subrev_f32 v12,s13,v12\n"
"v_fma_f32 v12,v12,v11,s13\n"
AW "a45,v12\n"
AR "v12,a65\n"
"v_subrev_f32 v12,s13,v12\n"
"v_fma_f32 v12,v12,v11,s13\n"
AW "a65,v12\n"
AR "v12,a85\n"
"v_subrev_f32 v12,s13,v12\n"
"v_fma_f32 v12,v12,v11,s13\n"
AW "a85,v12\n"
AR "v12,a105\n"
"v_subrev_f32 v12,s13,v12\n"
"v_fma_f32 v12,v12,v11,s13\n"
AW "a105,v12\n"
AR "v12,a125\n"
"v_subrev_f32 v12,s13,v12\n"
"v_fma_f32 v12,v12,v11,s13\n"
AW "a125,v12\n"
AR "v12,a145\n"
"v_subrev_f32 v12,s13,v12\n"
"v_fma_f32 v12,v12,v11,s13\n"
AW "a145,v12\n"
AR "v12,a165\n"
"v_subrev_f32 v12,s13,v12\n"
"v_fma_f32 v12,v12,v11,s13\n"
AW "a165,v12\n"
AR "v12,a185\n"
"v_subrev_f32 v12,s13,v12\n"
"v_fma_f32 v12,v12,v11,s13\n"
AW "a185,v12\n"
AR "v12,a205\n"
"v_subrev_f32 v12,s13,v12\n"
"v_fma_f32 v12,v12,v11,s13\n"
AW "a205,v12\n"
AR "v12,a225\n"
"v_subrev_f32 v12,s13,v12\n"
"v_fma_f32 v12,v12,v11,s13\n"
AW "a225,v12\n"
AR "v12,a6\n"
"v_subrev_f32 v12,s13,v12\n"
"v_fma_f32 v12,v12,v11,s13\n"
AW "a6,v12\n"
AR "v12,a26\n"
"v_subrev_f32 v12,s13,v12\n"
"v_fma_f32 v12,v12,v11,s13\n"
AW "a26,v12\n"
AR "v12,a46\n"
"v_subrev_f32 v12,s13,v12\n"
"v_fma_f32 v12,v12,v11,s13\n"
AW "a46,v12\n"
AR "v12,a66\n"
"v_subrev_f32 v12,s13,v12\n"
"v_fma_f32 v12,v12,v11,s13\n"
AW "a66,v12\n"
AR "v12,a86\n"
"v_subrev_f32 v12,s13,v12\n"
"v_fma_f32 v12,v12,v11,s13\n"
AW "a86,v12\n"
AR "v12,a106\n"
"v_subrev_f32 v12,s13,v12\n"
"v_fma_f32 v12,v12,v11,s13\n"
AW "a106,v12\n"
AR "v12,a126\n"
"v_subrev_f32 v12,s13,v12\n"
"v_fma_f32 v12,v12,v11,s13\n"
AW "a126,v12\n"
AR "v12,a146\n"
"v_subrev_f32 v12,s13,v12\n"
"v_fma_f32 v12,v12,v11,s13\n"
AW "a146,v12\n"
AR "v12,a166\n"
"v_subrev_f32 v12,s13,v12\n"
"v_fma_f32 v12,v12,v11,s13\n"
AW "a166,v12\n"
AR "v12,a186\n"
"v_subrev_f32 v12,s13,v12\n"
"v_fma_f32 v12,v12,v11,s13\n"
AW "a186,v12\n"
AR "v12,a206\n"
"v_subrev_f32 v12,s13,v12\n"
"v_fma_f32 v12,v12,v11,s13\n"
AW "a206,v12\n"
AR "v12,a226\n"
"v_subrev_f32 v12,s13,v12\n"
"v_fma_f32 v12,v12,v11,s13\n"
AW "a226,v12\n"
AR "v12,a7\n"
"v_subrev_f32 v12,s13,v12\n"
"v_fma_f32 v12,v12,v11,s13\n"
AW "a7,v12\n"
AR "v12,a27\n"
"v_subrev_f32 v12,s13,v12\n"
"v_fma_f32 v12,v12,v11,s13\n"
AW "a27,v12\n"
AR "v12,a47\n"
"v_subrev_f32 v12,s13,v12\n"
"v_fma_f32 v12,v12,v11,s13\n"
AW "a47,v12\n"
AR "v12,a67\n"
"v_subrev_f32 v12,s13,v12\n"
"v_fma_f32 v12,v12,v11,s13\n"
AW "a67,v12\n"
AR "v12,a87\n"
"v_subrev_f32 v12,s13,v12\n"
"v_fma_f32 v12,v12,v11,s13\n"
AW "a87,v12\n"
AR "v12,a107\n"
"v_subrev_f32 v12,s13,v12\n"
"v_fma_f32 v12,v12,v11,s13\n"
AW "a107,v12\n"
AR "v12,a127\n"
"v_subrev_f32 v12,s13,v12\n"
"v_fma_f32 v12,v12,v11,s13\n"
AW "a127,v12\n"
AR "v12,a147\n"
"v_subrev_f32 v12,s13,v12\n"
"v_fma_f32 v12,v12,v11,s13\n"
AW "a147,v12\n"
AR "v12,a167\n"
"v_subrev_f32 v12,s13,v12\n"
"v_fma_f32 v12,v12,v11,s13\n"
AW "a167,v12\n"
AR "v12,a187\n"
"v_subrev_f32 v12,s13,v12\n"
"v_fma_f32 v12,v12,v11,s13\n"
AW "a187,v12\n"
AR "v12,a207\n"
"v_subrev_f32 v12,s13,v12\n"
"v_fma_f32 v12,v12,v11,s13\n"
AW "a207,v12\n"
AR "v12,a227\n"
"v_subrev_f32 v12,s13,v12\n"
"v_fma_f32 v12,v12,v11,s13\n"
AW "a227,v12\n"
"v_mul_f32 v58,v53,v11\n"
"v_sub_f32 v36,v36,v10\n"
"v_sub_f32 v37,v37,v10\n"
"v_sub_f32 v38,v38,v10\n"
"v_sub_f32 v39,v39,v10\n"
"v_sub_f32 v76,v76,v10\n"
"v_sub_f32 v77,v77,v10\n"
"v_sub_f32 v78,v78,v10\n"
"v_sub_f32 v79,v79,v10\n"
"v_sub_f32 v96,v96,v10\n"
"v_sub_f32 v97,v97,v10\n"
"v_sub_f32 v98,v98,v10\n"
"v_sub_f32 v99,v99,v10\n"
"v_sub_f32 v116,v116,v10\n"
"v_sub_f32 v117,v117,v10\n"
"v_sub_f32 v118,v118,v10\n"
"v_sub_f32 v119,v119,v10\n"
"v_exp_f32 v96,v96\n"
"v_exp_f32 v97,v97\n"
"v_exp_f32 v98,v98\n"
"v_exp_f32 v99,v99\n"
"v_exp_f32 v116,v116\n"
"v_exp_f32 v117,v117\n"
"v_exp_f32 v118,v118\n"
"v_exp_f32 v119,v119\n"
"v_add_f32 v58,v58,v96\n"
"v_add_f32 v58,v58,v97\n"
"v_add_f32 v58,v58,v98\n"
"v_add_f32 v58,v58,v99\n"
"v_add_f32 v58,v58,v116\n"
"v_add_f32 v58,v58,v117\n"
"v_add_f32 v58,v58,v118\n"
"v_add_f32_e64 v58,v58,v119\n"
CV "v96,v96,v97\n"
CV "v97,v98,v99\n"
CV "v98,v116,v117\n"
CV "v99,v118,v119\n"
"v_max3_f32 v10,v100,v101,v102\n"
"v_max3_f32 v11,v103,v120,v121\n"
"v_max3_f32 v10,v10,v122,v123\n"
"v_max_f32 v10,v10,v11\n"
"v_mov_b32 v11,v10\n"
"s_nop 1\n"
"v_permlane16_swap_b32 v10,v11\n"
"v_max_f32 v10,v10,v11\n"
"v_mov_b32 v11,v10\n"
"s_nop 1\n"
"v_permlane32_swap_b32 v10,v11\n"
"v_max_f32 v10,v10,v11\n"
"v_add_f32 v10,0x41300000,v10\n"
"v_max_f32 v10,0,v10\n"
"v_exp_f32 v11,-v10\n"
AR "v12,a8\n"
"v_subrev_f32 v12,s13,v12\n"
"v_fma_f32 v12,v12,v11,s13\n"
AW "a8,v12\n"
AR "v12,a28\n"
"v_subrev_f32 v12,s13,v12\n"
"v_fma_f32 v12,v12,v11,s13\n"
AW "a28,v12\n"
AR "v12,a48\n"
"v_subrev_f32 v12,s13,v12\n"
"v_fma_f32 v12,v12,v11,s13\n"
AW "a48,v12\n"
AR "v12,a68\n"
"v_subrev_f32 v12,s13,v12\n"
"v_fma_f32 v12,v12,v11,s13\n"
AW "a68,v12\n"
AR "v12,a88\n"
"v_subrev_f32 v12,s13,v12\n"
"v_fma_f32 v12,v12,v11,s13\n"
AW "a88,v12\n"
AR "v12,a108\n"
"v_subrev_f32 v12,s13,v12\n"
"v_fma_f32 v12,v12,v11,s13\n"
AW "a108,v12\n"
AR "v12,a128\n"
"v_subrev_f32 v12,s13,v12\n"
"v_fma_f32 v12,v12,v11,s13\n"
AW "a128,v12\n"
AR "v12,a148\n"
"v_subrev_f32 v12,s13,v12\n"
"v_fma_f32 v12,v12,v11,s13\n"
AW "a148,v12\n"
AR "v12,a168\n"
"v_subrev_f32 v12,s13,v12\n"
"v_fma_f32 v12,v12,v11,s13\n"
AW "a168,v12\n"
AR "v12,a188\n"
"v_subrev_f32 v12,s13,v12\n"
"v_fma_f32 v12,v12,v11,s13\n"
AW "a188,v12\n"
AR "v12,a208\n"
"v_subrev_f32 v12,s13,v12\n"
"v_fma_f32 v12,v12,v11,s13\n"
AW "a208,v12\n"
AR "v12,a228\n"
"v_subrev_f32 v12,s13,v12\n"
"v_fma_f32 v12,v12,v11,s13\n"
AW "a228,v12\n"
AR "v12,a9\n"
"v_subrev_f32 v12,s13,v12\n"
"v_fma_f32 v12,v12,v11,s13\n"
AW "a9,v12\n"
AR "v12,a29\n"
"v_subrev_f32 v12,s13,v12\n"
"v_fma_f32 v12,v12,v11,s13\n"
AW "a29,v12\n"
AR "v12,a49\n"
"v_subrev_f32 v12,s13,v12\n"
"v_fma_f32 v12,v12,v11,s13\n"
AW "a49,v12\n"
AR "v12,a69\n"
"v_subrev_f32 v12,s13,v12\n"
"v_fma_f32 v12,v12,v11,s13\n"
AW "a69,v12\n"
AR "v12,a89\n"
"v_subrev_f32 v12,s13,v12\n"
"v_fma_f32 v12,v12,v11,s13\n"
AW "a89,v12\n"
AR "v12,a109\n"
"v_subrev_f32 v12,s13,v12\n"
"v_fma_f32 v12,v12,v11,s13\n"
AW "a109,v12\n"
AR "v12,a129\n"
"v_subrev_f32 v12,s13,v12\n"
"v_fma_f32 v12,v12,v11,s13\n"
AW "a129,v12\n"
AR "v12,a149\n"
"v_subrev_f32 v12,s13,v12\n"
"v_fma_f32 v12,v12,v11,s13\n"
AW "a149,v12\n"
AR "v12,a169\n"
"v_subrev_f32 v12,s13,v12\n"
"v_fma_f32 v12,v12,v11,s13\n"
AW "a169,v12\n"
AR "v12,a189\n"
"v_subrev_f32 v12,s13,v12\n"
"v_fma_f32 v12,v12,v11,s13\n"
AW "a189,v12\n"
AR "v12,a209\n"
"v_subrev_f32 v12,s13,v12\n"
"v_fma_f32 v12,v12,v11,s13\n"
AW "a209,v12\n"
AR "v12,a229\n"
"v_subrev_f32 v12,s13,v12\n"
"v_fma_f32 v12,v12,v11,s13\n"
AW "a229,v12\n"
AR "v12,a10\n"
"v_subrev_f32 v12,s13,v12\n"
"v_fma_f32 v12,v12,v11,s13\n"
AW "a10,v12\n"
AR "v12,a30\n"
"v_subrev_f32 v12,s13,v12\n"
"v_fma_f32 v12,v12,v11,s13\n"
AW "a30,v12\n"
AR "v12,a50\n"
"v_subrev_f32 v12,s13,v12\n"
"v_fma_f32 v12,v12,v11,s13\n"
AW "a50,v12\n"
AR "v12,a70\n"
"v_subrev_f32 v12,s13,v12\n"
"v_fma_f32 v12,v12,v11,s13\n"
AW "a70,v12\n"
AR "v12,a90\n"
"v_subrev_f32 v12,s13,v12\n"
"v_fma_f32 v12,v12,v11,s13\n"
AW "a90,v12\n"
AR "v12,a110\n"
"v_subrev_f32 v12,s13,v12\n"
"v_fma_f32 v12,v12,v11,s13\n"
AW "a110,v12\n"
AR "v12,a130\n"
"v_subrev_f32 v12,s13,v12\n"
"v_fma_f32 v12,v12,v11,s13\n"
AW "a130,v12\n"
AR "v12,a150\n"
"v_subrev_f32 v12,s13,v12\n"
"v_fma_f32 v12,v12,v11,s13\n"
AW "a150,v12\n"
AR "v12,a170\n"
"v_subrev_f32 v12,s13,v12\n"
"v_fma_f32 v12,v12,v11,s13\n"
AW "a170,v12\n"
AR "v12,a190\n"
"v_subrev_f32 v12,s13,v12\n"
"v_fma_f32 v12,v12,v11,s13\n"
AW "a190,v12\n"
AR "v12,a210\n"
"v_subrev_f32 v12,s13,v12\n"
"v_fma_f32 v12,v12,v11,s13\n"
AW "a210,v12\n"
AR "v12,a230\n"
"v_subrev_f32 v12,s13,v12\n"
"v_fma_f32 v12,v12,v11,s13\n"
AW "a230,v12\n"
AR "v12,a11\n"
"v_subrev_f32 v12,s13,v12\n"
"v_fma_f32 v12,v12,v11,s13\n"
AW "a11,v12\n"
AR "v12,a31\n"
"v_subrev_f32 v12,s13,v12\n"
"v_fma_f32 v12,v12,v11,s13\n"
AW "a31,v12\n"
AR "v12,a51\n"
"v_subrev_f32 v12,s13,v12\n"
"v_fma_f32 v12,v12,v11,s13\n"
AW "a51,v12\n"
AR "v12,a71\n"
"v_subrev_f32 v12,s13,v12\n"
"v_fma_f32 v12,v12,v11,s13\n"
AW "a71,v12\n"
AR "v12,a91\n"
"v_subrev_f32 v12,s13,v12\n"
"v_fma_f32 v12,v12,v11,s13\n"
AW "a91,v12\n"
AR "v12,a111\n"
"v_subrev_f32 v12,s13,v12\n"
"v_fma_f32 v12,v12,v11,s13\n"
AW "a111,v12\n"
AR "v12,a131\n"
"v_subrev_f32 v12,s13,v12\n"
"v_fma_f32 v12,v12,v11,s13\n"
AW "a131,v12\n"
AR "v12,a151\n"
"v_subrev_f32 v12,s13,v12\n"
"v_fma_f32 v12,v12,v11,s13\n"
AW "a151,v12\n"
AR "v12,a171\n"
"v_subrev_f32 v12,s13,v12\n"
"v_fma_f32 v12,v12,v11,s13\n"
AW "a171,v12\n"
AR "v12,a191\n"
"v_subrev_f32 v12,s13,v12\n"
"v_fma_f32 v12,v12,v11,s13\n"
AW "a191,v12\n"
AR "v12,a211\n"
"v_subrev_f32 v12,s13,v12\n"
"v_fma_f32 v12,v12,v11,s13\n"
AW "a211,v12\n"
AR "v12,a231\n"
"v_subrev_f32 v12,s13,v12\n"
"v_fma_f32 v12,v12,v11,s13\n"
AW "a231,v12\n"
"v_mul_f32 v59,v54,v11\n"
"v_sub_f32 v40,v40,v10\n"
"v_sub_f32 v41,v41,v10\n"
"v_sub_f32 v42,v42,v10\n"
"v_sub_f32 v43,v43,v10\n"
"v_sub_f32 v80,v80,v10\n"
"v_sub_f32 v81,v81,v10\n"
"v_sub_f32 v82,v82,v10\n"
"v_sub_f32 v83,v83,v10\n"
"v_sub_f32 v100,v100,v10\n"
"v_sub_f32 v101,v101,v10\n"
"v_sub_f32 v102,v102,v10\n"
"v_sub_f32 v103,v103,v10\n"
"v_sub_f32 v120,v120,v10\n"
"v_sub_f32 v121,v121,v10\n"
"v_sub_f32 v122,v122,v10\n"
"v_sub_f32 v123,v123,v10\n"
"v_exp_f32 v100,v100\n"
"v_exp_f32 v101,v101\n"
"v_exp_f32 v102,v102\n"
"v_exp_f32 v103,v103\n"
"v_exp_f32 v120,v120\n"
"v_exp_f32 v121,v121\n"
"v_exp_f32 v122,v122\n"
"v_exp_f32 v123,v123\n"
"v_add_f32 v59,v59,v100\n"
"v_add_f32 v59,v59,v101\n"
"v_add_f32 v59,v59,v102\n"
"v_add_f32 v59,v59,v103\n"
"v_add_f32 v59,v59,v120\n"
"v_add_f32 v59,v59,v121\n"
"v_add_f32 v59,v59,v122\n"
"v_add_f32_e64 v59,v59,v123\n"
CV "v100,v100,v101\n"
CV "v101,v102,v103\n"
CV "v102,v120,v121\n"
CV "v103,v122,v123\n"
"v_max3_f32 v10,v104,v105,v106\n"
"v_max3_f32 v11,v107,v124,v125\n"
"v_max3_f32 v10,v10,v126,v127\n"
"v_max_f32 v10,v10,v11\n"
"v_mov_b32 v11,v10\n"
"s_nop 1\n"
"v_permlane16_swap_b32 v10,v11\n"
"v_max_f32 v10,v10,v11\n"
"v_mov_b32 v11,v10\n"
"s_nop 1\n"
"v_permlane32_swap_b32 v10,v11\n"
"v_max_f32_e64 v10,v10,v11\n"
"v_add_f32 v10,0x41300000,v10\n"
"v_max_f32 v10,0,v10\n"
"v_exp_f32 v11,-v10\n"
AR "v12,a12\n"
"v_subrev_f32 v12,s13,v12\n"
"v_fma_f32 v12,v12,v11,s13\n"
AW "a12,v12\n"
AR "v12,a32\n"
"v_subrev_f32 v12,s13,v12\n"
"v_fma_f32 v12,v12,v11,s13\n"
AW "a32,v12\n"
AR "v12,a52\n"
"v_subrev_f32 v12,s13,v12\n"
"v_fma_f32 v12,v12,v11,s13\n"
AW "a52,v12\n"
AR "v12,a72\n"
"v_subrev_f32 v12,s13,v12\n"
"v_fma_f32 v12,v12,v11,s13\n"
AW "a72,v12\n"
AR "v12,a92\n"
"v_subrev_f32 v12,s13,v12\n"
"v_fma_f32 v12,v12,v11,s13\n"
AW "a92,v12\n"
AR "v12,a112\n"
"v_subrev_f32 v12,s13,v12\n"
"v_fma_f32 v12,v12,v11,s13\n"
AW "a112,v12\n"
AR "v12,a132\n"
"v_subrev_f32 v12,s13,v12\n"
"v_fma_f32 v12,v12,v11,s13\n"
AW "a132,v12\n"
AR "v12,a152\n"
"v_subrev_f32 v12,s13,v12\n"
"v_fma_f32 v12,v12,v11,s13\n"
AW "a152,v12\n"
AR "v12,a172\n"
"v_subrev_f32 v12,s13,v12\n"
"v_fma_f32 v12,v12,v11,s13\n"
AW "a172,v12\n"
AR "v12,a192\n"
"v_subrev_f32 v12,s13,v12\n"
"v_fma_f32 v12,v12,v11,s13\n"
AW "a192,v12\n"
AR "v12,a212\n"
"v_subrev_f32 v12,s13,v12\n"
"v_fma_f32 v12,v12,v11,s13\n"
AW "a212,v12\n"
AR "v12,a232\n"
"v_subrev_f32 v12,s13,v12\n"
"v_fma_f32 v12,v12,v11,s13\n"
AW "a232,v12\n"
AR "v12,a13\n"
"v_subrev_f32 v12,s13,v12\n"
"v_fma_f32 v12,v12,v11,s13\n"
AW "a13,v12\n"
AR "v12,a33\n"
"v_subrev_f32 v12,s13,v12\n"
"v_fma_f32 v12,v12,v11,s13\n"
AW "a33,v12\n"
AR "v12,a53\n"
"v_subrev_f32 v12,s13,v12\n"
"v_fma_f32 v12,v12,v11,s13\n"
AW "a53,v12\n"
AR "v12,a73\n"
"v_subrev_f32 v12,s13,v12\n"
"v_fma_f32 v12,v12,v11,s13\n"
AW "a73,v12\n"
AR "v12,a93\n"
"v_subrev_f32 v12,s13,v12\n"
"v_fma_f32 v12,v12,v11,s13\n"
AW "a93,v12\n"
AR "v12,a113\n"
"v_subrev_f32 v12,s13,v12\n"
"v_fma_f32 v12,v12,v11,s13\n"
AW "a113,v12\n"
AR "v12,a133\n"
"v_subrev_f32 v12,s13,v12\n"
"v_fma_f32 v12,v12,v11,s13\n"
AW "a133,v12\n"
AR "v12,a153\n"
"v_subrev_f32 v12,s13,v12\n"
"v_fma_f32 v12,v12,v11,s13\n"
AW "a153,v12\n"
AR "v12,a173\n"
"v_subrev_f32 v12,s13,v12\n"
"v_fma_f32 v12,v12,v11,s13\n"
AW "a173,v12\n"
AR "v12,a193\n"
"v_subrev_f32 v12,s13,v12\n"
"v_fma_f32 v12,v12,v11,s13\n"
AW "a193,v12\n"
AR "v12,a213\n"
"v_subrev_f32 v12,s13,v12\n"
"v_fma_f32 v12,v12,v11,s13\n"
AW "a213,v12\n"
AR "v12,a233\n"
"v_subrev_f32 v12,s13,v12\n"
"v_fma_f32 v12,v12,v11,s13\n"
AW "a233,v12\n"
AR "v12,a14\n"
"v_subrev_f32 v12,s13,v12\n"
"v_fma_f32 v12,v12,v11,s13\n"
AW "a14,v12\n"
AR "v12,a34\n"
"v_subrev_f32 v12,s13,v12\n"
"v_fma_f32 v12,v12,v11,s13\n"
AW "a34,v12\n"
AR "v12,a54\n"
"v_subrev_f32 v12,s13,v12\n"
"v_fma_f32 v12,v12,v11,s13\n"
AW "a54,v12\n"
AR "v12,a74\n"
"v_subrev_f32 v12,s13,v12\n"
"v_fma_f32 v12,v12,v11,s13\n"
AW "a74,v12\n"
AR "v12,a94\n"
"v_subrev_f32 v12,s13,v12\n"
"v_fma_f32 v12,v12,v11,s13\n"
AW "a94,v12\n"
AR "v12,a114\n"
"v_subrev_f32 v12,s13,v12\n"
"v_fma_f32 v12,v12,v11,s13\n"
AW "a114,v12\n"
AR "v12,a134\n"
"v_subrev_f32 v12,s13,v12\n"
"v_fma_f32 v12,v12,v11,s13\n"
AW "a134,v12\n"
AR "v12,a154\n"
"v_subrev_f32 v12,s13,v12\n"
"v_fma_f32 v12,v12,v11,s13\n"
AW "a154,v12\n"
AR "v12,a174\n"
"v_subrev_f32 v12,s13,v12\n"
"v_fma_f32 v12,v12,v11,s13\n"
AW "a174,v12\n"
AR "v12,a194\n"
"v_subrev_f32 v12,s13,v12\n"
"v_fma_f32 v12,v12,v11,s13\n"
AW "a194,v12\n"
AR "v12,a214\n"
"v_subrev_f32 v12,s13,v12\n"
"v_fma_f32 v12,v12,v11,s13\n"
AW "a214,v12\n"
AR "v12,a234\n"
"v_subrev_f32 v12,s13,v12\n"
"v_fma_f32 v12,v12,v11,s13\n"
AW "a234,v12\n"
AR "v12,a15\n"
"v_subrev_f32 v12,s13,v12\n"
"v_fma_f32 v12,v12,v11,s13\n"
AW "a15,v12\n"
AR "v12,a35\n"
"v_subrev_f32 v12,s13,v12\n"
"v_fma_f32 v12,v12,v11,s13\n"
AW "a35,v12\n"
AR "v12,a55\n"
"v_subrev_f32 v12,s13,v12\n"
"v_fma_f32 v12,v12,v11,s13\n"
AW "a55,v12\n"
AR "v12,a75\n"
"v_subrev_f32 v12,s13,v12\n"
"v_fma_f32 v12,v12,v11,s13\n"
AW "a75,v12\n"
AR "v12,a95\n"
"v_subrev_f32 v12,s13,v12\n"
"v_fma_f32 v12,v12,v11,s13\n"
AW "a95,v12\n"
AR "v12,a115\n"
"v_subrev_f32 v12,s13,v12\n"
"v_fma_f32 v12,v12,v11,s13\n"
AW "a115,v12\n"
AR "v12,a135\n"
"v_subrev_f32 v12,s13,v12\n"
"v_fma_f32 v12,v12,v11,s13\n"
AW "a135,v12\n"
AR "v12,a155\n"
"v_subrev_f32 v12,s13,v12\n"
"v_fma_f32 v12,v12,v11,s13\n"
AW "a155,v12\n"
AR "v12,a175\n"
"v_subrev_f32 v12,s13,v12\n"
"v_fma_f32 v12,v12,v11,s13\n"
AW "a175,v12\n"
AR "v12,a195\n"
"v_subrev_f32 v12,s13,v12\n"
"v_fma_f32 v12,v12,v11,s13\n"
AW "a195,v12\n"
AR "v12,a215\n"
"v_subrev_f32 v12,s13,v12\n"
"v_fma_f32 v12,v12,v11,s13\n"
AW "a215,v12\n"
AR "v12,a235\n"
"v_subrev_f32 v12,s13,v12\n"
"v_fma_f32 v12,v12,v11,s13\n"
AW "a235,v12\n"
"v_mul_f32 v60,v55,v11\n"
"v_sub_f32 v44,v44,v10\n"
"v_sub_f32 v45,v45,v10\n"
"v_sub_f32 v46,v46,v10\n"
"v_sub_f32 v47,v47,v10\n"
"v_sub_f32 v84,v84,v10\n"
"v_sub_f32 v85,v85,v10\n"
"v_sub_f32 v86,v86,v10\n"
"v_sub_f32 v87,v87,v10\n"
"v_sub_f32 v104,v104,v10\n"
"v_sub_f32 v105,v105,v10\n"
"v_sub_f32 v106,v106,v10\n"
"v_sub_f32 v107,v107,v10\n"
"v_sub_f32 v124,v124,v10\n"
"v_sub_f32 v125,v125,v10\n"
"v_sub_f32 v126,v126,v10\n"
"v_sub_f32 v127,v127,v10\n"
"v_exp_f32 v104,v104\n"
"v_exp_f32 v105,v105\n"
"v_exp_f32 v106,v106\n"
"v_exp_f32 v107,v107\n"
"v_exp_f32 v124,v124\n"
"v_exp_f32 v125,v125\n"
"v_exp_f32 v126,v126\n"
"v_exp_f32 v127,v127\n"
"v_add_f32 v60,v60,v104\n"
"v_add_f32 v60,v60,v105\n"
"v_add_f32 v60,v60,v106\n"
"v_add_f32 v60,v60,v107\n"
"v_add_f32 v60,v60,v124\n"
"v_add_f32 v60,v60,v125\n"
"v_add_f32 v60,v60,v126\n"
"v_add_f32 v60,v60,v127\n"
CV "v104,v104,v105\n"
CV "v105,v106,v107\n"
CV "v106,v124,v125\n"
CV "v107,v126,v127\n"
"v_max3_f32 v10,v108,v109,v110\n"
"v_max3_f32 v11,v111,v128,v129\n"
"v_max3_f32 v10,v10,v130,v131\n"
"v_max_f32 v10,v10,v11\n"
"v_mov_b32 v11,v10\n"
"s_nop 1\n"
"v_permlane16_swap_b32 v10,v11\n"
"v_max_f32 v10,v10,v11\n"
"v_mov_b32 v11,v10\n"
"s_nop 1\n"
"v_permlane32_swap_b32 v10,v11\n"
"v_max_f32 v10,v10,v11\n"
"v_add_f32 v10,0x41300000,v10\n"
"v_max_f32 v10,0,v10\n"
"v_exp_f32 v11,-v10\n"
AR "v12,a16\n"
"v_subrev_f32 v12,s13,v12\n"
"v_fma_f32 v12,v12,v11,s13\n"
AW "a16,v12\n"
AR "v12,a36\n"
"v_subrev_f32 v12,s13,v12\n"
"v_fma_f32 v12,v12,v11,s13\n"
AW "a36,v12\n"
AR "v12,a56\n"
"v_subrev_f32 v12,s13,v12\n"
"v_fma_f32 v12,v12,v11,s13\n"
AW "a56,v12\n"
AR "v12,a76\n"
"v_subrev_f32 v12,s13,v12\n"
"v_fma_f32 v12,v12,v11,s13\n"
AW "a76,v12\n"
AR "v12,a96\n"
"v_subrev_f32 v12,s13,v12\n"
"v_fma_f32 v12,v12,v11,s13\n"
AW "a96,v12\n"
AR "v12,a116\n"
"v_subrev_f32 v12,s13,v12\n"
"v_fma_f32 v12,v12,v11,s13\n"
AW "a116,v12\n"
AR "v12,a136\n"
"v_subrev_f32 v12,s13,v12\n"
"v_fma_f32 v12,v12,v11,s13\n"
AW "a136,v12\n"
AR "v12,a156\n"
"v_subrev_f32 v12,s13,v12\n"
"v_fma_f32 v12,v12,v11,s13\n"
AW "a156,v12\n"
AR "v12,a176\n"
"v_subrev_f32 v12,s13,v12\n"
"v_fma_f32 v12,v12,v11,s13\n"
AW "a176,v12\n"
AR "v12,a196\n"
"v_subrev_f32 v12,s13,v12\n"
"v_fma_f32 v12,v12,v11,s13\n"
AW "a196,v12\n"
AR "v12,a216\n"
"v_subrev_f32 v12,s13,v12\n"
"v_fma_f32 v12,v12,v11,s13\n"
AW "a216,v12\n"
AR "v12,a236\n"
"v_subrev_f32 v12,s13,v12\n"
"v_fma_f32 v12,v12,v11,s13\n"
AW "a236,v12\n"
AR "v12,a17\n"
"v_subrev_f32 v12,s13,v12\n"
"v_fma_f32 v12,v12,v11,s13\n"
AW "a17,v12\n"
AR "v12,a37\n"
"v_subrev_f32 v12,s13,v12\n"
"v_fma_f32 v12,v12,v11,s13\n"
AW "a37,v12\n"
AR "v12,a57\n"
"v_subrev_f32 v12,s13,v12\n"
"v_fma_f32 v12,v12,v11,s13\n"
AW "a57,v12\n"
AR "v12,a77\n"
"v_subrev_f32 v12,s13,v12\n"
"v_fma_f32 v12,v12,v11,s13\n"
AW "a77,v12\n"
AR "v12,a97\n"
"v_subrev_f32 v12,s13,v12\n"
"v_fma_f32 v12,v12,v11,s13\n"
AW "a97,v12\n"
AR "v12,a117\n"
"v_subrev_f32 v12,s13,v12\n"
"v_fma_f32 v12,v12,v11,s13\n"
AW "a117,v12\n"
AR "v12,a137\n"
"v_subrev_f32 v12,s13,v12\n"
"v_fma_f32 v12,v12,v11,s13\n"
AW "a137,v12\n"
AR "v12,a157\n"
"v_subrev_f32 v12,s13,v12\n"
"v_fma_f32 v12,v12,v11,s13\n"
AW "a157,v12\n"
AR "v12,a177\n"
"v_subrev_f32 v12,s13,v12\n"
"v_fma_f32 v12,v12,v11,s13\n"
AW "a177,v12\n"
AR "v12,a197\n"
"v_subrev_f32 v12,s13,v12\n"
"v_fma_f32 v12,v12,v11,s13\n"
AW "a197,v12\n"
AR "v12,a217\n"
"v_subrev_f32 v12,s13,v12\n"
"v_fma_f32 v12,v12,v11,s13\n"
AW "a217,v12\n"
AR "v12,a237\n"
"v_subrev_f32 v12,s13,v12\n"
"v_fma_f32 v12,v12,v11,s13\n"
AW "a237,v12\n"
AR "v12,a18\n"
"v_subrev_f32 v12,s13,v12\n"
"v_fma_f32 v12,v12,v11,s13\n"
AW "a18,v12\n"
AR "v12,a38\n"
"v_subrev_f32 v12,s13,v12\n"
"v_fma_f32 v12,v12,v11,s13\n"
AW "a38,v12\n"
AR "v12,a58\n"
"v_subrev_f32 v12,s13,v12\n"
"v_fma_f32 v12,v12,v11,s13\n"
AW "a58,v12\n"
AR "v12,a78\n"
"v_subrev_f32 v12,s13,v12\n"
"v_fma_f32 v12,v12,v11,s13\n"
AW "a78,v12\n"
AR "v12,a98\n"
"v_subrev_f32 v12,s13,v12\n"
"v_fma_f32 v12,v12,v11,s13\n"
AW "a98,v12\n"
AR "v12,a118\n"
"v_subrev_f32 v12,s13,v12\n"
"v_fma_f32 v12,v12,v11,s13\n"
AW "a118,v12\n"
AR "v12,a138\n"
"v_subrev_f32 v12,s13,v12\n"
"v_fma_f32 v12,v12,v11,s13\n"
AW "a138,v12\n"
AR "v12,a158\n"
"v_subrev_f32 v12,s13,v12\n"
"v_fma_f32 v12,v12,v11,s13\n"
AW "a158,v12\n"
AR "v12,a178\n"
"v_subrev_f32 v12,s13,v12\n"
"v_fma_f32 v12,v12,v11,s13\n"
AW "a178,v12\n"
AR "v12,a198\n"
"v_subrev_f32 v12,s13,v12\n"
"v_fma_f32 v12,v12,v11,s13\n"
AW "a198,v12\n"
AR "v12,a218\n"
"v_subrev_f32 v12,s13,v12\n"
"v_fma_f32 v12,v12,v11,s13\n"
AW "a218,v12\n"
AR "v12,a238\n"
"v_subrev_f32 v12,s13,v12\n"
"v_fma_f32 v12,v12,v11,s13\n"
AW "a238,v12\n"
AR "v12,a19\n"
"v_subrev_f32 v12,s13,v12\n"
"v_fma_f32 v12,v12,v11,s13\n"
AW "a19,v12\n"
AR "v12,a39\n"
"v_subrev_f32 v12,s13,v12\n"
"v_fma_f32 v12,v12,v11,s13\n"
AW "a39,v12\n"
AR "v12,a59\n"
"v_subrev_f32 v12,s13,v12\n"
"v_fma_f32 v12,v12,v11,s13\n"
AW "a59,v12\n"
AR "v12,a79\n"
"v_subrev_f32 v12,s13,v12\n"
"v_fma_f32 v12,v12,v11,s13\n"
AW "a79,v12\n"
AR "v12,a99\n"
"v_subrev_f32 v12,s13,v12\n"
"v_fma_f32 v12,v12,v11,s13\n"
AW "a99,v12\n"
AR "v12,a119\n"
"v_subrev_f32 v12,s13,v12\n"
"v_fma_f32 v12,v12,v11,s13\n"
AW "a119,v12\n"
AR "v12,a139\n"
"v_subrev_f32 v12,s13,v12\n"
"v_fma_f32 v12,v12,v11,s13\n"
AW "a139,v12\n"
AR "v12,a159\n"
"v_subrev_f32 v12,s13,v12\n"
"v_fma_f32 v12,v12,v11,s13\n"
AW "a159,v12\n"
AR "v12,a179\n"
"v_subrev_f32 v12,s13,v12\n"
"v_fma_f32 v12,v12,v11,s13\n"
AW "a179,v12\n"
AR "v12,a199\n"
"v_subrev_f32 v12,s13,v12\n"
"v_fma_f32 v12,v12,v11,s13\n"
AW "a199,v12\n"
AR "v12,a219\n"
"v_subrev_f32 v12,s13,v12\n"
"v_fma_f32 v12,v12,v11,s13\n"
AW "a219,v12\n"
AR "v12,a239\n"
"v_subrev_f32 v12,s13,v12\n"
"v_fma_f32 v12,v12,v11,s13\n"
AW "a239,v12\n"
"v_mul_f32 v61,v56,v11\n"
"v_sub_f32 v48,v48,v10\n"
"v_sub_f32 v49,v49,v10\n"
"v_sub_f32 v50,v50,v10\n"
"v_sub_f32 v51,v51,v10\n"
"v_sub_f32 v88,v88,v10\n"
"v_sub_f32 v89,v89,v10\n"
"v_sub_f32 v90,v90,v10\n"
"v_sub_f32 v91,v91,v10\n"
"v_sub_f32 v108,v108,v10\n"
"v_sub_f32 v109,v109,v10\n"
"v_sub_f32 v110,v110,v10\n"
"v_sub_f32 v111,v111,v10\n"
"v_sub_f32 v128,v128,v10\n"
"v_sub_f32 v129,v129,v10\n"
"v_sub_f32 v130,v130,v10\n"
"v_sub_f32 v131,v131,v10\n"
"v_exp_f32 v108,v108\n"
"v_exp_f32 v109,v109\n"
"v_exp_f32 v110,v110\n"
"v_exp_f32 v111,v111\n"
"v_exp_f32 v128,v128\n"
"v_exp_f32 v129,v129\n"
"v_exp_f32 v130,v130\n"
"v_exp_f32 v131,v131\n"
"v_add_f32 v61,v61,v108\n"
"v_add_f32 v61,v61,v109\n"
"v_add_f32 v61,v61,v110\n"
"v_add_f32 v61,v61,v111\n"
"v_add_f32 v61,v61,v128\n"
"v_add_f32 v61,v61,v129\n"
"v_add_f32 v61,v61,v130\n"
"v_add_f32_e64 v61,v61,v131\n"
CV "v108,v108,v109\n"
CV "v109,v110,v111\n"
CV "v110,v128,v129\n"
CV "v111,v130,v131\n"
"s_nop 7\n"
"s_setpc_b64 s[52:53]\n"
".Ldone:\n"
    : "+{v0}"(tid), "+{s23}"(bid), "+{s[20:21]}"(ka)
    :
    : "v1", "v2", "v3", "v4", "v5", "v6", "v7", "v8", "v9", "v10", "v11", "v12", "v13", "v14", "v15", "v16", "v17", "v18", "v19", "v20", "v21", "v22", "v23", "v24", "v25", "v26", "v27", "v28", "v29", "v30", "v31", "v32", "v33", "v34", "v35", "v36", "v37", "v38", "v39", "v40", "v41", "v42", "v43", "v44", "v45", "v46", "v47", "v48", "v49", "v50", "v51", "v52", "v53", "v54", "v55", "v56", "v57", "v58", "v59", "v60", "v61", "v62", "v63", "v64", "v65", "v66", "v67", "v68", "v69", "v70", "v71", "v72", "v73", "v74", "v75", "v76", "v77", "v78", "v79", "v80", "v81", "v82", "v83", "v84", "v85", "v86", "v87", "v88", "v89", "v90", "v91", "v92", "v93", "v94", "v95", "v96", "v97", "v98", "v99", "v100", "v101", "v102", "v103", "v104", "v105", "v106", "v107", "v108", "v109", "v110", "v111", "v112", "v113", "v114", "v115", "v116", "v117", "v118", "v119", "v120", "v121", "v122", "v123", "v124", "v125", "v126", "v127", "v128", "v129", "v130", "v131", "v132", "v133", "v134", "v135", "v136", "v137", "v138", "v139", "v140", "v141", "v142", "v143", "v144", "v145", "v146", "v147", "v148", "v149", "v150", "v151", "v152", "v153", "v154", "v155", "v156", "v157", "v158", "v159", "v160", "v161", "v162", "v163", "v164", "v165", "v166", "v167", "v168", "v169", "v170", "v171", "v172", "v173", "v174", "v175", "v176", "v177", "v178", "v179", "v180", "v181", "v182", "v183", "v184", "v185", "v186", "v187", "v188", "v189", "v190", "v191", "v192", "v193", "v194", "v195", "v196", "v197", "v198", "v199", "v200", "v201", "v202", "v203", "v204", "v205", "v206", "v207", "v208", "v209", "v210", "v211", "v212", "v213", "v214", "v215", "v216", "v217", "v218", "v219", "v220", "v221", "v222", "v223", "v224", "v225", "v226", "v227", "v228", "v229", "v230", "v231", "v232", "v233", "v234", "v235", "v236", "v237", "v238", "v239", "v240", "v241", "v242", "v243", "v244", "v245", "v246", "v247", "v248", "v249", "v250", "v251", "v252", "v253", "v254", "v255", "a0", "a1", "a2", "a3", "a4", "a5", "a6", "a7", "a8", "a9", "a10", "a11", "a12", "a13", "a14", "a15", "a16", "a17", "a18", "a19", "a20", "a21", "a22", "a23", "a24", "a25", "a26", "a27", "a28", "a29", "a30", "a31", "a32", "a33", "a34", "a35", "a36", "a37", "a38", "a39", "a40", "a41", "a42", "a43", "a44", "a45", "a46", "a47", "a48", "a49", "a50", "a51", "a52", "a53", "a54", "a55", "a56", "a57", "a58", "a59", "a60", "a61", "a62", "a63", "a64", "a65", "a66", "a67", "a68", "a69", "a70", "a71", "a72", "a73", "a74", "a75", "a76", "a77", "a78", "a79", "a80", "a81", "a82", "a83", "a84", "a85", "a86", "a87", "a88", "a89", "a90", "a91", "a92", "a93", "a94", "a95", "a96", "a97", "a98", "a99", "a100", "a101", "a102", "a103", "a104", "a105", "a106", "a107", "a108", "a109", "a110", "a111", "a112", "a113", "a114", "a115", "a116", "a117", "a118", "a119", "a120", "a121", "a122", "a123", "a124", "a125", "a126", "a127", "a128", "a129", "a130", "a131", "a132", "a133", "a134", "a135", "a136", "a137", "a138", "a139", "a140", "a141", "a142", "a143", "a144", "a145", "a146", "a147", "a148", "a149", "a150", "a151", "a152", "a153", "a154", "a155", "a156", "a157", "a158", "a159", "a160", "a161", "a162", "a163", "a164", "a165", "a166", "a167", "a168", "a169", "a170", "a171", "a172", "a173", "a174", "a175", "a176", "a177", "a178", "a179", "a180", "a181", "a182", "a183", "a184", "a185", "a186", "a187", "a188", "a189", "a190", "a191", "a192", "a193", "a194", "a195", "a196", "a197", "a198", "a199", "a200", "a201", "a202", "a203", "a204", "a205", "a206", "a207", "a208", "a209", "a210", "a211", "a212", "a213", "a214", "a215", "a216", "a217", "a218", "a219", "a220", "a221", "a222", "a223", "a224", "a225", "a226", "a227", "a228", "a229", "a230", "a231", "a232", "a233", "a234", "a235", "a236", "a237", "a238", "a239", "a240", "a241", "a242", "a243", "a244", "a245", "a246", "a247", "a248", "a249", "a250", "a251", "a252", "a253", "a254", "a255", "s0", "s1", "s2", "s3", "s4", "s5", "s6", "s7", "s8", "s9", "s10", "s11", "s12", "s13", "s14", "s15", "s16", "s17", "s18", "s19", "s22", "s24", "s25", "s26", "s27", "s28", "s29", "s30", "s31", "s32", "s33", "s34", "s35", "s36", "s37", "s38", "s39", "s40", "s41", "s42", "s43", "s44", "s45", "s46", "s47", "s48", "s49", "s50", "s51", "s52", "s53", "s54", "s55", "s56", "s57", "s58", "s59", "s60", "s61", "s62", "s63", "s64", "s65", "s66", "s67", "s68", "s69", "s70", "s71", "s72", "s73", "s74", "s75", "s76", "s77", "s78", "s79", "s80", "s81", "s82", "s83", "s84", "s85", "s86", "s87", "s88", "s89", "s90", "s91", "s92", "s93", "s94", "s95", "s96", "s97", "s98", "s99", "s100", "s101", "vcc", "scc", "memory");
}

extern "C" void kernel_launch(void* const* d_in, const int* in_sizes, int n_in, void* d_out, int out_size, void* d_ws, size_t ws_size, hipStream_t stream) {
  Ptrs P;
  for (int br = 0; br < 2; ++br) { P.x[br] = d_in[5 * br]; P.k[br] = d_in[5 * br + 1]; P.v[br] = d_in[5 * br + 2]; P.wq[br] = d_in[5 * br + 3]; P.wo[br] = d_in[5 * br + 4]; }
  (void)hipFuncSetAttribute((const void*)fused_kernel, hipFuncAttributeMaxDynamicSharedMemorySize, LDS1);
  fused_kernel<<<256, 256, LDS1, stream>>>(P, (char*)d_ws, (char*)d_out);
}
```
